```python
import torch

def _softmax(x, axis=-1):
    return torch.softmax(x, dim=axis)
def _gelu(x):
    return torch.nn.functional.gelu(x, approximate="tanh")
def _silu(x):
    return torch.nn.functional.silu(x)
def _sigmoid(x):
    return torch.sigmoid(x)
def _layernorm(x, axis=-1, eps=1e-5):
    m = x.mean(dim=axis, keepdim=True)
    v = x.var(dim=axis, keepdim=True, unbiased=False)
    return (x - m) / torch.sqrt(v + eps)
def _scatter_add(tgt, idx, src, axis=0):
    return tgt.clone().index_add_(axis, idx, src)
def _segment_sum(src, seg, n):
    out = torch.zeros((n,) + src.shape[1:], dtype=src.dtype)
    return out.index_add_(0, seg, src)
def _pad(x, pw):
    flat = tuple(v for pair in reversed(pw) for v in pair)
    return torch.nn.functional.pad(x, flat)

kernel_name = 'interleave_layers__ssm_conv_h3__mixer_s_mlp__prompt'

def setup_inputs():

    _w = (lambda a: (lambda t: t.to(torch.bfloat16) if t.is_floating_point() else t)(torch.as_tensor(a)))
    def _A_setup():
        B, S, D = 1, 2048, 1024
        ks = 4
        return {
            "q": _w((lambda *s: torch.randn(*s, dtype=torch.bfloat16) * 0.05)(B, S, D)),
            "k": _w((lambda *s: torch.randn(*s, dtype=torch.bfloat16) * 0.4)(B, S, D)),
            "v": _w((lambda *s: torch.randn(*s, dtype=torch.bfloat16) * 0.4)(B, S, D)),
            "shift_k": _w((lambda *s: torch.randn(*s, dtype=torch.bfloat16) * 0.4)(D, ks) * 0.5),
            "ssm_k": _w((lambda *s: torch.randn(*s, dtype=torch.bfloat16) * 0.4)(D, S) * (1.0 / S ** 0.5)),
            "ssm_D": _w((lambda *s: torch.randn(*s, dtype=torch.bfloat16) * 0.4)(D)),
        }
    _A_in = _A_setup()

    def _B_setup():
        B, S, D, F = 1, 2048, 1024, 3584
        return {
            "x":  _w((lambda *s: torch.randn(*s, dtype=torch.bfloat16) * 0.05)(B, S, D)),
            "w1": _w((lambda *s: torch.randn(*s, dtype=torch.bfloat16) * 1.6)(D, D) * (D ** -0.5)),
            "w2": _w((lambda *s: torch.randn(*s, dtype=torch.bfloat16) * 1.6)(D, D) * (D ** -0.5)),
            "w3": _w((lambda *s: torch.randn(*s, dtype=torch.bfloat16) * 1.6)(D, F) * (D ** -0.5)),
            "w4": _w((lambda *s: torch.randn(*s, dtype=torch.bfloat16) * 1.6)(F, D) * (F ** -0.5)),
        }
    _B_in = _B_setup()

    return {
        "A_q": _A_in["q"],
        "A_k": _A_in["k"],
        "A_v": _A_in["v"],
        "A_shift_k": _A_in["shift_k"],
        "A_ssm_k": _A_in["ssm_k"],
        "A_ssm_D": _A_in["ssm_D"],
        "B_x": _B_in["x"],
        "B_w1": _B_in["w1"],
        "B_w2": _B_in["w2"],
        "B_w3": _B_in["w3"],
        "B_w4": _B_in["w4"],
    }

def reference(A_q, A_k, A_v, A_shift_k, A_ssm_k, A_ssm_D, B_x, B_w1, B_w2, B_w3, B_w4):
    def _A_ref(q, k, v, shift_k, ssm_k, ssm_D):
        B, S, D = q.shape
        ks = shift_k.shape[-1]
        k_pad = _pad(k, ((0, 0), (ks - 1, 0), (0, 0)))
        k_shift = torch.zeros_like(k)
        for t in range(ks):
            k_shift = k_shift + k_pad[:, t:t + S, :] * shift_k[:, ks - 1 - t][None, None, :]
        kv = k_shift * v
        L = 2 * S
        kv_pad = _pad(kv, ((0, 0), (0, S), (0, 0)))
        kf_pad = _pad(ssm_k, ((0, 0), (0, S)))
        kv_f = (lambda x, axes=(-2,-1): torch.fft.rfft2(x.float(), dim=axes))(kv_pad[:, None, :, :], axes=(1, 2))
        kf_f = (lambda x, axes=(-2,-1): torch.fft.rfft2(x.float(), dim=axes))(kf_pad[:, None, :], axes=(1, 2))
        kf_f = (lambda x, *d: x.permute(*d))(kf_f, 1, 2, 0)
        y_f = kv_f * kf_f[None, :, :, :]
        y = (lambda x, s, axes=(-2,-1): torch.fft.irfft2(x, s=s, dim=axes).to(torch.bfloat16))(y_f, (1, L), axes=(1, 2))[:, 0, :S, :]
        y = y + kv * ssm_D[None, None, :]
        return q * y

    def _B_ref(x, w1, w2, w3, w4):
        B, S, D = x.shape
        h = _gelu(_layernorm(x, axis=-1) @ w1)
        c = D // 4
        g0 = _pad(h[:, :, 0*c:1*c], ((0, 0), (1, 0), (0, 0)))[:, :S, :]
        g1 = _pad(h[:, :, 1*c:2*c], ((0, 0), (0, 1), (0, 0)))[:, 1:, :]
        g2 = _pad(h[:, :, 2*c:3*c], ((0, 0), (2, 0), (0, 0)))[:, :S, :]
        g3 = _pad(h[:, :, 3*c:   ], ((0, 0), (0, 2), (0, 0)))[:, 2:, :]
        hs = torch.concatenate([g0, g1, g2, g3], axis=-1) @ w2
        x1 = x + hs
        h2 = _gelu(_layernorm(x1, axis=-1) @ w3) @ w4
        return x1 + h2

    x = A_q
    x = x + _A_ref(_layernorm(x, axis=-1), A_k, A_v, A_shift_k, A_ssm_k, A_ssm_D)
    x = x + _B_ref(_layernorm(x, axis=-1), B_w1, B_w2, B_w3, B_w4)
    x = x + _A_ref(_layernorm(x, axis=-1), A_k, A_v, A_shift_k, A_ssm_k, A_ssm_D)
    x = x + _B_ref(_layernorm(x, axis=-1), B_w1, B_w2, B_w3, B_w4)
    return x
```

```cpp
#include <hip/hip_runtime.h>
#include <hip/hip_bf16.h>
#include <stdint.h>
#include <type_traits>

typedef short s16x8 __attribute__((ext_vector_type(8)));
typedef short s16x4 __attribute__((ext_vector_type(4)));
typedef float f32x4 __attribute__((ext_vector_type(4)));
typedef unsigned short u16;
typedef unsigned short u16x8 __attribute__((ext_vector_type(8)));
typedef unsigned short u16x4 __attribute__((ext_vector_type(4)));
typedef float f32x2 __attribute__((ext_vector_type(2)));
typedef __bf16 bf16x2 __attribute__((ext_vector_type(2)));
typedef unsigned u32x4v __attribute__((ext_vector_type(4)));
typedef __attribute__((address_space(1))) unsigned gu32;

constexpr int S_ = 2048, D_ = 1024, F_ = 3584;
#define PF 1

constexpr int WS_USED = 111243392;
constexpr int OFF_KVR = 4194304, OFF_YF = 8388608, OFF_XP = 12582912, OFF_HS = 22020096, OFF_X1 = 31457280, OFF_G = 40894464, OFF_P0 = 71303168, OFF_P1 = 89128960, OFF_Y = 98566144, OFF_S1 = 106954752, OFF_ST = 107479040, OFF_FL = 109051904, OFF_FXP = 109056000, OFF_FH = 109334528, OFF_FX1 = 109613056, OFF_FG = 109891584, OFF_FP = 110587904, OFF_FKV = 111144960, OFF_FY = 111177728, OFF_EP = 111243264, OFF_DBG = 115343360;

#define WS_DIAG 0

__device__ __forceinline__ float bf2f(u16 v) { return __uint_as_float(((unsigned)v) << 16); }
__device__ __forceinline__ u16 f2bf(float f) { return __builtin_bit_cast(u16, (__bf16)f); }
__device__ __forceinline__ float rbf(float f) { return bf2f(f2bf(f)); }
__device__ __forceinline__ void st16(void* p, u16x8 v) {
  asm volatile("global_store_dwordx4 %0, %1, off sc0 sc1\n\ts_nop 1" :: "v"(p), "v"(v) : "memory");
}
#define WAIT_VM(n) asm volatile("s_waitcnt vmcnt(%0)" ::"n"(n) : "memory")
#define WAIT_LGKM0() asm volatile("s_waitcnt lgkmcnt(0)" ::: "memory")

#define CODE_RANGE(B, E, p, n)                                                                                   \
  const char* p; unsigned n;                                                                                     \
  { unsigned lo_, hi_;                                                                                           \
    asm volatile("s_getpc_b64 s[98:99]\nLpc%=:\n\ts_add_u32 %0, s98, (" B "-Lpc%=)&4294967295\n\ts_addc_u32 %1, s99, (" B "-Lpc%=)>>32\n\t" \
                 "s_mov_b32 %2, " E "-" B : "=s"(lo_), "=s"(hi_), "=s"(n) :: "s98", "s99", "scc");               \
    p = (const char*)(((unsigned long long)hi_ << 32) | lo_); }
__device__ __forceinline__ unsigned touch(const char* p, unsigned n, unsigned off) {
  unsigned t;
  asm volatile("global_load_dword %0, %1, off" : "=v"(t) : "v"(p + (off < n ? off : 0u)) : "memory");
  return t;
}

__device__ __forceinline__ void touch_lds(const char* p, unsigned a) {
  asm volatile("s_mov_b32 m0, %1\n\ts_nop 0\n\tglobal_load_lds_dword %0, off" :: "v"(p), "s"(a) : "memory");
}
#define PFPOL ""
__device__ __forceinline__ void touch_pol(const char* p, unsigned a) {
  asm volatile("s_mov_b32 m0, %1\n\ts_nop 0\n\tglobal_load_lds_dword %0, off " PFPOL :: "v"(p), "s"(a) : "memory");
}
#define YSLEEP 4
#define KSLEEP 2
#define CTS(i)
__device__ __forceinline__ f32x2 up2(unsigned p) { return (f32x2){__uint_as_float(p << 16), __uint_as_float(p & 0xffff0000u)}; }
__device__ __forceinline__ unsigned pk2(f32x2 v) { return __builtin_bit_cast(unsigned, __builtin_convertvector(v, bf16x2)); }
__device__ __forceinline__ f32x2 rbf2(f32x2 v) { return up2(pk2(v)); }
template <int N, class F> __device__ __forceinline__ void sfor(F&& f) {
  if constexpr (N > 0) { sfor<N - 1>(f); f(std::integral_constant<int, N - 1>{}); }
}
__device__ __forceinline__ void set_flag(char* p, int i, unsigned ep) {
  if (i < 32) __hip_atomic_store((gu32*)p + i, ep, __ATOMIC_RELAXED, __HIP_MEMORY_SCOPE_AGENT);
}
template <int NL>
__device__ __forceinline__ void poll_flags(const char* p, int n, int lane, unsigned ep) {
  for (int it = 0; it < 20000; ++it) {
    bool ok = true;
#pragma unroll
    for (int i = 0; i < NL; ++i) {
      const int f = lane + 64 * i;
      ok &= __hip_atomic_load((gu32*)(p + (f < n ? f : 0) * 128), __ATOMIC_RELAXED, __HIP_MEMORY_SCOPE_AGENT) == ep;
    }
    if (__all(ok)) break;
    __builtin_amdgcn_s_sleep(YSLEEP);
  }
  asm volatile("" ::: "memory");
}

constexpr int CV_HP = (2048 + 32) * 2 + 0;
constexpr int CV_UR = (2048 + 256) * 2;
constexpr int CV_CH = 4 * CV_HP + CV_UR;
#define PFW_R 2
constexpr int NCV = 171;
constexpr int CV_STG = 6 * CV_CH;

struct KvRegs { u32x4v k0, k1, k2, k3, k4, k5, k6, v0, v1, v2, v3, s0, s1, s2, s3; };
#define LD16(d, p, pol) asm volatile("global_load_dwordx4 %0, %1, off " pol : "=v"(d) : "v"(p) : "memory")
__device__ __forceinline__ void kv_load(KvRegs& R, const u16* __restrict__ kin, const u16* __restrict__ vin, const u16* __restrict__ shift_k, int b, int tid) {
  const int tr = (b >> 4) * 128 + 4 * (tid >> 3), cc = (b & 15) * 64 + 8 * (tid & 7);
  const u16* kp = kin + (size_t)tr * D_ + cc;
  const u16* vp = vin + (size_t)tr * D_ + cc;
  const u16* sp = shift_k + (size_t)cc * 4;
  LD16(R.k3, kp, "nt"); LD16(R.k4, kp + D_, "nt"); LD16(R.k5, kp + 2 * D_, "nt"); LD16(R.k6, kp + 3 * D_, "nt");
  LD16(R.v0, vp, "nt"); LD16(R.v1, vp + D_, "nt"); LD16(R.v2, vp + 2 * D_, "nt"); LD16(R.v3, vp + 3 * D_, "nt");
  const u16* kq = tr ? kp - 3 * D_ : kp;
  LD16(R.k0, kq, "nt"); LD16(R.k1, kq + D_, "nt"); LD16(R.k2, kq + 2 * D_, "nt");
  LD16(R.s0, sp, ""); LD16(R.s1, sp + 8, ""); LD16(R.s2, sp + 16, ""); LD16(R.s3, sp + 24, "");
}
template <int N>
__device__ __forceinline__ void kv_wait(KvRegs& R) {
  asm volatile("s_waitcnt vmcnt(%15)" : "+v"(R.k0), "+v"(R.k1), "+v"(R.k2), "+v"(R.k3), "+v"(R.k4), "+v"(R.k5), "+v"(R.k6), "+v"(R.v0), "+v"(R.v1), "+v"(R.v2),
               "+v"(R.v3), "+v"(R.s0), "+v"(R.s1), "+v"(R.s2), "+v"(R.s3) : "n"(N) : "memory");
}
__device__ __forceinline__ void kv_store(KvRegs& R, char* __restrict__ ws, char* T, int b, int tid) {
  const int t0 = (b >> 4) * 128, c0 = (b & 15) * 64, ch8 = tid & 7, rg = tid >> 3;
  if (t0 + 4 * rg < 3) R.k0 = R.k1 = R.k2 = (u32x4v){0u, 0u, 0u, 0u};
  typedef unsigned u32x2 __attribute__((ext_vector_type(2)));
  const u32x4v kr[7] = {R.k0, R.k1, R.k2, R.k3, R.k4, R.k5, R.k6}, vr[4] = {R.v0, R.v1, R.v2, R.v3}, sk[4] = {R.s0, R.s1, R.s2, R.s3};
#pragma unroll
  for (int j2 = 0; j2 < 4; ++j2) {
    const f32x2 sa = up2(sk[j2][0]), sb = up2(sk[j2][1]), sc = up2(sk[j2][2]), sd = up2(sk[j2][3]);
    const f32x2 s0 = {sa[0], sc[0]}, s1 = {sa[1], sc[1]}, s2 = {sb[0], sd[0]}, s3 = {sb[1], sd[1]};
    f32x2 kf[7], o[4];
#pragma unroll
    for (int r = 0; r < 7; ++r) kf[r] = up2(kr[r][j2]);
#pragma unroll
    for (int r = 0; r < 4; ++r)
      o[r] = __builtin_elementwise_fma(kf[r + 3], s0, __builtin_elementwise_fma(kf[r + 2], s1, __builtin_elementwise_fma(kf[r + 1], s2, kf[r] * s3))) * up2(vr[r][j2]);
#pragma unroll
    for (int e = 0; e < 2; ++e)
      *(u32x2*)(T + (8 * ch8 + 2 * j2 + e) * 264 + (124 - 4 * rg) * 2) = (u32x2){pk2((f32x2){o[3][e], o[2][e]}), pk2((f32x2){o[1][e], o[0][e]})};
  }
  __syncthreads();
  u16* kvr = (u16*)(ws + OFF_KVR);
#pragma unroll
  for (int i = 0; i < 4; ++i) {
    const int idx = tid + 256 * i, ch = idx >> 4, cb = idx & 15;
    const u16x4 lo = *(const u16x4*)(T + ch * 264 + cb * 16), hi = *(const u16x4*)(T + ch * 264 + cb * 16 + 8);
    st16(kvr + (size_t)(c0 + ch) * S_ + (1920 - t0) + cb * 8, __builtin_shufflevector(lo, hi, 0, 1, 2, 3, 4, 5, 6, 7));
  }
}

typedef unsigned u32x2v __attribute__((ext_vector_type(2)));
struct HRegs { u32x4v hv0, hv1, hv2, hv3, hv4, hv5; u32x2v hn0, hn1, hn2, hn3, hn4, hn5; unsigned d0; };
#define H_EACH(F) F(0) F(1) F(2) F(3) F(4) F(5)
__device__ __forceinline__ void h_load(HRegs& H, const u16* __restrict__ ssm_k, const u16* __restrict__ ssm_D, int ci, int tid) {
  const int c0 = 6 * ci;
#define F(i) { const u16* hp = ssm_k + (size_t)min(c0 + i, D_ - 1) * S_ + tid * 8; \
    asm volatile("global_load_dwordx4 %0, %1, off nt" : "=v"(H.hv##i) : "v"(hp) : "memory"); \
    asm volatile("global_load_dwordx2 %0, %1, off nt" : "=v"(H.hn##i) : "v"(hp + (tid < 255 ? 8 : 0)) : "memory"); }
  H_EACH(F)
#undef F
  const u16* dp = ssm_D + min(c0 + ((tid & 32) ? 4 + (tid >> 7) : (tid >> 6)), D_ - 1);
  asm volatile("global_load_ushort %0, %1, off" : "=v"(H.d0) : "v"(dp) : "memory");
}
__device__ __forceinline__ void h_spread(HRegs& H, char* lds, int tid) {
  if (tid < 48) *(u32x4v*)(lds + (tid >> 3) * CV_CH + ((tid >> 1) & 3) * CV_HP + (tid & 1) * 16) = (u32x4v){0u, 0u, 0u, 0u};
  if (tid >= 64) *(u32x4v*)(lds + ((tid - 64) >> 5) * CV_CH + 4 * CV_HP + 4096 + (tid & 31) * 16) = (u32x4v){0u, 0u, 0u, 0u};
  auto one = [&](int i, u32x4v hv, u32x2v hn) {
    char* base = lds + i * CV_CH + 32 + tid * 16;
    const unsigned w0 = hv[0], w1 = hv[1], w2 = hv[2], w3 = hv[3], w4 = hn[0], w5 = hn[1];
#define AB(a, b) __builtin_amdgcn_alignbyte(a, b, 2)
    *(u32x4v*)(base) = hv;
    *(u32x4v*)(base + CV_HP) = (u32x4v){AB(w1, w0), AB(w2, w1), AB(w3, w2), AB(w4, w3)};
    *(u32x4v*)(base + 2 * CV_HP) = (u32x4v){w1, w2, w3, w4};
    *(u32x4v*)(base + 3 * CV_HP) = (u32x4v){AB(w2, w1), AB(w3, w2), AB(w4, w3), AB(w5, w4)};
#undef AB
  };
#define F(i) one(i, H.hv##i, H.hn##i);
  H_EACH(F)
#undef F
}
__device__ __forceinline__ void h_patch(HRegs& H, char* lds, int tid) {
  if (tid == 0) {
    auto one = [&](int i, u32x4v hv) {
      u16* base = (u16*)(lds + i * CV_CH);
      const u16 h0 = (u16)(hv[0] & 0xffff), h1 = (u16)(hv[0] >> 16), h2 = (u16)(hv[1] & 0xffff);
      base[CV_HP / 2 + 15] = h0;
      base[2 * (CV_HP / 2) + 14] = h0; base[2 * (CV_HP / 2) + 15] = h1;
      base[3 * (CV_HP / 2) + 13] = h0; base[3 * (CV_HP / 2) + 14] = h1; base[3 * (CV_HP / 2) + 15] = h2;
    };
#define F(i) one(i, H.hv##i);
    H_EACH(F)
#undef F
  }
}

__device__ __forceinline__ void conv_part(char* __restrict__ ws, HRegs& H, char* lds, int ci, int tid, int lane, int wave, unsigned ep,
                                          const u16* __restrict__ w3, const u16* __restrict__ w4) {
  const int c0 = 6 * ci;
  const u16* kvr = (const u16*)(ws + OFF_KVR);
  auto kv_fetch = [&](bool late) {
    if (wave == 0) {
      const int cg = lane < 16 ? c0 >> 6 : min(c0 + 5, D_ - 1) >> 6, ts = late ? 14 + (lane & 1) : min(lane & 15, 13);
      gu32* f = (gu32*)(ws + OFF_FKV + (ts * 16 + cg) * 128);
      for (int it = 0; it < 20000; ++it) {
        if (__all(__hip_atomic_load(f, __ATOMIC_RELAXED, __HIP_MEMORY_SCOPE_AGENT) == ep)) break;
        __builtin_amdgcn_s_sleep(KSLEEP);
      }
    }
    if (!late) __syncthreads();
    if (late ? (wave == 0 && lane < 32) : (wave > 0 || lane >= 32)) {
#pragma unroll
      for (int i = 0; i < 6; ++i)
        __builtin_amdgcn_global_load_lds((const unsigned*)(kvr + (size_t)min(c0 + i, D_ - 1) * S_ + wave * 512 + lane * 8),
                                         (unsigned*)(lds + i * CV_CH + 4 * CV_HP + wave * 1024), 16, 0, 16);
    }
  };
  kv_fetch(false);
  CTS(3);
  WAIT_VM(0);
  __syncthreads();
  CTS(4);
  const int l15 = lane & 15, lg = lane >> 4, wsh = (l15 + 1) & 3, sh = 4 + (wave >> 1);
  const unsigned lb = (unsigned)(size_t)lds;
  const unsigned ao = wsh * CV_HP + 2 * (8 * lg + (l15 + 1 - wsh)), bo = 4 * CV_HP + 2 * (2032 - 16 * l15 + 8 * lg) - 2 * 256 * 7;
  const unsigned own = lb + wave * CV_CH, shd = lb + sh * CV_CH + 256 * (wave & 1);
  char* stg = lds + CV_STG;
  char* fy = ws + OFF_FY + ci * 128;
  auto rd1 = [&](unsigned base, int i, s16x4 (&ah)[8][2], s16x8 (&bfr)[8]) {
    const int m = i / 3, e = i % 3;
    if (e < 2) asm volatile("ds_read_b64 %0, %1 offset:%2" : "=v"(ah[m][e & 1]) : "v"(base + ao), "n"(512 * m + 8 * e));
    else asm volatile("ds_read_b128 %0, %1 offset:%2" : "=v"(bfr[m]) : "v"(base + bo), "n"(512 * (7 - m)));
  };
  const float dOwn = bf2f((u16)__builtin_amdgcn_readlane(H.d0, 0)), dShd = (wave & 1) ? 0.f : bf2f((u16)__builtin_amdgcn_readlane(H.d0, 32));
  auto finish = [&](auto s0_c, auto ns_c, f32x4 (&acc)[2][7]) {
    constexpr int S0 = decltype(s0_c)::value, NS = decltype(ns_c)::value;
    u32x2v ku[2][NS];
    const unsigned uo = 4 * CV_HP + 2 * (2044 - 16 * l15 - 4 * lg) - 512 * 7;
#pragma unroll
    for (int s = 0; s < NS; ++s) {
      asm volatile("ds_read_b64 %0, %1 offset:%2" : "=v"(ku[0][s]) : "v"(lb + wave * CV_CH + uo), "n"(512 * (7 - S0 - s)));
      asm volatile("ds_read_b64 %0, %1 offset:%2" : "=v"(ku[1][s]) : "v"(lb + sh * CV_CH + uo), "n"(512 * (7 - S0 - s)));
    }
    WAIT_LGKM0();
    __builtin_amdgcn_sched_barrier(0);
#pragma unroll
    for (int u = 0; u < 2; ++u)
#pragma unroll
      for (int s = 0; s < NS; ++s) {
        const f32x2 k0 = up2(ku[u][s][0]), k1 = up2(ku[u][s][1]);
        const float d = u ? dShd : dOwn;
        *(u32x2v*)(stg + u * 14336 + wave * 3584 + 2 * (256 * s + 16 * l15 + 4 * lg)) =
            (u32x2v){pk2((f32x2){fmaf(k1[1], d, acc[u][s][0]), fmaf(k1[0], d, acc[u][s][1])}), pk2((f32x2){fmaf(k0[1], d, acc[u][s][2]), fmaf(k0[0], d, acc[u][s][3])})};
      }
    __syncthreads();
    const int ch = (tid & 7) < 6 ? (tid & 7) : 0, tb0 = tid >> 3;
    const char* p0 = stg + (ch < 4 ? ch * 3584 : 14336 + (ch - 4) * 7168) + tb0 * 16;
    char* yo = ws + OFF_Y + ((size_t)(32 * S0 + tb0) * NCV + ci) * 128 + (tid & 7) * 16;
#pragma unroll
    for (int i = 0; i < NS; ++i) {
      const u32x4v ya = *(const u32x4v*)(p0 + 512 * i), yb = *(const u32x4v*)(p0 + 512 * i + 3584);
      u32x4v o;
#pragma unroll
      for (int r = 0; r < 4; ++r) o[r] = ch < 4 ? ya[r] : pk2(up2(ya[r]) + up2(yb[r]));
      st16(yo + (size_t)i * 32 * NCV * 128, __builtin_bit_cast(u16x8, o));
    }
  };
  s16x4 a0[8][2], a1[8][2];
  s16x8 b0[8], b1[8];
  f32x4 acc[2][7];
#pragma unroll
  for (int i = 0; i < 4; ++i) acc[i >> 1][i & 1] = (f32x4){0.f, 0.f, 0.f, 0.f};
  {
    s16x4 ax[12][2][2];
    s16x8 bx[12][2];
#pragma unroll
    for (int q = 0; q < 12; ++q) {
      const unsigned base = q < 8 ? own + 64 * q : shd + 64 * (q - 8);
#pragma unroll
      for (int m = 0; m < 2; ++m) {
        asm volatile("ds_read_b64 %0, %1 offset:%2" : "=v"(ax[q][m][0]) : "v"(base + ao), "n"(512 * m));
        asm volatile("ds_read_b64 %0, %1 offset:%2" : "=v"(ax[q][m][1]) : "v"(base + ao), "n"(512 * m + 8));
        asm volatile("ds_read_b128 %0, %1 offset:%2" : "=v"(bx[q][m]) : "v"(base + bo), "n"(512 * (7 - m)));
      }
    }
    WAIT_LGKM0();
    __builtin_amdgcn_sched_barrier(0);
#pragma unroll
    for (int q = 0; q < 12; ++q) {
      const s16x8 a0f = __builtin_shufflevector(ax[q][0][0], ax[q][0][1], 0, 1, 2, 3, 4, 5, 6, 7), a1f = __builtin_shufflevector(ax[q][1][0], ax[q][1][1], 0, 1, 2, 3, 4, 5, 6, 7);
      acc[q >= 8][0] = __builtin_amdgcn_mfma_f32_16x16x32_bf16(a0f, bx[q][0], acc[q >= 8][0], 0, 0, 0);
      acc[q >= 8][1] = __builtin_amdgcn_mfma_f32_16x16x32_bf16(a0f, bx[q][1], acc[q >= 8][1], 0, 0, 0);
      acc[q >= 8][1] = __builtin_amdgcn_mfma_f32_16x16x32_bf16(a1f, bx[q][0], acc[q >= 8][1], 0, 0, 0);
    }
  }
  finish(std::integral_constant<int, 0>{}, std::integral_constant<int, 2>{}, acc);
  CTS(5);
  WAIT_VM(0);
  __syncthreads();
  set_flag(fy, tid, ep);
  CTS(6);
  kv_fetch(true);
  WAIT_VM(0);
  __syncthreads();
  auto round = [&](auto u_c, s16x4 (&ah)[8][2], s16x8 (&bfr)[8], s16x4 (&ahn)[8][2], s16x8 (&bfn)[8], unsigned nb) {
    constexpr int u = decltype(u_c)::value;
    int k = 0;
#pragma unroll
    for (int s = 2; s <= 7; ++s)
#pragma unroll
      for (int m = 0; m <= s; ++m) {
        const s16x8 af = __builtin_shufflevector(ah[m][0], ah[m][1], 0, 1, 2, 3, 4, 5, 6, 7);
        acc[u][s - 2] = __builtin_amdgcn_mfma_f32_16x16x32_bf16(af, bfr[s - m], acc[u][s - 2], 0, 0, 0);
        if (k < 24) rd1(nb, k, ahn, bfn);
        ++k;
        __builtin_amdgcn_sched_barrier(0);
      }
    WAIT_LGKM0();
    __builtin_amdgcn_sched_barrier(0);
  };
#pragma unroll
  for (int i = 0; i < 24; ++i) rd1(own, i, a0, b0);
  WAIT_LGKM0();
  __builtin_amdgcn_sched_barrier(0);
#pragma unroll
  for (int i = 0; i < 14; ++i) acc[i / 7][i % 7] = (f32x4){0.f, 0.f, 0.f, 0.f};
#pragma unroll 1
  for (int r = 0; r < 8; r += 2) {
    round(std::integral_constant<int, 0>{}, a0, b0, a1, b1, own + 64 * r + 64);
    round(std::integral_constant<int, 0>{}, a1, b1, a0, b0, r < 6 ? own + 64 * r + 128 : shd);
    if (r == PFW_R) {
#pragma unroll
      for (int i = 0; i < 3; ++i) {
        const unsigned g = ci * 256 + tid + NCV * 256 * i;
        if (g < 114688) touch_pol((const char*)(g < 57344 ? w3 : w4) + (size_t)(g < 57344 ? g : g - 57344) * 128, lb + 163584);
      }
    }
  }
#pragma unroll 1
  for (int r = 0; r < 4; r += 2) {
    round(std::integral_constant<int, 1>{}, a0, b0, a1, b1, shd + 64 * r + 64);
    round(std::integral_constant<int, 1>{}, a1, b1, a0, b0, shd + 64 * r + 128);
  }
  CTS(11);
  finish(std::integral_constant<int, 2>{}, std::integral_constant<int, 6>{}, acc);
  CTS(7);
  WAIT_VM(0);
  __syncthreads();
  set_flag(fy + 256 * 128, tid, ep);
}
__device__ __forceinline__ float sum2_seg(float s, float q) {
  s += __builtin_bit_cast(float, __builtin_amdgcn_update_dpp(0, __builtin_bit_cast(int, s), 0x128, 0xf, 0xf, true));
  q += __builtin_bit_cast(float, __builtin_amdgcn_update_dpp(0, __builtin_bit_cast(int, q), 0x128, 0xf, 0xf, true));
  asm volatile("s_nop 1\n\tv_permlane16_swap_b32 %0, %1" : "+v"(s), "+v"(q));
  s += q;
  q = s;
  asm volatile("s_nop 1\n\tv_permlane32_swap_b32 %0, %1" : "+v"(s), "+v"(q));
  return s + q;
}
#define LASTSLEEP 8
__device__ __forceinline__ void wait_flags(const char* p, int lane, int n, unsigned ep) {
  gu32* f = (gu32*)(p + (lane < n ? lane : 0) * 128);
  for (int it = 0; it < 10000; ++it) {
    if (__all(__hip_atomic_load(f, __ATOMIC_RELAXED, __HIP_MEMORY_SCOPE_AGENT) == ep)) break;
    __builtin_amdgcn_s_sleep(LASTSLEEP);
  }
  asm volatile("" ::: "memory");
}
__device__ __forceinline__ void row0_q(const u16* __restrict__ q, int t, int lane, int wave, u32x4v (&xq)[4]) {
#pragma unroll
  for (int i = 0; i < 4; ++i) xq[i] = __builtin_nontemporal_load((const u32x4v*)(q + (size_t)(t * 8 + (lane & 7)) * D_ + wave * 256 + i * 64 + (lane >> 3) * 8));
}
__device__ __forceinline__ void row0_y(char* __restrict__ ws, int t, int tid, u32x4v (&yc)[6]) {
  const __amdgpu_buffer_rsrc_t rs = __builtin_amdgcn_make_buffer_rsrc(ws, 0, WS_USED, 0x00020000);
#pragma unroll
  for (int i = 0; i < 6; ++i) {
    const int x = tid + 256 * i;
    yc[i] = __builtin_bit_cast(u32x4v, __builtin_amdgcn_raw_buffer_load_b128(rs, t * (NCV * 128) + (x < NCV * 8 ? x : 0) * 16, OFF_Y, 16));
  }
}
__device__ __forceinline__ void row_stats(const u32x4v (&xq)[4], char* lds, int k, int lane, int wave, float& mean, float& var) {
  f32x2* red = (f32x2*)(lds + 16640);
  const int r8 = lane & 7, seg = lane >> 3;
  f32x2 s = {0.f, 0.f}, sq = {0.f, 0.f};
#pragma unroll
  for (int i = 0; i < 16; ++i) { const f32x2 x = up2(xq[i >> 2][i & 3]); s += x; sq = __builtin_elementwise_fma(x, x, sq); }
  const float a = sum2_seg(s[0] + s[1], sq[0] + sq[1]);
  if ((seg & 5) == 0) ((float*)red)[((k * 4 + wave) * 8 + r8) * 2 + (seg >> 1)] = a;
  __syncthreads();
  const f32x2 t = (red[(k * 4) * 8 + r8] + red[(k * 4 + 1) * 8 + r8]) + (red[(k * 4 + 2) * 8 + r8] + red[(k * 4 + 3) * 8 + r8]);
  mean = t[0] * (1.0f / 1024.0f);
  var = fmaxf(t[1] * (1.0f / 1024.0f) - mean * mean, 0.f);
}
__device__ __forceinline__ void row0_body(char* __restrict__ ws, char* lds, int t, int k, int lane, int wave, u32x4v (&xq)[4], const u32x4v (&yc)[6],
                                          float mean, float var) {
  const __amdgpu_buffer_rsrc_t rs = __builtin_amdgcn_make_buffer_rsrc(ws, 0, WS_USED, 0x00020000);
  const int r8 = lane & 7, seg = lane >> 3, tid = wave * 64 + lane, blk = t >> 2, qt = t & 3;
  const unsigned fo = blk * 65536 + (qt >> 1) * 32768 + (wave * 8 + (seg >> 2)) * 1024 + ((seg & 3) * 16 + (qt & 1) * 8 + r8) * 16;
  const bool odd = lane & 1;
#pragma unroll
  for (int i = 0; i < 6; ++i) {
    const int x = tid + 256 * i, c = (x >> 3) * 6 + (x & 6);
    const unsigned s0 = odd ? yc[i][0] : yc[i][2], s1 = odd ? yc[i][1] : yc[i][3];
    const unsigned r0 = __builtin_amdgcn_update_dpp(0, (int)s0, 0xb1, 0xf, 0xf, true), r1 = __builtin_amdgcn_update_dpp(0, (int)s1, 0xb1, 0xf, 0xf, true);
    const unsigned lo0 = odd ? r0 : yc[i][0], lo1 = odd ? r1 : yc[i][1], hi0 = odd ? yc[i][2] : r0, hi1 = odd ? yc[i][3] : r1;
    if (x < NCV * 8 && (x & 7) < 6 && c < D_) {
      char* p = lds + (odd ? 4 : 0) * 2064 + c * 2;
      *(unsigned*)(p) = __builtin_amdgcn_perm(hi0, lo0, 0x05040100u);
      *(unsigned*)(p + 2064) = __builtin_amdgcn_perm(hi0, lo0, 0x07060302u);
      *(unsigned*)(p + 2 * 2064) = __builtin_amdgcn_perm(hi1, lo1, 0x05040100u);
      *(unsigned*)(p + 3 * 2064) = __builtin_amdgcn_perm(hi1, lo1, 0x07060302u);
    }
  }
  __syncthreads();
  const float rd = __builtin_amdgcn_rsqf(var + 1e-5f);
  const f32x2 rd2 = {rd, rd}, nm2 = {-mean * rd, -mean * rd};
#pragma unroll
  for (int i = 0; i < 4; ++i) {
    const u32x4v y4 = *(const u32x4v*)(lds + r8 * 2064 + wave * 512 + i * 128 + seg * 16);
    __builtin_amdgcn_raw_buffer_store_b128(y4, rs, fo + i * 2048, OFF_YF, 17);
#pragma unroll
    for (int e = 0; e < 4; ++e) {
      const f32x2 x = up2(xq[i][e]);
      xq[i][e] = pk2(__builtin_elementwise_fma(__builtin_elementwise_fma(x, rd2, nm2), up2(y4[e]), x));
    }
    __builtin_amdgcn_raw_buffer_store_b128(xq[i], rs, fo + i * 2048, OFF_XP, 17);
  }
  row_stats(xq, lds, k, lane, wave, mean, var);
  if (wave == 0 && seg < 2) {
    const float a = var + 1e-5f, b = fmaf(1e-5f, a, var), rd1 = __builtin_amdgcn_rsqf(a), rdd = __builtin_amdgcn_rsqf(b);
    const float k1 = -2.0f * 0.7978845608028654f * 1.4426950408889634f;
    const f32x4 o = seg == 0 ? (f32x4){mean, k1 * rdd, k1 * 0.044715f * rdd * rdd * rdd, __builtin_amdgcn_sqrtf(b)} : (f32x4){-mean * rd1, rd1, 0.f, 0.f};
    __builtin_amdgcn_raw_buffer_store_b128(__builtin_bit_cast(u32x4v, o), rs, blk * 1024 + (qt * 8 + r8) * 32 + seg * 16, OFF_S1, 17);
  }
}
__device__ __forceinline__ void row0_flag(char* __restrict__ ws, int t, int lane, int wave, unsigned ep) {
  set_flag(ws + OFF_FXP + t * 512 + wave * 128, lane, ep);
}
__device__ __forceinline__ void row_last(char* __restrict__ ws, u16* __restrict__ out, int t, int lane, int wave, unsigned ep) {
  const __amdgpu_buffer_rsrc_t rs = __builtin_amdgcn_make_buffer_rsrc(ws, 0, WS_USED, 0x00020000);
  const int r8 = lane & 7, seg = lane >> 3, un = 128 + (t >> 1);
  const unsigned fo = un * 32768 + (wave * 8 + (seg >> 2)) * 1024 + ((seg & 3) * 16 + (t & 1) * 8 + r8) * 16;
  wait_flags(ws + OFF_FP + un * 2048, lane, 16, ep);
  u32x4v p0[4], p1[4];
#pragma unroll
  for (int i = 0; i < 4; ++i) {
    p0[i] = __builtin_bit_cast(u32x4v, __builtin_amdgcn_raw_buffer_load_b128(rs, fo + i * 2048, OFF_P0, 16));
    p1[i] = __builtin_bit_cast(u32x4v, __builtin_amdgcn_raw_buffer_load_b128(rs, fo + i * 2048, OFF_P1, 16));
  }
#pragma unroll
  for (int i = 0; i < 4; ++i) {
    u32x4v o;
#pragma unroll
    for (int e = 0; e < 4; ++e) o[e] = pk2(up2(p0[i][e]) + up2(p1[i][e]));
    st16(out + (size_t)(t * 8 + r8) * D_ + wave * 256 + i * 64 + seg * 8, __builtin_bit_cast(u16x8, o));
  }
}

__device__ __forceinline__ int swzB(int r) { return ((((r >> 1) & 1) | (((r >> 3) & 1) << 1)) << 1); }
#define TCH 1
constexpr int NRW = 18;
__global__ __launch_bounds__(256, 1) void flow_kernel(const u16* __restrict__ q, const u16* __restrict__ kin, const u16* __restrict__ vin,
                                                      const u16* __restrict__ shift_k, const u16* __restrict__ ssm_k, const u16* __restrict__ ssm_D,
                                                      const u16* __restrict__ w1, const u16* __restrict__ w2,
                                                      const u16* __restrict__ w3, const u16* __restrict__ w4, char* __restrict__ ws,
                                                      u16* __restrict__ out) {
  __shared__ __attribute__((aligned(16))) char lds[163840];
  const int tid = threadIdx.x, lane = tid & 63, wave = __builtin_amdgcn_readfirstlane(tid >> 6);
  const int b = blockIdx.x, xcd = b & 7, idx = b >> 3;
  const unsigned sLds = (unsigned)(size_t)lds;
  const int o8 = idx & 7;
  int kind, rep, slice, kh = 0;
  int j3 = 0, j4 = 0, shk = o8, shn = 8;
  if (idx < 8) { kind = 1 + (xcd & 1); rep = xcd >> 1; slice = o8; }
  else if (idx < 16 || (xcd >= 6 && idx < 24)) {
    const int h = idx < 16 ? xcd : 2 + xcd;
    kind = 4; rep = h >> 1; kh = h & 1; slice = o8; j4 = h * 8 + o8;
  } else if (idx < 30) {
    const int i = idx - 16;
    j3 = xcd < 6 ? xcd * 14 + i : 40 + xcd * 6 + i;
    kind = j3 < 90 ? 3 : j3 < 94 ? 5 : 6;
    if (xcd == 7) { slice = j3 < 94 ? 18 : j3 - 96 + NRW; rep = j3 < 94 ? j3 - 90 : 0; shk = 0; shn = 1; }
    else if (xcd == 6) { rep = 3 + (i >= 11); shk = i - 8 - 3 * (i >= 11); slice = (i >= 11 ? 5 : 15) + shk; shn = 3; }
    else if (i < 9) { rep = xcd >> 1; slice = (xcd & 1) * 9 + i; shk = i; shn = 9; }
    else { rep = 3 + (xcd >= 3); shk = i - 9; slice = ((0xd085a0 >> (4 * xcd)) & 15) + shk; shn = 5; }
  } else { kind = 6; rep = 0; slice = xcd * 2 + idx - 30; }
  CTS(0);
  {
    CODE_RANGE("Lpc%=", "Lcode_g12_b", c0, n0) CODE_RANGE("Lcode_r_e", "Lcode_g4_b", c7, n7)
    touch_lds(tid < 224 ? c0 + (tid * 128 < n0 ? tid * 128 : 0) : c7 + ((tid - 224) * 128 < n7 ? (tid - 224) * 128 : 0), sLds + 163584);
  }
  const unsigned sScr = sLds + 163584;
  auto code_touch = [&]() {
    CODE_RANGE("Lcode_g12_b", "Lcode_g2_b", c1, n1) CODE_RANGE("Lcode_g2_b", "Lcode_g12_e", c2, n2) CODE_RANGE("Lcode_g3_b", "Lcode_g3_e", c3, n3)
    CODE_RANGE("Lcode_g3o_b", "Lcode_g3o_e", c5, n5) CODE_RANGE("Lcode_g4_b", "Lcode_g4_e", c4, n4) CODE_RANGE("Lcode_r_b", "Lcode_r_e", c6, n6)
    const char* cp = kind == 1 ? c1 : kind == 2 ? c2 : kind == 3 ? c3 : kind == 4 ? c4 : kind == 5 ? c5 : c6;
    const unsigned cn = kind == 1 ? n1 : kind == 2 ? n2 : kind == 3 ? n3 : kind == 4 ? n4 : kind == 5 ? n5 : n6;
    touch_lds(cp + (tid * 128 < cn ? tid * 128 : 0), sScr);
    touch_lds(cp + (32768 + tid * 128 < cn ? 32768 + tid * 128 : 0), sScr);
  };
  unsigned ep = 0;
  if (kind == 1) code_touch();
  else {
  asm volatile("global_load_dword %0, %1, %2 sc1" : "=v"(ep) : "v"(0u), "s"(ws + OFF_EP) : "memory");
  const int ci = kind == 4 ? j4 : 80 + j3;
  const bool isconv = kind >= 3 && kind <= 5 && ci < NCV;
  const int tile = idx >= 8 ? (idx - 8) * 8 + xcd : 192 + (xcd >> 1) * 8 + idx;
  KvRegs KR;
  HRegs HR;
  kv_load(KR, kin, vin, shift_k, tile, tid);
  const bool helper = kind == 6 || (kind == 5 && !isconv);
  const int rw = kind == 6 ? slice : j3 - 73;
  const int tq0 = kind == 2 ? rep * 8 + slice : kind == 6 ? 35 + slice : 14 + rw;
  h_load(HR, ssm_k, ssm_D, isconv ? ci : 0, tid);
  code_touch();
  kv_wait<15>(KR);
  CTS(12);
  kv_store(KR, ws, lds + CV_STG, tile, tid);
  CTS(1);
  asm volatile("s_waitcnt vmcnt(4)" : "+v"(HR.hv0), "+v"(HR.hv1), "+v"(HR.hv2), "+v"(HR.hv3), "+v"(HR.hv4), "+v"(HR.hv5), "+v"(HR.hn0), "+v"(HR.hn1),
               "+v"(HR.hn2), "+v"(HR.hn3), "+v"(HR.hn4), "+v"(HR.hn5), "+v"(HR.d0), "+v"(ep) :: "memory");
  if (isconv) h_spread(HR, lds, tid);
  WAIT_VM(0);
  ep = __builtin_amdgcn_readfirstlane(ep) + 1u;
  if (ep == 0u) ep = 1u;
  __syncthreads();
  set_flag(ws + OFF_FKV + tile * 128, tid, ep);
  CTS(2);
  u32x4v qa[4], qb[4], ya[6], yb[6];
  if (!isconv) row0_q(q, tq0, lane, wave, qa);
  if (kind == 2) {
    kv_load(KR, kin, vin, shift_k, 224 + rep * 8 + slice, tid);
    kv_wait<0>(KR);
    kv_store(KR, ws, lds + CV_STG, 224 + rep * 8 + slice, tid);
    WAIT_VM(0);
    __syncthreads();
    set_flag(ws + OFF_FKV + (224 + rep * 8 + slice) * 128, tid, ep);
    CTS(3);
  }
  if (helper) {
    const int pw = rw * 256 + tid;
#pragma unroll
    for (int i = 0; i < 4; ++i) {
      const unsigned g = pw + 21 * 256 * i;
      if (g < 16384) touch_lds((const char*)w2 + (size_t)g * 128, sScr);
    }
  }
  if (isconv) {
    h_patch(HR, lds, tid);
    conv_part(ws, HR, lds, ci, tid, lane, wave, ep, w3, w4);
  }
  CTS(8);
  char* rl = lds + 144384;
  auto wait_y = [&](int g) {
    if (wave == 0) poll_flags<3>(ws + OFF_FY + g * 256 * 128, NCV, lane, ep);
    __syncthreads();
  };
  auto two = [&](int ta, int tb, bool have_qa, auto&& wait) {
    float ma, va, mb = 0.f, vb_ = 0.f;
    if (!have_qa) row0_q(q, ta, lane, wave, qa);
    if (tb >= 0) row0_q(q, tb, lane, wave, qb);
    row_stats(qa, rl, 0, lane, wave, ma, va);
    if (tb >= 0) row_stats(qb, rl, 1, lane, wave, mb, vb_);
    wait();
    row0_y(ws, ta, tid, ya);
    if (tb >= 0) row0_y(ws, tb, tid, yb);
    row0_body(ws, rl, ta, 0, lane, wave, qa, ya, ma, va);
    if (tb >= 0) row0_body(ws, rl, tb, 1, lane, wave, qb, yb, mb, vb_);
    WAIT_VM(0);
    row0_flag(ws, ta, lane, wave, ep);
    if (tb >= 0) row0_flag(ws, tb, lane, wave, ep);
    __syncthreads();
  };
  if (kind == 4) two(64 + j4, -1, false, [&]() { wait_y(1); CTS(13); });
  else if (kind == 6) {
    two(tq0, slice < 11 ? 53 + slice : -1, true, [&]() { wait_y(0); CTS(13); });
    bool first = true;
    for (int t = 144 + slice; t < 256; t += 36) {
      two(t, t + 18 < 256 ? t + 18 : -1, false, [&]() { if (first) wait_y(1); });
      first = false;
    }
  } else if (!isconv) two(tq0, -1, true, [&]() { wait_y(0); CTS(13); });
  WAIT_VM(0);
  __syncthreads();
  CTS(10);
  }
  const unsigned sDbg0 = 110u * 1048576u + (b * 4 + wave) * 8192;
  constexpr unsigned OOB = 0x80000000u;
#define LANE_ID(x) int x; asm volatile("v_mbcnt_lo_u32_b32 %0, -1, 0\n\tv_mbcnt_hi_u32_b32 %0, -1, %0" : "=v"(x))
#define COMMON(NF, FOFF, FSTR, SNAP, LDW, NL, TOFF)                                                                                            \
  LANE_ID(ln);                                                                                                                       \
  const int l15 = ln & 15, lg = ln >> 4, lq = l15 >> 2, lp = ln & 3, rr = 8 * lg + lq, fj = ln & 31;                                 \
  const unsigned vL16 = ln * 16, vFlag = (ln < 32 && wave == 0) ? ln * 4 : OOB;                                                      \
  const unsigned vSnapOff = (wave == 0 && fj < (NF)) ? (FOFF) + (ln >> 5) * sStep * (FSTR) : OOB;                                    \
  const unsigned vSnapRd = (SNAP) + (fj < (NF) ? ln : ln & 32) * 4;                                                                  \
  const unsigned vWd0 = (ln >> 3) * (LDW) + ((ln & 7) ^ swzB(ln >> 3)) * 16, vWd1 = ((ln >> 3) + 8) * (LDW) + ((ln & 7) ^ swzB(8 + (ln >> 3))) * 16; \
  const unsigned vWr0 = (wave >> 1) * 8192 + rr * 128 + (((wave & 1) * 4 + lp) ^ swzB(rr)) * 16, vWr1 = vWr0 + 8;                    \
  const unsigned vWr2 = 16384 + rr * 128 + ((2 * wave + (lp >> 1)) ^ swzB(rr)) * 16 + (lp & 1) * 8;                                  \
  const unsigned sWave = wave, sRk = shk * 8 / shn, sG0 = rep, sDbg = sDbg0
  if (kind == 3 || kind == 5) {
    const unsigned sStep = kind == 3 ? 5 : 4;
    COMMON(8, fj * 128, 1024, 131072, 7168, 256, 0);
    const unsigned vOutS = (lg & 1) ? OOB : (((wave & 1) * 2 + (lg >> 1)) * 16 + l15) * 16 + (4 + (wave >> 1) - wave) * 1024;
    const unsigned vAux0 = (ln & 7) * 128 + (4 * wave + lg) * 8 + ((ln >> 3) & 1) * 4, vAux1 = 139264 + (l15 == 0 ? (4 * wave + lg) * 16 : 512 + ln * 16);
    const unsigned vAux2 = 139264 + l15 * 16, vAux3 = 0, vAn = 0, vAe = 0;
    const unsigned sOutOff = OFF_G + (slice * 6 + wave) * 1024, sFoutOff = OFF_FG + slice * 128, sAuxOff = 0, sWw = 16 * wave * 7168 + slice * 384, sBuf = 0;
    if (kind == 3) { asm volatile(
R"(.macro M d,w,x
v_mfma_f32_16x16x32_bf16 v[\d:\d+3], \w, v[\x:\x+3], v[\d:\d+3]
.endm
.macro R d,a,o
ds_read_b128 v[\d:\d+3], v\a offset:\o
.endm
.macro T d,a,o
ds_read_b64_tr_b16 \d, v\a offset:\o
.endm
.macro D a,s,o
buffer_load_dwordx4 v\a, s[36:39], s\s offen offset:\o sc1 lds
.endm
.macro L c
s_waitcnt lgkmcnt(\c)
.endm
Lcode_g3_b:
s_mov_b64 s[36:37],%0
s_mov_b32 s38,111243392
s_mov_b32 s39,0x00020000
s_mov_b32 s44,%1
s_mov_b32 s45,%2
s_mov_b32 s46,%3
s_mov_b32 s47,%4
s_mov_b32 s48,%5
s_mov_b32 s49,%6
s_mov_b32 s50,%7
s_mov_b32 s51,%8
s_mov_b32 s93,%9
v_mov_b32 v0,%10
v_mov_b32 v3,%11
v_mov_b32 v5,%12
v_mov_b32 v6,%13
v_mov_b32 v18,%14
v_mov_b32 v19,%15
v_mov_b32 v20,%16
v_mov_b32 v21,%17
v_mov_b32 v22,%18
v_mov_b32 v10,%19
v_mov_b32 v11,%20
v_mov_b32 v12,%21
v_mov_b32 v13,%22
v_mov_b32 v14,%23
v_mov_b32 v15,%24
v_mov_b32 v16,%25
s_mov_b64 s[40:41],%26
s_mov_b32 s42,7340032
s_mov_b32 s43,0x00020000
s_lshl_b32 s57,s44,11
s_mov_b32 s79,111243264
buffer_load_dword v4,off,s[36:39],s79 sc1
s_add_u32 s55,s50,0
s_add_u32 m0,s57,0
s_nop 0
buffer_load_dwordx4 v18,s[40:43],s55 offen offset:0  lds
s_add_u32 m0,s57,1024
s_nop 0
buffer_load_dwordx4 v19,s[40:43],s55 offen offset:0  lds
s_add_u32 m0,s57,8064
s_nop 0
buffer_load_dwordx4 v18,s[40:43],s55 offen offset:128  lds
s_add_u32 m0,s57,9088
s_nop 0
buffer_load_dwordx4 v19,s[40:43],s55 offen offset:128  lds
s_add_u32 m0,s57,16128
s_nop 0
buffer_load_dwordx4 v18,s[40:43],s55 offen offset:256  lds
s_add_u32 m0,s57,17152
s_nop 0
buffer_load_dwordx4 v19,s[40:43],s55 offen offset:256  lds
s_add_u32 s55,s50,458752
s_add_u32 m0,s57,24576
s_nop 0
buffer_load_dwordx4 v18,s[40:43],s55 offen offset:0  lds
s_add_u32 m0,s57,25600
s_nop 0
buffer_load_dwordx4 v19,s[40:43],s55 offen offset:0  lds
s_add_u32 m0,s57,32640
s_nop 0
buffer_load_dwordx4 v18,s[40:43],s55 offen offset:128  lds
s_add_u32 m0,s57,33664
s_nop 0
buffer_load_dwordx4 v19,s[40:43],s55 offen offset:128  lds
s_add_u32 m0,s57,40704
s_nop 0
buffer_load_dwordx4 v18,s[40:43],s55 offen offset:256  lds
s_add_u32 m0,s57,41728
s_nop 0
buffer_load_dwordx4 v19,s[40:43],s55 offen offset:256  lds
s_add_u32 s55,s50,917504
s_add_u32 m0,s57,49152
s_nop 0
buffer_load_dwordx4 v18,s[40:43],s55 offen offset:0  lds
s_add_u32 m0,s57,50176
s_nop 0
buffer_load_dwordx4 v19,s[40:43],s55 offen offset:0  lds
s_add_u32 m0,s57,57216
s_nop 0
buffer_load_dwordx4 v18,s[40:43],s55 offen offset:128  lds
s_add_u32 m0,s57,58240
s_nop 0
buffer_load_dwordx4 v19,s[40:43],s55 offen offset:128  lds
s_add_u32 m0,s57,65280
s_nop 0
buffer_load_dwordx4 v18,s[40:43],s55 offen offset:256  lds
s_add_u32 m0,s57,66304
s_nop 0
buffer_load_dwordx4 v19,s[40:43],s55 offen offset:256  lds
s_add_u32 s55,s50,1376256
s_add_u32 m0,s57,73728
s_nop 0
buffer_load_dwordx4 v18,s[40:43],s55 offen offset:0  lds
s_add_u32 m0,s57,74752
s_nop 0
buffer_load_dwordx4 v19,s[40:43],s55 offen offset:0  lds
s_add_u32 m0,s57,81792
s_nop 0
buffer_load_dwordx4 v18,s[40:43],s55 offen offset:128  lds
s_add_u32 m0,s57,82816
s_nop 0
buffer_load_dwordx4 v19,s[40:43],s55 offen offset:128  lds
s_add_u32 m0,s57,89856
s_nop 0
buffer_load_dwordx4 v18,s[40:43],s55 offen offset:256  lds
s_add_u32 m0,s57,90880
s_nop 0
buffer_load_dwordx4 v19,s[40:43],s55 offen offset:256  lds
s_add_u32 s55,s50,1835008
s_add_u32 m0,s57,98304
s_nop 0
buffer_load_dwordx4 v18,s[40:43],s55 offen offset:0  lds
s_add_u32 m0,s57,99328
s_nop 0
buffer_load_dwordx4 v19,s[40:43],s55 offen offset:0  lds
s_add_u32 m0,s57,106368
s_nop 0
buffer_load_dwordx4 v18,s[40:43],s55 offen offset:128  lds
s_add_u32 m0,s57,107392
s_nop 0
buffer_load_dwordx4 v19,s[40:43],s55 offen offset:128  lds
s_add_u32 m0,s57,114432
s_nop 0
buffer_load_dwordx4 v18,s[40:43],s55 offen offset:256  lds
s_add_u32 m0,s57,115456
s_nop 0
buffer_load_dwordx4 v19,s[40:43],s55 offen offset:256  lds
s_waitcnt vmcnt(24)
L 0
s_barrier
s_add_u32 s55,s50,2293760
s_add_u32 m0,s57,122880
s_nop 0
buffer_load_dwordx4 v18,s[40:43],s55 offen offset:0  lds
s_add_u32 m0,s57,123904
s_nop 0
buffer_load_dwordx4 v19,s[40:43],s55 offen offset:0  lds
s_add_u32 m0,s57,130944
s_nop 0
buffer_load_dwordx4 v18,s[40:43],s55 offen offset:128  lds
s_add_u32 m0,s57,131968
s_nop 0
buffer_load_dwordx4 v19,s[40:43],s55 offen offset:128  lds
s_add_u32 m0,s57,139008
s_nop 0
buffer_load_dwordx4 v18,s[40:43],s55 offen offset:256  lds
s_add_u32 m0,s57,140032
s_nop 0
buffer_load_dwordx4 v19,s[40:43],s55 offen offset:256  lds
v_add_u32 v26,0,v20
v_add_u32 v27,0,v21
v_add_u32 v28,0,v22
T a[0:1],26,0
T a[2:3],26,512
T a[4:5],27,0
T a[6:7],27,512
T a[8:9],28,0
T a[10:11],28,512
T a[12:13],26,4096
T a[14:15],26,4608
T a[16:17],27,4096
T a[18:19],27,4608
T a[20:21],28,4096
T a[22:23],28,4608
s_waitcnt vmcnt(24)
L 0
s_barrier
s_add_u32 s55,s50,2752512
s_add_u32 m0,s57,0
s_nop 0
buffer_load_dwordx4 v18,s[40:43],s55 offen offset:0  lds
s_add_u32 m0,s57,1024
s_nop 0
buffer_load_dwordx4 v19,s[40:43],s55 offen offset:0  lds
s_add_u32 m0,s57,8064
s_nop 0
buffer_load_dwordx4 v18,s[40:43],s55 offen offset:128  lds
s_add_u32 m0,s57,9088
s_nop 0
buffer_load_dwordx4 v19,s[40:43],s55 offen offset:128  lds
s_add_u32 m0,s57,16128
s_nop 0
buffer_load_dwordx4 v18,s[40:43],s55 offen offset:256  lds
s_add_u32 m0,s57,17152
s_nop 0
buffer_load_dwordx4 v19,s[40:43],s55 offen offset:256  lds
v_add_u32 v26,24576,v20
v_add_u32 v27,24576,v21
v_add_u32 v28,24576,v22
T a[24:25],26,0
T a[26:27],26,512
T a[28:29],27,0
T a[30:31],27,512
T a[32:33],28,0
T a[34:35],28,512
T a[36:37],26,4096
T a[38:39],26,4608
T a[40:41],27,4096
T a[42:43],27,4608
T a[44:45],28,4096
T a[46:47],28,4608
s_waitcnt vmcnt(24)
L 0
s_barrier
s_add_u32 s55,s50,3211264
s_add_u32 m0,s57,24576
s_nop 0
buffer_load_dwordx4 v18,s[40:43],s55 offen offset:0  lds
s_add_u32 m0,s57,25600
s_nop 0
buffer_load_dwordx4 v19,s[40:43],s55 offen offset:0  lds
s_add_u32 m0,s57,32640
s_nop 0
buffer_load_dwordx4 v18,s[40:43],s55 offen offset:128  lds
s_add_u32 m0,s57,33664
s_nop 0
buffer_load_dwordx4 v19,s[40:43],s55 offen offset:128  lds
s_add_u32 m0,s57,40704
s_nop 0
buffer_load_dwordx4 v18,s[40:43],s55 offen offset:256  lds
s_add_u32 m0,s57,41728
s_nop 0
buffer_load_dwordx4 v19,s[40:43],s55 offen offset:256  lds
v_add_u32 v26,49152,v20
v_add_u32 v27,49152,v21
v_add_u32 v28,49152,v22
T a[48:49],26,0
T a[50:51],26,512
T a[52:53],27,0
T a[54:55],27,512
T a[56:57],28,0
T a[58:59],28,512
T a[60:61],26,4096
T a[62:63],26,4608
T a[64:65],27,4096
T a[66:67],27,4608
T a[68:69],28,4096
T a[70:71],28,4608
s_waitcnt vmcnt(24)
L 0
s_barrier
s_add_u32 s55,s50,3670016
s_add_u32 m0,s57,49152
s_nop 0
buffer_load_dwordx4 v18,s[40:43],s55 offen offset:0  lds
s_add_u32 m0,s57,50176
s_nop 0
buffer_load_dwordx4 v19,s[40:43],s55 offen offset:0  lds
s_add_u32 m0,s57,57216
s_nop 0
buffer_load_dwordx4 v18,s[40:43],s55 offen offset:128  lds
s_add_u32 m0,s57,58240
s_nop 0
buffer_load_dwordx4 v19,s[40:43],s55 offen offset:128  lds
s_add_u32 m0,s57,65280
s_nop 0
buffer_load_dwordx4 v18,s[40:43],s55 offen offset:256  lds
s_add_u32 m0,s57,66304
s_nop 0
buffer_load_dwordx4 v19,s[40:43],s55 offen offset:256  lds
v_add_u32 v26,73728,v20
v_add_u32 v27,73728,v21
v_add_u32 v28,73728,v22
T a[72:73],26,0
T a[74:75],26,512
T a[76:77],27,0
T a[78:79],27,512
T a[80:81],28,0
T a[82:83],28,512
T a[84:85],26,4096
T a[86:87],26,4608
T a[88:89],27,4096
T a[90:91],27,4608
T a[92:93],28,4096
T a[94:95],28,4608
s_waitcnt vmcnt(24)
L 0
s_barrier
s_add_u32 s55,s50,4128768
s_add_u32 m0,s57,73728
s_nop 0
buffer_load_dwordx4 v18,s[40:43],s55 offen offset:0  lds
s_add_u32 m0,s57,74752
s_nop 0
buffer_load_dwordx4 v19,s[40:43],s55 offen offset:0  lds
s_add_u32 m0,s57,81792
s_nop 0
buffer_load_dwordx4 v18,s[40:43],s55 offen offset:128  lds
s_add_u32 m0,s57,82816
s_nop 0
buffer_load_dwordx4 v19,s[40:43],s55 offen offset:128  lds
s_add_u32 m0,s57,89856
s_nop 0
buffer_load_dwordx4 v18,s[40:43],s55 offen offset:256  lds
s_add_u32 m0,s57,90880
s_nop 0
buffer_load_dwordx4 v19,s[40:43],s55 offen offset:256  lds
v_add_u32 v26,98304,v20
v_add_u32 v27,98304,v21
v_add_u32 v28,98304,v22
T a[96:97],26,0
T a[98:99],26,512
T a[100:101],27,0
T a[102:103],27,512
T a[104:105],28,0
T a[106:107],28,512
T a[108:109],26,4096
T a[110:111],26,4608
T a[112:113],27,4096
T a[114:115],27,4608
T a[116:117],28,4096
T a[118:119],28,4608
s_waitcnt vmcnt(24)
L 0
s_barrier
s_add_u32 s55,s50,4587520
s_add_u32 m0,s57,98304
s_nop 0
buffer_load_dwordx4 v18,s[40:43],s55 offen offset:0  lds
s_add_u32 m0,s57,99328
s_nop 0
buffer_load_dwordx4 v19,s[40:43],s55 offen offset:0  lds
s_add_u32 m0,s57,106368
s_nop 0
buffer_load_dwordx4 v18,s[40:43],s55 offen offset:128  lds
s_add_u32 m0,s57,107392
s_nop 0
buffer_load_dwordx4 v19,s[40:43],s55 offen offset:128  lds
s_add_u32 m0,s57,114432
s_nop 0
buffer_load_dwordx4 v18,s[40:43],s55 offen offset:256  lds
s_add_u32 m0,s57,115456
s_nop 0
buffer_load_dwordx4 v19,s[40:43],s55 offen offset:256  lds
v_add_u32 v26,122880,v20
v_add_u32 v27,122880,v21
v_add_u32 v28,122880,v22
T a[120:121],26,0
T a[122:123],26,512
T a[124:125],27,0
T a[126:127],27,512
T a[128:129],28,0
T a[130:131],28,512
T a[132:133],26,4096
T a[134:135],26,4608
T a[136:137],27,4096
T a[138:139],27,4608
T a[140:141],28,4096
T a[142:143],28,4608
s_waitcnt vmcnt(24)
L 0
s_barrier
s_add_u32 s55,s50,5046272
s_add_u32 m0,s57,122880
s_nop 0
buffer_load_dwordx4 v18,s[40:43],s55 offen offset:0  lds
s_add_u32 m0,s57,123904
s_nop 0
buffer_load_dwordx4 v19,s[40:43],s55 offen offset:0  lds
s_add_u32 m0,s57,130944
s_nop 0
buffer_load_dwordx4 v18,s[40:43],s55 offen offset:128  lds
s_add_u32 m0,s57,131968
s_nop 0
buffer_load_dwordx4 v19,s[40:43],s55 offen offset:128  lds
s_add_u32 m0,s57,139008
s_nop 0
buffer_load_dwordx4 v18,s[40:43],s55 offen offset:256  lds
s_add_u32 m0,s57,140032
s_nop 0
buffer_load_dwordx4 v19,s[40:43],s55 offen offset:256  lds
v_add_u32 v26,0,v20
v_add_u32 v27,0,v21
v_add_u32 v28,0,v22
T a[144:145],26,0
T a[146:147],26,512
T a[148:149],27,0
T a[150:151],27,512
T a[152:153],28,0
T a[154:155],28,512
T a[156:157],26,4096
T a[158:159],26,4608
T a[160:161],27,4096
T a[162:163],27,4608
T a[164:165],28,4096
T a[166:167],28,4608
s_waitcnt vmcnt(24)
L 0
s_barrier
s_add_u32 s55,s50,5505024
s_add_u32 m0,s57,0
s_nop 0
buffer_load_dwordx4 v18,s[40:43],s55 offen offset:0  lds
s_add_u32 m0,s57,1024
s_nop 0
buffer_load_dwordx4 v19,s[40:43],s55 offen offset:0  lds
s_add_u32 m0,s57,8064
s_nop 0
buffer_load_dwordx4 v18,s[40:43],s55 offen offset:128  lds
s_add_u32 m0,s57,9088
s_nop 0
buffer_load_dwordx4 v19,s[40:43],s55 offen offset:128  lds
s_add_u32 m0,s57,16128
s_nop 0
buffer_load_dwordx4 v18,s[40:43],s55 offen offset:256  lds
s_add_u32 m0,s57,17152
s_nop 0
buffer_load_dwordx4 v19,s[40:43],s55 offen offset:256  lds
v_add_u32 v26,24576,v20
v_add_u32 v27,24576,v21
v_add_u32 v28,24576,v22
T a[168:169],26,0
T a[170:171],26,512
T a[172:173],27,0
T a[174:175],27,512
T a[176:177],28,0
T a[178:179],28,512
T a[180:181],26,4096
T a[182:183],26,4608
T a[184:185],27,4096
T a[186:187],27,4608
T a[188:189],28,4096
T a[190:191],28,4608
s_waitcnt vmcnt(24)
L 0
s_barrier
s_add_u32 s55,s50,5963776
s_add_u32 m0,s57,24576
s_nop 0
buffer_load_dwordx4 v18,s[40:43],s55 offen offset:0  lds
s_add_u32 m0,s57,25600
s_nop 0
buffer_load_dwordx4 v19,s[40:43],s55 offen offset:0  lds
s_add_u32 m0,s57,32640
s_nop 0
buffer_load_dwordx4 v18,s[40:43],s55 offen offset:128  lds
s_add_u32 m0,s57,33664
s_nop 0
buffer_load_dwordx4 v19,s[40:43],s55 offen offset:128  lds
s_add_u32 m0,s57,40704
s_nop 0
buffer_load_dwordx4 v18,s[40:43],s55 offen offset:256  lds
s_add_u32 m0,s57,41728
s_nop 0
buffer_load_dwordx4 v19,s[40:43],s55 offen offset:256  lds
v_add_u32 v26,49152,v20
v_add_u32 v27,49152,v21
v_add_u32 v28,49152,v22
T a[192:193],26,0
T a[194:195],26,512
T a[196:197],27,0
T a[198:199],27,512
T a[200:201],28,0
T a[202:203],28,512
T a[204:205],26,4096
T a[206:207],26,4608
T a[208:209],27,4096
T a[210:211],27,4608
T a[212:213],28,4096
T a[214:215],28,4608
s_waitcnt vmcnt(24)
L 0
s_barrier
s_add_u32 s55,s50,6422528
s_add_u32 m0,s57,49152
s_nop 0
buffer_load_dwordx4 v18,s[40:43],s55 offen offset:0  lds
s_add_u32 m0,s57,50176
s_nop 0
buffer_load_dwordx4 v19,s[40:43],s55 offen offset:0  lds
s_add_u32 m0,s57,57216
s_nop 0
buffer_load_dwordx4 v18,s[40:43],s55 offen offset:128  lds
s_add_u32 m0,s57,58240
s_nop 0
buffer_load_dwordx4 v19,s[40:43],s55 offen offset:128  lds
s_add_u32 m0,s57,65280
s_nop 0
buffer_load_dwordx4 v18,s[40:43],s55 offen offset:256  lds
s_add_u32 m0,s57,66304
s_nop 0
buffer_load_dwordx4 v19,s[40:43],s55 offen offset:256  lds
)"
R"(v_add_u32 v26,73728,v20
v_add_u32 v27,73728,v21
v_add_u32 v28,73728,v22
T a[216:217],26,0
T a[218:219],26,512
T a[220:221],27,0
T a[222:223],27,512
T a[224:225],28,0
T a[226:227],28,512
T a[228:229],26,4096
T a[230:231],26,4608
T a[232:233],27,4096
T a[234:235],27,4608
T a[236:237],28,4096
T a[238:239],28,4608
s_waitcnt vmcnt(24)
L 0
s_barrier
s_add_u32 s55,s50,6881280
s_add_u32 m0,s57,73728
s_nop 0
buffer_load_dwordx4 v18,s[40:43],s55 offen offset:0  lds
s_add_u32 m0,s57,74752
s_nop 0
buffer_load_dwordx4 v19,s[40:43],s55 offen offset:0  lds
s_add_u32 m0,s57,81792
s_nop 0
buffer_load_dwordx4 v18,s[40:43],s55 offen offset:128  lds
s_add_u32 m0,s57,82816
s_nop 0
buffer_load_dwordx4 v19,s[40:43],s55 offen offset:128  lds
s_add_u32 m0,s57,89856
s_nop 0
buffer_load_dwordx4 v18,s[40:43],s55 offen offset:256  lds
s_add_u32 m0,s57,90880
s_nop 0
buffer_load_dwordx4 v19,s[40:43],s55 offen offset:256  lds
v_add_u32 v26,98304,v20
v_add_u32 v27,98304,v21
v_add_u32 v28,98304,v22
T a[240:241],26,0
T a[242:243],26,512
T a[244:245],27,0
T a[246:247],27,512
T a[248:249],28,0
T a[250:251],28,512
T a[252:253],26,4096
T a[254:255],26,4608
T v[128:129],27,4096
T v[130:131],27,4608
T v[132:133],28,4096
T v[134:135],28,4608
s_waitcnt vmcnt(24)
L 0
s_barrier
v_add_u32 v26,122880,v20
v_add_u32 v27,122880,v21
v_add_u32 v28,122880,v22
T v[136:137],26,0
T v[138:139],26,512
T v[140:141],27,0
T v[142:143],27,512
T v[144:145],28,0
T v[146:147],28,512
T v[148:149],26,4096
T v[150:151],26,4608
T v[152:153],27,4096
T v[154:155],27,4608
T v[156:157],28,4096
T v[158:159],28,4608
s_waitcnt vmcnt(18)
L 0
s_barrier
v_add_u32 v26,0,v20
v_add_u32 v27,0,v21
v_add_u32 v28,0,v22
T v[160:161],26,0
T v[162:163],26,512
T v[164:165],27,0
T v[166:167],27,512
T v[168:169],28,0
T v[170:171],28,512
T v[172:173],26,4096
T v[174:175],26,4608
T v[176:177],27,4096
T v[178:179],27,4608
T v[180:181],28,4096
T v[182:183],28,4608
s_waitcnt vmcnt(12)
L 0
s_barrier
v_add_u32 v26,24576,v20
v_add_u32 v27,24576,v21
v_add_u32 v28,24576,v22
T v[184:185],26,0
T v[186:187],26,512
T v[188:189],27,0
T v[190:191],27,512
T v[192:193],28,0
T v[194:195],28,512
T v[196:197],26,4096
T v[198:199],26,4608
T v[200:201],27,4096
T v[202:203],27,4608
T v[204:205],28,4096
T v[206:207],28,4608
s_waitcnt vmcnt(6)
L 0
s_barrier
v_add_u32 v26,49152,v20
v_add_u32 v27,49152,v21
v_add_u32 v28,49152,v22
T v[208:209],26,0
T v[210:211],26,512
T v[212:213],27,0
T v[214:215],27,512
T v[216:217],28,0
T v[218:219],28,512
T v[220:221],26,4096
T v[222:223],26,4608
T v[224:225],27,4096
T v[226:227],27,4608
T v[228:229],28,4096
T v[230:231],28,4608
s_waitcnt vmcnt(0)
L 0
s_barrier
v_add_u32 v26,73728,v20
v_add_u32 v27,73728,v21
v_add_u32 v28,73728,v22
T v[232:233],26,0
T v[234:235],26,512
T v[236:237],27,0
T v[238:239],27,512
T v[240:241],28,0
T v[242:243],28,512
T v[244:245],26,4096
T v[246:247],26,4608
T v[248:249],27,4096
T v[250:251],27,4608
T v[252:253],28,4096
T v[254:255],28,4608
L 0
s_barrier
v_readfirstlane_b32 s53,v4
s_nop 3
s_add_u32 s53,s53,1
s_cmp_eq_u32 s53,0
s_cselect_b32 s53,1,s53
v_mov_b32 v42,0x3f803f80
v_mov_b32 v43,0x3f803f80
v_mov_b32 v44,0x3f803f80
v_mov_b32 v45,0x3f803f80
s_nop 1
v_mfma_f32_16x16x32_bf16 v[82:85],a[0:3],v[42:45],0
v_mfma_f32_16x16x32_bf16 v[86:89],a[4:7],v[42:45],0
v_mfma_f32_16x16x32_bf16 v[90:93],a[8:11],v[42:45],0
M 82,a[12:15],42
M 86,a[16:19],42
M 90,a[20:23],42
M 82,a[24:27],42
M 86,a[28:31],42
M 90,a[32:35],42
M 82,a[36:39],42
M 86,a[40:43],42
M 90,a[44:47],42
M 82,a[48:51],42
M 86,a[52:55],42
M 90,a[56:59],42
M 82,a[60:63],42
M 86,a[64:67],42
M 90,a[68:71],42
M 82,a[72:75],42
M 86,a[76:79],42
M 90,a[80:83],42
M 82,a[84:87],42
M 86,a[88:91],42
M 90,a[92:95],42
M 82,a[96:99],42
M 86,a[100:103],42
M 90,a[104:107],42
M 82,a[108:111],42
M 86,a[112:115],42
M 90,a[116:119],42
M 82,a[120:123],42
M 86,a[124:127],42
M 90,a[128:131],42
M 82,a[132:135],42
M 86,a[136:139],42
M 90,a[140:143],42
M 82,a[144:147],42
M 86,a[148:151],42
M 90,a[152:155],42
M 82,a[156:159],42
M 86,a[160:163],42
M 90,a[164:167],42
M 82,a[168:171],42
M 86,a[172:175],42
M 90,a[176:179],42
M 82,a[180:183],42
M 86,a[184:187],42
M 90,a[188:191],42
M 82,a[192:195],42
M 86,a[196:199],42
M 90,a[200:203],42
M 82,a[204:207],42
M 86,a[208:211],42
M 90,a[212:215],42
M 82,a[216:219],42
M 86,a[220:223],42
M 90,a[224:227],42
M 82,a[228:231],42
M 86,a[232:235],42
M 90,a[236:239],42
M 82,a[240:243],42
M 86,a[244:247],42
M 90,a[248:251],42
M 82,a[252:255],42
M 86,v[128:131],42
M 90,v[132:135],42
M 82,v[136:139],42
M 86,v[140:143],42
M 90,v[144:147],42
M 82,v[148:151],42
M 86,v[152:155],42
M 90,v[156:159],42
M 82,v[160:163],42
M 86,v[164:167],42
M 90,v[168:171],42
M 82,v[172:175],42
M 86,v[176:179],42
M 90,v[180:183],42
M 82,v[184:187],42
M 86,v[188:191],42
M 90,v[192:195],42
M 82,v[196:199],42
M 86,v[200:203],42
M 90,v[204:207],42
M 82,v[208:211],42
M 86,v[212:215],42
M 90,v[216:219],42
M 82,v[220:223],42
M 86,v[224:227],42
M 90,v[228:231],42
M 82,v[232:235],42
M 86,v[236:239],42
M 90,v[240:243],42
M 82,v[244:247],42
M 86,v[248:251],42
M 90,v[252:255],42
v_mov_b32 v4,s53
s_mov_b32 s82,0
s_mov_b32 s80,0
s_mov_b32 s54,s45
s_mul_i32 s57,s44,8192
s_mul_i32 s55,s45,32768
s_add_u32 s55,s55,s57
s_add_u32 s55,s55,31457280
s_mul_i32 s60,s46,32768
s_mul_i32 s61,s46,1024
s_mul_i32 s62,s46,114688
s_mul_i32 s63,s46,2560
s_mul_i32 s64,s46,1024
s_lshl_b32 s59,s44,8
s_add_u32 s59,s59,131072
s_mul_i32 s58,s45,1024
s_add_u32 s58,s58,109613056
s_mul_i32 s65,s45,114688
s_add_u32 s65,s65,s47
s_mul_i32 s68,s45,2560
s_add_u32 s68,s68,s48
s_mul_i32 s73,s45,1024
s_add_u32 s73,s73,107479040
s_mul_i32 s74,s45,32768
s_add_u32 s74,s74,s49
s_mul_i32 s75,s45,1024
s_add_u32 s75,s75,107479040
s_mov_b32 s66,0x7fff0000
s_mov_b32 s67,0x7fff0000
s_mov_b32 s69,0x7fff0000
s_mov_b32 s76,0x7fff0000
s_mov_b32 s77,0x7fff0000
s_mov_b32 s70,0x7fff0000
s_mov_b32 s71,0x7fff0000
s_mov_b32 s72,0x7fff0000
v_mov_b32 v1,v0
v_add_u32 v2,32768,v0
s_mov_b32 s84,32768
s_mov_b32 s86,0x3a800000
s_mov_b32 s87,0x3727c5ac
s_mov_b32 s88,0xc0135761
s_mov_b32 s89,0x3d372713
s_and_b32 s85,s44,1
s_mul_i32 s85,s85,127
v_lshlrev_b32 v8,22,v0
v_and_b32 v8,0x80000000,v8
v_or_b32 v8,v8,v5
s_add_u32 m0,s59,4096
s_nop 0
buffer_load_dword v5,s[36:39],s58 offen offset:0 sc1 lds
buffer_store_dword v4,v3,s[36:39],s71 offen offset:0 sc0 sc1
s_add_u32 m0,s59,5120
s_nop 0
buffer_load_dword v5,s[36:39],s58 offen offset:0 sc1 lds
buffer_store_dword v4,v3,s[36:39],s71 offen offset:0 sc0 sc1
s_mov_b32 s83,0
Lg3_poll_%=:
s_add_u32 s78,s83,2
s_and_b32 s78,s78,3
s_lshl_b32 s78,s78,10
s_add_u32 s78,s78,4096
s_add_u32 m0,s59,s78
s_nop 0
buffer_load_dword v5,s[36:39],s58 offen offset:0 sc1 lds
s_and_b32 s78,s83,3
s_lshl_b32 s78,s78,10
v_add_u32 v94,s78,v6
s_waitcnt vmcnt(4)
s_barrier
ds_read_b32 v7,v94 offset:4096
buffer_store_dword v4,v3,s[36:39],s70 offen offset:0 sc0 sc1
s_mov_b32 s70,s71
s_mov_b32 s71,s72
s_mov_b32 s72,0x7fff0000
s_add_u32 s83,s83,1
s_add_u32 s82,s82,1
L 0
v_cmp_eq_u32 vcc,s53,v7
s_nop 0
s_cmp_eq_u32 vcc_lo,-1
s_cbranch_scc1 Lg3_go_%=
s_cmp_lt_u32 s82,20000
s_cbranch_scc1 Lg3_poll_%=
Lg3_go_%=:
buffer_load_dword v114,v11,s[36:39],s73 offen offset:0 sc1
s_add_u32 s73,s73,s64
s_mov_b32 m0,s57
s_add_u32 s56,s55,4096
D 0,55,0
D 0,55,1024
D 0,55,2048
D 0,55,3072
s_add_u32 m0,s57,4096
s_nop 0
D 0,56,0
D 0,56,1024
D 0,56,2048
D 0,56,3072
s_add_u32 s55,s55,s60
s_add_u32 s57,s57,32768
s_and_b32 s57,s57,131071
s_add_u32 s54,s54,s46
Lg3_gox_%=:
s_add_u32 s58,s58,s61
s_cmp_eq_u32 vcc_hi,-1
s_cbranch_scc1 Lg3_go2_%=
s_add_u32 m0,s59,2048
s_nop 0
buffer_load_dword v5,s[36:39],s58 offen offset:0 sc1 lds
s_waitcnt vmcnt(0)
s_barrier
buffer_store_dword v4,v3,s[36:39],s70 offen offset:0 sc0 sc1
s_min_u32 s71,s71,s72
buffer_store_dword v4,v3,s[36:39],s71 offen offset:0 sc0 sc1
s_mov_b32 s70,0x7fff0000
s_mov_b32 s71,0x7fff0000
s_mov_b32 s72,0x7fff0000
s_cmp_eq_u32 s80,1
s_cbranch_scc1 Lg3_st11_%=
Lg3_st10_%=:
ds_read_b32 v7,v6 offset:2048
R 42,1,0
R 46,1,1024
R 50,1,2048
R 54,1,3072
R 58,1,4096
L 5
v_cmp_eq_u32 vcc,s53,v7
L 3
s_cmp_eq_u64 vcc,-1
s_cbranch_scc1 Lg3_L20_%=
s_cmp_eq_u32 vcc_lo,-1
s_cbranch_scc1 Lg3_L10_%=
s_branch Lg3_X0_%=
Lg3_st11_%=:
ds_read_b32 v7,v6 offset:2048
R 42,2,0
R 46,2,1024
R 50,2,2048
R 54,2,3072
R 58,2,4096
L 5
v_cmp_eq_u32 vcc,s53,v7
L 3
s_cmp_eq_u64 vcc,-1
s_cbranch_scc1 Lg3_L21_%=
s_cmp_eq_u32 vcc_lo,-1
s_cbranch_scc1 Lg3_L11_%=
s_branch Lg3_X1_%=
Lg3_go2_%=:
s_add_u32 s58,s58,s61
s_add_u32 m0,s59,2048
s_nop 0
buffer_load_dword v5,s[36:39],s58 offen offset:0 sc1 lds
s_mov_b32 m0,s57
s_add_u32 s56,s55,4096
D 0,55,0
D 0,55,1024
D 0,55,2048
D 0,55,3072
s_add_u32 m0,s57,4096
s_nop 0
D 0,56,0
D 0,56,1024
D 0,56,2048
D 0,56,3072
s_add_u32 s55,s55,s60
s_add_u32 s57,s57,32768
s_and_b32 s57,s57,131071
s_add_u32 s54,s54,s46
s_waitcnt vmcnt(8)
s_barrier
buffer_store_dword v4,v3,s[36:39],s70 offen offset:0 sc0 sc1
s_min_u32 s71,s71,s72
buffer_store_dword v4,v3,s[36:39],s71 offen offset:0 sc0 sc1
s_mov_b32 s70,0x7fff0000
s_mov_b32 s71,0x7fff0000
s_mov_b32 s72,0x7fff0000
s_cmp_eq_u32 s80,1
s_cbranch_scc1 Lg3_st21_%=
Lg3_st20_%=:
ds_read_b32 v7,v6 offset:2048
R 42,1,0
R 46,1,1024
R 50,1,2048
R 54,1,3072
R 58,1,4096
L 5
v_cmp_eq_u32 vcc,s53,v7
L 3
s_cmp_eq_u32 vcc_lo,-1
s_cbranch_scc1 Lg3_S0_%=
s_branch Lg3_N0_%=
Lg3_st21_%=:
ds_read_b32 v7,v6 offset:2048
R 42,2,0
R 46,2,1024
R 50,2,2048
R 54,2,3072
R 58,2,4096
L 5
v_cmp_eq_u32 vcc,s53,v7
L 3
s_cmp_eq_u32 vcc_lo,-1
s_cbranch_scc1 Lg3_S1_%=
s_branch Lg3_N1_%=
Lg3_L20_%=:
v_mfma_f32_16x16x32_bf16 v[18:21],a[0:3],v[42:45],0
s_add_u32 s58,s58,s61
s_add_u32 s58,s58,s61
R 62,1,5120
v_mfma_f32_16x16x32_bf16 v[22:25],a[4:7],v[42:45],0
s_add_u32 m0,s59,0
s_add_u32 s56,s55,4096
s_waitcnt vmcnt(3)
v_mfma_f32_16x16x32_bf16 v[26:29],a[8:11],v[42:45],0
s_nop 0
buffer_load_dword v8,s[36:39],s58 offen offset:0 sc1 lds
M 18,a[12:15],46
s_mov_b32 m0,s57
R 66,1,6144
v_add_f32_dpp v115,v114,v114 quad_perm:[1,0,3,2] row_mask:0xf bank_mask:0xf
M 22,a[16:19],46
s_mov_b32 s66,s65
buffer_load_dword v114,v11,s[36:39],s73 offen offset:0 sc1
M 26,a[20:23],46
L 3
v_add_f32_dpp v115,v115,v115 quad_perm:[2,3,0,1] row_mask:0xf bank_mask:0xf
v_fma_f32 v30,-v78,v82,v30
v_fma_f32 v31,-v78,v83,v31
M 18,a[24:27],50
R 70,1,7168
v_fma_f32 v32,-v78,v84,v32
M 22,a[28:31],50
D 0,55,0
M 26,a[32:35],50
v_add_f32_dpp v118,v115,v115 row_half_mirror row_mask:0xf bank_mask:0xf
v_fma_f32 v33,-v78,v85,v33
M 18,a[36:39],54
R 42,1,8192
v_mul_f32 v94,v30,v30
M 22,a[40:43],54
v_mov_b32_dpp v115,v118 row_ror:8 row_mask:0xf bank_mask:0xf
v_fma_f32 v115,-v118,v118,v115
M 26,a[44:47],54
L 3
D 0,55,1024
v_max_f32 v115,s87,v115
M 18,a[48:51],58
R 46,1,9216
)"
R"(v_mul_f32 v95,v31,v31
M 22,a[52:55],58
v_rsq_f32 v116,v115
M 26,a[56:59],58
v_mul_f32 v121,v115,v116
v_mul_f32 v119,s88,v116
M 18,a[60:63],62
R 50,1,10240
v_mul_f32 v115,s89,v116
M 22,a[64:67],62
D 0,55,2048
M 26,a[68:71],62
L 3
v_mul_f32 v120,v119,v116
v_mul_f32 v120,v120,v115
v_mul_f32 v96,v32,v32
M 18,a[72:75],66
R 54,1,11264
v_mul_f32 v97,v33,v33
M 22,a[76:79],66
ds_write_b128 v12,v[118:121] offset:0
M 26,a[80:83],66
D 0,55,3072
M 18,a[84:87],70
s_add_u32 m0,s57,4096
R 58,1,12288
v_fma_f32 v94,v80,v94,v79
M 22,a[88:91],70
s_add_u32 s55,s55,s60
v_fma_f32 v95,v80,v95,v79
v_fma_f32 v96,v80,v96,v79
v_fma_f32 v97,v80,v97,v79
M 26,a[92:95],70
L 4
D 0,56,0
M 18,a[96:99],42
s_add_u32 s57,s57,32768
R 62,1,13312
v_mul_f32 v94,v30,v94
M 22,a[100:103],42
s_and_b32 s57,s57,131071
v_exp_f32 v94,v94
M 26,a[104:107],42
s_add_u32 s54,s54,s46
v_mul_f32 v95,v31,v95
v_mul_f32 v96,v32,v96
M 18,a[108:111],46
R 66,1,14336
v_mul_f32 v97,v33,v97
M 22,a[112:115],46
D 0,56,1024
v_fma_f32 v94,v94,v81,v81
M 26,a[116:119],46
L 4
v_exp_f32 v95,v95
M 18,a[120:123],50
R 70,1,15360
v_fma_f32 v95,v95,v81,v81
M 22,a[124:127],50
D 0,56,2048
M 26,a[128:131],50
v_exp_f32 v96,v96
M 18,a[132:135],54
R 42,1,16384
v_fma_f32 v96,v96,v81,v81
M 22,a[136:139],54
v_exp_f32 v97,v97
v_fma_f32 v34,-v78,v86,v34
M 26,a[140:143],54
L 3
D 0,56,3072
M 18,a[144:147],58
s_mov_b32 m0,s57
R 46,1,17408
v_fma_f32 v97,v97,v81,v81
M 22,a[148:151],58
s_add_u32 s56,s55,4096
v_rcp_f32 v94,v94
M 26,a[152:155],58
v_rcp_f32 v95,v95
M 18,a[156:159],62
R 50,1,18432
v_rcp_f32 v96,v96
M 22,a[160:163],62
D 0,55,0
M 26,a[164:167],62
L 3
v_rcp_f32 v97,v97
M 18,a[168:171],66
R 54,1,19456
v_mul_f32 v30,v30,v94
M 22,a[172:175],66
v_mul_f32 v31,v31,v95
v_mul_f32 v32,v32,v96
M 26,a[176:179],66
v_mul_f32 v33,v33,v97
v_cvt_pk_bf16_f32 v106,v30,v31
M 18,a[180:183],70
R 58,1,20480
v_cvt_pk_bf16_f32 v107,v32,v33
v_fma_f32 v35,-v78,v87,v35
M 22,a[184:187],70
D 0,55,1024
M 26,a[188:191],70
L 3
v_fma_f32 v36,-v78,v88,v36
v_fma_f32 v37,-v78,v89,v37
M 18,a[192:195],42
R 62,1,21504
v_mul_f32 v98,v34,v34
M 22,a[196:199],42
v_mul_f32 v99,v35,v35
v_mul_f32 v100,v36,v36
M 26,a[200:203],42
v_mul_f32 v101,v37,v37
v_fma_f32 v98,v80,v98,v79
M 18,a[204:207],46
R 66,1,22528
v_fma_f32 v99,v80,v99,v79
v_fma_f32 v100,v80,v100,v79
M 22,a[208:211],46
D 0,55,2048
M 26,a[212:215],46
L 3
v_fma_f32 v101,v80,v101,v79
v_mul_f32 v98,v34,v98
M 18,a[216:219],50
R 70,1,23552
v_mul_f32 v99,v35,v99
M 22,a[220:223],50
v_exp_f32 v98,v98
M 26,a[224:227],50
v_exp_f32 v99,v99
M 18,a[228:231],54
R 42,1,24576
v_mul_f32 v100,v36,v100
v_mul_f32 v101,v37,v101
M 22,a[232:235],54
v_exp_f32 v100,v100
M 26,a[236:239],54
L 3
D 0,55,3072
M 18,a[240:243],58
s_add_u32 m0,s57,4096
R 46,1,25600
v_fma_f32 v98,v98,v81,v81
M 22,a[244:247],58
s_add_u32 s55,s55,s60
v_exp_f32 v101,v101
M 26,a[248:251],58
s_add_u32 s57,s57,32768
v_fma_f32 v99,v99,v81,v81
v_rcp_f32 v98,v98
M 18,a[252:255],62
s_and_b32 s57,s57,131071
R 50,1,26624
v_fma_f32 v100,v100,v81,v81
M 22,v[128:131],62
s_add_u32 s54,s54,s46
v_rcp_f32 v99,v99
M 26,v[132:135],62
L 3
v_rcp_f32 v100,v100
M 18,v[136:139],66
s_add_u32 s73,s73,s64
R 54,1,27648
v_fma_f32 v101,v101,v81,v81
M 22,v[140:143],66
s_add_u32 s65,s65,s62
D 0,56,0
M 26,v[144:147],66
v_rcp_f32 v101,v101
v_mul_f32 v34,v34,v98
M 18,v[148:151],70
R 58,1,28672
v_mul_f32 v35,v35,v99
M 22,v[152:155],70
v_mul_f32 v36,v36,v100
v_mul_f32 v37,v37,v101
M 26,v[156:159],70
L 3
v_cvt_pk_bf16_f32 v108,v34,v35
v_cvt_pk_bf16_f32 v109,v36,v37
M 18,v[160:163],42
R 62,1,29696
v_fma_f32 v38,-v78,v90,v38
M 22,v[164:167],42
D 0,56,1024
M 26,v[168:171],42
buffer_store_dwordx4 v[106:109],v0,s[36:39],s67 offen offset:0 sc0 sc1
v_fma_f32 v39,-v78,v91,v39
M 18,v[172:175],46
R 66,1,30720
v_fma_f32 v40,-v78,v92,v40
M 22,v[176:179],46
v_fma_f32 v41,-v78,v93,v41
v_mul_f32 v102,v38,v38
M 26,v[180:183],46
L 3
v_mul_f32 v103,v39,v39
v_mul_f32 v104,v40,v40
M 18,v[184:187],50
s_nop 0
R 70,1,31744
v_mul_f32 v105,v41,v41
M 22,v[188:191],50
s_waitcnt vmcnt(7)
v_fma_f32 v102,v80,v102,v79
v_fma_f32 v103,v80,v103,v79
v_fma_f32 v104,v80,v104,v79
M 26,v[192:195],50
s_barrier
R 74,13,0
ds_read_b32 v7,v6 offset:0
M 18,v[196:199],54
R 42,2,0
v_fma_f32 v105,v80,v105,v79
M 22,v[200:203],54
buffer_store_dword v4,v3,s[36:39],s70 offen offset:0 sc0 sc1
M 26,v[204:207],54
L 5
D 0,56,2048
M 18,v[208:211],58
s_mov_b32 s70,s69
R 46,2,1024
v_mul_f32 v102,v38,v102
M 22,v[212:215],58
s_mov_b32 s69,s68
v_mul_f32 v103,v39,v103
v_exp_f32 v102,v102
M 26,v[216:219],58
L 2
v_mul_f32 v104,v40,v104
v_mul_f32 v105,v41,v105
M 18,v[220:223],62
s_add_u32 s68,s68,s63
R 50,2,2048
v_fma_f32 v102,v102,v81,v81
M 22,v[224:227],62
D 0,56,3072
M 26,v[228:231],62
s_nop 0
v_exp_f32 v103,v103
M 18,v[232:235],66
R 54,2,3072
v_exp_f32 v104,v104
v_exp_f32 v105,v105
M 22,v[236:239],66
v_fma_f32 v103,v103,v81,v81
v_fma_f32 v104,v104,v81,v81
v_fma_f32 v105,v105,v81,v81
v_rcp_f32 v102,v102
v_rcp_f32 v103,v103
v_rcp_f32 v104,v104
v_rcp_f32 v105,v105
M 26,v[240:243],66
v_mul_f32 v38,v38,v102
v_mul_f32 v39,v39,v103
v_mul_f32 v40,v40,v104
v_mul_f32 v41,v41,v105
v_cvt_pk_bf16_f32 v110,v38,v39
v_cvt_pk_bf16_f32 v111,v40,v41
M 18,v[244:247],70
R 58,2,4096
v_cmp_eq_u32 vcc,s53,v7
M 22,v[248:251],70
s_cmp_eq_u32 vcc_lo,-1
v_permlane16_swap_b32 v110,v112
v_permlane16_swap_b32 v111,v113
M 26,v[252:255],70
L 3
buffer_store_dwordx4 v[110:113],v10,s[36:39],s67 offen offset:0 sc0 sc1
v_xor_b32 v1,65536,v1
s_cbranch_scc0 Lg3_N1_%=
Lg3_S1_%=:
v_mfma_f32_16x16x32_bf16 v[30:33],a[0:3],v[42:45],0
s_add_u32 s58,s58,s61
s_add_u32 m0,s59,1024
R 62,2,5120
v_mfma_f32_16x16x32_bf16 v[34:37],a[4:7],v[42:45],0
s_waitcnt vmcnt(11)
buffer_load_dword v8,s[36:39],s58 offen offset:0 sc1 lds
v_mfma_f32_16x16x32_bf16 v[38:41],a[8:11],v[42:45],0
s_nop 0
v_add_f32_dpp v115,v114,v114 quad_perm:[1,0,3,2] row_mask:0xf bank_mask:0xf
v_fma_f32 v18,-v74,v82,v18
M 30,a[12:15],46
s_mov_b32 m0,s57
R 66,2,6144
v_fma_f32 v19,-v74,v83,v19
M 34,a[16:19],46
s_add_u32 s56,s55,4096
buffer_load_dword v114,v11,s[36:39],s73 offen offset:0 sc1
M 38,a[20:23],46
L 3
v_add_f32_dpp v115,v115,v115 quad_perm:[2,3,0,1] row_mask:0xf bank_mask:0xf
v_fma_f32 v20,-v74,v84,v20
M 30,a[24:27],50
s_mov_b32 s67,s65
R 70,2,7168
v_fma_f32 v21,-v74,v85,v21
v_mul_f32 v94,v18,v18
M 34,a[28:31],50
D 0,55,0
M 38,a[32:35],50
v_add_f32_dpp v118,v115,v115 row_half_mirror row_mask:0xf bank_mask:0xf
v_mul_f32 v95,v19,v19
M 30,a[36:39],54
R 42,2,8192
v_mul_f32 v96,v20,v20
M 34,a[40:43],54
v_mov_b32_dpp v115,v118 row_ror:8 row_mask:0xf bank_mask:0xf
v_fma_f32 v115,-v118,v118,v115
M 38,a[44:47],54
L 3
v_max_f32 v115,s87,v115
v_mul_f32 v97,v21,v21
M 30,a[48:51],58
R 46,2,9216
v_fma_f32 v94,v76,v94,v75
M 34,a[52:55],58
v_rsq_f32 v116,v115
v_fma_f32 v95,v76,v95,v75
M 38,a[56:59],58
v_mul_f32 v121,v115,v116
v_mul_f32 v119,s88,v116
M 30,a[60:63],62
R 50,2,10240
v_mul_f32 v115,s89,v116
M 34,a[64:67],62
v_mul_f32 v120,v119,v116
v_mul_f32 v120,v120,v115
M 38,a[68:71],62
L 3
ds_write_b128 v12,v[118:121] offset:2048
M 30,a[72:75],66
R 54,2,11264
v_fma_f32 v96,v76,v96,v75
M 34,a[76:79],66
D 0,55,1024
v_fma_f32 v97,v76,v97,v75
M 38,a[80:83],66
v_mul_f32 v94,v18,v94
v_mul_f32 v95,v19,v95
M 30,a[84:87],70
R 58,2,12288
v_mul_f32 v96,v20,v96
M 34,a[88:91],70
v_exp_f32 v94,v94
M 38,a[92:95],70
L 4
v_exp_f32 v95,v95
M 30,a[96:99],42
R 62,2,13312
v_mul_f32 v97,v21,v97
M 34,a[100:103],42
v_exp_f32 v96,v96
M 38,a[104:107],42
v_exp_f32 v97,v97
v_fma_f32 v94,v94,v77,v77
M 30,a[108:111],46
R 66,2,14336
v_fma_f32 v95,v95,v77,v77
M 34,a[112:115],46
D 0,55,2048
M 38,a[116:119],46
L 3
v_rcp_f32 v94,v94
M 30,a[120:123],50
R 70,2,15360
v_fma_f32 v96,v96,v77,v77
M 34,a[124:127],50
v_rcp_f32 v95,v95
M 38,a[128:131],50
v_rcp_f32 v96,v96
M 30,a[132:135],54
R 42,2,16384
v_fma_f32 v97,v97,v77,v77
v_mul_f32 v18,v18,v94
M 34,a[136:139],54
v_rcp_f32 v97,v97
M 38,a[140:143],54
L 3
v_mul_f32 v19,v19,v95
v_mul_f32 v20,v20,v96
M 30,a[144:147],58
R 46,2,17408
v_mul_f32 v21,v21,v97
M 34,a[148:151],58
v_cvt_pk_bf16_f32 v106,v18,v19
v_cvt_pk_bf16_f32 v107,v20,v21
M 38,a[152:155],58
v_fma_f32 v22,-v74,v86,v22
v_fma_f32 v23,-v74,v87,v23
M 30,a[156:159],62
R 50,2,18432
v_fma_f32 v24,-v74,v88,v24
v_fma_f32 v25,-v74,v89,v25
M 34,a[160:163],62
D 0,55,3072
M 38,a[164:167],62
L 3
v_mul_f32 v98,v22,v22
v_mul_f32 v99,v23,v23
M 30,a[168:171],66
s_add_u32 m0,s57,4096
R 54,2,19456
v_mul_f32 v100,v24,v24
M 34,a[172:175],66
s_add_u32 s55,s55,s60
v_mul_f32 v101,v25,v25
v_fma_f32 v98,v76,v98,v75
M 38,a[176:179],66
s_add_u32 s57,s57,32768
v_fma_f32 v99,v76,v99,v75
v_fma_f32 v100,v76,v100,v75
M 30,a[180:183],70
s_and_b32 s57,s57,131071
R 58,2,20480
v_fma_f32 v101,v76,v101,v75
M 34,a[184:187],70
s_add_u32 s54,s54,s46
v_mul_f32 v98,v22,v98
v_exp_f32 v98,v98
M 38,a[188:191],70
L 3
v_mul_f32 v99,v23,v99
v_mul_f32 v100,v24,v100
M 30,a[192:195],42
s_add_u32 s73,s73,s64
R 62,2,21504
v_mul_f32 v101,v25,v101
M 34,a[196:199],42
s_add_u32 s65,s65,s62
v_exp_f32 v99,v99
M 38,a[200:203],42
D 0,56,0
M 30,a[204:207],46
R 66,2,22528
v_fma_f32 v98,v98,v77,v77
M 34,a[208:211],46
v_exp_f32 v100,v100
M 38,a[212:215],46
L 3
v_exp_f32 v101,v101
v_fma_f32 v99,v99,v77,v77
M 30,a[216:219],50
R 70,2,23552
v_fma_f32 v100,v100,v77,v77
M 34,a[220:223],50
v_rcp_f32 v98,v98
M 38,a[224:227],50
v_rcp_f32 v99,v99
M 30,a[228:231],54
R 42,2,24576
v_fma_f32 v101,v101,v77,v77
M 34,a[232:235],54
v_rcp_f32 v100,v100
M 38,a[236:239],54
L 3
v_rcp_f32 v101,v101
v_mul_f32 v22,v22,v98
M 30,a[240:243],58
R 46,2,25600
v_mul_f32 v23,v23,v99
M 34,a[244:247],58
D 0,56,1024
M 38,a[248:251],58
v_mul_f32 v24,v24,v100
v_mul_f32 v25,v25,v101
M 30,a[252:255],62
R 50,2,26624
v_cvt_pk_bf16_f32 v108,v22,v23
M 34,v[128:131],62
v_cvt_pk_bf16_f32 v109,v24,v25
v_fma_f32 v26,-v74,v90,v26
M 38,v[132:135],62
L 3
buffer_store_dwordx4 v[106:109],v0,s[36:39],s66 offen offset:0 sc0 sc1
v_fma_f32 v27,-v74,v91,v27
M 30,v[136:139],66
R 54,2,27648
v_fma_f32 v28,-v74,v92,v28
v_fma_f32 v29,-v74,v93,v29
M 34,v[140:143],66
v_mul_f32 v102,v26,v26
v_mul_f32 v103,v27,v27
M 38,v[144:147],66
v_mul_f32 v104,v28,v28
v_mul_f32 v105,v29,v29
M 30,v[148:151],70
R 58,2,28672
v_fma_f32 v102,v76,v102,v75
M 34,v[152:155],70
v_fma_f32 v103,v76,v103,v75
v_fma_f32 v104,v76,v104,v75
M 38,v[156:159],70
L 3
v_fma_f32 v105,v76,v105,v75
v_mul_f32 v102,v26,v102
)"
R"(M 30,v[160:163],42
R 62,2,29696
v_mul_f32 v103,v27,v103
M 34,v[164:167],42
D 0,56,2048
v_mul_f32 v104,v28,v104
M 38,v[168:171],42
v_exp_f32 v102,v102
M 30,v[172:175],46
R 66,2,30720
v_mul_f32 v105,v29,v105
M 34,v[176:179],46
v_exp_f32 v103,v103
M 38,v[180:183],46
L 3
v_exp_f32 v104,v104
M 30,v[184:187],50
s_nop 0
R 70,2,31744
v_fma_f32 v102,v102,v77,v77
M 34,v[188:191],50
s_waitcnt vmcnt(9)
v_exp_f32 v105,v105
v_fma_f32 v103,v103,v77,v77
M 38,v[192:195],50
s_barrier
R 78,13,2048
ds_read_b32 v7,v6 offset:1024
M 30,v[196:199],54
R 42,1,0
v_fma_f32 v104,v104,v77,v77
M 34,v[200:203],54
buffer_store_dword v4,v3,s[36:39],s70 offen offset:0 sc0 sc1
M 38,v[204:207],54
L 5
D 0,56,3072
M 30,v[208:211],58
s_mov_b32 s70,s69
R 46,1,1024
v_fma_f32 v105,v105,v77,v77
M 34,v[212:215],58
s_mov_b32 s69,s68
v_rcp_f32 v102,v102
M 38,v[216:219],58
L 2
v_rcp_f32 v103,v103
v_mul_f32 v26,v26,v102
M 30,v[220:223],62
s_add_u32 s68,s68,s63
R 50,1,2048
v_mul_f32 v27,v27,v103
M 34,v[224:227],62
v_rcp_f32 v104,v104
M 38,v[228:231],62
s_nop 0
v_rcp_f32 v105,v105
M 30,v[232:235],66
R 54,1,3072
v_mul_f32 v28,v28,v104
M 34,v[236:239],66
v_mul_f32 v29,v29,v105
v_cvt_pk_bf16_f32 v110,v26,v27
M 38,v[240:243],66
v_cvt_pk_bf16_f32 v111,v28,v29
v_cmp_eq_u32 vcc,s53,v7
M 30,v[244:247],70
s_cmp_eq_u32 vcc_lo,-1
R 58,1,4096
v_permlane16_swap_b32 v110,v112
M 34,v[248:251],70
v_permlane16_swap_b32 v111,v113
M 38,v[252:255],70
L 3
buffer_store_dwordx4 v[110:113],v10,s[36:39],s66 offen offset:0 sc0 sc1
v_xor_b32 v2,65536,v2
s_cbranch_scc0 Lg3_N0_%=
Lg3_S0_%=:
v_mfma_f32_16x16x32_bf16 v[18:21],a[0:3],v[42:45],0
s_add_u32 s58,s58,s61
s_add_u32 m0,s59,0
R 62,1,5120
v_mfma_f32_16x16x32_bf16 v[22:25],a[4:7],v[42:45],0
s_waitcnt vmcnt(11)
buffer_load_dword v8,s[36:39],s58 offen offset:0 sc1 lds
v_mfma_f32_16x16x32_bf16 v[26:29],a[8:11],v[42:45],0
s_nop 0
v_add_f32_dpp v115,v114,v114 quad_perm:[1,0,3,2] row_mask:0xf bank_mask:0xf
v_fma_f32 v30,-v78,v82,v30
M 18,a[12:15],46
s_mov_b32 m0,s57
R 66,1,6144
v_fma_f32 v31,-v78,v83,v31
M 22,a[16:19],46
s_add_u32 s56,s55,4096
buffer_load_dword v114,v11,s[36:39],s73 offen offset:0 sc1
M 26,a[20:23],46
L 3
v_add_f32_dpp v115,v115,v115 quad_perm:[2,3,0,1] row_mask:0xf bank_mask:0xf
v_fma_f32 v32,-v78,v84,v32
M 18,a[24:27],50
s_mov_b32 s66,s65
R 70,1,7168
v_fma_f32 v33,-v78,v85,v33
v_mul_f32 v94,v30,v30
M 22,a[28:31],50
D 0,55,0
M 26,a[32:35],50
v_add_f32_dpp v118,v115,v115 row_half_mirror row_mask:0xf bank_mask:0xf
v_mul_f32 v95,v31,v31
M 18,a[36:39],54
R 42,1,8192
v_mul_f32 v96,v32,v32
M 22,a[40:43],54
v_mov_b32_dpp v115,v118 row_ror:8 row_mask:0xf bank_mask:0xf
v_fma_f32 v115,-v118,v118,v115
M 26,a[44:47],54
L 3
v_max_f32 v115,s87,v115
v_mul_f32 v97,v33,v33
M 18,a[48:51],58
R 46,1,9216
v_fma_f32 v94,v80,v94,v79
M 22,a[52:55],58
v_rsq_f32 v116,v115
v_fma_f32 v95,v80,v95,v79
M 26,a[56:59],58
v_mul_f32 v121,v115,v116
v_mul_f32 v119,s88,v116
M 18,a[60:63],62
R 50,1,10240
v_mul_f32 v115,s89,v116
M 22,a[64:67],62
v_mul_f32 v120,v119,v116
v_mul_f32 v120,v120,v115
M 26,a[68:71],62
L 3
ds_write_b128 v12,v[118:121] offset:0
M 18,a[72:75],66
R 54,1,11264
v_fma_f32 v96,v80,v96,v79
M 22,a[76:79],66
D 0,55,1024
v_fma_f32 v97,v80,v97,v79
M 26,a[80:83],66
v_mul_f32 v94,v30,v94
v_mul_f32 v95,v31,v95
M 18,a[84:87],70
R 58,1,12288
v_mul_f32 v96,v32,v96
M 22,a[88:91],70
v_exp_f32 v94,v94
M 26,a[92:95],70
L 4
v_exp_f32 v95,v95
M 18,a[96:99],42
R 62,1,13312
v_mul_f32 v97,v33,v97
M 22,a[100:103],42
v_exp_f32 v96,v96
M 26,a[104:107],42
v_exp_f32 v97,v97
v_fma_f32 v94,v94,v81,v81
M 18,a[108:111],46
R 66,1,14336
v_fma_f32 v95,v95,v81,v81
M 22,a[112:115],46
D 0,55,2048
M 26,a[116:119],46
L 3
v_rcp_f32 v94,v94
M 18,a[120:123],50
R 70,1,15360
v_fma_f32 v96,v96,v81,v81
M 22,a[124:127],50
v_rcp_f32 v95,v95
M 26,a[128:131],50
v_rcp_f32 v96,v96
M 18,a[132:135],54
R 42,1,16384
v_fma_f32 v97,v97,v81,v81
v_mul_f32 v30,v30,v94
M 22,a[136:139],54
v_rcp_f32 v97,v97
M 26,a[140:143],54
L 3
v_mul_f32 v31,v31,v95
v_mul_f32 v32,v32,v96
M 18,a[144:147],58
R 46,1,17408
v_mul_f32 v33,v33,v97
M 22,a[148:151],58
v_cvt_pk_bf16_f32 v106,v30,v31
v_cvt_pk_bf16_f32 v107,v32,v33
M 26,a[152:155],58
v_fma_f32 v34,-v78,v86,v34
v_fma_f32 v35,-v78,v87,v35
M 18,a[156:159],62
R 50,1,18432
v_fma_f32 v36,-v78,v88,v36
v_fma_f32 v37,-v78,v89,v37
M 22,a[160:163],62
D 0,55,3072
M 26,a[164:167],62
L 3
v_mul_f32 v98,v34,v34
v_mul_f32 v99,v35,v35
M 18,a[168:171],66
s_add_u32 m0,s57,4096
R 54,1,19456
v_mul_f32 v100,v36,v36
M 22,a[172:175],66
s_add_u32 s55,s55,s60
v_mul_f32 v101,v37,v37
v_fma_f32 v98,v80,v98,v79
M 26,a[176:179],66
s_add_u32 s57,s57,32768
v_fma_f32 v99,v80,v99,v79
v_fma_f32 v100,v80,v100,v79
M 18,a[180:183],70
s_and_b32 s57,s57,131071
R 58,1,20480
v_fma_f32 v101,v80,v101,v79
M 22,a[184:187],70
s_add_u32 s54,s54,s46
v_mul_f32 v98,v34,v98
v_exp_f32 v98,v98
M 26,a[188:191],70
L 3
v_mul_f32 v99,v35,v99
v_mul_f32 v100,v36,v100
M 18,a[192:195],42
s_add_u32 s73,s73,s64
R 62,1,21504
v_mul_f32 v101,v37,v101
M 22,a[196:199],42
s_add_u32 s65,s65,s62
v_exp_f32 v99,v99
M 26,a[200:203],42
D 0,56,0
M 18,a[204:207],46
R 66,1,22528
v_fma_f32 v98,v98,v81,v81
M 22,a[208:211],46
v_exp_f32 v100,v100
M 26,a[212:215],46
L 3
v_exp_f32 v101,v101
v_fma_f32 v99,v99,v81,v81
M 18,a[216:219],50
R 70,1,23552
v_fma_f32 v100,v100,v81,v81
M 22,a[220:223],50
v_rcp_f32 v98,v98
M 26,a[224:227],50
v_rcp_f32 v99,v99
M 18,a[228:231],54
R 42,1,24576
v_fma_f32 v101,v101,v81,v81
M 22,a[232:235],54
v_rcp_f32 v100,v100
M 26,a[236:239],54
L 3
v_rcp_f32 v101,v101
v_mul_f32 v34,v34,v98
M 18,a[240:243],58
R 46,1,25600
v_mul_f32 v35,v35,v99
M 22,a[244:247],58
D 0,56,1024
M 26,a[248:251],58
v_mul_f32 v36,v36,v100
v_mul_f32 v37,v37,v101
M 18,a[252:255],62
R 50,1,26624
v_cvt_pk_bf16_f32 v108,v34,v35
M 22,v[128:131],62
v_cvt_pk_bf16_f32 v109,v36,v37
v_fma_f32 v38,-v78,v90,v38
M 26,v[132:135],62
L 3
buffer_store_dwordx4 v[106:109],v0,s[36:39],s67 offen offset:0 sc0 sc1
v_fma_f32 v39,-v78,v91,v39
M 18,v[136:139],66
R 54,1,27648
v_fma_f32 v40,-v78,v92,v40
v_fma_f32 v41,-v78,v93,v41
M 22,v[140:143],66
v_mul_f32 v102,v38,v38
v_mul_f32 v103,v39,v39
M 26,v[144:147],66
v_mul_f32 v104,v40,v40
v_mul_f32 v105,v41,v41
M 18,v[148:151],70
R 58,1,28672
v_fma_f32 v102,v80,v102,v79
M 22,v[152:155],70
v_fma_f32 v103,v80,v103,v79
v_fma_f32 v104,v80,v104,v79
M 26,v[156:159],70
L 3
v_fma_f32 v105,v80,v105,v79
v_mul_f32 v102,v38,v102
M 18,v[160:163],42
R 62,1,29696
v_mul_f32 v103,v39,v103
M 22,v[164:167],42
D 0,56,2048
v_mul_f32 v104,v40,v104
M 26,v[168:171],42
v_exp_f32 v102,v102
M 18,v[172:175],46
R 66,1,30720
v_mul_f32 v105,v41,v105
M 22,v[176:179],46
v_exp_f32 v103,v103
M 26,v[180:183],46
L 3
v_exp_f32 v104,v104
M 18,v[184:187],50
s_nop 0
R 70,1,31744
v_fma_f32 v102,v102,v81,v81
M 22,v[188:191],50
s_waitcnt vmcnt(9)
v_exp_f32 v105,v105
v_fma_f32 v103,v103,v81,v81
M 26,v[192:195],50
s_barrier
R 74,13,0
ds_read_b32 v7,v6 offset:0
M 18,v[196:199],54
R 42,2,0
v_fma_f32 v104,v104,v81,v81
M 22,v[200:203],54
buffer_store_dword v4,v3,s[36:39],s70 offen offset:0 sc0 sc1
M 26,v[204:207],54
L 5
D 0,56,3072
M 18,v[208:211],58
s_mov_b32 s70,s69
R 46,2,1024
v_fma_f32 v105,v105,v81,v81
M 22,v[212:215],58
s_mov_b32 s69,s68
v_rcp_f32 v102,v102
M 26,v[216:219],58
L 2
v_rcp_f32 v103,v103
v_mul_f32 v38,v38,v102
M 18,v[220:223],62
s_add_u32 s68,s68,s63
R 50,2,2048
v_mul_f32 v39,v39,v103
M 22,v[224:227],62
v_rcp_f32 v104,v104
M 26,v[228:231],62
s_nop 0
v_rcp_f32 v105,v105
M 18,v[232:235],66
R 54,2,3072
v_mul_f32 v40,v40,v104
M 22,v[236:239],66
v_mul_f32 v41,v41,v105
v_cvt_pk_bf16_f32 v110,v38,v39
M 26,v[240:243],66
v_cvt_pk_bf16_f32 v111,v40,v41
v_cmp_eq_u32 vcc,s53,v7
M 18,v[244:247],70
s_cmp_eq_u32 vcc_lo,-1
R 58,2,4096
v_permlane16_swap_b32 v110,v112
M 22,v[248:251],70
v_permlane16_swap_b32 v111,v113
M 26,v[252:255],70
L 3
buffer_store_dwordx4 v[110:113],v10,s[36:39],s67 offen offset:0 sc0 sc1
v_xor_b32 v1,65536,v1
s_cbranch_scc1 Lg3_S1_%=
Lg3_N1_%=:
v_mfma_f32_16x16x32_bf16 v[30:33],a[0:3],v[42:45],0
s_add_u32 m0,s59,1024
s_waitcnt vmcnt(11)
R 62,2,5120
v_mfma_f32_16x16x32_bf16 v[34:37],a[4:7],v[42:45],0
s_nop 0
buffer_load_dword v5,s[36:39],s58 offen offset:0 sc1 lds
v_mfma_f32_16x16x32_bf16 v[38:41],a[8:11],v[42:45],0
s_mov_b32 s67,s65
v_add_f32_dpp v115,v114,v114 quad_perm:[1,0,3,2] row_mask:0xf bank_mask:0xf
v_fma_f32 v18,-v74,v82,v18
M 30,a[12:15],46
R 66,2,6144
v_fma_f32 v19,-v74,v83,v19
M 34,a[16:19],46
buffer_load_dword v114,v11,s[36:39],s73 offen offset:0 sc1
M 38,a[20:23],46
L 3
v_add_f32_dpp v115,v115,v115 quad_perm:[2,3,0,1] row_mask:0xf bank_mask:0xf
v_fma_f32 v20,-v74,v84,v20
M 30,a[24:27],50
s_add_u32 s73,s73,s64
R 70,2,7168
v_fma_f32 v21,-v74,v85,v21
M 34,a[28:31],50
s_add_u32 s65,s65,s62
v_add_f32_dpp v118,v115,v115 row_half_mirror row_mask:0xf bank_mask:0xf
v_mul_f32 v94,v18,v18
M 38,a[32:35],50
v_mul_f32 v95,v19,v19
v_mul_f32 v96,v20,v20
M 30,a[36:39],54
R 42,2,8192
v_mov_b32_dpp v115,v118 row_ror:8 row_mask:0xf bank_mask:0xf
M 34,a[40:43],54
v_fma_f32 v115,-v118,v118,v115
v_max_f32 v115,s87,v115
M 38,a[44:47],54
L 3
v_rsq_f32 v116,v115
M 30,a[48:51],58
R 46,2,9216
v_mul_f32 v121,v115,v116
M 34,a[52:55],58
v_mul_f32 v119,s88,v116
v_mul_f32 v115,s89,v116
M 38,a[56:59],58
v_mul_f32 v120,v119,v116
v_mul_f32 v120,v120,v115
M 30,a[60:63],62
R 50,2,10240
v_mul_f32 v97,v21,v21
M 34,a[64:67],62
ds_write_b128 v12,v[118:121] offset:2048
M 38,a[68:71],62
L 4
v_fma_f32 v94,v76,v94,v75
v_fma_f32 v95,v76,v95,v75
M 30,a[72:75],66
R 54,2,11264
v_fma_f32 v96,v76,v96,v75
M 34,a[76:79],66
v_fma_f32 v97,v76,v97,v75
v_mul_f32 v94,v18,v94
M 38,a[80:83],66
v_exp_f32 v94,v94
M 30,a[84:87],70
R 58,2,12288
v_mul_f32 v95,v19,v95
M 34,a[88:91],70
v_exp_f32 v95,v95
M 38,a[92:95],70
L 4
v_mul_f32 v96,v20,v96
v_mul_f32 v97,v21,v97
M 30,a[96:99],42
R 62,2,13312
v_fma_f32 v94,v94,v77,v77
M 34,a[100:103],42
v_exp_f32 v96,v96
M 38,a[104:107],42
v_exp_f32 v97,v97
M 30,a[108:111],46
R 66,2,14336
v_fma_f32 v95,v95,v77,v77
M 34,a[112:115],46
v_rcp_f32 v94,v94
M 38,a[116:119],46
L 3
v_rcp_f32 v95,v95
M 30,a[120:123],50
R 70,2,15360
v_fma_f32 v96,v96,v77,v77
M 34,a[124:127],50
v_rcp_f32 v96,v96
M 38,a[128:131],50
v_fma_f32 v97,v97,v77,v77
v_mul_f32 v18,v18,v94
M 30,a[132:135],54
R 42,2,16384
v_mul_f32 v19,v19,v95
M 34,a[136:139],54
v_rcp_f32 v97,v97
M 38,a[140:143],54
L 3
v_mul_f32 v20,v20,v96
v_mul_f32 v21,v21,v97
M 30,a[144:147],58
R 46,2,17408
v_cvt_pk_bf16_f32 v106,v18,v19
M 34,a[148:151],58
v_cvt_pk_bf16_f32 v107,v20,v21
v_fma_f32 v22,-v74,v86,v22
M 38,a[152:155],58
v_fma_f32 v23,-v74,v87,v23
v_fma_f32 v24,-v74,v88,v24
M 30,a[156:159],62
R 50,2,18432
v_fma_f32 v25,-v74,v89,v25
M 34,a[160:163],62
v_mul_f32 v98,v22,v22
)"
R"(v_mul_f32 v99,v23,v23
M 38,a[164:167],62
L 3
v_mul_f32 v100,v24,v24
v_mul_f32 v101,v25,v25
M 30,a[168:171],66
R 54,2,19456
v_fma_f32 v98,v76,v98,v75
M 34,a[172:175],66
v_fma_f32 v99,v76,v99,v75
v_fma_f32 v100,v76,v100,v75
M 38,a[176:179],66
v_fma_f32 v101,v76,v101,v75
v_mul_f32 v98,v22,v98
M 30,a[180:183],70
R 58,2,20480
v_mul_f32 v99,v23,v99
M 34,a[184:187],70
v_exp_f32 v98,v98
M 38,a[188:191],70
L 3
v_exp_f32 v99,v99
M 30,a[192:195],42
R 62,2,21504
v_mul_f32 v100,v24,v100
M 34,a[196:199],42
v_exp_f32 v100,v100
M 38,a[200:203],42
v_mul_f32 v101,v25,v101
v_fma_f32 v98,v98,v77,v77
M 30,a[204:207],46
R 66,2,22528
v_fma_f32 v99,v99,v77,v77
M 34,a[208:211],46
v_exp_f32 v101,v101
M 38,a[212:215],46
L 3
v_rcp_f32 v98,v98
M 30,a[216:219],50
R 70,2,23552
v_fma_f32 v100,v100,v77,v77
M 34,a[220:223],50
v_rcp_f32 v99,v99
M 38,a[224:227],50
v_rcp_f32 v100,v100
M 30,a[228:231],54
R 42,2,24576
v_fma_f32 v101,v101,v77,v77
M 34,a[232:235],54
v_rcp_f32 v101,v101
M 38,a[236:239],54
L 3
v_mul_f32 v22,v22,v98
v_mul_f32 v23,v23,v99
M 30,a[240:243],58
R 46,2,25600
v_mul_f32 v24,v24,v100
M 34,a[244:247],58
v_mul_f32 v25,v25,v101
v_cvt_pk_bf16_f32 v108,v22,v23
M 38,a[248:251],58
v_cvt_pk_bf16_f32 v109,v24,v25
v_fma_f32 v26,-v74,v90,v26
M 30,a[252:255],62
R 50,2,26624
v_fma_f32 v27,-v74,v91,v27
M 34,v[128:131],62
buffer_store_dwordx4 v[106:109],v0,s[36:39],s66 offen offset:0 sc0 sc1
v_fma_f32 v28,-v74,v92,v28
M 38,v[132:135],62
L 3
v_fma_f32 v29,-v74,v93,v29
v_mul_f32 v102,v26,v26
M 30,v[136:139],66
R 54,2,27648
v_mul_f32 v103,v27,v27
M 34,v[140:143],66
v_mul_f32 v104,v28,v28
v_mul_f32 v105,v29,v29
M 38,v[144:147],66
v_fma_f32 v102,v76,v102,v75
v_fma_f32 v103,v76,v103,v75
M 30,v[148:151],70
R 58,2,28672
v_fma_f32 v104,v76,v104,v75
M 34,v[152:155],70
v_fma_f32 v105,v76,v105,v75
v_mul_f32 v102,v26,v102
M 38,v[156:159],70
L 3
v_exp_f32 v102,v102
M 30,v[160:163],42
R 62,2,29696
v_mul_f32 v103,v27,v103
M 34,v[164:167],42
v_exp_f32 v103,v103
M 38,v[168:171],42
v_mul_f32 v104,v28,v104
v_mul_f32 v105,v29,v105
M 30,v[172:175],46
R 66,2,30720
v_fma_f32 v102,v102,v77,v77
M 34,v[176:179],46
v_exp_f32 v104,v104
M 38,v[180:183],46
L 3
v_exp_f32 v105,v105
M 30,v[184:187],50
s_nop 0
R 70,2,31744
v_fma_f32 v103,v103,v77,v77
M 34,v[188:191],50
s_waitcnt vmcnt(2)
v_rcp_f32 v102,v102
M 38,v[192:195],50
s_barrier
R 78,13,2048
ds_read_b32 v7,v6 offset:1024
M 30,v[196:199],54
R 42,1,0
v_fma_f32 v104,v104,v77,v77
M 34,v[200:203],54
buffer_store_dword v4,v3,s[36:39],s70 offen offset:0 sc0 sc1
M 38,v[204:207],54
L 5
v_rcp_f32 v103,v103
M 30,v[208:211],58
s_mov_b32 s70,s69
R 46,1,1024
v_fma_f32 v105,v105,v77,v77
M 34,v[212:215],58
s_mov_b32 s69,s68
v_rcp_f32 v104,v104
M 38,v[216:219],58
L 2
v_rcp_f32 v105,v105
M 30,v[220:223],62
s_add_u32 s68,s68,s63
R 50,1,2048
v_mul_f32 v26,v26,v102
M 34,v[224:227],62
v_mul_f32 v27,v27,v103
v_mul_f32 v28,v28,v104
M 38,v[228:231],62
s_nop 0
v_mul_f32 v29,v29,v105
v_cvt_pk_bf16_f32 v110,v26,v27
M 30,v[232:235],66
R 54,1,3072
v_cvt_pk_bf16_f32 v111,v28,v29
M 34,v[236:239],66
v_permlane16_swap_b32 v110,v112
M 38,v[240:243],66
v_xor_b32 v2,65536,v2
v_cmp_eq_u32 vcc,s53,v7
M 30,v[244:247],70
s_cmp_eq_u64 vcc,-1
R 58,1,4096
M 34,v[248:251],70
v_permlane16_swap_b32 v111,v113
M 38,v[252:255],70
L 3
buffer_store_dwordx4 v[110:113],v10,s[36:39],s66 offen offset:0 sc0 sc1
s_cbranch_scc1 Lg3_L20_%=
s_cmp_eq_u32 vcc_lo,-1
s_cbranch_scc1 Lg3_L10_%=
Lg3_X0_%=:
v_mfma_f32_16x16x32_bf16 v[18:21],a[0:3],v[42:45],0
s_add_u32 m0,s59,0
s_waitcnt vmcnt(3)
R 62,1,5120
v_mfma_f32_16x16x32_bf16 v[22:25],a[4:7],v[42:45],0
s_nop 0
buffer_load_dword v5,s[36:39],s58 offen offset:0 sc1 lds
v_mfma_f32_16x16x32_bf16 v[26:29],a[8:11],v[42:45],0
s_mov_b32 s66,s65
v_add_f32_dpp v115,v114,v114 quad_perm:[1,0,3,2] row_mask:0xf bank_mask:0xf
v_fma_f32 v30,-v78,v82,v30
M 18,a[12:15],46
s_add_u32 s65,s65,s62
R 66,1,6144
v_fma_f32 v31,-v78,v83,v31
M 22,a[16:19],46
v_add_f32_dpp v115,v115,v115 quad_perm:[2,3,0,1] row_mask:0xf bank_mask:0xf
v_fma_f32 v32,-v78,v84,v32
M 26,a[20:23],46
L 3
v_fma_f32 v33,-v78,v85,v33
v_mul_f32 v94,v30,v30
M 18,a[24:27],50
R 70,1,7168
v_add_f32_dpp v118,v115,v115 row_half_mirror row_mask:0xf bank_mask:0xf
M 22,a[28:31],50
v_mul_f32 v95,v31,v31
v_mul_f32 v96,v32,v32
M 26,a[32:35],50
v_mov_b32_dpp v115,v118 row_ror:8 row_mask:0xf bank_mask:0xf
v_fma_f32 v115,-v118,v118,v115
M 18,a[36:39],54
R 42,1,8192
v_max_f32 v115,s87,v115
M 22,a[40:43],54
v_rsq_f32 v116,v115
M 26,a[44:47],54
L 3
v_mul_f32 v121,v115,v116
v_mul_f32 v119,s88,v116
M 18,a[48:51],58
R 46,1,9216
v_mul_f32 v115,s89,v116
M 22,a[52:55],58
v_mul_f32 v120,v119,v116
v_mul_f32 v120,v120,v115
M 26,a[56:59],58
ds_write_b128 v12,v[118:121] offset:0
M 18,a[60:63],62
R 50,1,10240
v_mul_f32 v97,v33,v33
M 22,a[64:67],62
v_fma_f32 v94,v80,v94,v79
v_fma_f32 v95,v80,v95,v79
M 26,a[68:71],62
L 4
v_fma_f32 v96,v80,v96,v79
v_fma_f32 v97,v80,v97,v79
M 18,a[72:75],66
R 54,1,11264
v_mul_f32 v94,v30,v94
M 22,a[76:79],66
v_exp_f32 v94,v94
M 26,a[80:83],66
v_mul_f32 v95,v31,v95
v_mul_f32 v96,v32,v96
M 18,a[84:87],70
R 58,1,12288
v_mul_f32 v97,v33,v97
M 22,a[88:91],70
v_exp_f32 v95,v95
M 26,a[92:95],70
L 4
v_exp_f32 v96,v96
M 18,a[96:99],42
R 62,1,13312
v_fma_f32 v94,v94,v81,v81
M 22,a[100:103],42
v_exp_f32 v97,v97
M 26,a[104:107],42
v_rcp_f32 v94,v94
M 18,a[108:111],46
R 66,1,14336
v_fma_f32 v95,v95,v81,v81
M 22,a[112:115],46
v_rcp_f32 v95,v95
M 26,a[116:119],46
L 3
v_fma_f32 v96,v96,v81,v81
v_fma_f32 v97,v97,v81,v81
M 18,a[120:123],50
R 70,1,15360
v_mul_f32 v30,v30,v94
M 22,a[124:127],50
v_rcp_f32 v96,v96
M 26,a[128:131],50
v_rcp_f32 v97,v97
M 18,a[132:135],54
R 42,1,16384
v_mul_f32 v31,v31,v95
M 22,a[136:139],54
v_mul_f32 v32,v32,v96
v_mul_f32 v33,v33,v97
M 26,a[140:143],54
L 3
v_cvt_pk_bf16_f32 v106,v30,v31
v_cvt_pk_bf16_f32 v107,v32,v33
M 18,a[144:147],58
R 46,1,17408
v_fma_f32 v34,-v78,v86,v34
M 22,a[148:151],58
v_fma_f32 v35,-v78,v87,v35
v_fma_f32 v36,-v78,v88,v36
M 26,a[152:155],58
v_fma_f32 v37,-v78,v89,v37
v_mul_f32 v98,v34,v34
M 18,a[156:159],62
R 50,1,18432
v_mul_f32 v99,v35,v35
M 22,a[160:163],62
v_mul_f32 v100,v36,v36
v_mul_f32 v101,v37,v37
M 26,a[164:167],62
L 3
v_fma_f32 v98,v80,v98,v79
v_fma_f32 v99,v80,v99,v79
M 18,a[168:171],66
R 54,1,19456
v_fma_f32 v100,v80,v100,v79
M 22,a[172:175],66
v_fma_f32 v101,v80,v101,v79
v_mul_f32 v98,v34,v98
M 26,a[176:179],66
v_exp_f32 v98,v98
M 18,a[180:183],70
R 58,1,20480
v_mul_f32 v99,v35,v99
M 22,a[184:187],70
v_exp_f32 v99,v99
M 26,a[188:191],70
L 3
v_mul_f32 v100,v36,v100
v_mul_f32 v101,v37,v101
M 18,a[192:195],42
R 62,1,21504
v_fma_f32 v98,v98,v81,v81
M 22,a[196:199],42
v_exp_f32 v100,v100
M 26,a[200:203],42
v_exp_f32 v101,v101
M 18,a[204:207],46
R 66,1,22528
v_fma_f32 v99,v99,v81,v81
M 22,a[208:211],46
v_rcp_f32 v98,v98
M 26,a[212:215],46
L 3
v_rcp_f32 v99,v99
M 18,a[216:219],50
R 70,1,23552
v_fma_f32 v100,v100,v81,v81
M 22,a[220:223],50
v_rcp_f32 v100,v100
M 26,a[224:227],50
v_fma_f32 v101,v101,v81,v81
v_mul_f32 v34,v34,v98
M 18,a[228:231],54
R 42,1,24576
v_mul_f32 v35,v35,v99
M 22,a[232:235],54
v_rcp_f32 v101,v101
M 26,a[236:239],54
L 3
v_mul_f32 v36,v36,v100
v_mul_f32 v37,v37,v101
M 18,a[240:243],58
R 46,1,25600
v_cvt_pk_bf16_f32 v108,v34,v35
M 22,a[244:247],58
v_cvt_pk_bf16_f32 v109,v36,v37
v_fma_f32 v38,-v78,v90,v38
M 26,a[248:251],58
buffer_store_dwordx4 v[106:109],v0,s[36:39],s67 offen offset:0 sc0 sc1
v_fma_f32 v39,-v78,v91,v39
M 18,a[252:255],62
R 50,1,26624
v_fma_f32 v40,-v78,v92,v40
M 22,v[128:131],62
v_fma_f32 v41,-v78,v93,v41
v_mul_f32 v102,v38,v38
M 26,v[132:135],62
L 3
v_mul_f32 v103,v39,v39
v_mul_f32 v104,v40,v40
M 18,v[136:139],66
R 54,1,27648
v_mul_f32 v105,v41,v41
M 22,v[140:143],66
v_fma_f32 v102,v80,v102,v79
v_fma_f32 v103,v80,v103,v79
M 26,v[144:147],66
v_fma_f32 v104,v80,v104,v79
v_fma_f32 v105,v80,v105,v79
M 18,v[148:151],70
R 58,1,28672
v_mul_f32 v102,v38,v102
M 22,v[152:155],70
v_exp_f32 v102,v102
M 26,v[156:159],70
L 3
v_mul_f32 v103,v39,v103
v_mul_f32 v104,v40,v104
M 18,v[160:163],42
R 62,1,29696
v_mul_f32 v105,v41,v105
M 22,v[164:167],42
v_exp_f32 v103,v103
M 26,v[168:171],42
v_exp_f32 v104,v104
M 18,v[172:175],46
R 66,1,30720
v_fma_f32 v102,v102,v81,v81
M 22,v[176:179],46
v_exp_f32 v105,v105
M 26,v[180:183],46
L 3
v_rcp_f32 v102,v102
M 18,v[184:187],50
s_nop 0
R 70,1,31744
v_fma_f32 v103,v103,v81,v81
M 22,v[188:191],50
s_waitcnt vmcnt(1)
v_rcp_f32 v103,v103
M 26,v[192:195],50
s_barrier
R 74,13,0
ds_read_b32 v7,v6 offset:0
M 18,v[196:199],54
buffer_store_dword v4,v3,s[36:39],s70 offen offset:0 sc0 sc1
M 22,v[200:203],54
s_mov_b32 s70,s69
v_fma_f32 v104,v104,v81,v81
v_fma_f32 v105,v105,v81,v81
M 26,v[204:207],54
L 4
v_rcp_f32 v104,v104
M 18,v[208:211],58
s_mov_b32 s69,s68
v_rcp_f32 v105,v105
M 22,v[212:215],58
L 0
v_mul_f32 v38,v38,v102
v_mul_f32 v39,v39,v103
M 26,v[216:219],58
s_add_u32 s68,s68,s63
v_mul_f32 v40,v40,v104
v_mul_f32 v41,v41,v105
M 18,v[220:223],62
v_cvt_pk_bf16_f32 v110,v38,v39
v_cvt_pk_bf16_f32 v111,v40,v41
M 22,v[224:227],62
v_xor_b32 v1,65536,v1
v_cmp_eq_u32 vcc,s53,v7
M 26,v[228:231],62
s_nop 0
v_permlane16_swap_b32 v110,v112
M 18,v[232:235],66
v_permlane16_swap_b32 v111,v113
M 22,v[236:239],66
buffer_store_dwordx4 v[110:113],v10,s[36:39],s67 offen offset:0 sc0 sc1
M 26,v[240:243],66
M 18,v[244:247],70
M 22,v[248:251],70
M 26,v[252:255],70
s_mov_b32 s80,1
s_cmp_eq_u32 vcc_lo,-1
s_cbranch_scc0 Lg3_xn0_%=
buffer_load_dword v114,v11,s[36:39],s73 offen offset:0 sc1
s_nop 0
s_nop 0
v_fma_f32 v18,-v74,v82,v18
v_fma_f32 v19,-v74,v83,v19
v_fma_f32 v20,-v74,v84,v20
v_fma_f32 v21,-v74,v85,v21
v_mul_f32 v94,v18,v18
v_mul_f32 v95,v19,v19
v_mul_f32 v96,v20,v20
v_mul_f32 v97,v21,v21
s_add_u32 s73,s73,s64
s_mov_b32 m0,s57
s_add_u32 s56,s55,4096
D 0,55,0
v_fma_f32 v94,v76,v94,v75
v_fma_f32 v95,v76,v95,v75
v_fma_f32 v96,v76,v96,v75
v_fma_f32 v97,v76,v97,v75
v_mul_f32 v94,v18,v94
v_mul_f32 v95,v19,v95
v_mul_f32 v96,v20,v96
v_mul_f32 v97,v21,v97
D 0,55,1024
v_exp_f32 v94,v94
v_exp_f32 v95,v95
v_exp_f32 v96,v96
v_exp_f32 v97,v97
v_fma_f32 v94,v94,v77,v77
v_fma_f32 v95,v95,v77,v77
v_fma_f32 v96,v96,v77,v77
v_fma_f32 v97,v97,v77,v77
D 0,55,2048
v_rcp_f32 v94,v94
v_rcp_f32 v95,v95
v_rcp_f32 v96,v96
v_rcp_f32 v97,v97
v_mul_f32 v18,v18,v94
v_mul_f32 v19,v19,v95
v_mul_f32 v20,v20,v96
v_mul_f32 v21,v21,v97
D 0,55,3072
v_cvt_pk_bf16_f32 v106,v18,v19
v_cvt_pk_bf16_f32 v107,v20,v21
v_fma_f32 v22,-v74,v86,v22
v_fma_f32 v23,-v74,v87,v23
v_fma_f32 v24,-v74,v88,v24
v_fma_f32 v25,-v74,v89,v25
v_mul_f32 v98,v22,v22
v_mul_f32 v99,v23,v23
s_add_u32 m0,s57,4096
s_nop 0
D 0,56,0
v_mul_f32 v100,v24,v24
v_mul_f32 v101,v25,v25
)"
R"(v_fma_f32 v98,v76,v98,v75
v_fma_f32 v99,v76,v99,v75
v_fma_f32 v100,v76,v100,v75
v_fma_f32 v101,v76,v101,v75
v_mul_f32 v98,v22,v98
v_mul_f32 v99,v23,v99
D 0,56,1024
v_mul_f32 v100,v24,v100
v_mul_f32 v101,v25,v101
v_exp_f32 v98,v98
v_exp_f32 v99,v99
v_exp_f32 v100,v100
v_exp_f32 v101,v101
v_fma_f32 v98,v98,v77,v77
v_fma_f32 v99,v99,v77,v77
D 0,56,2048
v_fma_f32 v100,v100,v77,v77
v_fma_f32 v101,v101,v77,v77
v_rcp_f32 v98,v98
v_rcp_f32 v99,v99
v_rcp_f32 v100,v100
v_rcp_f32 v101,v101
v_mul_f32 v22,v22,v98
v_mul_f32 v23,v23,v99
D 0,56,3072
v_mul_f32 v24,v24,v100
v_mul_f32 v25,v25,v101
v_cvt_pk_bf16_f32 v108,v22,v23
v_cvt_pk_bf16_f32 v109,v24,v25
buffer_store_dwordx4 v[106:109],v0,s[36:39],s66 offen offset:0 sc0 sc1
v_fma_f32 v26,-v74,v90,v26
v_fma_f32 v27,-v74,v91,v27
v_fma_f32 v28,-v74,v92,v28
v_fma_f32 v29,-v74,v93,v29
s_add_u32 s55,s55,s60
s_add_u32 s57,s57,32768
s_and_b32 s57,s57,131071
s_add_u32 s54,s54,s46
v_mul_f32 v102,v26,v26
v_mul_f32 v103,v27,v27
v_mul_f32 v104,v28,v28
v_mul_f32 v105,v29,v29
v_fma_f32 v102,v76,v102,v75
v_fma_f32 v103,v76,v103,v75
v_fma_f32 v104,v76,v104,v75
v_fma_f32 v105,v76,v105,v75
v_mul_f32 v102,v26,v102
v_mul_f32 v103,v27,v103
v_mul_f32 v104,v28,v104
v_mul_f32 v105,v29,v105
v_exp_f32 v102,v102
v_exp_f32 v103,v103
v_exp_f32 v104,v104
v_exp_f32 v105,v105
v_fma_f32 v102,v102,v77,v77
v_fma_f32 v103,v103,v77,v77
v_fma_f32 v104,v104,v77,v77
v_fma_f32 v105,v105,v77,v77
v_rcp_f32 v102,v102
v_rcp_f32 v103,v103
v_rcp_f32 v104,v104
v_rcp_f32 v105,v105
v_mul_f32 v26,v26,v102
v_mul_f32 v27,v27,v103
v_mul_f32 v28,v28,v104
v_mul_f32 v29,v29,v105
v_cvt_pk_bf16_f32 v110,v26,v27
v_cvt_pk_bf16_f32 v111,v28,v29
s_nop 0
v_permlane16_swap_b32 v110,v112
v_permlane16_swap_b32 v111,v113
buffer_store_dwordx4 v[110:113],v10,s[36:39],s66 offen offset:0 sc0 sc1
s_mov_b32 s72,s69
s_mov_b32 s69,0x7fff0000
s_mov_b32 s66,0x7fff0000
s_mov_b32 s76,0x7fff0000
s_branch Lg3_gox_%=
Lg3_xn0_%=:
s_cmp_ge_u32 s54,256
s_cbranch_scc1 Lg3_fin0_%=
s_add_u32 m0,s59,4096
s_nop 0
buffer_load_dword v5,s[36:39],s58 offen offset:0 sc1 lds
buffer_store_dword v4,v3,s[36:39],s71 offen offset:0 sc0 sc1
s_add_u32 m0,s59,5120
s_nop 0
buffer_load_dword v5,s[36:39],s58 offen offset:0 sc1 lds
buffer_store_dword v4,v3,s[36:39],s71 offen offset:0 sc0 sc1
s_mov_b32 s83,0
s_nop 0
v_fma_f32 v18,-v74,v82,v18
v_fma_f32 v19,-v74,v83,v19
v_fma_f32 v20,-v74,v84,v20
v_fma_f32 v21,-v74,v85,v21
v_mul_f32 v94,v18,v18
v_mul_f32 v95,v19,v19
v_mul_f32 v96,v20,v20
v_mul_f32 v97,v21,v21
v_fma_f32 v94,v76,v94,v75
v_fma_f32 v95,v76,v95,v75
v_fma_f32 v96,v76,v96,v75
v_fma_f32 v97,v76,v97,v75
v_mul_f32 v94,v18,v94
v_mul_f32 v95,v19,v95
v_mul_f32 v96,v20,v96
v_mul_f32 v97,v21,v97
v_exp_f32 v94,v94
v_exp_f32 v95,v95
v_exp_f32 v96,v96
v_exp_f32 v97,v97
v_fma_f32 v94,v94,v77,v77
v_fma_f32 v95,v95,v77,v77
v_fma_f32 v96,v96,v77,v77
v_fma_f32 v97,v97,v77,v77
v_rcp_f32 v94,v94
v_rcp_f32 v95,v95
v_rcp_f32 v96,v96
v_rcp_f32 v97,v97
v_mul_f32 v18,v18,v94
v_mul_f32 v19,v19,v95
v_mul_f32 v20,v20,v96
v_mul_f32 v21,v21,v97
v_cvt_pk_bf16_f32 v106,v18,v19
v_cvt_pk_bf16_f32 v107,v20,v21
v_fma_f32 v22,-v74,v86,v22
v_fma_f32 v23,-v74,v87,v23
v_fma_f32 v24,-v74,v88,v24
v_fma_f32 v25,-v74,v89,v25
v_mul_f32 v98,v22,v22
v_mul_f32 v99,v23,v23
v_mul_f32 v100,v24,v24
v_mul_f32 v101,v25,v25
v_fma_f32 v98,v76,v98,v75
v_fma_f32 v99,v76,v99,v75
v_fma_f32 v100,v76,v100,v75
v_fma_f32 v101,v76,v101,v75
v_mul_f32 v98,v22,v98
v_mul_f32 v99,v23,v99
v_mul_f32 v100,v24,v100
v_mul_f32 v101,v25,v101
v_exp_f32 v98,v98
v_exp_f32 v99,v99
v_exp_f32 v100,v100
v_exp_f32 v101,v101
v_fma_f32 v98,v98,v77,v77
v_fma_f32 v99,v99,v77,v77
v_fma_f32 v100,v100,v77,v77
v_fma_f32 v101,v101,v77,v77
v_rcp_f32 v98,v98
v_rcp_f32 v99,v99
v_rcp_f32 v100,v100
v_rcp_f32 v101,v101
v_mul_f32 v22,v22,v98
v_mul_f32 v23,v23,v99
v_mul_f32 v24,v24,v100
v_mul_f32 v25,v25,v101
v_cvt_pk_bf16_f32 v108,v22,v23
v_cvt_pk_bf16_f32 v109,v24,v25
buffer_store_dwordx4 v[106:109],v0,s[36:39],s66 offen offset:0 sc0 sc1
v_fma_f32 v26,-v74,v90,v26
v_fma_f32 v27,-v74,v91,v27
v_fma_f32 v28,-v74,v92,v28
v_fma_f32 v29,-v74,v93,v29
v_mul_f32 v102,v26,v26
v_mul_f32 v103,v27,v27
v_mul_f32 v104,v28,v28
v_mul_f32 v105,v29,v29
v_fma_f32 v102,v76,v102,v75
v_fma_f32 v103,v76,v103,v75
v_fma_f32 v104,v76,v104,v75
v_fma_f32 v105,v76,v105,v75
v_mul_f32 v102,v26,v102
v_mul_f32 v103,v27,v103
v_mul_f32 v104,v28,v104
v_mul_f32 v105,v29,v105
v_exp_f32 v102,v102
v_exp_f32 v103,v103
v_exp_f32 v104,v104
v_exp_f32 v105,v105
v_fma_f32 v102,v102,v77,v77
v_fma_f32 v103,v103,v77,v77
v_fma_f32 v104,v104,v77,v77
v_fma_f32 v105,v105,v77,v77
v_rcp_f32 v102,v102
v_rcp_f32 v103,v103
v_rcp_f32 v104,v104
v_rcp_f32 v105,v105
v_mul_f32 v26,v26,v102
v_mul_f32 v27,v27,v103
v_mul_f32 v28,v28,v104
v_mul_f32 v29,v29,v105
v_cvt_pk_bf16_f32 v110,v26,v27
v_cvt_pk_bf16_f32 v111,v28,v29
s_nop 0
v_permlane16_swap_b32 v110,v112
v_permlane16_swap_b32 v111,v113
buffer_store_dwordx4 v[110:113],v10,s[36:39],s66 offen offset:0 sc0 sc1
s_mov_b32 s72,s69
s_mov_b32 s69,0x7fff0000
s_mov_b32 s66,0x7fff0000
s_mov_b32 s76,0x7fff0000
s_branch Lg3_poll_%=
Lg3_fin0_%=:
s_nop 0
v_fma_f32 v18,-v74,v82,v18
v_fma_f32 v19,-v74,v83,v19
v_fma_f32 v20,-v74,v84,v20
v_fma_f32 v21,-v74,v85,v21
v_mul_f32 v94,v18,v18
v_mul_f32 v95,v19,v19
v_mul_f32 v96,v20,v20
v_mul_f32 v97,v21,v21
v_fma_f32 v94,v76,v94,v75
v_fma_f32 v95,v76,v95,v75
v_fma_f32 v96,v76,v96,v75
v_fma_f32 v97,v76,v97,v75
v_mul_f32 v94,v18,v94
v_mul_f32 v95,v19,v95
v_mul_f32 v96,v20,v96
v_mul_f32 v97,v21,v97
v_exp_f32 v94,v94
v_exp_f32 v95,v95
v_exp_f32 v96,v96
v_exp_f32 v97,v97
v_fma_f32 v94,v94,v77,v77
v_fma_f32 v95,v95,v77,v77
v_fma_f32 v96,v96,v77,v77
v_fma_f32 v97,v97,v77,v77
v_rcp_f32 v94,v94
v_rcp_f32 v95,v95
v_rcp_f32 v96,v96
v_rcp_f32 v97,v97
v_mul_f32 v18,v18,v94
v_mul_f32 v19,v19,v95
v_mul_f32 v20,v20,v96
v_mul_f32 v21,v21,v97
v_cvt_pk_bf16_f32 v106,v18,v19
v_cvt_pk_bf16_f32 v107,v20,v21
v_fma_f32 v22,-v74,v86,v22
v_fma_f32 v23,-v74,v87,v23
v_fma_f32 v24,-v74,v88,v24
v_fma_f32 v25,-v74,v89,v25
v_mul_f32 v98,v22,v22
v_mul_f32 v99,v23,v23
v_mul_f32 v100,v24,v24
v_mul_f32 v101,v25,v25
v_fma_f32 v98,v76,v98,v75
v_fma_f32 v99,v76,v99,v75
v_fma_f32 v100,v76,v100,v75
v_fma_f32 v101,v76,v101,v75
v_mul_f32 v98,v22,v98
v_mul_f32 v99,v23,v99
v_mul_f32 v100,v24,v100
v_mul_f32 v101,v25,v101
v_exp_f32 v98,v98
v_exp_f32 v99,v99
v_exp_f32 v100,v100
v_exp_f32 v101,v101
v_fma_f32 v98,v98,v77,v77
v_fma_f32 v99,v99,v77,v77
v_fma_f32 v100,v100,v77,v77
v_fma_f32 v101,v101,v77,v77
v_rcp_f32 v98,v98
v_rcp_f32 v99,v99
v_rcp_f32 v100,v100
v_rcp_f32 v101,v101
v_mul_f32 v22,v22,v98
v_mul_f32 v23,v23,v99
v_mul_f32 v24,v24,v100
v_mul_f32 v25,v25,v101
v_cvt_pk_bf16_f32 v108,v22,v23
v_cvt_pk_bf16_f32 v109,v24,v25
buffer_store_dwordx4 v[106:109],v0,s[36:39],s66 offen offset:0 sc0 sc1
v_fma_f32 v26,-v74,v90,v26
v_fma_f32 v27,-v74,v91,v27
v_fma_f32 v28,-v74,v92,v28
v_fma_f32 v29,-v74,v93,v29
v_mul_f32 v102,v26,v26
v_mul_f32 v103,v27,v27
v_mul_f32 v104,v28,v28
v_mul_f32 v105,v29,v29
v_fma_f32 v102,v76,v102,v75
v_fma_f32 v103,v76,v103,v75
v_fma_f32 v104,v76,v104,v75
v_fma_f32 v105,v76,v105,v75
v_mul_f32 v102,v26,v102
v_mul_f32 v103,v27,v103
v_mul_f32 v104,v28,v104
v_mul_f32 v105,v29,v105
v_exp_f32 v102,v102
v_exp_f32 v103,v103
v_exp_f32 v104,v104
v_exp_f32 v105,v105
v_fma_f32 v102,v102,v77,v77
v_fma_f32 v103,v103,v77,v77
v_fma_f32 v104,v104,v77,v77
v_fma_f32 v105,v105,v77,v77
v_rcp_f32 v102,v102
v_rcp_f32 v103,v103
v_rcp_f32 v104,v104
v_rcp_f32 v105,v105
v_mul_f32 v26,v26,v102
v_mul_f32 v27,v27,v103
v_mul_f32 v28,v28,v104
v_mul_f32 v29,v29,v105
v_cvt_pk_bf16_f32 v110,v26,v27
v_cvt_pk_bf16_f32 v111,v28,v29
s_nop 0
v_permlane16_swap_b32 v110,v112
v_permlane16_swap_b32 v111,v113
buffer_store_dwordx4 v[110:113],v10,s[36:39],s66 offen offset:0 sc0 sc1
s_mov_b32 s72,s69
s_mov_b32 s69,0x7fff0000
s_mov_b32 s66,0x7fff0000
s_mov_b32 s76,0x7fff0000
s_branch Lg3_done_%=
Lg3_L21_%=:
v_mfma_f32_16x16x32_bf16 v[30:33],a[0:3],v[42:45],0
s_add_u32 s58,s58,s61
s_add_u32 s58,s58,s61
R 62,2,5120
v_mfma_f32_16x16x32_bf16 v[34:37],a[4:7],v[42:45],0
s_add_u32 m0,s59,1024
s_add_u32 s56,s55,4096
s_waitcnt vmcnt(3)
v_mfma_f32_16x16x32_bf16 v[38:41],a[8:11],v[42:45],0
s_nop 0
buffer_load_dword v8,s[36:39],s58 offen offset:0 sc1 lds
M 30,a[12:15],46
s_mov_b32 m0,s57
R 66,2,6144
v_add_f32_dpp v115,v114,v114 quad_perm:[1,0,3,2] row_mask:0xf bank_mask:0xf
M 34,a[16:19],46
s_mov_b32 s67,s65
buffer_load_dword v114,v11,s[36:39],s73 offen offset:0 sc1
M 38,a[20:23],46
L 3
v_add_f32_dpp v115,v115,v115 quad_perm:[2,3,0,1] row_mask:0xf bank_mask:0xf
v_fma_f32 v18,-v74,v82,v18
v_fma_f32 v19,-v74,v83,v19
M 30,a[24:27],50
R 70,2,7168
v_fma_f32 v20,-v74,v84,v20
M 34,a[28:31],50
D 0,55,0
M 38,a[32:35],50
v_add_f32_dpp v118,v115,v115 row_half_mirror row_mask:0xf bank_mask:0xf
v_fma_f32 v21,-v74,v85,v21
M 30,a[36:39],54
R 42,2,8192
v_mul_f32 v94,v18,v18
M 34,a[40:43],54
v_mov_b32_dpp v115,v118 row_ror:8 row_mask:0xf bank_mask:0xf
v_fma_f32 v115,-v118,v118,v115
M 38,a[44:47],54
L 3
D 0,55,1024
v_max_f32 v115,s87,v115
M 30,a[48:51],58
R 46,2,9216
v_mul_f32 v95,v19,v19
M 34,a[52:55],58
v_rsq_f32 v116,v115
M 38,a[56:59],58
v_mul_f32 v121,v115,v116
v_mul_f32 v119,s88,v116
M 30,a[60:63],62
R 50,2,10240
v_mul_f32 v115,s89,v116
M 34,a[64:67],62
D 0,55,2048
M 38,a[68:71],62
L 3
v_mul_f32 v120,v119,v116
v_mul_f32 v120,v120,v115
v_mul_f32 v96,v20,v20
M 30,a[72:75],66
R 54,2,11264
v_mul_f32 v97,v21,v21
M 34,a[76:79],66
ds_write_b128 v12,v[118:121] offset:2048
M 38,a[80:83],66
D 0,55,3072
M 30,a[84:87],70
s_add_u32 m0,s57,4096
R 58,2,12288
v_fma_f32 v94,v76,v94,v75
M 34,a[88:91],70
s_add_u32 s55,s55,s60
v_fma_f32 v95,v76,v95,v75
v_fma_f32 v96,v76,v96,v75
v_fma_f32 v97,v76,v97,v75
M 38,a[92:95],70
L 4
D 0,56,0
M 30,a[96:99],42
s_add_u32 s57,s57,32768
R 62,2,13312
v_mul_f32 v94,v18,v94
M 34,a[100:103],42
s_and_b32 s57,s57,131071
v_exp_f32 v94,v94
M 38,a[104:107],42
s_add_u32 s54,s54,s46
v_mul_f32 v95,v19,v95
v_mul_f32 v96,v20,v96
M 30,a[108:111],46
R 66,2,14336
v_mul_f32 v97,v21,v97
M 34,a[112:115],46
D 0,56,1024
v_fma_f32 v94,v94,v77,v77
M 38,a[116:119],46
L 4
v_exp_f32 v95,v95
M 30,a[120:123],50
R 70,2,15360
v_fma_f32 v95,v95,v77,v77
M 34,a[124:127],50
D 0,56,2048
M 38,a[128:131],50
v_exp_f32 v96,v96
M 30,a[132:135],54
R 42,2,16384
v_fma_f32 v96,v96,v77,v77
M 34,a[136:139],54
v_exp_f32 v97,v97
v_fma_f32 v22,-v74,v86,v22
M 38,a[140:143],54
L 3
D 0,56,3072
M 30,a[144:147],58
s_mov_b32 m0,s57
R 46,2,17408
v_fma_f32 v97,v97,v77,v77
M 34,a[148:151],58
s_add_u32 s56,s55,4096
v_rcp_f32 v94,v94
M 38,a[152:155],58
v_rcp_f32 v95,v95
M 30,a[156:159],62
R 50,2,18432
v_rcp_f32 v96,v96
M 34,a[160:163],62
D 0,55,0
M 38,a[164:167],62
L 3
v_rcp_f32 v97,v97
M 30,a[168:171],66
R 54,2,19456
v_mul_f32 v18,v18,v94
M 34,a[172:175],66
v_mul_f32 v19,v19,v95
v_mul_f32 v20,v20,v96
M 38,a[176:179],66
v_mul_f32 v21,v21,v97
v_cvt_pk_bf16_f32 v106,v18,v19
M 30,a[180:183],70
R 58,2,20480
v_cvt_pk_bf16_f32 v107,v20,v21
v_fma_f32 v23,-v74,v87,v23
M 34,a[184:187],70
D 0,55,1024
M 38,a[188:191],70
L 3
v_fma_f32 v24,-v74,v88,v24
v_fma_f32 v25,-v74,v89,v25
M 30,a[192:195],42
R 62,2,21504
v_mul_f32 v98,v22,v22
M 34,a[196:199],42
v_mul_f32 v99,v23,v23
v_mul_f32 v100,v24,v24
M 38,a[200:203],42
v_mul_f32 v101,v25,v25
v_fma_f32 v98,v76,v98,v75
M 30,a[204:207],46
R 66,2,22528
v_fma_f32 v99,v76,v99,v75
v_fma_f32 v100,v76,v100,v75
M 34,a[208:211],46
D 0,55,2048
M 38,a[212:215],46
L 3
v_fma_f32 v101,v76,v101,v75
v_mul_f32 v98,v22,v98
M 30,a[216:219],50
R 70,2,23552
v_mul_f32 v99,v23,v99
M 34,a[220:223],50
v_exp_f32 v98,v98
M 38,a[224:227],50
v_exp_f32 v99,v99
M 30,a[228:231],54
R 42,2,24576
v_mul_f32 v100,v24,v100
v_mul_f32 v101,v25,v101
M 34,a[232:235],54
v_exp_f32 v100,v100
M 38,a[236:239],54
)"
R"(L 3
D 0,55,3072
M 30,a[240:243],58
s_add_u32 m0,s57,4096
R 46,2,25600
v_fma_f32 v98,v98,v77,v77
M 34,a[244:247],58
s_add_u32 s55,s55,s60
v_exp_f32 v101,v101
M 38,a[248:251],58
s_add_u32 s57,s57,32768
v_fma_f32 v99,v99,v77,v77
v_rcp_f32 v98,v98
M 30,a[252:255],62
s_and_b32 s57,s57,131071
R 50,2,26624
v_fma_f32 v100,v100,v77,v77
M 34,v[128:131],62
s_add_u32 s54,s54,s46
v_rcp_f32 v99,v99
M 38,v[132:135],62
L 3
v_rcp_f32 v100,v100
M 30,v[136:139],66
s_add_u32 s73,s73,s64
R 54,2,27648
v_fma_f32 v101,v101,v77,v77
M 34,v[140:143],66
s_add_u32 s65,s65,s62
D 0,56,0
M 38,v[144:147],66
v_rcp_f32 v101,v101
v_mul_f32 v22,v22,v98
M 30,v[148:151],70
R 58,2,28672
v_mul_f32 v23,v23,v99
M 34,v[152:155],70
v_mul_f32 v24,v24,v100
v_mul_f32 v25,v25,v101
M 38,v[156:159],70
L 3
v_cvt_pk_bf16_f32 v108,v22,v23
v_cvt_pk_bf16_f32 v109,v24,v25
M 30,v[160:163],42
R 62,2,29696
v_fma_f32 v26,-v74,v90,v26
M 34,v[164:167],42
D 0,56,1024
M 38,v[168:171],42
buffer_store_dwordx4 v[106:109],v0,s[36:39],s66 offen offset:0 sc0 sc1
v_fma_f32 v27,-v74,v91,v27
M 30,v[172:175],46
R 66,2,30720
v_fma_f32 v28,-v74,v92,v28
M 34,v[176:179],46
v_fma_f32 v29,-v74,v93,v29
v_mul_f32 v102,v26,v26
M 38,v[180:183],46
L 3
v_mul_f32 v103,v27,v27
v_mul_f32 v104,v28,v28
M 30,v[184:187],50
s_nop 0
R 70,2,31744
v_mul_f32 v105,v29,v29
M 34,v[188:191],50
s_waitcnt vmcnt(7)
v_fma_f32 v102,v76,v102,v75
v_fma_f32 v103,v76,v103,v75
v_fma_f32 v104,v76,v104,v75
M 38,v[192:195],50
s_barrier
R 78,13,2048
ds_read_b32 v7,v6 offset:1024
M 30,v[196:199],54
R 42,1,0
v_fma_f32 v105,v76,v105,v75
M 34,v[200:203],54
buffer_store_dword v4,v3,s[36:39],s70 offen offset:0 sc0 sc1
M 38,v[204:207],54
L 5
D 0,56,2048
M 30,v[208:211],58
s_mov_b32 s70,s69
R 46,1,1024
v_mul_f32 v102,v26,v102
M 34,v[212:215],58
s_mov_b32 s69,s68
v_mul_f32 v103,v27,v103
v_exp_f32 v102,v102
M 38,v[216:219],58
L 2
v_mul_f32 v104,v28,v104
v_mul_f32 v105,v29,v105
M 30,v[220:223],62
s_add_u32 s68,s68,s63
R 50,1,2048
v_fma_f32 v102,v102,v77,v77
M 34,v[224:227],62
D 0,56,3072
M 38,v[228:231],62
s_nop 0
v_exp_f32 v103,v103
M 30,v[232:235],66
R 54,1,3072
v_exp_f32 v104,v104
v_exp_f32 v105,v105
M 34,v[236:239],66
v_fma_f32 v103,v103,v77,v77
v_fma_f32 v104,v104,v77,v77
v_fma_f32 v105,v105,v77,v77
v_rcp_f32 v102,v102
v_rcp_f32 v103,v103
v_rcp_f32 v104,v104
v_rcp_f32 v105,v105
M 38,v[240:243],66
v_mul_f32 v26,v26,v102
v_mul_f32 v27,v27,v103
v_mul_f32 v28,v28,v104
v_mul_f32 v29,v29,v105
v_cvt_pk_bf16_f32 v110,v26,v27
v_cvt_pk_bf16_f32 v111,v28,v29
M 30,v[244:247],70
R 58,1,4096
v_cmp_eq_u32 vcc,s53,v7
M 34,v[248:251],70
s_cmp_eq_u32 vcc_lo,-1
v_permlane16_swap_b32 v110,v112
v_permlane16_swap_b32 v111,v113
M 38,v[252:255],70
L 3
buffer_store_dwordx4 v[110:113],v10,s[36:39],s66 offen offset:0 sc0 sc1
v_xor_b32 v2,65536,v2
s_cbranch_scc1 Lg3_S0_%=
Lg3_N0_%=:
v_mfma_f32_16x16x32_bf16 v[18:21],a[0:3],v[42:45],0
s_add_u32 m0,s59,0
s_waitcnt vmcnt(11)
R 62,1,5120
v_mfma_f32_16x16x32_bf16 v[22:25],a[4:7],v[42:45],0
s_nop 0
buffer_load_dword v5,s[36:39],s58 offen offset:0 sc1 lds
v_mfma_f32_16x16x32_bf16 v[26:29],a[8:11],v[42:45],0
s_mov_b32 s66,s65
v_add_f32_dpp v115,v114,v114 quad_perm:[1,0,3,2] row_mask:0xf bank_mask:0xf
v_fma_f32 v30,-v78,v82,v30
M 18,a[12:15],46
R 66,1,6144
v_fma_f32 v31,-v78,v83,v31
M 22,a[16:19],46
buffer_load_dword v114,v11,s[36:39],s73 offen offset:0 sc1
M 26,a[20:23],46
L 3
v_add_f32_dpp v115,v115,v115 quad_perm:[2,3,0,1] row_mask:0xf bank_mask:0xf
v_fma_f32 v32,-v78,v84,v32
M 18,a[24:27],50
s_add_u32 s73,s73,s64
R 70,1,7168
v_fma_f32 v33,-v78,v85,v33
M 22,a[28:31],50
s_add_u32 s65,s65,s62
v_add_f32_dpp v118,v115,v115 row_half_mirror row_mask:0xf bank_mask:0xf
v_mul_f32 v94,v30,v30
M 26,a[32:35],50
v_mul_f32 v95,v31,v31
v_mul_f32 v96,v32,v32
M 18,a[36:39],54
R 42,1,8192
v_mov_b32_dpp v115,v118 row_ror:8 row_mask:0xf bank_mask:0xf
M 22,a[40:43],54
v_fma_f32 v115,-v118,v118,v115
v_max_f32 v115,s87,v115
M 26,a[44:47],54
L 3
v_rsq_f32 v116,v115
M 18,a[48:51],58
R 46,1,9216
v_mul_f32 v121,v115,v116
M 22,a[52:55],58
v_mul_f32 v119,s88,v116
v_mul_f32 v115,s89,v116
M 26,a[56:59],58
v_mul_f32 v120,v119,v116
v_mul_f32 v120,v120,v115
M 18,a[60:63],62
R 50,1,10240
v_mul_f32 v97,v33,v33
M 22,a[64:67],62
ds_write_b128 v12,v[118:121] offset:0
M 26,a[68:71],62
L 4
v_fma_f32 v94,v80,v94,v79
v_fma_f32 v95,v80,v95,v79
M 18,a[72:75],66
R 54,1,11264
v_fma_f32 v96,v80,v96,v79
M 22,a[76:79],66
v_fma_f32 v97,v80,v97,v79
v_mul_f32 v94,v30,v94
M 26,a[80:83],66
v_exp_f32 v94,v94
M 18,a[84:87],70
R 58,1,12288
v_mul_f32 v95,v31,v95
M 22,a[88:91],70
v_exp_f32 v95,v95
M 26,a[92:95],70
L 4
v_mul_f32 v96,v32,v96
v_mul_f32 v97,v33,v97
M 18,a[96:99],42
R 62,1,13312
v_fma_f32 v94,v94,v81,v81
M 22,a[100:103],42
v_exp_f32 v96,v96
M 26,a[104:107],42
v_exp_f32 v97,v97
M 18,a[108:111],46
R 66,1,14336
v_fma_f32 v95,v95,v81,v81
M 22,a[112:115],46
v_rcp_f32 v94,v94
M 26,a[116:119],46
L 3
v_rcp_f32 v95,v95
M 18,a[120:123],50
R 70,1,15360
v_fma_f32 v96,v96,v81,v81
M 22,a[124:127],50
v_rcp_f32 v96,v96
M 26,a[128:131],50
v_fma_f32 v97,v97,v81,v81
v_mul_f32 v30,v30,v94
M 18,a[132:135],54
R 42,1,16384
v_mul_f32 v31,v31,v95
M 22,a[136:139],54
v_rcp_f32 v97,v97
M 26,a[140:143],54
L 3
v_mul_f32 v32,v32,v96
v_mul_f32 v33,v33,v97
M 18,a[144:147],58
R 46,1,17408
v_cvt_pk_bf16_f32 v106,v30,v31
M 22,a[148:151],58
v_cvt_pk_bf16_f32 v107,v32,v33
v_fma_f32 v34,-v78,v86,v34
M 26,a[152:155],58
v_fma_f32 v35,-v78,v87,v35
v_fma_f32 v36,-v78,v88,v36
M 18,a[156:159],62
R 50,1,18432
v_fma_f32 v37,-v78,v89,v37
M 22,a[160:163],62
v_mul_f32 v98,v34,v34
v_mul_f32 v99,v35,v35
M 26,a[164:167],62
L 3
v_mul_f32 v100,v36,v36
v_mul_f32 v101,v37,v37
M 18,a[168:171],66
R 54,1,19456
v_fma_f32 v98,v80,v98,v79
M 22,a[172:175],66
v_fma_f32 v99,v80,v99,v79
v_fma_f32 v100,v80,v100,v79
M 26,a[176:179],66
v_fma_f32 v101,v80,v101,v79
v_mul_f32 v98,v34,v98
M 18,a[180:183],70
R 58,1,20480
v_mul_f32 v99,v35,v99
M 22,a[184:187],70
v_exp_f32 v98,v98
M 26,a[188:191],70
L 3
v_exp_f32 v99,v99
M 18,a[192:195],42
R 62,1,21504
v_mul_f32 v100,v36,v100
M 22,a[196:199],42
v_exp_f32 v100,v100
M 26,a[200:203],42
v_mul_f32 v101,v37,v101
v_fma_f32 v98,v98,v81,v81
M 18,a[204:207],46
R 66,1,22528
v_fma_f32 v99,v99,v81,v81
M 22,a[208:211],46
v_exp_f32 v101,v101
M 26,a[212:215],46
L 3
v_rcp_f32 v98,v98
M 18,a[216:219],50
R 70,1,23552
v_fma_f32 v100,v100,v81,v81
M 22,a[220:223],50
v_rcp_f32 v99,v99
M 26,a[224:227],50
v_rcp_f32 v100,v100
M 18,a[228:231],54
R 42,1,24576
v_fma_f32 v101,v101,v81,v81
M 22,a[232:235],54
v_rcp_f32 v101,v101
M 26,a[236:239],54
L 3
v_mul_f32 v34,v34,v98
v_mul_f32 v35,v35,v99
M 18,a[240:243],58
R 46,1,25600
v_mul_f32 v36,v36,v100
M 22,a[244:247],58
v_mul_f32 v37,v37,v101
v_cvt_pk_bf16_f32 v108,v34,v35
M 26,a[248:251],58
v_cvt_pk_bf16_f32 v109,v36,v37
v_fma_f32 v38,-v78,v90,v38
M 18,a[252:255],62
R 50,1,26624
v_fma_f32 v39,-v78,v91,v39
M 22,v[128:131],62
buffer_store_dwordx4 v[106:109],v0,s[36:39],s67 offen offset:0 sc0 sc1
v_fma_f32 v40,-v78,v92,v40
M 26,v[132:135],62
L 3
v_fma_f32 v41,-v78,v93,v41
v_mul_f32 v102,v38,v38
M 18,v[136:139],66
R 54,1,27648
v_mul_f32 v103,v39,v39
M 22,v[140:143],66
v_mul_f32 v104,v40,v40
v_mul_f32 v105,v41,v41
M 26,v[144:147],66
v_fma_f32 v102,v80,v102,v79
v_fma_f32 v103,v80,v103,v79
M 18,v[148:151],70
R 58,1,28672
v_fma_f32 v104,v80,v104,v79
M 22,v[152:155],70
v_fma_f32 v105,v80,v105,v79
v_mul_f32 v102,v38,v102
M 26,v[156:159],70
L 3
v_exp_f32 v102,v102
M 18,v[160:163],42
R 62,1,29696
v_mul_f32 v103,v39,v103
M 22,v[164:167],42
v_exp_f32 v103,v103
M 26,v[168:171],42
v_mul_f32 v104,v40,v104
v_mul_f32 v105,v41,v105
M 18,v[172:175],46
R 66,1,30720
v_fma_f32 v102,v102,v81,v81
M 22,v[176:179],46
v_exp_f32 v104,v104
M 26,v[180:183],46
L 3
v_exp_f32 v105,v105
M 18,v[184:187],50
s_nop 0
R 70,1,31744
v_fma_f32 v103,v103,v81,v81
M 22,v[188:191],50
s_waitcnt vmcnt(2)
v_rcp_f32 v102,v102
M 26,v[192:195],50
s_barrier
R 74,13,0
ds_read_b32 v7,v6 offset:0
M 18,v[196:199],54
R 42,2,0
v_fma_f32 v104,v104,v81,v81
M 22,v[200:203],54
buffer_store_dword v4,v3,s[36:39],s70 offen offset:0 sc0 sc1
M 26,v[204:207],54
L 5
v_rcp_f32 v103,v103
M 18,v[208:211],58
s_mov_b32 s70,s69
R 46,2,1024
v_fma_f32 v105,v105,v81,v81
M 22,v[212:215],58
s_mov_b32 s69,s68
v_rcp_f32 v104,v104
M 26,v[216:219],58
L 2
v_rcp_f32 v105,v105
M 18,v[220:223],62
s_add_u32 s68,s68,s63
R 50,2,2048
v_mul_f32 v38,v38,v102
M 22,v[224:227],62
v_mul_f32 v39,v39,v103
v_mul_f32 v40,v40,v104
M 26,v[228:231],62
s_nop 0
v_mul_f32 v41,v41,v105
v_cvt_pk_bf16_f32 v110,v38,v39
M 18,v[232:235],66
R 54,2,3072
v_cvt_pk_bf16_f32 v111,v40,v41
M 22,v[236:239],66
v_permlane16_swap_b32 v110,v112
M 26,v[240:243],66
v_xor_b32 v1,65536,v1
v_cmp_eq_u32 vcc,s53,v7
M 18,v[244:247],70
s_cmp_eq_u64 vcc,-1
R 58,2,4096
M 22,v[248:251],70
v_permlane16_swap_b32 v111,v113
M 26,v[252:255],70
L 3
buffer_store_dwordx4 v[110:113],v10,s[36:39],s67 offen offset:0 sc0 sc1
s_cbranch_scc1 Lg3_L21_%=
s_cmp_eq_u32 vcc_lo,-1
s_cbranch_scc1 Lg3_L11_%=
Lg3_X1_%=:
v_mfma_f32_16x16x32_bf16 v[30:33],a[0:3],v[42:45],0
s_add_u32 m0,s59,1024
s_waitcnt vmcnt(3)
R 62,2,5120
v_mfma_f32_16x16x32_bf16 v[34:37],a[4:7],v[42:45],0
s_nop 0
buffer_load_dword v5,s[36:39],s58 offen offset:0 sc1 lds
v_mfma_f32_16x16x32_bf16 v[38:41],a[8:11],v[42:45],0
s_mov_b32 s67,s65
v_add_f32_dpp v115,v114,v114 quad_perm:[1,0,3,2] row_mask:0xf bank_mask:0xf
v_fma_f32 v18,-v74,v82,v18
M 30,a[12:15],46
s_add_u32 s65,s65,s62
R 66,2,6144
v_fma_f32 v19,-v74,v83,v19
M 34,a[16:19],46
v_add_f32_dpp v115,v115,v115 quad_perm:[2,3,0,1] row_mask:0xf bank_mask:0xf
v_fma_f32 v20,-v74,v84,v20
M 38,a[20:23],46
L 3
v_fma_f32 v21,-v74,v85,v21
v_mul_f32 v94,v18,v18
M 30,a[24:27],50
R 70,2,7168
v_add_f32_dpp v118,v115,v115 row_half_mirror row_mask:0xf bank_mask:0xf
M 34,a[28:31],50
v_mul_f32 v95,v19,v19
v_mul_f32 v96,v20,v20
M 38,a[32:35],50
v_mov_b32_dpp v115,v118 row_ror:8 row_mask:0xf bank_mask:0xf
v_fma_f32 v115,-v118,v118,v115
M 30,a[36:39],54
R 42,2,8192
v_max_f32 v115,s87,v115
M 34,a[40:43],54
v_rsq_f32 v116,v115
M 38,a[44:47],54
L 3
v_mul_f32 v121,v115,v116
v_mul_f32 v119,s88,v116
M 30,a[48:51],58
R 46,2,9216
v_mul_f32 v115,s89,v116
M 34,a[52:55],58
v_mul_f32 v120,v119,v116
v_mul_f32 v120,v120,v115
M 38,a[56:59],58
ds_write_b128 v12,v[118:121] offset:2048
M 30,a[60:63],62
R 50,2,10240
v_mul_f32 v97,v21,v21
M 34,a[64:67],62
v_fma_f32 v94,v76,v94,v75
v_fma_f32 v95,v76,v95,v75
M 38,a[68:71],62
L 4
v_fma_f32 v96,v76,v96,v75
v_fma_f32 v97,v76,v97,v75
M 30,a[72:75],66
R 54,2,11264
v_mul_f32 v94,v18,v94
M 34,a[76:79],66
v_exp_f32 v94,v94
M 38,a[80:83],66
v_mul_f32 v95,v19,v95
v_mul_f32 v96,v20,v96
M 30,a[84:87],70
R 58,2,12288
v_mul_f32 v97,v21,v97
M 34,a[88:91],70
v_exp_f32 v95,v95
M 38,a[92:95],70
L 4
v_exp_f32 v96,v96
M 30,a[96:99],42
R 62,2,13312
v_fma_f32 v94,v94,v77,v77
)"
R"(M 34,a[100:103],42
v_exp_f32 v97,v97
M 38,a[104:107],42
v_rcp_f32 v94,v94
M 30,a[108:111],46
R 66,2,14336
v_fma_f32 v95,v95,v77,v77
M 34,a[112:115],46
v_rcp_f32 v95,v95
M 38,a[116:119],46
L 3
v_fma_f32 v96,v96,v77,v77
v_fma_f32 v97,v97,v77,v77
M 30,a[120:123],50
R 70,2,15360
v_mul_f32 v18,v18,v94
M 34,a[124:127],50
v_rcp_f32 v96,v96
M 38,a[128:131],50
v_rcp_f32 v97,v97
M 30,a[132:135],54
R 42,2,16384
v_mul_f32 v19,v19,v95
M 34,a[136:139],54
v_mul_f32 v20,v20,v96
v_mul_f32 v21,v21,v97
M 38,a[140:143],54
L 3
v_cvt_pk_bf16_f32 v106,v18,v19
v_cvt_pk_bf16_f32 v107,v20,v21
M 30,a[144:147],58
R 46,2,17408
v_fma_f32 v22,-v74,v86,v22
M 34,a[148:151],58
v_fma_f32 v23,-v74,v87,v23
v_fma_f32 v24,-v74,v88,v24
M 38,a[152:155],58
v_fma_f32 v25,-v74,v89,v25
v_mul_f32 v98,v22,v22
M 30,a[156:159],62
R 50,2,18432
v_mul_f32 v99,v23,v23
M 34,a[160:163],62
v_mul_f32 v100,v24,v24
v_mul_f32 v101,v25,v25
M 38,a[164:167],62
L 3
v_fma_f32 v98,v76,v98,v75
v_fma_f32 v99,v76,v99,v75
M 30,a[168:171],66
R 54,2,19456
v_fma_f32 v100,v76,v100,v75
M 34,a[172:175],66
v_fma_f32 v101,v76,v101,v75
v_mul_f32 v98,v22,v98
M 38,a[176:179],66
v_exp_f32 v98,v98
M 30,a[180:183],70
R 58,2,20480
v_mul_f32 v99,v23,v99
M 34,a[184:187],70
v_exp_f32 v99,v99
M 38,a[188:191],70
L 3
v_mul_f32 v100,v24,v100
v_mul_f32 v101,v25,v101
M 30,a[192:195],42
R 62,2,21504
v_fma_f32 v98,v98,v77,v77
M 34,a[196:199],42
v_exp_f32 v100,v100
M 38,a[200:203],42
v_exp_f32 v101,v101
M 30,a[204:207],46
R 66,2,22528
v_fma_f32 v99,v99,v77,v77
M 34,a[208:211],46
v_rcp_f32 v98,v98
M 38,a[212:215],46
L 3
v_rcp_f32 v99,v99
M 30,a[216:219],50
R 70,2,23552
v_fma_f32 v100,v100,v77,v77
M 34,a[220:223],50
v_rcp_f32 v100,v100
M 38,a[224:227],50
v_fma_f32 v101,v101,v77,v77
v_mul_f32 v22,v22,v98
M 30,a[228:231],54
R 42,2,24576
v_mul_f32 v23,v23,v99
M 34,a[232:235],54
v_rcp_f32 v101,v101
M 38,a[236:239],54
L 3
v_mul_f32 v24,v24,v100
v_mul_f32 v25,v25,v101
M 30,a[240:243],58
R 46,2,25600
v_cvt_pk_bf16_f32 v108,v22,v23
M 34,a[244:247],58
v_cvt_pk_bf16_f32 v109,v24,v25
v_fma_f32 v26,-v74,v90,v26
M 38,a[248:251],58
buffer_store_dwordx4 v[106:109],v0,s[36:39],s66 offen offset:0 sc0 sc1
v_fma_f32 v27,-v74,v91,v27
M 30,a[252:255],62
R 50,2,26624
v_fma_f32 v28,-v74,v92,v28
M 34,v[128:131],62
v_fma_f32 v29,-v74,v93,v29
v_mul_f32 v102,v26,v26
M 38,v[132:135],62
L 3
v_mul_f32 v103,v27,v27
v_mul_f32 v104,v28,v28
M 30,v[136:139],66
R 54,2,27648
v_mul_f32 v105,v29,v29
M 34,v[140:143],66
v_fma_f32 v102,v76,v102,v75
v_fma_f32 v103,v76,v103,v75
M 38,v[144:147],66
v_fma_f32 v104,v76,v104,v75
v_fma_f32 v105,v76,v105,v75
M 30,v[148:151],70
R 58,2,28672
v_mul_f32 v102,v26,v102
M 34,v[152:155],70
v_exp_f32 v102,v102
M 38,v[156:159],70
L 3
v_mul_f32 v103,v27,v103
v_mul_f32 v104,v28,v104
M 30,v[160:163],42
R 62,2,29696
v_mul_f32 v105,v29,v105
M 34,v[164:167],42
v_exp_f32 v103,v103
M 38,v[168:171],42
v_exp_f32 v104,v104
M 30,v[172:175],46
R 66,2,30720
v_fma_f32 v102,v102,v77,v77
M 34,v[176:179],46
v_exp_f32 v105,v105
M 38,v[180:183],46
L 3
v_rcp_f32 v102,v102
M 30,v[184:187],50
s_nop 0
R 70,2,31744
v_fma_f32 v103,v103,v77,v77
M 34,v[188:191],50
s_waitcnt vmcnt(1)
v_rcp_f32 v103,v103
M 38,v[192:195],50
s_barrier
R 78,13,2048
ds_read_b32 v7,v6 offset:1024
M 30,v[196:199],54
buffer_store_dword v4,v3,s[36:39],s70 offen offset:0 sc0 sc1
M 34,v[200:203],54
s_mov_b32 s70,s69
v_fma_f32 v104,v104,v77,v77
v_fma_f32 v105,v105,v77,v77
M 38,v[204:207],54
L 4
v_rcp_f32 v104,v104
M 30,v[208:211],58
s_mov_b32 s69,s68
v_rcp_f32 v105,v105
M 34,v[212:215],58
L 0
v_mul_f32 v26,v26,v102
v_mul_f32 v27,v27,v103
M 38,v[216:219],58
s_add_u32 s68,s68,s63
v_mul_f32 v28,v28,v104
v_mul_f32 v29,v29,v105
M 30,v[220:223],62
v_cvt_pk_bf16_f32 v110,v26,v27
v_cvt_pk_bf16_f32 v111,v28,v29
M 34,v[224:227],62
v_xor_b32 v2,65536,v2
v_cmp_eq_u32 vcc,s53,v7
M 38,v[228:231],62
s_nop 0
v_permlane16_swap_b32 v110,v112
M 30,v[232:235],66
v_permlane16_swap_b32 v111,v113
M 34,v[236:239],66
buffer_store_dwordx4 v[110:113],v10,s[36:39],s66 offen offset:0 sc0 sc1
M 38,v[240:243],66
M 30,v[244:247],70
M 34,v[248:251],70
M 38,v[252:255],70
s_mov_b32 s80,0
s_cmp_eq_u32 vcc_lo,-1
s_cbranch_scc0 Lg3_xn1_%=
buffer_load_dword v114,v11,s[36:39],s73 offen offset:0 sc1
s_nop 0
s_nop 0
v_fma_f32 v30,-v78,v82,v30
v_fma_f32 v31,-v78,v83,v31
v_fma_f32 v32,-v78,v84,v32
v_fma_f32 v33,-v78,v85,v33
v_mul_f32 v94,v30,v30
v_mul_f32 v95,v31,v31
v_mul_f32 v96,v32,v32
v_mul_f32 v97,v33,v33
s_add_u32 s73,s73,s64
s_mov_b32 m0,s57
s_add_u32 s56,s55,4096
D 0,55,0
v_fma_f32 v94,v80,v94,v79
v_fma_f32 v95,v80,v95,v79
v_fma_f32 v96,v80,v96,v79
v_fma_f32 v97,v80,v97,v79
v_mul_f32 v94,v30,v94
v_mul_f32 v95,v31,v95
v_mul_f32 v96,v32,v96
v_mul_f32 v97,v33,v97
D 0,55,1024
v_exp_f32 v94,v94
v_exp_f32 v95,v95
v_exp_f32 v96,v96
v_exp_f32 v97,v97
v_fma_f32 v94,v94,v81,v81
v_fma_f32 v95,v95,v81,v81
v_fma_f32 v96,v96,v81,v81
v_fma_f32 v97,v97,v81,v81
D 0,55,2048
v_rcp_f32 v94,v94
v_rcp_f32 v95,v95
v_rcp_f32 v96,v96
v_rcp_f32 v97,v97
v_mul_f32 v30,v30,v94
v_mul_f32 v31,v31,v95
v_mul_f32 v32,v32,v96
v_mul_f32 v33,v33,v97
D 0,55,3072
v_cvt_pk_bf16_f32 v106,v30,v31
v_cvt_pk_bf16_f32 v107,v32,v33
v_fma_f32 v34,-v78,v86,v34
v_fma_f32 v35,-v78,v87,v35
v_fma_f32 v36,-v78,v88,v36
v_fma_f32 v37,-v78,v89,v37
v_mul_f32 v98,v34,v34
v_mul_f32 v99,v35,v35
s_add_u32 m0,s57,4096
s_nop 0
D 0,56,0
v_mul_f32 v100,v36,v36
v_mul_f32 v101,v37,v37
v_fma_f32 v98,v80,v98,v79
v_fma_f32 v99,v80,v99,v79
v_fma_f32 v100,v80,v100,v79
v_fma_f32 v101,v80,v101,v79
v_mul_f32 v98,v34,v98
v_mul_f32 v99,v35,v99
D 0,56,1024
v_mul_f32 v100,v36,v100
v_mul_f32 v101,v37,v101
v_exp_f32 v98,v98
v_exp_f32 v99,v99
v_exp_f32 v100,v100
v_exp_f32 v101,v101
v_fma_f32 v98,v98,v81,v81
v_fma_f32 v99,v99,v81,v81
D 0,56,2048
v_fma_f32 v100,v100,v81,v81
v_fma_f32 v101,v101,v81,v81
v_rcp_f32 v98,v98
v_rcp_f32 v99,v99
v_rcp_f32 v100,v100
v_rcp_f32 v101,v101
v_mul_f32 v34,v34,v98
v_mul_f32 v35,v35,v99
D 0,56,3072
v_mul_f32 v36,v36,v100
v_mul_f32 v37,v37,v101
v_cvt_pk_bf16_f32 v108,v34,v35
v_cvt_pk_bf16_f32 v109,v36,v37
buffer_store_dwordx4 v[106:109],v0,s[36:39],s67 offen offset:0 sc0 sc1
v_fma_f32 v38,-v78,v90,v38
v_fma_f32 v39,-v78,v91,v39
v_fma_f32 v40,-v78,v92,v40
v_fma_f32 v41,-v78,v93,v41
s_add_u32 s55,s55,s60
s_add_u32 s57,s57,32768
s_and_b32 s57,s57,131071
s_add_u32 s54,s54,s46
v_mul_f32 v102,v38,v38
v_mul_f32 v103,v39,v39
v_mul_f32 v104,v40,v40
v_mul_f32 v105,v41,v41
v_fma_f32 v102,v80,v102,v79
v_fma_f32 v103,v80,v103,v79
v_fma_f32 v104,v80,v104,v79
v_fma_f32 v105,v80,v105,v79
v_mul_f32 v102,v38,v102
v_mul_f32 v103,v39,v103
v_mul_f32 v104,v40,v104
v_mul_f32 v105,v41,v105
v_exp_f32 v102,v102
v_exp_f32 v103,v103
v_exp_f32 v104,v104
v_exp_f32 v105,v105
v_fma_f32 v102,v102,v81,v81
v_fma_f32 v103,v103,v81,v81
v_fma_f32 v104,v104,v81,v81
v_fma_f32 v105,v105,v81,v81
v_rcp_f32 v102,v102
v_rcp_f32 v103,v103
v_rcp_f32 v104,v104
v_rcp_f32 v105,v105
v_mul_f32 v38,v38,v102
v_mul_f32 v39,v39,v103
v_mul_f32 v40,v40,v104
v_mul_f32 v41,v41,v105
v_cvt_pk_bf16_f32 v110,v38,v39
v_cvt_pk_bf16_f32 v111,v40,v41
s_nop 0
v_permlane16_swap_b32 v110,v112
v_permlane16_swap_b32 v111,v113
buffer_store_dwordx4 v[110:113],v10,s[36:39],s67 offen offset:0 sc0 sc1
s_mov_b32 s72,s69
s_mov_b32 s69,0x7fff0000
s_mov_b32 s67,0x7fff0000
s_mov_b32 s77,0x7fff0000
s_branch Lg3_gox_%=
Lg3_xn1_%=:
s_cmp_ge_u32 s54,256
s_cbranch_scc1 Lg3_fin1_%=
s_add_u32 m0,s59,4096
s_nop 0
buffer_load_dword v5,s[36:39],s58 offen offset:0 sc1 lds
buffer_store_dword v4,v3,s[36:39],s71 offen offset:0 sc0 sc1
s_add_u32 m0,s59,5120
s_nop 0
buffer_load_dword v5,s[36:39],s58 offen offset:0 sc1 lds
buffer_store_dword v4,v3,s[36:39],s71 offen offset:0 sc0 sc1
s_mov_b32 s83,0
s_nop 0
v_fma_f32 v30,-v78,v82,v30
v_fma_f32 v31,-v78,v83,v31
v_fma_f32 v32,-v78,v84,v32
v_fma_f32 v33,-v78,v85,v33
v_mul_f32 v94,v30,v30
v_mul_f32 v95,v31,v31
v_mul_f32 v96,v32,v32
v_mul_f32 v97,v33,v33
v_fma_f32 v94,v80,v94,v79
v_fma_f32 v95,v80,v95,v79
v_fma_f32 v96,v80,v96,v79
v_fma_f32 v97,v80,v97,v79
v_mul_f32 v94,v30,v94
v_mul_f32 v95,v31,v95
v_mul_f32 v96,v32,v96
v_mul_f32 v97,v33,v97
v_exp_f32 v94,v94
v_exp_f32 v95,v95
v_exp_f32 v96,v96
v_exp_f32 v97,v97
v_fma_f32 v94,v94,v81,v81
v_fma_f32 v95,v95,v81,v81
v_fma_f32 v96,v96,v81,v81
v_fma_f32 v97,v97,v81,v81
v_rcp_f32 v94,v94
v_rcp_f32 v95,v95
v_rcp_f32 v96,v96
v_rcp_f32 v97,v97
v_mul_f32 v30,v30,v94
v_mul_f32 v31,v31,v95
v_mul_f32 v32,v32,v96
v_mul_f32 v33,v33,v97
v_cvt_pk_bf16_f32 v106,v30,v31
v_cvt_pk_bf16_f32 v107,v32,v33
v_fma_f32 v34,-v78,v86,v34
v_fma_f32 v35,-v78,v87,v35
v_fma_f32 v36,-v78,v88,v36
v_fma_f32 v37,-v78,v89,v37
v_mul_f32 v98,v34,v34
v_mul_f32 v99,v35,v35
v_mul_f32 v100,v36,v36
v_mul_f32 v101,v37,v37
v_fma_f32 v98,v80,v98,v79
v_fma_f32 v99,v80,v99,v79
v_fma_f32 v100,v80,v100,v79
v_fma_f32 v101,v80,v101,v79
v_mul_f32 v98,v34,v98
v_mul_f32 v99,v35,v99
v_mul_f32 v100,v36,v100
v_mul_f32 v101,v37,v101
v_exp_f32 v98,v98
v_exp_f32 v99,v99
v_exp_f32 v100,v100
v_exp_f32 v101,v101
v_fma_f32 v98,v98,v81,v81
v_fma_f32 v99,v99,v81,v81
v_fma_f32 v100,v100,v81,v81
v_fma_f32 v101,v101,v81,v81
v_rcp_f32 v98,v98
v_rcp_f32 v99,v99
v_rcp_f32 v100,v100
v_rcp_f32 v101,v101
v_mul_f32 v34,v34,v98
v_mul_f32 v35,v35,v99
v_mul_f32 v36,v36,v100
v_mul_f32 v37,v37,v101
v_cvt_pk_bf16_f32 v108,v34,v35
v_cvt_pk_bf16_f32 v109,v36,v37
buffer_store_dwordx4 v[106:109],v0,s[36:39],s67 offen offset:0 sc0 sc1
v_fma_f32 v38,-v78,v90,v38
v_fma_f32 v39,-v78,v91,v39
v_fma_f32 v40,-v78,v92,v40
v_fma_f32 v41,-v78,v93,v41
v_mul_f32 v102,v38,v38
v_mul_f32 v103,v39,v39
v_mul_f32 v104,v40,v40
v_mul_f32 v105,v41,v41
v_fma_f32 v102,v80,v102,v79
v_fma_f32 v103,v80,v103,v79
v_fma_f32 v104,v80,v104,v79
v_fma_f32 v105,v80,v105,v79
v_mul_f32 v102,v38,v102
v_mul_f32 v103,v39,v103
v_mul_f32 v104,v40,v104
v_mul_f32 v105,v41,v105
v_exp_f32 v102,v102
v_exp_f32 v103,v103
v_exp_f32 v104,v104
v_exp_f32 v105,v105
v_fma_f32 v102,v102,v81,v81
v_fma_f32 v103,v103,v81,v81
v_fma_f32 v104,v104,v81,v81
v_fma_f32 v105,v105,v81,v81
v_rcp_f32 v102,v102
v_rcp_f32 v103,v103
v_rcp_f32 v104,v104
v_rcp_f32 v105,v105
v_mul_f32 v38,v38,v102
v_mul_f32 v39,v39,v103
v_mul_f32 v40,v40,v104
v_mul_f32 v41,v41,v105
v_cvt_pk_bf16_f32 v110,v38,v39
v_cvt_pk_bf16_f32 v111,v40,v41
s_nop 0
v_permlane16_swap_b32 v110,v112
v_permlane16_swap_b32 v111,v113
buffer_store_dwordx4 v[110:113],v10,s[36:39],s67 offen offset:0 sc0 sc1
s_mov_b32 s72,s69
s_mov_b32 s69,0x7fff0000
s_mov_b32 s67,0x7fff0000
s_mov_b32 s77,0x7fff0000
s_branch Lg3_poll_%=
Lg3_fin1_%=:
s_nop 0
v_fma_f32 v30,-v78,v82,v30
v_fma_f32 v31,-v78,v83,v31
v_fma_f32 v32,-v78,v84,v32
v_fma_f32 v33,-v78,v85,v33
v_mul_f32 v94,v30,v30
v_mul_f32 v95,v31,v31
v_mul_f32 v96,v32,v32
v_mul_f32 v97,v33,v33
v_fma_f32 v94,v80,v94,v79
v_fma_f32 v95,v80,v95,v79
v_fma_f32 v96,v80,v96,v79
v_fma_f32 v97,v80,v97,v79
v_mul_f32 v94,v30,v94
v_mul_f32 v95,v31,v95
v_mul_f32 v96,v32,v96
v_mul_f32 v97,v33,v97
v_exp_f32 v94,v94
v_exp_f32 v95,v95
v_exp_f32 v96,v96
v_exp_f32 v97,v97
v_fma_f32 v94,v94,v81,v81
v_fma_f32 v95,v95,v81,v81
v_fma_f32 v96,v96,v81,v81
v_fma_f32 v97,v97,v81,v81
v_rcp_f32 v94,v94
v_rcp_f32 v95,v95
v_rcp_f32 v96,v96
v_rcp_f32 v97,v97
v_mul_f32 v30,v30,v94
v_mul_f32 v31,v31,v95
v_mul_f32 v32,v32,v96
v_mul_f32 v33,v33,v97
v_cvt_pk_bf16_f32 v106,v30,v31
v_cvt_pk_bf16_f32 v107,v32,v33
v_fma_f32 v34,-v78,v86,v34
v_fma_f32 v35,-v78,v87,v35
v_fma_f32 v36,-v78,v88,v36
v_fma_f32 v37,-v78,v89,v37
v_mul_f32 v98,v34,v34
v_mul_f32 v99,v35,v35
)"
R"(v_mul_f32 v100,v36,v36
v_mul_f32 v101,v37,v37
v_fma_f32 v98,v80,v98,v79
v_fma_f32 v99,v80,v99,v79
v_fma_f32 v100,v80,v100,v79
v_fma_f32 v101,v80,v101,v79
v_mul_f32 v98,v34,v98
v_mul_f32 v99,v35,v99
v_mul_f32 v100,v36,v100
v_mul_f32 v101,v37,v101
v_exp_f32 v98,v98
v_exp_f32 v99,v99
v_exp_f32 v100,v100
v_exp_f32 v101,v101
v_fma_f32 v98,v98,v81,v81
v_fma_f32 v99,v99,v81,v81
v_fma_f32 v100,v100,v81,v81
v_fma_f32 v101,v101,v81,v81
v_rcp_f32 v98,v98
v_rcp_f32 v99,v99
v_rcp_f32 v100,v100
v_rcp_f32 v101,v101
v_mul_f32 v34,v34,v98
v_mul_f32 v35,v35,v99
v_mul_f32 v36,v36,v100
v_mul_f32 v37,v37,v101
v_cvt_pk_bf16_f32 v108,v34,v35
v_cvt_pk_bf16_f32 v109,v36,v37
buffer_store_dwordx4 v[106:109],v0,s[36:39],s67 offen offset:0 sc0 sc1
v_fma_f32 v38,-v78,v90,v38
v_fma_f32 v39,-v78,v91,v39
v_fma_f32 v40,-v78,v92,v40
v_fma_f32 v41,-v78,v93,v41
v_mul_f32 v102,v38,v38
v_mul_f32 v103,v39,v39
v_mul_f32 v104,v40,v40
v_mul_f32 v105,v41,v41
v_fma_f32 v102,v80,v102,v79
v_fma_f32 v103,v80,v103,v79
v_fma_f32 v104,v80,v104,v79
v_fma_f32 v105,v80,v105,v79
v_mul_f32 v102,v38,v102
v_mul_f32 v103,v39,v103
v_mul_f32 v104,v40,v104
v_mul_f32 v105,v41,v105
v_exp_f32 v102,v102
v_exp_f32 v103,v103
v_exp_f32 v104,v104
v_exp_f32 v105,v105
v_fma_f32 v102,v102,v81,v81
v_fma_f32 v103,v103,v81,v81
v_fma_f32 v104,v104,v81,v81
v_fma_f32 v105,v105,v81,v81
v_rcp_f32 v102,v102
v_rcp_f32 v103,v103
v_rcp_f32 v104,v104
v_rcp_f32 v105,v105
v_mul_f32 v38,v38,v102
v_mul_f32 v39,v39,v103
v_mul_f32 v40,v40,v104
v_mul_f32 v41,v41,v105
v_cvt_pk_bf16_f32 v110,v38,v39
v_cvt_pk_bf16_f32 v111,v40,v41
s_nop 0
v_permlane16_swap_b32 v110,v112
v_permlane16_swap_b32 v111,v113
buffer_store_dwordx4 v[110:113],v10,s[36:39],s67 offen offset:0 sc0 sc1
s_mov_b32 s72,s69
s_mov_b32 s69,0x7fff0000
s_mov_b32 s67,0x7fff0000
s_mov_b32 s77,0x7fff0000
s_branch Lg3_done_%=
Lg3_L10_%=:
v_mfma_f32_16x16x32_bf16 v[18:21],a[0:3],v[42:45],0
s_add_u32 s58,s58,s61
s_add_u32 m0,s59,0
R 62,1,5120
v_mfma_f32_16x16x32_bf16 v[22:25],a[4:7],v[42:45],0
s_waitcnt vmcnt(3)
buffer_load_dword v5,s[36:39],s58 offen offset:0 sc1 lds
v_mfma_f32_16x16x32_bf16 v[26:29],a[8:11],v[42:45],0
s_mov_b32 m0,s57
s_add_u32 s56,s55,4096
v_add_f32_dpp v115,v114,v114 quad_perm:[1,0,3,2] row_mask:0xf bank_mask:0xf
M 18,a[12:15],46
s_nop 0
R 66,1,6144
v_fma_f32 v30,-v78,v82,v30
M 22,a[16:19],46
s_mov_b32 s66,s65
buffer_load_dword v114,v11,s[36:39],s73 offen offset:0 sc1
M 26,a[20:23],46
L 3
v_add_f32_dpp v115,v115,v115 quad_perm:[2,3,0,1] row_mask:0xf bank_mask:0xf
v_fma_f32 v31,-v78,v83,v31
M 18,a[24:27],50
R 70,1,7168
v_fma_f32 v32,-v78,v84,v32
M 22,a[28:31],50
D 0,55,0
v_add_f32_dpp v118,v115,v115 row_half_mirror row_mask:0xf bank_mask:0xf
M 26,a[32:35],50
v_fma_f32 v33,-v78,v85,v33
v_mul_f32 v94,v30,v30
M 18,a[36:39],54
R 42,1,8192
v_mov_b32_dpp v115,v118 row_ror:8 row_mask:0xf bank_mask:0xf
M 22,a[40:43],54
v_fma_f32 v115,-v118,v118,v115
v_max_f32 v115,s87,v115
M 26,a[44:47],54
L 3
D 0,55,1024
M 18,a[48:51],58
R 46,1,9216
v_mul_f32 v95,v31,v31
M 22,a[52:55],58
v_rsq_f32 v116,v115
M 26,a[56:59],58
v_mul_f32 v121,v115,v116
v_mul_f32 v119,s88,v116
M 18,a[60:63],62
R 50,1,10240
v_mul_f32 v115,s89,v116
v_mul_f32 v120,v119,v116
M 22,a[64:67],62
D 0,55,2048
M 26,a[68:71],62
L 3
v_mul_f32 v120,v120,v115
v_mul_f32 v96,v32,v32
M 18,a[72:75],66
R 54,1,11264
v_mul_f32 v97,v33,v33
M 22,a[76:79],66
ds_write_b128 v12,v[118:121] offset:0
M 26,a[80:83],66
D 0,55,3072
M 18,a[84:87],70
s_add_u32 m0,s57,4096
R 58,1,12288
v_fma_f32 v94,v80,v94,v79
M 22,a[88:91],70
s_add_u32 s55,s55,s60
v_fma_f32 v95,v80,v95,v79
v_fma_f32 v96,v80,v96,v79
M 26,a[92:95],70
L 4
D 0,56,0
v_fma_f32 v97,v80,v97,v79
M 18,a[96:99],42
s_add_u32 s57,s57,32768
R 62,1,13312
v_mul_f32 v94,v30,v94
M 22,a[100:103],42
s_and_b32 s57,s57,131071
v_exp_f32 v94,v94
M 26,a[104:107],42
s_add_u32 s54,s54,s46
v_mul_f32 v95,v31,v95
v_mul_f32 v96,v32,v96
M 18,a[108:111],46
s_add_u32 s73,s73,s64
R 66,1,14336
v_mul_f32 v97,v33,v97
M 22,a[112:115],46
s_add_u32 s65,s65,s62
D 0,56,1024
M 26,a[116:119],46
L 4
v_exp_f32 v95,v95
M 18,a[120:123],50
R 70,1,15360
v_fma_f32 v94,v94,v81,v81
M 22,a[124:127],50
D 0,56,2048
v_fma_f32 v95,v95,v81,v81
M 26,a[128:131],50
v_exp_f32 v96,v96
M 18,a[132:135],54
R 42,1,16384
v_fma_f32 v96,v96,v81,v81
M 22,a[136:139],54
v_exp_f32 v97,v97
M 26,a[140:143],54
L 3
D 0,56,3072
M 18,a[144:147],58
R 46,1,17408
v_fma_f32 v97,v97,v81,v81
M 22,a[148:151],58
v_rcp_f32 v94,v94
M 26,a[152:155],58
v_rcp_f32 v95,v95
M 18,a[156:159],62
R 50,1,18432
v_rcp_f32 v96,v96
M 22,a[160:163],62
v_rcp_f32 v97,v97
M 26,a[164:167],62
L 3
v_mul_f32 v30,v30,v94
v_mul_f32 v31,v31,v95
M 18,a[168:171],66
R 54,1,19456
v_mul_f32 v32,v32,v96
M 22,a[172:175],66
v_mul_f32 v33,v33,v97
v_cvt_pk_bf16_f32 v106,v30,v31
M 26,a[176:179],66
v_cvt_pk_bf16_f32 v107,v32,v33
v_fma_f32 v34,-v78,v86,v34
M 18,a[180:183],70
R 58,1,20480
v_fma_f32 v35,-v78,v87,v35
M 22,a[184:187],70
v_fma_f32 v36,-v78,v88,v36
v_fma_f32 v37,-v78,v89,v37
M 26,a[188:191],70
L 3
v_mul_f32 v98,v34,v34
v_mul_f32 v99,v35,v35
v_mul_f32 v100,v36,v36
M 18,a[192:195],42
R 62,1,21504
v_mul_f32 v101,v37,v37
M 22,a[196:199],42
v_fma_f32 v98,v80,v98,v79
v_fma_f32 v99,v80,v99,v79
M 26,a[200:203],42
v_fma_f32 v100,v80,v100,v79
v_fma_f32 v101,v80,v101,v79
M 18,a[204:207],46
R 66,1,22528
v_mul_f32 v98,v34,v98
M 22,a[208:211],46
v_exp_f32 v98,v98
M 26,a[212:215],46
L 3
v_mul_f32 v99,v35,v99
v_mul_f32 v100,v36,v100
M 18,a[216:219],50
R 70,1,23552
v_mul_f32 v101,v37,v101
M 22,a[220:223],50
v_exp_f32 v99,v99
v_fma_f32 v98,v98,v81,v81
M 26,a[224:227],50
v_exp_f32 v100,v100
M 18,a[228:231],54
R 42,1,24576
v_fma_f32 v99,v99,v81,v81
M 22,a[232:235],54
v_exp_f32 v101,v101
M 26,a[236:239],54
L 3
v_rcp_f32 v98,v98
M 18,a[240:243],58
R 46,1,25600
v_fma_f32 v100,v100,v81,v81
M 22,a[244:247],58
v_rcp_f32 v99,v99
M 26,a[248:251],58
v_rcp_f32 v100,v100
M 18,a[252:255],62
R 50,1,26624
v_fma_f32 v101,v101,v81,v81
v_mul_f32 v34,v34,v98
M 22,v[128:131],62
v_rcp_f32 v101,v101
M 26,v[132:135],62
L 3
v_mul_f32 v35,v35,v99
v_mul_f32 v36,v36,v100
M 18,v[136:139],66
R 54,1,27648
v_mul_f32 v37,v37,v101
M 22,v[140:143],66
v_cvt_pk_bf16_f32 v108,v34,v35
v_cvt_pk_bf16_f32 v109,v36,v37
M 26,v[144:147],66
buffer_store_dwordx4 v[106:109],v0,s[36:39],s67 offen offset:0 sc0 sc1
v_fma_f32 v38,-v78,v90,v38
M 18,v[148:151],70
R 58,1,28672
v_fma_f32 v39,-v78,v91,v39
M 22,v[152:155],70
v_fma_f32 v40,-v78,v92,v40
v_fma_f32 v41,-v78,v93,v41
M 26,v[156:159],70
L 3
v_mul_f32 v102,v38,v38
v_mul_f32 v103,v39,v39
v_mul_f32 v104,v40,v40
M 18,v[160:163],42
R 62,1,29696
v_mul_f32 v105,v41,v41
M 22,v[164:167],42
v_fma_f32 v102,v80,v102,v79
v_fma_f32 v103,v80,v103,v79
M 26,v[168:171],42
v_fma_f32 v104,v80,v104,v79
v_fma_f32 v105,v80,v105,v79
M 18,v[172:175],46
R 66,1,30720
v_mul_f32 v102,v38,v102
M 22,v[176:179],46
v_exp_f32 v102,v102
M 26,v[180:183],46
L 3
v_mul_f32 v103,v39,v103
v_mul_f32 v104,v40,v104
M 18,v[184:187],50
s_nop 0
R 70,1,31744
v_mul_f32 v105,v41,v105
M 22,v[188:191],50
s_waitcnt vmcnt(1)
v_exp_f32 v103,v103
v_fma_f32 v102,v102,v81,v81
M 26,v[192:195],50
s_barrier
R 74,13,0
ds_read_b32 v7,v6 offset:0
M 18,v[196:199],54
R 42,2,0
v_fma_f32 v103,v103,v81,v81
M 22,v[200:203],54
buffer_store_dword v4,v3,s[36:39],s70 offen offset:0 sc0 sc1
M 26,v[204:207],54
L 5
v_exp_f32 v104,v104
M 18,v[208:211],58
s_mov_b32 s70,s69
R 46,2,1024
v_fma_f32 v104,v104,v81,v81
M 22,v[212:215],58
s_mov_b32 s69,s68
v_exp_f32 v105,v105
M 26,v[216:219],58
L 2
v_rcp_f32 v102,v102
M 18,v[220:223],62
s_add_u32 s68,s68,s63
R 50,2,2048
v_rcp_f32 v103,v103
M 22,v[224:227],62
v_fma_f32 v105,v105,v81,v81
v_mul_f32 v38,v38,v102
M 26,v[228:231],62
s_nop 0
v_rcp_f32 v104,v104
M 18,v[232:235],66
R 54,2,3072
v_mul_f32 v39,v39,v103
M 22,v[236:239],66
v_rcp_f32 v105,v105
M 26,v[240:243],66
v_mul_f32 v40,v40,v104
v_mul_f32 v41,v41,v105
v_cvt_pk_bf16_f32 v110,v38,v39
v_cvt_pk_bf16_f32 v111,v40,v41
M 18,v[244:247],70
R 58,2,4096
v_cmp_eq_u32 vcc,s53,v7
M 22,v[248:251],70
s_cmp_eq_u64 vcc,-1
v_permlane16_swap_b32 v110,v112
v_permlane16_swap_b32 v111,v113
M 26,v[252:255],70
L 3
buffer_store_dwordx4 v[110:113],v10,s[36:39],s67 offen offset:0 sc0 sc1
v_xor_b32 v1,65536,v1
s_cbranch_scc1 Lg3_L21_%=
s_cmp_eq_u32 vcc_lo,-1
s_cbranch_scc1 Lg3_L11_%=
s_branch Lg3_X1_%=
Lg3_L11_%=:
v_mfma_f32_16x16x32_bf16 v[30:33],a[0:3],v[42:45],0
s_add_u32 s58,s58,s61
s_add_u32 m0,s59,1024
R 62,2,5120
v_mfma_f32_16x16x32_bf16 v[34:37],a[4:7],v[42:45],0
s_waitcnt vmcnt(3)
buffer_load_dword v5,s[36:39],s58 offen offset:0 sc1 lds
v_mfma_f32_16x16x32_bf16 v[38:41],a[8:11],v[42:45],0
s_mov_b32 m0,s57
s_add_u32 s56,s55,4096
v_add_f32_dpp v115,v114,v114 quad_perm:[1,0,3,2] row_mask:0xf bank_mask:0xf
M 30,a[12:15],46
s_nop 0
R 66,2,6144
v_fma_f32 v18,-v74,v82,v18
M 34,a[16:19],46
s_mov_b32 s67,s65
buffer_load_dword v114,v11,s[36:39],s73 offen offset:0 sc1
M 38,a[20:23],46
L 3
v_add_f32_dpp v115,v115,v115 quad_perm:[2,3,0,1] row_mask:0xf bank_mask:0xf
v_fma_f32 v19,-v74,v83,v19
M 30,a[24:27],50
R 70,2,7168
v_fma_f32 v20,-v74,v84,v20
M 34,a[28:31],50
D 0,55,0
v_add_f32_dpp v118,v115,v115 row_half_mirror row_mask:0xf bank_mask:0xf
M 38,a[32:35],50
v_fma_f32 v21,-v74,v85,v21
v_mul_f32 v94,v18,v18
M 30,a[36:39],54
R 42,2,8192
v_mov_b32_dpp v115,v118 row_ror:8 row_mask:0xf bank_mask:0xf
M 34,a[40:43],54
v_fma_f32 v115,-v118,v118,v115
v_max_f32 v115,s87,v115
M 38,a[44:47],54
L 3
D 0,55,1024
M 30,a[48:51],58
R 46,2,9216
v_mul_f32 v95,v19,v19
M 34,a[52:55],58
v_rsq_f32 v116,v115
M 38,a[56:59],58
v_mul_f32 v121,v115,v116
v_mul_f32 v119,s88,v116
M 30,a[60:63],62
R 50,2,10240
v_mul_f32 v115,s89,v116
v_mul_f32 v120,v119,v116
M 34,a[64:67],62
D 0,55,2048
M 38,a[68:71],62
L 3
v_mul_f32 v120,v120,v115
v_mul_f32 v96,v20,v20
M 30,a[72:75],66
R 54,2,11264
v_mul_f32 v97,v21,v21
M 34,a[76:79],66
ds_write_b128 v12,v[118:121] offset:2048
M 38,a[80:83],66
D 0,55,3072
M 30,a[84:87],70
s_add_u32 m0,s57,4096
R 58,2,12288
v_fma_f32 v94,v76,v94,v75
M 34,a[88:91],70
s_add_u32 s55,s55,s60
v_fma_f32 v95,v76,v95,v75
v_fma_f32 v96,v76,v96,v75
M 38,a[92:95],70
L 4
D 0,56,0
v_fma_f32 v97,v76,v97,v75
M 30,a[96:99],42
s_add_u32 s57,s57,32768
R 62,2,13312
v_mul_f32 v94,v18,v94
M 34,a[100:103],42
s_and_b32 s57,s57,131071
v_exp_f32 v94,v94
M 38,a[104:107],42
s_add_u32 s54,s54,s46
v_mul_f32 v95,v19,v95
v_mul_f32 v96,v20,v96
M 30,a[108:111],46
s_add_u32 s73,s73,s64
R 66,2,14336
v_mul_f32 v97,v21,v97
M 34,a[112:115],46
s_add_u32 s65,s65,s62
D 0,56,1024
M 38,a[116:119],46
L 4
v_exp_f32 v95,v95
M 30,a[120:123],50
R 70,2,15360
v_fma_f32 v94,v94,v77,v77
M 34,a[124:127],50
D 0,56,2048
v_fma_f32 v95,v95,v77,v77
M 38,a[128:131],50
v_exp_f32 v96,v96
M 30,a[132:135],54
R 42,2,16384
v_fma_f32 v96,v96,v77,v77
M 34,a[136:139],54
v_exp_f32 v97,v97
M 38,a[140:143],54
L 3
D 0,56,3072
M 30,a[144:147],58
R 46,2,17408
v_fma_f32 v97,v97,v77,v77
M 34,a[148:151],58
v_rcp_f32 v94,v94
M 38,a[152:155],58
v_rcp_f32 v95,v95
M 30,a[156:159],62
)"
R"(R 50,2,18432
v_rcp_f32 v96,v96
M 34,a[160:163],62
v_rcp_f32 v97,v97
M 38,a[164:167],62
L 3
v_mul_f32 v18,v18,v94
v_mul_f32 v19,v19,v95
M 30,a[168:171],66
R 54,2,19456
v_mul_f32 v20,v20,v96
M 34,a[172:175],66
v_mul_f32 v21,v21,v97
v_cvt_pk_bf16_f32 v106,v18,v19
M 38,a[176:179],66
v_cvt_pk_bf16_f32 v107,v20,v21
v_fma_f32 v22,-v74,v86,v22
M 30,a[180:183],70
R 58,2,20480
v_fma_f32 v23,-v74,v87,v23
M 34,a[184:187],70
v_fma_f32 v24,-v74,v88,v24
v_fma_f32 v25,-v74,v89,v25
M 38,a[188:191],70
L 3
v_mul_f32 v98,v22,v22
v_mul_f32 v99,v23,v23
v_mul_f32 v100,v24,v24
M 30,a[192:195],42
R 62,2,21504
v_mul_f32 v101,v25,v25
M 34,a[196:199],42
v_fma_f32 v98,v76,v98,v75
v_fma_f32 v99,v76,v99,v75
M 38,a[200:203],42
v_fma_f32 v100,v76,v100,v75
v_fma_f32 v101,v76,v101,v75
M 30,a[204:207],46
R 66,2,22528
v_mul_f32 v98,v22,v98
M 34,a[208:211],46
v_exp_f32 v98,v98
M 38,a[212:215],46
L 3
v_mul_f32 v99,v23,v99
v_mul_f32 v100,v24,v100
M 30,a[216:219],50
R 70,2,23552
v_mul_f32 v101,v25,v101
M 34,a[220:223],50
v_exp_f32 v99,v99
v_fma_f32 v98,v98,v77,v77
M 38,a[224:227],50
v_exp_f32 v100,v100
M 30,a[228:231],54
R 42,2,24576
v_fma_f32 v99,v99,v77,v77
M 34,a[232:235],54
v_exp_f32 v101,v101
M 38,a[236:239],54
L 3
v_rcp_f32 v98,v98
M 30,a[240:243],58
R 46,2,25600
v_fma_f32 v100,v100,v77,v77
M 34,a[244:247],58
v_rcp_f32 v99,v99
M 38,a[248:251],58
v_rcp_f32 v100,v100
M 30,a[252:255],62
R 50,2,26624
v_fma_f32 v101,v101,v77,v77
v_mul_f32 v22,v22,v98
M 34,v[128:131],62
v_rcp_f32 v101,v101
M 38,v[132:135],62
L 3
v_mul_f32 v23,v23,v99
v_mul_f32 v24,v24,v100
M 30,v[136:139],66
R 54,2,27648
v_mul_f32 v25,v25,v101
M 34,v[140:143],66
v_cvt_pk_bf16_f32 v108,v22,v23
v_cvt_pk_bf16_f32 v109,v24,v25
M 38,v[144:147],66
buffer_store_dwordx4 v[106:109],v0,s[36:39],s66 offen offset:0 sc0 sc1
v_fma_f32 v26,-v74,v90,v26
M 30,v[148:151],70
R 58,2,28672
v_fma_f32 v27,-v74,v91,v27
M 34,v[152:155],70
v_fma_f32 v28,-v74,v92,v28
v_fma_f32 v29,-v74,v93,v29
M 38,v[156:159],70
L 3
v_mul_f32 v102,v26,v26
v_mul_f32 v103,v27,v27
v_mul_f32 v104,v28,v28
M 30,v[160:163],42
R 62,2,29696
v_mul_f32 v105,v29,v29
M 34,v[164:167],42
v_fma_f32 v102,v76,v102,v75
v_fma_f32 v103,v76,v103,v75
M 38,v[168:171],42
v_fma_f32 v104,v76,v104,v75
v_fma_f32 v105,v76,v105,v75
M 30,v[172:175],46
R 66,2,30720
v_mul_f32 v102,v26,v102
M 34,v[176:179],46
v_exp_f32 v102,v102
M 38,v[180:183],46
L 3
v_mul_f32 v103,v27,v103
v_mul_f32 v104,v28,v104
M 30,v[184:187],50
s_nop 0
R 70,2,31744
v_mul_f32 v105,v29,v105
M 34,v[188:191],50
s_waitcnt vmcnt(1)
v_exp_f32 v103,v103
v_fma_f32 v102,v102,v77,v77
M 38,v[192:195],50
s_barrier
R 78,13,2048
ds_read_b32 v7,v6 offset:1024
M 30,v[196:199],54
R 42,1,0
v_fma_f32 v103,v103,v77,v77
M 34,v[200:203],54
buffer_store_dword v4,v3,s[36:39],s70 offen offset:0 sc0 sc1
M 38,v[204:207],54
L 5
v_exp_f32 v104,v104
M 30,v[208:211],58
s_mov_b32 s70,s69
R 46,1,1024
v_fma_f32 v104,v104,v77,v77
M 34,v[212:215],58
s_mov_b32 s69,s68
v_exp_f32 v105,v105
M 38,v[216:219],58
L 2
v_rcp_f32 v102,v102
M 30,v[220:223],62
s_add_u32 s68,s68,s63
R 50,1,2048
v_rcp_f32 v103,v103
M 34,v[224:227],62
v_fma_f32 v105,v105,v77,v77
v_mul_f32 v26,v26,v102
M 38,v[228:231],62
s_nop 0
v_rcp_f32 v104,v104
M 30,v[232:235],66
R 54,1,3072
v_mul_f32 v27,v27,v103
M 34,v[236:239],66
v_rcp_f32 v105,v105
M 38,v[240:243],66
v_mul_f32 v28,v28,v104
v_mul_f32 v29,v29,v105
v_cvt_pk_bf16_f32 v110,v26,v27
v_cvt_pk_bf16_f32 v111,v28,v29
M 30,v[244:247],70
R 58,1,4096
v_cmp_eq_u32 vcc,s53,v7
M 34,v[248:251],70
s_cmp_eq_u64 vcc,-1
v_permlane16_swap_b32 v110,v112
v_permlane16_swap_b32 v111,v113
M 38,v[252:255],70
L 3
buffer_store_dwordx4 v[110:113],v10,s[36:39],s66 offen offset:0 sc0 sc1
v_xor_b32 v2,65536,v2
s_cbranch_scc1 Lg3_L20_%=
s_cmp_eq_u32 vcc_lo,-1
s_cbranch_scc1 Lg3_L10_%=
s_branch Lg3_X0_%=
Lg3_done_%=:
s_waitcnt vmcnt(2)
s_barrier
buffer_store_dword v4,v3,s[36:39],s70 offen offset:0 sc0 sc1
s_waitcnt vmcnt(1)
s_barrier
buffer_store_dword v4,v3,s[36:39],s72 offen offset:0 sc0 sc1
s_waitcnt vmcnt(0) lgkmcnt(0)
s_barrier
s_endpgm
Lcode_g3_e:
.purgem M
.purgem R
.purgem T
.purgem D
.purgem L
)"
  :: "s"(ws), "s"(sWave), "s"(sG0), "s"(sStep), "s"(sOutOff), "s"(sFoutOff), "s"(sAuxOff), "s"(sWw), "s"(sBuf), "s"(sRk), "v"(vL16), "v"(vFlag), "v"(vSnapOff), "v"(vSnapRd), "v"(vWd0), "v"(vWd1), "v"(vWr0), "v"(vWr1), "v"(vWr2), "v"(vOutS), "v"(vAux0), "v"(vAux1), "v"(vAux2), "v"(vAux3), "v"(vAn), "v"(vAe), "s"(w3) : "v0", "v1", "v2", "v3", "v4", "v5", "v6", "v7", "v8", "v9", "v10", "v11", "v12", "v13", "v14", "v15", "v16", "v17", "v18", "v19", "v20", "v21", "v22", "v23", "v24", "v25", "v26", "v27", "v28", "v29", "v30", "v31", "v32", "v33", "v34", "v35", "v36", "v37", "v38", "v39", "v40", "v41", "v42", "v43", "v44", "v45", "v46", "v47", "v48", "v49", "v50", "v51", "v52", "v53", "v54", "v55", "v56", "v57", "v58", "v59", "v60", "v61", "v62", "v63", "v64", "v65", "v66", "v67", "v68", "v69", "v70", "v71", "v72", "v73", "v74", "v75", "v76", "v77", "v78", "v79", "v80", "v81", "v82", "v83", "v84", "v85", "v86", "v87", "v88", "v89", "v90", "v91", "v92", "v93", "v94", "v95", "v96", "v97", "v98", "v99", "v100", "v101", "v102", "v103", "v104", "v105", "v106", "v107", "v108", "v109", "v110", "v111", "v112", "v113", "v114", "v115", "v116", "v117", "v118", "v119", "v120", "v121", "v122", "v123", "v124", "v125", "v126", "v127", "v128", "v129", "v130", "v131", "v132", "v133", "v134", "v135", "v136", "v137", "v138", "v139", "v140", "v141", "v142", "v143", "v144", "v145", "v146", "v147", "v148", "v149", "v150", "v151", "v152", "v153", "v154", "v155", "v156", "v157", "v158", "v159", "v160", "v161", "v162", "v163", "v164", "v165", "v166", "v167", "v168", "v169", "v170", "v171", "v172", "v173", "v174", "v175", "v176", "v177", "v178", "v179", "v180", "v181", "v182", "v183", "v184", "v185", "v186", "v187", "v188", "v189", "v190", "v191", "v192", "v193", "v194", "v195", "v196", "v197", "v198", "v199", "v200", "v201", "v202", "v203", "v204", "v205", "v206", "v207", "v208", "v209", "v210", "v211", "v212", "v213", "v214", "v215", "v216", "v217", "v218", "v219", "v220", "v221", "v222", "v223", "v224", "v225", "v226", "v227", "v228", "v229", "v230", "v231", "v232", "v233", "v234", "v235", "v236", "v237", "v238", "v239", "a0", "a1", "a2", "a3", "a4", "a5", "a6", "a7", "a8", "a9", "a10", "a11", "a12", "a13", "a14", "a15", "a16", "a17", "a18", "a19", "a20", "a21", "a22", "a23", "a24", "a25", "a26", "a27", "a28", "a29", "a30", "a31", "a32", "a33", "a34", "a35", "a36", "a37", "a38", "a39", "a40", "a41", "a42", "a43", "a44", "a45", "a46", "a47", "a48", "a49", "a50", "a51", "a52", "a53", "a54", "a55", "a56", "a57", "a58", "a59", "a60", "a61", "a62", "a63", "a64", "a65", "a66", "a67", "a68", "a69", "a70", "a71", "a72", "a73", "a74", "a75", "a76", "a77", "a78", "a79", "a80", "a81", "a82", "a83", "a84", "a85", "a86", "a87", "a88", "a89", "a90", "a91", "a92", "a93", "a94", "a95", "a96", "a97", "a98", "a99", "a100", "a101", "a102", "a103", "a104", "a105", "a106", "a107", "a108", "a109", "a110", "a111", "a112", "a113", "a114", "a115", "a116", "a117", "a118", "a119", "a120", "a121", "a122", "a123", "a124", "a125", "a126", "a127", "a128", "a129", "a130", "a131", "a132", "a133", "a134", "a135", "a136", "a137", "a138", "a139", "a140", "a141", "a142", "a143", "a144", "a145", "a146", "a147", "a148", "a149", "a150", "a151", "a152", "a153", "a154", "a155", "a156", "a157", "a158", "a159", "a160", "a161", "a162", "a163", "a164", "a165", "a166", "a167", "a168", "a169", "a170", "a171", "a172", "a173", "a174", "a175", "a176", "a177", "a178", "a179", "a180", "a181", "a182", "a183", "a184", "a185", "a186", "a187", "a188", "a189", "a190", "a191", "a192", "a193", "a194", "a195", "a196", "a197", "a198", "a199", "a200", "a201", "a202", "a203", "a204", "a205", "a206", "a207", "a208", "a209", "a210", "a211", "a212", "a213", "a214", "a215", "a216", "a217", "a218", "a219", "a220", "a221", "a222", "a223", "a224", "a225", "a226", "a227", "a228", "a229", "a230", "a231", "a232", "a233", "a234", "a235", "a236", "a237", "a238", "a239", "a240", "a241", "a242", "a243", "a244", "a245", "a246", "a247", "a248", "a249", "a250", "a251", "a252", "a253", "a254", "a255", "s36", "s37", "s38", "s39", "s40", "s41", "s42", "s43", "s44", "s45", "s46", "s47", "s48", "s49", "s50", "s51", "s52", "s53", "s54", "s55", "s56", "s57", "s58", "s59", "s60", "s61", "s62", "s63", "s64", "s65", "s66", "s67", "s68", "s69", "s70", "s71", "s72", "s73", "s74", "s75", "s76", "s77", "s78", "s79", "s80", "s81", "s82", "s83", "s84", "s85", "s86", "s87", "s88", "s89", "s90", "s91", "s92", "s93", "s94", "s95", "s96", "s97", "vcc", "scc", "memory"); } else { asm volatile(
R"(.macro M d,w,x
v_mfma_f32_16x16x32_bf16 v[\d:\d+3], \w, v[\x:\x+3], v[\d:\d+3]
.endm
.macro R d,a,o
ds_read_b128 v[\d:\d+3], v\a offset:\o
.endm
.macro T d,a,o
ds_read_b64_tr_b16 \d, v\a offset:\o
.endm
.macro D a,s,o
buffer_load_dwordx4 v\a, s[36:39], s\s offen offset:\o sc1 lds
.endm
.macro L c
s_waitcnt lgkmcnt(\c)
.endm
Lcode_g3o_b:
s_mov_b64 s[36:37],%0
s_mov_b32 s38,111243392
s_mov_b32 s39,0x00020000
s_mov_b32 s44,%1
s_mov_b32 s45,%2
s_mov_b32 s46,%3
s_mov_b32 s47,%4
s_mov_b32 s48,%5
s_mov_b32 s49,%6
s_mov_b32 s50,%7
s_mov_b32 s51,%8
s_mov_b32 s93,%9
v_mov_b32 v0,%10
v_mov_b32 v3,%11
v_mov_b32 v5,%12
v_mov_b32 v6,%13
v_mov_b32 v18,%14
v_mov_b32 v19,%15
v_mov_b32 v20,%16
v_mov_b32 v21,%17
v_mov_b32 v22,%18
v_mov_b32 v10,%19
v_mov_b32 v11,%20
v_mov_b32 v12,%21
v_mov_b32 v13,%22
v_mov_b32 v14,%23
v_mov_b32 v15,%24
v_mov_b32 v16,%25
s_mov_b64 s[40:41],%26
s_mov_b32 s42,7340032
s_mov_b32 s43,0x00020000
s_lshl_b32 s57,s44,11
s_mov_b32 s79,111243264
buffer_load_dword v4,off,s[36:39],s79 sc1
s_add_u32 s55,s50,0
s_add_u32 m0,s57,0
s_nop 0
buffer_load_dwordx4 v18,s[40:43],s55 offen offset:0  lds
s_add_u32 m0,s57,1024
s_nop 0
buffer_load_dwordx4 v19,s[40:43],s55 offen offset:0  lds
s_add_u32 m0,s57,8064
s_nop 0
buffer_load_dwordx4 v18,s[40:43],s55 offen offset:128  lds
s_add_u32 m0,s57,9088
s_nop 0
buffer_load_dwordx4 v19,s[40:43],s55 offen offset:128  lds
s_add_u32 s55,s50,458752
s_add_u32 m0,s57,16384
s_nop 0
buffer_load_dwordx4 v18,s[40:43],s55 offen offset:0  lds
s_add_u32 m0,s57,17408
s_nop 0
buffer_load_dwordx4 v19,s[40:43],s55 offen offset:0  lds
s_add_u32 m0,s57,24448
s_nop 0
buffer_load_dwordx4 v18,s[40:43],s55 offen offset:128  lds
s_add_u32 m0,s57,25472
s_nop 0
buffer_load_dwordx4 v19,s[40:43],s55 offen offset:128  lds
s_add_u32 s55,s50,917504
s_add_u32 m0,s57,32768
s_nop 0
buffer_load_dwordx4 v18,s[40:43],s55 offen offset:0  lds
s_add_u32 m0,s57,33792
s_nop 0
buffer_load_dwordx4 v19,s[40:43],s55 offen offset:0  lds
s_add_u32 m0,s57,40832
s_nop 0
buffer_load_dwordx4 v18,s[40:43],s55 offen offset:128  lds
s_add_u32 m0,s57,41856
s_nop 0
buffer_load_dwordx4 v19,s[40:43],s55 offen offset:128  lds
s_add_u32 s55,s50,1376256
s_add_u32 m0,s57,49152
s_nop 0
buffer_load_dwordx4 v18,s[40:43],s55 offen offset:0  lds
s_add_u32 m0,s57,50176
s_nop 0
buffer_load_dwordx4 v19,s[40:43],s55 offen offset:0  lds
s_add_u32 m0,s57,57216
s_nop 0
buffer_load_dwordx4 v18,s[40:43],s55 offen offset:128  lds
s_add_u32 m0,s57,58240
s_nop 0
buffer_load_dwordx4 v19,s[40:43],s55 offen offset:128  lds
s_add_u32 s55,s50,1835008
s_add_u32 m0,s57,65536
s_nop 0
buffer_load_dwordx4 v18,s[40:43],s55 offen offset:0  lds
s_add_u32 m0,s57,66560
s_nop 0
buffer_load_dwordx4 v19,s[40:43],s55 offen offset:0  lds
s_add_u32 m0,s57,73600
s_nop 0
buffer_load_dwordx4 v18,s[40:43],s55 offen offset:128  lds
s_add_u32 m0,s57,74624
s_nop 0
buffer_load_dwordx4 v19,s[40:43],s55 offen offset:128  lds
s_add_u32 s55,s50,2293760
s_add_u32 m0,s57,81920
s_nop 0
buffer_load_dwordx4 v18,s[40:43],s55 offen offset:0  lds
s_add_u32 m0,s57,82944
s_nop 0
buffer_load_dwordx4 v19,s[40:43],s55 offen offset:0  lds
s_add_u32 m0,s57,89984
s_nop 0
buffer_load_dwordx4 v18,s[40:43],s55 offen offset:128  lds
s_add_u32 m0,s57,91008
s_nop 0
buffer_load_dwordx4 v19,s[40:43],s55 offen offset:128  lds
s_waitcnt vmcnt(20)
L 0
s_barrier
s_add_u32 s55,s50,2752512
s_add_u32 m0,s57,98304
s_nop 0
buffer_load_dwordx4 v18,s[40:43],s55 offen offset:0  lds
s_add_u32 m0,s57,99328
s_nop 0
buffer_load_dwordx4 v19,s[40:43],s55 offen offset:0  lds
s_add_u32 m0,s57,106368
s_nop 0
buffer_load_dwordx4 v18,s[40:43],s55 offen offset:128  lds
s_add_u32 m0,s57,107392
s_nop 0
buffer_load_dwordx4 v19,s[40:43],s55 offen offset:128  lds
v_add_u32 v26,0,v20
v_add_u32 v27,0,v21
T a[0:1],26,0
T a[2:3],26,512
T a[4:5],27,0
T a[6:7],27,512
T a[8:9],26,4096
T a[10:11],26,4608
T a[12:13],27,4096
T a[14:15],27,4608
s_waitcnt vmcnt(20)
L 0
s_barrier
s_add_u32 s55,s50,3211264
s_add_u32 m0,s57,0
s_nop 0
buffer_load_dwordx4 v18,s[40:43],s55 offen offset:0  lds
s_add_u32 m0,s57,1024
s_nop 0
buffer_load_dwordx4 v19,s[40:43],s55 offen offset:0  lds
s_add_u32 m0,s57,8064
s_nop 0
buffer_load_dwordx4 v18,s[40:43],s55 offen offset:128  lds
s_add_u32 m0,s57,9088
s_nop 0
buffer_load_dwordx4 v19,s[40:43],s55 offen offset:128  lds
v_add_u32 v26,16384,v20
v_add_u32 v27,16384,v21
T a[16:17],26,0
T a[18:19],26,512
T a[20:21],27,0
T a[22:23],27,512
T a[24:25],26,4096
T a[26:27],26,4608
T a[28:29],27,4096
T a[30:31],27,4608
s_waitcnt vmcnt(20)
L 0
s_barrier
s_add_u32 s55,s50,3670016
s_add_u32 m0,s57,16384
s_nop 0
buffer_load_dwordx4 v18,s[40:43],s55 offen offset:0  lds
s_add_u32 m0,s57,17408
s_nop 0
buffer_load_dwordx4 v19,s[40:43],s55 offen offset:0  lds
s_add_u32 m0,s57,24448
s_nop 0
buffer_load_dwordx4 v18,s[40:43],s55 offen offset:128  lds
s_add_u32 m0,s57,25472
s_nop 0
buffer_load_dwordx4 v19,s[40:43],s55 offen offset:128  lds
v_add_u32 v26,32768,v20
v_add_u32 v27,32768,v21
T a[32:33],26,0
T a[34:35],26,512
T a[36:37],27,0
T a[38:39],27,512
T a[40:41],26,4096
T a[42:43],26,4608
T a[44:45],27,4096
T a[46:47],27,4608
s_waitcnt vmcnt(20)
L 0
s_barrier
s_add_u32 s55,s50,4128768
s_add_u32 m0,s57,32768
s_nop 0
buffer_load_dwordx4 v18,s[40:43],s55 offen offset:0  lds
s_add_u32 m0,s57,33792
s_nop 0
buffer_load_dwordx4 v19,s[40:43],s55 offen offset:0  lds
s_add_u32 m0,s57,40832
s_nop 0
buffer_load_dwordx4 v18,s[40:43],s55 offen offset:128  lds
s_add_u32 m0,s57,41856
s_nop 0
buffer_load_dwordx4 v19,s[40:43],s55 offen offset:128  lds
v_add_u32 v26,49152,v20
v_add_u32 v27,49152,v21
T a[48:49],26,0
T a[50:51],26,512
T a[52:53],27,0
T a[54:55],27,512
T a[56:57],26,4096
T a[58:59],26,4608
T a[60:61],27,4096
T a[62:63],27,4608
s_waitcnt vmcnt(20)
L 0
s_barrier
s_add_u32 s55,s50,4587520
s_add_u32 m0,s57,49152
s_nop 0
buffer_load_dwordx4 v18,s[40:43],s55 offen offset:0  lds
s_add_u32 m0,s57,50176
s_nop 0
buffer_load_dwordx4 v19,s[40:43],s55 offen offset:0  lds
s_add_u32 m0,s57,57216
s_nop 0
buffer_load_dwordx4 v18,s[40:43],s55 offen offset:128  lds
s_add_u32 m0,s57,58240
s_nop 0
buffer_load_dwordx4 v19,s[40:43],s55 offen offset:128  lds
v_add_u32 v26,65536,v20
v_add_u32 v27,65536,v21
T a[64:65],26,0
T a[66:67],26,512
T a[68:69],27,0
T a[70:71],27,512
T a[72:73],26,4096
T a[74:75],26,4608
T a[76:77],27,4096
T a[78:79],27,4608
s_waitcnt vmcnt(20)
L 0
s_barrier
s_add_u32 s55,s50,5046272
s_add_u32 m0,s57,65536
s_nop 0
buffer_load_dwordx4 v18,s[40:43],s55 offen offset:0  lds
s_add_u32 m0,s57,66560
s_nop 0
buffer_load_dwordx4 v19,s[40:43],s55 offen offset:0  lds
s_add_u32 m0,s57,73600
s_nop 0
buffer_load_dwordx4 v18,s[40:43],s55 offen offset:128  lds
s_add_u32 m0,s57,74624
s_nop 0
buffer_load_dwordx4 v19,s[40:43],s55 offen offset:128  lds
v_add_u32 v26,81920,v20
v_add_u32 v27,81920,v21
T a[80:81],26,0
T a[82:83],26,512
T a[84:85],27,0
T a[86:87],27,512
T a[88:89],26,4096
T a[90:91],26,4608
T a[92:93],27,4096
T a[94:95],27,4608
s_waitcnt vmcnt(20)
L 0
s_barrier
s_add_u32 s55,s50,5505024
s_add_u32 m0,s57,81920
s_nop 0
buffer_load_dwordx4 v18,s[40:43],s55 offen offset:0  lds
s_add_u32 m0,s57,82944
s_nop 0
buffer_load_dwordx4 v19,s[40:43],s55 offen offset:0  lds
s_add_u32 m0,s57,89984
s_nop 0
buffer_load_dwordx4 v18,s[40:43],s55 offen offset:128  lds
s_add_u32 m0,s57,91008
s_nop 0
buffer_load_dwordx4 v19,s[40:43],s55 offen offset:128  lds
v_add_u32 v26,98304,v20
v_add_u32 v27,98304,v21
T a[96:97],26,0
T a[98:99],26,512
T a[100:101],27,0
T a[102:103],27,512
T a[104:105],26,4096
T a[106:107],26,4608
T a[108:109],27,4096
T a[110:111],27,4608
s_waitcnt vmcnt(20)
L 0
s_barrier
s_add_u32 s55,s50,5963776
s_add_u32 m0,s57,98304
s_nop 0
buffer_load_dwordx4 v18,s[40:43],s55 offen offset:0  lds
s_add_u32 m0,s57,99328
s_nop 0
buffer_load_dwordx4 v19,s[40:43],s55 offen offset:0  lds
s_add_u32 m0,s57,106368
s_nop 0
buffer_load_dwordx4 v18,s[40:43],s55 offen offset:128  lds
s_add_u32 m0,s57,107392
s_nop 0
buffer_load_dwordx4 v19,s[40:43],s55 offen offset:128  lds
v_add_u32 v26,0,v20
v_add_u32 v27,0,v21
T a[112:113],26,0
T a[114:115],26,512
T a[116:117],27,0
T a[118:119],27,512
T a[120:121],26,4096
T a[122:123],26,4608
T a[124:125],27,4096
T a[126:127],27,4608
s_waitcnt vmcnt(20)
L 0
s_barrier
s_add_u32 s55,s50,6422528
s_add_u32 m0,s57,0
s_nop 0
buffer_load_dwordx4 v18,s[40:43],s55 offen offset:0  lds
s_add_u32 m0,s57,1024
s_nop 0
buffer_load_dwordx4 v19,s[40:43],s55 offen offset:0  lds
s_add_u32 m0,s57,8064
s_nop 0
buffer_load_dwordx4 v18,s[40:43],s55 offen offset:128  lds
s_add_u32 m0,s57,9088
s_nop 0
buffer_load_dwordx4 v19,s[40:43],s55 offen offset:128  lds
v_add_u32 v26,16384,v20
v_add_u32 v27,16384,v21
T a[128:129],26,0
T a[130:131],26,512
T a[132:133],27,0
T a[134:135],27,512
T a[136:137],26,4096
T a[138:139],26,4608
T a[140:141],27,4096
T a[142:143],27,4608
s_waitcnt vmcnt(20)
L 0
s_barrier
s_add_u32 s55,s50,6881280
s_add_u32 m0,s57,16384
s_nop 0
buffer_load_dwordx4 v18,s[40:43],s55 offen offset:0  lds
s_add_u32 m0,s57,17408
s_nop 0
buffer_load_dwordx4 v19,s[40:43],s55 offen offset:0  lds
s_add_u32 m0,s57,24448
s_nop 0
buffer_load_dwordx4 v18,s[40:43],s55 offen offset:128  lds
s_add_u32 m0,s57,25472
s_nop 0
buffer_load_dwordx4 v19,s[40:43],s55 offen offset:128  lds
v_add_u32 v26,32768,v20
v_add_u32 v27,32768,v21
T a[144:145],26,0
T a[146:147],26,512
T a[148:149],27,0
T a[150:151],27,512
T a[152:153],26,4096
T a[154:155],26,4608
T a[156:157],27,4096
T a[158:159],27,4608
s_waitcnt vmcnt(20)
L 0
s_barrier
v_add_u32 v26,49152,v20
v_add_u32 v27,49152,v21
T a[160:161],26,0
T a[162:163],26,512
T a[164:165],27,0
T a[166:167],27,512
T a[168:169],26,4096
T a[170:171],26,4608
T a[172:173],27,4096
T a[174:175],27,4608
s_waitcnt vmcnt(16)
L 0
s_barrier
v_add_u32 v26,65536,v20
v_add_u32 v27,65536,v21
T a[176:177],26,0
T a[178:179],26,512
T a[180:181],27,0
T a[182:183],27,512
T a[184:185],26,4096
T a[186:187],26,4608
T a[188:189],27,4096
T a[190:191],27,4608
s_waitcnt vmcnt(12)
L 0
s_barrier
v_add_u32 v26,81920,v20
v_add_u32 v27,81920,v21
T a[192:193],26,0
T a[194:195],26,512
T a[196:197],27,0
T a[198:199],27,512
T a[200:201],26,4096
T a[202:203],26,4608
T a[204:205],27,4096
T a[206:207],27,4608
s_waitcnt vmcnt(8)
L 0
s_barrier
v_add_u32 v26,98304,v20
v_add_u32 v27,98304,v21
T a[208:209],26,0
T a[210:211],26,512
T a[212:213],27,0
T a[214:215],27,512
T a[216:217],26,4096
T a[218:219],26,4608
T a[220:221],27,4096
T a[222:223],27,4608
s_waitcnt vmcnt(4)
L 0
s_barrier
v_add_u32 v26,0,v20
v_add_u32 v27,0,v21
T a[224:225],26,0
T a[226:227],26,512
T a[228:229],27,0
T a[230:231],27,512
T a[232:233],26,4096
T a[234:235],26,4608
T a[236:237],27,4096
T a[238:239],27,4608
s_waitcnt vmcnt(0)
L 0
s_barrier
v_add_u32 v26,16384,v20
v_add_u32 v27,16384,v21
T a[240:241],26,0
T a[242:243],26,512
T a[244:245],27,0
T a[246:247],27,512
T a[248:249],26,4096
T a[250:251],26,4608
T a[252:253],27,4096
T a[254:255],27,4608
L 0
s_barrier
v_readfirstlane_b32 s53,v4
s_nop 3
s_add_u32 s53,s53,1
s_cmp_eq_u32 s53,0
s_cselect_b32 s53,1,s53
v_mov_b32 v34,0x3f803f80
v_mov_b32 v35,0x3f803f80
v_mov_b32 v36,0x3f803f80
v_mov_b32 v37,0x3f803f80
s_nop 1
v_mfma_f32_16x16x32_bf16 v[74:77],a[0:3],v[34:37],0
v_mfma_f32_16x16x32_bf16 v[78:81],a[4:7],v[34:37],0
M 74,a[8:11],34
M 78,a[12:15],34
M 74,a[16:19],34
M 78,a[20:23],34
M 74,a[24:27],34
M 78,a[28:31],34
M 74,a[32:35],34
M 78,a[36:39],34
M 74,a[40:43],34
M 78,a[44:47],34
M 74,a[48:51],34
M 78,a[52:55],34
M 74,a[56:59],34
M 78,a[60:63],34
M 74,a[64:67],34
M 78,a[68:71],34
M 74,a[72:75],34
M 78,a[76:79],34
M 74,a[80:83],34
M 78,a[84:87],34
)"
R"(M 74,a[88:91],34
M 78,a[92:95],34
M 74,a[96:99],34
M 78,a[100:103],34
M 74,a[104:107],34
M 78,a[108:111],34
M 74,a[112:115],34
M 78,a[116:119],34
M 74,a[120:123],34
M 78,a[124:127],34
M 74,a[128:131],34
M 78,a[132:135],34
M 74,a[136:139],34
M 78,a[140:143],34
M 74,a[144:147],34
M 78,a[148:151],34
M 74,a[152:155],34
M 78,a[156:159],34
M 74,a[160:163],34
M 78,a[164:167],34
M 74,a[168:171],34
M 78,a[172:175],34
M 74,a[176:179],34
M 78,a[180:183],34
M 74,a[184:187],34
M 78,a[188:191],34
M 74,a[192:195],34
M 78,a[196:199],34
M 74,a[200:203],34
M 78,a[204:207],34
M 74,a[208:211],34
M 78,a[212:215],34
M 74,a[216:219],34
M 78,a[220:223],34
M 74,a[224:227],34
M 78,a[228:231],34
M 74,a[232:235],34
M 78,a[236:239],34
M 74,a[240:243],34
M 78,a[244:247],34
M 74,a[248:251],34
M 78,a[252:255],34
v_mov_b32 v4,s53
s_mov_b32 s82,0
s_mov_b32 s80,0
s_mov_b32 s54,s45
s_mul_i32 s57,s44,8192
s_mul_i32 s55,s45,32768
s_add_u32 s55,s55,s57
s_add_u32 s55,s55,31457280
s_mul_i32 s60,s46,32768
s_mul_i32 s61,s46,1024
s_mul_i32 s62,s46,114688
s_mul_i32 s63,s46,2560
s_mul_i32 s64,s46,1024
s_lshl_b32 s59,s44,8
s_add_u32 s59,s59,131072
s_mul_i32 s58,s45,1024
s_add_u32 s58,s58,109613056
s_mul_i32 s65,s45,114688
s_add_u32 s65,s65,s47
s_mul_i32 s68,s45,2560
s_add_u32 s68,s68,s48
s_mul_i32 s73,s45,1024
s_add_u32 s73,s73,107479040
s_mul_i32 s74,s45,32768
s_add_u32 s74,s74,s49
s_mul_i32 s75,s45,1024
s_add_u32 s75,s75,107479040
s_mov_b32 s66,0x7fff0000
s_mov_b32 s67,0x7fff0000
s_mov_b32 s69,0x7fff0000
s_mov_b32 s76,0x7fff0000
s_mov_b32 s77,0x7fff0000
s_mov_b32 s70,0x7fff0000
s_mov_b32 s71,0x7fff0000
s_mov_b32 s72,0x7fff0000
v_mov_b32 v1,v0
v_add_u32 v2,32768,v0
s_mov_b32 s84,32768
s_mov_b32 s86,0x3a800000
s_mov_b32 s87,0x3727c5ac
s_mov_b32 s88,0xc0135761
s_mov_b32 s89,0x3d372713
s_and_b32 s85,s44,1
s_mul_i32 s85,s85,127
v_lshlrev_b32 v8,22,v0
v_and_b32 v8,0x80000000,v8
v_or_b32 v8,v8,v5
s_add_u32 m0,s59,4096
s_nop 0
buffer_load_dword v5,s[36:39],s58 offen offset:0 sc1 lds
buffer_store_dword v4,v3,s[36:39],s71 offen offset:0 sc0 sc1
s_add_u32 m0,s59,5120
s_nop 0
buffer_load_dword v5,s[36:39],s58 offen offset:0 sc1 lds
buffer_store_dword v4,v3,s[36:39],s71 offen offset:0 sc0 sc1
s_mov_b32 s83,0
Lg3o_poll_%=:
s_add_u32 s78,s83,2
s_and_b32 s78,s78,3
s_lshl_b32 s78,s78,10
s_add_u32 s78,s78,4096
s_add_u32 m0,s59,s78
s_nop 0
buffer_load_dword v5,s[36:39],s58 offen offset:0 sc1 lds
s_and_b32 s78,s83,3
s_lshl_b32 s78,s78,10
v_add_u32 v82,s78,v6
s_waitcnt vmcnt(4)
s_barrier
ds_read_b32 v7,v82 offset:4096
buffer_store_dword v4,v3,s[36:39],s70 offen offset:0 sc0 sc1
s_mov_b32 s70,s71
s_mov_b32 s71,s72
s_mov_b32 s72,0x7fff0000
s_add_u32 s83,s83,1
s_add_u32 s82,s82,1
L 0
v_cmp_eq_u32 vcc,s53,v7
s_nop 0
s_cmp_eq_u32 vcc_lo,-1
s_cbranch_scc1 Lg3o_go_%=
s_cmp_lt_u32 s82,20000
s_cbranch_scc1 Lg3o_poll_%=
Lg3o_go_%=:
buffer_load_dword v98,v11,s[36:39],s73 offen offset:0 sc1
s_add_u32 s73,s73,s64
s_mov_b32 m0,s57
s_add_u32 s56,s55,4096
D 0,55,0
D 0,55,1024
D 0,55,2048
D 0,55,3072
s_add_u32 m0,s57,4096
s_nop 0
D 0,56,0
D 0,56,1024
D 0,56,2048
D 0,56,3072
s_add_u32 s55,s55,s60
s_add_u32 s57,s57,32768
s_and_b32 s57,s57,131071
s_add_u32 s54,s54,s46
Lg3o_gox_%=:
s_add_u32 s58,s58,s61
s_cmp_eq_u32 vcc_hi,-1
s_cbranch_scc1 Lg3o_go2_%=
s_add_u32 m0,s59,2048
s_nop 0
buffer_load_dword v5,s[36:39],s58 offen offset:0 sc1 lds
s_waitcnt vmcnt(0)
s_barrier
buffer_store_dword v4,v3,s[36:39],s70 offen offset:0 sc0 sc1
s_min_u32 s71,s71,s72
buffer_store_dword v4,v3,s[36:39],s71 offen offset:0 sc0 sc1
s_mov_b32 s70,0x7fff0000
s_mov_b32 s71,0x7fff0000
s_mov_b32 s72,0x7fff0000
s_cmp_eq_u32 s80,1
s_cbranch_scc1 Lg3o_st11_%=
Lg3o_st10_%=:
ds_read_b32 v7,v6 offset:2048
R 34,1,0
R 38,1,1024
R 42,1,2048
R 46,1,3072
R 50,1,4096
R 54,1,5120
R 58,1,6144
L 7
v_cmp_eq_u32 vcc,s53,v7
L 5
s_cmp_eq_u64 vcc,-1
s_cbranch_scc1 Lg3o_L20_%=
s_cmp_eq_u32 vcc_lo,-1
s_cbranch_scc1 Lg3o_L10_%=
s_branch Lg3o_X0_%=
Lg3o_st11_%=:
ds_read_b32 v7,v6 offset:2048
R 34,2,0
R 38,2,1024
R 42,2,2048
R 46,2,3072
R 50,2,4096
R 54,2,5120
R 58,2,6144
L 7
v_cmp_eq_u32 vcc,s53,v7
L 5
s_cmp_eq_u64 vcc,-1
s_cbranch_scc1 Lg3o_L21_%=
s_cmp_eq_u32 vcc_lo,-1
s_cbranch_scc1 Lg3o_L11_%=
s_branch Lg3o_X1_%=
Lg3o_go2_%=:
s_add_u32 s58,s58,s61
s_add_u32 m0,s59,2048
s_nop 0
buffer_load_dword v5,s[36:39],s58 offen offset:0 sc1 lds
s_mov_b32 m0,s57
s_add_u32 s56,s55,4096
D 0,55,0
D 0,55,1024
D 0,55,2048
D 0,55,3072
s_add_u32 m0,s57,4096
s_nop 0
D 0,56,0
D 0,56,1024
D 0,56,2048
D 0,56,3072
s_add_u32 s55,s55,s60
s_add_u32 s57,s57,32768
s_and_b32 s57,s57,131071
s_add_u32 s54,s54,s46
s_waitcnt vmcnt(8)
s_barrier
buffer_store_dword v4,v3,s[36:39],s70 offen offset:0 sc0 sc1
s_min_u32 s71,s71,s72
buffer_store_dword v4,v3,s[36:39],s71 offen offset:0 sc0 sc1
s_mov_b32 s70,0x7fff0000
s_mov_b32 s71,0x7fff0000
s_mov_b32 s72,0x7fff0000
s_cmp_eq_u32 s80,1
s_cbranch_scc1 Lg3o_st21_%=
Lg3o_st20_%=:
ds_read_b32 v7,v6 offset:2048
R 34,1,0
R 38,1,1024
R 42,1,2048
R 46,1,3072
R 50,1,4096
R 54,1,5120
R 58,1,6144
L 7
v_cmp_eq_u32 vcc,s53,v7
L 5
s_cmp_eq_u32 vcc_lo,-1
s_cbranch_scc1 Lg3o_S0_%=
s_branch Lg3o_N0_%=
Lg3o_st21_%=:
ds_read_b32 v7,v6 offset:2048
R 34,2,0
R 38,2,1024
R 42,2,2048
R 46,2,3072
R 50,2,4096
R 54,2,5120
R 58,2,6144
L 7
v_cmp_eq_u32 vcc,s53,v7
L 5
s_cmp_eq_u32 vcc_lo,-1
s_cbranch_scc1 Lg3o_S1_%=
s_branch Lg3o_N1_%=
Lg3o_L20_%=:
v_mfma_f32_16x16x32_bf16 v[18:21],a[0:3],v[34:37],0
s_add_u32 s58,s58,s61
s_add_u32 s58,s58,s61
R 62,1,7168
v_mfma_f32_16x16x32_bf16 v[22:25],a[4:7],v[34:37],0
s_add_u32 m0,s59,0
s_add_u32 s56,s55,4096
s_waitcnt vmcnt(2)
M 18,a[8:11],38
s_nop 0
R 34,1,8192
v_add_f32_dpp v99,v98,v98 quad_perm:[1,0,3,2] row_mask:0xf bank_mask:0xf
M 22,a[12:15],38
L 5
buffer_load_dword v8,s[36:39],s58 offen offset:0 sc1 lds
M 18,a[16:19],42
s_mov_b32 m0,s57
R 38,1,9216
v_add_f32_dpp v99,v99,v99 quad_perm:[2,3,0,1] row_mask:0xf bank_mask:0xf
M 22,a[20:23],42
s_mov_b32 s66,s65
buffer_load_dword v98,v11,s[36:39],s73 offen offset:0 sc1
M 18,a[24:27],46
R 42,1,10240
v_add_f32_dpp v102,v99,v99 row_half_mirror row_mask:0xf bank_mask:0xf
M 22,a[28:31],46
L 5
D 0,55,0
M 18,a[32:35],50
R 46,1,11264
v_mov_b32_dpp v99,v102 row_ror:8 row_mask:0xf bank_mask:0xf
M 22,a[36:39],50
v_fma_f32 v99,-v102,v102,v99
v_max_f32 v99,s87,v99
M 18,a[40:43],54
R 50,1,12288
v_fma_f32 v26,-v70,v74,v26
M 22,a[44:47],54
L 5
D 0,55,1024
M 18,a[48:51],58
R 54,1,13312
v_fma_f32 v27,-v70,v75,v27
M 22,a[52:55],58
v_rsq_f32 v100,v99
M 18,a[56:59],62
R 58,1,14336
v_mul_f32 v105,v99,v100
M 22,a[60:63],62
L 5
D 0,55,2048
M 18,a[64:67],34
R 62,1,15360
v_mul_f32 v103,s88,v100
M 22,a[68:71],34
v_mul_f32 v99,s89,v100
v_mul_f32 v104,v103,v100
M 18,a[72:75],38
R 34,1,16384
v_mul_f32 v104,v104,v99
M 22,a[76:79],38
L 5
ds_write_b128 v12,v[102:105] offset:0
M 18,a[80:83],42
R 38,1,17408
v_fma_f32 v28,-v70,v76,v28
M 22,a[84:87],42
D 0,55,3072
M 18,a[88:91],46
s_add_u32 m0,s57,4096
R 42,1,18432
v_fma_f32 v29,-v70,v77,v29
M 22,a[92:95],46
L 6
D 0,56,0
M 18,a[96:99],50
s_add_u32 s55,s55,s60
R 46,1,19456
v_mul_f32 v82,v26,v26
M 22,a[100:103],50
s_add_u32 s57,s57,32768
v_mul_f32 v83,v27,v27
v_mul_f32 v84,v28,v28
M 18,a[104:107],54
s_and_b32 s57,s57,131071
R 50,1,20480
v_mul_f32 v85,v29,v29
M 22,a[108:111],54
L 6
D 0,56,1024
M 18,a[112:115],58
s_add_u32 s54,s54,s46
R 54,1,21504
v_fma_f32 v82,v72,v82,v71
M 22,a[116:119],58
v_fma_f32 v83,v72,v83,v71
v_fma_f32 v84,v72,v84,v71
M 18,a[120:123],62
R 58,1,22528
v_fma_f32 v85,v72,v85,v71
M 22,a[124:127],62
L 5
D 0,56,2048
M 18,a[128:131],34
R 62,1,23552
v_mul_f32 v82,v26,v82
M 22,a[132:135],34
v_exp_f32 v82,v82
M 18,a[136:139],38
R 34,1,24576
v_mul_f32 v83,v27,v83
M 22,a[140:143],38
L 5
D 0,56,3072
M 18,a[144:147],42
s_mov_b32 m0,s57
R 38,1,25600
v_mul_f32 v84,v28,v84
M 22,a[148:151],42
s_add_u32 s56,s55,4096
v_exp_f32 v83,v83
M 18,a[152:155],46
R 42,1,26624
v_mul_f32 v85,v29,v85
M 22,a[156:159],46
L 5
D 0,55,0
M 18,a[160:163],50
R 46,1,27648
v_fma_f32 v82,v82,v73,v73
M 22,a[164:167],50
D 0,55,1024
M 18,a[168:171],54
R 50,1,28672
v_fma_f32 v83,v83,v73,v73
M 22,a[172:175],54
L 5
v_exp_f32 v84,v84
M 18,a[176:179],58
R 54,1,29696
v_fma_f32 v84,v84,v73,v73
M 22,a[180:183],58
D 0,55,2048
M 18,a[184:187],62
R 58,1,30720
v_fma_f32 v30,-v70,v78,v30
M 22,a[188:191],62
L 5
s_nop 0
D 0,55,3072
M 18,a[192:195],34
s_waitcnt vmcnt(4)
R 62,1,31744
v_fma_f32 v31,-v70,v79,v31
M 22,a[196:199],34
s_barrier
R 66,13,0
ds_read_b32 v7,v6 offset:0
M 18,a[200:203],38
s_add_u32 m0,s57,4096
R 34,2,0
v_fma_f32 v32,-v70,v80,v32
M 22,a[204:207],38
L 7
buffer_store_dword v4,v3,s[36:39],s70 offen offset:0 sc0 sc1
M 18,a[208:211],42
s_add_u32 s55,s55,s60
R 38,2,1024
v_fma_f32 v33,-v70,v81,v33
M 22,a[212:215],42
s_add_u32 s57,s57,32768
D 0,56,0
M 18,a[216:219],46
s_and_b32 s57,s57,131071
R 42,2,2048
v_mul_f32 v86,v30,v30
M 22,a[220:223],46
L 7
D 0,56,1024
M 18,a[224:227],50
s_add_u32 s54,s54,s46
R 46,2,3072
v_mul_f32 v87,v31,v31
M 22,a[228:231],50
s_add_u32 s73,s73,s64
D 0,56,2048
M 18,a[232:235],54
s_mov_b32 s70,s69
R 50,2,4096
v_mul_f32 v88,v32,v32
M 22,a[236:239],54
L 7
D 0,56,3072
M 18,a[240:243],58
s_add_u32 s65,s65,s62
R 54,2,5120
v_exp_f32 v85,v85
v_mul_f32 v89,v33,v33
v_fma_f32 v86,v72,v86,v71
v_fma_f32 v87,v72,v87,v71
v_fma_f32 v88,v72,v88,v71
v_fma_f32 v89,v72,v89,v71
v_mul_f32 v86,v30,v86
v_mul_f32 v87,v31,v87
v_mul_f32 v88,v32,v88
v_mul_f32 v89,v33,v89
v_exp_f32 v86,v86
v_exp_f32 v87,v87
v_exp_f32 v88,v88
v_exp_f32 v89,v89
M 22,a[244:247],58
s_mov_b32 s69,s68
L 6
v_fma_f32 v85,v85,v73,v73
v_rcp_f32 v82,v82
v_rcp_f32 v83,v83
v_rcp_f32 v84,v84
v_rcp_f32 v85,v85
v_fma_f32 v86,v86,v73,v73
v_fma_f32 v87,v87,v73,v73
v_fma_f32 v88,v88,v73,v73
v_fma_f32 v89,v89,v73,v73
v_rcp_f32 v86,v86
v_rcp_f32 v87,v87
v_rcp_f32 v88,v88
v_rcp_f32 v89,v89
v_cmp_eq_u32 vcc,s53,v7
M 18,a[248:251],62
s_add_u32 s68,s68,s63
s_cmp_eq_u32 vcc_lo,-1
R 58,2,6144
v_mul_f32 v26,v26,v82
v_mul_f32 v27,v27,v83
v_mul_f32 v28,v28,v84
v_mul_f32 v29,v29,v85
v_cvt_pk_bf16_f32 v90,v26,v27
v_cvt_pk_bf16_f32 v91,v28,v29
v_mul_f32 v30,v30,v86
v_mul_f32 v31,v31,v87
v_mul_f32 v32,v32,v88
v_mul_f32 v33,v33,v89
v_cvt_pk_bf16_f32 v92,v30,v31
v_cvt_pk_bf16_f32 v93,v32,v33
)"
R"(M 22,a[252:255],62
L 5
buffer_store_dwordx4 v[90:93],v0,s[36:39],s67 offen offset:0 sc0 sc1
v_xor_b32 v1,65536,v1
s_cbranch_scc0 Lg3o_N1_%=
Lg3o_S1_%=:
v_mfma_f32_16x16x32_bf16 v[26:29],a[0:3],v[34:37],0
s_add_u32 s58,s58,s61
s_add_u32 m0,s59,1024
R 62,2,7168
v_mfma_f32_16x16x32_bf16 v[30:33],a[4:7],v[34:37],0
s_waitcnt vmcnt(10)
buffer_load_dword v8,s[36:39],s58 offen offset:0 sc1 lds
M 26,a[8:11],38
s_mov_b32 m0,s57
R 34,2,8192
v_add_f32_dpp v99,v98,v98 quad_perm:[1,0,3,2] row_mask:0xf bank_mask:0xf
M 30,a[12:15],38
L 5
buffer_load_dword v98,v11,s[36:39],s73 offen offset:0 sc1
M 26,a[16:19],42
s_add_u32 s56,s55,4096
R 38,2,9216
v_add_f32_dpp v99,v99,v99 quad_perm:[2,3,0,1] row_mask:0xf bank_mask:0xf
M 30,a[20:23],42
s_nop 0
v_fma_f32 v18,-v66,v74,v18
v_fma_f32 v19,-v66,v75,v19
M 26,a[24:27],46
s_mov_b32 s67,s65
R 42,2,10240
v_add_f32_dpp v102,v99,v99 row_half_mirror row_mask:0xf bank_mask:0xf
M 30,a[28:31],46
L 5
D 0,55,0
M 26,a[32:35],50
R 46,2,11264
v_mov_b32_dpp v99,v102 row_ror:8 row_mask:0xf bank_mask:0xf
M 30,a[36:39],50
v_fma_f32 v99,-v102,v102,v99
v_max_f32 v99,s87,v99
M 26,a[40:43],54
R 50,2,12288
v_fma_f32 v20,-v66,v76,v20
M 30,a[44:47],54
L 5
v_rsq_f32 v100,v99
M 26,a[48:51],58
R 54,2,13312
v_mul_f32 v105,v99,v100
M 30,a[52:55],58
D 0,55,1024
M 26,a[56:59],62
R 58,2,14336
v_mul_f32 v103,s88,v100
M 30,a[60:63],62
L 5
v_mul_f32 v99,s89,v100
v_mul_f32 v104,v103,v100
M 26,a[64:67],34
R 62,2,15360
v_mul_f32 v104,v104,v99
M 30,a[68:71],34
ds_write_b128 v12,v[102:105] offset:2048
M 26,a[72:75],38
R 34,2,16384
v_fma_f32 v21,-v66,v77,v21
M 30,a[76:79],38
L 6
v_mul_f32 v82,v18,v18
v_mul_f32 v83,v19,v19
M 26,a[80:83],42
R 38,2,17408
v_mul_f32 v84,v20,v20
M 30,a[84:87],42
D 0,55,2048
M 26,a[88:91],46
R 42,2,18432
v_mul_f32 v85,v21,v21
M 30,a[92:95],46
L 6
v_fma_f32 v82,v68,v82,v67
v_fma_f32 v83,v68,v83,v67
M 26,a[96:99],50
R 46,2,19456
v_fma_f32 v84,v68,v84,v67
M 30,a[100:103],50
v_fma_f32 v85,v68,v85,v67
v_mul_f32 v82,v18,v82
M 26,a[104:107],54
R 50,2,20480
v_mul_f32 v83,v19,v83
M 30,a[108:111],54
L 6
D 0,55,3072
M 26,a[112:115],58
s_add_u32 m0,s57,4096
R 54,2,21504
v_mul_f32 v84,v20,v84
M 30,a[116:119],58
s_add_u32 s55,s55,s60
v_exp_f32 v82,v82
M 26,a[120:123],62
s_add_u32 s57,s57,32768
R 58,2,22528
v_mul_f32 v85,v21,v85
M 30,a[124:127],62
L 5
v_exp_f32 v83,v83
M 26,a[128:131],34
s_and_b32 s57,s57,131071
R 62,2,23552
v_fma_f32 v82,v82,v69,v69
M 30,a[132:135],34
s_add_u32 s54,s54,s46
v_exp_f32 v84,v84
M 26,a[136:139],38
s_add_u32 s73,s73,s64
R 34,2,24576
v_fma_f32 v83,v83,v69,v69
M 30,a[140:143],38
L 5
D 0,56,0
M 26,a[144:147],42
s_add_u32 s65,s65,s62
R 38,2,25600
v_fma_f32 v84,v84,v69,v69
M 30,a[148:151],42
v_exp_f32 v85,v85
M 26,a[152:155],46
R 42,2,26624
v_fma_f32 v85,v85,v69,v69
M 30,a[156:159],46
L 5
v_rcp_f32 v82,v82
M 26,a[160:163],50
R 46,2,27648
v_mul_f32 v18,v18,v82
M 30,a[164:167],50
D 0,56,1024
M 26,a[168:171],54
R 50,2,28672
v_fma_f32 v22,-v66,v78,v22
M 30,a[172:175],54
L 5
v_rcp_f32 v83,v83
M 26,a[176:179],58
R 54,2,29696
v_mul_f32 v19,v19,v83
M 30,a[180:183],58
v_rcp_f32 v84,v84
M 26,a[184:187],62
R 58,2,30720
v_mul_f32 v20,v20,v84
M 30,a[188:191],62
L 5
s_nop 0
v_cvt_pk_bf16_f32 v90,v18,v19
M 26,a[192:195],34
s_waitcnt vmcnt(7)
R 62,2,31744
v_fma_f32 v23,-v66,v79,v23
M 30,a[196:199],34
s_barrier
R 70,13,2048
ds_read_b32 v7,v6 offset:1024
M 26,a[200:203],38
R 34,1,0
v_fma_f32 v24,-v66,v80,v24
M 30,a[204:207],38
L 7
buffer_store_dword v4,v3,s[36:39],s70 offen offset:0 sc0 sc1
M 26,a[208:211],42
s_mov_b32 s70,s69
R 38,1,1024
v_fma_f32 v25,-v66,v81,v25
M 30,a[212:215],42
s_mov_b32 s69,s68
D 0,56,2048
M 26,a[216:219],46
L 2
R 42,1,2048
v_mul_f32 v86,v22,v22
M 30,a[220:223],46
s_nop 0
D 0,56,3072
M 26,a[224:227],50
s_add_u32 s68,s68,s63
R 46,1,3072
v_mul_f32 v87,v23,v23
M 30,a[228:231],50
v_mul_f32 v88,v24,v24
v_mul_f32 v89,v25,v25
M 26,a[232:235],54
R 50,1,4096
v_fma_f32 v86,v68,v86,v67
M 30,a[236:239],54
s_nop 0
v_fma_f32 v87,v68,v87,v67
v_fma_f32 v88,v68,v88,v67
M 26,a[240:243],58
R 54,1,5120
v_fma_f32 v89,v68,v89,v67
v_mul_f32 v86,v22,v86
v_mul_f32 v87,v23,v87
v_mul_f32 v88,v24,v88
v_mul_f32 v89,v25,v89
v_exp_f32 v86,v86
v_exp_f32 v87,v87
v_exp_f32 v88,v88
v_exp_f32 v89,v89
M 30,a[244:247],58
v_rcp_f32 v85,v85
v_fma_f32 v86,v86,v69,v69
v_fma_f32 v87,v87,v69,v69
v_fma_f32 v88,v88,v69,v69
v_fma_f32 v89,v89,v69,v69
v_rcp_f32 v86,v86
v_rcp_f32 v87,v87
v_rcp_f32 v88,v88
v_rcp_f32 v89,v89
v_cmp_eq_u32 vcc,s53,v7
M 26,a[248:251],62
s_cmp_eq_u32 vcc_lo,-1
R 58,1,6144
v_mul_f32 v21,v21,v85
v_cvt_pk_bf16_f32 v91,v20,v21
v_mul_f32 v22,v22,v86
v_mul_f32 v23,v23,v87
v_mul_f32 v24,v24,v88
v_mul_f32 v25,v25,v89
v_cvt_pk_bf16_f32 v92,v22,v23
v_cvt_pk_bf16_f32 v93,v24,v25
M 30,a[252:255],62
L 5
buffer_store_dwordx4 v[90:93],v0,s[36:39],s66 offen offset:0 sc0 sc1
v_xor_b32 v2,65536,v2
s_cbranch_scc0 Lg3o_N0_%=
Lg3o_S0_%=:
v_mfma_f32_16x16x32_bf16 v[18:21],a[0:3],v[34:37],0
s_add_u32 s58,s58,s61
s_add_u32 m0,s59,0
R 62,1,7168
v_mfma_f32_16x16x32_bf16 v[22:25],a[4:7],v[34:37],0
s_waitcnt vmcnt(10)
buffer_load_dword v8,s[36:39],s58 offen offset:0 sc1 lds
M 18,a[8:11],38
s_mov_b32 m0,s57
R 34,1,8192
v_add_f32_dpp v99,v98,v98 quad_perm:[1,0,3,2] row_mask:0xf bank_mask:0xf
M 22,a[12:15],38
L 5
buffer_load_dword v98,v11,s[36:39],s73 offen offset:0 sc1
M 18,a[16:19],42
s_add_u32 s56,s55,4096
R 38,1,9216
v_add_f32_dpp v99,v99,v99 quad_perm:[2,3,0,1] row_mask:0xf bank_mask:0xf
M 22,a[20:23],42
s_nop 0
v_fma_f32 v26,-v70,v74,v26
v_fma_f32 v27,-v70,v75,v27
M 18,a[24:27],46
s_mov_b32 s66,s65
R 42,1,10240
v_add_f32_dpp v102,v99,v99 row_half_mirror row_mask:0xf bank_mask:0xf
M 22,a[28:31],46
L 5
D 0,55,0
M 18,a[32:35],50
R 46,1,11264
v_mov_b32_dpp v99,v102 row_ror:8 row_mask:0xf bank_mask:0xf
M 22,a[36:39],50
v_fma_f32 v99,-v102,v102,v99
v_max_f32 v99,s87,v99
M 18,a[40:43],54
R 50,1,12288
v_fma_f32 v28,-v70,v76,v28
M 22,a[44:47],54
L 5
v_rsq_f32 v100,v99
M 18,a[48:51],58
R 54,1,13312
v_mul_f32 v105,v99,v100
M 22,a[52:55],58
D 0,55,1024
M 18,a[56:59],62
R 58,1,14336
v_mul_f32 v103,s88,v100
M 22,a[60:63],62
L 5
v_mul_f32 v99,s89,v100
v_mul_f32 v104,v103,v100
M 18,a[64:67],34
R 62,1,15360
v_mul_f32 v104,v104,v99
M 22,a[68:71],34
ds_write_b128 v12,v[102:105] offset:0
M 18,a[72:75],38
R 34,1,16384
v_fma_f32 v29,-v70,v77,v29
M 22,a[76:79],38
L 6
v_mul_f32 v82,v26,v26
v_mul_f32 v83,v27,v27
M 18,a[80:83],42
R 38,1,17408
v_mul_f32 v84,v28,v28
M 22,a[84:87],42
D 0,55,2048
M 18,a[88:91],46
R 42,1,18432
v_mul_f32 v85,v29,v29
M 22,a[92:95],46
L 6
v_fma_f32 v82,v72,v82,v71
v_fma_f32 v83,v72,v83,v71
M 18,a[96:99],50
R 46,1,19456
v_fma_f32 v84,v72,v84,v71
M 22,a[100:103],50
v_fma_f32 v85,v72,v85,v71
v_mul_f32 v82,v26,v82
M 18,a[104:107],54
R 50,1,20480
v_mul_f32 v83,v27,v83
M 22,a[108:111],54
L 6
D 0,55,3072
M 18,a[112:115],58
s_add_u32 m0,s57,4096
R 54,1,21504
v_mul_f32 v84,v28,v84
M 22,a[116:119],58
s_add_u32 s55,s55,s60
v_exp_f32 v82,v82
M 18,a[120:123],62
s_add_u32 s57,s57,32768
R 58,1,22528
v_mul_f32 v85,v29,v85
M 22,a[124:127],62
L 5
v_exp_f32 v83,v83
M 18,a[128:131],34
s_and_b32 s57,s57,131071
R 62,1,23552
v_fma_f32 v82,v82,v73,v73
M 22,a[132:135],34
s_add_u32 s54,s54,s46
v_exp_f32 v84,v84
M 18,a[136:139],38
s_add_u32 s73,s73,s64
R 34,1,24576
v_fma_f32 v83,v83,v73,v73
M 22,a[140:143],38
L 5
D 0,56,0
M 18,a[144:147],42
s_add_u32 s65,s65,s62
R 38,1,25600
v_fma_f32 v84,v84,v73,v73
M 22,a[148:151],42
v_exp_f32 v85,v85
M 18,a[152:155],46
R 42,1,26624
v_fma_f32 v85,v85,v73,v73
M 22,a[156:159],46
L 5
v_rcp_f32 v82,v82
M 18,a[160:163],50
R 46,1,27648
v_mul_f32 v26,v26,v82
M 22,a[164:167],50
D 0,56,1024
M 18,a[168:171],54
R 50,1,28672
v_fma_f32 v30,-v70,v78,v30
M 22,a[172:175],54
L 5
v_rcp_f32 v83,v83
M 18,a[176:179],58
R 54,1,29696
v_mul_f32 v27,v27,v83
M 22,a[180:183],58
v_rcp_f32 v84,v84
M 18,a[184:187],62
R 58,1,30720
v_mul_f32 v28,v28,v84
M 22,a[188:191],62
L 5
s_nop 0
v_cvt_pk_bf16_f32 v90,v26,v27
M 18,a[192:195],34
s_waitcnt vmcnt(7)
R 62,1,31744
v_fma_f32 v31,-v70,v79,v31
M 22,a[196:199],34
s_barrier
R 66,13,0
ds_read_b32 v7,v6 offset:0
M 18,a[200:203],38
R 34,2,0
v_fma_f32 v32,-v70,v80,v32
M 22,a[204:207],38
L 7
buffer_store_dword v4,v3,s[36:39],s70 offen offset:0 sc0 sc1
M 18,a[208:211],42
s_mov_b32 s70,s69
R 38,2,1024
v_fma_f32 v33,-v70,v81,v33
M 22,a[212:215],42
s_mov_b32 s69,s68
D 0,56,2048
M 18,a[216:219],46
L 2
R 42,2,2048
v_mul_f32 v86,v30,v30
M 22,a[220:223],46
s_nop 0
D 0,56,3072
M 18,a[224:227],50
s_add_u32 s68,s68,s63
R 46,2,3072
v_mul_f32 v87,v31,v31
M 22,a[228:231],50
v_mul_f32 v88,v32,v32
v_mul_f32 v89,v33,v33
M 18,a[232:235],54
R 50,2,4096
v_fma_f32 v86,v72,v86,v71
M 22,a[236:239],54
s_nop 0
v_fma_f32 v87,v72,v87,v71
v_fma_f32 v88,v72,v88,v71
M 18,a[240:243],58
R 54,2,5120
v_fma_f32 v89,v72,v89,v71
v_mul_f32 v86,v30,v86
v_mul_f32 v87,v31,v87
v_mul_f32 v88,v32,v88
v_mul_f32 v89,v33,v89
v_exp_f32 v86,v86
v_exp_f32 v87,v87
v_exp_f32 v88,v88
v_exp_f32 v89,v89
M 22,a[244:247],58
v_rcp_f32 v85,v85
v_fma_f32 v86,v86,v73,v73
v_fma_f32 v87,v87,v73,v73
v_fma_f32 v88,v88,v73,v73
v_fma_f32 v89,v89,v73,v73
v_rcp_f32 v86,v86
v_rcp_f32 v87,v87
v_rcp_f32 v88,v88
v_rcp_f32 v89,v89
v_cmp_eq_u32 vcc,s53,v7
M 18,a[248:251],62
s_cmp_eq_u32 vcc_lo,-1
R 58,2,6144
v_mul_f32 v29,v29,v85
v_cvt_pk_bf16_f32 v91,v28,v29
v_mul_f32 v30,v30,v86
v_mul_f32 v31,v31,v87
v_mul_f32 v32,v32,v88
v_mul_f32 v33,v33,v89
v_cvt_pk_bf16_f32 v92,v30,v31
v_cvt_pk_bf16_f32 v93,v32,v33
M 22,a[252:255],62
L 5
buffer_store_dwordx4 v[90:93],v0,s[36:39],s67 offen offset:0 sc0 sc1
v_xor_b32 v1,65536,v1
s_cbranch_scc1 Lg3o_S1_%=
Lg3o_N1_%=:
v_mfma_f32_16x16x32_bf16 v[26:29],a[0:3],v[34:37],0
s_add_u32 m0,s59,1024
s_waitcnt vmcnt(10)
R 62,2,7168
v_mfma_f32_16x16x32_bf16 v[30:33],a[4:7],v[34:37],0
s_nop 0
buffer_load_dword v5,s[36:39],s58 offen offset:0 sc1 lds
M 26,a[8:11],38
s_mov_b32 s67,s65
R 34,2,8192
v_add_f32_dpp v99,v98,v98 quad_perm:[1,0,3,2] row_mask:0xf bank_mask:0xf
M 30,a[12:15],38
L 5
buffer_load_dword v98,v11,s[36:39],s73 offen offset:0 sc1
M 26,a[16:19],42
s_add_u32 s73,s73,s64
R 38,2,9216
v_add_f32_dpp v99,v99,v99 quad_perm:[2,3,0,1] row_mask:0xf bank_mask:0xf
M 30,a[20:23],42
s_add_u32 s65,s65,s62
v_fma_f32 v18,-v66,v74,v18
v_fma_f32 v19,-v66,v75,v19
M 26,a[24:27],46
R 42,2,10240
v_add_f32_dpp v102,v99,v99 row_half_mirror row_mask:0xf bank_mask:0xf
M 30,a[28:31],46
L 5
v_fma_f32 v20,-v66,v76,v20
)"
R"(v_fma_f32 v21,-v66,v77,v21
M 26,a[32:35],50
R 46,2,11264
v_mov_b32_dpp v99,v102 row_ror:8 row_mask:0xf bank_mask:0xf
M 30,a[36:39],50
v_fma_f32 v99,-v102,v102,v99
v_max_f32 v99,s87,v99
M 26,a[40:43],54
R 50,2,12288
v_mul_f32 v82,v18,v18
M 30,a[44:47],54
L 5
v_rsq_f32 v100,v99
M 26,a[48:51],58
R 54,2,13312
v_mul_f32 v105,v99,v100
M 30,a[52:55],58
v_mul_f32 v103,s88,v100
v_mul_f32 v99,s89,v100
M 26,a[56:59],62
R 58,2,14336
v_mul_f32 v104,v103,v100
M 30,a[60:63],62
L 5
v_mul_f32 v104,v104,v99
v_mul_f32 v83,v19,v19
M 26,a[64:67],34
R 62,2,15360
v_mul_f32 v84,v20,v20
M 30,a[68:71],34
ds_write_b128 v12,v[102:105] offset:2048
M 26,a[72:75],38
R 34,2,16384
v_mul_f32 v85,v21,v21
M 30,a[76:79],38
L 6
v_fma_f32 v82,v68,v82,v67
v_fma_f32 v83,v68,v83,v67
M 26,a[80:83],42
R 38,2,17408
v_fma_f32 v84,v68,v84,v67
M 30,a[84:87],42
v_fma_f32 v85,v68,v85,v67
v_mul_f32 v82,v18,v82
M 26,a[88:91],46
R 42,2,18432
v_mul_f32 v83,v19,v83
M 30,a[92:95],46
L 6
v_exp_f32 v82,v82
M 26,a[96:99],50
R 46,2,19456
v_mul_f32 v84,v20,v84
M 30,a[100:103],50
v_exp_f32 v83,v83
M 26,a[104:107],54
R 50,2,20480
v_mul_f32 v85,v21,v85
M 30,a[108:111],54
L 6
v_exp_f32 v84,v84
M 26,a[112:115],58
R 54,2,21504
v_fma_f32 v82,v82,v69,v69
M 30,a[116:119],58
v_exp_f32 v85,v85
M 26,a[120:123],62
R 58,2,22528
v_fma_f32 v83,v83,v69,v69
M 30,a[124:127],62
L 5
v_rcp_f32 v82,v82
M 26,a[128:131],34
R 62,2,23552
v_fma_f32 v84,v84,v69,v69
M 30,a[132:135],34
v_rcp_f32 v83,v83
M 26,a[136:139],38
R 34,2,24576
v_fma_f32 v85,v85,v69,v69
M 30,a[140:143],38
L 5
v_rcp_f32 v84,v84
M 26,a[144:147],42
R 38,2,25600
v_mul_f32 v18,v18,v82
M 30,a[148:151],42
v_rcp_f32 v85,v85
M 26,a[152:155],46
R 42,2,26624
v_mul_f32 v19,v19,v83
M 30,a[156:159],46
L 5
v_mul_f32 v20,v20,v84
v_mul_f32 v21,v21,v85
M 26,a[160:163],50
R 46,2,27648
v_cvt_pk_bf16_f32 v90,v18,v19
M 30,a[164:167],50
v_cvt_pk_bf16_f32 v91,v20,v21
v_fma_f32 v22,-v66,v78,v22
M 26,a[168:171],54
R 50,2,28672
v_fma_f32 v23,-v66,v79,v23
M 30,a[172:175],54
L 5
v_fma_f32 v24,-v66,v80,v24
v_fma_f32 v25,-v66,v81,v25
M 26,a[176:179],58
R 54,2,29696
v_mul_f32 v86,v22,v22
M 30,a[180:183],58
v_mul_f32 v87,v23,v23
v_mul_f32 v88,v24,v24
M 26,a[184:187],62
R 58,2,30720
v_mul_f32 v89,v25,v25
M 30,a[188:191],62
L 5
s_nop 0
v_fma_f32 v86,v68,v86,v67
M 26,a[192:195],34
s_waitcnt vmcnt(1)
R 62,2,31744
v_fma_f32 v87,v68,v87,v67
M 30,a[196:199],34
s_barrier
R 70,13,2048
ds_read_b32 v7,v6 offset:1024
M 26,a[200:203],38
R 34,1,0
v_fma_f32 v88,v68,v88,v67
M 30,a[204:207],38
L 7
buffer_store_dword v4,v3,s[36:39],s70 offen offset:0 sc0 sc1
M 26,a[208:211],42
s_mov_b32 s70,s69
R 38,1,1024
v_fma_f32 v89,v68,v89,v67
M 30,a[212:215],42
s_mov_b32 s69,s68
v_mul_f32 v86,v22,v86
v_mul_f32 v87,v23,v87
M 26,a[216:219],46
L 2
R 42,1,2048
v_mul_f32 v88,v24,v88
M 30,a[220:223],46
s_nop 0
v_exp_f32 v86,v86
M 26,a[224:227],50
s_add_u32 s68,s68,s63
R 46,1,3072
v_mul_f32 v89,v25,v89
M 30,a[228:231],50
v_exp_f32 v87,v87
M 26,a[232:235],54
R 50,1,4096
v_fma_f32 v86,v86,v69,v69
M 30,a[236:239],54
s_nop 0
v_exp_f32 v88,v88
M 26,a[240:243],58
R 54,1,5120
v_exp_f32 v89,v89
M 30,a[244:247],58
v_fma_f32 v87,v87,v69,v69
v_fma_f32 v88,v88,v69,v69
v_fma_f32 v89,v89,v69,v69
v_rcp_f32 v86,v86
v_rcp_f32 v87,v87
v_rcp_f32 v88,v88
v_rcp_f32 v89,v89
v_cmp_eq_u32 vcc,s53,v7
M 26,a[248:251],62
s_cmp_eq_u64 vcc,-1
R 58,1,6144
v_mul_f32 v22,v22,v86
v_mul_f32 v23,v23,v87
v_mul_f32 v24,v24,v88
v_mul_f32 v25,v25,v89
v_cvt_pk_bf16_f32 v92,v22,v23
v_cvt_pk_bf16_f32 v93,v24,v25
M 30,a[252:255],62
L 5
buffer_store_dwordx4 v[90:93],v0,s[36:39],s66 offen offset:0 sc0 sc1
v_xor_b32 v2,65536,v2
s_cbranch_scc1 Lg3o_L20_%=
s_cmp_eq_u32 vcc_lo,-1
s_cbranch_scc1 Lg3o_L10_%=
Lg3o_X0_%=:
v_mfma_f32_16x16x32_bf16 v[18:21],a[0:3],v[34:37],0
s_add_u32 m0,s59,0
s_waitcnt vmcnt(2)
R 62,1,7168
v_mfma_f32_16x16x32_bf16 v[22:25],a[4:7],v[34:37],0
s_nop 0
buffer_load_dword v5,s[36:39],s58 offen offset:0 sc1 lds
M 18,a[8:11],38
s_mov_b32 s66,s65
R 34,1,8192
v_add_f32_dpp v99,v98,v98 quad_perm:[1,0,3,2] row_mask:0xf bank_mask:0xf
M 22,a[12:15],38
L 5
v_fma_f32 v26,-v70,v74,v26
v_fma_f32 v27,-v70,v75,v27
M 18,a[16:19],42
s_add_u32 s65,s65,s62
R 38,1,9216
v_add_f32_dpp v99,v99,v99 quad_perm:[2,3,0,1] row_mask:0xf bank_mask:0xf
M 22,a[20:23],42
v_fma_f32 v28,-v70,v76,v28
v_fma_f32 v29,-v70,v77,v29
M 18,a[24:27],46
R 42,1,10240
v_add_f32_dpp v102,v99,v99 row_half_mirror row_mask:0xf bank_mask:0xf
M 22,a[28:31],46
L 5
v_mul_f32 v82,v26,v26
v_mul_f32 v83,v27,v27
M 18,a[32:35],50
R 46,1,11264
v_mov_b32_dpp v99,v102 row_ror:8 row_mask:0xf bank_mask:0xf
M 22,a[36:39],50
v_fma_f32 v99,-v102,v102,v99
v_max_f32 v99,s87,v99
M 18,a[40:43],54
R 50,1,12288
v_mul_f32 v84,v28,v28
M 22,a[44:47],54
L 5
v_rsq_f32 v100,v99
M 18,a[48:51],58
R 54,1,13312
v_mul_f32 v105,v99,v100
M 22,a[52:55],58
v_mul_f32 v103,s88,v100
v_mul_f32 v99,s89,v100
M 18,a[56:59],62
R 58,1,14336
v_mul_f32 v104,v103,v100
M 22,a[60:63],62
L 5
v_mul_f32 v104,v104,v99
v_mul_f32 v85,v29,v29
M 18,a[64:67],34
R 62,1,15360
v_fma_f32 v82,v72,v82,v71
M 22,a[68:71],34
ds_write_b128 v12,v[102:105] offset:0
M 18,a[72:75],38
R 34,1,16384
v_fma_f32 v83,v72,v83,v71
M 22,a[76:79],38
L 6
v_fma_f32 v84,v72,v84,v71
v_fma_f32 v85,v72,v85,v71
M 18,a[80:83],42
R 38,1,17408
v_mul_f32 v82,v26,v82
M 22,a[84:87],42
v_exp_f32 v82,v82
M 18,a[88:91],46
R 42,1,18432
v_mul_f32 v83,v27,v83
M 22,a[92:95],46
L 6
v_exp_f32 v83,v83
M 18,a[96:99],50
R 46,1,19456
v_mul_f32 v84,v28,v84
M 22,a[100:103],50
v_exp_f32 v84,v84
M 18,a[104:107],54
R 50,1,20480
v_mul_f32 v85,v29,v85
M 22,a[108:111],54
L 6
v_exp_f32 v85,v85
M 18,a[112:115],58
R 54,1,21504
v_fma_f32 v82,v82,v73,v73
M 22,a[116:119],58
v_rcp_f32 v82,v82
M 18,a[120:123],62
R 58,1,22528
v_fma_f32 v83,v83,v73,v73
M 22,a[124:127],62
L 5
v_rcp_f32 v83,v83
M 18,a[128:131],34
R 62,1,23552
v_fma_f32 v84,v84,v73,v73
M 22,a[132:135],34
v_rcp_f32 v84,v84
M 18,a[136:139],38
R 34,1,24576
v_fma_f32 v85,v85,v73,v73
M 22,a[140:143],38
L 5
v_rcp_f32 v85,v85
M 18,a[144:147],42
R 38,1,25600
v_mul_f32 v26,v26,v82
M 22,a[148:151],42
v_mul_f32 v27,v27,v83
v_mul_f32 v28,v28,v84
M 18,a[152:155],46
R 42,1,26624
v_mul_f32 v29,v29,v85
M 22,a[156:159],46
L 5
v_cvt_pk_bf16_f32 v90,v26,v27
v_cvt_pk_bf16_f32 v91,v28,v29
M 18,a[160:163],50
R 46,1,27648
v_fma_f32 v30,-v70,v78,v30
M 22,a[164:167],50
v_fma_f32 v31,-v70,v79,v31
v_fma_f32 v32,-v70,v80,v32
M 18,a[168:171],54
R 50,1,28672
v_fma_f32 v33,-v70,v81,v33
M 22,a[172:175],54
L 5
v_mul_f32 v86,v30,v30
v_mul_f32 v87,v31,v31
M 18,a[176:179],58
R 54,1,29696
v_mul_f32 v88,v32,v32
M 22,a[180:183],58
v_mul_f32 v89,v33,v33
v_fma_f32 v86,v72,v86,v71
M 18,a[184:187],62
R 58,1,30720
v_fma_f32 v87,v72,v87,v71
M 22,a[188:191],62
L 5
s_nop 0
v_fma_f32 v88,v72,v88,v71
M 18,a[192:195],34
s_waitcnt vmcnt(0)
R 62,1,31744
v_fma_f32 v89,v72,v89,v71
M 22,a[196:199],34
s_barrier
R 66,13,0
ds_read_b32 v7,v6 offset:0
M 18,a[200:203],38
buffer_store_dword v4,v3,s[36:39],s70 offen offset:0 sc0 sc1
M 22,a[204:207],38
L 6
v_mul_f32 v86,v30,v86
v_mul_f32 v87,v31,v87
M 18,a[208:211],42
s_mov_b32 s70,s69
v_exp_f32 v86,v86
M 22,a[212:215],42
s_mov_b32 s69,s68
v_exp_f32 v87,v87
M 18,a[216:219],46
L 0
v_mul_f32 v88,v32,v88
v_mul_f32 v89,v33,v89
M 22,a[220:223],46
s_nop 0
v_exp_f32 v88,v88
M 18,a[224:227],50
s_add_u32 s68,s68,s63
v_exp_f32 v89,v89
M 22,a[228:231],50
v_fma_f32 v86,v86,v73,v73
v_fma_f32 v87,v87,v73,v73
M 18,a[232:235],54
v_fma_f32 v88,v88,v73,v73
v_fma_f32 v89,v89,v73,v73
M 22,a[236:239],54
s_nop 0
v_rcp_f32 v86,v86
M 18,a[240:243],58
v_rcp_f32 v87,v87
M 22,a[244:247],58
v_rcp_f32 v88,v88
v_rcp_f32 v89,v89
M 18,a[248:251],62
v_mul_f32 v30,v30,v86
v_mul_f32 v31,v31,v87
v_mul_f32 v32,v32,v88
v_mul_f32 v33,v33,v89
v_cvt_pk_bf16_f32 v92,v30,v31
v_cvt_pk_bf16_f32 v93,v32,v33
M 22,a[252:255],62
buffer_store_dwordx4 v[90:93],v0,s[36:39],s67 offen offset:0 sc0 sc1
v_xor_b32 v1,65536,v1
v_cmp_eq_u32 vcc,s53,v7
s_mov_b32 s80,1
s_cmp_eq_u32 vcc_lo,-1
s_cbranch_scc0 Lg3o_xn0_%=
buffer_load_dword v98,v11,s[36:39],s73 offen offset:0 sc1
s_nop 0
v_fma_f32 v18,-v66,v74,v18
v_fma_f32 v19,-v66,v75,v19
v_fma_f32 v20,-v66,v76,v20
v_fma_f32 v21,-v66,v77,v21
v_mul_f32 v82,v18,v18
v_mul_f32 v83,v19,v19
s_add_u32 s73,s73,s64
s_mov_b32 m0,s57
s_add_u32 s56,s55,4096
D 0,55,0
v_mul_f32 v84,v20,v20
v_mul_f32 v85,v21,v21
v_fma_f32 v82,v68,v82,v67
v_fma_f32 v83,v68,v83,v67
v_fma_f32 v84,v68,v84,v67
v_fma_f32 v85,v68,v85,v67
D 0,55,1024
v_mul_f32 v82,v18,v82
v_mul_f32 v83,v19,v83
v_mul_f32 v84,v20,v84
v_mul_f32 v85,v21,v85
v_exp_f32 v82,v82
v_exp_f32 v83,v83
D 0,55,2048
v_exp_f32 v84,v84
v_exp_f32 v85,v85
v_fma_f32 v82,v82,v69,v69
v_fma_f32 v83,v83,v69,v69
v_fma_f32 v84,v84,v69,v69
v_fma_f32 v85,v85,v69,v69
D 0,55,3072
v_rcp_f32 v82,v82
v_rcp_f32 v83,v83
v_rcp_f32 v84,v84
v_rcp_f32 v85,v85
v_mul_f32 v18,v18,v82
v_mul_f32 v19,v19,v83
s_add_u32 m0,s57,4096
s_nop 0
D 0,56,0
v_mul_f32 v20,v20,v84
v_mul_f32 v21,v21,v85
v_cvt_pk_bf16_f32 v90,v18,v19
v_cvt_pk_bf16_f32 v91,v20,v21
v_fma_f32 v22,-v66,v78,v22
v_fma_f32 v23,-v66,v79,v23
D 0,56,1024
v_fma_f32 v24,-v66,v80,v24
v_fma_f32 v25,-v66,v81,v25
v_mul_f32 v86,v22,v22
v_mul_f32 v87,v23,v23
v_mul_f32 v88,v24,v24
v_mul_f32 v89,v25,v25
D 0,56,2048
v_fma_f32 v86,v68,v86,v67
v_fma_f32 v87,v68,v87,v67
v_fma_f32 v88,v68,v88,v67
v_fma_f32 v89,v68,v89,v67
v_mul_f32 v86,v22,v86
v_mul_f32 v87,v23,v87
D 0,56,3072
v_mul_f32 v88,v24,v88
v_mul_f32 v89,v25,v89
v_exp_f32 v86,v86
v_exp_f32 v87,v87
v_exp_f32 v88,v88
v_exp_f32 v89,v89
s_add_u32 s55,s55,s60
s_add_u32 s57,s57,32768
s_and_b32 s57,s57,131071
s_add_u32 s54,s54,s46
v_fma_f32 v86,v86,v69,v69
v_fma_f32 v87,v87,v69,v69
v_fma_f32 v88,v88,v69,v69
v_fma_f32 v89,v89,v69,v69
v_rcp_f32 v86,v86
v_rcp_f32 v87,v87
v_rcp_f32 v88,v88
v_rcp_f32 v89,v89
v_mul_f32 v22,v22,v86
v_mul_f32 v23,v23,v87
v_mul_f32 v24,v24,v88
v_mul_f32 v25,v25,v89
v_cvt_pk_bf16_f32 v92,v22,v23
v_cvt_pk_bf16_f32 v93,v24,v25
buffer_store_dwordx4 v[90:93],v0,s[36:39],s66 offen offset:0 sc0 sc1
s_mov_b32 s72,s69
s_mov_b32 s69,0x7fff0000
s_mov_b32 s66,0x7fff0000
s_mov_b32 s76,0x7fff0000
s_branch Lg3o_gox_%=
Lg3o_xn0_%=:
s_cmp_ge_u32 s54,256
s_cbranch_scc1 Lg3o_fin0_%=
s_add_u32 m0,s59,4096
s_nop 0
buffer_load_dword v5,s[36:39],s58 offen offset:0 sc1 lds
buffer_store_dword v4,v3,s[36:39],s71 offen offset:0 sc0 sc1
s_add_u32 m0,s59,5120
s_nop 0
)"
R"(buffer_load_dword v5,s[36:39],s58 offen offset:0 sc1 lds
buffer_store_dword v4,v3,s[36:39],s71 offen offset:0 sc0 sc1
s_mov_b32 s83,0
s_nop 0
v_fma_f32 v18,-v66,v74,v18
v_fma_f32 v19,-v66,v75,v19
v_fma_f32 v20,-v66,v76,v20
v_fma_f32 v21,-v66,v77,v21
v_mul_f32 v82,v18,v18
v_mul_f32 v83,v19,v19
v_mul_f32 v84,v20,v20
v_mul_f32 v85,v21,v21
v_fma_f32 v82,v68,v82,v67
v_fma_f32 v83,v68,v83,v67
v_fma_f32 v84,v68,v84,v67
v_fma_f32 v85,v68,v85,v67
v_mul_f32 v82,v18,v82
v_mul_f32 v83,v19,v83
v_mul_f32 v84,v20,v84
v_mul_f32 v85,v21,v85
v_exp_f32 v82,v82
v_exp_f32 v83,v83
v_exp_f32 v84,v84
v_exp_f32 v85,v85
v_fma_f32 v82,v82,v69,v69
v_fma_f32 v83,v83,v69,v69
v_fma_f32 v84,v84,v69,v69
v_fma_f32 v85,v85,v69,v69
v_rcp_f32 v82,v82
v_rcp_f32 v83,v83
v_rcp_f32 v84,v84
v_rcp_f32 v85,v85
v_mul_f32 v18,v18,v82
v_mul_f32 v19,v19,v83
v_mul_f32 v20,v20,v84
v_mul_f32 v21,v21,v85
v_cvt_pk_bf16_f32 v90,v18,v19
v_cvt_pk_bf16_f32 v91,v20,v21
v_fma_f32 v22,-v66,v78,v22
v_fma_f32 v23,-v66,v79,v23
v_fma_f32 v24,-v66,v80,v24
v_fma_f32 v25,-v66,v81,v25
v_mul_f32 v86,v22,v22
v_mul_f32 v87,v23,v23
v_mul_f32 v88,v24,v24
v_mul_f32 v89,v25,v25
v_fma_f32 v86,v68,v86,v67
v_fma_f32 v87,v68,v87,v67
v_fma_f32 v88,v68,v88,v67
v_fma_f32 v89,v68,v89,v67
v_mul_f32 v86,v22,v86
v_mul_f32 v87,v23,v87
v_mul_f32 v88,v24,v88
v_mul_f32 v89,v25,v89
v_exp_f32 v86,v86
v_exp_f32 v87,v87
v_exp_f32 v88,v88
v_exp_f32 v89,v89
v_fma_f32 v86,v86,v69,v69
v_fma_f32 v87,v87,v69,v69
v_fma_f32 v88,v88,v69,v69
v_fma_f32 v89,v89,v69,v69
v_rcp_f32 v86,v86
v_rcp_f32 v87,v87
v_rcp_f32 v88,v88
v_rcp_f32 v89,v89
v_mul_f32 v22,v22,v86
v_mul_f32 v23,v23,v87
v_mul_f32 v24,v24,v88
v_mul_f32 v25,v25,v89
v_cvt_pk_bf16_f32 v92,v22,v23
v_cvt_pk_bf16_f32 v93,v24,v25
buffer_store_dwordx4 v[90:93],v0,s[36:39],s66 offen offset:0 sc0 sc1
s_mov_b32 s72,s69
s_mov_b32 s69,0x7fff0000
s_mov_b32 s66,0x7fff0000
s_mov_b32 s76,0x7fff0000
s_branch Lg3o_poll_%=
Lg3o_fin0_%=:
s_nop 0
v_fma_f32 v18,-v66,v74,v18
v_fma_f32 v19,-v66,v75,v19
v_fma_f32 v20,-v66,v76,v20
v_fma_f32 v21,-v66,v77,v21
v_mul_f32 v82,v18,v18
v_mul_f32 v83,v19,v19
v_mul_f32 v84,v20,v20
v_mul_f32 v85,v21,v21
v_fma_f32 v82,v68,v82,v67
v_fma_f32 v83,v68,v83,v67
v_fma_f32 v84,v68,v84,v67
v_fma_f32 v85,v68,v85,v67
v_mul_f32 v82,v18,v82
v_mul_f32 v83,v19,v83
v_mul_f32 v84,v20,v84
v_mul_f32 v85,v21,v85
v_exp_f32 v82,v82
v_exp_f32 v83,v83
v_exp_f32 v84,v84
v_exp_f32 v85,v85
v_fma_f32 v82,v82,v69,v69
v_fma_f32 v83,v83,v69,v69
v_fma_f32 v84,v84,v69,v69
v_fma_f32 v85,v85,v69,v69
v_rcp_f32 v82,v82
v_rcp_f32 v83,v83
v_rcp_f32 v84,v84
v_rcp_f32 v85,v85
v_mul_f32 v18,v18,v82
v_mul_f32 v19,v19,v83
v_mul_f32 v20,v20,v84
v_mul_f32 v21,v21,v85
v_cvt_pk_bf16_f32 v90,v18,v19
v_cvt_pk_bf16_f32 v91,v20,v21
v_fma_f32 v22,-v66,v78,v22
v_fma_f32 v23,-v66,v79,v23
v_fma_f32 v24,-v66,v80,v24
v_fma_f32 v25,-v66,v81,v25
v_mul_f32 v86,v22,v22
v_mul_f32 v87,v23,v23
v_mul_f32 v88,v24,v24
v_mul_f32 v89,v25,v25
v_fma_f32 v86,v68,v86,v67
v_fma_f32 v87,v68,v87,v67
v_fma_f32 v88,v68,v88,v67
v_fma_f32 v89,v68,v89,v67
v_mul_f32 v86,v22,v86
v_mul_f32 v87,v23,v87
v_mul_f32 v88,v24,v88
v_mul_f32 v89,v25,v89
v_exp_f32 v86,v86
v_exp_f32 v87,v87
v_exp_f32 v88,v88
v_exp_f32 v89,v89
v_fma_f32 v86,v86,v69,v69
v_fma_f32 v87,v87,v69,v69
v_fma_f32 v88,v88,v69,v69
v_fma_f32 v89,v89,v69,v69
v_rcp_f32 v86,v86
v_rcp_f32 v87,v87
v_rcp_f32 v88,v88
v_rcp_f32 v89,v89
v_mul_f32 v22,v22,v86
v_mul_f32 v23,v23,v87
v_mul_f32 v24,v24,v88
v_mul_f32 v25,v25,v89
v_cvt_pk_bf16_f32 v92,v22,v23
v_cvt_pk_bf16_f32 v93,v24,v25
buffer_store_dwordx4 v[90:93],v0,s[36:39],s66 offen offset:0 sc0 sc1
s_mov_b32 s72,s69
s_mov_b32 s69,0x7fff0000
s_mov_b32 s66,0x7fff0000
s_mov_b32 s76,0x7fff0000
s_branch Lg3o_done_%=
Lg3o_L21_%=:
v_mfma_f32_16x16x32_bf16 v[26:29],a[0:3],v[34:37],0
s_add_u32 s58,s58,s61
s_add_u32 s58,s58,s61
R 62,2,7168
v_mfma_f32_16x16x32_bf16 v[30:33],a[4:7],v[34:37],0
s_add_u32 m0,s59,1024
s_add_u32 s56,s55,4096
s_waitcnt vmcnt(2)
M 26,a[8:11],38
s_nop 0
R 34,2,8192
v_add_f32_dpp v99,v98,v98 quad_perm:[1,0,3,2] row_mask:0xf bank_mask:0xf
M 30,a[12:15],38
L 5
buffer_load_dword v8,s[36:39],s58 offen offset:0 sc1 lds
M 26,a[16:19],42
s_mov_b32 m0,s57
R 38,2,9216
v_add_f32_dpp v99,v99,v99 quad_perm:[2,3,0,1] row_mask:0xf bank_mask:0xf
M 30,a[20:23],42
s_mov_b32 s67,s65
buffer_load_dword v98,v11,s[36:39],s73 offen offset:0 sc1
M 26,a[24:27],46
R 42,2,10240
v_add_f32_dpp v102,v99,v99 row_half_mirror row_mask:0xf bank_mask:0xf
M 30,a[28:31],46
L 5
D 0,55,0
M 26,a[32:35],50
R 46,2,11264
v_mov_b32_dpp v99,v102 row_ror:8 row_mask:0xf bank_mask:0xf
M 30,a[36:39],50
v_fma_f32 v99,-v102,v102,v99
v_max_f32 v99,s87,v99
M 26,a[40:43],54
R 50,2,12288
v_fma_f32 v18,-v66,v74,v18
M 30,a[44:47],54
L 5
D 0,55,1024
M 26,a[48:51],58
R 54,2,13312
v_fma_f32 v19,-v66,v75,v19
M 30,a[52:55],58
v_rsq_f32 v100,v99
M 26,a[56:59],62
R 58,2,14336
v_mul_f32 v105,v99,v100
M 30,a[60:63],62
L 5
D 0,55,2048
M 26,a[64:67],34
R 62,2,15360
v_mul_f32 v103,s88,v100
M 30,a[68:71],34
v_mul_f32 v99,s89,v100
v_mul_f32 v104,v103,v100
M 26,a[72:75],38
R 34,2,16384
v_mul_f32 v104,v104,v99
M 30,a[76:79],38
L 5
ds_write_b128 v12,v[102:105] offset:2048
M 26,a[80:83],42
R 38,2,17408
v_fma_f32 v20,-v66,v76,v20
M 30,a[84:87],42
D 0,55,3072
M 26,a[88:91],46
s_add_u32 m0,s57,4096
R 42,2,18432
v_fma_f32 v21,-v66,v77,v21
M 30,a[92:95],46
L 6
D 0,56,0
M 26,a[96:99],50
s_add_u32 s55,s55,s60
R 46,2,19456
v_mul_f32 v82,v18,v18
M 30,a[100:103],50
s_add_u32 s57,s57,32768
v_mul_f32 v83,v19,v19
v_mul_f32 v84,v20,v20
M 26,a[104:107],54
s_and_b32 s57,s57,131071
R 50,2,20480
v_mul_f32 v85,v21,v21
M 30,a[108:111],54
L 6
D 0,56,1024
M 26,a[112:115],58
s_add_u32 s54,s54,s46
R 54,2,21504
v_fma_f32 v82,v68,v82,v67
M 30,a[116:119],58
v_fma_f32 v83,v68,v83,v67
v_fma_f32 v84,v68,v84,v67
M 26,a[120:123],62
R 58,2,22528
v_fma_f32 v85,v68,v85,v67
M 30,a[124:127],62
L 5
D 0,56,2048
M 26,a[128:131],34
R 62,2,23552
v_mul_f32 v82,v18,v82
M 30,a[132:135],34
v_exp_f32 v82,v82
M 26,a[136:139],38
R 34,2,24576
v_mul_f32 v83,v19,v83
M 30,a[140:143],38
L 5
D 0,56,3072
M 26,a[144:147],42
s_mov_b32 m0,s57
R 38,2,25600
v_mul_f32 v84,v20,v84
M 30,a[148:151],42
s_add_u32 s56,s55,4096
v_exp_f32 v83,v83
M 26,a[152:155],46
R 42,2,26624
v_mul_f32 v85,v21,v85
M 30,a[156:159],46
L 5
D 0,55,0
M 26,a[160:163],50
R 46,2,27648
v_fma_f32 v82,v82,v69,v69
M 30,a[164:167],50
D 0,55,1024
M 26,a[168:171],54
R 50,2,28672
v_fma_f32 v83,v83,v69,v69
M 30,a[172:175],54
L 5
v_exp_f32 v84,v84
M 26,a[176:179],58
R 54,2,29696
v_fma_f32 v84,v84,v69,v69
M 30,a[180:183],58
D 0,55,2048
M 26,a[184:187],62
R 58,2,30720
v_fma_f32 v22,-v66,v78,v22
M 30,a[188:191],62
L 5
s_nop 0
D 0,55,3072
M 26,a[192:195],34
s_waitcnt vmcnt(4)
R 62,2,31744
v_fma_f32 v23,-v66,v79,v23
M 30,a[196:199],34
s_barrier
R 70,13,2048
ds_read_b32 v7,v6 offset:1024
M 26,a[200:203],38
s_add_u32 m0,s57,4096
R 34,1,0
v_fma_f32 v24,-v66,v80,v24
M 30,a[204:207],38
L 7
buffer_store_dword v4,v3,s[36:39],s70 offen offset:0 sc0 sc1
M 26,a[208:211],42
s_add_u32 s55,s55,s60
R 38,1,1024
v_fma_f32 v25,-v66,v81,v25
M 30,a[212:215],42
s_add_u32 s57,s57,32768
D 0,56,0
M 26,a[216:219],46
s_and_b32 s57,s57,131071
R 42,1,2048
v_mul_f32 v86,v22,v22
M 30,a[220:223],46
L 7
D 0,56,1024
M 26,a[224:227],50
s_add_u32 s54,s54,s46
R 46,1,3072
v_mul_f32 v87,v23,v23
M 30,a[228:231],50
s_add_u32 s73,s73,s64
D 0,56,2048
M 26,a[232:235],54
s_mov_b32 s70,s69
R 50,1,4096
v_mul_f32 v88,v24,v24
M 30,a[236:239],54
L 7
D 0,56,3072
M 26,a[240:243],58
s_add_u32 s65,s65,s62
R 54,1,5120
v_exp_f32 v85,v85
v_mul_f32 v89,v25,v25
v_fma_f32 v86,v68,v86,v67
v_fma_f32 v87,v68,v87,v67
v_fma_f32 v88,v68,v88,v67
v_fma_f32 v89,v68,v89,v67
v_mul_f32 v86,v22,v86
v_mul_f32 v87,v23,v87
v_mul_f32 v88,v24,v88
v_mul_f32 v89,v25,v89
v_exp_f32 v86,v86
v_exp_f32 v87,v87
v_exp_f32 v88,v88
v_exp_f32 v89,v89
M 30,a[244:247],58
s_mov_b32 s69,s68
L 6
v_fma_f32 v85,v85,v69,v69
v_rcp_f32 v82,v82
v_rcp_f32 v83,v83
v_rcp_f32 v84,v84
v_rcp_f32 v85,v85
v_fma_f32 v86,v86,v69,v69
v_fma_f32 v87,v87,v69,v69
v_fma_f32 v88,v88,v69,v69
v_fma_f32 v89,v89,v69,v69
v_rcp_f32 v86,v86
v_rcp_f32 v87,v87
v_rcp_f32 v88,v88
v_rcp_f32 v89,v89
v_cmp_eq_u32 vcc,s53,v7
M 26,a[248:251],62
s_add_u32 s68,s68,s63
s_cmp_eq_u32 vcc_lo,-1
R 58,1,6144
v_mul_f32 v18,v18,v82
v_mul_f32 v19,v19,v83
v_mul_f32 v20,v20,v84
v_mul_f32 v21,v21,v85
v_cvt_pk_bf16_f32 v90,v18,v19
v_cvt_pk_bf16_f32 v91,v20,v21
v_mul_f32 v22,v22,v86
v_mul_f32 v23,v23,v87
v_mul_f32 v24,v24,v88
v_mul_f32 v25,v25,v89
v_cvt_pk_bf16_f32 v92,v22,v23
v_cvt_pk_bf16_f32 v93,v24,v25
M 30,a[252:255],62
L 5
buffer_store_dwordx4 v[90:93],v0,s[36:39],s66 offen offset:0 sc0 sc1
v_xor_b32 v2,65536,v2
s_cbranch_scc1 Lg3o_S0_%=
Lg3o_N0_%=:
v_mfma_f32_16x16x32_bf16 v[18:21],a[0:3],v[34:37],0
s_add_u32 m0,s59,0
s_waitcnt vmcnt(10)
R 62,1,7168
v_mfma_f32_16x16x32_bf16 v[22:25],a[4:7],v[34:37],0
s_nop 0
buffer_load_dword v5,s[36:39],s58 offen offset:0 sc1 lds
M 18,a[8:11],38
s_mov_b32 s66,s65
R 34,1,8192
v_add_f32_dpp v99,v98,v98 quad_perm:[1,0,3,2] row_mask:0xf bank_mask:0xf
M 22,a[12:15],38
L 5
buffer_load_dword v98,v11,s[36:39],s73 offen offset:0 sc1
M 18,a[16:19],42
s_add_u32 s73,s73,s64
R 38,1,9216
v_add_f32_dpp v99,v99,v99 quad_perm:[2,3,0,1] row_mask:0xf bank_mask:0xf
M 22,a[20:23],42
s_add_u32 s65,s65,s62
v_fma_f32 v26,-v70,v74,v26
v_fma_f32 v27,-v70,v75,v27
M 18,a[24:27],46
R 42,1,10240
v_add_f32_dpp v102,v99,v99 row_half_mirror row_mask:0xf bank_mask:0xf
M 22,a[28:31],46
L 5
v_fma_f32 v28,-v70,v76,v28
v_fma_f32 v29,-v70,v77,v29
M 18,a[32:35],50
R 46,1,11264
v_mov_b32_dpp v99,v102 row_ror:8 row_mask:0xf bank_mask:0xf
M 22,a[36:39],50
v_fma_f32 v99,-v102,v102,v99
v_max_f32 v99,s87,v99
M 18,a[40:43],54
R 50,1,12288
v_mul_f32 v82,v26,v26
M 22,a[44:47],54
L 5
v_rsq_f32 v100,v99
M 18,a[48:51],58
R 54,1,13312
v_mul_f32 v105,v99,v100
M 22,a[52:55],58
v_mul_f32 v103,s88,v100
v_mul_f32 v99,s89,v100
M 18,a[56:59],62
R 58,1,14336
v_mul_f32 v104,v103,v100
M 22,a[60:63],62
L 5
v_mul_f32 v104,v104,v99
v_mul_f32 v83,v27,v27
M 18,a[64:67],34
R 62,1,15360
v_mul_f32 v84,v28,v28
M 22,a[68:71],34
ds_write_b128 v12,v[102:105] offset:0
M 18,a[72:75],38
R 34,1,16384
v_mul_f32 v85,v29,v29
M 22,a[76:79],38
L 6
v_fma_f32 v82,v72,v82,v71
v_fma_f32 v83,v72,v83,v71
M 18,a[80:83],42
R 38,1,17408
v_fma_f32 v84,v72,v84,v71
M 22,a[84:87],42
v_fma_f32 v85,v72,v85,v71
v_mul_f32 v82,v26,v82
M 18,a[88:91],46
R 42,1,18432
v_mul_f32 v83,v27,v83
M 22,a[92:95],46
L 6
v_exp_f32 v82,v82
M 18,a[96:99],50
R 46,1,19456
v_mul_f32 v84,v28,v84
M 22,a[100:103],50
v_exp_f32 v83,v83
M 18,a[104:107],54
R 50,1,20480
v_mul_f32 v85,v29,v85
M 22,a[108:111],54
L 6
v_exp_f32 v84,v84
M 18,a[112:115],58
R 54,1,21504
v_fma_f32 v82,v82,v73,v73
M 22,a[116:119],58
v_exp_f32 v85,v85
M 18,a[120:123],62
R 58,1,22528
)"
R"(v_fma_f32 v83,v83,v73,v73
M 22,a[124:127],62
L 5
v_rcp_f32 v82,v82
M 18,a[128:131],34
R 62,1,23552
v_fma_f32 v84,v84,v73,v73
M 22,a[132:135],34
v_rcp_f32 v83,v83
M 18,a[136:139],38
R 34,1,24576
v_fma_f32 v85,v85,v73,v73
M 22,a[140:143],38
L 5
v_rcp_f32 v84,v84
M 18,a[144:147],42
R 38,1,25600
v_mul_f32 v26,v26,v82
M 22,a[148:151],42
v_rcp_f32 v85,v85
M 18,a[152:155],46
R 42,1,26624
v_mul_f32 v27,v27,v83
M 22,a[156:159],46
L 5
v_mul_f32 v28,v28,v84
v_mul_f32 v29,v29,v85
M 18,a[160:163],50
R 46,1,27648
v_cvt_pk_bf16_f32 v90,v26,v27
M 22,a[164:167],50
v_cvt_pk_bf16_f32 v91,v28,v29
v_fma_f32 v30,-v70,v78,v30
M 18,a[168:171],54
R 50,1,28672
v_fma_f32 v31,-v70,v79,v31
M 22,a[172:175],54
L 5
v_fma_f32 v32,-v70,v80,v32
v_fma_f32 v33,-v70,v81,v33
M 18,a[176:179],58
R 54,1,29696
v_mul_f32 v86,v30,v30
M 22,a[180:183],58
v_mul_f32 v87,v31,v31
v_mul_f32 v88,v32,v32
M 18,a[184:187],62
R 58,1,30720
v_mul_f32 v89,v33,v33
M 22,a[188:191],62
L 5
s_nop 0
v_fma_f32 v86,v72,v86,v71
M 18,a[192:195],34
s_waitcnt vmcnt(1)
R 62,1,31744
v_fma_f32 v87,v72,v87,v71
M 22,a[196:199],34
s_barrier
R 66,13,0
ds_read_b32 v7,v6 offset:0
M 18,a[200:203],38
R 34,2,0
v_fma_f32 v88,v72,v88,v71
M 22,a[204:207],38
L 7
buffer_store_dword v4,v3,s[36:39],s70 offen offset:0 sc0 sc1
M 18,a[208:211],42
s_mov_b32 s70,s69
R 38,2,1024
v_fma_f32 v89,v72,v89,v71
M 22,a[212:215],42
s_mov_b32 s69,s68
v_mul_f32 v86,v30,v86
v_mul_f32 v87,v31,v87
M 18,a[216:219],46
L 2
R 42,2,2048
v_mul_f32 v88,v32,v88
M 22,a[220:223],46
s_nop 0
v_exp_f32 v86,v86
M 18,a[224:227],50
s_add_u32 s68,s68,s63
R 46,2,3072
v_mul_f32 v89,v33,v89
M 22,a[228:231],50
v_exp_f32 v87,v87
M 18,a[232:235],54
R 50,2,4096
v_fma_f32 v86,v86,v73,v73
M 22,a[236:239],54
s_nop 0
v_exp_f32 v88,v88
M 18,a[240:243],58
R 54,2,5120
v_exp_f32 v89,v89
M 22,a[244:247],58
v_fma_f32 v87,v87,v73,v73
v_fma_f32 v88,v88,v73,v73
v_fma_f32 v89,v89,v73,v73
v_rcp_f32 v86,v86
v_rcp_f32 v87,v87
v_rcp_f32 v88,v88
v_rcp_f32 v89,v89
v_cmp_eq_u32 vcc,s53,v7
M 18,a[248:251],62
s_cmp_eq_u64 vcc,-1
R 58,2,6144
v_mul_f32 v30,v30,v86
v_mul_f32 v31,v31,v87
v_mul_f32 v32,v32,v88
v_mul_f32 v33,v33,v89
v_cvt_pk_bf16_f32 v92,v30,v31
v_cvt_pk_bf16_f32 v93,v32,v33
M 22,a[252:255],62
L 5
buffer_store_dwordx4 v[90:93],v0,s[36:39],s67 offen offset:0 sc0 sc1
v_xor_b32 v1,65536,v1
s_cbranch_scc1 Lg3o_L21_%=
s_cmp_eq_u32 vcc_lo,-1
s_cbranch_scc1 Lg3o_L11_%=
Lg3o_X1_%=:
v_mfma_f32_16x16x32_bf16 v[26:29],a[0:3],v[34:37],0
s_add_u32 m0,s59,1024
s_waitcnt vmcnt(2)
R 62,2,7168
v_mfma_f32_16x16x32_bf16 v[30:33],a[4:7],v[34:37],0
s_nop 0
buffer_load_dword v5,s[36:39],s58 offen offset:0 sc1 lds
M 26,a[8:11],38
s_mov_b32 s67,s65
R 34,2,8192
v_add_f32_dpp v99,v98,v98 quad_perm:[1,0,3,2] row_mask:0xf bank_mask:0xf
M 30,a[12:15],38
L 5
v_fma_f32 v18,-v66,v74,v18
v_fma_f32 v19,-v66,v75,v19
M 26,a[16:19],42
s_add_u32 s65,s65,s62
R 38,2,9216
v_add_f32_dpp v99,v99,v99 quad_perm:[2,3,0,1] row_mask:0xf bank_mask:0xf
M 30,a[20:23],42
v_fma_f32 v20,-v66,v76,v20
v_fma_f32 v21,-v66,v77,v21
M 26,a[24:27],46
R 42,2,10240
v_add_f32_dpp v102,v99,v99 row_half_mirror row_mask:0xf bank_mask:0xf
M 30,a[28:31],46
L 5
v_mul_f32 v82,v18,v18
v_mul_f32 v83,v19,v19
M 26,a[32:35],50
R 46,2,11264
v_mov_b32_dpp v99,v102 row_ror:8 row_mask:0xf bank_mask:0xf
M 30,a[36:39],50
v_fma_f32 v99,-v102,v102,v99
v_max_f32 v99,s87,v99
M 26,a[40:43],54
R 50,2,12288
v_mul_f32 v84,v20,v20
M 30,a[44:47],54
L 5
v_rsq_f32 v100,v99
M 26,a[48:51],58
R 54,2,13312
v_mul_f32 v105,v99,v100
M 30,a[52:55],58
v_mul_f32 v103,s88,v100
v_mul_f32 v99,s89,v100
M 26,a[56:59],62
R 58,2,14336
v_mul_f32 v104,v103,v100
M 30,a[60:63],62
L 5
v_mul_f32 v104,v104,v99
v_mul_f32 v85,v21,v21
M 26,a[64:67],34
R 62,2,15360
v_fma_f32 v82,v68,v82,v67
M 30,a[68:71],34
ds_write_b128 v12,v[102:105] offset:2048
M 26,a[72:75],38
R 34,2,16384
v_fma_f32 v83,v68,v83,v67
M 30,a[76:79],38
L 6
v_fma_f32 v84,v68,v84,v67
v_fma_f32 v85,v68,v85,v67
M 26,a[80:83],42
R 38,2,17408
v_mul_f32 v82,v18,v82
M 30,a[84:87],42
v_exp_f32 v82,v82
M 26,a[88:91],46
R 42,2,18432
v_mul_f32 v83,v19,v83
M 30,a[92:95],46
L 6
v_exp_f32 v83,v83
M 26,a[96:99],50
R 46,2,19456
v_mul_f32 v84,v20,v84
M 30,a[100:103],50
v_exp_f32 v84,v84
M 26,a[104:107],54
R 50,2,20480
v_mul_f32 v85,v21,v85
M 30,a[108:111],54
L 6
v_exp_f32 v85,v85
M 26,a[112:115],58
R 54,2,21504
v_fma_f32 v82,v82,v69,v69
M 30,a[116:119],58
v_rcp_f32 v82,v82
M 26,a[120:123],62
R 58,2,22528
v_fma_f32 v83,v83,v69,v69
M 30,a[124:127],62
L 5
v_rcp_f32 v83,v83
M 26,a[128:131],34
R 62,2,23552
v_fma_f32 v84,v84,v69,v69
M 30,a[132:135],34
v_rcp_f32 v84,v84
M 26,a[136:139],38
R 34,2,24576
v_fma_f32 v85,v85,v69,v69
M 30,a[140:143],38
L 5
v_rcp_f32 v85,v85
M 26,a[144:147],42
R 38,2,25600
v_mul_f32 v18,v18,v82
M 30,a[148:151],42
v_mul_f32 v19,v19,v83
v_mul_f32 v20,v20,v84
M 26,a[152:155],46
R 42,2,26624
v_mul_f32 v21,v21,v85
M 30,a[156:159],46
L 5
v_cvt_pk_bf16_f32 v90,v18,v19
v_cvt_pk_bf16_f32 v91,v20,v21
M 26,a[160:163],50
R 46,2,27648
v_fma_f32 v22,-v66,v78,v22
M 30,a[164:167],50
v_fma_f32 v23,-v66,v79,v23
v_fma_f32 v24,-v66,v80,v24
M 26,a[168:171],54
R 50,2,28672
v_fma_f32 v25,-v66,v81,v25
M 30,a[172:175],54
L 5
v_mul_f32 v86,v22,v22
v_mul_f32 v87,v23,v23
M 26,a[176:179],58
R 54,2,29696
v_mul_f32 v88,v24,v24
M 30,a[180:183],58
v_mul_f32 v89,v25,v25
v_fma_f32 v86,v68,v86,v67
M 26,a[184:187],62
R 58,2,30720
v_fma_f32 v87,v68,v87,v67
M 30,a[188:191],62
L 5
s_nop 0
v_fma_f32 v88,v68,v88,v67
M 26,a[192:195],34
s_waitcnt vmcnt(0)
R 62,2,31744
v_fma_f32 v89,v68,v89,v67
M 30,a[196:199],34
s_barrier
R 70,13,2048
ds_read_b32 v7,v6 offset:1024
M 26,a[200:203],38
buffer_store_dword v4,v3,s[36:39],s70 offen offset:0 sc0 sc1
M 30,a[204:207],38
L 6
v_mul_f32 v86,v22,v86
v_mul_f32 v87,v23,v87
M 26,a[208:211],42
s_mov_b32 s70,s69
v_exp_f32 v86,v86
M 30,a[212:215],42
s_mov_b32 s69,s68
v_exp_f32 v87,v87
M 26,a[216:219],46
L 0
v_mul_f32 v88,v24,v88
v_mul_f32 v89,v25,v89
M 30,a[220:223],46
s_nop 0
v_exp_f32 v88,v88
M 26,a[224:227],50
s_add_u32 s68,s68,s63
v_exp_f32 v89,v89
M 30,a[228:231],50
v_fma_f32 v86,v86,v69,v69
v_fma_f32 v87,v87,v69,v69
M 26,a[232:235],54
v_fma_f32 v88,v88,v69,v69
v_fma_f32 v89,v89,v69,v69
M 30,a[236:239],54
s_nop 0
v_rcp_f32 v86,v86
M 26,a[240:243],58
v_rcp_f32 v87,v87
M 30,a[244:247],58
v_rcp_f32 v88,v88
v_rcp_f32 v89,v89
M 26,a[248:251],62
v_mul_f32 v22,v22,v86
v_mul_f32 v23,v23,v87
v_mul_f32 v24,v24,v88
v_mul_f32 v25,v25,v89
v_cvt_pk_bf16_f32 v92,v22,v23
v_cvt_pk_bf16_f32 v93,v24,v25
M 30,a[252:255],62
buffer_store_dwordx4 v[90:93],v0,s[36:39],s66 offen offset:0 sc0 sc1
v_xor_b32 v2,65536,v2
v_cmp_eq_u32 vcc,s53,v7
s_mov_b32 s80,0
s_cmp_eq_u32 vcc_lo,-1
s_cbranch_scc0 Lg3o_xn1_%=
buffer_load_dword v98,v11,s[36:39],s73 offen offset:0 sc1
s_nop 0
v_fma_f32 v26,-v70,v74,v26
v_fma_f32 v27,-v70,v75,v27
v_fma_f32 v28,-v70,v76,v28
v_fma_f32 v29,-v70,v77,v29
v_mul_f32 v82,v26,v26
v_mul_f32 v83,v27,v27
s_add_u32 s73,s73,s64
s_mov_b32 m0,s57
s_add_u32 s56,s55,4096
D 0,55,0
v_mul_f32 v84,v28,v28
v_mul_f32 v85,v29,v29
v_fma_f32 v82,v72,v82,v71
v_fma_f32 v83,v72,v83,v71
v_fma_f32 v84,v72,v84,v71
v_fma_f32 v85,v72,v85,v71
D 0,55,1024
v_mul_f32 v82,v26,v82
v_mul_f32 v83,v27,v83
v_mul_f32 v84,v28,v84
v_mul_f32 v85,v29,v85
v_exp_f32 v82,v82
v_exp_f32 v83,v83
D 0,55,2048
v_exp_f32 v84,v84
v_exp_f32 v85,v85
v_fma_f32 v82,v82,v73,v73
v_fma_f32 v83,v83,v73,v73
v_fma_f32 v84,v84,v73,v73
v_fma_f32 v85,v85,v73,v73
D 0,55,3072
v_rcp_f32 v82,v82
v_rcp_f32 v83,v83
v_rcp_f32 v84,v84
v_rcp_f32 v85,v85
v_mul_f32 v26,v26,v82
v_mul_f32 v27,v27,v83
s_add_u32 m0,s57,4096
s_nop 0
D 0,56,0
v_mul_f32 v28,v28,v84
v_mul_f32 v29,v29,v85
v_cvt_pk_bf16_f32 v90,v26,v27
v_cvt_pk_bf16_f32 v91,v28,v29
v_fma_f32 v30,-v70,v78,v30
v_fma_f32 v31,-v70,v79,v31
D 0,56,1024
v_fma_f32 v32,-v70,v80,v32
v_fma_f32 v33,-v70,v81,v33
v_mul_f32 v86,v30,v30
v_mul_f32 v87,v31,v31
v_mul_f32 v88,v32,v32
v_mul_f32 v89,v33,v33
D 0,56,2048
v_fma_f32 v86,v72,v86,v71
v_fma_f32 v87,v72,v87,v71
v_fma_f32 v88,v72,v88,v71
v_fma_f32 v89,v72,v89,v71
v_mul_f32 v86,v30,v86
v_mul_f32 v87,v31,v87
D 0,56,3072
v_mul_f32 v88,v32,v88
v_mul_f32 v89,v33,v89
v_exp_f32 v86,v86
v_exp_f32 v87,v87
v_exp_f32 v88,v88
v_exp_f32 v89,v89
s_add_u32 s55,s55,s60
s_add_u32 s57,s57,32768
s_and_b32 s57,s57,131071
s_add_u32 s54,s54,s46
v_fma_f32 v86,v86,v73,v73
v_fma_f32 v87,v87,v73,v73
v_fma_f32 v88,v88,v73,v73
v_fma_f32 v89,v89,v73,v73
v_rcp_f32 v86,v86
v_rcp_f32 v87,v87
v_rcp_f32 v88,v88
v_rcp_f32 v89,v89
v_mul_f32 v30,v30,v86
v_mul_f32 v31,v31,v87
v_mul_f32 v32,v32,v88
v_mul_f32 v33,v33,v89
v_cvt_pk_bf16_f32 v92,v30,v31
v_cvt_pk_bf16_f32 v93,v32,v33
buffer_store_dwordx4 v[90:93],v0,s[36:39],s67 offen offset:0 sc0 sc1
s_mov_b32 s72,s69
s_mov_b32 s69,0x7fff0000
s_mov_b32 s67,0x7fff0000
s_mov_b32 s77,0x7fff0000
s_branch Lg3o_gox_%=
Lg3o_xn1_%=:
s_cmp_ge_u32 s54,256
s_cbranch_scc1 Lg3o_fin1_%=
s_add_u32 m0,s59,4096
s_nop 0
buffer_load_dword v5,s[36:39],s58 offen offset:0 sc1 lds
buffer_store_dword v4,v3,s[36:39],s71 offen offset:0 sc0 sc1
s_add_u32 m0,s59,5120
s_nop 0
buffer_load_dword v5,s[36:39],s58 offen offset:0 sc1 lds
buffer_store_dword v4,v3,s[36:39],s71 offen offset:0 sc0 sc1
s_mov_b32 s83,0
s_nop 0
v_fma_f32 v26,-v70,v74,v26
v_fma_f32 v27,-v70,v75,v27
v_fma_f32 v28,-v70,v76,v28
v_fma_f32 v29,-v70,v77,v29
v_mul_f32 v82,v26,v26
v_mul_f32 v83,v27,v27
v_mul_f32 v84,v28,v28
v_mul_f32 v85,v29,v29
v_fma_f32 v82,v72,v82,v71
v_fma_f32 v83,v72,v83,v71
v_fma_f32 v84,v72,v84,v71
v_fma_f32 v85,v72,v85,v71
v_mul_f32 v82,v26,v82
v_mul_f32 v83,v27,v83
v_mul_f32 v84,v28,v84
v_mul_f32 v85,v29,v85
v_exp_f32 v82,v82
v_exp_f32 v83,v83
v_exp_f32 v84,v84
v_exp_f32 v85,v85
v_fma_f32 v82,v82,v73,v73
v_fma_f32 v83,v83,v73,v73
v_fma_f32 v84,v84,v73,v73
v_fma_f32 v85,v85,v73,v73
v_rcp_f32 v82,v82
v_rcp_f32 v83,v83
v_rcp_f32 v84,v84
v_rcp_f32 v85,v85
v_mul_f32 v26,v26,v82
v_mul_f32 v27,v27,v83
v_mul_f32 v28,v28,v84
v_mul_f32 v29,v29,v85
v_cvt_pk_bf16_f32 v90,v26,v27
v_cvt_pk_bf16_f32 v91,v28,v29
v_fma_f32 v30,-v70,v78,v30
v_fma_f32 v31,-v70,v79,v31
v_fma_f32 v32,-v70,v80,v32
v_fma_f32 v33,-v70,v81,v33
v_mul_f32 v86,v30,v30
v_mul_f32 v87,v31,v31
v_mul_f32 v88,v32,v32
v_mul_f32 v89,v33,v33
v_fma_f32 v86,v72,v86,v71
v_fma_f32 v87,v72,v87,v71
v_fma_f32 v88,v72,v88,v71
v_fma_f32 v89,v72,v89,v71
v_mul_f32 v86,v30,v86
v_mul_f32 v87,v31,v87
v_mul_f32 v88,v32,v88
v_mul_f32 v89,v33,v89
v_exp_f32 v86,v86
v_exp_f32 v87,v87
v_exp_f32 v88,v88
v_exp_f32 v89,v89
v_fma_f32 v86,v86,v73,v73
v_fma_f32 v87,v87,v73,v73
v_fma_f32 v88,v88,v73,v73
v_fma_f32 v89,v89,v73,v73
v_rcp_f32 v86,v86
v_rcp_f32 v87,v87
v_rcp_f32 v88,v88
v_rcp_f32 v89,v89
v_mul_f32 v30,v30,v86
v_mul_f32 v31,v31,v87
)"
R"(v_mul_f32 v32,v32,v88
v_mul_f32 v33,v33,v89
v_cvt_pk_bf16_f32 v92,v30,v31
v_cvt_pk_bf16_f32 v93,v32,v33
buffer_store_dwordx4 v[90:93],v0,s[36:39],s67 offen offset:0 sc0 sc1
s_mov_b32 s72,s69
s_mov_b32 s69,0x7fff0000
s_mov_b32 s67,0x7fff0000
s_mov_b32 s77,0x7fff0000
s_branch Lg3o_poll_%=
Lg3o_fin1_%=:
s_nop 0
v_fma_f32 v26,-v70,v74,v26
v_fma_f32 v27,-v70,v75,v27
v_fma_f32 v28,-v70,v76,v28
v_fma_f32 v29,-v70,v77,v29
v_mul_f32 v82,v26,v26
v_mul_f32 v83,v27,v27
v_mul_f32 v84,v28,v28
v_mul_f32 v85,v29,v29
v_fma_f32 v82,v72,v82,v71
v_fma_f32 v83,v72,v83,v71
v_fma_f32 v84,v72,v84,v71
v_fma_f32 v85,v72,v85,v71
v_mul_f32 v82,v26,v82
v_mul_f32 v83,v27,v83
v_mul_f32 v84,v28,v84
v_mul_f32 v85,v29,v85
v_exp_f32 v82,v82
v_exp_f32 v83,v83
v_exp_f32 v84,v84
v_exp_f32 v85,v85
v_fma_f32 v82,v82,v73,v73
v_fma_f32 v83,v83,v73,v73
v_fma_f32 v84,v84,v73,v73
v_fma_f32 v85,v85,v73,v73
v_rcp_f32 v82,v82
v_rcp_f32 v83,v83
v_rcp_f32 v84,v84
v_rcp_f32 v85,v85
v_mul_f32 v26,v26,v82
v_mul_f32 v27,v27,v83
v_mul_f32 v28,v28,v84
v_mul_f32 v29,v29,v85
v_cvt_pk_bf16_f32 v90,v26,v27
v_cvt_pk_bf16_f32 v91,v28,v29
v_fma_f32 v30,-v70,v78,v30
v_fma_f32 v31,-v70,v79,v31
v_fma_f32 v32,-v70,v80,v32
v_fma_f32 v33,-v70,v81,v33
v_mul_f32 v86,v30,v30
v_mul_f32 v87,v31,v31
v_mul_f32 v88,v32,v32
v_mul_f32 v89,v33,v33
v_fma_f32 v86,v72,v86,v71
v_fma_f32 v87,v72,v87,v71
v_fma_f32 v88,v72,v88,v71
v_fma_f32 v89,v72,v89,v71
v_mul_f32 v86,v30,v86
v_mul_f32 v87,v31,v87
v_mul_f32 v88,v32,v88
v_mul_f32 v89,v33,v89
v_exp_f32 v86,v86
v_exp_f32 v87,v87
v_exp_f32 v88,v88
v_exp_f32 v89,v89
v_fma_f32 v86,v86,v73,v73
v_fma_f32 v87,v87,v73,v73
v_fma_f32 v88,v88,v73,v73
v_fma_f32 v89,v89,v73,v73
v_rcp_f32 v86,v86
v_rcp_f32 v87,v87
v_rcp_f32 v88,v88
v_rcp_f32 v89,v89
v_mul_f32 v30,v30,v86
v_mul_f32 v31,v31,v87
v_mul_f32 v32,v32,v88
v_mul_f32 v33,v33,v89
v_cvt_pk_bf16_f32 v92,v30,v31
v_cvt_pk_bf16_f32 v93,v32,v33
buffer_store_dwordx4 v[90:93],v0,s[36:39],s67 offen offset:0 sc0 sc1
s_mov_b32 s72,s69
s_mov_b32 s69,0x7fff0000
s_mov_b32 s67,0x7fff0000
s_mov_b32 s77,0x7fff0000
s_branch Lg3o_done_%=
Lg3o_L10_%=:
v_mfma_f32_16x16x32_bf16 v[18:21],a[0:3],v[34:37],0
s_add_u32 s58,s58,s61
s_add_u32 m0,s59,0
R 62,1,7168
v_mfma_f32_16x16x32_bf16 v[22:25],a[4:7],v[34:37],0
s_waitcnt vmcnt(2)
buffer_load_dword v5,s[36:39],s58 offen offset:0 sc1 lds
M 18,a[8:11],38
s_mov_b32 m0,s57
R 34,1,8192
v_add_f32_dpp v99,v98,v98 quad_perm:[1,0,3,2] row_mask:0xf bank_mask:0xf
M 22,a[12:15],38
L 5
buffer_load_dword v98,v11,s[36:39],s73 offen offset:0 sc1
M 18,a[16:19],42
s_add_u32 s56,s55,4096
R 38,1,9216
v_add_f32_dpp v99,v99,v99 quad_perm:[2,3,0,1] row_mask:0xf bank_mask:0xf
M 22,a[20:23],42
s_nop 0
v_fma_f32 v26,-v70,v74,v26
v_fma_f32 v27,-v70,v75,v27
M 18,a[24:27],46
s_mov_b32 s66,s65
R 42,1,10240
v_add_f32_dpp v102,v99,v99 row_half_mirror row_mask:0xf bank_mask:0xf
M 22,a[28:31],46
L 5
D 0,55,0
M 18,a[32:35],50
R 46,1,11264
v_mov_b32_dpp v99,v102 row_ror:8 row_mask:0xf bank_mask:0xf
M 22,a[36:39],50
v_fma_f32 v99,-v102,v102,v99
v_max_f32 v99,s87,v99
M 18,a[40:43],54
R 50,1,12288
v_fma_f32 v28,-v70,v76,v28
M 22,a[44:47],54
L 5
D 0,55,1024
M 18,a[48:51],58
R 54,1,13312
v_fma_f32 v29,-v70,v77,v29
M 22,a[52:55],58
v_rsq_f32 v100,v99
M 18,a[56:59],62
R 58,1,14336
v_mul_f32 v105,v99,v100
M 22,a[60:63],62
L 5
D 0,55,2048
M 18,a[64:67],34
R 62,1,15360
v_mul_f32 v103,s88,v100
M 22,a[68:71],34
v_mul_f32 v99,s89,v100
v_mul_f32 v104,v103,v100
M 18,a[72:75],38
R 34,1,16384
v_mul_f32 v104,v104,v99
M 22,a[76:79],38
L 5
ds_write_b128 v12,v[102:105] offset:0
M 18,a[80:83],42
R 38,1,17408
v_mul_f32 v82,v26,v26
M 22,a[84:87],42
D 0,55,3072
M 18,a[88:91],46
s_add_u32 m0,s57,4096
R 42,1,18432
v_mul_f32 v83,v27,v27
M 22,a[92:95],46
L 6
D 0,56,0
M 18,a[96:99],50
s_add_u32 s55,s55,s60
R 46,1,19456
v_mul_f32 v84,v28,v28
M 22,a[100:103],50
s_add_u32 s57,s57,32768
v_mul_f32 v85,v29,v29
v_fma_f32 v82,v72,v82,v71
M 18,a[104:107],54
s_and_b32 s57,s57,131071
R 50,1,20480
v_fma_f32 v83,v72,v83,v71
M 22,a[108:111],54
L 6
D 0,56,1024
M 18,a[112:115],58
s_add_u32 s54,s54,s46
R 54,1,21504
v_fma_f32 v84,v72,v84,v71
M 22,a[116:119],58
s_add_u32 s73,s73,s64
v_fma_f32 v85,v72,v85,v71
v_mul_f32 v82,v26,v82
M 18,a[120:123],62
s_add_u32 s65,s65,s62
R 58,1,22528
v_mul_f32 v83,v27,v83
M 22,a[124:127],62
L 5
D 0,56,2048
M 18,a[128:131],34
R 62,1,23552
v_mul_f32 v84,v28,v84
M 22,a[132:135],34
v_exp_f32 v82,v82
M 18,a[136:139],38
R 34,1,24576
v_mul_f32 v85,v29,v85
M 22,a[140:143],38
L 5
D 0,56,3072
M 18,a[144:147],42
R 38,1,25600
v_fma_f32 v82,v82,v73,v73
M 22,a[148:151],42
v_exp_f32 v83,v83
M 18,a[152:155],46
R 42,1,26624
v_fma_f32 v83,v83,v73,v73
M 22,a[156:159],46
L 5
v_exp_f32 v84,v84
M 18,a[160:163],50
R 46,1,27648
v_fma_f32 v84,v84,v73,v73
M 22,a[164:167],50
v_exp_f32 v85,v85
M 18,a[168:171],54
R 50,1,28672
v_fma_f32 v85,v85,v73,v73
M 22,a[172:175],54
L 5
v_rcp_f32 v82,v82
M 18,a[176:179],58
R 54,1,29696
v_mul_f32 v26,v26,v82
M 22,a[180:183],58
v_rcp_f32 v83,v83
M 18,a[184:187],62
R 58,1,30720
v_mul_f32 v27,v27,v83
M 22,a[188:191],62
L 5
s_nop 0
v_cvt_pk_bf16_f32 v90,v26,v27
M 18,a[192:195],34
s_waitcnt vmcnt(0)
R 62,1,31744
v_fma_f32 v30,-v70,v78,v30
M 22,a[196:199],34
s_barrier
R 66,13,0
ds_read_b32 v7,v6 offset:0
M 18,a[200:203],38
R 34,2,0
v_fma_f32 v31,-v70,v79,v31
M 22,a[204:207],38
L 7
buffer_store_dword v4,v3,s[36:39],s70 offen offset:0 sc0 sc1
M 18,a[208:211],42
s_mov_b32 s70,s69
R 38,2,1024
v_fma_f32 v32,-v70,v80,v32
M 22,a[212:215],42
s_mov_b32 s69,s68
v_fma_f32 v33,-v70,v81,v33
v_mul_f32 v86,v30,v30
M 18,a[216:219],46
L 2
R 42,2,2048
v_mul_f32 v87,v31,v31
M 22,a[220:223],46
s_nop 0
v_mul_f32 v88,v32,v32
v_mul_f32 v89,v33,v33
M 18,a[224:227],50
s_add_u32 s68,s68,s63
R 46,2,3072
v_fma_f32 v86,v72,v86,v71
M 22,a[228:231],50
v_fma_f32 v87,v72,v87,v71
v_fma_f32 v88,v72,v88,v71
M 18,a[232:235],54
R 50,2,4096
v_fma_f32 v89,v72,v89,v71
M 22,a[236:239],54
s_nop 0
v_mul_f32 v86,v30,v86
v_mul_f32 v87,v31,v87
M 18,a[240:243],58
R 54,2,5120
v_mul_f32 v88,v32,v88
v_mul_f32 v89,v33,v89
v_exp_f32 v86,v86
v_exp_f32 v87,v87
v_exp_f32 v88,v88
v_exp_f32 v89,v89
M 22,a[244:247],58
v_rcp_f32 v84,v84
v_rcp_f32 v85,v85
v_fma_f32 v86,v86,v73,v73
v_fma_f32 v87,v87,v73,v73
v_fma_f32 v88,v88,v73,v73
v_fma_f32 v89,v89,v73,v73
v_rcp_f32 v86,v86
v_rcp_f32 v87,v87
v_rcp_f32 v88,v88
v_rcp_f32 v89,v89
v_cmp_eq_u32 vcc,s53,v7
M 18,a[248:251],62
s_cmp_eq_u64 vcc,-1
R 58,2,6144
v_mul_f32 v28,v28,v84
v_mul_f32 v29,v29,v85
v_cvt_pk_bf16_f32 v91,v28,v29
v_mul_f32 v30,v30,v86
v_mul_f32 v31,v31,v87
v_mul_f32 v32,v32,v88
v_mul_f32 v33,v33,v89
v_cvt_pk_bf16_f32 v92,v30,v31
v_cvt_pk_bf16_f32 v93,v32,v33
M 22,a[252:255],62
L 5
buffer_store_dwordx4 v[90:93],v0,s[36:39],s67 offen offset:0 sc0 sc1
v_xor_b32 v1,65536,v1
s_cbranch_scc1 Lg3o_L21_%=
s_cmp_eq_u32 vcc_lo,-1
s_cbranch_scc1 Lg3o_L11_%=
s_branch Lg3o_X1_%=
Lg3o_L11_%=:
v_mfma_f32_16x16x32_bf16 v[26:29],a[0:3],v[34:37],0
s_add_u32 s58,s58,s61
s_add_u32 m0,s59,1024
R 62,2,7168
v_mfma_f32_16x16x32_bf16 v[30:33],a[4:7],v[34:37],0
s_waitcnt vmcnt(2)
buffer_load_dword v5,s[36:39],s58 offen offset:0 sc1 lds
M 26,a[8:11],38
s_mov_b32 m0,s57
R 34,2,8192
v_add_f32_dpp v99,v98,v98 quad_perm:[1,0,3,2] row_mask:0xf bank_mask:0xf
M 30,a[12:15],38
L 5
buffer_load_dword v98,v11,s[36:39],s73 offen offset:0 sc1
M 26,a[16:19],42
s_add_u32 s56,s55,4096
R 38,2,9216
v_add_f32_dpp v99,v99,v99 quad_perm:[2,3,0,1] row_mask:0xf bank_mask:0xf
M 30,a[20:23],42
s_nop 0
v_fma_f32 v18,-v66,v74,v18
v_fma_f32 v19,-v66,v75,v19
M 26,a[24:27],46
s_mov_b32 s67,s65
R 42,2,10240
v_add_f32_dpp v102,v99,v99 row_half_mirror row_mask:0xf bank_mask:0xf
M 30,a[28:31],46
L 5
D 0,55,0
M 26,a[32:35],50
R 46,2,11264
v_mov_b32_dpp v99,v102 row_ror:8 row_mask:0xf bank_mask:0xf
M 30,a[36:39],50
v_fma_f32 v99,-v102,v102,v99
v_max_f32 v99,s87,v99
M 26,a[40:43],54
R 50,2,12288
v_fma_f32 v20,-v66,v76,v20
M 30,a[44:47],54
L 5
D 0,55,1024
M 26,a[48:51],58
R 54,2,13312
v_fma_f32 v21,-v66,v77,v21
M 30,a[52:55],58
v_rsq_f32 v100,v99
M 26,a[56:59],62
R 58,2,14336
v_mul_f32 v105,v99,v100
M 30,a[60:63],62
L 5
D 0,55,2048
M 26,a[64:67],34
R 62,2,15360
v_mul_f32 v103,s88,v100
M 30,a[68:71],34
v_mul_f32 v99,s89,v100
v_mul_f32 v104,v103,v100
M 26,a[72:75],38
R 34,2,16384
v_mul_f32 v104,v104,v99
M 30,a[76:79],38
L 5
ds_write_b128 v12,v[102:105] offset:2048
M 26,a[80:83],42
R 38,2,17408
v_mul_f32 v82,v18,v18
M 30,a[84:87],42
D 0,55,3072
M 26,a[88:91],46
s_add_u32 m0,s57,4096
R 42,2,18432
v_mul_f32 v83,v19,v19
M 30,a[92:95],46
L 6
D 0,56,0
M 26,a[96:99],50
s_add_u32 s55,s55,s60
R 46,2,19456
v_mul_f32 v84,v20,v20
M 30,a[100:103],50
s_add_u32 s57,s57,32768
v_mul_f32 v85,v21,v21
v_fma_f32 v82,v68,v82,v67
M 26,a[104:107],54
s_and_b32 s57,s57,131071
R 50,2,20480
v_fma_f32 v83,v68,v83,v67
M 30,a[108:111],54
L 6
D 0,56,1024
M 26,a[112:115],58
s_add_u32 s54,s54,s46
R 54,2,21504
v_fma_f32 v84,v68,v84,v67
M 30,a[116:119],58
s_add_u32 s73,s73,s64
v_fma_f32 v85,v68,v85,v67
v_mul_f32 v82,v18,v82
M 26,a[120:123],62
s_add_u32 s65,s65,s62
R 58,2,22528
v_mul_f32 v83,v19,v83
M 30,a[124:127],62
L 5
D 0,56,2048
M 26,a[128:131],34
R 62,2,23552
v_mul_f32 v84,v20,v84
M 30,a[132:135],34
v_exp_f32 v82,v82
M 26,a[136:139],38
R 34,2,24576
v_mul_f32 v85,v21,v85
M 30,a[140:143],38
L 5
D 0,56,3072
M 26,a[144:147],42
R 38,2,25600
v_fma_f32 v82,v82,v69,v69
M 30,a[148:151],42
v_exp_f32 v83,v83
M 26,a[152:155],46
R 42,2,26624
v_fma_f32 v83,v83,v69,v69
M 30,a[156:159],46
L 5
v_exp_f32 v84,v84
M 26,a[160:163],50
R 46,2,27648
v_fma_f32 v84,v84,v69,v69
M 30,a[164:167],50
v_exp_f32 v85,v85
M 26,a[168:171],54
R 50,2,28672
v_fma_f32 v85,v85,v69,v69
M 30,a[172:175],54
L 5
v_rcp_f32 v82,v82
M 26,a[176:179],58
R 54,2,29696
v_mul_f32 v18,v18,v82
M 30,a[180:183],58
v_rcp_f32 v83,v83
M 26,a[184:187],62
R 58,2,30720
v_mul_f32 v19,v19,v83
M 30,a[188:191],62
L 5
s_nop 0
v_cvt_pk_bf16_f32 v90,v18,v19
M 26,a[192:195],34
s_waitcnt vmcnt(0)
R 62,2,31744
v_fma_f32 v22,-v66,v78,v22
M 30,a[196:199],34
s_barrier
R 70,13,2048
ds_read_b32 v7,v6 offset:1024
M 26,a[200:203],38
R 34,1,0
v_fma_f32 v23,-v66,v79,v23
M 30,a[204:207],38
L 7
buffer_store_dword v4,v3,s[36:39],s70 offen offset:0 sc0 sc1
M 26,a[208:211],42
s_mov_b32 s70,s69
R 38,1,1024
v_fma_f32 v24,-v66,v80,v24
M 30,a[212:215],42
s_mov_b32 s69,s68
v_fma_f32 v25,-v66,v81,v25
v_mul_f32 v86,v22,v22
M 26,a[216:219],46
L 2
R 42,1,2048
v_mul_f32 v87,v23,v23
)"
R"(M 30,a[220:223],46
s_nop 0
v_mul_f32 v88,v24,v24
v_mul_f32 v89,v25,v25
M 26,a[224:227],50
s_add_u32 s68,s68,s63
R 46,1,3072
v_fma_f32 v86,v68,v86,v67
M 30,a[228:231],50
v_fma_f32 v87,v68,v87,v67
v_fma_f32 v88,v68,v88,v67
M 26,a[232:235],54
R 50,1,4096
v_fma_f32 v89,v68,v89,v67
M 30,a[236:239],54
s_nop 0
v_mul_f32 v86,v22,v86
v_mul_f32 v87,v23,v87
M 26,a[240:243],58
R 54,1,5120
v_mul_f32 v88,v24,v88
v_mul_f32 v89,v25,v89
v_exp_f32 v86,v86
v_exp_f32 v87,v87
v_exp_f32 v88,v88
v_exp_f32 v89,v89
M 30,a[244:247],58
v_rcp_f32 v84,v84
v_rcp_f32 v85,v85
v_fma_f32 v86,v86,v69,v69
v_fma_f32 v87,v87,v69,v69
v_fma_f32 v88,v88,v69,v69
v_fma_f32 v89,v89,v69,v69
v_rcp_f32 v86,v86
v_rcp_f32 v87,v87
v_rcp_f32 v88,v88
v_rcp_f32 v89,v89
v_cmp_eq_u32 vcc,s53,v7
M 26,a[248:251],62
s_cmp_eq_u64 vcc,-1
R 58,1,6144
v_mul_f32 v20,v20,v84
v_mul_f32 v21,v21,v85
v_cvt_pk_bf16_f32 v91,v20,v21
v_mul_f32 v22,v22,v86
v_mul_f32 v23,v23,v87
v_mul_f32 v24,v24,v88
v_mul_f32 v25,v25,v89
v_cvt_pk_bf16_f32 v92,v22,v23
v_cvt_pk_bf16_f32 v93,v24,v25
M 30,a[252:255],62
L 5
buffer_store_dwordx4 v[90:93],v0,s[36:39],s66 offen offset:0 sc0 sc1
v_xor_b32 v2,65536,v2
s_cbranch_scc1 Lg3o_L20_%=
s_cmp_eq_u32 vcc_lo,-1
s_cbranch_scc1 Lg3o_L10_%=
s_branch Lg3o_X0_%=
Lg3o_done_%=:
s_waitcnt vmcnt(1)
s_barrier
buffer_store_dword v4,v3,s[36:39],s70 offen offset:0 sc0 sc1
s_waitcnt vmcnt(1)
s_barrier
buffer_store_dword v4,v3,s[36:39],s72 offen offset:0 sc0 sc1
s_waitcnt vmcnt(0) lgkmcnt(0)
s_barrier
s_endpgm
Lcode_g3o_e:
.purgem M
.purgem R
.purgem T
.purgem D
.purgem L
)"
  :: "s"(ws), "s"(sWave), "s"(sG0), "s"(sStep), "s"(sOutOff), "s"(sFoutOff), "s"(sAuxOff), "s"(sWw), "s"(sBuf), "s"(sRk), "v"(vL16), "v"(vFlag), "v"(vSnapOff), "v"(vSnapRd), "v"(vWd0), "v"(vWd1), "v"(vWr0), "v"(vWr1), "v"(vWr2), "v"(vOutS), "v"(vAux0), "v"(vAux1), "v"(vAux2), "v"(vAux3), "v"(vAn), "v"(vAe), "s"(w3) : "v0", "v1", "v2", "v3", "v4", "v5", "v6", "v7", "v8", "v9", "v10", "v11", "v12", "v13", "v14", "v15", "v16", "v17", "v18", "v19", "v20", "v21", "v22", "v23", "v24", "v25", "v26", "v27", "v28", "v29", "v30", "v31", "v32", "v33", "v34", "v35", "v36", "v37", "v38", "v39", "v40", "v41", "v42", "v43", "v44", "v45", "v46", "v47", "v48", "v49", "v50", "v51", "v52", "v53", "v54", "v55", "v56", "v57", "v58", "v59", "v60", "v61", "v62", "v63", "v64", "v65", "v66", "v67", "v68", "v69", "v70", "v71", "v72", "v73", "v74", "v75", "v76", "v77", "v78", "v79", "v80", "v81", "v82", "v83", "v84", "v85", "v86", "v87", "v88", "v89", "v90", "v91", "v92", "v93", "v94", "v95", "v96", "v97", "v98", "v99", "v100", "v101", "v102", "v103", "v104", "v105", "v106", "v107", "v108", "v109", "v110", "v111", "v112", "v113", "v114", "v115", "v116", "v117", "v118", "v119", "v120", "v121", "v122", "v123", "v124", "v125", "v126", "v127", "v128", "v129", "v130", "v131", "v132", "v133", "v134", "v135", "v136", "v137", "v138", "v139", "v140", "v141", "v142", "v143", "v144", "v145", "v146", "v147", "v148", "v149", "v150", "v151", "v152", "v153", "v154", "v155", "v156", "v157", "v158", "v159", "v160", "v161", "v162", "v163", "v164", "v165", "v166", "v167", "v168", "v169", "v170", "v171", "v172", "v173", "v174", "v175", "v176", "v177", "v178", "v179", "v180", "v181", "v182", "v183", "v184", "v185", "v186", "v187", "v188", "v189", "v190", "v191", "v192", "v193", "v194", "v195", "v196", "v197", "v198", "v199", "v200", "v201", "v202", "v203", "v204", "v205", "v206", "v207", "v208", "v209", "v210", "v211", "v212", "v213", "v214", "v215", "v216", "v217", "v218", "v219", "v220", "v221", "v222", "v223", "v224", "v225", "v226", "v227", "v228", "v229", "v230", "v231", "v232", "v233", "v234", "v235", "v236", "v237", "v238", "v239", "a0", "a1", "a2", "a3", "a4", "a5", "a6", "a7", "a8", "a9", "a10", "a11", "a12", "a13", "a14", "a15", "a16", "a17", "a18", "a19", "a20", "a21", "a22", "a23", "a24", "a25", "a26", "a27", "a28", "a29", "a30", "a31", "a32", "a33", "a34", "a35", "a36", "a37", "a38", "a39", "a40", "a41", "a42", "a43", "a44", "a45", "a46", "a47", "a48", "a49", "a50", "a51", "a52", "a53", "a54", "a55", "a56", "a57", "a58", "a59", "a60", "a61", "a62", "a63", "a64", "a65", "a66", "a67", "a68", "a69", "a70", "a71", "a72", "a73", "a74", "a75", "a76", "a77", "a78", "a79", "a80", "a81", "a82", "a83", "a84", "a85", "a86", "a87", "a88", "a89", "a90", "a91", "a92", "a93", "a94", "a95", "a96", "a97", "a98", "a99", "a100", "a101", "a102", "a103", "a104", "a105", "a106", "a107", "a108", "a109", "a110", "a111", "a112", "a113", "a114", "a115", "a116", "a117", "a118", "a119", "a120", "a121", "a122", "a123", "a124", "a125", "a126", "a127", "a128", "a129", "a130", "a131", "a132", "a133", "a134", "a135", "a136", "a137", "a138", "a139", "a140", "a141", "a142", "a143", "a144", "a145", "a146", "a147", "a148", "a149", "a150", "a151", "a152", "a153", "a154", "a155", "a156", "a157", "a158", "a159", "a160", "a161", "a162", "a163", "a164", "a165", "a166", "a167", "a168", "a169", "a170", "a171", "a172", "a173", "a174", "a175", "a176", "a177", "a178", "a179", "a180", "a181", "a182", "a183", "a184", "a185", "a186", "a187", "a188", "a189", "a190", "a191", "a192", "a193", "a194", "a195", "a196", "a197", "a198", "a199", "a200", "a201", "a202", "a203", "a204", "a205", "a206", "a207", "a208", "a209", "a210", "a211", "a212", "a213", "a214", "a215", "a216", "a217", "a218", "a219", "a220", "a221", "a222", "a223", "a224", "a225", "a226", "a227", "a228", "a229", "a230", "a231", "a232", "a233", "a234", "a235", "a236", "a237", "a238", "a239", "a240", "a241", "a242", "a243", "a244", "a245", "a246", "a247", "a248", "a249", "a250", "a251", "a252", "a253", "a254", "a255", "s36", "s37", "s38", "s39", "s40", "s41", "s42", "s43", "s44", "s45", "s46", "s47", "s48", "s49", "s50", "s51", "s52", "s53", "s54", "s55", "s56", "s57", "s58", "s59", "s60", "s61", "s62", "s63", "s64", "s65", "s66", "s67", "s68", "s69", "s70", "s71", "s72", "s73", "s74", "s75", "s76", "s77", "s78", "s79", "s80", "s81", "s82", "s83", "s84", "s85", "s86", "s87", "s88", "s89", "s90", "s91", "s92", "s93", "s94", "s95", "s96", "s97", "vcc", "scc", "memory"); }
    __builtin_unreachable();
  } else if (kind == 4) {
    const unsigned sStep = 5;
    COMMON(10, (kh * 9 + fj) * 128, 2560, 143360, 2048, 448, 0);
    const int cs = slice;
    const unsigned sOutOff = (kh ? OFF_P1 : OFF_P0) + (cs * 4 + wave) * 1024, sFoutOff = OFF_FP + (kh * 8 + cs) * 128;
    const unsigned sAuxOff = (kh ? OFF_XP : OFF_X1) + (cs * 4 + wave) * 1024, sWw = (kh * 1792 + 16 * wave) * 2048 + cs * 256, sBuf = kh * 57344;
    (void)vWr2;
    asm volatile(
R"(.macro M d,w,x
v_mfma_f32_16x16x32_bf16 v[\d:\d+3], \w, v[\x:\x+3], v[\d:\d+3]
.endm
.macro R d,a,o
ds_read_b128 v[\d:\d+3], v\a offset:\o
.endm
.macro T d,a,o
ds_read_b64_tr_b16 \d, v\a offset:\o
.endm
.macro D a,s,o
buffer_load_dwordx4 v\a, s[36:39], s\s offen offset:\o sc1 lds
.endm
.macro L c
s_waitcnt lgkmcnt(\c)
.endm
Lcode_g4_b:
s_mov_b64 s[36:37],%0
s_mov_b32 s38,111243392
s_mov_b32 s39,0x00020000
s_mov_b32 s44,%1
s_mov_b32 s45,%2
s_mov_b32 s46,%3
s_mov_b32 s47,%4
s_mov_b32 s48,%5
s_mov_b32 s49,%6
s_mov_b32 s50,%7
s_mov_b32 s51,%8
s_mov_b32 s93,%9
v_mov_b32 v0,%10
v_mov_b32 v3,%11
v_mov_b32 v5,%12
v_mov_b32 v6,%13
v_mov_b32 v10,%14
v_mov_b32 v11,%15
v_mov_b32 v12,%16
v_mov_b32 v13,%17
s_mov_b64 s[40:41],%18
s_mov_b32 s42,7340032
s_mov_b32 s43,0x00020000
s_lshl_b32 s57,s44,11
s_mov_b32 s79,111243264
buffer_load_dword v4,off,s[36:39],s79 sc1
s_add_u32 s55,s50,0
s_add_u32 m0,s57,0
s_nop 0
buffer_load_dwordx4 v10,s[40:43],s55 offen offset:0  lds
s_add_u32 m0,s57,1024
s_nop 0
buffer_load_dwordx4 v11,s[40:43],s55 offen offset:0  lds
s_add_u32 m0,s57,8064
s_nop 0
buffer_load_dwordx4 v10,s[40:43],s55 offen offset:128  lds
s_add_u32 m0,s57,9088
s_nop 0
buffer_load_dwordx4 v11,s[40:43],s55 offen offset:128  lds
s_add_u32 s55,s50,131072
s_add_u32 m0,s57,16384
s_nop 0
buffer_load_dwordx4 v10,s[40:43],s55 offen offset:0  lds
s_add_u32 m0,s57,17408
s_nop 0
buffer_load_dwordx4 v11,s[40:43],s55 offen offset:0  lds
s_add_u32 m0,s57,24448
s_nop 0
buffer_load_dwordx4 v10,s[40:43],s55 offen offset:128  lds
s_add_u32 m0,s57,25472
s_nop 0
buffer_load_dwordx4 v11,s[40:43],s55 offen offset:128  lds
s_add_u32 s55,s50,262144
s_add_u32 m0,s57,32768
s_nop 0
buffer_load_dwordx4 v10,s[40:43],s55 offen offset:0  lds
s_add_u32 m0,s57,33792
s_nop 0
buffer_load_dwordx4 v11,s[40:43],s55 offen offset:0  lds
s_add_u32 m0,s57,40832
s_nop 0
buffer_load_dwordx4 v10,s[40:43],s55 offen offset:128  lds
s_add_u32 m0,s57,41856
s_nop 0
buffer_load_dwordx4 v11,s[40:43],s55 offen offset:128  lds
s_add_u32 s55,s50,393216
s_add_u32 m0,s57,49152
s_nop 0
buffer_load_dwordx4 v10,s[40:43],s55 offen offset:0  lds
s_add_u32 m0,s57,50176
s_nop 0
buffer_load_dwordx4 v11,s[40:43],s55 offen offset:0  lds
s_add_u32 m0,s57,57216
s_nop 0
buffer_load_dwordx4 v10,s[40:43],s55 offen offset:128  lds
s_add_u32 m0,s57,58240
s_nop 0
buffer_load_dwordx4 v11,s[40:43],s55 offen offset:128  lds
s_add_u32 s55,s50,524288
s_add_u32 m0,s57,65536
s_nop 0
buffer_load_dwordx4 v10,s[40:43],s55 offen offset:0  lds
s_add_u32 m0,s57,66560
s_nop 0
buffer_load_dwordx4 v11,s[40:43],s55 offen offset:0  lds
s_add_u32 m0,s57,73600
s_nop 0
buffer_load_dwordx4 v10,s[40:43],s55 offen offset:128  lds
s_add_u32 m0,s57,74624
s_nop 0
buffer_load_dwordx4 v11,s[40:43],s55 offen offset:128  lds
s_add_u32 s55,s50,655360
s_add_u32 m0,s57,81920
s_nop 0
buffer_load_dwordx4 v10,s[40:43],s55 offen offset:0  lds
s_add_u32 m0,s57,82944
s_nop 0
buffer_load_dwordx4 v11,s[40:43],s55 offen offset:0  lds
s_add_u32 m0,s57,89984
s_nop 0
buffer_load_dwordx4 v10,s[40:43],s55 offen offset:128  lds
s_add_u32 m0,s57,91008
s_nop 0
buffer_load_dwordx4 v11,s[40:43],s55 offen offset:128  lds
s_waitcnt vmcnt(20)
L 0
s_barrier
s_add_u32 s55,s50,786432
s_add_u32 m0,s57,98304
s_nop 0
buffer_load_dwordx4 v10,s[40:43],s55 offen offset:0  lds
s_add_u32 m0,s57,99328
s_nop 0
buffer_load_dwordx4 v11,s[40:43],s55 offen offset:0  lds
s_add_u32 m0,s57,106368
s_nop 0
buffer_load_dwordx4 v10,s[40:43],s55 offen offset:128  lds
s_add_u32 m0,s57,107392
s_nop 0
buffer_load_dwordx4 v11,s[40:43],s55 offen offset:128  lds
v_add_u32 v18,0,v12
v_add_u32 v19,0,v13
T a[0:1],18,0
T a[2:3],18,512
T a[4:5],19,0
T a[6:7],19,512
T a[8:9],18,4096
T a[10:11],18,4608
T a[12:13],19,4096
T a[14:15],19,4608
s_waitcnt vmcnt(20)
L 0
s_barrier
s_add_u32 s55,s50,917504
s_add_u32 m0,s57,0
s_nop 0
buffer_load_dwordx4 v10,s[40:43],s55 offen offset:0  lds
s_add_u32 m0,s57,1024
s_nop 0
buffer_load_dwordx4 v11,s[40:43],s55 offen offset:0  lds
s_add_u32 m0,s57,8064
s_nop 0
buffer_load_dwordx4 v10,s[40:43],s55 offen offset:128  lds
s_add_u32 m0,s57,9088
s_nop 0
buffer_load_dwordx4 v11,s[40:43],s55 offen offset:128  lds
v_add_u32 v18,16384,v12
v_add_u32 v19,16384,v13
T a[16:17],18,0
T a[18:19],18,512
T a[20:21],19,0
T a[22:23],19,512
T a[24:25],18,4096
T a[26:27],18,4608
T a[28:29],19,4096
T a[30:31],19,4608
s_waitcnt vmcnt(20)
L 0
s_barrier
s_add_u32 s55,s50,1048576
s_add_u32 m0,s57,16384
s_nop 0
buffer_load_dwordx4 v10,s[40:43],s55 offen offset:0  lds
s_add_u32 m0,s57,17408
s_nop 0
buffer_load_dwordx4 v11,s[40:43],s55 offen offset:0  lds
s_add_u32 m0,s57,24448
s_nop 0
buffer_load_dwordx4 v10,s[40:43],s55 offen offset:128  lds
s_add_u32 m0,s57,25472
s_nop 0
buffer_load_dwordx4 v11,s[40:43],s55 offen offset:128  lds
v_add_u32 v18,32768,v12
v_add_u32 v19,32768,v13
T a[32:33],18,0
T a[34:35],18,512
T a[36:37],19,0
T a[38:39],19,512
T a[40:41],18,4096
T a[42:43],18,4608
T a[44:45],19,4096
T a[46:47],19,4608
s_waitcnt vmcnt(20)
L 0
s_barrier
s_add_u32 s55,s50,1179648
s_add_u32 m0,s57,32768
s_nop 0
buffer_load_dwordx4 v10,s[40:43],s55 offen offset:0  lds
s_add_u32 m0,s57,33792
s_nop 0
buffer_load_dwordx4 v11,s[40:43],s55 offen offset:0  lds
s_add_u32 m0,s57,40832
s_nop 0
buffer_load_dwordx4 v10,s[40:43],s55 offen offset:128  lds
s_add_u32 m0,s57,41856
s_nop 0
buffer_load_dwordx4 v11,s[40:43],s55 offen offset:128  lds
v_add_u32 v18,49152,v12
v_add_u32 v19,49152,v13
T a[48:49],18,0
T a[50:51],18,512
T a[52:53],19,0
T a[54:55],19,512
T a[56:57],18,4096
T a[58:59],18,4608
T a[60:61],19,4096
T a[62:63],19,4608
s_waitcnt vmcnt(20)
L 0
s_barrier
s_add_u32 s55,s50,1310720
s_add_u32 m0,s57,49152
s_nop 0
buffer_load_dwordx4 v10,s[40:43],s55 offen offset:0  lds
s_add_u32 m0,s57,50176
s_nop 0
buffer_load_dwordx4 v11,s[40:43],s55 offen offset:0  lds
s_add_u32 m0,s57,57216
s_nop 0
buffer_load_dwordx4 v10,s[40:43],s55 offen offset:128  lds
s_add_u32 m0,s57,58240
s_nop 0
buffer_load_dwordx4 v11,s[40:43],s55 offen offset:128  lds
v_add_u32 v18,65536,v12
v_add_u32 v19,65536,v13
T a[64:65],18,0
T a[66:67],18,512
T a[68:69],19,0
T a[70:71],19,512
T a[72:73],18,4096
T a[74:75],18,4608
T a[76:77],19,4096
T a[78:79],19,4608
s_waitcnt vmcnt(20)
L 0
s_barrier
s_add_u32 s55,s50,1441792
s_add_u32 m0,s57,65536
s_nop 0
buffer_load_dwordx4 v10,s[40:43],s55 offen offset:0  lds
s_add_u32 m0,s57,66560
s_nop 0
buffer_load_dwordx4 v11,s[40:43],s55 offen offset:0  lds
s_add_u32 m0,s57,73600
s_nop 0
buffer_load_dwordx4 v10,s[40:43],s55 offen offset:128  lds
s_add_u32 m0,s57,74624
s_nop 0
buffer_load_dwordx4 v11,s[40:43],s55 offen offset:128  lds
v_add_u32 v18,81920,v12
v_add_u32 v19,81920,v13
T a[80:81],18,0
T a[82:83],18,512
T a[84:85],19,0
T a[86:87],19,512
T a[88:89],18,4096
T a[90:91],18,4608
T a[92:93],19,4096
T a[94:95],19,4608
s_waitcnt vmcnt(20)
L 0
s_barrier
s_add_u32 s55,s50,1572864
s_add_u32 m0,s57,81920
s_nop 0
buffer_load_dwordx4 v10,s[40:43],s55 offen offset:0  lds
s_add_u32 m0,s57,82944
s_nop 0
buffer_load_dwordx4 v11,s[40:43],s55 offen offset:0  lds
s_add_u32 m0,s57,89984
s_nop 0
buffer_load_dwordx4 v10,s[40:43],s55 offen offset:128  lds
s_add_u32 m0,s57,91008
s_nop 0
buffer_load_dwordx4 v11,s[40:43],s55 offen offset:128  lds
v_add_u32 v18,98304,v12
v_add_u32 v19,98304,v13
T a[96:97],18,0
T a[98:99],18,512
T a[100:101],19,0
T a[102:103],19,512
T a[104:105],18,4096
T a[106:107],18,4608
T a[108:109],19,4096
T a[110:111],19,4608
s_waitcnt vmcnt(20)
L 0
s_barrier
s_add_u32 s55,s50,1703936
s_add_u32 m0,s57,98304
s_nop 0
buffer_load_dwordx4 v10,s[40:43],s55 offen offset:0  lds
s_add_u32 m0,s57,99328
s_nop 0
buffer_load_dwordx4 v11,s[40:43],s55 offen offset:0  lds
s_add_u32 m0,s57,106368
s_nop 0
buffer_load_dwordx4 v10,s[40:43],s55 offen offset:128  lds
s_add_u32 m0,s57,107392
s_nop 0
buffer_load_dwordx4 v11,s[40:43],s55 offen offset:128  lds
v_add_u32 v18,0,v12
v_add_u32 v19,0,v13
T a[112:113],18,0
T a[114:115],18,512
T a[116:117],19,0
T a[118:119],19,512
T a[120:121],18,4096
T a[122:123],18,4608
T a[124:125],19,4096
T a[126:127],19,4608
s_waitcnt vmcnt(20)
L 0
s_barrier
s_add_u32 s55,s50,1835008
s_add_u32 m0,s57,0
s_nop 0
buffer_load_dwordx4 v10,s[40:43],s55 offen offset:0  lds
s_add_u32 m0,s57,1024
s_nop 0
buffer_load_dwordx4 v11,s[40:43],s55 offen offset:0  lds
s_add_u32 m0,s57,8064
s_nop 0
buffer_load_dwordx4 v10,s[40:43],s55 offen offset:128  lds
s_add_u32 m0,s57,9088
s_nop 0
buffer_load_dwordx4 v11,s[40:43],s55 offen offset:128  lds
v_add_u32 v18,16384,v12
v_add_u32 v19,16384,v13
T a[128:129],18,0
T a[130:131],18,512
T a[132:133],19,0
T a[134:135],19,512
T a[136:137],18,4096
T a[138:139],18,4608
T a[140:141],19,4096
T a[142:143],19,4608
s_waitcnt vmcnt(20)
L 0
s_barrier
s_add_u32 s55,s50,1966080
s_add_u32 m0,s57,16384
s_nop 0
buffer_load_dwordx4 v10,s[40:43],s55 offen offset:0  lds
s_add_u32 m0,s57,17408
s_nop 0
buffer_load_dwordx4 v11,s[40:43],s55 offen offset:0  lds
s_add_u32 m0,s57,24448
s_nop 0
buffer_load_dwordx4 v10,s[40:43],s55 offen offset:128  lds
s_add_u32 m0,s57,25472
s_nop 0
buffer_load_dwordx4 v11,s[40:43],s55 offen offset:128  lds
v_add_u32 v18,32768,v12
v_add_u32 v19,32768,v13
T a[144:145],18,0
T a[146:147],18,512
T a[148:149],19,0
T a[150:151],19,512
T a[152:153],18,4096
T a[154:155],18,4608
T a[156:157],19,4096
T a[158:159],19,4608
s_waitcnt vmcnt(20)
L 0
s_barrier
s_add_u32 s55,s50,2097152
s_add_u32 m0,s57,32768
s_nop 0
buffer_load_dwordx4 v10,s[40:43],s55 offen offset:0  lds
s_add_u32 m0,s57,33792
s_nop 0
buffer_load_dwordx4 v11,s[40:43],s55 offen offset:0  lds
s_add_u32 m0,s57,40832
s_nop 0
buffer_load_dwordx4 v10,s[40:43],s55 offen offset:128  lds
s_add_u32 m0,s57,41856
s_nop 0
buffer_load_dwordx4 v11,s[40:43],s55 offen offset:128  lds
v_add_u32 v18,49152,v12
v_add_u32 v19,49152,v13
T a[160:161],18,0
T a[162:163],18,512
T a[164:165],19,0
T a[166:167],19,512
T a[168:169],18,4096
T a[170:171],18,4608
T a[172:173],19,4096
T a[174:175],19,4608
s_waitcnt vmcnt(20)
L 0
s_barrier
s_add_u32 s55,s50,2228224
s_add_u32 m0,s57,49152
s_nop 0
buffer_load_dwordx4 v10,s[40:43],s55 offen offset:0  lds
s_add_u32 m0,s57,50176
s_nop 0
buffer_load_dwordx4 v11,s[40:43],s55 offen offset:0  lds
s_add_u32 m0,s57,57216
s_nop 0
buffer_load_dwordx4 v10,s[40:43],s55 offen offset:128  lds
s_add_u32 m0,s57,58240
s_nop 0
buffer_load_dwordx4 v11,s[40:43],s55 offen offset:128  lds
v_add_u32 v18,65536,v12
v_add_u32 v19,65536,v13
T a[176:177],18,0
T a[178:179],18,512
T a[180:181],19,0
T a[182:183],19,512
T a[184:185],18,4096
T a[186:187],18,4608
T a[188:189],19,4096
T a[190:191],19,4608
s_waitcnt vmcnt(20)
L 0
s_barrier
s_add_u32 s55,s50,2359296
s_add_u32 m0,s57,65536
s_nop 0
buffer_load_dwordx4 v10,s[40:43],s55 offen offset:0  lds
s_add_u32 m0,s57,66560
s_nop 0
buffer_load_dwordx4 v11,s[40:43],s55 offen offset:0  lds
s_add_u32 m0,s57,73600
s_nop 0
buffer_load_dwordx4 v10,s[40:43],s55 offen offset:128  lds
s_add_u32 m0,s57,74624
s_nop 0
buffer_load_dwordx4 v11,s[40:43],s55 offen offset:128  lds
v_add_u32 v18,81920,v12
v_add_u32 v19,81920,v13
T a[192:193],18,0
T a[194:195],18,512
T a[196:197],19,0
T a[198:199],19,512
T a[200:201],18,4096
T a[202:203],18,4608
T a[204:205],19,4096
T a[206:207],19,4608
s_waitcnt vmcnt(20)
L 0
s_barrier
s_add_u32 s55,s50,2490368
s_add_u32 m0,s57,81920
s_nop 0
buffer_load_dwordx4 v10,s[40:43],s55 offen offset:0  lds
s_add_u32 m0,s57,82944
s_nop 0
buffer_load_dwordx4 v11,s[40:43],s55 offen offset:0  lds
s_add_u32 m0,s57,89984
s_nop 0
buffer_load_dwordx4 v10,s[40:43],s55 offen offset:128  lds
s_add_u32 m0,s57,91008
s_nop 0
buffer_load_dwordx4 v11,s[40:43],s55 offen offset:128  lds
v_add_u32 v18,98304,v12
v_add_u32 v19,98304,v13
T a[208:209],18,0
T a[210:211],18,512
T a[212:213],19,0
T a[214:215],19,512
T a[216:217],18,4096
T a[218:219],18,4608
T a[220:221],19,4096
T a[222:223],19,4608
s_waitcnt vmcnt(20)
L 0
s_barrier
s_add_u32 s55,s50,2621440
s_add_u32 m0,s57,98304
s_nop 0
buffer_load_dwordx4 v10,s[40:43],s55 offen offset:0  lds
s_add_u32 m0,s57,99328
s_nop 0
buffer_load_dwordx4 v11,s[40:43],s55 offen offset:0  lds
s_add_u32 m0,s57,106368
s_nop 0
buffer_load_dwordx4 v10,s[40:43],s55 offen offset:128  lds
s_add_u32 m0,s57,107392
s_nop 0
buffer_load_dwordx4 v11,s[40:43],s55 offen offset:128  lds
)"
R"(v_add_u32 v18,0,v12
v_add_u32 v19,0,v13
T a[224:225],18,0
T a[226:227],18,512
T a[228:229],19,0
T a[230:231],19,512
T a[232:233],18,4096
T a[234:235],18,4608
T a[236:237],19,4096
T a[238:239],19,4608
s_waitcnt vmcnt(20)
L 0
s_barrier
s_add_u32 s55,s50,2752512
s_add_u32 m0,s57,0
s_nop 0
buffer_load_dwordx4 v10,s[40:43],s55 offen offset:0  lds
s_add_u32 m0,s57,1024
s_nop 0
buffer_load_dwordx4 v11,s[40:43],s55 offen offset:0  lds
s_add_u32 m0,s57,8064
s_nop 0
buffer_load_dwordx4 v10,s[40:43],s55 offen offset:128  lds
s_add_u32 m0,s57,9088
s_nop 0
buffer_load_dwordx4 v11,s[40:43],s55 offen offset:128  lds
v_add_u32 v18,16384,v12
v_add_u32 v19,16384,v13
T a[240:241],18,0
T a[242:243],18,512
T a[244:245],19,0
T a[246:247],19,512
T a[248:249],18,4096
T a[250:251],18,4608
T a[252:253],19,4096
T a[254:255],19,4608
s_waitcnt vmcnt(20)
L 0
s_barrier
s_add_u32 s55,s50,2883584
s_add_u32 m0,s57,16384
s_nop 0
buffer_load_dwordx4 v10,s[40:43],s55 offen offset:0  lds
s_add_u32 m0,s57,17408
s_nop 0
buffer_load_dwordx4 v11,s[40:43],s55 offen offset:0  lds
s_add_u32 m0,s57,24448
s_nop 0
buffer_load_dwordx4 v10,s[40:43],s55 offen offset:128  lds
s_add_u32 m0,s57,25472
s_nop 0
buffer_load_dwordx4 v11,s[40:43],s55 offen offset:128  lds
v_add_u32 v18,32768,v12
v_add_u32 v19,32768,v13
T v[64:65],18,0
T v[66:67],18,512
T v[68:69],19,0
T v[70:71],19,512
T v[72:73],18,4096
T v[74:75],18,4608
T v[76:77],19,4096
T v[78:79],19,4608
s_waitcnt vmcnt(20)
L 0
s_barrier
s_add_u32 s55,s50,3014656
s_add_u32 m0,s57,32768
s_nop 0
buffer_load_dwordx4 v10,s[40:43],s55 offen offset:0  lds
s_add_u32 m0,s57,33792
s_nop 0
buffer_load_dwordx4 v11,s[40:43],s55 offen offset:0  lds
s_add_u32 m0,s57,40832
s_nop 0
buffer_load_dwordx4 v10,s[40:43],s55 offen offset:128  lds
s_add_u32 m0,s57,41856
s_nop 0
buffer_load_dwordx4 v11,s[40:43],s55 offen offset:128  lds
v_add_u32 v18,49152,v12
v_add_u32 v19,49152,v13
T v[80:81],18,0
T v[82:83],18,512
T v[84:85],19,0
T v[86:87],19,512
T v[88:89],18,4096
T v[90:91],18,4608
T v[92:93],19,4096
T v[94:95],19,4608
s_waitcnt vmcnt(20)
L 0
s_barrier
s_add_u32 s55,s50,3145728
s_add_u32 m0,s57,49152
s_nop 0
buffer_load_dwordx4 v10,s[40:43],s55 offen offset:0  lds
s_add_u32 m0,s57,50176
s_nop 0
buffer_load_dwordx4 v11,s[40:43],s55 offen offset:0  lds
s_add_u32 m0,s57,57216
s_nop 0
buffer_load_dwordx4 v10,s[40:43],s55 offen offset:128  lds
s_add_u32 m0,s57,58240
s_nop 0
buffer_load_dwordx4 v11,s[40:43],s55 offen offset:128  lds
v_add_u32 v18,65536,v12
v_add_u32 v19,65536,v13
T v[96:97],18,0
T v[98:99],18,512
T v[100:101],19,0
T v[102:103],19,512
T v[104:105],18,4096
T v[106:107],18,4608
T v[108:109],19,4096
T v[110:111],19,4608
s_waitcnt vmcnt(20)
L 0
s_barrier
s_add_u32 s55,s50,3276800
s_add_u32 m0,s57,65536
s_nop 0
buffer_load_dwordx4 v10,s[40:43],s55 offen offset:0  lds
s_add_u32 m0,s57,66560
s_nop 0
buffer_load_dwordx4 v11,s[40:43],s55 offen offset:0  lds
s_add_u32 m0,s57,73600
s_nop 0
buffer_load_dwordx4 v10,s[40:43],s55 offen offset:128  lds
s_add_u32 m0,s57,74624
s_nop 0
buffer_load_dwordx4 v11,s[40:43],s55 offen offset:128  lds
v_add_u32 v18,81920,v12
v_add_u32 v19,81920,v13
T v[112:113],18,0
T v[114:115],18,512
T v[116:117],19,0
T v[118:119],19,512
T v[120:121],18,4096
T v[122:123],18,4608
T v[124:125],19,4096
T v[126:127],19,4608
s_waitcnt vmcnt(20)
L 0
s_barrier
s_add_u32 s55,s50,3407872
s_add_u32 m0,s57,81920
s_nop 0
buffer_load_dwordx4 v10,s[40:43],s55 offen offset:0  lds
s_add_u32 m0,s57,82944
s_nop 0
buffer_load_dwordx4 v11,s[40:43],s55 offen offset:0  lds
s_add_u32 m0,s57,89984
s_nop 0
buffer_load_dwordx4 v10,s[40:43],s55 offen offset:128  lds
s_add_u32 m0,s57,91008
s_nop 0
buffer_load_dwordx4 v11,s[40:43],s55 offen offset:128  lds
v_add_u32 v18,98304,v12
v_add_u32 v19,98304,v13
T v[128:129],18,0
T v[130:131],18,512
T v[132:133],19,0
T v[134:135],19,512
T v[136:137],18,4096
T v[138:139],18,4608
T v[140:141],19,4096
T v[142:143],19,4608
s_waitcnt vmcnt(20)
L 0
s_barrier
s_add_u32 s55,s50,3538944
s_add_u32 m0,s57,98304
s_nop 0
buffer_load_dwordx4 v10,s[40:43],s55 offen offset:0  lds
s_add_u32 m0,s57,99328
s_nop 0
buffer_load_dwordx4 v11,s[40:43],s55 offen offset:0  lds
s_add_u32 m0,s57,106368
s_nop 0
buffer_load_dwordx4 v10,s[40:43],s55 offen offset:128  lds
s_add_u32 m0,s57,107392
s_nop 0
buffer_load_dwordx4 v11,s[40:43],s55 offen offset:128  lds
v_add_u32 v18,0,v12
v_add_u32 v19,0,v13
T v[144:145],18,0
T v[146:147],18,512
T v[148:149],19,0
T v[150:151],19,512
T v[152:153],18,4096
T v[154:155],18,4608
T v[156:157],19,4096
T v[158:159],19,4608
s_waitcnt vmcnt(20)
L 0
s_barrier
v_add_u32 v18,16384,v12
v_add_u32 v19,16384,v13
T v[160:161],18,0
T v[162:163],18,512
T v[164:165],19,0
T v[166:167],19,512
T v[168:169],18,4096
T v[170:171],18,4608
T v[172:173],19,4096
T v[174:175],19,4608
s_waitcnt vmcnt(16)
L 0
s_barrier
v_add_u32 v18,32768,v12
v_add_u32 v19,32768,v13
T v[176:177],18,0
T v[178:179],18,512
T v[180:181],19,0
T v[182:183],19,512
T v[184:185],18,4096
T v[186:187],18,4608
T v[188:189],19,4096
T v[190:191],19,4608
s_waitcnt vmcnt(12)
L 0
s_barrier
v_add_u32 v18,49152,v12
v_add_u32 v19,49152,v13
T v[192:193],18,0
T v[194:195],18,512
T v[196:197],19,0
T v[198:199],19,512
T v[200:201],18,4096
T v[202:203],18,4608
T v[204:205],19,4096
T v[206:207],19,4608
s_waitcnt vmcnt(8)
L 0
s_barrier
v_add_u32 v18,65536,v12
v_add_u32 v19,65536,v13
T v[208:209],18,0
T v[210:211],18,512
T v[212:213],19,0
T v[214:215],19,512
T v[216:217],18,4096
T v[218:219],18,4608
T v[220:221],19,4096
T v[222:223],19,4608
s_waitcnt vmcnt(4)
L 0
s_barrier
v_add_u32 v18,81920,v12
v_add_u32 v19,81920,v13
T v[224:225],18,0
T v[226:227],18,512
T v[228:229],19,0
T v[230:231],19,512
T v[232:233],18,4096
T v[234:235],18,4608
T v[236:237],19,4096
T v[238:239],19,4608
s_waitcnt vmcnt(0)
L 0
s_barrier
v_add_u32 v18,98304,v12
v_add_u32 v19,98304,v13
T v[240:241],18,0
T v[242:243],18,512
T v[244:245],19,0
T v[246:247],19,512
T v[248:249],18,4096
T v[250:251],18,4608
T v[252:253],19,4096
T v[254:255],19,4608
L 0
s_barrier
v_readfirstlane_b32 s53,v4
s_nop 3
s_add_u32 s53,s53,1
s_cmp_eq_u32 s53,0
s_cselect_b32 s53,1,s53
v_mov_b32 v4,s53
s_mov_b32 s82,0
s_mov_b32 s80,0
s_mov_b32 s54,s45
s_mul_i32 s57,s44,7168
s_mul_i32 s55,s45,114688
s_add_u32 s55,s55,s57
s_add_u32 s55,s55,40894464
s_mul_i32 s60,s46,114688
s_mul_i32 s61,s46,2560
s_mul_i32 s62,s46,32768
s_mul_i32 s63,s46,2048
s_mul_i32 s64,s46,512
s_lshl_b32 s59,s44,8
s_add_u32 s59,s59,143360
s_mul_i32 s58,s45,2560
s_add_u32 s58,s58,109891584
s_mul_i32 s65,s45,32768
s_add_u32 s65,s65,s47
s_mul_i32 s68,s45,2048
s_add_u32 s68,s68,s48
s_mul_i32 s73,s45,512
s_add_u32 s73,s73,106954752
s_mul_i32 s74,s45,32768
s_add_u32 s74,s74,s49
s_mul_i32 s75,s45,1024
s_add_u32 s75,s75,107479040
s_mov_b32 s66,0x7fff0000
s_mov_b32 s67,0x7fff0000
s_mov_b32 s69,0x7fff0000
s_mov_b32 s76,0x7fff0000
s_mov_b32 s77,0x7fff0000
s_mov_b32 s70,0x7fff0000
s_mov_b32 s71,0x7fff0000
s_mov_b32 s72,0x7fff0000
v_mov_b32 v1,v0
v_add_u32 v2,28672,v0
s_mov_b32 s84,28672
s_mov_b32 s86,0x3a800000
s_mov_b32 s87,0x3727c5ac
s_mov_b32 s88,0xc0135761
s_mov_b32 s89,0x3d372713
s_and_b32 s85,s44,1
s_mul_i32 s85,s85,127
s_add_u32 s55,s55,s51
v_lshlrev_b32 v8,22,v0
v_and_b32 v8,0x80000000,v8
v_or_b32 v8,v8,v5
s_sub_u32 s60,s60,28672
s_add_u32 m0,s59,4096
s_nop 0
buffer_load_dword v5,s[36:39],s58 offen offset:0 sc1 lds
buffer_store_dword v4,v3,s[36:39],s71 offen offset:0 sc0 sc1
s_add_u32 m0,s59,5120
s_nop 0
buffer_load_dword v5,s[36:39],s58 offen offset:0 sc1 lds
buffer_store_dword v4,v3,s[36:39],s71 offen offset:0 sc0 sc1
s_mov_b32 s83,0
Lg4_poll_%=:
s_add_u32 s78,s83,2
s_and_b32 s78,s78,3
s_lshl_b32 s78,s78,10
s_add_u32 s78,s78,4096
s_add_u32 m0,s59,s78
s_nop 0
buffer_load_dword v5,s[36:39],s58 offen offset:0 sc1 lds
s_and_b32 s78,s83,3
s_lshl_b32 s78,s78,10
v_add_u32 v62,s78,v6
s_waitcnt vmcnt(4)
s_barrier
ds_read_b32 v7,v62 offset:4096
buffer_store_dword v4,v3,s[36:39],s70 offen offset:0 sc0 sc1
s_mov_b32 s70,s71
s_mov_b32 s71,s72
s_mov_b32 s72,0x7fff0000
s_add_u32 s83,s83,1
s_add_u32 s82,s82,1
L 0
v_cmp_eq_u32 vcc,s53,v7
s_nop 0
s_cmp_eq_u32 vcc_lo,-1
s_cbranch_scc1 Lg4_go_%=
s_cmp_lt_u32 s82,20000
s_cbranch_scc1 Lg4_poll_%=
Lg4_go_%=:
s_mov_b32 m0,s57
s_add_u32 s56,s55,4096
D 0,55,0
D 0,55,1024
D 0,55,2048
D 0,55,3072
s_add_u32 m0,s57,4096
s_nop 0
D 0,56,0
D 0,56,1024
D 0,56,2048
s_add_u32 s55,s55,28672
s_add_u32 s57,s57,28672
s_sub_u32 s78,s57,143360
s_min_u32 s57,s57,s78
s_add_u32 s54,s54,s46
s_mov_b32 m0,s57
s_add_u32 s56,s55,4096
D 0,55,0
D 0,55,1024
D 0,55,2048
D 0,55,3072
s_add_u32 m0,s57,4096
s_nop 0
D 0,56,0
D 0,56,1024
D 0,56,2048
s_add_u32 s55,s55,s60
s_add_u32 s57,s57,28672
s_sub_u32 s78,s57,143360
s_min_u32 s57,s57,s78
Lg4_gox_%=:
s_add_u32 s58,s58,s61
s_cmp_eq_u32 vcc_hi,-1
s_cbranch_scc1 Lg4_go2_%=
s_add_u32 m0,s59,2048
s_nop 0
buffer_load_dword v5,s[36:39],s58 offen offset:0 sc1 lds
s_waitcnt vmcnt(0)
s_barrier
buffer_store_dword v4,v3,s[36:39],s70 offen offset:0 sc0 sc1
s_min_u32 s71,s71,s72
buffer_store_dword v4,v3,s[36:39],s71 offen offset:0 sc0 sc1
s_mov_b32 s70,0x7fff0000
s_mov_b32 s71,0x7fff0000
s_mov_b32 s72,0x7fff0000
s_cmp_eq_u32 s80,1
s_cbranch_scc1 Lg4_st11_%=
Lg4_st10_%=:
ds_read_b32 v7,v6 offset:2048
R 26,1,0
R 30,1,1024
R 34,1,2048
R 38,1,3072
R 42,1,4096
R 46,1,5120
R 50,1,6144
L 7
v_cmp_eq_u32 vcc,s53,v7
L 5
s_cmp_eq_u64 vcc,-1
s_cbranch_scc1 Lg4_L20_%=
s_cmp_eq_u32 vcc_lo,-1
s_cbranch_scc1 Lg4_L10_%=
s_branch Lg4_X0_%=
Lg4_st11_%=:
ds_read_b32 v7,v6 offset:2048
R 26,1,0
R 30,1,1024
R 34,1,2048
R 38,1,3072
R 42,1,4096
R 46,1,5120
R 50,1,6144
L 7
v_cmp_eq_u32 vcc,s53,v7
L 5
s_cmp_eq_u64 vcc,-1
s_cbranch_scc1 Lg4_L21_%=
s_cmp_eq_u32 vcc_lo,-1
s_cbranch_scc1 Lg4_L11_%=
s_branch Lg4_X1_%=
Lg4_go2_%=:
s_add_u32 s58,s58,s61
s_add_u32 m0,s59,2048
s_nop 0
buffer_load_dword v5,s[36:39],s58 offen offset:0 sc1 lds
s_mov_b32 m0,s57
s_add_u32 s56,s55,4096
D 0,55,0
D 0,55,1024
D 0,55,2048
D 0,55,3072
s_add_u32 m0,s57,4096
s_nop 0
D 0,56,0
D 0,56,1024
D 0,56,2048
s_add_u32 s55,s55,28672
s_add_u32 s57,s57,28672
s_sub_u32 s78,s57,143360
s_min_u32 s57,s57,s78
s_add_u32 s54,s54,s46
s_waitcnt vmcnt(7)
s_barrier
buffer_store_dword v4,v3,s[36:39],s70 offen offset:0 sc0 sc1
s_min_u32 s71,s71,s72
buffer_store_dword v4,v3,s[36:39],s71 offen offset:0 sc0 sc1
s_mov_b32 s70,0x7fff0000
s_mov_b32 s71,0x7fff0000
s_mov_b32 s72,0x7fff0000
s_cmp_eq_u32 s80,1
s_cbranch_scc1 Lg4_st21_%=
Lg4_st20_%=:
ds_read_b32 v7,v6 offset:2048
R 26,1,0
R 30,1,1024
R 34,1,2048
R 38,1,3072
R 42,1,4096
R 46,1,5120
R 50,1,6144
L 7
v_cmp_eq_u32 vcc,s53,v7
L 5
s_cmp_eq_u32 vcc_lo,-1
s_cbranch_scc1 Lg4_S0_%=
s_branch Lg4_N0_%=
Lg4_st21_%=:
ds_read_b32 v7,v6 offset:2048
R 26,1,0
R 30,1,1024
R 34,1,2048
R 38,1,3072
R 42,1,4096
R 46,1,5120
R 50,1,6144
)"
R"(L 7
v_cmp_eq_u32 vcc,s53,v7
L 5
s_cmp_eq_u32 vcc_lo,-1
s_cbranch_scc1 Lg4_S1_%=
s_branch Lg4_N1_%=
Lg4_L20_%=:
v_mfma_f32_16x16x32_bf16 v[10:13],a[0:3],v[26:29],0
s_add_u32 s58,s58,s61
R 54,1,7168
v_lshlrev_b32 v62,16,v58
v_mfma_f32_16x16x32_bf16 v[14:17],a[4:7],v[26:29],0
s_add_u32 s58,s58,s61
s_add_u32 m0,s59,0
v_and_b32 v63,0xffff0000,v58
M 10,a[8:11],30
s_mov_b32 s66,s65
R 26,1,8192
v_add_f32 v18,v18,v62
M 14,a[12:15],30
L 5
buffer_load_dword v8,s[36:39],s58 offen offset:0 sc1 lds
M 10,a[16:19],34
s_mov_b32 m0,s57
R 30,1,9216
v_add_f32 v19,v19,v63
M 14,a[20:23],34
D 0,55,0
M 10,a[24:27],38
R 34,1,10240
v_lshlrev_b32 v62,16,v59
M 14,a[28:31],38
L 5
v_add_f32 v20,v20,v62
v_and_b32 v63,0xffff0000,v59
M 10,a[32:35],42
R 38,1,11264
v_add_f32 v21,v21,v63
M 14,a[36:39],42
D 0,55,1024
M 10,a[40:43],46
R 42,1,12288
v_lshlrev_b32 v62,16,v60
M 14,a[44:47],46
L 5
v_add_f32 v22,v22,v62
v_and_b32 v63,0xffff0000,v60
M 10,a[48:51],50
R 46,1,13312
v_add_f32 v23,v23,v63
M 14,a[52:55],50
D 0,55,2048
M 10,a[56:59],54
R 50,1,14336
v_lshlrev_b32 v62,16,v61
M 14,a[60:63],54
L 5
v_add_f32 v24,v24,v62
v_and_b32 v63,0xffff0000,v61
M 10,a[64:67],26
R 54,1,15360
v_add_f32 v25,v25,v63
M 14,a[68:71],26
buffer_load_dwordx4 v[58:61],v0,s[36:39],s74 offen offset:0 sc1
M 10,a[72:75],30
s_add_u32 s74,s74,s62
R 26,1,16384
v_cvt_pk_bf16_f32 v18,v18,v19
M 14,a[76:79],30
L 5
D 0,55,3072
M 10,a[80:83],34
s_add_u32 s56,s55,4096
R 30,1,17408
v_cvt_pk_bf16_f32 v19,v20,v21
M 14,a[84:87],34
s_add_u32 m0,s57,4096
v_cvt_pk_bf16_f32 v20,v22,v23
v_cvt_pk_bf16_f32 v21,v24,v25
M 10,a[88:91],38
s_add_u32 s55,s55,28672
s_add_u32 s57,s57,28672
R 34,1,18432
M 14,a[92:95],38
s_sub_u32 s78,s57,143360
L 5
buffer_store_dwordx4 v[18:21],v0,s[36:39],s67 offen offset:0 sc0 sc1
M 10,a[96:99],42
s_min_u32 s57,s57,s78
s_add_u32 s54,s54,s46
R 38,1,19456
M 14,a[100:103],42
D 0,56,0
M 10,a[104:107],46
R 42,1,20480
M 14,a[108:111],46
L 5
D 0,56,1024
M 10,a[112:115],50
R 46,1,21504
M 14,a[116:119],50
D 0,56,2048
M 10,a[120:123],54
s_mov_b32 m0,s57
s_add_u32 s56,s55,4096
R 50,1,22528
M 14,a[124:127],54
L 5
M 10,a[128:131],26
R 54,1,23552
M 14,a[132:135],26
D 0,55,0
M 10,a[136:139],30
R 26,1,24576
M 14,a[140:143],30
L 5
M 10,a[144:147],34
R 30,1,25600
M 14,a[148:151],34
D 0,55,1024
M 10,a[152:155],38
R 34,1,26624
M 14,a[156:159],38
L 5
M 10,a[160:163],42
R 38,1,27648
M 14,a[164:167],42
D 0,55,2048
M 10,a[168:171],46
R 42,2,0
M 14,a[172:175],46
L 5
M 10,a[176:179],50
R 46,2,1024
M 14,a[180:183],50
D 0,55,3072
M 10,a[184:187],54
s_add_u32 m0,s57,4096
s_add_u32 s55,s55,s60
R 50,2,2048
M 14,a[188:191],54
s_add_u32 s57,s57,28672
s_sub_u32 s78,s57,143360
L 5
M 10,a[192:195],26
s_min_u32 s57,s57,s78
R 54,2,3072
M 14,a[196:199],26
D 0,56,0
M 10,a[200:203],30
R 26,2,4096
M 14,a[204:207],30
L 5
M 10,a[208:211],34
R 30,2,5120
M 14,a[212:215],34
D 0,56,1024
M 10,a[216:219],38
R 34,2,6144
M 14,a[220:223],38
L 5
M 10,a[224:227],42
R 38,2,7168
M 14,a[228:231],42
D 0,56,2048
M 10,a[232:235],46
s_mov_b32 m0,s57
s_add_u32 s56,s55,4096
R 42,2,8192
M 14,a[236:239],46
L 5
M 10,a[240:243],50
R 46,2,9216
M 14,a[244:247],50
D 0,55,0
M 10,a[248:251],54
R 50,2,10240
M 14,a[252:255],54
L 5
M 10,v[64:67],26
R 54,2,11264
M 14,v[68:71],26
M 10,v[72:75],30
R 26,2,12288
M 14,v[76:79],30
L 5
M 10,v[80:83],34
R 30,2,13312
M 14,v[84:87],34
D 0,55,1024
M 10,v[88:91],38
R 34,2,14336
M 14,v[92:95],38
L 5
M 10,v[96:99],42
R 38,2,15360
M 14,v[100:103],42
M 10,v[104:107],46
R 42,2,16384
M 14,v[108:111],46
L 5
D 0,55,2048
M 10,v[112:115],50
R 46,2,17408
M 14,v[116:119],50
M 10,v[120:123],54
R 50,2,18432
M 14,v[124:127],54
L 5
M 10,v[128:131],26
R 54,2,19456
M 14,v[132:135],26
M 10,v[136:139],30
R 26,2,20480
M 14,v[140:143],30
L 5
D 0,55,3072
M 10,v[144:147],34
s_add_u32 m0,s57,4096
s_add_u32 s55,s55,28672
R 30,2,21504
M 14,v[148:151],34
s_add_u32 s57,s57,28672
s_sub_u32 s78,s57,143360
s_min_u32 s57,s57,s78
M 10,v[152:155],38
s_add_u32 s54,s54,s46
s_add_u32 s65,s65,s62
R 34,2,22528
M 14,v[156:159],38
s_add_u32 s84,s84,28672
s_sub_u32 s79,s84,143360
L 5
M 10,v[160:163],42
s_min_u32 s84,s84,s79
R 38,2,23552
v_add_u32 v1,s84,v0
M 14,v[164:167],42
s_add_u32 s84,s84,28672
D 0,56,0
M 10,v[168:171],46
s_sub_u32 s79,s84,143360
s_min_u32 s84,s84,s79
R 42,2,24576
M 14,v[172:175],46
L 5
M 10,v[176:179],50
R 46,2,25600
M 14,v[180:183],50
M 10,v[184:187],54
R 50,2,26624
M 14,v[188:191],54
L 5
M 10,v[192:195],26
s_waitcnt vmcnt(5)
R 54,2,27648
v_add_u32 v2,s84,v0
M 14,v[196:199],26
s_barrier
ds_read_b32 v7,v6 offset:0
M 10,v[200:203],30
R 26,1,0
M 14,v[204:207],30
L 6
buffer_store_dword v4,v3,s[36:39],s69 offen offset:0 sc0 sc1
M 10,v[208:211],34
s_mov_b32 s69,s68
s_add_u32 s68,s68,s63
R 30,1,1024
M 14,v[212:215],34
D 0,56,1024
M 10,v[216:219],38
L 2
R 34,1,2048
v_cmp_eq_u32 vcc,s53,v7
M 14,v[220:223],38
s_nop 0
D 0,56,2048
M 10,v[224:227],42
s_cmp_eq_u32 vcc_lo,-1
R 38,1,3072
M 14,v[228:231],42
M 10,v[232:235],46
R 42,1,4096
M 14,v[236:239],46
s_nop 0
M 10,v[240:243],50
R 46,1,5120
M 14,v[244:247],50
M 10,v[248:251],54
R 50,1,6144
M 14,v[252:255],54
L 5
s_cbranch_scc0 Lg4_N1_%=
Lg4_S1_%=:
v_mfma_f32_16x16x32_bf16 v[18:21],a[0:3],v[26:29],0
s_add_u32 s58,s58,s61
R 54,1,7168
v_lshlrev_b32 v62,16,v58
v_mfma_f32_16x16x32_bf16 v[22:25],a[4:7],v[26:29],0
s_add_u32 m0,s59,1024
s_mov_b32 s67,s65
v_and_b32 v63,0xffff0000,v58
M 18,a[8:11],30
R 26,1,8192
v_add_f32 v10,v10,v62
M 22,a[12:15],30
L 5
buffer_load_dword v8,s[36:39],s58 offen offset:0 sc1 lds
M 18,a[16:19],34
s_mov_b32 m0,s57
R 30,1,9216
v_add_f32 v11,v11,v63
M 22,a[20:23],34
D 0,55,0
M 18,a[24:27],38
R 34,1,10240
v_lshlrev_b32 v62,16,v59
M 22,a[28:31],38
L 5
v_add_f32 v12,v12,v62
v_and_b32 v63,0xffff0000,v59
M 18,a[32:35],42
R 38,1,11264
v_add_f32 v13,v13,v63
M 22,a[36:39],42
v_lshlrev_b32 v62,16,v60
v_add_f32 v14,v14,v62
M 18,a[40:43],46
R 42,1,12288
v_and_b32 v63,0xffff0000,v60
M 22,a[44:47],46
L 5
v_add_f32 v15,v15,v63
v_lshlrev_b32 v62,16,v61
M 18,a[48:51],50
R 46,1,13312
v_add_f32 v16,v16,v62
M 22,a[52:55],50
D 0,55,1024
M 18,a[56:59],54
R 50,1,14336
v_and_b32 v63,0xffff0000,v61
M 22,a[60:63],54
L 5
buffer_load_dwordx4 v[58:61],v0,s[36:39],s74 offen offset:0 sc1
M 18,a[64:67],26
s_add_u32 s74,s74,s62
R 54,1,15360
v_add_f32 v17,v17,v63
M 22,a[68:71],26
s_add_u32 s56,s55,4096
v_cvt_pk_bf16_f32 v10,v10,v11
v_cvt_pk_bf16_f32 v11,v12,v13
M 18,a[72:75],30
R 26,1,16384
v_cvt_pk_bf16_f32 v12,v14,v15
M 22,a[76:79],30
L 5
v_cvt_pk_bf16_f32 v13,v16,v17
M 18,a[80:83],34
R 30,1,17408
M 22,a[84:87],34
buffer_store_dwordx4 v[10:13],v0,s[36:39],s66 offen offset:0 sc0 sc1
M 18,a[88:91],38
R 34,1,18432
M 22,a[92:95],38
L 5
D 0,55,2048
M 18,a[96:99],42
R 38,1,19456
M 22,a[100:103],42
M 18,a[104:107],46
R 42,1,20480
M 22,a[108:111],46
L 5
M 18,a[112:115],50
R 46,1,21504
M 22,a[116:119],50
D 0,55,3072
M 18,a[120:123],54
s_add_u32 m0,s57,4096
s_add_u32 s55,s55,s60
R 50,1,22528
M 22,a[124:127],54
s_add_u32 s57,s57,28672
s_sub_u32 s78,s57,143360
L 5
M 18,a[128:131],26
s_min_u32 s57,s57,s78
R 54,1,23552
M 22,a[132:135],26
M 18,a[136:139],30
R 26,1,24576
M 22,a[140:143],30
L 5
M 18,a[144:147],34
R 30,1,25600
M 22,a[148:151],34
D 0,56,0
M 18,a[152:155],38
R 34,1,26624
M 22,a[156:159],38
L 5
M 18,a[160:163],42
R 38,1,27648
M 22,a[164:167],42
M 18,a[168:171],46
R 42,2,0
M 22,a[172:175],46
L 5
M 18,a[176:179],50
R 46,2,1024
M 22,a[180:183],50
D 0,56,1024
M 18,a[184:187],54
R 50,2,2048
M 22,a[188:191],54
L 5
M 18,a[192:195],26
R 54,2,3072
M 22,a[196:199],26
M 18,a[200:203],30
R 26,2,4096
M 22,a[204:207],30
L 5
M 18,a[208:211],34
R 30,2,5120
M 22,a[212:215],34
D 0,56,2048
M 18,a[216:219],38
s_mov_b32 m0,s57
s_add_u32 s56,s55,4096
R 34,2,6144
M 22,a[220:223],38
L 5
M 18,a[224:227],42
R 38,2,7168
M 22,a[228:231],42
M 18,a[232:235],46
R 42,2,8192
M 22,a[236:239],46
L 5
M 18,a[240:243],50
R 46,2,9216
M 22,a[244:247],50
D 0,55,0
M 18,a[248:251],54
R 50,2,10240
M 22,a[252:255],54
L 5
M 18,v[64:67],26
R 54,2,11264
M 22,v[68:71],26
M 18,v[72:75],30
R 26,2,12288
M 22,v[76:79],30
L 5
M 18,v[80:83],34
R 30,2,13312
M 22,v[84:87],34
D 0,55,1024
M 18,v[88:91],38
R 34,2,14336
M 22,v[92:95],38
L 5
M 18,v[96:99],42
R 38,2,15360
M 22,v[100:103],42
M 18,v[104:107],46
R 42,2,16384
M 22,v[108:111],46
L 5
D 0,55,2048
M 18,v[112:115],50
R 46,2,17408
M 22,v[116:119],50
M 18,v[120:123],54
R 50,2,18432
M 22,v[124:127],54
L 5
M 18,v[128:131],26
R 54,2,19456
M 22,v[132:135],26
M 18,v[136:139],30
R 26,2,20480
M 22,v[140:143],30
L 5
D 0,55,3072
M 18,v[144:147],34
s_add_u32 m0,s57,4096
s_add_u32 s55,s55,28672
R 30,2,21504
M 22,v[148:151],34
s_add_u32 s57,s57,28672
s_sub_u32 s78,s57,143360
s_min_u32 s57,s57,s78
M 18,v[152:155],38
s_add_u32 s54,s54,s46
s_add_u32 s65,s65,s62
)"
R"(R 34,2,22528
M 22,v[156:159],38
s_add_u32 s84,s84,28672
s_sub_u32 s79,s84,143360
L 5
M 18,v[160:163],42
s_min_u32 s84,s84,s79
R 38,2,23552
v_add_u32 v1,s84,v0
M 22,v[164:167],42
s_add_u32 s84,s84,28672
D 0,56,0
M 18,v[168:171],46
s_sub_u32 s79,s84,143360
s_min_u32 s84,s84,s79
R 42,2,24576
M 22,v[172:175],46
L 5
M 18,v[176:179],50
R 46,2,25600
M 22,v[180:183],50
M 18,v[184:187],54
R 50,2,26624
M 22,v[188:191],54
L 5
M 18,v[192:195],26
s_waitcnt vmcnt(5)
R 54,2,27648
v_add_u32 v2,s84,v0
M 22,v[196:199],26
s_barrier
ds_read_b32 v7,v6 offset:1024
M 18,v[200:203],30
R 26,1,0
M 22,v[204:207],30
L 6
buffer_store_dword v4,v3,s[36:39],s69 offen offset:0 sc0 sc1
M 18,v[208:211],34
s_mov_b32 s69,s68
s_add_u32 s68,s68,s63
R 30,1,1024
M 22,v[212:215],34
D 0,56,1024
M 18,v[216:219],38
L 2
R 34,1,2048
v_cmp_eq_u32 vcc,s53,v7
M 22,v[220:223],38
s_nop 0
D 0,56,2048
M 18,v[224:227],42
s_cmp_eq_u32 vcc_lo,-1
R 38,1,3072
M 22,v[228:231],42
M 18,v[232:235],46
R 42,1,4096
M 22,v[236:239],46
s_nop 0
M 18,v[240:243],50
R 46,1,5120
M 22,v[244:247],50
M 18,v[248:251],54
R 50,1,6144
M 22,v[252:255],54
L 5
s_cbranch_scc0 Lg4_N0_%=
Lg4_S0_%=:
v_mfma_f32_16x16x32_bf16 v[10:13],a[0:3],v[26:29],0
s_add_u32 s58,s58,s61
R 54,1,7168
v_lshlrev_b32 v62,16,v58
v_mfma_f32_16x16x32_bf16 v[14:17],a[4:7],v[26:29],0
s_add_u32 m0,s59,0
s_mov_b32 s66,s65
v_and_b32 v63,0xffff0000,v58
M 10,a[8:11],30
R 26,1,8192
v_add_f32 v18,v18,v62
M 14,a[12:15],30
L 5
buffer_load_dword v8,s[36:39],s58 offen offset:0 sc1 lds
M 10,a[16:19],34
s_mov_b32 m0,s57
R 30,1,9216
v_add_f32 v19,v19,v63
M 14,a[20:23],34
D 0,55,0
M 10,a[24:27],38
R 34,1,10240
v_lshlrev_b32 v62,16,v59
M 14,a[28:31],38
L 5
v_add_f32 v20,v20,v62
v_and_b32 v63,0xffff0000,v59
M 10,a[32:35],42
R 38,1,11264
v_add_f32 v21,v21,v63
M 14,a[36:39],42
v_lshlrev_b32 v62,16,v60
v_add_f32 v22,v22,v62
M 10,a[40:43],46
R 42,1,12288
v_and_b32 v63,0xffff0000,v60
M 14,a[44:47],46
L 5
v_add_f32 v23,v23,v63
v_lshlrev_b32 v62,16,v61
M 10,a[48:51],50
R 46,1,13312
v_add_f32 v24,v24,v62
M 14,a[52:55],50
D 0,55,1024
M 10,a[56:59],54
R 50,1,14336
v_and_b32 v63,0xffff0000,v61
M 14,a[60:63],54
L 5
buffer_load_dwordx4 v[58:61],v0,s[36:39],s74 offen offset:0 sc1
M 10,a[64:67],26
s_add_u32 s74,s74,s62
R 54,1,15360
v_add_f32 v25,v25,v63
M 14,a[68:71],26
s_add_u32 s56,s55,4096
v_cvt_pk_bf16_f32 v18,v18,v19
v_cvt_pk_bf16_f32 v19,v20,v21
M 10,a[72:75],30
R 26,1,16384
v_cvt_pk_bf16_f32 v20,v22,v23
M 14,a[76:79],30
L 5
v_cvt_pk_bf16_f32 v21,v24,v25
M 10,a[80:83],34
R 30,1,17408
M 14,a[84:87],34
buffer_store_dwordx4 v[18:21],v0,s[36:39],s67 offen offset:0 sc0 sc1
M 10,a[88:91],38
R 34,1,18432
M 14,a[92:95],38
L 5
D 0,55,2048
M 10,a[96:99],42
R 38,1,19456
M 14,a[100:103],42
M 10,a[104:107],46
R 42,1,20480
M 14,a[108:111],46
L 5
M 10,a[112:115],50
R 46,1,21504
M 14,a[116:119],50
D 0,55,3072
M 10,a[120:123],54
s_add_u32 m0,s57,4096
s_add_u32 s55,s55,s60
R 50,1,22528
M 14,a[124:127],54
s_add_u32 s57,s57,28672
s_sub_u32 s78,s57,143360
L 5
M 10,a[128:131],26
s_min_u32 s57,s57,s78
R 54,1,23552
M 14,a[132:135],26
M 10,a[136:139],30
R 26,1,24576
M 14,a[140:143],30
L 5
M 10,a[144:147],34
R 30,1,25600
M 14,a[148:151],34
D 0,56,0
M 10,a[152:155],38
R 34,1,26624
M 14,a[156:159],38
L 5
M 10,a[160:163],42
R 38,1,27648
M 14,a[164:167],42
M 10,a[168:171],46
R 42,2,0
M 14,a[172:175],46
L 5
M 10,a[176:179],50
R 46,2,1024
M 14,a[180:183],50
D 0,56,1024
M 10,a[184:187],54
R 50,2,2048
M 14,a[188:191],54
L 5
M 10,a[192:195],26
R 54,2,3072
M 14,a[196:199],26
M 10,a[200:203],30
R 26,2,4096
M 14,a[204:207],30
L 5
M 10,a[208:211],34
R 30,2,5120
M 14,a[212:215],34
D 0,56,2048
M 10,a[216:219],38
s_mov_b32 m0,s57
s_add_u32 s56,s55,4096
R 34,2,6144
M 14,a[220:223],38
L 5
M 10,a[224:227],42
R 38,2,7168
M 14,a[228:231],42
M 10,a[232:235],46
R 42,2,8192
M 14,a[236:239],46
L 5
M 10,a[240:243],50
R 46,2,9216
M 14,a[244:247],50
D 0,55,0
M 10,a[248:251],54
R 50,2,10240
M 14,a[252:255],54
L 5
M 10,v[64:67],26
R 54,2,11264
M 14,v[68:71],26
M 10,v[72:75],30
R 26,2,12288
M 14,v[76:79],30
L 5
M 10,v[80:83],34
R 30,2,13312
M 14,v[84:87],34
D 0,55,1024
M 10,v[88:91],38
R 34,2,14336
M 14,v[92:95],38
L 5
M 10,v[96:99],42
R 38,2,15360
M 14,v[100:103],42
M 10,v[104:107],46
R 42,2,16384
M 14,v[108:111],46
L 5
D 0,55,2048
M 10,v[112:115],50
R 46,2,17408
M 14,v[116:119],50
M 10,v[120:123],54
R 50,2,18432
M 14,v[124:127],54
L 5
M 10,v[128:131],26
R 54,2,19456
M 14,v[132:135],26
M 10,v[136:139],30
R 26,2,20480
M 14,v[140:143],30
L 5
D 0,55,3072
M 10,v[144:147],34
s_add_u32 m0,s57,4096
s_add_u32 s55,s55,28672
R 30,2,21504
M 14,v[148:151],34
s_add_u32 s57,s57,28672
s_sub_u32 s78,s57,143360
s_min_u32 s57,s57,s78
M 10,v[152:155],38
s_add_u32 s54,s54,s46
s_add_u32 s65,s65,s62
R 34,2,22528
M 14,v[156:159],38
s_add_u32 s84,s84,28672
s_sub_u32 s79,s84,143360
L 5
M 10,v[160:163],42
s_min_u32 s84,s84,s79
R 38,2,23552
v_add_u32 v1,s84,v0
M 14,v[164:167],42
s_add_u32 s84,s84,28672
D 0,56,0
M 10,v[168:171],46
s_sub_u32 s79,s84,143360
s_min_u32 s84,s84,s79
R 42,2,24576
M 14,v[172:175],46
L 5
M 10,v[176:179],50
R 46,2,25600
M 14,v[180:183],50
M 10,v[184:187],54
R 50,2,26624
M 14,v[188:191],54
L 5
M 10,v[192:195],26
s_waitcnt vmcnt(5)
R 54,2,27648
v_add_u32 v2,s84,v0
M 14,v[196:199],26
s_barrier
ds_read_b32 v7,v6 offset:0
M 10,v[200:203],30
R 26,1,0
M 14,v[204:207],30
L 6
buffer_store_dword v4,v3,s[36:39],s69 offen offset:0 sc0 sc1
M 10,v[208:211],34
s_mov_b32 s69,s68
s_add_u32 s68,s68,s63
R 30,1,1024
M 14,v[212:215],34
D 0,56,1024
M 10,v[216:219],38
L 2
R 34,1,2048
v_cmp_eq_u32 vcc,s53,v7
M 14,v[220:223],38
s_nop 0
D 0,56,2048
M 10,v[224:227],42
s_cmp_eq_u32 vcc_lo,-1
R 38,1,3072
M 14,v[228:231],42
M 10,v[232:235],46
R 42,1,4096
M 14,v[236:239],46
s_nop 0
M 10,v[240:243],50
R 46,1,5120
M 14,v[244:247],50
M 10,v[248:251],54
R 50,1,6144
M 14,v[252:255],54
L 5
s_cbranch_scc1 Lg4_S1_%=
Lg4_N1_%=:
v_mfma_f32_16x16x32_bf16 v[18:21],a[0:3],v[26:29],0
s_add_u32 m0,s59,1024
R 54,1,7168
v_lshlrev_b32 v62,16,v58
v_mfma_f32_16x16x32_bf16 v[22:25],a[4:7],v[26:29],0
s_mov_b32 s67,s65
buffer_load_dword v5,s[36:39],s58 offen offset:0 sc1 lds
M 18,a[8:11],30
s_mov_b32 m0,s57
R 26,1,8192
v_add_f32 v10,v10,v62
M 22,a[12:15],30
L 5
v_and_b32 v63,0xffff0000,v58
v_add_f32 v11,v11,v63
M 18,a[16:19],34
R 30,1,9216
v_lshlrev_b32 v62,16,v59
M 22,a[20:23],34
D 0,55,0
M 18,a[24:27],38
R 34,1,10240
v_add_f32 v12,v12,v62
M 22,a[28:31],38
L 5
v_and_b32 v63,0xffff0000,v59
v_add_f32 v13,v13,v63
M 18,a[32:35],42
R 38,1,11264
v_lshlrev_b32 v62,16,v60
M 22,a[36:39],42
v_add_f32 v14,v14,v62
v_and_b32 v63,0xffff0000,v60
M 18,a[40:43],46
R 42,1,12288
v_add_f32 v15,v15,v63
M 22,a[44:47],46
L 5
v_lshlrev_b32 v62,16,v61
v_add_f32 v16,v16,v62
M 18,a[48:51],50
R 46,1,13312
v_and_b32 v63,0xffff0000,v61
M 22,a[52:55],50
buffer_load_dwordx4 v[58:61],v0,s[36:39],s74 offen offset:0 sc1
M 18,a[56:59],54
s_add_u32 s74,s74,s62
R 50,1,14336
v_add_f32 v17,v17,v63
M 22,a[60:63],54
L 5
D 0,55,1024
M 18,a[64:67],26
s_add_u32 s56,s55,4096
R 54,1,15360
v_cvt_pk_bf16_f32 v10,v10,v11
M 22,a[68:71],26
v_cvt_pk_bf16_f32 v11,v12,v13
v_cvt_pk_bf16_f32 v12,v14,v15
M 18,a[72:75],30
R 26,1,16384
v_cvt_pk_bf16_f32 v13,v16,v17
M 22,a[76:79],30
L 5
buffer_store_dwordx4 v[10:13],v0,s[36:39],s66 offen offset:0 sc0 sc1
M 18,a[80:83],34
R 30,1,17408
M 22,a[84:87],34
D 0,55,2048
M 18,a[88:91],38
R 34,1,18432
M 22,a[92:95],38
L 5
M 18,a[96:99],42
R 38,1,19456
M 22,a[100:103],42
M 18,a[104:107],46
R 42,1,20480
M 22,a[108:111],46
L 5
M 18,a[112:115],50
R 46,1,21504
M 22,a[116:119],50
D 0,55,3072
M 18,a[120:123],54
s_add_u32 m0,s57,4096
s_add_u32 s55,s55,s60
R 50,1,22528
M 22,a[124:127],54
s_add_u32 s57,s57,28672
s_sub_u32 s78,s57,143360
L 5
M 18,a[128:131],26
s_min_u32 s57,s57,s78
s_add_u32 s65,s65,s62
R 54,1,23552
M 22,a[132:135],26
s_add_u32 s84,s84,28672
s_sub_u32 s79,s84,143360
s_min_u32 s84,s84,s79
M 18,a[136:139],30
R 26,1,24576
M 22,a[140:143],30
L 5
M 18,a[144:147],34
R 30,1,25600
M 22,a[148:151],34
D 0,56,0
M 18,a[152:155],38
R 34,1,26624
M 22,a[156:159],38
L 5
M 18,a[160:163],42
R 38,1,27648
v_add_u32 v1,s84,v0
M 22,a[164:167],42
s_add_u32 s84,s84,28672
s_sub_u32 s79,s84,143360
s_min_u32 s84,s84,s79
M 18,a[168:171],46
R 42,2,0
M 22,a[172:175],46
L 5
M 18,a[176:179],50
R 46,2,1024
M 22,a[180:183],50
D 0,56,1024
M 18,a[184:187],54
R 50,2,2048
M 22,a[188:191],54
L 5
M 18,a[192:195],26
R 54,2,3072
M 22,a[196:199],26
M 18,a[200:203],30
R 26,2,4096
M 22,a[204:207],30
L 5
M 18,a[208:211],34
R 30,2,5120
M 22,a[212:215],34
D 0,56,2048
M 18,a[216:219],38
R 34,2,6144
M 22,a[220:223],38
L 5
M 18,a[224:227],42
R 38,2,7168
M 22,a[228:231],42
M 18,a[232:235],46
R 42,2,8192
M 22,a[236:239],46
L 5
M 18,a[240:243],50
R 46,2,9216
M 22,a[244:247],50
M 18,a[248:251],54
R 50,2,10240
M 22,a[252:255],54
L 5
M 18,v[64:67],26
)"
R"(R 54,2,11264
M 22,v[68:71],26
M 18,v[72:75],30
R 26,2,12288
M 22,v[76:79],30
L 5
M 18,v[80:83],34
R 30,2,13312
M 22,v[84:87],34
M 18,v[88:91],38
R 34,2,14336
M 22,v[92:95],38
L 5
M 18,v[96:99],42
R 38,2,15360
M 22,v[100:103],42
M 18,v[104:107],46
R 42,2,16384
M 22,v[108:111],46
L 5
M 18,v[112:115],50
R 46,2,17408
M 22,v[116:119],50
M 18,v[120:123],54
R 50,2,18432
M 22,v[124:127],54
L 5
M 18,v[128:131],26
R 54,2,19456
M 22,v[132:135],26
M 18,v[136:139],30
R 26,2,20480
M 22,v[140:143],30
L 5
M 18,v[144:147],34
R 30,2,21504
M 22,v[148:151],34
M 18,v[152:155],38
R 34,2,22528
M 22,v[156:159],38
L 5
M 18,v[160:163],42
R 38,2,23552
M 22,v[164:167],42
M 18,v[168:171],46
R 42,2,24576
M 22,v[172:175],46
L 5
M 18,v[176:179],50
R 46,2,25600
M 22,v[180:183],50
M 18,v[184:187],54
R 50,2,26624
M 22,v[188:191],54
L 5
M 18,v[192:195],26
s_waitcnt vmcnt(0)
R 54,2,27648
v_add_u32 v2,s84,v0
M 22,v[196:199],26
s_barrier
ds_read_b32 v7,v6 offset:1024
M 18,v[200:203],30
R 26,1,0
M 22,v[204:207],30
L 6
buffer_store_dword v4,v3,s[36:39],s69 offen offset:0 sc0 sc1
M 18,v[208:211],34
s_mov_b32 s69,s68
s_add_u32 s68,s68,s63
R 30,1,1024
M 22,v[212:215],34
M 18,v[216:219],38
L 2
R 34,1,2048
v_cmp_eq_u32 vcc,s53,v7
M 22,v[220:223],38
s_nop 0
s_cmp_eq_u64 vcc,-1
M 18,v[224:227],42
R 38,1,3072
M 22,v[228:231],42
M 18,v[232:235],46
R 42,1,4096
M 22,v[236:239],46
s_nop 0
M 18,v[240:243],50
R 46,1,5120
M 22,v[244:247],50
M 18,v[248:251],54
R 50,1,6144
M 22,v[252:255],54
L 5
s_cbranch_scc1 Lg4_L20_%=
s_cmp_eq_u32 vcc_lo,-1
s_cbranch_scc1 Lg4_L10_%=
Lg4_X0_%=:
v_mfma_f32_16x16x32_bf16 v[10:13],a[0:3],v[26:29],0
s_add_u32 m0,s59,0
R 54,1,7168
v_lshlrev_b32 v62,16,v58
v_mfma_f32_16x16x32_bf16 v[14:17],a[4:7],v[26:29],0
s_mov_b32 s66,s65
buffer_load_dword v5,s[36:39],s58 offen offset:0 sc1 lds
M 10,a[8:11],30
R 26,1,8192
v_add_f32 v18,v18,v62
M 14,a[12:15],30
L 5
v_and_b32 v63,0xffff0000,v58
v_add_f32 v19,v19,v63
M 10,a[16:19],34
R 30,1,9216
v_lshlrev_b32 v62,16,v59
M 14,a[20:23],34
v_add_f32 v20,v20,v62
v_and_b32 v63,0xffff0000,v59
M 10,a[24:27],38
R 34,1,10240
v_add_f32 v21,v21,v63
M 14,a[28:31],38
L 5
v_lshlrev_b32 v62,16,v60
v_add_f32 v22,v22,v62
M 10,a[32:35],42
R 38,1,11264
v_and_b32 v63,0xffff0000,v60
M 14,a[36:39],42
v_add_f32 v23,v23,v63
v_lshlrev_b32 v62,16,v61
M 10,a[40:43],46
R 42,1,12288
v_add_f32 v24,v24,v62
M 14,a[44:47],46
L 5
v_and_b32 v63,0xffff0000,v61
v_add_f32 v25,v25,v63
M 10,a[48:51],50
R 46,1,13312
v_cvt_pk_bf16_f32 v18,v18,v19
M 14,a[52:55],50
buffer_load_dwordx4 v[58:61],v0,s[36:39],s74 offen offset:0 sc1
M 10,a[56:59],54
s_add_u32 s74,s74,s62
R 50,1,14336
v_cvt_pk_bf16_f32 v19,v20,v21
M 14,a[60:63],54
L 5
v_cvt_pk_bf16_f32 v20,v22,v23
v_cvt_pk_bf16_f32 v21,v24,v25
M 10,a[64:67],26
s_add_u32 s65,s65,s62
s_add_u32 s84,s84,28672
R 54,1,15360
M 14,a[68:71],26
s_sub_u32 s79,s84,143360
s_min_u32 s84,s84,s79
buffer_store_dwordx4 v[18:21],v0,s[36:39],s67 offen offset:0 sc0 sc1
M 10,a[72:75],30
R 26,1,16384
M 14,a[76:79],30
L 5
M 10,a[80:83],34
R 30,1,17408
M 14,a[84:87],34
M 10,a[88:91],38
R 34,1,18432
M 14,a[92:95],38
L 5
M 10,a[96:99],42
R 38,1,19456
M 14,a[100:103],42
M 10,a[104:107],46
R 42,1,20480
M 14,a[108:111],46
L 5
M 10,a[112:115],50
R 46,1,21504
M 14,a[116:119],50
M 10,a[120:123],54
R 50,1,22528
M 14,a[124:127],54
L 5
M 10,a[128:131],26
R 54,1,23552
M 14,a[132:135],26
M 10,a[136:139],30
R 26,1,24576
M 14,a[140:143],30
L 5
M 10,a[144:147],34
R 30,1,25600
M 14,a[148:151],34
M 10,a[152:155],38
R 34,1,26624
M 14,a[156:159],38
L 5
M 10,a[160:163],42
R 38,1,27648
v_add_u32 v1,s84,v0
M 14,a[164:167],42
s_add_u32 s84,s84,28672
s_sub_u32 s79,s84,143360
s_min_u32 s84,s84,s79
M 10,a[168:171],46
R 42,2,0
M 14,a[172:175],46
L 5
M 10,a[176:179],50
R 46,2,1024
M 14,a[180:183],50
M 10,a[184:187],54
R 50,2,2048
M 14,a[188:191],54
L 5
M 10,a[192:195],26
R 54,2,3072
M 14,a[196:199],26
M 10,a[200:203],30
R 26,2,4096
M 14,a[204:207],30
L 5
M 10,a[208:211],34
R 30,2,5120
M 14,a[212:215],34
M 10,a[216:219],38
R 34,2,6144
M 14,a[220:223],38
L 5
M 10,a[224:227],42
R 38,2,7168
M 14,a[228:231],42
M 10,a[232:235],46
R 42,2,8192
M 14,a[236:239],46
L 5
M 10,a[240:243],50
R 46,2,9216
M 14,a[244:247],50
M 10,a[248:251],54
R 50,2,10240
M 14,a[252:255],54
L 5
M 10,v[64:67],26
R 54,2,11264
M 14,v[68:71],26
M 10,v[72:75],30
R 26,2,12288
M 14,v[76:79],30
L 5
M 10,v[80:83],34
R 30,2,13312
M 14,v[84:87],34
M 10,v[88:91],38
R 34,2,14336
M 14,v[92:95],38
L 5
M 10,v[96:99],42
R 38,2,15360
M 14,v[100:103],42
M 10,v[104:107],46
R 42,2,16384
M 14,v[108:111],46
L 5
M 10,v[112:115],50
R 46,2,17408
M 14,v[116:119],50
M 10,v[120:123],54
R 50,2,18432
M 14,v[124:127],54
L 5
M 10,v[128:131],26
R 54,2,19456
M 14,v[132:135],26
M 10,v[136:139],30
R 26,2,20480
M 14,v[140:143],30
L 5
M 10,v[144:147],34
R 30,2,21504
M 14,v[148:151],34
M 10,v[152:155],38
R 34,2,22528
M 14,v[156:159],38
L 5
M 10,v[160:163],42
R 38,2,23552
M 14,v[164:167],42
M 10,v[168:171],46
R 42,2,24576
M 14,v[172:175],46
L 5
M 10,v[176:179],50
R 46,2,25600
M 14,v[180:183],50
M 10,v[184:187],54
R 50,2,26624
M 14,v[188:191],54
L 5
M 10,v[192:195],26
s_waitcnt vmcnt(0)
R 54,2,27648
v_add_u32 v2,s84,v0
M 14,v[196:199],26
s_barrier
ds_read_b32 v7,v6 offset:0
M 10,v[200:203],30
buffer_store_dword v4,v3,s[36:39],s69 offen offset:0 sc0 sc1
M 14,v[204:207],30
L 5
s_mov_b32 s69,s68
s_add_u32 s68,s68,s63
M 10,v[208:211],34
M 14,v[212:215],34
M 10,v[216:219],38
L 0
v_cmp_eq_u32 vcc,s53,v7
M 14,v[220:223],38
s_nop 0
M 10,v[224:227],42
M 14,v[228:231],42
M 10,v[232:235],46
M 14,v[236:239],46
s_nop 0
M 10,v[240:243],50
M 14,v[244:247],50
M 10,v[248:251],54
M 14,v[252:255],54
s_mov_b32 s80,1
s_cmp_eq_u32 vcc_lo,-1
s_cbranch_scc0 Lg4_xn0_%=
s_mov_b32 m0,s57
s_add_u32 s56,s55,4096
D 0,55,0
v_lshlrev_b32 v62,16,v58
D 0,55,1024
v_add_f32 v10,v10,v62
D 0,55,2048
v_and_b32 v63,0xffff0000,v58
D 0,55,3072
v_add_f32 v11,v11,v63
s_add_u32 m0,s57,4096
s_nop 0
D 0,56,0
v_lshlrev_b32 v62,16,v59
D 0,56,1024
v_add_f32 v12,v12,v62
D 0,56,2048
v_and_b32 v63,0xffff0000,v59
s_add_u32 s55,s55,28672
s_add_u32 s57,s57,28672
s_sub_u32 s78,s57,143360
s_min_u32 s57,s57,s78
s_add_u32 s54,s54,s46
s_mov_b32 m0,s57
s_add_u32 s56,s55,4096
D 0,55,0
v_add_f32 v13,v13,v63
D 0,55,1024
v_lshlrev_b32 v62,16,v60
D 0,55,2048
v_add_f32 v14,v14,v62
D 0,55,3072
v_and_b32 v63,0xffff0000,v60
s_add_u32 m0,s57,4096
s_nop 0
D 0,56,0
v_add_f32 v15,v15,v63
D 0,56,1024
v_lshlrev_b32 v62,16,v61
D 0,56,2048
v_add_f32 v16,v16,v62
s_add_u32 s55,s55,s60
s_add_u32 s57,s57,28672
s_sub_u32 s78,s57,143360
s_min_u32 s57,s57,s78
v_and_b32 v63,0xffff0000,v61
v_add_f32 v17,v17,v63
v_cvt_pk_bf16_f32 v10,v10,v11
v_cvt_pk_bf16_f32 v11,v12,v13
v_cvt_pk_bf16_f32 v12,v14,v15
v_cvt_pk_bf16_f32 v13,v16,v17
buffer_store_dwordx4 v[10:13],v0,s[36:39],s66 offen offset:0 sc0 sc1
s_mov_b32 s72,s69
s_mov_b32 s69,0x7fff0000
s_mov_b32 s66,0x7fff0000
s_mov_b32 s76,0x7fff0000
s_branch Lg4_gox_%=
Lg4_xn0_%=:
s_cmp_ge_u32 s54,256
s_cbranch_scc1 Lg4_fin0_%=
s_add_u32 m0,s59,4096
s_nop 0
buffer_load_dword v5,s[36:39],s58 offen offset:0 sc1 lds
buffer_store_dword v4,v3,s[36:39],s71 offen offset:0 sc0 sc1
s_add_u32 m0,s59,5120
s_nop 0
buffer_load_dword v5,s[36:39],s58 offen offset:0 sc1 lds
buffer_store_dword v4,v3,s[36:39],s71 offen offset:0 sc0 sc1
s_mov_b32 s83,0
v_lshlrev_b32 v62,16,v58
v_add_f32 v10,v10,v62
v_and_b32 v63,0xffff0000,v58
v_add_f32 v11,v11,v63
v_lshlrev_b32 v62,16,v59
v_add_f32 v12,v12,v62
v_and_b32 v63,0xffff0000,v59
v_add_f32 v13,v13,v63
v_lshlrev_b32 v62,16,v60
v_add_f32 v14,v14,v62
v_and_b32 v63,0xffff0000,v60
v_add_f32 v15,v15,v63
v_lshlrev_b32 v62,16,v61
v_add_f32 v16,v16,v62
v_and_b32 v63,0xffff0000,v61
v_add_f32 v17,v17,v63
v_cvt_pk_bf16_f32 v10,v10,v11
v_cvt_pk_bf16_f32 v11,v12,v13
v_cvt_pk_bf16_f32 v12,v14,v15
v_cvt_pk_bf16_f32 v13,v16,v17
buffer_store_dwordx4 v[10:13],v0,s[36:39],s66 offen offset:0 sc0 sc1
s_mov_b32 s72,s69
s_mov_b32 s69,0x7fff0000
s_mov_b32 s66,0x7fff0000
s_mov_b32 s76,0x7fff0000
s_branch Lg4_poll_%=
Lg4_fin0_%=:
v_lshlrev_b32 v62,16,v58
v_add_f32 v10,v10,v62
v_and_b32 v63,0xffff0000,v58
v_add_f32 v11,v11,v63
v_lshlrev_b32 v62,16,v59
v_add_f32 v12,v12,v62
v_and_b32 v63,0xffff0000,v59
v_add_f32 v13,v13,v63
v_lshlrev_b32 v62,16,v60
v_add_f32 v14,v14,v62
v_and_b32 v63,0xffff0000,v60
v_add_f32 v15,v15,v63
v_lshlrev_b32 v62,16,v61
v_add_f32 v16,v16,v62
v_and_b32 v63,0xffff0000,v61
v_add_f32 v17,v17,v63
v_cvt_pk_bf16_f32 v10,v10,v11
v_cvt_pk_bf16_f32 v11,v12,v13
v_cvt_pk_bf16_f32 v12,v14,v15
v_cvt_pk_bf16_f32 v13,v16,v17
buffer_store_dwordx4 v[10:13],v0,s[36:39],s66 offen offset:0 sc0 sc1
s_mov_b32 s72,s69
s_mov_b32 s69,0x7fff0000
s_mov_b32 s66,0x7fff0000
s_mov_b32 s76,0x7fff0000
s_branch Lg4_done_%=
Lg4_L21_%=:
v_mfma_f32_16x16x32_bf16 v[18:21],a[0:3],v[26:29],0
s_add_u32 s58,s58,s61
R 54,1,7168
v_lshlrev_b32 v62,16,v58
v_mfma_f32_16x16x32_bf16 v[22:25],a[4:7],v[26:29],0
s_add_u32 s58,s58,s61
s_add_u32 m0,s59,1024
v_and_b32 v63,0xffff0000,v58
M 18,a[8:11],30
s_mov_b32 s67,s65
R 26,1,8192
v_add_f32 v10,v10,v62
M 22,a[12:15],30
L 5
buffer_load_dword v8,s[36:39],s58 offen offset:0 sc1 lds
M 18,a[16:19],34
s_mov_b32 m0,s57
R 30,1,9216
v_add_f32 v11,v11,v63
M 22,a[20:23],34
D 0,55,0
M 18,a[24:27],38
R 34,1,10240
v_lshlrev_b32 v62,16,v59
M 22,a[28:31],38
L 5
v_add_f32 v12,v12,v62
v_and_b32 v63,0xffff0000,v59
M 18,a[32:35],42
R 38,1,11264
v_add_f32 v13,v13,v63
M 22,a[36:39],42
D 0,55,1024
M 18,a[40:43],46
R 42,1,12288
v_lshlrev_b32 v62,16,v60
M 22,a[44:47],46
L 5
v_add_f32 v14,v14,v62
v_and_b32 v63,0xffff0000,v60
M 18,a[48:51],50
R 46,1,13312
v_add_f32 v15,v15,v63
M 22,a[52:55],50
D 0,55,2048
M 18,a[56:59],54
)"
R"(R 50,1,14336
v_lshlrev_b32 v62,16,v61
M 22,a[60:63],54
L 5
v_add_f32 v16,v16,v62
v_and_b32 v63,0xffff0000,v61
M 18,a[64:67],26
R 54,1,15360
v_add_f32 v17,v17,v63
M 22,a[68:71],26
buffer_load_dwordx4 v[58:61],v0,s[36:39],s74 offen offset:0 sc1
M 18,a[72:75],30
s_add_u32 s74,s74,s62
R 26,1,16384
v_cvt_pk_bf16_f32 v10,v10,v11
M 22,a[76:79],30
L 5
D 0,55,3072
M 18,a[80:83],34
s_add_u32 s56,s55,4096
R 30,1,17408
v_cvt_pk_bf16_f32 v11,v12,v13
M 22,a[84:87],34
s_add_u32 m0,s57,4096
v_cvt_pk_bf16_f32 v12,v14,v15
v_cvt_pk_bf16_f32 v13,v16,v17
M 18,a[88:91],38
s_add_u32 s55,s55,28672
s_add_u32 s57,s57,28672
R 34,1,18432
M 22,a[92:95],38
s_sub_u32 s78,s57,143360
L 5
buffer_store_dwordx4 v[10:13],v0,s[36:39],s66 offen offset:0 sc0 sc1
M 18,a[96:99],42
s_min_u32 s57,s57,s78
s_add_u32 s54,s54,s46
R 38,1,19456
M 22,a[100:103],42
D 0,56,0
M 18,a[104:107],46
R 42,1,20480
M 22,a[108:111],46
L 5
D 0,56,1024
M 18,a[112:115],50
R 46,1,21504
M 22,a[116:119],50
D 0,56,2048
M 18,a[120:123],54
s_mov_b32 m0,s57
s_add_u32 s56,s55,4096
R 50,1,22528
M 22,a[124:127],54
L 5
M 18,a[128:131],26
R 54,1,23552
M 22,a[132:135],26
D 0,55,0
M 18,a[136:139],30
R 26,1,24576
M 22,a[140:143],30
L 5
M 18,a[144:147],34
R 30,1,25600
M 22,a[148:151],34
D 0,55,1024
M 18,a[152:155],38
R 34,1,26624
M 22,a[156:159],38
L 5
M 18,a[160:163],42
R 38,1,27648
M 22,a[164:167],42
D 0,55,2048
M 18,a[168:171],46
R 42,2,0
M 22,a[172:175],46
L 5
M 18,a[176:179],50
R 46,2,1024
M 22,a[180:183],50
D 0,55,3072
M 18,a[184:187],54
s_add_u32 m0,s57,4096
s_add_u32 s55,s55,s60
R 50,2,2048
M 22,a[188:191],54
s_add_u32 s57,s57,28672
s_sub_u32 s78,s57,143360
L 5
M 18,a[192:195],26
s_min_u32 s57,s57,s78
R 54,2,3072
M 22,a[196:199],26
D 0,56,0
M 18,a[200:203],30
R 26,2,4096
M 22,a[204:207],30
L 5
M 18,a[208:211],34
R 30,2,5120
M 22,a[212:215],34
D 0,56,1024
M 18,a[216:219],38
R 34,2,6144
M 22,a[220:223],38
L 5
M 18,a[224:227],42
R 38,2,7168
M 22,a[228:231],42
D 0,56,2048
M 18,a[232:235],46
s_mov_b32 m0,s57
s_add_u32 s56,s55,4096
R 42,2,8192
M 22,a[236:239],46
L 5
M 18,a[240:243],50
R 46,2,9216
M 22,a[244:247],50
D 0,55,0
M 18,a[248:251],54
R 50,2,10240
M 22,a[252:255],54
L 5
M 18,v[64:67],26
R 54,2,11264
M 22,v[68:71],26
M 18,v[72:75],30
R 26,2,12288
M 22,v[76:79],30
L 5
M 18,v[80:83],34
R 30,2,13312
M 22,v[84:87],34
D 0,55,1024
M 18,v[88:91],38
R 34,2,14336
M 22,v[92:95],38
L 5
M 18,v[96:99],42
R 38,2,15360
M 22,v[100:103],42
M 18,v[104:107],46
R 42,2,16384
M 22,v[108:111],46
L 5
D 0,55,2048
M 18,v[112:115],50
R 46,2,17408
M 22,v[116:119],50
M 18,v[120:123],54
R 50,2,18432
M 22,v[124:127],54
L 5
M 18,v[128:131],26
R 54,2,19456
M 22,v[132:135],26
M 18,v[136:139],30
R 26,2,20480
M 22,v[140:143],30
L 5
D 0,55,3072
M 18,v[144:147],34
s_add_u32 m0,s57,4096
s_add_u32 s55,s55,28672
R 30,2,21504
M 22,v[148:151],34
s_add_u32 s57,s57,28672
s_sub_u32 s78,s57,143360
s_min_u32 s57,s57,s78
M 18,v[152:155],38
s_add_u32 s54,s54,s46
s_add_u32 s65,s65,s62
R 34,2,22528
M 22,v[156:159],38
s_add_u32 s84,s84,28672
s_sub_u32 s79,s84,143360
L 5
M 18,v[160:163],42
s_min_u32 s84,s84,s79
R 38,2,23552
v_add_u32 v1,s84,v0
M 22,v[164:167],42
s_add_u32 s84,s84,28672
D 0,56,0
M 18,v[168:171],46
s_sub_u32 s79,s84,143360
s_min_u32 s84,s84,s79
R 42,2,24576
M 22,v[172:175],46
L 5
M 18,v[176:179],50
R 46,2,25600
M 22,v[180:183],50
M 18,v[184:187],54
R 50,2,26624
M 22,v[188:191],54
L 5
M 18,v[192:195],26
s_waitcnt vmcnt(5)
R 54,2,27648
v_add_u32 v2,s84,v0
M 22,v[196:199],26
s_barrier
ds_read_b32 v7,v6 offset:1024
M 18,v[200:203],30
R 26,1,0
M 22,v[204:207],30
L 6
buffer_store_dword v4,v3,s[36:39],s69 offen offset:0 sc0 sc1
M 18,v[208:211],34
s_mov_b32 s69,s68
s_add_u32 s68,s68,s63
R 30,1,1024
M 22,v[212:215],34
D 0,56,1024
M 18,v[216:219],38
L 2
R 34,1,2048
v_cmp_eq_u32 vcc,s53,v7
M 22,v[220:223],38
s_nop 0
D 0,56,2048
M 18,v[224:227],42
s_cmp_eq_u32 vcc_lo,-1
R 38,1,3072
M 22,v[228:231],42
M 18,v[232:235],46
R 42,1,4096
M 22,v[236:239],46
s_nop 0
M 18,v[240:243],50
R 46,1,5120
M 22,v[244:247],50
M 18,v[248:251],54
R 50,1,6144
M 22,v[252:255],54
L 5
s_cbranch_scc1 Lg4_S0_%=
Lg4_N0_%=:
v_mfma_f32_16x16x32_bf16 v[10:13],a[0:3],v[26:29],0
s_add_u32 m0,s59,0
R 54,1,7168
v_lshlrev_b32 v62,16,v58
v_mfma_f32_16x16x32_bf16 v[14:17],a[4:7],v[26:29],0
s_mov_b32 s66,s65
buffer_load_dword v5,s[36:39],s58 offen offset:0 sc1 lds
M 10,a[8:11],30
s_mov_b32 m0,s57
R 26,1,8192
v_add_f32 v18,v18,v62
M 14,a[12:15],30
L 5
v_and_b32 v63,0xffff0000,v58
v_add_f32 v19,v19,v63
M 10,a[16:19],34
R 30,1,9216
v_lshlrev_b32 v62,16,v59
M 14,a[20:23],34
D 0,55,0
M 10,a[24:27],38
R 34,1,10240
v_add_f32 v20,v20,v62
M 14,a[28:31],38
L 5
v_and_b32 v63,0xffff0000,v59
v_add_f32 v21,v21,v63
M 10,a[32:35],42
R 38,1,11264
v_lshlrev_b32 v62,16,v60
M 14,a[36:39],42
v_add_f32 v22,v22,v62
v_and_b32 v63,0xffff0000,v60
M 10,a[40:43],46
R 42,1,12288
v_add_f32 v23,v23,v63
M 14,a[44:47],46
L 5
v_lshlrev_b32 v62,16,v61
v_add_f32 v24,v24,v62
M 10,a[48:51],50
R 46,1,13312
v_and_b32 v63,0xffff0000,v61
M 14,a[52:55],50
buffer_load_dwordx4 v[58:61],v0,s[36:39],s74 offen offset:0 sc1
M 10,a[56:59],54
s_add_u32 s74,s74,s62
R 50,1,14336
v_add_f32 v25,v25,v63
M 14,a[60:63],54
L 5
D 0,55,1024
M 10,a[64:67],26
s_add_u32 s56,s55,4096
R 54,1,15360
v_cvt_pk_bf16_f32 v18,v18,v19
M 14,a[68:71],26
v_cvt_pk_bf16_f32 v19,v20,v21
v_cvt_pk_bf16_f32 v20,v22,v23
M 10,a[72:75],30
R 26,1,16384
v_cvt_pk_bf16_f32 v21,v24,v25
M 14,a[76:79],30
L 5
buffer_store_dwordx4 v[18:21],v0,s[36:39],s67 offen offset:0 sc0 sc1
M 10,a[80:83],34
R 30,1,17408
M 14,a[84:87],34
D 0,55,2048
M 10,a[88:91],38
R 34,1,18432
M 14,a[92:95],38
L 5
M 10,a[96:99],42
R 38,1,19456
M 14,a[100:103],42
M 10,a[104:107],46
R 42,1,20480
M 14,a[108:111],46
L 5
M 10,a[112:115],50
R 46,1,21504
M 14,a[116:119],50
D 0,55,3072
M 10,a[120:123],54
s_add_u32 m0,s57,4096
s_add_u32 s55,s55,s60
R 50,1,22528
M 14,a[124:127],54
s_add_u32 s57,s57,28672
s_sub_u32 s78,s57,143360
L 5
M 10,a[128:131],26
s_min_u32 s57,s57,s78
s_add_u32 s65,s65,s62
R 54,1,23552
M 14,a[132:135],26
s_add_u32 s84,s84,28672
s_sub_u32 s79,s84,143360
s_min_u32 s84,s84,s79
M 10,a[136:139],30
R 26,1,24576
M 14,a[140:143],30
L 5
M 10,a[144:147],34
R 30,1,25600
M 14,a[148:151],34
D 0,56,0
M 10,a[152:155],38
R 34,1,26624
M 14,a[156:159],38
L 5
M 10,a[160:163],42
R 38,1,27648
v_add_u32 v1,s84,v0
M 14,a[164:167],42
s_add_u32 s84,s84,28672
s_sub_u32 s79,s84,143360
s_min_u32 s84,s84,s79
M 10,a[168:171],46
R 42,2,0
M 14,a[172:175],46
L 5
M 10,a[176:179],50
R 46,2,1024
M 14,a[180:183],50
D 0,56,1024
M 10,a[184:187],54
R 50,2,2048
M 14,a[188:191],54
L 5
M 10,a[192:195],26
R 54,2,3072
M 14,a[196:199],26
M 10,a[200:203],30
R 26,2,4096
M 14,a[204:207],30
L 5
M 10,a[208:211],34
R 30,2,5120
M 14,a[212:215],34
D 0,56,2048
M 10,a[216:219],38
R 34,2,6144
M 14,a[220:223],38
L 5
M 10,a[224:227],42
R 38,2,7168
M 14,a[228:231],42
M 10,a[232:235],46
R 42,2,8192
M 14,a[236:239],46
L 5
M 10,a[240:243],50
R 46,2,9216
M 14,a[244:247],50
M 10,a[248:251],54
R 50,2,10240
M 14,a[252:255],54
L 5
M 10,v[64:67],26
R 54,2,11264
M 14,v[68:71],26
M 10,v[72:75],30
R 26,2,12288
M 14,v[76:79],30
L 5
M 10,v[80:83],34
R 30,2,13312
M 14,v[84:87],34
M 10,v[88:91],38
R 34,2,14336
M 14,v[92:95],38
L 5
M 10,v[96:99],42
R 38,2,15360
M 14,v[100:103],42
M 10,v[104:107],46
R 42,2,16384
M 14,v[108:111],46
L 5
M 10,v[112:115],50
R 46,2,17408
M 14,v[116:119],50
M 10,v[120:123],54
R 50,2,18432
M 14,v[124:127],54
L 5
M 10,v[128:131],26
R 54,2,19456
M 14,v[132:135],26
M 10,v[136:139],30
R 26,2,20480
M 14,v[140:143],30
L 5
M 10,v[144:147],34
R 30,2,21504
M 14,v[148:151],34
M 10,v[152:155],38
R 34,2,22528
M 14,v[156:159],38
L 5
M 10,v[160:163],42
R 38,2,23552
M 14,v[164:167],42
M 10,v[168:171],46
R 42,2,24576
M 14,v[172:175],46
L 5
M 10,v[176:179],50
R 46,2,25600
M 14,v[180:183],50
M 10,v[184:187],54
R 50,2,26624
M 14,v[188:191],54
L 5
M 10,v[192:195],26
s_waitcnt vmcnt(0)
R 54,2,27648
v_add_u32 v2,s84,v0
M 14,v[196:199],26
s_barrier
ds_read_b32 v7,v6 offset:0
M 10,v[200:203],30
R 26,1,0
M 14,v[204:207],30
L 6
buffer_store_dword v4,v3,s[36:39],s69 offen offset:0 sc0 sc1
M 10,v[208:211],34
s_mov_b32 s69,s68
s_add_u32 s68,s68,s63
R 30,1,1024
M 14,v[212:215],34
M 10,v[216:219],38
L 2
R 34,1,2048
v_cmp_eq_u32 vcc,s53,v7
M 14,v[220:223],38
s_nop 0
s_cmp_eq_u64 vcc,-1
M 10,v[224:227],42
R 38,1,3072
M 14,v[228:231],42
M 10,v[232:235],46
R 42,1,4096
M 14,v[236:239],46
s_nop 0
M 10,v[240:243],50
R 46,1,5120
M 14,v[244:247],50
M 10,v[248:251],54
R 50,1,6144
M 14,v[252:255],54
L 5
s_cbranch_scc1 Lg4_L21_%=
s_cmp_eq_u32 vcc_lo,-1
s_cbranch_scc1 Lg4_L11_%=
Lg4_X1_%=:
)"
R"(v_mfma_f32_16x16x32_bf16 v[18:21],a[0:3],v[26:29],0
s_add_u32 m0,s59,1024
R 54,1,7168
v_lshlrev_b32 v62,16,v58
v_mfma_f32_16x16x32_bf16 v[22:25],a[4:7],v[26:29],0
s_mov_b32 s67,s65
buffer_load_dword v5,s[36:39],s58 offen offset:0 sc1 lds
M 18,a[8:11],30
R 26,1,8192
v_add_f32 v10,v10,v62
M 22,a[12:15],30
L 5
v_and_b32 v63,0xffff0000,v58
v_add_f32 v11,v11,v63
M 18,a[16:19],34
R 30,1,9216
v_lshlrev_b32 v62,16,v59
M 22,a[20:23],34
v_add_f32 v12,v12,v62
v_and_b32 v63,0xffff0000,v59
M 18,a[24:27],38
R 34,1,10240
v_add_f32 v13,v13,v63
M 22,a[28:31],38
L 5
v_lshlrev_b32 v62,16,v60
v_add_f32 v14,v14,v62
M 18,a[32:35],42
R 38,1,11264
v_and_b32 v63,0xffff0000,v60
M 22,a[36:39],42
v_add_f32 v15,v15,v63
v_lshlrev_b32 v62,16,v61
M 18,a[40:43],46
R 42,1,12288
v_add_f32 v16,v16,v62
M 22,a[44:47],46
L 5
v_and_b32 v63,0xffff0000,v61
v_add_f32 v17,v17,v63
M 18,a[48:51],50
R 46,1,13312
v_cvt_pk_bf16_f32 v10,v10,v11
M 22,a[52:55],50
buffer_load_dwordx4 v[58:61],v0,s[36:39],s74 offen offset:0 sc1
M 18,a[56:59],54
s_add_u32 s74,s74,s62
R 50,1,14336
v_cvt_pk_bf16_f32 v11,v12,v13
M 22,a[60:63],54
L 5
v_cvt_pk_bf16_f32 v12,v14,v15
v_cvt_pk_bf16_f32 v13,v16,v17
M 18,a[64:67],26
s_add_u32 s65,s65,s62
s_add_u32 s84,s84,28672
R 54,1,15360
M 22,a[68:71],26
s_sub_u32 s79,s84,143360
s_min_u32 s84,s84,s79
buffer_store_dwordx4 v[10:13],v0,s[36:39],s66 offen offset:0 sc0 sc1
M 18,a[72:75],30
R 26,1,16384
M 22,a[76:79],30
L 5
M 18,a[80:83],34
R 30,1,17408
M 22,a[84:87],34
M 18,a[88:91],38
R 34,1,18432
M 22,a[92:95],38
L 5
M 18,a[96:99],42
R 38,1,19456
M 22,a[100:103],42
M 18,a[104:107],46
R 42,1,20480
M 22,a[108:111],46
L 5
M 18,a[112:115],50
R 46,1,21504
M 22,a[116:119],50
M 18,a[120:123],54
R 50,1,22528
M 22,a[124:127],54
L 5
M 18,a[128:131],26
R 54,1,23552
M 22,a[132:135],26
M 18,a[136:139],30
R 26,1,24576
M 22,a[140:143],30
L 5
M 18,a[144:147],34
R 30,1,25600
M 22,a[148:151],34
M 18,a[152:155],38
R 34,1,26624
M 22,a[156:159],38
L 5
M 18,a[160:163],42
R 38,1,27648
v_add_u32 v1,s84,v0
M 22,a[164:167],42
s_add_u32 s84,s84,28672
s_sub_u32 s79,s84,143360
s_min_u32 s84,s84,s79
M 18,a[168:171],46
R 42,2,0
M 22,a[172:175],46
L 5
M 18,a[176:179],50
R 46,2,1024
M 22,a[180:183],50
M 18,a[184:187],54
R 50,2,2048
M 22,a[188:191],54
L 5
M 18,a[192:195],26
R 54,2,3072
M 22,a[196:199],26
M 18,a[200:203],30
R 26,2,4096
M 22,a[204:207],30
L 5
M 18,a[208:211],34
R 30,2,5120
M 22,a[212:215],34
M 18,a[216:219],38
R 34,2,6144
M 22,a[220:223],38
L 5
M 18,a[224:227],42
R 38,2,7168
M 22,a[228:231],42
M 18,a[232:235],46
R 42,2,8192
M 22,a[236:239],46
L 5
M 18,a[240:243],50
R 46,2,9216
M 22,a[244:247],50
M 18,a[248:251],54
R 50,2,10240
M 22,a[252:255],54
L 5
M 18,v[64:67],26
R 54,2,11264
M 22,v[68:71],26
M 18,v[72:75],30
R 26,2,12288
M 22,v[76:79],30
L 5
M 18,v[80:83],34
R 30,2,13312
M 22,v[84:87],34
M 18,v[88:91],38
R 34,2,14336
M 22,v[92:95],38
L 5
M 18,v[96:99],42
R 38,2,15360
M 22,v[100:103],42
M 18,v[104:107],46
R 42,2,16384
M 22,v[108:111],46
L 5
M 18,v[112:115],50
R 46,2,17408
M 22,v[116:119],50
M 18,v[120:123],54
R 50,2,18432
M 22,v[124:127],54
L 5
M 18,v[128:131],26
R 54,2,19456
M 22,v[132:135],26
M 18,v[136:139],30
R 26,2,20480
M 22,v[140:143],30
L 5
M 18,v[144:147],34
R 30,2,21504
M 22,v[148:151],34
M 18,v[152:155],38
R 34,2,22528
M 22,v[156:159],38
L 5
M 18,v[160:163],42
R 38,2,23552
M 22,v[164:167],42
M 18,v[168:171],46
R 42,2,24576
M 22,v[172:175],46
L 5
M 18,v[176:179],50
R 46,2,25600
M 22,v[180:183],50
M 18,v[184:187],54
R 50,2,26624
M 22,v[188:191],54
L 5
M 18,v[192:195],26
s_waitcnt vmcnt(0)
R 54,2,27648
v_add_u32 v2,s84,v0
M 22,v[196:199],26
s_barrier
ds_read_b32 v7,v6 offset:1024
M 18,v[200:203],30
buffer_store_dword v4,v3,s[36:39],s69 offen offset:0 sc0 sc1
M 22,v[204:207],30
L 5
s_mov_b32 s69,s68
s_add_u32 s68,s68,s63
M 18,v[208:211],34
M 22,v[212:215],34
M 18,v[216:219],38
L 0
v_cmp_eq_u32 vcc,s53,v7
M 22,v[220:223],38
s_nop 0
M 18,v[224:227],42
M 22,v[228:231],42
M 18,v[232:235],46
M 22,v[236:239],46
s_nop 0
M 18,v[240:243],50
M 22,v[244:247],50
M 18,v[248:251],54
M 22,v[252:255],54
s_mov_b32 s80,0
s_cmp_eq_u32 vcc_lo,-1
s_cbranch_scc0 Lg4_xn1_%=
s_mov_b32 m0,s57
s_add_u32 s56,s55,4096
D 0,55,0
v_lshlrev_b32 v62,16,v58
D 0,55,1024
v_add_f32 v18,v18,v62
D 0,55,2048
v_and_b32 v63,0xffff0000,v58
D 0,55,3072
v_add_f32 v19,v19,v63
s_add_u32 m0,s57,4096
s_nop 0
D 0,56,0
v_lshlrev_b32 v62,16,v59
D 0,56,1024
v_add_f32 v20,v20,v62
D 0,56,2048
v_and_b32 v63,0xffff0000,v59
s_add_u32 s55,s55,28672
s_add_u32 s57,s57,28672
s_sub_u32 s78,s57,143360
s_min_u32 s57,s57,s78
s_add_u32 s54,s54,s46
s_mov_b32 m0,s57
s_add_u32 s56,s55,4096
D 0,55,0
v_add_f32 v21,v21,v63
D 0,55,1024
v_lshlrev_b32 v62,16,v60
D 0,55,2048
v_add_f32 v22,v22,v62
D 0,55,3072
v_and_b32 v63,0xffff0000,v60
s_add_u32 m0,s57,4096
s_nop 0
D 0,56,0
v_add_f32 v23,v23,v63
D 0,56,1024
v_lshlrev_b32 v62,16,v61
D 0,56,2048
v_add_f32 v24,v24,v62
s_add_u32 s55,s55,s60
s_add_u32 s57,s57,28672
s_sub_u32 s78,s57,143360
s_min_u32 s57,s57,s78
v_and_b32 v63,0xffff0000,v61
v_add_f32 v25,v25,v63
v_cvt_pk_bf16_f32 v18,v18,v19
v_cvt_pk_bf16_f32 v19,v20,v21
v_cvt_pk_bf16_f32 v20,v22,v23
v_cvt_pk_bf16_f32 v21,v24,v25
buffer_store_dwordx4 v[18:21],v0,s[36:39],s67 offen offset:0 sc0 sc1
s_mov_b32 s72,s69
s_mov_b32 s69,0x7fff0000
s_mov_b32 s67,0x7fff0000
s_mov_b32 s77,0x7fff0000
s_branch Lg4_gox_%=
Lg4_xn1_%=:
s_cmp_ge_u32 s54,256
s_cbranch_scc1 Lg4_fin1_%=
s_add_u32 m0,s59,4096
s_nop 0
buffer_load_dword v5,s[36:39],s58 offen offset:0 sc1 lds
buffer_store_dword v4,v3,s[36:39],s71 offen offset:0 sc0 sc1
s_add_u32 m0,s59,5120
s_nop 0
buffer_load_dword v5,s[36:39],s58 offen offset:0 sc1 lds
buffer_store_dword v4,v3,s[36:39],s71 offen offset:0 sc0 sc1
s_mov_b32 s83,0
v_lshlrev_b32 v62,16,v58
v_add_f32 v18,v18,v62
v_and_b32 v63,0xffff0000,v58
v_add_f32 v19,v19,v63
v_lshlrev_b32 v62,16,v59
v_add_f32 v20,v20,v62
v_and_b32 v63,0xffff0000,v59
v_add_f32 v21,v21,v63
v_lshlrev_b32 v62,16,v60
v_add_f32 v22,v22,v62
v_and_b32 v63,0xffff0000,v60
v_add_f32 v23,v23,v63
v_lshlrev_b32 v62,16,v61
v_add_f32 v24,v24,v62
v_and_b32 v63,0xffff0000,v61
v_add_f32 v25,v25,v63
v_cvt_pk_bf16_f32 v18,v18,v19
v_cvt_pk_bf16_f32 v19,v20,v21
v_cvt_pk_bf16_f32 v20,v22,v23
v_cvt_pk_bf16_f32 v21,v24,v25
buffer_store_dwordx4 v[18:21],v0,s[36:39],s67 offen offset:0 sc0 sc1
s_mov_b32 s72,s69
s_mov_b32 s69,0x7fff0000
s_mov_b32 s67,0x7fff0000
s_mov_b32 s77,0x7fff0000
s_branch Lg4_poll_%=
Lg4_fin1_%=:
v_lshlrev_b32 v62,16,v58
v_add_f32 v18,v18,v62
v_and_b32 v63,0xffff0000,v58
v_add_f32 v19,v19,v63
v_lshlrev_b32 v62,16,v59
v_add_f32 v20,v20,v62
v_and_b32 v63,0xffff0000,v59
v_add_f32 v21,v21,v63
v_lshlrev_b32 v62,16,v60
v_add_f32 v22,v22,v62
v_and_b32 v63,0xffff0000,v60
v_add_f32 v23,v23,v63
v_lshlrev_b32 v62,16,v61
v_add_f32 v24,v24,v62
v_and_b32 v63,0xffff0000,v61
v_add_f32 v25,v25,v63
v_cvt_pk_bf16_f32 v18,v18,v19
v_cvt_pk_bf16_f32 v19,v20,v21
v_cvt_pk_bf16_f32 v20,v22,v23
v_cvt_pk_bf16_f32 v21,v24,v25
buffer_store_dwordx4 v[18:21],v0,s[36:39],s67 offen offset:0 sc0 sc1
s_mov_b32 s72,s69
s_mov_b32 s69,0x7fff0000
s_mov_b32 s67,0x7fff0000
s_mov_b32 s77,0x7fff0000
s_branch Lg4_done_%=
Lg4_L10_%=:
v_mfma_f32_16x16x32_bf16 v[10:13],a[0:3],v[26:29],0
s_add_u32 s58,s58,s61
R 54,1,7168
v_lshlrev_b32 v62,16,v58
v_mfma_f32_16x16x32_bf16 v[14:17],a[4:7],v[26:29],0
s_add_u32 m0,s59,0
s_mov_b32 s66,s65
v_and_b32 v63,0xffff0000,v58
M 10,a[8:11],30
R 26,1,8192
v_add_f32 v18,v18,v62
M 14,a[12:15],30
L 5
buffer_load_dword v5,s[36:39],s58 offen offset:0 sc1 lds
M 10,a[16:19],34
s_mov_b32 m0,s57
R 30,1,9216
v_add_f32 v19,v19,v63
M 14,a[20:23],34
D 0,55,0
M 10,a[24:27],38
R 34,1,10240
v_lshlrev_b32 v62,16,v59
M 14,a[28:31],38
L 5
v_add_f32 v20,v20,v62
v_and_b32 v63,0xffff0000,v59
M 10,a[32:35],42
R 38,1,11264
v_add_f32 v21,v21,v63
M 14,a[36:39],42
D 0,55,1024
M 10,a[40:43],46
R 42,1,12288
v_lshlrev_b32 v62,16,v60
M 14,a[44:47],46
L 5
v_add_f32 v22,v22,v62
v_and_b32 v63,0xffff0000,v60
M 10,a[48:51],50
R 46,1,13312
v_add_f32 v23,v23,v63
M 14,a[52:55],50
D 0,55,2048
M 10,a[56:59],54
R 50,1,14336
v_lshlrev_b32 v62,16,v61
M 14,a[60:63],54
L 5
v_add_f32 v24,v24,v62
v_and_b32 v63,0xffff0000,v61
M 10,a[64:67],26
R 54,1,15360
v_add_f32 v25,v25,v63
M 14,a[68:71],26
buffer_load_dwordx4 v[58:61],v0,s[36:39],s74 offen offset:0 sc1
M 10,a[72:75],30
s_add_u32 s74,s74,s62
R 26,1,16384
v_cvt_pk_bf16_f32 v18,v18,v19
M 14,a[76:79],30
L 5
D 0,55,3072
M 10,a[80:83],34
s_add_u32 s56,s55,4096
R 30,1,17408
v_cvt_pk_bf16_f32 v19,v20,v21
M 14,a[84:87],34
s_add_u32 m0,s57,4096
v_cvt_pk_bf16_f32 v20,v22,v23
v_cvt_pk_bf16_f32 v21,v24,v25
M 10,a[88:91],38
s_add_u32 s55,s55,28672
s_add_u32 s57,s57,28672
R 34,1,18432
M 14,a[92:95],38
s_sub_u32 s78,s57,143360
L 5
buffer_store_dwordx4 v[18:21],v0,s[36:39],s67 offen offset:0 sc0 sc1
M 10,a[96:99],42
s_min_u32 s57,s57,s78
s_add_u32 s54,s54,s46
R 38,1,19456
M 14,a[100:103],42
D 0,56,0
M 10,a[104:107],46
R 42,1,20480
M 14,a[108:111],46
L 5
D 0,56,1024
M 10,a[112:115],50
R 46,1,21504
M 14,a[116:119],50
D 0,56,2048
M 10,a[120:123],54
s_mov_b32 m0,s57
s_add_u32 s56,s55,4096
R 50,1,22528
M 14,a[124:127],54
L 5
M 10,a[128:131],26
R 54,1,23552
M 14,a[132:135],26
D 0,55,0
M 10,a[136:139],30
R 26,1,24576
M 14,a[140:143],30
L 5
M 10,a[144:147],34
R 30,1,25600
M 14,a[148:151],34
D 0,55,1024
M 10,a[152:155],38
R 34,1,26624
M 14,a[156:159],38
L 5
M 10,a[160:163],42
R 38,1,27648
M 14,a[164:167],42
D 0,55,2048
M 10,a[168:171],46
R 42,2,0
M 14,a[172:175],46
L 5
M 10,a[176:179],50
R 46,2,1024
M 14,a[180:183],50
D 0,55,3072
M 10,a[184:187],54
s_add_u32 m0,s57,4096
s_add_u32 s55,s55,s60
R 50,2,2048
M 14,a[188:191],54
s_add_u32 s57,s57,28672
s_sub_u32 s78,s57,143360
L 5
M 10,a[192:195],26
s_min_u32 s57,s57,s78
s_add_u32 s65,s65,s62
R 54,2,3072
M 14,a[196:199],26
s_add_u32 s84,s84,28672
D 0,56,0
)"
R"(M 10,a[200:203],30
s_sub_u32 s79,s84,143360
s_min_u32 s84,s84,s79
R 26,2,4096
M 14,a[204:207],30
L 5
v_add_u32 v1,s84,v0
M 10,a[208:211],34
s_add_u32 s84,s84,28672
s_sub_u32 s79,s84,143360
R 30,2,5120
M 14,a[212:215],34
s_min_u32 s84,s84,s79
D 0,56,1024
M 10,a[216:219],38
R 34,2,6144
M 14,a[220:223],38
L 5
M 10,a[224:227],42
R 38,2,7168
M 14,a[228:231],42
D 0,56,2048
M 10,a[232:235],46
R 42,2,8192
M 14,a[236:239],46
L 5
M 10,a[240:243],50
R 46,2,9216
M 14,a[244:247],50
M 10,a[248:251],54
R 50,2,10240
M 14,a[252:255],54
L 5
M 10,v[64:67],26
R 54,2,11264
M 14,v[68:71],26
M 10,v[72:75],30
R 26,2,12288
M 14,v[76:79],30
L 5
M 10,v[80:83],34
R 30,2,13312
M 14,v[84:87],34
M 10,v[88:91],38
R 34,2,14336
M 14,v[92:95],38
L 5
M 10,v[96:99],42
R 38,2,15360
M 14,v[100:103],42
M 10,v[104:107],46
R 42,2,16384
M 14,v[108:111],46
L 5
M 10,v[112:115],50
R 46,2,17408
M 14,v[116:119],50
M 10,v[120:123],54
R 50,2,18432
M 14,v[124:127],54
L 5
M 10,v[128:131],26
R 54,2,19456
M 14,v[132:135],26
M 10,v[136:139],30
R 26,2,20480
M 14,v[140:143],30
L 5
M 10,v[144:147],34
R 30,2,21504
M 14,v[148:151],34
M 10,v[152:155],38
R 34,2,22528
M 14,v[156:159],38
L 5
M 10,v[160:163],42
R 38,2,23552
M 14,v[164:167],42
M 10,v[168:171],46
R 42,2,24576
M 14,v[172:175],46
L 5
M 10,v[176:179],50
R 46,2,25600
M 14,v[180:183],50
M 10,v[184:187],54
R 50,2,26624
M 14,v[188:191],54
L 5
M 10,v[192:195],26
s_waitcnt vmcnt(0)
R 54,2,27648
v_add_u32 v2,s84,v0
M 14,v[196:199],26
s_barrier
ds_read_b32 v7,v6 offset:0
M 10,v[200:203],30
R 26,1,0
M 14,v[204:207],30
L 6
buffer_store_dword v4,v3,s[36:39],s69 offen offset:0 sc0 sc1
M 10,v[208:211],34
s_mov_b32 s69,s68
s_add_u32 s68,s68,s63
R 30,1,1024
M 14,v[212:215],34
M 10,v[216:219],38
L 2
R 34,1,2048
v_cmp_eq_u32 vcc,s53,v7
M 14,v[220:223],38
s_nop 0
s_cmp_eq_u64 vcc,-1
M 10,v[224:227],42
R 38,1,3072
M 14,v[228:231],42
M 10,v[232:235],46
R 42,1,4096
M 14,v[236:239],46
s_nop 0
M 10,v[240:243],50
R 46,1,5120
M 14,v[244:247],50
M 10,v[248:251],54
R 50,1,6144
M 14,v[252:255],54
L 5
s_cbranch_scc1 Lg4_L21_%=
s_cmp_eq_u32 vcc_lo,-1
s_cbranch_scc1 Lg4_L11_%=
s_branch Lg4_X1_%=
Lg4_L11_%=:
v_mfma_f32_16x16x32_bf16 v[18:21],a[0:3],v[26:29],0
s_add_u32 s58,s58,s61
R 54,1,7168
v_lshlrev_b32 v62,16,v58
v_mfma_f32_16x16x32_bf16 v[22:25],a[4:7],v[26:29],0
s_add_u32 m0,s59,1024
s_mov_b32 s67,s65
v_and_b32 v63,0xffff0000,v58
M 18,a[8:11],30
R 26,1,8192
v_add_f32 v10,v10,v62
M 22,a[12:15],30
L 5
buffer_load_dword v5,s[36:39],s58 offen offset:0 sc1 lds
M 18,a[16:19],34
s_mov_b32 m0,s57
R 30,1,9216
v_add_f32 v11,v11,v63
M 22,a[20:23],34
D 0,55,0
M 18,a[24:27],38
R 34,1,10240
v_lshlrev_b32 v62,16,v59
M 22,a[28:31],38
L 5
v_add_f32 v12,v12,v62
v_and_b32 v63,0xffff0000,v59
M 18,a[32:35],42
R 38,1,11264
v_add_f32 v13,v13,v63
M 22,a[36:39],42
D 0,55,1024
M 18,a[40:43],46
R 42,1,12288
v_lshlrev_b32 v62,16,v60
M 22,a[44:47],46
L 5
v_add_f32 v14,v14,v62
v_and_b32 v63,0xffff0000,v60
M 18,a[48:51],50
R 46,1,13312
v_add_f32 v15,v15,v63
M 22,a[52:55],50
D 0,55,2048
M 18,a[56:59],54
R 50,1,14336
v_lshlrev_b32 v62,16,v61
M 22,a[60:63],54
L 5
v_add_f32 v16,v16,v62
v_and_b32 v63,0xffff0000,v61
M 18,a[64:67],26
R 54,1,15360
v_add_f32 v17,v17,v63
M 22,a[68:71],26
buffer_load_dwordx4 v[58:61],v0,s[36:39],s74 offen offset:0 sc1
M 18,a[72:75],30
s_add_u32 s74,s74,s62
R 26,1,16384
v_cvt_pk_bf16_f32 v10,v10,v11
M 22,a[76:79],30
L 5
D 0,55,3072
M 18,a[80:83],34
s_add_u32 s56,s55,4096
R 30,1,17408
v_cvt_pk_bf16_f32 v11,v12,v13
M 22,a[84:87],34
s_add_u32 m0,s57,4096
v_cvt_pk_bf16_f32 v12,v14,v15
v_cvt_pk_bf16_f32 v13,v16,v17
M 18,a[88:91],38
s_add_u32 s55,s55,28672
s_add_u32 s57,s57,28672
R 34,1,18432
M 22,a[92:95],38
s_sub_u32 s78,s57,143360
L 5
buffer_store_dwordx4 v[10:13],v0,s[36:39],s66 offen offset:0 sc0 sc1
M 18,a[96:99],42
s_min_u32 s57,s57,s78
s_add_u32 s54,s54,s46
R 38,1,19456
M 22,a[100:103],42
D 0,56,0
M 18,a[104:107],46
R 42,1,20480
M 22,a[108:111],46
L 5
D 0,56,1024
M 18,a[112:115],50
R 46,1,21504
M 22,a[116:119],50
D 0,56,2048
M 18,a[120:123],54
s_mov_b32 m0,s57
s_add_u32 s56,s55,4096
R 50,1,22528
M 22,a[124:127],54
L 5
M 18,a[128:131],26
R 54,1,23552
M 22,a[132:135],26
D 0,55,0
M 18,a[136:139],30
R 26,1,24576
M 22,a[140:143],30
L 5
M 18,a[144:147],34
R 30,1,25600
M 22,a[148:151],34
D 0,55,1024
M 18,a[152:155],38
R 34,1,26624
M 22,a[156:159],38
L 5
M 18,a[160:163],42
R 38,1,27648
M 22,a[164:167],42
D 0,55,2048
M 18,a[168:171],46
R 42,2,0
M 22,a[172:175],46
L 5
M 18,a[176:179],50
R 46,2,1024
M 22,a[180:183],50
D 0,55,3072
M 18,a[184:187],54
s_add_u32 m0,s57,4096
s_add_u32 s55,s55,s60
R 50,2,2048
M 22,a[188:191],54
s_add_u32 s57,s57,28672
s_sub_u32 s78,s57,143360
L 5
M 18,a[192:195],26
s_min_u32 s57,s57,s78
s_add_u32 s65,s65,s62
R 54,2,3072
M 22,a[196:199],26
s_add_u32 s84,s84,28672
D 0,56,0
M 18,a[200:203],30
s_sub_u32 s79,s84,143360
s_min_u32 s84,s84,s79
R 26,2,4096
M 22,a[204:207],30
L 5
v_add_u32 v1,s84,v0
M 18,a[208:211],34
s_add_u32 s84,s84,28672
s_sub_u32 s79,s84,143360
R 30,2,5120
M 22,a[212:215],34
s_min_u32 s84,s84,s79
D 0,56,1024
M 18,a[216:219],38
R 34,2,6144
M 22,a[220:223],38
L 5
M 18,a[224:227],42
R 38,2,7168
M 22,a[228:231],42
D 0,56,2048
M 18,a[232:235],46
R 42,2,8192
M 22,a[236:239],46
L 5
M 18,a[240:243],50
R 46,2,9216
M 22,a[244:247],50
M 18,a[248:251],54
R 50,2,10240
M 22,a[252:255],54
L 5
M 18,v[64:67],26
R 54,2,11264
M 22,v[68:71],26
M 18,v[72:75],30
R 26,2,12288
M 22,v[76:79],30
L 5
M 18,v[80:83],34
R 30,2,13312
M 22,v[84:87],34
M 18,v[88:91],38
R 34,2,14336
M 22,v[92:95],38
L 5
M 18,v[96:99],42
R 38,2,15360
M 22,v[100:103],42
M 18,v[104:107],46
R 42,2,16384
M 22,v[108:111],46
L 5
M 18,v[112:115],50
R 46,2,17408
M 22,v[116:119],50
M 18,v[120:123],54
R 50,2,18432
M 22,v[124:127],54
L 5
M 18,v[128:131],26
R 54,2,19456
M 22,v[132:135],26
M 18,v[136:139],30
R 26,2,20480
M 22,v[140:143],30
L 5
M 18,v[144:147],34
R 30,2,21504
M 22,v[148:151],34
M 18,v[152:155],38
R 34,2,22528
M 22,v[156:159],38
L 5
M 18,v[160:163],42
R 38,2,23552
M 22,v[164:167],42
M 18,v[168:171],46
R 42,2,24576
M 22,v[172:175],46
L 5
M 18,v[176:179],50
R 46,2,25600
M 22,v[180:183],50
M 18,v[184:187],54
R 50,2,26624
M 22,v[188:191],54
L 5
M 18,v[192:195],26
s_waitcnt vmcnt(0)
R 54,2,27648
v_add_u32 v2,s84,v0
M 22,v[196:199],26
s_barrier
ds_read_b32 v7,v6 offset:1024
M 18,v[200:203],30
R 26,1,0
M 22,v[204:207],30
L 6
buffer_store_dword v4,v3,s[36:39],s69 offen offset:0 sc0 sc1
M 18,v[208:211],34
s_mov_b32 s69,s68
s_add_u32 s68,s68,s63
R 30,1,1024
M 22,v[212:215],34
M 18,v[216:219],38
L 2
R 34,1,2048
v_cmp_eq_u32 vcc,s53,v7
M 22,v[220:223],38
s_nop 0
s_cmp_eq_u64 vcc,-1
M 18,v[224:227],42
R 38,1,3072
M 22,v[228:231],42
M 18,v[232:235],46
R 42,1,4096
M 22,v[236:239],46
s_nop 0
M 18,v[240:243],50
R 46,1,5120
M 22,v[244:247],50
M 18,v[248:251],54
R 50,1,6144
M 22,v[252:255],54
L 5
s_cbranch_scc1 Lg4_L20_%=
s_cmp_eq_u32 vcc_lo,-1
s_cbranch_scc1 Lg4_L10_%=
s_branch Lg4_X0_%=
Lg4_done_%=:
s_nop 0
s_barrier
buffer_store_dword v4,v3,s[36:39],s70 offen offset:0 sc0 sc1
s_waitcnt vmcnt(1)
s_barrier
buffer_store_dword v4,v3,s[36:39],s72 offen offset:0 sc0 sc1
s_waitcnt vmcnt(0) lgkmcnt(0)
s_barrier
s_endpgm
Lcode_g4_e:
.purgem M
.purgem R
.purgem T
.purgem D
.purgem L
)"
  :: "s"(ws), "s"(sWave), "s"(sG0), "s"(sStep), "s"(sOutOff), "s"(sFoutOff), "s"(sAuxOff), "s"(sWw), "s"(sBuf), "s"(sRk), "v"(vL16), "v"(vFlag), "v"(vSnapOff), "v"(vSnapRd), "v"(vWd0), "v"(vWd1), "v"(vWr0), "v"(vWr1), "s"(w4) : "v0", "v1", "v2", "v3", "v4", "v5", "v6", "v7", "v8", "v9", "v10", "v11", "v12", "v13", "v14", "v15", "v16", "v17", "v18", "v19", "v20", "v21", "v22", "v23", "v24", "v25", "v26", "v27", "v28", "v29", "v30", "v31", "v32", "v33", "v34", "v35", "v36", "v37", "v38", "v39", "v40", "v41", "v42", "v43", "v44", "v45", "v46", "v47", "v48", "v49", "v50", "v51", "v52", "v53", "v54", "v55", "v56", "v57", "v58", "v59", "v60", "v61", "v62", "v63", "v64", "v65", "v66", "v67", "v68", "v69", "v70", "v71", "v72", "v73", "v74", "v75", "v76", "v77", "v78", "v79", "v80", "v81", "v82", "v83", "v84", "v85", "v86", "v87", "v88", "v89", "v90", "v91", "v92", "v93", "v94", "v95", "v96", "v97", "v98", "v99", "v100", "v101", "v102", "v103", "v104", "v105", "v106", "v107", "v108", "v109", "v110", "v111", "v112", "v113", "v114", "v115", "v116", "v117", "v118", "v119", "v120", "v121", "v122", "v123", "v124", "v125", "v126", "v127", "v128", "v129", "v130", "v131", "v132", "v133", "v134", "v135", "v136", "v137", "v138", "v139", "v140", "v141", "v142", "v143", "v144", "v145", "v146", "v147", "v148", "v149", "v150", "v151", "v152", "v153", "v154", "v155", "v156", "v157", "v158", "v159", "v160", "v161", "v162", "v163", "v164", "v165", "v166", "v167", "v168", "v169", "v170", "v171", "v172", "v173", "v174", "v175", "v176", "v177", "v178", "v179", "v180", "v181", "v182", "v183", "v184", "v185", "v186", "v187", "v188", "v189", "v190", "v191", "v192", "v193", "v194", "v195", "v196", "v197", "v198", "v199", "v200", "v201", "v202", "v203", "v204", "v205", "v206", "v207", "v208", "v209", "v210", "v211", "v212", "v213", "v214", "v215", "v216", "v217", "v218", "v219", "v220", "v221", "v222", "v223", "v224", "v225", "v226", "v227", "v228", "v229", "v230", "v231", "v232", "v233", "v234", "v235", "v236", "v237", "v238", "v239", "a0", "a1", "a2", "a3", "a4", "a5", "a6", "a7", "a8", "a9", "a10", "a11", "a12", "a13", "a14", "a15", "a16", "a17", "a18", "a19", "a20", "a21", "a22", "a23", "a24", "a25", "a26", "a27", "a28", "a29", "a30", "a31", "a32", "a33", "a34", "a35", "a36", "a37", "a38", "a39", "a40", "a41", "a42", "a43", "a44", "a45", "a46", "a47", "a48", "a49", "a50", "a51", "a52", "a53", "a54", "a55", "a56", "a57", "a58", "a59", "a60", "a61", "a62", "a63", "a64", "a65", "a66", "a67", "a68", "a69", "a70", "a71", "a72", "a73", "a74", "a75", "a76", "a77", "a78", "a79", "a80", "a81", "a82", "a83", "a84", "a85", "a86", "a87", "a88", "a89", "a90", "a91", "a92", "a93", "a94", "a95", "a96", "a97", "a98", "a99", "a100", "a101", "a102", "a103", "a104", "a105", "a106", "a107", "a108", "a109", "a110", "a111", "a112", "a113", "a114", "a115", "a116", "a117", "a118", "a119", "a120", "a121", "a122", "a123", "a124", "a125", "a126", "a127", "a128", "a129", "a130", "a131", "a132", "a133", "a134", "a135", "a136", "a137", "a138", "a139", "a140", "a141", "a142", "a143", "a144", "a145", "a146", "a147", "a148", "a149", "a150", "a151", "a152", "a153", "a154", "a155", "a156", "a157", "a158", "a159", "a160", "a161", "a162", "a163", "a164", "a165", "a166", "a167", "a168", "a169", "a170", "a171", "a172", "a173", "a174", "a175", "a176", "a177", "a178", "a179", "a180", "a181", "a182", "a183", "a184", "a185", "a186", "a187", "a188", "a189", "a190", "a191", "a192", "a193", "a194", "a195", "a196", "a197", "a198", "a199", "a200", "a201", "a202", "a203", "a204", "a205", "a206", "a207", "a208", "a209", "a210", "a211", "a212", "a213", "a214", "a215", "a216", "a217", "a218", "a219", "a220", "a221", "a222", "a223", "a224", "a225", "a226", "a227", "a228", "a229", "a230", "a231", "a232", "a233", "a234", "a235", "a236", "a237", "a238", "a239", "a240", "a241", "a242", "a243", "a244", "a245", "a246", "a247", "a248", "a249", "a250", "a251", "a252", "a253", "a254", "a255", "s36", "s37", "s38", "s39", "s40", "s41", "s42", "s43", "s44", "s45", "s46", "s47", "s48", "s49", "s50", "s51", "s52", "s53", "s54", "s55", "s56", "s57", "s58", "s59", "s60", "s61", "s62", "s63", "s64", "s65", "s66", "s67", "s68", "s69", "s70", "s71", "s72", "s73", "s74", "s75", "s76", "s77", "s78", "s79", "s80", "s81", "s82", "s83", "s84", "s85", "s86", "s87", "s88", "s89", "s90", "s91", "s92", "s93", "s94", "s95", "s96", "s97", "vcc", "scc", "memory");
    __builtin_unreachable();
  } else {
    if (kind != 6) {
      const unsigned sStep = 4;
      COMMON(kind == 1 ? 8 : 16, kind == 1 ? fj * 128 : fj < 8 ? 1024 + fj * 128 : (fj & 2) * 1024 + (fj - 8) * 128, 1024, 131072, 2048, 256, kind == 1 ? 0 : 32768);
#define SHM 1
      const int sr = 16 + l15 - SHM * ((wave & 1) ? -1 : 1) * (1 + (wave >> 1));
      const unsigned vAn = (sr >> 4) * 32768 + (lg * 16 + (sr & 15)) * 16, vAe = (sr >> 4) == 1 ? vAn : OOB;
      const unsigned vOutS = 0, vAux0 = l15 * 32, vAux1 = 139264 + (l15 * 2 + (lg & 1)) * 16 + wave * 4 + (lg >> 1) * 512, vAux2 = 139264 + (l15 * 2 + (lg & 1)) * 16;
      const unsigned vAux3 = (wave == 0 && lg < 2) ? slice * 128 + l15 * 8 + lg * 4 : OOB;
      const unsigned sOutOff = (kind == 1 ? OFF_HS : OFF_X1) + (slice * 4 + wave) * 1024, sFoutOff = (kind == 1 ? OFF_FH : OFF_FX1) + slice * 128;
      const unsigned sAuxOff = OFF_XP + (slice * 4 + wave) * 1024, sBuf = kind, sWw = 16 * wave * 2048 + slice * 256;
      const u16* w12 = kind == 1 ? w1 : w2;
      unsigned epOut;
      asm volatile(
R"(.macro M d,w,x
v_mfma_f32_16x16x32_bf16 v[\d:\d+3], \w, v[\x:\x+3], v[\d:\d+3]
.endm
.macro R d,a,o
ds_read_b128 v[\d:\d+3], v\a offset:\o
.endm
.macro T d,a,o
ds_read_b64_tr_b16 \d, v\a offset:\o
.endm
.macro D a,s,o
buffer_load_dwordx4 v\a, s[36:39], s\s offen offset:\o sc1 lds
.endm
.macro L c
s_waitcnt lgkmcnt(\c)
.endm
Lcode_g12_b:
s_cmp_eq_u32 %9,2
s_cbranch_scc1 Lg2_begin_%=
s_mov_b64 s[36:37],%1
s_mov_b32 s38,111243392
s_mov_b32 s39,0x00020000
s_mov_b32 s44,%2
s_mov_b32 s45,%3
s_mov_b32 s46,%4
s_mov_b32 s47,%5
s_mov_b32 s48,%6
s_mov_b32 s49,%7
s_mov_b32 s50,%8
s_mov_b32 s51,%9
s_mov_b32 s93,%10
v_mov_b32 v0,%11
v_mov_b32 v3,%12
v_mov_b32 v5,%13
v_mov_b32 v6,%14
v_mov_b32 v18,%15
v_mov_b32 v19,%16
v_mov_b32 v20,%17
v_mov_b32 v21,%18
v_mov_b32 v22,%19
v_mov_b32 v10,%20
v_mov_b32 v11,%21
v_mov_b32 v12,%22
v_mov_b32 v13,%23
v_mov_b32 v14,%24
v_mov_b32 v15,%25
v_mov_b32 v16,%26
s_mov_b64 s[40:41],%27
s_mov_b32 s42,2097152
s_mov_b32 s43,0x00020000
s_lshl_b32 s57,s44,11
s_mov_b32 s79,111243264
buffer_load_dword v4,off,s[36:39],s79 sc1
s_add_u32 s55,s50,0
s_add_u32 m0,s57,0
s_nop 0
buffer_load_dwordx4 v18,s[40:43],s55 offen offset:0  lds
s_add_u32 m0,s57,1024
s_nop 0
buffer_load_dwordx4 v19,s[40:43],s55 offen offset:0  lds
s_add_u32 m0,s57,8064
s_nop 0
buffer_load_dwordx4 v18,s[40:43],s55 offen offset:128  lds
s_add_u32 m0,s57,9088
s_nop 0
buffer_load_dwordx4 v19,s[40:43],s55 offen offset:128  lds
s_add_u32 s55,s50,131072
s_add_u32 m0,s57,16384
s_nop 0
buffer_load_dwordx4 v18,s[40:43],s55 offen offset:0  lds
s_add_u32 m0,s57,17408
s_nop 0
buffer_load_dwordx4 v19,s[40:43],s55 offen offset:0  lds
s_add_u32 m0,s57,24448
s_nop 0
buffer_load_dwordx4 v18,s[40:43],s55 offen offset:128  lds
s_add_u32 m0,s57,25472
s_nop 0
buffer_load_dwordx4 v19,s[40:43],s55 offen offset:128  lds
s_add_u32 s55,s50,262144
s_add_u32 m0,s57,32768
s_nop 0
buffer_load_dwordx4 v18,s[40:43],s55 offen offset:0  lds
s_add_u32 m0,s57,33792
s_nop 0
buffer_load_dwordx4 v19,s[40:43],s55 offen offset:0  lds
s_add_u32 m0,s57,40832
s_nop 0
buffer_load_dwordx4 v18,s[40:43],s55 offen offset:128  lds
s_add_u32 m0,s57,41856
s_nop 0
buffer_load_dwordx4 v19,s[40:43],s55 offen offset:128  lds
s_add_u32 s55,s50,393216
s_add_u32 m0,s57,49152
s_nop 0
buffer_load_dwordx4 v18,s[40:43],s55 offen offset:0  lds
s_add_u32 m0,s57,50176
s_nop 0
buffer_load_dwordx4 v19,s[40:43],s55 offen offset:0  lds
s_add_u32 m0,s57,57216
s_nop 0
buffer_load_dwordx4 v18,s[40:43],s55 offen offset:128  lds
s_add_u32 m0,s57,58240
s_nop 0
buffer_load_dwordx4 v19,s[40:43],s55 offen offset:128  lds
s_add_u32 s55,s50,524288
s_add_u32 m0,s57,65536
s_nop 0
buffer_load_dwordx4 v18,s[40:43],s55 offen offset:0  lds
s_add_u32 m0,s57,66560
s_nop 0
buffer_load_dwordx4 v19,s[40:43],s55 offen offset:0  lds
s_add_u32 m0,s57,73600
s_nop 0
buffer_load_dwordx4 v18,s[40:43],s55 offen offset:128  lds
s_add_u32 m0,s57,74624
s_nop 0
buffer_load_dwordx4 v19,s[40:43],s55 offen offset:128  lds
s_add_u32 s55,s50,655360
s_add_u32 m0,s57,81920
s_nop 0
buffer_load_dwordx4 v18,s[40:43],s55 offen offset:0  lds
s_add_u32 m0,s57,82944
s_nop 0
buffer_load_dwordx4 v19,s[40:43],s55 offen offset:0  lds
s_add_u32 m0,s57,89984
s_nop 0
buffer_load_dwordx4 v18,s[40:43],s55 offen offset:128  lds
s_add_u32 m0,s57,91008
s_nop 0
buffer_load_dwordx4 v19,s[40:43],s55 offen offset:128  lds
s_waitcnt vmcnt(20)
L 0
s_barrier
s_add_u32 s55,s50,786432
s_add_u32 m0,s57,98304
s_nop 0
buffer_load_dwordx4 v18,s[40:43],s55 offen offset:0  lds
s_add_u32 m0,s57,99328
s_nop 0
buffer_load_dwordx4 v19,s[40:43],s55 offen offset:0  lds
s_add_u32 m0,s57,106368
s_nop 0
buffer_load_dwordx4 v18,s[40:43],s55 offen offset:128  lds
s_add_u32 m0,s57,107392
s_nop 0
buffer_load_dwordx4 v19,s[40:43],s55 offen offset:128  lds
v_add_u32 v26,0,v20
v_add_u32 v27,0,v21
T a[0:1],26,0
T a[2:3],26,512
T a[4:5],27,0
T a[6:7],27,512
T a[8:9],26,4096
T a[10:11],26,4608
T a[12:13],27,4096
T a[14:15],27,4608
s_waitcnt vmcnt(20)
L 0
s_barrier
s_add_u32 s55,s50,917504
s_add_u32 m0,s57,0
s_nop 0
buffer_load_dwordx4 v18,s[40:43],s55 offen offset:0  lds
s_add_u32 m0,s57,1024
s_nop 0
buffer_load_dwordx4 v19,s[40:43],s55 offen offset:0  lds
s_add_u32 m0,s57,8064
s_nop 0
buffer_load_dwordx4 v18,s[40:43],s55 offen offset:128  lds
s_add_u32 m0,s57,9088
s_nop 0
buffer_load_dwordx4 v19,s[40:43],s55 offen offset:128  lds
v_add_u32 v26,16384,v20
v_add_u32 v27,16384,v21
T a[16:17],26,0
T a[18:19],26,512
T a[20:21],27,0
T a[22:23],27,512
T a[24:25],26,4096
T a[26:27],26,4608
T a[28:29],27,4096
T a[30:31],27,4608
s_waitcnt vmcnt(20)
L 0
s_barrier
s_add_u32 s55,s50,1048576
s_add_u32 m0,s57,16384
s_nop 0
buffer_load_dwordx4 v18,s[40:43],s55 offen offset:0  lds
s_add_u32 m0,s57,17408
s_nop 0
buffer_load_dwordx4 v19,s[40:43],s55 offen offset:0  lds
s_add_u32 m0,s57,24448
s_nop 0
buffer_load_dwordx4 v18,s[40:43],s55 offen offset:128  lds
s_add_u32 m0,s57,25472
s_nop 0
buffer_load_dwordx4 v19,s[40:43],s55 offen offset:128  lds
v_add_u32 v26,32768,v20
v_add_u32 v27,32768,v21
T a[32:33],26,0
T a[34:35],26,512
T a[36:37],27,0
T a[38:39],27,512
T a[40:41],26,4096
T a[42:43],26,4608
T a[44:45],27,4096
T a[46:47],27,4608
s_waitcnt vmcnt(20)
L 0
s_barrier
s_add_u32 s55,s50,1179648
s_add_u32 m0,s57,32768
s_nop 0
buffer_load_dwordx4 v18,s[40:43],s55 offen offset:0  lds
s_add_u32 m0,s57,33792
s_nop 0
buffer_load_dwordx4 v19,s[40:43],s55 offen offset:0  lds
s_add_u32 m0,s57,40832
s_nop 0
buffer_load_dwordx4 v18,s[40:43],s55 offen offset:128  lds
s_add_u32 m0,s57,41856
s_nop 0
buffer_load_dwordx4 v19,s[40:43],s55 offen offset:128  lds
v_add_u32 v26,49152,v20
v_add_u32 v27,49152,v21
T a[48:49],26,0
T a[50:51],26,512
T a[52:53],27,0
T a[54:55],27,512
T a[56:57],26,4096
T a[58:59],26,4608
T a[60:61],27,4096
T a[62:63],27,4608
s_waitcnt vmcnt(20)
L 0
s_barrier
s_add_u32 s55,s50,1310720
s_add_u32 m0,s57,49152
s_nop 0
buffer_load_dwordx4 v18,s[40:43],s55 offen offset:0  lds
s_add_u32 m0,s57,50176
s_nop 0
buffer_load_dwordx4 v19,s[40:43],s55 offen offset:0  lds
s_add_u32 m0,s57,57216
s_nop 0
buffer_load_dwordx4 v18,s[40:43],s55 offen offset:128  lds
s_add_u32 m0,s57,58240
s_nop 0
buffer_load_dwordx4 v19,s[40:43],s55 offen offset:128  lds
v_add_u32 v26,65536,v20
v_add_u32 v27,65536,v21
T a[64:65],26,0
T a[66:67],26,512
T a[68:69],27,0
T a[70:71],27,512
T a[72:73],26,4096
T a[74:75],26,4608
T a[76:77],27,4096
T a[78:79],27,4608
s_waitcnt vmcnt(20)
L 0
s_barrier
s_add_u32 s55,s50,1441792
s_add_u32 m0,s57,65536
s_nop 0
buffer_load_dwordx4 v18,s[40:43],s55 offen offset:0  lds
s_add_u32 m0,s57,66560
s_nop 0
buffer_load_dwordx4 v19,s[40:43],s55 offen offset:0  lds
s_add_u32 m0,s57,73600
s_nop 0
buffer_load_dwordx4 v18,s[40:43],s55 offen offset:128  lds
s_add_u32 m0,s57,74624
s_nop 0
buffer_load_dwordx4 v19,s[40:43],s55 offen offset:128  lds
v_add_u32 v26,81920,v20
v_add_u32 v27,81920,v21
T a[80:81],26,0
T a[82:83],26,512
T a[84:85],27,0
T a[86:87],27,512
T a[88:89],26,4096
T a[90:91],26,4608
T a[92:93],27,4096
T a[94:95],27,4608
s_waitcnt vmcnt(20)
L 0
s_barrier
s_add_u32 s55,s50,1572864
s_add_u32 m0,s57,81920
s_nop 0
buffer_load_dwordx4 v18,s[40:43],s55 offen offset:0  lds
s_add_u32 m0,s57,82944
s_nop 0
buffer_load_dwordx4 v19,s[40:43],s55 offen offset:0  lds
s_add_u32 m0,s57,89984
s_nop 0
buffer_load_dwordx4 v18,s[40:43],s55 offen offset:128  lds
s_add_u32 m0,s57,91008
s_nop 0
buffer_load_dwordx4 v19,s[40:43],s55 offen offset:128  lds
v_add_u32 v26,98304,v20
v_add_u32 v27,98304,v21
T a[96:97],26,0
T a[98:99],26,512
T a[100:101],27,0
T a[102:103],27,512
T a[104:105],26,4096
T a[106:107],26,4608
T a[108:109],27,4096
T a[110:111],27,4608
s_waitcnt vmcnt(20)
L 0
s_barrier
s_add_u32 s55,s50,1703936
s_add_u32 m0,s57,98304
s_nop 0
buffer_load_dwordx4 v18,s[40:43],s55 offen offset:0  lds
s_add_u32 m0,s57,99328
s_nop 0
buffer_load_dwordx4 v19,s[40:43],s55 offen offset:0  lds
s_add_u32 m0,s57,106368
s_nop 0
buffer_load_dwordx4 v18,s[40:43],s55 offen offset:128  lds
s_add_u32 m0,s57,107392
s_nop 0
buffer_load_dwordx4 v19,s[40:43],s55 offen offset:128  lds
v_add_u32 v26,0,v20
v_add_u32 v27,0,v21
T a[112:113],26,0
T a[114:115],26,512
T a[116:117],27,0
T a[118:119],27,512
T a[120:121],26,4096
T a[122:123],26,4608
T a[124:125],27,4096
T a[126:127],27,4608
s_waitcnt vmcnt(20)
L 0
s_barrier
s_add_u32 s55,s50,1835008
s_add_u32 m0,s57,0
s_nop 0
buffer_load_dwordx4 v18,s[40:43],s55 offen offset:0  lds
s_add_u32 m0,s57,1024
s_nop 0
buffer_load_dwordx4 v19,s[40:43],s55 offen offset:0  lds
s_add_u32 m0,s57,8064
s_nop 0
buffer_load_dwordx4 v18,s[40:43],s55 offen offset:128  lds
s_add_u32 m0,s57,9088
s_nop 0
buffer_load_dwordx4 v19,s[40:43],s55 offen offset:128  lds
v_add_u32 v26,16384,v20
v_add_u32 v27,16384,v21
T a[128:129],26,0
T a[130:131],26,512
T a[132:133],27,0
T a[134:135],27,512
T a[136:137],26,4096
T a[138:139],26,4608
T a[140:141],27,4096
T a[142:143],27,4608
s_waitcnt vmcnt(20)
L 0
s_barrier
s_add_u32 s55,s50,1966080
s_add_u32 m0,s57,16384
s_nop 0
buffer_load_dwordx4 v18,s[40:43],s55 offen offset:0  lds
s_add_u32 m0,s57,17408
s_nop 0
buffer_load_dwordx4 v19,s[40:43],s55 offen offset:0  lds
s_add_u32 m0,s57,24448
s_nop 0
buffer_load_dwordx4 v18,s[40:43],s55 offen offset:128  lds
s_add_u32 m0,s57,25472
s_nop 0
buffer_load_dwordx4 v19,s[40:43],s55 offen offset:128  lds
v_add_u32 v26,32768,v20
v_add_u32 v27,32768,v21
T a[144:145],26,0
T a[146:147],26,512
T a[148:149],27,0
T a[150:151],27,512
T a[152:153],26,4096
T a[154:155],26,4608
T a[156:157],27,4096
T a[158:159],27,4608
s_waitcnt vmcnt(20)
L 0
s_barrier
v_add_u32 v26,49152,v20
v_add_u32 v27,49152,v21
T a[160:161],26,0
T a[162:163],26,512
T a[164:165],27,0
T a[166:167],27,512
T a[168:169],26,4096
T a[170:171],26,4608
T a[172:173],27,4096
T a[174:175],27,4608
s_waitcnt vmcnt(16)
L 0
s_barrier
v_add_u32 v26,65536,v20
v_add_u32 v27,65536,v21
T a[176:177],26,0
T a[178:179],26,512
T a[180:181],27,0
T a[182:183],27,512
T a[184:185],26,4096
T a[186:187],26,4608
T a[188:189],27,4096
T a[190:191],27,4608
s_waitcnt vmcnt(12)
L 0
s_barrier
v_add_u32 v26,81920,v20
v_add_u32 v27,81920,v21
T a[192:193],26,0
T a[194:195],26,512
T a[196:197],27,0
T a[198:199],27,512
T a[200:201],26,4096
T a[202:203],26,4608
T a[204:205],27,4096
T a[206:207],27,4608
s_waitcnt vmcnt(8)
L 0
s_barrier
v_add_u32 v26,98304,v20
v_add_u32 v27,98304,v21
T a[208:209],26,0
T a[210:211],26,512
T a[212:213],27,0
T a[214:215],27,512
T a[216:217],26,4096
T a[218:219],26,4608
T a[220:221],27,4096
T a[222:223],27,4608
s_waitcnt vmcnt(4)
L 0
s_barrier
v_add_u32 v26,0,v20
v_add_u32 v27,0,v21
T a[224:225],26,0
T a[226:227],26,512
T a[228:229],27,0
T a[230:231],27,512
T a[232:233],26,4096
T a[234:235],26,4608
T a[236:237],27,4096
T a[238:239],27,4608
s_waitcnt vmcnt(0)
L 0
s_barrier
v_add_u32 v26,16384,v20
v_add_u32 v27,16384,v21
T a[240:241],26,0
T a[242:243],26,512
T a[244:245],27,0
T a[246:247],27,512
T a[248:249],26,4096
T a[250:251],26,4608
T a[252:253],27,4096
T a[254:255],27,4608
L 0
s_barrier
v_readfirstlane_b32 s53,v4
s_nop 3
s_add_u32 s53,s53,1
s_cmp_eq_u32 s53,0
s_cselect_b32 s53,1,s53
v_mov_b32 v34,0x3f803f80
v_mov_b32 v35,0x3f803f80
v_mov_b32 v36,0x3f803f80
v_mov_b32 v37,0x3f803f80
s_nop 1
v_mfma_f32_16x16x32_bf16 v[74:77],a[0:3],v[34:37],0
v_mfma_f32_16x16x32_bf16 v[78:81],a[4:7],v[34:37],0
M 74,a[8:11],34
M 78,a[12:15],34
M 74,a[16:19],34
M 78,a[20:23],34
M 74,a[24:27],34
M 78,a[28:31],34
M 74,a[32:35],34
M 78,a[36:39],34
M 74,a[40:43],34
M 78,a[44:47],34
M 74,a[48:51],34
M 78,a[52:55],34
M 74,a[56:59],34
M 78,a[60:63],34
M 74,a[64:67],34
M 78,a[68:71],34
M 74,a[72:75],34
M 78,a[76:79],34
)"
R"(M 74,a[80:83],34
M 78,a[84:87],34
M 74,a[88:91],34
M 78,a[92:95],34
M 74,a[96:99],34
M 78,a[100:103],34
M 74,a[104:107],34
M 78,a[108:111],34
M 74,a[112:115],34
M 78,a[116:119],34
M 74,a[120:123],34
M 78,a[124:127],34
M 74,a[128:131],34
M 78,a[132:135],34
M 74,a[136:139],34
M 78,a[140:143],34
M 74,a[144:147],34
M 78,a[148:151],34
M 74,a[152:155],34
M 78,a[156:159],34
M 74,a[160:163],34
M 78,a[164:167],34
M 74,a[168:171],34
M 78,a[172:175],34
M 74,a[176:179],34
M 78,a[180:183],34
M 74,a[184:187],34
M 78,a[188:191],34
M 74,a[192:195],34
M 78,a[196:199],34
M 74,a[200:203],34
M 78,a[204:207],34
M 74,a[208:211],34
M 78,a[212:215],34
M 74,a[216:219],34
M 78,a[220:223],34
M 74,a[224:227],34
M 78,a[228:231],34
M 74,a[232:235],34
M 78,a[236:239],34
M 74,a[240:243],34
M 78,a[244:247],34
M 74,a[248:251],34
M 78,a[252:255],34
v_mov_b32 v4,s53
s_mov_b32 s82,0
s_mov_b32 s80,0
s_mov_b32 s54,s45
s_mul_i32 s57,s44,8192
s_mul_i32 s55,s45,32768
s_add_u32 s55,s55,s57
s_add_u32 s55,s55,12582912
s_mul_i32 s60,s46,32768
s_mul_i32 s61,s46,1024
s_mul_i32 s62,s46,32768
s_mul_i32 s63,s46,1024
s_mul_i32 s64,s46,512
s_lshl_b32 s59,s44,8
s_add_u32 s59,s59,131072
s_mul_i32 s58,s45,1024
s_add_u32 s58,s58,109056000
s_mul_i32 s65,s45,32768
s_add_u32 s65,s65,s47
s_mul_i32 s68,s45,1024
s_add_u32 s68,s68,s48
s_mul_i32 s73,s45,512
s_add_u32 s73,s73,106954752
s_mul_i32 s74,s45,32768
s_add_u32 s74,s74,s49
s_mul_i32 s75,s45,1024
s_add_u32 s75,s75,107479040
s_mov_b32 s66,0x7fff0000
s_mov_b32 s67,0x7fff0000
s_mov_b32 s69,0x7fff0000
s_mov_b32 s76,0x7fff0000
s_mov_b32 s77,0x7fff0000
s_mov_b32 s70,0x7fff0000
s_mov_b32 s71,0x7fff0000
s_mov_b32 s72,0x7fff0000
v_mov_b32 v1,v0
v_add_u32 v2,32768,v0
s_mov_b32 s84,32768
s_mov_b32 s86,0x3a800000
s_mov_b32 s87,0x3727c5ac
s_mov_b32 s88,0xc0135761
s_mov_b32 s89,0x3d372713
s_and_b32 s85,s44,1
s_mul_i32 s85,s85,127
v_lshlrev_b32 v8,22,v0
v_and_b32 v8,0x80000000,v8
v_or_b32 v8,v8,v5
s_add_u32 m0,s59,4096
s_nop 0
buffer_load_dword v5,s[36:39],s58 offen offset:0 sc1 lds
buffer_store_dword v4,v3,s[36:39],s71 offen offset:0 sc0 sc1
s_add_u32 m0,s59,5120
s_nop 0
buffer_load_dword v5,s[36:39],s58 offen offset:0 sc1 lds
buffer_store_dword v4,v3,s[36:39],s71 offen offset:0 sc0 sc1
s_mov_b32 s83,0
Lg1_poll_%=:
s_add_u32 s78,s83,2
s_and_b32 s78,s78,3
s_lshl_b32 s78,s78,10
s_add_u32 s78,s78,4096
s_add_u32 m0,s59,s78
s_nop 0
buffer_load_dword v5,s[36:39],s58 offen offset:0 sc1 lds
s_and_b32 s78,s83,3
s_lshl_b32 s78,s78,10
v_add_u32 v82,s78,v6
s_waitcnt vmcnt(4)
s_barrier
ds_read_b32 v7,v82 offset:4096
buffer_store_dword v4,v3,s[36:39],s70 offen offset:0 sc0 sc1
s_mov_b32 s70,s71
s_mov_b32 s71,s72
s_mov_b32 s72,0x7fff0000
s_add_u32 s83,s83,1
s_add_u32 s82,s82,1
L 0
v_cmp_eq_u32 vcc,s53,v7
s_nop 0
s_cmp_eq_u32 vcc_lo,-1
s_cbranch_scc1 Lg1_go_%=
s_cmp_lt_u32 s82,20000
s_cbranch_scc1 Lg1_poll_%=
Lg1_go_%=:
s_mov_b32 m0,s57
s_add_u32 s56,s55,4096
D 0,55,0
D 0,55,1024
D 0,55,2048
D 0,55,3072
s_add_u32 m0,s57,4096
s_nop 0
D 0,56,0
D 0,56,1024
D 0,56,2048
D 0,56,3072
s_add_u32 s55,s55,s60
s_add_u32 s57,s57,32768
s_and_b32 s57,s57,131071
s_add_u32 s54,s54,s46
Lg1_gox_%=:
s_add_u32 s58,s58,s61
s_cmp_eq_u32 vcc_hi,-1
s_cbranch_scc1 Lg1_go2_%=
s_add_u32 m0,s59,2048
s_nop 0
buffer_load_dword v5,s[36:39],s58 offen offset:0 sc1 lds
s_waitcnt vmcnt(0)
s_barrier
buffer_store_dword v4,v3,s[36:39],s70 offen offset:0 sc0 sc1
s_min_u32 s71,s71,s72
buffer_store_dword v4,v3,s[36:39],s71 offen offset:0 sc0 sc1
s_mov_b32 s70,0x7fff0000
s_mov_b32 s71,0x7fff0000
s_mov_b32 s72,0x7fff0000
s_cmp_eq_u32 s80,1
s_cbranch_scc1 Lg1_st11_%=
Lg1_st10_%=:
ds_read_b32 v7,v6 offset:2048
R 34,1,0
R 38,1,1024
R 42,1,2048
R 46,1,3072
R 50,1,4096
R 54,1,5120
R 58,1,6144
L 7
v_cmp_eq_u32 vcc,s53,v7
L 5
s_cmp_eq_u64 vcc,-1
s_cbranch_scc1 Lg1_L20_%=
s_cmp_eq_u32 vcc_lo,-1
s_cbranch_scc1 Lg1_L10_%=
s_branch Lg1_X0_%=
Lg1_st11_%=:
ds_read_b32 v7,v6 offset:2048
R 34,2,0
R 38,2,1024
R 42,2,2048
R 46,2,3072
R 50,2,4096
R 54,2,5120
R 58,2,6144
L 7
v_cmp_eq_u32 vcc,s53,v7
L 5
s_cmp_eq_u64 vcc,-1
s_cbranch_scc1 Lg1_L21_%=
s_cmp_eq_u32 vcc_lo,-1
s_cbranch_scc1 Lg1_L11_%=
s_branch Lg1_X1_%=
Lg1_go2_%=:
s_add_u32 s58,s58,s61
s_add_u32 m0,s59,2048
s_nop 0
buffer_load_dword v5,s[36:39],s58 offen offset:0 sc1 lds
s_mov_b32 m0,s57
s_add_u32 s56,s55,4096
D 0,55,0
D 0,55,1024
D 0,55,2048
D 0,55,3072
s_add_u32 m0,s57,4096
s_nop 0
D 0,56,0
D 0,56,1024
D 0,56,2048
D 0,56,3072
s_add_u32 s55,s55,s60
s_add_u32 s57,s57,32768
s_and_b32 s57,s57,131071
s_add_u32 s54,s54,s46
s_waitcnt vmcnt(8)
s_barrier
buffer_store_dword v4,v3,s[36:39],s70 offen offset:0 sc0 sc1
s_min_u32 s71,s71,s72
buffer_store_dword v4,v3,s[36:39],s71 offen offset:0 sc0 sc1
s_mov_b32 s70,0x7fff0000
s_mov_b32 s71,0x7fff0000
s_mov_b32 s72,0x7fff0000
s_cmp_eq_u32 s80,1
s_cbranch_scc1 Lg1_st21_%=
Lg1_st20_%=:
ds_read_b32 v7,v6 offset:2048
R 34,1,0
R 38,1,1024
R 42,1,2048
R 46,1,3072
R 50,1,4096
R 54,1,5120
R 58,1,6144
L 7
v_cmp_eq_u32 vcc,s53,v7
L 5
s_cmp_eq_u32 vcc_lo,-1
s_cbranch_scc1 Lg1_S0_%=
s_branch Lg1_N0_%=
Lg1_st21_%=:
ds_read_b32 v7,v6 offset:2048
R 34,2,0
R 38,2,1024
R 42,2,2048
R 46,2,3072
R 50,2,4096
R 54,2,5120
R 58,2,6144
L 7
v_cmp_eq_u32 vcc,s53,v7
L 5
s_cmp_eq_u32 vcc_lo,-1
s_cbranch_scc1 Lg1_S1_%=
s_branch Lg1_N1_%=
Lg1_L20_%=:
v_mfma_f32_16x16x32_bf16 v[18:21],a[0:3],v[34:37],0
s_add_u32 s58,s58,s61
s_add_u32 s58,s58,s61
R 62,1,7168
v_mfma_f32_16x16x32_bf16 v[22:25],a[4:7],v[34:37],0
s_add_u32 m0,s59,0
buffer_load_dwordx4 v[66:69],v11,s[36:39],s73 offen offset:0 sc1
M 18,a[8:11],38
s_add_u32 s73,s73,s64
R 34,1,8192
v_fma_f32 v26,-v70,v74,v26
M 22,a[12:15],38
L 5
buffer_load_dword v8,s[36:39],s58 offen offset:0 sc1 lds
M 18,a[16:19],42
s_mov_b32 m0,s57
R 38,1,9216
v_fma_f32 v27,-v70,v75,v27
M 22,a[20:23],42
s_add_u32 s56,s55,4096
v_fma_f32 v28,-v70,v76,v28
v_fma_f32 v29,-v70,v77,v29
M 18,a[24:27],46
s_mov_b32 s66,s65
R 42,1,10240
v_mul_f32 v82,v26,v26
M 22,a[28:31],46
L 5
D 0,55,0
M 18,a[32:35],50
R 46,1,11264
v_mul_f32 v83,v27,v27
M 22,a[36:39],50
v_mul_f32 v84,v28,v28
v_mul_f32 v85,v29,v29
M 18,a[40:43],54
R 50,1,12288
v_fma_f32 v82,v72,v82,v71
M 22,a[44:47],54
L 5
D 0,55,1024
M 18,a[48:51],58
R 54,1,13312
v_fma_f32 v83,v72,v83,v71
M 22,a[52:55],58
v_fma_f32 v84,v72,v84,v71
v_fma_f32 v85,v72,v85,v71
M 18,a[56:59],62
R 58,1,14336
v_mul_f32 v82,v26,v82
M 22,a[60:63],62
L 5
D 0,55,2048
M 18,a[64:67],34
R 62,1,15360
v_mul_f32 v83,v27,v83
M 22,a[68:71],34
v_exp_f32 v82,v82
M 18,a[72:75],38
R 34,1,16384
v_mul_f32 v84,v28,v84
M 22,a[76:79],38
L 5
D 0,55,3072
M 18,a[80:83],42
s_add_u32 m0,s57,4096
R 38,1,17408
v_mul_f32 v85,v29,v85
M 22,a[84:87],42
s_add_u32 s55,s55,s60
v_exp_f32 v83,v83
M 18,a[88:91],46
s_add_u32 s57,s57,32768
R 42,1,18432
v_fma_f32 v82,v82,v73,v73
M 22,a[92:95],46
L 5
D 0,56,0
M 18,a[96:99],50
s_and_b32 s57,s57,131071
R 46,1,19456
v_fma_f32 v83,v83,v73,v73
M 22,a[100:103],50
s_add_u32 s54,s54,s46
v_exp_f32 v84,v84
M 18,a[104:107],54
R 50,1,20480
v_fma_f32 v84,v84,v73,v73
M 22,a[108:111],54
L 5
D 0,56,1024
M 18,a[112:115],58
R 54,1,21504
v_fma_f32 v30,-v70,v78,v30
M 22,a[116:119],58
v_exp_f32 v85,v85
M 18,a[120:123],62
R 58,1,22528
v_fma_f32 v85,v85,v73,v73
M 22,a[124:127],62
L 5
D 0,56,2048
M 18,a[128:131],34
R 62,1,23552
v_fma_f32 v31,-v70,v79,v31
M 22,a[132:135],34
v_rcp_f32 v82,v82
M 18,a[136:139],38
R 34,1,24576
v_mul_f32 v26,v26,v82
M 22,a[140:143],38
L 5
D 0,56,3072
M 18,a[144:147],42
s_mov_b32 m0,s57
R 38,1,25600
v_fma_f32 v32,-v70,v80,v32
M 22,a[148:151],42
s_add_u32 s56,s55,4096
v_rcp_f32 v83,v83
M 18,a[152:155],46
R 42,1,26624
v_mul_f32 v27,v27,v83
M 22,a[156:159],46
L 5
D 0,55,0
M 18,a[160:163],50
R 46,1,27648
v_cvt_pk_bf16_f32 v90,v26,v27
M 22,a[164:167],50
D 0,55,1024
M 18,a[168:171],54
R 50,1,28672
v_fma_f32 v33,-v70,v81,v33
M 22,a[172:175],54
L 5
v_rcp_f32 v84,v84
M 18,a[176:179],58
R 54,1,29696
v_mul_f32 v28,v28,v84
M 22,a[180:183],58
D 0,55,2048
M 18,a[184:187],62
R 58,1,30720
v_mul_f32 v86,v30,v30
M 22,a[188:191],62
L 5
D 0,55,3072
M 18,a[192:195],34
s_waitcnt vmcnt(4)
R 62,1,31744
v_mul_f32 v87,v31,v31
M 22,a[196:199],34
s_barrier
ds_read_b32 v7,v6 offset:0
v_mul_f32 v88,v32,v32
M 18,a[200:203],38
s_add_u32 m0,s57,4096
R 34,2,0
v_mul_f32 v89,v33,v33
M 22,a[204:207],38
L 6
buffer_store_dword v4,v3,s[36:39],s70 offen offset:0 sc0 sc1
M 18,a[208:211],42
s_add_u32 s55,s55,s60
R 38,2,1024
v_fma_f32 v86,v72,v86,v71
M 22,a[212:215],42
s_add_u32 s57,s57,32768
D 0,56,0
M 18,a[216:219],46
s_and_b32 s57,s57,131071
R 42,2,2048
v_fma_f32 v87,v72,v87,v71
M 22,a[220:223],46
L 6
D 0,56,1024
M 18,a[224:227],50
s_add_u32 s54,s54,s46
R 46,2,3072
v_fma_f32 v88,v72,v88,v71
M 22,a[228:231],50
s_mov_b32 s70,s69
D 0,56,2048
M 18,a[232:235],54
s_add_u32 s65,s65,s62
R 50,2,4096
v_fma_f32 v89,v72,v89,v71
M 22,a[236:239],54
L 6
D 0,56,3072
M 18,a[240:243],58
s_mov_b32 s69,s68
R 54,2,5120
v_mul_f32 v86,v30,v86
v_mul_f32 v87,v31,v87
v_mul_f32 v88,v32,v88
v_mul_f32 v89,v33,v89
v_exp_f32 v86,v86
v_exp_f32 v87,v87
v_exp_f32 v88,v88
v_exp_f32 v89,v89
M 22,a[244:247],58
L 6
v_rcp_f32 v85,v85
v_fma_f32 v86,v86,v73,v73
v_fma_f32 v87,v87,v73,v73
v_fma_f32 v88,v88,v73,v73
v_fma_f32 v89,v89,v73,v73
v_rcp_f32 v86,v86
v_rcp_f32 v87,v87
v_rcp_f32 v88,v88
v_rcp_f32 v89,v89
v_cmp_eq_u32 vcc,s53,v7
M 18,a[248:251],62
s_add_u32 s68,s68,s63
s_cmp_eq_u32 vcc_lo,-1
R 58,2,6144
v_mul_f32 v29,v29,v85
v_cvt_pk_bf16_f32 v91,v28,v29
v_mul_f32 v30,v30,v86
v_mul_f32 v31,v31,v87
v_mul_f32 v32,v32,v88
v_mul_f32 v33,v33,v89
v_cvt_pk_bf16_f32 v92,v30,v31
v_cvt_pk_bf16_f32 v93,v32,v33
M 22,a[252:255],62
L 5
buffer_store_dwordx4 v[90:93],v0,s[36:39],s67 offen offset:0 sc0 sc1
v_xor_b32 v1,65536,v1
s_cbranch_scc0 Lg1_N1_%=
Lg1_S1_%=:
v_mfma_f32_16x16x32_bf16 v[26:29],a[0:3],v[34:37],0
s_add_u32 s58,s58,s61
s_add_u32 m0,s59,1024
R 62,2,7168
v_mfma_f32_16x16x32_bf16 v[30:33],a[4:7],v[34:37],0
s_mov_b32 s67,s65
buffer_load_dword v8,s[36:39],s58 offen offset:0 sc1 lds
M 26,a[8:11],38
s_mov_b32 m0,s57
R 34,2,8192
v_fma_f32 v18,-v66,v74,v18
)"
R"(M 30,a[12:15],38
L 5
buffer_load_dwordx4 v[70:73],v11,s[36:39],s73 offen offset:0 sc1
M 26,a[16:19],42
s_add_u32 s73,s73,s64
R 38,2,9216
v_fma_f32 v19,-v66,v75,v19
v_fma_f32 v20,-v66,v76,v20
M 30,a[20:23],42
s_add_u32 s56,s55,4096
v_fma_f32 v21,-v66,v77,v21
v_mul_f32 v82,v18,v18
M 26,a[24:27],46
R 42,2,10240
v_mul_f32 v83,v19,v19
M 30,a[28:31],46
L 5
D 0,55,0
M 26,a[32:35],50
R 46,2,11264
v_mul_f32 v84,v20,v20
M 30,a[36:39],50
v_mul_f32 v85,v21,v21
v_fma_f32 v82,v68,v82,v67
v_fma_f32 v83,v68,v83,v67
M 26,a[40:43],54
R 50,2,12288
v_fma_f32 v84,v68,v84,v67
M 30,a[44:47],54
L 5
v_fma_f32 v85,v68,v85,v67
v_mul_f32 v82,v18,v82
M 26,a[48:51],58
R 54,2,13312
v_mul_f32 v83,v19,v83
M 30,a[52:55],58
D 0,55,1024
M 26,a[56:59],62
R 58,2,14336
v_exp_f32 v82,v82
M 30,a[60:63],62
L 5
v_exp_f32 v83,v83
M 26,a[64:67],34
R 62,2,15360
v_mul_f32 v84,v20,v84
M 30,a[68:71],34
v_exp_f32 v84,v84
M 26,a[72:75],38
R 34,2,16384
v_mul_f32 v85,v21,v85
M 30,a[76:79],38
L 5
v_exp_f32 v85,v85
v_fma_f32 v82,v82,v69,v69
M 26,a[80:83],42
R 38,2,17408
v_fma_f32 v83,v83,v69,v69
M 30,a[84:87],42
D 0,55,2048
M 26,a[88:91],46
R 42,2,18432
v_fma_f32 v84,v84,v69,v69
M 30,a[92:95],46
L 5
v_rcp_f32 v82,v82
M 26,a[96:99],50
R 46,2,19456
v_rcp_f32 v83,v83
M 30,a[100:103],50
v_rcp_f32 v84,v84
M 26,a[104:107],54
R 50,2,20480
v_fma_f32 v85,v85,v69,v69
M 30,a[108:111],54
L 5
D 0,55,3072
M 26,a[112:115],58
s_add_u32 m0,s57,4096
R 54,2,21504
v_mul_f32 v18,v18,v82
M 30,a[116:119],58
s_add_u32 s55,s55,s60
v_rcp_f32 v85,v85
v_mul_f32 v19,v19,v83
M 26,a[120:123],62
s_add_u32 s57,s57,32768
R 58,2,22528
v_mul_f32 v20,v20,v84
M 30,a[124:127],62
L 5
v_mul_f32 v21,v21,v85
v_cvt_pk_bf16_f32 v90,v18,v19
M 26,a[128:131],34
s_and_b32 s57,s57,131071
R 62,2,23552
v_cvt_pk_bf16_f32 v91,v20,v21
M 30,a[132:135],34
s_add_u32 s54,s54,s46
v_fma_f32 v22,-v66,v78,v22
v_fma_f32 v23,-v66,v79,v23
M 26,a[136:139],38
s_add_u32 s65,s65,s62
R 34,2,24576
v_fma_f32 v24,-v66,v80,v24
v_fma_f32 v25,-v66,v81,v25
M 30,a[140:143],38
L 5
D 0,56,0
M 26,a[144:147],42
R 38,2,25600
v_mul_f32 v86,v22,v22
M 30,a[148:151],42
v_mul_f32 v87,v23,v23
v_mul_f32 v88,v24,v24
M 26,a[152:155],46
R 42,2,26624
v_mul_f32 v89,v25,v25
M 30,a[156:159],46
L 5
v_fma_f32 v86,v68,v86,v67
v_fma_f32 v87,v68,v87,v67
v_fma_f32 v88,v68,v88,v67
M 26,a[160:163],50
R 46,2,27648
v_fma_f32 v89,v68,v89,v67
M 30,a[164:167],50
D 0,56,1024
M 26,a[168:171],54
R 50,2,28672
v_mul_f32 v86,v22,v86
M 30,a[172:175],54
L 5
v_exp_f32 v86,v86
M 26,a[176:179],58
R 54,2,29696
v_mul_f32 v87,v23,v87
v_mul_f32 v88,v24,v88
M 30,a[180:183],58
v_exp_f32 v87,v87
M 26,a[184:187],62
R 58,2,30720
v_mul_f32 v89,v25,v89
M 30,a[188:191],62
L 5
v_exp_f32 v88,v88
M 26,a[192:195],34
s_waitcnt vmcnt(6)
R 62,2,31744
v_fma_f32 v86,v86,v69,v69
M 30,a[196:199],34
s_barrier
ds_read_b32 v7,v6 offset:1024
v_exp_f32 v89,v89
M 26,a[200:203],38
R 34,1,0
v_fma_f32 v87,v87,v69,v69
M 30,a[204:207],38
L 6
buffer_store_dword v4,v3,s[36:39],s70 offen offset:0 sc0 sc1
M 26,a[208:211],42
s_mov_b32 s70,s69
R 38,1,1024
v_fma_f32 v88,v88,v69,v69
M 30,a[212:215],42
s_mov_b32 s69,s68
D 0,56,2048
M 26,a[216:219],46
L 2
R 42,1,2048
v_rcp_f32 v86,v86
M 30,a[220:223],46
s_nop 0
D 0,56,3072
M 26,a[224:227],50
s_add_u32 s68,s68,s63
R 46,1,3072
v_fma_f32 v89,v89,v69,v69
M 30,a[228:231],50
v_rcp_f32 v87,v87
M 26,a[232:235],54
R 50,1,4096
v_mul_f32 v22,v22,v86
M 30,a[236:239],54
s_nop 0
v_rcp_f32 v88,v88
v_mul_f32 v23,v23,v87
M 26,a[240:243],58
R 54,1,5120
v_mul_f32 v24,v24,v88
M 30,a[244:247],58
v_rcp_f32 v89,v89
v_cmp_eq_u32 vcc,s53,v7
M 26,a[248:251],62
s_cmp_eq_u32 vcc_lo,-1
R 58,1,6144
v_mul_f32 v25,v25,v89
v_cvt_pk_bf16_f32 v92,v22,v23
v_cvt_pk_bf16_f32 v93,v24,v25
M 30,a[252:255],62
L 5
buffer_store_dwordx4 v[90:93],v0,s[36:39],s66 offen offset:0 sc0 sc1
v_xor_b32 v2,65536,v2
s_cbranch_scc0 Lg1_N0_%=
Lg1_S0_%=:
v_mfma_f32_16x16x32_bf16 v[18:21],a[0:3],v[34:37],0
s_add_u32 s58,s58,s61
s_add_u32 m0,s59,0
R 62,1,7168
v_mfma_f32_16x16x32_bf16 v[22:25],a[4:7],v[34:37],0
s_mov_b32 s66,s65
buffer_load_dword v8,s[36:39],s58 offen offset:0 sc1 lds
M 18,a[8:11],38
s_mov_b32 m0,s57
R 34,1,8192
v_fma_f32 v26,-v70,v74,v26
M 22,a[12:15],38
L 5
buffer_load_dwordx4 v[66:69],v11,s[36:39],s73 offen offset:0 sc1
M 18,a[16:19],42
s_add_u32 s73,s73,s64
R 38,1,9216
v_fma_f32 v27,-v70,v75,v27
v_fma_f32 v28,-v70,v76,v28
M 22,a[20:23],42
s_add_u32 s56,s55,4096
v_fma_f32 v29,-v70,v77,v29
v_mul_f32 v82,v26,v26
M 18,a[24:27],46
R 42,1,10240
v_mul_f32 v83,v27,v27
M 22,a[28:31],46
L 5
D 0,55,0
M 18,a[32:35],50
R 46,1,11264
v_mul_f32 v84,v28,v28
M 22,a[36:39],50
v_mul_f32 v85,v29,v29
v_fma_f32 v82,v72,v82,v71
v_fma_f32 v83,v72,v83,v71
M 18,a[40:43],54
R 50,1,12288
v_fma_f32 v84,v72,v84,v71
M 22,a[44:47],54
L 5
v_fma_f32 v85,v72,v85,v71
v_mul_f32 v82,v26,v82
M 18,a[48:51],58
R 54,1,13312
v_mul_f32 v83,v27,v83
M 22,a[52:55],58
D 0,55,1024
M 18,a[56:59],62
R 58,1,14336
v_exp_f32 v82,v82
M 22,a[60:63],62
L 5
v_exp_f32 v83,v83
M 18,a[64:67],34
R 62,1,15360
v_mul_f32 v84,v28,v84
M 22,a[68:71],34
v_exp_f32 v84,v84
M 18,a[72:75],38
R 34,1,16384
v_mul_f32 v85,v29,v85
M 22,a[76:79],38
L 5
v_exp_f32 v85,v85
v_fma_f32 v82,v82,v73,v73
M 18,a[80:83],42
R 38,1,17408
v_fma_f32 v83,v83,v73,v73
M 22,a[84:87],42
D 0,55,2048
M 18,a[88:91],46
R 42,1,18432
v_fma_f32 v84,v84,v73,v73
M 22,a[92:95],46
L 5
v_rcp_f32 v82,v82
M 18,a[96:99],50
R 46,1,19456
v_rcp_f32 v83,v83
M 22,a[100:103],50
v_rcp_f32 v84,v84
M 18,a[104:107],54
R 50,1,20480
v_fma_f32 v85,v85,v73,v73
M 22,a[108:111],54
L 5
D 0,55,3072
M 18,a[112:115],58
s_add_u32 m0,s57,4096
R 54,1,21504
v_mul_f32 v26,v26,v82
M 22,a[116:119],58
s_add_u32 s55,s55,s60
v_rcp_f32 v85,v85
v_mul_f32 v27,v27,v83
M 18,a[120:123],62
s_add_u32 s57,s57,32768
R 58,1,22528
v_mul_f32 v28,v28,v84
M 22,a[124:127],62
L 5
v_mul_f32 v29,v29,v85
v_cvt_pk_bf16_f32 v90,v26,v27
M 18,a[128:131],34
s_and_b32 s57,s57,131071
R 62,1,23552
v_cvt_pk_bf16_f32 v91,v28,v29
M 22,a[132:135],34
s_add_u32 s54,s54,s46
v_fma_f32 v30,-v70,v78,v30
v_fma_f32 v31,-v70,v79,v31
M 18,a[136:139],38
s_add_u32 s65,s65,s62
R 34,1,24576
v_fma_f32 v32,-v70,v80,v32
v_fma_f32 v33,-v70,v81,v33
M 22,a[140:143],38
L 5
D 0,56,0
M 18,a[144:147],42
R 38,1,25600
v_mul_f32 v86,v30,v30
M 22,a[148:151],42
v_mul_f32 v87,v31,v31
v_mul_f32 v88,v32,v32
M 18,a[152:155],46
R 42,1,26624
v_mul_f32 v89,v33,v33
M 22,a[156:159],46
L 5
v_fma_f32 v86,v72,v86,v71
v_fma_f32 v87,v72,v87,v71
v_fma_f32 v88,v72,v88,v71
M 18,a[160:163],50
R 46,1,27648
v_fma_f32 v89,v72,v89,v71
M 22,a[164:167],50
D 0,56,1024
M 18,a[168:171],54
R 50,1,28672
v_mul_f32 v86,v30,v86
M 22,a[172:175],54
L 5
v_exp_f32 v86,v86
M 18,a[176:179],58
R 54,1,29696
v_mul_f32 v87,v31,v87
v_mul_f32 v88,v32,v88
M 22,a[180:183],58
v_exp_f32 v87,v87
M 18,a[184:187],62
R 58,1,30720
v_mul_f32 v89,v33,v89
M 22,a[188:191],62
L 5
v_exp_f32 v88,v88
M 18,a[192:195],34
s_waitcnt vmcnt(6)
R 62,1,31744
v_fma_f32 v86,v86,v73,v73
M 22,a[196:199],34
s_barrier
ds_read_b32 v7,v6 offset:0
v_exp_f32 v89,v89
M 18,a[200:203],38
R 34,2,0
v_fma_f32 v87,v87,v73,v73
M 22,a[204:207],38
L 6
buffer_store_dword v4,v3,s[36:39],s70 offen offset:0 sc0 sc1
M 18,a[208:211],42
s_mov_b32 s70,s69
R 38,2,1024
v_fma_f32 v88,v88,v73,v73
M 22,a[212:215],42
s_mov_b32 s69,s68
D 0,56,2048
M 18,a[216:219],46
L 2
R 42,2,2048
v_rcp_f32 v86,v86
M 22,a[220:223],46
s_nop 0
D 0,56,3072
M 18,a[224:227],50
s_add_u32 s68,s68,s63
R 46,2,3072
v_fma_f32 v89,v89,v73,v73
M 22,a[228:231],50
v_rcp_f32 v87,v87
M 18,a[232:235],54
R 50,2,4096
v_mul_f32 v30,v30,v86
M 22,a[236:239],54
s_nop 0
v_rcp_f32 v88,v88
v_mul_f32 v31,v31,v87
M 18,a[240:243],58
R 54,2,5120
v_mul_f32 v32,v32,v88
M 22,a[244:247],58
v_rcp_f32 v89,v89
v_cmp_eq_u32 vcc,s53,v7
M 18,a[248:251],62
s_cmp_eq_u32 vcc_lo,-1
R 58,2,6144
v_mul_f32 v33,v33,v89
v_cvt_pk_bf16_f32 v92,v30,v31
v_cvt_pk_bf16_f32 v93,v32,v33
M 22,a[252:255],62
L 5
buffer_store_dwordx4 v[90:93],v0,s[36:39],s67 offen offset:0 sc0 sc1
v_xor_b32 v1,65536,v1
s_cbranch_scc1 Lg1_S1_%=
Lg1_N1_%=:
v_mfma_f32_16x16x32_bf16 v[26:29],a[0:3],v[34:37],0
s_add_u32 m0,s59,1024
s_mov_b32 s67,s65
R 62,2,7168
v_mfma_f32_16x16x32_bf16 v[30:33],a[4:7],v[34:37],0
buffer_load_dword v5,s[36:39],s58 offen offset:0 sc1 lds
M 26,a[8:11],38
R 34,2,8192
v_fma_f32 v18,-v66,v74,v18
M 30,a[12:15],38
L 5
buffer_load_dwordx4 v[70:73],v11,s[36:39],s73 offen offset:0 sc1
M 26,a[16:19],42
s_add_u32 s73,s73,s64
R 38,2,9216
v_fma_f32 v19,-v66,v75,v19
M 30,a[20:23],42
s_add_u32 s65,s65,s62
v_fma_f32 v20,-v66,v76,v20
v_fma_f32 v21,-v66,v77,v21
M 26,a[24:27],46
R 42,2,10240
v_mul_f32 v82,v18,v18
M 30,a[28:31],46
L 5
v_mul_f32 v83,v19,v19
v_mul_f32 v84,v20,v20
M 26,a[32:35],50
R 46,2,11264
v_mul_f32 v85,v21,v21
M 30,a[36:39],50
v_fma_f32 v82,v68,v82,v67
v_fma_f32 v83,v68,v83,v67
M 26,a[40:43],54
R 50,2,12288
v_fma_f32 v84,v68,v84,v67
M 30,a[44:47],54
L 5
v_fma_f32 v85,v68,v85,v67
v_mul_f32 v82,v18,v82
M 26,a[48:51],58
R 54,2,13312
v_mul_f32 v83,v19,v83
M 30,a[52:55],58
v_exp_f32 v82,v82
M 26,a[56:59],62
R 58,2,14336
v_mul_f32 v84,v20,v84
M 30,a[60:63],62
L 5
v_exp_f32 v83,v83
M 26,a[64:67],34
R 62,2,15360
v_mul_f32 v85,v21,v85
M 30,a[68:71],34
v_exp_f32 v84,v84
M 26,a[72:75],38
R 34,2,16384
v_fma_f32 v82,v82,v69,v69
M 30,a[76:79],38
L 5
v_exp_f32 v85,v85
M 26,a[80:83],42
R 38,2,17408
v_fma_f32 v83,v83,v69,v69
M 30,a[84:87],42
v_rcp_f32 v82,v82
M 26,a[88:91],46
R 42,2,18432
v_fma_f32 v84,v84,v69,v69
M 30,a[92:95],46
L 5
v_rcp_f32 v83,v83
M 26,a[96:99],50
R 46,2,19456
v_fma_f32 v85,v85,v69,v69
M 30,a[100:103],50
v_rcp_f32 v84,v84
M 26,a[104:107],54
)"
R"(R 50,2,20480
v_mul_f32 v18,v18,v82
M 30,a[108:111],54
L 5
v_rcp_f32 v85,v85
M 26,a[112:115],58
R 54,2,21504
v_mul_f32 v19,v19,v83
M 30,a[116:119],58
v_mul_f32 v20,v20,v84
v_mul_f32 v21,v21,v85
M 26,a[120:123],62
R 58,2,22528
v_cvt_pk_bf16_f32 v90,v18,v19
M 30,a[124:127],62
L 5
v_cvt_pk_bf16_f32 v91,v20,v21
v_fma_f32 v22,-v66,v78,v22
M 26,a[128:131],34
R 62,2,23552
v_fma_f32 v23,-v66,v79,v23
M 30,a[132:135],34
v_fma_f32 v24,-v66,v80,v24
v_fma_f32 v25,-v66,v81,v25
M 26,a[136:139],38
R 34,2,24576
v_mul_f32 v86,v22,v22
M 30,a[140:143],38
L 5
v_mul_f32 v87,v23,v23
v_mul_f32 v88,v24,v24
M 26,a[144:147],42
R 38,2,25600
v_mul_f32 v89,v25,v25
M 30,a[148:151],42
v_fma_f32 v86,v68,v86,v67
v_fma_f32 v87,v68,v87,v67
M 26,a[152:155],46
R 42,2,26624
v_fma_f32 v88,v68,v88,v67
M 30,a[156:159],46
L 5
v_fma_f32 v89,v68,v89,v67
v_mul_f32 v86,v22,v86
M 26,a[160:163],50
R 46,2,27648
v_mul_f32 v87,v23,v87
M 30,a[164:167],50
v_exp_f32 v86,v86
M 26,a[168:171],54
R 50,2,28672
v_mul_f32 v88,v24,v88
M 30,a[172:175],54
L 5
v_exp_f32 v87,v87
M 26,a[176:179],58
R 54,2,29696
v_mul_f32 v89,v25,v89
M 30,a[180:183],58
v_exp_f32 v88,v88
M 26,a[184:187],62
R 58,2,30720
v_fma_f32 v86,v86,v69,v69
M 30,a[188:191],62
L 5
v_exp_f32 v89,v89
M 26,a[192:195],34
s_waitcnt vmcnt(0)
R 62,2,31744
v_fma_f32 v87,v87,v69,v69
M 30,a[196:199],34
s_barrier
ds_read_b32 v7,v6 offset:1024
v_fma_f32 v88,v88,v69,v69
M 26,a[200:203],38
R 34,1,0
v_fma_f32 v89,v89,v69,v69
M 30,a[204:207],38
L 6
buffer_store_dword v4,v3,s[36:39],s70 offen offset:0 sc0 sc1
M 26,a[208:211],42
s_mov_b32 s70,s69
R 38,1,1024
v_xor_b32 v2,65536,v2
M 30,a[212:215],42
s_mov_b32 s69,s68
v_rcp_f32 v86,v86
M 26,a[216:219],46
L 2
R 42,1,2048
v_mul_f32 v22,v22,v86
M 30,a[220:223],46
s_nop 0
v_rcp_f32 v87,v87
M 26,a[224:227],50
s_add_u32 s68,s68,s63
R 46,1,3072
v_mul_f32 v23,v23,v87
M 30,a[228:231],50
v_rcp_f32 v88,v88
M 26,a[232:235],54
R 50,1,4096
v_mul_f32 v24,v24,v88
M 30,a[236:239],54
s_nop 0
v_rcp_f32 v89,v89
M 26,a[240:243],58
R 54,1,5120
v_mul_f32 v25,v25,v89
M 30,a[244:247],58
v_cvt_pk_bf16_f32 v92,v22,v23
v_cmp_eq_u32 vcc,s53,v7
M 26,a[248:251],62
s_cmp_eq_u64 vcc,-1
R 58,1,6144
v_cvt_pk_bf16_f32 v93,v24,v25
M 30,a[252:255],62
L 5
buffer_store_dwordx4 v[90:93],v0,s[36:39],s66 offen offset:0 sc0 sc1
s_cbranch_scc1 Lg1_L20_%=
s_cmp_eq_u32 vcc_lo,-1
s_cbranch_scc1 Lg1_L10_%=
Lg1_X0_%=:
v_mfma_f32_16x16x32_bf16 v[18:21],a[0:3],v[34:37],0
s_add_u32 m0,s59,0
s_mov_b32 s66,s65
R 62,1,7168
v_mfma_f32_16x16x32_bf16 v[22:25],a[4:7],v[34:37],0
buffer_load_dword v5,s[36:39],s58 offen offset:0 sc1 lds
M 18,a[8:11],38
R 34,1,8192
v_fma_f32 v26,-v70,v74,v26
M 22,a[12:15],38
L 5
buffer_load_dwordx4 v[66:69],v11,s[36:39],s73 offen offset:0 sc1
M 18,a[16:19],42
s_add_u32 s73,s73,s64
R 38,1,9216
v_fma_f32 v27,-v70,v75,v27
M 22,a[20:23],42
s_add_u32 s65,s65,s62
v_fma_f32 v28,-v70,v76,v28
v_fma_f32 v29,-v70,v77,v29
M 18,a[24:27],46
R 42,1,10240
v_mul_f32 v82,v26,v26
M 22,a[28:31],46
L 5
v_mul_f32 v83,v27,v27
v_mul_f32 v84,v28,v28
M 18,a[32:35],50
R 46,1,11264
v_mul_f32 v85,v29,v29
M 22,a[36:39],50
v_fma_f32 v82,v72,v82,v71
v_fma_f32 v83,v72,v83,v71
M 18,a[40:43],54
R 50,1,12288
v_fma_f32 v84,v72,v84,v71
M 22,a[44:47],54
L 5
v_fma_f32 v85,v72,v85,v71
v_mul_f32 v82,v26,v82
M 18,a[48:51],58
R 54,1,13312
v_mul_f32 v83,v27,v83
M 22,a[52:55],58
v_exp_f32 v82,v82
M 18,a[56:59],62
R 58,1,14336
v_mul_f32 v84,v28,v84
M 22,a[60:63],62
L 5
v_exp_f32 v83,v83
M 18,a[64:67],34
R 62,1,15360
v_mul_f32 v85,v29,v85
M 22,a[68:71],34
v_exp_f32 v84,v84
M 18,a[72:75],38
R 34,1,16384
v_fma_f32 v82,v82,v73,v73
M 22,a[76:79],38
L 5
v_exp_f32 v85,v85
M 18,a[80:83],42
R 38,1,17408
v_fma_f32 v83,v83,v73,v73
M 22,a[84:87],42
v_rcp_f32 v82,v82
M 18,a[88:91],46
R 42,1,18432
v_fma_f32 v84,v84,v73,v73
M 22,a[92:95],46
L 5
v_rcp_f32 v83,v83
M 18,a[96:99],50
R 46,1,19456
v_fma_f32 v85,v85,v73,v73
M 22,a[100:103],50
v_rcp_f32 v84,v84
M 18,a[104:107],54
R 50,1,20480
v_mul_f32 v26,v26,v82
M 22,a[108:111],54
L 5
v_rcp_f32 v85,v85
M 18,a[112:115],58
R 54,1,21504
v_mul_f32 v27,v27,v83
M 22,a[116:119],58
v_mul_f32 v28,v28,v84
v_mul_f32 v29,v29,v85
M 18,a[120:123],62
R 58,1,22528
v_cvt_pk_bf16_f32 v90,v26,v27
M 22,a[124:127],62
L 5
v_cvt_pk_bf16_f32 v91,v28,v29
v_fma_f32 v30,-v70,v78,v30
M 18,a[128:131],34
R 62,1,23552
v_fma_f32 v31,-v70,v79,v31
M 22,a[132:135],34
v_fma_f32 v32,-v70,v80,v32
v_fma_f32 v33,-v70,v81,v33
M 18,a[136:139],38
R 34,1,24576
v_mul_f32 v86,v30,v30
M 22,a[140:143],38
L 5
v_mul_f32 v87,v31,v31
v_mul_f32 v88,v32,v32
M 18,a[144:147],42
R 38,1,25600
v_mul_f32 v89,v33,v33
M 22,a[148:151],42
v_fma_f32 v86,v72,v86,v71
v_fma_f32 v87,v72,v87,v71
M 18,a[152:155],46
R 42,1,26624
v_fma_f32 v88,v72,v88,v71
M 22,a[156:159],46
L 5
v_fma_f32 v89,v72,v89,v71
v_mul_f32 v86,v30,v86
M 18,a[160:163],50
R 46,1,27648
v_mul_f32 v87,v31,v87
M 22,a[164:167],50
v_exp_f32 v86,v86
M 18,a[168:171],54
R 50,1,28672
v_mul_f32 v88,v32,v88
M 22,a[172:175],54
L 5
v_exp_f32 v87,v87
M 18,a[176:179],58
R 54,1,29696
v_mul_f32 v89,v33,v89
M 22,a[180:183],58
v_exp_f32 v88,v88
M 18,a[184:187],62
R 58,1,30720
v_fma_f32 v86,v86,v73,v73
M 22,a[188:191],62
L 5
v_exp_f32 v89,v89
M 18,a[192:195],34
s_waitcnt vmcnt(0)
R 62,1,31744
v_fma_f32 v87,v87,v73,v73
M 22,a[196:199],34
s_barrier
ds_read_b32 v7,v6 offset:0
v_fma_f32 v88,v88,v73,v73
M 18,a[200:203],38
buffer_store_dword v4,v3,s[36:39],s70 offen offset:0 sc0 sc1
M 22,a[204:207],38
L 5
v_rcp_f32 v86,v86
M 18,a[208:211],42
s_mov_b32 s70,s69
v_rcp_f32 v87,v87
M 22,a[212:215],42
s_mov_b32 s69,s68
v_rcp_f32 v88,v88
M 18,a[216:219],46
L 0
v_fma_f32 v89,v89,v73,v73
v_mul_f32 v30,v30,v86
M 22,a[220:223],46
s_nop 0
v_rcp_f32 v89,v89
M 18,a[224:227],50
s_add_u32 s68,s68,s63
v_mul_f32 v31,v31,v87
v_mul_f32 v32,v32,v88
M 22,a[228:231],50
v_mul_f32 v33,v33,v89
v_cvt_pk_bf16_f32 v92,v30,v31
M 18,a[232:235],54
v_cvt_pk_bf16_f32 v93,v32,v33
v_xor_b32 v1,65536,v1
M 22,a[236:239],54
s_nop 0
buffer_store_dwordx4 v[90:93],v0,s[36:39],s67 offen offset:0 sc0 sc1
v_cmp_eq_u32 vcc,s53,v7
M 18,a[240:243],58
M 22,a[244:247],58
M 18,a[248:251],62
M 22,a[252:255],62
s_mov_b32 s80,1
s_cmp_eq_u32 vcc_lo,-1
s_cbranch_scc0 Lg1_xn0_%=
s_mov_b32 m0,s57
s_add_u32 s56,s55,4096
D 0,55,0
s_nop 0
v_fma_f32 v18,-v66,v74,v18
v_fma_f32 v19,-v66,v75,v19
v_fma_f32 v20,-v66,v76,v20
v_fma_f32 v21,-v66,v77,v21
v_mul_f32 v82,v18,v18
v_mul_f32 v83,v19,v19
v_mul_f32 v84,v20,v20
D 0,55,1024
v_mul_f32 v85,v21,v21
v_fma_f32 v82,v68,v82,v67
v_fma_f32 v83,v68,v83,v67
v_fma_f32 v84,v68,v84,v67
v_fma_f32 v85,v68,v85,v67
v_mul_f32 v82,v18,v82
v_mul_f32 v83,v19,v83
D 0,55,2048
v_mul_f32 v84,v20,v84
v_mul_f32 v85,v21,v85
v_exp_f32 v82,v82
v_exp_f32 v83,v83
v_exp_f32 v84,v84
v_exp_f32 v85,v85
v_fma_f32 v82,v82,v69,v69
D 0,55,3072
v_fma_f32 v83,v83,v69,v69
v_fma_f32 v84,v84,v69,v69
v_fma_f32 v85,v85,v69,v69
v_rcp_f32 v82,v82
v_rcp_f32 v83,v83
v_rcp_f32 v84,v84
v_rcp_f32 v85,v85
s_add_u32 m0,s57,4096
s_nop 0
D 0,56,0
v_mul_f32 v18,v18,v82
v_mul_f32 v19,v19,v83
v_mul_f32 v20,v20,v84
v_mul_f32 v21,v21,v85
v_cvt_pk_bf16_f32 v90,v18,v19
v_cvt_pk_bf16_f32 v91,v20,v21
v_fma_f32 v22,-v66,v78,v22
D 0,56,1024
v_fma_f32 v23,-v66,v79,v23
v_fma_f32 v24,-v66,v80,v24
v_fma_f32 v25,-v66,v81,v25
v_mul_f32 v86,v22,v22
v_mul_f32 v87,v23,v23
v_mul_f32 v88,v24,v24
v_mul_f32 v89,v25,v25
D 0,56,2048
v_fma_f32 v86,v68,v86,v67
v_fma_f32 v87,v68,v87,v67
v_fma_f32 v88,v68,v88,v67
v_fma_f32 v89,v68,v89,v67
v_mul_f32 v86,v22,v86
v_mul_f32 v87,v23,v87
v_mul_f32 v88,v24,v88
D 0,56,3072
v_mul_f32 v89,v25,v89
v_exp_f32 v86,v86
v_exp_f32 v87,v87
v_exp_f32 v88,v88
v_exp_f32 v89,v89
v_fma_f32 v86,v86,v69,v69
v_fma_f32 v87,v87,v69,v69
s_add_u32 s55,s55,s60
s_add_u32 s57,s57,32768
s_and_b32 s57,s57,131071
s_add_u32 s54,s54,s46
v_fma_f32 v88,v88,v69,v69
v_fma_f32 v89,v89,v69,v69
v_rcp_f32 v86,v86
v_rcp_f32 v87,v87
v_rcp_f32 v88,v88
v_rcp_f32 v89,v89
v_mul_f32 v22,v22,v86
v_mul_f32 v23,v23,v87
v_mul_f32 v24,v24,v88
v_mul_f32 v25,v25,v89
v_cvt_pk_bf16_f32 v92,v22,v23
v_cvt_pk_bf16_f32 v93,v24,v25
buffer_store_dwordx4 v[90:93],v0,s[36:39],s66 offen offset:0 sc0 sc1
s_mov_b32 s72,s69
s_mov_b32 s69,0x7fff0000
s_mov_b32 s66,0x7fff0000
s_mov_b32 s76,0x7fff0000
s_branch Lg1_gox_%=
Lg1_xn0_%=:
s_cmp_ge_u32 s54,256
s_cbranch_scc1 Lg1_fin0_%=
s_add_u32 m0,s59,4096
s_nop 0
buffer_load_dword v5,s[36:39],s58 offen offset:0 sc1 lds
buffer_store_dword v4,v3,s[36:39],s71 offen offset:0 sc0 sc1
s_add_u32 m0,s59,5120
s_nop 0
buffer_load_dword v5,s[36:39],s58 offen offset:0 sc1 lds
buffer_store_dword v4,v3,s[36:39],s71 offen offset:0 sc0 sc1
s_mov_b32 s83,0
v_fma_f32 v18,-v66,v74,v18
v_fma_f32 v19,-v66,v75,v19
v_fma_f32 v20,-v66,v76,v20
v_fma_f32 v21,-v66,v77,v21
v_mul_f32 v82,v18,v18
v_mul_f32 v83,v19,v19
v_mul_f32 v84,v20,v20
v_mul_f32 v85,v21,v21
v_fma_f32 v82,v68,v82,v67
v_fma_f32 v83,v68,v83,v67
v_fma_f32 v84,v68,v84,v67
v_fma_f32 v85,v68,v85,v67
v_mul_f32 v82,v18,v82
v_mul_f32 v83,v19,v83
v_mul_f32 v84,v20,v84
v_mul_f32 v85,v21,v85
v_exp_f32 v82,v82
v_exp_f32 v83,v83
v_exp_f32 v84,v84
v_exp_f32 v85,v85
v_fma_f32 v82,v82,v69,v69
v_fma_f32 v83,v83,v69,v69
v_fma_f32 v84,v84,v69,v69
v_fma_f32 v85,v85,v69,v69
v_rcp_f32 v82,v82
v_rcp_f32 v83,v83
v_rcp_f32 v84,v84
v_rcp_f32 v85,v85
v_mul_f32 v18,v18,v82
v_mul_f32 v19,v19,v83
v_mul_f32 v20,v20,v84
v_mul_f32 v21,v21,v85
v_cvt_pk_bf16_f32 v90,v18,v19
v_cvt_pk_bf16_f32 v91,v20,v21
v_fma_f32 v22,-v66,v78,v22
v_fma_f32 v23,-v66,v79,v23
v_fma_f32 v24,-v66,v80,v24
v_fma_f32 v25,-v66,v81,v25
v_mul_f32 v86,v22,v22
v_mul_f32 v87,v23,v23
v_mul_f32 v88,v24,v24
v_mul_f32 v89,v25,v25
v_fma_f32 v86,v68,v86,v67
v_fma_f32 v87,v68,v87,v67
v_fma_f32 v88,v68,v88,v67
v_fma_f32 v89,v68,v89,v67
v_mul_f32 v86,v22,v86
v_mul_f32 v87,v23,v87
v_mul_f32 v88,v24,v88
v_mul_f32 v89,v25,v89
v_exp_f32 v86,v86
v_exp_f32 v87,v87
v_exp_f32 v88,v88
v_exp_f32 v89,v89
v_fma_f32 v86,v86,v69,v69
v_fma_f32 v87,v87,v69,v69
v_fma_f32 v88,v88,v69,v69
v_fma_f32 v89,v89,v69,v69
v_rcp_f32 v86,v86
v_rcp_f32 v87,v87
v_rcp_f32 v88,v88
v_rcp_f32 v89,v89
v_mul_f32 v22,v22,v86
v_mul_f32 v23,v23,v87
v_mul_f32 v24,v24,v88
v_mul_f32 v25,v25,v89
v_cvt_pk_bf16_f32 v92,v22,v23
v_cvt_pk_bf16_f32 v93,v24,v25
buffer_store_dwordx4 v[90:93],v0,s[36:39],s66 offen offset:0 sc0 sc1
s_mov_b32 s72,s69
s_mov_b32 s69,0x7fff0000
s_mov_b32 s66,0x7fff0000
s_mov_b32 s76,0x7fff0000
s_branch Lg1_poll_%=
Lg1_fin0_%=:
v_fma_f32 v18,-v66,v74,v18
v_fma_f32 v19,-v66,v75,v19
v_fma_f32 v20,-v66,v76,v20
v_fma_f32 v21,-v66,v77,v21
v_mul_f32 v82,v18,v18
v_mul_f32 v83,v19,v19
v_mul_f32 v84,v20,v20
)"
R"(v_mul_f32 v85,v21,v21
v_fma_f32 v82,v68,v82,v67
v_fma_f32 v83,v68,v83,v67
v_fma_f32 v84,v68,v84,v67
v_fma_f32 v85,v68,v85,v67
v_mul_f32 v82,v18,v82
v_mul_f32 v83,v19,v83
v_mul_f32 v84,v20,v84
v_mul_f32 v85,v21,v85
v_exp_f32 v82,v82
v_exp_f32 v83,v83
v_exp_f32 v84,v84
v_exp_f32 v85,v85
v_fma_f32 v82,v82,v69,v69
v_fma_f32 v83,v83,v69,v69
v_fma_f32 v84,v84,v69,v69
v_fma_f32 v85,v85,v69,v69
v_rcp_f32 v82,v82
v_rcp_f32 v83,v83
v_rcp_f32 v84,v84
v_rcp_f32 v85,v85
v_mul_f32 v18,v18,v82
v_mul_f32 v19,v19,v83
v_mul_f32 v20,v20,v84
v_mul_f32 v21,v21,v85
v_cvt_pk_bf16_f32 v90,v18,v19
v_cvt_pk_bf16_f32 v91,v20,v21
v_fma_f32 v22,-v66,v78,v22
v_fma_f32 v23,-v66,v79,v23
v_fma_f32 v24,-v66,v80,v24
v_fma_f32 v25,-v66,v81,v25
v_mul_f32 v86,v22,v22
v_mul_f32 v87,v23,v23
v_mul_f32 v88,v24,v24
v_mul_f32 v89,v25,v25
v_fma_f32 v86,v68,v86,v67
v_fma_f32 v87,v68,v87,v67
v_fma_f32 v88,v68,v88,v67
v_fma_f32 v89,v68,v89,v67
v_mul_f32 v86,v22,v86
v_mul_f32 v87,v23,v87
v_mul_f32 v88,v24,v88
v_mul_f32 v89,v25,v89
v_exp_f32 v86,v86
v_exp_f32 v87,v87
v_exp_f32 v88,v88
v_exp_f32 v89,v89
v_fma_f32 v86,v86,v69,v69
v_fma_f32 v87,v87,v69,v69
v_fma_f32 v88,v88,v69,v69
v_fma_f32 v89,v89,v69,v69
v_rcp_f32 v86,v86
v_rcp_f32 v87,v87
v_rcp_f32 v88,v88
v_rcp_f32 v89,v89
v_mul_f32 v22,v22,v86
v_mul_f32 v23,v23,v87
v_mul_f32 v24,v24,v88
v_mul_f32 v25,v25,v89
v_cvt_pk_bf16_f32 v92,v22,v23
v_cvt_pk_bf16_f32 v93,v24,v25
buffer_store_dwordx4 v[90:93],v0,s[36:39],s66 offen offset:0 sc0 sc1
s_mov_b32 s72,s69
s_mov_b32 s69,0x7fff0000
s_mov_b32 s66,0x7fff0000
s_mov_b32 s76,0x7fff0000
s_branch Lg1_done_%=
Lg1_L21_%=:
v_mfma_f32_16x16x32_bf16 v[26:29],a[0:3],v[34:37],0
s_add_u32 s58,s58,s61
s_add_u32 s58,s58,s61
R 62,2,7168
v_mfma_f32_16x16x32_bf16 v[30:33],a[4:7],v[34:37],0
s_add_u32 m0,s59,1024
buffer_load_dwordx4 v[70:73],v11,s[36:39],s73 offen offset:0 sc1
M 26,a[8:11],38
s_add_u32 s73,s73,s64
R 34,2,8192
v_fma_f32 v18,-v66,v74,v18
M 30,a[12:15],38
L 5
buffer_load_dword v8,s[36:39],s58 offen offset:0 sc1 lds
M 26,a[16:19],42
s_mov_b32 m0,s57
R 38,2,9216
v_fma_f32 v19,-v66,v75,v19
M 30,a[20:23],42
s_add_u32 s56,s55,4096
v_fma_f32 v20,-v66,v76,v20
v_fma_f32 v21,-v66,v77,v21
M 26,a[24:27],46
s_mov_b32 s67,s65
R 42,2,10240
v_mul_f32 v82,v18,v18
M 30,a[28:31],46
L 5
D 0,55,0
M 26,a[32:35],50
R 46,2,11264
v_mul_f32 v83,v19,v19
M 30,a[36:39],50
v_mul_f32 v84,v20,v20
v_mul_f32 v85,v21,v21
M 26,a[40:43],54
R 50,2,12288
v_fma_f32 v82,v68,v82,v67
M 30,a[44:47],54
L 5
D 0,55,1024
M 26,a[48:51],58
R 54,2,13312
v_fma_f32 v83,v68,v83,v67
M 30,a[52:55],58
v_fma_f32 v84,v68,v84,v67
v_fma_f32 v85,v68,v85,v67
M 26,a[56:59],62
R 58,2,14336
v_mul_f32 v82,v18,v82
M 30,a[60:63],62
L 5
D 0,55,2048
M 26,a[64:67],34
R 62,2,15360
v_mul_f32 v83,v19,v83
M 30,a[68:71],34
v_exp_f32 v82,v82
M 26,a[72:75],38
R 34,2,16384
v_mul_f32 v84,v20,v84
M 30,a[76:79],38
L 5
D 0,55,3072
M 26,a[80:83],42
s_add_u32 m0,s57,4096
R 38,2,17408
v_mul_f32 v85,v21,v85
M 30,a[84:87],42
s_add_u32 s55,s55,s60
v_exp_f32 v83,v83
M 26,a[88:91],46
s_add_u32 s57,s57,32768
R 42,2,18432
v_fma_f32 v82,v82,v69,v69
M 30,a[92:95],46
L 5
D 0,56,0
M 26,a[96:99],50
s_and_b32 s57,s57,131071
R 46,2,19456
v_fma_f32 v83,v83,v69,v69
M 30,a[100:103],50
s_add_u32 s54,s54,s46
v_exp_f32 v84,v84
M 26,a[104:107],54
R 50,2,20480
v_fma_f32 v84,v84,v69,v69
M 30,a[108:111],54
L 5
D 0,56,1024
M 26,a[112:115],58
R 54,2,21504
v_fma_f32 v22,-v66,v78,v22
M 30,a[116:119],58
v_exp_f32 v85,v85
M 26,a[120:123],62
R 58,2,22528
v_fma_f32 v85,v85,v69,v69
M 30,a[124:127],62
L 5
D 0,56,2048
M 26,a[128:131],34
R 62,2,23552
v_fma_f32 v23,-v66,v79,v23
M 30,a[132:135],34
v_rcp_f32 v82,v82
M 26,a[136:139],38
R 34,2,24576
v_mul_f32 v18,v18,v82
M 30,a[140:143],38
L 5
D 0,56,3072
M 26,a[144:147],42
s_mov_b32 m0,s57
R 38,2,25600
v_fma_f32 v24,-v66,v80,v24
M 30,a[148:151],42
s_add_u32 s56,s55,4096
v_rcp_f32 v83,v83
M 26,a[152:155],46
R 42,2,26624
v_mul_f32 v19,v19,v83
M 30,a[156:159],46
L 5
D 0,55,0
M 26,a[160:163],50
R 46,2,27648
v_cvt_pk_bf16_f32 v90,v18,v19
M 30,a[164:167],50
D 0,55,1024
M 26,a[168:171],54
R 50,2,28672
v_fma_f32 v25,-v66,v81,v25
M 30,a[172:175],54
L 5
v_rcp_f32 v84,v84
M 26,a[176:179],58
R 54,2,29696
v_mul_f32 v20,v20,v84
M 30,a[180:183],58
D 0,55,2048
M 26,a[184:187],62
R 58,2,30720
v_mul_f32 v86,v22,v22
M 30,a[188:191],62
L 5
D 0,55,3072
M 26,a[192:195],34
s_waitcnt vmcnt(4)
R 62,2,31744
v_mul_f32 v87,v23,v23
M 30,a[196:199],34
s_barrier
ds_read_b32 v7,v6 offset:1024
v_mul_f32 v88,v24,v24
M 26,a[200:203],38
s_add_u32 m0,s57,4096
R 34,1,0
v_mul_f32 v89,v25,v25
M 30,a[204:207],38
L 6
buffer_store_dword v4,v3,s[36:39],s70 offen offset:0 sc0 sc1
M 26,a[208:211],42
s_add_u32 s55,s55,s60
R 38,1,1024
v_fma_f32 v86,v68,v86,v67
M 30,a[212:215],42
s_add_u32 s57,s57,32768
D 0,56,0
M 26,a[216:219],46
s_and_b32 s57,s57,131071
R 42,1,2048
v_fma_f32 v87,v68,v87,v67
M 30,a[220:223],46
L 6
D 0,56,1024
M 26,a[224:227],50
s_add_u32 s54,s54,s46
R 46,1,3072
v_fma_f32 v88,v68,v88,v67
M 30,a[228:231],50
s_mov_b32 s70,s69
D 0,56,2048
M 26,a[232:235],54
s_add_u32 s65,s65,s62
R 50,1,4096
v_fma_f32 v89,v68,v89,v67
M 30,a[236:239],54
L 6
D 0,56,3072
M 26,a[240:243],58
s_mov_b32 s69,s68
R 54,1,5120
v_mul_f32 v86,v22,v86
v_mul_f32 v87,v23,v87
v_mul_f32 v88,v24,v88
v_mul_f32 v89,v25,v89
v_exp_f32 v86,v86
v_exp_f32 v87,v87
v_exp_f32 v88,v88
v_exp_f32 v89,v89
M 30,a[244:247],58
L 6
v_rcp_f32 v85,v85
v_fma_f32 v86,v86,v69,v69
v_fma_f32 v87,v87,v69,v69
v_fma_f32 v88,v88,v69,v69
v_fma_f32 v89,v89,v69,v69
v_rcp_f32 v86,v86
v_rcp_f32 v87,v87
v_rcp_f32 v88,v88
v_rcp_f32 v89,v89
v_cmp_eq_u32 vcc,s53,v7
M 26,a[248:251],62
s_add_u32 s68,s68,s63
s_cmp_eq_u32 vcc_lo,-1
R 58,1,6144
v_mul_f32 v21,v21,v85
v_cvt_pk_bf16_f32 v91,v20,v21
v_mul_f32 v22,v22,v86
v_mul_f32 v23,v23,v87
v_mul_f32 v24,v24,v88
v_mul_f32 v25,v25,v89
v_cvt_pk_bf16_f32 v92,v22,v23
v_cvt_pk_bf16_f32 v93,v24,v25
M 30,a[252:255],62
L 5
buffer_store_dwordx4 v[90:93],v0,s[36:39],s66 offen offset:0 sc0 sc1
v_xor_b32 v2,65536,v2
s_cbranch_scc1 Lg1_S0_%=
Lg1_N0_%=:
v_mfma_f32_16x16x32_bf16 v[18:21],a[0:3],v[34:37],0
s_add_u32 m0,s59,0
s_mov_b32 s66,s65
R 62,1,7168
v_mfma_f32_16x16x32_bf16 v[22:25],a[4:7],v[34:37],0
buffer_load_dword v5,s[36:39],s58 offen offset:0 sc1 lds
M 18,a[8:11],38
R 34,1,8192
v_fma_f32 v26,-v70,v74,v26
M 22,a[12:15],38
L 5
buffer_load_dwordx4 v[66:69],v11,s[36:39],s73 offen offset:0 sc1
M 18,a[16:19],42
s_add_u32 s73,s73,s64
R 38,1,9216
v_fma_f32 v27,-v70,v75,v27
M 22,a[20:23],42
s_add_u32 s65,s65,s62
v_fma_f32 v28,-v70,v76,v28
v_fma_f32 v29,-v70,v77,v29
M 18,a[24:27],46
R 42,1,10240
v_mul_f32 v82,v26,v26
M 22,a[28:31],46
L 5
v_mul_f32 v83,v27,v27
v_mul_f32 v84,v28,v28
M 18,a[32:35],50
R 46,1,11264
v_mul_f32 v85,v29,v29
M 22,a[36:39],50
v_fma_f32 v82,v72,v82,v71
v_fma_f32 v83,v72,v83,v71
M 18,a[40:43],54
R 50,1,12288
v_fma_f32 v84,v72,v84,v71
M 22,a[44:47],54
L 5
v_fma_f32 v85,v72,v85,v71
v_mul_f32 v82,v26,v82
M 18,a[48:51],58
R 54,1,13312
v_mul_f32 v83,v27,v83
M 22,a[52:55],58
v_exp_f32 v82,v82
M 18,a[56:59],62
R 58,1,14336
v_mul_f32 v84,v28,v84
M 22,a[60:63],62
L 5
v_exp_f32 v83,v83
M 18,a[64:67],34
R 62,1,15360
v_mul_f32 v85,v29,v85
M 22,a[68:71],34
v_exp_f32 v84,v84
M 18,a[72:75],38
R 34,1,16384
v_fma_f32 v82,v82,v73,v73
M 22,a[76:79],38
L 5
v_exp_f32 v85,v85
M 18,a[80:83],42
R 38,1,17408
v_fma_f32 v83,v83,v73,v73
M 22,a[84:87],42
v_rcp_f32 v82,v82
M 18,a[88:91],46
R 42,1,18432
v_fma_f32 v84,v84,v73,v73
M 22,a[92:95],46
L 5
v_rcp_f32 v83,v83
M 18,a[96:99],50
R 46,1,19456
v_fma_f32 v85,v85,v73,v73
M 22,a[100:103],50
v_rcp_f32 v84,v84
M 18,a[104:107],54
R 50,1,20480
v_mul_f32 v26,v26,v82
M 22,a[108:111],54
L 5
v_rcp_f32 v85,v85
M 18,a[112:115],58
R 54,1,21504
v_mul_f32 v27,v27,v83
M 22,a[116:119],58
v_mul_f32 v28,v28,v84
v_mul_f32 v29,v29,v85
M 18,a[120:123],62
R 58,1,22528
v_cvt_pk_bf16_f32 v90,v26,v27
M 22,a[124:127],62
L 5
v_cvt_pk_bf16_f32 v91,v28,v29
v_fma_f32 v30,-v70,v78,v30
M 18,a[128:131],34
R 62,1,23552
v_fma_f32 v31,-v70,v79,v31
M 22,a[132:135],34
v_fma_f32 v32,-v70,v80,v32
v_fma_f32 v33,-v70,v81,v33
M 18,a[136:139],38
R 34,1,24576
v_mul_f32 v86,v30,v30
M 22,a[140:143],38
L 5
v_mul_f32 v87,v31,v31
v_mul_f32 v88,v32,v32
M 18,a[144:147],42
R 38,1,25600
v_mul_f32 v89,v33,v33
M 22,a[148:151],42
v_fma_f32 v86,v72,v86,v71
v_fma_f32 v87,v72,v87,v71
M 18,a[152:155],46
R 42,1,26624
v_fma_f32 v88,v72,v88,v71
M 22,a[156:159],46
L 5
v_fma_f32 v89,v72,v89,v71
v_mul_f32 v86,v30,v86
M 18,a[160:163],50
R 46,1,27648
v_mul_f32 v87,v31,v87
M 22,a[164:167],50
v_exp_f32 v86,v86
M 18,a[168:171],54
R 50,1,28672
v_mul_f32 v88,v32,v88
M 22,a[172:175],54
L 5
v_exp_f32 v87,v87
M 18,a[176:179],58
R 54,1,29696
v_mul_f32 v89,v33,v89
M 22,a[180:183],58
v_exp_f32 v88,v88
M 18,a[184:187],62
R 58,1,30720
v_fma_f32 v86,v86,v73,v73
M 22,a[188:191],62
L 5
v_exp_f32 v89,v89
M 18,a[192:195],34
s_waitcnt vmcnt(0)
R 62,1,31744
v_fma_f32 v87,v87,v73,v73
M 22,a[196:199],34
s_barrier
ds_read_b32 v7,v6 offset:0
v_fma_f32 v88,v88,v73,v73
M 18,a[200:203],38
R 34,2,0
v_fma_f32 v89,v89,v73,v73
M 22,a[204:207],38
L 6
buffer_store_dword v4,v3,s[36:39],s70 offen offset:0 sc0 sc1
M 18,a[208:211],42
s_mov_b32 s70,s69
R 38,2,1024
v_xor_b32 v1,65536,v1
M 22,a[212:215],42
s_mov_b32 s69,s68
v_rcp_f32 v86,v86
M 18,a[216:219],46
L 2
R 42,2,2048
v_mul_f32 v30,v30,v86
M 22,a[220:223],46
s_nop 0
v_rcp_f32 v87,v87
M 18,a[224:227],50
s_add_u32 s68,s68,s63
R 46,2,3072
v_mul_f32 v31,v31,v87
M 22,a[228:231],50
v_rcp_f32 v88,v88
M 18,a[232:235],54
R 50,2,4096
v_mul_f32 v32,v32,v88
M 22,a[236:239],54
s_nop 0
v_rcp_f32 v89,v89
M 18,a[240:243],58
R 54,2,5120
v_mul_f32 v33,v33,v89
M 22,a[244:247],58
v_cvt_pk_bf16_f32 v92,v30,v31
v_cmp_eq_u32 vcc,s53,v7
M 18,a[248:251],62
s_cmp_eq_u64 vcc,-1
R 58,2,6144
v_cvt_pk_bf16_f32 v93,v32,v33
M 22,a[252:255],62
L 5
buffer_store_dwordx4 v[90:93],v0,s[36:39],s67 offen offset:0 sc0 sc1
s_cbranch_scc1 Lg1_L21_%=
s_cmp_eq_u32 vcc_lo,-1
)"
R"(s_cbranch_scc1 Lg1_L11_%=
Lg1_X1_%=:
v_mfma_f32_16x16x32_bf16 v[26:29],a[0:3],v[34:37],0
s_add_u32 m0,s59,1024
s_mov_b32 s67,s65
R 62,2,7168
v_mfma_f32_16x16x32_bf16 v[30:33],a[4:7],v[34:37],0
buffer_load_dword v5,s[36:39],s58 offen offset:0 sc1 lds
M 26,a[8:11],38
R 34,2,8192
v_fma_f32 v18,-v66,v74,v18
M 30,a[12:15],38
L 5
buffer_load_dwordx4 v[70:73],v11,s[36:39],s73 offen offset:0 sc1
M 26,a[16:19],42
s_add_u32 s73,s73,s64
R 38,2,9216
v_fma_f32 v19,-v66,v75,v19
M 30,a[20:23],42
s_add_u32 s65,s65,s62
v_fma_f32 v20,-v66,v76,v20
v_fma_f32 v21,-v66,v77,v21
M 26,a[24:27],46
R 42,2,10240
v_mul_f32 v82,v18,v18
M 30,a[28:31],46
L 5
v_mul_f32 v83,v19,v19
v_mul_f32 v84,v20,v20
M 26,a[32:35],50
R 46,2,11264
v_mul_f32 v85,v21,v21
M 30,a[36:39],50
v_fma_f32 v82,v68,v82,v67
v_fma_f32 v83,v68,v83,v67
M 26,a[40:43],54
R 50,2,12288
v_fma_f32 v84,v68,v84,v67
M 30,a[44:47],54
L 5
v_fma_f32 v85,v68,v85,v67
v_mul_f32 v82,v18,v82
M 26,a[48:51],58
R 54,2,13312
v_mul_f32 v83,v19,v83
M 30,a[52:55],58
v_exp_f32 v82,v82
M 26,a[56:59],62
R 58,2,14336
v_mul_f32 v84,v20,v84
M 30,a[60:63],62
L 5
v_exp_f32 v83,v83
M 26,a[64:67],34
R 62,2,15360
v_mul_f32 v85,v21,v85
M 30,a[68:71],34
v_exp_f32 v84,v84
M 26,a[72:75],38
R 34,2,16384
v_fma_f32 v82,v82,v69,v69
M 30,a[76:79],38
L 5
v_exp_f32 v85,v85
M 26,a[80:83],42
R 38,2,17408
v_fma_f32 v83,v83,v69,v69
M 30,a[84:87],42
v_rcp_f32 v82,v82
M 26,a[88:91],46
R 42,2,18432
v_fma_f32 v84,v84,v69,v69
M 30,a[92:95],46
L 5
v_rcp_f32 v83,v83
M 26,a[96:99],50
R 46,2,19456
v_fma_f32 v85,v85,v69,v69
M 30,a[100:103],50
v_rcp_f32 v84,v84
M 26,a[104:107],54
R 50,2,20480
v_mul_f32 v18,v18,v82
M 30,a[108:111],54
L 5
v_rcp_f32 v85,v85
M 26,a[112:115],58
R 54,2,21504
v_mul_f32 v19,v19,v83
M 30,a[116:119],58
v_mul_f32 v20,v20,v84
v_mul_f32 v21,v21,v85
M 26,a[120:123],62
R 58,2,22528
v_cvt_pk_bf16_f32 v90,v18,v19
M 30,a[124:127],62
L 5
v_cvt_pk_bf16_f32 v91,v20,v21
v_fma_f32 v22,-v66,v78,v22
M 26,a[128:131],34
R 62,2,23552
v_fma_f32 v23,-v66,v79,v23
M 30,a[132:135],34
v_fma_f32 v24,-v66,v80,v24
v_fma_f32 v25,-v66,v81,v25
M 26,a[136:139],38
R 34,2,24576
v_mul_f32 v86,v22,v22
M 30,a[140:143],38
L 5
v_mul_f32 v87,v23,v23
v_mul_f32 v88,v24,v24
M 26,a[144:147],42
R 38,2,25600
v_mul_f32 v89,v25,v25
M 30,a[148:151],42
v_fma_f32 v86,v68,v86,v67
v_fma_f32 v87,v68,v87,v67
M 26,a[152:155],46
R 42,2,26624
v_fma_f32 v88,v68,v88,v67
M 30,a[156:159],46
L 5
v_fma_f32 v89,v68,v89,v67
v_mul_f32 v86,v22,v86
M 26,a[160:163],50
R 46,2,27648
v_mul_f32 v87,v23,v87
M 30,a[164:167],50
v_exp_f32 v86,v86
M 26,a[168:171],54
R 50,2,28672
v_mul_f32 v88,v24,v88
M 30,a[172:175],54
L 5
v_exp_f32 v87,v87
M 26,a[176:179],58
R 54,2,29696
v_mul_f32 v89,v25,v89
M 30,a[180:183],58
v_exp_f32 v88,v88
M 26,a[184:187],62
R 58,2,30720
v_fma_f32 v86,v86,v69,v69
M 30,a[188:191],62
L 5
v_exp_f32 v89,v89
M 26,a[192:195],34
s_waitcnt vmcnt(0)
R 62,2,31744
v_fma_f32 v87,v87,v69,v69
M 30,a[196:199],34
s_barrier
ds_read_b32 v7,v6 offset:1024
v_fma_f32 v88,v88,v69,v69
M 26,a[200:203],38
buffer_store_dword v4,v3,s[36:39],s70 offen offset:0 sc0 sc1
M 30,a[204:207],38
L 5
v_rcp_f32 v86,v86
M 26,a[208:211],42
s_mov_b32 s70,s69
v_rcp_f32 v87,v87
M 30,a[212:215],42
s_mov_b32 s69,s68
v_rcp_f32 v88,v88
M 26,a[216:219],46
L 0
v_fma_f32 v89,v89,v69,v69
v_mul_f32 v22,v22,v86
M 30,a[220:223],46
s_nop 0
v_rcp_f32 v89,v89
M 26,a[224:227],50
s_add_u32 s68,s68,s63
v_mul_f32 v23,v23,v87
v_mul_f32 v24,v24,v88
M 30,a[228:231],50
v_mul_f32 v25,v25,v89
v_cvt_pk_bf16_f32 v92,v22,v23
M 26,a[232:235],54
v_cvt_pk_bf16_f32 v93,v24,v25
v_xor_b32 v2,65536,v2
M 30,a[236:239],54
s_nop 0
buffer_store_dwordx4 v[90:93],v0,s[36:39],s66 offen offset:0 sc0 sc1
v_cmp_eq_u32 vcc,s53,v7
M 26,a[240:243],58
M 30,a[244:247],58
M 26,a[248:251],62
M 30,a[252:255],62
s_mov_b32 s80,0
s_cmp_eq_u32 vcc_lo,-1
s_cbranch_scc0 Lg1_xn1_%=
s_mov_b32 m0,s57
s_add_u32 s56,s55,4096
D 0,55,0
s_nop 0
v_fma_f32 v26,-v70,v74,v26
v_fma_f32 v27,-v70,v75,v27
v_fma_f32 v28,-v70,v76,v28
v_fma_f32 v29,-v70,v77,v29
v_mul_f32 v82,v26,v26
v_mul_f32 v83,v27,v27
v_mul_f32 v84,v28,v28
D 0,55,1024
v_mul_f32 v85,v29,v29
v_fma_f32 v82,v72,v82,v71
v_fma_f32 v83,v72,v83,v71
v_fma_f32 v84,v72,v84,v71
v_fma_f32 v85,v72,v85,v71
v_mul_f32 v82,v26,v82
v_mul_f32 v83,v27,v83
D 0,55,2048
v_mul_f32 v84,v28,v84
v_mul_f32 v85,v29,v85
v_exp_f32 v82,v82
v_exp_f32 v83,v83
v_exp_f32 v84,v84
v_exp_f32 v85,v85
v_fma_f32 v82,v82,v73,v73
D 0,55,3072
v_fma_f32 v83,v83,v73,v73
v_fma_f32 v84,v84,v73,v73
v_fma_f32 v85,v85,v73,v73
v_rcp_f32 v82,v82
v_rcp_f32 v83,v83
v_rcp_f32 v84,v84
v_rcp_f32 v85,v85
s_add_u32 m0,s57,4096
s_nop 0
D 0,56,0
v_mul_f32 v26,v26,v82
v_mul_f32 v27,v27,v83
v_mul_f32 v28,v28,v84
v_mul_f32 v29,v29,v85
v_cvt_pk_bf16_f32 v90,v26,v27
v_cvt_pk_bf16_f32 v91,v28,v29
v_fma_f32 v30,-v70,v78,v30
D 0,56,1024
v_fma_f32 v31,-v70,v79,v31
v_fma_f32 v32,-v70,v80,v32
v_fma_f32 v33,-v70,v81,v33
v_mul_f32 v86,v30,v30
v_mul_f32 v87,v31,v31
v_mul_f32 v88,v32,v32
v_mul_f32 v89,v33,v33
D 0,56,2048
v_fma_f32 v86,v72,v86,v71
v_fma_f32 v87,v72,v87,v71
v_fma_f32 v88,v72,v88,v71
v_fma_f32 v89,v72,v89,v71
v_mul_f32 v86,v30,v86
v_mul_f32 v87,v31,v87
v_mul_f32 v88,v32,v88
D 0,56,3072
v_mul_f32 v89,v33,v89
v_exp_f32 v86,v86
v_exp_f32 v87,v87
v_exp_f32 v88,v88
v_exp_f32 v89,v89
v_fma_f32 v86,v86,v73,v73
v_fma_f32 v87,v87,v73,v73
s_add_u32 s55,s55,s60
s_add_u32 s57,s57,32768
s_and_b32 s57,s57,131071
s_add_u32 s54,s54,s46
v_fma_f32 v88,v88,v73,v73
v_fma_f32 v89,v89,v73,v73
v_rcp_f32 v86,v86
v_rcp_f32 v87,v87
v_rcp_f32 v88,v88
v_rcp_f32 v89,v89
v_mul_f32 v30,v30,v86
v_mul_f32 v31,v31,v87
v_mul_f32 v32,v32,v88
v_mul_f32 v33,v33,v89
v_cvt_pk_bf16_f32 v92,v30,v31
v_cvt_pk_bf16_f32 v93,v32,v33
buffer_store_dwordx4 v[90:93],v0,s[36:39],s67 offen offset:0 sc0 sc1
s_mov_b32 s72,s69
s_mov_b32 s69,0x7fff0000
s_mov_b32 s67,0x7fff0000
s_mov_b32 s77,0x7fff0000
s_branch Lg1_gox_%=
Lg1_xn1_%=:
s_cmp_ge_u32 s54,256
s_cbranch_scc1 Lg1_fin1_%=
s_add_u32 m0,s59,4096
s_nop 0
buffer_load_dword v5,s[36:39],s58 offen offset:0 sc1 lds
buffer_store_dword v4,v3,s[36:39],s71 offen offset:0 sc0 sc1
s_add_u32 m0,s59,5120
s_nop 0
buffer_load_dword v5,s[36:39],s58 offen offset:0 sc1 lds
buffer_store_dword v4,v3,s[36:39],s71 offen offset:0 sc0 sc1
s_mov_b32 s83,0
v_fma_f32 v26,-v70,v74,v26
v_fma_f32 v27,-v70,v75,v27
v_fma_f32 v28,-v70,v76,v28
v_fma_f32 v29,-v70,v77,v29
v_mul_f32 v82,v26,v26
v_mul_f32 v83,v27,v27
v_mul_f32 v84,v28,v28
v_mul_f32 v85,v29,v29
v_fma_f32 v82,v72,v82,v71
v_fma_f32 v83,v72,v83,v71
v_fma_f32 v84,v72,v84,v71
v_fma_f32 v85,v72,v85,v71
v_mul_f32 v82,v26,v82
v_mul_f32 v83,v27,v83
v_mul_f32 v84,v28,v84
v_mul_f32 v85,v29,v85
v_exp_f32 v82,v82
v_exp_f32 v83,v83
v_exp_f32 v84,v84
v_exp_f32 v85,v85
v_fma_f32 v82,v82,v73,v73
v_fma_f32 v83,v83,v73,v73
v_fma_f32 v84,v84,v73,v73
v_fma_f32 v85,v85,v73,v73
v_rcp_f32 v82,v82
v_rcp_f32 v83,v83
v_rcp_f32 v84,v84
v_rcp_f32 v85,v85
v_mul_f32 v26,v26,v82
v_mul_f32 v27,v27,v83
v_mul_f32 v28,v28,v84
v_mul_f32 v29,v29,v85
v_cvt_pk_bf16_f32 v90,v26,v27
v_cvt_pk_bf16_f32 v91,v28,v29
v_fma_f32 v30,-v70,v78,v30
v_fma_f32 v31,-v70,v79,v31
v_fma_f32 v32,-v70,v80,v32
v_fma_f32 v33,-v70,v81,v33
v_mul_f32 v86,v30,v30
v_mul_f32 v87,v31,v31
v_mul_f32 v88,v32,v32
v_mul_f32 v89,v33,v33
v_fma_f32 v86,v72,v86,v71
v_fma_f32 v87,v72,v87,v71
v_fma_f32 v88,v72,v88,v71
v_fma_f32 v89,v72,v89,v71
v_mul_f32 v86,v30,v86
v_mul_f32 v87,v31,v87
v_mul_f32 v88,v32,v88
v_mul_f32 v89,v33,v89
v_exp_f32 v86,v86
v_exp_f32 v87,v87
v_exp_f32 v88,v88
v_exp_f32 v89,v89
v_fma_f32 v86,v86,v73,v73
v_fma_f32 v87,v87,v73,v73
v_fma_f32 v88,v88,v73,v73
v_fma_f32 v89,v89,v73,v73
v_rcp_f32 v86,v86
v_rcp_f32 v87,v87
v_rcp_f32 v88,v88
v_rcp_f32 v89,v89
v_mul_f32 v30,v30,v86
v_mul_f32 v31,v31,v87
v_mul_f32 v32,v32,v88
v_mul_f32 v33,v33,v89
v_cvt_pk_bf16_f32 v92,v30,v31
v_cvt_pk_bf16_f32 v93,v32,v33
buffer_store_dwordx4 v[90:93],v0,s[36:39],s67 offen offset:0 sc0 sc1
s_mov_b32 s72,s69
s_mov_b32 s69,0x7fff0000
s_mov_b32 s67,0x7fff0000
s_mov_b32 s77,0x7fff0000
s_branch Lg1_poll_%=
Lg1_fin1_%=:
v_fma_f32 v26,-v70,v74,v26
v_fma_f32 v27,-v70,v75,v27
v_fma_f32 v28,-v70,v76,v28
v_fma_f32 v29,-v70,v77,v29
v_mul_f32 v82,v26,v26
v_mul_f32 v83,v27,v27
v_mul_f32 v84,v28,v28
v_mul_f32 v85,v29,v29
v_fma_f32 v82,v72,v82,v71
v_fma_f32 v83,v72,v83,v71
v_fma_f32 v84,v72,v84,v71
v_fma_f32 v85,v72,v85,v71
v_mul_f32 v82,v26,v82
v_mul_f32 v83,v27,v83
v_mul_f32 v84,v28,v84
v_mul_f32 v85,v29,v85
v_exp_f32 v82,v82
v_exp_f32 v83,v83
v_exp_f32 v84,v84
v_exp_f32 v85,v85
v_fma_f32 v82,v82,v73,v73
v_fma_f32 v83,v83,v73,v73
v_fma_f32 v84,v84,v73,v73
v_fma_f32 v85,v85,v73,v73
v_rcp_f32 v82,v82
v_rcp_f32 v83,v83
v_rcp_f32 v84,v84
v_rcp_f32 v85,v85
v_mul_f32 v26,v26,v82
v_mul_f32 v27,v27,v83
v_mul_f32 v28,v28,v84
v_mul_f32 v29,v29,v85
v_cvt_pk_bf16_f32 v90,v26,v27
v_cvt_pk_bf16_f32 v91,v28,v29
v_fma_f32 v30,-v70,v78,v30
v_fma_f32 v31,-v70,v79,v31
v_fma_f32 v32,-v70,v80,v32
v_fma_f32 v33,-v70,v81,v33
v_mul_f32 v86,v30,v30
v_mul_f32 v87,v31,v31
v_mul_f32 v88,v32,v32
v_mul_f32 v89,v33,v33
v_fma_f32 v86,v72,v86,v71
v_fma_f32 v87,v72,v87,v71
v_fma_f32 v88,v72,v88,v71
v_fma_f32 v89,v72,v89,v71
v_mul_f32 v86,v30,v86
v_mul_f32 v87,v31,v87
v_mul_f32 v88,v32,v88
v_mul_f32 v89,v33,v89
v_exp_f32 v86,v86
v_exp_f32 v87,v87
v_exp_f32 v88,v88
v_exp_f32 v89,v89
v_fma_f32 v86,v86,v73,v73
v_fma_f32 v87,v87,v73,v73
v_fma_f32 v88,v88,v73,v73
v_fma_f32 v89,v89,v73,v73
v_rcp_f32 v86,v86
v_rcp_f32 v87,v87
v_rcp_f32 v88,v88
v_rcp_f32 v89,v89
v_mul_f32 v30,v30,v86
v_mul_f32 v31,v31,v87
v_mul_f32 v32,v32,v88
v_mul_f32 v33,v33,v89
v_cvt_pk_bf16_f32 v92,v30,v31
v_cvt_pk_bf16_f32 v93,v32,v33
buffer_store_dwordx4 v[90:93],v0,s[36:39],s67 offen offset:0 sc0 sc1
s_mov_b32 s72,s69
s_mov_b32 s69,0x7fff0000
s_mov_b32 s67,0x7fff0000
s_mov_b32 s77,0x7fff0000
s_branch Lg1_done_%=
Lg1_L10_%=:
v_mfma_f32_16x16x32_bf16 v[18:21],a[0:3],v[34:37],0
s_add_u32 s58,s58,s61
s_add_u32 m0,s59,0
R 62,1,7168
v_mfma_f32_16x16x32_bf16 v[22:25],a[4:7],v[34:37],0
s_mov_b32 s66,s65
buffer_load_dword v5,s[36:39],s58 offen offset:0 sc1 lds
M 18,a[8:11],38
s_mov_b32 m0,s57
R 34,1,8192
v_fma_f32 v26,-v70,v74,v26
M 22,a[12:15],38
L 5
buffer_load_dwordx4 v[66:69],v11,s[36:39],s73 offen offset:0 sc1
M 18,a[16:19],42
s_add_u32 s73,s73,s64
R 38,1,9216
v_fma_f32 v27,-v70,v75,v27
M 22,a[20:23],42
s_add_u32 s56,s55,4096
v_fma_f32 v28,-v70,v76,v28
v_fma_f32 v29,-v70,v77,v29
M 18,a[24:27],46
R 42,1,10240
v_mul_f32 v82,v26,v26
M 22,a[28:31],46
L 5
D 0,55,0
M 18,a[32:35],50
R 46,1,11264
v_mul_f32 v83,v27,v27
M 22,a[36:39],50
v_mul_f32 v84,v28,v28
v_mul_f32 v85,v29,v29
M 18,a[40:43],54
R 50,1,12288
v_fma_f32 v82,v72,v82,v71
M 22,a[44:47],54
L 5
D 0,55,1024
M 18,a[48:51],58
R 54,1,13312
v_fma_f32 v83,v72,v83,v71
M 22,a[52:55],58
v_fma_f32 v84,v72,v84,v71
v_fma_f32 v85,v72,v85,v71
M 18,a[56:59],62
R 58,1,14336
v_mul_f32 v82,v26,v82
M 22,a[60:63],62
L 5
D 0,55,2048
M 18,a[64:67],34
)"
R"(R 62,1,15360
v_mul_f32 v83,v27,v83
M 22,a[68:71],34
v_exp_f32 v82,v82
M 18,a[72:75],38
R 34,1,16384
v_mul_f32 v84,v28,v84
M 22,a[76:79],38
L 5
D 0,55,3072
M 18,a[80:83],42
s_add_u32 m0,s57,4096
R 38,1,17408
v_mul_f32 v85,v29,v85
M 22,a[84:87],42
s_add_u32 s55,s55,s60
v_exp_f32 v83,v83
M 18,a[88:91],46
s_add_u32 s57,s57,32768
R 42,1,18432
v_fma_f32 v82,v82,v73,v73
M 22,a[92:95],46
L 5
D 0,56,0
M 18,a[96:99],50
s_and_b32 s57,s57,131071
R 46,1,19456
v_fma_f32 v83,v83,v73,v73
M 22,a[100:103],50
s_add_u32 s54,s54,s46
v_exp_f32 v84,v84
M 18,a[104:107],54
s_add_u32 s65,s65,s62
R 50,1,20480
v_fma_f32 v84,v84,v73,v73
M 22,a[108:111],54
L 5
D 0,56,1024
M 18,a[112:115],58
R 54,1,21504
v_fma_f32 v30,-v70,v78,v30
M 22,a[116:119],58
v_exp_f32 v85,v85
M 18,a[120:123],62
R 58,1,22528
v_fma_f32 v85,v85,v73,v73
M 22,a[124:127],62
L 5
D 0,56,2048
M 18,a[128:131],34
R 62,1,23552
v_fma_f32 v31,-v70,v79,v31
M 22,a[132:135],34
v_rcp_f32 v82,v82
M 18,a[136:139],38
R 34,1,24576
v_mul_f32 v26,v26,v82
M 22,a[140:143],38
L 5
D 0,56,3072
M 18,a[144:147],42
R 38,1,25600
v_fma_f32 v32,-v70,v80,v32
M 22,a[148:151],42
v_rcp_f32 v83,v83
M 18,a[152:155],46
R 42,1,26624
v_mul_f32 v27,v27,v83
M 22,a[156:159],46
L 5
v_rcp_f32 v84,v84
M 18,a[160:163],50
R 46,1,27648
v_mul_f32 v28,v28,v84
M 22,a[164:167],50
v_rcp_f32 v85,v85
M 18,a[168:171],54
R 50,1,28672
v_mul_f32 v29,v29,v85
M 22,a[172:175],54
L 5
v_cvt_pk_bf16_f32 v90,v26,v27
v_cvt_pk_bf16_f32 v91,v28,v29
M 18,a[176:179],58
R 54,1,29696
v_fma_f32 v33,-v70,v81,v33
M 22,a[180:183],58
v_mul_f32 v86,v30,v30
v_mul_f32 v87,v31,v31
M 18,a[184:187],62
R 58,1,30720
v_mul_f32 v88,v32,v32
M 22,a[188:191],62
L 5
v_mul_f32 v89,v33,v33
v_fma_f32 v86,v72,v86,v71
M 18,a[192:195],34
s_waitcnt vmcnt(0)
R 62,1,31744
v_fma_f32 v87,v72,v87,v71
M 22,a[196:199],34
s_barrier
ds_read_b32 v7,v6 offset:0
v_fma_f32 v88,v72,v88,v71
M 18,a[200:203],38
R 34,2,0
v_fma_f32 v89,v72,v89,v71
M 22,a[204:207],38
L 6
buffer_store_dword v4,v3,s[36:39],s70 offen offset:0 sc0 sc1
M 18,a[208:211],42
s_mov_b32 s70,s69
R 38,2,1024
v_mul_f32 v86,v30,v86
M 22,a[212:215],42
s_mov_b32 s69,s68
v_exp_f32 v86,v86
M 18,a[216:219],46
L 2
R 42,2,2048
v_mul_f32 v87,v31,v87
M 22,a[220:223],46
s_nop 0
v_exp_f32 v87,v87
M 18,a[224:227],50
s_add_u32 s68,s68,s63
R 46,2,3072
v_mul_f32 v88,v32,v88
M 22,a[228:231],50
v_mul_f32 v89,v33,v89
v_fma_f32 v86,v86,v73,v73
M 18,a[232:235],54
R 50,2,4096
v_fma_f32 v87,v87,v73,v73
M 22,a[236:239],54
s_nop 0
v_exp_f32 v88,v88
M 18,a[240:243],58
R 54,2,5120
v_exp_f32 v89,v89
M 22,a[244:247],58
v_fma_f32 v88,v88,v73,v73
v_fma_f32 v89,v89,v73,v73
v_rcp_f32 v86,v86
v_rcp_f32 v87,v87
v_rcp_f32 v88,v88
v_rcp_f32 v89,v89
v_cmp_eq_u32 vcc,s53,v7
M 18,a[248:251],62
s_cmp_eq_u64 vcc,-1
R 58,2,6144
v_mul_f32 v30,v30,v86
v_mul_f32 v31,v31,v87
v_mul_f32 v32,v32,v88
v_mul_f32 v33,v33,v89
v_cvt_pk_bf16_f32 v92,v30,v31
v_cvt_pk_bf16_f32 v93,v32,v33
M 22,a[252:255],62
L 5
buffer_store_dwordx4 v[90:93],v0,s[36:39],s67 offen offset:0 sc0 sc1
v_xor_b32 v1,65536,v1
s_cbranch_scc1 Lg1_L21_%=
s_cmp_eq_u32 vcc_lo,-1
s_cbranch_scc1 Lg1_L11_%=
s_branch Lg1_X1_%=
Lg1_L11_%=:
v_mfma_f32_16x16x32_bf16 v[26:29],a[0:3],v[34:37],0
s_add_u32 s58,s58,s61
s_add_u32 m0,s59,1024
R 62,2,7168
v_mfma_f32_16x16x32_bf16 v[30:33],a[4:7],v[34:37],0
s_mov_b32 s67,s65
buffer_load_dword v5,s[36:39],s58 offen offset:0 sc1 lds
M 26,a[8:11],38
s_mov_b32 m0,s57
R 34,2,8192
v_fma_f32 v18,-v66,v74,v18
M 30,a[12:15],38
L 5
buffer_load_dwordx4 v[70:73],v11,s[36:39],s73 offen offset:0 sc1
M 26,a[16:19],42
s_add_u32 s73,s73,s64
R 38,2,9216
v_fma_f32 v19,-v66,v75,v19
M 30,a[20:23],42
s_add_u32 s56,s55,4096
v_fma_f32 v20,-v66,v76,v20
v_fma_f32 v21,-v66,v77,v21
M 26,a[24:27],46
R 42,2,10240
v_mul_f32 v82,v18,v18
M 30,a[28:31],46
L 5
D 0,55,0
M 26,a[32:35],50
R 46,2,11264
v_mul_f32 v83,v19,v19
M 30,a[36:39],50
v_mul_f32 v84,v20,v20
v_mul_f32 v85,v21,v21
M 26,a[40:43],54
R 50,2,12288
v_fma_f32 v82,v68,v82,v67
M 30,a[44:47],54
L 5
D 0,55,1024
M 26,a[48:51],58
R 54,2,13312
v_fma_f32 v83,v68,v83,v67
M 30,a[52:55],58
v_fma_f32 v84,v68,v84,v67
v_fma_f32 v85,v68,v85,v67
M 26,a[56:59],62
R 58,2,14336
v_mul_f32 v82,v18,v82
M 30,a[60:63],62
L 5
D 0,55,2048
M 26,a[64:67],34
R 62,2,15360
v_mul_f32 v83,v19,v83
M 30,a[68:71],34
v_exp_f32 v82,v82
M 26,a[72:75],38
R 34,2,16384
v_mul_f32 v84,v20,v84
M 30,a[76:79],38
L 5
D 0,55,3072
M 26,a[80:83],42
s_add_u32 m0,s57,4096
R 38,2,17408
v_mul_f32 v85,v21,v85
M 30,a[84:87],42
s_add_u32 s55,s55,s60
v_exp_f32 v83,v83
M 26,a[88:91],46
s_add_u32 s57,s57,32768
R 42,2,18432
v_fma_f32 v82,v82,v69,v69
M 30,a[92:95],46
L 5
D 0,56,0
M 26,a[96:99],50
s_and_b32 s57,s57,131071
R 46,2,19456
v_fma_f32 v83,v83,v69,v69
M 30,a[100:103],50
s_add_u32 s54,s54,s46
v_exp_f32 v84,v84
M 26,a[104:107],54
s_add_u32 s65,s65,s62
R 50,2,20480
v_fma_f32 v84,v84,v69,v69
M 30,a[108:111],54
L 5
D 0,56,1024
M 26,a[112:115],58
R 54,2,21504
v_fma_f32 v22,-v66,v78,v22
M 30,a[116:119],58
v_exp_f32 v85,v85
M 26,a[120:123],62
R 58,2,22528
v_fma_f32 v85,v85,v69,v69
M 30,a[124:127],62
L 5
D 0,56,2048
M 26,a[128:131],34
R 62,2,23552
v_fma_f32 v23,-v66,v79,v23
M 30,a[132:135],34
v_rcp_f32 v82,v82
M 26,a[136:139],38
R 34,2,24576
v_mul_f32 v18,v18,v82
M 30,a[140:143],38
L 5
D 0,56,3072
M 26,a[144:147],42
R 38,2,25600
v_fma_f32 v24,-v66,v80,v24
M 30,a[148:151],42
v_rcp_f32 v83,v83
M 26,a[152:155],46
R 42,2,26624
v_mul_f32 v19,v19,v83
M 30,a[156:159],46
L 5
v_rcp_f32 v84,v84
M 26,a[160:163],50
R 46,2,27648
v_mul_f32 v20,v20,v84
M 30,a[164:167],50
v_rcp_f32 v85,v85
M 26,a[168:171],54
R 50,2,28672
v_mul_f32 v21,v21,v85
M 30,a[172:175],54
L 5
v_cvt_pk_bf16_f32 v90,v18,v19
v_cvt_pk_bf16_f32 v91,v20,v21
M 26,a[176:179],58
R 54,2,29696
v_fma_f32 v25,-v66,v81,v25
M 30,a[180:183],58
v_mul_f32 v86,v22,v22
v_mul_f32 v87,v23,v23
M 26,a[184:187],62
R 58,2,30720
v_mul_f32 v88,v24,v24
M 30,a[188:191],62
L 5
v_mul_f32 v89,v25,v25
v_fma_f32 v86,v68,v86,v67
M 26,a[192:195],34
s_waitcnt vmcnt(0)
R 62,2,31744
v_fma_f32 v87,v68,v87,v67
M 30,a[196:199],34
s_barrier
ds_read_b32 v7,v6 offset:1024
v_fma_f32 v88,v68,v88,v67
M 26,a[200:203],38
R 34,1,0
v_fma_f32 v89,v68,v89,v67
M 30,a[204:207],38
L 6
buffer_store_dword v4,v3,s[36:39],s70 offen offset:0 sc0 sc1
M 26,a[208:211],42
s_mov_b32 s70,s69
R 38,1,1024
v_mul_f32 v86,v22,v86
M 30,a[212:215],42
s_mov_b32 s69,s68
v_exp_f32 v86,v86
M 26,a[216:219],46
L 2
R 42,1,2048
v_mul_f32 v87,v23,v87
M 30,a[220:223],46
s_nop 0
v_exp_f32 v87,v87
M 26,a[224:227],50
s_add_u32 s68,s68,s63
R 46,1,3072
v_mul_f32 v88,v24,v88
M 30,a[228:231],50
v_mul_f32 v89,v25,v89
v_fma_f32 v86,v86,v69,v69
M 26,a[232:235],54
R 50,1,4096
v_fma_f32 v87,v87,v69,v69
M 30,a[236:239],54
s_nop 0
v_exp_f32 v88,v88
M 26,a[240:243],58
R 54,1,5120
v_exp_f32 v89,v89
M 30,a[244:247],58
v_fma_f32 v88,v88,v69,v69
v_fma_f32 v89,v89,v69,v69
v_rcp_f32 v86,v86
v_rcp_f32 v87,v87
v_rcp_f32 v88,v88
v_rcp_f32 v89,v89
v_cmp_eq_u32 vcc,s53,v7
M 26,a[248:251],62
s_cmp_eq_u64 vcc,-1
R 58,1,6144
v_mul_f32 v22,v22,v86
v_mul_f32 v23,v23,v87
v_mul_f32 v24,v24,v88
v_mul_f32 v25,v25,v89
v_cvt_pk_bf16_f32 v92,v22,v23
v_cvt_pk_bf16_f32 v93,v24,v25
M 30,a[252:255],62
L 5
buffer_store_dwordx4 v[90:93],v0,s[36:39],s66 offen offset:0 sc0 sc1
v_xor_b32 v2,65536,v2
s_cbranch_scc1 Lg1_L20_%=
s_cmp_eq_u32 vcc_lo,-1
s_cbranch_scc1 Lg1_L10_%=
s_branch Lg1_X0_%=
Lg1_done_%=:
s_waitcnt vmcnt(1)
s_barrier
buffer_store_dword v4,v3,s[36:39],s70 offen offset:0 sc0 sc1
s_waitcnt vmcnt(1)
s_barrier
buffer_store_dword v4,v3,s[36:39],s72 offen offset:0 sc0 sc1
s_waitcnt vmcnt(0) lgkmcnt(0)
s_barrier
s_branch Lg2_end_%=
Lg2_begin_%=:
Lcode_g2_b:
s_mov_b64 s[36:37],%1
s_mov_b32 s38,111243392
s_mov_b32 s39,0x00020000
s_mov_b32 s44,%2
s_mov_b32 s45,%3
s_mov_b32 s46,%4
s_mov_b32 s47,%5
s_mov_b32 s48,%6
s_mov_b32 s49,%7
s_mov_b32 s50,%8
s_mov_b32 s51,%9
s_mov_b32 s93,%10
v_mov_b32 v0,%11
v_mov_b32 v3,%12
v_mov_b32 v5,%13
v_mov_b32 v6,%14
v_mov_b32 v18,%15
v_mov_b32 v19,%16
v_mov_b32 v20,%17
v_mov_b32 v21,%18
v_mov_b32 v22,%19
v_mov_b32 v10,%20
v_mov_b32 v11,%21
v_mov_b32 v12,%22
v_mov_b32 v13,%23
v_mov_b32 v14,%24
v_mov_b32 v15,%25
v_mov_b32 v16,%26
s_mov_b64 s[40:41],%27
s_mov_b32 s42,2097152
s_mov_b32 s43,0x00020000
s_lshl_b32 s57,s44,11
s_mov_b32 s79,111243264
buffer_load_dword v4,off,s[36:39],s79 sc1
s_add_u32 s55,s50,0
s_add_u32 m0,s57,0
s_nop 0
buffer_load_dwordx4 v18,s[40:43],s55 offen offset:0  lds
s_add_u32 m0,s57,1024
s_nop 0
buffer_load_dwordx4 v19,s[40:43],s55 offen offset:0  lds
s_add_u32 m0,s57,8064
s_nop 0
buffer_load_dwordx4 v18,s[40:43],s55 offen offset:128  lds
s_add_u32 m0,s57,9088
s_nop 0
buffer_load_dwordx4 v19,s[40:43],s55 offen offset:128  lds
s_add_u32 s55,s50,131072
s_add_u32 m0,s57,16384
s_nop 0
buffer_load_dwordx4 v18,s[40:43],s55 offen offset:0  lds
s_add_u32 m0,s57,17408
s_nop 0
buffer_load_dwordx4 v19,s[40:43],s55 offen offset:0  lds
s_add_u32 m0,s57,24448
s_nop 0
buffer_load_dwordx4 v18,s[40:43],s55 offen offset:128  lds
s_add_u32 m0,s57,25472
s_nop 0
buffer_load_dwordx4 v19,s[40:43],s55 offen offset:128  lds
s_add_u32 s55,s50,262144
s_add_u32 m0,s57,32768
s_nop 0
buffer_load_dwordx4 v18,s[40:43],s55 offen offset:0  lds
s_add_u32 m0,s57,33792
s_nop 0
buffer_load_dwordx4 v19,s[40:43],s55 offen offset:0  lds
s_add_u32 m0,s57,40832
s_nop 0
buffer_load_dwordx4 v18,s[40:43],s55 offen offset:128  lds
s_add_u32 m0,s57,41856
s_nop 0
buffer_load_dwordx4 v19,s[40:43],s55 offen offset:128  lds
s_add_u32 s55,s50,393216
s_add_u32 m0,s57,49152
s_nop 0
buffer_load_dwordx4 v18,s[40:43],s55 offen offset:0  lds
s_add_u32 m0,s57,50176
s_nop 0
buffer_load_dwordx4 v19,s[40:43],s55 offen offset:0  lds
s_add_u32 m0,s57,57216
s_nop 0
buffer_load_dwordx4 v18,s[40:43],s55 offen offset:128  lds
s_add_u32 m0,s57,58240
s_nop 0
buffer_load_dwordx4 v19,s[40:43],s55 offen offset:128  lds
s_add_u32 s55,s50,524288
s_add_u32 m0,s57,65536
s_nop 0
buffer_load_dwordx4 v18,s[40:43],s55 offen offset:0  lds
s_add_u32 m0,s57,66560
s_nop 0
buffer_load_dwordx4 v19,s[40:43],s55 offen offset:0  lds
s_add_u32 m0,s57,73600
s_nop 0
buffer_load_dwordx4 v18,s[40:43],s55 offen offset:128  lds
s_add_u32 m0,s57,74624
s_nop 0
buffer_load_dwordx4 v19,s[40:43],s55 offen offset:128  lds
s_add_u32 s55,s50,655360
s_add_u32 m0,s57,81920
s_nop 0
buffer_load_dwordx4 v18,s[40:43],s55 offen offset:0  lds
s_add_u32 m0,s57,82944
)"
R"(s_nop 0
buffer_load_dwordx4 v19,s[40:43],s55 offen offset:0  lds
s_add_u32 m0,s57,89984
s_nop 0
buffer_load_dwordx4 v18,s[40:43],s55 offen offset:128  lds
s_add_u32 m0,s57,91008
s_nop 0
buffer_load_dwordx4 v19,s[40:43],s55 offen offset:128  lds
s_waitcnt vmcnt(20)
L 0
s_barrier
s_add_u32 s55,s50,786432
s_add_u32 m0,s57,98304
s_nop 0
buffer_load_dwordx4 v18,s[40:43],s55 offen offset:0  lds
s_add_u32 m0,s57,99328
s_nop 0
buffer_load_dwordx4 v19,s[40:43],s55 offen offset:0  lds
s_add_u32 m0,s57,106368
s_nop 0
buffer_load_dwordx4 v18,s[40:43],s55 offen offset:128  lds
s_add_u32 m0,s57,107392
s_nop 0
buffer_load_dwordx4 v19,s[40:43],s55 offen offset:128  lds
v_add_u32 v26,0,v20
v_add_u32 v27,0,v21
T a[0:1],26,0
T a[2:3],26,512
T a[4:5],27,0
T a[6:7],27,512
T a[8:9],26,4096
T a[10:11],26,4608
T a[12:13],27,4096
T a[14:15],27,4608
s_waitcnt vmcnt(20)
L 0
s_barrier
s_add_u32 s55,s50,917504
s_add_u32 m0,s57,0
s_nop 0
buffer_load_dwordx4 v18,s[40:43],s55 offen offset:0  lds
s_add_u32 m0,s57,1024
s_nop 0
buffer_load_dwordx4 v19,s[40:43],s55 offen offset:0  lds
s_add_u32 m0,s57,8064
s_nop 0
buffer_load_dwordx4 v18,s[40:43],s55 offen offset:128  lds
s_add_u32 m0,s57,9088
s_nop 0
buffer_load_dwordx4 v19,s[40:43],s55 offen offset:128  lds
v_add_u32 v26,16384,v20
v_add_u32 v27,16384,v21
T a[16:17],26,0
T a[18:19],26,512
T a[20:21],27,0
T a[22:23],27,512
T a[24:25],26,4096
T a[26:27],26,4608
T a[28:29],27,4096
T a[30:31],27,4608
s_waitcnt vmcnt(20)
L 0
s_barrier
s_add_u32 s55,s50,1048576
s_add_u32 m0,s57,16384
s_nop 0
buffer_load_dwordx4 v18,s[40:43],s55 offen offset:0  lds
s_add_u32 m0,s57,17408
s_nop 0
buffer_load_dwordx4 v19,s[40:43],s55 offen offset:0  lds
s_add_u32 m0,s57,24448
s_nop 0
buffer_load_dwordx4 v18,s[40:43],s55 offen offset:128  lds
s_add_u32 m0,s57,25472
s_nop 0
buffer_load_dwordx4 v19,s[40:43],s55 offen offset:128  lds
v_add_u32 v26,32768,v20
v_add_u32 v27,32768,v21
T a[32:33],26,0
T a[34:35],26,512
T a[36:37],27,0
T a[38:39],27,512
T a[40:41],26,4096
T a[42:43],26,4608
T a[44:45],27,4096
T a[46:47],27,4608
s_waitcnt vmcnt(20)
L 0
s_barrier
s_add_u32 s55,s50,1179648
s_add_u32 m0,s57,32768
s_nop 0
buffer_load_dwordx4 v18,s[40:43],s55 offen offset:0  lds
s_add_u32 m0,s57,33792
s_nop 0
buffer_load_dwordx4 v19,s[40:43],s55 offen offset:0  lds
s_add_u32 m0,s57,40832
s_nop 0
buffer_load_dwordx4 v18,s[40:43],s55 offen offset:128  lds
s_add_u32 m0,s57,41856
s_nop 0
buffer_load_dwordx4 v19,s[40:43],s55 offen offset:128  lds
v_add_u32 v26,49152,v20
v_add_u32 v27,49152,v21
T a[48:49],26,0
T a[50:51],26,512
T a[52:53],27,0
T a[54:55],27,512
T a[56:57],26,4096
T a[58:59],26,4608
T a[60:61],27,4096
T a[62:63],27,4608
s_waitcnt vmcnt(20)
L 0
s_barrier
s_add_u32 s55,s50,1310720
s_add_u32 m0,s57,49152
s_nop 0
buffer_load_dwordx4 v18,s[40:43],s55 offen offset:0  lds
s_add_u32 m0,s57,50176
s_nop 0
buffer_load_dwordx4 v19,s[40:43],s55 offen offset:0  lds
s_add_u32 m0,s57,57216
s_nop 0
buffer_load_dwordx4 v18,s[40:43],s55 offen offset:128  lds
s_add_u32 m0,s57,58240
s_nop 0
buffer_load_dwordx4 v19,s[40:43],s55 offen offset:128  lds
v_add_u32 v26,65536,v20
v_add_u32 v27,65536,v21
T a[64:65],26,0
T a[66:67],26,512
T a[68:69],27,0
T a[70:71],27,512
T a[72:73],26,4096
T a[74:75],26,4608
T a[76:77],27,4096
T a[78:79],27,4608
s_waitcnt vmcnt(20)
L 0
s_barrier
s_add_u32 s55,s50,1441792
s_add_u32 m0,s57,65536
s_nop 0
buffer_load_dwordx4 v18,s[40:43],s55 offen offset:0  lds
s_add_u32 m0,s57,66560
s_nop 0
buffer_load_dwordx4 v19,s[40:43],s55 offen offset:0  lds
s_add_u32 m0,s57,73600
s_nop 0
buffer_load_dwordx4 v18,s[40:43],s55 offen offset:128  lds
s_add_u32 m0,s57,74624
s_nop 0
buffer_load_dwordx4 v19,s[40:43],s55 offen offset:128  lds
v_add_u32 v26,81920,v20
v_add_u32 v27,81920,v21
T a[80:81],26,0
T a[82:83],26,512
T a[84:85],27,0
T a[86:87],27,512
T a[88:89],26,4096
T a[90:91],26,4608
T a[92:93],27,4096
T a[94:95],27,4608
s_waitcnt vmcnt(20)
L 0
s_barrier
s_add_u32 s55,s50,1572864
s_add_u32 m0,s57,81920
s_nop 0
buffer_load_dwordx4 v18,s[40:43],s55 offen offset:0  lds
s_add_u32 m0,s57,82944
s_nop 0
buffer_load_dwordx4 v19,s[40:43],s55 offen offset:0  lds
s_add_u32 m0,s57,89984
s_nop 0
buffer_load_dwordx4 v18,s[40:43],s55 offen offset:128  lds
s_add_u32 m0,s57,91008
s_nop 0
buffer_load_dwordx4 v19,s[40:43],s55 offen offset:128  lds
v_add_u32 v26,98304,v20
v_add_u32 v27,98304,v21
T a[96:97],26,0
T a[98:99],26,512
T a[100:101],27,0
T a[102:103],27,512
T a[104:105],26,4096
T a[106:107],26,4608
T a[108:109],27,4096
T a[110:111],27,4608
s_waitcnt vmcnt(20)
L 0
s_barrier
s_add_u32 s55,s50,1703936
s_add_u32 m0,s57,98304
s_nop 0
buffer_load_dwordx4 v18,s[40:43],s55 offen offset:0  lds
s_add_u32 m0,s57,99328
s_nop 0
buffer_load_dwordx4 v19,s[40:43],s55 offen offset:0  lds
s_add_u32 m0,s57,106368
s_nop 0
buffer_load_dwordx4 v18,s[40:43],s55 offen offset:128  lds
s_add_u32 m0,s57,107392
s_nop 0
buffer_load_dwordx4 v19,s[40:43],s55 offen offset:128  lds
v_add_u32 v26,0,v20
v_add_u32 v27,0,v21
T a[112:113],26,0
T a[114:115],26,512
T a[116:117],27,0
T a[118:119],27,512
T a[120:121],26,4096
T a[122:123],26,4608
T a[124:125],27,4096
T a[126:127],27,4608
s_waitcnt vmcnt(20)
L 0
s_barrier
s_add_u32 s55,s50,1835008
s_add_u32 m0,s57,0
s_nop 0
buffer_load_dwordx4 v18,s[40:43],s55 offen offset:0  lds
s_add_u32 m0,s57,1024
s_nop 0
buffer_load_dwordx4 v19,s[40:43],s55 offen offset:0  lds
s_add_u32 m0,s57,8064
s_nop 0
buffer_load_dwordx4 v18,s[40:43],s55 offen offset:128  lds
s_add_u32 m0,s57,9088
s_nop 0
buffer_load_dwordx4 v19,s[40:43],s55 offen offset:128  lds
v_add_u32 v26,16384,v20
v_add_u32 v27,16384,v21
T a[128:129],26,0
T a[130:131],26,512
T a[132:133],27,0
T a[134:135],27,512
T a[136:137],26,4096
T a[138:139],26,4608
T a[140:141],27,4096
T a[142:143],27,4608
s_waitcnt vmcnt(20)
L 0
s_barrier
s_add_u32 s55,s50,1966080
s_add_u32 m0,s57,16384
s_nop 0
buffer_load_dwordx4 v18,s[40:43],s55 offen offset:0  lds
s_add_u32 m0,s57,17408
s_nop 0
buffer_load_dwordx4 v19,s[40:43],s55 offen offset:0  lds
s_add_u32 m0,s57,24448
s_nop 0
buffer_load_dwordx4 v18,s[40:43],s55 offen offset:128  lds
s_add_u32 m0,s57,25472
s_nop 0
buffer_load_dwordx4 v19,s[40:43],s55 offen offset:128  lds
v_add_u32 v26,32768,v20
v_add_u32 v27,32768,v21
T a[144:145],26,0
T a[146:147],26,512
T a[148:149],27,0
T a[150:151],27,512
T a[152:153],26,4096
T a[154:155],26,4608
T a[156:157],27,4096
T a[158:159],27,4608
s_waitcnt vmcnt(20)
L 0
s_barrier
v_add_u32 v26,49152,v20
v_add_u32 v27,49152,v21
T a[160:161],26,0
T a[162:163],26,512
T a[164:165],27,0
T a[166:167],27,512
T a[168:169],26,4096
T a[170:171],26,4608
T a[172:173],27,4096
T a[174:175],27,4608
s_waitcnt vmcnt(16)
L 0
s_barrier
v_add_u32 v26,65536,v20
v_add_u32 v27,65536,v21
T a[176:177],26,0
T a[178:179],26,512
T a[180:181],27,0
T a[182:183],27,512
T a[184:185],26,4096
T a[186:187],26,4608
T a[188:189],27,4096
T a[190:191],27,4608
s_waitcnt vmcnt(12)
L 0
s_barrier
v_add_u32 v26,81920,v20
v_add_u32 v27,81920,v21
T a[192:193],26,0
T a[194:195],26,512
T a[196:197],27,0
T a[198:199],27,512
T a[200:201],26,4096
T a[202:203],26,4608
T a[204:205],27,4096
T a[206:207],27,4608
s_waitcnt vmcnt(8)
L 0
s_barrier
v_add_u32 v26,98304,v20
v_add_u32 v27,98304,v21
T a[208:209],26,0
T a[210:211],26,512
T a[212:213],27,0
T a[214:215],27,512
T a[216:217],26,4096
T a[218:219],26,4608
T a[220:221],27,4096
T a[222:223],27,4608
s_waitcnt vmcnt(4)
L 0
s_barrier
v_add_u32 v26,0,v20
v_add_u32 v27,0,v21
T a[224:225],26,0
T a[226:227],26,512
T a[228:229],27,0
T a[230:231],27,512
T a[232:233],26,4096
T a[234:235],26,4608
T a[236:237],27,4096
T a[238:239],27,4608
s_waitcnt vmcnt(0)
L 0
s_barrier
v_add_u32 v26,16384,v20
v_add_u32 v27,16384,v21
T a[240:241],26,0
T a[242:243],26,512
T a[244:245],27,0
T a[246:247],27,512
T a[248:249],26,4096
T a[250:251],26,4608
T a[252:253],27,4096
T a[254:255],27,4608
L 0
s_barrier
v_readfirstlane_b32 s53,v4
s_nop 3
s_add_u32 s53,s53,1
s_cmp_eq_u32 s53,0
s_cselect_b32 s53,1,s53
v_mov_b32 v4,s53
s_mov_b32 s82,0
s_mov_b32 s80,0
s_mov_b32 s54,s45
s_mul_i32 s57,s44,8192
s_mul_i32 s55,s45,32768
s_add_u32 s55,s55,s57
s_add_u32 s55,s55,21987328
s_mul_i32 s60,s46,32768
s_mul_i32 s61,s46,1024
s_mul_i32 s62,s46,32768
s_mul_i32 s63,s46,1024
s_mul_i32 s64,s46,512
s_lshl_b32 s59,s44,8
s_add_u32 s59,s59,131072
s_mul_i32 s58,s45,1024
s_add_u32 s58,s58,109333504
s_mul_i32 s65,s45,32768
s_add_u32 s65,s65,s47
s_mul_i32 s68,s45,1024
s_add_u32 s68,s68,s48
s_mul_i32 s73,s45,512
s_add_u32 s73,s73,106954752
s_mul_i32 s74,s45,32768
s_add_u32 s74,s74,s49
s_mul_i32 s75,s45,1024
s_add_u32 s75,s75,107479040
s_mov_b32 s66,0x7fff0000
s_mov_b32 s67,0x7fff0000
s_mov_b32 s69,0x7fff0000
s_mov_b32 s76,0x7fff0000
s_mov_b32 s77,0x7fff0000
s_mov_b32 s70,0x7fff0000
s_mov_b32 s71,0x7fff0000
s_mov_b32 s72,0x7fff0000
v_mov_b32 v1,v0
v_add_u32 v2,32768,v0
s_mov_b32 s84,32768
s_mov_b32 s86,0x3a800000
s_mov_b32 s87,0x38a7c5ac
s_mov_b32 s88,0xc0135761
s_mov_b32 s89,0x3d372713
s_and_b32 s85,s44,1
s_mul_i32 s85,s85,127
v_lshlrev_b32 v8,22,v0
v_and_b32 v8,0x80000000,v8
v_or_b32 v8,v8,v5
s_and_b32 s79,s54,127
s_cmp_eq_u32 s79,0
s_cselect_b32 s94,0x3300,0
s_cmp_eq_u32 s79,127
s_cselect_b32 s94,0xcc00,s94
s_add_u32 s79,s54,s46
s_and_b32 s79,s79,127
s_cmp_eq_u32 s79,0
s_cselect_b32 s95,0x3300,0
s_cmp_eq_u32 s79,127
s_cselect_b32 s95,0xcc00,s95
s_add_u32 m0,s59,4096
s_nop 0
buffer_load_dword v5,s[36:39],s58 offen offset:0 sc1 lds
buffer_store_dword v4,v3,s[36:39],s71 offen offset:0 sc0 sc1
s_add_u32 m0,s59,5120
s_nop 0
buffer_load_dword v5,s[36:39],s58 offen offset:0 sc1 lds
buffer_store_dword v4,v3,s[36:39],s71 offen offset:0 sc0 sc1
s_mov_b32 s83,0
Lg2_poll_%=:
s_add_u32 s78,s83,2
s_and_b32 s78,s78,3
s_lshl_b32 s78,s78,10
s_add_u32 s78,s78,4096
s_add_u32 m0,s59,s78
s_nop 0
buffer_load_dword v5,s[36:39],s58 offen offset:0 sc1 lds
s_and_b32 s78,s83,3
s_lshl_b32 s78,s78,10
v_add_u32 v74,s78,v6
s_waitcnt vmcnt(4)
s_barrier
ds_read_b32 v7,v74 offset:4096
buffer_store_dword v4,v3,s[36:39],s70 offen offset:0 sc0 sc1
s_mov_b32 s70,s71
s_mov_b32 s71,s72
s_mov_b32 s72,0x7fff0000
s_add_u32 s83,s83,1
s_add_u32 s82,s82,1
L 0
v_cmp_eq_u32 vcc,s53,v7
s_nop 0
s_or_b64 vcc,vcc,s[94:95]
s_cmp_eq_u32 vcc_lo,-1
s_cbranch_scc1 Lg2_go_%=
s_cmp_lt_u32 s82,20000
s_cbranch_scc1 Lg2_poll_%=
Lg2_go_%=:
s_and_b32 s78,s54,127
s_cmp_eq_u32 s78,s85
s_cselect_b64 s[40:41],-1,0
v_cndmask_b32 v17,v15,v16,s[40:41]
s_mov_b32 m0,s57
s_add_u32 s56,s55,4096
D 17,55,0
D 17,55,1024
D 17,55,2048
D 17,55,3072
s_add_u32 m0,s57,4096
s_nop 0
D 17,56,0
D 17,56,1024
D 17,56,2048
D 17,56,3072
s_add_u32 s55,s55,s60
s_add_u32 s57,s57,32768
s_and_b32 s57,s57,131071
s_add_u32 s54,s54,s46
Lg2_gox_%=:
s_add_u32 s58,s58,s61
s_cmp_eq_u32 vcc_hi,-1
s_cbranch_scc1 Lg2_go2_%=
s_add_u32 m0,s59,2048
s_nop 0
buffer_load_dword v5,s[36:39],s58 offen offset:0 sc1 lds
s_and_b32 s79,s54,127
s_cmp_eq_u32 s79,0
s_cselect_b32 s94,0x3300,0
s_cmp_eq_u32 s79,127
s_cselect_b32 s94,0xcc00,s94
s_add_u32 s79,s54,s46
s_and_b32 s79,s79,127
s_cmp_eq_u32 s79,0
s_cselect_b32 s95,0x3300,0
s_cmp_eq_u32 s79,127
s_cselect_b32 s95,0xcc00,s95
s_waitcnt vmcnt(0)
s_barrier
buffer_store_dword v4,v3,s[36:39],s70 offen offset:0 sc0 sc1
s_min_u32 s71,s71,s72
buffer_store_dword v4,v3,s[36:39],s71 offen offset:0 sc0 sc1
s_mov_b32 s70,0x7fff0000
s_mov_b32 s71,0x7fff0000
s_mov_b32 s72,0x7fff0000
s_cmp_eq_u32 s80,1
s_cbranch_scc1 Lg2_st11_%=
Lg2_st10_%=:
ds_read_b32 v7,v6 offset:2048
R 34,1,0
)"
R"(R 38,1,1024
R 42,1,2048
R 46,1,3072
R 50,1,4096
R 54,1,5120
R 58,1,6144
L 7
v_cmp_eq_u32 vcc,s53,v7
s_nop 0
s_or_b64 vcc,vcc,s[94:95]
L 5
s_cmp_eq_u64 vcc,-1
s_cbranch_scc1 Lg2_L20_%=
s_cmp_eq_u32 vcc_lo,-1
s_cbranch_scc1 Lg2_L10_%=
s_branch Lg2_X0_%=
Lg2_st11_%=:
ds_read_b32 v7,v6 offset:2048
R 34,2,0
R 38,2,1024
R 42,2,2048
R 46,2,3072
R 50,2,4096
R 54,2,5120
R 58,2,6144
L 7
v_cmp_eq_u32 vcc,s53,v7
s_nop 0
s_or_b64 vcc,vcc,s[94:95]
L 5
s_cmp_eq_u64 vcc,-1
s_cbranch_scc1 Lg2_L21_%=
s_cmp_eq_u32 vcc_lo,-1
s_cbranch_scc1 Lg2_L11_%=
s_branch Lg2_X1_%=
Lg2_go2_%=:
s_add_u32 s58,s58,s61
s_add_u32 m0,s59,2048
s_nop 0
buffer_load_dword v5,s[36:39],s58 offen offset:0 sc1 lds
s_and_b32 s78,s54,127
s_cmp_eq_u32 s78,s85
s_cselect_b64 s[40:41],-1,0
v_cndmask_b32 v17,v15,v16,s[40:41]
s_mov_b32 m0,s57
s_add_u32 s56,s55,4096
D 17,55,0
D 17,55,1024
D 17,55,2048
D 17,55,3072
s_add_u32 m0,s57,4096
s_nop 0
D 17,56,0
D 17,56,1024
D 17,56,2048
D 17,56,3072
s_add_u32 s55,s55,s60
s_add_u32 s57,s57,32768
s_and_b32 s57,s57,131071
s_add_u32 s54,s54,s46
s_and_b32 s79,s54,127
s_cmp_eq_u32 s79,0
s_cselect_b32 s94,0x3300,0
s_cmp_eq_u32 s79,127
s_cselect_b32 s94,0xcc00,s94
s_add_u32 s79,s54,s46
s_and_b32 s79,s79,127
s_cmp_eq_u32 s79,0
s_cselect_b32 s95,0x3300,0
s_cmp_eq_u32 s79,127
s_cselect_b32 s95,0xcc00,s95
s_waitcnt vmcnt(8)
s_barrier
buffer_store_dword v4,v3,s[36:39],s70 offen offset:0 sc0 sc1
s_min_u32 s71,s71,s72
buffer_store_dword v4,v3,s[36:39],s71 offen offset:0 sc0 sc1
s_mov_b32 s70,0x7fff0000
s_mov_b32 s71,0x7fff0000
s_mov_b32 s72,0x7fff0000
s_cmp_eq_u32 s80,1
s_cbranch_scc1 Lg2_st21_%=
Lg2_st20_%=:
ds_read_b32 v7,v6 offset:2048
R 34,1,0
R 38,1,1024
R 42,1,2048
R 46,1,3072
R 50,1,4096
R 54,1,5120
R 58,1,6144
L 7
v_cmp_eq_u32 vcc,s53,v7
s_nop 0
s_or_b64 vcc,vcc,s[94:95]
L 5
s_cmp_eq_u32 vcc_lo,-1
s_cbranch_scc1 Lg2_S0_%=
s_branch Lg2_N0_%=
Lg2_st21_%=:
ds_read_b32 v7,v6 offset:2048
R 34,2,0
R 38,2,1024
R 42,2,2048
R 46,2,3072
R 50,2,4096
R 54,2,5120
R 58,2,6144
L 7
v_cmp_eq_u32 vcc,s53,v7
s_nop 0
s_or_b64 vcc,vcc,s[94:95]
L 5
s_cmp_eq_u32 vcc_lo,-1
s_cbranch_scc1 Lg2_S1_%=
s_branch Lg2_N1_%=
Lg2_L20_%=:
v_mfma_f32_16x16x32_bf16 v[18:21],a[0:3],v[34:37],0
s_add_u32 s58,s58,s61
R 62,1,7168
v_lshlrev_b32 v74,16,v94
v_mfma_f32_16x16x32_bf16 v[22:25],a[4:7],v[34:37],0
s_add_u32 s58,s58,s61
buffer_load_dwordx4 v[90:93],v0,s[36:39],s74 offen offset:0 sc1
M 18,a[8:11],38
s_add_u32 m0,s59,0
R 34,1,8192
v_and_b32 v75,0xffff0000,v94
M 22,a[12:15],38
L 5
buffer_load_dword v8,s[36:39],s58 offen offset:0 sc1 lds
M 18,a[16:19],42
s_mov_b32 m0,s57
R 38,1,9216
v_lshlrev_b32 v76,16,v95
M 22,a[20:23],42
s_mov_b32 s66,s65
buffer_load_dwordx2 v[66:67],v11,s[36:39],s73 offen offset:16 sc1
M 18,a[24:27],46
s_add_u32 s73,s73,s64
R 42,1,10240
v_and_b32 v77,0xffff0000,v95
M 22,a[28:31],46
L 5
v_lshlrev_b32 v78,16,v96
v_and_b32 v79,0xffff0000,v96
M 18,a[32:35],50
s_add_u32 s74,s74,s62
R 46,1,11264
v_lshlrev_b32 v80,16,v97
M 22,a[36:39],50
s_and_b32 s78,s54,127
v_and_b32 v81,0xffff0000,v97
v_fma_f32 v74,v74,v71,v70
M 18,a[40:43],54
s_cmp_eq_u32 s78,s85
R 50,1,12288
v_fma_f32 v75,v75,v71,v70
M 22,a[44:47],54
L 5
v_fma_f32 v76,v76,v71,v70
v_fma_f32 v77,v77,v71,v70
M 18,a[48:51],58
s_cselect_b64 s[40:41],-1,0
R 54,1,13312
v_cndmask_b32 v17,v15,v16,s[40:41]
M 22,a[52:55],58
s_add_u32 s56,s55,4096
D 17,55,0
M 18,a[56:59],62
s_mov_b32 s76,s75
R 58,1,14336
v_fma_f32 v78,v78,v71,v70
M 22,a[60:63],62
L 5
D 17,55,1024
M 18,a[64:67],34
R 62,1,15360
v_fma_f32 v79,v79,v71,v70
M 22,a[68:71],34
D 17,55,2048
M 18,a[72:75],38
R 34,1,16384
v_fma_f32 v80,v80,v71,v70
M 22,a[76:79],38
L 5
D 17,55,3072
M 18,a[80:83],42
s_add_u32 m0,s57,4096
R 38,1,17408
v_fma_f32 v81,v81,v71,v70
M 22,a[84:87],42
s_add_u32 s55,s55,s60
v_add_f32 v26,v26,v74
v_add_f32 v27,v27,v75
M 18,a[88:91],46
s_add_u32 s57,s57,32768
R 42,1,18432
v_add_f32 v28,v28,v76
M 22,a[92:95],46
L 5
D 17,56,0
M 18,a[96:99],50
s_and_b32 s57,s57,131071
R 46,1,19456
v_add_f32 v29,v29,v77
M 22,a[100:103],50
s_add_u32 s54,s54,s46
v_add_f32 v30,v30,v78
v_add_f32 v31,v31,v79
M 18,a[104:107],54
s_and_b32 s78,s54,127
R 50,1,20480
v_add_f32 v32,v32,v80
M 22,a[108:111],54
L 5
D 17,56,1024
M 18,a[112:115],58
s_cmp_eq_u32 s78,s85
R 54,1,21504
v_fma_f32 v75,v26,v26,s87
M 22,a[116:119],58
s_cselect_b64 s[40:41],-1,0
v_add_f32 v74,v26,v27
v_fma_f32 v75,v27,v27,v75
M 18,a[120:123],62
R 58,1,22528
v_add_f32 v74,v74,v28
M 22,a[124:127],62
L 5
D 17,56,2048
M 18,a[128:131],34
R 62,1,23552
v_fma_f32 v75,v28,v28,v75
M 22,a[132:135],34
v_add_f32 v74,v74,v29
v_fma_f32 v75,v29,v29,v75
M 18,a[136:139],38
R 34,1,24576
v_add_f32 v74,v74,v30
M 22,a[140:143],38
L 5
D 17,56,3072
M 18,a[144:147],42
s_mov_b32 m0,s57
R 38,1,25600
v_fma_f32 v75,v30,v30,v75
M 22,a[148:151],42
s_add_u32 s56,s55,4096
v_add_f32 v74,v74,v31
v_fma_f32 v75,v31,v31,v75
M 18,a[152:155],46
R 42,1,26624
v_add_f32 v33,v33,v81
M 22,a[156:159],46
L 5
v_add_f32 v74,v74,v32
v_fma_f32 v75,v32,v32,v75
v_add_f32 v74,v74,v33
v_fma_f32 v75,v33,v33,v75
M 18,a[160:163],50
R 46,1,27648
v_cndmask_b32 v17,v15,v16,s[40:41]
M 22,a[164:167],50
v_permlane16_swap_b32 v74,v75
v_add_f32 v74,v74,v75
v_mov_b32 v75,v74
M 18,a[168:171],54
R 50,1,28672
v_cvt_pk_bf16_f32 v82,v26,v27
M 22,a[172:175],54
L 5
v_permlane32_swap_b32 v74,v75
v_add_f32 v74,v74,v75
M 18,a[176:179],58
ds_write_b32 v12,v74 offset:2048
R 54,1,29696
M 22,a[180:183],58
D 17,55,0
M 18,a[184:187],62
R 58,1,30720
v_cvt_pk_bf16_f32 v83,v28,v29
M 22,a[188:191],62
L 6
L 2
D 17,55,1024
M 18,a[192:195],34
s_waitcnt vmcnt(2)
R 62,1,31744
v_cvt_pk_bf16_f32 v84,v30,v31
M 22,a[196:199],34
s_barrier
D 17,55,2048
M 18,a[200:203],38
R 34,2,0
R 86,13,2048
M 22,a[204:207],38
s_nop 0
D 17,55,3072
M 18,a[208:211],42
s_add_u32 m0,s57,4096
s_add_u32 s55,s55,s60
s_add_u32 s57,s57,32768
R 38,2,1024
M 22,a[212:215],42
s_and_b32 s57,s57,131071
s_add_u32 s54,s54,s46
s_and_b32 s79,s54,127
ds_read_b32 v7,v6 offset:0
M 18,a[216:219],46
s_cmp_eq_u32 s79,0
s_cselect_b32 s94,0x3300,0
s_cmp_eq_u32 s79,127
R 42,2,2048
M 22,a[220:223],46
s_cselect_b32 s94,0xcc00,s94
s_add_u32 s79,s54,s46
L 7
buffer_store_dword v4,v3,s[36:39],s70 offen offset:0 sc0 sc1
M 18,a[224:227],50
s_and_b32 s79,s79,127
s_cmp_eq_u32 s79,0
s_cselect_b32 s95,0x3300,0
R 46,2,3072
M 22,a[228:231],50
s_cmp_eq_u32 s79,127
D 17,56,0
M 18,a[232:235],54
s_cselect_b32 s95,0xcc00,s95
R 50,2,4096
v_cvt_pk_bf16_f32 v85,v32,v33
M 22,a[236:239],54
s_add_u32 s65,s65,s62
L 7
s_mov_b32 s70,s69
D 17,56,1024
M 18,a[240:243],58
s_add_u32 s75,s75,s63
L 5
s_mov_b32 s69,s68
R 54,2,5120
M 22,a[244:247],58
s_add_u32 s68,s68,s63
L 4
v_cmp_eq_u32 vcc,s53,v7
M 18,a[248:251],62
s_or_b64 vcc,vcc,s[94:95]
s_cmp_eq_u32 vcc_lo,-1
R 58,2,6144
v_add_f32 v86,v86,v87
v_add_f32 v88,v88,v89
v_add_f32 v86,v86,v88
v_mul_f32 v86,s86,v86
M 22,a[252:255],62
s_nop 0
D 17,56,2048
D 17,56,3072
buffer_store_dwordx4 v[82:85],v0,s[36:39],s67 offen offset:0 sc0 sc1
buffer_store_dword v86,v14,s[36:39],s77 offen offset:0 sc0 sc1
v_xor_b32 v1,65536,v1
s_cbranch_scc0 Lg2_N1_%=
Lg2_S1_%=:
v_mfma_f32_16x16x32_bf16 v[26:29],a[0:3],v[34:37],0
s_add_u32 s58,s58,s61
R 62,2,7168
v_lshlrev_b32 v74,16,v90
v_mfma_f32_16x16x32_bf16 v[30:33],a[4:7],v[34:37],0
s_add_u32 m0,s59,1024
buffer_load_dwordx4 v[94:97],v0,s[36:39],s74 offen offset:0 sc1
M 26,a[8:11],38
s_mov_b32 s67,s65
R 34,2,8192
v_and_b32 v75,0xffff0000,v90
M 30,a[12:15],38
L 5
buffer_load_dword v8,s[36:39],s58 offen offset:0 sc1 lds
M 26,a[16:19],42
s_mov_b32 m0,s57
R 38,2,9216
v_lshlrev_b32 v76,16,v91
M 30,a[20:23],42
s_mov_b32 s77,s75
buffer_load_dwordx2 v[70:71],v11,s[36:39],s73 offen offset:16 sc1
M 26,a[24:27],46
s_add_u32 s73,s73,s64
R 42,2,10240
v_and_b32 v77,0xffff0000,v91
M 30,a[28:31],46
L 5
v_lshlrev_b32 v78,16,v92
v_and_b32 v79,0xffff0000,v92
M 26,a[32:35],50
s_add_u32 s74,s74,s62
R 46,2,11264
v_lshlrev_b32 v80,16,v93
M 30,a[36:39],50
s_and_b32 s78,s54,127
v_and_b32 v81,0xffff0000,v93
v_fma_f32 v74,v74,v67,v66
M 26,a[40:43],54
s_cmp_eq_u32 s78,s85
R 50,2,12288
v_fma_f32 v75,v75,v67,v66
M 30,a[44:47],54
L 5
v_fma_f32 v76,v76,v67,v66
v_fma_f32 v77,v77,v67,v66
M 26,a[48:51],58
s_cselect_b64 s[40:41],-1,0
R 54,2,13312
v_cndmask_b32 v17,v15,v16,s[40:41]
M 30,a[52:55],58
s_add_u32 s56,s55,4096
D 17,55,0
M 26,a[56:59],62
R 58,2,14336
v_fma_f32 v78,v78,v67,v66
M 30,a[60:63],62
L 5
D 17,55,1024
M 26,a[64:67],34
R 62,2,15360
v_fma_f32 v79,v79,v67,v66
M 30,a[68:71],34
v_fma_f32 v80,v80,v67,v66
v_fma_f32 v81,v81,v67,v66
M 26,a[72:75],38
R 34,2,16384
v_add_f32 v18,v18,v74
M 30,a[76:79],38
L 5
v_add_f32 v19,v19,v75
v_add_f32 v20,v20,v76
M 26,a[80:83],42
R 38,2,17408
v_add_f32 v21,v21,v77
M 30,a[84:87],42
D 17,55,2048
M 26,a[88:91],46
R 42,2,18432
v_add_f32 v22,v22,v78
M 30,a[92:95],46
L 5
v_add_f32 v23,v23,v79
v_add_f32 v24,v24,v80
M 26,a[96:99],50
R 46,2,19456
v_add_f32 v25,v25,v81
M 30,a[100:103],50
v_cvt_pk_bf16_f32 v82,v18,v19
v_cvt_pk_bf16_f32 v83,v20,v21
M 26,a[104:107],54
R 50,2,20480
v_cvt_pk_bf16_f32 v84,v22,v23
M 30,a[108:111],54
L 5
D 17,55,3072
M 26,a[112:115],58
s_add_u32 m0,s57,4096
R 54,2,21504
v_fma_f32 v75,v18,v18,s87
M 30,a[116:119],58
s_add_u32 s55,s55,s60
v_add_f32 v74,v18,v19
v_fma_f32 v75,v19,v19,v75
M 26,a[120:123],62
s_add_u32 s57,s57,32768
R 58,2,22528
v_add_f32 v74,v74,v20
M 30,a[124:127],62
L 5
v_fma_f32 v75,v20,v20,v75
v_add_f32 v74,v74,v21
M 26,a[128:131],34
s_and_b32 s57,s57,131071
R 62,2,23552
v_fma_f32 v75,v21,v21,v75
M 30,a[132:135],34
s_add_u32 s54,s54,s46
v_add_f32 v74,v74,v22
v_fma_f32 v75,v22,v22,v75
M 26,a[136:139],38
s_and_b32 s79,s54,127
R 34,2,24576
v_add_f32 v74,v74,v23
M 30,a[140:143],38
L 5
D 17,56,0
M 26,a[144:147],42
s_cmp_eq_u32 s79,0
R 38,2,25600
v_fma_f32 v75,v23,v23,v75
M 30,a[148:151],42
s_cselect_b32 s94,0x3300,0
v_add_f32 v74,v74,v24
v_fma_f32 v75,v24,v24,v75
M 26,a[152:155],46
s_cmp_eq_u32 s79,127
R 42,2,26624
v_add_f32 v74,v74,v25
M 30,a[156:159],46
)"
R"(L 5
v_cvt_pk_bf16_f32 v85,v24,v25
v_fma_f32 v75,v25,v25,v75
M 26,a[160:163],50
s_cselect_b32 s94,0xcc00,s94
s_add_u32 s79,s54,s46
R 46,2,27648
M 30,a[164:167],50
s_and_b32 s79,s79,127
v_permlane16_swap_b32 v74,v75
v_add_f32 v74,v74,v75
v_mov_b32 v75,v74
M 26,a[168:171],54
s_cmp_eq_u32 s79,0
s_cselect_b32 s95,0x3300,0
R 50,2,28672
M 30,a[172:175],54
L 5
v_permlane32_swap_b32 v74,v75
v_add_f32 v74,v74,v75
M 26,a[176:179],58
s_cmp_eq_u32 s79,127
ds_write_b32 v12,v74 offset:0
R 54,2,29696
M 30,a[180:183],58
s_cselect_b32 s95,0xcc00,s95
D 17,56,1024
M 26,a[184:187],62
s_add_u32 s65,s65,s62
s_add_u32 s75,s75,s63
R 58,2,30720
M 30,a[188:191],62
L 6
L 2
buffer_store_dwordx4 v[82:85],v0,s[36:39],s66 offen offset:0 sc0 sc1
M 26,a[192:195],34
s_waitcnt vmcnt(7)
R 62,2,31744
v_xor_b32 v2,65536,v2
M 30,a[196:199],34
s_barrier
R 86,13,0
ds_read_b32 v7,v6 offset:1024
M 26,a[200:203],38
R 34,1,0
M 30,a[204:207],38
s_nop 0
buffer_store_dword v4,v3,s[36:39],s70 offen offset:0 sc0 sc1
M 26,a[208:211],42
s_mov_b32 s70,s69
s_mov_b32 s69,s68
R 38,1,1024
M 30,a[212:215],42
s_add_u32 s68,s68,s63
D 17,56,2048
M 26,a[216:219],46
L 3
R 42,1,2048
v_add_f32 v86,v86,v87
M 30,a[220:223],46
s_nop 0
D 17,56,3072
M 26,a[224:227],50
L 3
R 46,1,3072
v_add_f32 v88,v88,v89
M 30,a[228:231],50
v_add_f32 v86,v86,v88
v_mul_f32 v86,s86,v86
M 26,a[232:235],54
R 50,1,4096
v_cmp_eq_u32 vcc,s53,v7
M 30,a[236:239],54
s_nop 0
buffer_store_dword v86,v14,s[36:39],s76 offen offset:0 sc0 sc1
M 26,a[240:243],58
s_or_b64 vcc,vcc,s[94:95]
s_cmp_eq_u32 vcc_lo,-1
R 54,1,5120
M 30,a[244:247],58
M 26,a[248:251],62
R 58,1,6144
M 30,a[252:255],62
L 5
s_cbranch_scc0 Lg2_N0_%=
Lg2_S0_%=:
v_mfma_f32_16x16x32_bf16 v[18:21],a[0:3],v[34:37],0
s_add_u32 s58,s58,s61
R 62,1,7168
v_lshlrev_b32 v74,16,v94
v_mfma_f32_16x16x32_bf16 v[22:25],a[4:7],v[34:37],0
s_add_u32 m0,s59,0
buffer_load_dwordx4 v[90:93],v0,s[36:39],s74 offen offset:0 sc1
M 18,a[8:11],38
s_mov_b32 s66,s65
R 34,1,8192
v_and_b32 v75,0xffff0000,v94
M 22,a[12:15],38
L 5
buffer_load_dword v8,s[36:39],s58 offen offset:0 sc1 lds
M 18,a[16:19],42
s_mov_b32 m0,s57
R 38,1,9216
v_lshlrev_b32 v76,16,v95
M 22,a[20:23],42
s_mov_b32 s76,s75
buffer_load_dwordx2 v[66:67],v11,s[36:39],s73 offen offset:16 sc1
M 18,a[24:27],46
s_add_u32 s73,s73,s64
R 42,1,10240
v_and_b32 v77,0xffff0000,v95
M 22,a[28:31],46
L 5
v_lshlrev_b32 v78,16,v96
v_and_b32 v79,0xffff0000,v96
M 18,a[32:35],50
s_add_u32 s74,s74,s62
R 46,1,11264
v_lshlrev_b32 v80,16,v97
M 22,a[36:39],50
s_and_b32 s78,s54,127
v_and_b32 v81,0xffff0000,v97
v_fma_f32 v74,v74,v71,v70
M 18,a[40:43],54
s_cmp_eq_u32 s78,s85
R 50,1,12288
v_fma_f32 v75,v75,v71,v70
M 22,a[44:47],54
L 5
v_fma_f32 v76,v76,v71,v70
v_fma_f32 v77,v77,v71,v70
M 18,a[48:51],58
s_cselect_b64 s[40:41],-1,0
R 54,1,13312
v_cndmask_b32 v17,v15,v16,s[40:41]
M 22,a[52:55],58
s_add_u32 s56,s55,4096
D 17,55,0
M 18,a[56:59],62
R 58,1,14336
v_fma_f32 v78,v78,v71,v70
M 22,a[60:63],62
L 5
D 17,55,1024
M 18,a[64:67],34
R 62,1,15360
v_fma_f32 v79,v79,v71,v70
M 22,a[68:71],34
v_fma_f32 v80,v80,v71,v70
v_fma_f32 v81,v81,v71,v70
M 18,a[72:75],38
R 34,1,16384
v_add_f32 v26,v26,v74
M 22,a[76:79],38
L 5
v_add_f32 v27,v27,v75
v_add_f32 v28,v28,v76
M 18,a[80:83],42
R 38,1,17408
v_add_f32 v29,v29,v77
M 22,a[84:87],42
D 17,55,2048
M 18,a[88:91],46
R 42,1,18432
v_add_f32 v30,v30,v78
M 22,a[92:95],46
L 5
v_add_f32 v31,v31,v79
v_add_f32 v32,v32,v80
M 18,a[96:99],50
R 46,1,19456
v_add_f32 v33,v33,v81
M 22,a[100:103],50
v_cvt_pk_bf16_f32 v82,v26,v27
v_cvt_pk_bf16_f32 v83,v28,v29
M 18,a[104:107],54
R 50,1,20480
v_cvt_pk_bf16_f32 v84,v30,v31
M 22,a[108:111],54
L 5
D 17,55,3072
M 18,a[112:115],58
s_add_u32 m0,s57,4096
R 54,1,21504
v_fma_f32 v75,v26,v26,s87
M 22,a[116:119],58
s_add_u32 s55,s55,s60
v_add_f32 v74,v26,v27
v_fma_f32 v75,v27,v27,v75
M 18,a[120:123],62
s_add_u32 s57,s57,32768
R 58,1,22528
v_add_f32 v74,v74,v28
M 22,a[124:127],62
L 5
v_fma_f32 v75,v28,v28,v75
v_add_f32 v74,v74,v29
M 18,a[128:131],34
s_and_b32 s57,s57,131071
R 62,1,23552
v_fma_f32 v75,v29,v29,v75
M 22,a[132:135],34
s_add_u32 s54,s54,s46
v_add_f32 v74,v74,v30
v_fma_f32 v75,v30,v30,v75
M 18,a[136:139],38
s_and_b32 s79,s54,127
R 34,1,24576
v_add_f32 v74,v74,v31
M 22,a[140:143],38
L 5
D 17,56,0
M 18,a[144:147],42
s_cmp_eq_u32 s79,0
R 38,1,25600
v_fma_f32 v75,v31,v31,v75
M 22,a[148:151],42
s_cselect_b32 s94,0x3300,0
v_add_f32 v74,v74,v32
v_fma_f32 v75,v32,v32,v75
M 18,a[152:155],46
s_cmp_eq_u32 s79,127
R 42,1,26624
v_add_f32 v74,v74,v33
M 22,a[156:159],46
L 5
v_cvt_pk_bf16_f32 v85,v32,v33
v_fma_f32 v75,v33,v33,v75
M 18,a[160:163],50
s_cselect_b32 s94,0xcc00,s94
s_add_u32 s79,s54,s46
R 46,1,27648
M 22,a[164:167],50
s_and_b32 s79,s79,127
v_permlane16_swap_b32 v74,v75
v_add_f32 v74,v74,v75
v_mov_b32 v75,v74
M 18,a[168:171],54
s_cmp_eq_u32 s79,0
s_cselect_b32 s95,0x3300,0
R 50,1,28672
M 22,a[172:175],54
L 5
v_permlane32_swap_b32 v74,v75
v_add_f32 v74,v74,v75
M 18,a[176:179],58
s_cmp_eq_u32 s79,127
ds_write_b32 v12,v74 offset:2048
R 54,1,29696
M 22,a[180:183],58
s_cselect_b32 s95,0xcc00,s95
D 17,56,1024
M 18,a[184:187],62
s_add_u32 s65,s65,s62
s_add_u32 s75,s75,s63
R 58,1,30720
M 22,a[188:191],62
L 6
L 2
buffer_store_dwordx4 v[82:85],v0,s[36:39],s67 offen offset:0 sc0 sc1
M 18,a[192:195],34
s_waitcnt vmcnt(7)
R 62,1,31744
v_xor_b32 v1,65536,v1
M 22,a[196:199],34
s_barrier
R 86,13,2048
ds_read_b32 v7,v6 offset:0
M 18,a[200:203],38
R 34,2,0
M 22,a[204:207],38
s_nop 0
buffer_store_dword v4,v3,s[36:39],s70 offen offset:0 sc0 sc1
M 18,a[208:211],42
s_mov_b32 s70,s69
s_mov_b32 s69,s68
R 38,2,1024
M 22,a[212:215],42
s_add_u32 s68,s68,s63
D 17,56,2048
M 18,a[216:219],46
L 3
R 42,2,2048
v_add_f32 v86,v86,v87
M 22,a[220:223],46
s_nop 0
D 17,56,3072
M 18,a[224:227],50
L 3
R 46,2,3072
v_add_f32 v88,v88,v89
M 22,a[228:231],50
v_add_f32 v86,v86,v88
v_mul_f32 v86,s86,v86
M 18,a[232:235],54
R 50,2,4096
v_cmp_eq_u32 vcc,s53,v7
M 22,a[236:239],54
s_nop 0
buffer_store_dword v86,v14,s[36:39],s77 offen offset:0 sc0 sc1
M 18,a[240:243],58
s_or_b64 vcc,vcc,s[94:95]
s_cmp_eq_u32 vcc_lo,-1
R 54,2,5120
M 22,a[244:247],58
M 18,a[248:251],62
R 58,2,6144
M 22,a[252:255],62
L 5
s_cbranch_scc1 Lg2_S1_%=
Lg2_N1_%=:
v_mfma_f32_16x16x32_bf16 v[26:29],a[0:3],v[34:37],0
s_add_u32 m0,s59,1024
R 62,2,7168
v_lshlrev_b32 v74,16,v90
v_mfma_f32_16x16x32_bf16 v[30:33],a[4:7],v[34:37],0
s_mov_b32 s67,s65
buffer_load_dword v5,s[36:39],s58 offen offset:0 sc1 lds
M 26,a[8:11],38
s_mov_b32 s77,s75
R 34,2,8192
v_and_b32 v75,0xffff0000,v90
M 30,a[12:15],38
L 5
buffer_load_dwordx4 v[94:97],v0,s[36:39],s74 offen offset:0 sc1
M 26,a[16:19],42
R 38,2,9216
v_lshlrev_b32 v76,16,v91
M 30,a[20:23],42
buffer_load_dwordx2 v[70:71],v11,s[36:39],s73 offen offset:16 sc1
M 26,a[24:27],46
s_add_u32 s73,s73,s64
R 42,2,10240
v_and_b32 v77,0xffff0000,v91
M 30,a[28:31],46
L 5
v_lshlrev_b32 v78,16,v92
v_and_b32 v79,0xffff0000,v92
M 26,a[32:35],50
s_add_u32 s74,s74,s62
R 46,2,11264
v_lshlrev_b32 v80,16,v93
M 30,a[36:39],50
s_and_b32 s79,s54,127
v_and_b32 v81,0xffff0000,v93
v_fma_f32 v74,v74,v67,v66
M 26,a[40:43],54
s_cmp_eq_u32 s79,0
R 50,2,12288
v_fma_f32 v75,v75,v67,v66
M 30,a[44:47],54
L 5
v_fma_f32 v76,v76,v67,v66
v_fma_f32 v77,v77,v67,v66
M 26,a[48:51],58
s_cselect_b32 s94,0x3300,0
R 54,2,13312
v_fma_f32 v78,v78,v67,v66
M 30,a[52:55],58
s_cmp_eq_u32 s79,127
v_fma_f32 v79,v79,v67,v66
v_fma_f32 v80,v80,v67,v66
M 26,a[56:59],62
s_cselect_b32 s94,0xcc00,s94
R 58,2,14336
v_fma_f32 v81,v81,v67,v66
M 30,a[60:63],62
L 5
v_add_f32 v18,v18,v74
v_add_f32 v19,v19,v75
M 26,a[64:67],34
s_add_u32 s79,s54,s46
R 62,2,15360
v_add_f32 v20,v20,v76
M 30,a[68:71],34
s_and_b32 s79,s79,127
v_add_f32 v21,v21,v77
v_add_f32 v22,v22,v78
M 26,a[72:75],38
s_cmp_eq_u32 s79,0
R 34,2,16384
v_add_f32 v23,v23,v79
M 30,a[76:79],38
L 5
v_add_f32 v24,v24,v80
v_add_f32 v25,v25,v81
M 26,a[80:83],42
s_cselect_b32 s95,0x3300,0
R 38,2,17408
v_cvt_pk_bf16_f32 v82,v18,v19
M 30,a[84:87],42
s_cmp_eq_u32 s79,127
v_cvt_pk_bf16_f32 v83,v20,v21
v_cvt_pk_bf16_f32 v84,v22,v23
M 26,a[88:91],46
s_cselect_b32 s95,0xcc00,s95
R 42,2,18432
v_cvt_pk_bf16_f32 v85,v24,v25
M 30,a[92:95],46
L 5
buffer_store_dwordx4 v[82:85],v0,s[36:39],s66 offen offset:0 sc0 sc1
v_add_f32 v74,v18,v19
M 26,a[96:99],50
s_add_u32 s65,s65,s62
R 46,2,19456
v_fma_f32 v75,v18,v18,s87
M 30,a[100:103],50
s_add_u32 s75,s75,s63
v_fma_f32 v75,v19,v19,v75
v_add_f32 v74,v74,v20
M 26,a[104:107],54
R 50,2,20480
v_fma_f32 v75,v20,v20,v75
M 30,a[108:111],54
L 5
v_add_f32 v74,v74,v21
v_fma_f32 v75,v21,v21,v75
M 26,a[112:115],58
R 54,2,21504
v_add_f32 v74,v74,v22
M 30,a[116:119],58
v_fma_f32 v75,v22,v22,v75
v_add_f32 v74,v74,v23
M 26,a[120:123],62
R 58,2,22528
v_fma_f32 v75,v23,v23,v75
M 30,a[124:127],62
L 5
v_add_f32 v74,v74,v24
v_fma_f32 v75,v24,v24,v75
M 26,a[128:131],34
R 62,2,23552
v_add_f32 v74,v74,v25
M 30,a[132:135],34
v_fma_f32 v75,v25,v25,v75
M 26,a[136:139],38
R 34,2,24576
M 30,a[140:143],38
L 5
v_permlane16_swap_b32 v74,v75
M 26,a[144:147],42
R 38,2,25600
v_add_f32 v74,v74,v75
M 30,a[148:151],42
v_mov_b32 v75,v74
M 26,a[152:155],46
R 42,2,26624
M 30,a[156:159],46
L 5
v_permlane32_swap_b32 v74,v75
M 26,a[160:163],50
R 46,2,27648
v_add_f32 v74,v74,v75
M 30,a[164:167],50
ds_write_b32 v12,v74 offset:0
M 26,a[168:171],54
R 50,2,28672
M 30,a[172:175],54
L 6
M 26,a[176:179],58
R 54,2,29696
M 30,a[180:183],58
M 26,a[184:187],62
R 58,2,30720
M 30,a[188:191],62
L 6
L 3
M 26,a[192:195],34
s_waitcnt vmcnt(1)
R 62,2,31744
v_xor_b32 v2,65536,v2
M 30,a[196:199],34
s_barrier
R 86,13,0
ds_read_b32 v7,v6 offset:1024
M 26,a[200:203],38
R 34,1,0
M 30,a[204:207],38
s_nop 0
buffer_store_dword v4,v3,s[36:39],s70 offen offset:0 sc0 sc1
M 26,a[208:211],42
s_mov_b32 s70,s69
s_mov_b32 s69,s68
R 38,1,1024
M 30,a[212:215],42
s_add_u32 s68,s68,s63
M 26,a[216:219],46
L 3
R 42,1,2048
v_add_f32 v86,v86,v87
M 30,a[220:223],46
s_nop 0
v_add_f32 v88,v88,v89
v_add_f32 v86,v86,v88
M 26,a[224:227],50
L 3
R 46,1,3072
v_mul_f32 v86,s86,v86
M 30,a[228:231],50
buffer_store_dword v86,v14,s[36:39],s76 offen offset:0 sc0 sc1
)"
R"(M 26,a[232:235],54
R 50,1,4096
v_cmp_eq_u32 vcc,s53,v7
M 30,a[236:239],54
s_nop 0
s_or_b64 vcc,vcc,s[94:95]
s_cmp_eq_u64 vcc,-1
M 26,a[240:243],58
R 54,1,5120
M 30,a[244:247],58
M 26,a[248:251],62
R 58,1,6144
M 30,a[252:255],62
L 5
s_cbranch_scc1 Lg2_L20_%=
s_cmp_eq_u32 vcc_lo,-1
s_cbranch_scc1 Lg2_L10_%=
Lg2_X0_%=:
v_mfma_f32_16x16x32_bf16 v[18:21],a[0:3],v[34:37],0
s_add_u32 m0,s59,0
R 62,1,7168
v_lshlrev_b32 v74,16,v94
v_mfma_f32_16x16x32_bf16 v[22:25],a[4:7],v[34:37],0
s_mov_b32 s66,s65
buffer_load_dword v5,s[36:39],s58 offen offset:0 sc1 lds
M 18,a[8:11],38
s_mov_b32 s76,s75
R 34,1,8192
v_and_b32 v75,0xffff0000,v94
M 22,a[12:15],38
L 5
buffer_load_dwordx4 v[90:93],v0,s[36:39],s74 offen offset:0 sc1
M 18,a[16:19],42
R 38,1,9216
v_lshlrev_b32 v76,16,v95
M 22,a[20:23],42
buffer_load_dwordx2 v[66:67],v11,s[36:39],s73 offen offset:16 sc1
M 18,a[24:27],46
s_add_u32 s73,s73,s64
R 42,1,10240
v_and_b32 v77,0xffff0000,v95
M 22,a[28:31],46
L 5
v_lshlrev_b32 v78,16,v96
v_and_b32 v79,0xffff0000,v96
M 18,a[32:35],50
s_add_u32 s74,s74,s62
R 46,1,11264
v_lshlrev_b32 v80,16,v97
M 22,a[36:39],50
s_and_b32 s79,s54,127
v_and_b32 v81,0xffff0000,v97
v_fma_f32 v74,v74,v71,v70
M 18,a[40:43],54
s_cmp_eq_u32 s79,0
R 50,1,12288
v_fma_f32 v75,v75,v71,v70
M 22,a[44:47],54
L 5
v_fma_f32 v76,v76,v71,v70
v_fma_f32 v77,v77,v71,v70
M 18,a[48:51],58
s_cselect_b32 s94,0x3300,0
R 54,1,13312
v_fma_f32 v78,v78,v71,v70
M 22,a[52:55],58
s_cmp_eq_u32 s79,127
v_fma_f32 v79,v79,v71,v70
v_fma_f32 v80,v80,v71,v70
M 18,a[56:59],62
s_cselect_b32 s94,0xcc00,s94
R 58,1,14336
v_fma_f32 v81,v81,v71,v70
M 22,a[60:63],62
L 5
v_add_f32 v26,v26,v74
v_add_f32 v27,v27,v75
M 18,a[64:67],34
s_add_u32 s79,s54,s46
R 62,1,15360
v_add_f32 v28,v28,v76
M 22,a[68:71],34
s_and_b32 s79,s79,127
v_add_f32 v29,v29,v77
v_add_f32 v30,v30,v78
M 18,a[72:75],38
s_cmp_eq_u32 s79,0
R 34,1,16384
v_add_f32 v31,v31,v79
M 22,a[76:79],38
L 5
v_add_f32 v32,v32,v80
v_add_f32 v33,v33,v81
M 18,a[80:83],42
s_cselect_b32 s95,0x3300,0
R 38,1,17408
v_cvt_pk_bf16_f32 v82,v26,v27
M 22,a[84:87],42
s_cmp_eq_u32 s79,127
v_cvt_pk_bf16_f32 v83,v28,v29
v_cvt_pk_bf16_f32 v84,v30,v31
M 18,a[88:91],46
s_cselect_b32 s95,0xcc00,s95
R 42,1,18432
v_cvt_pk_bf16_f32 v85,v32,v33
M 22,a[92:95],46
L 5
buffer_store_dwordx4 v[82:85],v0,s[36:39],s67 offen offset:0 sc0 sc1
v_add_f32 v74,v26,v27
M 18,a[96:99],50
s_add_u32 s65,s65,s62
R 46,1,19456
v_fma_f32 v75,v26,v26,s87
M 22,a[100:103],50
s_add_u32 s75,s75,s63
v_fma_f32 v75,v27,v27,v75
v_add_f32 v74,v74,v28
M 18,a[104:107],54
R 50,1,20480
v_fma_f32 v75,v28,v28,v75
M 22,a[108:111],54
L 5
v_add_f32 v74,v74,v29
v_fma_f32 v75,v29,v29,v75
M 18,a[112:115],58
R 54,1,21504
v_add_f32 v74,v74,v30
M 22,a[116:119],58
v_fma_f32 v75,v30,v30,v75
v_add_f32 v74,v74,v31
M 18,a[120:123],62
R 58,1,22528
v_fma_f32 v75,v31,v31,v75
M 22,a[124:127],62
L 5
v_add_f32 v74,v74,v32
v_fma_f32 v75,v32,v32,v75
M 18,a[128:131],34
R 62,1,23552
v_add_f32 v74,v74,v33
M 22,a[132:135],34
v_fma_f32 v75,v33,v33,v75
M 18,a[136:139],38
R 34,1,24576
M 22,a[140:143],38
L 5
v_permlane16_swap_b32 v74,v75
M 18,a[144:147],42
R 38,1,25600
v_add_f32 v74,v74,v75
M 22,a[148:151],42
v_mov_b32 v75,v74
M 18,a[152:155],46
R 42,1,26624
M 22,a[156:159],46
L 5
v_permlane32_swap_b32 v74,v75
M 18,a[160:163],50
R 46,1,27648
v_add_f32 v74,v74,v75
M 22,a[164:167],50
ds_write_b32 v12,v74 offset:2048
M 18,a[168:171],54
R 50,1,28672
M 22,a[172:175],54
L 6
M 18,a[176:179],58
R 54,1,29696
M 22,a[180:183],58
M 18,a[184:187],62
R 58,1,30720
M 22,a[188:191],62
L 6
L 3
M 18,a[192:195],34
s_waitcnt vmcnt(1)
R 62,1,31744
v_xor_b32 v1,65536,v1
M 22,a[196:199],34
s_barrier
R 86,13,2048
ds_read_b32 v7,v6 offset:0
M 18,a[200:203],38
buffer_store_dword v4,v3,s[36:39],s70 offen offset:0 sc0 sc1
M 22,a[204:207],38
s_nop 0
s_mov_b32 s70,s69
s_mov_b32 s69,s68
M 18,a[208:211],42
s_add_u32 s68,s68,s63
M 22,a[212:215],42
M 18,a[216:219],46
L 1
v_add_f32 v86,v86,v87
v_add_f32 v88,v88,v89
M 22,a[220:223],46
s_nop 0
v_add_f32 v86,v86,v88
v_mul_f32 v86,s86,v86
M 18,a[224:227],50
L 0
buffer_store_dword v86,v14,s[36:39],s77 offen offset:0 sc0 sc1
M 22,a[228:231],50
v_cmp_eq_u32 vcc,s53,v7
M 18,a[232:235],54
s_or_b64 vcc,vcc,s[94:95]
M 22,a[236:239],54
s_nop 0
M 18,a[240:243],58
M 22,a[244:247],58
M 18,a[248:251],62
M 22,a[252:255],62
s_mov_b32 s80,1
s_cmp_eq_u32 vcc_lo,-1
s_cbranch_scc0 Lg2_xn0_%=
s_and_b32 s78,s54,127
s_cmp_eq_u32 s78,s85
s_cselect_b64 s[40:41],-1,0
v_cndmask_b32 v17,v15,v16,s[40:41]
s_mov_b32 m0,s57
s_add_u32 s56,s55,4096
D 17,55,0
v_lshlrev_b32 v74,16,v90
v_and_b32 v75,0xffff0000,v90
v_lshlrev_b32 v76,16,v91
v_and_b32 v77,0xffff0000,v91
v_lshlrev_b32 v78,16,v92
D 17,55,1024
v_and_b32 v79,0xffff0000,v92
v_lshlrev_b32 v80,16,v93
v_and_b32 v81,0xffff0000,v93
v_fma_f32 v74,v74,v67,v66
v_fma_f32 v75,v75,v67,v66
D 17,55,2048
v_fma_f32 v76,v76,v67,v66
v_fma_f32 v77,v77,v67,v66
v_fma_f32 v78,v78,v67,v66
v_fma_f32 v79,v79,v67,v66
v_fma_f32 v80,v80,v67,v66
D 17,55,3072
v_fma_f32 v81,v81,v67,v66
v_add_f32 v18,v18,v74
v_add_f32 v19,v19,v75
v_add_f32 v20,v20,v76
v_add_f32 v21,v21,v77
s_add_u32 m0,s57,4096
s_nop 0
D 17,56,0
v_add_f32 v22,v22,v78
v_add_f32 v23,v23,v79
v_add_f32 v24,v24,v80
v_add_f32 v25,v25,v81
v_cvt_pk_bf16_f32 v82,v18,v19
D 17,56,1024
v_cvt_pk_bf16_f32 v83,v20,v21
v_cvt_pk_bf16_f32 v84,v22,v23
v_cvt_pk_bf16_f32 v85,v24,v25
D 17,56,2048
D 17,56,3072
buffer_store_dwordx4 v[82:85],v0,s[36:39],s66 offen offset:0 sc0 sc1
v_add_f32 v74,v18,v19
v_fma_f32 v75,v18,v18,s87
s_add_u32 s55,s55,s60
s_add_u32 s57,s57,32768
s_and_b32 s57,s57,131071
s_add_u32 s54,s54,s46
v_fma_f32 v75,v19,v19,v75
v_add_f32 v74,v74,v20
v_fma_f32 v75,v20,v20,v75
v_add_f32 v74,v74,v21
v_fma_f32 v75,v21,v21,v75
v_add_f32 v74,v74,v22
v_fma_f32 v75,v22,v22,v75
v_add_f32 v74,v74,v23
v_fma_f32 v75,v23,v23,v75
v_add_f32 v74,v74,v24
v_fma_f32 v75,v24,v24,v75
v_add_f32 v74,v74,v25
v_fma_f32 v75,v25,v25,v75
s_nop 1
v_permlane16_swap_b32 v74,v75
v_add_f32 v74,v74,v75
v_mov_b32 v75,v74
s_nop 1
v_permlane32_swap_b32 v74,v75
v_add_f32 v74,v74,v75
ds_write_b32 v12,v74 offset:0
L 0
s_barrier
R 86,13,0
L 0
v_add_f32 v86,v86,v87
v_add_f32 v88,v88,v89
v_add_f32 v86,v86,v88
v_mul_f32 v86,s86,v86
buffer_store_dword v86,v14,s[36:39],s76 offen offset:0 sc0 sc1
s_mov_b32 s72,s69
s_mov_b32 s69,0x7fff0000
s_mov_b32 s66,0x7fff0000
s_mov_b32 s76,0x7fff0000
s_branch Lg2_gox_%=
Lg2_xn0_%=:
s_cmp_ge_u32 s54,256
s_cbranch_scc1 Lg2_fin0_%=
s_add_u32 m0,s59,4096
s_nop 0
buffer_load_dword v5,s[36:39],s58 offen offset:0 sc1 lds
buffer_store_dword v4,v3,s[36:39],s71 offen offset:0 sc0 sc1
s_add_u32 m0,s59,5120
s_nop 0
buffer_load_dword v5,s[36:39],s58 offen offset:0 sc1 lds
buffer_store_dword v4,v3,s[36:39],s71 offen offset:0 sc0 sc1
s_mov_b32 s83,0
v_lshlrev_b32 v74,16,v90
v_and_b32 v75,0xffff0000,v90
v_lshlrev_b32 v76,16,v91
v_and_b32 v77,0xffff0000,v91
v_lshlrev_b32 v78,16,v92
v_and_b32 v79,0xffff0000,v92
v_lshlrev_b32 v80,16,v93
v_and_b32 v81,0xffff0000,v93
v_fma_f32 v74,v74,v67,v66
v_fma_f32 v75,v75,v67,v66
v_fma_f32 v76,v76,v67,v66
v_fma_f32 v77,v77,v67,v66
v_fma_f32 v78,v78,v67,v66
v_fma_f32 v79,v79,v67,v66
v_fma_f32 v80,v80,v67,v66
v_fma_f32 v81,v81,v67,v66
v_add_f32 v18,v18,v74
v_add_f32 v19,v19,v75
v_add_f32 v20,v20,v76
v_add_f32 v21,v21,v77
v_add_f32 v22,v22,v78
v_add_f32 v23,v23,v79
v_add_f32 v24,v24,v80
v_add_f32 v25,v25,v81
v_cvt_pk_bf16_f32 v82,v18,v19
v_cvt_pk_bf16_f32 v83,v20,v21
v_cvt_pk_bf16_f32 v84,v22,v23
v_cvt_pk_bf16_f32 v85,v24,v25
buffer_store_dwordx4 v[82:85],v0,s[36:39],s66 offen offset:0 sc0 sc1
v_add_f32 v74,v18,v19
v_fma_f32 v75,v18,v18,s87
v_fma_f32 v75,v19,v19,v75
v_add_f32 v74,v74,v20
v_fma_f32 v75,v20,v20,v75
v_add_f32 v74,v74,v21
v_fma_f32 v75,v21,v21,v75
v_add_f32 v74,v74,v22
v_fma_f32 v75,v22,v22,v75
v_add_f32 v74,v74,v23
v_fma_f32 v75,v23,v23,v75
v_add_f32 v74,v74,v24
v_fma_f32 v75,v24,v24,v75
v_add_f32 v74,v74,v25
v_fma_f32 v75,v25,v25,v75
s_nop 1
v_permlane16_swap_b32 v74,v75
v_add_f32 v74,v74,v75
v_mov_b32 v75,v74
s_nop 1
v_permlane32_swap_b32 v74,v75
v_add_f32 v74,v74,v75
ds_write_b32 v12,v74 offset:0
L 0
s_barrier
R 86,13,0
L 0
v_add_f32 v86,v86,v87
v_add_f32 v88,v88,v89
v_add_f32 v86,v86,v88
v_mul_f32 v86,s86,v86
buffer_store_dword v86,v14,s[36:39],s76 offen offset:0 sc0 sc1
s_mov_b32 s72,s69
s_mov_b32 s69,0x7fff0000
s_mov_b32 s66,0x7fff0000
s_mov_b32 s76,0x7fff0000
s_branch Lg2_poll_%=
Lg2_fin0_%=:
v_lshlrev_b32 v74,16,v90
v_and_b32 v75,0xffff0000,v90
v_lshlrev_b32 v76,16,v91
v_and_b32 v77,0xffff0000,v91
v_lshlrev_b32 v78,16,v92
v_and_b32 v79,0xffff0000,v92
v_lshlrev_b32 v80,16,v93
v_and_b32 v81,0xffff0000,v93
v_fma_f32 v74,v74,v67,v66
v_fma_f32 v75,v75,v67,v66
v_fma_f32 v76,v76,v67,v66
v_fma_f32 v77,v77,v67,v66
v_fma_f32 v78,v78,v67,v66
v_fma_f32 v79,v79,v67,v66
v_fma_f32 v80,v80,v67,v66
v_fma_f32 v81,v81,v67,v66
v_add_f32 v18,v18,v74
v_add_f32 v19,v19,v75
v_add_f32 v20,v20,v76
v_add_f32 v21,v21,v77
v_add_f32 v22,v22,v78
v_add_f32 v23,v23,v79
v_add_f32 v24,v24,v80
v_add_f32 v25,v25,v81
v_cvt_pk_bf16_f32 v82,v18,v19
v_cvt_pk_bf16_f32 v83,v20,v21
v_cvt_pk_bf16_f32 v84,v22,v23
v_cvt_pk_bf16_f32 v85,v24,v25
buffer_store_dwordx4 v[82:85],v0,s[36:39],s66 offen offset:0 sc0 sc1
v_add_f32 v74,v18,v19
v_fma_f32 v75,v18,v18,s87
v_fma_f32 v75,v19,v19,v75
v_add_f32 v74,v74,v20
v_fma_f32 v75,v20,v20,v75
v_add_f32 v74,v74,v21
v_fma_f32 v75,v21,v21,v75
v_add_f32 v74,v74,v22
v_fma_f32 v75,v22,v22,v75
v_add_f32 v74,v74,v23
v_fma_f32 v75,v23,v23,v75
v_add_f32 v74,v74,v24
v_fma_f32 v75,v24,v24,v75
v_add_f32 v74,v74,v25
v_fma_f32 v75,v25,v25,v75
s_nop 1
v_permlane16_swap_b32 v74,v75
v_add_f32 v74,v74,v75
v_mov_b32 v75,v74
s_nop 1
v_permlane32_swap_b32 v74,v75
v_add_f32 v74,v74,v75
ds_write_b32 v12,v74 offset:0
L 0
s_barrier
R 86,13,0
L 0
v_add_f32 v86,v86,v87
v_add_f32 v88,v88,v89
v_add_f32 v86,v86,v88
v_mul_f32 v86,s86,v86
buffer_store_dword v86,v14,s[36:39],s76 offen offset:0 sc0 sc1
s_mov_b32 s72,s69
s_mov_b32 s69,0x7fff0000
s_mov_b32 s66,0x7fff0000
s_mov_b32 s76,0x7fff0000
s_branch Lg2_done_%=
Lg2_L21_%=:
v_mfma_f32_16x16x32_bf16 v[26:29],a[0:3],v[34:37],0
s_add_u32 s58,s58,s61
R 62,2,7168
v_lshlrev_b32 v74,16,v90
v_mfma_f32_16x16x32_bf16 v[30:33],a[4:7],v[34:37],0
s_add_u32 s58,s58,s61
buffer_load_dwordx4 v[94:97],v0,s[36:39],s74 offen offset:0 sc1
M 26,a[8:11],38
s_add_u32 m0,s59,1024
R 34,2,8192
v_and_b32 v75,0xffff0000,v90
M 30,a[12:15],38
L 5
buffer_load_dword v8,s[36:39],s58 offen offset:0 sc1 lds
M 26,a[16:19],42
s_mov_b32 m0,s57
R 38,2,9216
v_lshlrev_b32 v76,16,v91
M 30,a[20:23],42
s_mov_b32 s67,s65
buffer_load_dwordx2 v[70:71],v11,s[36:39],s73 offen offset:16 sc1
M 26,a[24:27],46
s_add_u32 s73,s73,s64
R 42,2,10240
v_and_b32 v77,0xffff0000,v91
M 30,a[28:31],46
L 5
v_lshlrev_b32 v78,16,v92
v_and_b32 v79,0xffff0000,v92
M 26,a[32:35],50
s_add_u32 s74,s74,s62
R 46,2,11264
v_lshlrev_b32 v80,16,v93
M 30,a[36:39],50
s_and_b32 s78,s54,127
v_and_b32 v81,0xffff0000,v93
v_fma_f32 v74,v74,v67,v66
M 26,a[40:43],54
s_cmp_eq_u32 s78,s85
R 50,2,12288
v_fma_f32 v75,v75,v67,v66
M 30,a[44:47],54
L 5
v_fma_f32 v76,v76,v67,v66
v_fma_f32 v77,v77,v67,v66
M 26,a[48:51],58
s_cselect_b64 s[40:41],-1,0
R 54,2,13312
v_cndmask_b32 v17,v15,v16,s[40:41]
M 30,a[52:55],58
s_add_u32 s56,s55,4096
D 17,55,0
M 26,a[56:59],62
)"
R"(s_mov_b32 s77,s75
R 58,2,14336
v_fma_f32 v78,v78,v67,v66
M 30,a[60:63],62
L 5
D 17,55,1024
M 26,a[64:67],34
R 62,2,15360
v_fma_f32 v79,v79,v67,v66
M 30,a[68:71],34
D 17,55,2048
M 26,a[72:75],38
R 34,2,16384
v_fma_f32 v80,v80,v67,v66
M 30,a[76:79],38
L 5
D 17,55,3072
M 26,a[80:83],42
s_add_u32 m0,s57,4096
R 38,2,17408
v_fma_f32 v81,v81,v67,v66
M 30,a[84:87],42
s_add_u32 s55,s55,s60
v_add_f32 v18,v18,v74
v_add_f32 v19,v19,v75
M 26,a[88:91],46
s_add_u32 s57,s57,32768
R 42,2,18432
v_add_f32 v20,v20,v76
M 30,a[92:95],46
L 5
D 17,56,0
M 26,a[96:99],50
s_and_b32 s57,s57,131071
R 46,2,19456
v_add_f32 v21,v21,v77
M 30,a[100:103],50
s_add_u32 s54,s54,s46
v_add_f32 v22,v22,v78
v_add_f32 v23,v23,v79
M 26,a[104:107],54
s_and_b32 s78,s54,127
R 50,2,20480
v_add_f32 v24,v24,v80
M 30,a[108:111],54
L 5
D 17,56,1024
M 26,a[112:115],58
s_cmp_eq_u32 s78,s85
R 54,2,21504
v_fma_f32 v75,v18,v18,s87
M 30,a[116:119],58
s_cselect_b64 s[40:41],-1,0
v_add_f32 v74,v18,v19
v_fma_f32 v75,v19,v19,v75
M 26,a[120:123],62
R 58,2,22528
v_add_f32 v74,v74,v20
M 30,a[124:127],62
L 5
D 17,56,2048
M 26,a[128:131],34
R 62,2,23552
v_fma_f32 v75,v20,v20,v75
M 30,a[132:135],34
v_add_f32 v74,v74,v21
v_fma_f32 v75,v21,v21,v75
M 26,a[136:139],38
R 34,2,24576
v_add_f32 v74,v74,v22
M 30,a[140:143],38
L 5
D 17,56,3072
M 26,a[144:147],42
s_mov_b32 m0,s57
R 38,2,25600
v_fma_f32 v75,v22,v22,v75
M 30,a[148:151],42
s_add_u32 s56,s55,4096
v_add_f32 v74,v74,v23
v_fma_f32 v75,v23,v23,v75
M 26,a[152:155],46
R 42,2,26624
v_add_f32 v25,v25,v81
M 30,a[156:159],46
L 5
v_add_f32 v74,v74,v24
v_fma_f32 v75,v24,v24,v75
v_add_f32 v74,v74,v25
v_fma_f32 v75,v25,v25,v75
M 26,a[160:163],50
R 46,2,27648
v_cndmask_b32 v17,v15,v16,s[40:41]
M 30,a[164:167],50
v_permlane16_swap_b32 v74,v75
v_add_f32 v74,v74,v75
v_mov_b32 v75,v74
M 26,a[168:171],54
R 50,2,28672
v_cvt_pk_bf16_f32 v82,v18,v19
M 30,a[172:175],54
L 5
v_permlane32_swap_b32 v74,v75
v_add_f32 v74,v74,v75
M 26,a[176:179],58
ds_write_b32 v12,v74 offset:0
R 54,2,29696
M 30,a[180:183],58
D 17,55,0
M 26,a[184:187],62
R 58,2,30720
v_cvt_pk_bf16_f32 v83,v20,v21
M 30,a[188:191],62
L 6
L 2
D 17,55,1024
M 26,a[192:195],34
s_waitcnt vmcnt(2)
R 62,2,31744
v_cvt_pk_bf16_f32 v84,v22,v23
M 30,a[196:199],34
s_barrier
D 17,55,2048
M 26,a[200:203],38
R 34,1,0
R 86,13,0
M 30,a[204:207],38
s_nop 0
D 17,55,3072
M 26,a[208:211],42
s_add_u32 m0,s57,4096
s_add_u32 s55,s55,s60
s_add_u32 s57,s57,32768
R 38,1,1024
M 30,a[212:215],42
s_and_b32 s57,s57,131071
s_add_u32 s54,s54,s46
s_and_b32 s79,s54,127
ds_read_b32 v7,v6 offset:1024
M 26,a[216:219],46
s_cmp_eq_u32 s79,0
s_cselect_b32 s94,0x3300,0
s_cmp_eq_u32 s79,127
R 42,1,2048
M 30,a[220:223],46
s_cselect_b32 s94,0xcc00,s94
s_add_u32 s79,s54,s46
L 7
buffer_store_dword v4,v3,s[36:39],s70 offen offset:0 sc0 sc1
M 26,a[224:227],50
s_and_b32 s79,s79,127
s_cmp_eq_u32 s79,0
s_cselect_b32 s95,0x3300,0
R 46,1,3072
M 30,a[228:231],50
s_cmp_eq_u32 s79,127
D 17,56,0
M 26,a[232:235],54
s_cselect_b32 s95,0xcc00,s95
R 50,1,4096
v_cvt_pk_bf16_f32 v85,v24,v25
M 30,a[236:239],54
s_add_u32 s65,s65,s62
L 7
s_mov_b32 s70,s69
D 17,56,1024
M 26,a[240:243],58
s_add_u32 s75,s75,s63
L 5
s_mov_b32 s69,s68
R 54,1,5120
M 30,a[244:247],58
s_add_u32 s68,s68,s63
L 4
v_cmp_eq_u32 vcc,s53,v7
M 26,a[248:251],62
s_or_b64 vcc,vcc,s[94:95]
s_cmp_eq_u32 vcc_lo,-1
R 58,1,6144
v_add_f32 v86,v86,v87
v_add_f32 v88,v88,v89
v_add_f32 v86,v86,v88
v_mul_f32 v86,s86,v86
M 30,a[252:255],62
s_nop 0
D 17,56,2048
D 17,56,3072
buffer_store_dwordx4 v[82:85],v0,s[36:39],s66 offen offset:0 sc0 sc1
buffer_store_dword v86,v14,s[36:39],s76 offen offset:0 sc0 sc1
v_xor_b32 v2,65536,v2
s_cbranch_scc1 Lg2_S0_%=
Lg2_N0_%=:
v_mfma_f32_16x16x32_bf16 v[18:21],a[0:3],v[34:37],0
s_add_u32 m0,s59,0
R 62,1,7168
v_lshlrev_b32 v74,16,v94
v_mfma_f32_16x16x32_bf16 v[22:25],a[4:7],v[34:37],0
s_mov_b32 s66,s65
buffer_load_dword v5,s[36:39],s58 offen offset:0 sc1 lds
M 18,a[8:11],38
s_mov_b32 s76,s75
R 34,1,8192
v_and_b32 v75,0xffff0000,v94
M 22,a[12:15],38
L 5
buffer_load_dwordx4 v[90:93],v0,s[36:39],s74 offen offset:0 sc1
M 18,a[16:19],42
R 38,1,9216
v_lshlrev_b32 v76,16,v95
M 22,a[20:23],42
buffer_load_dwordx2 v[66:67],v11,s[36:39],s73 offen offset:16 sc1
M 18,a[24:27],46
s_add_u32 s73,s73,s64
R 42,1,10240
v_and_b32 v77,0xffff0000,v95
M 22,a[28:31],46
L 5
v_lshlrev_b32 v78,16,v96
v_and_b32 v79,0xffff0000,v96
M 18,a[32:35],50
s_add_u32 s74,s74,s62
R 46,1,11264
v_lshlrev_b32 v80,16,v97
M 22,a[36:39],50
s_and_b32 s79,s54,127
v_and_b32 v81,0xffff0000,v97
v_fma_f32 v74,v74,v71,v70
M 18,a[40:43],54
s_cmp_eq_u32 s79,0
R 50,1,12288
v_fma_f32 v75,v75,v71,v70
M 22,a[44:47],54
L 5
v_fma_f32 v76,v76,v71,v70
v_fma_f32 v77,v77,v71,v70
M 18,a[48:51],58
s_cselect_b32 s94,0x3300,0
R 54,1,13312
v_fma_f32 v78,v78,v71,v70
M 22,a[52:55],58
s_cmp_eq_u32 s79,127
v_fma_f32 v79,v79,v71,v70
v_fma_f32 v80,v80,v71,v70
M 18,a[56:59],62
s_cselect_b32 s94,0xcc00,s94
R 58,1,14336
v_fma_f32 v81,v81,v71,v70
M 22,a[60:63],62
L 5
v_add_f32 v26,v26,v74
v_add_f32 v27,v27,v75
M 18,a[64:67],34
s_add_u32 s79,s54,s46
R 62,1,15360
v_add_f32 v28,v28,v76
M 22,a[68:71],34
s_and_b32 s79,s79,127
v_add_f32 v29,v29,v77
v_add_f32 v30,v30,v78
M 18,a[72:75],38
s_cmp_eq_u32 s79,0
R 34,1,16384
v_add_f32 v31,v31,v79
M 22,a[76:79],38
L 5
v_add_f32 v32,v32,v80
v_add_f32 v33,v33,v81
M 18,a[80:83],42
s_cselect_b32 s95,0x3300,0
R 38,1,17408
v_cvt_pk_bf16_f32 v82,v26,v27
M 22,a[84:87],42
s_cmp_eq_u32 s79,127
v_cvt_pk_bf16_f32 v83,v28,v29
v_cvt_pk_bf16_f32 v84,v30,v31
M 18,a[88:91],46
s_cselect_b32 s95,0xcc00,s95
R 42,1,18432
v_cvt_pk_bf16_f32 v85,v32,v33
M 22,a[92:95],46
L 5
buffer_store_dwordx4 v[82:85],v0,s[36:39],s67 offen offset:0 sc0 sc1
v_add_f32 v74,v26,v27
M 18,a[96:99],50
s_add_u32 s65,s65,s62
R 46,1,19456
v_fma_f32 v75,v26,v26,s87
M 22,a[100:103],50
s_add_u32 s75,s75,s63
v_fma_f32 v75,v27,v27,v75
v_add_f32 v74,v74,v28
M 18,a[104:107],54
R 50,1,20480
v_fma_f32 v75,v28,v28,v75
M 22,a[108:111],54
L 5
v_add_f32 v74,v74,v29
v_fma_f32 v75,v29,v29,v75
M 18,a[112:115],58
R 54,1,21504
v_add_f32 v74,v74,v30
M 22,a[116:119],58
v_fma_f32 v75,v30,v30,v75
v_add_f32 v74,v74,v31
M 18,a[120:123],62
R 58,1,22528
v_fma_f32 v75,v31,v31,v75
M 22,a[124:127],62
L 5
v_add_f32 v74,v74,v32
v_fma_f32 v75,v32,v32,v75
M 18,a[128:131],34
R 62,1,23552
v_add_f32 v74,v74,v33
M 22,a[132:135],34
v_fma_f32 v75,v33,v33,v75
M 18,a[136:139],38
R 34,1,24576
M 22,a[140:143],38
L 5
v_permlane16_swap_b32 v74,v75
M 18,a[144:147],42
R 38,1,25600
v_add_f32 v74,v74,v75
M 22,a[148:151],42
v_mov_b32 v75,v74
M 18,a[152:155],46
R 42,1,26624
M 22,a[156:159],46
L 5
v_permlane32_swap_b32 v74,v75
M 18,a[160:163],50
R 46,1,27648
v_add_f32 v74,v74,v75
M 22,a[164:167],50
ds_write_b32 v12,v74 offset:2048
M 18,a[168:171],54
R 50,1,28672
M 22,a[172:175],54
L 6
M 18,a[176:179],58
R 54,1,29696
M 22,a[180:183],58
M 18,a[184:187],62
R 58,1,30720
M 22,a[188:191],62
L 6
L 3
M 18,a[192:195],34
s_waitcnt vmcnt(1)
R 62,1,31744
v_xor_b32 v1,65536,v1
M 22,a[196:199],34
s_barrier
R 86,13,2048
ds_read_b32 v7,v6 offset:0
M 18,a[200:203],38
R 34,2,0
M 22,a[204:207],38
s_nop 0
buffer_store_dword v4,v3,s[36:39],s70 offen offset:0 sc0 sc1
M 18,a[208:211],42
s_mov_b32 s70,s69
s_mov_b32 s69,s68
R 38,2,1024
M 22,a[212:215],42
s_add_u32 s68,s68,s63
M 18,a[216:219],46
L 3
R 42,2,2048
v_add_f32 v86,v86,v87
M 22,a[220:223],46
s_nop 0
v_add_f32 v88,v88,v89
v_add_f32 v86,v86,v88
M 18,a[224:227],50
L 3
R 46,2,3072
v_mul_f32 v86,s86,v86
M 22,a[228:231],50
buffer_store_dword v86,v14,s[36:39],s77 offen offset:0 sc0 sc1
M 18,a[232:235],54
R 50,2,4096
v_cmp_eq_u32 vcc,s53,v7
M 22,a[236:239],54
s_nop 0
s_or_b64 vcc,vcc,s[94:95]
s_cmp_eq_u64 vcc,-1
M 18,a[240:243],58
R 54,2,5120
M 22,a[244:247],58
M 18,a[248:251],62
R 58,2,6144
M 22,a[252:255],62
L 5
s_cbranch_scc1 Lg2_L21_%=
s_cmp_eq_u32 vcc_lo,-1
s_cbranch_scc1 Lg2_L11_%=
Lg2_X1_%=:
v_mfma_f32_16x16x32_bf16 v[26:29],a[0:3],v[34:37],0
s_add_u32 m0,s59,1024
R 62,2,7168
v_lshlrev_b32 v74,16,v90
v_mfma_f32_16x16x32_bf16 v[30:33],a[4:7],v[34:37],0
s_mov_b32 s67,s65
buffer_load_dword v5,s[36:39],s58 offen offset:0 sc1 lds
M 26,a[8:11],38
s_mov_b32 s77,s75
R 34,2,8192
v_and_b32 v75,0xffff0000,v90
M 30,a[12:15],38
L 5
buffer_load_dwordx4 v[94:97],v0,s[36:39],s74 offen offset:0 sc1
M 26,a[16:19],42
R 38,2,9216
v_lshlrev_b32 v76,16,v91
M 30,a[20:23],42
buffer_load_dwordx2 v[70:71],v11,s[36:39],s73 offen offset:16 sc1
M 26,a[24:27],46
s_add_u32 s73,s73,s64
R 42,2,10240
v_and_b32 v77,0xffff0000,v91
M 30,a[28:31],46
L 5
v_lshlrev_b32 v78,16,v92
v_and_b32 v79,0xffff0000,v92
M 26,a[32:35],50
s_add_u32 s74,s74,s62
R 46,2,11264
v_lshlrev_b32 v80,16,v93
M 30,a[36:39],50
s_and_b32 s79,s54,127
v_and_b32 v81,0xffff0000,v93
v_fma_f32 v74,v74,v67,v66
M 26,a[40:43],54
s_cmp_eq_u32 s79,0
R 50,2,12288
v_fma_f32 v75,v75,v67,v66
M 30,a[44:47],54
L 5
v_fma_f32 v76,v76,v67,v66
v_fma_f32 v77,v77,v67,v66
M 26,a[48:51],58
s_cselect_b32 s94,0x3300,0
R 54,2,13312
v_fma_f32 v78,v78,v67,v66
M 30,a[52:55],58
s_cmp_eq_u32 s79,127
v_fma_f32 v79,v79,v67,v66
v_fma_f32 v80,v80,v67,v66
M 26,a[56:59],62
s_cselect_b32 s94,0xcc00,s94
R 58,2,14336
v_fma_f32 v81,v81,v67,v66
M 30,a[60:63],62
L 5
v_add_f32 v18,v18,v74
v_add_f32 v19,v19,v75
M 26,a[64:67],34
s_add_u32 s79,s54,s46
R 62,2,15360
v_add_f32 v20,v20,v76
M 30,a[68:71],34
s_and_b32 s79,s79,127
v_add_f32 v21,v21,v77
v_add_f32 v22,v22,v78
M 26,a[72:75],38
s_cmp_eq_u32 s79,0
R 34,2,16384
v_add_f32 v23,v23,v79
M 30,a[76:79],38
L 5
v_add_f32 v24,v24,v80
v_add_f32 v25,v25,v81
M 26,a[80:83],42
s_cselect_b32 s95,0x3300,0
R 38,2,17408
v_cvt_pk_bf16_f32 v82,v18,v19
M 30,a[84:87],42
s_cmp_eq_u32 s79,127
v_cvt_pk_bf16_f32 v83,v20,v21
v_cvt_pk_bf16_f32 v84,v22,v23
M 26,a[88:91],46
s_cselect_b32 s95,0xcc00,s95
R 42,2,18432
v_cvt_pk_bf16_f32 v85,v24,v25
M 30,a[92:95],46
L 5
buffer_store_dwordx4 v[82:85],v0,s[36:39],s66 offen offset:0 sc0 sc1
v_add_f32 v74,v18,v19
M 26,a[96:99],50
s_add_u32 s65,s65,s62
R 46,2,19456
v_fma_f32 v75,v18,v18,s87
M 30,a[100:103],50
s_add_u32 s75,s75,s63
v_fma_f32 v75,v19,v19,v75
v_add_f32 v74,v74,v20
M 26,a[104:107],54
R 50,2,20480
)"
R"(v_fma_f32 v75,v20,v20,v75
M 30,a[108:111],54
L 5
v_add_f32 v74,v74,v21
v_fma_f32 v75,v21,v21,v75
M 26,a[112:115],58
R 54,2,21504
v_add_f32 v74,v74,v22
M 30,a[116:119],58
v_fma_f32 v75,v22,v22,v75
v_add_f32 v74,v74,v23
M 26,a[120:123],62
R 58,2,22528
v_fma_f32 v75,v23,v23,v75
M 30,a[124:127],62
L 5
v_add_f32 v74,v74,v24
v_fma_f32 v75,v24,v24,v75
M 26,a[128:131],34
R 62,2,23552
v_add_f32 v74,v74,v25
M 30,a[132:135],34
v_fma_f32 v75,v25,v25,v75
M 26,a[136:139],38
R 34,2,24576
M 30,a[140:143],38
L 5
v_permlane16_swap_b32 v74,v75
M 26,a[144:147],42
R 38,2,25600
v_add_f32 v74,v74,v75
M 30,a[148:151],42
v_mov_b32 v75,v74
M 26,a[152:155],46
R 42,2,26624
M 30,a[156:159],46
L 5
v_permlane32_swap_b32 v74,v75
M 26,a[160:163],50
R 46,2,27648
v_add_f32 v74,v74,v75
M 30,a[164:167],50
ds_write_b32 v12,v74 offset:0
M 26,a[168:171],54
R 50,2,28672
M 30,a[172:175],54
L 6
M 26,a[176:179],58
R 54,2,29696
M 30,a[180:183],58
M 26,a[184:187],62
R 58,2,30720
M 30,a[188:191],62
L 6
L 3
M 26,a[192:195],34
s_waitcnt vmcnt(1)
R 62,2,31744
v_xor_b32 v2,65536,v2
M 30,a[196:199],34
s_barrier
R 86,13,0
ds_read_b32 v7,v6 offset:1024
M 26,a[200:203],38
buffer_store_dword v4,v3,s[36:39],s70 offen offset:0 sc0 sc1
M 30,a[204:207],38
s_nop 0
s_mov_b32 s70,s69
s_mov_b32 s69,s68
M 26,a[208:211],42
s_add_u32 s68,s68,s63
M 30,a[212:215],42
M 26,a[216:219],46
L 1
v_add_f32 v86,v86,v87
v_add_f32 v88,v88,v89
M 30,a[220:223],46
s_nop 0
v_add_f32 v86,v86,v88
v_mul_f32 v86,s86,v86
M 26,a[224:227],50
L 0
buffer_store_dword v86,v14,s[36:39],s76 offen offset:0 sc0 sc1
M 30,a[228:231],50
v_cmp_eq_u32 vcc,s53,v7
M 26,a[232:235],54
s_or_b64 vcc,vcc,s[94:95]
M 30,a[236:239],54
s_nop 0
M 26,a[240:243],58
M 30,a[244:247],58
M 26,a[248:251],62
M 30,a[252:255],62
s_mov_b32 s80,0
s_cmp_eq_u32 vcc_lo,-1
s_cbranch_scc0 Lg2_xn1_%=
s_and_b32 s78,s54,127
s_cmp_eq_u32 s78,s85
s_cselect_b64 s[40:41],-1,0
v_cndmask_b32 v17,v15,v16,s[40:41]
s_mov_b32 m0,s57
s_add_u32 s56,s55,4096
D 17,55,0
v_lshlrev_b32 v74,16,v94
v_and_b32 v75,0xffff0000,v94
v_lshlrev_b32 v76,16,v95
v_and_b32 v77,0xffff0000,v95
v_lshlrev_b32 v78,16,v96
D 17,55,1024
v_and_b32 v79,0xffff0000,v96
v_lshlrev_b32 v80,16,v97
v_and_b32 v81,0xffff0000,v97
v_fma_f32 v74,v74,v71,v70
v_fma_f32 v75,v75,v71,v70
D 17,55,2048
v_fma_f32 v76,v76,v71,v70
v_fma_f32 v77,v77,v71,v70
v_fma_f32 v78,v78,v71,v70
v_fma_f32 v79,v79,v71,v70
v_fma_f32 v80,v80,v71,v70
D 17,55,3072
v_fma_f32 v81,v81,v71,v70
v_add_f32 v26,v26,v74
v_add_f32 v27,v27,v75
v_add_f32 v28,v28,v76
v_add_f32 v29,v29,v77
s_add_u32 m0,s57,4096
s_nop 0
D 17,56,0
v_add_f32 v30,v30,v78
v_add_f32 v31,v31,v79
v_add_f32 v32,v32,v80
v_add_f32 v33,v33,v81
v_cvt_pk_bf16_f32 v82,v26,v27
D 17,56,1024
v_cvt_pk_bf16_f32 v83,v28,v29
v_cvt_pk_bf16_f32 v84,v30,v31
v_cvt_pk_bf16_f32 v85,v32,v33
D 17,56,2048
D 17,56,3072
buffer_store_dwordx4 v[82:85],v0,s[36:39],s67 offen offset:0 sc0 sc1
v_add_f32 v74,v26,v27
v_fma_f32 v75,v26,v26,s87
s_add_u32 s55,s55,s60
s_add_u32 s57,s57,32768
s_and_b32 s57,s57,131071
s_add_u32 s54,s54,s46
v_fma_f32 v75,v27,v27,v75
v_add_f32 v74,v74,v28
v_fma_f32 v75,v28,v28,v75
v_add_f32 v74,v74,v29
v_fma_f32 v75,v29,v29,v75
v_add_f32 v74,v74,v30
v_fma_f32 v75,v30,v30,v75
v_add_f32 v74,v74,v31
v_fma_f32 v75,v31,v31,v75
v_add_f32 v74,v74,v32
v_fma_f32 v75,v32,v32,v75
v_add_f32 v74,v74,v33
v_fma_f32 v75,v33,v33,v75
s_nop 1
v_permlane16_swap_b32 v74,v75
v_add_f32 v74,v74,v75
v_mov_b32 v75,v74
s_nop 1
v_permlane32_swap_b32 v74,v75
v_add_f32 v74,v74,v75
ds_write_b32 v12,v74 offset:2048
L 0
s_barrier
R 86,13,2048
L 0
v_add_f32 v86,v86,v87
v_add_f32 v88,v88,v89
v_add_f32 v86,v86,v88
v_mul_f32 v86,s86,v86
buffer_store_dword v86,v14,s[36:39],s77 offen offset:0 sc0 sc1
s_mov_b32 s72,s69
s_mov_b32 s69,0x7fff0000
s_mov_b32 s67,0x7fff0000
s_mov_b32 s77,0x7fff0000
s_branch Lg2_gox_%=
Lg2_xn1_%=:
s_cmp_ge_u32 s54,256
s_cbranch_scc1 Lg2_fin1_%=
s_add_u32 m0,s59,4096
s_nop 0
buffer_load_dword v5,s[36:39],s58 offen offset:0 sc1 lds
buffer_store_dword v4,v3,s[36:39],s71 offen offset:0 sc0 sc1
s_add_u32 m0,s59,5120
s_nop 0
buffer_load_dword v5,s[36:39],s58 offen offset:0 sc1 lds
buffer_store_dword v4,v3,s[36:39],s71 offen offset:0 sc0 sc1
s_mov_b32 s83,0
v_lshlrev_b32 v74,16,v94
v_and_b32 v75,0xffff0000,v94
v_lshlrev_b32 v76,16,v95
v_and_b32 v77,0xffff0000,v95
v_lshlrev_b32 v78,16,v96
v_and_b32 v79,0xffff0000,v96
v_lshlrev_b32 v80,16,v97
v_and_b32 v81,0xffff0000,v97
v_fma_f32 v74,v74,v71,v70
v_fma_f32 v75,v75,v71,v70
v_fma_f32 v76,v76,v71,v70
v_fma_f32 v77,v77,v71,v70
v_fma_f32 v78,v78,v71,v70
v_fma_f32 v79,v79,v71,v70
v_fma_f32 v80,v80,v71,v70
v_fma_f32 v81,v81,v71,v70
v_add_f32 v26,v26,v74
v_add_f32 v27,v27,v75
v_add_f32 v28,v28,v76
v_add_f32 v29,v29,v77
v_add_f32 v30,v30,v78
v_add_f32 v31,v31,v79
v_add_f32 v32,v32,v80
v_add_f32 v33,v33,v81
v_cvt_pk_bf16_f32 v82,v26,v27
v_cvt_pk_bf16_f32 v83,v28,v29
v_cvt_pk_bf16_f32 v84,v30,v31
v_cvt_pk_bf16_f32 v85,v32,v33
buffer_store_dwordx4 v[82:85],v0,s[36:39],s67 offen offset:0 sc0 sc1
v_add_f32 v74,v26,v27
v_fma_f32 v75,v26,v26,s87
v_fma_f32 v75,v27,v27,v75
v_add_f32 v74,v74,v28
v_fma_f32 v75,v28,v28,v75
v_add_f32 v74,v74,v29
v_fma_f32 v75,v29,v29,v75
v_add_f32 v74,v74,v30
v_fma_f32 v75,v30,v30,v75
v_add_f32 v74,v74,v31
v_fma_f32 v75,v31,v31,v75
v_add_f32 v74,v74,v32
v_fma_f32 v75,v32,v32,v75
v_add_f32 v74,v74,v33
v_fma_f32 v75,v33,v33,v75
s_nop 1
v_permlane16_swap_b32 v74,v75
v_add_f32 v74,v74,v75
v_mov_b32 v75,v74
s_nop 1
v_permlane32_swap_b32 v74,v75
v_add_f32 v74,v74,v75
ds_write_b32 v12,v74 offset:2048
L 0
s_barrier
R 86,13,2048
L 0
v_add_f32 v86,v86,v87
v_add_f32 v88,v88,v89
v_add_f32 v86,v86,v88
v_mul_f32 v86,s86,v86
buffer_store_dword v86,v14,s[36:39],s77 offen offset:0 sc0 sc1
s_mov_b32 s72,s69
s_mov_b32 s69,0x7fff0000
s_mov_b32 s67,0x7fff0000
s_mov_b32 s77,0x7fff0000
s_branch Lg2_poll_%=
Lg2_fin1_%=:
v_lshlrev_b32 v74,16,v94
v_and_b32 v75,0xffff0000,v94
v_lshlrev_b32 v76,16,v95
v_and_b32 v77,0xffff0000,v95
v_lshlrev_b32 v78,16,v96
v_and_b32 v79,0xffff0000,v96
v_lshlrev_b32 v80,16,v97
v_and_b32 v81,0xffff0000,v97
v_fma_f32 v74,v74,v71,v70
v_fma_f32 v75,v75,v71,v70
v_fma_f32 v76,v76,v71,v70
v_fma_f32 v77,v77,v71,v70
v_fma_f32 v78,v78,v71,v70
v_fma_f32 v79,v79,v71,v70
v_fma_f32 v80,v80,v71,v70
v_fma_f32 v81,v81,v71,v70
v_add_f32 v26,v26,v74
v_add_f32 v27,v27,v75
v_add_f32 v28,v28,v76
v_add_f32 v29,v29,v77
v_add_f32 v30,v30,v78
v_add_f32 v31,v31,v79
v_add_f32 v32,v32,v80
v_add_f32 v33,v33,v81
v_cvt_pk_bf16_f32 v82,v26,v27
v_cvt_pk_bf16_f32 v83,v28,v29
v_cvt_pk_bf16_f32 v84,v30,v31
v_cvt_pk_bf16_f32 v85,v32,v33
buffer_store_dwordx4 v[82:85],v0,s[36:39],s67 offen offset:0 sc0 sc1
v_add_f32 v74,v26,v27
v_fma_f32 v75,v26,v26,s87
v_fma_f32 v75,v27,v27,v75
v_add_f32 v74,v74,v28
v_fma_f32 v75,v28,v28,v75
v_add_f32 v74,v74,v29
v_fma_f32 v75,v29,v29,v75
v_add_f32 v74,v74,v30
v_fma_f32 v75,v30,v30,v75
v_add_f32 v74,v74,v31
v_fma_f32 v75,v31,v31,v75
v_add_f32 v74,v74,v32
v_fma_f32 v75,v32,v32,v75
v_add_f32 v74,v74,v33
v_fma_f32 v75,v33,v33,v75
s_nop 1
v_permlane16_swap_b32 v74,v75
v_add_f32 v74,v74,v75
v_mov_b32 v75,v74
s_nop 1
v_permlane32_swap_b32 v74,v75
v_add_f32 v74,v74,v75
ds_write_b32 v12,v74 offset:2048
L 0
s_barrier
R 86,13,2048
L 0
v_add_f32 v86,v86,v87
v_add_f32 v88,v88,v89
v_add_f32 v86,v86,v88
v_mul_f32 v86,s86,v86
buffer_store_dword v86,v14,s[36:39],s77 offen offset:0 sc0 sc1
s_mov_b32 s72,s69
s_mov_b32 s69,0x7fff0000
s_mov_b32 s67,0x7fff0000
s_mov_b32 s77,0x7fff0000
s_branch Lg2_done_%=
Lg2_L10_%=:
v_mfma_f32_16x16x32_bf16 v[18:21],a[0:3],v[34:37],0
s_add_u32 s58,s58,s61
R 62,1,7168
v_lshlrev_b32 v74,16,v94
v_mfma_f32_16x16x32_bf16 v[22:25],a[4:7],v[34:37],0
s_add_u32 m0,s59,0
buffer_load_dwordx4 v[90:93],v0,s[36:39],s74 offen offset:0 sc1
M 18,a[8:11],38
s_mov_b32 s66,s65
R 34,1,8192
v_and_b32 v75,0xffff0000,v94
M 22,a[12:15],38
L 5
buffer_load_dword v5,s[36:39],s58 offen offset:0 sc1 lds
M 18,a[16:19],42
s_mov_b32 m0,s57
R 38,1,9216
v_lshlrev_b32 v76,16,v95
M 22,a[20:23],42
s_mov_b32 s76,s75
buffer_load_dwordx2 v[66:67],v11,s[36:39],s73 offen offset:16 sc1
M 18,a[24:27],46
s_add_u32 s73,s73,s64
R 42,1,10240
v_and_b32 v77,0xffff0000,v95
M 22,a[28:31],46
L 5
v_lshlrev_b32 v78,16,v96
v_and_b32 v79,0xffff0000,v96
M 18,a[32:35],50
s_add_u32 s74,s74,s62
R 46,1,11264
v_lshlrev_b32 v80,16,v97
M 22,a[36:39],50
s_and_b32 s78,s54,127
v_and_b32 v81,0xffff0000,v97
v_fma_f32 v74,v74,v71,v70
M 18,a[40:43],54
s_cmp_eq_u32 s78,s85
R 50,1,12288
v_fma_f32 v75,v75,v71,v70
M 22,a[44:47],54
L 5
v_fma_f32 v76,v76,v71,v70
v_fma_f32 v77,v77,v71,v70
M 18,a[48:51],58
s_cselect_b64 s[40:41],-1,0
R 54,1,13312
v_cndmask_b32 v17,v15,v16,s[40:41]
M 22,a[52:55],58
s_add_u32 s56,s55,4096
D 17,55,0
M 18,a[56:59],62
R 58,1,14336
v_fma_f32 v78,v78,v71,v70
M 22,a[60:63],62
L 5
D 17,55,1024
M 18,a[64:67],34
R 62,1,15360
v_fma_f32 v79,v79,v71,v70
M 22,a[68:71],34
D 17,55,2048
M 18,a[72:75],38
R 34,1,16384
v_fma_f32 v80,v80,v71,v70
M 22,a[76:79],38
L 5
D 17,55,3072
M 18,a[80:83],42
s_add_u32 m0,s57,4096
R 38,1,17408
v_fma_f32 v81,v81,v71,v70
M 22,a[84:87],42
s_add_u32 s55,s55,s60
v_add_f32 v26,v26,v74
v_add_f32 v27,v27,v75
M 18,a[88:91],46
s_add_u32 s57,s57,32768
R 42,1,18432
v_add_f32 v28,v28,v76
M 22,a[92:95],46
L 5
D 17,56,0
M 18,a[96:99],50
s_and_b32 s57,s57,131071
R 46,1,19456
v_add_f32 v29,v29,v77
M 22,a[100:103],50
s_add_u32 s54,s54,s46
v_add_f32 v30,v30,v78
v_add_f32 v31,v31,v79
M 18,a[104:107],54
s_and_b32 s79,s54,127
R 50,1,20480
v_add_f32 v32,v32,v80
M 22,a[108:111],54
L 5
D 17,56,1024
M 18,a[112:115],58
s_cmp_eq_u32 s79,0
R 54,1,21504
v_fma_f32 v75,v26,v26,s87
M 22,a[116:119],58
s_cselect_b32 s94,0x3300,0
v_add_f32 v74,v26,v27
v_fma_f32 v75,v27,v27,v75
M 18,a[120:123],62
s_cmp_eq_u32 s79,127
R 58,1,22528
v_add_f32 v74,v74,v28
M 22,a[124:127],62
L 5
D 17,56,2048
M 18,a[128:131],34
s_cselect_b32 s94,0xcc00,s94
R 62,1,23552
v_fma_f32 v75,v28,v28,v75
M 22,a[132:135],34
s_add_u32 s79,s54,s46
v_add_f32 v74,v74,v29
v_fma_f32 v75,v29,v29,v75
M 18,a[136:139],38
s_and_b32 s79,s79,127
R 34,1,24576
v_add_f32 v74,v74,v30
M 22,a[140:143],38
L 5
D 17,56,3072
M 18,a[144:147],42
s_cmp_eq_u32 s79,0
R 38,1,25600
v_fma_f32 v75,v30,v30,v75
M 22,a[148:151],42
s_cselect_b32 s95,0x3300,0
v_add_f32 v74,v74,v31
v_fma_f32 v75,v31,v31,v75
M 18,a[152:155],46
s_cmp_eq_u32 s79,127
R 42,1,26624
v_add_f32 v33,v33,v81
M 22,a[156:159],46
L 5
v_add_f32 v74,v74,v32
v_fma_f32 v75,v32,v32,v75
v_add_f32 v74,v74,v33
v_fma_f32 v75,v33,v33,v75
M 18,a[160:163],50
s_cselect_b32 s95,0xcc00,s95
R 46,1,27648
v_cvt_pk_bf16_f32 v82,v26,v27
M 22,a[164:167],50
s_add_u32 s65,s65,s62
v_permlane16_swap_b32 v74,v75
v_add_f32 v74,v74,v75
v_mov_b32 v75,v74
M 18,a[168:171],54
s_add_u32 s75,s75,s63
R 50,1,28672
v_cvt_pk_bf16_f32 v83,v28,v29
M 22,a[172:175],54
L 5
v_permlane32_swap_b32 v74,v75
v_add_f32 v74,v74,v75
M 18,a[176:179],58
ds_write_b32 v12,v74 offset:2048
R 54,1,29696
M 22,a[180:183],58
v_cvt_pk_bf16_f32 v84,v30,v31
v_cvt_pk_bf16_f32 v85,v32,v33
M 18,a[184:187],62
R 58,1,30720
)"
R"(M 22,a[188:191],62
L 6
L 2
buffer_store_dwordx4 v[82:85],v0,s[36:39],s67 offen offset:0 sc0 sc1
M 18,a[192:195],34
s_waitcnt vmcnt(1)
R 62,1,31744
v_xor_b32 v1,65536,v1
M 22,a[196:199],34
s_barrier
R 86,13,2048
ds_read_b32 v7,v6 offset:0
M 18,a[200:203],38
R 34,2,0
M 22,a[204:207],38
s_nop 0
buffer_store_dword v4,v3,s[36:39],s70 offen offset:0 sc0 sc1
M 18,a[208:211],42
s_mov_b32 s70,s69
s_mov_b32 s69,s68
R 38,2,1024
M 22,a[212:215],42
s_add_u32 s68,s68,s63
M 18,a[216:219],46
L 3
R 42,2,2048
v_add_f32 v86,v86,v87
M 22,a[220:223],46
s_nop 0
v_add_f32 v88,v88,v89
v_add_f32 v86,v86,v88
M 18,a[224:227],50
L 3
R 46,2,3072
v_mul_f32 v86,s86,v86
M 22,a[228:231],50
buffer_store_dword v86,v14,s[36:39],s77 offen offset:0 sc0 sc1
M 18,a[232:235],54
R 50,2,4096
v_cmp_eq_u32 vcc,s53,v7
M 22,a[236:239],54
s_nop 0
s_or_b64 vcc,vcc,s[94:95]
s_cmp_eq_u64 vcc,-1
M 18,a[240:243],58
R 54,2,5120
M 22,a[244:247],58
M 18,a[248:251],62
R 58,2,6144
M 22,a[252:255],62
L 5
s_cbranch_scc1 Lg2_L21_%=
s_cmp_eq_u32 vcc_lo,-1
s_cbranch_scc1 Lg2_L11_%=
s_branch Lg2_X1_%=
Lg2_L11_%=:
v_mfma_f32_16x16x32_bf16 v[26:29],a[0:3],v[34:37],0
s_add_u32 s58,s58,s61
R 62,2,7168
v_lshlrev_b32 v74,16,v90
v_mfma_f32_16x16x32_bf16 v[30:33],a[4:7],v[34:37],0
s_add_u32 m0,s59,1024
buffer_load_dwordx4 v[94:97],v0,s[36:39],s74 offen offset:0 sc1
M 26,a[8:11],38
s_mov_b32 s67,s65
R 34,2,8192
v_and_b32 v75,0xffff0000,v90
M 30,a[12:15],38
L 5
buffer_load_dword v5,s[36:39],s58 offen offset:0 sc1 lds
M 26,a[16:19],42
s_mov_b32 m0,s57
R 38,2,9216
v_lshlrev_b32 v76,16,v91
M 30,a[20:23],42
s_mov_b32 s77,s75
buffer_load_dwordx2 v[70:71],v11,s[36:39],s73 offen offset:16 sc1
M 26,a[24:27],46
s_add_u32 s73,s73,s64
R 42,2,10240
v_and_b32 v77,0xffff0000,v91
M 30,a[28:31],46
L 5
v_lshlrev_b32 v78,16,v92
v_and_b32 v79,0xffff0000,v92
M 26,a[32:35],50
s_add_u32 s74,s74,s62
R 46,2,11264
v_lshlrev_b32 v80,16,v93
M 30,a[36:39],50
s_and_b32 s78,s54,127
v_and_b32 v81,0xffff0000,v93
v_fma_f32 v74,v74,v67,v66
M 26,a[40:43],54
s_cmp_eq_u32 s78,s85
R 50,2,12288
v_fma_f32 v75,v75,v67,v66
M 30,a[44:47],54
L 5
v_fma_f32 v76,v76,v67,v66
v_fma_f32 v77,v77,v67,v66
M 26,a[48:51],58
s_cselect_b64 s[40:41],-1,0
R 54,2,13312
v_cndmask_b32 v17,v15,v16,s[40:41]
M 30,a[52:55],58
s_add_u32 s56,s55,4096
D 17,55,0
M 26,a[56:59],62
R 58,2,14336
v_fma_f32 v78,v78,v67,v66
M 30,a[60:63],62
L 5
D 17,55,1024
M 26,a[64:67],34
R 62,2,15360
v_fma_f32 v79,v79,v67,v66
M 30,a[68:71],34
D 17,55,2048
M 26,a[72:75],38
R 34,2,16384
v_fma_f32 v80,v80,v67,v66
M 30,a[76:79],38
L 5
D 17,55,3072
M 26,a[80:83],42
s_add_u32 m0,s57,4096
R 38,2,17408
v_fma_f32 v81,v81,v67,v66
M 30,a[84:87],42
s_add_u32 s55,s55,s60
v_add_f32 v18,v18,v74
v_add_f32 v19,v19,v75
M 26,a[88:91],46
s_add_u32 s57,s57,32768
R 42,2,18432
v_add_f32 v20,v20,v76
M 30,a[92:95],46
L 5
D 17,56,0
M 26,a[96:99],50
s_and_b32 s57,s57,131071
R 46,2,19456
v_add_f32 v21,v21,v77
M 30,a[100:103],50
s_add_u32 s54,s54,s46
v_add_f32 v22,v22,v78
v_add_f32 v23,v23,v79
M 26,a[104:107],54
s_and_b32 s79,s54,127
R 50,2,20480
v_add_f32 v24,v24,v80
M 30,a[108:111],54
L 5
D 17,56,1024
M 26,a[112:115],58
s_cmp_eq_u32 s79,0
R 54,2,21504
v_fma_f32 v75,v18,v18,s87
M 30,a[116:119],58
s_cselect_b32 s94,0x3300,0
v_add_f32 v74,v18,v19
v_fma_f32 v75,v19,v19,v75
M 26,a[120:123],62
s_cmp_eq_u32 s79,127
R 58,2,22528
v_add_f32 v74,v74,v20
M 30,a[124:127],62
L 5
D 17,56,2048
M 26,a[128:131],34
s_cselect_b32 s94,0xcc00,s94
R 62,2,23552
v_fma_f32 v75,v20,v20,v75
M 30,a[132:135],34
s_add_u32 s79,s54,s46
v_add_f32 v74,v74,v21
v_fma_f32 v75,v21,v21,v75
M 26,a[136:139],38
s_and_b32 s79,s79,127
R 34,2,24576
v_add_f32 v74,v74,v22
M 30,a[140:143],38
L 5
D 17,56,3072
M 26,a[144:147],42
s_cmp_eq_u32 s79,0
R 38,2,25600
v_fma_f32 v75,v22,v22,v75
M 30,a[148:151],42
s_cselect_b32 s95,0x3300,0
v_add_f32 v74,v74,v23
v_fma_f32 v75,v23,v23,v75
M 26,a[152:155],46
s_cmp_eq_u32 s79,127
R 42,2,26624
v_add_f32 v25,v25,v81
M 30,a[156:159],46
L 5
v_add_f32 v74,v74,v24
v_fma_f32 v75,v24,v24,v75
v_add_f32 v74,v74,v25
v_fma_f32 v75,v25,v25,v75
M 26,a[160:163],50
s_cselect_b32 s95,0xcc00,s95
R 46,2,27648
v_cvt_pk_bf16_f32 v82,v18,v19
M 30,a[164:167],50
s_add_u32 s65,s65,s62
v_permlane16_swap_b32 v74,v75
v_add_f32 v74,v74,v75
v_mov_b32 v75,v74
M 26,a[168:171],54
s_add_u32 s75,s75,s63
R 50,2,28672
v_cvt_pk_bf16_f32 v83,v20,v21
M 30,a[172:175],54
L 5
v_permlane32_swap_b32 v74,v75
v_add_f32 v74,v74,v75
M 26,a[176:179],58
ds_write_b32 v12,v74 offset:0
R 54,2,29696
M 30,a[180:183],58
v_cvt_pk_bf16_f32 v84,v22,v23
v_cvt_pk_bf16_f32 v85,v24,v25
M 26,a[184:187],62
R 58,2,30720
M 30,a[188:191],62
L 6
L 2
buffer_store_dwordx4 v[82:85],v0,s[36:39],s66 offen offset:0 sc0 sc1
M 26,a[192:195],34
s_waitcnt vmcnt(1)
R 62,2,31744
v_xor_b32 v2,65536,v2
M 30,a[196:199],34
s_barrier
R 86,13,0
ds_read_b32 v7,v6 offset:1024
M 26,a[200:203],38
R 34,1,0
M 30,a[204:207],38
s_nop 0
buffer_store_dword v4,v3,s[36:39],s70 offen offset:0 sc0 sc1
M 26,a[208:211],42
s_mov_b32 s70,s69
s_mov_b32 s69,s68
R 38,1,1024
M 30,a[212:215],42
s_add_u32 s68,s68,s63
M 26,a[216:219],46
L 3
R 42,1,2048
v_add_f32 v86,v86,v87
M 30,a[220:223],46
s_nop 0
v_add_f32 v88,v88,v89
v_add_f32 v86,v86,v88
M 26,a[224:227],50
L 3
R 46,1,3072
v_mul_f32 v86,s86,v86
M 30,a[228:231],50
buffer_store_dword v86,v14,s[36:39],s76 offen offset:0 sc0 sc1
M 26,a[232:235],54
R 50,1,4096
v_cmp_eq_u32 vcc,s53,v7
M 30,a[236:239],54
s_nop 0
s_or_b64 vcc,vcc,s[94:95]
s_cmp_eq_u64 vcc,-1
M 26,a[240:243],58
R 54,1,5120
M 30,a[244:247],58
M 26,a[248:251],62
R 58,1,6144
M 30,a[252:255],62
L 5
s_cbranch_scc1 Lg2_L20_%=
s_cmp_eq_u32 vcc_lo,-1
s_cbranch_scc1 Lg2_L10_%=
s_branch Lg2_X0_%=
Lg2_done_%=:
s_waitcnt vmcnt(2)
s_barrier
buffer_store_dword v4,v3,s[36:39],s70 offen offset:0 sc0 sc1
s_waitcnt vmcnt(1)
s_barrier
buffer_store_dword v4,v3,s[36:39],s72 offen offset:0 sc0 sc1
s_waitcnt vmcnt(0) lgkmcnt(0)
s_barrier
Lg2_end_%=:
s_mov_b32 %0,s53
Lcode_g12_e:
.purgem M
.purgem R
.purgem T
.purgem D
.purgem L
)"
  : "=&s"(epOut) : "s"(ws), "s"(sWave), "s"(sG0), "s"(sStep), "s"(sOutOff), "s"(sFoutOff), "s"(sAuxOff), "s"(sWw), "s"(sBuf), "s"(sRk), "v"(vL16), "v"(vFlag), "v"(vSnapOff), "v"(vSnapRd), "v"(vWd0), "v"(vWd1), "v"(vWr0), "v"(vWr1), "v"(vWr2), "v"(vOutS), "v"(vAux0), "v"(vAux1), "v"(vAux2), "v"(vAux3), "v"(vAn), "v"(vAe), "s"(w12) : "v0", "v1", "v2", "v3", "v4", "v5", "v6", "v7", "v8", "v9", "v10", "v11", "v12", "v13", "v14", "v15", "v16", "v17", "v18", "v19", "v20", "v21", "v22", "v23", "v24", "v25", "v26", "v27", "v28", "v29", "v30", "v31", "v32", "v33", "v34", "v35", "v36", "v37", "v38", "v39", "v40", "v41", "v42", "v43", "v44", "v45", "v46", "v47", "v48", "v49", "v50", "v51", "v52", "v53", "v54", "v55", "v56", "v57", "v58", "v59", "v60", "v61", "v62", "v63", "v64", "v65", "v66", "v67", "v68", "v69", "v70", "v71", "v72", "v73", "v74", "v75", "v76", "v77", "v78", "v79", "v80", "v81", "v82", "v83", "v84", "v85", "v86", "v87", "v88", "v89", "v90", "v91", "v92", "v93", "v94", "v95", "v96", "v97", "v98", "v99", "v100", "v101", "v102", "v103", "v104", "v105", "v106", "v107", "v108", "v109", "v110", "v111", "v112", "v113", "v114", "v115", "v116", "v117", "v118", "v119", "v120", "v121", "v122", "v123", "v124", "v125", "v126", "v127", "v128", "v129", "v130", "v131", "v132", "v133", "v134", "v135", "v136", "v137", "v138", "v139", "v140", "v141", "v142", "v143", "v144", "v145", "v146", "v147", "v148", "v149", "v150", "v151", "v152", "v153", "v154", "v155", "v156", "v157", "v158", "v159", "v160", "v161", "v162", "v163", "v164", "v165", "v166", "v167", "v168", "v169", "v170", "v171", "v172", "v173", "v174", "v175", "v176", "v177", "v178", "v179", "v180", "v181", "v182", "v183", "v184", "v185", "v186", "v187", "v188", "v189", "v190", "v191", "v192", "v193", "v194", "v195", "v196", "v197", "v198", "v199", "v200", "v201", "v202", "v203", "v204", "v205", "v206", "v207", "v208", "v209", "v210", "v211", "v212", "v213", "v214", "v215", "v216", "v217", "v218", "v219", "v220", "v221", "v222", "v223", "v224", "v225", "v226", "v227", "v228", "v229", "v230", "v231", "a0", "a1", "a2", "a3", "a4", "a5", "a6", "a7", "a8", "a9", "a10", "a11", "a12", "a13", "a14", "a15", "a16", "a17", "a18", "a19", "a20", "a21", "a22", "a23", "a24", "a25", "a26", "a27", "a28", "a29", "a30", "a31", "a32", "a33", "a34", "a35", "a36", "a37", "a38", "a39", "a40", "a41", "a42", "a43", "a44", "a45", "a46", "a47", "a48", "a49", "a50", "a51", "a52", "a53", "a54", "a55", "a56", "a57", "a58", "a59", "a60", "a61", "a62", "a63", "a64", "a65", "a66", "a67", "a68", "a69", "a70", "a71", "a72", "a73", "a74", "a75", "a76", "a77", "a78", "a79", "a80", "a81", "a82", "a83", "a84", "a85", "a86", "a87", "a88", "a89", "a90", "a91", "a92", "a93", "a94", "a95", "a96", "a97", "a98", "a99", "a100", "a101", "a102", "a103", "a104", "a105", "a106", "a107", "a108", "a109", "a110", "a111", "a112", "a113", "a114", "a115", "a116", "a117", "a118", "a119", "a120", "a121", "a122", "a123", "a124", "a125", "a126", "a127", "a128", "a129", "a130", "a131", "a132", "a133", "a134", "a135", "a136", "a137", "a138", "a139", "a140", "a141", "a142", "a143", "a144", "a145", "a146", "a147", "a148", "a149", "a150", "a151", "a152", "a153", "a154", "a155", "a156", "a157", "a158", "a159", "a160", "a161", "a162", "a163", "a164", "a165", "a166", "a167", "a168", "a169", "a170", "a171", "a172", "a173", "a174", "a175", "a176", "a177", "a178", "a179", "a180", "a181", "a182", "a183", "a184", "a185", "a186", "a187", "a188", "a189", "a190", "a191", "a192", "a193", "a194", "a195", "a196", "a197", "a198", "a199", "a200", "a201", "a202", "a203", "a204", "a205", "a206", "a207", "a208", "a209", "a210", "a211", "a212", "a213", "a214", "a215", "a216", "a217", "a218", "a219", "a220", "a221", "a222", "a223", "a224", "a225", "a226", "a227", "a228", "a229", "a230", "a231", "a232", "a233", "a234", "a235", "a236", "a237", "a238", "a239", "a240", "a241", "a242", "a243", "a244", "a245", "a246", "a247", "a248", "a249", "a250", "a251", "a252", "a253", "a254", "a255", "s36", "s37", "s38", "s39", "s40", "s41", "s42", "s43", "s44", "s45", "s46", "s47", "s48", "s49", "s50", "s51", "s52", "s53", "s54", "s55", "s56", "s57", "s58", "s59", "s60", "s61", "s62", "s63", "s64", "s65", "s66", "s67", "s68", "s69", "s70", "s71", "s72", "s73", "s74", "s75", "s76", "s77", "s78", "s79", "s80", "s81", "s82", "s83", "s84", "s85", "s86", "s87", "s88", "s89", "s90", "s91", "s92", "s93", "s94", "s95", "s96", "s97", "vcc", "scc", "memory");
      ep = epOut;
    } else {
      LANE_ID(lr);
      const int r8 = lr & 7, seg = lr >> 3;
      const unsigned sExp = ep, sG0 = slice, sStep = NRW, sFoutOff = OFF_FXP + 128 * 1024 + wave * 128, sDbg = sDbg0;
      const unsigned vRF = (wave * 8 + (seg >> 2)) * 1024 + ((seg & 3) * 16 + r8) * 16;
      const unsigned vRwr = seg == 0 ? r8 * 64 + wave * 4 : 512 + lr * 4, vRrd = r8 * 64;
      const unsigned vRP = (lr >> 4) == ((lr & 15) >> 2) ? r8 * 64 + 16 + wave * 8 + (seg & 1) * 4 : 768 + lr * 4;
      const unsigned vRS = (wave == 0 && seg < 2) ? r8 * 32 + seg * 16 : OOB, vFlag = lr < 32 ? lr * 4 : OOB, vL4 = (lr & 15) * 128;
      (void)sDbg;
      asm volatile(
R"(.macro M d,w,x
v_mfma_f32_16x16x32_bf16 v[\d:\d+3], \w, v[\x:\x+3], v[\d:\d+3]
.endm
.macro R d,a,o
ds_read_b128 v[\d:\d+3], v\a offset:\o
.endm
.macro T d,a,o
ds_read_b64_tr_b16 \d, v\a offset:\o
.endm
.macro D a,s,o
buffer_load_dwordx4 v\a, s[36:39], s\s offen offset:\o sc1 lds
.endm
.macro L c
s_waitcnt lgkmcnt(\c)
.endm
Lcode_r_b:
s_mov_b64 s[36:37],%0
s_mov_b32 s38,111243392
s_mov_b32 s39,0x00020000
s_mov_b32 s45,%1
s_mov_b32 s46,%2
s_mov_b32 s48,%3
s_mov_b32 s53,%4
v_mov_b32 v0,%5
v_mov_b32 v1,%6
v_mov_b32 v2,%7
v_mov_b32 v3,%8
v_mov_b32 v4,%9
v_mov_b32 v5,%10
v_mov_b32 v6,%11
v_mov_b32 v7,s53
v_mbcnt_lo_u32_b32 v9,-1,0
v_mbcnt_hi_u32_b32 v9,-1,v9
v_lshlrev_b32 v9,4,v9
s_mov_b32 s82,0
s_mov_b32 s66,0
s_mov_b32 s65,0
s_mov_b32 s67,0x00003f80
s_mov_b32 s68,0x3f800000
s_mov_b32 s70,0xaaaaaaaa
s_mov_b32 s71,0xaaaaaaaa
s_mov_b32 s72,0xcccccccc
s_mov_b32 s73,0xcccccccc
s_mov_b32 s74,0xff00ff00
s_mov_b32 s75,0xff00ff00
s_mov_b32 s86,0x3a800000
s_mov_b32 s87,0x3727c5ac
s_mov_b32 s88,0xc0135761
s_mov_b32 s89,0x3d372713
v_mov_b32 v176,0x3f803f80
v_mov_b32 v177,0x3f803f80
v_mov_b32 v178,0x3f803f80
v_mov_b32 v179,0x3f803f80
s_lshr_b32 s78,s45,1
s_and_b32 s79,s45,1
s_lshl_b32 s79,s79,7
s_lshl_b32 s55,s78,15
s_add_u32 s55,s55,s79
s_add_u32 s61,s55,16777216
s_lshl_b32 s62,s45,8
s_add_u32 s62,s62,107020288
s_lshl_b32 s63,s45,9
s_add_u32 s63,s63,s48
s_lshl_b32 s59,s78,11
s_add_u32 s59,s59,110587904
s_add_u32 s78,s45,s46
s_lshr_b32 s78,s78,1
s_lshl_b32 s64,s78,11
s_add_u32 s64,s64,110587904
Lr_bp9_%=:
buffer_load_dword v8,v6,s[36:39],s59 offen offset:0 sc1
s_waitcnt vmcnt(0)
v_cmp_eq_u32 vcc,s53,v8
s_nop 0
s_cmp_eq_u64 vcc,exec
s_cselect_b32 s78,1,s66
s_cmp_lg_u32 s78,0
s_cbranch_scc1 Lr_bq9_%=
s_add_u32 s82,s82,1
s_cmp_ge_u32 s82,20000
s_cselect_b32 s66,1,0
s_sleep 4
s_branch Lr_bp9_%=
Lr_bq9_%=:
s_add_u32 s78,s55,71303168
buffer_load_dwordx4 v[32:35],v1,s[36:39],s78 offen offset:0 sc1
buffer_load_dwordx4 v[36:39],v1,s[36:39],s78 offen offset:2048 sc1
s_add_u32 s78,s55,71307264
buffer_load_dwordx4 v[40:43],v1,s[36:39],s78 offen offset:0 sc1
buffer_load_dwordx4 v[44:47],v1,s[36:39],s78 offen offset:2048 sc1
s_add_u32 s78,s55,89128960
buffer_load_dwordx4 v[48:51],v1,s[36:39],s78 offen offset:0 sc1
buffer_load_dwordx4 v[52:55],v1,s[36:39],s78 offen offset:2048 sc1
s_add_u32 s78,s55,89133056
buffer_load_dwordx4 v[56:59],v1,s[36:39],s78 offen offset:0 sc1
buffer_load_dwordx4 v[60:63],v1,s[36:39],s78 offen offset:2048 sc1
s_add_u32 s78,s55,8388608
buffer_load_dwordx4 v[64:67],v1,s[36:39],s78 offen offset:0 sc1
buffer_load_dwordx4 v[68:71],v1,s[36:39],s78 offen offset:2048 sc1
s_add_u32 s78,s55,8392704
buffer_load_dwordx4 v[72:75],v1,s[36:39],s78 offen offset:0 sc1
buffer_load_dwordx4 v[76:79],v1,s[36:39],s78 offen offset:2048 sc1
buffer_load_dword v8,v6,s[36:39],s64 offen offset:0 sc1
s_waitcnt vmcnt(0)
Lr_it0_%=:
s_mov_b32 s56,s61
s_mov_b32 s57,s62
s_mov_b32 s58,s63
s_add_u32 s45,s45,s46
s_lshr_b32 s78,s45,1
s_and_b32 s79,s45,1
s_lshl_b32 s79,s79,7
s_lshl_b32 s55,s78,15
s_add_u32 s55,s55,s79
s_add_u32 s61,s55,16777216
s_lshl_b32 s62,s45,8
s_add_u32 s62,s62,107020288
s_lshl_b32 s63,s45,9
s_add_u32 s63,s63,s48
s_lshl_b32 s59,s78,11
s_add_u32 s59,s59,110587904
s_add_u32 s78,s45,s46
s_lshr_b32 s78,s78,1
s_lshl_b32 s64,s78,11
s_add_u32 s64,s64,110587904
v_cmp_eq_u32 vcc,s53,v8
s_nop 0
s_cmp_eq_u64 vcc,exec
s_cselect_b32 s78,s45,256
s_cmp_lt_u32 s78,256
s_cbranch_scc1 Lr_ta0_%=
buffer_load_dword v8,v6,s[36:39],s59 offen offset:0 sc1
v_dot2_f32_bf16 v128,v32,s67,0
v_dot2_f32_bf16 v129,v32,s68,0
v_dot2_f32_bf16 v130,v33,s67,0
v_dot2_f32_bf16 v131,v33,s68,0
v_dot2_f32_bf16 v132,v34,s67,0
v_dot2_f32_bf16 v133,v34,s68,0
v_dot2_f32_bf16 v134,v35,s67,0
v_dot2_f32_bf16 v135,v35,s68,0
v_dot2_f32_bf16 v128,v48,s67,v128
v_dot2_f32_bf16 v129,v48,s68,v129
v_dot2_f32_bf16 v130,v49,s67,v130
v_dot2_f32_bf16 v131,v49,s68,v131
v_dot2_f32_bf16 v132,v50,s67,v132
v_dot2_f32_bf16 v133,v50,s68,v133
v_dot2_f32_bf16 v134,v51,s67,v134
v_dot2_f32_bf16 v135,v51,s68,v135
v_dot2_f32_bf16 v136,v36,s67,0
v_dot2_f32_bf16 v137,v36,s68,0
v_dot2_f32_bf16 v138,v37,s67,0
v_dot2_f32_bf16 v139,v37,s68,0
v_dot2_f32_bf16 v140,v38,s67,0
v_dot2_f32_bf16 v141,v38,s68,0
v_dot2_f32_bf16 v142,v39,s67,0
v_dot2_f32_bf16 v143,v39,s68,0
v_dot2_f32_bf16 v136,v52,s67,v136
v_dot2_f32_bf16 v137,v52,s68,v137
v_dot2_f32_bf16 v138,v53,s67,v138
v_dot2_f32_bf16 v139,v53,s68,v139
v_dot2_f32_bf16 v140,v54,s67,v140
v_dot2_f32_bf16 v141,v54,s68,v141
v_dot2_f32_bf16 v142,v55,s67,v142
v_dot2_f32_bf16 v143,v55,s68,v143
v_cvt_pk_bf16_f32 v208,v128,v129
v_cvt_pk_bf16_f32 v209,v130,v131
v_cvt_pk_bf16_f32 v210,v132,v133
v_cvt_pk_bf16_f32 v211,v134,v135
s_nop 1
v_mfma_f32_16x16x32_bf16 v[180:183],v[176:179],v[208:211],0
v_mfma_f32_16x16x32_bf16 v[184:187],v[208:211],v[208:211],0
v_dot2_f32_bf16 v144,v40,s67,0
v_dot2_f32_bf16 v145,v40,s68,0
v_dot2_f32_bf16 v146,v41,s67,0
v_dot2_f32_bf16 v147,v41,s68,0
v_dot2_f32_bf16 v148,v42,s67,0
v_dot2_f32_bf16 v149,v42,s68,0
v_dot2_f32_bf16 v150,v43,s67,0
v_dot2_f32_bf16 v151,v43,s68,0
v_dot2_f32_bf16 v144,v56,s67,v144
v_dot2_f32_bf16 v145,v56,s68,v145
v_dot2_f32_bf16 v146,v57,s67,v146
v_dot2_f32_bf16 v147,v57,s68,v147
v_dot2_f32_bf16 v148,v58,s67,v148
v_dot2_f32_bf16 v149,v58,s68,v149
v_dot2_f32_bf16 v150,v59,s67,v150
v_dot2_f32_bf16 v151,v59,s68,v151
v_cvt_pk_bf16_f32 v212,v136,v137
v_cvt_pk_bf16_f32 v213,v138,v139
v_cvt_pk_bf16_f32 v214,v140,v141
v_cvt_pk_bf16_f32 v215,v142,v143
s_nop 1
M 180,v[176:179],212
M 184,v[212:215],212
v_dot2_f32_bf16 v152,v44,s67,0
v_dot2_f32_bf16 v153,v44,s68,0
v_dot2_f32_bf16 v154,v45,s67,0
v_dot2_f32_bf16 v155,v45,s68,0
v_dot2_f32_bf16 v156,v46,s67,0
v_dot2_f32_bf16 v157,v46,s68,0
v_dot2_f32_bf16 v158,v47,s67,0
v_dot2_f32_bf16 v159,v47,s68,0
v_dot2_f32_bf16 v152,v60,s67,v152
v_dot2_f32_bf16 v153,v60,s68,v153
v_dot2_f32_bf16 v154,v61,s67,v154
v_dot2_f32_bf16 v155,v61,s68,v155
v_dot2_f32_bf16 v156,v62,s67,v156
v_dot2_f32_bf16 v157,v62,s68,v157
v_dot2_f32_bf16 v158,v63,s67,v158
v_dot2_f32_bf16 v159,v63,s68,v159
v_cvt_pk_bf16_f32 v216,v144,v145
v_cvt_pk_bf16_f32 v217,v146,v147
v_cvt_pk_bf16_f32 v218,v148,v149
v_cvt_pk_bf16_f32 v219,v150,v151
s_nop 1
M 180,v[176:179],216
M 184,v[216:219],216
v_cvt_pk_bf16_f32 v220,v152,v153
v_cvt_pk_bf16_f32 v221,v154,v155
v_cvt_pk_bf16_f32 v222,v156,v157
v_cvt_pk_bf16_f32 v223,v158,v159
s_nop 1
M 180,v[176:179],220
M 184,v[220:223],220
s_nop 6
v_add_f32_dpp v224,v180,v180 row_ror:8 row_mask:0xf bank_mask:0xf
v_cndmask_b32 v225,v184,v185,s[70:71]
v_cndmask_b32 v226,v186,v187,s[70:71]
ds_write_b32 v2,v224 offset:0
v_cndmask_b32 v225,v225,v226,s[72:73]
ds_write_b32 v0,v225 offset:0
L 0
s_barrier
R 160,3,0
R 164,3,16
R 168,3,32
L 0
v_pk_add_f32 v[160:161],v[160:161],v[162:163]
v_pk_add_f32 v[164:165],v[164:165],v[166:167]
v_pk_add_f32 v[168:169],v[168:169],v[170:171]
v_add_f32 v160,v160,v161
v_pk_add_f32 v[164:165],v[164:165],v[168:169]
v_mul_f32 v160,s86,v160
v_add_f32 v161,v164,v165
v_mul_f32 v161,s86,v161
v_fma_f32 v161,-v160,v160,v161
v_max_f32 v161,0,v161
s_waitcnt vmcnt(0)
s_cmp_eq_u32 s65,0
s_cbranch_scc1 Lr_pb1_%=
buffer_store_dword v7,v5,s[36:39],s60 offen offset:0 sc0 sc1
s_mov_b32 s65,0
Lr_pb1_%=:
v_cmp_eq_u32 vcc,s53,v8
s_nop 0
s_cmp_eq_u64 vcc,exec
s_cselect_b32 s78,s45,256
s_cmp_lt_u32 s78,256
s_cbranch_scc1 Lr_tb0_%=
buffer_load_dword v8,v6,s[36:39],s59 offen offset:0 sc1
v_add_f32 v161,s87,v161
v_rsq_f32 v228,v161
s_nop 0
v_mul_f32_e64 v230,-v160,v228
v_mov_b32 v229,v228
v_mov_b32 v231,v230
v_lshlrev_b32 v160,16,v64
v_and_b32 v161,0xffff0000,v64
v_pk_fma_f32 v[162:163],v[128:129],v[228:229],v[230:231]
v_pk_fma_f32 v[128:129],v[162:163],v[160:161],v[128:129]
v_lshlrev_b32 v164,16,v65
v_and_b32 v165,0xffff0000,v65
v_pk_fma_f32 v[166:167],v[130:131],v[228:229],v[230:231]
v_pk_fma_f32 v[130:131],v[166:167],v[164:165],v[130:131]
v_lshlrev_b32 v168,16,v66
v_and_b32 v169,0xffff0000,v66
v_pk_fma_f32 v[170:171],v[132:133],v[228:229],v[230:231]
v_pk_fma_f32 v[132:133],v[170:171],v[168:169],v[132:133]
v_lshlrev_b32 v172,16,v67
v_and_b32 v173,0xffff0000,v67
v_pk_fma_f32 v[174:175],v[134:135],v[228:229],v[230:231]
v_pk_fma_f32 v[134:135],v[174:175],v[172:173],v[134:135]
v_lshlrev_b32 v160,16,v68
v_and_b32 v161,0xffff0000,v68
v_pk_fma_f32 v[162:163],v[136:137],v[228:229],v[230:231]
v_pk_fma_f32 v[136:137],v[162:163],v[160:161],v[136:137]
v_lshlrev_b32 v164,16,v69
v_and_b32 v165,0xffff0000,v69
v_pk_fma_f32 v[166:167],v[138:139],v[228:229],v[230:231]
v_pk_fma_f32 v[138:139],v[166:167],v[164:165],v[138:139]
v_lshlrev_b32 v168,16,v70
v_and_b32 v169,0xffff0000,v70
v_pk_fma_f32 v[170:171],v[140:141],v[228:229],v[230:231]
v_pk_fma_f32 v[140:141],v[170:171],v[168:169],v[140:141]
v_lshlrev_b32 v172,16,v71
v_and_b32 v173,0xffff0000,v71
v_pk_fma_f32 v[174:175],v[142:143],v[228:229],v[230:231]
v_pk_fma_f32 v[142:143],v[174:175],v[172:173],v[142:143]
v_lshlrev_b32 v160,16,v72
v_and_b32 v161,0xffff0000,v72
v_pk_fma_f32 v[162:163],v[144:145],v[228:229],v[230:231]
v_pk_fma_f32 v[144:145],v[162:163],v[160:161],v[144:145]
v_lshlrev_b32 v164,16,v73
v_and_b32 v165,0xffff0000,v73
v_pk_fma_f32 v[166:167],v[146:147],v[228:229],v[230:231]
v_pk_fma_f32 v[146:147],v[166:167],v[164:165],v[146:147]
v_lshlrev_b32 v168,16,v74
v_and_b32 v169,0xffff0000,v74
v_pk_fma_f32 v[170:171],v[148:149],v[228:229],v[230:231]
v_pk_fma_f32 v[148:149],v[170:171],v[168:169],v[148:149]
v_lshlrev_b32 v172,16,v75
v_and_b32 v173,0xffff0000,v75
v_pk_fma_f32 v[174:175],v[150:151],v[228:229],v[230:231]
v_pk_fma_f32 v[150:151],v[174:175],v[172:173],v[150:151]
v_lshlrev_b32 v160,16,v76
v_and_b32 v161,0xffff0000,v76
v_pk_fma_f32 v[162:163],v[152:153],v[228:229],v[230:231]
v_pk_fma_f32 v[152:153],v[162:163],v[160:161],v[152:153]
v_lshlrev_b32 v164,16,v77
v_and_b32 v165,0xffff0000,v77
v_pk_fma_f32 v[166:167],v[154:155],v[228:229],v[230:231]
v_pk_fma_f32 v[154:155],v[166:167],v[164:165],v[154:155]
v_lshlrev_b32 v168,16,v78
v_and_b32 v169,0xffff0000,v78
v_pk_fma_f32 v[170:171],v[156:157],v[228:229],v[230:231]
v_pk_fma_f32 v[156:157],v[170:171],v[168:169],v[156:157]
v_lshlrev_b32 v172,16,v79
v_and_b32 v173,0xffff0000,v79
v_pk_fma_f32 v[174:175],v[158:159],v[228:229],v[230:231]
v_pk_fma_f32 v[158:159],v[174:175],v[172:173],v[158:159]
v_cvt_pk_bf16_f32 v208,v128,v129
v_cvt_pk_bf16_f32 v209,v130,v131
v_cvt_pk_bf16_f32 v210,v132,v133
v_cvt_pk_bf16_f32 v211,v134,v135
s_nop 1
v_mfma_f32_16x16x32_bf16 v[180:183],v[176:179],v[208:211],0
v_mfma_f32_16x16x32_bf16 v[184:187],v[208:211],v[208:211],0
buffer_store_dwordx4 v[208:211],v1,s[36:39],s56 offen offset:0 sc0 sc1
v_cvt_pk_bf16_f32 v212,v136,v137
v_cvt_pk_bf16_f32 v213,v138,v139
v_cvt_pk_bf16_f32 v214,v140,v141
v_cvt_pk_bf16_f32 v215,v142,v143
s_nop 1
M 180,v[176:179],212
M 184,v[212:215],212
buffer_store_dwordx4 v[212:215],v1,s[36:39],s56 offen offset:2048 sc0 sc1
v_cvt_pk_bf16_f32 v216,v144,v145
v_cvt_pk_bf16_f32 v217,v146,v147
v_cvt_pk_bf16_f32 v218,v148,v149
v_cvt_pk_bf16_f32 v219,v150,v151
s_nop 1
M 180,v[176:179],216
M 184,v[216:219],216
s_add_u32 s56,s56,4096
buffer_store_dwordx4 v[216:219],v1,s[36:39],s56 offen offset:0 sc0 sc1
v_cvt_pk_bf16_f32 v220,v152,v153
v_cvt_pk_bf16_f32 v221,v154,v155
v_cvt_pk_bf16_f32 v222,v156,v157
v_cvt_pk_bf16_f32 v223,v158,v159
s_nop 1
M 180,v[176:179],220
M 184,v[220:223],220
buffer_store_dwordx4 v[220:223],v1,s[36:39],s56 offen offset:2048 sc0 sc1
s_nop 5
v_add_f32_dpp v224,v180,v180 row_ror:8 row_mask:0xf bank_mask:0xf
v_cndmask_b32 v225,v184,v185,s[70:71]
v_cndmask_b32 v226,v186,v187,s[70:71]
ds_write_b32 v2,v224 offset:1024
v_cndmask_b32 v225,v225,v226,s[72:73]
ds_write_b32 v0,v225 offset:1024
L 0
s_barrier
R 160,3,1024
R 164,3,1040
R 168,3,1056
L 0
v_pk_add_f32 v[160:161],v[160:161],v[162:163]
v_pk_add_f32 v[164:165],v[164:165],v[166:167]
v_pk_add_f32 v[168:169],v[168:169],v[170:171]
v_add_f32 v160,v160,v161
v_pk_add_f32 v[164:165],v[164:165],v[168:169]
v_mul_f32 v160,s86,v160
v_add_f32 v161,v164,v165
v_mul_f32 v161,s86,v161
v_fma_f32 v161,-v160,v160,v161
v_max_f32 v161,0,v161
v_add_f32 v162,s87,v161
v_fma_f32 v163,s87,v162,v161
v_rsq_f32 v173,v162
v_sqrt_f32 v171,v163
v_rsq_f32 v162,v163
v_mul_f32_e64 v172,-v160,v173
v_mul_f32 v169,s88,v162
v_mul_f32 v163,v162,v162
v_mul_f32 v170,s89,v163
v_mul_f32 v170,v170,v169
v_cndmask_b32 v168,v160,v172,s[74:75]
v_cndmask_b32 v169,v169,v173,s[74:75]
buffer_store_dwordx4 v[168:171],v4,s[36:39],s57 offen offset:0 sc0 sc1
s_mov_b32 s60,s58
s_mov_b32 s65,1
s_waitcnt vmcnt(5)
v_cmp_eq_u32 vcc,s53,v8
s_nop 0
s_cmp_eq_u64 vcc,exec
s_cselect_b32 s78,s45,256
s_cmp_lt_u32 s78,256
s_cbranch_scc1 Lr_tc0_%=
s_waitcnt vmcnt(0)
s_cmp_eq_u32 s65,0
s_cbranch_scc1 Lr_pb2_%=
buffer_store_dword v7,v5,s[36:39],s60 offen offset:0 sc0 sc1
s_mov_b32 s65,0
Lr_pb2_%=:
s_cmp_ge_u32 s45,256
s_cbranch_scc1 Lr_done_%=
Lr_bp0_%=:
buffer_load_dword v8,v6,s[36:39],s59 offen offset:0 sc1
s_waitcnt vmcnt(0)
v_cmp_eq_u32 vcc,s53,v8
s_nop 0
s_cmp_eq_u64 vcc,exec
s_cselect_b32 s78,1,s66
s_cmp_lg_u32 s78,0
s_cbranch_scc1 Lr_bq0_%=
s_add_u32 s82,s82,1
s_cmp_ge_u32 s82,20000
s_cselect_b32 s66,1,0
s_sleep 4
s_branch Lr_bp0_%=
Lr_bq0_%=:
s_add_u32 s78,s55,71303168
buffer_load_dwordx4 v[80:83],v1,s[36:39],s78 offen offset:0 sc1
buffer_load_dwordx4 v[84:87],v1,s[36:39],s78 offen offset:2048 sc1
s_add_u32 s78,s55,71307264
buffer_load_dwordx4 v[88:91],v1,s[36:39],s78 offen offset:0 sc1
buffer_load_dwordx4 v[92:95],v1,s[36:39],s78 offen offset:2048 sc1
s_add_u32 s78,s55,89128960
buffer_load_dwordx4 v[96:99],v1,s[36:39],s78 offen offset:0 sc1
buffer_load_dwordx4 v[100:103],v1,s[36:39],s78 offen offset:2048 sc1
s_add_u32 s78,s55,89133056
buffer_load_dwordx4 v[104:107],v1,s[36:39],s78 offen offset:0 sc1
buffer_load_dwordx4 v[108:111],v1,s[36:39],s78 offen offset:2048 sc1
s_add_u32 s78,s55,8388608
buffer_load_dwordx4 v[112:115],v1,s[36:39],s78 offen offset:0 sc1
buffer_load_dwordx4 v[116:119],v1,s[36:39],s78 offen offset:2048 sc1
s_add_u32 s78,s55,8392704
buffer_load_dwordx4 v[120:123],v1,s[36:39],s78 offen offset:0 sc1
buffer_load_dwordx4 v[124:127],v1,s[36:39],s78 offen offset:2048 sc1
buffer_load_dword v8,v6,s[36:39],s64 offen offset:0 sc1
s_waitcnt vmcnt(0)
s_branch Lr_it1_%=
Lr_ta0_%=:
v_dot2_f32_bf16 v128,v32,s67,0
v_dot2_f32_bf16 v129,v32,s68,0
v_dot2_f32_bf16 v130,v33,s67,0
v_dot2_f32_bf16 v131,v33,s68,0
v_dot2_f32_bf16 v132,v34,s67,0
v_dot2_f32_bf16 v133,v34,s68,0
s_add_u32 s78,s55,71303168
buffer_load_dwordx4 v[80:83],v1,s[36:39],s78 offen offset:0 sc1
v_dot2_f32_bf16 v134,v35,s67,0
v_dot2_f32_bf16 v135,v35,s68,0
v_dot2_f32_bf16 v128,v48,s67,v128
v_dot2_f32_bf16 v129,v48,s68,v129
v_dot2_f32_bf16 v130,v49,s67,v130
v_dot2_f32_bf16 v131,v49,s68,v131
buffer_load_dwordx4 v[84:87],v1,s[36:39],s78 offen offset:2048 sc1
v_dot2_f32_bf16 v132,v50,s67,v132
v_dot2_f32_bf16 v133,v50,s68,v133
v_dot2_f32_bf16 v134,v51,s67,v134
v_dot2_f32_bf16 v135,v51,s68,v135
v_dot2_f32_bf16 v136,v36,s67,0
v_dot2_f32_bf16 v137,v36,s68,0
s_add_u32 s78,s55,71307264
buffer_load_dwordx4 v[88:91],v1,s[36:39],s78 offen offset:0 sc1
v_dot2_f32_bf16 v138,v37,s67,0
v_dot2_f32_bf16 v139,v37,s68,0
v_dot2_f32_bf16 v140,v38,s67,0
v_dot2_f32_bf16 v141,v38,s68,0
v_dot2_f32_bf16 v142,v39,s67,0
v_dot2_f32_bf16 v143,v39,s68,0
buffer_load_dwordx4 v[92:95],v1,s[36:39],s78 offen offset:2048 sc1
v_dot2_f32_bf16 v136,v52,s67,v136
v_dot2_f32_bf16 v137,v52,s68,v137
v_dot2_f32_bf16 v138,v53,s67,v138
v_dot2_f32_bf16 v139,v53,s68,v139
v_dot2_f32_bf16 v140,v54,s67,v140
v_dot2_f32_bf16 v141,v54,s68,v141
s_add_u32 s78,s55,89128960
buffer_load_dwordx4 v[96:99],v1,s[36:39],s78 offen offset:0 sc1
v_dot2_f32_bf16 v142,v55,s67,v142
v_dot2_f32_bf16 v143,v55,s68,v143
v_cvt_pk_bf16_f32 v208,v128,v129
v_cvt_pk_bf16_f32 v209,v130,v131
v_cvt_pk_bf16_f32 v210,v132,v133
v_cvt_pk_bf16_f32 v211,v134,v135
buffer_load_dwordx4 v[100:103],v1,s[36:39],s78 offen offset:2048 sc1
s_nop 0
v_mfma_f32_16x16x32_bf16 v[180:183],v[176:179],v[208:211],0
)"
R"(v_mfma_f32_16x16x32_bf16 v[184:187],v[208:211],v[208:211],0
v_dot2_f32_bf16 v144,v40,s67,0
v_dot2_f32_bf16 v145,v40,s68,0
v_dot2_f32_bf16 v146,v41,s67,0
v_dot2_f32_bf16 v147,v41,s68,0
s_add_u32 s78,s55,89133056
buffer_load_dwordx4 v[104:107],v1,s[36:39],s78 offen offset:0 sc1
v_dot2_f32_bf16 v148,v42,s67,0
v_dot2_f32_bf16 v149,v42,s68,0
v_dot2_f32_bf16 v150,v43,s67,0
v_dot2_f32_bf16 v151,v43,s68,0
v_dot2_f32_bf16 v144,v56,s67,v144
v_dot2_f32_bf16 v145,v56,s68,v145
buffer_load_dwordx4 v[108:111],v1,s[36:39],s78 offen offset:2048 sc1
v_dot2_f32_bf16 v146,v57,s67,v146
v_dot2_f32_bf16 v147,v57,s68,v147
v_dot2_f32_bf16 v148,v58,s67,v148
v_dot2_f32_bf16 v149,v58,s68,v149
v_dot2_f32_bf16 v150,v59,s67,v150
v_dot2_f32_bf16 v151,v59,s68,v151
s_add_u32 s78,s55,8388608
buffer_load_dwordx4 v[112:115],v1,s[36:39],s78 offen offset:0 sc1
v_cvt_pk_bf16_f32 v212,v136,v137
v_cvt_pk_bf16_f32 v213,v138,v139
v_cvt_pk_bf16_f32 v214,v140,v141
v_cvt_pk_bf16_f32 v215,v142,v143
s_nop 1
M 180,v[176:179],212
M 184,v[212:215],212
buffer_load_dwordx4 v[116:119],v1,s[36:39],s78 offen offset:2048 sc1
v_dot2_f32_bf16 v152,v44,s67,0
v_dot2_f32_bf16 v153,v44,s68,0
v_dot2_f32_bf16 v154,v45,s67,0
v_dot2_f32_bf16 v155,v45,s68,0
v_dot2_f32_bf16 v156,v46,s67,0
v_dot2_f32_bf16 v157,v46,s68,0
s_add_u32 s78,s55,8392704
buffer_load_dwordx4 v[120:123],v1,s[36:39],s78 offen offset:0 sc1
v_dot2_f32_bf16 v158,v47,s67,0
v_dot2_f32_bf16 v159,v47,s68,0
v_dot2_f32_bf16 v152,v60,s67,v152
v_dot2_f32_bf16 v153,v60,s68,v153
v_dot2_f32_bf16 v154,v61,s67,v154
v_dot2_f32_bf16 v155,v61,s68,v155
buffer_load_dwordx4 v[124:127],v1,s[36:39],s78 offen offset:2048 sc1
v_dot2_f32_bf16 v156,v62,s67,v156
v_dot2_f32_bf16 v157,v62,s68,v157
v_dot2_f32_bf16 v158,v63,s67,v158
v_dot2_f32_bf16 v159,v63,s68,v159
v_cvt_pk_bf16_f32 v216,v144,v145
v_cvt_pk_bf16_f32 v217,v146,v147
v_cvt_pk_bf16_f32 v218,v148,v149
v_cvt_pk_bf16_f32 v219,v150,v151
s_nop 1
M 180,v[176:179],216
M 184,v[216:219],216
v_cvt_pk_bf16_f32 v220,v152,v153
v_cvt_pk_bf16_f32 v221,v154,v155
v_cvt_pk_bf16_f32 v222,v156,v157
v_cvt_pk_bf16_f32 v223,v158,v159
s_nop 1
M 180,v[176:179],220
M 184,v[220:223],220
s_nop 6
v_add_f32_dpp v224,v180,v180 row_ror:8 row_mask:0xf bank_mask:0xf
v_cndmask_b32 v225,v184,v185,s[70:71]
v_cndmask_b32 v226,v186,v187,s[70:71]
ds_write_b32 v2,v224 offset:0
v_cndmask_b32 v225,v225,v226,s[72:73]
ds_write_b32 v0,v225 offset:0
L 0
s_barrier
R 160,3,0
R 164,3,16
R 168,3,32
L 0
v_pk_add_f32 v[160:161],v[160:161],v[162:163]
v_pk_add_f32 v[164:165],v[164:165],v[166:167]
v_pk_add_f32 v[168:169],v[168:169],v[170:171]
v_add_f32 v160,v160,v161
v_pk_add_f32 v[164:165],v[164:165],v[168:169]
v_mul_f32 v160,s86,v160
v_add_f32 v161,v164,v165
v_mul_f32 v161,s86,v161
v_fma_f32 v161,-v160,v160,v161
v_max_f32 v161,0,v161
s_waitcnt vmcnt(12)
s_cmp_eq_u32 s65,0
s_cbranch_scc1 Lr_pb3_%=
buffer_store_dword v7,v5,s[36:39],s60 offen offset:0 sc0 sc1
s_mov_b32 s65,0
Lr_pb3_%=:
v_add_f32 v161,s87,v161
v_rsq_f32 v228,v161
s_nop 0
v_mul_f32_e64 v230,-v160,v228
v_mov_b32 v229,v228
v_mov_b32 v231,v230
v_lshlrev_b32 v160,16,v64
v_and_b32 v161,0xffff0000,v64
v_pk_fma_f32 v[162:163],v[128:129],v[228:229],v[230:231]
v_pk_fma_f32 v[128:129],v[162:163],v[160:161],v[128:129]
v_lshlrev_b32 v164,16,v65
v_and_b32 v165,0xffff0000,v65
v_pk_fma_f32 v[166:167],v[130:131],v[228:229],v[230:231]
v_pk_fma_f32 v[130:131],v[166:167],v[164:165],v[130:131]
v_lshlrev_b32 v168,16,v66
v_and_b32 v169,0xffff0000,v66
v_pk_fma_f32 v[170:171],v[132:133],v[228:229],v[230:231]
v_pk_fma_f32 v[132:133],v[170:171],v[168:169],v[132:133]
v_lshlrev_b32 v172,16,v67
v_and_b32 v173,0xffff0000,v67
v_pk_fma_f32 v[174:175],v[134:135],v[228:229],v[230:231]
v_pk_fma_f32 v[134:135],v[174:175],v[172:173],v[134:135]
v_lshlrev_b32 v160,16,v68
v_and_b32 v161,0xffff0000,v68
v_pk_fma_f32 v[162:163],v[136:137],v[228:229],v[230:231]
v_pk_fma_f32 v[136:137],v[162:163],v[160:161],v[136:137]
v_lshlrev_b32 v164,16,v69
v_and_b32 v165,0xffff0000,v69
v_pk_fma_f32 v[166:167],v[138:139],v[228:229],v[230:231]
v_pk_fma_f32 v[138:139],v[166:167],v[164:165],v[138:139]
v_lshlrev_b32 v168,16,v70
v_and_b32 v169,0xffff0000,v70
v_pk_fma_f32 v[170:171],v[140:141],v[228:229],v[230:231]
v_pk_fma_f32 v[140:141],v[170:171],v[168:169],v[140:141]
v_lshlrev_b32 v172,16,v71
v_and_b32 v173,0xffff0000,v71
v_pk_fma_f32 v[174:175],v[142:143],v[228:229],v[230:231]
v_pk_fma_f32 v[142:143],v[174:175],v[172:173],v[142:143]
v_lshlrev_b32 v160,16,v72
v_and_b32 v161,0xffff0000,v72
v_pk_fma_f32 v[162:163],v[144:145],v[228:229],v[230:231]
v_pk_fma_f32 v[144:145],v[162:163],v[160:161],v[144:145]
v_lshlrev_b32 v164,16,v73
v_and_b32 v165,0xffff0000,v73
v_pk_fma_f32 v[166:167],v[146:147],v[228:229],v[230:231]
v_pk_fma_f32 v[146:147],v[166:167],v[164:165],v[146:147]
v_lshlrev_b32 v168,16,v74
v_and_b32 v169,0xffff0000,v74
v_pk_fma_f32 v[170:171],v[148:149],v[228:229],v[230:231]
v_pk_fma_f32 v[148:149],v[170:171],v[168:169],v[148:149]
v_lshlrev_b32 v172,16,v75
v_and_b32 v173,0xffff0000,v75
v_pk_fma_f32 v[174:175],v[150:151],v[228:229],v[230:231]
v_pk_fma_f32 v[150:151],v[174:175],v[172:173],v[150:151]
v_lshlrev_b32 v160,16,v76
v_and_b32 v161,0xffff0000,v76
v_pk_fma_f32 v[162:163],v[152:153],v[228:229],v[230:231]
v_pk_fma_f32 v[152:153],v[162:163],v[160:161],v[152:153]
v_lshlrev_b32 v164,16,v77
v_and_b32 v165,0xffff0000,v77
v_pk_fma_f32 v[166:167],v[154:155],v[228:229],v[230:231]
v_pk_fma_f32 v[154:155],v[166:167],v[164:165],v[154:155]
v_lshlrev_b32 v168,16,v78
v_and_b32 v169,0xffff0000,v78
v_pk_fma_f32 v[170:171],v[156:157],v[228:229],v[230:231]
v_pk_fma_f32 v[156:157],v[170:171],v[168:169],v[156:157]
v_lshlrev_b32 v172,16,v79
v_and_b32 v173,0xffff0000,v79
v_pk_fma_f32 v[174:175],v[158:159],v[228:229],v[230:231]
v_pk_fma_f32 v[158:159],v[174:175],v[172:173],v[158:159]
buffer_load_dword v8,v6,s[36:39],s64 offen offset:0 sc1
v_cvt_pk_bf16_f32 v208,v128,v129
v_cvt_pk_bf16_f32 v209,v130,v131
v_cvt_pk_bf16_f32 v210,v132,v133
v_cvt_pk_bf16_f32 v211,v134,v135
s_nop 1
v_mfma_f32_16x16x32_bf16 v[180:183],v[176:179],v[208:211],0
v_mfma_f32_16x16x32_bf16 v[184:187],v[208:211],v[208:211],0
buffer_store_dwordx4 v[208:211],v1,s[36:39],s56 offen offset:0 sc0 sc1
v_cvt_pk_bf16_f32 v212,v136,v137
v_cvt_pk_bf16_f32 v213,v138,v139
v_cvt_pk_bf16_f32 v214,v140,v141
v_cvt_pk_bf16_f32 v215,v142,v143
s_nop 1
M 180,v[176:179],212
M 184,v[212:215],212
buffer_store_dwordx4 v[212:215],v1,s[36:39],s56 offen offset:2048 sc0 sc1
v_cvt_pk_bf16_f32 v216,v144,v145
v_cvt_pk_bf16_f32 v217,v146,v147
v_cvt_pk_bf16_f32 v218,v148,v149
v_cvt_pk_bf16_f32 v219,v150,v151
s_nop 1
M 180,v[176:179],216
M 184,v[216:219],216
s_add_u32 s56,s56,4096
buffer_store_dwordx4 v[216:219],v1,s[36:39],s56 offen offset:0 sc0 sc1
v_cvt_pk_bf16_f32 v220,v152,v153
v_cvt_pk_bf16_f32 v221,v154,v155
v_cvt_pk_bf16_f32 v222,v156,v157
v_cvt_pk_bf16_f32 v223,v158,v159
s_nop 1
M 180,v[176:179],220
M 184,v[220:223],220
buffer_store_dwordx4 v[220:223],v1,s[36:39],s56 offen offset:2048 sc0 sc1
s_nop 5
v_add_f32_dpp v224,v180,v180 row_ror:8 row_mask:0xf bank_mask:0xf
v_cndmask_b32 v225,v184,v185,s[70:71]
v_cndmask_b32 v226,v186,v187,s[70:71]
ds_write_b32 v2,v224 offset:1024
v_cndmask_b32 v225,v225,v226,s[72:73]
ds_write_b32 v0,v225 offset:1024
L 0
s_barrier
R 160,3,1024
R 164,3,1040
R 168,3,1056
L 0
v_pk_add_f32 v[160:161],v[160:161],v[162:163]
v_pk_add_f32 v[164:165],v[164:165],v[166:167]
v_pk_add_f32 v[168:169],v[168:169],v[170:171]
v_add_f32 v160,v160,v161
v_pk_add_f32 v[164:165],v[164:165],v[168:169]
v_mul_f32 v160,s86,v160
v_add_f32 v161,v164,v165
v_mul_f32 v161,s86,v161
v_fma_f32 v161,-v160,v160,v161
v_max_f32 v161,0,v161
v_add_f32 v162,s87,v161
v_fma_f32 v163,s87,v162,v161
v_rsq_f32 v173,v162
v_sqrt_f32 v171,v163
v_rsq_f32 v162,v163
v_mul_f32_e64 v172,-v160,v173
v_mul_f32 v169,s88,v162
v_mul_f32 v163,v162,v162
v_mul_f32 v170,s89,v163
v_mul_f32 v170,v170,v169
v_cndmask_b32 v168,v160,v172,s[74:75]
v_cndmask_b32 v169,v169,v173,s[74:75]
buffer_store_dwordx4 v[168:171],v4,s[36:39],s57 offen offset:0 sc0 sc1
s_mov_b32 s60,s58
s_mov_b32 s65,1
s_waitcnt vmcnt(5)
s_branch Lr_it1_%=
Lr_tb0_%=:
v_add_f32 v161,s87,v161
v_rsq_f32 v228,v161
s_nop 0
v_mul_f32_e64 v230,-v160,v228
v_mov_b32 v229,v228
v_mov_b32 v231,v230
s_add_u32 s78,s55,71303168
buffer_load_dwordx4 v[80:83],v1,s[36:39],s78 offen offset:0 sc1
v_lshlrev_b32 v160,16,v64
v_and_b32 v161,0xffff0000,v64
v_pk_fma_f32 v[162:163],v[128:129],v[228:229],v[230:231]
v_pk_fma_f32 v[128:129],v[162:163],v[160:161],v[128:129]
v_lshlrev_b32 v164,16,v65
buffer_load_dwordx4 v[84:87],v1,s[36:39],s78 offen offset:2048 sc1
v_and_b32 v165,0xffff0000,v65
v_pk_fma_f32 v[166:167],v[130:131],v[228:229],v[230:231]
v_pk_fma_f32 v[130:131],v[166:167],v[164:165],v[130:131]
v_lshlrev_b32 v168,16,v66
v_and_b32 v169,0xffff0000,v66
s_add_u32 s78,s55,71307264
buffer_load_dwordx4 v[88:91],v1,s[36:39],s78 offen offset:0 sc1
v_pk_fma_f32 v[170:171],v[132:133],v[228:229],v[230:231]
v_pk_fma_f32 v[132:133],v[170:171],v[168:169],v[132:133]
v_lshlrev_b32 v172,16,v67
v_and_b32 v173,0xffff0000,v67
v_pk_fma_f32 v[174:175],v[134:135],v[228:229],v[230:231]
buffer_load_dwordx4 v[92:95],v1,s[36:39],s78 offen offset:2048 sc1
v_pk_fma_f32 v[134:135],v[174:175],v[172:173],v[134:135]
v_lshlrev_b32 v160,16,v68
v_and_b32 v161,0xffff0000,v68
v_pk_fma_f32 v[162:163],v[136:137],v[228:229],v[230:231]
v_pk_fma_f32 v[136:137],v[162:163],v[160:161],v[136:137]
s_add_u32 s78,s55,89128960
buffer_load_dwordx4 v[96:99],v1,s[36:39],s78 offen offset:0 sc1
v_lshlrev_b32 v164,16,v69
v_and_b32 v165,0xffff0000,v69
v_pk_fma_f32 v[166:167],v[138:139],v[228:229],v[230:231]
v_pk_fma_f32 v[138:139],v[166:167],v[164:165],v[138:139]
v_lshlrev_b32 v168,16,v70
buffer_load_dwordx4 v[100:103],v1,s[36:39],s78 offen offset:2048 sc1
v_and_b32 v169,0xffff0000,v70
v_pk_fma_f32 v[170:171],v[140:141],v[228:229],v[230:231]
v_pk_fma_f32 v[140:141],v[170:171],v[168:169],v[140:141]
v_lshlrev_b32 v172,16,v71
v_and_b32 v173,0xffff0000,v71
s_add_u32 s78,s55,89133056
buffer_load_dwordx4 v[104:107],v1,s[36:39],s78 offen offset:0 sc1
v_pk_fma_f32 v[174:175],v[142:143],v[228:229],v[230:231]
v_pk_fma_f32 v[142:143],v[174:175],v[172:173],v[142:143]
v_lshlrev_b32 v160,16,v72
v_and_b32 v161,0xffff0000,v72
v_pk_fma_f32 v[162:163],v[144:145],v[228:229],v[230:231]
buffer_load_dwordx4 v[108:111],v1,s[36:39],s78 offen offset:2048 sc1
v_pk_fma_f32 v[144:145],v[162:163],v[160:161],v[144:145]
v_lshlrev_b32 v164,16,v73
v_and_b32 v165,0xffff0000,v73
v_pk_fma_f32 v[166:167],v[146:147],v[228:229],v[230:231]
v_pk_fma_f32 v[146:147],v[166:167],v[164:165],v[146:147]
s_add_u32 s78,s55,8388608
buffer_load_dwordx4 v[112:115],v1,s[36:39],s78 offen offset:0 sc1
v_lshlrev_b32 v168,16,v74
v_and_b32 v169,0xffff0000,v74
v_pk_fma_f32 v[170:171],v[148:149],v[228:229],v[230:231]
v_pk_fma_f32 v[148:149],v[170:171],v[168:169],v[148:149]
v_lshlrev_b32 v172,16,v75
buffer_load_dwordx4 v[116:119],v1,s[36:39],s78 offen offset:2048 sc1
v_and_b32 v173,0xffff0000,v75
v_pk_fma_f32 v[174:175],v[150:151],v[228:229],v[230:231]
v_pk_fma_f32 v[150:151],v[174:175],v[172:173],v[150:151]
v_lshlrev_b32 v160,16,v76
v_and_b32 v161,0xffff0000,v76
s_add_u32 s78,s55,8392704
buffer_load_dwordx4 v[120:123],v1,s[36:39],s78 offen offset:0 sc1
v_pk_fma_f32 v[162:163],v[152:153],v[228:229],v[230:231]
v_pk_fma_f32 v[152:153],v[162:163],v[160:161],v[152:153]
v_lshlrev_b32 v164,16,v77
v_and_b32 v165,0xffff0000,v77
v_pk_fma_f32 v[166:167],v[154:155],v[228:229],v[230:231]
buffer_load_dwordx4 v[124:127],v1,s[36:39],s78 offen offset:2048 sc1
v_pk_fma_f32 v[154:155],v[166:167],v[164:165],v[154:155]
v_lshlrev_b32 v168,16,v78
v_and_b32 v169,0xffff0000,v78
v_pk_fma_f32 v[170:171],v[156:157],v[228:229],v[230:231]
v_pk_fma_f32 v[156:157],v[170:171],v[168:169],v[156:157]
v_lshlrev_b32 v172,16,v79
v_and_b32 v173,0xffff0000,v79
v_pk_fma_f32 v[174:175],v[158:159],v[228:229],v[230:231]
v_pk_fma_f32 v[158:159],v[174:175],v[172:173],v[158:159]
buffer_load_dword v8,v6,s[36:39],s64 offen offset:0 sc1
v_cvt_pk_bf16_f32 v208,v128,v129
v_cvt_pk_bf16_f32 v209,v130,v131
v_cvt_pk_bf16_f32 v210,v132,v133
v_cvt_pk_bf16_f32 v211,v134,v135
s_nop 1
v_mfma_f32_16x16x32_bf16 v[180:183],v[176:179],v[208:211],0
v_mfma_f32_16x16x32_bf16 v[184:187],v[208:211],v[208:211],0
buffer_store_dwordx4 v[208:211],v1,s[36:39],s56 offen offset:0 sc0 sc1
v_cvt_pk_bf16_f32 v212,v136,v137
v_cvt_pk_bf16_f32 v213,v138,v139
v_cvt_pk_bf16_f32 v214,v140,v141
v_cvt_pk_bf16_f32 v215,v142,v143
s_nop 1
M 180,v[176:179],212
M 184,v[212:215],212
buffer_store_dwordx4 v[212:215],v1,s[36:39],s56 offen offset:2048 sc0 sc1
v_cvt_pk_bf16_f32 v216,v144,v145
v_cvt_pk_bf16_f32 v217,v146,v147
v_cvt_pk_bf16_f32 v218,v148,v149
v_cvt_pk_bf16_f32 v219,v150,v151
s_nop 1
M 180,v[176:179],216
M 184,v[216:219],216
s_add_u32 s56,s56,4096
buffer_store_dwordx4 v[216:219],v1,s[36:39],s56 offen offset:0 sc0 sc1
v_cvt_pk_bf16_f32 v220,v152,v153
v_cvt_pk_bf16_f32 v221,v154,v155
v_cvt_pk_bf16_f32 v222,v156,v157
v_cvt_pk_bf16_f32 v223,v158,v159
s_nop 1
M 180,v[176:179],220
M 184,v[220:223],220
buffer_store_dwordx4 v[220:223],v1,s[36:39],s56 offen offset:2048 sc0 sc1
s_nop 5
v_add_f32_dpp v224,v180,v180 row_ror:8 row_mask:0xf bank_mask:0xf
v_cndmask_b32 v225,v184,v185,s[70:71]
v_cndmask_b32 v226,v186,v187,s[70:71]
ds_write_b32 v2,v224 offset:1024
v_cndmask_b32 v225,v225,v226,s[72:73]
ds_write_b32 v0,v225 offset:1024
L 0
s_barrier
R 160,3,1024
R 164,3,1040
R 168,3,1056
L 0
v_pk_add_f32 v[160:161],v[160:161],v[162:163]
v_pk_add_f32 v[164:165],v[164:165],v[166:167]
v_pk_add_f32 v[168:169],v[168:169],v[170:171]
v_add_f32 v160,v160,v161
v_pk_add_f32 v[164:165],v[164:165],v[168:169]
v_mul_f32 v160,s86,v160
v_add_f32 v161,v164,v165
v_mul_f32 v161,s86,v161
v_fma_f32 v161,-v160,v160,v161
v_max_f32 v161,0,v161
v_add_f32 v162,s87,v161
v_fma_f32 v163,s87,v162,v161
v_rsq_f32 v173,v162
v_sqrt_f32 v171,v163
v_rsq_f32 v162,v163
v_mul_f32_e64 v172,-v160,v173
v_mul_f32 v169,s88,v162
v_mul_f32 v163,v162,v162
v_mul_f32 v170,s89,v163
v_mul_f32 v170,v170,v169
v_cndmask_b32 v168,v160,v172,s[74:75]
v_cndmask_b32 v169,v169,v173,s[74:75]
buffer_store_dwordx4 v[168:171],v4,s[36:39],s57 offen offset:0 sc0 sc1
s_mov_b32 s60,s58
s_mov_b32 s65,1
s_waitcnt vmcnt(5)
s_branch Lr_it1_%=
Lr_tc0_%=:
s_add_u32 s78,s55,71303168
buffer_load_dwordx4 v[80:83],v1,s[36:39],s78 offen offset:0 sc1
buffer_load_dwordx4 v[84:87],v1,s[36:39],s78 offen offset:2048 sc1
s_add_u32 s78,s55,71307264
buffer_load_dwordx4 v[88:91],v1,s[36:39],s78 offen offset:0 sc1
buffer_load_dwordx4 v[92:95],v1,s[36:39],s78 offen offset:2048 sc1
s_add_u32 s78,s55,89128960
buffer_load_dwordx4 v[96:99],v1,s[36:39],s78 offen offset:0 sc1
buffer_load_dwordx4 v[100:103],v1,s[36:39],s78 offen offset:2048 sc1
s_add_u32 s78,s55,89133056
buffer_load_dwordx4 v[104:107],v1,s[36:39],s78 offen offset:0 sc1
buffer_load_dwordx4 v[108:111],v1,s[36:39],s78 offen offset:2048 sc1
s_add_u32 s78,s55,8388608
buffer_load_dwordx4 v[112:115],v1,s[36:39],s78 offen offset:0 sc1
buffer_load_dwordx4 v[116:119],v1,s[36:39],s78 offen offset:2048 sc1
s_add_u32 s78,s55,8392704
buffer_load_dwordx4 v[120:123],v1,s[36:39],s78 offen offset:0 sc1
buffer_load_dwordx4 v[124:127],v1,s[36:39],s78 offen offset:2048 sc1
buffer_load_dword v8,v6,s[36:39],s64 offen offset:0 sc1
s_waitcnt vmcnt(13)
s_cmp_eq_u32 s65,0
s_cbranch_scc1 Lr_pb4_%=
buffer_store_dword v7,v5,s[36:39],s60 offen offset:0 sc0 sc1
s_mov_b32 s65,0
Lr_pb4_%=:
s_waitcnt vmcnt(0)
s_branch Lr_it1_%=
Lr_it1_%=:
s_mov_b32 s56,s61
s_mov_b32 s57,s62
s_mov_b32 s58,s63
s_add_u32 s45,s45,s46
s_lshr_b32 s78,s45,1
s_and_b32 s79,s45,1
s_lshl_b32 s79,s79,7
s_lshl_b32 s55,s78,15
s_add_u32 s55,s55,s79
s_add_u32 s61,s55,16777216
s_lshl_b32 s62,s45,8
s_add_u32 s62,s62,107020288
s_lshl_b32 s63,s45,9
s_add_u32 s63,s63,s48
s_lshl_b32 s59,s78,11
s_add_u32 s59,s59,110587904
s_add_u32 s78,s45,s46
s_lshr_b32 s78,s78,1
s_lshl_b32 s64,s78,11
s_add_u32 s64,s64,110587904
v_cmp_eq_u32 vcc,s53,v8
s_nop 0
s_cmp_eq_u64 vcc,exec
s_cselect_b32 s78,s45,256
s_cmp_lt_u32 s78,256
s_cbranch_scc1 Lr_ta1_%=
buffer_load_dword v8,v6,s[36:39],s59 offen offset:0 sc1
v_dot2_f32_bf16 v128,v80,s67,0
v_dot2_f32_bf16 v129,v80,s68,0
v_dot2_f32_bf16 v130,v81,s67,0
v_dot2_f32_bf16 v131,v81,s68,0
v_dot2_f32_bf16 v132,v82,s67,0
v_dot2_f32_bf16 v133,v82,s68,0
v_dot2_f32_bf16 v134,v83,s67,0
v_dot2_f32_bf16 v135,v83,s68,0
v_dot2_f32_bf16 v128,v96,s67,v128
v_dot2_f32_bf16 v129,v96,s68,v129
v_dot2_f32_bf16 v130,v97,s67,v130
v_dot2_f32_bf16 v131,v97,s68,v131
v_dot2_f32_bf16 v132,v98,s67,v132
v_dot2_f32_bf16 v133,v98,s68,v133
v_dot2_f32_bf16 v134,v99,s67,v134
v_dot2_f32_bf16 v135,v99,s68,v135
v_dot2_f32_bf16 v136,v84,s67,0
v_dot2_f32_bf16 v137,v84,s68,0
v_dot2_f32_bf16 v138,v85,s67,0
v_dot2_f32_bf16 v139,v85,s68,0
v_dot2_f32_bf16 v140,v86,s67,0
v_dot2_f32_bf16 v141,v86,s68,0
v_dot2_f32_bf16 v142,v87,s67,0
v_dot2_f32_bf16 v143,v87,s68,0
v_dot2_f32_bf16 v136,v100,s67,v136
v_dot2_f32_bf16 v137,v100,s68,v137
v_dot2_f32_bf16 v138,v101,s67,v138
v_dot2_f32_bf16 v139,v101,s68,v139
v_dot2_f32_bf16 v140,v102,s67,v140
v_dot2_f32_bf16 v141,v102,s68,v141
v_dot2_f32_bf16 v142,v103,s67,v142
v_dot2_f32_bf16 v143,v103,s68,v143
v_cvt_pk_bf16_f32 v208,v128,v129
v_cvt_pk_bf16_f32 v209,v130,v131
v_cvt_pk_bf16_f32 v210,v132,v133
v_cvt_pk_bf16_f32 v211,v134,v135
s_nop 1
v_mfma_f32_16x16x32_bf16 v[180:183],v[176:179],v[208:211],0
v_mfma_f32_16x16x32_bf16 v[184:187],v[208:211],v[208:211],0
v_dot2_f32_bf16 v144,v88,s67,0
v_dot2_f32_bf16 v145,v88,s68,0
v_dot2_f32_bf16 v146,v89,s67,0
v_dot2_f32_bf16 v147,v89,s68,0
v_dot2_f32_bf16 v148,v90,s67,0
v_dot2_f32_bf16 v149,v90,s68,0
)"
R"(v_dot2_f32_bf16 v150,v91,s67,0
v_dot2_f32_bf16 v151,v91,s68,0
v_dot2_f32_bf16 v144,v104,s67,v144
v_dot2_f32_bf16 v145,v104,s68,v145
v_dot2_f32_bf16 v146,v105,s67,v146
v_dot2_f32_bf16 v147,v105,s68,v147
v_dot2_f32_bf16 v148,v106,s67,v148
v_dot2_f32_bf16 v149,v106,s68,v149
v_dot2_f32_bf16 v150,v107,s67,v150
v_dot2_f32_bf16 v151,v107,s68,v151
v_cvt_pk_bf16_f32 v212,v136,v137
v_cvt_pk_bf16_f32 v213,v138,v139
v_cvt_pk_bf16_f32 v214,v140,v141
v_cvt_pk_bf16_f32 v215,v142,v143
s_nop 1
M 180,v[176:179],212
M 184,v[212:215],212
v_dot2_f32_bf16 v152,v92,s67,0
v_dot2_f32_bf16 v153,v92,s68,0
v_dot2_f32_bf16 v154,v93,s67,0
v_dot2_f32_bf16 v155,v93,s68,0
v_dot2_f32_bf16 v156,v94,s67,0
v_dot2_f32_bf16 v157,v94,s68,0
v_dot2_f32_bf16 v158,v95,s67,0
v_dot2_f32_bf16 v159,v95,s68,0
v_dot2_f32_bf16 v152,v108,s67,v152
v_dot2_f32_bf16 v153,v108,s68,v153
v_dot2_f32_bf16 v154,v109,s67,v154
v_dot2_f32_bf16 v155,v109,s68,v155
v_dot2_f32_bf16 v156,v110,s67,v156
v_dot2_f32_bf16 v157,v110,s68,v157
v_dot2_f32_bf16 v158,v111,s67,v158
v_dot2_f32_bf16 v159,v111,s68,v159
v_cvt_pk_bf16_f32 v216,v144,v145
v_cvt_pk_bf16_f32 v217,v146,v147
v_cvt_pk_bf16_f32 v218,v148,v149
v_cvt_pk_bf16_f32 v219,v150,v151
s_nop 1
M 180,v[176:179],216
M 184,v[216:219],216
v_cvt_pk_bf16_f32 v220,v152,v153
v_cvt_pk_bf16_f32 v221,v154,v155
v_cvt_pk_bf16_f32 v222,v156,v157
v_cvt_pk_bf16_f32 v223,v158,v159
s_nop 1
M 180,v[176:179],220
M 184,v[220:223],220
s_nop 6
v_add_f32_dpp v224,v180,v180 row_ror:8 row_mask:0xf bank_mask:0xf
v_cndmask_b32 v225,v184,v185,s[70:71]
v_cndmask_b32 v226,v186,v187,s[70:71]
ds_write_b32 v2,v224 offset:0
v_cndmask_b32 v225,v225,v226,s[72:73]
ds_write_b32 v0,v225 offset:0
L 0
s_barrier
R 160,3,0
R 164,3,16
R 168,3,32
L 0
v_pk_add_f32 v[160:161],v[160:161],v[162:163]
v_pk_add_f32 v[164:165],v[164:165],v[166:167]
v_pk_add_f32 v[168:169],v[168:169],v[170:171]
v_add_f32 v160,v160,v161
v_pk_add_f32 v[164:165],v[164:165],v[168:169]
v_mul_f32 v160,s86,v160
v_add_f32 v161,v164,v165
v_mul_f32 v161,s86,v161
v_fma_f32 v161,-v160,v160,v161
v_max_f32 v161,0,v161
s_waitcnt vmcnt(0)
s_cmp_eq_u32 s65,0
s_cbranch_scc1 Lr_pb5_%=
buffer_store_dword v7,v5,s[36:39],s60 offen offset:0 sc0 sc1
s_mov_b32 s65,0
Lr_pb5_%=:
v_cmp_eq_u32 vcc,s53,v8
s_nop 0
s_cmp_eq_u64 vcc,exec
s_cselect_b32 s78,s45,256
s_cmp_lt_u32 s78,256
s_cbranch_scc1 Lr_tb1_%=
buffer_load_dword v8,v6,s[36:39],s59 offen offset:0 sc1
v_add_f32 v161,s87,v161
v_rsq_f32 v228,v161
s_nop 0
v_mul_f32_e64 v230,-v160,v228
v_mov_b32 v229,v228
v_mov_b32 v231,v230
v_lshlrev_b32 v160,16,v112
v_and_b32 v161,0xffff0000,v112
v_pk_fma_f32 v[162:163],v[128:129],v[228:229],v[230:231]
v_pk_fma_f32 v[128:129],v[162:163],v[160:161],v[128:129]
v_lshlrev_b32 v164,16,v113
v_and_b32 v165,0xffff0000,v113
v_pk_fma_f32 v[166:167],v[130:131],v[228:229],v[230:231]
v_pk_fma_f32 v[130:131],v[166:167],v[164:165],v[130:131]
v_lshlrev_b32 v168,16,v114
v_and_b32 v169,0xffff0000,v114
v_pk_fma_f32 v[170:171],v[132:133],v[228:229],v[230:231]
v_pk_fma_f32 v[132:133],v[170:171],v[168:169],v[132:133]
v_lshlrev_b32 v172,16,v115
v_and_b32 v173,0xffff0000,v115
v_pk_fma_f32 v[174:175],v[134:135],v[228:229],v[230:231]
v_pk_fma_f32 v[134:135],v[174:175],v[172:173],v[134:135]
v_lshlrev_b32 v160,16,v116
v_and_b32 v161,0xffff0000,v116
v_pk_fma_f32 v[162:163],v[136:137],v[228:229],v[230:231]
v_pk_fma_f32 v[136:137],v[162:163],v[160:161],v[136:137]
v_lshlrev_b32 v164,16,v117
v_and_b32 v165,0xffff0000,v117
v_pk_fma_f32 v[166:167],v[138:139],v[228:229],v[230:231]
v_pk_fma_f32 v[138:139],v[166:167],v[164:165],v[138:139]
v_lshlrev_b32 v168,16,v118
v_and_b32 v169,0xffff0000,v118
v_pk_fma_f32 v[170:171],v[140:141],v[228:229],v[230:231]
v_pk_fma_f32 v[140:141],v[170:171],v[168:169],v[140:141]
v_lshlrev_b32 v172,16,v119
v_and_b32 v173,0xffff0000,v119
v_pk_fma_f32 v[174:175],v[142:143],v[228:229],v[230:231]
v_pk_fma_f32 v[142:143],v[174:175],v[172:173],v[142:143]
v_lshlrev_b32 v160,16,v120
v_and_b32 v161,0xffff0000,v120
v_pk_fma_f32 v[162:163],v[144:145],v[228:229],v[230:231]
v_pk_fma_f32 v[144:145],v[162:163],v[160:161],v[144:145]
v_lshlrev_b32 v164,16,v121
v_and_b32 v165,0xffff0000,v121
v_pk_fma_f32 v[166:167],v[146:147],v[228:229],v[230:231]
v_pk_fma_f32 v[146:147],v[166:167],v[164:165],v[146:147]
v_lshlrev_b32 v168,16,v122
v_and_b32 v169,0xffff0000,v122
v_pk_fma_f32 v[170:171],v[148:149],v[228:229],v[230:231]
v_pk_fma_f32 v[148:149],v[170:171],v[168:169],v[148:149]
v_lshlrev_b32 v172,16,v123
v_and_b32 v173,0xffff0000,v123
v_pk_fma_f32 v[174:175],v[150:151],v[228:229],v[230:231]
v_pk_fma_f32 v[150:151],v[174:175],v[172:173],v[150:151]
v_lshlrev_b32 v160,16,v124
v_and_b32 v161,0xffff0000,v124
v_pk_fma_f32 v[162:163],v[152:153],v[228:229],v[230:231]
v_pk_fma_f32 v[152:153],v[162:163],v[160:161],v[152:153]
v_lshlrev_b32 v164,16,v125
v_and_b32 v165,0xffff0000,v125
v_pk_fma_f32 v[166:167],v[154:155],v[228:229],v[230:231]
v_pk_fma_f32 v[154:155],v[166:167],v[164:165],v[154:155]
v_lshlrev_b32 v168,16,v126
v_and_b32 v169,0xffff0000,v126
v_pk_fma_f32 v[170:171],v[156:157],v[228:229],v[230:231]
v_pk_fma_f32 v[156:157],v[170:171],v[168:169],v[156:157]
v_lshlrev_b32 v172,16,v127
v_and_b32 v173,0xffff0000,v127
v_pk_fma_f32 v[174:175],v[158:159],v[228:229],v[230:231]
v_pk_fma_f32 v[158:159],v[174:175],v[172:173],v[158:159]
v_cvt_pk_bf16_f32 v208,v128,v129
v_cvt_pk_bf16_f32 v209,v130,v131
v_cvt_pk_bf16_f32 v210,v132,v133
v_cvt_pk_bf16_f32 v211,v134,v135
s_nop 1
v_mfma_f32_16x16x32_bf16 v[180:183],v[176:179],v[208:211],0
v_mfma_f32_16x16x32_bf16 v[184:187],v[208:211],v[208:211],0
buffer_store_dwordx4 v[208:211],v1,s[36:39],s56 offen offset:0 sc0 sc1
v_cvt_pk_bf16_f32 v212,v136,v137
v_cvt_pk_bf16_f32 v213,v138,v139
v_cvt_pk_bf16_f32 v214,v140,v141
v_cvt_pk_bf16_f32 v215,v142,v143
s_nop 1
M 180,v[176:179],212
M 184,v[212:215],212
buffer_store_dwordx4 v[212:215],v1,s[36:39],s56 offen offset:2048 sc0 sc1
v_cvt_pk_bf16_f32 v216,v144,v145
v_cvt_pk_bf16_f32 v217,v146,v147
v_cvt_pk_bf16_f32 v218,v148,v149
v_cvt_pk_bf16_f32 v219,v150,v151
s_nop 1
M 180,v[176:179],216
M 184,v[216:219],216
s_add_u32 s56,s56,4096
buffer_store_dwordx4 v[216:219],v1,s[36:39],s56 offen offset:0 sc0 sc1
v_cvt_pk_bf16_f32 v220,v152,v153
v_cvt_pk_bf16_f32 v221,v154,v155
v_cvt_pk_bf16_f32 v222,v156,v157
v_cvt_pk_bf16_f32 v223,v158,v159
s_nop 1
M 180,v[176:179],220
M 184,v[220:223],220
buffer_store_dwordx4 v[220:223],v1,s[36:39],s56 offen offset:2048 sc0 sc1
s_nop 5
v_add_f32_dpp v224,v180,v180 row_ror:8 row_mask:0xf bank_mask:0xf
v_cndmask_b32 v225,v184,v185,s[70:71]
v_cndmask_b32 v226,v186,v187,s[70:71]
ds_write_b32 v2,v224 offset:1024
v_cndmask_b32 v225,v225,v226,s[72:73]
ds_write_b32 v0,v225 offset:1024
L 0
s_barrier
R 160,3,1024
R 164,3,1040
R 168,3,1056
L 0
v_pk_add_f32 v[160:161],v[160:161],v[162:163]
v_pk_add_f32 v[164:165],v[164:165],v[166:167]
v_pk_add_f32 v[168:169],v[168:169],v[170:171]
v_add_f32 v160,v160,v161
v_pk_add_f32 v[164:165],v[164:165],v[168:169]
v_mul_f32 v160,s86,v160
v_add_f32 v161,v164,v165
v_mul_f32 v161,s86,v161
v_fma_f32 v161,-v160,v160,v161
v_max_f32 v161,0,v161
v_add_f32 v162,s87,v161
v_fma_f32 v163,s87,v162,v161
v_rsq_f32 v173,v162
v_sqrt_f32 v171,v163
v_rsq_f32 v162,v163
v_mul_f32_e64 v172,-v160,v173
v_mul_f32 v169,s88,v162
v_mul_f32 v163,v162,v162
v_mul_f32 v170,s89,v163
v_mul_f32 v170,v170,v169
v_cndmask_b32 v168,v160,v172,s[74:75]
v_cndmask_b32 v169,v169,v173,s[74:75]
buffer_store_dwordx4 v[168:171],v4,s[36:39],s57 offen offset:0 sc0 sc1
s_mov_b32 s60,s58
s_mov_b32 s65,1
s_waitcnt vmcnt(5)
v_cmp_eq_u32 vcc,s53,v8
s_nop 0
s_cmp_eq_u64 vcc,exec
s_cselect_b32 s78,s45,256
s_cmp_lt_u32 s78,256
s_cbranch_scc1 Lr_tc1_%=
s_waitcnt vmcnt(0)
s_cmp_eq_u32 s65,0
s_cbranch_scc1 Lr_pb6_%=
buffer_store_dword v7,v5,s[36:39],s60 offen offset:0 sc0 sc1
s_mov_b32 s65,0
Lr_pb6_%=:
s_cmp_ge_u32 s45,256
s_cbranch_scc1 Lr_done_%=
Lr_bp1_%=:
buffer_load_dword v8,v6,s[36:39],s59 offen offset:0 sc1
s_waitcnt vmcnt(0)
v_cmp_eq_u32 vcc,s53,v8
s_nop 0
s_cmp_eq_u64 vcc,exec
s_cselect_b32 s78,1,s66
s_cmp_lg_u32 s78,0
s_cbranch_scc1 Lr_bq1_%=
s_add_u32 s82,s82,1
s_cmp_ge_u32 s82,20000
s_cselect_b32 s66,1,0
s_sleep 4
s_branch Lr_bp1_%=
Lr_bq1_%=:
s_add_u32 s78,s55,71303168
buffer_load_dwordx4 v[32:35],v1,s[36:39],s78 offen offset:0 sc1
buffer_load_dwordx4 v[36:39],v1,s[36:39],s78 offen offset:2048 sc1
s_add_u32 s78,s55,71307264
buffer_load_dwordx4 v[40:43],v1,s[36:39],s78 offen offset:0 sc1
buffer_load_dwordx4 v[44:47],v1,s[36:39],s78 offen offset:2048 sc1
s_add_u32 s78,s55,89128960
buffer_load_dwordx4 v[48:51],v1,s[36:39],s78 offen offset:0 sc1
buffer_load_dwordx4 v[52:55],v1,s[36:39],s78 offen offset:2048 sc1
s_add_u32 s78,s55,89133056
buffer_load_dwordx4 v[56:59],v1,s[36:39],s78 offen offset:0 sc1
buffer_load_dwordx4 v[60:63],v1,s[36:39],s78 offen offset:2048 sc1
s_add_u32 s78,s55,8388608
buffer_load_dwordx4 v[64:67],v1,s[36:39],s78 offen offset:0 sc1
buffer_load_dwordx4 v[68:71],v1,s[36:39],s78 offen offset:2048 sc1
s_add_u32 s78,s55,8392704
buffer_load_dwordx4 v[72:75],v1,s[36:39],s78 offen offset:0 sc1
buffer_load_dwordx4 v[76:79],v1,s[36:39],s78 offen offset:2048 sc1
buffer_load_dword v8,v6,s[36:39],s64 offen offset:0 sc1
s_waitcnt vmcnt(0)
s_branch Lr_it0_%=
Lr_ta1_%=:
v_dot2_f32_bf16 v128,v80,s67,0
v_dot2_f32_bf16 v129,v80,s68,0
v_dot2_f32_bf16 v130,v81,s67,0
v_dot2_f32_bf16 v131,v81,s68,0
v_dot2_f32_bf16 v132,v82,s67,0
v_dot2_f32_bf16 v133,v82,s68,0
s_add_u32 s78,s55,71303168
buffer_load_dwordx4 v[32:35],v1,s[36:39],s78 offen offset:0 sc1
v_dot2_f32_bf16 v134,v83,s67,0
v_dot2_f32_bf16 v135,v83,s68,0
v_dot2_f32_bf16 v128,v96,s67,v128
v_dot2_f32_bf16 v129,v96,s68,v129
v_dot2_f32_bf16 v130,v97,s67,v130
v_dot2_f32_bf16 v131,v97,s68,v131
buffer_load_dwordx4 v[36:39],v1,s[36:39],s78 offen offset:2048 sc1
v_dot2_f32_bf16 v132,v98,s67,v132
v_dot2_f32_bf16 v133,v98,s68,v133
v_dot2_f32_bf16 v134,v99,s67,v134
v_dot2_f32_bf16 v135,v99,s68,v135
v_dot2_f32_bf16 v136,v84,s67,0
v_dot2_f32_bf16 v137,v84,s68,0
s_add_u32 s78,s55,71307264
buffer_load_dwordx4 v[40:43],v1,s[36:39],s78 offen offset:0 sc1
v_dot2_f32_bf16 v138,v85,s67,0
v_dot2_f32_bf16 v139,v85,s68,0
v_dot2_f32_bf16 v140,v86,s67,0
v_dot2_f32_bf16 v141,v86,s68,0
v_dot2_f32_bf16 v142,v87,s67,0
v_dot2_f32_bf16 v143,v87,s68,0
buffer_load_dwordx4 v[44:47],v1,s[36:39],s78 offen offset:2048 sc1
v_dot2_f32_bf16 v136,v100,s67,v136
v_dot2_f32_bf16 v137,v100,s68,v137
v_dot2_f32_bf16 v138,v101,s67,v138
v_dot2_f32_bf16 v139,v101,s68,v139
v_dot2_f32_bf16 v140,v102,s67,v140
v_dot2_f32_bf16 v141,v102,s68,v141
s_add_u32 s78,s55,89128960
buffer_load_dwordx4 v[48:51],v1,s[36:39],s78 offen offset:0 sc1
v_dot2_f32_bf16 v142,v103,s67,v142
v_dot2_f32_bf16 v143,v103,s68,v143
v_cvt_pk_bf16_f32 v208,v128,v129
v_cvt_pk_bf16_f32 v209,v130,v131
v_cvt_pk_bf16_f32 v210,v132,v133
v_cvt_pk_bf16_f32 v211,v134,v135
buffer_load_dwordx4 v[52:55],v1,s[36:39],s78 offen offset:2048 sc1
s_nop 0
v_mfma_f32_16x16x32_bf16 v[180:183],v[176:179],v[208:211],0
v_mfma_f32_16x16x32_bf16 v[184:187],v[208:211],v[208:211],0
v_dot2_f32_bf16 v144,v88,s67,0
v_dot2_f32_bf16 v145,v88,s68,0
v_dot2_f32_bf16 v146,v89,s67,0
v_dot2_f32_bf16 v147,v89,s68,0
s_add_u32 s78,s55,89133056
buffer_load_dwordx4 v[56:59],v1,s[36:39],s78 offen offset:0 sc1
v_dot2_f32_bf16 v148,v90,s67,0
v_dot2_f32_bf16 v149,v90,s68,0
v_dot2_f32_bf16 v150,v91,s67,0
v_dot2_f32_bf16 v151,v91,s68,0
v_dot2_f32_bf16 v144,v104,s67,v144
v_dot2_f32_bf16 v145,v104,s68,v145
buffer_load_dwordx4 v[60:63],v1,s[36:39],s78 offen offset:2048 sc1
v_dot2_f32_bf16 v146,v105,s67,v146
v_dot2_f32_bf16 v147,v105,s68,v147
v_dot2_f32_bf16 v148,v106,s67,v148
v_dot2_f32_bf16 v149,v106,s68,v149
v_dot2_f32_bf16 v150,v107,s67,v150
v_dot2_f32_bf16 v151,v107,s68,v151
s_add_u32 s78,s55,8388608
buffer_load_dwordx4 v[64:67],v1,s[36:39],s78 offen offset:0 sc1
v_cvt_pk_bf16_f32 v212,v136,v137
v_cvt_pk_bf16_f32 v213,v138,v139
v_cvt_pk_bf16_f32 v214,v140,v141
v_cvt_pk_bf16_f32 v215,v142,v143
s_nop 1
M 180,v[176:179],212
M 184,v[212:215],212
buffer_load_dwordx4 v[68:71],v1,s[36:39],s78 offen offset:2048 sc1
v_dot2_f32_bf16 v152,v92,s67,0
v_dot2_f32_bf16 v153,v92,s68,0
v_dot2_f32_bf16 v154,v93,s67,0
v_dot2_f32_bf16 v155,v93,s68,0
v_dot2_f32_bf16 v156,v94,s67,0
v_dot2_f32_bf16 v157,v94,s68,0
s_add_u32 s78,s55,8392704
buffer_load_dwordx4 v[72:75],v1,s[36:39],s78 offen offset:0 sc1
v_dot2_f32_bf16 v158,v95,s67,0
v_dot2_f32_bf16 v159,v95,s68,0
v_dot2_f32_bf16 v152,v108,s67,v152
v_dot2_f32_bf16 v153,v108,s68,v153
v_dot2_f32_bf16 v154,v109,s67,v154
v_dot2_f32_bf16 v155,v109,s68,v155
buffer_load_dwordx4 v[76:79],v1,s[36:39],s78 offen offset:2048 sc1
v_dot2_f32_bf16 v156,v110,s67,v156
v_dot2_f32_bf16 v157,v110,s68,v157
v_dot2_f32_bf16 v158,v111,s67,v158
v_dot2_f32_bf16 v159,v111,s68,v159
v_cvt_pk_bf16_f32 v216,v144,v145
v_cvt_pk_bf16_f32 v217,v146,v147
v_cvt_pk_bf16_f32 v218,v148,v149
v_cvt_pk_bf16_f32 v219,v150,v151
s_nop 1
M 180,v[176:179],216
M 184,v[216:219],216
v_cvt_pk_bf16_f32 v220,v152,v153
v_cvt_pk_bf16_f32 v221,v154,v155
v_cvt_pk_bf16_f32 v222,v156,v157
v_cvt_pk_bf16_f32 v223,v158,v159
s_nop 1
M 180,v[176:179],220
M 184,v[220:223],220
s_nop 6
v_add_f32_dpp v224,v180,v180 row_ror:8 row_mask:0xf bank_mask:0xf
v_cndmask_b32 v225,v184,v185,s[70:71]
v_cndmask_b32 v226,v186,v187,s[70:71]
ds_write_b32 v2,v224 offset:0
v_cndmask_b32 v225,v225,v226,s[72:73]
ds_write_b32 v0,v225 offset:0
L 0
s_barrier
R 160,3,0
R 164,3,16
R 168,3,32
L 0
v_pk_add_f32 v[160:161],v[160:161],v[162:163]
v_pk_add_f32 v[164:165],v[164:165],v[166:167]
v_pk_add_f32 v[168:169],v[168:169],v[170:171]
v_add_f32 v160,v160,v161
v_pk_add_f32 v[164:165],v[164:165],v[168:169]
v_mul_f32 v160,s86,v160
v_add_f32 v161,v164,v165
v_mul_f32 v161,s86,v161
v_fma_f32 v161,-v160,v160,v161
v_max_f32 v161,0,v161
s_waitcnt vmcnt(12)
s_cmp_eq_u32 s65,0
s_cbranch_scc1 Lr_pb7_%=
buffer_store_dword v7,v5,s[36:39],s60 offen offset:0 sc0 sc1
s_mov_b32 s65,0
Lr_pb7_%=:
v_add_f32 v161,s87,v161
v_rsq_f32 v228,v161
s_nop 0
v_mul_f32_e64 v230,-v160,v228
v_mov_b32 v229,v228
v_mov_b32 v231,v230
v_lshlrev_b32 v160,16,v112
v_and_b32 v161,0xffff0000,v112
v_pk_fma_f32 v[162:163],v[128:129],v[228:229],v[230:231]
v_pk_fma_f32 v[128:129],v[162:163],v[160:161],v[128:129]
v_lshlrev_b32 v164,16,v113
v_and_b32 v165,0xffff0000,v113
v_pk_fma_f32 v[166:167],v[130:131],v[228:229],v[230:231]
v_pk_fma_f32 v[130:131],v[166:167],v[164:165],v[130:131]
v_lshlrev_b32 v168,16,v114
v_and_b32 v169,0xffff0000,v114
v_pk_fma_f32 v[170:171],v[132:133],v[228:229],v[230:231]
v_pk_fma_f32 v[132:133],v[170:171],v[168:169],v[132:133]
v_lshlrev_b32 v172,16,v115
v_and_b32 v173,0xffff0000,v115
v_pk_fma_f32 v[174:175],v[134:135],v[228:229],v[230:231]
v_pk_fma_f32 v[134:135],v[174:175],v[172:173],v[134:135]
v_lshlrev_b32 v160,16,v116
v_and_b32 v161,0xffff0000,v116
v_pk_fma_f32 v[162:163],v[136:137],v[228:229],v[230:231]
v_pk_fma_f32 v[136:137],v[162:163],v[160:161],v[136:137]
v_lshlrev_b32 v164,16,v117
v_and_b32 v165,0xffff0000,v117
v_pk_fma_f32 v[166:167],v[138:139],v[228:229],v[230:231]
v_pk_fma_f32 v[138:139],v[166:167],v[164:165],v[138:139]
v_lshlrev_b32 v168,16,v118
v_and_b32 v169,0xffff0000,v118
v_pk_fma_f32 v[170:171],v[140:141],v[228:229],v[230:231]
v_pk_fma_f32 v[140:141],v[170:171],v[168:169],v[140:141]
v_lshlrev_b32 v172,16,v119
v_and_b32 v173,0xffff0000,v119
v_pk_fma_f32 v[174:175],v[142:143],v[228:229],v[230:231]
v_pk_fma_f32 v[142:143],v[174:175],v[172:173],v[142:143]
v_lshlrev_b32 v160,16,v120
v_and_b32 v161,0xffff0000,v120
v_pk_fma_f32 v[162:163],v[144:145],v[228:229],v[230:231]
v_pk_fma_f32 v[144:145],v[162:163],v[160:161],v[144:145]
v_lshlrev_b32 v164,16,v121
v_and_b32 v165,0xffff0000,v121
v_pk_fma_f32 v[166:167],v[146:147],v[228:229],v[230:231]
v_pk_fma_f32 v[146:147],v[166:167],v[164:165],v[146:147]
v_lshlrev_b32 v168,16,v122
v_and_b32 v169,0xffff0000,v122
v_pk_fma_f32 v[170:171],v[148:149],v[228:229],v[230:231]
v_pk_fma_f32 v[148:149],v[170:171],v[168:169],v[148:149]
v_lshlrev_b32 v172,16,v123
v_and_b32 v173,0xffff0000,v123
v_pk_fma_f32 v[174:175],v[150:151],v[228:229],v[230:231]
v_pk_fma_f32 v[150:151],v[174:175],v[172:173],v[150:151]
v_lshlrev_b32 v160,16,v124
v_and_b32 v161,0xffff0000,v124
v_pk_fma_f32 v[162:163],v[152:153],v[228:229],v[230:231]
v_pk_fma_f32 v[152:153],v[162:163],v[160:161],v[152:153]
v_lshlrev_b32 v164,16,v125
v_and_b32 v165,0xffff0000,v125
v_pk_fma_f32 v[166:167],v[154:155],v[228:229],v[230:231]
v_pk_fma_f32 v[154:155],v[166:167],v[164:165],v[154:155]
v_lshlrev_b32 v168,16,v126
v_and_b32 v169,0xffff0000,v126
v_pk_fma_f32 v[170:171],v[156:157],v[228:229],v[230:231]
v_pk_fma_f32 v[156:157],v[170:171],v[168:169],v[156:157]
v_lshlrev_b32 v172,16,v127
v_and_b32 v173,0xffff0000,v127
v_pk_fma_f32 v[174:175],v[158:159],v[228:229],v[230:231]
v_pk_fma_f32 v[158:159],v[174:175],v[172:173],v[158:159]
buffer_load_dword v8,v6,s[36:39],s64 offen offset:0 sc1
v_cvt_pk_bf16_f32 v208,v128,v129
v_cvt_pk_bf16_f32 v209,v130,v131
v_cvt_pk_bf16_f32 v210,v132,v133
v_cvt_pk_bf16_f32 v211,v134,v135
s_nop 1
v_mfma_f32_16x16x32_bf16 v[180:183],v[176:179],v[208:211],0
v_mfma_f32_16x16x32_bf16 v[184:187],v[208:211],v[208:211],0
buffer_store_dwordx4 v[208:211],v1,s[36:39],s56 offen offset:0 sc0 sc1
v_cvt_pk_bf16_f32 v212,v136,v137
v_cvt_pk_bf16_f32 v213,v138,v139
v_cvt_pk_bf16_f32 v214,v140,v141
v_cvt_pk_bf16_f32 v215,v142,v143
s_nop 1
M 180,v[176:179],212
)"
R"(M 184,v[212:215],212
buffer_store_dwordx4 v[212:215],v1,s[36:39],s56 offen offset:2048 sc0 sc1
v_cvt_pk_bf16_f32 v216,v144,v145
v_cvt_pk_bf16_f32 v217,v146,v147
v_cvt_pk_bf16_f32 v218,v148,v149
v_cvt_pk_bf16_f32 v219,v150,v151
s_nop 1
M 180,v[176:179],216
M 184,v[216:219],216
s_add_u32 s56,s56,4096
buffer_store_dwordx4 v[216:219],v1,s[36:39],s56 offen offset:0 sc0 sc1
v_cvt_pk_bf16_f32 v220,v152,v153
v_cvt_pk_bf16_f32 v221,v154,v155
v_cvt_pk_bf16_f32 v222,v156,v157
v_cvt_pk_bf16_f32 v223,v158,v159
s_nop 1
M 180,v[176:179],220
M 184,v[220:223],220
buffer_store_dwordx4 v[220:223],v1,s[36:39],s56 offen offset:2048 sc0 sc1
s_nop 5
v_add_f32_dpp v224,v180,v180 row_ror:8 row_mask:0xf bank_mask:0xf
v_cndmask_b32 v225,v184,v185,s[70:71]
v_cndmask_b32 v226,v186,v187,s[70:71]
ds_write_b32 v2,v224 offset:1024
v_cndmask_b32 v225,v225,v226,s[72:73]
ds_write_b32 v0,v225 offset:1024
L 0
s_barrier
R 160,3,1024
R 164,3,1040
R 168,3,1056
L 0
v_pk_add_f32 v[160:161],v[160:161],v[162:163]
v_pk_add_f32 v[164:165],v[164:165],v[166:167]
v_pk_add_f32 v[168:169],v[168:169],v[170:171]
v_add_f32 v160,v160,v161
v_pk_add_f32 v[164:165],v[164:165],v[168:169]
v_mul_f32 v160,s86,v160
v_add_f32 v161,v164,v165
v_mul_f32 v161,s86,v161
v_fma_f32 v161,-v160,v160,v161
v_max_f32 v161,0,v161
v_add_f32 v162,s87,v161
v_fma_f32 v163,s87,v162,v161
v_rsq_f32 v173,v162
v_sqrt_f32 v171,v163
v_rsq_f32 v162,v163
v_mul_f32_e64 v172,-v160,v173
v_mul_f32 v169,s88,v162
v_mul_f32 v163,v162,v162
v_mul_f32 v170,s89,v163
v_mul_f32 v170,v170,v169
v_cndmask_b32 v168,v160,v172,s[74:75]
v_cndmask_b32 v169,v169,v173,s[74:75]
buffer_store_dwordx4 v[168:171],v4,s[36:39],s57 offen offset:0 sc0 sc1
s_mov_b32 s60,s58
s_mov_b32 s65,1
s_waitcnt vmcnt(5)
s_branch Lr_it0_%=
Lr_tb1_%=:
v_add_f32 v161,s87,v161
v_rsq_f32 v228,v161
s_nop 0
v_mul_f32_e64 v230,-v160,v228
v_mov_b32 v229,v228
v_mov_b32 v231,v230
s_add_u32 s78,s55,71303168
buffer_load_dwordx4 v[32:35],v1,s[36:39],s78 offen offset:0 sc1
v_lshlrev_b32 v160,16,v112
v_and_b32 v161,0xffff0000,v112
v_pk_fma_f32 v[162:163],v[128:129],v[228:229],v[230:231]
v_pk_fma_f32 v[128:129],v[162:163],v[160:161],v[128:129]
v_lshlrev_b32 v164,16,v113
buffer_load_dwordx4 v[36:39],v1,s[36:39],s78 offen offset:2048 sc1
v_and_b32 v165,0xffff0000,v113
v_pk_fma_f32 v[166:167],v[130:131],v[228:229],v[230:231]
v_pk_fma_f32 v[130:131],v[166:167],v[164:165],v[130:131]
v_lshlrev_b32 v168,16,v114
v_and_b32 v169,0xffff0000,v114
s_add_u32 s78,s55,71307264
buffer_load_dwordx4 v[40:43],v1,s[36:39],s78 offen offset:0 sc1
v_pk_fma_f32 v[170:171],v[132:133],v[228:229],v[230:231]
v_pk_fma_f32 v[132:133],v[170:171],v[168:169],v[132:133]
v_lshlrev_b32 v172,16,v115
v_and_b32 v173,0xffff0000,v115
v_pk_fma_f32 v[174:175],v[134:135],v[228:229],v[230:231]
buffer_load_dwordx4 v[44:47],v1,s[36:39],s78 offen offset:2048 sc1
v_pk_fma_f32 v[134:135],v[174:175],v[172:173],v[134:135]
v_lshlrev_b32 v160,16,v116
v_and_b32 v161,0xffff0000,v116
v_pk_fma_f32 v[162:163],v[136:137],v[228:229],v[230:231]
v_pk_fma_f32 v[136:137],v[162:163],v[160:161],v[136:137]
s_add_u32 s78,s55,89128960
buffer_load_dwordx4 v[48:51],v1,s[36:39],s78 offen offset:0 sc1
v_lshlrev_b32 v164,16,v117
v_and_b32 v165,0xffff0000,v117
v_pk_fma_f32 v[166:167],v[138:139],v[228:229],v[230:231]
v_pk_fma_f32 v[138:139],v[166:167],v[164:165],v[138:139]
v_lshlrev_b32 v168,16,v118
buffer_load_dwordx4 v[52:55],v1,s[36:39],s78 offen offset:2048 sc1
v_and_b32 v169,0xffff0000,v118
v_pk_fma_f32 v[170:171],v[140:141],v[228:229],v[230:231]
v_pk_fma_f32 v[140:141],v[170:171],v[168:169],v[140:141]
v_lshlrev_b32 v172,16,v119
v_and_b32 v173,0xffff0000,v119
s_add_u32 s78,s55,89133056
buffer_load_dwordx4 v[56:59],v1,s[36:39],s78 offen offset:0 sc1
v_pk_fma_f32 v[174:175],v[142:143],v[228:229],v[230:231]
v_pk_fma_f32 v[142:143],v[174:175],v[172:173],v[142:143]
v_lshlrev_b32 v160,16,v120
v_and_b32 v161,0xffff0000,v120
v_pk_fma_f32 v[162:163],v[144:145],v[228:229],v[230:231]
buffer_load_dwordx4 v[60:63],v1,s[36:39],s78 offen offset:2048 sc1
v_pk_fma_f32 v[144:145],v[162:163],v[160:161],v[144:145]
v_lshlrev_b32 v164,16,v121
v_and_b32 v165,0xffff0000,v121
v_pk_fma_f32 v[166:167],v[146:147],v[228:229],v[230:231]
v_pk_fma_f32 v[146:147],v[166:167],v[164:165],v[146:147]
s_add_u32 s78,s55,8388608
buffer_load_dwordx4 v[64:67],v1,s[36:39],s78 offen offset:0 sc1
v_lshlrev_b32 v168,16,v122
v_and_b32 v169,0xffff0000,v122
v_pk_fma_f32 v[170:171],v[148:149],v[228:229],v[230:231]
v_pk_fma_f32 v[148:149],v[170:171],v[168:169],v[148:149]
v_lshlrev_b32 v172,16,v123
buffer_load_dwordx4 v[68:71],v1,s[36:39],s78 offen offset:2048 sc1
v_and_b32 v173,0xffff0000,v123
v_pk_fma_f32 v[174:175],v[150:151],v[228:229],v[230:231]
v_pk_fma_f32 v[150:151],v[174:175],v[172:173],v[150:151]
v_lshlrev_b32 v160,16,v124
v_and_b32 v161,0xffff0000,v124
s_add_u32 s78,s55,8392704
buffer_load_dwordx4 v[72:75],v1,s[36:39],s78 offen offset:0 sc1
v_pk_fma_f32 v[162:163],v[152:153],v[228:229],v[230:231]
v_pk_fma_f32 v[152:153],v[162:163],v[160:161],v[152:153]
v_lshlrev_b32 v164,16,v125
v_and_b32 v165,0xffff0000,v125
v_pk_fma_f32 v[166:167],v[154:155],v[228:229],v[230:231]
buffer_load_dwordx4 v[76:79],v1,s[36:39],s78 offen offset:2048 sc1
v_pk_fma_f32 v[154:155],v[166:167],v[164:165],v[154:155]
v_lshlrev_b32 v168,16,v126
v_and_b32 v169,0xffff0000,v126
v_pk_fma_f32 v[170:171],v[156:157],v[228:229],v[230:231]
v_pk_fma_f32 v[156:157],v[170:171],v[168:169],v[156:157]
v_lshlrev_b32 v172,16,v127
v_and_b32 v173,0xffff0000,v127
v_pk_fma_f32 v[174:175],v[158:159],v[228:229],v[230:231]
v_pk_fma_f32 v[158:159],v[174:175],v[172:173],v[158:159]
buffer_load_dword v8,v6,s[36:39],s64 offen offset:0 sc1
v_cvt_pk_bf16_f32 v208,v128,v129
v_cvt_pk_bf16_f32 v209,v130,v131
v_cvt_pk_bf16_f32 v210,v132,v133
v_cvt_pk_bf16_f32 v211,v134,v135
s_nop 1
v_mfma_f32_16x16x32_bf16 v[180:183],v[176:179],v[208:211],0
v_mfma_f32_16x16x32_bf16 v[184:187],v[208:211],v[208:211],0
buffer_store_dwordx4 v[208:211],v1,s[36:39],s56 offen offset:0 sc0 sc1
v_cvt_pk_bf16_f32 v212,v136,v137
v_cvt_pk_bf16_f32 v213,v138,v139
v_cvt_pk_bf16_f32 v214,v140,v141
v_cvt_pk_bf16_f32 v215,v142,v143
s_nop 1
M 180,v[176:179],212
M 184,v[212:215],212
buffer_store_dwordx4 v[212:215],v1,s[36:39],s56 offen offset:2048 sc0 sc1
v_cvt_pk_bf16_f32 v216,v144,v145
v_cvt_pk_bf16_f32 v217,v146,v147
v_cvt_pk_bf16_f32 v218,v148,v149
v_cvt_pk_bf16_f32 v219,v150,v151
s_nop 1
M 180,v[176:179],216
M 184,v[216:219],216
s_add_u32 s56,s56,4096
buffer_store_dwordx4 v[216:219],v1,s[36:39],s56 offen offset:0 sc0 sc1
v_cvt_pk_bf16_f32 v220,v152,v153
v_cvt_pk_bf16_f32 v221,v154,v155
v_cvt_pk_bf16_f32 v222,v156,v157
v_cvt_pk_bf16_f32 v223,v158,v159
s_nop 1
M 180,v[176:179],220
M 184,v[220:223],220
buffer_store_dwordx4 v[220:223],v1,s[36:39],s56 offen offset:2048 sc0 sc1
s_nop 5
v_add_f32_dpp v224,v180,v180 row_ror:8 row_mask:0xf bank_mask:0xf
v_cndmask_b32 v225,v184,v185,s[70:71]
v_cndmask_b32 v226,v186,v187,s[70:71]
ds_write_b32 v2,v224 offset:1024
v_cndmask_b32 v225,v225,v226,s[72:73]
ds_write_b32 v0,v225 offset:1024
L 0
s_barrier
R 160,3,1024
R 164,3,1040
R 168,3,1056
L 0
v_pk_add_f32 v[160:161],v[160:161],v[162:163]
v_pk_add_f32 v[164:165],v[164:165],v[166:167]
v_pk_add_f32 v[168:169],v[168:169],v[170:171]
v_add_f32 v160,v160,v161
v_pk_add_f32 v[164:165],v[164:165],v[168:169]
v_mul_f32 v160,s86,v160
v_add_f32 v161,v164,v165
v_mul_f32 v161,s86,v161
v_fma_f32 v161,-v160,v160,v161
v_max_f32 v161,0,v161
v_add_f32 v162,s87,v161
v_fma_f32 v163,s87,v162,v161
v_rsq_f32 v173,v162
v_sqrt_f32 v171,v163
v_rsq_f32 v162,v163
v_mul_f32_e64 v172,-v160,v173
v_mul_f32 v169,s88,v162
v_mul_f32 v163,v162,v162
v_mul_f32 v170,s89,v163
v_mul_f32 v170,v170,v169
v_cndmask_b32 v168,v160,v172,s[74:75]
v_cndmask_b32 v169,v169,v173,s[74:75]
buffer_store_dwordx4 v[168:171],v4,s[36:39],s57 offen offset:0 sc0 sc1
s_mov_b32 s60,s58
s_mov_b32 s65,1
s_waitcnt vmcnt(5)
s_branch Lr_it0_%=
Lr_tc1_%=:
s_add_u32 s78,s55,71303168
buffer_load_dwordx4 v[32:35],v1,s[36:39],s78 offen offset:0 sc1
buffer_load_dwordx4 v[36:39],v1,s[36:39],s78 offen offset:2048 sc1
s_add_u32 s78,s55,71307264
buffer_load_dwordx4 v[40:43],v1,s[36:39],s78 offen offset:0 sc1
buffer_load_dwordx4 v[44:47],v1,s[36:39],s78 offen offset:2048 sc1
s_add_u32 s78,s55,89128960
buffer_load_dwordx4 v[48:51],v1,s[36:39],s78 offen offset:0 sc1
buffer_load_dwordx4 v[52:55],v1,s[36:39],s78 offen offset:2048 sc1
s_add_u32 s78,s55,89133056
buffer_load_dwordx4 v[56:59],v1,s[36:39],s78 offen offset:0 sc1
buffer_load_dwordx4 v[60:63],v1,s[36:39],s78 offen offset:2048 sc1
s_add_u32 s78,s55,8388608
buffer_load_dwordx4 v[64:67],v1,s[36:39],s78 offen offset:0 sc1
buffer_load_dwordx4 v[68:71],v1,s[36:39],s78 offen offset:2048 sc1
s_add_u32 s78,s55,8392704
buffer_load_dwordx4 v[72:75],v1,s[36:39],s78 offen offset:0 sc1
buffer_load_dwordx4 v[76:79],v1,s[36:39],s78 offen offset:2048 sc1
buffer_load_dword v8,v6,s[36:39],s64 offen offset:0 sc1
s_waitcnt vmcnt(13)
s_cmp_eq_u32 s65,0
s_cbranch_scc1 Lr_pb8_%=
buffer_store_dword v7,v5,s[36:39],s60 offen offset:0 sc0 sc1
s_mov_b32 s65,0
Lr_pb8_%=:
s_waitcnt vmcnt(0)
s_branch Lr_it0_%=
Lr_done_%=:
s_waitcnt vmcnt(0) lgkmcnt(0)
s_barrier
Lcode_r_e:
.purgem M
.purgem R
.purgem T
.purgem D
.purgem L
)"
  :: "s"(ws), "s"(sG0), "s"(sStep), "s"(sFoutOff), "s"(sExp), "v"(vRP), "v"(vRF), "v"(vRwr), "v"(vRrd), "v"(vRS), "v"(vFlag), "v"(vL4) : "v0", "v1", "v2", "v3", "v4", "v5", "v6", "v7", "v8", "v9", "v10", "v11", "v12", "v13", "v14", "v15", "v16", "v17", "v18", "v19", "v20", "v21", "v22", "v23", "v24", "v25", "v26", "v27", "v28", "v29", "v30", "v31", "v32", "v33", "v34", "v35", "v36", "v37", "v38", "v39", "v40", "v41", "v42", "v43", "v44", "v45", "v46", "v47", "v48", "v49", "v50", "v51", "v52", "v53", "v54", "v55", "v56", "v57", "v58", "v59", "v60", "v61", "v62", "v63", "v64", "v65", "v66", "v67", "v68", "v69", "v70", "v71", "v72", "v73", "v74", "v75", "v76", "v77", "v78", "v79", "v80", "v81", "v82", "v83", "v84", "v85", "v86", "v87", "v88", "v89", "v90", "v91", "v92", "v93", "v94", "v95", "v96", "v97", "v98", "v99", "v100", "v101", "v102", "v103", "v104", "v105", "v106", "v107", "v108", "v109", "v110", "v111", "v112", "v113", "v114", "v115", "v116", "v117", "v118", "v119", "v120", "v121", "v122", "v123", "v124", "v125", "v126", "v127", "v128", "v129", "v130", "v131", "v132", "v133", "v134", "v135", "v136", "v137", "v138", "v139", "v140", "v141", "v142", "v143", "v144", "v145", "v146", "v147", "v148", "v149", "v150", "v151", "v152", "v153", "v154", "v155", "v156", "v157", "v158", "v159", "v160", "v161", "v162", "v163", "v164", "v165", "v166", "v167", "v168", "v169", "v170", "v171", "v172", "v173", "v174", "v175", "v176", "v177", "v178", "v179", "v180", "v181", "v182", "v183", "v184", "v185", "v186", "v187", "v188", "v189", "v190", "v191", "v192", "v193", "v194", "v195", "v196", "v197", "v198", "v199", "v200", "v201", "v202", "v203", "v204", "v205", "v206", "v207", "v208", "v209", "v210", "v211", "v212", "v213", "v214", "v215", "v216", "v217", "v218", "v219", "v220", "v221", "v222", "v223", "v224", "v225", "v226", "v227", "v228", "v229", "v230", "v231", "s36", "s37", "s38", "s39", "s40", "s41", "s42", "s43", "s44", "s45", "s46", "s47", "s48", "s49", "s50", "s51", "s52", "s53", "s54", "s55", "s56", "s57", "s58", "s59", "s60", "s61", "s62", "s63", "s64", "s65", "s66", "s67", "s68", "s69", "s70", "s71", "s72", "s73", "s74", "s75", "s76", "s77", "s78", "s79", "s80", "s81", "s82", "s83", "s84", "s85", "s86", "s87", "s88", "s89", "s90", "s91", "s92", "s93", "s94", "s95", "s96", "s97", "vcc", "scc", "memory");
    }
    LANE_ID(lane2);
    for (int t = kind == 6 ? 64 + slice : (kind - 1) * 32 + rep * 8 + slice; t < 256; t += 82)
      row_last(ws, out, t, lane2, wave, ep);
    CTS(14);
    if (b == 0 && lane2 == 0 && wave == 0) __hip_atomic_store((gu32*)(ws + OFF_EP), ep, __ATOMIC_RELAXED, __HIP_MEMORY_SCOPE_AGENT);
    asm volatile("s_endpgm" ::: "memory");
    __builtin_unreachable();
  }
}

extern "C" void kernel_launch(void* const* d_in, const int* in_sizes, int n_in, void* d_out, int out_size,
                              void* d_ws, size_t ws_size, hipStream_t stream) {
  const u16* q = (const u16*)d_in[0];
  const u16* k = (const u16*)d_in[1];
  const u16* v = (const u16*)d_in[2];
  const u16* shift_k = (const u16*)d_in[3];
  const u16* ssm_k = (const u16*)d_in[4];
  const u16* ssm_D = (const u16*)d_in[5];
  const u16* w1 = (const u16*)d_in[7];
  const u16* w2 = (const u16*)d_in[8];
  const u16* w3 = (const u16*)d_in[9];
  const u16* w4 = (const u16*)d_in[10];
  flow_kernel<<<256, 256, 0, stream>>>(q, k, v, shift_k, ssm_k, ssm_D, w1, w2, w3, w4, (char*)d_ws, (u16*)d_out);
}
```

```cpp
#include <hip/hip_runtime.h>
#include <hip/hip_bf16.h>
#include <stdint.h>
#include <type_traits>

typedef short s16x8 __attribute__((ext_vector_type(8)));
typedef short s16x4 __attribute__((ext_vector_type(4)));
typedef float f32x4 __attribute__((ext_vector_type(4)));
typedef unsigned short u16;
typedef unsigned short u16x8 __attribute__((ext_vector_type(8)));
typedef unsigned short u16x4 __attribute__((ext_vector_type(4)));
typedef float f32x2 __attribute__((ext_vector_type(2)));
typedef __bf16 bf16x2 __attribute__((ext_vector_type(2)));
typedef unsigned u32x4v __attribute__((ext_vector_type(4)));
typedef __attribute__((address_space(1))) unsigned gu32;

constexpr int S_ = 2048, D_ = 1024, F_ = 3584;
#define PF 1

constexpr int WS_USED = 111243392;
constexpr int OFF_KVR = 4194304, OFF_YF = 8388608, OFF_XP = 12582912, OFF_HS = 22020096, OFF_X1 = 31457280, OFF_G = 40894464, OFF_P0 = 71303168, OFF_P1 = 89128960, OFF_Y = 98566144, OFF_S1 = 106954752, OFF_ST = 107479040, OFF_FL = 109051904, OFF_FXP = 109056000, OFF_FH = 109334528, OFF_FX1 = 109613056, OFF_FG = 109891584, OFF_FP = 110587904, OFF_FKV = 111144960, OFF_FY = 111177728, OFF_EP = 111243264, OFF_DBG = 115343360;

#define WS_DIAG 0

__device__ __forceinline__ float bf2f(u16 v) { return __uint_as_float(((unsigned)v) << 16); }
__device__ __forceinline__ u16 f2bf(float f) { return __builtin_bit_cast(u16, (__bf16)f); }
__device__ __forceinline__ float rbf(float f) { return bf2f(f2bf(f)); }
__device__ __forceinline__ void st16(void* p, u16x8 v) {
  asm volatile("global_store_dwordx4 %0, %1, off sc0 sc1\n\ts_nop 1" :: "v"(p), "v"(v) : "memory");
}
#define WAIT_VM(n) asm volatile("s_waitcnt vmcnt(%0)" ::"n"(n) : "memory")
#define WAIT_LGKM0() asm volatile("s_waitcnt lgkmcnt(0)" ::: "memory")

#define CODE_RANGE(B, E, p, n)                                                                                   \
  const char* p; unsigned n;                                                                                     \
  { unsigned lo_, hi_;                                                                                           \
    asm volatile("s_getpc_b64 s[98:99]\nLpc%=:\n\ts_add_u32 %0, s98, (" B "-Lpc%=)&4294967295\n\ts_addc_u32 %1, s99, (" B "-Lpc%=)>>32\n\t" \
                 "s_mov_b32 %2, " E "-" B : "=s"(lo_), "=s"(hi_), "=s"(n) :: "s98", "s99", "scc");               \
    p = (const char*)(((unsigned long long)hi_ << 32) | lo_); }
__device__ __forceinline__ unsigned touch(const char* p, unsigned n, unsigned off) {
  unsigned t;
  asm volatile("global_load_dword %0, %1, off" : "=v"(t) : "v"(p + (off < n ? off : 0u)) : "memory");
  return t;
}

__device__ __forceinline__ void touch_lds(const char* p, unsigned a) {
  asm volatile("s_mov_b32 m0, %1\n\ts_nop 0\n\tglobal_load_lds_dword %0, off" :: "v"(p), "s"(a) : "memory");
}
#define PFPOL ""
__device__ __forceinline__ void touch_pol(const char* p, unsigned a) {
  asm volatile("s_mov_b32 m0, %1\n\ts_nop 0\n\tglobal_load_lds_dword %0, off " PFPOL :: "v"(p), "s"(a) : "memory");
}
#define YSLEEP 4
#define KSLEEP 2
#define CTS(i)
__device__ __forceinline__ f32x2 up2(unsigned p) { return (f32x2){__uint_as_float(p << 16), __uint_as_float(p & 0xffff0000u)}; }
__device__ __forceinline__ unsigned pk2(f32x2 v) { return __builtin_bit_cast(unsigned, __builtin_convertvector(v, bf16x2)); }
__device__ __forceinline__ f32x2 rbf2(f32x2 v) { return up2(pk2(v)); }
template <int N, class F> __device__ __forceinline__ void sfor(F&& f) {
  if constexpr (N > 0) { sfor<N - 1>(f); f(std::integral_constant<int, N - 1>{}); }
}
__device__ __forceinline__ void set_flag(char* p, int i, unsigned ep) {
  if (i < 32) __hip_atomic_store((gu32*)p + i, ep, __ATOMIC_RELAXED, __HIP_MEMORY_SCOPE_AGENT);
}
template <int NL>
__device__ __forceinline__ void poll_flags(const char* p, int n, int lane, unsigned ep) {
  for (int it = 0; it < 20000; ++it) {
    bool ok = true;
#pragma unroll
    for (int i = 0; i < NL; ++i) {
      const int f = lane + 64 * i;
      ok &= __hip_atomic_load((gu32*)(p + (f < n ? f : 0) * 128), __ATOMIC_RELAXED, __HIP_MEMORY_SCOPE_AGENT) == ep;
    }
    if (__all(ok)) break;
    __builtin_amdgcn_s_sleep(YSLEEP);
  }
  asm volatile("" ::: "memory");
}

#define NSA 3
constexpr int CV_HP = (2048 + 32) * 2, CV_HQ = (256 * NSA + 32) * 2;
constexpr int CV_UR = (2048 + 256) * 2;
constexpr int CV_Q = 4 * CV_HP, CV_U = CV_Q + 4 * CV_HQ, CV_W = CV_U + 2 * CV_UR;
constexpr int NCV = 174;
constexpr int CV_STG = 4 * CV_W;
constexpr int YP = D_ * 16;

struct KvRegs { u32x4v k0, k1, k2, k3, k4, k5, k6, v0, v1, v2, v3, s0, s1, s2, s3; };
#define LD16(d, p, pol) asm volatile("global_load_dwordx4 %0, %1, off " pol : "=v"(d) : "v"(p) : "memory")
__device__ __forceinline__ void kv_load(KvRegs& R, const u16* __restrict__ kin, const u16* __restrict__ vin, const u16* __restrict__ shift_k, int b, int tid) {
  const int tr = (b >> 4) * 128 + 4 * (tid >> 3), cc = (b & 15) * 64 + 8 * (tid & 7);
  const u16* kp = kin + (size_t)tr * D_ + cc;
  const u16* vp = vin + (size_t)tr * D_ + cc;
  const u16* sp = shift_k + (size_t)cc * 4;
  LD16(R.k3, kp, "nt"); LD16(R.k4, kp + D_, "nt"); LD16(R.k5, kp + 2 * D_, "nt"); LD16(R.k6, kp + 3 * D_, "nt");
  LD16(R.v0, vp, "nt"); LD16(R.v1, vp + D_, "nt"); LD16(R.v2, vp + 2 * D_, "nt"); LD16(R.v3, vp + 3 * D_, "nt");
  const u16* kq = tr ? kp - 3 * D_ : kp;
  LD16(R.k0, kq, "nt"); LD16(R.k1, kq + D_, "nt"); LD16(R.k2, kq + 2 * D_, "nt");
  LD16(R.s0, sp, ""); LD16(R.s1, sp + 8, ""); LD16(R.s2, sp + 16, ""); LD16(R.s3, sp + 24, "");
}
template <int N>
__device__ __forceinline__ void kv_wait(KvRegs& R) {
  asm volatile("s_waitcnt vmcnt(%15)" : "+v"(R.k0), "+v"(R.k1), "+v"(R.k2), "+v"(R.k3), "+v"(R.k4), "+v"(R.k5), "+v"(R.k6), "+v"(R.v0), "+v"(R.v1), "+v"(R.v2),
               "+v"(R.v3), "+v"(R.s0), "+v"(R.s1), "+v"(R.s2), "+v"(R.s3) : "n"(N) : "memory");
}
__device__ __forceinline__ void kv_store(KvRegs& R, char* __restrict__ ws, char* T, int b, int tid) {
  const int t0 = (b >> 4) * 128, c0 = (b & 15) * 64, ch8 = tid & 7, rg = tid >> 3;
  if (t0 + 4 * rg < 3) R.k0 = R.k1 = R.k2 = (u32x4v){0u, 0u, 0u, 0u};
  typedef unsigned u32x2 __attribute__((ext_vector_type(2)));
  const u32x4v kr[7] = {R.k0, R.k1, R.k2, R.k3, R.k4, R.k5, R.k6}, vr[4] = {R.v0, R.v1, R.v2, R.v3}, sk[4] = {R.s0, R.s1, R.s2, R.s3};
#pragma unroll
  for (int j2 = 0; j2 < 4; ++j2) {
    const f32x2 sa = up2(sk[j2][0]), sb = up2(sk[j2][1]), sc = up2(sk[j2][2]), sd = up2(sk[j2][3]);
    const f32x2 s0 = {sa[0], sc[0]}, s1 = {sa[1], sc[1]}, s2 = {sb[0], sd[0]}, s3 = {sb[1], sd[1]};
    f32x2 kf[7], o[4];
#pragma unroll
    for (int r = 0; r < 7; ++r) kf[r] = up2(kr[r][j2]);
#pragma unroll
    for (int r = 0; r < 4; ++r)
      o[r] = __builtin_elementwise_fma(kf[r + 3], s0, __builtin_elementwise_fma(kf[r + 2], s1, __builtin_elementwise_fma(kf[r + 1], s2, kf[r] * s3))) * up2(vr[r][j2]);
#pragma unroll
    for (int e = 0; e < 2; ++e)
      *(u32x2*)(T + (8 * ch8 + 2 * j2 + e) * 264 + (124 - 4 * rg) * 2) = (u32x2){pk2((f32x2){o[3][e], o[2][e]}), pk2((f32x2){o[1][e], o[0][e]})};
  }
  __syncthreads();
  u16* kvr = (u16*)(ws + OFF_KVR);
#pragma unroll
  for (int i = 0; i < 4; ++i) {
    const int idx = tid + 256 * i, ch = idx >> 4, cb = idx & 15;
    const u16x4 lo = *(const u16x4*)(T + ch * 264 + cb * 16), hi = *(const u16x4*)(T + ch * 264 + cb * 16 + 8);
    st16(kvr + (size_t)(c0 + ch) * S_ + (1920 - t0) + cb * 8, __builtin_shufflevector(lo, hi, 0, 1, 2, 3, 4, 5, 6, 7));
  }
}

typedef unsigned u32x2v __attribute__((ext_vector_type(2)));
__device__ __forceinline__ void h_load(u32x4v (&hv)[8], unsigned& dD, const u16* __restrict__ ssm_k, const u16* __restrict__ ssm_D, int cw, int nw, bool act, int lane) {
#pragma unroll
  for (int i = 0; i < 8; ++i) LD16(hv[i], ssm_k + (size_t)cw * S_ + (act && i < 4 * nw ? i * 512 + lane * 8 : 0), "nt");
  asm volatile("global_load_ushort %0, %1, off" : "=v"(dD) : "v"(ssm_D + cw + min(lane, nw - 1)) : "memory");
}
template <int NL, int PITCH>
__device__ __forceinline__ void h_spread(const u32x4v* hv, char* dst, int lane) {
#define AB(a, b) __builtin_amdgcn_alignbyte(a, b, 2)
  const unsigned f0 = __builtin_amdgcn_readfirstlane(hv[0][0]), f1 = __builtin_amdgcn_readfirstlane(hv[0][1]);
  if (lane < 8) *(u32x4v*)(dst + (lane >> 1) * PITCH + (lane & 1) * 16) = (u32x4v){0u, 0u, lane == 7 ? f0 << 16 : 0u, lane == 3 ? f0 << 16 : lane == 5 ? f0 : lane == 7 ? AB(f1, f0) : 0u};
#pragma unroll
  for (int i = 0; i < (NL + 31) / 32; ++i) {
    const unsigned w0 = hv[i][0], w1 = hv[i][1], w2 = hv[i][2], w3 = hv[i][3];
    const unsigned w4 = __builtin_amdgcn_update_dpp((int)__builtin_amdgcn_readfirstlane(hv[i < 3 ? i + 1 : i][0]), (int)w0, 0x130, 0xf, 0xf, false);
    const unsigned w5 = __builtin_amdgcn_update_dpp((int)__builtin_amdgcn_readfirstlane(hv[i < 3 ? i + 1 : i][1]), (int)w1, 0x130, 0xf, 0xf, false);
    if (64 * i + lane < 2 * NL) {
      char* base = dst + 32 + i * 1024 + lane * 16;
      *(u32x4v*)(base) = hv[i];
      *(u32x4v*)(base + PITCH) = (u32x4v){AB(w1, w0), AB(w2, w1), AB(w3, w2), AB(w4, w3)};
      *(u32x4v*)(base + 2 * PITCH) = (u32x4v){w1, w2, w3, w4};
      *(u32x4v*)(base + 3 * PITCH) = (u32x4v){AB(w2, w1), AB(w3, w2), AB(w4, w3), AB(w5, w4)};
    }
  }
#undef AB
}

#define PT2 168
#define PT3 168
#define PTE 6
struct Conv {
  char* ws; char* lds; int c0, nw, iw, tid, lane, wave; unsigned ep, dD;
  int l15, lg; unsigned ao, aq, bo, own;
  const u16* w3; const u16* w4;
  template <int B, int N>
  __device__ __forceinline__ void pf(int k) {
    const unsigned g = (B + k * N) * NCV + iw * N + wave * (N / 4) + min(lane, N / 4 - 1), gg = g < 114688 ? g : 0;
    touch_lds((const char*)(gg < 57344 ? w3 : w4) + (size_t)(gg < 57344 ? gg : gg - 57344) * 128, (unsigned)(size_t)lds + 163584);
  }
  __device__ __forceinline__ void u_fetch(int qu) {
    const u16* kvr = (const u16*)(ws + OFF_KVR);
#pragma unroll
    for (int j = 0; j < 2; ++j)
      __builtin_amdgcn_global_load_lds((const unsigned*)(kvr + (size_t)(c0 + nw * wave + min(j, nw - 1)) * S_ + qu * 512 + lane * 8),
                                       (unsigned*)(lds + wave * CV_W + CV_U + j * CV_UR + qu * 1024), 16, 0, 16);
  }
  __device__ __forceinline__ void rd1(unsigned pa, unsigned pb, int i, s16x4 (&ah)[8][2], s16x8 (&bfr)[8]) {
    const int m = i / 3, e = i % 3;
    if (e < 2) asm volatile("ds_read_b64 %0, %1 offset:%2" : "=v"(ah[m][e & 1]) : "v"(pa), "n"(512 * m + 8 * e));
    else asm volatile("ds_read_b128 %0, %1 offset:%2" : "=v"(bfr[m]) : "v"(pb), "n"(512 * (7 - m)));
  }
  template <int S0, int NS>
  __device__ __forceinline__ void put(int j, f32x4 (&acc)[6], char* dst) {
    u32x2v ku[NS];
    const unsigned uo = own + CV_U + j * CV_UR + 2 * (2044 - 16 * l15 - 4 * lg) - 512 * 7;
#pragma unroll
    for (int s = 0; s < NS; ++s) asm volatile("ds_read_b64 %0, %1 offset:%2" : "=v"(ku[s]) : "v"(uo), "n"(512 * (7 - S0 - s)));
    WAIT_LGKM0();
    __builtin_amdgcn_sched_barrier(0);
    const float d = bf2f((u16)__builtin_amdgcn_readlane(dD, j));
#pragma unroll
    for (int s = 0; s < NS; ++s) {
      const f32x2 k0 = up2(ku[s][0]), k1 = up2(ku[s][1]);
      *(u32x2v*)(dst + s * 512 + 2 * (16 * l15 + 4 * lg)) =
          (u32x2v){pk2((f32x2){fmaf(k1[1], d, acc[s][0]), fmaf(k1[0], d, acc[s][1])}), pk2((f32x2){fmaf(k0[1], d, acc[s][2]), fmaf(k0[0], d, acc[s][3])})};
    }
  }
  template <int S0, int NS>
  __device__ __forceinline__ void out_segs(const char* src, int wp, int jp) {
    const int ch = tid & (4 * nw - 1), tb = nw == 2 ? tid >> 3 : tid >> 2;
    const char* p = src + (nw == 2 ? (ch >> 1) * wp + (ch & 1) * jp : ch * wp) + tb * 16;
    char* yo = ws + OFF_Y + (size_t)(32 * S0 + tb) * YP + (c0 + ch) * 16;
#pragma unroll
    for (int s = 0; s < NS; ++s)
      if (nw == 2) st16(yo + (size_t)s * 32 * YP, *(const u16x8*)(p + s * 512));
      else if (!(s & 1) && (s + 1 < NS || tb < 32)) st16(yo + (size_t)s * 32 * YP, *(const u16x8*)(p + s * 512));
  }
  __device__ __forceinline__ void init(char* ws_, char* lds_, int c0_, int nw_, int iw_, int tid_, unsigned ep_, unsigned dD_, const u16* w3_, const u16* w4_) {
    dD = dD_; w3 = w3_; w4 = w4_;
    ws = ws_; lds = lds_; c0 = c0_; nw = nw_; iw = iw_; tid = tid_; lane = tid & 63; wave = __builtin_amdgcn_readfirstlane(tid >> 6); ep = ep_;
    l15 = lane & 15; lg = lane >> 4;
    const int wsh = (l15 + 1) & 3;
    own = (unsigned)(size_t)lds + wave * CV_W;
    ao = own + wsh * CV_HP + 2 * (8 * lg + (l15 + 1 - wsh));
    aq = own + CV_Q + wsh * CV_HQ + 2 * (8 * lg + (l15 + 1 - wsh));
    bo = own + CV_U + 2 * (2032 - 16 * l15 + 8 * lg) - 2 * 256 * 7;
  }
  __device__ __forceinline__ void first() {
    constexpr int TA = NSA == 2 ? 4 : 8;
    gu32* fa = (gu32*)(ws + OFF_FKV + ((lane & (TA - 1)) * 16 + (c0 >> 6)) * 128);
    gu32* fb = (gu32*)(ws + OFF_FKV + ((TA + lane % (16 - TA)) * 16 + (c0 >> 6)) * 128);
    for (int it = 0; it < 20000; ++it) {
      if (__all(__hip_atomic_load(fa, __ATOMIC_RELAXED, __HIP_MEMORY_SCOPE_AGENT) == ep)) break;
      __builtin_amdgcn_s_sleep(KSLEEP);
    }
    asm volatile("" ::: "memory");
    CTS(3);
    u_fetch(3);
    if (TA == 8) u_fetch(2);
    unsigned fl;
    asm volatile("global_load_dword %0, %1, off sc1\n\ts_waitcnt vmcnt(0)" : "=v"(fl) : "v"(fb) : "memory");
    const bool have = __all(fl == ep);
    if (have) { if (TA == 4) u_fetch(2); u_fetch(1); u_fetch(0); }
    CTS(4);
    f32x4 acc[6];
    s16x4 ax[3][NSA][2];
    s16x8 bx[3][NSA];
#pragma unroll 1
    for (int j = 0; j < nw; ++j) {
      const unsigned pa = j ? aq : ao, pb = bo + j * CV_UR;
      auto rdA = [&](int q) {
#pragma unroll
        for (int m = 0; m < NSA; ++m) {
          asm volatile("ds_read_b64 %0, %1 offset:%2" : "=v"(ax[q % 3][m][0]) : "v"(pa), "n"(512 * m + 64 * q));
          asm volatile("ds_read_b64 %0, %1 offset:%2" : "=v"(ax[q % 3][m][1]) : "v"(pa), "n"(512 * m + 64 * q + 8));
          asm volatile("ds_read_b128 %0, %1 offset:%2" : "=v"(bx[q % 3][m]) : "v"(pb), "n"(512 * (7 - m) + 64 * q));
        }
      };
      rdA(0);
      rdA(1);
#pragma unroll
      for (int s = 0; s < NSA; ++s) acc[s] = (f32x4){0.f, 0.f, 0.f, 0.f};
      sfor<8>([&](auto q_c) {
        constexpr int q = decltype(q_c)::value, p = q % 3;
        asm volatile("s_waitcnt lgkmcnt(%0)" :: "n"(q + 1 < 8 ? 3 * NSA : 0) : "memory");
        __builtin_amdgcn_sched_barrier(0);
        if (q + 2 < 8) rdA(q + 2);
#pragma unroll
        for (int s = 0; s < NSA; ++s)
#pragma unroll
          for (int m = 0; m <= s; ++m)
            acc[s] = __builtin_amdgcn_mfma_f32_16x16x32_bf16(__builtin_shufflevector(ax[p][m][0], ax[p][m][1], 0, 1, 2, 3, 4, 5, 6, 7), bx[p][s - m], acc[s], 0, 0, 0);
        __builtin_amdgcn_sched_barrier(0);
      });
      put<0, NSA>(j, acc, lds + CV_STG + (2 * wave + j) * 2048);
    }
    WAIT_LGKM0();
    __builtin_amdgcn_s_barrier();
    asm volatile("" ::: "memory");
    out_segs<0, NSA>(lds + CV_STG, 4096, 2048);
    CTS(5);
    WAIT_VM(0);
    __builtin_amdgcn_s_barrier();
    set_flag(ws + OFF_FY + iw * 128, tid, ep);
    CTS(6);
    if (!have) {
      for (int it = 0; it < 20000; ++it) {
        if (__all(__hip_atomic_load(fb, __ATOMIC_RELAXED, __HIP_MEMORY_SCOPE_AGENT) == ep)) break;
        __builtin_amdgcn_s_sleep(KSLEEP);
      }
      asm volatile("" ::: "memory");
      if (TA == 4) u_fetch(2);
      u_fetch(1); u_fetch(0);
      WAIT_VM(0);
    }
    if (PT2) pf<0, PT2 ? PT2 : 4>(0);
  }
  __device__ __forceinline__ void second(const u32x4v* hv) {
    s16x4 a0[8][2], a1[8][2];
    s16x8 b0[8], b1[8];
    f32x4 acc[6];
    auto round = [&](s16x4 (&ah)[8][2], s16x8 (&bfr)[8], s16x4 (&ahn)[8][2], s16x8 (&bfn)[8], unsigned pa, unsigned pb) {
      int k = 0;
#pragma unroll
      for (int s = NSA; s <= 7; ++s)
#pragma unroll
        for (int m = 0; m <= s; ++m) {
          const s16x8 af = __builtin_shufflevector(ah[m][0], ah[m][1], 0, 1, 2, 3, 4, 5, 6, 7);
          acc[s - NSA] = __builtin_amdgcn_mfma_f32_16x16x32_bf16(af, bfr[s - m], acc[s - NSA], 0, 0, 0);
          if (k < 24) rd1(pa, pb, k, ahn, bfn);
          ++k;
          __builtin_amdgcn_sched_barrier(0);
        }
      WAIT_LGKM0();
      __builtin_amdgcn_sched_barrier(0);
    };
    CTS(13);
#pragma unroll 1
    for (int j = 0; j < nw; ++j) {
      const unsigned pb = bo + j * CV_UR;
      if (j) {
        h_spread<128, CV_HP>(hv + 4, lds + wave * CV_W, lane);
        asm volatile("" ::: "memory");
      }
#pragma unroll
      for (int i = 0; i < 24; ++i) rd1(ao, pb, i, a0, b0);
#pragma unroll
      for (int i = 0; i < 6; ++i) acc[i] = (f32x4){0.f, 0.f, 0.f, 0.f};
      WAIT_LGKM0();
      __builtin_amdgcn_sched_barrier(0);
#pragma unroll 1
      for (int r = 0; r < 8; r += 2) {
        round(a0, b0, a1, b1, ao + 64 * r + 64, pb + 64 * r + 64);
        round(a1, b1, a0, b0, ao + 64 * r + 128, pb + 64 * r + 128);
        if (PT3 && r + 8 * j < PTE * nw && (nw == 1 || !(r & 2))) pf<PT2, PT3 ? PT3 : 4>(nw == 1 ? r >> 1 : 2 * j + (r >> 2));
      }
      put<NSA, 8 - NSA>(j, acc, lds + wave * CV_W + (j ? 0 : CV_Q));
    }
    CTS(11);
    WAIT_LGKM0();
    __builtin_amdgcn_s_barrier();
    asm volatile("" ::: "memory");
    out_segs<NSA, 8 - NSA>(lds + CV_Q, CV_W, -CV_Q);
    CTS(7);
  }
};
#define LASTSLEEP 8
__device__ __forceinline__ void wait_flags(const char* p, int lane, int n, unsigned ep) {
  gu32* f = (gu32*)(p + (lane < n ? lane : 0) * 128);
  for (int it = 0; it < 10000; ++it) {
    if (__all(__hip_atomic_load(f, __ATOMIC_RELAXED, __HIP_MEMORY_SCOPE_AGENT) == ep)) break;
    __builtin_amdgcn_s_sleep(LASTSLEEP);
  }
  asm volatile("" ::: "memory");
}
__device__ __forceinline__ void row_last(char* __restrict__ ws, u16* __restrict__ out, int t, int lane, int wave, unsigned ep) {
  const __amdgpu_buffer_rsrc_t rs = __builtin_amdgcn_make_buffer_rsrc(ws, 0, WS_USED, 0x00020000);
  const int r8 = lane & 7, seg = lane >> 3, un = 128 + (t >> 1);
  const unsigned fo = un * 32768 + (wave * 8 + (seg >> 2)) * 1024 + ((seg & 3) * 16 + (t & 1) * 8 + r8) * 16;
  wait_flags(ws + OFF_FP + un * 2048, lane, 16, ep);
  u32x4v p0[4], p1[4];
#pragma unroll
  for (int i = 0; i < 4; ++i) {
    p0[i] = __builtin_bit_cast(u32x4v, __builtin_amdgcn_raw_buffer_load_b128(rs, fo + i * 2048, OFF_P0, 16));
    p1[i] = __builtin_bit_cast(u32x4v, __builtin_amdgcn_raw_buffer_load_b128(rs, fo + i * 2048, OFF_P1, 16));
  }
#pragma unroll
  for (int i = 0; i < 4; ++i) {
    u32x4v o;
#pragma unroll
    for (int e = 0; e < 4; ++e) o[e] = pk2(up2(p0[i][e]) + up2(p1[i][e]));
    st16(out + (size_t)(t * 8 + r8) * D_ + wave * 256 + i * 64 + seg * 8, __builtin_bit_cast(u16x8, o));
  }
}

__device__ __forceinline__ int swzB(int r) { return ((((r >> 1) & 1) | (((r >> 3) & 1) << 1)) << 1); }
#define TCH 1
constexpr int NRW = 18;
__global__ __launch_bounds__(256, 1) void flow_kernel(const u16* __restrict__ q, const u16* __restrict__ kin, const u16* __restrict__ vin,
                                                      const u16* __restrict__ shift_k, const u16* __restrict__ ssm_k, const u16* __restrict__ ssm_D,
                                                      const u16* __restrict__ w1, const u16* __restrict__ w2,
                                                      const u16* __restrict__ w3, const u16* __restrict__ w4, char* __restrict__ ws,
                                                      u16* __restrict__ out) {
  __shared__ __attribute__((aligned(16))) char lds[163840];
  const int tid = threadIdx.x, lane = tid & 63, wave = __builtin_amdgcn_readfirstlane(tid >> 6);
  const int b = blockIdx.x, xcd = b & 7, idx = b >> 3;
  const unsigned sLds = (unsigned)(size_t)lds;
  const int o8 = idx & 7;
  int kind, rep, slice, kh = 0;
  int j3 = 0, j4 = 0, shk = o8, shn = 8;
  if (idx < 8) { kind = 1 + (xcd & 1); rep = xcd >> 1; slice = o8; }
  else if (idx < 16 || (xcd >= 6 && idx < 24)) {
    const int h = idx < 16 ? xcd : 2 + xcd;
    kind = 4; rep = h >> 1; kh = h & 1; slice = o8; j4 = h * 8 + o8;
  } else if (idx < 30) {
    const int i = idx - 16;
    j3 = xcd < 6 ? xcd * 14 + i : 40 + xcd * 6 + i;
    kind = j3 < 90 ? 3 : j3 < 94 ? 5 : 6;
    if (xcd == 7) { slice = j3 < 94 ? 18 : j3 - 96 + NRW; rep = j3 < 94 ? j3 - 90 : 0; shk = 0; shn = 1; }
    else if (xcd == 6) { rep = 3 + (i >= 11); shk = i - 8 - 3 * (i >= 11); slice = (i >= 11 ? 5 : 15) + shk; shn = 3; }
    else if (i < 9) { rep = xcd >> 1; slice = (xcd & 1) * 9 + i; shk = i; shn = 9; }
    else { rep = 3 + (xcd >= 3); shk = i - 9; slice = ((0xd085a0 >> (4 * xcd)) & 15) + shk; shn = 5; }
  } else { kind = 6; rep = 0; slice = xcd * 2 + idx - 30; }
  CTS(0);
  {
    CODE_RANGE("Lpc%=", "Lcode_r_b", c0, n0) CODE_RANGE("(Lcode_end-3968)", "Lcode_end", c7, n7)
    touch_lds(tid < 224 ? c0 + (tid * 128 < n0 ? tid * 128 : 0) : c7 + ((tid - 224) * 128 < n7 ? (tid - 224) * 128 : 0), sLds + 163584);
  }
  const unsigned sScr = sLds + 163584;
  auto code_touch = [&]() {
    CODE_RANGE("Lcode_g12_b", "Lcode_g2_b", c1, n1) CODE_RANGE("Lcode_g2_b", "Lcode_g12_e", c2, n2) CODE_RANGE("Lcode_g3_b", "Lcode_g3_e", c3, n3)
    CODE_RANGE("Lcode_g3o_b", "Lcode_g3o_e", c5, n5) CODE_RANGE("Lcode_g4_b", "Lcode_g4_e", c4, n4) CODE_RANGE("Lcode_r_b", "Lcode_r_e", c6, n6)
    const char* cp = kind == 1 ? c1 : kind == 2 ? c2 : kind == 3 ? c3 : kind == 4 ? c4 : kind == 5 ? c5 : c6;
    const unsigned cn = kind == 1 ? n1 : kind == 2 ? n2 : kind == 3 ? n3 : kind == 4 ? n4 : kind == 5 ? n5 : n6;
    touch_lds(cp + (tid * 128 < cn ? tid * 128 : 0), sScr);
    touch_lds(kind == 4 && tid < 64 ? c6 + tid * 128 : cp + (32768 + tid * 128 < cn ? 32768 + tid * 128 : 0), sScr);
  };
  unsigned ep = 0;
  if (kind == 1) code_touch();
  else {
  const int tile = idx >= 8 ? (idx - 8) * 8 + xcd : 192 + (xcd >> 1) * 8 + idx;
#define EP_LOAD unsigned ew; asm volatile("global_load_dword %0, %1, %2 sc1" : "=v"(ew) : "v"(0u), "s"(ws + OFF_EP) : "memory")
#define EP_TAKE asm volatile("s_waitcnt vmcnt(0)" : "+v"(ew) :: "memory"); ep = __builtin_amdgcn_readfirstlane(ew) + 1u; if (ep == 0u) ep = 1u
  auto q_touch = [&]() {
    const int g = tile * 147 + min(tid, 146);
    touch_lds((const char*)q + (size_t)(g < 32768 ? g : 0) * 128, sScr);
  };
  if (kind == 2) {
    KvRegs KR, KR2;
    EP_LOAD;
    kv_load(KR, kin, vin, shift_k, tile, tid);
    kv_load(KR2, kin, vin, shift_k, tile + 32, tid);
    code_touch();
    kv_wait<17>(KR);
    CTS(12);
    kv_store(KR, ws, lds + CV_STG, tile, tid);
    kv_wait<6>(KR2);
    kv_store(KR2, ws, lds, tile + 32, tid);
    CTS(1);
    EP_TAKE;
    __syncthreads();
    set_flag(ws + OFF_FKV + (tile + (wave & 1) * 32) * 128, wave < 2 ? lane : 64, ep);
    CTS(2);
    q_touch();
  } else {
    KvRegs KR;
    EP_LOAD;
    kv_load(KR, kin, vin, shift_k, tile, tid);
    const bool isconv = kind != 6;
    const int nw = (kind == 4 || j3 >= 92) ? 2 : 1, c0 = kind == 4 ? 8 * j4 : j3 < 92 ? 640 + 4 * j3 : j3 < 94 ? 272 + 8 * j3 : 0;
    u32x4v hv[8];
    unsigned dD;
    h_load(hv, dD, ssm_k, ssm_D, c0 + nw * wave, nw, isconv, lane);
    code_touch();
    kv_wait<11>(KR);
    CTS(12);
    kv_store(KR, ws, lds + CV_STG, tile, tid);
    CTS(1);
    asm volatile("s_waitcnt vmcnt(4)" : "+v"(hv[0]), "+v"(hv[1]), "+v"(hv[2]), "+v"(hv[3]), "+v"(hv[4]), "+v"(hv[5]), "+v"(hv[6]), "+v"(hv[7]), "+v"(dD) :: "memory");
    if (isconv) {
      *(u32x4v*)(lds + wave * CV_W + CV_U + (lane >> 5) * CV_UR + 4096 + (lane & 31) * 16) = (u32x4v){0u, 0u, 0u, 0u};
      h_spread<16 * NSA, CV_HQ>(hv + 4, lds + wave * CV_W + CV_Q, lane);
    }
    EP_TAKE;
    __syncthreads();
    set_flag(ws + OFF_FKV + tile * 128, tid, ep);
    CTS(2);
    if (isconv) {
      Conv cv;
      cv.init(ws, lds, c0, nw, kind == 4 ? j4 : 80 + j3, tid, ep, dD, w3, w4);
#define QT 1
      h_spread<128, CV_HP>(hv, lds + wave * CV_W, lane);
      cv.first();
      if (QT == 1) q_touch();
      cv.second(hv);
      if (QT == 2) q_touch();
    } else {
      q_touch();
#pragma unroll
      for (int i = 0; i < 4; ++i) {
        const unsigned g = slice * 256 + tid + NRW * 256 * i;
        if (g < 16384) touch_lds((const char*)w2 + (size_t)g * 128, sScr);
      }
    }
  }
  WAIT_LGKM0();
  __builtin_amdgcn_s_barrier();
  CTS(10);
  }
  const unsigned sDbg0 = 110u * 1048576u + (b * 4 + wave) * 8192;
  constexpr unsigned OOB = 0x80000000u;
#define LANE_ID(x) int x; asm volatile("v_mbcnt_lo_u32_b32 %0, -1, 0\n\tv_mbcnt_hi_u32_b32 %0, -1, %0" : "=v"(x))
#define HLP 0
  if (kind == 6 || (kind == 4 && j4 < HLP)) {
    LANE_ID(lr);
    const int r8 = lr & 7, seg = lr >> 3, j = lr & 15, g = lr >> 4;
    const unsigned sWave = wave, sExp = ep, sG0 = kind == 6 ? slice : 64 + j4, sStep = NRW, sOutOff = kind == 6 ? 256 : 65 + j4, sAuxOff = HLP, sBuf = kind & 2, sRk = slice;
    const unsigned sDbg = sDbg0 + (kind == 6 ? 0 : 1024);
    const unsigned vRF = (wave * 8 + (seg >> 2)) * 1024 + ((seg & 3) * 16 + r8) * 16;
    const unsigned vRwr = seg == 0 ? r8 * 64 + wave * 4 : 512 + lr * 4, vRrd = r8 * 64;
    const unsigned vRP = g == (j >> 2) ? r8 * 64 + 16 + wave * 8 + (seg & 1) * 4 : 768 + lr * 4;
    const unsigned vRS = (wave == 0 && seg < 2) ? r8 * 32 + seg * 16 : OOB, vFlag = lr < 32 ? lr * 4 : OOB;
    const unsigned vL4 = ((lr & 2) * 4 + wave * 2 + (lr & 1)) * 128, vLY = min((wave < 3 ? wave * 32 : 112) + lr, wave < 2 ? wave * 32 + 31 : wave == 2 ? 111 : NCV - 1) * 128;
    const unsigned vQ = r8 * 2048 + wave * 512 + seg * 16;
    const unsigned vY0 = wave * 4096 + lr * 16;
    const unsigned vTr = 4096 + wave * 4096 + ((2 * g + ((j & 3) >> 1)) * 8 + (j >> 2)) * 16 + (j & 1) * 8;
    const unsigned one = g == (j >> 2) ? 0x3f80u << (16 * (j & 1)) : 0u, vSa = (j & 2) ? 0u : one, vSb = (j & 2) ? one : 0u;
    (void)sDbg;
    asm volatile(
R"(.macro M d,w,x
v_mfma_f32_16x16x32_bf16 v[\d:\d+3], \w, v[\x:\x+3], v[\d:\d+3]
.endm
.macro R d,a,o
ds_read_b128 v[\d:\d+3], v\a offset:\o
.endm
.macro T d,a,o
ds_read_b64_tr_b16 \d, v\a offset:\o
.endm
.macro D a,s,o
buffer_load_dwordx4 v\a, s[36:39], s\s offen offset:\o sc1 lds
.endm
.macro L c
s_waitcnt lgkmcnt(\c)
.endm
Lcode_r_b:
s_mov_b64 s[36:37],%0
s_mov_b32 s38,111243392
s_mov_b32 s39,0x00020000
s_mov_b32 s44,%1
s_mov_b32 s45,%2
s_mov_b32 s46,%3
s_mov_b32 s47,%4
s_mov_b32 s49,%5
s_mov_b32 s51,%6
s_mov_b32 s93,%7
s_mov_b32 s53,%8
v_mov_b32 v0,%9
v_mov_b32 v1,%10
v_mov_b32 v2,%11
v_mov_b32 v3,%12
v_mov_b32 v4,%13
v_mov_b32 v5,%14
v_mov_b32 v6,%15
v_mov_b32 v7,%16
v_mov_b32 v8,%17
v_mov_b32 v9,%18
v_mov_b32 v10,%19
v_mov_b32 v11,%20
v_mov_b32 v12,%21
s_mov_b64 s[84:85],%22
s_mov_b32 s86,4194304
s_mov_b32 s87,0x00020000
s_lshl_b32 s55,s45,14
buffer_load_dwordx4 v[164:167],v8,s[84:87],s55 offen offset:0 nt
buffer_load_dwordx4 v[168:171],v8,s[84:87],s55 offen offset:128 nt
buffer_load_dwordx4 v[172:175],v8,s[84:87],s55 offen offset:256 nt
buffer_load_dwordx4 v[176:179],v8,s[84:87],s55 offen offset:384 nt
v_mov_b32 v13,s53
v_mbcnt_lo_u32_b32 v15,-1,0
v_mbcnt_hi_u32_b32 v15,-1,v15
v_lshlrev_b32 v15,4,v15
s_mov_b32 s82,0
s_mov_b32 s63,0x7fff0000
s_mov_b32 s64,0
s_mov_b32 s67,0
s_lshl_b32 s65,s44,12
s_add_u32 s65,s65,4096
s_lshl_b32 s66,s44,7
s_add_u32 s66,s66,109056000
s_mov_b32 s70,0xaaaaaaaa
s_mov_b32 s71,0xaaaaaaaa
s_mov_b32 s72,0xcccccccc
s_mov_b32 s73,0xcccccccc
s_mov_b32 s74,0xff00ff00
s_mov_b32 s75,0xff00ff00
s_mov_b32 s68,0x3a800000
s_mov_b32 s69,0x3727c5ac
s_mov_b32 s76,0xc0135761
s_mov_b32 s77,0x3d372713
v_mov_b32 v32,0x3f803f80
v_mov_b32 v33,0x3f803f80
v_mov_b32 v34,0x3f803f80
v_mov_b32 v35,0x3f803f80
v_mov_b32 v24,0
v_mov_b32 v25,0
v_mov_b32 v26,v11
v_mov_b32 v27,v12
v_mov_b32 v28,0
v_mov_b32 v29,0
s_waitcnt vmcnt(0)
Lr_a0_%=:
s_waitcnt vmcnt(10)
v_mfma_f32_16x16x32_bf16 v[36:39],v[32:35],v[164:167],0
s_add_u32 s79,s45,s46
s_cmp_lt_u32 s45,64
s_cselect_b32 s78,s49,0
v_mfma_f32_16x16x32_bf16 v[40:43],v[164:167],v[164:167],0
s_cmp_ge_u32 s79,64
s_cselect_b32 s78,s78,0
s_add_u32 s62,s79,s78
M 36,v[32:35],168
s_lshl_b32 s55,s62,14
s_cmp_lt_u32 s62,s47
s_cselect_b32 s55,s55,0x7fff0000
M 40,v[168:171],168
s_lshr_b32 s78,s45,1
buffer_load_dwordx4 v[196:199],v8,s[84:87],s55 offen offset:0 nt
M 36,v[32:35],172
s_and_b32 s79,s45,1
buffer_load_dwordx4 v[200:203],v8,s[84:87],s55 offen offset:128 nt
M 40,v[172:175],172
s_lshl_b32 s79,s79,7
buffer_load_dwordx4 v[204:207],v8,s[84:87],s55 offen offset:256 nt
M 36,v[32:35],176
s_lshl_b32 s57,s78,15
buffer_load_dwordx4 v[208:211],v8,s[84:87],s55 offen offset:384 nt
M 40,v[176:179],176
s_add_u32 s57,s57,s79
s_add_u32 s57,s57,12582912
s_add_u32 s58,s57,4096
v_mfma_f32_16x16x32_bf16 v[116:119],v[26:29],v[164:167],0
s_lshl_b32 s59,s45,8
s_add_u32 s59,s59,106954752
s_lshl_b32 s60,s45,9
v_mfma_f32_16x16x32_bf16 v[120:123],v[24:27],v[164:167],0
s_add_u32 s60,s60,s66
v_mfma_f32_16x16x32_bf16 v[124:127],v[26:29],v[168:171],0
v_mfma_f32_16x16x32_bf16 v[128:131],v[24:27],v[168:171],0
v_add_f32_dpp v44,v36,v36 row_ror:8 row_mask:0xf bank_mask:0xf
v_cndmask_b32 v45,v40,v41,s[70:71]
v_mfma_f32_16x16x32_bf16 v[132:135],v[26:29],v[172:175],0
ds_write_b32 v2,v44 offset:0
v_mfma_f32_16x16x32_bf16 v[136:139],v[24:27],v[172:175],0
v_cndmask_b32 v46,v42,v43,s[70:71]
v_cndmask_b32 v45,v45,v46,s[72:73]
v_mfma_f32_16x16x32_bf16 v[140:143],v[26:29],v[176:179],0
ds_write_b32 v0,v45 offset:0
v_mfma_f32_16x16x32_bf16 v[144:147],v[24:27],v[176:179],0
L 0
s_barrier
R 52,3,0
R 56,3,16
R 60,3,32
s_cmp_lg_u32 s67,0
s_cbranch_scc1 Lr_b0_%=
s_waitcnt vmcnt(4)
buffer_store_dword v13,v5,s[36:39],s63 offen offset:0 sc0 sc1
s_mov_b32 s63,0x7fff0000
s_cmp_lt_u32 s45,96
s_cselect_b32 s64,-160,0
s_add_u32 s64,s64,256
s_cmp_lt_u32 s45,96
s_cselect_b32 s61,0,32768
s_add_u32 s61,s61,111177728
s_mul_i32 s56,s45,16384
s_add_u32 s56,s56,98566144
Lr_bpy0_%=:
buffer_load_dword v14,v7,s[36:39],s61 offen offset:0 sc1
s_add_u32 s82,s82,1
s_waitcnt vmcnt(0)
v_cmp_eq_u32 vcc,s53,v14
s_nop 0
s_cmp_eq_u64 vcc,-1
s_cbranch_scc1 Lr_bqy0_%=
s_sleep 2
s_cmp_lt_u32 s82,20000
s_cbranch_scc1 Lr_bpy0_%=
Lr_bqy0_%=:
s_add_u32 m0,s65,0
s_nop 0
D 9,56,0
D 9,56,1024
D 9,56,2048
D 9,56,3072
s_waitcnt vmcnt(0)
Lr_b0_%=:
s_waitcnt vmcnt(10)
T v[68:69],10,0
T v[70:71],10,64
T v[72:73],10,1024
T v[74:75],10,1088
T v[76:77],10,2048
T v[78:79],10,2112
T v[80:81],10,3072
T v[82:83],10,3136
s_mov_b32 s45,s62
s_min_u32 s78,s64,s47
s_mul_i32 s56,s45,16384
s_add_u32 s56,s56,98566144
s_cmp_lt_u32 s45,s78
s_cselect_b32 s67,1,0
s_cselect_b32 s56,s56,0x7fff0000
s_add_u32 m0,s65,16384
s_nop 0
D 9,56,0
D 9,56,1024
D 9,56,2048
D 9,56,3072
L 8
v_pk_add_f32 v[52:53],v[52:53],v[54:55]
v_pk_add_f32 v[56:57],v[56:57],v[58:59]
v_pk_add_f32 v[60:61],v[60:61],v[62:63]
v_add_f32 v52,v52,v53
v_pk_add_f32 v[56:57],v[56:57],v[60:61]
v_mul_f32 v52,s68,v52
v_add_f32 v53,v56,v57
v_mul_f32 v53,s68,v53
v_fma_f32 v53,-v52,v52,v53
v_max_f32 v53,0,v53
L 6
v_lshlrev_b32 v84,16,v68
v_and_b32 v85,0xffff0000,v68
v_lshlrev_b32 v86,16,v69
v_and_b32 v87,0xffff0000,v69
v_lshlrev_b32 v88,16,v70
v_and_b32 v89,0xffff0000,v70
v_lshlrev_b32 v90,16,v71
v_and_b32 v91,0xffff0000,v71
L 4
v_lshlrev_b32 v92,16,v72
v_and_b32 v93,0xffff0000,v72
v_lshlrev_b32 v94,16,v73
v_and_b32 v95,0xffff0000,v73
v_lshlrev_b32 v96,16,v74
v_and_b32 v97,0xffff0000,v74
v_lshlrev_b32 v98,16,v75
v_and_b32 v99,0xffff0000,v75
L 2
v_lshlrev_b32 v100,16,v76
v_and_b32 v101,0xffff0000,v76
v_lshlrev_b32 v102,16,v77
v_and_b32 v103,0xffff0000,v77
v_lshlrev_b32 v104,16,v78
v_and_b32 v105,0xffff0000,v78
v_lshlrev_b32 v106,16,v79
v_and_b32 v107,0xffff0000,v79
L 0
v_lshlrev_b32 v108,16,v80
v_and_b32 v109,0xffff0000,v80
v_lshlrev_b32 v110,16,v81
v_and_b32 v111,0xffff0000,v81
v_lshlrev_b32 v112,16,v82
v_and_b32 v113,0xffff0000,v82
v_lshlrev_b32 v114,16,v83
v_and_b32 v115,0xffff0000,v83
v_add_f32 v53,s69,v53
v_rsq_f32 v48,v53
s_nop 0
v_mul_f32_e64 v50,-v52,v48
v_mov_b32 v49,v48
v_mov_b32 v51,v50
v_pk_fma_f32 v[52:53],v[116:117],v[48:49],v[50:51]
v_pk_fma_f32 v[54:55],v[118:119],v[48:49],v[50:51]
v_pk_fma_f32 v[56:57],v[120:121],v[48:49],v[50:51]
v_pk_fma_f32 v[58:59],v[122:123],v[48:49],v[50:51]
v_pk_fma_f32 v[116:117],v[52:53],v[84:85],v[116:117]
v_pk_fma_f32 v[118:119],v[54:55],v[86:87],v[118:119]
v_pk_fma_f32 v[120:121],v[56:57],v[88:89],v[120:121]
v_pk_fma_f32 v[122:123],v[58:59],v[90:91],v[122:123]
v_cvt_pk_bf16_f32 v148,v116,v117
v_cvt_pk_bf16_f32 v149,v118,v119
v_cvt_pk_bf16_f32 v150,v120,v121
v_cvt_pk_bf16_f32 v151,v122,v123
v_pk_fma_f32 v[52:53],v[124:125],v[48:49],v[50:51]
v_pk_fma_f32 v[54:55],v[126:127],v[48:49],v[50:51]
v_pk_fma_f32 v[56:57],v[128:129],v[48:49],v[50:51]
v_pk_fma_f32 v[58:59],v[130:131],v[48:49],v[50:51]
v_pk_fma_f32 v[124:125],v[52:53],v[92:93],v[124:125]
v_pk_fma_f32 v[126:127],v[54:55],v[94:95],v[126:127]
v_pk_fma_f32 v[128:129],v[56:57],v[96:97],v[128:129]
v_pk_fma_f32 v[130:131],v[58:59],v[98:99],v[130:131]
v_cvt_pk_bf16_f32 v152,v124,v125
v_cvt_pk_bf16_f32 v153,v126,v127
v_cvt_pk_bf16_f32 v154,v128,v129
v_cvt_pk_bf16_f32 v155,v130,v131
v_pk_fma_f32 v[52:53],v[132:133],v[48:49],v[50:51]
v_pk_fma_f32 v[54:55],v[134:135],v[48:49],v[50:51]
v_pk_fma_f32 v[56:57],v[136:137],v[48:49],v[50:51]
v_pk_fma_f32 v[58:59],v[138:139],v[48:49],v[50:51]
v_pk_fma_f32 v[132:133],v[52:53],v[100:101],v[132:133]
v_pk_fma_f32 v[134:135],v[54:55],v[102:103],v[134:135]
v_pk_fma_f32 v[136:137],v[56:57],v[104:105],v[136:137]
v_pk_fma_f32 v[138:139],v[58:59],v[106:107],v[138:139]
v_cvt_pk_bf16_f32 v156,v132,v133
v_cvt_pk_bf16_f32 v157,v134,v135
v_cvt_pk_bf16_f32 v158,v136,v137
v_cvt_pk_bf16_f32 v159,v138,v139
v_pk_fma_f32 v[52:53],v[140:141],v[48:49],v[50:51]
v_pk_fma_f32 v[54:55],v[142:143],v[48:49],v[50:51]
v_pk_fma_f32 v[56:57],v[144:145],v[48:49],v[50:51]
v_pk_fma_f32 v[58:59],v[146:147],v[48:49],v[50:51]
v_pk_fma_f32 v[140:141],v[52:53],v[108:109],v[140:141]
v_pk_fma_f32 v[142:143],v[54:55],v[110:111],v[142:143]
v_pk_fma_f32 v[144:145],v[56:57],v[112:113],v[144:145]
v_pk_fma_f32 v[146:147],v[58:59],v[114:115],v[146:147]
v_cvt_pk_bf16_f32 v160,v140,v141
v_cvt_pk_bf16_f32 v161,v142,v143
v_cvt_pk_bf16_f32 v162,v144,v145
v_cvt_pk_bf16_f32 v163,v146,v147
s_waitcnt vmcnt(8)
buffer_store_dword v13,v5,s[36:39],s63 offen offset:0 sc0 sc1
s_mov_b32 s63,0x7fff0000
v_mfma_f32_16x16x32_bf16 v[36:39],v[32:35],v[148:151],0
buffer_store_dwordx4 v[148:151],v1,s[36:39],s57 offen offset:0 sc0 sc1
v_mfma_f32_16x16x32_bf16 v[40:43],v[148:151],v[148:151],0
M 36,v[32:35],152
buffer_store_dwordx4 v[152:155],v1,s[36:39],s57 offen offset:2048 sc0 sc1
M 40,v[152:155],152
M 36,v[32:35],156
buffer_store_dwordx4 v[156:159],v1,s[36:39],s58 offen offset:0 sc0 sc1
M 40,v[156:159],156
M 36,v[32:35],160
buffer_store_dwordx4 v[160:163],v1,s[36:39],s58 offen offset:2048 sc0 sc1
M 40,v[160:163],160
s_nop 5
v_add_f32_dpp v44,v36,v36 row_ror:8 row_mask:0xf bank_mask:0xf
s_nop 0
v_cndmask_b32 v45,v40,v41,s[70:71]
v_cndmask_b32 v46,v42,v43,s[70:71]
ds_write_b32 v2,v44 offset:1024
v_cndmask_b32 v45,v45,v46,s[72:73]
ds_write_b32 v0,v45 offset:1024
L 0
s_barrier
R 52,3,1024
R 56,3,1040
R 60,3,1056
L 0
v_pk_add_f32 v[52:53],v[52:53],v[54:55]
v_pk_add_f32 v[56:57],v[56:57],v[58:59]
v_pk_add_f32 v[60:61],v[60:61],v[62:63]
v_add_f32 v52,v52,v53
v_pk_add_f32 v[56:57],v[56:57],v[60:61]
v_mul_f32 v52,s68,v52
v_add_f32 v53,v56,v57
v_mul_f32 v53,s68,v53
v_fma_f32 v53,-v52,v52,v53
v_max_f32 v53,0,v53
v_add_f32 v54,s69,v53
v_fma_f32 v55,s69,v54,v53
v_rsq_f32 v65,v54
v_rsq_f32 v54,v55
v_mul_f32_e64 v64,-v52,v65
v_mul_f32 v63,v55,v54
v_mul_f32 v61,s76,v54
v_mul_f32 v55,v54,v54
v_mul_f32 v62,s77,v55
v_mul_f32 v62,v62,v61
v_cndmask_b32 v60,v52,v64,s[74:75]
v_cndmask_b32 v61,v61,v65,s[74:75]
buffer_store_dwordx4 v[60:63],v4,s[36:39],s59 offen offset:0 sc0 sc1
s_mov_b32 s63,s60
s_cmp_lt_u32 s45,s47
s_cbranch_scc0 Lr_mid_%=
Lr_a1_%=:
s_waitcnt vmcnt(10)
v_mfma_f32_16x16x32_bf16 v[36:39],v[32:35],v[196:199],0
s_add_u32 s79,s45,s46
s_cmp_lt_u32 s45,64
s_cselect_b32 s78,s49,0
v_mfma_f32_16x16x32_bf16 v[40:43],v[196:199],v[196:199],0
s_cmp_ge_u32 s79,64
s_cselect_b32 s78,s78,0
s_add_u32 s62,s79,s78
M 36,v[32:35],200
s_lshl_b32 s55,s62,14
s_cmp_lt_u32 s62,s47
s_cselect_b32 s55,s55,0x7fff0000
M 40,v[200:203],200
s_lshr_b32 s78,s45,1
buffer_load_dwordx4 v[164:167],v8,s[84:87],s55 offen offset:0 nt
M 36,v[32:35],204
s_and_b32 s79,s45,1
buffer_load_dwordx4 v[168:171],v8,s[84:87],s55 offen offset:128 nt
M 40,v[204:207],204
s_lshl_b32 s79,s79,7
buffer_load_dwordx4 v[172:175],v8,s[84:87],s55 offen offset:256 nt
M 36,v[32:35],208
s_lshl_b32 s57,s78,15
buffer_load_dwordx4 v[176:179],v8,s[84:87],s55 offen offset:384 nt
M 40,v[208:211],208
s_add_u32 s57,s57,s79
s_add_u32 s57,s57,12582912
s_add_u32 s58,s57,4096
v_mfma_f32_16x16x32_bf16 v[116:119],v[26:29],v[196:199],0
s_lshl_b32 s59,s45,8
s_add_u32 s59,s59,106954752
s_lshl_b32 s60,s45,9
v_mfma_f32_16x16x32_bf16 v[120:123],v[24:27],v[196:199],0
s_add_u32 s60,s60,s66
v_mfma_f32_16x16x32_bf16 v[124:127],v[26:29],v[200:203],0
v_mfma_f32_16x16x32_bf16 v[128:131],v[24:27],v[200:203],0
v_add_f32_dpp v44,v36,v36 row_ror:8 row_mask:0xf bank_mask:0xf
v_cndmask_b32 v45,v40,v41,s[70:71]
v_mfma_f32_16x16x32_bf16 v[132:135],v[26:29],v[204:207],0
ds_write_b32 v2,v44 offset:0
v_mfma_f32_16x16x32_bf16 v[136:139],v[24:27],v[204:207],0
v_cndmask_b32 v46,v42,v43,s[70:71]
v_cndmask_b32 v45,v45,v46,s[72:73]
v_mfma_f32_16x16x32_bf16 v[140:143],v[26:29],v[208:211],0
ds_write_b32 v0,v45 offset:0
v_mfma_f32_16x16x32_bf16 v[144:147],v[24:27],v[208:211],0
L 0
s_barrier
R 52,3,0
R 56,3,16
R 60,3,32
s_cmp_lg_u32 s67,0
s_cbranch_scc1 Lr_b1_%=
s_waitcnt vmcnt(4)
buffer_store_dword v13,v5,s[36:39],s63 offen offset:0 sc0 sc1
s_mov_b32 s63,0x7fff0000
s_cmp_lt_u32 s45,96
s_cselect_b32 s64,-160,0
s_add_u32 s64,s64,256
s_cmp_lt_u32 s45,96
s_cselect_b32 s61,0,32768
s_add_u32 s61,s61,111177728
s_mul_i32 s56,s45,16384
s_add_u32 s56,s56,98566144
Lr_bpy1_%=:
buffer_load_dword v14,v7,s[36:39],s61 offen offset:0 sc1
s_add_u32 s82,s82,1
s_waitcnt vmcnt(0)
v_cmp_eq_u32 vcc,s53,v14
s_nop 0
s_cmp_eq_u64 vcc,-1
s_cbranch_scc1 Lr_bqy1_%=
s_sleep 2
s_cmp_lt_u32 s82,20000
s_cbranch_scc1 Lr_bpy1_%=
Lr_bqy1_%=:
s_add_u32 m0,s65,16384
s_nop 0
D 9,56,0
D 9,56,1024
D 9,56,2048
D 9,56,3072
s_waitcnt vmcnt(0)
Lr_b1_%=:
s_waitcnt vmcnt(10)
T v[68:69],10,16384
T v[70:71],10,16448
T v[72:73],10,17408
T v[74:75],10,17472
T v[76:77],10,18432
T v[78:79],10,18496
T v[80:81],10,19456
T v[82:83],10,19520
s_mov_b32 s45,s62
s_min_u32 s78,s64,s47
s_mul_i32 s56,s45,16384
s_add_u32 s56,s56,98566144
s_cmp_lt_u32 s45,s78
s_cselect_b32 s67,1,0
s_cselect_b32 s56,s56,0x7fff0000
s_add_u32 m0,s65,0
s_nop 0
D 9,56,0
D 9,56,1024
D 9,56,2048
D 9,56,3072
L 8
v_pk_add_f32 v[52:53],v[52:53],v[54:55]
v_pk_add_f32 v[56:57],v[56:57],v[58:59]
v_pk_add_f32 v[60:61],v[60:61],v[62:63]
v_add_f32 v52,v52,v53
v_pk_add_f32 v[56:57],v[56:57],v[60:61]
v_mul_f32 v52,s68,v52
v_add_f32 v53,v56,v57
v_mul_f32 v53,s68,v53
v_fma_f32 v53,-v52,v52,v53
v_max_f32 v53,0,v53
L 6
v_lshlrev_b32 v84,16,v68
v_and_b32 v85,0xffff0000,v68
v_lshlrev_b32 v86,16,v69
v_and_b32 v87,0xffff0000,v69
v_lshlrev_b32 v88,16,v70
v_and_b32 v89,0xffff0000,v70
v_lshlrev_b32 v90,16,v71
v_and_b32 v91,0xffff0000,v71
L 4
v_lshlrev_b32 v92,16,v72
v_and_b32 v93,0xffff0000,v72
v_lshlrev_b32 v94,16,v73
v_and_b32 v95,0xffff0000,v73
v_lshlrev_b32 v96,16,v74
v_and_b32 v97,0xffff0000,v74
v_lshlrev_b32 v98,16,v75
v_and_b32 v99,0xffff0000,v75
L 2
v_lshlrev_b32 v100,16,v76
v_and_b32 v101,0xffff0000,v76
v_lshlrev_b32 v102,16,v77
v_and_b32 v103,0xffff0000,v77
v_lshlrev_b32 v104,16,v78
v_and_b32 v105,0xffff0000,v78
v_lshlrev_b32 v106,16,v79
v_and_b32 v107,0xffff0000,v79
L 0
v_lshlrev_b32 v108,16,v80
v_and_b32 v109,0xffff0000,v80
v_lshlrev_b32 v110,16,v81
v_and_b32 v111,0xffff0000,v81
v_lshlrev_b32 v112,16,v82
v_and_b32 v113,0xffff0000,v82
v_lshlrev_b32 v114,16,v83
v_and_b32 v115,0xffff0000,v83
)"
R"(v_add_f32 v53,s69,v53
v_rsq_f32 v48,v53
s_nop 0
v_mul_f32_e64 v50,-v52,v48
v_mov_b32 v49,v48
v_mov_b32 v51,v50
v_pk_fma_f32 v[52:53],v[116:117],v[48:49],v[50:51]
v_pk_fma_f32 v[54:55],v[118:119],v[48:49],v[50:51]
v_pk_fma_f32 v[56:57],v[120:121],v[48:49],v[50:51]
v_pk_fma_f32 v[58:59],v[122:123],v[48:49],v[50:51]
v_pk_fma_f32 v[116:117],v[52:53],v[84:85],v[116:117]
v_pk_fma_f32 v[118:119],v[54:55],v[86:87],v[118:119]
v_pk_fma_f32 v[120:121],v[56:57],v[88:89],v[120:121]
v_pk_fma_f32 v[122:123],v[58:59],v[90:91],v[122:123]
v_cvt_pk_bf16_f32 v148,v116,v117
v_cvt_pk_bf16_f32 v149,v118,v119
v_cvt_pk_bf16_f32 v150,v120,v121
v_cvt_pk_bf16_f32 v151,v122,v123
v_pk_fma_f32 v[52:53],v[124:125],v[48:49],v[50:51]
v_pk_fma_f32 v[54:55],v[126:127],v[48:49],v[50:51]
v_pk_fma_f32 v[56:57],v[128:129],v[48:49],v[50:51]
v_pk_fma_f32 v[58:59],v[130:131],v[48:49],v[50:51]
v_pk_fma_f32 v[124:125],v[52:53],v[92:93],v[124:125]
v_pk_fma_f32 v[126:127],v[54:55],v[94:95],v[126:127]
v_pk_fma_f32 v[128:129],v[56:57],v[96:97],v[128:129]
v_pk_fma_f32 v[130:131],v[58:59],v[98:99],v[130:131]
v_cvt_pk_bf16_f32 v152,v124,v125
v_cvt_pk_bf16_f32 v153,v126,v127
v_cvt_pk_bf16_f32 v154,v128,v129
v_cvt_pk_bf16_f32 v155,v130,v131
v_pk_fma_f32 v[52:53],v[132:133],v[48:49],v[50:51]
v_pk_fma_f32 v[54:55],v[134:135],v[48:49],v[50:51]
v_pk_fma_f32 v[56:57],v[136:137],v[48:49],v[50:51]
v_pk_fma_f32 v[58:59],v[138:139],v[48:49],v[50:51]
v_pk_fma_f32 v[132:133],v[52:53],v[100:101],v[132:133]
v_pk_fma_f32 v[134:135],v[54:55],v[102:103],v[134:135]
v_pk_fma_f32 v[136:137],v[56:57],v[104:105],v[136:137]
v_pk_fma_f32 v[138:139],v[58:59],v[106:107],v[138:139]
v_cvt_pk_bf16_f32 v156,v132,v133
v_cvt_pk_bf16_f32 v157,v134,v135
v_cvt_pk_bf16_f32 v158,v136,v137
v_cvt_pk_bf16_f32 v159,v138,v139
v_pk_fma_f32 v[52:53],v[140:141],v[48:49],v[50:51]
v_pk_fma_f32 v[54:55],v[142:143],v[48:49],v[50:51]
v_pk_fma_f32 v[56:57],v[144:145],v[48:49],v[50:51]
v_pk_fma_f32 v[58:59],v[146:147],v[48:49],v[50:51]
v_pk_fma_f32 v[140:141],v[52:53],v[108:109],v[140:141]
v_pk_fma_f32 v[142:143],v[54:55],v[110:111],v[142:143]
v_pk_fma_f32 v[144:145],v[56:57],v[112:113],v[144:145]
v_pk_fma_f32 v[146:147],v[58:59],v[114:115],v[146:147]
v_cvt_pk_bf16_f32 v160,v140,v141
v_cvt_pk_bf16_f32 v161,v142,v143
v_cvt_pk_bf16_f32 v162,v144,v145
v_cvt_pk_bf16_f32 v163,v146,v147
s_waitcnt vmcnt(8)
buffer_store_dword v13,v5,s[36:39],s63 offen offset:0 sc0 sc1
s_mov_b32 s63,0x7fff0000
v_mfma_f32_16x16x32_bf16 v[36:39],v[32:35],v[148:151],0
buffer_store_dwordx4 v[148:151],v1,s[36:39],s57 offen offset:0 sc0 sc1
v_mfma_f32_16x16x32_bf16 v[40:43],v[148:151],v[148:151],0
M 36,v[32:35],152
buffer_store_dwordx4 v[152:155],v1,s[36:39],s57 offen offset:2048 sc0 sc1
M 40,v[152:155],152
M 36,v[32:35],156
buffer_store_dwordx4 v[156:159],v1,s[36:39],s58 offen offset:0 sc0 sc1
M 40,v[156:159],156
M 36,v[32:35],160
buffer_store_dwordx4 v[160:163],v1,s[36:39],s58 offen offset:2048 sc0 sc1
M 40,v[160:163],160
s_nop 5
v_add_f32_dpp v44,v36,v36 row_ror:8 row_mask:0xf bank_mask:0xf
s_nop 0
v_cndmask_b32 v45,v40,v41,s[70:71]
v_cndmask_b32 v46,v42,v43,s[70:71]
ds_write_b32 v2,v44 offset:1024
v_cndmask_b32 v45,v45,v46,s[72:73]
ds_write_b32 v0,v45 offset:1024
L 0
s_barrier
R 52,3,1024
R 56,3,1040
R 60,3,1056
L 0
v_pk_add_f32 v[52:53],v[52:53],v[54:55]
v_pk_add_f32 v[56:57],v[56:57],v[58:59]
v_pk_add_f32 v[60:61],v[60:61],v[62:63]
v_add_f32 v52,v52,v53
v_pk_add_f32 v[56:57],v[56:57],v[60:61]
v_mul_f32 v52,s68,v52
v_add_f32 v53,v56,v57
v_mul_f32 v53,s68,v53
v_fma_f32 v53,-v52,v52,v53
v_max_f32 v53,0,v53
v_add_f32 v54,s69,v53
v_fma_f32 v55,s69,v54,v53
v_rsq_f32 v65,v54
v_rsq_f32 v54,v55
v_mul_f32_e64 v64,-v52,v65
v_mul_f32 v63,v55,v54
v_mul_f32 v61,s76,v54
v_mul_f32 v55,v54,v54
v_mul_f32 v62,s77,v55
v_mul_f32 v62,v62,v61
v_cndmask_b32 v60,v52,v64,s[74:75]
v_cndmask_b32 v61,v61,v65,s[74:75]
buffer_store_dwordx4 v[60:63],v4,s[36:39],s59 offen offset:0 sc0 sc1
s_mov_b32 s63,s60
s_cmp_lt_u32 s45,s47
s_cbranch_scc1 Lr_a0_%=
Lr_mid_%=:
s_waitcnt vmcnt(0)
buffer_store_dword v13,v5,s[36:39],s63 offen offset:0 sc0 sc1
s_mov_b32 s63,0x7fff0000
s_cmp_eq_u32 s51,0
s_cbranch_scc1 Lr_done_%=
s_mov_b32 s45,s93
s_lshr_b32 s78,s45,1
s_and_b32 s79,s45,1
s_lshl_b32 s79,s79,7
s_lshl_b32 s55,s78,15
s_add_u32 s55,s55,s79
s_mul_i32 s56,s45,16384
s_add_u32 s56,s56,98566144
s_lshl_b32 s61,s78,11
s_add_u32 s61,s61,110587904
s_add_u32 s78,s45,s46
s_lshr_b32 s78,s78,1
s_lshl_b32 s62,s78,11
s_add_u32 s62,s62,110587904
Lr_bps_%=:
buffer_load_dword v14,v6,s[36:39],s61 offen offset:0 sc1
s_add_u32 s82,s82,1
s_waitcnt vmcnt(0)
v_cmp_eq_u32 vcc,s53,v14
s_nop 0
s_cmp_eq_u64 vcc,-1
s_cbranch_scc1 Lr_bqs_%=
s_sleep 2
s_cmp_lt_u32 s82,20000
s_cbranch_scc1 Lr_bps_%=
Lr_bqs_%=:
s_add_u32 s78,s55,71303168
buffer_load_dwordx4 v[164:167],v1,s[36:39],s78 offen offset:0 sc1
buffer_load_dwordx4 v[168:171],v1,s[36:39],s78 offen offset:2048 sc1
s_add_u32 s78,s55,71307264
buffer_load_dwordx4 v[172:175],v1,s[36:39],s78 offen offset:0 sc1
buffer_load_dwordx4 v[176:179],v1,s[36:39],s78 offen offset:2048 sc1
s_add_u32 s78,s55,89128960
buffer_load_dwordx4 v[180:183],v1,s[36:39],s78 offen offset:0 sc1
buffer_load_dwordx4 v[184:187],v1,s[36:39],s78 offen offset:2048 sc1
s_add_u32 s78,s55,89133056
buffer_load_dwordx4 v[188:191],v1,s[36:39],s78 offen offset:0 sc1
buffer_load_dwordx4 v[192:195],v1,s[36:39],s78 offen offset:2048 sc1
s_add_u32 m0,s65,0
s_nop 0
D 9,56,0
D 9,56,1024
D 9,56,2048
D 9,56,3072
buffer_load_dword v14,v6,s[36:39],s62 offen offset:0 sc1
s_lshr_b32 s78,s45,1
s_and_b32 s79,s45,1
s_lshl_b32 s79,s79,7
s_lshl_b32 s57,s78,15
s_add_u32 s57,s57,s79
s_add_u32 s57,s57,16777216
s_add_u32 s58,s57,4096
s_lshl_b32 s59,s45,8
s_add_u32 s59,s59,107020288
s_lshl_b32 s60,s45,9
s_add_u32 s60,s60,s66
s_add_u32 s60,s60,131072
s_add_u32 s45,s45,s46
s_lshr_b32 s78,s45,1
s_and_b32 s79,s45,1
s_lshl_b32 s79,s79,7
s_lshl_b32 s55,s78,15
s_add_u32 s55,s55,s79
s_mul_i32 s56,s45,16384
s_add_u32 s56,s56,98566144
s_lshl_b32 s61,s78,11
s_add_u32 s61,s61,110587904
s_add_u32 s78,s45,s46
s_lshr_b32 s78,s78,1
s_lshl_b32 s62,s78,11
s_add_u32 s62,s62,110587904
s_waitcnt vmcnt(0)
Lr_it0_%=:
T v[68:69],10,0
T v[70:71],10,64
T v[72:73],10,1024
T v[74:75],10,1088
T v[76:77],10,2048
T v[78:79],10,2112
T v[80:81],10,3072
T v[82:83],10,3136
s_nop 0
v_cmp_eq_u32 vcc,s53,v14
s_nop 0
s_cmp_eq_u64 vcc,-1
s_cselect_b32 s78,s45,256
s_cmp_lt_u32 s78,256
s_cbranch_scc1 Lr_ta0_%=
buffer_load_dword v14,v6,s[36:39],s61 offen offset:0 sc1
v_mfma_f32_16x16x32_bf16 v[116:119],v[26:29],v[164:167],0
L 6
v_lshlrev_b32 v84,16,v68
v_and_b32 v85,0xffff0000,v68
v_mfma_f32_16x16x32_bf16 v[120:123],v[24:27],v[164:167],0
L 4
v_lshlrev_b32 v86,16,v69
v_and_b32 v87,0xffff0000,v69
M 116,v[26:29],180
L 2
v_lshlrev_b32 v88,16,v70
v_and_b32 v89,0xffff0000,v70
M 120,v[24:27],180
L 0
v_lshlrev_b32 v90,16,v71
v_and_b32 v91,0xffff0000,v71
v_mfma_f32_16x16x32_bf16 v[124:127],v[26:29],v[168:171],0
v_lshlrev_b32 v92,16,v72
v_and_b32 v93,0xffff0000,v72
v_mfma_f32_16x16x32_bf16 v[128:131],v[24:27],v[168:171],0
v_lshlrev_b32 v94,16,v73
v_and_b32 v95,0xffff0000,v73
M 124,v[26:29],184
v_cvt_pk_bf16_f32 v148,v116,v117
v_cvt_pk_bf16_f32 v149,v118,v119
M 128,v[24:27],184
v_cvt_pk_bf16_f32 v150,v120,v121
v_cvt_pk_bf16_f32 v151,v122,v123
v_mfma_f32_16x16x32_bf16 v[132:135],v[26:29],v[172:175],0
v_lshlrev_b32 v96,16,v74
v_and_b32 v97,0xffff0000,v74
v_mfma_f32_16x16x32_bf16 v[136:139],v[24:27],v[172:175],0
v_lshlrev_b32 v98,16,v75
v_and_b32 v99,0xffff0000,v75
M 132,v[26:29],188
v_cvt_pk_bf16_f32 v152,v124,v125
v_cvt_pk_bf16_f32 v153,v126,v127
M 136,v[24:27],188
v_cvt_pk_bf16_f32 v154,v128,v129
v_cvt_pk_bf16_f32 v155,v130,v131
v_mfma_f32_16x16x32_bf16 v[140:143],v[26:29],v[176:179],0
v_lshlrev_b32 v100,16,v76
v_and_b32 v101,0xffff0000,v76
v_mfma_f32_16x16x32_bf16 v[144:147],v[24:27],v[176:179],0
v_lshlrev_b32 v102,16,v77
v_and_b32 v103,0xffff0000,v77
M 140,v[26:29],192
v_cvt_pk_bf16_f32 v156,v132,v133
v_cvt_pk_bf16_f32 v157,v134,v135
M 144,v[24:27],192
v_cvt_pk_bf16_f32 v158,v136,v137
v_cvt_pk_bf16_f32 v159,v138,v139
v_mfma_f32_16x16x32_bf16 v[36:39],v[32:35],v[148:151],0
v_lshlrev_b32 v104,16,v78
v_and_b32 v105,0xffff0000,v78
v_mfma_f32_16x16x32_bf16 v[40:43],v[148:151],v[148:151],0
v_lshlrev_b32 v106,16,v79
v_and_b32 v107,0xffff0000,v79
M 36,v[32:35],152
v_cvt_pk_bf16_f32 v160,v140,v141
v_cvt_pk_bf16_f32 v161,v142,v143
M 40,v[152:155],152
v_cvt_pk_bf16_f32 v162,v144,v145
v_cvt_pk_bf16_f32 v163,v146,v147
M 36,v[32:35],156
v_lshlrev_b32 v108,16,v80
v_and_b32 v109,0xffff0000,v80
M 40,v[156:159],156
v_lshlrev_b32 v110,16,v81
v_and_b32 v111,0xffff0000,v81
M 36,v[32:35],160
v_lshlrev_b32 v112,16,v82
v_and_b32 v113,0xffff0000,v82
M 40,v[160:163],160
v_lshlrev_b32 v114,16,v83
v_and_b32 v115,0xffff0000,v83
s_nop 2
v_add_f32_dpp v44,v36,v36 row_ror:8 row_mask:0xf bank_mask:0xf
s_nop 1
v_cndmask_b32 v45,v40,v41,s[70:71]
v_cndmask_b32 v46,v42,v43,s[70:71]
ds_write_b32 v2,v44 offset:0
v_cndmask_b32 v45,v45,v46,s[72:73]
ds_write_b32 v0,v45 offset:0
L 0
s_barrier
R 52,3,0
R 56,3,16
R 60,3,32
L 0
v_pk_add_f32 v[52:53],v[52:53],v[54:55]
v_pk_add_f32 v[56:57],v[56:57],v[58:59]
v_pk_add_f32 v[60:61],v[60:61],v[62:63]
v_add_f32 v52,v52,v53
v_pk_add_f32 v[56:57],v[56:57],v[60:61]
v_mul_f32 v52,s68,v52
v_add_f32 v53,v56,v57
v_mul_f32 v53,s68,v53
v_fma_f32 v53,-v52,v52,v53
v_max_f32 v53,0,v53
s_waitcnt vmcnt(1)
buffer_store_dword v13,v5,s[36:39],s63 offen offset:0 sc0 sc1
s_mov_b32 s63,0x7fff0000
s_waitcnt vmcnt(1)
v_cmp_eq_u32 vcc,s53,v14
s_nop 0
s_cmp_eq_u64 vcc,-1
s_cselect_b32 s78,s45,256
s_cmp_lt_u32 s78,256
s_cbranch_scc1 Lr_tb0_%=
buffer_load_dword v14,v6,s[36:39],s61 offen offset:0 sc1
v_add_f32 v53,s69,v53
v_rsq_f32 v48,v53
s_nop 0
v_mul_f32_e64 v50,-v52,v48
v_mov_b32 v49,v48
v_mov_b32 v51,v50
v_pk_fma_f32 v[52:53],v[116:117],v[48:49],v[50:51]
v_pk_fma_f32 v[54:55],v[118:119],v[48:49],v[50:51]
v_pk_fma_f32 v[56:57],v[120:121],v[48:49],v[50:51]
v_pk_fma_f32 v[58:59],v[122:123],v[48:49],v[50:51]
v_pk_fma_f32 v[116:117],v[52:53],v[84:85],v[116:117]
v_pk_fma_f32 v[118:119],v[54:55],v[86:87],v[118:119]
v_pk_fma_f32 v[120:121],v[56:57],v[88:89],v[120:121]
v_pk_fma_f32 v[122:123],v[58:59],v[90:91],v[122:123]
v_cvt_pk_bf16_f32 v148,v116,v117
v_cvt_pk_bf16_f32 v149,v118,v119
v_cvt_pk_bf16_f32 v150,v120,v121
v_cvt_pk_bf16_f32 v151,v122,v123
v_pk_fma_f32 v[52:53],v[124:125],v[48:49],v[50:51]
v_pk_fma_f32 v[54:55],v[126:127],v[48:49],v[50:51]
v_pk_fma_f32 v[56:57],v[128:129],v[48:49],v[50:51]
v_pk_fma_f32 v[58:59],v[130:131],v[48:49],v[50:51]
v_pk_fma_f32 v[124:125],v[52:53],v[92:93],v[124:125]
v_pk_fma_f32 v[126:127],v[54:55],v[94:95],v[126:127]
v_pk_fma_f32 v[128:129],v[56:57],v[96:97],v[128:129]
v_pk_fma_f32 v[130:131],v[58:59],v[98:99],v[130:131]
v_cvt_pk_bf16_f32 v152,v124,v125
v_cvt_pk_bf16_f32 v153,v126,v127
v_cvt_pk_bf16_f32 v154,v128,v129
v_cvt_pk_bf16_f32 v155,v130,v131
v_pk_fma_f32 v[52:53],v[132:133],v[48:49],v[50:51]
v_pk_fma_f32 v[54:55],v[134:135],v[48:49],v[50:51]
v_pk_fma_f32 v[56:57],v[136:137],v[48:49],v[50:51]
v_pk_fma_f32 v[58:59],v[138:139],v[48:49],v[50:51]
v_pk_fma_f32 v[132:133],v[52:53],v[100:101],v[132:133]
v_pk_fma_f32 v[134:135],v[54:55],v[102:103],v[134:135]
v_pk_fma_f32 v[136:137],v[56:57],v[104:105],v[136:137]
v_pk_fma_f32 v[138:139],v[58:59],v[106:107],v[138:139]
v_cvt_pk_bf16_f32 v156,v132,v133
v_cvt_pk_bf16_f32 v157,v134,v135
v_cvt_pk_bf16_f32 v158,v136,v137
v_cvt_pk_bf16_f32 v159,v138,v139
v_pk_fma_f32 v[52:53],v[140:141],v[48:49],v[50:51]
v_pk_fma_f32 v[54:55],v[142:143],v[48:49],v[50:51]
v_pk_fma_f32 v[56:57],v[144:145],v[48:49],v[50:51]
v_pk_fma_f32 v[58:59],v[146:147],v[48:49],v[50:51]
v_pk_fma_f32 v[140:141],v[52:53],v[108:109],v[140:141]
v_pk_fma_f32 v[142:143],v[54:55],v[110:111],v[142:143]
v_pk_fma_f32 v[144:145],v[56:57],v[112:113],v[144:145]
v_pk_fma_f32 v[146:147],v[58:59],v[114:115],v[146:147]
v_cvt_pk_bf16_f32 v160,v140,v141
v_cvt_pk_bf16_f32 v161,v142,v143
v_cvt_pk_bf16_f32 v162,v144,v145
v_cvt_pk_bf16_f32 v163,v146,v147
v_mfma_f32_16x16x32_bf16 v[36:39],v[32:35],v[148:151],0
buffer_store_dwordx4 v[148:151],v1,s[36:39],s57 offen offset:0 sc0 sc1
v_mfma_f32_16x16x32_bf16 v[40:43],v[148:151],v[148:151],0
M 36,v[32:35],152
buffer_store_dwordx4 v[152:155],v1,s[36:39],s57 offen offset:2048 sc0 sc1
M 40,v[152:155],152
M 36,v[32:35],156
buffer_store_dwordx4 v[156:159],v1,s[36:39],s58 offen offset:0 sc0 sc1
M 40,v[156:159],156
M 36,v[32:35],160
buffer_store_dwordx4 v[160:163],v1,s[36:39],s58 offen offset:2048 sc0 sc1
M 40,v[160:163],160
s_nop 5
v_add_f32_dpp v44,v36,v36 row_ror:8 row_mask:0xf bank_mask:0xf
s_nop 0
v_cndmask_b32 v45,v40,v41,s[70:71]
v_cndmask_b32 v46,v42,v43,s[70:71]
ds_write_b32 v2,v44 offset:1024
v_cndmask_b32 v45,v45,v46,s[72:73]
ds_write_b32 v0,v45 offset:1024
L 0
s_barrier
R 52,3,1024
R 56,3,1040
R 60,3,1056
L 0
v_pk_add_f32 v[52:53],v[52:53],v[54:55]
v_pk_add_f32 v[56:57],v[56:57],v[58:59]
v_pk_add_f32 v[60:61],v[60:61],v[62:63]
v_add_f32 v52,v52,v53
v_pk_add_f32 v[56:57],v[56:57],v[60:61]
v_mul_f32 v52,s68,v52
v_add_f32 v53,v56,v57
v_mul_f32 v53,s68,v53
v_fma_f32 v53,-v52,v52,v53
v_max_f32 v53,0,v53
v_add_f32 v54,s69,v53
v_fma_f32 v55,s69,v54,v53
v_rsq_f32 v65,v54
v_rsq_f32 v54,v55
v_mul_f32_e64 v64,-v52,v65
v_mul_f32 v63,v55,v54
v_mul_f32 v61,s76,v54
v_mul_f32 v55,v54,v54
v_mul_f32 v62,s77,v55
v_mul_f32 v62,v62,v61
v_cndmask_b32 v60,v52,v64,s[74:75]
v_cndmask_b32 v61,v61,v65,s[74:75]
buffer_store_dwordx4 v[60:63],v4,s[36:39],s59 offen offset:0 sc0 sc1
s_mov_b32 s63,s60
s_waitcnt vmcnt(5)
v_cmp_eq_u32 vcc,s53,v14
s_nop 0
s_cmp_eq_u64 vcc,-1
s_cselect_b32 s78,s45,256
s_cmp_lt_u32 s78,256
s_cbranch_scc1 Lr_tc0_%=
s_waitcnt vmcnt(0)
buffer_store_dword v13,v5,s[36:39],s63 offen offset:0 sc0 sc1
s_mov_b32 s63,0x7fff0000
s_cmp_ge_u32 s45,256
s_cbranch_scc1 Lr_done_%=
Lr_bp0_%=:
buffer_load_dword v14,v6,s[36:39],s61 offen offset:0 sc1
s_add_u32 s82,s82,1
s_waitcnt vmcnt(0)
v_cmp_eq_u32 vcc,s53,v14
s_nop 0
s_cmp_eq_u64 vcc,-1
s_cbranch_scc1 Lr_bq0_%=
s_sleep 2
s_cmp_lt_u32 s82,20000
s_cbranch_scc1 Lr_bp0_%=
Lr_bq0_%=:
s_add_u32 s78,s55,71303168
buffer_load_dwordx4 v[196:199],v1,s[36:39],s78 offen offset:0 sc1
buffer_load_dwordx4 v[200:203],v1,s[36:39],s78 offen offset:2048 sc1
s_add_u32 s78,s55,71307264
buffer_load_dwordx4 v[204:207],v1,s[36:39],s78 offen offset:0 sc1
buffer_load_dwordx4 v[208:211],v1,s[36:39],s78 offen offset:2048 sc1
s_add_u32 s78,s55,89128960
buffer_load_dwordx4 v[212:215],v1,s[36:39],s78 offen offset:0 sc1
buffer_load_dwordx4 v[216:219],v1,s[36:39],s78 offen offset:2048 sc1
s_add_u32 s78,s55,89133056
buffer_load_dwordx4 v[220:223],v1,s[36:39],s78 offen offset:0 sc1
buffer_load_dwordx4 v[224:227],v1,s[36:39],s78 offen offset:2048 sc1
s_add_u32 m0,s65,16384
s_nop 0
D 9,56,0
D 9,56,1024
D 9,56,2048
D 9,56,3072
buffer_load_dword v14,v6,s[36:39],s62 offen offset:0 sc1
s_lshr_b32 s78,s45,1
s_and_b32 s79,s45,1
s_lshl_b32 s79,s79,7
s_lshl_b32 s57,s78,15
s_add_u32 s57,s57,s79
s_add_u32 s57,s57,16777216
s_add_u32 s58,s57,4096
s_lshl_b32 s59,s45,8
s_add_u32 s59,s59,107020288
s_lshl_b32 s60,s45,9
s_add_u32 s60,s60,s66
s_add_u32 s60,s60,131072
s_add_u32 s45,s45,s46
s_lshr_b32 s78,s45,1
s_and_b32 s79,s45,1
s_lshl_b32 s79,s79,7
s_lshl_b32 s55,s78,15
s_add_u32 s55,s55,s79
s_mul_i32 s56,s45,16384
s_add_u32 s56,s56,98566144
s_lshl_b32 s61,s78,11
s_add_u32 s61,s61,110587904
s_add_u32 s78,s45,s46
s_lshr_b32 s78,s78,1
s_lshl_b32 s62,s78,11
s_add_u32 s62,s62,110587904
s_waitcnt vmcnt(0)
s_branch Lr_it1_%=
Lr_ta0_%=:
v_mfma_f32_16x16x32_bf16 v[116:119],v[26:29],v[164:167],0
s_add_u32 s78,s55,71303168
buffer_load_dwordx4 v[196:199],v1,s[36:39],s78 offen offset:0 sc1
v_mfma_f32_16x16x32_bf16 v[120:123],v[24:27],v[164:167],0
L 6
buffer_load_dwordx4 v[200:203],v1,s[36:39],s78 offen offset:2048 sc1
M 116,v[26:29],180
s_add_u32 s78,s55,71307264
buffer_load_dwordx4 v[204:207],v1,s[36:39],s78 offen offset:0 sc1
)"
R"(M 120,v[24:27],180
L 4
buffer_load_dwordx4 v[208:211],v1,s[36:39],s78 offen offset:2048 sc1
v_mfma_f32_16x16x32_bf16 v[124:127],v[26:29],v[168:171],0
s_add_u32 s78,s55,89128960
buffer_load_dwordx4 v[212:215],v1,s[36:39],s78 offen offset:0 sc1
v_mfma_f32_16x16x32_bf16 v[128:131],v[24:27],v[168:171],0
L 2
buffer_load_dwordx4 v[216:219],v1,s[36:39],s78 offen offset:2048 sc1
M 124,v[26:29],184
s_add_u32 s78,s55,89133056
buffer_load_dwordx4 v[220:223],v1,s[36:39],s78 offen offset:0 sc1
M 128,v[24:27],184
L 0
buffer_load_dwordx4 v[224:227],v1,s[36:39],s78 offen offset:2048 sc1
v_mfma_f32_16x16x32_bf16 v[132:135],v[26:29],v[172:175],0
s_add_u32 m0,s65,16384
v_cvt_pk_bf16_f32 v148,v116,v117
v_cvt_pk_bf16_f32 v149,v118,v119
v_mfma_f32_16x16x32_bf16 v[136:139],v[24:27],v[172:175],0
D 9,56,0
M 132,v[26:29],188
D 9,56,1024
M 136,v[24:27],188
D 9,56,2048
v_mfma_f32_16x16x32_bf16 v[140:143],v[26:29],v[176:179],0
D 9,56,3072
v_mfma_f32_16x16x32_bf16 v[144:147],v[24:27],v[176:179],0
v_cvt_pk_bf16_f32 v150,v120,v121
v_cvt_pk_bf16_f32 v151,v122,v123
M 140,v[26:29],192
v_cvt_pk_bf16_f32 v152,v124,v125
v_cvt_pk_bf16_f32 v153,v126,v127
M 144,v[24:27],192
v_cvt_pk_bf16_f32 v154,v128,v129
v_cvt_pk_bf16_f32 v155,v130,v131
v_mfma_f32_16x16x32_bf16 v[36:39],v[32:35],v[148:151],0
v_cvt_pk_bf16_f32 v156,v132,v133
v_cvt_pk_bf16_f32 v157,v134,v135
v_mfma_f32_16x16x32_bf16 v[40:43],v[148:151],v[148:151],0
v_cvt_pk_bf16_f32 v158,v136,v137
v_cvt_pk_bf16_f32 v159,v138,v139
M 36,v[32:35],152
v_cvt_pk_bf16_f32 v160,v140,v141
v_cvt_pk_bf16_f32 v161,v142,v143
M 40,v[152:155],152
v_cvt_pk_bf16_f32 v162,v144,v145
v_cvt_pk_bf16_f32 v163,v146,v147
M 36,v[32:35],156
v_lshlrev_b32 v84,16,v68
v_and_b32 v85,0xffff0000,v68
M 40,v[156:159],156
v_lshlrev_b32 v86,16,v69
v_and_b32 v87,0xffff0000,v69
M 36,v[32:35],160
v_lshlrev_b32 v88,16,v70
v_and_b32 v89,0xffff0000,v70
M 40,v[160:163],160
v_lshlrev_b32 v90,16,v71
v_and_b32 v91,0xffff0000,v71
v_lshlrev_b32 v92,16,v72
v_and_b32 v93,0xffff0000,v72
v_lshlrev_b32 v94,16,v73
v_and_b32 v95,0xffff0000,v73
v_lshlrev_b32 v96,16,v74
v_and_b32 v97,0xffff0000,v74
v_lshlrev_b32 v98,16,v75
v_and_b32 v99,0xffff0000,v75
v_lshlrev_b32 v100,16,v76
v_and_b32 v101,0xffff0000,v76
v_lshlrev_b32 v102,16,v77
v_and_b32 v103,0xffff0000,v77
v_lshlrev_b32 v104,16,v78
v_and_b32 v105,0xffff0000,v78
v_lshlrev_b32 v106,16,v79
v_and_b32 v107,0xffff0000,v79
v_lshlrev_b32 v108,16,v80
v_and_b32 v109,0xffff0000,v80
v_lshlrev_b32 v110,16,v81
v_and_b32 v111,0xffff0000,v81
v_lshlrev_b32 v112,16,v82
v_and_b32 v113,0xffff0000,v82
v_lshlrev_b32 v114,16,v83
v_and_b32 v115,0xffff0000,v83
v_add_f32_dpp v44,v36,v36 row_ror:8 row_mask:0xf bank_mask:0xf
v_cndmask_b32 v45,v40,v41,s[70:71]
v_cndmask_b32 v46,v42,v43,s[70:71]
ds_write_b32 v2,v44 offset:0
v_cndmask_b32 v45,v45,v46,s[72:73]
ds_write_b32 v0,v45 offset:0
L 0
s_barrier
R 52,3,0
R 56,3,16
R 60,3,32
L 0
v_pk_add_f32 v[52:53],v[52:53],v[54:55]
v_pk_add_f32 v[56:57],v[56:57],v[58:59]
v_pk_add_f32 v[60:61],v[60:61],v[62:63]
v_add_f32 v52,v52,v53
v_pk_add_f32 v[56:57],v[56:57],v[60:61]
v_mul_f32 v52,s68,v52
v_add_f32 v53,v56,v57
v_mul_f32 v53,s68,v53
v_fma_f32 v53,-v52,v52,v53
v_max_f32 v53,0,v53
buffer_load_dword v14,v6,s[36:39],s62 offen offset:0 sc1
v_add_f32 v53,s69,v53
v_rsq_f32 v48,v53
s_nop 0
v_mul_f32_e64 v50,-v52,v48
v_mov_b32 v49,v48
v_mov_b32 v51,v50
v_pk_fma_f32 v[52:53],v[116:117],v[48:49],v[50:51]
v_pk_fma_f32 v[54:55],v[118:119],v[48:49],v[50:51]
v_pk_fma_f32 v[56:57],v[120:121],v[48:49],v[50:51]
v_pk_fma_f32 v[58:59],v[122:123],v[48:49],v[50:51]
v_pk_fma_f32 v[116:117],v[52:53],v[84:85],v[116:117]
v_pk_fma_f32 v[118:119],v[54:55],v[86:87],v[118:119]
v_pk_fma_f32 v[120:121],v[56:57],v[88:89],v[120:121]
v_pk_fma_f32 v[122:123],v[58:59],v[90:91],v[122:123]
v_cvt_pk_bf16_f32 v148,v116,v117
v_cvt_pk_bf16_f32 v149,v118,v119
v_cvt_pk_bf16_f32 v150,v120,v121
v_cvt_pk_bf16_f32 v151,v122,v123
v_pk_fma_f32 v[52:53],v[124:125],v[48:49],v[50:51]
v_pk_fma_f32 v[54:55],v[126:127],v[48:49],v[50:51]
v_pk_fma_f32 v[56:57],v[128:129],v[48:49],v[50:51]
v_pk_fma_f32 v[58:59],v[130:131],v[48:49],v[50:51]
v_pk_fma_f32 v[124:125],v[52:53],v[92:93],v[124:125]
v_pk_fma_f32 v[126:127],v[54:55],v[94:95],v[126:127]
v_pk_fma_f32 v[128:129],v[56:57],v[96:97],v[128:129]
v_pk_fma_f32 v[130:131],v[58:59],v[98:99],v[130:131]
v_cvt_pk_bf16_f32 v152,v124,v125
v_cvt_pk_bf16_f32 v153,v126,v127
v_cvt_pk_bf16_f32 v154,v128,v129
v_cvt_pk_bf16_f32 v155,v130,v131
v_pk_fma_f32 v[52:53],v[132:133],v[48:49],v[50:51]
v_pk_fma_f32 v[54:55],v[134:135],v[48:49],v[50:51]
v_pk_fma_f32 v[56:57],v[136:137],v[48:49],v[50:51]
v_pk_fma_f32 v[58:59],v[138:139],v[48:49],v[50:51]
v_pk_fma_f32 v[132:133],v[52:53],v[100:101],v[132:133]
v_pk_fma_f32 v[134:135],v[54:55],v[102:103],v[134:135]
v_pk_fma_f32 v[136:137],v[56:57],v[104:105],v[136:137]
v_pk_fma_f32 v[138:139],v[58:59],v[106:107],v[138:139]
v_cvt_pk_bf16_f32 v156,v132,v133
v_cvt_pk_bf16_f32 v157,v134,v135
v_cvt_pk_bf16_f32 v158,v136,v137
v_cvt_pk_bf16_f32 v159,v138,v139
v_pk_fma_f32 v[52:53],v[140:141],v[48:49],v[50:51]
v_pk_fma_f32 v[54:55],v[142:143],v[48:49],v[50:51]
v_pk_fma_f32 v[56:57],v[144:145],v[48:49],v[50:51]
v_pk_fma_f32 v[58:59],v[146:147],v[48:49],v[50:51]
v_pk_fma_f32 v[140:141],v[52:53],v[108:109],v[140:141]
v_pk_fma_f32 v[142:143],v[54:55],v[110:111],v[142:143]
v_pk_fma_f32 v[144:145],v[56:57],v[112:113],v[144:145]
v_pk_fma_f32 v[146:147],v[58:59],v[114:115],v[146:147]
v_cvt_pk_bf16_f32 v160,v140,v141
v_cvt_pk_bf16_f32 v161,v142,v143
v_cvt_pk_bf16_f32 v162,v144,v145
v_cvt_pk_bf16_f32 v163,v146,v147
s_waitcnt vmcnt(13)
buffer_store_dword v13,v5,s[36:39],s63 offen offset:0 sc0 sc1
s_mov_b32 s63,0x7fff0000
v_mfma_f32_16x16x32_bf16 v[36:39],v[32:35],v[148:151],0
buffer_store_dwordx4 v[148:151],v1,s[36:39],s57 offen offset:0 sc0 sc1
v_mfma_f32_16x16x32_bf16 v[40:43],v[148:151],v[148:151],0
M 36,v[32:35],152
buffer_store_dwordx4 v[152:155],v1,s[36:39],s57 offen offset:2048 sc0 sc1
M 40,v[152:155],152
M 36,v[32:35],156
buffer_store_dwordx4 v[156:159],v1,s[36:39],s58 offen offset:0 sc0 sc1
M 40,v[156:159],156
M 36,v[32:35],160
buffer_store_dwordx4 v[160:163],v1,s[36:39],s58 offen offset:2048 sc0 sc1
M 40,v[160:163],160
s_nop 5
v_add_f32_dpp v44,v36,v36 row_ror:8 row_mask:0xf bank_mask:0xf
s_nop 0
v_cndmask_b32 v45,v40,v41,s[70:71]
v_cndmask_b32 v46,v42,v43,s[70:71]
ds_write_b32 v2,v44 offset:1024
v_cndmask_b32 v45,v45,v46,s[72:73]
ds_write_b32 v0,v45 offset:1024
L 0
s_barrier
R 52,3,1024
R 56,3,1040
R 60,3,1056
L 0
v_pk_add_f32 v[52:53],v[52:53],v[54:55]
v_pk_add_f32 v[56:57],v[56:57],v[58:59]
v_pk_add_f32 v[60:61],v[60:61],v[62:63]
v_add_f32 v52,v52,v53
v_pk_add_f32 v[56:57],v[56:57],v[60:61]
v_mul_f32 v52,s68,v52
v_add_f32 v53,v56,v57
v_mul_f32 v53,s68,v53
v_fma_f32 v53,-v52,v52,v53
v_max_f32 v53,0,v53
v_add_f32 v54,s69,v53
v_fma_f32 v55,s69,v54,v53
v_rsq_f32 v65,v54
v_rsq_f32 v54,v55
v_mul_f32_e64 v64,-v52,v65
v_mul_f32 v63,v55,v54
v_mul_f32 v61,s76,v54
v_mul_f32 v55,v54,v54
v_mul_f32 v62,s77,v55
v_mul_f32 v62,v62,v61
v_cndmask_b32 v60,v52,v64,s[74:75]
v_cndmask_b32 v61,v61,v65,s[74:75]
buffer_store_dwordx4 v[60:63],v4,s[36:39],s59 offen offset:0 sc0 sc1
s_mov_b32 s63,s60
s_lshr_b32 s78,s45,1
s_and_b32 s79,s45,1
s_lshl_b32 s79,s79,7
s_lshl_b32 s57,s78,15
s_add_u32 s57,s57,s79
s_add_u32 s57,s57,16777216
s_add_u32 s58,s57,4096
s_lshl_b32 s59,s45,8
s_add_u32 s59,s59,107020288
s_lshl_b32 s60,s45,9
s_add_u32 s60,s60,s66
s_add_u32 s60,s60,131072
s_add_u32 s45,s45,s46
s_lshr_b32 s78,s45,1
s_and_b32 s79,s45,1
s_lshl_b32 s79,s79,7
s_lshl_b32 s55,s78,15
s_add_u32 s55,s55,s79
s_mul_i32 s56,s45,16384
s_add_u32 s56,s56,98566144
s_lshl_b32 s61,s78,11
s_add_u32 s61,s61,110587904
s_add_u32 s78,s45,s46
s_lshr_b32 s78,s78,1
s_lshl_b32 s62,s78,11
s_add_u32 s62,s62,110587904
s_waitcnt vmcnt(6)
s_branch Lr_it1_%=
Lr_tb0_%=:
v_add_f32 v53,s69,v53
v_rsq_f32 v48,v53
s_nop 0
v_mul_f32_e64 v50,-v52,v48
v_mov_b32 v49,v48
v_mov_b32 v51,v50
v_pk_fma_f32 v[52:53],v[116:117],v[48:49],v[50:51]
v_pk_fma_f32 v[54:55],v[118:119],v[48:49],v[50:51]
v_pk_fma_f32 v[56:57],v[120:121],v[48:49],v[50:51]
v_pk_fma_f32 v[58:59],v[122:123],v[48:49],v[50:51]
s_add_u32 s78,s55,71303168
buffer_load_dwordx4 v[196:199],v1,s[36:39],s78 offen offset:0 sc1
buffer_load_dwordx4 v[200:203],v1,s[36:39],s78 offen offset:2048 sc1
s_add_u32 s78,s55,71307264
buffer_load_dwordx4 v[204:207],v1,s[36:39],s78 offen offset:0 sc1
buffer_load_dwordx4 v[208:211],v1,s[36:39],s78 offen offset:2048 sc1
v_pk_fma_f32 v[116:117],v[52:53],v[84:85],v[116:117]
v_pk_fma_f32 v[118:119],v[54:55],v[86:87],v[118:119]
v_pk_fma_f32 v[120:121],v[56:57],v[88:89],v[120:121]
v_pk_fma_f32 v[122:123],v[58:59],v[90:91],v[122:123]
v_cvt_pk_bf16_f32 v148,v116,v117
v_cvt_pk_bf16_f32 v149,v118,v119
v_cvt_pk_bf16_f32 v150,v120,v121
v_cvt_pk_bf16_f32 v151,v122,v123
v_pk_fma_f32 v[52:53],v[124:125],v[48:49],v[50:51]
v_pk_fma_f32 v[54:55],v[126:127],v[48:49],v[50:51]
v_pk_fma_f32 v[56:57],v[128:129],v[48:49],v[50:51]
v_pk_fma_f32 v[58:59],v[130:131],v[48:49],v[50:51]
s_add_u32 s78,s55,89128960
buffer_load_dwordx4 v[212:215],v1,s[36:39],s78 offen offset:0 sc1
buffer_load_dwordx4 v[216:219],v1,s[36:39],s78 offen offset:2048 sc1
s_add_u32 s78,s55,89133056
buffer_load_dwordx4 v[220:223],v1,s[36:39],s78 offen offset:0 sc1
buffer_load_dwordx4 v[224:227],v1,s[36:39],s78 offen offset:2048 sc1
v_pk_fma_f32 v[124:125],v[52:53],v[92:93],v[124:125]
v_pk_fma_f32 v[126:127],v[54:55],v[94:95],v[126:127]
v_pk_fma_f32 v[128:129],v[56:57],v[96:97],v[128:129]
v_pk_fma_f32 v[130:131],v[58:59],v[98:99],v[130:131]
v_cvt_pk_bf16_f32 v152,v124,v125
v_cvt_pk_bf16_f32 v153,v126,v127
v_cvt_pk_bf16_f32 v154,v128,v129
v_cvt_pk_bf16_f32 v155,v130,v131
v_pk_fma_f32 v[52:53],v[132:133],v[48:49],v[50:51]
v_pk_fma_f32 v[54:55],v[134:135],v[48:49],v[50:51]
v_pk_fma_f32 v[56:57],v[136:137],v[48:49],v[50:51]
v_pk_fma_f32 v[58:59],v[138:139],v[48:49],v[50:51]
s_add_u32 m0,s65,16384
s_nop 0
D 9,56,0
D 9,56,1024
D 9,56,2048
D 9,56,3072
v_pk_fma_f32 v[132:133],v[52:53],v[100:101],v[132:133]
v_pk_fma_f32 v[134:135],v[54:55],v[102:103],v[134:135]
v_pk_fma_f32 v[136:137],v[56:57],v[104:105],v[136:137]
v_pk_fma_f32 v[138:139],v[58:59],v[106:107],v[138:139]
v_cvt_pk_bf16_f32 v156,v132,v133
v_cvt_pk_bf16_f32 v157,v134,v135
v_cvt_pk_bf16_f32 v158,v136,v137
v_cvt_pk_bf16_f32 v159,v138,v139
v_pk_fma_f32 v[52:53],v[140:141],v[48:49],v[50:51]
v_pk_fma_f32 v[54:55],v[142:143],v[48:49],v[50:51]
v_pk_fma_f32 v[56:57],v[144:145],v[48:49],v[50:51]
v_pk_fma_f32 v[58:59],v[146:147],v[48:49],v[50:51]
v_pk_fma_f32 v[140:141],v[52:53],v[108:109],v[140:141]
v_pk_fma_f32 v[142:143],v[54:55],v[110:111],v[142:143]
v_pk_fma_f32 v[144:145],v[56:57],v[112:113],v[144:145]
v_pk_fma_f32 v[146:147],v[58:59],v[114:115],v[146:147]
v_cvt_pk_bf16_f32 v160,v140,v141
v_cvt_pk_bf16_f32 v161,v142,v143
v_cvt_pk_bf16_f32 v162,v144,v145
v_cvt_pk_bf16_f32 v163,v146,v147
v_mfma_f32_16x16x32_bf16 v[36:39],v[32:35],v[148:151],0
buffer_load_dword v14,v6,s[36:39],s62 offen offset:0 sc1
buffer_store_dwordx4 v[148:151],v1,s[36:39],s57 offen offset:0 sc0 sc1
v_mfma_f32_16x16x32_bf16 v[40:43],v[148:151],v[148:151],0
M 36,v[32:35],152
buffer_store_dwordx4 v[152:155],v1,s[36:39],s57 offen offset:2048 sc0 sc1
M 40,v[152:155],152
M 36,v[32:35],156
buffer_store_dwordx4 v[156:159],v1,s[36:39],s58 offen offset:0 sc0 sc1
M 40,v[156:159],156
M 36,v[32:35],160
buffer_store_dwordx4 v[160:163],v1,s[36:39],s58 offen offset:2048 sc0 sc1
M 40,v[160:163],160
s_nop 5
v_add_f32_dpp v44,v36,v36 row_ror:8 row_mask:0xf bank_mask:0xf
s_nop 0
v_cndmask_b32 v45,v40,v41,s[70:71]
v_cndmask_b32 v46,v42,v43,s[70:71]
ds_write_b32 v2,v44 offset:1024
v_cndmask_b32 v45,v45,v46,s[72:73]
ds_write_b32 v0,v45 offset:1024
L 0
s_barrier
R 52,3,1024
R 56,3,1040
R 60,3,1056
L 0
v_pk_add_f32 v[52:53],v[52:53],v[54:55]
v_pk_add_f32 v[56:57],v[56:57],v[58:59]
v_pk_add_f32 v[60:61],v[60:61],v[62:63]
v_add_f32 v52,v52,v53
v_pk_add_f32 v[56:57],v[56:57],v[60:61]
v_mul_f32 v52,s68,v52
v_add_f32 v53,v56,v57
v_mul_f32 v53,s68,v53
v_fma_f32 v53,-v52,v52,v53
v_max_f32 v53,0,v53
v_add_f32 v54,s69,v53
v_fma_f32 v55,s69,v54,v53
v_rsq_f32 v65,v54
v_rsq_f32 v54,v55
v_mul_f32_e64 v64,-v52,v65
v_mul_f32 v63,v55,v54
v_mul_f32 v61,s76,v54
v_mul_f32 v55,v54,v54
v_mul_f32 v62,s77,v55
v_mul_f32 v62,v62,v61
v_cndmask_b32 v60,v52,v64,s[74:75]
v_cndmask_b32 v61,v61,v65,s[74:75]
buffer_store_dwordx4 v[60:63],v4,s[36:39],s59 offen offset:0 sc0 sc1
s_mov_b32 s63,s60
s_lshr_b32 s78,s45,1
s_and_b32 s79,s45,1
s_lshl_b32 s79,s79,7
s_lshl_b32 s57,s78,15
s_add_u32 s57,s57,s79
s_add_u32 s57,s57,16777216
s_add_u32 s58,s57,4096
s_lshl_b32 s59,s45,8
s_add_u32 s59,s59,107020288
s_lshl_b32 s60,s45,9
s_add_u32 s60,s60,s66
s_add_u32 s60,s60,131072
s_add_u32 s45,s45,s46
s_lshr_b32 s78,s45,1
s_and_b32 s79,s45,1
s_lshl_b32 s79,s79,7
s_lshl_b32 s55,s78,15
s_add_u32 s55,s55,s79
s_mul_i32 s56,s45,16384
s_add_u32 s56,s56,98566144
s_lshl_b32 s61,s78,11
s_add_u32 s61,s61,110587904
s_add_u32 s78,s45,s46
s_lshr_b32 s78,s78,1
s_lshl_b32 s62,s78,11
s_add_u32 s62,s62,110587904
s_waitcnt vmcnt(5)
s_branch Lr_it1_%=
Lr_tc0_%=:
s_add_u32 s78,s55,71303168
buffer_load_dwordx4 v[196:199],v1,s[36:39],s78 offen offset:0 sc1
buffer_load_dwordx4 v[200:203],v1,s[36:39],s78 offen offset:2048 sc1
s_add_u32 s78,s55,71307264
buffer_load_dwordx4 v[204:207],v1,s[36:39],s78 offen offset:0 sc1
buffer_load_dwordx4 v[208:211],v1,s[36:39],s78 offen offset:2048 sc1
s_add_u32 s78,s55,89128960
buffer_load_dwordx4 v[212:215],v1,s[36:39],s78 offen offset:0 sc1
buffer_load_dwordx4 v[216:219],v1,s[36:39],s78 offen offset:2048 sc1
s_add_u32 s78,s55,89133056
buffer_load_dwordx4 v[220:223],v1,s[36:39],s78 offen offset:0 sc1
buffer_load_dwordx4 v[224:227],v1,s[36:39],s78 offen offset:2048 sc1
s_add_u32 m0,s65,16384
s_nop 0
D 9,56,0
D 9,56,1024
D 9,56,2048
D 9,56,3072
buffer_load_dword v14,v6,s[36:39],s62 offen offset:0 sc1
s_waitcnt vmcnt(13)
buffer_store_dword v13,v5,s[36:39],s63 offen offset:0 sc0 sc1
s_mov_b32 s63,0x7fff0000
s_lshr_b32 s78,s45,1
s_and_b32 s79,s45,1
s_lshl_b32 s79,s79,7
s_lshl_b32 s57,s78,15
s_add_u32 s57,s57,s79
s_add_u32 s57,s57,16777216
s_add_u32 s58,s57,4096
s_lshl_b32 s59,s45,8
s_add_u32 s59,s59,107020288
s_lshl_b32 s60,s45,9
s_add_u32 s60,s60,s66
s_add_u32 s60,s60,131072
s_add_u32 s45,s45,s46
s_lshr_b32 s78,s45,1
s_and_b32 s79,s45,1
s_lshl_b32 s79,s79,7
s_lshl_b32 s55,s78,15
s_add_u32 s55,s55,s79
s_mul_i32 s56,s45,16384
s_add_u32 s56,s56,98566144
s_lshl_b32 s61,s78,11
s_add_u32 s61,s61,110587904
s_add_u32 s78,s45,s46
s_lshr_b32 s78,s78,1
s_lshl_b32 s62,s78,11
s_add_u32 s62,s62,110587904
s_waitcnt vmcnt(1)
s_branch Lr_it1_%=
Lr_it1_%=:
T v[68:69],10,16384
T v[70:71],10,16448
T v[72:73],10,17408
T v[74:75],10,17472
T v[76:77],10,18432
T v[78:79],10,18496
T v[80:81],10,19456
T v[82:83],10,19520
s_nop 0
v_cmp_eq_u32 vcc,s53,v14
s_nop 0
s_cmp_eq_u64 vcc,-1
s_cselect_b32 s78,s45,256
s_cmp_lt_u32 s78,256
s_cbranch_scc1 Lr_ta1_%=
buffer_load_dword v14,v6,s[36:39],s61 offen offset:0 sc1
v_mfma_f32_16x16x32_bf16 v[116:119],v[26:29],v[196:199],0
L 6
v_lshlrev_b32 v84,16,v68
v_and_b32 v85,0xffff0000,v68
v_mfma_f32_16x16x32_bf16 v[120:123],v[24:27],v[196:199],0
L 4
v_lshlrev_b32 v86,16,v69
v_and_b32 v87,0xffff0000,v69
M 116,v[26:29],212
L 2
v_lshlrev_b32 v88,16,v70
v_and_b32 v89,0xffff0000,v70
M 120,v[24:27],212
L 0
v_lshlrev_b32 v90,16,v71
v_and_b32 v91,0xffff0000,v71
v_mfma_f32_16x16x32_bf16 v[124:127],v[26:29],v[200:203],0
v_lshlrev_b32 v92,16,v72
v_and_b32 v93,0xffff0000,v72
v_mfma_f32_16x16x32_bf16 v[128:131],v[24:27],v[200:203],0
v_lshlrev_b32 v94,16,v73
v_and_b32 v95,0xffff0000,v73
M 124,v[26:29],216
v_cvt_pk_bf16_f32 v148,v116,v117
v_cvt_pk_bf16_f32 v149,v118,v119
M 128,v[24:27],216
v_cvt_pk_bf16_f32 v150,v120,v121
v_cvt_pk_bf16_f32 v151,v122,v123
v_mfma_f32_16x16x32_bf16 v[132:135],v[26:29],v[204:207],0
v_lshlrev_b32 v96,16,v74
v_and_b32 v97,0xffff0000,v74
v_mfma_f32_16x16x32_bf16 v[136:139],v[24:27],v[204:207],0
v_lshlrev_b32 v98,16,v75
v_and_b32 v99,0xffff0000,v75
M 132,v[26:29],220
v_cvt_pk_bf16_f32 v152,v124,v125
v_cvt_pk_bf16_f32 v153,v126,v127
M 136,v[24:27],220
)"
R"(v_cvt_pk_bf16_f32 v154,v128,v129
v_cvt_pk_bf16_f32 v155,v130,v131
v_mfma_f32_16x16x32_bf16 v[140:143],v[26:29],v[208:211],0
v_lshlrev_b32 v100,16,v76
v_and_b32 v101,0xffff0000,v76
v_mfma_f32_16x16x32_bf16 v[144:147],v[24:27],v[208:211],0
v_lshlrev_b32 v102,16,v77
v_and_b32 v103,0xffff0000,v77
M 140,v[26:29],224
v_cvt_pk_bf16_f32 v156,v132,v133
v_cvt_pk_bf16_f32 v157,v134,v135
M 144,v[24:27],224
v_cvt_pk_bf16_f32 v158,v136,v137
v_cvt_pk_bf16_f32 v159,v138,v139
v_mfma_f32_16x16x32_bf16 v[36:39],v[32:35],v[148:151],0
v_lshlrev_b32 v104,16,v78
v_and_b32 v105,0xffff0000,v78
v_mfma_f32_16x16x32_bf16 v[40:43],v[148:151],v[148:151],0
v_lshlrev_b32 v106,16,v79
v_and_b32 v107,0xffff0000,v79
M 36,v[32:35],152
v_cvt_pk_bf16_f32 v160,v140,v141
v_cvt_pk_bf16_f32 v161,v142,v143
M 40,v[152:155],152
v_cvt_pk_bf16_f32 v162,v144,v145
v_cvt_pk_bf16_f32 v163,v146,v147
M 36,v[32:35],156
v_lshlrev_b32 v108,16,v80
v_and_b32 v109,0xffff0000,v80
M 40,v[156:159],156
v_lshlrev_b32 v110,16,v81
v_and_b32 v111,0xffff0000,v81
M 36,v[32:35],160
v_lshlrev_b32 v112,16,v82
v_and_b32 v113,0xffff0000,v82
M 40,v[160:163],160
v_lshlrev_b32 v114,16,v83
v_and_b32 v115,0xffff0000,v83
s_nop 2
v_add_f32_dpp v44,v36,v36 row_ror:8 row_mask:0xf bank_mask:0xf
s_nop 1
v_cndmask_b32 v45,v40,v41,s[70:71]
v_cndmask_b32 v46,v42,v43,s[70:71]
ds_write_b32 v2,v44 offset:0
v_cndmask_b32 v45,v45,v46,s[72:73]
ds_write_b32 v0,v45 offset:0
L 0
s_barrier
R 52,3,0
R 56,3,16
R 60,3,32
L 0
v_pk_add_f32 v[52:53],v[52:53],v[54:55]
v_pk_add_f32 v[56:57],v[56:57],v[58:59]
v_pk_add_f32 v[60:61],v[60:61],v[62:63]
v_add_f32 v52,v52,v53
v_pk_add_f32 v[56:57],v[56:57],v[60:61]
v_mul_f32 v52,s68,v52
v_add_f32 v53,v56,v57
v_mul_f32 v53,s68,v53
v_fma_f32 v53,-v52,v52,v53
v_max_f32 v53,0,v53
s_waitcnt vmcnt(1)
buffer_store_dword v13,v5,s[36:39],s63 offen offset:0 sc0 sc1
s_mov_b32 s63,0x7fff0000
s_waitcnt vmcnt(1)
v_cmp_eq_u32 vcc,s53,v14
s_nop 0
s_cmp_eq_u64 vcc,-1
s_cselect_b32 s78,s45,256
s_cmp_lt_u32 s78,256
s_cbranch_scc1 Lr_tb1_%=
buffer_load_dword v14,v6,s[36:39],s61 offen offset:0 sc1
v_add_f32 v53,s69,v53
v_rsq_f32 v48,v53
s_nop 0
v_mul_f32_e64 v50,-v52,v48
v_mov_b32 v49,v48
v_mov_b32 v51,v50
v_pk_fma_f32 v[52:53],v[116:117],v[48:49],v[50:51]
v_pk_fma_f32 v[54:55],v[118:119],v[48:49],v[50:51]
v_pk_fma_f32 v[56:57],v[120:121],v[48:49],v[50:51]
v_pk_fma_f32 v[58:59],v[122:123],v[48:49],v[50:51]
v_pk_fma_f32 v[116:117],v[52:53],v[84:85],v[116:117]
v_pk_fma_f32 v[118:119],v[54:55],v[86:87],v[118:119]
v_pk_fma_f32 v[120:121],v[56:57],v[88:89],v[120:121]
v_pk_fma_f32 v[122:123],v[58:59],v[90:91],v[122:123]
v_cvt_pk_bf16_f32 v148,v116,v117
v_cvt_pk_bf16_f32 v149,v118,v119
v_cvt_pk_bf16_f32 v150,v120,v121
v_cvt_pk_bf16_f32 v151,v122,v123
v_pk_fma_f32 v[52:53],v[124:125],v[48:49],v[50:51]
v_pk_fma_f32 v[54:55],v[126:127],v[48:49],v[50:51]
v_pk_fma_f32 v[56:57],v[128:129],v[48:49],v[50:51]
v_pk_fma_f32 v[58:59],v[130:131],v[48:49],v[50:51]
v_pk_fma_f32 v[124:125],v[52:53],v[92:93],v[124:125]
v_pk_fma_f32 v[126:127],v[54:55],v[94:95],v[126:127]
v_pk_fma_f32 v[128:129],v[56:57],v[96:97],v[128:129]
v_pk_fma_f32 v[130:131],v[58:59],v[98:99],v[130:131]
v_cvt_pk_bf16_f32 v152,v124,v125
v_cvt_pk_bf16_f32 v153,v126,v127
v_cvt_pk_bf16_f32 v154,v128,v129
v_cvt_pk_bf16_f32 v155,v130,v131
v_pk_fma_f32 v[52:53],v[132:133],v[48:49],v[50:51]
v_pk_fma_f32 v[54:55],v[134:135],v[48:49],v[50:51]
v_pk_fma_f32 v[56:57],v[136:137],v[48:49],v[50:51]
v_pk_fma_f32 v[58:59],v[138:139],v[48:49],v[50:51]
v_pk_fma_f32 v[132:133],v[52:53],v[100:101],v[132:133]
v_pk_fma_f32 v[134:135],v[54:55],v[102:103],v[134:135]
v_pk_fma_f32 v[136:137],v[56:57],v[104:105],v[136:137]
v_pk_fma_f32 v[138:139],v[58:59],v[106:107],v[138:139]
v_cvt_pk_bf16_f32 v156,v132,v133
v_cvt_pk_bf16_f32 v157,v134,v135
v_cvt_pk_bf16_f32 v158,v136,v137
v_cvt_pk_bf16_f32 v159,v138,v139
v_pk_fma_f32 v[52:53],v[140:141],v[48:49],v[50:51]
v_pk_fma_f32 v[54:55],v[142:143],v[48:49],v[50:51]
v_pk_fma_f32 v[56:57],v[144:145],v[48:49],v[50:51]
v_pk_fma_f32 v[58:59],v[146:147],v[48:49],v[50:51]
v_pk_fma_f32 v[140:141],v[52:53],v[108:109],v[140:141]
v_pk_fma_f32 v[142:143],v[54:55],v[110:111],v[142:143]
v_pk_fma_f32 v[144:145],v[56:57],v[112:113],v[144:145]
v_pk_fma_f32 v[146:147],v[58:59],v[114:115],v[146:147]
v_cvt_pk_bf16_f32 v160,v140,v141
v_cvt_pk_bf16_f32 v161,v142,v143
v_cvt_pk_bf16_f32 v162,v144,v145
v_cvt_pk_bf16_f32 v163,v146,v147
v_mfma_f32_16x16x32_bf16 v[36:39],v[32:35],v[148:151],0
buffer_store_dwordx4 v[148:151],v1,s[36:39],s57 offen offset:0 sc0 sc1
v_mfma_f32_16x16x32_bf16 v[40:43],v[148:151],v[148:151],0
M 36,v[32:35],152
buffer_store_dwordx4 v[152:155],v1,s[36:39],s57 offen offset:2048 sc0 sc1
M 40,v[152:155],152
M 36,v[32:35],156
buffer_store_dwordx4 v[156:159],v1,s[36:39],s58 offen offset:0 sc0 sc1
M 40,v[156:159],156
M 36,v[32:35],160
buffer_store_dwordx4 v[160:163],v1,s[36:39],s58 offen offset:2048 sc0 sc1
M 40,v[160:163],160
s_nop 5
v_add_f32_dpp v44,v36,v36 row_ror:8 row_mask:0xf bank_mask:0xf
s_nop 0
v_cndmask_b32 v45,v40,v41,s[70:71]
v_cndmask_b32 v46,v42,v43,s[70:71]
ds_write_b32 v2,v44 offset:1024
v_cndmask_b32 v45,v45,v46,s[72:73]
ds_write_b32 v0,v45 offset:1024
L 0
s_barrier
R 52,3,1024
R 56,3,1040
R 60,3,1056
L 0
v_pk_add_f32 v[52:53],v[52:53],v[54:55]
v_pk_add_f32 v[56:57],v[56:57],v[58:59]
v_pk_add_f32 v[60:61],v[60:61],v[62:63]
v_add_f32 v52,v52,v53
v_pk_add_f32 v[56:57],v[56:57],v[60:61]
v_mul_f32 v52,s68,v52
v_add_f32 v53,v56,v57
v_mul_f32 v53,s68,v53
v_fma_f32 v53,-v52,v52,v53
v_max_f32 v53,0,v53
v_add_f32 v54,s69,v53
v_fma_f32 v55,s69,v54,v53
v_rsq_f32 v65,v54
v_rsq_f32 v54,v55
v_mul_f32_e64 v64,-v52,v65
v_mul_f32 v63,v55,v54
v_mul_f32 v61,s76,v54
v_mul_f32 v55,v54,v54
v_mul_f32 v62,s77,v55
v_mul_f32 v62,v62,v61
v_cndmask_b32 v60,v52,v64,s[74:75]
v_cndmask_b32 v61,v61,v65,s[74:75]
buffer_store_dwordx4 v[60:63],v4,s[36:39],s59 offen offset:0 sc0 sc1
s_mov_b32 s63,s60
s_waitcnt vmcnt(5)
v_cmp_eq_u32 vcc,s53,v14
s_nop 0
s_cmp_eq_u64 vcc,-1
s_cselect_b32 s78,s45,256
s_cmp_lt_u32 s78,256
s_cbranch_scc1 Lr_tc1_%=
s_waitcnt vmcnt(0)
buffer_store_dword v13,v5,s[36:39],s63 offen offset:0 sc0 sc1
s_mov_b32 s63,0x7fff0000
s_cmp_ge_u32 s45,256
s_cbranch_scc1 Lr_done_%=
Lr_bp1_%=:
buffer_load_dword v14,v6,s[36:39],s61 offen offset:0 sc1
s_add_u32 s82,s82,1
s_waitcnt vmcnt(0)
v_cmp_eq_u32 vcc,s53,v14
s_nop 0
s_cmp_eq_u64 vcc,-1
s_cbranch_scc1 Lr_bq1_%=
s_sleep 2
s_cmp_lt_u32 s82,20000
s_cbranch_scc1 Lr_bp1_%=
Lr_bq1_%=:
s_add_u32 s78,s55,71303168
buffer_load_dwordx4 v[164:167],v1,s[36:39],s78 offen offset:0 sc1
buffer_load_dwordx4 v[168:171],v1,s[36:39],s78 offen offset:2048 sc1
s_add_u32 s78,s55,71307264
buffer_load_dwordx4 v[172:175],v1,s[36:39],s78 offen offset:0 sc1
buffer_load_dwordx4 v[176:179],v1,s[36:39],s78 offen offset:2048 sc1
s_add_u32 s78,s55,89128960
buffer_load_dwordx4 v[180:183],v1,s[36:39],s78 offen offset:0 sc1
buffer_load_dwordx4 v[184:187],v1,s[36:39],s78 offen offset:2048 sc1
s_add_u32 s78,s55,89133056
buffer_load_dwordx4 v[188:191],v1,s[36:39],s78 offen offset:0 sc1
buffer_load_dwordx4 v[192:195],v1,s[36:39],s78 offen offset:2048 sc1
s_add_u32 m0,s65,0
s_nop 0
D 9,56,0
D 9,56,1024
D 9,56,2048
D 9,56,3072
buffer_load_dword v14,v6,s[36:39],s62 offen offset:0 sc1
s_lshr_b32 s78,s45,1
s_and_b32 s79,s45,1
s_lshl_b32 s79,s79,7
s_lshl_b32 s57,s78,15
s_add_u32 s57,s57,s79
s_add_u32 s57,s57,16777216
s_add_u32 s58,s57,4096
s_lshl_b32 s59,s45,8
s_add_u32 s59,s59,107020288
s_lshl_b32 s60,s45,9
s_add_u32 s60,s60,s66
s_add_u32 s60,s60,131072
s_add_u32 s45,s45,s46
s_lshr_b32 s78,s45,1
s_and_b32 s79,s45,1
s_lshl_b32 s79,s79,7
s_lshl_b32 s55,s78,15
s_add_u32 s55,s55,s79
s_mul_i32 s56,s45,16384
s_add_u32 s56,s56,98566144
s_lshl_b32 s61,s78,11
s_add_u32 s61,s61,110587904
s_add_u32 s78,s45,s46
s_lshr_b32 s78,s78,1
s_lshl_b32 s62,s78,11
s_add_u32 s62,s62,110587904
s_waitcnt vmcnt(0)
s_branch Lr_it0_%=
Lr_ta1_%=:
v_mfma_f32_16x16x32_bf16 v[116:119],v[26:29],v[196:199],0
s_add_u32 s78,s55,71303168
buffer_load_dwordx4 v[164:167],v1,s[36:39],s78 offen offset:0 sc1
v_mfma_f32_16x16x32_bf16 v[120:123],v[24:27],v[196:199],0
L 6
buffer_load_dwordx4 v[168:171],v1,s[36:39],s78 offen offset:2048 sc1
M 116,v[26:29],212
s_add_u32 s78,s55,71307264
buffer_load_dwordx4 v[172:175],v1,s[36:39],s78 offen offset:0 sc1
M 120,v[24:27],212
L 4
buffer_load_dwordx4 v[176:179],v1,s[36:39],s78 offen offset:2048 sc1
v_mfma_f32_16x16x32_bf16 v[124:127],v[26:29],v[200:203],0
s_add_u32 s78,s55,89128960
buffer_load_dwordx4 v[180:183],v1,s[36:39],s78 offen offset:0 sc1
v_mfma_f32_16x16x32_bf16 v[128:131],v[24:27],v[200:203],0
L 2
buffer_load_dwordx4 v[184:187],v1,s[36:39],s78 offen offset:2048 sc1
M 124,v[26:29],216
s_add_u32 s78,s55,89133056
buffer_load_dwordx4 v[188:191],v1,s[36:39],s78 offen offset:0 sc1
M 128,v[24:27],216
L 0
buffer_load_dwordx4 v[192:195],v1,s[36:39],s78 offen offset:2048 sc1
v_mfma_f32_16x16x32_bf16 v[132:135],v[26:29],v[204:207],0
s_add_u32 m0,s65,0
v_cvt_pk_bf16_f32 v148,v116,v117
v_cvt_pk_bf16_f32 v149,v118,v119
v_mfma_f32_16x16x32_bf16 v[136:139],v[24:27],v[204:207],0
D 9,56,0
M 132,v[26:29],220
D 9,56,1024
M 136,v[24:27],220
D 9,56,2048
v_mfma_f32_16x16x32_bf16 v[140:143],v[26:29],v[208:211],0
D 9,56,3072
v_mfma_f32_16x16x32_bf16 v[144:147],v[24:27],v[208:211],0
v_cvt_pk_bf16_f32 v150,v120,v121
v_cvt_pk_bf16_f32 v151,v122,v123
M 140,v[26:29],224
v_cvt_pk_bf16_f32 v152,v124,v125
v_cvt_pk_bf16_f32 v153,v126,v127
M 144,v[24:27],224
v_cvt_pk_bf16_f32 v154,v128,v129
v_cvt_pk_bf16_f32 v155,v130,v131
v_mfma_f32_16x16x32_bf16 v[36:39],v[32:35],v[148:151],0
v_cvt_pk_bf16_f32 v156,v132,v133
v_cvt_pk_bf16_f32 v157,v134,v135
v_mfma_f32_16x16x32_bf16 v[40:43],v[148:151],v[148:151],0
v_cvt_pk_bf16_f32 v158,v136,v137
v_cvt_pk_bf16_f32 v159,v138,v139
M 36,v[32:35],152
v_cvt_pk_bf16_f32 v160,v140,v141
v_cvt_pk_bf16_f32 v161,v142,v143
M 40,v[152:155],152
v_cvt_pk_bf16_f32 v162,v144,v145
v_cvt_pk_bf16_f32 v163,v146,v147
M 36,v[32:35],156
v_lshlrev_b32 v84,16,v68
v_and_b32 v85,0xffff0000,v68
M 40,v[156:159],156
v_lshlrev_b32 v86,16,v69
v_and_b32 v87,0xffff0000,v69
M 36,v[32:35],160
v_lshlrev_b32 v88,16,v70
v_and_b32 v89,0xffff0000,v70
M 40,v[160:163],160
v_lshlrev_b32 v90,16,v71
v_and_b32 v91,0xffff0000,v71
v_lshlrev_b32 v92,16,v72
v_and_b32 v93,0xffff0000,v72
v_lshlrev_b32 v94,16,v73
v_and_b32 v95,0xffff0000,v73
v_lshlrev_b32 v96,16,v74
v_and_b32 v97,0xffff0000,v74
v_lshlrev_b32 v98,16,v75
v_and_b32 v99,0xffff0000,v75
v_lshlrev_b32 v100,16,v76
v_and_b32 v101,0xffff0000,v76
v_lshlrev_b32 v102,16,v77
v_and_b32 v103,0xffff0000,v77
v_lshlrev_b32 v104,16,v78
v_and_b32 v105,0xffff0000,v78
v_lshlrev_b32 v106,16,v79
v_and_b32 v107,0xffff0000,v79
v_lshlrev_b32 v108,16,v80
v_and_b32 v109,0xffff0000,v80
v_lshlrev_b32 v110,16,v81
v_and_b32 v111,0xffff0000,v81
v_lshlrev_b32 v112,16,v82
v_and_b32 v113,0xffff0000,v82
v_lshlrev_b32 v114,16,v83
v_and_b32 v115,0xffff0000,v83
v_add_f32_dpp v44,v36,v36 row_ror:8 row_mask:0xf bank_mask:0xf
v_cndmask_b32 v45,v40,v41,s[70:71]
v_cndmask_b32 v46,v42,v43,s[70:71]
ds_write_b32 v2,v44 offset:0
v_cndmask_b32 v45,v45,v46,s[72:73]
ds_write_b32 v0,v45 offset:0
L 0
s_barrier
R 52,3,0
R 56,3,16
R 60,3,32
L 0
v_pk_add_f32 v[52:53],v[52:53],v[54:55]
v_pk_add_f32 v[56:57],v[56:57],v[58:59]
v_pk_add_f32 v[60:61],v[60:61],v[62:63]
v_add_f32 v52,v52,v53
v_pk_add_f32 v[56:57],v[56:57],v[60:61]
v_mul_f32 v52,s68,v52
v_add_f32 v53,v56,v57
v_mul_f32 v53,s68,v53
v_fma_f32 v53,-v52,v52,v53
v_max_f32 v53,0,v53
buffer_load_dword v14,v6,s[36:39],s62 offen offset:0 sc1
v_add_f32 v53,s69,v53
v_rsq_f32 v48,v53
s_nop 0
v_mul_f32_e64 v50,-v52,v48
v_mov_b32 v49,v48
v_mov_b32 v51,v50
v_pk_fma_f32 v[52:53],v[116:117],v[48:49],v[50:51]
v_pk_fma_f32 v[54:55],v[118:119],v[48:49],v[50:51]
v_pk_fma_f32 v[56:57],v[120:121],v[48:49],v[50:51]
v_pk_fma_f32 v[58:59],v[122:123],v[48:49],v[50:51]
v_pk_fma_f32 v[116:117],v[52:53],v[84:85],v[116:117]
v_pk_fma_f32 v[118:119],v[54:55],v[86:87],v[118:119]
v_pk_fma_f32 v[120:121],v[56:57],v[88:89],v[120:121]
v_pk_fma_f32 v[122:123],v[58:59],v[90:91],v[122:123]
v_cvt_pk_bf16_f32 v148,v116,v117
v_cvt_pk_bf16_f32 v149,v118,v119
v_cvt_pk_bf16_f32 v150,v120,v121
v_cvt_pk_bf16_f32 v151,v122,v123
v_pk_fma_f32 v[52:53],v[124:125],v[48:49],v[50:51]
v_pk_fma_f32 v[54:55],v[126:127],v[48:49],v[50:51]
v_pk_fma_f32 v[56:57],v[128:129],v[48:49],v[50:51]
v_pk_fma_f32 v[58:59],v[130:131],v[48:49],v[50:51]
v_pk_fma_f32 v[124:125],v[52:53],v[92:93],v[124:125]
v_pk_fma_f32 v[126:127],v[54:55],v[94:95],v[126:127]
v_pk_fma_f32 v[128:129],v[56:57],v[96:97],v[128:129]
v_pk_fma_f32 v[130:131],v[58:59],v[98:99],v[130:131]
v_cvt_pk_bf16_f32 v152,v124,v125
v_cvt_pk_bf16_f32 v153,v126,v127
v_cvt_pk_bf16_f32 v154,v128,v129
v_cvt_pk_bf16_f32 v155,v130,v131
v_pk_fma_f32 v[52:53],v[132:133],v[48:49],v[50:51]
v_pk_fma_f32 v[54:55],v[134:135],v[48:49],v[50:51]
v_pk_fma_f32 v[56:57],v[136:137],v[48:49],v[50:51]
v_pk_fma_f32 v[58:59],v[138:139],v[48:49],v[50:51]
v_pk_fma_f32 v[132:133],v[52:53],v[100:101],v[132:133]
v_pk_fma_f32 v[134:135],v[54:55],v[102:103],v[134:135]
v_pk_fma_f32 v[136:137],v[56:57],v[104:105],v[136:137]
v_pk_fma_f32 v[138:139],v[58:59],v[106:107],v[138:139]
v_cvt_pk_bf16_f32 v156,v132,v133
v_cvt_pk_bf16_f32 v157,v134,v135
v_cvt_pk_bf16_f32 v158,v136,v137
v_cvt_pk_bf16_f32 v159,v138,v139
v_pk_fma_f32 v[52:53],v[140:141],v[48:49],v[50:51]
v_pk_fma_f32 v[54:55],v[142:143],v[48:49],v[50:51]
v_pk_fma_f32 v[56:57],v[144:145],v[48:49],v[50:51]
v_pk_fma_f32 v[58:59],v[146:147],v[48:49],v[50:51]
v_pk_fma_f32 v[140:141],v[52:53],v[108:109],v[140:141]
v_pk_fma_f32 v[142:143],v[54:55],v[110:111],v[142:143]
v_pk_fma_f32 v[144:145],v[56:57],v[112:113],v[144:145]
v_pk_fma_f32 v[146:147],v[58:59],v[114:115],v[146:147]
v_cvt_pk_bf16_f32 v160,v140,v141
v_cvt_pk_bf16_f32 v161,v142,v143
v_cvt_pk_bf16_f32 v162,v144,v145
v_cvt_pk_bf16_f32 v163,v146,v147
s_waitcnt vmcnt(13)
buffer_store_dword v13,v5,s[36:39],s63 offen offset:0 sc0 sc1
s_mov_b32 s63,0x7fff0000
v_mfma_f32_16x16x32_bf16 v[36:39],v[32:35],v[148:151],0
buffer_store_dwordx4 v[148:151],v1,s[36:39],s57 offen offset:0 sc0 sc1
v_mfma_f32_16x16x32_bf16 v[40:43],v[148:151],v[148:151],0
M 36,v[32:35],152
buffer_store_dwordx4 v[152:155],v1,s[36:39],s57 offen offset:2048 sc0 sc1
M 40,v[152:155],152
M 36,v[32:35],156
buffer_store_dwordx4 v[156:159],v1,s[36:39],s58 offen offset:0 sc0 sc1
M 40,v[156:159],156
M 36,v[32:35],160
buffer_store_dwordx4 v[160:163],v1,s[36:39],s58 offen offset:2048 sc0 sc1
M 40,v[160:163],160
s_nop 5
v_add_f32_dpp v44,v36,v36 row_ror:8 row_mask:0xf bank_mask:0xf
s_nop 0
v_cndmask_b32 v45,v40,v41,s[70:71]
v_cndmask_b32 v46,v42,v43,s[70:71]
ds_write_b32 v2,v44 offset:1024
v_cndmask_b32 v45,v45,v46,s[72:73]
ds_write_b32 v0,v45 offset:1024
L 0
s_barrier
R 52,3,1024
R 56,3,1040
R 60,3,1056
L 0
v_pk_add_f32 v[52:53],v[52:53],v[54:55]
v_pk_add_f32 v[56:57],v[56:57],v[58:59]
v_pk_add_f32 v[60:61],v[60:61],v[62:63]
v_add_f32 v52,v52,v53
v_pk_add_f32 v[56:57],v[56:57],v[60:61]
v_mul_f32 v52,s68,v52
v_add_f32 v53,v56,v57
v_mul_f32 v53,s68,v53
v_fma_f32 v53,-v52,v52,v53
v_max_f32 v53,0,v53
v_add_f32 v54,s69,v53
v_fma_f32 v55,s69,v54,v53
v_rsq_f32 v65,v54
v_rsq_f32 v54,v55
v_mul_f32_e64 v64,-v52,v65
v_mul_f32 v63,v55,v54
v_mul_f32 v61,s76,v54
v_mul_f32 v55,v54,v54
v_mul_f32 v62,s77,v55
v_mul_f32 v62,v62,v61
v_cndmask_b32 v60,v52,v64,s[74:75]
v_cndmask_b32 v61,v61,v65,s[74:75]
buffer_store_dwordx4 v[60:63],v4,s[36:39],s59 offen offset:0 sc0 sc1
s_mov_b32 s63,s60
s_lshr_b32 s78,s45,1
s_and_b32 s79,s45,1
s_lshl_b32 s79,s79,7
s_lshl_b32 s57,s78,15
s_add_u32 s57,s57,s79
s_add_u32 s57,s57,16777216
s_add_u32 s58,s57,4096
s_lshl_b32 s59,s45,8
s_add_u32 s59,s59,107020288
s_lshl_b32 s60,s45,9
s_add_u32 s60,s60,s66
s_add_u32 s60,s60,131072
s_add_u32 s45,s45,s46
s_lshr_b32 s78,s45,1
s_and_b32 s79,s45,1
s_lshl_b32 s79,s79,7
s_lshl_b32 s55,s78,15
s_add_u32 s55,s55,s79
s_mul_i32 s56,s45,16384
s_add_u32 s56,s56,98566144
s_lshl_b32 s61,s78,11
s_add_u32 s61,s61,110587904
s_add_u32 s78,s45,s46
s_lshr_b32 s78,s78,1
s_lshl_b32 s62,s78,11
s_add_u32 s62,s62,110587904
s_waitcnt vmcnt(6)
s_branch Lr_it0_%=
)"
R"(Lr_tb1_%=:
v_add_f32 v53,s69,v53
v_rsq_f32 v48,v53
s_nop 0
v_mul_f32_e64 v50,-v52,v48
v_mov_b32 v49,v48
v_mov_b32 v51,v50
v_pk_fma_f32 v[52:53],v[116:117],v[48:49],v[50:51]
v_pk_fma_f32 v[54:55],v[118:119],v[48:49],v[50:51]
v_pk_fma_f32 v[56:57],v[120:121],v[48:49],v[50:51]
v_pk_fma_f32 v[58:59],v[122:123],v[48:49],v[50:51]
s_add_u32 s78,s55,71303168
buffer_load_dwordx4 v[164:167],v1,s[36:39],s78 offen offset:0 sc1
buffer_load_dwordx4 v[168:171],v1,s[36:39],s78 offen offset:2048 sc1
s_add_u32 s78,s55,71307264
buffer_load_dwordx4 v[172:175],v1,s[36:39],s78 offen offset:0 sc1
buffer_load_dwordx4 v[176:179],v1,s[36:39],s78 offen offset:2048 sc1
v_pk_fma_f32 v[116:117],v[52:53],v[84:85],v[116:117]
v_pk_fma_f32 v[118:119],v[54:55],v[86:87],v[118:119]
v_pk_fma_f32 v[120:121],v[56:57],v[88:89],v[120:121]
v_pk_fma_f32 v[122:123],v[58:59],v[90:91],v[122:123]
v_cvt_pk_bf16_f32 v148,v116,v117
v_cvt_pk_bf16_f32 v149,v118,v119
v_cvt_pk_bf16_f32 v150,v120,v121
v_cvt_pk_bf16_f32 v151,v122,v123
v_pk_fma_f32 v[52:53],v[124:125],v[48:49],v[50:51]
v_pk_fma_f32 v[54:55],v[126:127],v[48:49],v[50:51]
v_pk_fma_f32 v[56:57],v[128:129],v[48:49],v[50:51]
v_pk_fma_f32 v[58:59],v[130:131],v[48:49],v[50:51]
s_add_u32 s78,s55,89128960
buffer_load_dwordx4 v[180:183],v1,s[36:39],s78 offen offset:0 sc1
buffer_load_dwordx4 v[184:187],v1,s[36:39],s78 offen offset:2048 sc1
s_add_u32 s78,s55,89133056
buffer_load_dwordx4 v[188:191],v1,s[36:39],s78 offen offset:0 sc1
buffer_load_dwordx4 v[192:195],v1,s[36:39],s78 offen offset:2048 sc1
v_pk_fma_f32 v[124:125],v[52:53],v[92:93],v[124:125]
v_pk_fma_f32 v[126:127],v[54:55],v[94:95],v[126:127]
v_pk_fma_f32 v[128:129],v[56:57],v[96:97],v[128:129]
v_pk_fma_f32 v[130:131],v[58:59],v[98:99],v[130:131]
v_cvt_pk_bf16_f32 v152,v124,v125
v_cvt_pk_bf16_f32 v153,v126,v127
v_cvt_pk_bf16_f32 v154,v128,v129
v_cvt_pk_bf16_f32 v155,v130,v131
v_pk_fma_f32 v[52:53],v[132:133],v[48:49],v[50:51]
v_pk_fma_f32 v[54:55],v[134:135],v[48:49],v[50:51]
v_pk_fma_f32 v[56:57],v[136:137],v[48:49],v[50:51]
v_pk_fma_f32 v[58:59],v[138:139],v[48:49],v[50:51]
s_add_u32 m0,s65,0
s_nop 0
D 9,56,0
D 9,56,1024
D 9,56,2048
D 9,56,3072
v_pk_fma_f32 v[132:133],v[52:53],v[100:101],v[132:133]
v_pk_fma_f32 v[134:135],v[54:55],v[102:103],v[134:135]
v_pk_fma_f32 v[136:137],v[56:57],v[104:105],v[136:137]
v_pk_fma_f32 v[138:139],v[58:59],v[106:107],v[138:139]
v_cvt_pk_bf16_f32 v156,v132,v133
v_cvt_pk_bf16_f32 v157,v134,v135
v_cvt_pk_bf16_f32 v158,v136,v137
v_cvt_pk_bf16_f32 v159,v138,v139
v_pk_fma_f32 v[52:53],v[140:141],v[48:49],v[50:51]
v_pk_fma_f32 v[54:55],v[142:143],v[48:49],v[50:51]
v_pk_fma_f32 v[56:57],v[144:145],v[48:49],v[50:51]
v_pk_fma_f32 v[58:59],v[146:147],v[48:49],v[50:51]
v_pk_fma_f32 v[140:141],v[52:53],v[108:109],v[140:141]
v_pk_fma_f32 v[142:143],v[54:55],v[110:111],v[142:143]
v_pk_fma_f32 v[144:145],v[56:57],v[112:113],v[144:145]
v_pk_fma_f32 v[146:147],v[58:59],v[114:115],v[146:147]
v_cvt_pk_bf16_f32 v160,v140,v141
v_cvt_pk_bf16_f32 v161,v142,v143
v_cvt_pk_bf16_f32 v162,v144,v145
v_cvt_pk_bf16_f32 v163,v146,v147
v_mfma_f32_16x16x32_bf16 v[36:39],v[32:35],v[148:151],0
buffer_load_dword v14,v6,s[36:39],s62 offen offset:0 sc1
buffer_store_dwordx4 v[148:151],v1,s[36:39],s57 offen offset:0 sc0 sc1
v_mfma_f32_16x16x32_bf16 v[40:43],v[148:151],v[148:151],0
M 36,v[32:35],152
buffer_store_dwordx4 v[152:155],v1,s[36:39],s57 offen offset:2048 sc0 sc1
M 40,v[152:155],152
M 36,v[32:35],156
buffer_store_dwordx4 v[156:159],v1,s[36:39],s58 offen offset:0 sc0 sc1
M 40,v[156:159],156
M 36,v[32:35],160
buffer_store_dwordx4 v[160:163],v1,s[36:39],s58 offen offset:2048 sc0 sc1
M 40,v[160:163],160
s_nop 5
v_add_f32_dpp v44,v36,v36 row_ror:8 row_mask:0xf bank_mask:0xf
s_nop 0
v_cndmask_b32 v45,v40,v41,s[70:71]
v_cndmask_b32 v46,v42,v43,s[70:71]
ds_write_b32 v2,v44 offset:1024
v_cndmask_b32 v45,v45,v46,s[72:73]
ds_write_b32 v0,v45 offset:1024
L 0
s_barrier
R 52,3,1024
R 56,3,1040
R 60,3,1056
L 0
v_pk_add_f32 v[52:53],v[52:53],v[54:55]
v_pk_add_f32 v[56:57],v[56:57],v[58:59]
v_pk_add_f32 v[60:61],v[60:61],v[62:63]
v_add_f32 v52,v52,v53
v_pk_add_f32 v[56:57],v[56:57],v[60:61]
v_mul_f32 v52,s68,v52
v_add_f32 v53,v56,v57
v_mul_f32 v53,s68,v53
v_fma_f32 v53,-v52,v52,v53
v_max_f32 v53,0,v53
v_add_f32 v54,s69,v53
v_fma_f32 v55,s69,v54,v53
v_rsq_f32 v65,v54
v_rsq_f32 v54,v55
v_mul_f32_e64 v64,-v52,v65
v_mul_f32 v63,v55,v54
v_mul_f32 v61,s76,v54
v_mul_f32 v55,v54,v54
v_mul_f32 v62,s77,v55
v_mul_f32 v62,v62,v61
v_cndmask_b32 v60,v52,v64,s[74:75]
v_cndmask_b32 v61,v61,v65,s[74:75]
buffer_store_dwordx4 v[60:63],v4,s[36:39],s59 offen offset:0 sc0 sc1
s_mov_b32 s63,s60
s_lshr_b32 s78,s45,1
s_and_b32 s79,s45,1
s_lshl_b32 s79,s79,7
s_lshl_b32 s57,s78,15
s_add_u32 s57,s57,s79
s_add_u32 s57,s57,16777216
s_add_u32 s58,s57,4096
s_lshl_b32 s59,s45,8
s_add_u32 s59,s59,107020288
s_lshl_b32 s60,s45,9
s_add_u32 s60,s60,s66
s_add_u32 s60,s60,131072
s_add_u32 s45,s45,s46
s_lshr_b32 s78,s45,1
s_and_b32 s79,s45,1
s_lshl_b32 s79,s79,7
s_lshl_b32 s55,s78,15
s_add_u32 s55,s55,s79
s_mul_i32 s56,s45,16384
s_add_u32 s56,s56,98566144
s_lshl_b32 s61,s78,11
s_add_u32 s61,s61,110587904
s_add_u32 s78,s45,s46
s_lshr_b32 s78,s78,1
s_lshl_b32 s62,s78,11
s_add_u32 s62,s62,110587904
s_waitcnt vmcnt(5)
s_branch Lr_it0_%=
Lr_tc1_%=:
s_add_u32 s78,s55,71303168
buffer_load_dwordx4 v[164:167],v1,s[36:39],s78 offen offset:0 sc1
buffer_load_dwordx4 v[168:171],v1,s[36:39],s78 offen offset:2048 sc1
s_add_u32 s78,s55,71307264
buffer_load_dwordx4 v[172:175],v1,s[36:39],s78 offen offset:0 sc1
buffer_load_dwordx4 v[176:179],v1,s[36:39],s78 offen offset:2048 sc1
s_add_u32 s78,s55,89128960
buffer_load_dwordx4 v[180:183],v1,s[36:39],s78 offen offset:0 sc1
buffer_load_dwordx4 v[184:187],v1,s[36:39],s78 offen offset:2048 sc1
s_add_u32 s78,s55,89133056
buffer_load_dwordx4 v[188:191],v1,s[36:39],s78 offen offset:0 sc1
buffer_load_dwordx4 v[192:195],v1,s[36:39],s78 offen offset:2048 sc1
s_add_u32 m0,s65,0
s_nop 0
D 9,56,0
D 9,56,1024
D 9,56,2048
D 9,56,3072
buffer_load_dword v14,v6,s[36:39],s62 offen offset:0 sc1
s_waitcnt vmcnt(13)
buffer_store_dword v13,v5,s[36:39],s63 offen offset:0 sc0 sc1
s_mov_b32 s63,0x7fff0000
s_lshr_b32 s78,s45,1
s_and_b32 s79,s45,1
s_lshl_b32 s79,s79,7
s_lshl_b32 s57,s78,15
s_add_u32 s57,s57,s79
s_add_u32 s57,s57,16777216
s_add_u32 s58,s57,4096
s_lshl_b32 s59,s45,8
s_add_u32 s59,s59,107020288
s_lshl_b32 s60,s45,9
s_add_u32 s60,s60,s66
s_add_u32 s60,s60,131072
s_add_u32 s45,s45,s46
s_lshr_b32 s78,s45,1
s_and_b32 s79,s45,1
s_lshl_b32 s79,s79,7
s_lshl_b32 s55,s78,15
s_add_u32 s55,s55,s79
s_mul_i32 s56,s45,16384
s_add_u32 s56,s56,98566144
s_lshl_b32 s61,s78,11
s_add_u32 s61,s61,110587904
s_add_u32 s78,s45,s46
s_lshr_b32 s78,s78,1
s_lshl_b32 s62,s78,11
s_add_u32 s62,s62,110587904
s_waitcnt vmcnt(1)
s_branch Lr_it0_%=
Lr_done_%=:
s_waitcnt vmcnt(0) lgkmcnt(0)
s_barrier
Lcode_r_e:
.purgem M
.purgem R
.purgem T
.purgem D
.purgem L
)"
  :: "s"(ws), "s"(sWave), "s"(sG0), "s"(sStep), "s"(sOutOff), "s"(sAuxOff), "s"(sBuf), "s"(sRk), "s"(sExp), "v"(vRP), "v"(vRF), "v"(vRwr), "v"(vRrd), "v"(vRS), "v"(vFlag), "v"(vL4), "v"(vLY), "v"(vQ), "v"(vY0), "v"(vTr), "v"(vSa), "v"(vSb), "s"(q) : "v0", "v1", "v2", "v3", "v4", "v5", "v6", "v7", "v8", "v9", "v10", "v11", "v12", "v13", "v14", "v15", "v16", "v17", "v18", "v19", "v20", "v21", "v22", "v23", "v24", "v25", "v26", "v27", "v28", "v29", "v30", "v31", "v32", "v33", "v34", "v35", "v36", "v37", "v38", "v39", "v40", "v41", "v42", "v43", "v44", "v45", "v46", "v47", "v48", "v49", "v50", "v51", "v52", "v53", "v54", "v55", "v56", "v57", "v58", "v59", "v60", "v61", "v62", "v63", "v64", "v65", "v66", "v67", "v68", "v69", "v70", "v71", "v72", "v73", "v74", "v75", "v76", "v77", "v78", "v79", "v80", "v81", "v82", "v83", "v84", "v85", "v86", "v87", "v88", "v89", "v90", "v91", "v92", "v93", "v94", "v95", "v96", "v97", "v98", "v99", "v100", "v101", "v102", "v103", "v104", "v105", "v106", "v107", "v108", "v109", "v110", "v111", "v112", "v113", "v114", "v115", "v116", "v117", "v118", "v119", "v120", "v121", "v122", "v123", "v124", "v125", "v126", "v127", "v128", "v129", "v130", "v131", "v132", "v133", "v134", "v135", "v136", "v137", "v138", "v139", "v140", "v141", "v142", "v143", "v144", "v145", "v146", "v147", "v148", "v149", "v150", "v151", "v152", "v153", "v154", "v155", "v156", "v157", "v158", "v159", "v160", "v161", "v162", "v163", "v164", "v165", "v166", "v167", "v168", "v169", "v170", "v171", "v172", "v173", "v174", "v175", "v176", "v177", "v178", "v179", "v180", "v181", "v182", "v183", "v184", "v185", "v186", "v187", "v188", "v189", "v190", "v191", "v192", "v193", "v194", "v195", "v196", "v197", "v198", "v199", "v200", "v201", "v202", "v203", "v204", "v205", "v206", "v207", "v208", "v209", "v210", "v211", "v212", "v213", "v214", "v215", "v216", "v217", "v218", "v219", "v220", "v221", "v222", "v223", "v224", "v225", "v226", "v227", "v228", "v229", "v230", "v231", "s36", "s37", "s38", "s39", "s40", "s41", "s42", "s43", "s44", "s45", "s46", "s47", "s48", "s49", "s50", "s51", "s52", "s53", "s54", "s55", "s56", "s57", "s58", "s59", "s60", "s61", "s62", "s63", "s64", "s65", "s66", "s67", "s68", "s69", "s70", "s71", "s72", "s73", "s74", "s75", "s76", "s77", "s78", "s79", "s80", "s81", "s82", "s83", "s84", "s85", "s86", "s87", "s88", "s89", "s90", "s91", "s92", "s93", "s94", "s95", "s96", "s97", "vcc", "scc", "memory");
  }
  CTS(9);
#define COMMON(NF, FOFF, FSTR, SNAP, LDW, NL, TOFF)                                                                                            \
  LANE_ID(ln);                                                                                                                       \
  const int l15 = ln & 15, lg = ln >> 4, lq = l15 >> 2, lp = ln & 3, rr = 8 * lg + lq, fj = ln & 31;                                 \
  const unsigned vL16 = ln * 16, vFlag = (ln < 32 && wave == 0) ? ln * 4 : OOB;                                                      \
  const unsigned vSnapOff = (wave == 0 && fj < (NF)) ? (FOFF) + (ln >> 5) * sStep * (FSTR) : OOB;                                    \
  const unsigned vSnapRd = (SNAP) + (fj < (NF) ? ln : ln & 32) * 4;                                                                  \
  const unsigned vWd0 = (ln >> 3) * (LDW) + ((ln & 7) ^ swzB(ln >> 3)) * 16, vWd1 = ((ln >> 3) + 8) * (LDW) + ((ln & 7) ^ swzB(8 + (ln >> 3))) * 16; \
  const unsigned vWr0 = (wave >> 1) * 8192 + rr * 128 + (((wave & 1) * 4 + lp) ^ swzB(rr)) * 16, vWr1 = vWr0 + 8;                    \
  const unsigned vWr2 = 16384 + rr * 128 + ((2 * wave + (lp >> 1)) ^ swzB(rr)) * 16 + (lp & 1) * 8;                                  \
  const unsigned sWave = wave, sRk = kind < 3 ? 0x7fff0000u : OFF_FY + (256 + (kind == 4 ? j4 : 80 + j3)) * 128, sG0 = rep, sDbg = sDbg0
  if (kind == 3 || kind == 5) {
    const unsigned sStep = kind == 3 ? 5 : 4;
    COMMON(8, fj * 128, 1024, 131072, 7168, 256, 0);
    const unsigned vOutS = (lg & 1) ? OOB : (((wave & 1) * 2 + (lg >> 1)) * 16 + l15) * 16 + (4 + (wave >> 1) - wave) * 1024;
    const unsigned vAux0 = (ln & 7) * 128 + (4 * wave + lg) * 8 + ((ln >> 3) & 1) * 4, vAux1 = 139264 + (l15 == 0 ? (4 * wave + lg) * 16 : 512 + ln * 16);
    const unsigned vAux2 = 139264 + l15 * 16, vAux3 = 0, vAn = 0, vAe = 0;
    const unsigned sOutOff = OFF_G + (slice * 6 + wave) * 1024, sFoutOff = OFF_FG + slice * 128, sAuxOff = 0, sWw = 16 * wave * 7168 + slice * 384, sBuf = 0;
    if (kind == 3) { asm volatile(
R"(.macro M d,w,x
v_mfma_f32_16x16x32_bf16 v[\d:\d+3], \w, v[\x:\x+3], v[\d:\d+3]
.endm
.macro R d,a,o
ds_read_b128 v[\d:\d+3], v\a offset:\o
.endm
.macro T d,a,o
ds_read_b64_tr_b16 \d, v\a offset:\o
.endm
.macro D a,s,o
buffer_load_dwordx4 v\a, s[36:39], s\s offen offset:\o sc1 lds
.endm
.macro L c
s_waitcnt lgkmcnt(\c)
.endm
Lcode_g3_b:
s_mov_b64 s[36:37],%0
s_mov_b32 s38,111243392
s_mov_b32 s39,0x00020000
s_mov_b32 s44,%1
s_mov_b32 s45,%2
s_mov_b32 s46,%3
s_mov_b32 s47,%4
s_mov_b32 s48,%5
s_mov_b32 s49,%6
s_mov_b32 s50,%7
s_mov_b32 s51,%8
s_mov_b32 s93,%9
v_mov_b32 v0,%10
v_mov_b32 v3,%11
v_mov_b32 v5,%12
v_mov_b32 v6,%13
v_mov_b32 v18,%14
v_mov_b32 v19,%15
v_mov_b32 v20,%16
v_mov_b32 v21,%17
v_mov_b32 v22,%18
v_mov_b32 v10,%19
v_mov_b32 v11,%20
v_mov_b32 v12,%21
v_mov_b32 v13,%22
v_mov_b32 v14,%23
v_mov_b32 v15,%24
v_mov_b32 v16,%25
s_mov_b64 s[40:41],%26
s_mov_b32 s42,7340032
s_mov_b32 s43,0x00020000
s_lshl_b32 s57,s44,11
s_mov_b32 s79,111243264
buffer_load_dword v4,off,s[36:39],s79 sc1
s_add_u32 s55,s50,0
s_add_u32 m0,s57,0
s_nop 0
buffer_load_dwordx4 v18,s[40:43],s55 offen offset:0  lds
s_add_u32 m0,s57,1024
s_nop 0
buffer_load_dwordx4 v19,s[40:43],s55 offen offset:0  lds
s_add_u32 m0,s57,8064
s_nop 0
buffer_load_dwordx4 v18,s[40:43],s55 offen offset:128  lds
s_add_u32 m0,s57,9088
s_nop 0
buffer_load_dwordx4 v19,s[40:43],s55 offen offset:128  lds
s_add_u32 m0,s57,16128
s_nop 0
buffer_load_dwordx4 v18,s[40:43],s55 offen offset:256  lds
s_add_u32 m0,s57,17152
s_nop 0
buffer_load_dwordx4 v19,s[40:43],s55 offen offset:256  lds
s_add_u32 s55,s50,458752
s_add_u32 m0,s57,24576
s_nop 0
buffer_load_dwordx4 v18,s[40:43],s55 offen offset:0  lds
s_add_u32 m0,s57,25600
s_nop 0
buffer_load_dwordx4 v19,s[40:43],s55 offen offset:0  lds
s_add_u32 m0,s57,32640
s_nop 0
buffer_load_dwordx4 v18,s[40:43],s55 offen offset:128  lds
s_add_u32 m0,s57,33664
s_nop 0
buffer_load_dwordx4 v19,s[40:43],s55 offen offset:128  lds
s_add_u32 m0,s57,40704
s_nop 0
buffer_load_dwordx4 v18,s[40:43],s55 offen offset:256  lds
s_add_u32 m0,s57,41728
s_nop 0
buffer_load_dwordx4 v19,s[40:43],s55 offen offset:256  lds
s_waitcnt vmcnt(12)
s_barrier
v_readfirstlane_b32 s53,v4
s_nop 3
s_add_u32 s53,s53,1
s_cmp_eq_u32 s53,0
s_cselect_b32 s53,1,s53
v_mov_b32 v4,s53
buffer_store_dword v4,v3,s[36:39],s93 offen offset:0 sc0 sc1
s_add_u32 s55,s50,917504
s_add_u32 m0,s57,49152
s_nop 0
buffer_load_dwordx4 v18,s[40:43],s55 offen offset:0  lds
s_add_u32 m0,s57,50176
s_nop 0
buffer_load_dwordx4 v19,s[40:43],s55 offen offset:0  lds
s_add_u32 m0,s57,57216
s_nop 0
buffer_load_dwordx4 v18,s[40:43],s55 offen offset:128  lds
s_add_u32 m0,s57,58240
s_nop 0
buffer_load_dwordx4 v19,s[40:43],s55 offen offset:128  lds
s_add_u32 m0,s57,65280
s_nop 0
buffer_load_dwordx4 v18,s[40:43],s55 offen offset:256  lds
s_add_u32 m0,s57,66304
s_nop 0
buffer_load_dwordx4 v19,s[40:43],s55 offen offset:256  lds
s_add_u32 s55,s50,1376256
s_add_u32 m0,s57,73728
s_nop 0
buffer_load_dwordx4 v18,s[40:43],s55 offen offset:0  lds
s_add_u32 m0,s57,74752
s_nop 0
buffer_load_dwordx4 v19,s[40:43],s55 offen offset:0  lds
s_add_u32 m0,s57,81792
s_nop 0
buffer_load_dwordx4 v18,s[40:43],s55 offen offset:128  lds
s_add_u32 m0,s57,82816
s_nop 0
buffer_load_dwordx4 v19,s[40:43],s55 offen offset:128  lds
s_add_u32 m0,s57,89856
s_nop 0
buffer_load_dwordx4 v18,s[40:43],s55 offen offset:256  lds
s_add_u32 m0,s57,90880
s_nop 0
buffer_load_dwordx4 v19,s[40:43],s55 offen offset:256  lds
s_add_u32 s55,s50,1835008
s_add_u32 m0,s57,98304
s_nop 0
buffer_load_dwordx4 v18,s[40:43],s55 offen offset:0  lds
s_add_u32 m0,s57,99328
s_nop 0
buffer_load_dwordx4 v19,s[40:43],s55 offen offset:0  lds
s_add_u32 m0,s57,106368
s_nop 0
buffer_load_dwordx4 v18,s[40:43],s55 offen offset:128  lds
s_add_u32 m0,s57,107392
s_nop 0
buffer_load_dwordx4 v19,s[40:43],s55 offen offset:128  lds
s_add_u32 m0,s57,114432
s_nop 0
buffer_load_dwordx4 v18,s[40:43],s55 offen offset:256  lds
s_add_u32 m0,s57,115456
s_nop 0
buffer_load_dwordx4 v19,s[40:43],s55 offen offset:256  lds
s_waitcnt vmcnt(25)
L 0
s_barrier
s_add_u32 s55,s50,2293760
s_add_u32 m0,s57,122880
s_nop 0
buffer_load_dwordx4 v18,s[40:43],s55 offen offset:0  lds
s_add_u32 m0,s57,123904
s_nop 0
buffer_load_dwordx4 v19,s[40:43],s55 offen offset:0  lds
s_add_u32 m0,s57,130944
s_nop 0
buffer_load_dwordx4 v18,s[40:43],s55 offen offset:128  lds
s_add_u32 m0,s57,131968
s_nop 0
buffer_load_dwordx4 v19,s[40:43],s55 offen offset:128  lds
s_add_u32 m0,s57,139008
s_nop 0
buffer_load_dwordx4 v18,s[40:43],s55 offen offset:256  lds
s_add_u32 m0,s57,140032
s_nop 0
buffer_load_dwordx4 v19,s[40:43],s55 offen offset:256  lds
v_add_u32 v26,0,v20
v_add_u32 v27,0,v21
v_add_u32 v28,0,v22
T a[0:1],26,0
T a[2:3],26,512
T a[4:5],27,0
T a[6:7],27,512
T a[8:9],28,0
T a[10:11],28,512
T a[12:13],26,4096
T a[14:15],26,4608
T a[16:17],27,4096
T a[18:19],27,4608
T a[20:21],28,4096
T a[22:23],28,4608
s_waitcnt vmcnt(25)
L 0
s_barrier
s_add_u32 s55,s50,2752512
s_add_u32 m0,s57,0
s_nop 0
buffer_load_dwordx4 v18,s[40:43],s55 offen offset:0  lds
s_add_u32 m0,s57,1024
s_nop 0
buffer_load_dwordx4 v19,s[40:43],s55 offen offset:0  lds
s_add_u32 m0,s57,8064
s_nop 0
buffer_load_dwordx4 v18,s[40:43],s55 offen offset:128  lds
s_add_u32 m0,s57,9088
s_nop 0
buffer_load_dwordx4 v19,s[40:43],s55 offen offset:128  lds
s_add_u32 m0,s57,16128
s_nop 0
buffer_load_dwordx4 v18,s[40:43],s55 offen offset:256  lds
s_add_u32 m0,s57,17152
s_nop 0
buffer_load_dwordx4 v19,s[40:43],s55 offen offset:256  lds
v_add_u32 v26,24576,v20
v_add_u32 v27,24576,v21
v_add_u32 v28,24576,v22
T a[24:25],26,0
T a[26:27],26,512
T a[28:29],27,0
T a[30:31],27,512
T a[32:33],28,0
T a[34:35],28,512
T a[36:37],26,4096
T a[38:39],26,4608
T a[40:41],27,4096
T a[42:43],27,4608
T a[44:45],28,4096
T a[46:47],28,4608
s_waitcnt vmcnt(24)
L 0
s_barrier
s_add_u32 s55,s50,3211264
s_add_u32 m0,s57,24576
s_nop 0
buffer_load_dwordx4 v18,s[40:43],s55 offen offset:0  lds
s_add_u32 m0,s57,25600
s_nop 0
buffer_load_dwordx4 v19,s[40:43],s55 offen offset:0  lds
s_add_u32 m0,s57,32640
s_nop 0
buffer_load_dwordx4 v18,s[40:43],s55 offen offset:128  lds
s_add_u32 m0,s57,33664
s_nop 0
buffer_load_dwordx4 v19,s[40:43],s55 offen offset:128  lds
s_add_u32 m0,s57,40704
s_nop 0
buffer_load_dwordx4 v18,s[40:43],s55 offen offset:256  lds
s_add_u32 m0,s57,41728
s_nop 0
buffer_load_dwordx4 v19,s[40:43],s55 offen offset:256  lds
v_add_u32 v26,49152,v20
v_add_u32 v27,49152,v21
v_add_u32 v28,49152,v22
T a[48:49],26,0
T a[50:51],26,512
T a[52:53],27,0
T a[54:55],27,512
T a[56:57],28,0
T a[58:59],28,512
T a[60:61],26,4096
T a[62:63],26,4608
T a[64:65],27,4096
T a[66:67],27,4608
T a[68:69],28,4096
T a[70:71],28,4608
s_waitcnt vmcnt(24)
L 0
s_barrier
s_add_u32 s55,s50,3670016
s_add_u32 m0,s57,49152
s_nop 0
buffer_load_dwordx4 v18,s[40:43],s55 offen offset:0  lds
s_add_u32 m0,s57,50176
s_nop 0
buffer_load_dwordx4 v19,s[40:43],s55 offen offset:0  lds
s_add_u32 m0,s57,57216
s_nop 0
buffer_load_dwordx4 v18,s[40:43],s55 offen offset:128  lds
s_add_u32 m0,s57,58240
s_nop 0
buffer_load_dwordx4 v19,s[40:43],s55 offen offset:128  lds
s_add_u32 m0,s57,65280
s_nop 0
buffer_load_dwordx4 v18,s[40:43],s55 offen offset:256  lds
s_add_u32 m0,s57,66304
s_nop 0
buffer_load_dwordx4 v19,s[40:43],s55 offen offset:256  lds
v_add_u32 v26,73728,v20
v_add_u32 v27,73728,v21
v_add_u32 v28,73728,v22
T a[72:73],26,0
T a[74:75],26,512
T a[76:77],27,0
T a[78:79],27,512
T a[80:81],28,0
T a[82:83],28,512
T a[84:85],26,4096
T a[86:87],26,4608
T a[88:89],27,4096
T a[90:91],27,4608
T a[92:93],28,4096
T a[94:95],28,4608
s_waitcnt vmcnt(24)
L 0
s_barrier
s_add_u32 s55,s50,4128768
s_add_u32 m0,s57,73728
s_nop 0
buffer_load_dwordx4 v18,s[40:43],s55 offen offset:0  lds
s_add_u32 m0,s57,74752
s_nop 0
buffer_load_dwordx4 v19,s[40:43],s55 offen offset:0  lds
s_add_u32 m0,s57,81792
s_nop 0
buffer_load_dwordx4 v18,s[40:43],s55 offen offset:128  lds
s_add_u32 m0,s57,82816
s_nop 0
buffer_load_dwordx4 v19,s[40:43],s55 offen offset:128  lds
s_add_u32 m0,s57,89856
s_nop 0
buffer_load_dwordx4 v18,s[40:43],s55 offen offset:256  lds
s_add_u32 m0,s57,90880
s_nop 0
buffer_load_dwordx4 v19,s[40:43],s55 offen offset:256  lds
v_add_u32 v26,98304,v20
v_add_u32 v27,98304,v21
v_add_u32 v28,98304,v22
T a[96:97],26,0
T a[98:99],26,512
T a[100:101],27,0
T a[102:103],27,512
T a[104:105],28,0
T a[106:107],28,512
T a[108:109],26,4096
T a[110:111],26,4608
T a[112:113],27,4096
T a[114:115],27,4608
T a[116:117],28,4096
T a[118:119],28,4608
s_waitcnt vmcnt(24)
L 0
s_barrier
s_add_u32 s55,s50,4587520
s_add_u32 m0,s57,98304
s_nop 0
buffer_load_dwordx4 v18,s[40:43],s55 offen offset:0  lds
s_add_u32 m0,s57,99328
s_nop 0
buffer_load_dwordx4 v19,s[40:43],s55 offen offset:0  lds
s_add_u32 m0,s57,106368
s_nop 0
buffer_load_dwordx4 v18,s[40:43],s55 offen offset:128  lds
s_add_u32 m0,s57,107392
s_nop 0
buffer_load_dwordx4 v19,s[40:43],s55 offen offset:128  lds
s_add_u32 m0,s57,114432
s_nop 0
buffer_load_dwordx4 v18,s[40:43],s55 offen offset:256  lds
s_add_u32 m0,s57,115456
s_nop 0
buffer_load_dwordx4 v19,s[40:43],s55 offen offset:256  lds
v_add_u32 v26,122880,v20
v_add_u32 v27,122880,v21
v_add_u32 v28,122880,v22
T a[120:121],26,0
T a[122:123],26,512
T a[124:125],27,0
T a[126:127],27,512
T a[128:129],28,0
T a[130:131],28,512
T a[132:133],26,4096
T a[134:135],26,4608
T a[136:137],27,4096
T a[138:139],27,4608
T a[140:141],28,4096
T a[142:143],28,4608
s_waitcnt vmcnt(24)
L 0
s_barrier
s_add_u32 s55,s50,5046272
s_add_u32 m0,s57,122880
s_nop 0
buffer_load_dwordx4 v18,s[40:43],s55 offen offset:0  lds
s_add_u32 m0,s57,123904
s_nop 0
buffer_load_dwordx4 v19,s[40:43],s55 offen offset:0  lds
s_add_u32 m0,s57,130944
s_nop 0
buffer_load_dwordx4 v18,s[40:43],s55 offen offset:128  lds
s_add_u32 m0,s57,131968
s_nop 0
buffer_load_dwordx4 v19,s[40:43],s55 offen offset:128  lds
s_add_u32 m0,s57,139008
s_nop 0
buffer_load_dwordx4 v18,s[40:43],s55 offen offset:256  lds
s_add_u32 m0,s57,140032
s_nop 0
buffer_load_dwordx4 v19,s[40:43],s55 offen offset:256  lds
v_add_u32 v26,0,v20
v_add_u32 v27,0,v21
v_add_u32 v28,0,v22
T a[144:145],26,0
T a[146:147],26,512
T a[148:149],27,0
T a[150:151],27,512
T a[152:153],28,0
T a[154:155],28,512
T a[156:157],26,4096
T a[158:159],26,4608
T a[160:161],27,4096
T a[162:163],27,4608
T a[164:165],28,4096
T a[166:167],28,4608
s_waitcnt vmcnt(24)
L 0
s_barrier
s_add_u32 s55,s50,5505024
s_add_u32 m0,s57,0
s_nop 0
buffer_load_dwordx4 v18,s[40:43],s55 offen offset:0  lds
s_add_u32 m0,s57,1024
s_nop 0
buffer_load_dwordx4 v19,s[40:43],s55 offen offset:0  lds
s_add_u32 m0,s57,8064
s_nop 0
buffer_load_dwordx4 v18,s[40:43],s55 offen offset:128  lds
s_add_u32 m0,s57,9088
s_nop 0
buffer_load_dwordx4 v19,s[40:43],s55 offen offset:128  lds
s_add_u32 m0,s57,16128
s_nop 0
buffer_load_dwordx4 v18,s[40:43],s55 offen offset:256  lds
s_add_u32 m0,s57,17152
s_nop 0
buffer_load_dwordx4 v19,s[40:43],s55 offen offset:256  lds
v_add_u32 v26,24576,v20
v_add_u32 v27,24576,v21
v_add_u32 v28,24576,v22
T a[168:169],26,0
T a[170:171],26,512
T a[172:173],27,0
T a[174:175],27,512
T a[176:177],28,0
T a[178:179],28,512
T a[180:181],26,4096
T a[182:183],26,4608
T a[184:185],27,4096
T a[186:187],27,4608
T a[188:189],28,4096
T a[190:191],28,4608
s_waitcnt vmcnt(24)
L 0
s_barrier
s_add_u32 s55,s50,5963776
s_add_u32 m0,s57,24576
s_nop 0
buffer_load_dwordx4 v18,s[40:43],s55 offen offset:0  lds
s_add_u32 m0,s57,25600
s_nop 0
buffer_load_dwordx4 v19,s[40:43],s55 offen offset:0  lds
s_add_u32 m0,s57,32640
s_nop 0
buffer_load_dwordx4 v18,s[40:43],s55 offen offset:128  lds
s_add_u32 m0,s57,33664
s_nop 0
buffer_load_dwordx4 v19,s[40:43],s55 offen offset:128  lds
s_add_u32 m0,s57,40704
s_nop 0
buffer_load_dwordx4 v18,s[40:43],s55 offen offset:256  lds
s_add_u32 m0,s57,41728
s_nop 0
buffer_load_dwordx4 v19,s[40:43],s55 offen offset:256  lds
v_add_u32 v26,49152,v20
v_add_u32 v27,49152,v21
v_add_u32 v28,49152,v22
T a[192:193],26,0
T a[194:195],26,512
T a[196:197],27,0
T a[198:199],27,512
T a[200:201],28,0
T a[202:203],28,512
T a[204:205],26,4096
T a[206:207],26,4608
T a[208:209],27,4096
T a[210:211],27,4608
T a[212:213],28,4096
T a[214:215],28,4608
s_waitcnt vmcnt(24)
L 0
s_barrier
s_add_u32 s55,s50,6422528
s_add_u32 m0,s57,49152
s_nop 0
buffer_load_dwordx4 v18,s[40:43],s55 offen offset:0  lds
s_add_u32 m0,s57,50176
s_nop 0
buffer_load_dwordx4 v19,s[40:43],s55 offen offset:0  lds
s_add_u32 m0,s57,57216
s_nop 0
buffer_load_dwordx4 v18,s[40:43],s55 offen offset:128  lds
)"
R"(s_add_u32 m0,s57,58240
s_nop 0
buffer_load_dwordx4 v19,s[40:43],s55 offen offset:128  lds
s_add_u32 m0,s57,65280
s_nop 0
buffer_load_dwordx4 v18,s[40:43],s55 offen offset:256  lds
s_add_u32 m0,s57,66304
s_nop 0
buffer_load_dwordx4 v19,s[40:43],s55 offen offset:256  lds
v_add_u32 v26,73728,v20
v_add_u32 v27,73728,v21
v_add_u32 v28,73728,v22
T a[216:217],26,0
T a[218:219],26,512
T a[220:221],27,0
T a[222:223],27,512
T a[224:225],28,0
T a[226:227],28,512
T a[228:229],26,4096
T a[230:231],26,4608
T a[232:233],27,4096
T a[234:235],27,4608
T a[236:237],28,4096
T a[238:239],28,4608
s_waitcnt vmcnt(24)
L 0
s_barrier
s_add_u32 s55,s50,6881280
s_add_u32 m0,s57,73728
s_nop 0
buffer_load_dwordx4 v18,s[40:43],s55 offen offset:0  lds
s_add_u32 m0,s57,74752
s_nop 0
buffer_load_dwordx4 v19,s[40:43],s55 offen offset:0  lds
s_add_u32 m0,s57,81792
s_nop 0
buffer_load_dwordx4 v18,s[40:43],s55 offen offset:128  lds
s_add_u32 m0,s57,82816
s_nop 0
buffer_load_dwordx4 v19,s[40:43],s55 offen offset:128  lds
s_add_u32 m0,s57,89856
s_nop 0
buffer_load_dwordx4 v18,s[40:43],s55 offen offset:256  lds
s_add_u32 m0,s57,90880
s_nop 0
buffer_load_dwordx4 v19,s[40:43],s55 offen offset:256  lds
v_add_u32 v26,98304,v20
v_add_u32 v27,98304,v21
v_add_u32 v28,98304,v22
T a[240:241],26,0
T a[242:243],26,512
T a[244:245],27,0
T a[246:247],27,512
T a[248:249],28,0
T a[250:251],28,512
T a[252:253],26,4096
T a[254:255],26,4608
T v[128:129],27,4096
T v[130:131],27,4608
T v[132:133],28,4096
T v[134:135],28,4608
s_waitcnt vmcnt(24)
L 0
s_barrier
v_add_u32 v26,122880,v20
v_add_u32 v27,122880,v21
v_add_u32 v28,122880,v22
T v[136:137],26,0
T v[138:139],26,512
T v[140:141],27,0
T v[142:143],27,512
T v[144:145],28,0
T v[146:147],28,512
T v[148:149],26,4096
T v[150:151],26,4608
T v[152:153],27,4096
T v[154:155],27,4608
T v[156:157],28,4096
T v[158:159],28,4608
s_waitcnt vmcnt(18)
L 0
s_barrier
v_add_u32 v26,0,v20
v_add_u32 v27,0,v21
v_add_u32 v28,0,v22
T v[160:161],26,0
T v[162:163],26,512
T v[164:165],27,0
T v[166:167],27,512
T v[168:169],28,0
T v[170:171],28,512
T v[172:173],26,4096
T v[174:175],26,4608
T v[176:177],27,4096
T v[178:179],27,4608
T v[180:181],28,4096
T v[182:183],28,4608
s_waitcnt vmcnt(12)
L 0
s_barrier
v_add_u32 v26,24576,v20
v_add_u32 v27,24576,v21
v_add_u32 v28,24576,v22
T v[184:185],26,0
T v[186:187],26,512
T v[188:189],27,0
T v[190:191],27,512
T v[192:193],28,0
T v[194:195],28,512
T v[196:197],26,4096
T v[198:199],26,4608
T v[200:201],27,4096
T v[202:203],27,4608
T v[204:205],28,4096
T v[206:207],28,4608
s_waitcnt vmcnt(6)
L 0
s_barrier
v_add_u32 v26,49152,v20
v_add_u32 v27,49152,v21
v_add_u32 v28,49152,v22
T v[208:209],26,0
T v[210:211],26,512
T v[212:213],27,0
T v[214:215],27,512
T v[216:217],28,0
T v[218:219],28,512
T v[220:221],26,4096
T v[222:223],26,4608
T v[224:225],27,4096
T v[226:227],27,4608
T v[228:229],28,4096
T v[230:231],28,4608
s_waitcnt vmcnt(0)
L 0
s_barrier
v_add_u32 v26,73728,v20
v_add_u32 v27,73728,v21
v_add_u32 v28,73728,v22
T v[232:233],26,0
T v[234:235],26,512
T v[236:237],27,0
T v[238:239],27,512
T v[240:241],28,0
T v[242:243],28,512
T v[244:245],26,4096
T v[246:247],26,4608
T v[248:249],27,4096
T v[250:251],27,4608
T v[252:253],28,4096
T v[254:255],28,4608
L 0
s_barrier
v_mov_b32 v42,0x3f803f80
v_mov_b32 v43,0x3f803f80
v_mov_b32 v44,0x3f803f80
v_mov_b32 v45,0x3f803f80
s_nop 1
v_mfma_f32_16x16x32_bf16 v[82:85],a[0:3],v[42:45],0
v_mfma_f32_16x16x32_bf16 v[86:89],a[4:7],v[42:45],0
v_mfma_f32_16x16x32_bf16 v[90:93],a[8:11],v[42:45],0
M 82,a[12:15],42
M 86,a[16:19],42
M 90,a[20:23],42
M 82,a[24:27],42
M 86,a[28:31],42
M 90,a[32:35],42
M 82,a[36:39],42
M 86,a[40:43],42
M 90,a[44:47],42
M 82,a[48:51],42
M 86,a[52:55],42
M 90,a[56:59],42
M 82,a[60:63],42
M 86,a[64:67],42
M 90,a[68:71],42
M 82,a[72:75],42
M 86,a[76:79],42
M 90,a[80:83],42
M 82,a[84:87],42
M 86,a[88:91],42
M 90,a[92:95],42
M 82,a[96:99],42
M 86,a[100:103],42
M 90,a[104:107],42
M 82,a[108:111],42
M 86,a[112:115],42
M 90,a[116:119],42
M 82,a[120:123],42
M 86,a[124:127],42
M 90,a[128:131],42
M 82,a[132:135],42
M 86,a[136:139],42
M 90,a[140:143],42
M 82,a[144:147],42
M 86,a[148:151],42
M 90,a[152:155],42
M 82,a[156:159],42
M 86,a[160:163],42
M 90,a[164:167],42
M 82,a[168:171],42
M 86,a[172:175],42
M 90,a[176:179],42
M 82,a[180:183],42
M 86,a[184:187],42
M 90,a[188:191],42
M 82,a[192:195],42
M 86,a[196:199],42
M 90,a[200:203],42
M 82,a[204:207],42
M 86,a[208:211],42
M 90,a[212:215],42
M 82,a[216:219],42
M 86,a[220:223],42
M 90,a[224:227],42
M 82,a[228:231],42
M 86,a[232:235],42
M 90,a[236:239],42
M 82,a[240:243],42
M 86,a[244:247],42
M 90,a[248:251],42
M 82,a[252:255],42
M 86,v[128:131],42
M 90,v[132:135],42
M 82,v[136:139],42
M 86,v[140:143],42
M 90,v[144:147],42
M 82,v[148:151],42
M 86,v[152:155],42
M 90,v[156:159],42
M 82,v[160:163],42
M 86,v[164:167],42
M 90,v[168:171],42
M 82,v[172:175],42
M 86,v[176:179],42
M 90,v[180:183],42
M 82,v[184:187],42
M 86,v[188:191],42
M 90,v[192:195],42
M 82,v[196:199],42
M 86,v[200:203],42
M 90,v[204:207],42
M 82,v[208:211],42
M 86,v[212:215],42
M 90,v[216:219],42
M 82,v[220:223],42
M 86,v[224:227],42
M 90,v[228:231],42
M 82,v[232:235],42
M 86,v[236:239],42
M 90,v[240:243],42
M 82,v[244:247],42
M 86,v[248:251],42
M 90,v[252:255],42
v_mov_b32 v4,s53
s_mov_b32 s82,0
s_mov_b32 s80,0
s_mov_b32 s54,s45
s_mul_i32 s57,s44,4096
s_mul_i32 s55,s45,32768
s_add_u32 s55,s55,s57
s_add_u32 s55,s55,31457280
s_mul_i32 s60,s46,32768
s_mul_i32 s61,s46,1024
s_mul_i32 s62,s46,114688
s_mul_i32 s63,s46,2560
s_mul_i32 s64,s46,1024
s_lshl_b32 s59,s44,8
s_add_u32 s59,s59,131072
s_mul_i32 s58,s45,1024
s_add_u32 s58,s58,109613056
s_mul_i32 s65,s45,114688
s_add_u32 s65,s65,s47
s_mul_i32 s68,s45,2560
s_add_u32 s68,s68,s48
s_mul_i32 s73,s45,1024
s_add_u32 s73,s73,107479040
s_mul_i32 s74,s45,32768
s_add_u32 s74,s74,s49
s_mul_i32 s75,s45,1024
s_add_u32 s75,s75,107479040
s_mov_b32 s66,0x7fff0000
s_mov_b32 s67,0x7fff0000
s_mov_b32 s69,0x7fff0000
s_mov_b32 s76,0x7fff0000
s_mov_b32 s77,0x7fff0000
s_mov_b32 s70,0x7fff0000
s_mov_b32 s71,0x7fff0000
s_mov_b32 s72,0x7fff0000
v_mov_b32 v1,v0
v_add_u32 v2,16384,v0
s_mov_b32 s84,16384
s_mov_b32 s86,0x3a800000
s_mov_b32 s87,0x3727c5ac
s_mov_b32 s88,0xc0135761
s_mov_b32 s89,0x3d372713
s_and_b32 s85,s44,1
s_mul_i32 s85,s85,127
v_lshlrev_b32 v8,22,v0
v_and_b32 v8,0x80000000,v8
v_or_b32 v8,v8,v5
v_add_u32 v2,32768,v0
s_lshl_b32 s84,s57,1
s_add_u32 s84,s84,32768
s_add_u32 m0,s59,4096
s_nop 0
buffer_load_dword v8,s[36:39],s58 offen offset:0 sc1 lds
buffer_store_dword v4,v3,s[36:39],s71 offen offset:0 sc0 sc1
s_add_u32 m0,s59,5120
s_nop 0
buffer_load_dword v8,s[36:39],s58 offen offset:0 sc1 lds
buffer_store_dword v4,v3,s[36:39],s71 offen offset:0 sc0 sc1
s_mov_b32 s83,0
Lg3_poll_%=:
s_add_u32 s78,s83,2
s_and_b32 s78,s78,3
s_lshl_b32 s78,s78,10
s_add_u32 s78,s78,4096
s_add_u32 m0,s59,s78
s_nop 0
buffer_load_dword v8,s[36:39],s58 offen offset:0 sc1 lds
s_and_b32 s78,s83,3
s_lshl_b32 s78,s78,10
v_add_u32 v94,s78,v6
s_waitcnt vmcnt(4)
s_barrier
ds_read_b32 v7,v94 offset:4096
buffer_store_dword v4,v3,s[36:39],s70 offen offset:0 sc0 sc1
s_mov_b32 s70,s71
s_mov_b32 s71,s72
s_mov_b32 s72,0x7fff0000
s_add_u32 s83,s83,1
s_add_u32 s82,s82,1
L 0
v_cmp_eq_u32 vcc,s53,v7
s_nop 0
s_cmp_eq_u32 vcc_lo,-1
s_cbranch_scc1 Lg3_go_%=
s_cmp_lt_u32 s82,20000
s_cbranch_scc1 Lg3_poll_%=
Lg3_go_%=:
buffer_load_dword v114,v11,s[36:39],s73 offen offset:0 sc1
s_add_u32 s73,s73,s64
s_add_u32 m0,s57,0
s_add_u32 s56,s55,0
D 0,56,0
D 0,56,1024
D 0,56,2048
D 0,56,3072
s_add_u32 s58,s58,s61
s_add_u32 m0,s59,2048
s_nop 0
buffer_load_dword v8,s[36:39],s58 offen offset:0 sc1 lds
s_add_u32 m0,s57,16384
s_add_u32 s56,s55,16384
D 0,56,0
D 0,56,1024
D 0,56,2048
D 0,56,3072
s_add_u32 s55,s55,s60
s_sub_u32 s57,s84,s57
s_add_u32 s54,s54,s46
s_waitcnt vmcnt(4)
s_barrier
buffer_store_dword v4,v3,s[36:39],s70 offen offset:0 sc0 sc1
s_min_u32 s71,s71,s72
buffer_store_dword v4,v3,s[36:39],s71 offen offset:0 sc0 sc1
s_mov_b32 s70,0x7fff0000
s_mov_b32 s71,0x7fff0000
s_mov_b32 s72,0x7fff0000
s_cmp_eq_u32 s80,1
s_cbranch_scc1 Lg3_st1_%=
s_branch Lg3_st0_%=
Lg3_gox_%=:
s_add_u32 s58,s58,s61
s_add_u32 m0,s59,2048
s_nop 0
buffer_load_dword v8,s[36:39],s58 offen offset:0 sc1 lds
s_waitcnt vmcnt(0)
s_barrier
buffer_store_dword v4,v3,s[36:39],s70 offen offset:0 sc0 sc1
s_min_u32 s71,s71,s72
buffer_store_dword v4,v3,s[36:39],s71 offen offset:0 sc0 sc1
s_mov_b32 s70,0x7fff0000
s_mov_b32 s71,0x7fff0000
s_mov_b32 s72,0x7fff0000
s_cmp_eq_u32 s80,1
s_cbranch_scc1 Lg3_st1_%=
Lg3_st0_%=:
ds_read_b32 v7,v6 offset:2048
R 42,1,0
R 46,1,1024
R 50,1,2048
R 54,1,3072
R 58,1,4096
L 5
v_cmp_eq_u32 vcc,s53,v7
L 3
s_cmp_eq_u32 vcc_lo,-1
s_cbranch_scc1 Lg3_F0_%=
s_branch Lg3_L0_%=
Lg3_st1_%=:
ds_read_b32 v7,v6 offset:2048
R 42,2,0
R 46,2,1024
R 50,2,2048
R 54,2,3072
R 58,2,4096
L 5
v_cmp_eq_u32 vcc,s53,v7
L 3
s_cmp_eq_u32 vcc_lo,-1
s_cbranch_scc1 Lg3_F1_%=
s_branch Lg3_L1_%=
Lg3_F0_%=:
v_mfma_f32_16x16x32_bf16 v[18:21],a[0:3],v[42:45],0
s_add_u32 s58,s58,s61
R 62,1,5120
v_add_f32_dpp v115,v114,v114 quad_perm:[1,0,3,2] row_mask:0xf bank_mask:0xf
v_mfma_f32_16x16x32_bf16 v[22:25],a[4:7],v[42:45],0
s_add_u32 m0,s57,0
buffer_load_dword v114,v11,s[36:39],s73 offen offset:0 sc1
v_mfma_f32_16x16x32_bf16 v[26:29],a[8:11],v[42:45],0
s_add_u32 s56,s55,0
D 0,56,0
M 18,a[12:15],46
s_mov_b32 s66,s65
R 66,1,6144
v_add_f32_dpp v115,v115,v115 quad_perm:[2,3,0,1] row_mask:0xf bank_mask:0xf
M 22,a[16:19],46
s_add_u32 s65,s65,s62
v_fma_f32 v30,-v78,v82,v30
v_fma_f32 v31,-v78,v83,v31
M 26,a[20:23],46
L 3
v_add_f32_dpp v118,v115,v115 row_half_mirror row_mask:0xf bank_mask:0xf
v_fma_f32 v32,-v78,v84,v32
M 18,a[24:27],50
R 70,1,7168
v_fma_f32 v33,-v78,v85,v33
M 22,a[28:31],50
v_mov_b32_dpp v115,v118 row_ror:8 row_mask:0xf bank_mask:0xf
v_fma_f32 v115,-v118,v118,v115
v_max_f32 v115,s87,v115
M 26,a[32:35],50
v_rsq_f32 v116,v115
M 18,a[36:39],54
R 42,1,8192
v_mul_f32 v121,v115,v116
M 22,a[40:43],54
v_mul_f32 v119,s88,v116
v_mul_f32 v115,s89,v116
M 26,a[44:47],54
L 3
v_mul_f32 v120,v119,v116
v_mul_f32 v120,v120,v115
M 18,a[48:51],58
R 46,1,9216
v_mul_f32 v94,v30,v30
M 22,a[52:55],58
D 0,56,1024
M 26,a[56:59],58
ds_write_b128 v12,v[118:121] offset:0
M 18,a[60:63],62
R 50,1,10240
v_mul_f32 v95,v31,v31
v_mul_f32 v96,v32,v32
M 22,a[64:67],62
v_mul_f32 v97,v33,v33
v_fma_f32 v94,v80,v94,v79
M 26,a[68:71],62
L 4
v_fma_f32 v95,v80,v95,v79
v_fma_f32 v96,v80,v96,v79
M 18,a[72:75],66
R 54,1,11264
v_fma_f32 v97,v80,v97,v79
M 22,a[76:79],66
v_mul_f32 v94,v30,v94
v_mul_f32 v95,v31,v95
M 26,a[80:83],66
v_exp_f32 v94,v94
M 18,a[84:87],70
R 58,1,12288
v_mul_f32 v96,v32,v96
M 22,a[88:91],70
v_exp_f32 v95,v95
M 26,a[92:95],70
L 4
v_mul_f32 v97,v33,v97
v_exp_f32 v96,v96
M 18,a[96:99],42
R 62,1,13312
v_fma_f32 v94,v94,v81,v81
M 22,a[100:103],42
D 0,56,2048
M 26,a[104:107],42
v_exp_f32 v97,v97
M 18,a[108:111],46
R 66,1,14336
)"
R"(v_fma_f32 v95,v95,v81,v81
M 22,a[112:115],46
v_rcp_f32 v94,v94
M 26,a[116:119],46
L 3
v_rcp_f32 v95,v95
M 18,a[120:123],50
R 70,1,15360
v_fma_f32 v96,v96,v81,v81
M 22,a[124:127],50
s_waitcnt vmcnt(5)
v_fma_f32 v97,v97,v81,v81
v_rcp_f32 v96,v96
M 26,a[128:131],50
s_barrier
v_rcp_f32 v97,v97
M 18,a[132:135],54
R 42,1,16384
v_mul_f32 v30,v30,v94
M 22,a[136:139],54
v_mul_f32 v31,v31,v95
v_mul_f32 v32,v32,v96
M 26,a[140:143],54
L 3
D 0,56,3072
M 18,a[144:147],58
s_add_u32 m0,s59,0
R 46,1,17408
v_mul_f32 v33,v33,v97
M 22,a[148:151],58
v_cvt_pk_bf16_f32 v106,v30,v31
v_cvt_pk_bf16_f32 v107,v32,v33
M 26,a[152:155],58
v_fma_f32 v34,-v78,v86,v34
v_fma_f32 v35,-v78,v87,v35
M 18,a[156:159],62
R 50,1,18432
v_fma_f32 v36,-v78,v88,v36
v_fma_f32 v37,-v78,v89,v37
M 22,a[160:163],62
v_mul_f32 v98,v34,v34
v_mul_f32 v99,v35,v35
M 26,a[164:167],62
L 3
v_mul_f32 v100,v36,v36
v_mul_f32 v101,v37,v37
M 18,a[168:171],66
R 54,1,19456
v_fma_f32 v98,v80,v98,v79
M 22,a[172:175],66
v_fma_f32 v99,v80,v99,v79
v_fma_f32 v100,v80,v100,v79
M 26,a[176:179],66
v_fma_f32 v101,v80,v101,v79
v_mul_f32 v98,v34,v98
M 18,a[180:183],70
R 58,1,20480
v_mul_f32 v99,v35,v99
M 22,a[184:187],70
buffer_load_dword v8,s[36:39],s58 offen offset:0 sc1 lds
M 26,a[188:191],70
L 3
v_mul_f32 v100,v36,v100
v_exp_f32 v98,v98
M 18,a[192:195],42
s_add_u32 m0,s57,16384
R 62,1,21504
v_mul_f32 v101,v37,v101
M 22,a[196:199],42
s_add_u32 s56,s55,16384
D 0,56,0
M 26,a[200:203],42
s_add_u32 s55,s55,s60
v_exp_f32 v99,v99
M 18,a[204:207],46
s_sub_u32 s57,s84,s57
R 66,1,22528
v_fma_f32 v98,v98,v81,v81
M 22,a[208:211],46
s_add_u32 s54,s54,s46
v_exp_f32 v100,v100
M 26,a[212:215],46
L 3
v_exp_f32 v101,v101
M 18,a[216:219],50
s_add_u32 s73,s73,s64
R 70,1,23552
v_fma_f32 v99,v99,v81,v81
M 22,a[220:223],50
v_fma_f32 v100,v100,v81,v81
v_rcp_f32 v98,v98
M 26,a[224:227],50
v_rcp_f32 v99,v99
M 18,a[228:231],54
R 42,1,24576
v_fma_f32 v101,v101,v81,v81
M 22,a[232:235],54
v_rcp_f32 v100,v100
M 26,a[236:239],54
L 3
v_rcp_f32 v101,v101
M 18,a[240:243],58
R 46,1,25600
v_mul_f32 v34,v34,v98
M 22,a[244:247],58
D 0,56,1024
M 26,a[248:251],58
v_mul_f32 v35,v35,v99
v_mul_f32 v36,v36,v100
M 18,a[252:255],62
R 50,1,26624
v_mul_f32 v37,v37,v101
v_cvt_pk_bf16_f32 v108,v34,v35
M 22,v[128:131],62
v_cvt_pk_bf16_f32 v109,v36,v37
v_fma_f32 v38,-v78,v90,v38
M 26,v[132:135],62
L 3
buffer_store_dwordx4 v[106:109],v0,s[36:39],s67 offen offset:0 sc0 sc1
v_fma_f32 v39,-v78,v91,v39
M 18,v[136:139],66
R 54,1,27648
v_fma_f32 v40,-v78,v92,v40
M 22,v[140:143],66
v_fma_f32 v41,-v78,v93,v41
v_mul_f32 v102,v38,v38
M 26,v[144:147],66
v_mul_f32 v103,v39,v39
v_mul_f32 v104,v40,v40
M 18,v[148:151],70
R 58,1,28672
v_mul_f32 v105,v41,v41
M 22,v[152:155],70
v_fma_f32 v102,v80,v102,v79
v_fma_f32 v103,v80,v103,v79
M 26,v[156:159],70
L 3
D 0,56,2048
v_fma_f32 v104,v80,v104,v79
M 18,v[160:163],42
R 62,1,29696
v_fma_f32 v105,v80,v105,v79
M 22,v[164:167],42
v_mul_f32 v102,v38,v102
v_mul_f32 v103,v39,v103
M 26,v[168:171],42
v_exp_f32 v102,v102
M 18,v[172:175],46
R 66,1,30720
v_mul_f32 v104,v40,v104
M 22,v[176:179],46
v_exp_f32 v103,v103
M 26,v[180:183],46
L 3
v_exp_f32 v104,v104
M 18,v[184:187],50
s_nop 0
R 70,1,31744
v_mul_f32 v105,v41,v105
M 22,v[188:191],50
s_waitcnt vmcnt(4)
v_exp_f32 v105,v105
v_fma_f32 v102,v102,v81,v81
M 26,v[192:195],50
s_barrier
R 74,13,0
ds_read_b32 v7,v6 offset:0
M 18,v[196:199],54
R 42,2,0
v_fma_f32 v103,v103,v81,v81
M 22,v[200:203],54
buffer_store_dword v4,v3,s[36:39],s70 offen offset:0 sc0 sc1
M 26,v[204:207],54
L 5
D 0,56,3072
M 18,v[208:211],58
s_mov_b32 s70,s69
R 46,2,1024
v_fma_f32 v104,v104,v81,v81
M 22,v[212:215],58
s_mov_b32 s69,s68
v_rcp_f32 v102,v102
M 26,v[216:219],58
L 2
v_rcp_f32 v103,v103
M 18,v[220:223],62
s_add_u32 s68,s68,s63
R 50,2,2048
v_rcp_f32 v104,v104
M 22,v[224:227],62
v_fma_f32 v105,v105,v81,v81
v_mul_f32 v38,v38,v102
M 26,v[228:231],62
s_nop 0
v_rcp_f32 v105,v105
M 18,v[232:235],66
R 54,2,3072
v_mul_f32 v39,v39,v103
M 22,v[236:239],66
v_mul_f32 v40,v40,v104
v_mul_f32 v41,v41,v105
M 26,v[240:243],66
v_cvt_pk_bf16_f32 v110,v38,v39
v_cvt_pk_bf16_f32 v111,v40,v41
M 18,v[244:247],70
R 58,2,4096
v_cmp_eq_u32 vcc,s53,v7
M 22,v[248:251],70
s_cmp_eq_u32 vcc_lo,-1
v_permlane16_swap_b32 v110,v112
v_permlane16_swap_b32 v111,v113
M 26,v[252:255],70
L 3
buffer_store_dwordx4 v[110:113],v10,s[36:39],s67 offen offset:0 sc0 sc1
s_cbranch_scc0 Lg3_L1_%=
Lg3_F1_%=:
v_mfma_f32_16x16x32_bf16 v[30:33],a[0:3],v[42:45],0
s_add_u32 s58,s58,s61
R 62,2,5120
v_add_f32_dpp v115,v114,v114 quad_perm:[1,0,3,2] row_mask:0xf bank_mask:0xf
v_mfma_f32_16x16x32_bf16 v[34:37],a[4:7],v[42:45],0
s_add_u32 m0,s57,0
buffer_load_dword v114,v11,s[36:39],s73 offen offset:0 sc1
v_mfma_f32_16x16x32_bf16 v[38:41],a[8:11],v[42:45],0
s_add_u32 s56,s55,0
D 0,56,0
M 30,a[12:15],46
s_mov_b32 s67,s65
R 66,2,6144
v_add_f32_dpp v115,v115,v115 quad_perm:[2,3,0,1] row_mask:0xf bank_mask:0xf
M 34,a[16:19],46
s_add_u32 s65,s65,s62
v_fma_f32 v18,-v74,v82,v18
v_fma_f32 v19,-v74,v83,v19
M 38,a[20:23],46
L 3
v_add_f32_dpp v118,v115,v115 row_half_mirror row_mask:0xf bank_mask:0xf
v_fma_f32 v20,-v74,v84,v20
M 30,a[24:27],50
R 70,2,7168
v_fma_f32 v21,-v74,v85,v21
M 34,a[28:31],50
v_mov_b32_dpp v115,v118 row_ror:8 row_mask:0xf bank_mask:0xf
v_fma_f32 v115,-v118,v118,v115
v_max_f32 v115,s87,v115
M 38,a[32:35],50
v_rsq_f32 v116,v115
M 30,a[36:39],54
R 42,2,8192
v_mul_f32 v121,v115,v116
M 34,a[40:43],54
v_mul_f32 v119,s88,v116
v_mul_f32 v115,s89,v116
M 38,a[44:47],54
L 3
v_mul_f32 v120,v119,v116
v_mul_f32 v120,v120,v115
M 30,a[48:51],58
R 46,2,9216
v_mul_f32 v94,v18,v18
M 34,a[52:55],58
D 0,56,1024
M 38,a[56:59],58
ds_write_b128 v12,v[118:121] offset:2048
M 30,a[60:63],62
R 50,2,10240
v_mul_f32 v95,v19,v19
v_mul_f32 v96,v20,v20
M 34,a[64:67],62
v_mul_f32 v97,v21,v21
v_fma_f32 v94,v76,v94,v75
M 38,a[68:71],62
L 4
v_fma_f32 v95,v76,v95,v75
v_fma_f32 v96,v76,v96,v75
M 30,a[72:75],66
R 54,2,11264
v_fma_f32 v97,v76,v97,v75
M 34,a[76:79],66
v_mul_f32 v94,v18,v94
v_mul_f32 v95,v19,v95
M 38,a[80:83],66
v_exp_f32 v94,v94
M 30,a[84:87],70
R 58,2,12288
v_mul_f32 v96,v20,v96
M 34,a[88:91],70
v_exp_f32 v95,v95
M 38,a[92:95],70
L 4
v_mul_f32 v97,v21,v97
v_exp_f32 v96,v96
M 30,a[96:99],42
R 62,2,13312
v_fma_f32 v94,v94,v77,v77
M 34,a[100:103],42
D 0,56,2048
M 38,a[104:107],42
v_exp_f32 v97,v97
M 30,a[108:111],46
R 66,2,14336
v_fma_f32 v95,v95,v77,v77
M 34,a[112:115],46
v_rcp_f32 v94,v94
M 38,a[116:119],46
L 3
v_rcp_f32 v95,v95
M 30,a[120:123],50
R 70,2,15360
v_fma_f32 v96,v96,v77,v77
M 34,a[124:127],50
s_waitcnt vmcnt(5)
v_fma_f32 v97,v97,v77,v77
v_rcp_f32 v96,v96
M 38,a[128:131],50
s_barrier
v_rcp_f32 v97,v97
M 30,a[132:135],54
R 42,2,16384
v_mul_f32 v18,v18,v94
M 34,a[136:139],54
v_mul_f32 v19,v19,v95
v_mul_f32 v20,v20,v96
M 38,a[140:143],54
L 3
D 0,56,3072
M 30,a[144:147],58
s_add_u32 m0,s59,1024
R 46,2,17408
v_mul_f32 v21,v21,v97
M 34,a[148:151],58
v_cvt_pk_bf16_f32 v106,v18,v19
v_cvt_pk_bf16_f32 v107,v20,v21
M 38,a[152:155],58
v_fma_f32 v22,-v74,v86,v22
v_fma_f32 v23,-v74,v87,v23
M 30,a[156:159],62
R 50,2,18432
v_fma_f32 v24,-v74,v88,v24
v_fma_f32 v25,-v74,v89,v25
M 34,a[160:163],62
v_mul_f32 v98,v22,v22
v_mul_f32 v99,v23,v23
M 38,a[164:167],62
L 3
v_mul_f32 v100,v24,v24
v_mul_f32 v101,v25,v25
M 30,a[168:171],66
R 54,2,19456
v_fma_f32 v98,v76,v98,v75
M 34,a[172:175],66
v_fma_f32 v99,v76,v99,v75
v_fma_f32 v100,v76,v100,v75
M 38,a[176:179],66
v_fma_f32 v101,v76,v101,v75
v_mul_f32 v98,v22,v98
M 30,a[180:183],70
R 58,2,20480
v_mul_f32 v99,v23,v99
M 34,a[184:187],70
buffer_load_dword v8,s[36:39],s58 offen offset:0 sc1 lds
M 38,a[188:191],70
L 3
v_mul_f32 v100,v24,v100
v_exp_f32 v98,v98
M 30,a[192:195],42
s_add_u32 m0,s57,16384
R 62,2,21504
v_mul_f32 v101,v25,v101
M 34,a[196:199],42
s_add_u32 s56,s55,16384
D 0,56,0
M 38,a[200:203],42
s_add_u32 s55,s55,s60
v_exp_f32 v99,v99
M 30,a[204:207],46
s_sub_u32 s57,s84,s57
R 66,2,22528
v_fma_f32 v98,v98,v77,v77
M 34,a[208:211],46
s_add_u32 s54,s54,s46
v_exp_f32 v100,v100
M 38,a[212:215],46
L 3
v_exp_f32 v101,v101
M 30,a[216:219],50
s_add_u32 s73,s73,s64
R 70,2,23552
v_fma_f32 v99,v99,v77,v77
M 34,a[220:223],50
v_fma_f32 v100,v100,v77,v77
v_rcp_f32 v98,v98
M 38,a[224:227],50
v_rcp_f32 v99,v99
M 30,a[228:231],54
R 42,2,24576
v_fma_f32 v101,v101,v77,v77
M 34,a[232:235],54
v_rcp_f32 v100,v100
M 38,a[236:239],54
L 3
v_rcp_f32 v101,v101
M 30,a[240:243],58
R 46,2,25600
v_mul_f32 v22,v22,v98
M 34,a[244:247],58
D 0,56,1024
M 38,a[248:251],58
v_mul_f32 v23,v23,v99
v_mul_f32 v24,v24,v100
M 30,a[252:255],62
R 50,2,26624
v_mul_f32 v25,v25,v101
v_cvt_pk_bf16_f32 v108,v22,v23
M 34,v[128:131],62
v_cvt_pk_bf16_f32 v109,v24,v25
v_fma_f32 v26,-v74,v90,v26
M 38,v[132:135],62
L 3
buffer_store_dwordx4 v[106:109],v0,s[36:39],s66 offen offset:0 sc0 sc1
v_fma_f32 v27,-v74,v91,v27
M 30,v[136:139],66
R 54,2,27648
v_fma_f32 v28,-v74,v92,v28
M 34,v[140:143],66
v_fma_f32 v29,-v74,v93,v29
v_mul_f32 v102,v26,v26
M 38,v[144:147],66
v_mul_f32 v103,v27,v27
v_mul_f32 v104,v28,v28
M 30,v[148:151],70
R 58,2,28672
v_mul_f32 v105,v29,v29
M 34,v[152:155],70
v_fma_f32 v102,v76,v102,v75
v_fma_f32 v103,v76,v103,v75
M 38,v[156:159],70
L 3
D 0,56,2048
v_fma_f32 v104,v76,v104,v75
M 30,v[160:163],42
R 62,2,29696
v_fma_f32 v105,v76,v105,v75
M 34,v[164:167],42
v_mul_f32 v102,v26,v102
v_mul_f32 v103,v27,v103
M 38,v[168:171],42
v_exp_f32 v102,v102
M 30,v[172:175],46
R 66,2,30720
v_mul_f32 v104,v28,v104
M 34,v[176:179],46
v_exp_f32 v103,v103
M 38,v[180:183],46
L 3
v_exp_f32 v104,v104
M 30,v[184:187],50
s_nop 0
R 70,2,31744
v_mul_f32 v105,v29,v105
M 34,v[188:191],50
s_waitcnt vmcnt(4)
v_exp_f32 v105,v105
v_fma_f32 v102,v102,v77,v77
M 38,v[192:195],50
s_barrier
R 78,13,2048
ds_read_b32 v7,v6 offset:1024
M 30,v[196:199],54
R 42,1,0
v_fma_f32 v103,v103,v77,v77
M 34,v[200:203],54
buffer_store_dword v4,v3,s[36:39],s70 offen offset:0 sc0 sc1
M 38,v[204:207],54
L 5
D 0,56,3072
M 30,v[208:211],58
s_mov_b32 s70,s69
R 46,1,1024
v_fma_f32 v104,v104,v77,v77
M 34,v[212:215],58
s_mov_b32 s69,s68
v_rcp_f32 v102,v102
M 38,v[216:219],58
L 2
v_rcp_f32 v103,v103
M 30,v[220:223],62
s_add_u32 s68,s68,s63
R 50,1,2048
v_rcp_f32 v104,v104
M 34,v[224:227],62
v_fma_f32 v105,v105,v77,v77
v_mul_f32 v26,v26,v102
M 38,v[228:231],62
s_nop 0
v_rcp_f32 v105,v105
M 30,v[232:235],66
R 54,1,3072
v_mul_f32 v27,v27,v103
M 34,v[236:239],66
)"
R"(v_mul_f32 v28,v28,v104
v_mul_f32 v29,v29,v105
M 38,v[240:243],66
v_cvt_pk_bf16_f32 v110,v26,v27
v_cvt_pk_bf16_f32 v111,v28,v29
M 30,v[244:247],70
R 58,1,4096
v_cmp_eq_u32 vcc,s53,v7
M 34,v[248:251],70
s_cmp_eq_u32 vcc_lo,-1
v_permlane16_swap_b32 v110,v112
v_permlane16_swap_b32 v111,v113
M 38,v[252:255],70
L 3
buffer_store_dwordx4 v[110:113],v10,s[36:39],s66 offen offset:0 sc0 sc1
s_cbranch_scc1 Lg3_F0_%=
Lg3_L0_%=:
v_mfma_f32_16x16x32_bf16 v[18:21],a[0:3],v[42:45],0
s_add_u32 m0,s59,3072
R 62,1,5120
v_add_f32_dpp v115,v114,v114 quad_perm:[1,0,3,2] row_mask:0xf bank_mask:0xf
v_mfma_f32_16x16x32_bf16 v[22:25],a[4:7],v[42:45],0
s_mov_b32 s66,s65
buffer_load_dword v8,s[36:39],s58 offen offset:0 sc1 lds
v_mfma_f32_16x16x32_bf16 v[26:29],a[8:11],v[42:45],0
s_add_u32 s65,s65,s62
v_add_f32_dpp v115,v115,v115 quad_perm:[2,3,0,1] row_mask:0xf bank_mask:0xf
v_fma_f32 v30,-v78,v82,v30
M 18,a[12:15],46
R 66,1,6144
v_fma_f32 v31,-v78,v83,v31
M 22,a[16:19],46
v_add_f32_dpp v118,v115,v115 row_half_mirror row_mask:0xf bank_mask:0xf
v_fma_f32 v32,-v78,v84,v32
M 26,a[20:23],46
L 3
v_fma_f32 v33,-v78,v85,v33
v_mul_f32 v94,v30,v30
v_mul_f32 v95,v31,v31
M 18,a[24:27],50
R 70,1,7168
v_mov_b32_dpp v115,v118 row_ror:8 row_mask:0xf bank_mask:0xf
M 22,a[28:31],50
v_fma_f32 v115,-v118,v118,v115
v_max_f32 v115,s87,v115
M 26,a[32:35],50
v_rsq_f32 v116,v115
M 18,a[36:39],54
R 42,1,8192
v_mul_f32 v121,v115,v116
M 22,a[40:43],54
v_mul_f32 v119,s88,v116
v_mul_f32 v115,s89,v116
M 26,a[44:47],54
L 3
v_mul_f32 v120,v119,v116
v_mul_f32 v120,v120,v115
v_mul_f32 v96,v32,v32
M 18,a[48:51],58
R 46,1,9216
v_mul_f32 v97,v33,v33
M 22,a[52:55],58
ds_write_b128 v12,v[118:121] offset:0
M 26,a[56:59],58
v_fma_f32 v94,v80,v94,v79
v_fma_f32 v95,v80,v95,v79
M 18,a[60:63],62
R 50,1,10240
v_fma_f32 v96,v80,v96,v79
M 22,a[64:67],62
v_fma_f32 v97,v80,v97,v79
v_mul_f32 v94,v30,v94
M 26,a[68:71],62
L 4
v_mul_f32 v95,v31,v95
v_exp_f32 v94,v94
M 18,a[72:75],66
R 54,1,11264
v_mul_f32 v96,v32,v96
M 22,a[76:79],66
v_exp_f32 v95,v95
M 26,a[80:83],66
v_exp_f32 v96,v96
M 18,a[84:87],70
R 58,1,12288
v_mul_f32 v97,v33,v97
M 22,a[88:91],70
v_exp_f32 v97,v97
v_fma_f32 v94,v94,v81,v81
M 26,a[92:95],70
L 4
v_rcp_f32 v94,v94
M 18,a[96:99],42
R 62,1,13312
v_fma_f32 v95,v95,v81,v81
M 22,a[100:103],42
v_rcp_f32 v95,v95
M 26,a[104:107],42
v_fma_f32 v96,v96,v81,v81
v_fma_f32 v97,v97,v81,v81
M 18,a[108:111],46
R 66,1,14336
v_mul_f32 v30,v30,v94
M 22,a[112:115],46
v_rcp_f32 v96,v96
v_mul_f32 v31,v31,v95
M 26,a[116:119],46
L 3
v_rcp_f32 v97,v97
M 18,a[120:123],50
R 70,1,15360
v_mul_f32 v32,v32,v96
M 22,a[124:127],50
s_waitcnt vmcnt(0)
v_mul_f32 v33,v33,v97
v_cvt_pk_bf16_f32 v106,v30,v31
M 26,a[128:131],50
s_barrier
ds_read_b32 v7,v6 offset:3072
v_cvt_pk_bf16_f32 v107,v32,v33
M 18,a[132:135],54
R 42,1,16384
v_fma_f32 v34,-v78,v86,v34
M 22,a[136:139],54
buffer_store_dword v4,v3,s[36:39],s70 offen offset:0 sc0 sc1
v_fma_f32 v35,-v78,v87,v35
M 26,a[140:143],54
L 4
v_fma_f32 v36,-v78,v88,v36
v_fma_f32 v37,-v78,v89,v37
M 18,a[144:147],58
s_mov_b32 s70,0x7fff0000
R 46,1,17408
v_mul_f32 v98,v34,v34
M 22,a[148:151],58
L 2
v_mul_f32 v99,v35,v35
v_mul_f32 v100,v36,v36
M 26,a[152:155],58
v_mul_f32 v101,v37,v37
v_cmp_eq_u32 vcc,s53,v7
M 18,a[156:159],62
R 50,1,18432
v_fma_f32 v98,v80,v98,v79
v_fma_f32 v99,v80,v99,v79
M 22,a[160:163],62
s_cmp_eq_u32 vcc_lo,-1
v_fma_f32 v100,v80,v100,v79
v_fma_f32 v101,v80,v101,v79
s_cbranch_scc0 Lg3_X0_%=
M 26,a[164:167],62
s_nop 0
buffer_load_dword v114,v11,s[36:39],s73 offen offset:0 sc1
M 18,a[168:171],66
s_add_u32 s58,s58,s61
R 54,1,19456
v_mul_f32 v98,v34,v98
M 22,a[172:175],66
s_add_u32 m0,s57,0
v_exp_f32 v98,v98
M 26,a[176:179],66
s_add_u32 s56,s55,0
D 0,56,0
M 18,a[180:183],70
R 58,1,20480
v_mul_f32 v99,v35,v99
v_mul_f32 v100,v36,v100
M 22,a[184:187],70
v_exp_f32 v99,v99
M 26,a[188:191],70
L 3
D 0,56,1024
M 18,a[192:195],42
R 62,1,21504
v_mul_f32 v101,v37,v101
M 22,a[196:199],42
v_exp_f32 v100,v100
M 26,a[200:203],42
v_exp_f32 v101,v101
M 18,a[204:207],46
R 66,1,22528
v_fma_f32 v98,v98,v81,v81
v_fma_f32 v99,v99,v81,v81
M 22,a[208:211],46
D 0,56,2048
M 26,a[212:215],46
L 3
v_rcp_f32 v98,v98
M 18,a[216:219],50
R 70,1,23552
v_fma_f32 v100,v100,v81,v81
M 22,a[220:223],50
D 0,56,3072
M 26,a[224:227],50
s_add_u32 m0,s59,0
v_rcp_f32 v99,v99
M 18,a[228:231],54
R 42,1,24576
v_rcp_f32 v100,v100
M 22,a[232:235],54
buffer_load_dword v8,s[36:39],s58 offen offset:0 sc1 lds
M 26,a[236:239],54
L 3
v_fma_f32 v101,v101,v81,v81
v_mul_f32 v34,v34,v98
M 18,a[240:243],58
s_add_u32 m0,s57,16384
R 46,1,25600
v_mul_f32 v35,v35,v99
M 22,a[244:247],58
s_add_u32 s56,s55,16384
D 0,56,0
M 26,a[248:251],58
s_add_u32 s55,s55,s60
v_rcp_f32 v101,v101
v_mul_f32 v36,v36,v100
M 18,a[252:255],62
s_sub_u32 s57,s84,s57
R 50,1,26624
v_mul_f32 v37,v37,v101
M 22,v[128:131],62
s_add_u32 s54,s54,s46
v_cvt_pk_bf16_f32 v108,v34,v35
v_cvt_pk_bf16_f32 v109,v36,v37
M 26,v[132:135],62
L 3
buffer_store_dwordx4 v[106:109],v0,s[36:39],s67 offen offset:0 sc0 sc1
v_fma_f32 v38,-v78,v90,v38
M 18,v[136:139],66
s_add_u32 s73,s73,s64
R 54,1,27648
v_fma_f32 v39,-v78,v91,v39
M 22,v[140:143],66
D 0,56,1024
M 26,v[144:147],66
v_fma_f32 v40,-v78,v92,v40
v_fma_f32 v41,-v78,v93,v41
v_mul_f32 v102,v38,v38
M 18,v[148:151],70
R 58,1,28672
v_mul_f32 v103,v39,v39
M 22,v[152:155],70
v_mul_f32 v104,v40,v40
v_mul_f32 v105,v41,v41
M 26,v[156:159],70
L 3
D 0,56,2048
M 18,v[160:163],42
R 62,1,29696
v_fma_f32 v102,v80,v102,v79
M 22,v[164:167],42
v_fma_f32 v103,v80,v103,v79
v_fma_f32 v104,v80,v104,v79
M 26,v[168:171],42
v_fma_f32 v105,v80,v105,v79
v_mul_f32 v102,v38,v102
v_mul_f32 v103,v39,v103
M 18,v[172:175],46
R 66,1,30720
v_mul_f32 v104,v40,v104
M 22,v[176:179],46
D 0,56,3072
M 26,v[180:183],46
L 3
v_exp_f32 v102,v102
M 18,v[184:187],50
s_nop 0
R 70,1,31744
v_mul_f32 v105,v41,v105
M 22,v[188:191],50
s_waitcnt vmcnt(5)
v_exp_f32 v103,v103
v_fma_f32 v102,v102,v81,v81
M 26,v[192:195],50
s_barrier
R 74,13,0
ds_read_b32 v7,v6 offset:0
M 18,v[196:199],54
R 42,2,0
v_fma_f32 v103,v103,v81,v81
M 22,v[200:203],54
buffer_store_dword v4,v3,s[36:39],s70 offen offset:0 sc0 sc1
M 26,v[204:207],54
L 5
v_exp_f32 v104,v104
M 18,v[208:211],58
s_mov_b32 s70,s69
R 46,2,1024
v_fma_f32 v104,v104,v81,v81
M 22,v[212:215],58
s_mov_b32 s69,s68
v_exp_f32 v105,v105
M 26,v[216:219],58
L 2
v_fma_f32 v105,v105,v81,v81
v_rcp_f32 v102,v102
M 18,v[220:223],62
s_add_u32 s68,s68,s63
R 50,2,2048
v_mul_f32 v38,v38,v102
M 22,v[224:227],62
v_rcp_f32 v103,v103
M 26,v[228:231],62
s_nop 0
v_rcp_f32 v104,v104
M 18,v[232:235],66
R 54,2,3072
v_mul_f32 v39,v39,v103
M 22,v[236:239],66
v_rcp_f32 v105,v105
v_mul_f32 v40,v40,v104
M 26,v[240:243],66
v_mul_f32 v41,v41,v105
v_cvt_pk_bf16_f32 v110,v38,v39
v_cvt_pk_bf16_f32 v111,v40,v41
M 18,v[244:247],70
R 58,2,4096
v_cmp_eq_u32 vcc,s53,v7
M 22,v[248:251],70
s_cmp_eq_u32 vcc_lo,-1
v_permlane16_swap_b32 v110,v112
v_permlane16_swap_b32 v111,v113
M 26,v[252:255],70
L 3
buffer_store_dwordx4 v[110:113],v10,s[36:39],s67 offen offset:0 sc0 sc1
s_cbranch_scc1 Lg3_F1_%=
s_branch Lg3_L1_%=
Lg3_X0_%=:
M 26,a[164:167],62
s_nop 0
v_mul_f32 v98,v34,v98
v_mul_f32 v99,v35,v99
M 18,a[168:171],66
s_add_u32 m0,s59,0
R 54,1,19456
v_mul_f32 v100,v36,v100
M 22,a[172:175],66
buffer_load_dword v8,s[36:39],s58 offen offset:0 sc1 lds
M 26,a[176:179],66
v_exp_f32 v98,v98
M 18,a[180:183],70
R 58,1,20480
v_mul_f32 v101,v37,v101
M 22,a[184:187],70
v_exp_f32 v99,v99
M 26,a[188:191],70
L 3
v_exp_f32 v100,v100
M 18,a[192:195],42
R 62,1,21504
v_fma_f32 v98,v98,v81,v81
M 22,a[196:199],42
v_exp_f32 v101,v101
M 26,a[200:203],42
v_rcp_f32 v98,v98
M 18,a[204:207],46
R 66,1,22528
v_fma_f32 v99,v99,v81,v81
M 22,a[208:211],46
v_rcp_f32 v99,v99
M 26,a[212:215],46
L 3
v_fma_f32 v100,v100,v81,v81
v_fma_f32 v101,v101,v81,v81
M 18,a[216:219],50
R 70,1,23552
v_mul_f32 v34,v34,v98
M 22,a[220:223],50
v_rcp_f32 v100,v100
M 26,a[224:227],50
v_rcp_f32 v101,v101
M 18,a[228:231],54
R 42,1,24576
v_mul_f32 v35,v35,v99
M 22,a[232:235],54
v_mul_f32 v36,v36,v100
v_mul_f32 v37,v37,v101
M 26,a[236:239],54
L 3
v_cvt_pk_bf16_f32 v108,v34,v35
v_cvt_pk_bf16_f32 v109,v36,v37
M 18,a[240:243],58
R 46,1,25600
v_fma_f32 v38,-v78,v90,v38
M 22,a[244:247],58
buffer_store_dwordx4 v[106:109],v0,s[36:39],s67 offen offset:0 sc0 sc1
v_fma_f32 v39,-v78,v91,v39
M 26,a[248:251],58
v_fma_f32 v40,-v78,v92,v40
v_fma_f32 v41,-v78,v93,v41
M 18,a[252:255],62
R 50,1,26624
v_mul_f32 v102,v38,v38
M 22,v[128:131],62
v_mul_f32 v103,v39,v39
v_mul_f32 v104,v40,v40
M 26,v[132:135],62
L 3
v_mul_f32 v105,v41,v41
v_fma_f32 v102,v80,v102,v79
M 18,v[136:139],66
R 54,1,27648
v_fma_f32 v103,v80,v103,v79
M 22,v[140:143],66
v_fma_f32 v104,v80,v104,v79
v_fma_f32 v105,v80,v105,v79
M 26,v[144:147],66
v_mul_f32 v102,v38,v102
v_mul_f32 v103,v39,v103
M 18,v[148:151],70
R 58,1,28672
v_mul_f32 v104,v40,v104
M 22,v[152:155],70
v_exp_f32 v102,v102
M 26,v[156:159],70
L 3
v_exp_f32 v103,v103
M 18,v[160:163],42
R 62,1,29696
v_mul_f32 v105,v41,v105
M 22,v[164:167],42
v_exp_f32 v104,v104
M 26,v[168:171],42
v_exp_f32 v105,v105
M 18,v[172:175],46
R 66,1,30720
v_fma_f32 v102,v102,v81,v81
M 22,v[176:179],46
v_rcp_f32 v102,v102
M 26,v[180:183],46
L 3
v_fma_f32 v103,v103,v81,v81
v_fma_f32 v104,v104,v81,v81
M 18,v[184:187],50
s_nop 0
R 70,1,31744
v_fma_f32 v105,v105,v81,v81
M 22,v[188:191],50
s_waitcnt vmcnt(1)
v_rcp_f32 v103,v103
M 26,v[192:195],50
s_barrier
R 74,13,0
ds_read_b32 v7,v6 offset:0
M 18,v[196:199],54
buffer_store_dword v4,v3,s[36:39],s70 offen offset:0 sc0 sc1
M 22,v[200:203],54
s_mov_b32 s70,s69
v_rcp_f32 v104,v104
M 26,v[204:207],54
L 4
v_rcp_f32 v105,v105
M 18,v[208:211],58
s_mov_b32 s69,s68
v_mul_f32 v38,v38,v102
v_mul_f32 v39,v39,v103
M 22,v[212:215],58
L 0
v_mul_f32 v40,v40,v104
v_mul_f32 v41,v41,v105
M 26,v[216:219],58
s_add_u32 s68,s68,s63
v_cvt_pk_bf16_f32 v110,v38,v39
v_cvt_pk_bf16_f32 v111,v40,v41
M 18,v[220:223],62
v_cmp_eq_u32 vcc,s53,v7
M 22,v[224:227],62
v_permlane16_swap_b32 v110,v112
M 26,v[228:231],62
s_nop 0
v_permlane16_swap_b32 v111,v113
M 18,v[232:235],66
buffer_store_dwordx4 v[110:113],v10,s[36:39],s67 offen offset:0 sc0 sc1
M 22,v[236:239],66
M 26,v[240:243],66
M 18,v[244:247],70
M 22,v[248:251],70
M 26,v[252:255],70
s_mov_b32 s80,1
s_cmp_eq_u32 vcc_lo,-1
s_cbranch_scc0 Lg3_xn0_%=
buffer_load_dword v114,v11,s[36:39],s73 offen offset:0 sc1
s_nop 0
s_nop 0
v_fma_f32 v18,-v74,v82,v18
v_fma_f32 v19,-v74,v83,v19
v_fma_f32 v20,-v74,v84,v20
v_fma_f32 v21,-v74,v85,v21
v_mul_f32 v94,v18,v18
v_mul_f32 v95,v19,v19
v_mul_f32 v96,v20,v20
v_mul_f32 v97,v21,v21
s_add_u32 s73,s73,s64
s_add_u32 m0,s57,0
s_add_u32 s56,s55,0
D 0,56,0
v_fma_f32 v94,v76,v94,v75
v_fma_f32 v95,v76,v95,v75
v_fma_f32 v96,v76,v96,v75
v_fma_f32 v97,v76,v97,v75
)"
R"(v_mul_f32 v94,v18,v94
v_mul_f32 v95,v19,v95
v_mul_f32 v96,v20,v96
v_mul_f32 v97,v21,v97
D 0,56,1024
v_exp_f32 v94,v94
v_exp_f32 v95,v95
v_exp_f32 v96,v96
v_exp_f32 v97,v97
v_fma_f32 v94,v94,v77,v77
v_fma_f32 v95,v95,v77,v77
v_fma_f32 v96,v96,v77,v77
v_fma_f32 v97,v97,v77,v77
D 0,56,2048
v_rcp_f32 v94,v94
v_rcp_f32 v95,v95
v_rcp_f32 v96,v96
v_rcp_f32 v97,v97
v_mul_f32 v18,v18,v94
v_mul_f32 v19,v19,v95
v_mul_f32 v20,v20,v96
v_mul_f32 v21,v21,v97
D 0,56,3072
v_cvt_pk_bf16_f32 v106,v18,v19
v_cvt_pk_bf16_f32 v107,v20,v21
v_fma_f32 v22,-v74,v86,v22
v_fma_f32 v23,-v74,v87,v23
v_fma_f32 v24,-v74,v88,v24
v_fma_f32 v25,-v74,v89,v25
v_mul_f32 v98,v22,v22
v_mul_f32 v99,v23,v23
s_add_u32 m0,s57,16384
s_add_u32 s56,s55,16384
D 0,56,0
v_mul_f32 v100,v24,v24
v_mul_f32 v101,v25,v25
v_fma_f32 v98,v76,v98,v75
v_fma_f32 v99,v76,v99,v75
v_fma_f32 v100,v76,v100,v75
v_fma_f32 v101,v76,v101,v75
v_mul_f32 v98,v22,v98
v_mul_f32 v99,v23,v99
D 0,56,1024
v_mul_f32 v100,v24,v100
v_mul_f32 v101,v25,v101
v_exp_f32 v98,v98
v_exp_f32 v99,v99
v_exp_f32 v100,v100
v_exp_f32 v101,v101
v_fma_f32 v98,v98,v77,v77
v_fma_f32 v99,v99,v77,v77
D 0,56,2048
v_fma_f32 v100,v100,v77,v77
v_fma_f32 v101,v101,v77,v77
v_rcp_f32 v98,v98
v_rcp_f32 v99,v99
v_rcp_f32 v100,v100
v_rcp_f32 v101,v101
v_mul_f32 v22,v22,v98
v_mul_f32 v23,v23,v99
D 0,56,3072
v_mul_f32 v24,v24,v100
v_mul_f32 v25,v25,v101
v_cvt_pk_bf16_f32 v108,v22,v23
v_cvt_pk_bf16_f32 v109,v24,v25
buffer_store_dwordx4 v[106:109],v0,s[36:39],s66 offen offset:0 sc0 sc1
v_fma_f32 v26,-v74,v90,v26
v_fma_f32 v27,-v74,v91,v27
v_fma_f32 v28,-v74,v92,v28
v_fma_f32 v29,-v74,v93,v29
s_add_u32 s55,s55,s60
s_sub_u32 s57,s84,s57
s_add_u32 s54,s54,s46
v_mul_f32 v102,v26,v26
v_mul_f32 v103,v27,v27
v_mul_f32 v104,v28,v28
v_mul_f32 v105,v29,v29
v_fma_f32 v102,v76,v102,v75
v_fma_f32 v103,v76,v103,v75
v_fma_f32 v104,v76,v104,v75
v_fma_f32 v105,v76,v105,v75
v_mul_f32 v102,v26,v102
v_mul_f32 v103,v27,v103
v_mul_f32 v104,v28,v104
v_mul_f32 v105,v29,v105
v_exp_f32 v102,v102
v_exp_f32 v103,v103
v_exp_f32 v104,v104
v_exp_f32 v105,v105
v_fma_f32 v102,v102,v77,v77
v_fma_f32 v103,v103,v77,v77
v_fma_f32 v104,v104,v77,v77
v_fma_f32 v105,v105,v77,v77
v_rcp_f32 v102,v102
v_rcp_f32 v103,v103
v_rcp_f32 v104,v104
v_rcp_f32 v105,v105
v_mul_f32 v26,v26,v102
v_mul_f32 v27,v27,v103
v_mul_f32 v28,v28,v104
v_mul_f32 v29,v29,v105
v_cvt_pk_bf16_f32 v110,v26,v27
v_cvt_pk_bf16_f32 v111,v28,v29
s_nop 0
v_permlane16_swap_b32 v110,v112
v_permlane16_swap_b32 v111,v113
buffer_store_dwordx4 v[110:113],v10,s[36:39],s66 offen offset:0 sc0 sc1
s_mov_b32 s72,s69
s_mov_b32 s69,0x7fff0000
s_mov_b32 s66,0x7fff0000
s_mov_b32 s76,0x7fff0000
s_branch Lg3_gox_%=
Lg3_xn0_%=:
s_cmp_ge_u32 s54,256
s_cbranch_scc1 Lg3_fin0_%=
s_add_u32 m0,s59,4096
s_nop 0
buffer_load_dword v8,s[36:39],s58 offen offset:0 sc1 lds
buffer_store_dword v4,v3,s[36:39],s71 offen offset:0 sc0 sc1
s_add_u32 m0,s59,5120
s_nop 0
buffer_load_dword v8,s[36:39],s58 offen offset:0 sc1 lds
buffer_store_dword v4,v3,s[36:39],s71 offen offset:0 sc0 sc1
s_mov_b32 s83,0
s_nop 0
v_fma_f32 v18,-v74,v82,v18
v_fma_f32 v19,-v74,v83,v19
v_fma_f32 v20,-v74,v84,v20
v_fma_f32 v21,-v74,v85,v21
v_mul_f32 v94,v18,v18
v_mul_f32 v95,v19,v19
v_mul_f32 v96,v20,v20
v_mul_f32 v97,v21,v21
v_fma_f32 v94,v76,v94,v75
v_fma_f32 v95,v76,v95,v75
v_fma_f32 v96,v76,v96,v75
v_fma_f32 v97,v76,v97,v75
v_mul_f32 v94,v18,v94
v_mul_f32 v95,v19,v95
v_mul_f32 v96,v20,v96
v_mul_f32 v97,v21,v97
v_exp_f32 v94,v94
v_exp_f32 v95,v95
v_exp_f32 v96,v96
v_exp_f32 v97,v97
v_fma_f32 v94,v94,v77,v77
v_fma_f32 v95,v95,v77,v77
v_fma_f32 v96,v96,v77,v77
v_fma_f32 v97,v97,v77,v77
v_rcp_f32 v94,v94
v_rcp_f32 v95,v95
v_rcp_f32 v96,v96
v_rcp_f32 v97,v97
v_mul_f32 v18,v18,v94
v_mul_f32 v19,v19,v95
v_mul_f32 v20,v20,v96
v_mul_f32 v21,v21,v97
v_cvt_pk_bf16_f32 v106,v18,v19
v_cvt_pk_bf16_f32 v107,v20,v21
v_fma_f32 v22,-v74,v86,v22
v_fma_f32 v23,-v74,v87,v23
v_fma_f32 v24,-v74,v88,v24
v_fma_f32 v25,-v74,v89,v25
v_mul_f32 v98,v22,v22
v_mul_f32 v99,v23,v23
v_mul_f32 v100,v24,v24
v_mul_f32 v101,v25,v25
v_fma_f32 v98,v76,v98,v75
v_fma_f32 v99,v76,v99,v75
v_fma_f32 v100,v76,v100,v75
v_fma_f32 v101,v76,v101,v75
v_mul_f32 v98,v22,v98
v_mul_f32 v99,v23,v99
v_mul_f32 v100,v24,v100
v_mul_f32 v101,v25,v101
v_exp_f32 v98,v98
v_exp_f32 v99,v99
v_exp_f32 v100,v100
v_exp_f32 v101,v101
v_fma_f32 v98,v98,v77,v77
v_fma_f32 v99,v99,v77,v77
v_fma_f32 v100,v100,v77,v77
v_fma_f32 v101,v101,v77,v77
v_rcp_f32 v98,v98
v_rcp_f32 v99,v99
v_rcp_f32 v100,v100
v_rcp_f32 v101,v101
v_mul_f32 v22,v22,v98
v_mul_f32 v23,v23,v99
v_mul_f32 v24,v24,v100
v_mul_f32 v25,v25,v101
v_cvt_pk_bf16_f32 v108,v22,v23
v_cvt_pk_bf16_f32 v109,v24,v25
buffer_store_dwordx4 v[106:109],v0,s[36:39],s66 offen offset:0 sc0 sc1
v_fma_f32 v26,-v74,v90,v26
v_fma_f32 v27,-v74,v91,v27
v_fma_f32 v28,-v74,v92,v28
v_fma_f32 v29,-v74,v93,v29
v_mul_f32 v102,v26,v26
v_mul_f32 v103,v27,v27
v_mul_f32 v104,v28,v28
v_mul_f32 v105,v29,v29
v_fma_f32 v102,v76,v102,v75
v_fma_f32 v103,v76,v103,v75
v_fma_f32 v104,v76,v104,v75
v_fma_f32 v105,v76,v105,v75
v_mul_f32 v102,v26,v102
v_mul_f32 v103,v27,v103
v_mul_f32 v104,v28,v104
v_mul_f32 v105,v29,v105
v_exp_f32 v102,v102
v_exp_f32 v103,v103
v_exp_f32 v104,v104
v_exp_f32 v105,v105
v_fma_f32 v102,v102,v77,v77
v_fma_f32 v103,v103,v77,v77
v_fma_f32 v104,v104,v77,v77
v_fma_f32 v105,v105,v77,v77
v_rcp_f32 v102,v102
v_rcp_f32 v103,v103
v_rcp_f32 v104,v104
v_rcp_f32 v105,v105
v_mul_f32 v26,v26,v102
v_mul_f32 v27,v27,v103
v_mul_f32 v28,v28,v104
v_mul_f32 v29,v29,v105
v_cvt_pk_bf16_f32 v110,v26,v27
v_cvt_pk_bf16_f32 v111,v28,v29
s_nop 0
v_permlane16_swap_b32 v110,v112
v_permlane16_swap_b32 v111,v113
buffer_store_dwordx4 v[110:113],v10,s[36:39],s66 offen offset:0 sc0 sc1
s_mov_b32 s72,s69
s_mov_b32 s69,0x7fff0000
s_mov_b32 s66,0x7fff0000
s_mov_b32 s76,0x7fff0000
s_branch Lg3_poll_%=
Lg3_fin0_%=:
s_nop 0
v_fma_f32 v18,-v74,v82,v18
v_fma_f32 v19,-v74,v83,v19
v_fma_f32 v20,-v74,v84,v20
v_fma_f32 v21,-v74,v85,v21
v_mul_f32 v94,v18,v18
v_mul_f32 v95,v19,v19
v_mul_f32 v96,v20,v20
v_mul_f32 v97,v21,v21
v_fma_f32 v94,v76,v94,v75
v_fma_f32 v95,v76,v95,v75
v_fma_f32 v96,v76,v96,v75
v_fma_f32 v97,v76,v97,v75
v_mul_f32 v94,v18,v94
v_mul_f32 v95,v19,v95
v_mul_f32 v96,v20,v96
v_mul_f32 v97,v21,v97
v_exp_f32 v94,v94
v_exp_f32 v95,v95
v_exp_f32 v96,v96
v_exp_f32 v97,v97
v_fma_f32 v94,v94,v77,v77
v_fma_f32 v95,v95,v77,v77
v_fma_f32 v96,v96,v77,v77
v_fma_f32 v97,v97,v77,v77
v_rcp_f32 v94,v94
v_rcp_f32 v95,v95
v_rcp_f32 v96,v96
v_rcp_f32 v97,v97
v_mul_f32 v18,v18,v94
v_mul_f32 v19,v19,v95
v_mul_f32 v20,v20,v96
v_mul_f32 v21,v21,v97
v_cvt_pk_bf16_f32 v106,v18,v19
v_cvt_pk_bf16_f32 v107,v20,v21
v_fma_f32 v22,-v74,v86,v22
v_fma_f32 v23,-v74,v87,v23
v_fma_f32 v24,-v74,v88,v24
v_fma_f32 v25,-v74,v89,v25
v_mul_f32 v98,v22,v22
v_mul_f32 v99,v23,v23
v_mul_f32 v100,v24,v24
v_mul_f32 v101,v25,v25
v_fma_f32 v98,v76,v98,v75
v_fma_f32 v99,v76,v99,v75
v_fma_f32 v100,v76,v100,v75
v_fma_f32 v101,v76,v101,v75
v_mul_f32 v98,v22,v98
v_mul_f32 v99,v23,v99
v_mul_f32 v100,v24,v100
v_mul_f32 v101,v25,v101
v_exp_f32 v98,v98
v_exp_f32 v99,v99
v_exp_f32 v100,v100
v_exp_f32 v101,v101
v_fma_f32 v98,v98,v77,v77
v_fma_f32 v99,v99,v77,v77
v_fma_f32 v100,v100,v77,v77
v_fma_f32 v101,v101,v77,v77
v_rcp_f32 v98,v98
v_rcp_f32 v99,v99
v_rcp_f32 v100,v100
v_rcp_f32 v101,v101
v_mul_f32 v22,v22,v98
v_mul_f32 v23,v23,v99
v_mul_f32 v24,v24,v100
v_mul_f32 v25,v25,v101
v_cvt_pk_bf16_f32 v108,v22,v23
v_cvt_pk_bf16_f32 v109,v24,v25
buffer_store_dwordx4 v[106:109],v0,s[36:39],s66 offen offset:0 sc0 sc1
v_fma_f32 v26,-v74,v90,v26
v_fma_f32 v27,-v74,v91,v27
v_fma_f32 v28,-v74,v92,v28
v_fma_f32 v29,-v74,v93,v29
v_mul_f32 v102,v26,v26
v_mul_f32 v103,v27,v27
v_mul_f32 v104,v28,v28
v_mul_f32 v105,v29,v29
v_fma_f32 v102,v76,v102,v75
v_fma_f32 v103,v76,v103,v75
v_fma_f32 v104,v76,v104,v75
v_fma_f32 v105,v76,v105,v75
v_mul_f32 v102,v26,v102
v_mul_f32 v103,v27,v103
v_mul_f32 v104,v28,v104
v_mul_f32 v105,v29,v105
v_exp_f32 v102,v102
v_exp_f32 v103,v103
v_exp_f32 v104,v104
v_exp_f32 v105,v105
v_fma_f32 v102,v102,v77,v77
v_fma_f32 v103,v103,v77,v77
v_fma_f32 v104,v104,v77,v77
v_fma_f32 v105,v105,v77,v77
v_rcp_f32 v102,v102
v_rcp_f32 v103,v103
v_rcp_f32 v104,v104
v_rcp_f32 v105,v105
v_mul_f32 v26,v26,v102
v_mul_f32 v27,v27,v103
v_mul_f32 v28,v28,v104
v_mul_f32 v29,v29,v105
v_cvt_pk_bf16_f32 v110,v26,v27
v_cvt_pk_bf16_f32 v111,v28,v29
s_nop 0
v_permlane16_swap_b32 v110,v112
v_permlane16_swap_b32 v111,v113
buffer_store_dwordx4 v[110:113],v10,s[36:39],s66 offen offset:0 sc0 sc1
s_mov_b32 s72,s69
s_mov_b32 s69,0x7fff0000
s_mov_b32 s66,0x7fff0000
s_mov_b32 s76,0x7fff0000
s_branch Lg3_done_%=
Lg3_L1_%=:
v_mfma_f32_16x16x32_bf16 v[30:33],a[0:3],v[42:45],0
s_add_u32 m0,s59,3072
R 62,2,5120
v_add_f32_dpp v115,v114,v114 quad_perm:[1,0,3,2] row_mask:0xf bank_mask:0xf
v_mfma_f32_16x16x32_bf16 v[34:37],a[4:7],v[42:45],0
s_mov_b32 s67,s65
buffer_load_dword v8,s[36:39],s58 offen offset:0 sc1 lds
v_mfma_f32_16x16x32_bf16 v[38:41],a[8:11],v[42:45],0
s_add_u32 s65,s65,s62
v_add_f32_dpp v115,v115,v115 quad_perm:[2,3,0,1] row_mask:0xf bank_mask:0xf
v_fma_f32 v18,-v74,v82,v18
M 30,a[12:15],46
R 66,2,6144
v_fma_f32 v19,-v74,v83,v19
M 34,a[16:19],46
v_add_f32_dpp v118,v115,v115 row_half_mirror row_mask:0xf bank_mask:0xf
v_fma_f32 v20,-v74,v84,v20
M 38,a[20:23],46
L 3
v_fma_f32 v21,-v74,v85,v21
v_mul_f32 v94,v18,v18
v_mul_f32 v95,v19,v19
M 30,a[24:27],50
R 70,2,7168
v_mov_b32_dpp v115,v118 row_ror:8 row_mask:0xf bank_mask:0xf
M 34,a[28:31],50
v_fma_f32 v115,-v118,v118,v115
v_max_f32 v115,s87,v115
M 38,a[32:35],50
v_rsq_f32 v116,v115
M 30,a[36:39],54
R 42,2,8192
v_mul_f32 v121,v115,v116
M 34,a[40:43],54
v_mul_f32 v119,s88,v116
v_mul_f32 v115,s89,v116
M 38,a[44:47],54
L 3
v_mul_f32 v120,v119,v116
v_mul_f32 v120,v120,v115
v_mul_f32 v96,v20,v20
M 30,a[48:51],58
R 46,2,9216
v_mul_f32 v97,v21,v21
M 34,a[52:55],58
ds_write_b128 v12,v[118:121] offset:2048
M 38,a[56:59],58
v_fma_f32 v94,v76,v94,v75
v_fma_f32 v95,v76,v95,v75
M 30,a[60:63],62
R 50,2,10240
v_fma_f32 v96,v76,v96,v75
M 34,a[64:67],62
v_fma_f32 v97,v76,v97,v75
v_mul_f32 v94,v18,v94
M 38,a[68:71],62
L 4
v_mul_f32 v95,v19,v95
v_exp_f32 v94,v94
M 30,a[72:75],66
R 54,2,11264
v_mul_f32 v96,v20,v96
M 34,a[76:79],66
v_exp_f32 v95,v95
M 38,a[80:83],66
v_exp_f32 v96,v96
M 30,a[84:87],70
R 58,2,12288
v_mul_f32 v97,v21,v97
M 34,a[88:91],70
v_exp_f32 v97,v97
v_fma_f32 v94,v94,v77,v77
M 38,a[92:95],70
L 4
v_rcp_f32 v94,v94
M 30,a[96:99],42
R 62,2,13312
v_fma_f32 v95,v95,v77,v77
M 34,a[100:103],42
v_rcp_f32 v95,v95
M 38,a[104:107],42
v_fma_f32 v96,v96,v77,v77
v_fma_f32 v97,v97,v77,v77
M 30,a[108:111],46
R 66,2,14336
v_mul_f32 v18,v18,v94
M 34,a[112:115],46
v_rcp_f32 v96,v96
v_mul_f32 v19,v19,v95
M 38,a[116:119],46
L 3
v_rcp_f32 v97,v97
M 30,a[120:123],50
R 70,2,15360
v_mul_f32 v20,v20,v96
M 34,a[124:127],50
s_waitcnt vmcnt(0)
v_mul_f32 v21,v21,v97
v_cvt_pk_bf16_f32 v106,v18,v19
M 38,a[128:131],50
s_barrier
ds_read_b32 v7,v6 offset:3072
v_cvt_pk_bf16_f32 v107,v20,v21
M 30,a[132:135],54
R 42,2,16384
v_fma_f32 v22,-v74,v86,v22
M 34,a[136:139],54
buffer_store_dword v4,v3,s[36:39],s70 offen offset:0 sc0 sc1
v_fma_f32 v23,-v74,v87,v23
M 38,a[140:143],54
L 4
v_fma_f32 v24,-v74,v88,v24
v_fma_f32 v25,-v74,v89,v25
M 30,a[144:147],58
s_mov_b32 s70,0x7fff0000
R 46,2,17408
v_mul_f32 v98,v22,v22
M 34,a[148:151],58
L 2
v_mul_f32 v99,v23,v23
v_mul_f32 v100,v24,v24
M 38,a[152:155],58
v_mul_f32 v101,v25,v25
v_cmp_eq_u32 vcc,s53,v7
M 30,a[156:159],62
R 50,2,18432
v_fma_f32 v98,v76,v98,v75
v_fma_f32 v99,v76,v99,v75
M 34,a[160:163],62
s_cmp_eq_u32 vcc_lo,-1
v_fma_f32 v100,v76,v100,v75
v_fma_f32 v101,v76,v101,v75
s_cbranch_scc0 Lg3_X1_%=
M 38,a[164:167],62
s_nop 0
buffer_load_dword v114,v11,s[36:39],s73 offen offset:0 sc1
M 30,a[168:171],66
s_add_u32 s58,s58,s61
R 54,2,19456
v_mul_f32 v98,v22,v98
M 34,a[172:175],66
s_add_u32 m0,s57,0
v_exp_f32 v98,v98
M 38,a[176:179],66
s_add_u32 s56,s55,0
D 0,56,0
M 30,a[180:183],70
R 58,2,20480
)"
R"(v_mul_f32 v99,v23,v99
v_mul_f32 v100,v24,v100
M 34,a[184:187],70
v_exp_f32 v99,v99
M 38,a[188:191],70
L 3
D 0,56,1024
M 30,a[192:195],42
R 62,2,21504
v_mul_f32 v101,v25,v101
M 34,a[196:199],42
v_exp_f32 v100,v100
M 38,a[200:203],42
v_exp_f32 v101,v101
M 30,a[204:207],46
R 66,2,22528
v_fma_f32 v98,v98,v77,v77
v_fma_f32 v99,v99,v77,v77
M 34,a[208:211],46
D 0,56,2048
M 38,a[212:215],46
L 3
v_rcp_f32 v98,v98
M 30,a[216:219],50
R 70,2,23552
v_fma_f32 v100,v100,v77,v77
M 34,a[220:223],50
D 0,56,3072
M 38,a[224:227],50
s_add_u32 m0,s59,1024
v_rcp_f32 v99,v99
M 30,a[228:231],54
R 42,2,24576
v_rcp_f32 v100,v100
M 34,a[232:235],54
buffer_load_dword v8,s[36:39],s58 offen offset:0 sc1 lds
M 38,a[236:239],54
L 3
v_fma_f32 v101,v101,v77,v77
v_mul_f32 v22,v22,v98
M 30,a[240:243],58
s_add_u32 m0,s57,16384
R 46,2,25600
v_mul_f32 v23,v23,v99
M 34,a[244:247],58
s_add_u32 s56,s55,16384
D 0,56,0
M 38,a[248:251],58
s_add_u32 s55,s55,s60
v_rcp_f32 v101,v101
v_mul_f32 v24,v24,v100
M 30,a[252:255],62
s_sub_u32 s57,s84,s57
R 50,2,26624
v_mul_f32 v25,v25,v101
M 34,v[128:131],62
s_add_u32 s54,s54,s46
v_cvt_pk_bf16_f32 v108,v22,v23
v_cvt_pk_bf16_f32 v109,v24,v25
M 38,v[132:135],62
L 3
buffer_store_dwordx4 v[106:109],v0,s[36:39],s66 offen offset:0 sc0 sc1
v_fma_f32 v26,-v74,v90,v26
M 30,v[136:139],66
s_add_u32 s73,s73,s64
R 54,2,27648
v_fma_f32 v27,-v74,v91,v27
M 34,v[140:143],66
D 0,56,1024
M 38,v[144:147],66
v_fma_f32 v28,-v74,v92,v28
v_fma_f32 v29,-v74,v93,v29
v_mul_f32 v102,v26,v26
M 30,v[148:151],70
R 58,2,28672
v_mul_f32 v103,v27,v27
M 34,v[152:155],70
v_mul_f32 v104,v28,v28
v_mul_f32 v105,v29,v29
M 38,v[156:159],70
L 3
D 0,56,2048
M 30,v[160:163],42
R 62,2,29696
v_fma_f32 v102,v76,v102,v75
M 34,v[164:167],42
v_fma_f32 v103,v76,v103,v75
v_fma_f32 v104,v76,v104,v75
M 38,v[168:171],42
v_fma_f32 v105,v76,v105,v75
v_mul_f32 v102,v26,v102
v_mul_f32 v103,v27,v103
M 30,v[172:175],46
R 66,2,30720
v_mul_f32 v104,v28,v104
M 34,v[176:179],46
D 0,56,3072
M 38,v[180:183],46
L 3
v_exp_f32 v102,v102
M 30,v[184:187],50
s_nop 0
R 70,2,31744
v_mul_f32 v105,v29,v105
M 34,v[188:191],50
s_waitcnt vmcnt(5)
v_exp_f32 v103,v103
v_fma_f32 v102,v102,v77,v77
M 38,v[192:195],50
s_barrier
R 78,13,2048
ds_read_b32 v7,v6 offset:1024
M 30,v[196:199],54
R 42,1,0
v_fma_f32 v103,v103,v77,v77
M 34,v[200:203],54
buffer_store_dword v4,v3,s[36:39],s70 offen offset:0 sc0 sc1
M 38,v[204:207],54
L 5
v_exp_f32 v104,v104
M 30,v[208:211],58
s_mov_b32 s70,s69
R 46,1,1024
v_fma_f32 v104,v104,v77,v77
M 34,v[212:215],58
s_mov_b32 s69,s68
v_exp_f32 v105,v105
M 38,v[216:219],58
L 2
v_fma_f32 v105,v105,v77,v77
v_rcp_f32 v102,v102
M 30,v[220:223],62
s_add_u32 s68,s68,s63
R 50,1,2048
v_mul_f32 v26,v26,v102
M 34,v[224:227],62
v_rcp_f32 v103,v103
M 38,v[228:231],62
s_nop 0
v_rcp_f32 v104,v104
M 30,v[232:235],66
R 54,1,3072
v_mul_f32 v27,v27,v103
M 34,v[236:239],66
v_rcp_f32 v105,v105
v_mul_f32 v28,v28,v104
M 38,v[240:243],66
v_mul_f32 v29,v29,v105
v_cvt_pk_bf16_f32 v110,v26,v27
v_cvt_pk_bf16_f32 v111,v28,v29
M 30,v[244:247],70
R 58,1,4096
v_cmp_eq_u32 vcc,s53,v7
M 34,v[248:251],70
s_cmp_eq_u32 vcc_lo,-1
v_permlane16_swap_b32 v110,v112
v_permlane16_swap_b32 v111,v113
M 38,v[252:255],70
L 3
buffer_store_dwordx4 v[110:113],v10,s[36:39],s66 offen offset:0 sc0 sc1
s_cbranch_scc1 Lg3_F0_%=
s_branch Lg3_L0_%=
Lg3_X1_%=:
M 38,a[164:167],62
s_nop 0
v_mul_f32 v98,v22,v98
v_mul_f32 v99,v23,v99
M 30,a[168:171],66
s_add_u32 m0,s59,1024
R 54,2,19456
v_mul_f32 v100,v24,v100
M 34,a[172:175],66
buffer_load_dword v8,s[36:39],s58 offen offset:0 sc1 lds
M 38,a[176:179],66
v_exp_f32 v98,v98
M 30,a[180:183],70
R 58,2,20480
v_mul_f32 v101,v25,v101
M 34,a[184:187],70
v_exp_f32 v99,v99
M 38,a[188:191],70
L 3
v_exp_f32 v100,v100
M 30,a[192:195],42
R 62,2,21504
v_fma_f32 v98,v98,v77,v77
M 34,a[196:199],42
v_exp_f32 v101,v101
M 38,a[200:203],42
v_rcp_f32 v98,v98
M 30,a[204:207],46
R 66,2,22528
v_fma_f32 v99,v99,v77,v77
M 34,a[208:211],46
v_rcp_f32 v99,v99
M 38,a[212:215],46
L 3
v_fma_f32 v100,v100,v77,v77
v_fma_f32 v101,v101,v77,v77
M 30,a[216:219],50
R 70,2,23552
v_mul_f32 v22,v22,v98
M 34,a[220:223],50
v_rcp_f32 v100,v100
M 38,a[224:227],50
v_rcp_f32 v101,v101
M 30,a[228:231],54
R 42,2,24576
v_mul_f32 v23,v23,v99
M 34,a[232:235],54
v_mul_f32 v24,v24,v100
v_mul_f32 v25,v25,v101
M 38,a[236:239],54
L 3
v_cvt_pk_bf16_f32 v108,v22,v23
v_cvt_pk_bf16_f32 v109,v24,v25
M 30,a[240:243],58
R 46,2,25600
v_fma_f32 v26,-v74,v90,v26
M 34,a[244:247],58
buffer_store_dwordx4 v[106:109],v0,s[36:39],s66 offen offset:0 sc0 sc1
v_fma_f32 v27,-v74,v91,v27
M 38,a[248:251],58
v_fma_f32 v28,-v74,v92,v28
v_fma_f32 v29,-v74,v93,v29
M 30,a[252:255],62
R 50,2,26624
v_mul_f32 v102,v26,v26
M 34,v[128:131],62
v_mul_f32 v103,v27,v27
v_mul_f32 v104,v28,v28
M 38,v[132:135],62
L 3
v_mul_f32 v105,v29,v29
v_fma_f32 v102,v76,v102,v75
M 30,v[136:139],66
R 54,2,27648
v_fma_f32 v103,v76,v103,v75
M 34,v[140:143],66
v_fma_f32 v104,v76,v104,v75
v_fma_f32 v105,v76,v105,v75
M 38,v[144:147],66
v_mul_f32 v102,v26,v102
v_mul_f32 v103,v27,v103
M 30,v[148:151],70
R 58,2,28672
v_mul_f32 v104,v28,v104
M 34,v[152:155],70
v_exp_f32 v102,v102
M 38,v[156:159],70
L 3
v_exp_f32 v103,v103
M 30,v[160:163],42
R 62,2,29696
v_mul_f32 v105,v29,v105
M 34,v[164:167],42
v_exp_f32 v104,v104
M 38,v[168:171],42
v_exp_f32 v105,v105
M 30,v[172:175],46
R 66,2,30720
v_fma_f32 v102,v102,v77,v77
M 34,v[176:179],46
v_rcp_f32 v102,v102
M 38,v[180:183],46
L 3
v_fma_f32 v103,v103,v77,v77
v_fma_f32 v104,v104,v77,v77
M 30,v[184:187],50
s_nop 0
R 70,2,31744
v_fma_f32 v105,v105,v77,v77
M 34,v[188:191],50
s_waitcnt vmcnt(1)
v_rcp_f32 v103,v103
M 38,v[192:195],50
s_barrier
R 78,13,2048
ds_read_b32 v7,v6 offset:1024
M 30,v[196:199],54
buffer_store_dword v4,v3,s[36:39],s70 offen offset:0 sc0 sc1
M 34,v[200:203],54
s_mov_b32 s70,s69
v_rcp_f32 v104,v104
M 38,v[204:207],54
L 4
v_rcp_f32 v105,v105
M 30,v[208:211],58
s_mov_b32 s69,s68
v_mul_f32 v26,v26,v102
v_mul_f32 v27,v27,v103
M 34,v[212:215],58
L 0
v_mul_f32 v28,v28,v104
v_mul_f32 v29,v29,v105
M 38,v[216:219],58
s_add_u32 s68,s68,s63
v_cvt_pk_bf16_f32 v110,v26,v27
v_cvt_pk_bf16_f32 v111,v28,v29
M 30,v[220:223],62
v_cmp_eq_u32 vcc,s53,v7
M 34,v[224:227],62
v_permlane16_swap_b32 v110,v112
M 38,v[228:231],62
s_nop 0
v_permlane16_swap_b32 v111,v113
M 30,v[232:235],66
buffer_store_dwordx4 v[110:113],v10,s[36:39],s66 offen offset:0 sc0 sc1
M 34,v[236:239],66
M 38,v[240:243],66
M 30,v[244:247],70
M 34,v[248:251],70
M 38,v[252:255],70
s_mov_b32 s80,0
s_cmp_eq_u32 vcc_lo,-1
s_cbranch_scc0 Lg3_xn1_%=
buffer_load_dword v114,v11,s[36:39],s73 offen offset:0 sc1
s_nop 0
s_nop 0
v_fma_f32 v30,-v78,v82,v30
v_fma_f32 v31,-v78,v83,v31
v_fma_f32 v32,-v78,v84,v32
v_fma_f32 v33,-v78,v85,v33
v_mul_f32 v94,v30,v30
v_mul_f32 v95,v31,v31
v_mul_f32 v96,v32,v32
v_mul_f32 v97,v33,v33
s_add_u32 s73,s73,s64
s_add_u32 m0,s57,0
s_add_u32 s56,s55,0
D 0,56,0
v_fma_f32 v94,v80,v94,v79
v_fma_f32 v95,v80,v95,v79
v_fma_f32 v96,v80,v96,v79
v_fma_f32 v97,v80,v97,v79
v_mul_f32 v94,v30,v94
v_mul_f32 v95,v31,v95
v_mul_f32 v96,v32,v96
v_mul_f32 v97,v33,v97
D 0,56,1024
v_exp_f32 v94,v94
v_exp_f32 v95,v95
v_exp_f32 v96,v96
v_exp_f32 v97,v97
v_fma_f32 v94,v94,v81,v81
v_fma_f32 v95,v95,v81,v81
v_fma_f32 v96,v96,v81,v81
v_fma_f32 v97,v97,v81,v81
D 0,56,2048
v_rcp_f32 v94,v94
v_rcp_f32 v95,v95
v_rcp_f32 v96,v96
v_rcp_f32 v97,v97
v_mul_f32 v30,v30,v94
v_mul_f32 v31,v31,v95
v_mul_f32 v32,v32,v96
v_mul_f32 v33,v33,v97
D 0,56,3072
v_cvt_pk_bf16_f32 v106,v30,v31
v_cvt_pk_bf16_f32 v107,v32,v33
v_fma_f32 v34,-v78,v86,v34
v_fma_f32 v35,-v78,v87,v35
v_fma_f32 v36,-v78,v88,v36
v_fma_f32 v37,-v78,v89,v37
v_mul_f32 v98,v34,v34
v_mul_f32 v99,v35,v35
s_add_u32 m0,s57,16384
s_add_u32 s56,s55,16384
D 0,56,0
v_mul_f32 v100,v36,v36
v_mul_f32 v101,v37,v37
v_fma_f32 v98,v80,v98,v79
v_fma_f32 v99,v80,v99,v79
v_fma_f32 v100,v80,v100,v79
v_fma_f32 v101,v80,v101,v79
v_mul_f32 v98,v34,v98
v_mul_f32 v99,v35,v99
D 0,56,1024
v_mul_f32 v100,v36,v100
v_mul_f32 v101,v37,v101
v_exp_f32 v98,v98
v_exp_f32 v99,v99
v_exp_f32 v100,v100
v_exp_f32 v101,v101
v_fma_f32 v98,v98,v81,v81
v_fma_f32 v99,v99,v81,v81
D 0,56,2048
v_fma_f32 v100,v100,v81,v81
v_fma_f32 v101,v101,v81,v81
v_rcp_f32 v98,v98
v_rcp_f32 v99,v99
v_rcp_f32 v100,v100
v_rcp_f32 v101,v101
v_mul_f32 v34,v34,v98
v_mul_f32 v35,v35,v99
D 0,56,3072
v_mul_f32 v36,v36,v100
v_mul_f32 v37,v37,v101
v_cvt_pk_bf16_f32 v108,v34,v35
v_cvt_pk_bf16_f32 v109,v36,v37
buffer_store_dwordx4 v[106:109],v0,s[36:39],s67 offen offset:0 sc0 sc1
v_fma_f32 v38,-v78,v90,v38
v_fma_f32 v39,-v78,v91,v39
v_fma_f32 v40,-v78,v92,v40
v_fma_f32 v41,-v78,v93,v41
s_add_u32 s55,s55,s60
s_sub_u32 s57,s84,s57
s_add_u32 s54,s54,s46
v_mul_f32 v102,v38,v38
v_mul_f32 v103,v39,v39
v_mul_f32 v104,v40,v40
v_mul_f32 v105,v41,v41
v_fma_f32 v102,v80,v102,v79
v_fma_f32 v103,v80,v103,v79
v_fma_f32 v104,v80,v104,v79
v_fma_f32 v105,v80,v105,v79
v_mul_f32 v102,v38,v102
v_mul_f32 v103,v39,v103
v_mul_f32 v104,v40,v104
v_mul_f32 v105,v41,v105
v_exp_f32 v102,v102
v_exp_f32 v103,v103
v_exp_f32 v104,v104
v_exp_f32 v105,v105
v_fma_f32 v102,v102,v81,v81
v_fma_f32 v103,v103,v81,v81
v_fma_f32 v104,v104,v81,v81
v_fma_f32 v105,v105,v81,v81
v_rcp_f32 v102,v102
v_rcp_f32 v103,v103
v_rcp_f32 v104,v104
v_rcp_f32 v105,v105
v_mul_f32 v38,v38,v102
v_mul_f32 v39,v39,v103
v_mul_f32 v40,v40,v104
v_mul_f32 v41,v41,v105
v_cvt_pk_bf16_f32 v110,v38,v39
v_cvt_pk_bf16_f32 v111,v40,v41
s_nop 0
v_permlane16_swap_b32 v110,v112
v_permlane16_swap_b32 v111,v113
buffer_store_dwordx4 v[110:113],v10,s[36:39],s67 offen offset:0 sc0 sc1
s_mov_b32 s72,s69
s_mov_b32 s69,0x7fff0000
s_mov_b32 s67,0x7fff0000
s_mov_b32 s77,0x7fff0000
s_branch Lg3_gox_%=
Lg3_xn1_%=:
s_cmp_ge_u32 s54,256
s_cbranch_scc1 Lg3_fin1_%=
s_add_u32 m0,s59,4096
s_nop 0
buffer_load_dword v8,s[36:39],s58 offen offset:0 sc1 lds
buffer_store_dword v4,v3,s[36:39],s71 offen offset:0 sc0 sc1
s_add_u32 m0,s59,5120
s_nop 0
buffer_load_dword v8,s[36:39],s58 offen offset:0 sc1 lds
buffer_store_dword v4,v3,s[36:39],s71 offen offset:0 sc0 sc1
s_mov_b32 s83,0
s_nop 0
v_fma_f32 v30,-v78,v82,v30
v_fma_f32 v31,-v78,v83,v31
v_fma_f32 v32,-v78,v84,v32
v_fma_f32 v33,-v78,v85,v33
v_mul_f32 v94,v30,v30
v_mul_f32 v95,v31,v31
v_mul_f32 v96,v32,v32
v_mul_f32 v97,v33,v33
v_fma_f32 v94,v80,v94,v79
v_fma_f32 v95,v80,v95,v79
v_fma_f32 v96,v80,v96,v79
v_fma_f32 v97,v80,v97,v79
v_mul_f32 v94,v30,v94
v_mul_f32 v95,v31,v95
v_mul_f32 v96,v32,v96
v_mul_f32 v97,v33,v97
v_exp_f32 v94,v94
v_exp_f32 v95,v95
v_exp_f32 v96,v96
v_exp_f32 v97,v97
v_fma_f32 v94,v94,v81,v81
v_fma_f32 v95,v95,v81,v81
v_fma_f32 v96,v96,v81,v81
v_fma_f32 v97,v97,v81,v81
v_rcp_f32 v94,v94
v_rcp_f32 v95,v95
v_rcp_f32 v96,v96
v_rcp_f32 v97,v97
v_mul_f32 v30,v30,v94
v_mul_f32 v31,v31,v95
v_mul_f32 v32,v32,v96
v_mul_f32 v33,v33,v97
v_cvt_pk_bf16_f32 v106,v30,v31
v_cvt_pk_bf16_f32 v107,v32,v33
v_fma_f32 v34,-v78,v86,v34
v_fma_f32 v35,-v78,v87,v35
v_fma_f32 v36,-v78,v88,v36
v_fma_f32 v37,-v78,v89,v37
v_mul_f32 v98,v34,v34
v_mul_f32 v99,v35,v35
)"
R"(v_mul_f32 v100,v36,v36
v_mul_f32 v101,v37,v37
v_fma_f32 v98,v80,v98,v79
v_fma_f32 v99,v80,v99,v79
v_fma_f32 v100,v80,v100,v79
v_fma_f32 v101,v80,v101,v79
v_mul_f32 v98,v34,v98
v_mul_f32 v99,v35,v99
v_mul_f32 v100,v36,v100
v_mul_f32 v101,v37,v101
v_exp_f32 v98,v98
v_exp_f32 v99,v99
v_exp_f32 v100,v100
v_exp_f32 v101,v101
v_fma_f32 v98,v98,v81,v81
v_fma_f32 v99,v99,v81,v81
v_fma_f32 v100,v100,v81,v81
v_fma_f32 v101,v101,v81,v81
v_rcp_f32 v98,v98
v_rcp_f32 v99,v99
v_rcp_f32 v100,v100
v_rcp_f32 v101,v101
v_mul_f32 v34,v34,v98
v_mul_f32 v35,v35,v99
v_mul_f32 v36,v36,v100
v_mul_f32 v37,v37,v101
v_cvt_pk_bf16_f32 v108,v34,v35
v_cvt_pk_bf16_f32 v109,v36,v37
buffer_store_dwordx4 v[106:109],v0,s[36:39],s67 offen offset:0 sc0 sc1
v_fma_f32 v38,-v78,v90,v38
v_fma_f32 v39,-v78,v91,v39
v_fma_f32 v40,-v78,v92,v40
v_fma_f32 v41,-v78,v93,v41
v_mul_f32 v102,v38,v38
v_mul_f32 v103,v39,v39
v_mul_f32 v104,v40,v40
v_mul_f32 v105,v41,v41
v_fma_f32 v102,v80,v102,v79
v_fma_f32 v103,v80,v103,v79
v_fma_f32 v104,v80,v104,v79
v_fma_f32 v105,v80,v105,v79
v_mul_f32 v102,v38,v102
v_mul_f32 v103,v39,v103
v_mul_f32 v104,v40,v104
v_mul_f32 v105,v41,v105
v_exp_f32 v102,v102
v_exp_f32 v103,v103
v_exp_f32 v104,v104
v_exp_f32 v105,v105
v_fma_f32 v102,v102,v81,v81
v_fma_f32 v103,v103,v81,v81
v_fma_f32 v104,v104,v81,v81
v_fma_f32 v105,v105,v81,v81
v_rcp_f32 v102,v102
v_rcp_f32 v103,v103
v_rcp_f32 v104,v104
v_rcp_f32 v105,v105
v_mul_f32 v38,v38,v102
v_mul_f32 v39,v39,v103
v_mul_f32 v40,v40,v104
v_mul_f32 v41,v41,v105
v_cvt_pk_bf16_f32 v110,v38,v39
v_cvt_pk_bf16_f32 v111,v40,v41
s_nop 0
v_permlane16_swap_b32 v110,v112
v_permlane16_swap_b32 v111,v113
buffer_store_dwordx4 v[110:113],v10,s[36:39],s67 offen offset:0 sc0 sc1
s_mov_b32 s72,s69
s_mov_b32 s69,0x7fff0000
s_mov_b32 s67,0x7fff0000
s_mov_b32 s77,0x7fff0000
s_branch Lg3_poll_%=
Lg3_fin1_%=:
s_nop 0
v_fma_f32 v30,-v78,v82,v30
v_fma_f32 v31,-v78,v83,v31
v_fma_f32 v32,-v78,v84,v32
v_fma_f32 v33,-v78,v85,v33
v_mul_f32 v94,v30,v30
v_mul_f32 v95,v31,v31
v_mul_f32 v96,v32,v32
v_mul_f32 v97,v33,v33
v_fma_f32 v94,v80,v94,v79
v_fma_f32 v95,v80,v95,v79
v_fma_f32 v96,v80,v96,v79
v_fma_f32 v97,v80,v97,v79
v_mul_f32 v94,v30,v94
v_mul_f32 v95,v31,v95
v_mul_f32 v96,v32,v96
v_mul_f32 v97,v33,v97
v_exp_f32 v94,v94
v_exp_f32 v95,v95
v_exp_f32 v96,v96
v_exp_f32 v97,v97
v_fma_f32 v94,v94,v81,v81
v_fma_f32 v95,v95,v81,v81
v_fma_f32 v96,v96,v81,v81
v_fma_f32 v97,v97,v81,v81
v_rcp_f32 v94,v94
v_rcp_f32 v95,v95
v_rcp_f32 v96,v96
v_rcp_f32 v97,v97
v_mul_f32 v30,v30,v94
v_mul_f32 v31,v31,v95
v_mul_f32 v32,v32,v96
v_mul_f32 v33,v33,v97
v_cvt_pk_bf16_f32 v106,v30,v31
v_cvt_pk_bf16_f32 v107,v32,v33
v_fma_f32 v34,-v78,v86,v34
v_fma_f32 v35,-v78,v87,v35
v_fma_f32 v36,-v78,v88,v36
v_fma_f32 v37,-v78,v89,v37
v_mul_f32 v98,v34,v34
v_mul_f32 v99,v35,v35
v_mul_f32 v100,v36,v36
v_mul_f32 v101,v37,v37
v_fma_f32 v98,v80,v98,v79
v_fma_f32 v99,v80,v99,v79
v_fma_f32 v100,v80,v100,v79
v_fma_f32 v101,v80,v101,v79
v_mul_f32 v98,v34,v98
v_mul_f32 v99,v35,v99
v_mul_f32 v100,v36,v100
v_mul_f32 v101,v37,v101
v_exp_f32 v98,v98
v_exp_f32 v99,v99
v_exp_f32 v100,v100
v_exp_f32 v101,v101
v_fma_f32 v98,v98,v81,v81
v_fma_f32 v99,v99,v81,v81
v_fma_f32 v100,v100,v81,v81
v_fma_f32 v101,v101,v81,v81
v_rcp_f32 v98,v98
v_rcp_f32 v99,v99
v_rcp_f32 v100,v100
v_rcp_f32 v101,v101
v_mul_f32 v34,v34,v98
v_mul_f32 v35,v35,v99
v_mul_f32 v36,v36,v100
v_mul_f32 v37,v37,v101
v_cvt_pk_bf16_f32 v108,v34,v35
v_cvt_pk_bf16_f32 v109,v36,v37
buffer_store_dwordx4 v[106:109],v0,s[36:39],s67 offen offset:0 sc0 sc1
v_fma_f32 v38,-v78,v90,v38
v_fma_f32 v39,-v78,v91,v39
v_fma_f32 v40,-v78,v92,v40
v_fma_f32 v41,-v78,v93,v41
v_mul_f32 v102,v38,v38
v_mul_f32 v103,v39,v39
v_mul_f32 v104,v40,v40
v_mul_f32 v105,v41,v41
v_fma_f32 v102,v80,v102,v79
v_fma_f32 v103,v80,v103,v79
v_fma_f32 v104,v80,v104,v79
v_fma_f32 v105,v80,v105,v79
v_mul_f32 v102,v38,v102
v_mul_f32 v103,v39,v103
v_mul_f32 v104,v40,v104
v_mul_f32 v105,v41,v105
v_exp_f32 v102,v102
v_exp_f32 v103,v103
v_exp_f32 v104,v104
v_exp_f32 v105,v105
v_fma_f32 v102,v102,v81,v81
v_fma_f32 v103,v103,v81,v81
v_fma_f32 v104,v104,v81,v81
v_fma_f32 v105,v105,v81,v81
v_rcp_f32 v102,v102
v_rcp_f32 v103,v103
v_rcp_f32 v104,v104
v_rcp_f32 v105,v105
v_mul_f32 v38,v38,v102
v_mul_f32 v39,v39,v103
v_mul_f32 v40,v40,v104
v_mul_f32 v41,v41,v105
v_cvt_pk_bf16_f32 v110,v38,v39
v_cvt_pk_bf16_f32 v111,v40,v41
s_nop 0
v_permlane16_swap_b32 v110,v112
v_permlane16_swap_b32 v111,v113
buffer_store_dwordx4 v[110:113],v10,s[36:39],s67 offen offset:0 sc0 sc1
s_mov_b32 s72,s69
s_mov_b32 s69,0x7fff0000
s_mov_b32 s67,0x7fff0000
s_mov_b32 s77,0x7fff0000
s_branch Lg3_done_%=
Lg3_done_%=:
s_waitcnt vmcnt(2)
s_barrier
buffer_store_dword v4,v3,s[36:39],s70 offen offset:0 sc0 sc1
s_waitcnt vmcnt(1)
s_barrier
buffer_store_dword v4,v3,s[36:39],s72 offen offset:0 sc0 sc1
s_waitcnt vmcnt(0) lgkmcnt(0)
s_barrier
s_endpgm
Lcode_g3_e:
.purgem M
.purgem R
.purgem T
.purgem D
.purgem L
)"
  :: "s"(ws), "s"(sWave), "s"(sG0), "s"(sStep), "s"(sOutOff), "s"(sFoutOff), "s"(sAuxOff), "s"(sWw), "s"(sBuf), "s"(sRk), "v"(vL16), "v"(vFlag), "v"(vSnapOff), "v"(vSnapRd), "v"(vWd0), "v"(vWd1), "v"(vWr0), "v"(vWr1), "v"(vWr2), "v"(vOutS), "v"(vAux0), "v"(vAux1), "v"(vAux2), "v"(vAux3), "v"(vAn), "v"(vAe), "s"(w3) : "v0", "v1", "v2", "v3", "v4", "v5", "v6", "v7", "v8", "v9", "v10", "v11", "v12", "v13", "v14", "v15", "v16", "v17", "v18", "v19", "v20", "v21", "v22", "v23", "v24", "v25", "v26", "v27", "v28", "v29", "v30", "v31", "v32", "v33", "v34", "v35", "v36", "v37", "v38", "v39", "v40", "v41", "v42", "v43", "v44", "v45", "v46", "v47", "v48", "v49", "v50", "v51", "v52", "v53", "v54", "v55", "v56", "v57", "v58", "v59", "v60", "v61", "v62", "v63", "v64", "v65", "v66", "v67", "v68", "v69", "v70", "v71", "v72", "v73", "v74", "v75", "v76", "v77", "v78", "v79", "v80", "v81", "v82", "v83", "v84", "v85", "v86", "v87", "v88", "v89", "v90", "v91", "v92", "v93", "v94", "v95", "v96", "v97", "v98", "v99", "v100", "v101", "v102", "v103", "v104", "v105", "v106", "v107", "v108", "v109", "v110", "v111", "v112", "v113", "v114", "v115", "v116", "v117", "v118", "v119", "v120", "v121", "v122", "v123", "v124", "v125", "v126", "v127", "v128", "v129", "v130", "v131", "v132", "v133", "v134", "v135", "v136", "v137", "v138", "v139", "v140", "v141", "v142", "v143", "v144", "v145", "v146", "v147", "v148", "v149", "v150", "v151", "v152", "v153", "v154", "v155", "v156", "v157", "v158", "v159", "v160", "v161", "v162", "v163", "v164", "v165", "v166", "v167", "v168", "v169", "v170", "v171", "v172", "v173", "v174", "v175", "v176", "v177", "v178", "v179", "v180", "v181", "v182", "v183", "v184", "v185", "v186", "v187", "v188", "v189", "v190", "v191", "v192", "v193", "v194", "v195", "v196", "v197", "v198", "v199", "v200", "v201", "v202", "v203", "v204", "v205", "v206", "v207", "v208", "v209", "v210", "v211", "v212", "v213", "v214", "v215", "v216", "v217", "v218", "v219", "v220", "v221", "v222", "v223", "v224", "v225", "v226", "v227", "v228", "v229", "v230", "v231", "v232", "v233", "v234", "v235", "v236", "v237", "v238", "v239", "a0", "a1", "a2", "a3", "a4", "a5", "a6", "a7", "a8", "a9", "a10", "a11", "a12", "a13", "a14", "a15", "a16", "a17", "a18", "a19", "a20", "a21", "a22", "a23", "a24", "a25", "a26", "a27", "a28", "a29", "a30", "a31", "a32", "a33", "a34", "a35", "a36", "a37", "a38", "a39", "a40", "a41", "a42", "a43", "a44", "a45", "a46", "a47", "a48", "a49", "a50", "a51", "a52", "a53", "a54", "a55", "a56", "a57", "a58", "a59", "a60", "a61", "a62", "a63", "a64", "a65", "a66", "a67", "a68", "a69", "a70", "a71", "a72", "a73", "a74", "a75", "a76", "a77", "a78", "a79", "a80", "a81", "a82", "a83", "a84", "a85", "a86", "a87", "a88", "a89", "a90", "a91", "a92", "a93", "a94", "a95", "a96", "a97", "a98", "a99", "a100", "a101", "a102", "a103", "a104", "a105", "a106", "a107", "a108", "a109", "a110", "a111", "a112", "a113", "a114", "a115", "a116", "a117", "a118", "a119", "a120", "a121", "a122", "a123", "a124", "a125", "a126", "a127", "a128", "a129", "a130", "a131", "a132", "a133", "a134", "a135", "a136", "a137", "a138", "a139", "a140", "a141", "a142", "a143", "a144", "a145", "a146", "a147", "a148", "a149", "a150", "a151", "a152", "a153", "a154", "a155", "a156", "a157", "a158", "a159", "a160", "a161", "a162", "a163", "a164", "a165", "a166", "a167", "a168", "a169", "a170", "a171", "a172", "a173", "a174", "a175", "a176", "a177", "a178", "a179", "a180", "a181", "a182", "a183", "a184", "a185", "a186", "a187", "a188", "a189", "a190", "a191", "a192", "a193", "a194", "a195", "a196", "a197", "a198", "a199", "a200", "a201", "a202", "a203", "a204", "a205", "a206", "a207", "a208", "a209", "a210", "a211", "a212", "a213", "a214", "a215", "a216", "a217", "a218", "a219", "a220", "a221", "a222", "a223", "a224", "a225", "a226", "a227", "a228", "a229", "a230", "a231", "a232", "a233", "a234", "a235", "a236", "a237", "a238", "a239", "a240", "a241", "a242", "a243", "a244", "a245", "a246", "a247", "a248", "a249", "a250", "a251", "a252", "a253", "a254", "a255", "s36", "s37", "s38", "s39", "s40", "s41", "s42", "s43", "s44", "s45", "s46", "s47", "s48", "s49", "s50", "s51", "s52", "s53", "s54", "s55", "s56", "s57", "s58", "s59", "s60", "s61", "s62", "s63", "s64", "s65", "s66", "s67", "s68", "s69", "s70", "s71", "s72", "s73", "s74", "s75", "s76", "s77", "s78", "s79", "s80", "s81", "s82", "s83", "s84", "s85", "s86", "s87", "s88", "s89", "s90", "s91", "s92", "s93", "s94", "s95", "s96", "s97", "vcc", "scc", "memory"); } else { asm volatile(
R"(.macro M d,w,x
v_mfma_f32_16x16x32_bf16 v[\d:\d+3], \w, v[\x:\x+3], v[\d:\d+3]
.endm
.macro R d,a,o
ds_read_b128 v[\d:\d+3], v\a offset:\o
.endm
.macro T d,a,o
ds_read_b64_tr_b16 \d, v\a offset:\o
.endm
.macro D a,s,o
buffer_load_dwordx4 v\a, s[36:39], s\s offen offset:\o sc1 lds
.endm
.macro L c
s_waitcnt lgkmcnt(\c)
.endm
Lcode_g3o_b:
s_mov_b64 s[36:37],%0
s_mov_b32 s38,111243392
s_mov_b32 s39,0x00020000
s_mov_b32 s44,%1
s_mov_b32 s45,%2
s_mov_b32 s46,%3
s_mov_b32 s47,%4
s_mov_b32 s48,%5
s_mov_b32 s49,%6
s_mov_b32 s50,%7
s_mov_b32 s51,%8
s_mov_b32 s93,%9
v_mov_b32 v0,%10
v_mov_b32 v3,%11
v_mov_b32 v5,%12
v_mov_b32 v6,%13
v_mov_b32 v18,%14
v_mov_b32 v19,%15
v_mov_b32 v20,%16
v_mov_b32 v21,%17
v_mov_b32 v22,%18
v_mov_b32 v10,%19
v_mov_b32 v11,%20
v_mov_b32 v12,%21
v_mov_b32 v13,%22
v_mov_b32 v14,%23
v_mov_b32 v15,%24
v_mov_b32 v16,%25
s_mov_b64 s[40:41],%26
s_mov_b32 s42,7340032
s_mov_b32 s43,0x00020000
s_lshl_b32 s57,s44,11
s_mov_b32 s79,111243264
buffer_load_dword v4,off,s[36:39],s79 sc1
s_add_u32 s55,s50,0
s_add_u32 m0,s57,0
s_nop 0
buffer_load_dwordx4 v18,s[40:43],s55 offen offset:0  lds
s_add_u32 m0,s57,1024
s_nop 0
buffer_load_dwordx4 v19,s[40:43],s55 offen offset:0  lds
s_add_u32 m0,s57,8064
s_nop 0
buffer_load_dwordx4 v18,s[40:43],s55 offen offset:128  lds
s_add_u32 m0,s57,9088
s_nop 0
buffer_load_dwordx4 v19,s[40:43],s55 offen offset:128  lds
s_add_u32 s55,s50,458752
s_add_u32 m0,s57,16384
s_nop 0
buffer_load_dwordx4 v18,s[40:43],s55 offen offset:0  lds
s_add_u32 m0,s57,17408
s_nop 0
buffer_load_dwordx4 v19,s[40:43],s55 offen offset:0  lds
s_add_u32 m0,s57,24448
s_nop 0
buffer_load_dwordx4 v18,s[40:43],s55 offen offset:128  lds
s_add_u32 m0,s57,25472
s_nop 0
buffer_load_dwordx4 v19,s[40:43],s55 offen offset:128  lds
s_waitcnt vmcnt(8)
s_barrier
v_readfirstlane_b32 s53,v4
s_nop 3
s_add_u32 s53,s53,1
s_cmp_eq_u32 s53,0
s_cselect_b32 s53,1,s53
v_mov_b32 v4,s53
buffer_store_dword v4,v3,s[36:39],s93 offen offset:0 sc0 sc1
s_add_u32 s55,s50,917504
s_add_u32 m0,s57,32768
s_nop 0
buffer_load_dwordx4 v18,s[40:43],s55 offen offset:0  lds
s_add_u32 m0,s57,33792
s_nop 0
buffer_load_dwordx4 v19,s[40:43],s55 offen offset:0  lds
s_add_u32 m0,s57,40832
s_nop 0
buffer_load_dwordx4 v18,s[40:43],s55 offen offset:128  lds
s_add_u32 m0,s57,41856
s_nop 0
buffer_load_dwordx4 v19,s[40:43],s55 offen offset:128  lds
s_add_u32 s55,s50,1376256
s_add_u32 m0,s57,49152
s_nop 0
buffer_load_dwordx4 v18,s[40:43],s55 offen offset:0  lds
s_add_u32 m0,s57,50176
s_nop 0
buffer_load_dwordx4 v19,s[40:43],s55 offen offset:0  lds
s_add_u32 m0,s57,57216
s_nop 0
buffer_load_dwordx4 v18,s[40:43],s55 offen offset:128  lds
s_add_u32 m0,s57,58240
s_nop 0
buffer_load_dwordx4 v19,s[40:43],s55 offen offset:128  lds
s_add_u32 s55,s50,1835008
s_add_u32 m0,s57,65536
s_nop 0
buffer_load_dwordx4 v18,s[40:43],s55 offen offset:0  lds
s_add_u32 m0,s57,66560
s_nop 0
buffer_load_dwordx4 v19,s[40:43],s55 offen offset:0  lds
s_add_u32 m0,s57,73600
s_nop 0
buffer_load_dwordx4 v18,s[40:43],s55 offen offset:128  lds
s_add_u32 m0,s57,74624
s_nop 0
buffer_load_dwordx4 v19,s[40:43],s55 offen offset:128  lds
s_add_u32 s55,s50,2293760
s_add_u32 m0,s57,81920
s_nop 0
buffer_load_dwordx4 v18,s[40:43],s55 offen offset:0  lds
s_add_u32 m0,s57,82944
s_nop 0
buffer_load_dwordx4 v19,s[40:43],s55 offen offset:0  lds
s_add_u32 m0,s57,89984
s_nop 0
buffer_load_dwordx4 v18,s[40:43],s55 offen offset:128  lds
s_add_u32 m0,s57,91008
s_nop 0
buffer_load_dwordx4 v19,s[40:43],s55 offen offset:128  lds
s_waitcnt vmcnt(21)
L 0
s_barrier
s_add_u32 s55,s50,2752512
s_add_u32 m0,s57,98304
s_nop 0
buffer_load_dwordx4 v18,s[40:43],s55 offen offset:0  lds
s_add_u32 m0,s57,99328
s_nop 0
buffer_load_dwordx4 v19,s[40:43],s55 offen offset:0  lds
s_add_u32 m0,s57,106368
s_nop 0
buffer_load_dwordx4 v18,s[40:43],s55 offen offset:128  lds
s_add_u32 m0,s57,107392
s_nop 0
buffer_load_dwordx4 v19,s[40:43],s55 offen offset:128  lds
v_add_u32 v26,0,v20
v_add_u32 v27,0,v21
T a[0:1],26,0
T a[2:3],26,512
T a[4:5],27,0
T a[6:7],27,512
T a[8:9],26,4096
T a[10:11],26,4608
T a[12:13],27,4096
T a[14:15],27,4608
s_waitcnt vmcnt(21)
L 0
s_barrier
s_add_u32 s55,s50,3211264
s_add_u32 m0,s57,0
s_nop 0
buffer_load_dwordx4 v18,s[40:43],s55 offen offset:0  lds
s_add_u32 m0,s57,1024
s_nop 0
buffer_load_dwordx4 v19,s[40:43],s55 offen offset:0  lds
s_add_u32 m0,s57,8064
s_nop 0
buffer_load_dwordx4 v18,s[40:43],s55 offen offset:128  lds
s_add_u32 m0,s57,9088
s_nop 0
buffer_load_dwordx4 v19,s[40:43],s55 offen offset:128  lds
v_add_u32 v26,16384,v20
v_add_u32 v27,16384,v21
T a[16:17],26,0
T a[18:19],26,512
T a[20:21],27,0
T a[22:23],27,512
T a[24:25],26,4096
T a[26:27],26,4608
T a[28:29],27,4096
T a[30:31],27,4608
s_waitcnt vmcnt(20)
L 0
s_barrier
s_add_u32 s55,s50,3670016
s_add_u32 m0,s57,16384
s_nop 0
buffer_load_dwordx4 v18,s[40:43],s55 offen offset:0  lds
s_add_u32 m0,s57,17408
s_nop 0
buffer_load_dwordx4 v19,s[40:43],s55 offen offset:0  lds
s_add_u32 m0,s57,24448
s_nop 0
buffer_load_dwordx4 v18,s[40:43],s55 offen offset:128  lds
s_add_u32 m0,s57,25472
s_nop 0
buffer_load_dwordx4 v19,s[40:43],s55 offen offset:128  lds
v_add_u32 v26,32768,v20
v_add_u32 v27,32768,v21
T a[32:33],26,0
T a[34:35],26,512
T a[36:37],27,0
T a[38:39],27,512
T a[40:41],26,4096
T a[42:43],26,4608
T a[44:45],27,4096
T a[46:47],27,4608
s_waitcnt vmcnt(20)
L 0
s_barrier
s_add_u32 s55,s50,4128768
s_add_u32 m0,s57,32768
s_nop 0
buffer_load_dwordx4 v18,s[40:43],s55 offen offset:0  lds
s_add_u32 m0,s57,33792
s_nop 0
buffer_load_dwordx4 v19,s[40:43],s55 offen offset:0  lds
s_add_u32 m0,s57,40832
s_nop 0
buffer_load_dwordx4 v18,s[40:43],s55 offen offset:128  lds
s_add_u32 m0,s57,41856
s_nop 0
buffer_load_dwordx4 v19,s[40:43],s55 offen offset:128  lds
v_add_u32 v26,49152,v20
v_add_u32 v27,49152,v21
T a[48:49],26,0
T a[50:51],26,512
T a[52:53],27,0
T a[54:55],27,512
T a[56:57],26,4096
T a[58:59],26,4608
T a[60:61],27,4096
T a[62:63],27,4608
s_waitcnt vmcnt(20)
L 0
s_barrier
s_add_u32 s55,s50,4587520
s_add_u32 m0,s57,49152
s_nop 0
buffer_load_dwordx4 v18,s[40:43],s55 offen offset:0  lds
s_add_u32 m0,s57,50176
s_nop 0
buffer_load_dwordx4 v19,s[40:43],s55 offen offset:0  lds
s_add_u32 m0,s57,57216
s_nop 0
buffer_load_dwordx4 v18,s[40:43],s55 offen offset:128  lds
s_add_u32 m0,s57,58240
s_nop 0
buffer_load_dwordx4 v19,s[40:43],s55 offen offset:128  lds
v_add_u32 v26,65536,v20
v_add_u32 v27,65536,v21
T a[64:65],26,0
T a[66:67],26,512
T a[68:69],27,0
T a[70:71],27,512
T a[72:73],26,4096
T a[74:75],26,4608
T a[76:77],27,4096
T a[78:79],27,4608
s_waitcnt vmcnt(20)
L 0
s_barrier
s_add_u32 s55,s50,5046272
s_add_u32 m0,s57,65536
s_nop 0
buffer_load_dwordx4 v18,s[40:43],s55 offen offset:0  lds
s_add_u32 m0,s57,66560
s_nop 0
buffer_load_dwordx4 v19,s[40:43],s55 offen offset:0  lds
s_add_u32 m0,s57,73600
s_nop 0
buffer_load_dwordx4 v18,s[40:43],s55 offen offset:128  lds
s_add_u32 m0,s57,74624
s_nop 0
buffer_load_dwordx4 v19,s[40:43],s55 offen offset:128  lds
v_add_u32 v26,81920,v20
v_add_u32 v27,81920,v21
T a[80:81],26,0
T a[82:83],26,512
T a[84:85],27,0
T a[86:87],27,512
T a[88:89],26,4096
T a[90:91],26,4608
T a[92:93],27,4096
T a[94:95],27,4608
s_waitcnt vmcnt(20)
L 0
s_barrier
s_add_u32 s55,s50,5505024
s_add_u32 m0,s57,81920
s_nop 0
buffer_load_dwordx4 v18,s[40:43],s55 offen offset:0  lds
s_add_u32 m0,s57,82944
s_nop 0
buffer_load_dwordx4 v19,s[40:43],s55 offen offset:0  lds
s_add_u32 m0,s57,89984
s_nop 0
buffer_load_dwordx4 v18,s[40:43],s55 offen offset:128  lds
s_add_u32 m0,s57,91008
s_nop 0
buffer_load_dwordx4 v19,s[40:43],s55 offen offset:128  lds
v_add_u32 v26,98304,v20
v_add_u32 v27,98304,v21
T a[96:97],26,0
T a[98:99],26,512
T a[100:101],27,0
T a[102:103],27,512
T a[104:105],26,4096
T a[106:107],26,4608
T a[108:109],27,4096
T a[110:111],27,4608
s_waitcnt vmcnt(20)
L 0
s_barrier
s_add_u32 s55,s50,5963776
s_add_u32 m0,s57,98304
s_nop 0
buffer_load_dwordx4 v18,s[40:43],s55 offen offset:0  lds
s_add_u32 m0,s57,99328
s_nop 0
buffer_load_dwordx4 v19,s[40:43],s55 offen offset:0  lds
s_add_u32 m0,s57,106368
s_nop 0
buffer_load_dwordx4 v18,s[40:43],s55 offen offset:128  lds
s_add_u32 m0,s57,107392
s_nop 0
buffer_load_dwordx4 v19,s[40:43],s55 offen offset:128  lds
v_add_u32 v26,0,v20
v_add_u32 v27,0,v21
T a[112:113],26,0
T a[114:115],26,512
T a[116:117],27,0
T a[118:119],27,512
T a[120:121],26,4096
T a[122:123],26,4608
T a[124:125],27,4096
T a[126:127],27,4608
s_waitcnt vmcnt(20)
L 0
s_barrier
s_add_u32 s55,s50,6422528
s_add_u32 m0,s57,0
s_nop 0
buffer_load_dwordx4 v18,s[40:43],s55 offen offset:0  lds
s_add_u32 m0,s57,1024
s_nop 0
buffer_load_dwordx4 v19,s[40:43],s55 offen offset:0  lds
s_add_u32 m0,s57,8064
s_nop 0
buffer_load_dwordx4 v18,s[40:43],s55 offen offset:128  lds
s_add_u32 m0,s57,9088
s_nop 0
buffer_load_dwordx4 v19,s[40:43],s55 offen offset:128  lds
v_add_u32 v26,16384,v20
v_add_u32 v27,16384,v21
T a[128:129],26,0
T a[130:131],26,512
T a[132:133],27,0
T a[134:135],27,512
T a[136:137],26,4096
T a[138:139],26,4608
T a[140:141],27,4096
T a[142:143],27,4608
s_waitcnt vmcnt(20)
L 0
s_barrier
s_add_u32 s55,s50,6881280
s_add_u32 m0,s57,16384
s_nop 0
buffer_load_dwordx4 v18,s[40:43],s55 offen offset:0  lds
s_add_u32 m0,s57,17408
s_nop 0
buffer_load_dwordx4 v19,s[40:43],s55 offen offset:0  lds
s_add_u32 m0,s57,24448
s_nop 0
buffer_load_dwordx4 v18,s[40:43],s55 offen offset:128  lds
s_add_u32 m0,s57,25472
s_nop 0
buffer_load_dwordx4 v19,s[40:43],s55 offen offset:128  lds
v_add_u32 v26,32768,v20
v_add_u32 v27,32768,v21
T a[144:145],26,0
T a[146:147],26,512
T a[148:149],27,0
T a[150:151],27,512
T a[152:153],26,4096
T a[154:155],26,4608
T a[156:157],27,4096
T a[158:159],27,4608
s_waitcnt vmcnt(20)
L 0
s_barrier
v_add_u32 v26,49152,v20
v_add_u32 v27,49152,v21
T a[160:161],26,0
T a[162:163],26,512
T a[164:165],27,0
T a[166:167],27,512
T a[168:169],26,4096
T a[170:171],26,4608
T a[172:173],27,4096
T a[174:175],27,4608
s_waitcnt vmcnt(16)
L 0
s_barrier
v_add_u32 v26,65536,v20
v_add_u32 v27,65536,v21
T a[176:177],26,0
T a[178:179],26,512
T a[180:181],27,0
T a[182:183],27,512
T a[184:185],26,4096
T a[186:187],26,4608
T a[188:189],27,4096
T a[190:191],27,4608
s_waitcnt vmcnt(12)
L 0
s_barrier
v_add_u32 v26,81920,v20
v_add_u32 v27,81920,v21
T a[192:193],26,0
T a[194:195],26,512
T a[196:197],27,0
T a[198:199],27,512
T a[200:201],26,4096
T a[202:203],26,4608
T a[204:205],27,4096
T a[206:207],27,4608
s_waitcnt vmcnt(8)
L 0
s_barrier
v_add_u32 v26,98304,v20
v_add_u32 v27,98304,v21
T a[208:209],26,0
T a[210:211],26,512
T a[212:213],27,0
T a[214:215],27,512
T a[216:217],26,4096
T a[218:219],26,4608
T a[220:221],27,4096
T a[222:223],27,4608
s_waitcnt vmcnt(4)
L 0
s_barrier
v_add_u32 v26,0,v20
v_add_u32 v27,0,v21
T a[224:225],26,0
T a[226:227],26,512
T a[228:229],27,0
T a[230:231],27,512
T a[232:233],26,4096
T a[234:235],26,4608
T a[236:237],27,4096
T a[238:239],27,4608
s_waitcnt vmcnt(0)
L 0
s_barrier
v_add_u32 v26,16384,v20
v_add_u32 v27,16384,v21
T a[240:241],26,0
T a[242:243],26,512
T a[244:245],27,0
T a[246:247],27,512
T a[248:249],26,4096
T a[250:251],26,4608
T a[252:253],27,4096
T a[254:255],27,4608
L 0
s_barrier
v_mov_b32 v34,0x3f803f80
v_mov_b32 v35,0x3f803f80
v_mov_b32 v36,0x3f803f80
v_mov_b32 v37,0x3f803f80
s_nop 1
v_mfma_f32_16x16x32_bf16 v[74:77],a[0:3],v[34:37],0
v_mfma_f32_16x16x32_bf16 v[78:81],a[4:7],v[34:37],0
M 74,a[8:11],34
M 78,a[12:15],34
M 74,a[16:19],34
M 78,a[20:23],34
M 74,a[24:27],34
M 78,a[28:31],34
M 74,a[32:35],34
M 78,a[36:39],34
M 74,a[40:43],34
M 78,a[44:47],34
M 74,a[48:51],34
M 78,a[52:55],34
M 74,a[56:59],34
M 78,a[60:63],34
M 74,a[64:67],34
M 78,a[68:71],34
)"
R"(M 74,a[72:75],34
M 78,a[76:79],34
M 74,a[80:83],34
M 78,a[84:87],34
M 74,a[88:91],34
M 78,a[92:95],34
M 74,a[96:99],34
M 78,a[100:103],34
M 74,a[104:107],34
M 78,a[108:111],34
M 74,a[112:115],34
M 78,a[116:119],34
M 74,a[120:123],34
M 78,a[124:127],34
M 74,a[128:131],34
M 78,a[132:135],34
M 74,a[136:139],34
M 78,a[140:143],34
M 74,a[144:147],34
M 78,a[148:151],34
M 74,a[152:155],34
M 78,a[156:159],34
M 74,a[160:163],34
M 78,a[164:167],34
M 74,a[168:171],34
M 78,a[172:175],34
M 74,a[176:179],34
M 78,a[180:183],34
M 74,a[184:187],34
M 78,a[188:191],34
M 74,a[192:195],34
M 78,a[196:199],34
M 74,a[200:203],34
M 78,a[204:207],34
M 74,a[208:211],34
M 78,a[212:215],34
M 74,a[216:219],34
M 78,a[220:223],34
M 74,a[224:227],34
M 78,a[228:231],34
M 74,a[232:235],34
M 78,a[236:239],34
M 74,a[240:243],34
M 78,a[244:247],34
M 74,a[248:251],34
M 78,a[252:255],34
v_mov_b32 v4,s53
s_mov_b32 s82,0
s_mov_b32 s80,0
s_mov_b32 s54,s45
s_mul_i32 s57,s44,8192
s_mul_i32 s55,s45,32768
s_add_u32 s55,s55,s57
s_add_u32 s55,s55,31457280
s_mul_i32 s60,s46,32768
s_mul_i32 s61,s46,1024
s_mul_i32 s62,s46,114688
s_mul_i32 s63,s46,2560
s_mul_i32 s64,s46,1024
s_lshl_b32 s59,s44,8
s_add_u32 s59,s59,131072
s_mul_i32 s58,s45,1024
s_add_u32 s58,s58,109613056
s_mul_i32 s65,s45,114688
s_add_u32 s65,s65,s47
s_mul_i32 s68,s45,2560
s_add_u32 s68,s68,s48
s_mul_i32 s73,s45,1024
s_add_u32 s73,s73,107479040
s_mul_i32 s74,s45,32768
s_add_u32 s74,s74,s49
s_mul_i32 s75,s45,1024
s_add_u32 s75,s75,107479040
s_mov_b32 s66,0x7fff0000
s_mov_b32 s67,0x7fff0000
s_mov_b32 s69,0x7fff0000
s_mov_b32 s76,0x7fff0000
s_mov_b32 s77,0x7fff0000
s_mov_b32 s70,0x7fff0000
s_mov_b32 s71,0x7fff0000
s_mov_b32 s72,0x7fff0000
v_mov_b32 v1,v0
v_add_u32 v2,32768,v0
s_mov_b32 s84,32768
s_mov_b32 s86,0x3a800000
s_mov_b32 s87,0x3727c5ac
s_mov_b32 s88,0xc0135761
s_mov_b32 s89,0x3d372713
s_and_b32 s85,s44,1
s_mul_i32 s85,s85,127
v_lshlrev_b32 v8,22,v0
v_and_b32 v8,0x80000000,v8
v_or_b32 v8,v8,v5
s_add_u32 m0,s59,4096
s_nop 0
buffer_load_dword v5,s[36:39],s58 offen offset:0 sc1 lds
buffer_store_dword v4,v3,s[36:39],s71 offen offset:0 sc0 sc1
s_add_u32 m0,s59,5120
s_nop 0
buffer_load_dword v5,s[36:39],s58 offen offset:0 sc1 lds
buffer_store_dword v4,v3,s[36:39],s71 offen offset:0 sc0 sc1
s_mov_b32 s83,0
Lg3o_poll_%=:
s_add_u32 s78,s83,2
s_and_b32 s78,s78,3
s_lshl_b32 s78,s78,10
s_add_u32 s78,s78,4096
s_add_u32 m0,s59,s78
s_nop 0
buffer_load_dword v5,s[36:39],s58 offen offset:0 sc1 lds
s_and_b32 s78,s83,3
s_lshl_b32 s78,s78,10
v_add_u32 v82,s78,v6
s_waitcnt vmcnt(4)
s_barrier
ds_read_b32 v7,v82 offset:4096
buffer_store_dword v4,v3,s[36:39],s70 offen offset:0 sc0 sc1
s_mov_b32 s70,s71
s_mov_b32 s71,s72
s_mov_b32 s72,0x7fff0000
s_add_u32 s83,s83,1
s_add_u32 s82,s82,1
L 0
v_cmp_eq_u32 vcc,s53,v7
s_nop 0
s_cmp_eq_u32 vcc_lo,-1
s_cbranch_scc1 Lg3o_go_%=
s_cmp_lt_u32 s82,20000
s_cbranch_scc1 Lg3o_poll_%=
Lg3o_go_%=:
buffer_load_dword v98,v11,s[36:39],s73 offen offset:0 sc1
s_add_u32 s73,s73,s64
s_mov_b32 m0,s57
s_add_u32 s56,s55,4096
D 0,55,0
D 0,55,1024
D 0,55,2048
D 0,55,3072
s_add_u32 m0,s57,4096
s_nop 0
D 0,56,0
D 0,56,1024
D 0,56,2048
D 0,56,3072
s_add_u32 s55,s55,s60
s_add_u32 s57,s57,32768
s_and_b32 s57,s57,131071
s_add_u32 s54,s54,s46
Lg3o_gox_%=:
s_add_u32 s58,s58,s61
s_cmp_eq_u32 vcc_hi,-1
s_cbranch_scc1 Lg3o_go2_%=
s_add_u32 m0,s59,2048
s_nop 0
buffer_load_dword v5,s[36:39],s58 offen offset:0 sc1 lds
s_waitcnt vmcnt(0)
s_barrier
buffer_store_dword v4,v3,s[36:39],s70 offen offset:0 sc0 sc1
s_min_u32 s71,s71,s72
buffer_store_dword v4,v3,s[36:39],s71 offen offset:0 sc0 sc1
s_mov_b32 s70,0x7fff0000
s_mov_b32 s71,0x7fff0000
s_mov_b32 s72,0x7fff0000
s_cmp_eq_u32 s80,1
s_cbranch_scc1 Lg3o_st11_%=
Lg3o_st10_%=:
ds_read_b32 v7,v6 offset:2048
R 34,1,0
R 38,1,1024
R 42,1,2048
R 46,1,3072
R 50,1,4096
R 54,1,5120
R 58,1,6144
L 7
v_cmp_eq_u32 vcc,s53,v7
L 5
s_cmp_eq_u64 vcc,-1
s_cbranch_scc1 Lg3o_L20_%=
s_cmp_eq_u32 vcc_lo,-1
s_cbranch_scc1 Lg3o_L10_%=
s_branch Lg3o_X0_%=
Lg3o_st11_%=:
ds_read_b32 v7,v6 offset:2048
R 34,2,0
R 38,2,1024
R 42,2,2048
R 46,2,3072
R 50,2,4096
R 54,2,5120
R 58,2,6144
L 7
v_cmp_eq_u32 vcc,s53,v7
L 5
s_cmp_eq_u64 vcc,-1
s_cbranch_scc1 Lg3o_L21_%=
s_cmp_eq_u32 vcc_lo,-1
s_cbranch_scc1 Lg3o_L11_%=
s_branch Lg3o_X1_%=
Lg3o_go2_%=:
s_add_u32 s58,s58,s61
s_add_u32 m0,s59,2048
s_nop 0
buffer_load_dword v5,s[36:39],s58 offen offset:0 sc1 lds
s_mov_b32 m0,s57
s_add_u32 s56,s55,4096
D 0,55,0
D 0,55,1024
D 0,55,2048
D 0,55,3072
s_add_u32 m0,s57,4096
s_nop 0
D 0,56,0
D 0,56,1024
D 0,56,2048
D 0,56,3072
s_add_u32 s55,s55,s60
s_add_u32 s57,s57,32768
s_and_b32 s57,s57,131071
s_add_u32 s54,s54,s46
s_waitcnt vmcnt(8)
s_barrier
buffer_store_dword v4,v3,s[36:39],s70 offen offset:0 sc0 sc1
s_min_u32 s71,s71,s72
buffer_store_dword v4,v3,s[36:39],s71 offen offset:0 sc0 sc1
s_mov_b32 s70,0x7fff0000
s_mov_b32 s71,0x7fff0000
s_mov_b32 s72,0x7fff0000
s_cmp_eq_u32 s80,1
s_cbranch_scc1 Lg3o_st21_%=
Lg3o_st20_%=:
ds_read_b32 v7,v6 offset:2048
R 34,1,0
R 38,1,1024
R 42,1,2048
R 46,1,3072
R 50,1,4096
R 54,1,5120
R 58,1,6144
L 7
v_cmp_eq_u32 vcc,s53,v7
L 5
s_cmp_eq_u32 vcc_lo,-1
s_cbranch_scc1 Lg3o_S0_%=
s_branch Lg3o_N0_%=
Lg3o_st21_%=:
ds_read_b32 v7,v6 offset:2048
R 34,2,0
R 38,2,1024
R 42,2,2048
R 46,2,3072
R 50,2,4096
R 54,2,5120
R 58,2,6144
L 7
v_cmp_eq_u32 vcc,s53,v7
L 5
s_cmp_eq_u32 vcc_lo,-1
s_cbranch_scc1 Lg3o_S1_%=
s_branch Lg3o_N1_%=
Lg3o_L20_%=:
v_mfma_f32_16x16x32_bf16 v[18:21],a[0:3],v[34:37],0
s_add_u32 s58,s58,s61
s_add_u32 s58,s58,s61
R 62,1,7168
v_mfma_f32_16x16x32_bf16 v[22:25],a[4:7],v[34:37],0
s_add_u32 m0,s59,0
s_add_u32 s56,s55,4096
s_waitcnt vmcnt(2)
M 18,a[8:11],38
s_nop 0
R 34,1,8192
v_add_f32_dpp v99,v98,v98 quad_perm:[1,0,3,2] row_mask:0xf bank_mask:0xf
M 22,a[12:15],38
L 5
buffer_load_dword v8,s[36:39],s58 offen offset:0 sc1 lds
M 18,a[16:19],42
s_mov_b32 m0,s57
R 38,1,9216
v_add_f32_dpp v99,v99,v99 quad_perm:[2,3,0,1] row_mask:0xf bank_mask:0xf
M 22,a[20:23],42
s_mov_b32 s66,s65
buffer_load_dword v98,v11,s[36:39],s73 offen offset:0 sc1
M 18,a[24:27],46
R 42,1,10240
v_add_f32_dpp v102,v99,v99 row_half_mirror row_mask:0xf bank_mask:0xf
M 22,a[28:31],46
L 5
D 0,55,0
M 18,a[32:35],50
R 46,1,11264
v_mov_b32_dpp v99,v102 row_ror:8 row_mask:0xf bank_mask:0xf
M 22,a[36:39],50
v_fma_f32 v99,-v102,v102,v99
v_max_f32 v99,s87,v99
M 18,a[40:43],54
R 50,1,12288
v_fma_f32 v26,-v70,v74,v26
M 22,a[44:47],54
L 5
D 0,55,1024
M 18,a[48:51],58
R 54,1,13312
v_fma_f32 v27,-v70,v75,v27
M 22,a[52:55],58
v_rsq_f32 v100,v99
M 18,a[56:59],62
R 58,1,14336
v_mul_f32 v105,v99,v100
M 22,a[60:63],62
L 5
D 0,55,2048
M 18,a[64:67],34
R 62,1,15360
v_mul_f32 v103,s88,v100
M 22,a[68:71],34
v_mul_f32 v99,s89,v100
v_mul_f32 v104,v103,v100
M 18,a[72:75],38
R 34,1,16384
v_mul_f32 v104,v104,v99
M 22,a[76:79],38
L 5
ds_write_b128 v12,v[102:105] offset:0
M 18,a[80:83],42
R 38,1,17408
v_fma_f32 v28,-v70,v76,v28
M 22,a[84:87],42
D 0,55,3072
M 18,a[88:91],46
s_add_u32 m0,s57,4096
R 42,1,18432
v_fma_f32 v29,-v70,v77,v29
M 22,a[92:95],46
L 6
D 0,56,0
M 18,a[96:99],50
s_add_u32 s55,s55,s60
R 46,1,19456
v_mul_f32 v82,v26,v26
M 22,a[100:103],50
s_add_u32 s57,s57,32768
v_mul_f32 v83,v27,v27
v_mul_f32 v84,v28,v28
M 18,a[104:107],54
s_and_b32 s57,s57,131071
R 50,1,20480
v_mul_f32 v85,v29,v29
M 22,a[108:111],54
L 6
D 0,56,1024
M 18,a[112:115],58
s_add_u32 s54,s54,s46
R 54,1,21504
v_fma_f32 v82,v72,v82,v71
M 22,a[116:119],58
v_fma_f32 v83,v72,v83,v71
v_fma_f32 v84,v72,v84,v71
M 18,a[120:123],62
R 58,1,22528
v_fma_f32 v85,v72,v85,v71
M 22,a[124:127],62
L 5
D 0,56,2048
M 18,a[128:131],34
R 62,1,23552
v_mul_f32 v82,v26,v82
M 22,a[132:135],34
v_exp_f32 v82,v82
M 18,a[136:139],38
R 34,1,24576
v_mul_f32 v83,v27,v83
M 22,a[140:143],38
L 5
D 0,56,3072
M 18,a[144:147],42
s_mov_b32 m0,s57
R 38,1,25600
v_mul_f32 v84,v28,v84
M 22,a[148:151],42
s_add_u32 s56,s55,4096
v_exp_f32 v83,v83
M 18,a[152:155],46
R 42,1,26624
v_mul_f32 v85,v29,v85
M 22,a[156:159],46
L 5
D 0,55,0
M 18,a[160:163],50
R 46,1,27648
v_fma_f32 v82,v82,v73,v73
M 22,a[164:167],50
D 0,55,1024
M 18,a[168:171],54
R 50,1,28672
v_fma_f32 v83,v83,v73,v73
M 22,a[172:175],54
L 5
v_exp_f32 v84,v84
M 18,a[176:179],58
R 54,1,29696
v_fma_f32 v84,v84,v73,v73
M 22,a[180:183],58
D 0,55,2048
M 18,a[184:187],62
R 58,1,30720
v_fma_f32 v30,-v70,v78,v30
M 22,a[188:191],62
L 5
s_nop 0
D 0,55,3072
M 18,a[192:195],34
s_waitcnt vmcnt(4)
R 62,1,31744
v_fma_f32 v31,-v70,v79,v31
M 22,a[196:199],34
s_barrier
R 66,13,0
ds_read_b32 v7,v6 offset:0
M 18,a[200:203],38
s_add_u32 m0,s57,4096
R 34,2,0
v_fma_f32 v32,-v70,v80,v32
M 22,a[204:207],38
L 7
buffer_store_dword v4,v3,s[36:39],s70 offen offset:0 sc0 sc1
M 18,a[208:211],42
s_add_u32 s55,s55,s60
R 38,2,1024
v_fma_f32 v33,-v70,v81,v33
M 22,a[212:215],42
s_add_u32 s57,s57,32768
D 0,56,0
M 18,a[216:219],46
s_and_b32 s57,s57,131071
R 42,2,2048
v_mul_f32 v86,v30,v30
M 22,a[220:223],46
L 7
D 0,56,1024
M 18,a[224:227],50
s_add_u32 s54,s54,s46
R 46,2,3072
v_mul_f32 v87,v31,v31
M 22,a[228:231],50
s_add_u32 s73,s73,s64
D 0,56,2048
M 18,a[232:235],54
s_mov_b32 s70,s69
R 50,2,4096
v_mul_f32 v88,v32,v32
M 22,a[236:239],54
L 7
D 0,56,3072
M 18,a[240:243],58
s_add_u32 s65,s65,s62
R 54,2,5120
v_exp_f32 v85,v85
v_mul_f32 v89,v33,v33
v_fma_f32 v86,v72,v86,v71
v_fma_f32 v87,v72,v87,v71
v_fma_f32 v88,v72,v88,v71
v_fma_f32 v89,v72,v89,v71
v_mul_f32 v86,v30,v86
v_mul_f32 v87,v31,v87
v_mul_f32 v88,v32,v88
v_mul_f32 v89,v33,v89
v_exp_f32 v86,v86
v_exp_f32 v87,v87
v_exp_f32 v88,v88
v_exp_f32 v89,v89
M 22,a[244:247],58
s_mov_b32 s69,s68
L 6
v_fma_f32 v85,v85,v73,v73
v_rcp_f32 v82,v82
v_rcp_f32 v83,v83
v_rcp_f32 v84,v84
v_rcp_f32 v85,v85
v_fma_f32 v86,v86,v73,v73
v_fma_f32 v87,v87,v73,v73
v_fma_f32 v88,v88,v73,v73
v_fma_f32 v89,v89,v73,v73
v_rcp_f32 v86,v86
v_rcp_f32 v87,v87
v_rcp_f32 v88,v88
v_rcp_f32 v89,v89
v_cmp_eq_u32 vcc,s53,v7
M 18,a[248:251],62
s_add_u32 s68,s68,s63
s_cmp_eq_u32 vcc_lo,-1
R 58,2,6144
v_mul_f32 v26,v26,v82
v_mul_f32 v27,v27,v83
v_mul_f32 v28,v28,v84
v_mul_f32 v29,v29,v85
v_cvt_pk_bf16_f32 v90,v26,v27
v_cvt_pk_bf16_f32 v91,v28,v29
v_mul_f32 v30,v30,v86
v_mul_f32 v31,v31,v87
)"
R"(v_mul_f32 v32,v32,v88
v_mul_f32 v33,v33,v89
v_cvt_pk_bf16_f32 v92,v30,v31
v_cvt_pk_bf16_f32 v93,v32,v33
M 22,a[252:255],62
L 5
buffer_store_dwordx4 v[90:93],v0,s[36:39],s67 offen offset:0 sc0 sc1
v_xor_b32 v1,65536,v1
s_cbranch_scc0 Lg3o_N1_%=
Lg3o_S1_%=:
v_mfma_f32_16x16x32_bf16 v[26:29],a[0:3],v[34:37],0
s_add_u32 s58,s58,s61
s_add_u32 m0,s59,1024
R 62,2,7168
v_mfma_f32_16x16x32_bf16 v[30:33],a[4:7],v[34:37],0
s_waitcnt vmcnt(10)
buffer_load_dword v8,s[36:39],s58 offen offset:0 sc1 lds
M 26,a[8:11],38
s_mov_b32 m0,s57
R 34,2,8192
v_add_f32_dpp v99,v98,v98 quad_perm:[1,0,3,2] row_mask:0xf bank_mask:0xf
M 30,a[12:15],38
L 5
buffer_load_dword v98,v11,s[36:39],s73 offen offset:0 sc1
M 26,a[16:19],42
s_add_u32 s56,s55,4096
R 38,2,9216
v_add_f32_dpp v99,v99,v99 quad_perm:[2,3,0,1] row_mask:0xf bank_mask:0xf
M 30,a[20:23],42
s_nop 0
v_fma_f32 v18,-v66,v74,v18
v_fma_f32 v19,-v66,v75,v19
M 26,a[24:27],46
s_mov_b32 s67,s65
R 42,2,10240
v_add_f32_dpp v102,v99,v99 row_half_mirror row_mask:0xf bank_mask:0xf
M 30,a[28:31],46
L 5
D 0,55,0
M 26,a[32:35],50
R 46,2,11264
v_mov_b32_dpp v99,v102 row_ror:8 row_mask:0xf bank_mask:0xf
M 30,a[36:39],50
v_fma_f32 v99,-v102,v102,v99
v_max_f32 v99,s87,v99
M 26,a[40:43],54
R 50,2,12288
v_fma_f32 v20,-v66,v76,v20
M 30,a[44:47],54
L 5
v_rsq_f32 v100,v99
M 26,a[48:51],58
R 54,2,13312
v_mul_f32 v105,v99,v100
M 30,a[52:55],58
D 0,55,1024
M 26,a[56:59],62
R 58,2,14336
v_mul_f32 v103,s88,v100
M 30,a[60:63],62
L 5
v_mul_f32 v99,s89,v100
v_mul_f32 v104,v103,v100
M 26,a[64:67],34
R 62,2,15360
v_mul_f32 v104,v104,v99
M 30,a[68:71],34
ds_write_b128 v12,v[102:105] offset:2048
M 26,a[72:75],38
R 34,2,16384
v_fma_f32 v21,-v66,v77,v21
M 30,a[76:79],38
L 6
v_mul_f32 v82,v18,v18
v_mul_f32 v83,v19,v19
M 26,a[80:83],42
R 38,2,17408
v_mul_f32 v84,v20,v20
M 30,a[84:87],42
D 0,55,2048
M 26,a[88:91],46
R 42,2,18432
v_mul_f32 v85,v21,v21
M 30,a[92:95],46
L 6
v_fma_f32 v82,v68,v82,v67
v_fma_f32 v83,v68,v83,v67
M 26,a[96:99],50
R 46,2,19456
v_fma_f32 v84,v68,v84,v67
M 30,a[100:103],50
v_fma_f32 v85,v68,v85,v67
v_mul_f32 v82,v18,v82
M 26,a[104:107],54
R 50,2,20480
v_mul_f32 v83,v19,v83
M 30,a[108:111],54
L 6
D 0,55,3072
M 26,a[112:115],58
s_add_u32 m0,s57,4096
R 54,2,21504
v_mul_f32 v84,v20,v84
M 30,a[116:119],58
s_add_u32 s55,s55,s60
v_exp_f32 v82,v82
M 26,a[120:123],62
s_add_u32 s57,s57,32768
R 58,2,22528
v_mul_f32 v85,v21,v85
M 30,a[124:127],62
L 5
v_exp_f32 v83,v83
M 26,a[128:131],34
s_and_b32 s57,s57,131071
R 62,2,23552
v_fma_f32 v82,v82,v69,v69
M 30,a[132:135],34
s_add_u32 s54,s54,s46
v_exp_f32 v84,v84
M 26,a[136:139],38
s_add_u32 s73,s73,s64
R 34,2,24576
v_fma_f32 v83,v83,v69,v69
M 30,a[140:143],38
L 5
D 0,56,0
M 26,a[144:147],42
s_add_u32 s65,s65,s62
R 38,2,25600
v_fma_f32 v84,v84,v69,v69
M 30,a[148:151],42
v_exp_f32 v85,v85
M 26,a[152:155],46
R 42,2,26624
v_fma_f32 v85,v85,v69,v69
M 30,a[156:159],46
L 5
v_rcp_f32 v82,v82
M 26,a[160:163],50
R 46,2,27648
v_mul_f32 v18,v18,v82
M 30,a[164:167],50
D 0,56,1024
M 26,a[168:171],54
R 50,2,28672
v_fma_f32 v22,-v66,v78,v22
M 30,a[172:175],54
L 5
v_rcp_f32 v83,v83
M 26,a[176:179],58
R 54,2,29696
v_mul_f32 v19,v19,v83
M 30,a[180:183],58
v_rcp_f32 v84,v84
M 26,a[184:187],62
R 58,2,30720
v_mul_f32 v20,v20,v84
M 30,a[188:191],62
L 5
s_nop 0
v_cvt_pk_bf16_f32 v90,v18,v19
M 26,a[192:195],34
s_waitcnt vmcnt(7)
R 62,2,31744
v_fma_f32 v23,-v66,v79,v23
M 30,a[196:199],34
s_barrier
R 70,13,2048
ds_read_b32 v7,v6 offset:1024
M 26,a[200:203],38
R 34,1,0
v_fma_f32 v24,-v66,v80,v24
M 30,a[204:207],38
L 7
buffer_store_dword v4,v3,s[36:39],s70 offen offset:0 sc0 sc1
M 26,a[208:211],42
s_mov_b32 s70,s69
R 38,1,1024
v_fma_f32 v25,-v66,v81,v25
M 30,a[212:215],42
s_mov_b32 s69,s68
D 0,56,2048
M 26,a[216:219],46
L 2
R 42,1,2048
v_mul_f32 v86,v22,v22
M 30,a[220:223],46
s_nop 0
D 0,56,3072
M 26,a[224:227],50
s_add_u32 s68,s68,s63
R 46,1,3072
v_mul_f32 v87,v23,v23
M 30,a[228:231],50
v_mul_f32 v88,v24,v24
v_mul_f32 v89,v25,v25
M 26,a[232:235],54
R 50,1,4096
v_fma_f32 v86,v68,v86,v67
M 30,a[236:239],54
s_nop 0
v_fma_f32 v87,v68,v87,v67
v_fma_f32 v88,v68,v88,v67
M 26,a[240:243],58
R 54,1,5120
v_fma_f32 v89,v68,v89,v67
v_mul_f32 v86,v22,v86
v_mul_f32 v87,v23,v87
v_mul_f32 v88,v24,v88
v_mul_f32 v89,v25,v89
v_exp_f32 v86,v86
v_exp_f32 v87,v87
v_exp_f32 v88,v88
v_exp_f32 v89,v89
M 30,a[244:247],58
v_rcp_f32 v85,v85
v_fma_f32 v86,v86,v69,v69
v_fma_f32 v87,v87,v69,v69
v_fma_f32 v88,v88,v69,v69
v_fma_f32 v89,v89,v69,v69
v_rcp_f32 v86,v86
v_rcp_f32 v87,v87
v_rcp_f32 v88,v88
v_rcp_f32 v89,v89
v_cmp_eq_u32 vcc,s53,v7
M 26,a[248:251],62
s_cmp_eq_u32 vcc_lo,-1
R 58,1,6144
v_mul_f32 v21,v21,v85
v_cvt_pk_bf16_f32 v91,v20,v21
v_mul_f32 v22,v22,v86
v_mul_f32 v23,v23,v87
v_mul_f32 v24,v24,v88
v_mul_f32 v25,v25,v89
v_cvt_pk_bf16_f32 v92,v22,v23
v_cvt_pk_bf16_f32 v93,v24,v25
M 30,a[252:255],62
L 5
buffer_store_dwordx4 v[90:93],v0,s[36:39],s66 offen offset:0 sc0 sc1
v_xor_b32 v2,65536,v2
s_cbranch_scc0 Lg3o_N0_%=
Lg3o_S0_%=:
v_mfma_f32_16x16x32_bf16 v[18:21],a[0:3],v[34:37],0
s_add_u32 s58,s58,s61
s_add_u32 m0,s59,0
R 62,1,7168
v_mfma_f32_16x16x32_bf16 v[22:25],a[4:7],v[34:37],0
s_waitcnt vmcnt(10)
buffer_load_dword v8,s[36:39],s58 offen offset:0 sc1 lds
M 18,a[8:11],38
s_mov_b32 m0,s57
R 34,1,8192
v_add_f32_dpp v99,v98,v98 quad_perm:[1,0,3,2] row_mask:0xf bank_mask:0xf
M 22,a[12:15],38
L 5
buffer_load_dword v98,v11,s[36:39],s73 offen offset:0 sc1
M 18,a[16:19],42
s_add_u32 s56,s55,4096
R 38,1,9216
v_add_f32_dpp v99,v99,v99 quad_perm:[2,3,0,1] row_mask:0xf bank_mask:0xf
M 22,a[20:23],42
s_nop 0
v_fma_f32 v26,-v70,v74,v26
v_fma_f32 v27,-v70,v75,v27
M 18,a[24:27],46
s_mov_b32 s66,s65
R 42,1,10240
v_add_f32_dpp v102,v99,v99 row_half_mirror row_mask:0xf bank_mask:0xf
M 22,a[28:31],46
L 5
D 0,55,0
M 18,a[32:35],50
R 46,1,11264
v_mov_b32_dpp v99,v102 row_ror:8 row_mask:0xf bank_mask:0xf
M 22,a[36:39],50
v_fma_f32 v99,-v102,v102,v99
v_max_f32 v99,s87,v99
M 18,a[40:43],54
R 50,1,12288
v_fma_f32 v28,-v70,v76,v28
M 22,a[44:47],54
L 5
v_rsq_f32 v100,v99
M 18,a[48:51],58
R 54,1,13312
v_mul_f32 v105,v99,v100
M 22,a[52:55],58
D 0,55,1024
M 18,a[56:59],62
R 58,1,14336
v_mul_f32 v103,s88,v100
M 22,a[60:63],62
L 5
v_mul_f32 v99,s89,v100
v_mul_f32 v104,v103,v100
M 18,a[64:67],34
R 62,1,15360
v_mul_f32 v104,v104,v99
M 22,a[68:71],34
ds_write_b128 v12,v[102:105] offset:0
M 18,a[72:75],38
R 34,1,16384
v_fma_f32 v29,-v70,v77,v29
M 22,a[76:79],38
L 6
v_mul_f32 v82,v26,v26
v_mul_f32 v83,v27,v27
M 18,a[80:83],42
R 38,1,17408
v_mul_f32 v84,v28,v28
M 22,a[84:87],42
D 0,55,2048
M 18,a[88:91],46
R 42,1,18432
v_mul_f32 v85,v29,v29
M 22,a[92:95],46
L 6
v_fma_f32 v82,v72,v82,v71
v_fma_f32 v83,v72,v83,v71
M 18,a[96:99],50
R 46,1,19456
v_fma_f32 v84,v72,v84,v71
M 22,a[100:103],50
v_fma_f32 v85,v72,v85,v71
v_mul_f32 v82,v26,v82
M 18,a[104:107],54
R 50,1,20480
v_mul_f32 v83,v27,v83
M 22,a[108:111],54
L 6
D 0,55,3072
M 18,a[112:115],58
s_add_u32 m0,s57,4096
R 54,1,21504
v_mul_f32 v84,v28,v84
M 22,a[116:119],58
s_add_u32 s55,s55,s60
v_exp_f32 v82,v82
M 18,a[120:123],62
s_add_u32 s57,s57,32768
R 58,1,22528
v_mul_f32 v85,v29,v85
M 22,a[124:127],62
L 5
v_exp_f32 v83,v83
M 18,a[128:131],34
s_and_b32 s57,s57,131071
R 62,1,23552
v_fma_f32 v82,v82,v73,v73
M 22,a[132:135],34
s_add_u32 s54,s54,s46
v_exp_f32 v84,v84
M 18,a[136:139],38
s_add_u32 s73,s73,s64
R 34,1,24576
v_fma_f32 v83,v83,v73,v73
M 22,a[140:143],38
L 5
D 0,56,0
M 18,a[144:147],42
s_add_u32 s65,s65,s62
R 38,1,25600
v_fma_f32 v84,v84,v73,v73
M 22,a[148:151],42
v_exp_f32 v85,v85
M 18,a[152:155],46
R 42,1,26624
v_fma_f32 v85,v85,v73,v73
M 22,a[156:159],46
L 5
v_rcp_f32 v82,v82
M 18,a[160:163],50
R 46,1,27648
v_mul_f32 v26,v26,v82
M 22,a[164:167],50
D 0,56,1024
M 18,a[168:171],54
R 50,1,28672
v_fma_f32 v30,-v70,v78,v30
M 22,a[172:175],54
L 5
v_rcp_f32 v83,v83
M 18,a[176:179],58
R 54,1,29696
v_mul_f32 v27,v27,v83
M 22,a[180:183],58
v_rcp_f32 v84,v84
M 18,a[184:187],62
R 58,1,30720
v_mul_f32 v28,v28,v84
M 22,a[188:191],62
L 5
s_nop 0
v_cvt_pk_bf16_f32 v90,v26,v27
M 18,a[192:195],34
s_waitcnt vmcnt(7)
R 62,1,31744
v_fma_f32 v31,-v70,v79,v31
M 22,a[196:199],34
s_barrier
R 66,13,0
ds_read_b32 v7,v6 offset:0
M 18,a[200:203],38
R 34,2,0
v_fma_f32 v32,-v70,v80,v32
M 22,a[204:207],38
L 7
buffer_store_dword v4,v3,s[36:39],s70 offen offset:0 sc0 sc1
M 18,a[208:211],42
s_mov_b32 s70,s69
R 38,2,1024
v_fma_f32 v33,-v70,v81,v33
M 22,a[212:215],42
s_mov_b32 s69,s68
D 0,56,2048
M 18,a[216:219],46
L 2
R 42,2,2048
v_mul_f32 v86,v30,v30
M 22,a[220:223],46
s_nop 0
D 0,56,3072
M 18,a[224:227],50
s_add_u32 s68,s68,s63
R 46,2,3072
v_mul_f32 v87,v31,v31
M 22,a[228:231],50
v_mul_f32 v88,v32,v32
v_mul_f32 v89,v33,v33
M 18,a[232:235],54
R 50,2,4096
v_fma_f32 v86,v72,v86,v71
M 22,a[236:239],54
s_nop 0
v_fma_f32 v87,v72,v87,v71
v_fma_f32 v88,v72,v88,v71
M 18,a[240:243],58
R 54,2,5120
v_fma_f32 v89,v72,v89,v71
v_mul_f32 v86,v30,v86
v_mul_f32 v87,v31,v87
v_mul_f32 v88,v32,v88
v_mul_f32 v89,v33,v89
v_exp_f32 v86,v86
v_exp_f32 v87,v87
v_exp_f32 v88,v88
v_exp_f32 v89,v89
M 22,a[244:247],58
v_rcp_f32 v85,v85
v_fma_f32 v86,v86,v73,v73
v_fma_f32 v87,v87,v73,v73
v_fma_f32 v88,v88,v73,v73
v_fma_f32 v89,v89,v73,v73
v_rcp_f32 v86,v86
v_rcp_f32 v87,v87
v_rcp_f32 v88,v88
v_rcp_f32 v89,v89
v_cmp_eq_u32 vcc,s53,v7
M 18,a[248:251],62
s_cmp_eq_u32 vcc_lo,-1
R 58,2,6144
v_mul_f32 v29,v29,v85
v_cvt_pk_bf16_f32 v91,v28,v29
v_mul_f32 v30,v30,v86
v_mul_f32 v31,v31,v87
v_mul_f32 v32,v32,v88
v_mul_f32 v33,v33,v89
v_cvt_pk_bf16_f32 v92,v30,v31
v_cvt_pk_bf16_f32 v93,v32,v33
M 22,a[252:255],62
L 5
buffer_store_dwordx4 v[90:93],v0,s[36:39],s67 offen offset:0 sc0 sc1
v_xor_b32 v1,65536,v1
s_cbranch_scc1 Lg3o_S1_%=
Lg3o_N1_%=:
v_mfma_f32_16x16x32_bf16 v[26:29],a[0:3],v[34:37],0
s_add_u32 m0,s59,1024
s_waitcnt vmcnt(10)
R 62,2,7168
v_mfma_f32_16x16x32_bf16 v[30:33],a[4:7],v[34:37],0
s_nop 0
buffer_load_dword v5,s[36:39],s58 offen offset:0 sc1 lds
M 26,a[8:11],38
s_mov_b32 s67,s65
R 34,2,8192
v_add_f32_dpp v99,v98,v98 quad_perm:[1,0,3,2] row_mask:0xf bank_mask:0xf
M 30,a[12:15],38
L 5
buffer_load_dword v98,v11,s[36:39],s73 offen offset:0 sc1
M 26,a[16:19],42
s_add_u32 s73,s73,s64
R 38,2,9216
v_add_f32_dpp v99,v99,v99 quad_perm:[2,3,0,1] row_mask:0xf bank_mask:0xf
M 30,a[20:23],42
s_add_u32 s65,s65,s62
v_fma_f32 v18,-v66,v74,v18
v_fma_f32 v19,-v66,v75,v19
M 26,a[24:27],46
R 42,2,10240
)"
R"(v_add_f32_dpp v102,v99,v99 row_half_mirror row_mask:0xf bank_mask:0xf
M 30,a[28:31],46
L 5
v_fma_f32 v20,-v66,v76,v20
v_fma_f32 v21,-v66,v77,v21
M 26,a[32:35],50
R 46,2,11264
v_mov_b32_dpp v99,v102 row_ror:8 row_mask:0xf bank_mask:0xf
M 30,a[36:39],50
v_fma_f32 v99,-v102,v102,v99
v_max_f32 v99,s87,v99
M 26,a[40:43],54
R 50,2,12288
v_mul_f32 v82,v18,v18
M 30,a[44:47],54
L 5
v_rsq_f32 v100,v99
M 26,a[48:51],58
R 54,2,13312
v_mul_f32 v105,v99,v100
M 30,a[52:55],58
v_mul_f32 v103,s88,v100
v_mul_f32 v99,s89,v100
M 26,a[56:59],62
R 58,2,14336
v_mul_f32 v104,v103,v100
M 30,a[60:63],62
L 5
v_mul_f32 v104,v104,v99
v_mul_f32 v83,v19,v19
M 26,a[64:67],34
R 62,2,15360
v_mul_f32 v84,v20,v20
M 30,a[68:71],34
ds_write_b128 v12,v[102:105] offset:2048
M 26,a[72:75],38
R 34,2,16384
v_mul_f32 v85,v21,v21
M 30,a[76:79],38
L 6
v_fma_f32 v82,v68,v82,v67
v_fma_f32 v83,v68,v83,v67
M 26,a[80:83],42
R 38,2,17408
v_fma_f32 v84,v68,v84,v67
M 30,a[84:87],42
v_fma_f32 v85,v68,v85,v67
v_mul_f32 v82,v18,v82
M 26,a[88:91],46
R 42,2,18432
v_mul_f32 v83,v19,v83
M 30,a[92:95],46
L 6
v_exp_f32 v82,v82
M 26,a[96:99],50
R 46,2,19456
v_mul_f32 v84,v20,v84
M 30,a[100:103],50
v_exp_f32 v83,v83
M 26,a[104:107],54
R 50,2,20480
v_mul_f32 v85,v21,v85
M 30,a[108:111],54
L 6
v_exp_f32 v84,v84
M 26,a[112:115],58
R 54,2,21504
v_fma_f32 v82,v82,v69,v69
M 30,a[116:119],58
v_exp_f32 v85,v85
M 26,a[120:123],62
R 58,2,22528
v_fma_f32 v83,v83,v69,v69
M 30,a[124:127],62
L 5
v_rcp_f32 v82,v82
M 26,a[128:131],34
R 62,2,23552
v_fma_f32 v84,v84,v69,v69
M 30,a[132:135],34
v_rcp_f32 v83,v83
M 26,a[136:139],38
R 34,2,24576
v_fma_f32 v85,v85,v69,v69
M 30,a[140:143],38
L 5
v_rcp_f32 v84,v84
M 26,a[144:147],42
R 38,2,25600
v_mul_f32 v18,v18,v82
M 30,a[148:151],42
v_rcp_f32 v85,v85
M 26,a[152:155],46
R 42,2,26624
v_mul_f32 v19,v19,v83
M 30,a[156:159],46
L 5
v_mul_f32 v20,v20,v84
v_mul_f32 v21,v21,v85
M 26,a[160:163],50
R 46,2,27648
v_cvt_pk_bf16_f32 v90,v18,v19
M 30,a[164:167],50
v_cvt_pk_bf16_f32 v91,v20,v21
v_fma_f32 v22,-v66,v78,v22
M 26,a[168:171],54
R 50,2,28672
v_fma_f32 v23,-v66,v79,v23
M 30,a[172:175],54
L 5
v_fma_f32 v24,-v66,v80,v24
v_fma_f32 v25,-v66,v81,v25
M 26,a[176:179],58
R 54,2,29696
v_mul_f32 v86,v22,v22
M 30,a[180:183],58
v_mul_f32 v87,v23,v23
v_mul_f32 v88,v24,v24
M 26,a[184:187],62
R 58,2,30720
v_mul_f32 v89,v25,v25
M 30,a[188:191],62
L 5
s_nop 0
v_fma_f32 v86,v68,v86,v67
M 26,a[192:195],34
s_waitcnt vmcnt(1)
R 62,2,31744
v_fma_f32 v87,v68,v87,v67
M 30,a[196:199],34
s_barrier
R 70,13,2048
ds_read_b32 v7,v6 offset:1024
M 26,a[200:203],38
R 34,1,0
v_fma_f32 v88,v68,v88,v67
M 30,a[204:207],38
L 7
buffer_store_dword v4,v3,s[36:39],s70 offen offset:0 sc0 sc1
M 26,a[208:211],42
s_mov_b32 s70,s69
R 38,1,1024
v_fma_f32 v89,v68,v89,v67
M 30,a[212:215],42
s_mov_b32 s69,s68
v_mul_f32 v86,v22,v86
v_mul_f32 v87,v23,v87
M 26,a[216:219],46
L 2
R 42,1,2048
v_mul_f32 v88,v24,v88
M 30,a[220:223],46
s_nop 0
v_exp_f32 v86,v86
M 26,a[224:227],50
s_add_u32 s68,s68,s63
R 46,1,3072
v_mul_f32 v89,v25,v89
M 30,a[228:231],50
v_exp_f32 v87,v87
M 26,a[232:235],54
R 50,1,4096
v_fma_f32 v86,v86,v69,v69
M 30,a[236:239],54
s_nop 0
v_exp_f32 v88,v88
M 26,a[240:243],58
R 54,1,5120
v_exp_f32 v89,v89
M 30,a[244:247],58
v_fma_f32 v87,v87,v69,v69
v_fma_f32 v88,v88,v69,v69
v_fma_f32 v89,v89,v69,v69
v_rcp_f32 v86,v86
v_rcp_f32 v87,v87
v_rcp_f32 v88,v88
v_rcp_f32 v89,v89
v_cmp_eq_u32 vcc,s53,v7
M 26,a[248:251],62
s_cmp_eq_u64 vcc,-1
R 58,1,6144
v_mul_f32 v22,v22,v86
v_mul_f32 v23,v23,v87
v_mul_f32 v24,v24,v88
v_mul_f32 v25,v25,v89
v_cvt_pk_bf16_f32 v92,v22,v23
v_cvt_pk_bf16_f32 v93,v24,v25
M 30,a[252:255],62
L 5
buffer_store_dwordx4 v[90:93],v0,s[36:39],s66 offen offset:0 sc0 sc1
v_xor_b32 v2,65536,v2
s_cbranch_scc1 Lg3o_L20_%=
s_cmp_eq_u32 vcc_lo,-1
s_cbranch_scc1 Lg3o_L10_%=
Lg3o_X0_%=:
v_mfma_f32_16x16x32_bf16 v[18:21],a[0:3],v[34:37],0
s_add_u32 m0,s59,0
s_waitcnt vmcnt(2)
R 62,1,7168
v_mfma_f32_16x16x32_bf16 v[22:25],a[4:7],v[34:37],0
s_nop 0
buffer_load_dword v5,s[36:39],s58 offen offset:0 sc1 lds
M 18,a[8:11],38
s_mov_b32 s66,s65
R 34,1,8192
v_add_f32_dpp v99,v98,v98 quad_perm:[1,0,3,2] row_mask:0xf bank_mask:0xf
M 22,a[12:15],38
L 5
v_fma_f32 v26,-v70,v74,v26
v_fma_f32 v27,-v70,v75,v27
M 18,a[16:19],42
s_add_u32 s65,s65,s62
R 38,1,9216
v_add_f32_dpp v99,v99,v99 quad_perm:[2,3,0,1] row_mask:0xf bank_mask:0xf
M 22,a[20:23],42
v_fma_f32 v28,-v70,v76,v28
v_fma_f32 v29,-v70,v77,v29
M 18,a[24:27],46
R 42,1,10240
v_add_f32_dpp v102,v99,v99 row_half_mirror row_mask:0xf bank_mask:0xf
M 22,a[28:31],46
L 5
v_mul_f32 v82,v26,v26
v_mul_f32 v83,v27,v27
M 18,a[32:35],50
R 46,1,11264
v_mov_b32_dpp v99,v102 row_ror:8 row_mask:0xf bank_mask:0xf
M 22,a[36:39],50
v_fma_f32 v99,-v102,v102,v99
v_max_f32 v99,s87,v99
M 18,a[40:43],54
R 50,1,12288
v_mul_f32 v84,v28,v28
M 22,a[44:47],54
L 5
v_rsq_f32 v100,v99
M 18,a[48:51],58
R 54,1,13312
v_mul_f32 v105,v99,v100
M 22,a[52:55],58
v_mul_f32 v103,s88,v100
v_mul_f32 v99,s89,v100
M 18,a[56:59],62
R 58,1,14336
v_mul_f32 v104,v103,v100
M 22,a[60:63],62
L 5
v_mul_f32 v104,v104,v99
v_mul_f32 v85,v29,v29
M 18,a[64:67],34
R 62,1,15360
v_fma_f32 v82,v72,v82,v71
M 22,a[68:71],34
ds_write_b128 v12,v[102:105] offset:0
M 18,a[72:75],38
R 34,1,16384
v_fma_f32 v83,v72,v83,v71
M 22,a[76:79],38
L 6
v_fma_f32 v84,v72,v84,v71
v_fma_f32 v85,v72,v85,v71
M 18,a[80:83],42
R 38,1,17408
v_mul_f32 v82,v26,v82
M 22,a[84:87],42
v_exp_f32 v82,v82
M 18,a[88:91],46
R 42,1,18432
v_mul_f32 v83,v27,v83
M 22,a[92:95],46
L 6
v_exp_f32 v83,v83
M 18,a[96:99],50
R 46,1,19456
v_mul_f32 v84,v28,v84
M 22,a[100:103],50
v_exp_f32 v84,v84
M 18,a[104:107],54
R 50,1,20480
v_mul_f32 v85,v29,v85
M 22,a[108:111],54
L 6
v_exp_f32 v85,v85
M 18,a[112:115],58
R 54,1,21504
v_fma_f32 v82,v82,v73,v73
M 22,a[116:119],58
v_rcp_f32 v82,v82
M 18,a[120:123],62
R 58,1,22528
v_fma_f32 v83,v83,v73,v73
M 22,a[124:127],62
L 5
v_rcp_f32 v83,v83
M 18,a[128:131],34
R 62,1,23552
v_fma_f32 v84,v84,v73,v73
M 22,a[132:135],34
v_rcp_f32 v84,v84
M 18,a[136:139],38
R 34,1,24576
v_fma_f32 v85,v85,v73,v73
M 22,a[140:143],38
L 5
v_rcp_f32 v85,v85
M 18,a[144:147],42
R 38,1,25600
v_mul_f32 v26,v26,v82
M 22,a[148:151],42
v_mul_f32 v27,v27,v83
v_mul_f32 v28,v28,v84
M 18,a[152:155],46
R 42,1,26624
v_mul_f32 v29,v29,v85
M 22,a[156:159],46
L 5
v_cvt_pk_bf16_f32 v90,v26,v27
v_cvt_pk_bf16_f32 v91,v28,v29
M 18,a[160:163],50
R 46,1,27648
v_fma_f32 v30,-v70,v78,v30
M 22,a[164:167],50
v_fma_f32 v31,-v70,v79,v31
v_fma_f32 v32,-v70,v80,v32
M 18,a[168:171],54
R 50,1,28672
v_fma_f32 v33,-v70,v81,v33
M 22,a[172:175],54
L 5
v_mul_f32 v86,v30,v30
v_mul_f32 v87,v31,v31
M 18,a[176:179],58
R 54,1,29696
v_mul_f32 v88,v32,v32
M 22,a[180:183],58
v_mul_f32 v89,v33,v33
v_fma_f32 v86,v72,v86,v71
M 18,a[184:187],62
R 58,1,30720
v_fma_f32 v87,v72,v87,v71
M 22,a[188:191],62
L 5
s_nop 0
v_fma_f32 v88,v72,v88,v71
M 18,a[192:195],34
s_waitcnt vmcnt(0)
R 62,1,31744
v_fma_f32 v89,v72,v89,v71
M 22,a[196:199],34
s_barrier
R 66,13,0
ds_read_b32 v7,v6 offset:0
M 18,a[200:203],38
buffer_store_dword v4,v3,s[36:39],s70 offen offset:0 sc0 sc1
M 22,a[204:207],38
L 6
v_mul_f32 v86,v30,v86
v_mul_f32 v87,v31,v87
M 18,a[208:211],42
s_mov_b32 s70,s69
v_exp_f32 v86,v86
M 22,a[212:215],42
s_mov_b32 s69,s68
v_exp_f32 v87,v87
M 18,a[216:219],46
L 0
v_mul_f32 v88,v32,v88
v_mul_f32 v89,v33,v89
M 22,a[220:223],46
s_nop 0
v_exp_f32 v88,v88
M 18,a[224:227],50
s_add_u32 s68,s68,s63
v_exp_f32 v89,v89
M 22,a[228:231],50
v_fma_f32 v86,v86,v73,v73
v_fma_f32 v87,v87,v73,v73
M 18,a[232:235],54
v_fma_f32 v88,v88,v73,v73
v_fma_f32 v89,v89,v73,v73
M 22,a[236:239],54
s_nop 0
v_rcp_f32 v86,v86
M 18,a[240:243],58
v_rcp_f32 v87,v87
M 22,a[244:247],58
v_rcp_f32 v88,v88
v_rcp_f32 v89,v89
M 18,a[248:251],62
v_mul_f32 v30,v30,v86
v_mul_f32 v31,v31,v87
v_mul_f32 v32,v32,v88
v_mul_f32 v33,v33,v89
v_cvt_pk_bf16_f32 v92,v30,v31
v_cvt_pk_bf16_f32 v93,v32,v33
M 22,a[252:255],62
buffer_store_dwordx4 v[90:93],v0,s[36:39],s67 offen offset:0 sc0 sc1
v_xor_b32 v1,65536,v1
v_cmp_eq_u32 vcc,s53,v7
s_mov_b32 s80,1
s_cmp_eq_u32 vcc_lo,-1
s_cbranch_scc0 Lg3o_xn0_%=
buffer_load_dword v98,v11,s[36:39],s73 offen offset:0 sc1
s_nop 0
v_fma_f32 v18,-v66,v74,v18
v_fma_f32 v19,-v66,v75,v19
v_fma_f32 v20,-v66,v76,v20
v_fma_f32 v21,-v66,v77,v21
v_mul_f32 v82,v18,v18
v_mul_f32 v83,v19,v19
s_add_u32 s73,s73,s64
s_mov_b32 m0,s57
s_add_u32 s56,s55,4096
D 0,55,0
v_mul_f32 v84,v20,v20
v_mul_f32 v85,v21,v21
v_fma_f32 v82,v68,v82,v67
v_fma_f32 v83,v68,v83,v67
v_fma_f32 v84,v68,v84,v67
v_fma_f32 v85,v68,v85,v67
D 0,55,1024
v_mul_f32 v82,v18,v82
v_mul_f32 v83,v19,v83
v_mul_f32 v84,v20,v84
v_mul_f32 v85,v21,v85
v_exp_f32 v82,v82
v_exp_f32 v83,v83
D 0,55,2048
v_exp_f32 v84,v84
v_exp_f32 v85,v85
v_fma_f32 v82,v82,v69,v69
v_fma_f32 v83,v83,v69,v69
v_fma_f32 v84,v84,v69,v69
v_fma_f32 v85,v85,v69,v69
D 0,55,3072
v_rcp_f32 v82,v82
v_rcp_f32 v83,v83
v_rcp_f32 v84,v84
v_rcp_f32 v85,v85
v_mul_f32 v18,v18,v82
v_mul_f32 v19,v19,v83
s_add_u32 m0,s57,4096
s_nop 0
D 0,56,0
v_mul_f32 v20,v20,v84
v_mul_f32 v21,v21,v85
v_cvt_pk_bf16_f32 v90,v18,v19
v_cvt_pk_bf16_f32 v91,v20,v21
v_fma_f32 v22,-v66,v78,v22
v_fma_f32 v23,-v66,v79,v23
D 0,56,1024
v_fma_f32 v24,-v66,v80,v24
v_fma_f32 v25,-v66,v81,v25
v_mul_f32 v86,v22,v22
v_mul_f32 v87,v23,v23
v_mul_f32 v88,v24,v24
v_mul_f32 v89,v25,v25
D 0,56,2048
v_fma_f32 v86,v68,v86,v67
v_fma_f32 v87,v68,v87,v67
v_fma_f32 v88,v68,v88,v67
v_fma_f32 v89,v68,v89,v67
v_mul_f32 v86,v22,v86
v_mul_f32 v87,v23,v87
D 0,56,3072
v_mul_f32 v88,v24,v88
v_mul_f32 v89,v25,v89
v_exp_f32 v86,v86
v_exp_f32 v87,v87
v_exp_f32 v88,v88
v_exp_f32 v89,v89
s_add_u32 s55,s55,s60
s_add_u32 s57,s57,32768
s_and_b32 s57,s57,131071
s_add_u32 s54,s54,s46
v_fma_f32 v86,v86,v69,v69
v_fma_f32 v87,v87,v69,v69
v_fma_f32 v88,v88,v69,v69
v_fma_f32 v89,v89,v69,v69
v_rcp_f32 v86,v86
v_rcp_f32 v87,v87
v_rcp_f32 v88,v88
v_rcp_f32 v89,v89
v_mul_f32 v22,v22,v86
v_mul_f32 v23,v23,v87
v_mul_f32 v24,v24,v88
v_mul_f32 v25,v25,v89
v_cvt_pk_bf16_f32 v92,v22,v23
v_cvt_pk_bf16_f32 v93,v24,v25
buffer_store_dwordx4 v[90:93],v0,s[36:39],s66 offen offset:0 sc0 sc1
s_mov_b32 s72,s69
s_mov_b32 s69,0x7fff0000
s_mov_b32 s66,0x7fff0000
s_mov_b32 s76,0x7fff0000
s_branch Lg3o_gox_%=
Lg3o_xn0_%=:
s_cmp_ge_u32 s54,256
s_cbranch_scc1 Lg3o_fin0_%=
s_add_u32 m0,s59,4096
s_nop 0
)"
R"(buffer_load_dword v5,s[36:39],s58 offen offset:0 sc1 lds
buffer_store_dword v4,v3,s[36:39],s71 offen offset:0 sc0 sc1
s_add_u32 m0,s59,5120
s_nop 0
buffer_load_dword v5,s[36:39],s58 offen offset:0 sc1 lds
buffer_store_dword v4,v3,s[36:39],s71 offen offset:0 sc0 sc1
s_mov_b32 s83,0
s_nop 0
v_fma_f32 v18,-v66,v74,v18
v_fma_f32 v19,-v66,v75,v19
v_fma_f32 v20,-v66,v76,v20
v_fma_f32 v21,-v66,v77,v21
v_mul_f32 v82,v18,v18
v_mul_f32 v83,v19,v19
v_mul_f32 v84,v20,v20
v_mul_f32 v85,v21,v21
v_fma_f32 v82,v68,v82,v67
v_fma_f32 v83,v68,v83,v67
v_fma_f32 v84,v68,v84,v67
v_fma_f32 v85,v68,v85,v67
v_mul_f32 v82,v18,v82
v_mul_f32 v83,v19,v83
v_mul_f32 v84,v20,v84
v_mul_f32 v85,v21,v85
v_exp_f32 v82,v82
v_exp_f32 v83,v83
v_exp_f32 v84,v84
v_exp_f32 v85,v85
v_fma_f32 v82,v82,v69,v69
v_fma_f32 v83,v83,v69,v69
v_fma_f32 v84,v84,v69,v69
v_fma_f32 v85,v85,v69,v69
v_rcp_f32 v82,v82
v_rcp_f32 v83,v83
v_rcp_f32 v84,v84
v_rcp_f32 v85,v85
v_mul_f32 v18,v18,v82
v_mul_f32 v19,v19,v83
v_mul_f32 v20,v20,v84
v_mul_f32 v21,v21,v85
v_cvt_pk_bf16_f32 v90,v18,v19
v_cvt_pk_bf16_f32 v91,v20,v21
v_fma_f32 v22,-v66,v78,v22
v_fma_f32 v23,-v66,v79,v23
v_fma_f32 v24,-v66,v80,v24
v_fma_f32 v25,-v66,v81,v25
v_mul_f32 v86,v22,v22
v_mul_f32 v87,v23,v23
v_mul_f32 v88,v24,v24
v_mul_f32 v89,v25,v25
v_fma_f32 v86,v68,v86,v67
v_fma_f32 v87,v68,v87,v67
v_fma_f32 v88,v68,v88,v67
v_fma_f32 v89,v68,v89,v67
v_mul_f32 v86,v22,v86
v_mul_f32 v87,v23,v87
v_mul_f32 v88,v24,v88
v_mul_f32 v89,v25,v89
v_exp_f32 v86,v86
v_exp_f32 v87,v87
v_exp_f32 v88,v88
v_exp_f32 v89,v89
v_fma_f32 v86,v86,v69,v69
v_fma_f32 v87,v87,v69,v69
v_fma_f32 v88,v88,v69,v69
v_fma_f32 v89,v89,v69,v69
v_rcp_f32 v86,v86
v_rcp_f32 v87,v87
v_rcp_f32 v88,v88
v_rcp_f32 v89,v89
v_mul_f32 v22,v22,v86
v_mul_f32 v23,v23,v87
v_mul_f32 v24,v24,v88
v_mul_f32 v25,v25,v89
v_cvt_pk_bf16_f32 v92,v22,v23
v_cvt_pk_bf16_f32 v93,v24,v25
buffer_store_dwordx4 v[90:93],v0,s[36:39],s66 offen offset:0 sc0 sc1
s_mov_b32 s72,s69
s_mov_b32 s69,0x7fff0000
s_mov_b32 s66,0x7fff0000
s_mov_b32 s76,0x7fff0000
s_branch Lg3o_poll_%=
Lg3o_fin0_%=:
s_nop 0
v_fma_f32 v18,-v66,v74,v18
v_fma_f32 v19,-v66,v75,v19
v_fma_f32 v20,-v66,v76,v20
v_fma_f32 v21,-v66,v77,v21
v_mul_f32 v82,v18,v18
v_mul_f32 v83,v19,v19
v_mul_f32 v84,v20,v20
v_mul_f32 v85,v21,v21
v_fma_f32 v82,v68,v82,v67
v_fma_f32 v83,v68,v83,v67
v_fma_f32 v84,v68,v84,v67
v_fma_f32 v85,v68,v85,v67
v_mul_f32 v82,v18,v82
v_mul_f32 v83,v19,v83
v_mul_f32 v84,v20,v84
v_mul_f32 v85,v21,v85
v_exp_f32 v82,v82
v_exp_f32 v83,v83
v_exp_f32 v84,v84
v_exp_f32 v85,v85
v_fma_f32 v82,v82,v69,v69
v_fma_f32 v83,v83,v69,v69
v_fma_f32 v84,v84,v69,v69
v_fma_f32 v85,v85,v69,v69
v_rcp_f32 v82,v82
v_rcp_f32 v83,v83
v_rcp_f32 v84,v84
v_rcp_f32 v85,v85
v_mul_f32 v18,v18,v82
v_mul_f32 v19,v19,v83
v_mul_f32 v20,v20,v84
v_mul_f32 v21,v21,v85
v_cvt_pk_bf16_f32 v90,v18,v19
v_cvt_pk_bf16_f32 v91,v20,v21
v_fma_f32 v22,-v66,v78,v22
v_fma_f32 v23,-v66,v79,v23
v_fma_f32 v24,-v66,v80,v24
v_fma_f32 v25,-v66,v81,v25
v_mul_f32 v86,v22,v22
v_mul_f32 v87,v23,v23
v_mul_f32 v88,v24,v24
v_mul_f32 v89,v25,v25
v_fma_f32 v86,v68,v86,v67
v_fma_f32 v87,v68,v87,v67
v_fma_f32 v88,v68,v88,v67
v_fma_f32 v89,v68,v89,v67
v_mul_f32 v86,v22,v86
v_mul_f32 v87,v23,v87
v_mul_f32 v88,v24,v88
v_mul_f32 v89,v25,v89
v_exp_f32 v86,v86
v_exp_f32 v87,v87
v_exp_f32 v88,v88
v_exp_f32 v89,v89
v_fma_f32 v86,v86,v69,v69
v_fma_f32 v87,v87,v69,v69
v_fma_f32 v88,v88,v69,v69
v_fma_f32 v89,v89,v69,v69
v_rcp_f32 v86,v86
v_rcp_f32 v87,v87
v_rcp_f32 v88,v88
v_rcp_f32 v89,v89
v_mul_f32 v22,v22,v86
v_mul_f32 v23,v23,v87
v_mul_f32 v24,v24,v88
v_mul_f32 v25,v25,v89
v_cvt_pk_bf16_f32 v92,v22,v23
v_cvt_pk_bf16_f32 v93,v24,v25
buffer_store_dwordx4 v[90:93],v0,s[36:39],s66 offen offset:0 sc0 sc1
s_mov_b32 s72,s69
s_mov_b32 s69,0x7fff0000
s_mov_b32 s66,0x7fff0000
s_mov_b32 s76,0x7fff0000
s_branch Lg3o_done_%=
Lg3o_L21_%=:
v_mfma_f32_16x16x32_bf16 v[26:29],a[0:3],v[34:37],0
s_add_u32 s58,s58,s61
s_add_u32 s58,s58,s61
R 62,2,7168
v_mfma_f32_16x16x32_bf16 v[30:33],a[4:7],v[34:37],0
s_add_u32 m0,s59,1024
s_add_u32 s56,s55,4096
s_waitcnt vmcnt(2)
M 26,a[8:11],38
s_nop 0
R 34,2,8192
v_add_f32_dpp v99,v98,v98 quad_perm:[1,0,3,2] row_mask:0xf bank_mask:0xf
M 30,a[12:15],38
L 5
buffer_load_dword v8,s[36:39],s58 offen offset:0 sc1 lds
M 26,a[16:19],42
s_mov_b32 m0,s57
R 38,2,9216
v_add_f32_dpp v99,v99,v99 quad_perm:[2,3,0,1] row_mask:0xf bank_mask:0xf
M 30,a[20:23],42
s_mov_b32 s67,s65
buffer_load_dword v98,v11,s[36:39],s73 offen offset:0 sc1
M 26,a[24:27],46
R 42,2,10240
v_add_f32_dpp v102,v99,v99 row_half_mirror row_mask:0xf bank_mask:0xf
M 30,a[28:31],46
L 5
D 0,55,0
M 26,a[32:35],50
R 46,2,11264
v_mov_b32_dpp v99,v102 row_ror:8 row_mask:0xf bank_mask:0xf
M 30,a[36:39],50
v_fma_f32 v99,-v102,v102,v99
v_max_f32 v99,s87,v99
M 26,a[40:43],54
R 50,2,12288
v_fma_f32 v18,-v66,v74,v18
M 30,a[44:47],54
L 5
D 0,55,1024
M 26,a[48:51],58
R 54,2,13312
v_fma_f32 v19,-v66,v75,v19
M 30,a[52:55],58
v_rsq_f32 v100,v99
M 26,a[56:59],62
R 58,2,14336
v_mul_f32 v105,v99,v100
M 30,a[60:63],62
L 5
D 0,55,2048
M 26,a[64:67],34
R 62,2,15360
v_mul_f32 v103,s88,v100
M 30,a[68:71],34
v_mul_f32 v99,s89,v100
v_mul_f32 v104,v103,v100
M 26,a[72:75],38
R 34,2,16384
v_mul_f32 v104,v104,v99
M 30,a[76:79],38
L 5
ds_write_b128 v12,v[102:105] offset:2048
M 26,a[80:83],42
R 38,2,17408
v_fma_f32 v20,-v66,v76,v20
M 30,a[84:87],42
D 0,55,3072
M 26,a[88:91],46
s_add_u32 m0,s57,4096
R 42,2,18432
v_fma_f32 v21,-v66,v77,v21
M 30,a[92:95],46
L 6
D 0,56,0
M 26,a[96:99],50
s_add_u32 s55,s55,s60
R 46,2,19456
v_mul_f32 v82,v18,v18
M 30,a[100:103],50
s_add_u32 s57,s57,32768
v_mul_f32 v83,v19,v19
v_mul_f32 v84,v20,v20
M 26,a[104:107],54
s_and_b32 s57,s57,131071
R 50,2,20480
v_mul_f32 v85,v21,v21
M 30,a[108:111],54
L 6
D 0,56,1024
M 26,a[112:115],58
s_add_u32 s54,s54,s46
R 54,2,21504
v_fma_f32 v82,v68,v82,v67
M 30,a[116:119],58
v_fma_f32 v83,v68,v83,v67
v_fma_f32 v84,v68,v84,v67
M 26,a[120:123],62
R 58,2,22528
v_fma_f32 v85,v68,v85,v67
M 30,a[124:127],62
L 5
D 0,56,2048
M 26,a[128:131],34
R 62,2,23552
v_mul_f32 v82,v18,v82
M 30,a[132:135],34
v_exp_f32 v82,v82
M 26,a[136:139],38
R 34,2,24576
v_mul_f32 v83,v19,v83
M 30,a[140:143],38
L 5
D 0,56,3072
M 26,a[144:147],42
s_mov_b32 m0,s57
R 38,2,25600
v_mul_f32 v84,v20,v84
M 30,a[148:151],42
s_add_u32 s56,s55,4096
v_exp_f32 v83,v83
M 26,a[152:155],46
R 42,2,26624
v_mul_f32 v85,v21,v85
M 30,a[156:159],46
L 5
D 0,55,0
M 26,a[160:163],50
R 46,2,27648
v_fma_f32 v82,v82,v69,v69
M 30,a[164:167],50
D 0,55,1024
M 26,a[168:171],54
R 50,2,28672
v_fma_f32 v83,v83,v69,v69
M 30,a[172:175],54
L 5
v_exp_f32 v84,v84
M 26,a[176:179],58
R 54,2,29696
v_fma_f32 v84,v84,v69,v69
M 30,a[180:183],58
D 0,55,2048
M 26,a[184:187],62
R 58,2,30720
v_fma_f32 v22,-v66,v78,v22
M 30,a[188:191],62
L 5
s_nop 0
D 0,55,3072
M 26,a[192:195],34
s_waitcnt vmcnt(4)
R 62,2,31744
v_fma_f32 v23,-v66,v79,v23
M 30,a[196:199],34
s_barrier
R 70,13,2048
ds_read_b32 v7,v6 offset:1024
M 26,a[200:203],38
s_add_u32 m0,s57,4096
R 34,1,0
v_fma_f32 v24,-v66,v80,v24
M 30,a[204:207],38
L 7
buffer_store_dword v4,v3,s[36:39],s70 offen offset:0 sc0 sc1
M 26,a[208:211],42
s_add_u32 s55,s55,s60
R 38,1,1024
v_fma_f32 v25,-v66,v81,v25
M 30,a[212:215],42
s_add_u32 s57,s57,32768
D 0,56,0
M 26,a[216:219],46
s_and_b32 s57,s57,131071
R 42,1,2048
v_mul_f32 v86,v22,v22
M 30,a[220:223],46
L 7
D 0,56,1024
M 26,a[224:227],50
s_add_u32 s54,s54,s46
R 46,1,3072
v_mul_f32 v87,v23,v23
M 30,a[228:231],50
s_add_u32 s73,s73,s64
D 0,56,2048
M 26,a[232:235],54
s_mov_b32 s70,s69
R 50,1,4096
v_mul_f32 v88,v24,v24
M 30,a[236:239],54
L 7
D 0,56,3072
M 26,a[240:243],58
s_add_u32 s65,s65,s62
R 54,1,5120
v_exp_f32 v85,v85
v_mul_f32 v89,v25,v25
v_fma_f32 v86,v68,v86,v67
v_fma_f32 v87,v68,v87,v67
v_fma_f32 v88,v68,v88,v67
v_fma_f32 v89,v68,v89,v67
v_mul_f32 v86,v22,v86
v_mul_f32 v87,v23,v87
v_mul_f32 v88,v24,v88
v_mul_f32 v89,v25,v89
v_exp_f32 v86,v86
v_exp_f32 v87,v87
v_exp_f32 v88,v88
v_exp_f32 v89,v89
M 30,a[244:247],58
s_mov_b32 s69,s68
L 6
v_fma_f32 v85,v85,v69,v69
v_rcp_f32 v82,v82
v_rcp_f32 v83,v83
v_rcp_f32 v84,v84
v_rcp_f32 v85,v85
v_fma_f32 v86,v86,v69,v69
v_fma_f32 v87,v87,v69,v69
v_fma_f32 v88,v88,v69,v69
v_fma_f32 v89,v89,v69,v69
v_rcp_f32 v86,v86
v_rcp_f32 v87,v87
v_rcp_f32 v88,v88
v_rcp_f32 v89,v89
v_cmp_eq_u32 vcc,s53,v7
M 26,a[248:251],62
s_add_u32 s68,s68,s63
s_cmp_eq_u32 vcc_lo,-1
R 58,1,6144
v_mul_f32 v18,v18,v82
v_mul_f32 v19,v19,v83
v_mul_f32 v20,v20,v84
v_mul_f32 v21,v21,v85
v_cvt_pk_bf16_f32 v90,v18,v19
v_cvt_pk_bf16_f32 v91,v20,v21
v_mul_f32 v22,v22,v86
v_mul_f32 v23,v23,v87
v_mul_f32 v24,v24,v88
v_mul_f32 v25,v25,v89
v_cvt_pk_bf16_f32 v92,v22,v23
v_cvt_pk_bf16_f32 v93,v24,v25
M 30,a[252:255],62
L 5
buffer_store_dwordx4 v[90:93],v0,s[36:39],s66 offen offset:0 sc0 sc1
v_xor_b32 v2,65536,v2
s_cbranch_scc1 Lg3o_S0_%=
Lg3o_N0_%=:
v_mfma_f32_16x16x32_bf16 v[18:21],a[0:3],v[34:37],0
s_add_u32 m0,s59,0
s_waitcnt vmcnt(10)
R 62,1,7168
v_mfma_f32_16x16x32_bf16 v[22:25],a[4:7],v[34:37],0
s_nop 0
buffer_load_dword v5,s[36:39],s58 offen offset:0 sc1 lds
M 18,a[8:11],38
s_mov_b32 s66,s65
R 34,1,8192
v_add_f32_dpp v99,v98,v98 quad_perm:[1,0,3,2] row_mask:0xf bank_mask:0xf
M 22,a[12:15],38
L 5
buffer_load_dword v98,v11,s[36:39],s73 offen offset:0 sc1
M 18,a[16:19],42
s_add_u32 s73,s73,s64
R 38,1,9216
v_add_f32_dpp v99,v99,v99 quad_perm:[2,3,0,1] row_mask:0xf bank_mask:0xf
M 22,a[20:23],42
s_add_u32 s65,s65,s62
v_fma_f32 v26,-v70,v74,v26
v_fma_f32 v27,-v70,v75,v27
M 18,a[24:27],46
R 42,1,10240
v_add_f32_dpp v102,v99,v99 row_half_mirror row_mask:0xf bank_mask:0xf
M 22,a[28:31],46
L 5
v_fma_f32 v28,-v70,v76,v28
v_fma_f32 v29,-v70,v77,v29
M 18,a[32:35],50
R 46,1,11264
v_mov_b32_dpp v99,v102 row_ror:8 row_mask:0xf bank_mask:0xf
M 22,a[36:39],50
v_fma_f32 v99,-v102,v102,v99
v_max_f32 v99,s87,v99
M 18,a[40:43],54
R 50,1,12288
v_mul_f32 v82,v26,v26
M 22,a[44:47],54
L 5
v_rsq_f32 v100,v99
M 18,a[48:51],58
R 54,1,13312
v_mul_f32 v105,v99,v100
M 22,a[52:55],58
v_mul_f32 v103,s88,v100
v_mul_f32 v99,s89,v100
M 18,a[56:59],62
R 58,1,14336
v_mul_f32 v104,v103,v100
M 22,a[60:63],62
L 5
v_mul_f32 v104,v104,v99
v_mul_f32 v83,v27,v27
M 18,a[64:67],34
R 62,1,15360
v_mul_f32 v84,v28,v28
M 22,a[68:71],34
ds_write_b128 v12,v[102:105] offset:0
M 18,a[72:75],38
R 34,1,16384
v_mul_f32 v85,v29,v29
M 22,a[76:79],38
L 6
v_fma_f32 v82,v72,v82,v71
v_fma_f32 v83,v72,v83,v71
M 18,a[80:83],42
R 38,1,17408
v_fma_f32 v84,v72,v84,v71
M 22,a[84:87],42
v_fma_f32 v85,v72,v85,v71
v_mul_f32 v82,v26,v82
M 18,a[88:91],46
R 42,1,18432
v_mul_f32 v83,v27,v83
M 22,a[92:95],46
L 6
v_exp_f32 v82,v82
M 18,a[96:99],50
R 46,1,19456
v_mul_f32 v84,v28,v84
M 22,a[100:103],50
v_exp_f32 v83,v83
M 18,a[104:107],54
R 50,1,20480
v_mul_f32 v85,v29,v85
M 22,a[108:111],54
L 6
v_exp_f32 v84,v84
M 18,a[112:115],58
R 54,1,21504
v_fma_f32 v82,v82,v73,v73
)"
R"(M 22,a[116:119],58
v_exp_f32 v85,v85
M 18,a[120:123],62
R 58,1,22528
v_fma_f32 v83,v83,v73,v73
M 22,a[124:127],62
L 5
v_rcp_f32 v82,v82
M 18,a[128:131],34
R 62,1,23552
v_fma_f32 v84,v84,v73,v73
M 22,a[132:135],34
v_rcp_f32 v83,v83
M 18,a[136:139],38
R 34,1,24576
v_fma_f32 v85,v85,v73,v73
M 22,a[140:143],38
L 5
v_rcp_f32 v84,v84
M 18,a[144:147],42
R 38,1,25600
v_mul_f32 v26,v26,v82
M 22,a[148:151],42
v_rcp_f32 v85,v85
M 18,a[152:155],46
R 42,1,26624
v_mul_f32 v27,v27,v83
M 22,a[156:159],46
L 5
v_mul_f32 v28,v28,v84
v_mul_f32 v29,v29,v85
M 18,a[160:163],50
R 46,1,27648
v_cvt_pk_bf16_f32 v90,v26,v27
M 22,a[164:167],50
v_cvt_pk_bf16_f32 v91,v28,v29
v_fma_f32 v30,-v70,v78,v30
M 18,a[168:171],54
R 50,1,28672
v_fma_f32 v31,-v70,v79,v31
M 22,a[172:175],54
L 5
v_fma_f32 v32,-v70,v80,v32
v_fma_f32 v33,-v70,v81,v33
M 18,a[176:179],58
R 54,1,29696
v_mul_f32 v86,v30,v30
M 22,a[180:183],58
v_mul_f32 v87,v31,v31
v_mul_f32 v88,v32,v32
M 18,a[184:187],62
R 58,1,30720
v_mul_f32 v89,v33,v33
M 22,a[188:191],62
L 5
s_nop 0
v_fma_f32 v86,v72,v86,v71
M 18,a[192:195],34
s_waitcnt vmcnt(1)
R 62,1,31744
v_fma_f32 v87,v72,v87,v71
M 22,a[196:199],34
s_barrier
R 66,13,0
ds_read_b32 v7,v6 offset:0
M 18,a[200:203],38
R 34,2,0
v_fma_f32 v88,v72,v88,v71
M 22,a[204:207],38
L 7
buffer_store_dword v4,v3,s[36:39],s70 offen offset:0 sc0 sc1
M 18,a[208:211],42
s_mov_b32 s70,s69
R 38,2,1024
v_fma_f32 v89,v72,v89,v71
M 22,a[212:215],42
s_mov_b32 s69,s68
v_mul_f32 v86,v30,v86
v_mul_f32 v87,v31,v87
M 18,a[216:219],46
L 2
R 42,2,2048
v_mul_f32 v88,v32,v88
M 22,a[220:223],46
s_nop 0
v_exp_f32 v86,v86
M 18,a[224:227],50
s_add_u32 s68,s68,s63
R 46,2,3072
v_mul_f32 v89,v33,v89
M 22,a[228:231],50
v_exp_f32 v87,v87
M 18,a[232:235],54
R 50,2,4096
v_fma_f32 v86,v86,v73,v73
M 22,a[236:239],54
s_nop 0
v_exp_f32 v88,v88
M 18,a[240:243],58
R 54,2,5120
v_exp_f32 v89,v89
M 22,a[244:247],58
v_fma_f32 v87,v87,v73,v73
v_fma_f32 v88,v88,v73,v73
v_fma_f32 v89,v89,v73,v73
v_rcp_f32 v86,v86
v_rcp_f32 v87,v87
v_rcp_f32 v88,v88
v_rcp_f32 v89,v89
v_cmp_eq_u32 vcc,s53,v7
M 18,a[248:251],62
s_cmp_eq_u64 vcc,-1
R 58,2,6144
v_mul_f32 v30,v30,v86
v_mul_f32 v31,v31,v87
v_mul_f32 v32,v32,v88
v_mul_f32 v33,v33,v89
v_cvt_pk_bf16_f32 v92,v30,v31
v_cvt_pk_bf16_f32 v93,v32,v33
M 22,a[252:255],62
L 5
buffer_store_dwordx4 v[90:93],v0,s[36:39],s67 offen offset:0 sc0 sc1
v_xor_b32 v1,65536,v1
s_cbranch_scc1 Lg3o_L21_%=
s_cmp_eq_u32 vcc_lo,-1
s_cbranch_scc1 Lg3o_L11_%=
Lg3o_X1_%=:
v_mfma_f32_16x16x32_bf16 v[26:29],a[0:3],v[34:37],0
s_add_u32 m0,s59,1024
s_waitcnt vmcnt(2)
R 62,2,7168
v_mfma_f32_16x16x32_bf16 v[30:33],a[4:7],v[34:37],0
s_nop 0
buffer_load_dword v5,s[36:39],s58 offen offset:0 sc1 lds
M 26,a[8:11],38
s_mov_b32 s67,s65
R 34,2,8192
v_add_f32_dpp v99,v98,v98 quad_perm:[1,0,3,2] row_mask:0xf bank_mask:0xf
M 30,a[12:15],38
L 5
v_fma_f32 v18,-v66,v74,v18
v_fma_f32 v19,-v66,v75,v19
M 26,a[16:19],42
s_add_u32 s65,s65,s62
R 38,2,9216
v_add_f32_dpp v99,v99,v99 quad_perm:[2,3,0,1] row_mask:0xf bank_mask:0xf
M 30,a[20:23],42
v_fma_f32 v20,-v66,v76,v20
v_fma_f32 v21,-v66,v77,v21
M 26,a[24:27],46
R 42,2,10240
v_add_f32_dpp v102,v99,v99 row_half_mirror row_mask:0xf bank_mask:0xf
M 30,a[28:31],46
L 5
v_mul_f32 v82,v18,v18
v_mul_f32 v83,v19,v19
M 26,a[32:35],50
R 46,2,11264
v_mov_b32_dpp v99,v102 row_ror:8 row_mask:0xf bank_mask:0xf
M 30,a[36:39],50
v_fma_f32 v99,-v102,v102,v99
v_max_f32 v99,s87,v99
M 26,a[40:43],54
R 50,2,12288
v_mul_f32 v84,v20,v20
M 30,a[44:47],54
L 5
v_rsq_f32 v100,v99
M 26,a[48:51],58
R 54,2,13312
v_mul_f32 v105,v99,v100
M 30,a[52:55],58
v_mul_f32 v103,s88,v100
v_mul_f32 v99,s89,v100
M 26,a[56:59],62
R 58,2,14336
v_mul_f32 v104,v103,v100
M 30,a[60:63],62
L 5
v_mul_f32 v104,v104,v99
v_mul_f32 v85,v21,v21
M 26,a[64:67],34
R 62,2,15360
v_fma_f32 v82,v68,v82,v67
M 30,a[68:71],34
ds_write_b128 v12,v[102:105] offset:2048
M 26,a[72:75],38
R 34,2,16384
v_fma_f32 v83,v68,v83,v67
M 30,a[76:79],38
L 6
v_fma_f32 v84,v68,v84,v67
v_fma_f32 v85,v68,v85,v67
M 26,a[80:83],42
R 38,2,17408
v_mul_f32 v82,v18,v82
M 30,a[84:87],42
v_exp_f32 v82,v82
M 26,a[88:91],46
R 42,2,18432
v_mul_f32 v83,v19,v83
M 30,a[92:95],46
L 6
v_exp_f32 v83,v83
M 26,a[96:99],50
R 46,2,19456
v_mul_f32 v84,v20,v84
M 30,a[100:103],50
v_exp_f32 v84,v84
M 26,a[104:107],54
R 50,2,20480
v_mul_f32 v85,v21,v85
M 30,a[108:111],54
L 6
v_exp_f32 v85,v85
M 26,a[112:115],58
R 54,2,21504
v_fma_f32 v82,v82,v69,v69
M 30,a[116:119],58
v_rcp_f32 v82,v82
M 26,a[120:123],62
R 58,2,22528
v_fma_f32 v83,v83,v69,v69
M 30,a[124:127],62
L 5
v_rcp_f32 v83,v83
M 26,a[128:131],34
R 62,2,23552
v_fma_f32 v84,v84,v69,v69
M 30,a[132:135],34
v_rcp_f32 v84,v84
M 26,a[136:139],38
R 34,2,24576
v_fma_f32 v85,v85,v69,v69
M 30,a[140:143],38
L 5
v_rcp_f32 v85,v85
M 26,a[144:147],42
R 38,2,25600
v_mul_f32 v18,v18,v82
M 30,a[148:151],42
v_mul_f32 v19,v19,v83
v_mul_f32 v20,v20,v84
M 26,a[152:155],46
R 42,2,26624
v_mul_f32 v21,v21,v85
M 30,a[156:159],46
L 5
v_cvt_pk_bf16_f32 v90,v18,v19
v_cvt_pk_bf16_f32 v91,v20,v21
M 26,a[160:163],50
R 46,2,27648
v_fma_f32 v22,-v66,v78,v22
M 30,a[164:167],50
v_fma_f32 v23,-v66,v79,v23
v_fma_f32 v24,-v66,v80,v24
M 26,a[168:171],54
R 50,2,28672
v_fma_f32 v25,-v66,v81,v25
M 30,a[172:175],54
L 5
v_mul_f32 v86,v22,v22
v_mul_f32 v87,v23,v23
M 26,a[176:179],58
R 54,2,29696
v_mul_f32 v88,v24,v24
M 30,a[180:183],58
v_mul_f32 v89,v25,v25
v_fma_f32 v86,v68,v86,v67
M 26,a[184:187],62
R 58,2,30720
v_fma_f32 v87,v68,v87,v67
M 30,a[188:191],62
L 5
s_nop 0
v_fma_f32 v88,v68,v88,v67
M 26,a[192:195],34
s_waitcnt vmcnt(0)
R 62,2,31744
v_fma_f32 v89,v68,v89,v67
M 30,a[196:199],34
s_barrier
R 70,13,2048
ds_read_b32 v7,v6 offset:1024
M 26,a[200:203],38
buffer_store_dword v4,v3,s[36:39],s70 offen offset:0 sc0 sc1
M 30,a[204:207],38
L 6
v_mul_f32 v86,v22,v86
v_mul_f32 v87,v23,v87
M 26,a[208:211],42
s_mov_b32 s70,s69
v_exp_f32 v86,v86
M 30,a[212:215],42
s_mov_b32 s69,s68
v_exp_f32 v87,v87
M 26,a[216:219],46
L 0
v_mul_f32 v88,v24,v88
v_mul_f32 v89,v25,v89
M 30,a[220:223],46
s_nop 0
v_exp_f32 v88,v88
M 26,a[224:227],50
s_add_u32 s68,s68,s63
v_exp_f32 v89,v89
M 30,a[228:231],50
v_fma_f32 v86,v86,v69,v69
v_fma_f32 v87,v87,v69,v69
M 26,a[232:235],54
v_fma_f32 v88,v88,v69,v69
v_fma_f32 v89,v89,v69,v69
M 30,a[236:239],54
s_nop 0
v_rcp_f32 v86,v86
M 26,a[240:243],58
v_rcp_f32 v87,v87
M 30,a[244:247],58
v_rcp_f32 v88,v88
v_rcp_f32 v89,v89
M 26,a[248:251],62
v_mul_f32 v22,v22,v86
v_mul_f32 v23,v23,v87
v_mul_f32 v24,v24,v88
v_mul_f32 v25,v25,v89
v_cvt_pk_bf16_f32 v92,v22,v23
v_cvt_pk_bf16_f32 v93,v24,v25
M 30,a[252:255],62
buffer_store_dwordx4 v[90:93],v0,s[36:39],s66 offen offset:0 sc0 sc1
v_xor_b32 v2,65536,v2
v_cmp_eq_u32 vcc,s53,v7
s_mov_b32 s80,0
s_cmp_eq_u32 vcc_lo,-1
s_cbranch_scc0 Lg3o_xn1_%=
buffer_load_dword v98,v11,s[36:39],s73 offen offset:0 sc1
s_nop 0
v_fma_f32 v26,-v70,v74,v26
v_fma_f32 v27,-v70,v75,v27
v_fma_f32 v28,-v70,v76,v28
v_fma_f32 v29,-v70,v77,v29
v_mul_f32 v82,v26,v26
v_mul_f32 v83,v27,v27
s_add_u32 s73,s73,s64
s_mov_b32 m0,s57
s_add_u32 s56,s55,4096
D 0,55,0
v_mul_f32 v84,v28,v28
v_mul_f32 v85,v29,v29
v_fma_f32 v82,v72,v82,v71
v_fma_f32 v83,v72,v83,v71
v_fma_f32 v84,v72,v84,v71
v_fma_f32 v85,v72,v85,v71
D 0,55,1024
v_mul_f32 v82,v26,v82
v_mul_f32 v83,v27,v83
v_mul_f32 v84,v28,v84
v_mul_f32 v85,v29,v85
v_exp_f32 v82,v82
v_exp_f32 v83,v83
D 0,55,2048
v_exp_f32 v84,v84
v_exp_f32 v85,v85
v_fma_f32 v82,v82,v73,v73
v_fma_f32 v83,v83,v73,v73
v_fma_f32 v84,v84,v73,v73
v_fma_f32 v85,v85,v73,v73
D 0,55,3072
v_rcp_f32 v82,v82
v_rcp_f32 v83,v83
v_rcp_f32 v84,v84
v_rcp_f32 v85,v85
v_mul_f32 v26,v26,v82
v_mul_f32 v27,v27,v83
s_add_u32 m0,s57,4096
s_nop 0
D 0,56,0
v_mul_f32 v28,v28,v84
v_mul_f32 v29,v29,v85
v_cvt_pk_bf16_f32 v90,v26,v27
v_cvt_pk_bf16_f32 v91,v28,v29
v_fma_f32 v30,-v70,v78,v30
v_fma_f32 v31,-v70,v79,v31
D 0,56,1024
v_fma_f32 v32,-v70,v80,v32
v_fma_f32 v33,-v70,v81,v33
v_mul_f32 v86,v30,v30
v_mul_f32 v87,v31,v31
v_mul_f32 v88,v32,v32
v_mul_f32 v89,v33,v33
D 0,56,2048
v_fma_f32 v86,v72,v86,v71
v_fma_f32 v87,v72,v87,v71
v_fma_f32 v88,v72,v88,v71
v_fma_f32 v89,v72,v89,v71
v_mul_f32 v86,v30,v86
v_mul_f32 v87,v31,v87
D 0,56,3072
v_mul_f32 v88,v32,v88
v_mul_f32 v89,v33,v89
v_exp_f32 v86,v86
v_exp_f32 v87,v87
v_exp_f32 v88,v88
v_exp_f32 v89,v89
s_add_u32 s55,s55,s60
s_add_u32 s57,s57,32768
s_and_b32 s57,s57,131071
s_add_u32 s54,s54,s46
v_fma_f32 v86,v86,v73,v73
v_fma_f32 v87,v87,v73,v73
v_fma_f32 v88,v88,v73,v73
v_fma_f32 v89,v89,v73,v73
v_rcp_f32 v86,v86
v_rcp_f32 v87,v87
v_rcp_f32 v88,v88
v_rcp_f32 v89,v89
v_mul_f32 v30,v30,v86
v_mul_f32 v31,v31,v87
v_mul_f32 v32,v32,v88
v_mul_f32 v33,v33,v89
v_cvt_pk_bf16_f32 v92,v30,v31
v_cvt_pk_bf16_f32 v93,v32,v33
buffer_store_dwordx4 v[90:93],v0,s[36:39],s67 offen offset:0 sc0 sc1
s_mov_b32 s72,s69
s_mov_b32 s69,0x7fff0000
s_mov_b32 s67,0x7fff0000
s_mov_b32 s77,0x7fff0000
s_branch Lg3o_gox_%=
Lg3o_xn1_%=:
s_cmp_ge_u32 s54,256
s_cbranch_scc1 Lg3o_fin1_%=
s_add_u32 m0,s59,4096
s_nop 0
buffer_load_dword v5,s[36:39],s58 offen offset:0 sc1 lds
buffer_store_dword v4,v3,s[36:39],s71 offen offset:0 sc0 sc1
s_add_u32 m0,s59,5120
s_nop 0
buffer_load_dword v5,s[36:39],s58 offen offset:0 sc1 lds
buffer_store_dword v4,v3,s[36:39],s71 offen offset:0 sc0 sc1
s_mov_b32 s83,0
s_nop 0
v_fma_f32 v26,-v70,v74,v26
v_fma_f32 v27,-v70,v75,v27
v_fma_f32 v28,-v70,v76,v28
v_fma_f32 v29,-v70,v77,v29
v_mul_f32 v82,v26,v26
v_mul_f32 v83,v27,v27
v_mul_f32 v84,v28,v28
v_mul_f32 v85,v29,v29
v_fma_f32 v82,v72,v82,v71
v_fma_f32 v83,v72,v83,v71
v_fma_f32 v84,v72,v84,v71
v_fma_f32 v85,v72,v85,v71
v_mul_f32 v82,v26,v82
v_mul_f32 v83,v27,v83
v_mul_f32 v84,v28,v84
v_mul_f32 v85,v29,v85
v_exp_f32 v82,v82
v_exp_f32 v83,v83
v_exp_f32 v84,v84
v_exp_f32 v85,v85
v_fma_f32 v82,v82,v73,v73
v_fma_f32 v83,v83,v73,v73
v_fma_f32 v84,v84,v73,v73
v_fma_f32 v85,v85,v73,v73
v_rcp_f32 v82,v82
v_rcp_f32 v83,v83
v_rcp_f32 v84,v84
v_rcp_f32 v85,v85
v_mul_f32 v26,v26,v82
v_mul_f32 v27,v27,v83
v_mul_f32 v28,v28,v84
v_mul_f32 v29,v29,v85
v_cvt_pk_bf16_f32 v90,v26,v27
v_cvt_pk_bf16_f32 v91,v28,v29
v_fma_f32 v30,-v70,v78,v30
v_fma_f32 v31,-v70,v79,v31
v_fma_f32 v32,-v70,v80,v32
v_fma_f32 v33,-v70,v81,v33
v_mul_f32 v86,v30,v30
v_mul_f32 v87,v31,v31
v_mul_f32 v88,v32,v32
v_mul_f32 v89,v33,v33
v_fma_f32 v86,v72,v86,v71
v_fma_f32 v87,v72,v87,v71
v_fma_f32 v88,v72,v88,v71
v_fma_f32 v89,v72,v89,v71
v_mul_f32 v86,v30,v86
v_mul_f32 v87,v31,v87
v_mul_f32 v88,v32,v88
v_mul_f32 v89,v33,v89
v_exp_f32 v86,v86
v_exp_f32 v87,v87
v_exp_f32 v88,v88
v_exp_f32 v89,v89
v_fma_f32 v86,v86,v73,v73
v_fma_f32 v87,v87,v73,v73
v_fma_f32 v88,v88,v73,v73
v_fma_f32 v89,v89,v73,v73
v_rcp_f32 v86,v86
v_rcp_f32 v87,v87
)"
R"(v_rcp_f32 v88,v88
v_rcp_f32 v89,v89
v_mul_f32 v30,v30,v86
v_mul_f32 v31,v31,v87
v_mul_f32 v32,v32,v88
v_mul_f32 v33,v33,v89
v_cvt_pk_bf16_f32 v92,v30,v31
v_cvt_pk_bf16_f32 v93,v32,v33
buffer_store_dwordx4 v[90:93],v0,s[36:39],s67 offen offset:0 sc0 sc1
s_mov_b32 s72,s69
s_mov_b32 s69,0x7fff0000
s_mov_b32 s67,0x7fff0000
s_mov_b32 s77,0x7fff0000
s_branch Lg3o_poll_%=
Lg3o_fin1_%=:
s_nop 0
v_fma_f32 v26,-v70,v74,v26
v_fma_f32 v27,-v70,v75,v27
v_fma_f32 v28,-v70,v76,v28
v_fma_f32 v29,-v70,v77,v29
v_mul_f32 v82,v26,v26
v_mul_f32 v83,v27,v27
v_mul_f32 v84,v28,v28
v_mul_f32 v85,v29,v29
v_fma_f32 v82,v72,v82,v71
v_fma_f32 v83,v72,v83,v71
v_fma_f32 v84,v72,v84,v71
v_fma_f32 v85,v72,v85,v71
v_mul_f32 v82,v26,v82
v_mul_f32 v83,v27,v83
v_mul_f32 v84,v28,v84
v_mul_f32 v85,v29,v85
v_exp_f32 v82,v82
v_exp_f32 v83,v83
v_exp_f32 v84,v84
v_exp_f32 v85,v85
v_fma_f32 v82,v82,v73,v73
v_fma_f32 v83,v83,v73,v73
v_fma_f32 v84,v84,v73,v73
v_fma_f32 v85,v85,v73,v73
v_rcp_f32 v82,v82
v_rcp_f32 v83,v83
v_rcp_f32 v84,v84
v_rcp_f32 v85,v85
v_mul_f32 v26,v26,v82
v_mul_f32 v27,v27,v83
v_mul_f32 v28,v28,v84
v_mul_f32 v29,v29,v85
v_cvt_pk_bf16_f32 v90,v26,v27
v_cvt_pk_bf16_f32 v91,v28,v29
v_fma_f32 v30,-v70,v78,v30
v_fma_f32 v31,-v70,v79,v31
v_fma_f32 v32,-v70,v80,v32
v_fma_f32 v33,-v70,v81,v33
v_mul_f32 v86,v30,v30
v_mul_f32 v87,v31,v31
v_mul_f32 v88,v32,v32
v_mul_f32 v89,v33,v33
v_fma_f32 v86,v72,v86,v71
v_fma_f32 v87,v72,v87,v71
v_fma_f32 v88,v72,v88,v71
v_fma_f32 v89,v72,v89,v71
v_mul_f32 v86,v30,v86
v_mul_f32 v87,v31,v87
v_mul_f32 v88,v32,v88
v_mul_f32 v89,v33,v89
v_exp_f32 v86,v86
v_exp_f32 v87,v87
v_exp_f32 v88,v88
v_exp_f32 v89,v89
v_fma_f32 v86,v86,v73,v73
v_fma_f32 v87,v87,v73,v73
v_fma_f32 v88,v88,v73,v73
v_fma_f32 v89,v89,v73,v73
v_rcp_f32 v86,v86
v_rcp_f32 v87,v87
v_rcp_f32 v88,v88
v_rcp_f32 v89,v89
v_mul_f32 v30,v30,v86
v_mul_f32 v31,v31,v87
v_mul_f32 v32,v32,v88
v_mul_f32 v33,v33,v89
v_cvt_pk_bf16_f32 v92,v30,v31
v_cvt_pk_bf16_f32 v93,v32,v33
buffer_store_dwordx4 v[90:93],v0,s[36:39],s67 offen offset:0 sc0 sc1
s_mov_b32 s72,s69
s_mov_b32 s69,0x7fff0000
s_mov_b32 s67,0x7fff0000
s_mov_b32 s77,0x7fff0000
s_branch Lg3o_done_%=
Lg3o_L10_%=:
v_mfma_f32_16x16x32_bf16 v[18:21],a[0:3],v[34:37],0
s_add_u32 s58,s58,s61
s_add_u32 m0,s59,0
R 62,1,7168
v_mfma_f32_16x16x32_bf16 v[22:25],a[4:7],v[34:37],0
s_waitcnt vmcnt(2)
buffer_load_dword v5,s[36:39],s58 offen offset:0 sc1 lds
M 18,a[8:11],38
s_mov_b32 m0,s57
R 34,1,8192
v_add_f32_dpp v99,v98,v98 quad_perm:[1,0,3,2] row_mask:0xf bank_mask:0xf
M 22,a[12:15],38
L 5
buffer_load_dword v98,v11,s[36:39],s73 offen offset:0 sc1
M 18,a[16:19],42
s_add_u32 s56,s55,4096
R 38,1,9216
v_add_f32_dpp v99,v99,v99 quad_perm:[2,3,0,1] row_mask:0xf bank_mask:0xf
M 22,a[20:23],42
s_nop 0
v_fma_f32 v26,-v70,v74,v26
v_fma_f32 v27,-v70,v75,v27
M 18,a[24:27],46
s_mov_b32 s66,s65
R 42,1,10240
v_add_f32_dpp v102,v99,v99 row_half_mirror row_mask:0xf bank_mask:0xf
M 22,a[28:31],46
L 5
D 0,55,0
M 18,a[32:35],50
R 46,1,11264
v_mov_b32_dpp v99,v102 row_ror:8 row_mask:0xf bank_mask:0xf
M 22,a[36:39],50
v_fma_f32 v99,-v102,v102,v99
v_max_f32 v99,s87,v99
M 18,a[40:43],54
R 50,1,12288
v_fma_f32 v28,-v70,v76,v28
M 22,a[44:47],54
L 5
D 0,55,1024
M 18,a[48:51],58
R 54,1,13312
v_fma_f32 v29,-v70,v77,v29
M 22,a[52:55],58
v_rsq_f32 v100,v99
M 18,a[56:59],62
R 58,1,14336
v_mul_f32 v105,v99,v100
M 22,a[60:63],62
L 5
D 0,55,2048
M 18,a[64:67],34
R 62,1,15360
v_mul_f32 v103,s88,v100
M 22,a[68:71],34
v_mul_f32 v99,s89,v100
v_mul_f32 v104,v103,v100
M 18,a[72:75],38
R 34,1,16384
v_mul_f32 v104,v104,v99
M 22,a[76:79],38
L 5
ds_write_b128 v12,v[102:105] offset:0
M 18,a[80:83],42
R 38,1,17408
v_mul_f32 v82,v26,v26
M 22,a[84:87],42
D 0,55,3072
M 18,a[88:91],46
s_add_u32 m0,s57,4096
R 42,1,18432
v_mul_f32 v83,v27,v27
M 22,a[92:95],46
L 6
D 0,56,0
M 18,a[96:99],50
s_add_u32 s55,s55,s60
R 46,1,19456
v_mul_f32 v84,v28,v28
M 22,a[100:103],50
s_add_u32 s57,s57,32768
v_mul_f32 v85,v29,v29
v_fma_f32 v82,v72,v82,v71
M 18,a[104:107],54
s_and_b32 s57,s57,131071
R 50,1,20480
v_fma_f32 v83,v72,v83,v71
M 22,a[108:111],54
L 6
D 0,56,1024
M 18,a[112:115],58
s_add_u32 s54,s54,s46
R 54,1,21504
v_fma_f32 v84,v72,v84,v71
M 22,a[116:119],58
s_add_u32 s73,s73,s64
v_fma_f32 v85,v72,v85,v71
v_mul_f32 v82,v26,v82
M 18,a[120:123],62
s_add_u32 s65,s65,s62
R 58,1,22528
v_mul_f32 v83,v27,v83
M 22,a[124:127],62
L 5
D 0,56,2048
M 18,a[128:131],34
R 62,1,23552
v_mul_f32 v84,v28,v84
M 22,a[132:135],34
v_exp_f32 v82,v82
M 18,a[136:139],38
R 34,1,24576
v_mul_f32 v85,v29,v85
M 22,a[140:143],38
L 5
D 0,56,3072
M 18,a[144:147],42
R 38,1,25600
v_fma_f32 v82,v82,v73,v73
M 22,a[148:151],42
v_exp_f32 v83,v83
M 18,a[152:155],46
R 42,1,26624
v_fma_f32 v83,v83,v73,v73
M 22,a[156:159],46
L 5
v_exp_f32 v84,v84
M 18,a[160:163],50
R 46,1,27648
v_fma_f32 v84,v84,v73,v73
M 22,a[164:167],50
v_exp_f32 v85,v85
M 18,a[168:171],54
R 50,1,28672
v_fma_f32 v85,v85,v73,v73
M 22,a[172:175],54
L 5
v_rcp_f32 v82,v82
M 18,a[176:179],58
R 54,1,29696
v_mul_f32 v26,v26,v82
M 22,a[180:183],58
v_rcp_f32 v83,v83
M 18,a[184:187],62
R 58,1,30720
v_mul_f32 v27,v27,v83
M 22,a[188:191],62
L 5
s_nop 0
v_cvt_pk_bf16_f32 v90,v26,v27
M 18,a[192:195],34
s_waitcnt vmcnt(0)
R 62,1,31744
v_fma_f32 v30,-v70,v78,v30
M 22,a[196:199],34
s_barrier
R 66,13,0
ds_read_b32 v7,v6 offset:0
M 18,a[200:203],38
R 34,2,0
v_fma_f32 v31,-v70,v79,v31
M 22,a[204:207],38
L 7
buffer_store_dword v4,v3,s[36:39],s70 offen offset:0 sc0 sc1
M 18,a[208:211],42
s_mov_b32 s70,s69
R 38,2,1024
v_fma_f32 v32,-v70,v80,v32
M 22,a[212:215],42
s_mov_b32 s69,s68
v_fma_f32 v33,-v70,v81,v33
v_mul_f32 v86,v30,v30
M 18,a[216:219],46
L 2
R 42,2,2048
v_mul_f32 v87,v31,v31
M 22,a[220:223],46
s_nop 0
v_mul_f32 v88,v32,v32
v_mul_f32 v89,v33,v33
M 18,a[224:227],50
s_add_u32 s68,s68,s63
R 46,2,3072
v_fma_f32 v86,v72,v86,v71
M 22,a[228:231],50
v_fma_f32 v87,v72,v87,v71
v_fma_f32 v88,v72,v88,v71
M 18,a[232:235],54
R 50,2,4096
v_fma_f32 v89,v72,v89,v71
M 22,a[236:239],54
s_nop 0
v_mul_f32 v86,v30,v86
v_mul_f32 v87,v31,v87
M 18,a[240:243],58
R 54,2,5120
v_mul_f32 v88,v32,v88
v_mul_f32 v89,v33,v89
v_exp_f32 v86,v86
v_exp_f32 v87,v87
v_exp_f32 v88,v88
v_exp_f32 v89,v89
M 22,a[244:247],58
v_rcp_f32 v84,v84
v_rcp_f32 v85,v85
v_fma_f32 v86,v86,v73,v73
v_fma_f32 v87,v87,v73,v73
v_fma_f32 v88,v88,v73,v73
v_fma_f32 v89,v89,v73,v73
v_rcp_f32 v86,v86
v_rcp_f32 v87,v87
v_rcp_f32 v88,v88
v_rcp_f32 v89,v89
v_cmp_eq_u32 vcc,s53,v7
M 18,a[248:251],62
s_cmp_eq_u64 vcc,-1
R 58,2,6144
v_mul_f32 v28,v28,v84
v_mul_f32 v29,v29,v85
v_cvt_pk_bf16_f32 v91,v28,v29
v_mul_f32 v30,v30,v86
v_mul_f32 v31,v31,v87
v_mul_f32 v32,v32,v88
v_mul_f32 v33,v33,v89
v_cvt_pk_bf16_f32 v92,v30,v31
v_cvt_pk_bf16_f32 v93,v32,v33
M 22,a[252:255],62
L 5
buffer_store_dwordx4 v[90:93],v0,s[36:39],s67 offen offset:0 sc0 sc1
v_xor_b32 v1,65536,v1
s_cbranch_scc1 Lg3o_L21_%=
s_cmp_eq_u32 vcc_lo,-1
s_cbranch_scc1 Lg3o_L11_%=
s_branch Lg3o_X1_%=
Lg3o_L11_%=:
v_mfma_f32_16x16x32_bf16 v[26:29],a[0:3],v[34:37],0
s_add_u32 s58,s58,s61
s_add_u32 m0,s59,1024
R 62,2,7168
v_mfma_f32_16x16x32_bf16 v[30:33],a[4:7],v[34:37],0
s_waitcnt vmcnt(2)
buffer_load_dword v5,s[36:39],s58 offen offset:0 sc1 lds
M 26,a[8:11],38
s_mov_b32 m0,s57
R 34,2,8192
v_add_f32_dpp v99,v98,v98 quad_perm:[1,0,3,2] row_mask:0xf bank_mask:0xf
M 30,a[12:15],38
L 5
buffer_load_dword v98,v11,s[36:39],s73 offen offset:0 sc1
M 26,a[16:19],42
s_add_u32 s56,s55,4096
R 38,2,9216
v_add_f32_dpp v99,v99,v99 quad_perm:[2,3,0,1] row_mask:0xf bank_mask:0xf
M 30,a[20:23],42
s_nop 0
v_fma_f32 v18,-v66,v74,v18
v_fma_f32 v19,-v66,v75,v19
M 26,a[24:27],46
s_mov_b32 s67,s65
R 42,2,10240
v_add_f32_dpp v102,v99,v99 row_half_mirror row_mask:0xf bank_mask:0xf
M 30,a[28:31],46
L 5
D 0,55,0
M 26,a[32:35],50
R 46,2,11264
v_mov_b32_dpp v99,v102 row_ror:8 row_mask:0xf bank_mask:0xf
M 30,a[36:39],50
v_fma_f32 v99,-v102,v102,v99
v_max_f32 v99,s87,v99
M 26,a[40:43],54
R 50,2,12288
v_fma_f32 v20,-v66,v76,v20
M 30,a[44:47],54
L 5
D 0,55,1024
M 26,a[48:51],58
R 54,2,13312
v_fma_f32 v21,-v66,v77,v21
M 30,a[52:55],58
v_rsq_f32 v100,v99
M 26,a[56:59],62
R 58,2,14336
v_mul_f32 v105,v99,v100
M 30,a[60:63],62
L 5
D 0,55,2048
M 26,a[64:67],34
R 62,2,15360
v_mul_f32 v103,s88,v100
M 30,a[68:71],34
v_mul_f32 v99,s89,v100
v_mul_f32 v104,v103,v100
M 26,a[72:75],38
R 34,2,16384
v_mul_f32 v104,v104,v99
M 30,a[76:79],38
L 5
ds_write_b128 v12,v[102:105] offset:2048
M 26,a[80:83],42
R 38,2,17408
v_mul_f32 v82,v18,v18
M 30,a[84:87],42
D 0,55,3072
M 26,a[88:91],46
s_add_u32 m0,s57,4096
R 42,2,18432
v_mul_f32 v83,v19,v19
M 30,a[92:95],46
L 6
D 0,56,0
M 26,a[96:99],50
s_add_u32 s55,s55,s60
R 46,2,19456
v_mul_f32 v84,v20,v20
M 30,a[100:103],50
s_add_u32 s57,s57,32768
v_mul_f32 v85,v21,v21
v_fma_f32 v82,v68,v82,v67
M 26,a[104:107],54
s_and_b32 s57,s57,131071
R 50,2,20480
v_fma_f32 v83,v68,v83,v67
M 30,a[108:111],54
L 6
D 0,56,1024
M 26,a[112:115],58
s_add_u32 s54,s54,s46
R 54,2,21504
v_fma_f32 v84,v68,v84,v67
M 30,a[116:119],58
s_add_u32 s73,s73,s64
v_fma_f32 v85,v68,v85,v67
v_mul_f32 v82,v18,v82
M 26,a[120:123],62
s_add_u32 s65,s65,s62
R 58,2,22528
v_mul_f32 v83,v19,v83
M 30,a[124:127],62
L 5
D 0,56,2048
M 26,a[128:131],34
R 62,2,23552
v_mul_f32 v84,v20,v84
M 30,a[132:135],34
v_exp_f32 v82,v82
M 26,a[136:139],38
R 34,2,24576
v_mul_f32 v85,v21,v85
M 30,a[140:143],38
L 5
D 0,56,3072
M 26,a[144:147],42
R 38,2,25600
v_fma_f32 v82,v82,v69,v69
M 30,a[148:151],42
v_exp_f32 v83,v83
M 26,a[152:155],46
R 42,2,26624
v_fma_f32 v83,v83,v69,v69
M 30,a[156:159],46
L 5
v_exp_f32 v84,v84
M 26,a[160:163],50
R 46,2,27648
v_fma_f32 v84,v84,v69,v69
M 30,a[164:167],50
v_exp_f32 v85,v85
M 26,a[168:171],54
R 50,2,28672
v_fma_f32 v85,v85,v69,v69
M 30,a[172:175],54
L 5
v_rcp_f32 v82,v82
M 26,a[176:179],58
R 54,2,29696
v_mul_f32 v18,v18,v82
M 30,a[180:183],58
v_rcp_f32 v83,v83
M 26,a[184:187],62
R 58,2,30720
v_mul_f32 v19,v19,v83
M 30,a[188:191],62
L 5
s_nop 0
v_cvt_pk_bf16_f32 v90,v18,v19
M 26,a[192:195],34
s_waitcnt vmcnt(0)
R 62,2,31744
v_fma_f32 v22,-v66,v78,v22
M 30,a[196:199],34
s_barrier
R 70,13,2048
ds_read_b32 v7,v6 offset:1024
M 26,a[200:203],38
R 34,1,0
v_fma_f32 v23,-v66,v79,v23
M 30,a[204:207],38
L 7
buffer_store_dword v4,v3,s[36:39],s70 offen offset:0 sc0 sc1
M 26,a[208:211],42
s_mov_b32 s70,s69
R 38,1,1024
v_fma_f32 v24,-v66,v80,v24
M 30,a[212:215],42
s_mov_b32 s69,s68
v_fma_f32 v25,-v66,v81,v25
v_mul_f32 v86,v22,v22
)"
R"(M 26,a[216:219],46
L 2
R 42,1,2048
v_mul_f32 v87,v23,v23
M 30,a[220:223],46
s_nop 0
v_mul_f32 v88,v24,v24
v_mul_f32 v89,v25,v25
M 26,a[224:227],50
s_add_u32 s68,s68,s63
R 46,1,3072
v_fma_f32 v86,v68,v86,v67
M 30,a[228:231],50
v_fma_f32 v87,v68,v87,v67
v_fma_f32 v88,v68,v88,v67
M 26,a[232:235],54
R 50,1,4096
v_fma_f32 v89,v68,v89,v67
M 30,a[236:239],54
s_nop 0
v_mul_f32 v86,v22,v86
v_mul_f32 v87,v23,v87
M 26,a[240:243],58
R 54,1,5120
v_mul_f32 v88,v24,v88
v_mul_f32 v89,v25,v89
v_exp_f32 v86,v86
v_exp_f32 v87,v87
v_exp_f32 v88,v88
v_exp_f32 v89,v89
M 30,a[244:247],58
v_rcp_f32 v84,v84
v_rcp_f32 v85,v85
v_fma_f32 v86,v86,v69,v69
v_fma_f32 v87,v87,v69,v69
v_fma_f32 v88,v88,v69,v69
v_fma_f32 v89,v89,v69,v69
v_rcp_f32 v86,v86
v_rcp_f32 v87,v87
v_rcp_f32 v88,v88
v_rcp_f32 v89,v89
v_cmp_eq_u32 vcc,s53,v7
M 26,a[248:251],62
s_cmp_eq_u64 vcc,-1
R 58,1,6144
v_mul_f32 v20,v20,v84
v_mul_f32 v21,v21,v85
v_cvt_pk_bf16_f32 v91,v20,v21
v_mul_f32 v22,v22,v86
v_mul_f32 v23,v23,v87
v_mul_f32 v24,v24,v88
v_mul_f32 v25,v25,v89
v_cvt_pk_bf16_f32 v92,v22,v23
v_cvt_pk_bf16_f32 v93,v24,v25
M 30,a[252:255],62
L 5
buffer_store_dwordx4 v[90:93],v0,s[36:39],s66 offen offset:0 sc0 sc1
v_xor_b32 v2,65536,v2
s_cbranch_scc1 Lg3o_L20_%=
s_cmp_eq_u32 vcc_lo,-1
s_cbranch_scc1 Lg3o_L10_%=
s_branch Lg3o_X0_%=
Lg3o_done_%=:
s_waitcnt vmcnt(1)
s_barrier
buffer_store_dword v4,v3,s[36:39],s70 offen offset:0 sc0 sc1
s_waitcnt vmcnt(1)
s_barrier
buffer_store_dword v4,v3,s[36:39],s72 offen offset:0 sc0 sc1
s_waitcnt vmcnt(0) lgkmcnt(0)
s_barrier
s_endpgm
Lcode_g3o_e:
.purgem M
.purgem R
.purgem T
.purgem D
.purgem L
)"
  :: "s"(ws), "s"(sWave), "s"(sG0), "s"(sStep), "s"(sOutOff), "s"(sFoutOff), "s"(sAuxOff), "s"(sWw), "s"(sBuf), "s"(sRk), "v"(vL16), "v"(vFlag), "v"(vSnapOff), "v"(vSnapRd), "v"(vWd0), "v"(vWd1), "v"(vWr0), "v"(vWr1), "v"(vWr2), "v"(vOutS), "v"(vAux0), "v"(vAux1), "v"(vAux2), "v"(vAux3), "v"(vAn), "v"(vAe), "s"(w3) : "v0", "v1", "v2", "v3", "v4", "v5", "v6", "v7", "v8", "v9", "v10", "v11", "v12", "v13", "v14", "v15", "v16", "v17", "v18", "v19", "v20", "v21", "v22", "v23", "v24", "v25", "v26", "v27", "v28", "v29", "v30", "v31", "v32", "v33", "v34", "v35", "v36", "v37", "v38", "v39", "v40", "v41", "v42", "v43", "v44", "v45", "v46", "v47", "v48", "v49", "v50", "v51", "v52", "v53", "v54", "v55", "v56", "v57", "v58", "v59", "v60", "v61", "v62", "v63", "v64", "v65", "v66", "v67", "v68", "v69", "v70", "v71", "v72", "v73", "v74", "v75", "v76", "v77", "v78", "v79", "v80", "v81", "v82", "v83", "v84", "v85", "v86", "v87", "v88", "v89", "v90", "v91", "v92", "v93", "v94", "v95", "v96", "v97", "v98", "v99", "v100", "v101", "v102", "v103", "v104", "v105", "v106", "v107", "v108", "v109", "v110", "v111", "v112", "v113", "v114", "v115", "v116", "v117", "v118", "v119", "v120", "v121", "v122", "v123", "v124", "v125", "v126", "v127", "v128", "v129", "v130", "v131", "v132", "v133", "v134", "v135", "v136", "v137", "v138", "v139", "v140", "v141", "v142", "v143", "v144", "v145", "v146", "v147", "v148", "v149", "v150", "v151", "v152", "v153", "v154", "v155", "v156", "v157", "v158", "v159", "v160", "v161", "v162", "v163", "v164", "v165", "v166", "v167", "v168", "v169", "v170", "v171", "v172", "v173", "v174", "v175", "v176", "v177", "v178", "v179", "v180", "v181", "v182", "v183", "v184", "v185", "v186", "v187", "v188", "v189", "v190", "v191", "v192", "v193", "v194", "v195", "v196", "v197", "v198", "v199", "v200", "v201", "v202", "v203", "v204", "v205", "v206", "v207", "v208", "v209", "v210", "v211", "v212", "v213", "v214", "v215", "v216", "v217", "v218", "v219", "v220", "v221", "v222", "v223", "v224", "v225", "v226", "v227", "v228", "v229", "v230", "v231", "v232", "v233", "v234", "v235", "v236", "v237", "v238", "v239", "a0", "a1", "a2", "a3", "a4", "a5", "a6", "a7", "a8", "a9", "a10", "a11", "a12", "a13", "a14", "a15", "a16", "a17", "a18", "a19", "a20", "a21", "a22", "a23", "a24", "a25", "a26", "a27", "a28", "a29", "a30", "a31", "a32", "a33", "a34", "a35", "a36", "a37", "a38", "a39", "a40", "a41", "a42", "a43", "a44", "a45", "a46", "a47", "a48", "a49", "a50", "a51", "a52", "a53", "a54", "a55", "a56", "a57", "a58", "a59", "a60", "a61", "a62", "a63", "a64", "a65", "a66", "a67", "a68", "a69", "a70", "a71", "a72", "a73", "a74", "a75", "a76", "a77", "a78", "a79", "a80", "a81", "a82", "a83", "a84", "a85", "a86", "a87", "a88", "a89", "a90", "a91", "a92", "a93", "a94", "a95", "a96", "a97", "a98", "a99", "a100", "a101", "a102", "a103", "a104", "a105", "a106", "a107", "a108", "a109", "a110", "a111", "a112", "a113", "a114", "a115", "a116", "a117", "a118", "a119", "a120", "a121", "a122", "a123", "a124", "a125", "a126", "a127", "a128", "a129", "a130", "a131", "a132", "a133", "a134", "a135", "a136", "a137", "a138", "a139", "a140", "a141", "a142", "a143", "a144", "a145", "a146", "a147", "a148", "a149", "a150", "a151", "a152", "a153", "a154", "a155", "a156", "a157", "a158", "a159", "a160", "a161", "a162", "a163", "a164", "a165", "a166", "a167", "a168", "a169", "a170", "a171", "a172", "a173", "a174", "a175", "a176", "a177", "a178", "a179", "a180", "a181", "a182", "a183", "a184", "a185", "a186", "a187", "a188", "a189", "a190", "a191", "a192", "a193", "a194", "a195", "a196", "a197", "a198", "a199", "a200", "a201", "a202", "a203", "a204", "a205", "a206", "a207", "a208", "a209", "a210", "a211", "a212", "a213", "a214", "a215", "a216", "a217", "a218", "a219", "a220", "a221", "a222", "a223", "a224", "a225", "a226", "a227", "a228", "a229", "a230", "a231", "a232", "a233", "a234", "a235", "a236", "a237", "a238", "a239", "a240", "a241", "a242", "a243", "a244", "a245", "a246", "a247", "a248", "a249", "a250", "a251", "a252", "a253", "a254", "a255", "s36", "s37", "s38", "s39", "s40", "s41", "s42", "s43", "s44", "s45", "s46", "s47", "s48", "s49", "s50", "s51", "s52", "s53", "s54", "s55", "s56", "s57", "s58", "s59", "s60", "s61", "s62", "s63", "s64", "s65", "s66", "s67", "s68", "s69", "s70", "s71", "s72", "s73", "s74", "s75", "s76", "s77", "s78", "s79", "s80", "s81", "s82", "s83", "s84", "s85", "s86", "s87", "s88", "s89", "s90", "s91", "s92", "s93", "s94", "s95", "s96", "s97", "vcc", "scc", "memory"); }
    __builtin_unreachable();
  } else if (kind == 4) {
    const unsigned sStep = 5;
    COMMON(10, (kh * 9 + fj) * 128, 2560, 143360, 2048, 448, 0);
    const int cs = slice;
    const unsigned sOutOff = (kh ? OFF_P1 : OFF_P0) + (cs * 4 + wave) * 1024, sFoutOff = OFF_FP + (kh * 8 + cs) * 128;
    const unsigned sAuxOff = (kh ? OFF_XP : OFF_X1) + (cs * 4 + wave) * 1024, sWw = (kh * 1792 + 16 * wave) * 2048 + cs * 256, sBuf = kh * 57344;
    (void)vWr2;
    asm volatile(
R"(.macro M d,w,x
v_mfma_f32_16x16x32_bf16 v[\d:\d+3], \w, v[\x:\x+3], v[\d:\d+3]
.endm
.macro R d,a,o
ds_read_b128 v[\d:\d+3], v\a offset:\o
.endm
.macro T d,a,o
ds_read_b64_tr_b16 \d, v\a offset:\o
.endm
.macro D a,s,o
buffer_load_dwordx4 v\a, s[36:39], s\s offen offset:\o sc1 lds
.endm
.macro L c
s_waitcnt lgkmcnt(\c)
.endm
Lcode_g4_b:
s_mov_b64 s[36:37],%0
s_mov_b32 s38,111243392
s_mov_b32 s39,0x00020000
s_mov_b32 s44,%1
s_mov_b32 s45,%2
s_mov_b32 s46,%3
s_mov_b32 s47,%4
s_mov_b32 s48,%5
s_mov_b32 s49,%6
s_mov_b32 s50,%7
s_mov_b32 s51,%8
s_mov_b32 s93,%9
v_mov_b32 v0,%10
v_mov_b32 v3,%11
v_mov_b32 v5,%12
v_mov_b32 v6,%13
v_mov_b32 v10,%14
v_mov_b32 v11,%15
v_mov_b32 v12,%16
v_mov_b32 v13,%17
s_mov_b64 s[40:41],%18
s_mov_b32 s42,7340032
s_mov_b32 s43,0x00020000
s_lshl_b32 s57,s44,11
s_mov_b32 s79,111243264
buffer_load_dword v4,off,s[36:39],s79 sc1
s_add_u32 s55,s50,0
s_add_u32 m0,s57,0
s_nop 0
buffer_load_dwordx4 v10,s[40:43],s55 offen offset:0  lds
s_add_u32 m0,s57,1024
s_nop 0
buffer_load_dwordx4 v11,s[40:43],s55 offen offset:0  lds
s_add_u32 m0,s57,8064
s_nop 0
buffer_load_dwordx4 v10,s[40:43],s55 offen offset:128  lds
s_add_u32 m0,s57,9088
s_nop 0
buffer_load_dwordx4 v11,s[40:43],s55 offen offset:128  lds
s_add_u32 s55,s50,131072
s_add_u32 m0,s57,16384
s_nop 0
buffer_load_dwordx4 v10,s[40:43],s55 offen offset:0  lds
s_add_u32 m0,s57,17408
s_nop 0
buffer_load_dwordx4 v11,s[40:43],s55 offen offset:0  lds
s_add_u32 m0,s57,24448
s_nop 0
buffer_load_dwordx4 v10,s[40:43],s55 offen offset:128  lds
s_add_u32 m0,s57,25472
s_nop 0
buffer_load_dwordx4 v11,s[40:43],s55 offen offset:128  lds
s_waitcnt vmcnt(8)
s_barrier
v_readfirstlane_b32 s53,v4
s_nop 3
s_add_u32 s53,s53,1
s_cmp_eq_u32 s53,0
s_cselect_b32 s53,1,s53
v_mov_b32 v4,s53
buffer_store_dword v4,v3,s[36:39],s93 offen offset:0 sc0 sc1
s_add_u32 s55,s50,262144
s_add_u32 m0,s57,32768
s_nop 0
buffer_load_dwordx4 v10,s[40:43],s55 offen offset:0  lds
s_add_u32 m0,s57,33792
s_nop 0
buffer_load_dwordx4 v11,s[40:43],s55 offen offset:0  lds
s_add_u32 m0,s57,40832
s_nop 0
buffer_load_dwordx4 v10,s[40:43],s55 offen offset:128  lds
s_add_u32 m0,s57,41856
s_nop 0
buffer_load_dwordx4 v11,s[40:43],s55 offen offset:128  lds
s_add_u32 s55,s50,393216
s_add_u32 m0,s57,49152
s_nop 0
buffer_load_dwordx4 v10,s[40:43],s55 offen offset:0  lds
s_add_u32 m0,s57,50176
s_nop 0
buffer_load_dwordx4 v11,s[40:43],s55 offen offset:0  lds
s_add_u32 m0,s57,57216
s_nop 0
buffer_load_dwordx4 v10,s[40:43],s55 offen offset:128  lds
s_add_u32 m0,s57,58240
s_nop 0
buffer_load_dwordx4 v11,s[40:43],s55 offen offset:128  lds
s_add_u32 s55,s50,524288
s_add_u32 m0,s57,65536
s_nop 0
buffer_load_dwordx4 v10,s[40:43],s55 offen offset:0  lds
s_add_u32 m0,s57,66560
s_nop 0
buffer_load_dwordx4 v11,s[40:43],s55 offen offset:0  lds
s_add_u32 m0,s57,73600
s_nop 0
buffer_load_dwordx4 v10,s[40:43],s55 offen offset:128  lds
s_add_u32 m0,s57,74624
s_nop 0
buffer_load_dwordx4 v11,s[40:43],s55 offen offset:128  lds
s_add_u32 s55,s50,655360
s_add_u32 m0,s57,81920
s_nop 0
buffer_load_dwordx4 v10,s[40:43],s55 offen offset:0  lds
s_add_u32 m0,s57,82944
s_nop 0
buffer_load_dwordx4 v11,s[40:43],s55 offen offset:0  lds
s_add_u32 m0,s57,89984
s_nop 0
buffer_load_dwordx4 v10,s[40:43],s55 offen offset:128  lds
s_add_u32 m0,s57,91008
s_nop 0
buffer_load_dwordx4 v11,s[40:43],s55 offen offset:128  lds
s_waitcnt vmcnt(21)
L 0
s_barrier
s_add_u32 s55,s50,786432
s_add_u32 m0,s57,98304
s_nop 0
buffer_load_dwordx4 v10,s[40:43],s55 offen offset:0  lds
s_add_u32 m0,s57,99328
s_nop 0
buffer_load_dwordx4 v11,s[40:43],s55 offen offset:0  lds
s_add_u32 m0,s57,106368
s_nop 0
buffer_load_dwordx4 v10,s[40:43],s55 offen offset:128  lds
s_add_u32 m0,s57,107392
s_nop 0
buffer_load_dwordx4 v11,s[40:43],s55 offen offset:128  lds
v_add_u32 v18,0,v12
v_add_u32 v19,0,v13
T a[0:1],18,0
T a[2:3],18,512
T a[4:5],19,0
T a[6:7],19,512
T a[8:9],18,4096
T a[10:11],18,4608
T a[12:13],19,4096
T a[14:15],19,4608
s_waitcnt vmcnt(21)
L 0
s_barrier
s_add_u32 s55,s50,917504
s_add_u32 m0,s57,0
s_nop 0
buffer_load_dwordx4 v10,s[40:43],s55 offen offset:0  lds
s_add_u32 m0,s57,1024
s_nop 0
buffer_load_dwordx4 v11,s[40:43],s55 offen offset:0  lds
s_add_u32 m0,s57,8064
s_nop 0
buffer_load_dwordx4 v10,s[40:43],s55 offen offset:128  lds
s_add_u32 m0,s57,9088
s_nop 0
buffer_load_dwordx4 v11,s[40:43],s55 offen offset:128  lds
v_add_u32 v18,16384,v12
v_add_u32 v19,16384,v13
T a[16:17],18,0
T a[18:19],18,512
T a[20:21],19,0
T a[22:23],19,512
T a[24:25],18,4096
T a[26:27],18,4608
T a[28:29],19,4096
T a[30:31],19,4608
s_waitcnt vmcnt(20)
L 0
s_barrier
s_add_u32 s55,s50,1048576
s_add_u32 m0,s57,16384
s_nop 0
buffer_load_dwordx4 v10,s[40:43],s55 offen offset:0  lds
s_add_u32 m0,s57,17408
s_nop 0
buffer_load_dwordx4 v11,s[40:43],s55 offen offset:0  lds
s_add_u32 m0,s57,24448
s_nop 0
buffer_load_dwordx4 v10,s[40:43],s55 offen offset:128  lds
s_add_u32 m0,s57,25472
s_nop 0
buffer_load_dwordx4 v11,s[40:43],s55 offen offset:128  lds
v_add_u32 v18,32768,v12
v_add_u32 v19,32768,v13
T a[32:33],18,0
T a[34:35],18,512
T a[36:37],19,0
T a[38:39],19,512
T a[40:41],18,4096
T a[42:43],18,4608
T a[44:45],19,4096
T a[46:47],19,4608
s_waitcnt vmcnt(20)
L 0
s_barrier
s_add_u32 s55,s50,1179648
s_add_u32 m0,s57,32768
s_nop 0
buffer_load_dwordx4 v10,s[40:43],s55 offen offset:0  lds
s_add_u32 m0,s57,33792
s_nop 0
buffer_load_dwordx4 v11,s[40:43],s55 offen offset:0  lds
s_add_u32 m0,s57,40832
s_nop 0
buffer_load_dwordx4 v10,s[40:43],s55 offen offset:128  lds
s_add_u32 m0,s57,41856
s_nop 0
buffer_load_dwordx4 v11,s[40:43],s55 offen offset:128  lds
v_add_u32 v18,49152,v12
v_add_u32 v19,49152,v13
T a[48:49],18,0
T a[50:51],18,512
T a[52:53],19,0
T a[54:55],19,512
T a[56:57],18,4096
T a[58:59],18,4608
T a[60:61],19,4096
T a[62:63],19,4608
s_waitcnt vmcnt(20)
L 0
s_barrier
s_add_u32 s55,s50,1310720
s_add_u32 m0,s57,49152
s_nop 0
buffer_load_dwordx4 v10,s[40:43],s55 offen offset:0  lds
s_add_u32 m0,s57,50176
s_nop 0
buffer_load_dwordx4 v11,s[40:43],s55 offen offset:0  lds
s_add_u32 m0,s57,57216
s_nop 0
buffer_load_dwordx4 v10,s[40:43],s55 offen offset:128  lds
s_add_u32 m0,s57,58240
s_nop 0
buffer_load_dwordx4 v11,s[40:43],s55 offen offset:128  lds
v_add_u32 v18,65536,v12
v_add_u32 v19,65536,v13
T a[64:65],18,0
T a[66:67],18,512
T a[68:69],19,0
T a[70:71],19,512
T a[72:73],18,4096
T a[74:75],18,4608
T a[76:77],19,4096
T a[78:79],19,4608
s_waitcnt vmcnt(20)
L 0
s_barrier
s_add_u32 s55,s50,1441792
s_add_u32 m0,s57,65536
s_nop 0
buffer_load_dwordx4 v10,s[40:43],s55 offen offset:0  lds
s_add_u32 m0,s57,66560
s_nop 0
buffer_load_dwordx4 v11,s[40:43],s55 offen offset:0  lds
s_add_u32 m0,s57,73600
s_nop 0
buffer_load_dwordx4 v10,s[40:43],s55 offen offset:128  lds
s_add_u32 m0,s57,74624
s_nop 0
buffer_load_dwordx4 v11,s[40:43],s55 offen offset:128  lds
v_add_u32 v18,81920,v12
v_add_u32 v19,81920,v13
T a[80:81],18,0
T a[82:83],18,512
T a[84:85],19,0
T a[86:87],19,512
T a[88:89],18,4096
T a[90:91],18,4608
T a[92:93],19,4096
T a[94:95],19,4608
s_waitcnt vmcnt(20)
L 0
s_barrier
s_add_u32 s55,s50,1572864
s_add_u32 m0,s57,81920
s_nop 0
buffer_load_dwordx4 v10,s[40:43],s55 offen offset:0  lds
s_add_u32 m0,s57,82944
s_nop 0
buffer_load_dwordx4 v11,s[40:43],s55 offen offset:0  lds
s_add_u32 m0,s57,89984
s_nop 0
buffer_load_dwordx4 v10,s[40:43],s55 offen offset:128  lds
s_add_u32 m0,s57,91008
s_nop 0
buffer_load_dwordx4 v11,s[40:43],s55 offen offset:128  lds
v_add_u32 v18,98304,v12
v_add_u32 v19,98304,v13
T a[96:97],18,0
T a[98:99],18,512
T a[100:101],19,0
T a[102:103],19,512
T a[104:105],18,4096
T a[106:107],18,4608
T a[108:109],19,4096
T a[110:111],19,4608
s_waitcnt vmcnt(20)
L 0
s_barrier
s_add_u32 s55,s50,1703936
s_add_u32 m0,s57,98304
s_nop 0
buffer_load_dwordx4 v10,s[40:43],s55 offen offset:0  lds
s_add_u32 m0,s57,99328
s_nop 0
buffer_load_dwordx4 v11,s[40:43],s55 offen offset:0  lds
s_add_u32 m0,s57,106368
s_nop 0
buffer_load_dwordx4 v10,s[40:43],s55 offen offset:128  lds
s_add_u32 m0,s57,107392
s_nop 0
buffer_load_dwordx4 v11,s[40:43],s55 offen offset:128  lds
v_add_u32 v18,0,v12
v_add_u32 v19,0,v13
T a[112:113],18,0
T a[114:115],18,512
T a[116:117],19,0
T a[118:119],19,512
T a[120:121],18,4096
T a[122:123],18,4608
T a[124:125],19,4096
T a[126:127],19,4608
s_waitcnt vmcnt(20)
L 0
s_barrier
s_add_u32 s55,s50,1835008
s_add_u32 m0,s57,0
s_nop 0
buffer_load_dwordx4 v10,s[40:43],s55 offen offset:0  lds
s_add_u32 m0,s57,1024
s_nop 0
buffer_load_dwordx4 v11,s[40:43],s55 offen offset:0  lds
s_add_u32 m0,s57,8064
s_nop 0
buffer_load_dwordx4 v10,s[40:43],s55 offen offset:128  lds
s_add_u32 m0,s57,9088
s_nop 0
buffer_load_dwordx4 v11,s[40:43],s55 offen offset:128  lds
v_add_u32 v18,16384,v12
v_add_u32 v19,16384,v13
T a[128:129],18,0
T a[130:131],18,512
T a[132:133],19,0
T a[134:135],19,512
T a[136:137],18,4096
T a[138:139],18,4608
T a[140:141],19,4096
T a[142:143],19,4608
s_waitcnt vmcnt(20)
L 0
s_barrier
s_add_u32 s55,s50,1966080
s_add_u32 m0,s57,16384
s_nop 0
buffer_load_dwordx4 v10,s[40:43],s55 offen offset:0  lds
s_add_u32 m0,s57,17408
s_nop 0
buffer_load_dwordx4 v11,s[40:43],s55 offen offset:0  lds
s_add_u32 m0,s57,24448
s_nop 0
buffer_load_dwordx4 v10,s[40:43],s55 offen offset:128  lds
s_add_u32 m0,s57,25472
s_nop 0
buffer_load_dwordx4 v11,s[40:43],s55 offen offset:128  lds
v_add_u32 v18,32768,v12
v_add_u32 v19,32768,v13
T a[144:145],18,0
T a[146:147],18,512
T a[148:149],19,0
T a[150:151],19,512
T a[152:153],18,4096
T a[154:155],18,4608
T a[156:157],19,4096
T a[158:159],19,4608
s_waitcnt vmcnt(20)
L 0
s_barrier
s_add_u32 s55,s50,2097152
s_add_u32 m0,s57,32768
s_nop 0
buffer_load_dwordx4 v10,s[40:43],s55 offen offset:0  lds
s_add_u32 m0,s57,33792
s_nop 0
buffer_load_dwordx4 v11,s[40:43],s55 offen offset:0  lds
s_add_u32 m0,s57,40832
s_nop 0
buffer_load_dwordx4 v10,s[40:43],s55 offen offset:128  lds
s_add_u32 m0,s57,41856
s_nop 0
buffer_load_dwordx4 v11,s[40:43],s55 offen offset:128  lds
v_add_u32 v18,49152,v12
v_add_u32 v19,49152,v13
T a[160:161],18,0
T a[162:163],18,512
T a[164:165],19,0
T a[166:167],19,512
T a[168:169],18,4096
T a[170:171],18,4608
T a[172:173],19,4096
T a[174:175],19,4608
s_waitcnt vmcnt(20)
L 0
s_barrier
s_add_u32 s55,s50,2228224
s_add_u32 m0,s57,49152
s_nop 0
buffer_load_dwordx4 v10,s[40:43],s55 offen offset:0  lds
s_add_u32 m0,s57,50176
s_nop 0
buffer_load_dwordx4 v11,s[40:43],s55 offen offset:0  lds
s_add_u32 m0,s57,57216
s_nop 0
buffer_load_dwordx4 v10,s[40:43],s55 offen offset:128  lds
s_add_u32 m0,s57,58240
s_nop 0
buffer_load_dwordx4 v11,s[40:43],s55 offen offset:128  lds
v_add_u32 v18,65536,v12
v_add_u32 v19,65536,v13
T a[176:177],18,0
T a[178:179],18,512
T a[180:181],19,0
T a[182:183],19,512
T a[184:185],18,4096
T a[186:187],18,4608
T a[188:189],19,4096
T a[190:191],19,4608
s_waitcnt vmcnt(20)
L 0
s_barrier
s_add_u32 s55,s50,2359296
s_add_u32 m0,s57,65536
s_nop 0
buffer_load_dwordx4 v10,s[40:43],s55 offen offset:0  lds
s_add_u32 m0,s57,66560
s_nop 0
buffer_load_dwordx4 v11,s[40:43],s55 offen offset:0  lds
s_add_u32 m0,s57,73600
s_nop 0
buffer_load_dwordx4 v10,s[40:43],s55 offen offset:128  lds
s_add_u32 m0,s57,74624
s_nop 0
buffer_load_dwordx4 v11,s[40:43],s55 offen offset:128  lds
v_add_u32 v18,81920,v12
v_add_u32 v19,81920,v13
T a[192:193],18,0
T a[194:195],18,512
T a[196:197],19,0
T a[198:199],19,512
T a[200:201],18,4096
T a[202:203],18,4608
T a[204:205],19,4096
T a[206:207],19,4608
s_waitcnt vmcnt(20)
L 0
s_barrier
s_add_u32 s55,s50,2490368
s_add_u32 m0,s57,81920
s_nop 0
buffer_load_dwordx4 v10,s[40:43],s55 offen offset:0  lds
s_add_u32 m0,s57,82944
s_nop 0
buffer_load_dwordx4 v11,s[40:43],s55 offen offset:0  lds
s_add_u32 m0,s57,89984
s_nop 0
buffer_load_dwordx4 v10,s[40:43],s55 offen offset:128  lds
s_add_u32 m0,s57,91008
s_nop 0
buffer_load_dwordx4 v11,s[40:43],s55 offen offset:128  lds
v_add_u32 v18,98304,v12
v_add_u32 v19,98304,v13
T a[208:209],18,0
T a[210:211],18,512
T a[212:213],19,0
T a[214:215],19,512
T a[216:217],18,4096
T a[218:219],18,4608
T a[220:221],19,4096
T a[222:223],19,4608
s_waitcnt vmcnt(20)
L 0
s_barrier
s_add_u32 s55,s50,2621440
s_add_u32 m0,s57,98304
s_nop 0
buffer_load_dwordx4 v10,s[40:43],s55 offen offset:0  lds
)"
R"(s_add_u32 m0,s57,99328
s_nop 0
buffer_load_dwordx4 v11,s[40:43],s55 offen offset:0  lds
s_add_u32 m0,s57,106368
s_nop 0
buffer_load_dwordx4 v10,s[40:43],s55 offen offset:128  lds
s_add_u32 m0,s57,107392
s_nop 0
buffer_load_dwordx4 v11,s[40:43],s55 offen offset:128  lds
v_add_u32 v18,0,v12
v_add_u32 v19,0,v13
T a[224:225],18,0
T a[226:227],18,512
T a[228:229],19,0
T a[230:231],19,512
T a[232:233],18,4096
T a[234:235],18,4608
T a[236:237],19,4096
T a[238:239],19,4608
s_waitcnt vmcnt(20)
L 0
s_barrier
s_add_u32 s55,s50,2752512
s_add_u32 m0,s57,0
s_nop 0
buffer_load_dwordx4 v10,s[40:43],s55 offen offset:0  lds
s_add_u32 m0,s57,1024
s_nop 0
buffer_load_dwordx4 v11,s[40:43],s55 offen offset:0  lds
s_add_u32 m0,s57,8064
s_nop 0
buffer_load_dwordx4 v10,s[40:43],s55 offen offset:128  lds
s_add_u32 m0,s57,9088
s_nop 0
buffer_load_dwordx4 v11,s[40:43],s55 offen offset:128  lds
v_add_u32 v18,16384,v12
v_add_u32 v19,16384,v13
T a[240:241],18,0
T a[242:243],18,512
T a[244:245],19,0
T a[246:247],19,512
T a[248:249],18,4096
T a[250:251],18,4608
T a[252:253],19,4096
T a[254:255],19,4608
s_waitcnt vmcnt(20)
L 0
s_barrier
s_add_u32 s55,s50,2883584
s_add_u32 m0,s57,16384
s_nop 0
buffer_load_dwordx4 v10,s[40:43],s55 offen offset:0  lds
s_add_u32 m0,s57,17408
s_nop 0
buffer_load_dwordx4 v11,s[40:43],s55 offen offset:0  lds
s_add_u32 m0,s57,24448
s_nop 0
buffer_load_dwordx4 v10,s[40:43],s55 offen offset:128  lds
s_add_u32 m0,s57,25472
s_nop 0
buffer_load_dwordx4 v11,s[40:43],s55 offen offset:128  lds
v_add_u32 v18,32768,v12
v_add_u32 v19,32768,v13
T v[64:65],18,0
T v[66:67],18,512
T v[68:69],19,0
T v[70:71],19,512
T v[72:73],18,4096
T v[74:75],18,4608
T v[76:77],19,4096
T v[78:79],19,4608
s_waitcnt vmcnt(20)
L 0
s_barrier
s_add_u32 s55,s50,3014656
s_add_u32 m0,s57,32768
s_nop 0
buffer_load_dwordx4 v10,s[40:43],s55 offen offset:0  lds
s_add_u32 m0,s57,33792
s_nop 0
buffer_load_dwordx4 v11,s[40:43],s55 offen offset:0  lds
s_add_u32 m0,s57,40832
s_nop 0
buffer_load_dwordx4 v10,s[40:43],s55 offen offset:128  lds
s_add_u32 m0,s57,41856
s_nop 0
buffer_load_dwordx4 v11,s[40:43],s55 offen offset:128  lds
v_add_u32 v18,49152,v12
v_add_u32 v19,49152,v13
T v[80:81],18,0
T v[82:83],18,512
T v[84:85],19,0
T v[86:87],19,512
T v[88:89],18,4096
T v[90:91],18,4608
T v[92:93],19,4096
T v[94:95],19,4608
s_waitcnt vmcnt(20)
L 0
s_barrier
s_add_u32 s55,s50,3145728
s_add_u32 m0,s57,49152
s_nop 0
buffer_load_dwordx4 v10,s[40:43],s55 offen offset:0  lds
s_add_u32 m0,s57,50176
s_nop 0
buffer_load_dwordx4 v11,s[40:43],s55 offen offset:0  lds
s_add_u32 m0,s57,57216
s_nop 0
buffer_load_dwordx4 v10,s[40:43],s55 offen offset:128  lds
s_add_u32 m0,s57,58240
s_nop 0
buffer_load_dwordx4 v11,s[40:43],s55 offen offset:128  lds
v_add_u32 v18,65536,v12
v_add_u32 v19,65536,v13
T v[96:97],18,0
T v[98:99],18,512
T v[100:101],19,0
T v[102:103],19,512
T v[104:105],18,4096
T v[106:107],18,4608
T v[108:109],19,4096
T v[110:111],19,4608
s_waitcnt vmcnt(20)
L 0
s_barrier
s_add_u32 s55,s50,3276800
s_add_u32 m0,s57,65536
s_nop 0
buffer_load_dwordx4 v10,s[40:43],s55 offen offset:0  lds
s_add_u32 m0,s57,66560
s_nop 0
buffer_load_dwordx4 v11,s[40:43],s55 offen offset:0  lds
s_add_u32 m0,s57,73600
s_nop 0
buffer_load_dwordx4 v10,s[40:43],s55 offen offset:128  lds
s_add_u32 m0,s57,74624
s_nop 0
buffer_load_dwordx4 v11,s[40:43],s55 offen offset:128  lds
v_add_u32 v18,81920,v12
v_add_u32 v19,81920,v13
T v[112:113],18,0
T v[114:115],18,512
T v[116:117],19,0
T v[118:119],19,512
T v[120:121],18,4096
T v[122:123],18,4608
T v[124:125],19,4096
T v[126:127],19,4608
s_waitcnt vmcnt(20)
L 0
s_barrier
s_add_u32 s55,s50,3407872
s_add_u32 m0,s57,81920
s_nop 0
buffer_load_dwordx4 v10,s[40:43],s55 offen offset:0  lds
s_add_u32 m0,s57,82944
s_nop 0
buffer_load_dwordx4 v11,s[40:43],s55 offen offset:0  lds
s_add_u32 m0,s57,89984
s_nop 0
buffer_load_dwordx4 v10,s[40:43],s55 offen offset:128  lds
s_add_u32 m0,s57,91008
s_nop 0
buffer_load_dwordx4 v11,s[40:43],s55 offen offset:128  lds
v_add_u32 v18,98304,v12
v_add_u32 v19,98304,v13
T v[128:129],18,0
T v[130:131],18,512
T v[132:133],19,0
T v[134:135],19,512
T v[136:137],18,4096
T v[138:139],18,4608
T v[140:141],19,4096
T v[142:143],19,4608
s_waitcnt vmcnt(20)
L 0
s_barrier
s_add_u32 s55,s50,3538944
s_add_u32 m0,s57,98304
s_nop 0
buffer_load_dwordx4 v10,s[40:43],s55 offen offset:0  lds
s_add_u32 m0,s57,99328
s_nop 0
buffer_load_dwordx4 v11,s[40:43],s55 offen offset:0  lds
s_add_u32 m0,s57,106368
s_nop 0
buffer_load_dwordx4 v10,s[40:43],s55 offen offset:128  lds
s_add_u32 m0,s57,107392
s_nop 0
buffer_load_dwordx4 v11,s[40:43],s55 offen offset:128  lds
v_add_u32 v18,0,v12
v_add_u32 v19,0,v13
T v[144:145],18,0
T v[146:147],18,512
T v[148:149],19,0
T v[150:151],19,512
T v[152:153],18,4096
T v[154:155],18,4608
T v[156:157],19,4096
T v[158:159],19,4608
s_waitcnt vmcnt(20)
L 0
s_barrier
v_add_u32 v18,16384,v12
v_add_u32 v19,16384,v13
T v[160:161],18,0
T v[162:163],18,512
T v[164:165],19,0
T v[166:167],19,512
T v[168:169],18,4096
T v[170:171],18,4608
T v[172:173],19,4096
T v[174:175],19,4608
s_waitcnt vmcnt(16)
L 0
s_barrier
v_add_u32 v18,32768,v12
v_add_u32 v19,32768,v13
T v[176:177],18,0
T v[178:179],18,512
T v[180:181],19,0
T v[182:183],19,512
T v[184:185],18,4096
T v[186:187],18,4608
T v[188:189],19,4096
T v[190:191],19,4608
s_waitcnt vmcnt(12)
L 0
s_barrier
v_add_u32 v18,49152,v12
v_add_u32 v19,49152,v13
T v[192:193],18,0
T v[194:195],18,512
T v[196:197],19,0
T v[198:199],19,512
T v[200:201],18,4096
T v[202:203],18,4608
T v[204:205],19,4096
T v[206:207],19,4608
s_waitcnt vmcnt(8)
L 0
s_barrier
v_add_u32 v18,65536,v12
v_add_u32 v19,65536,v13
T v[208:209],18,0
T v[210:211],18,512
T v[212:213],19,0
T v[214:215],19,512
T v[216:217],18,4096
T v[218:219],18,4608
T v[220:221],19,4096
T v[222:223],19,4608
s_waitcnt vmcnt(4)
L 0
s_barrier
v_add_u32 v18,81920,v12
v_add_u32 v19,81920,v13
T v[224:225],18,0
T v[226:227],18,512
T v[228:229],19,0
T v[230:231],19,512
T v[232:233],18,4096
T v[234:235],18,4608
T v[236:237],19,4096
T v[238:239],19,4608
s_waitcnt vmcnt(0)
L 0
s_barrier
v_add_u32 v18,98304,v12
v_add_u32 v19,98304,v13
T v[240:241],18,0
T v[242:243],18,512
T v[244:245],19,0
T v[246:247],19,512
T v[248:249],18,4096
T v[250:251],18,4608
T v[252:253],19,4096
T v[254:255],19,4608
L 0
s_barrier
v_mov_b32 v4,s53
s_mov_b32 s82,0
s_mov_b32 s80,0
s_mov_b32 s54,s45
s_mul_i32 s57,s44,7168
s_mul_i32 s55,s45,114688
s_add_u32 s55,s55,s57
s_add_u32 s55,s55,40894464
s_mul_i32 s60,s46,114688
s_mul_i32 s61,s46,2560
s_mul_i32 s62,s46,32768
s_mul_i32 s63,s46,2048
s_mul_i32 s64,s46,512
s_lshl_b32 s59,s44,8
s_add_u32 s59,s59,143360
s_mul_i32 s58,s45,2560
s_add_u32 s58,s58,109891584
s_mul_i32 s65,s45,32768
s_add_u32 s65,s65,s47
s_mul_i32 s68,s45,2048
s_add_u32 s68,s68,s48
s_mul_i32 s73,s45,512
s_add_u32 s73,s73,106954752
s_mul_i32 s74,s45,32768
s_add_u32 s74,s74,s49
s_mul_i32 s75,s45,1024
s_add_u32 s75,s75,107479040
s_mov_b32 s66,0x7fff0000
s_mov_b32 s67,0x7fff0000
s_mov_b32 s69,0x7fff0000
s_mov_b32 s76,0x7fff0000
s_mov_b32 s77,0x7fff0000
s_mov_b32 s70,0x7fff0000
s_mov_b32 s71,0x7fff0000
s_mov_b32 s72,0x7fff0000
v_mov_b32 v1,v0
v_add_u32 v2,28672,v0
s_mov_b32 s84,28672
s_mov_b32 s86,0x3a800000
s_mov_b32 s87,0x3727c5ac
s_mov_b32 s88,0xc0135761
s_mov_b32 s89,0x3d372713
s_and_b32 s85,s44,1
s_mul_i32 s85,s85,127
s_add_u32 s55,s55,s51
v_lshlrev_b32 v8,22,v0
v_and_b32 v8,0x80000000,v8
v_or_b32 v8,v8,v5
v_add_u32 v2,57344,v0
s_lshl_b32 s84,s57,1
s_add_u32 s84,s84,57344
s_add_u32 m0,s59,4096
s_nop 0
buffer_load_dword v8,s[36:39],s58 offen offset:0 sc1 lds
buffer_store_dword v4,v3,s[36:39],s71 offen offset:0 sc0 sc1
s_add_u32 m0,s59,5120
s_nop 0
buffer_load_dword v8,s[36:39],s58 offen offset:0 sc1 lds
buffer_store_dword v4,v3,s[36:39],s71 offen offset:0 sc0 sc1
s_mov_b32 s83,0
Lg4_poll_%=:
s_add_u32 s78,s83,2
s_and_b32 s78,s78,3
s_lshl_b32 s78,s78,10
s_add_u32 s78,s78,4096
s_add_u32 m0,s59,s78
s_nop 0
buffer_load_dword v8,s[36:39],s58 offen offset:0 sc1 lds
s_and_b32 s78,s83,3
s_lshl_b32 s78,s78,10
v_add_u32 v62,s78,v6
s_waitcnt vmcnt(4)
s_barrier
ds_read_b32 v7,v62 offset:4096
buffer_store_dword v4,v3,s[36:39],s70 offen offset:0 sc0 sc1
s_mov_b32 s70,s71
s_mov_b32 s71,s72
s_mov_b32 s72,0x7fff0000
s_add_u32 s83,s83,1
s_add_u32 s82,s82,1
L 0
v_cmp_eq_u32 vcc,s53,v7
s_nop 0
s_cmp_eq_u32 vcc_lo,-1
s_cbranch_scc1 Lg4_go_%=
s_cmp_lt_u32 s82,20000
s_cbranch_scc1 Lg4_poll_%=
Lg4_go_%=:
s_add_u32 m0,s57,0
s_add_u32 s56,s55,0
D 0,56,0
D 0,56,1024
D 0,56,2048
D 0,56,3072
s_add_u32 m0,s57,4096
s_add_u32 s56,s56,4096
D 0,56,0
D 0,56,1024
D 0,56,2048
s_add_u32 s58,s58,s61
s_add_u32 m0,s59,2048
s_nop 0
buffer_load_dword v8,s[36:39],s58 offen offset:0 sc1 lds
s_add_u32 m0,s57,28672
s_add_u32 s56,s55,28672
D 0,56,0
D 0,56,1024
D 0,56,2048
D 0,56,3072
s_add_u32 m0,s57,32768
s_add_u32 s56,s56,4096
D 0,56,0
D 0,56,1024
D 0,56,2048
s_add_u32 s55,s55,s60
s_sub_u32 s57,s84,s57
s_add_u32 s54,s54,s46
s_waitcnt vmcnt(7)
s_barrier
buffer_store_dword v4,v3,s[36:39],s70 offen offset:0 sc0 sc1
s_min_u32 s71,s71,s72
buffer_store_dword v4,v3,s[36:39],s71 offen offset:0 sc0 sc1
s_mov_b32 s70,0x7fff0000
s_mov_b32 s71,0x7fff0000
s_mov_b32 s72,0x7fff0000
s_cmp_eq_u32 s80,1
s_cbranch_scc1 Lg4_st1_%=
s_branch Lg4_st0_%=
Lg4_gox_%=:
s_add_u32 s58,s58,s61
s_add_u32 m0,s59,2048
s_nop 0
buffer_load_dword v8,s[36:39],s58 offen offset:0 sc1 lds
s_waitcnt vmcnt(0)
s_barrier
buffer_store_dword v4,v3,s[36:39],s70 offen offset:0 sc0 sc1
s_min_u32 s71,s71,s72
buffer_store_dword v4,v3,s[36:39],s71 offen offset:0 sc0 sc1
s_mov_b32 s70,0x7fff0000
s_mov_b32 s71,0x7fff0000
s_mov_b32 s72,0x7fff0000
s_cmp_eq_u32 s80,1
s_cbranch_scc1 Lg4_st1_%=
Lg4_st0_%=:
ds_read_b32 v7,v6 offset:2048
R 26,1,0
R 30,1,1024
R 34,1,2048
R 38,1,3072
R 42,1,4096
R 46,1,5120
R 50,1,6144
L 7
v_cmp_eq_u32 vcc,s53,v7
L 5
s_cmp_eq_u32 vcc_lo,-1
s_cbranch_scc1 Lg4_F0_%=
s_branch Lg4_L0_%=
Lg4_st1_%=:
ds_read_b32 v7,v6 offset:2048
R 26,2,0
R 30,2,1024
R 34,2,2048
R 38,2,3072
R 42,2,4096
R 46,2,5120
R 50,2,6144
L 7
v_cmp_eq_u32 vcc,s53,v7
L 5
s_cmp_eq_u32 vcc_lo,-1
s_cbranch_scc1 Lg4_F1_%=
s_branch Lg4_L1_%=
Lg4_F0_%=:
v_mfma_f32_16x16x32_bf16 v[10:13],a[0:3],v[26:29],0
s_add_u32 s58,s58,s61
R 54,1,7168
v_lshlrev_b32 v62,16,v58
v_mfma_f32_16x16x32_bf16 v[14:17],a[4:7],v[26:29],0
s_add_u32 m0,s57,0
s_add_u32 s56,s55,0
v_and_b32 v63,0xffff0000,v58
M 10,a[8:11],30
s_mov_b32 s66,s65
R 26,1,8192
v_add_f32 v18,v18,v62
M 14,a[12:15],30
L 5
D 0,56,0
M 10,a[16:19],34
R 30,1,9216
v_add_f32 v19,v19,v63
M 14,a[20:23],34
v_lshlrev_b32 v62,16,v59
v_add_f32 v20,v20,v62
M 10,a[24:27],38
R 34,1,10240
v_and_b32 v63,0xffff0000,v59
M 14,a[28:31],38
L 5
v_add_f32 v21,v21,v63
v_lshlrev_b32 v62,16,v60
M 10,a[32:35],42
R 38,1,11264
v_add_f32 v22,v22,v62
M 14,a[36:39],42
D 0,56,1024
M 10,a[40:43],46
R 42,1,12288
v_and_b32 v63,0xffff0000,v60
M 14,a[44:47],46
L 5
v_add_f32 v23,v23,v63
v_lshlrev_b32 v62,16,v61
M 10,a[48:51],50
R 46,1,13312
v_add_f32 v24,v24,v62
M 14,a[52:55],50
v_and_b32 v63,0xffff0000,v61
)"
R"(v_add_f32 v25,v25,v63
M 10,a[56:59],54
R 50,1,14336
v_cvt_pk_bf16_f32 v18,v18,v19
M 14,a[60:63],54
L 5
buffer_load_dwordx4 v[58:61],v0,s[36:39],s74 offen offset:0 sc1
M 10,a[64:67],26
R 54,1,15360
v_cvt_pk_bf16_f32 v19,v20,v21
M 14,a[68:71],26
D 0,56,2048
M 10,a[72:75],30
R 26,1,16384
v_cvt_pk_bf16_f32 v20,v22,v23
M 14,a[76:79],30
L 5
v_cvt_pk_bf16_f32 v21,v24,v25
M 10,a[80:83],34
R 30,1,17408
M 14,a[84:87],34
buffer_store_dwordx4 v[18:21],v0,s[36:39],s67 offen offset:0 sc0 sc1
M 10,a[88:91],38
R 34,1,18432
M 14,a[92:95],38
L 5
M 10,a[96:99],42
R 38,1,19456
M 14,a[100:103],42
D 0,56,3072
M 10,a[104:107],46
s_add_u32 m0,s57,4096
s_add_u32 s56,s56,4096
R 42,1,20480
M 14,a[108:111],46
s_add_u32 s74,s74,s62
s_add_u32 s65,s65,s62
L 5
M 10,a[112:115],50
R 46,1,21504
M 14,a[116:119],50
M 10,a[120:123],54
R 50,1,22528
M 14,a[124:127],54
L 5
D 0,56,0
M 10,a[128:131],26
R 54,1,23552
M 14,a[132:135],26
M 10,a[136:139],30
R 26,1,24576
M 14,a[140:143],30
L 5
M 10,a[144:147],34
R 30,1,25600
M 14,a[148:151],34
M 10,a[152:155],38
R 34,1,26624
M 14,a[156:159],38
L 5
D 0,56,1024
M 10,a[160:163],42
s_waitcnt vmcnt(8)
R 38,1,27648
M 14,a[164:167],42
s_barrier
M 10,a[168:171],46
R 42,1,28672
M 14,a[172:175],46
L 5
M 10,a[176:179],50
R 46,1,29696
M 14,a[180:183],50
M 10,a[184:187],54
R 50,1,30720
M 14,a[188:191],54
L 5
D 0,56,2048
M 10,a[192:195],26
s_add_u32 m0,s59,0
R 54,1,31744
M 14,a[196:199],26
M 10,a[200:203],30
R 26,1,32768
M 14,a[204:207],30
L 5
M 10,a[208:211],34
R 30,1,33792
M 14,a[212:215],34
M 10,a[216:219],38
R 34,1,34816
M 14,a[220:223],38
L 5
buffer_load_dword v8,s[36:39],s58 offen offset:0 sc1 lds
M 10,a[224:227],42
s_add_u32 m0,s57,28672
s_add_u32 s56,s55,28672
R 38,1,35840
M 14,a[228:231],42
D 0,56,0
M 10,a[232:235],46
R 42,1,36864
M 14,a[236:239],46
L 5
M 10,a[240:243],50
R 46,1,37888
M 14,a[244:247],50
M 10,a[248:251],54
R 50,1,38912
M 14,a[252:255],54
L 5
M 10,v[64:67],26
R 54,1,39936
M 14,v[68:71],26
D 0,56,1024
M 10,v[72:75],30
R 26,1,40960
M 14,v[76:79],30
L 5
M 10,v[80:83],34
R 30,1,41984
M 14,v[84:87],34
M 10,v[88:91],38
R 34,1,43008
M 14,v[92:95],38
L 5
M 10,v[96:99],42
R 38,1,44032
M 14,v[100:103],42
D 0,56,2048
M 10,v[104:107],46
R 42,1,45056
M 14,v[108:111],46
L 5
M 10,v[112:115],50
R 46,1,46080
M 14,v[116:119],50
M 10,v[120:123],54
R 50,1,47104
M 14,v[124:127],54
L 5
D 0,56,3072
M 10,v[128:131],26
s_add_u32 m0,s57,32768
s_add_u32 s56,s56,4096
R 54,1,48128
M 14,v[132:135],26
s_add_u32 s55,s55,s60
s_sub_u32 s57,s84,s57
s_add_u32 s54,s54,s46
M 10,v[136:139],30
R 26,1,49152
M 14,v[140:143],30
L 5
M 10,v[144:147],34
R 30,1,50176
M 14,v[148:151],34
M 10,v[152:155],38
R 34,1,51200
M 14,v[156:159],38
L 5
D 0,56,0
M 10,v[160:163],42
R 38,1,52224
M 14,v[164:167],42
M 10,v[168:171],46
R 42,1,53248
M 14,v[172:175],46
L 5
M 10,v[176:179],50
R 46,1,54272
M 14,v[180:183],50
M 10,v[184:187],54
R 50,1,55296
M 14,v[188:191],54
L 5
D 0,56,1024
M 10,v[192:195],26
s_waitcnt vmcnt(6)
R 54,1,56320
M 14,v[196:199],26
s_barrier
ds_read_b32 v7,v6 offset:0
M 10,v[200:203],30
R 26,2,0
M 14,v[204:207],30
L 6
buffer_store_dword v4,v3,s[36:39],s69 offen offset:0 sc0 sc1
M 10,v[208:211],34
s_mov_b32 s69,s68
s_add_u32 s68,s68,s63
R 30,2,1024
M 14,v[212:215],34
M 10,v[216:219],38
L 2
R 34,2,2048
v_cmp_eq_u32 vcc,s53,v7
M 14,v[220:223],38
s_nop 0
D 0,56,2048
M 10,v[224:227],42
s_cmp_eq_u32 vcc_lo,-1
R 38,2,3072
M 14,v[228:231],42
M 10,v[232:235],46
R 42,2,4096
M 14,v[236:239],46
s_nop 0
M 10,v[240:243],50
R 46,2,5120
M 14,v[244:247],50
M 10,v[248:251],54
R 50,2,6144
M 14,v[252:255],54
L 5
s_cbranch_scc0 Lg4_L1_%=
Lg4_F1_%=:
v_mfma_f32_16x16x32_bf16 v[18:21],a[0:3],v[26:29],0
s_add_u32 s58,s58,s61
R 54,2,7168
v_lshlrev_b32 v62,16,v58
v_mfma_f32_16x16x32_bf16 v[22:25],a[4:7],v[26:29],0
s_add_u32 m0,s57,0
s_add_u32 s56,s55,0
v_and_b32 v63,0xffff0000,v58
M 18,a[8:11],30
s_mov_b32 s67,s65
R 26,2,8192
v_add_f32 v10,v10,v62
M 22,a[12:15],30
L 5
D 0,56,0
M 18,a[16:19],34
R 30,2,9216
v_add_f32 v11,v11,v63
M 22,a[20:23],34
v_lshlrev_b32 v62,16,v59
v_add_f32 v12,v12,v62
M 18,a[24:27],38
R 34,2,10240
v_and_b32 v63,0xffff0000,v59
M 22,a[28:31],38
L 5
v_add_f32 v13,v13,v63
v_lshlrev_b32 v62,16,v60
M 18,a[32:35],42
R 38,2,11264
v_add_f32 v14,v14,v62
M 22,a[36:39],42
D 0,56,1024
M 18,a[40:43],46
R 42,2,12288
v_and_b32 v63,0xffff0000,v60
M 22,a[44:47],46
L 5
v_add_f32 v15,v15,v63
v_lshlrev_b32 v62,16,v61
M 18,a[48:51],50
R 46,2,13312
v_add_f32 v16,v16,v62
M 22,a[52:55],50
v_and_b32 v63,0xffff0000,v61
v_add_f32 v17,v17,v63
M 18,a[56:59],54
R 50,2,14336
v_cvt_pk_bf16_f32 v10,v10,v11
M 22,a[60:63],54
L 5
buffer_load_dwordx4 v[58:61],v0,s[36:39],s74 offen offset:0 sc1
M 18,a[64:67],26
R 54,2,15360
v_cvt_pk_bf16_f32 v11,v12,v13
M 22,a[68:71],26
D 0,56,2048
M 18,a[72:75],30
R 26,2,16384
v_cvt_pk_bf16_f32 v12,v14,v15
M 22,a[76:79],30
L 5
v_cvt_pk_bf16_f32 v13,v16,v17
M 18,a[80:83],34
R 30,2,17408
M 22,a[84:87],34
buffer_store_dwordx4 v[10:13],v0,s[36:39],s66 offen offset:0 sc0 sc1
M 18,a[88:91],38
R 34,2,18432
M 22,a[92:95],38
L 5
M 18,a[96:99],42
R 38,2,19456
M 22,a[100:103],42
D 0,56,3072
M 18,a[104:107],46
s_add_u32 m0,s57,4096
s_add_u32 s56,s56,4096
R 42,2,20480
M 22,a[108:111],46
s_add_u32 s74,s74,s62
s_add_u32 s65,s65,s62
L 5
M 18,a[112:115],50
R 46,2,21504
M 22,a[116:119],50
M 18,a[120:123],54
R 50,2,22528
M 22,a[124:127],54
L 5
D 0,56,0
M 18,a[128:131],26
R 54,2,23552
M 22,a[132:135],26
M 18,a[136:139],30
R 26,2,24576
M 22,a[140:143],30
L 5
M 18,a[144:147],34
R 30,2,25600
M 22,a[148:151],34
M 18,a[152:155],38
R 34,2,26624
M 22,a[156:159],38
L 5
D 0,56,1024
M 18,a[160:163],42
s_waitcnt vmcnt(8)
R 38,2,27648
M 22,a[164:167],42
s_barrier
M 18,a[168:171],46
R 42,2,28672
M 22,a[172:175],46
L 5
M 18,a[176:179],50
R 46,2,29696
M 22,a[180:183],50
M 18,a[184:187],54
R 50,2,30720
M 22,a[188:191],54
L 5
D 0,56,2048
M 18,a[192:195],26
s_add_u32 m0,s59,1024
R 54,2,31744
M 22,a[196:199],26
M 18,a[200:203],30
R 26,2,32768
M 22,a[204:207],30
L 5
M 18,a[208:211],34
R 30,2,33792
M 22,a[212:215],34
M 18,a[216:219],38
R 34,2,34816
M 22,a[220:223],38
L 5
buffer_load_dword v8,s[36:39],s58 offen offset:0 sc1 lds
M 18,a[224:227],42
s_add_u32 m0,s57,28672
s_add_u32 s56,s55,28672
R 38,2,35840
M 22,a[228:231],42
D 0,56,0
M 18,a[232:235],46
R 42,2,36864
M 22,a[236:239],46
L 5
M 18,a[240:243],50
R 46,2,37888
M 22,a[244:247],50
M 18,a[248:251],54
R 50,2,38912
M 22,a[252:255],54
L 5
M 18,v[64:67],26
R 54,2,39936
M 22,v[68:71],26
D 0,56,1024
M 18,v[72:75],30
R 26,2,40960
M 22,v[76:79],30
L 5
M 18,v[80:83],34
R 30,2,41984
M 22,v[84:87],34
M 18,v[88:91],38
R 34,2,43008
M 22,v[92:95],38
L 5
M 18,v[96:99],42
R 38,2,44032
M 22,v[100:103],42
D 0,56,2048
M 18,v[104:107],46
R 42,2,45056
M 22,v[108:111],46
L 5
M 18,v[112:115],50
R 46,2,46080
M 22,v[116:119],50
M 18,v[120:123],54
R 50,2,47104
M 22,v[124:127],54
L 5
D 0,56,3072
M 18,v[128:131],26
s_add_u32 m0,s57,32768
s_add_u32 s56,s56,4096
R 54,2,48128
M 22,v[132:135],26
s_add_u32 s55,s55,s60
s_sub_u32 s57,s84,s57
s_add_u32 s54,s54,s46
M 18,v[136:139],30
R 26,2,49152
M 22,v[140:143],30
L 5
M 18,v[144:147],34
R 30,2,50176
M 22,v[148:151],34
M 18,v[152:155],38
R 34,2,51200
M 22,v[156:159],38
L 5
D 0,56,0
M 18,v[160:163],42
R 38,2,52224
M 22,v[164:167],42
M 18,v[168:171],46
R 42,2,53248
M 22,v[172:175],46
L 5
M 18,v[176:179],50
R 46,2,54272
M 22,v[180:183],50
M 18,v[184:187],54
R 50,2,55296
M 22,v[188:191],54
L 5
D 0,56,1024
M 18,v[192:195],26
s_waitcnt vmcnt(6)
R 54,2,56320
M 22,v[196:199],26
s_barrier
ds_read_b32 v7,v6 offset:1024
M 18,v[200:203],30
R 26,1,0
M 22,v[204:207],30
L 6
buffer_store_dword v4,v3,s[36:39],s69 offen offset:0 sc0 sc1
M 18,v[208:211],34
s_mov_b32 s69,s68
s_add_u32 s68,s68,s63
R 30,1,1024
M 22,v[212:215],34
M 18,v[216:219],38
L 2
R 34,1,2048
v_cmp_eq_u32 vcc,s53,v7
M 22,v[220:223],38
s_nop 0
D 0,56,2048
M 18,v[224:227],42
s_cmp_eq_u32 vcc_lo,-1
R 38,1,3072
M 22,v[228:231],42
M 18,v[232:235],46
R 42,1,4096
M 22,v[236:239],46
s_nop 0
M 18,v[240:243],50
R 46,1,5120
M 22,v[244:247],50
M 18,v[248:251],54
R 50,1,6144
M 22,v[252:255],54
L 5
s_cbranch_scc1 Lg4_F0_%=
Lg4_L0_%=:
v_mfma_f32_16x16x32_bf16 v[10:13],a[0:3],v[26:29],0
s_add_u32 m0,s59,3072
R 54,1,7168
v_lshlrev_b32 v62,16,v58
v_mfma_f32_16x16x32_bf16 v[14:17],a[4:7],v[26:29],0
s_mov_b32 s66,s65
buffer_load_dword v8,s[36:39],s58 offen offset:0 sc1 lds
M 10,a[8:11],30
R 26,1,8192
v_add_f32 v18,v18,v62
M 14,a[12:15],30
L 5
v_and_b32 v63,0xffff0000,v58
v_add_f32 v19,v19,v63
M 10,a[16:19],34
R 30,1,9216
v_lshlrev_b32 v62,16,v59
M 14,a[20:23],34
v_add_f32 v20,v20,v62
v_and_b32 v63,0xffff0000,v59
M 10,a[24:27],38
)"
R"(R 34,1,10240
v_add_f32 v21,v21,v63
M 14,a[28:31],38
L 5
v_lshlrev_b32 v62,16,v60
v_add_f32 v22,v22,v62
M 10,a[32:35],42
R 38,1,11264
v_and_b32 v63,0xffff0000,v60
M 14,a[36:39],42
v_add_f32 v23,v23,v63
v_lshlrev_b32 v62,16,v61
M 10,a[40:43],46
R 42,1,12288
v_add_f32 v24,v24,v62
M 14,a[44:47],46
L 5
v_and_b32 v63,0xffff0000,v61
v_add_f32 v25,v25,v63
M 10,a[48:51],50
R 46,1,13312
v_cvt_pk_bf16_f32 v18,v18,v19
M 14,a[52:55],50
buffer_load_dwordx4 v[58:61],v0,s[36:39],s74 offen offset:0 sc1
M 10,a[56:59],54
s_add_u32 s74,s74,s62
R 50,1,14336
v_cvt_pk_bf16_f32 v19,v20,v21
M 14,a[60:63],54
L 5
v_cvt_pk_bf16_f32 v20,v22,v23
v_cvt_pk_bf16_f32 v21,v24,v25
M 10,a[64:67],26
s_add_u32 s65,s65,s62
R 54,1,15360
M 14,a[68:71],26
buffer_store_dwordx4 v[18:21],v0,s[36:39],s67 offen offset:0 sc0 sc1
M 10,a[72:75],30
R 26,1,16384
M 14,a[76:79],30
L 5
M 10,a[80:83],34
R 30,1,17408
M 14,a[84:87],34
M 10,a[88:91],38
R 34,1,18432
M 14,a[92:95],38
L 5
M 10,a[96:99],42
R 38,1,19456
M 14,a[100:103],42
M 10,a[104:107],46
R 42,1,20480
M 14,a[108:111],46
L 5
M 10,a[112:115],50
R 46,1,21504
M 14,a[116:119],50
M 10,a[120:123],54
R 50,1,22528
M 14,a[124:127],54
L 5
M 10,a[128:131],26
R 54,1,23552
M 14,a[132:135],26
M 10,a[136:139],30
R 26,1,24576
M 14,a[140:143],30
L 5
M 10,a[144:147],34
R 30,1,25600
M 14,a[148:151],34
M 10,a[152:155],38
R 34,1,26624
M 14,a[156:159],38
L 5
M 10,a[160:163],42
s_waitcnt vmcnt(2)
R 38,1,27648
M 14,a[164:167],42
s_barrier
ds_read_b32 v7,v6 offset:3072
M 10,a[168:171],46
R 42,1,28672
M 14,a[172:175],46
L 6
M 10,a[176:179],50
R 46,1,29696
M 14,a[180:183],50
M 10,a[184:187],54
L 2
R 50,1,30720
v_cmp_eq_u32 vcc,s53,v7
M 14,a[188:191],54
s_nop 0
M 10,a[192:195],26
R 54,1,31744
M 14,a[196:199],26
s_cmp_eq_u32 vcc_lo,-1
s_cbranch_scc0 Lg4_X0_%=
M 10,a[200:203],30
s_add_u32 s58,s58,s61
s_add_u32 m0,s57,0
R 26,1,32768
M 14,a[204:207],30
s_add_u32 s56,s55,0
s_nop 0
M 10,a[208:211],34
R 30,1,33792
M 14,a[212:215],34
D 0,56,0
M 10,a[216:219],38
R 34,1,34816
M 14,a[220:223],38
L 5
M 10,a[224:227],42
R 38,1,35840
M 14,a[228:231],42
D 0,56,1024
M 10,a[232:235],46
R 42,1,36864
M 14,a[236:239],46
L 5
M 10,a[240:243],50
R 46,1,37888
M 14,a[244:247],50
D 0,56,2048
M 10,a[248:251],54
R 50,1,38912
M 14,a[252:255],54
L 5
M 10,v[64:67],26
R 54,1,39936
M 14,v[68:71],26
D 0,56,3072
M 10,v[72:75],30
s_add_u32 m0,s57,4096
s_add_u32 s56,s56,4096
R 26,1,40960
M 14,v[76:79],30
L 5
M 10,v[80:83],34
R 30,1,41984
M 14,v[84:87],34
D 0,56,0
M 10,v[88:91],38
R 34,1,43008
M 14,v[92:95],38
L 5
M 10,v[96:99],42
R 38,1,44032
M 14,v[100:103],42
D 0,56,1024
M 10,v[104:107],46
R 42,1,45056
M 14,v[108:111],46
L 5
M 10,v[112:115],50
R 46,1,46080
M 14,v[116:119],50
D 0,56,2048
M 10,v[120:123],54
s_add_u32 m0,s59,0
R 50,1,47104
M 14,v[124:127],54
L 5
buffer_load_dword v8,s[36:39],s58 offen offset:0 sc1 lds
M 10,v[128:131],26
s_add_u32 m0,s57,28672
s_add_u32 s56,s55,28672
R 54,1,48128
M 14,v[132:135],26
M 10,v[136:139],30
R 26,1,49152
M 14,v[140:143],30
L 5
D 0,56,0
M 10,v[144:147],34
R 30,1,50176
M 14,v[148:151],34
M 10,v[152:155],38
R 34,1,51200
M 14,v[156:159],38
L 5
D 0,56,1024
M 10,v[160:163],42
R 38,1,52224
M 14,v[164:167],42
M 10,v[168:171],46
R 42,1,53248
M 14,v[172:175],46
L 5
D 0,56,2048
M 10,v[176:179],50
R 46,1,54272
M 14,v[180:183],50
M 10,v[184:187],54
R 50,1,55296
M 14,v[188:191],54
L 5
D 0,56,3072
M 10,v[192:195],26
s_add_u32 m0,s57,32768
s_waitcnt vmcnt(4)
R 54,1,56320
M 14,v[196:199],26
s_add_u32 s56,s56,4096
s_barrier
ds_read_b32 v7,v6 offset:0
M 10,v[200:203],30
s_add_u32 s55,s55,s60
s_sub_u32 s57,s84,s57
R 26,2,0
M 14,v[204:207],30
L 6
buffer_store_dword v4,v3,s[36:39],s69 offen offset:0 sc0 sc1
M 10,v[208:211],34
s_add_u32 s54,s54,s46
s_mov_b32 s69,s68
R 30,2,1024
M 14,v[212:215],34
s_add_u32 s68,s68,s63
D 0,56,0
M 10,v[216:219],38
L 2
R 34,2,2048
v_cmp_eq_u32 vcc,s53,v7
M 14,v[220:223],38
s_nop 0
D 0,56,1024
M 10,v[224:227],42
s_cmp_eq_u32 vcc_lo,-1
R 38,2,3072
M 14,v[228:231],42
M 10,v[232:235],46
R 42,2,4096
M 14,v[236:239],46
s_nop 0
D 0,56,2048
M 10,v[240:243],50
R 46,2,5120
M 14,v[244:247],50
M 10,v[248:251],54
R 50,2,6144
M 14,v[252:255],54
L 5
s_cbranch_scc1 Lg4_F1_%=
s_branch Lg4_L1_%=
Lg4_X0_%=:
M 10,a[200:203],30
s_add_u32 m0,s59,0
R 26,1,32768
M 14,a[204:207],30
s_nop 0
buffer_load_dword v8,s[36:39],s58 offen offset:0 sc1 lds
M 10,a[208:211],34
R 30,1,33792
M 14,a[212:215],34
M 10,a[216:219],38
R 34,1,34816
M 14,a[220:223],38
L 5
M 10,a[224:227],42
R 38,1,35840
M 14,a[228:231],42
M 10,a[232:235],46
R 42,1,36864
M 14,a[236:239],46
L 5
M 10,a[240:243],50
R 46,1,37888
M 14,a[244:247],50
M 10,a[248:251],54
R 50,1,38912
M 14,a[252:255],54
L 5
M 10,v[64:67],26
R 54,1,39936
M 14,v[68:71],26
M 10,v[72:75],30
R 26,1,40960
M 14,v[76:79],30
L 5
M 10,v[80:83],34
R 30,1,41984
M 14,v[84:87],34
M 10,v[88:91],38
R 34,1,43008
M 14,v[92:95],38
L 5
M 10,v[96:99],42
R 38,1,44032
M 14,v[100:103],42
M 10,v[104:107],46
R 42,1,45056
M 14,v[108:111],46
L 5
M 10,v[112:115],50
R 46,1,46080
M 14,v[116:119],50
M 10,v[120:123],54
R 50,1,47104
M 14,v[124:127],54
L 5
M 10,v[128:131],26
R 54,1,48128
M 14,v[132:135],26
M 10,v[136:139],30
R 26,1,49152
M 14,v[140:143],30
L 5
M 10,v[144:147],34
R 30,1,50176
M 14,v[148:151],34
M 10,v[152:155],38
R 34,1,51200
M 14,v[156:159],38
L 5
M 10,v[160:163],42
R 38,1,52224
M 14,v[164:167],42
M 10,v[168:171],46
R 42,1,53248
M 14,v[172:175],46
L 5
M 10,v[176:179],50
R 46,1,54272
M 14,v[180:183],50
M 10,v[184:187],54
R 50,1,55296
M 14,v[188:191],54
L 5
M 10,v[192:195],26
s_waitcnt vmcnt(0)
R 54,1,56320
M 14,v[196:199],26
s_barrier
ds_read_b32 v7,v6 offset:0
M 10,v[200:203],30
buffer_store_dword v4,v3,s[36:39],s69 offen offset:0 sc0 sc1
M 14,v[204:207],30
L 5
s_mov_b32 s69,s68
s_add_u32 s68,s68,s63
M 10,v[208:211],34
M 14,v[212:215],34
M 10,v[216:219],38
L 0
v_cmp_eq_u32 vcc,s53,v7
M 14,v[220:223],38
s_nop 0
M 10,v[224:227],42
M 14,v[228:231],42
M 10,v[232:235],46
M 14,v[236:239],46
s_nop 0
M 10,v[240:243],50
M 14,v[244:247],50
M 10,v[248:251],54
M 14,v[252:255],54
s_mov_b32 s80,1
s_cmp_eq_u32 vcc_lo,-1
s_cbranch_scc0 Lg4_xn0_%=
s_add_u32 m0,s57,0
s_add_u32 s56,s55,0
D 0,56,0
v_lshlrev_b32 v62,16,v58
D 0,56,1024
v_add_f32 v10,v10,v62
D 0,56,2048
v_and_b32 v63,0xffff0000,v58
D 0,56,3072
v_add_f32 v11,v11,v63
s_add_u32 m0,s57,4096
s_add_u32 s56,s56,4096
D 0,56,0
v_lshlrev_b32 v62,16,v59
D 0,56,1024
v_add_f32 v12,v12,v62
D 0,56,2048
v_and_b32 v63,0xffff0000,v59
s_add_u32 m0,s57,28672
s_add_u32 s56,s55,28672
D 0,56,0
v_add_f32 v13,v13,v63
D 0,56,1024
v_lshlrev_b32 v62,16,v60
D 0,56,2048
v_add_f32 v14,v14,v62
D 0,56,3072
v_and_b32 v63,0xffff0000,v60
s_add_u32 m0,s57,32768
s_add_u32 s56,s56,4096
D 0,56,0
v_add_f32 v15,v15,v63
D 0,56,1024
v_lshlrev_b32 v62,16,v61
D 0,56,2048
v_add_f32 v16,v16,v62
s_add_u32 s55,s55,s60
s_sub_u32 s57,s84,s57
s_add_u32 s54,s54,s46
v_and_b32 v63,0xffff0000,v61
v_add_f32 v17,v17,v63
v_cvt_pk_bf16_f32 v10,v10,v11
v_cvt_pk_bf16_f32 v11,v12,v13
v_cvt_pk_bf16_f32 v12,v14,v15
v_cvt_pk_bf16_f32 v13,v16,v17
buffer_store_dwordx4 v[10:13],v0,s[36:39],s66 offen offset:0 sc0 sc1
s_mov_b32 s72,s69
s_mov_b32 s69,0x7fff0000
s_mov_b32 s66,0x7fff0000
s_mov_b32 s76,0x7fff0000
s_branch Lg4_gox_%=
Lg4_xn0_%=:
s_cmp_ge_u32 s54,256
s_cbranch_scc1 Lg4_fin0_%=
s_add_u32 m0,s59,4096
s_nop 0
buffer_load_dword v8,s[36:39],s58 offen offset:0 sc1 lds
buffer_store_dword v4,v3,s[36:39],s71 offen offset:0 sc0 sc1
s_add_u32 m0,s59,5120
s_nop 0
buffer_load_dword v8,s[36:39],s58 offen offset:0 sc1 lds
buffer_store_dword v4,v3,s[36:39],s71 offen offset:0 sc0 sc1
s_mov_b32 s83,0
v_lshlrev_b32 v62,16,v58
v_add_f32 v10,v10,v62
v_and_b32 v63,0xffff0000,v58
v_add_f32 v11,v11,v63
v_lshlrev_b32 v62,16,v59
v_add_f32 v12,v12,v62
v_and_b32 v63,0xffff0000,v59
v_add_f32 v13,v13,v63
v_lshlrev_b32 v62,16,v60
v_add_f32 v14,v14,v62
v_and_b32 v63,0xffff0000,v60
v_add_f32 v15,v15,v63
v_lshlrev_b32 v62,16,v61
v_add_f32 v16,v16,v62
v_and_b32 v63,0xffff0000,v61
v_add_f32 v17,v17,v63
v_cvt_pk_bf16_f32 v10,v10,v11
v_cvt_pk_bf16_f32 v11,v12,v13
v_cvt_pk_bf16_f32 v12,v14,v15
v_cvt_pk_bf16_f32 v13,v16,v17
buffer_store_dwordx4 v[10:13],v0,s[36:39],s66 offen offset:0 sc0 sc1
s_mov_b32 s72,s69
s_mov_b32 s69,0x7fff0000
s_mov_b32 s66,0x7fff0000
s_mov_b32 s76,0x7fff0000
s_branch Lg4_poll_%=
Lg4_fin0_%=:
v_lshlrev_b32 v62,16,v58
v_add_f32 v10,v10,v62
v_and_b32 v63,0xffff0000,v58
v_add_f32 v11,v11,v63
v_lshlrev_b32 v62,16,v59
v_add_f32 v12,v12,v62
v_and_b32 v63,0xffff0000,v59
v_add_f32 v13,v13,v63
v_lshlrev_b32 v62,16,v60
v_add_f32 v14,v14,v62
v_and_b32 v63,0xffff0000,v60
v_add_f32 v15,v15,v63
v_lshlrev_b32 v62,16,v61
v_add_f32 v16,v16,v62
v_and_b32 v63,0xffff0000,v61
v_add_f32 v17,v17,v63
v_cvt_pk_bf16_f32 v10,v10,v11
v_cvt_pk_bf16_f32 v11,v12,v13
v_cvt_pk_bf16_f32 v12,v14,v15
v_cvt_pk_bf16_f32 v13,v16,v17
buffer_store_dwordx4 v[10:13],v0,s[36:39],s66 offen offset:0 sc0 sc1
s_mov_b32 s72,s69
s_mov_b32 s69,0x7fff0000
s_mov_b32 s66,0x7fff0000
s_mov_b32 s76,0x7fff0000
s_branch Lg4_done_%=
Lg4_L1_%=:
v_mfma_f32_16x16x32_bf16 v[18:21],a[0:3],v[26:29],0
s_add_u32 m0,s59,3072
R 54,2,7168
v_lshlrev_b32 v62,16,v58
v_mfma_f32_16x16x32_bf16 v[22:25],a[4:7],v[26:29],0
s_mov_b32 s67,s65
buffer_load_dword v8,s[36:39],s58 offen offset:0 sc1 lds
M 18,a[8:11],30
R 26,2,8192
v_add_f32 v10,v10,v62
M 22,a[12:15],30
L 5
v_and_b32 v63,0xffff0000,v58
v_add_f32 v11,v11,v63
M 18,a[16:19],34
R 30,2,9216
v_lshlrev_b32 v62,16,v59
M 22,a[20:23],34
v_add_f32 v12,v12,v62
v_and_b32 v63,0xffff0000,v59
)"
R"(M 18,a[24:27],38
R 34,2,10240
v_add_f32 v13,v13,v63
M 22,a[28:31],38
L 5
v_lshlrev_b32 v62,16,v60
v_add_f32 v14,v14,v62
M 18,a[32:35],42
R 38,2,11264
v_and_b32 v63,0xffff0000,v60
M 22,a[36:39],42
v_add_f32 v15,v15,v63
v_lshlrev_b32 v62,16,v61
M 18,a[40:43],46
R 42,2,12288
v_add_f32 v16,v16,v62
M 22,a[44:47],46
L 5
v_and_b32 v63,0xffff0000,v61
v_add_f32 v17,v17,v63
M 18,a[48:51],50
R 46,2,13312
v_cvt_pk_bf16_f32 v10,v10,v11
M 22,a[52:55],50
buffer_load_dwordx4 v[58:61],v0,s[36:39],s74 offen offset:0 sc1
M 18,a[56:59],54
s_add_u32 s74,s74,s62
R 50,2,14336
v_cvt_pk_bf16_f32 v11,v12,v13
M 22,a[60:63],54
L 5
v_cvt_pk_bf16_f32 v12,v14,v15
v_cvt_pk_bf16_f32 v13,v16,v17
M 18,a[64:67],26
s_add_u32 s65,s65,s62
R 54,2,15360
M 22,a[68:71],26
buffer_store_dwordx4 v[10:13],v0,s[36:39],s66 offen offset:0 sc0 sc1
M 18,a[72:75],30
R 26,2,16384
M 22,a[76:79],30
L 5
M 18,a[80:83],34
R 30,2,17408
M 22,a[84:87],34
M 18,a[88:91],38
R 34,2,18432
M 22,a[92:95],38
L 5
M 18,a[96:99],42
R 38,2,19456
M 22,a[100:103],42
M 18,a[104:107],46
R 42,2,20480
M 22,a[108:111],46
L 5
M 18,a[112:115],50
R 46,2,21504
M 22,a[116:119],50
M 18,a[120:123],54
R 50,2,22528
M 22,a[124:127],54
L 5
M 18,a[128:131],26
R 54,2,23552
M 22,a[132:135],26
M 18,a[136:139],30
R 26,2,24576
M 22,a[140:143],30
L 5
M 18,a[144:147],34
R 30,2,25600
M 22,a[148:151],34
M 18,a[152:155],38
R 34,2,26624
M 22,a[156:159],38
L 5
M 18,a[160:163],42
s_waitcnt vmcnt(2)
R 38,2,27648
M 22,a[164:167],42
s_barrier
ds_read_b32 v7,v6 offset:3072
M 18,a[168:171],46
R 42,2,28672
M 22,a[172:175],46
L 6
M 18,a[176:179],50
R 46,2,29696
M 22,a[180:183],50
M 18,a[184:187],54
L 2
R 50,2,30720
v_cmp_eq_u32 vcc,s53,v7
M 22,a[188:191],54
s_nop 0
M 18,a[192:195],26
R 54,2,31744
M 22,a[196:199],26
s_cmp_eq_u32 vcc_lo,-1
s_cbranch_scc0 Lg4_X1_%=
M 18,a[200:203],30
s_add_u32 s58,s58,s61
s_add_u32 m0,s57,0
R 26,2,32768
M 22,a[204:207],30
s_add_u32 s56,s55,0
s_nop 0
M 18,a[208:211],34
R 30,2,33792
M 22,a[212:215],34
D 0,56,0
M 18,a[216:219],38
R 34,2,34816
M 22,a[220:223],38
L 5
M 18,a[224:227],42
R 38,2,35840
M 22,a[228:231],42
D 0,56,1024
M 18,a[232:235],46
R 42,2,36864
M 22,a[236:239],46
L 5
M 18,a[240:243],50
R 46,2,37888
M 22,a[244:247],50
D 0,56,2048
M 18,a[248:251],54
R 50,2,38912
M 22,a[252:255],54
L 5
M 18,v[64:67],26
R 54,2,39936
M 22,v[68:71],26
D 0,56,3072
M 18,v[72:75],30
s_add_u32 m0,s57,4096
s_add_u32 s56,s56,4096
R 26,2,40960
M 22,v[76:79],30
L 5
M 18,v[80:83],34
R 30,2,41984
M 22,v[84:87],34
D 0,56,0
M 18,v[88:91],38
R 34,2,43008
M 22,v[92:95],38
L 5
M 18,v[96:99],42
R 38,2,44032
M 22,v[100:103],42
D 0,56,1024
M 18,v[104:107],46
R 42,2,45056
M 22,v[108:111],46
L 5
M 18,v[112:115],50
R 46,2,46080
M 22,v[116:119],50
D 0,56,2048
M 18,v[120:123],54
s_add_u32 m0,s59,1024
R 50,2,47104
M 22,v[124:127],54
L 5
buffer_load_dword v8,s[36:39],s58 offen offset:0 sc1 lds
M 18,v[128:131],26
s_add_u32 m0,s57,28672
s_add_u32 s56,s55,28672
R 54,2,48128
M 22,v[132:135],26
M 18,v[136:139],30
R 26,2,49152
M 22,v[140:143],30
L 5
D 0,56,0
M 18,v[144:147],34
R 30,2,50176
M 22,v[148:151],34
M 18,v[152:155],38
R 34,2,51200
M 22,v[156:159],38
L 5
D 0,56,1024
M 18,v[160:163],42
R 38,2,52224
M 22,v[164:167],42
M 18,v[168:171],46
R 42,2,53248
M 22,v[172:175],46
L 5
D 0,56,2048
M 18,v[176:179],50
R 46,2,54272
M 22,v[180:183],50
M 18,v[184:187],54
R 50,2,55296
M 22,v[188:191],54
L 5
D 0,56,3072
M 18,v[192:195],26
s_add_u32 m0,s57,32768
s_waitcnt vmcnt(4)
R 54,2,56320
M 22,v[196:199],26
s_add_u32 s56,s56,4096
s_barrier
ds_read_b32 v7,v6 offset:1024
M 18,v[200:203],30
s_add_u32 s55,s55,s60
s_sub_u32 s57,s84,s57
R 26,1,0
M 22,v[204:207],30
L 6
buffer_store_dword v4,v3,s[36:39],s69 offen offset:0 sc0 sc1
M 18,v[208:211],34
s_add_u32 s54,s54,s46
s_mov_b32 s69,s68
R 30,1,1024
M 22,v[212:215],34
s_add_u32 s68,s68,s63
D 0,56,0
M 18,v[216:219],38
L 2
R 34,1,2048
v_cmp_eq_u32 vcc,s53,v7
M 22,v[220:223],38
s_nop 0
D 0,56,1024
M 18,v[224:227],42
s_cmp_eq_u32 vcc_lo,-1
R 38,1,3072
M 22,v[228:231],42
M 18,v[232:235],46
R 42,1,4096
M 22,v[236:239],46
s_nop 0
D 0,56,2048
M 18,v[240:243],50
R 46,1,5120
M 22,v[244:247],50
M 18,v[248:251],54
R 50,1,6144
M 22,v[252:255],54
L 5
s_cbranch_scc1 Lg4_F0_%=
s_branch Lg4_L0_%=
Lg4_X1_%=:
M 18,a[200:203],30
s_add_u32 m0,s59,1024
R 26,2,32768
M 22,a[204:207],30
s_nop 0
buffer_load_dword v8,s[36:39],s58 offen offset:0 sc1 lds
M 18,a[208:211],34
R 30,2,33792
M 22,a[212:215],34
M 18,a[216:219],38
R 34,2,34816
M 22,a[220:223],38
L 5
M 18,a[224:227],42
R 38,2,35840
M 22,a[228:231],42
M 18,a[232:235],46
R 42,2,36864
M 22,a[236:239],46
L 5
M 18,a[240:243],50
R 46,2,37888
M 22,a[244:247],50
M 18,a[248:251],54
R 50,2,38912
M 22,a[252:255],54
L 5
M 18,v[64:67],26
R 54,2,39936
M 22,v[68:71],26
M 18,v[72:75],30
R 26,2,40960
M 22,v[76:79],30
L 5
M 18,v[80:83],34
R 30,2,41984
M 22,v[84:87],34
M 18,v[88:91],38
R 34,2,43008
M 22,v[92:95],38
L 5
M 18,v[96:99],42
R 38,2,44032
M 22,v[100:103],42
M 18,v[104:107],46
R 42,2,45056
M 22,v[108:111],46
L 5
M 18,v[112:115],50
R 46,2,46080
M 22,v[116:119],50
M 18,v[120:123],54
R 50,2,47104
M 22,v[124:127],54
L 5
M 18,v[128:131],26
R 54,2,48128
M 22,v[132:135],26
M 18,v[136:139],30
R 26,2,49152
M 22,v[140:143],30
L 5
M 18,v[144:147],34
R 30,2,50176
M 22,v[148:151],34
M 18,v[152:155],38
R 34,2,51200
M 22,v[156:159],38
L 5
M 18,v[160:163],42
R 38,2,52224
M 22,v[164:167],42
M 18,v[168:171],46
R 42,2,53248
M 22,v[172:175],46
L 5
M 18,v[176:179],50
R 46,2,54272
M 22,v[180:183],50
M 18,v[184:187],54
R 50,2,55296
M 22,v[188:191],54
L 5
M 18,v[192:195],26
s_waitcnt vmcnt(0)
R 54,2,56320
M 22,v[196:199],26
s_barrier
ds_read_b32 v7,v6 offset:1024
M 18,v[200:203],30
buffer_store_dword v4,v3,s[36:39],s69 offen offset:0 sc0 sc1
M 22,v[204:207],30
L 5
s_mov_b32 s69,s68
s_add_u32 s68,s68,s63
M 18,v[208:211],34
M 22,v[212:215],34
M 18,v[216:219],38
L 0
v_cmp_eq_u32 vcc,s53,v7
M 22,v[220:223],38
s_nop 0
M 18,v[224:227],42
M 22,v[228:231],42
M 18,v[232:235],46
M 22,v[236:239],46
s_nop 0
M 18,v[240:243],50
M 22,v[244:247],50
M 18,v[248:251],54
M 22,v[252:255],54
s_mov_b32 s80,0
s_cmp_eq_u32 vcc_lo,-1
s_cbranch_scc0 Lg4_xn1_%=
s_add_u32 m0,s57,0
s_add_u32 s56,s55,0
D 0,56,0
v_lshlrev_b32 v62,16,v58
D 0,56,1024
v_add_f32 v18,v18,v62
D 0,56,2048
v_and_b32 v63,0xffff0000,v58
D 0,56,3072
v_add_f32 v19,v19,v63
s_add_u32 m0,s57,4096
s_add_u32 s56,s56,4096
D 0,56,0
v_lshlrev_b32 v62,16,v59
D 0,56,1024
v_add_f32 v20,v20,v62
D 0,56,2048
v_and_b32 v63,0xffff0000,v59
s_add_u32 m0,s57,28672
s_add_u32 s56,s55,28672
D 0,56,0
v_add_f32 v21,v21,v63
D 0,56,1024
v_lshlrev_b32 v62,16,v60
D 0,56,2048
v_add_f32 v22,v22,v62
D 0,56,3072
v_and_b32 v63,0xffff0000,v60
s_add_u32 m0,s57,32768
s_add_u32 s56,s56,4096
D 0,56,0
v_add_f32 v23,v23,v63
D 0,56,1024
v_lshlrev_b32 v62,16,v61
D 0,56,2048
v_add_f32 v24,v24,v62
s_add_u32 s55,s55,s60
s_sub_u32 s57,s84,s57
s_add_u32 s54,s54,s46
v_and_b32 v63,0xffff0000,v61
v_add_f32 v25,v25,v63
v_cvt_pk_bf16_f32 v18,v18,v19
v_cvt_pk_bf16_f32 v19,v20,v21
v_cvt_pk_bf16_f32 v20,v22,v23
v_cvt_pk_bf16_f32 v21,v24,v25
buffer_store_dwordx4 v[18:21],v0,s[36:39],s67 offen offset:0 sc0 sc1
s_mov_b32 s72,s69
s_mov_b32 s69,0x7fff0000
s_mov_b32 s67,0x7fff0000
s_mov_b32 s77,0x7fff0000
s_branch Lg4_gox_%=
Lg4_xn1_%=:
s_cmp_ge_u32 s54,256
s_cbranch_scc1 Lg4_fin1_%=
s_add_u32 m0,s59,4096
s_nop 0
buffer_load_dword v8,s[36:39],s58 offen offset:0 sc1 lds
buffer_store_dword v4,v3,s[36:39],s71 offen offset:0 sc0 sc1
s_add_u32 m0,s59,5120
s_nop 0
buffer_load_dword v8,s[36:39],s58 offen offset:0 sc1 lds
buffer_store_dword v4,v3,s[36:39],s71 offen offset:0 sc0 sc1
s_mov_b32 s83,0
v_lshlrev_b32 v62,16,v58
v_add_f32 v18,v18,v62
v_and_b32 v63,0xffff0000,v58
v_add_f32 v19,v19,v63
v_lshlrev_b32 v62,16,v59
v_add_f32 v20,v20,v62
v_and_b32 v63,0xffff0000,v59
v_add_f32 v21,v21,v63
v_lshlrev_b32 v62,16,v60
v_add_f32 v22,v22,v62
v_and_b32 v63,0xffff0000,v60
v_add_f32 v23,v23,v63
v_lshlrev_b32 v62,16,v61
v_add_f32 v24,v24,v62
v_and_b32 v63,0xffff0000,v61
v_add_f32 v25,v25,v63
v_cvt_pk_bf16_f32 v18,v18,v19
v_cvt_pk_bf16_f32 v19,v20,v21
v_cvt_pk_bf16_f32 v20,v22,v23
v_cvt_pk_bf16_f32 v21,v24,v25
buffer_store_dwordx4 v[18:21],v0,s[36:39],s67 offen offset:0 sc0 sc1
s_mov_b32 s72,s69
s_mov_b32 s69,0x7fff0000
s_mov_b32 s67,0x7fff0000
s_mov_b32 s77,0x7fff0000
s_branch Lg4_poll_%=
Lg4_fin1_%=:
v_lshlrev_b32 v62,16,v58
v_add_f32 v18,v18,v62
v_and_b32 v63,0xffff0000,v58
v_add_f32 v19,v19,v63
v_lshlrev_b32 v62,16,v59
v_add_f32 v20,v20,v62
v_and_b32 v63,0xffff0000,v59
v_add_f32 v21,v21,v63
v_lshlrev_b32 v62,16,v60
v_add_f32 v22,v22,v62
v_and_b32 v63,0xffff0000,v60
v_add_f32 v23,v23,v63
v_lshlrev_b32 v62,16,v61
v_add_f32 v24,v24,v62
v_and_b32 v63,0xffff0000,v61
v_add_f32 v25,v25,v63
v_cvt_pk_bf16_f32 v18,v18,v19
v_cvt_pk_bf16_f32 v19,v20,v21
v_cvt_pk_bf16_f32 v20,v22,v23
v_cvt_pk_bf16_f32 v21,v24,v25
buffer_store_dwordx4 v[18:21],v0,s[36:39],s67 offen offset:0 sc0 sc1
s_mov_b32 s72,s69
s_mov_b32 s69,0x7fff0000
s_mov_b32 s67,0x7fff0000
s_mov_b32 s77,0x7fff0000
s_branch Lg4_done_%=
Lg4_done_%=:
s_nop 0
s_barrier
buffer_store_dword v4,v3,s[36:39],s70 offen offset:0 sc0 sc1
s_waitcnt vmcnt(1)
s_barrier
buffer_store_dword v4,v3,s[36:39],s72 offen offset:0 sc0 sc1
s_waitcnt vmcnt(0) lgkmcnt(0)
s_barrier
s_endpgm
Lcode_g4_e:
.purgem M
.purgem R
.purgem T
.purgem D
.purgem L
)"
  :: "s"(ws), "s"(sWave), "s"(sG0), "s"(sStep), "s"(sOutOff), "s"(sFoutOff), "s"(sAuxOff), "s"(sWw), "s"(sBuf), "s"(sRk), "v"(vL16), "v"(vFlag), "v"(vSnapOff), "v"(vSnapRd), "v"(vWd0), "v"(vWd1), "v"(vWr0), "v"(vWr1), "s"(w4) : "v0", "v1", "v2", "v3", "v4", "v5", "v6", "v7", "v8", "v9", "v10", "v11", "v12", "v13", "v14", "v15", "v16", "v17", "v18", "v19", "v20", "v21", "v22", "v23", "v24", "v25", "v26", "v27", "v28", "v29", "v30", "v31", "v32", "v33", "v34", "v35", "v36", "v37", "v38", "v39", "v40", "v41", "v42", "v43", "v44", "v45", "v46", "v47", "v48", "v49", "v50", "v51", "v52", "v53", "v54", "v55", "v56", "v57", "v58", "v59", "v60", "v61", "v62", "v63", "v64", "v65", "v66", "v67", "v68", "v69", "v70", "v71", "v72", "v73", "v74", "v75", "v76", "v77", "v78", "v79", "v80", "v81", "v82", "v83", "v84", "v85", "v86", "v87", "v88", "v89", "v90", "v91", "v92", "v93", "v94", "v95", "v96", "v97", "v98", "v99", "v100", "v101", "v102", "v103", "v104", "v105", "v106", "v107", "v108", "v109", "v110", "v111", "v112", "v113", "v114", "v115", "v116", "v117", "v118", "v119", "v120", "v121", "v122", "v123", "v124", "v125", "v126", "v127", "v128", "v129", "v130", "v131", "v132", "v133", "v134", "v135", "v136", "v137", "v138", "v139", "v140", "v141", "v142", "v143", "v144", "v145", "v146", "v147", "v148", "v149", "v150", "v151", "v152", "v153", "v154", "v155", "v156", "v157", "v158", "v159", "v160", "v161", "v162", "v163", "v164", "v165", "v166", "v167", "v168", "v169", "v170", "v171", "v172", "v173", "v174", "v175", "v176", "v177", "v178", "v179", "v180", "v181", "v182", "v183", "v184", "v185", "v186", "v187", "v188", "v189", "v190", "v191", "v192", "v193", "v194", "v195", "v196", "v197", "v198", "v199", "v200", "v201", "v202", "v203", "v204", "v205", "v206", "v207", "v208", "v209", "v210", "v211", "v212", "v213", "v214", "v215", "v216", "v217", "v218", "v219", "v220", "v221", "v222", "v223", "v224", "v225", "v226", "v227", "v228", "v229", "v230", "v231", "v232", "v233", "v234", "v235", "v236", "v237", "v238", "v239", "a0", "a1", "a2", "a3", "a4", "a5", "a6", "a7", "a8", "a9", "a10", "a11", "a12", "a13", "a14", "a15", "a16", "a17", "a18", "a19", "a20", "a21", "a22", "a23", "a24", "a25", "a26", "a27", "a28", "a29", "a30", "a31", "a32", "a33", "a34", "a35", "a36", "a37", "a38", "a39", "a40", "a41", "a42", "a43", "a44", "a45", "a46", "a47", "a48", "a49", "a50", "a51", "a52", "a53", "a54", "a55", "a56", "a57", "a58", "a59", "a60", "a61", "a62", "a63", "a64", "a65", "a66", "a67", "a68", "a69", "a70", "a71", "a72", "a73", "a74", "a75", "a76", "a77", "a78", "a79", "a80", "a81", "a82", "a83", "a84", "a85", "a86", "a87", "a88", "a89", "a90", "a91", "a92", "a93", "a94", "a95", "a96", "a97", "a98", "a99", "a100", "a101", "a102", "a103", "a104", "a105", "a106", "a107", "a108", "a109", "a110", "a111", "a112", "a113", "a114", "a115", "a116", "a117", "a118", "a119", "a120", "a121", "a122", "a123", "a124", "a125", "a126", "a127", "a128", "a129", "a130", "a131", "a132", "a133", "a134", "a135", "a136", "a137", "a138", "a139", "a140", "a141", "a142", "a143", "a144", "a145", "a146", "a147", "a148", "a149", "a150", "a151", "a152", "a153", "a154", "a155", "a156", "a157", "a158", "a159", "a160", "a161", "a162", "a163", "a164", "a165", "a166", "a167", "a168", "a169", "a170", "a171", "a172", "a173", "a174", "a175", "a176", "a177", "a178", "a179", "a180", "a181", "a182", "a183", "a184", "a185", "a186", "a187", "a188", "a189", "a190", "a191", "a192", "a193", "a194", "a195", "a196", "a197", "a198", "a199", "a200", "a201", "a202", "a203", "a204", "a205", "a206", "a207", "a208", "a209", "a210", "a211", "a212", "a213", "a214", "a215", "a216", "a217", "a218", "a219", "a220", "a221", "a222", "a223", "a224", "a225", "a226", "a227", "a228", "a229", "a230", "a231", "a232", "a233", "a234", "a235", "a236", "a237", "a238", "a239", "a240", "a241", "a242", "a243", "a244", "a245", "a246", "a247", "a248", "a249", "a250", "a251", "a252", "a253", "a254", "a255", "s36", "s37", "s38", "s39", "s40", "s41", "s42", "s43", "s44", "s45", "s46", "s47", "s48", "s49", "s50", "s51", "s52", "s53", "s54", "s55", "s56", "s57", "s58", "s59", "s60", "s61", "s62", "s63", "s64", "s65", "s66", "s67", "s68", "s69", "s70", "s71", "s72", "s73", "s74", "s75", "s76", "s77", "s78", "s79", "s80", "s81", "s82", "s83", "s84", "s85", "s86", "s87", "s88", "s89", "s90", "s91", "s92", "s93", "s94", "s95", "s96", "s97", "vcc", "scc", "memory");
    __builtin_unreachable();
  } else {
    if (kind != 6) {
      const unsigned sStep = 4;
      COMMON(kind == 1 ? 8 : 16, kind == 1 ? fj * 128 : fj < 8 ? 1024 + fj * 128 : (fj & 2) * 1024 + (fj - 8) * 128, 1024, 131072, 2048, 256, kind == 1 ? 0 : 32768);
#define SHM 1
      const int sr = 16 + l15 - SHM * ((wave & 1) ? -1 : 1) * (1 + (wave >> 1));
      const unsigned vAn = (sr >> 4) * 32768 + (lg * 16 + (sr & 15)) * 16, vAe = (sr >> 4) == 1 ? vAn : OOB;
      const unsigned vOutS = 0, vAux0 = l15 * 32, vAux1 = 139264 + (l15 * 2 + (lg & 1)) * 16 + wave * 4 + (lg >> 1) * 512, vAux2 = 139264 + (l15 * 2 + (lg & 1)) * 16;
      const unsigned vAux3 = (wave == 0 && lg < 2) ? slice * 128 + l15 * 8 + lg * 4 : OOB;
      const unsigned sOutOff = (kind == 1 ? OFF_HS : OFF_X1) + (slice * 4 + wave) * 1024, sFoutOff = (kind == 1 ? OFF_FH : OFF_FX1) + slice * 128;
      const unsigned sAuxOff = OFF_XP + (slice * 4 + wave) * 1024, sBuf = kind, sWw = 16 * wave * 2048 + slice * 256;
      const u16* w12 = kind == 1 ? w1 : w2;
      unsigned epOut;
      asm volatile(
R"(.macro M d,w,x
v_mfma_f32_16x16x32_bf16 v[\d:\d+3], \w, v[\x:\x+3], v[\d:\d+3]
.endm
.macro R d,a,o
ds_read_b128 v[\d:\d+3], v\a offset:\o
.endm
.macro T d,a,o
ds_read_b64_tr_b16 \d, v\a offset:\o
.endm
.macro D a,s,o
buffer_load_dwordx4 v\a, s[36:39], s\s offen offset:\o sc1 lds
.endm
.macro L c
s_waitcnt lgkmcnt(\c)
.endm
Lcode_g12_b:
s_cmp_eq_u32 %9,2
s_cbranch_scc1 Lg2_begin_%=
s_mov_b64 s[36:37],%1
s_mov_b32 s38,111243392
s_mov_b32 s39,0x00020000
s_mov_b32 s44,%2
s_mov_b32 s45,%3
s_mov_b32 s46,%4
s_mov_b32 s47,%5
s_mov_b32 s48,%6
s_mov_b32 s49,%7
s_mov_b32 s50,%8
s_mov_b32 s51,%9
s_mov_b32 s93,%10
v_mov_b32 v0,%11
v_mov_b32 v3,%12
v_mov_b32 v5,%13
v_mov_b32 v6,%14
v_mov_b32 v18,%15
v_mov_b32 v19,%16
v_mov_b32 v20,%17
v_mov_b32 v21,%18
v_mov_b32 v22,%19
v_mov_b32 v10,%20
v_mov_b32 v11,%21
v_mov_b32 v12,%22
v_mov_b32 v13,%23
v_mov_b32 v14,%24
v_mov_b32 v15,%25
v_mov_b32 v16,%26
s_mov_b64 s[40:41],%27
s_mov_b32 s42,2097152
s_mov_b32 s43,0x00020000
s_lshl_b32 s57,s44,11
s_mov_b32 s79,111243264
buffer_load_dword v4,off,s[36:39],s79 sc1
s_add_u32 s55,s50,0
s_add_u32 m0,s57,0
s_nop 0
buffer_load_dwordx4 v18,s[40:43],s55 offen offset:0  lds
s_add_u32 m0,s57,1024
s_nop 0
buffer_load_dwordx4 v19,s[40:43],s55 offen offset:0  lds
s_add_u32 m0,s57,8064
s_nop 0
buffer_load_dwordx4 v18,s[40:43],s55 offen offset:128  lds
s_add_u32 m0,s57,9088
s_nop 0
buffer_load_dwordx4 v19,s[40:43],s55 offen offset:128  lds
s_add_u32 s55,s50,131072
s_add_u32 m0,s57,16384
s_nop 0
buffer_load_dwordx4 v18,s[40:43],s55 offen offset:0  lds
s_add_u32 m0,s57,17408
s_nop 0
buffer_load_dwordx4 v19,s[40:43],s55 offen offset:0  lds
s_add_u32 m0,s57,24448
s_nop 0
buffer_load_dwordx4 v18,s[40:43],s55 offen offset:128  lds
s_add_u32 m0,s57,25472
s_nop 0
buffer_load_dwordx4 v19,s[40:43],s55 offen offset:128  lds
s_waitcnt vmcnt(8)
s_barrier
v_readfirstlane_b32 s53,v4
s_nop 3
s_add_u32 s53,s53,1
s_cmp_eq_u32 s53,0
s_cselect_b32 s53,1,s53
v_mov_b32 v4,s53
buffer_store_dword v4,v3,s[36:39],s93 offen offset:0 sc0 sc1
s_add_u32 s55,s50,262144
s_add_u32 m0,s57,32768
s_nop 0
buffer_load_dwordx4 v18,s[40:43],s55 offen offset:0  lds
s_add_u32 m0,s57,33792
s_nop 0
buffer_load_dwordx4 v19,s[40:43],s55 offen offset:0  lds
s_add_u32 m0,s57,40832
s_nop 0
buffer_load_dwordx4 v18,s[40:43],s55 offen offset:128  lds
s_add_u32 m0,s57,41856
s_nop 0
buffer_load_dwordx4 v19,s[40:43],s55 offen offset:128  lds
s_add_u32 s55,s50,393216
s_add_u32 m0,s57,49152
s_nop 0
buffer_load_dwordx4 v18,s[40:43],s55 offen offset:0  lds
s_add_u32 m0,s57,50176
s_nop 0
buffer_load_dwordx4 v19,s[40:43],s55 offen offset:0  lds
s_add_u32 m0,s57,57216
s_nop 0
buffer_load_dwordx4 v18,s[40:43],s55 offen offset:128  lds
s_add_u32 m0,s57,58240
s_nop 0
buffer_load_dwordx4 v19,s[40:43],s55 offen offset:128  lds
s_add_u32 s55,s50,524288
s_add_u32 m0,s57,65536
s_nop 0
buffer_load_dwordx4 v18,s[40:43],s55 offen offset:0  lds
s_add_u32 m0,s57,66560
s_nop 0
buffer_load_dwordx4 v19,s[40:43],s55 offen offset:0  lds
s_add_u32 m0,s57,73600
s_nop 0
buffer_load_dwordx4 v18,s[40:43],s55 offen offset:128  lds
s_add_u32 m0,s57,74624
s_nop 0
buffer_load_dwordx4 v19,s[40:43],s55 offen offset:128  lds
s_add_u32 s55,s50,655360
s_add_u32 m0,s57,81920
s_nop 0
buffer_load_dwordx4 v18,s[40:43],s55 offen offset:0  lds
s_add_u32 m0,s57,82944
s_nop 0
buffer_load_dwordx4 v19,s[40:43],s55 offen offset:0  lds
s_add_u32 m0,s57,89984
s_nop 0
buffer_load_dwordx4 v18,s[40:43],s55 offen offset:128  lds
s_add_u32 m0,s57,91008
s_nop 0
buffer_load_dwordx4 v19,s[40:43],s55 offen offset:128  lds
s_waitcnt vmcnt(21)
L 0
s_barrier
s_add_u32 s55,s50,786432
s_add_u32 m0,s57,98304
s_nop 0
buffer_load_dwordx4 v18,s[40:43],s55 offen offset:0  lds
s_add_u32 m0,s57,99328
s_nop 0
buffer_load_dwordx4 v19,s[40:43],s55 offen offset:0  lds
s_add_u32 m0,s57,106368
s_nop 0
buffer_load_dwordx4 v18,s[40:43],s55 offen offset:128  lds
s_add_u32 m0,s57,107392
s_nop 0
buffer_load_dwordx4 v19,s[40:43],s55 offen offset:128  lds
v_add_u32 v26,0,v20
v_add_u32 v27,0,v21
T a[0:1],26,0
T a[2:3],26,512
T a[4:5],27,0
T a[6:7],27,512
T a[8:9],26,4096
T a[10:11],26,4608
T a[12:13],27,4096
T a[14:15],27,4608
s_waitcnt vmcnt(21)
L 0
s_barrier
s_add_u32 s55,s50,917504
s_add_u32 m0,s57,0
s_nop 0
buffer_load_dwordx4 v18,s[40:43],s55 offen offset:0  lds
s_add_u32 m0,s57,1024
s_nop 0
buffer_load_dwordx4 v19,s[40:43],s55 offen offset:0  lds
s_add_u32 m0,s57,8064
s_nop 0
buffer_load_dwordx4 v18,s[40:43],s55 offen offset:128  lds
s_add_u32 m0,s57,9088
s_nop 0
buffer_load_dwordx4 v19,s[40:43],s55 offen offset:128  lds
v_add_u32 v26,16384,v20
v_add_u32 v27,16384,v21
T a[16:17],26,0
T a[18:19],26,512
T a[20:21],27,0
T a[22:23],27,512
T a[24:25],26,4096
T a[26:27],26,4608
T a[28:29],27,4096
T a[30:31],27,4608
s_waitcnt vmcnt(20)
L 0
s_barrier
s_add_u32 s55,s50,1048576
s_add_u32 m0,s57,16384
s_nop 0
buffer_load_dwordx4 v18,s[40:43],s55 offen offset:0  lds
s_add_u32 m0,s57,17408
s_nop 0
buffer_load_dwordx4 v19,s[40:43],s55 offen offset:0  lds
s_add_u32 m0,s57,24448
s_nop 0
buffer_load_dwordx4 v18,s[40:43],s55 offen offset:128  lds
s_add_u32 m0,s57,25472
s_nop 0
buffer_load_dwordx4 v19,s[40:43],s55 offen offset:128  lds
v_add_u32 v26,32768,v20
v_add_u32 v27,32768,v21
T a[32:33],26,0
T a[34:35],26,512
T a[36:37],27,0
T a[38:39],27,512
T a[40:41],26,4096
T a[42:43],26,4608
T a[44:45],27,4096
T a[46:47],27,4608
s_waitcnt vmcnt(20)
L 0
s_barrier
s_add_u32 s55,s50,1179648
s_add_u32 m0,s57,32768
s_nop 0
buffer_load_dwordx4 v18,s[40:43],s55 offen offset:0  lds
s_add_u32 m0,s57,33792
s_nop 0
buffer_load_dwordx4 v19,s[40:43],s55 offen offset:0  lds
s_add_u32 m0,s57,40832
s_nop 0
buffer_load_dwordx4 v18,s[40:43],s55 offen offset:128  lds
s_add_u32 m0,s57,41856
s_nop 0
buffer_load_dwordx4 v19,s[40:43],s55 offen offset:128  lds
v_add_u32 v26,49152,v20
v_add_u32 v27,49152,v21
T a[48:49],26,0
T a[50:51],26,512
T a[52:53],27,0
T a[54:55],27,512
T a[56:57],26,4096
T a[58:59],26,4608
T a[60:61],27,4096
T a[62:63],27,4608
s_waitcnt vmcnt(20)
L 0
s_barrier
s_add_u32 s55,s50,1310720
s_add_u32 m0,s57,49152
s_nop 0
buffer_load_dwordx4 v18,s[40:43],s55 offen offset:0  lds
s_add_u32 m0,s57,50176
s_nop 0
buffer_load_dwordx4 v19,s[40:43],s55 offen offset:0  lds
s_add_u32 m0,s57,57216
s_nop 0
buffer_load_dwordx4 v18,s[40:43],s55 offen offset:128  lds
s_add_u32 m0,s57,58240
s_nop 0
buffer_load_dwordx4 v19,s[40:43],s55 offen offset:128  lds
v_add_u32 v26,65536,v20
v_add_u32 v27,65536,v21
T a[64:65],26,0
T a[66:67],26,512
T a[68:69],27,0
T a[70:71],27,512
T a[72:73],26,4096
T a[74:75],26,4608
T a[76:77],27,4096
T a[78:79],27,4608
s_waitcnt vmcnt(20)
L 0
s_barrier
s_add_u32 s55,s50,1441792
s_add_u32 m0,s57,65536
s_nop 0
buffer_load_dwordx4 v18,s[40:43],s55 offen offset:0  lds
s_add_u32 m0,s57,66560
s_nop 0
buffer_load_dwordx4 v19,s[40:43],s55 offen offset:0  lds
s_add_u32 m0,s57,73600
s_nop 0
buffer_load_dwordx4 v18,s[40:43],s55 offen offset:128  lds
s_add_u32 m0,s57,74624
s_nop 0
buffer_load_dwordx4 v19,s[40:43],s55 offen offset:128  lds
v_add_u32 v26,81920,v20
v_add_u32 v27,81920,v21
T a[80:81],26,0
T a[82:83],26,512
T a[84:85],27,0
T a[86:87],27,512
T a[88:89],26,4096
T a[90:91],26,4608
T a[92:93],27,4096
T a[94:95],27,4608
s_waitcnt vmcnt(20)
L 0
s_barrier
s_add_u32 s55,s50,1572864
s_add_u32 m0,s57,81920
s_nop 0
buffer_load_dwordx4 v18,s[40:43],s55 offen offset:0  lds
s_add_u32 m0,s57,82944
s_nop 0
buffer_load_dwordx4 v19,s[40:43],s55 offen offset:0  lds
s_add_u32 m0,s57,89984
s_nop 0
buffer_load_dwordx4 v18,s[40:43],s55 offen offset:128  lds
s_add_u32 m0,s57,91008
s_nop 0
buffer_load_dwordx4 v19,s[40:43],s55 offen offset:128  lds
v_add_u32 v26,98304,v20
v_add_u32 v27,98304,v21
T a[96:97],26,0
T a[98:99],26,512
T a[100:101],27,0
T a[102:103],27,512
T a[104:105],26,4096
T a[106:107],26,4608
T a[108:109],27,4096
T a[110:111],27,4608
s_waitcnt vmcnt(20)
L 0
s_barrier
s_add_u32 s55,s50,1703936
s_add_u32 m0,s57,98304
s_nop 0
buffer_load_dwordx4 v18,s[40:43],s55 offen offset:0  lds
s_add_u32 m0,s57,99328
s_nop 0
buffer_load_dwordx4 v19,s[40:43],s55 offen offset:0  lds
s_add_u32 m0,s57,106368
s_nop 0
buffer_load_dwordx4 v18,s[40:43],s55 offen offset:128  lds
s_add_u32 m0,s57,107392
s_nop 0
buffer_load_dwordx4 v19,s[40:43],s55 offen offset:128  lds
v_add_u32 v26,0,v20
v_add_u32 v27,0,v21
T a[112:113],26,0
T a[114:115],26,512
T a[116:117],27,0
T a[118:119],27,512
T a[120:121],26,4096
T a[122:123],26,4608
T a[124:125],27,4096
T a[126:127],27,4608
s_waitcnt vmcnt(20)
L 0
s_barrier
s_add_u32 s55,s50,1835008
s_add_u32 m0,s57,0
s_nop 0
buffer_load_dwordx4 v18,s[40:43],s55 offen offset:0  lds
s_add_u32 m0,s57,1024
s_nop 0
buffer_load_dwordx4 v19,s[40:43],s55 offen offset:0  lds
s_add_u32 m0,s57,8064
s_nop 0
buffer_load_dwordx4 v18,s[40:43],s55 offen offset:128  lds
s_add_u32 m0,s57,9088
s_nop 0
buffer_load_dwordx4 v19,s[40:43],s55 offen offset:128  lds
v_add_u32 v26,16384,v20
v_add_u32 v27,16384,v21
T a[128:129],26,0
T a[130:131],26,512
T a[132:133],27,0
T a[134:135],27,512
T a[136:137],26,4096
T a[138:139],26,4608
T a[140:141],27,4096
T a[142:143],27,4608
s_waitcnt vmcnt(20)
L 0
s_barrier
s_add_u32 s55,s50,1966080
s_add_u32 m0,s57,16384
s_nop 0
buffer_load_dwordx4 v18,s[40:43],s55 offen offset:0  lds
s_add_u32 m0,s57,17408
s_nop 0
buffer_load_dwordx4 v19,s[40:43],s55 offen offset:0  lds
s_add_u32 m0,s57,24448
s_nop 0
buffer_load_dwordx4 v18,s[40:43],s55 offen offset:128  lds
s_add_u32 m0,s57,25472
s_nop 0
buffer_load_dwordx4 v19,s[40:43],s55 offen offset:128  lds
v_add_u32 v26,32768,v20
v_add_u32 v27,32768,v21
T a[144:145],26,0
T a[146:147],26,512
T a[148:149],27,0
T a[150:151],27,512
T a[152:153],26,4096
T a[154:155],26,4608
T a[156:157],27,4096
T a[158:159],27,4608
s_waitcnt vmcnt(20)
L 0
s_barrier
v_add_u32 v26,49152,v20
v_add_u32 v27,49152,v21
T a[160:161],26,0
T a[162:163],26,512
T a[164:165],27,0
T a[166:167],27,512
T a[168:169],26,4096
T a[170:171],26,4608
T a[172:173],27,4096
T a[174:175],27,4608
s_waitcnt vmcnt(16)
L 0
s_barrier
v_add_u32 v26,65536,v20
v_add_u32 v27,65536,v21
T a[176:177],26,0
T a[178:179],26,512
T a[180:181],27,0
T a[182:183],27,512
T a[184:185],26,4096
T a[186:187],26,4608
T a[188:189],27,4096
T a[190:191],27,4608
s_waitcnt vmcnt(12)
L 0
s_barrier
v_add_u32 v26,81920,v20
v_add_u32 v27,81920,v21
T a[192:193],26,0
T a[194:195],26,512
T a[196:197],27,0
T a[198:199],27,512
T a[200:201],26,4096
T a[202:203],26,4608
T a[204:205],27,4096
T a[206:207],27,4608
s_waitcnt vmcnt(8)
L 0
s_barrier
v_add_u32 v26,98304,v20
v_add_u32 v27,98304,v21
T a[208:209],26,0
T a[210:211],26,512
T a[212:213],27,0
T a[214:215],27,512
T a[216:217],26,4096
T a[218:219],26,4608
T a[220:221],27,4096
T a[222:223],27,4608
s_waitcnt vmcnt(4)
L 0
s_barrier
v_add_u32 v26,0,v20
v_add_u32 v27,0,v21
T a[224:225],26,0
T a[226:227],26,512
T a[228:229],27,0
T a[230:231],27,512
T a[232:233],26,4096
T a[234:235],26,4608
T a[236:237],27,4096
T a[238:239],27,4608
s_waitcnt vmcnt(0)
L 0
s_barrier
v_add_u32 v26,16384,v20
v_add_u32 v27,16384,v21
T a[240:241],26,0
T a[242:243],26,512
T a[244:245],27,0
T a[246:247],27,512
T a[248:249],26,4096
T a[250:251],26,4608
T a[252:253],27,4096
T a[254:255],27,4608
L 0
s_barrier
v_mov_b32 v34,0x3f803f80
v_mov_b32 v35,0x3f803f80
v_mov_b32 v36,0x3f803f80
v_mov_b32 v37,0x3f803f80
s_nop 1
v_mfma_f32_16x16x32_bf16 v[74:77],a[0:3],v[34:37],0
v_mfma_f32_16x16x32_bf16 v[78:81],a[4:7],v[34:37],0
M 74,a[8:11],34
M 78,a[12:15],34
M 74,a[16:19],34
M 78,a[20:23],34
M 74,a[24:27],34
M 78,a[28:31],34
M 74,a[32:35],34
M 78,a[36:39],34
M 74,a[40:43],34
M 78,a[44:47],34
M 74,a[48:51],34
M 78,a[52:55],34
M 74,a[56:59],34
M 78,a[60:63],34
)"
R"(M 74,a[64:67],34
M 78,a[68:71],34
M 74,a[72:75],34
M 78,a[76:79],34
M 74,a[80:83],34
M 78,a[84:87],34
M 74,a[88:91],34
M 78,a[92:95],34
M 74,a[96:99],34
M 78,a[100:103],34
M 74,a[104:107],34
M 78,a[108:111],34
M 74,a[112:115],34
M 78,a[116:119],34
M 74,a[120:123],34
M 78,a[124:127],34
M 74,a[128:131],34
M 78,a[132:135],34
M 74,a[136:139],34
M 78,a[140:143],34
M 74,a[144:147],34
M 78,a[148:151],34
M 74,a[152:155],34
M 78,a[156:159],34
M 74,a[160:163],34
M 78,a[164:167],34
M 74,a[168:171],34
M 78,a[172:175],34
M 74,a[176:179],34
M 78,a[180:183],34
M 74,a[184:187],34
M 78,a[188:191],34
M 74,a[192:195],34
M 78,a[196:199],34
M 74,a[200:203],34
M 78,a[204:207],34
M 74,a[208:211],34
M 78,a[212:215],34
M 74,a[216:219],34
M 78,a[220:223],34
M 74,a[224:227],34
M 78,a[228:231],34
M 74,a[232:235],34
M 78,a[236:239],34
M 74,a[240:243],34
M 78,a[244:247],34
M 74,a[248:251],34
M 78,a[252:255],34
v_mov_b32 v4,s53
s_mov_b32 s82,0
s_mov_b32 s80,0
s_mov_b32 s54,s45
s_mul_i32 s57,s44,8192
s_mul_i32 s55,s45,32768
s_add_u32 s55,s55,s57
s_add_u32 s55,s55,12582912
s_mul_i32 s60,s46,32768
s_mul_i32 s61,s46,1024
s_mul_i32 s62,s46,32768
s_mul_i32 s63,s46,1024
s_mul_i32 s64,s46,512
s_lshl_b32 s59,s44,8
s_add_u32 s59,s59,131072
s_mul_i32 s58,s45,1024
s_add_u32 s58,s58,109056000
s_mul_i32 s65,s45,32768
s_add_u32 s65,s65,s47
s_mul_i32 s68,s45,1024
s_add_u32 s68,s68,s48
s_mul_i32 s73,s45,512
s_add_u32 s73,s73,106954752
s_mul_i32 s74,s45,32768
s_add_u32 s74,s74,s49
s_mul_i32 s75,s45,1024
s_add_u32 s75,s75,107479040
s_mov_b32 s66,0x7fff0000
s_mov_b32 s67,0x7fff0000
s_mov_b32 s69,0x7fff0000
s_mov_b32 s76,0x7fff0000
s_mov_b32 s77,0x7fff0000
s_mov_b32 s70,0x7fff0000
s_mov_b32 s71,0x7fff0000
s_mov_b32 s72,0x7fff0000
v_mov_b32 v1,v0
v_add_u32 v2,32768,v0
s_mov_b32 s84,32768
s_mov_b32 s86,0x3a800000
s_mov_b32 s87,0x3727c5ac
s_mov_b32 s88,0xc0135761
s_mov_b32 s89,0x3d372713
s_and_b32 s85,s44,1
s_mul_i32 s85,s85,127
v_lshlrev_b32 v8,22,v0
v_and_b32 v8,0x80000000,v8
v_or_b32 v8,v8,v5
s_add_u32 m0,s59,4096
s_nop 0
buffer_load_dword v5,s[36:39],s58 offen offset:0 sc1 lds
buffer_store_dword v4,v3,s[36:39],s71 offen offset:0 sc0 sc1
s_add_u32 m0,s59,5120
s_nop 0
buffer_load_dword v5,s[36:39],s58 offen offset:0 sc1 lds
buffer_store_dword v4,v3,s[36:39],s71 offen offset:0 sc0 sc1
s_mov_b32 s83,0
Lg1_poll_%=:
s_add_u32 s78,s83,2
s_and_b32 s78,s78,3
s_lshl_b32 s78,s78,10
s_add_u32 s78,s78,4096
s_add_u32 m0,s59,s78
s_nop 0
buffer_load_dword v5,s[36:39],s58 offen offset:0 sc1 lds
s_and_b32 s78,s83,3
s_lshl_b32 s78,s78,10
v_add_u32 v82,s78,v6
s_waitcnt vmcnt(4)
s_barrier
ds_read_b32 v7,v82 offset:4096
buffer_store_dword v4,v3,s[36:39],s70 offen offset:0 sc0 sc1
s_mov_b32 s70,s71
s_mov_b32 s71,s72
s_mov_b32 s72,0x7fff0000
s_add_u32 s83,s83,1
s_add_u32 s82,s82,1
L 0
v_cmp_eq_u32 vcc,s53,v7
s_nop 0
s_cmp_eq_u32 vcc_lo,-1
s_cbranch_scc1 Lg1_go_%=
s_cmp_lt_u32 s82,20000
s_cbranch_scc1 Lg1_poll_%=
Lg1_go_%=:
s_mov_b32 m0,s57
s_add_u32 s56,s55,4096
D 0,55,0
D 0,55,1024
D 0,55,2048
D 0,55,3072
s_add_u32 m0,s57,4096
s_nop 0
D 0,56,0
D 0,56,1024
D 0,56,2048
D 0,56,3072
s_add_u32 s55,s55,s60
s_add_u32 s57,s57,32768
s_and_b32 s57,s57,131071
s_add_u32 s54,s54,s46
Lg1_gox_%=:
s_add_u32 s58,s58,s61
s_cmp_eq_u32 vcc_hi,-1
s_cbranch_scc1 Lg1_go2_%=
s_add_u32 m0,s59,2048
s_nop 0
buffer_load_dword v5,s[36:39],s58 offen offset:0 sc1 lds
s_waitcnt vmcnt(0)
s_barrier
buffer_store_dword v4,v3,s[36:39],s70 offen offset:0 sc0 sc1
s_min_u32 s71,s71,s72
buffer_store_dword v4,v3,s[36:39],s71 offen offset:0 sc0 sc1
s_mov_b32 s70,0x7fff0000
s_mov_b32 s71,0x7fff0000
s_mov_b32 s72,0x7fff0000
s_cmp_eq_u32 s80,1
s_cbranch_scc1 Lg1_st11_%=
Lg1_st10_%=:
ds_read_b32 v7,v6 offset:2048
R 34,1,0
R 38,1,1024
R 42,1,2048
R 46,1,3072
R 50,1,4096
R 54,1,5120
R 58,1,6144
L 7
v_cmp_eq_u32 vcc,s53,v7
L 5
s_cmp_eq_u64 vcc,-1
s_cbranch_scc1 Lg1_L20_%=
s_cmp_eq_u32 vcc_lo,-1
s_cbranch_scc1 Lg1_L10_%=
s_branch Lg1_X0_%=
Lg1_st11_%=:
ds_read_b32 v7,v6 offset:2048
R 34,2,0
R 38,2,1024
R 42,2,2048
R 46,2,3072
R 50,2,4096
R 54,2,5120
R 58,2,6144
L 7
v_cmp_eq_u32 vcc,s53,v7
L 5
s_cmp_eq_u64 vcc,-1
s_cbranch_scc1 Lg1_L21_%=
s_cmp_eq_u32 vcc_lo,-1
s_cbranch_scc1 Lg1_L11_%=
s_branch Lg1_X1_%=
Lg1_go2_%=:
s_add_u32 s58,s58,s61
s_add_u32 m0,s59,2048
s_nop 0
buffer_load_dword v5,s[36:39],s58 offen offset:0 sc1 lds
s_mov_b32 m0,s57
s_add_u32 s56,s55,4096
D 0,55,0
D 0,55,1024
D 0,55,2048
D 0,55,3072
s_add_u32 m0,s57,4096
s_nop 0
D 0,56,0
D 0,56,1024
D 0,56,2048
D 0,56,3072
s_add_u32 s55,s55,s60
s_add_u32 s57,s57,32768
s_and_b32 s57,s57,131071
s_add_u32 s54,s54,s46
s_waitcnt vmcnt(8)
s_barrier
buffer_store_dword v4,v3,s[36:39],s70 offen offset:0 sc0 sc1
s_min_u32 s71,s71,s72
buffer_store_dword v4,v3,s[36:39],s71 offen offset:0 sc0 sc1
s_mov_b32 s70,0x7fff0000
s_mov_b32 s71,0x7fff0000
s_mov_b32 s72,0x7fff0000
s_cmp_eq_u32 s80,1
s_cbranch_scc1 Lg1_st21_%=
Lg1_st20_%=:
ds_read_b32 v7,v6 offset:2048
R 34,1,0
R 38,1,1024
R 42,1,2048
R 46,1,3072
R 50,1,4096
R 54,1,5120
R 58,1,6144
L 7
v_cmp_eq_u32 vcc,s53,v7
L 5
s_cmp_eq_u32 vcc_lo,-1
s_cbranch_scc1 Lg1_S0_%=
s_branch Lg1_N0_%=
Lg1_st21_%=:
ds_read_b32 v7,v6 offset:2048
R 34,2,0
R 38,2,1024
R 42,2,2048
R 46,2,3072
R 50,2,4096
R 54,2,5120
R 58,2,6144
L 7
v_cmp_eq_u32 vcc,s53,v7
L 5
s_cmp_eq_u32 vcc_lo,-1
s_cbranch_scc1 Lg1_S1_%=
s_branch Lg1_N1_%=
Lg1_L20_%=:
v_mfma_f32_16x16x32_bf16 v[18:21],a[0:3],v[34:37],0
s_add_u32 s58,s58,s61
s_add_u32 s58,s58,s61
R 62,1,7168
v_mfma_f32_16x16x32_bf16 v[22:25],a[4:7],v[34:37],0
s_add_u32 m0,s59,0
buffer_load_dwordx4 v[66:69],v11,s[36:39],s73 offen offset:0 sc1
M 18,a[8:11],38
s_add_u32 s73,s73,s64
R 34,1,8192
v_fma_f32 v26,-v70,v74,v26
M 22,a[12:15],38
L 5
buffer_load_dword v8,s[36:39],s58 offen offset:0 sc1 lds
M 18,a[16:19],42
s_mov_b32 m0,s57
R 38,1,9216
v_fma_f32 v27,-v70,v75,v27
M 22,a[20:23],42
s_add_u32 s56,s55,4096
v_fma_f32 v28,-v70,v76,v28
v_fma_f32 v29,-v70,v77,v29
M 18,a[24:27],46
s_mov_b32 s66,s65
R 42,1,10240
v_mul_f32 v82,v26,v26
M 22,a[28:31],46
L 5
D 0,55,0
M 18,a[32:35],50
R 46,1,11264
v_mul_f32 v83,v27,v27
M 22,a[36:39],50
v_mul_f32 v84,v28,v28
v_mul_f32 v85,v29,v29
M 18,a[40:43],54
R 50,1,12288
v_fma_f32 v82,v72,v82,v71
M 22,a[44:47],54
L 5
D 0,55,1024
M 18,a[48:51],58
R 54,1,13312
v_fma_f32 v83,v72,v83,v71
M 22,a[52:55],58
v_fma_f32 v84,v72,v84,v71
v_fma_f32 v85,v72,v85,v71
M 18,a[56:59],62
R 58,1,14336
v_mul_f32 v82,v26,v82
M 22,a[60:63],62
L 5
D 0,55,2048
M 18,a[64:67],34
R 62,1,15360
v_mul_f32 v83,v27,v83
M 22,a[68:71],34
v_exp_f32 v82,v82
M 18,a[72:75],38
R 34,1,16384
v_mul_f32 v84,v28,v84
M 22,a[76:79],38
L 5
D 0,55,3072
M 18,a[80:83],42
s_add_u32 m0,s57,4096
R 38,1,17408
v_mul_f32 v85,v29,v85
M 22,a[84:87],42
s_add_u32 s55,s55,s60
v_exp_f32 v83,v83
M 18,a[88:91],46
s_add_u32 s57,s57,32768
R 42,1,18432
v_fma_f32 v82,v82,v73,v73
M 22,a[92:95],46
L 5
D 0,56,0
M 18,a[96:99],50
s_and_b32 s57,s57,131071
R 46,1,19456
v_fma_f32 v83,v83,v73,v73
M 22,a[100:103],50
s_add_u32 s54,s54,s46
v_exp_f32 v84,v84
M 18,a[104:107],54
R 50,1,20480
v_fma_f32 v84,v84,v73,v73
M 22,a[108:111],54
L 5
D 0,56,1024
M 18,a[112:115],58
R 54,1,21504
v_fma_f32 v30,-v70,v78,v30
M 22,a[116:119],58
v_exp_f32 v85,v85
M 18,a[120:123],62
R 58,1,22528
v_fma_f32 v85,v85,v73,v73
M 22,a[124:127],62
L 5
D 0,56,2048
M 18,a[128:131],34
R 62,1,23552
v_fma_f32 v31,-v70,v79,v31
M 22,a[132:135],34
v_rcp_f32 v82,v82
M 18,a[136:139],38
R 34,1,24576
v_mul_f32 v26,v26,v82
M 22,a[140:143],38
L 5
D 0,56,3072
M 18,a[144:147],42
s_mov_b32 m0,s57
R 38,1,25600
v_fma_f32 v32,-v70,v80,v32
M 22,a[148:151],42
s_add_u32 s56,s55,4096
v_rcp_f32 v83,v83
M 18,a[152:155],46
R 42,1,26624
v_mul_f32 v27,v27,v83
M 22,a[156:159],46
L 5
D 0,55,0
M 18,a[160:163],50
R 46,1,27648
v_cvt_pk_bf16_f32 v90,v26,v27
M 22,a[164:167],50
D 0,55,1024
M 18,a[168:171],54
R 50,1,28672
v_fma_f32 v33,-v70,v81,v33
M 22,a[172:175],54
L 5
v_rcp_f32 v84,v84
M 18,a[176:179],58
R 54,1,29696
v_mul_f32 v28,v28,v84
M 22,a[180:183],58
D 0,55,2048
M 18,a[184:187],62
R 58,1,30720
v_mul_f32 v86,v30,v30
M 22,a[188:191],62
L 5
D 0,55,3072
M 18,a[192:195],34
s_waitcnt vmcnt(4)
R 62,1,31744
v_mul_f32 v87,v31,v31
M 22,a[196:199],34
s_barrier
ds_read_b32 v7,v6 offset:0
v_mul_f32 v88,v32,v32
M 18,a[200:203],38
s_add_u32 m0,s57,4096
R 34,2,0
v_mul_f32 v89,v33,v33
M 22,a[204:207],38
L 6
buffer_store_dword v4,v3,s[36:39],s70 offen offset:0 sc0 sc1
M 18,a[208:211],42
s_add_u32 s55,s55,s60
R 38,2,1024
v_fma_f32 v86,v72,v86,v71
M 22,a[212:215],42
s_add_u32 s57,s57,32768
D 0,56,0
M 18,a[216:219],46
s_and_b32 s57,s57,131071
R 42,2,2048
v_fma_f32 v87,v72,v87,v71
M 22,a[220:223],46
L 6
D 0,56,1024
M 18,a[224:227],50
s_add_u32 s54,s54,s46
R 46,2,3072
v_fma_f32 v88,v72,v88,v71
M 22,a[228:231],50
s_mov_b32 s70,s69
D 0,56,2048
M 18,a[232:235],54
s_add_u32 s65,s65,s62
R 50,2,4096
v_fma_f32 v89,v72,v89,v71
M 22,a[236:239],54
L 6
D 0,56,3072
M 18,a[240:243],58
s_mov_b32 s69,s68
R 54,2,5120
v_mul_f32 v86,v30,v86
v_mul_f32 v87,v31,v87
v_mul_f32 v88,v32,v88
v_mul_f32 v89,v33,v89
v_exp_f32 v86,v86
v_exp_f32 v87,v87
v_exp_f32 v88,v88
v_exp_f32 v89,v89
M 22,a[244:247],58
L 6
v_rcp_f32 v85,v85
v_fma_f32 v86,v86,v73,v73
v_fma_f32 v87,v87,v73,v73
v_fma_f32 v88,v88,v73,v73
v_fma_f32 v89,v89,v73,v73
v_rcp_f32 v86,v86
v_rcp_f32 v87,v87
v_rcp_f32 v88,v88
v_rcp_f32 v89,v89
v_cmp_eq_u32 vcc,s53,v7
M 18,a[248:251],62
s_add_u32 s68,s68,s63
s_cmp_eq_u32 vcc_lo,-1
R 58,2,6144
v_mul_f32 v29,v29,v85
v_cvt_pk_bf16_f32 v91,v28,v29
v_mul_f32 v30,v30,v86
v_mul_f32 v31,v31,v87
v_mul_f32 v32,v32,v88
v_mul_f32 v33,v33,v89
v_cvt_pk_bf16_f32 v92,v30,v31
v_cvt_pk_bf16_f32 v93,v32,v33
M 22,a[252:255],62
L 5
buffer_store_dwordx4 v[90:93],v0,s[36:39],s67 offen offset:0 sc0 sc1
v_xor_b32 v1,65536,v1
s_cbranch_scc0 Lg1_N1_%=
Lg1_S1_%=:
v_mfma_f32_16x16x32_bf16 v[26:29],a[0:3],v[34:37],0
s_add_u32 s58,s58,s61
s_add_u32 m0,s59,1024
R 62,2,7168
v_mfma_f32_16x16x32_bf16 v[30:33],a[4:7],v[34:37],0
s_mov_b32 s67,s65
buffer_load_dword v8,s[36:39],s58 offen offset:0 sc1 lds
)"
R"(M 26,a[8:11],38
s_mov_b32 m0,s57
R 34,2,8192
v_fma_f32 v18,-v66,v74,v18
M 30,a[12:15],38
L 5
buffer_load_dwordx4 v[70:73],v11,s[36:39],s73 offen offset:0 sc1
M 26,a[16:19],42
s_add_u32 s73,s73,s64
R 38,2,9216
v_fma_f32 v19,-v66,v75,v19
v_fma_f32 v20,-v66,v76,v20
M 30,a[20:23],42
s_add_u32 s56,s55,4096
v_fma_f32 v21,-v66,v77,v21
v_mul_f32 v82,v18,v18
M 26,a[24:27],46
R 42,2,10240
v_mul_f32 v83,v19,v19
M 30,a[28:31],46
L 5
D 0,55,0
M 26,a[32:35],50
R 46,2,11264
v_mul_f32 v84,v20,v20
M 30,a[36:39],50
v_mul_f32 v85,v21,v21
v_fma_f32 v82,v68,v82,v67
v_fma_f32 v83,v68,v83,v67
M 26,a[40:43],54
R 50,2,12288
v_fma_f32 v84,v68,v84,v67
M 30,a[44:47],54
L 5
v_fma_f32 v85,v68,v85,v67
v_mul_f32 v82,v18,v82
M 26,a[48:51],58
R 54,2,13312
v_mul_f32 v83,v19,v83
M 30,a[52:55],58
D 0,55,1024
M 26,a[56:59],62
R 58,2,14336
v_exp_f32 v82,v82
M 30,a[60:63],62
L 5
v_exp_f32 v83,v83
M 26,a[64:67],34
R 62,2,15360
v_mul_f32 v84,v20,v84
M 30,a[68:71],34
v_exp_f32 v84,v84
M 26,a[72:75],38
R 34,2,16384
v_mul_f32 v85,v21,v85
M 30,a[76:79],38
L 5
v_exp_f32 v85,v85
v_fma_f32 v82,v82,v69,v69
M 26,a[80:83],42
R 38,2,17408
v_fma_f32 v83,v83,v69,v69
M 30,a[84:87],42
D 0,55,2048
M 26,a[88:91],46
R 42,2,18432
v_fma_f32 v84,v84,v69,v69
M 30,a[92:95],46
L 5
v_rcp_f32 v82,v82
M 26,a[96:99],50
R 46,2,19456
v_rcp_f32 v83,v83
M 30,a[100:103],50
v_rcp_f32 v84,v84
M 26,a[104:107],54
R 50,2,20480
v_fma_f32 v85,v85,v69,v69
M 30,a[108:111],54
L 5
D 0,55,3072
M 26,a[112:115],58
s_add_u32 m0,s57,4096
R 54,2,21504
v_mul_f32 v18,v18,v82
M 30,a[116:119],58
s_add_u32 s55,s55,s60
v_rcp_f32 v85,v85
v_mul_f32 v19,v19,v83
M 26,a[120:123],62
s_add_u32 s57,s57,32768
R 58,2,22528
v_mul_f32 v20,v20,v84
M 30,a[124:127],62
L 5
v_mul_f32 v21,v21,v85
v_cvt_pk_bf16_f32 v90,v18,v19
M 26,a[128:131],34
s_and_b32 s57,s57,131071
R 62,2,23552
v_cvt_pk_bf16_f32 v91,v20,v21
M 30,a[132:135],34
s_add_u32 s54,s54,s46
v_fma_f32 v22,-v66,v78,v22
v_fma_f32 v23,-v66,v79,v23
M 26,a[136:139],38
s_add_u32 s65,s65,s62
R 34,2,24576
v_fma_f32 v24,-v66,v80,v24
v_fma_f32 v25,-v66,v81,v25
M 30,a[140:143],38
L 5
D 0,56,0
M 26,a[144:147],42
R 38,2,25600
v_mul_f32 v86,v22,v22
M 30,a[148:151],42
v_mul_f32 v87,v23,v23
v_mul_f32 v88,v24,v24
M 26,a[152:155],46
R 42,2,26624
v_mul_f32 v89,v25,v25
M 30,a[156:159],46
L 5
v_fma_f32 v86,v68,v86,v67
v_fma_f32 v87,v68,v87,v67
v_fma_f32 v88,v68,v88,v67
M 26,a[160:163],50
R 46,2,27648
v_fma_f32 v89,v68,v89,v67
M 30,a[164:167],50
D 0,56,1024
M 26,a[168:171],54
R 50,2,28672
v_mul_f32 v86,v22,v86
M 30,a[172:175],54
L 5
v_exp_f32 v86,v86
M 26,a[176:179],58
R 54,2,29696
v_mul_f32 v87,v23,v87
v_mul_f32 v88,v24,v88
M 30,a[180:183],58
v_exp_f32 v87,v87
M 26,a[184:187],62
R 58,2,30720
v_mul_f32 v89,v25,v89
M 30,a[188:191],62
L 5
v_exp_f32 v88,v88
M 26,a[192:195],34
s_waitcnt vmcnt(6)
R 62,2,31744
v_fma_f32 v86,v86,v69,v69
M 30,a[196:199],34
s_barrier
ds_read_b32 v7,v6 offset:1024
v_exp_f32 v89,v89
M 26,a[200:203],38
R 34,1,0
v_fma_f32 v87,v87,v69,v69
M 30,a[204:207],38
L 6
buffer_store_dword v4,v3,s[36:39],s70 offen offset:0 sc0 sc1
M 26,a[208:211],42
s_mov_b32 s70,s69
R 38,1,1024
v_fma_f32 v88,v88,v69,v69
M 30,a[212:215],42
s_mov_b32 s69,s68
D 0,56,2048
M 26,a[216:219],46
L 2
R 42,1,2048
v_rcp_f32 v86,v86
M 30,a[220:223],46
s_nop 0
D 0,56,3072
M 26,a[224:227],50
s_add_u32 s68,s68,s63
R 46,1,3072
v_fma_f32 v89,v89,v69,v69
M 30,a[228:231],50
v_rcp_f32 v87,v87
M 26,a[232:235],54
R 50,1,4096
v_mul_f32 v22,v22,v86
M 30,a[236:239],54
s_nop 0
v_rcp_f32 v88,v88
v_mul_f32 v23,v23,v87
M 26,a[240:243],58
R 54,1,5120
v_mul_f32 v24,v24,v88
M 30,a[244:247],58
v_rcp_f32 v89,v89
v_cmp_eq_u32 vcc,s53,v7
M 26,a[248:251],62
s_cmp_eq_u32 vcc_lo,-1
R 58,1,6144
v_mul_f32 v25,v25,v89
v_cvt_pk_bf16_f32 v92,v22,v23
v_cvt_pk_bf16_f32 v93,v24,v25
M 30,a[252:255],62
L 5
buffer_store_dwordx4 v[90:93],v0,s[36:39],s66 offen offset:0 sc0 sc1
v_xor_b32 v2,65536,v2
s_cbranch_scc0 Lg1_N0_%=
Lg1_S0_%=:
v_mfma_f32_16x16x32_bf16 v[18:21],a[0:3],v[34:37],0
s_add_u32 s58,s58,s61
s_add_u32 m0,s59,0
R 62,1,7168
v_mfma_f32_16x16x32_bf16 v[22:25],a[4:7],v[34:37],0
s_mov_b32 s66,s65
buffer_load_dword v8,s[36:39],s58 offen offset:0 sc1 lds
M 18,a[8:11],38
s_mov_b32 m0,s57
R 34,1,8192
v_fma_f32 v26,-v70,v74,v26
M 22,a[12:15],38
L 5
buffer_load_dwordx4 v[66:69],v11,s[36:39],s73 offen offset:0 sc1
M 18,a[16:19],42
s_add_u32 s73,s73,s64
R 38,1,9216
v_fma_f32 v27,-v70,v75,v27
v_fma_f32 v28,-v70,v76,v28
M 22,a[20:23],42
s_add_u32 s56,s55,4096
v_fma_f32 v29,-v70,v77,v29
v_mul_f32 v82,v26,v26
M 18,a[24:27],46
R 42,1,10240
v_mul_f32 v83,v27,v27
M 22,a[28:31],46
L 5
D 0,55,0
M 18,a[32:35],50
R 46,1,11264
v_mul_f32 v84,v28,v28
M 22,a[36:39],50
v_mul_f32 v85,v29,v29
v_fma_f32 v82,v72,v82,v71
v_fma_f32 v83,v72,v83,v71
M 18,a[40:43],54
R 50,1,12288
v_fma_f32 v84,v72,v84,v71
M 22,a[44:47],54
L 5
v_fma_f32 v85,v72,v85,v71
v_mul_f32 v82,v26,v82
M 18,a[48:51],58
R 54,1,13312
v_mul_f32 v83,v27,v83
M 22,a[52:55],58
D 0,55,1024
M 18,a[56:59],62
R 58,1,14336
v_exp_f32 v82,v82
M 22,a[60:63],62
L 5
v_exp_f32 v83,v83
M 18,a[64:67],34
R 62,1,15360
v_mul_f32 v84,v28,v84
M 22,a[68:71],34
v_exp_f32 v84,v84
M 18,a[72:75],38
R 34,1,16384
v_mul_f32 v85,v29,v85
M 22,a[76:79],38
L 5
v_exp_f32 v85,v85
v_fma_f32 v82,v82,v73,v73
M 18,a[80:83],42
R 38,1,17408
v_fma_f32 v83,v83,v73,v73
M 22,a[84:87],42
D 0,55,2048
M 18,a[88:91],46
R 42,1,18432
v_fma_f32 v84,v84,v73,v73
M 22,a[92:95],46
L 5
v_rcp_f32 v82,v82
M 18,a[96:99],50
R 46,1,19456
v_rcp_f32 v83,v83
M 22,a[100:103],50
v_rcp_f32 v84,v84
M 18,a[104:107],54
R 50,1,20480
v_fma_f32 v85,v85,v73,v73
M 22,a[108:111],54
L 5
D 0,55,3072
M 18,a[112:115],58
s_add_u32 m0,s57,4096
R 54,1,21504
v_mul_f32 v26,v26,v82
M 22,a[116:119],58
s_add_u32 s55,s55,s60
v_rcp_f32 v85,v85
v_mul_f32 v27,v27,v83
M 18,a[120:123],62
s_add_u32 s57,s57,32768
R 58,1,22528
v_mul_f32 v28,v28,v84
M 22,a[124:127],62
L 5
v_mul_f32 v29,v29,v85
v_cvt_pk_bf16_f32 v90,v26,v27
M 18,a[128:131],34
s_and_b32 s57,s57,131071
R 62,1,23552
v_cvt_pk_bf16_f32 v91,v28,v29
M 22,a[132:135],34
s_add_u32 s54,s54,s46
v_fma_f32 v30,-v70,v78,v30
v_fma_f32 v31,-v70,v79,v31
M 18,a[136:139],38
s_add_u32 s65,s65,s62
R 34,1,24576
v_fma_f32 v32,-v70,v80,v32
v_fma_f32 v33,-v70,v81,v33
M 22,a[140:143],38
L 5
D 0,56,0
M 18,a[144:147],42
R 38,1,25600
v_mul_f32 v86,v30,v30
M 22,a[148:151],42
v_mul_f32 v87,v31,v31
v_mul_f32 v88,v32,v32
M 18,a[152:155],46
R 42,1,26624
v_mul_f32 v89,v33,v33
M 22,a[156:159],46
L 5
v_fma_f32 v86,v72,v86,v71
v_fma_f32 v87,v72,v87,v71
v_fma_f32 v88,v72,v88,v71
M 18,a[160:163],50
R 46,1,27648
v_fma_f32 v89,v72,v89,v71
M 22,a[164:167],50
D 0,56,1024
M 18,a[168:171],54
R 50,1,28672
v_mul_f32 v86,v30,v86
M 22,a[172:175],54
L 5
v_exp_f32 v86,v86
M 18,a[176:179],58
R 54,1,29696
v_mul_f32 v87,v31,v87
v_mul_f32 v88,v32,v88
M 22,a[180:183],58
v_exp_f32 v87,v87
M 18,a[184:187],62
R 58,1,30720
v_mul_f32 v89,v33,v89
M 22,a[188:191],62
L 5
v_exp_f32 v88,v88
M 18,a[192:195],34
s_waitcnt vmcnt(6)
R 62,1,31744
v_fma_f32 v86,v86,v73,v73
M 22,a[196:199],34
s_barrier
ds_read_b32 v7,v6 offset:0
v_exp_f32 v89,v89
M 18,a[200:203],38
R 34,2,0
v_fma_f32 v87,v87,v73,v73
M 22,a[204:207],38
L 6
buffer_store_dword v4,v3,s[36:39],s70 offen offset:0 sc0 sc1
M 18,a[208:211],42
s_mov_b32 s70,s69
R 38,2,1024
v_fma_f32 v88,v88,v73,v73
M 22,a[212:215],42
s_mov_b32 s69,s68
D 0,56,2048
M 18,a[216:219],46
L 2
R 42,2,2048
v_rcp_f32 v86,v86
M 22,a[220:223],46
s_nop 0
D 0,56,3072
M 18,a[224:227],50
s_add_u32 s68,s68,s63
R 46,2,3072
v_fma_f32 v89,v89,v73,v73
M 22,a[228:231],50
v_rcp_f32 v87,v87
M 18,a[232:235],54
R 50,2,4096
v_mul_f32 v30,v30,v86
M 22,a[236:239],54
s_nop 0
v_rcp_f32 v88,v88
v_mul_f32 v31,v31,v87
M 18,a[240:243],58
R 54,2,5120
v_mul_f32 v32,v32,v88
M 22,a[244:247],58
v_rcp_f32 v89,v89
v_cmp_eq_u32 vcc,s53,v7
M 18,a[248:251],62
s_cmp_eq_u32 vcc_lo,-1
R 58,2,6144
v_mul_f32 v33,v33,v89
v_cvt_pk_bf16_f32 v92,v30,v31
v_cvt_pk_bf16_f32 v93,v32,v33
M 22,a[252:255],62
L 5
buffer_store_dwordx4 v[90:93],v0,s[36:39],s67 offen offset:0 sc0 sc1
v_xor_b32 v1,65536,v1
s_cbranch_scc1 Lg1_S1_%=
Lg1_N1_%=:
v_mfma_f32_16x16x32_bf16 v[26:29],a[0:3],v[34:37],0
s_add_u32 m0,s59,1024
s_mov_b32 s67,s65
R 62,2,7168
v_mfma_f32_16x16x32_bf16 v[30:33],a[4:7],v[34:37],0
buffer_load_dword v5,s[36:39],s58 offen offset:0 sc1 lds
M 26,a[8:11],38
R 34,2,8192
v_fma_f32 v18,-v66,v74,v18
M 30,a[12:15],38
L 5
buffer_load_dwordx4 v[70:73],v11,s[36:39],s73 offen offset:0 sc1
M 26,a[16:19],42
s_add_u32 s73,s73,s64
R 38,2,9216
v_fma_f32 v19,-v66,v75,v19
M 30,a[20:23],42
s_add_u32 s65,s65,s62
v_fma_f32 v20,-v66,v76,v20
v_fma_f32 v21,-v66,v77,v21
M 26,a[24:27],46
R 42,2,10240
v_mul_f32 v82,v18,v18
M 30,a[28:31],46
L 5
v_mul_f32 v83,v19,v19
v_mul_f32 v84,v20,v20
M 26,a[32:35],50
R 46,2,11264
v_mul_f32 v85,v21,v21
M 30,a[36:39],50
v_fma_f32 v82,v68,v82,v67
v_fma_f32 v83,v68,v83,v67
M 26,a[40:43],54
R 50,2,12288
v_fma_f32 v84,v68,v84,v67
M 30,a[44:47],54
L 5
v_fma_f32 v85,v68,v85,v67
v_mul_f32 v82,v18,v82
M 26,a[48:51],58
R 54,2,13312
v_mul_f32 v83,v19,v83
M 30,a[52:55],58
v_exp_f32 v82,v82
M 26,a[56:59],62
R 58,2,14336
v_mul_f32 v84,v20,v84
M 30,a[60:63],62
L 5
v_exp_f32 v83,v83
M 26,a[64:67],34
R 62,2,15360
v_mul_f32 v85,v21,v85
M 30,a[68:71],34
v_exp_f32 v84,v84
M 26,a[72:75],38
R 34,2,16384
v_fma_f32 v82,v82,v69,v69
M 30,a[76:79],38
L 5
v_exp_f32 v85,v85
M 26,a[80:83],42
R 38,2,17408
v_fma_f32 v83,v83,v69,v69
M 30,a[84:87],42
v_rcp_f32 v82,v82
M 26,a[88:91],46
R 42,2,18432
v_fma_f32 v84,v84,v69,v69
M 30,a[92:95],46
L 5
v_rcp_f32 v83,v83
M 26,a[96:99],50
R 46,2,19456
)"
R"(v_fma_f32 v85,v85,v69,v69
M 30,a[100:103],50
v_rcp_f32 v84,v84
M 26,a[104:107],54
R 50,2,20480
v_mul_f32 v18,v18,v82
M 30,a[108:111],54
L 5
v_rcp_f32 v85,v85
M 26,a[112:115],58
R 54,2,21504
v_mul_f32 v19,v19,v83
M 30,a[116:119],58
v_mul_f32 v20,v20,v84
v_mul_f32 v21,v21,v85
M 26,a[120:123],62
R 58,2,22528
v_cvt_pk_bf16_f32 v90,v18,v19
M 30,a[124:127],62
L 5
v_cvt_pk_bf16_f32 v91,v20,v21
v_fma_f32 v22,-v66,v78,v22
M 26,a[128:131],34
R 62,2,23552
v_fma_f32 v23,-v66,v79,v23
M 30,a[132:135],34
v_fma_f32 v24,-v66,v80,v24
v_fma_f32 v25,-v66,v81,v25
M 26,a[136:139],38
R 34,2,24576
v_mul_f32 v86,v22,v22
M 30,a[140:143],38
L 5
v_mul_f32 v87,v23,v23
v_mul_f32 v88,v24,v24
M 26,a[144:147],42
R 38,2,25600
v_mul_f32 v89,v25,v25
M 30,a[148:151],42
v_fma_f32 v86,v68,v86,v67
v_fma_f32 v87,v68,v87,v67
M 26,a[152:155],46
R 42,2,26624
v_fma_f32 v88,v68,v88,v67
M 30,a[156:159],46
L 5
v_fma_f32 v89,v68,v89,v67
v_mul_f32 v86,v22,v86
M 26,a[160:163],50
R 46,2,27648
v_mul_f32 v87,v23,v87
M 30,a[164:167],50
v_exp_f32 v86,v86
M 26,a[168:171],54
R 50,2,28672
v_mul_f32 v88,v24,v88
M 30,a[172:175],54
L 5
v_exp_f32 v87,v87
M 26,a[176:179],58
R 54,2,29696
v_mul_f32 v89,v25,v89
M 30,a[180:183],58
v_exp_f32 v88,v88
M 26,a[184:187],62
R 58,2,30720
v_fma_f32 v86,v86,v69,v69
M 30,a[188:191],62
L 5
v_exp_f32 v89,v89
M 26,a[192:195],34
s_waitcnt vmcnt(0)
R 62,2,31744
v_fma_f32 v87,v87,v69,v69
M 30,a[196:199],34
s_barrier
ds_read_b32 v7,v6 offset:1024
v_fma_f32 v88,v88,v69,v69
M 26,a[200:203],38
R 34,1,0
v_fma_f32 v89,v89,v69,v69
M 30,a[204:207],38
L 6
buffer_store_dword v4,v3,s[36:39],s70 offen offset:0 sc0 sc1
M 26,a[208:211],42
s_mov_b32 s70,s69
R 38,1,1024
v_xor_b32 v2,65536,v2
M 30,a[212:215],42
s_mov_b32 s69,s68
v_rcp_f32 v86,v86
M 26,a[216:219],46
L 2
R 42,1,2048
v_mul_f32 v22,v22,v86
M 30,a[220:223],46
s_nop 0
v_rcp_f32 v87,v87
M 26,a[224:227],50
s_add_u32 s68,s68,s63
R 46,1,3072
v_mul_f32 v23,v23,v87
M 30,a[228:231],50
v_rcp_f32 v88,v88
M 26,a[232:235],54
R 50,1,4096
v_mul_f32 v24,v24,v88
M 30,a[236:239],54
s_nop 0
v_rcp_f32 v89,v89
M 26,a[240:243],58
R 54,1,5120
v_mul_f32 v25,v25,v89
M 30,a[244:247],58
v_cvt_pk_bf16_f32 v92,v22,v23
v_cmp_eq_u32 vcc,s53,v7
M 26,a[248:251],62
s_cmp_eq_u64 vcc,-1
R 58,1,6144
v_cvt_pk_bf16_f32 v93,v24,v25
M 30,a[252:255],62
L 5
buffer_store_dwordx4 v[90:93],v0,s[36:39],s66 offen offset:0 sc0 sc1
s_cbranch_scc1 Lg1_L20_%=
s_cmp_eq_u32 vcc_lo,-1
s_cbranch_scc1 Lg1_L10_%=
Lg1_X0_%=:
v_mfma_f32_16x16x32_bf16 v[18:21],a[0:3],v[34:37],0
s_add_u32 m0,s59,0
s_mov_b32 s66,s65
R 62,1,7168
v_mfma_f32_16x16x32_bf16 v[22:25],a[4:7],v[34:37],0
buffer_load_dword v5,s[36:39],s58 offen offset:0 sc1 lds
M 18,a[8:11],38
R 34,1,8192
v_fma_f32 v26,-v70,v74,v26
M 22,a[12:15],38
L 5
buffer_load_dwordx4 v[66:69],v11,s[36:39],s73 offen offset:0 sc1
M 18,a[16:19],42
s_add_u32 s73,s73,s64
R 38,1,9216
v_fma_f32 v27,-v70,v75,v27
M 22,a[20:23],42
s_add_u32 s65,s65,s62
v_fma_f32 v28,-v70,v76,v28
v_fma_f32 v29,-v70,v77,v29
M 18,a[24:27],46
R 42,1,10240
v_mul_f32 v82,v26,v26
M 22,a[28:31],46
L 5
v_mul_f32 v83,v27,v27
v_mul_f32 v84,v28,v28
M 18,a[32:35],50
R 46,1,11264
v_mul_f32 v85,v29,v29
M 22,a[36:39],50
v_fma_f32 v82,v72,v82,v71
v_fma_f32 v83,v72,v83,v71
M 18,a[40:43],54
R 50,1,12288
v_fma_f32 v84,v72,v84,v71
M 22,a[44:47],54
L 5
v_fma_f32 v85,v72,v85,v71
v_mul_f32 v82,v26,v82
M 18,a[48:51],58
R 54,1,13312
v_mul_f32 v83,v27,v83
M 22,a[52:55],58
v_exp_f32 v82,v82
M 18,a[56:59],62
R 58,1,14336
v_mul_f32 v84,v28,v84
M 22,a[60:63],62
L 5
v_exp_f32 v83,v83
M 18,a[64:67],34
R 62,1,15360
v_mul_f32 v85,v29,v85
M 22,a[68:71],34
v_exp_f32 v84,v84
M 18,a[72:75],38
R 34,1,16384
v_fma_f32 v82,v82,v73,v73
M 22,a[76:79],38
L 5
v_exp_f32 v85,v85
M 18,a[80:83],42
R 38,1,17408
v_fma_f32 v83,v83,v73,v73
M 22,a[84:87],42
v_rcp_f32 v82,v82
M 18,a[88:91],46
R 42,1,18432
v_fma_f32 v84,v84,v73,v73
M 22,a[92:95],46
L 5
v_rcp_f32 v83,v83
M 18,a[96:99],50
R 46,1,19456
v_fma_f32 v85,v85,v73,v73
M 22,a[100:103],50
v_rcp_f32 v84,v84
M 18,a[104:107],54
R 50,1,20480
v_mul_f32 v26,v26,v82
M 22,a[108:111],54
L 5
v_rcp_f32 v85,v85
M 18,a[112:115],58
R 54,1,21504
v_mul_f32 v27,v27,v83
M 22,a[116:119],58
v_mul_f32 v28,v28,v84
v_mul_f32 v29,v29,v85
M 18,a[120:123],62
R 58,1,22528
v_cvt_pk_bf16_f32 v90,v26,v27
M 22,a[124:127],62
L 5
v_cvt_pk_bf16_f32 v91,v28,v29
v_fma_f32 v30,-v70,v78,v30
M 18,a[128:131],34
R 62,1,23552
v_fma_f32 v31,-v70,v79,v31
M 22,a[132:135],34
v_fma_f32 v32,-v70,v80,v32
v_fma_f32 v33,-v70,v81,v33
M 18,a[136:139],38
R 34,1,24576
v_mul_f32 v86,v30,v30
M 22,a[140:143],38
L 5
v_mul_f32 v87,v31,v31
v_mul_f32 v88,v32,v32
M 18,a[144:147],42
R 38,1,25600
v_mul_f32 v89,v33,v33
M 22,a[148:151],42
v_fma_f32 v86,v72,v86,v71
v_fma_f32 v87,v72,v87,v71
M 18,a[152:155],46
R 42,1,26624
v_fma_f32 v88,v72,v88,v71
M 22,a[156:159],46
L 5
v_fma_f32 v89,v72,v89,v71
v_mul_f32 v86,v30,v86
M 18,a[160:163],50
R 46,1,27648
v_mul_f32 v87,v31,v87
M 22,a[164:167],50
v_exp_f32 v86,v86
M 18,a[168:171],54
R 50,1,28672
v_mul_f32 v88,v32,v88
M 22,a[172:175],54
L 5
v_exp_f32 v87,v87
M 18,a[176:179],58
R 54,1,29696
v_mul_f32 v89,v33,v89
M 22,a[180:183],58
v_exp_f32 v88,v88
M 18,a[184:187],62
R 58,1,30720
v_fma_f32 v86,v86,v73,v73
M 22,a[188:191],62
L 5
v_exp_f32 v89,v89
M 18,a[192:195],34
s_waitcnt vmcnt(0)
R 62,1,31744
v_fma_f32 v87,v87,v73,v73
M 22,a[196:199],34
s_barrier
ds_read_b32 v7,v6 offset:0
v_fma_f32 v88,v88,v73,v73
M 18,a[200:203],38
buffer_store_dword v4,v3,s[36:39],s70 offen offset:0 sc0 sc1
M 22,a[204:207],38
L 5
v_rcp_f32 v86,v86
M 18,a[208:211],42
s_mov_b32 s70,s69
v_rcp_f32 v87,v87
M 22,a[212:215],42
s_mov_b32 s69,s68
v_rcp_f32 v88,v88
M 18,a[216:219],46
L 0
v_fma_f32 v89,v89,v73,v73
v_mul_f32 v30,v30,v86
M 22,a[220:223],46
s_nop 0
v_rcp_f32 v89,v89
M 18,a[224:227],50
s_add_u32 s68,s68,s63
v_mul_f32 v31,v31,v87
v_mul_f32 v32,v32,v88
M 22,a[228:231],50
v_mul_f32 v33,v33,v89
v_cvt_pk_bf16_f32 v92,v30,v31
M 18,a[232:235],54
v_cvt_pk_bf16_f32 v93,v32,v33
v_xor_b32 v1,65536,v1
M 22,a[236:239],54
s_nop 0
buffer_store_dwordx4 v[90:93],v0,s[36:39],s67 offen offset:0 sc0 sc1
v_cmp_eq_u32 vcc,s53,v7
M 18,a[240:243],58
M 22,a[244:247],58
M 18,a[248:251],62
M 22,a[252:255],62
s_mov_b32 s80,1
s_cmp_eq_u32 vcc_lo,-1
s_cbranch_scc0 Lg1_xn0_%=
s_mov_b32 m0,s57
s_add_u32 s56,s55,4096
D 0,55,0
s_nop 0
v_fma_f32 v18,-v66,v74,v18
v_fma_f32 v19,-v66,v75,v19
v_fma_f32 v20,-v66,v76,v20
v_fma_f32 v21,-v66,v77,v21
v_mul_f32 v82,v18,v18
v_mul_f32 v83,v19,v19
v_mul_f32 v84,v20,v20
D 0,55,1024
v_mul_f32 v85,v21,v21
v_fma_f32 v82,v68,v82,v67
v_fma_f32 v83,v68,v83,v67
v_fma_f32 v84,v68,v84,v67
v_fma_f32 v85,v68,v85,v67
v_mul_f32 v82,v18,v82
v_mul_f32 v83,v19,v83
D 0,55,2048
v_mul_f32 v84,v20,v84
v_mul_f32 v85,v21,v85
v_exp_f32 v82,v82
v_exp_f32 v83,v83
v_exp_f32 v84,v84
v_exp_f32 v85,v85
v_fma_f32 v82,v82,v69,v69
D 0,55,3072
v_fma_f32 v83,v83,v69,v69
v_fma_f32 v84,v84,v69,v69
v_fma_f32 v85,v85,v69,v69
v_rcp_f32 v82,v82
v_rcp_f32 v83,v83
v_rcp_f32 v84,v84
v_rcp_f32 v85,v85
s_add_u32 m0,s57,4096
s_nop 0
D 0,56,0
v_mul_f32 v18,v18,v82
v_mul_f32 v19,v19,v83
v_mul_f32 v20,v20,v84
v_mul_f32 v21,v21,v85
v_cvt_pk_bf16_f32 v90,v18,v19
v_cvt_pk_bf16_f32 v91,v20,v21
v_fma_f32 v22,-v66,v78,v22
D 0,56,1024
v_fma_f32 v23,-v66,v79,v23
v_fma_f32 v24,-v66,v80,v24
v_fma_f32 v25,-v66,v81,v25
v_mul_f32 v86,v22,v22
v_mul_f32 v87,v23,v23
v_mul_f32 v88,v24,v24
v_mul_f32 v89,v25,v25
D 0,56,2048
v_fma_f32 v86,v68,v86,v67
v_fma_f32 v87,v68,v87,v67
v_fma_f32 v88,v68,v88,v67
v_fma_f32 v89,v68,v89,v67
v_mul_f32 v86,v22,v86
v_mul_f32 v87,v23,v87
v_mul_f32 v88,v24,v88
D 0,56,3072
v_mul_f32 v89,v25,v89
v_exp_f32 v86,v86
v_exp_f32 v87,v87
v_exp_f32 v88,v88
v_exp_f32 v89,v89
v_fma_f32 v86,v86,v69,v69
v_fma_f32 v87,v87,v69,v69
s_add_u32 s55,s55,s60
s_add_u32 s57,s57,32768
s_and_b32 s57,s57,131071
s_add_u32 s54,s54,s46
v_fma_f32 v88,v88,v69,v69
v_fma_f32 v89,v89,v69,v69
v_rcp_f32 v86,v86
v_rcp_f32 v87,v87
v_rcp_f32 v88,v88
v_rcp_f32 v89,v89
v_mul_f32 v22,v22,v86
v_mul_f32 v23,v23,v87
v_mul_f32 v24,v24,v88
v_mul_f32 v25,v25,v89
v_cvt_pk_bf16_f32 v92,v22,v23
v_cvt_pk_bf16_f32 v93,v24,v25
buffer_store_dwordx4 v[90:93],v0,s[36:39],s66 offen offset:0 sc0 sc1
s_mov_b32 s72,s69
s_mov_b32 s69,0x7fff0000
s_mov_b32 s66,0x7fff0000
s_mov_b32 s76,0x7fff0000
s_branch Lg1_gox_%=
Lg1_xn0_%=:
s_cmp_ge_u32 s54,256
s_cbranch_scc1 Lg1_fin0_%=
s_add_u32 m0,s59,4096
s_nop 0
buffer_load_dword v5,s[36:39],s58 offen offset:0 sc1 lds
buffer_store_dword v4,v3,s[36:39],s71 offen offset:0 sc0 sc1
s_add_u32 m0,s59,5120
s_nop 0
buffer_load_dword v5,s[36:39],s58 offen offset:0 sc1 lds
buffer_store_dword v4,v3,s[36:39],s71 offen offset:0 sc0 sc1
s_mov_b32 s83,0
v_fma_f32 v18,-v66,v74,v18
v_fma_f32 v19,-v66,v75,v19
v_fma_f32 v20,-v66,v76,v20
v_fma_f32 v21,-v66,v77,v21
v_mul_f32 v82,v18,v18
v_mul_f32 v83,v19,v19
v_mul_f32 v84,v20,v20
v_mul_f32 v85,v21,v21
v_fma_f32 v82,v68,v82,v67
v_fma_f32 v83,v68,v83,v67
v_fma_f32 v84,v68,v84,v67
v_fma_f32 v85,v68,v85,v67
v_mul_f32 v82,v18,v82
v_mul_f32 v83,v19,v83
v_mul_f32 v84,v20,v84
v_mul_f32 v85,v21,v85
v_exp_f32 v82,v82
v_exp_f32 v83,v83
v_exp_f32 v84,v84
v_exp_f32 v85,v85
v_fma_f32 v82,v82,v69,v69
v_fma_f32 v83,v83,v69,v69
v_fma_f32 v84,v84,v69,v69
v_fma_f32 v85,v85,v69,v69
v_rcp_f32 v82,v82
v_rcp_f32 v83,v83
v_rcp_f32 v84,v84
v_rcp_f32 v85,v85
v_mul_f32 v18,v18,v82
v_mul_f32 v19,v19,v83
v_mul_f32 v20,v20,v84
v_mul_f32 v21,v21,v85
v_cvt_pk_bf16_f32 v90,v18,v19
v_cvt_pk_bf16_f32 v91,v20,v21
v_fma_f32 v22,-v66,v78,v22
v_fma_f32 v23,-v66,v79,v23
v_fma_f32 v24,-v66,v80,v24
v_fma_f32 v25,-v66,v81,v25
v_mul_f32 v86,v22,v22
v_mul_f32 v87,v23,v23
v_mul_f32 v88,v24,v24
v_mul_f32 v89,v25,v25
v_fma_f32 v86,v68,v86,v67
v_fma_f32 v87,v68,v87,v67
v_fma_f32 v88,v68,v88,v67
v_fma_f32 v89,v68,v89,v67
v_mul_f32 v86,v22,v86
v_mul_f32 v87,v23,v87
v_mul_f32 v88,v24,v88
v_mul_f32 v89,v25,v89
v_exp_f32 v86,v86
v_exp_f32 v87,v87
v_exp_f32 v88,v88
v_exp_f32 v89,v89
v_fma_f32 v86,v86,v69,v69
v_fma_f32 v87,v87,v69,v69
v_fma_f32 v88,v88,v69,v69
v_fma_f32 v89,v89,v69,v69
v_rcp_f32 v86,v86
v_rcp_f32 v87,v87
v_rcp_f32 v88,v88
v_rcp_f32 v89,v89
v_mul_f32 v22,v22,v86
v_mul_f32 v23,v23,v87
v_mul_f32 v24,v24,v88
v_mul_f32 v25,v25,v89
v_cvt_pk_bf16_f32 v92,v22,v23
v_cvt_pk_bf16_f32 v93,v24,v25
buffer_store_dwordx4 v[90:93],v0,s[36:39],s66 offen offset:0 sc0 sc1
s_mov_b32 s72,s69
s_mov_b32 s69,0x7fff0000
s_mov_b32 s66,0x7fff0000
s_mov_b32 s76,0x7fff0000
s_branch Lg1_poll_%=
Lg1_fin0_%=:
v_fma_f32 v18,-v66,v74,v18
v_fma_f32 v19,-v66,v75,v19
v_fma_f32 v20,-v66,v76,v20
)"
R"(v_fma_f32 v21,-v66,v77,v21
v_mul_f32 v82,v18,v18
v_mul_f32 v83,v19,v19
v_mul_f32 v84,v20,v20
v_mul_f32 v85,v21,v21
v_fma_f32 v82,v68,v82,v67
v_fma_f32 v83,v68,v83,v67
v_fma_f32 v84,v68,v84,v67
v_fma_f32 v85,v68,v85,v67
v_mul_f32 v82,v18,v82
v_mul_f32 v83,v19,v83
v_mul_f32 v84,v20,v84
v_mul_f32 v85,v21,v85
v_exp_f32 v82,v82
v_exp_f32 v83,v83
v_exp_f32 v84,v84
v_exp_f32 v85,v85
v_fma_f32 v82,v82,v69,v69
v_fma_f32 v83,v83,v69,v69
v_fma_f32 v84,v84,v69,v69
v_fma_f32 v85,v85,v69,v69
v_rcp_f32 v82,v82
v_rcp_f32 v83,v83
v_rcp_f32 v84,v84
v_rcp_f32 v85,v85
v_mul_f32 v18,v18,v82
v_mul_f32 v19,v19,v83
v_mul_f32 v20,v20,v84
v_mul_f32 v21,v21,v85
v_cvt_pk_bf16_f32 v90,v18,v19
v_cvt_pk_bf16_f32 v91,v20,v21
v_fma_f32 v22,-v66,v78,v22
v_fma_f32 v23,-v66,v79,v23
v_fma_f32 v24,-v66,v80,v24
v_fma_f32 v25,-v66,v81,v25
v_mul_f32 v86,v22,v22
v_mul_f32 v87,v23,v23
v_mul_f32 v88,v24,v24
v_mul_f32 v89,v25,v25
v_fma_f32 v86,v68,v86,v67
v_fma_f32 v87,v68,v87,v67
v_fma_f32 v88,v68,v88,v67
v_fma_f32 v89,v68,v89,v67
v_mul_f32 v86,v22,v86
v_mul_f32 v87,v23,v87
v_mul_f32 v88,v24,v88
v_mul_f32 v89,v25,v89
v_exp_f32 v86,v86
v_exp_f32 v87,v87
v_exp_f32 v88,v88
v_exp_f32 v89,v89
v_fma_f32 v86,v86,v69,v69
v_fma_f32 v87,v87,v69,v69
v_fma_f32 v88,v88,v69,v69
v_fma_f32 v89,v89,v69,v69
v_rcp_f32 v86,v86
v_rcp_f32 v87,v87
v_rcp_f32 v88,v88
v_rcp_f32 v89,v89
v_mul_f32 v22,v22,v86
v_mul_f32 v23,v23,v87
v_mul_f32 v24,v24,v88
v_mul_f32 v25,v25,v89
v_cvt_pk_bf16_f32 v92,v22,v23
v_cvt_pk_bf16_f32 v93,v24,v25
buffer_store_dwordx4 v[90:93],v0,s[36:39],s66 offen offset:0 sc0 sc1
s_mov_b32 s72,s69
s_mov_b32 s69,0x7fff0000
s_mov_b32 s66,0x7fff0000
s_mov_b32 s76,0x7fff0000
s_branch Lg1_done_%=
Lg1_L21_%=:
v_mfma_f32_16x16x32_bf16 v[26:29],a[0:3],v[34:37],0
s_add_u32 s58,s58,s61
s_add_u32 s58,s58,s61
R 62,2,7168
v_mfma_f32_16x16x32_bf16 v[30:33],a[4:7],v[34:37],0
s_add_u32 m0,s59,1024
buffer_load_dwordx4 v[70:73],v11,s[36:39],s73 offen offset:0 sc1
M 26,a[8:11],38
s_add_u32 s73,s73,s64
R 34,2,8192
v_fma_f32 v18,-v66,v74,v18
M 30,a[12:15],38
L 5
buffer_load_dword v8,s[36:39],s58 offen offset:0 sc1 lds
M 26,a[16:19],42
s_mov_b32 m0,s57
R 38,2,9216
v_fma_f32 v19,-v66,v75,v19
M 30,a[20:23],42
s_add_u32 s56,s55,4096
v_fma_f32 v20,-v66,v76,v20
v_fma_f32 v21,-v66,v77,v21
M 26,a[24:27],46
s_mov_b32 s67,s65
R 42,2,10240
v_mul_f32 v82,v18,v18
M 30,a[28:31],46
L 5
D 0,55,0
M 26,a[32:35],50
R 46,2,11264
v_mul_f32 v83,v19,v19
M 30,a[36:39],50
v_mul_f32 v84,v20,v20
v_mul_f32 v85,v21,v21
M 26,a[40:43],54
R 50,2,12288
v_fma_f32 v82,v68,v82,v67
M 30,a[44:47],54
L 5
D 0,55,1024
M 26,a[48:51],58
R 54,2,13312
v_fma_f32 v83,v68,v83,v67
M 30,a[52:55],58
v_fma_f32 v84,v68,v84,v67
v_fma_f32 v85,v68,v85,v67
M 26,a[56:59],62
R 58,2,14336
v_mul_f32 v82,v18,v82
M 30,a[60:63],62
L 5
D 0,55,2048
M 26,a[64:67],34
R 62,2,15360
v_mul_f32 v83,v19,v83
M 30,a[68:71],34
v_exp_f32 v82,v82
M 26,a[72:75],38
R 34,2,16384
v_mul_f32 v84,v20,v84
M 30,a[76:79],38
L 5
D 0,55,3072
M 26,a[80:83],42
s_add_u32 m0,s57,4096
R 38,2,17408
v_mul_f32 v85,v21,v85
M 30,a[84:87],42
s_add_u32 s55,s55,s60
v_exp_f32 v83,v83
M 26,a[88:91],46
s_add_u32 s57,s57,32768
R 42,2,18432
v_fma_f32 v82,v82,v69,v69
M 30,a[92:95],46
L 5
D 0,56,0
M 26,a[96:99],50
s_and_b32 s57,s57,131071
R 46,2,19456
v_fma_f32 v83,v83,v69,v69
M 30,a[100:103],50
s_add_u32 s54,s54,s46
v_exp_f32 v84,v84
M 26,a[104:107],54
R 50,2,20480
v_fma_f32 v84,v84,v69,v69
M 30,a[108:111],54
L 5
D 0,56,1024
M 26,a[112:115],58
R 54,2,21504
v_fma_f32 v22,-v66,v78,v22
M 30,a[116:119],58
v_exp_f32 v85,v85
M 26,a[120:123],62
R 58,2,22528
v_fma_f32 v85,v85,v69,v69
M 30,a[124:127],62
L 5
D 0,56,2048
M 26,a[128:131],34
R 62,2,23552
v_fma_f32 v23,-v66,v79,v23
M 30,a[132:135],34
v_rcp_f32 v82,v82
M 26,a[136:139],38
R 34,2,24576
v_mul_f32 v18,v18,v82
M 30,a[140:143],38
L 5
D 0,56,3072
M 26,a[144:147],42
s_mov_b32 m0,s57
R 38,2,25600
v_fma_f32 v24,-v66,v80,v24
M 30,a[148:151],42
s_add_u32 s56,s55,4096
v_rcp_f32 v83,v83
M 26,a[152:155],46
R 42,2,26624
v_mul_f32 v19,v19,v83
M 30,a[156:159],46
L 5
D 0,55,0
M 26,a[160:163],50
R 46,2,27648
v_cvt_pk_bf16_f32 v90,v18,v19
M 30,a[164:167],50
D 0,55,1024
M 26,a[168:171],54
R 50,2,28672
v_fma_f32 v25,-v66,v81,v25
M 30,a[172:175],54
L 5
v_rcp_f32 v84,v84
M 26,a[176:179],58
R 54,2,29696
v_mul_f32 v20,v20,v84
M 30,a[180:183],58
D 0,55,2048
M 26,a[184:187],62
R 58,2,30720
v_mul_f32 v86,v22,v22
M 30,a[188:191],62
L 5
D 0,55,3072
M 26,a[192:195],34
s_waitcnt vmcnt(4)
R 62,2,31744
v_mul_f32 v87,v23,v23
M 30,a[196:199],34
s_barrier
ds_read_b32 v7,v6 offset:1024
v_mul_f32 v88,v24,v24
M 26,a[200:203],38
s_add_u32 m0,s57,4096
R 34,1,0
v_mul_f32 v89,v25,v25
M 30,a[204:207],38
L 6
buffer_store_dword v4,v3,s[36:39],s70 offen offset:0 sc0 sc1
M 26,a[208:211],42
s_add_u32 s55,s55,s60
R 38,1,1024
v_fma_f32 v86,v68,v86,v67
M 30,a[212:215],42
s_add_u32 s57,s57,32768
D 0,56,0
M 26,a[216:219],46
s_and_b32 s57,s57,131071
R 42,1,2048
v_fma_f32 v87,v68,v87,v67
M 30,a[220:223],46
L 6
D 0,56,1024
M 26,a[224:227],50
s_add_u32 s54,s54,s46
R 46,1,3072
v_fma_f32 v88,v68,v88,v67
M 30,a[228:231],50
s_mov_b32 s70,s69
D 0,56,2048
M 26,a[232:235],54
s_add_u32 s65,s65,s62
R 50,1,4096
v_fma_f32 v89,v68,v89,v67
M 30,a[236:239],54
L 6
D 0,56,3072
M 26,a[240:243],58
s_mov_b32 s69,s68
R 54,1,5120
v_mul_f32 v86,v22,v86
v_mul_f32 v87,v23,v87
v_mul_f32 v88,v24,v88
v_mul_f32 v89,v25,v89
v_exp_f32 v86,v86
v_exp_f32 v87,v87
v_exp_f32 v88,v88
v_exp_f32 v89,v89
M 30,a[244:247],58
L 6
v_rcp_f32 v85,v85
v_fma_f32 v86,v86,v69,v69
v_fma_f32 v87,v87,v69,v69
v_fma_f32 v88,v88,v69,v69
v_fma_f32 v89,v89,v69,v69
v_rcp_f32 v86,v86
v_rcp_f32 v87,v87
v_rcp_f32 v88,v88
v_rcp_f32 v89,v89
v_cmp_eq_u32 vcc,s53,v7
M 26,a[248:251],62
s_add_u32 s68,s68,s63
s_cmp_eq_u32 vcc_lo,-1
R 58,1,6144
v_mul_f32 v21,v21,v85
v_cvt_pk_bf16_f32 v91,v20,v21
v_mul_f32 v22,v22,v86
v_mul_f32 v23,v23,v87
v_mul_f32 v24,v24,v88
v_mul_f32 v25,v25,v89
v_cvt_pk_bf16_f32 v92,v22,v23
v_cvt_pk_bf16_f32 v93,v24,v25
M 30,a[252:255],62
L 5
buffer_store_dwordx4 v[90:93],v0,s[36:39],s66 offen offset:0 sc0 sc1
v_xor_b32 v2,65536,v2
s_cbranch_scc1 Lg1_S0_%=
Lg1_N0_%=:
v_mfma_f32_16x16x32_bf16 v[18:21],a[0:3],v[34:37],0
s_add_u32 m0,s59,0
s_mov_b32 s66,s65
R 62,1,7168
v_mfma_f32_16x16x32_bf16 v[22:25],a[4:7],v[34:37],0
buffer_load_dword v5,s[36:39],s58 offen offset:0 sc1 lds
M 18,a[8:11],38
R 34,1,8192
v_fma_f32 v26,-v70,v74,v26
M 22,a[12:15],38
L 5
buffer_load_dwordx4 v[66:69],v11,s[36:39],s73 offen offset:0 sc1
M 18,a[16:19],42
s_add_u32 s73,s73,s64
R 38,1,9216
v_fma_f32 v27,-v70,v75,v27
M 22,a[20:23],42
s_add_u32 s65,s65,s62
v_fma_f32 v28,-v70,v76,v28
v_fma_f32 v29,-v70,v77,v29
M 18,a[24:27],46
R 42,1,10240
v_mul_f32 v82,v26,v26
M 22,a[28:31],46
L 5
v_mul_f32 v83,v27,v27
v_mul_f32 v84,v28,v28
M 18,a[32:35],50
R 46,1,11264
v_mul_f32 v85,v29,v29
M 22,a[36:39],50
v_fma_f32 v82,v72,v82,v71
v_fma_f32 v83,v72,v83,v71
M 18,a[40:43],54
R 50,1,12288
v_fma_f32 v84,v72,v84,v71
M 22,a[44:47],54
L 5
v_fma_f32 v85,v72,v85,v71
v_mul_f32 v82,v26,v82
M 18,a[48:51],58
R 54,1,13312
v_mul_f32 v83,v27,v83
M 22,a[52:55],58
v_exp_f32 v82,v82
M 18,a[56:59],62
R 58,1,14336
v_mul_f32 v84,v28,v84
M 22,a[60:63],62
L 5
v_exp_f32 v83,v83
M 18,a[64:67],34
R 62,1,15360
v_mul_f32 v85,v29,v85
M 22,a[68:71],34
v_exp_f32 v84,v84
M 18,a[72:75],38
R 34,1,16384
v_fma_f32 v82,v82,v73,v73
M 22,a[76:79],38
L 5
v_exp_f32 v85,v85
M 18,a[80:83],42
R 38,1,17408
v_fma_f32 v83,v83,v73,v73
M 22,a[84:87],42
v_rcp_f32 v82,v82
M 18,a[88:91],46
R 42,1,18432
v_fma_f32 v84,v84,v73,v73
M 22,a[92:95],46
L 5
v_rcp_f32 v83,v83
M 18,a[96:99],50
R 46,1,19456
v_fma_f32 v85,v85,v73,v73
M 22,a[100:103],50
v_rcp_f32 v84,v84
M 18,a[104:107],54
R 50,1,20480
v_mul_f32 v26,v26,v82
M 22,a[108:111],54
L 5
v_rcp_f32 v85,v85
M 18,a[112:115],58
R 54,1,21504
v_mul_f32 v27,v27,v83
M 22,a[116:119],58
v_mul_f32 v28,v28,v84
v_mul_f32 v29,v29,v85
M 18,a[120:123],62
R 58,1,22528
v_cvt_pk_bf16_f32 v90,v26,v27
M 22,a[124:127],62
L 5
v_cvt_pk_bf16_f32 v91,v28,v29
v_fma_f32 v30,-v70,v78,v30
M 18,a[128:131],34
R 62,1,23552
v_fma_f32 v31,-v70,v79,v31
M 22,a[132:135],34
v_fma_f32 v32,-v70,v80,v32
v_fma_f32 v33,-v70,v81,v33
M 18,a[136:139],38
R 34,1,24576
v_mul_f32 v86,v30,v30
M 22,a[140:143],38
L 5
v_mul_f32 v87,v31,v31
v_mul_f32 v88,v32,v32
M 18,a[144:147],42
R 38,1,25600
v_mul_f32 v89,v33,v33
M 22,a[148:151],42
v_fma_f32 v86,v72,v86,v71
v_fma_f32 v87,v72,v87,v71
M 18,a[152:155],46
R 42,1,26624
v_fma_f32 v88,v72,v88,v71
M 22,a[156:159],46
L 5
v_fma_f32 v89,v72,v89,v71
v_mul_f32 v86,v30,v86
M 18,a[160:163],50
R 46,1,27648
v_mul_f32 v87,v31,v87
M 22,a[164:167],50
v_exp_f32 v86,v86
M 18,a[168:171],54
R 50,1,28672
v_mul_f32 v88,v32,v88
M 22,a[172:175],54
L 5
v_exp_f32 v87,v87
M 18,a[176:179],58
R 54,1,29696
v_mul_f32 v89,v33,v89
M 22,a[180:183],58
v_exp_f32 v88,v88
M 18,a[184:187],62
R 58,1,30720
v_fma_f32 v86,v86,v73,v73
M 22,a[188:191],62
L 5
v_exp_f32 v89,v89
M 18,a[192:195],34
s_waitcnt vmcnt(0)
R 62,1,31744
v_fma_f32 v87,v87,v73,v73
M 22,a[196:199],34
s_barrier
ds_read_b32 v7,v6 offset:0
v_fma_f32 v88,v88,v73,v73
M 18,a[200:203],38
R 34,2,0
v_fma_f32 v89,v89,v73,v73
M 22,a[204:207],38
L 6
buffer_store_dword v4,v3,s[36:39],s70 offen offset:0 sc0 sc1
M 18,a[208:211],42
s_mov_b32 s70,s69
R 38,2,1024
v_xor_b32 v1,65536,v1
M 22,a[212:215],42
s_mov_b32 s69,s68
v_rcp_f32 v86,v86
M 18,a[216:219],46
L 2
R 42,2,2048
v_mul_f32 v30,v30,v86
M 22,a[220:223],46
s_nop 0
v_rcp_f32 v87,v87
M 18,a[224:227],50
s_add_u32 s68,s68,s63
R 46,2,3072
v_mul_f32 v31,v31,v87
M 22,a[228:231],50
v_rcp_f32 v88,v88
M 18,a[232:235],54
R 50,2,4096
v_mul_f32 v32,v32,v88
M 22,a[236:239],54
s_nop 0
v_rcp_f32 v89,v89
M 18,a[240:243],58
R 54,2,5120
v_mul_f32 v33,v33,v89
M 22,a[244:247],58
v_cvt_pk_bf16_f32 v92,v30,v31
v_cmp_eq_u32 vcc,s53,v7
M 18,a[248:251],62
s_cmp_eq_u64 vcc,-1
R 58,2,6144
v_cvt_pk_bf16_f32 v93,v32,v33
M 22,a[252:255],62
)"
R"(L 5
buffer_store_dwordx4 v[90:93],v0,s[36:39],s67 offen offset:0 sc0 sc1
s_cbranch_scc1 Lg1_L21_%=
s_cmp_eq_u32 vcc_lo,-1
s_cbranch_scc1 Lg1_L11_%=
Lg1_X1_%=:
v_mfma_f32_16x16x32_bf16 v[26:29],a[0:3],v[34:37],0
s_add_u32 m0,s59,1024
s_mov_b32 s67,s65
R 62,2,7168
v_mfma_f32_16x16x32_bf16 v[30:33],a[4:7],v[34:37],0
buffer_load_dword v5,s[36:39],s58 offen offset:0 sc1 lds
M 26,a[8:11],38
R 34,2,8192
v_fma_f32 v18,-v66,v74,v18
M 30,a[12:15],38
L 5
buffer_load_dwordx4 v[70:73],v11,s[36:39],s73 offen offset:0 sc1
M 26,a[16:19],42
s_add_u32 s73,s73,s64
R 38,2,9216
v_fma_f32 v19,-v66,v75,v19
M 30,a[20:23],42
s_add_u32 s65,s65,s62
v_fma_f32 v20,-v66,v76,v20
v_fma_f32 v21,-v66,v77,v21
M 26,a[24:27],46
R 42,2,10240
v_mul_f32 v82,v18,v18
M 30,a[28:31],46
L 5
v_mul_f32 v83,v19,v19
v_mul_f32 v84,v20,v20
M 26,a[32:35],50
R 46,2,11264
v_mul_f32 v85,v21,v21
M 30,a[36:39],50
v_fma_f32 v82,v68,v82,v67
v_fma_f32 v83,v68,v83,v67
M 26,a[40:43],54
R 50,2,12288
v_fma_f32 v84,v68,v84,v67
M 30,a[44:47],54
L 5
v_fma_f32 v85,v68,v85,v67
v_mul_f32 v82,v18,v82
M 26,a[48:51],58
R 54,2,13312
v_mul_f32 v83,v19,v83
M 30,a[52:55],58
v_exp_f32 v82,v82
M 26,a[56:59],62
R 58,2,14336
v_mul_f32 v84,v20,v84
M 30,a[60:63],62
L 5
v_exp_f32 v83,v83
M 26,a[64:67],34
R 62,2,15360
v_mul_f32 v85,v21,v85
M 30,a[68:71],34
v_exp_f32 v84,v84
M 26,a[72:75],38
R 34,2,16384
v_fma_f32 v82,v82,v69,v69
M 30,a[76:79],38
L 5
v_exp_f32 v85,v85
M 26,a[80:83],42
R 38,2,17408
v_fma_f32 v83,v83,v69,v69
M 30,a[84:87],42
v_rcp_f32 v82,v82
M 26,a[88:91],46
R 42,2,18432
v_fma_f32 v84,v84,v69,v69
M 30,a[92:95],46
L 5
v_rcp_f32 v83,v83
M 26,a[96:99],50
R 46,2,19456
v_fma_f32 v85,v85,v69,v69
M 30,a[100:103],50
v_rcp_f32 v84,v84
M 26,a[104:107],54
R 50,2,20480
v_mul_f32 v18,v18,v82
M 30,a[108:111],54
L 5
v_rcp_f32 v85,v85
M 26,a[112:115],58
R 54,2,21504
v_mul_f32 v19,v19,v83
M 30,a[116:119],58
v_mul_f32 v20,v20,v84
v_mul_f32 v21,v21,v85
M 26,a[120:123],62
R 58,2,22528
v_cvt_pk_bf16_f32 v90,v18,v19
M 30,a[124:127],62
L 5
v_cvt_pk_bf16_f32 v91,v20,v21
v_fma_f32 v22,-v66,v78,v22
M 26,a[128:131],34
R 62,2,23552
v_fma_f32 v23,-v66,v79,v23
M 30,a[132:135],34
v_fma_f32 v24,-v66,v80,v24
v_fma_f32 v25,-v66,v81,v25
M 26,a[136:139],38
R 34,2,24576
v_mul_f32 v86,v22,v22
M 30,a[140:143],38
L 5
v_mul_f32 v87,v23,v23
v_mul_f32 v88,v24,v24
M 26,a[144:147],42
R 38,2,25600
v_mul_f32 v89,v25,v25
M 30,a[148:151],42
v_fma_f32 v86,v68,v86,v67
v_fma_f32 v87,v68,v87,v67
M 26,a[152:155],46
R 42,2,26624
v_fma_f32 v88,v68,v88,v67
M 30,a[156:159],46
L 5
v_fma_f32 v89,v68,v89,v67
v_mul_f32 v86,v22,v86
M 26,a[160:163],50
R 46,2,27648
v_mul_f32 v87,v23,v87
M 30,a[164:167],50
v_exp_f32 v86,v86
M 26,a[168:171],54
R 50,2,28672
v_mul_f32 v88,v24,v88
M 30,a[172:175],54
L 5
v_exp_f32 v87,v87
M 26,a[176:179],58
R 54,2,29696
v_mul_f32 v89,v25,v89
M 30,a[180:183],58
v_exp_f32 v88,v88
M 26,a[184:187],62
R 58,2,30720
v_fma_f32 v86,v86,v69,v69
M 30,a[188:191],62
L 5
v_exp_f32 v89,v89
M 26,a[192:195],34
s_waitcnt vmcnt(0)
R 62,2,31744
v_fma_f32 v87,v87,v69,v69
M 30,a[196:199],34
s_barrier
ds_read_b32 v7,v6 offset:1024
v_fma_f32 v88,v88,v69,v69
M 26,a[200:203],38
buffer_store_dword v4,v3,s[36:39],s70 offen offset:0 sc0 sc1
M 30,a[204:207],38
L 5
v_rcp_f32 v86,v86
M 26,a[208:211],42
s_mov_b32 s70,s69
v_rcp_f32 v87,v87
M 30,a[212:215],42
s_mov_b32 s69,s68
v_rcp_f32 v88,v88
M 26,a[216:219],46
L 0
v_fma_f32 v89,v89,v69,v69
v_mul_f32 v22,v22,v86
M 30,a[220:223],46
s_nop 0
v_rcp_f32 v89,v89
M 26,a[224:227],50
s_add_u32 s68,s68,s63
v_mul_f32 v23,v23,v87
v_mul_f32 v24,v24,v88
M 30,a[228:231],50
v_mul_f32 v25,v25,v89
v_cvt_pk_bf16_f32 v92,v22,v23
M 26,a[232:235],54
v_cvt_pk_bf16_f32 v93,v24,v25
v_xor_b32 v2,65536,v2
M 30,a[236:239],54
s_nop 0
buffer_store_dwordx4 v[90:93],v0,s[36:39],s66 offen offset:0 sc0 sc1
v_cmp_eq_u32 vcc,s53,v7
M 26,a[240:243],58
M 30,a[244:247],58
M 26,a[248:251],62
M 30,a[252:255],62
s_mov_b32 s80,0
s_cmp_eq_u32 vcc_lo,-1
s_cbranch_scc0 Lg1_xn1_%=
s_mov_b32 m0,s57
s_add_u32 s56,s55,4096
D 0,55,0
s_nop 0
v_fma_f32 v26,-v70,v74,v26
v_fma_f32 v27,-v70,v75,v27
v_fma_f32 v28,-v70,v76,v28
v_fma_f32 v29,-v70,v77,v29
v_mul_f32 v82,v26,v26
v_mul_f32 v83,v27,v27
v_mul_f32 v84,v28,v28
D 0,55,1024
v_mul_f32 v85,v29,v29
v_fma_f32 v82,v72,v82,v71
v_fma_f32 v83,v72,v83,v71
v_fma_f32 v84,v72,v84,v71
v_fma_f32 v85,v72,v85,v71
v_mul_f32 v82,v26,v82
v_mul_f32 v83,v27,v83
D 0,55,2048
v_mul_f32 v84,v28,v84
v_mul_f32 v85,v29,v85
v_exp_f32 v82,v82
v_exp_f32 v83,v83
v_exp_f32 v84,v84
v_exp_f32 v85,v85
v_fma_f32 v82,v82,v73,v73
D 0,55,3072
v_fma_f32 v83,v83,v73,v73
v_fma_f32 v84,v84,v73,v73
v_fma_f32 v85,v85,v73,v73
v_rcp_f32 v82,v82
v_rcp_f32 v83,v83
v_rcp_f32 v84,v84
v_rcp_f32 v85,v85
s_add_u32 m0,s57,4096
s_nop 0
D 0,56,0
v_mul_f32 v26,v26,v82
v_mul_f32 v27,v27,v83
v_mul_f32 v28,v28,v84
v_mul_f32 v29,v29,v85
v_cvt_pk_bf16_f32 v90,v26,v27
v_cvt_pk_bf16_f32 v91,v28,v29
v_fma_f32 v30,-v70,v78,v30
D 0,56,1024
v_fma_f32 v31,-v70,v79,v31
v_fma_f32 v32,-v70,v80,v32
v_fma_f32 v33,-v70,v81,v33
v_mul_f32 v86,v30,v30
v_mul_f32 v87,v31,v31
v_mul_f32 v88,v32,v32
v_mul_f32 v89,v33,v33
D 0,56,2048
v_fma_f32 v86,v72,v86,v71
v_fma_f32 v87,v72,v87,v71
v_fma_f32 v88,v72,v88,v71
v_fma_f32 v89,v72,v89,v71
v_mul_f32 v86,v30,v86
v_mul_f32 v87,v31,v87
v_mul_f32 v88,v32,v88
D 0,56,3072
v_mul_f32 v89,v33,v89
v_exp_f32 v86,v86
v_exp_f32 v87,v87
v_exp_f32 v88,v88
v_exp_f32 v89,v89
v_fma_f32 v86,v86,v73,v73
v_fma_f32 v87,v87,v73,v73
s_add_u32 s55,s55,s60
s_add_u32 s57,s57,32768
s_and_b32 s57,s57,131071
s_add_u32 s54,s54,s46
v_fma_f32 v88,v88,v73,v73
v_fma_f32 v89,v89,v73,v73
v_rcp_f32 v86,v86
v_rcp_f32 v87,v87
v_rcp_f32 v88,v88
v_rcp_f32 v89,v89
v_mul_f32 v30,v30,v86
v_mul_f32 v31,v31,v87
v_mul_f32 v32,v32,v88
v_mul_f32 v33,v33,v89
v_cvt_pk_bf16_f32 v92,v30,v31
v_cvt_pk_bf16_f32 v93,v32,v33
buffer_store_dwordx4 v[90:93],v0,s[36:39],s67 offen offset:0 sc0 sc1
s_mov_b32 s72,s69
s_mov_b32 s69,0x7fff0000
s_mov_b32 s67,0x7fff0000
s_mov_b32 s77,0x7fff0000
s_branch Lg1_gox_%=
Lg1_xn1_%=:
s_cmp_ge_u32 s54,256
s_cbranch_scc1 Lg1_fin1_%=
s_add_u32 m0,s59,4096
s_nop 0
buffer_load_dword v5,s[36:39],s58 offen offset:0 sc1 lds
buffer_store_dword v4,v3,s[36:39],s71 offen offset:0 sc0 sc1
s_add_u32 m0,s59,5120
s_nop 0
buffer_load_dword v5,s[36:39],s58 offen offset:0 sc1 lds
buffer_store_dword v4,v3,s[36:39],s71 offen offset:0 sc0 sc1
s_mov_b32 s83,0
v_fma_f32 v26,-v70,v74,v26
v_fma_f32 v27,-v70,v75,v27
v_fma_f32 v28,-v70,v76,v28
v_fma_f32 v29,-v70,v77,v29
v_mul_f32 v82,v26,v26
v_mul_f32 v83,v27,v27
v_mul_f32 v84,v28,v28
v_mul_f32 v85,v29,v29
v_fma_f32 v82,v72,v82,v71
v_fma_f32 v83,v72,v83,v71
v_fma_f32 v84,v72,v84,v71
v_fma_f32 v85,v72,v85,v71
v_mul_f32 v82,v26,v82
v_mul_f32 v83,v27,v83
v_mul_f32 v84,v28,v84
v_mul_f32 v85,v29,v85
v_exp_f32 v82,v82
v_exp_f32 v83,v83
v_exp_f32 v84,v84
v_exp_f32 v85,v85
v_fma_f32 v82,v82,v73,v73
v_fma_f32 v83,v83,v73,v73
v_fma_f32 v84,v84,v73,v73
v_fma_f32 v85,v85,v73,v73
v_rcp_f32 v82,v82
v_rcp_f32 v83,v83
v_rcp_f32 v84,v84
v_rcp_f32 v85,v85
v_mul_f32 v26,v26,v82
v_mul_f32 v27,v27,v83
v_mul_f32 v28,v28,v84
v_mul_f32 v29,v29,v85
v_cvt_pk_bf16_f32 v90,v26,v27
v_cvt_pk_bf16_f32 v91,v28,v29
v_fma_f32 v30,-v70,v78,v30
v_fma_f32 v31,-v70,v79,v31
v_fma_f32 v32,-v70,v80,v32
v_fma_f32 v33,-v70,v81,v33
v_mul_f32 v86,v30,v30
v_mul_f32 v87,v31,v31
v_mul_f32 v88,v32,v32
v_mul_f32 v89,v33,v33
v_fma_f32 v86,v72,v86,v71
v_fma_f32 v87,v72,v87,v71
v_fma_f32 v88,v72,v88,v71
v_fma_f32 v89,v72,v89,v71
v_mul_f32 v86,v30,v86
v_mul_f32 v87,v31,v87
v_mul_f32 v88,v32,v88
v_mul_f32 v89,v33,v89
v_exp_f32 v86,v86
v_exp_f32 v87,v87
v_exp_f32 v88,v88
v_exp_f32 v89,v89
v_fma_f32 v86,v86,v73,v73
v_fma_f32 v87,v87,v73,v73
v_fma_f32 v88,v88,v73,v73
v_fma_f32 v89,v89,v73,v73
v_rcp_f32 v86,v86
v_rcp_f32 v87,v87
v_rcp_f32 v88,v88
v_rcp_f32 v89,v89
v_mul_f32 v30,v30,v86
v_mul_f32 v31,v31,v87
v_mul_f32 v32,v32,v88
v_mul_f32 v33,v33,v89
v_cvt_pk_bf16_f32 v92,v30,v31
v_cvt_pk_bf16_f32 v93,v32,v33
buffer_store_dwordx4 v[90:93],v0,s[36:39],s67 offen offset:0 sc0 sc1
s_mov_b32 s72,s69
s_mov_b32 s69,0x7fff0000
s_mov_b32 s67,0x7fff0000
s_mov_b32 s77,0x7fff0000
s_branch Lg1_poll_%=
Lg1_fin1_%=:
v_fma_f32 v26,-v70,v74,v26
v_fma_f32 v27,-v70,v75,v27
v_fma_f32 v28,-v70,v76,v28
v_fma_f32 v29,-v70,v77,v29
v_mul_f32 v82,v26,v26
v_mul_f32 v83,v27,v27
v_mul_f32 v84,v28,v28
v_mul_f32 v85,v29,v29
v_fma_f32 v82,v72,v82,v71
v_fma_f32 v83,v72,v83,v71
v_fma_f32 v84,v72,v84,v71
v_fma_f32 v85,v72,v85,v71
v_mul_f32 v82,v26,v82
v_mul_f32 v83,v27,v83
v_mul_f32 v84,v28,v84
v_mul_f32 v85,v29,v85
v_exp_f32 v82,v82
v_exp_f32 v83,v83
v_exp_f32 v84,v84
v_exp_f32 v85,v85
v_fma_f32 v82,v82,v73,v73
v_fma_f32 v83,v83,v73,v73
v_fma_f32 v84,v84,v73,v73
v_fma_f32 v85,v85,v73,v73
v_rcp_f32 v82,v82
v_rcp_f32 v83,v83
v_rcp_f32 v84,v84
v_rcp_f32 v85,v85
v_mul_f32 v26,v26,v82
v_mul_f32 v27,v27,v83
v_mul_f32 v28,v28,v84
v_mul_f32 v29,v29,v85
v_cvt_pk_bf16_f32 v90,v26,v27
v_cvt_pk_bf16_f32 v91,v28,v29
v_fma_f32 v30,-v70,v78,v30
v_fma_f32 v31,-v70,v79,v31
v_fma_f32 v32,-v70,v80,v32
v_fma_f32 v33,-v70,v81,v33
v_mul_f32 v86,v30,v30
v_mul_f32 v87,v31,v31
v_mul_f32 v88,v32,v32
v_mul_f32 v89,v33,v33
v_fma_f32 v86,v72,v86,v71
v_fma_f32 v87,v72,v87,v71
v_fma_f32 v88,v72,v88,v71
v_fma_f32 v89,v72,v89,v71
v_mul_f32 v86,v30,v86
v_mul_f32 v87,v31,v87
v_mul_f32 v88,v32,v88
v_mul_f32 v89,v33,v89
v_exp_f32 v86,v86
v_exp_f32 v87,v87
v_exp_f32 v88,v88
v_exp_f32 v89,v89
v_fma_f32 v86,v86,v73,v73
v_fma_f32 v87,v87,v73,v73
v_fma_f32 v88,v88,v73,v73
v_fma_f32 v89,v89,v73,v73
v_rcp_f32 v86,v86
v_rcp_f32 v87,v87
v_rcp_f32 v88,v88
v_rcp_f32 v89,v89
v_mul_f32 v30,v30,v86
v_mul_f32 v31,v31,v87
v_mul_f32 v32,v32,v88
v_mul_f32 v33,v33,v89
v_cvt_pk_bf16_f32 v92,v30,v31
v_cvt_pk_bf16_f32 v93,v32,v33
buffer_store_dwordx4 v[90:93],v0,s[36:39],s67 offen offset:0 sc0 sc1
s_mov_b32 s72,s69
s_mov_b32 s69,0x7fff0000
s_mov_b32 s67,0x7fff0000
s_mov_b32 s77,0x7fff0000
s_branch Lg1_done_%=
Lg1_L10_%=:
v_mfma_f32_16x16x32_bf16 v[18:21],a[0:3],v[34:37],0
s_add_u32 s58,s58,s61
s_add_u32 m0,s59,0
R 62,1,7168
v_mfma_f32_16x16x32_bf16 v[22:25],a[4:7],v[34:37],0
s_mov_b32 s66,s65
buffer_load_dword v5,s[36:39],s58 offen offset:0 sc1 lds
M 18,a[8:11],38
s_mov_b32 m0,s57
R 34,1,8192
v_fma_f32 v26,-v70,v74,v26
M 22,a[12:15],38
L 5
buffer_load_dwordx4 v[66:69],v11,s[36:39],s73 offen offset:0 sc1
M 18,a[16:19],42
s_add_u32 s73,s73,s64
R 38,1,9216
v_fma_f32 v27,-v70,v75,v27
M 22,a[20:23],42
s_add_u32 s56,s55,4096
v_fma_f32 v28,-v70,v76,v28
v_fma_f32 v29,-v70,v77,v29
M 18,a[24:27],46
R 42,1,10240
v_mul_f32 v82,v26,v26
M 22,a[28:31],46
L 5
D 0,55,0
M 18,a[32:35],50
R 46,1,11264
v_mul_f32 v83,v27,v27
M 22,a[36:39],50
v_mul_f32 v84,v28,v28
v_mul_f32 v85,v29,v29
M 18,a[40:43],54
R 50,1,12288
v_fma_f32 v82,v72,v82,v71
M 22,a[44:47],54
L 5
D 0,55,1024
M 18,a[48:51],58
R 54,1,13312
v_fma_f32 v83,v72,v83,v71
M 22,a[52:55],58
v_fma_f32 v84,v72,v84,v71
v_fma_f32 v85,v72,v85,v71
M 18,a[56:59],62
R 58,1,14336
v_mul_f32 v82,v26,v82
)"
R"(M 22,a[60:63],62
L 5
D 0,55,2048
M 18,a[64:67],34
R 62,1,15360
v_mul_f32 v83,v27,v83
M 22,a[68:71],34
v_exp_f32 v82,v82
M 18,a[72:75],38
R 34,1,16384
v_mul_f32 v84,v28,v84
M 22,a[76:79],38
L 5
D 0,55,3072
M 18,a[80:83],42
s_add_u32 m0,s57,4096
R 38,1,17408
v_mul_f32 v85,v29,v85
M 22,a[84:87],42
s_add_u32 s55,s55,s60
v_exp_f32 v83,v83
M 18,a[88:91],46
s_add_u32 s57,s57,32768
R 42,1,18432
v_fma_f32 v82,v82,v73,v73
M 22,a[92:95],46
L 5
D 0,56,0
M 18,a[96:99],50
s_and_b32 s57,s57,131071
R 46,1,19456
v_fma_f32 v83,v83,v73,v73
M 22,a[100:103],50
s_add_u32 s54,s54,s46
v_exp_f32 v84,v84
M 18,a[104:107],54
s_add_u32 s65,s65,s62
R 50,1,20480
v_fma_f32 v84,v84,v73,v73
M 22,a[108:111],54
L 5
D 0,56,1024
M 18,a[112:115],58
R 54,1,21504
v_fma_f32 v30,-v70,v78,v30
M 22,a[116:119],58
v_exp_f32 v85,v85
M 18,a[120:123],62
R 58,1,22528
v_fma_f32 v85,v85,v73,v73
M 22,a[124:127],62
L 5
D 0,56,2048
M 18,a[128:131],34
R 62,1,23552
v_fma_f32 v31,-v70,v79,v31
M 22,a[132:135],34
v_rcp_f32 v82,v82
M 18,a[136:139],38
R 34,1,24576
v_mul_f32 v26,v26,v82
M 22,a[140:143],38
L 5
D 0,56,3072
M 18,a[144:147],42
R 38,1,25600
v_fma_f32 v32,-v70,v80,v32
M 22,a[148:151],42
v_rcp_f32 v83,v83
M 18,a[152:155],46
R 42,1,26624
v_mul_f32 v27,v27,v83
M 22,a[156:159],46
L 5
v_rcp_f32 v84,v84
M 18,a[160:163],50
R 46,1,27648
v_mul_f32 v28,v28,v84
M 22,a[164:167],50
v_rcp_f32 v85,v85
M 18,a[168:171],54
R 50,1,28672
v_mul_f32 v29,v29,v85
M 22,a[172:175],54
L 5
v_cvt_pk_bf16_f32 v90,v26,v27
v_cvt_pk_bf16_f32 v91,v28,v29
M 18,a[176:179],58
R 54,1,29696
v_fma_f32 v33,-v70,v81,v33
M 22,a[180:183],58
v_mul_f32 v86,v30,v30
v_mul_f32 v87,v31,v31
M 18,a[184:187],62
R 58,1,30720
v_mul_f32 v88,v32,v32
M 22,a[188:191],62
L 5
v_mul_f32 v89,v33,v33
v_fma_f32 v86,v72,v86,v71
M 18,a[192:195],34
s_waitcnt vmcnt(0)
R 62,1,31744
v_fma_f32 v87,v72,v87,v71
M 22,a[196:199],34
s_barrier
ds_read_b32 v7,v6 offset:0
v_fma_f32 v88,v72,v88,v71
M 18,a[200:203],38
R 34,2,0
v_fma_f32 v89,v72,v89,v71
M 22,a[204:207],38
L 6
buffer_store_dword v4,v3,s[36:39],s70 offen offset:0 sc0 sc1
M 18,a[208:211],42
s_mov_b32 s70,s69
R 38,2,1024
v_mul_f32 v86,v30,v86
M 22,a[212:215],42
s_mov_b32 s69,s68
v_exp_f32 v86,v86
M 18,a[216:219],46
L 2
R 42,2,2048
v_mul_f32 v87,v31,v87
M 22,a[220:223],46
s_nop 0
v_exp_f32 v87,v87
M 18,a[224:227],50
s_add_u32 s68,s68,s63
R 46,2,3072
v_mul_f32 v88,v32,v88
M 22,a[228:231],50
v_mul_f32 v89,v33,v89
v_fma_f32 v86,v86,v73,v73
M 18,a[232:235],54
R 50,2,4096
v_fma_f32 v87,v87,v73,v73
M 22,a[236:239],54
s_nop 0
v_exp_f32 v88,v88
M 18,a[240:243],58
R 54,2,5120
v_exp_f32 v89,v89
M 22,a[244:247],58
v_fma_f32 v88,v88,v73,v73
v_fma_f32 v89,v89,v73,v73
v_rcp_f32 v86,v86
v_rcp_f32 v87,v87
v_rcp_f32 v88,v88
v_rcp_f32 v89,v89
v_cmp_eq_u32 vcc,s53,v7
M 18,a[248:251],62
s_cmp_eq_u64 vcc,-1
R 58,2,6144
v_mul_f32 v30,v30,v86
v_mul_f32 v31,v31,v87
v_mul_f32 v32,v32,v88
v_mul_f32 v33,v33,v89
v_cvt_pk_bf16_f32 v92,v30,v31
v_cvt_pk_bf16_f32 v93,v32,v33
M 22,a[252:255],62
L 5
buffer_store_dwordx4 v[90:93],v0,s[36:39],s67 offen offset:0 sc0 sc1
v_xor_b32 v1,65536,v1
s_cbranch_scc1 Lg1_L21_%=
s_cmp_eq_u32 vcc_lo,-1
s_cbranch_scc1 Lg1_L11_%=
s_branch Lg1_X1_%=
Lg1_L11_%=:
v_mfma_f32_16x16x32_bf16 v[26:29],a[0:3],v[34:37],0
s_add_u32 s58,s58,s61
s_add_u32 m0,s59,1024
R 62,2,7168
v_mfma_f32_16x16x32_bf16 v[30:33],a[4:7],v[34:37],0
s_mov_b32 s67,s65
buffer_load_dword v5,s[36:39],s58 offen offset:0 sc1 lds
M 26,a[8:11],38
s_mov_b32 m0,s57
R 34,2,8192
v_fma_f32 v18,-v66,v74,v18
M 30,a[12:15],38
L 5
buffer_load_dwordx4 v[70:73],v11,s[36:39],s73 offen offset:0 sc1
M 26,a[16:19],42
s_add_u32 s73,s73,s64
R 38,2,9216
v_fma_f32 v19,-v66,v75,v19
M 30,a[20:23],42
s_add_u32 s56,s55,4096
v_fma_f32 v20,-v66,v76,v20
v_fma_f32 v21,-v66,v77,v21
M 26,a[24:27],46
R 42,2,10240
v_mul_f32 v82,v18,v18
M 30,a[28:31],46
L 5
D 0,55,0
M 26,a[32:35],50
R 46,2,11264
v_mul_f32 v83,v19,v19
M 30,a[36:39],50
v_mul_f32 v84,v20,v20
v_mul_f32 v85,v21,v21
M 26,a[40:43],54
R 50,2,12288
v_fma_f32 v82,v68,v82,v67
M 30,a[44:47],54
L 5
D 0,55,1024
M 26,a[48:51],58
R 54,2,13312
v_fma_f32 v83,v68,v83,v67
M 30,a[52:55],58
v_fma_f32 v84,v68,v84,v67
v_fma_f32 v85,v68,v85,v67
M 26,a[56:59],62
R 58,2,14336
v_mul_f32 v82,v18,v82
M 30,a[60:63],62
L 5
D 0,55,2048
M 26,a[64:67],34
R 62,2,15360
v_mul_f32 v83,v19,v83
M 30,a[68:71],34
v_exp_f32 v82,v82
M 26,a[72:75],38
R 34,2,16384
v_mul_f32 v84,v20,v84
M 30,a[76:79],38
L 5
D 0,55,3072
M 26,a[80:83],42
s_add_u32 m0,s57,4096
R 38,2,17408
v_mul_f32 v85,v21,v85
M 30,a[84:87],42
s_add_u32 s55,s55,s60
v_exp_f32 v83,v83
M 26,a[88:91],46
s_add_u32 s57,s57,32768
R 42,2,18432
v_fma_f32 v82,v82,v69,v69
M 30,a[92:95],46
L 5
D 0,56,0
M 26,a[96:99],50
s_and_b32 s57,s57,131071
R 46,2,19456
v_fma_f32 v83,v83,v69,v69
M 30,a[100:103],50
s_add_u32 s54,s54,s46
v_exp_f32 v84,v84
M 26,a[104:107],54
s_add_u32 s65,s65,s62
R 50,2,20480
v_fma_f32 v84,v84,v69,v69
M 30,a[108:111],54
L 5
D 0,56,1024
M 26,a[112:115],58
R 54,2,21504
v_fma_f32 v22,-v66,v78,v22
M 30,a[116:119],58
v_exp_f32 v85,v85
M 26,a[120:123],62
R 58,2,22528
v_fma_f32 v85,v85,v69,v69
M 30,a[124:127],62
L 5
D 0,56,2048
M 26,a[128:131],34
R 62,2,23552
v_fma_f32 v23,-v66,v79,v23
M 30,a[132:135],34
v_rcp_f32 v82,v82
M 26,a[136:139],38
R 34,2,24576
v_mul_f32 v18,v18,v82
M 30,a[140:143],38
L 5
D 0,56,3072
M 26,a[144:147],42
R 38,2,25600
v_fma_f32 v24,-v66,v80,v24
M 30,a[148:151],42
v_rcp_f32 v83,v83
M 26,a[152:155],46
R 42,2,26624
v_mul_f32 v19,v19,v83
M 30,a[156:159],46
L 5
v_rcp_f32 v84,v84
M 26,a[160:163],50
R 46,2,27648
v_mul_f32 v20,v20,v84
M 30,a[164:167],50
v_rcp_f32 v85,v85
M 26,a[168:171],54
R 50,2,28672
v_mul_f32 v21,v21,v85
M 30,a[172:175],54
L 5
v_cvt_pk_bf16_f32 v90,v18,v19
v_cvt_pk_bf16_f32 v91,v20,v21
M 26,a[176:179],58
R 54,2,29696
v_fma_f32 v25,-v66,v81,v25
M 30,a[180:183],58
v_mul_f32 v86,v22,v22
v_mul_f32 v87,v23,v23
M 26,a[184:187],62
R 58,2,30720
v_mul_f32 v88,v24,v24
M 30,a[188:191],62
L 5
v_mul_f32 v89,v25,v25
v_fma_f32 v86,v68,v86,v67
M 26,a[192:195],34
s_waitcnt vmcnt(0)
R 62,2,31744
v_fma_f32 v87,v68,v87,v67
M 30,a[196:199],34
s_barrier
ds_read_b32 v7,v6 offset:1024
v_fma_f32 v88,v68,v88,v67
M 26,a[200:203],38
R 34,1,0
v_fma_f32 v89,v68,v89,v67
M 30,a[204:207],38
L 6
buffer_store_dword v4,v3,s[36:39],s70 offen offset:0 sc0 sc1
M 26,a[208:211],42
s_mov_b32 s70,s69
R 38,1,1024
v_mul_f32 v86,v22,v86
M 30,a[212:215],42
s_mov_b32 s69,s68
v_exp_f32 v86,v86
M 26,a[216:219],46
L 2
R 42,1,2048
v_mul_f32 v87,v23,v87
M 30,a[220:223],46
s_nop 0
v_exp_f32 v87,v87
M 26,a[224:227],50
s_add_u32 s68,s68,s63
R 46,1,3072
v_mul_f32 v88,v24,v88
M 30,a[228:231],50
v_mul_f32 v89,v25,v89
v_fma_f32 v86,v86,v69,v69
M 26,a[232:235],54
R 50,1,4096
v_fma_f32 v87,v87,v69,v69
M 30,a[236:239],54
s_nop 0
v_exp_f32 v88,v88
M 26,a[240:243],58
R 54,1,5120
v_exp_f32 v89,v89
M 30,a[244:247],58
v_fma_f32 v88,v88,v69,v69
v_fma_f32 v89,v89,v69,v69
v_rcp_f32 v86,v86
v_rcp_f32 v87,v87
v_rcp_f32 v88,v88
v_rcp_f32 v89,v89
v_cmp_eq_u32 vcc,s53,v7
M 26,a[248:251],62
s_cmp_eq_u64 vcc,-1
R 58,1,6144
v_mul_f32 v22,v22,v86
v_mul_f32 v23,v23,v87
v_mul_f32 v24,v24,v88
v_mul_f32 v25,v25,v89
v_cvt_pk_bf16_f32 v92,v22,v23
v_cvt_pk_bf16_f32 v93,v24,v25
M 30,a[252:255],62
L 5
buffer_store_dwordx4 v[90:93],v0,s[36:39],s66 offen offset:0 sc0 sc1
v_xor_b32 v2,65536,v2
s_cbranch_scc1 Lg1_L20_%=
s_cmp_eq_u32 vcc_lo,-1
s_cbranch_scc1 Lg1_L10_%=
s_branch Lg1_X0_%=
Lg1_done_%=:
s_waitcnt vmcnt(1)
s_barrier
buffer_store_dword v4,v3,s[36:39],s70 offen offset:0 sc0 sc1
s_waitcnt vmcnt(1)
s_barrier
buffer_store_dword v4,v3,s[36:39],s72 offen offset:0 sc0 sc1
s_waitcnt vmcnt(0) lgkmcnt(0)
s_barrier
s_branch Lg2_end_%=
Lg2_begin_%=:
Lcode_g2_b:
s_mov_b64 s[36:37],%1
s_mov_b32 s38,111243392
s_mov_b32 s39,0x00020000
s_mov_b32 s44,%2
s_mov_b32 s45,%3
s_mov_b32 s46,%4
s_mov_b32 s47,%5
s_mov_b32 s48,%6
s_mov_b32 s49,%7
s_mov_b32 s50,%8
s_mov_b32 s51,%9
s_mov_b32 s93,%10
v_mov_b32 v0,%11
v_mov_b32 v3,%12
v_mov_b32 v5,%13
v_mov_b32 v6,%14
v_mov_b32 v18,%15
v_mov_b32 v19,%16
v_mov_b32 v20,%17
v_mov_b32 v21,%18
v_mov_b32 v22,%19
v_mov_b32 v10,%20
v_mov_b32 v11,%21
v_mov_b32 v12,%22
v_mov_b32 v13,%23
v_mov_b32 v14,%24
v_mov_b32 v15,%25
v_mov_b32 v16,%26
s_mov_b64 s[40:41],%27
s_mov_b32 s42,2097152
s_mov_b32 s43,0x00020000
s_lshl_b32 s57,s44,11
s_mov_b32 s79,111243264
buffer_load_dword v4,off,s[36:39],s79 sc1
s_add_u32 s55,s50,0
s_add_u32 m0,s57,0
s_nop 0
buffer_load_dwordx4 v18,s[40:43],s55 offen offset:0  lds
s_add_u32 m0,s57,1024
s_nop 0
buffer_load_dwordx4 v19,s[40:43],s55 offen offset:0  lds
s_add_u32 m0,s57,8064
s_nop 0
buffer_load_dwordx4 v18,s[40:43],s55 offen offset:128  lds
s_add_u32 m0,s57,9088
s_nop 0
buffer_load_dwordx4 v19,s[40:43],s55 offen offset:128  lds
s_add_u32 s55,s50,131072
s_add_u32 m0,s57,16384
s_nop 0
buffer_load_dwordx4 v18,s[40:43],s55 offen offset:0  lds
s_add_u32 m0,s57,17408
s_nop 0
buffer_load_dwordx4 v19,s[40:43],s55 offen offset:0  lds
s_add_u32 m0,s57,24448
s_nop 0
buffer_load_dwordx4 v18,s[40:43],s55 offen offset:128  lds
s_add_u32 m0,s57,25472
s_nop 0
buffer_load_dwordx4 v19,s[40:43],s55 offen offset:128  lds
s_waitcnt vmcnt(8)
s_barrier
v_readfirstlane_b32 s53,v4
s_nop 3
s_add_u32 s53,s53,1
s_cmp_eq_u32 s53,0
s_cselect_b32 s53,1,s53
v_mov_b32 v4,s53
buffer_store_dword v4,v3,s[36:39],s93 offen offset:0 sc0 sc1
s_add_u32 s55,s50,262144
s_add_u32 m0,s57,32768
s_nop 0
buffer_load_dwordx4 v18,s[40:43],s55 offen offset:0  lds
s_add_u32 m0,s57,33792
s_nop 0
buffer_load_dwordx4 v19,s[40:43],s55 offen offset:0  lds
s_add_u32 m0,s57,40832
s_nop 0
buffer_load_dwordx4 v18,s[40:43],s55 offen offset:128  lds
s_add_u32 m0,s57,41856
s_nop 0
buffer_load_dwordx4 v19,s[40:43],s55 offen offset:128  lds
s_add_u32 s55,s50,393216
s_add_u32 m0,s57,49152
s_nop 0
buffer_load_dwordx4 v18,s[40:43],s55 offen offset:0  lds
s_add_u32 m0,s57,50176
s_nop 0
buffer_load_dwordx4 v19,s[40:43],s55 offen offset:0  lds
s_add_u32 m0,s57,57216
s_nop 0
buffer_load_dwordx4 v18,s[40:43],s55 offen offset:128  lds
s_add_u32 m0,s57,58240
s_nop 0
buffer_load_dwordx4 v19,s[40:43],s55 offen offset:128  lds
s_add_u32 s55,s50,524288
s_add_u32 m0,s57,65536
s_nop 0
buffer_load_dwordx4 v18,s[40:43],s55 offen offset:0  lds
s_add_u32 m0,s57,66560
)"
R"(s_nop 0
buffer_load_dwordx4 v19,s[40:43],s55 offen offset:0  lds
s_add_u32 m0,s57,73600
s_nop 0
buffer_load_dwordx4 v18,s[40:43],s55 offen offset:128  lds
s_add_u32 m0,s57,74624
s_nop 0
buffer_load_dwordx4 v19,s[40:43],s55 offen offset:128  lds
s_add_u32 s55,s50,655360
s_add_u32 m0,s57,81920
s_nop 0
buffer_load_dwordx4 v18,s[40:43],s55 offen offset:0  lds
s_add_u32 m0,s57,82944
s_nop 0
buffer_load_dwordx4 v19,s[40:43],s55 offen offset:0  lds
s_add_u32 m0,s57,89984
s_nop 0
buffer_load_dwordx4 v18,s[40:43],s55 offen offset:128  lds
s_add_u32 m0,s57,91008
s_nop 0
buffer_load_dwordx4 v19,s[40:43],s55 offen offset:128  lds
s_waitcnt vmcnt(21)
L 0
s_barrier
s_add_u32 s55,s50,786432
s_add_u32 m0,s57,98304
s_nop 0
buffer_load_dwordx4 v18,s[40:43],s55 offen offset:0  lds
s_add_u32 m0,s57,99328
s_nop 0
buffer_load_dwordx4 v19,s[40:43],s55 offen offset:0  lds
s_add_u32 m0,s57,106368
s_nop 0
buffer_load_dwordx4 v18,s[40:43],s55 offen offset:128  lds
s_add_u32 m0,s57,107392
s_nop 0
buffer_load_dwordx4 v19,s[40:43],s55 offen offset:128  lds
v_add_u32 v26,0,v20
v_add_u32 v27,0,v21
T a[0:1],26,0
T a[2:3],26,512
T a[4:5],27,0
T a[6:7],27,512
T a[8:9],26,4096
T a[10:11],26,4608
T a[12:13],27,4096
T a[14:15],27,4608
s_waitcnt vmcnt(21)
L 0
s_barrier
s_add_u32 s55,s50,917504
s_add_u32 m0,s57,0
s_nop 0
buffer_load_dwordx4 v18,s[40:43],s55 offen offset:0  lds
s_add_u32 m0,s57,1024
s_nop 0
buffer_load_dwordx4 v19,s[40:43],s55 offen offset:0  lds
s_add_u32 m0,s57,8064
s_nop 0
buffer_load_dwordx4 v18,s[40:43],s55 offen offset:128  lds
s_add_u32 m0,s57,9088
s_nop 0
buffer_load_dwordx4 v19,s[40:43],s55 offen offset:128  lds
v_add_u32 v26,16384,v20
v_add_u32 v27,16384,v21
T a[16:17],26,0
T a[18:19],26,512
T a[20:21],27,0
T a[22:23],27,512
T a[24:25],26,4096
T a[26:27],26,4608
T a[28:29],27,4096
T a[30:31],27,4608
s_waitcnt vmcnt(20)
L 0
s_barrier
s_add_u32 s55,s50,1048576
s_add_u32 m0,s57,16384
s_nop 0
buffer_load_dwordx4 v18,s[40:43],s55 offen offset:0  lds
s_add_u32 m0,s57,17408
s_nop 0
buffer_load_dwordx4 v19,s[40:43],s55 offen offset:0  lds
s_add_u32 m0,s57,24448
s_nop 0
buffer_load_dwordx4 v18,s[40:43],s55 offen offset:128  lds
s_add_u32 m0,s57,25472
s_nop 0
buffer_load_dwordx4 v19,s[40:43],s55 offen offset:128  lds
v_add_u32 v26,32768,v20
v_add_u32 v27,32768,v21
T a[32:33],26,0
T a[34:35],26,512
T a[36:37],27,0
T a[38:39],27,512
T a[40:41],26,4096
T a[42:43],26,4608
T a[44:45],27,4096
T a[46:47],27,4608
s_waitcnt vmcnt(20)
L 0
s_barrier
s_add_u32 s55,s50,1179648
s_add_u32 m0,s57,32768
s_nop 0
buffer_load_dwordx4 v18,s[40:43],s55 offen offset:0  lds
s_add_u32 m0,s57,33792
s_nop 0
buffer_load_dwordx4 v19,s[40:43],s55 offen offset:0  lds
s_add_u32 m0,s57,40832
s_nop 0
buffer_load_dwordx4 v18,s[40:43],s55 offen offset:128  lds
s_add_u32 m0,s57,41856
s_nop 0
buffer_load_dwordx4 v19,s[40:43],s55 offen offset:128  lds
v_add_u32 v26,49152,v20
v_add_u32 v27,49152,v21
T a[48:49],26,0
T a[50:51],26,512
T a[52:53],27,0
T a[54:55],27,512
T a[56:57],26,4096
T a[58:59],26,4608
T a[60:61],27,4096
T a[62:63],27,4608
s_waitcnt vmcnt(20)
L 0
s_barrier
s_add_u32 s55,s50,1310720
s_add_u32 m0,s57,49152
s_nop 0
buffer_load_dwordx4 v18,s[40:43],s55 offen offset:0  lds
s_add_u32 m0,s57,50176
s_nop 0
buffer_load_dwordx4 v19,s[40:43],s55 offen offset:0  lds
s_add_u32 m0,s57,57216
s_nop 0
buffer_load_dwordx4 v18,s[40:43],s55 offen offset:128  lds
s_add_u32 m0,s57,58240
s_nop 0
buffer_load_dwordx4 v19,s[40:43],s55 offen offset:128  lds
v_add_u32 v26,65536,v20
v_add_u32 v27,65536,v21
T a[64:65],26,0
T a[66:67],26,512
T a[68:69],27,0
T a[70:71],27,512
T a[72:73],26,4096
T a[74:75],26,4608
T a[76:77],27,4096
T a[78:79],27,4608
s_waitcnt vmcnt(20)
L 0
s_barrier
s_add_u32 s55,s50,1441792
s_add_u32 m0,s57,65536
s_nop 0
buffer_load_dwordx4 v18,s[40:43],s55 offen offset:0  lds
s_add_u32 m0,s57,66560
s_nop 0
buffer_load_dwordx4 v19,s[40:43],s55 offen offset:0  lds
s_add_u32 m0,s57,73600
s_nop 0
buffer_load_dwordx4 v18,s[40:43],s55 offen offset:128  lds
s_add_u32 m0,s57,74624
s_nop 0
buffer_load_dwordx4 v19,s[40:43],s55 offen offset:128  lds
v_add_u32 v26,81920,v20
v_add_u32 v27,81920,v21
T a[80:81],26,0
T a[82:83],26,512
T a[84:85],27,0
T a[86:87],27,512
T a[88:89],26,4096
T a[90:91],26,4608
T a[92:93],27,4096
T a[94:95],27,4608
s_waitcnt vmcnt(20)
L 0
s_barrier
s_add_u32 s55,s50,1572864
s_add_u32 m0,s57,81920
s_nop 0
buffer_load_dwordx4 v18,s[40:43],s55 offen offset:0  lds
s_add_u32 m0,s57,82944
s_nop 0
buffer_load_dwordx4 v19,s[40:43],s55 offen offset:0  lds
s_add_u32 m0,s57,89984
s_nop 0
buffer_load_dwordx4 v18,s[40:43],s55 offen offset:128  lds
s_add_u32 m0,s57,91008
s_nop 0
buffer_load_dwordx4 v19,s[40:43],s55 offen offset:128  lds
v_add_u32 v26,98304,v20
v_add_u32 v27,98304,v21
T a[96:97],26,0
T a[98:99],26,512
T a[100:101],27,0
T a[102:103],27,512
T a[104:105],26,4096
T a[106:107],26,4608
T a[108:109],27,4096
T a[110:111],27,4608
s_waitcnt vmcnt(20)
L 0
s_barrier
s_add_u32 s55,s50,1703936
s_add_u32 m0,s57,98304
s_nop 0
buffer_load_dwordx4 v18,s[40:43],s55 offen offset:0  lds
s_add_u32 m0,s57,99328
s_nop 0
buffer_load_dwordx4 v19,s[40:43],s55 offen offset:0  lds
s_add_u32 m0,s57,106368
s_nop 0
buffer_load_dwordx4 v18,s[40:43],s55 offen offset:128  lds
s_add_u32 m0,s57,107392
s_nop 0
buffer_load_dwordx4 v19,s[40:43],s55 offen offset:128  lds
v_add_u32 v26,0,v20
v_add_u32 v27,0,v21
T a[112:113],26,0
T a[114:115],26,512
T a[116:117],27,0
T a[118:119],27,512
T a[120:121],26,4096
T a[122:123],26,4608
T a[124:125],27,4096
T a[126:127],27,4608
s_waitcnt vmcnt(20)
L 0
s_barrier
s_add_u32 s55,s50,1835008
s_add_u32 m0,s57,0
s_nop 0
buffer_load_dwordx4 v18,s[40:43],s55 offen offset:0  lds
s_add_u32 m0,s57,1024
s_nop 0
buffer_load_dwordx4 v19,s[40:43],s55 offen offset:0  lds
s_add_u32 m0,s57,8064
s_nop 0
buffer_load_dwordx4 v18,s[40:43],s55 offen offset:128  lds
s_add_u32 m0,s57,9088
s_nop 0
buffer_load_dwordx4 v19,s[40:43],s55 offen offset:128  lds
v_add_u32 v26,16384,v20
v_add_u32 v27,16384,v21
T a[128:129],26,0
T a[130:131],26,512
T a[132:133],27,0
T a[134:135],27,512
T a[136:137],26,4096
T a[138:139],26,4608
T a[140:141],27,4096
T a[142:143],27,4608
s_waitcnt vmcnt(20)
L 0
s_barrier
s_add_u32 s55,s50,1966080
s_add_u32 m0,s57,16384
s_nop 0
buffer_load_dwordx4 v18,s[40:43],s55 offen offset:0  lds
s_add_u32 m0,s57,17408
s_nop 0
buffer_load_dwordx4 v19,s[40:43],s55 offen offset:0  lds
s_add_u32 m0,s57,24448
s_nop 0
buffer_load_dwordx4 v18,s[40:43],s55 offen offset:128  lds
s_add_u32 m0,s57,25472
s_nop 0
buffer_load_dwordx4 v19,s[40:43],s55 offen offset:128  lds
v_add_u32 v26,32768,v20
v_add_u32 v27,32768,v21
T a[144:145],26,0
T a[146:147],26,512
T a[148:149],27,0
T a[150:151],27,512
T a[152:153],26,4096
T a[154:155],26,4608
T a[156:157],27,4096
T a[158:159],27,4608
s_waitcnt vmcnt(20)
L 0
s_barrier
v_add_u32 v26,49152,v20
v_add_u32 v27,49152,v21
T a[160:161],26,0
T a[162:163],26,512
T a[164:165],27,0
T a[166:167],27,512
T a[168:169],26,4096
T a[170:171],26,4608
T a[172:173],27,4096
T a[174:175],27,4608
s_waitcnt vmcnt(16)
L 0
s_barrier
v_add_u32 v26,65536,v20
v_add_u32 v27,65536,v21
T a[176:177],26,0
T a[178:179],26,512
T a[180:181],27,0
T a[182:183],27,512
T a[184:185],26,4096
T a[186:187],26,4608
T a[188:189],27,4096
T a[190:191],27,4608
s_waitcnt vmcnt(12)
L 0
s_barrier
v_add_u32 v26,81920,v20
v_add_u32 v27,81920,v21
T a[192:193],26,0
T a[194:195],26,512
T a[196:197],27,0
T a[198:199],27,512
T a[200:201],26,4096
T a[202:203],26,4608
T a[204:205],27,4096
T a[206:207],27,4608
s_waitcnt vmcnt(8)
L 0
s_barrier
v_add_u32 v26,98304,v20
v_add_u32 v27,98304,v21
T a[208:209],26,0
T a[210:211],26,512
T a[212:213],27,0
T a[214:215],27,512
T a[216:217],26,4096
T a[218:219],26,4608
T a[220:221],27,4096
T a[222:223],27,4608
s_waitcnt vmcnt(4)
L 0
s_barrier
v_add_u32 v26,0,v20
v_add_u32 v27,0,v21
T a[224:225],26,0
T a[226:227],26,512
T a[228:229],27,0
T a[230:231],27,512
T a[232:233],26,4096
T a[234:235],26,4608
T a[236:237],27,4096
T a[238:239],27,4608
s_waitcnt vmcnt(0)
L 0
s_barrier
v_add_u32 v26,16384,v20
v_add_u32 v27,16384,v21
T a[240:241],26,0
T a[242:243],26,512
T a[244:245],27,0
T a[246:247],27,512
T a[248:249],26,4096
T a[250:251],26,4608
T a[252:253],27,4096
T a[254:255],27,4608
L 0
s_barrier
v_mov_b32 v4,s53
s_mov_b32 s82,0
s_mov_b32 s80,0
s_mov_b32 s54,s45
s_mul_i32 s57,s44,8192
s_mul_i32 s55,s45,32768
s_add_u32 s55,s55,s57
s_add_u32 s55,s55,21987328
s_mul_i32 s60,s46,32768
s_mul_i32 s61,s46,1024
s_mul_i32 s62,s46,32768
s_mul_i32 s63,s46,1024
s_mul_i32 s64,s46,512
s_lshl_b32 s59,s44,8
s_add_u32 s59,s59,131072
s_mul_i32 s58,s45,1024
s_add_u32 s58,s58,109333504
s_mul_i32 s65,s45,32768
s_add_u32 s65,s65,s47
s_mul_i32 s68,s45,1024
s_add_u32 s68,s68,s48
s_mul_i32 s73,s45,512
s_add_u32 s73,s73,106954752
s_mul_i32 s74,s45,32768
s_add_u32 s74,s74,s49
s_mul_i32 s75,s45,1024
s_add_u32 s75,s75,107479040
s_mov_b32 s66,0x7fff0000
s_mov_b32 s67,0x7fff0000
s_mov_b32 s69,0x7fff0000
s_mov_b32 s76,0x7fff0000
s_mov_b32 s77,0x7fff0000
s_mov_b32 s70,0x7fff0000
s_mov_b32 s71,0x7fff0000
s_mov_b32 s72,0x7fff0000
v_mov_b32 v1,v0
v_add_u32 v2,32768,v0
s_mov_b32 s84,32768
s_mov_b32 s86,0x3a800000
s_mov_b32 s87,0x38a7c5ac
s_mov_b32 s88,0xc0135761
s_mov_b32 s89,0x3d372713
s_and_b32 s85,s44,1
s_mul_i32 s85,s85,127
v_lshlrev_b32 v8,22,v0
v_and_b32 v8,0x80000000,v8
v_or_b32 v8,v8,v5
s_and_b32 s79,s54,127
s_cmp_eq_u32 s79,0
s_cselect_b32 s94,0x3300,0
s_cmp_eq_u32 s79,127
s_cselect_b32 s94,0xcc00,s94
s_add_u32 s79,s54,s46
s_and_b32 s79,s79,127
s_cmp_eq_u32 s79,0
s_cselect_b32 s95,0x3300,0
s_cmp_eq_u32 s79,127
s_cselect_b32 s95,0xcc00,s95
s_add_u32 m0,s59,4096
s_nop 0
buffer_load_dword v5,s[36:39],s58 offen offset:0 sc1 lds
buffer_store_dword v4,v3,s[36:39],s71 offen offset:0 sc0 sc1
s_add_u32 m0,s59,5120
s_nop 0
buffer_load_dword v5,s[36:39],s58 offen offset:0 sc1 lds
buffer_store_dword v4,v3,s[36:39],s71 offen offset:0 sc0 sc1
s_mov_b32 s83,0
Lg2_poll_%=:
s_add_u32 s78,s83,2
s_and_b32 s78,s78,3
s_lshl_b32 s78,s78,10
s_add_u32 s78,s78,4096
s_add_u32 m0,s59,s78
s_nop 0
buffer_load_dword v5,s[36:39],s58 offen offset:0 sc1 lds
s_and_b32 s78,s83,3
s_lshl_b32 s78,s78,10
v_add_u32 v74,s78,v6
s_waitcnt vmcnt(4)
s_barrier
ds_read_b32 v7,v74 offset:4096
buffer_store_dword v4,v3,s[36:39],s70 offen offset:0 sc0 sc1
s_mov_b32 s70,s71
s_mov_b32 s71,s72
s_mov_b32 s72,0x7fff0000
s_add_u32 s83,s83,1
s_add_u32 s82,s82,1
L 0
v_cmp_eq_u32 vcc,s53,v7
s_nop 0
s_or_b64 vcc,vcc,s[94:95]
s_cmp_eq_u32 vcc_lo,-1
s_cbranch_scc1 Lg2_go_%=
s_cmp_lt_u32 s82,20000
s_cbranch_scc1 Lg2_poll_%=
Lg2_go_%=:
s_and_b32 s78,s54,127
s_cmp_eq_u32 s78,s85
s_cselect_b64 s[40:41],-1,0
v_cndmask_b32 v17,v15,v16,s[40:41]
s_mov_b32 m0,s57
s_add_u32 s56,s55,4096
D 17,55,0
D 17,55,1024
D 17,55,2048
D 17,55,3072
s_add_u32 m0,s57,4096
s_nop 0
D 17,56,0
D 17,56,1024
D 17,56,2048
D 17,56,3072
s_add_u32 s55,s55,s60
s_add_u32 s57,s57,32768
s_and_b32 s57,s57,131071
s_add_u32 s54,s54,s46
Lg2_gox_%=:
s_add_u32 s58,s58,s61
s_cmp_eq_u32 vcc_hi,-1
s_cbranch_scc1 Lg2_go2_%=
s_add_u32 m0,s59,2048
s_nop 0
buffer_load_dword v5,s[36:39],s58 offen offset:0 sc1 lds
s_and_b32 s79,s54,127
s_cmp_eq_u32 s79,0
s_cselect_b32 s94,0x3300,0
s_cmp_eq_u32 s79,127
s_cselect_b32 s94,0xcc00,s94
s_add_u32 s79,s54,s46
s_and_b32 s79,s79,127
s_cmp_eq_u32 s79,0
s_cselect_b32 s95,0x3300,0
s_cmp_eq_u32 s79,127
s_cselect_b32 s95,0xcc00,s95
s_waitcnt vmcnt(0)
s_barrier
buffer_store_dword v4,v3,s[36:39],s70 offen offset:0 sc0 sc1
s_min_u32 s71,s71,s72
buffer_store_dword v4,v3,s[36:39],s71 offen offset:0 sc0 sc1
)"
R"(s_mov_b32 s70,0x7fff0000
s_mov_b32 s71,0x7fff0000
s_mov_b32 s72,0x7fff0000
s_cmp_eq_u32 s80,1
s_cbranch_scc1 Lg2_st11_%=
Lg2_st10_%=:
ds_read_b32 v7,v6 offset:2048
R 34,1,0
R 38,1,1024
R 42,1,2048
R 46,1,3072
R 50,1,4096
R 54,1,5120
R 58,1,6144
L 7
v_cmp_eq_u32 vcc,s53,v7
s_nop 0
s_or_b64 vcc,vcc,s[94:95]
L 5
s_cmp_eq_u64 vcc,-1
s_cbranch_scc1 Lg2_L20_%=
s_cmp_eq_u32 vcc_lo,-1
s_cbranch_scc1 Lg2_L10_%=
s_branch Lg2_X0_%=
Lg2_st11_%=:
ds_read_b32 v7,v6 offset:2048
R 34,2,0
R 38,2,1024
R 42,2,2048
R 46,2,3072
R 50,2,4096
R 54,2,5120
R 58,2,6144
L 7
v_cmp_eq_u32 vcc,s53,v7
s_nop 0
s_or_b64 vcc,vcc,s[94:95]
L 5
s_cmp_eq_u64 vcc,-1
s_cbranch_scc1 Lg2_L21_%=
s_cmp_eq_u32 vcc_lo,-1
s_cbranch_scc1 Lg2_L11_%=
s_branch Lg2_X1_%=
Lg2_go2_%=:
s_add_u32 s58,s58,s61
s_add_u32 m0,s59,2048
s_nop 0
buffer_load_dword v5,s[36:39],s58 offen offset:0 sc1 lds
s_and_b32 s78,s54,127
s_cmp_eq_u32 s78,s85
s_cselect_b64 s[40:41],-1,0
v_cndmask_b32 v17,v15,v16,s[40:41]
s_mov_b32 m0,s57
s_add_u32 s56,s55,4096
D 17,55,0
D 17,55,1024
D 17,55,2048
D 17,55,3072
s_add_u32 m0,s57,4096
s_nop 0
D 17,56,0
D 17,56,1024
D 17,56,2048
D 17,56,3072
s_add_u32 s55,s55,s60
s_add_u32 s57,s57,32768
s_and_b32 s57,s57,131071
s_add_u32 s54,s54,s46
s_and_b32 s79,s54,127
s_cmp_eq_u32 s79,0
s_cselect_b32 s94,0x3300,0
s_cmp_eq_u32 s79,127
s_cselect_b32 s94,0xcc00,s94
s_add_u32 s79,s54,s46
s_and_b32 s79,s79,127
s_cmp_eq_u32 s79,0
s_cselect_b32 s95,0x3300,0
s_cmp_eq_u32 s79,127
s_cselect_b32 s95,0xcc00,s95
s_waitcnt vmcnt(8)
s_barrier
buffer_store_dword v4,v3,s[36:39],s70 offen offset:0 sc0 sc1
s_min_u32 s71,s71,s72
buffer_store_dword v4,v3,s[36:39],s71 offen offset:0 sc0 sc1
s_mov_b32 s70,0x7fff0000
s_mov_b32 s71,0x7fff0000
s_mov_b32 s72,0x7fff0000
s_cmp_eq_u32 s80,1
s_cbranch_scc1 Lg2_st21_%=
Lg2_st20_%=:
ds_read_b32 v7,v6 offset:2048
R 34,1,0
R 38,1,1024
R 42,1,2048
R 46,1,3072
R 50,1,4096
R 54,1,5120
R 58,1,6144
L 7
v_cmp_eq_u32 vcc,s53,v7
s_nop 0
s_or_b64 vcc,vcc,s[94:95]
L 5
s_cmp_eq_u32 vcc_lo,-1
s_cbranch_scc1 Lg2_S0_%=
s_branch Lg2_N0_%=
Lg2_st21_%=:
ds_read_b32 v7,v6 offset:2048
R 34,2,0
R 38,2,1024
R 42,2,2048
R 46,2,3072
R 50,2,4096
R 54,2,5120
R 58,2,6144
L 7
v_cmp_eq_u32 vcc,s53,v7
s_nop 0
s_or_b64 vcc,vcc,s[94:95]
L 5
s_cmp_eq_u32 vcc_lo,-1
s_cbranch_scc1 Lg2_S1_%=
s_branch Lg2_N1_%=
Lg2_L20_%=:
v_mfma_f32_16x16x32_bf16 v[18:21],a[0:3],v[34:37],0
s_add_u32 s58,s58,s61
R 62,1,7168
v_lshlrev_b32 v74,16,v94
v_mfma_f32_16x16x32_bf16 v[22:25],a[4:7],v[34:37],0
s_add_u32 s58,s58,s61
buffer_load_dwordx4 v[90:93],v0,s[36:39],s74 offen offset:0 sc1
M 18,a[8:11],38
s_add_u32 m0,s59,0
R 34,1,8192
v_and_b32 v75,0xffff0000,v94
M 22,a[12:15],38
L 5
buffer_load_dword v8,s[36:39],s58 offen offset:0 sc1 lds
M 18,a[16:19],42
s_mov_b32 m0,s57
R 38,1,9216
v_lshlrev_b32 v76,16,v95
M 22,a[20:23],42
s_mov_b32 s66,s65
buffer_load_dwordx2 v[66:67],v11,s[36:39],s73 offen offset:16 sc1
M 18,a[24:27],46
s_add_u32 s73,s73,s64
R 42,1,10240
v_and_b32 v77,0xffff0000,v95
M 22,a[28:31],46
L 5
v_lshlrev_b32 v78,16,v96
v_and_b32 v79,0xffff0000,v96
M 18,a[32:35],50
s_add_u32 s74,s74,s62
R 46,1,11264
v_lshlrev_b32 v80,16,v97
M 22,a[36:39],50
s_and_b32 s78,s54,127
v_and_b32 v81,0xffff0000,v97
v_fma_f32 v74,v74,v71,v70
M 18,a[40:43],54
s_cmp_eq_u32 s78,s85
R 50,1,12288
v_fma_f32 v75,v75,v71,v70
M 22,a[44:47],54
L 5
v_fma_f32 v76,v76,v71,v70
v_fma_f32 v77,v77,v71,v70
M 18,a[48:51],58
s_cselect_b64 s[40:41],-1,0
R 54,1,13312
v_cndmask_b32 v17,v15,v16,s[40:41]
M 22,a[52:55],58
s_add_u32 s56,s55,4096
D 17,55,0
M 18,a[56:59],62
s_mov_b32 s76,s75
R 58,1,14336
v_fma_f32 v78,v78,v71,v70
M 22,a[60:63],62
L 5
D 17,55,1024
M 18,a[64:67],34
R 62,1,15360
v_fma_f32 v79,v79,v71,v70
M 22,a[68:71],34
D 17,55,2048
M 18,a[72:75],38
R 34,1,16384
v_fma_f32 v80,v80,v71,v70
M 22,a[76:79],38
L 5
D 17,55,3072
M 18,a[80:83],42
s_add_u32 m0,s57,4096
R 38,1,17408
v_fma_f32 v81,v81,v71,v70
M 22,a[84:87],42
s_add_u32 s55,s55,s60
v_add_f32 v26,v26,v74
v_add_f32 v27,v27,v75
M 18,a[88:91],46
s_add_u32 s57,s57,32768
R 42,1,18432
v_add_f32 v28,v28,v76
M 22,a[92:95],46
L 5
D 17,56,0
M 18,a[96:99],50
s_and_b32 s57,s57,131071
R 46,1,19456
v_add_f32 v29,v29,v77
M 22,a[100:103],50
s_add_u32 s54,s54,s46
v_add_f32 v30,v30,v78
v_add_f32 v31,v31,v79
M 18,a[104:107],54
s_and_b32 s78,s54,127
R 50,1,20480
v_add_f32 v32,v32,v80
M 22,a[108:111],54
L 5
D 17,56,1024
M 18,a[112:115],58
s_cmp_eq_u32 s78,s85
R 54,1,21504
v_fma_f32 v75,v26,v26,s87
M 22,a[116:119],58
s_cselect_b64 s[40:41],-1,0
v_add_f32 v74,v26,v27
v_fma_f32 v75,v27,v27,v75
M 18,a[120:123],62
R 58,1,22528
v_add_f32 v74,v74,v28
M 22,a[124:127],62
L 5
D 17,56,2048
M 18,a[128:131],34
R 62,1,23552
v_fma_f32 v75,v28,v28,v75
M 22,a[132:135],34
v_add_f32 v74,v74,v29
v_fma_f32 v75,v29,v29,v75
M 18,a[136:139],38
R 34,1,24576
v_add_f32 v74,v74,v30
M 22,a[140:143],38
L 5
D 17,56,3072
M 18,a[144:147],42
s_mov_b32 m0,s57
R 38,1,25600
v_fma_f32 v75,v30,v30,v75
M 22,a[148:151],42
s_add_u32 s56,s55,4096
v_add_f32 v74,v74,v31
v_fma_f32 v75,v31,v31,v75
M 18,a[152:155],46
R 42,1,26624
v_add_f32 v33,v33,v81
M 22,a[156:159],46
L 5
v_add_f32 v74,v74,v32
v_fma_f32 v75,v32,v32,v75
v_add_f32 v74,v74,v33
v_fma_f32 v75,v33,v33,v75
M 18,a[160:163],50
R 46,1,27648
v_cndmask_b32 v17,v15,v16,s[40:41]
M 22,a[164:167],50
v_permlane16_swap_b32 v74,v75
v_add_f32 v74,v74,v75
v_mov_b32 v75,v74
M 18,a[168:171],54
R 50,1,28672
v_cvt_pk_bf16_f32 v82,v26,v27
M 22,a[172:175],54
L 5
v_permlane32_swap_b32 v74,v75
v_add_f32 v74,v74,v75
M 18,a[176:179],58
ds_write_b32 v12,v74 offset:2048
R 54,1,29696
M 22,a[180:183],58
D 17,55,0
M 18,a[184:187],62
R 58,1,30720
v_cvt_pk_bf16_f32 v83,v28,v29
M 22,a[188:191],62
L 6
L 2
D 17,55,1024
M 18,a[192:195],34
s_waitcnt vmcnt(2)
R 62,1,31744
v_cvt_pk_bf16_f32 v84,v30,v31
M 22,a[196:199],34
s_barrier
D 17,55,2048
M 18,a[200:203],38
R 34,2,0
R 86,13,2048
M 22,a[204:207],38
s_nop 0
D 17,55,3072
M 18,a[208:211],42
s_add_u32 m0,s57,4096
s_add_u32 s55,s55,s60
s_add_u32 s57,s57,32768
R 38,2,1024
M 22,a[212:215],42
s_and_b32 s57,s57,131071
s_add_u32 s54,s54,s46
s_and_b32 s79,s54,127
ds_read_b32 v7,v6 offset:0
M 18,a[216:219],46
s_cmp_eq_u32 s79,0
s_cselect_b32 s94,0x3300,0
s_cmp_eq_u32 s79,127
R 42,2,2048
M 22,a[220:223],46
s_cselect_b32 s94,0xcc00,s94
s_add_u32 s79,s54,s46
L 7
buffer_store_dword v4,v3,s[36:39],s70 offen offset:0 sc0 sc1
M 18,a[224:227],50
s_and_b32 s79,s79,127
s_cmp_eq_u32 s79,0
s_cselect_b32 s95,0x3300,0
R 46,2,3072
M 22,a[228:231],50
s_cmp_eq_u32 s79,127
D 17,56,0
M 18,a[232:235],54
s_cselect_b32 s95,0xcc00,s95
R 50,2,4096
v_cvt_pk_bf16_f32 v85,v32,v33
M 22,a[236:239],54
s_add_u32 s65,s65,s62
L 7
s_mov_b32 s70,s69
D 17,56,1024
M 18,a[240:243],58
s_add_u32 s75,s75,s63
L 5
s_mov_b32 s69,s68
R 54,2,5120
M 22,a[244:247],58
s_add_u32 s68,s68,s63
L 4
v_cmp_eq_u32 vcc,s53,v7
M 18,a[248:251],62
s_or_b64 vcc,vcc,s[94:95]
s_cmp_eq_u32 vcc_lo,-1
R 58,2,6144
v_add_f32 v86,v86,v87
v_add_f32 v88,v88,v89
v_add_f32 v86,v86,v88
v_mul_f32 v86,s86,v86
M 22,a[252:255],62
s_nop 0
D 17,56,2048
D 17,56,3072
buffer_store_dwordx4 v[82:85],v0,s[36:39],s67 offen offset:0 sc0 sc1
buffer_store_dword v86,v14,s[36:39],s77 offen offset:0 sc0 sc1
v_xor_b32 v1,65536,v1
s_cbranch_scc0 Lg2_N1_%=
Lg2_S1_%=:
v_mfma_f32_16x16x32_bf16 v[26:29],a[0:3],v[34:37],0
s_add_u32 s58,s58,s61
R 62,2,7168
v_lshlrev_b32 v74,16,v90
v_mfma_f32_16x16x32_bf16 v[30:33],a[4:7],v[34:37],0
s_add_u32 m0,s59,1024
buffer_load_dwordx4 v[94:97],v0,s[36:39],s74 offen offset:0 sc1
M 26,a[8:11],38
s_mov_b32 s67,s65
R 34,2,8192
v_and_b32 v75,0xffff0000,v90
M 30,a[12:15],38
L 5
buffer_load_dword v8,s[36:39],s58 offen offset:0 sc1 lds
M 26,a[16:19],42
s_mov_b32 m0,s57
R 38,2,9216
v_lshlrev_b32 v76,16,v91
M 30,a[20:23],42
s_mov_b32 s77,s75
buffer_load_dwordx2 v[70:71],v11,s[36:39],s73 offen offset:16 sc1
M 26,a[24:27],46
s_add_u32 s73,s73,s64
R 42,2,10240
v_and_b32 v77,0xffff0000,v91
M 30,a[28:31],46
L 5
v_lshlrev_b32 v78,16,v92
v_and_b32 v79,0xffff0000,v92
M 26,a[32:35],50
s_add_u32 s74,s74,s62
R 46,2,11264
v_lshlrev_b32 v80,16,v93
M 30,a[36:39],50
s_and_b32 s78,s54,127
v_and_b32 v81,0xffff0000,v93
v_fma_f32 v74,v74,v67,v66
M 26,a[40:43],54
s_cmp_eq_u32 s78,s85
R 50,2,12288
v_fma_f32 v75,v75,v67,v66
M 30,a[44:47],54
L 5
v_fma_f32 v76,v76,v67,v66
v_fma_f32 v77,v77,v67,v66
M 26,a[48:51],58
s_cselect_b64 s[40:41],-1,0
R 54,2,13312
v_cndmask_b32 v17,v15,v16,s[40:41]
M 30,a[52:55],58
s_add_u32 s56,s55,4096
D 17,55,0
M 26,a[56:59],62
R 58,2,14336
v_fma_f32 v78,v78,v67,v66
M 30,a[60:63],62
L 5
D 17,55,1024
M 26,a[64:67],34
R 62,2,15360
v_fma_f32 v79,v79,v67,v66
M 30,a[68:71],34
v_fma_f32 v80,v80,v67,v66
v_fma_f32 v81,v81,v67,v66
M 26,a[72:75],38
R 34,2,16384
v_add_f32 v18,v18,v74
M 30,a[76:79],38
L 5
v_add_f32 v19,v19,v75
v_add_f32 v20,v20,v76
M 26,a[80:83],42
R 38,2,17408
v_add_f32 v21,v21,v77
M 30,a[84:87],42
D 17,55,2048
M 26,a[88:91],46
R 42,2,18432
v_add_f32 v22,v22,v78
M 30,a[92:95],46
L 5
v_add_f32 v23,v23,v79
v_add_f32 v24,v24,v80
M 26,a[96:99],50
R 46,2,19456
v_add_f32 v25,v25,v81
M 30,a[100:103],50
v_cvt_pk_bf16_f32 v82,v18,v19
v_cvt_pk_bf16_f32 v83,v20,v21
M 26,a[104:107],54
R 50,2,20480
v_cvt_pk_bf16_f32 v84,v22,v23
M 30,a[108:111],54
L 5
D 17,55,3072
M 26,a[112:115],58
s_add_u32 m0,s57,4096
R 54,2,21504
v_fma_f32 v75,v18,v18,s87
M 30,a[116:119],58
s_add_u32 s55,s55,s60
v_add_f32 v74,v18,v19
v_fma_f32 v75,v19,v19,v75
M 26,a[120:123],62
s_add_u32 s57,s57,32768
R 58,2,22528
v_add_f32 v74,v74,v20
M 30,a[124:127],62
L 5
v_fma_f32 v75,v20,v20,v75
v_add_f32 v74,v74,v21
M 26,a[128:131],34
s_and_b32 s57,s57,131071
R 62,2,23552
v_fma_f32 v75,v21,v21,v75
M 30,a[132:135],34
s_add_u32 s54,s54,s46
v_add_f32 v74,v74,v22
v_fma_f32 v75,v22,v22,v75
M 26,a[136:139],38
s_and_b32 s79,s54,127
R 34,2,24576
v_add_f32 v74,v74,v23
M 30,a[140:143],38
L 5
D 17,56,0
M 26,a[144:147],42
s_cmp_eq_u32 s79,0
R 38,2,25600
v_fma_f32 v75,v23,v23,v75
M 30,a[148:151],42
)"
R"(s_cselect_b32 s94,0x3300,0
v_add_f32 v74,v74,v24
v_fma_f32 v75,v24,v24,v75
M 26,a[152:155],46
s_cmp_eq_u32 s79,127
R 42,2,26624
v_add_f32 v74,v74,v25
M 30,a[156:159],46
L 5
v_cvt_pk_bf16_f32 v85,v24,v25
v_fma_f32 v75,v25,v25,v75
M 26,a[160:163],50
s_cselect_b32 s94,0xcc00,s94
s_add_u32 s79,s54,s46
R 46,2,27648
M 30,a[164:167],50
s_and_b32 s79,s79,127
v_permlane16_swap_b32 v74,v75
v_add_f32 v74,v74,v75
v_mov_b32 v75,v74
M 26,a[168:171],54
s_cmp_eq_u32 s79,0
s_cselect_b32 s95,0x3300,0
R 50,2,28672
M 30,a[172:175],54
L 5
v_permlane32_swap_b32 v74,v75
v_add_f32 v74,v74,v75
M 26,a[176:179],58
s_cmp_eq_u32 s79,127
ds_write_b32 v12,v74 offset:0
R 54,2,29696
M 30,a[180:183],58
s_cselect_b32 s95,0xcc00,s95
D 17,56,1024
M 26,a[184:187],62
s_add_u32 s65,s65,s62
s_add_u32 s75,s75,s63
R 58,2,30720
M 30,a[188:191],62
L 6
L 2
buffer_store_dwordx4 v[82:85],v0,s[36:39],s66 offen offset:0 sc0 sc1
M 26,a[192:195],34
s_waitcnt vmcnt(7)
R 62,2,31744
v_xor_b32 v2,65536,v2
M 30,a[196:199],34
s_barrier
R 86,13,0
ds_read_b32 v7,v6 offset:1024
M 26,a[200:203],38
R 34,1,0
M 30,a[204:207],38
s_nop 0
buffer_store_dword v4,v3,s[36:39],s70 offen offset:0 sc0 sc1
M 26,a[208:211],42
s_mov_b32 s70,s69
s_mov_b32 s69,s68
R 38,1,1024
M 30,a[212:215],42
s_add_u32 s68,s68,s63
D 17,56,2048
M 26,a[216:219],46
L 3
R 42,1,2048
v_add_f32 v86,v86,v87
M 30,a[220:223],46
s_nop 0
D 17,56,3072
M 26,a[224:227],50
L 3
R 46,1,3072
v_add_f32 v88,v88,v89
M 30,a[228:231],50
v_add_f32 v86,v86,v88
v_mul_f32 v86,s86,v86
M 26,a[232:235],54
R 50,1,4096
v_cmp_eq_u32 vcc,s53,v7
M 30,a[236:239],54
s_nop 0
buffer_store_dword v86,v14,s[36:39],s76 offen offset:0 sc0 sc1
M 26,a[240:243],58
s_or_b64 vcc,vcc,s[94:95]
s_cmp_eq_u32 vcc_lo,-1
R 54,1,5120
M 30,a[244:247],58
M 26,a[248:251],62
R 58,1,6144
M 30,a[252:255],62
L 5
s_cbranch_scc0 Lg2_N0_%=
Lg2_S0_%=:
v_mfma_f32_16x16x32_bf16 v[18:21],a[0:3],v[34:37],0
s_add_u32 s58,s58,s61
R 62,1,7168
v_lshlrev_b32 v74,16,v94
v_mfma_f32_16x16x32_bf16 v[22:25],a[4:7],v[34:37],0
s_add_u32 m0,s59,0
buffer_load_dwordx4 v[90:93],v0,s[36:39],s74 offen offset:0 sc1
M 18,a[8:11],38
s_mov_b32 s66,s65
R 34,1,8192
v_and_b32 v75,0xffff0000,v94
M 22,a[12:15],38
L 5
buffer_load_dword v8,s[36:39],s58 offen offset:0 sc1 lds
M 18,a[16:19],42
s_mov_b32 m0,s57
R 38,1,9216
v_lshlrev_b32 v76,16,v95
M 22,a[20:23],42
s_mov_b32 s76,s75
buffer_load_dwordx2 v[66:67],v11,s[36:39],s73 offen offset:16 sc1
M 18,a[24:27],46
s_add_u32 s73,s73,s64
R 42,1,10240
v_and_b32 v77,0xffff0000,v95
M 22,a[28:31],46
L 5
v_lshlrev_b32 v78,16,v96
v_and_b32 v79,0xffff0000,v96
M 18,a[32:35],50
s_add_u32 s74,s74,s62
R 46,1,11264
v_lshlrev_b32 v80,16,v97
M 22,a[36:39],50
s_and_b32 s78,s54,127
v_and_b32 v81,0xffff0000,v97
v_fma_f32 v74,v74,v71,v70
M 18,a[40:43],54
s_cmp_eq_u32 s78,s85
R 50,1,12288
v_fma_f32 v75,v75,v71,v70
M 22,a[44:47],54
L 5
v_fma_f32 v76,v76,v71,v70
v_fma_f32 v77,v77,v71,v70
M 18,a[48:51],58
s_cselect_b64 s[40:41],-1,0
R 54,1,13312
v_cndmask_b32 v17,v15,v16,s[40:41]
M 22,a[52:55],58
s_add_u32 s56,s55,4096
D 17,55,0
M 18,a[56:59],62
R 58,1,14336
v_fma_f32 v78,v78,v71,v70
M 22,a[60:63],62
L 5
D 17,55,1024
M 18,a[64:67],34
R 62,1,15360
v_fma_f32 v79,v79,v71,v70
M 22,a[68:71],34
v_fma_f32 v80,v80,v71,v70
v_fma_f32 v81,v81,v71,v70
M 18,a[72:75],38
R 34,1,16384
v_add_f32 v26,v26,v74
M 22,a[76:79],38
L 5
v_add_f32 v27,v27,v75
v_add_f32 v28,v28,v76
M 18,a[80:83],42
R 38,1,17408
v_add_f32 v29,v29,v77
M 22,a[84:87],42
D 17,55,2048
M 18,a[88:91],46
R 42,1,18432
v_add_f32 v30,v30,v78
M 22,a[92:95],46
L 5
v_add_f32 v31,v31,v79
v_add_f32 v32,v32,v80
M 18,a[96:99],50
R 46,1,19456
v_add_f32 v33,v33,v81
M 22,a[100:103],50
v_cvt_pk_bf16_f32 v82,v26,v27
v_cvt_pk_bf16_f32 v83,v28,v29
M 18,a[104:107],54
R 50,1,20480
v_cvt_pk_bf16_f32 v84,v30,v31
M 22,a[108:111],54
L 5
D 17,55,3072
M 18,a[112:115],58
s_add_u32 m0,s57,4096
R 54,1,21504
v_fma_f32 v75,v26,v26,s87
M 22,a[116:119],58
s_add_u32 s55,s55,s60
v_add_f32 v74,v26,v27
v_fma_f32 v75,v27,v27,v75
M 18,a[120:123],62
s_add_u32 s57,s57,32768
R 58,1,22528
v_add_f32 v74,v74,v28
M 22,a[124:127],62
L 5
v_fma_f32 v75,v28,v28,v75
v_add_f32 v74,v74,v29
M 18,a[128:131],34
s_and_b32 s57,s57,131071
R 62,1,23552
v_fma_f32 v75,v29,v29,v75
M 22,a[132:135],34
s_add_u32 s54,s54,s46
v_add_f32 v74,v74,v30
v_fma_f32 v75,v30,v30,v75
M 18,a[136:139],38
s_and_b32 s79,s54,127
R 34,1,24576
v_add_f32 v74,v74,v31
M 22,a[140:143],38
L 5
D 17,56,0
M 18,a[144:147],42
s_cmp_eq_u32 s79,0
R 38,1,25600
v_fma_f32 v75,v31,v31,v75
M 22,a[148:151],42
s_cselect_b32 s94,0x3300,0
v_add_f32 v74,v74,v32
v_fma_f32 v75,v32,v32,v75
M 18,a[152:155],46
s_cmp_eq_u32 s79,127
R 42,1,26624
v_add_f32 v74,v74,v33
M 22,a[156:159],46
L 5
v_cvt_pk_bf16_f32 v85,v32,v33
v_fma_f32 v75,v33,v33,v75
M 18,a[160:163],50
s_cselect_b32 s94,0xcc00,s94
s_add_u32 s79,s54,s46
R 46,1,27648
M 22,a[164:167],50
s_and_b32 s79,s79,127
v_permlane16_swap_b32 v74,v75
v_add_f32 v74,v74,v75
v_mov_b32 v75,v74
M 18,a[168:171],54
s_cmp_eq_u32 s79,0
s_cselect_b32 s95,0x3300,0
R 50,1,28672
M 22,a[172:175],54
L 5
v_permlane32_swap_b32 v74,v75
v_add_f32 v74,v74,v75
M 18,a[176:179],58
s_cmp_eq_u32 s79,127
ds_write_b32 v12,v74 offset:2048
R 54,1,29696
M 22,a[180:183],58
s_cselect_b32 s95,0xcc00,s95
D 17,56,1024
M 18,a[184:187],62
s_add_u32 s65,s65,s62
s_add_u32 s75,s75,s63
R 58,1,30720
M 22,a[188:191],62
L 6
L 2
buffer_store_dwordx4 v[82:85],v0,s[36:39],s67 offen offset:0 sc0 sc1
M 18,a[192:195],34
s_waitcnt vmcnt(7)
R 62,1,31744
v_xor_b32 v1,65536,v1
M 22,a[196:199],34
s_barrier
R 86,13,2048
ds_read_b32 v7,v6 offset:0
M 18,a[200:203],38
R 34,2,0
M 22,a[204:207],38
s_nop 0
buffer_store_dword v4,v3,s[36:39],s70 offen offset:0 sc0 sc1
M 18,a[208:211],42
s_mov_b32 s70,s69
s_mov_b32 s69,s68
R 38,2,1024
M 22,a[212:215],42
s_add_u32 s68,s68,s63
D 17,56,2048
M 18,a[216:219],46
L 3
R 42,2,2048
v_add_f32 v86,v86,v87
M 22,a[220:223],46
s_nop 0
D 17,56,3072
M 18,a[224:227],50
L 3
R 46,2,3072
v_add_f32 v88,v88,v89
M 22,a[228:231],50
v_add_f32 v86,v86,v88
v_mul_f32 v86,s86,v86
M 18,a[232:235],54
R 50,2,4096
v_cmp_eq_u32 vcc,s53,v7
M 22,a[236:239],54
s_nop 0
buffer_store_dword v86,v14,s[36:39],s77 offen offset:0 sc0 sc1
M 18,a[240:243],58
s_or_b64 vcc,vcc,s[94:95]
s_cmp_eq_u32 vcc_lo,-1
R 54,2,5120
M 22,a[244:247],58
M 18,a[248:251],62
R 58,2,6144
M 22,a[252:255],62
L 5
s_cbranch_scc1 Lg2_S1_%=
Lg2_N1_%=:
v_mfma_f32_16x16x32_bf16 v[26:29],a[0:3],v[34:37],0
s_add_u32 m0,s59,1024
R 62,2,7168
v_lshlrev_b32 v74,16,v90
v_mfma_f32_16x16x32_bf16 v[30:33],a[4:7],v[34:37],0
s_mov_b32 s67,s65
buffer_load_dword v5,s[36:39],s58 offen offset:0 sc1 lds
M 26,a[8:11],38
s_mov_b32 s77,s75
R 34,2,8192
v_and_b32 v75,0xffff0000,v90
M 30,a[12:15],38
L 5
buffer_load_dwordx4 v[94:97],v0,s[36:39],s74 offen offset:0 sc1
M 26,a[16:19],42
R 38,2,9216
v_lshlrev_b32 v76,16,v91
M 30,a[20:23],42
buffer_load_dwordx2 v[70:71],v11,s[36:39],s73 offen offset:16 sc1
M 26,a[24:27],46
s_add_u32 s73,s73,s64
R 42,2,10240
v_and_b32 v77,0xffff0000,v91
M 30,a[28:31],46
L 5
v_lshlrev_b32 v78,16,v92
v_and_b32 v79,0xffff0000,v92
M 26,a[32:35],50
s_add_u32 s74,s74,s62
R 46,2,11264
v_lshlrev_b32 v80,16,v93
M 30,a[36:39],50
s_and_b32 s79,s54,127
v_and_b32 v81,0xffff0000,v93
v_fma_f32 v74,v74,v67,v66
M 26,a[40:43],54
s_cmp_eq_u32 s79,0
R 50,2,12288
v_fma_f32 v75,v75,v67,v66
M 30,a[44:47],54
L 5
v_fma_f32 v76,v76,v67,v66
v_fma_f32 v77,v77,v67,v66
M 26,a[48:51],58
s_cselect_b32 s94,0x3300,0
R 54,2,13312
v_fma_f32 v78,v78,v67,v66
M 30,a[52:55],58
s_cmp_eq_u32 s79,127
v_fma_f32 v79,v79,v67,v66
v_fma_f32 v80,v80,v67,v66
M 26,a[56:59],62
s_cselect_b32 s94,0xcc00,s94
R 58,2,14336
v_fma_f32 v81,v81,v67,v66
M 30,a[60:63],62
L 5
v_add_f32 v18,v18,v74
v_add_f32 v19,v19,v75
M 26,a[64:67],34
s_add_u32 s79,s54,s46
R 62,2,15360
v_add_f32 v20,v20,v76
M 30,a[68:71],34
s_and_b32 s79,s79,127
v_add_f32 v21,v21,v77
v_add_f32 v22,v22,v78
M 26,a[72:75],38
s_cmp_eq_u32 s79,0
R 34,2,16384
v_add_f32 v23,v23,v79
M 30,a[76:79],38
L 5
v_add_f32 v24,v24,v80
v_add_f32 v25,v25,v81
M 26,a[80:83],42
s_cselect_b32 s95,0x3300,0
R 38,2,17408
v_cvt_pk_bf16_f32 v82,v18,v19
M 30,a[84:87],42
s_cmp_eq_u32 s79,127
v_cvt_pk_bf16_f32 v83,v20,v21
v_cvt_pk_bf16_f32 v84,v22,v23
M 26,a[88:91],46
s_cselect_b32 s95,0xcc00,s95
R 42,2,18432
v_cvt_pk_bf16_f32 v85,v24,v25
M 30,a[92:95],46
L 5
buffer_store_dwordx4 v[82:85],v0,s[36:39],s66 offen offset:0 sc0 sc1
v_add_f32 v74,v18,v19
M 26,a[96:99],50
s_add_u32 s65,s65,s62
R 46,2,19456
v_fma_f32 v75,v18,v18,s87
M 30,a[100:103],50
s_add_u32 s75,s75,s63
v_fma_f32 v75,v19,v19,v75
v_add_f32 v74,v74,v20
M 26,a[104:107],54
R 50,2,20480
v_fma_f32 v75,v20,v20,v75
M 30,a[108:111],54
L 5
v_add_f32 v74,v74,v21
v_fma_f32 v75,v21,v21,v75
M 26,a[112:115],58
R 54,2,21504
v_add_f32 v74,v74,v22
M 30,a[116:119],58
v_fma_f32 v75,v22,v22,v75
v_add_f32 v74,v74,v23
M 26,a[120:123],62
R 58,2,22528
v_fma_f32 v75,v23,v23,v75
M 30,a[124:127],62
L 5
v_add_f32 v74,v74,v24
v_fma_f32 v75,v24,v24,v75
M 26,a[128:131],34
R 62,2,23552
v_add_f32 v74,v74,v25
M 30,a[132:135],34
v_fma_f32 v75,v25,v25,v75
M 26,a[136:139],38
R 34,2,24576
M 30,a[140:143],38
L 5
v_permlane16_swap_b32 v74,v75
M 26,a[144:147],42
R 38,2,25600
v_add_f32 v74,v74,v75
M 30,a[148:151],42
v_mov_b32 v75,v74
M 26,a[152:155],46
R 42,2,26624
M 30,a[156:159],46
L 5
v_permlane32_swap_b32 v74,v75
M 26,a[160:163],50
R 46,2,27648
v_add_f32 v74,v74,v75
M 30,a[164:167],50
ds_write_b32 v12,v74 offset:0
M 26,a[168:171],54
R 50,2,28672
M 30,a[172:175],54
L 6
M 26,a[176:179],58
R 54,2,29696
M 30,a[180:183],58
M 26,a[184:187],62
R 58,2,30720
M 30,a[188:191],62
L 6
L 3
M 26,a[192:195],34
s_waitcnt vmcnt(1)
R 62,2,31744
v_xor_b32 v2,65536,v2
M 30,a[196:199],34
s_barrier
R 86,13,0
ds_read_b32 v7,v6 offset:1024
M 26,a[200:203],38
R 34,1,0
M 30,a[204:207],38
s_nop 0
buffer_store_dword v4,v3,s[36:39],s70 offen offset:0 sc0 sc1
M 26,a[208:211],42
s_mov_b32 s70,s69
s_mov_b32 s69,s68
R 38,1,1024
M 30,a[212:215],42
s_add_u32 s68,s68,s63
M 26,a[216:219],46
L 3
R 42,1,2048
v_add_f32 v86,v86,v87
M 30,a[220:223],46
s_nop 0
)"
R"(v_add_f32 v88,v88,v89
v_add_f32 v86,v86,v88
M 26,a[224:227],50
L 3
R 46,1,3072
v_mul_f32 v86,s86,v86
M 30,a[228:231],50
buffer_store_dword v86,v14,s[36:39],s76 offen offset:0 sc0 sc1
M 26,a[232:235],54
R 50,1,4096
v_cmp_eq_u32 vcc,s53,v7
M 30,a[236:239],54
s_nop 0
s_or_b64 vcc,vcc,s[94:95]
s_cmp_eq_u64 vcc,-1
M 26,a[240:243],58
R 54,1,5120
M 30,a[244:247],58
M 26,a[248:251],62
R 58,1,6144
M 30,a[252:255],62
L 5
s_cbranch_scc1 Lg2_L20_%=
s_cmp_eq_u32 vcc_lo,-1
s_cbranch_scc1 Lg2_L10_%=
Lg2_X0_%=:
v_mfma_f32_16x16x32_bf16 v[18:21],a[0:3],v[34:37],0
s_add_u32 m0,s59,0
R 62,1,7168
v_lshlrev_b32 v74,16,v94
v_mfma_f32_16x16x32_bf16 v[22:25],a[4:7],v[34:37],0
s_mov_b32 s66,s65
buffer_load_dword v5,s[36:39],s58 offen offset:0 sc1 lds
M 18,a[8:11],38
s_mov_b32 s76,s75
R 34,1,8192
v_and_b32 v75,0xffff0000,v94
M 22,a[12:15],38
L 5
buffer_load_dwordx4 v[90:93],v0,s[36:39],s74 offen offset:0 sc1
M 18,a[16:19],42
R 38,1,9216
v_lshlrev_b32 v76,16,v95
M 22,a[20:23],42
buffer_load_dwordx2 v[66:67],v11,s[36:39],s73 offen offset:16 sc1
M 18,a[24:27],46
s_add_u32 s73,s73,s64
R 42,1,10240
v_and_b32 v77,0xffff0000,v95
M 22,a[28:31],46
L 5
v_lshlrev_b32 v78,16,v96
v_and_b32 v79,0xffff0000,v96
M 18,a[32:35],50
s_add_u32 s74,s74,s62
R 46,1,11264
v_lshlrev_b32 v80,16,v97
M 22,a[36:39],50
s_and_b32 s79,s54,127
v_and_b32 v81,0xffff0000,v97
v_fma_f32 v74,v74,v71,v70
M 18,a[40:43],54
s_cmp_eq_u32 s79,0
R 50,1,12288
v_fma_f32 v75,v75,v71,v70
M 22,a[44:47],54
L 5
v_fma_f32 v76,v76,v71,v70
v_fma_f32 v77,v77,v71,v70
M 18,a[48:51],58
s_cselect_b32 s94,0x3300,0
R 54,1,13312
v_fma_f32 v78,v78,v71,v70
M 22,a[52:55],58
s_cmp_eq_u32 s79,127
v_fma_f32 v79,v79,v71,v70
v_fma_f32 v80,v80,v71,v70
M 18,a[56:59],62
s_cselect_b32 s94,0xcc00,s94
R 58,1,14336
v_fma_f32 v81,v81,v71,v70
M 22,a[60:63],62
L 5
v_add_f32 v26,v26,v74
v_add_f32 v27,v27,v75
M 18,a[64:67],34
s_add_u32 s79,s54,s46
R 62,1,15360
v_add_f32 v28,v28,v76
M 22,a[68:71],34
s_and_b32 s79,s79,127
v_add_f32 v29,v29,v77
v_add_f32 v30,v30,v78
M 18,a[72:75],38
s_cmp_eq_u32 s79,0
R 34,1,16384
v_add_f32 v31,v31,v79
M 22,a[76:79],38
L 5
v_add_f32 v32,v32,v80
v_add_f32 v33,v33,v81
M 18,a[80:83],42
s_cselect_b32 s95,0x3300,0
R 38,1,17408
v_cvt_pk_bf16_f32 v82,v26,v27
M 22,a[84:87],42
s_cmp_eq_u32 s79,127
v_cvt_pk_bf16_f32 v83,v28,v29
v_cvt_pk_bf16_f32 v84,v30,v31
M 18,a[88:91],46
s_cselect_b32 s95,0xcc00,s95
R 42,1,18432
v_cvt_pk_bf16_f32 v85,v32,v33
M 22,a[92:95],46
L 5
buffer_store_dwordx4 v[82:85],v0,s[36:39],s67 offen offset:0 sc0 sc1
v_add_f32 v74,v26,v27
M 18,a[96:99],50
s_add_u32 s65,s65,s62
R 46,1,19456
v_fma_f32 v75,v26,v26,s87
M 22,a[100:103],50
s_add_u32 s75,s75,s63
v_fma_f32 v75,v27,v27,v75
v_add_f32 v74,v74,v28
M 18,a[104:107],54
R 50,1,20480
v_fma_f32 v75,v28,v28,v75
M 22,a[108:111],54
L 5
v_add_f32 v74,v74,v29
v_fma_f32 v75,v29,v29,v75
M 18,a[112:115],58
R 54,1,21504
v_add_f32 v74,v74,v30
M 22,a[116:119],58
v_fma_f32 v75,v30,v30,v75
v_add_f32 v74,v74,v31
M 18,a[120:123],62
R 58,1,22528
v_fma_f32 v75,v31,v31,v75
M 22,a[124:127],62
L 5
v_add_f32 v74,v74,v32
v_fma_f32 v75,v32,v32,v75
M 18,a[128:131],34
R 62,1,23552
v_add_f32 v74,v74,v33
M 22,a[132:135],34
v_fma_f32 v75,v33,v33,v75
M 18,a[136:139],38
R 34,1,24576
M 22,a[140:143],38
L 5
v_permlane16_swap_b32 v74,v75
M 18,a[144:147],42
R 38,1,25600
v_add_f32 v74,v74,v75
M 22,a[148:151],42
v_mov_b32 v75,v74
M 18,a[152:155],46
R 42,1,26624
M 22,a[156:159],46
L 5
v_permlane32_swap_b32 v74,v75
M 18,a[160:163],50
R 46,1,27648
v_add_f32 v74,v74,v75
M 22,a[164:167],50
ds_write_b32 v12,v74 offset:2048
M 18,a[168:171],54
R 50,1,28672
M 22,a[172:175],54
L 6
M 18,a[176:179],58
R 54,1,29696
M 22,a[180:183],58
M 18,a[184:187],62
R 58,1,30720
M 22,a[188:191],62
L 6
L 3
M 18,a[192:195],34
s_waitcnt vmcnt(1)
R 62,1,31744
v_xor_b32 v1,65536,v1
M 22,a[196:199],34
s_barrier
R 86,13,2048
ds_read_b32 v7,v6 offset:0
M 18,a[200:203],38
buffer_store_dword v4,v3,s[36:39],s70 offen offset:0 sc0 sc1
M 22,a[204:207],38
s_nop 0
s_mov_b32 s70,s69
s_mov_b32 s69,s68
M 18,a[208:211],42
s_add_u32 s68,s68,s63
M 22,a[212:215],42
M 18,a[216:219],46
L 1
v_add_f32 v86,v86,v87
v_add_f32 v88,v88,v89
M 22,a[220:223],46
s_nop 0
v_add_f32 v86,v86,v88
v_mul_f32 v86,s86,v86
M 18,a[224:227],50
L 0
buffer_store_dword v86,v14,s[36:39],s77 offen offset:0 sc0 sc1
M 22,a[228:231],50
v_cmp_eq_u32 vcc,s53,v7
M 18,a[232:235],54
s_or_b64 vcc,vcc,s[94:95]
M 22,a[236:239],54
s_nop 0
M 18,a[240:243],58
M 22,a[244:247],58
M 18,a[248:251],62
M 22,a[252:255],62
s_mov_b32 s80,1
s_cmp_eq_u32 vcc_lo,-1
s_cbranch_scc0 Lg2_xn0_%=
s_and_b32 s78,s54,127
s_cmp_eq_u32 s78,s85
s_cselect_b64 s[40:41],-1,0
v_cndmask_b32 v17,v15,v16,s[40:41]
s_mov_b32 m0,s57
s_add_u32 s56,s55,4096
D 17,55,0
v_lshlrev_b32 v74,16,v90
v_and_b32 v75,0xffff0000,v90
v_lshlrev_b32 v76,16,v91
v_and_b32 v77,0xffff0000,v91
v_lshlrev_b32 v78,16,v92
D 17,55,1024
v_and_b32 v79,0xffff0000,v92
v_lshlrev_b32 v80,16,v93
v_and_b32 v81,0xffff0000,v93
v_fma_f32 v74,v74,v67,v66
v_fma_f32 v75,v75,v67,v66
D 17,55,2048
v_fma_f32 v76,v76,v67,v66
v_fma_f32 v77,v77,v67,v66
v_fma_f32 v78,v78,v67,v66
v_fma_f32 v79,v79,v67,v66
v_fma_f32 v80,v80,v67,v66
D 17,55,3072
v_fma_f32 v81,v81,v67,v66
v_add_f32 v18,v18,v74
v_add_f32 v19,v19,v75
v_add_f32 v20,v20,v76
v_add_f32 v21,v21,v77
s_add_u32 m0,s57,4096
s_nop 0
D 17,56,0
v_add_f32 v22,v22,v78
v_add_f32 v23,v23,v79
v_add_f32 v24,v24,v80
v_add_f32 v25,v25,v81
v_cvt_pk_bf16_f32 v82,v18,v19
D 17,56,1024
v_cvt_pk_bf16_f32 v83,v20,v21
v_cvt_pk_bf16_f32 v84,v22,v23
v_cvt_pk_bf16_f32 v85,v24,v25
D 17,56,2048
D 17,56,3072
buffer_store_dwordx4 v[82:85],v0,s[36:39],s66 offen offset:0 sc0 sc1
v_add_f32 v74,v18,v19
v_fma_f32 v75,v18,v18,s87
s_add_u32 s55,s55,s60
s_add_u32 s57,s57,32768
s_and_b32 s57,s57,131071
s_add_u32 s54,s54,s46
v_fma_f32 v75,v19,v19,v75
v_add_f32 v74,v74,v20
v_fma_f32 v75,v20,v20,v75
v_add_f32 v74,v74,v21
v_fma_f32 v75,v21,v21,v75
v_add_f32 v74,v74,v22
v_fma_f32 v75,v22,v22,v75
v_add_f32 v74,v74,v23
v_fma_f32 v75,v23,v23,v75
v_add_f32 v74,v74,v24
v_fma_f32 v75,v24,v24,v75
v_add_f32 v74,v74,v25
v_fma_f32 v75,v25,v25,v75
s_nop 1
v_permlane16_swap_b32 v74,v75
v_add_f32 v74,v74,v75
v_mov_b32 v75,v74
s_nop 1
v_permlane32_swap_b32 v74,v75
v_add_f32 v74,v74,v75
ds_write_b32 v12,v74 offset:0
L 0
s_barrier
R 86,13,0
L 0
v_add_f32 v86,v86,v87
v_add_f32 v88,v88,v89
v_add_f32 v86,v86,v88
v_mul_f32 v86,s86,v86
buffer_store_dword v86,v14,s[36:39],s76 offen offset:0 sc0 sc1
s_mov_b32 s72,s69
s_mov_b32 s69,0x7fff0000
s_mov_b32 s66,0x7fff0000
s_mov_b32 s76,0x7fff0000
s_branch Lg2_gox_%=
Lg2_xn0_%=:
s_cmp_ge_u32 s54,256
s_cbranch_scc1 Lg2_fin0_%=
s_add_u32 m0,s59,4096
s_nop 0
buffer_load_dword v5,s[36:39],s58 offen offset:0 sc1 lds
buffer_store_dword v4,v3,s[36:39],s71 offen offset:0 sc0 sc1
s_add_u32 m0,s59,5120
s_nop 0
buffer_load_dword v5,s[36:39],s58 offen offset:0 sc1 lds
buffer_store_dword v4,v3,s[36:39],s71 offen offset:0 sc0 sc1
s_mov_b32 s83,0
v_lshlrev_b32 v74,16,v90
v_and_b32 v75,0xffff0000,v90
v_lshlrev_b32 v76,16,v91
v_and_b32 v77,0xffff0000,v91
v_lshlrev_b32 v78,16,v92
v_and_b32 v79,0xffff0000,v92
v_lshlrev_b32 v80,16,v93
v_and_b32 v81,0xffff0000,v93
v_fma_f32 v74,v74,v67,v66
v_fma_f32 v75,v75,v67,v66
v_fma_f32 v76,v76,v67,v66
v_fma_f32 v77,v77,v67,v66
v_fma_f32 v78,v78,v67,v66
v_fma_f32 v79,v79,v67,v66
v_fma_f32 v80,v80,v67,v66
v_fma_f32 v81,v81,v67,v66
v_add_f32 v18,v18,v74
v_add_f32 v19,v19,v75
v_add_f32 v20,v20,v76
v_add_f32 v21,v21,v77
v_add_f32 v22,v22,v78
v_add_f32 v23,v23,v79
v_add_f32 v24,v24,v80
v_add_f32 v25,v25,v81
v_cvt_pk_bf16_f32 v82,v18,v19
v_cvt_pk_bf16_f32 v83,v20,v21
v_cvt_pk_bf16_f32 v84,v22,v23
v_cvt_pk_bf16_f32 v85,v24,v25
buffer_store_dwordx4 v[82:85],v0,s[36:39],s66 offen offset:0 sc0 sc1
v_add_f32 v74,v18,v19
v_fma_f32 v75,v18,v18,s87
v_fma_f32 v75,v19,v19,v75
v_add_f32 v74,v74,v20
v_fma_f32 v75,v20,v20,v75
v_add_f32 v74,v74,v21
v_fma_f32 v75,v21,v21,v75
v_add_f32 v74,v74,v22
v_fma_f32 v75,v22,v22,v75
v_add_f32 v74,v74,v23
v_fma_f32 v75,v23,v23,v75
v_add_f32 v74,v74,v24
v_fma_f32 v75,v24,v24,v75
v_add_f32 v74,v74,v25
v_fma_f32 v75,v25,v25,v75
s_nop 1
v_permlane16_swap_b32 v74,v75
v_add_f32 v74,v74,v75
v_mov_b32 v75,v74
s_nop 1
v_permlane32_swap_b32 v74,v75
v_add_f32 v74,v74,v75
ds_write_b32 v12,v74 offset:0
L 0
s_barrier
R 86,13,0
L 0
v_add_f32 v86,v86,v87
v_add_f32 v88,v88,v89
v_add_f32 v86,v86,v88
v_mul_f32 v86,s86,v86
buffer_store_dword v86,v14,s[36:39],s76 offen offset:0 sc0 sc1
s_mov_b32 s72,s69
s_mov_b32 s69,0x7fff0000
s_mov_b32 s66,0x7fff0000
s_mov_b32 s76,0x7fff0000
s_branch Lg2_poll_%=
Lg2_fin0_%=:
v_lshlrev_b32 v74,16,v90
v_and_b32 v75,0xffff0000,v90
v_lshlrev_b32 v76,16,v91
v_and_b32 v77,0xffff0000,v91
v_lshlrev_b32 v78,16,v92
v_and_b32 v79,0xffff0000,v92
v_lshlrev_b32 v80,16,v93
v_and_b32 v81,0xffff0000,v93
v_fma_f32 v74,v74,v67,v66
v_fma_f32 v75,v75,v67,v66
v_fma_f32 v76,v76,v67,v66
v_fma_f32 v77,v77,v67,v66
v_fma_f32 v78,v78,v67,v66
v_fma_f32 v79,v79,v67,v66
v_fma_f32 v80,v80,v67,v66
v_fma_f32 v81,v81,v67,v66
v_add_f32 v18,v18,v74
v_add_f32 v19,v19,v75
v_add_f32 v20,v20,v76
v_add_f32 v21,v21,v77
v_add_f32 v22,v22,v78
v_add_f32 v23,v23,v79
v_add_f32 v24,v24,v80
v_add_f32 v25,v25,v81
v_cvt_pk_bf16_f32 v82,v18,v19
v_cvt_pk_bf16_f32 v83,v20,v21
v_cvt_pk_bf16_f32 v84,v22,v23
v_cvt_pk_bf16_f32 v85,v24,v25
buffer_store_dwordx4 v[82:85],v0,s[36:39],s66 offen offset:0 sc0 sc1
v_add_f32 v74,v18,v19
v_fma_f32 v75,v18,v18,s87
v_fma_f32 v75,v19,v19,v75
v_add_f32 v74,v74,v20
v_fma_f32 v75,v20,v20,v75
v_add_f32 v74,v74,v21
v_fma_f32 v75,v21,v21,v75
v_add_f32 v74,v74,v22
v_fma_f32 v75,v22,v22,v75
v_add_f32 v74,v74,v23
v_fma_f32 v75,v23,v23,v75
v_add_f32 v74,v74,v24
v_fma_f32 v75,v24,v24,v75
v_add_f32 v74,v74,v25
v_fma_f32 v75,v25,v25,v75
s_nop 1
v_permlane16_swap_b32 v74,v75
v_add_f32 v74,v74,v75
v_mov_b32 v75,v74
s_nop 1
v_permlane32_swap_b32 v74,v75
v_add_f32 v74,v74,v75
ds_write_b32 v12,v74 offset:0
L 0
s_barrier
R 86,13,0
L 0
v_add_f32 v86,v86,v87
v_add_f32 v88,v88,v89
v_add_f32 v86,v86,v88
v_mul_f32 v86,s86,v86
buffer_store_dword v86,v14,s[36:39],s76 offen offset:0 sc0 sc1
s_mov_b32 s72,s69
s_mov_b32 s69,0x7fff0000
s_mov_b32 s66,0x7fff0000
s_mov_b32 s76,0x7fff0000
s_branch Lg2_done_%=
Lg2_L21_%=:
v_mfma_f32_16x16x32_bf16 v[26:29],a[0:3],v[34:37],0
s_add_u32 s58,s58,s61
R 62,2,7168
v_lshlrev_b32 v74,16,v90
v_mfma_f32_16x16x32_bf16 v[30:33],a[4:7],v[34:37],0
s_add_u32 s58,s58,s61
buffer_load_dwordx4 v[94:97],v0,s[36:39],s74 offen offset:0 sc1
M 26,a[8:11],38
s_add_u32 m0,s59,1024
R 34,2,8192
v_and_b32 v75,0xffff0000,v90
M 30,a[12:15],38
L 5
buffer_load_dword v8,s[36:39],s58 offen offset:0 sc1 lds
M 26,a[16:19],42
s_mov_b32 m0,s57
R 38,2,9216
v_lshlrev_b32 v76,16,v91
M 30,a[20:23],42
s_mov_b32 s67,s65
buffer_load_dwordx2 v[70:71],v11,s[36:39],s73 offen offset:16 sc1
M 26,a[24:27],46
s_add_u32 s73,s73,s64
R 42,2,10240
v_and_b32 v77,0xffff0000,v91
M 30,a[28:31],46
L 5
v_lshlrev_b32 v78,16,v92
v_and_b32 v79,0xffff0000,v92
M 26,a[32:35],50
s_add_u32 s74,s74,s62
R 46,2,11264
v_lshlrev_b32 v80,16,v93
M 30,a[36:39],50
s_and_b32 s78,s54,127
v_and_b32 v81,0xffff0000,v93
v_fma_f32 v74,v74,v67,v66
M 26,a[40:43],54
s_cmp_eq_u32 s78,s85
R 50,2,12288
v_fma_f32 v75,v75,v67,v66
M 30,a[44:47],54
L 5
v_fma_f32 v76,v76,v67,v66
v_fma_f32 v77,v77,v67,v66
)"
R"(M 26,a[48:51],58
s_cselect_b64 s[40:41],-1,0
R 54,2,13312
v_cndmask_b32 v17,v15,v16,s[40:41]
M 30,a[52:55],58
s_add_u32 s56,s55,4096
D 17,55,0
M 26,a[56:59],62
s_mov_b32 s77,s75
R 58,2,14336
v_fma_f32 v78,v78,v67,v66
M 30,a[60:63],62
L 5
D 17,55,1024
M 26,a[64:67],34
R 62,2,15360
v_fma_f32 v79,v79,v67,v66
M 30,a[68:71],34
D 17,55,2048
M 26,a[72:75],38
R 34,2,16384
v_fma_f32 v80,v80,v67,v66
M 30,a[76:79],38
L 5
D 17,55,3072
M 26,a[80:83],42
s_add_u32 m0,s57,4096
R 38,2,17408
v_fma_f32 v81,v81,v67,v66
M 30,a[84:87],42
s_add_u32 s55,s55,s60
v_add_f32 v18,v18,v74
v_add_f32 v19,v19,v75
M 26,a[88:91],46
s_add_u32 s57,s57,32768
R 42,2,18432
v_add_f32 v20,v20,v76
M 30,a[92:95],46
L 5
D 17,56,0
M 26,a[96:99],50
s_and_b32 s57,s57,131071
R 46,2,19456
v_add_f32 v21,v21,v77
M 30,a[100:103],50
s_add_u32 s54,s54,s46
v_add_f32 v22,v22,v78
v_add_f32 v23,v23,v79
M 26,a[104:107],54
s_and_b32 s78,s54,127
R 50,2,20480
v_add_f32 v24,v24,v80
M 30,a[108:111],54
L 5
D 17,56,1024
M 26,a[112:115],58
s_cmp_eq_u32 s78,s85
R 54,2,21504
v_fma_f32 v75,v18,v18,s87
M 30,a[116:119],58
s_cselect_b64 s[40:41],-1,0
v_add_f32 v74,v18,v19
v_fma_f32 v75,v19,v19,v75
M 26,a[120:123],62
R 58,2,22528
v_add_f32 v74,v74,v20
M 30,a[124:127],62
L 5
D 17,56,2048
M 26,a[128:131],34
R 62,2,23552
v_fma_f32 v75,v20,v20,v75
M 30,a[132:135],34
v_add_f32 v74,v74,v21
v_fma_f32 v75,v21,v21,v75
M 26,a[136:139],38
R 34,2,24576
v_add_f32 v74,v74,v22
M 30,a[140:143],38
L 5
D 17,56,3072
M 26,a[144:147],42
s_mov_b32 m0,s57
R 38,2,25600
v_fma_f32 v75,v22,v22,v75
M 30,a[148:151],42
s_add_u32 s56,s55,4096
v_add_f32 v74,v74,v23
v_fma_f32 v75,v23,v23,v75
M 26,a[152:155],46
R 42,2,26624
v_add_f32 v25,v25,v81
M 30,a[156:159],46
L 5
v_add_f32 v74,v74,v24
v_fma_f32 v75,v24,v24,v75
v_add_f32 v74,v74,v25
v_fma_f32 v75,v25,v25,v75
M 26,a[160:163],50
R 46,2,27648
v_cndmask_b32 v17,v15,v16,s[40:41]
M 30,a[164:167],50
v_permlane16_swap_b32 v74,v75
v_add_f32 v74,v74,v75
v_mov_b32 v75,v74
M 26,a[168:171],54
R 50,2,28672
v_cvt_pk_bf16_f32 v82,v18,v19
M 30,a[172:175],54
L 5
v_permlane32_swap_b32 v74,v75
v_add_f32 v74,v74,v75
M 26,a[176:179],58
ds_write_b32 v12,v74 offset:0
R 54,2,29696
M 30,a[180:183],58
D 17,55,0
M 26,a[184:187],62
R 58,2,30720
v_cvt_pk_bf16_f32 v83,v20,v21
M 30,a[188:191],62
L 6
L 2
D 17,55,1024
M 26,a[192:195],34
s_waitcnt vmcnt(2)
R 62,2,31744
v_cvt_pk_bf16_f32 v84,v22,v23
M 30,a[196:199],34
s_barrier
D 17,55,2048
M 26,a[200:203],38
R 34,1,0
R 86,13,0
M 30,a[204:207],38
s_nop 0
D 17,55,3072
M 26,a[208:211],42
s_add_u32 m0,s57,4096
s_add_u32 s55,s55,s60
s_add_u32 s57,s57,32768
R 38,1,1024
M 30,a[212:215],42
s_and_b32 s57,s57,131071
s_add_u32 s54,s54,s46
s_and_b32 s79,s54,127
ds_read_b32 v7,v6 offset:1024
M 26,a[216:219],46
s_cmp_eq_u32 s79,0
s_cselect_b32 s94,0x3300,0
s_cmp_eq_u32 s79,127
R 42,1,2048
M 30,a[220:223],46
s_cselect_b32 s94,0xcc00,s94
s_add_u32 s79,s54,s46
L 7
buffer_store_dword v4,v3,s[36:39],s70 offen offset:0 sc0 sc1
M 26,a[224:227],50
s_and_b32 s79,s79,127
s_cmp_eq_u32 s79,0
s_cselect_b32 s95,0x3300,0
R 46,1,3072
M 30,a[228:231],50
s_cmp_eq_u32 s79,127
D 17,56,0
M 26,a[232:235],54
s_cselect_b32 s95,0xcc00,s95
R 50,1,4096
v_cvt_pk_bf16_f32 v85,v24,v25
M 30,a[236:239],54
s_add_u32 s65,s65,s62
L 7
s_mov_b32 s70,s69
D 17,56,1024
M 26,a[240:243],58
s_add_u32 s75,s75,s63
L 5
s_mov_b32 s69,s68
R 54,1,5120
M 30,a[244:247],58
s_add_u32 s68,s68,s63
L 4
v_cmp_eq_u32 vcc,s53,v7
M 26,a[248:251],62
s_or_b64 vcc,vcc,s[94:95]
s_cmp_eq_u32 vcc_lo,-1
R 58,1,6144
v_add_f32 v86,v86,v87
v_add_f32 v88,v88,v89
v_add_f32 v86,v86,v88
v_mul_f32 v86,s86,v86
M 30,a[252:255],62
s_nop 0
D 17,56,2048
D 17,56,3072
buffer_store_dwordx4 v[82:85],v0,s[36:39],s66 offen offset:0 sc0 sc1
buffer_store_dword v86,v14,s[36:39],s76 offen offset:0 sc0 sc1
v_xor_b32 v2,65536,v2
s_cbranch_scc1 Lg2_S0_%=
Lg2_N0_%=:
v_mfma_f32_16x16x32_bf16 v[18:21],a[0:3],v[34:37],0
s_add_u32 m0,s59,0
R 62,1,7168
v_lshlrev_b32 v74,16,v94
v_mfma_f32_16x16x32_bf16 v[22:25],a[4:7],v[34:37],0
s_mov_b32 s66,s65
buffer_load_dword v5,s[36:39],s58 offen offset:0 sc1 lds
M 18,a[8:11],38
s_mov_b32 s76,s75
R 34,1,8192
v_and_b32 v75,0xffff0000,v94
M 22,a[12:15],38
L 5
buffer_load_dwordx4 v[90:93],v0,s[36:39],s74 offen offset:0 sc1
M 18,a[16:19],42
R 38,1,9216
v_lshlrev_b32 v76,16,v95
M 22,a[20:23],42
buffer_load_dwordx2 v[66:67],v11,s[36:39],s73 offen offset:16 sc1
M 18,a[24:27],46
s_add_u32 s73,s73,s64
R 42,1,10240
v_and_b32 v77,0xffff0000,v95
M 22,a[28:31],46
L 5
v_lshlrev_b32 v78,16,v96
v_and_b32 v79,0xffff0000,v96
M 18,a[32:35],50
s_add_u32 s74,s74,s62
R 46,1,11264
v_lshlrev_b32 v80,16,v97
M 22,a[36:39],50
s_and_b32 s79,s54,127
v_and_b32 v81,0xffff0000,v97
v_fma_f32 v74,v74,v71,v70
M 18,a[40:43],54
s_cmp_eq_u32 s79,0
R 50,1,12288
v_fma_f32 v75,v75,v71,v70
M 22,a[44:47],54
L 5
v_fma_f32 v76,v76,v71,v70
v_fma_f32 v77,v77,v71,v70
M 18,a[48:51],58
s_cselect_b32 s94,0x3300,0
R 54,1,13312
v_fma_f32 v78,v78,v71,v70
M 22,a[52:55],58
s_cmp_eq_u32 s79,127
v_fma_f32 v79,v79,v71,v70
v_fma_f32 v80,v80,v71,v70
M 18,a[56:59],62
s_cselect_b32 s94,0xcc00,s94
R 58,1,14336
v_fma_f32 v81,v81,v71,v70
M 22,a[60:63],62
L 5
v_add_f32 v26,v26,v74
v_add_f32 v27,v27,v75
M 18,a[64:67],34
s_add_u32 s79,s54,s46
R 62,1,15360
v_add_f32 v28,v28,v76
M 22,a[68:71],34
s_and_b32 s79,s79,127
v_add_f32 v29,v29,v77
v_add_f32 v30,v30,v78
M 18,a[72:75],38
s_cmp_eq_u32 s79,0
R 34,1,16384
v_add_f32 v31,v31,v79
M 22,a[76:79],38
L 5
v_add_f32 v32,v32,v80
v_add_f32 v33,v33,v81
M 18,a[80:83],42
s_cselect_b32 s95,0x3300,0
R 38,1,17408
v_cvt_pk_bf16_f32 v82,v26,v27
M 22,a[84:87],42
s_cmp_eq_u32 s79,127
v_cvt_pk_bf16_f32 v83,v28,v29
v_cvt_pk_bf16_f32 v84,v30,v31
M 18,a[88:91],46
s_cselect_b32 s95,0xcc00,s95
R 42,1,18432
v_cvt_pk_bf16_f32 v85,v32,v33
M 22,a[92:95],46
L 5
buffer_store_dwordx4 v[82:85],v0,s[36:39],s67 offen offset:0 sc0 sc1
v_add_f32 v74,v26,v27
M 18,a[96:99],50
s_add_u32 s65,s65,s62
R 46,1,19456
v_fma_f32 v75,v26,v26,s87
M 22,a[100:103],50
s_add_u32 s75,s75,s63
v_fma_f32 v75,v27,v27,v75
v_add_f32 v74,v74,v28
M 18,a[104:107],54
R 50,1,20480
v_fma_f32 v75,v28,v28,v75
M 22,a[108:111],54
L 5
v_add_f32 v74,v74,v29
v_fma_f32 v75,v29,v29,v75
M 18,a[112:115],58
R 54,1,21504
v_add_f32 v74,v74,v30
M 22,a[116:119],58
v_fma_f32 v75,v30,v30,v75
v_add_f32 v74,v74,v31
M 18,a[120:123],62
R 58,1,22528
v_fma_f32 v75,v31,v31,v75
M 22,a[124:127],62
L 5
v_add_f32 v74,v74,v32
v_fma_f32 v75,v32,v32,v75
M 18,a[128:131],34
R 62,1,23552
v_add_f32 v74,v74,v33
M 22,a[132:135],34
v_fma_f32 v75,v33,v33,v75
M 18,a[136:139],38
R 34,1,24576
M 22,a[140:143],38
L 5
v_permlane16_swap_b32 v74,v75
M 18,a[144:147],42
R 38,1,25600
v_add_f32 v74,v74,v75
M 22,a[148:151],42
v_mov_b32 v75,v74
M 18,a[152:155],46
R 42,1,26624
M 22,a[156:159],46
L 5
v_permlane32_swap_b32 v74,v75
M 18,a[160:163],50
R 46,1,27648
v_add_f32 v74,v74,v75
M 22,a[164:167],50
ds_write_b32 v12,v74 offset:2048
M 18,a[168:171],54
R 50,1,28672
M 22,a[172:175],54
L 6
M 18,a[176:179],58
R 54,1,29696
M 22,a[180:183],58
M 18,a[184:187],62
R 58,1,30720
M 22,a[188:191],62
L 6
L 3
M 18,a[192:195],34
s_waitcnt vmcnt(1)
R 62,1,31744
v_xor_b32 v1,65536,v1
M 22,a[196:199],34
s_barrier
R 86,13,2048
ds_read_b32 v7,v6 offset:0
M 18,a[200:203],38
R 34,2,0
M 22,a[204:207],38
s_nop 0
buffer_store_dword v4,v3,s[36:39],s70 offen offset:0 sc0 sc1
M 18,a[208:211],42
s_mov_b32 s70,s69
s_mov_b32 s69,s68
R 38,2,1024
M 22,a[212:215],42
s_add_u32 s68,s68,s63
M 18,a[216:219],46
L 3
R 42,2,2048
v_add_f32 v86,v86,v87
M 22,a[220:223],46
s_nop 0
v_add_f32 v88,v88,v89
v_add_f32 v86,v86,v88
M 18,a[224:227],50
L 3
R 46,2,3072
v_mul_f32 v86,s86,v86
M 22,a[228:231],50
buffer_store_dword v86,v14,s[36:39],s77 offen offset:0 sc0 sc1
M 18,a[232:235],54
R 50,2,4096
v_cmp_eq_u32 vcc,s53,v7
M 22,a[236:239],54
s_nop 0
s_or_b64 vcc,vcc,s[94:95]
s_cmp_eq_u64 vcc,-1
M 18,a[240:243],58
R 54,2,5120
M 22,a[244:247],58
M 18,a[248:251],62
R 58,2,6144
M 22,a[252:255],62
L 5
s_cbranch_scc1 Lg2_L21_%=
s_cmp_eq_u32 vcc_lo,-1
s_cbranch_scc1 Lg2_L11_%=
Lg2_X1_%=:
v_mfma_f32_16x16x32_bf16 v[26:29],a[0:3],v[34:37],0
s_add_u32 m0,s59,1024
R 62,2,7168
v_lshlrev_b32 v74,16,v90
v_mfma_f32_16x16x32_bf16 v[30:33],a[4:7],v[34:37],0
s_mov_b32 s67,s65
buffer_load_dword v5,s[36:39],s58 offen offset:0 sc1 lds
M 26,a[8:11],38
s_mov_b32 s77,s75
R 34,2,8192
v_and_b32 v75,0xffff0000,v90
M 30,a[12:15],38
L 5
buffer_load_dwordx4 v[94:97],v0,s[36:39],s74 offen offset:0 sc1
M 26,a[16:19],42
R 38,2,9216
v_lshlrev_b32 v76,16,v91
M 30,a[20:23],42
buffer_load_dwordx2 v[70:71],v11,s[36:39],s73 offen offset:16 sc1
M 26,a[24:27],46
s_add_u32 s73,s73,s64
R 42,2,10240
v_and_b32 v77,0xffff0000,v91
M 30,a[28:31],46
L 5
v_lshlrev_b32 v78,16,v92
v_and_b32 v79,0xffff0000,v92
M 26,a[32:35],50
s_add_u32 s74,s74,s62
R 46,2,11264
v_lshlrev_b32 v80,16,v93
M 30,a[36:39],50
s_and_b32 s79,s54,127
v_and_b32 v81,0xffff0000,v93
v_fma_f32 v74,v74,v67,v66
M 26,a[40:43],54
s_cmp_eq_u32 s79,0
R 50,2,12288
v_fma_f32 v75,v75,v67,v66
M 30,a[44:47],54
L 5
v_fma_f32 v76,v76,v67,v66
v_fma_f32 v77,v77,v67,v66
M 26,a[48:51],58
s_cselect_b32 s94,0x3300,0
R 54,2,13312
v_fma_f32 v78,v78,v67,v66
M 30,a[52:55],58
s_cmp_eq_u32 s79,127
v_fma_f32 v79,v79,v67,v66
v_fma_f32 v80,v80,v67,v66
M 26,a[56:59],62
s_cselect_b32 s94,0xcc00,s94
R 58,2,14336
v_fma_f32 v81,v81,v67,v66
M 30,a[60:63],62
L 5
v_add_f32 v18,v18,v74
v_add_f32 v19,v19,v75
M 26,a[64:67],34
s_add_u32 s79,s54,s46
R 62,2,15360
v_add_f32 v20,v20,v76
M 30,a[68:71],34
s_and_b32 s79,s79,127
v_add_f32 v21,v21,v77
v_add_f32 v22,v22,v78
M 26,a[72:75],38
s_cmp_eq_u32 s79,0
R 34,2,16384
v_add_f32 v23,v23,v79
M 30,a[76:79],38
L 5
v_add_f32 v24,v24,v80
v_add_f32 v25,v25,v81
M 26,a[80:83],42
s_cselect_b32 s95,0x3300,0
R 38,2,17408
v_cvt_pk_bf16_f32 v82,v18,v19
M 30,a[84:87],42
s_cmp_eq_u32 s79,127
v_cvt_pk_bf16_f32 v83,v20,v21
v_cvt_pk_bf16_f32 v84,v22,v23
M 26,a[88:91],46
s_cselect_b32 s95,0xcc00,s95
R 42,2,18432
v_cvt_pk_bf16_f32 v85,v24,v25
M 30,a[92:95],46
L 5
buffer_store_dwordx4 v[82:85],v0,s[36:39],s66 offen offset:0 sc0 sc1
v_add_f32 v74,v18,v19
M 26,a[96:99],50
s_add_u32 s65,s65,s62
)"
R"(R 46,2,19456
v_fma_f32 v75,v18,v18,s87
M 30,a[100:103],50
s_add_u32 s75,s75,s63
v_fma_f32 v75,v19,v19,v75
v_add_f32 v74,v74,v20
M 26,a[104:107],54
R 50,2,20480
v_fma_f32 v75,v20,v20,v75
M 30,a[108:111],54
L 5
v_add_f32 v74,v74,v21
v_fma_f32 v75,v21,v21,v75
M 26,a[112:115],58
R 54,2,21504
v_add_f32 v74,v74,v22
M 30,a[116:119],58
v_fma_f32 v75,v22,v22,v75
v_add_f32 v74,v74,v23
M 26,a[120:123],62
R 58,2,22528
v_fma_f32 v75,v23,v23,v75
M 30,a[124:127],62
L 5
v_add_f32 v74,v74,v24
v_fma_f32 v75,v24,v24,v75
M 26,a[128:131],34
R 62,2,23552
v_add_f32 v74,v74,v25
M 30,a[132:135],34
v_fma_f32 v75,v25,v25,v75
M 26,a[136:139],38
R 34,2,24576
M 30,a[140:143],38
L 5
v_permlane16_swap_b32 v74,v75
M 26,a[144:147],42
R 38,2,25600
v_add_f32 v74,v74,v75
M 30,a[148:151],42
v_mov_b32 v75,v74
M 26,a[152:155],46
R 42,2,26624
M 30,a[156:159],46
L 5
v_permlane32_swap_b32 v74,v75
M 26,a[160:163],50
R 46,2,27648
v_add_f32 v74,v74,v75
M 30,a[164:167],50
ds_write_b32 v12,v74 offset:0
M 26,a[168:171],54
R 50,2,28672
M 30,a[172:175],54
L 6
M 26,a[176:179],58
R 54,2,29696
M 30,a[180:183],58
M 26,a[184:187],62
R 58,2,30720
M 30,a[188:191],62
L 6
L 3
M 26,a[192:195],34
s_waitcnt vmcnt(1)
R 62,2,31744
v_xor_b32 v2,65536,v2
M 30,a[196:199],34
s_barrier
R 86,13,0
ds_read_b32 v7,v6 offset:1024
M 26,a[200:203],38
buffer_store_dword v4,v3,s[36:39],s70 offen offset:0 sc0 sc1
M 30,a[204:207],38
s_nop 0
s_mov_b32 s70,s69
s_mov_b32 s69,s68
M 26,a[208:211],42
s_add_u32 s68,s68,s63
M 30,a[212:215],42
M 26,a[216:219],46
L 1
v_add_f32 v86,v86,v87
v_add_f32 v88,v88,v89
M 30,a[220:223],46
s_nop 0
v_add_f32 v86,v86,v88
v_mul_f32 v86,s86,v86
M 26,a[224:227],50
L 0
buffer_store_dword v86,v14,s[36:39],s76 offen offset:0 sc0 sc1
M 30,a[228:231],50
v_cmp_eq_u32 vcc,s53,v7
M 26,a[232:235],54
s_or_b64 vcc,vcc,s[94:95]
M 30,a[236:239],54
s_nop 0
M 26,a[240:243],58
M 30,a[244:247],58
M 26,a[248:251],62
M 30,a[252:255],62
s_mov_b32 s80,0
s_cmp_eq_u32 vcc_lo,-1
s_cbranch_scc0 Lg2_xn1_%=
s_and_b32 s78,s54,127
s_cmp_eq_u32 s78,s85
s_cselect_b64 s[40:41],-1,0
v_cndmask_b32 v17,v15,v16,s[40:41]
s_mov_b32 m0,s57
s_add_u32 s56,s55,4096
D 17,55,0
v_lshlrev_b32 v74,16,v94
v_and_b32 v75,0xffff0000,v94
v_lshlrev_b32 v76,16,v95
v_and_b32 v77,0xffff0000,v95
v_lshlrev_b32 v78,16,v96
D 17,55,1024
v_and_b32 v79,0xffff0000,v96
v_lshlrev_b32 v80,16,v97
v_and_b32 v81,0xffff0000,v97
v_fma_f32 v74,v74,v71,v70
v_fma_f32 v75,v75,v71,v70
D 17,55,2048
v_fma_f32 v76,v76,v71,v70
v_fma_f32 v77,v77,v71,v70
v_fma_f32 v78,v78,v71,v70
v_fma_f32 v79,v79,v71,v70
v_fma_f32 v80,v80,v71,v70
D 17,55,3072
v_fma_f32 v81,v81,v71,v70
v_add_f32 v26,v26,v74
v_add_f32 v27,v27,v75
v_add_f32 v28,v28,v76
v_add_f32 v29,v29,v77
s_add_u32 m0,s57,4096
s_nop 0
D 17,56,0
v_add_f32 v30,v30,v78
v_add_f32 v31,v31,v79
v_add_f32 v32,v32,v80
v_add_f32 v33,v33,v81
v_cvt_pk_bf16_f32 v82,v26,v27
D 17,56,1024
v_cvt_pk_bf16_f32 v83,v28,v29
v_cvt_pk_bf16_f32 v84,v30,v31
v_cvt_pk_bf16_f32 v85,v32,v33
D 17,56,2048
D 17,56,3072
buffer_store_dwordx4 v[82:85],v0,s[36:39],s67 offen offset:0 sc0 sc1
v_add_f32 v74,v26,v27
v_fma_f32 v75,v26,v26,s87
s_add_u32 s55,s55,s60
s_add_u32 s57,s57,32768
s_and_b32 s57,s57,131071
s_add_u32 s54,s54,s46
v_fma_f32 v75,v27,v27,v75
v_add_f32 v74,v74,v28
v_fma_f32 v75,v28,v28,v75
v_add_f32 v74,v74,v29
v_fma_f32 v75,v29,v29,v75
v_add_f32 v74,v74,v30
v_fma_f32 v75,v30,v30,v75
v_add_f32 v74,v74,v31
v_fma_f32 v75,v31,v31,v75
v_add_f32 v74,v74,v32
v_fma_f32 v75,v32,v32,v75
v_add_f32 v74,v74,v33
v_fma_f32 v75,v33,v33,v75
s_nop 1
v_permlane16_swap_b32 v74,v75
v_add_f32 v74,v74,v75
v_mov_b32 v75,v74
s_nop 1
v_permlane32_swap_b32 v74,v75
v_add_f32 v74,v74,v75
ds_write_b32 v12,v74 offset:2048
L 0
s_barrier
R 86,13,2048
L 0
v_add_f32 v86,v86,v87
v_add_f32 v88,v88,v89
v_add_f32 v86,v86,v88
v_mul_f32 v86,s86,v86
buffer_store_dword v86,v14,s[36:39],s77 offen offset:0 sc0 sc1
s_mov_b32 s72,s69
s_mov_b32 s69,0x7fff0000
s_mov_b32 s67,0x7fff0000
s_mov_b32 s77,0x7fff0000
s_branch Lg2_gox_%=
Lg2_xn1_%=:
s_cmp_ge_u32 s54,256
s_cbranch_scc1 Lg2_fin1_%=
s_add_u32 m0,s59,4096
s_nop 0
buffer_load_dword v5,s[36:39],s58 offen offset:0 sc1 lds
buffer_store_dword v4,v3,s[36:39],s71 offen offset:0 sc0 sc1
s_add_u32 m0,s59,5120
s_nop 0
buffer_load_dword v5,s[36:39],s58 offen offset:0 sc1 lds
buffer_store_dword v4,v3,s[36:39],s71 offen offset:0 sc0 sc1
s_mov_b32 s83,0
v_lshlrev_b32 v74,16,v94
v_and_b32 v75,0xffff0000,v94
v_lshlrev_b32 v76,16,v95
v_and_b32 v77,0xffff0000,v95
v_lshlrev_b32 v78,16,v96
v_and_b32 v79,0xffff0000,v96
v_lshlrev_b32 v80,16,v97
v_and_b32 v81,0xffff0000,v97
v_fma_f32 v74,v74,v71,v70
v_fma_f32 v75,v75,v71,v70
v_fma_f32 v76,v76,v71,v70
v_fma_f32 v77,v77,v71,v70
v_fma_f32 v78,v78,v71,v70
v_fma_f32 v79,v79,v71,v70
v_fma_f32 v80,v80,v71,v70
v_fma_f32 v81,v81,v71,v70
v_add_f32 v26,v26,v74
v_add_f32 v27,v27,v75
v_add_f32 v28,v28,v76
v_add_f32 v29,v29,v77
v_add_f32 v30,v30,v78
v_add_f32 v31,v31,v79
v_add_f32 v32,v32,v80
v_add_f32 v33,v33,v81
v_cvt_pk_bf16_f32 v82,v26,v27
v_cvt_pk_bf16_f32 v83,v28,v29
v_cvt_pk_bf16_f32 v84,v30,v31
v_cvt_pk_bf16_f32 v85,v32,v33
buffer_store_dwordx4 v[82:85],v0,s[36:39],s67 offen offset:0 sc0 sc1
v_add_f32 v74,v26,v27
v_fma_f32 v75,v26,v26,s87
v_fma_f32 v75,v27,v27,v75
v_add_f32 v74,v74,v28
v_fma_f32 v75,v28,v28,v75
v_add_f32 v74,v74,v29
v_fma_f32 v75,v29,v29,v75
v_add_f32 v74,v74,v30
v_fma_f32 v75,v30,v30,v75
v_add_f32 v74,v74,v31
v_fma_f32 v75,v31,v31,v75
v_add_f32 v74,v74,v32
v_fma_f32 v75,v32,v32,v75
v_add_f32 v74,v74,v33
v_fma_f32 v75,v33,v33,v75
s_nop 1
v_permlane16_swap_b32 v74,v75
v_add_f32 v74,v74,v75
v_mov_b32 v75,v74
s_nop 1
v_permlane32_swap_b32 v74,v75
v_add_f32 v74,v74,v75
ds_write_b32 v12,v74 offset:2048
L 0
s_barrier
R 86,13,2048
L 0
v_add_f32 v86,v86,v87
v_add_f32 v88,v88,v89
v_add_f32 v86,v86,v88
v_mul_f32 v86,s86,v86
buffer_store_dword v86,v14,s[36:39],s77 offen offset:0 sc0 sc1
s_mov_b32 s72,s69
s_mov_b32 s69,0x7fff0000
s_mov_b32 s67,0x7fff0000
s_mov_b32 s77,0x7fff0000
s_branch Lg2_poll_%=
Lg2_fin1_%=:
v_lshlrev_b32 v74,16,v94
v_and_b32 v75,0xffff0000,v94
v_lshlrev_b32 v76,16,v95
v_and_b32 v77,0xffff0000,v95
v_lshlrev_b32 v78,16,v96
v_and_b32 v79,0xffff0000,v96
v_lshlrev_b32 v80,16,v97
v_and_b32 v81,0xffff0000,v97
v_fma_f32 v74,v74,v71,v70
v_fma_f32 v75,v75,v71,v70
v_fma_f32 v76,v76,v71,v70
v_fma_f32 v77,v77,v71,v70
v_fma_f32 v78,v78,v71,v70
v_fma_f32 v79,v79,v71,v70
v_fma_f32 v80,v80,v71,v70
v_fma_f32 v81,v81,v71,v70
v_add_f32 v26,v26,v74
v_add_f32 v27,v27,v75
v_add_f32 v28,v28,v76
v_add_f32 v29,v29,v77
v_add_f32 v30,v30,v78
v_add_f32 v31,v31,v79
v_add_f32 v32,v32,v80
v_add_f32 v33,v33,v81
v_cvt_pk_bf16_f32 v82,v26,v27
v_cvt_pk_bf16_f32 v83,v28,v29
v_cvt_pk_bf16_f32 v84,v30,v31
v_cvt_pk_bf16_f32 v85,v32,v33
buffer_store_dwordx4 v[82:85],v0,s[36:39],s67 offen offset:0 sc0 sc1
v_add_f32 v74,v26,v27
v_fma_f32 v75,v26,v26,s87
v_fma_f32 v75,v27,v27,v75
v_add_f32 v74,v74,v28
v_fma_f32 v75,v28,v28,v75
v_add_f32 v74,v74,v29
v_fma_f32 v75,v29,v29,v75
v_add_f32 v74,v74,v30
v_fma_f32 v75,v30,v30,v75
v_add_f32 v74,v74,v31
v_fma_f32 v75,v31,v31,v75
v_add_f32 v74,v74,v32
v_fma_f32 v75,v32,v32,v75
v_add_f32 v74,v74,v33
v_fma_f32 v75,v33,v33,v75
s_nop 1
v_permlane16_swap_b32 v74,v75
v_add_f32 v74,v74,v75
v_mov_b32 v75,v74
s_nop 1
v_permlane32_swap_b32 v74,v75
v_add_f32 v74,v74,v75
ds_write_b32 v12,v74 offset:2048
L 0
s_barrier
R 86,13,2048
L 0
v_add_f32 v86,v86,v87
v_add_f32 v88,v88,v89
v_add_f32 v86,v86,v88
v_mul_f32 v86,s86,v86
buffer_store_dword v86,v14,s[36:39],s77 offen offset:0 sc0 sc1
s_mov_b32 s72,s69
s_mov_b32 s69,0x7fff0000
s_mov_b32 s67,0x7fff0000
s_mov_b32 s77,0x7fff0000
s_branch Lg2_done_%=
Lg2_L10_%=:
v_mfma_f32_16x16x32_bf16 v[18:21],a[0:3],v[34:37],0
s_add_u32 s58,s58,s61
R 62,1,7168
v_lshlrev_b32 v74,16,v94
v_mfma_f32_16x16x32_bf16 v[22:25],a[4:7],v[34:37],0
s_add_u32 m0,s59,0
buffer_load_dwordx4 v[90:93],v0,s[36:39],s74 offen offset:0 sc1
M 18,a[8:11],38
s_mov_b32 s66,s65
R 34,1,8192
v_and_b32 v75,0xffff0000,v94
M 22,a[12:15],38
L 5
buffer_load_dword v5,s[36:39],s58 offen offset:0 sc1 lds
M 18,a[16:19],42
s_mov_b32 m0,s57
R 38,1,9216
v_lshlrev_b32 v76,16,v95
M 22,a[20:23],42
s_mov_b32 s76,s75
buffer_load_dwordx2 v[66:67],v11,s[36:39],s73 offen offset:16 sc1
M 18,a[24:27],46
s_add_u32 s73,s73,s64
R 42,1,10240
v_and_b32 v77,0xffff0000,v95
M 22,a[28:31],46
L 5
v_lshlrev_b32 v78,16,v96
v_and_b32 v79,0xffff0000,v96
M 18,a[32:35],50
s_add_u32 s74,s74,s62
R 46,1,11264
v_lshlrev_b32 v80,16,v97
M 22,a[36:39],50
s_and_b32 s78,s54,127
v_and_b32 v81,0xffff0000,v97
v_fma_f32 v74,v74,v71,v70
M 18,a[40:43],54
s_cmp_eq_u32 s78,s85
R 50,1,12288
v_fma_f32 v75,v75,v71,v70
M 22,a[44:47],54
L 5
v_fma_f32 v76,v76,v71,v70
v_fma_f32 v77,v77,v71,v70
M 18,a[48:51],58
s_cselect_b64 s[40:41],-1,0
R 54,1,13312
v_cndmask_b32 v17,v15,v16,s[40:41]
M 22,a[52:55],58
s_add_u32 s56,s55,4096
D 17,55,0
M 18,a[56:59],62
R 58,1,14336
v_fma_f32 v78,v78,v71,v70
M 22,a[60:63],62
L 5
D 17,55,1024
M 18,a[64:67],34
R 62,1,15360
v_fma_f32 v79,v79,v71,v70
M 22,a[68:71],34
D 17,55,2048
M 18,a[72:75],38
R 34,1,16384
v_fma_f32 v80,v80,v71,v70
M 22,a[76:79],38
L 5
D 17,55,3072
M 18,a[80:83],42
s_add_u32 m0,s57,4096
R 38,1,17408
v_fma_f32 v81,v81,v71,v70
M 22,a[84:87],42
s_add_u32 s55,s55,s60
v_add_f32 v26,v26,v74
v_add_f32 v27,v27,v75
M 18,a[88:91],46
s_add_u32 s57,s57,32768
R 42,1,18432
v_add_f32 v28,v28,v76
M 22,a[92:95],46
L 5
D 17,56,0
M 18,a[96:99],50
s_and_b32 s57,s57,131071
R 46,1,19456
v_add_f32 v29,v29,v77
M 22,a[100:103],50
s_add_u32 s54,s54,s46
v_add_f32 v30,v30,v78
v_add_f32 v31,v31,v79
M 18,a[104:107],54
s_and_b32 s79,s54,127
R 50,1,20480
v_add_f32 v32,v32,v80
M 22,a[108:111],54
L 5
D 17,56,1024
M 18,a[112:115],58
s_cmp_eq_u32 s79,0
R 54,1,21504
v_fma_f32 v75,v26,v26,s87
M 22,a[116:119],58
s_cselect_b32 s94,0x3300,0
v_add_f32 v74,v26,v27
v_fma_f32 v75,v27,v27,v75
M 18,a[120:123],62
s_cmp_eq_u32 s79,127
R 58,1,22528
v_add_f32 v74,v74,v28
M 22,a[124:127],62
L 5
D 17,56,2048
M 18,a[128:131],34
s_cselect_b32 s94,0xcc00,s94
R 62,1,23552
v_fma_f32 v75,v28,v28,v75
M 22,a[132:135],34
s_add_u32 s79,s54,s46
v_add_f32 v74,v74,v29
v_fma_f32 v75,v29,v29,v75
M 18,a[136:139],38
s_and_b32 s79,s79,127
R 34,1,24576
v_add_f32 v74,v74,v30
M 22,a[140:143],38
L 5
D 17,56,3072
M 18,a[144:147],42
s_cmp_eq_u32 s79,0
R 38,1,25600
v_fma_f32 v75,v30,v30,v75
M 22,a[148:151],42
s_cselect_b32 s95,0x3300,0
v_add_f32 v74,v74,v31
v_fma_f32 v75,v31,v31,v75
M 18,a[152:155],46
s_cmp_eq_u32 s79,127
R 42,1,26624
v_add_f32 v33,v33,v81
M 22,a[156:159],46
L 5
v_add_f32 v74,v74,v32
v_fma_f32 v75,v32,v32,v75
v_add_f32 v74,v74,v33
v_fma_f32 v75,v33,v33,v75
M 18,a[160:163],50
s_cselect_b32 s95,0xcc00,s95
R 46,1,27648
v_cvt_pk_bf16_f32 v82,v26,v27
M 22,a[164:167],50
s_add_u32 s65,s65,s62
v_permlane16_swap_b32 v74,v75
v_add_f32 v74,v74,v75
v_mov_b32 v75,v74
M 18,a[168:171],54
s_add_u32 s75,s75,s63
R 50,1,28672
v_cvt_pk_bf16_f32 v83,v28,v29
M 22,a[172:175],54
L 5
v_permlane32_swap_b32 v74,v75
v_add_f32 v74,v74,v75
)"
R"(M 18,a[176:179],58
ds_write_b32 v12,v74 offset:2048
R 54,1,29696
M 22,a[180:183],58
v_cvt_pk_bf16_f32 v84,v30,v31
v_cvt_pk_bf16_f32 v85,v32,v33
M 18,a[184:187],62
R 58,1,30720
M 22,a[188:191],62
L 6
L 2
buffer_store_dwordx4 v[82:85],v0,s[36:39],s67 offen offset:0 sc0 sc1
M 18,a[192:195],34
s_waitcnt vmcnt(1)
R 62,1,31744
v_xor_b32 v1,65536,v1
M 22,a[196:199],34
s_barrier
R 86,13,2048
ds_read_b32 v7,v6 offset:0
M 18,a[200:203],38
R 34,2,0
M 22,a[204:207],38
s_nop 0
buffer_store_dword v4,v3,s[36:39],s70 offen offset:0 sc0 sc1
M 18,a[208:211],42
s_mov_b32 s70,s69
s_mov_b32 s69,s68
R 38,2,1024
M 22,a[212:215],42
s_add_u32 s68,s68,s63
M 18,a[216:219],46
L 3
R 42,2,2048
v_add_f32 v86,v86,v87
M 22,a[220:223],46
s_nop 0
v_add_f32 v88,v88,v89
v_add_f32 v86,v86,v88
M 18,a[224:227],50
L 3
R 46,2,3072
v_mul_f32 v86,s86,v86
M 22,a[228:231],50
buffer_store_dword v86,v14,s[36:39],s77 offen offset:0 sc0 sc1
M 18,a[232:235],54
R 50,2,4096
v_cmp_eq_u32 vcc,s53,v7
M 22,a[236:239],54
s_nop 0
s_or_b64 vcc,vcc,s[94:95]
s_cmp_eq_u64 vcc,-1
M 18,a[240:243],58
R 54,2,5120
M 22,a[244:247],58
M 18,a[248:251],62
R 58,2,6144
M 22,a[252:255],62
L 5
s_cbranch_scc1 Lg2_L21_%=
s_cmp_eq_u32 vcc_lo,-1
s_cbranch_scc1 Lg2_L11_%=
s_branch Lg2_X1_%=
Lg2_L11_%=:
v_mfma_f32_16x16x32_bf16 v[26:29],a[0:3],v[34:37],0
s_add_u32 s58,s58,s61
R 62,2,7168
v_lshlrev_b32 v74,16,v90
v_mfma_f32_16x16x32_bf16 v[30:33],a[4:7],v[34:37],0
s_add_u32 m0,s59,1024
buffer_load_dwordx4 v[94:97],v0,s[36:39],s74 offen offset:0 sc1
M 26,a[8:11],38
s_mov_b32 s67,s65
R 34,2,8192
v_and_b32 v75,0xffff0000,v90
M 30,a[12:15],38
L 5
buffer_load_dword v5,s[36:39],s58 offen offset:0 sc1 lds
M 26,a[16:19],42
s_mov_b32 m0,s57
R 38,2,9216
v_lshlrev_b32 v76,16,v91
M 30,a[20:23],42
s_mov_b32 s77,s75
buffer_load_dwordx2 v[70:71],v11,s[36:39],s73 offen offset:16 sc1
M 26,a[24:27],46
s_add_u32 s73,s73,s64
R 42,2,10240
v_and_b32 v77,0xffff0000,v91
M 30,a[28:31],46
L 5
v_lshlrev_b32 v78,16,v92
v_and_b32 v79,0xffff0000,v92
M 26,a[32:35],50
s_add_u32 s74,s74,s62
R 46,2,11264
v_lshlrev_b32 v80,16,v93
M 30,a[36:39],50
s_and_b32 s78,s54,127
v_and_b32 v81,0xffff0000,v93
v_fma_f32 v74,v74,v67,v66
M 26,a[40:43],54
s_cmp_eq_u32 s78,s85
R 50,2,12288
v_fma_f32 v75,v75,v67,v66
M 30,a[44:47],54
L 5
v_fma_f32 v76,v76,v67,v66
v_fma_f32 v77,v77,v67,v66
M 26,a[48:51],58
s_cselect_b64 s[40:41],-1,0
R 54,2,13312
v_cndmask_b32 v17,v15,v16,s[40:41]
M 30,a[52:55],58
s_add_u32 s56,s55,4096
D 17,55,0
M 26,a[56:59],62
R 58,2,14336
v_fma_f32 v78,v78,v67,v66
M 30,a[60:63],62
L 5
D 17,55,1024
M 26,a[64:67],34
R 62,2,15360
v_fma_f32 v79,v79,v67,v66
M 30,a[68:71],34
D 17,55,2048
M 26,a[72:75],38
R 34,2,16384
v_fma_f32 v80,v80,v67,v66
M 30,a[76:79],38
L 5
D 17,55,3072
M 26,a[80:83],42
s_add_u32 m0,s57,4096
R 38,2,17408
v_fma_f32 v81,v81,v67,v66
M 30,a[84:87],42
s_add_u32 s55,s55,s60
v_add_f32 v18,v18,v74
v_add_f32 v19,v19,v75
M 26,a[88:91],46
s_add_u32 s57,s57,32768
R 42,2,18432
v_add_f32 v20,v20,v76
M 30,a[92:95],46
L 5
D 17,56,0
M 26,a[96:99],50
s_and_b32 s57,s57,131071
R 46,2,19456
v_add_f32 v21,v21,v77
M 30,a[100:103],50
s_add_u32 s54,s54,s46
v_add_f32 v22,v22,v78
v_add_f32 v23,v23,v79
M 26,a[104:107],54
s_and_b32 s79,s54,127
R 50,2,20480
v_add_f32 v24,v24,v80
M 30,a[108:111],54
L 5
D 17,56,1024
M 26,a[112:115],58
s_cmp_eq_u32 s79,0
R 54,2,21504
v_fma_f32 v75,v18,v18,s87
M 30,a[116:119],58
s_cselect_b32 s94,0x3300,0
v_add_f32 v74,v18,v19
v_fma_f32 v75,v19,v19,v75
M 26,a[120:123],62
s_cmp_eq_u32 s79,127
R 58,2,22528
v_add_f32 v74,v74,v20
M 30,a[124:127],62
L 5
D 17,56,2048
M 26,a[128:131],34
s_cselect_b32 s94,0xcc00,s94
R 62,2,23552
v_fma_f32 v75,v20,v20,v75
M 30,a[132:135],34
s_add_u32 s79,s54,s46
v_add_f32 v74,v74,v21
v_fma_f32 v75,v21,v21,v75
M 26,a[136:139],38
s_and_b32 s79,s79,127
R 34,2,24576
v_add_f32 v74,v74,v22
M 30,a[140:143],38
L 5
D 17,56,3072
M 26,a[144:147],42
s_cmp_eq_u32 s79,0
R 38,2,25600
v_fma_f32 v75,v22,v22,v75
M 30,a[148:151],42
s_cselect_b32 s95,0x3300,0
v_add_f32 v74,v74,v23
v_fma_f32 v75,v23,v23,v75
M 26,a[152:155],46
s_cmp_eq_u32 s79,127
R 42,2,26624
v_add_f32 v25,v25,v81
M 30,a[156:159],46
L 5
v_add_f32 v74,v74,v24
v_fma_f32 v75,v24,v24,v75
v_add_f32 v74,v74,v25
v_fma_f32 v75,v25,v25,v75
M 26,a[160:163],50
s_cselect_b32 s95,0xcc00,s95
R 46,2,27648
v_cvt_pk_bf16_f32 v82,v18,v19
M 30,a[164:167],50
s_add_u32 s65,s65,s62
v_permlane16_swap_b32 v74,v75
v_add_f32 v74,v74,v75
v_mov_b32 v75,v74
M 26,a[168:171],54
s_add_u32 s75,s75,s63
R 50,2,28672
v_cvt_pk_bf16_f32 v83,v20,v21
M 30,a[172:175],54
L 5
v_permlane32_swap_b32 v74,v75
v_add_f32 v74,v74,v75
M 26,a[176:179],58
ds_write_b32 v12,v74 offset:0
R 54,2,29696
M 30,a[180:183],58
v_cvt_pk_bf16_f32 v84,v22,v23
v_cvt_pk_bf16_f32 v85,v24,v25
M 26,a[184:187],62
R 58,2,30720
M 30,a[188:191],62
L 6
L 2
buffer_store_dwordx4 v[82:85],v0,s[36:39],s66 offen offset:0 sc0 sc1
M 26,a[192:195],34
s_waitcnt vmcnt(1)
R 62,2,31744
v_xor_b32 v2,65536,v2
M 30,a[196:199],34
s_barrier
R 86,13,0
ds_read_b32 v7,v6 offset:1024
M 26,a[200:203],38
R 34,1,0
M 30,a[204:207],38
s_nop 0
buffer_store_dword v4,v3,s[36:39],s70 offen offset:0 sc0 sc1
M 26,a[208:211],42
s_mov_b32 s70,s69
s_mov_b32 s69,s68
R 38,1,1024
M 30,a[212:215],42
s_add_u32 s68,s68,s63
M 26,a[216:219],46
L 3
R 42,1,2048
v_add_f32 v86,v86,v87
M 30,a[220:223],46
s_nop 0
v_add_f32 v88,v88,v89
v_add_f32 v86,v86,v88
M 26,a[224:227],50
L 3
R 46,1,3072
v_mul_f32 v86,s86,v86
M 30,a[228:231],50
buffer_store_dword v86,v14,s[36:39],s76 offen offset:0 sc0 sc1
M 26,a[232:235],54
R 50,1,4096
v_cmp_eq_u32 vcc,s53,v7
M 30,a[236:239],54
s_nop 0
s_or_b64 vcc,vcc,s[94:95]
s_cmp_eq_u64 vcc,-1
M 26,a[240:243],58
R 54,1,5120
M 30,a[244:247],58
M 26,a[248:251],62
R 58,1,6144
M 30,a[252:255],62
L 5
s_cbranch_scc1 Lg2_L20_%=
s_cmp_eq_u32 vcc_lo,-1
s_cbranch_scc1 Lg2_L10_%=
s_branch Lg2_X0_%=
Lg2_done_%=:
s_waitcnt vmcnt(2)
s_barrier
buffer_store_dword v4,v3,s[36:39],s70 offen offset:0 sc0 sc1
s_waitcnt vmcnt(1)
s_barrier
buffer_store_dword v4,v3,s[36:39],s72 offen offset:0 sc0 sc1
s_waitcnt vmcnt(0) lgkmcnt(0)
s_barrier
Lg2_end_%=:
s_mov_b32 %0,s53
Lcode_g12_e:
.purgem M
.purgem R
.purgem T
.purgem D
.purgem L
)"
  : "=&s"(epOut) : "s"(ws), "s"(sWave), "s"(sG0), "s"(sStep), "s"(sOutOff), "s"(sFoutOff), "s"(sAuxOff), "s"(sWw), "s"(sBuf), "s"(sRk), "v"(vL16), "v"(vFlag), "v"(vSnapOff), "v"(vSnapRd), "v"(vWd0), "v"(vWd1), "v"(vWr0), "v"(vWr1), "v"(vWr2), "v"(vOutS), "v"(vAux0), "v"(vAux1), "v"(vAux2), "v"(vAux3), "v"(vAn), "v"(vAe), "s"(w12) : "v0", "v1", "v2", "v3", "v4", "v5", "v6", "v7", "v8", "v9", "v10", "v11", "v12", "v13", "v14", "v15", "v16", "v17", "v18", "v19", "v20", "v21", "v22", "v23", "v24", "v25", "v26", "v27", "v28", "v29", "v30", "v31", "v32", "v33", "v34", "v35", "v36", "v37", "v38", "v39", "v40", "v41", "v42", "v43", "v44", "v45", "v46", "v47", "v48", "v49", "v50", "v51", "v52", "v53", "v54", "v55", "v56", "v57", "v58", "v59", "v60", "v61", "v62", "v63", "v64", "v65", "v66", "v67", "v68", "v69", "v70", "v71", "v72", "v73", "v74", "v75", "v76", "v77", "v78", "v79", "v80", "v81", "v82", "v83", "v84", "v85", "v86", "v87", "v88", "v89", "v90", "v91", "v92", "v93", "v94", "v95", "v96", "v97", "v98", "v99", "v100", "v101", "v102", "v103", "v104", "v105", "v106", "v107", "v108", "v109", "v110", "v111", "v112", "v113", "v114", "v115", "v116", "v117", "v118", "v119", "v120", "v121", "v122", "v123", "v124", "v125", "v126", "v127", "v128", "v129", "v130", "v131", "v132", "v133", "v134", "v135", "v136", "v137", "v138", "v139", "v140", "v141", "v142", "v143", "v144", "v145", "v146", "v147", "v148", "v149", "v150", "v151", "v152", "v153", "v154", "v155", "v156", "v157", "v158", "v159", "v160", "v161", "v162", "v163", "v164", "v165", "v166", "v167", "v168", "v169", "v170", "v171", "v172", "v173", "v174", "v175", "v176", "v177", "v178", "v179", "v180", "v181", "v182", "v183", "v184", "v185", "v186", "v187", "v188", "v189", "v190", "v191", "v192", "v193", "v194", "v195", "v196", "v197", "v198", "v199", "v200", "v201", "v202", "v203", "v204", "v205", "v206", "v207", "v208", "v209", "v210", "v211", "v212", "v213", "v214", "v215", "v216", "v217", "v218", "v219", "v220", "v221", "v222", "v223", "v224", "v225", "v226", "v227", "v228", "v229", "v230", "v231", "a0", "a1", "a2", "a3", "a4", "a5", "a6", "a7", "a8", "a9", "a10", "a11", "a12", "a13", "a14", "a15", "a16", "a17", "a18", "a19", "a20", "a21", "a22", "a23", "a24", "a25", "a26", "a27", "a28", "a29", "a30", "a31", "a32", "a33", "a34", "a35", "a36", "a37", "a38", "a39", "a40", "a41", "a42", "a43", "a44", "a45", "a46", "a47", "a48", "a49", "a50", "a51", "a52", "a53", "a54", "a55", "a56", "a57", "a58", "a59", "a60", "a61", "a62", "a63", "a64", "a65", "a66", "a67", "a68", "a69", "a70", "a71", "a72", "a73", "a74", "a75", "a76", "a77", "a78", "a79", "a80", "a81", "a82", "a83", "a84", "a85", "a86", "a87", "a88", "a89", "a90", "a91", "a92", "a93", "a94", "a95", "a96", "a97", "a98", "a99", "a100", "a101", "a102", "a103", "a104", "a105", "a106", "a107", "a108", "a109", "a110", "a111", "a112", "a113", "a114", "a115", "a116", "a117", "a118", "a119", "a120", "a121", "a122", "a123", "a124", "a125", "a126", "a127", "a128", "a129", "a130", "a131", "a132", "a133", "a134", "a135", "a136", "a137", "a138", "a139", "a140", "a141", "a142", "a143", "a144", "a145", "a146", "a147", "a148", "a149", "a150", "a151", "a152", "a153", "a154", "a155", "a156", "a157", "a158", "a159", "a160", "a161", "a162", "a163", "a164", "a165", "a166", "a167", "a168", "a169", "a170", "a171", "a172", "a173", "a174", "a175", "a176", "a177", "a178", "a179", "a180", "a181", "a182", "a183", "a184", "a185", "a186", "a187", "a188", "a189", "a190", "a191", "a192", "a193", "a194", "a195", "a196", "a197", "a198", "a199", "a200", "a201", "a202", "a203", "a204", "a205", "a206", "a207", "a208", "a209", "a210", "a211", "a212", "a213", "a214", "a215", "a216", "a217", "a218", "a219", "a220", "a221", "a222", "a223", "a224", "a225", "a226", "a227", "a228", "a229", "a230", "a231", "a232", "a233", "a234", "a235", "a236", "a237", "a238", "a239", "a240", "a241", "a242", "a243", "a244", "a245", "a246", "a247", "a248", "a249", "a250", "a251", "a252", "a253", "a254", "a255", "s36", "s37", "s38", "s39", "s40", "s41", "s42", "s43", "s44", "s45", "s46", "s47", "s48", "s49", "s50", "s51", "s52", "s53", "s54", "s55", "s56", "s57", "s58", "s59", "s60", "s61", "s62", "s63", "s64", "s65", "s66", "s67", "s68", "s69", "s70", "s71", "s72", "s73", "s74", "s75", "s76", "s77", "s78", "s79", "s80", "s81", "s82", "s83", "s84", "s85", "s86", "s87", "s88", "s89", "s90", "s91", "s92", "s93", "s94", "s95", "s96", "s97", "vcc", "scc", "memory");
      ep = epOut;
    }
    LANE_ID(lane2);
    for (int t = kind == 6 ? 64 + slice : (kind - 1) * 32 + rep * 8 + slice; t < 256; t += 82)
      row_last(ws, out, t, lane2, wave, ep);
    CTS(14);
    if (b == 0 && lane2 == 0 && wave == 0) __hip_atomic_store((gu32*)(ws + OFF_EP), ep, __ATOMIC_RELAXED, __HIP_MEMORY_SCOPE_AGENT);
    asm volatile("s_endpgm\nLcode_end:" ::: "memory");
    __builtin_unreachable();
  }
}

extern "C" void kernel_launch(void* const* d_in, const int* in_sizes, int n_in, void* d_out, int out_size,
                              void* d_ws, size_t ws_size, hipStream_t stream) {
  const u16* q = (const u16*)d_in[0];
  const u16* k = (const u16*)d_in[1];
  const u16* v = (const u16*)d_in[2];
  const u16* shift_k = (const u16*)d_in[3];
  const u16* ssm_k = (const u16*)d_in[4];
  const u16* ssm_D = (const u16*)d_in[5];
  const u16* w1 = (const u16*)d_in[7];
  const u16* w2 = (const u16*)d_in[8];
  const u16* w3 = (const u16*)d_in[9];
  const u16* w4 = (const u16*)d_in[10];
  flow_kernel<<<256, 256, 0, stream>>>(q, k, v, shift_k, ssm_k, ssm_D, w1, w2, w3, w4, (char*)d_ws, (u16*)d_out);
}
```

```cpp
#include <hip/hip_runtime.h>
#include <hip/hip_bf16.h>
#include <stdint.h>
#include <type_traits>

typedef short s16x8 __attribute__((ext_vector_type(8)));
typedef short s16x4 __attribute__((ext_vector_type(4)));
typedef float f32x4 __attribute__((ext_vector_type(4)));
typedef unsigned short u16;
typedef unsigned short u16x8 __attribute__((ext_vector_type(8)));
typedef unsigned short u16x4 __attribute__((ext_vector_type(4)));
typedef float f32x2 __attribute__((ext_vector_type(2)));
typedef __bf16 bf16x2 __attribute__((ext_vector_type(2)));
typedef unsigned u32x4v __attribute__((ext_vector_type(4)));
typedef __attribute__((address_space(1))) unsigned gu32;

constexpr int S_ = 2048, D_ = 1024, F_ = 3584;
#define PF 1

constexpr int WS_USED = 111243392;
constexpr int OFF_KVR = 4194304, OFF_XP = 8388608, OFF_HS = 17825792, OFF_X1 = 31457280, OFF_G = 40894464, OFF_P0 = 71303168, OFF_P1 = 89128960, OFF_Y = 98566144, OFF_S1 = 106954752, OFF_ST = 107479040, OFF_FL = 109051904, OFF_FXP = 109056000, OFF_FH = 109334528, OFF_FX1 = 109613056, OFF_FG = 109891584, OFF_FP = 110587904, OFF_FKV = 111144960, OFF_FY = 111177728, OFF_EP = 111243264, OFF_DBG = 115343360;

#define WS_DIAG 0

__device__ __forceinline__ float bf2f(u16 v) { return __uint_as_float(((unsigned)v) << 16); }
__device__ __forceinline__ u16 f2bf(float f) { return __builtin_bit_cast(u16, (__bf16)f); }
__device__ __forceinline__ float rbf(float f) { return bf2f(f2bf(f)); }
__device__ __forceinline__ void st16(void* p, u16x8 v) {
  asm volatile("global_store_dwordx4 %0, %1, off sc0 sc1\n\ts_nop 1" :: "v"(p), "v"(v) : "memory");
}
#define WAIT_VM(n) asm volatile("s_waitcnt vmcnt(%0)" ::"n"(n) : "memory")
#define WAIT_LGKM0() asm volatile("s_waitcnt lgkmcnt(0)" ::: "memory")

#define CODE_RANGE(B, E, p, n)                                                                                   \
  const char* p; unsigned n;                                                                                     \
  { unsigned lo_, hi_;                                                                                           \
    asm volatile("s_getpc_b64 s[98:99]\nLpc%=:\n\ts_add_u32 %0, s98, (" B "-Lpc%=)&4294967295\n\ts_addc_u32 %1, s99, (" B "-Lpc%=)>>32\n\t" \
                 "s_mov_b32 %2, " E "-" B : "=s"(lo_), "=s"(hi_), "=s"(n) :: "s98", "s99", "scc");               \
    p = (const char*)(((unsigned long long)hi_ << 32) | lo_); }
__device__ __forceinline__ unsigned touch(const char* p, unsigned n, unsigned off) {
  unsigned t;
  asm volatile("global_load_dword %0, %1, off" : "=v"(t) : "v"(p + (off < n ? off : 0u)) : "memory");
  return t;
}

__device__ __forceinline__ void touch_lds(const char* p, unsigned a) {
  asm volatile("s_mov_b32 m0, %1\n\ts_nop 0\n\tglobal_load_lds_dword %0, off" :: "v"(p), "s"(a) : "memory");
}
#define PFPOL ""
__device__ __forceinline__ void touch_pol(const char* p, unsigned a) {
  asm volatile("s_mov_b32 m0, %1\n\ts_nop 0\n\tglobal_load_lds_dword %0, off " PFPOL :: "v"(p), "s"(a) : "memory");
}
#define YSLEEP 4
#define KSLEEP 2
#define CTS(i)
__device__ __forceinline__ f32x2 up2(unsigned p) { return (f32x2){__uint_as_float(p << 16), __uint_as_float(p & 0xffff0000u)}; }
__device__ __forceinline__ unsigned pk2(f32x2 v) { return __builtin_bit_cast(unsigned, __builtin_convertvector(v, bf16x2)); }
__device__ __forceinline__ f32x2 rbf2(f32x2 v) { return up2(pk2(v)); }
template <int N, class F> __device__ __forceinline__ void sfor(F&& f) {
  if constexpr (N > 0) { sfor<N - 1>(f); f(std::integral_constant<int, N - 1>{}); }
}
__device__ __forceinline__ void set_flag(char* p, int i, unsigned ep) {
  if (i < 32) __hip_atomic_store((gu32*)p + i, ep, __ATOMIC_RELAXED, __HIP_MEMORY_SCOPE_AGENT);
}
template <int NL>
__device__ __forceinline__ void poll_flags(const char* p, int n, int lane, unsigned ep) {
  for (int it = 0; it < 20000; ++it) {
    bool ok = true;
#pragma unroll
    for (int i = 0; i < NL; ++i) {
      const int f = lane + 64 * i;
      ok &= __hip_atomic_load((gu32*)(p + (f < n ? f : 0) * 128), __ATOMIC_RELAXED, __HIP_MEMORY_SCOPE_AGENT) == ep;
    }
    if (__all(ok)) break;
    __builtin_amdgcn_s_sleep(YSLEEP);
  }
  asm volatile("" ::: "memory");
}

constexpr int CV_HP = (2048 + 32) * 2, CV_UR = (2048 + 256) * 2, CV_CH = 2 * CV_HP + CV_UR;
constexpr int NCV = 174;
constexpr int CV_STG = 8 * CV_CH;
constexpr int YP = D_ * 16;

struct KvRegs { u32x4v k0, k1, k2, k3, k4, k5, k6, v0, v1, v2, v3, s0, s1, s2, s3; };
#define LD16(d, p, pol) asm volatile("global_load_dwordx4 %0, %1, off " pol : "=v"(d) : "v"(p) : "memory")
__device__ __forceinline__ void kv_load(KvRegs& R, const u16* __restrict__ kin, const u16* __restrict__ vin, const u16* __restrict__ shift_k, int b, int tid) {
  const int tr = (b >> 4) * 128 + 4 * (tid >> 3), cc = (b & 15) * 64 + 8 * (tid & 7);
  const u16* kp = kin + (size_t)tr * D_ + cc;
  const u16* vp = vin + (size_t)tr * D_ + cc;
  const u16* sp = shift_k + (size_t)cc * 4;
  LD16(R.k3, kp, "nt"); LD16(R.k4, kp + D_, "nt"); LD16(R.k5, kp + 2 * D_, "nt"); LD16(R.k6, kp + 3 * D_, "nt");
  LD16(R.v0, vp, "nt"); LD16(R.v1, vp + D_, "nt"); LD16(R.v2, vp + 2 * D_, "nt"); LD16(R.v3, vp + 3 * D_, "nt");
  const u16* kq = tr ? kp - 3 * D_ : kp;
  LD16(R.k0, kq, "nt"); LD16(R.k1, kq + D_, "nt"); LD16(R.k2, kq + 2 * D_, "nt");
  LD16(R.s0, sp, ""); LD16(R.s1, sp + 8, ""); LD16(R.s2, sp + 16, ""); LD16(R.s3, sp + 24, "");
}
template <int N>
__device__ __forceinline__ void kv_wait(KvRegs& R) {
  asm volatile("s_waitcnt vmcnt(%15)" : "+v"(R.k0), "+v"(R.k1), "+v"(R.k2), "+v"(R.k3), "+v"(R.k4), "+v"(R.k5), "+v"(R.k6), "+v"(R.v0), "+v"(R.v1), "+v"(R.v2),
               "+v"(R.v3), "+v"(R.s0), "+v"(R.s1), "+v"(R.s2), "+v"(R.s3) : "n"(N) : "memory");
}
__device__ __forceinline__ void kv_store(KvRegs& R, char* __restrict__ ws, char* T, int b, int tid) {
  const int t0 = (b >> 4) * 128, c0 = (b & 15) * 64, ch8 = tid & 7, rg = tid >> 3;
  if (t0 + 4 * rg < 3) R.k0 = R.k1 = R.k2 = (u32x4v){0u, 0u, 0u, 0u};
  typedef unsigned u32x2 __attribute__((ext_vector_type(2)));
  const u32x4v kr[7] = {R.k0, R.k1, R.k2, R.k3, R.k4, R.k5, R.k6}, vr[4] = {R.v0, R.v1, R.v2, R.v3}, sk[4] = {R.s0, R.s1, R.s2, R.s3};
#pragma unroll
  for (int j2 = 0; j2 < 4; ++j2) {
    const f32x2 sa = up2(sk[j2][0]), sb = up2(sk[j2][1]), sc = up2(sk[j2][2]), sd = up2(sk[j2][3]);
    const f32x2 s0 = {sa[0], sc[0]}, s1 = {sa[1], sc[1]}, s2 = {sb[0], sd[0]}, s3 = {sb[1], sd[1]};
    f32x2 kf[7], o[4];
#pragma unroll
    for (int r = 0; r < 7; ++r) kf[r] = up2(kr[r][j2]);
#pragma unroll
    for (int r = 0; r < 4; ++r)
      o[r] = __builtin_elementwise_fma(kf[r + 3], s0, __builtin_elementwise_fma(kf[r + 2], s1, __builtin_elementwise_fma(kf[r + 1], s2, kf[r] * s3))) * up2(vr[r][j2]);
#pragma unroll
    for (int e = 0; e < 2; ++e)
      *(u32x2*)(T + (8 * ch8 + 2 * j2 + e) * 264 + (124 - 4 * rg) * 2) = (u32x2){pk2((f32x2){o[3][e], o[2][e]}), pk2((f32x2){o[1][e], o[0][e]})};
  }
  __syncthreads();
  u16* kvr = (u16*)(ws + OFF_KVR);
#pragma unroll
  for (int i = 0; i < 4; ++i) {
    const int idx = tid + 256 * i, ch = idx >> 4, cb = idx & 15;
    const u16x4 lo = *(const u16x4*)(T + ch * 264 + cb * 16), hi = *(const u16x4*)(T + ch * 264 + cb * 16 + 8);
    st16(kvr + (size_t)(c0 + ch) * S_ + (1920 - t0) + cb * 8, __builtin_shufflevector(lo, hi, 0, 1, 2, 3, 4, 5, 6, 7));
  }
}

#define LASTSLEEP 8
__device__ __forceinline__ void wait_flags(const char* p, int lane, int n, unsigned ep) {
  gu32* f = (gu32*)(p + (lane < n ? lane : 0) * 128);
  for (int it = 0; it < 10000; ++it) {
    if (__all(__hip_atomic_load(f, __ATOMIC_RELAXED, __HIP_MEMORY_SCOPE_AGENT) == ep)) break;
    __builtin_amdgcn_s_sleep(LASTSLEEP);
  }
  asm volatile("" ::: "memory");
}
__device__ __forceinline__ void row_last(char* __restrict__ ws, u16* __restrict__ out, int t, int lane, int wave, unsigned ep) {
  const __amdgpu_buffer_rsrc_t rs = __builtin_amdgcn_make_buffer_rsrc(ws, 0, WS_USED, 0x00020000);
  const int r8 = lane & 7, seg = lane >> 3, un = 128 + (t >> 1);
  const unsigned fo = un * 32768 + (wave * 8 + (seg >> 2)) * 1024 + ((seg & 3) * 16 + (t & 1) * 8 + r8) * 16;
  wait_flags(ws + OFF_FP + un * 2048, lane, 16, ep);
  u32x4v p0[4], p1[4];
#pragma unroll
  for (int i = 0; i < 4; ++i) {
    p0[i] = __builtin_bit_cast(u32x4v, __builtin_amdgcn_raw_buffer_load_b128(rs, fo + i * 2048, OFF_P0, 16));
    p1[i] = __builtin_bit_cast(u32x4v, __builtin_amdgcn_raw_buffer_load_b128(rs, fo + i * 2048, OFF_P1, 16));
  }
#pragma unroll
  for (int i = 0; i < 4; ++i) {
    u32x4v o;
#pragma unroll
    for (int e = 0; e < 4; ++e) o[e] = pk2(up2(p0[i][e]) + up2(p1[i][e]));
    st16(out + (size_t)(t * 8 + r8) * D_ + wave * 256 + i * 64 + seg * 8, __builtin_bit_cast(u16x8, o));
  }
}

__device__ __forceinline__ int swzB(int r) { return ((((r >> 1) & 1) | (((r >> 3) & 1) << 1)) << 1); }
#define TCH 1
#define LANE_ID(x) int x; asm volatile("v_mbcnt_lo_u32_b32 %0, -1, 0\n\tv_mbcnt_hi_u32_b32 %0, -1, %0" : "=v"(x))
#define T2X 5
#define W12 3
#define HL 1
#define QTB 1
constexpr int QT0 = 34 * 128;
constexpr int HU = 40960;
#define G3OR 5
constexpr int NRW = 22 - G3OR;
__global__ __launch_bounds__(256, 1) void flow_kernel(const u16* __restrict__ q, const u16* __restrict__ kin, const u16* __restrict__ vin,
                                                      const u16* __restrict__ shift_k, const u16* __restrict__ ssm_k, const u16* __restrict__ ssm_D,
                                                      const u16* __restrict__ w1, const u16* __restrict__ w2,
                                                      const u16* __restrict__ w3, const u16* __restrict__ w4, char* __restrict__ ws,
                                                      u16* __restrict__ out) {
  __shared__ __attribute__((aligned(16))) char lds[163840];
  const int tid = threadIdx.x, lane = tid & 63, wave = __builtin_amdgcn_readfirstlane(tid >> 6);
  const int b = blockIdx.x, xcd = b & 7, idx = b >> 3;
  const unsigned sLds = (unsigned)(size_t)lds;
  const int o8 = idx & 7;
  int kind, rep, slice, kh = 0;
  int j3 = 0, j4 = 0;
  const int xp = xcd >> 1, pr = xp & 1;
  if (!(xcd & 1)) {
    if (idx < (xp < 2 ? 24 : 16)) { kind = 4; rep = (xp < 2 ? 0 : 3) + (idx >> 3); kh = pr; slice = o8; j4 = rep * 16 + kh * 8 + o8; }
    else { kind = 3; rep = xp < 2 ? 4 : pr * 2 + ((idx >> 3) & 1); slice = xp < 2 ? pr * 8 + o8 : o8; j3 = rep * 18 + slice; }
  } else if (idx < 16) { kind = 1 + (idx >> 3); rep = xp; slice = o8; }
  else {
    const int k = idx - 16, n3 = xp < 2 ? 10 : 11, no = xp == 2 ? 2 : 1;
    if (k < n3) { kind = 3; rep = k < 10 ? pr * 2 + (k >= 5) : 4; slice = k < 10 ? (xp < 2 ? 8 : 13) + k - 5 * (k >= 5) : 16 + pr; j3 = rep * 18 + slice; }
    else if (k < n3 + no) { kind = 5; rep = k > n3 ? 4 : pr * 2 + (xp >> 1); slice = 18; j3 = 90 + rep; }
    else { kind = 6; rep = 0; slice = (xp < 3 ? xp * 5 : 13) + k - n3 - no; }
  }
  CTS(0);
  {
    CODE_RANGE("Lpc%=", "Lcode_r_b", c0, n0) CODE_RANGE("(Lcode_end-3968)", "Lcode_end", c7, n7)
    touch_lds(tid < 224 ? c0 + (tid * 128 < n0 ? tid * 128 : 0) : c7 + ((tid - 224) * 128 < n7 ? (tid - 224) * 128 : 0), sLds + 163584);
  }
  const unsigned sScr = sLds + 163584;
  auto code_touch = [&]() {
    CODE_RANGE("Lcode_g12_b", "Lcode_g2_b", c1, n1) CODE_RANGE("Lcode_g2_b", "Lcode_g12_e", c2, n2) CODE_RANGE("Lcode_g3_b", "Lcode_g3_e", c3, n3)
    CODE_RANGE("Lcode_g3o_b", "Lcode_g3o_e", c5, n5) CODE_RANGE("Lcode_g4_b", "Lcode_g4_e", c4, n4) CODE_RANGE("Lcode_r_b", "Lcode_r_e", c6, n6)
    const char* cp = kind == 1 ? c1 : kind == 2 ? c2 : kind == 3 ? c3 : kind == 4 ? c4 : kind == 5 ? c5 : c6;
    const unsigned cn = kind == 1 ? n1 : kind == 2 ? n2 : kind == 3 ? n3 : kind == 4 ? n4 : kind == 5 ? n5 : n6;
    touch_lds(cp + (tid * 128 < cn ? tid * 128 : 0), sScr);
    touch_lds(kind == 4 && tid < 64 ? c6 + tid * 128 : cp + (32768 + tid * 128 < cn ? 32768 + tid * 128 : 0), sScr);
  };
  unsigned ep = 0;
  auto w_touch = [&](const u16* w) {
#pragma unroll
    for (int i = 0; i < 2; ++i) touch_lds((const char*)w + (size_t)(rep * 256 + i * 128 + (tid >> 1)) * 2048 + slice * 256 + (tid & 1) * 128, sScr);
  };
  if (kind == 1) { code_touch(); if (W12 & 1) w_touch(w1); }
  else {
  const bool isconv = kind >= 3 && kind != 6 && j3 < 94;
  const int nw = (kind == 4 || j3 >= 92) ? 2 : 1, c0 = kind == 4 ? 8 * j4 : j3 < 92 ? 640 + 4 * j3 : j3 < 94 ? 272 + 8 * j3 : 0, cw = c0 + nw * wave;
  const unsigned dw = ((const unsigned*)ssm_D)[cw >> 1];
  const int tile = !(xcd & 1) ? idx * 4 + xp : idx >= 16 ? 64 + idx * 4 + xp : 192 + xp * 8 + o8;
#define EP_LOAD unsigned ew; asm volatile("global_load_dword %0, %1, %2 sc1" : "=v"(ew) : "v"(0u), "s"(ws + OFF_EP) : "memory")
#define EP_TAKE(N) asm volatile("s_waitcnt vmcnt(%1)" : "+v"(ew) : "n"(N) : "memory"); ep = __builtin_amdgcn_readfirstlane(ew) + 1u; if (ep == 0u) ep = 1u
  if (kind == 2) {
    const int tile2 = (tile + 32) ^ T2X;
    KvRegs KR, KR2;
    EP_LOAD;
    kv_load(KR, kin, vin, shift_k, tile, tid);
    kv_load(KR2, kin, vin, shift_k, tile2, tid);
    code_touch();
    kv_wait<17>(KR);
    CTS(12);
    kv_store(KR, ws, lds + CV_STG, tile, tid);
    kv_wait<6>(KR2);
    kv_store(KR2, ws, lds, tile2, tid);
    CTS(1);
    EP_TAKE(0);
    __syncthreads();
    set_flag(ws + OFF_FKV + ((wave & 1) ? tile2 : tile) * 128, wave < 2 ? lane : 64, ep);
    CTS(2);
    if (W12 & 2) w_touch(w2);
  } else {
    KvRegs KR;
    EP_LOAD;
    kv_load(KR, kin, vin, shift_k, tile, tid);
    auto h_dma = [&](bool first) {
#pragma unroll
      for (int j = 0; j < 2; ++j) {
        const u16* hp = ssm_k + (size_t)cw * S_ + (isconv && j < nw ? j * S_ + lane * 8 : 0);
        if (first) asm volatile("s_mov_b32 m0, %1\n\ts_nop 0\n\tglobal_load_lds_dwordx4 %0, off nt" :: "v"(hp), "s"(sLds + (2 * wave + j) * CV_CH + 32) : "memory");
        else asm volatile("s_mov_b32 m0, %1\n\ts_nop 0\n\tglobal_load_lds_dwordx4 %0, off offset:1024 nt\n\t"
                          "global_load_lds_dwordx4 %0, off offset:2048 nt\n\tglobal_load_lds_dwordx4 %0, off offset:3072 nt" :: "v"(hp), "s"(sLds + (2 * wave + j) * CV_CH + 32) : "memory");
      }
    };
    h_dma(true);
    if (!HL) h_dma(false);
    code_touch();
    *(u32x4v*)(lds + (2 * wave + (lane >> 5)) * CV_CH + 2 * CV_HP + 4096 + (lane & 31) * 16) = (u32x4v){0u, 0u, 0u, 0u};
    if (lane < 8) *(u32x4v*)(lds + (2 * wave + (lane >> 2)) * CV_CH + ((lane >> 1) & 1) * CV_HP + (lane & 1) * 16) = (u32x4v){0u, 0u, 0u, 0u};
    kv_wait<HL ? 4 : 10>(KR);
    CTS(12);
    kv_store(KR, ws, lds + CV_STG, tile, tid);
    CTS(1);
    if (HL) h_dma(false);
    EP_TAKE(HL ? 6 : 0);
    __builtin_amdgcn_s_barrier();
    set_flag(ws + OFF_FKV + tile * 128, tid, ep);
    CTS(2);
  }
  if (isconv) {
    LANE_ID(lane);
    const int tid = wave * 64 + lane;
    const unsigned sC0 = (cw & 1) ? dw & 0xffff0000u : dw << 16, sC1 = dw & 0xffff0000u;
    const int l15 = lane & 15, lg = lane >> 4, cp = (l15 + 1) & 1, iw = kind == 4 ? j4 : 80 + j3, own = 2 * wave * CV_CH;
    const int ch = tid & (4 * nw - 1), tb = nw == 2 ? tid >> 3 : tid >> 2, slot = nw == 2 ? ch : 2 * ch;
    const unsigned sWave = wave, sBuf = nw, sAuxOff = OFF_KVR + cw * 4096, sFoutOff = OFF_FY + iw * 128, sExp = __builtin_amdgcn_readfirstlane(ep), sDbg = 110u * 1048576u + (b * 4 + wave) * 8192;
    const unsigned vL16 = lane * 16, vPA = own + cp * CV_HP + 2 * (8 * lg + l15 + 1 - cp), vPB = own + 2 * CV_HP + 2 * (2032 - 16 * l15 + 8 * lg) - 512 * 7;
    const unsigned vPU = own + 2 * CV_HP + 2 * (2044 - 16 * l15 - 4 * lg) - 512 * 7, vPW = 32 * l15 + 8 * lg;
    const unsigned vOR = slot * CV_CH + tb * 16, vORA = CV_STG + slot * 1024 + tb * 16, vOG = tb * YP + (c0 + ch) * 16;
    const unsigned vFA = ((lane & 3) * 16 + (c0 >> 6)) * 128, vFB = ((4 + lane % 12) * 16 + (c0 >> 6)) * 128, vFlag = tid < 32 ? tid * 4 : 0x80000000u;
    const unsigned vTW = lane < 42 ? (iw * 168 + wave * 42 + lane) * 128 : 0x80000000u;
    const unsigned vTX = lane < 44 ? ((kind == 4 ? j4 : j3 - 12) * 176 + wave * 44 + lane) * 128 : 0x80000000u;
    (void)sDbg;
    asm volatile(
R"(.macro M d,w,x
v_mfma_f32_16x16x32_bf16 v[\d:\d+3], \w, v[\x:\x+3], v[\d:\d+3]
.endm
.macro R d,a,o
ds_read_b128 v[\d:\d+3], v\a offset:\o
.endm
.macro T d,a,o
ds_read_b64_tr_b16 \d, v\a offset:\o
.endm
.macro D a,s,o
buffer_load_dwordx4 v\a, s[36:39], s\s offen offset:\o sc1 lds
.endm
.macro L c
s_waitcnt lgkmcnt(\c)
.endm
Lcode_cv_b:
s_mov_b64 s[36:37],%0
s_mov_b32 s38,111243392
s_mov_b32 s39,0x00020000
s_mov_b32 s44,%1
s_mov_b32 s51,%2
s_mov_b32 s49,%3
s_mov_b32 s48,%4
s_mov_b32 s86,%5
s_mov_b32 s87,%6
s_mov_b32 s53,%7
v_mov_b32 v0,%8
v_mov_b32 v1,%9
v_mov_b32 v2,%10
v_mov_b32 v3,%11
v_mov_b32 v4,%12
v_mov_b32 v5,%13
v_mov_b32 v6,%14
v_mov_b32 v7,%15
v_mov_b32 v8,%16
v_mov_b32 v9,%17
v_mov_b32 v10,%18
v_mov_b32 v11,%19
v_mov_b32 v12,%20
s_mov_b32 s58,111144960
buffer_load_dword v13,v8,s[36:39],s58 offen offset:0 sc1
s_mul_i32 s57,s44,25856
v_add_u32 v18,s57,v0
v_add_u32 v18,32,v18
v_mov_b32 v19,s57
R 88,18,0
ds_read_b32 v92,v18 offset:16
R 152,18,12928
ds_read_b32 v156,v18 offset:12944
ds_read_b32 v24,v19 offset:32
ds_read_b32 v25,v19 offset:12960
s_mov_b64 s[40:41],%21
s_mov_b32 s42,7340032
s_mov_b32 s43,0x00020000
s_mov_b64 s[60:61],%22
s_mov_b32 s62,7340032
s_mov_b32 s63,0x00020000
v_mov_b32 v23,s53
s_mov_b32 s82,0
s_add_u32 s55,s49,4096
s_cmp_eq_u32 s51,2
s_cselect_b32 s55,s55,0x7fff0000
s_cselect_b32 s56,0,0x70000000
s_add_u32 s57,s57,8320
v_add_u32 v14,1024,v1
v_add_u32 v15,2048,v1
v_add_u32 v16,3072,v1
v_add_u32 v17,12928,v1
L 4
v_alignbyte_b32 v96,v89,v88,2
v_alignbyte_b32 v97,v90,v89,2
v_alignbyte_b32 v98,v91,v90,2
v_alignbyte_b32 v99,v92,v91,2
ds_write_b128 v18,v[96:99] offset:4160
L 3
v_alignbyte_b32 v160,v153,v152,2
v_alignbyte_b32 v161,v154,v153,2
v_alignbyte_b32 v162,v155,v154,2
v_alignbyte_b32 v163,v156,v155,2
ds_write_b128 v18,v[160:163] offset:17088
L 2
v_lshlrev_b32 v24,16,v24
v_lshlrev_b32 v25,16,v25
s_mov_b64 exec,1
ds_write_b32 v19,v24 offset:4188
ds_write_b32 v19,v25 offset:17116
s_mov_b64 exec,-1
ds_read2_b32 a[0:1],v1 offset0:0 offset1:1
ds_read2_b32 a[2:3],v1 offset0:2 offset1:3
ds_read2_b32 a[4:5],v1 offset0:128 offset1:129
ds_read2_b32 a[6:7],v1 offset0:130 offset1:131
ds_read2_b32 a[8:9],v1 offset0:16 offset1:17
ds_read2_b32 a[10:11],v1 offset0:18 offset1:19
ds_read2_b32 a[12:13],v1 offset0:144 offset1:145
ds_read2_b32 a[14:15],v1 offset0:146 offset1:147
ds_read2_b32 a[16:17],v1 offset0:32 offset1:33
ds_read2_b32 a[18:19],v1 offset0:34 offset1:35
ds_read2_b32 a[20:21],v1 offset0:160 offset1:161
ds_read2_b32 a[22:23],v1 offset0:162 offset1:163
ds_read2_b32 a[24:25],v1 offset0:48 offset1:49
ds_read2_b32 a[26:27],v1 offset0:50 offset1:51
ds_read2_b32 a[28:29],v1 offset0:176 offset1:177
ds_read2_b32 a[30:31],v1 offset0:178 offset1:179
ds_read2_b32 a[32:33],v1 offset0:64 offset1:65
ds_read2_b32 a[34:35],v1 offset0:66 offset1:67
ds_read2_b32 a[36:37],v1 offset0:192 offset1:193
ds_read2_b32 a[38:39],v1 offset0:194 offset1:195
ds_read2_b32 a[40:41],v1 offset0:80 offset1:81
ds_read2_b32 a[42:43],v1 offset0:82 offset1:83
ds_read2_b32 a[44:45],v1 offset0:208 offset1:209
ds_read2_b32 a[46:47],v1 offset0:210 offset1:211
ds_read2_b32 a[48:49],v1 offset0:96 offset1:97
ds_read2_b32 a[50:51],v1 offset0:98 offset1:99
ds_read2_b32 a[52:53],v1 offset0:224 offset1:225
ds_read2_b32 a[54:55],v1 offset0:226 offset1:227
ds_read2_b32 a[56:57],v1 offset0:112 offset1:113
ds_read2_b32 a[58:59],v1 offset0:114 offset1:115
ds_read2_b32 a[60:61],v1 offset0:240 offset1:241
ds_read2_b32 a[62:63],v1 offset0:242 offset1:243
ds_read2_b32 a[64:65],v17 offset0:0 offset1:1
ds_read2_b32 a[66:67],v17 offset0:2 offset1:3
ds_read2_b32 a[68:69],v17 offset0:128 offset1:129
ds_read2_b32 a[70:71],v17 offset0:130 offset1:131
ds_read2_b32 a[72:73],v17 offset0:16 offset1:17
ds_read2_b32 a[74:75],v17 offset0:18 offset1:19
ds_read2_b32 a[76:77],v17 offset0:144 offset1:145
ds_read2_b32 a[78:79],v17 offset0:146 offset1:147
ds_read2_b32 a[80:81],v17 offset0:32 offset1:33
ds_read2_b32 a[82:83],v17 offset0:34 offset1:35
ds_read2_b32 a[84:85],v17 offset0:160 offset1:161
ds_read2_b32 a[86:87],v17 offset0:162 offset1:163
ds_read2_b32 a[88:89],v17 offset0:48 offset1:49
ds_read2_b32 a[90:91],v17 offset0:50 offset1:51
ds_read2_b32 a[92:93],v17 offset0:176 offset1:177
ds_read2_b32 a[94:95],v17 offset0:178 offset1:179
ds_read2_b32 a[96:97],v17 offset0:64 offset1:65
ds_read2_b32 a[98:99],v17 offset0:66 offset1:67
ds_read2_b32 a[100:101],v17 offset0:192 offset1:193
ds_read2_b32 a[102:103],v17 offset0:194 offset1:195
ds_read2_b32 a[104:105],v17 offset0:80 offset1:81
ds_read2_b32 a[106:107],v17 offset0:82 offset1:83
ds_read2_b32 a[108:109],v17 offset0:208 offset1:209
ds_read2_b32 a[110:111],v17 offset0:210 offset1:211
ds_read2_b32 a[112:113],v17 offset0:96 offset1:97
ds_read2_b32 a[114:115],v17 offset0:98 offset1:99
ds_read2_b32 a[116:117],v17 offset0:224 offset1:225
ds_read2_b32 a[118:119],v17 offset0:226 offset1:227
ds_read2_b32 a[120:121],v17 offset0:112 offset1:113
ds_read2_b32 a[122:123],v17 offset0:114 offset1:115
ds_read2_b32 a[124:125],v17 offset0:240 offset1:241
ds_read2_b32 a[126:127],v17 offset0:242 offset1:243
Lcv_bpa_%=:
s_waitcnt vmcnt(0)
v_cmp_eq_u32 vcc,s53,v13
s_add_u32 s82,s82,1
s_cmp_eq_u64 vcc,-1
s_cbranch_scc1 Lcv_bqa_%=
s_sleep 1
buffer_load_dword v13,v8,s[36:39],s58 offen offset:0 sc1
s_cmp_lt_u32 s82,20000
s_cbranch_scc1 Lcv_bpa_%=
s_waitcnt vmcnt(0)
Lcv_bqa_%=:
s_add_u32 m0,s57,0
s_nop 0
D 0,49,3072
s_add_u32 m0,s57,12928
s_nop 0
D 0,55,3072
buffer_load_dword v13,v9,s[36:39],s58 offen offset:0 sc1
R 88,18,0
ds_read_b32 v92,v18 offset:16
R 104,18,1024
ds_read_b32 v108,v18 offset:1040
R 120,18,2048
ds_read_b32 v124,v18 offset:2064
R 136,18,3072
ds_read_b32 v140,v18 offset:3088
L 6
v_alignbyte_b32 v96,v89,v88,2
v_alignbyte_b32 v97,v90,v89,2
v_alignbyte_b32 v98,v91,v90,2
v_alignbyte_b32 v99,v92,v91,2
ds_write_b128 v18,v[96:99] offset:4160
L 5
v_alignbyte_b32 v112,v105,v104,2
v_alignbyte_b32 v113,v106,v105,2
v_alignbyte_b32 v114,v107,v106,2
v_alignbyte_b32 v115,v108,v107,2
ds_write_b128 v18,v[112:115] offset:5184
L 4
v_alignbyte_b32 v128,v121,v120,2
v_alignbyte_b32 v129,v122,v121,2
v_alignbyte_b32 v130,v123,v122,2
v_alignbyte_b32 v131,v124,v123,2
ds_write_b128 v18,v[128:131] offset:6208
L 3
v_alignbyte_b32 v144,v137,v136,2
v_alignbyte_b32 v145,v138,v137,2
v_alignbyte_b32 v146,v139,v138,2
v_alignbyte_b32 v147,v140,v139,2
ds_write_b128 v18,v[144:147] offset:7232
R 152,18,12928
ds_read_b32 v156,v18 offset:12944
R 168,18,13952
ds_read_b32 v172,v18 offset:13968
R 184,18,14976
ds_read_b32 v188,v18 offset:14992
R 200,18,16000
ds_read_b32 v204,v18 offset:16016
L 6
v_alignbyte_b32 v160,v153,v152,2
v_alignbyte_b32 v161,v154,v153,2
v_alignbyte_b32 v162,v155,v154,2
v_alignbyte_b32 v163,v156,v155,2
ds_write_b128 v18,v[160:163] offset:17088
L 5
v_alignbyte_b32 v176,v169,v168,2
v_alignbyte_b32 v177,v170,v169,2
v_alignbyte_b32 v178,v171,v170,2
v_alignbyte_b32 v179,v172,v171,2
ds_write_b128 v18,v[176:179] offset:18112
L 4
v_alignbyte_b32 v192,v185,v184,2
v_alignbyte_b32 v193,v186,v185,2
v_alignbyte_b32 v194,v187,v186,2
v_alignbyte_b32 v195,v188,v187,2
ds_write_b128 v18,v[192:195] offset:19136
L 3
v_alignbyte_b32 v208,v201,v200,2
v_alignbyte_b32 v209,v202,v201,2
v_alignbyte_b32 v210,v203,v202,2
v_alignbyte_b32 v211,v204,v203,2
ds_write_b128 v18,v[208:211] offset:20160
s_lshl_b32 s78,s44,11
s_add_u32 s78,s78,103424
v_add_u32 v18,s78,v4
v_add_u32 v4,v19,v4
s_waitcnt vmcnt(0)
v_cmp_eq_u32 vcc,s53,v13
s_mov_b32 s83,0
s_cmp_eq_u64 vcc,-1
s_cbranch_scc0 Lcv_a_%=
s_mov_b32 s83,1
s_add_u32 m0,s57,0
s_nop 0
D 0,49,2048
D 0,49,1024
D 0,49,0
s_add_u32 m0,s57,12928
s_nop 0
D 0,55,2048
D 0,55,1024
D 0,55,0
Lcv_a_%=:
s_cmp_eq_u32 s51,2
s_cbranch_scc1 Lcv_a2_%=
L 0
R 88,2,3584
R 92,2,3072
R 96,2,3648
R 100,2,3136
R 104,2,3712
R 108,2,3200
R 112,2,3776
R 116,2,3264
L 6
v_mfma_f32_16x16x32_bf16 v[40:43],a[0:3],v[88:91],0
R 120,2,3840
R 124,2,3328
v_mfma_f32_16x16x32_bf16 v[44:47],a[0:3],v[92:95],0
M 44,a[4:7],88
L 6
M 40,a[8:11],96
R 128,2,3904
R 132,2,3392
M 44,a[8:11],100
M 44,a[12:15],96
L 6
M 40,a[16:19],104
R 136,2,3968
R 140,2,3456
M 44,a[16:19],108
M 44,a[20:23],104
L 6
M 40,a[24:27],112
R 144,2,4032
R 148,2,3520
M 44,a[24:27],116
M 44,a[28:31],112
L 6
M 40,a[32:35],120
M 44,a[32:35],124
M 44,a[36:39],120
L 4
M 40,a[40:43],128
M 44,a[40:43],132
M 44,a[44:47],128
L 2
M 40,a[48:51],136
ds_read_b64 v[24:25],v3 offset:3584
ds_read_b64 v[26:27],v3 offset:3072
M 44,a[48:51],140
M 44,a[52:55],136
L 2
M 40,a[56:59],144
M 44,a[56:59],148
M 44,a[60:63],144
L 0
v_and_b32 v36,0xffff0000,v25
v_lshlrev_b32 v37,16,v25
v_and_b32 v38,0xffff0000,v24
v_lshlrev_b32 v39,16,v24
s_nop 0
v_fma_f32 v40,v36,s86,v40
v_fma_f32 v41,v37,s86,v41
v_fma_f32 v42,v38,s86,v42
v_fma_f32 v43,v39,s86,v43
v_cvt_pk_bf16_f32 v40,v40,v41
v_cvt_pk_bf16_f32 v41,v42,v43
ds_write_b64 v18,v[40:41] offset:0
v_and_b32 v36,0xffff0000,v27
v_lshlrev_b32 v37,16,v27
v_and_b32 v38,0xffff0000,v26
v_lshlrev_b32 v39,16,v26
v_fma_f32 v44,v36,s86,v44
v_fma_f32 v45,v37,s86,v45
v_fma_f32 v46,v38,s86,v46
v_fma_f32 v47,v39,s86,v47
v_cvt_pk_bf16_f32 v44,v44,v45
v_cvt_pk_bf16_f32 v45,v46,v47
ds_write_b64 v18,v[44:45] offset:512
s_branch Lcv_a3_%=
Lcv_a2_%=:
L 0
R 88,2,3584
R 92,2,3072
R 96,2,3648
R 100,2,3136
R 104,2,3712
R 108,2,3200
R 112,2,3776
R 116,2,3264
L 6
v_mfma_f32_16x16x32_bf16 v[40:43],a[0:3],v[88:91],0
R 120,2,3840
R 124,2,3328
v_mfma_f32_16x16x32_bf16 v[44:47],a[0:3],v[92:95],0
M 44,a[4:7],88
L 6
M 40,a[8:11],96
R 128,2,3904
R 132,2,3392
M 44,a[8:11],100
M 44,a[12:15],96
L 6
M 40,a[16:19],104
R 136,2,3968
R 140,2,3456
M 44,a[16:19],108
M 44,a[20:23],104
L 6
M 40,a[24:27],112
R 144,2,4032
R 148,2,3520
M 44,a[24:27],116
M 44,a[28:31],112
L 6
M 40,a[32:35],120
R 152,2,16512
R 156,2,16000
M 44,a[32:35],124
M 44,a[36:39],120
L 6
M 40,a[40:43],128
R 160,2,16576
R 164,2,16064
M 44,a[40:43],132
M 44,a[44:47],128
L 6
M 40,a[48:51],136
R 168,2,16640
R 172,2,16128
M 44,a[48:51],140
ds_read_b64 v[24:25],v3 offset:3584
ds_read_b64 v[26:27],v3 offset:3072
M 44,a[52:55],136
L 8
M 40,a[56:59],144
R 176,2,16704
R 180,2,16192
M 44,a[56:59],148
M 44,a[60:63],144
L 8
v_mfma_f32_16x16x32_bf16 v[48:51],a[64:67],v[152:155],0
R 184,2,16768
R 188,2,16256
v_mfma_f32_16x16x32_bf16 v[52:55],a[64:67],v[156:159],0
M 52,a[68:71],152
L 8
M 48,a[72:75],160
R 192,2,16832
R 196,2,16320
M 52,a[72:75],164
L 6
v_and_b32 v36,0xffff0000,v25
M 52,a[76:79],160
v_lshlrev_b32 v37,16,v25
v_and_b32 v38,0xffff0000,v24
v_lshlrev_b32 v39,16,v24
s_nop 0
M 48,a[80:83],168
R 200,2,16896
R 204,2,16384
M 52,a[80:83],172
v_fma_f32 v40,v36,s86,v40
v_fma_f32 v41,v37,s86,v41
M 52,a[84:87],168
v_fma_f32 v42,v38,s86,v42
v_fma_f32 v43,v39,s86,v43
v_cvt_pk_bf16_f32 v40,v40,v41
L 6
M 48,a[88:91],176
R 208,2,16960
R 212,2,16448
M 52,a[88:91],180
v_cvt_pk_bf16_f32 v41,v42,v43
ds_write_b64 v18,v[40:41] offset:0
M 52,a[92:95],176
v_and_b32 v36,0xffff0000,v27
v_lshlrev_b32 v37,16,v27
v_and_b32 v38,0xffff0000,v26
L 7
M 48,a[96:99],184
M 52,a[96:99],188
v_lshlrev_b32 v39,16,v26
v_fma_f32 v44,v36,s86,v44
M 52,a[100:103],184
v_fma_f32 v45,v37,s86,v45
v_fma_f32 v46,v38,s86,v46
v_fma_f32 v47,v39,s86,v47
L 5
M 48,a[104:107],192
M 52,a[104:107],196
v_cvt_pk_bf16_f32 v44,v44,v45
v_cvt_pk_bf16_f32 v45,v46,v47
M 52,a[108:111],192
ds_write_b64 v18,v[44:45] offset:512
L 4
M 48,a[112:115],200
ds_read_b64 v[24:25],v3 offset:16512
ds_read_b64 v[26:27],v3 offset:16000
M 52,a[112:115],204
M 52,a[116:119],200
L 4
M 48,a[120:123],208
M 52,a[120:123],212
M 52,a[124:127],208
L 0
v_and_b32 v36,0xffff0000,v25
v_lshlrev_b32 v37,16,v25
v_and_b32 v38,0xffff0000,v24
v_lshlrev_b32 v39,16,v24
s_nop 0
v_fma_f32 v48,v36,s87,v48
v_fma_f32 v49,v37,s87,v49
v_fma_f32 v50,v38,s87,v50
v_fma_f32 v51,v39,s87,v51
v_cvt_pk_bf16_f32 v48,v48,v49
v_cvt_pk_bf16_f32 v49,v50,v51
ds_write_b64 v18,v[48:49] offset:1024
v_and_b32 v36,0xffff0000,v27
v_lshlrev_b32 v37,16,v27
v_and_b32 v38,0xffff0000,v26
v_lshlrev_b32 v39,16,v26
v_fma_f32 v52,v36,s87,v52
v_fma_f32 v53,v37,s87,v53
v_fma_f32 v54,v38,s87,v54
v_fma_f32 v55,v39,s87,v55
v_cvt_pk_bf16_f32 v52,v52,v53
v_cvt_pk_bf16_f32 v53,v54,v55
ds_write_b64 v18,v[52:53] offset:1536
Lcv_a3_%=:
L 0
s_barrier
R 152,6,0
R 156,6,512
s_mov_b32 s78,98566144
L 1
buffer_store_dwordx4 v[152:155],v7,s[36:39],s78 offen offset:0 sc0 sc1
s_add_u32 s78,s56,99090432
L 0
buffer_store_dwordx4 v[156:159],v7,s[36:39],s78 offen offset:0 sc0 sc1
s_cmp_eq_u32 s83,1
s_cbranch_scc1 Lcv_b_%=
s_waitcnt vmcnt(0)
s_barrier
buffer_store_dword v23,v10,s[36:39],s48 offen offset:0 sc0 sc1
buffer_load_dword v13,v9,s[36:39],s58 offen offset:0 sc1
Lcv_bpb_%=:
)"
R"(s_waitcnt vmcnt(0)
v_cmp_eq_u32 vcc,s53,v13
s_add_u32 s82,s82,1
s_cmp_eq_u64 vcc,-1
s_cbranch_scc1 Lcv_bqb_%=
s_sleep 1
buffer_load_dword v13,v9,s[36:39],s58 offen offset:0 sc1
s_cmp_lt_u32 s82,20000
s_cbranch_scc1 Lcv_bpb_%=
s_waitcnt vmcnt(0)
Lcv_bqb_%=:
s_add_u32 m0,s57,0
s_nop 0
D 0,49,2048
D 0,49,1024
D 0,49,0
s_add_u32 m0,s57,12928
s_nop 0
D 0,55,2048
D 0,55,1024
D 0,55,0
s_waitcnt vmcnt(0)
Lcv_b_%=:
s_waitcnt vmcnt(2)
ds_read2_b32 v[88:89],v1 offset0:0 offset1:1
ds_read2_b32 v[90:91],v1 offset0:2 offset1:3
R 120,2,3584
ds_read2_b32 v[92:93],v1 offset0:128 offset1:129
ds_read2_b32 v[94:95],v1 offset0:130 offset1:131
R 124,2,3072
ds_read2_b32 v[96:97],v14 offset0:0 offset1:1
ds_read2_b32 v[98:99],v14 offset0:2 offset1:3
R 128,2,2560
ds_read2_b32 v[100:101],v14 offset0:128 offset1:129
ds_read2_b32 v[102:103],v14 offset0:130 offset1:131
R 132,2,2048
ds_read2_b32 v[104:105],v15 offset0:0 offset1:1
ds_read2_b32 v[106:107],v15 offset0:2 offset1:3
R 136,2,1536
ds_read2_b32 v[108:109],v15 offset0:128 offset1:129
ds_read2_b32 v[110:111],v15 offset0:130 offset1:131
R 140,2,1024
ds_read2_b32 v[112:113],v16 offset0:0 offset1:1
ds_read2_b32 v[114:115],v16 offset0:2 offset1:3
R 144,2,512
ds_read2_b32 v[116:117],v16 offset0:128 offset1:129
ds_read2_b32 v[118:119],v16 offset0:130 offset1:131
R 148,2,0
L 15
v_mfma_f32_16x16x32_bf16 v[40:43],v[88:91],v[128:131],0
ds_read2_b32 v[152:153],v1 offset0:16 offset1:17
M 40,v[92:95],124
ds_read2_b32 v[154:155],v1 offset0:18 offset1:19
M 40,v[96:99],120
L 14
R 184,2,3648
v_mfma_f32_16x16x32_bf16 v[44:47],v[88:91],v[132:135],0
ds_read2_b32 v[156:157],v1 offset0:144 offset1:145
M 44,v[92:95],128
M 44,v[96:99],124
ds_read2_b32 v[158:159],v1 offset0:146 offset1:147
M 44,v[100:103],120
L 14
R 188,2,3136
v_mfma_f32_16x16x32_bf16 v[48:51],v[88:91],v[136:139],0
ds_read2_b32 v[160:161],v14 offset0:16 offset1:17
M 48,v[92:95],132
ds_read2_b32 v[162:163],v14 offset0:18 offset1:19
M 48,v[96:99],128
M 48,v[100:103],124
R 192,2,2624
M 48,v[104:107],120
L 15
ds_read2_b32 v[164:165],v14 offset0:144 offset1:145
v_mfma_f32_16x16x32_bf16 v[52:55],v[88:91],v[140:143],0
ds_read2_b32 v[166:167],v14 offset0:146 offset1:147
M 52,v[92:95],136
R 196,2,2112
M 52,v[96:99],132
M 52,v[100:103],128
ds_read2_b32 v[168:169],v15 offset0:16 offset1:17
M 52,v[104:107],124
ds_read2_b32 v[170:171],v15 offset0:18 offset1:19
M 52,v[108:111],120
L 15
R 200,2,1600
v_mfma_f32_16x16x32_bf16 v[56:59],v[88:91],v[144:147],0
ds_read2_b32 v[172:173],v15 offset0:144 offset1:145
M 56,v[92:95],140
M 56,v[96:99],136
ds_read2_b32 v[174:175],v15 offset0:146 offset1:147
M 56,v[100:103],132
R 204,2,1088
M 56,v[104:107],128
ds_read2_b32 v[176:177],v16 offset0:16 offset1:17
M 56,v[108:111],124
ds_read2_b32 v[178:179],v16 offset0:18 offset1:19
M 56,v[112:115],120
L 15
v_mfma_f32_16x16x32_bf16 v[60:63],v[88:91],v[148:151],0
R 208,2,576
M 60,v[92:95],144
ds_read2_b32 v[180:181],v16 offset0:144 offset1:145
M 60,v[96:99],140
ds_read2_b32 v[182:183],v16 offset0:146 offset1:147
M 60,v[100:103],136
R 212,2,64
M 60,v[104:107],132
M 60,v[108:111],128
M 60,v[112:115],124
M 60,v[116:119],120
L 15
M 40,v[152:155],192
ds_read2_b32 v[88:89],v1 offset0:32 offset1:33
M 40,v[156:159],188
ds_read2_b32 v[90:91],v1 offset0:34 offset1:35
M 40,v[160:163],184
L 14
R 120,2,3712
M 44,v[152:155],196
ds_read2_b32 v[92:93],v1 offset0:160 offset1:161
M 44,v[156:159],192
M 44,v[160:163],188
ds_read2_b32 v[94:95],v1 offset0:162 offset1:163
M 44,v[164:167],184
L 14
R 124,2,3200
M 48,v[152:155],200
ds_read2_b32 v[96:97],v14 offset0:32 offset1:33
M 48,v[156:159],196
ds_read2_b32 v[98:99],v14 offset0:34 offset1:35
M 48,v[160:163],192
M 48,v[164:167],188
R 128,2,2688
M 48,v[168:171],184
L 15
ds_read2_b32 v[100:101],v14 offset0:160 offset1:161
M 52,v[152:155],204
ds_read2_b32 v[102:103],v14 offset0:162 offset1:163
M 52,v[156:159],200
R 132,2,2176
M 52,v[160:163],196
M 52,v[164:167],192
ds_read2_b32 v[104:105],v15 offset0:32 offset1:33
M 52,v[168:171],188
ds_read2_b32 v[106:107],v15 offset0:34 offset1:35
M 52,v[172:175],184
L 15
R 136,2,1664
M 56,v[152:155],208
ds_read2_b32 v[108:109],v15 offset0:160 offset1:161
M 56,v[156:159],204
M 56,v[160:163],200
ds_read2_b32 v[110:111],v15 offset0:162 offset1:163
M 56,v[164:167],196
R 140,2,1152
M 56,v[168:171],192
ds_read2_b32 v[112:113],v16 offset0:32 offset1:33
M 56,v[172:175],188
ds_read2_b32 v[114:115],v16 offset0:34 offset1:35
M 56,v[176:179],184
L 15
M 60,v[152:155],212
R 144,2,640
M 60,v[156:159],208
ds_read2_b32 v[116:117],v16 offset0:160 offset1:161
s_cmp_eq_u32 s83,0
s_cbranch_scc1 Lcv_c_%=
s_waitcnt vmcnt(0)
s_barrier
buffer_store_dword v23,v10,s[36:39],s48 offen offset:0 sc0 sc1
Lcv_c_%=:
M 60,v[160:163],204
ds_read2_b32 v[118:119],v16 offset0:162 offset1:163
M 60,v[164:167],200
R 148,2,128
M 60,v[168:171],196
M 60,v[172:175],192
M 60,v[176:179],188
M 60,v[180:183],184
L 15
M 40,v[88:91],128
ds_read2_b32 v[152:153],v1 offset0:48 offset1:49
M 40,v[92:95],124
ds_read2_b32 v[154:155],v1 offset0:50 offset1:51
M 40,v[96:99],120
L 14
R 184,2,3776
M 44,v[88:91],132
ds_read2_b32 v[156:157],v1 offset0:176 offset1:177
M 44,v[92:95],128
M 44,v[96:99],124
ds_read2_b32 v[158:159],v1 offset0:178 offset1:179
M 44,v[100:103],120
L 14
R 188,2,3264
M 48,v[88:91],136
ds_read2_b32 v[160:161],v14 offset0:48 offset1:49
M 48,v[92:95],132
ds_read2_b32 v[162:163],v14 offset0:50 offset1:51
M 48,v[96:99],128
M 48,v[100:103],124
R 192,2,2752
M 48,v[104:107],120
L 15
ds_read2_b32 v[164:165],v14 offset0:176 offset1:177
M 52,v[88:91],140
s_mov_b32 m0,163584
s_mov_b32 s78,0
ds_read2_b32 v[166:167],v14 offset0:178 offset1:179
M 52,v[92:95],136
R 196,2,2240
M 52,v[96:99],132
buffer_load_dword v11,s[40:43],s78 offen offset:0  lds
M 52,v[100:103],128
ds_read2_b32 v[168:169],v15 offset0:48 offset1:49
M 52,v[104:107],124
ds_read2_b32 v[170:171],v15 offset0:50 offset1:51
M 52,v[108:111],120
L 15
R 200,2,1728
M 56,v[88:91],144
ds_read2_b32 v[172:173],v15 offset0:176 offset1:177
M 56,v[92:95],140
M 56,v[96:99],136
ds_read2_b32 v[174:175],v15 offset0:178 offset1:179
M 56,v[100:103],132
R 204,2,1216
M 56,v[104:107],128
ds_read2_b32 v[176:177],v16 offset0:48 offset1:49
M 56,v[108:111],124
ds_read2_b32 v[178:179],v16 offset0:50 offset1:51
M 56,v[112:115],120
L 15
M 60,v[88:91],148
R 208,2,704
M 60,v[92:95],144
ds_read2_b32 v[180:181],v16 offset0:176 offset1:177
M 60,v[96:99],140
ds_read2_b32 v[182:183],v16 offset0:178 offset1:179
M 60,v[100:103],136
R 212,2,192
M 60,v[104:107],132
M 60,v[108:111],128
M 60,v[112:115],124
M 60,v[116:119],120
L 15
M 40,v[152:155],192
ds_read2_b32 v[88:89],v1 offset0:64 offset1:65
M 40,v[156:159],188
ds_read2_b32 v[90:91],v1 offset0:66 offset1:67
M 40,v[160:163],184
L 14
R 120,2,3840
M 44,v[152:155],196
ds_read2_b32 v[92:93],v1 offset0:192 offset1:193
M 44,v[156:159],192
M 44,v[160:163],188
ds_read2_b32 v[94:95],v1 offset0:194 offset1:195
M 44,v[164:167],184
L 14
R 124,2,3328
M 48,v[152:155],200
ds_read2_b32 v[96:97],v14 offset0:64 offset1:65
M 48,v[156:159],196
ds_read2_b32 v[98:99],v14 offset0:66 offset1:67
M 48,v[160:163],192
M 48,v[164:167],188
R 128,2,2816
M 48,v[168:171],184
L 15
ds_read2_b32 v[100:101],v14 offset0:192 offset1:193
M 52,v[152:155],204
ds_read2_b32 v[102:103],v14 offset0:194 offset1:195
M 52,v[156:159],200
R 132,2,2304
M 52,v[160:163],196
M 52,v[164:167],192
ds_read2_b32 v[104:105],v15 offset0:64 offset1:65
M 52,v[168:171],188
ds_read2_b32 v[106:107],v15 offset0:66 offset1:67
M 52,v[172:175],184
L 15
R 136,2,1792
M 56,v[152:155],208
ds_read2_b32 v[108:109],v15 offset0:192 offset1:193
M 56,v[156:159],204
M 56,v[160:163],200
ds_read2_b32 v[110:111],v15 offset0:194 offset1:195
M 56,v[164:167],196
R 140,2,1280
M 56,v[168:171],192
ds_read2_b32 v[112:113],v16 offset0:64 offset1:65
M 56,v[172:175],188
ds_read2_b32 v[114:115],v16 offset0:66 offset1:67
M 56,v[176:179],184
L 15
M 60,v[152:155],212
R 144,2,768
M 60,v[156:159],208
ds_read2_b32 v[116:117],v16 offset0:192 offset1:193
M 60,v[160:163],204
ds_read2_b32 v[118:119],v16 offset0:194 offset1:195
M 60,v[164:167],200
R 148,2,256
M 60,v[168:171],196
M 60,v[172:175],192
M 60,v[176:179],188
M 60,v[180:183],184
L 15
M 40,v[88:91],128
ds_read2_b32 v[152:153],v1 offset0:80 offset1:81
M 40,v[92:95],124
ds_read2_b32 v[154:155],v1 offset0:82 offset1:83
M 40,v[96:99],120
L 14
R 184,2,3904
M 44,v[88:91],132
ds_read2_b32 v[156:157],v1 offset0:208 offset1:209
M 44,v[92:95],128
M 44,v[96:99],124
ds_read2_b32 v[158:159],v1 offset0:210 offset1:211
M 44,v[100:103],120
L 14
R 188,2,3392
M 48,v[88:91],136
ds_read2_b32 v[160:161],v14 offset0:80 offset1:81
M 48,v[92:95],132
ds_read2_b32 v[162:163],v14 offset0:82 offset1:83
M 48,v[96:99],128
M 48,v[100:103],124
R 192,2,2880
M 48,v[104:107],120
L 15
ds_read2_b32 v[164:165],v14 offset0:208 offset1:209
M 52,v[88:91],140
ds_read2_b32 v[166:167],v14 offset0:210 offset1:211
M 52,v[92:95],136
R 196,2,2368
M 52,v[96:99],132
M 52,v[100:103],128
ds_read2_b32 v[168:169],v15 offset0:80 offset1:81
M 52,v[104:107],124
ds_read2_b32 v[170:171],v15 offset0:82 offset1:83
M 52,v[108:111],120
L 15
R 200,2,1856
M 56,v[88:91],144
ds_read2_b32 v[172:173],v15 offset0:208 offset1:209
M 56,v[92:95],140
M 56,v[96:99],136
ds_read2_b32 v[174:175],v15 offset0:210 offset1:211
M 56,v[100:103],132
R 204,2,1344
M 56,v[104:107],128
ds_read2_b32 v[176:177],v16 offset0:80 offset1:81
M 56,v[108:111],124
ds_read2_b32 v[178:179],v16 offset0:82 offset1:83
M 56,v[112:115],120
L 15
M 60,v[88:91],148
R 208,2,832
M 60,v[92:95],144
ds_read2_b32 v[180:181],v16 offset0:208 offset1:209
M 60,v[96:99],140
ds_read2_b32 v[182:183],v16 offset0:210 offset1:211
M 60,v[100:103],136
R 212,2,320
M 60,v[104:107],132
M 60,v[108:111],128
M 60,v[112:115],124
M 60,v[116:119],120
L 15
M 40,v[152:155],192
ds_read2_b32 v[88:89],v1 offset0:96 offset1:97
M 40,v[156:159],188
ds_read2_b32 v[90:91],v1 offset0:98 offset1:99
M 40,v[160:163],184
L 14
R 120,2,3968
M 44,v[152:155],196
s_mov_b32 m0,163584
s_mov_b32 s78,3741696
ds_read2_b32 v[92:93],v1 offset0:224 offset1:225
M 44,v[156:159],192
M 44,v[160:163],188
ds_read2_b32 v[94:95],v1 offset0:226 offset1:227
M 44,v[164:167],184
L 14
R 124,2,3456
M 48,v[152:155],200
ds_read2_b32 v[96:97],v14 offset0:96 offset1:97
M 48,v[156:159],196
ds_read2_b32 v[98:99],v14 offset0:98 offset1:99
M 48,v[160:163],192
buffer_load_dword v11,s[40:43],s78 offen offset:0  lds
M 48,v[164:167],188
R 128,2,2944
M 48,v[168:171],184
L 15
ds_read2_b32 v[100:101],v14 offset0:224 offset1:225
M 52,v[152:155],204
ds_read2_b32 v[102:103],v14 offset0:226 offset1:227
M 52,v[156:159],200
R 132,2,2432
M 52,v[160:163],196
M 52,v[164:167],192
ds_read2_b32 v[104:105],v15 offset0:96 offset1:97
M 52,v[168:171],188
ds_read2_b32 v[106:107],v15 offset0:98 offset1:99
M 52,v[172:175],184
L 15
R 136,2,1920
M 56,v[152:155],208
ds_read2_b32 v[108:109],v15 offset0:224 offset1:225
M 56,v[156:159],204
M 56,v[160:163],200
ds_read2_b32 v[110:111],v15 offset0:226 offset1:227
M 56,v[164:167],196
R 140,2,1408
M 56,v[168:171],192
ds_read2_b32 v[112:113],v16 offset0:96 offset1:97
M 56,v[172:175],188
ds_read2_b32 v[114:115],v16 offset0:98 offset1:99
M 56,v[176:179],184
L 15
M 60,v[152:155],212
R 144,2,896
M 60,v[156:159],208
ds_read2_b32 v[116:117],v16 offset0:224 offset1:225
M 60,v[160:163],204
ds_read2_b32 v[118:119],v16 offset0:226 offset1:227
M 60,v[164:167],200
R 148,2,384
M 60,v[168:171],196
M 60,v[172:175],192
M 60,v[176:179],188
M 60,v[180:183],184
L 15
M 40,v[88:91],128
ds_read2_b32 v[152:153],v1 offset0:112 offset1:113
M 40,v[92:95],124
ds_read2_b32 v[154:155],v1 offset0:114 offset1:115
M 40,v[96:99],120
L 14
R 184,2,4032
M 44,v[88:91],132
ds_read2_b32 v[156:157],v1 offset0:240 offset1:241
M 44,v[92:95],128
M 44,v[96:99],124
ds_read2_b32 v[158:159],v1 offset0:242 offset1:243
v_add_u32 v1,12928,v1
M 44,v[100:103],120
L 14
R 188,2,3520
M 48,v[88:91],136
ds_read2_b32 v[160:161],v14 offset0:112 offset1:113
M 48,v[92:95],132
ds_read2_b32 v[162:163],v14 offset0:114 offset1:115
M 48,v[96:99],128
M 48,v[100:103],124
R 192,2,3008
M 48,v[104:107],120
L 15
ds_read2_b32 v[164:165],v14 offset0:240 offset1:241
M 52,v[88:91],140
ds_read2_b32 v[166:167],v14 offset0:242 offset1:243
v_add_u32 v14,12928,v14
M 52,v[92:95],136
R 196,2,2496
M 52,v[96:99],132
M 52,v[100:103],128
ds_read2_b32 v[168:169],v15 offset0:112 offset1:113
M 52,v[104:107],124
ds_read2_b32 v[170:171],v15 offset0:114 offset1:115
M 52,v[108:111],120
L 15
R 200,2,1984
M 56,v[88:91],144
ds_read2_b32 v[172:173],v15 offset0:240 offset1:241
M 56,v[92:95],140
M 56,v[96:99],136
ds_read2_b32 v[174:175],v15 offset0:242 offset1:243
v_add_u32 v15,12928,v15
M 56,v[100:103],132
R 204,2,1472
M 56,v[104:107],128
ds_read2_b32 v[176:177],v16 offset0:112 offset1:113
M 56,v[108:111],124
ds_read2_b32 v[178:179],v16 offset0:114 offset1:115
M 56,v[112:115],120
L 15
M 60,v[88:91],148
R 208,2,960
M 60,v[92:95],144
ds_read2_b32 v[180:181],v16 offset0:240 offset1:241
M 60,v[96:99],140
ds_read2_b32 v[182:183],v16 offset0:242 offset1:243
v_add_u32 v16,12928,v16
M 60,v[100:103],136
)"
R"(R 212,2,448
M 60,v[104:107],132
M 60,v[108:111],128
M 60,v[112:115],124
M 60,v[116:119],120
L 15
M 40,v[152:155],192
ds_read2_b32 v[88:89],v1 offset0:0 offset1:1
M 40,v[156:159],188
ds_read2_b32 v[90:91],v1 offset0:2 offset1:3
M 40,v[160:163],184
L 14
R 120,2,16512
M 44,v[152:155],196
ds_read2_b32 v[92:93],v1 offset0:128 offset1:129
M 44,v[156:159],192
M 44,v[160:163],188
ds_read2_b32 v[94:95],v1 offset0:130 offset1:131
M 44,v[164:167],184
L 14
R 124,2,16000
M 48,v[152:155],200
ds_read2_b32 v[96:97],v14 offset0:0 offset1:1
M 48,v[156:159],196
ds_read2_b32 v[98:99],v14 offset0:2 offset1:3
M 48,v[160:163],192
M 48,v[164:167],188
R 128,2,15488
M 48,v[168:171],184
L 15
ds_read2_b32 v[100:101],v14 offset0:128 offset1:129
M 52,v[152:155],204
ds_read2_b32 v[102:103],v14 offset0:130 offset1:131
M 52,v[156:159],200
R 132,2,14976
M 52,v[160:163],196
M 52,v[164:167],192
ds_read2_b32 v[104:105],v15 offset0:0 offset1:1
M 52,v[168:171],188
ds_read2_b32 v[106:107],v15 offset0:2 offset1:3
M 52,v[172:175],184
L 15
R 136,2,14464
M 56,v[152:155],208
ds_read2_b32 v[108:109],v15 offset0:128 offset1:129
M 56,v[156:159],204
M 56,v[160:163],200
ds_read2_b32 v[110:111],v15 offset0:130 offset1:131
M 56,v[164:167],196
R 140,2,13952
M 56,v[168:171],192
ds_read2_b32 v[112:113],v16 offset0:0 offset1:1
M 56,v[172:175],188
ds_read2_b32 v[114:115],v16 offset0:2 offset1:3
M 56,v[176:179],184
L 15
M 60,v[152:155],212
R 144,2,13440
M 60,v[156:159],208
ds_read2_b32 v[116:117],v16 offset0:128 offset1:129
M 60,v[160:163],204
ds_read2_b32 v[118:119],v16 offset0:130 offset1:131
M 60,v[164:167],200
R 148,2,12928
M 60,v[168:171],196
M 60,v[172:175],192
M 60,v[176:179],188
M 60,v[180:183],184
s_cmp_eq_u32 s51,2
s_cbranch_scc1 Lcv_d_%=
ds_read_b64 v[24:25],v3 offset:2560
ds_read_b64 v[26:27],v3 offset:2048
ds_read_b64 v[28:29],v3 offset:1536
ds_read_b64 v[30:31],v3 offset:1024
ds_read_b64 v[32:33],v3 offset:512
ds_read_b64 v[34:35],v3 offset:0
L 0
v_and_b32 v36,0xffff0000,v25
v_lshlrev_b32 v37,16,v25
v_and_b32 v38,0xffff0000,v24
v_lshlrev_b32 v39,16,v24
v_fma_f32 v40,v36,s86,v40
v_fma_f32 v41,v37,s86,v41
v_fma_f32 v42,v38,s86,v42
v_fma_f32 v43,v39,s86,v43
v_cvt_pk_bf16_f32 v40,v40,v41
v_cvt_pk_bf16_f32 v41,v42,v43
ds_write_b64 v4,v[40:41] offset:0
v_and_b32 v36,0xffff0000,v27
v_lshlrev_b32 v37,16,v27
v_and_b32 v38,0xffff0000,v26
v_lshlrev_b32 v39,16,v26
v_fma_f32 v44,v36,s86,v44
v_fma_f32 v45,v37,s86,v45
v_fma_f32 v46,v38,s86,v46
v_fma_f32 v47,v39,s86,v47
v_cvt_pk_bf16_f32 v44,v44,v45
v_cvt_pk_bf16_f32 v45,v46,v47
ds_write_b64 v4,v[44:45] offset:512
v_and_b32 v36,0xffff0000,v29
v_lshlrev_b32 v37,16,v29
v_and_b32 v38,0xffff0000,v28
v_lshlrev_b32 v39,16,v28
v_fma_f32 v48,v36,s86,v48
v_fma_f32 v49,v37,s86,v49
v_fma_f32 v50,v38,s86,v50
v_fma_f32 v51,v39,s86,v51
v_cvt_pk_bf16_f32 v48,v48,v49
v_cvt_pk_bf16_f32 v49,v50,v51
ds_write_b64 v4,v[48:49] offset:1024
v_and_b32 v36,0xffff0000,v31
v_lshlrev_b32 v37,16,v31
v_and_b32 v38,0xffff0000,v30
v_lshlrev_b32 v39,16,v30
v_fma_f32 v52,v36,s86,v52
v_fma_f32 v53,v37,s86,v53
v_fma_f32 v54,v38,s86,v54
v_fma_f32 v55,v39,s86,v55
v_cvt_pk_bf16_f32 v52,v52,v53
v_cvt_pk_bf16_f32 v53,v54,v55
ds_write_b64 v4,v[52:53] offset:1536
v_and_b32 v36,0xffff0000,v33
v_lshlrev_b32 v37,16,v33
v_and_b32 v38,0xffff0000,v32
v_lshlrev_b32 v39,16,v32
v_fma_f32 v56,v36,s86,v56
v_fma_f32 v57,v37,s86,v57
v_fma_f32 v58,v38,s86,v58
v_fma_f32 v59,v39,s86,v59
v_cvt_pk_bf16_f32 v56,v56,v57
v_cvt_pk_bf16_f32 v57,v58,v59
ds_write_b64 v4,v[56:57] offset:2048
v_and_b32 v36,0xffff0000,v35
v_lshlrev_b32 v37,16,v35
v_and_b32 v38,0xffff0000,v34
v_lshlrev_b32 v39,16,v34
v_fma_f32 v60,v36,s86,v60
v_fma_f32 v61,v37,s86,v61
v_fma_f32 v62,v38,s86,v62
v_fma_f32 v63,v39,s86,v63
v_cvt_pk_bf16_f32 v60,v60,v61
v_cvt_pk_bf16_f32 v61,v62,v63
ds_write_b64 v4,v[60:61] offset:2560
s_branch Lcv_e_%=
Lcv_d_%=:
L 15
v_mfma_f32_16x16x32_bf16 v[64:67],v[88:91],v[128:131],0
ds_read2_b32 v[152:153],v1 offset0:16 offset1:17
ds_read_b64 v[24:25],v3 offset:2560
M 64,v[92:95],124
ds_read2_b32 v[154:155],v1 offset0:18 offset1:19
ds_read_b64 v[26:27],v3 offset:2048
M 64,v[96:99],120
L 15
R 184,2,16576
ds_read_b64 v[28:29],v3 offset:1536
v_mfma_f32_16x16x32_bf16 v[68:71],v[88:91],v[132:135],0
ds_read2_b32 v[156:157],v1 offset0:144 offset1:145
ds_read_b64 v[30:31],v3 offset:1024
M 68,v[92:95],128
ds_read_b64 v[32:33],v3 offset:512
ds_read_b64 v[34:35],v3 offset:0
M 68,v[96:99],124
ds_read2_b32 v[158:159],v1 offset0:146 offset1:147
M 68,v[100:103],120
L 15
R 188,2,16064
v_mfma_f32_16x16x32_bf16 v[72:75],v[88:91],v[136:139],0
ds_read2_b32 v[160:161],v14 offset0:16 offset1:17
M 72,v[92:95],132
s_mov_b32 m0,163584
s_mov_b32 s78,0
ds_read2_b32 v[162:163],v14 offset0:18 offset1:19
M 72,v[96:99],128
M 72,v[100:103],124
L 4
R 192,2,15552
v_and_b32 v36,0xffff0000,v25
M 72,v[104:107],120
s_nop 0
ds_read2_b32 v[164:165],v14 offset0:144 offset1:145
v_lshlrev_b32 v37,16,v25
v_mfma_f32_16x16x32_bf16 v[76:79],v[88:91],v[140:143],0
ds_read2_b32 v[166:167],v14 offset0:146 offset1:147
v_and_b32 v38,0xffff0000,v24
M 76,v[92:95],136
R 196,2,15040
v_lshlrev_b32 v39,16,v24
M 76,v[96:99],132
buffer_load_dword v12,s[60:63],s78 offen offset:0  lds
M 76,v[100:103],128
ds_read2_b32 v[168:169],v15 offset0:16 offset1:17
v_fma_f32 v40,v36,s86,v40
M 76,v[104:107],124
ds_read2_b32 v[170:171],v15 offset0:18 offset1:19
v_fma_f32 v41,v37,s86,v41
M 76,v[108:111],120
s_nop 0
R 200,2,14528
v_fma_f32 v42,v38,s86,v42
v_mfma_f32_16x16x32_bf16 v[80:83],v[88:91],v[144:147],0
ds_read2_b32 v[172:173],v15 offset0:144 offset1:145
v_fma_f32 v43,v39,s86,v43
M 80,v[92:95],140
v_cvt_pk_bf16_f32 v40,v40,v41
v_cvt_pk_bf16_f32 v41,v42,v43
M 80,v[96:99],136
ds_read2_b32 v[174:175],v15 offset0:146 offset1:147
v_and_b32 v36,0xffff0000,v27
M 80,v[100:103],132
R 204,2,14016
v_lshlrev_b32 v37,16,v27
M 80,v[104:107],128
ds_read2_b32 v[176:177],v16 offset0:16 offset1:17
v_and_b32 v38,0xffff0000,v26
M 80,v[108:111],124
ds_read2_b32 v[178:179],v16 offset0:18 offset1:19
v_lshlrev_b32 v39,16,v26
M 80,v[112:115],120
s_nop 0
ds_write_b64 v4,v[40:41] offset:0
v_mfma_f32_16x16x32_bf16 v[84:87],v[88:91],v[148:151],0
R 208,2,13504
v_fma_f32 v44,v36,s86,v44
M 84,v[92:95],144
ds_read2_b32 v[180:181],v16 offset0:144 offset1:145
v_fma_f32 v45,v37,s86,v45
M 84,v[96:99],140
ds_read2_b32 v[182:183],v16 offset0:146 offset1:147
v_fma_f32 v46,v38,s86,v46
M 84,v[100:103],136
R 212,2,12992
v_fma_f32 v47,v39,s86,v47
M 84,v[104:107],132
v_cvt_pk_bf16_f32 v44,v44,v45
v_cvt_pk_bf16_f32 v45,v46,v47
M 84,v[108:111],128
ds_write_b64 v4,v[44:45] offset:512
M 84,v[112:115],124
v_and_b32 v36,0xffff0000,v29
v_lshlrev_b32 v37,16,v29
M 84,v[116:119],120
L 15
v_and_b32 v38,0xffff0000,v28
v_lshlrev_b32 v39,16,v28
M 64,v[152:155],192
ds_read2_b32 v[88:89],v1 offset0:32 offset1:33
v_fma_f32 v48,v36,s86,v48
M 64,v[156:159],188
ds_read2_b32 v[90:91],v1 offset0:34 offset1:35
v_fma_f32 v49,v37,s86,v49
M 64,v[160:163],184
L 15
R 120,2,16640
v_fma_f32 v50,v38,s86,v50
M 68,v[152:155],196
ds_read2_b32 v[92:93],v1 offset0:160 offset1:161
v_fma_f32 v51,v39,s86,v51
M 68,v[156:159],192
v_cvt_pk_bf16_f32 v48,v48,v49
v_cvt_pk_bf16_f32 v49,v50,v51
M 68,v[160:163],188
ds_read2_b32 v[94:95],v1 offset0:162 offset1:163
v_and_b32 v36,0xffff0000,v31
M 68,v[164:167],184
L 15
R 124,2,16128
v_lshlrev_b32 v37,16,v31
M 72,v[152:155],200
ds_read2_b32 v[96:97],v14 offset0:32 offset1:33
v_and_b32 v38,0xffff0000,v30
M 72,v[156:159],196
ds_read2_b32 v[98:99],v14 offset0:34 offset1:35
v_lshlrev_b32 v39,16,v30
M 72,v[160:163],192
ds_write_b64 v4,v[48:49] offset:1024
M 72,v[164:167],188
R 128,2,15616
v_fma_f32 v52,v36,s86,v52
M 72,v[168:171],184
L 15
ds_read2_b32 v[100:101],v14 offset0:160 offset1:161
v_fma_f32 v53,v37,s86,v53
M 76,v[152:155],204
ds_read2_b32 v[102:103],v14 offset0:162 offset1:163
v_fma_f32 v54,v38,s86,v54
M 76,v[156:159],200
R 132,2,15104
v_fma_f32 v55,v39,s86,v55
M 76,v[160:163],196
v_cvt_pk_bf16_f32 v52,v52,v53
v_cvt_pk_bf16_f32 v53,v54,v55
M 76,v[164:167],192
ds_read2_b32 v[104:105],v15 offset0:32 offset1:33
v_and_b32 v36,0xffff0000,v33
M 76,v[168:171],188
ds_read2_b32 v[106:107],v15 offset0:34 offset1:35
v_lshlrev_b32 v37,16,v33
M 76,v[172:175],184
L 15
R 136,2,14592
v_and_b32 v38,0xffff0000,v32
M 80,v[152:155],208
ds_read2_b32 v[108:109],v15 offset0:160 offset1:161
v_lshlrev_b32 v39,16,v32
M 80,v[156:159],204
ds_write_b64 v4,v[52:53] offset:1536
M 80,v[160:163],200
ds_read2_b32 v[110:111],v15 offset0:162 offset1:163
v_fma_f32 v56,v36,s86,v56
M 80,v[164:167],196
R 140,2,14080
v_fma_f32 v57,v37,s86,v57
M 80,v[168:171],192
ds_read2_b32 v[112:113],v16 offset0:32 offset1:33
v_fma_f32 v58,v38,s86,v58
M 80,v[172:175],188
ds_read2_b32 v[114:115],v16 offset0:34 offset1:35
v_fma_f32 v59,v39,s86,v59
M 80,v[176:179],184
L 15
v_cvt_pk_bf16_f32 v56,v56,v57
v_cvt_pk_bf16_f32 v57,v58,v59
M 84,v[152:155],212
R 144,2,13568
v_and_b32 v36,0xffff0000,v35
M 84,v[156:159],208
ds_read2_b32 v[116:117],v16 offset0:160 offset1:161
v_lshlrev_b32 v37,16,v35
M 84,v[160:163],204
ds_read2_b32 v[118:119],v16 offset0:162 offset1:163
v_and_b32 v38,0xffff0000,v34
M 84,v[164:167],200
R 148,2,13056
v_lshlrev_b32 v39,16,v34
M 84,v[168:171],196
ds_write_b64 v4,v[56:57] offset:2048
M 84,v[172:175],192
v_fma_f32 v60,v36,s86,v60
v_fma_f32 v61,v37,s86,v61
M 84,v[176:179],188
v_fma_f32 v62,v38,s86,v62
v_fma_f32 v63,v39,s86,v63
M 84,v[180:183],184
L 15
v_cvt_pk_bf16_f32 v60,v60,v61
v_cvt_pk_bf16_f32 v61,v62,v63
M 64,v[88:91],128
ds_read2_b32 v[152:153],v1 offset0:48 offset1:49
M 64,v[92:95],124
ds_read2_b32 v[154:155],v1 offset0:50 offset1:51
M 64,v[96:99],120
L 15
R 184,2,16704
M 68,v[88:91],132
ds_read2_b32 v[156:157],v1 offset0:176 offset1:177
M 68,v[92:95],128
ds_write_b64 v4,v[60:61] offset:2560
M 68,v[96:99],124
ds_read2_b32 v[158:159],v1 offset0:178 offset1:179
M 68,v[100:103],120
L 15
R 188,2,16192
M 72,v[88:91],136
ds_read2_b32 v[160:161],v14 offset0:48 offset1:49
M 72,v[92:95],132
ds_read2_b32 v[162:163],v14 offset0:50 offset1:51
M 72,v[96:99],128
M 72,v[100:103],124
R 192,2,15680
M 72,v[104:107],120
L 15
ds_read2_b32 v[164:165],v14 offset0:176 offset1:177
M 76,v[88:91],140
ds_read2_b32 v[166:167],v14 offset0:178 offset1:179
M 76,v[92:95],136
R 196,2,15168
M 76,v[96:99],132
M 76,v[100:103],128
ds_read2_b32 v[168:169],v15 offset0:48 offset1:49
M 76,v[104:107],124
ds_read2_b32 v[170:171],v15 offset0:50 offset1:51
M 76,v[108:111],120
L 15
R 200,2,14656
M 80,v[88:91],144
ds_read2_b32 v[172:173],v15 offset0:176 offset1:177
M 80,v[92:95],140
M 80,v[96:99],136
ds_read2_b32 v[174:175],v15 offset0:178 offset1:179
M 80,v[100:103],132
R 204,2,14144
M 80,v[104:107],128
s_mov_b32 m0,163584
s_mov_b32 s78,1847296
ds_read2_b32 v[176:177],v16 offset0:48 offset1:49
M 80,v[108:111],124
ds_read2_b32 v[178:179],v16 offset0:50 offset1:51
M 80,v[112:115],120
L 15
buffer_load_dword v12,s[60:63],s78 offen offset:0  lds
M 84,v[88:91],148
R 208,2,13632
M 84,v[92:95],144
ds_read2_b32 v[180:181],v16 offset0:176 offset1:177
M 84,v[96:99],140
ds_read2_b32 v[182:183],v16 offset0:178 offset1:179
M 84,v[100:103],136
R 212,2,13120
M 84,v[104:107],132
M 84,v[108:111],128
M 84,v[112:115],124
M 84,v[116:119],120
L 15
M 64,v[152:155],192
ds_read2_b32 v[88:89],v1 offset0:64 offset1:65
M 64,v[156:159],188
ds_read2_b32 v[90:91],v1 offset0:66 offset1:67
M 64,v[160:163],184
L 14
R 120,2,16768
M 68,v[152:155],196
ds_read2_b32 v[92:93],v1 offset0:192 offset1:193
M 68,v[156:159],192
M 68,v[160:163],188
ds_read2_b32 v[94:95],v1 offset0:194 offset1:195
M 68,v[164:167],184
L 14
R 124,2,16256
M 72,v[152:155],200
ds_read2_b32 v[96:97],v14 offset0:64 offset1:65
M 72,v[156:159],196
ds_read2_b32 v[98:99],v14 offset0:66 offset1:67
M 72,v[160:163],192
M 72,v[164:167],188
R 128,2,15744
M 72,v[168:171],184
L 15
ds_read2_b32 v[100:101],v14 offset0:192 offset1:193
M 76,v[152:155],204
ds_read2_b32 v[102:103],v14 offset0:194 offset1:195
M 76,v[156:159],200
R 132,2,15232
M 76,v[160:163],196
M 76,v[164:167],192
ds_read2_b32 v[104:105],v15 offset0:64 offset1:65
M 76,v[168:171],188
ds_read2_b32 v[106:107],v15 offset0:66 offset1:67
M 76,v[172:175],184
L 15
R 136,2,14720
M 80,v[152:155],208
ds_read2_b32 v[108:109],v15 offset0:192 offset1:193
M 80,v[156:159],204
M 80,v[160:163],200
ds_read2_b32 v[110:111],v15 offset0:194 offset1:195
M 80,v[164:167],196
R 140,2,14208
M 80,v[168:171],192
ds_read2_b32 v[112:113],v16 offset0:64 offset1:65
M 80,v[172:175],188
ds_read2_b32 v[114:115],v16 offset0:66 offset1:67
M 80,v[176:179],184
L 15
M 84,v[152:155],212
R 144,2,13696
M 84,v[156:159],208
ds_read2_b32 v[116:117],v16 offset0:192 offset1:193
M 84,v[160:163],204
ds_read2_b32 v[118:119],v16 offset0:194 offset1:195
M 84,v[164:167],200
R 148,2,13184
M 84,v[168:171],196
M 84,v[172:175],192
M 84,v[176:179],188
M 84,v[180:183],184
L 15
M 64,v[88:91],128
ds_read2_b32 v[152:153],v1 offset0:80 offset1:81
M 64,v[92:95],124
ds_read2_b32 v[154:155],v1 offset0:82 offset1:83
M 64,v[96:99],120
L 14
R 184,2,16832
M 68,v[88:91],132
ds_read2_b32 v[156:157],v1 offset0:208 offset1:209
M 68,v[92:95],128
M 68,v[96:99],124
ds_read2_b32 v[158:159],v1 offset0:210 offset1:211
M 68,v[100:103],120
L 14
R 188,2,16320
M 72,v[88:91],136
ds_read2_b32 v[160:161],v14 offset0:80 offset1:81
M 72,v[92:95],132
ds_read2_b32 v[162:163],v14 offset0:82 offset1:83
M 72,v[96:99],128
M 72,v[100:103],124
R 192,2,15808
M 72,v[104:107],120
)"
R"(L 15
ds_read2_b32 v[164:165],v14 offset0:208 offset1:209
M 76,v[88:91],140
ds_read2_b32 v[166:167],v14 offset0:210 offset1:211
M 76,v[92:95],136
R 196,2,15296
M 76,v[96:99],132
M 76,v[100:103],128
ds_read2_b32 v[168:169],v15 offset0:80 offset1:81
M 76,v[104:107],124
ds_read2_b32 v[170:171],v15 offset0:82 offset1:83
M 76,v[108:111],120
L 15
R 200,2,14784
M 80,v[88:91],144
ds_read2_b32 v[172:173],v15 offset0:208 offset1:209
M 80,v[92:95],140
M 80,v[96:99],136
ds_read2_b32 v[174:175],v15 offset0:210 offset1:211
M 80,v[100:103],132
R 204,2,14272
M 80,v[104:107],128
ds_read2_b32 v[176:177],v16 offset0:80 offset1:81
M 80,v[108:111],124
ds_read2_b32 v[178:179],v16 offset0:82 offset1:83
M 80,v[112:115],120
L 15
M 84,v[88:91],148
R 208,2,13760
M 84,v[92:95],144
ds_read2_b32 v[180:181],v16 offset0:208 offset1:209
M 84,v[96:99],140
ds_read2_b32 v[182:183],v16 offset0:210 offset1:211
M 84,v[100:103],136
R 212,2,13248
M 84,v[104:107],132
M 84,v[108:111],128
M 84,v[112:115],124
M 84,v[116:119],120
L 15
M 64,v[152:155],192
ds_read2_b32 v[88:89],v1 offset0:96 offset1:97
M 64,v[156:159],188
ds_read2_b32 v[90:91],v1 offset0:98 offset1:99
M 64,v[160:163],184
L 14
R 120,2,16896
M 68,v[152:155],196
ds_read2_b32 v[92:93],v1 offset0:224 offset1:225
M 68,v[156:159],192
M 68,v[160:163],188
ds_read2_b32 v[94:95],v1 offset0:226 offset1:227
M 68,v[164:167],184
L 14
R 124,2,16384
M 72,v[152:155],200
ds_read2_b32 v[96:97],v14 offset0:96 offset1:97
M 72,v[156:159],196
ds_read2_b32 v[98:99],v14 offset0:98 offset1:99
M 72,v[160:163],192
M 72,v[164:167],188
R 128,2,15872
M 72,v[168:171],184
L 15
ds_read2_b32 v[100:101],v14 offset0:224 offset1:225
M 76,v[152:155],204
ds_read2_b32 v[102:103],v14 offset0:226 offset1:227
M 76,v[156:159],200
R 132,2,15360
M 76,v[160:163],196
M 76,v[164:167],192
ds_read2_b32 v[104:105],v15 offset0:96 offset1:97
M 76,v[168:171],188
ds_read2_b32 v[106:107],v15 offset0:98 offset1:99
M 76,v[172:175],184
L 15
R 136,2,14848
M 80,v[152:155],208
ds_read2_b32 v[108:109],v15 offset0:224 offset1:225
M 80,v[156:159],204
M 80,v[160:163],200
ds_read2_b32 v[110:111],v15 offset0:226 offset1:227
M 80,v[164:167],196
R 140,2,14336
M 80,v[168:171],192
ds_read2_b32 v[112:113],v16 offset0:96 offset1:97
M 80,v[172:175],188
ds_read2_b32 v[114:115],v16 offset0:98 offset1:99
M 80,v[176:179],184
L 15
M 84,v[152:155],212
R 144,2,13824
M 84,v[156:159],208
ds_read2_b32 v[116:117],v16 offset0:224 offset1:225
M 84,v[160:163],204
ds_read2_b32 v[118:119],v16 offset0:226 offset1:227
M 84,v[164:167],200
R 148,2,13312
M 84,v[168:171],196
M 84,v[172:175],192
M 84,v[176:179],188
M 84,v[180:183],184
L 15
M 64,v[88:91],128
ds_read2_b32 v[152:153],v1 offset0:112 offset1:113
M 64,v[92:95],124
ds_read2_b32 v[154:155],v1 offset0:114 offset1:115
M 64,v[96:99],120
L 14
R 184,2,16960
M 68,v[88:91],132
ds_read2_b32 v[156:157],v1 offset0:240 offset1:241
M 68,v[92:95],128
M 68,v[96:99],124
ds_read2_b32 v[158:159],v1 offset0:242 offset1:243
M 68,v[100:103],120
L 14
R 188,2,16448
M 72,v[88:91],136
ds_read2_b32 v[160:161],v14 offset0:112 offset1:113
M 72,v[92:95],132
ds_read2_b32 v[162:163],v14 offset0:114 offset1:115
M 72,v[96:99],128
M 72,v[100:103],124
R 192,2,15936
M 72,v[104:107],120
L 15
ds_read2_b32 v[164:165],v14 offset0:240 offset1:241
M 76,v[88:91],140
ds_read2_b32 v[166:167],v14 offset0:242 offset1:243
M 76,v[92:95],136
R 196,2,15424
M 76,v[96:99],132
M 76,v[100:103],128
ds_read2_b32 v[168:169],v15 offset0:112 offset1:113
M 76,v[104:107],124
ds_read2_b32 v[170:171],v15 offset0:114 offset1:115
M 76,v[108:111],120
L 15
R 200,2,14912
M 80,v[88:91],144
ds_read2_b32 v[172:173],v15 offset0:240 offset1:241
M 80,v[92:95],140
M 80,v[96:99],136
ds_read2_b32 v[174:175],v15 offset0:242 offset1:243
M 80,v[100:103],132
R 204,2,14400
M 80,v[104:107],128
ds_read2_b32 v[176:177],v16 offset0:112 offset1:113
M 80,v[108:111],124
ds_read2_b32 v[178:179],v16 offset0:114 offset1:115
M 80,v[112:115],120
L 15
M 84,v[88:91],148
R 208,2,13888
M 84,v[92:95],144
ds_read2_b32 v[180:181],v16 offset0:240 offset1:241
M 84,v[96:99],140
ds_read2_b32 v[182:183],v16 offset0:242 offset1:243
M 84,v[100:103],136
R 212,2,13376
M 84,v[104:107],132
M 84,v[108:111],128
M 84,v[112:115],124
M 84,v[116:119],120
L 15
M 64,v[152:155],192
M 64,v[156:159],188
M 64,v[160:163],184
L 12
M 68,v[152:155],196
M 68,v[156:159],192
M 68,v[160:163],188
M 68,v[164:167],184
L 9
M 72,v[152:155],200
M 72,v[156:159],196
M 72,v[160:163],192
ds_read_b64 v[24:25],v3 offset:15488
ds_read_b64 v[26:27],v3 offset:14976
M 72,v[164:167],188
ds_read_b64 v[28:29],v3 offset:14464
ds_read_b64 v[30:31],v3 offset:13952
M 72,v[168:171],184
L 10
ds_read_b64 v[32:33],v3 offset:13440
ds_read_b64 v[34:35],v3 offset:12928
M 76,v[152:155],204
M 76,v[156:159],200
M 76,v[160:163],196
M 76,v[164:167],192
M 76,v[168:171],188
M 76,v[172:175],184
L 9
M 80,v[152:155],208
M 80,v[156:159],204
M 80,v[160:163],200
M 80,v[164:167],196
M 80,v[168:171],192
M 80,v[172:175],188
M 80,v[176:179],184
L 6
M 84,v[152:155],212
M 84,v[156:159],208
M 84,v[160:163],204
M 84,v[164:167],200
M 84,v[168:171],196
M 84,v[172:175],192
M 84,v[176:179],188
M 84,v[180:183],184
L 0
v_and_b32 v36,0xffff0000,v25
v_lshlrev_b32 v37,16,v25
v_and_b32 v38,0xffff0000,v24
v_lshlrev_b32 v39,16,v24
v_fma_f32 v64,v36,s87,v64
v_fma_f32 v65,v37,s87,v65
v_fma_f32 v66,v38,s87,v66
v_fma_f32 v67,v39,s87,v67
v_cvt_pk_bf16_f32 v64,v64,v65
v_cvt_pk_bf16_f32 v65,v66,v67
ds_write_b64 v4,v[64:65] offset:12928
v_and_b32 v36,0xffff0000,v27
v_lshlrev_b32 v37,16,v27
v_and_b32 v38,0xffff0000,v26
v_lshlrev_b32 v39,16,v26
v_fma_f32 v68,v36,s87,v68
v_fma_f32 v69,v37,s87,v69
v_fma_f32 v70,v38,s87,v70
v_fma_f32 v71,v39,s87,v71
v_cvt_pk_bf16_f32 v68,v68,v69
v_cvt_pk_bf16_f32 v69,v70,v71
ds_write_b64 v4,v[68:69] offset:13440
v_and_b32 v36,0xffff0000,v29
v_lshlrev_b32 v37,16,v29
v_and_b32 v38,0xffff0000,v28
v_lshlrev_b32 v39,16,v28
v_fma_f32 v72,v36,s87,v72
v_fma_f32 v73,v37,s87,v73
v_fma_f32 v74,v38,s87,v74
v_fma_f32 v75,v39,s87,v75
v_cvt_pk_bf16_f32 v72,v72,v73
v_cvt_pk_bf16_f32 v73,v74,v75
ds_write_b64 v4,v[72:73] offset:13952
v_and_b32 v36,0xffff0000,v31
v_lshlrev_b32 v37,16,v31
v_and_b32 v38,0xffff0000,v30
v_lshlrev_b32 v39,16,v30
v_fma_f32 v76,v36,s87,v76
v_fma_f32 v77,v37,s87,v77
v_fma_f32 v78,v38,s87,v78
v_fma_f32 v79,v39,s87,v79
v_cvt_pk_bf16_f32 v76,v76,v77
v_cvt_pk_bf16_f32 v77,v78,v79
ds_write_b64 v4,v[76:77] offset:14464
v_and_b32 v36,0xffff0000,v33
v_lshlrev_b32 v37,16,v33
v_and_b32 v38,0xffff0000,v32
v_lshlrev_b32 v39,16,v32
v_fma_f32 v80,v36,s87,v80
v_fma_f32 v81,v37,s87,v81
v_fma_f32 v82,v38,s87,v82
v_fma_f32 v83,v39,s87,v83
v_cvt_pk_bf16_f32 v80,v80,v81
v_cvt_pk_bf16_f32 v81,v82,v83
ds_write_b64 v4,v[80:81] offset:14976
v_and_b32 v36,0xffff0000,v35
v_lshlrev_b32 v37,16,v35
v_and_b32 v38,0xffff0000,v34
v_lshlrev_b32 v39,16,v34
v_fma_f32 v84,v36,s87,v84
v_fma_f32 v85,v37,s87,v85
v_fma_f32 v86,v38,s87,v86
v_fma_f32 v87,v39,s87,v87
v_cvt_pk_bf16_f32 v84,v84,v85
v_cvt_pk_bf16_f32 v85,v86,v87
ds_write_b64 v4,v[84:85] offset:15488
Lcv_e_%=:
L 0
s_barrier
R 152,5,0
R 156,5,512
R 160,5,1024
R 164,5,1536
R 168,5,2048
R 172,5,2560
s_mov_b32 s78,99614720
L 5
buffer_store_dwordx4 v[152:155],v7,s[36:39],s78 offen offset:0 sc0 sc1
s_add_u32 s78,s56,100139008
L 4
buffer_store_dwordx4 v[156:159],v7,s[36:39],s78 offen offset:0 sc0 sc1
s_mov_b32 s78,100663296
L 3
buffer_store_dwordx4 v[160:163],v7,s[36:39],s78 offen offset:0 sc0 sc1
s_add_u32 s78,s56,101187584
L 2
buffer_store_dwordx4 v[164:167],v7,s[36:39],s78 offen offset:0 sc0 sc1
s_mov_b32 s78,101711872
L 1
buffer_store_dwordx4 v[168:171],v7,s[36:39],s78 offen offset:0 sc0 sc1
s_add_u32 s78,s56,102236160
L 0
buffer_store_dwordx4 v[172:175],v7,s[36:39],s78 offen offset:0 sc0 sc1
s_waitcnt vmcnt(0)
s_barrier
s_add_u32 s48,s48,32768
buffer_store_dword v23,v10,s[36:39],s48 offen offset:0 sc0 sc1
s_cmp_eq_u32 s51,2
s_cbranch_scc0 Lcv_f_%=
s_mov_b32 m0,163584
s_mov_b32 s78,3694592
buffer_load_dword v12,s[60:63],s78 offen offset:0  lds
s_mov_b32 m0,163584
s_mov_b32 s78,5541888
buffer_load_dword v12,s[60:63],s78 offen offset:0  lds
Lcv_f_%=:
Lcode_cv_e:
.purgem M
.purgem R
.purgem T
.purgem D
.purgem L
)"
  :: "s"(ws), "s"(sWave), "s"(sBuf), "s"(sAuxOff), "s"(sFoutOff), "s"(sC0), "s"(sC1), "s"(sExp), "v"(vL16), "v"(vPA), "v"(vPB), "v"(vPU), "v"(vPW), "v"(vOR), "v"(vORA), "v"(vOG), "v"(vFA), "v"(vFB), "v"(vFlag), "v"(vTW), "v"(vTX), "s"(w3), "s"(w4) : "v0", "v1", "v2", "v3", "v4", "v5", "v6", "v7", "v8", "v9", "v10", "v11", "v12", "v13", "v14", "v15", "v16", "v17", "v18", "v19", "v20", "v21", "v22", "v23", "v24", "v25", "v26", "v27", "v28", "v29", "v30", "v31", "v32", "v33", "v34", "v35", "v36", "v37", "v38", "v39", "v40", "v41", "v42", "v43", "v44", "v45", "v46", "v47", "v48", "v49", "v50", "v51", "v52", "v53", "v54", "v55", "v56", "v57", "v58", "v59", "v60", "v61", "v62", "v63", "v64", "v65", "v66", "v67", "v68", "v69", "v70", "v71", "v72", "v73", "v74", "v75", "v76", "v77", "v78", "v79", "v80", "v81", "v82", "v83", "v84", "v85", "v86", "v87", "v88", "v89", "v90", "v91", "v92", "v93", "v94", "v95", "v96", "v97", "v98", "v99", "v100", "v101", "v102", "v103", "v104", "v105", "v106", "v107", "v108", "v109", "v110", "v111", "v112", "v113", "v114", "v115", "v116", "v117", "v118", "v119", "v120", "v121", "v122", "v123", "v124", "v125", "v126", "v127", "v128", "v129", "v130", "v131", "v132", "v133", "v134", "v135", "v136", "v137", "v138", "v139", "v140", "v141", "v142", "v143", "v144", "v145", "v146", "v147", "v148", "v149", "v150", "v151", "v152", "v153", "v154", "v155", "v156", "v157", "v158", "v159", "v160", "v161", "v162", "v163", "v164", "v165", "v166", "v167", "v168", "v169", "v170", "v171", "v172", "v173", "v174", "v175", "v176", "v177", "v178", "v179", "v180", "v181", "v182", "v183", "v184", "v185", "v186", "v187", "v188", "v189", "v190", "v191", "v192", "v193", "v194", "v195", "v196", "v197", "v198", "v199", "v200", "v201", "v202", "v203", "v204", "v205", "v206", "v207", "v208", "v209", "v210", "v211", "v212", "v213", "v214", "v215", "v216", "v217", "v218", "v219", "v220", "v221", "v222", "v223", "v224", "v225", "v226", "v227", "v228", "v229", "v230", "v231", "a0", "a1", "a2", "a3", "a4", "a5", "a6", "a7", "a8", "a9", "a10", "a11", "a12", "a13", "a14", "a15", "a16", "a17", "a18", "a19", "a20", "a21", "a22", "a23", "a24", "a25", "a26", "a27", "a28", "a29", "a30", "a31", "a32", "a33", "a34", "a35", "a36", "a37", "a38", "a39", "a40", "a41", "a42", "a43", "a44", "a45", "a46", "a47", "a48", "a49", "a50", "a51", "a52", "a53", "a54", "a55", "a56", "a57", "a58", "a59", "a60", "a61", "a62", "a63", "a64", "a65", "a66", "a67", "a68", "a69", "a70", "a71", "a72", "a73", "a74", "a75", "a76", "a77", "a78", "a79", "a80", "a81", "a82", "a83", "a84", "a85", "a86", "a87", "a88", "a89", "a90", "a91", "a92", "a93", "a94", "a95", "a96", "a97", "a98", "a99", "a100", "a101", "a102", "a103", "a104", "a105", "a106", "a107", "a108", "a109", "a110", "a111", "a112", "a113", "a114", "a115", "a116", "a117", "a118", "a119", "a120", "a121", "a122", "a123", "a124", "a125", "a126", "a127", "a128", "a129", "a130", "a131", "a132", "a133", "a134", "a135", "a136", "a137", "a138", "a139", "a140", "a141", "a142", "a143", "a144", "a145", "a146", "a147", "a148", "a149", "a150", "a151", "a152", "a153", "a154", "a155", "a156", "a157", "a158", "a159", "a160", "a161", "a162", "a163", "a164", "a165", "a166", "a167", "a168", "a169", "a170", "a171", "a172", "a173", "a174", "a175", "a176", "a177", "a178", "a179", "a180", "a181", "a182", "a183", "a184", "a185", "a186", "a187", "a188", "a189", "a190", "a191", "a192", "a193", "a194", "a195", "a196", "a197", "a198", "a199", "a200", "a201", "a202", "a203", "a204", "a205", "a206", "a207", "a208", "a209", "a210", "a211", "a212", "a213", "a214", "a215", "a216", "a217", "a218", "a219", "a220", "a221", "a222", "a223", "a224", "a225", "a226", "a227", "a228", "a229", "a230", "a231", "a232", "a233", "a234", "a235", "a236", "a237", "a238", "a239", "a240", "a241", "a242", "a243", "a244", "a245", "a246", "a247", "a248", "a249", "a250", "a251", "a252", "a253", "a254", "a255", "s36", "s37", "s38", "s39", "s40", "s41", "s42", "s43", "s44", "s45", "s46", "s47", "s48", "s49", "s50", "s51", "s52", "s53", "s54", "s55", "s56", "s57", "s58", "s59", "s60", "s61", "s62", "s63", "s64", "s65", "s66", "s67", "s68", "s69", "s70", "s71", "s72", "s73", "s74", "s75", "s76", "s77", "s78", "s79", "s80", "s81", "s82", "s83", "s84", "s85", "s86", "s87", "s88", "s89", "s90", "s91", "s92", "s93", "s94", "s95", "s96", "s97", "vcc", "scc", "memory");
    LANE_ID(lq_);
    const int g = QT0 + (NCV - 1 - iw) * 164 + min(wave * 64 + lq_, 163);
    if (QTB) touch_lds((const char*)q + (size_t)(g < 32768 ? g : 0) * 128, sScr);
  }
  WAIT_LGKM0();
  __builtin_amdgcn_s_barrier();
  CTS(10);
  }
  const unsigned sDbg0 = 110u * 1048576u + (b * 4 + wave) * 8192;
  constexpr unsigned OOB = 0x80000000u;
#define HLP 0
  if (kind == 6 || (kind == 4 && j4 < HLP)) {
    LANE_ID(lr);
    const int r8 = lr & 7, seg = lr >> 3, j = lr & 15, g = lr >> 4;
    const unsigned sWave = wave, sExp = __builtin_amdgcn_readfirstlane(ep), sG0 = kind == 6 ? slice : 64 + j4, sStep = NRW, sOutOff = kind == 6 ? 256 : 65 + j4, sAuxOff = HLP, sBuf = kind & 2, sRk = slice;
    const unsigned sDbg = sDbg0 + (kind == 6 ? 0 : 1024);
    const unsigned vRF = (wave * 8 + (seg >> 2)) * 1024 + ((seg & 3) * 16 + r8) * 16;
    const unsigned vRwr = seg == 0 ? r8 * 64 + wave * 4 : 512 + lr * 4, vRrd = r8 * 64;
    const unsigned vRP = g == (j >> 2) ? r8 * 64 + 16 + wave * 8 + (seg & 1) * 4 : 768 + lr * 4;
    const unsigned vRS = (wave == 0 && seg < 2) ? r8 * 16 + seg * 256 : OOB, vFlag = lr < 32 ? lr * 4 : OOB;
    const unsigned vL4 = ((lr & 2) * 4 + wave * 2 + (lr & 1)) * 128, vLY = min((wave < 3 ? wave * 32 : 112) + lr, wave < 2 ? wave * 32 + 31 : wave == 2 ? 111 : NCV - 1) * 128;
    const unsigned vQ = r8 * 2048 + wave * 512 + seg * 16;
    const unsigned vY0 = wave * 4096 + lr * 16;
    const unsigned vTr = 4096 + wave * 4096 + ((2 * g + ((j & 3) >> 1)) * 8 + (j >> 2)) * 16 + (j & 1) * 8;
    const unsigned one = g == (j >> 2) ? 0x3f80u << (16 * (j & 1)) : 0u, vSa = (j & 2) ? 0u : one, vSb = (j & 2) ? one : 0u;
    (void)sDbg;
    asm volatile(
R"(.macro M d,w,x
v_mfma_f32_16x16x32_bf16 v[\d:\d+3], \w, v[\x:\x+3], v[\d:\d+3]
.endm
.macro R d,a,o
ds_read_b128 v[\d:\d+3], v\a offset:\o
.endm
.macro T d,a,o
ds_read_b64_tr_b16 \d, v\a offset:\o
.endm
.macro D a,s,o
buffer_load_dwordx4 v\a, s[36:39], s\s offen offset:\o sc1 lds
.endm
.macro L c
s_waitcnt lgkmcnt(\c)
.endm
Lcode_r_b:
s_mov_b64 s[36:37],%0
s_mov_b32 s38,111243392
s_mov_b32 s39,0x00020000
s_mov_b32 s44,%1
s_mov_b32 s45,%2
s_mov_b32 s46,%3
s_mov_b32 s47,%4
s_mov_b32 s49,%5
s_mov_b32 s51,%6
s_mov_b32 s93,%7
s_mov_b32 s53,%8
v_mov_b32 v0,%9
v_mov_b32 v1,%10
v_mov_b32 v2,%11
v_mov_b32 v3,%12
v_mov_b32 v4,%13
v_mov_b32 v5,%14
v_mov_b32 v6,%15
v_mov_b32 v7,%16
v_mov_b32 v8,%17
v_mov_b32 v9,%18
v_mov_b32 v10,%19
v_mov_b32 v11,%20
v_mov_b32 v12,%21
s_mov_b64 s[84:85],%22
s_mov_b32 s86,4194304
s_mov_b32 s87,0x00020000
s_lshl_b32 s55,s45,14
buffer_load_dwordx4 v[164:167],v8,s[84:87],s55 offen offset:0 nt
buffer_load_dwordx4 v[168:171],v8,s[84:87],s55 offen offset:128 nt
buffer_load_dwordx4 v[172:175],v8,s[84:87],s55 offen offset:256 nt
buffer_load_dwordx4 v[176:179],v8,s[84:87],s55 offen offset:384 nt
v_mov_b32 v13,s53
v_mbcnt_lo_u32_b32 v15,-1,0
v_mbcnt_hi_u32_b32 v15,-1,v15
v_lshlrev_b32 v15,4,v15
s_mov_b32 s82,0
s_mov_b32 s63,0x7fff0000
s_mov_b32 s64,0
s_mov_b32 s67,0
s_lshl_b32 s65,s44,12
s_add_u32 s65,s65,4096
s_lshl_b32 s66,s44,7
s_add_u32 s66,s66,109056000
s_mov_b32 s70,0xaaaaaaaa
s_mov_b32 s71,0xaaaaaaaa
s_mov_b32 s72,0xcccccccc
s_mov_b32 s73,0xcccccccc
s_mov_b32 s74,0xff00ff00
s_mov_b32 s75,0xff00ff00
s_mov_b32 s68,0x3a800000
s_mov_b32 s69,0x3727c5ac
s_mov_b32 s76,0xc0135761
s_mov_b32 s77,0x3d372713
v_mov_b32 v32,0x3f803f80
v_mov_b32 v33,0x3f803f80
v_mov_b32 v34,0x3f803f80
v_mov_b32 v35,0x3f803f80
v_mov_b32 v24,0
v_mov_b32 v25,0
v_mov_b32 v26,v11
v_mov_b32 v27,v12
v_mov_b32 v28,0
v_mov_b32 v29,0
s_waitcnt vmcnt(0)
Lr_a0_%=:
s_waitcnt vmcnt(10)
v_mfma_f32_16x16x32_bf16 v[36:39],v[32:35],v[164:167],0
s_add_u32 s79,s45,s46
s_cmp_lt_u32 s45,64
s_cselect_b32 s78,s49,0
v_mfma_f32_16x16x32_bf16 v[40:43],v[164:167],v[164:167],0
s_cmp_ge_u32 s79,64
s_cselect_b32 s78,s78,0
s_add_u32 s62,s79,s78
M 36,v[32:35],168
s_lshr_b32 s78,s45,1
s_and_b32 s79,s45,1
s_lshl_b32 s79,s79,7
M 40,v[168:171],168
s_lshl_b32 s57,s78,15
s_lshl_b32 s55,s62,14
s_cmp_lt_u32 s62,s47
M 36,v[32:35],172
s_cselect_b32 s55,s55,0x7fff0000
buffer_load_dwordx4 v[196:199],v8,s[84:87],s55 offen offset:0 nt
M 40,v[172:175],172
s_add_u32 s57,s57,s79
buffer_load_dwordx4 v[200:203],v8,s[84:87],s55 offen offset:128 nt
M 36,v[32:35],176
s_lshl_b32 s59,s78,9
buffer_load_dwordx4 v[204:207],v8,s[84:87],s55 offen offset:256 nt
M 40,v[176:179],176
s_add_u32 s57,s57,8388608
buffer_load_dwordx4 v[208:211],v8,s[84:87],s55 offen offset:384 nt
v_mfma_f32_16x16x32_bf16 v[116:119],v[26:29],v[164:167],0
s_add_u32 s59,s59,s79
s_lshl_b32 s60,s45,9
s_add_u32 s58,s57,4096
v_mfma_f32_16x16x32_bf16 v[120:123],v[24:27],v[164:167],0
s_add_u32 s59,s59,106954752
s_add_u32 s60,s60,s66
v_mfma_f32_16x16x32_bf16 v[124:127],v[26:29],v[168:171],0
v_mfma_f32_16x16x32_bf16 v[128:131],v[24:27],v[168:171],0
v_add_f32_dpp v44,v36,v36 row_ror:8 row_mask:0xf bank_mask:0xf
v_cndmask_b32 v45,v40,v41,s[70:71]
v_mfma_f32_16x16x32_bf16 v[132:135],v[26:29],v[172:175],0
ds_write_b32 v2,v44 offset:0
v_mfma_f32_16x16x32_bf16 v[136:139],v[24:27],v[172:175],0
v_cndmask_b32 v46,v42,v43,s[70:71]
v_cndmask_b32 v45,v45,v46,s[72:73]
v_mfma_f32_16x16x32_bf16 v[140:143],v[26:29],v[176:179],0
ds_write_b32 v0,v45 offset:0
v_mfma_f32_16x16x32_bf16 v[144:147],v[24:27],v[176:179],0
L 0
s_barrier
R 52,3,0
R 56,3,16
R 60,3,32
s_cmp_lg_u32 s67,0
s_cbranch_scc1 Lr_b0_%=
s_waitcnt vmcnt(4)
buffer_store_dword v13,v5,s[36:39],s63 offen offset:0 sc0 sc1
s_mov_b32 s63,0x7fff0000
s_cmp_lt_u32 s45,64
s_cselect_b32 s64,-192,0
s_add_u32 s64,s64,256
s_cmp_lt_u32 s45,64
s_cselect_b32 s61,0,32768
s_add_u32 s61,s61,111177728
s_mul_i32 s56,s45,16384
s_add_u32 s56,s56,98566144
Lr_bpy0_%=:
buffer_load_dword v14,v7,s[36:39],s61 offen offset:0 sc1
s_add_u32 s82,s82,1
s_waitcnt vmcnt(0)
v_cmp_eq_u32 vcc,s53,v14
s_nop 0
s_cmp_eq_u64 vcc,-1
s_cbranch_scc1 Lr_bqy0_%=
s_sleep 2
s_cmp_lt_u32 s82,20000
s_cbranch_scc1 Lr_bpy0_%=
Lr_bqy0_%=:
s_add_u32 m0,s65,0
s_nop 0
D 9,56,0
D 9,56,1024
D 9,56,2048
D 9,56,3072
s_waitcnt vmcnt(0)
Lr_b0_%=:
s_waitcnt vmcnt(10)
T v[68:69],10,0
T v[70:71],10,64
T v[72:73],10,1024
T v[74:75],10,1088
T v[76:77],10,2048
T v[78:79],10,2112
T v[80:81],10,3072
T v[82:83],10,3136
s_mov_b32 s45,s62
s_min_u32 s78,s64,s47
s_mul_i32 s56,s45,16384
s_add_u32 s56,s56,98566144
s_cmp_lt_u32 s45,s78
s_cselect_b32 s67,1,0
s_cselect_b32 s56,s56,0x7fff0000
s_add_u32 m0,s65,16384
s_nop 0
D 9,56,0
D 9,56,1024
D 9,56,2048
D 9,56,3072
L 8
v_pk_add_f32 v[52:53],v[52:53],v[54:55]
v_pk_add_f32 v[56:57],v[56:57],v[58:59]
v_pk_add_f32 v[60:61],v[60:61],v[62:63]
v_add_f32 v52,v52,v53
v_pk_add_f32 v[56:57],v[56:57],v[60:61]
v_mul_f32 v52,s68,v52
v_add_f32 v53,v56,v57
v_mul_f32 v53,s68,v53
v_fma_f32 v53,-v52,v52,v53
v_max_f32 v53,0,v53
L 6
v_lshlrev_b32 v84,16,v68
v_and_b32 v85,0xffff0000,v68
v_lshlrev_b32 v86,16,v69
v_and_b32 v87,0xffff0000,v69
v_lshlrev_b32 v88,16,v70
v_and_b32 v89,0xffff0000,v70
v_lshlrev_b32 v90,16,v71
v_and_b32 v91,0xffff0000,v71
L 4
v_lshlrev_b32 v92,16,v72
v_and_b32 v93,0xffff0000,v72
v_lshlrev_b32 v94,16,v73
v_and_b32 v95,0xffff0000,v73
v_lshlrev_b32 v96,16,v74
v_and_b32 v97,0xffff0000,v74
v_lshlrev_b32 v98,16,v75
v_and_b32 v99,0xffff0000,v75
L 2
v_lshlrev_b32 v100,16,v76
v_and_b32 v101,0xffff0000,v76
v_lshlrev_b32 v102,16,v77
v_and_b32 v103,0xffff0000,v77
v_lshlrev_b32 v104,16,v78
v_and_b32 v105,0xffff0000,v78
v_lshlrev_b32 v106,16,v79
v_and_b32 v107,0xffff0000,v79
L 0
v_lshlrev_b32 v108,16,v80
v_and_b32 v109,0xffff0000,v80
v_lshlrev_b32 v110,16,v81
v_and_b32 v111,0xffff0000,v81
v_lshlrev_b32 v112,16,v82
v_and_b32 v113,0xffff0000,v82
v_lshlrev_b32 v114,16,v83
v_and_b32 v115,0xffff0000,v83
v_add_f32 v53,s69,v53
v_rsq_f32 v48,v53
s_nop 0
v_mul_f32_e64 v50,-v52,v48
v_mov_b32 v49,v48
v_mov_b32 v51,v50
v_pk_fma_f32 v[52:53],v[116:117],v[48:49],v[50:51]
v_pk_fma_f32 v[54:55],v[118:119],v[48:49],v[50:51]
v_pk_fma_f32 v[56:57],v[120:121],v[48:49],v[50:51]
v_pk_fma_f32 v[58:59],v[122:123],v[48:49],v[50:51]
v_pk_fma_f32 v[116:117],v[52:53],v[84:85],v[116:117]
v_pk_fma_f32 v[118:119],v[54:55],v[86:87],v[118:119]
v_pk_fma_f32 v[120:121],v[56:57],v[88:89],v[120:121]
v_pk_fma_f32 v[122:123],v[58:59],v[90:91],v[122:123]
v_cvt_pk_bf16_f32 v148,v116,v117
v_cvt_pk_bf16_f32 v149,v118,v119
v_cvt_pk_bf16_f32 v150,v120,v121
v_cvt_pk_bf16_f32 v151,v122,v123
v_pk_fma_f32 v[52:53],v[124:125],v[48:49],v[50:51]
v_pk_fma_f32 v[54:55],v[126:127],v[48:49],v[50:51]
v_pk_fma_f32 v[56:57],v[128:129],v[48:49],v[50:51]
v_pk_fma_f32 v[58:59],v[130:131],v[48:49],v[50:51]
v_pk_fma_f32 v[124:125],v[52:53],v[92:93],v[124:125]
v_pk_fma_f32 v[126:127],v[54:55],v[94:95],v[126:127]
v_pk_fma_f32 v[128:129],v[56:57],v[96:97],v[128:129]
v_pk_fma_f32 v[130:131],v[58:59],v[98:99],v[130:131]
v_cvt_pk_bf16_f32 v152,v124,v125
v_cvt_pk_bf16_f32 v153,v126,v127
v_cvt_pk_bf16_f32 v154,v128,v129
v_cvt_pk_bf16_f32 v155,v130,v131
v_pk_fma_f32 v[52:53],v[132:133],v[48:49],v[50:51]
v_pk_fma_f32 v[54:55],v[134:135],v[48:49],v[50:51]
v_pk_fma_f32 v[56:57],v[136:137],v[48:49],v[50:51]
v_pk_fma_f32 v[58:59],v[138:139],v[48:49],v[50:51]
v_pk_fma_f32 v[132:133],v[52:53],v[100:101],v[132:133]
v_pk_fma_f32 v[134:135],v[54:55],v[102:103],v[134:135]
v_pk_fma_f32 v[136:137],v[56:57],v[104:105],v[136:137]
v_pk_fma_f32 v[138:139],v[58:59],v[106:107],v[138:139]
v_cvt_pk_bf16_f32 v156,v132,v133
v_cvt_pk_bf16_f32 v157,v134,v135
v_cvt_pk_bf16_f32 v158,v136,v137
v_cvt_pk_bf16_f32 v159,v138,v139
v_pk_fma_f32 v[52:53],v[140:141],v[48:49],v[50:51]
v_pk_fma_f32 v[54:55],v[142:143],v[48:49],v[50:51]
v_pk_fma_f32 v[56:57],v[144:145],v[48:49],v[50:51]
v_pk_fma_f32 v[58:59],v[146:147],v[48:49],v[50:51]
v_pk_fma_f32 v[140:141],v[52:53],v[108:109],v[140:141]
v_pk_fma_f32 v[142:143],v[54:55],v[110:111],v[142:143]
v_pk_fma_f32 v[144:145],v[56:57],v[112:113],v[144:145]
v_pk_fma_f32 v[146:147],v[58:59],v[114:115],v[146:147]
v_cvt_pk_bf16_f32 v160,v140,v141
v_cvt_pk_bf16_f32 v161,v142,v143
v_cvt_pk_bf16_f32 v162,v144,v145
v_cvt_pk_bf16_f32 v163,v146,v147
s_waitcnt vmcnt(8)
buffer_store_dword v13,v5,s[36:39],s63 offen offset:0 sc0 sc1
s_mov_b32 s63,0x7fff0000
v_mfma_f32_16x16x32_bf16 v[36:39],v[32:35],v[148:151],0
buffer_store_dwordx4 v[148:151],v1,s[36:39],s57 offen offset:0 sc0 sc1
v_mfma_f32_16x16x32_bf16 v[40:43],v[148:151],v[148:151],0
M 36,v[32:35],152
buffer_store_dwordx4 v[152:155],v1,s[36:39],s57 offen offset:2048 sc0 sc1
M 40,v[152:155],152
M 36,v[32:35],156
buffer_store_dwordx4 v[156:159],v1,s[36:39],s58 offen offset:0 sc0 sc1
M 40,v[156:159],156
M 36,v[32:35],160
buffer_store_dwordx4 v[160:163],v1,s[36:39],s58 offen offset:2048 sc0 sc1
M 40,v[160:163],160
s_nop 5
v_add_f32_dpp v44,v36,v36 row_ror:8 row_mask:0xf bank_mask:0xf
s_nop 0
v_cndmask_b32 v45,v40,v41,s[70:71]
v_cndmask_b32 v46,v42,v43,s[70:71]
ds_write_b32 v2,v44 offset:1024
v_cndmask_b32 v45,v45,v46,s[72:73]
ds_write_b32 v0,v45 offset:1024
L 0
s_barrier
R 52,3,1024
R 56,3,1040
R 60,3,1056
L 0
v_pk_add_f32 v[52:53],v[52:53],v[54:55]
v_pk_add_f32 v[56:57],v[56:57],v[58:59]
v_pk_add_f32 v[60:61],v[60:61],v[62:63]
v_add_f32 v52,v52,v53
v_pk_add_f32 v[56:57],v[56:57],v[60:61]
v_mul_f32 v52,s68,v52
v_add_f32 v53,v56,v57
v_mul_f32 v53,s68,v53
v_fma_f32 v53,-v52,v52,v53
v_max_f32 v53,0,v53
v_add_f32 v54,s69,v53
v_fma_f32 v55,s69,v54,v53
v_rsq_f32 v65,v54
v_rsq_f32 v54,v55
v_mul_f32_e64 v64,-v52,v65
v_mul_f32 v63,v55,v54
v_mul_f32 v61,s76,v54
v_mul_f32 v55,v54,v54
v_mul_f32 v62,s77,v55
v_mul_f32 v62,v62,v61
v_cndmask_b32 v60,v52,v64,s[74:75]
v_cndmask_b32 v61,v61,v65,s[74:75]
buffer_store_dwordx4 v[60:63],v4,s[36:39],s59 offen offset:0 sc0 sc1
s_mov_b32 s63,s60
s_cmp_lt_u32 s45,s47
s_cbranch_scc0 Lr_mid_%=
Lr_a1_%=:
s_waitcnt vmcnt(10)
v_mfma_f32_16x16x32_bf16 v[36:39],v[32:35],v[196:199],0
s_add_u32 s79,s45,s46
s_cmp_lt_u32 s45,64
s_cselect_b32 s78,s49,0
v_mfma_f32_16x16x32_bf16 v[40:43],v[196:199],v[196:199],0
s_cmp_ge_u32 s79,64
s_cselect_b32 s78,s78,0
s_add_u32 s62,s79,s78
M 36,v[32:35],200
s_lshr_b32 s78,s45,1
s_and_b32 s79,s45,1
s_lshl_b32 s79,s79,7
M 40,v[200:203],200
s_lshl_b32 s57,s78,15
s_lshl_b32 s55,s62,14
s_cmp_lt_u32 s62,s47
M 36,v[32:35],204
s_cselect_b32 s55,s55,0x7fff0000
buffer_load_dwordx4 v[164:167],v8,s[84:87],s55 offen offset:0 nt
M 40,v[204:207],204
s_add_u32 s57,s57,s79
buffer_load_dwordx4 v[168:171],v8,s[84:87],s55 offen offset:128 nt
M 36,v[32:35],208
s_lshl_b32 s59,s78,9
buffer_load_dwordx4 v[172:175],v8,s[84:87],s55 offen offset:256 nt
M 40,v[208:211],208
s_add_u32 s57,s57,8388608
buffer_load_dwordx4 v[176:179],v8,s[84:87],s55 offen offset:384 nt
v_mfma_f32_16x16x32_bf16 v[116:119],v[26:29],v[196:199],0
s_add_u32 s59,s59,s79
s_lshl_b32 s60,s45,9
s_add_u32 s58,s57,4096
v_mfma_f32_16x16x32_bf16 v[120:123],v[24:27],v[196:199],0
s_add_u32 s59,s59,106954752
s_add_u32 s60,s60,s66
v_mfma_f32_16x16x32_bf16 v[124:127],v[26:29],v[200:203],0
v_mfma_f32_16x16x32_bf16 v[128:131],v[24:27],v[200:203],0
v_add_f32_dpp v44,v36,v36 row_ror:8 row_mask:0xf bank_mask:0xf
v_cndmask_b32 v45,v40,v41,s[70:71]
v_mfma_f32_16x16x32_bf16 v[132:135],v[26:29],v[204:207],0
ds_write_b32 v2,v44 offset:0
v_mfma_f32_16x16x32_bf16 v[136:139],v[24:27],v[204:207],0
v_cndmask_b32 v46,v42,v43,s[70:71]
v_cndmask_b32 v45,v45,v46,s[72:73]
v_mfma_f32_16x16x32_bf16 v[140:143],v[26:29],v[208:211],0
ds_write_b32 v0,v45 offset:0
v_mfma_f32_16x16x32_bf16 v[144:147],v[24:27],v[208:211],0
L 0
s_barrier
R 52,3,0
R 56,3,16
R 60,3,32
s_cmp_lg_u32 s67,0
s_cbranch_scc1 Lr_b1_%=
s_waitcnt vmcnt(4)
buffer_store_dword v13,v5,s[36:39],s63 offen offset:0 sc0 sc1
s_mov_b32 s63,0x7fff0000
s_cmp_lt_u32 s45,64
s_cselect_b32 s64,-192,0
s_add_u32 s64,s64,256
s_cmp_lt_u32 s45,64
s_cselect_b32 s61,0,32768
s_add_u32 s61,s61,111177728
s_mul_i32 s56,s45,16384
s_add_u32 s56,s56,98566144
Lr_bpy1_%=:
buffer_load_dword v14,v7,s[36:39],s61 offen offset:0 sc1
s_add_u32 s82,s82,1
s_waitcnt vmcnt(0)
v_cmp_eq_u32 vcc,s53,v14
s_nop 0
s_cmp_eq_u64 vcc,-1
s_cbranch_scc1 Lr_bqy1_%=
s_sleep 2
s_cmp_lt_u32 s82,20000
s_cbranch_scc1 Lr_bpy1_%=
Lr_bqy1_%=:
s_add_u32 m0,s65,16384
s_nop 0
D 9,56,0
D 9,56,1024
D 9,56,2048
D 9,56,3072
s_waitcnt vmcnt(0)
Lr_b1_%=:
s_waitcnt vmcnt(10)
T v[68:69],10,16384
T v[70:71],10,16448
T v[72:73],10,17408
T v[74:75],10,17472
T v[76:77],10,18432
T v[78:79],10,18496
T v[80:81],10,19456
T v[82:83],10,19520
s_mov_b32 s45,s62
s_min_u32 s78,s64,s47
s_mul_i32 s56,s45,16384
s_add_u32 s56,s56,98566144
s_cmp_lt_u32 s45,s78
s_cselect_b32 s67,1,0
s_cselect_b32 s56,s56,0x7fff0000
s_add_u32 m0,s65,0
s_nop 0
D 9,56,0
D 9,56,1024
D 9,56,2048
D 9,56,3072
L 8
v_pk_add_f32 v[52:53],v[52:53],v[54:55]
v_pk_add_f32 v[56:57],v[56:57],v[58:59]
v_pk_add_f32 v[60:61],v[60:61],v[62:63]
v_add_f32 v52,v52,v53
v_pk_add_f32 v[56:57],v[56:57],v[60:61]
v_mul_f32 v52,s68,v52
v_add_f32 v53,v56,v57
v_mul_f32 v53,s68,v53
v_fma_f32 v53,-v52,v52,v53
v_max_f32 v53,0,v53
L 6
v_lshlrev_b32 v84,16,v68
v_and_b32 v85,0xffff0000,v68
v_lshlrev_b32 v86,16,v69
v_and_b32 v87,0xffff0000,v69
v_lshlrev_b32 v88,16,v70
v_and_b32 v89,0xffff0000,v70
v_lshlrev_b32 v90,16,v71
v_and_b32 v91,0xffff0000,v71
L 4
v_lshlrev_b32 v92,16,v72
v_and_b32 v93,0xffff0000,v72
v_lshlrev_b32 v94,16,v73
v_and_b32 v95,0xffff0000,v73
v_lshlrev_b32 v96,16,v74
v_and_b32 v97,0xffff0000,v74
v_lshlrev_b32 v98,16,v75
v_and_b32 v99,0xffff0000,v75
L 2
v_lshlrev_b32 v100,16,v76
v_and_b32 v101,0xffff0000,v76
v_lshlrev_b32 v102,16,v77
v_and_b32 v103,0xffff0000,v77
v_lshlrev_b32 v104,16,v78
v_and_b32 v105,0xffff0000,v78
v_lshlrev_b32 v106,16,v79
v_and_b32 v107,0xffff0000,v79
L 0
v_lshlrev_b32 v108,16,v80
v_and_b32 v109,0xffff0000,v80
v_lshlrev_b32 v110,16,v81
v_and_b32 v111,0xffff0000,v81
v_lshlrev_b32 v112,16,v82
v_and_b32 v113,0xffff0000,v82
)"
R"(v_lshlrev_b32 v114,16,v83
v_and_b32 v115,0xffff0000,v83
v_add_f32 v53,s69,v53
v_rsq_f32 v48,v53
s_nop 0
v_mul_f32_e64 v50,-v52,v48
v_mov_b32 v49,v48
v_mov_b32 v51,v50
v_pk_fma_f32 v[52:53],v[116:117],v[48:49],v[50:51]
v_pk_fma_f32 v[54:55],v[118:119],v[48:49],v[50:51]
v_pk_fma_f32 v[56:57],v[120:121],v[48:49],v[50:51]
v_pk_fma_f32 v[58:59],v[122:123],v[48:49],v[50:51]
v_pk_fma_f32 v[116:117],v[52:53],v[84:85],v[116:117]
v_pk_fma_f32 v[118:119],v[54:55],v[86:87],v[118:119]
v_pk_fma_f32 v[120:121],v[56:57],v[88:89],v[120:121]
v_pk_fma_f32 v[122:123],v[58:59],v[90:91],v[122:123]
v_cvt_pk_bf16_f32 v148,v116,v117
v_cvt_pk_bf16_f32 v149,v118,v119
v_cvt_pk_bf16_f32 v150,v120,v121
v_cvt_pk_bf16_f32 v151,v122,v123
v_pk_fma_f32 v[52:53],v[124:125],v[48:49],v[50:51]
v_pk_fma_f32 v[54:55],v[126:127],v[48:49],v[50:51]
v_pk_fma_f32 v[56:57],v[128:129],v[48:49],v[50:51]
v_pk_fma_f32 v[58:59],v[130:131],v[48:49],v[50:51]
v_pk_fma_f32 v[124:125],v[52:53],v[92:93],v[124:125]
v_pk_fma_f32 v[126:127],v[54:55],v[94:95],v[126:127]
v_pk_fma_f32 v[128:129],v[56:57],v[96:97],v[128:129]
v_pk_fma_f32 v[130:131],v[58:59],v[98:99],v[130:131]
v_cvt_pk_bf16_f32 v152,v124,v125
v_cvt_pk_bf16_f32 v153,v126,v127
v_cvt_pk_bf16_f32 v154,v128,v129
v_cvt_pk_bf16_f32 v155,v130,v131
v_pk_fma_f32 v[52:53],v[132:133],v[48:49],v[50:51]
v_pk_fma_f32 v[54:55],v[134:135],v[48:49],v[50:51]
v_pk_fma_f32 v[56:57],v[136:137],v[48:49],v[50:51]
v_pk_fma_f32 v[58:59],v[138:139],v[48:49],v[50:51]
v_pk_fma_f32 v[132:133],v[52:53],v[100:101],v[132:133]
v_pk_fma_f32 v[134:135],v[54:55],v[102:103],v[134:135]
v_pk_fma_f32 v[136:137],v[56:57],v[104:105],v[136:137]
v_pk_fma_f32 v[138:139],v[58:59],v[106:107],v[138:139]
v_cvt_pk_bf16_f32 v156,v132,v133
v_cvt_pk_bf16_f32 v157,v134,v135
v_cvt_pk_bf16_f32 v158,v136,v137
v_cvt_pk_bf16_f32 v159,v138,v139
v_pk_fma_f32 v[52:53],v[140:141],v[48:49],v[50:51]
v_pk_fma_f32 v[54:55],v[142:143],v[48:49],v[50:51]
v_pk_fma_f32 v[56:57],v[144:145],v[48:49],v[50:51]
v_pk_fma_f32 v[58:59],v[146:147],v[48:49],v[50:51]
v_pk_fma_f32 v[140:141],v[52:53],v[108:109],v[140:141]
v_pk_fma_f32 v[142:143],v[54:55],v[110:111],v[142:143]
v_pk_fma_f32 v[144:145],v[56:57],v[112:113],v[144:145]
v_pk_fma_f32 v[146:147],v[58:59],v[114:115],v[146:147]
v_cvt_pk_bf16_f32 v160,v140,v141
v_cvt_pk_bf16_f32 v161,v142,v143
v_cvt_pk_bf16_f32 v162,v144,v145
v_cvt_pk_bf16_f32 v163,v146,v147
s_waitcnt vmcnt(8)
buffer_store_dword v13,v5,s[36:39],s63 offen offset:0 sc0 sc1
s_mov_b32 s63,0x7fff0000
v_mfma_f32_16x16x32_bf16 v[36:39],v[32:35],v[148:151],0
buffer_store_dwordx4 v[148:151],v1,s[36:39],s57 offen offset:0 sc0 sc1
v_mfma_f32_16x16x32_bf16 v[40:43],v[148:151],v[148:151],0
M 36,v[32:35],152
buffer_store_dwordx4 v[152:155],v1,s[36:39],s57 offen offset:2048 sc0 sc1
M 40,v[152:155],152
M 36,v[32:35],156
buffer_store_dwordx4 v[156:159],v1,s[36:39],s58 offen offset:0 sc0 sc1
M 40,v[156:159],156
M 36,v[32:35],160
buffer_store_dwordx4 v[160:163],v1,s[36:39],s58 offen offset:2048 sc0 sc1
M 40,v[160:163],160
s_nop 5
v_add_f32_dpp v44,v36,v36 row_ror:8 row_mask:0xf bank_mask:0xf
s_nop 0
v_cndmask_b32 v45,v40,v41,s[70:71]
v_cndmask_b32 v46,v42,v43,s[70:71]
ds_write_b32 v2,v44 offset:1024
v_cndmask_b32 v45,v45,v46,s[72:73]
ds_write_b32 v0,v45 offset:1024
L 0
s_barrier
R 52,3,1024
R 56,3,1040
R 60,3,1056
L 0
v_pk_add_f32 v[52:53],v[52:53],v[54:55]
v_pk_add_f32 v[56:57],v[56:57],v[58:59]
v_pk_add_f32 v[60:61],v[60:61],v[62:63]
v_add_f32 v52,v52,v53
v_pk_add_f32 v[56:57],v[56:57],v[60:61]
v_mul_f32 v52,s68,v52
v_add_f32 v53,v56,v57
v_mul_f32 v53,s68,v53
v_fma_f32 v53,-v52,v52,v53
v_max_f32 v53,0,v53
v_add_f32 v54,s69,v53
v_fma_f32 v55,s69,v54,v53
v_rsq_f32 v65,v54
v_rsq_f32 v54,v55
v_mul_f32_e64 v64,-v52,v65
v_mul_f32 v63,v55,v54
v_mul_f32 v61,s76,v54
v_mul_f32 v55,v54,v54
v_mul_f32 v62,s77,v55
v_mul_f32 v62,v62,v61
v_cndmask_b32 v60,v52,v64,s[74:75]
v_cndmask_b32 v61,v61,v65,s[74:75]
buffer_store_dwordx4 v[60:63],v4,s[36:39],s59 offen offset:0 sc0 sc1
s_mov_b32 s63,s60
s_cmp_lt_u32 s45,s47
s_cbranch_scc1 Lr_a0_%=
Lr_mid_%=:
s_waitcnt vmcnt(0)
buffer_store_dword v13,v5,s[36:39],s63 offen offset:0 sc0 sc1
s_mov_b32 s63,0x7fff0000
s_cmp_eq_u32 s51,0
s_cbranch_scc1 Lr_done_%=
s_mov_b32 s45,s93
s_lshr_b32 s78,s45,1
s_and_b32 s79,s45,1
s_lshl_b32 s79,s79,7
s_lshl_b32 s55,s78,15
s_add_u32 s55,s55,s79
s_mul_i32 s56,s45,16384
s_add_u32 s56,s56,98566144
s_lshl_b32 s61,s78,11
s_add_u32 s61,s61,110587904
s_add_u32 s78,s45,s46
s_lshr_b32 s78,s78,1
s_lshl_b32 s62,s78,11
s_add_u32 s62,s62,110587904
Lr_bps_%=:
buffer_load_dword v14,v6,s[36:39],s61 offen offset:0 sc1
s_add_u32 s82,s82,1
s_waitcnt vmcnt(0)
v_cmp_eq_u32 vcc,s53,v14
s_nop 0
s_cmp_eq_u64 vcc,-1
s_cbranch_scc1 Lr_bqs_%=
s_sleep 2
s_cmp_lt_u32 s82,20000
s_cbranch_scc1 Lr_bps_%=
Lr_bqs_%=:
s_add_u32 s78,s55,71303168
buffer_load_dwordx4 v[164:167],v1,s[36:39],s78 offen offset:0 sc1
buffer_load_dwordx4 v[168:171],v1,s[36:39],s78 offen offset:2048 sc1
s_add_u32 s78,s55,71307264
buffer_load_dwordx4 v[172:175],v1,s[36:39],s78 offen offset:0 sc1
buffer_load_dwordx4 v[176:179],v1,s[36:39],s78 offen offset:2048 sc1
s_add_u32 s78,s55,89128960
buffer_load_dwordx4 v[180:183],v1,s[36:39],s78 offen offset:0 sc1
buffer_load_dwordx4 v[184:187],v1,s[36:39],s78 offen offset:2048 sc1
s_add_u32 s78,s55,89133056
buffer_load_dwordx4 v[188:191],v1,s[36:39],s78 offen offset:0 sc1
buffer_load_dwordx4 v[192:195],v1,s[36:39],s78 offen offset:2048 sc1
s_add_u32 m0,s65,0
s_nop 0
D 9,56,0
D 9,56,1024
D 9,56,2048
D 9,56,3072
buffer_load_dword v14,v6,s[36:39],s62 offen offset:0 sc1
s_lshr_b32 s78,s45,1
s_and_b32 s79,s45,1
s_lshl_b32 s79,s79,7
s_lshl_b32 s57,s78,15
s_add_u32 s57,s57,s79
s_add_u32 s57,s57,12582912
s_add_u32 s58,s57,4096
s_lshl_b32 s59,s78,9
s_add_u32 s59,s59,s79
s_add_u32 s59,s59,107020288
s_lshl_b32 s60,s45,9
s_add_u32 s60,s60,s66
s_add_u32 s60,s60,131072
s_add_u32 s45,s45,s46
s_lshr_b32 s78,s45,1
s_and_b32 s79,s45,1
s_lshl_b32 s79,s79,7
s_lshl_b32 s55,s78,15
s_add_u32 s55,s55,s79
s_mul_i32 s56,s45,16384
s_add_u32 s56,s56,98566144
s_lshl_b32 s61,s78,11
s_add_u32 s61,s61,110587904
s_add_u32 s78,s45,s46
s_lshr_b32 s78,s78,1
s_lshl_b32 s62,s78,11
s_add_u32 s62,s62,110587904
s_waitcnt vmcnt(0)
Lr_it0_%=:
T v[68:69],10,0
T v[70:71],10,64
T v[72:73],10,1024
T v[74:75],10,1088
T v[76:77],10,2048
T v[78:79],10,2112
T v[80:81],10,3072
T v[82:83],10,3136
s_nop 0
v_cmp_eq_u32 vcc,s53,v14
s_nop 0
s_cmp_eq_u64 vcc,-1
s_cselect_b32 s78,s45,256
s_cmp_lt_u32 s78,256
s_cbranch_scc1 Lr_ta0_%=
buffer_load_dword v14,v6,s[36:39],s61 offen offset:0 sc1
v_mfma_f32_16x16x32_bf16 v[116:119],v[26:29],v[164:167],0
L 6
v_lshlrev_b32 v84,16,v68
v_and_b32 v85,0xffff0000,v68
v_mfma_f32_16x16x32_bf16 v[120:123],v[24:27],v[164:167],0
L 4
v_lshlrev_b32 v86,16,v69
v_and_b32 v87,0xffff0000,v69
M 116,v[26:29],180
L 2
v_lshlrev_b32 v88,16,v70
v_and_b32 v89,0xffff0000,v70
M 120,v[24:27],180
L 0
v_lshlrev_b32 v90,16,v71
v_and_b32 v91,0xffff0000,v71
v_mfma_f32_16x16x32_bf16 v[124:127],v[26:29],v[168:171],0
v_lshlrev_b32 v92,16,v72
v_and_b32 v93,0xffff0000,v72
v_mfma_f32_16x16x32_bf16 v[128:131],v[24:27],v[168:171],0
v_lshlrev_b32 v94,16,v73
v_and_b32 v95,0xffff0000,v73
M 124,v[26:29],184
v_cvt_pk_bf16_f32 v148,v116,v117
v_cvt_pk_bf16_f32 v149,v118,v119
M 128,v[24:27],184
v_cvt_pk_bf16_f32 v150,v120,v121
v_cvt_pk_bf16_f32 v151,v122,v123
v_mfma_f32_16x16x32_bf16 v[132:135],v[26:29],v[172:175],0
v_lshlrev_b32 v96,16,v74
v_and_b32 v97,0xffff0000,v74
v_mfma_f32_16x16x32_bf16 v[136:139],v[24:27],v[172:175],0
v_lshlrev_b32 v98,16,v75
v_and_b32 v99,0xffff0000,v75
M 132,v[26:29],188
v_cvt_pk_bf16_f32 v152,v124,v125
v_cvt_pk_bf16_f32 v153,v126,v127
M 136,v[24:27],188
v_cvt_pk_bf16_f32 v154,v128,v129
v_cvt_pk_bf16_f32 v155,v130,v131
v_mfma_f32_16x16x32_bf16 v[140:143],v[26:29],v[176:179],0
v_lshlrev_b32 v100,16,v76
v_and_b32 v101,0xffff0000,v76
v_mfma_f32_16x16x32_bf16 v[144:147],v[24:27],v[176:179],0
v_lshlrev_b32 v102,16,v77
v_and_b32 v103,0xffff0000,v77
M 140,v[26:29],192
v_cvt_pk_bf16_f32 v156,v132,v133
v_cvt_pk_bf16_f32 v157,v134,v135
M 144,v[24:27],192
v_cvt_pk_bf16_f32 v158,v136,v137
v_cvt_pk_bf16_f32 v159,v138,v139
v_mfma_f32_16x16x32_bf16 v[36:39],v[32:35],v[148:151],0
v_lshlrev_b32 v104,16,v78
v_and_b32 v105,0xffff0000,v78
v_mfma_f32_16x16x32_bf16 v[40:43],v[148:151],v[148:151],0
v_lshlrev_b32 v106,16,v79
v_and_b32 v107,0xffff0000,v79
M 36,v[32:35],152
v_cvt_pk_bf16_f32 v160,v140,v141
v_cvt_pk_bf16_f32 v161,v142,v143
M 40,v[152:155],152
v_cvt_pk_bf16_f32 v162,v144,v145
v_cvt_pk_bf16_f32 v163,v146,v147
M 36,v[32:35],156
v_lshlrev_b32 v108,16,v80
v_and_b32 v109,0xffff0000,v80
M 40,v[156:159],156
v_lshlrev_b32 v110,16,v81
v_and_b32 v111,0xffff0000,v81
M 36,v[32:35],160
v_lshlrev_b32 v112,16,v82
v_and_b32 v113,0xffff0000,v82
M 40,v[160:163],160
v_lshlrev_b32 v114,16,v83
v_and_b32 v115,0xffff0000,v83
s_nop 2
v_add_f32_dpp v44,v36,v36 row_ror:8 row_mask:0xf bank_mask:0xf
s_nop 1
v_cndmask_b32 v45,v40,v41,s[70:71]
v_cndmask_b32 v46,v42,v43,s[70:71]
ds_write_b32 v2,v44 offset:0
v_cndmask_b32 v45,v45,v46,s[72:73]
ds_write_b32 v0,v45 offset:0
L 0
s_barrier
R 52,3,0
R 56,3,16
R 60,3,32
L 0
v_pk_add_f32 v[52:53],v[52:53],v[54:55]
v_pk_add_f32 v[56:57],v[56:57],v[58:59]
v_pk_add_f32 v[60:61],v[60:61],v[62:63]
v_add_f32 v52,v52,v53
v_pk_add_f32 v[56:57],v[56:57],v[60:61]
v_mul_f32 v52,s68,v52
v_add_f32 v53,v56,v57
v_mul_f32 v53,s68,v53
v_fma_f32 v53,-v52,v52,v53
v_max_f32 v53,0,v53
s_waitcnt vmcnt(1)
buffer_store_dword v13,v5,s[36:39],s63 offen offset:0 sc0 sc1
s_mov_b32 s63,0x7fff0000
s_waitcnt vmcnt(1)
v_cmp_eq_u32 vcc,s53,v14
s_nop 0
s_cmp_eq_u64 vcc,-1
s_cselect_b32 s78,s45,256
s_cmp_lt_u32 s78,256
s_cbranch_scc1 Lr_tb0_%=
buffer_load_dword v14,v6,s[36:39],s61 offen offset:0 sc1
v_add_f32 v53,s69,v53
v_rsq_f32 v48,v53
s_nop 0
v_mul_f32_e64 v50,-v52,v48
v_mov_b32 v49,v48
v_mov_b32 v51,v50
v_pk_fma_f32 v[52:53],v[116:117],v[48:49],v[50:51]
v_pk_fma_f32 v[54:55],v[118:119],v[48:49],v[50:51]
v_pk_fma_f32 v[56:57],v[120:121],v[48:49],v[50:51]
v_pk_fma_f32 v[58:59],v[122:123],v[48:49],v[50:51]
v_pk_fma_f32 v[116:117],v[52:53],v[84:85],v[116:117]
v_pk_fma_f32 v[118:119],v[54:55],v[86:87],v[118:119]
v_pk_fma_f32 v[120:121],v[56:57],v[88:89],v[120:121]
v_pk_fma_f32 v[122:123],v[58:59],v[90:91],v[122:123]
v_cvt_pk_bf16_f32 v148,v116,v117
v_cvt_pk_bf16_f32 v149,v118,v119
v_cvt_pk_bf16_f32 v150,v120,v121
v_cvt_pk_bf16_f32 v151,v122,v123
v_pk_fma_f32 v[52:53],v[124:125],v[48:49],v[50:51]
v_pk_fma_f32 v[54:55],v[126:127],v[48:49],v[50:51]
v_pk_fma_f32 v[56:57],v[128:129],v[48:49],v[50:51]
v_pk_fma_f32 v[58:59],v[130:131],v[48:49],v[50:51]
v_pk_fma_f32 v[124:125],v[52:53],v[92:93],v[124:125]
v_pk_fma_f32 v[126:127],v[54:55],v[94:95],v[126:127]
v_pk_fma_f32 v[128:129],v[56:57],v[96:97],v[128:129]
v_pk_fma_f32 v[130:131],v[58:59],v[98:99],v[130:131]
v_cvt_pk_bf16_f32 v152,v124,v125
v_cvt_pk_bf16_f32 v153,v126,v127
v_cvt_pk_bf16_f32 v154,v128,v129
v_cvt_pk_bf16_f32 v155,v130,v131
v_pk_fma_f32 v[52:53],v[132:133],v[48:49],v[50:51]
v_pk_fma_f32 v[54:55],v[134:135],v[48:49],v[50:51]
v_pk_fma_f32 v[56:57],v[136:137],v[48:49],v[50:51]
v_pk_fma_f32 v[58:59],v[138:139],v[48:49],v[50:51]
v_pk_fma_f32 v[132:133],v[52:53],v[100:101],v[132:133]
v_pk_fma_f32 v[134:135],v[54:55],v[102:103],v[134:135]
v_pk_fma_f32 v[136:137],v[56:57],v[104:105],v[136:137]
v_pk_fma_f32 v[138:139],v[58:59],v[106:107],v[138:139]
v_cvt_pk_bf16_f32 v156,v132,v133
v_cvt_pk_bf16_f32 v157,v134,v135
v_cvt_pk_bf16_f32 v158,v136,v137
v_cvt_pk_bf16_f32 v159,v138,v139
v_pk_fma_f32 v[52:53],v[140:141],v[48:49],v[50:51]
v_pk_fma_f32 v[54:55],v[142:143],v[48:49],v[50:51]
v_pk_fma_f32 v[56:57],v[144:145],v[48:49],v[50:51]
v_pk_fma_f32 v[58:59],v[146:147],v[48:49],v[50:51]
v_pk_fma_f32 v[140:141],v[52:53],v[108:109],v[140:141]
v_pk_fma_f32 v[142:143],v[54:55],v[110:111],v[142:143]
v_pk_fma_f32 v[144:145],v[56:57],v[112:113],v[144:145]
v_pk_fma_f32 v[146:147],v[58:59],v[114:115],v[146:147]
v_cvt_pk_bf16_f32 v160,v140,v141
v_cvt_pk_bf16_f32 v161,v142,v143
v_cvt_pk_bf16_f32 v162,v144,v145
v_cvt_pk_bf16_f32 v163,v146,v147
v_mfma_f32_16x16x32_bf16 v[36:39],v[32:35],v[148:151],0
buffer_store_dwordx4 v[148:151],v1,s[36:39],s57 offen offset:0 sc0 sc1
v_mfma_f32_16x16x32_bf16 v[40:43],v[148:151],v[148:151],0
M 36,v[32:35],152
buffer_store_dwordx4 v[152:155],v1,s[36:39],s57 offen offset:2048 sc0 sc1
M 40,v[152:155],152
M 36,v[32:35],156
buffer_store_dwordx4 v[156:159],v1,s[36:39],s58 offen offset:0 sc0 sc1
M 40,v[156:159],156
M 36,v[32:35],160
buffer_store_dwordx4 v[160:163],v1,s[36:39],s58 offen offset:2048 sc0 sc1
M 40,v[160:163],160
s_nop 5
v_add_f32_dpp v44,v36,v36 row_ror:8 row_mask:0xf bank_mask:0xf
s_nop 0
v_cndmask_b32 v45,v40,v41,s[70:71]
v_cndmask_b32 v46,v42,v43,s[70:71]
ds_write_b32 v2,v44 offset:1024
v_cndmask_b32 v45,v45,v46,s[72:73]
ds_write_b32 v0,v45 offset:1024
L 0
s_barrier
R 52,3,1024
R 56,3,1040
R 60,3,1056
L 0
v_pk_add_f32 v[52:53],v[52:53],v[54:55]
v_pk_add_f32 v[56:57],v[56:57],v[58:59]
v_pk_add_f32 v[60:61],v[60:61],v[62:63]
v_add_f32 v52,v52,v53
v_pk_add_f32 v[56:57],v[56:57],v[60:61]
v_mul_f32 v52,s68,v52
v_add_f32 v53,v56,v57
v_mul_f32 v53,s68,v53
v_fma_f32 v53,-v52,v52,v53
v_max_f32 v53,0,v53
v_add_f32 v54,s69,v53
v_fma_f32 v55,s69,v54,v53
v_rsq_f32 v65,v54
v_rsq_f32 v54,v55
v_mul_f32_e64 v64,-v52,v65
v_mul_f32 v63,v55,v54
v_mul_f32 v61,s76,v54
v_mul_f32 v55,v54,v54
v_mul_f32 v62,s77,v55
v_mul_f32 v62,v62,v61
v_cndmask_b32 v60,v52,v64,s[74:75]
v_cndmask_b32 v61,v61,v65,s[74:75]
buffer_store_dwordx4 v[60:63],v4,s[36:39],s59 offen offset:0 sc0 sc1
s_mov_b32 s63,s60
s_waitcnt vmcnt(5)
v_cmp_eq_u32 vcc,s53,v14
s_nop 0
s_cmp_eq_u64 vcc,-1
s_cselect_b32 s78,s45,256
s_cmp_lt_u32 s78,256
s_cbranch_scc1 Lr_tc0_%=
s_waitcnt vmcnt(0)
buffer_store_dword v13,v5,s[36:39],s63 offen offset:0 sc0 sc1
s_mov_b32 s63,0x7fff0000
s_cmp_ge_u32 s45,256
s_cbranch_scc1 Lr_done_%=
Lr_bp0_%=:
buffer_load_dword v14,v6,s[36:39],s61 offen offset:0 sc1
s_add_u32 s82,s82,1
s_waitcnt vmcnt(0)
v_cmp_eq_u32 vcc,s53,v14
s_nop 0
s_cmp_eq_u64 vcc,-1
s_cbranch_scc1 Lr_bq0_%=
s_sleep 2
s_cmp_lt_u32 s82,20000
s_cbranch_scc1 Lr_bp0_%=
Lr_bq0_%=:
s_add_u32 s78,s55,71303168
buffer_load_dwordx4 v[196:199],v1,s[36:39],s78 offen offset:0 sc1
buffer_load_dwordx4 v[200:203],v1,s[36:39],s78 offen offset:2048 sc1
s_add_u32 s78,s55,71307264
buffer_load_dwordx4 v[204:207],v1,s[36:39],s78 offen offset:0 sc1
buffer_load_dwordx4 v[208:211],v1,s[36:39],s78 offen offset:2048 sc1
s_add_u32 s78,s55,89128960
buffer_load_dwordx4 v[212:215],v1,s[36:39],s78 offen offset:0 sc1
buffer_load_dwordx4 v[216:219],v1,s[36:39],s78 offen offset:2048 sc1
s_add_u32 s78,s55,89133056
buffer_load_dwordx4 v[220:223],v1,s[36:39],s78 offen offset:0 sc1
buffer_load_dwordx4 v[224:227],v1,s[36:39],s78 offen offset:2048 sc1
s_add_u32 m0,s65,16384
s_nop 0
D 9,56,0
D 9,56,1024
D 9,56,2048
D 9,56,3072
buffer_load_dword v14,v6,s[36:39],s62 offen offset:0 sc1
s_lshr_b32 s78,s45,1
s_and_b32 s79,s45,1
s_lshl_b32 s79,s79,7
s_lshl_b32 s57,s78,15
s_add_u32 s57,s57,s79
s_add_u32 s57,s57,12582912
s_add_u32 s58,s57,4096
s_lshl_b32 s59,s78,9
s_add_u32 s59,s59,s79
s_add_u32 s59,s59,107020288
s_lshl_b32 s60,s45,9
s_add_u32 s60,s60,s66
s_add_u32 s60,s60,131072
s_add_u32 s45,s45,s46
s_lshr_b32 s78,s45,1
s_and_b32 s79,s45,1
s_lshl_b32 s79,s79,7
s_lshl_b32 s55,s78,15
s_add_u32 s55,s55,s79
s_mul_i32 s56,s45,16384
s_add_u32 s56,s56,98566144
s_lshl_b32 s61,s78,11
s_add_u32 s61,s61,110587904
s_add_u32 s78,s45,s46
s_lshr_b32 s78,s78,1
s_lshl_b32 s62,s78,11
s_add_u32 s62,s62,110587904
s_waitcnt vmcnt(0)
s_branch Lr_it1_%=
Lr_ta0_%=:
v_mfma_f32_16x16x32_bf16 v[116:119],v[26:29],v[164:167],0
s_add_u32 s78,s55,71303168
buffer_load_dwordx4 v[196:199],v1,s[36:39],s78 offen offset:0 sc1
v_mfma_f32_16x16x32_bf16 v[120:123],v[24:27],v[164:167],0
L 6
)"
R"(buffer_load_dwordx4 v[200:203],v1,s[36:39],s78 offen offset:2048 sc1
M 116,v[26:29],180
s_add_u32 s78,s55,71307264
buffer_load_dwordx4 v[204:207],v1,s[36:39],s78 offen offset:0 sc1
M 120,v[24:27],180
L 4
buffer_load_dwordx4 v[208:211],v1,s[36:39],s78 offen offset:2048 sc1
v_mfma_f32_16x16x32_bf16 v[124:127],v[26:29],v[168:171],0
s_add_u32 s78,s55,89128960
buffer_load_dwordx4 v[212:215],v1,s[36:39],s78 offen offset:0 sc1
v_mfma_f32_16x16x32_bf16 v[128:131],v[24:27],v[168:171],0
L 2
buffer_load_dwordx4 v[216:219],v1,s[36:39],s78 offen offset:2048 sc1
M 124,v[26:29],184
L 0
v_cvt_pk_bf16_f32 v148,v116,v117
v_cvt_pk_bf16_f32 v149,v118,v119
M 128,v[24:27],184
s_add_u32 s78,s55,89133056
buffer_load_dwordx4 v[220:223],v1,s[36:39],s78 offen offset:0 sc1
v_mfma_f32_16x16x32_bf16 v[132:135],v[26:29],v[172:175],0
s_add_u32 m0,s65,16384
buffer_load_dwordx4 v[224:227],v1,s[36:39],s78 offen offset:2048 sc1
v_mfma_f32_16x16x32_bf16 v[136:139],v[24:27],v[172:175],0
D 9,56,0
M 132,v[26:29],188
D 9,56,1024
M 136,v[24:27],188
D 9,56,2048
v_mfma_f32_16x16x32_bf16 v[140:143],v[26:29],v[176:179],0
D 9,56,3072
v_mfma_f32_16x16x32_bf16 v[144:147],v[24:27],v[176:179],0
v_cvt_pk_bf16_f32 v150,v120,v121
v_cvt_pk_bf16_f32 v151,v122,v123
M 140,v[26:29],192
v_cvt_pk_bf16_f32 v152,v124,v125
v_cvt_pk_bf16_f32 v153,v126,v127
M 144,v[24:27],192
v_cvt_pk_bf16_f32 v154,v128,v129
v_cvt_pk_bf16_f32 v155,v130,v131
v_mfma_f32_16x16x32_bf16 v[36:39],v[32:35],v[148:151],0
v_cvt_pk_bf16_f32 v156,v132,v133
v_cvt_pk_bf16_f32 v157,v134,v135
v_mfma_f32_16x16x32_bf16 v[40:43],v[148:151],v[148:151],0
v_cvt_pk_bf16_f32 v158,v136,v137
v_cvt_pk_bf16_f32 v159,v138,v139
M 36,v[32:35],152
v_cvt_pk_bf16_f32 v160,v140,v141
v_cvt_pk_bf16_f32 v161,v142,v143
M 40,v[152:155],152
v_cvt_pk_bf16_f32 v162,v144,v145
v_cvt_pk_bf16_f32 v163,v146,v147
M 36,v[32:35],156
v_lshlrev_b32 v84,16,v68
v_and_b32 v85,0xffff0000,v68
M 40,v[156:159],156
v_lshlrev_b32 v86,16,v69
v_and_b32 v87,0xffff0000,v69
M 36,v[32:35],160
v_lshlrev_b32 v88,16,v70
v_and_b32 v89,0xffff0000,v70
M 40,v[160:163],160
v_lshlrev_b32 v90,16,v71
v_and_b32 v91,0xffff0000,v71
v_lshlrev_b32 v92,16,v72
v_and_b32 v93,0xffff0000,v72
v_lshlrev_b32 v94,16,v73
v_and_b32 v95,0xffff0000,v73
v_lshlrev_b32 v96,16,v74
v_and_b32 v97,0xffff0000,v74
v_lshlrev_b32 v98,16,v75
v_and_b32 v99,0xffff0000,v75
v_lshlrev_b32 v100,16,v76
v_and_b32 v101,0xffff0000,v76
v_lshlrev_b32 v102,16,v77
v_and_b32 v103,0xffff0000,v77
v_lshlrev_b32 v104,16,v78
v_and_b32 v105,0xffff0000,v78
v_lshlrev_b32 v106,16,v79
v_and_b32 v107,0xffff0000,v79
v_lshlrev_b32 v108,16,v80
v_and_b32 v109,0xffff0000,v80
v_lshlrev_b32 v110,16,v81
v_and_b32 v111,0xffff0000,v81
v_lshlrev_b32 v112,16,v82
v_and_b32 v113,0xffff0000,v82
v_lshlrev_b32 v114,16,v83
v_and_b32 v115,0xffff0000,v83
v_add_f32_dpp v44,v36,v36 row_ror:8 row_mask:0xf bank_mask:0xf
v_cndmask_b32 v45,v40,v41,s[70:71]
v_cndmask_b32 v46,v42,v43,s[70:71]
ds_write_b32 v2,v44 offset:0
v_cndmask_b32 v45,v45,v46,s[72:73]
ds_write_b32 v0,v45 offset:0
L 0
s_barrier
R 52,3,0
R 56,3,16
R 60,3,32
L 0
v_pk_add_f32 v[52:53],v[52:53],v[54:55]
v_pk_add_f32 v[56:57],v[56:57],v[58:59]
v_pk_add_f32 v[60:61],v[60:61],v[62:63]
v_add_f32 v52,v52,v53
v_pk_add_f32 v[56:57],v[56:57],v[60:61]
v_mul_f32 v52,s68,v52
v_add_f32 v53,v56,v57
v_mul_f32 v53,s68,v53
v_fma_f32 v53,-v52,v52,v53
v_max_f32 v53,0,v53
buffer_load_dword v14,v6,s[36:39],s62 offen offset:0 sc1
v_add_f32 v53,s69,v53
v_rsq_f32 v48,v53
s_nop 0
v_mul_f32_e64 v50,-v52,v48
v_mov_b32 v49,v48
v_mov_b32 v51,v50
v_pk_fma_f32 v[52:53],v[116:117],v[48:49],v[50:51]
v_pk_fma_f32 v[54:55],v[118:119],v[48:49],v[50:51]
v_pk_fma_f32 v[56:57],v[120:121],v[48:49],v[50:51]
v_pk_fma_f32 v[58:59],v[122:123],v[48:49],v[50:51]
v_pk_fma_f32 v[116:117],v[52:53],v[84:85],v[116:117]
v_pk_fma_f32 v[118:119],v[54:55],v[86:87],v[118:119]
v_pk_fma_f32 v[120:121],v[56:57],v[88:89],v[120:121]
v_pk_fma_f32 v[122:123],v[58:59],v[90:91],v[122:123]
v_cvt_pk_bf16_f32 v148,v116,v117
v_cvt_pk_bf16_f32 v149,v118,v119
v_cvt_pk_bf16_f32 v150,v120,v121
v_cvt_pk_bf16_f32 v151,v122,v123
v_pk_fma_f32 v[52:53],v[124:125],v[48:49],v[50:51]
v_pk_fma_f32 v[54:55],v[126:127],v[48:49],v[50:51]
v_pk_fma_f32 v[56:57],v[128:129],v[48:49],v[50:51]
v_pk_fma_f32 v[58:59],v[130:131],v[48:49],v[50:51]
v_pk_fma_f32 v[124:125],v[52:53],v[92:93],v[124:125]
v_pk_fma_f32 v[126:127],v[54:55],v[94:95],v[126:127]
v_pk_fma_f32 v[128:129],v[56:57],v[96:97],v[128:129]
v_pk_fma_f32 v[130:131],v[58:59],v[98:99],v[130:131]
v_cvt_pk_bf16_f32 v152,v124,v125
v_cvt_pk_bf16_f32 v153,v126,v127
v_cvt_pk_bf16_f32 v154,v128,v129
v_cvt_pk_bf16_f32 v155,v130,v131
v_pk_fma_f32 v[52:53],v[132:133],v[48:49],v[50:51]
v_pk_fma_f32 v[54:55],v[134:135],v[48:49],v[50:51]
v_pk_fma_f32 v[56:57],v[136:137],v[48:49],v[50:51]
v_pk_fma_f32 v[58:59],v[138:139],v[48:49],v[50:51]
v_pk_fma_f32 v[132:133],v[52:53],v[100:101],v[132:133]
v_pk_fma_f32 v[134:135],v[54:55],v[102:103],v[134:135]
v_pk_fma_f32 v[136:137],v[56:57],v[104:105],v[136:137]
v_pk_fma_f32 v[138:139],v[58:59],v[106:107],v[138:139]
v_cvt_pk_bf16_f32 v156,v132,v133
v_cvt_pk_bf16_f32 v157,v134,v135
v_cvt_pk_bf16_f32 v158,v136,v137
v_cvt_pk_bf16_f32 v159,v138,v139
v_pk_fma_f32 v[52:53],v[140:141],v[48:49],v[50:51]
v_pk_fma_f32 v[54:55],v[142:143],v[48:49],v[50:51]
v_pk_fma_f32 v[56:57],v[144:145],v[48:49],v[50:51]
v_pk_fma_f32 v[58:59],v[146:147],v[48:49],v[50:51]
v_pk_fma_f32 v[140:141],v[52:53],v[108:109],v[140:141]
v_pk_fma_f32 v[142:143],v[54:55],v[110:111],v[142:143]
v_pk_fma_f32 v[144:145],v[56:57],v[112:113],v[144:145]
v_pk_fma_f32 v[146:147],v[58:59],v[114:115],v[146:147]
v_cvt_pk_bf16_f32 v160,v140,v141
v_cvt_pk_bf16_f32 v161,v142,v143
v_cvt_pk_bf16_f32 v162,v144,v145
v_cvt_pk_bf16_f32 v163,v146,v147
s_waitcnt vmcnt(13)
buffer_store_dword v13,v5,s[36:39],s63 offen offset:0 sc0 sc1
s_mov_b32 s63,0x7fff0000
v_mfma_f32_16x16x32_bf16 v[36:39],v[32:35],v[148:151],0
buffer_store_dwordx4 v[148:151],v1,s[36:39],s57 offen offset:0 sc0 sc1
v_mfma_f32_16x16x32_bf16 v[40:43],v[148:151],v[148:151],0
M 36,v[32:35],152
buffer_store_dwordx4 v[152:155],v1,s[36:39],s57 offen offset:2048 sc0 sc1
M 40,v[152:155],152
M 36,v[32:35],156
buffer_store_dwordx4 v[156:159],v1,s[36:39],s58 offen offset:0 sc0 sc1
M 40,v[156:159],156
M 36,v[32:35],160
buffer_store_dwordx4 v[160:163],v1,s[36:39],s58 offen offset:2048 sc0 sc1
M 40,v[160:163],160
s_nop 5
v_add_f32_dpp v44,v36,v36 row_ror:8 row_mask:0xf bank_mask:0xf
s_nop 0
v_cndmask_b32 v45,v40,v41,s[70:71]
v_cndmask_b32 v46,v42,v43,s[70:71]
ds_write_b32 v2,v44 offset:1024
v_cndmask_b32 v45,v45,v46,s[72:73]
ds_write_b32 v0,v45 offset:1024
L 0
s_barrier
R 52,3,1024
R 56,3,1040
R 60,3,1056
L 0
v_pk_add_f32 v[52:53],v[52:53],v[54:55]
v_pk_add_f32 v[56:57],v[56:57],v[58:59]
v_pk_add_f32 v[60:61],v[60:61],v[62:63]
v_add_f32 v52,v52,v53
v_pk_add_f32 v[56:57],v[56:57],v[60:61]
v_mul_f32 v52,s68,v52
v_add_f32 v53,v56,v57
v_mul_f32 v53,s68,v53
v_fma_f32 v53,-v52,v52,v53
v_max_f32 v53,0,v53
v_add_f32 v54,s69,v53
v_fma_f32 v55,s69,v54,v53
v_rsq_f32 v65,v54
v_rsq_f32 v54,v55
v_mul_f32_e64 v64,-v52,v65
v_mul_f32 v63,v55,v54
v_mul_f32 v61,s76,v54
v_mul_f32 v55,v54,v54
v_mul_f32 v62,s77,v55
v_mul_f32 v62,v62,v61
v_cndmask_b32 v60,v52,v64,s[74:75]
v_cndmask_b32 v61,v61,v65,s[74:75]
buffer_store_dwordx4 v[60:63],v4,s[36:39],s59 offen offset:0 sc0 sc1
s_mov_b32 s63,s60
s_lshr_b32 s78,s45,1
s_and_b32 s79,s45,1
s_lshl_b32 s79,s79,7
s_lshl_b32 s57,s78,15
s_add_u32 s57,s57,s79
s_add_u32 s57,s57,12582912
s_add_u32 s58,s57,4096
s_lshl_b32 s59,s78,9
s_add_u32 s59,s59,s79
s_add_u32 s59,s59,107020288
s_lshl_b32 s60,s45,9
s_add_u32 s60,s60,s66
s_add_u32 s60,s60,131072
s_add_u32 s45,s45,s46
s_lshr_b32 s78,s45,1
s_and_b32 s79,s45,1
s_lshl_b32 s79,s79,7
s_lshl_b32 s55,s78,15
s_add_u32 s55,s55,s79
s_mul_i32 s56,s45,16384
s_add_u32 s56,s56,98566144
s_lshl_b32 s61,s78,11
s_add_u32 s61,s61,110587904
s_add_u32 s78,s45,s46
s_lshr_b32 s78,s78,1
s_lshl_b32 s62,s78,11
s_add_u32 s62,s62,110587904
s_waitcnt vmcnt(6)
s_branch Lr_it1_%=
Lr_tb0_%=:
v_add_f32 v53,s69,v53
v_rsq_f32 v48,v53
s_nop 0
v_mul_f32_e64 v50,-v52,v48
v_mov_b32 v49,v48
v_mov_b32 v51,v50
v_pk_fma_f32 v[52:53],v[116:117],v[48:49],v[50:51]
v_pk_fma_f32 v[54:55],v[118:119],v[48:49],v[50:51]
v_pk_fma_f32 v[56:57],v[120:121],v[48:49],v[50:51]
v_pk_fma_f32 v[58:59],v[122:123],v[48:49],v[50:51]
s_add_u32 s78,s55,71303168
buffer_load_dwordx4 v[196:199],v1,s[36:39],s78 offen offset:0 sc1
buffer_load_dwordx4 v[200:203],v1,s[36:39],s78 offen offset:2048 sc1
s_add_u32 s78,s55,71307264
buffer_load_dwordx4 v[204:207],v1,s[36:39],s78 offen offset:0 sc1
buffer_load_dwordx4 v[208:211],v1,s[36:39],s78 offen offset:2048 sc1
v_pk_fma_f32 v[116:117],v[52:53],v[84:85],v[116:117]
v_pk_fma_f32 v[118:119],v[54:55],v[86:87],v[118:119]
v_pk_fma_f32 v[120:121],v[56:57],v[88:89],v[120:121]
v_pk_fma_f32 v[122:123],v[58:59],v[90:91],v[122:123]
v_cvt_pk_bf16_f32 v148,v116,v117
v_cvt_pk_bf16_f32 v149,v118,v119
v_cvt_pk_bf16_f32 v150,v120,v121
v_cvt_pk_bf16_f32 v151,v122,v123
v_pk_fma_f32 v[52:53],v[124:125],v[48:49],v[50:51]
v_pk_fma_f32 v[54:55],v[126:127],v[48:49],v[50:51]
v_pk_fma_f32 v[56:57],v[128:129],v[48:49],v[50:51]
v_pk_fma_f32 v[58:59],v[130:131],v[48:49],v[50:51]
s_add_u32 s78,s55,89128960
buffer_load_dwordx4 v[212:215],v1,s[36:39],s78 offen offset:0 sc1
buffer_load_dwordx4 v[216:219],v1,s[36:39],s78 offen offset:2048 sc1
s_add_u32 s78,s55,89133056
buffer_load_dwordx4 v[220:223],v1,s[36:39],s78 offen offset:0 sc1
buffer_load_dwordx4 v[224:227],v1,s[36:39],s78 offen offset:2048 sc1
v_pk_fma_f32 v[124:125],v[52:53],v[92:93],v[124:125]
v_pk_fma_f32 v[126:127],v[54:55],v[94:95],v[126:127]
v_pk_fma_f32 v[128:129],v[56:57],v[96:97],v[128:129]
v_pk_fma_f32 v[130:131],v[58:59],v[98:99],v[130:131]
v_cvt_pk_bf16_f32 v152,v124,v125
v_cvt_pk_bf16_f32 v153,v126,v127
v_cvt_pk_bf16_f32 v154,v128,v129
v_cvt_pk_bf16_f32 v155,v130,v131
v_pk_fma_f32 v[52:53],v[132:133],v[48:49],v[50:51]
v_pk_fma_f32 v[54:55],v[134:135],v[48:49],v[50:51]
v_pk_fma_f32 v[56:57],v[136:137],v[48:49],v[50:51]
v_pk_fma_f32 v[58:59],v[138:139],v[48:49],v[50:51]
s_add_u32 m0,s65,16384
s_nop 0
D 9,56,0
D 9,56,1024
D 9,56,2048
D 9,56,3072
v_pk_fma_f32 v[132:133],v[52:53],v[100:101],v[132:133]
v_pk_fma_f32 v[134:135],v[54:55],v[102:103],v[134:135]
v_pk_fma_f32 v[136:137],v[56:57],v[104:105],v[136:137]
v_pk_fma_f32 v[138:139],v[58:59],v[106:107],v[138:139]
v_cvt_pk_bf16_f32 v156,v132,v133
v_cvt_pk_bf16_f32 v157,v134,v135
v_cvt_pk_bf16_f32 v158,v136,v137
v_cvt_pk_bf16_f32 v159,v138,v139
v_pk_fma_f32 v[52:53],v[140:141],v[48:49],v[50:51]
v_pk_fma_f32 v[54:55],v[142:143],v[48:49],v[50:51]
v_pk_fma_f32 v[56:57],v[144:145],v[48:49],v[50:51]
v_pk_fma_f32 v[58:59],v[146:147],v[48:49],v[50:51]
v_pk_fma_f32 v[140:141],v[52:53],v[108:109],v[140:141]
v_pk_fma_f32 v[142:143],v[54:55],v[110:111],v[142:143]
v_pk_fma_f32 v[144:145],v[56:57],v[112:113],v[144:145]
v_pk_fma_f32 v[146:147],v[58:59],v[114:115],v[146:147]
v_cvt_pk_bf16_f32 v160,v140,v141
v_cvt_pk_bf16_f32 v161,v142,v143
v_cvt_pk_bf16_f32 v162,v144,v145
v_cvt_pk_bf16_f32 v163,v146,v147
v_mfma_f32_16x16x32_bf16 v[36:39],v[32:35],v[148:151],0
buffer_load_dword v14,v6,s[36:39],s62 offen offset:0 sc1
buffer_store_dwordx4 v[148:151],v1,s[36:39],s57 offen offset:0 sc0 sc1
v_mfma_f32_16x16x32_bf16 v[40:43],v[148:151],v[148:151],0
M 36,v[32:35],152
buffer_store_dwordx4 v[152:155],v1,s[36:39],s57 offen offset:2048 sc0 sc1
M 40,v[152:155],152
M 36,v[32:35],156
buffer_store_dwordx4 v[156:159],v1,s[36:39],s58 offen offset:0 sc0 sc1
M 40,v[156:159],156
M 36,v[32:35],160
buffer_store_dwordx4 v[160:163],v1,s[36:39],s58 offen offset:2048 sc0 sc1
M 40,v[160:163],160
s_nop 5
v_add_f32_dpp v44,v36,v36 row_ror:8 row_mask:0xf bank_mask:0xf
s_nop 0
v_cndmask_b32 v45,v40,v41,s[70:71]
v_cndmask_b32 v46,v42,v43,s[70:71]
ds_write_b32 v2,v44 offset:1024
v_cndmask_b32 v45,v45,v46,s[72:73]
ds_write_b32 v0,v45 offset:1024
L 0
s_barrier
R 52,3,1024
R 56,3,1040
R 60,3,1056
L 0
v_pk_add_f32 v[52:53],v[52:53],v[54:55]
v_pk_add_f32 v[56:57],v[56:57],v[58:59]
v_pk_add_f32 v[60:61],v[60:61],v[62:63]
v_add_f32 v52,v52,v53
v_pk_add_f32 v[56:57],v[56:57],v[60:61]
v_mul_f32 v52,s68,v52
v_add_f32 v53,v56,v57
v_mul_f32 v53,s68,v53
v_fma_f32 v53,-v52,v52,v53
v_max_f32 v53,0,v53
v_add_f32 v54,s69,v53
v_fma_f32 v55,s69,v54,v53
v_rsq_f32 v65,v54
v_rsq_f32 v54,v55
v_mul_f32_e64 v64,-v52,v65
v_mul_f32 v63,v55,v54
v_mul_f32 v61,s76,v54
v_mul_f32 v55,v54,v54
v_mul_f32 v62,s77,v55
v_mul_f32 v62,v62,v61
v_cndmask_b32 v60,v52,v64,s[74:75]
v_cndmask_b32 v61,v61,v65,s[74:75]
buffer_store_dwordx4 v[60:63],v4,s[36:39],s59 offen offset:0 sc0 sc1
s_mov_b32 s63,s60
s_lshr_b32 s78,s45,1
s_and_b32 s79,s45,1
s_lshl_b32 s79,s79,7
s_lshl_b32 s57,s78,15
s_add_u32 s57,s57,s79
s_add_u32 s57,s57,12582912
s_add_u32 s58,s57,4096
s_lshl_b32 s59,s78,9
s_add_u32 s59,s59,s79
s_add_u32 s59,s59,107020288
s_lshl_b32 s60,s45,9
s_add_u32 s60,s60,s66
s_add_u32 s60,s60,131072
s_add_u32 s45,s45,s46
s_lshr_b32 s78,s45,1
s_and_b32 s79,s45,1
s_lshl_b32 s79,s79,7
s_lshl_b32 s55,s78,15
s_add_u32 s55,s55,s79
s_mul_i32 s56,s45,16384
s_add_u32 s56,s56,98566144
s_lshl_b32 s61,s78,11
s_add_u32 s61,s61,110587904
s_add_u32 s78,s45,s46
s_lshr_b32 s78,s78,1
s_lshl_b32 s62,s78,11
s_add_u32 s62,s62,110587904
s_waitcnt vmcnt(5)
s_branch Lr_it1_%=
Lr_tc0_%=:
s_add_u32 s78,s55,71303168
buffer_load_dwordx4 v[196:199],v1,s[36:39],s78 offen offset:0 sc1
buffer_load_dwordx4 v[200:203],v1,s[36:39],s78 offen offset:2048 sc1
s_add_u32 s78,s55,71307264
buffer_load_dwordx4 v[204:207],v1,s[36:39],s78 offen offset:0 sc1
buffer_load_dwordx4 v[208:211],v1,s[36:39],s78 offen offset:2048 sc1
s_add_u32 s78,s55,89128960
buffer_load_dwordx4 v[212:215],v1,s[36:39],s78 offen offset:0 sc1
buffer_load_dwordx4 v[216:219],v1,s[36:39],s78 offen offset:2048 sc1
s_add_u32 s78,s55,89133056
buffer_load_dwordx4 v[220:223],v1,s[36:39],s78 offen offset:0 sc1
buffer_load_dwordx4 v[224:227],v1,s[36:39],s78 offen offset:2048 sc1
s_add_u32 m0,s65,16384
s_nop 0
D 9,56,0
D 9,56,1024
D 9,56,2048
D 9,56,3072
buffer_load_dword v14,v6,s[36:39],s62 offen offset:0 sc1
s_waitcnt vmcnt(13)
buffer_store_dword v13,v5,s[36:39],s63 offen offset:0 sc0 sc1
s_mov_b32 s63,0x7fff0000
s_lshr_b32 s78,s45,1
s_and_b32 s79,s45,1
s_lshl_b32 s79,s79,7
s_lshl_b32 s57,s78,15
s_add_u32 s57,s57,s79
s_add_u32 s57,s57,12582912
s_add_u32 s58,s57,4096
s_lshl_b32 s59,s78,9
s_add_u32 s59,s59,s79
s_add_u32 s59,s59,107020288
s_lshl_b32 s60,s45,9
s_add_u32 s60,s60,s66
s_add_u32 s60,s60,131072
s_add_u32 s45,s45,s46
s_lshr_b32 s78,s45,1
s_and_b32 s79,s45,1
s_lshl_b32 s79,s79,7
s_lshl_b32 s55,s78,15
s_add_u32 s55,s55,s79
s_mul_i32 s56,s45,16384
s_add_u32 s56,s56,98566144
s_lshl_b32 s61,s78,11
s_add_u32 s61,s61,110587904
s_add_u32 s78,s45,s46
s_lshr_b32 s78,s78,1
s_lshl_b32 s62,s78,11
s_add_u32 s62,s62,110587904
s_waitcnt vmcnt(1)
s_branch Lr_it1_%=
Lr_it1_%=:
T v[68:69],10,16384
T v[70:71],10,16448
T v[72:73],10,17408
T v[74:75],10,17472
T v[76:77],10,18432
T v[78:79],10,18496
T v[80:81],10,19456
T v[82:83],10,19520
s_nop 0
v_cmp_eq_u32 vcc,s53,v14
s_nop 0
s_cmp_eq_u64 vcc,-1
s_cselect_b32 s78,s45,256
s_cmp_lt_u32 s78,256
s_cbranch_scc1 Lr_ta1_%=
buffer_load_dword v14,v6,s[36:39],s61 offen offset:0 sc1
v_mfma_f32_16x16x32_bf16 v[116:119],v[26:29],v[196:199],0
L 6
v_lshlrev_b32 v84,16,v68
v_and_b32 v85,0xffff0000,v68
v_mfma_f32_16x16x32_bf16 v[120:123],v[24:27],v[196:199],0
L 4
v_lshlrev_b32 v86,16,v69
v_and_b32 v87,0xffff0000,v69
M 116,v[26:29],212
L 2
v_lshlrev_b32 v88,16,v70
v_and_b32 v89,0xffff0000,v70
M 120,v[24:27],212
L 0
v_lshlrev_b32 v90,16,v71
v_and_b32 v91,0xffff0000,v71
v_mfma_f32_16x16x32_bf16 v[124:127],v[26:29],v[200:203],0
v_lshlrev_b32 v92,16,v72
v_and_b32 v93,0xffff0000,v72
v_mfma_f32_16x16x32_bf16 v[128:131],v[24:27],v[200:203],0
v_lshlrev_b32 v94,16,v73
v_and_b32 v95,0xffff0000,v73
M 124,v[26:29],216
v_cvt_pk_bf16_f32 v148,v116,v117
v_cvt_pk_bf16_f32 v149,v118,v119
M 128,v[24:27],216
v_cvt_pk_bf16_f32 v150,v120,v121
v_cvt_pk_bf16_f32 v151,v122,v123
v_mfma_f32_16x16x32_bf16 v[132:135],v[26:29],v[204:207],0
v_lshlrev_b32 v96,16,v74
v_and_b32 v97,0xffff0000,v74
)"
R"(v_mfma_f32_16x16x32_bf16 v[136:139],v[24:27],v[204:207],0
v_lshlrev_b32 v98,16,v75
v_and_b32 v99,0xffff0000,v75
M 132,v[26:29],220
v_cvt_pk_bf16_f32 v152,v124,v125
v_cvt_pk_bf16_f32 v153,v126,v127
M 136,v[24:27],220
v_cvt_pk_bf16_f32 v154,v128,v129
v_cvt_pk_bf16_f32 v155,v130,v131
v_mfma_f32_16x16x32_bf16 v[140:143],v[26:29],v[208:211],0
v_lshlrev_b32 v100,16,v76
v_and_b32 v101,0xffff0000,v76
v_mfma_f32_16x16x32_bf16 v[144:147],v[24:27],v[208:211],0
v_lshlrev_b32 v102,16,v77
v_and_b32 v103,0xffff0000,v77
M 140,v[26:29],224
v_cvt_pk_bf16_f32 v156,v132,v133
v_cvt_pk_bf16_f32 v157,v134,v135
M 144,v[24:27],224
v_cvt_pk_bf16_f32 v158,v136,v137
v_cvt_pk_bf16_f32 v159,v138,v139
v_mfma_f32_16x16x32_bf16 v[36:39],v[32:35],v[148:151],0
v_lshlrev_b32 v104,16,v78
v_and_b32 v105,0xffff0000,v78
v_mfma_f32_16x16x32_bf16 v[40:43],v[148:151],v[148:151],0
v_lshlrev_b32 v106,16,v79
v_and_b32 v107,0xffff0000,v79
M 36,v[32:35],152
v_cvt_pk_bf16_f32 v160,v140,v141
v_cvt_pk_bf16_f32 v161,v142,v143
M 40,v[152:155],152
v_cvt_pk_bf16_f32 v162,v144,v145
v_cvt_pk_bf16_f32 v163,v146,v147
M 36,v[32:35],156
v_lshlrev_b32 v108,16,v80
v_and_b32 v109,0xffff0000,v80
M 40,v[156:159],156
v_lshlrev_b32 v110,16,v81
v_and_b32 v111,0xffff0000,v81
M 36,v[32:35],160
v_lshlrev_b32 v112,16,v82
v_and_b32 v113,0xffff0000,v82
M 40,v[160:163],160
v_lshlrev_b32 v114,16,v83
v_and_b32 v115,0xffff0000,v83
s_nop 2
v_add_f32_dpp v44,v36,v36 row_ror:8 row_mask:0xf bank_mask:0xf
s_nop 1
v_cndmask_b32 v45,v40,v41,s[70:71]
v_cndmask_b32 v46,v42,v43,s[70:71]
ds_write_b32 v2,v44 offset:0
v_cndmask_b32 v45,v45,v46,s[72:73]
ds_write_b32 v0,v45 offset:0
L 0
s_barrier
R 52,3,0
R 56,3,16
R 60,3,32
L 0
v_pk_add_f32 v[52:53],v[52:53],v[54:55]
v_pk_add_f32 v[56:57],v[56:57],v[58:59]
v_pk_add_f32 v[60:61],v[60:61],v[62:63]
v_add_f32 v52,v52,v53
v_pk_add_f32 v[56:57],v[56:57],v[60:61]
v_mul_f32 v52,s68,v52
v_add_f32 v53,v56,v57
v_mul_f32 v53,s68,v53
v_fma_f32 v53,-v52,v52,v53
v_max_f32 v53,0,v53
s_waitcnt vmcnt(1)
buffer_store_dword v13,v5,s[36:39],s63 offen offset:0 sc0 sc1
s_mov_b32 s63,0x7fff0000
s_waitcnt vmcnt(1)
v_cmp_eq_u32 vcc,s53,v14
s_nop 0
s_cmp_eq_u64 vcc,-1
s_cselect_b32 s78,s45,256
s_cmp_lt_u32 s78,256
s_cbranch_scc1 Lr_tb1_%=
buffer_load_dword v14,v6,s[36:39],s61 offen offset:0 sc1
v_add_f32 v53,s69,v53
v_rsq_f32 v48,v53
s_nop 0
v_mul_f32_e64 v50,-v52,v48
v_mov_b32 v49,v48
v_mov_b32 v51,v50
v_pk_fma_f32 v[52:53],v[116:117],v[48:49],v[50:51]
v_pk_fma_f32 v[54:55],v[118:119],v[48:49],v[50:51]
v_pk_fma_f32 v[56:57],v[120:121],v[48:49],v[50:51]
v_pk_fma_f32 v[58:59],v[122:123],v[48:49],v[50:51]
v_pk_fma_f32 v[116:117],v[52:53],v[84:85],v[116:117]
v_pk_fma_f32 v[118:119],v[54:55],v[86:87],v[118:119]
v_pk_fma_f32 v[120:121],v[56:57],v[88:89],v[120:121]
v_pk_fma_f32 v[122:123],v[58:59],v[90:91],v[122:123]
v_cvt_pk_bf16_f32 v148,v116,v117
v_cvt_pk_bf16_f32 v149,v118,v119
v_cvt_pk_bf16_f32 v150,v120,v121
v_cvt_pk_bf16_f32 v151,v122,v123
v_pk_fma_f32 v[52:53],v[124:125],v[48:49],v[50:51]
v_pk_fma_f32 v[54:55],v[126:127],v[48:49],v[50:51]
v_pk_fma_f32 v[56:57],v[128:129],v[48:49],v[50:51]
v_pk_fma_f32 v[58:59],v[130:131],v[48:49],v[50:51]
v_pk_fma_f32 v[124:125],v[52:53],v[92:93],v[124:125]
v_pk_fma_f32 v[126:127],v[54:55],v[94:95],v[126:127]
v_pk_fma_f32 v[128:129],v[56:57],v[96:97],v[128:129]
v_pk_fma_f32 v[130:131],v[58:59],v[98:99],v[130:131]
v_cvt_pk_bf16_f32 v152,v124,v125
v_cvt_pk_bf16_f32 v153,v126,v127
v_cvt_pk_bf16_f32 v154,v128,v129
v_cvt_pk_bf16_f32 v155,v130,v131
v_pk_fma_f32 v[52:53],v[132:133],v[48:49],v[50:51]
v_pk_fma_f32 v[54:55],v[134:135],v[48:49],v[50:51]
v_pk_fma_f32 v[56:57],v[136:137],v[48:49],v[50:51]
v_pk_fma_f32 v[58:59],v[138:139],v[48:49],v[50:51]
v_pk_fma_f32 v[132:133],v[52:53],v[100:101],v[132:133]
v_pk_fma_f32 v[134:135],v[54:55],v[102:103],v[134:135]
v_pk_fma_f32 v[136:137],v[56:57],v[104:105],v[136:137]
v_pk_fma_f32 v[138:139],v[58:59],v[106:107],v[138:139]
v_cvt_pk_bf16_f32 v156,v132,v133
v_cvt_pk_bf16_f32 v157,v134,v135
v_cvt_pk_bf16_f32 v158,v136,v137
v_cvt_pk_bf16_f32 v159,v138,v139
v_pk_fma_f32 v[52:53],v[140:141],v[48:49],v[50:51]
v_pk_fma_f32 v[54:55],v[142:143],v[48:49],v[50:51]
v_pk_fma_f32 v[56:57],v[144:145],v[48:49],v[50:51]
v_pk_fma_f32 v[58:59],v[146:147],v[48:49],v[50:51]
v_pk_fma_f32 v[140:141],v[52:53],v[108:109],v[140:141]
v_pk_fma_f32 v[142:143],v[54:55],v[110:111],v[142:143]
v_pk_fma_f32 v[144:145],v[56:57],v[112:113],v[144:145]
v_pk_fma_f32 v[146:147],v[58:59],v[114:115],v[146:147]
v_cvt_pk_bf16_f32 v160,v140,v141
v_cvt_pk_bf16_f32 v161,v142,v143
v_cvt_pk_bf16_f32 v162,v144,v145
v_cvt_pk_bf16_f32 v163,v146,v147
v_mfma_f32_16x16x32_bf16 v[36:39],v[32:35],v[148:151],0
buffer_store_dwordx4 v[148:151],v1,s[36:39],s57 offen offset:0 sc0 sc1
v_mfma_f32_16x16x32_bf16 v[40:43],v[148:151],v[148:151],0
M 36,v[32:35],152
buffer_store_dwordx4 v[152:155],v1,s[36:39],s57 offen offset:2048 sc0 sc1
M 40,v[152:155],152
M 36,v[32:35],156
buffer_store_dwordx4 v[156:159],v1,s[36:39],s58 offen offset:0 sc0 sc1
M 40,v[156:159],156
M 36,v[32:35],160
buffer_store_dwordx4 v[160:163],v1,s[36:39],s58 offen offset:2048 sc0 sc1
M 40,v[160:163],160
s_nop 5
v_add_f32_dpp v44,v36,v36 row_ror:8 row_mask:0xf bank_mask:0xf
s_nop 0
v_cndmask_b32 v45,v40,v41,s[70:71]
v_cndmask_b32 v46,v42,v43,s[70:71]
ds_write_b32 v2,v44 offset:1024
v_cndmask_b32 v45,v45,v46,s[72:73]
ds_write_b32 v0,v45 offset:1024
L 0
s_barrier
R 52,3,1024
R 56,3,1040
R 60,3,1056
L 0
v_pk_add_f32 v[52:53],v[52:53],v[54:55]
v_pk_add_f32 v[56:57],v[56:57],v[58:59]
v_pk_add_f32 v[60:61],v[60:61],v[62:63]
v_add_f32 v52,v52,v53
v_pk_add_f32 v[56:57],v[56:57],v[60:61]
v_mul_f32 v52,s68,v52
v_add_f32 v53,v56,v57
v_mul_f32 v53,s68,v53
v_fma_f32 v53,-v52,v52,v53
v_max_f32 v53,0,v53
v_add_f32 v54,s69,v53
v_fma_f32 v55,s69,v54,v53
v_rsq_f32 v65,v54
v_rsq_f32 v54,v55
v_mul_f32_e64 v64,-v52,v65
v_mul_f32 v63,v55,v54
v_mul_f32 v61,s76,v54
v_mul_f32 v55,v54,v54
v_mul_f32 v62,s77,v55
v_mul_f32 v62,v62,v61
v_cndmask_b32 v60,v52,v64,s[74:75]
v_cndmask_b32 v61,v61,v65,s[74:75]
buffer_store_dwordx4 v[60:63],v4,s[36:39],s59 offen offset:0 sc0 sc1
s_mov_b32 s63,s60
s_waitcnt vmcnt(5)
v_cmp_eq_u32 vcc,s53,v14
s_nop 0
s_cmp_eq_u64 vcc,-1
s_cselect_b32 s78,s45,256
s_cmp_lt_u32 s78,256
s_cbranch_scc1 Lr_tc1_%=
s_waitcnt vmcnt(0)
buffer_store_dword v13,v5,s[36:39],s63 offen offset:0 sc0 sc1
s_mov_b32 s63,0x7fff0000
s_cmp_ge_u32 s45,256
s_cbranch_scc1 Lr_done_%=
Lr_bp1_%=:
buffer_load_dword v14,v6,s[36:39],s61 offen offset:0 sc1
s_add_u32 s82,s82,1
s_waitcnt vmcnt(0)
v_cmp_eq_u32 vcc,s53,v14
s_nop 0
s_cmp_eq_u64 vcc,-1
s_cbranch_scc1 Lr_bq1_%=
s_sleep 2
s_cmp_lt_u32 s82,20000
s_cbranch_scc1 Lr_bp1_%=
Lr_bq1_%=:
s_add_u32 s78,s55,71303168
buffer_load_dwordx4 v[164:167],v1,s[36:39],s78 offen offset:0 sc1
buffer_load_dwordx4 v[168:171],v1,s[36:39],s78 offen offset:2048 sc1
s_add_u32 s78,s55,71307264
buffer_load_dwordx4 v[172:175],v1,s[36:39],s78 offen offset:0 sc1
buffer_load_dwordx4 v[176:179],v1,s[36:39],s78 offen offset:2048 sc1
s_add_u32 s78,s55,89128960
buffer_load_dwordx4 v[180:183],v1,s[36:39],s78 offen offset:0 sc1
buffer_load_dwordx4 v[184:187],v1,s[36:39],s78 offen offset:2048 sc1
s_add_u32 s78,s55,89133056
buffer_load_dwordx4 v[188:191],v1,s[36:39],s78 offen offset:0 sc1
buffer_load_dwordx4 v[192:195],v1,s[36:39],s78 offen offset:2048 sc1
s_add_u32 m0,s65,0
s_nop 0
D 9,56,0
D 9,56,1024
D 9,56,2048
D 9,56,3072
buffer_load_dword v14,v6,s[36:39],s62 offen offset:0 sc1
s_lshr_b32 s78,s45,1
s_and_b32 s79,s45,1
s_lshl_b32 s79,s79,7
s_lshl_b32 s57,s78,15
s_add_u32 s57,s57,s79
s_add_u32 s57,s57,12582912
s_add_u32 s58,s57,4096
s_lshl_b32 s59,s78,9
s_add_u32 s59,s59,s79
s_add_u32 s59,s59,107020288
s_lshl_b32 s60,s45,9
s_add_u32 s60,s60,s66
s_add_u32 s60,s60,131072
s_add_u32 s45,s45,s46
s_lshr_b32 s78,s45,1
s_and_b32 s79,s45,1
s_lshl_b32 s79,s79,7
s_lshl_b32 s55,s78,15
s_add_u32 s55,s55,s79
s_mul_i32 s56,s45,16384
s_add_u32 s56,s56,98566144
s_lshl_b32 s61,s78,11
s_add_u32 s61,s61,110587904
s_add_u32 s78,s45,s46
s_lshr_b32 s78,s78,1
s_lshl_b32 s62,s78,11
s_add_u32 s62,s62,110587904
s_waitcnt vmcnt(0)
s_branch Lr_it0_%=
Lr_ta1_%=:
v_mfma_f32_16x16x32_bf16 v[116:119],v[26:29],v[196:199],0
s_add_u32 s78,s55,71303168
buffer_load_dwordx4 v[164:167],v1,s[36:39],s78 offen offset:0 sc1
v_mfma_f32_16x16x32_bf16 v[120:123],v[24:27],v[196:199],0
L 6
buffer_load_dwordx4 v[168:171],v1,s[36:39],s78 offen offset:2048 sc1
M 116,v[26:29],212
s_add_u32 s78,s55,71307264
buffer_load_dwordx4 v[172:175],v1,s[36:39],s78 offen offset:0 sc1
M 120,v[24:27],212
L 4
buffer_load_dwordx4 v[176:179],v1,s[36:39],s78 offen offset:2048 sc1
v_mfma_f32_16x16x32_bf16 v[124:127],v[26:29],v[200:203],0
s_add_u32 s78,s55,89128960
buffer_load_dwordx4 v[180:183],v1,s[36:39],s78 offen offset:0 sc1
v_mfma_f32_16x16x32_bf16 v[128:131],v[24:27],v[200:203],0
L 2
buffer_load_dwordx4 v[184:187],v1,s[36:39],s78 offen offset:2048 sc1
M 124,v[26:29],216
L 0
v_cvt_pk_bf16_f32 v148,v116,v117
v_cvt_pk_bf16_f32 v149,v118,v119
M 128,v[24:27],216
s_add_u32 s78,s55,89133056
buffer_load_dwordx4 v[188:191],v1,s[36:39],s78 offen offset:0 sc1
v_mfma_f32_16x16x32_bf16 v[132:135],v[26:29],v[204:207],0
s_add_u32 m0,s65,0
buffer_load_dwordx4 v[192:195],v1,s[36:39],s78 offen offset:2048 sc1
v_mfma_f32_16x16x32_bf16 v[136:139],v[24:27],v[204:207],0
D 9,56,0
M 132,v[26:29],220
D 9,56,1024
M 136,v[24:27],220
D 9,56,2048
v_mfma_f32_16x16x32_bf16 v[140:143],v[26:29],v[208:211],0
D 9,56,3072
v_mfma_f32_16x16x32_bf16 v[144:147],v[24:27],v[208:211],0
v_cvt_pk_bf16_f32 v150,v120,v121
v_cvt_pk_bf16_f32 v151,v122,v123
M 140,v[26:29],224
v_cvt_pk_bf16_f32 v152,v124,v125
v_cvt_pk_bf16_f32 v153,v126,v127
M 144,v[24:27],224
v_cvt_pk_bf16_f32 v154,v128,v129
v_cvt_pk_bf16_f32 v155,v130,v131
v_mfma_f32_16x16x32_bf16 v[36:39],v[32:35],v[148:151],0
v_cvt_pk_bf16_f32 v156,v132,v133
v_cvt_pk_bf16_f32 v157,v134,v135
v_mfma_f32_16x16x32_bf16 v[40:43],v[148:151],v[148:151],0
v_cvt_pk_bf16_f32 v158,v136,v137
v_cvt_pk_bf16_f32 v159,v138,v139
M 36,v[32:35],152
v_cvt_pk_bf16_f32 v160,v140,v141
v_cvt_pk_bf16_f32 v161,v142,v143
M 40,v[152:155],152
v_cvt_pk_bf16_f32 v162,v144,v145
v_cvt_pk_bf16_f32 v163,v146,v147
M 36,v[32:35],156
v_lshlrev_b32 v84,16,v68
v_and_b32 v85,0xffff0000,v68
M 40,v[156:159],156
v_lshlrev_b32 v86,16,v69
v_and_b32 v87,0xffff0000,v69
M 36,v[32:35],160
v_lshlrev_b32 v88,16,v70
v_and_b32 v89,0xffff0000,v70
M 40,v[160:163],160
v_lshlrev_b32 v90,16,v71
v_and_b32 v91,0xffff0000,v71
v_lshlrev_b32 v92,16,v72
v_and_b32 v93,0xffff0000,v72
v_lshlrev_b32 v94,16,v73
v_and_b32 v95,0xffff0000,v73
v_lshlrev_b32 v96,16,v74
v_and_b32 v97,0xffff0000,v74
v_lshlrev_b32 v98,16,v75
v_and_b32 v99,0xffff0000,v75
v_lshlrev_b32 v100,16,v76
v_and_b32 v101,0xffff0000,v76
v_lshlrev_b32 v102,16,v77
v_and_b32 v103,0xffff0000,v77
v_lshlrev_b32 v104,16,v78
v_and_b32 v105,0xffff0000,v78
v_lshlrev_b32 v106,16,v79
v_and_b32 v107,0xffff0000,v79
v_lshlrev_b32 v108,16,v80
v_and_b32 v109,0xffff0000,v80
v_lshlrev_b32 v110,16,v81
v_and_b32 v111,0xffff0000,v81
v_lshlrev_b32 v112,16,v82
v_and_b32 v113,0xffff0000,v82
v_lshlrev_b32 v114,16,v83
v_and_b32 v115,0xffff0000,v83
v_add_f32_dpp v44,v36,v36 row_ror:8 row_mask:0xf bank_mask:0xf
v_cndmask_b32 v45,v40,v41,s[70:71]
v_cndmask_b32 v46,v42,v43,s[70:71]
ds_write_b32 v2,v44 offset:0
v_cndmask_b32 v45,v45,v46,s[72:73]
ds_write_b32 v0,v45 offset:0
L 0
s_barrier
R 52,3,0
R 56,3,16
R 60,3,32
L 0
v_pk_add_f32 v[52:53],v[52:53],v[54:55]
v_pk_add_f32 v[56:57],v[56:57],v[58:59]
v_pk_add_f32 v[60:61],v[60:61],v[62:63]
v_add_f32 v52,v52,v53
v_pk_add_f32 v[56:57],v[56:57],v[60:61]
v_mul_f32 v52,s68,v52
v_add_f32 v53,v56,v57
v_mul_f32 v53,s68,v53
v_fma_f32 v53,-v52,v52,v53
v_max_f32 v53,0,v53
buffer_load_dword v14,v6,s[36:39],s62 offen offset:0 sc1
v_add_f32 v53,s69,v53
v_rsq_f32 v48,v53
s_nop 0
v_mul_f32_e64 v50,-v52,v48
v_mov_b32 v49,v48
v_mov_b32 v51,v50
v_pk_fma_f32 v[52:53],v[116:117],v[48:49],v[50:51]
v_pk_fma_f32 v[54:55],v[118:119],v[48:49],v[50:51]
v_pk_fma_f32 v[56:57],v[120:121],v[48:49],v[50:51]
v_pk_fma_f32 v[58:59],v[122:123],v[48:49],v[50:51]
v_pk_fma_f32 v[116:117],v[52:53],v[84:85],v[116:117]
v_pk_fma_f32 v[118:119],v[54:55],v[86:87],v[118:119]
v_pk_fma_f32 v[120:121],v[56:57],v[88:89],v[120:121]
v_pk_fma_f32 v[122:123],v[58:59],v[90:91],v[122:123]
v_cvt_pk_bf16_f32 v148,v116,v117
v_cvt_pk_bf16_f32 v149,v118,v119
v_cvt_pk_bf16_f32 v150,v120,v121
v_cvt_pk_bf16_f32 v151,v122,v123
v_pk_fma_f32 v[52:53],v[124:125],v[48:49],v[50:51]
v_pk_fma_f32 v[54:55],v[126:127],v[48:49],v[50:51]
v_pk_fma_f32 v[56:57],v[128:129],v[48:49],v[50:51]
v_pk_fma_f32 v[58:59],v[130:131],v[48:49],v[50:51]
v_pk_fma_f32 v[124:125],v[52:53],v[92:93],v[124:125]
v_pk_fma_f32 v[126:127],v[54:55],v[94:95],v[126:127]
v_pk_fma_f32 v[128:129],v[56:57],v[96:97],v[128:129]
v_pk_fma_f32 v[130:131],v[58:59],v[98:99],v[130:131]
v_cvt_pk_bf16_f32 v152,v124,v125
v_cvt_pk_bf16_f32 v153,v126,v127
v_cvt_pk_bf16_f32 v154,v128,v129
v_cvt_pk_bf16_f32 v155,v130,v131
v_pk_fma_f32 v[52:53],v[132:133],v[48:49],v[50:51]
v_pk_fma_f32 v[54:55],v[134:135],v[48:49],v[50:51]
v_pk_fma_f32 v[56:57],v[136:137],v[48:49],v[50:51]
v_pk_fma_f32 v[58:59],v[138:139],v[48:49],v[50:51]
v_pk_fma_f32 v[132:133],v[52:53],v[100:101],v[132:133]
v_pk_fma_f32 v[134:135],v[54:55],v[102:103],v[134:135]
v_pk_fma_f32 v[136:137],v[56:57],v[104:105],v[136:137]
v_pk_fma_f32 v[138:139],v[58:59],v[106:107],v[138:139]
v_cvt_pk_bf16_f32 v156,v132,v133
v_cvt_pk_bf16_f32 v157,v134,v135
v_cvt_pk_bf16_f32 v158,v136,v137
v_cvt_pk_bf16_f32 v159,v138,v139
v_pk_fma_f32 v[52:53],v[140:141],v[48:49],v[50:51]
v_pk_fma_f32 v[54:55],v[142:143],v[48:49],v[50:51]
v_pk_fma_f32 v[56:57],v[144:145],v[48:49],v[50:51]
v_pk_fma_f32 v[58:59],v[146:147],v[48:49],v[50:51]
v_pk_fma_f32 v[140:141],v[52:53],v[108:109],v[140:141]
v_pk_fma_f32 v[142:143],v[54:55],v[110:111],v[142:143]
v_pk_fma_f32 v[144:145],v[56:57],v[112:113],v[144:145]
v_pk_fma_f32 v[146:147],v[58:59],v[114:115],v[146:147]
v_cvt_pk_bf16_f32 v160,v140,v141
v_cvt_pk_bf16_f32 v161,v142,v143
v_cvt_pk_bf16_f32 v162,v144,v145
v_cvt_pk_bf16_f32 v163,v146,v147
s_waitcnt vmcnt(13)
buffer_store_dword v13,v5,s[36:39],s63 offen offset:0 sc0 sc1
s_mov_b32 s63,0x7fff0000
v_mfma_f32_16x16x32_bf16 v[36:39],v[32:35],v[148:151],0
buffer_store_dwordx4 v[148:151],v1,s[36:39],s57 offen offset:0 sc0 sc1
v_mfma_f32_16x16x32_bf16 v[40:43],v[148:151],v[148:151],0
M 36,v[32:35],152
buffer_store_dwordx4 v[152:155],v1,s[36:39],s57 offen offset:2048 sc0 sc1
M 40,v[152:155],152
M 36,v[32:35],156
buffer_store_dwordx4 v[156:159],v1,s[36:39],s58 offen offset:0 sc0 sc1
M 40,v[156:159],156
M 36,v[32:35],160
buffer_store_dwordx4 v[160:163],v1,s[36:39],s58 offen offset:2048 sc0 sc1
M 40,v[160:163],160
s_nop 5
v_add_f32_dpp v44,v36,v36 row_ror:8 row_mask:0xf bank_mask:0xf
s_nop 0
v_cndmask_b32 v45,v40,v41,s[70:71]
v_cndmask_b32 v46,v42,v43,s[70:71]
ds_write_b32 v2,v44 offset:1024
v_cndmask_b32 v45,v45,v46,s[72:73]
ds_write_b32 v0,v45 offset:1024
L 0
s_barrier
R 52,3,1024
R 56,3,1040
R 60,3,1056
L 0
v_pk_add_f32 v[52:53],v[52:53],v[54:55]
v_pk_add_f32 v[56:57],v[56:57],v[58:59]
v_pk_add_f32 v[60:61],v[60:61],v[62:63]
v_add_f32 v52,v52,v53
v_pk_add_f32 v[56:57],v[56:57],v[60:61]
v_mul_f32 v52,s68,v52
v_add_f32 v53,v56,v57
v_mul_f32 v53,s68,v53
v_fma_f32 v53,-v52,v52,v53
v_max_f32 v53,0,v53
v_add_f32 v54,s69,v53
v_fma_f32 v55,s69,v54,v53
v_rsq_f32 v65,v54
v_rsq_f32 v54,v55
v_mul_f32_e64 v64,-v52,v65
v_mul_f32 v63,v55,v54
v_mul_f32 v61,s76,v54
v_mul_f32 v55,v54,v54
v_mul_f32 v62,s77,v55
v_mul_f32 v62,v62,v61
v_cndmask_b32 v60,v52,v64,s[74:75]
v_cndmask_b32 v61,v61,v65,s[74:75]
buffer_store_dwordx4 v[60:63],v4,s[36:39],s59 offen offset:0 sc0 sc1
s_mov_b32 s63,s60
s_lshr_b32 s78,s45,1
s_and_b32 s79,s45,1
s_lshl_b32 s79,s79,7
s_lshl_b32 s57,s78,15
s_add_u32 s57,s57,s79
s_add_u32 s57,s57,12582912
s_add_u32 s58,s57,4096
s_lshl_b32 s59,s78,9
s_add_u32 s59,s59,s79
s_add_u32 s59,s59,107020288
s_lshl_b32 s60,s45,9
s_add_u32 s60,s60,s66
s_add_u32 s60,s60,131072
s_add_u32 s45,s45,s46
s_lshr_b32 s78,s45,1
s_and_b32 s79,s45,1
s_lshl_b32 s79,s79,7
s_lshl_b32 s55,s78,15
s_add_u32 s55,s55,s79
s_mul_i32 s56,s45,16384
)"
R"(s_add_u32 s56,s56,98566144
s_lshl_b32 s61,s78,11
s_add_u32 s61,s61,110587904
s_add_u32 s78,s45,s46
s_lshr_b32 s78,s78,1
s_lshl_b32 s62,s78,11
s_add_u32 s62,s62,110587904
s_waitcnt vmcnt(6)
s_branch Lr_it0_%=
Lr_tb1_%=:
v_add_f32 v53,s69,v53
v_rsq_f32 v48,v53
s_nop 0
v_mul_f32_e64 v50,-v52,v48
v_mov_b32 v49,v48
v_mov_b32 v51,v50
v_pk_fma_f32 v[52:53],v[116:117],v[48:49],v[50:51]
v_pk_fma_f32 v[54:55],v[118:119],v[48:49],v[50:51]
v_pk_fma_f32 v[56:57],v[120:121],v[48:49],v[50:51]
v_pk_fma_f32 v[58:59],v[122:123],v[48:49],v[50:51]
s_add_u32 s78,s55,71303168
buffer_load_dwordx4 v[164:167],v1,s[36:39],s78 offen offset:0 sc1
buffer_load_dwordx4 v[168:171],v1,s[36:39],s78 offen offset:2048 sc1
s_add_u32 s78,s55,71307264
buffer_load_dwordx4 v[172:175],v1,s[36:39],s78 offen offset:0 sc1
buffer_load_dwordx4 v[176:179],v1,s[36:39],s78 offen offset:2048 sc1
v_pk_fma_f32 v[116:117],v[52:53],v[84:85],v[116:117]
v_pk_fma_f32 v[118:119],v[54:55],v[86:87],v[118:119]
v_pk_fma_f32 v[120:121],v[56:57],v[88:89],v[120:121]
v_pk_fma_f32 v[122:123],v[58:59],v[90:91],v[122:123]
v_cvt_pk_bf16_f32 v148,v116,v117
v_cvt_pk_bf16_f32 v149,v118,v119
v_cvt_pk_bf16_f32 v150,v120,v121
v_cvt_pk_bf16_f32 v151,v122,v123
v_pk_fma_f32 v[52:53],v[124:125],v[48:49],v[50:51]
v_pk_fma_f32 v[54:55],v[126:127],v[48:49],v[50:51]
v_pk_fma_f32 v[56:57],v[128:129],v[48:49],v[50:51]
v_pk_fma_f32 v[58:59],v[130:131],v[48:49],v[50:51]
s_add_u32 s78,s55,89128960
buffer_load_dwordx4 v[180:183],v1,s[36:39],s78 offen offset:0 sc1
buffer_load_dwordx4 v[184:187],v1,s[36:39],s78 offen offset:2048 sc1
s_add_u32 s78,s55,89133056
buffer_load_dwordx4 v[188:191],v1,s[36:39],s78 offen offset:0 sc1
buffer_load_dwordx4 v[192:195],v1,s[36:39],s78 offen offset:2048 sc1
v_pk_fma_f32 v[124:125],v[52:53],v[92:93],v[124:125]
v_pk_fma_f32 v[126:127],v[54:55],v[94:95],v[126:127]
v_pk_fma_f32 v[128:129],v[56:57],v[96:97],v[128:129]
v_pk_fma_f32 v[130:131],v[58:59],v[98:99],v[130:131]
v_cvt_pk_bf16_f32 v152,v124,v125
v_cvt_pk_bf16_f32 v153,v126,v127
v_cvt_pk_bf16_f32 v154,v128,v129
v_cvt_pk_bf16_f32 v155,v130,v131
v_pk_fma_f32 v[52:53],v[132:133],v[48:49],v[50:51]
v_pk_fma_f32 v[54:55],v[134:135],v[48:49],v[50:51]
v_pk_fma_f32 v[56:57],v[136:137],v[48:49],v[50:51]
v_pk_fma_f32 v[58:59],v[138:139],v[48:49],v[50:51]
s_add_u32 m0,s65,0
s_nop 0
D 9,56,0
D 9,56,1024
D 9,56,2048
D 9,56,3072
v_pk_fma_f32 v[132:133],v[52:53],v[100:101],v[132:133]
v_pk_fma_f32 v[134:135],v[54:55],v[102:103],v[134:135]
v_pk_fma_f32 v[136:137],v[56:57],v[104:105],v[136:137]
v_pk_fma_f32 v[138:139],v[58:59],v[106:107],v[138:139]
v_cvt_pk_bf16_f32 v156,v132,v133
v_cvt_pk_bf16_f32 v157,v134,v135
v_cvt_pk_bf16_f32 v158,v136,v137
v_cvt_pk_bf16_f32 v159,v138,v139
v_pk_fma_f32 v[52:53],v[140:141],v[48:49],v[50:51]
v_pk_fma_f32 v[54:55],v[142:143],v[48:49],v[50:51]
v_pk_fma_f32 v[56:57],v[144:145],v[48:49],v[50:51]
v_pk_fma_f32 v[58:59],v[146:147],v[48:49],v[50:51]
v_pk_fma_f32 v[140:141],v[52:53],v[108:109],v[140:141]
v_pk_fma_f32 v[142:143],v[54:55],v[110:111],v[142:143]
v_pk_fma_f32 v[144:145],v[56:57],v[112:113],v[144:145]
v_pk_fma_f32 v[146:147],v[58:59],v[114:115],v[146:147]
v_cvt_pk_bf16_f32 v160,v140,v141
v_cvt_pk_bf16_f32 v161,v142,v143
v_cvt_pk_bf16_f32 v162,v144,v145
v_cvt_pk_bf16_f32 v163,v146,v147
v_mfma_f32_16x16x32_bf16 v[36:39],v[32:35],v[148:151],0
buffer_load_dword v14,v6,s[36:39],s62 offen offset:0 sc1
buffer_store_dwordx4 v[148:151],v1,s[36:39],s57 offen offset:0 sc0 sc1
v_mfma_f32_16x16x32_bf16 v[40:43],v[148:151],v[148:151],0
M 36,v[32:35],152
buffer_store_dwordx4 v[152:155],v1,s[36:39],s57 offen offset:2048 sc0 sc1
M 40,v[152:155],152
M 36,v[32:35],156
buffer_store_dwordx4 v[156:159],v1,s[36:39],s58 offen offset:0 sc0 sc1
M 40,v[156:159],156
M 36,v[32:35],160
buffer_store_dwordx4 v[160:163],v1,s[36:39],s58 offen offset:2048 sc0 sc1
M 40,v[160:163],160
s_nop 5
v_add_f32_dpp v44,v36,v36 row_ror:8 row_mask:0xf bank_mask:0xf
s_nop 0
v_cndmask_b32 v45,v40,v41,s[70:71]
v_cndmask_b32 v46,v42,v43,s[70:71]
ds_write_b32 v2,v44 offset:1024
v_cndmask_b32 v45,v45,v46,s[72:73]
ds_write_b32 v0,v45 offset:1024
L 0
s_barrier
R 52,3,1024
R 56,3,1040
R 60,3,1056
L 0
v_pk_add_f32 v[52:53],v[52:53],v[54:55]
v_pk_add_f32 v[56:57],v[56:57],v[58:59]
v_pk_add_f32 v[60:61],v[60:61],v[62:63]
v_add_f32 v52,v52,v53
v_pk_add_f32 v[56:57],v[56:57],v[60:61]
v_mul_f32 v52,s68,v52
v_add_f32 v53,v56,v57
v_mul_f32 v53,s68,v53
v_fma_f32 v53,-v52,v52,v53
v_max_f32 v53,0,v53
v_add_f32 v54,s69,v53
v_fma_f32 v55,s69,v54,v53
v_rsq_f32 v65,v54
v_rsq_f32 v54,v55
v_mul_f32_e64 v64,-v52,v65
v_mul_f32 v63,v55,v54
v_mul_f32 v61,s76,v54
v_mul_f32 v55,v54,v54
v_mul_f32 v62,s77,v55
v_mul_f32 v62,v62,v61
v_cndmask_b32 v60,v52,v64,s[74:75]
v_cndmask_b32 v61,v61,v65,s[74:75]
buffer_store_dwordx4 v[60:63],v4,s[36:39],s59 offen offset:0 sc0 sc1
s_mov_b32 s63,s60
s_lshr_b32 s78,s45,1
s_and_b32 s79,s45,1
s_lshl_b32 s79,s79,7
s_lshl_b32 s57,s78,15
s_add_u32 s57,s57,s79
s_add_u32 s57,s57,12582912
s_add_u32 s58,s57,4096
s_lshl_b32 s59,s78,9
s_add_u32 s59,s59,s79
s_add_u32 s59,s59,107020288
s_lshl_b32 s60,s45,9
s_add_u32 s60,s60,s66
s_add_u32 s60,s60,131072
s_add_u32 s45,s45,s46
s_lshr_b32 s78,s45,1
s_and_b32 s79,s45,1
s_lshl_b32 s79,s79,7
s_lshl_b32 s55,s78,15
s_add_u32 s55,s55,s79
s_mul_i32 s56,s45,16384
s_add_u32 s56,s56,98566144
s_lshl_b32 s61,s78,11
s_add_u32 s61,s61,110587904
s_add_u32 s78,s45,s46
s_lshr_b32 s78,s78,1
s_lshl_b32 s62,s78,11
s_add_u32 s62,s62,110587904
s_waitcnt vmcnt(5)
s_branch Lr_it0_%=
Lr_tc1_%=:
s_add_u32 s78,s55,71303168
buffer_load_dwordx4 v[164:167],v1,s[36:39],s78 offen offset:0 sc1
buffer_load_dwordx4 v[168:171],v1,s[36:39],s78 offen offset:2048 sc1
s_add_u32 s78,s55,71307264
buffer_load_dwordx4 v[172:175],v1,s[36:39],s78 offen offset:0 sc1
buffer_load_dwordx4 v[176:179],v1,s[36:39],s78 offen offset:2048 sc1
s_add_u32 s78,s55,89128960
buffer_load_dwordx4 v[180:183],v1,s[36:39],s78 offen offset:0 sc1
buffer_load_dwordx4 v[184:187],v1,s[36:39],s78 offen offset:2048 sc1
s_add_u32 s78,s55,89133056
buffer_load_dwordx4 v[188:191],v1,s[36:39],s78 offen offset:0 sc1
buffer_load_dwordx4 v[192:195],v1,s[36:39],s78 offen offset:2048 sc1
s_add_u32 m0,s65,0
s_nop 0
D 9,56,0
D 9,56,1024
D 9,56,2048
D 9,56,3072
buffer_load_dword v14,v6,s[36:39],s62 offen offset:0 sc1
s_waitcnt vmcnt(13)
buffer_store_dword v13,v5,s[36:39],s63 offen offset:0 sc0 sc1
s_mov_b32 s63,0x7fff0000
s_lshr_b32 s78,s45,1
s_and_b32 s79,s45,1
s_lshl_b32 s79,s79,7
s_lshl_b32 s57,s78,15
s_add_u32 s57,s57,s79
s_add_u32 s57,s57,12582912
s_add_u32 s58,s57,4096
s_lshl_b32 s59,s78,9
s_add_u32 s59,s59,s79
s_add_u32 s59,s59,107020288
s_lshl_b32 s60,s45,9
s_add_u32 s60,s60,s66
s_add_u32 s60,s60,131072
s_add_u32 s45,s45,s46
s_lshr_b32 s78,s45,1
s_and_b32 s79,s45,1
s_lshl_b32 s79,s79,7
s_lshl_b32 s55,s78,15
s_add_u32 s55,s55,s79
s_mul_i32 s56,s45,16384
s_add_u32 s56,s56,98566144
s_lshl_b32 s61,s78,11
s_add_u32 s61,s61,110587904
s_add_u32 s78,s45,s46
s_lshr_b32 s78,s78,1
s_lshl_b32 s62,s78,11
s_add_u32 s62,s62,110587904
s_waitcnt vmcnt(1)
s_branch Lr_it0_%=
Lr_done_%=:
s_waitcnt vmcnt(0) lgkmcnt(0)
s_barrier
Lcode_r_e:
.purgem M
.purgem R
.purgem T
.purgem D
.purgem L
)"
  :: "s"(ws), "s"(sWave), "s"(sG0), "s"(sStep), "s"(sOutOff), "s"(sAuxOff), "s"(sBuf), "s"(sRk), "s"(sExp), "v"(vRP), "v"(vRF), "v"(vRwr), "v"(vRrd), "v"(vRS), "v"(vFlag), "v"(vL4), "v"(vLY), "v"(vQ), "v"(vY0), "v"(vTr), "v"(vSa), "v"(vSb), "s"(q) : "v0", "v1", "v2", "v3", "v4", "v5", "v6", "v7", "v8", "v9", "v10", "v11", "v12", "v13", "v14", "v15", "v16", "v17", "v18", "v19", "v20", "v21", "v22", "v23", "v24", "v25", "v26", "v27", "v28", "v29", "v30", "v31", "v32", "v33", "v34", "v35", "v36", "v37", "v38", "v39", "v40", "v41", "v42", "v43", "v44", "v45", "v46", "v47", "v48", "v49", "v50", "v51", "v52", "v53", "v54", "v55", "v56", "v57", "v58", "v59", "v60", "v61", "v62", "v63", "v64", "v65", "v66", "v67", "v68", "v69", "v70", "v71", "v72", "v73", "v74", "v75", "v76", "v77", "v78", "v79", "v80", "v81", "v82", "v83", "v84", "v85", "v86", "v87", "v88", "v89", "v90", "v91", "v92", "v93", "v94", "v95", "v96", "v97", "v98", "v99", "v100", "v101", "v102", "v103", "v104", "v105", "v106", "v107", "v108", "v109", "v110", "v111", "v112", "v113", "v114", "v115", "v116", "v117", "v118", "v119", "v120", "v121", "v122", "v123", "v124", "v125", "v126", "v127", "v128", "v129", "v130", "v131", "v132", "v133", "v134", "v135", "v136", "v137", "v138", "v139", "v140", "v141", "v142", "v143", "v144", "v145", "v146", "v147", "v148", "v149", "v150", "v151", "v152", "v153", "v154", "v155", "v156", "v157", "v158", "v159", "v160", "v161", "v162", "v163", "v164", "v165", "v166", "v167", "v168", "v169", "v170", "v171", "v172", "v173", "v174", "v175", "v176", "v177", "v178", "v179", "v180", "v181", "v182", "v183", "v184", "v185", "v186", "v187", "v188", "v189", "v190", "v191", "v192", "v193", "v194", "v195", "v196", "v197", "v198", "v199", "v200", "v201", "v202", "v203", "v204", "v205", "v206", "v207", "v208", "v209", "v210", "v211", "v212", "v213", "v214", "v215", "v216", "v217", "v218", "v219", "v220", "v221", "v222", "v223", "v224", "v225", "v226", "v227", "v228", "v229", "v230", "v231", "s36", "s37", "s38", "s39", "s40", "s41", "s42", "s43", "s44", "s45", "s46", "s47", "s48", "s49", "s50", "s51", "s52", "s53", "s54", "s55", "s56", "s57", "s58", "s59", "s60", "s61", "s62", "s63", "s64", "s65", "s66", "s67", "s68", "s69", "s70", "s71", "s72", "s73", "s74", "s75", "s76", "s77", "s78", "s79", "s80", "s81", "s82", "s83", "s84", "s85", "s86", "s87", "s88", "s89", "s90", "s91", "s92", "s93", "s94", "s95", "s96", "s97", "vcc", "scc", "memory");
  }
  CTS(9);
#define COMMON(NF, FOFF, FSTR, SNAP, LDW, NL, TOFF)                                                                                            \
  LANE_ID(ln);                                                                                                                       \
  const int l15 = ln & 15, lg = ln >> 4, lq = l15 >> 2, lp = ln & 3, rr = 8 * lg + lq, fj = ln & 31;                                 \
  const unsigned vL16 = ln * 16, vFlag = (ln < 32 && wave == 0) ? ln * 4 : OOB;                                                      \
  const unsigned vSnapOff = (wave == 0 && fj < (NF)) ? (FOFF) + (ln >> 5) * sStep * (FSTR) : OOB;                                    \
  const unsigned vSnapRd = (SNAP) + (fj < (NF) ? ln : ln & 32) * 4;                                                                  \
  const unsigned vWd0 = (ln >> 3) * (LDW) + ((ln & 7) ^ swzB(ln >> 3)) * 16, vWd1 = ((ln >> 3) + 8) * (LDW) + ((ln & 7) ^ swzB(8 + (ln >> 3))) * 16; \
  const unsigned vWr0 = (wave >> 1) * 8192 + rr * 128 + (((wave & 1) * 4 + lp) ^ swzB(rr)) * 16, vWr1 = vWr0 + 8;                    \
  const unsigned vWr2 = 16384 + rr * 128 + ((2 * wave + (lp >> 1)) ^ swzB(rr)) * 16 + (lp & 1) * 8;                                  \
  const unsigned sWave = wave, sRk = kind < 3 ? 0x7fff0000u : OFF_FY + (256 + (kind == 4 ? j4 : 80 + j3)) * 128, sG0 = rep, sDbg = sDbg0
  if (kind == 3 || kind == 5) {
    const unsigned sStep = kind == 3 ? 5 : G3OR;
    COMMON(8, fj * 128, 1024, 131072, 7168, 256, 0);
    const unsigned vOutS = (lg & 1) ? OOB : (((wave & 1) * 2 + (lg >> 1)) * 16 + l15) * 16 + (4 + (wave >> 1) - wave) * 1024;
    const unsigned vAux0 = (ln & 7) * 128 + (4 * wave + lg) * 8 + ((ln >> 3) & 1) * 4, vAux1 = 139264 + (l15 == 0 ? (4 * wave + lg) * 16 : 512 + ln * 16);
    const unsigned vAux2 = 139264 + l15 * 16, vAux3 = 0, vAn = 0, vAe = 0;
    const unsigned sOutOff = OFF_G + (slice * 6 + wave) * 1024, sFoutOff = OFF_FG + slice * 128, sAuxOff = 0, sWw = 16 * wave * 7168 + slice * 384, sBuf = 0;
    if (kind == 3) { asm volatile(
R"(.macro M d,w,x
v_mfma_f32_16x16x32_bf16 v[\d:\d+3], \w, v[\x:\x+3], v[\d:\d+3]
.endm
.macro R d,a,o
ds_read_b128 v[\d:\d+3], v\a offset:\o
.endm
.macro T d,a,o
ds_read_b64_tr_b16 \d, v\a offset:\o
.endm
.macro D a,s,o
buffer_load_dwordx4 v\a, s[36:39], s\s offen offset:\o sc1 lds
.endm
.macro L c
s_waitcnt lgkmcnt(\c)
.endm
Lcode_g3_b:
s_mov_b64 s[36:37],%0
s_mov_b32 s38,111243392
s_mov_b32 s39,0x00020000
s_mov_b32 s44,%1
s_mov_b32 s45,%2
s_mov_b32 s46,%3
s_mov_b32 s47,%4
s_mov_b32 s48,%5
s_mov_b32 s49,%6
s_mov_b32 s50,%7
s_mov_b32 s51,%8
s_mov_b32 s93,%9
v_mov_b32 v0,%10
v_mov_b32 v3,%11
v_mov_b32 v5,%12
v_mov_b32 v6,%13
v_mov_b32 v18,%14
v_mov_b32 v19,%15
v_mov_b32 v20,%16
v_mov_b32 v21,%17
v_mov_b32 v22,%18
v_mov_b32 v10,%19
v_mov_b32 v11,%20
v_mov_b32 v12,%21
v_mov_b32 v13,%22
v_mov_b32 v14,%23
v_mov_b32 v15,%24
v_mov_b32 v16,%25
s_mov_b64 s[40:41],%26
s_mov_b32 s42,7340032
s_mov_b32 s43,0x00020000
s_lshl_b32 s57,s44,11
s_mov_b32 s79,111243264
buffer_load_dword v4,off,s[36:39],s79 sc1
s_add_u32 s55,s50,0
s_add_u32 m0,s57,0
s_nop 0
buffer_load_dwordx4 v18,s[40:43],s55 offen offset:0  lds
s_add_u32 m0,s57,1024
s_nop 0
buffer_load_dwordx4 v19,s[40:43],s55 offen offset:0  lds
s_add_u32 m0,s57,8064
s_nop 0
buffer_load_dwordx4 v18,s[40:43],s55 offen offset:128  lds
s_add_u32 m0,s57,9088
s_nop 0
buffer_load_dwordx4 v19,s[40:43],s55 offen offset:128  lds
s_add_u32 m0,s57,16128
s_nop 0
buffer_load_dwordx4 v18,s[40:43],s55 offen offset:256  lds
s_add_u32 m0,s57,17152
s_nop 0
buffer_load_dwordx4 v19,s[40:43],s55 offen offset:256  lds
s_add_u32 s55,s50,458752
s_add_u32 m0,s57,24576
s_nop 0
buffer_load_dwordx4 v18,s[40:43],s55 offen offset:0  lds
s_add_u32 m0,s57,25600
s_nop 0
buffer_load_dwordx4 v19,s[40:43],s55 offen offset:0  lds
s_add_u32 m0,s57,32640
s_nop 0
buffer_load_dwordx4 v18,s[40:43],s55 offen offset:128  lds
s_add_u32 m0,s57,33664
s_nop 0
buffer_load_dwordx4 v19,s[40:43],s55 offen offset:128  lds
s_add_u32 m0,s57,40704
s_nop 0
buffer_load_dwordx4 v18,s[40:43],s55 offen offset:256  lds
s_add_u32 m0,s57,41728
s_nop 0
buffer_load_dwordx4 v19,s[40:43],s55 offen offset:256  lds
s_add_u32 s55,s50,917504
s_add_u32 m0,s57,49152
s_nop 0
buffer_load_dwordx4 v18,s[40:43],s55 offen offset:0  lds
s_add_u32 m0,s57,50176
s_nop 0
buffer_load_dwordx4 v19,s[40:43],s55 offen offset:0  lds
s_add_u32 m0,s57,57216
s_nop 0
buffer_load_dwordx4 v18,s[40:43],s55 offen offset:128  lds
s_add_u32 m0,s57,58240
s_nop 0
buffer_load_dwordx4 v19,s[40:43],s55 offen offset:128  lds
s_add_u32 m0,s57,65280
s_nop 0
buffer_load_dwordx4 v18,s[40:43],s55 offen offset:256  lds
s_add_u32 m0,s57,66304
s_nop 0
buffer_load_dwordx4 v19,s[40:43],s55 offen offset:256  lds
s_add_u32 s55,s50,1376256
s_add_u32 m0,s57,73728
s_nop 0
buffer_load_dwordx4 v18,s[40:43],s55 offen offset:0  lds
s_add_u32 m0,s57,74752
s_nop 0
buffer_load_dwordx4 v19,s[40:43],s55 offen offset:0  lds
s_add_u32 m0,s57,81792
s_nop 0
buffer_load_dwordx4 v18,s[40:43],s55 offen offset:128  lds
s_add_u32 m0,s57,82816
s_nop 0
buffer_load_dwordx4 v19,s[40:43],s55 offen offset:128  lds
s_add_u32 m0,s57,89856
s_nop 0
buffer_load_dwordx4 v18,s[40:43],s55 offen offset:256  lds
s_add_u32 m0,s57,90880
s_nop 0
buffer_load_dwordx4 v19,s[40:43],s55 offen offset:256  lds
s_add_u32 s55,s50,1835008
s_add_u32 m0,s57,98304
s_nop 0
buffer_load_dwordx4 v18,s[40:43],s55 offen offset:0  lds
s_add_u32 m0,s57,99328
s_nop 0
buffer_load_dwordx4 v19,s[40:43],s55 offen offset:0  lds
s_add_u32 m0,s57,106368
s_nop 0
buffer_load_dwordx4 v18,s[40:43],s55 offen offset:128  lds
s_add_u32 m0,s57,107392
s_nop 0
buffer_load_dwordx4 v19,s[40:43],s55 offen offset:128  lds
s_add_u32 m0,s57,114432
s_nop 0
buffer_load_dwordx4 v18,s[40:43],s55 offen offset:256  lds
s_add_u32 m0,s57,115456
s_nop 0
buffer_load_dwordx4 v19,s[40:43],s55 offen offset:256  lds
s_waitcnt vmcnt(30)
s_barrier
v_readfirstlane_b32 s53,v4
s_nop 3
s_add_u32 s53,s53,1
s_cmp_eq_u32 s53,0
s_cselect_b32 s53,1,s53
v_mov_b32 v4,s53
s_waitcnt vmcnt(24)
L 0
s_barrier
s_add_u32 s55,s50,2293760
s_add_u32 m0,s57,122880
s_nop 0
buffer_load_dwordx4 v18,s[40:43],s55 offen offset:0  lds
s_add_u32 m0,s57,123904
s_nop 0
buffer_load_dwordx4 v19,s[40:43],s55 offen offset:0  lds
s_add_u32 m0,s57,130944
s_nop 0
buffer_load_dwordx4 v18,s[40:43],s55 offen offset:128  lds
s_add_u32 m0,s57,131968
s_nop 0
buffer_load_dwordx4 v19,s[40:43],s55 offen offset:128  lds
s_add_u32 m0,s57,139008
s_nop 0
buffer_load_dwordx4 v18,s[40:43],s55 offen offset:256  lds
s_add_u32 m0,s57,140032
s_nop 0
buffer_load_dwordx4 v19,s[40:43],s55 offen offset:256  lds
v_add_u32 v26,0,v20
v_add_u32 v27,0,v21
v_add_u32 v28,0,v22
T a[0:1],26,0
T a[2:3],26,512
T a[4:5],27,0
T a[6:7],27,512
T a[8:9],28,0
T a[10:11],28,512
T a[12:13],26,4096
T a[14:15],26,4608
T a[16:17],27,4096
T a[18:19],27,4608
T a[20:21],28,4096
T a[22:23],28,4608
s_waitcnt vmcnt(24)
L 0
s_barrier
s_add_u32 s55,s50,2752512
s_add_u32 m0,s57,0
s_nop 0
buffer_load_dwordx4 v18,s[40:43],s55 offen offset:0  lds
s_add_u32 m0,s57,1024
s_nop 0
buffer_load_dwordx4 v19,s[40:43],s55 offen offset:0  lds
s_add_u32 m0,s57,8064
s_nop 0
buffer_load_dwordx4 v18,s[40:43],s55 offen offset:128  lds
s_add_u32 m0,s57,9088
s_nop 0
buffer_load_dwordx4 v19,s[40:43],s55 offen offset:128  lds
s_add_u32 m0,s57,16128
s_nop 0
buffer_load_dwordx4 v18,s[40:43],s55 offen offset:256  lds
s_add_u32 m0,s57,17152
s_nop 0
buffer_load_dwordx4 v19,s[40:43],s55 offen offset:256  lds
v_add_u32 v26,24576,v20
v_add_u32 v27,24576,v21
v_add_u32 v28,24576,v22
T a[24:25],26,0
T a[26:27],26,512
T a[28:29],27,0
T a[30:31],27,512
T a[32:33],28,0
T a[34:35],28,512
T a[36:37],26,4096
T a[38:39],26,4608
T a[40:41],27,4096
T a[42:43],27,4608
T a[44:45],28,4096
T a[46:47],28,4608
s_waitcnt vmcnt(24)
L 0
s_barrier
s_add_u32 s55,s50,3211264
s_add_u32 m0,s57,24576
s_nop 0
buffer_load_dwordx4 v18,s[40:43],s55 offen offset:0  lds
s_add_u32 m0,s57,25600
s_nop 0
buffer_load_dwordx4 v19,s[40:43],s55 offen offset:0  lds
s_add_u32 m0,s57,32640
s_nop 0
buffer_load_dwordx4 v18,s[40:43],s55 offen offset:128  lds
s_add_u32 m0,s57,33664
s_nop 0
buffer_load_dwordx4 v19,s[40:43],s55 offen offset:128  lds
s_add_u32 m0,s57,40704
s_nop 0
buffer_load_dwordx4 v18,s[40:43],s55 offen offset:256  lds
s_add_u32 m0,s57,41728
s_nop 0
buffer_load_dwordx4 v19,s[40:43],s55 offen offset:256  lds
v_add_u32 v26,49152,v20
v_add_u32 v27,49152,v21
v_add_u32 v28,49152,v22
T a[48:49],26,0
T a[50:51],26,512
T a[52:53],27,0
T a[54:55],27,512
T a[56:57],28,0
T a[58:59],28,512
T a[60:61],26,4096
T a[62:63],26,4608
T a[64:65],27,4096
T a[66:67],27,4608
T a[68:69],28,4096
T a[70:71],28,4608
s_waitcnt vmcnt(24)
L 0
s_barrier
s_add_u32 s55,s50,3670016
s_add_u32 m0,s57,49152
s_nop 0
buffer_load_dwordx4 v18,s[40:43],s55 offen offset:0  lds
s_add_u32 m0,s57,50176
s_nop 0
buffer_load_dwordx4 v19,s[40:43],s55 offen offset:0  lds
s_add_u32 m0,s57,57216
s_nop 0
buffer_load_dwordx4 v18,s[40:43],s55 offen offset:128  lds
s_add_u32 m0,s57,58240
s_nop 0
buffer_load_dwordx4 v19,s[40:43],s55 offen offset:128  lds
s_add_u32 m0,s57,65280
s_nop 0
buffer_load_dwordx4 v18,s[40:43],s55 offen offset:256  lds
s_add_u32 m0,s57,66304
s_nop 0
buffer_load_dwordx4 v19,s[40:43],s55 offen offset:256  lds
v_add_u32 v26,73728,v20
v_add_u32 v27,73728,v21
v_add_u32 v28,73728,v22
T a[72:73],26,0
T a[74:75],26,512
T a[76:77],27,0
T a[78:79],27,512
T a[80:81],28,0
T a[82:83],28,512
T a[84:85],26,4096
T a[86:87],26,4608
T a[88:89],27,4096
T a[90:91],27,4608
T a[92:93],28,4096
T a[94:95],28,4608
s_waitcnt vmcnt(24)
L 0
s_barrier
s_add_u32 s55,s50,4128768
s_add_u32 m0,s57,73728
s_nop 0
buffer_load_dwordx4 v18,s[40:43],s55 offen offset:0  lds
s_add_u32 m0,s57,74752
s_nop 0
buffer_load_dwordx4 v19,s[40:43],s55 offen offset:0  lds
s_add_u32 m0,s57,81792
s_nop 0
buffer_load_dwordx4 v18,s[40:43],s55 offen offset:128  lds
s_add_u32 m0,s57,82816
s_nop 0
buffer_load_dwordx4 v19,s[40:43],s55 offen offset:128  lds
s_add_u32 m0,s57,89856
s_nop 0
buffer_load_dwordx4 v18,s[40:43],s55 offen offset:256  lds
s_add_u32 m0,s57,90880
s_nop 0
buffer_load_dwordx4 v19,s[40:43],s55 offen offset:256  lds
v_add_u32 v26,98304,v20
v_add_u32 v27,98304,v21
v_add_u32 v28,98304,v22
T a[96:97],26,0
T a[98:99],26,512
T a[100:101],27,0
T a[102:103],27,512
T a[104:105],28,0
T a[106:107],28,512
T a[108:109],26,4096
T a[110:111],26,4608
T a[112:113],27,4096
T a[114:115],27,4608
T a[116:117],28,4096
T a[118:119],28,4608
s_waitcnt vmcnt(24)
L 0
s_barrier
s_add_u32 s55,s50,4587520
s_add_u32 m0,s57,98304
s_nop 0
buffer_load_dwordx4 v18,s[40:43],s55 offen offset:0  lds
s_add_u32 m0,s57,99328
s_nop 0
buffer_load_dwordx4 v19,s[40:43],s55 offen offset:0  lds
s_add_u32 m0,s57,106368
s_nop 0
buffer_load_dwordx4 v18,s[40:43],s55 offen offset:128  lds
s_add_u32 m0,s57,107392
s_nop 0
buffer_load_dwordx4 v19,s[40:43],s55 offen offset:128  lds
s_add_u32 m0,s57,114432
s_nop 0
buffer_load_dwordx4 v18,s[40:43],s55 offen offset:256  lds
s_add_u32 m0,s57,115456
s_nop 0
buffer_load_dwordx4 v19,s[40:43],s55 offen offset:256  lds
v_add_u32 v26,122880,v20
v_add_u32 v27,122880,v21
v_add_u32 v28,122880,v22
T a[120:121],26,0
T a[122:123],26,512
T a[124:125],27,0
T a[126:127],27,512
T a[128:129],28,0
T a[130:131],28,512
T a[132:133],26,4096
T a[134:135],26,4608
T a[136:137],27,4096
T a[138:139],27,4608
T a[140:141],28,4096
T a[142:143],28,4608
s_waitcnt vmcnt(24)
L 0
s_barrier
s_add_u32 s55,s50,5046272
s_add_u32 m0,s57,122880
s_nop 0
buffer_load_dwordx4 v18,s[40:43],s55 offen offset:0  lds
s_add_u32 m0,s57,123904
s_nop 0
buffer_load_dwordx4 v19,s[40:43],s55 offen offset:0  lds
s_add_u32 m0,s57,130944
s_nop 0
buffer_load_dwordx4 v18,s[40:43],s55 offen offset:128  lds
s_add_u32 m0,s57,131968
s_nop 0
buffer_load_dwordx4 v19,s[40:43],s55 offen offset:128  lds
s_add_u32 m0,s57,139008
s_nop 0
buffer_load_dwordx4 v18,s[40:43],s55 offen offset:256  lds
s_add_u32 m0,s57,140032
s_nop 0
buffer_load_dwordx4 v19,s[40:43],s55 offen offset:256  lds
v_add_u32 v26,0,v20
v_add_u32 v27,0,v21
v_add_u32 v28,0,v22
T a[144:145],26,0
T a[146:147],26,512
T a[148:149],27,0
T a[150:151],27,512
T a[152:153],28,0
T a[154:155],28,512
T a[156:157],26,4096
T a[158:159],26,4608
T a[160:161],27,4096
T a[162:163],27,4608
T a[164:165],28,4096
T a[166:167],28,4608
s_waitcnt vmcnt(24)
L 0
s_barrier
s_add_u32 s55,s50,5505024
s_add_u32 m0,s57,0
s_nop 0
buffer_load_dwordx4 v18,s[40:43],s55 offen offset:0  lds
s_add_u32 m0,s57,1024
s_nop 0
buffer_load_dwordx4 v19,s[40:43],s55 offen offset:0  lds
s_add_u32 m0,s57,8064
s_nop 0
buffer_load_dwordx4 v18,s[40:43],s55 offen offset:128  lds
s_add_u32 m0,s57,9088
s_nop 0
buffer_load_dwordx4 v19,s[40:43],s55 offen offset:128  lds
s_add_u32 m0,s57,16128
s_nop 0
buffer_load_dwordx4 v18,s[40:43],s55 offen offset:256  lds
s_add_u32 m0,s57,17152
s_nop 0
buffer_load_dwordx4 v19,s[40:43],s55 offen offset:256  lds
v_add_u32 v26,24576,v20
v_add_u32 v27,24576,v21
v_add_u32 v28,24576,v22
T a[168:169],26,0
T a[170:171],26,512
T a[172:173],27,0
T a[174:175],27,512
T a[176:177],28,0
T a[178:179],28,512
T a[180:181],26,4096
T a[182:183],26,4608
T a[184:185],27,4096
T a[186:187],27,4608
T a[188:189],28,4096
T a[190:191],28,4608
s_waitcnt vmcnt(24)
L 0
s_barrier
s_add_u32 s55,s50,5963776
s_add_u32 m0,s57,24576
s_nop 0
buffer_load_dwordx4 v18,s[40:43],s55 offen offset:0  lds
s_add_u32 m0,s57,25600
s_nop 0
buffer_load_dwordx4 v19,s[40:43],s55 offen offset:0  lds
s_add_u32 m0,s57,32640
s_nop 0
buffer_load_dwordx4 v18,s[40:43],s55 offen offset:128  lds
s_add_u32 m0,s57,33664
s_nop 0
buffer_load_dwordx4 v19,s[40:43],s55 offen offset:128  lds
s_add_u32 m0,s57,40704
s_nop 0
buffer_load_dwordx4 v18,s[40:43],s55 offen offset:256  lds
s_add_u32 m0,s57,41728
s_nop 0
buffer_load_dwordx4 v19,s[40:43],s55 offen offset:256  lds
v_add_u32 v26,49152,v20
v_add_u32 v27,49152,v21
v_add_u32 v28,49152,v22
T a[192:193],26,0
T a[194:195],26,512
T a[196:197],27,0
T a[198:199],27,512
T a[200:201],28,0
T a[202:203],28,512
T a[204:205],26,4096
T a[206:207],26,4608
T a[208:209],27,4096
T a[210:211],27,4608
T a[212:213],28,4096
T a[214:215],28,4608
s_waitcnt vmcnt(24)
L 0
s_barrier
s_add_u32 s55,s50,6422528
s_add_u32 m0,s57,49152
s_nop 0
buffer_load_dwordx4 v18,s[40:43],s55 offen offset:0  lds
s_add_u32 m0,s57,50176
s_nop 0
buffer_load_dwordx4 v19,s[40:43],s55 offen offset:0  lds
s_add_u32 m0,s57,57216
s_nop 0
buffer_load_dwordx4 v18,s[40:43],s55 offen offset:128  lds
s_add_u32 m0,s57,58240
)"
R"(s_nop 0
buffer_load_dwordx4 v19,s[40:43],s55 offen offset:128  lds
s_add_u32 m0,s57,65280
s_nop 0
buffer_load_dwordx4 v18,s[40:43],s55 offen offset:256  lds
s_add_u32 m0,s57,66304
s_nop 0
buffer_load_dwordx4 v19,s[40:43],s55 offen offset:256  lds
v_add_u32 v26,73728,v20
v_add_u32 v27,73728,v21
v_add_u32 v28,73728,v22
T a[216:217],26,0
T a[218:219],26,512
T a[220:221],27,0
T a[222:223],27,512
T a[224:225],28,0
T a[226:227],28,512
T a[228:229],26,4096
T a[230:231],26,4608
T a[232:233],27,4096
T a[234:235],27,4608
T a[236:237],28,4096
T a[238:239],28,4608
s_waitcnt vmcnt(24)
L 0
s_barrier
s_add_u32 s55,s50,6881280
s_add_u32 m0,s57,73728
s_nop 0
buffer_load_dwordx4 v18,s[40:43],s55 offen offset:0  lds
s_add_u32 m0,s57,74752
s_nop 0
buffer_load_dwordx4 v19,s[40:43],s55 offen offset:0  lds
s_add_u32 m0,s57,81792
s_nop 0
buffer_load_dwordx4 v18,s[40:43],s55 offen offset:128  lds
s_add_u32 m0,s57,82816
s_nop 0
buffer_load_dwordx4 v19,s[40:43],s55 offen offset:128  lds
s_add_u32 m0,s57,89856
s_nop 0
buffer_load_dwordx4 v18,s[40:43],s55 offen offset:256  lds
s_add_u32 m0,s57,90880
s_nop 0
buffer_load_dwordx4 v19,s[40:43],s55 offen offset:256  lds
v_add_u32 v26,98304,v20
v_add_u32 v27,98304,v21
v_add_u32 v28,98304,v22
T a[240:241],26,0
T a[242:243],26,512
T a[244:245],27,0
T a[246:247],27,512
T a[248:249],28,0
T a[250:251],28,512
T a[252:253],26,4096
T a[254:255],26,4608
T v[128:129],27,4096
T v[130:131],27,4608
T v[132:133],28,4096
T v[134:135],28,4608
s_waitcnt vmcnt(24)
L 0
s_barrier
v_add_u32 v26,122880,v20
v_add_u32 v27,122880,v21
v_add_u32 v28,122880,v22
T v[136:137],26,0
T v[138:139],26,512
T v[140:141],27,0
T v[142:143],27,512
T v[144:145],28,0
T v[146:147],28,512
T v[148:149],26,4096
T v[150:151],26,4608
T v[152:153],27,4096
T v[154:155],27,4608
T v[156:157],28,4096
T v[158:159],28,4608
s_waitcnt vmcnt(18)
L 0
s_barrier
v_add_u32 v26,0,v20
v_add_u32 v27,0,v21
v_add_u32 v28,0,v22
T v[160:161],26,0
T v[162:163],26,512
T v[164:165],27,0
T v[166:167],27,512
T v[168:169],28,0
T v[170:171],28,512
T v[172:173],26,4096
T v[174:175],26,4608
T v[176:177],27,4096
T v[178:179],27,4608
T v[180:181],28,4096
T v[182:183],28,4608
s_waitcnt vmcnt(12)
L 0
s_barrier
v_add_u32 v26,24576,v20
v_add_u32 v27,24576,v21
v_add_u32 v28,24576,v22
T v[184:185],26,0
T v[186:187],26,512
T v[188:189],27,0
T v[190:191],27,512
T v[192:193],28,0
T v[194:195],28,512
T v[196:197],26,4096
T v[198:199],26,4608
T v[200:201],27,4096
T v[202:203],27,4608
T v[204:205],28,4096
T v[206:207],28,4608
s_waitcnt vmcnt(6)
L 0
s_barrier
v_add_u32 v26,49152,v20
v_add_u32 v27,49152,v21
v_add_u32 v28,49152,v22
T v[208:209],26,0
T v[210:211],26,512
T v[212:213],27,0
T v[214:215],27,512
T v[216:217],28,0
T v[218:219],28,512
T v[220:221],26,4096
T v[222:223],26,4608
T v[224:225],27,4096
T v[226:227],27,4608
T v[228:229],28,4096
T v[230:231],28,4608
s_waitcnt vmcnt(0)
L 0
s_barrier
v_add_u32 v26,73728,v20
v_add_u32 v27,73728,v21
v_add_u32 v28,73728,v22
T v[232:233],26,0
T v[234:235],26,512
T v[236:237],27,0
T v[238:239],27,512
T v[240:241],28,0
T v[242:243],28,512
T v[244:245],26,4096
T v[246:247],26,4608
T v[248:249],27,4096
T v[250:251],27,4608
T v[252:253],28,4096
T v[254:255],28,4608
L 0
s_barrier
v_mov_b32 v42,0x3f803f80
v_mov_b32 v43,0x3f803f80
v_mov_b32 v44,0x3f803f80
v_mov_b32 v45,0x3f803f80
s_nop 1
v_mfma_f32_16x16x32_bf16 v[82:85],a[0:3],v[42:45],0
v_mfma_f32_16x16x32_bf16 v[86:89],a[4:7],v[42:45],0
v_mfma_f32_16x16x32_bf16 v[90:93],a[8:11],v[42:45],0
M 82,a[12:15],42
M 86,a[16:19],42
M 90,a[20:23],42
M 82,a[24:27],42
M 86,a[28:31],42
M 90,a[32:35],42
M 82,a[36:39],42
M 86,a[40:43],42
M 90,a[44:47],42
M 82,a[48:51],42
M 86,a[52:55],42
M 90,a[56:59],42
M 82,a[60:63],42
M 86,a[64:67],42
M 90,a[68:71],42
M 82,a[72:75],42
M 86,a[76:79],42
M 90,a[80:83],42
M 82,a[84:87],42
M 86,a[88:91],42
M 90,a[92:95],42
M 82,a[96:99],42
M 86,a[100:103],42
M 90,a[104:107],42
M 82,a[108:111],42
M 86,a[112:115],42
M 90,a[116:119],42
M 82,a[120:123],42
M 86,a[124:127],42
M 90,a[128:131],42
M 82,a[132:135],42
M 86,a[136:139],42
M 90,a[140:143],42
M 82,a[144:147],42
M 86,a[148:151],42
M 90,a[152:155],42
M 82,a[156:159],42
M 86,a[160:163],42
M 90,a[164:167],42
M 82,a[168:171],42
M 86,a[172:175],42
M 90,a[176:179],42
M 82,a[180:183],42
M 86,a[184:187],42
M 90,a[188:191],42
M 82,a[192:195],42
M 86,a[196:199],42
M 90,a[200:203],42
M 82,a[204:207],42
M 86,a[208:211],42
M 90,a[212:215],42
M 82,a[216:219],42
M 86,a[220:223],42
M 90,a[224:227],42
M 82,a[228:231],42
M 86,a[232:235],42
M 90,a[236:239],42
M 82,a[240:243],42
M 86,a[244:247],42
M 90,a[248:251],42
M 82,a[252:255],42
M 86,v[128:131],42
M 90,v[132:135],42
M 82,v[136:139],42
M 86,v[140:143],42
M 90,v[144:147],42
M 82,v[148:151],42
M 86,v[152:155],42
M 90,v[156:159],42
M 82,v[160:163],42
M 86,v[164:167],42
M 90,v[168:171],42
M 82,v[172:175],42
M 86,v[176:179],42
M 90,v[180:183],42
M 82,v[184:187],42
M 86,v[188:191],42
M 90,v[192:195],42
M 82,v[196:199],42
M 86,v[200:203],42
M 90,v[204:207],42
M 82,v[208:211],42
M 86,v[212:215],42
M 90,v[216:219],42
M 82,v[220:223],42
M 86,v[224:227],42
M 90,v[228:231],42
M 82,v[232:235],42
M 86,v[236:239],42
M 90,v[240:243],42
M 82,v[244:247],42
M 86,v[248:251],42
M 90,v[252:255],42
v_mov_b32 v4,s53
s_mov_b32 s82,0
s_mov_b32 s80,0
s_mov_b32 s54,s45
s_mul_i32 s57,s44,4096
s_mul_i32 s55,s45,32768
s_add_u32 s55,s55,s57
s_add_u32 s55,s55,31457280
s_mul_i32 s57,s44,4096
s_mul_i32 s60,s46,32768
s_mul_i32 s61,s46,1024
s_mul_i32 s62,s46,114688
s_mul_i32 s63,s46,2560
s_mul_i32 s64,s46,1024
s_lshl_b32 s59,s44,8
s_add_u32 s59,s59,131072
s_mul_i32 s58,s45,1024
s_add_u32 s58,s58,109613056
s_mul_i32 s65,s45,114688
s_add_u32 s65,s65,s47
s_mul_i32 s68,s45,2560
s_add_u32 s68,s68,s48
s_mul_i32 s73,s45,1024
s_add_u32 s73,s73,107479040
s_mul_i32 s74,s45,32768
s_add_u32 s74,s74,s49
s_mul_i32 s75,s45,1024
s_add_u32 s75,s75,107479040
s_mov_b32 s66,0x7fff0000
s_mov_b32 s67,0x7fff0000
s_mov_b32 s69,0x7fff0000
s_mov_b32 s76,0x7fff0000
s_mov_b32 s77,0x7fff0000
s_mov_b32 s70,0x7fff0000
s_mov_b32 s71,0x7fff0000
s_mov_b32 s72,0x7fff0000
v_mov_b32 v1,v0
v_add_u32 v2,16384,v0
s_mov_b32 s84,16384
s_mov_b32 s86,0x3a800000
s_mov_b32 s87,0x3727c5ac
s_mov_b32 s88,0xc0135761
s_mov_b32 s89,0x3d372713
s_and_b32 s85,s44,1
s_mul_i32 s85,s85,127
v_lshlrev_b32 v8,22,v0
v_and_b32 v8,0x80000000,v8
v_or_b32 v8,v8,v5
v_add_u32 v2,32768,v0
s_lshl_b32 s84,s57,1
s_add_u32 s84,s84,32768
s_add_u32 m0,s59,4096
s_nop 0
buffer_load_dword v8,s[36:39],s58 offen offset:0 sc1 lds
buffer_store_dword v4,v3,s[36:39],s71 offen offset:0 sc0 sc1
s_add_u32 m0,s59,5120
s_nop 0
buffer_load_dword v8,s[36:39],s58 offen offset:0 sc1 lds
buffer_store_dword v4,v3,s[36:39],s71 offen offset:0 sc0 sc1
s_mov_b32 s83,0
Lg3_poll_%=:
s_add_u32 s78,s83,2
s_and_b32 s78,s78,3
s_lshl_b32 s78,s78,10
s_add_u32 s78,s78,4096
s_add_u32 m0,s59,s78
s_nop 0
buffer_load_dword v8,s[36:39],s58 offen offset:0 sc1 lds
s_and_b32 s78,s83,3
s_lshl_b32 s78,s78,10
v_add_u32 v94,s78,v6
s_waitcnt vmcnt(4)
s_barrier
ds_read_b32 v7,v94 offset:4096
buffer_store_dword v4,v3,s[36:39],s70 offen offset:0 sc0 sc1
s_mov_b32 s70,s71
s_mov_b32 s71,s72
s_mov_b32 s72,0x7fff0000
s_add_u32 s83,s83,1
s_add_u32 s82,s82,1
L 0
v_cmp_eq_u32 vcc,s53,v7
s_nop 0
s_cmp_eq_u32 vcc_lo,-1
s_cbranch_scc1 Lg3_go_%=
s_cmp_lt_u32 s82,20000
s_cbranch_scc1 Lg3_poll_%=
Lg3_go_%=:
buffer_load_dword v114,v11,s[36:39],s73 offen offset:0 sc1
s_add_u32 s73,s73,s64
s_add_u32 m0,s57,0
s_add_u32 s56,s55,0
D 0,56,0
D 0,56,1024
D 0,56,2048
D 0,56,3072
s_add_u32 s58,s58,s61
s_add_u32 m0,s59,2048
s_nop 0
buffer_load_dword v8,s[36:39],s58 offen offset:0 sc1 lds
s_add_u32 m0,s57,16384
s_add_u32 s56,s55,16384
D 0,56,0
D 0,56,1024
D 0,56,2048
D 0,56,3072
s_add_u32 s55,s55,s60
s_sub_u32 s57,s84,s57
s_add_u32 s54,s54,s46
s_waitcnt vmcnt(4)
s_barrier
buffer_store_dword v4,v3,s[36:39],s70 offen offset:0 sc0 sc1
s_min_u32 s71,s71,s72
buffer_store_dword v4,v3,s[36:39],s71 offen offset:0 sc0 sc1
s_mov_b32 s70,0x7fff0000
s_mov_b32 s71,0x7fff0000
s_mov_b32 s72,0x7fff0000
s_cmp_eq_u32 s80,1
s_cbranch_scc1 Lg3_st1_%=
s_branch Lg3_st0_%=
Lg3_gox_%=:
s_add_u32 s58,s58,s61
s_add_u32 m0,s59,2048
s_nop 0
buffer_load_dword v8,s[36:39],s58 offen offset:0 sc1 lds
s_waitcnt vmcnt(0)
s_barrier
buffer_store_dword v4,v3,s[36:39],s70 offen offset:0 sc0 sc1
s_min_u32 s71,s71,s72
buffer_store_dword v4,v3,s[36:39],s71 offen offset:0 sc0 sc1
s_mov_b32 s70,0x7fff0000
s_mov_b32 s71,0x7fff0000
s_mov_b32 s72,0x7fff0000
s_cmp_eq_u32 s80,1
s_cbranch_scc1 Lg3_st1_%=
Lg3_st0_%=:
ds_read_b32 v7,v6 offset:2048
R 42,1,0
R 46,1,1024
R 50,1,2048
R 54,1,3072
R 58,1,4096
L 5
v_cmp_eq_u32 vcc,s53,v7
L 3
s_cmp_eq_u32 vcc_lo,-1
s_cbranch_scc1 Lg3_F0_%=
s_branch Lg3_L0_%=
Lg3_st1_%=:
ds_read_b32 v7,v6 offset:2048
R 42,2,0
R 46,2,1024
R 50,2,2048
R 54,2,3072
R 58,2,4096
L 5
v_cmp_eq_u32 vcc,s53,v7
L 3
s_cmp_eq_u32 vcc_lo,-1
s_cbranch_scc1 Lg3_F1_%=
s_branch Lg3_L1_%=
Lg3_F0_%=:
v_mfma_f32_16x16x32_bf16 v[26:29],a[8:11],v[42:45],0
s_add_u32 m0,s57,0
R 62,1,5120
v_add_f32_dpp v115,v114,v114 quad_perm:[1,0,3,2] row_mask:0xf bank_mask:0xf
M 26,a[20:23],46
s_add_u32 s56,s55,0
buffer_load_dword v114,v11,s[36:39],s73 offen offset:0 sc1
v_mfma_f32_16x16x32_bf16 v[22:25],a[4:7],v[42:45],0
s_add_u32 s58,s58,s61
D 0,56,0
M 22,a[16:19],46
s_mov_b32 s66,s65
R 66,1,6144
v_add_f32_dpp v115,v115,v115 quad_perm:[2,3,0,1] row_mask:0xf bank_mask:0xf
v_mfma_f32_16x16x32_bf16 v[18:21],a[0:3],v[42:45],0
s_add_u32 s65,s65,s62
v_fma_f32 v30,-v78,v82,v30
v_fma_f32 v31,-v78,v83,v31
M 18,a[12:15],46
L 3
v_add_f32_dpp v118,v115,v115 row_half_mirror row_mask:0xf bank_mask:0xf
v_fma_f32 v32,-v78,v84,v32
M 18,a[24:27],50
s_add_u32 s54,s54,s46
R 70,1,7168
v_fma_f32 v33,-v78,v85,v33
v_mul_f32 v94,v30,v30
M 18,a[36:39],54
s_add_u32 s73,s73,s64
v_mov_b32_dpp v115,v118 row_ror:8 row_mask:0xf bank_mask:0xf
v_fma_f32 v115,-v118,v118,v115
M 22,a[28:31],50
v_max_f32 v115,s87,v115
v_mul_f32 v95,v31,v31
M 22,a[40:43],54
R 42,1,8192
v_mul_f32 v96,v32,v32
M 26,a[32:35],50
v_rsq_f32 v116,v115
M 26,a[44:47],54
L 3
D 0,56,1024
M 26,a[56:59],58
R 46,1,9216
v_mul_f32 v121,v115,v116
M 26,a[68:71],62
v_mul_f32 v119,s88,v116
v_mul_f32 v115,s89,v116
v_mul_f32 v120,v119,v116
M 22,a[52:55],58
v_mul_f32 v120,v120,v115
v_mul_f32 v97,v33,v33
M 22,a[64:67],62
R 50,1,10240
v_fma_f32 v94,v80,v94,v79
M 18,a[48:51],58
ds_write_b128 v12,v[118:121] offset:0
M 18,a[60:63],62
L 4
v_fma_f32 v95,v80,v95,v79
v_fma_f32 v96,v80,v96,v79
M 18,a[72:75],66
R 54,1,11264
v_fma_f32 v97,v80,v97,v79
M 18,a[84:87],70
v_mul_f32 v94,v30,v94
v_exp_f32 v94,v94
M 22,a[76:79],66
v_mul_f32 v95,v31,v95
v_mul_f32 v96,v32,v96
M 22,a[88:91],70
R 58,1,12288
v_mul_f32 v97,v33,v97
M 26,a[80:83],66
D 0,56,2048
M 26,a[92:95],70
L 4
v_exp_f32 v95,v95
M 26,a[104:107],42
R 62,1,13312
v_fma_f32 v94,v94,v81,v81
M 26,a[116:119],46
v_exp_f32 v96,v96
M 22,a[100:103],42
v_exp_f32 v97,v97
)"
R"(v_fma_f32 v95,v95,v81,v81
M 22,a[112:115],46
R 66,1,14336
v_fma_f32 v96,v96,v81,v81
M 18,a[96:99],42
v_rcp_f32 v94,v94
M 18,a[108:111],46
L 3
v_rcp_f32 v95,v95
M 18,a[120:123],50
R 70,1,15360
v_fma_f32 v97,v97,v81,v81
M 18,a[132:135],54
s_waitcnt vmcnt(6)
D 0,56,3072
M 22,a[124:127],50
s_barrier
v_rcp_f32 v96,v96
M 22,a[136:139],54
s_add_u32 m0,s59,0
R 42,1,16384
v_rcp_f32 v97,v97
M 26,a[128:131],50
s_add_u32 s56,s55,16384
v_mul_f32 v30,v30,v94
v_mul_f32 v31,v31,v95
M 26,a[140:143],54
L 3
v_mul_f32 v32,v32,v96
v_mul_f32 v33,v33,v97
M 26,a[152:155],58
s_add_u32 s55,s55,s60
R 46,1,17408
v_cvt_pk_bf16_f32 v106,v30,v31
M 26,a[164:167],62
v_cvt_pk_bf16_f32 v107,v32,v33
v_fma_f32 v34,-v78,v86,v34
M 22,a[148:151],58
v_fma_f32 v35,-v78,v87,v35
v_fma_f32 v36,-v78,v88,v36
M 22,a[160:163],62
R 50,1,18432
v_fma_f32 v37,-v78,v89,v37
v_mul_f32 v98,v34,v34
M 18,a[144:147],58
buffer_load_dword v8,s[36:39],s58 offen offset:0 sc1 lds
M 18,a[156:159],62
L 3
v_mul_f32 v99,v35,v35
v_mul_f32 v100,v36,v36
M 18,a[168:171],66
s_add_u32 m0,s57,16384
R 54,1,19456
v_mul_f32 v101,v37,v37
M 18,a[180:183],70
s_sub_u32 s57,s84,s57
v_fma_f32 v98,v80,v98,v79
v_fma_f32 v99,v80,v99,v79
M 22,a[172:175],66
v_fma_f32 v100,v80,v100,v79
v_fma_f32 v101,v80,v101,v79
M 22,a[184:187],70
R 58,1,20480
v_mul_f32 v98,v34,v98
M 26,a[176:179],66
D 0,56,0
v_mul_f32 v99,v35,v99
M 26,a[188:191],70
L 3
v_exp_f32 v98,v98
M 26,a[200:203],42
R 62,1,21504
v_mul_f32 v100,v36,v100
M 26,a[212:215],46
v_exp_f32 v99,v99
M 22,a[196:199],42
v_exp_f32 v100,v100
M 22,a[208:211],46
R 66,1,22528
v_mul_f32 v101,v37,v101
M 18,a[192:195],42
v_exp_f32 v101,v101
M 18,a[204:207],46
L 3
v_fma_f32 v98,v98,v81,v81
v_rcp_f32 v98,v98
M 18,a[216:219],50
R 70,1,23552
v_fma_f32 v99,v99,v81,v81
M 18,a[228:231],54
D 0,56,1024
M 22,a[220:223],50
v_rcp_f32 v99,v99
M 22,a[232:235],54
R 42,1,24576
v_fma_f32 v100,v100,v81,v81
M 26,a[224:227],50
v_rcp_f32 v100,v100
M 26,a[236:239],54
L 3
v_fma_f32 v101,v101,v81,v81
v_rcp_f32 v101,v101
M 26,a[248:251],58
R 46,1,25600
v_mul_f32 v34,v34,v98
M 26,v[132:135],62
v_mul_f32 v35,v35,v99
v_mul_f32 v36,v36,v100
M 22,a[244:247],58
v_mul_f32 v37,v37,v101
v_cvt_pk_bf16_f32 v108,v34,v35
M 22,v[128:131],62
R 50,1,26624
v_cvt_pk_bf16_f32 v109,v36,v37
M 18,a[240:243],58
buffer_store_dwordx4 v[106:109],v0,s[36:39],s67 offen offset:0 sc0 sc1
v_fma_f32 v38,-v78,v90,v38
M 18,a[252:255],62
L 3
v_fma_f32 v39,-v78,v91,v39
v_fma_f32 v40,-v78,v92,v40
M 18,v[136:139],66
R 54,1,27648
v_fma_f32 v41,-v78,v93,v41
v_mul_f32 v102,v38,v38
M 18,v[148:151],70
D 0,56,2048
M 22,v[140:143],66
v_mul_f32 v103,v39,v39
v_mul_f32 v104,v40,v40
M 22,v[152:155],70
R 58,1,28672
v_mul_f32 v105,v41,v41
M 26,v[144:147],66
v_fma_f32 v102,v80,v102,v79
v_fma_f32 v103,v80,v103,v79
M 26,v[156:159],70
L 3
v_fma_f32 v104,v80,v104,v79
v_fma_f32 v105,v80,v105,v79
M 26,v[168:171],42
R 62,1,29696
v_mul_f32 v102,v38,v102
M 26,v[180:183],46
v_exp_f32 v102,v102
v_mul_f32 v103,v39,v103
M 22,v[164:167],42
v_exp_f32 v103,v103
M 22,v[176:179],46
R 66,1,30720
v_mul_f32 v104,v40,v104
M 18,v[160:163],42
v_exp_f32 v104,v104
M 18,v[172:175],46
L 3
D 0,56,3072
M 18,v[184:187],50
s_nop 0
R 70,1,31744
v_mul_f32 v105,v41,v105
M 18,v[196:199],54
s_waitcnt vmcnt(5)
v_exp_f32 v105,v105
v_fma_f32 v102,v102,v81,v81
M 22,v[188:191],50
s_barrier
buffer_store_dword v4,v3,s[36:39],s70 offen offset:0 sc0 sc1
M 22,v[200:203],54
s_mov_b32 s70,s69
R 42,2,0
ds_read_b32 v7,v6 offset:0
M 26,v[192:195],50
s_mov_b32 s69,s68
R 74,13,0
v_fma_f32 v103,v103,v81,v81
M 26,v[204:207],54
L 5
v_rcp_f32 v102,v102
M 26,v[216:219],58
s_add_u32 s68,s68,s63
R 46,2,1024
v_fma_f32 v104,v104,v81,v81
M 26,v[228:231],62
v_rcp_f32 v103,v103
M 22,v[212:215],58
v_rcp_f32 v104,v104
v_fma_f32 v105,v105,v81,v81
M 22,v[224:227],62
L 2
R 50,2,2048
v_mul_f32 v38,v38,v102
M 18,v[208:211],58
v_rcp_f32 v105,v105
M 18,v[220:223],62
s_nop 0
v_mul_f32 v39,v39,v103
v_mul_f32 v40,v40,v104
M 18,v[232:235],66
R 54,2,3072
v_mul_f32 v41,v41,v105
M 18,v[244:247],70
v_cvt_pk_bf16_f32 v110,v38,v39
v_cvt_pk_bf16_f32 v111,v40,v41
M 22,v[236:239],66
v_cmp_eq_u32 vcc,s53,v7
M 22,v[248:251],70
s_cmp_eq_u32 vcc_lo,-1
R 58,2,4096
v_permlane16_swap_b32 v110,v112
v_permlane16_swap_b32 v111,v113
M 26,v[240:243],66
buffer_store_dwordx4 v[110:113],v10,s[36:39],s67 offen offset:0 sc0 sc1
M 26,v[252:255],70
L 3
s_cbranch_scc0 Lg3_L1_%=
Lg3_F1_%=:
v_mfma_f32_16x16x32_bf16 v[38:41],a[8:11],v[42:45],0
s_add_u32 m0,s57,0
R 62,2,5120
v_add_f32_dpp v115,v114,v114 quad_perm:[1,0,3,2] row_mask:0xf bank_mask:0xf
M 38,a[20:23],46
s_add_u32 s56,s55,0
buffer_load_dword v114,v11,s[36:39],s73 offen offset:0 sc1
v_mfma_f32_16x16x32_bf16 v[34:37],a[4:7],v[42:45],0
s_add_u32 s58,s58,s61
D 0,56,0
M 34,a[16:19],46
s_mov_b32 s67,s65
R 66,2,6144
v_add_f32_dpp v115,v115,v115 quad_perm:[2,3,0,1] row_mask:0xf bank_mask:0xf
v_mfma_f32_16x16x32_bf16 v[30:33],a[0:3],v[42:45],0
s_add_u32 s65,s65,s62
v_fma_f32 v18,-v74,v82,v18
v_fma_f32 v19,-v74,v83,v19
M 30,a[12:15],46
L 3
v_add_f32_dpp v118,v115,v115 row_half_mirror row_mask:0xf bank_mask:0xf
v_fma_f32 v20,-v74,v84,v20
M 30,a[24:27],50
s_add_u32 s54,s54,s46
R 70,2,7168
v_fma_f32 v21,-v74,v85,v21
v_mul_f32 v94,v18,v18
M 30,a[36:39],54
s_add_u32 s73,s73,s64
v_mov_b32_dpp v115,v118 row_ror:8 row_mask:0xf bank_mask:0xf
v_fma_f32 v115,-v118,v118,v115
M 34,a[28:31],50
v_max_f32 v115,s87,v115
v_mul_f32 v95,v19,v19
M 34,a[40:43],54
R 42,2,8192
v_mul_f32 v96,v20,v20
M 38,a[32:35],50
v_rsq_f32 v116,v115
M 38,a[44:47],54
L 3
D 0,56,1024
M 38,a[56:59],58
R 46,2,9216
v_mul_f32 v121,v115,v116
M 38,a[68:71],62
v_mul_f32 v119,s88,v116
v_mul_f32 v115,s89,v116
v_mul_f32 v120,v119,v116
M 34,a[52:55],58
v_mul_f32 v120,v120,v115
v_mul_f32 v97,v21,v21
M 34,a[64:67],62
R 50,2,10240
v_fma_f32 v94,v76,v94,v75
M 30,a[48:51],58
ds_write_b128 v12,v[118:121] offset:2048
M 30,a[60:63],62
L 4
v_fma_f32 v95,v76,v95,v75
v_fma_f32 v96,v76,v96,v75
M 30,a[72:75],66
R 54,2,11264
v_fma_f32 v97,v76,v97,v75
M 30,a[84:87],70
v_mul_f32 v94,v18,v94
v_exp_f32 v94,v94
M 34,a[76:79],66
v_mul_f32 v95,v19,v95
v_mul_f32 v96,v20,v96
M 34,a[88:91],70
R 58,2,12288
v_mul_f32 v97,v21,v97
M 38,a[80:83],66
D 0,56,2048
M 38,a[92:95],70
L 4
v_exp_f32 v95,v95
M 38,a[104:107],42
R 62,2,13312
v_fma_f32 v94,v94,v77,v77
M 38,a[116:119],46
v_exp_f32 v96,v96
M 34,a[100:103],42
v_exp_f32 v97,v97
v_fma_f32 v95,v95,v77,v77
M 34,a[112:115],46
R 66,2,14336
v_fma_f32 v96,v96,v77,v77
M 30,a[96:99],42
v_rcp_f32 v94,v94
M 30,a[108:111],46
L 3
v_rcp_f32 v95,v95
M 30,a[120:123],50
R 70,2,15360
v_fma_f32 v97,v97,v77,v77
M 30,a[132:135],54
s_waitcnt vmcnt(6)
D 0,56,3072
M 34,a[124:127],50
s_barrier
v_rcp_f32 v96,v96
M 34,a[136:139],54
s_add_u32 m0,s59,1024
R 42,2,16384
v_rcp_f32 v97,v97
M 38,a[128:131],50
s_add_u32 s56,s55,16384
v_mul_f32 v18,v18,v94
v_mul_f32 v19,v19,v95
M 38,a[140:143],54
L 3
v_mul_f32 v20,v20,v96
v_mul_f32 v21,v21,v97
M 38,a[152:155],58
s_add_u32 s55,s55,s60
R 46,2,17408
v_cvt_pk_bf16_f32 v106,v18,v19
M 38,a[164:167],62
v_cvt_pk_bf16_f32 v107,v20,v21
v_fma_f32 v22,-v74,v86,v22
M 34,a[148:151],58
v_fma_f32 v23,-v74,v87,v23
v_fma_f32 v24,-v74,v88,v24
M 34,a[160:163],62
R 50,2,18432
v_fma_f32 v25,-v74,v89,v25
v_mul_f32 v98,v22,v22
M 30,a[144:147],58
buffer_load_dword v8,s[36:39],s58 offen offset:0 sc1 lds
M 30,a[156:159],62
L 3
v_mul_f32 v99,v23,v23
v_mul_f32 v100,v24,v24
M 30,a[168:171],66
s_add_u32 m0,s57,16384
R 54,2,19456
v_mul_f32 v101,v25,v25
M 30,a[180:183],70
s_sub_u32 s57,s84,s57
v_fma_f32 v98,v76,v98,v75
v_fma_f32 v99,v76,v99,v75
M 34,a[172:175],66
v_fma_f32 v100,v76,v100,v75
v_fma_f32 v101,v76,v101,v75
M 34,a[184:187],70
R 58,2,20480
v_mul_f32 v98,v22,v98
M 38,a[176:179],66
D 0,56,0
v_mul_f32 v99,v23,v99
M 38,a[188:191],70
L 3
v_exp_f32 v98,v98
M 38,a[200:203],42
R 62,2,21504
v_mul_f32 v100,v24,v100
M 38,a[212:215],46
v_exp_f32 v99,v99
M 34,a[196:199],42
v_exp_f32 v100,v100
M 34,a[208:211],46
R 66,2,22528
v_mul_f32 v101,v25,v101
M 30,a[192:195],42
v_exp_f32 v101,v101
M 30,a[204:207],46
L 3
v_fma_f32 v98,v98,v77,v77
v_rcp_f32 v98,v98
M 30,a[216:219],50
R 70,2,23552
v_fma_f32 v99,v99,v77,v77
M 30,a[228:231],54
D 0,56,1024
M 34,a[220:223],50
v_rcp_f32 v99,v99
M 34,a[232:235],54
R 42,2,24576
v_fma_f32 v100,v100,v77,v77
M 38,a[224:227],50
v_rcp_f32 v100,v100
M 38,a[236:239],54
L 3
v_fma_f32 v101,v101,v77,v77
v_rcp_f32 v101,v101
M 38,a[248:251],58
R 46,2,25600
v_mul_f32 v22,v22,v98
M 38,v[132:135],62
v_mul_f32 v23,v23,v99
v_mul_f32 v24,v24,v100
M 34,a[244:247],58
v_mul_f32 v25,v25,v101
v_cvt_pk_bf16_f32 v108,v22,v23
M 34,v[128:131],62
R 50,2,26624
v_cvt_pk_bf16_f32 v109,v24,v25
M 30,a[240:243],58
buffer_store_dwordx4 v[106:109],v0,s[36:39],s66 offen offset:0 sc0 sc1
v_fma_f32 v26,-v74,v90,v26
M 30,a[252:255],62
L 3
v_fma_f32 v27,-v74,v91,v27
v_fma_f32 v28,-v74,v92,v28
M 30,v[136:139],66
R 54,2,27648
v_fma_f32 v29,-v74,v93,v29
v_mul_f32 v102,v26,v26
M 30,v[148:151],70
D 0,56,2048
M 34,v[140:143],66
v_mul_f32 v103,v27,v27
v_mul_f32 v104,v28,v28
M 34,v[152:155],70
R 58,2,28672
v_mul_f32 v105,v29,v29
M 38,v[144:147],66
v_fma_f32 v102,v76,v102,v75
v_fma_f32 v103,v76,v103,v75
M 38,v[156:159],70
L 3
v_fma_f32 v104,v76,v104,v75
v_fma_f32 v105,v76,v105,v75
M 38,v[168:171],42
R 62,2,29696
v_mul_f32 v102,v26,v102
M 38,v[180:183],46
v_exp_f32 v102,v102
v_mul_f32 v103,v27,v103
M 34,v[164:167],42
v_exp_f32 v103,v103
M 34,v[176:179],46
R 66,2,30720
v_mul_f32 v104,v28,v104
M 30,v[160:163],42
v_exp_f32 v104,v104
M 30,v[172:175],46
L 3
D 0,56,3072
M 30,v[184:187],50
s_nop 0
R 70,2,31744
v_mul_f32 v105,v29,v105
M 30,v[196:199],54
s_waitcnt vmcnt(5)
v_exp_f32 v105,v105
v_fma_f32 v102,v102,v77,v77
M 34,v[188:191],50
s_barrier
buffer_store_dword v4,v3,s[36:39],s70 offen offset:0 sc0 sc1
M 34,v[200:203],54
s_mov_b32 s70,s69
R 42,1,0
ds_read_b32 v7,v6 offset:1024
M 38,v[192:195],50
s_mov_b32 s69,s68
R 78,13,2048
v_fma_f32 v103,v103,v77,v77
M 38,v[204:207],54
L 5
v_rcp_f32 v102,v102
M 38,v[216:219],58
s_add_u32 s68,s68,s63
R 46,1,1024
v_fma_f32 v104,v104,v77,v77
M 38,v[228:231],62
v_rcp_f32 v103,v103
M 34,v[212:215],58
v_rcp_f32 v104,v104
v_fma_f32 v105,v105,v77,v77
M 34,v[224:227],62
L 2
R 50,1,2048
v_mul_f32 v26,v26,v102
M 30,v[208:211],58
v_rcp_f32 v105,v105
M 30,v[220:223],62
s_nop 0
v_mul_f32 v27,v27,v103
v_mul_f32 v28,v28,v104
M 30,v[232:235],66
R 54,1,3072
)"
R"(v_mul_f32 v29,v29,v105
M 30,v[244:247],70
v_cvt_pk_bf16_f32 v110,v26,v27
v_cvt_pk_bf16_f32 v111,v28,v29
M 34,v[236:239],66
v_cmp_eq_u32 vcc,s53,v7
M 34,v[248:251],70
s_cmp_eq_u32 vcc_lo,-1
R 58,1,4096
v_permlane16_swap_b32 v110,v112
v_permlane16_swap_b32 v111,v113
M 38,v[240:243],66
buffer_store_dwordx4 v[110:113],v10,s[36:39],s66 offen offset:0 sc0 sc1
M 38,v[252:255],70
L 3
s_cbranch_scc1 Lg3_F0_%=
Lg3_L0_%=:
v_mfma_f32_16x16x32_bf16 v[26:29],a[8:11],v[42:45],0
s_add_u32 m0,s59,3072
R 62,1,5120
v_add_f32_dpp v115,v114,v114 quad_perm:[1,0,3,2] row_mask:0xf bank_mask:0xf
M 26,a[20:23],46
s_mov_b32 s66,s65
buffer_load_dword v8,s[36:39],s58 offen offset:0 sc1 lds
v_mfma_f32_16x16x32_bf16 v[22:25],a[4:7],v[42:45],0
s_add_u32 s65,s65,s62
v_add_f32_dpp v115,v115,v115 quad_perm:[2,3,0,1] row_mask:0xf bank_mask:0xf
v_fma_f32 v30,-v78,v82,v30
M 22,a[16:19],46
R 66,1,6144
v_fma_f32 v31,-v78,v83,v31
v_mfma_f32_16x16x32_bf16 v[18:21],a[0:3],v[42:45],0
v_add_f32_dpp v118,v115,v115 row_half_mirror row_mask:0xf bank_mask:0xf
v_fma_f32 v32,-v78,v84,v32
v_fma_f32 v33,-v78,v85,v33
M 18,a[12:15],46
L 3
v_mul_f32 v94,v30,v30
v_mul_f32 v95,v31,v31
M 18,a[24:27],50
R 70,1,7168
v_mov_b32_dpp v115,v118 row_ror:8 row_mask:0xf bank_mask:0xf
M 18,a[36:39],54
v_fma_f32 v115,-v118,v118,v115
v_max_f32 v115,s87,v115
M 22,a[28:31],50
v_rsq_f32 v116,v115
v_mul_f32 v96,v32,v32
M 22,a[40:43],54
R 42,1,8192
v_mul_f32 v121,v115,v116
M 26,a[32:35],50
v_mul_f32 v119,s88,v116
v_mul_f32 v115,s89,v116
M 26,a[44:47],54
L 3
v_mul_f32 v120,v119,v116
v_mul_f32 v120,v120,v115
M 26,a[56:59],58
R 46,1,9216
v_mul_f32 v97,v33,v33
M 26,a[68:71],62
ds_write_b128 v12,v[118:121] offset:0
M 22,a[52:55],58
v_fma_f32 v94,v80,v94,v79
v_fma_f32 v95,v80,v95,v79
M 22,a[64:67],62
R 50,1,10240
v_fma_f32 v96,v80,v96,v79
M 18,a[48:51],58
v_fma_f32 v97,v80,v97,v79
v_mul_f32 v94,v30,v94
M 18,a[60:63],62
L 4
v_exp_f32 v94,v94
v_mul_f32 v95,v31,v95
M 18,a[72:75],66
R 54,1,11264
v_mul_f32 v96,v32,v96
M 18,a[84:87],70
v_exp_f32 v95,v95
M 22,a[76:79],66
v_exp_f32 v96,v96
M 22,a[88:91],70
R 58,1,12288
v_mul_f32 v97,v33,v97
M 26,a[80:83],66
v_exp_f32 v97,v97
v_fma_f32 v94,v94,v81,v81
M 26,a[92:95],70
L 4
v_rcp_f32 v94,v94
M 26,a[104:107],42
R 62,1,13312
v_fma_f32 v95,v95,v81,v81
M 26,a[116:119],46
v_rcp_f32 v95,v95
M 22,a[100:103],42
v_fma_f32 v96,v96,v81,v81
v_rcp_f32 v96,v96
M 22,a[112:115],46
R 66,1,14336
v_fma_f32 v97,v97,v81,v81
M 18,a[96:99],42
v_rcp_f32 v97,v97
M 18,a[108:111],46
L 3
v_mul_f32 v30,v30,v94
v_mul_f32 v31,v31,v95
M 18,a[120:123],50
R 70,1,15360
v_mul_f32 v32,v32,v96
M 18,a[132:135],54
s_waitcnt vmcnt(0)
v_mul_f32 v33,v33,v97
v_cvt_pk_bf16_f32 v106,v30,v31
v_cvt_pk_bf16_f32 v107,v32,v33
M 22,a[124:127],50
s_barrier
ds_read_b32 v7,v6 offset:3072
v_fma_f32 v34,-v78,v86,v34
M 22,a[136:139],54
R 42,1,16384
v_fma_f32 v35,-v78,v87,v35
M 26,a[128:131],50
buffer_store_dword v4,v3,s[36:39],s70 offen offset:0 sc0 sc1
M 26,a[140:143],54
L 4
v_fma_f32 v36,-v78,v88,v36
v_fma_f32 v37,-v78,v89,v37
v_mul_f32 v98,v34,v34
M 26,a[152:155],58
s_mov_b32 s70,0x7fff0000
R 46,1,17408
v_mul_f32 v99,v35,v35
M 26,a[164:167],62
v_mul_f32 v100,v36,v36
v_mul_f32 v101,v37,v37
M 22,a[148:151],58
L 2
v_cmp_eq_u32 vcc,s53,v7
v_fma_f32 v98,v80,v98,v79
M 22,a[160:163],62
R 50,1,18432
v_fma_f32 v99,v80,v99,v79
M 18,a[144:147],58
s_cmp_eq_u32 vcc_lo,-1
v_fma_f32 v100,v80,v100,v79
v_fma_f32 v101,v80,v101,v79
v_mul_f32 v98,v34,v98
s_cbranch_scc0 Lg3_X0_%=
M 18,a[156:159],62
s_nop 0
s_add_u32 m0,s57,0
s_add_u32 s56,s55,0
buffer_load_dword v114,v11,s[36:39],s73 offen offset:0 sc1
M 18,a[168:171],66
s_add_u32 s58,s58,s61
R 54,1,19456
v_mul_f32 v99,v35,v99
M 18,a[180:183],70
s_add_u32 s54,s54,s46
D 0,56,0
M 22,a[172:175],66
s_add_u32 s73,s73,s64
v_exp_f32 v98,v98
v_mul_f32 v100,v36,v100
M 22,a[184:187],70
R 58,1,20480
v_mul_f32 v101,v37,v101
M 26,a[176:179],66
v_exp_f32 v99,v99
M 26,a[188:191],70
L 3
D 0,56,1024
M 26,a[200:203],42
R 62,1,21504
v_exp_f32 v100,v100
M 26,a[212:215],46
v_exp_f32 v101,v101
M 22,a[196:199],42
v_fma_f32 v98,v98,v81,v81
v_fma_f32 v99,v99,v81,v81
M 22,a[208:211],46
R 66,1,22528
v_fma_f32 v100,v100,v81,v81
M 18,a[192:195],42
D 0,56,2048
M 18,a[204:207],46
L 3
v_rcp_f32 v98,v98
v_fma_f32 v101,v101,v81,v81
M 18,a[216:219],50
R 70,1,23552
v_mul_f32 v34,v34,v98
M 18,a[228:231],54
D 0,56,3072
M 22,a[220:223],50
s_add_u32 m0,s59,0
v_rcp_f32 v99,v99
M 22,a[232:235],54
s_add_u32 s56,s55,16384
R 42,1,24576
v_rcp_f32 v100,v100
M 26,a[224:227],50
s_add_u32 s55,s55,s60
buffer_load_dword v8,s[36:39],s58 offen offset:0 sc1 lds
M 26,a[236:239],54
L 3
v_rcp_f32 v101,v101
M 26,a[248:251],58
s_add_u32 m0,s57,16384
R 46,1,25600
v_mul_f32 v35,v35,v99
M 26,v[132:135],62
s_sub_u32 s57,s84,s57
D 0,56,0
M 22,a[244:247],58
v_mul_f32 v36,v36,v100
v_mul_f32 v37,v37,v101
v_cvt_pk_bf16_f32 v108,v34,v35
M 22,v[128:131],62
R 50,1,26624
v_cvt_pk_bf16_f32 v109,v36,v37
M 18,a[240:243],58
buffer_store_dwordx4 v[106:109],v0,s[36:39],s67 offen offset:0 sc0 sc1
v_fma_f32 v38,-v78,v90,v38
M 18,a[252:255],62
L 3
v_fma_f32 v39,-v78,v91,v39
v_fma_f32 v40,-v78,v92,v40
M 18,v[136:139],66
R 54,1,27648
v_fma_f32 v41,-v78,v93,v41
v_mul_f32 v102,v38,v38
M 18,v[148:151],70
D 0,56,1024
M 22,v[140:143],66
v_mul_f32 v103,v39,v39
v_mul_f32 v104,v40,v40
M 22,v[152:155],70
R 58,1,28672
v_mul_f32 v105,v41,v41
M 26,v[144:147],66
v_fma_f32 v102,v80,v102,v79
v_fma_f32 v103,v80,v103,v79
M 26,v[156:159],70
L 3
D 0,56,2048
v_fma_f32 v104,v80,v104,v79
M 26,v[168:171],42
R 62,1,29696
v_fma_f32 v105,v80,v105,v79
M 26,v[180:183],46
v_mul_f32 v102,v38,v102
v_mul_f32 v103,v39,v103
M 22,v[164:167],42
v_exp_f32 v102,v102
M 22,v[176:179],46
R 66,1,30720
v_exp_f32 v103,v103
M 18,v[160:163],42
D 0,56,3072
M 18,v[172:175],46
L 3
v_mul_f32 v104,v40,v104
v_mul_f32 v105,v41,v105
M 18,v[184:187],50
s_nop 0
R 70,1,31744
v_fma_f32 v102,v102,v81,v81
M 18,v[196:199],54
s_waitcnt vmcnt(5)
v_exp_f32 v104,v104
M 22,v[188:191],50
s_barrier
buffer_store_dword v4,v3,s[36:39],s70 offen offset:0 sc0 sc1
v_fma_f32 v103,v103,v81,v81
M 22,v[200:203],54
s_mov_b32 s70,s69
R 42,2,0
ds_read_b32 v7,v6 offset:0
M 26,v[192:195],50
s_mov_b32 s69,s68
R 74,13,0
v_fma_f32 v104,v104,v81,v81
M 26,v[204:207],54
L 5
v_exp_f32 v105,v105
M 26,v[216:219],58
s_add_u32 s68,s68,s63
R 46,2,1024
v_rcp_f32 v102,v102
M 26,v[228:231],62
v_rcp_f32 v103,v103
M 22,v[212:215],58
v_rcp_f32 v104,v104
M 22,v[224:227],62
L 2
R 50,2,2048
v_fma_f32 v105,v105,v81,v81
M 18,v[208:211],58
v_rcp_f32 v105,v105
v_mul_f32 v38,v38,v102
M 18,v[220:223],62
s_nop 0
v_mul_f32 v39,v39,v103
v_mul_f32 v40,v40,v104
M 18,v[232:235],66
R 54,2,3072
v_mul_f32 v41,v41,v105
M 18,v[244:247],70
v_cvt_pk_bf16_f32 v110,v38,v39
v_cvt_pk_bf16_f32 v111,v40,v41
M 22,v[236:239],66
v_cmp_eq_u32 vcc,s53,v7
M 22,v[248:251],70
s_cmp_eq_u32 vcc_lo,-1
R 58,2,4096
v_permlane16_swap_b32 v110,v112
v_permlane16_swap_b32 v111,v113
M 26,v[240:243],66
buffer_store_dwordx4 v[110:113],v10,s[36:39],s67 offen offset:0 sc0 sc1
M 26,v[252:255],70
L 3
s_cbranch_scc1 Lg3_F1_%=
s_branch Lg3_L1_%=
Lg3_X0_%=:
M 18,a[156:159],62
s_nop 0
s_add_u32 m0,s59,0
v_mul_f32 v99,v35,v99
M 18,a[168:171],66
R 54,1,19456
v_mul_f32 v100,v36,v100
M 18,a[180:183],70
buffer_load_dword v8,s[36:39],s58 offen offset:0 sc1 lds
M 22,a[172:175],66
v_exp_f32 v98,v98
M 22,a[184:187],70
R 58,1,20480
v_mul_f32 v101,v37,v101
M 26,a[176:179],66
v_exp_f32 v99,v99
M 26,a[188:191],70
L 3
v_exp_f32 v100,v100
M 26,a[200:203],42
R 62,1,21504
v_fma_f32 v98,v98,v81,v81
M 26,a[212:215],46
v_exp_f32 v101,v101
M 22,a[196:199],42
v_rcp_f32 v98,v98
M 22,a[208:211],46
R 66,1,22528
v_fma_f32 v99,v99,v81,v81
M 18,a[192:195],42
v_rcp_f32 v99,v99
M 18,a[204:207],46
L 3
v_fma_f32 v100,v100,v81,v81
v_fma_f32 v101,v101,v81,v81
M 18,a[216:219],50
R 70,1,23552
v_mul_f32 v34,v34,v98
M 18,a[228:231],54
v_rcp_f32 v100,v100
M 22,a[220:223],50
v_rcp_f32 v101,v101
M 22,a[232:235],54
R 42,1,24576
v_mul_f32 v35,v35,v99
M 26,a[224:227],50
v_mul_f32 v36,v36,v100
v_mul_f32 v37,v37,v101
M 26,a[236:239],54
L 3
v_cvt_pk_bf16_f32 v108,v34,v35
v_cvt_pk_bf16_f32 v109,v36,v37
M 26,a[248:251],58
R 46,1,25600
v_fma_f32 v38,-v78,v90,v38
M 26,v[132:135],62
buffer_store_dwordx4 v[106:109],v0,s[36:39],s67 offen offset:0 sc0 sc1
v_fma_f32 v39,-v78,v91,v39
M 22,a[244:247],58
v_fma_f32 v40,-v78,v92,v40
v_fma_f32 v41,-v78,v93,v41
M 22,v[128:131],62
R 50,1,26624
v_mul_f32 v102,v38,v38
M 18,a[240:243],58
v_mul_f32 v103,v39,v39
v_mul_f32 v104,v40,v40
M 18,a[252:255],62
L 3
v_mul_f32 v105,v41,v41
v_fma_f32 v102,v80,v102,v79
M 18,v[136:139],66
R 54,1,27648
v_fma_f32 v103,v80,v103,v79
M 18,v[148:151],70
v_fma_f32 v104,v80,v104,v79
v_fma_f32 v105,v80,v105,v79
M 22,v[140:143],66
v_mul_f32 v102,v38,v102
v_mul_f32 v103,v39,v103
M 22,v[152:155],70
R 58,1,28672
v_mul_f32 v104,v40,v104
M 26,v[144:147],66
v_exp_f32 v102,v102
M 26,v[156:159],70
L 3
v_exp_f32 v103,v103
M 26,v[168:171],42
R 62,1,29696
v_mul_f32 v105,v41,v105
M 26,v[180:183],46
v_exp_f32 v104,v104
M 22,v[164:167],42
v_exp_f32 v105,v105
M 22,v[176:179],46
R 66,1,30720
v_fma_f32 v102,v102,v81,v81
M 18,v[160:163],42
v_rcp_f32 v102,v102
M 18,v[172:175],46
L 3
v_fma_f32 v103,v103,v81,v81
v_fma_f32 v104,v104,v81,v81
M 18,v[184:187],50
s_nop 0
R 70,1,31744
v_fma_f32 v105,v105,v81,v81
M 18,v[196:199],54
s_waitcnt vmcnt(1)
v_rcp_f32 v103,v103
M 22,v[188:191],50
s_barrier
buffer_store_dword v4,v3,s[36:39],s70 offen offset:0 sc0 sc1
M 22,v[200:203],54
s_mov_b32 s70,s69
ds_read_b32 v7,v6 offset:0
R 74,13,0
M 26,v[192:195],50
s_mov_b32 s69,s68
v_rcp_f32 v104,v104
M 26,v[204:207],54
L 4
v_rcp_f32 v105,v105
M 26,v[216:219],58
s_add_u32 s68,s68,s63
v_mul_f32 v38,v38,v102
v_mul_f32 v39,v39,v103
M 26,v[228:231],62
v_mul_f32 v40,v40,v104
v_mul_f32 v41,v41,v105
M 22,v[212:215],58
v_cvt_pk_bf16_f32 v110,v38,v39
v_cvt_pk_bf16_f32 v111,v40,v41
M 22,v[224:227],62
L 1
v_cmp_eq_u32 vcc,s53,v7
M 18,v[208:211],58
v_permlane16_swap_b32 v110,v112
M 18,v[220:223],62
s_nop 0
v_permlane16_swap_b32 v111,v113
M 18,v[232:235],66
buffer_store_dwordx4 v[110:113],v10,s[36:39],s67 offen offset:0 sc0 sc1
M 18,v[244:247],70
M 22,v[236:239],66
M 22,v[248:251],70
M 26,v[240:243],66
M 26,v[252:255],70
s_mov_b32 s80,1
s_cmp_eq_u32 vcc_lo,-1
s_cbranch_scc0 Lg3_xn0_%=
buffer_load_dword v114,v11,s[36:39],s73 offen offset:0 sc1
L 0
v_fma_f32 v18,-v74,v82,v18
v_fma_f32 v19,-v74,v83,v19
v_fma_f32 v20,-v74,v84,v20
v_fma_f32 v21,-v74,v85,v21
v_mul_f32 v94,v18,v18
v_mul_f32 v95,v19,v19
v_mul_f32 v96,v20,v20
v_mul_f32 v97,v21,v21
s_add_u32 s73,s73,s64
s_add_u32 m0,s57,0
s_add_u32 s56,s55,0
D 0,56,0
v_fma_f32 v94,v76,v94,v75
v_fma_f32 v95,v76,v95,v75
v_fma_f32 v96,v76,v96,v75
v_fma_f32 v97,v76,v97,v75
v_mul_f32 v94,v18,v94
v_mul_f32 v95,v19,v95
)"
R"(v_mul_f32 v96,v20,v96
v_mul_f32 v97,v21,v97
D 0,56,1024
v_exp_f32 v94,v94
v_exp_f32 v95,v95
v_exp_f32 v96,v96
v_exp_f32 v97,v97
v_fma_f32 v94,v94,v77,v77
v_fma_f32 v95,v95,v77,v77
v_fma_f32 v96,v96,v77,v77
v_fma_f32 v97,v97,v77,v77
D 0,56,2048
v_rcp_f32 v94,v94
v_rcp_f32 v95,v95
v_rcp_f32 v96,v96
v_rcp_f32 v97,v97
v_mul_f32 v18,v18,v94
v_mul_f32 v19,v19,v95
v_mul_f32 v20,v20,v96
v_mul_f32 v21,v21,v97
D 0,56,3072
v_cvt_pk_bf16_f32 v106,v18,v19
v_cvt_pk_bf16_f32 v107,v20,v21
v_fma_f32 v22,-v74,v86,v22
v_fma_f32 v23,-v74,v87,v23
v_fma_f32 v24,-v74,v88,v24
v_fma_f32 v25,-v74,v89,v25
v_mul_f32 v98,v22,v22
v_mul_f32 v99,v23,v23
s_add_u32 m0,s57,16384
s_add_u32 s56,s55,16384
D 0,56,0
v_mul_f32 v100,v24,v24
v_mul_f32 v101,v25,v25
v_fma_f32 v98,v76,v98,v75
v_fma_f32 v99,v76,v99,v75
v_fma_f32 v100,v76,v100,v75
v_fma_f32 v101,v76,v101,v75
v_mul_f32 v98,v22,v98
v_mul_f32 v99,v23,v99
D 0,56,1024
v_mul_f32 v100,v24,v100
v_mul_f32 v101,v25,v101
v_exp_f32 v98,v98
v_exp_f32 v99,v99
v_exp_f32 v100,v100
v_exp_f32 v101,v101
v_fma_f32 v98,v98,v77,v77
v_fma_f32 v99,v99,v77,v77
D 0,56,2048
v_fma_f32 v100,v100,v77,v77
v_fma_f32 v101,v101,v77,v77
v_rcp_f32 v98,v98
v_rcp_f32 v99,v99
v_rcp_f32 v100,v100
v_rcp_f32 v101,v101
v_mul_f32 v22,v22,v98
v_mul_f32 v23,v23,v99
D 0,56,3072
v_mul_f32 v24,v24,v100
v_mul_f32 v25,v25,v101
v_cvt_pk_bf16_f32 v108,v22,v23
v_cvt_pk_bf16_f32 v109,v24,v25
buffer_store_dwordx4 v[106:109],v0,s[36:39],s66 offen offset:0 sc0 sc1
v_fma_f32 v26,-v74,v90,v26
v_fma_f32 v27,-v74,v91,v27
v_fma_f32 v28,-v74,v92,v28
v_fma_f32 v29,-v74,v93,v29
s_add_u32 s55,s55,s60
s_sub_u32 s57,s84,s57
s_add_u32 s54,s54,s46
v_mul_f32 v102,v26,v26
v_mul_f32 v103,v27,v27
v_mul_f32 v104,v28,v28
v_mul_f32 v105,v29,v29
v_fma_f32 v102,v76,v102,v75
v_fma_f32 v103,v76,v103,v75
v_fma_f32 v104,v76,v104,v75
v_fma_f32 v105,v76,v105,v75
v_mul_f32 v102,v26,v102
v_mul_f32 v103,v27,v103
v_mul_f32 v104,v28,v104
v_mul_f32 v105,v29,v105
v_exp_f32 v102,v102
v_exp_f32 v103,v103
v_exp_f32 v104,v104
v_exp_f32 v105,v105
v_fma_f32 v102,v102,v77,v77
v_fma_f32 v103,v103,v77,v77
v_fma_f32 v104,v104,v77,v77
v_fma_f32 v105,v105,v77,v77
v_rcp_f32 v102,v102
v_rcp_f32 v103,v103
v_rcp_f32 v104,v104
v_rcp_f32 v105,v105
v_mul_f32 v26,v26,v102
v_mul_f32 v27,v27,v103
v_mul_f32 v28,v28,v104
v_mul_f32 v29,v29,v105
v_cvt_pk_bf16_f32 v110,v26,v27
v_cvt_pk_bf16_f32 v111,v28,v29
s_nop 0
v_permlane16_swap_b32 v110,v112
v_permlane16_swap_b32 v111,v113
buffer_store_dwordx4 v[110:113],v10,s[36:39],s66 offen offset:0 sc0 sc1
s_mov_b32 s72,s69
s_mov_b32 s69,0x7fff0000
s_mov_b32 s66,0x7fff0000
s_mov_b32 s76,0x7fff0000
s_branch Lg3_gox_%=
Lg3_xn0_%=:
s_cmp_ge_u32 s54,256
s_cbranch_scc1 Lg3_fin0_%=
s_add_u32 m0,s59,4096
s_nop 0
buffer_load_dword v8,s[36:39],s58 offen offset:0 sc1 lds
buffer_store_dword v4,v3,s[36:39],s71 offen offset:0 sc0 sc1
s_add_u32 m0,s59,5120
s_nop 0
buffer_load_dword v8,s[36:39],s58 offen offset:0 sc1 lds
buffer_store_dword v4,v3,s[36:39],s71 offen offset:0 sc0 sc1
s_mov_b32 s83,0
L 0
v_fma_f32 v18,-v74,v82,v18
v_fma_f32 v19,-v74,v83,v19
v_fma_f32 v20,-v74,v84,v20
v_fma_f32 v21,-v74,v85,v21
v_mul_f32 v94,v18,v18
v_mul_f32 v95,v19,v19
v_mul_f32 v96,v20,v20
v_mul_f32 v97,v21,v21
v_fma_f32 v94,v76,v94,v75
v_fma_f32 v95,v76,v95,v75
v_fma_f32 v96,v76,v96,v75
v_fma_f32 v97,v76,v97,v75
v_mul_f32 v94,v18,v94
v_mul_f32 v95,v19,v95
v_mul_f32 v96,v20,v96
v_mul_f32 v97,v21,v97
v_exp_f32 v94,v94
v_exp_f32 v95,v95
v_exp_f32 v96,v96
v_exp_f32 v97,v97
v_fma_f32 v94,v94,v77,v77
v_fma_f32 v95,v95,v77,v77
v_fma_f32 v96,v96,v77,v77
v_fma_f32 v97,v97,v77,v77
v_rcp_f32 v94,v94
v_rcp_f32 v95,v95
v_rcp_f32 v96,v96
v_rcp_f32 v97,v97
v_mul_f32 v18,v18,v94
v_mul_f32 v19,v19,v95
v_mul_f32 v20,v20,v96
v_mul_f32 v21,v21,v97
v_cvt_pk_bf16_f32 v106,v18,v19
v_cvt_pk_bf16_f32 v107,v20,v21
v_fma_f32 v22,-v74,v86,v22
v_fma_f32 v23,-v74,v87,v23
v_fma_f32 v24,-v74,v88,v24
v_fma_f32 v25,-v74,v89,v25
v_mul_f32 v98,v22,v22
v_mul_f32 v99,v23,v23
v_mul_f32 v100,v24,v24
v_mul_f32 v101,v25,v25
v_fma_f32 v98,v76,v98,v75
v_fma_f32 v99,v76,v99,v75
v_fma_f32 v100,v76,v100,v75
v_fma_f32 v101,v76,v101,v75
v_mul_f32 v98,v22,v98
v_mul_f32 v99,v23,v99
v_mul_f32 v100,v24,v100
v_mul_f32 v101,v25,v101
v_exp_f32 v98,v98
v_exp_f32 v99,v99
v_exp_f32 v100,v100
v_exp_f32 v101,v101
v_fma_f32 v98,v98,v77,v77
v_fma_f32 v99,v99,v77,v77
v_fma_f32 v100,v100,v77,v77
v_fma_f32 v101,v101,v77,v77
v_rcp_f32 v98,v98
v_rcp_f32 v99,v99
v_rcp_f32 v100,v100
v_rcp_f32 v101,v101
v_mul_f32 v22,v22,v98
v_mul_f32 v23,v23,v99
v_mul_f32 v24,v24,v100
v_mul_f32 v25,v25,v101
v_cvt_pk_bf16_f32 v108,v22,v23
v_cvt_pk_bf16_f32 v109,v24,v25
buffer_store_dwordx4 v[106:109],v0,s[36:39],s66 offen offset:0 sc0 sc1
v_fma_f32 v26,-v74,v90,v26
v_fma_f32 v27,-v74,v91,v27
v_fma_f32 v28,-v74,v92,v28
v_fma_f32 v29,-v74,v93,v29
v_mul_f32 v102,v26,v26
v_mul_f32 v103,v27,v27
v_mul_f32 v104,v28,v28
v_mul_f32 v105,v29,v29
v_fma_f32 v102,v76,v102,v75
v_fma_f32 v103,v76,v103,v75
v_fma_f32 v104,v76,v104,v75
v_fma_f32 v105,v76,v105,v75
v_mul_f32 v102,v26,v102
v_mul_f32 v103,v27,v103
v_mul_f32 v104,v28,v104
v_mul_f32 v105,v29,v105
v_exp_f32 v102,v102
v_exp_f32 v103,v103
v_exp_f32 v104,v104
v_exp_f32 v105,v105
v_fma_f32 v102,v102,v77,v77
v_fma_f32 v103,v103,v77,v77
v_fma_f32 v104,v104,v77,v77
v_fma_f32 v105,v105,v77,v77
v_rcp_f32 v102,v102
v_rcp_f32 v103,v103
v_rcp_f32 v104,v104
v_rcp_f32 v105,v105
v_mul_f32 v26,v26,v102
v_mul_f32 v27,v27,v103
v_mul_f32 v28,v28,v104
v_mul_f32 v29,v29,v105
v_cvt_pk_bf16_f32 v110,v26,v27
v_cvt_pk_bf16_f32 v111,v28,v29
s_nop 0
v_permlane16_swap_b32 v110,v112
v_permlane16_swap_b32 v111,v113
buffer_store_dwordx4 v[110:113],v10,s[36:39],s66 offen offset:0 sc0 sc1
s_mov_b32 s72,s69
s_mov_b32 s69,0x7fff0000
s_mov_b32 s66,0x7fff0000
s_mov_b32 s76,0x7fff0000
s_branch Lg3_poll_%=
Lg3_fin0_%=:
L 0
v_fma_f32 v18,-v74,v82,v18
v_fma_f32 v19,-v74,v83,v19
v_fma_f32 v20,-v74,v84,v20
v_fma_f32 v21,-v74,v85,v21
v_mul_f32 v94,v18,v18
v_mul_f32 v95,v19,v19
v_mul_f32 v96,v20,v20
v_mul_f32 v97,v21,v21
v_fma_f32 v94,v76,v94,v75
v_fma_f32 v95,v76,v95,v75
v_fma_f32 v96,v76,v96,v75
v_fma_f32 v97,v76,v97,v75
v_mul_f32 v94,v18,v94
v_mul_f32 v95,v19,v95
v_mul_f32 v96,v20,v96
v_mul_f32 v97,v21,v97
v_exp_f32 v94,v94
v_exp_f32 v95,v95
v_exp_f32 v96,v96
v_exp_f32 v97,v97
v_fma_f32 v94,v94,v77,v77
v_fma_f32 v95,v95,v77,v77
v_fma_f32 v96,v96,v77,v77
v_fma_f32 v97,v97,v77,v77
v_rcp_f32 v94,v94
v_rcp_f32 v95,v95
v_rcp_f32 v96,v96
v_rcp_f32 v97,v97
v_mul_f32 v18,v18,v94
v_mul_f32 v19,v19,v95
v_mul_f32 v20,v20,v96
v_mul_f32 v21,v21,v97
v_cvt_pk_bf16_f32 v106,v18,v19
v_cvt_pk_bf16_f32 v107,v20,v21
v_fma_f32 v22,-v74,v86,v22
v_fma_f32 v23,-v74,v87,v23
v_fma_f32 v24,-v74,v88,v24
v_fma_f32 v25,-v74,v89,v25
v_mul_f32 v98,v22,v22
v_mul_f32 v99,v23,v23
v_mul_f32 v100,v24,v24
v_mul_f32 v101,v25,v25
v_fma_f32 v98,v76,v98,v75
v_fma_f32 v99,v76,v99,v75
v_fma_f32 v100,v76,v100,v75
v_fma_f32 v101,v76,v101,v75
v_mul_f32 v98,v22,v98
v_mul_f32 v99,v23,v99
v_mul_f32 v100,v24,v100
v_mul_f32 v101,v25,v101
v_exp_f32 v98,v98
v_exp_f32 v99,v99
v_exp_f32 v100,v100
v_exp_f32 v101,v101
v_fma_f32 v98,v98,v77,v77
v_fma_f32 v99,v99,v77,v77
v_fma_f32 v100,v100,v77,v77
v_fma_f32 v101,v101,v77,v77
v_rcp_f32 v98,v98
v_rcp_f32 v99,v99
v_rcp_f32 v100,v100
v_rcp_f32 v101,v101
v_mul_f32 v22,v22,v98
v_mul_f32 v23,v23,v99
v_mul_f32 v24,v24,v100
v_mul_f32 v25,v25,v101
v_cvt_pk_bf16_f32 v108,v22,v23
v_cvt_pk_bf16_f32 v109,v24,v25
buffer_store_dwordx4 v[106:109],v0,s[36:39],s66 offen offset:0 sc0 sc1
v_fma_f32 v26,-v74,v90,v26
v_fma_f32 v27,-v74,v91,v27
v_fma_f32 v28,-v74,v92,v28
v_fma_f32 v29,-v74,v93,v29
v_mul_f32 v102,v26,v26
v_mul_f32 v103,v27,v27
v_mul_f32 v104,v28,v28
v_mul_f32 v105,v29,v29
v_fma_f32 v102,v76,v102,v75
v_fma_f32 v103,v76,v103,v75
v_fma_f32 v104,v76,v104,v75
v_fma_f32 v105,v76,v105,v75
v_mul_f32 v102,v26,v102
v_mul_f32 v103,v27,v103
v_mul_f32 v104,v28,v104
v_mul_f32 v105,v29,v105
v_exp_f32 v102,v102
v_exp_f32 v103,v103
v_exp_f32 v104,v104
v_exp_f32 v105,v105
v_fma_f32 v102,v102,v77,v77
v_fma_f32 v103,v103,v77,v77
v_fma_f32 v104,v104,v77,v77
v_fma_f32 v105,v105,v77,v77
v_rcp_f32 v102,v102
v_rcp_f32 v103,v103
v_rcp_f32 v104,v104
v_rcp_f32 v105,v105
v_mul_f32 v26,v26,v102
v_mul_f32 v27,v27,v103
v_mul_f32 v28,v28,v104
v_mul_f32 v29,v29,v105
v_cvt_pk_bf16_f32 v110,v26,v27
v_cvt_pk_bf16_f32 v111,v28,v29
s_nop 0
v_permlane16_swap_b32 v110,v112
v_permlane16_swap_b32 v111,v113
buffer_store_dwordx4 v[110:113],v10,s[36:39],s66 offen offset:0 sc0 sc1
s_mov_b32 s72,s69
s_mov_b32 s69,0x7fff0000
s_mov_b32 s66,0x7fff0000
s_mov_b32 s76,0x7fff0000
s_branch Lg3_done_%=
Lg3_L1_%=:
v_mfma_f32_16x16x32_bf16 v[38:41],a[8:11],v[42:45],0
s_add_u32 m0,s59,3072
R 62,2,5120
v_add_f32_dpp v115,v114,v114 quad_perm:[1,0,3,2] row_mask:0xf bank_mask:0xf
M 38,a[20:23],46
s_mov_b32 s67,s65
buffer_load_dword v8,s[36:39],s58 offen offset:0 sc1 lds
v_mfma_f32_16x16x32_bf16 v[34:37],a[4:7],v[42:45],0
s_add_u32 s65,s65,s62
v_add_f32_dpp v115,v115,v115 quad_perm:[2,3,0,1] row_mask:0xf bank_mask:0xf
v_fma_f32 v18,-v74,v82,v18
M 34,a[16:19],46
R 66,2,6144
v_fma_f32 v19,-v74,v83,v19
v_mfma_f32_16x16x32_bf16 v[30:33],a[0:3],v[42:45],0
v_add_f32_dpp v118,v115,v115 row_half_mirror row_mask:0xf bank_mask:0xf
v_fma_f32 v20,-v74,v84,v20
v_fma_f32 v21,-v74,v85,v21
M 30,a[12:15],46
L 3
v_mul_f32 v94,v18,v18
v_mul_f32 v95,v19,v19
M 30,a[24:27],50
R 70,2,7168
v_mov_b32_dpp v115,v118 row_ror:8 row_mask:0xf bank_mask:0xf
M 30,a[36:39],54
v_fma_f32 v115,-v118,v118,v115
v_max_f32 v115,s87,v115
M 34,a[28:31],50
v_rsq_f32 v116,v115
v_mul_f32 v96,v20,v20
M 34,a[40:43],54
R 42,2,8192
v_mul_f32 v121,v115,v116
M 38,a[32:35],50
v_mul_f32 v119,s88,v116
v_mul_f32 v115,s89,v116
M 38,a[44:47],54
L 3
v_mul_f32 v120,v119,v116
v_mul_f32 v120,v120,v115
M 38,a[56:59],58
R 46,2,9216
v_mul_f32 v97,v21,v21
M 38,a[68:71],62
ds_write_b128 v12,v[118:121] offset:2048
M 34,a[52:55],58
v_fma_f32 v94,v76,v94,v75
v_fma_f32 v95,v76,v95,v75
M 34,a[64:67],62
R 50,2,10240
v_fma_f32 v96,v76,v96,v75
M 30,a[48:51],58
v_fma_f32 v97,v76,v97,v75
v_mul_f32 v94,v18,v94
M 30,a[60:63],62
L 4
v_exp_f32 v94,v94
v_mul_f32 v95,v19,v95
M 30,a[72:75],66
R 54,2,11264
v_mul_f32 v96,v20,v96
M 30,a[84:87],70
v_exp_f32 v95,v95
M 34,a[76:79],66
v_exp_f32 v96,v96
M 34,a[88:91],70
R 58,2,12288
v_mul_f32 v97,v21,v97
M 38,a[80:83],66
v_exp_f32 v97,v97
v_fma_f32 v94,v94,v77,v77
M 38,a[92:95],70
L 4
v_rcp_f32 v94,v94
M 38,a[104:107],42
R 62,2,13312
v_fma_f32 v95,v95,v77,v77
M 38,a[116:119],46
v_rcp_f32 v95,v95
M 34,a[100:103],42
v_fma_f32 v96,v96,v77,v77
v_rcp_f32 v96,v96
M 34,a[112:115],46
R 66,2,14336
v_fma_f32 v97,v97,v77,v77
M 30,a[96:99],42
v_rcp_f32 v97,v97
M 30,a[108:111],46
L 3
v_mul_f32 v18,v18,v94
v_mul_f32 v19,v19,v95
M 30,a[120:123],50
R 70,2,15360
v_mul_f32 v20,v20,v96
M 30,a[132:135],54
s_waitcnt vmcnt(0)
v_mul_f32 v21,v21,v97
v_cvt_pk_bf16_f32 v106,v18,v19
v_cvt_pk_bf16_f32 v107,v20,v21
M 34,a[124:127],50
s_barrier
ds_read_b32 v7,v6 offset:3072
v_fma_f32 v22,-v74,v86,v22
M 34,a[136:139],54
R 42,2,16384
v_fma_f32 v23,-v74,v87,v23
M 38,a[128:131],50
buffer_store_dword v4,v3,s[36:39],s70 offen offset:0 sc0 sc1
M 38,a[140:143],54
L 4
v_fma_f32 v24,-v74,v88,v24
v_fma_f32 v25,-v74,v89,v25
v_mul_f32 v98,v22,v22
M 38,a[152:155],58
s_mov_b32 s70,0x7fff0000
R 46,2,17408
v_mul_f32 v99,v23,v23
M 38,a[164:167],62
v_mul_f32 v100,v24,v24
v_mul_f32 v101,v25,v25
M 34,a[148:151],58
L 2
v_cmp_eq_u32 vcc,s53,v7
v_fma_f32 v98,v76,v98,v75
M 34,a[160:163],62
R 50,2,18432
v_fma_f32 v99,v76,v99,v75
M 30,a[144:147],58
s_cmp_eq_u32 vcc_lo,-1
v_fma_f32 v100,v76,v100,v75
v_fma_f32 v101,v76,v101,v75
v_mul_f32 v98,v22,v98
s_cbranch_scc0 Lg3_X1_%=
M 30,a[156:159],62
s_nop 0
s_add_u32 m0,s57,0
s_add_u32 s56,s55,0
buffer_load_dword v114,v11,s[36:39],s73 offen offset:0 sc1
M 30,a[168:171],66
s_add_u32 s58,s58,s61
R 54,2,19456
v_mul_f32 v99,v23,v99
M 30,a[180:183],70
s_add_u32 s54,s54,s46
D 0,56,0
M 34,a[172:175],66
s_add_u32 s73,s73,s64
v_exp_f32 v98,v98
v_mul_f32 v100,v24,v100
)"
R"(M 34,a[184:187],70
R 58,2,20480
v_mul_f32 v101,v25,v101
M 38,a[176:179],66
v_exp_f32 v99,v99
M 38,a[188:191],70
L 3
D 0,56,1024
M 38,a[200:203],42
R 62,2,21504
v_exp_f32 v100,v100
M 38,a[212:215],46
v_exp_f32 v101,v101
M 34,a[196:199],42
v_fma_f32 v98,v98,v77,v77
v_fma_f32 v99,v99,v77,v77
M 34,a[208:211],46
R 66,2,22528
v_fma_f32 v100,v100,v77,v77
M 30,a[192:195],42
D 0,56,2048
M 30,a[204:207],46
L 3
v_rcp_f32 v98,v98
v_fma_f32 v101,v101,v77,v77
M 30,a[216:219],50
R 70,2,23552
v_mul_f32 v22,v22,v98
M 30,a[228:231],54
D 0,56,3072
M 34,a[220:223],50
s_add_u32 m0,s59,1024
v_rcp_f32 v99,v99
M 34,a[232:235],54
s_add_u32 s56,s55,16384
R 42,2,24576
v_rcp_f32 v100,v100
M 38,a[224:227],50
s_add_u32 s55,s55,s60
buffer_load_dword v8,s[36:39],s58 offen offset:0 sc1 lds
M 38,a[236:239],54
L 3
v_rcp_f32 v101,v101
M 38,a[248:251],58
s_add_u32 m0,s57,16384
R 46,2,25600
v_mul_f32 v23,v23,v99
M 38,v[132:135],62
s_sub_u32 s57,s84,s57
D 0,56,0
M 34,a[244:247],58
v_mul_f32 v24,v24,v100
v_mul_f32 v25,v25,v101
v_cvt_pk_bf16_f32 v108,v22,v23
M 34,v[128:131],62
R 50,2,26624
v_cvt_pk_bf16_f32 v109,v24,v25
M 30,a[240:243],58
buffer_store_dwordx4 v[106:109],v0,s[36:39],s66 offen offset:0 sc0 sc1
v_fma_f32 v26,-v74,v90,v26
M 30,a[252:255],62
L 3
v_fma_f32 v27,-v74,v91,v27
v_fma_f32 v28,-v74,v92,v28
M 30,v[136:139],66
R 54,2,27648
v_fma_f32 v29,-v74,v93,v29
v_mul_f32 v102,v26,v26
M 30,v[148:151],70
D 0,56,1024
M 34,v[140:143],66
v_mul_f32 v103,v27,v27
v_mul_f32 v104,v28,v28
M 34,v[152:155],70
R 58,2,28672
v_mul_f32 v105,v29,v29
M 38,v[144:147],66
v_fma_f32 v102,v76,v102,v75
v_fma_f32 v103,v76,v103,v75
M 38,v[156:159],70
L 3
D 0,56,2048
v_fma_f32 v104,v76,v104,v75
M 38,v[168:171],42
R 62,2,29696
v_fma_f32 v105,v76,v105,v75
M 38,v[180:183],46
v_mul_f32 v102,v26,v102
v_mul_f32 v103,v27,v103
M 34,v[164:167],42
v_exp_f32 v102,v102
M 34,v[176:179],46
R 66,2,30720
v_exp_f32 v103,v103
M 30,v[160:163],42
D 0,56,3072
M 30,v[172:175],46
L 3
v_mul_f32 v104,v28,v104
v_mul_f32 v105,v29,v105
M 30,v[184:187],50
s_nop 0
R 70,2,31744
v_fma_f32 v102,v102,v77,v77
M 30,v[196:199],54
s_waitcnt vmcnt(5)
v_exp_f32 v104,v104
M 34,v[188:191],50
s_barrier
buffer_store_dword v4,v3,s[36:39],s70 offen offset:0 sc0 sc1
v_fma_f32 v103,v103,v77,v77
M 34,v[200:203],54
s_mov_b32 s70,s69
R 42,1,0
ds_read_b32 v7,v6 offset:1024
M 38,v[192:195],50
s_mov_b32 s69,s68
R 78,13,2048
v_fma_f32 v104,v104,v77,v77
M 38,v[204:207],54
L 5
v_exp_f32 v105,v105
M 38,v[216:219],58
s_add_u32 s68,s68,s63
R 46,1,1024
v_rcp_f32 v102,v102
M 38,v[228:231],62
v_rcp_f32 v103,v103
M 34,v[212:215],58
v_rcp_f32 v104,v104
M 34,v[224:227],62
L 2
R 50,1,2048
v_fma_f32 v105,v105,v77,v77
M 30,v[208:211],58
v_rcp_f32 v105,v105
v_mul_f32 v26,v26,v102
M 30,v[220:223],62
s_nop 0
v_mul_f32 v27,v27,v103
v_mul_f32 v28,v28,v104
M 30,v[232:235],66
R 54,1,3072
v_mul_f32 v29,v29,v105
M 30,v[244:247],70
v_cvt_pk_bf16_f32 v110,v26,v27
v_cvt_pk_bf16_f32 v111,v28,v29
M 34,v[236:239],66
v_cmp_eq_u32 vcc,s53,v7
M 34,v[248:251],70
s_cmp_eq_u32 vcc_lo,-1
R 58,1,4096
v_permlane16_swap_b32 v110,v112
v_permlane16_swap_b32 v111,v113
M 38,v[240:243],66
buffer_store_dwordx4 v[110:113],v10,s[36:39],s66 offen offset:0 sc0 sc1
M 38,v[252:255],70
L 3
s_cbranch_scc1 Lg3_F0_%=
s_branch Lg3_L0_%=
Lg3_X1_%=:
M 30,a[156:159],62
s_nop 0
s_add_u32 m0,s59,1024
v_mul_f32 v99,v23,v99
M 30,a[168:171],66
R 54,2,19456
v_mul_f32 v100,v24,v100
M 30,a[180:183],70
buffer_load_dword v8,s[36:39],s58 offen offset:0 sc1 lds
M 34,a[172:175],66
v_exp_f32 v98,v98
M 34,a[184:187],70
R 58,2,20480
v_mul_f32 v101,v25,v101
M 38,a[176:179],66
v_exp_f32 v99,v99
M 38,a[188:191],70
L 3
v_exp_f32 v100,v100
M 38,a[200:203],42
R 62,2,21504
v_fma_f32 v98,v98,v77,v77
M 38,a[212:215],46
v_exp_f32 v101,v101
M 34,a[196:199],42
v_rcp_f32 v98,v98
M 34,a[208:211],46
R 66,2,22528
v_fma_f32 v99,v99,v77,v77
M 30,a[192:195],42
v_rcp_f32 v99,v99
M 30,a[204:207],46
L 3
v_fma_f32 v100,v100,v77,v77
v_fma_f32 v101,v101,v77,v77
M 30,a[216:219],50
R 70,2,23552
v_mul_f32 v22,v22,v98
M 30,a[228:231],54
v_rcp_f32 v100,v100
M 34,a[220:223],50
v_rcp_f32 v101,v101
M 34,a[232:235],54
R 42,2,24576
v_mul_f32 v23,v23,v99
M 38,a[224:227],50
v_mul_f32 v24,v24,v100
v_mul_f32 v25,v25,v101
M 38,a[236:239],54
L 3
v_cvt_pk_bf16_f32 v108,v22,v23
v_cvt_pk_bf16_f32 v109,v24,v25
M 38,a[248:251],58
R 46,2,25600
v_fma_f32 v26,-v74,v90,v26
M 38,v[132:135],62
buffer_store_dwordx4 v[106:109],v0,s[36:39],s66 offen offset:0 sc0 sc1
v_fma_f32 v27,-v74,v91,v27
M 34,a[244:247],58
v_fma_f32 v28,-v74,v92,v28
v_fma_f32 v29,-v74,v93,v29
M 34,v[128:131],62
R 50,2,26624
v_mul_f32 v102,v26,v26
M 30,a[240:243],58
v_mul_f32 v103,v27,v27
v_mul_f32 v104,v28,v28
M 30,a[252:255],62
L 3
v_mul_f32 v105,v29,v29
v_fma_f32 v102,v76,v102,v75
M 30,v[136:139],66
R 54,2,27648
v_fma_f32 v103,v76,v103,v75
M 30,v[148:151],70
v_fma_f32 v104,v76,v104,v75
v_fma_f32 v105,v76,v105,v75
M 34,v[140:143],66
v_mul_f32 v102,v26,v102
v_mul_f32 v103,v27,v103
M 34,v[152:155],70
R 58,2,28672
v_mul_f32 v104,v28,v104
M 38,v[144:147],66
v_exp_f32 v102,v102
M 38,v[156:159],70
L 3
v_exp_f32 v103,v103
M 38,v[168:171],42
R 62,2,29696
v_mul_f32 v105,v29,v105
M 38,v[180:183],46
v_exp_f32 v104,v104
M 34,v[164:167],42
v_exp_f32 v105,v105
M 34,v[176:179],46
R 66,2,30720
v_fma_f32 v102,v102,v77,v77
M 30,v[160:163],42
v_rcp_f32 v102,v102
M 30,v[172:175],46
L 3
v_fma_f32 v103,v103,v77,v77
v_fma_f32 v104,v104,v77,v77
M 30,v[184:187],50
s_nop 0
R 70,2,31744
v_fma_f32 v105,v105,v77,v77
M 30,v[196:199],54
s_waitcnt vmcnt(1)
v_rcp_f32 v103,v103
M 34,v[188:191],50
s_barrier
buffer_store_dword v4,v3,s[36:39],s70 offen offset:0 sc0 sc1
M 34,v[200:203],54
s_mov_b32 s70,s69
ds_read_b32 v7,v6 offset:1024
R 78,13,2048
M 38,v[192:195],50
s_mov_b32 s69,s68
v_rcp_f32 v104,v104
M 38,v[204:207],54
L 4
v_rcp_f32 v105,v105
M 38,v[216:219],58
s_add_u32 s68,s68,s63
v_mul_f32 v26,v26,v102
v_mul_f32 v27,v27,v103
M 38,v[228:231],62
v_mul_f32 v28,v28,v104
v_mul_f32 v29,v29,v105
M 34,v[212:215],58
v_cvt_pk_bf16_f32 v110,v26,v27
v_cvt_pk_bf16_f32 v111,v28,v29
M 34,v[224:227],62
L 1
v_cmp_eq_u32 vcc,s53,v7
M 30,v[208:211],58
v_permlane16_swap_b32 v110,v112
M 30,v[220:223],62
s_nop 0
v_permlane16_swap_b32 v111,v113
M 30,v[232:235],66
buffer_store_dwordx4 v[110:113],v10,s[36:39],s66 offen offset:0 sc0 sc1
M 30,v[244:247],70
M 34,v[236:239],66
M 34,v[248:251],70
M 38,v[240:243],66
M 38,v[252:255],70
s_mov_b32 s80,0
s_cmp_eq_u32 vcc_lo,-1
s_cbranch_scc0 Lg3_xn1_%=
buffer_load_dword v114,v11,s[36:39],s73 offen offset:0 sc1
L 0
v_fma_f32 v30,-v78,v82,v30
v_fma_f32 v31,-v78,v83,v31
v_fma_f32 v32,-v78,v84,v32
v_fma_f32 v33,-v78,v85,v33
v_mul_f32 v94,v30,v30
v_mul_f32 v95,v31,v31
v_mul_f32 v96,v32,v32
v_mul_f32 v97,v33,v33
s_add_u32 s73,s73,s64
s_add_u32 m0,s57,0
s_add_u32 s56,s55,0
D 0,56,0
v_fma_f32 v94,v80,v94,v79
v_fma_f32 v95,v80,v95,v79
v_fma_f32 v96,v80,v96,v79
v_fma_f32 v97,v80,v97,v79
v_mul_f32 v94,v30,v94
v_mul_f32 v95,v31,v95
v_mul_f32 v96,v32,v96
v_mul_f32 v97,v33,v97
D 0,56,1024
v_exp_f32 v94,v94
v_exp_f32 v95,v95
v_exp_f32 v96,v96
v_exp_f32 v97,v97
v_fma_f32 v94,v94,v81,v81
v_fma_f32 v95,v95,v81,v81
v_fma_f32 v96,v96,v81,v81
v_fma_f32 v97,v97,v81,v81
D 0,56,2048
v_rcp_f32 v94,v94
v_rcp_f32 v95,v95
v_rcp_f32 v96,v96
v_rcp_f32 v97,v97
v_mul_f32 v30,v30,v94
v_mul_f32 v31,v31,v95
v_mul_f32 v32,v32,v96
v_mul_f32 v33,v33,v97
D 0,56,3072
v_cvt_pk_bf16_f32 v106,v30,v31
v_cvt_pk_bf16_f32 v107,v32,v33
v_fma_f32 v34,-v78,v86,v34
v_fma_f32 v35,-v78,v87,v35
v_fma_f32 v36,-v78,v88,v36
v_fma_f32 v37,-v78,v89,v37
v_mul_f32 v98,v34,v34
v_mul_f32 v99,v35,v35
s_add_u32 m0,s57,16384
s_add_u32 s56,s55,16384
D 0,56,0
v_mul_f32 v100,v36,v36
v_mul_f32 v101,v37,v37
v_fma_f32 v98,v80,v98,v79
v_fma_f32 v99,v80,v99,v79
v_fma_f32 v100,v80,v100,v79
v_fma_f32 v101,v80,v101,v79
v_mul_f32 v98,v34,v98
v_mul_f32 v99,v35,v99
D 0,56,1024
v_mul_f32 v100,v36,v100
v_mul_f32 v101,v37,v101
v_exp_f32 v98,v98
v_exp_f32 v99,v99
v_exp_f32 v100,v100
v_exp_f32 v101,v101
v_fma_f32 v98,v98,v81,v81
v_fma_f32 v99,v99,v81,v81
D 0,56,2048
v_fma_f32 v100,v100,v81,v81
v_fma_f32 v101,v101,v81,v81
v_rcp_f32 v98,v98
v_rcp_f32 v99,v99
v_rcp_f32 v100,v100
v_rcp_f32 v101,v101
v_mul_f32 v34,v34,v98
v_mul_f32 v35,v35,v99
D 0,56,3072
v_mul_f32 v36,v36,v100
v_mul_f32 v37,v37,v101
v_cvt_pk_bf16_f32 v108,v34,v35
v_cvt_pk_bf16_f32 v109,v36,v37
buffer_store_dwordx4 v[106:109],v0,s[36:39],s67 offen offset:0 sc0 sc1
v_fma_f32 v38,-v78,v90,v38
v_fma_f32 v39,-v78,v91,v39
v_fma_f32 v40,-v78,v92,v40
v_fma_f32 v41,-v78,v93,v41
s_add_u32 s55,s55,s60
s_sub_u32 s57,s84,s57
s_add_u32 s54,s54,s46
v_mul_f32 v102,v38,v38
v_mul_f32 v103,v39,v39
v_mul_f32 v104,v40,v40
v_mul_f32 v105,v41,v41
v_fma_f32 v102,v80,v102,v79
v_fma_f32 v103,v80,v103,v79
v_fma_f32 v104,v80,v104,v79
v_fma_f32 v105,v80,v105,v79
v_mul_f32 v102,v38,v102
v_mul_f32 v103,v39,v103
v_mul_f32 v104,v40,v104
v_mul_f32 v105,v41,v105
v_exp_f32 v102,v102
v_exp_f32 v103,v103
v_exp_f32 v104,v104
v_exp_f32 v105,v105
v_fma_f32 v102,v102,v81,v81
v_fma_f32 v103,v103,v81,v81
v_fma_f32 v104,v104,v81,v81
v_fma_f32 v105,v105,v81,v81
v_rcp_f32 v102,v102
v_rcp_f32 v103,v103
v_rcp_f32 v104,v104
v_rcp_f32 v105,v105
v_mul_f32 v38,v38,v102
v_mul_f32 v39,v39,v103
v_mul_f32 v40,v40,v104
v_mul_f32 v41,v41,v105
v_cvt_pk_bf16_f32 v110,v38,v39
v_cvt_pk_bf16_f32 v111,v40,v41
s_nop 0
v_permlane16_swap_b32 v110,v112
v_permlane16_swap_b32 v111,v113
buffer_store_dwordx4 v[110:113],v10,s[36:39],s67 offen offset:0 sc0 sc1
s_mov_b32 s72,s69
s_mov_b32 s69,0x7fff0000
s_mov_b32 s67,0x7fff0000
s_mov_b32 s77,0x7fff0000
s_branch Lg3_gox_%=
Lg3_xn1_%=:
s_cmp_ge_u32 s54,256
s_cbranch_scc1 Lg3_fin1_%=
s_add_u32 m0,s59,4096
s_nop 0
buffer_load_dword v8,s[36:39],s58 offen offset:0 sc1 lds
buffer_store_dword v4,v3,s[36:39],s71 offen offset:0 sc0 sc1
s_add_u32 m0,s59,5120
s_nop 0
buffer_load_dword v8,s[36:39],s58 offen offset:0 sc1 lds
buffer_store_dword v4,v3,s[36:39],s71 offen offset:0 sc0 sc1
s_mov_b32 s83,0
L 0
v_fma_f32 v30,-v78,v82,v30
v_fma_f32 v31,-v78,v83,v31
v_fma_f32 v32,-v78,v84,v32
v_fma_f32 v33,-v78,v85,v33
v_mul_f32 v94,v30,v30
v_mul_f32 v95,v31,v31
v_mul_f32 v96,v32,v32
v_mul_f32 v97,v33,v33
v_fma_f32 v94,v80,v94,v79
v_fma_f32 v95,v80,v95,v79
v_fma_f32 v96,v80,v96,v79
v_fma_f32 v97,v80,v97,v79
v_mul_f32 v94,v30,v94
v_mul_f32 v95,v31,v95
v_mul_f32 v96,v32,v96
v_mul_f32 v97,v33,v97
v_exp_f32 v94,v94
v_exp_f32 v95,v95
v_exp_f32 v96,v96
v_exp_f32 v97,v97
v_fma_f32 v94,v94,v81,v81
v_fma_f32 v95,v95,v81,v81
v_fma_f32 v96,v96,v81,v81
v_fma_f32 v97,v97,v81,v81
v_rcp_f32 v94,v94
v_rcp_f32 v95,v95
v_rcp_f32 v96,v96
v_rcp_f32 v97,v97
v_mul_f32 v30,v30,v94
v_mul_f32 v31,v31,v95
v_mul_f32 v32,v32,v96
v_mul_f32 v33,v33,v97
v_cvt_pk_bf16_f32 v106,v30,v31
v_cvt_pk_bf16_f32 v107,v32,v33
v_fma_f32 v34,-v78,v86,v34
v_fma_f32 v35,-v78,v87,v35
v_fma_f32 v36,-v78,v88,v36
v_fma_f32 v37,-v78,v89,v37
v_mul_f32 v98,v34,v34
v_mul_f32 v99,v35,v35
v_mul_f32 v100,v36,v36
v_mul_f32 v101,v37,v37
v_fma_f32 v98,v80,v98,v79
v_fma_f32 v99,v80,v99,v79
)"
R"(v_fma_f32 v100,v80,v100,v79
v_fma_f32 v101,v80,v101,v79
v_mul_f32 v98,v34,v98
v_mul_f32 v99,v35,v99
v_mul_f32 v100,v36,v100
v_mul_f32 v101,v37,v101
v_exp_f32 v98,v98
v_exp_f32 v99,v99
v_exp_f32 v100,v100
v_exp_f32 v101,v101
v_fma_f32 v98,v98,v81,v81
v_fma_f32 v99,v99,v81,v81
v_fma_f32 v100,v100,v81,v81
v_fma_f32 v101,v101,v81,v81
v_rcp_f32 v98,v98
v_rcp_f32 v99,v99
v_rcp_f32 v100,v100
v_rcp_f32 v101,v101
v_mul_f32 v34,v34,v98
v_mul_f32 v35,v35,v99
v_mul_f32 v36,v36,v100
v_mul_f32 v37,v37,v101
v_cvt_pk_bf16_f32 v108,v34,v35
v_cvt_pk_bf16_f32 v109,v36,v37
buffer_store_dwordx4 v[106:109],v0,s[36:39],s67 offen offset:0 sc0 sc1
v_fma_f32 v38,-v78,v90,v38
v_fma_f32 v39,-v78,v91,v39
v_fma_f32 v40,-v78,v92,v40
v_fma_f32 v41,-v78,v93,v41
v_mul_f32 v102,v38,v38
v_mul_f32 v103,v39,v39
v_mul_f32 v104,v40,v40
v_mul_f32 v105,v41,v41
v_fma_f32 v102,v80,v102,v79
v_fma_f32 v103,v80,v103,v79
v_fma_f32 v104,v80,v104,v79
v_fma_f32 v105,v80,v105,v79
v_mul_f32 v102,v38,v102
v_mul_f32 v103,v39,v103
v_mul_f32 v104,v40,v104
v_mul_f32 v105,v41,v105
v_exp_f32 v102,v102
v_exp_f32 v103,v103
v_exp_f32 v104,v104
v_exp_f32 v105,v105
v_fma_f32 v102,v102,v81,v81
v_fma_f32 v103,v103,v81,v81
v_fma_f32 v104,v104,v81,v81
v_fma_f32 v105,v105,v81,v81
v_rcp_f32 v102,v102
v_rcp_f32 v103,v103
v_rcp_f32 v104,v104
v_rcp_f32 v105,v105
v_mul_f32 v38,v38,v102
v_mul_f32 v39,v39,v103
v_mul_f32 v40,v40,v104
v_mul_f32 v41,v41,v105
v_cvt_pk_bf16_f32 v110,v38,v39
v_cvt_pk_bf16_f32 v111,v40,v41
s_nop 0
v_permlane16_swap_b32 v110,v112
v_permlane16_swap_b32 v111,v113
buffer_store_dwordx4 v[110:113],v10,s[36:39],s67 offen offset:0 sc0 sc1
s_mov_b32 s72,s69
s_mov_b32 s69,0x7fff0000
s_mov_b32 s67,0x7fff0000
s_mov_b32 s77,0x7fff0000
s_branch Lg3_poll_%=
Lg3_fin1_%=:
L 0
v_fma_f32 v30,-v78,v82,v30
v_fma_f32 v31,-v78,v83,v31
v_fma_f32 v32,-v78,v84,v32
v_fma_f32 v33,-v78,v85,v33
v_mul_f32 v94,v30,v30
v_mul_f32 v95,v31,v31
v_mul_f32 v96,v32,v32
v_mul_f32 v97,v33,v33
v_fma_f32 v94,v80,v94,v79
v_fma_f32 v95,v80,v95,v79
v_fma_f32 v96,v80,v96,v79
v_fma_f32 v97,v80,v97,v79
v_mul_f32 v94,v30,v94
v_mul_f32 v95,v31,v95
v_mul_f32 v96,v32,v96
v_mul_f32 v97,v33,v97
v_exp_f32 v94,v94
v_exp_f32 v95,v95
v_exp_f32 v96,v96
v_exp_f32 v97,v97
v_fma_f32 v94,v94,v81,v81
v_fma_f32 v95,v95,v81,v81
v_fma_f32 v96,v96,v81,v81
v_fma_f32 v97,v97,v81,v81
v_rcp_f32 v94,v94
v_rcp_f32 v95,v95
v_rcp_f32 v96,v96
v_rcp_f32 v97,v97
v_mul_f32 v30,v30,v94
v_mul_f32 v31,v31,v95
v_mul_f32 v32,v32,v96
v_mul_f32 v33,v33,v97
v_cvt_pk_bf16_f32 v106,v30,v31
v_cvt_pk_bf16_f32 v107,v32,v33
v_fma_f32 v34,-v78,v86,v34
v_fma_f32 v35,-v78,v87,v35
v_fma_f32 v36,-v78,v88,v36
v_fma_f32 v37,-v78,v89,v37
v_mul_f32 v98,v34,v34
v_mul_f32 v99,v35,v35
v_mul_f32 v100,v36,v36
v_mul_f32 v101,v37,v37
v_fma_f32 v98,v80,v98,v79
v_fma_f32 v99,v80,v99,v79
v_fma_f32 v100,v80,v100,v79
v_fma_f32 v101,v80,v101,v79
v_mul_f32 v98,v34,v98
v_mul_f32 v99,v35,v99
v_mul_f32 v100,v36,v100
v_mul_f32 v101,v37,v101
v_exp_f32 v98,v98
v_exp_f32 v99,v99
v_exp_f32 v100,v100
v_exp_f32 v101,v101
v_fma_f32 v98,v98,v81,v81
v_fma_f32 v99,v99,v81,v81
v_fma_f32 v100,v100,v81,v81
v_fma_f32 v101,v101,v81,v81
v_rcp_f32 v98,v98
v_rcp_f32 v99,v99
v_rcp_f32 v100,v100
v_rcp_f32 v101,v101
v_mul_f32 v34,v34,v98
v_mul_f32 v35,v35,v99
v_mul_f32 v36,v36,v100
v_mul_f32 v37,v37,v101
v_cvt_pk_bf16_f32 v108,v34,v35
v_cvt_pk_bf16_f32 v109,v36,v37
buffer_store_dwordx4 v[106:109],v0,s[36:39],s67 offen offset:0 sc0 sc1
v_fma_f32 v38,-v78,v90,v38
v_fma_f32 v39,-v78,v91,v39
v_fma_f32 v40,-v78,v92,v40
v_fma_f32 v41,-v78,v93,v41
v_mul_f32 v102,v38,v38
v_mul_f32 v103,v39,v39
v_mul_f32 v104,v40,v40
v_mul_f32 v105,v41,v41
v_fma_f32 v102,v80,v102,v79
v_fma_f32 v103,v80,v103,v79
v_fma_f32 v104,v80,v104,v79
v_fma_f32 v105,v80,v105,v79
v_mul_f32 v102,v38,v102
v_mul_f32 v103,v39,v103
v_mul_f32 v104,v40,v104
v_mul_f32 v105,v41,v105
v_exp_f32 v102,v102
v_exp_f32 v103,v103
v_exp_f32 v104,v104
v_exp_f32 v105,v105
v_fma_f32 v102,v102,v81,v81
v_fma_f32 v103,v103,v81,v81
v_fma_f32 v104,v104,v81,v81
v_fma_f32 v105,v105,v81,v81
v_rcp_f32 v102,v102
v_rcp_f32 v103,v103
v_rcp_f32 v104,v104
v_rcp_f32 v105,v105
v_mul_f32 v38,v38,v102
v_mul_f32 v39,v39,v103
v_mul_f32 v40,v40,v104
v_mul_f32 v41,v41,v105
v_cvt_pk_bf16_f32 v110,v38,v39
v_cvt_pk_bf16_f32 v111,v40,v41
s_nop 0
v_permlane16_swap_b32 v110,v112
v_permlane16_swap_b32 v111,v113
buffer_store_dwordx4 v[110:113],v10,s[36:39],s67 offen offset:0 sc0 sc1
s_mov_b32 s72,s69
s_mov_b32 s69,0x7fff0000
s_mov_b32 s67,0x7fff0000
s_mov_b32 s77,0x7fff0000
s_branch Lg3_done_%=
Lg3_done_%=:
s_waitcnt vmcnt(2)
s_barrier
buffer_store_dword v4,v3,s[36:39],s70 offen offset:0 sc0 sc1
s_waitcnt vmcnt(1)
s_barrier
buffer_store_dword v4,v3,s[36:39],s72 offen offset:0 sc0 sc1
s_waitcnt vmcnt(0) lgkmcnt(0)
s_barrier
s_endpgm
Lcode_g3_e:
.purgem M
.purgem R
.purgem T
.purgem D
.purgem L
)"
  :: "s"(ws), "s"(sWave), "s"(sG0), "s"(sStep), "s"(sOutOff), "s"(sFoutOff), "s"(sAuxOff), "s"(sWw), "s"(sBuf), "s"(sRk), "v"(vL16), "v"(vFlag), "v"(vSnapOff), "v"(vSnapRd), "v"(vWd0), "v"(vWd1), "v"(vWr0), "v"(vWr1), "v"(vWr2), "v"(vOutS), "v"(vAux0), "v"(vAux1), "v"(vAux2), "v"(vAux3), "v"(vAn), "v"(vAe), "s"(w3) : "v0", "v1", "v2", "v3", "v4", "v5", "v6", "v7", "v8", "v9", "v10", "v11", "v12", "v13", "v14", "v15", "v16", "v17", "v18", "v19", "v20", "v21", "v22", "v23", "v24", "v25", "v26", "v27", "v28", "v29", "v30", "v31", "v32", "v33", "v34", "v35", "v36", "v37", "v38", "v39", "v40", "v41", "v42", "v43", "v44", "v45", "v46", "v47", "v48", "v49", "v50", "v51", "v52", "v53", "v54", "v55", "v56", "v57", "v58", "v59", "v60", "v61", "v62", "v63", "v64", "v65", "v66", "v67", "v68", "v69", "v70", "v71", "v72", "v73", "v74", "v75", "v76", "v77", "v78", "v79", "v80", "v81", "v82", "v83", "v84", "v85", "v86", "v87", "v88", "v89", "v90", "v91", "v92", "v93", "v94", "v95", "v96", "v97", "v98", "v99", "v100", "v101", "v102", "v103", "v104", "v105", "v106", "v107", "v108", "v109", "v110", "v111", "v112", "v113", "v114", "v115", "v116", "v117", "v118", "v119", "v120", "v121", "v122", "v123", "v124", "v125", "v126", "v127", "v128", "v129", "v130", "v131", "v132", "v133", "v134", "v135", "v136", "v137", "v138", "v139", "v140", "v141", "v142", "v143", "v144", "v145", "v146", "v147", "v148", "v149", "v150", "v151", "v152", "v153", "v154", "v155", "v156", "v157", "v158", "v159", "v160", "v161", "v162", "v163", "v164", "v165", "v166", "v167", "v168", "v169", "v170", "v171", "v172", "v173", "v174", "v175", "v176", "v177", "v178", "v179", "v180", "v181", "v182", "v183", "v184", "v185", "v186", "v187", "v188", "v189", "v190", "v191", "v192", "v193", "v194", "v195", "v196", "v197", "v198", "v199", "v200", "v201", "v202", "v203", "v204", "v205", "v206", "v207", "v208", "v209", "v210", "v211", "v212", "v213", "v214", "v215", "v216", "v217", "v218", "v219", "v220", "v221", "v222", "v223", "v224", "v225", "v226", "v227", "v228", "v229", "v230", "v231", "v232", "v233", "v234", "v235", "v236", "v237", "v238", "v239", "a0", "a1", "a2", "a3", "a4", "a5", "a6", "a7", "a8", "a9", "a10", "a11", "a12", "a13", "a14", "a15", "a16", "a17", "a18", "a19", "a20", "a21", "a22", "a23", "a24", "a25", "a26", "a27", "a28", "a29", "a30", "a31", "a32", "a33", "a34", "a35", "a36", "a37", "a38", "a39", "a40", "a41", "a42", "a43", "a44", "a45", "a46", "a47", "a48", "a49", "a50", "a51", "a52", "a53", "a54", "a55", "a56", "a57", "a58", "a59", "a60", "a61", "a62", "a63", "a64", "a65", "a66", "a67", "a68", "a69", "a70", "a71", "a72", "a73", "a74", "a75", "a76", "a77", "a78", "a79", "a80", "a81", "a82", "a83", "a84", "a85", "a86", "a87", "a88", "a89", "a90", "a91", "a92", "a93", "a94", "a95", "a96", "a97", "a98", "a99", "a100", "a101", "a102", "a103", "a104", "a105", "a106", "a107", "a108", "a109", "a110", "a111", "a112", "a113", "a114", "a115", "a116", "a117", "a118", "a119", "a120", "a121", "a122", "a123", "a124", "a125", "a126", "a127", "a128", "a129", "a130", "a131", "a132", "a133", "a134", "a135", "a136", "a137", "a138", "a139", "a140", "a141", "a142", "a143", "a144", "a145", "a146", "a147", "a148", "a149", "a150", "a151", "a152", "a153", "a154", "a155", "a156", "a157", "a158", "a159", "a160", "a161", "a162", "a163", "a164", "a165", "a166", "a167", "a168", "a169", "a170", "a171", "a172", "a173", "a174", "a175", "a176", "a177", "a178", "a179", "a180", "a181", "a182", "a183", "a184", "a185", "a186", "a187", "a188", "a189", "a190", "a191", "a192", "a193", "a194", "a195", "a196", "a197", "a198", "a199", "a200", "a201", "a202", "a203", "a204", "a205", "a206", "a207", "a208", "a209", "a210", "a211", "a212", "a213", "a214", "a215", "a216", "a217", "a218", "a219", "a220", "a221", "a222", "a223", "a224", "a225", "a226", "a227", "a228", "a229", "a230", "a231", "a232", "a233", "a234", "a235", "a236", "a237", "a238", "a239", "a240", "a241", "a242", "a243", "a244", "a245", "a246", "a247", "a248", "a249", "a250", "a251", "a252", "a253", "a254", "a255", "s36", "s37", "s38", "s39", "s40", "s41", "s42", "s43", "s44", "s45", "s46", "s47", "s48", "s49", "s50", "s51", "s52", "s53", "s54", "s55", "s56", "s57", "s58", "s59", "s60", "s61", "s62", "s63", "s64", "s65", "s66", "s67", "s68", "s69", "s70", "s71", "s72", "s73", "s74", "s75", "s76", "s77", "s78", "s79", "s80", "s81", "s82", "s83", "s84", "s85", "s86", "s87", "s88", "s89", "s90", "s91", "s92", "s93", "s94", "s95", "s96", "s97", "vcc", "scc", "memory"); } else { asm volatile(
R"(.macro M d,w,x
v_mfma_f32_16x16x32_bf16 v[\d:\d+3], \w, v[\x:\x+3], v[\d:\d+3]
.endm
.macro R d,a,o
ds_read_b128 v[\d:\d+3], v\a offset:\o
.endm
.macro T d,a,o
ds_read_b64_tr_b16 \d, v\a offset:\o
.endm
.macro D a,s,o
buffer_load_dwordx4 v\a, s[36:39], s\s offen offset:\o sc1 lds
.endm
.macro L c
s_waitcnt lgkmcnt(\c)
.endm
Lcode_g3o_b:
s_mov_b64 s[36:37],%0
s_mov_b32 s38,111243392
s_mov_b32 s39,0x00020000
s_mov_b32 s44,%1
s_mov_b32 s45,%2
s_mov_b32 s46,%3
s_mov_b32 s47,%4
s_mov_b32 s48,%5
s_mov_b32 s49,%6
s_mov_b32 s50,%7
s_mov_b32 s51,%8
s_mov_b32 s93,%9
v_mov_b32 v0,%10
v_mov_b32 v3,%11
v_mov_b32 v5,%12
v_mov_b32 v6,%13
v_mov_b32 v18,%14
v_mov_b32 v19,%15
v_mov_b32 v20,%16
v_mov_b32 v21,%17
v_mov_b32 v22,%18
v_mov_b32 v10,%19
v_mov_b32 v11,%20
v_mov_b32 v12,%21
v_mov_b32 v13,%22
v_mov_b32 v14,%23
v_mov_b32 v15,%24
v_mov_b32 v16,%25
s_mov_b64 s[40:41],%26
s_mov_b32 s42,7340032
s_mov_b32 s43,0x00020000
s_lshl_b32 s57,s44,11
s_mov_b32 s79,111243264
buffer_load_dword v4,off,s[36:39],s79 sc1
s_add_u32 s55,s50,0
s_add_u32 m0,s57,0
s_nop 0
buffer_load_dwordx4 v18,s[40:43],s55 offen offset:0  lds
s_add_u32 m0,s57,1024
s_nop 0
buffer_load_dwordx4 v19,s[40:43],s55 offen offset:0  lds
s_add_u32 m0,s57,8064
s_nop 0
buffer_load_dwordx4 v18,s[40:43],s55 offen offset:128  lds
s_add_u32 m0,s57,9088
s_nop 0
buffer_load_dwordx4 v19,s[40:43],s55 offen offset:128  lds
s_add_u32 s55,s50,458752
s_add_u32 m0,s57,16384
s_nop 0
buffer_load_dwordx4 v18,s[40:43],s55 offen offset:0  lds
s_add_u32 m0,s57,17408
s_nop 0
buffer_load_dwordx4 v19,s[40:43],s55 offen offset:0  lds
s_add_u32 m0,s57,24448
s_nop 0
buffer_load_dwordx4 v18,s[40:43],s55 offen offset:128  lds
s_add_u32 m0,s57,25472
s_nop 0
buffer_load_dwordx4 v19,s[40:43],s55 offen offset:128  lds
s_add_u32 s55,s50,917504
s_add_u32 m0,s57,32768
s_nop 0
buffer_load_dwordx4 v18,s[40:43],s55 offen offset:0  lds
s_add_u32 m0,s57,33792
s_nop 0
buffer_load_dwordx4 v19,s[40:43],s55 offen offset:0  lds
s_add_u32 m0,s57,40832
s_nop 0
buffer_load_dwordx4 v18,s[40:43],s55 offen offset:128  lds
s_add_u32 m0,s57,41856
s_nop 0
buffer_load_dwordx4 v19,s[40:43],s55 offen offset:128  lds
s_add_u32 s55,s50,1376256
s_add_u32 m0,s57,49152
s_nop 0
buffer_load_dwordx4 v18,s[40:43],s55 offen offset:0  lds
s_add_u32 m0,s57,50176
s_nop 0
buffer_load_dwordx4 v19,s[40:43],s55 offen offset:0  lds
s_add_u32 m0,s57,57216
s_nop 0
buffer_load_dwordx4 v18,s[40:43],s55 offen offset:128  lds
s_add_u32 m0,s57,58240
s_nop 0
buffer_load_dwordx4 v19,s[40:43],s55 offen offset:128  lds
s_add_u32 s55,s50,1835008
s_add_u32 m0,s57,65536
s_nop 0
buffer_load_dwordx4 v18,s[40:43],s55 offen offset:0  lds
s_add_u32 m0,s57,66560
s_nop 0
buffer_load_dwordx4 v19,s[40:43],s55 offen offset:0  lds
s_add_u32 m0,s57,73600
s_nop 0
buffer_load_dwordx4 v18,s[40:43],s55 offen offset:128  lds
s_add_u32 m0,s57,74624
s_nop 0
buffer_load_dwordx4 v19,s[40:43],s55 offen offset:128  lds
s_add_u32 s55,s50,2293760
s_add_u32 m0,s57,81920
s_nop 0
buffer_load_dwordx4 v18,s[40:43],s55 offen offset:0  lds
s_add_u32 m0,s57,82944
s_nop 0
buffer_load_dwordx4 v19,s[40:43],s55 offen offset:0  lds
s_add_u32 m0,s57,89984
s_nop 0
buffer_load_dwordx4 v18,s[40:43],s55 offen offset:128  lds
s_add_u32 m0,s57,91008
s_nop 0
buffer_load_dwordx4 v19,s[40:43],s55 offen offset:128  lds
s_waitcnt vmcnt(24)
s_barrier
v_readfirstlane_b32 s53,v4
s_nop 3
s_add_u32 s53,s53,1
s_cmp_eq_u32 s53,0
s_cselect_b32 s53,1,s53
v_mov_b32 v4,s53
s_waitcnt vmcnt(20)
L 0
s_barrier
s_add_u32 s55,s50,2752512
s_add_u32 m0,s57,98304
s_nop 0
buffer_load_dwordx4 v18,s[40:43],s55 offen offset:0  lds
s_add_u32 m0,s57,99328
s_nop 0
buffer_load_dwordx4 v19,s[40:43],s55 offen offset:0  lds
s_add_u32 m0,s57,106368
s_nop 0
buffer_load_dwordx4 v18,s[40:43],s55 offen offset:128  lds
s_add_u32 m0,s57,107392
s_nop 0
buffer_load_dwordx4 v19,s[40:43],s55 offen offset:128  lds
v_add_u32 v26,0,v20
v_add_u32 v27,0,v21
T a[0:1],26,0
T a[2:3],26,512
T a[4:5],27,0
T a[6:7],27,512
T a[8:9],26,4096
T a[10:11],26,4608
T a[12:13],27,4096
T a[14:15],27,4608
s_waitcnt vmcnt(20)
L 0
s_barrier
s_add_u32 s55,s50,3211264
s_add_u32 m0,s57,0
s_nop 0
buffer_load_dwordx4 v18,s[40:43],s55 offen offset:0  lds
s_add_u32 m0,s57,1024
s_nop 0
buffer_load_dwordx4 v19,s[40:43],s55 offen offset:0  lds
s_add_u32 m0,s57,8064
s_nop 0
buffer_load_dwordx4 v18,s[40:43],s55 offen offset:128  lds
s_add_u32 m0,s57,9088
s_nop 0
buffer_load_dwordx4 v19,s[40:43],s55 offen offset:128  lds
v_add_u32 v26,16384,v20
v_add_u32 v27,16384,v21
T a[16:17],26,0
T a[18:19],26,512
T a[20:21],27,0
T a[22:23],27,512
T a[24:25],26,4096
T a[26:27],26,4608
T a[28:29],27,4096
T a[30:31],27,4608
s_waitcnt vmcnt(20)
L 0
s_barrier
s_add_u32 s55,s50,3670016
s_add_u32 m0,s57,16384
s_nop 0
buffer_load_dwordx4 v18,s[40:43],s55 offen offset:0  lds
s_add_u32 m0,s57,17408
s_nop 0
buffer_load_dwordx4 v19,s[40:43],s55 offen offset:0  lds
s_add_u32 m0,s57,24448
s_nop 0
buffer_load_dwordx4 v18,s[40:43],s55 offen offset:128  lds
s_add_u32 m0,s57,25472
s_nop 0
buffer_load_dwordx4 v19,s[40:43],s55 offen offset:128  lds
v_add_u32 v26,32768,v20
v_add_u32 v27,32768,v21
T a[32:33],26,0
T a[34:35],26,512
T a[36:37],27,0
T a[38:39],27,512
T a[40:41],26,4096
T a[42:43],26,4608
T a[44:45],27,4096
T a[46:47],27,4608
s_waitcnt vmcnt(20)
L 0
s_barrier
s_add_u32 s55,s50,4128768
s_add_u32 m0,s57,32768
s_nop 0
buffer_load_dwordx4 v18,s[40:43],s55 offen offset:0  lds
s_add_u32 m0,s57,33792
s_nop 0
buffer_load_dwordx4 v19,s[40:43],s55 offen offset:0  lds
s_add_u32 m0,s57,40832
s_nop 0
buffer_load_dwordx4 v18,s[40:43],s55 offen offset:128  lds
s_add_u32 m0,s57,41856
s_nop 0
buffer_load_dwordx4 v19,s[40:43],s55 offen offset:128  lds
v_add_u32 v26,49152,v20
v_add_u32 v27,49152,v21
T a[48:49],26,0
T a[50:51],26,512
T a[52:53],27,0
T a[54:55],27,512
T a[56:57],26,4096
T a[58:59],26,4608
T a[60:61],27,4096
T a[62:63],27,4608
s_waitcnt vmcnt(20)
L 0
s_barrier
s_add_u32 s55,s50,4587520
s_add_u32 m0,s57,49152
s_nop 0
buffer_load_dwordx4 v18,s[40:43],s55 offen offset:0  lds
s_add_u32 m0,s57,50176
s_nop 0
buffer_load_dwordx4 v19,s[40:43],s55 offen offset:0  lds
s_add_u32 m0,s57,57216
s_nop 0
buffer_load_dwordx4 v18,s[40:43],s55 offen offset:128  lds
s_add_u32 m0,s57,58240
s_nop 0
buffer_load_dwordx4 v19,s[40:43],s55 offen offset:128  lds
v_add_u32 v26,65536,v20
v_add_u32 v27,65536,v21
T a[64:65],26,0
T a[66:67],26,512
T a[68:69],27,0
T a[70:71],27,512
T a[72:73],26,4096
T a[74:75],26,4608
T a[76:77],27,4096
T a[78:79],27,4608
s_waitcnt vmcnt(20)
L 0
s_barrier
s_add_u32 s55,s50,5046272
s_add_u32 m0,s57,65536
s_nop 0
buffer_load_dwordx4 v18,s[40:43],s55 offen offset:0  lds
s_add_u32 m0,s57,66560
s_nop 0
buffer_load_dwordx4 v19,s[40:43],s55 offen offset:0  lds
s_add_u32 m0,s57,73600
s_nop 0
buffer_load_dwordx4 v18,s[40:43],s55 offen offset:128  lds
s_add_u32 m0,s57,74624
s_nop 0
buffer_load_dwordx4 v19,s[40:43],s55 offen offset:128  lds
v_add_u32 v26,81920,v20
v_add_u32 v27,81920,v21
T a[80:81],26,0
T a[82:83],26,512
T a[84:85],27,0
T a[86:87],27,512
T a[88:89],26,4096
T a[90:91],26,4608
T a[92:93],27,4096
T a[94:95],27,4608
s_waitcnt vmcnt(20)
L 0
s_barrier
s_add_u32 s55,s50,5505024
s_add_u32 m0,s57,81920
s_nop 0
buffer_load_dwordx4 v18,s[40:43],s55 offen offset:0  lds
s_add_u32 m0,s57,82944
s_nop 0
buffer_load_dwordx4 v19,s[40:43],s55 offen offset:0  lds
s_add_u32 m0,s57,89984
s_nop 0
buffer_load_dwordx4 v18,s[40:43],s55 offen offset:128  lds
s_add_u32 m0,s57,91008
s_nop 0
buffer_load_dwordx4 v19,s[40:43],s55 offen offset:128  lds
v_add_u32 v26,98304,v20
v_add_u32 v27,98304,v21
T a[96:97],26,0
T a[98:99],26,512
T a[100:101],27,0
T a[102:103],27,512
T a[104:105],26,4096
T a[106:107],26,4608
T a[108:109],27,4096
T a[110:111],27,4608
s_waitcnt vmcnt(20)
L 0
s_barrier
s_add_u32 s55,s50,5963776
s_add_u32 m0,s57,98304
s_nop 0
buffer_load_dwordx4 v18,s[40:43],s55 offen offset:0  lds
s_add_u32 m0,s57,99328
s_nop 0
buffer_load_dwordx4 v19,s[40:43],s55 offen offset:0  lds
s_add_u32 m0,s57,106368
s_nop 0
buffer_load_dwordx4 v18,s[40:43],s55 offen offset:128  lds
s_add_u32 m0,s57,107392
s_nop 0
buffer_load_dwordx4 v19,s[40:43],s55 offen offset:128  lds
v_add_u32 v26,0,v20
v_add_u32 v27,0,v21
T a[112:113],26,0
T a[114:115],26,512
T a[116:117],27,0
T a[118:119],27,512
T a[120:121],26,4096
T a[122:123],26,4608
T a[124:125],27,4096
T a[126:127],27,4608
s_waitcnt vmcnt(20)
L 0
s_barrier
s_add_u32 s55,s50,6422528
s_add_u32 m0,s57,0
s_nop 0
buffer_load_dwordx4 v18,s[40:43],s55 offen offset:0  lds
s_add_u32 m0,s57,1024
s_nop 0
buffer_load_dwordx4 v19,s[40:43],s55 offen offset:0  lds
s_add_u32 m0,s57,8064
s_nop 0
buffer_load_dwordx4 v18,s[40:43],s55 offen offset:128  lds
s_add_u32 m0,s57,9088
s_nop 0
buffer_load_dwordx4 v19,s[40:43],s55 offen offset:128  lds
v_add_u32 v26,16384,v20
v_add_u32 v27,16384,v21
T a[128:129],26,0
T a[130:131],26,512
T a[132:133],27,0
T a[134:135],27,512
T a[136:137],26,4096
T a[138:139],26,4608
T a[140:141],27,4096
T a[142:143],27,4608
s_waitcnt vmcnt(20)
L 0
s_barrier
s_add_u32 s55,s50,6881280
s_add_u32 m0,s57,16384
s_nop 0
buffer_load_dwordx4 v18,s[40:43],s55 offen offset:0  lds
s_add_u32 m0,s57,17408
s_nop 0
buffer_load_dwordx4 v19,s[40:43],s55 offen offset:0  lds
s_add_u32 m0,s57,24448
s_nop 0
buffer_load_dwordx4 v18,s[40:43],s55 offen offset:128  lds
s_add_u32 m0,s57,25472
s_nop 0
buffer_load_dwordx4 v19,s[40:43],s55 offen offset:128  lds
v_add_u32 v26,32768,v20
v_add_u32 v27,32768,v21
T a[144:145],26,0
T a[146:147],26,512
T a[148:149],27,0
T a[150:151],27,512
T a[152:153],26,4096
T a[154:155],26,4608
T a[156:157],27,4096
T a[158:159],27,4608
s_waitcnt vmcnt(20)
L 0
s_barrier
v_add_u32 v26,49152,v20
v_add_u32 v27,49152,v21
T a[160:161],26,0
T a[162:163],26,512
T a[164:165],27,0
T a[166:167],27,512
T a[168:169],26,4096
T a[170:171],26,4608
T a[172:173],27,4096
T a[174:175],27,4608
s_waitcnt vmcnt(16)
L 0
s_barrier
v_add_u32 v26,65536,v20
v_add_u32 v27,65536,v21
T a[176:177],26,0
T a[178:179],26,512
T a[180:181],27,0
T a[182:183],27,512
T a[184:185],26,4096
T a[186:187],26,4608
T a[188:189],27,4096
T a[190:191],27,4608
s_waitcnt vmcnt(12)
L 0
s_barrier
v_add_u32 v26,81920,v20
v_add_u32 v27,81920,v21
T a[192:193],26,0
T a[194:195],26,512
T a[196:197],27,0
T a[198:199],27,512
T a[200:201],26,4096
T a[202:203],26,4608
T a[204:205],27,4096
T a[206:207],27,4608
s_waitcnt vmcnt(8)
L 0
s_barrier
v_add_u32 v26,98304,v20
v_add_u32 v27,98304,v21
T a[208:209],26,0
T a[210:211],26,512
T a[212:213],27,0
T a[214:215],27,512
T a[216:217],26,4096
T a[218:219],26,4608
T a[220:221],27,4096
T a[222:223],27,4608
s_waitcnt vmcnt(4)
L 0
s_barrier
v_add_u32 v26,0,v20
v_add_u32 v27,0,v21
T a[224:225],26,0
T a[226:227],26,512
T a[228:229],27,0
T a[230:231],27,512
T a[232:233],26,4096
T a[234:235],26,4608
T a[236:237],27,4096
T a[238:239],27,4608
s_waitcnt vmcnt(0)
L 0
s_barrier
v_add_u32 v26,16384,v20
v_add_u32 v27,16384,v21
T a[240:241],26,0
T a[242:243],26,512
T a[244:245],27,0
T a[246:247],27,512
T a[248:249],26,4096
T a[250:251],26,4608
T a[252:253],27,4096
T a[254:255],27,4608
L 0
s_barrier
v_mov_b32 v34,0x3f803f80
v_mov_b32 v35,0x3f803f80
v_mov_b32 v36,0x3f803f80
v_mov_b32 v37,0x3f803f80
s_nop 1
v_mfma_f32_16x16x32_bf16 v[106:109],a[0:3],v[34:37],0
v_mfma_f32_16x16x32_bf16 v[110:113],a[4:7],v[34:37],0
M 106,a[8:11],34
M 110,a[12:15],34
M 106,a[16:19],34
M 110,a[20:23],34
M 106,a[24:27],34
M 110,a[28:31],34
M 106,a[32:35],34
M 110,a[36:39],34
M 106,a[40:43],34
M 110,a[44:47],34
M 106,a[48:51],34
M 110,a[52:55],34
M 106,a[56:59],34
M 110,a[60:63],34
M 106,a[64:67],34
M 110,a[68:71],34
M 106,a[72:75],34
)"
R"(M 110,a[76:79],34
M 106,a[80:83],34
M 110,a[84:87],34
M 106,a[88:91],34
M 110,a[92:95],34
M 106,a[96:99],34
M 110,a[100:103],34
M 106,a[104:107],34
M 110,a[108:111],34
M 106,a[112:115],34
M 110,a[116:119],34
M 106,a[120:123],34
M 110,a[124:127],34
M 106,a[128:131],34
M 110,a[132:135],34
M 106,a[136:139],34
M 110,a[140:143],34
M 106,a[144:147],34
M 110,a[148:151],34
M 106,a[152:155],34
M 110,a[156:159],34
M 106,a[160:163],34
M 110,a[164:167],34
M 106,a[168:171],34
M 110,a[172:175],34
M 106,a[176:179],34
M 110,a[180:183],34
M 106,a[184:187],34
M 110,a[188:191],34
M 106,a[192:195],34
M 110,a[196:199],34
M 106,a[200:203],34
M 110,a[204:207],34
M 106,a[208:211],34
M 110,a[212:215],34
M 106,a[216:219],34
M 110,a[220:223],34
M 106,a[224:227],34
M 110,a[228:231],34
M 106,a[232:235],34
M 110,a[236:239],34
M 106,a[240:243],34
M 110,a[244:247],34
M 106,a[248:251],34
M 110,a[252:255],34
v_mov_b32 v4,s53
s_mov_b32 s82,0
s_mov_b32 s80,0
s_mov_b32 s54,s45
s_mul_i32 s57,s44,4096
s_mul_i32 s55,s45,32768
s_add_u32 s55,s55,s57
s_add_u32 s55,s55,31457280
s_mul_i32 s57,s44,4096
s_mul_i32 s60,s46,32768
s_mul_i32 s61,s46,1024
s_mul_i32 s62,s46,114688
s_mul_i32 s63,s46,2560
s_mul_i32 s64,s46,1024
s_lshl_b32 s59,s44,8
s_add_u32 s59,s59,131072
s_mul_i32 s58,s45,1024
s_add_u32 s58,s58,109613056
s_mul_i32 s65,s45,114688
s_add_u32 s65,s65,s47
s_mul_i32 s68,s45,2560
s_add_u32 s68,s68,s48
s_mul_i32 s73,s45,1024
s_add_u32 s73,s73,107479040
s_mul_i32 s74,s45,32768
s_add_u32 s74,s74,s49
s_mul_i32 s75,s45,1024
s_add_u32 s75,s75,107479040
s_mov_b32 s66,0x7fff0000
s_mov_b32 s67,0x7fff0000
s_mov_b32 s69,0x7fff0000
s_mov_b32 s76,0x7fff0000
s_mov_b32 s77,0x7fff0000
s_mov_b32 s70,0x7fff0000
s_mov_b32 s71,0x7fff0000
s_mov_b32 s72,0x7fff0000
v_mov_b32 v1,v0
v_add_u32 v2,16384,v0
s_mov_b32 s84,16384
s_mov_b32 s86,0x3a800000
s_mov_b32 s87,0x3727c5ac
s_mov_b32 s88,0xc0135761
s_mov_b32 s89,0x3d372713
s_and_b32 s85,s44,1
s_mul_i32 s85,s85,127
v_lshlrev_b32 v8,22,v0
v_and_b32 v8,0x80000000,v8
v_or_b32 v8,v8,v5
v_add_u32 v2,32768,v0
s_lshl_b32 s84,s57,1
s_add_u32 s84,s84,32768
s_add_u32 m0,s59,4096
s_nop 0
buffer_load_dword v8,s[36:39],s58 offen offset:0 sc1 lds
buffer_store_dword v4,v3,s[36:39],s71 offen offset:0 sc0 sc1
s_add_u32 m0,s59,5120
s_nop 0
buffer_load_dword v8,s[36:39],s58 offen offset:0 sc1 lds
buffer_store_dword v4,v3,s[36:39],s71 offen offset:0 sc0 sc1
s_mov_b32 s83,0
Lg3o_poll_%=:
s_add_u32 s78,s83,2
s_and_b32 s78,s78,3
s_lshl_b32 s78,s78,10
s_add_u32 s78,s78,4096
s_add_u32 m0,s59,s78
s_nop 0
buffer_load_dword v8,s[36:39],s58 offen offset:0 sc1 lds
s_and_b32 s78,s83,3
s_lshl_b32 s78,s78,10
v_add_u32 v114,s78,v6
s_waitcnt vmcnt(4)
s_barrier
ds_read_b32 v7,v114 offset:4096
buffer_store_dword v4,v3,s[36:39],s70 offen offset:0 sc0 sc1
s_mov_b32 s70,s71
s_mov_b32 s71,s72
s_mov_b32 s72,0x7fff0000
s_add_u32 s83,s83,1
s_add_u32 s82,s82,1
L 0
v_cmp_eq_u32 vcc,s53,v7
s_nop 0
s_cmp_eq_u32 vcc_lo,-1
s_cbranch_scc1 Lg3o_go_%=
s_cmp_lt_u32 s82,20000
s_cbranch_scc1 Lg3o_poll_%=
Lg3o_go_%=:
buffer_load_dword v130,v11,s[36:39],s73 offen offset:0 sc1
s_add_u32 s73,s73,s64
s_add_u32 m0,s57,0
s_add_u32 s56,s55,0
D 0,56,0
D 0,56,1024
D 0,56,2048
D 0,56,3072
s_add_u32 s58,s58,s61
s_add_u32 m0,s59,2048
s_nop 0
buffer_load_dword v8,s[36:39],s58 offen offset:0 sc1 lds
s_add_u32 m0,s57,16384
s_add_u32 s56,s55,16384
D 0,56,0
D 0,56,1024
D 0,56,2048
D 0,56,3072
s_add_u32 s55,s55,s60
s_sub_u32 s57,s84,s57
s_add_u32 s54,s54,s46
s_waitcnt vmcnt(4)
s_barrier
buffer_store_dword v4,v3,s[36:39],s70 offen offset:0 sc0 sc1
s_min_u32 s71,s71,s72
buffer_store_dword v4,v3,s[36:39],s71 offen offset:0 sc0 sc1
s_mov_b32 s70,0x7fff0000
s_mov_b32 s71,0x7fff0000
s_mov_b32 s72,0x7fff0000
s_cmp_eq_u32 s80,1
s_cbranch_scc1 Lg3o_st1_%=
s_branch Lg3o_st0_%=
Lg3o_gox_%=:
s_add_u32 s58,s58,s61
s_add_u32 m0,s59,2048
s_nop 0
buffer_load_dword v8,s[36:39],s58 offen offset:0 sc1 lds
s_waitcnt vmcnt(0)
s_barrier
buffer_store_dword v4,v3,s[36:39],s70 offen offset:0 sc0 sc1
s_min_u32 s71,s71,s72
buffer_store_dword v4,v3,s[36:39],s71 offen offset:0 sc0 sc1
s_mov_b32 s70,0x7fff0000
s_mov_b32 s71,0x7fff0000
s_mov_b32 s72,0x7fff0000
s_cmp_eq_u32 s80,1
s_cbranch_scc1 Lg3o_st1_%=
Lg3o_st0_%=:
ds_read_b32 v7,v6 offset:2048
R 34,1,0
R 38,1,1024
R 42,1,2048
R 46,1,3072
R 50,1,4096
R 54,1,5120
R 58,1,6144
L 7
v_cmp_eq_u32 vcc,s53,v7
L 5
s_cmp_eq_u32 vcc_lo,-1
s_cbranch_scc1 Lg3o_F0_%=
s_branch Lg3o_L0_%=
Lg3o_st1_%=:
ds_read_b32 v7,v6 offset:2048
R 34,2,0
R 38,2,1024
R 42,2,2048
R 46,2,3072
R 50,2,4096
R 54,2,5120
R 58,2,6144
L 7
v_cmp_eq_u32 vcc,s53,v7
L 5
s_cmp_eq_u32 vcc_lo,-1
s_cbranch_scc1 Lg3o_F1_%=
s_branch Lg3o_L1_%=
Lg3o_F0_%=:
v_mfma_f32_16x16x32_bf16 v[22:25],a[4:7],v[34:37],0
s_add_u32 m0,s57,0
R 62,1,7168
v_add_f32_dpp v131,v130,v130 quad_perm:[1,0,3,2] row_mask:0xf bank_mask:0xf
M 22,a[12:15],38
s_add_u32 s56,s55,0
buffer_load_dword v130,v11,s[36:39],s73 offen offset:0 sc1
v_mfma_f32_16x16x32_bf16 v[18:21],a[0:3],v[34:37],0
s_add_u32 s58,s58,s61
R 66,1,8192
v_add_f32_dpp v131,v131,v131 quad_perm:[2,3,0,1] row_mask:0xf bank_mask:0xf
M 18,a[8:11],38
L 5
D 0,56,0
M 18,a[16:19],42
s_mov_b32 s66,s65
R 70,1,9216
v_add_f32_dpp v134,v131,v131 row_half_mirror row_mask:0xf bank_mask:0xf
M 18,a[24:27],46
s_add_u32 s65,s65,s62
v_fma_f32 v26,-v102,v106,v26
v_fma_f32 v27,-v102,v107,v27
M 22,a[20:23],42
s_add_u32 s54,s54,s46
R 74,1,10240
v_mov_b32_dpp v131,v134 row_ror:8 row_mask:0xf bank_mask:0xf
M 22,a[28:31],46
L 5
v_fma_f32 v131,-v134,v134,v131
v_max_f32 v131,s87,v131
M 22,a[36:39],50
s_add_u32 s73,s73,s64
R 78,1,11264
v_fma_f32 v28,-v102,v108,v28
M 22,a[44:47],54
D 0,56,1024
M 18,a[32:35],50
R 82,1,12288
v_fma_f32 v29,-v102,v109,v29
M 18,a[40:43],54
L 5
v_rsq_f32 v132,v131
M 18,a[48:51],58
R 86,1,13312
v_mul_f32 v137,v131,v132
M 18,a[56:59],62
v_mul_f32 v135,s88,v132
v_mul_f32 v131,s89,v132
M 22,a[52:55],58
R 90,1,14336
v_mul_f32 v136,v135,v132
M 22,a[60:63],62
L 5
v_mul_f32 v136,v136,v131
v_mul_f32 v114,v26,v26
M 22,a[68:71],66
s_waitcnt vmcnt(4)
R 94,1,15360
v_mul_f32 v115,v27,v27
M 22,a[76:79],70
s_barrier
D 0,56,2048
M 18,a[64:67],66
R 34,1,16384
v_mul_f32 v116,v28,v28
M 18,a[72:75],70
L 5
ds_write_b128 v12,v[134:137] offset:0
M 18,a[80:83],74
R 38,1,17408
v_mul_f32 v117,v29,v29
M 18,a[88:91],78
v_fma_f32 v114,v104,v114,v103
v_fma_f32 v115,v104,v115,v103
M 22,a[84:87],74
R 42,1,18432
v_fma_f32 v116,v104,v116,v103
M 22,a[92:95],78
L 6
D 0,56,3072
M 22,a[100:103],82
s_add_u32 m0,s59,0
R 46,1,19456
v_fma_f32 v117,v104,v117,v103
M 22,a[108:111],86
s_add_u32 s56,s55,16384
v_mul_f32 v114,v26,v114
v_mul_f32 v115,v27,v115
M 18,a[96:99],82
s_add_u32 s55,s55,s60
R 50,1,20480
v_mul_f32 v116,v28,v116
M 18,a[104:107],86
L 6
v_exp_f32 v114,v114
M 18,a[112:115],90
R 54,1,21504
v_mul_f32 v117,v29,v117
M 18,a[120:123],94
v_exp_f32 v115,v115
M 22,a[116:119],90
R 58,1,22528
v_fma_f32 v114,v114,v105,v105
M 22,a[124:127],94
L 5
buffer_load_dword v8,s[36:39],s58 offen offset:0 sc1 lds
M 22,a[132:135],34
s_add_u32 m0,s57,16384
R 62,1,23552
v_fma_f32 v115,v115,v105,v105
M 22,a[140:143],38
s_sub_u32 s57,s84,s57
D 0,56,0
M 18,a[128:131],34
R 66,1,24576
v_fma_f32 v30,-v102,v110,v30
M 18,a[136:139],38
L 5
v_exp_f32 v116,v116
M 18,a[144:147],42
R 70,1,25600
v_fma_f32 v116,v116,v105,v105
M 18,a[152:155],46
v_exp_f32 v117,v117
M 22,a[148:151],42
R 74,1,26624
v_fma_f32 v117,v117,v105,v105
M 22,a[156:159],46
L 5
v_rcp_f32 v114,v114
M 22,a[164:167],50
R 78,1,27648
v_mul_f32 v26,v26,v114
M 22,a[172:175],54
D 0,56,1024
M 18,a[160:163],50
R 82,1,28672
v_fma_f32 v31,-v102,v111,v31
M 18,a[168:171],54
L 5
v_rcp_f32 v115,v115
M 18,a[176:179],58
R 86,1,29696
v_mul_f32 v27,v27,v115
M 18,a[184:187],62
v_rcp_f32 v116,v116
M 22,a[180:183],58
R 90,1,30720
v_mul_f32 v28,v28,v116
M 22,a[188:191],62
L 5
s_nop 0
D 0,56,2048
M 22,a[196:199],66
s_waitcnt vmcnt(3)
R 94,1,31744
v_cvt_pk_bf16_f32 v122,v26,v27
M 22,a[204:207],70
s_barrier
buffer_store_dword v4,v3,s[36:39],s70 offen offset:0 sc0 sc1
M 18,a[192:195],66
s_mov_b32 s70,s69
R 34,2,0
ds_read_b32 v7,v6 offset:0
M 18,a[200:203],70
L 6
R 98,13,0
v_fma_f32 v32,-v102,v112,v32
M 18,a[208:211],74
s_mov_b32 s69,s68
R 38,2,1024
v_fma_f32 v33,-v102,v113,v33
M 18,a[216:219],78
s_add_u32 s68,s68,s63
v_mul_f32 v118,v30,v30
v_mul_f32 v119,v31,v31
M 22,a[212:215],74
R 42,2,2048
v_mul_f32 v120,v32,v32
M 22,a[220:223],78
L 7
D 0,56,3072
M 22,a[228:231],82
L 3
R 46,2,3072
v_mul_f32 v121,v33,v33
M 22,a[236:239],86
v_fma_f32 v118,v104,v118,v103
v_fma_f32 v119,v104,v119,v103
M 18,a[224:227],82
R 50,2,4096
v_fma_f32 v120,v104,v120,v103
M 18,a[232:235],86
s_nop 0
v_fma_f32 v121,v104,v121,v103
v_mul_f32 v118,v30,v118
v_mul_f32 v119,v31,v119
v_mul_f32 v120,v32,v120
v_mul_f32 v121,v33,v121
v_exp_f32 v118,v118
v_exp_f32 v119,v119
v_exp_f32 v120,v120
v_exp_f32 v121,v121
M 18,a[240:243],90
R 54,2,5120
v_rcp_f32 v117,v117
v_fma_f32 v118,v118,v105,v105
v_fma_f32 v119,v119,v105,v105
v_fma_f32 v120,v120,v105,v105
v_fma_f32 v121,v121,v105,v105
v_rcp_f32 v118,v118
v_rcp_f32 v119,v119
v_rcp_f32 v120,v120
v_rcp_f32 v121,v121
M 18,a[248:251],94
v_mul_f32 v29,v29,v117
v_cvt_pk_bf16_f32 v123,v28,v29
v_mul_f32 v30,v30,v118
v_mul_f32 v31,v31,v119
v_mul_f32 v32,v32,v120
v_mul_f32 v33,v33,v121
v_cvt_pk_bf16_f32 v124,v30,v31
v_cvt_pk_bf16_f32 v125,v32,v33
v_cmp_eq_u32 vcc,s53,v7
M 22,a[244:247],90
s_cmp_eq_u32 vcc_lo,-1
R 58,2,6144
buffer_store_dwordx4 v[122:125],v0,s[36:39],s67 offen offset:0 sc0 sc1
M 22,a[252:255],94
L 5
s_cbranch_scc0 Lg3o_L1_%=
Lg3o_F1_%=:
v_mfma_f32_16x16x32_bf16 v[30:33],a[4:7],v[34:37],0
s_add_u32 m0,s57,0
R 62,2,7168
v_add_f32_dpp v131,v130,v130 quad_perm:[1,0,3,2] row_mask:0xf bank_mask:0xf
M 30,a[12:15],38
s_add_u32 s56,s55,0
buffer_load_dword v130,v11,s[36:39],s73 offen offset:0 sc1
v_mfma_f32_16x16x32_bf16 v[26:29],a[0:3],v[34:37],0
s_add_u32 s58,s58,s61
R 66,2,8192
v_add_f32_dpp v131,v131,v131 quad_perm:[2,3,0,1] row_mask:0xf bank_mask:0xf
M 26,a[8:11],38
L 5
D 0,56,0
M 26,a[16:19],42
s_mov_b32 s67,s65
R 70,2,9216
v_add_f32_dpp v134,v131,v131 row_half_mirror row_mask:0xf bank_mask:0xf
M 26,a[24:27],46
s_add_u32 s65,s65,s62
v_fma_f32 v18,-v98,v106,v18
v_fma_f32 v19,-v98,v107,v19
M 30,a[20:23],42
s_add_u32 s54,s54,s46
R 74,2,10240
v_mov_b32_dpp v131,v134 row_ror:8 row_mask:0xf bank_mask:0xf
M 30,a[28:31],46
L 5
v_fma_f32 v131,-v134,v134,v131
v_max_f32 v131,s87,v131
M 30,a[36:39],50
s_add_u32 s73,s73,s64
R 78,2,11264
v_fma_f32 v20,-v98,v108,v20
M 30,a[44:47],54
D 0,56,1024
M 26,a[32:35],50
R 82,2,12288
v_fma_f32 v21,-v98,v109,v21
M 26,a[40:43],54
L 5
v_rsq_f32 v132,v131
M 26,a[48:51],58
R 86,2,13312
v_mul_f32 v137,v131,v132
M 26,a[56:59],62
v_mul_f32 v135,s88,v132
v_mul_f32 v131,s89,v132
M 30,a[52:55],58
R 90,2,14336
v_mul_f32 v136,v135,v132
M 30,a[60:63],62
L 5
v_mul_f32 v136,v136,v131
v_mul_f32 v114,v18,v18
M 30,a[68:71],66
s_waitcnt vmcnt(4)
)"
R"(R 94,2,15360
v_mul_f32 v115,v19,v19
M 30,a[76:79],70
s_barrier
D 0,56,2048
M 26,a[64:67],66
R 34,2,16384
v_mul_f32 v116,v20,v20
M 26,a[72:75],70
L 5
ds_write_b128 v12,v[134:137] offset:2048
M 26,a[80:83],74
R 38,2,17408
v_mul_f32 v117,v21,v21
M 26,a[88:91],78
v_fma_f32 v114,v100,v114,v99
v_fma_f32 v115,v100,v115,v99
M 30,a[84:87],74
R 42,2,18432
v_fma_f32 v116,v100,v116,v99
M 30,a[92:95],78
L 6
D 0,56,3072
M 30,a[100:103],82
s_add_u32 m0,s59,1024
R 46,2,19456
v_fma_f32 v117,v100,v117,v99
M 30,a[108:111],86
s_add_u32 s56,s55,16384
v_mul_f32 v114,v18,v114
v_mul_f32 v115,v19,v115
M 26,a[96:99],82
s_add_u32 s55,s55,s60
R 50,2,20480
v_mul_f32 v116,v20,v116
M 26,a[104:107],86
L 6
v_exp_f32 v114,v114
M 26,a[112:115],90
R 54,2,21504
v_mul_f32 v117,v21,v117
M 26,a[120:123],94
v_exp_f32 v115,v115
M 30,a[116:119],90
R 58,2,22528
v_fma_f32 v114,v114,v101,v101
M 30,a[124:127],94
L 5
buffer_load_dword v8,s[36:39],s58 offen offset:0 sc1 lds
M 30,a[132:135],34
s_add_u32 m0,s57,16384
R 62,2,23552
v_fma_f32 v115,v115,v101,v101
M 30,a[140:143],38
s_sub_u32 s57,s84,s57
D 0,56,0
M 26,a[128:131],34
R 66,2,24576
v_fma_f32 v22,-v98,v110,v22
M 26,a[136:139],38
L 5
v_exp_f32 v116,v116
M 26,a[144:147],42
R 70,2,25600
v_fma_f32 v116,v116,v101,v101
M 26,a[152:155],46
v_exp_f32 v117,v117
M 30,a[148:151],42
R 74,2,26624
v_fma_f32 v117,v117,v101,v101
M 30,a[156:159],46
L 5
v_rcp_f32 v114,v114
M 30,a[164:167],50
R 78,2,27648
v_mul_f32 v18,v18,v114
M 30,a[172:175],54
D 0,56,1024
M 26,a[160:163],50
R 82,2,28672
v_fma_f32 v23,-v98,v111,v23
M 26,a[168:171],54
L 5
v_rcp_f32 v115,v115
M 26,a[176:179],58
R 86,2,29696
v_mul_f32 v19,v19,v115
M 26,a[184:187],62
v_rcp_f32 v116,v116
M 30,a[180:183],58
R 90,2,30720
v_mul_f32 v20,v20,v116
M 30,a[188:191],62
L 5
s_nop 0
D 0,56,2048
M 30,a[196:199],66
s_waitcnt vmcnt(3)
R 94,2,31744
v_cvt_pk_bf16_f32 v122,v18,v19
M 30,a[204:207],70
s_barrier
buffer_store_dword v4,v3,s[36:39],s70 offen offset:0 sc0 sc1
M 26,a[192:195],66
s_mov_b32 s70,s69
R 34,1,0
ds_read_b32 v7,v6 offset:1024
M 26,a[200:203],70
L 6
R 102,13,2048
v_fma_f32 v24,-v98,v112,v24
M 26,a[208:211],74
s_mov_b32 s69,s68
R 38,1,1024
v_fma_f32 v25,-v98,v113,v25
M 26,a[216:219],78
s_add_u32 s68,s68,s63
v_mul_f32 v118,v22,v22
v_mul_f32 v119,v23,v23
M 30,a[212:215],74
R 42,1,2048
v_mul_f32 v120,v24,v24
M 30,a[220:223],78
L 7
D 0,56,3072
M 30,a[228:231],82
L 3
R 46,1,3072
v_mul_f32 v121,v25,v25
M 30,a[236:239],86
v_fma_f32 v118,v100,v118,v99
v_fma_f32 v119,v100,v119,v99
M 26,a[224:227],82
R 50,1,4096
v_fma_f32 v120,v100,v120,v99
M 26,a[232:235],86
s_nop 0
v_fma_f32 v121,v100,v121,v99
v_mul_f32 v118,v22,v118
v_mul_f32 v119,v23,v119
v_mul_f32 v120,v24,v120
v_mul_f32 v121,v25,v121
v_exp_f32 v118,v118
v_exp_f32 v119,v119
v_exp_f32 v120,v120
v_exp_f32 v121,v121
M 26,a[240:243],90
R 54,1,5120
v_rcp_f32 v117,v117
v_fma_f32 v118,v118,v101,v101
v_fma_f32 v119,v119,v101,v101
v_fma_f32 v120,v120,v101,v101
v_fma_f32 v121,v121,v101,v101
v_rcp_f32 v118,v118
v_rcp_f32 v119,v119
v_rcp_f32 v120,v120
v_rcp_f32 v121,v121
M 26,a[248:251],94
v_mul_f32 v21,v21,v117
v_cvt_pk_bf16_f32 v123,v20,v21
v_mul_f32 v22,v22,v118
v_mul_f32 v23,v23,v119
v_mul_f32 v24,v24,v120
v_mul_f32 v25,v25,v121
v_cvt_pk_bf16_f32 v124,v22,v23
v_cvt_pk_bf16_f32 v125,v24,v25
v_cmp_eq_u32 vcc,s53,v7
M 30,a[244:247],90
s_cmp_eq_u32 vcc_lo,-1
R 58,1,6144
buffer_store_dwordx4 v[122:125],v0,s[36:39],s66 offen offset:0 sc0 sc1
M 30,a[252:255],94
L 5
s_cbranch_scc1 Lg3o_F0_%=
Lg3o_L0_%=:
v_mfma_f32_16x16x32_bf16 v[22:25],a[4:7],v[34:37],0
s_add_u32 m0,s59,3072
R 62,1,7168
v_add_f32_dpp v131,v130,v130 quad_perm:[1,0,3,2] row_mask:0xf bank_mask:0xf
M 22,a[12:15],38
s_mov_b32 s66,s65
buffer_load_dword v8,s[36:39],s58 offen offset:0 sc1 lds
v_mfma_f32_16x16x32_bf16 v[18:21],a[0:3],v[34:37],0
s_add_u32 s65,s65,s62
R 66,1,8192
v_add_f32_dpp v131,v131,v131 quad_perm:[2,3,0,1] row_mask:0xf bank_mask:0xf
M 18,a[8:11],38
L 5
v_fma_f32 v26,-v102,v106,v26
v_fma_f32 v27,-v102,v107,v27
M 18,a[16:19],42
R 70,1,9216
v_add_f32_dpp v134,v131,v131 row_half_mirror row_mask:0xf bank_mask:0xf
M 18,a[24:27],46
v_fma_f32 v28,-v102,v108,v28
v_fma_f32 v29,-v102,v109,v29
M 22,a[20:23],42
R 74,1,10240
v_mov_b32_dpp v131,v134 row_ror:8 row_mask:0xf bank_mask:0xf
M 22,a[28:31],46
L 5
v_fma_f32 v131,-v134,v134,v131
v_max_f32 v131,s87,v131
M 22,a[36:39],50
R 78,1,11264
v_mul_f32 v114,v26,v26
M 22,a[44:47],54
v_rsq_f32 v132,v131
M 18,a[32:35],50
R 82,1,12288
v_mul_f32 v137,v131,v132
M 18,a[40:43],54
L 5
v_mul_f32 v135,s88,v132
v_mul_f32 v131,s89,v132
M 18,a[48:51],58
R 86,1,13312
v_mul_f32 v136,v135,v132
M 18,a[56:59],62
v_mul_f32 v136,v136,v131
v_mul_f32 v115,v27,v27
M 22,a[52:55],58
R 90,1,14336
v_mul_f32 v116,v28,v28
M 22,a[60:63],62
L 5
v_mul_f32 v117,v29,v29
v_fma_f32 v114,v104,v114,v103
M 22,a[68:71],66
s_waitcnt vmcnt(0)
R 94,1,15360
v_fma_f32 v115,v104,v115,v103
M 22,a[76:79],70
s_barrier
ds_read_b32 v7,v6 offset:3072
v_fma_f32 v116,v104,v116,v103
M 18,a[64:67],66
R 34,1,16384
v_fma_f32 v117,v104,v117,v103
M 18,a[72:75],70
L 6
buffer_store_dword v4,v3,s[36:39],s70 offen offset:0 sc0 sc1
M 18,a[80:83],74
s_mov_b32 s70,0x7fff0000
R 38,1,17408
v_mul_f32 v114,v26,v114
M 18,a[88:91],78
ds_write_b128 v12,v[134:137] offset:0
M 22,a[84:87],74
R 42,1,18432
v_mul_f32 v115,v27,v115
M 22,a[92:95],78
L 7
L 4
v_cmp_eq_u32 vcc,s53,v7
M 22,a[100:103],82
R 46,1,19456
v_mul_f32 v116,v28,v116
M 22,a[108:111],86
s_cmp_eq_u32 vcc_lo,-1
v_exp_f32 v114,v114
s_cbranch_scc0 Lg3o_X0_%=
M 18,a[96:99],82
s_add_u32 m0,s57,0
s_add_u32 s56,s55,0
s_add_u32 s58,s58,s61
R 50,1,20480
M 18,a[104:107],86
s_nop 0
buffer_load_dword v130,v11,s[36:39],s73 offen offset:0 sc1
M 18,a[112:115],90
s_add_u32 s54,s54,s46
R 54,1,21504
v_mul_f32 v117,v29,v117
M 18,a[120:123],94
s_add_u32 s73,s73,s64
D 0,56,0
M 22,a[116:119],90
R 58,1,22528
v_fma_f32 v114,v114,v105,v105
M 22,a[124:127],94
L 6
v_exp_f32 v115,v115
M 22,a[132:135],34
R 62,1,23552
v_fma_f32 v115,v115,v105,v105
M 22,a[140:143],38
D 0,56,1024
M 18,a[128:131],34
R 66,1,24576
v_fma_f32 v30,-v102,v110,v30
M 18,a[136:139],38
L 5
v_exp_f32 v116,v116
M 18,a[144:147],42
R 70,1,25600
v_fma_f32 v116,v116,v105,v105
M 18,a[152:155],46
D 0,56,2048
M 22,a[148:151],42
R 74,1,26624
v_fma_f32 v31,-v102,v111,v31
M 22,a[156:159],46
L 5
v_exp_f32 v117,v117
M 22,a[164:167],50
R 78,1,27648
v_fma_f32 v117,v117,v105,v105
M 22,a[172:175],54
D 0,56,3072
M 18,a[160:163],50
s_add_u32 m0,s59,0
R 82,1,28672
v_fma_f32 v32,-v102,v112,v32
M 18,a[168:171],54
L 5
buffer_load_dword v8,s[36:39],s58 offen offset:0 sc1 lds
M 18,a[176:179],58
s_add_u32 m0,s57,16384
R 86,1,29696
v_fma_f32 v33,-v102,v113,v33
M 18,a[184:187],62
s_add_u32 s56,s55,16384
v_rcp_f32 v114,v114
M 22,a[180:183],58
s_add_u32 s55,s55,s60
R 90,1,30720
v_mul_f32 v26,v26,v114
M 22,a[188:191],62
L 5
s_nop 0
D 0,56,0
M 22,a[196:199],66
s_waitcnt vmcnt(1)
R 94,1,31744
v_mul_f32 v118,v30,v30
M 22,a[204:207],70
s_barrier
buffer_store_dword v4,v3,s[36:39],s70 offen offset:0 sc0 sc1
M 18,a[192:195],66
s_mov_b32 s70,s69
R 34,2,0
ds_read_b32 v7,v6 offset:0
M 18,a[200:203],70
L 6
D 0,56,1024
M 18,a[208:211],74
s_mov_b32 s69,s68
R 38,2,1024
R 98,13,0
M 18,a[216:219],78
s_sub_u32 s57,s84,s57
v_mul_f32 v119,v31,v31
v_mul_f32 v120,v32,v32
M 22,a[212:215],74
s_add_u32 s68,s68,s63
R 42,2,2048
v_mul_f32 v121,v33,v33
M 22,a[220:223],78
L 7
D 0,56,2048
M 22,a[228:231],82
L 3
R 46,2,3072
v_fma_f32 v118,v104,v118,v103
M 22,a[236:239],86
v_fma_f32 v119,v104,v119,v103
v_fma_f32 v120,v104,v120,v103
M 18,a[224:227],82
R 50,2,4096
v_fma_f32 v121,v104,v121,v103
M 18,a[232:235],86
s_nop 0
v_mul_f32 v118,v30,v118
v_mul_f32 v119,v31,v119
v_mul_f32 v120,v32,v120
v_mul_f32 v121,v33,v121
v_exp_f32 v118,v118
v_exp_f32 v119,v119
v_exp_f32 v120,v120
v_exp_f32 v121,v121
M 18,a[240:243],90
R 54,2,5120
v_rcp_f32 v115,v115
v_rcp_f32 v116,v116
v_rcp_f32 v117,v117
v_fma_f32 v118,v118,v105,v105
v_fma_f32 v119,v119,v105,v105
v_fma_f32 v120,v120,v105,v105
v_fma_f32 v121,v121,v105,v105
v_rcp_f32 v118,v118
v_rcp_f32 v119,v119
v_rcp_f32 v120,v120
v_rcp_f32 v121,v121
M 18,a[248:251],94
v_mul_f32 v27,v27,v115
v_mul_f32 v28,v28,v116
v_mul_f32 v29,v29,v117
v_cvt_pk_bf16_f32 v122,v26,v27
v_cvt_pk_bf16_f32 v123,v28,v29
v_mul_f32 v30,v30,v118
v_mul_f32 v31,v31,v119
v_mul_f32 v32,v32,v120
v_mul_f32 v33,v33,v121
v_cvt_pk_bf16_f32 v124,v30,v31
v_cvt_pk_bf16_f32 v125,v32,v33
v_cmp_eq_u32 vcc,s53,v7
M 22,a[244:247],90
s_cmp_eq_u32 vcc_lo,-1
R 58,2,6144
D 0,56,3072
buffer_store_dwordx4 v[122:125],v0,s[36:39],s67 offen offset:0 sc0 sc1
M 22,a[252:255],94
L 6
s_cbranch_scc1 Lg3o_F1_%=
s_branch Lg3o_L1_%=
Lg3o_X0_%=:
M 18,a[96:99],82
s_add_u32 m0,s59,0
R 50,1,20480
v_mul_f32 v117,v29,v117
M 18,a[104:107],86
s_nop 0
buffer_load_dword v8,s[36:39],s58 offen offset:0 sc1 lds
M 18,a[112:115],90
R 54,1,21504
v_fma_f32 v114,v114,v105,v105
M 18,a[120:123],94
v_exp_f32 v115,v115
M 22,a[116:119],90
R 58,1,22528
v_fma_f32 v115,v115,v105,v105
M 22,a[124:127],94
L 6
v_exp_f32 v116,v116
M 22,a[132:135],34
R 62,1,23552
v_fma_f32 v116,v116,v105,v105
M 22,a[140:143],38
v_exp_f32 v117,v117
M 18,a[128:131],34
R 66,1,24576
v_fma_f32 v117,v117,v105,v105
M 18,a[136:139],38
L 5
v_rcp_f32 v114,v114
M 18,a[144:147],42
R 70,1,25600
v_mul_f32 v26,v26,v114
M 18,a[152:155],46
v_rcp_f32 v115,v115
M 22,a[148:151],42
R 74,1,26624
v_mul_f32 v27,v27,v115
M 22,a[156:159],46
L 5
v_rcp_f32 v116,v116
M 22,a[164:167],50
R 78,1,27648
v_mul_f32 v28,v28,v116
M 22,a[172:175],54
v_rcp_f32 v117,v117
M 18,a[160:163],50
R 82,1,28672
v_mul_f32 v29,v29,v117
M 18,a[168:171],54
L 5
v_cvt_pk_bf16_f32 v122,v26,v27
v_cvt_pk_bf16_f32 v123,v28,v29
M 18,a[176:179],58
R 86,1,29696
v_fma_f32 v30,-v102,v110,v30
M 18,a[184:187],62
v_fma_f32 v31,-v102,v111,v31
v_fma_f32 v32,-v102,v112,v32
M 22,a[180:183],58
R 90,1,30720
v_fma_f32 v33,-v102,v113,v33
M 22,a[188:191],62
L 5
s_nop 0
v_mul_f32 v118,v30,v30
M 22,a[196:199],66
s_waitcnt vmcnt(0)
R 94,1,31744
v_mul_f32 v119,v31,v31
M 22,a[204:207],70
s_barrier
buffer_store_dword v4,v3,s[36:39],s70 offen offset:0 sc0 sc1
M 18,a[192:195],66
s_mov_b32 s70,s69
ds_read_b32 v7,v6 offset:0
R 98,13,0
M 18,a[200:203],70
L 6
v_mul_f32 v120,v32,v32
v_mul_f32 v121,v33,v33
M 18,a[208:211],74
s_mov_b32 s69,s68
v_fma_f32 v118,v104,v118,v103
)"
R"(v_fma_f32 v119,v104,v119,v103
M 18,a[216:219],78
s_add_u32 s68,s68,s63
v_fma_f32 v120,v104,v120,v103
v_fma_f32 v121,v104,v121,v103
M 22,a[212:215],74
v_mul_f32 v118,v30,v118
v_mul_f32 v119,v31,v119
M 22,a[220:223],78
L 4
v_mul_f32 v120,v32,v120
v_mul_f32 v121,v33,v121
M 22,a[228:231],82
L 1
v_exp_f32 v118,v118
M 22,a[236:239],86
v_exp_f32 v119,v119
M 18,a[224:227],82
v_exp_f32 v120,v120
M 18,a[232:235],86
s_nop 0
v_exp_f32 v121,v121
M 18,a[240:243],90
v_fma_f32 v118,v118,v105,v105
v_fma_f32 v119,v119,v105,v105
v_fma_f32 v120,v120,v105,v105
v_fma_f32 v121,v121,v105,v105
v_rcp_f32 v118,v118
v_rcp_f32 v119,v119
v_rcp_f32 v120,v120
v_rcp_f32 v121,v121
M 18,a[248:251],94
v_mul_f32 v30,v30,v118
v_mul_f32 v31,v31,v119
v_mul_f32 v32,v32,v120
v_mul_f32 v33,v33,v121
v_cvt_pk_bf16_f32 v124,v30,v31
v_cvt_pk_bf16_f32 v125,v32,v33
M 22,a[244:247],90
buffer_store_dwordx4 v[122:125],v0,s[36:39],s67 offen offset:0 sc0 sc1
v_cmp_eq_u32 vcc,s53,v7
M 22,a[252:255],94
s_mov_b32 s80,1
s_cmp_eq_u32 vcc_lo,-1
s_cbranch_scc0 Lg3o_xn0_%=
buffer_load_dword v130,v11,s[36:39],s73 offen offset:0 sc1
L 0
v_fma_f32 v18,-v98,v106,v18
v_fma_f32 v19,-v98,v107,v19
v_fma_f32 v20,-v98,v108,v20
v_fma_f32 v21,-v98,v109,v21
v_mul_f32 v114,v18,v18
v_mul_f32 v115,v19,v19
s_add_u32 s73,s73,s64
s_add_u32 m0,s57,0
s_add_u32 s56,s55,0
D 0,56,0
v_mul_f32 v116,v20,v20
v_mul_f32 v117,v21,v21
v_fma_f32 v114,v100,v114,v99
v_fma_f32 v115,v100,v115,v99
v_fma_f32 v116,v100,v116,v99
v_fma_f32 v117,v100,v117,v99
D 0,56,1024
v_mul_f32 v114,v18,v114
v_mul_f32 v115,v19,v115
v_mul_f32 v116,v20,v116
v_mul_f32 v117,v21,v117
v_exp_f32 v114,v114
v_exp_f32 v115,v115
D 0,56,2048
v_exp_f32 v116,v116
v_exp_f32 v117,v117
v_fma_f32 v114,v114,v101,v101
v_fma_f32 v115,v115,v101,v101
v_fma_f32 v116,v116,v101,v101
v_fma_f32 v117,v117,v101,v101
D 0,56,3072
v_rcp_f32 v114,v114
v_rcp_f32 v115,v115
v_rcp_f32 v116,v116
v_rcp_f32 v117,v117
v_mul_f32 v18,v18,v114
v_mul_f32 v19,v19,v115
s_add_u32 m0,s57,16384
s_add_u32 s56,s55,16384
D 0,56,0
v_mul_f32 v20,v20,v116
v_mul_f32 v21,v21,v117
v_cvt_pk_bf16_f32 v122,v18,v19
v_cvt_pk_bf16_f32 v123,v20,v21
v_fma_f32 v22,-v98,v110,v22
v_fma_f32 v23,-v98,v111,v23
D 0,56,1024
v_fma_f32 v24,-v98,v112,v24
v_fma_f32 v25,-v98,v113,v25
v_mul_f32 v118,v22,v22
v_mul_f32 v119,v23,v23
v_mul_f32 v120,v24,v24
v_mul_f32 v121,v25,v25
D 0,56,2048
v_fma_f32 v118,v100,v118,v99
v_fma_f32 v119,v100,v119,v99
v_fma_f32 v120,v100,v120,v99
v_fma_f32 v121,v100,v121,v99
v_mul_f32 v118,v22,v118
v_mul_f32 v119,v23,v119
D 0,56,3072
v_mul_f32 v120,v24,v120
v_mul_f32 v121,v25,v121
v_exp_f32 v118,v118
v_exp_f32 v119,v119
v_exp_f32 v120,v120
v_exp_f32 v121,v121
s_add_u32 s55,s55,s60
s_sub_u32 s57,s84,s57
s_add_u32 s54,s54,s46
v_fma_f32 v118,v118,v101,v101
v_fma_f32 v119,v119,v101,v101
v_fma_f32 v120,v120,v101,v101
v_fma_f32 v121,v121,v101,v101
v_rcp_f32 v118,v118
v_rcp_f32 v119,v119
v_rcp_f32 v120,v120
v_rcp_f32 v121,v121
v_mul_f32 v22,v22,v118
v_mul_f32 v23,v23,v119
v_mul_f32 v24,v24,v120
v_mul_f32 v25,v25,v121
v_cvt_pk_bf16_f32 v124,v22,v23
v_cvt_pk_bf16_f32 v125,v24,v25
buffer_store_dwordx4 v[122:125],v0,s[36:39],s66 offen offset:0 sc0 sc1
s_mov_b32 s72,s69
s_mov_b32 s69,0x7fff0000
s_mov_b32 s66,0x7fff0000
s_mov_b32 s76,0x7fff0000
s_branch Lg3o_gox_%=
Lg3o_xn0_%=:
s_cmp_ge_u32 s54,256
s_cbranch_scc1 Lg3o_fin0_%=
s_add_u32 m0,s59,4096
s_nop 0
buffer_load_dword v8,s[36:39],s58 offen offset:0 sc1 lds
buffer_store_dword v4,v3,s[36:39],s71 offen offset:0 sc0 sc1
s_add_u32 m0,s59,5120
s_nop 0
buffer_load_dword v8,s[36:39],s58 offen offset:0 sc1 lds
buffer_store_dword v4,v3,s[36:39],s71 offen offset:0 sc0 sc1
s_mov_b32 s83,0
L 0
v_fma_f32 v18,-v98,v106,v18
v_fma_f32 v19,-v98,v107,v19
v_fma_f32 v20,-v98,v108,v20
v_fma_f32 v21,-v98,v109,v21
v_mul_f32 v114,v18,v18
v_mul_f32 v115,v19,v19
v_mul_f32 v116,v20,v20
v_mul_f32 v117,v21,v21
v_fma_f32 v114,v100,v114,v99
v_fma_f32 v115,v100,v115,v99
v_fma_f32 v116,v100,v116,v99
v_fma_f32 v117,v100,v117,v99
v_mul_f32 v114,v18,v114
v_mul_f32 v115,v19,v115
v_mul_f32 v116,v20,v116
v_mul_f32 v117,v21,v117
v_exp_f32 v114,v114
v_exp_f32 v115,v115
v_exp_f32 v116,v116
v_exp_f32 v117,v117
v_fma_f32 v114,v114,v101,v101
v_fma_f32 v115,v115,v101,v101
v_fma_f32 v116,v116,v101,v101
v_fma_f32 v117,v117,v101,v101
v_rcp_f32 v114,v114
v_rcp_f32 v115,v115
v_rcp_f32 v116,v116
v_rcp_f32 v117,v117
v_mul_f32 v18,v18,v114
v_mul_f32 v19,v19,v115
v_mul_f32 v20,v20,v116
v_mul_f32 v21,v21,v117
v_cvt_pk_bf16_f32 v122,v18,v19
v_cvt_pk_bf16_f32 v123,v20,v21
v_fma_f32 v22,-v98,v110,v22
v_fma_f32 v23,-v98,v111,v23
v_fma_f32 v24,-v98,v112,v24
v_fma_f32 v25,-v98,v113,v25
v_mul_f32 v118,v22,v22
v_mul_f32 v119,v23,v23
v_mul_f32 v120,v24,v24
v_mul_f32 v121,v25,v25
v_fma_f32 v118,v100,v118,v99
v_fma_f32 v119,v100,v119,v99
v_fma_f32 v120,v100,v120,v99
v_fma_f32 v121,v100,v121,v99
v_mul_f32 v118,v22,v118
v_mul_f32 v119,v23,v119
v_mul_f32 v120,v24,v120
v_mul_f32 v121,v25,v121
v_exp_f32 v118,v118
v_exp_f32 v119,v119
v_exp_f32 v120,v120
v_exp_f32 v121,v121
v_fma_f32 v118,v118,v101,v101
v_fma_f32 v119,v119,v101,v101
v_fma_f32 v120,v120,v101,v101
v_fma_f32 v121,v121,v101,v101
v_rcp_f32 v118,v118
v_rcp_f32 v119,v119
v_rcp_f32 v120,v120
v_rcp_f32 v121,v121
v_mul_f32 v22,v22,v118
v_mul_f32 v23,v23,v119
v_mul_f32 v24,v24,v120
v_mul_f32 v25,v25,v121
v_cvt_pk_bf16_f32 v124,v22,v23
v_cvt_pk_bf16_f32 v125,v24,v25
buffer_store_dwordx4 v[122:125],v0,s[36:39],s66 offen offset:0 sc0 sc1
s_mov_b32 s72,s69
s_mov_b32 s69,0x7fff0000
s_mov_b32 s66,0x7fff0000
s_mov_b32 s76,0x7fff0000
s_branch Lg3o_poll_%=
Lg3o_fin0_%=:
L 0
v_fma_f32 v18,-v98,v106,v18
v_fma_f32 v19,-v98,v107,v19
v_fma_f32 v20,-v98,v108,v20
v_fma_f32 v21,-v98,v109,v21
v_mul_f32 v114,v18,v18
v_mul_f32 v115,v19,v19
v_mul_f32 v116,v20,v20
v_mul_f32 v117,v21,v21
v_fma_f32 v114,v100,v114,v99
v_fma_f32 v115,v100,v115,v99
v_fma_f32 v116,v100,v116,v99
v_fma_f32 v117,v100,v117,v99
v_mul_f32 v114,v18,v114
v_mul_f32 v115,v19,v115
v_mul_f32 v116,v20,v116
v_mul_f32 v117,v21,v117
v_exp_f32 v114,v114
v_exp_f32 v115,v115
v_exp_f32 v116,v116
v_exp_f32 v117,v117
v_fma_f32 v114,v114,v101,v101
v_fma_f32 v115,v115,v101,v101
v_fma_f32 v116,v116,v101,v101
v_fma_f32 v117,v117,v101,v101
v_rcp_f32 v114,v114
v_rcp_f32 v115,v115
v_rcp_f32 v116,v116
v_rcp_f32 v117,v117
v_mul_f32 v18,v18,v114
v_mul_f32 v19,v19,v115
v_mul_f32 v20,v20,v116
v_mul_f32 v21,v21,v117
v_cvt_pk_bf16_f32 v122,v18,v19
v_cvt_pk_bf16_f32 v123,v20,v21
v_fma_f32 v22,-v98,v110,v22
v_fma_f32 v23,-v98,v111,v23
v_fma_f32 v24,-v98,v112,v24
v_fma_f32 v25,-v98,v113,v25
v_mul_f32 v118,v22,v22
v_mul_f32 v119,v23,v23
v_mul_f32 v120,v24,v24
v_mul_f32 v121,v25,v25
v_fma_f32 v118,v100,v118,v99
v_fma_f32 v119,v100,v119,v99
v_fma_f32 v120,v100,v120,v99
v_fma_f32 v121,v100,v121,v99
v_mul_f32 v118,v22,v118
v_mul_f32 v119,v23,v119
v_mul_f32 v120,v24,v120
v_mul_f32 v121,v25,v121
v_exp_f32 v118,v118
v_exp_f32 v119,v119
v_exp_f32 v120,v120
v_exp_f32 v121,v121
v_fma_f32 v118,v118,v101,v101
v_fma_f32 v119,v119,v101,v101
v_fma_f32 v120,v120,v101,v101
v_fma_f32 v121,v121,v101,v101
v_rcp_f32 v118,v118
v_rcp_f32 v119,v119
v_rcp_f32 v120,v120
v_rcp_f32 v121,v121
v_mul_f32 v22,v22,v118
v_mul_f32 v23,v23,v119
v_mul_f32 v24,v24,v120
v_mul_f32 v25,v25,v121
v_cvt_pk_bf16_f32 v124,v22,v23
v_cvt_pk_bf16_f32 v125,v24,v25
buffer_store_dwordx4 v[122:125],v0,s[36:39],s66 offen offset:0 sc0 sc1
s_mov_b32 s72,s69
s_mov_b32 s69,0x7fff0000
s_mov_b32 s66,0x7fff0000
s_mov_b32 s76,0x7fff0000
s_branch Lg3o_done_%=
Lg3o_L1_%=:
v_mfma_f32_16x16x32_bf16 v[30:33],a[4:7],v[34:37],0
s_add_u32 m0,s59,3072
R 62,2,7168
v_add_f32_dpp v131,v130,v130 quad_perm:[1,0,3,2] row_mask:0xf bank_mask:0xf
M 30,a[12:15],38
s_mov_b32 s67,s65
buffer_load_dword v8,s[36:39],s58 offen offset:0 sc1 lds
v_mfma_f32_16x16x32_bf16 v[26:29],a[0:3],v[34:37],0
s_add_u32 s65,s65,s62
R 66,2,8192
v_add_f32_dpp v131,v131,v131 quad_perm:[2,3,0,1] row_mask:0xf bank_mask:0xf
M 26,a[8:11],38
L 5
v_fma_f32 v18,-v98,v106,v18
v_fma_f32 v19,-v98,v107,v19
M 26,a[16:19],42
R 70,2,9216
v_add_f32_dpp v134,v131,v131 row_half_mirror row_mask:0xf bank_mask:0xf
M 26,a[24:27],46
v_fma_f32 v20,-v98,v108,v20
v_fma_f32 v21,-v98,v109,v21
M 30,a[20:23],42
R 74,2,10240
v_mov_b32_dpp v131,v134 row_ror:8 row_mask:0xf bank_mask:0xf
M 30,a[28:31],46
L 5
v_fma_f32 v131,-v134,v134,v131
v_max_f32 v131,s87,v131
M 30,a[36:39],50
R 78,2,11264
v_mul_f32 v114,v18,v18
M 30,a[44:47],54
v_rsq_f32 v132,v131
M 26,a[32:35],50
R 82,2,12288
v_mul_f32 v137,v131,v132
M 26,a[40:43],54
L 5
v_mul_f32 v135,s88,v132
v_mul_f32 v131,s89,v132
M 26,a[48:51],58
R 86,2,13312
v_mul_f32 v136,v135,v132
M 26,a[56:59],62
v_mul_f32 v136,v136,v131
v_mul_f32 v115,v19,v19
M 30,a[52:55],58
R 90,2,14336
v_mul_f32 v116,v20,v20
M 30,a[60:63],62
L 5
v_mul_f32 v117,v21,v21
v_fma_f32 v114,v100,v114,v99
M 30,a[68:71],66
s_waitcnt vmcnt(0)
R 94,2,15360
v_fma_f32 v115,v100,v115,v99
M 30,a[76:79],70
s_barrier
ds_read_b32 v7,v6 offset:3072
v_fma_f32 v116,v100,v116,v99
M 26,a[64:67],66
R 34,2,16384
v_fma_f32 v117,v100,v117,v99
M 26,a[72:75],70
L 6
buffer_store_dword v4,v3,s[36:39],s70 offen offset:0 sc0 sc1
M 26,a[80:83],74
s_mov_b32 s70,0x7fff0000
R 38,2,17408
v_mul_f32 v114,v18,v114
M 26,a[88:91],78
ds_write_b128 v12,v[134:137] offset:2048
M 30,a[84:87],74
R 42,2,18432
v_mul_f32 v115,v19,v115
M 30,a[92:95],78
L 7
L 4
v_cmp_eq_u32 vcc,s53,v7
M 30,a[100:103],82
R 46,2,19456
v_mul_f32 v116,v20,v116
M 30,a[108:111],86
s_cmp_eq_u32 vcc_lo,-1
v_exp_f32 v114,v114
s_cbranch_scc0 Lg3o_X1_%=
M 26,a[96:99],82
s_add_u32 m0,s57,0
s_add_u32 s56,s55,0
s_add_u32 s58,s58,s61
R 50,2,20480
M 26,a[104:107],86
s_nop 0
buffer_load_dword v130,v11,s[36:39],s73 offen offset:0 sc1
M 26,a[112:115],90
s_add_u32 s54,s54,s46
R 54,2,21504
v_mul_f32 v117,v21,v117
M 26,a[120:123],94
s_add_u32 s73,s73,s64
D 0,56,0
M 30,a[116:119],90
R 58,2,22528
v_fma_f32 v114,v114,v101,v101
M 30,a[124:127],94
L 6
v_exp_f32 v115,v115
M 30,a[132:135],34
R 62,2,23552
v_fma_f32 v115,v115,v101,v101
M 30,a[140:143],38
D 0,56,1024
M 26,a[128:131],34
R 66,2,24576
v_fma_f32 v22,-v98,v110,v22
M 26,a[136:139],38
L 5
v_exp_f32 v116,v116
M 26,a[144:147],42
R 70,2,25600
v_fma_f32 v116,v116,v101,v101
M 26,a[152:155],46
D 0,56,2048
M 30,a[148:151],42
R 74,2,26624
v_fma_f32 v23,-v98,v111,v23
M 30,a[156:159],46
L 5
v_exp_f32 v117,v117
M 30,a[164:167],50
R 78,2,27648
v_fma_f32 v117,v117,v101,v101
M 30,a[172:175],54
D 0,56,3072
M 26,a[160:163],50
s_add_u32 m0,s59,1024
R 82,2,28672
v_fma_f32 v24,-v98,v112,v24
M 26,a[168:171],54
L 5
buffer_load_dword v8,s[36:39],s58 offen offset:0 sc1 lds
M 26,a[176:179],58
s_add_u32 m0,s57,16384
R 86,2,29696
v_fma_f32 v25,-v98,v113,v25
M 26,a[184:187],62
s_add_u32 s56,s55,16384
v_rcp_f32 v114,v114
M 30,a[180:183],58
s_add_u32 s55,s55,s60
R 90,2,30720
v_mul_f32 v18,v18,v114
M 30,a[188:191],62
L 5
s_nop 0
D 0,56,0
M 30,a[196:199],66
s_waitcnt vmcnt(1)
R 94,2,31744
v_mul_f32 v118,v22,v22
M 30,a[204:207],70
s_barrier
buffer_store_dword v4,v3,s[36:39],s70 offen offset:0 sc0 sc1
M 26,a[192:195],66
s_mov_b32 s70,s69
R 34,1,0
ds_read_b32 v7,v6 offset:1024
M 26,a[200:203],70
L 6
D 0,56,1024
M 26,a[208:211],74
s_mov_b32 s69,s68
R 38,1,1024
R 102,13,2048
M 26,a[216:219],78
s_sub_u32 s57,s84,s57
v_mul_f32 v119,v23,v23
v_mul_f32 v120,v24,v24
M 30,a[212:215],74
s_add_u32 s68,s68,s63
R 42,1,2048
v_mul_f32 v121,v25,v25
M 30,a[220:223],78
L 7
D 0,56,2048
M 30,a[228:231],82
L 3
R 46,1,3072
v_fma_f32 v118,v100,v118,v99
M 30,a[236:239],86
v_fma_f32 v119,v100,v119,v99
v_fma_f32 v120,v100,v120,v99
M 26,a[224:227],82
R 50,1,4096
v_fma_f32 v121,v100,v121,v99
M 26,a[232:235],86
s_nop 0
v_mul_f32 v118,v22,v118
)"
R"(v_mul_f32 v119,v23,v119
v_mul_f32 v120,v24,v120
v_mul_f32 v121,v25,v121
v_exp_f32 v118,v118
v_exp_f32 v119,v119
v_exp_f32 v120,v120
v_exp_f32 v121,v121
M 26,a[240:243],90
R 54,1,5120
v_rcp_f32 v115,v115
v_rcp_f32 v116,v116
v_rcp_f32 v117,v117
v_fma_f32 v118,v118,v101,v101
v_fma_f32 v119,v119,v101,v101
v_fma_f32 v120,v120,v101,v101
v_fma_f32 v121,v121,v101,v101
v_rcp_f32 v118,v118
v_rcp_f32 v119,v119
v_rcp_f32 v120,v120
v_rcp_f32 v121,v121
M 26,a[248:251],94
v_mul_f32 v19,v19,v115
v_mul_f32 v20,v20,v116
v_mul_f32 v21,v21,v117
v_cvt_pk_bf16_f32 v122,v18,v19
v_cvt_pk_bf16_f32 v123,v20,v21
v_mul_f32 v22,v22,v118
v_mul_f32 v23,v23,v119
v_mul_f32 v24,v24,v120
v_mul_f32 v25,v25,v121
v_cvt_pk_bf16_f32 v124,v22,v23
v_cvt_pk_bf16_f32 v125,v24,v25
v_cmp_eq_u32 vcc,s53,v7
M 30,a[244:247],90
s_cmp_eq_u32 vcc_lo,-1
R 58,1,6144
D 0,56,3072
buffer_store_dwordx4 v[122:125],v0,s[36:39],s66 offen offset:0 sc0 sc1
M 30,a[252:255],94
L 6
s_cbranch_scc1 Lg3o_F0_%=
s_branch Lg3o_L0_%=
Lg3o_X1_%=:
M 26,a[96:99],82
s_add_u32 m0,s59,1024
R 50,2,20480
v_mul_f32 v117,v21,v117
M 26,a[104:107],86
s_nop 0
buffer_load_dword v8,s[36:39],s58 offen offset:0 sc1 lds
M 26,a[112:115],90
R 54,2,21504
v_fma_f32 v114,v114,v101,v101
M 26,a[120:123],94
v_exp_f32 v115,v115
M 30,a[116:119],90
R 58,2,22528
v_fma_f32 v115,v115,v101,v101
M 30,a[124:127],94
L 6
v_exp_f32 v116,v116
M 30,a[132:135],34
R 62,2,23552
v_fma_f32 v116,v116,v101,v101
M 30,a[140:143],38
v_exp_f32 v117,v117
M 26,a[128:131],34
R 66,2,24576
v_fma_f32 v117,v117,v101,v101
M 26,a[136:139],38
L 5
v_rcp_f32 v114,v114
M 26,a[144:147],42
R 70,2,25600
v_mul_f32 v18,v18,v114
M 26,a[152:155],46
v_rcp_f32 v115,v115
M 30,a[148:151],42
R 74,2,26624
v_mul_f32 v19,v19,v115
M 30,a[156:159],46
L 5
v_rcp_f32 v116,v116
M 30,a[164:167],50
R 78,2,27648
v_mul_f32 v20,v20,v116
M 30,a[172:175],54
v_rcp_f32 v117,v117
M 26,a[160:163],50
R 82,2,28672
v_mul_f32 v21,v21,v117
M 26,a[168:171],54
L 5
v_cvt_pk_bf16_f32 v122,v18,v19
v_cvt_pk_bf16_f32 v123,v20,v21
M 26,a[176:179],58
R 86,2,29696
v_fma_f32 v22,-v98,v110,v22
M 26,a[184:187],62
v_fma_f32 v23,-v98,v111,v23
v_fma_f32 v24,-v98,v112,v24
M 30,a[180:183],58
R 90,2,30720
v_fma_f32 v25,-v98,v113,v25
M 30,a[188:191],62
L 5
s_nop 0
v_mul_f32 v118,v22,v22
M 30,a[196:199],66
s_waitcnt vmcnt(0)
R 94,2,31744
v_mul_f32 v119,v23,v23
M 30,a[204:207],70
s_barrier
buffer_store_dword v4,v3,s[36:39],s70 offen offset:0 sc0 sc1
M 26,a[192:195],66
s_mov_b32 s70,s69
ds_read_b32 v7,v6 offset:1024
R 102,13,2048
M 26,a[200:203],70
L 6
v_mul_f32 v120,v24,v24
v_mul_f32 v121,v25,v25
M 26,a[208:211],74
s_mov_b32 s69,s68
v_fma_f32 v118,v100,v118,v99
v_fma_f32 v119,v100,v119,v99
M 26,a[216:219],78
s_add_u32 s68,s68,s63
v_fma_f32 v120,v100,v120,v99
v_fma_f32 v121,v100,v121,v99
M 30,a[212:215],74
v_mul_f32 v118,v22,v118
v_mul_f32 v119,v23,v119
M 30,a[220:223],78
L 4
v_mul_f32 v120,v24,v120
v_mul_f32 v121,v25,v121
M 30,a[228:231],82
L 1
v_exp_f32 v118,v118
M 30,a[236:239],86
v_exp_f32 v119,v119
M 26,a[224:227],82
v_exp_f32 v120,v120
M 26,a[232:235],86
s_nop 0
v_exp_f32 v121,v121
M 26,a[240:243],90
v_fma_f32 v118,v118,v101,v101
v_fma_f32 v119,v119,v101,v101
v_fma_f32 v120,v120,v101,v101
v_fma_f32 v121,v121,v101,v101
v_rcp_f32 v118,v118
v_rcp_f32 v119,v119
v_rcp_f32 v120,v120
v_rcp_f32 v121,v121
M 26,a[248:251],94
v_mul_f32 v22,v22,v118
v_mul_f32 v23,v23,v119
v_mul_f32 v24,v24,v120
v_mul_f32 v25,v25,v121
v_cvt_pk_bf16_f32 v124,v22,v23
v_cvt_pk_bf16_f32 v125,v24,v25
M 30,a[244:247],90
buffer_store_dwordx4 v[122:125],v0,s[36:39],s66 offen offset:0 sc0 sc1
v_cmp_eq_u32 vcc,s53,v7
M 30,a[252:255],94
s_mov_b32 s80,0
s_cmp_eq_u32 vcc_lo,-1
s_cbranch_scc0 Lg3o_xn1_%=
buffer_load_dword v130,v11,s[36:39],s73 offen offset:0 sc1
L 0
v_fma_f32 v26,-v102,v106,v26
v_fma_f32 v27,-v102,v107,v27
v_fma_f32 v28,-v102,v108,v28
v_fma_f32 v29,-v102,v109,v29
v_mul_f32 v114,v26,v26
v_mul_f32 v115,v27,v27
s_add_u32 s73,s73,s64
s_add_u32 m0,s57,0
s_add_u32 s56,s55,0
D 0,56,0
v_mul_f32 v116,v28,v28
v_mul_f32 v117,v29,v29
v_fma_f32 v114,v104,v114,v103
v_fma_f32 v115,v104,v115,v103
v_fma_f32 v116,v104,v116,v103
v_fma_f32 v117,v104,v117,v103
D 0,56,1024
v_mul_f32 v114,v26,v114
v_mul_f32 v115,v27,v115
v_mul_f32 v116,v28,v116
v_mul_f32 v117,v29,v117
v_exp_f32 v114,v114
v_exp_f32 v115,v115
D 0,56,2048
v_exp_f32 v116,v116
v_exp_f32 v117,v117
v_fma_f32 v114,v114,v105,v105
v_fma_f32 v115,v115,v105,v105
v_fma_f32 v116,v116,v105,v105
v_fma_f32 v117,v117,v105,v105
D 0,56,3072
v_rcp_f32 v114,v114
v_rcp_f32 v115,v115
v_rcp_f32 v116,v116
v_rcp_f32 v117,v117
v_mul_f32 v26,v26,v114
v_mul_f32 v27,v27,v115
s_add_u32 m0,s57,16384
s_add_u32 s56,s55,16384
D 0,56,0
v_mul_f32 v28,v28,v116
v_mul_f32 v29,v29,v117
v_cvt_pk_bf16_f32 v122,v26,v27
v_cvt_pk_bf16_f32 v123,v28,v29
v_fma_f32 v30,-v102,v110,v30
v_fma_f32 v31,-v102,v111,v31
D 0,56,1024
v_fma_f32 v32,-v102,v112,v32
v_fma_f32 v33,-v102,v113,v33
v_mul_f32 v118,v30,v30
v_mul_f32 v119,v31,v31
v_mul_f32 v120,v32,v32
v_mul_f32 v121,v33,v33
D 0,56,2048
v_fma_f32 v118,v104,v118,v103
v_fma_f32 v119,v104,v119,v103
v_fma_f32 v120,v104,v120,v103
v_fma_f32 v121,v104,v121,v103
v_mul_f32 v118,v30,v118
v_mul_f32 v119,v31,v119
D 0,56,3072
v_mul_f32 v120,v32,v120
v_mul_f32 v121,v33,v121
v_exp_f32 v118,v118
v_exp_f32 v119,v119
v_exp_f32 v120,v120
v_exp_f32 v121,v121
s_add_u32 s55,s55,s60
s_sub_u32 s57,s84,s57
s_add_u32 s54,s54,s46
v_fma_f32 v118,v118,v105,v105
v_fma_f32 v119,v119,v105,v105
v_fma_f32 v120,v120,v105,v105
v_fma_f32 v121,v121,v105,v105
v_rcp_f32 v118,v118
v_rcp_f32 v119,v119
v_rcp_f32 v120,v120
v_rcp_f32 v121,v121
v_mul_f32 v30,v30,v118
v_mul_f32 v31,v31,v119
v_mul_f32 v32,v32,v120
v_mul_f32 v33,v33,v121
v_cvt_pk_bf16_f32 v124,v30,v31
v_cvt_pk_bf16_f32 v125,v32,v33
buffer_store_dwordx4 v[122:125],v0,s[36:39],s67 offen offset:0 sc0 sc1
s_mov_b32 s72,s69
s_mov_b32 s69,0x7fff0000
s_mov_b32 s67,0x7fff0000
s_mov_b32 s77,0x7fff0000
s_branch Lg3o_gox_%=
Lg3o_xn1_%=:
s_cmp_ge_u32 s54,256
s_cbranch_scc1 Lg3o_fin1_%=
s_add_u32 m0,s59,4096
s_nop 0
buffer_load_dword v8,s[36:39],s58 offen offset:0 sc1 lds
buffer_store_dword v4,v3,s[36:39],s71 offen offset:0 sc0 sc1
s_add_u32 m0,s59,5120
s_nop 0
buffer_load_dword v8,s[36:39],s58 offen offset:0 sc1 lds
buffer_store_dword v4,v3,s[36:39],s71 offen offset:0 sc0 sc1
s_mov_b32 s83,0
L 0
v_fma_f32 v26,-v102,v106,v26
v_fma_f32 v27,-v102,v107,v27
v_fma_f32 v28,-v102,v108,v28
v_fma_f32 v29,-v102,v109,v29
v_mul_f32 v114,v26,v26
v_mul_f32 v115,v27,v27
v_mul_f32 v116,v28,v28
v_mul_f32 v117,v29,v29
v_fma_f32 v114,v104,v114,v103
v_fma_f32 v115,v104,v115,v103
v_fma_f32 v116,v104,v116,v103
v_fma_f32 v117,v104,v117,v103
v_mul_f32 v114,v26,v114
v_mul_f32 v115,v27,v115
v_mul_f32 v116,v28,v116
v_mul_f32 v117,v29,v117
v_exp_f32 v114,v114
v_exp_f32 v115,v115
v_exp_f32 v116,v116
v_exp_f32 v117,v117
v_fma_f32 v114,v114,v105,v105
v_fma_f32 v115,v115,v105,v105
v_fma_f32 v116,v116,v105,v105
v_fma_f32 v117,v117,v105,v105
v_rcp_f32 v114,v114
v_rcp_f32 v115,v115
v_rcp_f32 v116,v116
v_rcp_f32 v117,v117
v_mul_f32 v26,v26,v114
v_mul_f32 v27,v27,v115
v_mul_f32 v28,v28,v116
v_mul_f32 v29,v29,v117
v_cvt_pk_bf16_f32 v122,v26,v27
v_cvt_pk_bf16_f32 v123,v28,v29
v_fma_f32 v30,-v102,v110,v30
v_fma_f32 v31,-v102,v111,v31
v_fma_f32 v32,-v102,v112,v32
v_fma_f32 v33,-v102,v113,v33
v_mul_f32 v118,v30,v30
v_mul_f32 v119,v31,v31
v_mul_f32 v120,v32,v32
v_mul_f32 v121,v33,v33
v_fma_f32 v118,v104,v118,v103
v_fma_f32 v119,v104,v119,v103
v_fma_f32 v120,v104,v120,v103
v_fma_f32 v121,v104,v121,v103
v_mul_f32 v118,v30,v118
v_mul_f32 v119,v31,v119
v_mul_f32 v120,v32,v120
v_mul_f32 v121,v33,v121
v_exp_f32 v118,v118
v_exp_f32 v119,v119
v_exp_f32 v120,v120
v_exp_f32 v121,v121
v_fma_f32 v118,v118,v105,v105
v_fma_f32 v119,v119,v105,v105
v_fma_f32 v120,v120,v105,v105
v_fma_f32 v121,v121,v105,v105
v_rcp_f32 v118,v118
v_rcp_f32 v119,v119
v_rcp_f32 v120,v120
v_rcp_f32 v121,v121
v_mul_f32 v30,v30,v118
v_mul_f32 v31,v31,v119
v_mul_f32 v32,v32,v120
v_mul_f32 v33,v33,v121
v_cvt_pk_bf16_f32 v124,v30,v31
v_cvt_pk_bf16_f32 v125,v32,v33
buffer_store_dwordx4 v[122:125],v0,s[36:39],s67 offen offset:0 sc0 sc1
s_mov_b32 s72,s69
s_mov_b32 s69,0x7fff0000
s_mov_b32 s67,0x7fff0000
s_mov_b32 s77,0x7fff0000
s_branch Lg3o_poll_%=
Lg3o_fin1_%=:
L 0
v_fma_f32 v26,-v102,v106,v26
v_fma_f32 v27,-v102,v107,v27
v_fma_f32 v28,-v102,v108,v28
v_fma_f32 v29,-v102,v109,v29
v_mul_f32 v114,v26,v26
v_mul_f32 v115,v27,v27
v_mul_f32 v116,v28,v28
v_mul_f32 v117,v29,v29
v_fma_f32 v114,v104,v114,v103
v_fma_f32 v115,v104,v115,v103
v_fma_f32 v116,v104,v116,v103
v_fma_f32 v117,v104,v117,v103
v_mul_f32 v114,v26,v114
v_mul_f32 v115,v27,v115
v_mul_f32 v116,v28,v116
v_mul_f32 v117,v29,v117
v_exp_f32 v114,v114
v_exp_f32 v115,v115
v_exp_f32 v116,v116
v_exp_f32 v117,v117
v_fma_f32 v114,v114,v105,v105
v_fma_f32 v115,v115,v105,v105
v_fma_f32 v116,v116,v105,v105
v_fma_f32 v117,v117,v105,v105
v_rcp_f32 v114,v114
v_rcp_f32 v115,v115
v_rcp_f32 v116,v116
v_rcp_f32 v117,v117
v_mul_f32 v26,v26,v114
v_mul_f32 v27,v27,v115
v_mul_f32 v28,v28,v116
v_mul_f32 v29,v29,v117
v_cvt_pk_bf16_f32 v122,v26,v27
v_cvt_pk_bf16_f32 v123,v28,v29
v_fma_f32 v30,-v102,v110,v30
v_fma_f32 v31,-v102,v111,v31
v_fma_f32 v32,-v102,v112,v32
v_fma_f32 v33,-v102,v113,v33
v_mul_f32 v118,v30,v30
v_mul_f32 v119,v31,v31
v_mul_f32 v120,v32,v32
v_mul_f32 v121,v33,v33
v_fma_f32 v118,v104,v118,v103
v_fma_f32 v119,v104,v119,v103
v_fma_f32 v120,v104,v120,v103
v_fma_f32 v121,v104,v121,v103
v_mul_f32 v118,v30,v118
v_mul_f32 v119,v31,v119
v_mul_f32 v120,v32,v120
v_mul_f32 v121,v33,v121
v_exp_f32 v118,v118
v_exp_f32 v119,v119
v_exp_f32 v120,v120
v_exp_f32 v121,v121
v_fma_f32 v118,v118,v105,v105
v_fma_f32 v119,v119,v105,v105
v_fma_f32 v120,v120,v105,v105
v_fma_f32 v121,v121,v105,v105
v_rcp_f32 v118,v118
v_rcp_f32 v119,v119
v_rcp_f32 v120,v120
v_rcp_f32 v121,v121
v_mul_f32 v30,v30,v118
v_mul_f32 v31,v31,v119
v_mul_f32 v32,v32,v120
v_mul_f32 v33,v33,v121
v_cvt_pk_bf16_f32 v124,v30,v31
v_cvt_pk_bf16_f32 v125,v32,v33
buffer_store_dwordx4 v[122:125],v0,s[36:39],s67 offen offset:0 sc0 sc1
s_mov_b32 s72,s69
s_mov_b32 s69,0x7fff0000
s_mov_b32 s67,0x7fff0000
s_mov_b32 s77,0x7fff0000
s_branch Lg3o_done_%=
Lg3o_done_%=:
s_waitcnt vmcnt(1)
s_barrier
buffer_store_dword v4,v3,s[36:39],s70 offen offset:0 sc0 sc1
s_waitcnt vmcnt(1)
s_barrier
buffer_store_dword v4,v3,s[36:39],s72 offen offset:0 sc0 sc1
s_waitcnt vmcnt(0) lgkmcnt(0)
s_barrier
s_endpgm
Lcode_g3o_e:
.purgem M
.purgem R
.purgem T
.purgem D
.purgem L
)"
  :: "s"(ws), "s"(sWave), "s"(sG0), "s"(sStep), "s"(sOutOff), "s"(sFoutOff), "s"(sAuxOff), "s"(sWw), "s"(sBuf), "s"(sRk), "v"(vL16), "v"(vFlag), "v"(vSnapOff), "v"(vSnapRd), "v"(vWd0), "v"(vWd1), "v"(vWr0), "v"(vWr1), "v"(vWr2), "v"(vOutS), "v"(vAux0), "v"(vAux1), "v"(vAux2), "v"(vAux3), "v"(vAn), "v"(vAe), "s"(w3) : "v0", "v1", "v2", "v3", "v4", "v5", "v6", "v7", "v8", "v9", "v10", "v11", "v12", "v13", "v14", "v15", "v16", "v17", "v18", "v19", "v20", "v21", "v22", "v23", "v24", "v25", "v26", "v27", "v28", "v29", "v30", "v31", "v32", "v33", "v34", "v35", "v36", "v37", "v38", "v39", "v40", "v41", "v42", "v43", "v44", "v45", "v46", "v47", "v48", "v49", "v50", "v51", "v52", "v53", "v54", "v55", "v56", "v57", "v58", "v59", "v60", "v61", "v62", "v63", "v64", "v65", "v66", "v67", "v68", "v69", "v70", "v71", "v72", "v73", "v74", "v75", "v76", "v77", "v78", "v79", "v80", "v81", "v82", "v83", "v84", "v85", "v86", "v87", "v88", "v89", "v90", "v91", "v92", "v93", "v94", "v95", "v96", "v97", "v98", "v99", "v100", "v101", "v102", "v103", "v104", "v105", "v106", "v107", "v108", "v109", "v110", "v111", "v112", "v113", "v114", "v115", "v116", "v117", "v118", "v119", "v120", "v121", "v122", "v123", "v124", "v125", "v126", "v127", "v128", "v129", "v130", "v131", "v132", "v133", "v134", "v135", "v136", "v137", "v138", "v139", "v140", "v141", "v142", "v143", "v144", "v145", "v146", "v147", "v148", "v149", "v150", "v151", "v152", "v153", "v154", "v155", "v156", "v157", "v158", "v159", "v160", "v161", "v162", "v163", "v164", "v165", "v166", "v167", "v168", "v169", "v170", "v171", "v172", "v173", "v174", "v175", "v176", "v177", "v178", "v179", "v180", "v181", "v182", "v183", "v184", "v185", "v186", "v187", "v188", "v189", "v190", "v191", "v192", "v193", "v194", "v195", "v196", "v197", "v198", "v199", "v200", "v201", "v202", "v203", "v204", "v205", "v206", "v207", "v208", "v209", "v210", "v211", "v212", "v213", "v214", "v215", "v216", "v217", "v218", "v219", "v220", "v221", "v222", "v223", "v224", "v225", "v226", "v227", "v228", "v229", "v230", "v231", "v232", "v233", "v234", "v235", "v236", "v237", "v238", "v239", "a0", "a1", "a2", "a3", "a4", "a5", "a6", "a7", "a8", "a9", "a10", "a11", "a12", "a13", "a14", "a15", "a16", "a17", "a18", "a19", "a20", "a21", "a22", "a23", "a24", "a25", "a26", "a27", "a28", "a29", "a30", "a31", "a32", "a33", "a34", "a35", "a36", "a37", "a38", "a39", "a40", "a41", "a42", "a43", "a44", "a45", "a46", "a47", "a48", "a49", "a50", "a51", "a52", "a53", "a54", "a55", "a56", "a57", "a58", "a59", "a60", "a61", "a62", "a63", "a64", "a65", "a66", "a67", "a68", "a69", "a70", "a71", "a72", "a73", "a74", "a75", "a76", "a77", "a78", "a79", "a80", "a81", "a82", "a83", "a84", "a85", "a86", "a87", "a88", "a89", "a90", "a91", "a92", "a93", "a94", "a95", "a96", "a97", "a98", "a99", "a100", "a101", "a102", "a103", "a104", "a105", "a106", "a107", "a108", "a109", "a110", "a111", "a112", "a113", "a114", "a115", "a116", "a117", "a118", "a119", "a120", "a121", "a122", "a123", "a124", "a125", "a126", "a127", "a128", "a129", "a130", "a131", "a132", "a133", "a134", "a135", "a136", "a137", "a138", "a139", "a140", "a141", "a142", "a143", "a144", "a145", "a146", "a147", "a148", "a149", "a150", "a151", "a152", "a153", "a154", "a155", "a156", "a157", "a158", "a159", "a160", "a161", "a162", "a163", "a164", "a165", "a166", "a167", "a168", "a169", "a170", "a171", "a172", "a173", "a174", "a175", "a176", "a177", "a178", "a179", "a180", "a181", "a182", "a183", "a184", "a185", "a186", "a187", "a188", "a189", "a190", "a191", "a192", "a193", "a194", "a195", "a196", "a197", "a198", "a199", "a200", "a201", "a202", "a203", "a204", "a205", "a206", "a207", "a208", "a209", "a210", "a211", "a212", "a213", "a214", "a215", "a216", "a217", "a218", "a219", "a220", "a221", "a222", "a223", "a224", "a225", "a226", "a227", "a228", "a229", "a230", "a231", "a232", "a233", "a234", "a235", "a236", "a237", "a238", "a239", "a240", "a241", "a242", "a243", "a244", "a245", "a246", "a247", "a248", "a249", "a250", "a251", "a252", "a253", "a254", "a255", "s36", "s37", "s38", "s39", "s40", "s41", "s42", "s43", "s44", "s45", "s46", "s47", "s48", "s49", "s50", "s51", "s52", "s53", "s54", "s55", "s56", "s57", "s58", "s59", "s60", "s61", "s62", "s63", "s64", "s65", "s66", "s67", "s68", "s69", "s70", "s71", "s72", "s73", "s74", "s75", "s76", "s77", "s78", "s79", "s80", "s81", "s82", "s83", "s84", "s85", "s86", "s87", "s88", "s89", "s90", "s91", "s92", "s93", "s94", "s95", "s96", "s97", "vcc", "scc", "memory"); }
    __builtin_unreachable();
  } else if (kind == 4) {
    const unsigned sStep = 5;
    COMMON(10, (kh * 9 + fj) * 128, 2560, 143360, 2048, 448, 0);
    const int cs = slice;
    const unsigned sOutOff = (kh ? OFF_P1 : OFF_P0) + (cs * 4 + wave) * 1024, sFoutOff = OFF_FP + (kh * 8 + cs) * 128;
    const unsigned sAuxOff = (kh ? OFF_XP : OFF_X1) + (cs * 4 + wave) * 1024, sWw = (kh * 1792 + 16 * wave) * 2048 + cs * 256, sBuf = kh * 57344;
    (void)vWr2;
    asm volatile(
R"(.macro M d,w,x
v_mfma_f32_16x16x32_bf16 v[\d:\d+3], \w, v[\x:\x+3], v[\d:\d+3]
.endm
.macro R d,a,o
ds_read_b128 v[\d:\d+3], v\a offset:\o
.endm
.macro T d,a,o
ds_read_b64_tr_b16 \d, v\a offset:\o
.endm
.macro D a,s,o
buffer_load_dwordx4 v\a, s[36:39], s\s offen offset:\o sc1 lds
.endm
.macro L c
s_waitcnt lgkmcnt(\c)
.endm
Lcode_g4_b:
s_mov_b64 s[36:37],%0
s_mov_b32 s38,111243392
s_mov_b32 s39,0x00020000
s_mov_b32 s44,%1
s_mov_b32 s45,%2
s_mov_b32 s46,%3
s_mov_b32 s47,%4
s_mov_b32 s48,%5
s_mov_b32 s49,%6
s_mov_b32 s50,%7
s_mov_b32 s51,%8
s_mov_b32 s93,%9
v_mov_b32 v0,%10
v_mov_b32 v3,%11
v_mov_b32 v5,%12
v_mov_b32 v6,%13
v_mov_b32 v10,%14
v_mov_b32 v11,%15
v_mov_b32 v12,%16
v_mov_b32 v13,%17
s_mov_b64 s[40:41],%18
s_mov_b32 s42,7340032
s_mov_b32 s43,0x00020000
s_lshl_b32 s57,s44,11
s_mov_b32 s79,111243264
buffer_load_dword v4,off,s[36:39],s79 sc1
s_add_u32 s55,s50,0
s_add_u32 m0,s57,0
s_nop 0
buffer_load_dwordx4 v10,s[40:43],s55 offen offset:0  lds
s_add_u32 m0,s57,1024
s_nop 0
buffer_load_dwordx4 v11,s[40:43],s55 offen offset:0  lds
s_add_u32 m0,s57,8064
s_nop 0
buffer_load_dwordx4 v10,s[40:43],s55 offen offset:128  lds
s_add_u32 m0,s57,9088
s_nop 0
buffer_load_dwordx4 v11,s[40:43],s55 offen offset:128  lds
s_add_u32 s55,s50,131072
s_add_u32 m0,s57,16384
s_nop 0
buffer_load_dwordx4 v10,s[40:43],s55 offen offset:0  lds
s_add_u32 m0,s57,17408
s_nop 0
buffer_load_dwordx4 v11,s[40:43],s55 offen offset:0  lds
s_add_u32 m0,s57,24448
s_nop 0
buffer_load_dwordx4 v10,s[40:43],s55 offen offset:128  lds
s_add_u32 m0,s57,25472
s_nop 0
buffer_load_dwordx4 v11,s[40:43],s55 offen offset:128  lds
s_add_u32 s55,s50,262144
s_add_u32 m0,s57,32768
s_nop 0
buffer_load_dwordx4 v10,s[40:43],s55 offen offset:0  lds
s_add_u32 m0,s57,33792
s_nop 0
buffer_load_dwordx4 v11,s[40:43],s55 offen offset:0  lds
s_add_u32 m0,s57,40832
s_nop 0
buffer_load_dwordx4 v10,s[40:43],s55 offen offset:128  lds
s_add_u32 m0,s57,41856
s_nop 0
buffer_load_dwordx4 v11,s[40:43],s55 offen offset:128  lds
s_add_u32 s55,s50,393216
s_add_u32 m0,s57,49152
s_nop 0
buffer_load_dwordx4 v10,s[40:43],s55 offen offset:0  lds
s_add_u32 m0,s57,50176
s_nop 0
buffer_load_dwordx4 v11,s[40:43],s55 offen offset:0  lds
s_add_u32 m0,s57,57216
s_nop 0
buffer_load_dwordx4 v10,s[40:43],s55 offen offset:128  lds
s_add_u32 m0,s57,58240
s_nop 0
buffer_load_dwordx4 v11,s[40:43],s55 offen offset:128  lds
s_add_u32 s55,s50,524288
s_add_u32 m0,s57,65536
s_nop 0
buffer_load_dwordx4 v10,s[40:43],s55 offen offset:0  lds
s_add_u32 m0,s57,66560
s_nop 0
buffer_load_dwordx4 v11,s[40:43],s55 offen offset:0  lds
s_add_u32 m0,s57,73600
s_nop 0
buffer_load_dwordx4 v10,s[40:43],s55 offen offset:128  lds
s_add_u32 m0,s57,74624
s_nop 0
buffer_load_dwordx4 v11,s[40:43],s55 offen offset:128  lds
s_add_u32 s55,s50,655360
s_add_u32 m0,s57,81920
s_nop 0
buffer_load_dwordx4 v10,s[40:43],s55 offen offset:0  lds
s_add_u32 m0,s57,82944
s_nop 0
buffer_load_dwordx4 v11,s[40:43],s55 offen offset:0  lds
s_add_u32 m0,s57,89984
s_nop 0
buffer_load_dwordx4 v10,s[40:43],s55 offen offset:128  lds
s_add_u32 m0,s57,91008
s_nop 0
buffer_load_dwordx4 v11,s[40:43],s55 offen offset:128  lds
s_waitcnt vmcnt(24)
s_barrier
v_readfirstlane_b32 s53,v4
s_nop 3
s_add_u32 s53,s53,1
s_cmp_eq_u32 s53,0
s_cselect_b32 s53,1,s53
v_mov_b32 v4,s53
s_waitcnt vmcnt(20)
L 0
s_barrier
s_add_u32 s55,s50,786432
s_add_u32 m0,s57,98304
s_nop 0
buffer_load_dwordx4 v10,s[40:43],s55 offen offset:0  lds
s_add_u32 m0,s57,99328
s_nop 0
buffer_load_dwordx4 v11,s[40:43],s55 offen offset:0  lds
s_add_u32 m0,s57,106368
s_nop 0
buffer_load_dwordx4 v10,s[40:43],s55 offen offset:128  lds
s_add_u32 m0,s57,107392
s_nop 0
buffer_load_dwordx4 v11,s[40:43],s55 offen offset:128  lds
v_add_u32 v18,0,v12
v_add_u32 v19,0,v13
T a[0:1],18,0
T a[2:3],18,512
T a[4:5],19,0
T a[6:7],19,512
T a[8:9],18,4096
T a[10:11],18,4608
T a[12:13],19,4096
T a[14:15],19,4608
s_waitcnt vmcnt(20)
L 0
s_barrier
s_add_u32 s55,s50,917504
s_add_u32 m0,s57,0
s_nop 0
buffer_load_dwordx4 v10,s[40:43],s55 offen offset:0  lds
s_add_u32 m0,s57,1024
s_nop 0
buffer_load_dwordx4 v11,s[40:43],s55 offen offset:0  lds
s_add_u32 m0,s57,8064
s_nop 0
buffer_load_dwordx4 v10,s[40:43],s55 offen offset:128  lds
s_add_u32 m0,s57,9088
s_nop 0
buffer_load_dwordx4 v11,s[40:43],s55 offen offset:128  lds
v_add_u32 v18,16384,v12
v_add_u32 v19,16384,v13
T a[16:17],18,0
T a[18:19],18,512
T a[20:21],19,0
T a[22:23],19,512
T a[24:25],18,4096
T a[26:27],18,4608
T a[28:29],19,4096
T a[30:31],19,4608
s_waitcnt vmcnt(20)
L 0
s_barrier
s_add_u32 s55,s50,1048576
s_add_u32 m0,s57,16384
s_nop 0
buffer_load_dwordx4 v10,s[40:43],s55 offen offset:0  lds
s_add_u32 m0,s57,17408
s_nop 0
buffer_load_dwordx4 v11,s[40:43],s55 offen offset:0  lds
s_add_u32 m0,s57,24448
s_nop 0
buffer_load_dwordx4 v10,s[40:43],s55 offen offset:128  lds
s_add_u32 m0,s57,25472
s_nop 0
buffer_load_dwordx4 v11,s[40:43],s55 offen offset:128  lds
v_add_u32 v18,32768,v12
v_add_u32 v19,32768,v13
T a[32:33],18,0
T a[34:35],18,512
T a[36:37],19,0
T a[38:39],19,512
T a[40:41],18,4096
T a[42:43],18,4608
T a[44:45],19,4096
T a[46:47],19,4608
s_waitcnt vmcnt(20)
L 0
s_barrier
s_add_u32 s55,s50,1179648
s_add_u32 m0,s57,32768
s_nop 0
buffer_load_dwordx4 v10,s[40:43],s55 offen offset:0  lds
s_add_u32 m0,s57,33792
s_nop 0
buffer_load_dwordx4 v11,s[40:43],s55 offen offset:0  lds
s_add_u32 m0,s57,40832
s_nop 0
buffer_load_dwordx4 v10,s[40:43],s55 offen offset:128  lds
s_add_u32 m0,s57,41856
s_nop 0
buffer_load_dwordx4 v11,s[40:43],s55 offen offset:128  lds
v_add_u32 v18,49152,v12
v_add_u32 v19,49152,v13
T a[48:49],18,0
T a[50:51],18,512
T a[52:53],19,0
T a[54:55],19,512
T a[56:57],18,4096
T a[58:59],18,4608
T a[60:61],19,4096
T a[62:63],19,4608
s_waitcnt vmcnt(20)
L 0
s_barrier
s_add_u32 s55,s50,1310720
s_add_u32 m0,s57,49152
s_nop 0
buffer_load_dwordx4 v10,s[40:43],s55 offen offset:0  lds
s_add_u32 m0,s57,50176
s_nop 0
buffer_load_dwordx4 v11,s[40:43],s55 offen offset:0  lds
s_add_u32 m0,s57,57216
s_nop 0
buffer_load_dwordx4 v10,s[40:43],s55 offen offset:128  lds
s_add_u32 m0,s57,58240
s_nop 0
buffer_load_dwordx4 v11,s[40:43],s55 offen offset:128  lds
v_add_u32 v18,65536,v12
v_add_u32 v19,65536,v13
T a[64:65],18,0
T a[66:67],18,512
T a[68:69],19,0
T a[70:71],19,512
T a[72:73],18,4096
T a[74:75],18,4608
T a[76:77],19,4096
T a[78:79],19,4608
s_waitcnt vmcnt(20)
L 0
s_barrier
s_add_u32 s55,s50,1441792
s_add_u32 m0,s57,65536
s_nop 0
buffer_load_dwordx4 v10,s[40:43],s55 offen offset:0  lds
s_add_u32 m0,s57,66560
s_nop 0
buffer_load_dwordx4 v11,s[40:43],s55 offen offset:0  lds
s_add_u32 m0,s57,73600
s_nop 0
buffer_load_dwordx4 v10,s[40:43],s55 offen offset:128  lds
s_add_u32 m0,s57,74624
s_nop 0
buffer_load_dwordx4 v11,s[40:43],s55 offen offset:128  lds
v_add_u32 v18,81920,v12
v_add_u32 v19,81920,v13
T a[80:81],18,0
T a[82:83],18,512
T a[84:85],19,0
T a[86:87],19,512
T a[88:89],18,4096
T a[90:91],18,4608
T a[92:93],19,4096
T a[94:95],19,4608
s_waitcnt vmcnt(20)
L 0
s_barrier
s_add_u32 s55,s50,1572864
s_add_u32 m0,s57,81920
s_nop 0
buffer_load_dwordx4 v10,s[40:43],s55 offen offset:0  lds
s_add_u32 m0,s57,82944
s_nop 0
buffer_load_dwordx4 v11,s[40:43],s55 offen offset:0  lds
s_add_u32 m0,s57,89984
s_nop 0
buffer_load_dwordx4 v10,s[40:43],s55 offen offset:128  lds
s_add_u32 m0,s57,91008
s_nop 0
buffer_load_dwordx4 v11,s[40:43],s55 offen offset:128  lds
v_add_u32 v18,98304,v12
v_add_u32 v19,98304,v13
T a[96:97],18,0
T a[98:99],18,512
T a[100:101],19,0
T a[102:103],19,512
T a[104:105],18,4096
T a[106:107],18,4608
T a[108:109],19,4096
T a[110:111],19,4608
s_waitcnt vmcnt(20)
L 0
s_barrier
s_add_u32 s55,s50,1703936
s_add_u32 m0,s57,98304
s_nop 0
buffer_load_dwordx4 v10,s[40:43],s55 offen offset:0  lds
s_add_u32 m0,s57,99328
s_nop 0
buffer_load_dwordx4 v11,s[40:43],s55 offen offset:0  lds
s_add_u32 m0,s57,106368
s_nop 0
buffer_load_dwordx4 v10,s[40:43],s55 offen offset:128  lds
s_add_u32 m0,s57,107392
s_nop 0
buffer_load_dwordx4 v11,s[40:43],s55 offen offset:128  lds
v_add_u32 v18,0,v12
v_add_u32 v19,0,v13
T a[112:113],18,0
T a[114:115],18,512
T a[116:117],19,0
T a[118:119],19,512
T a[120:121],18,4096
T a[122:123],18,4608
T a[124:125],19,4096
T a[126:127],19,4608
s_waitcnt vmcnt(20)
L 0
s_barrier
s_add_u32 s55,s50,1835008
s_add_u32 m0,s57,0
s_nop 0
buffer_load_dwordx4 v10,s[40:43],s55 offen offset:0  lds
s_add_u32 m0,s57,1024
s_nop 0
buffer_load_dwordx4 v11,s[40:43],s55 offen offset:0  lds
s_add_u32 m0,s57,8064
s_nop 0
buffer_load_dwordx4 v10,s[40:43],s55 offen offset:128  lds
s_add_u32 m0,s57,9088
s_nop 0
buffer_load_dwordx4 v11,s[40:43],s55 offen offset:128  lds
v_add_u32 v18,16384,v12
v_add_u32 v19,16384,v13
T a[128:129],18,0
T a[130:131],18,512
T a[132:133],19,0
T a[134:135],19,512
T a[136:137],18,4096
T a[138:139],18,4608
T a[140:141],19,4096
T a[142:143],19,4608
s_waitcnt vmcnt(20)
L 0
s_barrier
s_add_u32 s55,s50,1966080
s_add_u32 m0,s57,16384
s_nop 0
buffer_load_dwordx4 v10,s[40:43],s55 offen offset:0  lds
s_add_u32 m0,s57,17408
s_nop 0
buffer_load_dwordx4 v11,s[40:43],s55 offen offset:0  lds
s_add_u32 m0,s57,24448
s_nop 0
buffer_load_dwordx4 v10,s[40:43],s55 offen offset:128  lds
s_add_u32 m0,s57,25472
s_nop 0
buffer_load_dwordx4 v11,s[40:43],s55 offen offset:128  lds
v_add_u32 v18,32768,v12
v_add_u32 v19,32768,v13
T a[144:145],18,0
T a[146:147],18,512
T a[148:149],19,0
T a[150:151],19,512
T a[152:153],18,4096
T a[154:155],18,4608
T a[156:157],19,4096
T a[158:159],19,4608
s_waitcnt vmcnt(20)
L 0
s_barrier
s_add_u32 s55,s50,2097152
s_add_u32 m0,s57,32768
s_nop 0
buffer_load_dwordx4 v10,s[40:43],s55 offen offset:0  lds
s_add_u32 m0,s57,33792
s_nop 0
buffer_load_dwordx4 v11,s[40:43],s55 offen offset:0  lds
s_add_u32 m0,s57,40832
s_nop 0
buffer_load_dwordx4 v10,s[40:43],s55 offen offset:128  lds
s_add_u32 m0,s57,41856
s_nop 0
buffer_load_dwordx4 v11,s[40:43],s55 offen offset:128  lds
v_add_u32 v18,49152,v12
v_add_u32 v19,49152,v13
T a[160:161],18,0
T a[162:163],18,512
T a[164:165],19,0
T a[166:167],19,512
T a[168:169],18,4096
T a[170:171],18,4608
T a[172:173],19,4096
T a[174:175],19,4608
s_waitcnt vmcnt(20)
L 0
s_barrier
s_add_u32 s55,s50,2228224
s_add_u32 m0,s57,49152
s_nop 0
buffer_load_dwordx4 v10,s[40:43],s55 offen offset:0  lds
s_add_u32 m0,s57,50176
s_nop 0
buffer_load_dwordx4 v11,s[40:43],s55 offen offset:0  lds
s_add_u32 m0,s57,57216
s_nop 0
buffer_load_dwordx4 v10,s[40:43],s55 offen offset:128  lds
s_add_u32 m0,s57,58240
s_nop 0
buffer_load_dwordx4 v11,s[40:43],s55 offen offset:128  lds
v_add_u32 v18,65536,v12
v_add_u32 v19,65536,v13
T a[176:177],18,0
T a[178:179],18,512
T a[180:181],19,0
T a[182:183],19,512
T a[184:185],18,4096
T a[186:187],18,4608
T a[188:189],19,4096
T a[190:191],19,4608
s_waitcnt vmcnt(20)
L 0
s_barrier
s_add_u32 s55,s50,2359296
s_add_u32 m0,s57,65536
s_nop 0
buffer_load_dwordx4 v10,s[40:43],s55 offen offset:0  lds
s_add_u32 m0,s57,66560
s_nop 0
buffer_load_dwordx4 v11,s[40:43],s55 offen offset:0  lds
s_add_u32 m0,s57,73600
s_nop 0
buffer_load_dwordx4 v10,s[40:43],s55 offen offset:128  lds
s_add_u32 m0,s57,74624
s_nop 0
buffer_load_dwordx4 v11,s[40:43],s55 offen offset:128  lds
v_add_u32 v18,81920,v12
v_add_u32 v19,81920,v13
T a[192:193],18,0
T a[194:195],18,512
T a[196:197],19,0
T a[198:199],19,512
T a[200:201],18,4096
T a[202:203],18,4608
T a[204:205],19,4096
T a[206:207],19,4608
s_waitcnt vmcnt(20)
L 0
s_barrier
s_add_u32 s55,s50,2490368
s_add_u32 m0,s57,81920
s_nop 0
buffer_load_dwordx4 v10,s[40:43],s55 offen offset:0  lds
s_add_u32 m0,s57,82944
s_nop 0
buffer_load_dwordx4 v11,s[40:43],s55 offen offset:0  lds
s_add_u32 m0,s57,89984
s_nop 0
buffer_load_dwordx4 v10,s[40:43],s55 offen offset:128  lds
s_add_u32 m0,s57,91008
s_nop 0
buffer_load_dwordx4 v11,s[40:43],s55 offen offset:128  lds
v_add_u32 v18,98304,v12
v_add_u32 v19,98304,v13
T a[208:209],18,0
T a[210:211],18,512
T a[212:213],19,0
T a[214:215],19,512
T a[216:217],18,4096
T a[218:219],18,4608
T a[220:221],19,4096
T a[222:223],19,4608
s_waitcnt vmcnt(20)
L 0
s_barrier
s_add_u32 s55,s50,2621440
s_add_u32 m0,s57,98304
s_nop 0
buffer_load_dwordx4 v10,s[40:43],s55 offen offset:0  lds
s_add_u32 m0,s57,99328
)"
R"(s_nop 0
buffer_load_dwordx4 v11,s[40:43],s55 offen offset:0  lds
s_add_u32 m0,s57,106368
s_nop 0
buffer_load_dwordx4 v10,s[40:43],s55 offen offset:128  lds
s_add_u32 m0,s57,107392
s_nop 0
buffer_load_dwordx4 v11,s[40:43],s55 offen offset:128  lds
v_add_u32 v18,0,v12
v_add_u32 v19,0,v13
T a[224:225],18,0
T a[226:227],18,512
T a[228:229],19,0
T a[230:231],19,512
T a[232:233],18,4096
T a[234:235],18,4608
T a[236:237],19,4096
T a[238:239],19,4608
s_waitcnt vmcnt(20)
L 0
s_barrier
s_add_u32 s55,s50,2752512
s_add_u32 m0,s57,0
s_nop 0
buffer_load_dwordx4 v10,s[40:43],s55 offen offset:0  lds
s_add_u32 m0,s57,1024
s_nop 0
buffer_load_dwordx4 v11,s[40:43],s55 offen offset:0  lds
s_add_u32 m0,s57,8064
s_nop 0
buffer_load_dwordx4 v10,s[40:43],s55 offen offset:128  lds
s_add_u32 m0,s57,9088
s_nop 0
buffer_load_dwordx4 v11,s[40:43],s55 offen offset:128  lds
v_add_u32 v18,16384,v12
v_add_u32 v19,16384,v13
T a[240:241],18,0
T a[242:243],18,512
T a[244:245],19,0
T a[246:247],19,512
T a[248:249],18,4096
T a[250:251],18,4608
T a[252:253],19,4096
T a[254:255],19,4608
s_waitcnt vmcnt(20)
L 0
s_barrier
s_add_u32 s55,s50,2883584
s_add_u32 m0,s57,16384
s_nop 0
buffer_load_dwordx4 v10,s[40:43],s55 offen offset:0  lds
s_add_u32 m0,s57,17408
s_nop 0
buffer_load_dwordx4 v11,s[40:43],s55 offen offset:0  lds
s_add_u32 m0,s57,24448
s_nop 0
buffer_load_dwordx4 v10,s[40:43],s55 offen offset:128  lds
s_add_u32 m0,s57,25472
s_nop 0
buffer_load_dwordx4 v11,s[40:43],s55 offen offset:128  lds
v_add_u32 v18,32768,v12
v_add_u32 v19,32768,v13
T v[64:65],18,0
T v[66:67],18,512
T v[68:69],19,0
T v[70:71],19,512
T v[72:73],18,4096
T v[74:75],18,4608
T v[76:77],19,4096
T v[78:79],19,4608
s_waitcnt vmcnt(20)
L 0
s_barrier
s_add_u32 s55,s50,3014656
s_add_u32 m0,s57,32768
s_nop 0
buffer_load_dwordx4 v10,s[40:43],s55 offen offset:0  lds
s_add_u32 m0,s57,33792
s_nop 0
buffer_load_dwordx4 v11,s[40:43],s55 offen offset:0  lds
s_add_u32 m0,s57,40832
s_nop 0
buffer_load_dwordx4 v10,s[40:43],s55 offen offset:128  lds
s_add_u32 m0,s57,41856
s_nop 0
buffer_load_dwordx4 v11,s[40:43],s55 offen offset:128  lds
v_add_u32 v18,49152,v12
v_add_u32 v19,49152,v13
T v[80:81],18,0
T v[82:83],18,512
T v[84:85],19,0
T v[86:87],19,512
T v[88:89],18,4096
T v[90:91],18,4608
T v[92:93],19,4096
T v[94:95],19,4608
s_waitcnt vmcnt(20)
L 0
s_barrier
s_add_u32 s55,s50,3145728
s_add_u32 m0,s57,49152
s_nop 0
buffer_load_dwordx4 v10,s[40:43],s55 offen offset:0  lds
s_add_u32 m0,s57,50176
s_nop 0
buffer_load_dwordx4 v11,s[40:43],s55 offen offset:0  lds
s_add_u32 m0,s57,57216
s_nop 0
buffer_load_dwordx4 v10,s[40:43],s55 offen offset:128  lds
s_add_u32 m0,s57,58240
s_nop 0
buffer_load_dwordx4 v11,s[40:43],s55 offen offset:128  lds
v_add_u32 v18,65536,v12
v_add_u32 v19,65536,v13
T v[96:97],18,0
T v[98:99],18,512
T v[100:101],19,0
T v[102:103],19,512
T v[104:105],18,4096
T v[106:107],18,4608
T v[108:109],19,4096
T v[110:111],19,4608
s_waitcnt vmcnt(20)
L 0
s_barrier
s_add_u32 s55,s50,3276800
s_add_u32 m0,s57,65536
s_nop 0
buffer_load_dwordx4 v10,s[40:43],s55 offen offset:0  lds
s_add_u32 m0,s57,66560
s_nop 0
buffer_load_dwordx4 v11,s[40:43],s55 offen offset:0  lds
s_add_u32 m0,s57,73600
s_nop 0
buffer_load_dwordx4 v10,s[40:43],s55 offen offset:128  lds
s_add_u32 m0,s57,74624
s_nop 0
buffer_load_dwordx4 v11,s[40:43],s55 offen offset:128  lds
v_add_u32 v18,81920,v12
v_add_u32 v19,81920,v13
T v[112:113],18,0
T v[114:115],18,512
T v[116:117],19,0
T v[118:119],19,512
T v[120:121],18,4096
T v[122:123],18,4608
T v[124:125],19,4096
T v[126:127],19,4608
s_waitcnt vmcnt(20)
L 0
s_barrier
s_add_u32 s55,s50,3407872
s_add_u32 m0,s57,81920
s_nop 0
buffer_load_dwordx4 v10,s[40:43],s55 offen offset:0  lds
s_add_u32 m0,s57,82944
s_nop 0
buffer_load_dwordx4 v11,s[40:43],s55 offen offset:0  lds
s_add_u32 m0,s57,89984
s_nop 0
buffer_load_dwordx4 v10,s[40:43],s55 offen offset:128  lds
s_add_u32 m0,s57,91008
s_nop 0
buffer_load_dwordx4 v11,s[40:43],s55 offen offset:128  lds
v_add_u32 v18,98304,v12
v_add_u32 v19,98304,v13
T v[128:129],18,0
T v[130:131],18,512
T v[132:133],19,0
T v[134:135],19,512
T v[136:137],18,4096
T v[138:139],18,4608
T v[140:141],19,4096
T v[142:143],19,4608
s_waitcnt vmcnt(20)
L 0
s_barrier
s_add_u32 s55,s50,3538944
s_add_u32 m0,s57,98304
s_nop 0
buffer_load_dwordx4 v10,s[40:43],s55 offen offset:0  lds
s_add_u32 m0,s57,99328
s_nop 0
buffer_load_dwordx4 v11,s[40:43],s55 offen offset:0  lds
s_add_u32 m0,s57,106368
s_nop 0
buffer_load_dwordx4 v10,s[40:43],s55 offen offset:128  lds
s_add_u32 m0,s57,107392
s_nop 0
buffer_load_dwordx4 v11,s[40:43],s55 offen offset:128  lds
v_add_u32 v18,0,v12
v_add_u32 v19,0,v13
T v[144:145],18,0
T v[146:147],18,512
T v[148:149],19,0
T v[150:151],19,512
T v[152:153],18,4096
T v[154:155],18,4608
T v[156:157],19,4096
T v[158:159],19,4608
s_waitcnt vmcnt(20)
L 0
s_barrier
v_add_u32 v18,16384,v12
v_add_u32 v19,16384,v13
T v[160:161],18,0
T v[162:163],18,512
T v[164:165],19,0
T v[166:167],19,512
T v[168:169],18,4096
T v[170:171],18,4608
T v[172:173],19,4096
T v[174:175],19,4608
s_waitcnt vmcnt(16)
L 0
s_barrier
v_add_u32 v18,32768,v12
v_add_u32 v19,32768,v13
T v[176:177],18,0
T v[178:179],18,512
T v[180:181],19,0
T v[182:183],19,512
T v[184:185],18,4096
T v[186:187],18,4608
T v[188:189],19,4096
T v[190:191],19,4608
s_waitcnt vmcnt(12)
L 0
s_barrier
v_add_u32 v18,49152,v12
v_add_u32 v19,49152,v13
T v[192:193],18,0
T v[194:195],18,512
T v[196:197],19,0
T v[198:199],19,512
T v[200:201],18,4096
T v[202:203],18,4608
T v[204:205],19,4096
T v[206:207],19,4608
s_waitcnt vmcnt(8)
L 0
s_barrier
v_add_u32 v18,65536,v12
v_add_u32 v19,65536,v13
T v[208:209],18,0
T v[210:211],18,512
T v[212:213],19,0
T v[214:215],19,512
T v[216:217],18,4096
T v[218:219],18,4608
T v[220:221],19,4096
T v[222:223],19,4608
s_waitcnt vmcnt(4)
L 0
s_barrier
v_add_u32 v18,81920,v12
v_add_u32 v19,81920,v13
T v[224:225],18,0
T v[226:227],18,512
T v[228:229],19,0
T v[230:231],19,512
T v[232:233],18,4096
T v[234:235],18,4608
T v[236:237],19,4096
T v[238:239],19,4608
s_waitcnt vmcnt(0)
L 0
s_barrier
v_add_u32 v18,98304,v12
v_add_u32 v19,98304,v13
T v[240:241],18,0
T v[242:243],18,512
T v[244:245],19,0
T v[246:247],19,512
T v[248:249],18,4096
T v[250:251],18,4608
T v[252:253],19,4096
T v[254:255],19,4608
L 0
s_barrier
v_mov_b32 v4,s53
s_mov_b32 s82,0
s_mov_b32 s80,0
s_mov_b32 s54,s45
s_mul_i32 s57,s44,7168
s_mul_i32 s55,s45,114688
s_add_u32 s55,s55,s57
s_add_u32 s55,s55,40894464
s_mul_i32 s57,s44,7168
s_mul_i32 s60,s46,114688
s_mul_i32 s61,s46,2560
s_mul_i32 s62,s46,32768
s_mul_i32 s63,s46,2048
s_mul_i32 s64,s46,512
s_lshl_b32 s59,s44,8
s_add_u32 s59,s59,143360
s_mul_i32 s58,s45,2560
s_add_u32 s58,s58,109891584
s_mul_i32 s65,s45,32768
s_add_u32 s65,s65,s47
s_mul_i32 s68,s45,2048
s_add_u32 s68,s68,s48
s_mul_i32 s73,s45,512
s_add_u32 s73,s73,106954752
s_mul_i32 s74,s45,32768
s_add_u32 s74,s74,s49
s_mul_i32 s75,s45,1024
s_add_u32 s75,s75,107479040
s_mov_b32 s66,0x7fff0000
s_mov_b32 s67,0x7fff0000
s_mov_b32 s69,0x7fff0000
s_mov_b32 s76,0x7fff0000
s_mov_b32 s77,0x7fff0000
s_mov_b32 s70,0x7fff0000
s_mov_b32 s71,0x7fff0000
s_mov_b32 s72,0x7fff0000
v_mov_b32 v1,v0
v_add_u32 v2,28672,v0
s_mov_b32 s84,28672
s_mov_b32 s86,0x3a800000
s_mov_b32 s87,0x3727c5ac
s_mov_b32 s88,0xc0135761
s_mov_b32 s89,0x3d372713
s_and_b32 s85,s44,1
s_mul_i32 s85,s85,127
s_add_u32 s55,s55,s51
v_lshlrev_b32 v8,22,v0
v_and_b32 v8,0x80000000,v8
v_or_b32 v8,v8,v5
v_add_u32 v2,57344,v0
s_lshl_b32 s84,s57,1
s_add_u32 s84,s84,57344
s_add_u32 m0,s59,4096
s_nop 0
buffer_load_dword v8,s[36:39],s58 offen offset:0 sc1 lds
buffer_store_dword v4,v3,s[36:39],s71 offen offset:0 sc0 sc1
s_add_u32 m0,s59,5120
s_nop 0
buffer_load_dword v8,s[36:39],s58 offen offset:0 sc1 lds
buffer_store_dword v4,v3,s[36:39],s71 offen offset:0 sc0 sc1
s_mov_b32 s83,0
Lg4_poll_%=:
s_add_u32 s78,s83,2
s_and_b32 s78,s78,3
s_lshl_b32 s78,s78,10
s_add_u32 s78,s78,4096
s_add_u32 m0,s59,s78
s_nop 0
buffer_load_dword v8,s[36:39],s58 offen offset:0 sc1 lds
s_and_b32 s78,s83,3
s_lshl_b32 s78,s78,10
v_add_u32 v62,s78,v6
s_waitcnt vmcnt(4)
s_barrier
ds_read_b32 v7,v62 offset:4096
buffer_store_dword v4,v3,s[36:39],s70 offen offset:0 sc0 sc1
s_mov_b32 s70,s71
s_mov_b32 s71,s72
s_mov_b32 s72,0x7fff0000
s_add_u32 s83,s83,1
s_add_u32 s82,s82,1
L 0
v_cmp_eq_u32 vcc,s53,v7
s_nop 0
s_cmp_eq_u32 vcc_lo,-1
s_cbranch_scc1 Lg4_go_%=
s_cmp_lt_u32 s82,20000
s_cbranch_scc1 Lg4_poll_%=
Lg4_go_%=:
s_add_u32 m0,s57,0
s_add_u32 s56,s55,0
D 0,56,0
D 0,56,1024
D 0,56,2048
D 0,56,3072
s_add_u32 m0,s57,4096
s_add_u32 s56,s56,4096
D 0,56,0
D 0,56,1024
D 0,56,2048
s_add_u32 s58,s58,s61
s_add_u32 m0,s59,2048
s_nop 0
buffer_load_dword v8,s[36:39],s58 offen offset:0 sc1 lds
s_add_u32 m0,s57,28672
s_add_u32 s56,s55,28672
D 0,56,0
D 0,56,1024
D 0,56,2048
D 0,56,3072
s_add_u32 m0,s57,32768
s_add_u32 s56,s56,4096
D 0,56,0
D 0,56,1024
D 0,56,2048
s_add_u32 s55,s55,s60
s_sub_u32 s57,s84,s57
s_add_u32 s54,s54,s46
s_waitcnt vmcnt(7)
s_barrier
buffer_store_dword v4,v3,s[36:39],s70 offen offset:0 sc0 sc1
s_min_u32 s71,s71,s72
buffer_store_dword v4,v3,s[36:39],s71 offen offset:0 sc0 sc1
s_mov_b32 s70,0x7fff0000
s_mov_b32 s71,0x7fff0000
s_mov_b32 s72,0x7fff0000
s_cmp_eq_u32 s80,1
s_cbranch_scc1 Lg4_st1_%=
s_branch Lg4_st0_%=
Lg4_gox_%=:
s_add_u32 s58,s58,s61
s_add_u32 m0,s59,2048
s_nop 0
buffer_load_dword v8,s[36:39],s58 offen offset:0 sc1 lds
s_waitcnt vmcnt(0)
s_barrier
buffer_store_dword v4,v3,s[36:39],s70 offen offset:0 sc0 sc1
s_min_u32 s71,s71,s72
buffer_store_dword v4,v3,s[36:39],s71 offen offset:0 sc0 sc1
s_mov_b32 s70,0x7fff0000
s_mov_b32 s71,0x7fff0000
s_mov_b32 s72,0x7fff0000
s_cmp_eq_u32 s80,1
s_cbranch_scc1 Lg4_st1_%=
Lg4_st0_%=:
ds_read_b32 v7,v6 offset:2048
R 26,1,0
R 30,1,1024
R 34,1,2048
R 38,1,3072
R 42,1,4096
L 5
v_cmp_eq_u32 vcc,s53,v7
L 3
s_cmp_eq_u32 vcc_lo,-1
s_cbranch_scc1 Lg4_F0_%=
s_branch Lg4_L0_%=
Lg4_st1_%=:
ds_read_b32 v7,v6 offset:2048
R 26,2,0
R 30,2,1024
R 34,2,2048
R 38,2,3072
R 42,2,4096
L 5
v_cmp_eq_u32 vcc,s53,v7
L 3
s_cmp_eq_u32 vcc_lo,-1
s_cbranch_scc1 Lg4_F1_%=
s_branch Lg4_L1_%=
Lg4_F0_%=:
v_mfma_f32_16x16x32_bf16 v[14:17],a[4:7],v[26:29],0
s_add_u32 m0,s57,0
R 46,1,5120
v_lshlrev_b32 v62,16,v58
M 14,a[12:15],30
s_add_u32 s56,s55,0
v_add_f32 v18,v18,v62
v_and_b32 v63,0xffff0000,v58
v_mfma_f32_16x16x32_bf16 v[10:13],a[0:3],v[26:29],0
s_add_u32 s58,s58,s61
R 50,1,6144
v_add_f32 v19,v19,v63
M 10,a[8:11],30
L 3
D 0,56,0
M 10,a[16:19],34
s_mov_b32 s66,s65
R 54,1,7168
v_lshlrev_b32 v62,16,v59
M 10,a[24:27],38
s_add_u32 s65,s65,s62
v_add_f32 v20,v20,v62
v_and_b32 v63,0xffff0000,v59
M 14,a[20:23],34
s_add_u32 s54,s54,s46
R 26,1,8192
v_add_f32 v21,v21,v63
M 14,a[28:31],38
L 3
v_lshlrev_b32 v62,16,v60
v_add_f32 v22,v22,v62
M 14,a[36:39],42
R 30,1,9216
v_and_b32 v63,0xffff0000,v60
M 14,a[44:47],46
D 0,56,1024
M 10,a[32:35],42
R 34,1,10240
v_add_f32 v23,v23,v63
M 10,a[40:43],46
L 3
v_lshlrev_b32 v62,16,v61
v_add_f32 v24,v24,v62
M 10,a[48:51],50
R 38,1,11264
v_and_b32 v63,0xffff0000,v61
M 10,a[56:59],54
buffer_load_dwordx4 v[58:61],v0,s[36:39],s74 offen offset:0 sc1
M 14,a[52:55],50
)"
R"(s_add_u32 s74,s74,s62
R 42,1,12288
v_add_f32 v25,v25,v63
M 14,a[60:63],54
L 3
v_cvt_pk_bf16_f32 v18,v18,v19
v_cvt_pk_bf16_f32 v19,v20,v21
M 14,a[68:71],26
R 46,1,13312
v_cvt_pk_bf16_f32 v20,v22,v23
M 14,a[76:79],30
D 0,56,2048
M 10,a[64:67],26
R 50,1,14336
v_cvt_pk_bf16_f32 v21,v24,v25
M 10,a[72:75],30
L 3
buffer_store_dwordx4 v[18:21],v0,s[36:39],s67 offen offset:0 sc0 sc1
M 10,a[80:83],34
R 54,1,15360
M 10,a[88:91],38
M 14,a[84:87],34
R 26,1,16384
M 14,a[92:95],38
L 3
M 14,a[100:103],42
R 30,1,17408
M 14,a[108:111],46
D 0,56,3072
M 10,a[96:99],42
s_add_u32 m0,s57,4096
s_add_u32 s56,s56,4096
R 34,1,18432
M 10,a[104:107],46
L 3
M 10,a[112:115],50
R 38,1,19456
M 10,a[120:123],54
M 14,a[116:119],50
R 42,1,20480
M 14,a[124:127],54
L 3
D 0,56,0
M 14,a[132:135],26
R 46,1,21504
M 14,a[140:143],30
M 10,a[128:131],26
R 50,1,22528
M 10,a[136:139],30
L 3
M 10,a[144:147],34
R 54,1,23552
M 10,a[152:155],38
M 14,a[148:151],34
R 26,1,24576
M 14,a[156:159],38
L 3
D 0,56,1024
M 14,a[164:167],42
R 30,1,25600
M 14,a[172:175],46
M 10,a[160:163],42
R 34,1,26624
M 10,a[168:171],46
L 3
M 10,a[176:179],50
s_waitcnt vmcnt(8)
R 38,1,27648
M 10,a[184:187],54
s_barrier
M 14,a[180:183],50
R 42,1,28672
M 14,a[188:191],54
L 3
D 0,56,2048
M 14,a[196:199],26
s_add_u32 m0,s59,0
s_add_u32 s56,s55,28672
R 46,1,29696
M 14,a[204:207],30
s_add_u32 s55,s55,s60
M 10,a[192:195],26
R 50,1,30720
M 10,a[200:203],30
L 3
M 10,a[208:211],34
R 54,1,31744
M 10,a[216:219],38
M 14,a[212:215],34
R 26,1,32768
M 14,a[220:223],38
L 3
buffer_load_dword v8,s[36:39],s58 offen offset:0 sc1 lds
M 14,a[228:231],42
s_add_u32 m0,s57,28672
R 30,1,33792
M 14,a[236:239],46
D 0,56,0
M 10,a[224:227],42
R 34,1,34816
M 10,a[232:235],46
L 3
M 10,a[240:243],50
R 38,1,35840
M 10,a[248:251],54
M 14,a[244:247],50
R 42,1,36864
M 14,a[252:255],54
L 3
M 14,v[68:71],26
R 46,1,37888
M 14,v[76:79],30
D 0,56,1024
M 10,v[64:67],26
R 50,1,38912
M 10,v[72:75],30
L 3
M 10,v[80:83],34
R 54,1,39936
M 10,v[88:91],38
M 14,v[84:87],34
R 26,1,40960
M 14,v[92:95],38
L 3
M 14,v[100:103],42
R 30,1,41984
M 14,v[108:111],46
D 0,56,2048
M 10,v[96:99],42
R 34,1,43008
M 10,v[104:107],46
L 3
M 10,v[112:115],50
R 38,1,44032
M 10,v[120:123],54
M 14,v[116:119],50
R 42,1,45056
M 14,v[124:127],54
L 3
D 0,56,3072
M 14,v[132:135],26
s_add_u32 m0,s57,32768
s_add_u32 s56,s56,4096
R 46,1,46080
M 14,v[140:143],30
s_sub_u32 s57,s84,s57
M 10,v[128:131],26
R 50,1,47104
M 10,v[136:139],30
L 3
M 10,v[144:147],34
R 54,1,48128
M 10,v[152:155],38
M 14,v[148:151],34
R 26,1,49152
M 14,v[156:159],38
L 3
D 0,56,0
M 14,v[164:167],42
R 30,1,50176
M 14,v[172:175],46
M 10,v[160:163],42
R 34,1,51200
M 10,v[168:171],46
L 3
M 10,v[176:179],50
R 38,1,52224
M 10,v[184:187],54
M 14,v[180:183],50
R 42,1,53248
M 14,v[188:191],54
L 3
D 0,56,1024
M 14,v[196:199],26
R 46,1,54272
M 14,v[204:207],30
M 10,v[192:195],26
R 50,1,55296
M 10,v[200:203],30
L 3
M 10,v[208:211],34
s_waitcnt vmcnt(6)
R 54,1,56320
M 10,v[216:219],38
s_barrier
ds_read_b32 v7,v6 offset:0
M 14,v[212:215],34
R 26,2,0
M 14,v[220:223],38
L 4
buffer_store_dword v4,v3,s[36:39],s69 offen offset:0 sc0 sc1
M 14,v[228:231],42
s_mov_b32 s69,s68
s_add_u32 s68,s68,s63
R 30,2,1024
M 14,v[236:239],46
D 0,56,2048
M 10,v[224:227],42
R 34,2,2048
M 10,v[232:235],46
L 4
L 3
M 10,v[240:243],50
R 38,2,3072
v_cmp_eq_u32 vcc,s53,v7
M 10,v[248:251],54
s_cmp_eq_u32 vcc_lo,-1
M 14,v[244:247],50
R 42,2,4096
M 14,v[252:255],54
L 3
s_cbranch_scc0 Lg4_L1_%=
Lg4_F1_%=:
v_mfma_f32_16x16x32_bf16 v[22:25],a[4:7],v[26:29],0
s_add_u32 m0,s57,0
R 46,2,5120
v_lshlrev_b32 v62,16,v58
M 22,a[12:15],30
s_add_u32 s56,s55,0
v_add_f32 v10,v10,v62
v_and_b32 v63,0xffff0000,v58
v_mfma_f32_16x16x32_bf16 v[18:21],a[0:3],v[26:29],0
s_add_u32 s58,s58,s61
R 50,2,6144
v_add_f32 v11,v11,v63
M 18,a[8:11],30
L 3
D 0,56,0
M 18,a[16:19],34
s_mov_b32 s67,s65
R 54,2,7168
v_lshlrev_b32 v62,16,v59
M 18,a[24:27],38
s_add_u32 s65,s65,s62
v_add_f32 v12,v12,v62
v_and_b32 v63,0xffff0000,v59
M 22,a[20:23],34
s_add_u32 s54,s54,s46
R 26,2,8192
v_add_f32 v13,v13,v63
M 22,a[28:31],38
L 3
v_lshlrev_b32 v62,16,v60
v_add_f32 v14,v14,v62
M 22,a[36:39],42
R 30,2,9216
v_and_b32 v63,0xffff0000,v60
M 22,a[44:47],46
D 0,56,1024
M 18,a[32:35],42
R 34,2,10240
v_add_f32 v15,v15,v63
M 18,a[40:43],46
L 3
v_lshlrev_b32 v62,16,v61
v_add_f32 v16,v16,v62
M 18,a[48:51],50
R 38,2,11264
v_and_b32 v63,0xffff0000,v61
M 18,a[56:59],54
buffer_load_dwordx4 v[58:61],v0,s[36:39],s74 offen offset:0 sc1
M 22,a[52:55],50
s_add_u32 s74,s74,s62
R 42,2,12288
v_add_f32 v17,v17,v63
M 22,a[60:63],54
L 3
v_cvt_pk_bf16_f32 v10,v10,v11
v_cvt_pk_bf16_f32 v11,v12,v13
M 22,a[68:71],26
R 46,2,13312
v_cvt_pk_bf16_f32 v12,v14,v15
M 22,a[76:79],30
D 0,56,2048
M 18,a[64:67],26
R 50,2,14336
v_cvt_pk_bf16_f32 v13,v16,v17
M 18,a[72:75],30
L 3
buffer_store_dwordx4 v[10:13],v0,s[36:39],s66 offen offset:0 sc0 sc1
M 18,a[80:83],34
R 54,2,15360
M 18,a[88:91],38
M 22,a[84:87],34
R 26,2,16384
M 22,a[92:95],38
L 3
M 22,a[100:103],42
R 30,2,17408
M 22,a[108:111],46
D 0,56,3072
M 18,a[96:99],42
s_add_u32 m0,s57,4096
s_add_u32 s56,s56,4096
R 34,2,18432
M 18,a[104:107],46
L 3
M 18,a[112:115],50
R 38,2,19456
M 18,a[120:123],54
M 22,a[116:119],50
R 42,2,20480
M 22,a[124:127],54
L 3
D 0,56,0
M 22,a[132:135],26
R 46,2,21504
M 22,a[140:143],30
M 18,a[128:131],26
R 50,2,22528
M 18,a[136:139],30
L 3
M 18,a[144:147],34
R 54,2,23552
M 18,a[152:155],38
M 22,a[148:151],34
R 26,2,24576
M 22,a[156:159],38
L 3
D 0,56,1024
M 22,a[164:167],42
R 30,2,25600
M 22,a[172:175],46
M 18,a[160:163],42
R 34,2,26624
M 18,a[168:171],46
L 3
M 18,a[176:179],50
s_waitcnt vmcnt(8)
R 38,2,27648
M 18,a[184:187],54
s_barrier
M 22,a[180:183],50
R 42,2,28672
M 22,a[188:191],54
L 3
D 0,56,2048
M 22,a[196:199],26
s_add_u32 m0,s59,1024
s_add_u32 s56,s55,28672
R 46,2,29696
M 22,a[204:207],30
s_add_u32 s55,s55,s60
M 18,a[192:195],26
R 50,2,30720
M 18,a[200:203],30
L 3
M 18,a[208:211],34
R 54,2,31744
M 18,a[216:219],38
M 22,a[212:215],34
R 26,2,32768
M 22,a[220:223],38
L 3
buffer_load_dword v8,s[36:39],s58 offen offset:0 sc1 lds
M 22,a[228:231],42
s_add_u32 m0,s57,28672
R 30,2,33792
M 22,a[236:239],46
D 0,56,0
M 18,a[224:227],42
R 34,2,34816
M 18,a[232:235],46
L 3
M 18,a[240:243],50
R 38,2,35840
M 18,a[248:251],54
M 22,a[244:247],50
R 42,2,36864
M 22,a[252:255],54
L 3
M 22,v[68:71],26
R 46,2,37888
M 22,v[76:79],30
D 0,56,1024
M 18,v[64:67],26
R 50,2,38912
M 18,v[72:75],30
L 3
M 18,v[80:83],34
R 54,2,39936
M 18,v[88:91],38
M 22,v[84:87],34
R 26,2,40960
M 22,v[92:95],38
L 3
M 22,v[100:103],42
R 30,2,41984
M 22,v[108:111],46
D 0,56,2048
M 18,v[96:99],42
R 34,2,43008
M 18,v[104:107],46
L 3
M 18,v[112:115],50
R 38,2,44032
M 18,v[120:123],54
M 22,v[116:119],50
R 42,2,45056
M 22,v[124:127],54
L 3
D 0,56,3072
M 22,v[132:135],26
s_add_u32 m0,s57,32768
s_add_u32 s56,s56,4096
R 46,2,46080
M 22,v[140:143],30
s_sub_u32 s57,s84,s57
M 18,v[128:131],26
R 50,2,47104
M 18,v[136:139],30
L 3
M 18,v[144:147],34
R 54,2,48128
M 18,v[152:155],38
M 22,v[148:151],34
R 26,2,49152
M 22,v[156:159],38
L 3
D 0,56,0
M 22,v[164:167],42
R 30,2,50176
M 22,v[172:175],46
M 18,v[160:163],42
R 34,2,51200
M 18,v[168:171],46
L 3
M 18,v[176:179],50
R 38,2,52224
M 18,v[184:187],54
M 22,v[180:183],50
R 42,2,53248
M 22,v[188:191],54
L 3
D 0,56,1024
M 22,v[196:199],26
R 46,2,54272
M 22,v[204:207],30
M 18,v[192:195],26
R 50,2,55296
M 18,v[200:203],30
L 3
M 18,v[208:211],34
s_waitcnt vmcnt(6)
R 54,2,56320
M 18,v[216:219],38
s_barrier
ds_read_b32 v7,v6 offset:1024
M 22,v[212:215],34
R 26,1,0
M 22,v[220:223],38
L 4
buffer_store_dword v4,v3,s[36:39],s69 offen offset:0 sc0 sc1
M 22,v[228:231],42
s_mov_b32 s69,s68
s_add_u32 s68,s68,s63
R 30,1,1024
M 22,v[236:239],46
D 0,56,2048
M 18,v[224:227],42
R 34,1,2048
M 18,v[232:235],46
L 4
L 3
M 18,v[240:243],50
R 38,1,3072
v_cmp_eq_u32 vcc,s53,v7
M 18,v[248:251],54
s_cmp_eq_u32 vcc_lo,-1
M 22,v[244:247],50
R 42,1,4096
M 22,v[252:255],54
L 3
s_cbranch_scc1 Lg4_F0_%=
Lg4_L0_%=:
v_mfma_f32_16x16x32_bf16 v[14:17],a[4:7],v[26:29],0
s_add_u32 m0,s59,3072
R 46,1,5120
v_lshlrev_b32 v62,16,v58
M 14,a[12:15],30
s_mov_b32 s66,s65
buffer_load_dword v8,s[36:39],s58 offen offset:0 sc1 lds
v_mfma_f32_16x16x32_bf16 v[10:13],a[0:3],v[26:29],0
s_add_u32 s65,s65,s62
R 50,1,6144
v_add_f32 v18,v18,v62
M 10,a[8:11],30
L 3
v_and_b32 v63,0xffff0000,v58
v_add_f32 v19,v19,v63
M 10,a[16:19],34
R 54,1,7168
v_lshlrev_b32 v62,16,v59
M 10,a[24:27],38
v_add_f32 v20,v20,v62
v_and_b32 v63,0xffff0000,v59
M 14,a[20:23],34
R 26,1,8192
v_add_f32 v21,v21,v63
M 14,a[28:31],38
)"
R"(L 3
v_lshlrev_b32 v62,16,v60
v_add_f32 v22,v22,v62
M 14,a[36:39],42
R 30,1,9216
v_and_b32 v63,0xffff0000,v60
M 14,a[44:47],46
v_add_f32 v23,v23,v63
v_lshlrev_b32 v62,16,v61
M 10,a[32:35],42
R 34,1,10240
v_add_f32 v24,v24,v62
M 10,a[40:43],46
L 3
v_and_b32 v63,0xffff0000,v61
v_add_f32 v25,v25,v63
M 10,a[48:51],50
R 38,1,11264
v_cvt_pk_bf16_f32 v18,v18,v19
M 10,a[56:59],54
buffer_load_dwordx4 v[58:61],v0,s[36:39],s74 offen offset:0 sc1
M 14,a[52:55],50
s_add_u32 s74,s74,s62
R 42,1,12288
v_cvt_pk_bf16_f32 v19,v20,v21
M 14,a[60:63],54
L 3
v_cvt_pk_bf16_f32 v20,v22,v23
v_cvt_pk_bf16_f32 v21,v24,v25
M 14,a[68:71],26
R 46,1,13312
M 14,a[76:79],30
buffer_store_dwordx4 v[18:21],v0,s[36:39],s67 offen offset:0 sc0 sc1
M 10,a[64:67],26
R 50,1,14336
M 10,a[72:75],30
L 3
M 10,a[80:83],34
R 54,1,15360
M 10,a[88:91],38
M 14,a[84:87],34
R 26,1,16384
M 14,a[92:95],38
L 3
M 14,a[100:103],42
R 30,1,17408
M 14,a[108:111],46
M 10,a[96:99],42
R 34,1,18432
M 10,a[104:107],46
L 3
M 10,a[112:115],50
R 38,1,19456
M 10,a[120:123],54
M 14,a[116:119],50
R 42,1,20480
M 14,a[124:127],54
L 3
M 14,a[132:135],26
R 46,1,21504
M 14,a[140:143],30
M 10,a[128:131],26
R 50,1,22528
M 10,a[136:139],30
L 3
M 10,a[144:147],34
R 54,1,23552
M 10,a[152:155],38
M 14,a[148:151],34
R 26,1,24576
M 14,a[156:159],38
L 3
M 14,a[164:167],42
R 30,1,25600
M 14,a[172:175],46
M 10,a[160:163],42
R 34,1,26624
M 10,a[168:171],46
L 3
M 10,a[176:179],50
s_waitcnt vmcnt(2)
R 38,1,27648
M 10,a[184:187],54
s_barrier
ds_read_b32 v7,v6 offset:3072
M 14,a[180:183],50
R 42,1,28672
M 14,a[188:191],54
L 4
M 14,a[196:199],26
R 46,1,29696
M 14,a[204:207],30
M 10,a[192:195],26
R 50,1,30720
M 10,a[200:203],30
L 4
L 3
v_cmp_eq_u32 vcc,s53,v7
M 10,a[208:211],34
R 54,1,31744
M 10,a[216:219],38
s_cmp_eq_u32 vcc_lo,-1
s_cbranch_scc0 Lg4_X0_%=
M 14,a[212:215],34
s_add_u32 m0,s57,0
s_add_u32 s56,s55,0
s_add_u32 s58,s58,s61
R 26,1,32768
M 14,a[220:223],38
L 3
s_add_u32 s54,s54,s46
M 14,a[228:231],42
R 30,1,33792
M 14,a[236:239],46
D 0,56,0
M 10,a[224:227],42
R 34,1,34816
M 10,a[232:235],46
L 3
M 10,a[240:243],50
R 38,1,35840
M 10,a[248:251],54
D 0,56,1024
M 14,a[244:247],50
R 42,1,36864
M 14,a[252:255],54
L 3
M 14,v[68:71],26
R 46,1,37888
M 14,v[76:79],30
D 0,56,2048
M 10,v[64:67],26
R 50,1,38912
M 10,v[72:75],30
L 3
M 10,v[80:83],34
R 54,1,39936
M 10,v[88:91],38
D 0,56,3072
M 14,v[84:87],34
s_add_u32 m0,s57,4096
s_add_u32 s56,s56,4096
R 26,1,40960
M 14,v[92:95],38
L 3
M 14,v[100:103],42
R 30,1,41984
M 14,v[108:111],46
D 0,56,0
M 10,v[96:99],42
R 34,1,43008
M 10,v[104:107],46
L 3
M 10,v[112:115],50
R 38,1,44032
M 10,v[120:123],54
D 0,56,1024
M 14,v[116:119],50
R 42,1,45056
M 14,v[124:127],54
L 3
M 14,v[132:135],26
R 46,1,46080
M 14,v[140:143],30
D 0,56,2048
M 10,v[128:131],26
s_add_u32 m0,s59,0
s_add_u32 s56,s55,28672
R 50,1,47104
M 10,v[136:139],30
L 3
buffer_load_dword v8,s[36:39],s58 offen offset:0 sc1 lds
M 10,v[144:147],34
s_add_u32 m0,s57,28672
s_add_u32 s55,s55,s60
R 54,1,48128
M 10,v[152:155],38
M 14,v[148:151],34
R 26,1,49152
M 14,v[156:159],38
L 3
D 0,56,0
M 14,v[164:167],42
R 30,1,50176
M 14,v[172:175],46
D 0,56,1024
M 10,v[160:163],42
R 34,1,51200
M 10,v[168:171],46
L 3
M 10,v[176:179],50
R 38,1,52224
M 10,v[184:187],54
D 0,56,2048
M 14,v[180:183],50
R 42,1,53248
M 14,v[188:191],54
L 3
M 14,v[196:199],26
R 46,1,54272
M 14,v[204:207],30
D 0,56,3072
M 10,v[192:195],26
s_add_u32 m0,s57,32768
s_add_u32 s56,s56,4096
R 50,1,55296
M 10,v[200:203],30
L 3
s_sub_u32 s57,s84,s57
M 10,v[208:211],34
s_waitcnt vmcnt(4)
R 54,1,56320
M 10,v[216:219],38
s_barrier
ds_read_b32 v7,v6 offset:0
M 14,v[212:215],34
R 26,2,0
M 14,v[220:223],38
L 4
buffer_store_dword v4,v3,s[36:39],s69 offen offset:0 sc0 sc1
M 14,v[228:231],42
s_mov_b32 s69,s68
s_add_u32 s68,s68,s63
R 30,2,1024
M 14,v[236:239],46
D 0,56,0
M 10,v[224:227],42
R 34,2,2048
M 10,v[232:235],46
L 4
D 0,56,1024
M 10,v[240:243],50
L 3
R 38,2,3072
v_cmp_eq_u32 vcc,s53,v7
M 10,v[248:251],54
s_cmp_eq_u32 vcc_lo,-1
D 0,56,2048
M 14,v[244:247],50
R 42,2,4096
M 14,v[252:255],54
L 3
s_cbranch_scc1 Lg4_F1_%=
s_branch Lg4_L1_%=
Lg4_X0_%=:
M 14,a[212:215],34
s_add_u32 m0,s59,0
R 26,1,32768
M 14,a[220:223],38
L 3
buffer_load_dword v8,s[36:39],s58 offen offset:0 sc1 lds
M 14,a[228:231],42
R 30,1,33792
M 14,a[236:239],46
M 10,a[224:227],42
R 34,1,34816
M 10,a[232:235],46
L 3
M 10,a[240:243],50
R 38,1,35840
M 10,a[248:251],54
M 14,a[244:247],50
R 42,1,36864
M 14,a[252:255],54
L 3
M 14,v[68:71],26
R 46,1,37888
M 14,v[76:79],30
M 10,v[64:67],26
R 50,1,38912
M 10,v[72:75],30
L 3
M 10,v[80:83],34
R 54,1,39936
M 10,v[88:91],38
M 14,v[84:87],34
R 26,1,40960
M 14,v[92:95],38
L 3
M 14,v[100:103],42
R 30,1,41984
M 14,v[108:111],46
M 10,v[96:99],42
R 34,1,43008
M 10,v[104:107],46
L 3
M 10,v[112:115],50
R 38,1,44032
M 10,v[120:123],54
M 14,v[116:119],50
R 42,1,45056
M 14,v[124:127],54
L 3
M 14,v[132:135],26
R 46,1,46080
M 14,v[140:143],30
M 10,v[128:131],26
R 50,1,47104
M 10,v[136:139],30
L 3
M 10,v[144:147],34
R 54,1,48128
M 10,v[152:155],38
M 14,v[148:151],34
R 26,1,49152
M 14,v[156:159],38
L 3
M 14,v[164:167],42
R 30,1,50176
M 14,v[172:175],46
M 10,v[160:163],42
R 34,1,51200
M 10,v[168:171],46
L 3
M 10,v[176:179],50
R 38,1,52224
M 10,v[184:187],54
M 14,v[180:183],50
R 42,1,53248
M 14,v[188:191],54
L 3
M 14,v[196:199],26
R 46,1,54272
M 14,v[204:207],30
M 10,v[192:195],26
R 50,1,55296
M 10,v[200:203],30
L 3
M 10,v[208:211],34
s_waitcnt vmcnt(0)
R 54,1,56320
M 10,v[216:219],38
s_barrier
buffer_store_dword v4,v3,s[36:39],s69 offen offset:0 sc0 sc1
M 14,v[212:215],34
s_mov_b32 s69,s68
s_add_u32 s68,s68,s63
ds_read_b32 v7,v6 offset:0
M 14,v[220:223],38
L 3
M 14,v[228:231],42
M 14,v[236:239],46
M 10,v[224:227],42
M 10,v[232:235],46
L 1
M 10,v[240:243],50
L 0
v_cmp_eq_u32 vcc,s53,v7
M 10,v[248:251],54
M 14,v[244:247],50
M 14,v[252:255],54
s_mov_b32 s80,1
s_cmp_eq_u32 vcc_lo,-1
s_cbranch_scc0 Lg4_xn0_%=
s_add_u32 m0,s57,0
s_add_u32 s56,s55,0
D 0,56,0
v_lshlrev_b32 v62,16,v58
D 0,56,1024
v_add_f32 v10,v10,v62
D 0,56,2048
v_and_b32 v63,0xffff0000,v58
D 0,56,3072
v_add_f32 v11,v11,v63
s_add_u32 m0,s57,4096
s_add_u32 s56,s56,4096
D 0,56,0
v_lshlrev_b32 v62,16,v59
D 0,56,1024
v_add_f32 v12,v12,v62
D 0,56,2048
v_and_b32 v63,0xffff0000,v59
s_add_u32 m0,s57,28672
s_add_u32 s56,s55,28672
D 0,56,0
v_add_f32 v13,v13,v63
D 0,56,1024
v_lshlrev_b32 v62,16,v60
D 0,56,2048
v_add_f32 v14,v14,v62
D 0,56,3072
v_and_b32 v63,0xffff0000,v60
s_add_u32 m0,s57,32768
s_add_u32 s56,s56,4096
D 0,56,0
v_add_f32 v15,v15,v63
D 0,56,1024
v_lshlrev_b32 v62,16,v61
D 0,56,2048
v_add_f32 v16,v16,v62
s_add_u32 s55,s55,s60
s_sub_u32 s57,s84,s57
s_add_u32 s54,s54,s46
v_and_b32 v63,0xffff0000,v61
v_add_f32 v17,v17,v63
v_cvt_pk_bf16_f32 v10,v10,v11
v_cvt_pk_bf16_f32 v11,v12,v13
v_cvt_pk_bf16_f32 v12,v14,v15
v_cvt_pk_bf16_f32 v13,v16,v17
buffer_store_dwordx4 v[10:13],v0,s[36:39],s66 offen offset:0 sc0 sc1
s_mov_b32 s72,s69
s_mov_b32 s69,0x7fff0000
s_mov_b32 s66,0x7fff0000
s_mov_b32 s76,0x7fff0000
s_branch Lg4_gox_%=
Lg4_xn0_%=:
s_cmp_ge_u32 s54,256
s_cbranch_scc1 Lg4_fin0_%=
s_add_u32 m0,s59,4096
s_nop 0
buffer_load_dword v8,s[36:39],s58 offen offset:0 sc1 lds
buffer_store_dword v4,v3,s[36:39],s71 offen offset:0 sc0 sc1
s_add_u32 m0,s59,5120
s_nop 0
buffer_load_dword v8,s[36:39],s58 offen offset:0 sc1 lds
buffer_store_dword v4,v3,s[36:39],s71 offen offset:0 sc0 sc1
s_mov_b32 s83,0
v_lshlrev_b32 v62,16,v58
v_add_f32 v10,v10,v62
v_and_b32 v63,0xffff0000,v58
v_add_f32 v11,v11,v63
v_lshlrev_b32 v62,16,v59
v_add_f32 v12,v12,v62
v_and_b32 v63,0xffff0000,v59
v_add_f32 v13,v13,v63
v_lshlrev_b32 v62,16,v60
v_add_f32 v14,v14,v62
v_and_b32 v63,0xffff0000,v60
v_add_f32 v15,v15,v63
v_lshlrev_b32 v62,16,v61
v_add_f32 v16,v16,v62
v_and_b32 v63,0xffff0000,v61
v_add_f32 v17,v17,v63
v_cvt_pk_bf16_f32 v10,v10,v11
v_cvt_pk_bf16_f32 v11,v12,v13
v_cvt_pk_bf16_f32 v12,v14,v15
v_cvt_pk_bf16_f32 v13,v16,v17
buffer_store_dwordx4 v[10:13],v0,s[36:39],s66 offen offset:0 sc0 sc1
s_mov_b32 s72,s69
s_mov_b32 s69,0x7fff0000
s_mov_b32 s66,0x7fff0000
s_mov_b32 s76,0x7fff0000
s_branch Lg4_poll_%=
Lg4_fin0_%=:
v_lshlrev_b32 v62,16,v58
v_add_f32 v10,v10,v62
v_and_b32 v63,0xffff0000,v58
v_add_f32 v11,v11,v63
v_lshlrev_b32 v62,16,v59
v_add_f32 v12,v12,v62
v_and_b32 v63,0xffff0000,v59
v_add_f32 v13,v13,v63
v_lshlrev_b32 v62,16,v60
v_add_f32 v14,v14,v62
v_and_b32 v63,0xffff0000,v60
v_add_f32 v15,v15,v63
v_lshlrev_b32 v62,16,v61
v_add_f32 v16,v16,v62
v_and_b32 v63,0xffff0000,v61
v_add_f32 v17,v17,v63
v_cvt_pk_bf16_f32 v10,v10,v11
v_cvt_pk_bf16_f32 v11,v12,v13
v_cvt_pk_bf16_f32 v12,v14,v15
v_cvt_pk_bf16_f32 v13,v16,v17
buffer_store_dwordx4 v[10:13],v0,s[36:39],s66 offen offset:0 sc0 sc1
s_mov_b32 s72,s69
s_mov_b32 s69,0x7fff0000
s_mov_b32 s66,0x7fff0000
s_mov_b32 s76,0x7fff0000
s_branch Lg4_done_%=
Lg4_L1_%=:
v_mfma_f32_16x16x32_bf16 v[22:25],a[4:7],v[26:29],0
s_add_u32 m0,s59,3072
R 46,2,5120
v_lshlrev_b32 v62,16,v58
M 22,a[12:15],30
s_mov_b32 s67,s65
buffer_load_dword v8,s[36:39],s58 offen offset:0 sc1 lds
v_mfma_f32_16x16x32_bf16 v[18:21],a[0:3],v[26:29],0
s_add_u32 s65,s65,s62
R 50,2,6144
v_add_f32 v10,v10,v62
M 18,a[8:11],30
L 3
v_and_b32 v63,0xffff0000,v58
v_add_f32 v11,v11,v63
M 18,a[16:19],34
R 54,2,7168
v_lshlrev_b32 v62,16,v59
M 18,a[24:27],38
v_add_f32 v12,v12,v62
v_and_b32 v63,0xffff0000,v59
M 22,a[20:23],34
R 26,2,8192
v_add_f32 v13,v13,v63
M 22,a[28:31],38
L 3
v_lshlrev_b32 v62,16,v60
v_add_f32 v14,v14,v62
M 22,a[36:39],42
)"
R"(R 30,2,9216
v_and_b32 v63,0xffff0000,v60
M 22,a[44:47],46
v_add_f32 v15,v15,v63
v_lshlrev_b32 v62,16,v61
M 18,a[32:35],42
R 34,2,10240
v_add_f32 v16,v16,v62
M 18,a[40:43],46
L 3
v_and_b32 v63,0xffff0000,v61
v_add_f32 v17,v17,v63
M 18,a[48:51],50
R 38,2,11264
v_cvt_pk_bf16_f32 v10,v10,v11
M 18,a[56:59],54
buffer_load_dwordx4 v[58:61],v0,s[36:39],s74 offen offset:0 sc1
M 22,a[52:55],50
s_add_u32 s74,s74,s62
R 42,2,12288
v_cvt_pk_bf16_f32 v11,v12,v13
M 22,a[60:63],54
L 3
v_cvt_pk_bf16_f32 v12,v14,v15
v_cvt_pk_bf16_f32 v13,v16,v17
M 22,a[68:71],26
R 46,2,13312
M 22,a[76:79],30
buffer_store_dwordx4 v[10:13],v0,s[36:39],s66 offen offset:0 sc0 sc1
M 18,a[64:67],26
R 50,2,14336
M 18,a[72:75],30
L 3
M 18,a[80:83],34
R 54,2,15360
M 18,a[88:91],38
M 22,a[84:87],34
R 26,2,16384
M 22,a[92:95],38
L 3
M 22,a[100:103],42
R 30,2,17408
M 22,a[108:111],46
M 18,a[96:99],42
R 34,2,18432
M 18,a[104:107],46
L 3
M 18,a[112:115],50
R 38,2,19456
M 18,a[120:123],54
M 22,a[116:119],50
R 42,2,20480
M 22,a[124:127],54
L 3
M 22,a[132:135],26
R 46,2,21504
M 22,a[140:143],30
M 18,a[128:131],26
R 50,2,22528
M 18,a[136:139],30
L 3
M 18,a[144:147],34
R 54,2,23552
M 18,a[152:155],38
M 22,a[148:151],34
R 26,2,24576
M 22,a[156:159],38
L 3
M 22,a[164:167],42
R 30,2,25600
M 22,a[172:175],46
M 18,a[160:163],42
R 34,2,26624
M 18,a[168:171],46
L 3
M 18,a[176:179],50
s_waitcnt vmcnt(2)
R 38,2,27648
M 18,a[184:187],54
s_barrier
ds_read_b32 v7,v6 offset:3072
M 22,a[180:183],50
R 42,2,28672
M 22,a[188:191],54
L 4
M 22,a[196:199],26
R 46,2,29696
M 22,a[204:207],30
M 18,a[192:195],26
R 50,2,30720
M 18,a[200:203],30
L 4
L 3
v_cmp_eq_u32 vcc,s53,v7
M 18,a[208:211],34
R 54,2,31744
M 18,a[216:219],38
s_cmp_eq_u32 vcc_lo,-1
s_cbranch_scc0 Lg4_X1_%=
M 22,a[212:215],34
s_add_u32 m0,s57,0
s_add_u32 s56,s55,0
s_add_u32 s58,s58,s61
R 26,2,32768
M 22,a[220:223],38
L 3
s_add_u32 s54,s54,s46
M 22,a[228:231],42
R 30,2,33792
M 22,a[236:239],46
D 0,56,0
M 18,a[224:227],42
R 34,2,34816
M 18,a[232:235],46
L 3
M 18,a[240:243],50
R 38,2,35840
M 18,a[248:251],54
D 0,56,1024
M 22,a[244:247],50
R 42,2,36864
M 22,a[252:255],54
L 3
M 22,v[68:71],26
R 46,2,37888
M 22,v[76:79],30
D 0,56,2048
M 18,v[64:67],26
R 50,2,38912
M 18,v[72:75],30
L 3
M 18,v[80:83],34
R 54,2,39936
M 18,v[88:91],38
D 0,56,3072
M 22,v[84:87],34
s_add_u32 m0,s57,4096
s_add_u32 s56,s56,4096
R 26,2,40960
M 22,v[92:95],38
L 3
M 22,v[100:103],42
R 30,2,41984
M 22,v[108:111],46
D 0,56,0
M 18,v[96:99],42
R 34,2,43008
M 18,v[104:107],46
L 3
M 18,v[112:115],50
R 38,2,44032
M 18,v[120:123],54
D 0,56,1024
M 22,v[116:119],50
R 42,2,45056
M 22,v[124:127],54
L 3
M 22,v[132:135],26
R 46,2,46080
M 22,v[140:143],30
D 0,56,2048
M 18,v[128:131],26
s_add_u32 m0,s59,1024
s_add_u32 s56,s55,28672
R 50,2,47104
M 18,v[136:139],30
L 3
buffer_load_dword v8,s[36:39],s58 offen offset:0 sc1 lds
M 18,v[144:147],34
s_add_u32 m0,s57,28672
s_add_u32 s55,s55,s60
R 54,2,48128
M 18,v[152:155],38
M 22,v[148:151],34
R 26,2,49152
M 22,v[156:159],38
L 3
D 0,56,0
M 22,v[164:167],42
R 30,2,50176
M 22,v[172:175],46
D 0,56,1024
M 18,v[160:163],42
R 34,2,51200
M 18,v[168:171],46
L 3
M 18,v[176:179],50
R 38,2,52224
M 18,v[184:187],54
D 0,56,2048
M 22,v[180:183],50
R 42,2,53248
M 22,v[188:191],54
L 3
M 22,v[196:199],26
R 46,2,54272
M 22,v[204:207],30
D 0,56,3072
M 18,v[192:195],26
s_add_u32 m0,s57,32768
s_add_u32 s56,s56,4096
R 50,2,55296
M 18,v[200:203],30
L 3
s_sub_u32 s57,s84,s57
M 18,v[208:211],34
s_waitcnt vmcnt(4)
R 54,2,56320
M 18,v[216:219],38
s_barrier
ds_read_b32 v7,v6 offset:1024
M 22,v[212:215],34
R 26,1,0
M 22,v[220:223],38
L 4
buffer_store_dword v4,v3,s[36:39],s69 offen offset:0 sc0 sc1
M 22,v[228:231],42
s_mov_b32 s69,s68
s_add_u32 s68,s68,s63
R 30,1,1024
M 22,v[236:239],46
D 0,56,0
M 18,v[224:227],42
R 34,1,2048
M 18,v[232:235],46
L 4
D 0,56,1024
M 18,v[240:243],50
L 3
R 38,1,3072
v_cmp_eq_u32 vcc,s53,v7
M 18,v[248:251],54
s_cmp_eq_u32 vcc_lo,-1
D 0,56,2048
M 22,v[244:247],50
R 42,1,4096
M 22,v[252:255],54
L 3
s_cbranch_scc1 Lg4_F0_%=
s_branch Lg4_L0_%=
Lg4_X1_%=:
M 22,a[212:215],34
s_add_u32 m0,s59,1024
R 26,2,32768
M 22,a[220:223],38
L 3
buffer_load_dword v8,s[36:39],s58 offen offset:0 sc1 lds
M 22,a[228:231],42
R 30,2,33792
M 22,a[236:239],46
M 18,a[224:227],42
R 34,2,34816
M 18,a[232:235],46
L 3
M 18,a[240:243],50
R 38,2,35840
M 18,a[248:251],54
M 22,a[244:247],50
R 42,2,36864
M 22,a[252:255],54
L 3
M 22,v[68:71],26
R 46,2,37888
M 22,v[76:79],30
M 18,v[64:67],26
R 50,2,38912
M 18,v[72:75],30
L 3
M 18,v[80:83],34
R 54,2,39936
M 18,v[88:91],38
M 22,v[84:87],34
R 26,2,40960
M 22,v[92:95],38
L 3
M 22,v[100:103],42
R 30,2,41984
M 22,v[108:111],46
M 18,v[96:99],42
R 34,2,43008
M 18,v[104:107],46
L 3
M 18,v[112:115],50
R 38,2,44032
M 18,v[120:123],54
M 22,v[116:119],50
R 42,2,45056
M 22,v[124:127],54
L 3
M 22,v[132:135],26
R 46,2,46080
M 22,v[140:143],30
M 18,v[128:131],26
R 50,2,47104
M 18,v[136:139],30
L 3
M 18,v[144:147],34
R 54,2,48128
M 18,v[152:155],38
M 22,v[148:151],34
R 26,2,49152
M 22,v[156:159],38
L 3
M 22,v[164:167],42
R 30,2,50176
M 22,v[172:175],46
M 18,v[160:163],42
R 34,2,51200
M 18,v[168:171],46
L 3
M 18,v[176:179],50
R 38,2,52224
M 18,v[184:187],54
M 22,v[180:183],50
R 42,2,53248
M 22,v[188:191],54
L 3
M 22,v[196:199],26
R 46,2,54272
M 22,v[204:207],30
M 18,v[192:195],26
R 50,2,55296
M 18,v[200:203],30
L 3
M 18,v[208:211],34
s_waitcnt vmcnt(0)
R 54,2,56320
M 18,v[216:219],38
s_barrier
buffer_store_dword v4,v3,s[36:39],s69 offen offset:0 sc0 sc1
M 22,v[212:215],34
s_mov_b32 s69,s68
s_add_u32 s68,s68,s63
ds_read_b32 v7,v6 offset:1024
M 22,v[220:223],38
L 3
M 22,v[228:231],42
M 22,v[236:239],46
M 18,v[224:227],42
M 18,v[232:235],46
L 1
M 18,v[240:243],50
L 0
v_cmp_eq_u32 vcc,s53,v7
M 18,v[248:251],54
M 22,v[244:247],50
M 22,v[252:255],54
s_mov_b32 s80,0
s_cmp_eq_u32 vcc_lo,-1
s_cbranch_scc0 Lg4_xn1_%=
s_add_u32 m0,s57,0
s_add_u32 s56,s55,0
D 0,56,0
v_lshlrev_b32 v62,16,v58
D 0,56,1024
v_add_f32 v18,v18,v62
D 0,56,2048
v_and_b32 v63,0xffff0000,v58
D 0,56,3072
v_add_f32 v19,v19,v63
s_add_u32 m0,s57,4096
s_add_u32 s56,s56,4096
D 0,56,0
v_lshlrev_b32 v62,16,v59
D 0,56,1024
v_add_f32 v20,v20,v62
D 0,56,2048
v_and_b32 v63,0xffff0000,v59
s_add_u32 m0,s57,28672
s_add_u32 s56,s55,28672
D 0,56,0
v_add_f32 v21,v21,v63
D 0,56,1024
v_lshlrev_b32 v62,16,v60
D 0,56,2048
v_add_f32 v22,v22,v62
D 0,56,3072
v_and_b32 v63,0xffff0000,v60
s_add_u32 m0,s57,32768
s_add_u32 s56,s56,4096
D 0,56,0
v_add_f32 v23,v23,v63
D 0,56,1024
v_lshlrev_b32 v62,16,v61
D 0,56,2048
v_add_f32 v24,v24,v62
s_add_u32 s55,s55,s60
s_sub_u32 s57,s84,s57
s_add_u32 s54,s54,s46
v_and_b32 v63,0xffff0000,v61
v_add_f32 v25,v25,v63
v_cvt_pk_bf16_f32 v18,v18,v19
v_cvt_pk_bf16_f32 v19,v20,v21
v_cvt_pk_bf16_f32 v20,v22,v23
v_cvt_pk_bf16_f32 v21,v24,v25
buffer_store_dwordx4 v[18:21],v0,s[36:39],s67 offen offset:0 sc0 sc1
s_mov_b32 s72,s69
s_mov_b32 s69,0x7fff0000
s_mov_b32 s67,0x7fff0000
s_mov_b32 s77,0x7fff0000
s_branch Lg4_gox_%=
Lg4_xn1_%=:
s_cmp_ge_u32 s54,256
s_cbranch_scc1 Lg4_fin1_%=
s_add_u32 m0,s59,4096
s_nop 0
buffer_load_dword v8,s[36:39],s58 offen offset:0 sc1 lds
buffer_store_dword v4,v3,s[36:39],s71 offen offset:0 sc0 sc1
s_add_u32 m0,s59,5120
s_nop 0
buffer_load_dword v8,s[36:39],s58 offen offset:0 sc1 lds
buffer_store_dword v4,v3,s[36:39],s71 offen offset:0 sc0 sc1
s_mov_b32 s83,0
v_lshlrev_b32 v62,16,v58
v_add_f32 v18,v18,v62
v_and_b32 v63,0xffff0000,v58
v_add_f32 v19,v19,v63
v_lshlrev_b32 v62,16,v59
v_add_f32 v20,v20,v62
v_and_b32 v63,0xffff0000,v59
v_add_f32 v21,v21,v63
v_lshlrev_b32 v62,16,v60
v_add_f32 v22,v22,v62
v_and_b32 v63,0xffff0000,v60
v_add_f32 v23,v23,v63
v_lshlrev_b32 v62,16,v61
v_add_f32 v24,v24,v62
v_and_b32 v63,0xffff0000,v61
v_add_f32 v25,v25,v63
v_cvt_pk_bf16_f32 v18,v18,v19
v_cvt_pk_bf16_f32 v19,v20,v21
v_cvt_pk_bf16_f32 v20,v22,v23
v_cvt_pk_bf16_f32 v21,v24,v25
buffer_store_dwordx4 v[18:21],v0,s[36:39],s67 offen offset:0 sc0 sc1
s_mov_b32 s72,s69
s_mov_b32 s69,0x7fff0000
s_mov_b32 s67,0x7fff0000
s_mov_b32 s77,0x7fff0000
s_branch Lg4_poll_%=
Lg4_fin1_%=:
v_lshlrev_b32 v62,16,v58
v_add_f32 v18,v18,v62
v_and_b32 v63,0xffff0000,v58
v_add_f32 v19,v19,v63
v_lshlrev_b32 v62,16,v59
v_add_f32 v20,v20,v62
v_and_b32 v63,0xffff0000,v59
v_add_f32 v21,v21,v63
v_lshlrev_b32 v62,16,v60
v_add_f32 v22,v22,v62
v_and_b32 v63,0xffff0000,v60
v_add_f32 v23,v23,v63
v_lshlrev_b32 v62,16,v61
v_add_f32 v24,v24,v62
v_and_b32 v63,0xffff0000,v61
v_add_f32 v25,v25,v63
v_cvt_pk_bf16_f32 v18,v18,v19
v_cvt_pk_bf16_f32 v19,v20,v21
v_cvt_pk_bf16_f32 v20,v22,v23
v_cvt_pk_bf16_f32 v21,v24,v25
buffer_store_dwordx4 v[18:21],v0,s[36:39],s67 offen offset:0 sc0 sc1
s_mov_b32 s72,s69
s_mov_b32 s69,0x7fff0000
s_mov_b32 s67,0x7fff0000
s_mov_b32 s77,0x7fff0000
s_branch Lg4_done_%=
Lg4_done_%=:
s_nop 0
s_barrier
buffer_store_dword v4,v3,s[36:39],s70 offen offset:0 sc0 sc1
s_waitcnt vmcnt(1)
s_barrier
buffer_store_dword v4,v3,s[36:39],s72 offen offset:0 sc0 sc1
s_waitcnt vmcnt(0) lgkmcnt(0)
s_barrier
s_endpgm
Lcode_g4_e:
.purgem M
.purgem R
.purgem T
.purgem D
.purgem L
)"
  :: "s"(ws), "s"(sWave), "s"(sG0), "s"(sStep), "s"(sOutOff), "s"(sFoutOff), "s"(sAuxOff), "s"(sWw), "s"(sBuf), "s"(sRk), "v"(vL16), "v"(vFlag), "v"(vSnapOff), "v"(vSnapRd), "v"(vWd0), "v"(vWd1), "v"(vWr0), "v"(vWr1), "s"(w4) : "v0", "v1", "v2", "v3", "v4", "v5", "v6", "v7", "v8", "v9", "v10", "v11", "v12", "v13", "v14", "v15", "v16", "v17", "v18", "v19", "v20", "v21", "v22", "v23", "v24", "v25", "v26", "v27", "v28", "v29", "v30", "v31", "v32", "v33", "v34", "v35", "v36", "v37", "v38", "v39", "v40", "v41", "v42", "v43", "v44", "v45", "v46", "v47", "v48", "v49", "v50", "v51", "v52", "v53", "v54", "v55", "v56", "v57", "v58", "v59", "v60", "v61", "v62", "v63", "v64", "v65", "v66", "v67", "v68", "v69", "v70", "v71", "v72", "v73", "v74", "v75", "v76", "v77", "v78", "v79", "v80", "v81", "v82", "v83", "v84", "v85", "v86", "v87", "v88", "v89", "v90", "v91", "v92", "v93", "v94", "v95", "v96", "v97", "v98", "v99", "v100", "v101", "v102", "v103", "v104", "v105", "v106", "v107", "v108", "v109", "v110", "v111", "v112", "v113", "v114", "v115", "v116", "v117", "v118", "v119", "v120", "v121", "v122", "v123", "v124", "v125", "v126", "v127", "v128", "v129", "v130", "v131", "v132", "v133", "v134", "v135", "v136", "v137", "v138", "v139", "v140", "v141", "v142", "v143", "v144", "v145", "v146", "v147", "v148", "v149", "v150", "v151", "v152", "v153", "v154", "v155", "v156", "v157", "v158", "v159", "v160", "v161", "v162", "v163", "v164", "v165", "v166", "v167", "v168", "v169", "v170", "v171", "v172", "v173", "v174", "v175", "v176", "v177", "v178", "v179", "v180", "v181", "v182", "v183", "v184", "v185", "v186", "v187", "v188", "v189", "v190", "v191", "v192", "v193", "v194", "v195", "v196", "v197", "v198", "v199", "v200", "v201", "v202", "v203", "v204", "v205", "v206", "v207", "v208", "v209", "v210", "v211", "v212", "v213", "v214", "v215", "v216", "v217", "v218", "v219", "v220", "v221", "v222", "v223", "v224", "v225", "v226", "v227", "v228", "v229", "v230", "v231", "v232", "v233", "v234", "v235", "v236", "v237", "v238", "v239", "a0", "a1", "a2", "a3", "a4", "a5", "a6", "a7", "a8", "a9", "a10", "a11", "a12", "a13", "a14", "a15", "a16", "a17", "a18", "a19", "a20", "a21", "a22", "a23", "a24", "a25", "a26", "a27", "a28", "a29", "a30", "a31", "a32", "a33", "a34", "a35", "a36", "a37", "a38", "a39", "a40", "a41", "a42", "a43", "a44", "a45", "a46", "a47", "a48", "a49", "a50", "a51", "a52", "a53", "a54", "a55", "a56", "a57", "a58", "a59", "a60", "a61", "a62", "a63", "a64", "a65", "a66", "a67", "a68", "a69", "a70", "a71", "a72", "a73", "a74", "a75", "a76", "a77", "a78", "a79", "a80", "a81", "a82", "a83", "a84", "a85", "a86", "a87", "a88", "a89", "a90", "a91", "a92", "a93", "a94", "a95", "a96", "a97", "a98", "a99", "a100", "a101", "a102", "a103", "a104", "a105", "a106", "a107", "a108", "a109", "a110", "a111", "a112", "a113", "a114", "a115", "a116", "a117", "a118", "a119", "a120", "a121", "a122", "a123", "a124", "a125", "a126", "a127", "a128", "a129", "a130", "a131", "a132", "a133", "a134", "a135", "a136", "a137", "a138", "a139", "a140", "a141", "a142", "a143", "a144", "a145", "a146", "a147", "a148", "a149", "a150", "a151", "a152", "a153", "a154", "a155", "a156", "a157", "a158", "a159", "a160", "a161", "a162", "a163", "a164", "a165", "a166", "a167", "a168", "a169", "a170", "a171", "a172", "a173", "a174", "a175", "a176", "a177", "a178", "a179", "a180", "a181", "a182", "a183", "a184", "a185", "a186", "a187", "a188", "a189", "a190", "a191", "a192", "a193", "a194", "a195", "a196", "a197", "a198", "a199", "a200", "a201", "a202", "a203", "a204", "a205", "a206", "a207", "a208", "a209", "a210", "a211", "a212", "a213", "a214", "a215", "a216", "a217", "a218", "a219", "a220", "a221", "a222", "a223", "a224", "a225", "a226", "a227", "a228", "a229", "a230", "a231", "a232", "a233", "a234", "a235", "a236", "a237", "a238", "a239", "a240", "a241", "a242", "a243", "a244", "a245", "a246", "a247", "a248", "a249", "a250", "a251", "a252", "a253", "a254", "a255", "s36", "s37", "s38", "s39", "s40", "s41", "s42", "s43", "s44", "s45", "s46", "s47", "s48", "s49", "s50", "s51", "s52", "s53", "s54", "s55", "s56", "s57", "s58", "s59", "s60", "s61", "s62", "s63", "s64", "s65", "s66", "s67", "s68", "s69", "s70", "s71", "s72", "s73", "s74", "s75", "s76", "s77", "s78", "s79", "s80", "s81", "s82", "s83", "s84", "s85", "s86", "s87", "s88", "s89", "s90", "s91", "s92", "s93", "s94", "s95", "s96", "s97", "vcc", "scc", "memory");
    __builtin_unreachable();
  } else {
    if (kind != 6) {
      const unsigned sStep = 4;
      COMMON(kind == 1 ? 8 : 16, kind == 1 ? fj * 128 : fj < 8 ? 1024 + fj * 128 : (fj & 2) * 1024 + (fj - 8) * 128, 1024, 131072, 2048, 256, kind == 1 ? 0 : 32768);
      const int sr = l15 - ((wave & 1) ? -1 : 1) * (1 + (wave >> 1)), nx = (slice >> 1) & 1;
      const unsigned vAn = kind == 1 ? HU + vL16 : (sr & 16) ? 1024 + lg * 64 + (sr & 3) * 16 : (lg * 16 + sr) * 16;
      const unsigned vAe = kind == 1 ? ((l15 >> 2) == (nx ? 0 : 3) ? (nx ? 0 : 2 * HU) + 1024 + lg * 64 + (l15 & 3) * 16 : OOB) : (sr & 16) ? OOB : vAn;
      const unsigned vOutS = 0, vAux0 = l15 * 16, vAux1 = 139264 + (l15 * 2 + (lg & 1)) * 16 + wave * 4 + (lg >> 1) * 512, vAux2 = 139264 + (l15 * 2 + (lg & 1)) * 16;
      const unsigned vAux3 = (wave == 0 && lg < 2) ? slice * 128 + l15 * 8 + lg * 4 : OOB;
      const unsigned sOutOff = kind == 1 ? OFF_HS - HU + (slice * 4 + wave) * 1280 : OFF_X1 + (slice * 4 + wave) * 1024, sFoutOff = (kind == 1 ? OFF_FH : OFF_FX1) + slice * 128;
      const unsigned sAuxOff = OFF_XP + (slice * 4 + wave) * 1024, sBuf = kind, sWw = 16 * wave * 2048 + slice * 256;
      const u16* w12 = kind == 1 ? w1 : w2;
      unsigned epOut;
      asm volatile(
R"(.macro M d,w,x
v_mfma_f32_16x16x32_bf16 v[\d:\d+3], \w, v[\x:\x+3], v[\d:\d+3]
.endm
.macro R d,a,o
ds_read_b128 v[\d:\d+3], v\a offset:\o
.endm
.macro T d,a,o
ds_read_b64_tr_b16 \d, v\a offset:\o
.endm
.macro D a,s,o
buffer_load_dwordx4 v\a, s[36:39], s\s offen offset:\o sc1 lds
.endm
.macro L c
s_waitcnt lgkmcnt(\c)
.endm
Lcode_g12_b:
s_cmp_eq_u32 %9,2
s_cbranch_scc1 Lg2_begin_%=
s_mov_b64 s[36:37],%1
s_mov_b32 s38,111243392
s_mov_b32 s39,0x00020000
s_mov_b32 s44,%2
s_mov_b32 s45,%3
s_mov_b32 s46,%4
s_mov_b32 s47,%5
s_mov_b32 s48,%6
s_mov_b32 s49,%7
s_mov_b32 s50,%8
s_mov_b32 s51,%9
s_mov_b32 s93,%10
v_mov_b32 v0,%11
v_mov_b32 v3,%12
v_mov_b32 v5,%13
v_mov_b32 v6,%14
v_mov_b32 v18,%15
v_mov_b32 v19,%16
v_mov_b32 v20,%17
v_mov_b32 v21,%18
v_mov_b32 v22,%19
v_mov_b32 v10,%20
v_mov_b32 v11,%21
v_mov_b32 v12,%22
v_mov_b32 v13,%23
v_mov_b32 v14,%24
v_mov_b32 v15,%25
v_mov_b32 v16,%26
s_mov_b64 s[40:41],%27
s_mov_b32 s42,2097152
s_mov_b32 s43,0x00020000
s_lshl_b32 s57,s44,11
s_mov_b32 s79,111243264
buffer_load_dword v4,off,s[36:39],s79 sc1
s_add_u32 s55,s50,0
s_add_u32 m0,s57,0
s_nop 0
buffer_load_dwordx4 v18,s[40:43],s55 offen offset:0  lds
s_add_u32 m0,s57,1024
s_nop 0
buffer_load_dwordx4 v19,s[40:43],s55 offen offset:0  lds
s_add_u32 m0,s57,8064
s_nop 0
buffer_load_dwordx4 v18,s[40:43],s55 offen offset:128  lds
s_add_u32 m0,s57,9088
s_nop 0
buffer_load_dwordx4 v19,s[40:43],s55 offen offset:128  lds
s_add_u32 s55,s50,131072
s_add_u32 m0,s57,16384
s_nop 0
buffer_load_dwordx4 v18,s[40:43],s55 offen offset:0  lds
s_add_u32 m0,s57,17408
s_nop 0
buffer_load_dwordx4 v19,s[40:43],s55 offen offset:0  lds
s_add_u32 m0,s57,24448
s_nop 0
buffer_load_dwordx4 v18,s[40:43],s55 offen offset:128  lds
s_add_u32 m0,s57,25472
s_nop 0
buffer_load_dwordx4 v19,s[40:43],s55 offen offset:128  lds
s_add_u32 s55,s50,262144
s_add_u32 m0,s57,32768
s_nop 0
buffer_load_dwordx4 v18,s[40:43],s55 offen offset:0  lds
s_add_u32 m0,s57,33792
s_nop 0
buffer_load_dwordx4 v19,s[40:43],s55 offen offset:0  lds
s_add_u32 m0,s57,40832
s_nop 0
buffer_load_dwordx4 v18,s[40:43],s55 offen offset:128  lds
s_add_u32 m0,s57,41856
s_nop 0
buffer_load_dwordx4 v19,s[40:43],s55 offen offset:128  lds
s_add_u32 s55,s50,393216
s_add_u32 m0,s57,49152
s_nop 0
buffer_load_dwordx4 v18,s[40:43],s55 offen offset:0  lds
s_add_u32 m0,s57,50176
s_nop 0
buffer_load_dwordx4 v19,s[40:43],s55 offen offset:0  lds
s_add_u32 m0,s57,57216
s_nop 0
buffer_load_dwordx4 v18,s[40:43],s55 offen offset:128  lds
s_add_u32 m0,s57,58240
s_nop 0
buffer_load_dwordx4 v19,s[40:43],s55 offen offset:128  lds
s_add_u32 s55,s50,524288
s_add_u32 m0,s57,65536
s_nop 0
buffer_load_dwordx4 v18,s[40:43],s55 offen offset:0  lds
s_add_u32 m0,s57,66560
s_nop 0
buffer_load_dwordx4 v19,s[40:43],s55 offen offset:0  lds
s_add_u32 m0,s57,73600
s_nop 0
buffer_load_dwordx4 v18,s[40:43],s55 offen offset:128  lds
s_add_u32 m0,s57,74624
s_nop 0
buffer_load_dwordx4 v19,s[40:43],s55 offen offset:128  lds
s_add_u32 s55,s50,655360
s_add_u32 m0,s57,81920
s_nop 0
buffer_load_dwordx4 v18,s[40:43],s55 offen offset:0  lds
s_add_u32 m0,s57,82944
s_nop 0
buffer_load_dwordx4 v19,s[40:43],s55 offen offset:0  lds
s_add_u32 m0,s57,89984
s_nop 0
buffer_load_dwordx4 v18,s[40:43],s55 offen offset:128  lds
s_add_u32 m0,s57,91008
s_nop 0
buffer_load_dwordx4 v19,s[40:43],s55 offen offset:128  lds
s_waitcnt vmcnt(24)
s_barrier
v_readfirstlane_b32 s53,v4
s_nop 3
s_add_u32 s53,s53,1
s_cmp_eq_u32 s53,0
s_cselect_b32 s53,1,s53
v_mov_b32 v4,s53
s_waitcnt vmcnt(20)
L 0
s_barrier
s_add_u32 s55,s50,786432
s_add_u32 m0,s57,98304
s_nop 0
buffer_load_dwordx4 v18,s[40:43],s55 offen offset:0  lds
s_add_u32 m0,s57,99328
s_nop 0
buffer_load_dwordx4 v19,s[40:43],s55 offen offset:0  lds
s_add_u32 m0,s57,106368
s_nop 0
buffer_load_dwordx4 v18,s[40:43],s55 offen offset:128  lds
s_add_u32 m0,s57,107392
s_nop 0
buffer_load_dwordx4 v19,s[40:43],s55 offen offset:128  lds
v_add_u32 v26,0,v20
v_add_u32 v27,0,v21
T a[0:1],26,0
T a[2:3],26,512
T a[4:5],27,0
T a[6:7],27,512
T a[8:9],26,4096
T a[10:11],26,4608
T a[12:13],27,4096
T a[14:15],27,4608
s_waitcnt vmcnt(20)
L 0
s_barrier
s_add_u32 s55,s50,917504
s_add_u32 m0,s57,0
s_nop 0
buffer_load_dwordx4 v18,s[40:43],s55 offen offset:0  lds
s_add_u32 m0,s57,1024
s_nop 0
buffer_load_dwordx4 v19,s[40:43],s55 offen offset:0  lds
s_add_u32 m0,s57,8064
s_nop 0
buffer_load_dwordx4 v18,s[40:43],s55 offen offset:128  lds
s_add_u32 m0,s57,9088
s_nop 0
buffer_load_dwordx4 v19,s[40:43],s55 offen offset:128  lds
v_add_u32 v26,16384,v20
v_add_u32 v27,16384,v21
T a[16:17],26,0
T a[18:19],26,512
T a[20:21],27,0
T a[22:23],27,512
T a[24:25],26,4096
T a[26:27],26,4608
T a[28:29],27,4096
T a[30:31],27,4608
s_waitcnt vmcnt(20)
L 0
s_barrier
s_add_u32 s55,s50,1048576
s_add_u32 m0,s57,16384
s_nop 0
buffer_load_dwordx4 v18,s[40:43],s55 offen offset:0  lds
s_add_u32 m0,s57,17408
s_nop 0
buffer_load_dwordx4 v19,s[40:43],s55 offen offset:0  lds
s_add_u32 m0,s57,24448
s_nop 0
buffer_load_dwordx4 v18,s[40:43],s55 offen offset:128  lds
s_add_u32 m0,s57,25472
s_nop 0
buffer_load_dwordx4 v19,s[40:43],s55 offen offset:128  lds
v_add_u32 v26,32768,v20
v_add_u32 v27,32768,v21
T a[32:33],26,0
T a[34:35],26,512
T a[36:37],27,0
T a[38:39],27,512
T a[40:41],26,4096
T a[42:43],26,4608
T a[44:45],27,4096
T a[46:47],27,4608
s_waitcnt vmcnt(20)
L 0
s_barrier
s_add_u32 s55,s50,1179648
s_add_u32 m0,s57,32768
s_nop 0
buffer_load_dwordx4 v18,s[40:43],s55 offen offset:0  lds
s_add_u32 m0,s57,33792
s_nop 0
buffer_load_dwordx4 v19,s[40:43],s55 offen offset:0  lds
s_add_u32 m0,s57,40832
s_nop 0
buffer_load_dwordx4 v18,s[40:43],s55 offen offset:128  lds
s_add_u32 m0,s57,41856
s_nop 0
buffer_load_dwordx4 v19,s[40:43],s55 offen offset:128  lds
v_add_u32 v26,49152,v20
v_add_u32 v27,49152,v21
T a[48:49],26,0
T a[50:51],26,512
T a[52:53],27,0
T a[54:55],27,512
T a[56:57],26,4096
T a[58:59],26,4608
T a[60:61],27,4096
T a[62:63],27,4608
s_waitcnt vmcnt(20)
L 0
s_barrier
s_add_u32 s55,s50,1310720
s_add_u32 m0,s57,49152
s_nop 0
buffer_load_dwordx4 v18,s[40:43],s55 offen offset:0  lds
s_add_u32 m0,s57,50176
s_nop 0
buffer_load_dwordx4 v19,s[40:43],s55 offen offset:0  lds
s_add_u32 m0,s57,57216
s_nop 0
buffer_load_dwordx4 v18,s[40:43],s55 offen offset:128  lds
s_add_u32 m0,s57,58240
s_nop 0
buffer_load_dwordx4 v19,s[40:43],s55 offen offset:128  lds
v_add_u32 v26,65536,v20
v_add_u32 v27,65536,v21
T a[64:65],26,0
T a[66:67],26,512
T a[68:69],27,0
T a[70:71],27,512
T a[72:73],26,4096
T a[74:75],26,4608
T a[76:77],27,4096
T a[78:79],27,4608
s_waitcnt vmcnt(20)
L 0
s_barrier
s_add_u32 s55,s50,1441792
s_add_u32 m0,s57,65536
s_nop 0
buffer_load_dwordx4 v18,s[40:43],s55 offen offset:0  lds
s_add_u32 m0,s57,66560
s_nop 0
buffer_load_dwordx4 v19,s[40:43],s55 offen offset:0  lds
s_add_u32 m0,s57,73600
s_nop 0
buffer_load_dwordx4 v18,s[40:43],s55 offen offset:128  lds
s_add_u32 m0,s57,74624
s_nop 0
buffer_load_dwordx4 v19,s[40:43],s55 offen offset:128  lds
v_add_u32 v26,81920,v20
v_add_u32 v27,81920,v21
T a[80:81],26,0
T a[82:83],26,512
T a[84:85],27,0
T a[86:87],27,512
T a[88:89],26,4096
T a[90:91],26,4608
T a[92:93],27,4096
T a[94:95],27,4608
s_waitcnt vmcnt(20)
L 0
s_barrier
s_add_u32 s55,s50,1572864
s_add_u32 m0,s57,81920
s_nop 0
buffer_load_dwordx4 v18,s[40:43],s55 offen offset:0  lds
s_add_u32 m0,s57,82944
s_nop 0
buffer_load_dwordx4 v19,s[40:43],s55 offen offset:0  lds
s_add_u32 m0,s57,89984
s_nop 0
buffer_load_dwordx4 v18,s[40:43],s55 offen offset:128  lds
s_add_u32 m0,s57,91008
s_nop 0
buffer_load_dwordx4 v19,s[40:43],s55 offen offset:128  lds
v_add_u32 v26,98304,v20
v_add_u32 v27,98304,v21
T a[96:97],26,0
T a[98:99],26,512
T a[100:101],27,0
T a[102:103],27,512
T a[104:105],26,4096
T a[106:107],26,4608
T a[108:109],27,4096
T a[110:111],27,4608
s_waitcnt vmcnt(20)
L 0
s_barrier
s_add_u32 s55,s50,1703936
s_add_u32 m0,s57,98304
s_nop 0
buffer_load_dwordx4 v18,s[40:43],s55 offen offset:0  lds
s_add_u32 m0,s57,99328
s_nop 0
buffer_load_dwordx4 v19,s[40:43],s55 offen offset:0  lds
s_add_u32 m0,s57,106368
s_nop 0
buffer_load_dwordx4 v18,s[40:43],s55 offen offset:128  lds
s_add_u32 m0,s57,107392
s_nop 0
buffer_load_dwordx4 v19,s[40:43],s55 offen offset:128  lds
v_add_u32 v26,0,v20
v_add_u32 v27,0,v21
T a[112:113],26,0
T a[114:115],26,512
T a[116:117],27,0
T a[118:119],27,512
T a[120:121],26,4096
T a[122:123],26,4608
T a[124:125],27,4096
T a[126:127],27,4608
s_waitcnt vmcnt(20)
L 0
s_barrier
s_add_u32 s55,s50,1835008
s_add_u32 m0,s57,0
s_nop 0
buffer_load_dwordx4 v18,s[40:43],s55 offen offset:0  lds
s_add_u32 m0,s57,1024
s_nop 0
buffer_load_dwordx4 v19,s[40:43],s55 offen offset:0  lds
s_add_u32 m0,s57,8064
s_nop 0
buffer_load_dwordx4 v18,s[40:43],s55 offen offset:128  lds
s_add_u32 m0,s57,9088
s_nop 0
buffer_load_dwordx4 v19,s[40:43],s55 offen offset:128  lds
v_add_u32 v26,16384,v20
v_add_u32 v27,16384,v21
T a[128:129],26,0
T a[130:131],26,512
T a[132:133],27,0
T a[134:135],27,512
T a[136:137],26,4096
T a[138:139],26,4608
T a[140:141],27,4096
T a[142:143],27,4608
s_waitcnt vmcnt(20)
L 0
s_barrier
s_add_u32 s55,s50,1966080
s_add_u32 m0,s57,16384
s_nop 0
buffer_load_dwordx4 v18,s[40:43],s55 offen offset:0  lds
s_add_u32 m0,s57,17408
s_nop 0
buffer_load_dwordx4 v19,s[40:43],s55 offen offset:0  lds
s_add_u32 m0,s57,24448
s_nop 0
buffer_load_dwordx4 v18,s[40:43],s55 offen offset:128  lds
s_add_u32 m0,s57,25472
s_nop 0
buffer_load_dwordx4 v19,s[40:43],s55 offen offset:128  lds
v_add_u32 v26,32768,v20
v_add_u32 v27,32768,v21
T a[144:145],26,0
T a[146:147],26,512
T a[148:149],27,0
T a[150:151],27,512
T a[152:153],26,4096
T a[154:155],26,4608
T a[156:157],27,4096
T a[158:159],27,4608
s_waitcnt vmcnt(20)
L 0
s_barrier
v_add_u32 v26,49152,v20
v_add_u32 v27,49152,v21
T a[160:161],26,0
T a[162:163],26,512
T a[164:165],27,0
T a[166:167],27,512
T a[168:169],26,4096
T a[170:171],26,4608
T a[172:173],27,4096
T a[174:175],27,4608
s_waitcnt vmcnt(16)
L 0
s_barrier
v_add_u32 v26,65536,v20
v_add_u32 v27,65536,v21
T a[176:177],26,0
T a[178:179],26,512
T a[180:181],27,0
T a[182:183],27,512
T a[184:185],26,4096
T a[186:187],26,4608
T a[188:189],27,4096
T a[190:191],27,4608
s_waitcnt vmcnt(12)
L 0
s_barrier
v_add_u32 v26,81920,v20
v_add_u32 v27,81920,v21
T a[192:193],26,0
T a[194:195],26,512
T a[196:197],27,0
T a[198:199],27,512
T a[200:201],26,4096
T a[202:203],26,4608
T a[204:205],27,4096
T a[206:207],27,4608
s_waitcnt vmcnt(8)
L 0
s_barrier
v_add_u32 v26,98304,v20
v_add_u32 v27,98304,v21
T a[208:209],26,0
T a[210:211],26,512
T a[212:213],27,0
T a[214:215],27,512
T a[216:217],26,4096
T a[218:219],26,4608
T a[220:221],27,4096
T a[222:223],27,4608
s_waitcnt vmcnt(4)
L 0
s_barrier
v_add_u32 v26,0,v20
v_add_u32 v27,0,v21
T a[224:225],26,0
T a[226:227],26,512
T a[228:229],27,0
T a[230:231],27,512
T a[232:233],26,4096
T a[234:235],26,4608
T a[236:237],27,4096
T a[238:239],27,4608
s_waitcnt vmcnt(0)
L 0
s_barrier
v_add_u32 v26,16384,v20
v_add_u32 v27,16384,v21
T a[240:241],26,0
T a[242:243],26,512
T a[244:245],27,0
T a[246:247],27,512
T a[248:249],26,4096
T a[250:251],26,4608
T a[252:253],27,4096
T a[254:255],27,4608
L 0
s_barrier
v_mov_b32 v34,0x3f803f80
v_mov_b32 v35,0x3f803f80
v_mov_b32 v36,0x3f803f80
v_mov_b32 v37,0x3f803f80
s_nop 1
v_mfma_f32_16x16x32_bf16 v[106:109],a[0:3],v[34:37],0
v_mfma_f32_16x16x32_bf16 v[110:113],a[4:7],v[34:37],0
M 106,a[8:11],34
M 110,a[12:15],34
M 106,a[16:19],34
M 110,a[20:23],34
M 106,a[24:27],34
M 110,a[28:31],34
M 106,a[32:35],34
M 110,a[36:39],34
M 106,a[40:43],34
M 110,a[44:47],34
M 106,a[48:51],34
M 110,a[52:55],34
M 106,a[56:59],34
M 110,a[60:63],34
M 106,a[64:67],34
)"
R"(M 110,a[68:71],34
M 106,a[72:75],34
M 110,a[76:79],34
M 106,a[80:83],34
M 110,a[84:87],34
M 106,a[88:91],34
M 110,a[92:95],34
M 106,a[96:99],34
M 110,a[100:103],34
M 106,a[104:107],34
M 110,a[108:111],34
M 106,a[112:115],34
M 110,a[116:119],34
M 106,a[120:123],34
M 110,a[124:127],34
M 106,a[128:131],34
M 110,a[132:135],34
M 106,a[136:139],34
M 110,a[140:143],34
M 106,a[144:147],34
M 110,a[148:151],34
M 106,a[152:155],34
M 110,a[156:159],34
M 106,a[160:163],34
M 110,a[164:167],34
M 106,a[168:171],34
M 110,a[172:175],34
M 106,a[176:179],34
M 110,a[180:183],34
M 106,a[184:187],34
M 110,a[188:191],34
M 106,a[192:195],34
M 110,a[196:199],34
M 106,a[200:203],34
M 110,a[204:207],34
M 106,a[208:211],34
M 110,a[212:215],34
M 106,a[216:219],34
M 110,a[220:223],34
M 106,a[224:227],34
M 110,a[228:231],34
M 106,a[232:235],34
M 110,a[236:239],34
M 106,a[240:243],34
M 110,a[244:247],34
M 106,a[248:251],34
M 110,a[252:255],34
v_mov_b32 v4,s53
s_mov_b32 s82,0
s_mov_b32 s80,0
s_mov_b32 s54,s45
s_mul_i32 s57,s44,8192
s_mul_i32 s55,s45,32768
s_add_u32 s55,s55,s57
s_add_u32 s55,s55,8388608
s_mul_i32 s57,s44,8192
s_mul_i32 s60,s46,32768
s_mul_i32 s61,s46,1024
s_mul_i32 s62,s46,40960
s_mul_i32 s63,s46,1024
s_mul_i32 s64,s46,512
s_lshl_b32 s59,s44,8
s_add_u32 s59,s59,131072
s_mul_i32 s58,s45,1024
s_add_u32 s58,s58,109056000
s_mul_i32 s65,s45,40960
s_add_u32 s65,s65,s47
s_mul_i32 s68,s45,1024
s_add_u32 s68,s68,s48
s_mul_i32 s73,s45,512
s_add_u32 s73,s73,106954752
s_mul_i32 s74,s45,32768
s_add_u32 s74,s74,s49
s_mul_i32 s75,s45,1024
s_add_u32 s75,s75,107479040
s_mov_b32 s66,0x7fff0000
s_mov_b32 s67,0x7fff0000
s_mov_b32 s69,0x7fff0000
s_mov_b32 s76,0x7fff0000
s_mov_b32 s77,0x7fff0000
s_mov_b32 s70,0x7fff0000
s_mov_b32 s71,0x7fff0000
s_mov_b32 s72,0x7fff0000
v_mov_b32 v1,v0
v_add_u32 v2,32768,v0
s_mov_b32 s84,32768
s_mov_b32 s86,0x3a800000
s_mov_b32 s87,0x3727c5ac
s_mov_b32 s88,0xc0135761
s_mov_b32 s89,0x3d372713
s_and_b32 s85,s44,1
s_mul_i32 s85,s85,127
v_lshlrev_b32 v8,22,v0
v_and_b32 v8,0x80000000,v8
v_or_b32 v8,v8,v5
s_add_u32 m0,s59,4096
s_nop 0
buffer_load_dword v5,s[36:39],s58 offen offset:0 sc1 lds
buffer_store_dword v4,v3,s[36:39],s71 offen offset:0 sc0 sc1
s_add_u32 m0,s59,5120
s_nop 0
buffer_load_dword v5,s[36:39],s58 offen offset:0 sc1 lds
buffer_store_dword v4,v3,s[36:39],s71 offen offset:0 sc0 sc1
s_mov_b32 s83,0
Lg1_poll_%=:
s_add_u32 s78,s83,2
s_and_b32 s78,s78,3
s_lshl_b32 s78,s78,10
s_add_u32 s78,s78,4096
s_add_u32 m0,s59,s78
s_nop 0
buffer_load_dword v5,s[36:39],s58 offen offset:0 sc1 lds
s_and_b32 s78,s83,3
s_lshl_b32 s78,s78,10
v_add_u32 v114,s78,v6
s_waitcnt vmcnt(4)
s_barrier
ds_read_b32 v7,v114 offset:4096
buffer_store_dword v4,v3,s[36:39],s70 offen offset:0 sc0 sc1
s_mov_b32 s70,s71
s_mov_b32 s71,s72
s_mov_b32 s72,0x7fff0000
s_add_u32 s83,s83,1
s_add_u32 s82,s82,1
L 0
v_cmp_eq_u32 vcc,s53,v7
s_nop 0
s_cmp_eq_u32 vcc_lo,-1
s_cbranch_scc1 Lg1_go_%=
s_cmp_lt_u32 s82,20000
s_cbranch_scc1 Lg1_poll_%=
Lg1_go_%=:
s_mov_b32 m0,s57
s_add_u32 s56,s55,4096
D 0,55,0
D 0,55,1024
D 0,55,2048
D 0,55,3072
s_add_u32 m0,s57,4096
s_nop 0
D 0,56,0
D 0,56,1024
D 0,56,2048
D 0,56,3072
s_add_u32 s55,s55,s60
s_add_u32 s57,s57,32768
s_and_b32 s57,s57,131071
s_add_u32 s54,s54,s46
Lg1_gox_%=:
s_add_u32 s58,s58,s61
s_cmp_eq_u32 vcc_hi,-1
s_cbranch_scc1 Lg1_go2_%=
s_add_u32 m0,s59,2048
s_nop 0
buffer_load_dword v5,s[36:39],s58 offen offset:0 sc1 lds
s_waitcnt vmcnt(0)
s_barrier
buffer_store_dword v4,v3,s[36:39],s70 offen offset:0 sc0 sc1
s_min_u32 s71,s71,s72
buffer_store_dword v4,v3,s[36:39],s71 offen offset:0 sc0 sc1
s_mov_b32 s70,0x7fff0000
s_mov_b32 s71,0x7fff0000
s_mov_b32 s72,0x7fff0000
s_cmp_eq_u32 s80,1
s_cbranch_scc1 Lg1_st11_%=
Lg1_st10_%=:
ds_read_b32 v7,v6 offset:2048
R 34,1,0
R 38,1,1024
R 42,1,2048
R 46,1,3072
R 50,1,4096
R 54,1,5120
R 58,1,6144
L 7
v_cmp_eq_u32 vcc,s53,v7
L 5
s_cmp_eq_u64 vcc,-1
s_cbranch_scc1 Lg1_L20_%=
s_cmp_eq_u32 vcc_lo,-1
s_cbranch_scc1 Lg1_L10_%=
s_branch Lg1_X0_%=
Lg1_st11_%=:
ds_read_b32 v7,v6 offset:2048
R 34,2,0
R 38,2,1024
R 42,2,2048
R 46,2,3072
R 50,2,4096
R 54,2,5120
R 58,2,6144
L 7
v_cmp_eq_u32 vcc,s53,v7
L 5
s_cmp_eq_u64 vcc,-1
s_cbranch_scc1 Lg1_L21_%=
s_cmp_eq_u32 vcc_lo,-1
s_cbranch_scc1 Lg1_L11_%=
s_branch Lg1_X1_%=
Lg1_go2_%=:
s_add_u32 s58,s58,s61
s_add_u32 m0,s59,2048
s_nop 0
buffer_load_dword v5,s[36:39],s58 offen offset:0 sc1 lds
s_mov_b32 m0,s57
s_add_u32 s56,s55,4096
D 0,55,0
D 0,55,1024
D 0,55,2048
D 0,55,3072
s_add_u32 m0,s57,4096
s_nop 0
D 0,56,0
D 0,56,1024
D 0,56,2048
D 0,56,3072
s_add_u32 s55,s55,s60
s_add_u32 s57,s57,32768
s_and_b32 s57,s57,131071
s_add_u32 s54,s54,s46
s_waitcnt vmcnt(8)
s_barrier
buffer_store_dword v4,v3,s[36:39],s70 offen offset:0 sc0 sc1
s_min_u32 s71,s71,s72
buffer_store_dword v4,v3,s[36:39],s71 offen offset:0 sc0 sc1
s_mov_b32 s70,0x7fff0000
s_mov_b32 s71,0x7fff0000
s_mov_b32 s72,0x7fff0000
s_cmp_eq_u32 s80,1
s_cbranch_scc1 Lg1_st21_%=
Lg1_st20_%=:
ds_read_b32 v7,v6 offset:2048
R 34,1,0
R 38,1,1024
R 42,1,2048
R 46,1,3072
R 50,1,4096
R 54,1,5120
R 58,1,6144
L 7
v_cmp_eq_u32 vcc,s53,v7
L 5
s_cmp_eq_u32 vcc_lo,-1
s_cbranch_scc1 Lg1_S0_%=
s_branch Lg1_N0_%=
Lg1_st21_%=:
ds_read_b32 v7,v6 offset:2048
R 34,2,0
R 38,2,1024
R 42,2,2048
R 46,2,3072
R 50,2,4096
R 54,2,5120
R 58,2,6144
L 7
v_cmp_eq_u32 vcc,s53,v7
L 5
s_cmp_eq_u32 vcc_lo,-1
s_cbranch_scc1 Lg1_S1_%=
s_branch Lg1_N1_%=
Lg1_L20_%=:
v_mfma_f32_16x16x32_bf16 v[22:25],a[4:7],v[34:37],0
s_add_u32 s58,s58,s61
s_add_u32 s58,s58,s61
R 62,1,7168
M 22,a[12:15],38
s_add_u32 m0,s59,0
buffer_load_dwordx4 v[98:101],v11,s[36:39],s73 offen offset:0 sc1
v_mfma_f32_16x16x32_bf16 v[18:21],a[0:3],v[34:37],0
s_add_u32 s56,s55,4096
R 66,1,8192
v_fma_f32 v26,-v102,v106,v26
M 18,a[8:11],38
L 5
buffer_load_dword v8,s[36:39],s58 offen offset:0 sc1 lds
M 18,a[16:19],42
s_mov_b32 m0,s57
R 70,1,9216
v_fma_f32 v27,-v102,v107,v27
M 18,a[24:27],46
s_add_u32 s54,s54,s46
v_fma_f32 v28,-v102,v108,v28
v_fma_f32 v29,-v102,v109,v29
M 22,a[20:23],42
s_mov_b32 s66,s65
R 74,1,10240
v_mul_f32 v114,v26,v26
M 22,a[28:31],46
L 5
D 0,55,0
M 22,a[36:39],50
s_add_u32 s73,s73,s64
R 78,1,11264
v_mul_f32 v115,v27,v27
M 22,a[44:47],54
s_add_u32 s54,s54,s46
v_mul_f32 v116,v28,v28
v_mul_f32 v117,v29,v29
M 18,a[32:35],50
s_add_u32 s65,s65,s62
R 82,1,12288
v_fma_f32 v114,v104,v114,v103
M 18,a[40:43],54
L 5
D 0,55,1024
M 18,a[48:51],58
R 86,1,13312
v_fma_f32 v115,v104,v115,v103
M 18,a[56:59],62
v_fma_f32 v116,v104,v116,v103
v_fma_f32 v117,v104,v117,v103
M 22,a[52:55],58
R 90,1,14336
v_mul_f32 v114,v26,v114
M 22,a[60:63],62
L 5
D 0,55,2048
M 22,a[68:71],66
R 94,1,15360
v_mul_f32 v115,v27,v115
M 22,a[76:79],70
v_exp_f32 v114,v114
M 18,a[64:67],66
R 34,1,16384
v_mul_f32 v116,v28,v116
M 18,a[72:75],70
L 5
D 0,55,3072
M 18,a[80:83],74
s_add_u32 m0,s57,4096
R 38,1,17408
v_mul_f32 v117,v29,v117
M 18,a[88:91],78
s_add_u32 s57,s57,32768
v_exp_f32 v115,v115
M 22,a[84:87],74
s_and_b32 s57,s57,131071
R 42,1,18432
v_fma_f32 v114,v114,v105,v105
M 22,a[92:95],78
L 5
D 0,56,0
M 22,a[100:103],82
s_add_u32 s55,s55,s60
R 46,1,19456
v_fma_f32 v115,v115,v105,v105
M 22,a[108:111],86
v_exp_f32 v116,v116
M 18,a[96:99],82
R 50,1,20480
v_fma_f32 v116,v116,v105,v105
M 18,a[104:107],86
L 5
D 0,56,1024
M 18,a[112:115],90
R 54,1,21504
v_fma_f32 v30,-v102,v110,v30
M 18,a[120:123],94
v_exp_f32 v117,v117
M 22,a[116:119],90
R 58,1,22528
v_fma_f32 v117,v117,v105,v105
M 22,a[124:127],94
L 5
D 0,56,2048
M 22,a[132:135],34
R 62,1,23552
v_fma_f32 v31,-v102,v111,v31
M 22,a[140:143],38
v_rcp_f32 v114,v114
M 18,a[128:131],34
R 66,1,24576
v_mul_f32 v26,v26,v114
M 18,a[136:139],38
L 5
D 0,56,3072
M 18,a[144:147],42
s_mov_b32 m0,s57
R 70,1,25600
v_fma_f32 v32,-v102,v112,v32
M 18,a[152:155],46
s_add_u32 s56,s55,4096
v_rcp_f32 v115,v115
M 22,a[148:151],42
R 74,1,26624
v_mul_f32 v27,v27,v115
M 22,a[156:159],46
L 5
D 0,55,0
M 22,a[164:167],50
R 78,1,27648
v_cvt_pk_bf16_f32 v122,v26,v27
M 22,a[172:175],54
D 0,55,1024
M 18,a[160:163],50
R 82,1,28672
v_fma_f32 v33,-v102,v113,v33
M 18,a[168:171],54
L 5
v_rcp_f32 v116,v116
M 18,a[176:179],58
R 86,1,29696
v_mul_f32 v28,v28,v116
M 18,a[184:187],62
D 0,55,2048
M 22,a[180:183],58
R 90,1,30720
v_mul_f32 v118,v30,v30
M 22,a[188:191],62
L 5
D 0,55,3072
M 22,a[196:199],66
s_waitcnt vmcnt(4)
R 94,1,31744
v_mul_f32 v119,v31,v31
M 22,a[204:207],70
s_barrier
buffer_store_dword v4,v3,s[36:39],s70 offen offset:0 sc0 sc1
M 18,a[192:195],66
s_add_u32 m0,s57,4096
R 34,2,0
ds_read_b32 v7,v6 offset:0
M 18,a[200:203],70
L 6
D 0,56,0
M 18,a[208:211],74
s_mov_b32 s70,s69
R 38,2,1024
v_mul_f32 v120,v32,v32
M 18,a[216:219],78
s_add_u32 s57,s57,32768
D 0,56,1024
M 22,a[212:215],74
s_mov_b32 s69,s68
R 42,2,2048
v_mul_f32 v121,v33,v33
v_fma_f32 v118,v104,v118,v103
v_fma_f32 v119,v104,v119,v103
v_fma_f32 v120,v104,v120,v103
v_fma_f32 v121,v104,v121,v103
v_mul_f32 v118,v30,v118
v_mul_f32 v119,v31,v119
v_mul_f32 v120,v32,v120
v_mul_f32 v121,v33,v121
v_exp_f32 v118,v118
v_exp_f32 v119,v119
v_exp_f32 v120,v120
v_exp_f32 v121,v121
M 22,a[220:223],78
L 6
v_rcp_f32 v117,v117
v_fma_f32 v118,v118,v105,v105
v_fma_f32 v119,v119,v105,v105
v_fma_f32 v120,v120,v105,v105
v_fma_f32 v121,v121,v105,v105
v_rcp_f32 v118,v118
v_rcp_f32 v119,v119
v_rcp_f32 v120,v120
v_rcp_f32 v121,v121
M 22,a[228:231],82
L 2
R 46,2,3072
v_mul_f32 v29,v29,v117
v_cvt_pk_bf16_f32 v123,v28,v29
v_mul_f32 v30,v30,v118
v_mul_f32 v31,v31,v119
v_mul_f32 v32,v32,v120
v_mul_f32 v33,v33,v121
v_cvt_pk_bf16_f32 v124,v30,v31
v_cvt_pk_bf16_f32 v125,v32,v33
M 22,a[236:239],86
s_add_u32 s55,s55,s60
buffer_store_dwordx4 v[122:125],v15,s[36:39],s67 offen offset:0 sc0 sc1
v_xor_b32 v1,65536,v1
M 18,a[224:227],82
s_and_b32 s57,s57,131071
R 50,2,4096
v_cmp_eq_u32 vcc,s53,v7
M 18,a[232:235],86
s_nop 0
s_add_u32 s68,s68,s63
s_cmp_eq_u32 vcc_lo,-1
M 18,a[240:243],90
R 54,2,5120
M 18,a[248:251],94
buffer_store_dwordx4 v[122:125],v16,s[36:39],s67 offen offset:0 sc0 sc1
M 22,a[244:247],90
R 58,2,6144
M 22,a[252:255],94
L 5
D 0,56,2048
D 0,56,3072
s_cbranch_scc0 Lg1_N1_%=
Lg1_S1_%=:
v_mfma_f32_16x16x32_bf16 v[30:33],a[4:7],v[34:37],0
s_add_u32 s58,s58,s61
s_add_u32 m0,s59,1024
R 62,2,7168
M 30,a[12:15],38
s_add_u32 s56,s55,4096
)"
R"(buffer_load_dword v8,s[36:39],s58 offen offset:0 sc1 lds
v_mfma_f32_16x16x32_bf16 v[26:29],a[0:3],v[34:37],0
s_mov_b32 m0,s57
R 66,2,8192
v_fma_f32 v18,-v98,v106,v18
M 26,a[8:11],38
L 5
buffer_load_dwordx4 v[102:105],v11,s[36:39],s73 offen offset:0 sc1
M 26,a[16:19],42
s_mov_b32 s67,s65
R 70,2,9216
v_fma_f32 v19,-v98,v107,v19
M 26,a[24:27],46
s_add_u32 s73,s73,s64
v_fma_f32 v20,-v98,v108,v20
v_fma_f32 v21,-v98,v109,v21
M 30,a[20:23],42
s_add_u32 s54,s54,s46
R 74,2,10240
v_mul_f32 v114,v18,v18
M 30,a[28:31],46
L 5
D 0,55,0
M 30,a[36:39],50
s_add_u32 s65,s65,s62
R 78,2,11264
v_mul_f32 v115,v19,v19
M 30,a[44:47],54
v_mul_f32 v116,v20,v20
v_mul_f32 v117,v21,v21
M 26,a[32:35],50
R 82,2,12288
v_fma_f32 v114,v100,v114,v99
M 26,a[40:43],54
L 5
v_fma_f32 v115,v100,v115,v99
v_fma_f32 v116,v100,v116,v99
M 26,a[48:51],58
R 86,2,13312
v_fma_f32 v117,v100,v117,v99
M 26,a[56:59],62
D 0,55,1024
M 30,a[52:55],58
R 90,2,14336
v_mul_f32 v114,v18,v114
M 30,a[60:63],62
L 5
v_exp_f32 v114,v114
M 30,a[68:71],66
R 94,2,15360
v_mul_f32 v115,v19,v115
M 30,a[76:79],70
v_exp_f32 v115,v115
M 26,a[64:67],66
R 34,2,16384
v_mul_f32 v116,v20,v116
M 26,a[72:75],70
L 5
v_exp_f32 v116,v116
M 26,a[80:83],74
R 38,2,17408
v_mul_f32 v117,v21,v117
M 26,a[88:91],78
D 0,55,2048
M 30,a[84:87],74
R 42,2,18432
v_fma_f32 v114,v114,v101,v101
M 30,a[92:95],78
L 5
v_exp_f32 v117,v117
M 30,a[100:103],82
R 46,2,19456
v_fma_f32 v115,v115,v101,v101
M 30,a[108:111],86
v_rcp_f32 v114,v114
M 26,a[96:99],82
R 50,2,20480
v_fma_f32 v116,v116,v101,v101
M 26,a[104:107],86
L 5
D 0,55,3072
M 26,a[112:115],90
s_add_u32 m0,s57,4096
R 54,2,21504
v_fma_f32 v117,v117,v101,v101
M 26,a[120:123],94
s_add_u32 s57,s57,32768
v_rcp_f32 v115,v115
M 30,a[116:119],90
s_add_u32 s55,s55,s60
R 58,2,22528
v_mul_f32 v18,v18,v114
M 30,a[124:127],94
L 5
v_rcp_f32 v116,v116
M 30,a[132:135],34
s_and_b32 s57,s57,131071
R 62,2,23552
v_mul_f32 v19,v19,v115
M 30,a[140:143],38
v_rcp_f32 v117,v117
M 26,a[128:131],34
R 66,2,24576
v_mul_f32 v20,v20,v116
M 26,a[136:139],38
L 5
D 0,56,0
M 26,a[144:147],42
R 70,2,25600
v_mul_f32 v21,v21,v117
M 26,a[152:155],46
v_cvt_pk_bf16_f32 v122,v18,v19
v_cvt_pk_bf16_f32 v123,v20,v21
M 30,a[148:151],42
R 74,2,26624
v_fma_f32 v22,-v98,v110,v22
M 30,a[156:159],46
L 5
v_fma_f32 v23,-v98,v111,v23
v_fma_f32 v24,-v98,v112,v24
v_fma_f32 v25,-v98,v113,v25
M 30,a[164:167],50
R 78,2,27648
v_mul_f32 v118,v22,v22
M 30,a[172:175],54
D 0,56,1024
M 26,a[160:163],50
R 82,2,28672
v_mul_f32 v119,v23,v23
M 26,a[168:171],54
L 5
v_mul_f32 v120,v24,v24
v_mul_f32 v121,v25,v25
M 26,a[176:179],58
R 86,2,29696
v_fma_f32 v118,v100,v118,v99
M 26,a[184:187],62
v_fma_f32 v119,v100,v119,v99
v_fma_f32 v120,v100,v120,v99
M 30,a[180:183],58
R 90,2,30720
v_fma_f32 v121,v100,v121,v99
M 30,a[188:191],62
L 5
v_mul_f32 v118,v22,v118
v_mul_f32 v119,v23,v119
M 30,a[196:199],66
s_waitcnt vmcnt(6)
R 94,2,31744
v_mul_f32 v120,v24,v120
M 30,a[204:207],70
s_barrier
D 0,56,2048
M 26,a[192:195],66
R 34,1,0
ds_read_b32 v7,v6 offset:1024
M 26,a[200:203],70
L 6
buffer_store_dword v4,v3,s[36:39],s70 offen offset:0 sc0 sc1
M 26,a[208:211],74
s_mov_b32 s70,s69
R 38,1,1024
v_mul_f32 v121,v25,v121
M 26,a[216:219],78
s_mov_b32 s69,s68
v_exp_f32 v118,v118
M 30,a[212:215],74
s_add_u32 s68,s68,s63
R 42,1,2048
v_exp_f32 v119,v119
v_exp_f32 v120,v120
v_exp_f32 v121,v121
M 30,a[220:223],78
L 6
v_fma_f32 v118,v118,v101,v101
v_fma_f32 v119,v119,v101,v101
v_fma_f32 v120,v120,v101,v101
v_fma_f32 v121,v121,v101,v101
v_rcp_f32 v118,v118
v_rcp_f32 v119,v119
v_rcp_f32 v120,v120
v_rcp_f32 v121,v121
M 30,a[228:231],82
L 2
R 46,1,3072
v_mul_f32 v22,v22,v118
v_mul_f32 v23,v23,v119
v_mul_f32 v24,v24,v120
v_mul_f32 v25,v25,v121
v_cvt_pk_bf16_f32 v124,v22,v23
v_cvt_pk_bf16_f32 v125,v24,v25
M 30,a[236:239],86
buffer_store_dwordx4 v[122:125],v15,s[36:39],s66 offen offset:0 sc0 sc1
v_xor_b32 v2,65536,v2
M 26,a[224:227],82
R 50,1,4096
v_cmp_eq_u32 vcc,s53,v7
M 26,a[232:235],86
s_nop 0
s_cmp_eq_u32 vcc_lo,-1
M 26,a[240:243],90
R 54,1,5120
M 26,a[248:251],94
buffer_store_dwordx4 v[122:125],v16,s[36:39],s66 offen offset:0 sc0 sc1
M 30,a[244:247],90
R 58,1,6144
M 30,a[252:255],94
L 5
D 0,56,3072
s_cbranch_scc0 Lg1_N0_%=
Lg1_S0_%=:
v_mfma_f32_16x16x32_bf16 v[22:25],a[4:7],v[34:37],0
s_add_u32 s58,s58,s61
s_add_u32 m0,s59,0
R 62,1,7168
M 22,a[12:15],38
s_add_u32 s56,s55,4096
buffer_load_dword v8,s[36:39],s58 offen offset:0 sc1 lds
v_mfma_f32_16x16x32_bf16 v[18:21],a[0:3],v[34:37],0
s_mov_b32 m0,s57
R 66,1,8192
v_fma_f32 v26,-v102,v106,v26
M 18,a[8:11],38
L 5
buffer_load_dwordx4 v[98:101],v11,s[36:39],s73 offen offset:0 sc1
M 18,a[16:19],42
s_mov_b32 s66,s65
R 70,1,9216
v_fma_f32 v27,-v102,v107,v27
M 18,a[24:27],46
s_add_u32 s73,s73,s64
v_fma_f32 v28,-v102,v108,v28
v_fma_f32 v29,-v102,v109,v29
M 22,a[20:23],42
s_add_u32 s54,s54,s46
R 74,1,10240
v_mul_f32 v114,v26,v26
M 22,a[28:31],46
L 5
D 0,55,0
M 22,a[36:39],50
s_add_u32 s65,s65,s62
R 78,1,11264
v_mul_f32 v115,v27,v27
M 22,a[44:47],54
v_mul_f32 v116,v28,v28
v_mul_f32 v117,v29,v29
M 18,a[32:35],50
R 82,1,12288
v_fma_f32 v114,v104,v114,v103
M 18,a[40:43],54
L 5
v_fma_f32 v115,v104,v115,v103
v_fma_f32 v116,v104,v116,v103
M 18,a[48:51],58
R 86,1,13312
v_fma_f32 v117,v104,v117,v103
M 18,a[56:59],62
D 0,55,1024
M 22,a[52:55],58
R 90,1,14336
v_mul_f32 v114,v26,v114
M 22,a[60:63],62
L 5
v_exp_f32 v114,v114
M 22,a[68:71],66
R 94,1,15360
v_mul_f32 v115,v27,v115
M 22,a[76:79],70
v_exp_f32 v115,v115
M 18,a[64:67],66
R 34,1,16384
v_mul_f32 v116,v28,v116
M 18,a[72:75],70
L 5
v_exp_f32 v116,v116
M 18,a[80:83],74
R 38,1,17408
v_mul_f32 v117,v29,v117
M 18,a[88:91],78
D 0,55,2048
M 22,a[84:87],74
R 42,1,18432
v_fma_f32 v114,v114,v105,v105
M 22,a[92:95],78
L 5
v_exp_f32 v117,v117
M 22,a[100:103],82
R 46,1,19456
v_fma_f32 v115,v115,v105,v105
M 22,a[108:111],86
v_rcp_f32 v114,v114
M 18,a[96:99],82
R 50,1,20480
v_fma_f32 v116,v116,v105,v105
M 18,a[104:107],86
L 5
D 0,55,3072
M 18,a[112:115],90
s_add_u32 m0,s57,4096
R 54,1,21504
v_fma_f32 v117,v117,v105,v105
M 18,a[120:123],94
s_add_u32 s57,s57,32768
v_rcp_f32 v115,v115
M 22,a[116:119],90
s_add_u32 s55,s55,s60
R 58,1,22528
v_mul_f32 v26,v26,v114
M 22,a[124:127],94
L 5
v_rcp_f32 v116,v116
M 22,a[132:135],34
s_and_b32 s57,s57,131071
R 62,1,23552
v_mul_f32 v27,v27,v115
M 22,a[140:143],38
v_rcp_f32 v117,v117
M 18,a[128:131],34
R 66,1,24576
v_mul_f32 v28,v28,v116
M 18,a[136:139],38
L 5
D 0,56,0
M 18,a[144:147],42
R 70,1,25600
v_mul_f32 v29,v29,v117
M 18,a[152:155],46
v_cvt_pk_bf16_f32 v122,v26,v27
v_cvt_pk_bf16_f32 v123,v28,v29
M 22,a[148:151],42
R 74,1,26624
v_fma_f32 v30,-v102,v110,v30
M 22,a[156:159],46
L 5
v_fma_f32 v31,-v102,v111,v31
v_fma_f32 v32,-v102,v112,v32
v_fma_f32 v33,-v102,v113,v33
M 22,a[164:167],50
R 78,1,27648
v_mul_f32 v118,v30,v30
M 22,a[172:175],54
D 0,56,1024
M 18,a[160:163],50
R 82,1,28672
v_mul_f32 v119,v31,v31
M 18,a[168:171],54
L 5
v_mul_f32 v120,v32,v32
v_mul_f32 v121,v33,v33
M 18,a[176:179],58
R 86,1,29696
v_fma_f32 v118,v104,v118,v103
M 18,a[184:187],62
v_fma_f32 v119,v104,v119,v103
v_fma_f32 v120,v104,v120,v103
M 22,a[180:183],58
R 90,1,30720
v_fma_f32 v121,v104,v121,v103
M 22,a[188:191],62
L 5
v_mul_f32 v118,v30,v118
v_mul_f32 v119,v31,v119
M 22,a[196:199],66
s_waitcnt vmcnt(6)
R 94,1,31744
v_mul_f32 v120,v32,v120
M 22,a[204:207],70
s_barrier
D 0,56,2048
M 18,a[192:195],66
R 34,2,0
ds_read_b32 v7,v6 offset:0
M 18,a[200:203],70
L 6
buffer_store_dword v4,v3,s[36:39],s70 offen offset:0 sc0 sc1
M 18,a[208:211],74
s_mov_b32 s70,s69
R 38,2,1024
v_mul_f32 v121,v33,v121
M 18,a[216:219],78
s_mov_b32 s69,s68
v_exp_f32 v118,v118
M 22,a[212:215],74
s_add_u32 s68,s68,s63
R 42,2,2048
v_exp_f32 v119,v119
v_exp_f32 v120,v120
v_exp_f32 v121,v121
M 22,a[220:223],78
L 6
v_fma_f32 v118,v118,v105,v105
v_fma_f32 v119,v119,v105,v105
v_fma_f32 v120,v120,v105,v105
v_fma_f32 v121,v121,v105,v105
v_rcp_f32 v118,v118
v_rcp_f32 v119,v119
v_rcp_f32 v120,v120
v_rcp_f32 v121,v121
M 22,a[228:231],82
L 2
R 46,2,3072
v_mul_f32 v30,v30,v118
v_mul_f32 v31,v31,v119
v_mul_f32 v32,v32,v120
v_mul_f32 v33,v33,v121
v_cvt_pk_bf16_f32 v124,v30,v31
v_cvt_pk_bf16_f32 v125,v32,v33
M 22,a[236:239],86
buffer_store_dwordx4 v[122:125],v15,s[36:39],s67 offen offset:0 sc0 sc1
v_xor_b32 v1,65536,v1
M 18,a[224:227],82
R 50,2,4096
v_cmp_eq_u32 vcc,s53,v7
M 18,a[232:235],86
s_nop 0
s_cmp_eq_u32 vcc_lo,-1
M 18,a[240:243],90
R 54,2,5120
M 18,a[248:251],94
buffer_store_dwordx4 v[122:125],v16,s[36:39],s67 offen offset:0 sc0 sc1
M 22,a[244:247],90
R 58,2,6144
M 22,a[252:255],94
L 5
D 0,56,3072
s_cbranch_scc1 Lg1_S1_%=
Lg1_N1_%=:
v_mfma_f32_16x16x32_bf16 v[30:33],a[4:7],v[34:37],0
s_add_u32 m0,s59,1024
s_mov_b32 s67,s65
R 62,2,7168
M 30,a[12:15],38
s_add_u32 s65,s65,s62
buffer_load_dword v5,s[36:39],s58 offen offset:0 sc1 lds
v_mfma_f32_16x16x32_bf16 v[26:29],a[0:3],v[34:37],0
R 66,2,8192
v_fma_f32 v18,-v98,v106,v18
M 26,a[8:11],38
L 5
buffer_load_dwordx4 v[102:105],v11,s[36:39],s73 offen offset:0 sc1
M 26,a[16:19],42
s_add_u32 s73,s73,s64
R 70,2,9216
v_fma_f32 v19,-v98,v107,v19
M 26,a[24:27],46
v_fma_f32 v20,-v98,v108,v20
v_fma_f32 v21,-v98,v109,v21
M 30,a[20:23],42
R 74,2,10240
v_mul_f32 v114,v18,v18
M 30,a[28:31],46
L 5
v_mul_f32 v115,v19,v19
v_mul_f32 v116,v20,v20
M 30,a[36:39],50
R 78,2,11264
v_mul_f32 v117,v21,v21
M 30,a[44:47],54
v_fma_f32 v114,v100,v114,v99
v_fma_f32 v115,v100,v115,v99
M 26,a[32:35],50
R 82,2,12288
v_fma_f32 v116,v100,v116,v99
M 26,a[40:43],54
L 5
v_fma_f32 v117,v100,v117,v99
v_mul_f32 v114,v18,v114
M 26,a[48:51],58
R 86,2,13312
v_mul_f32 v115,v19,v115
M 26,a[56:59],62
v_exp_f32 v114,v114
M 30,a[52:55],58
R 90,2,14336
v_mul_f32 v116,v20,v116
M 30,a[60:63],62
L 5
v_exp_f32 v115,v115
M 30,a[68:71],66
R 94,2,15360
v_mul_f32 v117,v21,v117
M 30,a[76:79],70
v_exp_f32 v116,v116
M 26,a[64:67],66
R 34,2,16384
v_fma_f32 v114,v114,v101,v101
M 26,a[72:75],70
L 5
v_exp_f32 v117,v117
M 26,a[80:83],74
R 38,2,17408
v_fma_f32 v115,v115,v101,v101
M 26,a[88:91],78
v_rcp_f32 v114,v114
M 30,a[84:87],74
R 42,2,18432
v_fma_f32 v116,v116,v101,v101
M 30,a[92:95],78
L 5
)"
R"(v_rcp_f32 v115,v115
M 30,a[100:103],82
R 46,2,19456
v_fma_f32 v117,v117,v101,v101
M 30,a[108:111],86
v_rcp_f32 v116,v116
M 26,a[96:99],82
R 50,2,20480
v_mul_f32 v18,v18,v114
M 26,a[104:107],86
L 5
v_rcp_f32 v117,v117
M 26,a[112:115],90
R 54,2,21504
v_mul_f32 v19,v19,v115
M 26,a[120:123],94
v_mul_f32 v20,v20,v116
v_mul_f32 v21,v21,v117
M 30,a[116:119],90
R 58,2,22528
v_cvt_pk_bf16_f32 v122,v18,v19
M 30,a[124:127],94
L 5
v_cvt_pk_bf16_f32 v123,v20,v21
v_fma_f32 v22,-v98,v110,v22
M 30,a[132:135],34
R 62,2,23552
v_fma_f32 v23,-v98,v111,v23
M 30,a[140:143],38
v_fma_f32 v24,-v98,v112,v24
v_fma_f32 v25,-v98,v113,v25
M 26,a[128:131],34
R 66,2,24576
v_mul_f32 v118,v22,v22
M 26,a[136:139],38
L 5
v_mul_f32 v119,v23,v23
v_mul_f32 v120,v24,v24
M 26,a[144:147],42
R 70,2,25600
v_mul_f32 v121,v25,v25
M 26,a[152:155],46
v_fma_f32 v118,v100,v118,v99
v_fma_f32 v119,v100,v119,v99
M 30,a[148:151],42
R 74,2,26624
v_fma_f32 v120,v100,v120,v99
M 30,a[156:159],46
L 5
v_fma_f32 v121,v100,v121,v99
v_mul_f32 v118,v22,v118
v_mul_f32 v119,v23,v119
M 30,a[164:167],50
R 78,2,27648
v_mul_f32 v120,v24,v120
M 30,a[172:175],54
v_exp_f32 v118,v118
M 26,a[160:163],50
R 82,2,28672
v_mul_f32 v121,v25,v121
M 26,a[168:171],54
L 5
v_exp_f32 v119,v119
M 26,a[176:179],58
R 86,2,29696
v_fma_f32 v118,v118,v101,v101
M 26,a[184:187],62
v_exp_f32 v120,v120
M 30,a[180:183],58
R 90,2,30720
v_fma_f32 v119,v119,v101,v101
M 30,a[188:191],62
L 5
v_exp_f32 v121,v121
M 30,a[196:199],66
s_waitcnt vmcnt(0)
R 94,2,31744
v_fma_f32 v120,v120,v101,v101
M 30,a[204:207],70
s_barrier
buffer_store_dword v4,v3,s[36:39],s70 offen offset:0 sc0 sc1
M 26,a[192:195],66
s_mov_b32 s70,s69
R 34,1,0
ds_read_b32 v7,v6 offset:1024
M 26,a[200:203],70
L 6
v_rcp_f32 v118,v118
M 26,a[208:211],74
s_mov_b32 s69,s68
R 38,1,1024
v_fma_f32 v121,v121,v101,v101
M 26,a[216:219],78
s_add_u32 s68,s68,s63
v_rcp_f32 v119,v119
M 30,a[212:215],74
R 42,1,2048
v_mul_f32 v22,v22,v118
M 30,a[220:223],78
L 6
v_rcp_f32 v120,v120
v_rcp_f32 v121,v121
M 30,a[228:231],82
L 2
R 46,1,3072
v_mul_f32 v23,v23,v119
v_mul_f32 v24,v24,v120
v_mul_f32 v25,v25,v121
v_cvt_pk_bf16_f32 v124,v22,v23
v_cvt_pk_bf16_f32 v125,v24,v25
M 30,a[236:239],86
buffer_store_dwordx4 v[122:125],v15,s[36:39],s66 offen offset:0 sc0 sc1
v_xor_b32 v2,65536,v2
M 26,a[224:227],82
R 50,1,4096
v_cmp_eq_u32 vcc,s53,v7
M 26,a[232:235],86
s_nop 0
s_cmp_eq_u64 vcc,-1
M 26,a[240:243],90
R 54,1,5120
M 26,a[248:251],94
buffer_store_dwordx4 v[122:125],v16,s[36:39],s66 offen offset:0 sc0 sc1
M 30,a[244:247],90
R 58,1,6144
M 30,a[252:255],94
L 5
s_cbranch_scc1 Lg1_L20_%=
s_cmp_eq_u32 vcc_lo,-1
s_cbranch_scc1 Lg1_L10_%=
Lg1_X0_%=:
v_mfma_f32_16x16x32_bf16 v[22:25],a[4:7],v[34:37],0
s_add_u32 m0,s59,0
s_mov_b32 s66,s65
R 62,1,7168
M 22,a[12:15],38
s_add_u32 s65,s65,s62
buffer_load_dword v5,s[36:39],s58 offen offset:0 sc1 lds
v_mfma_f32_16x16x32_bf16 v[18:21],a[0:3],v[34:37],0
R 66,1,8192
v_fma_f32 v26,-v102,v106,v26
M 18,a[8:11],38
L 5
buffer_load_dwordx4 v[98:101],v11,s[36:39],s73 offen offset:0 sc1
M 18,a[16:19],42
s_add_u32 s73,s73,s64
R 70,1,9216
v_fma_f32 v27,-v102,v107,v27
M 18,a[24:27],46
v_fma_f32 v28,-v102,v108,v28
v_fma_f32 v29,-v102,v109,v29
M 22,a[20:23],42
R 74,1,10240
v_mul_f32 v114,v26,v26
M 22,a[28:31],46
L 5
v_mul_f32 v115,v27,v27
v_mul_f32 v116,v28,v28
M 22,a[36:39],50
R 78,1,11264
v_mul_f32 v117,v29,v29
M 22,a[44:47],54
v_fma_f32 v114,v104,v114,v103
v_fma_f32 v115,v104,v115,v103
M 18,a[32:35],50
R 82,1,12288
v_fma_f32 v116,v104,v116,v103
M 18,a[40:43],54
L 5
v_fma_f32 v117,v104,v117,v103
v_mul_f32 v114,v26,v114
M 18,a[48:51],58
R 86,1,13312
v_mul_f32 v115,v27,v115
M 18,a[56:59],62
v_exp_f32 v114,v114
M 22,a[52:55],58
R 90,1,14336
v_mul_f32 v116,v28,v116
M 22,a[60:63],62
L 5
v_exp_f32 v115,v115
M 22,a[68:71],66
R 94,1,15360
v_mul_f32 v117,v29,v117
M 22,a[76:79],70
v_exp_f32 v116,v116
M 18,a[64:67],66
R 34,1,16384
v_fma_f32 v114,v114,v105,v105
M 18,a[72:75],70
L 5
v_exp_f32 v117,v117
M 18,a[80:83],74
R 38,1,17408
v_fma_f32 v115,v115,v105,v105
M 18,a[88:91],78
v_rcp_f32 v114,v114
M 22,a[84:87],74
R 42,1,18432
v_fma_f32 v116,v116,v105,v105
M 22,a[92:95],78
L 5
v_rcp_f32 v115,v115
M 22,a[100:103],82
R 46,1,19456
v_fma_f32 v117,v117,v105,v105
M 22,a[108:111],86
v_rcp_f32 v116,v116
M 18,a[96:99],82
R 50,1,20480
v_mul_f32 v26,v26,v114
M 18,a[104:107],86
L 5
v_rcp_f32 v117,v117
M 18,a[112:115],90
R 54,1,21504
v_mul_f32 v27,v27,v115
M 18,a[120:123],94
v_mul_f32 v28,v28,v116
v_mul_f32 v29,v29,v117
M 22,a[116:119],90
R 58,1,22528
v_cvt_pk_bf16_f32 v122,v26,v27
M 22,a[124:127],94
L 5
v_cvt_pk_bf16_f32 v123,v28,v29
v_fma_f32 v30,-v102,v110,v30
M 22,a[132:135],34
R 62,1,23552
v_fma_f32 v31,-v102,v111,v31
M 22,a[140:143],38
v_fma_f32 v32,-v102,v112,v32
v_fma_f32 v33,-v102,v113,v33
M 18,a[128:131],34
R 66,1,24576
v_mul_f32 v118,v30,v30
M 18,a[136:139],38
L 5
v_mul_f32 v119,v31,v31
v_mul_f32 v120,v32,v32
M 18,a[144:147],42
R 70,1,25600
v_mul_f32 v121,v33,v33
M 18,a[152:155],46
v_fma_f32 v118,v104,v118,v103
v_fma_f32 v119,v104,v119,v103
M 22,a[148:151],42
R 74,1,26624
v_fma_f32 v120,v104,v120,v103
M 22,a[156:159],46
L 5
v_fma_f32 v121,v104,v121,v103
v_mul_f32 v118,v30,v118
M 22,a[164:167],50
R 78,1,27648
v_mul_f32 v119,v31,v119
M 22,a[172:175],54
v_exp_f32 v118,v118
M 18,a[160:163],50
R 82,1,28672
v_mul_f32 v120,v32,v120
M 18,a[168:171],54
L 5
v_exp_f32 v119,v119
M 18,a[176:179],58
R 86,1,29696
v_mul_f32 v121,v33,v121
M 18,a[184:187],62
v_exp_f32 v120,v120
M 22,a[180:183],58
R 90,1,30720
v_fma_f32 v118,v118,v105,v105
M 22,a[188:191],62
L 5
v_exp_f32 v121,v121
M 22,a[196:199],66
s_waitcnt vmcnt(0)
R 94,1,31744
v_fma_f32 v119,v119,v105,v105
M 22,a[204:207],70
s_barrier
buffer_store_dword v4,v3,s[36:39],s70 offen offset:0 sc0 sc1
M 18,a[192:195],66
s_mov_b32 s70,s69
ds_read_b32 v7,v6 offset:0
v_fma_f32 v120,v120,v105,v105
M 18,a[200:203],70
L 5
v_rcp_f32 v118,v118
M 18,a[208:211],74
s_mov_b32 s69,s68
v_fma_f32 v121,v121,v105,v105
v_mul_f32 v30,v30,v118
M 18,a[216:219],78
s_add_u32 s68,s68,s63
v_rcp_f32 v119,v119
M 22,a[212:215],74
v_rcp_f32 v120,v120
M 22,a[220:223],78
L 3
v_rcp_f32 v121,v121
M 22,a[228:231],82
L 0
v_mul_f32 v31,v31,v119
v_mul_f32 v32,v32,v120
v_mul_f32 v33,v33,v121
v_cvt_pk_bf16_f32 v124,v30,v31
v_cvt_pk_bf16_f32 v125,v32,v33
M 22,a[236:239],86
buffer_store_dwordx4 v[122:125],v15,s[36:39],s67 offen offset:0 sc0 sc1
v_xor_b32 v1,65536,v1
M 18,a[224:227],82
v_cmp_eq_u32 vcc,s53,v7
M 18,a[232:235],86
s_nop 0
M 18,a[240:243],90
buffer_store_dwordx4 v[122:125],v16,s[36:39],s67 offen offset:0 sc0 sc1
M 18,a[248:251],94
M 22,a[244:247],90
M 22,a[252:255],94
s_mov_b32 s80,1
s_cmp_eq_u32 vcc_lo,-1
s_cbranch_scc0 Lg1_xn0_%=
s_mov_b32 m0,s57
s_add_u32 s56,s55,4096
D 0,55,0
v_fma_f32 v18,-v98,v106,v18
v_fma_f32 v19,-v98,v107,v19
v_fma_f32 v20,-v98,v108,v20
v_fma_f32 v21,-v98,v109,v21
v_mul_f32 v114,v18,v18
v_mul_f32 v115,v19,v19
v_mul_f32 v116,v20,v20
D 0,55,1024
v_mul_f32 v117,v21,v21
v_fma_f32 v114,v100,v114,v99
v_fma_f32 v115,v100,v115,v99
v_fma_f32 v116,v100,v116,v99
v_fma_f32 v117,v100,v117,v99
v_mul_f32 v114,v18,v114
v_mul_f32 v115,v19,v115
D 0,55,2048
v_mul_f32 v116,v20,v116
v_mul_f32 v117,v21,v117
v_exp_f32 v114,v114
v_exp_f32 v115,v115
v_exp_f32 v116,v116
v_exp_f32 v117,v117
v_fma_f32 v114,v114,v101,v101
D 0,55,3072
v_fma_f32 v115,v115,v101,v101
v_fma_f32 v116,v116,v101,v101
v_fma_f32 v117,v117,v101,v101
v_rcp_f32 v114,v114
v_rcp_f32 v115,v115
v_rcp_f32 v116,v116
v_rcp_f32 v117,v117
s_add_u32 m0,s57,4096
s_nop 0
D 0,56,0
v_mul_f32 v18,v18,v114
v_mul_f32 v19,v19,v115
v_mul_f32 v20,v20,v116
v_mul_f32 v21,v21,v117
v_cvt_pk_bf16_f32 v122,v18,v19
v_cvt_pk_bf16_f32 v123,v20,v21
v_fma_f32 v22,-v98,v110,v22
D 0,56,1024
v_fma_f32 v23,-v98,v111,v23
v_fma_f32 v24,-v98,v112,v24
v_fma_f32 v25,-v98,v113,v25
v_mul_f32 v118,v22,v22
v_mul_f32 v119,v23,v23
v_mul_f32 v120,v24,v24
v_mul_f32 v121,v25,v25
D 0,56,2048
v_fma_f32 v118,v100,v118,v99
v_fma_f32 v119,v100,v119,v99
v_fma_f32 v120,v100,v120,v99
v_fma_f32 v121,v100,v121,v99
v_mul_f32 v118,v22,v118
v_mul_f32 v119,v23,v119
v_mul_f32 v120,v24,v120
D 0,56,3072
v_mul_f32 v121,v25,v121
v_exp_f32 v118,v118
v_exp_f32 v119,v119
v_exp_f32 v120,v120
v_exp_f32 v121,v121
v_fma_f32 v118,v118,v101,v101
v_fma_f32 v119,v119,v101,v101
s_add_u32 s55,s55,s60
s_add_u32 s57,s57,32768
s_and_b32 s57,s57,131071
s_add_u32 s54,s54,s46
v_fma_f32 v120,v120,v101,v101
v_fma_f32 v121,v121,v101,v101
v_rcp_f32 v118,v118
v_rcp_f32 v119,v119
v_rcp_f32 v120,v120
v_rcp_f32 v121,v121
v_mul_f32 v22,v22,v118
v_mul_f32 v23,v23,v119
v_mul_f32 v24,v24,v120
v_mul_f32 v25,v25,v121
v_cvt_pk_bf16_f32 v124,v22,v23
v_cvt_pk_bf16_f32 v125,v24,v25
buffer_store_dwordx4 v[122:125],v15,s[36:39],s66 offen offset:0 sc0 sc1
buffer_store_dwordx4 v[122:125],v16,s[36:39],s66 offen offset:0 sc0 sc1
s_mov_b32 s72,s69
s_mov_b32 s69,0x7fff0000
s_mov_b32 s66,0x7fff0000
s_mov_b32 s76,0x7fff0000
s_branch Lg1_gox_%=
Lg1_xn0_%=:
s_cmp_ge_u32 s54,256
s_cbranch_scc1 Lg1_fin0_%=
s_add_u32 m0,s59,4096
s_nop 0
buffer_load_dword v5,s[36:39],s58 offen offset:0 sc1 lds
buffer_store_dword v4,v3,s[36:39],s71 offen offset:0 sc0 sc1
s_add_u32 m0,s59,5120
s_nop 0
buffer_load_dword v5,s[36:39],s58 offen offset:0 sc1 lds
buffer_store_dword v4,v3,s[36:39],s71 offen offset:0 sc0 sc1
s_mov_b32 s83,0
v_fma_f32 v18,-v98,v106,v18
v_fma_f32 v19,-v98,v107,v19
v_fma_f32 v20,-v98,v108,v20
v_fma_f32 v21,-v98,v109,v21
v_mul_f32 v114,v18,v18
v_mul_f32 v115,v19,v19
v_mul_f32 v116,v20,v20
v_mul_f32 v117,v21,v21
v_fma_f32 v114,v100,v114,v99
v_fma_f32 v115,v100,v115,v99
v_fma_f32 v116,v100,v116,v99
v_fma_f32 v117,v100,v117,v99
v_mul_f32 v114,v18,v114
v_mul_f32 v115,v19,v115
v_mul_f32 v116,v20,v116
v_mul_f32 v117,v21,v117
v_exp_f32 v114,v114
v_exp_f32 v115,v115
v_exp_f32 v116,v116
v_exp_f32 v117,v117
v_fma_f32 v114,v114,v101,v101
v_fma_f32 v115,v115,v101,v101
v_fma_f32 v116,v116,v101,v101
v_fma_f32 v117,v117,v101,v101
v_rcp_f32 v114,v114
v_rcp_f32 v115,v115
v_rcp_f32 v116,v116
v_rcp_f32 v117,v117
v_mul_f32 v18,v18,v114
v_mul_f32 v19,v19,v115
v_mul_f32 v20,v20,v116
v_mul_f32 v21,v21,v117
v_cvt_pk_bf16_f32 v122,v18,v19
v_cvt_pk_bf16_f32 v123,v20,v21
v_fma_f32 v22,-v98,v110,v22
v_fma_f32 v23,-v98,v111,v23
v_fma_f32 v24,-v98,v112,v24
v_fma_f32 v25,-v98,v113,v25
v_mul_f32 v118,v22,v22
v_mul_f32 v119,v23,v23
v_mul_f32 v120,v24,v24
v_mul_f32 v121,v25,v25
v_fma_f32 v118,v100,v118,v99
v_fma_f32 v119,v100,v119,v99
v_fma_f32 v120,v100,v120,v99
v_fma_f32 v121,v100,v121,v99
v_mul_f32 v118,v22,v118
v_mul_f32 v119,v23,v119
v_mul_f32 v120,v24,v120
v_mul_f32 v121,v25,v121
v_exp_f32 v118,v118
v_exp_f32 v119,v119
v_exp_f32 v120,v120
v_exp_f32 v121,v121
v_fma_f32 v118,v118,v101,v101
v_fma_f32 v119,v119,v101,v101
v_fma_f32 v120,v120,v101,v101
v_fma_f32 v121,v121,v101,v101
v_rcp_f32 v118,v118
v_rcp_f32 v119,v119
v_rcp_f32 v120,v120
v_rcp_f32 v121,v121
v_mul_f32 v22,v22,v118
v_mul_f32 v23,v23,v119
v_mul_f32 v24,v24,v120
v_mul_f32 v25,v25,v121
v_cvt_pk_bf16_f32 v124,v22,v23
v_cvt_pk_bf16_f32 v125,v24,v25
buffer_store_dwordx4 v[122:125],v15,s[36:39],s66 offen offset:0 sc0 sc1
buffer_store_dwordx4 v[122:125],v16,s[36:39],s66 offen offset:0 sc0 sc1
s_mov_b32 s72,s69
s_mov_b32 s69,0x7fff0000
s_mov_b32 s66,0x7fff0000
)"
R"(s_mov_b32 s76,0x7fff0000
s_branch Lg1_poll_%=
Lg1_fin0_%=:
v_fma_f32 v18,-v98,v106,v18
v_fma_f32 v19,-v98,v107,v19
v_fma_f32 v20,-v98,v108,v20
v_fma_f32 v21,-v98,v109,v21
v_mul_f32 v114,v18,v18
v_mul_f32 v115,v19,v19
v_mul_f32 v116,v20,v20
v_mul_f32 v117,v21,v21
v_fma_f32 v114,v100,v114,v99
v_fma_f32 v115,v100,v115,v99
v_fma_f32 v116,v100,v116,v99
v_fma_f32 v117,v100,v117,v99
v_mul_f32 v114,v18,v114
v_mul_f32 v115,v19,v115
v_mul_f32 v116,v20,v116
v_mul_f32 v117,v21,v117
v_exp_f32 v114,v114
v_exp_f32 v115,v115
v_exp_f32 v116,v116
v_exp_f32 v117,v117
v_fma_f32 v114,v114,v101,v101
v_fma_f32 v115,v115,v101,v101
v_fma_f32 v116,v116,v101,v101
v_fma_f32 v117,v117,v101,v101
v_rcp_f32 v114,v114
v_rcp_f32 v115,v115
v_rcp_f32 v116,v116
v_rcp_f32 v117,v117
v_mul_f32 v18,v18,v114
v_mul_f32 v19,v19,v115
v_mul_f32 v20,v20,v116
v_mul_f32 v21,v21,v117
v_cvt_pk_bf16_f32 v122,v18,v19
v_cvt_pk_bf16_f32 v123,v20,v21
v_fma_f32 v22,-v98,v110,v22
v_fma_f32 v23,-v98,v111,v23
v_fma_f32 v24,-v98,v112,v24
v_fma_f32 v25,-v98,v113,v25
v_mul_f32 v118,v22,v22
v_mul_f32 v119,v23,v23
v_mul_f32 v120,v24,v24
v_mul_f32 v121,v25,v25
v_fma_f32 v118,v100,v118,v99
v_fma_f32 v119,v100,v119,v99
v_fma_f32 v120,v100,v120,v99
v_fma_f32 v121,v100,v121,v99
v_mul_f32 v118,v22,v118
v_mul_f32 v119,v23,v119
v_mul_f32 v120,v24,v120
v_mul_f32 v121,v25,v121
v_exp_f32 v118,v118
v_exp_f32 v119,v119
v_exp_f32 v120,v120
v_exp_f32 v121,v121
v_fma_f32 v118,v118,v101,v101
v_fma_f32 v119,v119,v101,v101
v_fma_f32 v120,v120,v101,v101
v_fma_f32 v121,v121,v101,v101
v_rcp_f32 v118,v118
v_rcp_f32 v119,v119
v_rcp_f32 v120,v120
v_rcp_f32 v121,v121
v_mul_f32 v22,v22,v118
v_mul_f32 v23,v23,v119
v_mul_f32 v24,v24,v120
v_mul_f32 v25,v25,v121
v_cvt_pk_bf16_f32 v124,v22,v23
v_cvt_pk_bf16_f32 v125,v24,v25
buffer_store_dwordx4 v[122:125],v15,s[36:39],s66 offen offset:0 sc0 sc1
buffer_store_dwordx4 v[122:125],v16,s[36:39],s66 offen offset:0 sc0 sc1
s_mov_b32 s72,s69
s_mov_b32 s69,0x7fff0000
s_mov_b32 s66,0x7fff0000
s_mov_b32 s76,0x7fff0000
s_branch Lg1_done_%=
Lg1_L21_%=:
v_mfma_f32_16x16x32_bf16 v[30:33],a[4:7],v[34:37],0
s_add_u32 s58,s58,s61
s_add_u32 s58,s58,s61
R 62,2,7168
M 30,a[12:15],38
s_add_u32 m0,s59,1024
buffer_load_dwordx4 v[102:105],v11,s[36:39],s73 offen offset:0 sc1
v_mfma_f32_16x16x32_bf16 v[26:29],a[0:3],v[34:37],0
s_add_u32 s56,s55,4096
R 66,2,8192
v_fma_f32 v18,-v98,v106,v18
M 26,a[8:11],38
L 5
buffer_load_dword v8,s[36:39],s58 offen offset:0 sc1 lds
M 26,a[16:19],42
s_mov_b32 m0,s57
R 70,2,9216
v_fma_f32 v19,-v98,v107,v19
M 26,a[24:27],46
s_add_u32 s54,s54,s46
v_fma_f32 v20,-v98,v108,v20
v_fma_f32 v21,-v98,v109,v21
M 30,a[20:23],42
s_mov_b32 s67,s65
R 74,2,10240
v_mul_f32 v114,v18,v18
M 30,a[28:31],46
L 5
D 0,55,0
M 30,a[36:39],50
s_add_u32 s73,s73,s64
R 78,2,11264
v_mul_f32 v115,v19,v19
M 30,a[44:47],54
s_add_u32 s54,s54,s46
v_mul_f32 v116,v20,v20
v_mul_f32 v117,v21,v21
M 26,a[32:35],50
s_add_u32 s65,s65,s62
R 82,2,12288
v_fma_f32 v114,v100,v114,v99
M 26,a[40:43],54
L 5
D 0,55,1024
M 26,a[48:51],58
R 86,2,13312
v_fma_f32 v115,v100,v115,v99
M 26,a[56:59],62
v_fma_f32 v116,v100,v116,v99
v_fma_f32 v117,v100,v117,v99
M 30,a[52:55],58
R 90,2,14336
v_mul_f32 v114,v18,v114
M 30,a[60:63],62
L 5
D 0,55,2048
M 30,a[68:71],66
R 94,2,15360
v_mul_f32 v115,v19,v115
M 30,a[76:79],70
v_exp_f32 v114,v114
M 26,a[64:67],66
R 34,2,16384
v_mul_f32 v116,v20,v116
M 26,a[72:75],70
L 5
D 0,55,3072
M 26,a[80:83],74
s_add_u32 m0,s57,4096
R 38,2,17408
v_mul_f32 v117,v21,v117
M 26,a[88:91],78
s_add_u32 s57,s57,32768
v_exp_f32 v115,v115
M 30,a[84:87],74
s_and_b32 s57,s57,131071
R 42,2,18432
v_fma_f32 v114,v114,v101,v101
M 30,a[92:95],78
L 5
D 0,56,0
M 30,a[100:103],82
s_add_u32 s55,s55,s60
R 46,2,19456
v_fma_f32 v115,v115,v101,v101
M 30,a[108:111],86
v_exp_f32 v116,v116
M 26,a[96:99],82
R 50,2,20480
v_fma_f32 v116,v116,v101,v101
M 26,a[104:107],86
L 5
D 0,56,1024
M 26,a[112:115],90
R 54,2,21504
v_fma_f32 v22,-v98,v110,v22
M 26,a[120:123],94
v_exp_f32 v117,v117
M 30,a[116:119],90
R 58,2,22528
v_fma_f32 v117,v117,v101,v101
M 30,a[124:127],94
L 5
D 0,56,2048
M 30,a[132:135],34
R 62,2,23552
v_fma_f32 v23,-v98,v111,v23
M 30,a[140:143],38
v_rcp_f32 v114,v114
M 26,a[128:131],34
R 66,2,24576
v_mul_f32 v18,v18,v114
M 26,a[136:139],38
L 5
D 0,56,3072
M 26,a[144:147],42
s_mov_b32 m0,s57
R 70,2,25600
v_fma_f32 v24,-v98,v112,v24
M 26,a[152:155],46
s_add_u32 s56,s55,4096
v_rcp_f32 v115,v115
M 30,a[148:151],42
R 74,2,26624
v_mul_f32 v19,v19,v115
M 30,a[156:159],46
L 5
D 0,55,0
M 30,a[164:167],50
R 78,2,27648
v_cvt_pk_bf16_f32 v122,v18,v19
M 30,a[172:175],54
D 0,55,1024
M 26,a[160:163],50
R 82,2,28672
v_fma_f32 v25,-v98,v113,v25
M 26,a[168:171],54
L 5
v_rcp_f32 v116,v116
M 26,a[176:179],58
R 86,2,29696
v_mul_f32 v20,v20,v116
M 26,a[184:187],62
D 0,55,2048
M 30,a[180:183],58
R 90,2,30720
v_mul_f32 v118,v22,v22
M 30,a[188:191],62
L 5
D 0,55,3072
M 30,a[196:199],66
s_waitcnt vmcnt(4)
R 94,2,31744
v_mul_f32 v119,v23,v23
M 30,a[204:207],70
s_barrier
buffer_store_dword v4,v3,s[36:39],s70 offen offset:0 sc0 sc1
M 26,a[192:195],66
s_add_u32 m0,s57,4096
R 34,1,0
ds_read_b32 v7,v6 offset:1024
M 26,a[200:203],70
L 6
D 0,56,0
M 26,a[208:211],74
s_mov_b32 s70,s69
R 38,1,1024
v_mul_f32 v120,v24,v24
M 26,a[216:219],78
s_add_u32 s57,s57,32768
D 0,56,1024
M 30,a[212:215],74
s_mov_b32 s69,s68
R 42,1,2048
v_mul_f32 v121,v25,v25
v_fma_f32 v118,v100,v118,v99
v_fma_f32 v119,v100,v119,v99
v_fma_f32 v120,v100,v120,v99
v_fma_f32 v121,v100,v121,v99
v_mul_f32 v118,v22,v118
v_mul_f32 v119,v23,v119
v_mul_f32 v120,v24,v120
v_mul_f32 v121,v25,v121
v_exp_f32 v118,v118
v_exp_f32 v119,v119
v_exp_f32 v120,v120
v_exp_f32 v121,v121
M 30,a[220:223],78
L 6
v_rcp_f32 v117,v117
v_fma_f32 v118,v118,v101,v101
v_fma_f32 v119,v119,v101,v101
v_fma_f32 v120,v120,v101,v101
v_fma_f32 v121,v121,v101,v101
v_rcp_f32 v118,v118
v_rcp_f32 v119,v119
v_rcp_f32 v120,v120
v_rcp_f32 v121,v121
M 30,a[228:231],82
L 2
R 46,1,3072
v_mul_f32 v21,v21,v117
v_cvt_pk_bf16_f32 v123,v20,v21
v_mul_f32 v22,v22,v118
v_mul_f32 v23,v23,v119
v_mul_f32 v24,v24,v120
v_mul_f32 v25,v25,v121
v_cvt_pk_bf16_f32 v124,v22,v23
v_cvt_pk_bf16_f32 v125,v24,v25
M 30,a[236:239],86
s_add_u32 s55,s55,s60
buffer_store_dwordx4 v[122:125],v15,s[36:39],s66 offen offset:0 sc0 sc1
v_xor_b32 v2,65536,v2
M 26,a[224:227],82
s_and_b32 s57,s57,131071
R 50,1,4096
v_cmp_eq_u32 vcc,s53,v7
M 26,a[232:235],86
s_nop 0
s_add_u32 s68,s68,s63
s_cmp_eq_u32 vcc_lo,-1
M 26,a[240:243],90
R 54,1,5120
M 26,a[248:251],94
buffer_store_dwordx4 v[122:125],v16,s[36:39],s66 offen offset:0 sc0 sc1
M 30,a[244:247],90
R 58,1,6144
M 30,a[252:255],94
L 5
D 0,56,2048
D 0,56,3072
s_cbranch_scc1 Lg1_S0_%=
Lg1_N0_%=:
v_mfma_f32_16x16x32_bf16 v[22:25],a[4:7],v[34:37],0
s_add_u32 m0,s59,0
s_mov_b32 s66,s65
R 62,1,7168
M 22,a[12:15],38
s_add_u32 s65,s65,s62
buffer_load_dword v5,s[36:39],s58 offen offset:0 sc1 lds
v_mfma_f32_16x16x32_bf16 v[18:21],a[0:3],v[34:37],0
R 66,1,8192
v_fma_f32 v26,-v102,v106,v26
M 18,a[8:11],38
L 5
buffer_load_dwordx4 v[98:101],v11,s[36:39],s73 offen offset:0 sc1
M 18,a[16:19],42
s_add_u32 s73,s73,s64
R 70,1,9216
v_fma_f32 v27,-v102,v107,v27
M 18,a[24:27],46
v_fma_f32 v28,-v102,v108,v28
v_fma_f32 v29,-v102,v109,v29
M 22,a[20:23],42
R 74,1,10240
v_mul_f32 v114,v26,v26
M 22,a[28:31],46
L 5
v_mul_f32 v115,v27,v27
v_mul_f32 v116,v28,v28
M 22,a[36:39],50
R 78,1,11264
v_mul_f32 v117,v29,v29
M 22,a[44:47],54
v_fma_f32 v114,v104,v114,v103
v_fma_f32 v115,v104,v115,v103
M 18,a[32:35],50
R 82,1,12288
v_fma_f32 v116,v104,v116,v103
M 18,a[40:43],54
L 5
v_fma_f32 v117,v104,v117,v103
v_mul_f32 v114,v26,v114
M 18,a[48:51],58
R 86,1,13312
v_mul_f32 v115,v27,v115
M 18,a[56:59],62
v_exp_f32 v114,v114
M 22,a[52:55],58
R 90,1,14336
v_mul_f32 v116,v28,v116
M 22,a[60:63],62
L 5
v_exp_f32 v115,v115
M 22,a[68:71],66
R 94,1,15360
v_mul_f32 v117,v29,v117
M 22,a[76:79],70
v_exp_f32 v116,v116
M 18,a[64:67],66
R 34,1,16384
v_fma_f32 v114,v114,v105,v105
M 18,a[72:75],70
L 5
v_exp_f32 v117,v117
M 18,a[80:83],74
R 38,1,17408
v_fma_f32 v115,v115,v105,v105
M 18,a[88:91],78
v_rcp_f32 v114,v114
M 22,a[84:87],74
R 42,1,18432
v_fma_f32 v116,v116,v105,v105
M 22,a[92:95],78
L 5
v_rcp_f32 v115,v115
M 22,a[100:103],82
R 46,1,19456
v_fma_f32 v117,v117,v105,v105
M 22,a[108:111],86
v_rcp_f32 v116,v116
M 18,a[96:99],82
R 50,1,20480
v_mul_f32 v26,v26,v114
M 18,a[104:107],86
L 5
v_rcp_f32 v117,v117
M 18,a[112:115],90
R 54,1,21504
v_mul_f32 v27,v27,v115
M 18,a[120:123],94
v_mul_f32 v28,v28,v116
v_mul_f32 v29,v29,v117
M 22,a[116:119],90
R 58,1,22528
v_cvt_pk_bf16_f32 v122,v26,v27
M 22,a[124:127],94
L 5
v_cvt_pk_bf16_f32 v123,v28,v29
v_fma_f32 v30,-v102,v110,v30
M 22,a[132:135],34
R 62,1,23552
v_fma_f32 v31,-v102,v111,v31
M 22,a[140:143],38
v_fma_f32 v32,-v102,v112,v32
v_fma_f32 v33,-v102,v113,v33
M 18,a[128:131],34
R 66,1,24576
v_mul_f32 v118,v30,v30
M 18,a[136:139],38
L 5
v_mul_f32 v119,v31,v31
v_mul_f32 v120,v32,v32
M 18,a[144:147],42
R 70,1,25600
v_mul_f32 v121,v33,v33
M 18,a[152:155],46
v_fma_f32 v118,v104,v118,v103
v_fma_f32 v119,v104,v119,v103
M 22,a[148:151],42
R 74,1,26624
v_fma_f32 v120,v104,v120,v103
M 22,a[156:159],46
L 5
v_fma_f32 v121,v104,v121,v103
v_mul_f32 v118,v30,v118
v_mul_f32 v119,v31,v119
M 22,a[164:167],50
R 78,1,27648
v_mul_f32 v120,v32,v120
M 22,a[172:175],54
v_exp_f32 v118,v118
M 18,a[160:163],50
R 82,1,28672
v_mul_f32 v121,v33,v121
M 18,a[168:171],54
L 5
v_exp_f32 v119,v119
M 18,a[176:179],58
R 86,1,29696
v_fma_f32 v118,v118,v105,v105
M 18,a[184:187],62
v_exp_f32 v120,v120
M 22,a[180:183],58
R 90,1,30720
v_fma_f32 v119,v119,v105,v105
M 22,a[188:191],62
L 5
v_exp_f32 v121,v121
M 22,a[196:199],66
s_waitcnt vmcnt(0)
R 94,1,31744
v_fma_f32 v120,v120,v105,v105
M 22,a[204:207],70
s_barrier
buffer_store_dword v4,v3,s[36:39],s70 offen offset:0 sc0 sc1
M 18,a[192:195],66
s_mov_b32 s70,s69
R 34,2,0
ds_read_b32 v7,v6 offset:0
M 18,a[200:203],70
L 6
v_rcp_f32 v118,v118
M 18,a[208:211],74
s_mov_b32 s69,s68
R 38,2,1024
v_fma_f32 v121,v121,v105,v105
M 18,a[216:219],78
s_add_u32 s68,s68,s63
v_rcp_f32 v119,v119
M 22,a[212:215],74
R 42,2,2048
v_mul_f32 v30,v30,v118
M 22,a[220:223],78
L 6
v_rcp_f32 v120,v120
v_rcp_f32 v121,v121
M 22,a[228:231],82
L 2
R 46,2,3072
v_mul_f32 v31,v31,v119
v_mul_f32 v32,v32,v120
v_mul_f32 v33,v33,v121
v_cvt_pk_bf16_f32 v124,v30,v31
v_cvt_pk_bf16_f32 v125,v32,v33
M 22,a[236:239],86
buffer_store_dwordx4 v[122:125],v15,s[36:39],s67 offen offset:0 sc0 sc1
v_xor_b32 v1,65536,v1
M 18,a[224:227],82
R 50,2,4096
v_cmp_eq_u32 vcc,s53,v7
)"
R"(M 18,a[232:235],86
s_nop 0
s_cmp_eq_u64 vcc,-1
M 18,a[240:243],90
R 54,2,5120
M 18,a[248:251],94
buffer_store_dwordx4 v[122:125],v16,s[36:39],s67 offen offset:0 sc0 sc1
M 22,a[244:247],90
R 58,2,6144
M 22,a[252:255],94
L 5
s_cbranch_scc1 Lg1_L21_%=
s_cmp_eq_u32 vcc_lo,-1
s_cbranch_scc1 Lg1_L11_%=
Lg1_X1_%=:
v_mfma_f32_16x16x32_bf16 v[30:33],a[4:7],v[34:37],0
s_add_u32 m0,s59,1024
s_mov_b32 s67,s65
R 62,2,7168
M 30,a[12:15],38
s_add_u32 s65,s65,s62
buffer_load_dword v5,s[36:39],s58 offen offset:0 sc1 lds
v_mfma_f32_16x16x32_bf16 v[26:29],a[0:3],v[34:37],0
R 66,2,8192
v_fma_f32 v18,-v98,v106,v18
M 26,a[8:11],38
L 5
buffer_load_dwordx4 v[102:105],v11,s[36:39],s73 offen offset:0 sc1
M 26,a[16:19],42
s_add_u32 s73,s73,s64
R 70,2,9216
v_fma_f32 v19,-v98,v107,v19
M 26,a[24:27],46
v_fma_f32 v20,-v98,v108,v20
v_fma_f32 v21,-v98,v109,v21
M 30,a[20:23],42
R 74,2,10240
v_mul_f32 v114,v18,v18
M 30,a[28:31],46
L 5
v_mul_f32 v115,v19,v19
v_mul_f32 v116,v20,v20
M 30,a[36:39],50
R 78,2,11264
v_mul_f32 v117,v21,v21
M 30,a[44:47],54
v_fma_f32 v114,v100,v114,v99
v_fma_f32 v115,v100,v115,v99
M 26,a[32:35],50
R 82,2,12288
v_fma_f32 v116,v100,v116,v99
M 26,a[40:43],54
L 5
v_fma_f32 v117,v100,v117,v99
v_mul_f32 v114,v18,v114
M 26,a[48:51],58
R 86,2,13312
v_mul_f32 v115,v19,v115
M 26,a[56:59],62
v_exp_f32 v114,v114
M 30,a[52:55],58
R 90,2,14336
v_mul_f32 v116,v20,v116
M 30,a[60:63],62
L 5
v_exp_f32 v115,v115
M 30,a[68:71],66
R 94,2,15360
v_mul_f32 v117,v21,v117
M 30,a[76:79],70
v_exp_f32 v116,v116
M 26,a[64:67],66
R 34,2,16384
v_fma_f32 v114,v114,v101,v101
M 26,a[72:75],70
L 5
v_exp_f32 v117,v117
M 26,a[80:83],74
R 38,2,17408
v_fma_f32 v115,v115,v101,v101
M 26,a[88:91],78
v_rcp_f32 v114,v114
M 30,a[84:87],74
R 42,2,18432
v_fma_f32 v116,v116,v101,v101
M 30,a[92:95],78
L 5
v_rcp_f32 v115,v115
M 30,a[100:103],82
R 46,2,19456
v_fma_f32 v117,v117,v101,v101
M 30,a[108:111],86
v_rcp_f32 v116,v116
M 26,a[96:99],82
R 50,2,20480
v_mul_f32 v18,v18,v114
M 26,a[104:107],86
L 5
v_rcp_f32 v117,v117
M 26,a[112:115],90
R 54,2,21504
v_mul_f32 v19,v19,v115
M 26,a[120:123],94
v_mul_f32 v20,v20,v116
v_mul_f32 v21,v21,v117
M 30,a[116:119],90
R 58,2,22528
v_cvt_pk_bf16_f32 v122,v18,v19
M 30,a[124:127],94
L 5
v_cvt_pk_bf16_f32 v123,v20,v21
v_fma_f32 v22,-v98,v110,v22
M 30,a[132:135],34
R 62,2,23552
v_fma_f32 v23,-v98,v111,v23
M 30,a[140:143],38
v_fma_f32 v24,-v98,v112,v24
v_fma_f32 v25,-v98,v113,v25
M 26,a[128:131],34
R 66,2,24576
v_mul_f32 v118,v22,v22
M 26,a[136:139],38
L 5
v_mul_f32 v119,v23,v23
v_mul_f32 v120,v24,v24
M 26,a[144:147],42
R 70,2,25600
v_mul_f32 v121,v25,v25
M 26,a[152:155],46
v_fma_f32 v118,v100,v118,v99
v_fma_f32 v119,v100,v119,v99
M 30,a[148:151],42
R 74,2,26624
v_fma_f32 v120,v100,v120,v99
M 30,a[156:159],46
L 5
v_fma_f32 v121,v100,v121,v99
v_mul_f32 v118,v22,v118
M 30,a[164:167],50
R 78,2,27648
v_mul_f32 v119,v23,v119
M 30,a[172:175],54
v_exp_f32 v118,v118
M 26,a[160:163],50
R 82,2,28672
v_mul_f32 v120,v24,v120
M 26,a[168:171],54
L 5
v_exp_f32 v119,v119
M 26,a[176:179],58
R 86,2,29696
v_mul_f32 v121,v25,v121
M 26,a[184:187],62
v_exp_f32 v120,v120
M 30,a[180:183],58
R 90,2,30720
v_fma_f32 v118,v118,v101,v101
M 30,a[188:191],62
L 5
v_exp_f32 v121,v121
M 30,a[196:199],66
s_waitcnt vmcnt(0)
R 94,2,31744
v_fma_f32 v119,v119,v101,v101
M 30,a[204:207],70
s_barrier
buffer_store_dword v4,v3,s[36:39],s70 offen offset:0 sc0 sc1
M 26,a[192:195],66
s_mov_b32 s70,s69
ds_read_b32 v7,v6 offset:1024
v_fma_f32 v120,v120,v101,v101
M 26,a[200:203],70
L 5
v_rcp_f32 v118,v118
M 26,a[208:211],74
s_mov_b32 s69,s68
v_fma_f32 v121,v121,v101,v101
v_mul_f32 v22,v22,v118
M 26,a[216:219],78
s_add_u32 s68,s68,s63
v_rcp_f32 v119,v119
M 30,a[212:215],74
v_rcp_f32 v120,v120
M 30,a[220:223],78
L 3
v_rcp_f32 v121,v121
M 30,a[228:231],82
L 0
v_mul_f32 v23,v23,v119
v_mul_f32 v24,v24,v120
v_mul_f32 v25,v25,v121
v_cvt_pk_bf16_f32 v124,v22,v23
v_cvt_pk_bf16_f32 v125,v24,v25
M 30,a[236:239],86
buffer_store_dwordx4 v[122:125],v15,s[36:39],s66 offen offset:0 sc0 sc1
v_xor_b32 v2,65536,v2
M 26,a[224:227],82
v_cmp_eq_u32 vcc,s53,v7
M 26,a[232:235],86
s_nop 0
M 26,a[240:243],90
buffer_store_dwordx4 v[122:125],v16,s[36:39],s66 offen offset:0 sc0 sc1
M 26,a[248:251],94
M 30,a[244:247],90
M 30,a[252:255],94
s_mov_b32 s80,0
s_cmp_eq_u32 vcc_lo,-1
s_cbranch_scc0 Lg1_xn1_%=
s_mov_b32 m0,s57
s_add_u32 s56,s55,4096
D 0,55,0
v_fma_f32 v26,-v102,v106,v26
v_fma_f32 v27,-v102,v107,v27
v_fma_f32 v28,-v102,v108,v28
v_fma_f32 v29,-v102,v109,v29
v_mul_f32 v114,v26,v26
v_mul_f32 v115,v27,v27
v_mul_f32 v116,v28,v28
D 0,55,1024
v_mul_f32 v117,v29,v29
v_fma_f32 v114,v104,v114,v103
v_fma_f32 v115,v104,v115,v103
v_fma_f32 v116,v104,v116,v103
v_fma_f32 v117,v104,v117,v103
v_mul_f32 v114,v26,v114
v_mul_f32 v115,v27,v115
D 0,55,2048
v_mul_f32 v116,v28,v116
v_mul_f32 v117,v29,v117
v_exp_f32 v114,v114
v_exp_f32 v115,v115
v_exp_f32 v116,v116
v_exp_f32 v117,v117
v_fma_f32 v114,v114,v105,v105
D 0,55,3072
v_fma_f32 v115,v115,v105,v105
v_fma_f32 v116,v116,v105,v105
v_fma_f32 v117,v117,v105,v105
v_rcp_f32 v114,v114
v_rcp_f32 v115,v115
v_rcp_f32 v116,v116
v_rcp_f32 v117,v117
s_add_u32 m0,s57,4096
s_nop 0
D 0,56,0
v_mul_f32 v26,v26,v114
v_mul_f32 v27,v27,v115
v_mul_f32 v28,v28,v116
v_mul_f32 v29,v29,v117
v_cvt_pk_bf16_f32 v122,v26,v27
v_cvt_pk_bf16_f32 v123,v28,v29
v_fma_f32 v30,-v102,v110,v30
D 0,56,1024
v_fma_f32 v31,-v102,v111,v31
v_fma_f32 v32,-v102,v112,v32
v_fma_f32 v33,-v102,v113,v33
v_mul_f32 v118,v30,v30
v_mul_f32 v119,v31,v31
v_mul_f32 v120,v32,v32
v_mul_f32 v121,v33,v33
D 0,56,2048
v_fma_f32 v118,v104,v118,v103
v_fma_f32 v119,v104,v119,v103
v_fma_f32 v120,v104,v120,v103
v_fma_f32 v121,v104,v121,v103
v_mul_f32 v118,v30,v118
v_mul_f32 v119,v31,v119
v_mul_f32 v120,v32,v120
D 0,56,3072
v_mul_f32 v121,v33,v121
v_exp_f32 v118,v118
v_exp_f32 v119,v119
v_exp_f32 v120,v120
v_exp_f32 v121,v121
v_fma_f32 v118,v118,v105,v105
v_fma_f32 v119,v119,v105,v105
s_add_u32 s55,s55,s60
s_add_u32 s57,s57,32768
s_and_b32 s57,s57,131071
s_add_u32 s54,s54,s46
v_fma_f32 v120,v120,v105,v105
v_fma_f32 v121,v121,v105,v105
v_rcp_f32 v118,v118
v_rcp_f32 v119,v119
v_rcp_f32 v120,v120
v_rcp_f32 v121,v121
v_mul_f32 v30,v30,v118
v_mul_f32 v31,v31,v119
v_mul_f32 v32,v32,v120
v_mul_f32 v33,v33,v121
v_cvt_pk_bf16_f32 v124,v30,v31
v_cvt_pk_bf16_f32 v125,v32,v33
buffer_store_dwordx4 v[122:125],v15,s[36:39],s67 offen offset:0 sc0 sc1
buffer_store_dwordx4 v[122:125],v16,s[36:39],s67 offen offset:0 sc0 sc1
s_mov_b32 s72,s69
s_mov_b32 s69,0x7fff0000
s_mov_b32 s67,0x7fff0000
s_mov_b32 s77,0x7fff0000
s_branch Lg1_gox_%=
Lg1_xn1_%=:
s_cmp_ge_u32 s54,256
s_cbranch_scc1 Lg1_fin1_%=
s_add_u32 m0,s59,4096
s_nop 0
buffer_load_dword v5,s[36:39],s58 offen offset:0 sc1 lds
buffer_store_dword v4,v3,s[36:39],s71 offen offset:0 sc0 sc1
s_add_u32 m0,s59,5120
s_nop 0
buffer_load_dword v5,s[36:39],s58 offen offset:0 sc1 lds
buffer_store_dword v4,v3,s[36:39],s71 offen offset:0 sc0 sc1
s_mov_b32 s83,0
v_fma_f32 v26,-v102,v106,v26
v_fma_f32 v27,-v102,v107,v27
v_fma_f32 v28,-v102,v108,v28
v_fma_f32 v29,-v102,v109,v29
v_mul_f32 v114,v26,v26
v_mul_f32 v115,v27,v27
v_mul_f32 v116,v28,v28
v_mul_f32 v117,v29,v29
v_fma_f32 v114,v104,v114,v103
v_fma_f32 v115,v104,v115,v103
v_fma_f32 v116,v104,v116,v103
v_fma_f32 v117,v104,v117,v103
v_mul_f32 v114,v26,v114
v_mul_f32 v115,v27,v115
v_mul_f32 v116,v28,v116
v_mul_f32 v117,v29,v117
v_exp_f32 v114,v114
v_exp_f32 v115,v115
v_exp_f32 v116,v116
v_exp_f32 v117,v117
v_fma_f32 v114,v114,v105,v105
v_fma_f32 v115,v115,v105,v105
v_fma_f32 v116,v116,v105,v105
v_fma_f32 v117,v117,v105,v105
v_rcp_f32 v114,v114
v_rcp_f32 v115,v115
v_rcp_f32 v116,v116
v_rcp_f32 v117,v117
v_mul_f32 v26,v26,v114
v_mul_f32 v27,v27,v115
v_mul_f32 v28,v28,v116
v_mul_f32 v29,v29,v117
v_cvt_pk_bf16_f32 v122,v26,v27
v_cvt_pk_bf16_f32 v123,v28,v29
v_fma_f32 v30,-v102,v110,v30
v_fma_f32 v31,-v102,v111,v31
v_fma_f32 v32,-v102,v112,v32
v_fma_f32 v33,-v102,v113,v33
v_mul_f32 v118,v30,v30
v_mul_f32 v119,v31,v31
v_mul_f32 v120,v32,v32
v_mul_f32 v121,v33,v33
v_fma_f32 v118,v104,v118,v103
v_fma_f32 v119,v104,v119,v103
v_fma_f32 v120,v104,v120,v103
v_fma_f32 v121,v104,v121,v103
v_mul_f32 v118,v30,v118
v_mul_f32 v119,v31,v119
v_mul_f32 v120,v32,v120
v_mul_f32 v121,v33,v121
v_exp_f32 v118,v118
v_exp_f32 v119,v119
v_exp_f32 v120,v120
v_exp_f32 v121,v121
v_fma_f32 v118,v118,v105,v105
v_fma_f32 v119,v119,v105,v105
v_fma_f32 v120,v120,v105,v105
v_fma_f32 v121,v121,v105,v105
v_rcp_f32 v118,v118
v_rcp_f32 v119,v119
v_rcp_f32 v120,v120
v_rcp_f32 v121,v121
v_mul_f32 v30,v30,v118
v_mul_f32 v31,v31,v119
v_mul_f32 v32,v32,v120
v_mul_f32 v33,v33,v121
v_cvt_pk_bf16_f32 v124,v30,v31
v_cvt_pk_bf16_f32 v125,v32,v33
buffer_store_dwordx4 v[122:125],v15,s[36:39],s67 offen offset:0 sc0 sc1
buffer_store_dwordx4 v[122:125],v16,s[36:39],s67 offen offset:0 sc0 sc1
s_mov_b32 s72,s69
s_mov_b32 s69,0x7fff0000
s_mov_b32 s67,0x7fff0000
s_mov_b32 s77,0x7fff0000
s_branch Lg1_poll_%=
Lg1_fin1_%=:
v_fma_f32 v26,-v102,v106,v26
v_fma_f32 v27,-v102,v107,v27
v_fma_f32 v28,-v102,v108,v28
v_fma_f32 v29,-v102,v109,v29
v_mul_f32 v114,v26,v26
v_mul_f32 v115,v27,v27
v_mul_f32 v116,v28,v28
v_mul_f32 v117,v29,v29
v_fma_f32 v114,v104,v114,v103
v_fma_f32 v115,v104,v115,v103
v_fma_f32 v116,v104,v116,v103
v_fma_f32 v117,v104,v117,v103
v_mul_f32 v114,v26,v114
v_mul_f32 v115,v27,v115
v_mul_f32 v116,v28,v116
v_mul_f32 v117,v29,v117
v_exp_f32 v114,v114
v_exp_f32 v115,v115
v_exp_f32 v116,v116
v_exp_f32 v117,v117
v_fma_f32 v114,v114,v105,v105
v_fma_f32 v115,v115,v105,v105
v_fma_f32 v116,v116,v105,v105
v_fma_f32 v117,v117,v105,v105
v_rcp_f32 v114,v114
v_rcp_f32 v115,v115
v_rcp_f32 v116,v116
v_rcp_f32 v117,v117
v_mul_f32 v26,v26,v114
v_mul_f32 v27,v27,v115
v_mul_f32 v28,v28,v116
v_mul_f32 v29,v29,v117
v_cvt_pk_bf16_f32 v122,v26,v27
v_cvt_pk_bf16_f32 v123,v28,v29
v_fma_f32 v30,-v102,v110,v30
v_fma_f32 v31,-v102,v111,v31
v_fma_f32 v32,-v102,v112,v32
v_fma_f32 v33,-v102,v113,v33
v_mul_f32 v118,v30,v30
v_mul_f32 v119,v31,v31
v_mul_f32 v120,v32,v32
v_mul_f32 v121,v33,v33
v_fma_f32 v118,v104,v118,v103
v_fma_f32 v119,v104,v119,v103
v_fma_f32 v120,v104,v120,v103
v_fma_f32 v121,v104,v121,v103
v_mul_f32 v118,v30,v118
v_mul_f32 v119,v31,v119
v_mul_f32 v120,v32,v120
v_mul_f32 v121,v33,v121
v_exp_f32 v118,v118
v_exp_f32 v119,v119
v_exp_f32 v120,v120
v_exp_f32 v121,v121
v_fma_f32 v118,v118,v105,v105
v_fma_f32 v119,v119,v105,v105
v_fma_f32 v120,v120,v105,v105
v_fma_f32 v121,v121,v105,v105
v_rcp_f32 v118,v118
v_rcp_f32 v119,v119
v_rcp_f32 v120,v120
v_rcp_f32 v121,v121
v_mul_f32 v30,v30,v118
v_mul_f32 v31,v31,v119
v_mul_f32 v32,v32,v120
v_mul_f32 v33,v33,v121
v_cvt_pk_bf16_f32 v124,v30,v31
v_cvt_pk_bf16_f32 v125,v32,v33
buffer_store_dwordx4 v[122:125],v15,s[36:39],s67 offen offset:0 sc0 sc1
buffer_store_dwordx4 v[122:125],v16,s[36:39],s67 offen offset:0 sc0 sc1
s_mov_b32 s72,s69
s_mov_b32 s69,0x7fff0000
s_mov_b32 s67,0x7fff0000
s_mov_b32 s77,0x7fff0000
s_branch Lg1_done_%=
Lg1_L10_%=:
v_mfma_f32_16x16x32_bf16 v[22:25],a[4:7],v[34:37],0
s_add_u32 s58,s58,s61
s_add_u32 m0,s59,0
R 62,1,7168
M 22,a[12:15],38
s_add_u32 s56,s55,4096
buffer_load_dword v5,s[36:39],s58 offen offset:0 sc1 lds
v_mfma_f32_16x16x32_bf16 v[18:21],a[0:3],v[34:37],0
s_mov_b32 m0,s57
R 66,1,8192
v_fma_f32 v26,-v102,v106,v26
M 18,a[8:11],38
L 5
buffer_load_dwordx4 v[98:101],v11,s[36:39],s73 offen offset:0 sc1
M 18,a[16:19],42
s_mov_b32 s66,s65
R 70,1,9216
v_fma_f32 v27,-v102,v107,v27
M 18,a[24:27],46
s_add_u32 s73,s73,s64
v_fma_f32 v28,-v102,v108,v28
v_fma_f32 v29,-v102,v109,v29
M 22,a[20:23],42
s_add_u32 s54,s54,s46
R 74,1,10240
v_mul_f32 v114,v26,v26
M 22,a[28:31],46
L 5
D 0,55,0
M 22,a[36:39],50
s_add_u32 s65,s65,s62
R 78,1,11264
v_mul_f32 v115,v27,v27
M 22,a[44:47],54
v_mul_f32 v116,v28,v28
v_mul_f32 v117,v29,v29
M 18,a[32:35],50
)"
R"(R 82,1,12288
v_fma_f32 v114,v104,v114,v103
M 18,a[40:43],54
L 5
D 0,55,1024
M 18,a[48:51],58
R 86,1,13312
v_fma_f32 v115,v104,v115,v103
M 18,a[56:59],62
v_fma_f32 v116,v104,v116,v103
v_fma_f32 v117,v104,v117,v103
M 22,a[52:55],58
R 90,1,14336
v_mul_f32 v114,v26,v114
M 22,a[60:63],62
L 5
D 0,55,2048
M 22,a[68:71],66
R 94,1,15360
v_mul_f32 v115,v27,v115
M 22,a[76:79],70
v_exp_f32 v114,v114
M 18,a[64:67],66
R 34,1,16384
v_mul_f32 v116,v28,v116
M 18,a[72:75],70
L 5
D 0,55,3072
M 18,a[80:83],74
s_add_u32 m0,s57,4096
R 38,1,17408
v_mul_f32 v117,v29,v117
M 18,a[88:91],78
s_add_u32 s57,s57,32768
v_exp_f32 v115,v115
M 22,a[84:87],74
s_add_u32 s55,s55,s60
R 42,1,18432
v_fma_f32 v114,v114,v105,v105
M 22,a[92:95],78
L 5
D 0,56,0
M 22,a[100:103],82
s_and_b32 s57,s57,131071
R 46,1,19456
v_fma_f32 v115,v115,v105,v105
M 22,a[108:111],86
v_exp_f32 v116,v116
M 18,a[96:99],82
R 50,1,20480
v_fma_f32 v116,v116,v105,v105
M 18,a[104:107],86
L 5
D 0,56,1024
M 18,a[112:115],90
R 54,1,21504
v_fma_f32 v30,-v102,v110,v30
M 18,a[120:123],94
v_exp_f32 v117,v117
M 22,a[116:119],90
R 58,1,22528
v_fma_f32 v117,v117,v105,v105
M 22,a[124:127],94
L 5
D 0,56,2048
M 22,a[132:135],34
R 62,1,23552
v_fma_f32 v31,-v102,v111,v31
M 22,a[140:143],38
v_rcp_f32 v114,v114
M 18,a[128:131],34
R 66,1,24576
v_mul_f32 v26,v26,v114
M 18,a[136:139],38
L 5
D 0,56,3072
M 18,a[144:147],42
R 70,1,25600
v_fma_f32 v32,-v102,v112,v32
M 18,a[152:155],46
v_rcp_f32 v115,v115
M 22,a[148:151],42
R 74,1,26624
v_mul_f32 v27,v27,v115
M 22,a[156:159],46
L 5
v_rcp_f32 v116,v116
M 22,a[164:167],50
R 78,1,27648
v_mul_f32 v28,v28,v116
M 22,a[172:175],54
v_rcp_f32 v117,v117
M 18,a[160:163],50
R 82,1,28672
v_mul_f32 v29,v29,v117
M 18,a[168:171],54
L 5
v_cvt_pk_bf16_f32 v122,v26,v27
v_cvt_pk_bf16_f32 v123,v28,v29
M 18,a[176:179],58
R 86,1,29696
v_fma_f32 v33,-v102,v113,v33
M 18,a[184:187],62
v_mul_f32 v118,v30,v30
v_mul_f32 v119,v31,v31
M 22,a[180:183],58
R 90,1,30720
v_mul_f32 v120,v32,v32
M 22,a[188:191],62
L 5
v_mul_f32 v121,v33,v33
v_fma_f32 v118,v104,v118,v103
M 22,a[196:199],66
s_waitcnt vmcnt(0)
R 94,1,31744
v_fma_f32 v119,v104,v119,v103
M 22,a[204:207],70
s_barrier
buffer_store_dword v4,v3,s[36:39],s70 offen offset:0 sc0 sc1
M 18,a[192:195],66
s_mov_b32 s70,s69
R 34,2,0
ds_read_b32 v7,v6 offset:0
M 18,a[200:203],70
L 6
v_fma_f32 v120,v104,v120,v103
v_fma_f32 v121,v104,v121,v103
M 18,a[208:211],74
s_mov_b32 s69,s68
R 38,2,1024
v_mul_f32 v118,v30,v118
M 18,a[216:219],78
s_add_u32 s68,s68,s63
v_mul_f32 v119,v31,v119
v_mul_f32 v120,v32,v120
M 22,a[212:215],74
R 42,2,2048
v_mul_f32 v121,v33,v121
v_exp_f32 v118,v118
v_exp_f32 v119,v119
v_exp_f32 v120,v120
v_exp_f32 v121,v121
M 22,a[220:223],78
L 6
v_fma_f32 v118,v118,v105,v105
v_fma_f32 v119,v119,v105,v105
v_fma_f32 v120,v120,v105,v105
v_fma_f32 v121,v121,v105,v105
v_rcp_f32 v118,v118
v_rcp_f32 v119,v119
v_rcp_f32 v120,v120
v_rcp_f32 v121,v121
M 22,a[228:231],82
L 2
R 46,2,3072
v_mul_f32 v30,v30,v118
v_mul_f32 v31,v31,v119
v_mul_f32 v32,v32,v120
v_mul_f32 v33,v33,v121
v_cvt_pk_bf16_f32 v124,v30,v31
v_cvt_pk_bf16_f32 v125,v32,v33
M 22,a[236:239],86
buffer_store_dwordx4 v[122:125],v15,s[36:39],s67 offen offset:0 sc0 sc1
v_xor_b32 v1,65536,v1
M 18,a[224:227],82
R 50,2,4096
v_cmp_eq_u32 vcc,s53,v7
M 18,a[232:235],86
s_nop 0
s_cmp_eq_u64 vcc,-1
M 18,a[240:243],90
R 54,2,5120
M 18,a[248:251],94
buffer_store_dwordx4 v[122:125],v16,s[36:39],s67 offen offset:0 sc0 sc1
M 22,a[244:247],90
R 58,2,6144
M 22,a[252:255],94
L 5
s_cbranch_scc1 Lg1_L21_%=
s_cmp_eq_u32 vcc_lo,-1
s_cbranch_scc1 Lg1_L11_%=
s_branch Lg1_X1_%=
Lg1_L11_%=:
v_mfma_f32_16x16x32_bf16 v[30:33],a[4:7],v[34:37],0
s_add_u32 s58,s58,s61
s_add_u32 m0,s59,1024
R 62,2,7168
M 30,a[12:15],38
s_add_u32 s56,s55,4096
buffer_load_dword v5,s[36:39],s58 offen offset:0 sc1 lds
v_mfma_f32_16x16x32_bf16 v[26:29],a[0:3],v[34:37],0
s_mov_b32 m0,s57
R 66,2,8192
v_fma_f32 v18,-v98,v106,v18
M 26,a[8:11],38
L 5
buffer_load_dwordx4 v[102:105],v11,s[36:39],s73 offen offset:0 sc1
M 26,a[16:19],42
s_mov_b32 s67,s65
R 70,2,9216
v_fma_f32 v19,-v98,v107,v19
M 26,a[24:27],46
s_add_u32 s73,s73,s64
v_fma_f32 v20,-v98,v108,v20
v_fma_f32 v21,-v98,v109,v21
M 30,a[20:23],42
s_add_u32 s54,s54,s46
R 74,2,10240
v_mul_f32 v114,v18,v18
M 30,a[28:31],46
L 5
D 0,55,0
M 30,a[36:39],50
s_add_u32 s65,s65,s62
R 78,2,11264
v_mul_f32 v115,v19,v19
M 30,a[44:47],54
v_mul_f32 v116,v20,v20
v_mul_f32 v117,v21,v21
M 26,a[32:35],50
R 82,2,12288
v_fma_f32 v114,v100,v114,v99
M 26,a[40:43],54
L 5
D 0,55,1024
M 26,a[48:51],58
R 86,2,13312
v_fma_f32 v115,v100,v115,v99
M 26,a[56:59],62
v_fma_f32 v116,v100,v116,v99
v_fma_f32 v117,v100,v117,v99
M 30,a[52:55],58
R 90,2,14336
v_mul_f32 v114,v18,v114
M 30,a[60:63],62
L 5
D 0,55,2048
M 30,a[68:71],66
R 94,2,15360
v_mul_f32 v115,v19,v115
M 30,a[76:79],70
v_exp_f32 v114,v114
M 26,a[64:67],66
R 34,2,16384
v_mul_f32 v116,v20,v116
M 26,a[72:75],70
L 5
D 0,55,3072
M 26,a[80:83],74
s_add_u32 m0,s57,4096
R 38,2,17408
v_mul_f32 v117,v21,v117
M 26,a[88:91],78
s_add_u32 s57,s57,32768
v_exp_f32 v115,v115
M 30,a[84:87],74
s_add_u32 s55,s55,s60
R 42,2,18432
v_fma_f32 v114,v114,v101,v101
M 30,a[92:95],78
L 5
D 0,56,0
M 30,a[100:103],82
s_and_b32 s57,s57,131071
R 46,2,19456
v_fma_f32 v115,v115,v101,v101
M 30,a[108:111],86
v_exp_f32 v116,v116
M 26,a[96:99],82
R 50,2,20480
v_fma_f32 v116,v116,v101,v101
M 26,a[104:107],86
L 5
D 0,56,1024
M 26,a[112:115],90
R 54,2,21504
v_fma_f32 v22,-v98,v110,v22
M 26,a[120:123],94
v_exp_f32 v117,v117
M 30,a[116:119],90
R 58,2,22528
v_fma_f32 v117,v117,v101,v101
M 30,a[124:127],94
L 5
D 0,56,2048
M 30,a[132:135],34
R 62,2,23552
v_fma_f32 v23,-v98,v111,v23
M 30,a[140:143],38
v_rcp_f32 v114,v114
M 26,a[128:131],34
R 66,2,24576
v_mul_f32 v18,v18,v114
M 26,a[136:139],38
L 5
D 0,56,3072
M 26,a[144:147],42
R 70,2,25600
v_fma_f32 v24,-v98,v112,v24
M 26,a[152:155],46
v_rcp_f32 v115,v115
M 30,a[148:151],42
R 74,2,26624
v_mul_f32 v19,v19,v115
M 30,a[156:159],46
L 5
v_rcp_f32 v116,v116
M 30,a[164:167],50
R 78,2,27648
v_mul_f32 v20,v20,v116
M 30,a[172:175],54
v_rcp_f32 v117,v117
M 26,a[160:163],50
R 82,2,28672
v_mul_f32 v21,v21,v117
M 26,a[168:171],54
L 5
v_cvt_pk_bf16_f32 v122,v18,v19
v_cvt_pk_bf16_f32 v123,v20,v21
M 26,a[176:179],58
R 86,2,29696
v_fma_f32 v25,-v98,v113,v25
M 26,a[184:187],62
v_mul_f32 v118,v22,v22
v_mul_f32 v119,v23,v23
M 30,a[180:183],58
R 90,2,30720
v_mul_f32 v120,v24,v24
M 30,a[188:191],62
L 5
v_mul_f32 v121,v25,v25
v_fma_f32 v118,v100,v118,v99
M 30,a[196:199],66
s_waitcnt vmcnt(0)
R 94,2,31744
v_fma_f32 v119,v100,v119,v99
M 30,a[204:207],70
s_barrier
buffer_store_dword v4,v3,s[36:39],s70 offen offset:0 sc0 sc1
M 26,a[192:195],66
s_mov_b32 s70,s69
R 34,1,0
ds_read_b32 v7,v6 offset:1024
M 26,a[200:203],70
L 6
v_fma_f32 v120,v100,v120,v99
v_fma_f32 v121,v100,v121,v99
M 26,a[208:211],74
s_mov_b32 s69,s68
R 38,1,1024
v_mul_f32 v118,v22,v118
M 26,a[216:219],78
s_add_u32 s68,s68,s63
v_mul_f32 v119,v23,v119
v_mul_f32 v120,v24,v120
M 30,a[212:215],74
R 42,1,2048
v_mul_f32 v121,v25,v121
v_exp_f32 v118,v118
v_exp_f32 v119,v119
v_exp_f32 v120,v120
v_exp_f32 v121,v121
M 30,a[220:223],78
L 6
v_fma_f32 v118,v118,v101,v101
v_fma_f32 v119,v119,v101,v101
v_fma_f32 v120,v120,v101,v101
v_fma_f32 v121,v121,v101,v101
v_rcp_f32 v118,v118
v_rcp_f32 v119,v119
v_rcp_f32 v120,v120
v_rcp_f32 v121,v121
M 30,a[228:231],82
L 2
R 46,1,3072
v_mul_f32 v22,v22,v118
v_mul_f32 v23,v23,v119
v_mul_f32 v24,v24,v120
v_mul_f32 v25,v25,v121
v_cvt_pk_bf16_f32 v124,v22,v23
v_cvt_pk_bf16_f32 v125,v24,v25
M 30,a[236:239],86
buffer_store_dwordx4 v[122:125],v15,s[36:39],s66 offen offset:0 sc0 sc1
v_xor_b32 v2,65536,v2
M 26,a[224:227],82
R 50,1,4096
v_cmp_eq_u32 vcc,s53,v7
M 26,a[232:235],86
s_nop 0
s_cmp_eq_u64 vcc,-1
M 26,a[240:243],90
R 54,1,5120
M 26,a[248:251],94
buffer_store_dwordx4 v[122:125],v16,s[36:39],s66 offen offset:0 sc0 sc1
M 30,a[244:247],90
R 58,1,6144
M 30,a[252:255],94
L 5
s_cbranch_scc1 Lg1_L20_%=
s_cmp_eq_u32 vcc_lo,-1
s_cbranch_scc1 Lg1_L10_%=
s_branch Lg1_X0_%=
Lg1_done_%=:
s_waitcnt vmcnt(2)
s_barrier
buffer_store_dword v4,v3,s[36:39],s70 offen offset:0 sc0 sc1
s_waitcnt vmcnt(1)
s_barrier
buffer_store_dword v4,v3,s[36:39],s72 offen offset:0 sc0 sc1
s_waitcnt vmcnt(0) lgkmcnt(0)
s_barrier
s_branch Lg2_end_%=
Lg2_begin_%=:
Lcode_g2_b:
s_mov_b64 s[36:37],%1
s_mov_b32 s38,111243392
s_mov_b32 s39,0x00020000
s_mov_b32 s44,%2
s_mov_b32 s45,%3
s_mov_b32 s46,%4
s_mov_b32 s47,%5
s_mov_b32 s48,%6
s_mov_b32 s49,%7
s_mov_b32 s50,%8
s_mov_b32 s51,%9
s_mov_b32 s93,%10
v_mov_b32 v0,%11
v_mov_b32 v3,%12
v_mov_b32 v5,%13
v_mov_b32 v6,%14
v_mov_b32 v18,%15
v_mov_b32 v19,%16
v_mov_b32 v20,%17
v_mov_b32 v21,%18
v_mov_b32 v22,%19
v_mov_b32 v10,%20
v_mov_b32 v11,%21
v_mov_b32 v12,%22
v_mov_b32 v13,%23
v_mov_b32 v14,%24
v_mov_b32 v15,%25
v_mov_b32 v16,%26
s_mov_b64 s[40:41],%27
s_mov_b32 s42,2097152
s_mov_b32 s43,0x00020000
s_lshl_b32 s57,s44,11
s_mov_b32 s79,111243264
buffer_load_dword v4,off,s[36:39],s79 sc1
s_add_u32 s55,s50,0
s_add_u32 m0,s57,0
s_nop 0
buffer_load_dwordx4 v18,s[40:43],s55 offen offset:0  lds
s_add_u32 m0,s57,1024
s_nop 0
buffer_load_dwordx4 v19,s[40:43],s55 offen offset:0  lds
s_add_u32 m0,s57,8064
s_nop 0
buffer_load_dwordx4 v18,s[40:43],s55 offen offset:128  lds
s_add_u32 m0,s57,9088
s_nop 0
buffer_load_dwordx4 v19,s[40:43],s55 offen offset:128  lds
s_add_u32 s55,s50,131072
s_add_u32 m0,s57,16384
s_nop 0
buffer_load_dwordx4 v18,s[40:43],s55 offen offset:0  lds
s_add_u32 m0,s57,17408
s_nop 0
buffer_load_dwordx4 v19,s[40:43],s55 offen offset:0  lds
s_add_u32 m0,s57,24448
s_nop 0
buffer_load_dwordx4 v18,s[40:43],s55 offen offset:128  lds
s_add_u32 m0,s57,25472
s_nop 0
buffer_load_dwordx4 v19,s[40:43],s55 offen offset:128  lds
s_add_u32 s55,s50,262144
s_add_u32 m0,s57,32768
s_nop 0
buffer_load_dwordx4 v18,s[40:43],s55 offen offset:0  lds
s_add_u32 m0,s57,33792
s_nop 0
buffer_load_dwordx4 v19,s[40:43],s55 offen offset:0  lds
s_add_u32 m0,s57,40832
s_nop 0
buffer_load_dwordx4 v18,s[40:43],s55 offen offset:128  lds
s_add_u32 m0,s57,41856
s_nop 0
buffer_load_dwordx4 v19,s[40:43],s55 offen offset:128  lds
s_add_u32 s55,s50,393216
s_add_u32 m0,s57,49152
s_nop 0
buffer_load_dwordx4 v18,s[40:43],s55 offen offset:0  lds
s_add_u32 m0,s57,50176
s_nop 0
buffer_load_dwordx4 v19,s[40:43],s55 offen offset:0  lds
s_add_u32 m0,s57,57216
s_nop 0
buffer_load_dwordx4 v18,s[40:43],s55 offen offset:128  lds
s_add_u32 m0,s57,58240
s_nop 0
buffer_load_dwordx4 v19,s[40:43],s55 offen offset:128  lds
)"
R"(s_add_u32 s55,s50,524288
s_add_u32 m0,s57,65536
s_nop 0
buffer_load_dwordx4 v18,s[40:43],s55 offen offset:0  lds
s_add_u32 m0,s57,66560
s_nop 0
buffer_load_dwordx4 v19,s[40:43],s55 offen offset:0  lds
s_add_u32 m0,s57,73600
s_nop 0
buffer_load_dwordx4 v18,s[40:43],s55 offen offset:128  lds
s_add_u32 m0,s57,74624
s_nop 0
buffer_load_dwordx4 v19,s[40:43],s55 offen offset:128  lds
s_add_u32 s55,s50,655360
s_add_u32 m0,s57,81920
s_nop 0
buffer_load_dwordx4 v18,s[40:43],s55 offen offset:0  lds
s_add_u32 m0,s57,82944
s_nop 0
buffer_load_dwordx4 v19,s[40:43],s55 offen offset:0  lds
s_add_u32 m0,s57,89984
s_nop 0
buffer_load_dwordx4 v18,s[40:43],s55 offen offset:128  lds
s_add_u32 m0,s57,91008
s_nop 0
buffer_load_dwordx4 v19,s[40:43],s55 offen offset:128  lds
s_waitcnt vmcnt(24)
s_barrier
v_readfirstlane_b32 s53,v4
s_nop 3
s_add_u32 s53,s53,1
s_cmp_eq_u32 s53,0
s_cselect_b32 s53,1,s53
v_mov_b32 v4,s53
s_waitcnt vmcnt(20)
L 0
s_barrier
s_add_u32 s55,s50,786432
s_add_u32 m0,s57,98304
s_nop 0
buffer_load_dwordx4 v18,s[40:43],s55 offen offset:0  lds
s_add_u32 m0,s57,99328
s_nop 0
buffer_load_dwordx4 v19,s[40:43],s55 offen offset:0  lds
s_add_u32 m0,s57,106368
s_nop 0
buffer_load_dwordx4 v18,s[40:43],s55 offen offset:128  lds
s_add_u32 m0,s57,107392
s_nop 0
buffer_load_dwordx4 v19,s[40:43],s55 offen offset:128  lds
v_add_u32 v26,0,v20
v_add_u32 v27,0,v21
T a[0:1],26,0
T a[2:3],26,512
T a[4:5],27,0
T a[6:7],27,512
T a[8:9],26,4096
T a[10:11],26,4608
T a[12:13],27,4096
T a[14:15],27,4608
s_waitcnt vmcnt(20)
L 0
s_barrier
s_add_u32 s55,s50,917504
s_add_u32 m0,s57,0
s_nop 0
buffer_load_dwordx4 v18,s[40:43],s55 offen offset:0  lds
s_add_u32 m0,s57,1024
s_nop 0
buffer_load_dwordx4 v19,s[40:43],s55 offen offset:0  lds
s_add_u32 m0,s57,8064
s_nop 0
buffer_load_dwordx4 v18,s[40:43],s55 offen offset:128  lds
s_add_u32 m0,s57,9088
s_nop 0
buffer_load_dwordx4 v19,s[40:43],s55 offen offset:128  lds
v_add_u32 v26,16384,v20
v_add_u32 v27,16384,v21
T a[16:17],26,0
T a[18:19],26,512
T a[20:21],27,0
T a[22:23],27,512
T a[24:25],26,4096
T a[26:27],26,4608
T a[28:29],27,4096
T a[30:31],27,4608
s_waitcnt vmcnt(20)
L 0
s_barrier
s_add_u32 s55,s50,1048576
s_add_u32 m0,s57,16384
s_nop 0
buffer_load_dwordx4 v18,s[40:43],s55 offen offset:0  lds
s_add_u32 m0,s57,17408
s_nop 0
buffer_load_dwordx4 v19,s[40:43],s55 offen offset:0  lds
s_add_u32 m0,s57,24448
s_nop 0
buffer_load_dwordx4 v18,s[40:43],s55 offen offset:128  lds
s_add_u32 m0,s57,25472
s_nop 0
buffer_load_dwordx4 v19,s[40:43],s55 offen offset:128  lds
v_add_u32 v26,32768,v20
v_add_u32 v27,32768,v21
T a[32:33],26,0
T a[34:35],26,512
T a[36:37],27,0
T a[38:39],27,512
T a[40:41],26,4096
T a[42:43],26,4608
T a[44:45],27,4096
T a[46:47],27,4608
s_waitcnt vmcnt(20)
L 0
s_barrier
s_add_u32 s55,s50,1179648
s_add_u32 m0,s57,32768
s_nop 0
buffer_load_dwordx4 v18,s[40:43],s55 offen offset:0  lds
s_add_u32 m0,s57,33792
s_nop 0
buffer_load_dwordx4 v19,s[40:43],s55 offen offset:0  lds
s_add_u32 m0,s57,40832
s_nop 0
buffer_load_dwordx4 v18,s[40:43],s55 offen offset:128  lds
s_add_u32 m0,s57,41856
s_nop 0
buffer_load_dwordx4 v19,s[40:43],s55 offen offset:128  lds
v_add_u32 v26,49152,v20
v_add_u32 v27,49152,v21
T a[48:49],26,0
T a[50:51],26,512
T a[52:53],27,0
T a[54:55],27,512
T a[56:57],26,4096
T a[58:59],26,4608
T a[60:61],27,4096
T a[62:63],27,4608
s_waitcnt vmcnt(20)
L 0
s_barrier
s_add_u32 s55,s50,1310720
s_add_u32 m0,s57,49152
s_nop 0
buffer_load_dwordx4 v18,s[40:43],s55 offen offset:0  lds
s_add_u32 m0,s57,50176
s_nop 0
buffer_load_dwordx4 v19,s[40:43],s55 offen offset:0  lds
s_add_u32 m0,s57,57216
s_nop 0
buffer_load_dwordx4 v18,s[40:43],s55 offen offset:128  lds
s_add_u32 m0,s57,58240
s_nop 0
buffer_load_dwordx4 v19,s[40:43],s55 offen offset:128  lds
v_add_u32 v26,65536,v20
v_add_u32 v27,65536,v21
T a[64:65],26,0
T a[66:67],26,512
T a[68:69],27,0
T a[70:71],27,512
T a[72:73],26,4096
T a[74:75],26,4608
T a[76:77],27,4096
T a[78:79],27,4608
s_waitcnt vmcnt(20)
L 0
s_barrier
s_add_u32 s55,s50,1441792
s_add_u32 m0,s57,65536
s_nop 0
buffer_load_dwordx4 v18,s[40:43],s55 offen offset:0  lds
s_add_u32 m0,s57,66560
s_nop 0
buffer_load_dwordx4 v19,s[40:43],s55 offen offset:0  lds
s_add_u32 m0,s57,73600
s_nop 0
buffer_load_dwordx4 v18,s[40:43],s55 offen offset:128  lds
s_add_u32 m0,s57,74624
s_nop 0
buffer_load_dwordx4 v19,s[40:43],s55 offen offset:128  lds
v_add_u32 v26,81920,v20
v_add_u32 v27,81920,v21
T a[80:81],26,0
T a[82:83],26,512
T a[84:85],27,0
T a[86:87],27,512
T a[88:89],26,4096
T a[90:91],26,4608
T a[92:93],27,4096
T a[94:95],27,4608
s_waitcnt vmcnt(20)
L 0
s_barrier
s_add_u32 s55,s50,1572864
s_add_u32 m0,s57,81920
s_nop 0
buffer_load_dwordx4 v18,s[40:43],s55 offen offset:0  lds
s_add_u32 m0,s57,82944
s_nop 0
buffer_load_dwordx4 v19,s[40:43],s55 offen offset:0  lds
s_add_u32 m0,s57,89984
s_nop 0
buffer_load_dwordx4 v18,s[40:43],s55 offen offset:128  lds
s_add_u32 m0,s57,91008
s_nop 0
buffer_load_dwordx4 v19,s[40:43],s55 offen offset:128  lds
v_add_u32 v26,98304,v20
v_add_u32 v27,98304,v21
T a[96:97],26,0
T a[98:99],26,512
T a[100:101],27,0
T a[102:103],27,512
T a[104:105],26,4096
T a[106:107],26,4608
T a[108:109],27,4096
T a[110:111],27,4608
s_waitcnt vmcnt(20)
L 0
s_barrier
s_add_u32 s55,s50,1703936
s_add_u32 m0,s57,98304
s_nop 0
buffer_load_dwordx4 v18,s[40:43],s55 offen offset:0  lds
s_add_u32 m0,s57,99328
s_nop 0
buffer_load_dwordx4 v19,s[40:43],s55 offen offset:0  lds
s_add_u32 m0,s57,106368
s_nop 0
buffer_load_dwordx4 v18,s[40:43],s55 offen offset:128  lds
s_add_u32 m0,s57,107392
s_nop 0
buffer_load_dwordx4 v19,s[40:43],s55 offen offset:128  lds
v_add_u32 v26,0,v20
v_add_u32 v27,0,v21
T a[112:113],26,0
T a[114:115],26,512
T a[116:117],27,0
T a[118:119],27,512
T a[120:121],26,4096
T a[122:123],26,4608
T a[124:125],27,4096
T a[126:127],27,4608
s_waitcnt vmcnt(20)
L 0
s_barrier
s_add_u32 s55,s50,1835008
s_add_u32 m0,s57,0
s_nop 0
buffer_load_dwordx4 v18,s[40:43],s55 offen offset:0  lds
s_add_u32 m0,s57,1024
s_nop 0
buffer_load_dwordx4 v19,s[40:43],s55 offen offset:0  lds
s_add_u32 m0,s57,8064
s_nop 0
buffer_load_dwordx4 v18,s[40:43],s55 offen offset:128  lds
s_add_u32 m0,s57,9088
s_nop 0
buffer_load_dwordx4 v19,s[40:43],s55 offen offset:128  lds
v_add_u32 v26,16384,v20
v_add_u32 v27,16384,v21
T a[128:129],26,0
T a[130:131],26,512
T a[132:133],27,0
T a[134:135],27,512
T a[136:137],26,4096
T a[138:139],26,4608
T a[140:141],27,4096
T a[142:143],27,4608
s_waitcnt vmcnt(20)
L 0
s_barrier
s_add_u32 s55,s50,1966080
s_add_u32 m0,s57,16384
s_nop 0
buffer_load_dwordx4 v18,s[40:43],s55 offen offset:0  lds
s_add_u32 m0,s57,17408
s_nop 0
buffer_load_dwordx4 v19,s[40:43],s55 offen offset:0  lds
s_add_u32 m0,s57,24448
s_nop 0
buffer_load_dwordx4 v18,s[40:43],s55 offen offset:128  lds
s_add_u32 m0,s57,25472
s_nop 0
buffer_load_dwordx4 v19,s[40:43],s55 offen offset:128  lds
v_add_u32 v26,32768,v20
v_add_u32 v27,32768,v21
T a[144:145],26,0
T a[146:147],26,512
T a[148:149],27,0
T a[150:151],27,512
T a[152:153],26,4096
T a[154:155],26,4608
T a[156:157],27,4096
T a[158:159],27,4608
s_waitcnt vmcnt(20)
L 0
s_barrier
v_add_u32 v26,49152,v20
v_add_u32 v27,49152,v21
T a[160:161],26,0
T a[162:163],26,512
T a[164:165],27,0
T a[166:167],27,512
T a[168:169],26,4096
T a[170:171],26,4608
T a[172:173],27,4096
T a[174:175],27,4608
s_waitcnt vmcnt(16)
L 0
s_barrier
v_add_u32 v26,65536,v20
v_add_u32 v27,65536,v21
T a[176:177],26,0
T a[178:179],26,512
T a[180:181],27,0
T a[182:183],27,512
T a[184:185],26,4096
T a[186:187],26,4608
T a[188:189],27,4096
T a[190:191],27,4608
s_waitcnt vmcnt(12)
L 0
s_barrier
v_add_u32 v26,81920,v20
v_add_u32 v27,81920,v21
T a[192:193],26,0
T a[194:195],26,512
T a[196:197],27,0
T a[198:199],27,512
T a[200:201],26,4096
T a[202:203],26,4608
T a[204:205],27,4096
T a[206:207],27,4608
s_waitcnt vmcnt(8)
L 0
s_barrier
v_add_u32 v26,98304,v20
v_add_u32 v27,98304,v21
T a[208:209],26,0
T a[210:211],26,512
T a[212:213],27,0
T a[214:215],27,512
T a[216:217],26,4096
T a[218:219],26,4608
T a[220:221],27,4096
T a[222:223],27,4608
s_waitcnt vmcnt(4)
L 0
s_barrier
v_add_u32 v26,0,v20
v_add_u32 v27,0,v21
T a[224:225],26,0
T a[226:227],26,512
T a[228:229],27,0
T a[230:231],27,512
T a[232:233],26,4096
T a[234:235],26,4608
T a[236:237],27,4096
T a[238:239],27,4608
s_waitcnt vmcnt(0)
L 0
s_barrier
v_add_u32 v26,16384,v20
v_add_u32 v27,16384,v21
T a[240:241],26,0
T a[242:243],26,512
T a[244:245],27,0
T a[246:247],27,512
T a[248:249],26,4096
T a[250:251],26,4608
T a[252:253],27,4096
T a[254:255],27,4608
L 0
s_barrier
v_mov_b32 v4,s53
s_mov_b32 s82,0
s_mov_b32 s80,0
s_mov_b32 s54,s45
s_mul_i32 s57,s44,10240
s_mul_i32 s55,s45,40960
s_add_u32 s55,s55,s57
s_add_u32 s55,s55,17825792
s_mul_i32 s57,s44,8192
s_mul_i32 s60,s46,40960
s_mul_i32 s61,s46,1024
s_mul_i32 s62,s46,32768
s_mul_i32 s63,s46,1024
s_mul_i32 s64,s46,512
s_lshl_b32 s59,s44,8
s_add_u32 s59,s59,131072
s_mul_i32 s58,s45,1024
s_add_u32 s58,s58,109333504
s_mul_i32 s65,s45,32768
s_add_u32 s65,s65,s47
s_mul_i32 s68,s45,1024
s_add_u32 s68,s68,s48
s_mul_i32 s73,s45,512
s_add_u32 s73,s73,106954752
s_mul_i32 s74,s45,32768
s_add_u32 s74,s74,s49
s_mul_i32 s75,s45,1024
s_add_u32 s75,s75,107479040
s_mov_b32 s66,0x7fff0000
s_mov_b32 s67,0x7fff0000
s_mov_b32 s69,0x7fff0000
s_mov_b32 s76,0x7fff0000
s_mov_b32 s77,0x7fff0000
s_mov_b32 s70,0x7fff0000
s_mov_b32 s71,0x7fff0000
s_mov_b32 s72,0x7fff0000
v_mov_b32 v1,v0
v_add_u32 v2,32768,v0
s_mov_b32 s84,32768
s_mov_b32 s86,0x3a800000
s_mov_b32 s87,0x38a7c5ac
s_mov_b32 s88,0xc0135761
s_mov_b32 s89,0x3d372713
s_and_b32 s85,s44,1
s_mul_i32 s85,s85,127
v_lshlrev_b32 v8,22,v0
v_and_b32 v8,0x80000000,v8
v_or_b32 v8,v8,v5
s_and_b32 s79,s54,127
s_cmp_eq_u32 s79,0
s_cselect_b32 s94,0x3300,0
s_cmp_eq_u32 s79,127
s_cselect_b32 s94,0xcc00,s94
s_add_u32 s79,s54,s46
s_and_b32 s79,s79,127
s_cmp_eq_u32 s79,0
s_cselect_b32 s95,0x3300,0
s_cmp_eq_u32 s79,127
s_cselect_b32 s95,0xcc00,s95
s_add_u32 m0,s59,4096
s_nop 0
buffer_load_dword v5,s[36:39],s58 offen offset:0 sc1 lds
buffer_store_dword v4,v3,s[36:39],s71 offen offset:0 sc0 sc1
s_add_u32 m0,s59,5120
s_nop 0
buffer_load_dword v5,s[36:39],s58 offen offset:0 sc1 lds
buffer_store_dword v4,v3,s[36:39],s71 offen offset:0 sc0 sc1
s_mov_b32 s83,0
Lg2_poll_%=:
s_add_u32 s78,s83,2
s_and_b32 s78,s78,3
s_lshl_b32 s78,s78,10
s_add_u32 s78,s78,4096
s_add_u32 m0,s59,s78
s_nop 0
buffer_load_dword v5,s[36:39],s58 offen offset:0 sc1 lds
s_and_b32 s78,s83,3
s_lshl_b32 s78,s78,10
v_add_u32 v106,s78,v6
s_waitcnt vmcnt(4)
s_barrier
ds_read_b32 v7,v106 offset:4096
buffer_store_dword v4,v3,s[36:39],s70 offen offset:0 sc0 sc1
s_mov_b32 s70,s71
s_mov_b32 s71,s72
s_mov_b32 s72,0x7fff0000
s_add_u32 s83,s83,1
s_add_u32 s82,s82,1
L 0
v_cmp_eq_u32 vcc,s53,v7
s_nop 0
s_or_b64 vcc,vcc,s[94:95]
s_cmp_eq_u32 vcc_lo,-1
s_cbranch_scc1 Lg2_go_%=
s_cmp_lt_u32 s82,20000
s_cbranch_scc1 Lg2_poll_%=
Lg2_go_%=:
s_and_b32 s78,s54,127
s_cmp_eq_u32 s78,s85
s_cselect_b64 s[40:41],-1,0
v_cndmask_b32 v17,v15,v16,s[40:41]
s_add_u32 m0,s57,0
s_add_u32 s56,s55,0
D 17,56,0
s_add_u32 s56,s55,256
D 17,56,1024
s_add_u32 s56,s55,512
D 17,56,2048
s_add_u32 s56,s55,768
D 17,56,3072
s_add_u32 m0,s57,4096
s_add_u32 s56,s55,5120
D 17,56,0
s_add_u32 s56,s55,5376
D 17,56,1024
s_add_u32 s56,s55,5632
D 17,56,2048
s_add_u32 s56,s55,5888
D 17,56,3072
s_add_u32 s55,s55,s60
s_add_u32 s57,s57,32768
s_and_b32 s57,s57,131071
s_add_u32 s54,s54,s46
Lg2_gox_%=:
s_add_u32 s58,s58,s61
s_cmp_eq_u32 vcc_hi,-1
)"
R"(s_cbranch_scc1 Lg2_go2_%=
s_add_u32 m0,s59,2048
s_nop 0
buffer_load_dword v5,s[36:39],s58 offen offset:0 sc1 lds
s_and_b32 s79,s54,127
s_cmp_eq_u32 s79,0
s_cselect_b32 s94,0x3300,0
s_cmp_eq_u32 s79,127
s_cselect_b32 s94,0xcc00,s94
s_add_u32 s79,s54,s46
s_and_b32 s79,s79,127
s_cmp_eq_u32 s79,0
s_cselect_b32 s95,0x3300,0
s_cmp_eq_u32 s79,127
s_cselect_b32 s95,0xcc00,s95
s_waitcnt vmcnt(0)
s_barrier
buffer_store_dword v4,v3,s[36:39],s70 offen offset:0 sc0 sc1
s_min_u32 s71,s71,s72
buffer_store_dword v4,v3,s[36:39],s71 offen offset:0 sc0 sc1
s_mov_b32 s70,0x7fff0000
s_mov_b32 s71,0x7fff0000
s_mov_b32 s72,0x7fff0000
s_cmp_eq_u32 s80,1
s_cbranch_scc1 Lg2_st11_%=
Lg2_st10_%=:
ds_read_b32 v7,v6 offset:2048
R 34,1,0
R 38,1,1024
R 42,1,2048
R 46,1,3072
R 50,1,4096
R 54,1,5120
R 58,1,6144
L 7
v_cmp_eq_u32 vcc,s53,v7
s_nop 0
s_or_b64 vcc,vcc,s[94:95]
L 5
s_cmp_eq_u64 vcc,-1
s_cbranch_scc1 Lg2_L20_%=
s_cmp_eq_u32 vcc_lo,-1
s_cbranch_scc1 Lg2_L10_%=
s_branch Lg2_X0_%=
Lg2_st11_%=:
ds_read_b32 v7,v6 offset:2048
R 34,2,0
R 38,2,1024
R 42,2,2048
R 46,2,3072
R 50,2,4096
R 54,2,5120
R 58,2,6144
L 7
v_cmp_eq_u32 vcc,s53,v7
s_nop 0
s_or_b64 vcc,vcc,s[94:95]
L 5
s_cmp_eq_u64 vcc,-1
s_cbranch_scc1 Lg2_L21_%=
s_cmp_eq_u32 vcc_lo,-1
s_cbranch_scc1 Lg2_L11_%=
s_branch Lg2_X1_%=
Lg2_go2_%=:
s_add_u32 s58,s58,s61
s_add_u32 m0,s59,2048
s_nop 0
buffer_load_dword v5,s[36:39],s58 offen offset:0 sc1 lds
s_and_b32 s78,s54,127
s_cmp_eq_u32 s78,s85
s_cselect_b64 s[40:41],-1,0
v_cndmask_b32 v17,v15,v16,s[40:41]
s_add_u32 m0,s57,0
s_add_u32 s56,s55,0
D 17,56,0
s_add_u32 s56,s55,256
D 17,56,1024
s_add_u32 s56,s55,512
D 17,56,2048
s_add_u32 s56,s55,768
D 17,56,3072
s_add_u32 m0,s57,4096
s_add_u32 s56,s55,5120
D 17,56,0
s_add_u32 s56,s55,5376
D 17,56,1024
s_add_u32 s56,s55,5632
D 17,56,2048
s_add_u32 s56,s55,5888
D 17,56,3072
s_add_u32 s55,s55,s60
s_add_u32 s57,s57,32768
s_and_b32 s57,s57,131071
s_add_u32 s54,s54,s46
s_and_b32 s79,s54,127
s_cmp_eq_u32 s79,0
s_cselect_b32 s94,0x3300,0
s_cmp_eq_u32 s79,127
s_cselect_b32 s94,0xcc00,s94
s_add_u32 s79,s54,s46
s_and_b32 s79,s79,127
s_cmp_eq_u32 s79,0
s_cselect_b32 s95,0x3300,0
s_cmp_eq_u32 s79,127
s_cselect_b32 s95,0xcc00,s95
s_waitcnt vmcnt(8)
s_barrier
buffer_store_dword v4,v3,s[36:39],s70 offen offset:0 sc0 sc1
s_min_u32 s71,s71,s72
buffer_store_dword v4,v3,s[36:39],s71 offen offset:0 sc0 sc1
s_mov_b32 s70,0x7fff0000
s_mov_b32 s71,0x7fff0000
s_mov_b32 s72,0x7fff0000
s_cmp_eq_u32 s80,1
s_cbranch_scc1 Lg2_st21_%=
Lg2_st20_%=:
ds_read_b32 v7,v6 offset:2048
R 34,1,0
R 38,1,1024
R 42,1,2048
R 46,1,3072
R 50,1,4096
R 54,1,5120
R 58,1,6144
L 7
v_cmp_eq_u32 vcc,s53,v7
s_nop 0
s_or_b64 vcc,vcc,s[94:95]
L 5
s_cmp_eq_u32 vcc_lo,-1
s_cbranch_scc1 Lg2_S0_%=
s_branch Lg2_N0_%=
Lg2_st21_%=:
ds_read_b32 v7,v6 offset:2048
R 34,2,0
R 38,2,1024
R 42,2,2048
R 46,2,3072
R 50,2,4096
R 54,2,5120
R 58,2,6144
L 7
v_cmp_eq_u32 vcc,s53,v7
s_nop 0
s_or_b64 vcc,vcc,s[94:95]
L 5
s_cmp_eq_u32 vcc_lo,-1
s_cbranch_scc1 Lg2_S1_%=
s_branch Lg2_N1_%=
Lg2_L20_%=:
v_mfma_f32_16x16x32_bf16 v[22:25],a[4:7],v[34:37],0
s_add_u32 s58,s58,s61
s_and_b32 s78,s54,127
s_add_u32 s58,s58,s61
R 62,1,7168
M 22,a[12:15],38
s_add_u32 m0,s59,0
s_cmp_eq_u32 s78,s85
s_cselect_b64 s[40:41],-1,0
buffer_load_dwordx4 v[122:125],v0,s[36:39],s74 offen offset:0 sc1
v_mfma_f32_16x16x32_bf16 v[18:21],a[0:3],v[34:37],0
s_add_u32 s56,s55,0
R 66,1,8192
v_cndmask_b32 v17,v15,v16,s[40:41]
M 18,a[8:11],38
L 5
buffer_load_dword v8,s[36:39],s58 offen offset:0 sc1 lds
M 18,a[16:19],42
s_add_u32 m0,s57,0
R 70,1,9216
v_lshlrev_b32 v106,16,v126
M 18,a[24:27],46
s_add_u32 s54,s54,s46
buffer_load_dwordx2 v[98:99],v11,s[36:39],s73 offen offset:256 sc1
M 22,a[20:23],42
s_and_b32 s78,s54,127
R 74,1,10240
v_and_b32 v107,0xffff0000,v126
M 22,a[28:31],46
L 5
D 17,56,0
M 22,a[36:39],50
s_add_u32 s56,s55,256
R 78,1,11264
v_lshlrev_b32 v108,16,v127
M 22,a[44:47],54
s_cmp_eq_u32 s78,s85
v_and_b32 v109,0xffff0000,v127
v_lshlrev_b32 v110,16,v128
M 18,a[32:35],50
s_cselect_b64 s[40:41],-1,0
R 82,1,12288
v_and_b32 v111,0xffff0000,v128
M 18,a[40:43],54
L 5
D 17,56,1024
M 18,a[48:51],58
s_add_u32 s56,s55,512
R 86,1,13312
v_lshlrev_b32 v112,16,v129
M 18,a[56:59],62
s_add_u32 s54,s54,s46
v_and_b32 v113,0xffff0000,v129
v_fma_f32 v106,v106,v103,v102
M 22,a[52:55],58
s_and_b32 s79,s54,127
R 90,1,14336
v_fma_f32 v107,v107,v103,v102
M 22,a[60:63],62
L 5
D 17,56,2048
M 22,a[68:71],66
s_add_u32 s56,s55,768
R 94,1,15360
v_fma_f32 v108,v108,v103,v102
M 22,a[76:79],70
s_cmp_eq_u32 s79,0
v_fma_f32 v109,v109,v103,v102
v_fma_f32 v110,v110,v103,v102
M 18,a[64:67],66
s_cselect_b32 s94,0x3300,0
R 34,1,16384
v_fma_f32 v111,v111,v103,v102
M 18,a[72:75],70
L 5
D 17,56,3072
M 18,a[80:83],74
s_add_u32 m0,s57,4096
s_add_u32 s56,s55,5120
R 38,1,17408
M 18,a[88:91],78
s_add_u32 s57,s57,32768
v_fma_f32 v112,v112,v103,v102
v_fma_f32 v113,v113,v103,v102
M 22,a[84:87],74
s_and_b32 s57,s57,131071
R 42,1,18432
v_add_f32 v26,v26,v106
M 22,a[92:95],78
L 5
D 17,56,0
M 22,a[100:103],82
s_add_u32 s56,s55,5376
R 46,1,19456
v_add_f32 v27,v27,v107
M 22,a[108:111],86
s_cmp_eq_u32 s79,127
v_add_f32 v28,v28,v108
v_add_f32 v29,v29,v109
M 18,a[96:99],82
s_cselect_b32 s94,0xcc00,s94
R 50,1,20480
v_fma_f32 v107,v26,v26,s87
M 18,a[104:107],86
L 5
D 17,56,1024
M 18,a[112:115],90
s_add_u32 s56,s55,5632
R 54,1,21504
v_add_f32 v106,v26,v27
M 18,a[120:123],94
s_add_u32 s79,s54,s46
v_fma_f32 v107,v27,v27,v107
v_add_f32 v106,v106,v28
M 22,a[116:119],90
s_and_b32 s79,s79,127
R 58,1,22528
v_fma_f32 v107,v28,v28,v107
M 22,a[124:127],94
L 5
D 17,56,2048
M 22,a[132:135],34
s_add_u32 s56,s55,5888
s_add_u32 s55,s55,s60
R 62,1,23552
M 22,a[140:143],38
s_cmp_eq_u32 s79,0
v_add_f32 v30,v30,v110
v_add_f32 v106,v106,v29
M 18,a[128:131],34
s_cselect_b32 s95,0x3300,0
R 66,1,24576
v_fma_f32 v107,v29,v29,v107
M 18,a[136:139],38
L 5
D 17,56,3072
M 18,a[144:147],42
s_add_u32 m0,s57,0
s_add_u32 s56,s55,0
R 70,1,25600
M 18,a[152:155],46
s_cmp_eq_u32 s79,127
v_add_f32 v31,v31,v111
v_add_f32 v32,v32,v112
v_add_f32 v33,v33,v113
v_add_f32 v106,v106,v30
v_fma_f32 v107,v30,v30,v107
v_add_f32 v106,v106,v31
v_fma_f32 v107,v31,v31,v107
v_add_f32 v106,v106,v32
v_fma_f32 v107,v32,v32,v107
v_add_f32 v106,v106,v33
v_fma_f32 v107,v33,v33,v107
M 22,a[148:151],42
s_cselect_b32 s95,0xcc00,s95
R 74,1,26624
v_cndmask_b32 v17,v15,v16,s[40:41]
M 22,a[156:159],46
L 5
v_permlane16_swap_b32 v106,v107
v_add_f32 v106,v106,v107
v_mov_b32 v107,v106
M 22,a[164:167],50
s_mov_b32 s66,s65
R 78,1,27648
v_cvt_pk_bf16_f32 v114,v26,v27
M 22,a[172:175],54
s_mov_b32 s76,s75
v_permlane32_swap_b32 v106,v107
v_add_f32 v106,v106,v107
M 18,a[160:163],50
s_add_u32 s73,s73,s64
R 82,1,28672
ds_write_b32 v12,v106 offset:2048
M 18,a[168:171],54
L 6
D 17,56,0
M 18,a[176:179],58
s_add_u32 s56,s55,256
R 86,1,29696
v_cvt_pk_bf16_f32 v115,v28,v29
M 18,a[184:187],62
s_add_u32 s74,s74,s62
D 17,56,1024
M 22,a[180:183],58
s_add_u32 s56,s55,512
R 90,1,30720
v_cvt_pk_bf16_f32 v116,v30,v31
M 22,a[188:191],62
L 6
L 2
D 17,56,2048
M 22,a[196:199],66
s_waitcnt vmcnt(3)
s_add_u32 s56,s55,768
R 94,1,31744
M 22,a[204:207],70
s_barrier
D 17,56,3072
M 18,a[192:195],66
s_add_u32 m0,s57,4096
s_add_u32 s56,s55,5120
R 34,2,0
M 18,a[200:203],70
s_nop 0
buffer_store_dword v4,v3,s[36:39],s70 offen offset:0 sc0 sc1
M 18,a[208:211],74
s_mov_b32 s70,s69
R 38,2,1024
R 118,13,2048
M 18,a[216:219],78
s_add_u32 s57,s57,32768
D 17,56,0
M 22,a[212:215],74
s_add_u32 s56,s55,5376
R 42,2,2048
ds_read_b32 v7,v6 offset:0
M 22,a[220:223],78
L 7
D 17,56,1024
M 22,a[228:231],82
s_add_u32 s56,s55,5632
R 46,2,3072
v_cvt_pk_bf16_f32 v117,v32,v33
M 22,a[236:239],86
s_mov_b32 s69,s68
D 17,56,2048
M 18,a[224:227],82
s_add_u32 s56,s55,5888
R 50,2,4096
v_xor_b32 v1,65536,v1
M 18,a[232:235],86
L 7
L 4
buffer_store_dwordx4 v[114:117],v0,s[36:39],s67 offen offset:0 sc0 sc1
M 18,a[240:243],90
L 2
R 54,2,5120
v_add_f32 v118,v118,v119
M 18,a[248:251],94
s_add_u32 s55,s55,s60
v_cmp_eq_u32 vcc,s53,v7
v_add_f32 v120,v120,v121
M 22,a[244:247],90
s_and_b32 s57,s57,131071
s_add_u32 s65,s65,s62
s_add_u32 s75,s75,s63
s_add_u32 s68,s68,s63
s_or_b64 vcc,vcc,s[94:95]
s_cmp_eq_u32 vcc_lo,-1
R 58,2,6144
v_add_f32 v118,v118,v120
v_mul_f32 v118,s86,v118
M 22,a[252:255],94
s_nop 0
D 17,56,3072
buffer_store_dword v118,v14,s[36:39],s77 offen offset:0 sc0 sc1
s_cbranch_scc0 Lg2_N1_%=
Lg2_S1_%=:
v_mfma_f32_16x16x32_bf16 v[30:33],a[4:7],v[34:37],0
s_add_u32 s58,s58,s61
R 62,2,7168
v_lshlrev_b32 v106,16,v122
M 30,a[12:15],38
s_add_u32 m0,s59,1024
buffer_load_dwordx4 v[126:129],v0,s[36:39],s74 offen offset:0 sc1
v_mfma_f32_16x16x32_bf16 v[26:29],a[0:3],v[34:37],0
s_and_b32 s78,s54,127
R 66,2,8192
v_and_b32 v107,0xffff0000,v122
M 26,a[8:11],38
L 5
buffer_load_dword v8,s[36:39],s58 offen offset:0 sc1 lds
M 26,a[16:19],42
s_cmp_eq_u32 s78,s85
R 70,2,9216
v_lshlrev_b32 v108,16,v123
M 26,a[24:27],46
s_cselect_b64 s[40:41],-1,0
buffer_load_dwordx2 v[102:103],v11,s[36:39],s73 offen offset:256 sc1
M 30,a[20:23],42
s_add_u32 m0,s57,0
R 74,2,10240
v_cndmask_b32 v17,v15,v16,s[40:41]
M 30,a[28:31],46
L 5
s_add_u32 s56,s55,0
D 17,56,0
M 30,a[36:39],50
s_add_u32 s56,s55,256
R 78,2,11264
v_and_b32 v109,0xffff0000,v123
M 30,a[44:47],54
s_add_u32 s54,s54,s46
v_lshlrev_b32 v110,16,v124
v_and_b32 v111,0xffff0000,v124
M 26,a[32:35],50
s_and_b32 s79,s54,127
R 82,2,12288
v_lshlrev_b32 v112,16,v125
M 26,a[40:43],54
L 5
v_and_b32 v113,0xffff0000,v125
v_fma_f32 v106,v106,v99,v98
M 26,a[48:51],58
s_cmp_eq_u32 s79,0
R 86,2,13312
v_fma_f32 v107,v107,v99,v98
M 26,a[56:59],62
s_cselect_b32 s94,0x3300,0
D 17,56,1024
M 30,a[52:55],58
s_add_u32 s56,s55,512
R 90,2,14336
v_fma_f32 v108,v108,v99,v98
M 30,a[60:63],62
L 5
v_fma_f32 v109,v109,v99,v98
v_fma_f32 v110,v110,v99,v98
M 30,a[68:71],66
s_cmp_eq_u32 s79,127
R 94,2,15360
v_fma_f32 v111,v111,v99,v98
M 30,a[76:79],70
s_cselect_b32 s94,0xcc00,s94
v_fma_f32 v112,v112,v99,v98
v_fma_f32 v113,v113,v99,v98
M 26,a[64:67],66
s_add_u32 s79,s54,s46
R 34,2,16384
v_add_f32 v18,v18,v106
M 26,a[72:75],70
L 5
v_add_f32 v19,v19,v107
v_add_f32 v20,v20,v108
M 26,a[80:83],74
s_and_b32 s79,s79,127
R 38,2,17408
v_add_f32 v21,v21,v109
M 26,a[88:91],78
s_cmp_eq_u32 s79,0
D 17,56,2048
M 30,a[84:87],74
s_cselect_b32 s95,0x3300,0
R 42,2,18432
v_add_f32 v22,v22,v110
M 30,a[92:95],78
L 5
v_add_f32 v23,v23,v111
v_add_f32 v24,v24,v112
)"
R"(M 30,a[100:103],82
s_add_u32 s56,s55,768
R 46,2,19456
v_add_f32 v25,v25,v113
M 30,a[108:111],86
s_cmp_eq_u32 s79,127
v_cvt_pk_bf16_f32 v114,v18,v19
v_cvt_pk_bf16_f32 v115,v20,v21
M 26,a[96:99],82
s_cselect_b32 s95,0xcc00,s95
R 50,2,20480
v_fma_f32 v107,v18,v18,s87
M 26,a[104:107],86
L 5
D 17,56,3072
M 26,a[112:115],90
s_add_u32 m0,s57,4096
R 54,2,21504
v_add_f32 v106,v18,v19
M 26,a[120:123],94
s_add_u32 s56,s55,5120
v_fma_f32 v107,v19,v19,v107
v_add_f32 v106,v106,v20
M 30,a[116:119],90
s_add_u32 s57,s57,32768
R 58,2,22528
v_fma_f32 v107,v20,v20,v107
M 30,a[124:127],94
L 5
v_add_f32 v106,v106,v21
v_fma_f32 v107,v21,v21,v107
M 30,a[132:135],34
s_mov_b32 s67,s65
R 62,2,23552
v_add_f32 v106,v106,v22
M 30,a[140:143],38
s_mov_b32 s77,s75
v_fma_f32 v107,v22,v22,v107
v_add_f32 v106,v106,v23
M 26,a[128:131],34
s_add_u32 s73,s73,s64
R 66,2,24576
v_fma_f32 v107,v23,v23,v107
M 26,a[136:139],38
L 5
D 17,56,0
M 26,a[144:147],42
s_add_u32 s56,s55,5376
R 70,2,25600
v_add_f32 v106,v106,v24
M 26,a[152:155],46
s_add_u32 s74,s74,s62
v_fma_f32 v107,v24,v24,v107
v_add_f32 v106,v106,v25
v_fma_f32 v107,v25,v25,v107
M 30,a[148:151],42
s_and_b32 s57,s57,131071
R 74,2,26624
v_cvt_pk_bf16_f32 v116,v22,v23
M 30,a[156:159],46
L 5
v_permlane16_swap_b32 v106,v107
v_add_f32 v106,v106,v107
v_mov_b32 v107,v106
M 30,a[164:167],50
s_add_u32 s65,s65,s62
R 78,2,27648
v_cvt_pk_bf16_f32 v117,v24,v25
M 30,a[172:175],54
s_add_u32 s75,s75,s63
v_permlane32_swap_b32 v106,v107
v_add_f32 v106,v106,v107
M 26,a[160:163],50
R 82,2,28672
ds_write_b32 v12,v106 offset:0
M 26,a[168:171],54
L 6
D 17,56,1024
M 26,a[176:179],58
s_add_u32 s56,s55,5632
R 86,2,29696
M 26,a[184:187],62
buffer_store_dwordx4 v[114:117],v0,s[36:39],s66 offen offset:0 sc0 sc1
M 30,a[180:183],58
R 90,2,30720
M 30,a[188:191],62
L 6
L 2
M 30,a[196:199],66
s_waitcnt vmcnt(7)
R 94,2,31744
v_xor_b32 v2,65536,v2
M 30,a[204:207],70
s_barrier
D 17,56,2048
M 26,a[192:195],66
s_add_u32 s56,s55,5888
R 34,1,0
R 118,13,0
M 26,a[200:203],70
s_nop 0
buffer_store_dword v4,v3,s[36:39],s70 offen offset:0 sc0 sc1
M 26,a[208:211],74
s_mov_b32 s70,s69
R 38,1,1024
ds_read_b32 v7,v6 offset:1024
M 26,a[216:219],78
s_mov_b32 s69,s68
s_add_u32 s55,s55,s60
s_add_u32 s68,s68,s63
M 30,a[212:215],74
R 42,1,2048
M 30,a[220:223],78
L 7
D 17,56,3072
M 30,a[228:231],82
L 3
R 46,1,3072
v_add_f32 v118,v118,v119
M 30,a[236:239],86
v_add_f32 v120,v120,v121
v_add_f32 v118,v118,v120
M 26,a[224:227],82
L 2
R 50,1,4096
v_mul_f32 v118,s86,v118
M 26,a[232:235],86
s_nop 0
buffer_store_dword v118,v14,s[36:39],s76 offen offset:0 sc0 sc1
M 26,a[240:243],90
R 54,1,5120
v_cmp_eq_u32 vcc,s53,v7
M 26,a[248:251],94
s_or_b64 vcc,vcc,s[94:95]
s_cmp_eq_u32 vcc_lo,-1
M 30,a[244:247],90
R 58,1,6144
M 30,a[252:255],94
s_nop 0
s_cbranch_scc0 Lg2_N0_%=
Lg2_S0_%=:
v_mfma_f32_16x16x32_bf16 v[22:25],a[4:7],v[34:37],0
s_add_u32 s58,s58,s61
R 62,1,7168
v_lshlrev_b32 v106,16,v126
M 22,a[12:15],38
s_add_u32 m0,s59,0
buffer_load_dwordx4 v[122:125],v0,s[36:39],s74 offen offset:0 sc1
v_mfma_f32_16x16x32_bf16 v[18:21],a[0:3],v[34:37],0
s_and_b32 s78,s54,127
R 66,1,8192
v_and_b32 v107,0xffff0000,v126
M 18,a[8:11],38
L 5
buffer_load_dword v8,s[36:39],s58 offen offset:0 sc1 lds
M 18,a[16:19],42
s_cmp_eq_u32 s78,s85
R 70,1,9216
v_lshlrev_b32 v108,16,v127
M 18,a[24:27],46
s_cselect_b64 s[40:41],-1,0
buffer_load_dwordx2 v[98:99],v11,s[36:39],s73 offen offset:256 sc1
M 22,a[20:23],42
s_add_u32 m0,s57,0
R 74,1,10240
v_cndmask_b32 v17,v15,v16,s[40:41]
M 22,a[28:31],46
L 5
s_add_u32 s56,s55,0
D 17,56,0
M 22,a[36:39],50
s_add_u32 s56,s55,256
R 78,1,11264
v_and_b32 v109,0xffff0000,v127
M 22,a[44:47],54
s_add_u32 s54,s54,s46
v_lshlrev_b32 v110,16,v128
v_and_b32 v111,0xffff0000,v128
M 18,a[32:35],50
s_and_b32 s79,s54,127
R 82,1,12288
v_lshlrev_b32 v112,16,v129
M 18,a[40:43],54
L 5
v_and_b32 v113,0xffff0000,v129
v_fma_f32 v106,v106,v103,v102
M 18,a[48:51],58
s_cmp_eq_u32 s79,0
R 86,1,13312
v_fma_f32 v107,v107,v103,v102
M 18,a[56:59],62
s_cselect_b32 s94,0x3300,0
D 17,56,1024
M 22,a[52:55],58
s_add_u32 s56,s55,512
R 90,1,14336
v_fma_f32 v108,v108,v103,v102
M 22,a[60:63],62
L 5
v_fma_f32 v109,v109,v103,v102
v_fma_f32 v110,v110,v103,v102
M 22,a[68:71],66
s_cmp_eq_u32 s79,127
R 94,1,15360
v_fma_f32 v111,v111,v103,v102
M 22,a[76:79],70
s_cselect_b32 s94,0xcc00,s94
v_fma_f32 v112,v112,v103,v102
v_fma_f32 v113,v113,v103,v102
M 18,a[64:67],66
s_add_u32 s79,s54,s46
R 34,1,16384
v_add_f32 v26,v26,v106
M 18,a[72:75],70
L 5
v_add_f32 v27,v27,v107
v_add_f32 v28,v28,v108
M 18,a[80:83],74
s_and_b32 s79,s79,127
R 38,1,17408
v_add_f32 v29,v29,v109
M 18,a[88:91],78
s_cmp_eq_u32 s79,0
D 17,56,2048
M 22,a[84:87],74
s_cselect_b32 s95,0x3300,0
R 42,1,18432
v_add_f32 v30,v30,v110
M 22,a[92:95],78
L 5
v_add_f32 v31,v31,v111
v_add_f32 v32,v32,v112
M 22,a[100:103],82
s_add_u32 s56,s55,768
R 46,1,19456
v_add_f32 v33,v33,v113
M 22,a[108:111],86
s_cmp_eq_u32 s79,127
v_cvt_pk_bf16_f32 v114,v26,v27
v_cvt_pk_bf16_f32 v115,v28,v29
M 18,a[96:99],82
s_cselect_b32 s95,0xcc00,s95
R 50,1,20480
v_fma_f32 v107,v26,v26,s87
M 18,a[104:107],86
L 5
D 17,56,3072
M 18,a[112:115],90
s_add_u32 m0,s57,4096
R 54,1,21504
v_add_f32 v106,v26,v27
M 18,a[120:123],94
s_add_u32 s56,s55,5120
v_fma_f32 v107,v27,v27,v107
v_add_f32 v106,v106,v28
M 22,a[116:119],90
s_add_u32 s57,s57,32768
R 58,1,22528
v_fma_f32 v107,v28,v28,v107
M 22,a[124:127],94
L 5
v_add_f32 v106,v106,v29
v_fma_f32 v107,v29,v29,v107
M 22,a[132:135],34
s_mov_b32 s66,s65
R 62,1,23552
v_add_f32 v106,v106,v30
M 22,a[140:143],38
s_mov_b32 s76,s75
v_fma_f32 v107,v30,v30,v107
v_add_f32 v106,v106,v31
M 18,a[128:131],34
s_add_u32 s73,s73,s64
R 66,1,24576
v_fma_f32 v107,v31,v31,v107
M 18,a[136:139],38
L 5
D 17,56,0
M 18,a[144:147],42
s_add_u32 s56,s55,5376
R 70,1,25600
v_add_f32 v106,v106,v32
M 18,a[152:155],46
s_add_u32 s74,s74,s62
v_fma_f32 v107,v32,v32,v107
v_add_f32 v106,v106,v33
v_fma_f32 v107,v33,v33,v107
M 22,a[148:151],42
s_and_b32 s57,s57,131071
R 74,1,26624
v_cvt_pk_bf16_f32 v116,v30,v31
M 22,a[156:159],46
L 5
v_permlane16_swap_b32 v106,v107
v_add_f32 v106,v106,v107
v_mov_b32 v107,v106
M 22,a[164:167],50
s_add_u32 s65,s65,s62
R 78,1,27648
v_cvt_pk_bf16_f32 v117,v32,v33
M 22,a[172:175],54
s_add_u32 s75,s75,s63
v_permlane32_swap_b32 v106,v107
v_add_f32 v106,v106,v107
M 18,a[160:163],50
R 82,1,28672
ds_write_b32 v12,v106 offset:2048
M 18,a[168:171],54
L 6
D 17,56,1024
M 18,a[176:179],58
s_add_u32 s56,s55,5632
R 86,1,29696
M 18,a[184:187],62
buffer_store_dwordx4 v[114:117],v0,s[36:39],s67 offen offset:0 sc0 sc1
M 22,a[180:183],58
R 90,1,30720
M 22,a[188:191],62
L 6
L 2
M 22,a[196:199],66
s_waitcnt vmcnt(7)
R 94,1,31744
v_xor_b32 v1,65536,v1
M 22,a[204:207],70
s_barrier
D 17,56,2048
M 18,a[192:195],66
s_add_u32 s56,s55,5888
R 34,2,0
R 118,13,2048
M 18,a[200:203],70
s_nop 0
buffer_store_dword v4,v3,s[36:39],s70 offen offset:0 sc0 sc1
M 18,a[208:211],74
s_mov_b32 s70,s69
R 38,2,1024
ds_read_b32 v7,v6 offset:0
M 18,a[216:219],78
s_mov_b32 s69,s68
s_add_u32 s55,s55,s60
s_add_u32 s68,s68,s63
M 22,a[212:215],74
R 42,2,2048
M 22,a[220:223],78
L 7
D 17,56,3072
M 22,a[228:231],82
L 3
R 46,2,3072
v_add_f32 v118,v118,v119
M 22,a[236:239],86
v_add_f32 v120,v120,v121
v_add_f32 v118,v118,v120
M 18,a[224:227],82
L 2
R 50,2,4096
v_mul_f32 v118,s86,v118
M 18,a[232:235],86
s_nop 0
buffer_store_dword v118,v14,s[36:39],s77 offen offset:0 sc0 sc1
M 18,a[240:243],90
R 54,2,5120
v_cmp_eq_u32 vcc,s53,v7
M 18,a[248:251],94
s_or_b64 vcc,vcc,s[94:95]
s_cmp_eq_u32 vcc_lo,-1
M 22,a[244:247],90
R 58,2,6144
M 22,a[252:255],94
s_nop 0
s_cbranch_scc1 Lg2_S1_%=
Lg2_N1_%=:
v_mfma_f32_16x16x32_bf16 v[30:33],a[4:7],v[34:37],0
s_add_u32 m0,s59,1024
R 62,2,7168
v_lshlrev_b32 v106,16,v122
M 30,a[12:15],38
s_and_b32 s79,s54,127
buffer_load_dword v5,s[36:39],s58 offen offset:0 sc1 lds
v_mfma_f32_16x16x32_bf16 v[26:29],a[0:3],v[34:37],0
s_cmp_eq_u32 s79,0
R 66,2,8192
v_and_b32 v107,0xffff0000,v122
M 26,a[8:11],38
L 5
buffer_load_dwordx4 v[126:129],v0,s[36:39],s74 offen offset:0 sc1
M 26,a[16:19],42
s_cselect_b32 s94,0x3300,0
R 70,2,9216
v_lshlrev_b32 v108,16,v123
M 26,a[24:27],46
s_cmp_eq_u32 s79,127
buffer_load_dwordx2 v[102:103],v11,s[36:39],s73 offen offset:256 sc1
M 30,a[20:23],42
s_cselect_b32 s94,0xcc00,s94
R 74,2,10240
v_and_b32 v109,0xffff0000,v123
M 30,a[28:31],46
L 5
v_lshlrev_b32 v110,16,v124
v_and_b32 v111,0xffff0000,v124
M 30,a[36:39],50
s_add_u32 s79,s54,s46
R 78,2,11264
v_lshlrev_b32 v112,16,v125
M 30,a[44:47],54
s_and_b32 s79,s79,127
v_and_b32 v113,0xffff0000,v125
v_fma_f32 v106,v106,v99,v98
M 26,a[32:35],50
s_cmp_eq_u32 s79,0
R 82,2,12288
v_fma_f32 v107,v107,v99,v98
M 26,a[40:43],54
L 5
v_fma_f32 v108,v108,v99,v98
v_fma_f32 v109,v109,v99,v98
M 26,a[48:51],58
s_cselect_b32 s95,0x3300,0
R 86,2,13312
v_fma_f32 v110,v110,v99,v98
M 26,a[56:59],62
s_cmp_eq_u32 s79,127
v_fma_f32 v111,v111,v99,v98
v_fma_f32 v112,v112,v99,v98
M 30,a[52:55],58
s_cselect_b32 s95,0xcc00,s95
R 90,2,14336
v_fma_f32 v113,v113,v99,v98
M 30,a[60:63],62
L 5
v_add_f32 v18,v18,v106
v_add_f32 v19,v19,v107
M 30,a[68:71],66
s_mov_b32 s67,s65
R 94,2,15360
v_add_f32 v20,v20,v108
M 30,a[76:79],70
s_mov_b32 s77,s75
v_add_f32 v21,v21,v109
v_add_f32 v22,v22,v110
M 26,a[64:67],66
s_add_u32 s73,s73,s64
R 34,2,16384
v_add_f32 v23,v23,v111
M 26,a[72:75],70
L 5
v_add_f32 v24,v24,v112
v_add_f32 v25,v25,v113
M 26,a[80:83],74
s_add_u32 s74,s74,s62
R 38,2,17408
v_cvt_pk_bf16_f32 v114,v18,v19
M 26,a[88:91],78
s_add_u32 s65,s65,s62
v_cvt_pk_bf16_f32 v115,v20,v21
v_cvt_pk_bf16_f32 v116,v22,v23
M 30,a[84:87],74
s_add_u32 s75,s75,s63
R 42,2,18432
v_cvt_pk_bf16_f32 v117,v24,v25
M 30,a[92:95],78
L 5
buffer_store_dwordx4 v[114:117],v0,s[36:39],s66 offen offset:0 sc0 sc1
v_add_f32 v106,v18,v19
M 30,a[100:103],82
R 46,2,19456
v_fma_f32 v107,v18,v18,s87
M 30,a[108:111],86
v_fma_f32 v107,v19,v19,v107
v_add_f32 v106,v106,v20
M 26,a[96:99],82
R 50,2,20480
v_fma_f32 v107,v20,v20,v107
M 26,a[104:107],86
L 5
v_add_f32 v106,v106,v21
v_fma_f32 v107,v21,v21,v107
M 26,a[112:115],90
R 54,2,21504
v_add_f32 v106,v106,v22
M 26,a[120:123],94
v_fma_f32 v107,v22,v22,v107
v_add_f32 v106,v106,v23
M 30,a[116:119],90
R 58,2,22528
v_fma_f32 v107,v23,v23,v107
M 30,a[124:127],94
L 5
v_add_f32 v106,v106,v24
v_fma_f32 v107,v24,v24,v107
M 30,a[132:135],34
R 62,2,23552
v_add_f32 v106,v106,v25
M 30,a[140:143],38
v_fma_f32 v107,v25,v25,v107
M 26,a[128:131],34
R 66,2,24576
M 26,a[136:139],38
)"
R"(L 5
v_permlane16_swap_b32 v106,v107
M 26,a[144:147],42
R 70,2,25600
v_add_f32 v106,v106,v107
M 26,a[152:155],46
v_mov_b32 v107,v106
M 30,a[148:151],42
R 74,2,26624
M 30,a[156:159],46
L 5
v_permlane32_swap_b32 v106,v107
M 30,a[164:167],50
R 78,2,27648
v_add_f32 v106,v106,v107
M 30,a[172:175],54
ds_write_b32 v12,v106 offset:0
M 26,a[160:163],50
R 82,2,28672
M 26,a[168:171],54
L 6
M 26,a[176:179],58
R 86,2,29696
M 26,a[184:187],62
M 30,a[180:183],58
R 90,2,30720
M 30,a[188:191],62
L 6
L 3
M 30,a[196:199],66
s_waitcnt vmcnt(1)
R 94,2,31744
v_xor_b32 v2,65536,v2
M 30,a[204:207],70
s_barrier
buffer_store_dword v4,v3,s[36:39],s70 offen offset:0 sc0 sc1
M 26,a[192:195],66
s_mov_b32 s70,s69
R 34,1,0
R 118,13,0
M 26,a[200:203],70
s_nop 0
s_mov_b32 s69,s68
ds_read_b32 v7,v6 offset:1024
M 26,a[208:211],74
s_add_u32 s68,s68,s63
R 38,1,1024
M 26,a[216:219],78
M 30,a[212:215],74
R 42,1,2048
M 30,a[220:223],78
L 7
M 30,a[228:231],82
L 3
R 46,1,3072
v_add_f32 v118,v118,v119
M 30,a[236:239],86
L 3
v_add_f32 v120,v120,v121
v_add_f32 v118,v118,v120
M 26,a[224:227],82
R 50,1,4096
v_mul_f32 v118,s86,v118
M 26,a[232:235],86
s_nop 0
buffer_store_dword v118,v14,s[36:39],s76 offen offset:0 sc0 sc1
M 26,a[240:243],90
R 54,1,5120
v_cmp_eq_u32 vcc,s53,v7
M 26,a[248:251],94
s_or_b64 vcc,vcc,s[94:95]
s_cmp_eq_u64 vcc,-1
M 30,a[244:247],90
R 58,1,6144
M 30,a[252:255],94
L 5
s_cbranch_scc1 Lg2_L20_%=
s_cmp_eq_u32 vcc_lo,-1
s_cbranch_scc1 Lg2_L10_%=
Lg2_X0_%=:
v_mfma_f32_16x16x32_bf16 v[22:25],a[4:7],v[34:37],0
s_add_u32 m0,s59,0
R 62,1,7168
v_lshlrev_b32 v106,16,v126
M 22,a[12:15],38
s_and_b32 s79,s54,127
buffer_load_dword v5,s[36:39],s58 offen offset:0 sc1 lds
v_mfma_f32_16x16x32_bf16 v[18:21],a[0:3],v[34:37],0
s_cmp_eq_u32 s79,0
R 66,1,8192
v_and_b32 v107,0xffff0000,v126
M 18,a[8:11],38
L 5
buffer_load_dwordx4 v[122:125],v0,s[36:39],s74 offen offset:0 sc1
M 18,a[16:19],42
s_cselect_b32 s94,0x3300,0
R 70,1,9216
v_lshlrev_b32 v108,16,v127
M 18,a[24:27],46
s_cmp_eq_u32 s79,127
buffer_load_dwordx2 v[98:99],v11,s[36:39],s73 offen offset:256 sc1
M 22,a[20:23],42
s_cselect_b32 s94,0xcc00,s94
R 74,1,10240
v_and_b32 v109,0xffff0000,v127
M 22,a[28:31],46
L 5
v_lshlrev_b32 v110,16,v128
v_and_b32 v111,0xffff0000,v128
M 22,a[36:39],50
s_add_u32 s79,s54,s46
R 78,1,11264
v_lshlrev_b32 v112,16,v129
M 22,a[44:47],54
s_and_b32 s79,s79,127
v_and_b32 v113,0xffff0000,v129
v_fma_f32 v106,v106,v103,v102
M 18,a[32:35],50
s_cmp_eq_u32 s79,0
R 82,1,12288
v_fma_f32 v107,v107,v103,v102
M 18,a[40:43],54
L 5
v_fma_f32 v108,v108,v103,v102
v_fma_f32 v109,v109,v103,v102
M 18,a[48:51],58
s_cselect_b32 s95,0x3300,0
R 86,1,13312
v_fma_f32 v110,v110,v103,v102
M 18,a[56:59],62
s_cmp_eq_u32 s79,127
v_fma_f32 v111,v111,v103,v102
v_fma_f32 v112,v112,v103,v102
M 22,a[52:55],58
s_cselect_b32 s95,0xcc00,s95
R 90,1,14336
v_fma_f32 v113,v113,v103,v102
M 22,a[60:63],62
L 5
v_add_f32 v26,v26,v106
v_add_f32 v27,v27,v107
M 22,a[68:71],66
s_mov_b32 s66,s65
R 94,1,15360
v_add_f32 v28,v28,v108
M 22,a[76:79],70
s_mov_b32 s76,s75
v_add_f32 v29,v29,v109
v_add_f32 v30,v30,v110
M 18,a[64:67],66
s_add_u32 s73,s73,s64
R 34,1,16384
v_add_f32 v31,v31,v111
M 18,a[72:75],70
L 5
v_add_f32 v32,v32,v112
v_add_f32 v33,v33,v113
M 18,a[80:83],74
s_add_u32 s74,s74,s62
R 38,1,17408
v_cvt_pk_bf16_f32 v114,v26,v27
M 18,a[88:91],78
s_add_u32 s65,s65,s62
v_cvt_pk_bf16_f32 v115,v28,v29
v_cvt_pk_bf16_f32 v116,v30,v31
M 22,a[84:87],74
s_add_u32 s75,s75,s63
R 42,1,18432
v_cvt_pk_bf16_f32 v117,v32,v33
M 22,a[92:95],78
L 5
buffer_store_dwordx4 v[114:117],v0,s[36:39],s67 offen offset:0 sc0 sc1
v_add_f32 v106,v26,v27
M 22,a[100:103],82
R 46,1,19456
v_fma_f32 v107,v26,v26,s87
M 22,a[108:111],86
v_fma_f32 v107,v27,v27,v107
v_add_f32 v106,v106,v28
M 18,a[96:99],82
R 50,1,20480
v_fma_f32 v107,v28,v28,v107
M 18,a[104:107],86
L 5
v_add_f32 v106,v106,v29
v_fma_f32 v107,v29,v29,v107
M 18,a[112:115],90
R 54,1,21504
v_add_f32 v106,v106,v30
M 18,a[120:123],94
v_fma_f32 v107,v30,v30,v107
v_add_f32 v106,v106,v31
M 22,a[116:119],90
R 58,1,22528
v_fma_f32 v107,v31,v31,v107
M 22,a[124:127],94
L 5
v_add_f32 v106,v106,v32
v_fma_f32 v107,v32,v32,v107
M 22,a[132:135],34
R 62,1,23552
v_add_f32 v106,v106,v33
M 22,a[140:143],38
v_fma_f32 v107,v33,v33,v107
M 18,a[128:131],34
R 66,1,24576
M 18,a[136:139],38
L 5
v_permlane16_swap_b32 v106,v107
M 18,a[144:147],42
R 70,1,25600
v_add_f32 v106,v106,v107
M 18,a[152:155],46
v_mov_b32 v107,v106
M 22,a[148:151],42
R 74,1,26624
M 22,a[156:159],46
L 5
v_permlane32_swap_b32 v106,v107
M 22,a[164:167],50
R 78,1,27648
v_add_f32 v106,v106,v107
M 22,a[172:175],54
ds_write_b32 v12,v106 offset:2048
M 18,a[160:163],50
R 82,1,28672
M 18,a[168:171],54
L 6
M 18,a[176:179],58
R 86,1,29696
M 18,a[184:187],62
M 22,a[180:183],58
R 90,1,30720
M 22,a[188:191],62
L 6
L 3
M 22,a[196:199],66
s_waitcnt vmcnt(1)
R 94,1,31744
v_xor_b32 v1,65536,v1
M 22,a[204:207],70
s_barrier
buffer_store_dword v4,v3,s[36:39],s70 offen offset:0 sc0 sc1
M 18,a[192:195],66
s_mov_b32 s70,s69
R 118,13,2048
ds_read_b32 v7,v6 offset:0
M 18,a[200:203],70
s_nop 0
s_mov_b32 s69,s68
s_add_u32 s68,s68,s63
M 18,a[208:211],74
M 18,a[216:219],78
M 22,a[212:215],74
M 22,a[220:223],78
L 4
M 22,a[228:231],82
L 1
v_add_f32 v118,v118,v119
v_add_f32 v120,v120,v121
M 22,a[236:239],86
L 0
v_add_f32 v118,v118,v120
v_mul_f32 v118,s86,v118
M 18,a[224:227],82
buffer_store_dword v118,v14,s[36:39],s77 offen offset:0 sc0 sc1
M 18,a[232:235],86
s_nop 0
v_cmp_eq_u32 vcc,s53,v7
M 18,a[240:243],90
s_or_b64 vcc,vcc,s[94:95]
M 18,a[248:251],94
M 22,a[244:247],90
M 22,a[252:255],94
s_mov_b32 s80,1
s_cmp_eq_u32 vcc_lo,-1
s_cbranch_scc0 Lg2_xn0_%=
s_and_b32 s78,s54,127
s_cmp_eq_u32 s78,s85
s_cselect_b64 s[40:41],-1,0
v_cndmask_b32 v17,v15,v16,s[40:41]
s_add_u32 m0,s57,0
s_add_u32 s56,s55,0
D 17,56,0
v_lshlrev_b32 v106,16,v122
v_and_b32 v107,0xffff0000,v122
v_lshlrev_b32 v108,16,v123
v_and_b32 v109,0xffff0000,v123
v_lshlrev_b32 v110,16,v124
s_add_u32 s56,s55,256
D 17,56,1024
v_and_b32 v111,0xffff0000,v124
v_lshlrev_b32 v112,16,v125
v_and_b32 v113,0xffff0000,v125
v_fma_f32 v106,v106,v99,v98
v_fma_f32 v107,v107,v99,v98
s_add_u32 s56,s55,512
D 17,56,2048
v_fma_f32 v108,v108,v99,v98
v_fma_f32 v109,v109,v99,v98
v_fma_f32 v110,v110,v99,v98
v_fma_f32 v111,v111,v99,v98
v_fma_f32 v112,v112,v99,v98
s_add_u32 s56,s55,768
D 17,56,3072
v_fma_f32 v113,v113,v99,v98
v_add_f32 v18,v18,v106
v_add_f32 v19,v19,v107
v_add_f32 v20,v20,v108
v_add_f32 v21,v21,v109
s_add_u32 m0,s57,4096
s_add_u32 s56,s55,5120
D 17,56,0
v_add_f32 v22,v22,v110
v_add_f32 v23,v23,v111
v_add_f32 v24,v24,v112
v_add_f32 v25,v25,v113
v_cvt_pk_bf16_f32 v114,v18,v19
s_add_u32 s56,s55,5376
D 17,56,1024
v_cvt_pk_bf16_f32 v115,v20,v21
v_cvt_pk_bf16_f32 v116,v22,v23
v_cvt_pk_bf16_f32 v117,v24,v25
s_add_u32 s56,s55,5632
D 17,56,2048
s_add_u32 s56,s55,5888
D 17,56,3072
buffer_store_dwordx4 v[114:117],v0,s[36:39],s66 offen offset:0 sc0 sc1
v_add_f32 v106,v18,v19
v_fma_f32 v107,v18,v18,s87
s_add_u32 s55,s55,s60
s_add_u32 s57,s57,32768
s_and_b32 s57,s57,131071
s_add_u32 s54,s54,s46
v_fma_f32 v107,v19,v19,v107
v_add_f32 v106,v106,v20
v_fma_f32 v107,v20,v20,v107
v_add_f32 v106,v106,v21
v_fma_f32 v107,v21,v21,v107
v_add_f32 v106,v106,v22
v_fma_f32 v107,v22,v22,v107
v_add_f32 v106,v106,v23
v_fma_f32 v107,v23,v23,v107
v_add_f32 v106,v106,v24
v_fma_f32 v107,v24,v24,v107
v_add_f32 v106,v106,v25
v_fma_f32 v107,v25,v25,v107
s_nop 1
v_permlane16_swap_b32 v106,v107
v_add_f32 v106,v106,v107
v_mov_b32 v107,v106
s_nop 1
v_permlane32_swap_b32 v106,v107
v_add_f32 v106,v106,v107
ds_write_b32 v12,v106 offset:0
L 0
s_barrier
R 118,13,0
L 0
v_add_f32 v118,v118,v119
v_add_f32 v120,v120,v121
v_add_f32 v118,v118,v120
v_mul_f32 v118,s86,v118
buffer_store_dword v118,v14,s[36:39],s76 offen offset:0 sc0 sc1
s_mov_b32 s72,s69
s_mov_b32 s69,0x7fff0000
s_mov_b32 s66,0x7fff0000
s_mov_b32 s76,0x7fff0000
s_branch Lg2_gox_%=
Lg2_xn0_%=:
s_cmp_ge_u32 s54,256
s_cbranch_scc1 Lg2_fin0_%=
s_add_u32 m0,s59,4096
s_nop 0
buffer_load_dword v5,s[36:39],s58 offen offset:0 sc1 lds
buffer_store_dword v4,v3,s[36:39],s71 offen offset:0 sc0 sc1
s_add_u32 m0,s59,5120
s_nop 0
buffer_load_dword v5,s[36:39],s58 offen offset:0 sc1 lds
buffer_store_dword v4,v3,s[36:39],s71 offen offset:0 sc0 sc1
s_mov_b32 s83,0
v_lshlrev_b32 v106,16,v122
v_and_b32 v107,0xffff0000,v122
v_lshlrev_b32 v108,16,v123
v_and_b32 v109,0xffff0000,v123
v_lshlrev_b32 v110,16,v124
v_and_b32 v111,0xffff0000,v124
v_lshlrev_b32 v112,16,v125
v_and_b32 v113,0xffff0000,v125
v_fma_f32 v106,v106,v99,v98
v_fma_f32 v107,v107,v99,v98
v_fma_f32 v108,v108,v99,v98
v_fma_f32 v109,v109,v99,v98
v_fma_f32 v110,v110,v99,v98
v_fma_f32 v111,v111,v99,v98
v_fma_f32 v112,v112,v99,v98
v_fma_f32 v113,v113,v99,v98
v_add_f32 v18,v18,v106
v_add_f32 v19,v19,v107
v_add_f32 v20,v20,v108
v_add_f32 v21,v21,v109
v_add_f32 v22,v22,v110
v_add_f32 v23,v23,v111
v_add_f32 v24,v24,v112
v_add_f32 v25,v25,v113
v_cvt_pk_bf16_f32 v114,v18,v19
v_cvt_pk_bf16_f32 v115,v20,v21
v_cvt_pk_bf16_f32 v116,v22,v23
v_cvt_pk_bf16_f32 v117,v24,v25
buffer_store_dwordx4 v[114:117],v0,s[36:39],s66 offen offset:0 sc0 sc1
v_add_f32 v106,v18,v19
v_fma_f32 v107,v18,v18,s87
v_fma_f32 v107,v19,v19,v107
v_add_f32 v106,v106,v20
v_fma_f32 v107,v20,v20,v107
v_add_f32 v106,v106,v21
v_fma_f32 v107,v21,v21,v107
v_add_f32 v106,v106,v22
v_fma_f32 v107,v22,v22,v107
v_add_f32 v106,v106,v23
v_fma_f32 v107,v23,v23,v107
v_add_f32 v106,v106,v24
v_fma_f32 v107,v24,v24,v107
v_add_f32 v106,v106,v25
v_fma_f32 v107,v25,v25,v107
s_nop 1
v_permlane16_swap_b32 v106,v107
v_add_f32 v106,v106,v107
v_mov_b32 v107,v106
s_nop 1
v_permlane32_swap_b32 v106,v107
v_add_f32 v106,v106,v107
ds_write_b32 v12,v106 offset:0
L 0
s_barrier
R 118,13,0
L 0
v_add_f32 v118,v118,v119
v_add_f32 v120,v120,v121
v_add_f32 v118,v118,v120
v_mul_f32 v118,s86,v118
buffer_store_dword v118,v14,s[36:39],s76 offen offset:0 sc0 sc1
s_mov_b32 s72,s69
s_mov_b32 s69,0x7fff0000
s_mov_b32 s66,0x7fff0000
s_mov_b32 s76,0x7fff0000
s_branch Lg2_poll_%=
Lg2_fin0_%=:
v_lshlrev_b32 v106,16,v122
v_and_b32 v107,0xffff0000,v122
v_lshlrev_b32 v108,16,v123
v_and_b32 v109,0xffff0000,v123
v_lshlrev_b32 v110,16,v124
v_and_b32 v111,0xffff0000,v124
v_lshlrev_b32 v112,16,v125
v_and_b32 v113,0xffff0000,v125
v_fma_f32 v106,v106,v99,v98
v_fma_f32 v107,v107,v99,v98
v_fma_f32 v108,v108,v99,v98
v_fma_f32 v109,v109,v99,v98
v_fma_f32 v110,v110,v99,v98
v_fma_f32 v111,v111,v99,v98
v_fma_f32 v112,v112,v99,v98
v_fma_f32 v113,v113,v99,v98
v_add_f32 v18,v18,v106
v_add_f32 v19,v19,v107
v_add_f32 v20,v20,v108
v_add_f32 v21,v21,v109
v_add_f32 v22,v22,v110
v_add_f32 v23,v23,v111
v_add_f32 v24,v24,v112
v_add_f32 v25,v25,v113
v_cvt_pk_bf16_f32 v114,v18,v19
v_cvt_pk_bf16_f32 v115,v20,v21
v_cvt_pk_bf16_f32 v116,v22,v23
v_cvt_pk_bf16_f32 v117,v24,v25
buffer_store_dwordx4 v[114:117],v0,s[36:39],s66 offen offset:0 sc0 sc1
v_add_f32 v106,v18,v19
v_fma_f32 v107,v18,v18,s87
v_fma_f32 v107,v19,v19,v107
v_add_f32 v106,v106,v20
v_fma_f32 v107,v20,v20,v107
v_add_f32 v106,v106,v21
v_fma_f32 v107,v21,v21,v107
v_add_f32 v106,v106,v22
v_fma_f32 v107,v22,v22,v107
v_add_f32 v106,v106,v23
v_fma_f32 v107,v23,v23,v107
v_add_f32 v106,v106,v24
v_fma_f32 v107,v24,v24,v107
v_add_f32 v106,v106,v25
v_fma_f32 v107,v25,v25,v107
s_nop 1
v_permlane16_swap_b32 v106,v107
v_add_f32 v106,v106,v107
v_mov_b32 v107,v106
s_nop 1
v_permlane32_swap_b32 v106,v107
v_add_f32 v106,v106,v107
ds_write_b32 v12,v106 offset:0
)"
R"(L 0
s_barrier
R 118,13,0
L 0
v_add_f32 v118,v118,v119
v_add_f32 v120,v120,v121
v_add_f32 v118,v118,v120
v_mul_f32 v118,s86,v118
buffer_store_dword v118,v14,s[36:39],s76 offen offset:0 sc0 sc1
s_mov_b32 s72,s69
s_mov_b32 s69,0x7fff0000
s_mov_b32 s66,0x7fff0000
s_mov_b32 s76,0x7fff0000
s_branch Lg2_done_%=
Lg2_L21_%=:
v_mfma_f32_16x16x32_bf16 v[30:33],a[4:7],v[34:37],0
s_add_u32 s58,s58,s61
s_and_b32 s78,s54,127
s_add_u32 s58,s58,s61
R 62,2,7168
M 30,a[12:15],38
s_add_u32 m0,s59,1024
s_cmp_eq_u32 s78,s85
s_cselect_b64 s[40:41],-1,0
buffer_load_dwordx4 v[126:129],v0,s[36:39],s74 offen offset:0 sc1
v_mfma_f32_16x16x32_bf16 v[26:29],a[0:3],v[34:37],0
s_add_u32 s56,s55,0
R 66,2,8192
v_cndmask_b32 v17,v15,v16,s[40:41]
M 26,a[8:11],38
L 5
buffer_load_dword v8,s[36:39],s58 offen offset:0 sc1 lds
M 26,a[16:19],42
s_add_u32 m0,s57,0
R 70,2,9216
v_lshlrev_b32 v106,16,v122
M 26,a[24:27],46
s_add_u32 s54,s54,s46
buffer_load_dwordx2 v[102:103],v11,s[36:39],s73 offen offset:256 sc1
M 30,a[20:23],42
s_and_b32 s78,s54,127
R 74,2,10240
v_and_b32 v107,0xffff0000,v122
M 30,a[28:31],46
L 5
D 17,56,0
M 30,a[36:39],50
s_add_u32 s56,s55,256
R 78,2,11264
v_lshlrev_b32 v108,16,v123
M 30,a[44:47],54
s_cmp_eq_u32 s78,s85
v_and_b32 v109,0xffff0000,v123
v_lshlrev_b32 v110,16,v124
M 26,a[32:35],50
s_cselect_b64 s[40:41],-1,0
R 82,2,12288
v_and_b32 v111,0xffff0000,v124
M 26,a[40:43],54
L 5
D 17,56,1024
M 26,a[48:51],58
s_add_u32 s56,s55,512
R 86,2,13312
v_lshlrev_b32 v112,16,v125
M 26,a[56:59],62
s_add_u32 s54,s54,s46
v_and_b32 v113,0xffff0000,v125
v_fma_f32 v106,v106,v99,v98
M 30,a[52:55],58
s_and_b32 s79,s54,127
R 90,2,14336
v_fma_f32 v107,v107,v99,v98
M 30,a[60:63],62
L 5
D 17,56,2048
M 30,a[68:71],66
s_add_u32 s56,s55,768
R 94,2,15360
v_fma_f32 v108,v108,v99,v98
M 30,a[76:79],70
s_cmp_eq_u32 s79,0
v_fma_f32 v109,v109,v99,v98
v_fma_f32 v110,v110,v99,v98
M 26,a[64:67],66
s_cselect_b32 s94,0x3300,0
R 34,2,16384
v_fma_f32 v111,v111,v99,v98
M 26,a[72:75],70
L 5
D 17,56,3072
M 26,a[80:83],74
s_add_u32 m0,s57,4096
s_add_u32 s56,s55,5120
R 38,2,17408
M 26,a[88:91],78
s_add_u32 s57,s57,32768
v_fma_f32 v112,v112,v99,v98
v_fma_f32 v113,v113,v99,v98
M 30,a[84:87],74
s_and_b32 s57,s57,131071
R 42,2,18432
v_add_f32 v18,v18,v106
M 30,a[92:95],78
L 5
D 17,56,0
M 30,a[100:103],82
s_add_u32 s56,s55,5376
R 46,2,19456
v_add_f32 v19,v19,v107
M 30,a[108:111],86
s_cmp_eq_u32 s79,127
v_add_f32 v20,v20,v108
v_add_f32 v21,v21,v109
M 26,a[96:99],82
s_cselect_b32 s94,0xcc00,s94
R 50,2,20480
v_fma_f32 v107,v18,v18,s87
M 26,a[104:107],86
L 5
D 17,56,1024
M 26,a[112:115],90
s_add_u32 s56,s55,5632
R 54,2,21504
v_add_f32 v106,v18,v19
M 26,a[120:123],94
s_add_u32 s79,s54,s46
v_fma_f32 v107,v19,v19,v107
v_add_f32 v106,v106,v20
M 30,a[116:119],90
s_and_b32 s79,s79,127
R 58,2,22528
v_fma_f32 v107,v20,v20,v107
M 30,a[124:127],94
L 5
D 17,56,2048
M 30,a[132:135],34
s_add_u32 s56,s55,5888
s_add_u32 s55,s55,s60
R 62,2,23552
M 30,a[140:143],38
s_cmp_eq_u32 s79,0
v_add_f32 v22,v22,v110
v_add_f32 v106,v106,v21
M 26,a[128:131],34
s_cselect_b32 s95,0x3300,0
R 66,2,24576
v_fma_f32 v107,v21,v21,v107
M 26,a[136:139],38
L 5
D 17,56,3072
M 26,a[144:147],42
s_add_u32 m0,s57,0
s_add_u32 s56,s55,0
R 70,2,25600
M 26,a[152:155],46
s_cmp_eq_u32 s79,127
v_add_f32 v23,v23,v111
v_add_f32 v24,v24,v112
v_add_f32 v25,v25,v113
v_add_f32 v106,v106,v22
v_fma_f32 v107,v22,v22,v107
v_add_f32 v106,v106,v23
v_fma_f32 v107,v23,v23,v107
v_add_f32 v106,v106,v24
v_fma_f32 v107,v24,v24,v107
v_add_f32 v106,v106,v25
v_fma_f32 v107,v25,v25,v107
M 30,a[148:151],42
s_cselect_b32 s95,0xcc00,s95
R 74,2,26624
v_cndmask_b32 v17,v15,v16,s[40:41]
M 30,a[156:159],46
L 5
v_permlane16_swap_b32 v106,v107
v_add_f32 v106,v106,v107
v_mov_b32 v107,v106
M 30,a[164:167],50
s_mov_b32 s67,s65
R 78,2,27648
v_cvt_pk_bf16_f32 v114,v18,v19
M 30,a[172:175],54
s_mov_b32 s77,s75
v_permlane32_swap_b32 v106,v107
v_add_f32 v106,v106,v107
M 26,a[160:163],50
s_add_u32 s73,s73,s64
R 82,2,28672
ds_write_b32 v12,v106 offset:0
M 26,a[168:171],54
L 6
D 17,56,0
M 26,a[176:179],58
s_add_u32 s56,s55,256
R 86,2,29696
v_cvt_pk_bf16_f32 v115,v20,v21
M 26,a[184:187],62
s_add_u32 s74,s74,s62
D 17,56,1024
M 30,a[180:183],58
s_add_u32 s56,s55,512
R 90,2,30720
v_cvt_pk_bf16_f32 v116,v22,v23
M 30,a[188:191],62
L 6
L 2
D 17,56,2048
M 30,a[196:199],66
s_waitcnt vmcnt(3)
s_add_u32 s56,s55,768
R 94,2,31744
M 30,a[204:207],70
s_barrier
D 17,56,3072
M 26,a[192:195],66
s_add_u32 m0,s57,4096
s_add_u32 s56,s55,5120
R 34,1,0
M 26,a[200:203],70
s_nop 0
buffer_store_dword v4,v3,s[36:39],s70 offen offset:0 sc0 sc1
M 26,a[208:211],74
s_mov_b32 s70,s69
R 38,1,1024
R 118,13,0
M 26,a[216:219],78
s_add_u32 s57,s57,32768
D 17,56,0
M 30,a[212:215],74
s_add_u32 s56,s55,5376
R 42,1,2048
ds_read_b32 v7,v6 offset:1024
M 30,a[220:223],78
L 7
D 17,56,1024
M 30,a[228:231],82
s_add_u32 s56,s55,5632
R 46,1,3072
v_cvt_pk_bf16_f32 v117,v24,v25
M 30,a[236:239],86
s_mov_b32 s69,s68
D 17,56,2048
M 26,a[224:227],82
s_add_u32 s56,s55,5888
R 50,1,4096
v_xor_b32 v2,65536,v2
M 26,a[232:235],86
L 7
L 4
buffer_store_dwordx4 v[114:117],v0,s[36:39],s66 offen offset:0 sc0 sc1
M 26,a[240:243],90
L 2
R 54,1,5120
v_add_f32 v118,v118,v119
M 26,a[248:251],94
s_add_u32 s55,s55,s60
v_cmp_eq_u32 vcc,s53,v7
v_add_f32 v120,v120,v121
M 30,a[244:247],90
s_and_b32 s57,s57,131071
s_add_u32 s65,s65,s62
s_add_u32 s75,s75,s63
s_add_u32 s68,s68,s63
s_or_b64 vcc,vcc,s[94:95]
s_cmp_eq_u32 vcc_lo,-1
R 58,1,6144
v_add_f32 v118,v118,v120
v_mul_f32 v118,s86,v118
M 30,a[252:255],94
s_nop 0
D 17,56,3072
buffer_store_dword v118,v14,s[36:39],s76 offen offset:0 sc0 sc1
s_cbranch_scc1 Lg2_S0_%=
Lg2_N0_%=:
v_mfma_f32_16x16x32_bf16 v[22:25],a[4:7],v[34:37],0
s_add_u32 m0,s59,0
R 62,1,7168
v_lshlrev_b32 v106,16,v126
M 22,a[12:15],38
s_and_b32 s79,s54,127
buffer_load_dword v5,s[36:39],s58 offen offset:0 sc1 lds
v_mfma_f32_16x16x32_bf16 v[18:21],a[0:3],v[34:37],0
s_cmp_eq_u32 s79,0
R 66,1,8192
v_and_b32 v107,0xffff0000,v126
M 18,a[8:11],38
L 5
buffer_load_dwordx4 v[122:125],v0,s[36:39],s74 offen offset:0 sc1
M 18,a[16:19],42
s_cselect_b32 s94,0x3300,0
R 70,1,9216
v_lshlrev_b32 v108,16,v127
M 18,a[24:27],46
s_cmp_eq_u32 s79,127
buffer_load_dwordx2 v[98:99],v11,s[36:39],s73 offen offset:256 sc1
M 22,a[20:23],42
s_cselect_b32 s94,0xcc00,s94
R 74,1,10240
v_and_b32 v109,0xffff0000,v127
M 22,a[28:31],46
L 5
v_lshlrev_b32 v110,16,v128
v_and_b32 v111,0xffff0000,v128
M 22,a[36:39],50
s_add_u32 s79,s54,s46
R 78,1,11264
v_lshlrev_b32 v112,16,v129
M 22,a[44:47],54
s_and_b32 s79,s79,127
v_and_b32 v113,0xffff0000,v129
v_fma_f32 v106,v106,v103,v102
M 18,a[32:35],50
s_cmp_eq_u32 s79,0
R 82,1,12288
v_fma_f32 v107,v107,v103,v102
M 18,a[40:43],54
L 5
v_fma_f32 v108,v108,v103,v102
v_fma_f32 v109,v109,v103,v102
M 18,a[48:51],58
s_cselect_b32 s95,0x3300,0
R 86,1,13312
v_fma_f32 v110,v110,v103,v102
M 18,a[56:59],62
s_cmp_eq_u32 s79,127
v_fma_f32 v111,v111,v103,v102
v_fma_f32 v112,v112,v103,v102
M 22,a[52:55],58
s_cselect_b32 s95,0xcc00,s95
R 90,1,14336
v_fma_f32 v113,v113,v103,v102
M 22,a[60:63],62
L 5
v_add_f32 v26,v26,v106
v_add_f32 v27,v27,v107
M 22,a[68:71],66
s_mov_b32 s66,s65
R 94,1,15360
v_add_f32 v28,v28,v108
M 22,a[76:79],70
s_mov_b32 s76,s75
v_add_f32 v29,v29,v109
v_add_f32 v30,v30,v110
M 18,a[64:67],66
s_add_u32 s73,s73,s64
R 34,1,16384
v_add_f32 v31,v31,v111
M 18,a[72:75],70
L 5
v_add_f32 v32,v32,v112
v_add_f32 v33,v33,v113
M 18,a[80:83],74
s_add_u32 s74,s74,s62
R 38,1,17408
v_cvt_pk_bf16_f32 v114,v26,v27
M 18,a[88:91],78
s_add_u32 s65,s65,s62
v_cvt_pk_bf16_f32 v115,v28,v29
v_cvt_pk_bf16_f32 v116,v30,v31
M 22,a[84:87],74
s_add_u32 s75,s75,s63
R 42,1,18432
v_cvt_pk_bf16_f32 v117,v32,v33
M 22,a[92:95],78
L 5
buffer_store_dwordx4 v[114:117],v0,s[36:39],s67 offen offset:0 sc0 sc1
v_add_f32 v106,v26,v27
M 22,a[100:103],82
R 46,1,19456
v_fma_f32 v107,v26,v26,s87
M 22,a[108:111],86
v_fma_f32 v107,v27,v27,v107
v_add_f32 v106,v106,v28
M 18,a[96:99],82
R 50,1,20480
v_fma_f32 v107,v28,v28,v107
M 18,a[104:107],86
L 5
v_add_f32 v106,v106,v29
v_fma_f32 v107,v29,v29,v107
M 18,a[112:115],90
R 54,1,21504
v_add_f32 v106,v106,v30
M 18,a[120:123],94
v_fma_f32 v107,v30,v30,v107
v_add_f32 v106,v106,v31
M 22,a[116:119],90
R 58,1,22528
v_fma_f32 v107,v31,v31,v107
M 22,a[124:127],94
L 5
v_add_f32 v106,v106,v32
v_fma_f32 v107,v32,v32,v107
M 22,a[132:135],34
R 62,1,23552
v_add_f32 v106,v106,v33
M 22,a[140:143],38
v_fma_f32 v107,v33,v33,v107
M 18,a[128:131],34
R 66,1,24576
M 18,a[136:139],38
L 5
v_permlane16_swap_b32 v106,v107
M 18,a[144:147],42
R 70,1,25600
v_add_f32 v106,v106,v107
M 18,a[152:155],46
v_mov_b32 v107,v106
M 22,a[148:151],42
R 74,1,26624
M 22,a[156:159],46
L 5
v_permlane32_swap_b32 v106,v107
M 22,a[164:167],50
R 78,1,27648
v_add_f32 v106,v106,v107
M 22,a[172:175],54
ds_write_b32 v12,v106 offset:2048
M 18,a[160:163],50
R 82,1,28672
M 18,a[168:171],54
L 6
M 18,a[176:179],58
R 86,1,29696
M 18,a[184:187],62
M 22,a[180:183],58
R 90,1,30720
M 22,a[188:191],62
L 6
L 3
M 22,a[196:199],66
s_waitcnt vmcnt(1)
R 94,1,31744
v_xor_b32 v1,65536,v1
M 22,a[204:207],70
s_barrier
buffer_store_dword v4,v3,s[36:39],s70 offen offset:0 sc0 sc1
M 18,a[192:195],66
s_mov_b32 s70,s69
R 34,2,0
R 118,13,2048
M 18,a[200:203],70
s_nop 0
s_mov_b32 s69,s68
ds_read_b32 v7,v6 offset:0
M 18,a[208:211],74
s_add_u32 s68,s68,s63
R 38,2,1024
M 18,a[216:219],78
M 22,a[212:215],74
R 42,2,2048
M 22,a[220:223],78
L 7
M 22,a[228:231],82
L 3
R 46,2,3072
v_add_f32 v118,v118,v119
M 22,a[236:239],86
L 3
v_add_f32 v120,v120,v121
v_add_f32 v118,v118,v120
M 18,a[224:227],82
R 50,2,4096
v_mul_f32 v118,s86,v118
M 18,a[232:235],86
s_nop 0
buffer_store_dword v118,v14,s[36:39],s77 offen offset:0 sc0 sc1
M 18,a[240:243],90
R 54,2,5120
v_cmp_eq_u32 vcc,s53,v7
M 18,a[248:251],94
s_or_b64 vcc,vcc,s[94:95]
s_cmp_eq_u64 vcc,-1
M 22,a[244:247],90
R 58,2,6144
M 22,a[252:255],94
L 5
s_cbranch_scc1 Lg2_L21_%=
s_cmp_eq_u32 vcc_lo,-1
s_cbranch_scc1 Lg2_L11_%=
Lg2_X1_%=:
v_mfma_f32_16x16x32_bf16 v[30:33],a[4:7],v[34:37],0
s_add_u32 m0,s59,1024
R 62,2,7168
v_lshlrev_b32 v106,16,v122
M 30,a[12:15],38
s_and_b32 s79,s54,127
buffer_load_dword v5,s[36:39],s58 offen offset:0 sc1 lds
v_mfma_f32_16x16x32_bf16 v[26:29],a[0:3],v[34:37],0
s_cmp_eq_u32 s79,0
R 66,2,8192
v_and_b32 v107,0xffff0000,v122
M 26,a[8:11],38
L 5
buffer_load_dwordx4 v[126:129],v0,s[36:39],s74 offen offset:0 sc1
M 26,a[16:19],42
s_cselect_b32 s94,0x3300,0
R 70,2,9216
v_lshlrev_b32 v108,16,v123
M 26,a[24:27],46
)"
R"(s_cmp_eq_u32 s79,127
buffer_load_dwordx2 v[102:103],v11,s[36:39],s73 offen offset:256 sc1
M 30,a[20:23],42
s_cselect_b32 s94,0xcc00,s94
R 74,2,10240
v_and_b32 v109,0xffff0000,v123
M 30,a[28:31],46
L 5
v_lshlrev_b32 v110,16,v124
v_and_b32 v111,0xffff0000,v124
M 30,a[36:39],50
s_add_u32 s79,s54,s46
R 78,2,11264
v_lshlrev_b32 v112,16,v125
M 30,a[44:47],54
s_and_b32 s79,s79,127
v_and_b32 v113,0xffff0000,v125
v_fma_f32 v106,v106,v99,v98
M 26,a[32:35],50
s_cmp_eq_u32 s79,0
R 82,2,12288
v_fma_f32 v107,v107,v99,v98
M 26,a[40:43],54
L 5
v_fma_f32 v108,v108,v99,v98
v_fma_f32 v109,v109,v99,v98
M 26,a[48:51],58
s_cselect_b32 s95,0x3300,0
R 86,2,13312
v_fma_f32 v110,v110,v99,v98
M 26,a[56:59],62
s_cmp_eq_u32 s79,127
v_fma_f32 v111,v111,v99,v98
v_fma_f32 v112,v112,v99,v98
M 30,a[52:55],58
s_cselect_b32 s95,0xcc00,s95
R 90,2,14336
v_fma_f32 v113,v113,v99,v98
M 30,a[60:63],62
L 5
v_add_f32 v18,v18,v106
v_add_f32 v19,v19,v107
M 30,a[68:71],66
s_mov_b32 s67,s65
R 94,2,15360
v_add_f32 v20,v20,v108
M 30,a[76:79],70
s_mov_b32 s77,s75
v_add_f32 v21,v21,v109
v_add_f32 v22,v22,v110
M 26,a[64:67],66
s_add_u32 s73,s73,s64
R 34,2,16384
v_add_f32 v23,v23,v111
M 26,a[72:75],70
L 5
v_add_f32 v24,v24,v112
v_add_f32 v25,v25,v113
M 26,a[80:83],74
s_add_u32 s74,s74,s62
R 38,2,17408
v_cvt_pk_bf16_f32 v114,v18,v19
M 26,a[88:91],78
s_add_u32 s65,s65,s62
v_cvt_pk_bf16_f32 v115,v20,v21
v_cvt_pk_bf16_f32 v116,v22,v23
M 30,a[84:87],74
s_add_u32 s75,s75,s63
R 42,2,18432
v_cvt_pk_bf16_f32 v117,v24,v25
M 30,a[92:95],78
L 5
buffer_store_dwordx4 v[114:117],v0,s[36:39],s66 offen offset:0 sc0 sc1
v_add_f32 v106,v18,v19
M 30,a[100:103],82
R 46,2,19456
v_fma_f32 v107,v18,v18,s87
M 30,a[108:111],86
v_fma_f32 v107,v19,v19,v107
v_add_f32 v106,v106,v20
M 26,a[96:99],82
R 50,2,20480
v_fma_f32 v107,v20,v20,v107
M 26,a[104:107],86
L 5
v_add_f32 v106,v106,v21
v_fma_f32 v107,v21,v21,v107
M 26,a[112:115],90
R 54,2,21504
v_add_f32 v106,v106,v22
M 26,a[120:123],94
v_fma_f32 v107,v22,v22,v107
v_add_f32 v106,v106,v23
M 30,a[116:119],90
R 58,2,22528
v_fma_f32 v107,v23,v23,v107
M 30,a[124:127],94
L 5
v_add_f32 v106,v106,v24
v_fma_f32 v107,v24,v24,v107
M 30,a[132:135],34
R 62,2,23552
v_add_f32 v106,v106,v25
M 30,a[140:143],38
v_fma_f32 v107,v25,v25,v107
M 26,a[128:131],34
R 66,2,24576
M 26,a[136:139],38
L 5
v_permlane16_swap_b32 v106,v107
M 26,a[144:147],42
R 70,2,25600
v_add_f32 v106,v106,v107
M 26,a[152:155],46
v_mov_b32 v107,v106
M 30,a[148:151],42
R 74,2,26624
M 30,a[156:159],46
L 5
v_permlane32_swap_b32 v106,v107
M 30,a[164:167],50
R 78,2,27648
v_add_f32 v106,v106,v107
M 30,a[172:175],54
ds_write_b32 v12,v106 offset:0
M 26,a[160:163],50
R 82,2,28672
M 26,a[168:171],54
L 6
M 26,a[176:179],58
R 86,2,29696
M 26,a[184:187],62
M 30,a[180:183],58
R 90,2,30720
M 30,a[188:191],62
L 6
L 3
M 30,a[196:199],66
s_waitcnt vmcnt(1)
R 94,2,31744
v_xor_b32 v2,65536,v2
M 30,a[204:207],70
s_barrier
buffer_store_dword v4,v3,s[36:39],s70 offen offset:0 sc0 sc1
M 26,a[192:195],66
s_mov_b32 s70,s69
R 118,13,0
ds_read_b32 v7,v6 offset:1024
M 26,a[200:203],70
s_nop 0
s_mov_b32 s69,s68
s_add_u32 s68,s68,s63
M 26,a[208:211],74
M 26,a[216:219],78
M 30,a[212:215],74
M 30,a[220:223],78
L 4
M 30,a[228:231],82
L 1
v_add_f32 v118,v118,v119
v_add_f32 v120,v120,v121
M 30,a[236:239],86
L 0
v_add_f32 v118,v118,v120
v_mul_f32 v118,s86,v118
M 26,a[224:227],82
buffer_store_dword v118,v14,s[36:39],s76 offen offset:0 sc0 sc1
M 26,a[232:235],86
s_nop 0
v_cmp_eq_u32 vcc,s53,v7
M 26,a[240:243],90
s_or_b64 vcc,vcc,s[94:95]
M 26,a[248:251],94
M 30,a[244:247],90
M 30,a[252:255],94
s_mov_b32 s80,0
s_cmp_eq_u32 vcc_lo,-1
s_cbranch_scc0 Lg2_xn1_%=
s_and_b32 s78,s54,127
s_cmp_eq_u32 s78,s85
s_cselect_b64 s[40:41],-1,0
v_cndmask_b32 v17,v15,v16,s[40:41]
s_add_u32 m0,s57,0
s_add_u32 s56,s55,0
D 17,56,0
v_lshlrev_b32 v106,16,v126
v_and_b32 v107,0xffff0000,v126
v_lshlrev_b32 v108,16,v127
v_and_b32 v109,0xffff0000,v127
v_lshlrev_b32 v110,16,v128
s_add_u32 s56,s55,256
D 17,56,1024
v_and_b32 v111,0xffff0000,v128
v_lshlrev_b32 v112,16,v129
v_and_b32 v113,0xffff0000,v129
v_fma_f32 v106,v106,v103,v102
v_fma_f32 v107,v107,v103,v102
s_add_u32 s56,s55,512
D 17,56,2048
v_fma_f32 v108,v108,v103,v102
v_fma_f32 v109,v109,v103,v102
v_fma_f32 v110,v110,v103,v102
v_fma_f32 v111,v111,v103,v102
v_fma_f32 v112,v112,v103,v102
s_add_u32 s56,s55,768
D 17,56,3072
v_fma_f32 v113,v113,v103,v102
v_add_f32 v26,v26,v106
v_add_f32 v27,v27,v107
v_add_f32 v28,v28,v108
v_add_f32 v29,v29,v109
s_add_u32 m0,s57,4096
s_add_u32 s56,s55,5120
D 17,56,0
v_add_f32 v30,v30,v110
v_add_f32 v31,v31,v111
v_add_f32 v32,v32,v112
v_add_f32 v33,v33,v113
v_cvt_pk_bf16_f32 v114,v26,v27
s_add_u32 s56,s55,5376
D 17,56,1024
v_cvt_pk_bf16_f32 v115,v28,v29
v_cvt_pk_bf16_f32 v116,v30,v31
v_cvt_pk_bf16_f32 v117,v32,v33
s_add_u32 s56,s55,5632
D 17,56,2048
s_add_u32 s56,s55,5888
D 17,56,3072
buffer_store_dwordx4 v[114:117],v0,s[36:39],s67 offen offset:0 sc0 sc1
v_add_f32 v106,v26,v27
v_fma_f32 v107,v26,v26,s87
s_add_u32 s55,s55,s60
s_add_u32 s57,s57,32768
s_and_b32 s57,s57,131071
s_add_u32 s54,s54,s46
v_fma_f32 v107,v27,v27,v107
v_add_f32 v106,v106,v28
v_fma_f32 v107,v28,v28,v107
v_add_f32 v106,v106,v29
v_fma_f32 v107,v29,v29,v107
v_add_f32 v106,v106,v30
v_fma_f32 v107,v30,v30,v107
v_add_f32 v106,v106,v31
v_fma_f32 v107,v31,v31,v107
v_add_f32 v106,v106,v32
v_fma_f32 v107,v32,v32,v107
v_add_f32 v106,v106,v33
v_fma_f32 v107,v33,v33,v107
s_nop 1
v_permlane16_swap_b32 v106,v107
v_add_f32 v106,v106,v107
v_mov_b32 v107,v106
s_nop 1
v_permlane32_swap_b32 v106,v107
v_add_f32 v106,v106,v107
ds_write_b32 v12,v106 offset:2048
L 0
s_barrier
R 118,13,2048
L 0
v_add_f32 v118,v118,v119
v_add_f32 v120,v120,v121
v_add_f32 v118,v118,v120
v_mul_f32 v118,s86,v118
buffer_store_dword v118,v14,s[36:39],s77 offen offset:0 sc0 sc1
s_mov_b32 s72,s69
s_mov_b32 s69,0x7fff0000
s_mov_b32 s67,0x7fff0000
s_mov_b32 s77,0x7fff0000
s_branch Lg2_gox_%=
Lg2_xn1_%=:
s_cmp_ge_u32 s54,256
s_cbranch_scc1 Lg2_fin1_%=
s_add_u32 m0,s59,4096
s_nop 0
buffer_load_dword v5,s[36:39],s58 offen offset:0 sc1 lds
buffer_store_dword v4,v3,s[36:39],s71 offen offset:0 sc0 sc1
s_add_u32 m0,s59,5120
s_nop 0
buffer_load_dword v5,s[36:39],s58 offen offset:0 sc1 lds
buffer_store_dword v4,v3,s[36:39],s71 offen offset:0 sc0 sc1
s_mov_b32 s83,0
v_lshlrev_b32 v106,16,v126
v_and_b32 v107,0xffff0000,v126
v_lshlrev_b32 v108,16,v127
v_and_b32 v109,0xffff0000,v127
v_lshlrev_b32 v110,16,v128
v_and_b32 v111,0xffff0000,v128
v_lshlrev_b32 v112,16,v129
v_and_b32 v113,0xffff0000,v129
v_fma_f32 v106,v106,v103,v102
v_fma_f32 v107,v107,v103,v102
v_fma_f32 v108,v108,v103,v102
v_fma_f32 v109,v109,v103,v102
v_fma_f32 v110,v110,v103,v102
v_fma_f32 v111,v111,v103,v102
v_fma_f32 v112,v112,v103,v102
v_fma_f32 v113,v113,v103,v102
v_add_f32 v26,v26,v106
v_add_f32 v27,v27,v107
v_add_f32 v28,v28,v108
v_add_f32 v29,v29,v109
v_add_f32 v30,v30,v110
v_add_f32 v31,v31,v111
v_add_f32 v32,v32,v112
v_add_f32 v33,v33,v113
v_cvt_pk_bf16_f32 v114,v26,v27
v_cvt_pk_bf16_f32 v115,v28,v29
v_cvt_pk_bf16_f32 v116,v30,v31
v_cvt_pk_bf16_f32 v117,v32,v33
buffer_store_dwordx4 v[114:117],v0,s[36:39],s67 offen offset:0 sc0 sc1
v_add_f32 v106,v26,v27
v_fma_f32 v107,v26,v26,s87
v_fma_f32 v107,v27,v27,v107
v_add_f32 v106,v106,v28
v_fma_f32 v107,v28,v28,v107
v_add_f32 v106,v106,v29
v_fma_f32 v107,v29,v29,v107
v_add_f32 v106,v106,v30
v_fma_f32 v107,v30,v30,v107
v_add_f32 v106,v106,v31
v_fma_f32 v107,v31,v31,v107
v_add_f32 v106,v106,v32
v_fma_f32 v107,v32,v32,v107
v_add_f32 v106,v106,v33
v_fma_f32 v107,v33,v33,v107
s_nop 1
v_permlane16_swap_b32 v106,v107
v_add_f32 v106,v106,v107
v_mov_b32 v107,v106
s_nop 1
v_permlane32_swap_b32 v106,v107
v_add_f32 v106,v106,v107
ds_write_b32 v12,v106 offset:2048
L 0
s_barrier
R 118,13,2048
L 0
v_add_f32 v118,v118,v119
v_add_f32 v120,v120,v121
v_add_f32 v118,v118,v120
v_mul_f32 v118,s86,v118
buffer_store_dword v118,v14,s[36:39],s77 offen offset:0 sc0 sc1
s_mov_b32 s72,s69
s_mov_b32 s69,0x7fff0000
s_mov_b32 s67,0x7fff0000
s_mov_b32 s77,0x7fff0000
s_branch Lg2_poll_%=
Lg2_fin1_%=:
v_lshlrev_b32 v106,16,v126
v_and_b32 v107,0xffff0000,v126
v_lshlrev_b32 v108,16,v127
v_and_b32 v109,0xffff0000,v127
v_lshlrev_b32 v110,16,v128
v_and_b32 v111,0xffff0000,v128
v_lshlrev_b32 v112,16,v129
v_and_b32 v113,0xffff0000,v129
v_fma_f32 v106,v106,v103,v102
v_fma_f32 v107,v107,v103,v102
v_fma_f32 v108,v108,v103,v102
v_fma_f32 v109,v109,v103,v102
v_fma_f32 v110,v110,v103,v102
v_fma_f32 v111,v111,v103,v102
v_fma_f32 v112,v112,v103,v102
v_fma_f32 v113,v113,v103,v102
v_add_f32 v26,v26,v106
v_add_f32 v27,v27,v107
v_add_f32 v28,v28,v108
v_add_f32 v29,v29,v109
v_add_f32 v30,v30,v110
v_add_f32 v31,v31,v111
v_add_f32 v32,v32,v112
v_add_f32 v33,v33,v113
v_cvt_pk_bf16_f32 v114,v26,v27
v_cvt_pk_bf16_f32 v115,v28,v29
v_cvt_pk_bf16_f32 v116,v30,v31
v_cvt_pk_bf16_f32 v117,v32,v33
buffer_store_dwordx4 v[114:117],v0,s[36:39],s67 offen offset:0 sc0 sc1
v_add_f32 v106,v26,v27
v_fma_f32 v107,v26,v26,s87
v_fma_f32 v107,v27,v27,v107
v_add_f32 v106,v106,v28
v_fma_f32 v107,v28,v28,v107
v_add_f32 v106,v106,v29
v_fma_f32 v107,v29,v29,v107
v_add_f32 v106,v106,v30
v_fma_f32 v107,v30,v30,v107
v_add_f32 v106,v106,v31
v_fma_f32 v107,v31,v31,v107
v_add_f32 v106,v106,v32
v_fma_f32 v107,v32,v32,v107
v_add_f32 v106,v106,v33
v_fma_f32 v107,v33,v33,v107
s_nop 1
v_permlane16_swap_b32 v106,v107
v_add_f32 v106,v106,v107
v_mov_b32 v107,v106
s_nop 1
v_permlane32_swap_b32 v106,v107
v_add_f32 v106,v106,v107
ds_write_b32 v12,v106 offset:2048
L 0
s_barrier
R 118,13,2048
L 0
v_add_f32 v118,v118,v119
v_add_f32 v120,v120,v121
v_add_f32 v118,v118,v120
v_mul_f32 v118,s86,v118
buffer_store_dword v118,v14,s[36:39],s77 offen offset:0 sc0 sc1
s_mov_b32 s72,s69
s_mov_b32 s69,0x7fff0000
s_mov_b32 s67,0x7fff0000
s_mov_b32 s77,0x7fff0000
s_branch Lg2_done_%=
Lg2_L10_%=:
v_mfma_f32_16x16x32_bf16 v[22:25],a[4:7],v[34:37],0
s_and_b32 s78,s54,127
s_add_u32 s58,s58,s61
s_add_u32 m0,s59,0
R 62,1,7168
M 22,a[12:15],38
s_cmp_eq_u32 s78,s85
s_cselect_b64 s[40:41],-1,0
s_add_u32 s56,s55,0
buffer_load_dword v5,s[36:39],s58 offen offset:0 sc1 lds
v_mfma_f32_16x16x32_bf16 v[18:21],a[0:3],v[34:37],0
s_add_u32 m0,s57,0
R 66,1,8192
v_cndmask_b32 v17,v15,v16,s[40:41]
M 18,a[8:11],38
L 5
buffer_load_dwordx4 v[122:125],v0,s[36:39],s74 offen offset:0 sc1
M 18,a[16:19],42
s_add_u32 s54,s54,s46
R 70,1,9216
v_lshlrev_b32 v106,16,v126
M 18,a[24:27],46
s_and_b32 s79,s54,127
buffer_load_dwordx2 v[98:99],v11,s[36:39],s73 offen offset:256 sc1
M 22,a[20:23],42
s_cmp_eq_u32 s79,0
R 74,1,10240
v_and_b32 v107,0xffff0000,v126
M 22,a[28:31],46
L 5
D 17,56,0
M 22,a[36:39],50
s_cselect_b32 s94,0x3300,0
s_add_u32 s56,s55,256
R 78,1,11264
M 22,a[44:47],54
s_cmp_eq_u32 s79,127
v_lshlrev_b32 v108,16,v127
v_and_b32 v109,0xffff0000,v127
M 18,a[32:35],50
s_cselect_b32 s94,0xcc00,s94
R 82,1,12288
v_lshlrev_b32 v110,16,v128
M 18,a[40:43],54
L 5
D 17,56,1024
M 18,a[48:51],58
s_add_u32 s56,s55,512
R 86,1,13312
v_and_b32 v111,0xffff0000,v128
M 18,a[56:59],62
s_add_u32 s79,s54,s46
v_lshlrev_b32 v112,16,v129
v_and_b32 v113,0xffff0000,v129
M 22,a[52:55],58
s_and_b32 s79,s79,127
R 90,1,14336
v_fma_f32 v106,v106,v103,v102
M 22,a[60:63],62
L 5
D 17,56,2048
M 22,a[68:71],66
s_add_u32 s56,s55,768
R 94,1,15360
v_fma_f32 v107,v107,v103,v102
M 22,a[76:79],70
s_cmp_eq_u32 s79,0
v_fma_f32 v108,v108,v103,v102
v_fma_f32 v109,v109,v103,v102
M 18,a[64:67],66
s_cselect_b32 s95,0x3300,0
R 34,1,16384
v_fma_f32 v110,v110,v103,v102
M 18,a[72:75],70
L 5
D 17,56,3072
M 18,a[80:83],74
s_add_u32 m0,s57,4096
s_add_u32 s56,s55,5120
R 38,1,17408
M 18,a[88:91],78
s_cmp_eq_u32 s79,127
v_fma_f32 v111,v111,v103,v102
v_fma_f32 v112,v112,v103,v102
)"
R"(M 22,a[84:87],74
s_cselect_b32 s95,0xcc00,s95
R 42,1,18432
v_fma_f32 v113,v113,v103,v102
M 22,a[92:95],78
L 5
D 17,56,0
M 22,a[100:103],82
s_add_u32 s56,s55,5376
R 46,1,19456
v_add_f32 v26,v26,v106
M 22,a[108:111],86
s_add_u32 s57,s57,32768
v_add_f32 v27,v27,v107
v_add_f32 v28,v28,v108
M 18,a[96:99],82
s_mov_b32 s66,s65
R 50,1,20480
v_fma_f32 v107,v26,v26,s87
M 18,a[104:107],86
L 5
D 17,56,1024
M 18,a[112:115],90
s_add_u32 s56,s55,5632
R 54,1,21504
v_add_f32 v106,v26,v27
M 18,a[120:123],94
s_mov_b32 s76,s75
v_fma_f32 v107,v27,v27,v107
v_add_f32 v29,v29,v109
M 22,a[116:119],90
s_add_u32 s73,s73,s64
R 58,1,22528
v_add_f32 v106,v106,v28
M 22,a[124:127],94
L 5
D 17,56,2048
M 22,a[132:135],34
s_add_u32 s56,s55,5888
R 62,1,23552
v_fma_f32 v107,v28,v28,v107
M 22,a[140:143],38
s_add_u32 s74,s74,s62
v_add_f32 v30,v30,v110
v_add_f32 v106,v106,v29
M 18,a[128:131],34
s_add_u32 s55,s55,s60
R 66,1,24576
v_fma_f32 v107,v29,v29,v107
M 18,a[136:139],38
L 5
D 17,56,3072
M 18,a[144:147],42
s_and_b32 s57,s57,131071
R 70,1,25600
v_add_f32 v31,v31,v111
M 18,a[152:155],46
s_add_u32 s65,s65,s62
v_add_f32 v32,v32,v112
v_add_f32 v33,v33,v113
v_add_f32 v106,v106,v30
v_fma_f32 v107,v30,v30,v107
v_add_f32 v106,v106,v31
v_fma_f32 v107,v31,v31,v107
v_add_f32 v106,v106,v32
v_fma_f32 v107,v32,v32,v107
v_add_f32 v106,v106,v33
v_fma_f32 v107,v33,v33,v107
M 22,a[148:151],42
s_add_u32 s75,s75,s63
R 74,1,26624
v_cvt_pk_bf16_f32 v114,v26,v27
M 22,a[156:159],46
L 5
v_permlane16_swap_b32 v106,v107
v_add_f32 v106,v106,v107
v_mov_b32 v107,v106
M 22,a[164:167],50
R 78,1,27648
v_cvt_pk_bf16_f32 v115,v28,v29
M 22,a[172:175],54
v_permlane32_swap_b32 v106,v107
v_add_f32 v106,v106,v107
M 18,a[160:163],50
R 82,1,28672
ds_write_b32 v12,v106 offset:2048
M 18,a[168:171],54
L 6
v_cvt_pk_bf16_f32 v116,v30,v31
v_cvt_pk_bf16_f32 v117,v32,v33
M 18,a[176:179],58
R 86,1,29696
M 18,a[184:187],62
buffer_store_dwordx4 v[114:117],v0,s[36:39],s67 offen offset:0 sc0 sc1
M 22,a[180:183],58
R 90,1,30720
M 22,a[188:191],62
L 6
L 2
M 22,a[196:199],66
s_waitcnt vmcnt(1)
R 94,1,31744
v_xor_b32 v1,65536,v1
M 22,a[204:207],70
s_barrier
buffer_store_dword v4,v3,s[36:39],s70 offen offset:0 sc0 sc1
M 18,a[192:195],66
s_mov_b32 s70,s69
R 34,2,0
R 118,13,2048
M 18,a[200:203],70
s_nop 0
s_mov_b32 s69,s68
ds_read_b32 v7,v6 offset:0
M 18,a[208:211],74
s_add_u32 s68,s68,s63
R 38,2,1024
M 18,a[216:219],78
M 22,a[212:215],74
R 42,2,2048
M 22,a[220:223],78
L 7
M 22,a[228:231],82
L 3
R 46,2,3072
v_add_f32 v118,v118,v119
M 22,a[236:239],86
L 3
v_add_f32 v120,v120,v121
v_add_f32 v118,v118,v120
M 18,a[224:227],82
R 50,2,4096
v_mul_f32 v118,s86,v118
M 18,a[232:235],86
s_nop 0
buffer_store_dword v118,v14,s[36:39],s77 offen offset:0 sc0 sc1
M 18,a[240:243],90
R 54,2,5120
v_cmp_eq_u32 vcc,s53,v7
M 18,a[248:251],94
s_or_b64 vcc,vcc,s[94:95]
s_cmp_eq_u64 vcc,-1
M 22,a[244:247],90
R 58,2,6144
M 22,a[252:255],94
L 5
s_cbranch_scc1 Lg2_L21_%=
s_cmp_eq_u32 vcc_lo,-1
s_cbranch_scc1 Lg2_L11_%=
s_branch Lg2_X1_%=
Lg2_L11_%=:
v_mfma_f32_16x16x32_bf16 v[30:33],a[4:7],v[34:37],0
s_and_b32 s78,s54,127
s_add_u32 s58,s58,s61
s_add_u32 m0,s59,1024
R 62,2,7168
M 30,a[12:15],38
s_cmp_eq_u32 s78,s85
s_cselect_b64 s[40:41],-1,0
s_add_u32 s56,s55,0
buffer_load_dword v5,s[36:39],s58 offen offset:0 sc1 lds
v_mfma_f32_16x16x32_bf16 v[26:29],a[0:3],v[34:37],0
s_add_u32 m0,s57,0
R 66,2,8192
v_cndmask_b32 v17,v15,v16,s[40:41]
M 26,a[8:11],38
L 5
buffer_load_dwordx4 v[126:129],v0,s[36:39],s74 offen offset:0 sc1
M 26,a[16:19],42
s_add_u32 s54,s54,s46
R 70,2,9216
v_lshlrev_b32 v106,16,v122
M 26,a[24:27],46
s_and_b32 s79,s54,127
buffer_load_dwordx2 v[102:103],v11,s[36:39],s73 offen offset:256 sc1
M 30,a[20:23],42
s_cmp_eq_u32 s79,0
R 74,2,10240
v_and_b32 v107,0xffff0000,v122
M 30,a[28:31],46
L 5
D 17,56,0
M 30,a[36:39],50
s_cselect_b32 s94,0x3300,0
s_add_u32 s56,s55,256
R 78,2,11264
M 30,a[44:47],54
s_cmp_eq_u32 s79,127
v_lshlrev_b32 v108,16,v123
v_and_b32 v109,0xffff0000,v123
M 26,a[32:35],50
s_cselect_b32 s94,0xcc00,s94
R 82,2,12288
v_lshlrev_b32 v110,16,v124
M 26,a[40:43],54
L 5
D 17,56,1024
M 26,a[48:51],58
s_add_u32 s56,s55,512
R 86,2,13312
v_and_b32 v111,0xffff0000,v124
M 26,a[56:59],62
s_add_u32 s79,s54,s46
v_lshlrev_b32 v112,16,v125
v_and_b32 v113,0xffff0000,v125
M 30,a[52:55],58
s_and_b32 s79,s79,127
R 90,2,14336
v_fma_f32 v106,v106,v99,v98
M 30,a[60:63],62
L 5
D 17,56,2048
M 30,a[68:71],66
s_add_u32 s56,s55,768
R 94,2,15360
v_fma_f32 v107,v107,v99,v98
M 30,a[76:79],70
s_cmp_eq_u32 s79,0
v_fma_f32 v108,v108,v99,v98
v_fma_f32 v109,v109,v99,v98
M 26,a[64:67],66
s_cselect_b32 s95,0x3300,0
R 34,2,16384
v_fma_f32 v110,v110,v99,v98
M 26,a[72:75],70
L 5
D 17,56,3072
M 26,a[80:83],74
s_add_u32 m0,s57,4096
s_add_u32 s56,s55,5120
R 38,2,17408
M 26,a[88:91],78
s_cmp_eq_u32 s79,127
v_fma_f32 v111,v111,v99,v98
v_fma_f32 v112,v112,v99,v98
M 30,a[84:87],74
s_cselect_b32 s95,0xcc00,s95
R 42,2,18432
v_fma_f32 v113,v113,v99,v98
M 30,a[92:95],78
L 5
D 17,56,0
M 30,a[100:103],82
s_add_u32 s56,s55,5376
R 46,2,19456
v_add_f32 v18,v18,v106
M 30,a[108:111],86
s_add_u32 s57,s57,32768
v_add_f32 v19,v19,v107
v_add_f32 v20,v20,v108
M 26,a[96:99],82
s_mov_b32 s67,s65
R 50,2,20480
v_fma_f32 v107,v18,v18,s87
M 26,a[104:107],86
L 5
D 17,56,1024
M 26,a[112:115],90
s_add_u32 s56,s55,5632
R 54,2,21504
v_add_f32 v106,v18,v19
M 26,a[120:123],94
s_mov_b32 s77,s75
v_fma_f32 v107,v19,v19,v107
v_add_f32 v21,v21,v109
M 30,a[116:119],90
s_add_u32 s73,s73,s64
R 58,2,22528
v_add_f32 v106,v106,v20
M 30,a[124:127],94
L 5
D 17,56,2048
M 30,a[132:135],34
s_add_u32 s56,s55,5888
R 62,2,23552
v_fma_f32 v107,v20,v20,v107
M 30,a[140:143],38
s_add_u32 s74,s74,s62
v_add_f32 v22,v22,v110
v_add_f32 v106,v106,v21
M 26,a[128:131],34
s_add_u32 s55,s55,s60
R 66,2,24576
v_fma_f32 v107,v21,v21,v107
M 26,a[136:139],38
L 5
D 17,56,3072
M 26,a[144:147],42
s_and_b32 s57,s57,131071
R 70,2,25600
v_add_f32 v23,v23,v111
M 26,a[152:155],46
s_add_u32 s65,s65,s62
v_add_f32 v24,v24,v112
v_add_f32 v25,v25,v113
v_add_f32 v106,v106,v22
v_fma_f32 v107,v22,v22,v107
v_add_f32 v106,v106,v23
v_fma_f32 v107,v23,v23,v107
v_add_f32 v106,v106,v24
v_fma_f32 v107,v24,v24,v107
v_add_f32 v106,v106,v25
v_fma_f32 v107,v25,v25,v107
M 30,a[148:151],42
s_add_u32 s75,s75,s63
R 74,2,26624
v_cvt_pk_bf16_f32 v114,v18,v19
M 30,a[156:159],46
L 5
v_permlane16_swap_b32 v106,v107
v_add_f32 v106,v106,v107
v_mov_b32 v107,v106
M 30,a[164:167],50
R 78,2,27648
v_cvt_pk_bf16_f32 v115,v20,v21
M 30,a[172:175],54
v_permlane32_swap_b32 v106,v107
v_add_f32 v106,v106,v107
M 26,a[160:163],50
R 82,2,28672
ds_write_b32 v12,v106 offset:0
M 26,a[168:171],54
L 6
v_cvt_pk_bf16_f32 v116,v22,v23
v_cvt_pk_bf16_f32 v117,v24,v25
M 26,a[176:179],58
R 86,2,29696
M 26,a[184:187],62
buffer_store_dwordx4 v[114:117],v0,s[36:39],s66 offen offset:0 sc0 sc1
M 30,a[180:183],58
R 90,2,30720
M 30,a[188:191],62
L 6
L 2
M 30,a[196:199],66
s_waitcnt vmcnt(1)
R 94,2,31744
v_xor_b32 v2,65536,v2
M 30,a[204:207],70
s_barrier
buffer_store_dword v4,v3,s[36:39],s70 offen offset:0 sc0 sc1
M 26,a[192:195],66
s_mov_b32 s70,s69
R 34,1,0
R 118,13,0
M 26,a[200:203],70
s_nop 0
s_mov_b32 s69,s68
ds_read_b32 v7,v6 offset:1024
M 26,a[208:211],74
s_add_u32 s68,s68,s63
R 38,1,1024
M 26,a[216:219],78
M 30,a[212:215],74
R 42,1,2048
M 30,a[220:223],78
L 7
M 30,a[228:231],82
L 3
R 46,1,3072
v_add_f32 v118,v118,v119
M 30,a[236:239],86
L 3
v_add_f32 v120,v120,v121
v_add_f32 v118,v118,v120
M 26,a[224:227],82
R 50,1,4096
v_mul_f32 v118,s86,v118
M 26,a[232:235],86
s_nop 0
buffer_store_dword v118,v14,s[36:39],s76 offen offset:0 sc0 sc1
M 26,a[240:243],90
R 54,1,5120
v_cmp_eq_u32 vcc,s53,v7
M 26,a[248:251],94
s_or_b64 vcc,vcc,s[94:95]
s_cmp_eq_u64 vcc,-1
M 30,a[244:247],90
R 58,1,6144
M 30,a[252:255],94
L 5
s_cbranch_scc1 Lg2_L20_%=
s_cmp_eq_u32 vcc_lo,-1
s_cbranch_scc1 Lg2_L10_%=
s_branch Lg2_X0_%=
Lg2_done_%=:
s_waitcnt vmcnt(2)
s_barrier
buffer_store_dword v4,v3,s[36:39],s70 offen offset:0 sc0 sc1
s_waitcnt vmcnt(1)
s_barrier
buffer_store_dword v4,v3,s[36:39],s72 offen offset:0 sc0 sc1
s_waitcnt vmcnt(0) lgkmcnt(0)
s_barrier
Lg2_end_%=:
s_mov_b32 %0,s53
Lcode_g12_e:
.purgem M
.purgem R
.purgem T
.purgem D
.purgem L
)"
  : "=&s"(epOut) : "s"(ws), "s"(sWave), "s"(sG0), "s"(sStep), "s"(sOutOff), "s"(sFoutOff), "s"(sAuxOff), "s"(sWw), "s"(sBuf), "s"(sRk), "v"(vL16), "v"(vFlag), "v"(vSnapOff), "v"(vSnapRd), "v"(vWd0), "v"(vWd1), "v"(vWr0), "v"(vWr1), "v"(vWr2), "v"(vOutS), "v"(vAux0), "v"(vAux1), "v"(vAux2), "v"(vAux3), "v"(vAn), "v"(vAe), "s"(w12) : "v0", "v1", "v2", "v3", "v4", "v5", "v6", "v7", "v8", "v9", "v10", "v11", "v12", "v13", "v14", "v15", "v16", "v17", "v18", "v19", "v20", "v21", "v22", "v23", "v24", "v25", "v26", "v27", "v28", "v29", "v30", "v31", "v32", "v33", "v34", "v35", "v36", "v37", "v38", "v39", "v40", "v41", "v42", "v43", "v44", "v45", "v46", "v47", "v48", "v49", "v50", "v51", "v52", "v53", "v54", "v55", "v56", "v57", "v58", "v59", "v60", "v61", "v62", "v63", "v64", "v65", "v66", "v67", "v68", "v69", "v70", "v71", "v72", "v73", "v74", "v75", "v76", "v77", "v78", "v79", "v80", "v81", "v82", "v83", "v84", "v85", "v86", "v87", "v88", "v89", "v90", "v91", "v92", "v93", "v94", "v95", "v96", "v97", "v98", "v99", "v100", "v101", "v102", "v103", "v104", "v105", "v106", "v107", "v108", "v109", "v110", "v111", "v112", "v113", "v114", "v115", "v116", "v117", "v118", "v119", "v120", "v121", "v122", "v123", "v124", "v125", "v126", "v127", "v128", "v129", "v130", "v131", "v132", "v133", "v134", "v135", "v136", "v137", "v138", "v139", "v140", "v141", "v142", "v143", "v144", "v145", "v146", "v147", "v148", "v149", "v150", "v151", "v152", "v153", "v154", "v155", "v156", "v157", "v158", "v159", "v160", "v161", "v162", "v163", "v164", "v165", "v166", "v167", "v168", "v169", "v170", "v171", "v172", "v173", "v174", "v175", "v176", "v177", "v178", "v179", "v180", "v181", "v182", "v183", "v184", "v185", "v186", "v187", "v188", "v189", "v190", "v191", "v192", "v193", "v194", "v195", "v196", "v197", "v198", "v199", "v200", "v201", "v202", "v203", "v204", "v205", "v206", "v207", "v208", "v209", "v210", "v211", "v212", "v213", "v214", "v215", "v216", "v217", "v218", "v219", "v220", "v221", "v222", "v223", "v224", "v225", "v226", "v227", "v228", "v229", "v230", "v231", "a0", "a1", "a2", "a3", "a4", "a5", "a6", "a7", "a8", "a9", "a10", "a11", "a12", "a13", "a14", "a15", "a16", "a17", "a18", "a19", "a20", "a21", "a22", "a23", "a24", "a25", "a26", "a27", "a28", "a29", "a30", "a31", "a32", "a33", "a34", "a35", "a36", "a37", "a38", "a39", "a40", "a41", "a42", "a43", "a44", "a45", "a46", "a47", "a48", "a49", "a50", "a51", "a52", "a53", "a54", "a55", "a56", "a57", "a58", "a59", "a60", "a61", "a62", "a63", "a64", "a65", "a66", "a67", "a68", "a69", "a70", "a71", "a72", "a73", "a74", "a75", "a76", "a77", "a78", "a79", "a80", "a81", "a82", "a83", "a84", "a85", "a86", "a87", "a88", "a89", "a90", "a91", "a92", "a93", "a94", "a95", "a96", "a97", "a98", "a99", "a100", "a101", "a102", "a103", "a104", "a105", "a106", "a107", "a108", "a109", "a110", "a111", "a112", "a113", "a114", "a115", "a116", "a117", "a118", "a119", "a120", "a121", "a122", "a123", "a124", "a125", "a126", "a127", "a128", "a129", "a130", "a131", "a132", "a133", "a134", "a135", "a136", "a137", "a138", "a139", "a140", "a141", "a142", "a143", "a144", "a145", "a146", "a147", "a148", "a149", "a150", "a151", "a152", "a153", "a154", "a155", "a156", "a157", "a158", "a159", "a160", "a161", "a162", "a163", "a164", "a165", "a166", "a167", "a168", "a169", "a170", "a171", "a172", "a173", "a174", "a175", "a176", "a177", "a178", "a179", "a180", "a181", "a182", "a183", "a184", "a185", "a186", "a187", "a188", "a189", "a190", "a191", "a192", "a193", "a194", "a195", "a196", "a197", "a198", "a199", "a200", "a201", "a202", "a203", "a204", "a205", "a206", "a207", "a208", "a209", "a210", "a211", "a212", "a213", "a214", "a215", "a216", "a217", "a218", "a219", "a220", "a221", "a222", "a223", "a224", "a225", "a226", "a227", "a228", "a229", "a230", "a231", "a232", "a233", "a234", "a235", "a236", "a237", "a238", "a239", "a240", "a241", "a242", "a243", "a244", "a245", "a246", "a247", "a248", "a249", "a250", "a251", "a252", "a253", "a254", "a255", "s36", "s37", "s38", "s39", "s40", "s41", "s42", "s43", "s44", "s45", "s46", "s47", "s48", "s49", "s50", "s51", "s52", "s53", "s54", "s55", "s56", "s57", "s58", "s59", "s60", "s61", "s62", "s63", "s64", "s65", "s66", "s67", "s68", "s69", "s70", "s71", "s72", "s73", "s74", "s75", "s76", "s77", "s78", "s79", "s80", "s81", "s82", "s83", "s84", "s85", "s86", "s87", "s88", "s89", "s90", "s91", "s92", "s93", "s94", "s95", "s96", "s97", "vcc", "scc", "memory");
      ep = epOut;
    }
    LANE_ID(lane2);
    for (int t = kind == 6 ? 64 + slice : (kind - 1) * 32 + rep * 8 + slice; t < 256; t += 64 + NRW)
      row_last(ws, out, t, lane2, wave, ep);
    CTS(14);
    if (b == 1 && lane2 == 0 && wave == 0) __hip_atomic_store((gu32*)(ws + OFF_EP), ep, __ATOMIC_RELAXED, __HIP_MEMORY_SCOPE_AGENT);
    asm volatile("s_endpgm\nLcode_end:" ::: "memory");
    __builtin_unreachable();
  }
}

extern "C" void kernel_launch(void* const* d_in, const int* in_sizes, int n_in, void* d_out, int out_size,
                              void* d_ws, size_t ws_size, hipStream_t stream) {
  const u16* q = (const u16*)d_in[0];
  const u16* k = (const u16*)d_in[1];
  const u16* v = (const u16*)d_in[2];
  const u16* shift_k = (const u16*)d_in[3];
  const u16* ssm_k = (const u16*)d_in[4];
  const u16* ssm_D = (const u16*)d_in[5];
  const u16* w1 = (const u16*)d_in[7];
  const u16* w2 = (const u16*)d_in[8];
  const u16* w3 = (const u16*)d_in[9];
  const u16* w4 = (const u16*)d_in[10];
  flow_kernel<<<256, 256, 0, stream>>>(q, k, v, shift_k, ssm_k, ssm_D, w1, w2, w3, w4, (char*)d_ws, (u16*)d_out);
}
```

```cpp
#include <hip/hip_runtime.h>
#include <hip/hip_bf16.h>
#include <stdint.h>
#include <type_traits>

typedef short s16x8 __attribute__((ext_vector_type(8)));
typedef short s16x4 __attribute__((ext_vector_type(4)));
typedef float f32x4 __attribute__((ext_vector_type(4)));
typedef unsigned short u16;
typedef unsigned short u16x8 __attribute__((ext_vector_type(8)));
typedef unsigned short u16x4 __attribute__((ext_vector_type(4)));
typedef float f32x2 __attribute__((ext_vector_type(2)));
typedef __bf16 bf16x2 __attribute__((ext_vector_type(2)));
typedef unsigned u32x4v __attribute__((ext_vector_type(4)));
typedef __attribute__((address_space(1))) unsigned gu32;

constexpr int S_ = 2048, D_ = 1024, F_ = 3584;
#define PF 1

constexpr int WS_USED = 111243392;
constexpr int OFF_KVR = 4194304, OFF_XP = 8388608, OFF_HS = 17825792, OFF_X1 = 31457280, OFF_G = 40894464, OFF_P0 = 71303168, OFF_P1 = 89128960, OFF_Y = 98566144, OFF_S1 = 106954752, OFF_ST = 107479040, OFF_FL = 109051904, OFF_FXP = 109056000, OFF_FH = 109334528, OFF_FX1 = 109613056, OFF_FG = 109891584, OFF_FP = 110587904, OFF_FKV = 111144960, OFF_FY = 111177728, OFF_EP = 111243264, OFF_DBG = 115343360;

#define WS_DIAG 0

__device__ __forceinline__ float bf2f(u16 v) { return __uint_as_float(((unsigned)v) << 16); }
__device__ __forceinline__ u16 f2bf(float f) { return __builtin_bit_cast(u16, (__bf16)f); }
__device__ __forceinline__ float rbf(float f) { return bf2f(f2bf(f)); }
__device__ __forceinline__ void st16(void* p, u16x8 v) {
  asm volatile("global_store_dwordx4 %0, %1, off sc0 sc1\n\ts_nop 1" :: "v"(p), "v"(v) : "memory");
}
#define WAIT_VM(n) asm volatile("s_waitcnt vmcnt(%0)" ::"n"(n) : "memory")
#define WAIT_LGKM0() asm volatile("s_waitcnt lgkmcnt(0)" ::: "memory")

#define CODE_RANGE(B, E, p, n)                                                                                   \
  const char* p; unsigned n;                                                                                     \
  { unsigned lo_, hi_;                                                                                           \
    asm volatile("s_getpc_b64 s[98:99]\nLpc%=:\n\ts_add_u32 %0, s98, (" B "-Lpc%=)&4294967295\n\ts_addc_u32 %1, s99, (" B "-Lpc%=)>>32\n\t" \
                 "s_mov_b32 %2, " E "-" B : "=s"(lo_), "=s"(hi_), "=s"(n) :: "s98", "s99", "scc");               \
    p = (const char*)(((unsigned long long)hi_ << 32) | lo_); }
__device__ __forceinline__ unsigned touch(const char* p, unsigned n, unsigned off) {
  unsigned t;
  asm volatile("global_load_dword %0, %1, off" : "=v"(t) : "v"(p + (off < n ? off : 0u)) : "memory");
  return t;
}

__device__ __forceinline__ void touch_lds(const char* p, unsigned a) {
  asm volatile("s_mov_b32 m0, %1\n\ts_nop 0\n\tglobal_load_lds_dword %0, off" :: "v"(p), "s"(a) : "memory");
}
#define PFPOL ""
__device__ __forceinline__ void touch_pol(const char* p, unsigned a) {
  asm volatile("s_mov_b32 m0, %1\n\ts_nop 0\n\tglobal_load_lds_dword %0, off " PFPOL :: "v"(p), "s"(a) : "memory");
}
#define YSLEEP 4
#define KSLEEP 2
#define CTS(i)
__device__ __forceinline__ f32x2 up2(unsigned p) { return (f32x2){__uint_as_float(p << 16), __uint_as_float(p & 0xffff0000u)}; }
__device__ __forceinline__ unsigned pk2(f32x2 v) { return __builtin_bit_cast(unsigned, __builtin_convertvector(v, bf16x2)); }
__device__ __forceinline__ f32x2 rbf2(f32x2 v) { return up2(pk2(v)); }
template <int N, class F> __device__ __forceinline__ void sfor(F&& f) {
  if constexpr (N > 0) { sfor<N - 1>(f); f(std::integral_constant<int, N - 1>{}); }
}
__device__ __forceinline__ void set_flag(char* p, int i, unsigned ep) {
  if (i < 32) __hip_atomic_store((gu32*)p + i, ep, __ATOMIC_RELAXED, __HIP_MEMORY_SCOPE_AGENT);
}
template <int NL>
__device__ __forceinline__ void poll_flags(const char* p, int n, int lane, unsigned ep) {
  for (int it = 0; it < 20000; ++it) {
    bool ok = true;
#pragma unroll
    for (int i = 0; i < NL; ++i) {
      const int f = lane + 64 * i;
      ok &= __hip_atomic_load((gu32*)(p + (f < n ? f : 0) * 128), __ATOMIC_RELAXED, __HIP_MEMORY_SCOPE_AGENT) == ep;
    }
    if (__all(ok)) break;
    __builtin_amdgcn_s_sleep(YSLEEP);
  }
  asm volatile("" ::: "memory");
}

constexpr int CV_HP = (2048 + 32) * 2, CV_UR = (2048 + 256) * 2, CV_CH = 2 * CV_HP + CV_UR;
constexpr int NCV = 174;
constexpr int CV_STG = 8 * CV_CH;
constexpr int YP = D_ * 16;

struct KvRegs { u32x4v k0, k1, k2, k3, k4, k5, k6, v0, v1, v2, v3, s0, s1, s2, s3; };
#define KV_LOADS                                                                                                                       \
  "v_lshrrev_b32 %[a], 3, %[tid]\n\tv_and_b32 %[c], 7, %[tid]\n\ts_lshr_b32 s90, %[tile], 4\n\ts_and_b32 s91, %[tile], 15\n\t"          \
  "v_lshlrev_b32 %[a], 13, %[a]\n\tv_lshlrev_b32 %[c], 4, %[c]\n\ts_lshl_b32 s90, s90, 18\n\ts_lshl_b32 s91, s91, 7\n\t"                 \
  "v_add_u32 %[a], s90, %[a]\n\tv_add_u32 %[c], s91, %[c]\n\tv_subrev_u32 %[h], 0x1000, %[a]\n\tv_max_i32 %[h], 0x800, %[h]\n\t"         \
  "v_add3_u32 %[a], %[a], %[c], s92\n\tv_add_u32 %[h], %[h], %[c]\n\tv_lshlrev_b32 %[c], 2, %[c]\n\ts_waitcnt lgkmcnt(0)\n\t"            \
  "global_load_dwordx4 %3, %[a], s[76:77] offset:-4096 nt\n\tglobal_load_dwordx4 %4, %[a], s[76:77] offset:-2048 nt\n\t"                \
  "global_load_dwordx4 %5, %[a], s[76:77] nt\n\tglobal_load_dwordx4 %6, %[a], s[76:77] offset:2048 nt\n\t"                              \
  "global_load_dwordx4 %7, %[a], s[78:79] offset:-4096 nt\n\tglobal_load_dwordx4 %8, %[a], s[78:79] offset:-2048 nt\n\t"                \
  "global_load_dwordx4 %9, %[a], s[78:79] nt\n\tglobal_load_dwordx4 %10, %[a], s[78:79] offset:2048 nt\n\t"                             \
  "global_load_dwordx4 %0, %[h], s[76:77] offset:-2048 nt\n\tglobal_load_dwordx4 %1, %[h], s[76:77] nt\n\t"                             \
  "global_load_dwordx4 %2, %[h], s[76:77] offset:2048 nt\n\t"                                                                           \
  "global_load_dwordx4 %11, %[c], s[80:81]\n\tglobal_load_dwordx4 %12, %[c], s[80:81] offset:16\n\t"                                    \
  "global_load_dwordx4 %13, %[c], s[80:81] offset:32\n\tglobal_load_dwordx4 %14, %[c], s[80:81] offset:48\n\t"
#define KV_OUTS(R) "=&v"(R.k0), "=&v"(R.k1), "=&v"(R.k2), "=&v"(R.k3), "=&v"(R.k4), "=&v"(R.k5), "=&v"(R.k6), "=&v"(R.v0), "=&v"(R.v1), "=&v"(R.v2), \
                   "=&v"(R.v3), "=&v"(R.s0), "=&v"(R.s1), "=&v"(R.s2), "=&v"(R.s3), [a] "=&v"(ta), [h] "=&v"(th), [c] "=&v"(tc)
#define KV_CLOB "s74", "s75", "s76", "s77", "s78", "s79", "s80", "s81", "s82", "s83", "s90", "s91", "s92", "scc", "vcc", "memory"
template <int N, int N2>
__device__ __forceinline__ void kv_wait(KvRegs& R, int tile) {
  asm volatile("s_cmp_ge_u32 %17, 192\n\ts_cbranch_scc1 Lkw%=\n\ts_waitcnt vmcnt(%15)\nLkw%=:\n\ts_waitcnt vmcnt(%16)"
               : "+v"(R.k0), "+v"(R.k1), "+v"(R.k2), "+v"(R.k3), "+v"(R.k4), "+v"(R.k5), "+v"(R.k6), "+v"(R.v0), "+v"(R.v1), "+v"(R.v2),
                 "+v"(R.v3), "+v"(R.s0), "+v"(R.s1), "+v"(R.s2), "+v"(R.s3) : "n"(N), "n"(N2), "s"(tile) : "scc", "memory");
}
__device__ __forceinline__ void kv_store(KvRegs& R, char* __restrict__ ws, char* T, int b, int tid) {
  const int t0 = (b >> 4) * 128, c0 = (b & 15) * 64, ch8 = tid & 7, rg = tid >> 3;
  if (t0 + 4 * rg < 3) R.k0 = R.k1 = R.k2 = (u32x4v){0u, 0u, 0u, 0u};
  typedef unsigned u32x2 __attribute__((ext_vector_type(2)));
  const u32x4v kr[7] = {R.k0, R.k1, R.k2, R.k3, R.k4, R.k5, R.k6}, vr[4] = {R.v0, R.v1, R.v2, R.v3}, sk[4] = {R.s0, R.s1, R.s2, R.s3};
#pragma unroll
  for (int j2 = 0; j2 < 4; ++j2) {
    const f32x2 sa = up2(sk[j2][0]), sb = up2(sk[j2][1]), sc = up2(sk[j2][2]), sd = up2(sk[j2][3]);
    const f32x2 s0 = {sa[0], sc[0]}, s1 = {sa[1], sc[1]}, s2 = {sb[0], sd[0]}, s3 = {sb[1], sd[1]};
    f32x2 kf[7], o[4];
#pragma unroll
    for (int r = 0; r < 7; ++r) kf[r] = up2(kr[r][j2]);
#pragma unroll
    for (int r = 0; r < 4; ++r)
      o[r] = __builtin_elementwise_fma(kf[r + 3], s0, __builtin_elementwise_fma(kf[r + 2], s1, __builtin_elementwise_fma(kf[r + 1], s2, kf[r] * s3))) * up2(vr[r][j2]);
#pragma unroll
    for (int e = 0; e < 2; ++e)
      *(u32x2*)(T + (8 * ch8 + 2 * j2 + e) * 264 + (124 - 4 * rg) * 2) = (u32x2){pk2((f32x2){o[3][e], o[2][e]}), pk2((f32x2){o[1][e], o[0][e]})};
  }
  __syncthreads();
  u16* kvr = (u16*)(ws + OFF_KVR);
#pragma unroll
  for (int i = 0; i < 4; ++i) {
    const int idx = tid + 256 * i, ch = idx >> 4, cb = idx & 15;
    const u16x4 lo = *(const u16x4*)(T + ch * 264 + cb * 16), hi = *(const u16x4*)(T + ch * 264 + cb * 16 + 8);
    st16(kvr + (size_t)(c0 + ch) * S_ + (1920 - t0) + cb * 8, __builtin_shufflevector(lo, hi, 0, 1, 2, 3, 4, 5, 6, 7));
  }
}

#define LASTSLEEP 8
__device__ __forceinline__ void wait_flags(const char* p, int lane, int n, unsigned ep) {
  gu32* f = (gu32*)(p + (lane < n ? lane : 0) * 128);
  for (int it = 0; it < 10000; ++it) {
    if (__all(__hip_atomic_load(f, __ATOMIC_RELAXED, __HIP_MEMORY_SCOPE_AGENT) == ep)) break;
    __builtin_amdgcn_s_sleep(LASTSLEEP);
  }
  asm volatile("" ::: "memory");
}
__device__ __forceinline__ void row_last(char* __restrict__ ws, u16* __restrict__ out, int t, int lane, int wave, unsigned ep) {
  const __amdgpu_buffer_rsrc_t rs = __builtin_amdgcn_make_buffer_rsrc(ws, 0, WS_USED, 0x00020000);
  const int r8 = lane & 7, seg = lane >> 3, un = 128 + (t >> 1);
  const unsigned fo = un * 32768 + (wave * 8 + (seg >> 2)) * 1024 + ((seg & 3) * 16 + (t & 1) * 8 + r8) * 16;
  wait_flags(ws + OFF_FP + un * 2048, lane, 16, ep);
  u32x4v p0[4], p1[4];
#pragma unroll
  for (int i = 0; i < 4; ++i) {
    p0[i] = __builtin_bit_cast(u32x4v, __builtin_amdgcn_raw_buffer_load_b128(rs, fo + i * 2048, OFF_P0, 16));
    p1[i] = __builtin_bit_cast(u32x4v, __builtin_amdgcn_raw_buffer_load_b128(rs, fo + i * 2048, OFF_P1, 16));
  }
#pragma unroll
  for (int i = 0; i < 4; ++i) {
    u32x4v o;
#pragma unroll
    for (int e = 0; e < 4; ++e) o[e] = pk2(up2(p0[i][e]) + up2(p1[i][e]));
    st16(out + (size_t)(t * 8 + r8) * D_ + wave * 256 + i * 64 + seg * 8, __builtin_bit_cast(u16x8, o));
  }
}

__device__ __forceinline__ int swzB(int r) { return ((((r >> 1) & 1) | (((r >> 3) & 1) << 1)) << 1); }
#define TCH 1
#define LANE_ID(x) int x; asm volatile("v_mbcnt_lo_u32_b32 %0, -1, 0\n\tv_mbcnt_hi_u32_b32 %0, -1, %0" : "=v"(x))
#define T2X 5
#define W12 3
#define HL 0
#define QTB 1
constexpr int QT0 = 34 * 128;
#define CODE_MAX(x, y) "(((" x ">" y ")&" x ")|((" x "<=" y ")&" y "))"
constexpr int HU = 40960;
#define G3OR 5
constexpr int NRW = 22 - G3OR;
__global__ __launch_bounds__(256, 1) void flow_kernel(const u16* __restrict__ q, const u16* __restrict__ kin, const u16* __restrict__ vin,
                                                      const u16* __restrict__ shift_k, const u16* __restrict__ ssm_k, const u16* __restrict__ ssm_D,
                                                      const u16* __restrict__ w1, const u16* __restrict__ w2,
                                                      const u16* __restrict__ w3, const u16* __restrict__ w4, char* __restrict__ ws,
                                                      u16* __restrict__ out) {
  __shared__ __attribute__((aligned(16))) char lds[163840];
  KvRegs KR, KR2;
  unsigned ew, ta, th, tc;
  int b, tile;
  asm volatile("Lcode_b:\n\ts_load_dwordx4 s[76:79], %[ka], 0x8\n\ts_load_dwordx2 s[82:83], %[ka], 0x50\n\t"
               "s_getpc_b64 s[74:75]\n\ts_lshr_b32 s90, %[bi], 3\n\ts_mov_b32 m0, 0x27f00\n\tv_lshlrev_b32 %[a], 5, %[tid]\n\tv_add_lshl_u32 %[a], %[a], s90, 7\n\t"
               "v_min_u32 %[a], " CODE_MAX(CODE_MAX(CODE_MAX("(Lcode_g3_e-Lcode_b)", "(Lcode_g3o_e-Lcode_b)"), CODE_MAX("(Lcode_g4_e-Lcode_b)", "(Lcode_g12_e-Lcode_b)")), "(Lcode_end-Lcode_b)") "-64, %[a]\n\t"
               "global_load_lds_dword %[a], s[74:75]\n\ts_load_dwordx2 s[80:81], %[ka], 0x18\n\t"
               "s_mov_b32 %[b], %[bi]\n\ts_bfe_u32 s91, %[bi], 0x20001\n\ts_lshl2_add_u32 %[tile], s90, s91\n\ts_movk_i32 s92, 0x1000\n\t"
               "s_bitcmp1_b32 %[bi], 0\n\ts_cbranch_scc0 Lk1%=\n\ts_add_u32 %[tile], %[tile], 64\n\ts_cmp_lt_u32 s90, 8\n\ts_cbranch_scc1 Lk2%=\n\t"
               "s_lshl3_add_u32 s91, s91, s90\n\ts_add_u32 s91, s91, 184\n\ts_cmp_lt_u32 s90, 16\n\ts_cselect_b32 %[tile], s91, %[tile]\nLk1%=:\n\t"
               KV_LOADS "v_mov_b32 %[a], %[eo]\n\tglobal_load_dword %[ew], %[a], s[82:83] sc1\nLk2%=:\n\ts_waitcnt lgkmcnt(0)"
               : KV_OUTS(KR), [ew] "=&v"(ew), [b] "=&s"(b), [tile] "=&s"(tile)
               : [ka] "s"(__builtin_amdgcn_kernarg_segment_ptr()), [bi] "s"(blockIdx.x), [tid] "v"(threadIdx.x), [eo] "n"(OFF_EP) : KV_CLOB);
  const int tid = threadIdx.x, lane = tid & 63, wave = __builtin_amdgcn_readfirstlane(tid >> 6);
  const int xcd = b & 7, idx = b >> 3, tile2 = (tile + 32) ^ T2X;
  asm volatile("s_cmp_lt_u32 %[tile], 224\n\ts_cbranch_scc1 Lk3%=\n\ts_load_dwordx4 s[76:79], %[ka], 0x8\n\ts_load_dwordx2 s[80:81], %[ka], 0x18\n\ts_movk_i32 s92, 0x1000\n\t"
               KV_LOADS "\nLk3%=:"
               : KV_OUTS(KR2) : [ka] "s"(__builtin_amdgcn_kernarg_segment_ptr()), [tile] "s"(tile2), [tid] "v"(threadIdx.x) : KV_CLOB);
  const unsigned sLds = (unsigned)(size_t)lds;
  const int o8 = idx & 7;
  int kind, rep, slice, kh = 0;
  int j3 = 0, j4 = 0;
  const int xp = xcd >> 1, pr = xp & 1;
  if (!(xcd & 1)) {
    if (idx < (xp < 2 ? 24 : 16)) { kind = 4; rep = (xp < 2 ? 0 : 3) + (idx >> 3); kh = pr; slice = o8; j4 = rep * 16 + kh * 8 + o8; }
    else { kind = 3; rep = xp < 2 ? 4 : pr * 2 + ((idx >> 3) & 1); slice = xp < 2 ? pr * 8 + o8 : o8; j3 = rep * 18 + slice; }
  } else if (idx < 16) { kind = 1 + (idx >> 3); rep = xp; slice = o8; }
  else {
    const int k = idx - 16, n3 = xp < 2 ? 10 : 11, no = xp == 2 ? 2 : 1;
    if (k < n3) { kind = 3; rep = k < 10 ? pr * 2 + (k >= 5) : 4; slice = k < 10 ? (xp < 2 ? 8 : 13) + k - 5 * (k >= 5) : 16 + pr; j3 = rep * 18 + slice; }
    else if (k < n3 + no) { kind = 5; rep = k > n3 ? 4 : pr * 2 + (xp >> 1); slice = 18; j3 = 90 + rep; }
    else { kind = 6; rep = 0; slice = (xp < 3 ? xp * 5 : 13) + k - n3 - no; }
  }
  CTS(0);
  const unsigned sScr = sLds + 163584;
  unsigned ep = 0;
  auto w_touch = [&](const u16* w) {
#pragma unroll
    for (int i = 0; i < 2; ++i) touch_lds((const char*)w + (size_t)(rep * 256 + i * 128 + (tid >> 1)) * 2048 + slice * 256 + (tid & 1) * 128, sScr);
  };
  if (kind == 1) { if (W12 & 1) w_touch(w1); }
  else {
  const bool isconv = kind >= 3 && kind != 6 && j3 < 94;
  const int nw = (kind == 4 || j3 >= 92) ? 2 : 1, c0 = kind == 4 ? 8 * j4 : j3 < 92 ? 640 + 4 * j3 : j3 < 94 ? 272 + 8 * j3 : 0, cw = c0 + nw * wave;
  const unsigned hd = isconv ? sLds + 2 * wave * CV_CH + 32 : 0x30000u;
  auto h_dma = [&](bool first) {
#pragma unroll
    for (int j = 0; j < 2; ++j) {
      const u16* hp = ssm_k + (size_t)cw * S_ + (isconv && j < nw ? j * S_ + lane * 8 : 0);
      if (first) asm volatile("s_mov_b32 m0, %1\n\ts_nop 0\n\tglobal_load_lds_dwordx4 %0, off nt" :: "v"(hp), "s"(hd + j * CV_CH) : "memory");
      else asm volatile("s_mov_b32 m0, %1\n\ts_nop 0\n\tglobal_load_lds_dwordx4 %0, off offset:1024 nt\n\t"
                        "global_load_lds_dwordx4 %0, off offset:2048 nt\n\tglobal_load_lds_dwordx4 %0, off offset:3072 nt" :: "v"(hp), "s"(hd + j * CV_CH) : "memory");
    }
  };
  h_dma(true);
  if (HL == 0) h_dma(false);
  *(u32x4v*)(lds + (2 * wave + (lane >> 5)) * CV_CH + 2 * CV_HP + 4096 + (lane & 31) * 16) = (u32x4v){0u, 0u, 0u, 0u};
  if (lane < 8) *(u32x4v*)(lds + (2 * wave + (lane >> 2)) * CV_CH + ((lane >> 1) & 1) * CV_HP + (lane & 1) * 16) = (u32x4v){0u, 0u, 0u, 0u};
  kv_wait<HL ? 3 : 9, HL ? 18 : 24>(KR, tile);
  CTS(12);
  kv_store(KR, ws, lds + CV_STG, tile, tid);
  kv_wait<63, HL ? 6 : 12>(KR2, tile);
  if (kind == 2) kv_store(KR2, ws, lds + CV_STG + 17408, tile2, tid);
  CTS(1);
  if (HL == 1) h_dma(false);
  const unsigned dw = ((const unsigned*)ssm_D)[cw >> 1];
  asm volatile("s_waitcnt vmcnt(%1)" : "+v"(ew) : "n"(HL == 1 ? 6 : 0) : "memory");
  ep = __builtin_amdgcn_readfirstlane(ew) + 1u;
  if (ep == 0u) ep = 1u;
  __builtin_amdgcn_s_barrier();
  set_flag(ws + OFF_FKV + ((wave & 1) ? tile2 : tile) * 128, wave < (kind == 2 ? 2 : 1) ? lane : 64, ep);
  CTS(2);
  if (HL == 2) h_dma(false);
  if (kind == 2 && (W12 & 2) != 0) w_touch(w2);
  if (isconv) {
    LANE_ID(lane);
    const int tid = wave * 64 + lane;
    const unsigned sC0 = (cw & 1) ? dw & 0xffff0000u : dw << 16, sC1 = dw & 0xffff0000u;
    const int l15 = lane & 15, lg = lane >> 4, cp = (l15 + 1) & 1, iw = kind == 4 ? j4 : 80 + j3, own = 2 * wave * CV_CH;
    const int ch = tid & (4 * nw - 1), tb = nw == 2 ? tid >> 3 : tid >> 2, slot = nw == 2 ? ch : 2 * ch;
    const unsigned sWave = wave, sBuf = nw, sAuxOff = OFF_KVR + cw * 4096, sFoutOff = OFF_FY + iw * 128, sExp = __builtin_amdgcn_readfirstlane(ep), sDbg = 110u * 1048576u + (b * 4 + wave) * 8192;
    const unsigned vL16 = lane * 16, vPA = own + cp * CV_HP + 2 * (8 * lg + l15 + 1 - cp), vPB = own + 2 * CV_HP + 2 * (2032 - 16 * l15 + 8 * lg) - 512 * 7;
    const unsigned vPU = own + 2 * CV_HP + 2 * (2044 - 16 * l15 - 4 * lg) - 512 * 7, vPW = 32 * l15 + 8 * lg;
    const unsigned vOR = slot * CV_CH + tb * 16, vORA = CV_STG + slot * 1024 + tb * 16, vOG = tb * YP + (c0 + ch) * 16;
    const unsigned vFA = ((lane & 3) * 16 + (c0 >> 6)) * 128, vFB = ((4 + lane % 12) * 16 + (c0 >> 6)) * 128, vFlag = tid < 32 ? tid * 4 : 0x80000000u;
    const unsigned vTW = lane < 42 ? (iw * 168 + wave * 42 + lane) * 128 : 0x80000000u;
    const unsigned vTX = lane < 44 ? ((kind == 4 ? j4 : j3 - 12) * 176 + wave * 44 + lane) * 128 : 0x80000000u;
    (void)sDbg;
    asm volatile(
R"(.macro M d,w,x
v_mfma_f32_16x16x32_bf16 v[\d:\d+3], \w, v[\x:\x+3], v[\d:\d+3]
.endm
.macro R d,a,o
ds_read_b128 v[\d:\d+3], v\a offset:\o
.endm
.macro T d,a,o
ds_read_b64_tr_b16 \d, v\a offset:\o
.endm
.macro D a,s,o
buffer_load_dwordx4 v\a, s[36:39], s\s offen offset:\o sc1 lds
.endm
.macro L c
s_waitcnt lgkmcnt(\c)
.endm
Lcode_cv_b:
s_mov_b64 s[36:37],%0
s_mov_b32 s38,111243392
s_mov_b32 s39,0x00020000
s_mov_b32 s44,%1
s_mov_b32 s51,%2
s_mov_b32 s49,%3
s_mov_b32 s48,%4
s_mov_b32 s86,%5
s_mov_b32 s87,%6
s_mov_b32 s53,%7
v_mov_b32 v0,%8
v_mov_b32 v1,%9
v_mov_b32 v2,%10
v_mov_b32 v3,%11
v_mov_b32 v4,%12
v_mov_b32 v5,%13
v_mov_b32 v6,%14
v_mov_b32 v7,%15
v_mov_b32 v8,%16
v_mov_b32 v9,%17
v_mov_b32 v10,%18
v_mov_b32 v11,%19
v_mov_b32 v12,%20
s_mov_b32 s58,111144960
buffer_load_dword v13,v8,s[36:39],s58 offen offset:0 sc1
s_mul_i32 s57,s44,25856
v_add_u32 v18,s57,v0
v_add_u32 v18,32,v18
v_mov_b32 v19,s57
R 88,18,0
ds_read_b32 v92,v18 offset:16
R 152,18,12928
ds_read_b32 v156,v18 offset:12944
ds_read_b32 v24,v19 offset:32
ds_read_b32 v25,v19 offset:12960
s_mov_b64 s[40:41],%21
s_mov_b32 s42,7340032
s_mov_b32 s43,0x00020000
s_mov_b64 s[60:61],%22
s_mov_b32 s62,7340032
s_mov_b32 s63,0x00020000
v_mov_b32 v23,s53
s_mov_b32 s82,0
s_add_u32 s55,s49,4096
s_cmp_eq_u32 s51,2
s_cselect_b32 s55,s55,0x7fff0000
s_cselect_b32 s56,0,0x70000000
s_add_u32 s57,s57,8320
v_add_u32 v14,1024,v1
v_add_u32 v15,2048,v1
v_add_u32 v16,3072,v1
v_add_u32 v17,12928,v1
L 4
v_alignbyte_b32 v96,v89,v88,2
v_alignbyte_b32 v97,v90,v89,2
v_alignbyte_b32 v98,v91,v90,2
v_alignbyte_b32 v99,v92,v91,2
ds_write_b128 v18,v[96:99] offset:4160
L 3
v_alignbyte_b32 v160,v153,v152,2
v_alignbyte_b32 v161,v154,v153,2
v_alignbyte_b32 v162,v155,v154,2
v_alignbyte_b32 v163,v156,v155,2
ds_write_b128 v18,v[160:163] offset:17088
L 2
v_lshlrev_b32 v24,16,v24
v_lshlrev_b32 v25,16,v25
s_mov_b64 exec,1
ds_write_b32 v19,v24 offset:4188
ds_write_b32 v19,v25 offset:17116
s_mov_b64 exec,-1
ds_read2_b32 a[0:1],v1 offset0:0 offset1:1
ds_read2_b32 a[2:3],v1 offset0:2 offset1:3
ds_read2_b32 a[4:5],v1 offset0:128 offset1:129
ds_read2_b32 a[6:7],v1 offset0:130 offset1:131
ds_read2_b32 a[8:9],v1 offset0:16 offset1:17
ds_read2_b32 a[10:11],v1 offset0:18 offset1:19
ds_read2_b32 a[12:13],v1 offset0:144 offset1:145
ds_read2_b32 a[14:15],v1 offset0:146 offset1:147
ds_read2_b32 a[16:17],v1 offset0:32 offset1:33
ds_read2_b32 a[18:19],v1 offset0:34 offset1:35
ds_read2_b32 a[20:21],v1 offset0:160 offset1:161
ds_read2_b32 a[22:23],v1 offset0:162 offset1:163
ds_read2_b32 a[24:25],v1 offset0:48 offset1:49
ds_read2_b32 a[26:27],v1 offset0:50 offset1:51
ds_read2_b32 a[28:29],v1 offset0:176 offset1:177
ds_read2_b32 a[30:31],v1 offset0:178 offset1:179
ds_read2_b32 a[32:33],v1 offset0:64 offset1:65
ds_read2_b32 a[34:35],v1 offset0:66 offset1:67
ds_read2_b32 a[36:37],v1 offset0:192 offset1:193
ds_read2_b32 a[38:39],v1 offset0:194 offset1:195
ds_read2_b32 a[40:41],v1 offset0:80 offset1:81
ds_read2_b32 a[42:43],v1 offset0:82 offset1:83
ds_read2_b32 a[44:45],v1 offset0:208 offset1:209
ds_read2_b32 a[46:47],v1 offset0:210 offset1:211
ds_read2_b32 a[48:49],v1 offset0:96 offset1:97
ds_read2_b32 a[50:51],v1 offset0:98 offset1:99
ds_read2_b32 a[52:53],v1 offset0:224 offset1:225
ds_read2_b32 a[54:55],v1 offset0:226 offset1:227
ds_read2_b32 a[56:57],v1 offset0:112 offset1:113
ds_read2_b32 a[58:59],v1 offset0:114 offset1:115
ds_read2_b32 a[60:61],v1 offset0:240 offset1:241
ds_read2_b32 a[62:63],v1 offset0:242 offset1:243
ds_read2_b32 a[64:65],v17 offset0:0 offset1:1
ds_read2_b32 a[66:67],v17 offset0:2 offset1:3
ds_read2_b32 a[68:69],v17 offset0:128 offset1:129
ds_read2_b32 a[70:71],v17 offset0:130 offset1:131
ds_read2_b32 a[72:73],v17 offset0:16 offset1:17
ds_read2_b32 a[74:75],v17 offset0:18 offset1:19
ds_read2_b32 a[76:77],v17 offset0:144 offset1:145
ds_read2_b32 a[78:79],v17 offset0:146 offset1:147
ds_read2_b32 a[80:81],v17 offset0:32 offset1:33
ds_read2_b32 a[82:83],v17 offset0:34 offset1:35
ds_read2_b32 a[84:85],v17 offset0:160 offset1:161
ds_read2_b32 a[86:87],v17 offset0:162 offset1:163
ds_read2_b32 a[88:89],v17 offset0:48 offset1:49
ds_read2_b32 a[90:91],v17 offset0:50 offset1:51
ds_read2_b32 a[92:93],v17 offset0:176 offset1:177
ds_read2_b32 a[94:95],v17 offset0:178 offset1:179
ds_read2_b32 a[96:97],v17 offset0:64 offset1:65
ds_read2_b32 a[98:99],v17 offset0:66 offset1:67
ds_read2_b32 a[100:101],v17 offset0:192 offset1:193
ds_read2_b32 a[102:103],v17 offset0:194 offset1:195
ds_read2_b32 a[104:105],v17 offset0:80 offset1:81
ds_read2_b32 a[106:107],v17 offset0:82 offset1:83
ds_read2_b32 a[108:109],v17 offset0:208 offset1:209
ds_read2_b32 a[110:111],v17 offset0:210 offset1:211
ds_read2_b32 a[112:113],v17 offset0:96 offset1:97
ds_read2_b32 a[114:115],v17 offset0:98 offset1:99
ds_read2_b32 a[116:117],v17 offset0:224 offset1:225
ds_read2_b32 a[118:119],v17 offset0:226 offset1:227
ds_read2_b32 a[120:121],v17 offset0:112 offset1:113
ds_read2_b32 a[122:123],v17 offset0:114 offset1:115
ds_read2_b32 a[124:125],v17 offset0:240 offset1:241
ds_read2_b32 a[126:127],v17 offset0:242 offset1:243
Lcv_bpa_%=:
s_waitcnt vmcnt(0)
v_cmp_eq_u32 vcc,s53,v13
s_add_u32 s82,s82,1
s_cmp_eq_u64 vcc,-1
s_cbranch_scc1 Lcv_bqa_%=
s_sleep 1
buffer_load_dword v13,v8,s[36:39],s58 offen offset:0 sc1
s_cmp_lt_u32 s82,20000
s_cbranch_scc1 Lcv_bpa_%=
s_waitcnt vmcnt(0)
Lcv_bqa_%=:
s_add_u32 m0,s57,0
s_nop 0
D 0,49,3072
s_add_u32 m0,s57,12928
s_nop 0
D 0,55,3072
buffer_load_dword v13,v9,s[36:39],s58 offen offset:0 sc1
R 88,18,0
ds_read_b32 v92,v18 offset:16
R 104,18,1024
ds_read_b32 v108,v18 offset:1040
R 120,18,2048
ds_read_b32 v124,v18 offset:2064
R 136,18,3072
ds_read_b32 v140,v18 offset:3088
L 6
v_alignbyte_b32 v96,v89,v88,2
v_alignbyte_b32 v97,v90,v89,2
v_alignbyte_b32 v98,v91,v90,2
v_alignbyte_b32 v99,v92,v91,2
ds_write_b128 v18,v[96:99] offset:4160
L 5
v_alignbyte_b32 v112,v105,v104,2
v_alignbyte_b32 v113,v106,v105,2
v_alignbyte_b32 v114,v107,v106,2
v_alignbyte_b32 v115,v108,v107,2
ds_write_b128 v18,v[112:115] offset:5184
L 4
v_alignbyte_b32 v128,v121,v120,2
v_alignbyte_b32 v129,v122,v121,2
v_alignbyte_b32 v130,v123,v122,2
v_alignbyte_b32 v131,v124,v123,2
ds_write_b128 v18,v[128:131] offset:6208
L 3
v_alignbyte_b32 v144,v137,v136,2
v_alignbyte_b32 v145,v138,v137,2
v_alignbyte_b32 v146,v139,v138,2
v_alignbyte_b32 v147,v140,v139,2
ds_write_b128 v18,v[144:147] offset:7232
R 152,18,12928
ds_read_b32 v156,v18 offset:12944
R 168,18,13952
ds_read_b32 v172,v18 offset:13968
R 184,18,14976
ds_read_b32 v188,v18 offset:14992
R 200,18,16000
ds_read_b32 v204,v18 offset:16016
L 6
v_alignbyte_b32 v160,v153,v152,2
v_alignbyte_b32 v161,v154,v153,2
v_alignbyte_b32 v162,v155,v154,2
v_alignbyte_b32 v163,v156,v155,2
ds_write_b128 v18,v[160:163] offset:17088
L 5
v_alignbyte_b32 v176,v169,v168,2
v_alignbyte_b32 v177,v170,v169,2
v_alignbyte_b32 v178,v171,v170,2
v_alignbyte_b32 v179,v172,v171,2
ds_write_b128 v18,v[176:179] offset:18112
L 4
v_alignbyte_b32 v192,v185,v184,2
v_alignbyte_b32 v193,v186,v185,2
v_alignbyte_b32 v194,v187,v186,2
v_alignbyte_b32 v195,v188,v187,2
ds_write_b128 v18,v[192:195] offset:19136
L 3
v_alignbyte_b32 v208,v201,v200,2
v_alignbyte_b32 v209,v202,v201,2
v_alignbyte_b32 v210,v203,v202,2
v_alignbyte_b32 v211,v204,v203,2
ds_write_b128 v18,v[208:211] offset:20160
s_lshl_b32 s78,s44,11
s_add_u32 s78,s78,103424
v_add_u32 v18,s78,v4
v_add_u32 v4,v19,v4
s_waitcnt vmcnt(0)
v_cmp_eq_u32 vcc,s53,v13
s_mov_b32 s83,0
s_cmp_eq_u64 vcc,-1
s_cbranch_scc0 Lcv_a_%=
s_mov_b32 s83,1
s_add_u32 m0,s57,0
s_nop 0
D 0,49,2048
D 0,49,1024
D 0,49,0
s_add_u32 m0,s57,12928
s_nop 0
D 0,55,2048
D 0,55,1024
D 0,55,0
Lcv_a_%=:
s_cmp_eq_u32 s51,2
s_cbranch_scc1 Lcv_a2_%=
L 0
R 88,2,3584
R 92,2,3072
R 96,2,3648
R 100,2,3136
R 104,2,3712
R 108,2,3200
R 112,2,3776
R 116,2,3264
L 6
v_mfma_f32_16x16x32_bf16 v[40:43],a[0:3],v[88:91],0
R 120,2,3840
R 124,2,3328
v_mfma_f32_16x16x32_bf16 v[44:47],a[0:3],v[92:95],0
M 44,a[4:7],88
L 6
M 40,a[8:11],96
R 128,2,3904
R 132,2,3392
M 44,a[8:11],100
M 44,a[12:15],96
L 6
M 40,a[16:19],104
R 136,2,3968
R 140,2,3456
M 44,a[16:19],108
M 44,a[20:23],104
L 6
M 40,a[24:27],112
R 144,2,4032
R 148,2,3520
M 44,a[24:27],116
M 44,a[28:31],112
L 6
M 40,a[32:35],120
M 44,a[32:35],124
M 44,a[36:39],120
L 4
M 40,a[40:43],128
M 44,a[40:43],132
M 44,a[44:47],128
L 2
M 40,a[48:51],136
ds_read_b64 v[24:25],v3 offset:3584
ds_read_b64 v[26:27],v3 offset:3072
M 44,a[48:51],140
M 44,a[52:55],136
L 2
M 40,a[56:59],144
M 44,a[56:59],148
M 44,a[60:63],144
L 0
v_and_b32 v36,0xffff0000,v25
v_lshlrev_b32 v37,16,v25
v_and_b32 v38,0xffff0000,v24
v_lshlrev_b32 v39,16,v24
s_nop 0
v_fma_f32 v40,v36,s86,v40
v_fma_f32 v41,v37,s86,v41
v_fma_f32 v42,v38,s86,v42
v_fma_f32 v43,v39,s86,v43
v_cvt_pk_bf16_f32 v40,v40,v41
v_cvt_pk_bf16_f32 v41,v42,v43
ds_write_b64 v18,v[40:41] offset:0
v_and_b32 v36,0xffff0000,v27
v_lshlrev_b32 v37,16,v27
v_and_b32 v38,0xffff0000,v26
v_lshlrev_b32 v39,16,v26
v_fma_f32 v44,v36,s86,v44
v_fma_f32 v45,v37,s86,v45
v_fma_f32 v46,v38,s86,v46
v_fma_f32 v47,v39,s86,v47
v_cvt_pk_bf16_f32 v44,v44,v45
v_cvt_pk_bf16_f32 v45,v46,v47
ds_write_b64 v18,v[44:45] offset:512
s_branch Lcv_a3_%=
Lcv_a2_%=:
L 0
R 88,2,3584
R 92,2,3072
R 96,2,3648
R 100,2,3136
R 104,2,3712
R 108,2,3200
R 112,2,3776
R 116,2,3264
L 6
v_mfma_f32_16x16x32_bf16 v[40:43],a[0:3],v[88:91],0
R 120,2,3840
R 124,2,3328
v_mfma_f32_16x16x32_bf16 v[44:47],a[0:3],v[92:95],0
M 44,a[4:7],88
L 6
M 40,a[8:11],96
R 128,2,3904
R 132,2,3392
M 44,a[8:11],100
M 44,a[12:15],96
L 6
M 40,a[16:19],104
R 136,2,3968
R 140,2,3456
M 44,a[16:19],108
M 44,a[20:23],104
L 6
M 40,a[24:27],112
R 144,2,4032
R 148,2,3520
M 44,a[24:27],116
M 44,a[28:31],112
L 6
M 40,a[32:35],120
R 152,2,16512
R 156,2,16000
M 44,a[32:35],124
M 44,a[36:39],120
L 6
M 40,a[40:43],128
R 160,2,16576
R 164,2,16064
M 44,a[40:43],132
M 44,a[44:47],128
L 6
M 40,a[48:51],136
R 168,2,16640
R 172,2,16128
M 44,a[48:51],140
ds_read_b64 v[24:25],v3 offset:3584
ds_read_b64 v[26:27],v3 offset:3072
M 44,a[52:55],136
L 8
M 40,a[56:59],144
R 176,2,16704
R 180,2,16192
M 44,a[56:59],148
M 44,a[60:63],144
L 8
v_mfma_f32_16x16x32_bf16 v[48:51],a[64:67],v[152:155],0
R 184,2,16768
R 188,2,16256
v_mfma_f32_16x16x32_bf16 v[52:55],a[64:67],v[156:159],0
M 52,a[68:71],152
L 8
M 48,a[72:75],160
R 192,2,16832
R 196,2,16320
M 52,a[72:75],164
L 6
v_and_b32 v36,0xffff0000,v25
M 52,a[76:79],160
v_lshlrev_b32 v37,16,v25
v_and_b32 v38,0xffff0000,v24
v_lshlrev_b32 v39,16,v24
s_nop 0
M 48,a[80:83],168
R 200,2,16896
R 204,2,16384
M 52,a[80:83],172
v_fma_f32 v40,v36,s86,v40
v_fma_f32 v41,v37,s86,v41
M 52,a[84:87],168
v_fma_f32 v42,v38,s86,v42
v_fma_f32 v43,v39,s86,v43
v_cvt_pk_bf16_f32 v40,v40,v41
L 6
M 48,a[88:91],176
R 208,2,16960
R 212,2,16448
M 52,a[88:91],180
v_cvt_pk_bf16_f32 v41,v42,v43
ds_write_b64 v18,v[40:41] offset:0
M 52,a[92:95],176
v_and_b32 v36,0xffff0000,v27
v_lshlrev_b32 v37,16,v27
v_and_b32 v38,0xffff0000,v26
L 7
M 48,a[96:99],184
M 52,a[96:99],188
v_lshlrev_b32 v39,16,v26
v_fma_f32 v44,v36,s86,v44
M 52,a[100:103],184
v_fma_f32 v45,v37,s86,v45
v_fma_f32 v46,v38,s86,v46
v_fma_f32 v47,v39,s86,v47
L 5
M 48,a[104:107],192
M 52,a[104:107],196
v_cvt_pk_bf16_f32 v44,v44,v45
v_cvt_pk_bf16_f32 v45,v46,v47
M 52,a[108:111],192
ds_write_b64 v18,v[44:45] offset:512
L 4
M 48,a[112:115],200
ds_read_b64 v[24:25],v3 offset:16512
ds_read_b64 v[26:27],v3 offset:16000
M 52,a[112:115],204
M 52,a[116:119],200
L 4
M 48,a[120:123],208
M 52,a[120:123],212
M 52,a[124:127],208
L 0
v_and_b32 v36,0xffff0000,v25
v_lshlrev_b32 v37,16,v25
v_and_b32 v38,0xffff0000,v24
v_lshlrev_b32 v39,16,v24
s_nop 0
v_fma_f32 v48,v36,s87,v48
v_fma_f32 v49,v37,s87,v49
v_fma_f32 v50,v38,s87,v50
v_fma_f32 v51,v39,s87,v51
v_cvt_pk_bf16_f32 v48,v48,v49
v_cvt_pk_bf16_f32 v49,v50,v51
ds_write_b64 v18,v[48:49] offset:1024
v_and_b32 v36,0xffff0000,v27
v_lshlrev_b32 v37,16,v27
v_and_b32 v38,0xffff0000,v26
v_lshlrev_b32 v39,16,v26
v_fma_f32 v52,v36,s87,v52
v_fma_f32 v53,v37,s87,v53
v_fma_f32 v54,v38,s87,v54
v_fma_f32 v55,v39,s87,v55
v_cvt_pk_bf16_f32 v52,v52,v53
v_cvt_pk_bf16_f32 v53,v54,v55
ds_write_b64 v18,v[52:53] offset:1536
Lcv_a3_%=:
L 0
s_barrier
R 152,6,0
R 156,6,512
s_mov_b32 s78,98566144
L 1
buffer_store_dwordx4 v[152:155],v7,s[36:39],s78 offen offset:0 sc0 sc1
s_add_u32 s78,s56,99090432
L 0
buffer_store_dwordx4 v[156:159],v7,s[36:39],s78 offen offset:0 sc0 sc1
s_cmp_eq_u32 s83,1
s_cbranch_scc1 Lcv_b_%=
s_waitcnt vmcnt(0)
s_barrier
buffer_store_dword v23,v10,s[36:39],s48 offen offset:0 sc0 sc1
buffer_load_dword v13,v9,s[36:39],s58 offen offset:0 sc1
Lcv_bpb_%=:
)"
R"(s_waitcnt vmcnt(0)
v_cmp_eq_u32 vcc,s53,v13
s_add_u32 s82,s82,1
s_cmp_eq_u64 vcc,-1
s_cbranch_scc1 Lcv_bqb_%=
s_sleep 1
buffer_load_dword v13,v9,s[36:39],s58 offen offset:0 sc1
s_cmp_lt_u32 s82,20000
s_cbranch_scc1 Lcv_bpb_%=
s_waitcnt vmcnt(0)
Lcv_bqb_%=:
s_add_u32 m0,s57,0
s_nop 0
D 0,49,2048
D 0,49,1024
D 0,49,0
s_add_u32 m0,s57,12928
s_nop 0
D 0,55,2048
D 0,55,1024
D 0,55,0
s_waitcnt vmcnt(0)
Lcv_b_%=:
s_waitcnt vmcnt(2)
ds_read2_b32 v[88:89],v1 offset0:0 offset1:1
ds_read2_b32 v[90:91],v1 offset0:2 offset1:3
R 120,2,3584
ds_read2_b32 v[92:93],v1 offset0:128 offset1:129
ds_read2_b32 v[94:95],v1 offset0:130 offset1:131
R 124,2,3072
ds_read2_b32 v[96:97],v14 offset0:0 offset1:1
ds_read2_b32 v[98:99],v14 offset0:2 offset1:3
R 128,2,2560
ds_read2_b32 v[100:101],v14 offset0:128 offset1:129
ds_read2_b32 v[102:103],v14 offset0:130 offset1:131
R 132,2,2048
ds_read2_b32 v[104:105],v15 offset0:0 offset1:1
ds_read2_b32 v[106:107],v15 offset0:2 offset1:3
R 136,2,1536
ds_read2_b32 v[108:109],v15 offset0:128 offset1:129
ds_read2_b32 v[110:111],v15 offset0:130 offset1:131
R 140,2,1024
ds_read2_b32 v[112:113],v16 offset0:0 offset1:1
ds_read2_b32 v[114:115],v16 offset0:2 offset1:3
R 144,2,512
ds_read2_b32 v[116:117],v16 offset0:128 offset1:129
ds_read2_b32 v[118:119],v16 offset0:130 offset1:131
R 148,2,0
L 15
v_mfma_f32_16x16x32_bf16 v[40:43],v[88:91],v[128:131],0
ds_read2_b32 v[152:153],v1 offset0:16 offset1:17
M 40,v[92:95],124
ds_read2_b32 v[154:155],v1 offset0:18 offset1:19
M 40,v[96:99],120
L 14
R 184,2,3648
v_mfma_f32_16x16x32_bf16 v[44:47],v[88:91],v[132:135],0
ds_read2_b32 v[156:157],v1 offset0:144 offset1:145
M 44,v[92:95],128
M 44,v[96:99],124
ds_read2_b32 v[158:159],v1 offset0:146 offset1:147
M 44,v[100:103],120
L 14
R 188,2,3136
v_mfma_f32_16x16x32_bf16 v[48:51],v[88:91],v[136:139],0
ds_read2_b32 v[160:161],v14 offset0:16 offset1:17
M 48,v[92:95],132
ds_read2_b32 v[162:163],v14 offset0:18 offset1:19
M 48,v[96:99],128
M 48,v[100:103],124
R 192,2,2624
M 48,v[104:107],120
L 15
ds_read2_b32 v[164:165],v14 offset0:144 offset1:145
v_mfma_f32_16x16x32_bf16 v[52:55],v[88:91],v[140:143],0
ds_read2_b32 v[166:167],v14 offset0:146 offset1:147
M 52,v[92:95],136
R 196,2,2112
M 52,v[96:99],132
M 52,v[100:103],128
ds_read2_b32 v[168:169],v15 offset0:16 offset1:17
M 52,v[104:107],124
ds_read2_b32 v[170:171],v15 offset0:18 offset1:19
M 52,v[108:111],120
L 15
R 200,2,1600
v_mfma_f32_16x16x32_bf16 v[56:59],v[88:91],v[144:147],0
ds_read2_b32 v[172:173],v15 offset0:144 offset1:145
M 56,v[92:95],140
M 56,v[96:99],136
ds_read2_b32 v[174:175],v15 offset0:146 offset1:147
M 56,v[100:103],132
R 204,2,1088
M 56,v[104:107],128
ds_read2_b32 v[176:177],v16 offset0:16 offset1:17
M 56,v[108:111],124
ds_read2_b32 v[178:179],v16 offset0:18 offset1:19
M 56,v[112:115],120
L 15
v_mfma_f32_16x16x32_bf16 v[60:63],v[88:91],v[148:151],0
R 208,2,576
M 60,v[92:95],144
ds_read2_b32 v[180:181],v16 offset0:144 offset1:145
M 60,v[96:99],140
ds_read2_b32 v[182:183],v16 offset0:146 offset1:147
M 60,v[100:103],136
R 212,2,64
M 60,v[104:107],132
M 60,v[108:111],128
M 60,v[112:115],124
M 60,v[116:119],120
L 15
M 40,v[152:155],192
ds_read2_b32 v[88:89],v1 offset0:32 offset1:33
M 40,v[156:159],188
ds_read2_b32 v[90:91],v1 offset0:34 offset1:35
M 40,v[160:163],184
L 14
R 120,2,3712
M 44,v[152:155],196
ds_read2_b32 v[92:93],v1 offset0:160 offset1:161
M 44,v[156:159],192
M 44,v[160:163],188
ds_read2_b32 v[94:95],v1 offset0:162 offset1:163
M 44,v[164:167],184
L 14
R 124,2,3200
M 48,v[152:155],200
ds_read2_b32 v[96:97],v14 offset0:32 offset1:33
M 48,v[156:159],196
ds_read2_b32 v[98:99],v14 offset0:34 offset1:35
M 48,v[160:163],192
M 48,v[164:167],188
R 128,2,2688
M 48,v[168:171],184
L 15
ds_read2_b32 v[100:101],v14 offset0:160 offset1:161
M 52,v[152:155],204
ds_read2_b32 v[102:103],v14 offset0:162 offset1:163
M 52,v[156:159],200
R 132,2,2176
M 52,v[160:163],196
M 52,v[164:167],192
ds_read2_b32 v[104:105],v15 offset0:32 offset1:33
M 52,v[168:171],188
ds_read2_b32 v[106:107],v15 offset0:34 offset1:35
M 52,v[172:175],184
L 15
R 136,2,1664
M 56,v[152:155],208
ds_read2_b32 v[108:109],v15 offset0:160 offset1:161
M 56,v[156:159],204
M 56,v[160:163],200
ds_read2_b32 v[110:111],v15 offset0:162 offset1:163
M 56,v[164:167],196
R 140,2,1152
M 56,v[168:171],192
ds_read2_b32 v[112:113],v16 offset0:32 offset1:33
M 56,v[172:175],188
ds_read2_b32 v[114:115],v16 offset0:34 offset1:35
M 56,v[176:179],184
L 15
M 60,v[152:155],212
R 144,2,640
M 60,v[156:159],208
ds_read2_b32 v[116:117],v16 offset0:160 offset1:161
s_cmp_eq_u32 s83,0
s_cbranch_scc1 Lcv_c_%=
s_waitcnt vmcnt(0)
s_barrier
buffer_store_dword v23,v10,s[36:39],s48 offen offset:0 sc0 sc1
Lcv_c_%=:
M 60,v[160:163],204
ds_read2_b32 v[118:119],v16 offset0:162 offset1:163
M 60,v[164:167],200
R 148,2,128
M 60,v[168:171],196
M 60,v[172:175],192
M 60,v[176:179],188
M 60,v[180:183],184
L 15
M 40,v[88:91],128
ds_read2_b32 v[152:153],v1 offset0:48 offset1:49
M 40,v[92:95],124
ds_read2_b32 v[154:155],v1 offset0:50 offset1:51
M 40,v[96:99],120
L 14
R 184,2,3776
M 44,v[88:91],132
ds_read2_b32 v[156:157],v1 offset0:176 offset1:177
M 44,v[92:95],128
M 44,v[96:99],124
ds_read2_b32 v[158:159],v1 offset0:178 offset1:179
M 44,v[100:103],120
L 14
R 188,2,3264
M 48,v[88:91],136
ds_read2_b32 v[160:161],v14 offset0:48 offset1:49
M 48,v[92:95],132
ds_read2_b32 v[162:163],v14 offset0:50 offset1:51
M 48,v[96:99],128
M 48,v[100:103],124
R 192,2,2752
M 48,v[104:107],120
L 15
ds_read2_b32 v[164:165],v14 offset0:176 offset1:177
M 52,v[88:91],140
s_mov_b32 m0,163584
s_mov_b32 s78,0
ds_read2_b32 v[166:167],v14 offset0:178 offset1:179
M 52,v[92:95],136
R 196,2,2240
M 52,v[96:99],132
buffer_load_dword v11,s[40:43],s78 offen offset:0  lds
M 52,v[100:103],128
ds_read2_b32 v[168:169],v15 offset0:48 offset1:49
M 52,v[104:107],124
ds_read2_b32 v[170:171],v15 offset0:50 offset1:51
M 52,v[108:111],120
L 15
R 200,2,1728
M 56,v[88:91],144
ds_read2_b32 v[172:173],v15 offset0:176 offset1:177
M 56,v[92:95],140
M 56,v[96:99],136
ds_read2_b32 v[174:175],v15 offset0:178 offset1:179
M 56,v[100:103],132
R 204,2,1216
M 56,v[104:107],128
ds_read2_b32 v[176:177],v16 offset0:48 offset1:49
M 56,v[108:111],124
ds_read2_b32 v[178:179],v16 offset0:50 offset1:51
M 56,v[112:115],120
L 15
M 60,v[88:91],148
R 208,2,704
M 60,v[92:95],144
ds_read2_b32 v[180:181],v16 offset0:176 offset1:177
M 60,v[96:99],140
ds_read2_b32 v[182:183],v16 offset0:178 offset1:179
M 60,v[100:103],136
R 212,2,192
M 60,v[104:107],132
M 60,v[108:111],128
M 60,v[112:115],124
M 60,v[116:119],120
L 15
M 40,v[152:155],192
ds_read2_b32 v[88:89],v1 offset0:64 offset1:65
M 40,v[156:159],188
ds_read2_b32 v[90:91],v1 offset0:66 offset1:67
M 40,v[160:163],184
L 14
R 120,2,3840
M 44,v[152:155],196
ds_read2_b32 v[92:93],v1 offset0:192 offset1:193
M 44,v[156:159],192
M 44,v[160:163],188
ds_read2_b32 v[94:95],v1 offset0:194 offset1:195
M 44,v[164:167],184
L 14
R 124,2,3328
M 48,v[152:155],200
ds_read2_b32 v[96:97],v14 offset0:64 offset1:65
M 48,v[156:159],196
ds_read2_b32 v[98:99],v14 offset0:66 offset1:67
M 48,v[160:163],192
M 48,v[164:167],188
R 128,2,2816
M 48,v[168:171],184
L 15
ds_read2_b32 v[100:101],v14 offset0:192 offset1:193
M 52,v[152:155],204
ds_read2_b32 v[102:103],v14 offset0:194 offset1:195
M 52,v[156:159],200
R 132,2,2304
M 52,v[160:163],196
M 52,v[164:167],192
ds_read2_b32 v[104:105],v15 offset0:64 offset1:65
M 52,v[168:171],188
ds_read2_b32 v[106:107],v15 offset0:66 offset1:67
M 52,v[172:175],184
L 15
R 136,2,1792
M 56,v[152:155],208
ds_read2_b32 v[108:109],v15 offset0:192 offset1:193
M 56,v[156:159],204
M 56,v[160:163],200
ds_read2_b32 v[110:111],v15 offset0:194 offset1:195
M 56,v[164:167],196
R 140,2,1280
M 56,v[168:171],192
ds_read2_b32 v[112:113],v16 offset0:64 offset1:65
M 56,v[172:175],188
ds_read2_b32 v[114:115],v16 offset0:66 offset1:67
M 56,v[176:179],184
L 15
M 60,v[152:155],212
R 144,2,768
M 60,v[156:159],208
ds_read2_b32 v[116:117],v16 offset0:192 offset1:193
M 60,v[160:163],204
ds_read2_b32 v[118:119],v16 offset0:194 offset1:195
M 60,v[164:167],200
R 148,2,256
M 60,v[168:171],196
M 60,v[172:175],192
M 60,v[176:179],188
M 60,v[180:183],184
L 15
M 40,v[88:91],128
ds_read2_b32 v[152:153],v1 offset0:80 offset1:81
M 40,v[92:95],124
ds_read2_b32 v[154:155],v1 offset0:82 offset1:83
M 40,v[96:99],120
L 14
R 184,2,3904
M 44,v[88:91],132
ds_read2_b32 v[156:157],v1 offset0:208 offset1:209
M 44,v[92:95],128
M 44,v[96:99],124
ds_read2_b32 v[158:159],v1 offset0:210 offset1:211
M 44,v[100:103],120
L 14
R 188,2,3392
M 48,v[88:91],136
ds_read2_b32 v[160:161],v14 offset0:80 offset1:81
M 48,v[92:95],132
ds_read2_b32 v[162:163],v14 offset0:82 offset1:83
M 48,v[96:99],128
M 48,v[100:103],124
R 192,2,2880
M 48,v[104:107],120
L 15
ds_read2_b32 v[164:165],v14 offset0:208 offset1:209
M 52,v[88:91],140
ds_read2_b32 v[166:167],v14 offset0:210 offset1:211
M 52,v[92:95],136
R 196,2,2368
M 52,v[96:99],132
M 52,v[100:103],128
ds_read2_b32 v[168:169],v15 offset0:80 offset1:81
M 52,v[104:107],124
ds_read2_b32 v[170:171],v15 offset0:82 offset1:83
M 52,v[108:111],120
L 15
R 200,2,1856
M 56,v[88:91],144
ds_read2_b32 v[172:173],v15 offset0:208 offset1:209
M 56,v[92:95],140
M 56,v[96:99],136
ds_read2_b32 v[174:175],v15 offset0:210 offset1:211
M 56,v[100:103],132
R 204,2,1344
M 56,v[104:107],128
ds_read2_b32 v[176:177],v16 offset0:80 offset1:81
M 56,v[108:111],124
ds_read2_b32 v[178:179],v16 offset0:82 offset1:83
M 56,v[112:115],120
L 15
M 60,v[88:91],148
R 208,2,832
M 60,v[92:95],144
ds_read2_b32 v[180:181],v16 offset0:208 offset1:209
M 60,v[96:99],140
ds_read2_b32 v[182:183],v16 offset0:210 offset1:211
M 60,v[100:103],136
R 212,2,320
M 60,v[104:107],132
M 60,v[108:111],128
M 60,v[112:115],124
M 60,v[116:119],120
L 15
M 40,v[152:155],192
ds_read2_b32 v[88:89],v1 offset0:96 offset1:97
M 40,v[156:159],188
ds_read2_b32 v[90:91],v1 offset0:98 offset1:99
M 40,v[160:163],184
L 14
R 120,2,3968
M 44,v[152:155],196
s_mov_b32 m0,163584
s_mov_b32 s78,3741696
ds_read2_b32 v[92:93],v1 offset0:224 offset1:225
M 44,v[156:159],192
M 44,v[160:163],188
ds_read2_b32 v[94:95],v1 offset0:226 offset1:227
M 44,v[164:167],184
L 14
R 124,2,3456
M 48,v[152:155],200
ds_read2_b32 v[96:97],v14 offset0:96 offset1:97
M 48,v[156:159],196
ds_read2_b32 v[98:99],v14 offset0:98 offset1:99
M 48,v[160:163],192
buffer_load_dword v11,s[40:43],s78 offen offset:0  lds
M 48,v[164:167],188
R 128,2,2944
M 48,v[168:171],184
L 15
ds_read2_b32 v[100:101],v14 offset0:224 offset1:225
M 52,v[152:155],204
ds_read2_b32 v[102:103],v14 offset0:226 offset1:227
M 52,v[156:159],200
R 132,2,2432
M 52,v[160:163],196
M 52,v[164:167],192
ds_read2_b32 v[104:105],v15 offset0:96 offset1:97
M 52,v[168:171],188
ds_read2_b32 v[106:107],v15 offset0:98 offset1:99
M 52,v[172:175],184
L 15
R 136,2,1920
M 56,v[152:155],208
ds_read2_b32 v[108:109],v15 offset0:224 offset1:225
M 56,v[156:159],204
M 56,v[160:163],200
ds_read2_b32 v[110:111],v15 offset0:226 offset1:227
M 56,v[164:167],196
R 140,2,1408
M 56,v[168:171],192
ds_read2_b32 v[112:113],v16 offset0:96 offset1:97
M 56,v[172:175],188
ds_read2_b32 v[114:115],v16 offset0:98 offset1:99
M 56,v[176:179],184
L 15
M 60,v[152:155],212
R 144,2,896
M 60,v[156:159],208
ds_read2_b32 v[116:117],v16 offset0:224 offset1:225
M 60,v[160:163],204
ds_read2_b32 v[118:119],v16 offset0:226 offset1:227
M 60,v[164:167],200
R 148,2,384
M 60,v[168:171],196
M 60,v[172:175],192
M 60,v[176:179],188
M 60,v[180:183],184
L 15
M 40,v[88:91],128
ds_read2_b32 v[152:153],v1 offset0:112 offset1:113
M 40,v[92:95],124
ds_read2_b32 v[154:155],v1 offset0:114 offset1:115
M 40,v[96:99],120
L 14
R 184,2,4032
M 44,v[88:91],132
ds_read2_b32 v[156:157],v1 offset0:240 offset1:241
M 44,v[92:95],128
M 44,v[96:99],124
ds_read2_b32 v[158:159],v1 offset0:242 offset1:243
v_add_u32 v1,12928,v1
M 44,v[100:103],120
L 14
R 188,2,3520
M 48,v[88:91],136
ds_read2_b32 v[160:161],v14 offset0:112 offset1:113
M 48,v[92:95],132
ds_read2_b32 v[162:163],v14 offset0:114 offset1:115
M 48,v[96:99],128
M 48,v[100:103],124
R 192,2,3008
M 48,v[104:107],120
L 15
ds_read2_b32 v[164:165],v14 offset0:240 offset1:241
M 52,v[88:91],140
ds_read2_b32 v[166:167],v14 offset0:242 offset1:243
v_add_u32 v14,12928,v14
M 52,v[92:95],136
R 196,2,2496
M 52,v[96:99],132
M 52,v[100:103],128
ds_read2_b32 v[168:169],v15 offset0:112 offset1:113
M 52,v[104:107],124
ds_read2_b32 v[170:171],v15 offset0:114 offset1:115
M 52,v[108:111],120
L 15
R 200,2,1984
M 56,v[88:91],144
ds_read2_b32 v[172:173],v15 offset0:240 offset1:241
M 56,v[92:95],140
M 56,v[96:99],136
ds_read2_b32 v[174:175],v15 offset0:242 offset1:243
v_add_u32 v15,12928,v15
M 56,v[100:103],132
R 204,2,1472
M 56,v[104:107],128
ds_read2_b32 v[176:177],v16 offset0:112 offset1:113
M 56,v[108:111],124
ds_read2_b32 v[178:179],v16 offset0:114 offset1:115
M 56,v[112:115],120
L 15
M 60,v[88:91],148
R 208,2,960
M 60,v[92:95],144
ds_read2_b32 v[180:181],v16 offset0:240 offset1:241
M 60,v[96:99],140
ds_read2_b32 v[182:183],v16 offset0:242 offset1:243
v_add_u32 v16,12928,v16
M 60,v[100:103],136
)"
R"(R 212,2,448
M 60,v[104:107],132
M 60,v[108:111],128
M 60,v[112:115],124
M 60,v[116:119],120
L 15
M 40,v[152:155],192
ds_read2_b32 v[88:89],v1 offset0:0 offset1:1
M 40,v[156:159],188
ds_read2_b32 v[90:91],v1 offset0:2 offset1:3
M 40,v[160:163],184
L 14
R 120,2,16512
M 44,v[152:155],196
ds_read2_b32 v[92:93],v1 offset0:128 offset1:129
M 44,v[156:159],192
M 44,v[160:163],188
ds_read2_b32 v[94:95],v1 offset0:130 offset1:131
M 44,v[164:167],184
L 14
R 124,2,16000
M 48,v[152:155],200
ds_read2_b32 v[96:97],v14 offset0:0 offset1:1
M 48,v[156:159],196
ds_read2_b32 v[98:99],v14 offset0:2 offset1:3
M 48,v[160:163],192
M 48,v[164:167],188
R 128,2,15488
M 48,v[168:171],184
L 15
ds_read2_b32 v[100:101],v14 offset0:128 offset1:129
M 52,v[152:155],204
ds_read2_b32 v[102:103],v14 offset0:130 offset1:131
M 52,v[156:159],200
R 132,2,14976
M 52,v[160:163],196
M 52,v[164:167],192
ds_read2_b32 v[104:105],v15 offset0:0 offset1:1
M 52,v[168:171],188
ds_read2_b32 v[106:107],v15 offset0:2 offset1:3
M 52,v[172:175],184
L 15
R 136,2,14464
M 56,v[152:155],208
ds_read2_b32 v[108:109],v15 offset0:128 offset1:129
M 56,v[156:159],204
M 56,v[160:163],200
ds_read2_b32 v[110:111],v15 offset0:130 offset1:131
M 56,v[164:167],196
R 140,2,13952
M 56,v[168:171],192
ds_read2_b32 v[112:113],v16 offset0:0 offset1:1
M 56,v[172:175],188
ds_read2_b32 v[114:115],v16 offset0:2 offset1:3
M 56,v[176:179],184
L 15
M 60,v[152:155],212
R 144,2,13440
M 60,v[156:159],208
ds_read2_b32 v[116:117],v16 offset0:128 offset1:129
M 60,v[160:163],204
ds_read2_b32 v[118:119],v16 offset0:130 offset1:131
M 60,v[164:167],200
R 148,2,12928
M 60,v[168:171],196
M 60,v[172:175],192
M 60,v[176:179],188
M 60,v[180:183],184
s_cmp_eq_u32 s51,2
s_cbranch_scc1 Lcv_d_%=
ds_read_b64 v[24:25],v3 offset:2560
ds_read_b64 v[26:27],v3 offset:2048
ds_read_b64 v[28:29],v3 offset:1536
ds_read_b64 v[30:31],v3 offset:1024
ds_read_b64 v[32:33],v3 offset:512
ds_read_b64 v[34:35],v3 offset:0
L 0
v_and_b32 v36,0xffff0000,v25
v_lshlrev_b32 v37,16,v25
v_and_b32 v38,0xffff0000,v24
v_lshlrev_b32 v39,16,v24
v_fma_f32 v40,v36,s86,v40
v_fma_f32 v41,v37,s86,v41
v_fma_f32 v42,v38,s86,v42
v_fma_f32 v43,v39,s86,v43
v_cvt_pk_bf16_f32 v40,v40,v41
v_cvt_pk_bf16_f32 v41,v42,v43
ds_write_b64 v4,v[40:41] offset:0
v_and_b32 v36,0xffff0000,v27
v_lshlrev_b32 v37,16,v27
v_and_b32 v38,0xffff0000,v26
v_lshlrev_b32 v39,16,v26
v_fma_f32 v44,v36,s86,v44
v_fma_f32 v45,v37,s86,v45
v_fma_f32 v46,v38,s86,v46
v_fma_f32 v47,v39,s86,v47
v_cvt_pk_bf16_f32 v44,v44,v45
v_cvt_pk_bf16_f32 v45,v46,v47
ds_write_b64 v4,v[44:45] offset:512
v_and_b32 v36,0xffff0000,v29
v_lshlrev_b32 v37,16,v29
v_and_b32 v38,0xffff0000,v28
v_lshlrev_b32 v39,16,v28
v_fma_f32 v48,v36,s86,v48
v_fma_f32 v49,v37,s86,v49
v_fma_f32 v50,v38,s86,v50
v_fma_f32 v51,v39,s86,v51
v_cvt_pk_bf16_f32 v48,v48,v49
v_cvt_pk_bf16_f32 v49,v50,v51
ds_write_b64 v4,v[48:49] offset:1024
v_and_b32 v36,0xffff0000,v31
v_lshlrev_b32 v37,16,v31
v_and_b32 v38,0xffff0000,v30
v_lshlrev_b32 v39,16,v30
v_fma_f32 v52,v36,s86,v52
v_fma_f32 v53,v37,s86,v53
v_fma_f32 v54,v38,s86,v54
v_fma_f32 v55,v39,s86,v55
v_cvt_pk_bf16_f32 v52,v52,v53
v_cvt_pk_bf16_f32 v53,v54,v55
ds_write_b64 v4,v[52:53] offset:1536
v_and_b32 v36,0xffff0000,v33
v_lshlrev_b32 v37,16,v33
v_and_b32 v38,0xffff0000,v32
v_lshlrev_b32 v39,16,v32
v_fma_f32 v56,v36,s86,v56
v_fma_f32 v57,v37,s86,v57
v_fma_f32 v58,v38,s86,v58
v_fma_f32 v59,v39,s86,v59
v_cvt_pk_bf16_f32 v56,v56,v57
v_cvt_pk_bf16_f32 v57,v58,v59
ds_write_b64 v4,v[56:57] offset:2048
v_and_b32 v36,0xffff0000,v35
v_lshlrev_b32 v37,16,v35
v_and_b32 v38,0xffff0000,v34
v_lshlrev_b32 v39,16,v34
v_fma_f32 v60,v36,s86,v60
v_fma_f32 v61,v37,s86,v61
v_fma_f32 v62,v38,s86,v62
v_fma_f32 v63,v39,s86,v63
v_cvt_pk_bf16_f32 v60,v60,v61
v_cvt_pk_bf16_f32 v61,v62,v63
ds_write_b64 v4,v[60:61] offset:2560
s_branch Lcv_e_%=
Lcv_d_%=:
L 15
v_mfma_f32_16x16x32_bf16 v[64:67],v[88:91],v[128:131],0
ds_read2_b32 v[152:153],v1 offset0:16 offset1:17
ds_read_b64 v[24:25],v3 offset:2560
M 64,v[92:95],124
ds_read2_b32 v[154:155],v1 offset0:18 offset1:19
ds_read_b64 v[26:27],v3 offset:2048
M 64,v[96:99],120
L 15
R 184,2,16576
ds_read_b64 v[28:29],v3 offset:1536
v_mfma_f32_16x16x32_bf16 v[68:71],v[88:91],v[132:135],0
ds_read2_b32 v[156:157],v1 offset0:144 offset1:145
ds_read_b64 v[30:31],v3 offset:1024
M 68,v[92:95],128
ds_read_b64 v[32:33],v3 offset:512
ds_read_b64 v[34:35],v3 offset:0
M 68,v[96:99],124
ds_read2_b32 v[158:159],v1 offset0:146 offset1:147
M 68,v[100:103],120
L 15
R 188,2,16064
v_mfma_f32_16x16x32_bf16 v[72:75],v[88:91],v[136:139],0
ds_read2_b32 v[160:161],v14 offset0:16 offset1:17
M 72,v[92:95],132
s_mov_b32 m0,163584
s_mov_b32 s78,0
ds_read2_b32 v[162:163],v14 offset0:18 offset1:19
M 72,v[96:99],128
M 72,v[100:103],124
L 4
R 192,2,15552
v_and_b32 v36,0xffff0000,v25
M 72,v[104:107],120
s_nop 0
ds_read2_b32 v[164:165],v14 offset0:144 offset1:145
v_lshlrev_b32 v37,16,v25
v_mfma_f32_16x16x32_bf16 v[76:79],v[88:91],v[140:143],0
ds_read2_b32 v[166:167],v14 offset0:146 offset1:147
v_and_b32 v38,0xffff0000,v24
M 76,v[92:95],136
R 196,2,15040
v_lshlrev_b32 v39,16,v24
M 76,v[96:99],132
buffer_load_dword v12,s[60:63],s78 offen offset:0  lds
M 76,v[100:103],128
ds_read2_b32 v[168:169],v15 offset0:16 offset1:17
v_fma_f32 v40,v36,s86,v40
M 76,v[104:107],124
ds_read2_b32 v[170:171],v15 offset0:18 offset1:19
v_fma_f32 v41,v37,s86,v41
M 76,v[108:111],120
s_nop 0
R 200,2,14528
v_fma_f32 v42,v38,s86,v42
v_mfma_f32_16x16x32_bf16 v[80:83],v[88:91],v[144:147],0
ds_read2_b32 v[172:173],v15 offset0:144 offset1:145
v_fma_f32 v43,v39,s86,v43
M 80,v[92:95],140
v_cvt_pk_bf16_f32 v40,v40,v41
v_cvt_pk_bf16_f32 v41,v42,v43
M 80,v[96:99],136
ds_read2_b32 v[174:175],v15 offset0:146 offset1:147
v_and_b32 v36,0xffff0000,v27
M 80,v[100:103],132
R 204,2,14016
v_lshlrev_b32 v37,16,v27
M 80,v[104:107],128
ds_read2_b32 v[176:177],v16 offset0:16 offset1:17
v_and_b32 v38,0xffff0000,v26
M 80,v[108:111],124
ds_read2_b32 v[178:179],v16 offset0:18 offset1:19
v_lshlrev_b32 v39,16,v26
M 80,v[112:115],120
s_nop 0
ds_write_b64 v4,v[40:41] offset:0
v_mfma_f32_16x16x32_bf16 v[84:87],v[88:91],v[148:151],0
R 208,2,13504
v_fma_f32 v44,v36,s86,v44
M 84,v[92:95],144
ds_read2_b32 v[180:181],v16 offset0:144 offset1:145
v_fma_f32 v45,v37,s86,v45
M 84,v[96:99],140
ds_read2_b32 v[182:183],v16 offset0:146 offset1:147
v_fma_f32 v46,v38,s86,v46
M 84,v[100:103],136
R 212,2,12992
v_fma_f32 v47,v39,s86,v47
M 84,v[104:107],132
v_cvt_pk_bf16_f32 v44,v44,v45
v_cvt_pk_bf16_f32 v45,v46,v47
M 84,v[108:111],128
ds_write_b64 v4,v[44:45] offset:512
M 84,v[112:115],124
v_and_b32 v36,0xffff0000,v29
v_lshlrev_b32 v37,16,v29
M 84,v[116:119],120
L 15
v_and_b32 v38,0xffff0000,v28
v_lshlrev_b32 v39,16,v28
M 64,v[152:155],192
ds_read2_b32 v[88:89],v1 offset0:32 offset1:33
v_fma_f32 v48,v36,s86,v48
M 64,v[156:159],188
ds_read2_b32 v[90:91],v1 offset0:34 offset1:35
v_fma_f32 v49,v37,s86,v49
M 64,v[160:163],184
L 15
R 120,2,16640
v_fma_f32 v50,v38,s86,v50
M 68,v[152:155],196
ds_read2_b32 v[92:93],v1 offset0:160 offset1:161
v_fma_f32 v51,v39,s86,v51
M 68,v[156:159],192
v_cvt_pk_bf16_f32 v48,v48,v49
v_cvt_pk_bf16_f32 v49,v50,v51
M 68,v[160:163],188
ds_read2_b32 v[94:95],v1 offset0:162 offset1:163
v_and_b32 v36,0xffff0000,v31
M 68,v[164:167],184
L 15
R 124,2,16128
v_lshlrev_b32 v37,16,v31
M 72,v[152:155],200
ds_read2_b32 v[96:97],v14 offset0:32 offset1:33
v_and_b32 v38,0xffff0000,v30
M 72,v[156:159],196
ds_read2_b32 v[98:99],v14 offset0:34 offset1:35
v_lshlrev_b32 v39,16,v30
M 72,v[160:163],192
ds_write_b64 v4,v[48:49] offset:1024
M 72,v[164:167],188
R 128,2,15616
v_fma_f32 v52,v36,s86,v52
M 72,v[168:171],184
L 15
ds_read2_b32 v[100:101],v14 offset0:160 offset1:161
v_fma_f32 v53,v37,s86,v53
M 76,v[152:155],204
ds_read2_b32 v[102:103],v14 offset0:162 offset1:163
v_fma_f32 v54,v38,s86,v54
M 76,v[156:159],200
R 132,2,15104
v_fma_f32 v55,v39,s86,v55
M 76,v[160:163],196
v_cvt_pk_bf16_f32 v52,v52,v53
v_cvt_pk_bf16_f32 v53,v54,v55
M 76,v[164:167],192
ds_read2_b32 v[104:105],v15 offset0:32 offset1:33
v_and_b32 v36,0xffff0000,v33
M 76,v[168:171],188
ds_read2_b32 v[106:107],v15 offset0:34 offset1:35
v_lshlrev_b32 v37,16,v33
M 76,v[172:175],184
L 15
R 136,2,14592
v_and_b32 v38,0xffff0000,v32
M 80,v[152:155],208
ds_read2_b32 v[108:109],v15 offset0:160 offset1:161
v_lshlrev_b32 v39,16,v32
M 80,v[156:159],204
ds_write_b64 v4,v[52:53] offset:1536
M 80,v[160:163],200
ds_read2_b32 v[110:111],v15 offset0:162 offset1:163
v_fma_f32 v56,v36,s86,v56
M 80,v[164:167],196
R 140,2,14080
v_fma_f32 v57,v37,s86,v57
M 80,v[168:171],192
ds_read2_b32 v[112:113],v16 offset0:32 offset1:33
v_fma_f32 v58,v38,s86,v58
M 80,v[172:175],188
ds_read2_b32 v[114:115],v16 offset0:34 offset1:35
v_fma_f32 v59,v39,s86,v59
M 80,v[176:179],184
L 15
v_cvt_pk_bf16_f32 v56,v56,v57
v_cvt_pk_bf16_f32 v57,v58,v59
M 84,v[152:155],212
R 144,2,13568
v_and_b32 v36,0xffff0000,v35
M 84,v[156:159],208
ds_read2_b32 v[116:117],v16 offset0:160 offset1:161
v_lshlrev_b32 v37,16,v35
M 84,v[160:163],204
ds_read2_b32 v[118:119],v16 offset0:162 offset1:163
v_and_b32 v38,0xffff0000,v34
M 84,v[164:167],200
R 148,2,13056
v_lshlrev_b32 v39,16,v34
M 84,v[168:171],196
ds_write_b64 v4,v[56:57] offset:2048
M 84,v[172:175],192
v_fma_f32 v60,v36,s86,v60
v_fma_f32 v61,v37,s86,v61
M 84,v[176:179],188
v_fma_f32 v62,v38,s86,v62
v_fma_f32 v63,v39,s86,v63
M 84,v[180:183],184
L 15
v_cvt_pk_bf16_f32 v60,v60,v61
v_cvt_pk_bf16_f32 v61,v62,v63
M 64,v[88:91],128
ds_read2_b32 v[152:153],v1 offset0:48 offset1:49
M 64,v[92:95],124
ds_read2_b32 v[154:155],v1 offset0:50 offset1:51
M 64,v[96:99],120
L 15
R 184,2,16704
M 68,v[88:91],132
ds_read2_b32 v[156:157],v1 offset0:176 offset1:177
M 68,v[92:95],128
ds_write_b64 v4,v[60:61] offset:2560
M 68,v[96:99],124
ds_read2_b32 v[158:159],v1 offset0:178 offset1:179
M 68,v[100:103],120
L 15
R 188,2,16192
M 72,v[88:91],136
ds_read2_b32 v[160:161],v14 offset0:48 offset1:49
M 72,v[92:95],132
ds_read2_b32 v[162:163],v14 offset0:50 offset1:51
M 72,v[96:99],128
M 72,v[100:103],124
R 192,2,15680
M 72,v[104:107],120
L 15
ds_read2_b32 v[164:165],v14 offset0:176 offset1:177
M 76,v[88:91],140
ds_read2_b32 v[166:167],v14 offset0:178 offset1:179
M 76,v[92:95],136
R 196,2,15168
M 76,v[96:99],132
M 76,v[100:103],128
ds_read2_b32 v[168:169],v15 offset0:48 offset1:49
M 76,v[104:107],124
ds_read2_b32 v[170:171],v15 offset0:50 offset1:51
M 76,v[108:111],120
L 15
R 200,2,14656
M 80,v[88:91],144
ds_read2_b32 v[172:173],v15 offset0:176 offset1:177
M 80,v[92:95],140
M 80,v[96:99],136
ds_read2_b32 v[174:175],v15 offset0:178 offset1:179
M 80,v[100:103],132
R 204,2,14144
M 80,v[104:107],128
s_mov_b32 m0,163584
s_mov_b32 s78,1847296
ds_read2_b32 v[176:177],v16 offset0:48 offset1:49
M 80,v[108:111],124
ds_read2_b32 v[178:179],v16 offset0:50 offset1:51
M 80,v[112:115],120
L 15
buffer_load_dword v12,s[60:63],s78 offen offset:0  lds
M 84,v[88:91],148
R 208,2,13632
M 84,v[92:95],144
ds_read2_b32 v[180:181],v16 offset0:176 offset1:177
M 84,v[96:99],140
ds_read2_b32 v[182:183],v16 offset0:178 offset1:179
M 84,v[100:103],136
R 212,2,13120
M 84,v[104:107],132
M 84,v[108:111],128
M 84,v[112:115],124
M 84,v[116:119],120
L 15
M 64,v[152:155],192
ds_read2_b32 v[88:89],v1 offset0:64 offset1:65
M 64,v[156:159],188
ds_read2_b32 v[90:91],v1 offset0:66 offset1:67
M 64,v[160:163],184
L 14
R 120,2,16768
M 68,v[152:155],196
ds_read2_b32 v[92:93],v1 offset0:192 offset1:193
M 68,v[156:159],192
M 68,v[160:163],188
ds_read2_b32 v[94:95],v1 offset0:194 offset1:195
M 68,v[164:167],184
L 14
R 124,2,16256
M 72,v[152:155],200
ds_read2_b32 v[96:97],v14 offset0:64 offset1:65
M 72,v[156:159],196
ds_read2_b32 v[98:99],v14 offset0:66 offset1:67
M 72,v[160:163],192
M 72,v[164:167],188
R 128,2,15744
M 72,v[168:171],184
L 15
ds_read2_b32 v[100:101],v14 offset0:192 offset1:193
M 76,v[152:155],204
ds_read2_b32 v[102:103],v14 offset0:194 offset1:195
M 76,v[156:159],200
R 132,2,15232
M 76,v[160:163],196
M 76,v[164:167],192
ds_read2_b32 v[104:105],v15 offset0:64 offset1:65
M 76,v[168:171],188
ds_read2_b32 v[106:107],v15 offset0:66 offset1:67
M 76,v[172:175],184
L 15
R 136,2,14720
M 80,v[152:155],208
ds_read2_b32 v[108:109],v15 offset0:192 offset1:193
M 80,v[156:159],204
M 80,v[160:163],200
ds_read2_b32 v[110:111],v15 offset0:194 offset1:195
M 80,v[164:167],196
R 140,2,14208
M 80,v[168:171],192
ds_read2_b32 v[112:113],v16 offset0:64 offset1:65
M 80,v[172:175],188
ds_read2_b32 v[114:115],v16 offset0:66 offset1:67
M 80,v[176:179],184
L 15
M 84,v[152:155],212
R 144,2,13696
M 84,v[156:159],208
ds_read2_b32 v[116:117],v16 offset0:192 offset1:193
M 84,v[160:163],204
ds_read2_b32 v[118:119],v16 offset0:194 offset1:195
M 84,v[164:167],200
R 148,2,13184
M 84,v[168:171],196
M 84,v[172:175],192
M 84,v[176:179],188
M 84,v[180:183],184
L 15
M 64,v[88:91],128
ds_read2_b32 v[152:153],v1 offset0:80 offset1:81
M 64,v[92:95],124
ds_read2_b32 v[154:155],v1 offset0:82 offset1:83
M 64,v[96:99],120
L 14
R 184,2,16832
M 68,v[88:91],132
ds_read2_b32 v[156:157],v1 offset0:208 offset1:209
M 68,v[92:95],128
M 68,v[96:99],124
ds_read2_b32 v[158:159],v1 offset0:210 offset1:211
M 68,v[100:103],120
L 14
R 188,2,16320
M 72,v[88:91],136
ds_read2_b32 v[160:161],v14 offset0:80 offset1:81
M 72,v[92:95],132
ds_read2_b32 v[162:163],v14 offset0:82 offset1:83
M 72,v[96:99],128
M 72,v[100:103],124
R 192,2,15808
M 72,v[104:107],120
)"
R"(L 15
ds_read2_b32 v[164:165],v14 offset0:208 offset1:209
M 76,v[88:91],140
ds_read2_b32 v[166:167],v14 offset0:210 offset1:211
M 76,v[92:95],136
R 196,2,15296
M 76,v[96:99],132
M 76,v[100:103],128
ds_read2_b32 v[168:169],v15 offset0:80 offset1:81
M 76,v[104:107],124
ds_read2_b32 v[170:171],v15 offset0:82 offset1:83
M 76,v[108:111],120
L 15
R 200,2,14784
M 80,v[88:91],144
ds_read2_b32 v[172:173],v15 offset0:208 offset1:209
M 80,v[92:95],140
M 80,v[96:99],136
ds_read2_b32 v[174:175],v15 offset0:210 offset1:211
M 80,v[100:103],132
R 204,2,14272
M 80,v[104:107],128
ds_read2_b32 v[176:177],v16 offset0:80 offset1:81
M 80,v[108:111],124
ds_read2_b32 v[178:179],v16 offset0:82 offset1:83
M 80,v[112:115],120
L 15
M 84,v[88:91],148
R 208,2,13760
M 84,v[92:95],144
ds_read2_b32 v[180:181],v16 offset0:208 offset1:209
M 84,v[96:99],140
ds_read2_b32 v[182:183],v16 offset0:210 offset1:211
M 84,v[100:103],136
R 212,2,13248
M 84,v[104:107],132
M 84,v[108:111],128
M 84,v[112:115],124
M 84,v[116:119],120
L 15
M 64,v[152:155],192
ds_read2_b32 v[88:89],v1 offset0:96 offset1:97
M 64,v[156:159],188
ds_read2_b32 v[90:91],v1 offset0:98 offset1:99
M 64,v[160:163],184
L 14
R 120,2,16896
M 68,v[152:155],196
ds_read2_b32 v[92:93],v1 offset0:224 offset1:225
M 68,v[156:159],192
M 68,v[160:163],188
ds_read2_b32 v[94:95],v1 offset0:226 offset1:227
M 68,v[164:167],184
L 14
R 124,2,16384
M 72,v[152:155],200
ds_read2_b32 v[96:97],v14 offset0:96 offset1:97
M 72,v[156:159],196
ds_read2_b32 v[98:99],v14 offset0:98 offset1:99
M 72,v[160:163],192
M 72,v[164:167],188
R 128,2,15872
M 72,v[168:171],184
L 15
ds_read2_b32 v[100:101],v14 offset0:224 offset1:225
M 76,v[152:155],204
ds_read2_b32 v[102:103],v14 offset0:226 offset1:227
M 76,v[156:159],200
R 132,2,15360
M 76,v[160:163],196
M 76,v[164:167],192
ds_read2_b32 v[104:105],v15 offset0:96 offset1:97
M 76,v[168:171],188
ds_read2_b32 v[106:107],v15 offset0:98 offset1:99
M 76,v[172:175],184
L 15
R 136,2,14848
M 80,v[152:155],208
ds_read2_b32 v[108:109],v15 offset0:224 offset1:225
M 80,v[156:159],204
M 80,v[160:163],200
ds_read2_b32 v[110:111],v15 offset0:226 offset1:227
M 80,v[164:167],196
R 140,2,14336
M 80,v[168:171],192
ds_read2_b32 v[112:113],v16 offset0:96 offset1:97
M 80,v[172:175],188
ds_read2_b32 v[114:115],v16 offset0:98 offset1:99
M 80,v[176:179],184
L 15
M 84,v[152:155],212
R 144,2,13824
M 84,v[156:159],208
ds_read2_b32 v[116:117],v16 offset0:224 offset1:225
M 84,v[160:163],204
ds_read2_b32 v[118:119],v16 offset0:226 offset1:227
M 84,v[164:167],200
R 148,2,13312
M 84,v[168:171],196
M 84,v[172:175],192
M 84,v[176:179],188
M 84,v[180:183],184
L 15
M 64,v[88:91],128
ds_read2_b32 v[152:153],v1 offset0:112 offset1:113
M 64,v[92:95],124
ds_read2_b32 v[154:155],v1 offset0:114 offset1:115
M 64,v[96:99],120
L 14
R 184,2,16960
M 68,v[88:91],132
ds_read2_b32 v[156:157],v1 offset0:240 offset1:241
M 68,v[92:95],128
M 68,v[96:99],124
ds_read2_b32 v[158:159],v1 offset0:242 offset1:243
M 68,v[100:103],120
L 14
R 188,2,16448
M 72,v[88:91],136
ds_read2_b32 v[160:161],v14 offset0:112 offset1:113
M 72,v[92:95],132
ds_read2_b32 v[162:163],v14 offset0:114 offset1:115
M 72,v[96:99],128
M 72,v[100:103],124
R 192,2,15936
M 72,v[104:107],120
L 15
ds_read2_b32 v[164:165],v14 offset0:240 offset1:241
M 76,v[88:91],140
ds_read2_b32 v[166:167],v14 offset0:242 offset1:243
M 76,v[92:95],136
R 196,2,15424
M 76,v[96:99],132
M 76,v[100:103],128
ds_read2_b32 v[168:169],v15 offset0:112 offset1:113
M 76,v[104:107],124
ds_read2_b32 v[170:171],v15 offset0:114 offset1:115
M 76,v[108:111],120
L 15
R 200,2,14912
M 80,v[88:91],144
ds_read2_b32 v[172:173],v15 offset0:240 offset1:241
M 80,v[92:95],140
M 80,v[96:99],136
ds_read2_b32 v[174:175],v15 offset0:242 offset1:243
M 80,v[100:103],132
R 204,2,14400
M 80,v[104:107],128
ds_read2_b32 v[176:177],v16 offset0:112 offset1:113
M 80,v[108:111],124
ds_read2_b32 v[178:179],v16 offset0:114 offset1:115
M 80,v[112:115],120
L 15
M 84,v[88:91],148
R 208,2,13888
M 84,v[92:95],144
ds_read2_b32 v[180:181],v16 offset0:240 offset1:241
M 84,v[96:99],140
ds_read2_b32 v[182:183],v16 offset0:242 offset1:243
M 84,v[100:103],136
R 212,2,13376
M 84,v[104:107],132
M 84,v[108:111],128
M 84,v[112:115],124
M 84,v[116:119],120
L 15
M 64,v[152:155],192
M 64,v[156:159],188
M 64,v[160:163],184
L 12
M 68,v[152:155],196
M 68,v[156:159],192
M 68,v[160:163],188
M 68,v[164:167],184
L 9
M 72,v[152:155],200
M 72,v[156:159],196
M 72,v[160:163],192
ds_read_b64 v[24:25],v3 offset:15488
ds_read_b64 v[26:27],v3 offset:14976
M 72,v[164:167],188
ds_read_b64 v[28:29],v3 offset:14464
ds_read_b64 v[30:31],v3 offset:13952
M 72,v[168:171],184
L 10
ds_read_b64 v[32:33],v3 offset:13440
ds_read_b64 v[34:35],v3 offset:12928
M 76,v[152:155],204
M 76,v[156:159],200
M 76,v[160:163],196
M 76,v[164:167],192
M 76,v[168:171],188
M 76,v[172:175],184
L 9
M 80,v[152:155],208
M 80,v[156:159],204
M 80,v[160:163],200
M 80,v[164:167],196
M 80,v[168:171],192
M 80,v[172:175],188
M 80,v[176:179],184
L 6
M 84,v[152:155],212
M 84,v[156:159],208
M 84,v[160:163],204
M 84,v[164:167],200
M 84,v[168:171],196
M 84,v[172:175],192
M 84,v[176:179],188
M 84,v[180:183],184
L 0
v_and_b32 v36,0xffff0000,v25
v_lshlrev_b32 v37,16,v25
v_and_b32 v38,0xffff0000,v24
v_lshlrev_b32 v39,16,v24
v_fma_f32 v64,v36,s87,v64
v_fma_f32 v65,v37,s87,v65
v_fma_f32 v66,v38,s87,v66
v_fma_f32 v67,v39,s87,v67
v_cvt_pk_bf16_f32 v64,v64,v65
v_cvt_pk_bf16_f32 v65,v66,v67
ds_write_b64 v4,v[64:65] offset:12928
v_and_b32 v36,0xffff0000,v27
v_lshlrev_b32 v37,16,v27
v_and_b32 v38,0xffff0000,v26
v_lshlrev_b32 v39,16,v26
v_fma_f32 v68,v36,s87,v68
v_fma_f32 v69,v37,s87,v69
v_fma_f32 v70,v38,s87,v70
v_fma_f32 v71,v39,s87,v71
v_cvt_pk_bf16_f32 v68,v68,v69
v_cvt_pk_bf16_f32 v69,v70,v71
ds_write_b64 v4,v[68:69] offset:13440
v_and_b32 v36,0xffff0000,v29
v_lshlrev_b32 v37,16,v29
v_and_b32 v38,0xffff0000,v28
v_lshlrev_b32 v39,16,v28
v_fma_f32 v72,v36,s87,v72
v_fma_f32 v73,v37,s87,v73
v_fma_f32 v74,v38,s87,v74
v_fma_f32 v75,v39,s87,v75
v_cvt_pk_bf16_f32 v72,v72,v73
v_cvt_pk_bf16_f32 v73,v74,v75
ds_write_b64 v4,v[72:73] offset:13952
v_and_b32 v36,0xffff0000,v31
v_lshlrev_b32 v37,16,v31
v_and_b32 v38,0xffff0000,v30
v_lshlrev_b32 v39,16,v30
v_fma_f32 v76,v36,s87,v76
v_fma_f32 v77,v37,s87,v77
v_fma_f32 v78,v38,s87,v78
v_fma_f32 v79,v39,s87,v79
v_cvt_pk_bf16_f32 v76,v76,v77
v_cvt_pk_bf16_f32 v77,v78,v79
ds_write_b64 v4,v[76:77] offset:14464
v_and_b32 v36,0xffff0000,v33
v_lshlrev_b32 v37,16,v33
v_and_b32 v38,0xffff0000,v32
v_lshlrev_b32 v39,16,v32
v_fma_f32 v80,v36,s87,v80
v_fma_f32 v81,v37,s87,v81
v_fma_f32 v82,v38,s87,v82
v_fma_f32 v83,v39,s87,v83
v_cvt_pk_bf16_f32 v80,v80,v81
v_cvt_pk_bf16_f32 v81,v82,v83
ds_write_b64 v4,v[80:81] offset:14976
v_and_b32 v36,0xffff0000,v35
v_lshlrev_b32 v37,16,v35
v_and_b32 v38,0xffff0000,v34
v_lshlrev_b32 v39,16,v34
v_fma_f32 v84,v36,s87,v84
v_fma_f32 v85,v37,s87,v85
v_fma_f32 v86,v38,s87,v86
v_fma_f32 v87,v39,s87,v87
v_cvt_pk_bf16_f32 v84,v84,v85
v_cvt_pk_bf16_f32 v85,v86,v87
ds_write_b64 v4,v[84:85] offset:15488
Lcv_e_%=:
L 0
s_barrier
R 152,5,0
R 156,5,512
R 160,5,1024
R 164,5,1536
R 168,5,2048
R 172,5,2560
s_mov_b32 s78,99614720
L 5
buffer_store_dwordx4 v[152:155],v7,s[36:39],s78 offen offset:0 sc0 sc1
s_add_u32 s78,s56,100139008
L 4
buffer_store_dwordx4 v[156:159],v7,s[36:39],s78 offen offset:0 sc0 sc1
s_mov_b32 s78,100663296
L 3
buffer_store_dwordx4 v[160:163],v7,s[36:39],s78 offen offset:0 sc0 sc1
s_add_u32 s78,s56,101187584
L 2
buffer_store_dwordx4 v[164:167],v7,s[36:39],s78 offen offset:0 sc0 sc1
s_mov_b32 s78,101711872
L 1
buffer_store_dwordx4 v[168:171],v7,s[36:39],s78 offen offset:0 sc0 sc1
s_add_u32 s78,s56,102236160
L 0
buffer_store_dwordx4 v[172:175],v7,s[36:39],s78 offen offset:0 sc0 sc1
s_waitcnt vmcnt(0)
s_barrier
s_add_u32 s48,s48,32768
buffer_store_dword v23,v10,s[36:39],s48 offen offset:0 sc0 sc1
s_cmp_eq_u32 s51,2
s_cbranch_scc0 Lcv_f_%=
s_mov_b32 m0,163584
s_mov_b32 s78,3694592
buffer_load_dword v12,s[60:63],s78 offen offset:0  lds
s_mov_b32 m0,163584
s_mov_b32 s78,5541888
buffer_load_dword v12,s[60:63],s78 offen offset:0  lds
Lcv_f_%=:
Lcode_cv_e:
.purgem M
.purgem R
.purgem T
.purgem D
.purgem L
)"
  :: "s"(ws), "s"(sWave), "s"(sBuf), "s"(sAuxOff), "s"(sFoutOff), "s"(sC0), "s"(sC1), "s"(sExp), "v"(vL16), "v"(vPA), "v"(vPB), "v"(vPU), "v"(vPW), "v"(vOR), "v"(vORA), "v"(vOG), "v"(vFA), "v"(vFB), "v"(vFlag), "v"(vTW), "v"(vTX), "s"(w3), "s"(w4) : "v0", "v1", "v2", "v3", "v4", "v5", "v6", "v7", "v8", "v9", "v10", "v11", "v12", "v13", "v14", "v15", "v16", "v17", "v18", "v19", "v20", "v21", "v22", "v23", "v24", "v25", "v26", "v27", "v28", "v29", "v30", "v31", "v32", "v33", "v34", "v35", "v36", "v37", "v38", "v39", "v40", "v41", "v42", "v43", "v44", "v45", "v46", "v47", "v48", "v49", "v50", "v51", "v52", "v53", "v54", "v55", "v56", "v57", "v58", "v59", "v60", "v61", "v62", "v63", "v64", "v65", "v66", "v67", "v68", "v69", "v70", "v71", "v72", "v73", "v74", "v75", "v76", "v77", "v78", "v79", "v80", "v81", "v82", "v83", "v84", "v85", "v86", "v87", "v88", "v89", "v90", "v91", "v92", "v93", "v94", "v95", "v96", "v97", "v98", "v99", "v100", "v101", "v102", "v103", "v104", "v105", "v106", "v107", "v108", "v109", "v110", "v111", "v112", "v113", "v114", "v115", "v116", "v117", "v118", "v119", "v120", "v121", "v122", "v123", "v124", "v125", "v126", "v127", "v128", "v129", "v130", "v131", "v132", "v133", "v134", "v135", "v136", "v137", "v138", "v139", "v140", "v141", "v142", "v143", "v144", "v145", "v146", "v147", "v148", "v149", "v150", "v151", "v152", "v153", "v154", "v155", "v156", "v157", "v158", "v159", "v160", "v161", "v162", "v163", "v164", "v165", "v166", "v167", "v168", "v169", "v170", "v171", "v172", "v173", "v174", "v175", "v176", "v177", "v178", "v179", "v180", "v181", "v182", "v183", "v184", "v185", "v186", "v187", "v188", "v189", "v190", "v191", "v192", "v193", "v194", "v195", "v196", "v197", "v198", "v199", "v200", "v201", "v202", "v203", "v204", "v205", "v206", "v207", "v208", "v209", "v210", "v211", "v212", "v213", "v214", "v215", "v216", "v217", "v218", "v219", "v220", "v221", "v222", "v223", "v224", "v225", "v226", "v227", "v228", "v229", "v230", "v231", "a0", "a1", "a2", "a3", "a4", "a5", "a6", "a7", "a8", "a9", "a10", "a11", "a12", "a13", "a14", "a15", "a16", "a17", "a18", "a19", "a20", "a21", "a22", "a23", "a24", "a25", "a26", "a27", "a28", "a29", "a30", "a31", "a32", "a33", "a34", "a35", "a36", "a37", "a38", "a39", "a40", "a41", "a42", "a43", "a44", "a45", "a46", "a47", "a48", "a49", "a50", "a51", "a52", "a53", "a54", "a55", "a56", "a57", "a58", "a59", "a60", "a61", "a62", "a63", "a64", "a65", "a66", "a67", "a68", "a69", "a70", "a71", "a72", "a73", "a74", "a75", "a76", "a77", "a78", "a79", "a80", "a81", "a82", "a83", "a84", "a85", "a86", "a87", "a88", "a89", "a90", "a91", "a92", "a93", "a94", "a95", "a96", "a97", "a98", "a99", "a100", "a101", "a102", "a103", "a104", "a105", "a106", "a107", "a108", "a109", "a110", "a111", "a112", "a113", "a114", "a115", "a116", "a117", "a118", "a119", "a120", "a121", "a122", "a123", "a124", "a125", "a126", "a127", "a128", "a129", "a130", "a131", "a132", "a133", "a134", "a135", "a136", "a137", "a138", "a139", "a140", "a141", "a142", "a143", "a144", "a145", "a146", "a147", "a148", "a149", "a150", "a151", "a152", "a153", "a154", "a155", "a156", "a157", "a158", "a159", "a160", "a161", "a162", "a163", "a164", "a165", "a166", "a167", "a168", "a169", "a170", "a171", "a172", "a173", "a174", "a175", "a176", "a177", "a178", "a179", "a180", "a181", "a182", "a183", "a184", "a185", "a186", "a187", "a188", "a189", "a190", "a191", "a192", "a193", "a194", "a195", "a196", "a197", "a198", "a199", "a200", "a201", "a202", "a203", "a204", "a205", "a206", "a207", "a208", "a209", "a210", "a211", "a212", "a213", "a214", "a215", "a216", "a217", "a218", "a219", "a220", "a221", "a222", "a223", "a224", "a225", "a226", "a227", "a228", "a229", "a230", "a231", "a232", "a233", "a234", "a235", "a236", "a237", "a238", "a239", "a240", "a241", "a242", "a243", "a244", "a245", "a246", "a247", "a248", "a249", "a250", "a251", "a252", "a253", "a254", "a255", "s36", "s37", "s38", "s39", "s40", "s41", "s42", "s43", "s44", "s45", "s46", "s47", "s48", "s49", "s50", "s51", "s52", "s53", "s54", "s55", "s56", "s57", "s58", "s59", "s60", "s61", "s62", "s63", "s64", "s65", "s66", "s67", "s68", "s69", "s70", "s71", "s72", "s73", "s74", "s75", "s76", "s77", "s78", "s79", "s80", "s81", "s82", "s83", "s84", "s85", "s86", "s87", "s88", "s89", "s90", "s91", "s92", "s93", "s94", "s95", "s96", "s97", "vcc", "scc", "memory");
    LANE_ID(lq_);
    const int g = QT0 + (NCV - 1 - iw) * 164 + min(wave * 64 + lq_, 163);
    if (QTB) touch_lds((const char*)q + (size_t)(g < 32768 ? g : 0) * 128, sScr);
  }
  WAIT_LGKM0();
  __builtin_amdgcn_s_barrier();
  CTS(10);
  }
  const unsigned sDbg0 = 110u * 1048576u + (b * 4 + wave) * 8192;
  constexpr unsigned OOB = 0x80000000u;
#define HLP 0
  if (kind == 6 || (kind == 4 && j4 < HLP)) {
    LANE_ID(lr);
    const int r8 = lr & 7, seg = lr >> 3, j = lr & 15, g = lr >> 4;
    const unsigned sWave = wave, sExp = __builtin_amdgcn_readfirstlane(ep), sG0 = kind == 6 ? slice : 64 + j4, sStep = NRW, sOutOff = kind == 6 ? 256 : 65 + j4, sAuxOff = HLP, sBuf = kind & 2, sRk = slice;
    const unsigned sDbg = sDbg0 + (kind == 6 ? 0 : 1024);
    const unsigned vRF = (wave * 8 + (seg >> 2)) * 1024 + ((seg & 3) * 16 + r8) * 16;
    const unsigned vRwr = seg == 0 ? r8 * 64 + wave * 4 : 512 + lr * 4, vRrd = r8 * 64;
    const unsigned vRP = g == (j >> 2) ? r8 * 64 + 16 + wave * 8 + (seg & 1) * 4 : 768 + lr * 4;
    const unsigned vRS = (wave == 0 && seg < 2) ? r8 * 16 + seg * 256 : OOB, vFlag = lr < 32 ? lr * 4 : OOB;
    const unsigned vL4 = ((lr & 2) * 4 + wave * 2 + (lr & 1)) * 128, vLY = min((wave < 3 ? wave * 32 : 112) + lr, wave < 2 ? wave * 32 + 31 : wave == 2 ? 111 : NCV - 1) * 128;
    const unsigned vQ = r8 * 2048 + wave * 512 + seg * 16;
    const unsigned vY0 = wave * 4096 + lr * 16;
    const unsigned vTr = 4096 + wave * 4096 + ((2 * g + ((j & 3) >> 1)) * 8 + (j >> 2)) * 16 + (j & 1) * 8;
    const unsigned one = g == (j >> 2) ? 0x3f80u << (16 * (j & 1)) : 0u, vSa = (j & 2) ? 0u : one, vSb = (j & 2) ? one : 0u;
    (void)sDbg;
    asm volatile(
R"(.macro M d,w,x
v_mfma_f32_16x16x32_bf16 v[\d:\d+3], \w, v[\x:\x+3], v[\d:\d+3]
.endm
.macro R d,a,o
ds_read_b128 v[\d:\d+3], v\a offset:\o
.endm
.macro T d,a,o
ds_read_b64_tr_b16 \d, v\a offset:\o
.endm
.macro D a,s,o
buffer_load_dwordx4 v\a, s[36:39], s\s offen offset:\o sc1 lds
.endm
.macro L c
s_waitcnt lgkmcnt(\c)
.endm
Lcode_r_b:
s_mov_b64 s[36:37],%0
s_mov_b32 s38,111243392
s_mov_b32 s39,0x00020000
s_mov_b32 s44,%1
s_mov_b32 s45,%2
s_mov_b32 s46,%3
s_mov_b32 s47,%4
s_mov_b32 s49,%5
s_mov_b32 s51,%6
s_mov_b32 s93,%7
s_mov_b32 s53,%8
v_mov_b32 v0,%9
v_mov_b32 v1,%10
v_mov_b32 v2,%11
v_mov_b32 v3,%12
v_mov_b32 v4,%13
v_mov_b32 v5,%14
v_mov_b32 v6,%15
v_mov_b32 v7,%16
v_mov_b32 v8,%17
v_mov_b32 v9,%18
v_mov_b32 v10,%19
v_mov_b32 v11,%20
v_mov_b32 v12,%21
s_mov_b64 s[84:85],%22
s_mov_b32 s86,4194304
s_mov_b32 s87,0x00020000
s_lshl_b32 s55,s45,14
buffer_load_dwordx4 v[164:167],v8,s[84:87],s55 offen offset:0 nt
buffer_load_dwordx4 v[168:171],v8,s[84:87],s55 offen offset:128 nt
buffer_load_dwordx4 v[172:175],v8,s[84:87],s55 offen offset:256 nt
buffer_load_dwordx4 v[176:179],v8,s[84:87],s55 offen offset:384 nt
v_mov_b32 v13,s53
v_mbcnt_lo_u32_b32 v15,-1,0
v_mbcnt_hi_u32_b32 v15,-1,v15
v_lshlrev_b32 v15,4,v15
s_mov_b32 s82,0
s_mov_b32 s63,0x7fff0000
s_mov_b32 s64,0
s_mov_b32 s67,0
s_lshl_b32 s65,s44,12
s_add_u32 s65,s65,4096
s_lshl_b32 s66,s44,7
s_add_u32 s66,s66,109056000
s_mov_b32 s70,0xaaaaaaaa
s_mov_b32 s71,0xaaaaaaaa
s_mov_b32 s72,0xcccccccc
s_mov_b32 s73,0xcccccccc
s_mov_b32 s74,0xff00ff00
s_mov_b32 s75,0xff00ff00
s_mov_b32 s68,0x3a800000
s_mov_b32 s69,0x3727c5ac
s_mov_b32 s76,0xc0135761
s_mov_b32 s77,0x3d372713
v_mov_b32 v32,0x3f803f80
v_mov_b32 v33,0x3f803f80
v_mov_b32 v34,0x3f803f80
v_mov_b32 v35,0x3f803f80
v_mov_b32 v24,0
v_mov_b32 v25,0
v_mov_b32 v26,v11
v_mov_b32 v27,v12
v_mov_b32 v28,0
v_mov_b32 v29,0
s_waitcnt vmcnt(0)
Lr_a0_%=:
s_waitcnt vmcnt(10)
v_mfma_f32_16x16x32_bf16 v[36:39],v[32:35],v[164:167],0
s_add_u32 s79,s45,s46
s_cmp_lt_u32 s45,64
s_cselect_b32 s78,s49,0
v_mfma_f32_16x16x32_bf16 v[40:43],v[164:167],v[164:167],0
s_cmp_ge_u32 s79,64
s_cselect_b32 s78,s78,0
s_add_u32 s62,s79,s78
M 36,v[32:35],168
s_lshr_b32 s78,s45,1
s_and_b32 s79,s45,1
s_lshl_b32 s79,s79,7
M 40,v[168:171],168
s_lshl_b32 s57,s78,15
s_lshl_b32 s55,s62,14
s_cmp_lt_u32 s62,s47
M 36,v[32:35],172
s_cselect_b32 s55,s55,0x7fff0000
buffer_load_dwordx4 v[196:199],v8,s[84:87],s55 offen offset:0 nt
M 40,v[172:175],172
s_add_u32 s57,s57,s79
buffer_load_dwordx4 v[200:203],v8,s[84:87],s55 offen offset:128 nt
M 36,v[32:35],176
s_lshl_b32 s59,s78,9
buffer_load_dwordx4 v[204:207],v8,s[84:87],s55 offen offset:256 nt
M 40,v[176:179],176
s_add_u32 s57,s57,8388608
buffer_load_dwordx4 v[208:211],v8,s[84:87],s55 offen offset:384 nt
v_mfma_f32_16x16x32_bf16 v[116:119],v[26:29],v[164:167],0
s_add_u32 s59,s59,s79
s_lshl_b32 s60,s45,9
s_add_u32 s58,s57,4096
v_mfma_f32_16x16x32_bf16 v[120:123],v[24:27],v[164:167],0
s_add_u32 s59,s59,106954752
s_add_u32 s60,s60,s66
v_mfma_f32_16x16x32_bf16 v[124:127],v[26:29],v[168:171],0
v_mfma_f32_16x16x32_bf16 v[128:131],v[24:27],v[168:171],0
v_add_f32_dpp v44,v36,v36 row_ror:8 row_mask:0xf bank_mask:0xf
v_cndmask_b32 v45,v40,v41,s[70:71]
v_mfma_f32_16x16x32_bf16 v[132:135],v[26:29],v[172:175],0
ds_write_b32 v2,v44 offset:0
v_mfma_f32_16x16x32_bf16 v[136:139],v[24:27],v[172:175],0
v_cndmask_b32 v46,v42,v43,s[70:71]
v_cndmask_b32 v45,v45,v46,s[72:73]
v_mfma_f32_16x16x32_bf16 v[140:143],v[26:29],v[176:179],0
ds_write_b32 v0,v45 offset:0
v_mfma_f32_16x16x32_bf16 v[144:147],v[24:27],v[176:179],0
L 0
s_barrier
R 52,3,0
R 56,3,16
R 60,3,32
s_cmp_lg_u32 s67,0
s_cbranch_scc1 Lr_b0_%=
s_waitcnt vmcnt(4)
buffer_store_dword v13,v5,s[36:39],s63 offen offset:0 sc0 sc1
s_mov_b32 s63,0x7fff0000
s_cmp_lt_u32 s45,64
s_cselect_b32 s64,-192,0
s_add_u32 s64,s64,256
s_cmp_lt_u32 s45,64
s_cselect_b32 s61,0,32768
s_add_u32 s61,s61,111177728
s_mul_i32 s56,s45,16384
s_add_u32 s56,s56,98566144
Lr_bpy0_%=:
buffer_load_dword v14,v7,s[36:39],s61 offen offset:0 sc1
s_add_u32 s82,s82,1
s_waitcnt vmcnt(0)
v_cmp_eq_u32 vcc,s53,v14
s_nop 0
s_cmp_eq_u64 vcc,-1
s_cbranch_scc1 Lr_bqy0_%=
s_sleep 2
s_cmp_lt_u32 s82,20000
s_cbranch_scc1 Lr_bpy0_%=
Lr_bqy0_%=:
s_add_u32 m0,s65,0
s_nop 0
D 9,56,0
D 9,56,1024
D 9,56,2048
D 9,56,3072
s_waitcnt vmcnt(0)
Lr_b0_%=:
s_waitcnt vmcnt(10)
T v[68:69],10,0
T v[70:71],10,64
T v[72:73],10,1024
T v[74:75],10,1088
T v[76:77],10,2048
T v[78:79],10,2112
T v[80:81],10,3072
T v[82:83],10,3136
s_mov_b32 s45,s62
s_min_u32 s78,s64,s47
s_mul_i32 s56,s45,16384
s_add_u32 s56,s56,98566144
s_cmp_lt_u32 s45,s78
s_cselect_b32 s67,1,0
s_cselect_b32 s56,s56,0x7fff0000
s_add_u32 m0,s65,16384
s_nop 0
D 9,56,0
D 9,56,1024
D 9,56,2048
D 9,56,3072
L 8
v_pk_add_f32 v[52:53],v[52:53],v[54:55]
v_pk_add_f32 v[56:57],v[56:57],v[58:59]
v_pk_add_f32 v[60:61],v[60:61],v[62:63]
v_add_f32 v52,v52,v53
v_pk_add_f32 v[56:57],v[56:57],v[60:61]
v_mul_f32 v52,s68,v52
v_add_f32 v53,v56,v57
v_mul_f32 v53,s68,v53
v_fma_f32 v53,-v52,v52,v53
v_max_f32 v53,0,v53
L 6
v_lshlrev_b32 v84,16,v68
v_and_b32 v85,0xffff0000,v68
v_lshlrev_b32 v86,16,v69
v_and_b32 v87,0xffff0000,v69
v_lshlrev_b32 v88,16,v70
v_and_b32 v89,0xffff0000,v70
v_lshlrev_b32 v90,16,v71
v_and_b32 v91,0xffff0000,v71
L 4
v_lshlrev_b32 v92,16,v72
v_and_b32 v93,0xffff0000,v72
v_lshlrev_b32 v94,16,v73
v_and_b32 v95,0xffff0000,v73
v_lshlrev_b32 v96,16,v74
v_and_b32 v97,0xffff0000,v74
v_lshlrev_b32 v98,16,v75
v_and_b32 v99,0xffff0000,v75
L 2
v_lshlrev_b32 v100,16,v76
v_and_b32 v101,0xffff0000,v76
v_lshlrev_b32 v102,16,v77
v_and_b32 v103,0xffff0000,v77
v_lshlrev_b32 v104,16,v78
v_and_b32 v105,0xffff0000,v78
v_lshlrev_b32 v106,16,v79
v_and_b32 v107,0xffff0000,v79
L 0
v_lshlrev_b32 v108,16,v80
v_and_b32 v109,0xffff0000,v80
v_lshlrev_b32 v110,16,v81
v_and_b32 v111,0xffff0000,v81
v_lshlrev_b32 v112,16,v82
v_and_b32 v113,0xffff0000,v82
v_lshlrev_b32 v114,16,v83
v_and_b32 v115,0xffff0000,v83
v_add_f32 v53,s69,v53
v_rsq_f32 v48,v53
s_nop 0
v_mul_f32_e64 v50,-v52,v48
v_mov_b32 v49,v48
v_mov_b32 v51,v50
v_pk_fma_f32 v[52:53],v[116:117],v[48:49],v[50:51]
v_pk_fma_f32 v[54:55],v[118:119],v[48:49],v[50:51]
v_pk_fma_f32 v[56:57],v[120:121],v[48:49],v[50:51]
v_pk_fma_f32 v[58:59],v[122:123],v[48:49],v[50:51]
v_pk_fma_f32 v[116:117],v[52:53],v[84:85],v[116:117]
v_pk_fma_f32 v[118:119],v[54:55],v[86:87],v[118:119]
v_pk_fma_f32 v[120:121],v[56:57],v[88:89],v[120:121]
v_pk_fma_f32 v[122:123],v[58:59],v[90:91],v[122:123]
v_cvt_pk_bf16_f32 v148,v116,v117
v_cvt_pk_bf16_f32 v149,v118,v119
v_cvt_pk_bf16_f32 v150,v120,v121
v_cvt_pk_bf16_f32 v151,v122,v123
v_pk_fma_f32 v[52:53],v[124:125],v[48:49],v[50:51]
v_pk_fma_f32 v[54:55],v[126:127],v[48:49],v[50:51]
v_pk_fma_f32 v[56:57],v[128:129],v[48:49],v[50:51]
v_pk_fma_f32 v[58:59],v[130:131],v[48:49],v[50:51]
v_pk_fma_f32 v[124:125],v[52:53],v[92:93],v[124:125]
v_pk_fma_f32 v[126:127],v[54:55],v[94:95],v[126:127]
v_pk_fma_f32 v[128:129],v[56:57],v[96:97],v[128:129]
v_pk_fma_f32 v[130:131],v[58:59],v[98:99],v[130:131]
v_cvt_pk_bf16_f32 v152,v124,v125
v_cvt_pk_bf16_f32 v153,v126,v127
v_cvt_pk_bf16_f32 v154,v128,v129
v_cvt_pk_bf16_f32 v155,v130,v131
v_pk_fma_f32 v[52:53],v[132:133],v[48:49],v[50:51]
v_pk_fma_f32 v[54:55],v[134:135],v[48:49],v[50:51]
v_pk_fma_f32 v[56:57],v[136:137],v[48:49],v[50:51]
v_pk_fma_f32 v[58:59],v[138:139],v[48:49],v[50:51]
v_pk_fma_f32 v[132:133],v[52:53],v[100:101],v[132:133]
v_pk_fma_f32 v[134:135],v[54:55],v[102:103],v[134:135]
v_pk_fma_f32 v[136:137],v[56:57],v[104:105],v[136:137]
v_pk_fma_f32 v[138:139],v[58:59],v[106:107],v[138:139]
v_cvt_pk_bf16_f32 v156,v132,v133
v_cvt_pk_bf16_f32 v157,v134,v135
v_cvt_pk_bf16_f32 v158,v136,v137
v_cvt_pk_bf16_f32 v159,v138,v139
v_pk_fma_f32 v[52:53],v[140:141],v[48:49],v[50:51]
v_pk_fma_f32 v[54:55],v[142:143],v[48:49],v[50:51]
v_pk_fma_f32 v[56:57],v[144:145],v[48:49],v[50:51]
v_pk_fma_f32 v[58:59],v[146:147],v[48:49],v[50:51]
v_pk_fma_f32 v[140:141],v[52:53],v[108:109],v[140:141]
v_pk_fma_f32 v[142:143],v[54:55],v[110:111],v[142:143]
v_pk_fma_f32 v[144:145],v[56:57],v[112:113],v[144:145]
v_pk_fma_f32 v[146:147],v[58:59],v[114:115],v[146:147]
v_cvt_pk_bf16_f32 v160,v140,v141
v_cvt_pk_bf16_f32 v161,v142,v143
v_cvt_pk_bf16_f32 v162,v144,v145
v_cvt_pk_bf16_f32 v163,v146,v147
s_waitcnt vmcnt(8)
buffer_store_dword v13,v5,s[36:39],s63 offen offset:0 sc0 sc1
s_mov_b32 s63,0x7fff0000
v_mfma_f32_16x16x32_bf16 v[36:39],v[32:35],v[148:151],0
buffer_store_dwordx4 v[148:151],v1,s[36:39],s57 offen offset:0 sc0 sc1
v_mfma_f32_16x16x32_bf16 v[40:43],v[148:151],v[148:151],0
M 36,v[32:35],152
buffer_store_dwordx4 v[152:155],v1,s[36:39],s57 offen offset:2048 sc0 sc1
M 40,v[152:155],152
M 36,v[32:35],156
buffer_store_dwordx4 v[156:159],v1,s[36:39],s58 offen offset:0 sc0 sc1
M 40,v[156:159],156
M 36,v[32:35],160
buffer_store_dwordx4 v[160:163],v1,s[36:39],s58 offen offset:2048 sc0 sc1
M 40,v[160:163],160
s_nop 5
v_add_f32_dpp v44,v36,v36 row_ror:8 row_mask:0xf bank_mask:0xf
s_nop 0
v_cndmask_b32 v45,v40,v41,s[70:71]
v_cndmask_b32 v46,v42,v43,s[70:71]
ds_write_b32 v2,v44 offset:1024
v_cndmask_b32 v45,v45,v46,s[72:73]
ds_write_b32 v0,v45 offset:1024
L 0
s_barrier
R 52,3,1024
R 56,3,1040
R 60,3,1056
L 0
v_pk_add_f32 v[52:53],v[52:53],v[54:55]
v_pk_add_f32 v[56:57],v[56:57],v[58:59]
v_pk_add_f32 v[60:61],v[60:61],v[62:63]
v_add_f32 v52,v52,v53
v_pk_add_f32 v[56:57],v[56:57],v[60:61]
v_mul_f32 v52,s68,v52
v_add_f32 v53,v56,v57
v_mul_f32 v53,s68,v53
v_fma_f32 v53,-v52,v52,v53
v_max_f32 v53,0,v53
v_add_f32 v54,s69,v53
v_fma_f32 v55,s69,v54,v53
v_rsq_f32 v65,v54
v_rsq_f32 v54,v55
v_mul_f32_e64 v64,-v52,v65
v_mul_f32 v63,v55,v54
v_mul_f32 v61,s76,v54
v_mul_f32 v55,v54,v54
v_mul_f32 v62,s77,v55
v_mul_f32 v62,v62,v61
v_cndmask_b32 v60,v52,v64,s[74:75]
v_cndmask_b32 v61,v61,v65,s[74:75]
buffer_store_dwordx4 v[60:63],v4,s[36:39],s59 offen offset:0 sc0 sc1
s_mov_b32 s63,s60
s_cmp_lt_u32 s45,s47
s_cbranch_scc0 Lr_mid_%=
Lr_a1_%=:
s_waitcnt vmcnt(10)
v_mfma_f32_16x16x32_bf16 v[36:39],v[32:35],v[196:199],0
s_add_u32 s79,s45,s46
s_cmp_lt_u32 s45,64
s_cselect_b32 s78,s49,0
v_mfma_f32_16x16x32_bf16 v[40:43],v[196:199],v[196:199],0
s_cmp_ge_u32 s79,64
s_cselect_b32 s78,s78,0
s_add_u32 s62,s79,s78
M 36,v[32:35],200
s_lshr_b32 s78,s45,1
s_and_b32 s79,s45,1
s_lshl_b32 s79,s79,7
M 40,v[200:203],200
s_lshl_b32 s57,s78,15
s_lshl_b32 s55,s62,14
s_cmp_lt_u32 s62,s47
M 36,v[32:35],204
s_cselect_b32 s55,s55,0x7fff0000
buffer_load_dwordx4 v[164:167],v8,s[84:87],s55 offen offset:0 nt
M 40,v[204:207],204
s_add_u32 s57,s57,s79
buffer_load_dwordx4 v[168:171],v8,s[84:87],s55 offen offset:128 nt
M 36,v[32:35],208
s_lshl_b32 s59,s78,9
buffer_load_dwordx4 v[172:175],v8,s[84:87],s55 offen offset:256 nt
M 40,v[208:211],208
s_add_u32 s57,s57,8388608
buffer_load_dwordx4 v[176:179],v8,s[84:87],s55 offen offset:384 nt
v_mfma_f32_16x16x32_bf16 v[116:119],v[26:29],v[196:199],0
s_add_u32 s59,s59,s79
s_lshl_b32 s60,s45,9
s_add_u32 s58,s57,4096
v_mfma_f32_16x16x32_bf16 v[120:123],v[24:27],v[196:199],0
s_add_u32 s59,s59,106954752
s_add_u32 s60,s60,s66
v_mfma_f32_16x16x32_bf16 v[124:127],v[26:29],v[200:203],0
v_mfma_f32_16x16x32_bf16 v[128:131],v[24:27],v[200:203],0
v_add_f32_dpp v44,v36,v36 row_ror:8 row_mask:0xf bank_mask:0xf
v_cndmask_b32 v45,v40,v41,s[70:71]
v_mfma_f32_16x16x32_bf16 v[132:135],v[26:29],v[204:207],0
ds_write_b32 v2,v44 offset:0
v_mfma_f32_16x16x32_bf16 v[136:139],v[24:27],v[204:207],0
v_cndmask_b32 v46,v42,v43,s[70:71]
v_cndmask_b32 v45,v45,v46,s[72:73]
v_mfma_f32_16x16x32_bf16 v[140:143],v[26:29],v[208:211],0
ds_write_b32 v0,v45 offset:0
v_mfma_f32_16x16x32_bf16 v[144:147],v[24:27],v[208:211],0
L 0
s_barrier
R 52,3,0
R 56,3,16
R 60,3,32
s_cmp_lg_u32 s67,0
s_cbranch_scc1 Lr_b1_%=
s_waitcnt vmcnt(4)
buffer_store_dword v13,v5,s[36:39],s63 offen offset:0 sc0 sc1
s_mov_b32 s63,0x7fff0000
s_cmp_lt_u32 s45,64
s_cselect_b32 s64,-192,0
s_add_u32 s64,s64,256
s_cmp_lt_u32 s45,64
s_cselect_b32 s61,0,32768
s_add_u32 s61,s61,111177728
s_mul_i32 s56,s45,16384
s_add_u32 s56,s56,98566144
Lr_bpy1_%=:
buffer_load_dword v14,v7,s[36:39],s61 offen offset:0 sc1
s_add_u32 s82,s82,1
s_waitcnt vmcnt(0)
v_cmp_eq_u32 vcc,s53,v14
s_nop 0
s_cmp_eq_u64 vcc,-1
s_cbranch_scc1 Lr_bqy1_%=
s_sleep 2
s_cmp_lt_u32 s82,20000
s_cbranch_scc1 Lr_bpy1_%=
Lr_bqy1_%=:
s_add_u32 m0,s65,16384
s_nop 0
D 9,56,0
D 9,56,1024
D 9,56,2048
D 9,56,3072
s_waitcnt vmcnt(0)
Lr_b1_%=:
s_waitcnt vmcnt(10)
T v[68:69],10,16384
T v[70:71],10,16448
T v[72:73],10,17408
T v[74:75],10,17472
T v[76:77],10,18432
T v[78:79],10,18496
T v[80:81],10,19456
T v[82:83],10,19520
s_mov_b32 s45,s62
s_min_u32 s78,s64,s47
s_mul_i32 s56,s45,16384
s_add_u32 s56,s56,98566144
s_cmp_lt_u32 s45,s78
s_cselect_b32 s67,1,0
s_cselect_b32 s56,s56,0x7fff0000
s_add_u32 m0,s65,0
s_nop 0
D 9,56,0
D 9,56,1024
D 9,56,2048
D 9,56,3072
L 8
v_pk_add_f32 v[52:53],v[52:53],v[54:55]
v_pk_add_f32 v[56:57],v[56:57],v[58:59]
v_pk_add_f32 v[60:61],v[60:61],v[62:63]
v_add_f32 v52,v52,v53
v_pk_add_f32 v[56:57],v[56:57],v[60:61]
v_mul_f32 v52,s68,v52
v_add_f32 v53,v56,v57
v_mul_f32 v53,s68,v53
v_fma_f32 v53,-v52,v52,v53
v_max_f32 v53,0,v53
L 6
v_lshlrev_b32 v84,16,v68
v_and_b32 v85,0xffff0000,v68
v_lshlrev_b32 v86,16,v69
v_and_b32 v87,0xffff0000,v69
v_lshlrev_b32 v88,16,v70
v_and_b32 v89,0xffff0000,v70
v_lshlrev_b32 v90,16,v71
v_and_b32 v91,0xffff0000,v71
L 4
v_lshlrev_b32 v92,16,v72
v_and_b32 v93,0xffff0000,v72
v_lshlrev_b32 v94,16,v73
v_and_b32 v95,0xffff0000,v73
v_lshlrev_b32 v96,16,v74
v_and_b32 v97,0xffff0000,v74
v_lshlrev_b32 v98,16,v75
v_and_b32 v99,0xffff0000,v75
L 2
v_lshlrev_b32 v100,16,v76
v_and_b32 v101,0xffff0000,v76
v_lshlrev_b32 v102,16,v77
v_and_b32 v103,0xffff0000,v77
v_lshlrev_b32 v104,16,v78
v_and_b32 v105,0xffff0000,v78
v_lshlrev_b32 v106,16,v79
v_and_b32 v107,0xffff0000,v79
L 0
v_lshlrev_b32 v108,16,v80
v_and_b32 v109,0xffff0000,v80
v_lshlrev_b32 v110,16,v81
v_and_b32 v111,0xffff0000,v81
v_lshlrev_b32 v112,16,v82
v_and_b32 v113,0xffff0000,v82
)"
R"(v_lshlrev_b32 v114,16,v83
v_and_b32 v115,0xffff0000,v83
v_add_f32 v53,s69,v53
v_rsq_f32 v48,v53
s_nop 0
v_mul_f32_e64 v50,-v52,v48
v_mov_b32 v49,v48
v_mov_b32 v51,v50
v_pk_fma_f32 v[52:53],v[116:117],v[48:49],v[50:51]
v_pk_fma_f32 v[54:55],v[118:119],v[48:49],v[50:51]
v_pk_fma_f32 v[56:57],v[120:121],v[48:49],v[50:51]
v_pk_fma_f32 v[58:59],v[122:123],v[48:49],v[50:51]
v_pk_fma_f32 v[116:117],v[52:53],v[84:85],v[116:117]
v_pk_fma_f32 v[118:119],v[54:55],v[86:87],v[118:119]
v_pk_fma_f32 v[120:121],v[56:57],v[88:89],v[120:121]
v_pk_fma_f32 v[122:123],v[58:59],v[90:91],v[122:123]
v_cvt_pk_bf16_f32 v148,v116,v117
v_cvt_pk_bf16_f32 v149,v118,v119
v_cvt_pk_bf16_f32 v150,v120,v121
v_cvt_pk_bf16_f32 v151,v122,v123
v_pk_fma_f32 v[52:53],v[124:125],v[48:49],v[50:51]
v_pk_fma_f32 v[54:55],v[126:127],v[48:49],v[50:51]
v_pk_fma_f32 v[56:57],v[128:129],v[48:49],v[50:51]
v_pk_fma_f32 v[58:59],v[130:131],v[48:49],v[50:51]
v_pk_fma_f32 v[124:125],v[52:53],v[92:93],v[124:125]
v_pk_fma_f32 v[126:127],v[54:55],v[94:95],v[126:127]
v_pk_fma_f32 v[128:129],v[56:57],v[96:97],v[128:129]
v_pk_fma_f32 v[130:131],v[58:59],v[98:99],v[130:131]
v_cvt_pk_bf16_f32 v152,v124,v125
v_cvt_pk_bf16_f32 v153,v126,v127
v_cvt_pk_bf16_f32 v154,v128,v129
v_cvt_pk_bf16_f32 v155,v130,v131
v_pk_fma_f32 v[52:53],v[132:133],v[48:49],v[50:51]
v_pk_fma_f32 v[54:55],v[134:135],v[48:49],v[50:51]
v_pk_fma_f32 v[56:57],v[136:137],v[48:49],v[50:51]
v_pk_fma_f32 v[58:59],v[138:139],v[48:49],v[50:51]
v_pk_fma_f32 v[132:133],v[52:53],v[100:101],v[132:133]
v_pk_fma_f32 v[134:135],v[54:55],v[102:103],v[134:135]
v_pk_fma_f32 v[136:137],v[56:57],v[104:105],v[136:137]
v_pk_fma_f32 v[138:139],v[58:59],v[106:107],v[138:139]
v_cvt_pk_bf16_f32 v156,v132,v133
v_cvt_pk_bf16_f32 v157,v134,v135
v_cvt_pk_bf16_f32 v158,v136,v137
v_cvt_pk_bf16_f32 v159,v138,v139
v_pk_fma_f32 v[52:53],v[140:141],v[48:49],v[50:51]
v_pk_fma_f32 v[54:55],v[142:143],v[48:49],v[50:51]
v_pk_fma_f32 v[56:57],v[144:145],v[48:49],v[50:51]
v_pk_fma_f32 v[58:59],v[146:147],v[48:49],v[50:51]
v_pk_fma_f32 v[140:141],v[52:53],v[108:109],v[140:141]
v_pk_fma_f32 v[142:143],v[54:55],v[110:111],v[142:143]
v_pk_fma_f32 v[144:145],v[56:57],v[112:113],v[144:145]
v_pk_fma_f32 v[146:147],v[58:59],v[114:115],v[146:147]
v_cvt_pk_bf16_f32 v160,v140,v141
v_cvt_pk_bf16_f32 v161,v142,v143
v_cvt_pk_bf16_f32 v162,v144,v145
v_cvt_pk_bf16_f32 v163,v146,v147
s_waitcnt vmcnt(8)
buffer_store_dword v13,v5,s[36:39],s63 offen offset:0 sc0 sc1
s_mov_b32 s63,0x7fff0000
v_mfma_f32_16x16x32_bf16 v[36:39],v[32:35],v[148:151],0
buffer_store_dwordx4 v[148:151],v1,s[36:39],s57 offen offset:0 sc0 sc1
v_mfma_f32_16x16x32_bf16 v[40:43],v[148:151],v[148:151],0
M 36,v[32:35],152
buffer_store_dwordx4 v[152:155],v1,s[36:39],s57 offen offset:2048 sc0 sc1
M 40,v[152:155],152
M 36,v[32:35],156
buffer_store_dwordx4 v[156:159],v1,s[36:39],s58 offen offset:0 sc0 sc1
M 40,v[156:159],156
M 36,v[32:35],160
buffer_store_dwordx4 v[160:163],v1,s[36:39],s58 offen offset:2048 sc0 sc1
M 40,v[160:163],160
s_nop 5
v_add_f32_dpp v44,v36,v36 row_ror:8 row_mask:0xf bank_mask:0xf
s_nop 0
v_cndmask_b32 v45,v40,v41,s[70:71]
v_cndmask_b32 v46,v42,v43,s[70:71]
ds_write_b32 v2,v44 offset:1024
v_cndmask_b32 v45,v45,v46,s[72:73]
ds_write_b32 v0,v45 offset:1024
L 0
s_barrier
R 52,3,1024
R 56,3,1040
R 60,3,1056
L 0
v_pk_add_f32 v[52:53],v[52:53],v[54:55]
v_pk_add_f32 v[56:57],v[56:57],v[58:59]
v_pk_add_f32 v[60:61],v[60:61],v[62:63]
v_add_f32 v52,v52,v53
v_pk_add_f32 v[56:57],v[56:57],v[60:61]
v_mul_f32 v52,s68,v52
v_add_f32 v53,v56,v57
v_mul_f32 v53,s68,v53
v_fma_f32 v53,-v52,v52,v53
v_max_f32 v53,0,v53
v_add_f32 v54,s69,v53
v_fma_f32 v55,s69,v54,v53
v_rsq_f32 v65,v54
v_rsq_f32 v54,v55
v_mul_f32_e64 v64,-v52,v65
v_mul_f32 v63,v55,v54
v_mul_f32 v61,s76,v54
v_mul_f32 v55,v54,v54
v_mul_f32 v62,s77,v55
v_mul_f32 v62,v62,v61
v_cndmask_b32 v60,v52,v64,s[74:75]
v_cndmask_b32 v61,v61,v65,s[74:75]
buffer_store_dwordx4 v[60:63],v4,s[36:39],s59 offen offset:0 sc0 sc1
s_mov_b32 s63,s60
s_cmp_lt_u32 s45,s47
s_cbranch_scc1 Lr_a0_%=
Lr_mid_%=:
s_waitcnt vmcnt(0)
buffer_store_dword v13,v5,s[36:39],s63 offen offset:0 sc0 sc1
s_mov_b32 s63,0x7fff0000
s_cmp_eq_u32 s51,0
s_cbranch_scc1 Lr_done_%=
s_mov_b32 s45,s93
s_lshr_b32 s78,s45,1
s_and_b32 s79,s45,1
s_lshl_b32 s79,s79,7
s_lshl_b32 s55,s78,15
s_add_u32 s55,s55,s79
s_mul_i32 s56,s45,16384
s_add_u32 s56,s56,98566144
s_lshl_b32 s61,s78,11
s_add_u32 s61,s61,110587904
s_add_u32 s78,s45,s46
s_lshr_b32 s78,s78,1
s_lshl_b32 s62,s78,11
s_add_u32 s62,s62,110587904
Lr_bps_%=:
buffer_load_dword v14,v6,s[36:39],s61 offen offset:0 sc1
s_add_u32 s82,s82,1
s_waitcnt vmcnt(0)
v_cmp_eq_u32 vcc,s53,v14
s_nop 0
s_cmp_eq_u64 vcc,-1
s_cbranch_scc1 Lr_bqs_%=
s_sleep 2
s_cmp_lt_u32 s82,20000
s_cbranch_scc1 Lr_bps_%=
Lr_bqs_%=:
s_add_u32 s78,s55,71303168
buffer_load_dwordx4 v[164:167],v1,s[36:39],s78 offen offset:0 sc1
buffer_load_dwordx4 v[168:171],v1,s[36:39],s78 offen offset:2048 sc1
s_add_u32 s78,s55,71307264
buffer_load_dwordx4 v[172:175],v1,s[36:39],s78 offen offset:0 sc1
buffer_load_dwordx4 v[176:179],v1,s[36:39],s78 offen offset:2048 sc1
s_add_u32 s78,s55,89128960
buffer_load_dwordx4 v[180:183],v1,s[36:39],s78 offen offset:0 sc1
buffer_load_dwordx4 v[184:187],v1,s[36:39],s78 offen offset:2048 sc1
s_add_u32 s78,s55,89133056
buffer_load_dwordx4 v[188:191],v1,s[36:39],s78 offen offset:0 sc1
buffer_load_dwordx4 v[192:195],v1,s[36:39],s78 offen offset:2048 sc1
s_add_u32 m0,s65,0
s_nop 0
D 9,56,0
D 9,56,1024
D 9,56,2048
D 9,56,3072
buffer_load_dword v14,v6,s[36:39],s62 offen offset:0 sc1
s_lshr_b32 s78,s45,1
s_and_b32 s79,s45,1
s_lshl_b32 s79,s79,7
s_lshl_b32 s57,s78,15
s_add_u32 s57,s57,s79
s_add_u32 s57,s57,12582912
s_add_u32 s58,s57,4096
s_lshl_b32 s59,s78,9
s_add_u32 s59,s59,s79
s_add_u32 s59,s59,107020288
s_lshl_b32 s60,s45,9
s_add_u32 s60,s60,s66
s_add_u32 s60,s60,131072
s_add_u32 s45,s45,s46
s_lshr_b32 s78,s45,1
s_and_b32 s79,s45,1
s_lshl_b32 s79,s79,7
s_lshl_b32 s55,s78,15
s_add_u32 s55,s55,s79
s_mul_i32 s56,s45,16384
s_add_u32 s56,s56,98566144
s_lshl_b32 s61,s78,11
s_add_u32 s61,s61,110587904
s_add_u32 s78,s45,s46
s_lshr_b32 s78,s78,1
s_lshl_b32 s62,s78,11
s_add_u32 s62,s62,110587904
s_waitcnt vmcnt(0)
Lr_it0_%=:
T v[68:69],10,0
T v[70:71],10,64
T v[72:73],10,1024
T v[74:75],10,1088
T v[76:77],10,2048
T v[78:79],10,2112
T v[80:81],10,3072
T v[82:83],10,3136
s_nop 0
v_cmp_eq_u32 vcc,s53,v14
s_nop 0
s_cmp_eq_u64 vcc,-1
s_cselect_b32 s78,s45,256
s_cmp_lt_u32 s78,256
s_cbranch_scc1 Lr_ta0_%=
buffer_load_dword v14,v6,s[36:39],s61 offen offset:0 sc1
v_mfma_f32_16x16x32_bf16 v[116:119],v[26:29],v[164:167],0
L 6
v_lshlrev_b32 v84,16,v68
v_and_b32 v85,0xffff0000,v68
v_mfma_f32_16x16x32_bf16 v[120:123],v[24:27],v[164:167],0
L 4
v_lshlrev_b32 v86,16,v69
v_and_b32 v87,0xffff0000,v69
M 116,v[26:29],180
L 2
v_lshlrev_b32 v88,16,v70
v_and_b32 v89,0xffff0000,v70
M 120,v[24:27],180
L 0
v_lshlrev_b32 v90,16,v71
v_and_b32 v91,0xffff0000,v71
v_mfma_f32_16x16x32_bf16 v[124:127],v[26:29],v[168:171],0
v_lshlrev_b32 v92,16,v72
v_and_b32 v93,0xffff0000,v72
v_mfma_f32_16x16x32_bf16 v[128:131],v[24:27],v[168:171],0
v_lshlrev_b32 v94,16,v73
v_and_b32 v95,0xffff0000,v73
M 124,v[26:29],184
v_cvt_pk_bf16_f32 v148,v116,v117
v_cvt_pk_bf16_f32 v149,v118,v119
M 128,v[24:27],184
v_cvt_pk_bf16_f32 v150,v120,v121
v_cvt_pk_bf16_f32 v151,v122,v123
v_mfma_f32_16x16x32_bf16 v[132:135],v[26:29],v[172:175],0
v_lshlrev_b32 v96,16,v74
v_and_b32 v97,0xffff0000,v74
v_mfma_f32_16x16x32_bf16 v[136:139],v[24:27],v[172:175],0
v_lshlrev_b32 v98,16,v75
v_and_b32 v99,0xffff0000,v75
M 132,v[26:29],188
v_cvt_pk_bf16_f32 v152,v124,v125
v_cvt_pk_bf16_f32 v153,v126,v127
M 136,v[24:27],188
v_cvt_pk_bf16_f32 v154,v128,v129
v_cvt_pk_bf16_f32 v155,v130,v131
v_mfma_f32_16x16x32_bf16 v[140:143],v[26:29],v[176:179],0
v_lshlrev_b32 v100,16,v76
v_and_b32 v101,0xffff0000,v76
v_mfma_f32_16x16x32_bf16 v[144:147],v[24:27],v[176:179],0
v_lshlrev_b32 v102,16,v77
v_and_b32 v103,0xffff0000,v77
M 140,v[26:29],192
v_cvt_pk_bf16_f32 v156,v132,v133
v_cvt_pk_bf16_f32 v157,v134,v135
M 144,v[24:27],192
v_cvt_pk_bf16_f32 v158,v136,v137
v_cvt_pk_bf16_f32 v159,v138,v139
v_mfma_f32_16x16x32_bf16 v[36:39],v[32:35],v[148:151],0
v_lshlrev_b32 v104,16,v78
v_and_b32 v105,0xffff0000,v78
v_mfma_f32_16x16x32_bf16 v[40:43],v[148:151],v[148:151],0
v_lshlrev_b32 v106,16,v79
v_and_b32 v107,0xffff0000,v79
M 36,v[32:35],152
v_cvt_pk_bf16_f32 v160,v140,v141
v_cvt_pk_bf16_f32 v161,v142,v143
M 40,v[152:155],152
v_cvt_pk_bf16_f32 v162,v144,v145
v_cvt_pk_bf16_f32 v163,v146,v147
M 36,v[32:35],156
v_lshlrev_b32 v108,16,v80
v_and_b32 v109,0xffff0000,v80
M 40,v[156:159],156
v_lshlrev_b32 v110,16,v81
v_and_b32 v111,0xffff0000,v81
M 36,v[32:35],160
v_lshlrev_b32 v112,16,v82
v_and_b32 v113,0xffff0000,v82
M 40,v[160:163],160
v_lshlrev_b32 v114,16,v83
v_and_b32 v115,0xffff0000,v83
s_nop 2
v_add_f32_dpp v44,v36,v36 row_ror:8 row_mask:0xf bank_mask:0xf
s_nop 1
v_cndmask_b32 v45,v40,v41,s[70:71]
v_cndmask_b32 v46,v42,v43,s[70:71]
ds_write_b32 v2,v44 offset:0
v_cndmask_b32 v45,v45,v46,s[72:73]
ds_write_b32 v0,v45 offset:0
L 0
s_barrier
R 52,3,0
R 56,3,16
R 60,3,32
L 0
v_pk_add_f32 v[52:53],v[52:53],v[54:55]
v_pk_add_f32 v[56:57],v[56:57],v[58:59]
v_pk_add_f32 v[60:61],v[60:61],v[62:63]
v_add_f32 v52,v52,v53
v_pk_add_f32 v[56:57],v[56:57],v[60:61]
v_mul_f32 v52,s68,v52
v_add_f32 v53,v56,v57
v_mul_f32 v53,s68,v53
v_fma_f32 v53,-v52,v52,v53
v_max_f32 v53,0,v53
s_waitcnt vmcnt(1)
buffer_store_dword v13,v5,s[36:39],s63 offen offset:0 sc0 sc1
s_mov_b32 s63,0x7fff0000
s_waitcnt vmcnt(1)
v_cmp_eq_u32 vcc,s53,v14
s_nop 0
s_cmp_eq_u64 vcc,-1
s_cselect_b32 s78,s45,256
s_cmp_lt_u32 s78,256
s_cbranch_scc1 Lr_tb0_%=
buffer_load_dword v14,v6,s[36:39],s61 offen offset:0 sc1
v_add_f32 v53,s69,v53
v_rsq_f32 v48,v53
s_nop 0
v_mul_f32_e64 v50,-v52,v48
v_mov_b32 v49,v48
v_mov_b32 v51,v50
v_pk_fma_f32 v[52:53],v[116:117],v[48:49],v[50:51]
v_pk_fma_f32 v[54:55],v[118:119],v[48:49],v[50:51]
v_pk_fma_f32 v[56:57],v[120:121],v[48:49],v[50:51]
v_pk_fma_f32 v[58:59],v[122:123],v[48:49],v[50:51]
v_pk_fma_f32 v[116:117],v[52:53],v[84:85],v[116:117]
v_pk_fma_f32 v[118:119],v[54:55],v[86:87],v[118:119]
v_pk_fma_f32 v[120:121],v[56:57],v[88:89],v[120:121]
v_pk_fma_f32 v[122:123],v[58:59],v[90:91],v[122:123]
v_cvt_pk_bf16_f32 v148,v116,v117
v_cvt_pk_bf16_f32 v149,v118,v119
v_cvt_pk_bf16_f32 v150,v120,v121
v_cvt_pk_bf16_f32 v151,v122,v123
v_pk_fma_f32 v[52:53],v[124:125],v[48:49],v[50:51]
v_pk_fma_f32 v[54:55],v[126:127],v[48:49],v[50:51]
v_pk_fma_f32 v[56:57],v[128:129],v[48:49],v[50:51]
v_pk_fma_f32 v[58:59],v[130:131],v[48:49],v[50:51]
v_pk_fma_f32 v[124:125],v[52:53],v[92:93],v[124:125]
v_pk_fma_f32 v[126:127],v[54:55],v[94:95],v[126:127]
v_pk_fma_f32 v[128:129],v[56:57],v[96:97],v[128:129]
v_pk_fma_f32 v[130:131],v[58:59],v[98:99],v[130:131]
v_cvt_pk_bf16_f32 v152,v124,v125
v_cvt_pk_bf16_f32 v153,v126,v127
v_cvt_pk_bf16_f32 v154,v128,v129
v_cvt_pk_bf16_f32 v155,v130,v131
v_pk_fma_f32 v[52:53],v[132:133],v[48:49],v[50:51]
v_pk_fma_f32 v[54:55],v[134:135],v[48:49],v[50:51]
v_pk_fma_f32 v[56:57],v[136:137],v[48:49],v[50:51]
v_pk_fma_f32 v[58:59],v[138:139],v[48:49],v[50:51]
v_pk_fma_f32 v[132:133],v[52:53],v[100:101],v[132:133]
v_pk_fma_f32 v[134:135],v[54:55],v[102:103],v[134:135]
v_pk_fma_f32 v[136:137],v[56:57],v[104:105],v[136:137]
v_pk_fma_f32 v[138:139],v[58:59],v[106:107],v[138:139]
v_cvt_pk_bf16_f32 v156,v132,v133
v_cvt_pk_bf16_f32 v157,v134,v135
v_cvt_pk_bf16_f32 v158,v136,v137
v_cvt_pk_bf16_f32 v159,v138,v139
v_pk_fma_f32 v[52:53],v[140:141],v[48:49],v[50:51]
v_pk_fma_f32 v[54:55],v[142:143],v[48:49],v[50:51]
v_pk_fma_f32 v[56:57],v[144:145],v[48:49],v[50:51]
v_pk_fma_f32 v[58:59],v[146:147],v[48:49],v[50:51]
v_pk_fma_f32 v[140:141],v[52:53],v[108:109],v[140:141]
v_pk_fma_f32 v[142:143],v[54:55],v[110:111],v[142:143]
v_pk_fma_f32 v[144:145],v[56:57],v[112:113],v[144:145]
v_pk_fma_f32 v[146:147],v[58:59],v[114:115],v[146:147]
v_cvt_pk_bf16_f32 v160,v140,v141
v_cvt_pk_bf16_f32 v161,v142,v143
v_cvt_pk_bf16_f32 v162,v144,v145
v_cvt_pk_bf16_f32 v163,v146,v147
v_mfma_f32_16x16x32_bf16 v[36:39],v[32:35],v[148:151],0
buffer_store_dwordx4 v[148:151],v1,s[36:39],s57 offen offset:0 sc0 sc1
v_mfma_f32_16x16x32_bf16 v[40:43],v[148:151],v[148:151],0
M 36,v[32:35],152
buffer_store_dwordx4 v[152:155],v1,s[36:39],s57 offen offset:2048 sc0 sc1
M 40,v[152:155],152
M 36,v[32:35],156
buffer_store_dwordx4 v[156:159],v1,s[36:39],s58 offen offset:0 sc0 sc1
M 40,v[156:159],156
M 36,v[32:35],160
buffer_store_dwordx4 v[160:163],v1,s[36:39],s58 offen offset:2048 sc0 sc1
M 40,v[160:163],160
s_nop 5
v_add_f32_dpp v44,v36,v36 row_ror:8 row_mask:0xf bank_mask:0xf
s_nop 0
v_cndmask_b32 v45,v40,v41,s[70:71]
v_cndmask_b32 v46,v42,v43,s[70:71]
ds_write_b32 v2,v44 offset:1024
v_cndmask_b32 v45,v45,v46,s[72:73]
ds_write_b32 v0,v45 offset:1024
L 0
s_barrier
R 52,3,1024
R 56,3,1040
R 60,3,1056
L 0
v_pk_add_f32 v[52:53],v[52:53],v[54:55]
v_pk_add_f32 v[56:57],v[56:57],v[58:59]
v_pk_add_f32 v[60:61],v[60:61],v[62:63]
v_add_f32 v52,v52,v53
v_pk_add_f32 v[56:57],v[56:57],v[60:61]
v_mul_f32 v52,s68,v52
v_add_f32 v53,v56,v57
v_mul_f32 v53,s68,v53
v_fma_f32 v53,-v52,v52,v53
v_max_f32 v53,0,v53
v_add_f32 v54,s69,v53
v_fma_f32 v55,s69,v54,v53
v_rsq_f32 v65,v54
v_rsq_f32 v54,v55
v_mul_f32_e64 v64,-v52,v65
v_mul_f32 v63,v55,v54
v_mul_f32 v61,s76,v54
v_mul_f32 v55,v54,v54
v_mul_f32 v62,s77,v55
v_mul_f32 v62,v62,v61
v_cndmask_b32 v60,v52,v64,s[74:75]
v_cndmask_b32 v61,v61,v65,s[74:75]
buffer_store_dwordx4 v[60:63],v4,s[36:39],s59 offen offset:0 sc0 sc1
s_mov_b32 s63,s60
s_waitcnt vmcnt(5)
v_cmp_eq_u32 vcc,s53,v14
s_nop 0
s_cmp_eq_u64 vcc,-1
s_cselect_b32 s78,s45,256
s_cmp_lt_u32 s78,256
s_cbranch_scc1 Lr_tc0_%=
s_waitcnt vmcnt(0)
buffer_store_dword v13,v5,s[36:39],s63 offen offset:0 sc0 sc1
s_mov_b32 s63,0x7fff0000
s_cmp_ge_u32 s45,256
s_cbranch_scc1 Lr_done_%=
Lr_bp0_%=:
buffer_load_dword v14,v6,s[36:39],s61 offen offset:0 sc1
s_add_u32 s82,s82,1
s_waitcnt vmcnt(0)
v_cmp_eq_u32 vcc,s53,v14
s_nop 0
s_cmp_eq_u64 vcc,-1
s_cbranch_scc1 Lr_bq0_%=
s_sleep 2
s_cmp_lt_u32 s82,20000
s_cbranch_scc1 Lr_bp0_%=
Lr_bq0_%=:
s_add_u32 s78,s55,71303168
buffer_load_dwordx4 v[196:199],v1,s[36:39],s78 offen offset:0 sc1
buffer_load_dwordx4 v[200:203],v1,s[36:39],s78 offen offset:2048 sc1
s_add_u32 s78,s55,71307264
buffer_load_dwordx4 v[204:207],v1,s[36:39],s78 offen offset:0 sc1
buffer_load_dwordx4 v[208:211],v1,s[36:39],s78 offen offset:2048 sc1
s_add_u32 s78,s55,89128960
buffer_load_dwordx4 v[212:215],v1,s[36:39],s78 offen offset:0 sc1
buffer_load_dwordx4 v[216:219],v1,s[36:39],s78 offen offset:2048 sc1
s_add_u32 s78,s55,89133056
buffer_load_dwordx4 v[220:223],v1,s[36:39],s78 offen offset:0 sc1
buffer_load_dwordx4 v[224:227],v1,s[36:39],s78 offen offset:2048 sc1
s_add_u32 m0,s65,16384
s_nop 0
D 9,56,0
D 9,56,1024
D 9,56,2048
D 9,56,3072
buffer_load_dword v14,v6,s[36:39],s62 offen offset:0 sc1
s_lshr_b32 s78,s45,1
s_and_b32 s79,s45,1
s_lshl_b32 s79,s79,7
s_lshl_b32 s57,s78,15
s_add_u32 s57,s57,s79
s_add_u32 s57,s57,12582912
s_add_u32 s58,s57,4096
s_lshl_b32 s59,s78,9
s_add_u32 s59,s59,s79
s_add_u32 s59,s59,107020288
s_lshl_b32 s60,s45,9
s_add_u32 s60,s60,s66
s_add_u32 s60,s60,131072
s_add_u32 s45,s45,s46
s_lshr_b32 s78,s45,1
s_and_b32 s79,s45,1
s_lshl_b32 s79,s79,7
s_lshl_b32 s55,s78,15
s_add_u32 s55,s55,s79
s_mul_i32 s56,s45,16384
s_add_u32 s56,s56,98566144
s_lshl_b32 s61,s78,11
s_add_u32 s61,s61,110587904
s_add_u32 s78,s45,s46
s_lshr_b32 s78,s78,1
s_lshl_b32 s62,s78,11
s_add_u32 s62,s62,110587904
s_waitcnt vmcnt(0)
s_branch Lr_it1_%=
Lr_ta0_%=:
v_mfma_f32_16x16x32_bf16 v[116:119],v[26:29],v[164:167],0
s_add_u32 s78,s55,71303168
buffer_load_dwordx4 v[196:199],v1,s[36:39],s78 offen offset:0 sc1
v_mfma_f32_16x16x32_bf16 v[120:123],v[24:27],v[164:167],0
L 6
)"
R"(buffer_load_dwordx4 v[200:203],v1,s[36:39],s78 offen offset:2048 sc1
M 116,v[26:29],180
s_add_u32 s78,s55,71307264
buffer_load_dwordx4 v[204:207],v1,s[36:39],s78 offen offset:0 sc1
M 120,v[24:27],180
L 4
buffer_load_dwordx4 v[208:211],v1,s[36:39],s78 offen offset:2048 sc1
v_mfma_f32_16x16x32_bf16 v[124:127],v[26:29],v[168:171],0
s_add_u32 s78,s55,89128960
buffer_load_dwordx4 v[212:215],v1,s[36:39],s78 offen offset:0 sc1
v_mfma_f32_16x16x32_bf16 v[128:131],v[24:27],v[168:171],0
L 2
buffer_load_dwordx4 v[216:219],v1,s[36:39],s78 offen offset:2048 sc1
M 124,v[26:29],184
L 0
v_cvt_pk_bf16_f32 v148,v116,v117
v_cvt_pk_bf16_f32 v149,v118,v119
M 128,v[24:27],184
s_add_u32 s78,s55,89133056
buffer_load_dwordx4 v[220:223],v1,s[36:39],s78 offen offset:0 sc1
v_mfma_f32_16x16x32_bf16 v[132:135],v[26:29],v[172:175],0
s_add_u32 m0,s65,16384
buffer_load_dwordx4 v[224:227],v1,s[36:39],s78 offen offset:2048 sc1
v_mfma_f32_16x16x32_bf16 v[136:139],v[24:27],v[172:175],0
D 9,56,0
M 132,v[26:29],188
D 9,56,1024
M 136,v[24:27],188
D 9,56,2048
v_mfma_f32_16x16x32_bf16 v[140:143],v[26:29],v[176:179],0
D 9,56,3072
v_mfma_f32_16x16x32_bf16 v[144:147],v[24:27],v[176:179],0
v_cvt_pk_bf16_f32 v150,v120,v121
v_cvt_pk_bf16_f32 v151,v122,v123
M 140,v[26:29],192
v_cvt_pk_bf16_f32 v152,v124,v125
v_cvt_pk_bf16_f32 v153,v126,v127
M 144,v[24:27],192
v_cvt_pk_bf16_f32 v154,v128,v129
v_cvt_pk_bf16_f32 v155,v130,v131
v_mfma_f32_16x16x32_bf16 v[36:39],v[32:35],v[148:151],0
v_cvt_pk_bf16_f32 v156,v132,v133
v_cvt_pk_bf16_f32 v157,v134,v135
v_mfma_f32_16x16x32_bf16 v[40:43],v[148:151],v[148:151],0
v_cvt_pk_bf16_f32 v158,v136,v137
v_cvt_pk_bf16_f32 v159,v138,v139
M 36,v[32:35],152
v_cvt_pk_bf16_f32 v160,v140,v141
v_cvt_pk_bf16_f32 v161,v142,v143
M 40,v[152:155],152
v_cvt_pk_bf16_f32 v162,v144,v145
v_cvt_pk_bf16_f32 v163,v146,v147
M 36,v[32:35],156
v_lshlrev_b32 v84,16,v68
v_and_b32 v85,0xffff0000,v68
M 40,v[156:159],156
v_lshlrev_b32 v86,16,v69
v_and_b32 v87,0xffff0000,v69
M 36,v[32:35],160
v_lshlrev_b32 v88,16,v70
v_and_b32 v89,0xffff0000,v70
M 40,v[160:163],160
v_lshlrev_b32 v90,16,v71
v_and_b32 v91,0xffff0000,v71
v_lshlrev_b32 v92,16,v72
v_and_b32 v93,0xffff0000,v72
v_lshlrev_b32 v94,16,v73
v_and_b32 v95,0xffff0000,v73
v_lshlrev_b32 v96,16,v74
v_and_b32 v97,0xffff0000,v74
v_lshlrev_b32 v98,16,v75
v_and_b32 v99,0xffff0000,v75
v_lshlrev_b32 v100,16,v76
v_and_b32 v101,0xffff0000,v76
v_lshlrev_b32 v102,16,v77
v_and_b32 v103,0xffff0000,v77
v_lshlrev_b32 v104,16,v78
v_and_b32 v105,0xffff0000,v78
v_lshlrev_b32 v106,16,v79
v_and_b32 v107,0xffff0000,v79
v_lshlrev_b32 v108,16,v80
v_and_b32 v109,0xffff0000,v80
v_lshlrev_b32 v110,16,v81
v_and_b32 v111,0xffff0000,v81
v_lshlrev_b32 v112,16,v82
v_and_b32 v113,0xffff0000,v82
v_lshlrev_b32 v114,16,v83
v_and_b32 v115,0xffff0000,v83
v_add_f32_dpp v44,v36,v36 row_ror:8 row_mask:0xf bank_mask:0xf
v_cndmask_b32 v45,v40,v41,s[70:71]
v_cndmask_b32 v46,v42,v43,s[70:71]
ds_write_b32 v2,v44 offset:0
v_cndmask_b32 v45,v45,v46,s[72:73]
ds_write_b32 v0,v45 offset:0
L 0
s_barrier
R 52,3,0
R 56,3,16
R 60,3,32
L 0
v_pk_add_f32 v[52:53],v[52:53],v[54:55]
v_pk_add_f32 v[56:57],v[56:57],v[58:59]
v_pk_add_f32 v[60:61],v[60:61],v[62:63]
v_add_f32 v52,v52,v53
v_pk_add_f32 v[56:57],v[56:57],v[60:61]
v_mul_f32 v52,s68,v52
v_add_f32 v53,v56,v57
v_mul_f32 v53,s68,v53
v_fma_f32 v53,-v52,v52,v53
v_max_f32 v53,0,v53
buffer_load_dword v14,v6,s[36:39],s62 offen offset:0 sc1
v_add_f32 v53,s69,v53
v_rsq_f32 v48,v53
s_nop 0
v_mul_f32_e64 v50,-v52,v48
v_mov_b32 v49,v48
v_mov_b32 v51,v50
v_pk_fma_f32 v[52:53],v[116:117],v[48:49],v[50:51]
v_pk_fma_f32 v[54:55],v[118:119],v[48:49],v[50:51]
v_pk_fma_f32 v[56:57],v[120:121],v[48:49],v[50:51]
v_pk_fma_f32 v[58:59],v[122:123],v[48:49],v[50:51]
v_pk_fma_f32 v[116:117],v[52:53],v[84:85],v[116:117]
v_pk_fma_f32 v[118:119],v[54:55],v[86:87],v[118:119]
v_pk_fma_f32 v[120:121],v[56:57],v[88:89],v[120:121]
v_pk_fma_f32 v[122:123],v[58:59],v[90:91],v[122:123]
v_cvt_pk_bf16_f32 v148,v116,v117
v_cvt_pk_bf16_f32 v149,v118,v119
v_cvt_pk_bf16_f32 v150,v120,v121
v_cvt_pk_bf16_f32 v151,v122,v123
v_pk_fma_f32 v[52:53],v[124:125],v[48:49],v[50:51]
v_pk_fma_f32 v[54:55],v[126:127],v[48:49],v[50:51]
v_pk_fma_f32 v[56:57],v[128:129],v[48:49],v[50:51]
v_pk_fma_f32 v[58:59],v[130:131],v[48:49],v[50:51]
v_pk_fma_f32 v[124:125],v[52:53],v[92:93],v[124:125]
v_pk_fma_f32 v[126:127],v[54:55],v[94:95],v[126:127]
v_pk_fma_f32 v[128:129],v[56:57],v[96:97],v[128:129]
v_pk_fma_f32 v[130:131],v[58:59],v[98:99],v[130:131]
v_cvt_pk_bf16_f32 v152,v124,v125
v_cvt_pk_bf16_f32 v153,v126,v127
v_cvt_pk_bf16_f32 v154,v128,v129
v_cvt_pk_bf16_f32 v155,v130,v131
v_pk_fma_f32 v[52:53],v[132:133],v[48:49],v[50:51]
v_pk_fma_f32 v[54:55],v[134:135],v[48:49],v[50:51]
v_pk_fma_f32 v[56:57],v[136:137],v[48:49],v[50:51]
v_pk_fma_f32 v[58:59],v[138:139],v[48:49],v[50:51]
v_pk_fma_f32 v[132:133],v[52:53],v[100:101],v[132:133]
v_pk_fma_f32 v[134:135],v[54:55],v[102:103],v[134:135]
v_pk_fma_f32 v[136:137],v[56:57],v[104:105],v[136:137]
v_pk_fma_f32 v[138:139],v[58:59],v[106:107],v[138:139]
v_cvt_pk_bf16_f32 v156,v132,v133
v_cvt_pk_bf16_f32 v157,v134,v135
v_cvt_pk_bf16_f32 v158,v136,v137
v_cvt_pk_bf16_f32 v159,v138,v139
v_pk_fma_f32 v[52:53],v[140:141],v[48:49],v[50:51]
v_pk_fma_f32 v[54:55],v[142:143],v[48:49],v[50:51]
v_pk_fma_f32 v[56:57],v[144:145],v[48:49],v[50:51]
v_pk_fma_f32 v[58:59],v[146:147],v[48:49],v[50:51]
v_pk_fma_f32 v[140:141],v[52:53],v[108:109],v[140:141]
v_pk_fma_f32 v[142:143],v[54:55],v[110:111],v[142:143]
v_pk_fma_f32 v[144:145],v[56:57],v[112:113],v[144:145]
v_pk_fma_f32 v[146:147],v[58:59],v[114:115],v[146:147]
v_cvt_pk_bf16_f32 v160,v140,v141
v_cvt_pk_bf16_f32 v161,v142,v143
v_cvt_pk_bf16_f32 v162,v144,v145
v_cvt_pk_bf16_f32 v163,v146,v147
s_waitcnt vmcnt(13)
buffer_store_dword v13,v5,s[36:39],s63 offen offset:0 sc0 sc1
s_mov_b32 s63,0x7fff0000
v_mfma_f32_16x16x32_bf16 v[36:39],v[32:35],v[148:151],0
buffer_store_dwordx4 v[148:151],v1,s[36:39],s57 offen offset:0 sc0 sc1
v_mfma_f32_16x16x32_bf16 v[40:43],v[148:151],v[148:151],0
M 36,v[32:35],152
buffer_store_dwordx4 v[152:155],v1,s[36:39],s57 offen offset:2048 sc0 sc1
M 40,v[152:155],152
M 36,v[32:35],156
buffer_store_dwordx4 v[156:159],v1,s[36:39],s58 offen offset:0 sc0 sc1
M 40,v[156:159],156
M 36,v[32:35],160
buffer_store_dwordx4 v[160:163],v1,s[36:39],s58 offen offset:2048 sc0 sc1
M 40,v[160:163],160
s_nop 5
v_add_f32_dpp v44,v36,v36 row_ror:8 row_mask:0xf bank_mask:0xf
s_nop 0
v_cndmask_b32 v45,v40,v41,s[70:71]
v_cndmask_b32 v46,v42,v43,s[70:71]
ds_write_b32 v2,v44 offset:1024
v_cndmask_b32 v45,v45,v46,s[72:73]
ds_write_b32 v0,v45 offset:1024
L 0
s_barrier
R 52,3,1024
R 56,3,1040
R 60,3,1056
L 0
v_pk_add_f32 v[52:53],v[52:53],v[54:55]
v_pk_add_f32 v[56:57],v[56:57],v[58:59]
v_pk_add_f32 v[60:61],v[60:61],v[62:63]
v_add_f32 v52,v52,v53
v_pk_add_f32 v[56:57],v[56:57],v[60:61]
v_mul_f32 v52,s68,v52
v_add_f32 v53,v56,v57
v_mul_f32 v53,s68,v53
v_fma_f32 v53,-v52,v52,v53
v_max_f32 v53,0,v53
v_add_f32 v54,s69,v53
v_fma_f32 v55,s69,v54,v53
v_rsq_f32 v65,v54
v_rsq_f32 v54,v55
v_mul_f32_e64 v64,-v52,v65
v_mul_f32 v63,v55,v54
v_mul_f32 v61,s76,v54
v_mul_f32 v55,v54,v54
v_mul_f32 v62,s77,v55
v_mul_f32 v62,v62,v61
v_cndmask_b32 v60,v52,v64,s[74:75]
v_cndmask_b32 v61,v61,v65,s[74:75]
buffer_store_dwordx4 v[60:63],v4,s[36:39],s59 offen offset:0 sc0 sc1
s_mov_b32 s63,s60
s_lshr_b32 s78,s45,1
s_and_b32 s79,s45,1
s_lshl_b32 s79,s79,7
s_lshl_b32 s57,s78,15
s_add_u32 s57,s57,s79
s_add_u32 s57,s57,12582912
s_add_u32 s58,s57,4096
s_lshl_b32 s59,s78,9
s_add_u32 s59,s59,s79
s_add_u32 s59,s59,107020288
s_lshl_b32 s60,s45,9
s_add_u32 s60,s60,s66
s_add_u32 s60,s60,131072
s_add_u32 s45,s45,s46
s_lshr_b32 s78,s45,1
s_and_b32 s79,s45,1
s_lshl_b32 s79,s79,7
s_lshl_b32 s55,s78,15
s_add_u32 s55,s55,s79
s_mul_i32 s56,s45,16384
s_add_u32 s56,s56,98566144
s_lshl_b32 s61,s78,11
s_add_u32 s61,s61,110587904
s_add_u32 s78,s45,s46
s_lshr_b32 s78,s78,1
s_lshl_b32 s62,s78,11
s_add_u32 s62,s62,110587904
s_waitcnt vmcnt(6)
s_branch Lr_it1_%=
Lr_tb0_%=:
v_add_f32 v53,s69,v53
v_rsq_f32 v48,v53
s_nop 0
v_mul_f32_e64 v50,-v52,v48
v_mov_b32 v49,v48
v_mov_b32 v51,v50
v_pk_fma_f32 v[52:53],v[116:117],v[48:49],v[50:51]
v_pk_fma_f32 v[54:55],v[118:119],v[48:49],v[50:51]
v_pk_fma_f32 v[56:57],v[120:121],v[48:49],v[50:51]
v_pk_fma_f32 v[58:59],v[122:123],v[48:49],v[50:51]
s_add_u32 s78,s55,71303168
buffer_load_dwordx4 v[196:199],v1,s[36:39],s78 offen offset:0 sc1
buffer_load_dwordx4 v[200:203],v1,s[36:39],s78 offen offset:2048 sc1
s_add_u32 s78,s55,71307264
buffer_load_dwordx4 v[204:207],v1,s[36:39],s78 offen offset:0 sc1
buffer_load_dwordx4 v[208:211],v1,s[36:39],s78 offen offset:2048 sc1
v_pk_fma_f32 v[116:117],v[52:53],v[84:85],v[116:117]
v_pk_fma_f32 v[118:119],v[54:55],v[86:87],v[118:119]
v_pk_fma_f32 v[120:121],v[56:57],v[88:89],v[120:121]
v_pk_fma_f32 v[122:123],v[58:59],v[90:91],v[122:123]
v_cvt_pk_bf16_f32 v148,v116,v117
v_cvt_pk_bf16_f32 v149,v118,v119
v_cvt_pk_bf16_f32 v150,v120,v121
v_cvt_pk_bf16_f32 v151,v122,v123
v_pk_fma_f32 v[52:53],v[124:125],v[48:49],v[50:51]
v_pk_fma_f32 v[54:55],v[126:127],v[48:49],v[50:51]
v_pk_fma_f32 v[56:57],v[128:129],v[48:49],v[50:51]
v_pk_fma_f32 v[58:59],v[130:131],v[48:49],v[50:51]
s_add_u32 s78,s55,89128960
buffer_load_dwordx4 v[212:215],v1,s[36:39],s78 offen offset:0 sc1
buffer_load_dwordx4 v[216:219],v1,s[36:39],s78 offen offset:2048 sc1
s_add_u32 s78,s55,89133056
buffer_load_dwordx4 v[220:223],v1,s[36:39],s78 offen offset:0 sc1
buffer_load_dwordx4 v[224:227],v1,s[36:39],s78 offen offset:2048 sc1
v_pk_fma_f32 v[124:125],v[52:53],v[92:93],v[124:125]
v_pk_fma_f32 v[126:127],v[54:55],v[94:95],v[126:127]
v_pk_fma_f32 v[128:129],v[56:57],v[96:97],v[128:129]
v_pk_fma_f32 v[130:131],v[58:59],v[98:99],v[130:131]
v_cvt_pk_bf16_f32 v152,v124,v125
v_cvt_pk_bf16_f32 v153,v126,v127
v_cvt_pk_bf16_f32 v154,v128,v129
v_cvt_pk_bf16_f32 v155,v130,v131
v_pk_fma_f32 v[52:53],v[132:133],v[48:49],v[50:51]
v_pk_fma_f32 v[54:55],v[134:135],v[48:49],v[50:51]
v_pk_fma_f32 v[56:57],v[136:137],v[48:49],v[50:51]
v_pk_fma_f32 v[58:59],v[138:139],v[48:49],v[50:51]
s_add_u32 m0,s65,16384
s_nop 0
D 9,56,0
D 9,56,1024
D 9,56,2048
D 9,56,3072
v_pk_fma_f32 v[132:133],v[52:53],v[100:101],v[132:133]
v_pk_fma_f32 v[134:135],v[54:55],v[102:103],v[134:135]
v_pk_fma_f32 v[136:137],v[56:57],v[104:105],v[136:137]
v_pk_fma_f32 v[138:139],v[58:59],v[106:107],v[138:139]
v_cvt_pk_bf16_f32 v156,v132,v133
v_cvt_pk_bf16_f32 v157,v134,v135
v_cvt_pk_bf16_f32 v158,v136,v137
v_cvt_pk_bf16_f32 v159,v138,v139
v_pk_fma_f32 v[52:53],v[140:141],v[48:49],v[50:51]
v_pk_fma_f32 v[54:55],v[142:143],v[48:49],v[50:51]
v_pk_fma_f32 v[56:57],v[144:145],v[48:49],v[50:51]
v_pk_fma_f32 v[58:59],v[146:147],v[48:49],v[50:51]
v_pk_fma_f32 v[140:141],v[52:53],v[108:109],v[140:141]
v_pk_fma_f32 v[142:143],v[54:55],v[110:111],v[142:143]
v_pk_fma_f32 v[144:145],v[56:57],v[112:113],v[144:145]
v_pk_fma_f32 v[146:147],v[58:59],v[114:115],v[146:147]
v_cvt_pk_bf16_f32 v160,v140,v141
v_cvt_pk_bf16_f32 v161,v142,v143
v_cvt_pk_bf16_f32 v162,v144,v145
v_cvt_pk_bf16_f32 v163,v146,v147
v_mfma_f32_16x16x32_bf16 v[36:39],v[32:35],v[148:151],0
buffer_load_dword v14,v6,s[36:39],s62 offen offset:0 sc1
buffer_store_dwordx4 v[148:151],v1,s[36:39],s57 offen offset:0 sc0 sc1
v_mfma_f32_16x16x32_bf16 v[40:43],v[148:151],v[148:151],0
M 36,v[32:35],152
buffer_store_dwordx4 v[152:155],v1,s[36:39],s57 offen offset:2048 sc0 sc1
M 40,v[152:155],152
M 36,v[32:35],156
buffer_store_dwordx4 v[156:159],v1,s[36:39],s58 offen offset:0 sc0 sc1
M 40,v[156:159],156
M 36,v[32:35],160
buffer_store_dwordx4 v[160:163],v1,s[36:39],s58 offen offset:2048 sc0 sc1
M 40,v[160:163],160
s_nop 5
v_add_f32_dpp v44,v36,v36 row_ror:8 row_mask:0xf bank_mask:0xf
s_nop 0
v_cndmask_b32 v45,v40,v41,s[70:71]
v_cndmask_b32 v46,v42,v43,s[70:71]
ds_write_b32 v2,v44 offset:1024
v_cndmask_b32 v45,v45,v46,s[72:73]
ds_write_b32 v0,v45 offset:1024
L 0
s_barrier
R 52,3,1024
R 56,3,1040
R 60,3,1056
L 0
v_pk_add_f32 v[52:53],v[52:53],v[54:55]
v_pk_add_f32 v[56:57],v[56:57],v[58:59]
v_pk_add_f32 v[60:61],v[60:61],v[62:63]
v_add_f32 v52,v52,v53
v_pk_add_f32 v[56:57],v[56:57],v[60:61]
v_mul_f32 v52,s68,v52
v_add_f32 v53,v56,v57
v_mul_f32 v53,s68,v53
v_fma_f32 v53,-v52,v52,v53
v_max_f32 v53,0,v53
v_add_f32 v54,s69,v53
v_fma_f32 v55,s69,v54,v53
v_rsq_f32 v65,v54
v_rsq_f32 v54,v55
v_mul_f32_e64 v64,-v52,v65
v_mul_f32 v63,v55,v54
v_mul_f32 v61,s76,v54
v_mul_f32 v55,v54,v54
v_mul_f32 v62,s77,v55
v_mul_f32 v62,v62,v61
v_cndmask_b32 v60,v52,v64,s[74:75]
v_cndmask_b32 v61,v61,v65,s[74:75]
buffer_store_dwordx4 v[60:63],v4,s[36:39],s59 offen offset:0 sc0 sc1
s_mov_b32 s63,s60
s_lshr_b32 s78,s45,1
s_and_b32 s79,s45,1
s_lshl_b32 s79,s79,7
s_lshl_b32 s57,s78,15
s_add_u32 s57,s57,s79
s_add_u32 s57,s57,12582912
s_add_u32 s58,s57,4096
s_lshl_b32 s59,s78,9
s_add_u32 s59,s59,s79
s_add_u32 s59,s59,107020288
s_lshl_b32 s60,s45,9
s_add_u32 s60,s60,s66
s_add_u32 s60,s60,131072
s_add_u32 s45,s45,s46
s_lshr_b32 s78,s45,1
s_and_b32 s79,s45,1
s_lshl_b32 s79,s79,7
s_lshl_b32 s55,s78,15
s_add_u32 s55,s55,s79
s_mul_i32 s56,s45,16384
s_add_u32 s56,s56,98566144
s_lshl_b32 s61,s78,11
s_add_u32 s61,s61,110587904
s_add_u32 s78,s45,s46
s_lshr_b32 s78,s78,1
s_lshl_b32 s62,s78,11
s_add_u32 s62,s62,110587904
s_waitcnt vmcnt(5)
s_branch Lr_it1_%=
Lr_tc0_%=:
s_add_u32 s78,s55,71303168
buffer_load_dwordx4 v[196:199],v1,s[36:39],s78 offen offset:0 sc1
buffer_load_dwordx4 v[200:203],v1,s[36:39],s78 offen offset:2048 sc1
s_add_u32 s78,s55,71307264
buffer_load_dwordx4 v[204:207],v1,s[36:39],s78 offen offset:0 sc1
buffer_load_dwordx4 v[208:211],v1,s[36:39],s78 offen offset:2048 sc1
s_add_u32 s78,s55,89128960
buffer_load_dwordx4 v[212:215],v1,s[36:39],s78 offen offset:0 sc1
buffer_load_dwordx4 v[216:219],v1,s[36:39],s78 offen offset:2048 sc1
s_add_u32 s78,s55,89133056
buffer_load_dwordx4 v[220:223],v1,s[36:39],s78 offen offset:0 sc1
buffer_load_dwordx4 v[224:227],v1,s[36:39],s78 offen offset:2048 sc1
s_add_u32 m0,s65,16384
s_nop 0
D 9,56,0
D 9,56,1024
D 9,56,2048
D 9,56,3072
buffer_load_dword v14,v6,s[36:39],s62 offen offset:0 sc1
s_waitcnt vmcnt(13)
buffer_store_dword v13,v5,s[36:39],s63 offen offset:0 sc0 sc1
s_mov_b32 s63,0x7fff0000
s_lshr_b32 s78,s45,1
s_and_b32 s79,s45,1
s_lshl_b32 s79,s79,7
s_lshl_b32 s57,s78,15
s_add_u32 s57,s57,s79
s_add_u32 s57,s57,12582912
s_add_u32 s58,s57,4096
s_lshl_b32 s59,s78,9
s_add_u32 s59,s59,s79
s_add_u32 s59,s59,107020288
s_lshl_b32 s60,s45,9
s_add_u32 s60,s60,s66
s_add_u32 s60,s60,131072
s_add_u32 s45,s45,s46
s_lshr_b32 s78,s45,1
s_and_b32 s79,s45,1
s_lshl_b32 s79,s79,7
s_lshl_b32 s55,s78,15
s_add_u32 s55,s55,s79
s_mul_i32 s56,s45,16384
s_add_u32 s56,s56,98566144
s_lshl_b32 s61,s78,11
s_add_u32 s61,s61,110587904
s_add_u32 s78,s45,s46
s_lshr_b32 s78,s78,1
s_lshl_b32 s62,s78,11
s_add_u32 s62,s62,110587904
s_waitcnt vmcnt(1)
s_branch Lr_it1_%=
Lr_it1_%=:
T v[68:69],10,16384
T v[70:71],10,16448
T v[72:73],10,17408
T v[74:75],10,17472
T v[76:77],10,18432
T v[78:79],10,18496
T v[80:81],10,19456
T v[82:83],10,19520
s_nop 0
v_cmp_eq_u32 vcc,s53,v14
s_nop 0
s_cmp_eq_u64 vcc,-1
s_cselect_b32 s78,s45,256
s_cmp_lt_u32 s78,256
s_cbranch_scc1 Lr_ta1_%=
buffer_load_dword v14,v6,s[36:39],s61 offen offset:0 sc1
v_mfma_f32_16x16x32_bf16 v[116:119],v[26:29],v[196:199],0
L 6
v_lshlrev_b32 v84,16,v68
v_and_b32 v85,0xffff0000,v68
v_mfma_f32_16x16x32_bf16 v[120:123],v[24:27],v[196:199],0
L 4
v_lshlrev_b32 v86,16,v69
v_and_b32 v87,0xffff0000,v69
M 116,v[26:29],212
L 2
v_lshlrev_b32 v88,16,v70
v_and_b32 v89,0xffff0000,v70
M 120,v[24:27],212
L 0
v_lshlrev_b32 v90,16,v71
v_and_b32 v91,0xffff0000,v71
v_mfma_f32_16x16x32_bf16 v[124:127],v[26:29],v[200:203],0
v_lshlrev_b32 v92,16,v72
v_and_b32 v93,0xffff0000,v72
v_mfma_f32_16x16x32_bf16 v[128:131],v[24:27],v[200:203],0
v_lshlrev_b32 v94,16,v73
v_and_b32 v95,0xffff0000,v73
M 124,v[26:29],216
v_cvt_pk_bf16_f32 v148,v116,v117
v_cvt_pk_bf16_f32 v149,v118,v119
M 128,v[24:27],216
v_cvt_pk_bf16_f32 v150,v120,v121
v_cvt_pk_bf16_f32 v151,v122,v123
v_mfma_f32_16x16x32_bf16 v[132:135],v[26:29],v[204:207],0
v_lshlrev_b32 v96,16,v74
v_and_b32 v97,0xffff0000,v74
)"
R"(v_mfma_f32_16x16x32_bf16 v[136:139],v[24:27],v[204:207],0
v_lshlrev_b32 v98,16,v75
v_and_b32 v99,0xffff0000,v75
M 132,v[26:29],220
v_cvt_pk_bf16_f32 v152,v124,v125
v_cvt_pk_bf16_f32 v153,v126,v127
M 136,v[24:27],220
v_cvt_pk_bf16_f32 v154,v128,v129
v_cvt_pk_bf16_f32 v155,v130,v131
v_mfma_f32_16x16x32_bf16 v[140:143],v[26:29],v[208:211],0
v_lshlrev_b32 v100,16,v76
v_and_b32 v101,0xffff0000,v76
v_mfma_f32_16x16x32_bf16 v[144:147],v[24:27],v[208:211],0
v_lshlrev_b32 v102,16,v77
v_and_b32 v103,0xffff0000,v77
M 140,v[26:29],224
v_cvt_pk_bf16_f32 v156,v132,v133
v_cvt_pk_bf16_f32 v157,v134,v135
M 144,v[24:27],224
v_cvt_pk_bf16_f32 v158,v136,v137
v_cvt_pk_bf16_f32 v159,v138,v139
v_mfma_f32_16x16x32_bf16 v[36:39],v[32:35],v[148:151],0
v_lshlrev_b32 v104,16,v78
v_and_b32 v105,0xffff0000,v78
v_mfma_f32_16x16x32_bf16 v[40:43],v[148:151],v[148:151],0
v_lshlrev_b32 v106,16,v79
v_and_b32 v107,0xffff0000,v79
M 36,v[32:35],152
v_cvt_pk_bf16_f32 v160,v140,v141
v_cvt_pk_bf16_f32 v161,v142,v143
M 40,v[152:155],152
v_cvt_pk_bf16_f32 v162,v144,v145
v_cvt_pk_bf16_f32 v163,v146,v147
M 36,v[32:35],156
v_lshlrev_b32 v108,16,v80
v_and_b32 v109,0xffff0000,v80
M 40,v[156:159],156
v_lshlrev_b32 v110,16,v81
v_and_b32 v111,0xffff0000,v81
M 36,v[32:35],160
v_lshlrev_b32 v112,16,v82
v_and_b32 v113,0xffff0000,v82
M 40,v[160:163],160
v_lshlrev_b32 v114,16,v83
v_and_b32 v115,0xffff0000,v83
s_nop 2
v_add_f32_dpp v44,v36,v36 row_ror:8 row_mask:0xf bank_mask:0xf
s_nop 1
v_cndmask_b32 v45,v40,v41,s[70:71]
v_cndmask_b32 v46,v42,v43,s[70:71]
ds_write_b32 v2,v44 offset:0
v_cndmask_b32 v45,v45,v46,s[72:73]
ds_write_b32 v0,v45 offset:0
L 0
s_barrier
R 52,3,0
R 56,3,16
R 60,3,32
L 0
v_pk_add_f32 v[52:53],v[52:53],v[54:55]
v_pk_add_f32 v[56:57],v[56:57],v[58:59]
v_pk_add_f32 v[60:61],v[60:61],v[62:63]
v_add_f32 v52,v52,v53
v_pk_add_f32 v[56:57],v[56:57],v[60:61]
v_mul_f32 v52,s68,v52
v_add_f32 v53,v56,v57
v_mul_f32 v53,s68,v53
v_fma_f32 v53,-v52,v52,v53
v_max_f32 v53,0,v53
s_waitcnt vmcnt(1)
buffer_store_dword v13,v5,s[36:39],s63 offen offset:0 sc0 sc1
s_mov_b32 s63,0x7fff0000
s_waitcnt vmcnt(1)
v_cmp_eq_u32 vcc,s53,v14
s_nop 0
s_cmp_eq_u64 vcc,-1
s_cselect_b32 s78,s45,256
s_cmp_lt_u32 s78,256
s_cbranch_scc1 Lr_tb1_%=
buffer_load_dword v14,v6,s[36:39],s61 offen offset:0 sc1
v_add_f32 v53,s69,v53
v_rsq_f32 v48,v53
s_nop 0
v_mul_f32_e64 v50,-v52,v48
v_mov_b32 v49,v48
v_mov_b32 v51,v50
v_pk_fma_f32 v[52:53],v[116:117],v[48:49],v[50:51]
v_pk_fma_f32 v[54:55],v[118:119],v[48:49],v[50:51]
v_pk_fma_f32 v[56:57],v[120:121],v[48:49],v[50:51]
v_pk_fma_f32 v[58:59],v[122:123],v[48:49],v[50:51]
v_pk_fma_f32 v[116:117],v[52:53],v[84:85],v[116:117]
v_pk_fma_f32 v[118:119],v[54:55],v[86:87],v[118:119]
v_pk_fma_f32 v[120:121],v[56:57],v[88:89],v[120:121]
v_pk_fma_f32 v[122:123],v[58:59],v[90:91],v[122:123]
v_cvt_pk_bf16_f32 v148,v116,v117
v_cvt_pk_bf16_f32 v149,v118,v119
v_cvt_pk_bf16_f32 v150,v120,v121
v_cvt_pk_bf16_f32 v151,v122,v123
v_pk_fma_f32 v[52:53],v[124:125],v[48:49],v[50:51]
v_pk_fma_f32 v[54:55],v[126:127],v[48:49],v[50:51]
v_pk_fma_f32 v[56:57],v[128:129],v[48:49],v[50:51]
v_pk_fma_f32 v[58:59],v[130:131],v[48:49],v[50:51]
v_pk_fma_f32 v[124:125],v[52:53],v[92:93],v[124:125]
v_pk_fma_f32 v[126:127],v[54:55],v[94:95],v[126:127]
v_pk_fma_f32 v[128:129],v[56:57],v[96:97],v[128:129]
v_pk_fma_f32 v[130:131],v[58:59],v[98:99],v[130:131]
v_cvt_pk_bf16_f32 v152,v124,v125
v_cvt_pk_bf16_f32 v153,v126,v127
v_cvt_pk_bf16_f32 v154,v128,v129
v_cvt_pk_bf16_f32 v155,v130,v131
v_pk_fma_f32 v[52:53],v[132:133],v[48:49],v[50:51]
v_pk_fma_f32 v[54:55],v[134:135],v[48:49],v[50:51]
v_pk_fma_f32 v[56:57],v[136:137],v[48:49],v[50:51]
v_pk_fma_f32 v[58:59],v[138:139],v[48:49],v[50:51]
v_pk_fma_f32 v[132:133],v[52:53],v[100:101],v[132:133]
v_pk_fma_f32 v[134:135],v[54:55],v[102:103],v[134:135]
v_pk_fma_f32 v[136:137],v[56:57],v[104:105],v[136:137]
v_pk_fma_f32 v[138:139],v[58:59],v[106:107],v[138:139]
v_cvt_pk_bf16_f32 v156,v132,v133
v_cvt_pk_bf16_f32 v157,v134,v135
v_cvt_pk_bf16_f32 v158,v136,v137
v_cvt_pk_bf16_f32 v159,v138,v139
v_pk_fma_f32 v[52:53],v[140:141],v[48:49],v[50:51]
v_pk_fma_f32 v[54:55],v[142:143],v[48:49],v[50:51]
v_pk_fma_f32 v[56:57],v[144:145],v[48:49],v[50:51]
v_pk_fma_f32 v[58:59],v[146:147],v[48:49],v[50:51]
v_pk_fma_f32 v[140:141],v[52:53],v[108:109],v[140:141]
v_pk_fma_f32 v[142:143],v[54:55],v[110:111],v[142:143]
v_pk_fma_f32 v[144:145],v[56:57],v[112:113],v[144:145]
v_pk_fma_f32 v[146:147],v[58:59],v[114:115],v[146:147]
v_cvt_pk_bf16_f32 v160,v140,v141
v_cvt_pk_bf16_f32 v161,v142,v143
v_cvt_pk_bf16_f32 v162,v144,v145
v_cvt_pk_bf16_f32 v163,v146,v147
v_mfma_f32_16x16x32_bf16 v[36:39],v[32:35],v[148:151],0
buffer_store_dwordx4 v[148:151],v1,s[36:39],s57 offen offset:0 sc0 sc1
v_mfma_f32_16x16x32_bf16 v[40:43],v[148:151],v[148:151],0
M 36,v[32:35],152
buffer_store_dwordx4 v[152:155],v1,s[36:39],s57 offen offset:2048 sc0 sc1
M 40,v[152:155],152
M 36,v[32:35],156
buffer_store_dwordx4 v[156:159],v1,s[36:39],s58 offen offset:0 sc0 sc1
M 40,v[156:159],156
M 36,v[32:35],160
buffer_store_dwordx4 v[160:163],v1,s[36:39],s58 offen offset:2048 sc0 sc1
M 40,v[160:163],160
s_nop 5
v_add_f32_dpp v44,v36,v36 row_ror:8 row_mask:0xf bank_mask:0xf
s_nop 0
v_cndmask_b32 v45,v40,v41,s[70:71]
v_cndmask_b32 v46,v42,v43,s[70:71]
ds_write_b32 v2,v44 offset:1024
v_cndmask_b32 v45,v45,v46,s[72:73]
ds_write_b32 v0,v45 offset:1024
L 0
s_barrier
R 52,3,1024
R 56,3,1040
R 60,3,1056
L 0
v_pk_add_f32 v[52:53],v[52:53],v[54:55]
v_pk_add_f32 v[56:57],v[56:57],v[58:59]
v_pk_add_f32 v[60:61],v[60:61],v[62:63]
v_add_f32 v52,v52,v53
v_pk_add_f32 v[56:57],v[56:57],v[60:61]
v_mul_f32 v52,s68,v52
v_add_f32 v53,v56,v57
v_mul_f32 v53,s68,v53
v_fma_f32 v53,-v52,v52,v53
v_max_f32 v53,0,v53
v_add_f32 v54,s69,v53
v_fma_f32 v55,s69,v54,v53
v_rsq_f32 v65,v54
v_rsq_f32 v54,v55
v_mul_f32_e64 v64,-v52,v65
v_mul_f32 v63,v55,v54
v_mul_f32 v61,s76,v54
v_mul_f32 v55,v54,v54
v_mul_f32 v62,s77,v55
v_mul_f32 v62,v62,v61
v_cndmask_b32 v60,v52,v64,s[74:75]
v_cndmask_b32 v61,v61,v65,s[74:75]
buffer_store_dwordx4 v[60:63],v4,s[36:39],s59 offen offset:0 sc0 sc1
s_mov_b32 s63,s60
s_waitcnt vmcnt(5)
v_cmp_eq_u32 vcc,s53,v14
s_nop 0
s_cmp_eq_u64 vcc,-1
s_cselect_b32 s78,s45,256
s_cmp_lt_u32 s78,256
s_cbranch_scc1 Lr_tc1_%=
s_waitcnt vmcnt(0)
buffer_store_dword v13,v5,s[36:39],s63 offen offset:0 sc0 sc1
s_mov_b32 s63,0x7fff0000
s_cmp_ge_u32 s45,256
s_cbranch_scc1 Lr_done_%=
Lr_bp1_%=:
buffer_load_dword v14,v6,s[36:39],s61 offen offset:0 sc1
s_add_u32 s82,s82,1
s_waitcnt vmcnt(0)
v_cmp_eq_u32 vcc,s53,v14
s_nop 0
s_cmp_eq_u64 vcc,-1
s_cbranch_scc1 Lr_bq1_%=
s_sleep 2
s_cmp_lt_u32 s82,20000
s_cbranch_scc1 Lr_bp1_%=
Lr_bq1_%=:
s_add_u32 s78,s55,71303168
buffer_load_dwordx4 v[164:167],v1,s[36:39],s78 offen offset:0 sc1
buffer_load_dwordx4 v[168:171],v1,s[36:39],s78 offen offset:2048 sc1
s_add_u32 s78,s55,71307264
buffer_load_dwordx4 v[172:175],v1,s[36:39],s78 offen offset:0 sc1
buffer_load_dwordx4 v[176:179],v1,s[36:39],s78 offen offset:2048 sc1
s_add_u32 s78,s55,89128960
buffer_load_dwordx4 v[180:183],v1,s[36:39],s78 offen offset:0 sc1
buffer_load_dwordx4 v[184:187],v1,s[36:39],s78 offen offset:2048 sc1
s_add_u32 s78,s55,89133056
buffer_load_dwordx4 v[188:191],v1,s[36:39],s78 offen offset:0 sc1
buffer_load_dwordx4 v[192:195],v1,s[36:39],s78 offen offset:2048 sc1
s_add_u32 m0,s65,0
s_nop 0
D 9,56,0
D 9,56,1024
D 9,56,2048
D 9,56,3072
buffer_load_dword v14,v6,s[36:39],s62 offen offset:0 sc1
s_lshr_b32 s78,s45,1
s_and_b32 s79,s45,1
s_lshl_b32 s79,s79,7
s_lshl_b32 s57,s78,15
s_add_u32 s57,s57,s79
s_add_u32 s57,s57,12582912
s_add_u32 s58,s57,4096
s_lshl_b32 s59,s78,9
s_add_u32 s59,s59,s79
s_add_u32 s59,s59,107020288
s_lshl_b32 s60,s45,9
s_add_u32 s60,s60,s66
s_add_u32 s60,s60,131072
s_add_u32 s45,s45,s46
s_lshr_b32 s78,s45,1
s_and_b32 s79,s45,1
s_lshl_b32 s79,s79,7
s_lshl_b32 s55,s78,15
s_add_u32 s55,s55,s79
s_mul_i32 s56,s45,16384
s_add_u32 s56,s56,98566144
s_lshl_b32 s61,s78,11
s_add_u32 s61,s61,110587904
s_add_u32 s78,s45,s46
s_lshr_b32 s78,s78,1
s_lshl_b32 s62,s78,11
s_add_u32 s62,s62,110587904
s_waitcnt vmcnt(0)
s_branch Lr_it0_%=
Lr_ta1_%=:
v_mfma_f32_16x16x32_bf16 v[116:119],v[26:29],v[196:199],0
s_add_u32 s78,s55,71303168
buffer_load_dwordx4 v[164:167],v1,s[36:39],s78 offen offset:0 sc1
v_mfma_f32_16x16x32_bf16 v[120:123],v[24:27],v[196:199],0
L 6
buffer_load_dwordx4 v[168:171],v1,s[36:39],s78 offen offset:2048 sc1
M 116,v[26:29],212
s_add_u32 s78,s55,71307264
buffer_load_dwordx4 v[172:175],v1,s[36:39],s78 offen offset:0 sc1
M 120,v[24:27],212
L 4
buffer_load_dwordx4 v[176:179],v1,s[36:39],s78 offen offset:2048 sc1
v_mfma_f32_16x16x32_bf16 v[124:127],v[26:29],v[200:203],0
s_add_u32 s78,s55,89128960
buffer_load_dwordx4 v[180:183],v1,s[36:39],s78 offen offset:0 sc1
v_mfma_f32_16x16x32_bf16 v[128:131],v[24:27],v[200:203],0
L 2
buffer_load_dwordx4 v[184:187],v1,s[36:39],s78 offen offset:2048 sc1
M 124,v[26:29],216
L 0
v_cvt_pk_bf16_f32 v148,v116,v117
v_cvt_pk_bf16_f32 v149,v118,v119
M 128,v[24:27],216
s_add_u32 s78,s55,89133056
buffer_load_dwordx4 v[188:191],v1,s[36:39],s78 offen offset:0 sc1
v_mfma_f32_16x16x32_bf16 v[132:135],v[26:29],v[204:207],0
s_add_u32 m0,s65,0
buffer_load_dwordx4 v[192:195],v1,s[36:39],s78 offen offset:2048 sc1
v_mfma_f32_16x16x32_bf16 v[136:139],v[24:27],v[204:207],0
D 9,56,0
M 132,v[26:29],220
D 9,56,1024
M 136,v[24:27],220
D 9,56,2048
v_mfma_f32_16x16x32_bf16 v[140:143],v[26:29],v[208:211],0
D 9,56,3072
v_mfma_f32_16x16x32_bf16 v[144:147],v[24:27],v[208:211],0
v_cvt_pk_bf16_f32 v150,v120,v121
v_cvt_pk_bf16_f32 v151,v122,v123
M 140,v[26:29],224
v_cvt_pk_bf16_f32 v152,v124,v125
v_cvt_pk_bf16_f32 v153,v126,v127
M 144,v[24:27],224
v_cvt_pk_bf16_f32 v154,v128,v129
v_cvt_pk_bf16_f32 v155,v130,v131
v_mfma_f32_16x16x32_bf16 v[36:39],v[32:35],v[148:151],0
v_cvt_pk_bf16_f32 v156,v132,v133
v_cvt_pk_bf16_f32 v157,v134,v135
v_mfma_f32_16x16x32_bf16 v[40:43],v[148:151],v[148:151],0
v_cvt_pk_bf16_f32 v158,v136,v137
v_cvt_pk_bf16_f32 v159,v138,v139
M 36,v[32:35],152
v_cvt_pk_bf16_f32 v160,v140,v141
v_cvt_pk_bf16_f32 v161,v142,v143
M 40,v[152:155],152
v_cvt_pk_bf16_f32 v162,v144,v145
v_cvt_pk_bf16_f32 v163,v146,v147
M 36,v[32:35],156
v_lshlrev_b32 v84,16,v68
v_and_b32 v85,0xffff0000,v68
M 40,v[156:159],156
v_lshlrev_b32 v86,16,v69
v_and_b32 v87,0xffff0000,v69
M 36,v[32:35],160
v_lshlrev_b32 v88,16,v70
v_and_b32 v89,0xffff0000,v70
M 40,v[160:163],160
v_lshlrev_b32 v90,16,v71
v_and_b32 v91,0xffff0000,v71
v_lshlrev_b32 v92,16,v72
v_and_b32 v93,0xffff0000,v72
v_lshlrev_b32 v94,16,v73
v_and_b32 v95,0xffff0000,v73
v_lshlrev_b32 v96,16,v74
v_and_b32 v97,0xffff0000,v74
v_lshlrev_b32 v98,16,v75
v_and_b32 v99,0xffff0000,v75
v_lshlrev_b32 v100,16,v76
v_and_b32 v101,0xffff0000,v76
v_lshlrev_b32 v102,16,v77
v_and_b32 v103,0xffff0000,v77
v_lshlrev_b32 v104,16,v78
v_and_b32 v105,0xffff0000,v78
v_lshlrev_b32 v106,16,v79
v_and_b32 v107,0xffff0000,v79
v_lshlrev_b32 v108,16,v80
v_and_b32 v109,0xffff0000,v80
v_lshlrev_b32 v110,16,v81
v_and_b32 v111,0xffff0000,v81
v_lshlrev_b32 v112,16,v82
v_and_b32 v113,0xffff0000,v82
v_lshlrev_b32 v114,16,v83
v_and_b32 v115,0xffff0000,v83
v_add_f32_dpp v44,v36,v36 row_ror:8 row_mask:0xf bank_mask:0xf
v_cndmask_b32 v45,v40,v41,s[70:71]
v_cndmask_b32 v46,v42,v43,s[70:71]
ds_write_b32 v2,v44 offset:0
v_cndmask_b32 v45,v45,v46,s[72:73]
ds_write_b32 v0,v45 offset:0
L 0
s_barrier
R 52,3,0
R 56,3,16
R 60,3,32
L 0
v_pk_add_f32 v[52:53],v[52:53],v[54:55]
v_pk_add_f32 v[56:57],v[56:57],v[58:59]
v_pk_add_f32 v[60:61],v[60:61],v[62:63]
v_add_f32 v52,v52,v53
v_pk_add_f32 v[56:57],v[56:57],v[60:61]
v_mul_f32 v52,s68,v52
v_add_f32 v53,v56,v57
v_mul_f32 v53,s68,v53
v_fma_f32 v53,-v52,v52,v53
v_max_f32 v53,0,v53
buffer_load_dword v14,v6,s[36:39],s62 offen offset:0 sc1
v_add_f32 v53,s69,v53
v_rsq_f32 v48,v53
s_nop 0
v_mul_f32_e64 v50,-v52,v48
v_mov_b32 v49,v48
v_mov_b32 v51,v50
v_pk_fma_f32 v[52:53],v[116:117],v[48:49],v[50:51]
v_pk_fma_f32 v[54:55],v[118:119],v[48:49],v[50:51]
v_pk_fma_f32 v[56:57],v[120:121],v[48:49],v[50:51]
v_pk_fma_f32 v[58:59],v[122:123],v[48:49],v[50:51]
v_pk_fma_f32 v[116:117],v[52:53],v[84:85],v[116:117]
v_pk_fma_f32 v[118:119],v[54:55],v[86:87],v[118:119]
v_pk_fma_f32 v[120:121],v[56:57],v[88:89],v[120:121]
v_pk_fma_f32 v[122:123],v[58:59],v[90:91],v[122:123]
v_cvt_pk_bf16_f32 v148,v116,v117
v_cvt_pk_bf16_f32 v149,v118,v119
v_cvt_pk_bf16_f32 v150,v120,v121
v_cvt_pk_bf16_f32 v151,v122,v123
v_pk_fma_f32 v[52:53],v[124:125],v[48:49],v[50:51]
v_pk_fma_f32 v[54:55],v[126:127],v[48:49],v[50:51]
v_pk_fma_f32 v[56:57],v[128:129],v[48:49],v[50:51]
v_pk_fma_f32 v[58:59],v[130:131],v[48:49],v[50:51]
v_pk_fma_f32 v[124:125],v[52:53],v[92:93],v[124:125]
v_pk_fma_f32 v[126:127],v[54:55],v[94:95],v[126:127]
v_pk_fma_f32 v[128:129],v[56:57],v[96:97],v[128:129]
v_pk_fma_f32 v[130:131],v[58:59],v[98:99],v[130:131]
v_cvt_pk_bf16_f32 v152,v124,v125
v_cvt_pk_bf16_f32 v153,v126,v127
v_cvt_pk_bf16_f32 v154,v128,v129
v_cvt_pk_bf16_f32 v155,v130,v131
v_pk_fma_f32 v[52:53],v[132:133],v[48:49],v[50:51]
v_pk_fma_f32 v[54:55],v[134:135],v[48:49],v[50:51]
v_pk_fma_f32 v[56:57],v[136:137],v[48:49],v[50:51]
v_pk_fma_f32 v[58:59],v[138:139],v[48:49],v[50:51]
v_pk_fma_f32 v[132:133],v[52:53],v[100:101],v[132:133]
v_pk_fma_f32 v[134:135],v[54:55],v[102:103],v[134:135]
v_pk_fma_f32 v[136:137],v[56:57],v[104:105],v[136:137]
v_pk_fma_f32 v[138:139],v[58:59],v[106:107],v[138:139]
v_cvt_pk_bf16_f32 v156,v132,v133
v_cvt_pk_bf16_f32 v157,v134,v135
v_cvt_pk_bf16_f32 v158,v136,v137
v_cvt_pk_bf16_f32 v159,v138,v139
v_pk_fma_f32 v[52:53],v[140:141],v[48:49],v[50:51]
v_pk_fma_f32 v[54:55],v[142:143],v[48:49],v[50:51]
v_pk_fma_f32 v[56:57],v[144:145],v[48:49],v[50:51]
v_pk_fma_f32 v[58:59],v[146:147],v[48:49],v[50:51]
v_pk_fma_f32 v[140:141],v[52:53],v[108:109],v[140:141]
v_pk_fma_f32 v[142:143],v[54:55],v[110:111],v[142:143]
v_pk_fma_f32 v[144:145],v[56:57],v[112:113],v[144:145]
v_pk_fma_f32 v[146:147],v[58:59],v[114:115],v[146:147]
v_cvt_pk_bf16_f32 v160,v140,v141
v_cvt_pk_bf16_f32 v161,v142,v143
v_cvt_pk_bf16_f32 v162,v144,v145
v_cvt_pk_bf16_f32 v163,v146,v147
s_waitcnt vmcnt(13)
buffer_store_dword v13,v5,s[36:39],s63 offen offset:0 sc0 sc1
s_mov_b32 s63,0x7fff0000
v_mfma_f32_16x16x32_bf16 v[36:39],v[32:35],v[148:151],0
buffer_store_dwordx4 v[148:151],v1,s[36:39],s57 offen offset:0 sc0 sc1
v_mfma_f32_16x16x32_bf16 v[40:43],v[148:151],v[148:151],0
M 36,v[32:35],152
buffer_store_dwordx4 v[152:155],v1,s[36:39],s57 offen offset:2048 sc0 sc1
M 40,v[152:155],152
M 36,v[32:35],156
buffer_store_dwordx4 v[156:159],v1,s[36:39],s58 offen offset:0 sc0 sc1
M 40,v[156:159],156
M 36,v[32:35],160
buffer_store_dwordx4 v[160:163],v1,s[36:39],s58 offen offset:2048 sc0 sc1
M 40,v[160:163],160
s_nop 5
v_add_f32_dpp v44,v36,v36 row_ror:8 row_mask:0xf bank_mask:0xf
s_nop 0
v_cndmask_b32 v45,v40,v41,s[70:71]
v_cndmask_b32 v46,v42,v43,s[70:71]
ds_write_b32 v2,v44 offset:1024
v_cndmask_b32 v45,v45,v46,s[72:73]
ds_write_b32 v0,v45 offset:1024
L 0
s_barrier
R 52,3,1024
R 56,3,1040
R 60,3,1056
L 0
v_pk_add_f32 v[52:53],v[52:53],v[54:55]
v_pk_add_f32 v[56:57],v[56:57],v[58:59]
v_pk_add_f32 v[60:61],v[60:61],v[62:63]
v_add_f32 v52,v52,v53
v_pk_add_f32 v[56:57],v[56:57],v[60:61]
v_mul_f32 v52,s68,v52
v_add_f32 v53,v56,v57
v_mul_f32 v53,s68,v53
v_fma_f32 v53,-v52,v52,v53
v_max_f32 v53,0,v53
v_add_f32 v54,s69,v53
v_fma_f32 v55,s69,v54,v53
v_rsq_f32 v65,v54
v_rsq_f32 v54,v55
v_mul_f32_e64 v64,-v52,v65
v_mul_f32 v63,v55,v54
v_mul_f32 v61,s76,v54
v_mul_f32 v55,v54,v54
v_mul_f32 v62,s77,v55
v_mul_f32 v62,v62,v61
v_cndmask_b32 v60,v52,v64,s[74:75]
v_cndmask_b32 v61,v61,v65,s[74:75]
buffer_store_dwordx4 v[60:63],v4,s[36:39],s59 offen offset:0 sc0 sc1
s_mov_b32 s63,s60
s_lshr_b32 s78,s45,1
s_and_b32 s79,s45,1
s_lshl_b32 s79,s79,7
s_lshl_b32 s57,s78,15
s_add_u32 s57,s57,s79
s_add_u32 s57,s57,12582912
s_add_u32 s58,s57,4096
s_lshl_b32 s59,s78,9
s_add_u32 s59,s59,s79
s_add_u32 s59,s59,107020288
s_lshl_b32 s60,s45,9
s_add_u32 s60,s60,s66
s_add_u32 s60,s60,131072
s_add_u32 s45,s45,s46
s_lshr_b32 s78,s45,1
s_and_b32 s79,s45,1
s_lshl_b32 s79,s79,7
s_lshl_b32 s55,s78,15
s_add_u32 s55,s55,s79
s_mul_i32 s56,s45,16384
)"
R"(s_add_u32 s56,s56,98566144
s_lshl_b32 s61,s78,11
s_add_u32 s61,s61,110587904
s_add_u32 s78,s45,s46
s_lshr_b32 s78,s78,1
s_lshl_b32 s62,s78,11
s_add_u32 s62,s62,110587904
s_waitcnt vmcnt(6)
s_branch Lr_it0_%=
Lr_tb1_%=:
v_add_f32 v53,s69,v53
v_rsq_f32 v48,v53
s_nop 0
v_mul_f32_e64 v50,-v52,v48
v_mov_b32 v49,v48
v_mov_b32 v51,v50
v_pk_fma_f32 v[52:53],v[116:117],v[48:49],v[50:51]
v_pk_fma_f32 v[54:55],v[118:119],v[48:49],v[50:51]
v_pk_fma_f32 v[56:57],v[120:121],v[48:49],v[50:51]
v_pk_fma_f32 v[58:59],v[122:123],v[48:49],v[50:51]
s_add_u32 s78,s55,71303168
buffer_load_dwordx4 v[164:167],v1,s[36:39],s78 offen offset:0 sc1
buffer_load_dwordx4 v[168:171],v1,s[36:39],s78 offen offset:2048 sc1
s_add_u32 s78,s55,71307264
buffer_load_dwordx4 v[172:175],v1,s[36:39],s78 offen offset:0 sc1
buffer_load_dwordx4 v[176:179],v1,s[36:39],s78 offen offset:2048 sc1
v_pk_fma_f32 v[116:117],v[52:53],v[84:85],v[116:117]
v_pk_fma_f32 v[118:119],v[54:55],v[86:87],v[118:119]
v_pk_fma_f32 v[120:121],v[56:57],v[88:89],v[120:121]
v_pk_fma_f32 v[122:123],v[58:59],v[90:91],v[122:123]
v_cvt_pk_bf16_f32 v148,v116,v117
v_cvt_pk_bf16_f32 v149,v118,v119
v_cvt_pk_bf16_f32 v150,v120,v121
v_cvt_pk_bf16_f32 v151,v122,v123
v_pk_fma_f32 v[52:53],v[124:125],v[48:49],v[50:51]
v_pk_fma_f32 v[54:55],v[126:127],v[48:49],v[50:51]
v_pk_fma_f32 v[56:57],v[128:129],v[48:49],v[50:51]
v_pk_fma_f32 v[58:59],v[130:131],v[48:49],v[50:51]
s_add_u32 s78,s55,89128960
buffer_load_dwordx4 v[180:183],v1,s[36:39],s78 offen offset:0 sc1
buffer_load_dwordx4 v[184:187],v1,s[36:39],s78 offen offset:2048 sc1
s_add_u32 s78,s55,89133056
buffer_load_dwordx4 v[188:191],v1,s[36:39],s78 offen offset:0 sc1
buffer_load_dwordx4 v[192:195],v1,s[36:39],s78 offen offset:2048 sc1
v_pk_fma_f32 v[124:125],v[52:53],v[92:93],v[124:125]
v_pk_fma_f32 v[126:127],v[54:55],v[94:95],v[126:127]
v_pk_fma_f32 v[128:129],v[56:57],v[96:97],v[128:129]
v_pk_fma_f32 v[130:131],v[58:59],v[98:99],v[130:131]
v_cvt_pk_bf16_f32 v152,v124,v125
v_cvt_pk_bf16_f32 v153,v126,v127
v_cvt_pk_bf16_f32 v154,v128,v129
v_cvt_pk_bf16_f32 v155,v130,v131
v_pk_fma_f32 v[52:53],v[132:133],v[48:49],v[50:51]
v_pk_fma_f32 v[54:55],v[134:135],v[48:49],v[50:51]
v_pk_fma_f32 v[56:57],v[136:137],v[48:49],v[50:51]
v_pk_fma_f32 v[58:59],v[138:139],v[48:49],v[50:51]
s_add_u32 m0,s65,0
s_nop 0
D 9,56,0
D 9,56,1024
D 9,56,2048
D 9,56,3072
v_pk_fma_f32 v[132:133],v[52:53],v[100:101],v[132:133]
v_pk_fma_f32 v[134:135],v[54:55],v[102:103],v[134:135]
v_pk_fma_f32 v[136:137],v[56:57],v[104:105],v[136:137]
v_pk_fma_f32 v[138:139],v[58:59],v[106:107],v[138:139]
v_cvt_pk_bf16_f32 v156,v132,v133
v_cvt_pk_bf16_f32 v157,v134,v135
v_cvt_pk_bf16_f32 v158,v136,v137
v_cvt_pk_bf16_f32 v159,v138,v139
v_pk_fma_f32 v[52:53],v[140:141],v[48:49],v[50:51]
v_pk_fma_f32 v[54:55],v[142:143],v[48:49],v[50:51]
v_pk_fma_f32 v[56:57],v[144:145],v[48:49],v[50:51]
v_pk_fma_f32 v[58:59],v[146:147],v[48:49],v[50:51]
v_pk_fma_f32 v[140:141],v[52:53],v[108:109],v[140:141]
v_pk_fma_f32 v[142:143],v[54:55],v[110:111],v[142:143]
v_pk_fma_f32 v[144:145],v[56:57],v[112:113],v[144:145]
v_pk_fma_f32 v[146:147],v[58:59],v[114:115],v[146:147]
v_cvt_pk_bf16_f32 v160,v140,v141
v_cvt_pk_bf16_f32 v161,v142,v143
v_cvt_pk_bf16_f32 v162,v144,v145
v_cvt_pk_bf16_f32 v163,v146,v147
v_mfma_f32_16x16x32_bf16 v[36:39],v[32:35],v[148:151],0
buffer_load_dword v14,v6,s[36:39],s62 offen offset:0 sc1
buffer_store_dwordx4 v[148:151],v1,s[36:39],s57 offen offset:0 sc0 sc1
v_mfma_f32_16x16x32_bf16 v[40:43],v[148:151],v[148:151],0
M 36,v[32:35],152
buffer_store_dwordx4 v[152:155],v1,s[36:39],s57 offen offset:2048 sc0 sc1
M 40,v[152:155],152
M 36,v[32:35],156
buffer_store_dwordx4 v[156:159],v1,s[36:39],s58 offen offset:0 sc0 sc1
M 40,v[156:159],156
M 36,v[32:35],160
buffer_store_dwordx4 v[160:163],v1,s[36:39],s58 offen offset:2048 sc0 sc1
M 40,v[160:163],160
s_nop 5
v_add_f32_dpp v44,v36,v36 row_ror:8 row_mask:0xf bank_mask:0xf
s_nop 0
v_cndmask_b32 v45,v40,v41,s[70:71]
v_cndmask_b32 v46,v42,v43,s[70:71]
ds_write_b32 v2,v44 offset:1024
v_cndmask_b32 v45,v45,v46,s[72:73]
ds_write_b32 v0,v45 offset:1024
L 0
s_barrier
R 52,3,1024
R 56,3,1040
R 60,3,1056
L 0
v_pk_add_f32 v[52:53],v[52:53],v[54:55]
v_pk_add_f32 v[56:57],v[56:57],v[58:59]
v_pk_add_f32 v[60:61],v[60:61],v[62:63]
v_add_f32 v52,v52,v53
v_pk_add_f32 v[56:57],v[56:57],v[60:61]
v_mul_f32 v52,s68,v52
v_add_f32 v53,v56,v57
v_mul_f32 v53,s68,v53
v_fma_f32 v53,-v52,v52,v53
v_max_f32 v53,0,v53
v_add_f32 v54,s69,v53
v_fma_f32 v55,s69,v54,v53
v_rsq_f32 v65,v54
v_rsq_f32 v54,v55
v_mul_f32_e64 v64,-v52,v65
v_mul_f32 v63,v55,v54
v_mul_f32 v61,s76,v54
v_mul_f32 v55,v54,v54
v_mul_f32 v62,s77,v55
v_mul_f32 v62,v62,v61
v_cndmask_b32 v60,v52,v64,s[74:75]
v_cndmask_b32 v61,v61,v65,s[74:75]
buffer_store_dwordx4 v[60:63],v4,s[36:39],s59 offen offset:0 sc0 sc1
s_mov_b32 s63,s60
s_lshr_b32 s78,s45,1
s_and_b32 s79,s45,1
s_lshl_b32 s79,s79,7
s_lshl_b32 s57,s78,15
s_add_u32 s57,s57,s79
s_add_u32 s57,s57,12582912
s_add_u32 s58,s57,4096
s_lshl_b32 s59,s78,9
s_add_u32 s59,s59,s79
s_add_u32 s59,s59,107020288
s_lshl_b32 s60,s45,9
s_add_u32 s60,s60,s66
s_add_u32 s60,s60,131072
s_add_u32 s45,s45,s46
s_lshr_b32 s78,s45,1
s_and_b32 s79,s45,1
s_lshl_b32 s79,s79,7
s_lshl_b32 s55,s78,15
s_add_u32 s55,s55,s79
s_mul_i32 s56,s45,16384
s_add_u32 s56,s56,98566144
s_lshl_b32 s61,s78,11
s_add_u32 s61,s61,110587904
s_add_u32 s78,s45,s46
s_lshr_b32 s78,s78,1
s_lshl_b32 s62,s78,11
s_add_u32 s62,s62,110587904
s_waitcnt vmcnt(5)
s_branch Lr_it0_%=
Lr_tc1_%=:
s_add_u32 s78,s55,71303168
buffer_load_dwordx4 v[164:167],v1,s[36:39],s78 offen offset:0 sc1
buffer_load_dwordx4 v[168:171],v1,s[36:39],s78 offen offset:2048 sc1
s_add_u32 s78,s55,71307264
buffer_load_dwordx4 v[172:175],v1,s[36:39],s78 offen offset:0 sc1
buffer_load_dwordx4 v[176:179],v1,s[36:39],s78 offen offset:2048 sc1
s_add_u32 s78,s55,89128960
buffer_load_dwordx4 v[180:183],v1,s[36:39],s78 offen offset:0 sc1
buffer_load_dwordx4 v[184:187],v1,s[36:39],s78 offen offset:2048 sc1
s_add_u32 s78,s55,89133056
buffer_load_dwordx4 v[188:191],v1,s[36:39],s78 offen offset:0 sc1
buffer_load_dwordx4 v[192:195],v1,s[36:39],s78 offen offset:2048 sc1
s_add_u32 m0,s65,0
s_nop 0
D 9,56,0
D 9,56,1024
D 9,56,2048
D 9,56,3072
buffer_load_dword v14,v6,s[36:39],s62 offen offset:0 sc1
s_waitcnt vmcnt(13)
buffer_store_dword v13,v5,s[36:39],s63 offen offset:0 sc0 sc1
s_mov_b32 s63,0x7fff0000
s_lshr_b32 s78,s45,1
s_and_b32 s79,s45,1
s_lshl_b32 s79,s79,7
s_lshl_b32 s57,s78,15
s_add_u32 s57,s57,s79
s_add_u32 s57,s57,12582912
s_add_u32 s58,s57,4096
s_lshl_b32 s59,s78,9
s_add_u32 s59,s59,s79
s_add_u32 s59,s59,107020288
s_lshl_b32 s60,s45,9
s_add_u32 s60,s60,s66
s_add_u32 s60,s60,131072
s_add_u32 s45,s45,s46
s_lshr_b32 s78,s45,1
s_and_b32 s79,s45,1
s_lshl_b32 s79,s79,7
s_lshl_b32 s55,s78,15
s_add_u32 s55,s55,s79
s_mul_i32 s56,s45,16384
s_add_u32 s56,s56,98566144
s_lshl_b32 s61,s78,11
s_add_u32 s61,s61,110587904
s_add_u32 s78,s45,s46
s_lshr_b32 s78,s78,1
s_lshl_b32 s62,s78,11
s_add_u32 s62,s62,110587904
s_waitcnt vmcnt(1)
s_branch Lr_it0_%=
Lr_done_%=:
s_waitcnt vmcnt(0) lgkmcnt(0)
s_barrier
Lcode_r_e:
.purgem M
.purgem R
.purgem T
.purgem D
.purgem L
)"
  :: "s"(ws), "s"(sWave), "s"(sG0), "s"(sStep), "s"(sOutOff), "s"(sAuxOff), "s"(sBuf), "s"(sRk), "s"(sExp), "v"(vRP), "v"(vRF), "v"(vRwr), "v"(vRrd), "v"(vRS), "v"(vFlag), "v"(vL4), "v"(vLY), "v"(vQ), "v"(vY0), "v"(vTr), "v"(vSa), "v"(vSb), "s"(q) : "v0", "v1", "v2", "v3", "v4", "v5", "v6", "v7", "v8", "v9", "v10", "v11", "v12", "v13", "v14", "v15", "v16", "v17", "v18", "v19", "v20", "v21", "v22", "v23", "v24", "v25", "v26", "v27", "v28", "v29", "v30", "v31", "v32", "v33", "v34", "v35", "v36", "v37", "v38", "v39", "v40", "v41", "v42", "v43", "v44", "v45", "v46", "v47", "v48", "v49", "v50", "v51", "v52", "v53", "v54", "v55", "v56", "v57", "v58", "v59", "v60", "v61", "v62", "v63", "v64", "v65", "v66", "v67", "v68", "v69", "v70", "v71", "v72", "v73", "v74", "v75", "v76", "v77", "v78", "v79", "v80", "v81", "v82", "v83", "v84", "v85", "v86", "v87", "v88", "v89", "v90", "v91", "v92", "v93", "v94", "v95", "v96", "v97", "v98", "v99", "v100", "v101", "v102", "v103", "v104", "v105", "v106", "v107", "v108", "v109", "v110", "v111", "v112", "v113", "v114", "v115", "v116", "v117", "v118", "v119", "v120", "v121", "v122", "v123", "v124", "v125", "v126", "v127", "v128", "v129", "v130", "v131", "v132", "v133", "v134", "v135", "v136", "v137", "v138", "v139", "v140", "v141", "v142", "v143", "v144", "v145", "v146", "v147", "v148", "v149", "v150", "v151", "v152", "v153", "v154", "v155", "v156", "v157", "v158", "v159", "v160", "v161", "v162", "v163", "v164", "v165", "v166", "v167", "v168", "v169", "v170", "v171", "v172", "v173", "v174", "v175", "v176", "v177", "v178", "v179", "v180", "v181", "v182", "v183", "v184", "v185", "v186", "v187", "v188", "v189", "v190", "v191", "v192", "v193", "v194", "v195", "v196", "v197", "v198", "v199", "v200", "v201", "v202", "v203", "v204", "v205", "v206", "v207", "v208", "v209", "v210", "v211", "v212", "v213", "v214", "v215", "v216", "v217", "v218", "v219", "v220", "v221", "v222", "v223", "v224", "v225", "v226", "v227", "v228", "v229", "v230", "v231", "s36", "s37", "s38", "s39", "s40", "s41", "s42", "s43", "s44", "s45", "s46", "s47", "s48", "s49", "s50", "s51", "s52", "s53", "s54", "s55", "s56", "s57", "s58", "s59", "s60", "s61", "s62", "s63", "s64", "s65", "s66", "s67", "s68", "s69", "s70", "s71", "s72", "s73", "s74", "s75", "s76", "s77", "s78", "s79", "s80", "s81", "s82", "s83", "s84", "s85", "s86", "s87", "s88", "s89", "s90", "s91", "s92", "s93", "s94", "s95", "s96", "s97", "vcc", "scc", "memory");
  }
  CTS(9);
#define COMMON(NF, FOFF, FSTR, SNAP, LDW, NL, TOFF)                                                                                            \
  LANE_ID(ln);                                                                                                                       \
  const int l15 = ln & 15, lg = ln >> 4, lq = l15 >> 2, lp = ln & 3, rr = 8 * lg + lq, fj = ln & 31;                                 \
  const unsigned vL16 = ln * 16, vFlag = (ln < 32 && wave == 0) ? ln * 4 : OOB;                                                      \
  const unsigned vSnapOff = (wave == 0 && fj < (NF)) ? (FOFF) + (ln >> 5) * sStep * (FSTR) : OOB;                                    \
  const unsigned vSnapRd = (SNAP) + (fj < (NF) ? ln : ln & 32) * 4;                                                                  \
  const unsigned vWd0 = (ln >> 3) * (LDW) + ((ln & 7) ^ swzB(ln >> 3)) * 16, vWd1 = ((ln >> 3) + 8) * (LDW) + ((ln & 7) ^ swzB(8 + (ln >> 3))) * 16; \
  const unsigned vWr0 = (wave >> 1) * 8192 + rr * 128 + (((wave & 1) * 4 + lp) ^ swzB(rr)) * 16, vWr1 = vWr0 + 8;                    \
  const unsigned vWr2 = 16384 + rr * 128 + ((2 * wave + (lp >> 1)) ^ swzB(rr)) * 16 + (lp & 1) * 8;                                  \
  const unsigned sWave = wave, sRk = kind < 3 ? 0x7fff0000u : OFF_FY + (256 + (kind == 4 ? j4 : 80 + j3)) * 128, sG0 = rep, sDbg = sDbg0
  if (kind == 3 || kind == 5) {
    const unsigned sStep = kind == 3 ? 5 : G3OR;
    COMMON(8, fj * 128, 1024, 131072, 7168, 256, 0);
    const unsigned vOutS = (lg & 1) ? OOB : (((wave & 1) * 2 + (lg >> 1)) * 16 + l15) * 16 + (4 + (wave >> 1) - wave) * 1024;
    const unsigned vAux0 = (ln & 7) * 128 + (4 * wave + lg) * 8 + ((ln >> 3) & 1) * 4, vAux1 = 139264 + (l15 == 0 ? (4 * wave + lg) * 16 : 512 + ln * 16);
    const unsigned vAux2 = 139264 + l15 * 16, vAux3 = 0, vAn = 0, vAe = 0;
    const unsigned sOutOff = OFF_G + (slice * 6 + wave) * 1024, sFoutOff = OFF_FG + slice * 128, sAuxOff = 0, sWw = 16 * wave * 7168 + slice * 384, sBuf = 0;
    if (kind == 3) { asm volatile(
R"(.macro M d,w,x
v_mfma_f32_16x16x32_bf16 v[\d:\d+3], \w, v[\x:\x+3], v[\d:\d+3]
.endm
.macro R d,a,o
ds_read_b128 v[\d:\d+3], v\a offset:\o
.endm
.macro T d,a,o
ds_read_b64_tr_b16 \d, v\a offset:\o
.endm
.macro D a,s,o
buffer_load_dwordx4 v\a, s[36:39], s\s offen offset:\o sc1 lds
.endm
.macro L c
s_waitcnt lgkmcnt(\c)
.endm
Lcode_g3_b:
s_mov_b64 s[36:37],%0
s_mov_b32 s38,111243392
s_mov_b32 s39,0x00020000
s_mov_b32 s44,%1
s_mov_b32 s45,%2
s_mov_b32 s46,%3
s_mov_b32 s47,%4
s_mov_b32 s48,%5
s_mov_b32 s49,%6
s_mov_b32 s50,%7
s_mov_b32 s51,%8
s_mov_b32 s93,%9
v_mov_b32 v0,%10
v_mov_b32 v3,%11
v_mov_b32 v5,%12
v_mov_b32 v6,%13
v_mov_b32 v18,%14
v_mov_b32 v19,%15
v_mov_b32 v20,%16
v_mov_b32 v21,%17
v_mov_b32 v22,%18
v_mov_b32 v10,%19
v_mov_b32 v11,%20
v_mov_b32 v12,%21
v_mov_b32 v13,%22
v_mov_b32 v14,%23
v_mov_b32 v15,%24
v_mov_b32 v16,%25
s_mov_b64 s[40:41],%26
s_mov_b32 s42,7340032
s_mov_b32 s43,0x00020000
s_lshl_b32 s57,s44,11
s_mov_b32 s79,111243264
buffer_load_dword v4,off,s[36:39],s79 sc1
s_add_u32 s55,s50,0
s_add_u32 m0,s57,0
s_nop 0
buffer_load_dwordx4 v18,s[40:43],s55 offen offset:0  lds
s_add_u32 m0,s57,1024
s_nop 0
buffer_load_dwordx4 v19,s[40:43],s55 offen offset:0  lds
s_add_u32 m0,s57,8064
s_nop 0
buffer_load_dwordx4 v18,s[40:43],s55 offen offset:128  lds
s_add_u32 m0,s57,9088
s_nop 0
buffer_load_dwordx4 v19,s[40:43],s55 offen offset:128  lds
s_add_u32 m0,s57,16128
s_nop 0
buffer_load_dwordx4 v18,s[40:43],s55 offen offset:256  lds
s_add_u32 m0,s57,17152
s_nop 0
buffer_load_dwordx4 v19,s[40:43],s55 offen offset:256  lds
s_add_u32 s55,s50,458752
s_add_u32 m0,s57,24576
s_nop 0
buffer_load_dwordx4 v18,s[40:43],s55 offen offset:0  lds
s_add_u32 m0,s57,25600
s_nop 0
buffer_load_dwordx4 v19,s[40:43],s55 offen offset:0  lds
s_add_u32 m0,s57,32640
s_nop 0
buffer_load_dwordx4 v18,s[40:43],s55 offen offset:128  lds
s_add_u32 m0,s57,33664
s_nop 0
buffer_load_dwordx4 v19,s[40:43],s55 offen offset:128  lds
s_add_u32 m0,s57,40704
s_nop 0
buffer_load_dwordx4 v18,s[40:43],s55 offen offset:256  lds
s_add_u32 m0,s57,41728
s_nop 0
buffer_load_dwordx4 v19,s[40:43],s55 offen offset:256  lds
s_add_u32 s55,s50,917504
s_add_u32 m0,s57,49152
s_nop 0
buffer_load_dwordx4 v18,s[40:43],s55 offen offset:0  lds
s_add_u32 m0,s57,50176
s_nop 0
buffer_load_dwordx4 v19,s[40:43],s55 offen offset:0  lds
s_add_u32 m0,s57,57216
s_nop 0
buffer_load_dwordx4 v18,s[40:43],s55 offen offset:128  lds
s_add_u32 m0,s57,58240
s_nop 0
buffer_load_dwordx4 v19,s[40:43],s55 offen offset:128  lds
s_add_u32 m0,s57,65280
s_nop 0
buffer_load_dwordx4 v18,s[40:43],s55 offen offset:256  lds
s_add_u32 m0,s57,66304
s_nop 0
buffer_load_dwordx4 v19,s[40:43],s55 offen offset:256  lds
s_add_u32 s55,s50,1376256
s_add_u32 m0,s57,73728
s_nop 0
buffer_load_dwordx4 v18,s[40:43],s55 offen offset:0  lds
s_add_u32 m0,s57,74752
s_nop 0
buffer_load_dwordx4 v19,s[40:43],s55 offen offset:0  lds
s_add_u32 m0,s57,81792
s_nop 0
buffer_load_dwordx4 v18,s[40:43],s55 offen offset:128  lds
s_add_u32 m0,s57,82816
s_nop 0
buffer_load_dwordx4 v19,s[40:43],s55 offen offset:128  lds
s_add_u32 m0,s57,89856
s_nop 0
buffer_load_dwordx4 v18,s[40:43],s55 offen offset:256  lds
s_add_u32 m0,s57,90880
s_nop 0
buffer_load_dwordx4 v19,s[40:43],s55 offen offset:256  lds
s_add_u32 s55,s50,1835008
s_add_u32 m0,s57,98304
s_nop 0
buffer_load_dwordx4 v18,s[40:43],s55 offen offset:0  lds
s_add_u32 m0,s57,99328
s_nop 0
buffer_load_dwordx4 v19,s[40:43],s55 offen offset:0  lds
s_add_u32 m0,s57,106368
s_nop 0
buffer_load_dwordx4 v18,s[40:43],s55 offen offset:128  lds
s_add_u32 m0,s57,107392
s_nop 0
buffer_load_dwordx4 v19,s[40:43],s55 offen offset:128  lds
s_add_u32 m0,s57,114432
s_nop 0
buffer_load_dwordx4 v18,s[40:43],s55 offen offset:256  lds
s_add_u32 m0,s57,115456
s_nop 0
buffer_load_dwordx4 v19,s[40:43],s55 offen offset:256  lds
s_waitcnt vmcnt(30)
s_barrier
v_readfirstlane_b32 s53,v4
s_nop 3
s_add_u32 s53,s53,1
s_cmp_eq_u32 s53,0
s_cselect_b32 s53,1,s53
v_mov_b32 v4,s53
s_waitcnt vmcnt(24)
L 0
s_barrier
s_add_u32 s55,s50,2293760
s_add_u32 m0,s57,122880
s_nop 0
buffer_load_dwordx4 v18,s[40:43],s55 offen offset:0  lds
s_add_u32 m0,s57,123904
s_nop 0
buffer_load_dwordx4 v19,s[40:43],s55 offen offset:0  lds
s_add_u32 m0,s57,130944
s_nop 0
buffer_load_dwordx4 v18,s[40:43],s55 offen offset:128  lds
s_add_u32 m0,s57,131968
s_nop 0
buffer_load_dwordx4 v19,s[40:43],s55 offen offset:128  lds
s_add_u32 m0,s57,139008
s_nop 0
buffer_load_dwordx4 v18,s[40:43],s55 offen offset:256  lds
s_add_u32 m0,s57,140032
s_nop 0
buffer_load_dwordx4 v19,s[40:43],s55 offen offset:256  lds
v_add_u32 v26,0,v20
v_add_u32 v27,0,v21
v_add_u32 v28,0,v22
T a[0:1],26,0
T a[2:3],26,512
T a[4:5],27,0
T a[6:7],27,512
T a[8:9],28,0
T a[10:11],28,512
T a[12:13],26,4096
T a[14:15],26,4608
T a[16:17],27,4096
T a[18:19],27,4608
T a[20:21],28,4096
T a[22:23],28,4608
s_waitcnt vmcnt(24)
L 0
s_barrier
s_add_u32 s55,s50,2752512
s_add_u32 m0,s57,0
s_nop 0
buffer_load_dwordx4 v18,s[40:43],s55 offen offset:0  lds
s_add_u32 m0,s57,1024
s_nop 0
buffer_load_dwordx4 v19,s[40:43],s55 offen offset:0  lds
s_add_u32 m0,s57,8064
s_nop 0
buffer_load_dwordx4 v18,s[40:43],s55 offen offset:128  lds
s_add_u32 m0,s57,9088
s_nop 0
buffer_load_dwordx4 v19,s[40:43],s55 offen offset:128  lds
s_add_u32 m0,s57,16128
s_nop 0
buffer_load_dwordx4 v18,s[40:43],s55 offen offset:256  lds
s_add_u32 m0,s57,17152
s_nop 0
buffer_load_dwordx4 v19,s[40:43],s55 offen offset:256  lds
v_add_u32 v26,24576,v20
v_add_u32 v27,24576,v21
v_add_u32 v28,24576,v22
T a[24:25],26,0
T a[26:27],26,512
T a[28:29],27,0
T a[30:31],27,512
T a[32:33],28,0
T a[34:35],28,512
T a[36:37],26,4096
T a[38:39],26,4608
T a[40:41],27,4096
T a[42:43],27,4608
T a[44:45],28,4096
T a[46:47],28,4608
s_waitcnt vmcnt(24)
L 0
s_barrier
s_add_u32 s55,s50,3211264
s_add_u32 m0,s57,24576
s_nop 0
buffer_load_dwordx4 v18,s[40:43],s55 offen offset:0  lds
s_add_u32 m0,s57,25600
s_nop 0
buffer_load_dwordx4 v19,s[40:43],s55 offen offset:0  lds
s_add_u32 m0,s57,32640
s_nop 0
buffer_load_dwordx4 v18,s[40:43],s55 offen offset:128  lds
s_add_u32 m0,s57,33664
s_nop 0
buffer_load_dwordx4 v19,s[40:43],s55 offen offset:128  lds
s_add_u32 m0,s57,40704
s_nop 0
buffer_load_dwordx4 v18,s[40:43],s55 offen offset:256  lds
s_add_u32 m0,s57,41728
s_nop 0
buffer_load_dwordx4 v19,s[40:43],s55 offen offset:256  lds
v_add_u32 v26,49152,v20
v_add_u32 v27,49152,v21
v_add_u32 v28,49152,v22
T a[48:49],26,0
T a[50:51],26,512
T a[52:53],27,0
T a[54:55],27,512
T a[56:57],28,0
T a[58:59],28,512
T a[60:61],26,4096
T a[62:63],26,4608
T a[64:65],27,4096
T a[66:67],27,4608
T a[68:69],28,4096
T a[70:71],28,4608
s_waitcnt vmcnt(24)
L 0
s_barrier
s_add_u32 s55,s50,3670016
s_add_u32 m0,s57,49152
s_nop 0
buffer_load_dwordx4 v18,s[40:43],s55 offen offset:0  lds
s_add_u32 m0,s57,50176
s_nop 0
buffer_load_dwordx4 v19,s[40:43],s55 offen offset:0  lds
s_add_u32 m0,s57,57216
s_nop 0
buffer_load_dwordx4 v18,s[40:43],s55 offen offset:128  lds
s_add_u32 m0,s57,58240
s_nop 0
buffer_load_dwordx4 v19,s[40:43],s55 offen offset:128  lds
s_add_u32 m0,s57,65280
s_nop 0
buffer_load_dwordx4 v18,s[40:43],s55 offen offset:256  lds
s_add_u32 m0,s57,66304
s_nop 0
buffer_load_dwordx4 v19,s[40:43],s55 offen offset:256  lds
v_add_u32 v26,73728,v20
v_add_u32 v27,73728,v21
v_add_u32 v28,73728,v22
T a[72:73],26,0
T a[74:75],26,512
T a[76:77],27,0
T a[78:79],27,512
T a[80:81],28,0
T a[82:83],28,512
T a[84:85],26,4096
T a[86:87],26,4608
T a[88:89],27,4096
T a[90:91],27,4608
T a[92:93],28,4096
T a[94:95],28,4608
s_waitcnt vmcnt(24)
L 0
s_barrier
s_add_u32 s55,s50,4128768
s_add_u32 m0,s57,73728
s_nop 0
buffer_load_dwordx4 v18,s[40:43],s55 offen offset:0  lds
s_add_u32 m0,s57,74752
s_nop 0
buffer_load_dwordx4 v19,s[40:43],s55 offen offset:0  lds
s_add_u32 m0,s57,81792
s_nop 0
buffer_load_dwordx4 v18,s[40:43],s55 offen offset:128  lds
s_add_u32 m0,s57,82816
s_nop 0
buffer_load_dwordx4 v19,s[40:43],s55 offen offset:128  lds
s_add_u32 m0,s57,89856
s_nop 0
buffer_load_dwordx4 v18,s[40:43],s55 offen offset:256  lds
s_add_u32 m0,s57,90880
s_nop 0
buffer_load_dwordx4 v19,s[40:43],s55 offen offset:256  lds
v_add_u32 v26,98304,v20
v_add_u32 v27,98304,v21
v_add_u32 v28,98304,v22
T a[96:97],26,0
T a[98:99],26,512
T a[100:101],27,0
T a[102:103],27,512
T a[104:105],28,0
T a[106:107],28,512
T a[108:109],26,4096
T a[110:111],26,4608
T a[112:113],27,4096
T a[114:115],27,4608
T a[116:117],28,4096
T a[118:119],28,4608
s_waitcnt vmcnt(24)
L 0
s_barrier
s_add_u32 s55,s50,4587520
s_add_u32 m0,s57,98304
s_nop 0
buffer_load_dwordx4 v18,s[40:43],s55 offen offset:0  lds
s_add_u32 m0,s57,99328
s_nop 0
buffer_load_dwordx4 v19,s[40:43],s55 offen offset:0  lds
s_add_u32 m0,s57,106368
s_nop 0
buffer_load_dwordx4 v18,s[40:43],s55 offen offset:128  lds
s_add_u32 m0,s57,107392
s_nop 0
buffer_load_dwordx4 v19,s[40:43],s55 offen offset:128  lds
s_add_u32 m0,s57,114432
s_nop 0
buffer_load_dwordx4 v18,s[40:43],s55 offen offset:256  lds
s_add_u32 m0,s57,115456
s_nop 0
buffer_load_dwordx4 v19,s[40:43],s55 offen offset:256  lds
v_add_u32 v26,122880,v20
v_add_u32 v27,122880,v21
v_add_u32 v28,122880,v22
T a[120:121],26,0
T a[122:123],26,512
T a[124:125],27,0
T a[126:127],27,512
T a[128:129],28,0
T a[130:131],28,512
T a[132:133],26,4096
T a[134:135],26,4608
T a[136:137],27,4096
T a[138:139],27,4608
T a[140:141],28,4096
T a[142:143],28,4608
s_waitcnt vmcnt(24)
L 0
s_barrier
s_add_u32 s55,s50,5046272
s_add_u32 m0,s57,122880
s_nop 0
buffer_load_dwordx4 v18,s[40:43],s55 offen offset:0  lds
s_add_u32 m0,s57,123904
s_nop 0
buffer_load_dwordx4 v19,s[40:43],s55 offen offset:0  lds
s_add_u32 m0,s57,130944
s_nop 0
buffer_load_dwordx4 v18,s[40:43],s55 offen offset:128  lds
s_add_u32 m0,s57,131968
s_nop 0
buffer_load_dwordx4 v19,s[40:43],s55 offen offset:128  lds
s_add_u32 m0,s57,139008
s_nop 0
buffer_load_dwordx4 v18,s[40:43],s55 offen offset:256  lds
s_add_u32 m0,s57,140032
s_nop 0
buffer_load_dwordx4 v19,s[40:43],s55 offen offset:256  lds
v_add_u32 v26,0,v20
v_add_u32 v27,0,v21
v_add_u32 v28,0,v22
T a[144:145],26,0
T a[146:147],26,512
T a[148:149],27,0
T a[150:151],27,512
T a[152:153],28,0
T a[154:155],28,512
T a[156:157],26,4096
T a[158:159],26,4608
T a[160:161],27,4096
T a[162:163],27,4608
T a[164:165],28,4096
T a[166:167],28,4608
s_waitcnt vmcnt(24)
L 0
s_barrier
s_add_u32 s55,s50,5505024
s_add_u32 m0,s57,0
s_nop 0
buffer_load_dwordx4 v18,s[40:43],s55 offen offset:0  lds
s_add_u32 m0,s57,1024
s_nop 0
buffer_load_dwordx4 v19,s[40:43],s55 offen offset:0  lds
s_add_u32 m0,s57,8064
s_nop 0
buffer_load_dwordx4 v18,s[40:43],s55 offen offset:128  lds
s_add_u32 m0,s57,9088
s_nop 0
buffer_load_dwordx4 v19,s[40:43],s55 offen offset:128  lds
s_add_u32 m0,s57,16128
s_nop 0
buffer_load_dwordx4 v18,s[40:43],s55 offen offset:256  lds
s_add_u32 m0,s57,17152
s_nop 0
buffer_load_dwordx4 v19,s[40:43],s55 offen offset:256  lds
v_add_u32 v26,24576,v20
v_add_u32 v27,24576,v21
v_add_u32 v28,24576,v22
T a[168:169],26,0
T a[170:171],26,512
T a[172:173],27,0
T a[174:175],27,512
T a[176:177],28,0
T a[178:179],28,512
T a[180:181],26,4096
T a[182:183],26,4608
T a[184:185],27,4096
T a[186:187],27,4608
T a[188:189],28,4096
T a[190:191],28,4608
s_waitcnt vmcnt(24)
L 0
s_barrier
s_add_u32 s55,s50,5963776
s_add_u32 m0,s57,24576
s_nop 0
buffer_load_dwordx4 v18,s[40:43],s55 offen offset:0  lds
s_add_u32 m0,s57,25600
s_nop 0
buffer_load_dwordx4 v19,s[40:43],s55 offen offset:0  lds
s_add_u32 m0,s57,32640
s_nop 0
buffer_load_dwordx4 v18,s[40:43],s55 offen offset:128  lds
s_add_u32 m0,s57,33664
s_nop 0
buffer_load_dwordx4 v19,s[40:43],s55 offen offset:128  lds
s_add_u32 m0,s57,40704
s_nop 0
buffer_load_dwordx4 v18,s[40:43],s55 offen offset:256  lds
s_add_u32 m0,s57,41728
s_nop 0
buffer_load_dwordx4 v19,s[40:43],s55 offen offset:256  lds
v_add_u32 v26,49152,v20
v_add_u32 v27,49152,v21
v_add_u32 v28,49152,v22
T a[192:193],26,0
T a[194:195],26,512
T a[196:197],27,0
T a[198:199],27,512
T a[200:201],28,0
T a[202:203],28,512
T a[204:205],26,4096
T a[206:207],26,4608
T a[208:209],27,4096
T a[210:211],27,4608
T a[212:213],28,4096
T a[214:215],28,4608
s_waitcnt vmcnt(24)
L 0
s_barrier
s_add_u32 s55,s50,6422528
s_add_u32 m0,s57,49152
s_nop 0
buffer_load_dwordx4 v18,s[40:43],s55 offen offset:0  lds
s_add_u32 m0,s57,50176
s_nop 0
buffer_load_dwordx4 v19,s[40:43],s55 offen offset:0  lds
s_add_u32 m0,s57,57216
s_nop 0
buffer_load_dwordx4 v18,s[40:43],s55 offen offset:128  lds
s_add_u32 m0,s57,58240
)"
R"(s_nop 0
buffer_load_dwordx4 v19,s[40:43],s55 offen offset:128  lds
s_add_u32 m0,s57,65280
s_nop 0
buffer_load_dwordx4 v18,s[40:43],s55 offen offset:256  lds
s_add_u32 m0,s57,66304
s_nop 0
buffer_load_dwordx4 v19,s[40:43],s55 offen offset:256  lds
v_add_u32 v26,73728,v20
v_add_u32 v27,73728,v21
v_add_u32 v28,73728,v22
T a[216:217],26,0
T a[218:219],26,512
T a[220:221],27,0
T a[222:223],27,512
T a[224:225],28,0
T a[226:227],28,512
T a[228:229],26,4096
T a[230:231],26,4608
T a[232:233],27,4096
T a[234:235],27,4608
T a[236:237],28,4096
T a[238:239],28,4608
s_waitcnt vmcnt(24)
L 0
s_barrier
s_add_u32 s55,s50,6881280
s_add_u32 m0,s57,73728
s_nop 0
buffer_load_dwordx4 v18,s[40:43],s55 offen offset:0  lds
s_add_u32 m0,s57,74752
s_nop 0
buffer_load_dwordx4 v19,s[40:43],s55 offen offset:0  lds
s_add_u32 m0,s57,81792
s_nop 0
buffer_load_dwordx4 v18,s[40:43],s55 offen offset:128  lds
s_add_u32 m0,s57,82816
s_nop 0
buffer_load_dwordx4 v19,s[40:43],s55 offen offset:128  lds
s_add_u32 m0,s57,89856
s_nop 0
buffer_load_dwordx4 v18,s[40:43],s55 offen offset:256  lds
s_add_u32 m0,s57,90880
s_nop 0
buffer_load_dwordx4 v19,s[40:43],s55 offen offset:256  lds
v_add_u32 v26,98304,v20
v_add_u32 v27,98304,v21
v_add_u32 v28,98304,v22
T a[240:241],26,0
T a[242:243],26,512
T a[244:245],27,0
T a[246:247],27,512
T a[248:249],28,0
T a[250:251],28,512
T a[252:253],26,4096
T a[254:255],26,4608
T v[128:129],27,4096
T v[130:131],27,4608
T v[132:133],28,4096
T v[134:135],28,4608
s_waitcnt vmcnt(24)
L 0
s_barrier
v_add_u32 v26,122880,v20
v_add_u32 v27,122880,v21
v_add_u32 v28,122880,v22
T v[136:137],26,0
T v[138:139],26,512
T v[140:141],27,0
T v[142:143],27,512
T v[144:145],28,0
T v[146:147],28,512
T v[148:149],26,4096
T v[150:151],26,4608
T v[152:153],27,4096
T v[154:155],27,4608
T v[156:157],28,4096
T v[158:159],28,4608
s_waitcnt vmcnt(18)
L 0
s_barrier
v_add_u32 v26,0,v20
v_add_u32 v27,0,v21
v_add_u32 v28,0,v22
T v[160:161],26,0
T v[162:163],26,512
T v[164:165],27,0
T v[166:167],27,512
T v[168:169],28,0
T v[170:171],28,512
T v[172:173],26,4096
T v[174:175],26,4608
T v[176:177],27,4096
T v[178:179],27,4608
T v[180:181],28,4096
T v[182:183],28,4608
s_waitcnt vmcnt(12)
L 0
s_barrier
v_add_u32 v26,24576,v20
v_add_u32 v27,24576,v21
v_add_u32 v28,24576,v22
T v[184:185],26,0
T v[186:187],26,512
T v[188:189],27,0
T v[190:191],27,512
T v[192:193],28,0
T v[194:195],28,512
T v[196:197],26,4096
T v[198:199],26,4608
T v[200:201],27,4096
T v[202:203],27,4608
T v[204:205],28,4096
T v[206:207],28,4608
s_waitcnt vmcnt(6)
L 0
s_barrier
v_add_u32 v26,49152,v20
v_add_u32 v27,49152,v21
v_add_u32 v28,49152,v22
T v[208:209],26,0
T v[210:211],26,512
T v[212:213],27,0
T v[214:215],27,512
T v[216:217],28,0
T v[218:219],28,512
T v[220:221],26,4096
T v[222:223],26,4608
T v[224:225],27,4096
T v[226:227],27,4608
T v[228:229],28,4096
T v[230:231],28,4608
s_waitcnt vmcnt(0)
L 0
s_barrier
v_add_u32 v26,73728,v20
v_add_u32 v27,73728,v21
v_add_u32 v28,73728,v22
T v[232:233],26,0
T v[234:235],26,512
T v[236:237],27,0
T v[238:239],27,512
T v[240:241],28,0
T v[242:243],28,512
T v[244:245],26,4096
T v[246:247],26,4608
T v[248:249],27,4096
T v[250:251],27,4608
T v[252:253],28,4096
T v[254:255],28,4608
L 0
s_barrier
v_mov_b32 v42,0x3f803f80
v_mov_b32 v43,0x3f803f80
v_mov_b32 v44,0x3f803f80
v_mov_b32 v45,0x3f803f80
s_nop 1
v_mfma_f32_16x16x32_bf16 v[82:85],a[0:3],v[42:45],0
v_mfma_f32_16x16x32_bf16 v[86:89],a[4:7],v[42:45],0
v_mfma_f32_16x16x32_bf16 v[90:93],a[8:11],v[42:45],0
M 82,a[12:15],42
M 86,a[16:19],42
M 90,a[20:23],42
M 82,a[24:27],42
M 86,a[28:31],42
M 90,a[32:35],42
M 82,a[36:39],42
M 86,a[40:43],42
M 90,a[44:47],42
M 82,a[48:51],42
M 86,a[52:55],42
M 90,a[56:59],42
M 82,a[60:63],42
M 86,a[64:67],42
M 90,a[68:71],42
M 82,a[72:75],42
M 86,a[76:79],42
M 90,a[80:83],42
M 82,a[84:87],42
M 86,a[88:91],42
M 90,a[92:95],42
M 82,a[96:99],42
M 86,a[100:103],42
M 90,a[104:107],42
M 82,a[108:111],42
M 86,a[112:115],42
M 90,a[116:119],42
M 82,a[120:123],42
M 86,a[124:127],42
M 90,a[128:131],42
M 82,a[132:135],42
M 86,a[136:139],42
M 90,a[140:143],42
M 82,a[144:147],42
M 86,a[148:151],42
M 90,a[152:155],42
M 82,a[156:159],42
M 86,a[160:163],42
M 90,a[164:167],42
M 82,a[168:171],42
M 86,a[172:175],42
M 90,a[176:179],42
M 82,a[180:183],42
M 86,a[184:187],42
M 90,a[188:191],42
M 82,a[192:195],42
M 86,a[196:199],42
M 90,a[200:203],42
M 82,a[204:207],42
M 86,a[208:211],42
M 90,a[212:215],42
M 82,a[216:219],42
M 86,a[220:223],42
M 90,a[224:227],42
M 82,a[228:231],42
M 86,a[232:235],42
M 90,a[236:239],42
M 82,a[240:243],42
M 86,a[244:247],42
M 90,a[248:251],42
M 82,a[252:255],42
M 86,v[128:131],42
M 90,v[132:135],42
M 82,v[136:139],42
M 86,v[140:143],42
M 90,v[144:147],42
M 82,v[148:151],42
M 86,v[152:155],42
M 90,v[156:159],42
M 82,v[160:163],42
M 86,v[164:167],42
M 90,v[168:171],42
M 82,v[172:175],42
M 86,v[176:179],42
M 90,v[180:183],42
M 82,v[184:187],42
M 86,v[188:191],42
M 90,v[192:195],42
M 82,v[196:199],42
M 86,v[200:203],42
M 90,v[204:207],42
M 82,v[208:211],42
M 86,v[212:215],42
M 90,v[216:219],42
M 82,v[220:223],42
M 86,v[224:227],42
M 90,v[228:231],42
M 82,v[232:235],42
M 86,v[236:239],42
M 90,v[240:243],42
M 82,v[244:247],42
M 86,v[248:251],42
M 90,v[252:255],42
v_mov_b32 v4,s53
s_mov_b32 s82,0
s_mov_b32 s80,0
s_mov_b32 s54,s45
s_mul_i32 s57,s44,4096
s_mul_i32 s55,s45,32768
s_add_u32 s55,s55,s57
s_add_u32 s55,s55,31457280
s_mul_i32 s57,s44,4096
s_mul_i32 s60,s46,32768
s_mul_i32 s61,s46,1024
s_mul_i32 s62,s46,114688
s_mul_i32 s63,s46,2560
s_mul_i32 s64,s46,1024
s_lshl_b32 s59,s44,8
s_add_u32 s59,s59,131072
s_mul_i32 s58,s45,1024
s_add_u32 s58,s58,109613056
s_mul_i32 s65,s45,114688
s_add_u32 s65,s65,s47
s_mul_i32 s68,s45,2560
s_add_u32 s68,s68,s48
s_mul_i32 s73,s45,1024
s_add_u32 s73,s73,107479040
s_mul_i32 s74,s45,32768
s_add_u32 s74,s74,s49
s_mul_i32 s75,s45,1024
s_add_u32 s75,s75,107479040
s_mov_b32 s66,0x7fff0000
s_mov_b32 s67,0x7fff0000
s_mov_b32 s69,0x7fff0000
s_mov_b32 s76,0x7fff0000
s_mov_b32 s77,0x7fff0000
s_mov_b32 s70,0x7fff0000
s_mov_b32 s71,0x7fff0000
s_mov_b32 s72,0x7fff0000
v_mov_b32 v1,v0
v_add_u32 v2,16384,v0
s_mov_b32 s84,16384
s_mov_b32 s86,0x3a800000
s_mov_b32 s87,0x3727c5ac
s_mov_b32 s88,0xc0135761
s_mov_b32 s89,0x3d372713
s_and_b32 s85,s44,1
s_mul_i32 s85,s85,127
v_lshlrev_b32 v8,22,v0
v_and_b32 v8,0x80000000,v8
v_or_b32 v8,v8,v5
v_add_u32 v2,32768,v0
s_lshl_b32 s84,s57,1
s_add_u32 s84,s84,32768
s_getpc_b64 s[40:41]
Lg3_pc_%=:
s_lshl_b32 s78,s44,13
v_lshlrev_b32 v94,3,v0
v_add_u32 v94,s78,v94
v_min_u32 v94,Lcode_g3_e-Lg3_pc_%=-4,v94
s_mov_b32 m0,163584
s_nop 0
global_load_lds_dword v94,s[40:41]
s_add_u32 m0,s59,4096
s_nop 0
buffer_load_dword v8,s[36:39],s58 offen offset:0 sc1 lds
buffer_store_dword v4,v3,s[36:39],s71 offen offset:0 sc0 sc1
s_add_u32 m0,s59,5120
s_nop 0
buffer_load_dword v8,s[36:39],s58 offen offset:0 sc1 lds
buffer_store_dword v4,v3,s[36:39],s71 offen offset:0 sc0 sc1
s_mov_b32 s83,0
Lg3_poll_%=:
s_add_u32 s78,s83,2
s_and_b32 s78,s78,3
s_lshl_b32 s78,s78,10
s_add_u32 s78,s78,4096
s_add_u32 m0,s59,s78
s_nop 0
buffer_load_dword v8,s[36:39],s58 offen offset:0 sc1 lds
s_and_b32 s78,s83,3
s_lshl_b32 s78,s78,10
v_add_u32 v94,s78,v6
s_waitcnt vmcnt(4)
s_barrier
ds_read_b32 v7,v94 offset:4096
buffer_store_dword v4,v3,s[36:39],s70 offen offset:0 sc0 sc1
s_mov_b32 s70,s71
s_mov_b32 s71,s72
s_mov_b32 s72,0x7fff0000
s_add_u32 s83,s83,1
s_add_u32 s82,s82,1
L 0
v_cmp_eq_u32 vcc,s53,v7
s_nop 0
s_cmp_eq_u32 vcc_lo,-1
s_cbranch_scc1 Lg3_go_%=
s_cmp_lt_u32 s82,20000
s_cbranch_scc1 Lg3_poll_%=
Lg3_go_%=:
buffer_load_dword v114,v11,s[36:39],s73 offen offset:0 sc1
s_add_u32 s73,s73,s64
s_add_u32 m0,s57,0
s_add_u32 s56,s55,0
D 0,56,0
D 0,56,1024
D 0,56,2048
D 0,56,3072
s_add_u32 s58,s58,s61
s_add_u32 m0,s59,2048
s_nop 0
buffer_load_dword v8,s[36:39],s58 offen offset:0 sc1 lds
s_add_u32 m0,s57,16384
s_add_u32 s56,s55,16384
D 0,56,0
D 0,56,1024
D 0,56,2048
D 0,56,3072
s_add_u32 s55,s55,s60
s_sub_u32 s57,s84,s57
s_add_u32 s54,s54,s46
s_waitcnt vmcnt(4)
s_barrier
buffer_store_dword v4,v3,s[36:39],s70 offen offset:0 sc0 sc1
s_min_u32 s71,s71,s72
buffer_store_dword v4,v3,s[36:39],s71 offen offset:0 sc0 sc1
s_mov_b32 s70,0x7fff0000
s_mov_b32 s71,0x7fff0000
s_mov_b32 s72,0x7fff0000
s_cmp_eq_u32 s80,1
s_cbranch_scc1 Lg3_st1_%=
s_branch Lg3_st0_%=
Lg3_gox_%=:
s_add_u32 s58,s58,s61
s_add_u32 m0,s59,2048
s_nop 0
buffer_load_dword v8,s[36:39],s58 offen offset:0 sc1 lds
s_waitcnt vmcnt(0)
s_barrier
buffer_store_dword v4,v3,s[36:39],s70 offen offset:0 sc0 sc1
s_min_u32 s71,s71,s72
buffer_store_dword v4,v3,s[36:39],s71 offen offset:0 sc0 sc1
s_mov_b32 s70,0x7fff0000
s_mov_b32 s71,0x7fff0000
s_mov_b32 s72,0x7fff0000
s_cmp_eq_u32 s80,1
s_cbranch_scc1 Lg3_st1_%=
Lg3_st0_%=:
ds_read_b32 v7,v6 offset:2048
R 42,1,0
R 46,1,1024
R 50,1,2048
R 54,1,3072
R 58,1,4096
L 5
v_cmp_eq_u32 vcc,s53,v7
L 3
s_cmp_eq_u32 vcc_lo,-1
s_cbranch_scc1 Lg3_F0_%=
s_branch Lg3_L0_%=
Lg3_st1_%=:
ds_read_b32 v7,v6 offset:2048
R 42,2,0
R 46,2,1024
R 50,2,2048
R 54,2,3072
R 58,2,4096
L 5
v_cmp_eq_u32 vcc,s53,v7
L 3
s_cmp_eq_u32 vcc_lo,-1
s_cbranch_scc1 Lg3_F1_%=
s_branch Lg3_L1_%=
Lg3_F0_%=:
v_mfma_f32_16x16x32_bf16 v[26:29],a[8:11],v[42:45],0
s_add_u32 m0,s57,0
R 62,1,5120
v_add_f32_dpp v115,v114,v114 quad_perm:[1,0,3,2] row_mask:0xf bank_mask:0xf
M 26,a[20:23],46
s_add_u32 s56,s55,0
buffer_load_dword v114,v11,s[36:39],s73 offen offset:0 sc1
v_mfma_f32_16x16x32_bf16 v[22:25],a[4:7],v[42:45],0
s_add_u32 s58,s58,s61
D 0,56,0
M 22,a[16:19],46
s_mov_b32 s66,s65
R 66,1,6144
v_add_f32_dpp v115,v115,v115 quad_perm:[2,3,0,1] row_mask:0xf bank_mask:0xf
v_mfma_f32_16x16x32_bf16 v[18:21],a[0:3],v[42:45],0
s_add_u32 s65,s65,s62
v_fma_f32 v30,-v78,v82,v30
v_fma_f32 v31,-v78,v83,v31
M 18,a[12:15],46
L 3
v_add_f32_dpp v118,v115,v115 row_half_mirror row_mask:0xf bank_mask:0xf
v_fma_f32 v32,-v78,v84,v32
M 18,a[24:27],50
s_add_u32 s54,s54,s46
R 70,1,7168
v_fma_f32 v33,-v78,v85,v33
v_mul_f32 v94,v30,v30
M 18,a[36:39],54
s_add_u32 s73,s73,s64
v_mov_b32_dpp v115,v118 row_ror:8 row_mask:0xf bank_mask:0xf
v_fma_f32 v115,-v118,v118,v115
M 22,a[28:31],50
v_max_f32 v115,s87,v115
v_mul_f32 v95,v31,v31
M 22,a[40:43],54
R 42,1,8192
v_mul_f32 v96,v32,v32
M 26,a[32:35],50
v_rsq_f32 v116,v115
M 26,a[44:47],54
L 3
D 0,56,1024
M 26,a[56:59],58
R 46,1,9216
v_mul_f32 v121,v115,v116
M 26,a[68:71],62
v_mul_f32 v119,s88,v116
v_mul_f32 v115,s89,v116
v_mul_f32 v120,v119,v116
M 22,a[52:55],58
v_mul_f32 v120,v120,v115
v_mul_f32 v97,v33,v33
M 22,a[64:67],62
R 50,1,10240
v_fma_f32 v94,v80,v94,v79
M 18,a[48:51],58
ds_write_b128 v12,v[118:121] offset:0
M 18,a[60:63],62
L 4
v_fma_f32 v95,v80,v95,v79
v_fma_f32 v96,v80,v96,v79
M 18,a[72:75],66
R 54,1,11264
v_fma_f32 v97,v80,v97,v79
M 18,a[84:87],70
v_mul_f32 v94,v30,v94
v_exp_f32 v94,v94
M 22,a[76:79],66
v_mul_f32 v95,v31,v95
v_mul_f32 v96,v32,v96
M 22,a[88:91],70
R 58,1,12288
v_mul_f32 v97,v33,v97
M 26,a[80:83],66
D 0,56,2048
M 26,a[92:95],70
)"
R"(L 4
v_exp_f32 v95,v95
M 26,a[104:107],42
R 62,1,13312
v_fma_f32 v94,v94,v81,v81
M 26,a[116:119],46
v_exp_f32 v96,v96
M 22,a[100:103],42
v_exp_f32 v97,v97
v_fma_f32 v95,v95,v81,v81
M 22,a[112:115],46
R 66,1,14336
v_fma_f32 v96,v96,v81,v81
M 18,a[96:99],42
v_rcp_f32 v94,v94
M 18,a[108:111],46
L 3
v_rcp_f32 v95,v95
M 18,a[120:123],50
R 70,1,15360
v_fma_f32 v97,v97,v81,v81
M 18,a[132:135],54
s_waitcnt vmcnt(6)
D 0,56,3072
M 22,a[124:127],50
s_barrier
v_rcp_f32 v96,v96
M 22,a[136:139],54
s_add_u32 m0,s59,0
R 42,1,16384
v_rcp_f32 v97,v97
M 26,a[128:131],50
s_add_u32 s56,s55,16384
v_mul_f32 v30,v30,v94
v_mul_f32 v31,v31,v95
M 26,a[140:143],54
L 3
v_mul_f32 v32,v32,v96
v_mul_f32 v33,v33,v97
M 26,a[152:155],58
s_add_u32 s55,s55,s60
R 46,1,17408
v_cvt_pk_bf16_f32 v106,v30,v31
M 26,a[164:167],62
v_cvt_pk_bf16_f32 v107,v32,v33
v_fma_f32 v34,-v78,v86,v34
M 22,a[148:151],58
v_fma_f32 v35,-v78,v87,v35
v_fma_f32 v36,-v78,v88,v36
M 22,a[160:163],62
R 50,1,18432
v_fma_f32 v37,-v78,v89,v37
v_mul_f32 v98,v34,v34
M 18,a[144:147],58
buffer_load_dword v8,s[36:39],s58 offen offset:0 sc1 lds
M 18,a[156:159],62
L 3
v_mul_f32 v99,v35,v35
v_mul_f32 v100,v36,v36
M 18,a[168:171],66
s_add_u32 m0,s57,16384
R 54,1,19456
v_mul_f32 v101,v37,v37
M 18,a[180:183],70
s_sub_u32 s57,s84,s57
v_fma_f32 v98,v80,v98,v79
v_fma_f32 v99,v80,v99,v79
M 22,a[172:175],66
v_fma_f32 v100,v80,v100,v79
v_fma_f32 v101,v80,v101,v79
M 22,a[184:187],70
R 58,1,20480
v_mul_f32 v98,v34,v98
M 26,a[176:179],66
D 0,56,0
v_mul_f32 v99,v35,v99
M 26,a[188:191],70
L 3
v_exp_f32 v98,v98
M 26,a[200:203],42
R 62,1,21504
v_mul_f32 v100,v36,v100
M 26,a[212:215],46
v_exp_f32 v99,v99
M 22,a[196:199],42
v_exp_f32 v100,v100
M 22,a[208:211],46
R 66,1,22528
v_mul_f32 v101,v37,v101
M 18,a[192:195],42
v_exp_f32 v101,v101
M 18,a[204:207],46
L 3
v_fma_f32 v98,v98,v81,v81
v_rcp_f32 v98,v98
M 18,a[216:219],50
R 70,1,23552
v_fma_f32 v99,v99,v81,v81
M 18,a[228:231],54
D 0,56,1024
M 22,a[220:223],50
v_rcp_f32 v99,v99
M 22,a[232:235],54
R 42,1,24576
v_fma_f32 v100,v100,v81,v81
M 26,a[224:227],50
v_rcp_f32 v100,v100
M 26,a[236:239],54
L 3
v_fma_f32 v101,v101,v81,v81
v_rcp_f32 v101,v101
M 26,a[248:251],58
R 46,1,25600
v_mul_f32 v34,v34,v98
M 26,v[132:135],62
v_mul_f32 v35,v35,v99
v_mul_f32 v36,v36,v100
M 22,a[244:247],58
v_mul_f32 v37,v37,v101
v_cvt_pk_bf16_f32 v108,v34,v35
M 22,v[128:131],62
R 50,1,26624
v_cvt_pk_bf16_f32 v109,v36,v37
M 18,a[240:243],58
buffer_store_dwordx4 v[106:109],v0,s[36:39],s67 offen offset:0 sc0 sc1
v_fma_f32 v38,-v78,v90,v38
M 18,a[252:255],62
L 3
v_fma_f32 v39,-v78,v91,v39
v_fma_f32 v40,-v78,v92,v40
M 18,v[136:139],66
R 54,1,27648
v_fma_f32 v41,-v78,v93,v41
v_mul_f32 v102,v38,v38
M 18,v[148:151],70
D 0,56,2048
M 22,v[140:143],66
v_mul_f32 v103,v39,v39
v_mul_f32 v104,v40,v40
M 22,v[152:155],70
R 58,1,28672
v_mul_f32 v105,v41,v41
M 26,v[144:147],66
v_fma_f32 v102,v80,v102,v79
v_fma_f32 v103,v80,v103,v79
M 26,v[156:159],70
L 3
v_fma_f32 v104,v80,v104,v79
v_fma_f32 v105,v80,v105,v79
M 26,v[168:171],42
R 62,1,29696
v_mul_f32 v102,v38,v102
M 26,v[180:183],46
v_exp_f32 v102,v102
v_mul_f32 v103,v39,v103
M 22,v[164:167],42
v_exp_f32 v103,v103
M 22,v[176:179],46
R 66,1,30720
v_mul_f32 v104,v40,v104
M 18,v[160:163],42
v_exp_f32 v104,v104
M 18,v[172:175],46
L 3
D 0,56,3072
M 18,v[184:187],50
s_nop 0
R 70,1,31744
v_mul_f32 v105,v41,v105
M 18,v[196:199],54
s_waitcnt vmcnt(5)
v_exp_f32 v105,v105
v_fma_f32 v102,v102,v81,v81
M 22,v[188:191],50
s_barrier
buffer_store_dword v4,v3,s[36:39],s70 offen offset:0 sc0 sc1
M 22,v[200:203],54
s_mov_b32 s70,s69
R 42,2,0
ds_read_b32 v7,v6 offset:0
M 26,v[192:195],50
s_mov_b32 s69,s68
R 74,13,0
v_fma_f32 v103,v103,v81,v81
M 26,v[204:207],54
L 5
v_rcp_f32 v102,v102
M 26,v[216:219],58
s_add_u32 s68,s68,s63
R 46,2,1024
v_fma_f32 v104,v104,v81,v81
M 26,v[228:231],62
v_rcp_f32 v103,v103
M 22,v[212:215],58
v_rcp_f32 v104,v104
v_fma_f32 v105,v105,v81,v81
M 22,v[224:227],62
L 2
R 50,2,2048
v_mul_f32 v38,v38,v102
M 18,v[208:211],58
v_rcp_f32 v105,v105
M 18,v[220:223],62
s_nop 0
v_mul_f32 v39,v39,v103
v_mul_f32 v40,v40,v104
M 18,v[232:235],66
R 54,2,3072
v_mul_f32 v41,v41,v105
M 18,v[244:247],70
v_cvt_pk_bf16_f32 v110,v38,v39
v_cvt_pk_bf16_f32 v111,v40,v41
M 22,v[236:239],66
v_cmp_eq_u32 vcc,s53,v7
M 22,v[248:251],70
s_cmp_eq_u32 vcc_lo,-1
R 58,2,4096
v_permlane16_swap_b32 v110,v112
v_permlane16_swap_b32 v111,v113
M 26,v[240:243],66
buffer_store_dwordx4 v[110:113],v10,s[36:39],s67 offen offset:0 sc0 sc1
M 26,v[252:255],70
L 3
s_cbranch_scc0 Lg3_L1_%=
Lg3_F1_%=:
v_mfma_f32_16x16x32_bf16 v[38:41],a[8:11],v[42:45],0
s_add_u32 m0,s57,0
R 62,2,5120
v_add_f32_dpp v115,v114,v114 quad_perm:[1,0,3,2] row_mask:0xf bank_mask:0xf
M 38,a[20:23],46
s_add_u32 s56,s55,0
buffer_load_dword v114,v11,s[36:39],s73 offen offset:0 sc1
v_mfma_f32_16x16x32_bf16 v[34:37],a[4:7],v[42:45],0
s_add_u32 s58,s58,s61
D 0,56,0
M 34,a[16:19],46
s_mov_b32 s67,s65
R 66,2,6144
v_add_f32_dpp v115,v115,v115 quad_perm:[2,3,0,1] row_mask:0xf bank_mask:0xf
v_mfma_f32_16x16x32_bf16 v[30:33],a[0:3],v[42:45],0
s_add_u32 s65,s65,s62
v_fma_f32 v18,-v74,v82,v18
v_fma_f32 v19,-v74,v83,v19
M 30,a[12:15],46
L 3
v_add_f32_dpp v118,v115,v115 row_half_mirror row_mask:0xf bank_mask:0xf
v_fma_f32 v20,-v74,v84,v20
M 30,a[24:27],50
s_add_u32 s54,s54,s46
R 70,2,7168
v_fma_f32 v21,-v74,v85,v21
v_mul_f32 v94,v18,v18
M 30,a[36:39],54
s_add_u32 s73,s73,s64
v_mov_b32_dpp v115,v118 row_ror:8 row_mask:0xf bank_mask:0xf
v_fma_f32 v115,-v118,v118,v115
M 34,a[28:31],50
v_max_f32 v115,s87,v115
v_mul_f32 v95,v19,v19
M 34,a[40:43],54
R 42,2,8192
v_mul_f32 v96,v20,v20
M 38,a[32:35],50
v_rsq_f32 v116,v115
M 38,a[44:47],54
L 3
D 0,56,1024
M 38,a[56:59],58
R 46,2,9216
v_mul_f32 v121,v115,v116
M 38,a[68:71],62
v_mul_f32 v119,s88,v116
v_mul_f32 v115,s89,v116
v_mul_f32 v120,v119,v116
M 34,a[52:55],58
v_mul_f32 v120,v120,v115
v_mul_f32 v97,v21,v21
M 34,a[64:67],62
R 50,2,10240
v_fma_f32 v94,v76,v94,v75
M 30,a[48:51],58
ds_write_b128 v12,v[118:121] offset:2048
M 30,a[60:63],62
L 4
v_fma_f32 v95,v76,v95,v75
v_fma_f32 v96,v76,v96,v75
M 30,a[72:75],66
R 54,2,11264
v_fma_f32 v97,v76,v97,v75
M 30,a[84:87],70
v_mul_f32 v94,v18,v94
v_exp_f32 v94,v94
M 34,a[76:79],66
v_mul_f32 v95,v19,v95
v_mul_f32 v96,v20,v96
M 34,a[88:91],70
R 58,2,12288
v_mul_f32 v97,v21,v97
M 38,a[80:83],66
D 0,56,2048
M 38,a[92:95],70
L 4
v_exp_f32 v95,v95
M 38,a[104:107],42
R 62,2,13312
v_fma_f32 v94,v94,v77,v77
M 38,a[116:119],46
v_exp_f32 v96,v96
M 34,a[100:103],42
v_exp_f32 v97,v97
v_fma_f32 v95,v95,v77,v77
M 34,a[112:115],46
R 66,2,14336
v_fma_f32 v96,v96,v77,v77
M 30,a[96:99],42
v_rcp_f32 v94,v94
M 30,a[108:111],46
L 3
v_rcp_f32 v95,v95
M 30,a[120:123],50
R 70,2,15360
v_fma_f32 v97,v97,v77,v77
M 30,a[132:135],54
s_waitcnt vmcnt(6)
D 0,56,3072
M 34,a[124:127],50
s_barrier
v_rcp_f32 v96,v96
M 34,a[136:139],54
s_add_u32 m0,s59,1024
R 42,2,16384
v_rcp_f32 v97,v97
M 38,a[128:131],50
s_add_u32 s56,s55,16384
v_mul_f32 v18,v18,v94
v_mul_f32 v19,v19,v95
M 38,a[140:143],54
L 3
v_mul_f32 v20,v20,v96
v_mul_f32 v21,v21,v97
M 38,a[152:155],58
s_add_u32 s55,s55,s60
R 46,2,17408
v_cvt_pk_bf16_f32 v106,v18,v19
M 38,a[164:167],62
v_cvt_pk_bf16_f32 v107,v20,v21
v_fma_f32 v22,-v74,v86,v22
M 34,a[148:151],58
v_fma_f32 v23,-v74,v87,v23
v_fma_f32 v24,-v74,v88,v24
M 34,a[160:163],62
R 50,2,18432
v_fma_f32 v25,-v74,v89,v25
v_mul_f32 v98,v22,v22
M 30,a[144:147],58
buffer_load_dword v8,s[36:39],s58 offen offset:0 sc1 lds
M 30,a[156:159],62
L 3
v_mul_f32 v99,v23,v23
v_mul_f32 v100,v24,v24
M 30,a[168:171],66
s_add_u32 m0,s57,16384
R 54,2,19456
v_mul_f32 v101,v25,v25
M 30,a[180:183],70
s_sub_u32 s57,s84,s57
v_fma_f32 v98,v76,v98,v75
v_fma_f32 v99,v76,v99,v75
M 34,a[172:175],66
v_fma_f32 v100,v76,v100,v75
v_fma_f32 v101,v76,v101,v75
M 34,a[184:187],70
R 58,2,20480
v_mul_f32 v98,v22,v98
M 38,a[176:179],66
D 0,56,0
v_mul_f32 v99,v23,v99
M 38,a[188:191],70
L 3
v_exp_f32 v98,v98
M 38,a[200:203],42
R 62,2,21504
v_mul_f32 v100,v24,v100
M 38,a[212:215],46
v_exp_f32 v99,v99
M 34,a[196:199],42
v_exp_f32 v100,v100
M 34,a[208:211],46
R 66,2,22528
v_mul_f32 v101,v25,v101
M 30,a[192:195],42
v_exp_f32 v101,v101
M 30,a[204:207],46
L 3
v_fma_f32 v98,v98,v77,v77
v_rcp_f32 v98,v98
M 30,a[216:219],50
R 70,2,23552
v_fma_f32 v99,v99,v77,v77
M 30,a[228:231],54
D 0,56,1024
M 34,a[220:223],50
v_rcp_f32 v99,v99
M 34,a[232:235],54
R 42,2,24576
v_fma_f32 v100,v100,v77,v77
M 38,a[224:227],50
v_rcp_f32 v100,v100
M 38,a[236:239],54
L 3
v_fma_f32 v101,v101,v77,v77
v_rcp_f32 v101,v101
M 38,a[248:251],58
R 46,2,25600
v_mul_f32 v22,v22,v98
M 38,v[132:135],62
v_mul_f32 v23,v23,v99
v_mul_f32 v24,v24,v100
M 34,a[244:247],58
v_mul_f32 v25,v25,v101
v_cvt_pk_bf16_f32 v108,v22,v23
M 34,v[128:131],62
R 50,2,26624
v_cvt_pk_bf16_f32 v109,v24,v25
M 30,a[240:243],58
buffer_store_dwordx4 v[106:109],v0,s[36:39],s66 offen offset:0 sc0 sc1
v_fma_f32 v26,-v74,v90,v26
M 30,a[252:255],62
L 3
v_fma_f32 v27,-v74,v91,v27
v_fma_f32 v28,-v74,v92,v28
M 30,v[136:139],66
R 54,2,27648
v_fma_f32 v29,-v74,v93,v29
v_mul_f32 v102,v26,v26
M 30,v[148:151],70
D 0,56,2048
M 34,v[140:143],66
v_mul_f32 v103,v27,v27
v_mul_f32 v104,v28,v28
M 34,v[152:155],70
R 58,2,28672
v_mul_f32 v105,v29,v29
M 38,v[144:147],66
v_fma_f32 v102,v76,v102,v75
v_fma_f32 v103,v76,v103,v75
M 38,v[156:159],70
L 3
v_fma_f32 v104,v76,v104,v75
v_fma_f32 v105,v76,v105,v75
M 38,v[168:171],42
R 62,2,29696
v_mul_f32 v102,v26,v102
M 38,v[180:183],46
v_exp_f32 v102,v102
v_mul_f32 v103,v27,v103
M 34,v[164:167],42
v_exp_f32 v103,v103
M 34,v[176:179],46
R 66,2,30720
v_mul_f32 v104,v28,v104
M 30,v[160:163],42
v_exp_f32 v104,v104
M 30,v[172:175],46
L 3
D 0,56,3072
M 30,v[184:187],50
s_nop 0
R 70,2,31744
v_mul_f32 v105,v29,v105
M 30,v[196:199],54
s_waitcnt vmcnt(5)
v_exp_f32 v105,v105
v_fma_f32 v102,v102,v77,v77
M 34,v[188:191],50
s_barrier
buffer_store_dword v4,v3,s[36:39],s70 offen offset:0 sc0 sc1
M 34,v[200:203],54
s_mov_b32 s70,s69
R 42,1,0
ds_read_b32 v7,v6 offset:1024
M 38,v[192:195],50
s_mov_b32 s69,s68
R 78,13,2048
v_fma_f32 v103,v103,v77,v77
M 38,v[204:207],54
L 5
v_rcp_f32 v102,v102
M 38,v[216:219],58
s_add_u32 s68,s68,s63
R 46,1,1024
v_fma_f32 v104,v104,v77,v77
M 38,v[228:231],62
v_rcp_f32 v103,v103
M 34,v[212:215],58
v_rcp_f32 v104,v104
v_fma_f32 v105,v105,v77,v77
M 34,v[224:227],62
L 2
R 50,1,2048
)"
R"(v_mul_f32 v26,v26,v102
M 30,v[208:211],58
v_rcp_f32 v105,v105
M 30,v[220:223],62
s_nop 0
v_mul_f32 v27,v27,v103
v_mul_f32 v28,v28,v104
M 30,v[232:235],66
R 54,1,3072
v_mul_f32 v29,v29,v105
M 30,v[244:247],70
v_cvt_pk_bf16_f32 v110,v26,v27
v_cvt_pk_bf16_f32 v111,v28,v29
M 34,v[236:239],66
v_cmp_eq_u32 vcc,s53,v7
M 34,v[248:251],70
s_cmp_eq_u32 vcc_lo,-1
R 58,1,4096
v_permlane16_swap_b32 v110,v112
v_permlane16_swap_b32 v111,v113
M 38,v[240:243],66
buffer_store_dwordx4 v[110:113],v10,s[36:39],s66 offen offset:0 sc0 sc1
M 38,v[252:255],70
L 3
s_cbranch_scc1 Lg3_F0_%=
Lg3_L0_%=:
v_mfma_f32_16x16x32_bf16 v[26:29],a[8:11],v[42:45],0
s_add_u32 m0,s59,3072
R 62,1,5120
v_add_f32_dpp v115,v114,v114 quad_perm:[1,0,3,2] row_mask:0xf bank_mask:0xf
M 26,a[20:23],46
s_mov_b32 s66,s65
buffer_load_dword v8,s[36:39],s58 offen offset:0 sc1 lds
v_mfma_f32_16x16x32_bf16 v[22:25],a[4:7],v[42:45],0
s_add_u32 s65,s65,s62
v_add_f32_dpp v115,v115,v115 quad_perm:[2,3,0,1] row_mask:0xf bank_mask:0xf
v_fma_f32 v30,-v78,v82,v30
M 22,a[16:19],46
R 66,1,6144
v_fma_f32 v31,-v78,v83,v31
v_mfma_f32_16x16x32_bf16 v[18:21],a[0:3],v[42:45],0
v_add_f32_dpp v118,v115,v115 row_half_mirror row_mask:0xf bank_mask:0xf
v_fma_f32 v32,-v78,v84,v32
v_fma_f32 v33,-v78,v85,v33
M 18,a[12:15],46
L 3
v_mul_f32 v94,v30,v30
v_mul_f32 v95,v31,v31
M 18,a[24:27],50
R 70,1,7168
v_mov_b32_dpp v115,v118 row_ror:8 row_mask:0xf bank_mask:0xf
M 18,a[36:39],54
v_fma_f32 v115,-v118,v118,v115
v_max_f32 v115,s87,v115
M 22,a[28:31],50
v_rsq_f32 v116,v115
v_mul_f32 v96,v32,v32
M 22,a[40:43],54
R 42,1,8192
v_mul_f32 v121,v115,v116
M 26,a[32:35],50
v_mul_f32 v119,s88,v116
v_mul_f32 v115,s89,v116
M 26,a[44:47],54
L 3
v_mul_f32 v120,v119,v116
v_mul_f32 v120,v120,v115
M 26,a[56:59],58
R 46,1,9216
v_mul_f32 v97,v33,v33
M 26,a[68:71],62
ds_write_b128 v12,v[118:121] offset:0
M 22,a[52:55],58
v_fma_f32 v94,v80,v94,v79
v_fma_f32 v95,v80,v95,v79
M 22,a[64:67],62
R 50,1,10240
v_fma_f32 v96,v80,v96,v79
M 18,a[48:51],58
v_fma_f32 v97,v80,v97,v79
v_mul_f32 v94,v30,v94
M 18,a[60:63],62
L 4
v_exp_f32 v94,v94
v_mul_f32 v95,v31,v95
M 18,a[72:75],66
R 54,1,11264
v_mul_f32 v96,v32,v96
M 18,a[84:87],70
v_exp_f32 v95,v95
M 22,a[76:79],66
v_exp_f32 v96,v96
M 22,a[88:91],70
R 58,1,12288
v_mul_f32 v97,v33,v97
M 26,a[80:83],66
v_exp_f32 v97,v97
v_fma_f32 v94,v94,v81,v81
M 26,a[92:95],70
L 4
v_rcp_f32 v94,v94
M 26,a[104:107],42
R 62,1,13312
v_fma_f32 v95,v95,v81,v81
M 26,a[116:119],46
v_rcp_f32 v95,v95
M 22,a[100:103],42
v_fma_f32 v96,v96,v81,v81
v_rcp_f32 v96,v96
M 22,a[112:115],46
R 66,1,14336
v_fma_f32 v97,v97,v81,v81
M 18,a[96:99],42
v_rcp_f32 v97,v97
M 18,a[108:111],46
L 3
v_mul_f32 v30,v30,v94
v_mul_f32 v31,v31,v95
M 18,a[120:123],50
R 70,1,15360
v_mul_f32 v32,v32,v96
M 18,a[132:135],54
s_waitcnt vmcnt(0)
v_mul_f32 v33,v33,v97
v_cvt_pk_bf16_f32 v106,v30,v31
v_cvt_pk_bf16_f32 v107,v32,v33
M 22,a[124:127],50
s_barrier
ds_read_b32 v7,v6 offset:3072
v_fma_f32 v34,-v78,v86,v34
M 22,a[136:139],54
R 42,1,16384
v_fma_f32 v35,-v78,v87,v35
M 26,a[128:131],50
buffer_store_dword v4,v3,s[36:39],s70 offen offset:0 sc0 sc1
M 26,a[140:143],54
L 4
v_fma_f32 v36,-v78,v88,v36
v_fma_f32 v37,-v78,v89,v37
v_mul_f32 v98,v34,v34
M 26,a[152:155],58
s_mov_b32 s70,0x7fff0000
R 46,1,17408
v_mul_f32 v99,v35,v35
M 26,a[164:167],62
v_mul_f32 v100,v36,v36
v_mul_f32 v101,v37,v37
M 22,a[148:151],58
L 2
v_cmp_eq_u32 vcc,s53,v7
v_fma_f32 v98,v80,v98,v79
M 22,a[160:163],62
R 50,1,18432
v_fma_f32 v99,v80,v99,v79
M 18,a[144:147],58
s_cmp_eq_u32 vcc_lo,-1
v_fma_f32 v100,v80,v100,v79
v_fma_f32 v101,v80,v101,v79
v_mul_f32 v98,v34,v98
s_cbranch_scc0 Lg3_X0_%=
M 18,a[156:159],62
s_nop 0
s_add_u32 m0,s57,0
s_add_u32 s56,s55,0
buffer_load_dword v114,v11,s[36:39],s73 offen offset:0 sc1
M 18,a[168:171],66
s_add_u32 s58,s58,s61
R 54,1,19456
v_mul_f32 v99,v35,v99
M 18,a[180:183],70
s_add_u32 s54,s54,s46
D 0,56,0
M 22,a[172:175],66
s_add_u32 s73,s73,s64
v_exp_f32 v98,v98
v_mul_f32 v100,v36,v100
M 22,a[184:187],70
R 58,1,20480
v_mul_f32 v101,v37,v101
M 26,a[176:179],66
v_exp_f32 v99,v99
M 26,a[188:191],70
L 3
D 0,56,1024
M 26,a[200:203],42
R 62,1,21504
v_exp_f32 v100,v100
M 26,a[212:215],46
v_exp_f32 v101,v101
M 22,a[196:199],42
v_fma_f32 v98,v98,v81,v81
v_fma_f32 v99,v99,v81,v81
M 22,a[208:211],46
R 66,1,22528
v_fma_f32 v100,v100,v81,v81
M 18,a[192:195],42
D 0,56,2048
M 18,a[204:207],46
L 3
v_rcp_f32 v98,v98
v_fma_f32 v101,v101,v81,v81
M 18,a[216:219],50
R 70,1,23552
v_mul_f32 v34,v34,v98
M 18,a[228:231],54
D 0,56,3072
M 22,a[220:223],50
s_add_u32 m0,s59,0
v_rcp_f32 v99,v99
M 22,a[232:235],54
s_add_u32 s56,s55,16384
R 42,1,24576
v_rcp_f32 v100,v100
M 26,a[224:227],50
s_add_u32 s55,s55,s60
buffer_load_dword v8,s[36:39],s58 offen offset:0 sc1 lds
M 26,a[236:239],54
L 3
v_rcp_f32 v101,v101
M 26,a[248:251],58
s_add_u32 m0,s57,16384
R 46,1,25600
v_mul_f32 v35,v35,v99
M 26,v[132:135],62
s_sub_u32 s57,s84,s57
D 0,56,0
M 22,a[244:247],58
v_mul_f32 v36,v36,v100
v_mul_f32 v37,v37,v101
v_cvt_pk_bf16_f32 v108,v34,v35
M 22,v[128:131],62
R 50,1,26624
v_cvt_pk_bf16_f32 v109,v36,v37
M 18,a[240:243],58
buffer_store_dwordx4 v[106:109],v0,s[36:39],s67 offen offset:0 sc0 sc1
v_fma_f32 v38,-v78,v90,v38
M 18,a[252:255],62
L 3
v_fma_f32 v39,-v78,v91,v39
v_fma_f32 v40,-v78,v92,v40
M 18,v[136:139],66
R 54,1,27648
v_fma_f32 v41,-v78,v93,v41
v_mul_f32 v102,v38,v38
M 18,v[148:151],70
D 0,56,1024
M 22,v[140:143],66
v_mul_f32 v103,v39,v39
v_mul_f32 v104,v40,v40
M 22,v[152:155],70
R 58,1,28672
v_mul_f32 v105,v41,v41
M 26,v[144:147],66
v_fma_f32 v102,v80,v102,v79
v_fma_f32 v103,v80,v103,v79
M 26,v[156:159],70
L 3
D 0,56,2048
v_fma_f32 v104,v80,v104,v79
M 26,v[168:171],42
R 62,1,29696
v_fma_f32 v105,v80,v105,v79
M 26,v[180:183],46
v_mul_f32 v102,v38,v102
v_mul_f32 v103,v39,v103
M 22,v[164:167],42
v_exp_f32 v102,v102
M 22,v[176:179],46
R 66,1,30720
v_exp_f32 v103,v103
M 18,v[160:163],42
D 0,56,3072
M 18,v[172:175],46
L 3
v_mul_f32 v104,v40,v104
v_mul_f32 v105,v41,v105
M 18,v[184:187],50
s_nop 0
R 70,1,31744
v_fma_f32 v102,v102,v81,v81
M 18,v[196:199],54
s_waitcnt vmcnt(5)
v_exp_f32 v104,v104
M 22,v[188:191],50
s_barrier
buffer_store_dword v4,v3,s[36:39],s70 offen offset:0 sc0 sc1
v_fma_f32 v103,v103,v81,v81
M 22,v[200:203],54
s_mov_b32 s70,s69
R 42,2,0
ds_read_b32 v7,v6 offset:0
M 26,v[192:195],50
s_mov_b32 s69,s68
R 74,13,0
v_fma_f32 v104,v104,v81,v81
M 26,v[204:207],54
L 5
v_exp_f32 v105,v105
M 26,v[216:219],58
s_add_u32 s68,s68,s63
R 46,2,1024
v_rcp_f32 v102,v102
M 26,v[228:231],62
v_rcp_f32 v103,v103
M 22,v[212:215],58
v_rcp_f32 v104,v104
M 22,v[224:227],62
L 2
R 50,2,2048
v_fma_f32 v105,v105,v81,v81
M 18,v[208:211],58
v_rcp_f32 v105,v105
v_mul_f32 v38,v38,v102
M 18,v[220:223],62
s_nop 0
v_mul_f32 v39,v39,v103
v_mul_f32 v40,v40,v104
M 18,v[232:235],66
R 54,2,3072
v_mul_f32 v41,v41,v105
M 18,v[244:247],70
v_cvt_pk_bf16_f32 v110,v38,v39
v_cvt_pk_bf16_f32 v111,v40,v41
M 22,v[236:239],66
v_cmp_eq_u32 vcc,s53,v7
M 22,v[248:251],70
s_cmp_eq_u32 vcc_lo,-1
R 58,2,4096
v_permlane16_swap_b32 v110,v112
v_permlane16_swap_b32 v111,v113
M 26,v[240:243],66
buffer_store_dwordx4 v[110:113],v10,s[36:39],s67 offen offset:0 sc0 sc1
M 26,v[252:255],70
L 3
s_cbranch_scc1 Lg3_F1_%=
s_branch Lg3_L1_%=
Lg3_X0_%=:
M 18,a[156:159],62
s_nop 0
s_add_u32 m0,s59,0
v_mul_f32 v99,v35,v99
M 18,a[168:171],66
R 54,1,19456
v_mul_f32 v100,v36,v100
M 18,a[180:183],70
buffer_load_dword v8,s[36:39],s58 offen offset:0 sc1 lds
M 22,a[172:175],66
v_exp_f32 v98,v98
M 22,a[184:187],70
R 58,1,20480
v_mul_f32 v101,v37,v101
M 26,a[176:179],66
v_exp_f32 v99,v99
M 26,a[188:191],70
L 3
v_exp_f32 v100,v100
M 26,a[200:203],42
R 62,1,21504
v_fma_f32 v98,v98,v81,v81
M 26,a[212:215],46
v_exp_f32 v101,v101
M 22,a[196:199],42
v_rcp_f32 v98,v98
M 22,a[208:211],46
R 66,1,22528
v_fma_f32 v99,v99,v81,v81
M 18,a[192:195],42
v_rcp_f32 v99,v99
M 18,a[204:207],46
L 3
v_fma_f32 v100,v100,v81,v81
v_fma_f32 v101,v101,v81,v81
M 18,a[216:219],50
R 70,1,23552
v_mul_f32 v34,v34,v98
M 18,a[228:231],54
v_rcp_f32 v100,v100
M 22,a[220:223],50
v_rcp_f32 v101,v101
M 22,a[232:235],54
R 42,1,24576
v_mul_f32 v35,v35,v99
M 26,a[224:227],50
v_mul_f32 v36,v36,v100
v_mul_f32 v37,v37,v101
M 26,a[236:239],54
L 3
v_cvt_pk_bf16_f32 v108,v34,v35
v_cvt_pk_bf16_f32 v109,v36,v37
M 26,a[248:251],58
R 46,1,25600
v_fma_f32 v38,-v78,v90,v38
M 26,v[132:135],62
buffer_store_dwordx4 v[106:109],v0,s[36:39],s67 offen offset:0 sc0 sc1
v_fma_f32 v39,-v78,v91,v39
M 22,a[244:247],58
v_fma_f32 v40,-v78,v92,v40
v_fma_f32 v41,-v78,v93,v41
M 22,v[128:131],62
R 50,1,26624
v_mul_f32 v102,v38,v38
M 18,a[240:243],58
v_mul_f32 v103,v39,v39
v_mul_f32 v104,v40,v40
M 18,a[252:255],62
L 3
v_mul_f32 v105,v41,v41
v_fma_f32 v102,v80,v102,v79
M 18,v[136:139],66
R 54,1,27648
v_fma_f32 v103,v80,v103,v79
M 18,v[148:151],70
v_fma_f32 v104,v80,v104,v79
v_fma_f32 v105,v80,v105,v79
M 22,v[140:143],66
v_mul_f32 v102,v38,v102
v_mul_f32 v103,v39,v103
M 22,v[152:155],70
R 58,1,28672
v_mul_f32 v104,v40,v104
M 26,v[144:147],66
v_exp_f32 v102,v102
M 26,v[156:159],70
L 3
v_exp_f32 v103,v103
M 26,v[168:171],42
R 62,1,29696
v_mul_f32 v105,v41,v105
M 26,v[180:183],46
v_exp_f32 v104,v104
M 22,v[164:167],42
v_exp_f32 v105,v105
M 22,v[176:179],46
R 66,1,30720
v_fma_f32 v102,v102,v81,v81
M 18,v[160:163],42
v_rcp_f32 v102,v102
M 18,v[172:175],46
L 3
v_fma_f32 v103,v103,v81,v81
v_fma_f32 v104,v104,v81,v81
M 18,v[184:187],50
s_nop 0
R 70,1,31744
v_fma_f32 v105,v105,v81,v81
M 18,v[196:199],54
s_waitcnt vmcnt(1)
v_rcp_f32 v103,v103
M 22,v[188:191],50
s_barrier
buffer_store_dword v4,v3,s[36:39],s70 offen offset:0 sc0 sc1
M 22,v[200:203],54
s_mov_b32 s70,s69
ds_read_b32 v7,v6 offset:0
R 74,13,0
M 26,v[192:195],50
s_mov_b32 s69,s68
v_rcp_f32 v104,v104
M 26,v[204:207],54
L 4
v_rcp_f32 v105,v105
M 26,v[216:219],58
s_add_u32 s68,s68,s63
v_mul_f32 v38,v38,v102
v_mul_f32 v39,v39,v103
M 26,v[228:231],62
v_mul_f32 v40,v40,v104
v_mul_f32 v41,v41,v105
M 22,v[212:215],58
v_cvt_pk_bf16_f32 v110,v38,v39
v_cvt_pk_bf16_f32 v111,v40,v41
M 22,v[224:227],62
L 1
v_cmp_eq_u32 vcc,s53,v7
M 18,v[208:211],58
v_permlane16_swap_b32 v110,v112
M 18,v[220:223],62
s_nop 0
v_permlane16_swap_b32 v111,v113
M 18,v[232:235],66
buffer_store_dwordx4 v[110:113],v10,s[36:39],s67 offen offset:0 sc0 sc1
M 18,v[244:247],70
M 22,v[236:239],66
M 22,v[248:251],70
M 26,v[240:243],66
M 26,v[252:255],70
s_mov_b32 s80,1
s_cmp_eq_u32 vcc_lo,-1
s_cbranch_scc0 Lg3_xn0_%=
buffer_load_dword v114,v11,s[36:39],s73 offen offset:0 sc1
L 0
v_fma_f32 v18,-v74,v82,v18
v_fma_f32 v19,-v74,v83,v19
v_fma_f32 v20,-v74,v84,v20
v_fma_f32 v21,-v74,v85,v21
v_mul_f32 v94,v18,v18
v_mul_f32 v95,v19,v19
v_mul_f32 v96,v20,v20
v_mul_f32 v97,v21,v21
s_add_u32 s73,s73,s64
)"
R"(s_add_u32 m0,s57,0
s_add_u32 s56,s55,0
D 0,56,0
v_fma_f32 v94,v76,v94,v75
v_fma_f32 v95,v76,v95,v75
v_fma_f32 v96,v76,v96,v75
v_fma_f32 v97,v76,v97,v75
v_mul_f32 v94,v18,v94
v_mul_f32 v95,v19,v95
v_mul_f32 v96,v20,v96
v_mul_f32 v97,v21,v97
D 0,56,1024
v_exp_f32 v94,v94
v_exp_f32 v95,v95
v_exp_f32 v96,v96
v_exp_f32 v97,v97
v_fma_f32 v94,v94,v77,v77
v_fma_f32 v95,v95,v77,v77
v_fma_f32 v96,v96,v77,v77
v_fma_f32 v97,v97,v77,v77
D 0,56,2048
v_rcp_f32 v94,v94
v_rcp_f32 v95,v95
v_rcp_f32 v96,v96
v_rcp_f32 v97,v97
v_mul_f32 v18,v18,v94
v_mul_f32 v19,v19,v95
v_mul_f32 v20,v20,v96
v_mul_f32 v21,v21,v97
D 0,56,3072
v_cvt_pk_bf16_f32 v106,v18,v19
v_cvt_pk_bf16_f32 v107,v20,v21
v_fma_f32 v22,-v74,v86,v22
v_fma_f32 v23,-v74,v87,v23
v_fma_f32 v24,-v74,v88,v24
v_fma_f32 v25,-v74,v89,v25
v_mul_f32 v98,v22,v22
v_mul_f32 v99,v23,v23
s_add_u32 m0,s57,16384
s_add_u32 s56,s55,16384
D 0,56,0
v_mul_f32 v100,v24,v24
v_mul_f32 v101,v25,v25
v_fma_f32 v98,v76,v98,v75
v_fma_f32 v99,v76,v99,v75
v_fma_f32 v100,v76,v100,v75
v_fma_f32 v101,v76,v101,v75
v_mul_f32 v98,v22,v98
v_mul_f32 v99,v23,v99
D 0,56,1024
v_mul_f32 v100,v24,v100
v_mul_f32 v101,v25,v101
v_exp_f32 v98,v98
v_exp_f32 v99,v99
v_exp_f32 v100,v100
v_exp_f32 v101,v101
v_fma_f32 v98,v98,v77,v77
v_fma_f32 v99,v99,v77,v77
D 0,56,2048
v_fma_f32 v100,v100,v77,v77
v_fma_f32 v101,v101,v77,v77
v_rcp_f32 v98,v98
v_rcp_f32 v99,v99
v_rcp_f32 v100,v100
v_rcp_f32 v101,v101
v_mul_f32 v22,v22,v98
v_mul_f32 v23,v23,v99
D 0,56,3072
v_mul_f32 v24,v24,v100
v_mul_f32 v25,v25,v101
v_cvt_pk_bf16_f32 v108,v22,v23
v_cvt_pk_bf16_f32 v109,v24,v25
buffer_store_dwordx4 v[106:109],v0,s[36:39],s66 offen offset:0 sc0 sc1
v_fma_f32 v26,-v74,v90,v26
v_fma_f32 v27,-v74,v91,v27
v_fma_f32 v28,-v74,v92,v28
v_fma_f32 v29,-v74,v93,v29
s_add_u32 s55,s55,s60
s_sub_u32 s57,s84,s57
s_add_u32 s54,s54,s46
v_mul_f32 v102,v26,v26
v_mul_f32 v103,v27,v27
v_mul_f32 v104,v28,v28
v_mul_f32 v105,v29,v29
v_fma_f32 v102,v76,v102,v75
v_fma_f32 v103,v76,v103,v75
v_fma_f32 v104,v76,v104,v75
v_fma_f32 v105,v76,v105,v75
v_mul_f32 v102,v26,v102
v_mul_f32 v103,v27,v103
v_mul_f32 v104,v28,v104
v_mul_f32 v105,v29,v105
v_exp_f32 v102,v102
v_exp_f32 v103,v103
v_exp_f32 v104,v104
v_exp_f32 v105,v105
v_fma_f32 v102,v102,v77,v77
v_fma_f32 v103,v103,v77,v77
v_fma_f32 v104,v104,v77,v77
v_fma_f32 v105,v105,v77,v77
v_rcp_f32 v102,v102
v_rcp_f32 v103,v103
v_rcp_f32 v104,v104
v_rcp_f32 v105,v105
v_mul_f32 v26,v26,v102
v_mul_f32 v27,v27,v103
v_mul_f32 v28,v28,v104
v_mul_f32 v29,v29,v105
v_cvt_pk_bf16_f32 v110,v26,v27
v_cvt_pk_bf16_f32 v111,v28,v29
s_nop 0
v_permlane16_swap_b32 v110,v112
v_permlane16_swap_b32 v111,v113
buffer_store_dwordx4 v[110:113],v10,s[36:39],s66 offen offset:0 sc0 sc1
s_mov_b32 s72,s69
s_mov_b32 s69,0x7fff0000
s_mov_b32 s66,0x7fff0000
s_mov_b32 s76,0x7fff0000
s_branch Lg3_gox_%=
Lg3_xn0_%=:
s_cmp_ge_u32 s54,256
s_cbranch_scc1 Lg3_fin0_%=
s_add_u32 m0,s59,4096
s_nop 0
buffer_load_dword v8,s[36:39],s58 offen offset:0 sc1 lds
buffer_store_dword v4,v3,s[36:39],s71 offen offset:0 sc0 sc1
s_add_u32 m0,s59,5120
s_nop 0
buffer_load_dword v8,s[36:39],s58 offen offset:0 sc1 lds
buffer_store_dword v4,v3,s[36:39],s71 offen offset:0 sc0 sc1
s_mov_b32 s83,0
L 0
v_fma_f32 v18,-v74,v82,v18
v_fma_f32 v19,-v74,v83,v19
v_fma_f32 v20,-v74,v84,v20
v_fma_f32 v21,-v74,v85,v21
v_mul_f32 v94,v18,v18
v_mul_f32 v95,v19,v19
v_mul_f32 v96,v20,v20
v_mul_f32 v97,v21,v21
v_fma_f32 v94,v76,v94,v75
v_fma_f32 v95,v76,v95,v75
v_fma_f32 v96,v76,v96,v75
v_fma_f32 v97,v76,v97,v75
v_mul_f32 v94,v18,v94
v_mul_f32 v95,v19,v95
v_mul_f32 v96,v20,v96
v_mul_f32 v97,v21,v97
v_exp_f32 v94,v94
v_exp_f32 v95,v95
v_exp_f32 v96,v96
v_exp_f32 v97,v97
v_fma_f32 v94,v94,v77,v77
v_fma_f32 v95,v95,v77,v77
v_fma_f32 v96,v96,v77,v77
v_fma_f32 v97,v97,v77,v77
v_rcp_f32 v94,v94
v_rcp_f32 v95,v95
v_rcp_f32 v96,v96
v_rcp_f32 v97,v97
v_mul_f32 v18,v18,v94
v_mul_f32 v19,v19,v95
v_mul_f32 v20,v20,v96
v_mul_f32 v21,v21,v97
v_cvt_pk_bf16_f32 v106,v18,v19
v_cvt_pk_bf16_f32 v107,v20,v21
v_fma_f32 v22,-v74,v86,v22
v_fma_f32 v23,-v74,v87,v23
v_fma_f32 v24,-v74,v88,v24
v_fma_f32 v25,-v74,v89,v25
v_mul_f32 v98,v22,v22
v_mul_f32 v99,v23,v23
v_mul_f32 v100,v24,v24
v_mul_f32 v101,v25,v25
v_fma_f32 v98,v76,v98,v75
v_fma_f32 v99,v76,v99,v75
v_fma_f32 v100,v76,v100,v75
v_fma_f32 v101,v76,v101,v75
v_mul_f32 v98,v22,v98
v_mul_f32 v99,v23,v99
v_mul_f32 v100,v24,v100
v_mul_f32 v101,v25,v101
v_exp_f32 v98,v98
v_exp_f32 v99,v99
v_exp_f32 v100,v100
v_exp_f32 v101,v101
v_fma_f32 v98,v98,v77,v77
v_fma_f32 v99,v99,v77,v77
v_fma_f32 v100,v100,v77,v77
v_fma_f32 v101,v101,v77,v77
v_rcp_f32 v98,v98
v_rcp_f32 v99,v99
v_rcp_f32 v100,v100
v_rcp_f32 v101,v101
v_mul_f32 v22,v22,v98
v_mul_f32 v23,v23,v99
v_mul_f32 v24,v24,v100
v_mul_f32 v25,v25,v101
v_cvt_pk_bf16_f32 v108,v22,v23
v_cvt_pk_bf16_f32 v109,v24,v25
buffer_store_dwordx4 v[106:109],v0,s[36:39],s66 offen offset:0 sc0 sc1
v_fma_f32 v26,-v74,v90,v26
v_fma_f32 v27,-v74,v91,v27
v_fma_f32 v28,-v74,v92,v28
v_fma_f32 v29,-v74,v93,v29
v_mul_f32 v102,v26,v26
v_mul_f32 v103,v27,v27
v_mul_f32 v104,v28,v28
v_mul_f32 v105,v29,v29
v_fma_f32 v102,v76,v102,v75
v_fma_f32 v103,v76,v103,v75
v_fma_f32 v104,v76,v104,v75
v_fma_f32 v105,v76,v105,v75
v_mul_f32 v102,v26,v102
v_mul_f32 v103,v27,v103
v_mul_f32 v104,v28,v104
v_mul_f32 v105,v29,v105
v_exp_f32 v102,v102
v_exp_f32 v103,v103
v_exp_f32 v104,v104
v_exp_f32 v105,v105
v_fma_f32 v102,v102,v77,v77
v_fma_f32 v103,v103,v77,v77
v_fma_f32 v104,v104,v77,v77
v_fma_f32 v105,v105,v77,v77
v_rcp_f32 v102,v102
v_rcp_f32 v103,v103
v_rcp_f32 v104,v104
v_rcp_f32 v105,v105
v_mul_f32 v26,v26,v102
v_mul_f32 v27,v27,v103
v_mul_f32 v28,v28,v104
v_mul_f32 v29,v29,v105
v_cvt_pk_bf16_f32 v110,v26,v27
v_cvt_pk_bf16_f32 v111,v28,v29
s_nop 0
v_permlane16_swap_b32 v110,v112
v_permlane16_swap_b32 v111,v113
buffer_store_dwordx4 v[110:113],v10,s[36:39],s66 offen offset:0 sc0 sc1
s_mov_b32 s72,s69
s_mov_b32 s69,0x7fff0000
s_mov_b32 s66,0x7fff0000
s_mov_b32 s76,0x7fff0000
s_branch Lg3_poll_%=
Lg3_fin0_%=:
L 0
v_fma_f32 v18,-v74,v82,v18
v_fma_f32 v19,-v74,v83,v19
v_fma_f32 v20,-v74,v84,v20
v_fma_f32 v21,-v74,v85,v21
v_mul_f32 v94,v18,v18
v_mul_f32 v95,v19,v19
v_mul_f32 v96,v20,v20
v_mul_f32 v97,v21,v21
v_fma_f32 v94,v76,v94,v75
v_fma_f32 v95,v76,v95,v75
v_fma_f32 v96,v76,v96,v75
v_fma_f32 v97,v76,v97,v75
v_mul_f32 v94,v18,v94
v_mul_f32 v95,v19,v95
v_mul_f32 v96,v20,v96
v_mul_f32 v97,v21,v97
v_exp_f32 v94,v94
v_exp_f32 v95,v95
v_exp_f32 v96,v96
v_exp_f32 v97,v97
v_fma_f32 v94,v94,v77,v77
v_fma_f32 v95,v95,v77,v77
v_fma_f32 v96,v96,v77,v77
v_fma_f32 v97,v97,v77,v77
v_rcp_f32 v94,v94
v_rcp_f32 v95,v95
v_rcp_f32 v96,v96
v_rcp_f32 v97,v97
v_mul_f32 v18,v18,v94
v_mul_f32 v19,v19,v95
v_mul_f32 v20,v20,v96
v_mul_f32 v21,v21,v97
v_cvt_pk_bf16_f32 v106,v18,v19
v_cvt_pk_bf16_f32 v107,v20,v21
v_fma_f32 v22,-v74,v86,v22
v_fma_f32 v23,-v74,v87,v23
v_fma_f32 v24,-v74,v88,v24
v_fma_f32 v25,-v74,v89,v25
v_mul_f32 v98,v22,v22
v_mul_f32 v99,v23,v23
v_mul_f32 v100,v24,v24
v_mul_f32 v101,v25,v25
v_fma_f32 v98,v76,v98,v75
v_fma_f32 v99,v76,v99,v75
v_fma_f32 v100,v76,v100,v75
v_fma_f32 v101,v76,v101,v75
v_mul_f32 v98,v22,v98
v_mul_f32 v99,v23,v99
v_mul_f32 v100,v24,v100
v_mul_f32 v101,v25,v101
v_exp_f32 v98,v98
v_exp_f32 v99,v99
v_exp_f32 v100,v100
v_exp_f32 v101,v101
v_fma_f32 v98,v98,v77,v77
v_fma_f32 v99,v99,v77,v77
v_fma_f32 v100,v100,v77,v77
v_fma_f32 v101,v101,v77,v77
v_rcp_f32 v98,v98
v_rcp_f32 v99,v99
v_rcp_f32 v100,v100
v_rcp_f32 v101,v101
v_mul_f32 v22,v22,v98
v_mul_f32 v23,v23,v99
v_mul_f32 v24,v24,v100
v_mul_f32 v25,v25,v101
v_cvt_pk_bf16_f32 v108,v22,v23
v_cvt_pk_bf16_f32 v109,v24,v25
buffer_store_dwordx4 v[106:109],v0,s[36:39],s66 offen offset:0 sc0 sc1
v_fma_f32 v26,-v74,v90,v26
v_fma_f32 v27,-v74,v91,v27
v_fma_f32 v28,-v74,v92,v28
v_fma_f32 v29,-v74,v93,v29
v_mul_f32 v102,v26,v26
v_mul_f32 v103,v27,v27
v_mul_f32 v104,v28,v28
v_mul_f32 v105,v29,v29
v_fma_f32 v102,v76,v102,v75
v_fma_f32 v103,v76,v103,v75
v_fma_f32 v104,v76,v104,v75
v_fma_f32 v105,v76,v105,v75
v_mul_f32 v102,v26,v102
v_mul_f32 v103,v27,v103
v_mul_f32 v104,v28,v104
v_mul_f32 v105,v29,v105
v_exp_f32 v102,v102
v_exp_f32 v103,v103
v_exp_f32 v104,v104
v_exp_f32 v105,v105
v_fma_f32 v102,v102,v77,v77
v_fma_f32 v103,v103,v77,v77
v_fma_f32 v104,v104,v77,v77
v_fma_f32 v105,v105,v77,v77
v_rcp_f32 v102,v102
v_rcp_f32 v103,v103
v_rcp_f32 v104,v104
v_rcp_f32 v105,v105
v_mul_f32 v26,v26,v102
v_mul_f32 v27,v27,v103
v_mul_f32 v28,v28,v104
v_mul_f32 v29,v29,v105
v_cvt_pk_bf16_f32 v110,v26,v27
v_cvt_pk_bf16_f32 v111,v28,v29
s_nop 0
v_permlane16_swap_b32 v110,v112
v_permlane16_swap_b32 v111,v113
buffer_store_dwordx4 v[110:113],v10,s[36:39],s66 offen offset:0 sc0 sc1
s_mov_b32 s72,s69
s_mov_b32 s69,0x7fff0000
s_mov_b32 s66,0x7fff0000
s_mov_b32 s76,0x7fff0000
s_branch Lg3_done_%=
Lg3_L1_%=:
v_mfma_f32_16x16x32_bf16 v[38:41],a[8:11],v[42:45],0
s_add_u32 m0,s59,3072
R 62,2,5120
v_add_f32_dpp v115,v114,v114 quad_perm:[1,0,3,2] row_mask:0xf bank_mask:0xf
M 38,a[20:23],46
s_mov_b32 s67,s65
buffer_load_dword v8,s[36:39],s58 offen offset:0 sc1 lds
v_mfma_f32_16x16x32_bf16 v[34:37],a[4:7],v[42:45],0
s_add_u32 s65,s65,s62
v_add_f32_dpp v115,v115,v115 quad_perm:[2,3,0,1] row_mask:0xf bank_mask:0xf
v_fma_f32 v18,-v74,v82,v18
M 34,a[16:19],46
R 66,2,6144
v_fma_f32 v19,-v74,v83,v19
v_mfma_f32_16x16x32_bf16 v[30:33],a[0:3],v[42:45],0
v_add_f32_dpp v118,v115,v115 row_half_mirror row_mask:0xf bank_mask:0xf
v_fma_f32 v20,-v74,v84,v20
v_fma_f32 v21,-v74,v85,v21
M 30,a[12:15],46
L 3
v_mul_f32 v94,v18,v18
v_mul_f32 v95,v19,v19
M 30,a[24:27],50
R 70,2,7168
v_mov_b32_dpp v115,v118 row_ror:8 row_mask:0xf bank_mask:0xf
M 30,a[36:39],54
v_fma_f32 v115,-v118,v118,v115
v_max_f32 v115,s87,v115
M 34,a[28:31],50
v_rsq_f32 v116,v115
v_mul_f32 v96,v20,v20
M 34,a[40:43],54
R 42,2,8192
v_mul_f32 v121,v115,v116
M 38,a[32:35],50
v_mul_f32 v119,s88,v116
v_mul_f32 v115,s89,v116
M 38,a[44:47],54
L 3
v_mul_f32 v120,v119,v116
v_mul_f32 v120,v120,v115
M 38,a[56:59],58
R 46,2,9216
v_mul_f32 v97,v21,v21
M 38,a[68:71],62
ds_write_b128 v12,v[118:121] offset:2048
M 34,a[52:55],58
v_fma_f32 v94,v76,v94,v75
v_fma_f32 v95,v76,v95,v75
M 34,a[64:67],62
R 50,2,10240
v_fma_f32 v96,v76,v96,v75
M 30,a[48:51],58
v_fma_f32 v97,v76,v97,v75
v_mul_f32 v94,v18,v94
M 30,a[60:63],62
L 4
v_exp_f32 v94,v94
v_mul_f32 v95,v19,v95
M 30,a[72:75],66
R 54,2,11264
v_mul_f32 v96,v20,v96
M 30,a[84:87],70
v_exp_f32 v95,v95
M 34,a[76:79],66
v_exp_f32 v96,v96
M 34,a[88:91],70
R 58,2,12288
v_mul_f32 v97,v21,v97
M 38,a[80:83],66
v_exp_f32 v97,v97
v_fma_f32 v94,v94,v77,v77
M 38,a[92:95],70
L 4
v_rcp_f32 v94,v94
M 38,a[104:107],42
R 62,2,13312
v_fma_f32 v95,v95,v77,v77
M 38,a[116:119],46
v_rcp_f32 v95,v95
M 34,a[100:103],42
v_fma_f32 v96,v96,v77,v77
v_rcp_f32 v96,v96
M 34,a[112:115],46
R 66,2,14336
v_fma_f32 v97,v97,v77,v77
M 30,a[96:99],42
v_rcp_f32 v97,v97
M 30,a[108:111],46
L 3
v_mul_f32 v18,v18,v94
v_mul_f32 v19,v19,v95
M 30,a[120:123],50
R 70,2,15360
v_mul_f32 v20,v20,v96
M 30,a[132:135],54
s_waitcnt vmcnt(0)
v_mul_f32 v21,v21,v97
v_cvt_pk_bf16_f32 v106,v18,v19
v_cvt_pk_bf16_f32 v107,v20,v21
M 34,a[124:127],50
s_barrier
ds_read_b32 v7,v6 offset:3072
v_fma_f32 v22,-v74,v86,v22
M 34,a[136:139],54
R 42,2,16384
v_fma_f32 v23,-v74,v87,v23
M 38,a[128:131],50
buffer_store_dword v4,v3,s[36:39],s70 offen offset:0 sc0 sc1
M 38,a[140:143],54
L 4
v_fma_f32 v24,-v74,v88,v24
v_fma_f32 v25,-v74,v89,v25
v_mul_f32 v98,v22,v22
M 38,a[152:155],58
s_mov_b32 s70,0x7fff0000
R 46,2,17408
v_mul_f32 v99,v23,v23
M 38,a[164:167],62
v_mul_f32 v100,v24,v24
v_mul_f32 v101,v25,v25
M 34,a[148:151],58
L 2
v_cmp_eq_u32 vcc,s53,v7
v_fma_f32 v98,v76,v98,v75
M 34,a[160:163],62
R 50,2,18432
v_fma_f32 v99,v76,v99,v75
M 30,a[144:147],58
s_cmp_eq_u32 vcc_lo,-1
v_fma_f32 v100,v76,v100,v75
v_fma_f32 v101,v76,v101,v75
v_mul_f32 v98,v22,v98
s_cbranch_scc0 Lg3_X1_%=
M 30,a[156:159],62
s_nop 0
s_add_u32 m0,s57,0
s_add_u32 s56,s55,0
buffer_load_dword v114,v11,s[36:39],s73 offen offset:0 sc1
M 30,a[168:171],66
s_add_u32 s58,s58,s61
)"
R"(R 54,2,19456
v_mul_f32 v99,v23,v99
M 30,a[180:183],70
s_add_u32 s54,s54,s46
D 0,56,0
M 34,a[172:175],66
s_add_u32 s73,s73,s64
v_exp_f32 v98,v98
v_mul_f32 v100,v24,v100
M 34,a[184:187],70
R 58,2,20480
v_mul_f32 v101,v25,v101
M 38,a[176:179],66
v_exp_f32 v99,v99
M 38,a[188:191],70
L 3
D 0,56,1024
M 38,a[200:203],42
R 62,2,21504
v_exp_f32 v100,v100
M 38,a[212:215],46
v_exp_f32 v101,v101
M 34,a[196:199],42
v_fma_f32 v98,v98,v77,v77
v_fma_f32 v99,v99,v77,v77
M 34,a[208:211],46
R 66,2,22528
v_fma_f32 v100,v100,v77,v77
M 30,a[192:195],42
D 0,56,2048
M 30,a[204:207],46
L 3
v_rcp_f32 v98,v98
v_fma_f32 v101,v101,v77,v77
M 30,a[216:219],50
R 70,2,23552
v_mul_f32 v22,v22,v98
M 30,a[228:231],54
D 0,56,3072
M 34,a[220:223],50
s_add_u32 m0,s59,1024
v_rcp_f32 v99,v99
M 34,a[232:235],54
s_add_u32 s56,s55,16384
R 42,2,24576
v_rcp_f32 v100,v100
M 38,a[224:227],50
s_add_u32 s55,s55,s60
buffer_load_dword v8,s[36:39],s58 offen offset:0 sc1 lds
M 38,a[236:239],54
L 3
v_rcp_f32 v101,v101
M 38,a[248:251],58
s_add_u32 m0,s57,16384
R 46,2,25600
v_mul_f32 v23,v23,v99
M 38,v[132:135],62
s_sub_u32 s57,s84,s57
D 0,56,0
M 34,a[244:247],58
v_mul_f32 v24,v24,v100
v_mul_f32 v25,v25,v101
v_cvt_pk_bf16_f32 v108,v22,v23
M 34,v[128:131],62
R 50,2,26624
v_cvt_pk_bf16_f32 v109,v24,v25
M 30,a[240:243],58
buffer_store_dwordx4 v[106:109],v0,s[36:39],s66 offen offset:0 sc0 sc1
v_fma_f32 v26,-v74,v90,v26
M 30,a[252:255],62
L 3
v_fma_f32 v27,-v74,v91,v27
v_fma_f32 v28,-v74,v92,v28
M 30,v[136:139],66
R 54,2,27648
v_fma_f32 v29,-v74,v93,v29
v_mul_f32 v102,v26,v26
M 30,v[148:151],70
D 0,56,1024
M 34,v[140:143],66
v_mul_f32 v103,v27,v27
v_mul_f32 v104,v28,v28
M 34,v[152:155],70
R 58,2,28672
v_mul_f32 v105,v29,v29
M 38,v[144:147],66
v_fma_f32 v102,v76,v102,v75
v_fma_f32 v103,v76,v103,v75
M 38,v[156:159],70
L 3
D 0,56,2048
v_fma_f32 v104,v76,v104,v75
M 38,v[168:171],42
R 62,2,29696
v_fma_f32 v105,v76,v105,v75
M 38,v[180:183],46
v_mul_f32 v102,v26,v102
v_mul_f32 v103,v27,v103
M 34,v[164:167],42
v_exp_f32 v102,v102
M 34,v[176:179],46
R 66,2,30720
v_exp_f32 v103,v103
M 30,v[160:163],42
D 0,56,3072
M 30,v[172:175],46
L 3
v_mul_f32 v104,v28,v104
v_mul_f32 v105,v29,v105
M 30,v[184:187],50
s_nop 0
R 70,2,31744
v_fma_f32 v102,v102,v77,v77
M 30,v[196:199],54
s_waitcnt vmcnt(5)
v_exp_f32 v104,v104
M 34,v[188:191],50
s_barrier
buffer_store_dword v4,v3,s[36:39],s70 offen offset:0 sc0 sc1
v_fma_f32 v103,v103,v77,v77
M 34,v[200:203],54
s_mov_b32 s70,s69
R 42,1,0
ds_read_b32 v7,v6 offset:1024
M 38,v[192:195],50
s_mov_b32 s69,s68
R 78,13,2048
v_fma_f32 v104,v104,v77,v77
M 38,v[204:207],54
L 5
v_exp_f32 v105,v105
M 38,v[216:219],58
s_add_u32 s68,s68,s63
R 46,1,1024
v_rcp_f32 v102,v102
M 38,v[228:231],62
v_rcp_f32 v103,v103
M 34,v[212:215],58
v_rcp_f32 v104,v104
M 34,v[224:227],62
L 2
R 50,1,2048
v_fma_f32 v105,v105,v77,v77
M 30,v[208:211],58
v_rcp_f32 v105,v105
v_mul_f32 v26,v26,v102
M 30,v[220:223],62
s_nop 0
v_mul_f32 v27,v27,v103
v_mul_f32 v28,v28,v104
M 30,v[232:235],66
R 54,1,3072
v_mul_f32 v29,v29,v105
M 30,v[244:247],70
v_cvt_pk_bf16_f32 v110,v26,v27
v_cvt_pk_bf16_f32 v111,v28,v29
M 34,v[236:239],66
v_cmp_eq_u32 vcc,s53,v7
M 34,v[248:251],70
s_cmp_eq_u32 vcc_lo,-1
R 58,1,4096
v_permlane16_swap_b32 v110,v112
v_permlane16_swap_b32 v111,v113
M 38,v[240:243],66
buffer_store_dwordx4 v[110:113],v10,s[36:39],s66 offen offset:0 sc0 sc1
M 38,v[252:255],70
L 3
s_cbranch_scc1 Lg3_F0_%=
s_branch Lg3_L0_%=
Lg3_X1_%=:
M 30,a[156:159],62
s_nop 0
s_add_u32 m0,s59,1024
v_mul_f32 v99,v23,v99
M 30,a[168:171],66
R 54,2,19456
v_mul_f32 v100,v24,v100
M 30,a[180:183],70
buffer_load_dword v8,s[36:39],s58 offen offset:0 sc1 lds
M 34,a[172:175],66
v_exp_f32 v98,v98
M 34,a[184:187],70
R 58,2,20480
v_mul_f32 v101,v25,v101
M 38,a[176:179],66
v_exp_f32 v99,v99
M 38,a[188:191],70
L 3
v_exp_f32 v100,v100
M 38,a[200:203],42
R 62,2,21504
v_fma_f32 v98,v98,v77,v77
M 38,a[212:215],46
v_exp_f32 v101,v101
M 34,a[196:199],42
v_rcp_f32 v98,v98
M 34,a[208:211],46
R 66,2,22528
v_fma_f32 v99,v99,v77,v77
M 30,a[192:195],42
v_rcp_f32 v99,v99
M 30,a[204:207],46
L 3
v_fma_f32 v100,v100,v77,v77
v_fma_f32 v101,v101,v77,v77
M 30,a[216:219],50
R 70,2,23552
v_mul_f32 v22,v22,v98
M 30,a[228:231],54
v_rcp_f32 v100,v100
M 34,a[220:223],50
v_rcp_f32 v101,v101
M 34,a[232:235],54
R 42,2,24576
v_mul_f32 v23,v23,v99
M 38,a[224:227],50
v_mul_f32 v24,v24,v100
v_mul_f32 v25,v25,v101
M 38,a[236:239],54
L 3
v_cvt_pk_bf16_f32 v108,v22,v23
v_cvt_pk_bf16_f32 v109,v24,v25
M 38,a[248:251],58
R 46,2,25600
v_fma_f32 v26,-v74,v90,v26
M 38,v[132:135],62
buffer_store_dwordx4 v[106:109],v0,s[36:39],s66 offen offset:0 sc0 sc1
v_fma_f32 v27,-v74,v91,v27
M 34,a[244:247],58
v_fma_f32 v28,-v74,v92,v28
v_fma_f32 v29,-v74,v93,v29
M 34,v[128:131],62
R 50,2,26624
v_mul_f32 v102,v26,v26
M 30,a[240:243],58
v_mul_f32 v103,v27,v27
v_mul_f32 v104,v28,v28
M 30,a[252:255],62
L 3
v_mul_f32 v105,v29,v29
v_fma_f32 v102,v76,v102,v75
M 30,v[136:139],66
R 54,2,27648
v_fma_f32 v103,v76,v103,v75
M 30,v[148:151],70
v_fma_f32 v104,v76,v104,v75
v_fma_f32 v105,v76,v105,v75
M 34,v[140:143],66
v_mul_f32 v102,v26,v102
v_mul_f32 v103,v27,v103
M 34,v[152:155],70
R 58,2,28672
v_mul_f32 v104,v28,v104
M 38,v[144:147],66
v_exp_f32 v102,v102
M 38,v[156:159],70
L 3
v_exp_f32 v103,v103
M 38,v[168:171],42
R 62,2,29696
v_mul_f32 v105,v29,v105
M 38,v[180:183],46
v_exp_f32 v104,v104
M 34,v[164:167],42
v_exp_f32 v105,v105
M 34,v[176:179],46
R 66,2,30720
v_fma_f32 v102,v102,v77,v77
M 30,v[160:163],42
v_rcp_f32 v102,v102
M 30,v[172:175],46
L 3
v_fma_f32 v103,v103,v77,v77
v_fma_f32 v104,v104,v77,v77
M 30,v[184:187],50
s_nop 0
R 70,2,31744
v_fma_f32 v105,v105,v77,v77
M 30,v[196:199],54
s_waitcnt vmcnt(1)
v_rcp_f32 v103,v103
M 34,v[188:191],50
s_barrier
buffer_store_dword v4,v3,s[36:39],s70 offen offset:0 sc0 sc1
M 34,v[200:203],54
s_mov_b32 s70,s69
ds_read_b32 v7,v6 offset:1024
R 78,13,2048
M 38,v[192:195],50
s_mov_b32 s69,s68
v_rcp_f32 v104,v104
M 38,v[204:207],54
L 4
v_rcp_f32 v105,v105
M 38,v[216:219],58
s_add_u32 s68,s68,s63
v_mul_f32 v26,v26,v102
v_mul_f32 v27,v27,v103
M 38,v[228:231],62
v_mul_f32 v28,v28,v104
v_mul_f32 v29,v29,v105
M 34,v[212:215],58
v_cvt_pk_bf16_f32 v110,v26,v27
v_cvt_pk_bf16_f32 v111,v28,v29
M 34,v[224:227],62
L 1
v_cmp_eq_u32 vcc,s53,v7
M 30,v[208:211],58
v_permlane16_swap_b32 v110,v112
M 30,v[220:223],62
s_nop 0
v_permlane16_swap_b32 v111,v113
M 30,v[232:235],66
buffer_store_dwordx4 v[110:113],v10,s[36:39],s66 offen offset:0 sc0 sc1
M 30,v[244:247],70
M 34,v[236:239],66
M 34,v[248:251],70
M 38,v[240:243],66
M 38,v[252:255],70
s_mov_b32 s80,0
s_cmp_eq_u32 vcc_lo,-1
s_cbranch_scc0 Lg3_xn1_%=
buffer_load_dword v114,v11,s[36:39],s73 offen offset:0 sc1
L 0
v_fma_f32 v30,-v78,v82,v30
v_fma_f32 v31,-v78,v83,v31
v_fma_f32 v32,-v78,v84,v32
v_fma_f32 v33,-v78,v85,v33
v_mul_f32 v94,v30,v30
v_mul_f32 v95,v31,v31
v_mul_f32 v96,v32,v32
v_mul_f32 v97,v33,v33
s_add_u32 s73,s73,s64
s_add_u32 m0,s57,0
s_add_u32 s56,s55,0
D 0,56,0
v_fma_f32 v94,v80,v94,v79
v_fma_f32 v95,v80,v95,v79
v_fma_f32 v96,v80,v96,v79
v_fma_f32 v97,v80,v97,v79
v_mul_f32 v94,v30,v94
v_mul_f32 v95,v31,v95
v_mul_f32 v96,v32,v96
v_mul_f32 v97,v33,v97
D 0,56,1024
v_exp_f32 v94,v94
v_exp_f32 v95,v95
v_exp_f32 v96,v96
v_exp_f32 v97,v97
v_fma_f32 v94,v94,v81,v81
v_fma_f32 v95,v95,v81,v81
v_fma_f32 v96,v96,v81,v81
v_fma_f32 v97,v97,v81,v81
D 0,56,2048
v_rcp_f32 v94,v94
v_rcp_f32 v95,v95
v_rcp_f32 v96,v96
v_rcp_f32 v97,v97
v_mul_f32 v30,v30,v94
v_mul_f32 v31,v31,v95
v_mul_f32 v32,v32,v96
v_mul_f32 v33,v33,v97
D 0,56,3072
v_cvt_pk_bf16_f32 v106,v30,v31
v_cvt_pk_bf16_f32 v107,v32,v33
v_fma_f32 v34,-v78,v86,v34
v_fma_f32 v35,-v78,v87,v35
v_fma_f32 v36,-v78,v88,v36
v_fma_f32 v37,-v78,v89,v37
v_mul_f32 v98,v34,v34
v_mul_f32 v99,v35,v35
s_add_u32 m0,s57,16384
s_add_u32 s56,s55,16384
D 0,56,0
v_mul_f32 v100,v36,v36
v_mul_f32 v101,v37,v37
v_fma_f32 v98,v80,v98,v79
v_fma_f32 v99,v80,v99,v79
v_fma_f32 v100,v80,v100,v79
v_fma_f32 v101,v80,v101,v79
v_mul_f32 v98,v34,v98
v_mul_f32 v99,v35,v99
D 0,56,1024
v_mul_f32 v100,v36,v100
v_mul_f32 v101,v37,v101
v_exp_f32 v98,v98
v_exp_f32 v99,v99
v_exp_f32 v100,v100
v_exp_f32 v101,v101
v_fma_f32 v98,v98,v81,v81
v_fma_f32 v99,v99,v81,v81
D 0,56,2048
v_fma_f32 v100,v100,v81,v81
v_fma_f32 v101,v101,v81,v81
v_rcp_f32 v98,v98
v_rcp_f32 v99,v99
v_rcp_f32 v100,v100
v_rcp_f32 v101,v101
v_mul_f32 v34,v34,v98
v_mul_f32 v35,v35,v99
D 0,56,3072
v_mul_f32 v36,v36,v100
v_mul_f32 v37,v37,v101
v_cvt_pk_bf16_f32 v108,v34,v35
v_cvt_pk_bf16_f32 v109,v36,v37
buffer_store_dwordx4 v[106:109],v0,s[36:39],s67 offen offset:0 sc0 sc1
v_fma_f32 v38,-v78,v90,v38
v_fma_f32 v39,-v78,v91,v39
v_fma_f32 v40,-v78,v92,v40
v_fma_f32 v41,-v78,v93,v41
s_add_u32 s55,s55,s60
s_sub_u32 s57,s84,s57
s_add_u32 s54,s54,s46
v_mul_f32 v102,v38,v38
v_mul_f32 v103,v39,v39
v_mul_f32 v104,v40,v40
v_mul_f32 v105,v41,v41
v_fma_f32 v102,v80,v102,v79
v_fma_f32 v103,v80,v103,v79
v_fma_f32 v104,v80,v104,v79
v_fma_f32 v105,v80,v105,v79
v_mul_f32 v102,v38,v102
v_mul_f32 v103,v39,v103
v_mul_f32 v104,v40,v104
v_mul_f32 v105,v41,v105
v_exp_f32 v102,v102
v_exp_f32 v103,v103
v_exp_f32 v104,v104
v_exp_f32 v105,v105
v_fma_f32 v102,v102,v81,v81
v_fma_f32 v103,v103,v81,v81
v_fma_f32 v104,v104,v81,v81
v_fma_f32 v105,v105,v81,v81
v_rcp_f32 v102,v102
v_rcp_f32 v103,v103
v_rcp_f32 v104,v104
v_rcp_f32 v105,v105
v_mul_f32 v38,v38,v102
v_mul_f32 v39,v39,v103
v_mul_f32 v40,v40,v104
v_mul_f32 v41,v41,v105
v_cvt_pk_bf16_f32 v110,v38,v39
v_cvt_pk_bf16_f32 v111,v40,v41
s_nop 0
v_permlane16_swap_b32 v110,v112
v_permlane16_swap_b32 v111,v113
buffer_store_dwordx4 v[110:113],v10,s[36:39],s67 offen offset:0 sc0 sc1
s_mov_b32 s72,s69
s_mov_b32 s69,0x7fff0000
s_mov_b32 s67,0x7fff0000
s_mov_b32 s77,0x7fff0000
s_branch Lg3_gox_%=
Lg3_xn1_%=:
s_cmp_ge_u32 s54,256
s_cbranch_scc1 Lg3_fin1_%=
s_add_u32 m0,s59,4096
s_nop 0
buffer_load_dword v8,s[36:39],s58 offen offset:0 sc1 lds
buffer_store_dword v4,v3,s[36:39],s71 offen offset:0 sc0 sc1
s_add_u32 m0,s59,5120
s_nop 0
buffer_load_dword v8,s[36:39],s58 offen offset:0 sc1 lds
buffer_store_dword v4,v3,s[36:39],s71 offen offset:0 sc0 sc1
s_mov_b32 s83,0
L 0
v_fma_f32 v30,-v78,v82,v30
v_fma_f32 v31,-v78,v83,v31
v_fma_f32 v32,-v78,v84,v32
v_fma_f32 v33,-v78,v85,v33
v_mul_f32 v94,v30,v30
v_mul_f32 v95,v31,v31
v_mul_f32 v96,v32,v32
v_mul_f32 v97,v33,v33
v_fma_f32 v94,v80,v94,v79
v_fma_f32 v95,v80,v95,v79
v_fma_f32 v96,v80,v96,v79
v_fma_f32 v97,v80,v97,v79
v_mul_f32 v94,v30,v94
v_mul_f32 v95,v31,v95
v_mul_f32 v96,v32,v96
v_mul_f32 v97,v33,v97
v_exp_f32 v94,v94
v_exp_f32 v95,v95
v_exp_f32 v96,v96
v_exp_f32 v97,v97
v_fma_f32 v94,v94,v81,v81
v_fma_f32 v95,v95,v81,v81
v_fma_f32 v96,v96,v81,v81
v_fma_f32 v97,v97,v81,v81
v_rcp_f32 v94,v94
v_rcp_f32 v95,v95
v_rcp_f32 v96,v96
v_rcp_f32 v97,v97
v_mul_f32 v30,v30,v94
v_mul_f32 v31,v31,v95
v_mul_f32 v32,v32,v96
v_mul_f32 v33,v33,v97
v_cvt_pk_bf16_f32 v106,v30,v31
v_cvt_pk_bf16_f32 v107,v32,v33
v_fma_f32 v34,-v78,v86,v34
)"
R"(v_fma_f32 v35,-v78,v87,v35
v_fma_f32 v36,-v78,v88,v36
v_fma_f32 v37,-v78,v89,v37
v_mul_f32 v98,v34,v34
v_mul_f32 v99,v35,v35
v_mul_f32 v100,v36,v36
v_mul_f32 v101,v37,v37
v_fma_f32 v98,v80,v98,v79
v_fma_f32 v99,v80,v99,v79
v_fma_f32 v100,v80,v100,v79
v_fma_f32 v101,v80,v101,v79
v_mul_f32 v98,v34,v98
v_mul_f32 v99,v35,v99
v_mul_f32 v100,v36,v100
v_mul_f32 v101,v37,v101
v_exp_f32 v98,v98
v_exp_f32 v99,v99
v_exp_f32 v100,v100
v_exp_f32 v101,v101
v_fma_f32 v98,v98,v81,v81
v_fma_f32 v99,v99,v81,v81
v_fma_f32 v100,v100,v81,v81
v_fma_f32 v101,v101,v81,v81
v_rcp_f32 v98,v98
v_rcp_f32 v99,v99
v_rcp_f32 v100,v100
v_rcp_f32 v101,v101
v_mul_f32 v34,v34,v98
v_mul_f32 v35,v35,v99
v_mul_f32 v36,v36,v100
v_mul_f32 v37,v37,v101
v_cvt_pk_bf16_f32 v108,v34,v35
v_cvt_pk_bf16_f32 v109,v36,v37
buffer_store_dwordx4 v[106:109],v0,s[36:39],s67 offen offset:0 sc0 sc1
v_fma_f32 v38,-v78,v90,v38
v_fma_f32 v39,-v78,v91,v39
v_fma_f32 v40,-v78,v92,v40
v_fma_f32 v41,-v78,v93,v41
v_mul_f32 v102,v38,v38
v_mul_f32 v103,v39,v39
v_mul_f32 v104,v40,v40
v_mul_f32 v105,v41,v41
v_fma_f32 v102,v80,v102,v79
v_fma_f32 v103,v80,v103,v79
v_fma_f32 v104,v80,v104,v79
v_fma_f32 v105,v80,v105,v79
v_mul_f32 v102,v38,v102
v_mul_f32 v103,v39,v103
v_mul_f32 v104,v40,v104
v_mul_f32 v105,v41,v105
v_exp_f32 v102,v102
v_exp_f32 v103,v103
v_exp_f32 v104,v104
v_exp_f32 v105,v105
v_fma_f32 v102,v102,v81,v81
v_fma_f32 v103,v103,v81,v81
v_fma_f32 v104,v104,v81,v81
v_fma_f32 v105,v105,v81,v81
v_rcp_f32 v102,v102
v_rcp_f32 v103,v103
v_rcp_f32 v104,v104
v_rcp_f32 v105,v105
v_mul_f32 v38,v38,v102
v_mul_f32 v39,v39,v103
v_mul_f32 v40,v40,v104
v_mul_f32 v41,v41,v105
v_cvt_pk_bf16_f32 v110,v38,v39
v_cvt_pk_bf16_f32 v111,v40,v41
s_nop 0
v_permlane16_swap_b32 v110,v112
v_permlane16_swap_b32 v111,v113
buffer_store_dwordx4 v[110:113],v10,s[36:39],s67 offen offset:0 sc0 sc1
s_mov_b32 s72,s69
s_mov_b32 s69,0x7fff0000
s_mov_b32 s67,0x7fff0000
s_mov_b32 s77,0x7fff0000
s_branch Lg3_poll_%=
Lg3_fin1_%=:
L 0
v_fma_f32 v30,-v78,v82,v30
v_fma_f32 v31,-v78,v83,v31
v_fma_f32 v32,-v78,v84,v32
v_fma_f32 v33,-v78,v85,v33
v_mul_f32 v94,v30,v30
v_mul_f32 v95,v31,v31
v_mul_f32 v96,v32,v32
v_mul_f32 v97,v33,v33
v_fma_f32 v94,v80,v94,v79
v_fma_f32 v95,v80,v95,v79
v_fma_f32 v96,v80,v96,v79
v_fma_f32 v97,v80,v97,v79
v_mul_f32 v94,v30,v94
v_mul_f32 v95,v31,v95
v_mul_f32 v96,v32,v96
v_mul_f32 v97,v33,v97
v_exp_f32 v94,v94
v_exp_f32 v95,v95
v_exp_f32 v96,v96
v_exp_f32 v97,v97
v_fma_f32 v94,v94,v81,v81
v_fma_f32 v95,v95,v81,v81
v_fma_f32 v96,v96,v81,v81
v_fma_f32 v97,v97,v81,v81
v_rcp_f32 v94,v94
v_rcp_f32 v95,v95
v_rcp_f32 v96,v96
v_rcp_f32 v97,v97
v_mul_f32 v30,v30,v94
v_mul_f32 v31,v31,v95
v_mul_f32 v32,v32,v96
v_mul_f32 v33,v33,v97
v_cvt_pk_bf16_f32 v106,v30,v31
v_cvt_pk_bf16_f32 v107,v32,v33
v_fma_f32 v34,-v78,v86,v34
v_fma_f32 v35,-v78,v87,v35
v_fma_f32 v36,-v78,v88,v36
v_fma_f32 v37,-v78,v89,v37
v_mul_f32 v98,v34,v34
v_mul_f32 v99,v35,v35
v_mul_f32 v100,v36,v36
v_mul_f32 v101,v37,v37
v_fma_f32 v98,v80,v98,v79
v_fma_f32 v99,v80,v99,v79
v_fma_f32 v100,v80,v100,v79
v_fma_f32 v101,v80,v101,v79
v_mul_f32 v98,v34,v98
v_mul_f32 v99,v35,v99
v_mul_f32 v100,v36,v100
v_mul_f32 v101,v37,v101
v_exp_f32 v98,v98
v_exp_f32 v99,v99
v_exp_f32 v100,v100
v_exp_f32 v101,v101
v_fma_f32 v98,v98,v81,v81
v_fma_f32 v99,v99,v81,v81
v_fma_f32 v100,v100,v81,v81
v_fma_f32 v101,v101,v81,v81
v_rcp_f32 v98,v98
v_rcp_f32 v99,v99
v_rcp_f32 v100,v100
v_rcp_f32 v101,v101
v_mul_f32 v34,v34,v98
v_mul_f32 v35,v35,v99
v_mul_f32 v36,v36,v100
v_mul_f32 v37,v37,v101
v_cvt_pk_bf16_f32 v108,v34,v35
v_cvt_pk_bf16_f32 v109,v36,v37
buffer_store_dwordx4 v[106:109],v0,s[36:39],s67 offen offset:0 sc0 sc1
v_fma_f32 v38,-v78,v90,v38
v_fma_f32 v39,-v78,v91,v39
v_fma_f32 v40,-v78,v92,v40
v_fma_f32 v41,-v78,v93,v41
v_mul_f32 v102,v38,v38
v_mul_f32 v103,v39,v39
v_mul_f32 v104,v40,v40
v_mul_f32 v105,v41,v41
v_fma_f32 v102,v80,v102,v79
v_fma_f32 v103,v80,v103,v79
v_fma_f32 v104,v80,v104,v79
v_fma_f32 v105,v80,v105,v79
v_mul_f32 v102,v38,v102
v_mul_f32 v103,v39,v103
v_mul_f32 v104,v40,v104
v_mul_f32 v105,v41,v105
v_exp_f32 v102,v102
v_exp_f32 v103,v103
v_exp_f32 v104,v104
v_exp_f32 v105,v105
v_fma_f32 v102,v102,v81,v81
v_fma_f32 v103,v103,v81,v81
v_fma_f32 v104,v104,v81,v81
v_fma_f32 v105,v105,v81,v81
v_rcp_f32 v102,v102
v_rcp_f32 v103,v103
v_rcp_f32 v104,v104
v_rcp_f32 v105,v105
v_mul_f32 v38,v38,v102
v_mul_f32 v39,v39,v103
v_mul_f32 v40,v40,v104
v_mul_f32 v41,v41,v105
v_cvt_pk_bf16_f32 v110,v38,v39
v_cvt_pk_bf16_f32 v111,v40,v41
s_nop 0
v_permlane16_swap_b32 v110,v112
v_permlane16_swap_b32 v111,v113
buffer_store_dwordx4 v[110:113],v10,s[36:39],s67 offen offset:0 sc0 sc1
s_mov_b32 s72,s69
s_mov_b32 s69,0x7fff0000
s_mov_b32 s67,0x7fff0000
s_mov_b32 s77,0x7fff0000
s_branch Lg3_done_%=
Lg3_done_%=:
s_waitcnt vmcnt(2)
s_barrier
buffer_store_dword v4,v3,s[36:39],s70 offen offset:0 sc0 sc1
s_waitcnt vmcnt(1)
s_barrier
buffer_store_dword v4,v3,s[36:39],s72 offen offset:0 sc0 sc1
s_waitcnt vmcnt(0) lgkmcnt(0)
s_barrier
s_endpgm
Lcode_g3_e:
.purgem M
.purgem R
.purgem T
.purgem D
.purgem L
)"
  :: "s"(ws), "s"(sWave), "s"(sG0), "s"(sStep), "s"(sOutOff), "s"(sFoutOff), "s"(sAuxOff), "s"(sWw), "s"(sBuf), "s"(sRk), "v"(vL16), "v"(vFlag), "v"(vSnapOff), "v"(vSnapRd), "v"(vWd0), "v"(vWd1), "v"(vWr0), "v"(vWr1), "v"(vWr2), "v"(vOutS), "v"(vAux0), "v"(vAux1), "v"(vAux2), "v"(vAux3), "v"(vAn), "v"(vAe), "s"(w3) : "v0", "v1", "v2", "v3", "v4", "v5", "v6", "v7", "v8", "v9", "v10", "v11", "v12", "v13", "v14", "v15", "v16", "v17", "v18", "v19", "v20", "v21", "v22", "v23", "v24", "v25", "v26", "v27", "v28", "v29", "v30", "v31", "v32", "v33", "v34", "v35", "v36", "v37", "v38", "v39", "v40", "v41", "v42", "v43", "v44", "v45", "v46", "v47", "v48", "v49", "v50", "v51", "v52", "v53", "v54", "v55", "v56", "v57", "v58", "v59", "v60", "v61", "v62", "v63", "v64", "v65", "v66", "v67", "v68", "v69", "v70", "v71", "v72", "v73", "v74", "v75", "v76", "v77", "v78", "v79", "v80", "v81", "v82", "v83", "v84", "v85", "v86", "v87", "v88", "v89", "v90", "v91", "v92", "v93", "v94", "v95", "v96", "v97", "v98", "v99", "v100", "v101", "v102", "v103", "v104", "v105", "v106", "v107", "v108", "v109", "v110", "v111", "v112", "v113", "v114", "v115", "v116", "v117", "v118", "v119", "v120", "v121", "v122", "v123", "v124", "v125", "v126", "v127", "v128", "v129", "v130", "v131", "v132", "v133", "v134", "v135", "v136", "v137", "v138", "v139", "v140", "v141", "v142", "v143", "v144", "v145", "v146", "v147", "v148", "v149", "v150", "v151", "v152", "v153", "v154", "v155", "v156", "v157", "v158", "v159", "v160", "v161", "v162", "v163", "v164", "v165", "v166", "v167", "v168", "v169", "v170", "v171", "v172", "v173", "v174", "v175", "v176", "v177", "v178", "v179", "v180", "v181", "v182", "v183", "v184", "v185", "v186", "v187", "v188", "v189", "v190", "v191", "v192", "v193", "v194", "v195", "v196", "v197", "v198", "v199", "v200", "v201", "v202", "v203", "v204", "v205", "v206", "v207", "v208", "v209", "v210", "v211", "v212", "v213", "v214", "v215", "v216", "v217", "v218", "v219", "v220", "v221", "v222", "v223", "v224", "v225", "v226", "v227", "v228", "v229", "v230", "v231", "v232", "v233", "v234", "v235", "v236", "v237", "v238", "v239", "a0", "a1", "a2", "a3", "a4", "a5", "a6", "a7", "a8", "a9", "a10", "a11", "a12", "a13", "a14", "a15", "a16", "a17", "a18", "a19", "a20", "a21", "a22", "a23", "a24", "a25", "a26", "a27", "a28", "a29", "a30", "a31", "a32", "a33", "a34", "a35", "a36", "a37", "a38", "a39", "a40", "a41", "a42", "a43", "a44", "a45", "a46", "a47", "a48", "a49", "a50", "a51", "a52", "a53", "a54", "a55", "a56", "a57", "a58", "a59", "a60", "a61", "a62", "a63", "a64", "a65", "a66", "a67", "a68", "a69", "a70", "a71", "a72", "a73", "a74", "a75", "a76", "a77", "a78", "a79", "a80", "a81", "a82", "a83", "a84", "a85", "a86", "a87", "a88", "a89", "a90", "a91", "a92", "a93", "a94", "a95", "a96", "a97", "a98", "a99", "a100", "a101", "a102", "a103", "a104", "a105", "a106", "a107", "a108", "a109", "a110", "a111", "a112", "a113", "a114", "a115", "a116", "a117", "a118", "a119", "a120", "a121", "a122", "a123", "a124", "a125", "a126", "a127", "a128", "a129", "a130", "a131", "a132", "a133", "a134", "a135", "a136", "a137", "a138", "a139", "a140", "a141", "a142", "a143", "a144", "a145", "a146", "a147", "a148", "a149", "a150", "a151", "a152", "a153", "a154", "a155", "a156", "a157", "a158", "a159", "a160", "a161", "a162", "a163", "a164", "a165", "a166", "a167", "a168", "a169", "a170", "a171", "a172", "a173", "a174", "a175", "a176", "a177", "a178", "a179", "a180", "a181", "a182", "a183", "a184", "a185", "a186", "a187", "a188", "a189", "a190", "a191", "a192", "a193", "a194", "a195", "a196", "a197", "a198", "a199", "a200", "a201", "a202", "a203", "a204", "a205", "a206", "a207", "a208", "a209", "a210", "a211", "a212", "a213", "a214", "a215", "a216", "a217", "a218", "a219", "a220", "a221", "a222", "a223", "a224", "a225", "a226", "a227", "a228", "a229", "a230", "a231", "a232", "a233", "a234", "a235", "a236", "a237", "a238", "a239", "a240", "a241", "a242", "a243", "a244", "a245", "a246", "a247", "a248", "a249", "a250", "a251", "a252", "a253", "a254", "a255", "s36", "s37", "s38", "s39", "s40", "s41", "s42", "s43", "s44", "s45", "s46", "s47", "s48", "s49", "s50", "s51", "s52", "s53", "s54", "s55", "s56", "s57", "s58", "s59", "s60", "s61", "s62", "s63", "s64", "s65", "s66", "s67", "s68", "s69", "s70", "s71", "s72", "s73", "s74", "s75", "s76", "s77", "s78", "s79", "s80", "s81", "s82", "s83", "s84", "s85", "s86", "s87", "s88", "s89", "s90", "s91", "s92", "s93", "s94", "s95", "s96", "s97", "vcc", "scc", "memory"); } else { asm volatile(
R"(.macro M d,w,x
v_mfma_f32_16x16x32_bf16 v[\d:\d+3], \w, v[\x:\x+3], v[\d:\d+3]
.endm
.macro R d,a,o
ds_read_b128 v[\d:\d+3], v\a offset:\o
.endm
.macro T d,a,o
ds_read_b64_tr_b16 \d, v\a offset:\o
.endm
.macro D a,s,o
buffer_load_dwordx4 v\a, s[36:39], s\s offen offset:\o sc1 lds
.endm
.macro L c
s_waitcnt lgkmcnt(\c)
.endm
Lcode_g3o_b:
s_mov_b64 s[36:37],%0
s_mov_b32 s38,111243392
s_mov_b32 s39,0x00020000
s_mov_b32 s44,%1
s_mov_b32 s45,%2
s_mov_b32 s46,%3
s_mov_b32 s47,%4
s_mov_b32 s48,%5
s_mov_b32 s49,%6
s_mov_b32 s50,%7
s_mov_b32 s51,%8
s_mov_b32 s93,%9
v_mov_b32 v0,%10
v_mov_b32 v3,%11
v_mov_b32 v5,%12
v_mov_b32 v6,%13
v_mov_b32 v18,%14
v_mov_b32 v19,%15
v_mov_b32 v20,%16
v_mov_b32 v21,%17
v_mov_b32 v22,%18
v_mov_b32 v10,%19
v_mov_b32 v11,%20
v_mov_b32 v12,%21
v_mov_b32 v13,%22
v_mov_b32 v14,%23
v_mov_b32 v15,%24
v_mov_b32 v16,%25
s_mov_b64 s[40:41],%26
s_mov_b32 s42,7340032
s_mov_b32 s43,0x00020000
s_lshl_b32 s57,s44,11
s_mov_b32 s79,111243264
buffer_load_dword v4,off,s[36:39],s79 sc1
s_add_u32 s55,s50,0
s_add_u32 m0,s57,0
s_nop 0
buffer_load_dwordx4 v18,s[40:43],s55 offen offset:0  lds
s_add_u32 m0,s57,1024
s_nop 0
buffer_load_dwordx4 v19,s[40:43],s55 offen offset:0  lds
s_add_u32 m0,s57,8064
s_nop 0
buffer_load_dwordx4 v18,s[40:43],s55 offen offset:128  lds
s_add_u32 m0,s57,9088
s_nop 0
buffer_load_dwordx4 v19,s[40:43],s55 offen offset:128  lds
s_add_u32 s55,s50,458752
s_add_u32 m0,s57,16384
s_nop 0
buffer_load_dwordx4 v18,s[40:43],s55 offen offset:0  lds
s_add_u32 m0,s57,17408
s_nop 0
buffer_load_dwordx4 v19,s[40:43],s55 offen offset:0  lds
s_add_u32 m0,s57,24448
s_nop 0
buffer_load_dwordx4 v18,s[40:43],s55 offen offset:128  lds
s_add_u32 m0,s57,25472
s_nop 0
buffer_load_dwordx4 v19,s[40:43],s55 offen offset:128  lds
s_add_u32 s55,s50,917504
s_add_u32 m0,s57,32768
s_nop 0
buffer_load_dwordx4 v18,s[40:43],s55 offen offset:0  lds
s_add_u32 m0,s57,33792
s_nop 0
buffer_load_dwordx4 v19,s[40:43],s55 offen offset:0  lds
s_add_u32 m0,s57,40832
s_nop 0
buffer_load_dwordx4 v18,s[40:43],s55 offen offset:128  lds
s_add_u32 m0,s57,41856
s_nop 0
buffer_load_dwordx4 v19,s[40:43],s55 offen offset:128  lds
s_add_u32 s55,s50,1376256
s_add_u32 m0,s57,49152
s_nop 0
buffer_load_dwordx4 v18,s[40:43],s55 offen offset:0  lds
s_add_u32 m0,s57,50176
s_nop 0
buffer_load_dwordx4 v19,s[40:43],s55 offen offset:0  lds
s_add_u32 m0,s57,57216
s_nop 0
buffer_load_dwordx4 v18,s[40:43],s55 offen offset:128  lds
s_add_u32 m0,s57,58240
s_nop 0
buffer_load_dwordx4 v19,s[40:43],s55 offen offset:128  lds
s_add_u32 s55,s50,1835008
s_add_u32 m0,s57,65536
s_nop 0
buffer_load_dwordx4 v18,s[40:43],s55 offen offset:0  lds
s_add_u32 m0,s57,66560
s_nop 0
buffer_load_dwordx4 v19,s[40:43],s55 offen offset:0  lds
s_add_u32 m0,s57,73600
s_nop 0
buffer_load_dwordx4 v18,s[40:43],s55 offen offset:128  lds
s_add_u32 m0,s57,74624
s_nop 0
buffer_load_dwordx4 v19,s[40:43],s55 offen offset:128  lds
s_add_u32 s55,s50,2293760
s_add_u32 m0,s57,81920
s_nop 0
buffer_load_dwordx4 v18,s[40:43],s55 offen offset:0  lds
s_add_u32 m0,s57,82944
s_nop 0
buffer_load_dwordx4 v19,s[40:43],s55 offen offset:0  lds
s_add_u32 m0,s57,89984
s_nop 0
buffer_load_dwordx4 v18,s[40:43],s55 offen offset:128  lds
s_add_u32 m0,s57,91008
s_nop 0
buffer_load_dwordx4 v19,s[40:43],s55 offen offset:128  lds
s_waitcnt vmcnt(24)
s_barrier
v_readfirstlane_b32 s53,v4
s_nop 3
s_add_u32 s53,s53,1
s_cmp_eq_u32 s53,0
s_cselect_b32 s53,1,s53
v_mov_b32 v4,s53
s_waitcnt vmcnt(20)
L 0
s_barrier
s_add_u32 s55,s50,2752512
s_add_u32 m0,s57,98304
s_nop 0
buffer_load_dwordx4 v18,s[40:43],s55 offen offset:0  lds
s_add_u32 m0,s57,99328
s_nop 0
buffer_load_dwordx4 v19,s[40:43],s55 offen offset:0  lds
s_add_u32 m0,s57,106368
s_nop 0
buffer_load_dwordx4 v18,s[40:43],s55 offen offset:128  lds
s_add_u32 m0,s57,107392
s_nop 0
buffer_load_dwordx4 v19,s[40:43],s55 offen offset:128  lds
v_add_u32 v26,0,v20
v_add_u32 v27,0,v21
T a[0:1],26,0
T a[2:3],26,512
T a[4:5],27,0
T a[6:7],27,512
T a[8:9],26,4096
T a[10:11],26,4608
T a[12:13],27,4096
T a[14:15],27,4608
s_waitcnt vmcnt(20)
L 0
s_barrier
s_add_u32 s55,s50,3211264
s_add_u32 m0,s57,0
s_nop 0
buffer_load_dwordx4 v18,s[40:43],s55 offen offset:0  lds
s_add_u32 m0,s57,1024
s_nop 0
buffer_load_dwordx4 v19,s[40:43],s55 offen offset:0  lds
s_add_u32 m0,s57,8064
s_nop 0
buffer_load_dwordx4 v18,s[40:43],s55 offen offset:128  lds
s_add_u32 m0,s57,9088
s_nop 0
buffer_load_dwordx4 v19,s[40:43],s55 offen offset:128  lds
v_add_u32 v26,16384,v20
v_add_u32 v27,16384,v21
T a[16:17],26,0
T a[18:19],26,512
T a[20:21],27,0
T a[22:23],27,512
T a[24:25],26,4096
T a[26:27],26,4608
T a[28:29],27,4096
T a[30:31],27,4608
s_waitcnt vmcnt(20)
L 0
s_barrier
s_add_u32 s55,s50,3670016
s_add_u32 m0,s57,16384
s_nop 0
buffer_load_dwordx4 v18,s[40:43],s55 offen offset:0  lds
s_add_u32 m0,s57,17408
s_nop 0
buffer_load_dwordx4 v19,s[40:43],s55 offen offset:0  lds
s_add_u32 m0,s57,24448
s_nop 0
buffer_load_dwordx4 v18,s[40:43],s55 offen offset:128  lds
s_add_u32 m0,s57,25472
s_nop 0
buffer_load_dwordx4 v19,s[40:43],s55 offen offset:128  lds
v_add_u32 v26,32768,v20
v_add_u32 v27,32768,v21
T a[32:33],26,0
T a[34:35],26,512
T a[36:37],27,0
T a[38:39],27,512
T a[40:41],26,4096
T a[42:43],26,4608
T a[44:45],27,4096
T a[46:47],27,4608
s_waitcnt vmcnt(20)
L 0
s_barrier
s_add_u32 s55,s50,4128768
s_add_u32 m0,s57,32768
s_nop 0
buffer_load_dwordx4 v18,s[40:43],s55 offen offset:0  lds
s_add_u32 m0,s57,33792
s_nop 0
buffer_load_dwordx4 v19,s[40:43],s55 offen offset:0  lds
s_add_u32 m0,s57,40832
s_nop 0
buffer_load_dwordx4 v18,s[40:43],s55 offen offset:128  lds
s_add_u32 m0,s57,41856
s_nop 0
buffer_load_dwordx4 v19,s[40:43],s55 offen offset:128  lds
v_add_u32 v26,49152,v20
v_add_u32 v27,49152,v21
T a[48:49],26,0
T a[50:51],26,512
T a[52:53],27,0
T a[54:55],27,512
T a[56:57],26,4096
T a[58:59],26,4608
T a[60:61],27,4096
T a[62:63],27,4608
s_waitcnt vmcnt(20)
L 0
s_barrier
s_add_u32 s55,s50,4587520
s_add_u32 m0,s57,49152
s_nop 0
buffer_load_dwordx4 v18,s[40:43],s55 offen offset:0  lds
s_add_u32 m0,s57,50176
s_nop 0
buffer_load_dwordx4 v19,s[40:43],s55 offen offset:0  lds
s_add_u32 m0,s57,57216
s_nop 0
buffer_load_dwordx4 v18,s[40:43],s55 offen offset:128  lds
s_add_u32 m0,s57,58240
s_nop 0
buffer_load_dwordx4 v19,s[40:43],s55 offen offset:128  lds
v_add_u32 v26,65536,v20
v_add_u32 v27,65536,v21
T a[64:65],26,0
T a[66:67],26,512
T a[68:69],27,0
T a[70:71],27,512
T a[72:73],26,4096
T a[74:75],26,4608
T a[76:77],27,4096
T a[78:79],27,4608
s_waitcnt vmcnt(20)
L 0
s_barrier
s_add_u32 s55,s50,5046272
s_add_u32 m0,s57,65536
s_nop 0
buffer_load_dwordx4 v18,s[40:43],s55 offen offset:0  lds
s_add_u32 m0,s57,66560
s_nop 0
buffer_load_dwordx4 v19,s[40:43],s55 offen offset:0  lds
s_add_u32 m0,s57,73600
s_nop 0
buffer_load_dwordx4 v18,s[40:43],s55 offen offset:128  lds
s_add_u32 m0,s57,74624
s_nop 0
buffer_load_dwordx4 v19,s[40:43],s55 offen offset:128  lds
v_add_u32 v26,81920,v20
v_add_u32 v27,81920,v21
T a[80:81],26,0
T a[82:83],26,512
T a[84:85],27,0
T a[86:87],27,512
T a[88:89],26,4096
T a[90:91],26,4608
T a[92:93],27,4096
T a[94:95],27,4608
s_waitcnt vmcnt(20)
L 0
s_barrier
s_add_u32 s55,s50,5505024
s_add_u32 m0,s57,81920
s_nop 0
buffer_load_dwordx4 v18,s[40:43],s55 offen offset:0  lds
s_add_u32 m0,s57,82944
s_nop 0
buffer_load_dwordx4 v19,s[40:43],s55 offen offset:0  lds
s_add_u32 m0,s57,89984
s_nop 0
buffer_load_dwordx4 v18,s[40:43],s55 offen offset:128  lds
s_add_u32 m0,s57,91008
s_nop 0
buffer_load_dwordx4 v19,s[40:43],s55 offen offset:128  lds
v_add_u32 v26,98304,v20
v_add_u32 v27,98304,v21
T a[96:97],26,0
T a[98:99],26,512
T a[100:101],27,0
T a[102:103],27,512
T a[104:105],26,4096
T a[106:107],26,4608
T a[108:109],27,4096
T a[110:111],27,4608
s_waitcnt vmcnt(20)
L 0
s_barrier
s_add_u32 s55,s50,5963776
s_add_u32 m0,s57,98304
s_nop 0
buffer_load_dwordx4 v18,s[40:43],s55 offen offset:0  lds
s_add_u32 m0,s57,99328
s_nop 0
buffer_load_dwordx4 v19,s[40:43],s55 offen offset:0  lds
s_add_u32 m0,s57,106368
s_nop 0
buffer_load_dwordx4 v18,s[40:43],s55 offen offset:128  lds
s_add_u32 m0,s57,107392
s_nop 0
buffer_load_dwordx4 v19,s[40:43],s55 offen offset:128  lds
v_add_u32 v26,0,v20
v_add_u32 v27,0,v21
T a[112:113],26,0
T a[114:115],26,512
T a[116:117],27,0
T a[118:119],27,512
T a[120:121],26,4096
T a[122:123],26,4608
T a[124:125],27,4096
T a[126:127],27,4608
s_waitcnt vmcnt(20)
L 0
s_barrier
s_add_u32 s55,s50,6422528
s_add_u32 m0,s57,0
s_nop 0
buffer_load_dwordx4 v18,s[40:43],s55 offen offset:0  lds
s_add_u32 m0,s57,1024
s_nop 0
buffer_load_dwordx4 v19,s[40:43],s55 offen offset:0  lds
s_add_u32 m0,s57,8064
s_nop 0
buffer_load_dwordx4 v18,s[40:43],s55 offen offset:128  lds
s_add_u32 m0,s57,9088
s_nop 0
buffer_load_dwordx4 v19,s[40:43],s55 offen offset:128  lds
v_add_u32 v26,16384,v20
v_add_u32 v27,16384,v21
T a[128:129],26,0
T a[130:131],26,512
T a[132:133],27,0
T a[134:135],27,512
T a[136:137],26,4096
T a[138:139],26,4608
T a[140:141],27,4096
T a[142:143],27,4608
s_waitcnt vmcnt(20)
L 0
s_barrier
s_add_u32 s55,s50,6881280
s_add_u32 m0,s57,16384
s_nop 0
buffer_load_dwordx4 v18,s[40:43],s55 offen offset:0  lds
s_add_u32 m0,s57,17408
s_nop 0
buffer_load_dwordx4 v19,s[40:43],s55 offen offset:0  lds
s_add_u32 m0,s57,24448
s_nop 0
buffer_load_dwordx4 v18,s[40:43],s55 offen offset:128  lds
s_add_u32 m0,s57,25472
s_nop 0
buffer_load_dwordx4 v19,s[40:43],s55 offen offset:128  lds
v_add_u32 v26,32768,v20
v_add_u32 v27,32768,v21
T a[144:145],26,0
T a[146:147],26,512
T a[148:149],27,0
T a[150:151],27,512
T a[152:153],26,4096
T a[154:155],26,4608
T a[156:157],27,4096
T a[158:159],27,4608
s_waitcnt vmcnt(20)
L 0
s_barrier
v_add_u32 v26,49152,v20
v_add_u32 v27,49152,v21
T a[160:161],26,0
T a[162:163],26,512
T a[164:165],27,0
T a[166:167],27,512
T a[168:169],26,4096
T a[170:171],26,4608
T a[172:173],27,4096
T a[174:175],27,4608
s_waitcnt vmcnt(16)
L 0
s_barrier
v_add_u32 v26,65536,v20
v_add_u32 v27,65536,v21
T a[176:177],26,0
T a[178:179],26,512
T a[180:181],27,0
T a[182:183],27,512
T a[184:185],26,4096
T a[186:187],26,4608
T a[188:189],27,4096
T a[190:191],27,4608
s_waitcnt vmcnt(12)
L 0
s_barrier
v_add_u32 v26,81920,v20
v_add_u32 v27,81920,v21
T a[192:193],26,0
T a[194:195],26,512
T a[196:197],27,0
T a[198:199],27,512
T a[200:201],26,4096
T a[202:203],26,4608
T a[204:205],27,4096
T a[206:207],27,4608
s_waitcnt vmcnt(8)
L 0
s_barrier
v_add_u32 v26,98304,v20
v_add_u32 v27,98304,v21
T a[208:209],26,0
T a[210:211],26,512
T a[212:213],27,0
T a[214:215],27,512
T a[216:217],26,4096
T a[218:219],26,4608
T a[220:221],27,4096
T a[222:223],27,4608
s_waitcnt vmcnt(4)
L 0
s_barrier
v_add_u32 v26,0,v20
v_add_u32 v27,0,v21
T a[224:225],26,0
T a[226:227],26,512
T a[228:229],27,0
T a[230:231],27,512
T a[232:233],26,4096
T a[234:235],26,4608
T a[236:237],27,4096
T a[238:239],27,4608
s_waitcnt vmcnt(0)
L 0
s_barrier
v_add_u32 v26,16384,v20
v_add_u32 v27,16384,v21
T a[240:241],26,0
T a[242:243],26,512
T a[244:245],27,0
T a[246:247],27,512
T a[248:249],26,4096
T a[250:251],26,4608
T a[252:253],27,4096
T a[254:255],27,4608
L 0
s_barrier
v_mov_b32 v34,0x3f803f80
v_mov_b32 v35,0x3f803f80
v_mov_b32 v36,0x3f803f80
v_mov_b32 v37,0x3f803f80
s_nop 1
v_mfma_f32_16x16x32_bf16 v[106:109],a[0:3],v[34:37],0
v_mfma_f32_16x16x32_bf16 v[110:113],a[4:7],v[34:37],0
M 106,a[8:11],34
M 110,a[12:15],34
M 106,a[16:19],34
M 110,a[20:23],34
M 106,a[24:27],34
M 110,a[28:31],34
M 106,a[32:35],34
M 110,a[36:39],34
M 106,a[40:43],34
M 110,a[44:47],34
M 106,a[48:51],34
M 110,a[52:55],34
M 106,a[56:59],34
M 110,a[60:63],34
M 106,a[64:67],34
M 110,a[68:71],34
M 106,a[72:75],34
)"
R"(M 110,a[76:79],34
M 106,a[80:83],34
M 110,a[84:87],34
M 106,a[88:91],34
M 110,a[92:95],34
M 106,a[96:99],34
M 110,a[100:103],34
M 106,a[104:107],34
M 110,a[108:111],34
M 106,a[112:115],34
M 110,a[116:119],34
M 106,a[120:123],34
M 110,a[124:127],34
M 106,a[128:131],34
M 110,a[132:135],34
M 106,a[136:139],34
M 110,a[140:143],34
M 106,a[144:147],34
M 110,a[148:151],34
M 106,a[152:155],34
M 110,a[156:159],34
M 106,a[160:163],34
M 110,a[164:167],34
M 106,a[168:171],34
M 110,a[172:175],34
M 106,a[176:179],34
M 110,a[180:183],34
M 106,a[184:187],34
M 110,a[188:191],34
M 106,a[192:195],34
M 110,a[196:199],34
M 106,a[200:203],34
M 110,a[204:207],34
M 106,a[208:211],34
M 110,a[212:215],34
M 106,a[216:219],34
M 110,a[220:223],34
M 106,a[224:227],34
M 110,a[228:231],34
M 106,a[232:235],34
M 110,a[236:239],34
M 106,a[240:243],34
M 110,a[244:247],34
M 106,a[248:251],34
M 110,a[252:255],34
v_mov_b32 v4,s53
s_mov_b32 s82,0
s_mov_b32 s80,0
s_mov_b32 s54,s45
s_mul_i32 s57,s44,4096
s_mul_i32 s55,s45,32768
s_add_u32 s55,s55,s57
s_add_u32 s55,s55,31457280
s_mul_i32 s57,s44,4096
s_mul_i32 s60,s46,32768
s_mul_i32 s61,s46,1024
s_mul_i32 s62,s46,114688
s_mul_i32 s63,s46,2560
s_mul_i32 s64,s46,1024
s_lshl_b32 s59,s44,8
s_add_u32 s59,s59,131072
s_mul_i32 s58,s45,1024
s_add_u32 s58,s58,109613056
s_mul_i32 s65,s45,114688
s_add_u32 s65,s65,s47
s_mul_i32 s68,s45,2560
s_add_u32 s68,s68,s48
s_mul_i32 s73,s45,1024
s_add_u32 s73,s73,107479040
s_mul_i32 s74,s45,32768
s_add_u32 s74,s74,s49
s_mul_i32 s75,s45,1024
s_add_u32 s75,s75,107479040
s_mov_b32 s66,0x7fff0000
s_mov_b32 s67,0x7fff0000
s_mov_b32 s69,0x7fff0000
s_mov_b32 s76,0x7fff0000
s_mov_b32 s77,0x7fff0000
s_mov_b32 s70,0x7fff0000
s_mov_b32 s71,0x7fff0000
s_mov_b32 s72,0x7fff0000
v_mov_b32 v1,v0
v_add_u32 v2,16384,v0
s_mov_b32 s84,16384
s_mov_b32 s86,0x3a800000
s_mov_b32 s87,0x3727c5ac
s_mov_b32 s88,0xc0135761
s_mov_b32 s89,0x3d372713
s_and_b32 s85,s44,1
s_mul_i32 s85,s85,127
v_lshlrev_b32 v8,22,v0
v_and_b32 v8,0x80000000,v8
v_or_b32 v8,v8,v5
v_add_u32 v2,32768,v0
s_lshl_b32 s84,s57,1
s_add_u32 s84,s84,32768
s_getpc_b64 s[40:41]
Lg3o_pc_%=:
s_lshl_b32 s78,s44,13
v_lshlrev_b32 v114,3,v0
v_add_u32 v114,s78,v114
v_min_u32 v114,Lcode_g3o_e-Lg3o_pc_%=-4,v114
s_mov_b32 m0,163584
s_nop 0
global_load_lds_dword v114,s[40:41]
s_add_u32 m0,s59,4096
s_nop 0
buffer_load_dword v8,s[36:39],s58 offen offset:0 sc1 lds
buffer_store_dword v4,v3,s[36:39],s71 offen offset:0 sc0 sc1
s_add_u32 m0,s59,5120
s_nop 0
buffer_load_dword v8,s[36:39],s58 offen offset:0 sc1 lds
buffer_store_dword v4,v3,s[36:39],s71 offen offset:0 sc0 sc1
s_mov_b32 s83,0
Lg3o_poll_%=:
s_add_u32 s78,s83,2
s_and_b32 s78,s78,3
s_lshl_b32 s78,s78,10
s_add_u32 s78,s78,4096
s_add_u32 m0,s59,s78
s_nop 0
buffer_load_dword v8,s[36:39],s58 offen offset:0 sc1 lds
s_and_b32 s78,s83,3
s_lshl_b32 s78,s78,10
v_add_u32 v114,s78,v6
s_waitcnt vmcnt(4)
s_barrier
ds_read_b32 v7,v114 offset:4096
buffer_store_dword v4,v3,s[36:39],s70 offen offset:0 sc0 sc1
s_mov_b32 s70,s71
s_mov_b32 s71,s72
s_mov_b32 s72,0x7fff0000
s_add_u32 s83,s83,1
s_add_u32 s82,s82,1
L 0
v_cmp_eq_u32 vcc,s53,v7
s_nop 0
s_cmp_eq_u32 vcc_lo,-1
s_cbranch_scc1 Lg3o_go_%=
s_cmp_lt_u32 s82,20000
s_cbranch_scc1 Lg3o_poll_%=
Lg3o_go_%=:
buffer_load_dword v130,v11,s[36:39],s73 offen offset:0 sc1
s_add_u32 s73,s73,s64
s_add_u32 m0,s57,0
s_add_u32 s56,s55,0
D 0,56,0
D 0,56,1024
D 0,56,2048
D 0,56,3072
s_add_u32 s58,s58,s61
s_add_u32 m0,s59,2048
s_nop 0
buffer_load_dword v8,s[36:39],s58 offen offset:0 sc1 lds
s_add_u32 m0,s57,16384
s_add_u32 s56,s55,16384
D 0,56,0
D 0,56,1024
D 0,56,2048
D 0,56,3072
s_add_u32 s55,s55,s60
s_sub_u32 s57,s84,s57
s_add_u32 s54,s54,s46
s_waitcnt vmcnt(4)
s_barrier
buffer_store_dword v4,v3,s[36:39],s70 offen offset:0 sc0 sc1
s_min_u32 s71,s71,s72
buffer_store_dword v4,v3,s[36:39],s71 offen offset:0 sc0 sc1
s_mov_b32 s70,0x7fff0000
s_mov_b32 s71,0x7fff0000
s_mov_b32 s72,0x7fff0000
s_cmp_eq_u32 s80,1
s_cbranch_scc1 Lg3o_st1_%=
s_branch Lg3o_st0_%=
Lg3o_gox_%=:
s_add_u32 s58,s58,s61
s_add_u32 m0,s59,2048
s_nop 0
buffer_load_dword v8,s[36:39],s58 offen offset:0 sc1 lds
s_waitcnt vmcnt(0)
s_barrier
buffer_store_dword v4,v3,s[36:39],s70 offen offset:0 sc0 sc1
s_min_u32 s71,s71,s72
buffer_store_dword v4,v3,s[36:39],s71 offen offset:0 sc0 sc1
s_mov_b32 s70,0x7fff0000
s_mov_b32 s71,0x7fff0000
s_mov_b32 s72,0x7fff0000
s_cmp_eq_u32 s80,1
s_cbranch_scc1 Lg3o_st1_%=
Lg3o_st0_%=:
ds_read_b32 v7,v6 offset:2048
R 34,1,0
R 38,1,1024
R 42,1,2048
R 46,1,3072
R 50,1,4096
R 54,1,5120
R 58,1,6144
L 7
v_cmp_eq_u32 vcc,s53,v7
L 5
s_cmp_eq_u32 vcc_lo,-1
s_cbranch_scc1 Lg3o_F0_%=
s_branch Lg3o_L0_%=
Lg3o_st1_%=:
ds_read_b32 v7,v6 offset:2048
R 34,2,0
R 38,2,1024
R 42,2,2048
R 46,2,3072
R 50,2,4096
R 54,2,5120
R 58,2,6144
L 7
v_cmp_eq_u32 vcc,s53,v7
L 5
s_cmp_eq_u32 vcc_lo,-1
s_cbranch_scc1 Lg3o_F1_%=
s_branch Lg3o_L1_%=
Lg3o_F0_%=:
v_mfma_f32_16x16x32_bf16 v[22:25],a[4:7],v[34:37],0
s_add_u32 m0,s57,0
R 62,1,7168
v_add_f32_dpp v131,v130,v130 quad_perm:[1,0,3,2] row_mask:0xf bank_mask:0xf
M 22,a[12:15],38
s_add_u32 s56,s55,0
buffer_load_dword v130,v11,s[36:39],s73 offen offset:0 sc1
v_mfma_f32_16x16x32_bf16 v[18:21],a[0:3],v[34:37],0
s_add_u32 s58,s58,s61
R 66,1,8192
v_add_f32_dpp v131,v131,v131 quad_perm:[2,3,0,1] row_mask:0xf bank_mask:0xf
M 18,a[8:11],38
L 5
D 0,56,0
M 18,a[16:19],42
s_mov_b32 s66,s65
R 70,1,9216
v_add_f32_dpp v134,v131,v131 row_half_mirror row_mask:0xf bank_mask:0xf
M 18,a[24:27],46
s_add_u32 s65,s65,s62
v_fma_f32 v26,-v102,v106,v26
v_fma_f32 v27,-v102,v107,v27
M 22,a[20:23],42
s_add_u32 s54,s54,s46
R 74,1,10240
v_mov_b32_dpp v131,v134 row_ror:8 row_mask:0xf bank_mask:0xf
M 22,a[28:31],46
L 5
v_fma_f32 v131,-v134,v134,v131
v_max_f32 v131,s87,v131
M 22,a[36:39],50
s_add_u32 s73,s73,s64
R 78,1,11264
v_fma_f32 v28,-v102,v108,v28
M 22,a[44:47],54
D 0,56,1024
M 18,a[32:35],50
R 82,1,12288
v_fma_f32 v29,-v102,v109,v29
M 18,a[40:43],54
L 5
v_rsq_f32 v132,v131
M 18,a[48:51],58
R 86,1,13312
v_mul_f32 v137,v131,v132
M 18,a[56:59],62
v_mul_f32 v135,s88,v132
v_mul_f32 v131,s89,v132
M 22,a[52:55],58
R 90,1,14336
v_mul_f32 v136,v135,v132
M 22,a[60:63],62
L 5
v_mul_f32 v136,v136,v131
v_mul_f32 v114,v26,v26
M 22,a[68:71],66
s_waitcnt vmcnt(4)
R 94,1,15360
v_mul_f32 v115,v27,v27
M 22,a[76:79],70
s_barrier
D 0,56,2048
M 18,a[64:67],66
R 34,1,16384
v_mul_f32 v116,v28,v28
M 18,a[72:75],70
L 5
ds_write_b128 v12,v[134:137] offset:0
M 18,a[80:83],74
R 38,1,17408
v_mul_f32 v117,v29,v29
M 18,a[88:91],78
v_fma_f32 v114,v104,v114,v103
v_fma_f32 v115,v104,v115,v103
M 22,a[84:87],74
R 42,1,18432
v_fma_f32 v116,v104,v116,v103
M 22,a[92:95],78
L 6
D 0,56,3072
M 22,a[100:103],82
s_add_u32 m0,s59,0
R 46,1,19456
v_fma_f32 v117,v104,v117,v103
M 22,a[108:111],86
s_add_u32 s56,s55,16384
v_mul_f32 v114,v26,v114
v_mul_f32 v115,v27,v115
M 18,a[96:99],82
s_add_u32 s55,s55,s60
R 50,1,20480
v_mul_f32 v116,v28,v116
M 18,a[104:107],86
L 6
v_exp_f32 v114,v114
M 18,a[112:115],90
R 54,1,21504
v_mul_f32 v117,v29,v117
M 18,a[120:123],94
v_exp_f32 v115,v115
M 22,a[116:119],90
R 58,1,22528
v_fma_f32 v114,v114,v105,v105
M 22,a[124:127],94
L 5
buffer_load_dword v8,s[36:39],s58 offen offset:0 sc1 lds
M 22,a[132:135],34
s_add_u32 m0,s57,16384
R 62,1,23552
v_fma_f32 v115,v115,v105,v105
M 22,a[140:143],38
s_sub_u32 s57,s84,s57
D 0,56,0
M 18,a[128:131],34
R 66,1,24576
v_fma_f32 v30,-v102,v110,v30
M 18,a[136:139],38
L 5
v_exp_f32 v116,v116
M 18,a[144:147],42
R 70,1,25600
v_fma_f32 v116,v116,v105,v105
M 18,a[152:155],46
v_exp_f32 v117,v117
M 22,a[148:151],42
R 74,1,26624
v_fma_f32 v117,v117,v105,v105
M 22,a[156:159],46
L 5
v_rcp_f32 v114,v114
M 22,a[164:167],50
R 78,1,27648
v_mul_f32 v26,v26,v114
M 22,a[172:175],54
D 0,56,1024
M 18,a[160:163],50
R 82,1,28672
v_fma_f32 v31,-v102,v111,v31
M 18,a[168:171],54
L 5
v_rcp_f32 v115,v115
M 18,a[176:179],58
R 86,1,29696
v_mul_f32 v27,v27,v115
M 18,a[184:187],62
v_rcp_f32 v116,v116
M 22,a[180:183],58
R 90,1,30720
v_mul_f32 v28,v28,v116
M 22,a[188:191],62
L 5
s_nop 0
D 0,56,2048
M 22,a[196:199],66
s_waitcnt vmcnt(3)
R 94,1,31744
v_cvt_pk_bf16_f32 v122,v26,v27
M 22,a[204:207],70
s_barrier
buffer_store_dword v4,v3,s[36:39],s70 offen offset:0 sc0 sc1
M 18,a[192:195],66
s_mov_b32 s70,s69
R 34,2,0
ds_read_b32 v7,v6 offset:0
M 18,a[200:203],70
L 6
R 98,13,0
v_fma_f32 v32,-v102,v112,v32
M 18,a[208:211],74
s_mov_b32 s69,s68
R 38,2,1024
v_fma_f32 v33,-v102,v113,v33
M 18,a[216:219],78
s_add_u32 s68,s68,s63
v_mul_f32 v118,v30,v30
v_mul_f32 v119,v31,v31
M 22,a[212:215],74
R 42,2,2048
v_mul_f32 v120,v32,v32
M 22,a[220:223],78
L 7
D 0,56,3072
M 22,a[228:231],82
L 3
R 46,2,3072
v_mul_f32 v121,v33,v33
M 22,a[236:239],86
v_fma_f32 v118,v104,v118,v103
v_fma_f32 v119,v104,v119,v103
M 18,a[224:227],82
R 50,2,4096
v_fma_f32 v120,v104,v120,v103
M 18,a[232:235],86
s_nop 0
v_fma_f32 v121,v104,v121,v103
v_mul_f32 v118,v30,v118
v_mul_f32 v119,v31,v119
v_mul_f32 v120,v32,v120
v_mul_f32 v121,v33,v121
v_exp_f32 v118,v118
v_exp_f32 v119,v119
v_exp_f32 v120,v120
v_exp_f32 v121,v121
M 18,a[240:243],90
R 54,2,5120
v_rcp_f32 v117,v117
v_fma_f32 v118,v118,v105,v105
v_fma_f32 v119,v119,v105,v105
v_fma_f32 v120,v120,v105,v105
v_fma_f32 v121,v121,v105,v105
v_rcp_f32 v118,v118
v_rcp_f32 v119,v119
v_rcp_f32 v120,v120
v_rcp_f32 v121,v121
M 18,a[248:251],94
v_mul_f32 v29,v29,v117
v_cvt_pk_bf16_f32 v123,v28,v29
v_mul_f32 v30,v30,v118
v_mul_f32 v31,v31,v119
v_mul_f32 v32,v32,v120
v_mul_f32 v33,v33,v121
v_cvt_pk_bf16_f32 v124,v30,v31
v_cvt_pk_bf16_f32 v125,v32,v33
v_cmp_eq_u32 vcc,s53,v7
M 22,a[244:247],90
s_cmp_eq_u32 vcc_lo,-1
R 58,2,6144
buffer_store_dwordx4 v[122:125],v0,s[36:39],s67 offen offset:0 sc0 sc1
M 22,a[252:255],94
L 5
s_cbranch_scc0 Lg3o_L1_%=
Lg3o_F1_%=:
v_mfma_f32_16x16x32_bf16 v[30:33],a[4:7],v[34:37],0
s_add_u32 m0,s57,0
R 62,2,7168
v_add_f32_dpp v131,v130,v130 quad_perm:[1,0,3,2] row_mask:0xf bank_mask:0xf
M 30,a[12:15],38
s_add_u32 s56,s55,0
buffer_load_dword v130,v11,s[36:39],s73 offen offset:0 sc1
v_mfma_f32_16x16x32_bf16 v[26:29],a[0:3],v[34:37],0
s_add_u32 s58,s58,s61
R 66,2,8192
v_add_f32_dpp v131,v131,v131 quad_perm:[2,3,0,1] row_mask:0xf bank_mask:0xf
M 26,a[8:11],38
L 5
D 0,56,0
M 26,a[16:19],42
s_mov_b32 s67,s65
R 70,2,9216
v_add_f32_dpp v134,v131,v131 row_half_mirror row_mask:0xf bank_mask:0xf
M 26,a[24:27],46
s_add_u32 s65,s65,s62
v_fma_f32 v18,-v98,v106,v18
v_fma_f32 v19,-v98,v107,v19
M 30,a[20:23],42
s_add_u32 s54,s54,s46
R 74,2,10240
v_mov_b32_dpp v131,v134 row_ror:8 row_mask:0xf bank_mask:0xf
M 30,a[28:31],46
L 5
v_fma_f32 v131,-v134,v134,v131
v_max_f32 v131,s87,v131
M 30,a[36:39],50
s_add_u32 s73,s73,s64
R 78,2,11264
v_fma_f32 v20,-v98,v108,v20
M 30,a[44:47],54
D 0,56,1024
M 26,a[32:35],50
R 82,2,12288
v_fma_f32 v21,-v98,v109,v21
M 26,a[40:43],54
L 5
v_rsq_f32 v132,v131
M 26,a[48:51],58
R 86,2,13312
v_mul_f32 v137,v131,v132
M 26,a[56:59],62
v_mul_f32 v135,s88,v132
v_mul_f32 v131,s89,v132
)"
R"(M 30,a[52:55],58
R 90,2,14336
v_mul_f32 v136,v135,v132
M 30,a[60:63],62
L 5
v_mul_f32 v136,v136,v131
v_mul_f32 v114,v18,v18
M 30,a[68:71],66
s_waitcnt vmcnt(4)
R 94,2,15360
v_mul_f32 v115,v19,v19
M 30,a[76:79],70
s_barrier
D 0,56,2048
M 26,a[64:67],66
R 34,2,16384
v_mul_f32 v116,v20,v20
M 26,a[72:75],70
L 5
ds_write_b128 v12,v[134:137] offset:2048
M 26,a[80:83],74
R 38,2,17408
v_mul_f32 v117,v21,v21
M 26,a[88:91],78
v_fma_f32 v114,v100,v114,v99
v_fma_f32 v115,v100,v115,v99
M 30,a[84:87],74
R 42,2,18432
v_fma_f32 v116,v100,v116,v99
M 30,a[92:95],78
L 6
D 0,56,3072
M 30,a[100:103],82
s_add_u32 m0,s59,1024
R 46,2,19456
v_fma_f32 v117,v100,v117,v99
M 30,a[108:111],86
s_add_u32 s56,s55,16384
v_mul_f32 v114,v18,v114
v_mul_f32 v115,v19,v115
M 26,a[96:99],82
s_add_u32 s55,s55,s60
R 50,2,20480
v_mul_f32 v116,v20,v116
M 26,a[104:107],86
L 6
v_exp_f32 v114,v114
M 26,a[112:115],90
R 54,2,21504
v_mul_f32 v117,v21,v117
M 26,a[120:123],94
v_exp_f32 v115,v115
M 30,a[116:119],90
R 58,2,22528
v_fma_f32 v114,v114,v101,v101
M 30,a[124:127],94
L 5
buffer_load_dword v8,s[36:39],s58 offen offset:0 sc1 lds
M 30,a[132:135],34
s_add_u32 m0,s57,16384
R 62,2,23552
v_fma_f32 v115,v115,v101,v101
M 30,a[140:143],38
s_sub_u32 s57,s84,s57
D 0,56,0
M 26,a[128:131],34
R 66,2,24576
v_fma_f32 v22,-v98,v110,v22
M 26,a[136:139],38
L 5
v_exp_f32 v116,v116
M 26,a[144:147],42
R 70,2,25600
v_fma_f32 v116,v116,v101,v101
M 26,a[152:155],46
v_exp_f32 v117,v117
M 30,a[148:151],42
R 74,2,26624
v_fma_f32 v117,v117,v101,v101
M 30,a[156:159],46
L 5
v_rcp_f32 v114,v114
M 30,a[164:167],50
R 78,2,27648
v_mul_f32 v18,v18,v114
M 30,a[172:175],54
D 0,56,1024
M 26,a[160:163],50
R 82,2,28672
v_fma_f32 v23,-v98,v111,v23
M 26,a[168:171],54
L 5
v_rcp_f32 v115,v115
M 26,a[176:179],58
R 86,2,29696
v_mul_f32 v19,v19,v115
M 26,a[184:187],62
v_rcp_f32 v116,v116
M 30,a[180:183],58
R 90,2,30720
v_mul_f32 v20,v20,v116
M 30,a[188:191],62
L 5
s_nop 0
D 0,56,2048
M 30,a[196:199],66
s_waitcnt vmcnt(3)
R 94,2,31744
v_cvt_pk_bf16_f32 v122,v18,v19
M 30,a[204:207],70
s_barrier
buffer_store_dword v4,v3,s[36:39],s70 offen offset:0 sc0 sc1
M 26,a[192:195],66
s_mov_b32 s70,s69
R 34,1,0
ds_read_b32 v7,v6 offset:1024
M 26,a[200:203],70
L 6
R 102,13,2048
v_fma_f32 v24,-v98,v112,v24
M 26,a[208:211],74
s_mov_b32 s69,s68
R 38,1,1024
v_fma_f32 v25,-v98,v113,v25
M 26,a[216:219],78
s_add_u32 s68,s68,s63
v_mul_f32 v118,v22,v22
v_mul_f32 v119,v23,v23
M 30,a[212:215],74
R 42,1,2048
v_mul_f32 v120,v24,v24
M 30,a[220:223],78
L 7
D 0,56,3072
M 30,a[228:231],82
L 3
R 46,1,3072
v_mul_f32 v121,v25,v25
M 30,a[236:239],86
v_fma_f32 v118,v100,v118,v99
v_fma_f32 v119,v100,v119,v99
M 26,a[224:227],82
R 50,1,4096
v_fma_f32 v120,v100,v120,v99
M 26,a[232:235],86
s_nop 0
v_fma_f32 v121,v100,v121,v99
v_mul_f32 v118,v22,v118
v_mul_f32 v119,v23,v119
v_mul_f32 v120,v24,v120
v_mul_f32 v121,v25,v121
v_exp_f32 v118,v118
v_exp_f32 v119,v119
v_exp_f32 v120,v120
v_exp_f32 v121,v121
M 26,a[240:243],90
R 54,1,5120
v_rcp_f32 v117,v117
v_fma_f32 v118,v118,v101,v101
v_fma_f32 v119,v119,v101,v101
v_fma_f32 v120,v120,v101,v101
v_fma_f32 v121,v121,v101,v101
v_rcp_f32 v118,v118
v_rcp_f32 v119,v119
v_rcp_f32 v120,v120
v_rcp_f32 v121,v121
M 26,a[248:251],94
v_mul_f32 v21,v21,v117
v_cvt_pk_bf16_f32 v123,v20,v21
v_mul_f32 v22,v22,v118
v_mul_f32 v23,v23,v119
v_mul_f32 v24,v24,v120
v_mul_f32 v25,v25,v121
v_cvt_pk_bf16_f32 v124,v22,v23
v_cvt_pk_bf16_f32 v125,v24,v25
v_cmp_eq_u32 vcc,s53,v7
M 30,a[244:247],90
s_cmp_eq_u32 vcc_lo,-1
R 58,1,6144
buffer_store_dwordx4 v[122:125],v0,s[36:39],s66 offen offset:0 sc0 sc1
M 30,a[252:255],94
L 5
s_cbranch_scc1 Lg3o_F0_%=
Lg3o_L0_%=:
v_mfma_f32_16x16x32_bf16 v[22:25],a[4:7],v[34:37],0
s_add_u32 m0,s59,3072
R 62,1,7168
v_add_f32_dpp v131,v130,v130 quad_perm:[1,0,3,2] row_mask:0xf bank_mask:0xf
M 22,a[12:15],38
s_mov_b32 s66,s65
buffer_load_dword v8,s[36:39],s58 offen offset:0 sc1 lds
v_mfma_f32_16x16x32_bf16 v[18:21],a[0:3],v[34:37],0
s_add_u32 s65,s65,s62
R 66,1,8192
v_add_f32_dpp v131,v131,v131 quad_perm:[2,3,0,1] row_mask:0xf bank_mask:0xf
M 18,a[8:11],38
L 5
v_fma_f32 v26,-v102,v106,v26
v_fma_f32 v27,-v102,v107,v27
M 18,a[16:19],42
R 70,1,9216
v_add_f32_dpp v134,v131,v131 row_half_mirror row_mask:0xf bank_mask:0xf
M 18,a[24:27],46
v_fma_f32 v28,-v102,v108,v28
v_fma_f32 v29,-v102,v109,v29
M 22,a[20:23],42
R 74,1,10240
v_mov_b32_dpp v131,v134 row_ror:8 row_mask:0xf bank_mask:0xf
M 22,a[28:31],46
L 5
v_fma_f32 v131,-v134,v134,v131
v_max_f32 v131,s87,v131
M 22,a[36:39],50
R 78,1,11264
v_mul_f32 v114,v26,v26
M 22,a[44:47],54
v_rsq_f32 v132,v131
M 18,a[32:35],50
R 82,1,12288
v_mul_f32 v137,v131,v132
M 18,a[40:43],54
L 5
v_mul_f32 v135,s88,v132
v_mul_f32 v131,s89,v132
M 18,a[48:51],58
R 86,1,13312
v_mul_f32 v136,v135,v132
M 18,a[56:59],62
v_mul_f32 v136,v136,v131
v_mul_f32 v115,v27,v27
M 22,a[52:55],58
R 90,1,14336
v_mul_f32 v116,v28,v28
M 22,a[60:63],62
L 5
v_mul_f32 v117,v29,v29
v_fma_f32 v114,v104,v114,v103
M 22,a[68:71],66
s_waitcnt vmcnt(0)
R 94,1,15360
v_fma_f32 v115,v104,v115,v103
M 22,a[76:79],70
s_barrier
ds_read_b32 v7,v6 offset:3072
v_fma_f32 v116,v104,v116,v103
M 18,a[64:67],66
R 34,1,16384
v_fma_f32 v117,v104,v117,v103
M 18,a[72:75],70
L 6
buffer_store_dword v4,v3,s[36:39],s70 offen offset:0 sc0 sc1
M 18,a[80:83],74
s_mov_b32 s70,0x7fff0000
R 38,1,17408
v_mul_f32 v114,v26,v114
M 18,a[88:91],78
ds_write_b128 v12,v[134:137] offset:0
M 22,a[84:87],74
R 42,1,18432
v_mul_f32 v115,v27,v115
M 22,a[92:95],78
L 7
L 4
v_cmp_eq_u32 vcc,s53,v7
M 22,a[100:103],82
R 46,1,19456
v_mul_f32 v116,v28,v116
M 22,a[108:111],86
s_cmp_eq_u32 vcc_lo,-1
v_exp_f32 v114,v114
s_cbranch_scc0 Lg3o_X0_%=
M 18,a[96:99],82
s_add_u32 m0,s57,0
s_add_u32 s56,s55,0
s_add_u32 s58,s58,s61
R 50,1,20480
M 18,a[104:107],86
s_nop 0
buffer_load_dword v130,v11,s[36:39],s73 offen offset:0 sc1
M 18,a[112:115],90
s_add_u32 s54,s54,s46
R 54,1,21504
v_mul_f32 v117,v29,v117
M 18,a[120:123],94
s_add_u32 s73,s73,s64
D 0,56,0
M 22,a[116:119],90
R 58,1,22528
v_fma_f32 v114,v114,v105,v105
M 22,a[124:127],94
L 6
v_exp_f32 v115,v115
M 22,a[132:135],34
R 62,1,23552
v_fma_f32 v115,v115,v105,v105
M 22,a[140:143],38
D 0,56,1024
M 18,a[128:131],34
R 66,1,24576
v_fma_f32 v30,-v102,v110,v30
M 18,a[136:139],38
L 5
v_exp_f32 v116,v116
M 18,a[144:147],42
R 70,1,25600
v_fma_f32 v116,v116,v105,v105
M 18,a[152:155],46
D 0,56,2048
M 22,a[148:151],42
R 74,1,26624
v_fma_f32 v31,-v102,v111,v31
M 22,a[156:159],46
L 5
v_exp_f32 v117,v117
M 22,a[164:167],50
R 78,1,27648
v_fma_f32 v117,v117,v105,v105
M 22,a[172:175],54
D 0,56,3072
M 18,a[160:163],50
s_add_u32 m0,s59,0
R 82,1,28672
v_fma_f32 v32,-v102,v112,v32
M 18,a[168:171],54
L 5
buffer_load_dword v8,s[36:39],s58 offen offset:0 sc1 lds
M 18,a[176:179],58
s_add_u32 m0,s57,16384
R 86,1,29696
v_fma_f32 v33,-v102,v113,v33
M 18,a[184:187],62
s_add_u32 s56,s55,16384
v_rcp_f32 v114,v114
M 22,a[180:183],58
s_add_u32 s55,s55,s60
R 90,1,30720
v_mul_f32 v26,v26,v114
M 22,a[188:191],62
L 5
s_nop 0
D 0,56,0
M 22,a[196:199],66
s_waitcnt vmcnt(1)
R 94,1,31744
v_mul_f32 v118,v30,v30
M 22,a[204:207],70
s_barrier
buffer_store_dword v4,v3,s[36:39],s70 offen offset:0 sc0 sc1
M 18,a[192:195],66
s_mov_b32 s70,s69
R 34,2,0
ds_read_b32 v7,v6 offset:0
M 18,a[200:203],70
L 6
D 0,56,1024
M 18,a[208:211],74
s_mov_b32 s69,s68
R 38,2,1024
R 98,13,0
M 18,a[216:219],78
s_sub_u32 s57,s84,s57
v_mul_f32 v119,v31,v31
v_mul_f32 v120,v32,v32
M 22,a[212:215],74
s_add_u32 s68,s68,s63
R 42,2,2048
v_mul_f32 v121,v33,v33
M 22,a[220:223],78
L 7
D 0,56,2048
M 22,a[228:231],82
L 3
R 46,2,3072
v_fma_f32 v118,v104,v118,v103
M 22,a[236:239],86
v_fma_f32 v119,v104,v119,v103
v_fma_f32 v120,v104,v120,v103
M 18,a[224:227],82
R 50,2,4096
v_fma_f32 v121,v104,v121,v103
M 18,a[232:235],86
s_nop 0
v_mul_f32 v118,v30,v118
v_mul_f32 v119,v31,v119
v_mul_f32 v120,v32,v120
v_mul_f32 v121,v33,v121
v_exp_f32 v118,v118
v_exp_f32 v119,v119
v_exp_f32 v120,v120
v_exp_f32 v121,v121
M 18,a[240:243],90
R 54,2,5120
v_rcp_f32 v115,v115
v_rcp_f32 v116,v116
v_rcp_f32 v117,v117
v_fma_f32 v118,v118,v105,v105
v_fma_f32 v119,v119,v105,v105
v_fma_f32 v120,v120,v105,v105
v_fma_f32 v121,v121,v105,v105
v_rcp_f32 v118,v118
v_rcp_f32 v119,v119
v_rcp_f32 v120,v120
v_rcp_f32 v121,v121
M 18,a[248:251],94
v_mul_f32 v27,v27,v115
v_mul_f32 v28,v28,v116
v_mul_f32 v29,v29,v117
v_cvt_pk_bf16_f32 v122,v26,v27
v_cvt_pk_bf16_f32 v123,v28,v29
v_mul_f32 v30,v30,v118
v_mul_f32 v31,v31,v119
v_mul_f32 v32,v32,v120
v_mul_f32 v33,v33,v121
v_cvt_pk_bf16_f32 v124,v30,v31
v_cvt_pk_bf16_f32 v125,v32,v33
v_cmp_eq_u32 vcc,s53,v7
M 22,a[244:247],90
s_cmp_eq_u32 vcc_lo,-1
R 58,2,6144
D 0,56,3072
buffer_store_dwordx4 v[122:125],v0,s[36:39],s67 offen offset:0 sc0 sc1
M 22,a[252:255],94
L 6
s_cbranch_scc1 Lg3o_F1_%=
s_branch Lg3o_L1_%=
Lg3o_X0_%=:
M 18,a[96:99],82
s_add_u32 m0,s59,0
R 50,1,20480
v_mul_f32 v117,v29,v117
M 18,a[104:107],86
s_nop 0
buffer_load_dword v8,s[36:39],s58 offen offset:0 sc1 lds
M 18,a[112:115],90
R 54,1,21504
v_fma_f32 v114,v114,v105,v105
M 18,a[120:123],94
v_exp_f32 v115,v115
M 22,a[116:119],90
R 58,1,22528
v_fma_f32 v115,v115,v105,v105
M 22,a[124:127],94
L 6
v_exp_f32 v116,v116
M 22,a[132:135],34
R 62,1,23552
v_fma_f32 v116,v116,v105,v105
M 22,a[140:143],38
v_exp_f32 v117,v117
M 18,a[128:131],34
R 66,1,24576
v_fma_f32 v117,v117,v105,v105
M 18,a[136:139],38
L 5
v_rcp_f32 v114,v114
M 18,a[144:147],42
R 70,1,25600
v_mul_f32 v26,v26,v114
M 18,a[152:155],46
v_rcp_f32 v115,v115
M 22,a[148:151],42
R 74,1,26624
v_mul_f32 v27,v27,v115
M 22,a[156:159],46
L 5
v_rcp_f32 v116,v116
M 22,a[164:167],50
R 78,1,27648
v_mul_f32 v28,v28,v116
M 22,a[172:175],54
v_rcp_f32 v117,v117
M 18,a[160:163],50
R 82,1,28672
v_mul_f32 v29,v29,v117
M 18,a[168:171],54
L 5
v_cvt_pk_bf16_f32 v122,v26,v27
v_cvt_pk_bf16_f32 v123,v28,v29
M 18,a[176:179],58
R 86,1,29696
v_fma_f32 v30,-v102,v110,v30
M 18,a[184:187],62
v_fma_f32 v31,-v102,v111,v31
v_fma_f32 v32,-v102,v112,v32
M 22,a[180:183],58
R 90,1,30720
v_fma_f32 v33,-v102,v113,v33
M 22,a[188:191],62
L 5
s_nop 0
v_mul_f32 v118,v30,v30
M 22,a[196:199],66
s_waitcnt vmcnt(0)
R 94,1,31744
v_mul_f32 v119,v31,v31
M 22,a[204:207],70
s_barrier
buffer_store_dword v4,v3,s[36:39],s70 offen offset:0 sc0 sc1
M 18,a[192:195],66
s_mov_b32 s70,s69
)"
R"(ds_read_b32 v7,v6 offset:0
R 98,13,0
M 18,a[200:203],70
L 6
v_mul_f32 v120,v32,v32
v_mul_f32 v121,v33,v33
M 18,a[208:211],74
s_mov_b32 s69,s68
v_fma_f32 v118,v104,v118,v103
v_fma_f32 v119,v104,v119,v103
M 18,a[216:219],78
s_add_u32 s68,s68,s63
v_fma_f32 v120,v104,v120,v103
v_fma_f32 v121,v104,v121,v103
M 22,a[212:215],74
v_mul_f32 v118,v30,v118
v_mul_f32 v119,v31,v119
M 22,a[220:223],78
L 4
v_mul_f32 v120,v32,v120
v_mul_f32 v121,v33,v121
M 22,a[228:231],82
L 1
v_exp_f32 v118,v118
M 22,a[236:239],86
v_exp_f32 v119,v119
M 18,a[224:227],82
v_exp_f32 v120,v120
M 18,a[232:235],86
s_nop 0
v_exp_f32 v121,v121
M 18,a[240:243],90
v_fma_f32 v118,v118,v105,v105
v_fma_f32 v119,v119,v105,v105
v_fma_f32 v120,v120,v105,v105
v_fma_f32 v121,v121,v105,v105
v_rcp_f32 v118,v118
v_rcp_f32 v119,v119
v_rcp_f32 v120,v120
v_rcp_f32 v121,v121
M 18,a[248:251],94
v_mul_f32 v30,v30,v118
v_mul_f32 v31,v31,v119
v_mul_f32 v32,v32,v120
v_mul_f32 v33,v33,v121
v_cvt_pk_bf16_f32 v124,v30,v31
v_cvt_pk_bf16_f32 v125,v32,v33
M 22,a[244:247],90
buffer_store_dwordx4 v[122:125],v0,s[36:39],s67 offen offset:0 sc0 sc1
v_cmp_eq_u32 vcc,s53,v7
M 22,a[252:255],94
s_mov_b32 s80,1
s_cmp_eq_u32 vcc_lo,-1
s_cbranch_scc0 Lg3o_xn0_%=
buffer_load_dword v130,v11,s[36:39],s73 offen offset:0 sc1
L 0
v_fma_f32 v18,-v98,v106,v18
v_fma_f32 v19,-v98,v107,v19
v_fma_f32 v20,-v98,v108,v20
v_fma_f32 v21,-v98,v109,v21
v_mul_f32 v114,v18,v18
v_mul_f32 v115,v19,v19
s_add_u32 s73,s73,s64
s_add_u32 m0,s57,0
s_add_u32 s56,s55,0
D 0,56,0
v_mul_f32 v116,v20,v20
v_mul_f32 v117,v21,v21
v_fma_f32 v114,v100,v114,v99
v_fma_f32 v115,v100,v115,v99
v_fma_f32 v116,v100,v116,v99
v_fma_f32 v117,v100,v117,v99
D 0,56,1024
v_mul_f32 v114,v18,v114
v_mul_f32 v115,v19,v115
v_mul_f32 v116,v20,v116
v_mul_f32 v117,v21,v117
v_exp_f32 v114,v114
v_exp_f32 v115,v115
D 0,56,2048
v_exp_f32 v116,v116
v_exp_f32 v117,v117
v_fma_f32 v114,v114,v101,v101
v_fma_f32 v115,v115,v101,v101
v_fma_f32 v116,v116,v101,v101
v_fma_f32 v117,v117,v101,v101
D 0,56,3072
v_rcp_f32 v114,v114
v_rcp_f32 v115,v115
v_rcp_f32 v116,v116
v_rcp_f32 v117,v117
v_mul_f32 v18,v18,v114
v_mul_f32 v19,v19,v115
s_add_u32 m0,s57,16384
s_add_u32 s56,s55,16384
D 0,56,0
v_mul_f32 v20,v20,v116
v_mul_f32 v21,v21,v117
v_cvt_pk_bf16_f32 v122,v18,v19
v_cvt_pk_bf16_f32 v123,v20,v21
v_fma_f32 v22,-v98,v110,v22
v_fma_f32 v23,-v98,v111,v23
D 0,56,1024
v_fma_f32 v24,-v98,v112,v24
v_fma_f32 v25,-v98,v113,v25
v_mul_f32 v118,v22,v22
v_mul_f32 v119,v23,v23
v_mul_f32 v120,v24,v24
v_mul_f32 v121,v25,v25
D 0,56,2048
v_fma_f32 v118,v100,v118,v99
v_fma_f32 v119,v100,v119,v99
v_fma_f32 v120,v100,v120,v99
v_fma_f32 v121,v100,v121,v99
v_mul_f32 v118,v22,v118
v_mul_f32 v119,v23,v119
D 0,56,3072
v_mul_f32 v120,v24,v120
v_mul_f32 v121,v25,v121
v_exp_f32 v118,v118
v_exp_f32 v119,v119
v_exp_f32 v120,v120
v_exp_f32 v121,v121
s_add_u32 s55,s55,s60
s_sub_u32 s57,s84,s57
s_add_u32 s54,s54,s46
v_fma_f32 v118,v118,v101,v101
v_fma_f32 v119,v119,v101,v101
v_fma_f32 v120,v120,v101,v101
v_fma_f32 v121,v121,v101,v101
v_rcp_f32 v118,v118
v_rcp_f32 v119,v119
v_rcp_f32 v120,v120
v_rcp_f32 v121,v121
v_mul_f32 v22,v22,v118
v_mul_f32 v23,v23,v119
v_mul_f32 v24,v24,v120
v_mul_f32 v25,v25,v121
v_cvt_pk_bf16_f32 v124,v22,v23
v_cvt_pk_bf16_f32 v125,v24,v25
buffer_store_dwordx4 v[122:125],v0,s[36:39],s66 offen offset:0 sc0 sc1
s_mov_b32 s72,s69
s_mov_b32 s69,0x7fff0000
s_mov_b32 s66,0x7fff0000
s_mov_b32 s76,0x7fff0000
s_branch Lg3o_gox_%=
Lg3o_xn0_%=:
s_cmp_ge_u32 s54,256
s_cbranch_scc1 Lg3o_fin0_%=
s_add_u32 m0,s59,4096
s_nop 0
buffer_load_dword v8,s[36:39],s58 offen offset:0 sc1 lds
buffer_store_dword v4,v3,s[36:39],s71 offen offset:0 sc0 sc1
s_add_u32 m0,s59,5120
s_nop 0
buffer_load_dword v8,s[36:39],s58 offen offset:0 sc1 lds
buffer_store_dword v4,v3,s[36:39],s71 offen offset:0 sc0 sc1
s_mov_b32 s83,0
L 0
v_fma_f32 v18,-v98,v106,v18
v_fma_f32 v19,-v98,v107,v19
v_fma_f32 v20,-v98,v108,v20
v_fma_f32 v21,-v98,v109,v21
v_mul_f32 v114,v18,v18
v_mul_f32 v115,v19,v19
v_mul_f32 v116,v20,v20
v_mul_f32 v117,v21,v21
v_fma_f32 v114,v100,v114,v99
v_fma_f32 v115,v100,v115,v99
v_fma_f32 v116,v100,v116,v99
v_fma_f32 v117,v100,v117,v99
v_mul_f32 v114,v18,v114
v_mul_f32 v115,v19,v115
v_mul_f32 v116,v20,v116
v_mul_f32 v117,v21,v117
v_exp_f32 v114,v114
v_exp_f32 v115,v115
v_exp_f32 v116,v116
v_exp_f32 v117,v117
v_fma_f32 v114,v114,v101,v101
v_fma_f32 v115,v115,v101,v101
v_fma_f32 v116,v116,v101,v101
v_fma_f32 v117,v117,v101,v101
v_rcp_f32 v114,v114
v_rcp_f32 v115,v115
v_rcp_f32 v116,v116
v_rcp_f32 v117,v117
v_mul_f32 v18,v18,v114
v_mul_f32 v19,v19,v115
v_mul_f32 v20,v20,v116
v_mul_f32 v21,v21,v117
v_cvt_pk_bf16_f32 v122,v18,v19
v_cvt_pk_bf16_f32 v123,v20,v21
v_fma_f32 v22,-v98,v110,v22
v_fma_f32 v23,-v98,v111,v23
v_fma_f32 v24,-v98,v112,v24
v_fma_f32 v25,-v98,v113,v25
v_mul_f32 v118,v22,v22
v_mul_f32 v119,v23,v23
v_mul_f32 v120,v24,v24
v_mul_f32 v121,v25,v25
v_fma_f32 v118,v100,v118,v99
v_fma_f32 v119,v100,v119,v99
v_fma_f32 v120,v100,v120,v99
v_fma_f32 v121,v100,v121,v99
v_mul_f32 v118,v22,v118
v_mul_f32 v119,v23,v119
v_mul_f32 v120,v24,v120
v_mul_f32 v121,v25,v121
v_exp_f32 v118,v118
v_exp_f32 v119,v119
v_exp_f32 v120,v120
v_exp_f32 v121,v121
v_fma_f32 v118,v118,v101,v101
v_fma_f32 v119,v119,v101,v101
v_fma_f32 v120,v120,v101,v101
v_fma_f32 v121,v121,v101,v101
v_rcp_f32 v118,v118
v_rcp_f32 v119,v119
v_rcp_f32 v120,v120
v_rcp_f32 v121,v121
v_mul_f32 v22,v22,v118
v_mul_f32 v23,v23,v119
v_mul_f32 v24,v24,v120
v_mul_f32 v25,v25,v121
v_cvt_pk_bf16_f32 v124,v22,v23
v_cvt_pk_bf16_f32 v125,v24,v25
buffer_store_dwordx4 v[122:125],v0,s[36:39],s66 offen offset:0 sc0 sc1
s_mov_b32 s72,s69
s_mov_b32 s69,0x7fff0000
s_mov_b32 s66,0x7fff0000
s_mov_b32 s76,0x7fff0000
s_branch Lg3o_poll_%=
Lg3o_fin0_%=:
L 0
v_fma_f32 v18,-v98,v106,v18
v_fma_f32 v19,-v98,v107,v19
v_fma_f32 v20,-v98,v108,v20
v_fma_f32 v21,-v98,v109,v21
v_mul_f32 v114,v18,v18
v_mul_f32 v115,v19,v19
v_mul_f32 v116,v20,v20
v_mul_f32 v117,v21,v21
v_fma_f32 v114,v100,v114,v99
v_fma_f32 v115,v100,v115,v99
v_fma_f32 v116,v100,v116,v99
v_fma_f32 v117,v100,v117,v99
v_mul_f32 v114,v18,v114
v_mul_f32 v115,v19,v115
v_mul_f32 v116,v20,v116
v_mul_f32 v117,v21,v117
v_exp_f32 v114,v114
v_exp_f32 v115,v115
v_exp_f32 v116,v116
v_exp_f32 v117,v117
v_fma_f32 v114,v114,v101,v101
v_fma_f32 v115,v115,v101,v101
v_fma_f32 v116,v116,v101,v101
v_fma_f32 v117,v117,v101,v101
v_rcp_f32 v114,v114
v_rcp_f32 v115,v115
v_rcp_f32 v116,v116
v_rcp_f32 v117,v117
v_mul_f32 v18,v18,v114
v_mul_f32 v19,v19,v115
v_mul_f32 v20,v20,v116
v_mul_f32 v21,v21,v117
v_cvt_pk_bf16_f32 v122,v18,v19
v_cvt_pk_bf16_f32 v123,v20,v21
v_fma_f32 v22,-v98,v110,v22
v_fma_f32 v23,-v98,v111,v23
v_fma_f32 v24,-v98,v112,v24
v_fma_f32 v25,-v98,v113,v25
v_mul_f32 v118,v22,v22
v_mul_f32 v119,v23,v23
v_mul_f32 v120,v24,v24
v_mul_f32 v121,v25,v25
v_fma_f32 v118,v100,v118,v99
v_fma_f32 v119,v100,v119,v99
v_fma_f32 v120,v100,v120,v99
v_fma_f32 v121,v100,v121,v99
v_mul_f32 v118,v22,v118
v_mul_f32 v119,v23,v119
v_mul_f32 v120,v24,v120
v_mul_f32 v121,v25,v121
v_exp_f32 v118,v118
v_exp_f32 v119,v119
v_exp_f32 v120,v120
v_exp_f32 v121,v121
v_fma_f32 v118,v118,v101,v101
v_fma_f32 v119,v119,v101,v101
v_fma_f32 v120,v120,v101,v101
v_fma_f32 v121,v121,v101,v101
v_rcp_f32 v118,v118
v_rcp_f32 v119,v119
v_rcp_f32 v120,v120
v_rcp_f32 v121,v121
v_mul_f32 v22,v22,v118
v_mul_f32 v23,v23,v119
v_mul_f32 v24,v24,v120
v_mul_f32 v25,v25,v121
v_cvt_pk_bf16_f32 v124,v22,v23
v_cvt_pk_bf16_f32 v125,v24,v25
buffer_store_dwordx4 v[122:125],v0,s[36:39],s66 offen offset:0 sc0 sc1
s_mov_b32 s72,s69
s_mov_b32 s69,0x7fff0000
s_mov_b32 s66,0x7fff0000
s_mov_b32 s76,0x7fff0000
s_branch Lg3o_done_%=
Lg3o_L1_%=:
v_mfma_f32_16x16x32_bf16 v[30:33],a[4:7],v[34:37],0
s_add_u32 m0,s59,3072
R 62,2,7168
v_add_f32_dpp v131,v130,v130 quad_perm:[1,0,3,2] row_mask:0xf bank_mask:0xf
M 30,a[12:15],38
s_mov_b32 s67,s65
buffer_load_dword v8,s[36:39],s58 offen offset:0 sc1 lds
v_mfma_f32_16x16x32_bf16 v[26:29],a[0:3],v[34:37],0
s_add_u32 s65,s65,s62
R 66,2,8192
v_add_f32_dpp v131,v131,v131 quad_perm:[2,3,0,1] row_mask:0xf bank_mask:0xf
M 26,a[8:11],38
L 5
v_fma_f32 v18,-v98,v106,v18
v_fma_f32 v19,-v98,v107,v19
M 26,a[16:19],42
R 70,2,9216
v_add_f32_dpp v134,v131,v131 row_half_mirror row_mask:0xf bank_mask:0xf
M 26,a[24:27],46
v_fma_f32 v20,-v98,v108,v20
v_fma_f32 v21,-v98,v109,v21
M 30,a[20:23],42
R 74,2,10240
v_mov_b32_dpp v131,v134 row_ror:8 row_mask:0xf bank_mask:0xf
M 30,a[28:31],46
L 5
v_fma_f32 v131,-v134,v134,v131
v_max_f32 v131,s87,v131
M 30,a[36:39],50
R 78,2,11264
v_mul_f32 v114,v18,v18
M 30,a[44:47],54
v_rsq_f32 v132,v131
M 26,a[32:35],50
R 82,2,12288
v_mul_f32 v137,v131,v132
M 26,a[40:43],54
L 5
v_mul_f32 v135,s88,v132
v_mul_f32 v131,s89,v132
M 26,a[48:51],58
R 86,2,13312
v_mul_f32 v136,v135,v132
M 26,a[56:59],62
v_mul_f32 v136,v136,v131
v_mul_f32 v115,v19,v19
M 30,a[52:55],58
R 90,2,14336
v_mul_f32 v116,v20,v20
M 30,a[60:63],62
L 5
v_mul_f32 v117,v21,v21
v_fma_f32 v114,v100,v114,v99
M 30,a[68:71],66
s_waitcnt vmcnt(0)
R 94,2,15360
v_fma_f32 v115,v100,v115,v99
M 30,a[76:79],70
s_barrier
ds_read_b32 v7,v6 offset:3072
v_fma_f32 v116,v100,v116,v99
M 26,a[64:67],66
R 34,2,16384
v_fma_f32 v117,v100,v117,v99
M 26,a[72:75],70
L 6
buffer_store_dword v4,v3,s[36:39],s70 offen offset:0 sc0 sc1
M 26,a[80:83],74
s_mov_b32 s70,0x7fff0000
R 38,2,17408
v_mul_f32 v114,v18,v114
M 26,a[88:91],78
ds_write_b128 v12,v[134:137] offset:2048
M 30,a[84:87],74
R 42,2,18432
v_mul_f32 v115,v19,v115
M 30,a[92:95],78
L 7
L 4
v_cmp_eq_u32 vcc,s53,v7
M 30,a[100:103],82
R 46,2,19456
v_mul_f32 v116,v20,v116
M 30,a[108:111],86
s_cmp_eq_u32 vcc_lo,-1
v_exp_f32 v114,v114
s_cbranch_scc0 Lg3o_X1_%=
M 26,a[96:99],82
s_add_u32 m0,s57,0
s_add_u32 s56,s55,0
s_add_u32 s58,s58,s61
R 50,2,20480
M 26,a[104:107],86
s_nop 0
buffer_load_dword v130,v11,s[36:39],s73 offen offset:0 sc1
M 26,a[112:115],90
s_add_u32 s54,s54,s46
R 54,2,21504
v_mul_f32 v117,v21,v117
M 26,a[120:123],94
s_add_u32 s73,s73,s64
D 0,56,0
M 30,a[116:119],90
R 58,2,22528
v_fma_f32 v114,v114,v101,v101
M 30,a[124:127],94
L 6
v_exp_f32 v115,v115
M 30,a[132:135],34
R 62,2,23552
v_fma_f32 v115,v115,v101,v101
M 30,a[140:143],38
D 0,56,1024
M 26,a[128:131],34
R 66,2,24576
v_fma_f32 v22,-v98,v110,v22
M 26,a[136:139],38
L 5
v_exp_f32 v116,v116
M 26,a[144:147],42
R 70,2,25600
v_fma_f32 v116,v116,v101,v101
M 26,a[152:155],46
D 0,56,2048
M 30,a[148:151],42
R 74,2,26624
v_fma_f32 v23,-v98,v111,v23
M 30,a[156:159],46
L 5
v_exp_f32 v117,v117
M 30,a[164:167],50
R 78,2,27648
v_fma_f32 v117,v117,v101,v101
M 30,a[172:175],54
D 0,56,3072
M 26,a[160:163],50
s_add_u32 m0,s59,1024
R 82,2,28672
v_fma_f32 v24,-v98,v112,v24
M 26,a[168:171],54
L 5
buffer_load_dword v8,s[36:39],s58 offen offset:0 sc1 lds
M 26,a[176:179],58
s_add_u32 m0,s57,16384
R 86,2,29696
v_fma_f32 v25,-v98,v113,v25
M 26,a[184:187],62
s_add_u32 s56,s55,16384
v_rcp_f32 v114,v114
M 30,a[180:183],58
s_add_u32 s55,s55,s60
R 90,2,30720
v_mul_f32 v18,v18,v114
M 30,a[188:191],62
L 5
s_nop 0
D 0,56,0
M 30,a[196:199],66
s_waitcnt vmcnt(1)
R 94,2,31744
v_mul_f32 v118,v22,v22
M 30,a[204:207],70
s_barrier
buffer_store_dword v4,v3,s[36:39],s70 offen offset:0 sc0 sc1
M 26,a[192:195],66
s_mov_b32 s70,s69
R 34,1,0
ds_read_b32 v7,v6 offset:1024
M 26,a[200:203],70
L 6
D 0,56,1024
M 26,a[208:211],74
s_mov_b32 s69,s68
R 38,1,1024
R 102,13,2048
M 26,a[216:219],78
s_sub_u32 s57,s84,s57
v_mul_f32 v119,v23,v23
v_mul_f32 v120,v24,v24
M 30,a[212:215],74
s_add_u32 s68,s68,s63
R 42,1,2048
v_mul_f32 v121,v25,v25
M 30,a[220:223],78
L 7
D 0,56,2048
M 30,a[228:231],82
L 3
R 46,1,3072
v_fma_f32 v118,v100,v118,v99
)"
R"(M 30,a[236:239],86
v_fma_f32 v119,v100,v119,v99
v_fma_f32 v120,v100,v120,v99
M 26,a[224:227],82
R 50,1,4096
v_fma_f32 v121,v100,v121,v99
M 26,a[232:235],86
s_nop 0
v_mul_f32 v118,v22,v118
v_mul_f32 v119,v23,v119
v_mul_f32 v120,v24,v120
v_mul_f32 v121,v25,v121
v_exp_f32 v118,v118
v_exp_f32 v119,v119
v_exp_f32 v120,v120
v_exp_f32 v121,v121
M 26,a[240:243],90
R 54,1,5120
v_rcp_f32 v115,v115
v_rcp_f32 v116,v116
v_rcp_f32 v117,v117
v_fma_f32 v118,v118,v101,v101
v_fma_f32 v119,v119,v101,v101
v_fma_f32 v120,v120,v101,v101
v_fma_f32 v121,v121,v101,v101
v_rcp_f32 v118,v118
v_rcp_f32 v119,v119
v_rcp_f32 v120,v120
v_rcp_f32 v121,v121
M 26,a[248:251],94
v_mul_f32 v19,v19,v115
v_mul_f32 v20,v20,v116
v_mul_f32 v21,v21,v117
v_cvt_pk_bf16_f32 v122,v18,v19
v_cvt_pk_bf16_f32 v123,v20,v21
v_mul_f32 v22,v22,v118
v_mul_f32 v23,v23,v119
v_mul_f32 v24,v24,v120
v_mul_f32 v25,v25,v121
v_cvt_pk_bf16_f32 v124,v22,v23
v_cvt_pk_bf16_f32 v125,v24,v25
v_cmp_eq_u32 vcc,s53,v7
M 30,a[244:247],90
s_cmp_eq_u32 vcc_lo,-1
R 58,1,6144
D 0,56,3072
buffer_store_dwordx4 v[122:125],v0,s[36:39],s66 offen offset:0 sc0 sc1
M 30,a[252:255],94
L 6
s_cbranch_scc1 Lg3o_F0_%=
s_branch Lg3o_L0_%=
Lg3o_X1_%=:
M 26,a[96:99],82
s_add_u32 m0,s59,1024
R 50,2,20480
v_mul_f32 v117,v21,v117
M 26,a[104:107],86
s_nop 0
buffer_load_dword v8,s[36:39],s58 offen offset:0 sc1 lds
M 26,a[112:115],90
R 54,2,21504
v_fma_f32 v114,v114,v101,v101
M 26,a[120:123],94
v_exp_f32 v115,v115
M 30,a[116:119],90
R 58,2,22528
v_fma_f32 v115,v115,v101,v101
M 30,a[124:127],94
L 6
v_exp_f32 v116,v116
M 30,a[132:135],34
R 62,2,23552
v_fma_f32 v116,v116,v101,v101
M 30,a[140:143],38
v_exp_f32 v117,v117
M 26,a[128:131],34
R 66,2,24576
v_fma_f32 v117,v117,v101,v101
M 26,a[136:139],38
L 5
v_rcp_f32 v114,v114
M 26,a[144:147],42
R 70,2,25600
v_mul_f32 v18,v18,v114
M 26,a[152:155],46
v_rcp_f32 v115,v115
M 30,a[148:151],42
R 74,2,26624
v_mul_f32 v19,v19,v115
M 30,a[156:159],46
L 5
v_rcp_f32 v116,v116
M 30,a[164:167],50
R 78,2,27648
v_mul_f32 v20,v20,v116
M 30,a[172:175],54
v_rcp_f32 v117,v117
M 26,a[160:163],50
R 82,2,28672
v_mul_f32 v21,v21,v117
M 26,a[168:171],54
L 5
v_cvt_pk_bf16_f32 v122,v18,v19
v_cvt_pk_bf16_f32 v123,v20,v21
M 26,a[176:179],58
R 86,2,29696
v_fma_f32 v22,-v98,v110,v22
M 26,a[184:187],62
v_fma_f32 v23,-v98,v111,v23
v_fma_f32 v24,-v98,v112,v24
M 30,a[180:183],58
R 90,2,30720
v_fma_f32 v25,-v98,v113,v25
M 30,a[188:191],62
L 5
s_nop 0
v_mul_f32 v118,v22,v22
M 30,a[196:199],66
s_waitcnt vmcnt(0)
R 94,2,31744
v_mul_f32 v119,v23,v23
M 30,a[204:207],70
s_barrier
buffer_store_dword v4,v3,s[36:39],s70 offen offset:0 sc0 sc1
M 26,a[192:195],66
s_mov_b32 s70,s69
ds_read_b32 v7,v6 offset:1024
R 102,13,2048
M 26,a[200:203],70
L 6
v_mul_f32 v120,v24,v24
v_mul_f32 v121,v25,v25
M 26,a[208:211],74
s_mov_b32 s69,s68
v_fma_f32 v118,v100,v118,v99
v_fma_f32 v119,v100,v119,v99
M 26,a[216:219],78
s_add_u32 s68,s68,s63
v_fma_f32 v120,v100,v120,v99
v_fma_f32 v121,v100,v121,v99
M 30,a[212:215],74
v_mul_f32 v118,v22,v118
v_mul_f32 v119,v23,v119
M 30,a[220:223],78
L 4
v_mul_f32 v120,v24,v120
v_mul_f32 v121,v25,v121
M 30,a[228:231],82
L 1
v_exp_f32 v118,v118
M 30,a[236:239],86
v_exp_f32 v119,v119
M 26,a[224:227],82
v_exp_f32 v120,v120
M 26,a[232:235],86
s_nop 0
v_exp_f32 v121,v121
M 26,a[240:243],90
v_fma_f32 v118,v118,v101,v101
v_fma_f32 v119,v119,v101,v101
v_fma_f32 v120,v120,v101,v101
v_fma_f32 v121,v121,v101,v101
v_rcp_f32 v118,v118
v_rcp_f32 v119,v119
v_rcp_f32 v120,v120
v_rcp_f32 v121,v121
M 26,a[248:251],94
v_mul_f32 v22,v22,v118
v_mul_f32 v23,v23,v119
v_mul_f32 v24,v24,v120
v_mul_f32 v25,v25,v121
v_cvt_pk_bf16_f32 v124,v22,v23
v_cvt_pk_bf16_f32 v125,v24,v25
M 30,a[244:247],90
buffer_store_dwordx4 v[122:125],v0,s[36:39],s66 offen offset:0 sc0 sc1
v_cmp_eq_u32 vcc,s53,v7
M 30,a[252:255],94
s_mov_b32 s80,0
s_cmp_eq_u32 vcc_lo,-1
s_cbranch_scc0 Lg3o_xn1_%=
buffer_load_dword v130,v11,s[36:39],s73 offen offset:0 sc1
L 0
v_fma_f32 v26,-v102,v106,v26
v_fma_f32 v27,-v102,v107,v27
v_fma_f32 v28,-v102,v108,v28
v_fma_f32 v29,-v102,v109,v29
v_mul_f32 v114,v26,v26
v_mul_f32 v115,v27,v27
s_add_u32 s73,s73,s64
s_add_u32 m0,s57,0
s_add_u32 s56,s55,0
D 0,56,0
v_mul_f32 v116,v28,v28
v_mul_f32 v117,v29,v29
v_fma_f32 v114,v104,v114,v103
v_fma_f32 v115,v104,v115,v103
v_fma_f32 v116,v104,v116,v103
v_fma_f32 v117,v104,v117,v103
D 0,56,1024
v_mul_f32 v114,v26,v114
v_mul_f32 v115,v27,v115
v_mul_f32 v116,v28,v116
v_mul_f32 v117,v29,v117
v_exp_f32 v114,v114
v_exp_f32 v115,v115
D 0,56,2048
v_exp_f32 v116,v116
v_exp_f32 v117,v117
v_fma_f32 v114,v114,v105,v105
v_fma_f32 v115,v115,v105,v105
v_fma_f32 v116,v116,v105,v105
v_fma_f32 v117,v117,v105,v105
D 0,56,3072
v_rcp_f32 v114,v114
v_rcp_f32 v115,v115
v_rcp_f32 v116,v116
v_rcp_f32 v117,v117
v_mul_f32 v26,v26,v114
v_mul_f32 v27,v27,v115
s_add_u32 m0,s57,16384
s_add_u32 s56,s55,16384
D 0,56,0
v_mul_f32 v28,v28,v116
v_mul_f32 v29,v29,v117
v_cvt_pk_bf16_f32 v122,v26,v27
v_cvt_pk_bf16_f32 v123,v28,v29
v_fma_f32 v30,-v102,v110,v30
v_fma_f32 v31,-v102,v111,v31
D 0,56,1024
v_fma_f32 v32,-v102,v112,v32
v_fma_f32 v33,-v102,v113,v33
v_mul_f32 v118,v30,v30
v_mul_f32 v119,v31,v31
v_mul_f32 v120,v32,v32
v_mul_f32 v121,v33,v33
D 0,56,2048
v_fma_f32 v118,v104,v118,v103
v_fma_f32 v119,v104,v119,v103
v_fma_f32 v120,v104,v120,v103
v_fma_f32 v121,v104,v121,v103
v_mul_f32 v118,v30,v118
v_mul_f32 v119,v31,v119
D 0,56,3072
v_mul_f32 v120,v32,v120
v_mul_f32 v121,v33,v121
v_exp_f32 v118,v118
v_exp_f32 v119,v119
v_exp_f32 v120,v120
v_exp_f32 v121,v121
s_add_u32 s55,s55,s60
s_sub_u32 s57,s84,s57
s_add_u32 s54,s54,s46
v_fma_f32 v118,v118,v105,v105
v_fma_f32 v119,v119,v105,v105
v_fma_f32 v120,v120,v105,v105
v_fma_f32 v121,v121,v105,v105
v_rcp_f32 v118,v118
v_rcp_f32 v119,v119
v_rcp_f32 v120,v120
v_rcp_f32 v121,v121
v_mul_f32 v30,v30,v118
v_mul_f32 v31,v31,v119
v_mul_f32 v32,v32,v120
v_mul_f32 v33,v33,v121
v_cvt_pk_bf16_f32 v124,v30,v31
v_cvt_pk_bf16_f32 v125,v32,v33
buffer_store_dwordx4 v[122:125],v0,s[36:39],s67 offen offset:0 sc0 sc1
s_mov_b32 s72,s69
s_mov_b32 s69,0x7fff0000
s_mov_b32 s67,0x7fff0000
s_mov_b32 s77,0x7fff0000
s_branch Lg3o_gox_%=
Lg3o_xn1_%=:
s_cmp_ge_u32 s54,256
s_cbranch_scc1 Lg3o_fin1_%=
s_add_u32 m0,s59,4096
s_nop 0
buffer_load_dword v8,s[36:39],s58 offen offset:0 sc1 lds
buffer_store_dword v4,v3,s[36:39],s71 offen offset:0 sc0 sc1
s_add_u32 m0,s59,5120
s_nop 0
buffer_load_dword v8,s[36:39],s58 offen offset:0 sc1 lds
buffer_store_dword v4,v3,s[36:39],s71 offen offset:0 sc0 sc1
s_mov_b32 s83,0
L 0
v_fma_f32 v26,-v102,v106,v26
v_fma_f32 v27,-v102,v107,v27
v_fma_f32 v28,-v102,v108,v28
v_fma_f32 v29,-v102,v109,v29
v_mul_f32 v114,v26,v26
v_mul_f32 v115,v27,v27
v_mul_f32 v116,v28,v28
v_mul_f32 v117,v29,v29
v_fma_f32 v114,v104,v114,v103
v_fma_f32 v115,v104,v115,v103
v_fma_f32 v116,v104,v116,v103
v_fma_f32 v117,v104,v117,v103
v_mul_f32 v114,v26,v114
v_mul_f32 v115,v27,v115
v_mul_f32 v116,v28,v116
v_mul_f32 v117,v29,v117
v_exp_f32 v114,v114
v_exp_f32 v115,v115
v_exp_f32 v116,v116
v_exp_f32 v117,v117
v_fma_f32 v114,v114,v105,v105
v_fma_f32 v115,v115,v105,v105
v_fma_f32 v116,v116,v105,v105
v_fma_f32 v117,v117,v105,v105
v_rcp_f32 v114,v114
v_rcp_f32 v115,v115
v_rcp_f32 v116,v116
v_rcp_f32 v117,v117
v_mul_f32 v26,v26,v114
v_mul_f32 v27,v27,v115
v_mul_f32 v28,v28,v116
v_mul_f32 v29,v29,v117
v_cvt_pk_bf16_f32 v122,v26,v27
v_cvt_pk_bf16_f32 v123,v28,v29
v_fma_f32 v30,-v102,v110,v30
v_fma_f32 v31,-v102,v111,v31
v_fma_f32 v32,-v102,v112,v32
v_fma_f32 v33,-v102,v113,v33
v_mul_f32 v118,v30,v30
v_mul_f32 v119,v31,v31
v_mul_f32 v120,v32,v32
v_mul_f32 v121,v33,v33
v_fma_f32 v118,v104,v118,v103
v_fma_f32 v119,v104,v119,v103
v_fma_f32 v120,v104,v120,v103
v_fma_f32 v121,v104,v121,v103
v_mul_f32 v118,v30,v118
v_mul_f32 v119,v31,v119
v_mul_f32 v120,v32,v120
v_mul_f32 v121,v33,v121
v_exp_f32 v118,v118
v_exp_f32 v119,v119
v_exp_f32 v120,v120
v_exp_f32 v121,v121
v_fma_f32 v118,v118,v105,v105
v_fma_f32 v119,v119,v105,v105
v_fma_f32 v120,v120,v105,v105
v_fma_f32 v121,v121,v105,v105
v_rcp_f32 v118,v118
v_rcp_f32 v119,v119
v_rcp_f32 v120,v120
v_rcp_f32 v121,v121
v_mul_f32 v30,v30,v118
v_mul_f32 v31,v31,v119
v_mul_f32 v32,v32,v120
v_mul_f32 v33,v33,v121
v_cvt_pk_bf16_f32 v124,v30,v31
v_cvt_pk_bf16_f32 v125,v32,v33
buffer_store_dwordx4 v[122:125],v0,s[36:39],s67 offen offset:0 sc0 sc1
s_mov_b32 s72,s69
s_mov_b32 s69,0x7fff0000
s_mov_b32 s67,0x7fff0000
s_mov_b32 s77,0x7fff0000
s_branch Lg3o_poll_%=
Lg3o_fin1_%=:
L 0
v_fma_f32 v26,-v102,v106,v26
v_fma_f32 v27,-v102,v107,v27
v_fma_f32 v28,-v102,v108,v28
v_fma_f32 v29,-v102,v109,v29
v_mul_f32 v114,v26,v26
v_mul_f32 v115,v27,v27
v_mul_f32 v116,v28,v28
v_mul_f32 v117,v29,v29
v_fma_f32 v114,v104,v114,v103
v_fma_f32 v115,v104,v115,v103
v_fma_f32 v116,v104,v116,v103
v_fma_f32 v117,v104,v117,v103
v_mul_f32 v114,v26,v114
v_mul_f32 v115,v27,v115
v_mul_f32 v116,v28,v116
v_mul_f32 v117,v29,v117
v_exp_f32 v114,v114
v_exp_f32 v115,v115
v_exp_f32 v116,v116
v_exp_f32 v117,v117
v_fma_f32 v114,v114,v105,v105
v_fma_f32 v115,v115,v105,v105
v_fma_f32 v116,v116,v105,v105
v_fma_f32 v117,v117,v105,v105
v_rcp_f32 v114,v114
v_rcp_f32 v115,v115
v_rcp_f32 v116,v116
v_rcp_f32 v117,v117
v_mul_f32 v26,v26,v114
v_mul_f32 v27,v27,v115
v_mul_f32 v28,v28,v116
v_mul_f32 v29,v29,v117
v_cvt_pk_bf16_f32 v122,v26,v27
v_cvt_pk_bf16_f32 v123,v28,v29
v_fma_f32 v30,-v102,v110,v30
v_fma_f32 v31,-v102,v111,v31
v_fma_f32 v32,-v102,v112,v32
v_fma_f32 v33,-v102,v113,v33
v_mul_f32 v118,v30,v30
v_mul_f32 v119,v31,v31
v_mul_f32 v120,v32,v32
v_mul_f32 v121,v33,v33
v_fma_f32 v118,v104,v118,v103
v_fma_f32 v119,v104,v119,v103
v_fma_f32 v120,v104,v120,v103
v_fma_f32 v121,v104,v121,v103
v_mul_f32 v118,v30,v118
v_mul_f32 v119,v31,v119
v_mul_f32 v120,v32,v120
v_mul_f32 v121,v33,v121
v_exp_f32 v118,v118
v_exp_f32 v119,v119
v_exp_f32 v120,v120
v_exp_f32 v121,v121
v_fma_f32 v118,v118,v105,v105
v_fma_f32 v119,v119,v105,v105
v_fma_f32 v120,v120,v105,v105
v_fma_f32 v121,v121,v105,v105
v_rcp_f32 v118,v118
v_rcp_f32 v119,v119
v_rcp_f32 v120,v120
v_rcp_f32 v121,v121
v_mul_f32 v30,v30,v118
v_mul_f32 v31,v31,v119
v_mul_f32 v32,v32,v120
v_mul_f32 v33,v33,v121
v_cvt_pk_bf16_f32 v124,v30,v31
v_cvt_pk_bf16_f32 v125,v32,v33
buffer_store_dwordx4 v[122:125],v0,s[36:39],s67 offen offset:0 sc0 sc1
s_mov_b32 s72,s69
s_mov_b32 s69,0x7fff0000
s_mov_b32 s67,0x7fff0000
s_mov_b32 s77,0x7fff0000
s_branch Lg3o_done_%=
Lg3o_done_%=:
s_waitcnt vmcnt(1)
s_barrier
buffer_store_dword v4,v3,s[36:39],s70 offen offset:0 sc0 sc1
s_waitcnt vmcnt(1)
s_barrier
buffer_store_dword v4,v3,s[36:39],s72 offen offset:0 sc0 sc1
s_waitcnt vmcnt(0) lgkmcnt(0)
s_barrier
s_endpgm
Lcode_g3o_e:
.purgem M
.purgem R
.purgem T
.purgem D
.purgem L
)"
  :: "s"(ws), "s"(sWave), "s"(sG0), "s"(sStep), "s"(sOutOff), "s"(sFoutOff), "s"(sAuxOff), "s"(sWw), "s"(sBuf), "s"(sRk), "v"(vL16), "v"(vFlag), "v"(vSnapOff), "v"(vSnapRd), "v"(vWd0), "v"(vWd1), "v"(vWr0), "v"(vWr1), "v"(vWr2), "v"(vOutS), "v"(vAux0), "v"(vAux1), "v"(vAux2), "v"(vAux3), "v"(vAn), "v"(vAe), "s"(w3) : "v0", "v1", "v2", "v3", "v4", "v5", "v6", "v7", "v8", "v9", "v10", "v11", "v12", "v13", "v14", "v15", "v16", "v17", "v18", "v19", "v20", "v21", "v22", "v23", "v24", "v25", "v26", "v27", "v28", "v29", "v30", "v31", "v32", "v33", "v34", "v35", "v36", "v37", "v38", "v39", "v40", "v41", "v42", "v43", "v44", "v45", "v46", "v47", "v48", "v49", "v50", "v51", "v52", "v53", "v54", "v55", "v56", "v57", "v58", "v59", "v60", "v61", "v62", "v63", "v64", "v65", "v66", "v67", "v68", "v69", "v70", "v71", "v72", "v73", "v74", "v75", "v76", "v77", "v78", "v79", "v80", "v81", "v82", "v83", "v84", "v85", "v86", "v87", "v88", "v89", "v90", "v91", "v92", "v93", "v94", "v95", "v96", "v97", "v98", "v99", "v100", "v101", "v102", "v103", "v104", "v105", "v106", "v107", "v108", "v109", "v110", "v111", "v112", "v113", "v114", "v115", "v116", "v117", "v118", "v119", "v120", "v121", "v122", "v123", "v124", "v125", "v126", "v127", "v128", "v129", "v130", "v131", "v132", "v133", "v134", "v135", "v136", "v137", "v138", "v139", "v140", "v141", "v142", "v143", "v144", "v145", "v146", "v147", "v148", "v149", "v150", "v151", "v152", "v153", "v154", "v155", "v156", "v157", "v158", "v159", "v160", "v161", "v162", "v163", "v164", "v165", "v166", "v167", "v168", "v169", "v170", "v171", "v172", "v173", "v174", "v175", "v176", "v177", "v178", "v179", "v180", "v181", "v182", "v183", "v184", "v185", "v186", "v187", "v188", "v189", "v190", "v191", "v192", "v193", "v194", "v195", "v196", "v197", "v198", "v199", "v200", "v201", "v202", "v203", "v204", "v205", "v206", "v207", "v208", "v209", "v210", "v211", "v212", "v213", "v214", "v215", "v216", "v217", "v218", "v219", "v220", "v221", "v222", "v223", "v224", "v225", "v226", "v227", "v228", "v229", "v230", "v231", "v232", "v233", "v234", "v235", "v236", "v237", "v238", "v239", "a0", "a1", "a2", "a3", "a4", "a5", "a6", "a7", "a8", "a9", "a10", "a11", "a12", "a13", "a14", "a15", "a16", "a17", "a18", "a19", "a20", "a21", "a22", "a23", "a24", "a25", "a26", "a27", "a28", "a29", "a30", "a31", "a32", "a33", "a34", "a35", "a36", "a37", "a38", "a39", "a40", "a41", "a42", "a43", "a44", "a45", "a46", "a47", "a48", "a49", "a50", "a51", "a52", "a53", "a54", "a55", "a56", "a57", "a58", "a59", "a60", "a61", "a62", "a63", "a64", "a65", "a66", "a67", "a68", "a69", "a70", "a71", "a72", "a73", "a74", "a75", "a76", "a77", "a78", "a79", "a80", "a81", "a82", "a83", "a84", "a85", "a86", "a87", "a88", "a89", "a90", "a91", "a92", "a93", "a94", "a95", "a96", "a97", "a98", "a99", "a100", "a101", "a102", "a103", "a104", "a105", "a106", "a107", "a108", "a109", "a110", "a111", "a112", "a113", "a114", "a115", "a116", "a117", "a118", "a119", "a120", "a121", "a122", "a123", "a124", "a125", "a126", "a127", "a128", "a129", "a130", "a131", "a132", "a133", "a134", "a135", "a136", "a137", "a138", "a139", "a140", "a141", "a142", "a143", "a144", "a145", "a146", "a147", "a148", "a149", "a150", "a151", "a152", "a153", "a154", "a155", "a156", "a157", "a158", "a159", "a160", "a161", "a162", "a163", "a164", "a165", "a166", "a167", "a168", "a169", "a170", "a171", "a172", "a173", "a174", "a175", "a176", "a177", "a178", "a179", "a180", "a181", "a182", "a183", "a184", "a185", "a186", "a187", "a188", "a189", "a190", "a191", "a192", "a193", "a194", "a195", "a196", "a197", "a198", "a199", "a200", "a201", "a202", "a203", "a204", "a205", "a206", "a207", "a208", "a209", "a210", "a211", "a212", "a213", "a214", "a215", "a216", "a217", "a218", "a219", "a220", "a221", "a222", "a223", "a224", "a225", "a226", "a227", "a228", "a229", "a230", "a231", "a232", "a233", "a234", "a235", "a236", "a237", "a238", "a239", "a240", "a241", "a242", "a243", "a244", "a245", "a246", "a247", "a248", "a249", "a250", "a251", "a252", "a253", "a254", "a255", "s36", "s37", "s38", "s39", "s40", "s41", "s42", "s43", "s44", "s45", "s46", "s47", "s48", "s49", "s50", "s51", "s52", "s53", "s54", "s55", "s56", "s57", "s58", "s59", "s60", "s61", "s62", "s63", "s64", "s65", "s66", "s67", "s68", "s69", "s70", "s71", "s72", "s73", "s74", "s75", "s76", "s77", "s78", "s79", "s80", "s81", "s82", "s83", "s84", "s85", "s86", "s87", "s88", "s89", "s90", "s91", "s92", "s93", "s94", "s95", "s96", "s97", "vcc", "scc", "memory"); }
    __builtin_unreachable();
  } else if (kind == 4) {
    const unsigned sStep = 5;
    COMMON(10, (kh * 9 + fj) * 128, 2560, 143360, 2048, 448, 0);
    const int cs = slice;
    const unsigned sOutOff = (kh ? OFF_P1 : OFF_P0) + (cs * 4 + wave) * 1024, sFoutOff = OFF_FP + (kh * 8 + cs) * 128;
    const unsigned sAuxOff = (kh ? OFF_XP : OFF_X1) + (cs * 4 + wave) * 1024, sWw = (kh * 1792 + 16 * wave) * 2048 + cs * 256, sBuf = kh * 57344;
    (void)vWr2;
    asm volatile(
R"(.macro M d,w,x
v_mfma_f32_16x16x32_bf16 v[\d:\d+3], \w, v[\x:\x+3], v[\d:\d+3]
.endm
.macro R d,a,o
ds_read_b128 v[\d:\d+3], v\a offset:\o
.endm
.macro T d,a,o
ds_read_b64_tr_b16 \d, v\a offset:\o
.endm
.macro D a,s,o
buffer_load_dwordx4 v\a, s[36:39], s\s offen offset:\o sc1 lds
.endm
.macro L c
s_waitcnt lgkmcnt(\c)
.endm
Lcode_g4_b:
s_mov_b64 s[36:37],%0
s_mov_b32 s38,111243392
s_mov_b32 s39,0x00020000
s_mov_b32 s44,%1
s_mov_b32 s45,%2
s_mov_b32 s46,%3
s_mov_b32 s47,%4
s_mov_b32 s48,%5
s_mov_b32 s49,%6
s_mov_b32 s50,%7
s_mov_b32 s51,%8
s_mov_b32 s93,%9
v_mov_b32 v0,%10
v_mov_b32 v3,%11
v_mov_b32 v5,%12
v_mov_b32 v6,%13
v_mov_b32 v10,%14
v_mov_b32 v11,%15
v_mov_b32 v12,%16
v_mov_b32 v13,%17
s_mov_b64 s[40:41],%18
s_mov_b32 s42,7340032
s_mov_b32 s43,0x00020000
s_lshl_b32 s57,s44,11
s_mov_b32 s79,111243264
buffer_load_dword v4,off,s[36:39],s79 sc1
s_add_u32 s55,s50,0
s_add_u32 m0,s57,0
s_nop 0
buffer_load_dwordx4 v10,s[40:43],s55 offen offset:0  lds
s_add_u32 m0,s57,1024
s_nop 0
buffer_load_dwordx4 v11,s[40:43],s55 offen offset:0  lds
s_add_u32 m0,s57,8064
s_nop 0
buffer_load_dwordx4 v10,s[40:43],s55 offen offset:128  lds
s_add_u32 m0,s57,9088
s_nop 0
buffer_load_dwordx4 v11,s[40:43],s55 offen offset:128  lds
s_add_u32 s55,s50,131072
s_add_u32 m0,s57,16384
s_nop 0
buffer_load_dwordx4 v10,s[40:43],s55 offen offset:0  lds
s_add_u32 m0,s57,17408
s_nop 0
buffer_load_dwordx4 v11,s[40:43],s55 offen offset:0  lds
s_add_u32 m0,s57,24448
s_nop 0
buffer_load_dwordx4 v10,s[40:43],s55 offen offset:128  lds
s_add_u32 m0,s57,25472
s_nop 0
buffer_load_dwordx4 v11,s[40:43],s55 offen offset:128  lds
s_add_u32 s55,s50,262144
s_add_u32 m0,s57,32768
s_nop 0
buffer_load_dwordx4 v10,s[40:43],s55 offen offset:0  lds
s_add_u32 m0,s57,33792
s_nop 0
buffer_load_dwordx4 v11,s[40:43],s55 offen offset:0  lds
s_add_u32 m0,s57,40832
s_nop 0
buffer_load_dwordx4 v10,s[40:43],s55 offen offset:128  lds
s_add_u32 m0,s57,41856
s_nop 0
buffer_load_dwordx4 v11,s[40:43],s55 offen offset:128  lds
s_add_u32 s55,s50,393216
s_add_u32 m0,s57,49152
s_nop 0
buffer_load_dwordx4 v10,s[40:43],s55 offen offset:0  lds
s_add_u32 m0,s57,50176
s_nop 0
buffer_load_dwordx4 v11,s[40:43],s55 offen offset:0  lds
s_add_u32 m0,s57,57216
s_nop 0
buffer_load_dwordx4 v10,s[40:43],s55 offen offset:128  lds
s_add_u32 m0,s57,58240
s_nop 0
buffer_load_dwordx4 v11,s[40:43],s55 offen offset:128  lds
s_add_u32 s55,s50,524288
s_add_u32 m0,s57,65536
s_nop 0
buffer_load_dwordx4 v10,s[40:43],s55 offen offset:0  lds
s_add_u32 m0,s57,66560
s_nop 0
buffer_load_dwordx4 v11,s[40:43],s55 offen offset:0  lds
s_add_u32 m0,s57,73600
s_nop 0
buffer_load_dwordx4 v10,s[40:43],s55 offen offset:128  lds
s_add_u32 m0,s57,74624
s_nop 0
buffer_load_dwordx4 v11,s[40:43],s55 offen offset:128  lds
s_add_u32 s55,s50,655360
s_add_u32 m0,s57,81920
s_nop 0
buffer_load_dwordx4 v10,s[40:43],s55 offen offset:0  lds
s_add_u32 m0,s57,82944
s_nop 0
buffer_load_dwordx4 v11,s[40:43],s55 offen offset:0  lds
s_add_u32 m0,s57,89984
s_nop 0
buffer_load_dwordx4 v10,s[40:43],s55 offen offset:128  lds
s_add_u32 m0,s57,91008
s_nop 0
buffer_load_dwordx4 v11,s[40:43],s55 offen offset:128  lds
s_waitcnt vmcnt(24)
s_barrier
v_readfirstlane_b32 s53,v4
s_nop 3
s_add_u32 s53,s53,1
s_cmp_eq_u32 s53,0
s_cselect_b32 s53,1,s53
v_mov_b32 v4,s53
s_waitcnt vmcnt(20)
L 0
s_barrier
s_add_u32 s55,s50,786432
s_add_u32 m0,s57,98304
s_nop 0
buffer_load_dwordx4 v10,s[40:43],s55 offen offset:0  lds
s_add_u32 m0,s57,99328
s_nop 0
buffer_load_dwordx4 v11,s[40:43],s55 offen offset:0  lds
s_add_u32 m0,s57,106368
s_nop 0
buffer_load_dwordx4 v10,s[40:43],s55 offen offset:128  lds
s_add_u32 m0,s57,107392
s_nop 0
buffer_load_dwordx4 v11,s[40:43],s55 offen offset:128  lds
v_add_u32 v18,0,v12
v_add_u32 v19,0,v13
T a[0:1],18,0
T a[2:3],18,512
T a[4:5],19,0
T a[6:7],19,512
T a[8:9],18,4096
T a[10:11],18,4608
T a[12:13],19,4096
T a[14:15],19,4608
s_waitcnt vmcnt(20)
L 0
s_barrier
s_add_u32 s55,s50,917504
s_add_u32 m0,s57,0
s_nop 0
buffer_load_dwordx4 v10,s[40:43],s55 offen offset:0  lds
s_add_u32 m0,s57,1024
s_nop 0
buffer_load_dwordx4 v11,s[40:43],s55 offen offset:0  lds
s_add_u32 m0,s57,8064
s_nop 0
buffer_load_dwordx4 v10,s[40:43],s55 offen offset:128  lds
s_add_u32 m0,s57,9088
s_nop 0
buffer_load_dwordx4 v11,s[40:43],s55 offen offset:128  lds
v_add_u32 v18,16384,v12
v_add_u32 v19,16384,v13
T a[16:17],18,0
T a[18:19],18,512
T a[20:21],19,0
T a[22:23],19,512
T a[24:25],18,4096
T a[26:27],18,4608
T a[28:29],19,4096
T a[30:31],19,4608
s_waitcnt vmcnt(20)
L 0
s_barrier
s_add_u32 s55,s50,1048576
s_add_u32 m0,s57,16384
s_nop 0
buffer_load_dwordx4 v10,s[40:43],s55 offen offset:0  lds
s_add_u32 m0,s57,17408
s_nop 0
buffer_load_dwordx4 v11,s[40:43],s55 offen offset:0  lds
s_add_u32 m0,s57,24448
s_nop 0
buffer_load_dwordx4 v10,s[40:43],s55 offen offset:128  lds
s_add_u32 m0,s57,25472
s_nop 0
buffer_load_dwordx4 v11,s[40:43],s55 offen offset:128  lds
v_add_u32 v18,32768,v12
v_add_u32 v19,32768,v13
T a[32:33],18,0
T a[34:35],18,512
T a[36:37],19,0
T a[38:39],19,512
T a[40:41],18,4096
T a[42:43],18,4608
T a[44:45],19,4096
T a[46:47],19,4608
s_waitcnt vmcnt(20)
L 0
s_barrier
s_add_u32 s55,s50,1179648
s_add_u32 m0,s57,32768
s_nop 0
buffer_load_dwordx4 v10,s[40:43],s55 offen offset:0  lds
s_add_u32 m0,s57,33792
s_nop 0
buffer_load_dwordx4 v11,s[40:43],s55 offen offset:0  lds
s_add_u32 m0,s57,40832
s_nop 0
buffer_load_dwordx4 v10,s[40:43],s55 offen offset:128  lds
s_add_u32 m0,s57,41856
s_nop 0
buffer_load_dwordx4 v11,s[40:43],s55 offen offset:128  lds
v_add_u32 v18,49152,v12
v_add_u32 v19,49152,v13
T a[48:49],18,0
T a[50:51],18,512
T a[52:53],19,0
T a[54:55],19,512
T a[56:57],18,4096
T a[58:59],18,4608
T a[60:61],19,4096
T a[62:63],19,4608
s_waitcnt vmcnt(20)
L 0
s_barrier
s_add_u32 s55,s50,1310720
s_add_u32 m0,s57,49152
s_nop 0
buffer_load_dwordx4 v10,s[40:43],s55 offen offset:0  lds
s_add_u32 m0,s57,50176
s_nop 0
buffer_load_dwordx4 v11,s[40:43],s55 offen offset:0  lds
s_add_u32 m0,s57,57216
s_nop 0
buffer_load_dwordx4 v10,s[40:43],s55 offen offset:128  lds
s_add_u32 m0,s57,58240
s_nop 0
buffer_load_dwordx4 v11,s[40:43],s55 offen offset:128  lds
v_add_u32 v18,65536,v12
v_add_u32 v19,65536,v13
T a[64:65],18,0
T a[66:67],18,512
T a[68:69],19,0
T a[70:71],19,512
T a[72:73],18,4096
T a[74:75],18,4608
T a[76:77],19,4096
T a[78:79],19,4608
s_waitcnt vmcnt(20)
L 0
s_barrier
s_add_u32 s55,s50,1441792
s_add_u32 m0,s57,65536
s_nop 0
buffer_load_dwordx4 v10,s[40:43],s55 offen offset:0  lds
s_add_u32 m0,s57,66560
s_nop 0
buffer_load_dwordx4 v11,s[40:43],s55 offen offset:0  lds
s_add_u32 m0,s57,73600
s_nop 0
buffer_load_dwordx4 v10,s[40:43],s55 offen offset:128  lds
s_add_u32 m0,s57,74624
s_nop 0
buffer_load_dwordx4 v11,s[40:43],s55 offen offset:128  lds
v_add_u32 v18,81920,v12
v_add_u32 v19,81920,v13
T a[80:81],18,0
T a[82:83],18,512
T a[84:85],19,0
T a[86:87],19,512
T a[88:89],18,4096
T a[90:91],18,4608
T a[92:93],19,4096
T a[94:95],19,4608
s_waitcnt vmcnt(20)
L 0
s_barrier
s_add_u32 s55,s50,1572864
s_add_u32 m0,s57,81920
s_nop 0
buffer_load_dwordx4 v10,s[40:43],s55 offen offset:0  lds
s_add_u32 m0,s57,82944
s_nop 0
buffer_load_dwordx4 v11,s[40:43],s55 offen offset:0  lds
s_add_u32 m0,s57,89984
s_nop 0
buffer_load_dwordx4 v10,s[40:43],s55 offen offset:128  lds
s_add_u32 m0,s57,91008
s_nop 0
buffer_load_dwordx4 v11,s[40:43],s55 offen offset:128  lds
v_add_u32 v18,98304,v12
v_add_u32 v19,98304,v13
T a[96:97],18,0
T a[98:99],18,512
T a[100:101],19,0
T a[102:103],19,512
T a[104:105],18,4096
T a[106:107],18,4608
T a[108:109],19,4096
T a[110:111],19,4608
s_waitcnt vmcnt(20)
L 0
s_barrier
s_add_u32 s55,s50,1703936
s_add_u32 m0,s57,98304
s_nop 0
buffer_load_dwordx4 v10,s[40:43],s55 offen offset:0  lds
s_add_u32 m0,s57,99328
s_nop 0
buffer_load_dwordx4 v11,s[40:43],s55 offen offset:0  lds
s_add_u32 m0,s57,106368
s_nop 0
buffer_load_dwordx4 v10,s[40:43],s55 offen offset:128  lds
s_add_u32 m0,s57,107392
s_nop 0
buffer_load_dwordx4 v11,s[40:43],s55 offen offset:128  lds
v_add_u32 v18,0,v12
v_add_u32 v19,0,v13
T a[112:113],18,0
T a[114:115],18,512
T a[116:117],19,0
T a[118:119],19,512
T a[120:121],18,4096
T a[122:123],18,4608
T a[124:125],19,4096
T a[126:127],19,4608
s_waitcnt vmcnt(20)
L 0
s_barrier
s_add_u32 s55,s50,1835008
s_add_u32 m0,s57,0
s_nop 0
buffer_load_dwordx4 v10,s[40:43],s55 offen offset:0  lds
s_add_u32 m0,s57,1024
s_nop 0
buffer_load_dwordx4 v11,s[40:43],s55 offen offset:0  lds
s_add_u32 m0,s57,8064
s_nop 0
buffer_load_dwordx4 v10,s[40:43],s55 offen offset:128  lds
s_add_u32 m0,s57,9088
s_nop 0
buffer_load_dwordx4 v11,s[40:43],s55 offen offset:128  lds
v_add_u32 v18,16384,v12
v_add_u32 v19,16384,v13
T a[128:129],18,0
T a[130:131],18,512
T a[132:133],19,0
T a[134:135],19,512
T a[136:137],18,4096
T a[138:139],18,4608
T a[140:141],19,4096
T a[142:143],19,4608
s_waitcnt vmcnt(20)
L 0
s_barrier
s_add_u32 s55,s50,1966080
s_add_u32 m0,s57,16384
s_nop 0
buffer_load_dwordx4 v10,s[40:43],s55 offen offset:0  lds
s_add_u32 m0,s57,17408
s_nop 0
buffer_load_dwordx4 v11,s[40:43],s55 offen offset:0  lds
s_add_u32 m0,s57,24448
s_nop 0
buffer_load_dwordx4 v10,s[40:43],s55 offen offset:128  lds
s_add_u32 m0,s57,25472
s_nop 0
buffer_load_dwordx4 v11,s[40:43],s55 offen offset:128  lds
v_add_u32 v18,32768,v12
v_add_u32 v19,32768,v13
T a[144:145],18,0
T a[146:147],18,512
T a[148:149],19,0
T a[150:151],19,512
T a[152:153],18,4096
T a[154:155],18,4608
T a[156:157],19,4096
T a[158:159],19,4608
s_waitcnt vmcnt(20)
L 0
s_barrier
s_add_u32 s55,s50,2097152
s_add_u32 m0,s57,32768
s_nop 0
buffer_load_dwordx4 v10,s[40:43],s55 offen offset:0  lds
s_add_u32 m0,s57,33792
s_nop 0
buffer_load_dwordx4 v11,s[40:43],s55 offen offset:0  lds
s_add_u32 m0,s57,40832
s_nop 0
buffer_load_dwordx4 v10,s[40:43],s55 offen offset:128  lds
s_add_u32 m0,s57,41856
s_nop 0
buffer_load_dwordx4 v11,s[40:43],s55 offen offset:128  lds
v_add_u32 v18,49152,v12
v_add_u32 v19,49152,v13
T a[160:161],18,0
T a[162:163],18,512
T a[164:165],19,0
T a[166:167],19,512
T a[168:169],18,4096
T a[170:171],18,4608
T a[172:173],19,4096
T a[174:175],19,4608
s_waitcnt vmcnt(20)
L 0
s_barrier
s_add_u32 s55,s50,2228224
s_add_u32 m0,s57,49152
s_nop 0
buffer_load_dwordx4 v10,s[40:43],s55 offen offset:0  lds
s_add_u32 m0,s57,50176
s_nop 0
buffer_load_dwordx4 v11,s[40:43],s55 offen offset:0  lds
s_add_u32 m0,s57,57216
s_nop 0
buffer_load_dwordx4 v10,s[40:43],s55 offen offset:128  lds
s_add_u32 m0,s57,58240
s_nop 0
buffer_load_dwordx4 v11,s[40:43],s55 offen offset:128  lds
v_add_u32 v18,65536,v12
v_add_u32 v19,65536,v13
T a[176:177],18,0
T a[178:179],18,512
T a[180:181],19,0
T a[182:183],19,512
T a[184:185],18,4096
T a[186:187],18,4608
T a[188:189],19,4096
T a[190:191],19,4608
s_waitcnt vmcnt(20)
L 0
s_barrier
s_add_u32 s55,s50,2359296
s_add_u32 m0,s57,65536
s_nop 0
buffer_load_dwordx4 v10,s[40:43],s55 offen offset:0  lds
s_add_u32 m0,s57,66560
s_nop 0
buffer_load_dwordx4 v11,s[40:43],s55 offen offset:0  lds
s_add_u32 m0,s57,73600
s_nop 0
buffer_load_dwordx4 v10,s[40:43],s55 offen offset:128  lds
s_add_u32 m0,s57,74624
s_nop 0
buffer_load_dwordx4 v11,s[40:43],s55 offen offset:128  lds
v_add_u32 v18,81920,v12
v_add_u32 v19,81920,v13
T a[192:193],18,0
T a[194:195],18,512
T a[196:197],19,0
T a[198:199],19,512
T a[200:201],18,4096
T a[202:203],18,4608
T a[204:205],19,4096
T a[206:207],19,4608
s_waitcnt vmcnt(20)
L 0
s_barrier
s_add_u32 s55,s50,2490368
s_add_u32 m0,s57,81920
s_nop 0
buffer_load_dwordx4 v10,s[40:43],s55 offen offset:0  lds
s_add_u32 m0,s57,82944
s_nop 0
buffer_load_dwordx4 v11,s[40:43],s55 offen offset:0  lds
s_add_u32 m0,s57,89984
s_nop 0
buffer_load_dwordx4 v10,s[40:43],s55 offen offset:128  lds
s_add_u32 m0,s57,91008
s_nop 0
buffer_load_dwordx4 v11,s[40:43],s55 offen offset:128  lds
v_add_u32 v18,98304,v12
v_add_u32 v19,98304,v13
T a[208:209],18,0
T a[210:211],18,512
T a[212:213],19,0
T a[214:215],19,512
T a[216:217],18,4096
T a[218:219],18,4608
T a[220:221],19,4096
T a[222:223],19,4608
s_waitcnt vmcnt(20)
L 0
s_barrier
s_add_u32 s55,s50,2621440
s_add_u32 m0,s57,98304
s_nop 0
buffer_load_dwordx4 v10,s[40:43],s55 offen offset:0  lds
s_add_u32 m0,s57,99328
)"
R"(s_nop 0
buffer_load_dwordx4 v11,s[40:43],s55 offen offset:0  lds
s_add_u32 m0,s57,106368
s_nop 0
buffer_load_dwordx4 v10,s[40:43],s55 offen offset:128  lds
s_add_u32 m0,s57,107392
s_nop 0
buffer_load_dwordx4 v11,s[40:43],s55 offen offset:128  lds
v_add_u32 v18,0,v12
v_add_u32 v19,0,v13
T a[224:225],18,0
T a[226:227],18,512
T a[228:229],19,0
T a[230:231],19,512
T a[232:233],18,4096
T a[234:235],18,4608
T a[236:237],19,4096
T a[238:239],19,4608
s_waitcnt vmcnt(20)
L 0
s_barrier
s_add_u32 s55,s50,2752512
s_add_u32 m0,s57,0
s_nop 0
buffer_load_dwordx4 v10,s[40:43],s55 offen offset:0  lds
s_add_u32 m0,s57,1024
s_nop 0
buffer_load_dwordx4 v11,s[40:43],s55 offen offset:0  lds
s_add_u32 m0,s57,8064
s_nop 0
buffer_load_dwordx4 v10,s[40:43],s55 offen offset:128  lds
s_add_u32 m0,s57,9088
s_nop 0
buffer_load_dwordx4 v11,s[40:43],s55 offen offset:128  lds
v_add_u32 v18,16384,v12
v_add_u32 v19,16384,v13
T a[240:241],18,0
T a[242:243],18,512
T a[244:245],19,0
T a[246:247],19,512
T a[248:249],18,4096
T a[250:251],18,4608
T a[252:253],19,4096
T a[254:255],19,4608
s_waitcnt vmcnt(20)
L 0
s_barrier
s_add_u32 s55,s50,2883584
s_add_u32 m0,s57,16384
s_nop 0
buffer_load_dwordx4 v10,s[40:43],s55 offen offset:0  lds
s_add_u32 m0,s57,17408
s_nop 0
buffer_load_dwordx4 v11,s[40:43],s55 offen offset:0  lds
s_add_u32 m0,s57,24448
s_nop 0
buffer_load_dwordx4 v10,s[40:43],s55 offen offset:128  lds
s_add_u32 m0,s57,25472
s_nop 0
buffer_load_dwordx4 v11,s[40:43],s55 offen offset:128  lds
v_add_u32 v18,32768,v12
v_add_u32 v19,32768,v13
T v[64:65],18,0
T v[66:67],18,512
T v[68:69],19,0
T v[70:71],19,512
T v[72:73],18,4096
T v[74:75],18,4608
T v[76:77],19,4096
T v[78:79],19,4608
s_waitcnt vmcnt(20)
L 0
s_barrier
s_add_u32 s55,s50,3014656
s_add_u32 m0,s57,32768
s_nop 0
buffer_load_dwordx4 v10,s[40:43],s55 offen offset:0  lds
s_add_u32 m0,s57,33792
s_nop 0
buffer_load_dwordx4 v11,s[40:43],s55 offen offset:0  lds
s_add_u32 m0,s57,40832
s_nop 0
buffer_load_dwordx4 v10,s[40:43],s55 offen offset:128  lds
s_add_u32 m0,s57,41856
s_nop 0
buffer_load_dwordx4 v11,s[40:43],s55 offen offset:128  lds
v_add_u32 v18,49152,v12
v_add_u32 v19,49152,v13
T v[80:81],18,0
T v[82:83],18,512
T v[84:85],19,0
T v[86:87],19,512
T v[88:89],18,4096
T v[90:91],18,4608
T v[92:93],19,4096
T v[94:95],19,4608
s_waitcnt vmcnt(20)
L 0
s_barrier
s_add_u32 s55,s50,3145728
s_add_u32 m0,s57,49152
s_nop 0
buffer_load_dwordx4 v10,s[40:43],s55 offen offset:0  lds
s_add_u32 m0,s57,50176
s_nop 0
buffer_load_dwordx4 v11,s[40:43],s55 offen offset:0  lds
s_add_u32 m0,s57,57216
s_nop 0
buffer_load_dwordx4 v10,s[40:43],s55 offen offset:128  lds
s_add_u32 m0,s57,58240
s_nop 0
buffer_load_dwordx4 v11,s[40:43],s55 offen offset:128  lds
v_add_u32 v18,65536,v12
v_add_u32 v19,65536,v13
T v[96:97],18,0
T v[98:99],18,512
T v[100:101],19,0
T v[102:103],19,512
T v[104:105],18,4096
T v[106:107],18,4608
T v[108:109],19,4096
T v[110:111],19,4608
s_waitcnt vmcnt(20)
L 0
s_barrier
s_add_u32 s55,s50,3276800
s_add_u32 m0,s57,65536
s_nop 0
buffer_load_dwordx4 v10,s[40:43],s55 offen offset:0  lds
s_add_u32 m0,s57,66560
s_nop 0
buffer_load_dwordx4 v11,s[40:43],s55 offen offset:0  lds
s_add_u32 m0,s57,73600
s_nop 0
buffer_load_dwordx4 v10,s[40:43],s55 offen offset:128  lds
s_add_u32 m0,s57,74624
s_nop 0
buffer_load_dwordx4 v11,s[40:43],s55 offen offset:128  lds
v_add_u32 v18,81920,v12
v_add_u32 v19,81920,v13
T v[112:113],18,0
T v[114:115],18,512
T v[116:117],19,0
T v[118:119],19,512
T v[120:121],18,4096
T v[122:123],18,4608
T v[124:125],19,4096
T v[126:127],19,4608
s_waitcnt vmcnt(20)
L 0
s_barrier
s_add_u32 s55,s50,3407872
s_add_u32 m0,s57,81920
s_nop 0
buffer_load_dwordx4 v10,s[40:43],s55 offen offset:0  lds
s_add_u32 m0,s57,82944
s_nop 0
buffer_load_dwordx4 v11,s[40:43],s55 offen offset:0  lds
s_add_u32 m0,s57,89984
s_nop 0
buffer_load_dwordx4 v10,s[40:43],s55 offen offset:128  lds
s_add_u32 m0,s57,91008
s_nop 0
buffer_load_dwordx4 v11,s[40:43],s55 offen offset:128  lds
v_add_u32 v18,98304,v12
v_add_u32 v19,98304,v13
T v[128:129],18,0
T v[130:131],18,512
T v[132:133],19,0
T v[134:135],19,512
T v[136:137],18,4096
T v[138:139],18,4608
T v[140:141],19,4096
T v[142:143],19,4608
s_waitcnt vmcnt(20)
L 0
s_barrier
s_add_u32 s55,s50,3538944
s_add_u32 m0,s57,98304
s_nop 0
buffer_load_dwordx4 v10,s[40:43],s55 offen offset:0  lds
s_add_u32 m0,s57,99328
s_nop 0
buffer_load_dwordx4 v11,s[40:43],s55 offen offset:0  lds
s_add_u32 m0,s57,106368
s_nop 0
buffer_load_dwordx4 v10,s[40:43],s55 offen offset:128  lds
s_add_u32 m0,s57,107392
s_nop 0
buffer_load_dwordx4 v11,s[40:43],s55 offen offset:128  lds
v_add_u32 v18,0,v12
v_add_u32 v19,0,v13
T v[144:145],18,0
T v[146:147],18,512
T v[148:149],19,0
T v[150:151],19,512
T v[152:153],18,4096
T v[154:155],18,4608
T v[156:157],19,4096
T v[158:159],19,4608
s_waitcnt vmcnt(20)
L 0
s_barrier
v_add_u32 v18,16384,v12
v_add_u32 v19,16384,v13
T v[160:161],18,0
T v[162:163],18,512
T v[164:165],19,0
T v[166:167],19,512
T v[168:169],18,4096
T v[170:171],18,4608
T v[172:173],19,4096
T v[174:175],19,4608
s_waitcnt vmcnt(16)
L 0
s_barrier
v_add_u32 v18,32768,v12
v_add_u32 v19,32768,v13
T v[176:177],18,0
T v[178:179],18,512
T v[180:181],19,0
T v[182:183],19,512
T v[184:185],18,4096
T v[186:187],18,4608
T v[188:189],19,4096
T v[190:191],19,4608
s_waitcnt vmcnt(12)
L 0
s_barrier
v_add_u32 v18,49152,v12
v_add_u32 v19,49152,v13
T v[192:193],18,0
T v[194:195],18,512
T v[196:197],19,0
T v[198:199],19,512
T v[200:201],18,4096
T v[202:203],18,4608
T v[204:205],19,4096
T v[206:207],19,4608
s_waitcnt vmcnt(8)
L 0
s_barrier
v_add_u32 v18,65536,v12
v_add_u32 v19,65536,v13
T v[208:209],18,0
T v[210:211],18,512
T v[212:213],19,0
T v[214:215],19,512
T v[216:217],18,4096
T v[218:219],18,4608
T v[220:221],19,4096
T v[222:223],19,4608
s_waitcnt vmcnt(4)
L 0
s_barrier
v_add_u32 v18,81920,v12
v_add_u32 v19,81920,v13
T v[224:225],18,0
T v[226:227],18,512
T v[228:229],19,0
T v[230:231],19,512
T v[232:233],18,4096
T v[234:235],18,4608
T v[236:237],19,4096
T v[238:239],19,4608
s_waitcnt vmcnt(0)
L 0
s_barrier
v_add_u32 v18,98304,v12
v_add_u32 v19,98304,v13
T v[240:241],18,0
T v[242:243],18,512
T v[244:245],19,0
T v[246:247],19,512
T v[248:249],18,4096
T v[250:251],18,4608
T v[252:253],19,4096
T v[254:255],19,4608
L 0
s_barrier
v_mov_b32 v4,s53
s_mov_b32 s82,0
s_mov_b32 s80,0
s_mov_b32 s54,s45
s_mul_i32 s57,s44,7168
s_mul_i32 s55,s45,114688
s_add_u32 s55,s55,s57
s_add_u32 s55,s55,40894464
s_mul_i32 s57,s44,7168
s_mul_i32 s60,s46,114688
s_mul_i32 s61,s46,2560
s_mul_i32 s62,s46,32768
s_mul_i32 s63,s46,2048
s_mul_i32 s64,s46,512
s_lshl_b32 s59,s44,8
s_add_u32 s59,s59,143360
s_mul_i32 s58,s45,2560
s_add_u32 s58,s58,109891584
s_mul_i32 s65,s45,32768
s_add_u32 s65,s65,s47
s_mul_i32 s68,s45,2048
s_add_u32 s68,s68,s48
s_mul_i32 s73,s45,512
s_add_u32 s73,s73,106954752
s_mul_i32 s74,s45,32768
s_add_u32 s74,s74,s49
s_mul_i32 s75,s45,1024
s_add_u32 s75,s75,107479040
s_mov_b32 s66,0x7fff0000
s_mov_b32 s67,0x7fff0000
s_mov_b32 s69,0x7fff0000
s_mov_b32 s76,0x7fff0000
s_mov_b32 s77,0x7fff0000
s_mov_b32 s70,0x7fff0000
s_mov_b32 s71,0x7fff0000
s_mov_b32 s72,0x7fff0000
v_mov_b32 v1,v0
v_add_u32 v2,28672,v0
s_mov_b32 s84,28672
s_mov_b32 s86,0x3a800000
s_mov_b32 s87,0x3727c5ac
s_mov_b32 s88,0xc0135761
s_mov_b32 s89,0x3d372713
s_and_b32 s85,s44,1
s_mul_i32 s85,s85,127
s_add_u32 s55,s55,s51
v_lshlrev_b32 v8,22,v0
v_and_b32 v8,0x80000000,v8
v_or_b32 v8,v8,v5
v_add_u32 v2,57344,v0
s_lshl_b32 s84,s57,1
s_add_u32 s84,s84,57344
s_getpc_b64 s[40:41]
Lg4_pc_%=:
s_lshl_b32 s78,s44,13
v_lshlrev_b32 v62,3,v0
v_add_u32 v62,s78,v62
v_min_u32 v62,Lcode_g4_e-Lg4_pc_%=-4,v62
s_mov_b32 m0,163584
s_nop 0
global_load_lds_dword v62,s[40:41]
s_add_u32 m0,s59,4096
s_nop 0
buffer_load_dword v8,s[36:39],s58 offen offset:0 sc1 lds
buffer_store_dword v4,v3,s[36:39],s71 offen offset:0 sc0 sc1
s_add_u32 m0,s59,5120
s_nop 0
buffer_load_dword v8,s[36:39],s58 offen offset:0 sc1 lds
buffer_store_dword v4,v3,s[36:39],s71 offen offset:0 sc0 sc1
s_mov_b32 s83,0
Lg4_poll_%=:
s_add_u32 s78,s83,2
s_and_b32 s78,s78,3
s_lshl_b32 s78,s78,10
s_add_u32 s78,s78,4096
s_add_u32 m0,s59,s78
s_nop 0
buffer_load_dword v8,s[36:39],s58 offen offset:0 sc1 lds
s_and_b32 s78,s83,3
s_lshl_b32 s78,s78,10
v_add_u32 v62,s78,v6
s_waitcnt vmcnt(4)
s_barrier
ds_read_b32 v7,v62 offset:4096
buffer_store_dword v4,v3,s[36:39],s70 offen offset:0 sc0 sc1
s_mov_b32 s70,s71
s_mov_b32 s71,s72
s_mov_b32 s72,0x7fff0000
s_add_u32 s83,s83,1
s_add_u32 s82,s82,1
L 0
v_cmp_eq_u32 vcc,s53,v7
s_nop 0
s_cmp_eq_u32 vcc_lo,-1
s_cbranch_scc1 Lg4_go_%=
s_cmp_lt_u32 s82,20000
s_cbranch_scc1 Lg4_poll_%=
Lg4_go_%=:
s_add_u32 m0,s57,0
s_add_u32 s56,s55,0
D 0,56,0
D 0,56,1024
D 0,56,2048
D 0,56,3072
s_add_u32 m0,s57,4096
s_add_u32 s56,s56,4096
D 0,56,0
D 0,56,1024
D 0,56,2048
s_add_u32 s58,s58,s61
s_add_u32 m0,s59,2048
s_nop 0
buffer_load_dword v8,s[36:39],s58 offen offset:0 sc1 lds
s_add_u32 m0,s57,28672
s_add_u32 s56,s55,28672
D 0,56,0
D 0,56,1024
D 0,56,2048
D 0,56,3072
s_add_u32 m0,s57,32768
s_add_u32 s56,s56,4096
D 0,56,0
D 0,56,1024
D 0,56,2048
s_add_u32 s55,s55,s60
s_sub_u32 s57,s84,s57
s_add_u32 s54,s54,s46
s_waitcnt vmcnt(7)
s_barrier
buffer_store_dword v4,v3,s[36:39],s70 offen offset:0 sc0 sc1
s_min_u32 s71,s71,s72
buffer_store_dword v4,v3,s[36:39],s71 offen offset:0 sc0 sc1
s_mov_b32 s70,0x7fff0000
s_mov_b32 s71,0x7fff0000
s_mov_b32 s72,0x7fff0000
s_cmp_eq_u32 s80,1
s_cbranch_scc1 Lg4_st1_%=
s_branch Lg4_st0_%=
Lg4_gox_%=:
s_add_u32 s58,s58,s61
s_add_u32 m0,s59,2048
s_nop 0
buffer_load_dword v8,s[36:39],s58 offen offset:0 sc1 lds
s_waitcnt vmcnt(0)
s_barrier
buffer_store_dword v4,v3,s[36:39],s70 offen offset:0 sc0 sc1
s_min_u32 s71,s71,s72
buffer_store_dword v4,v3,s[36:39],s71 offen offset:0 sc0 sc1
s_mov_b32 s70,0x7fff0000
s_mov_b32 s71,0x7fff0000
s_mov_b32 s72,0x7fff0000
s_cmp_eq_u32 s80,1
s_cbranch_scc1 Lg4_st1_%=
Lg4_st0_%=:
ds_read_b32 v7,v6 offset:2048
R 26,1,0
R 30,1,1024
R 34,1,2048
R 38,1,3072
R 42,1,4096
L 5
v_cmp_eq_u32 vcc,s53,v7
L 3
s_cmp_eq_u32 vcc_lo,-1
s_cbranch_scc1 Lg4_F0_%=
s_branch Lg4_L0_%=
Lg4_st1_%=:
ds_read_b32 v7,v6 offset:2048
R 26,2,0
R 30,2,1024
R 34,2,2048
R 38,2,3072
R 42,2,4096
L 5
v_cmp_eq_u32 vcc,s53,v7
L 3
s_cmp_eq_u32 vcc_lo,-1
s_cbranch_scc1 Lg4_F1_%=
s_branch Lg4_L1_%=
Lg4_F0_%=:
v_mfma_f32_16x16x32_bf16 v[14:17],a[4:7],v[26:29],0
s_add_u32 m0,s57,0
R 46,1,5120
v_lshlrev_b32 v62,16,v58
M 14,a[12:15],30
s_add_u32 s56,s55,0
v_add_f32 v18,v18,v62
v_and_b32 v63,0xffff0000,v58
v_mfma_f32_16x16x32_bf16 v[10:13],a[0:3],v[26:29],0
s_add_u32 s58,s58,s61
R 50,1,6144
v_add_f32 v19,v19,v63
M 10,a[8:11],30
L 3
D 0,56,0
M 10,a[16:19],34
s_mov_b32 s66,s65
R 54,1,7168
v_lshlrev_b32 v62,16,v59
M 10,a[24:27],38
s_add_u32 s65,s65,s62
v_add_f32 v20,v20,v62
v_and_b32 v63,0xffff0000,v59
M 14,a[20:23],34
s_add_u32 s54,s54,s46
R 26,1,8192
v_add_f32 v21,v21,v63
M 14,a[28:31],38
L 3
v_lshlrev_b32 v62,16,v60
v_add_f32 v22,v22,v62
M 14,a[36:39],42
R 30,1,9216
v_and_b32 v63,0xffff0000,v60
M 14,a[44:47],46
D 0,56,1024
M 10,a[32:35],42
R 34,1,10240
v_add_f32 v23,v23,v63
M 10,a[40:43],46
)"
R"(L 3
v_lshlrev_b32 v62,16,v61
v_add_f32 v24,v24,v62
M 10,a[48:51],50
R 38,1,11264
v_and_b32 v63,0xffff0000,v61
M 10,a[56:59],54
buffer_load_dwordx4 v[58:61],v0,s[36:39],s74 offen offset:0 sc1
M 14,a[52:55],50
s_add_u32 s74,s74,s62
R 42,1,12288
v_add_f32 v25,v25,v63
M 14,a[60:63],54
L 3
v_cvt_pk_bf16_f32 v18,v18,v19
v_cvt_pk_bf16_f32 v19,v20,v21
M 14,a[68:71],26
R 46,1,13312
v_cvt_pk_bf16_f32 v20,v22,v23
M 14,a[76:79],30
D 0,56,2048
M 10,a[64:67],26
R 50,1,14336
v_cvt_pk_bf16_f32 v21,v24,v25
M 10,a[72:75],30
L 3
buffer_store_dwordx4 v[18:21],v0,s[36:39],s67 offen offset:0 sc0 sc1
M 10,a[80:83],34
R 54,1,15360
M 10,a[88:91],38
M 14,a[84:87],34
R 26,1,16384
M 14,a[92:95],38
L 3
M 14,a[100:103],42
R 30,1,17408
M 14,a[108:111],46
D 0,56,3072
M 10,a[96:99],42
s_add_u32 m0,s57,4096
s_add_u32 s56,s56,4096
R 34,1,18432
M 10,a[104:107],46
L 3
M 10,a[112:115],50
R 38,1,19456
M 10,a[120:123],54
M 14,a[116:119],50
R 42,1,20480
M 14,a[124:127],54
L 3
D 0,56,0
M 14,a[132:135],26
R 46,1,21504
M 14,a[140:143],30
M 10,a[128:131],26
R 50,1,22528
M 10,a[136:139],30
L 3
M 10,a[144:147],34
R 54,1,23552
M 10,a[152:155],38
M 14,a[148:151],34
R 26,1,24576
M 14,a[156:159],38
L 3
D 0,56,1024
M 14,a[164:167],42
R 30,1,25600
M 14,a[172:175],46
M 10,a[160:163],42
R 34,1,26624
M 10,a[168:171],46
L 3
M 10,a[176:179],50
s_waitcnt vmcnt(8)
R 38,1,27648
M 10,a[184:187],54
s_barrier
M 14,a[180:183],50
R 42,1,28672
M 14,a[188:191],54
L 3
D 0,56,2048
M 14,a[196:199],26
s_add_u32 m0,s59,0
s_add_u32 s56,s55,28672
R 46,1,29696
M 14,a[204:207],30
s_add_u32 s55,s55,s60
M 10,a[192:195],26
R 50,1,30720
M 10,a[200:203],30
L 3
M 10,a[208:211],34
R 54,1,31744
M 10,a[216:219],38
M 14,a[212:215],34
R 26,1,32768
M 14,a[220:223],38
L 3
buffer_load_dword v8,s[36:39],s58 offen offset:0 sc1 lds
M 14,a[228:231],42
s_add_u32 m0,s57,28672
R 30,1,33792
M 14,a[236:239],46
D 0,56,0
M 10,a[224:227],42
R 34,1,34816
M 10,a[232:235],46
L 3
M 10,a[240:243],50
R 38,1,35840
M 10,a[248:251],54
M 14,a[244:247],50
R 42,1,36864
M 14,a[252:255],54
L 3
M 14,v[68:71],26
R 46,1,37888
M 14,v[76:79],30
D 0,56,1024
M 10,v[64:67],26
R 50,1,38912
M 10,v[72:75],30
L 3
M 10,v[80:83],34
R 54,1,39936
M 10,v[88:91],38
M 14,v[84:87],34
R 26,1,40960
M 14,v[92:95],38
L 3
M 14,v[100:103],42
R 30,1,41984
M 14,v[108:111],46
D 0,56,2048
M 10,v[96:99],42
R 34,1,43008
M 10,v[104:107],46
L 3
M 10,v[112:115],50
R 38,1,44032
M 10,v[120:123],54
M 14,v[116:119],50
R 42,1,45056
M 14,v[124:127],54
L 3
D 0,56,3072
M 14,v[132:135],26
s_add_u32 m0,s57,32768
s_add_u32 s56,s56,4096
R 46,1,46080
M 14,v[140:143],30
s_sub_u32 s57,s84,s57
M 10,v[128:131],26
R 50,1,47104
M 10,v[136:139],30
L 3
M 10,v[144:147],34
R 54,1,48128
M 10,v[152:155],38
M 14,v[148:151],34
R 26,1,49152
M 14,v[156:159],38
L 3
D 0,56,0
M 14,v[164:167],42
R 30,1,50176
M 14,v[172:175],46
M 10,v[160:163],42
R 34,1,51200
M 10,v[168:171],46
L 3
M 10,v[176:179],50
R 38,1,52224
M 10,v[184:187],54
M 14,v[180:183],50
R 42,1,53248
M 14,v[188:191],54
L 3
D 0,56,1024
M 14,v[196:199],26
R 46,1,54272
M 14,v[204:207],30
M 10,v[192:195],26
R 50,1,55296
M 10,v[200:203],30
L 3
M 10,v[208:211],34
s_waitcnt vmcnt(6)
R 54,1,56320
M 10,v[216:219],38
s_barrier
ds_read_b32 v7,v6 offset:0
M 14,v[212:215],34
R 26,2,0
M 14,v[220:223],38
L 4
buffer_store_dword v4,v3,s[36:39],s69 offen offset:0 sc0 sc1
M 14,v[228:231],42
s_mov_b32 s69,s68
s_add_u32 s68,s68,s63
R 30,2,1024
M 14,v[236:239],46
D 0,56,2048
M 10,v[224:227],42
R 34,2,2048
M 10,v[232:235],46
L 4
L 3
M 10,v[240:243],50
R 38,2,3072
v_cmp_eq_u32 vcc,s53,v7
M 10,v[248:251],54
s_cmp_eq_u32 vcc_lo,-1
M 14,v[244:247],50
R 42,2,4096
M 14,v[252:255],54
L 3
s_cbranch_scc0 Lg4_L1_%=
Lg4_F1_%=:
v_mfma_f32_16x16x32_bf16 v[22:25],a[4:7],v[26:29],0
s_add_u32 m0,s57,0
R 46,2,5120
v_lshlrev_b32 v62,16,v58
M 22,a[12:15],30
s_add_u32 s56,s55,0
v_add_f32 v10,v10,v62
v_and_b32 v63,0xffff0000,v58
v_mfma_f32_16x16x32_bf16 v[18:21],a[0:3],v[26:29],0
s_add_u32 s58,s58,s61
R 50,2,6144
v_add_f32 v11,v11,v63
M 18,a[8:11],30
L 3
D 0,56,0
M 18,a[16:19],34
s_mov_b32 s67,s65
R 54,2,7168
v_lshlrev_b32 v62,16,v59
M 18,a[24:27],38
s_add_u32 s65,s65,s62
v_add_f32 v12,v12,v62
v_and_b32 v63,0xffff0000,v59
M 22,a[20:23],34
s_add_u32 s54,s54,s46
R 26,2,8192
v_add_f32 v13,v13,v63
M 22,a[28:31],38
L 3
v_lshlrev_b32 v62,16,v60
v_add_f32 v14,v14,v62
M 22,a[36:39],42
R 30,2,9216
v_and_b32 v63,0xffff0000,v60
M 22,a[44:47],46
D 0,56,1024
M 18,a[32:35],42
R 34,2,10240
v_add_f32 v15,v15,v63
M 18,a[40:43],46
L 3
v_lshlrev_b32 v62,16,v61
v_add_f32 v16,v16,v62
M 18,a[48:51],50
R 38,2,11264
v_and_b32 v63,0xffff0000,v61
M 18,a[56:59],54
buffer_load_dwordx4 v[58:61],v0,s[36:39],s74 offen offset:0 sc1
M 22,a[52:55],50
s_add_u32 s74,s74,s62
R 42,2,12288
v_add_f32 v17,v17,v63
M 22,a[60:63],54
L 3
v_cvt_pk_bf16_f32 v10,v10,v11
v_cvt_pk_bf16_f32 v11,v12,v13
M 22,a[68:71],26
R 46,2,13312
v_cvt_pk_bf16_f32 v12,v14,v15
M 22,a[76:79],30
D 0,56,2048
M 18,a[64:67],26
R 50,2,14336
v_cvt_pk_bf16_f32 v13,v16,v17
M 18,a[72:75],30
L 3
buffer_store_dwordx4 v[10:13],v0,s[36:39],s66 offen offset:0 sc0 sc1
M 18,a[80:83],34
R 54,2,15360
M 18,a[88:91],38
M 22,a[84:87],34
R 26,2,16384
M 22,a[92:95],38
L 3
M 22,a[100:103],42
R 30,2,17408
M 22,a[108:111],46
D 0,56,3072
M 18,a[96:99],42
s_add_u32 m0,s57,4096
s_add_u32 s56,s56,4096
R 34,2,18432
M 18,a[104:107],46
L 3
M 18,a[112:115],50
R 38,2,19456
M 18,a[120:123],54
M 22,a[116:119],50
R 42,2,20480
M 22,a[124:127],54
L 3
D 0,56,0
M 22,a[132:135],26
R 46,2,21504
M 22,a[140:143],30
M 18,a[128:131],26
R 50,2,22528
M 18,a[136:139],30
L 3
M 18,a[144:147],34
R 54,2,23552
M 18,a[152:155],38
M 22,a[148:151],34
R 26,2,24576
M 22,a[156:159],38
L 3
D 0,56,1024
M 22,a[164:167],42
R 30,2,25600
M 22,a[172:175],46
M 18,a[160:163],42
R 34,2,26624
M 18,a[168:171],46
L 3
M 18,a[176:179],50
s_waitcnt vmcnt(8)
R 38,2,27648
M 18,a[184:187],54
s_barrier
M 22,a[180:183],50
R 42,2,28672
M 22,a[188:191],54
L 3
D 0,56,2048
M 22,a[196:199],26
s_add_u32 m0,s59,1024
s_add_u32 s56,s55,28672
R 46,2,29696
M 22,a[204:207],30
s_add_u32 s55,s55,s60
M 18,a[192:195],26
R 50,2,30720
M 18,a[200:203],30
L 3
M 18,a[208:211],34
R 54,2,31744
M 18,a[216:219],38
M 22,a[212:215],34
R 26,2,32768
M 22,a[220:223],38
L 3
buffer_load_dword v8,s[36:39],s58 offen offset:0 sc1 lds
M 22,a[228:231],42
s_add_u32 m0,s57,28672
R 30,2,33792
M 22,a[236:239],46
D 0,56,0
M 18,a[224:227],42
R 34,2,34816
M 18,a[232:235],46
L 3
M 18,a[240:243],50
R 38,2,35840
M 18,a[248:251],54
M 22,a[244:247],50
R 42,2,36864
M 22,a[252:255],54
L 3
M 22,v[68:71],26
R 46,2,37888
M 22,v[76:79],30
D 0,56,1024
M 18,v[64:67],26
R 50,2,38912
M 18,v[72:75],30
L 3
M 18,v[80:83],34
R 54,2,39936
M 18,v[88:91],38
M 22,v[84:87],34
R 26,2,40960
M 22,v[92:95],38
L 3
M 22,v[100:103],42
R 30,2,41984
M 22,v[108:111],46
D 0,56,2048
M 18,v[96:99],42
R 34,2,43008
M 18,v[104:107],46
L 3
M 18,v[112:115],50
R 38,2,44032
M 18,v[120:123],54
M 22,v[116:119],50
R 42,2,45056
M 22,v[124:127],54
L 3
D 0,56,3072
M 22,v[132:135],26
s_add_u32 m0,s57,32768
s_add_u32 s56,s56,4096
R 46,2,46080
M 22,v[140:143],30
s_sub_u32 s57,s84,s57
M 18,v[128:131],26
R 50,2,47104
M 18,v[136:139],30
L 3
M 18,v[144:147],34
R 54,2,48128
M 18,v[152:155],38
M 22,v[148:151],34
R 26,2,49152
M 22,v[156:159],38
L 3
D 0,56,0
M 22,v[164:167],42
R 30,2,50176
M 22,v[172:175],46
M 18,v[160:163],42
R 34,2,51200
M 18,v[168:171],46
L 3
M 18,v[176:179],50
R 38,2,52224
M 18,v[184:187],54
M 22,v[180:183],50
R 42,2,53248
M 22,v[188:191],54
L 3
D 0,56,1024
M 22,v[196:199],26
R 46,2,54272
M 22,v[204:207],30
M 18,v[192:195],26
R 50,2,55296
M 18,v[200:203],30
L 3
M 18,v[208:211],34
s_waitcnt vmcnt(6)
R 54,2,56320
M 18,v[216:219],38
s_barrier
ds_read_b32 v7,v6 offset:1024
M 22,v[212:215],34
R 26,1,0
M 22,v[220:223],38
L 4
buffer_store_dword v4,v3,s[36:39],s69 offen offset:0 sc0 sc1
M 22,v[228:231],42
s_mov_b32 s69,s68
s_add_u32 s68,s68,s63
R 30,1,1024
M 22,v[236:239],46
D 0,56,2048
M 18,v[224:227],42
R 34,1,2048
M 18,v[232:235],46
L 4
L 3
M 18,v[240:243],50
R 38,1,3072
v_cmp_eq_u32 vcc,s53,v7
M 18,v[248:251],54
s_cmp_eq_u32 vcc_lo,-1
M 22,v[244:247],50
R 42,1,4096
M 22,v[252:255],54
L 3
s_cbranch_scc1 Lg4_F0_%=
Lg4_L0_%=:
v_mfma_f32_16x16x32_bf16 v[14:17],a[4:7],v[26:29],0
s_add_u32 m0,s59,3072
R 46,1,5120
v_lshlrev_b32 v62,16,v58
M 14,a[12:15],30
s_mov_b32 s66,s65
buffer_load_dword v8,s[36:39],s58 offen offset:0 sc1 lds
v_mfma_f32_16x16x32_bf16 v[10:13],a[0:3],v[26:29],0
s_add_u32 s65,s65,s62
R 50,1,6144
v_add_f32 v18,v18,v62
M 10,a[8:11],30
L 3
v_and_b32 v63,0xffff0000,v58
v_add_f32 v19,v19,v63
M 10,a[16:19],34
)"
R"(R 54,1,7168
v_lshlrev_b32 v62,16,v59
M 10,a[24:27],38
v_add_f32 v20,v20,v62
v_and_b32 v63,0xffff0000,v59
M 14,a[20:23],34
R 26,1,8192
v_add_f32 v21,v21,v63
M 14,a[28:31],38
L 3
v_lshlrev_b32 v62,16,v60
v_add_f32 v22,v22,v62
M 14,a[36:39],42
R 30,1,9216
v_and_b32 v63,0xffff0000,v60
M 14,a[44:47],46
v_add_f32 v23,v23,v63
v_lshlrev_b32 v62,16,v61
M 10,a[32:35],42
R 34,1,10240
v_add_f32 v24,v24,v62
M 10,a[40:43],46
L 3
v_and_b32 v63,0xffff0000,v61
v_add_f32 v25,v25,v63
M 10,a[48:51],50
R 38,1,11264
v_cvt_pk_bf16_f32 v18,v18,v19
M 10,a[56:59],54
buffer_load_dwordx4 v[58:61],v0,s[36:39],s74 offen offset:0 sc1
M 14,a[52:55],50
s_add_u32 s74,s74,s62
R 42,1,12288
v_cvt_pk_bf16_f32 v19,v20,v21
M 14,a[60:63],54
L 3
v_cvt_pk_bf16_f32 v20,v22,v23
v_cvt_pk_bf16_f32 v21,v24,v25
M 14,a[68:71],26
R 46,1,13312
M 14,a[76:79],30
buffer_store_dwordx4 v[18:21],v0,s[36:39],s67 offen offset:0 sc0 sc1
M 10,a[64:67],26
R 50,1,14336
M 10,a[72:75],30
L 3
M 10,a[80:83],34
R 54,1,15360
M 10,a[88:91],38
M 14,a[84:87],34
R 26,1,16384
M 14,a[92:95],38
L 3
M 14,a[100:103],42
R 30,1,17408
M 14,a[108:111],46
M 10,a[96:99],42
R 34,1,18432
M 10,a[104:107],46
L 3
M 10,a[112:115],50
R 38,1,19456
M 10,a[120:123],54
M 14,a[116:119],50
R 42,1,20480
M 14,a[124:127],54
L 3
M 14,a[132:135],26
R 46,1,21504
M 14,a[140:143],30
M 10,a[128:131],26
R 50,1,22528
M 10,a[136:139],30
L 3
M 10,a[144:147],34
R 54,1,23552
M 10,a[152:155],38
M 14,a[148:151],34
R 26,1,24576
M 14,a[156:159],38
L 3
M 14,a[164:167],42
R 30,1,25600
M 14,a[172:175],46
M 10,a[160:163],42
R 34,1,26624
M 10,a[168:171],46
L 3
M 10,a[176:179],50
s_waitcnt vmcnt(2)
R 38,1,27648
M 10,a[184:187],54
s_barrier
ds_read_b32 v7,v6 offset:3072
M 14,a[180:183],50
R 42,1,28672
M 14,a[188:191],54
L 4
M 14,a[196:199],26
R 46,1,29696
M 14,a[204:207],30
M 10,a[192:195],26
R 50,1,30720
M 10,a[200:203],30
L 4
L 3
v_cmp_eq_u32 vcc,s53,v7
M 10,a[208:211],34
R 54,1,31744
M 10,a[216:219],38
s_cmp_eq_u32 vcc_lo,-1
s_cbranch_scc0 Lg4_X0_%=
M 14,a[212:215],34
s_add_u32 m0,s57,0
s_add_u32 s56,s55,0
s_add_u32 s58,s58,s61
R 26,1,32768
M 14,a[220:223],38
L 3
s_add_u32 s54,s54,s46
M 14,a[228:231],42
R 30,1,33792
M 14,a[236:239],46
D 0,56,0
M 10,a[224:227],42
R 34,1,34816
M 10,a[232:235],46
L 3
M 10,a[240:243],50
R 38,1,35840
M 10,a[248:251],54
D 0,56,1024
M 14,a[244:247],50
R 42,1,36864
M 14,a[252:255],54
L 3
M 14,v[68:71],26
R 46,1,37888
M 14,v[76:79],30
D 0,56,2048
M 10,v[64:67],26
R 50,1,38912
M 10,v[72:75],30
L 3
M 10,v[80:83],34
R 54,1,39936
M 10,v[88:91],38
D 0,56,3072
M 14,v[84:87],34
s_add_u32 m0,s57,4096
s_add_u32 s56,s56,4096
R 26,1,40960
M 14,v[92:95],38
L 3
M 14,v[100:103],42
R 30,1,41984
M 14,v[108:111],46
D 0,56,0
M 10,v[96:99],42
R 34,1,43008
M 10,v[104:107],46
L 3
M 10,v[112:115],50
R 38,1,44032
M 10,v[120:123],54
D 0,56,1024
M 14,v[116:119],50
R 42,1,45056
M 14,v[124:127],54
L 3
M 14,v[132:135],26
R 46,1,46080
M 14,v[140:143],30
D 0,56,2048
M 10,v[128:131],26
s_add_u32 m0,s59,0
s_add_u32 s56,s55,28672
R 50,1,47104
M 10,v[136:139],30
L 3
buffer_load_dword v8,s[36:39],s58 offen offset:0 sc1 lds
M 10,v[144:147],34
s_add_u32 m0,s57,28672
s_add_u32 s55,s55,s60
R 54,1,48128
M 10,v[152:155],38
M 14,v[148:151],34
R 26,1,49152
M 14,v[156:159],38
L 3
D 0,56,0
M 14,v[164:167],42
R 30,1,50176
M 14,v[172:175],46
D 0,56,1024
M 10,v[160:163],42
R 34,1,51200
M 10,v[168:171],46
L 3
M 10,v[176:179],50
R 38,1,52224
M 10,v[184:187],54
D 0,56,2048
M 14,v[180:183],50
R 42,1,53248
M 14,v[188:191],54
L 3
M 14,v[196:199],26
R 46,1,54272
M 14,v[204:207],30
D 0,56,3072
M 10,v[192:195],26
s_add_u32 m0,s57,32768
s_add_u32 s56,s56,4096
R 50,1,55296
M 10,v[200:203],30
L 3
s_sub_u32 s57,s84,s57
M 10,v[208:211],34
s_waitcnt vmcnt(4)
R 54,1,56320
M 10,v[216:219],38
s_barrier
ds_read_b32 v7,v6 offset:0
M 14,v[212:215],34
R 26,2,0
M 14,v[220:223],38
L 4
buffer_store_dword v4,v3,s[36:39],s69 offen offset:0 sc0 sc1
M 14,v[228:231],42
s_mov_b32 s69,s68
s_add_u32 s68,s68,s63
R 30,2,1024
M 14,v[236:239],46
D 0,56,0
M 10,v[224:227],42
R 34,2,2048
M 10,v[232:235],46
L 4
D 0,56,1024
M 10,v[240:243],50
L 3
R 38,2,3072
v_cmp_eq_u32 vcc,s53,v7
M 10,v[248:251],54
s_cmp_eq_u32 vcc_lo,-1
D 0,56,2048
M 14,v[244:247],50
R 42,2,4096
M 14,v[252:255],54
L 3
s_cbranch_scc1 Lg4_F1_%=
s_branch Lg4_L1_%=
Lg4_X0_%=:
M 14,a[212:215],34
s_add_u32 m0,s59,0
R 26,1,32768
M 14,a[220:223],38
L 3
buffer_load_dword v8,s[36:39],s58 offen offset:0 sc1 lds
M 14,a[228:231],42
R 30,1,33792
M 14,a[236:239],46
M 10,a[224:227],42
R 34,1,34816
M 10,a[232:235],46
L 3
M 10,a[240:243],50
R 38,1,35840
M 10,a[248:251],54
M 14,a[244:247],50
R 42,1,36864
M 14,a[252:255],54
L 3
M 14,v[68:71],26
R 46,1,37888
M 14,v[76:79],30
M 10,v[64:67],26
R 50,1,38912
M 10,v[72:75],30
L 3
M 10,v[80:83],34
R 54,1,39936
M 10,v[88:91],38
M 14,v[84:87],34
R 26,1,40960
M 14,v[92:95],38
L 3
M 14,v[100:103],42
R 30,1,41984
M 14,v[108:111],46
M 10,v[96:99],42
R 34,1,43008
M 10,v[104:107],46
L 3
M 10,v[112:115],50
R 38,1,44032
M 10,v[120:123],54
M 14,v[116:119],50
R 42,1,45056
M 14,v[124:127],54
L 3
M 14,v[132:135],26
R 46,1,46080
M 14,v[140:143],30
M 10,v[128:131],26
R 50,1,47104
M 10,v[136:139],30
L 3
M 10,v[144:147],34
R 54,1,48128
M 10,v[152:155],38
M 14,v[148:151],34
R 26,1,49152
M 14,v[156:159],38
L 3
M 14,v[164:167],42
R 30,1,50176
M 14,v[172:175],46
M 10,v[160:163],42
R 34,1,51200
M 10,v[168:171],46
L 3
M 10,v[176:179],50
R 38,1,52224
M 10,v[184:187],54
M 14,v[180:183],50
R 42,1,53248
M 14,v[188:191],54
L 3
M 14,v[196:199],26
R 46,1,54272
M 14,v[204:207],30
M 10,v[192:195],26
R 50,1,55296
M 10,v[200:203],30
L 3
M 10,v[208:211],34
s_waitcnt vmcnt(0)
R 54,1,56320
M 10,v[216:219],38
s_barrier
buffer_store_dword v4,v3,s[36:39],s69 offen offset:0 sc0 sc1
M 14,v[212:215],34
s_mov_b32 s69,s68
s_add_u32 s68,s68,s63
ds_read_b32 v7,v6 offset:0
M 14,v[220:223],38
L 3
M 14,v[228:231],42
M 14,v[236:239],46
M 10,v[224:227],42
M 10,v[232:235],46
L 1
M 10,v[240:243],50
L 0
v_cmp_eq_u32 vcc,s53,v7
M 10,v[248:251],54
M 14,v[244:247],50
M 14,v[252:255],54
s_mov_b32 s80,1
s_cmp_eq_u32 vcc_lo,-1
s_cbranch_scc0 Lg4_xn0_%=
s_add_u32 m0,s57,0
s_add_u32 s56,s55,0
D 0,56,0
v_lshlrev_b32 v62,16,v58
D 0,56,1024
v_add_f32 v10,v10,v62
D 0,56,2048
v_and_b32 v63,0xffff0000,v58
D 0,56,3072
v_add_f32 v11,v11,v63
s_add_u32 m0,s57,4096
s_add_u32 s56,s56,4096
D 0,56,0
v_lshlrev_b32 v62,16,v59
D 0,56,1024
v_add_f32 v12,v12,v62
D 0,56,2048
v_and_b32 v63,0xffff0000,v59
s_add_u32 m0,s57,28672
s_add_u32 s56,s55,28672
D 0,56,0
v_add_f32 v13,v13,v63
D 0,56,1024
v_lshlrev_b32 v62,16,v60
D 0,56,2048
v_add_f32 v14,v14,v62
D 0,56,3072
v_and_b32 v63,0xffff0000,v60
s_add_u32 m0,s57,32768
s_add_u32 s56,s56,4096
D 0,56,0
v_add_f32 v15,v15,v63
D 0,56,1024
v_lshlrev_b32 v62,16,v61
D 0,56,2048
v_add_f32 v16,v16,v62
s_add_u32 s55,s55,s60
s_sub_u32 s57,s84,s57
s_add_u32 s54,s54,s46
v_and_b32 v63,0xffff0000,v61
v_add_f32 v17,v17,v63
v_cvt_pk_bf16_f32 v10,v10,v11
v_cvt_pk_bf16_f32 v11,v12,v13
v_cvt_pk_bf16_f32 v12,v14,v15
v_cvt_pk_bf16_f32 v13,v16,v17
buffer_store_dwordx4 v[10:13],v0,s[36:39],s66 offen offset:0 sc0 sc1
s_mov_b32 s72,s69
s_mov_b32 s69,0x7fff0000
s_mov_b32 s66,0x7fff0000
s_mov_b32 s76,0x7fff0000
s_branch Lg4_gox_%=
Lg4_xn0_%=:
s_cmp_ge_u32 s54,256
s_cbranch_scc1 Lg4_fin0_%=
s_add_u32 m0,s59,4096
s_nop 0
buffer_load_dword v8,s[36:39],s58 offen offset:0 sc1 lds
buffer_store_dword v4,v3,s[36:39],s71 offen offset:0 sc0 sc1
s_add_u32 m0,s59,5120
s_nop 0
buffer_load_dword v8,s[36:39],s58 offen offset:0 sc1 lds
buffer_store_dword v4,v3,s[36:39],s71 offen offset:0 sc0 sc1
s_mov_b32 s83,0
v_lshlrev_b32 v62,16,v58
v_add_f32 v10,v10,v62
v_and_b32 v63,0xffff0000,v58
v_add_f32 v11,v11,v63
v_lshlrev_b32 v62,16,v59
v_add_f32 v12,v12,v62
v_and_b32 v63,0xffff0000,v59
v_add_f32 v13,v13,v63
v_lshlrev_b32 v62,16,v60
v_add_f32 v14,v14,v62
v_and_b32 v63,0xffff0000,v60
v_add_f32 v15,v15,v63
v_lshlrev_b32 v62,16,v61
v_add_f32 v16,v16,v62
v_and_b32 v63,0xffff0000,v61
v_add_f32 v17,v17,v63
v_cvt_pk_bf16_f32 v10,v10,v11
v_cvt_pk_bf16_f32 v11,v12,v13
v_cvt_pk_bf16_f32 v12,v14,v15
v_cvt_pk_bf16_f32 v13,v16,v17
buffer_store_dwordx4 v[10:13],v0,s[36:39],s66 offen offset:0 sc0 sc1
s_mov_b32 s72,s69
s_mov_b32 s69,0x7fff0000
s_mov_b32 s66,0x7fff0000
s_mov_b32 s76,0x7fff0000
s_branch Lg4_poll_%=
Lg4_fin0_%=:
v_lshlrev_b32 v62,16,v58
v_add_f32 v10,v10,v62
v_and_b32 v63,0xffff0000,v58
v_add_f32 v11,v11,v63
v_lshlrev_b32 v62,16,v59
v_add_f32 v12,v12,v62
v_and_b32 v63,0xffff0000,v59
v_add_f32 v13,v13,v63
v_lshlrev_b32 v62,16,v60
v_add_f32 v14,v14,v62
v_and_b32 v63,0xffff0000,v60
v_add_f32 v15,v15,v63
v_lshlrev_b32 v62,16,v61
v_add_f32 v16,v16,v62
v_and_b32 v63,0xffff0000,v61
v_add_f32 v17,v17,v63
v_cvt_pk_bf16_f32 v10,v10,v11
v_cvt_pk_bf16_f32 v11,v12,v13
v_cvt_pk_bf16_f32 v12,v14,v15
v_cvt_pk_bf16_f32 v13,v16,v17
buffer_store_dwordx4 v[10:13],v0,s[36:39],s66 offen offset:0 sc0 sc1
s_mov_b32 s72,s69
s_mov_b32 s69,0x7fff0000
s_mov_b32 s66,0x7fff0000
s_mov_b32 s76,0x7fff0000
s_branch Lg4_done_%=
Lg4_L1_%=:
v_mfma_f32_16x16x32_bf16 v[22:25],a[4:7],v[26:29],0
s_add_u32 m0,s59,3072
R 46,2,5120
v_lshlrev_b32 v62,16,v58
M 22,a[12:15],30
s_mov_b32 s67,s65
buffer_load_dword v8,s[36:39],s58 offen offset:0 sc1 lds
v_mfma_f32_16x16x32_bf16 v[18:21],a[0:3],v[26:29],0
s_add_u32 s65,s65,s62
R 50,2,6144
v_add_f32 v10,v10,v62
M 18,a[8:11],30
L 3
v_and_b32 v63,0xffff0000,v58
v_add_f32 v11,v11,v63
M 18,a[16:19],34
R 54,2,7168
v_lshlrev_b32 v62,16,v59
M 18,a[24:27],38
v_add_f32 v12,v12,v62
)"
R"(v_and_b32 v63,0xffff0000,v59
M 22,a[20:23],34
R 26,2,8192
v_add_f32 v13,v13,v63
M 22,a[28:31],38
L 3
v_lshlrev_b32 v62,16,v60
v_add_f32 v14,v14,v62
M 22,a[36:39],42
R 30,2,9216
v_and_b32 v63,0xffff0000,v60
M 22,a[44:47],46
v_add_f32 v15,v15,v63
v_lshlrev_b32 v62,16,v61
M 18,a[32:35],42
R 34,2,10240
v_add_f32 v16,v16,v62
M 18,a[40:43],46
L 3
v_and_b32 v63,0xffff0000,v61
v_add_f32 v17,v17,v63
M 18,a[48:51],50
R 38,2,11264
v_cvt_pk_bf16_f32 v10,v10,v11
M 18,a[56:59],54
buffer_load_dwordx4 v[58:61],v0,s[36:39],s74 offen offset:0 sc1
M 22,a[52:55],50
s_add_u32 s74,s74,s62
R 42,2,12288
v_cvt_pk_bf16_f32 v11,v12,v13
M 22,a[60:63],54
L 3
v_cvt_pk_bf16_f32 v12,v14,v15
v_cvt_pk_bf16_f32 v13,v16,v17
M 22,a[68:71],26
R 46,2,13312
M 22,a[76:79],30
buffer_store_dwordx4 v[10:13],v0,s[36:39],s66 offen offset:0 sc0 sc1
M 18,a[64:67],26
R 50,2,14336
M 18,a[72:75],30
L 3
M 18,a[80:83],34
R 54,2,15360
M 18,a[88:91],38
M 22,a[84:87],34
R 26,2,16384
M 22,a[92:95],38
L 3
M 22,a[100:103],42
R 30,2,17408
M 22,a[108:111],46
M 18,a[96:99],42
R 34,2,18432
M 18,a[104:107],46
L 3
M 18,a[112:115],50
R 38,2,19456
M 18,a[120:123],54
M 22,a[116:119],50
R 42,2,20480
M 22,a[124:127],54
L 3
M 22,a[132:135],26
R 46,2,21504
M 22,a[140:143],30
M 18,a[128:131],26
R 50,2,22528
M 18,a[136:139],30
L 3
M 18,a[144:147],34
R 54,2,23552
M 18,a[152:155],38
M 22,a[148:151],34
R 26,2,24576
M 22,a[156:159],38
L 3
M 22,a[164:167],42
R 30,2,25600
M 22,a[172:175],46
M 18,a[160:163],42
R 34,2,26624
M 18,a[168:171],46
L 3
M 18,a[176:179],50
s_waitcnt vmcnt(2)
R 38,2,27648
M 18,a[184:187],54
s_barrier
ds_read_b32 v7,v6 offset:3072
M 22,a[180:183],50
R 42,2,28672
M 22,a[188:191],54
L 4
M 22,a[196:199],26
R 46,2,29696
M 22,a[204:207],30
M 18,a[192:195],26
R 50,2,30720
M 18,a[200:203],30
L 4
L 3
v_cmp_eq_u32 vcc,s53,v7
M 18,a[208:211],34
R 54,2,31744
M 18,a[216:219],38
s_cmp_eq_u32 vcc_lo,-1
s_cbranch_scc0 Lg4_X1_%=
M 22,a[212:215],34
s_add_u32 m0,s57,0
s_add_u32 s56,s55,0
s_add_u32 s58,s58,s61
R 26,2,32768
M 22,a[220:223],38
L 3
s_add_u32 s54,s54,s46
M 22,a[228:231],42
R 30,2,33792
M 22,a[236:239],46
D 0,56,0
M 18,a[224:227],42
R 34,2,34816
M 18,a[232:235],46
L 3
M 18,a[240:243],50
R 38,2,35840
M 18,a[248:251],54
D 0,56,1024
M 22,a[244:247],50
R 42,2,36864
M 22,a[252:255],54
L 3
M 22,v[68:71],26
R 46,2,37888
M 22,v[76:79],30
D 0,56,2048
M 18,v[64:67],26
R 50,2,38912
M 18,v[72:75],30
L 3
M 18,v[80:83],34
R 54,2,39936
M 18,v[88:91],38
D 0,56,3072
M 22,v[84:87],34
s_add_u32 m0,s57,4096
s_add_u32 s56,s56,4096
R 26,2,40960
M 22,v[92:95],38
L 3
M 22,v[100:103],42
R 30,2,41984
M 22,v[108:111],46
D 0,56,0
M 18,v[96:99],42
R 34,2,43008
M 18,v[104:107],46
L 3
M 18,v[112:115],50
R 38,2,44032
M 18,v[120:123],54
D 0,56,1024
M 22,v[116:119],50
R 42,2,45056
M 22,v[124:127],54
L 3
M 22,v[132:135],26
R 46,2,46080
M 22,v[140:143],30
D 0,56,2048
M 18,v[128:131],26
s_add_u32 m0,s59,1024
s_add_u32 s56,s55,28672
R 50,2,47104
M 18,v[136:139],30
L 3
buffer_load_dword v8,s[36:39],s58 offen offset:0 sc1 lds
M 18,v[144:147],34
s_add_u32 m0,s57,28672
s_add_u32 s55,s55,s60
R 54,2,48128
M 18,v[152:155],38
M 22,v[148:151],34
R 26,2,49152
M 22,v[156:159],38
L 3
D 0,56,0
M 22,v[164:167],42
R 30,2,50176
M 22,v[172:175],46
D 0,56,1024
M 18,v[160:163],42
R 34,2,51200
M 18,v[168:171],46
L 3
M 18,v[176:179],50
R 38,2,52224
M 18,v[184:187],54
D 0,56,2048
M 22,v[180:183],50
R 42,2,53248
M 22,v[188:191],54
L 3
M 22,v[196:199],26
R 46,2,54272
M 22,v[204:207],30
D 0,56,3072
M 18,v[192:195],26
s_add_u32 m0,s57,32768
s_add_u32 s56,s56,4096
R 50,2,55296
M 18,v[200:203],30
L 3
s_sub_u32 s57,s84,s57
M 18,v[208:211],34
s_waitcnt vmcnt(4)
R 54,2,56320
M 18,v[216:219],38
s_barrier
ds_read_b32 v7,v6 offset:1024
M 22,v[212:215],34
R 26,1,0
M 22,v[220:223],38
L 4
buffer_store_dword v4,v3,s[36:39],s69 offen offset:0 sc0 sc1
M 22,v[228:231],42
s_mov_b32 s69,s68
s_add_u32 s68,s68,s63
R 30,1,1024
M 22,v[236:239],46
D 0,56,0
M 18,v[224:227],42
R 34,1,2048
M 18,v[232:235],46
L 4
D 0,56,1024
M 18,v[240:243],50
L 3
R 38,1,3072
v_cmp_eq_u32 vcc,s53,v7
M 18,v[248:251],54
s_cmp_eq_u32 vcc_lo,-1
D 0,56,2048
M 22,v[244:247],50
R 42,1,4096
M 22,v[252:255],54
L 3
s_cbranch_scc1 Lg4_F0_%=
s_branch Lg4_L0_%=
Lg4_X1_%=:
M 22,a[212:215],34
s_add_u32 m0,s59,1024
R 26,2,32768
M 22,a[220:223],38
L 3
buffer_load_dword v8,s[36:39],s58 offen offset:0 sc1 lds
M 22,a[228:231],42
R 30,2,33792
M 22,a[236:239],46
M 18,a[224:227],42
R 34,2,34816
M 18,a[232:235],46
L 3
M 18,a[240:243],50
R 38,2,35840
M 18,a[248:251],54
M 22,a[244:247],50
R 42,2,36864
M 22,a[252:255],54
L 3
M 22,v[68:71],26
R 46,2,37888
M 22,v[76:79],30
M 18,v[64:67],26
R 50,2,38912
M 18,v[72:75],30
L 3
M 18,v[80:83],34
R 54,2,39936
M 18,v[88:91],38
M 22,v[84:87],34
R 26,2,40960
M 22,v[92:95],38
L 3
M 22,v[100:103],42
R 30,2,41984
M 22,v[108:111],46
M 18,v[96:99],42
R 34,2,43008
M 18,v[104:107],46
L 3
M 18,v[112:115],50
R 38,2,44032
M 18,v[120:123],54
M 22,v[116:119],50
R 42,2,45056
M 22,v[124:127],54
L 3
M 22,v[132:135],26
R 46,2,46080
M 22,v[140:143],30
M 18,v[128:131],26
R 50,2,47104
M 18,v[136:139],30
L 3
M 18,v[144:147],34
R 54,2,48128
M 18,v[152:155],38
M 22,v[148:151],34
R 26,2,49152
M 22,v[156:159],38
L 3
M 22,v[164:167],42
R 30,2,50176
M 22,v[172:175],46
M 18,v[160:163],42
R 34,2,51200
M 18,v[168:171],46
L 3
M 18,v[176:179],50
R 38,2,52224
M 18,v[184:187],54
M 22,v[180:183],50
R 42,2,53248
M 22,v[188:191],54
L 3
M 22,v[196:199],26
R 46,2,54272
M 22,v[204:207],30
M 18,v[192:195],26
R 50,2,55296
M 18,v[200:203],30
L 3
M 18,v[208:211],34
s_waitcnt vmcnt(0)
R 54,2,56320
M 18,v[216:219],38
s_barrier
buffer_store_dword v4,v3,s[36:39],s69 offen offset:0 sc0 sc1
M 22,v[212:215],34
s_mov_b32 s69,s68
s_add_u32 s68,s68,s63
ds_read_b32 v7,v6 offset:1024
M 22,v[220:223],38
L 3
M 22,v[228:231],42
M 22,v[236:239],46
M 18,v[224:227],42
M 18,v[232:235],46
L 1
M 18,v[240:243],50
L 0
v_cmp_eq_u32 vcc,s53,v7
M 18,v[248:251],54
M 22,v[244:247],50
M 22,v[252:255],54
s_mov_b32 s80,0
s_cmp_eq_u32 vcc_lo,-1
s_cbranch_scc0 Lg4_xn1_%=
s_add_u32 m0,s57,0
s_add_u32 s56,s55,0
D 0,56,0
v_lshlrev_b32 v62,16,v58
D 0,56,1024
v_add_f32 v18,v18,v62
D 0,56,2048
v_and_b32 v63,0xffff0000,v58
D 0,56,3072
v_add_f32 v19,v19,v63
s_add_u32 m0,s57,4096
s_add_u32 s56,s56,4096
D 0,56,0
v_lshlrev_b32 v62,16,v59
D 0,56,1024
v_add_f32 v20,v20,v62
D 0,56,2048
v_and_b32 v63,0xffff0000,v59
s_add_u32 m0,s57,28672
s_add_u32 s56,s55,28672
D 0,56,0
v_add_f32 v21,v21,v63
D 0,56,1024
v_lshlrev_b32 v62,16,v60
D 0,56,2048
v_add_f32 v22,v22,v62
D 0,56,3072
v_and_b32 v63,0xffff0000,v60
s_add_u32 m0,s57,32768
s_add_u32 s56,s56,4096
D 0,56,0
v_add_f32 v23,v23,v63
D 0,56,1024
v_lshlrev_b32 v62,16,v61
D 0,56,2048
v_add_f32 v24,v24,v62
s_add_u32 s55,s55,s60
s_sub_u32 s57,s84,s57
s_add_u32 s54,s54,s46
v_and_b32 v63,0xffff0000,v61
v_add_f32 v25,v25,v63
v_cvt_pk_bf16_f32 v18,v18,v19
v_cvt_pk_bf16_f32 v19,v20,v21
v_cvt_pk_bf16_f32 v20,v22,v23
v_cvt_pk_bf16_f32 v21,v24,v25
buffer_store_dwordx4 v[18:21],v0,s[36:39],s67 offen offset:0 sc0 sc1
s_mov_b32 s72,s69
s_mov_b32 s69,0x7fff0000
s_mov_b32 s67,0x7fff0000
s_mov_b32 s77,0x7fff0000
s_branch Lg4_gox_%=
Lg4_xn1_%=:
s_cmp_ge_u32 s54,256
s_cbranch_scc1 Lg4_fin1_%=
s_add_u32 m0,s59,4096
s_nop 0
buffer_load_dword v8,s[36:39],s58 offen offset:0 sc1 lds
buffer_store_dword v4,v3,s[36:39],s71 offen offset:0 sc0 sc1
s_add_u32 m0,s59,5120
s_nop 0
buffer_load_dword v8,s[36:39],s58 offen offset:0 sc1 lds
buffer_store_dword v4,v3,s[36:39],s71 offen offset:0 sc0 sc1
s_mov_b32 s83,0
v_lshlrev_b32 v62,16,v58
v_add_f32 v18,v18,v62
v_and_b32 v63,0xffff0000,v58
v_add_f32 v19,v19,v63
v_lshlrev_b32 v62,16,v59
v_add_f32 v20,v20,v62
v_and_b32 v63,0xffff0000,v59
v_add_f32 v21,v21,v63
v_lshlrev_b32 v62,16,v60
v_add_f32 v22,v22,v62
v_and_b32 v63,0xffff0000,v60
v_add_f32 v23,v23,v63
v_lshlrev_b32 v62,16,v61
v_add_f32 v24,v24,v62
v_and_b32 v63,0xffff0000,v61
v_add_f32 v25,v25,v63
v_cvt_pk_bf16_f32 v18,v18,v19
v_cvt_pk_bf16_f32 v19,v20,v21
v_cvt_pk_bf16_f32 v20,v22,v23
v_cvt_pk_bf16_f32 v21,v24,v25
buffer_store_dwordx4 v[18:21],v0,s[36:39],s67 offen offset:0 sc0 sc1
s_mov_b32 s72,s69
s_mov_b32 s69,0x7fff0000
s_mov_b32 s67,0x7fff0000
s_mov_b32 s77,0x7fff0000
s_branch Lg4_poll_%=
Lg4_fin1_%=:
v_lshlrev_b32 v62,16,v58
v_add_f32 v18,v18,v62
v_and_b32 v63,0xffff0000,v58
v_add_f32 v19,v19,v63
v_lshlrev_b32 v62,16,v59
v_add_f32 v20,v20,v62
v_and_b32 v63,0xffff0000,v59
v_add_f32 v21,v21,v63
v_lshlrev_b32 v62,16,v60
v_add_f32 v22,v22,v62
v_and_b32 v63,0xffff0000,v60
v_add_f32 v23,v23,v63
v_lshlrev_b32 v62,16,v61
v_add_f32 v24,v24,v62
v_and_b32 v63,0xffff0000,v61
v_add_f32 v25,v25,v63
v_cvt_pk_bf16_f32 v18,v18,v19
v_cvt_pk_bf16_f32 v19,v20,v21
v_cvt_pk_bf16_f32 v20,v22,v23
v_cvt_pk_bf16_f32 v21,v24,v25
buffer_store_dwordx4 v[18:21],v0,s[36:39],s67 offen offset:0 sc0 sc1
s_mov_b32 s72,s69
s_mov_b32 s69,0x7fff0000
s_mov_b32 s67,0x7fff0000
s_mov_b32 s77,0x7fff0000
s_branch Lg4_done_%=
Lg4_done_%=:
s_nop 0
s_barrier
buffer_store_dword v4,v3,s[36:39],s70 offen offset:0 sc0 sc1
s_waitcnt vmcnt(1)
s_barrier
buffer_store_dword v4,v3,s[36:39],s72 offen offset:0 sc0 sc1
s_waitcnt vmcnt(0) lgkmcnt(0)
s_barrier
s_endpgm
Lcode_g4_e:
.purgem M
.purgem R
.purgem T
.purgem D
.purgem L
)"
  :: "s"(ws), "s"(sWave), "s"(sG0), "s"(sStep), "s"(sOutOff), "s"(sFoutOff), "s"(sAuxOff), "s"(sWw), "s"(sBuf), "s"(sRk), "v"(vL16), "v"(vFlag), "v"(vSnapOff), "v"(vSnapRd), "v"(vWd0), "v"(vWd1), "v"(vWr0), "v"(vWr1), "s"(w4) : "v0", "v1", "v2", "v3", "v4", "v5", "v6", "v7", "v8", "v9", "v10", "v11", "v12", "v13", "v14", "v15", "v16", "v17", "v18", "v19", "v20", "v21", "v22", "v23", "v24", "v25", "v26", "v27", "v28", "v29", "v30", "v31", "v32", "v33", "v34", "v35", "v36", "v37", "v38", "v39", "v40", "v41", "v42", "v43", "v44", "v45", "v46", "v47", "v48", "v49", "v50", "v51", "v52", "v53", "v54", "v55", "v56", "v57", "v58", "v59", "v60", "v61", "v62", "v63", "v64", "v65", "v66", "v67", "v68", "v69", "v70", "v71", "v72", "v73", "v74", "v75", "v76", "v77", "v78", "v79", "v80", "v81", "v82", "v83", "v84", "v85", "v86", "v87", "v88", "v89", "v90", "v91", "v92", "v93", "v94", "v95", "v96", "v97", "v98", "v99", "v100", "v101", "v102", "v103", "v104", "v105", "v106", "v107", "v108", "v109", "v110", "v111", "v112", "v113", "v114", "v115", "v116", "v117", "v118", "v119", "v120", "v121", "v122", "v123", "v124", "v125", "v126", "v127", "v128", "v129", "v130", "v131", "v132", "v133", "v134", "v135", "v136", "v137", "v138", "v139", "v140", "v141", "v142", "v143", "v144", "v145", "v146", "v147", "v148", "v149", "v150", "v151", "v152", "v153", "v154", "v155", "v156", "v157", "v158", "v159", "v160", "v161", "v162", "v163", "v164", "v165", "v166", "v167", "v168", "v169", "v170", "v171", "v172", "v173", "v174", "v175", "v176", "v177", "v178", "v179", "v180", "v181", "v182", "v183", "v184", "v185", "v186", "v187", "v188", "v189", "v190", "v191", "v192", "v193", "v194", "v195", "v196", "v197", "v198", "v199", "v200", "v201", "v202", "v203", "v204", "v205", "v206", "v207", "v208", "v209", "v210", "v211", "v212", "v213", "v214", "v215", "v216", "v217", "v218", "v219", "v220", "v221", "v222", "v223", "v224", "v225", "v226", "v227", "v228", "v229", "v230", "v231", "v232", "v233", "v234", "v235", "v236", "v237", "v238", "v239", "a0", "a1", "a2", "a3", "a4", "a5", "a6", "a7", "a8", "a9", "a10", "a11", "a12", "a13", "a14", "a15", "a16", "a17", "a18", "a19", "a20", "a21", "a22", "a23", "a24", "a25", "a26", "a27", "a28", "a29", "a30", "a31", "a32", "a33", "a34", "a35", "a36", "a37", "a38", "a39", "a40", "a41", "a42", "a43", "a44", "a45", "a46", "a47", "a48", "a49", "a50", "a51", "a52", "a53", "a54", "a55", "a56", "a57", "a58", "a59", "a60", "a61", "a62", "a63", "a64", "a65", "a66", "a67", "a68", "a69", "a70", "a71", "a72", "a73", "a74", "a75", "a76", "a77", "a78", "a79", "a80", "a81", "a82", "a83", "a84", "a85", "a86", "a87", "a88", "a89", "a90", "a91", "a92", "a93", "a94", "a95", "a96", "a97", "a98", "a99", "a100", "a101", "a102", "a103", "a104", "a105", "a106", "a107", "a108", "a109", "a110", "a111", "a112", "a113", "a114", "a115", "a116", "a117", "a118", "a119", "a120", "a121", "a122", "a123", "a124", "a125", "a126", "a127", "a128", "a129", "a130", "a131", "a132", "a133", "a134", "a135", "a136", "a137", "a138", "a139", "a140", "a141", "a142", "a143", "a144", "a145", "a146", "a147", "a148", "a149", "a150", "a151", "a152", "a153", "a154", "a155", "a156", "a157", "a158", "a159", "a160", "a161", "a162", "a163", "a164", "a165", "a166", "a167", "a168", "a169", "a170", "a171", "a172", "a173", "a174", "a175", "a176", "a177", "a178", "a179", "a180", "a181", "a182", "a183", "a184", "a185", "a186", "a187", "a188", "a189", "a190", "a191", "a192", "a193", "a194", "a195", "a196", "a197", "a198", "a199", "a200", "a201", "a202", "a203", "a204", "a205", "a206", "a207", "a208", "a209", "a210", "a211", "a212", "a213", "a214", "a215", "a216", "a217", "a218", "a219", "a220", "a221", "a222", "a223", "a224", "a225", "a226", "a227", "a228", "a229", "a230", "a231", "a232", "a233", "a234", "a235", "a236", "a237", "a238", "a239", "a240", "a241", "a242", "a243", "a244", "a245", "a246", "a247", "a248", "a249", "a250", "a251", "a252", "a253", "a254", "a255", "s36", "s37", "s38", "s39", "s40", "s41", "s42", "s43", "s44", "s45", "s46", "s47", "s48", "s49", "s50", "s51", "s52", "s53", "s54", "s55", "s56", "s57", "s58", "s59", "s60", "s61", "s62", "s63", "s64", "s65", "s66", "s67", "s68", "s69", "s70", "s71", "s72", "s73", "s74", "s75", "s76", "s77", "s78", "s79", "s80", "s81", "s82", "s83", "s84", "s85", "s86", "s87", "s88", "s89", "s90", "s91", "s92", "s93", "s94", "s95", "s96", "s97", "vcc", "scc", "memory");
    __builtin_unreachable();
  } else {
    if (kind != 6) {
      const unsigned sStep = 4;
      COMMON(kind == 1 ? 8 : 16, kind == 1 ? fj * 128 : fj < 8 ? 1024 + fj * 128 : (fj & 2) * 1024 + (fj - 8) * 128, 1024, 131072, 2048, 256, kind == 1 ? 0 : 32768);
      const int sr = l15 - ((wave & 1) ? -1 : 1) * (1 + (wave >> 1)), nx = (slice >> 1) & 1;
      const unsigned vAn = kind == 1 ? HU + vL16 : (sr & 16) ? 1024 + lg * 64 + (sr & 3) * 16 : (lg * 16 + sr) * 16;
      const unsigned vAe = kind == 1 ? ((l15 >> 2) == (nx ? 0 : 3) ? (nx ? 0 : 2 * HU) + 1024 + lg * 64 + (l15 & 3) * 16 : OOB) : (sr & 16) ? OOB : vAn;
      const unsigned vOutS = 0, vAux0 = l15 * 16, vAux1 = 139264 + (l15 * 2 + (lg & 1)) * 16 + wave * 4 + (lg >> 1) * 512, vAux2 = 139264 + (l15 * 2 + (lg & 1)) * 16;
      const unsigned vAux3 = (wave == 0 && lg < 2) ? slice * 128 + l15 * 8 + lg * 4 : OOB;
      const unsigned sOutOff = kind == 1 ? OFF_HS - HU + (slice * 4 + wave) * 1280 : OFF_X1 + (slice * 4 + wave) * 1024, sFoutOff = (kind == 1 ? OFF_FH : OFF_FX1) + slice * 128;
      const unsigned sAuxOff = OFF_XP + (slice * 4 + wave) * 1024, sBuf = kind, sWw = 16 * wave * 2048 + slice * 256;
      const u16* w12 = kind == 1 ? w1 : w2;
      unsigned epOut;
      asm volatile(
R"(.macro M d,w,x
v_mfma_f32_16x16x32_bf16 v[\d:\d+3], \w, v[\x:\x+3], v[\d:\d+3]
.endm
.macro R d,a,o
ds_read_b128 v[\d:\d+3], v\a offset:\o
.endm
.macro T d,a,o
ds_read_b64_tr_b16 \d, v\a offset:\o
.endm
.macro D a,s,o
buffer_load_dwordx4 v\a, s[36:39], s\s offen offset:\o sc1 lds
.endm
.macro L c
s_waitcnt lgkmcnt(\c)
.endm
Lcode_g12_b:
s_cmp_eq_u32 %9,2
s_cbranch_scc1 Lg2_begin_%=
s_mov_b64 s[36:37],%1
s_mov_b32 s38,111243392
s_mov_b32 s39,0x00020000
s_mov_b32 s44,%2
s_mov_b32 s45,%3
s_mov_b32 s46,%4
s_mov_b32 s47,%5
s_mov_b32 s48,%6
s_mov_b32 s49,%7
s_mov_b32 s50,%8
s_mov_b32 s51,%9
s_mov_b32 s93,%10
v_mov_b32 v0,%11
v_mov_b32 v3,%12
v_mov_b32 v5,%13
v_mov_b32 v6,%14
v_mov_b32 v18,%15
v_mov_b32 v19,%16
v_mov_b32 v20,%17
v_mov_b32 v21,%18
v_mov_b32 v22,%19
v_mov_b32 v10,%20
v_mov_b32 v11,%21
v_mov_b32 v12,%22
v_mov_b32 v13,%23
v_mov_b32 v14,%24
v_mov_b32 v15,%25
v_mov_b32 v16,%26
s_mov_b64 s[40:41],%27
s_mov_b32 s42,2097152
s_mov_b32 s43,0x00020000
s_lshl_b32 s57,s44,11
s_mov_b32 s79,111243264
buffer_load_dword v4,off,s[36:39],s79 sc1
s_add_u32 s55,s50,0
s_add_u32 m0,s57,0
s_nop 0
buffer_load_dwordx4 v18,s[40:43],s55 offen offset:0  lds
s_add_u32 m0,s57,1024
s_nop 0
buffer_load_dwordx4 v19,s[40:43],s55 offen offset:0  lds
s_add_u32 m0,s57,8064
s_nop 0
buffer_load_dwordx4 v18,s[40:43],s55 offen offset:128  lds
s_add_u32 m0,s57,9088
s_nop 0
buffer_load_dwordx4 v19,s[40:43],s55 offen offset:128  lds
s_add_u32 s55,s50,131072
s_add_u32 m0,s57,16384
s_nop 0
buffer_load_dwordx4 v18,s[40:43],s55 offen offset:0  lds
s_add_u32 m0,s57,17408
s_nop 0
buffer_load_dwordx4 v19,s[40:43],s55 offen offset:0  lds
s_add_u32 m0,s57,24448
s_nop 0
buffer_load_dwordx4 v18,s[40:43],s55 offen offset:128  lds
s_add_u32 m0,s57,25472
s_nop 0
buffer_load_dwordx4 v19,s[40:43],s55 offen offset:128  lds
s_add_u32 s55,s50,262144
s_add_u32 m0,s57,32768
s_nop 0
buffer_load_dwordx4 v18,s[40:43],s55 offen offset:0  lds
s_add_u32 m0,s57,33792
s_nop 0
buffer_load_dwordx4 v19,s[40:43],s55 offen offset:0  lds
s_add_u32 m0,s57,40832
s_nop 0
buffer_load_dwordx4 v18,s[40:43],s55 offen offset:128  lds
s_add_u32 m0,s57,41856
s_nop 0
buffer_load_dwordx4 v19,s[40:43],s55 offen offset:128  lds
s_add_u32 s55,s50,393216
s_add_u32 m0,s57,49152
s_nop 0
buffer_load_dwordx4 v18,s[40:43],s55 offen offset:0  lds
s_add_u32 m0,s57,50176
s_nop 0
buffer_load_dwordx4 v19,s[40:43],s55 offen offset:0  lds
s_add_u32 m0,s57,57216
s_nop 0
buffer_load_dwordx4 v18,s[40:43],s55 offen offset:128  lds
s_add_u32 m0,s57,58240
s_nop 0
buffer_load_dwordx4 v19,s[40:43],s55 offen offset:128  lds
s_add_u32 s55,s50,524288
s_add_u32 m0,s57,65536
s_nop 0
buffer_load_dwordx4 v18,s[40:43],s55 offen offset:0  lds
s_add_u32 m0,s57,66560
s_nop 0
buffer_load_dwordx4 v19,s[40:43],s55 offen offset:0  lds
s_add_u32 m0,s57,73600
s_nop 0
buffer_load_dwordx4 v18,s[40:43],s55 offen offset:128  lds
s_add_u32 m0,s57,74624
s_nop 0
buffer_load_dwordx4 v19,s[40:43],s55 offen offset:128  lds
s_add_u32 s55,s50,655360
s_add_u32 m0,s57,81920
s_nop 0
buffer_load_dwordx4 v18,s[40:43],s55 offen offset:0  lds
s_add_u32 m0,s57,82944
s_nop 0
buffer_load_dwordx4 v19,s[40:43],s55 offen offset:0  lds
s_add_u32 m0,s57,89984
s_nop 0
buffer_load_dwordx4 v18,s[40:43],s55 offen offset:128  lds
s_add_u32 m0,s57,91008
s_nop 0
buffer_load_dwordx4 v19,s[40:43],s55 offen offset:128  lds
s_waitcnt vmcnt(24)
s_barrier
v_readfirstlane_b32 s53,v4
s_nop 3
s_add_u32 s53,s53,1
s_cmp_eq_u32 s53,0
s_cselect_b32 s53,1,s53
v_mov_b32 v4,s53
s_waitcnt vmcnt(20)
L 0
s_barrier
s_add_u32 s55,s50,786432
s_add_u32 m0,s57,98304
s_nop 0
buffer_load_dwordx4 v18,s[40:43],s55 offen offset:0  lds
s_add_u32 m0,s57,99328
s_nop 0
buffer_load_dwordx4 v19,s[40:43],s55 offen offset:0  lds
s_add_u32 m0,s57,106368
s_nop 0
buffer_load_dwordx4 v18,s[40:43],s55 offen offset:128  lds
s_add_u32 m0,s57,107392
s_nop 0
buffer_load_dwordx4 v19,s[40:43],s55 offen offset:128  lds
v_add_u32 v26,0,v20
v_add_u32 v27,0,v21
T a[0:1],26,0
T a[2:3],26,512
T a[4:5],27,0
T a[6:7],27,512
T a[8:9],26,4096
T a[10:11],26,4608
T a[12:13],27,4096
T a[14:15],27,4608
s_waitcnt vmcnt(20)
L 0
s_barrier
s_add_u32 s55,s50,917504
s_add_u32 m0,s57,0
s_nop 0
buffer_load_dwordx4 v18,s[40:43],s55 offen offset:0  lds
s_add_u32 m0,s57,1024
s_nop 0
buffer_load_dwordx4 v19,s[40:43],s55 offen offset:0  lds
s_add_u32 m0,s57,8064
s_nop 0
buffer_load_dwordx4 v18,s[40:43],s55 offen offset:128  lds
s_add_u32 m0,s57,9088
s_nop 0
buffer_load_dwordx4 v19,s[40:43],s55 offen offset:128  lds
v_add_u32 v26,16384,v20
v_add_u32 v27,16384,v21
T a[16:17],26,0
T a[18:19],26,512
T a[20:21],27,0
T a[22:23],27,512
T a[24:25],26,4096
T a[26:27],26,4608
T a[28:29],27,4096
T a[30:31],27,4608
s_waitcnt vmcnt(20)
L 0
s_barrier
s_add_u32 s55,s50,1048576
s_add_u32 m0,s57,16384
s_nop 0
buffer_load_dwordx4 v18,s[40:43],s55 offen offset:0  lds
s_add_u32 m0,s57,17408
s_nop 0
buffer_load_dwordx4 v19,s[40:43],s55 offen offset:0  lds
s_add_u32 m0,s57,24448
s_nop 0
buffer_load_dwordx4 v18,s[40:43],s55 offen offset:128  lds
s_add_u32 m0,s57,25472
s_nop 0
buffer_load_dwordx4 v19,s[40:43],s55 offen offset:128  lds
v_add_u32 v26,32768,v20
v_add_u32 v27,32768,v21
T a[32:33],26,0
T a[34:35],26,512
T a[36:37],27,0
T a[38:39],27,512
T a[40:41],26,4096
T a[42:43],26,4608
T a[44:45],27,4096
T a[46:47],27,4608
s_waitcnt vmcnt(20)
L 0
s_barrier
s_add_u32 s55,s50,1179648
s_add_u32 m0,s57,32768
s_nop 0
buffer_load_dwordx4 v18,s[40:43],s55 offen offset:0  lds
s_add_u32 m0,s57,33792
s_nop 0
buffer_load_dwordx4 v19,s[40:43],s55 offen offset:0  lds
s_add_u32 m0,s57,40832
s_nop 0
buffer_load_dwordx4 v18,s[40:43],s55 offen offset:128  lds
s_add_u32 m0,s57,41856
s_nop 0
buffer_load_dwordx4 v19,s[40:43],s55 offen offset:128  lds
v_add_u32 v26,49152,v20
v_add_u32 v27,49152,v21
T a[48:49],26,0
T a[50:51],26,512
T a[52:53],27,0
T a[54:55],27,512
T a[56:57],26,4096
T a[58:59],26,4608
T a[60:61],27,4096
T a[62:63],27,4608
s_waitcnt vmcnt(20)
L 0
s_barrier
s_add_u32 s55,s50,1310720
s_add_u32 m0,s57,49152
s_nop 0
buffer_load_dwordx4 v18,s[40:43],s55 offen offset:0  lds
s_add_u32 m0,s57,50176
s_nop 0
buffer_load_dwordx4 v19,s[40:43],s55 offen offset:0  lds
s_add_u32 m0,s57,57216
s_nop 0
buffer_load_dwordx4 v18,s[40:43],s55 offen offset:128  lds
s_add_u32 m0,s57,58240
s_nop 0
buffer_load_dwordx4 v19,s[40:43],s55 offen offset:128  lds
v_add_u32 v26,65536,v20
v_add_u32 v27,65536,v21
T a[64:65],26,0
T a[66:67],26,512
T a[68:69],27,0
T a[70:71],27,512
T a[72:73],26,4096
T a[74:75],26,4608
T a[76:77],27,4096
T a[78:79],27,4608
s_waitcnt vmcnt(20)
L 0
s_barrier
s_add_u32 s55,s50,1441792
s_add_u32 m0,s57,65536
s_nop 0
buffer_load_dwordx4 v18,s[40:43],s55 offen offset:0  lds
s_add_u32 m0,s57,66560
s_nop 0
buffer_load_dwordx4 v19,s[40:43],s55 offen offset:0  lds
s_add_u32 m0,s57,73600
s_nop 0
buffer_load_dwordx4 v18,s[40:43],s55 offen offset:128  lds
s_add_u32 m0,s57,74624
s_nop 0
buffer_load_dwordx4 v19,s[40:43],s55 offen offset:128  lds
v_add_u32 v26,81920,v20
v_add_u32 v27,81920,v21
T a[80:81],26,0
T a[82:83],26,512
T a[84:85],27,0
T a[86:87],27,512
T a[88:89],26,4096
T a[90:91],26,4608
T a[92:93],27,4096
T a[94:95],27,4608
s_waitcnt vmcnt(20)
L 0
s_barrier
s_add_u32 s55,s50,1572864
s_add_u32 m0,s57,81920
s_nop 0
buffer_load_dwordx4 v18,s[40:43],s55 offen offset:0  lds
s_add_u32 m0,s57,82944
s_nop 0
buffer_load_dwordx4 v19,s[40:43],s55 offen offset:0  lds
s_add_u32 m0,s57,89984
s_nop 0
buffer_load_dwordx4 v18,s[40:43],s55 offen offset:128  lds
s_add_u32 m0,s57,91008
s_nop 0
buffer_load_dwordx4 v19,s[40:43],s55 offen offset:128  lds
v_add_u32 v26,98304,v20
v_add_u32 v27,98304,v21
T a[96:97],26,0
T a[98:99],26,512
T a[100:101],27,0
T a[102:103],27,512
T a[104:105],26,4096
T a[106:107],26,4608
T a[108:109],27,4096
T a[110:111],27,4608
s_waitcnt vmcnt(20)
L 0
s_barrier
s_add_u32 s55,s50,1703936
s_add_u32 m0,s57,98304
s_nop 0
buffer_load_dwordx4 v18,s[40:43],s55 offen offset:0  lds
s_add_u32 m0,s57,99328
s_nop 0
buffer_load_dwordx4 v19,s[40:43],s55 offen offset:0  lds
s_add_u32 m0,s57,106368
s_nop 0
buffer_load_dwordx4 v18,s[40:43],s55 offen offset:128  lds
s_add_u32 m0,s57,107392
s_nop 0
buffer_load_dwordx4 v19,s[40:43],s55 offen offset:128  lds
v_add_u32 v26,0,v20
v_add_u32 v27,0,v21
T a[112:113],26,0
T a[114:115],26,512
T a[116:117],27,0
T a[118:119],27,512
T a[120:121],26,4096
T a[122:123],26,4608
T a[124:125],27,4096
T a[126:127],27,4608
s_waitcnt vmcnt(20)
L 0
s_barrier
s_add_u32 s55,s50,1835008
s_add_u32 m0,s57,0
s_nop 0
buffer_load_dwordx4 v18,s[40:43],s55 offen offset:0  lds
s_add_u32 m0,s57,1024
s_nop 0
buffer_load_dwordx4 v19,s[40:43],s55 offen offset:0  lds
s_add_u32 m0,s57,8064
s_nop 0
buffer_load_dwordx4 v18,s[40:43],s55 offen offset:128  lds
s_add_u32 m0,s57,9088
s_nop 0
buffer_load_dwordx4 v19,s[40:43],s55 offen offset:128  lds
v_add_u32 v26,16384,v20
v_add_u32 v27,16384,v21
T a[128:129],26,0
T a[130:131],26,512
T a[132:133],27,0
T a[134:135],27,512
T a[136:137],26,4096
T a[138:139],26,4608
T a[140:141],27,4096
T a[142:143],27,4608
s_waitcnt vmcnt(20)
L 0
s_barrier
s_add_u32 s55,s50,1966080
s_add_u32 m0,s57,16384
s_nop 0
buffer_load_dwordx4 v18,s[40:43],s55 offen offset:0  lds
s_add_u32 m0,s57,17408
s_nop 0
buffer_load_dwordx4 v19,s[40:43],s55 offen offset:0  lds
s_add_u32 m0,s57,24448
s_nop 0
buffer_load_dwordx4 v18,s[40:43],s55 offen offset:128  lds
s_add_u32 m0,s57,25472
s_nop 0
buffer_load_dwordx4 v19,s[40:43],s55 offen offset:128  lds
v_add_u32 v26,32768,v20
v_add_u32 v27,32768,v21
T a[144:145],26,0
T a[146:147],26,512
T a[148:149],27,0
T a[150:151],27,512
T a[152:153],26,4096
T a[154:155],26,4608
T a[156:157],27,4096
T a[158:159],27,4608
s_waitcnt vmcnt(20)
L 0
s_barrier
v_add_u32 v26,49152,v20
v_add_u32 v27,49152,v21
T a[160:161],26,0
T a[162:163],26,512
T a[164:165],27,0
T a[166:167],27,512
T a[168:169],26,4096
T a[170:171],26,4608
T a[172:173],27,4096
T a[174:175],27,4608
s_waitcnt vmcnt(16)
L 0
s_barrier
v_add_u32 v26,65536,v20
v_add_u32 v27,65536,v21
T a[176:177],26,0
T a[178:179],26,512
T a[180:181],27,0
T a[182:183],27,512
T a[184:185],26,4096
T a[186:187],26,4608
T a[188:189],27,4096
T a[190:191],27,4608
s_waitcnt vmcnt(12)
L 0
s_barrier
v_add_u32 v26,81920,v20
v_add_u32 v27,81920,v21
T a[192:193],26,0
T a[194:195],26,512
T a[196:197],27,0
T a[198:199],27,512
T a[200:201],26,4096
T a[202:203],26,4608
T a[204:205],27,4096
T a[206:207],27,4608
s_waitcnt vmcnt(8)
L 0
s_barrier
v_add_u32 v26,98304,v20
v_add_u32 v27,98304,v21
T a[208:209],26,0
T a[210:211],26,512
T a[212:213],27,0
T a[214:215],27,512
T a[216:217],26,4096
T a[218:219],26,4608
T a[220:221],27,4096
T a[222:223],27,4608
s_waitcnt vmcnt(4)
L 0
s_barrier
v_add_u32 v26,0,v20
v_add_u32 v27,0,v21
T a[224:225],26,0
T a[226:227],26,512
T a[228:229],27,0
T a[230:231],27,512
T a[232:233],26,4096
T a[234:235],26,4608
T a[236:237],27,4096
T a[238:239],27,4608
s_waitcnt vmcnt(0)
L 0
s_barrier
v_add_u32 v26,16384,v20
v_add_u32 v27,16384,v21
T a[240:241],26,0
T a[242:243],26,512
T a[244:245],27,0
T a[246:247],27,512
T a[248:249],26,4096
T a[250:251],26,4608
T a[252:253],27,4096
T a[254:255],27,4608
L 0
s_barrier
v_mov_b32 v34,0x3f803f80
v_mov_b32 v35,0x3f803f80
v_mov_b32 v36,0x3f803f80
v_mov_b32 v37,0x3f803f80
s_nop 1
v_mfma_f32_16x16x32_bf16 v[106:109],a[0:3],v[34:37],0
v_mfma_f32_16x16x32_bf16 v[110:113],a[4:7],v[34:37],0
M 106,a[8:11],34
M 110,a[12:15],34
M 106,a[16:19],34
M 110,a[20:23],34
M 106,a[24:27],34
M 110,a[28:31],34
M 106,a[32:35],34
M 110,a[36:39],34
M 106,a[40:43],34
M 110,a[44:47],34
M 106,a[48:51],34
M 110,a[52:55],34
M 106,a[56:59],34
M 110,a[60:63],34
M 106,a[64:67],34
)"
R"(M 110,a[68:71],34
M 106,a[72:75],34
M 110,a[76:79],34
M 106,a[80:83],34
M 110,a[84:87],34
M 106,a[88:91],34
M 110,a[92:95],34
M 106,a[96:99],34
M 110,a[100:103],34
M 106,a[104:107],34
M 110,a[108:111],34
M 106,a[112:115],34
M 110,a[116:119],34
M 106,a[120:123],34
M 110,a[124:127],34
M 106,a[128:131],34
M 110,a[132:135],34
M 106,a[136:139],34
M 110,a[140:143],34
M 106,a[144:147],34
M 110,a[148:151],34
M 106,a[152:155],34
M 110,a[156:159],34
M 106,a[160:163],34
M 110,a[164:167],34
M 106,a[168:171],34
M 110,a[172:175],34
M 106,a[176:179],34
M 110,a[180:183],34
M 106,a[184:187],34
M 110,a[188:191],34
M 106,a[192:195],34
M 110,a[196:199],34
M 106,a[200:203],34
M 110,a[204:207],34
M 106,a[208:211],34
M 110,a[212:215],34
M 106,a[216:219],34
M 110,a[220:223],34
M 106,a[224:227],34
M 110,a[228:231],34
M 106,a[232:235],34
M 110,a[236:239],34
M 106,a[240:243],34
M 110,a[244:247],34
M 106,a[248:251],34
M 110,a[252:255],34
v_mov_b32 v4,s53
s_mov_b32 s82,0
s_mov_b32 s80,0
s_mov_b32 s54,s45
s_mul_i32 s57,s44,8192
s_mul_i32 s55,s45,32768
s_add_u32 s55,s55,s57
s_add_u32 s55,s55,8388608
s_mul_i32 s57,s44,8192
s_mul_i32 s60,s46,32768
s_mul_i32 s61,s46,1024
s_mul_i32 s62,s46,40960
s_mul_i32 s63,s46,1024
s_mul_i32 s64,s46,512
s_lshl_b32 s59,s44,8
s_add_u32 s59,s59,131072
s_mul_i32 s58,s45,1024
s_add_u32 s58,s58,109056000
s_mul_i32 s65,s45,40960
s_add_u32 s65,s65,s47
s_mul_i32 s68,s45,1024
s_add_u32 s68,s68,s48
s_mul_i32 s73,s45,512
s_add_u32 s73,s73,106954752
s_mul_i32 s74,s45,32768
s_add_u32 s74,s74,s49
s_mul_i32 s75,s45,1024
s_add_u32 s75,s75,107479040
s_mov_b32 s66,0x7fff0000
s_mov_b32 s67,0x7fff0000
s_mov_b32 s69,0x7fff0000
s_mov_b32 s76,0x7fff0000
s_mov_b32 s77,0x7fff0000
s_mov_b32 s70,0x7fff0000
s_mov_b32 s71,0x7fff0000
s_mov_b32 s72,0x7fff0000
v_mov_b32 v1,v0
v_add_u32 v2,32768,v0
s_mov_b32 s84,32768
s_mov_b32 s86,0x3a800000
s_mov_b32 s87,0x3727c5ac
s_mov_b32 s88,0xc0135761
s_mov_b32 s89,0x3d372713
s_and_b32 s85,s44,1
s_mul_i32 s85,s85,127
v_lshlrev_b32 v8,22,v0
v_and_b32 v8,0x80000000,v8
v_or_b32 v8,v8,v5
s_getpc_b64 s[40:41]
Lg1_pc_%=:
s_lshl_b32 s78,s44,13
v_lshlrev_b32 v114,3,v0
v_add_u32 v114,s78,v114
v_min_u32 v114,Lcode_g2_b-Lg1_pc_%=-4,v114
s_mov_b32 m0,163584
s_nop 0
global_load_lds_dword v114,s[40:41]
s_add_u32 m0,s59,4096
s_nop 0
buffer_load_dword v5,s[36:39],s58 offen offset:0 sc1 lds
buffer_store_dword v4,v3,s[36:39],s71 offen offset:0 sc0 sc1
s_add_u32 m0,s59,5120
s_nop 0
buffer_load_dword v5,s[36:39],s58 offen offset:0 sc1 lds
buffer_store_dword v4,v3,s[36:39],s71 offen offset:0 sc0 sc1
s_mov_b32 s83,0
Lg1_poll_%=:
s_add_u32 s78,s83,2
s_and_b32 s78,s78,3
s_lshl_b32 s78,s78,10
s_add_u32 s78,s78,4096
s_add_u32 m0,s59,s78
s_nop 0
buffer_load_dword v5,s[36:39],s58 offen offset:0 sc1 lds
s_and_b32 s78,s83,3
s_lshl_b32 s78,s78,10
v_add_u32 v114,s78,v6
s_waitcnt vmcnt(4)
s_barrier
ds_read_b32 v7,v114 offset:4096
buffer_store_dword v4,v3,s[36:39],s70 offen offset:0 sc0 sc1
s_mov_b32 s70,s71
s_mov_b32 s71,s72
s_mov_b32 s72,0x7fff0000
s_add_u32 s83,s83,1
s_add_u32 s82,s82,1
L 0
v_cmp_eq_u32 vcc,s53,v7
s_nop 0
s_cmp_eq_u32 vcc_lo,-1
s_cbranch_scc1 Lg1_go_%=
s_cmp_lt_u32 s82,20000
s_cbranch_scc1 Lg1_poll_%=
Lg1_go_%=:
s_mov_b32 m0,s57
s_add_u32 s56,s55,4096
D 0,55,0
D 0,55,1024
D 0,55,2048
D 0,55,3072
s_add_u32 m0,s57,4096
s_nop 0
D 0,56,0
D 0,56,1024
D 0,56,2048
D 0,56,3072
s_add_u32 s55,s55,s60
s_add_u32 s57,s57,32768
s_and_b32 s57,s57,131071
s_add_u32 s54,s54,s46
Lg1_gox_%=:
s_add_u32 s58,s58,s61
s_cmp_eq_u32 vcc_hi,-1
s_cbranch_scc1 Lg1_go2_%=
s_add_u32 m0,s59,2048
s_nop 0
buffer_load_dword v5,s[36:39],s58 offen offset:0 sc1 lds
s_waitcnt vmcnt(0)
s_barrier
buffer_store_dword v4,v3,s[36:39],s70 offen offset:0 sc0 sc1
s_min_u32 s71,s71,s72
buffer_store_dword v4,v3,s[36:39],s71 offen offset:0 sc0 sc1
s_mov_b32 s70,0x7fff0000
s_mov_b32 s71,0x7fff0000
s_mov_b32 s72,0x7fff0000
s_cmp_eq_u32 s80,1
s_cbranch_scc1 Lg1_st11_%=
Lg1_st10_%=:
ds_read_b32 v7,v6 offset:2048
R 34,1,0
R 38,1,1024
R 42,1,2048
R 46,1,3072
R 50,1,4096
R 54,1,5120
R 58,1,6144
L 7
v_cmp_eq_u32 vcc,s53,v7
L 5
s_cmp_eq_u64 vcc,-1
s_cbranch_scc1 Lg1_L20_%=
s_cmp_eq_u32 vcc_lo,-1
s_cbranch_scc1 Lg1_L10_%=
s_branch Lg1_X0_%=
Lg1_st11_%=:
ds_read_b32 v7,v6 offset:2048
R 34,2,0
R 38,2,1024
R 42,2,2048
R 46,2,3072
R 50,2,4096
R 54,2,5120
R 58,2,6144
L 7
v_cmp_eq_u32 vcc,s53,v7
L 5
s_cmp_eq_u64 vcc,-1
s_cbranch_scc1 Lg1_L21_%=
s_cmp_eq_u32 vcc_lo,-1
s_cbranch_scc1 Lg1_L11_%=
s_branch Lg1_X1_%=
Lg1_go2_%=:
s_add_u32 s58,s58,s61
s_add_u32 m0,s59,2048
s_nop 0
buffer_load_dword v5,s[36:39],s58 offen offset:0 sc1 lds
s_mov_b32 m0,s57
s_add_u32 s56,s55,4096
D 0,55,0
D 0,55,1024
D 0,55,2048
D 0,55,3072
s_add_u32 m0,s57,4096
s_nop 0
D 0,56,0
D 0,56,1024
D 0,56,2048
D 0,56,3072
s_add_u32 s55,s55,s60
s_add_u32 s57,s57,32768
s_and_b32 s57,s57,131071
s_add_u32 s54,s54,s46
s_waitcnt vmcnt(8)
s_barrier
buffer_store_dword v4,v3,s[36:39],s70 offen offset:0 sc0 sc1
s_min_u32 s71,s71,s72
buffer_store_dword v4,v3,s[36:39],s71 offen offset:0 sc0 sc1
s_mov_b32 s70,0x7fff0000
s_mov_b32 s71,0x7fff0000
s_mov_b32 s72,0x7fff0000
s_cmp_eq_u32 s80,1
s_cbranch_scc1 Lg1_st21_%=
Lg1_st20_%=:
ds_read_b32 v7,v6 offset:2048
R 34,1,0
R 38,1,1024
R 42,1,2048
R 46,1,3072
R 50,1,4096
R 54,1,5120
R 58,1,6144
L 7
v_cmp_eq_u32 vcc,s53,v7
L 5
s_cmp_eq_u32 vcc_lo,-1
s_cbranch_scc1 Lg1_S0_%=
s_branch Lg1_N0_%=
Lg1_st21_%=:
ds_read_b32 v7,v6 offset:2048
R 34,2,0
R 38,2,1024
R 42,2,2048
R 46,2,3072
R 50,2,4096
R 54,2,5120
R 58,2,6144
L 7
v_cmp_eq_u32 vcc,s53,v7
L 5
s_cmp_eq_u32 vcc_lo,-1
s_cbranch_scc1 Lg1_S1_%=
s_branch Lg1_N1_%=
Lg1_L20_%=:
v_mfma_f32_16x16x32_bf16 v[22:25],a[4:7],v[34:37],0
s_add_u32 s58,s58,s61
s_add_u32 s58,s58,s61
R 62,1,7168
M 22,a[12:15],38
s_add_u32 m0,s59,0
buffer_load_dwordx4 v[98:101],v11,s[36:39],s73 offen offset:0 sc1
v_mfma_f32_16x16x32_bf16 v[18:21],a[0:3],v[34:37],0
s_add_u32 s56,s55,4096
R 66,1,8192
v_fma_f32 v26,-v102,v106,v26
M 18,a[8:11],38
L 5
buffer_load_dword v8,s[36:39],s58 offen offset:0 sc1 lds
M 18,a[16:19],42
s_mov_b32 m0,s57
R 70,1,9216
v_fma_f32 v27,-v102,v107,v27
M 18,a[24:27],46
s_add_u32 s54,s54,s46
v_fma_f32 v28,-v102,v108,v28
v_fma_f32 v29,-v102,v109,v29
M 22,a[20:23],42
s_mov_b32 s66,s65
R 74,1,10240
v_mul_f32 v114,v26,v26
M 22,a[28:31],46
L 5
D 0,55,0
M 22,a[36:39],50
s_add_u32 s73,s73,s64
R 78,1,11264
v_mul_f32 v115,v27,v27
M 22,a[44:47],54
s_add_u32 s54,s54,s46
v_mul_f32 v116,v28,v28
v_mul_f32 v117,v29,v29
M 18,a[32:35],50
s_add_u32 s65,s65,s62
R 82,1,12288
v_fma_f32 v114,v104,v114,v103
M 18,a[40:43],54
L 5
D 0,55,1024
M 18,a[48:51],58
R 86,1,13312
v_fma_f32 v115,v104,v115,v103
M 18,a[56:59],62
v_fma_f32 v116,v104,v116,v103
v_fma_f32 v117,v104,v117,v103
M 22,a[52:55],58
R 90,1,14336
v_mul_f32 v114,v26,v114
M 22,a[60:63],62
L 5
D 0,55,2048
M 22,a[68:71],66
R 94,1,15360
v_mul_f32 v115,v27,v115
M 22,a[76:79],70
v_exp_f32 v114,v114
M 18,a[64:67],66
R 34,1,16384
v_mul_f32 v116,v28,v116
M 18,a[72:75],70
L 5
D 0,55,3072
M 18,a[80:83],74
s_add_u32 m0,s57,4096
R 38,1,17408
v_mul_f32 v117,v29,v117
M 18,a[88:91],78
s_add_u32 s57,s57,32768
v_exp_f32 v115,v115
M 22,a[84:87],74
s_and_b32 s57,s57,131071
R 42,1,18432
v_fma_f32 v114,v114,v105,v105
M 22,a[92:95],78
L 5
D 0,56,0
M 22,a[100:103],82
s_add_u32 s55,s55,s60
R 46,1,19456
v_fma_f32 v115,v115,v105,v105
M 22,a[108:111],86
v_exp_f32 v116,v116
M 18,a[96:99],82
R 50,1,20480
v_fma_f32 v116,v116,v105,v105
M 18,a[104:107],86
L 5
D 0,56,1024
M 18,a[112:115],90
R 54,1,21504
v_fma_f32 v30,-v102,v110,v30
M 18,a[120:123],94
v_exp_f32 v117,v117
M 22,a[116:119],90
R 58,1,22528
v_fma_f32 v117,v117,v105,v105
M 22,a[124:127],94
L 5
D 0,56,2048
M 22,a[132:135],34
R 62,1,23552
v_fma_f32 v31,-v102,v111,v31
M 22,a[140:143],38
v_rcp_f32 v114,v114
M 18,a[128:131],34
R 66,1,24576
v_mul_f32 v26,v26,v114
M 18,a[136:139],38
L 5
D 0,56,3072
M 18,a[144:147],42
s_mov_b32 m0,s57
R 70,1,25600
v_fma_f32 v32,-v102,v112,v32
M 18,a[152:155],46
s_add_u32 s56,s55,4096
v_rcp_f32 v115,v115
M 22,a[148:151],42
R 74,1,26624
v_mul_f32 v27,v27,v115
M 22,a[156:159],46
L 5
D 0,55,0
M 22,a[164:167],50
R 78,1,27648
v_cvt_pk_bf16_f32 v122,v26,v27
M 22,a[172:175],54
D 0,55,1024
M 18,a[160:163],50
R 82,1,28672
v_fma_f32 v33,-v102,v113,v33
M 18,a[168:171],54
L 5
v_rcp_f32 v116,v116
M 18,a[176:179],58
R 86,1,29696
v_mul_f32 v28,v28,v116
M 18,a[184:187],62
D 0,55,2048
M 22,a[180:183],58
R 90,1,30720
v_mul_f32 v118,v30,v30
M 22,a[188:191],62
L 5
D 0,55,3072
M 22,a[196:199],66
s_waitcnt vmcnt(4)
R 94,1,31744
v_mul_f32 v119,v31,v31
M 22,a[204:207],70
s_barrier
buffer_store_dword v4,v3,s[36:39],s70 offen offset:0 sc0 sc1
M 18,a[192:195],66
s_add_u32 m0,s57,4096
R 34,2,0
ds_read_b32 v7,v6 offset:0
M 18,a[200:203],70
L 6
D 0,56,0
M 18,a[208:211],74
s_mov_b32 s70,s69
R 38,2,1024
v_mul_f32 v120,v32,v32
M 18,a[216:219],78
s_add_u32 s57,s57,32768
D 0,56,1024
M 22,a[212:215],74
s_mov_b32 s69,s68
R 42,2,2048
v_mul_f32 v121,v33,v33
v_fma_f32 v118,v104,v118,v103
v_fma_f32 v119,v104,v119,v103
v_fma_f32 v120,v104,v120,v103
v_fma_f32 v121,v104,v121,v103
v_mul_f32 v118,v30,v118
v_mul_f32 v119,v31,v119
v_mul_f32 v120,v32,v120
v_mul_f32 v121,v33,v121
v_exp_f32 v118,v118
v_exp_f32 v119,v119
v_exp_f32 v120,v120
v_exp_f32 v121,v121
M 22,a[220:223],78
L 6
v_rcp_f32 v117,v117
v_fma_f32 v118,v118,v105,v105
v_fma_f32 v119,v119,v105,v105
v_fma_f32 v120,v120,v105,v105
v_fma_f32 v121,v121,v105,v105
v_rcp_f32 v118,v118
v_rcp_f32 v119,v119
v_rcp_f32 v120,v120
v_rcp_f32 v121,v121
M 22,a[228:231],82
L 2
R 46,2,3072
v_mul_f32 v29,v29,v117
v_cvt_pk_bf16_f32 v123,v28,v29
v_mul_f32 v30,v30,v118
v_mul_f32 v31,v31,v119
v_mul_f32 v32,v32,v120
v_mul_f32 v33,v33,v121
v_cvt_pk_bf16_f32 v124,v30,v31
v_cvt_pk_bf16_f32 v125,v32,v33
M 22,a[236:239],86
s_add_u32 s55,s55,s60
buffer_store_dwordx4 v[122:125],v15,s[36:39],s67 offen offset:0 sc0 sc1
v_xor_b32 v1,65536,v1
M 18,a[224:227],82
s_and_b32 s57,s57,131071
R 50,2,4096
v_cmp_eq_u32 vcc,s53,v7
M 18,a[232:235],86
s_nop 0
s_add_u32 s68,s68,s63
s_cmp_eq_u32 vcc_lo,-1
M 18,a[240:243],90
R 54,2,5120
M 18,a[248:251],94
buffer_store_dwordx4 v[122:125],v16,s[36:39],s67 offen offset:0 sc0 sc1
M 22,a[244:247],90
R 58,2,6144
M 22,a[252:255],94
L 5
D 0,56,2048
)"
R"(D 0,56,3072
s_cbranch_scc0 Lg1_N1_%=
Lg1_S1_%=:
v_mfma_f32_16x16x32_bf16 v[30:33],a[4:7],v[34:37],0
s_add_u32 s58,s58,s61
s_add_u32 m0,s59,1024
R 62,2,7168
M 30,a[12:15],38
s_add_u32 s56,s55,4096
buffer_load_dword v8,s[36:39],s58 offen offset:0 sc1 lds
v_mfma_f32_16x16x32_bf16 v[26:29],a[0:3],v[34:37],0
s_mov_b32 m0,s57
R 66,2,8192
v_fma_f32 v18,-v98,v106,v18
M 26,a[8:11],38
L 5
buffer_load_dwordx4 v[102:105],v11,s[36:39],s73 offen offset:0 sc1
M 26,a[16:19],42
s_mov_b32 s67,s65
R 70,2,9216
v_fma_f32 v19,-v98,v107,v19
M 26,a[24:27],46
s_add_u32 s73,s73,s64
v_fma_f32 v20,-v98,v108,v20
v_fma_f32 v21,-v98,v109,v21
M 30,a[20:23],42
s_add_u32 s54,s54,s46
R 74,2,10240
v_mul_f32 v114,v18,v18
M 30,a[28:31],46
L 5
D 0,55,0
M 30,a[36:39],50
s_add_u32 s65,s65,s62
R 78,2,11264
v_mul_f32 v115,v19,v19
M 30,a[44:47],54
v_mul_f32 v116,v20,v20
v_mul_f32 v117,v21,v21
M 26,a[32:35],50
R 82,2,12288
v_fma_f32 v114,v100,v114,v99
M 26,a[40:43],54
L 5
v_fma_f32 v115,v100,v115,v99
v_fma_f32 v116,v100,v116,v99
M 26,a[48:51],58
R 86,2,13312
v_fma_f32 v117,v100,v117,v99
M 26,a[56:59],62
D 0,55,1024
M 30,a[52:55],58
R 90,2,14336
v_mul_f32 v114,v18,v114
M 30,a[60:63],62
L 5
v_exp_f32 v114,v114
M 30,a[68:71],66
R 94,2,15360
v_mul_f32 v115,v19,v115
M 30,a[76:79],70
v_exp_f32 v115,v115
M 26,a[64:67],66
R 34,2,16384
v_mul_f32 v116,v20,v116
M 26,a[72:75],70
L 5
v_exp_f32 v116,v116
M 26,a[80:83],74
R 38,2,17408
v_mul_f32 v117,v21,v117
M 26,a[88:91],78
D 0,55,2048
M 30,a[84:87],74
R 42,2,18432
v_fma_f32 v114,v114,v101,v101
M 30,a[92:95],78
L 5
v_exp_f32 v117,v117
M 30,a[100:103],82
R 46,2,19456
v_fma_f32 v115,v115,v101,v101
M 30,a[108:111],86
v_rcp_f32 v114,v114
M 26,a[96:99],82
R 50,2,20480
v_fma_f32 v116,v116,v101,v101
M 26,a[104:107],86
L 5
D 0,55,3072
M 26,a[112:115],90
s_add_u32 m0,s57,4096
R 54,2,21504
v_fma_f32 v117,v117,v101,v101
M 26,a[120:123],94
s_add_u32 s57,s57,32768
v_rcp_f32 v115,v115
M 30,a[116:119],90
s_add_u32 s55,s55,s60
R 58,2,22528
v_mul_f32 v18,v18,v114
M 30,a[124:127],94
L 5
v_rcp_f32 v116,v116
M 30,a[132:135],34
s_and_b32 s57,s57,131071
R 62,2,23552
v_mul_f32 v19,v19,v115
M 30,a[140:143],38
v_rcp_f32 v117,v117
M 26,a[128:131],34
R 66,2,24576
v_mul_f32 v20,v20,v116
M 26,a[136:139],38
L 5
D 0,56,0
M 26,a[144:147],42
R 70,2,25600
v_mul_f32 v21,v21,v117
M 26,a[152:155],46
v_cvt_pk_bf16_f32 v122,v18,v19
v_cvt_pk_bf16_f32 v123,v20,v21
M 30,a[148:151],42
R 74,2,26624
v_fma_f32 v22,-v98,v110,v22
M 30,a[156:159],46
L 5
v_fma_f32 v23,-v98,v111,v23
v_fma_f32 v24,-v98,v112,v24
v_fma_f32 v25,-v98,v113,v25
M 30,a[164:167],50
R 78,2,27648
v_mul_f32 v118,v22,v22
M 30,a[172:175],54
D 0,56,1024
M 26,a[160:163],50
R 82,2,28672
v_mul_f32 v119,v23,v23
M 26,a[168:171],54
L 5
v_mul_f32 v120,v24,v24
v_mul_f32 v121,v25,v25
M 26,a[176:179],58
R 86,2,29696
v_fma_f32 v118,v100,v118,v99
M 26,a[184:187],62
v_fma_f32 v119,v100,v119,v99
v_fma_f32 v120,v100,v120,v99
M 30,a[180:183],58
R 90,2,30720
v_fma_f32 v121,v100,v121,v99
M 30,a[188:191],62
L 5
v_mul_f32 v118,v22,v118
v_mul_f32 v119,v23,v119
M 30,a[196:199],66
s_waitcnt vmcnt(6)
R 94,2,31744
v_mul_f32 v120,v24,v120
M 30,a[204:207],70
s_barrier
D 0,56,2048
M 26,a[192:195],66
R 34,1,0
ds_read_b32 v7,v6 offset:1024
M 26,a[200:203],70
L 6
buffer_store_dword v4,v3,s[36:39],s70 offen offset:0 sc0 sc1
M 26,a[208:211],74
s_mov_b32 s70,s69
R 38,1,1024
v_mul_f32 v121,v25,v121
M 26,a[216:219],78
s_mov_b32 s69,s68
v_exp_f32 v118,v118
M 30,a[212:215],74
s_add_u32 s68,s68,s63
R 42,1,2048
v_exp_f32 v119,v119
v_exp_f32 v120,v120
v_exp_f32 v121,v121
M 30,a[220:223],78
L 6
v_fma_f32 v118,v118,v101,v101
v_fma_f32 v119,v119,v101,v101
v_fma_f32 v120,v120,v101,v101
v_fma_f32 v121,v121,v101,v101
v_rcp_f32 v118,v118
v_rcp_f32 v119,v119
v_rcp_f32 v120,v120
v_rcp_f32 v121,v121
M 30,a[228:231],82
L 2
R 46,1,3072
v_mul_f32 v22,v22,v118
v_mul_f32 v23,v23,v119
v_mul_f32 v24,v24,v120
v_mul_f32 v25,v25,v121
v_cvt_pk_bf16_f32 v124,v22,v23
v_cvt_pk_bf16_f32 v125,v24,v25
M 30,a[236:239],86
buffer_store_dwordx4 v[122:125],v15,s[36:39],s66 offen offset:0 sc0 sc1
v_xor_b32 v2,65536,v2
M 26,a[224:227],82
R 50,1,4096
v_cmp_eq_u32 vcc,s53,v7
M 26,a[232:235],86
s_nop 0
s_cmp_eq_u32 vcc_lo,-1
M 26,a[240:243],90
R 54,1,5120
M 26,a[248:251],94
buffer_store_dwordx4 v[122:125],v16,s[36:39],s66 offen offset:0 sc0 sc1
M 30,a[244:247],90
R 58,1,6144
M 30,a[252:255],94
L 5
D 0,56,3072
s_cbranch_scc0 Lg1_N0_%=
Lg1_S0_%=:
v_mfma_f32_16x16x32_bf16 v[22:25],a[4:7],v[34:37],0
s_add_u32 s58,s58,s61
s_add_u32 m0,s59,0
R 62,1,7168
M 22,a[12:15],38
s_add_u32 s56,s55,4096
buffer_load_dword v8,s[36:39],s58 offen offset:0 sc1 lds
v_mfma_f32_16x16x32_bf16 v[18:21],a[0:3],v[34:37],0
s_mov_b32 m0,s57
R 66,1,8192
v_fma_f32 v26,-v102,v106,v26
M 18,a[8:11],38
L 5
buffer_load_dwordx4 v[98:101],v11,s[36:39],s73 offen offset:0 sc1
M 18,a[16:19],42
s_mov_b32 s66,s65
R 70,1,9216
v_fma_f32 v27,-v102,v107,v27
M 18,a[24:27],46
s_add_u32 s73,s73,s64
v_fma_f32 v28,-v102,v108,v28
v_fma_f32 v29,-v102,v109,v29
M 22,a[20:23],42
s_add_u32 s54,s54,s46
R 74,1,10240
v_mul_f32 v114,v26,v26
M 22,a[28:31],46
L 5
D 0,55,0
M 22,a[36:39],50
s_add_u32 s65,s65,s62
R 78,1,11264
v_mul_f32 v115,v27,v27
M 22,a[44:47],54
v_mul_f32 v116,v28,v28
v_mul_f32 v117,v29,v29
M 18,a[32:35],50
R 82,1,12288
v_fma_f32 v114,v104,v114,v103
M 18,a[40:43],54
L 5
v_fma_f32 v115,v104,v115,v103
v_fma_f32 v116,v104,v116,v103
M 18,a[48:51],58
R 86,1,13312
v_fma_f32 v117,v104,v117,v103
M 18,a[56:59],62
D 0,55,1024
M 22,a[52:55],58
R 90,1,14336
v_mul_f32 v114,v26,v114
M 22,a[60:63],62
L 5
v_exp_f32 v114,v114
M 22,a[68:71],66
R 94,1,15360
v_mul_f32 v115,v27,v115
M 22,a[76:79],70
v_exp_f32 v115,v115
M 18,a[64:67],66
R 34,1,16384
v_mul_f32 v116,v28,v116
M 18,a[72:75],70
L 5
v_exp_f32 v116,v116
M 18,a[80:83],74
R 38,1,17408
v_mul_f32 v117,v29,v117
M 18,a[88:91],78
D 0,55,2048
M 22,a[84:87],74
R 42,1,18432
v_fma_f32 v114,v114,v105,v105
M 22,a[92:95],78
L 5
v_exp_f32 v117,v117
M 22,a[100:103],82
R 46,1,19456
v_fma_f32 v115,v115,v105,v105
M 22,a[108:111],86
v_rcp_f32 v114,v114
M 18,a[96:99],82
R 50,1,20480
v_fma_f32 v116,v116,v105,v105
M 18,a[104:107],86
L 5
D 0,55,3072
M 18,a[112:115],90
s_add_u32 m0,s57,4096
R 54,1,21504
v_fma_f32 v117,v117,v105,v105
M 18,a[120:123],94
s_add_u32 s57,s57,32768
v_rcp_f32 v115,v115
M 22,a[116:119],90
s_add_u32 s55,s55,s60
R 58,1,22528
v_mul_f32 v26,v26,v114
M 22,a[124:127],94
L 5
v_rcp_f32 v116,v116
M 22,a[132:135],34
s_and_b32 s57,s57,131071
R 62,1,23552
v_mul_f32 v27,v27,v115
M 22,a[140:143],38
v_rcp_f32 v117,v117
M 18,a[128:131],34
R 66,1,24576
v_mul_f32 v28,v28,v116
M 18,a[136:139],38
L 5
D 0,56,0
M 18,a[144:147],42
R 70,1,25600
v_mul_f32 v29,v29,v117
M 18,a[152:155],46
v_cvt_pk_bf16_f32 v122,v26,v27
v_cvt_pk_bf16_f32 v123,v28,v29
M 22,a[148:151],42
R 74,1,26624
v_fma_f32 v30,-v102,v110,v30
M 22,a[156:159],46
L 5
v_fma_f32 v31,-v102,v111,v31
v_fma_f32 v32,-v102,v112,v32
v_fma_f32 v33,-v102,v113,v33
M 22,a[164:167],50
R 78,1,27648
v_mul_f32 v118,v30,v30
M 22,a[172:175],54
D 0,56,1024
M 18,a[160:163],50
R 82,1,28672
v_mul_f32 v119,v31,v31
M 18,a[168:171],54
L 5
v_mul_f32 v120,v32,v32
v_mul_f32 v121,v33,v33
M 18,a[176:179],58
R 86,1,29696
v_fma_f32 v118,v104,v118,v103
M 18,a[184:187],62
v_fma_f32 v119,v104,v119,v103
v_fma_f32 v120,v104,v120,v103
M 22,a[180:183],58
R 90,1,30720
v_fma_f32 v121,v104,v121,v103
M 22,a[188:191],62
L 5
v_mul_f32 v118,v30,v118
v_mul_f32 v119,v31,v119
M 22,a[196:199],66
s_waitcnt vmcnt(6)
R 94,1,31744
v_mul_f32 v120,v32,v120
M 22,a[204:207],70
s_barrier
D 0,56,2048
M 18,a[192:195],66
R 34,2,0
ds_read_b32 v7,v6 offset:0
M 18,a[200:203],70
L 6
buffer_store_dword v4,v3,s[36:39],s70 offen offset:0 sc0 sc1
M 18,a[208:211],74
s_mov_b32 s70,s69
R 38,2,1024
v_mul_f32 v121,v33,v121
M 18,a[216:219],78
s_mov_b32 s69,s68
v_exp_f32 v118,v118
M 22,a[212:215],74
s_add_u32 s68,s68,s63
R 42,2,2048
v_exp_f32 v119,v119
v_exp_f32 v120,v120
v_exp_f32 v121,v121
M 22,a[220:223],78
L 6
v_fma_f32 v118,v118,v105,v105
v_fma_f32 v119,v119,v105,v105
v_fma_f32 v120,v120,v105,v105
v_fma_f32 v121,v121,v105,v105
v_rcp_f32 v118,v118
v_rcp_f32 v119,v119
v_rcp_f32 v120,v120
v_rcp_f32 v121,v121
M 22,a[228:231],82
L 2
R 46,2,3072
v_mul_f32 v30,v30,v118
v_mul_f32 v31,v31,v119
v_mul_f32 v32,v32,v120
v_mul_f32 v33,v33,v121
v_cvt_pk_bf16_f32 v124,v30,v31
v_cvt_pk_bf16_f32 v125,v32,v33
M 22,a[236:239],86
buffer_store_dwordx4 v[122:125],v15,s[36:39],s67 offen offset:0 sc0 sc1
v_xor_b32 v1,65536,v1
M 18,a[224:227],82
R 50,2,4096
v_cmp_eq_u32 vcc,s53,v7
M 18,a[232:235],86
s_nop 0
s_cmp_eq_u32 vcc_lo,-1
M 18,a[240:243],90
R 54,2,5120
M 18,a[248:251],94
buffer_store_dwordx4 v[122:125],v16,s[36:39],s67 offen offset:0 sc0 sc1
M 22,a[244:247],90
R 58,2,6144
M 22,a[252:255],94
L 5
D 0,56,3072
s_cbranch_scc1 Lg1_S1_%=
Lg1_N1_%=:
v_mfma_f32_16x16x32_bf16 v[30:33],a[4:7],v[34:37],0
s_add_u32 m0,s59,1024
s_mov_b32 s67,s65
R 62,2,7168
M 30,a[12:15],38
s_add_u32 s65,s65,s62
buffer_load_dword v5,s[36:39],s58 offen offset:0 sc1 lds
v_mfma_f32_16x16x32_bf16 v[26:29],a[0:3],v[34:37],0
R 66,2,8192
v_fma_f32 v18,-v98,v106,v18
M 26,a[8:11],38
L 5
buffer_load_dwordx4 v[102:105],v11,s[36:39],s73 offen offset:0 sc1
M 26,a[16:19],42
s_add_u32 s73,s73,s64
R 70,2,9216
v_fma_f32 v19,-v98,v107,v19
M 26,a[24:27],46
v_fma_f32 v20,-v98,v108,v20
v_fma_f32 v21,-v98,v109,v21
M 30,a[20:23],42
R 74,2,10240
v_mul_f32 v114,v18,v18
M 30,a[28:31],46
L 5
v_mul_f32 v115,v19,v19
v_mul_f32 v116,v20,v20
M 30,a[36:39],50
R 78,2,11264
v_mul_f32 v117,v21,v21
M 30,a[44:47],54
v_fma_f32 v114,v100,v114,v99
v_fma_f32 v115,v100,v115,v99
M 26,a[32:35],50
R 82,2,12288
v_fma_f32 v116,v100,v116,v99
M 26,a[40:43],54
L 5
v_fma_f32 v117,v100,v117,v99
v_mul_f32 v114,v18,v114
M 26,a[48:51],58
R 86,2,13312
v_mul_f32 v115,v19,v115
M 26,a[56:59],62
v_exp_f32 v114,v114
M 30,a[52:55],58
R 90,2,14336
v_mul_f32 v116,v20,v116
M 30,a[60:63],62
L 5
v_exp_f32 v115,v115
M 30,a[68:71],66
R 94,2,15360
v_mul_f32 v117,v21,v117
M 30,a[76:79],70
v_exp_f32 v116,v116
M 26,a[64:67],66
R 34,2,16384
v_fma_f32 v114,v114,v101,v101
M 26,a[72:75],70
L 5
v_exp_f32 v117,v117
M 26,a[80:83],74
)"
R"(R 38,2,17408
v_fma_f32 v115,v115,v101,v101
M 26,a[88:91],78
v_rcp_f32 v114,v114
M 30,a[84:87],74
R 42,2,18432
v_fma_f32 v116,v116,v101,v101
M 30,a[92:95],78
L 5
v_rcp_f32 v115,v115
M 30,a[100:103],82
R 46,2,19456
v_fma_f32 v117,v117,v101,v101
M 30,a[108:111],86
v_rcp_f32 v116,v116
M 26,a[96:99],82
R 50,2,20480
v_mul_f32 v18,v18,v114
M 26,a[104:107],86
L 5
v_rcp_f32 v117,v117
M 26,a[112:115],90
R 54,2,21504
v_mul_f32 v19,v19,v115
M 26,a[120:123],94
v_mul_f32 v20,v20,v116
v_mul_f32 v21,v21,v117
M 30,a[116:119],90
R 58,2,22528
v_cvt_pk_bf16_f32 v122,v18,v19
M 30,a[124:127],94
L 5
v_cvt_pk_bf16_f32 v123,v20,v21
v_fma_f32 v22,-v98,v110,v22
M 30,a[132:135],34
R 62,2,23552
v_fma_f32 v23,-v98,v111,v23
M 30,a[140:143],38
v_fma_f32 v24,-v98,v112,v24
v_fma_f32 v25,-v98,v113,v25
M 26,a[128:131],34
R 66,2,24576
v_mul_f32 v118,v22,v22
M 26,a[136:139],38
L 5
v_mul_f32 v119,v23,v23
v_mul_f32 v120,v24,v24
M 26,a[144:147],42
R 70,2,25600
v_mul_f32 v121,v25,v25
M 26,a[152:155],46
v_fma_f32 v118,v100,v118,v99
v_fma_f32 v119,v100,v119,v99
M 30,a[148:151],42
R 74,2,26624
v_fma_f32 v120,v100,v120,v99
M 30,a[156:159],46
L 5
v_fma_f32 v121,v100,v121,v99
v_mul_f32 v118,v22,v118
v_mul_f32 v119,v23,v119
M 30,a[164:167],50
R 78,2,27648
v_mul_f32 v120,v24,v120
M 30,a[172:175],54
v_exp_f32 v118,v118
M 26,a[160:163],50
R 82,2,28672
v_mul_f32 v121,v25,v121
M 26,a[168:171],54
L 5
v_exp_f32 v119,v119
M 26,a[176:179],58
R 86,2,29696
v_fma_f32 v118,v118,v101,v101
M 26,a[184:187],62
v_exp_f32 v120,v120
M 30,a[180:183],58
R 90,2,30720
v_fma_f32 v119,v119,v101,v101
M 30,a[188:191],62
L 5
v_exp_f32 v121,v121
M 30,a[196:199],66
s_waitcnt vmcnt(0)
R 94,2,31744
v_fma_f32 v120,v120,v101,v101
M 30,a[204:207],70
s_barrier
buffer_store_dword v4,v3,s[36:39],s70 offen offset:0 sc0 sc1
M 26,a[192:195],66
s_mov_b32 s70,s69
R 34,1,0
ds_read_b32 v7,v6 offset:1024
M 26,a[200:203],70
L 6
v_rcp_f32 v118,v118
M 26,a[208:211],74
s_mov_b32 s69,s68
R 38,1,1024
v_fma_f32 v121,v121,v101,v101
M 26,a[216:219],78
s_add_u32 s68,s68,s63
v_rcp_f32 v119,v119
M 30,a[212:215],74
R 42,1,2048
v_mul_f32 v22,v22,v118
M 30,a[220:223],78
L 6
v_rcp_f32 v120,v120
v_rcp_f32 v121,v121
M 30,a[228:231],82
L 2
R 46,1,3072
v_mul_f32 v23,v23,v119
v_mul_f32 v24,v24,v120
v_mul_f32 v25,v25,v121
v_cvt_pk_bf16_f32 v124,v22,v23
v_cvt_pk_bf16_f32 v125,v24,v25
M 30,a[236:239],86
buffer_store_dwordx4 v[122:125],v15,s[36:39],s66 offen offset:0 sc0 sc1
v_xor_b32 v2,65536,v2
M 26,a[224:227],82
R 50,1,4096
v_cmp_eq_u32 vcc,s53,v7
M 26,a[232:235],86
s_nop 0
s_cmp_eq_u64 vcc,-1
M 26,a[240:243],90
R 54,1,5120
M 26,a[248:251],94
buffer_store_dwordx4 v[122:125],v16,s[36:39],s66 offen offset:0 sc0 sc1
M 30,a[244:247],90
R 58,1,6144
M 30,a[252:255],94
L 5
s_cbranch_scc1 Lg1_L20_%=
s_cmp_eq_u32 vcc_lo,-1
s_cbranch_scc1 Lg1_L10_%=
Lg1_X0_%=:
v_mfma_f32_16x16x32_bf16 v[22:25],a[4:7],v[34:37],0
s_add_u32 m0,s59,0
s_mov_b32 s66,s65
R 62,1,7168
M 22,a[12:15],38
s_add_u32 s65,s65,s62
buffer_load_dword v5,s[36:39],s58 offen offset:0 sc1 lds
v_mfma_f32_16x16x32_bf16 v[18:21],a[0:3],v[34:37],0
R 66,1,8192
v_fma_f32 v26,-v102,v106,v26
M 18,a[8:11],38
L 5
buffer_load_dwordx4 v[98:101],v11,s[36:39],s73 offen offset:0 sc1
M 18,a[16:19],42
s_add_u32 s73,s73,s64
R 70,1,9216
v_fma_f32 v27,-v102,v107,v27
M 18,a[24:27],46
v_fma_f32 v28,-v102,v108,v28
v_fma_f32 v29,-v102,v109,v29
M 22,a[20:23],42
R 74,1,10240
v_mul_f32 v114,v26,v26
M 22,a[28:31],46
L 5
v_mul_f32 v115,v27,v27
v_mul_f32 v116,v28,v28
M 22,a[36:39],50
R 78,1,11264
v_mul_f32 v117,v29,v29
M 22,a[44:47],54
v_fma_f32 v114,v104,v114,v103
v_fma_f32 v115,v104,v115,v103
M 18,a[32:35],50
R 82,1,12288
v_fma_f32 v116,v104,v116,v103
M 18,a[40:43],54
L 5
v_fma_f32 v117,v104,v117,v103
v_mul_f32 v114,v26,v114
M 18,a[48:51],58
R 86,1,13312
v_mul_f32 v115,v27,v115
M 18,a[56:59],62
v_exp_f32 v114,v114
M 22,a[52:55],58
R 90,1,14336
v_mul_f32 v116,v28,v116
M 22,a[60:63],62
L 5
v_exp_f32 v115,v115
M 22,a[68:71],66
R 94,1,15360
v_mul_f32 v117,v29,v117
M 22,a[76:79],70
v_exp_f32 v116,v116
M 18,a[64:67],66
R 34,1,16384
v_fma_f32 v114,v114,v105,v105
M 18,a[72:75],70
L 5
v_exp_f32 v117,v117
M 18,a[80:83],74
R 38,1,17408
v_fma_f32 v115,v115,v105,v105
M 18,a[88:91],78
v_rcp_f32 v114,v114
M 22,a[84:87],74
R 42,1,18432
v_fma_f32 v116,v116,v105,v105
M 22,a[92:95],78
L 5
v_rcp_f32 v115,v115
M 22,a[100:103],82
R 46,1,19456
v_fma_f32 v117,v117,v105,v105
M 22,a[108:111],86
v_rcp_f32 v116,v116
M 18,a[96:99],82
R 50,1,20480
v_mul_f32 v26,v26,v114
M 18,a[104:107],86
L 5
v_rcp_f32 v117,v117
M 18,a[112:115],90
R 54,1,21504
v_mul_f32 v27,v27,v115
M 18,a[120:123],94
v_mul_f32 v28,v28,v116
v_mul_f32 v29,v29,v117
M 22,a[116:119],90
R 58,1,22528
v_cvt_pk_bf16_f32 v122,v26,v27
M 22,a[124:127],94
L 5
v_cvt_pk_bf16_f32 v123,v28,v29
v_fma_f32 v30,-v102,v110,v30
M 22,a[132:135],34
R 62,1,23552
v_fma_f32 v31,-v102,v111,v31
M 22,a[140:143],38
v_fma_f32 v32,-v102,v112,v32
v_fma_f32 v33,-v102,v113,v33
M 18,a[128:131],34
R 66,1,24576
v_mul_f32 v118,v30,v30
M 18,a[136:139],38
L 5
v_mul_f32 v119,v31,v31
v_mul_f32 v120,v32,v32
M 18,a[144:147],42
R 70,1,25600
v_mul_f32 v121,v33,v33
M 18,a[152:155],46
v_fma_f32 v118,v104,v118,v103
v_fma_f32 v119,v104,v119,v103
M 22,a[148:151],42
R 74,1,26624
v_fma_f32 v120,v104,v120,v103
M 22,a[156:159],46
L 5
v_fma_f32 v121,v104,v121,v103
v_mul_f32 v118,v30,v118
M 22,a[164:167],50
R 78,1,27648
v_mul_f32 v119,v31,v119
M 22,a[172:175],54
v_exp_f32 v118,v118
M 18,a[160:163],50
R 82,1,28672
v_mul_f32 v120,v32,v120
M 18,a[168:171],54
L 5
v_exp_f32 v119,v119
M 18,a[176:179],58
R 86,1,29696
v_mul_f32 v121,v33,v121
M 18,a[184:187],62
v_exp_f32 v120,v120
M 22,a[180:183],58
R 90,1,30720
v_fma_f32 v118,v118,v105,v105
M 22,a[188:191],62
L 5
v_exp_f32 v121,v121
M 22,a[196:199],66
s_waitcnt vmcnt(0)
R 94,1,31744
v_fma_f32 v119,v119,v105,v105
M 22,a[204:207],70
s_barrier
buffer_store_dword v4,v3,s[36:39],s70 offen offset:0 sc0 sc1
M 18,a[192:195],66
s_mov_b32 s70,s69
ds_read_b32 v7,v6 offset:0
v_fma_f32 v120,v120,v105,v105
M 18,a[200:203],70
L 5
v_rcp_f32 v118,v118
M 18,a[208:211],74
s_mov_b32 s69,s68
v_fma_f32 v121,v121,v105,v105
v_mul_f32 v30,v30,v118
M 18,a[216:219],78
s_add_u32 s68,s68,s63
v_rcp_f32 v119,v119
M 22,a[212:215],74
v_rcp_f32 v120,v120
M 22,a[220:223],78
L 3
v_rcp_f32 v121,v121
M 22,a[228:231],82
L 0
v_mul_f32 v31,v31,v119
v_mul_f32 v32,v32,v120
v_mul_f32 v33,v33,v121
v_cvt_pk_bf16_f32 v124,v30,v31
v_cvt_pk_bf16_f32 v125,v32,v33
M 22,a[236:239],86
buffer_store_dwordx4 v[122:125],v15,s[36:39],s67 offen offset:0 sc0 sc1
v_xor_b32 v1,65536,v1
M 18,a[224:227],82
v_cmp_eq_u32 vcc,s53,v7
M 18,a[232:235],86
s_nop 0
M 18,a[240:243],90
buffer_store_dwordx4 v[122:125],v16,s[36:39],s67 offen offset:0 sc0 sc1
M 18,a[248:251],94
M 22,a[244:247],90
M 22,a[252:255],94
s_mov_b32 s80,1
s_cmp_eq_u32 vcc_lo,-1
s_cbranch_scc0 Lg1_xn0_%=
s_mov_b32 m0,s57
s_add_u32 s56,s55,4096
D 0,55,0
v_fma_f32 v18,-v98,v106,v18
v_fma_f32 v19,-v98,v107,v19
v_fma_f32 v20,-v98,v108,v20
v_fma_f32 v21,-v98,v109,v21
v_mul_f32 v114,v18,v18
v_mul_f32 v115,v19,v19
v_mul_f32 v116,v20,v20
D 0,55,1024
v_mul_f32 v117,v21,v21
v_fma_f32 v114,v100,v114,v99
v_fma_f32 v115,v100,v115,v99
v_fma_f32 v116,v100,v116,v99
v_fma_f32 v117,v100,v117,v99
v_mul_f32 v114,v18,v114
v_mul_f32 v115,v19,v115
D 0,55,2048
v_mul_f32 v116,v20,v116
v_mul_f32 v117,v21,v117
v_exp_f32 v114,v114
v_exp_f32 v115,v115
v_exp_f32 v116,v116
v_exp_f32 v117,v117
v_fma_f32 v114,v114,v101,v101
D 0,55,3072
v_fma_f32 v115,v115,v101,v101
v_fma_f32 v116,v116,v101,v101
v_fma_f32 v117,v117,v101,v101
v_rcp_f32 v114,v114
v_rcp_f32 v115,v115
v_rcp_f32 v116,v116
v_rcp_f32 v117,v117
s_add_u32 m0,s57,4096
s_nop 0
D 0,56,0
v_mul_f32 v18,v18,v114
v_mul_f32 v19,v19,v115
v_mul_f32 v20,v20,v116
v_mul_f32 v21,v21,v117
v_cvt_pk_bf16_f32 v122,v18,v19
v_cvt_pk_bf16_f32 v123,v20,v21
v_fma_f32 v22,-v98,v110,v22
D 0,56,1024
v_fma_f32 v23,-v98,v111,v23
v_fma_f32 v24,-v98,v112,v24
v_fma_f32 v25,-v98,v113,v25
v_mul_f32 v118,v22,v22
v_mul_f32 v119,v23,v23
v_mul_f32 v120,v24,v24
v_mul_f32 v121,v25,v25
D 0,56,2048
v_fma_f32 v118,v100,v118,v99
v_fma_f32 v119,v100,v119,v99
v_fma_f32 v120,v100,v120,v99
v_fma_f32 v121,v100,v121,v99
v_mul_f32 v118,v22,v118
v_mul_f32 v119,v23,v119
v_mul_f32 v120,v24,v120
D 0,56,3072
v_mul_f32 v121,v25,v121
v_exp_f32 v118,v118
v_exp_f32 v119,v119
v_exp_f32 v120,v120
v_exp_f32 v121,v121
v_fma_f32 v118,v118,v101,v101
v_fma_f32 v119,v119,v101,v101
s_add_u32 s55,s55,s60
s_add_u32 s57,s57,32768
s_and_b32 s57,s57,131071
s_add_u32 s54,s54,s46
v_fma_f32 v120,v120,v101,v101
v_fma_f32 v121,v121,v101,v101
v_rcp_f32 v118,v118
v_rcp_f32 v119,v119
v_rcp_f32 v120,v120
v_rcp_f32 v121,v121
v_mul_f32 v22,v22,v118
v_mul_f32 v23,v23,v119
v_mul_f32 v24,v24,v120
v_mul_f32 v25,v25,v121
v_cvt_pk_bf16_f32 v124,v22,v23
v_cvt_pk_bf16_f32 v125,v24,v25
buffer_store_dwordx4 v[122:125],v15,s[36:39],s66 offen offset:0 sc0 sc1
buffer_store_dwordx4 v[122:125],v16,s[36:39],s66 offen offset:0 sc0 sc1
s_mov_b32 s72,s69
s_mov_b32 s69,0x7fff0000
s_mov_b32 s66,0x7fff0000
s_mov_b32 s76,0x7fff0000
s_branch Lg1_gox_%=
Lg1_xn0_%=:
s_cmp_ge_u32 s54,256
s_cbranch_scc1 Lg1_fin0_%=
s_add_u32 m0,s59,4096
s_nop 0
buffer_load_dword v5,s[36:39],s58 offen offset:0 sc1 lds
buffer_store_dword v4,v3,s[36:39],s71 offen offset:0 sc0 sc1
s_add_u32 m0,s59,5120
s_nop 0
buffer_load_dword v5,s[36:39],s58 offen offset:0 sc1 lds
buffer_store_dword v4,v3,s[36:39],s71 offen offset:0 sc0 sc1
s_mov_b32 s83,0
v_fma_f32 v18,-v98,v106,v18
v_fma_f32 v19,-v98,v107,v19
v_fma_f32 v20,-v98,v108,v20
v_fma_f32 v21,-v98,v109,v21
v_mul_f32 v114,v18,v18
v_mul_f32 v115,v19,v19
v_mul_f32 v116,v20,v20
v_mul_f32 v117,v21,v21
v_fma_f32 v114,v100,v114,v99
v_fma_f32 v115,v100,v115,v99
v_fma_f32 v116,v100,v116,v99
v_fma_f32 v117,v100,v117,v99
v_mul_f32 v114,v18,v114
v_mul_f32 v115,v19,v115
v_mul_f32 v116,v20,v116
v_mul_f32 v117,v21,v117
v_exp_f32 v114,v114
v_exp_f32 v115,v115
v_exp_f32 v116,v116
v_exp_f32 v117,v117
v_fma_f32 v114,v114,v101,v101
v_fma_f32 v115,v115,v101,v101
v_fma_f32 v116,v116,v101,v101
v_fma_f32 v117,v117,v101,v101
v_rcp_f32 v114,v114
v_rcp_f32 v115,v115
v_rcp_f32 v116,v116
v_rcp_f32 v117,v117
v_mul_f32 v18,v18,v114
v_mul_f32 v19,v19,v115
v_mul_f32 v20,v20,v116
v_mul_f32 v21,v21,v117
v_cvt_pk_bf16_f32 v122,v18,v19
v_cvt_pk_bf16_f32 v123,v20,v21
v_fma_f32 v22,-v98,v110,v22
v_fma_f32 v23,-v98,v111,v23
v_fma_f32 v24,-v98,v112,v24
v_fma_f32 v25,-v98,v113,v25
v_mul_f32 v118,v22,v22
v_mul_f32 v119,v23,v23
v_mul_f32 v120,v24,v24
v_mul_f32 v121,v25,v25
v_fma_f32 v118,v100,v118,v99
v_fma_f32 v119,v100,v119,v99
v_fma_f32 v120,v100,v120,v99
v_fma_f32 v121,v100,v121,v99
v_mul_f32 v118,v22,v118
v_mul_f32 v119,v23,v119
v_mul_f32 v120,v24,v120
v_mul_f32 v121,v25,v121
v_exp_f32 v118,v118
v_exp_f32 v119,v119
v_exp_f32 v120,v120
v_exp_f32 v121,v121
v_fma_f32 v118,v118,v101,v101
v_fma_f32 v119,v119,v101,v101
v_fma_f32 v120,v120,v101,v101
v_fma_f32 v121,v121,v101,v101
v_rcp_f32 v118,v118
v_rcp_f32 v119,v119
v_rcp_f32 v120,v120
v_rcp_f32 v121,v121
v_mul_f32 v22,v22,v118
v_mul_f32 v23,v23,v119
)"
R"(v_mul_f32 v24,v24,v120
v_mul_f32 v25,v25,v121
v_cvt_pk_bf16_f32 v124,v22,v23
v_cvt_pk_bf16_f32 v125,v24,v25
buffer_store_dwordx4 v[122:125],v15,s[36:39],s66 offen offset:0 sc0 sc1
buffer_store_dwordx4 v[122:125],v16,s[36:39],s66 offen offset:0 sc0 sc1
s_mov_b32 s72,s69
s_mov_b32 s69,0x7fff0000
s_mov_b32 s66,0x7fff0000
s_mov_b32 s76,0x7fff0000
s_branch Lg1_poll_%=
Lg1_fin0_%=:
v_fma_f32 v18,-v98,v106,v18
v_fma_f32 v19,-v98,v107,v19
v_fma_f32 v20,-v98,v108,v20
v_fma_f32 v21,-v98,v109,v21
v_mul_f32 v114,v18,v18
v_mul_f32 v115,v19,v19
v_mul_f32 v116,v20,v20
v_mul_f32 v117,v21,v21
v_fma_f32 v114,v100,v114,v99
v_fma_f32 v115,v100,v115,v99
v_fma_f32 v116,v100,v116,v99
v_fma_f32 v117,v100,v117,v99
v_mul_f32 v114,v18,v114
v_mul_f32 v115,v19,v115
v_mul_f32 v116,v20,v116
v_mul_f32 v117,v21,v117
v_exp_f32 v114,v114
v_exp_f32 v115,v115
v_exp_f32 v116,v116
v_exp_f32 v117,v117
v_fma_f32 v114,v114,v101,v101
v_fma_f32 v115,v115,v101,v101
v_fma_f32 v116,v116,v101,v101
v_fma_f32 v117,v117,v101,v101
v_rcp_f32 v114,v114
v_rcp_f32 v115,v115
v_rcp_f32 v116,v116
v_rcp_f32 v117,v117
v_mul_f32 v18,v18,v114
v_mul_f32 v19,v19,v115
v_mul_f32 v20,v20,v116
v_mul_f32 v21,v21,v117
v_cvt_pk_bf16_f32 v122,v18,v19
v_cvt_pk_bf16_f32 v123,v20,v21
v_fma_f32 v22,-v98,v110,v22
v_fma_f32 v23,-v98,v111,v23
v_fma_f32 v24,-v98,v112,v24
v_fma_f32 v25,-v98,v113,v25
v_mul_f32 v118,v22,v22
v_mul_f32 v119,v23,v23
v_mul_f32 v120,v24,v24
v_mul_f32 v121,v25,v25
v_fma_f32 v118,v100,v118,v99
v_fma_f32 v119,v100,v119,v99
v_fma_f32 v120,v100,v120,v99
v_fma_f32 v121,v100,v121,v99
v_mul_f32 v118,v22,v118
v_mul_f32 v119,v23,v119
v_mul_f32 v120,v24,v120
v_mul_f32 v121,v25,v121
v_exp_f32 v118,v118
v_exp_f32 v119,v119
v_exp_f32 v120,v120
v_exp_f32 v121,v121
v_fma_f32 v118,v118,v101,v101
v_fma_f32 v119,v119,v101,v101
v_fma_f32 v120,v120,v101,v101
v_fma_f32 v121,v121,v101,v101
v_rcp_f32 v118,v118
v_rcp_f32 v119,v119
v_rcp_f32 v120,v120
v_rcp_f32 v121,v121
v_mul_f32 v22,v22,v118
v_mul_f32 v23,v23,v119
v_mul_f32 v24,v24,v120
v_mul_f32 v25,v25,v121
v_cvt_pk_bf16_f32 v124,v22,v23
v_cvt_pk_bf16_f32 v125,v24,v25
buffer_store_dwordx4 v[122:125],v15,s[36:39],s66 offen offset:0 sc0 sc1
buffer_store_dwordx4 v[122:125],v16,s[36:39],s66 offen offset:0 sc0 sc1
s_mov_b32 s72,s69
s_mov_b32 s69,0x7fff0000
s_mov_b32 s66,0x7fff0000
s_mov_b32 s76,0x7fff0000
s_branch Lg1_done_%=
Lg1_L21_%=:
v_mfma_f32_16x16x32_bf16 v[30:33],a[4:7],v[34:37],0
s_add_u32 s58,s58,s61
s_add_u32 s58,s58,s61
R 62,2,7168
M 30,a[12:15],38
s_add_u32 m0,s59,1024
buffer_load_dwordx4 v[102:105],v11,s[36:39],s73 offen offset:0 sc1
v_mfma_f32_16x16x32_bf16 v[26:29],a[0:3],v[34:37],0
s_add_u32 s56,s55,4096
R 66,2,8192
v_fma_f32 v18,-v98,v106,v18
M 26,a[8:11],38
L 5
buffer_load_dword v8,s[36:39],s58 offen offset:0 sc1 lds
M 26,a[16:19],42
s_mov_b32 m0,s57
R 70,2,9216
v_fma_f32 v19,-v98,v107,v19
M 26,a[24:27],46
s_add_u32 s54,s54,s46
v_fma_f32 v20,-v98,v108,v20
v_fma_f32 v21,-v98,v109,v21
M 30,a[20:23],42
s_mov_b32 s67,s65
R 74,2,10240
v_mul_f32 v114,v18,v18
M 30,a[28:31],46
L 5
D 0,55,0
M 30,a[36:39],50
s_add_u32 s73,s73,s64
R 78,2,11264
v_mul_f32 v115,v19,v19
M 30,a[44:47],54
s_add_u32 s54,s54,s46
v_mul_f32 v116,v20,v20
v_mul_f32 v117,v21,v21
M 26,a[32:35],50
s_add_u32 s65,s65,s62
R 82,2,12288
v_fma_f32 v114,v100,v114,v99
M 26,a[40:43],54
L 5
D 0,55,1024
M 26,a[48:51],58
R 86,2,13312
v_fma_f32 v115,v100,v115,v99
M 26,a[56:59],62
v_fma_f32 v116,v100,v116,v99
v_fma_f32 v117,v100,v117,v99
M 30,a[52:55],58
R 90,2,14336
v_mul_f32 v114,v18,v114
M 30,a[60:63],62
L 5
D 0,55,2048
M 30,a[68:71],66
R 94,2,15360
v_mul_f32 v115,v19,v115
M 30,a[76:79],70
v_exp_f32 v114,v114
M 26,a[64:67],66
R 34,2,16384
v_mul_f32 v116,v20,v116
M 26,a[72:75],70
L 5
D 0,55,3072
M 26,a[80:83],74
s_add_u32 m0,s57,4096
R 38,2,17408
v_mul_f32 v117,v21,v117
M 26,a[88:91],78
s_add_u32 s57,s57,32768
v_exp_f32 v115,v115
M 30,a[84:87],74
s_and_b32 s57,s57,131071
R 42,2,18432
v_fma_f32 v114,v114,v101,v101
M 30,a[92:95],78
L 5
D 0,56,0
M 30,a[100:103],82
s_add_u32 s55,s55,s60
R 46,2,19456
v_fma_f32 v115,v115,v101,v101
M 30,a[108:111],86
v_exp_f32 v116,v116
M 26,a[96:99],82
R 50,2,20480
v_fma_f32 v116,v116,v101,v101
M 26,a[104:107],86
L 5
D 0,56,1024
M 26,a[112:115],90
R 54,2,21504
v_fma_f32 v22,-v98,v110,v22
M 26,a[120:123],94
v_exp_f32 v117,v117
M 30,a[116:119],90
R 58,2,22528
v_fma_f32 v117,v117,v101,v101
M 30,a[124:127],94
L 5
D 0,56,2048
M 30,a[132:135],34
R 62,2,23552
v_fma_f32 v23,-v98,v111,v23
M 30,a[140:143],38
v_rcp_f32 v114,v114
M 26,a[128:131],34
R 66,2,24576
v_mul_f32 v18,v18,v114
M 26,a[136:139],38
L 5
D 0,56,3072
M 26,a[144:147],42
s_mov_b32 m0,s57
R 70,2,25600
v_fma_f32 v24,-v98,v112,v24
M 26,a[152:155],46
s_add_u32 s56,s55,4096
v_rcp_f32 v115,v115
M 30,a[148:151],42
R 74,2,26624
v_mul_f32 v19,v19,v115
M 30,a[156:159],46
L 5
D 0,55,0
M 30,a[164:167],50
R 78,2,27648
v_cvt_pk_bf16_f32 v122,v18,v19
M 30,a[172:175],54
D 0,55,1024
M 26,a[160:163],50
R 82,2,28672
v_fma_f32 v25,-v98,v113,v25
M 26,a[168:171],54
L 5
v_rcp_f32 v116,v116
M 26,a[176:179],58
R 86,2,29696
v_mul_f32 v20,v20,v116
M 26,a[184:187],62
D 0,55,2048
M 30,a[180:183],58
R 90,2,30720
v_mul_f32 v118,v22,v22
M 30,a[188:191],62
L 5
D 0,55,3072
M 30,a[196:199],66
s_waitcnt vmcnt(4)
R 94,2,31744
v_mul_f32 v119,v23,v23
M 30,a[204:207],70
s_barrier
buffer_store_dword v4,v3,s[36:39],s70 offen offset:0 sc0 sc1
M 26,a[192:195],66
s_add_u32 m0,s57,4096
R 34,1,0
ds_read_b32 v7,v6 offset:1024
M 26,a[200:203],70
L 6
D 0,56,0
M 26,a[208:211],74
s_mov_b32 s70,s69
R 38,1,1024
v_mul_f32 v120,v24,v24
M 26,a[216:219],78
s_add_u32 s57,s57,32768
D 0,56,1024
M 30,a[212:215],74
s_mov_b32 s69,s68
R 42,1,2048
v_mul_f32 v121,v25,v25
v_fma_f32 v118,v100,v118,v99
v_fma_f32 v119,v100,v119,v99
v_fma_f32 v120,v100,v120,v99
v_fma_f32 v121,v100,v121,v99
v_mul_f32 v118,v22,v118
v_mul_f32 v119,v23,v119
v_mul_f32 v120,v24,v120
v_mul_f32 v121,v25,v121
v_exp_f32 v118,v118
v_exp_f32 v119,v119
v_exp_f32 v120,v120
v_exp_f32 v121,v121
M 30,a[220:223],78
L 6
v_rcp_f32 v117,v117
v_fma_f32 v118,v118,v101,v101
v_fma_f32 v119,v119,v101,v101
v_fma_f32 v120,v120,v101,v101
v_fma_f32 v121,v121,v101,v101
v_rcp_f32 v118,v118
v_rcp_f32 v119,v119
v_rcp_f32 v120,v120
v_rcp_f32 v121,v121
M 30,a[228:231],82
L 2
R 46,1,3072
v_mul_f32 v21,v21,v117
v_cvt_pk_bf16_f32 v123,v20,v21
v_mul_f32 v22,v22,v118
v_mul_f32 v23,v23,v119
v_mul_f32 v24,v24,v120
v_mul_f32 v25,v25,v121
v_cvt_pk_bf16_f32 v124,v22,v23
v_cvt_pk_bf16_f32 v125,v24,v25
M 30,a[236:239],86
s_add_u32 s55,s55,s60
buffer_store_dwordx4 v[122:125],v15,s[36:39],s66 offen offset:0 sc0 sc1
v_xor_b32 v2,65536,v2
M 26,a[224:227],82
s_and_b32 s57,s57,131071
R 50,1,4096
v_cmp_eq_u32 vcc,s53,v7
M 26,a[232:235],86
s_nop 0
s_add_u32 s68,s68,s63
s_cmp_eq_u32 vcc_lo,-1
M 26,a[240:243],90
R 54,1,5120
M 26,a[248:251],94
buffer_store_dwordx4 v[122:125],v16,s[36:39],s66 offen offset:0 sc0 sc1
M 30,a[244:247],90
R 58,1,6144
M 30,a[252:255],94
L 5
D 0,56,2048
D 0,56,3072
s_cbranch_scc1 Lg1_S0_%=
Lg1_N0_%=:
v_mfma_f32_16x16x32_bf16 v[22:25],a[4:7],v[34:37],0
s_add_u32 m0,s59,0
s_mov_b32 s66,s65
R 62,1,7168
M 22,a[12:15],38
s_add_u32 s65,s65,s62
buffer_load_dword v5,s[36:39],s58 offen offset:0 sc1 lds
v_mfma_f32_16x16x32_bf16 v[18:21],a[0:3],v[34:37],0
R 66,1,8192
v_fma_f32 v26,-v102,v106,v26
M 18,a[8:11],38
L 5
buffer_load_dwordx4 v[98:101],v11,s[36:39],s73 offen offset:0 sc1
M 18,a[16:19],42
s_add_u32 s73,s73,s64
R 70,1,9216
v_fma_f32 v27,-v102,v107,v27
M 18,a[24:27],46
v_fma_f32 v28,-v102,v108,v28
v_fma_f32 v29,-v102,v109,v29
M 22,a[20:23],42
R 74,1,10240
v_mul_f32 v114,v26,v26
M 22,a[28:31],46
L 5
v_mul_f32 v115,v27,v27
v_mul_f32 v116,v28,v28
M 22,a[36:39],50
R 78,1,11264
v_mul_f32 v117,v29,v29
M 22,a[44:47],54
v_fma_f32 v114,v104,v114,v103
v_fma_f32 v115,v104,v115,v103
M 18,a[32:35],50
R 82,1,12288
v_fma_f32 v116,v104,v116,v103
M 18,a[40:43],54
L 5
v_fma_f32 v117,v104,v117,v103
v_mul_f32 v114,v26,v114
M 18,a[48:51],58
R 86,1,13312
v_mul_f32 v115,v27,v115
M 18,a[56:59],62
v_exp_f32 v114,v114
M 22,a[52:55],58
R 90,1,14336
v_mul_f32 v116,v28,v116
M 22,a[60:63],62
L 5
v_exp_f32 v115,v115
M 22,a[68:71],66
R 94,1,15360
v_mul_f32 v117,v29,v117
M 22,a[76:79],70
v_exp_f32 v116,v116
M 18,a[64:67],66
R 34,1,16384
v_fma_f32 v114,v114,v105,v105
M 18,a[72:75],70
L 5
v_exp_f32 v117,v117
M 18,a[80:83],74
R 38,1,17408
v_fma_f32 v115,v115,v105,v105
M 18,a[88:91],78
v_rcp_f32 v114,v114
M 22,a[84:87],74
R 42,1,18432
v_fma_f32 v116,v116,v105,v105
M 22,a[92:95],78
L 5
v_rcp_f32 v115,v115
M 22,a[100:103],82
R 46,1,19456
v_fma_f32 v117,v117,v105,v105
M 22,a[108:111],86
v_rcp_f32 v116,v116
M 18,a[96:99],82
R 50,1,20480
v_mul_f32 v26,v26,v114
M 18,a[104:107],86
L 5
v_rcp_f32 v117,v117
M 18,a[112:115],90
R 54,1,21504
v_mul_f32 v27,v27,v115
M 18,a[120:123],94
v_mul_f32 v28,v28,v116
v_mul_f32 v29,v29,v117
M 22,a[116:119],90
R 58,1,22528
v_cvt_pk_bf16_f32 v122,v26,v27
M 22,a[124:127],94
L 5
v_cvt_pk_bf16_f32 v123,v28,v29
v_fma_f32 v30,-v102,v110,v30
M 22,a[132:135],34
R 62,1,23552
v_fma_f32 v31,-v102,v111,v31
M 22,a[140:143],38
v_fma_f32 v32,-v102,v112,v32
v_fma_f32 v33,-v102,v113,v33
M 18,a[128:131],34
R 66,1,24576
v_mul_f32 v118,v30,v30
M 18,a[136:139],38
L 5
v_mul_f32 v119,v31,v31
v_mul_f32 v120,v32,v32
M 18,a[144:147],42
R 70,1,25600
v_mul_f32 v121,v33,v33
M 18,a[152:155],46
v_fma_f32 v118,v104,v118,v103
v_fma_f32 v119,v104,v119,v103
M 22,a[148:151],42
R 74,1,26624
v_fma_f32 v120,v104,v120,v103
M 22,a[156:159],46
L 5
v_fma_f32 v121,v104,v121,v103
v_mul_f32 v118,v30,v118
v_mul_f32 v119,v31,v119
M 22,a[164:167],50
R 78,1,27648
v_mul_f32 v120,v32,v120
M 22,a[172:175],54
v_exp_f32 v118,v118
M 18,a[160:163],50
R 82,1,28672
v_mul_f32 v121,v33,v121
M 18,a[168:171],54
L 5
v_exp_f32 v119,v119
M 18,a[176:179],58
R 86,1,29696
v_fma_f32 v118,v118,v105,v105
M 18,a[184:187],62
v_exp_f32 v120,v120
M 22,a[180:183],58
R 90,1,30720
v_fma_f32 v119,v119,v105,v105
M 22,a[188:191],62
L 5
v_exp_f32 v121,v121
M 22,a[196:199],66
s_waitcnt vmcnt(0)
R 94,1,31744
v_fma_f32 v120,v120,v105,v105
M 22,a[204:207],70
s_barrier
buffer_store_dword v4,v3,s[36:39],s70 offen offset:0 sc0 sc1
M 18,a[192:195],66
s_mov_b32 s70,s69
R 34,2,0
ds_read_b32 v7,v6 offset:0
M 18,a[200:203],70
L 6
v_rcp_f32 v118,v118
M 18,a[208:211],74
s_mov_b32 s69,s68
R 38,2,1024
v_fma_f32 v121,v121,v105,v105
M 18,a[216:219],78
s_add_u32 s68,s68,s63
v_rcp_f32 v119,v119
M 22,a[212:215],74
R 42,2,2048
v_mul_f32 v30,v30,v118
M 22,a[220:223],78
L 6
v_rcp_f32 v120,v120
v_rcp_f32 v121,v121
M 22,a[228:231],82
L 2
R 46,2,3072
v_mul_f32 v31,v31,v119
v_mul_f32 v32,v32,v120
)"
R"(v_mul_f32 v33,v33,v121
v_cvt_pk_bf16_f32 v124,v30,v31
v_cvt_pk_bf16_f32 v125,v32,v33
M 22,a[236:239],86
buffer_store_dwordx4 v[122:125],v15,s[36:39],s67 offen offset:0 sc0 sc1
v_xor_b32 v1,65536,v1
M 18,a[224:227],82
R 50,2,4096
v_cmp_eq_u32 vcc,s53,v7
M 18,a[232:235],86
s_nop 0
s_cmp_eq_u64 vcc,-1
M 18,a[240:243],90
R 54,2,5120
M 18,a[248:251],94
buffer_store_dwordx4 v[122:125],v16,s[36:39],s67 offen offset:0 sc0 sc1
M 22,a[244:247],90
R 58,2,6144
M 22,a[252:255],94
L 5
s_cbranch_scc1 Lg1_L21_%=
s_cmp_eq_u32 vcc_lo,-1
s_cbranch_scc1 Lg1_L11_%=
Lg1_X1_%=:
v_mfma_f32_16x16x32_bf16 v[30:33],a[4:7],v[34:37],0
s_add_u32 m0,s59,1024
s_mov_b32 s67,s65
R 62,2,7168
M 30,a[12:15],38
s_add_u32 s65,s65,s62
buffer_load_dword v5,s[36:39],s58 offen offset:0 sc1 lds
v_mfma_f32_16x16x32_bf16 v[26:29],a[0:3],v[34:37],0
R 66,2,8192
v_fma_f32 v18,-v98,v106,v18
M 26,a[8:11],38
L 5
buffer_load_dwordx4 v[102:105],v11,s[36:39],s73 offen offset:0 sc1
M 26,a[16:19],42
s_add_u32 s73,s73,s64
R 70,2,9216
v_fma_f32 v19,-v98,v107,v19
M 26,a[24:27],46
v_fma_f32 v20,-v98,v108,v20
v_fma_f32 v21,-v98,v109,v21
M 30,a[20:23],42
R 74,2,10240
v_mul_f32 v114,v18,v18
M 30,a[28:31],46
L 5
v_mul_f32 v115,v19,v19
v_mul_f32 v116,v20,v20
M 30,a[36:39],50
R 78,2,11264
v_mul_f32 v117,v21,v21
M 30,a[44:47],54
v_fma_f32 v114,v100,v114,v99
v_fma_f32 v115,v100,v115,v99
M 26,a[32:35],50
R 82,2,12288
v_fma_f32 v116,v100,v116,v99
M 26,a[40:43],54
L 5
v_fma_f32 v117,v100,v117,v99
v_mul_f32 v114,v18,v114
M 26,a[48:51],58
R 86,2,13312
v_mul_f32 v115,v19,v115
M 26,a[56:59],62
v_exp_f32 v114,v114
M 30,a[52:55],58
R 90,2,14336
v_mul_f32 v116,v20,v116
M 30,a[60:63],62
L 5
v_exp_f32 v115,v115
M 30,a[68:71],66
R 94,2,15360
v_mul_f32 v117,v21,v117
M 30,a[76:79],70
v_exp_f32 v116,v116
M 26,a[64:67],66
R 34,2,16384
v_fma_f32 v114,v114,v101,v101
M 26,a[72:75],70
L 5
v_exp_f32 v117,v117
M 26,a[80:83],74
R 38,2,17408
v_fma_f32 v115,v115,v101,v101
M 26,a[88:91],78
v_rcp_f32 v114,v114
M 30,a[84:87],74
R 42,2,18432
v_fma_f32 v116,v116,v101,v101
M 30,a[92:95],78
L 5
v_rcp_f32 v115,v115
M 30,a[100:103],82
R 46,2,19456
v_fma_f32 v117,v117,v101,v101
M 30,a[108:111],86
v_rcp_f32 v116,v116
M 26,a[96:99],82
R 50,2,20480
v_mul_f32 v18,v18,v114
M 26,a[104:107],86
L 5
v_rcp_f32 v117,v117
M 26,a[112:115],90
R 54,2,21504
v_mul_f32 v19,v19,v115
M 26,a[120:123],94
v_mul_f32 v20,v20,v116
v_mul_f32 v21,v21,v117
M 30,a[116:119],90
R 58,2,22528
v_cvt_pk_bf16_f32 v122,v18,v19
M 30,a[124:127],94
L 5
v_cvt_pk_bf16_f32 v123,v20,v21
v_fma_f32 v22,-v98,v110,v22
M 30,a[132:135],34
R 62,2,23552
v_fma_f32 v23,-v98,v111,v23
M 30,a[140:143],38
v_fma_f32 v24,-v98,v112,v24
v_fma_f32 v25,-v98,v113,v25
M 26,a[128:131],34
R 66,2,24576
v_mul_f32 v118,v22,v22
M 26,a[136:139],38
L 5
v_mul_f32 v119,v23,v23
v_mul_f32 v120,v24,v24
M 26,a[144:147],42
R 70,2,25600
v_mul_f32 v121,v25,v25
M 26,a[152:155],46
v_fma_f32 v118,v100,v118,v99
v_fma_f32 v119,v100,v119,v99
M 30,a[148:151],42
R 74,2,26624
v_fma_f32 v120,v100,v120,v99
M 30,a[156:159],46
L 5
v_fma_f32 v121,v100,v121,v99
v_mul_f32 v118,v22,v118
M 30,a[164:167],50
R 78,2,27648
v_mul_f32 v119,v23,v119
M 30,a[172:175],54
v_exp_f32 v118,v118
M 26,a[160:163],50
R 82,2,28672
v_mul_f32 v120,v24,v120
M 26,a[168:171],54
L 5
v_exp_f32 v119,v119
M 26,a[176:179],58
R 86,2,29696
v_mul_f32 v121,v25,v121
M 26,a[184:187],62
v_exp_f32 v120,v120
M 30,a[180:183],58
R 90,2,30720
v_fma_f32 v118,v118,v101,v101
M 30,a[188:191],62
L 5
v_exp_f32 v121,v121
M 30,a[196:199],66
s_waitcnt vmcnt(0)
R 94,2,31744
v_fma_f32 v119,v119,v101,v101
M 30,a[204:207],70
s_barrier
buffer_store_dword v4,v3,s[36:39],s70 offen offset:0 sc0 sc1
M 26,a[192:195],66
s_mov_b32 s70,s69
ds_read_b32 v7,v6 offset:1024
v_fma_f32 v120,v120,v101,v101
M 26,a[200:203],70
L 5
v_rcp_f32 v118,v118
M 26,a[208:211],74
s_mov_b32 s69,s68
v_fma_f32 v121,v121,v101,v101
v_mul_f32 v22,v22,v118
M 26,a[216:219],78
s_add_u32 s68,s68,s63
v_rcp_f32 v119,v119
M 30,a[212:215],74
v_rcp_f32 v120,v120
M 30,a[220:223],78
L 3
v_rcp_f32 v121,v121
M 30,a[228:231],82
L 0
v_mul_f32 v23,v23,v119
v_mul_f32 v24,v24,v120
v_mul_f32 v25,v25,v121
v_cvt_pk_bf16_f32 v124,v22,v23
v_cvt_pk_bf16_f32 v125,v24,v25
M 30,a[236:239],86
buffer_store_dwordx4 v[122:125],v15,s[36:39],s66 offen offset:0 sc0 sc1
v_xor_b32 v2,65536,v2
M 26,a[224:227],82
v_cmp_eq_u32 vcc,s53,v7
M 26,a[232:235],86
s_nop 0
M 26,a[240:243],90
buffer_store_dwordx4 v[122:125],v16,s[36:39],s66 offen offset:0 sc0 sc1
M 26,a[248:251],94
M 30,a[244:247],90
M 30,a[252:255],94
s_mov_b32 s80,0
s_cmp_eq_u32 vcc_lo,-1
s_cbranch_scc0 Lg1_xn1_%=
s_mov_b32 m0,s57
s_add_u32 s56,s55,4096
D 0,55,0
v_fma_f32 v26,-v102,v106,v26
v_fma_f32 v27,-v102,v107,v27
v_fma_f32 v28,-v102,v108,v28
v_fma_f32 v29,-v102,v109,v29
v_mul_f32 v114,v26,v26
v_mul_f32 v115,v27,v27
v_mul_f32 v116,v28,v28
D 0,55,1024
v_mul_f32 v117,v29,v29
v_fma_f32 v114,v104,v114,v103
v_fma_f32 v115,v104,v115,v103
v_fma_f32 v116,v104,v116,v103
v_fma_f32 v117,v104,v117,v103
v_mul_f32 v114,v26,v114
v_mul_f32 v115,v27,v115
D 0,55,2048
v_mul_f32 v116,v28,v116
v_mul_f32 v117,v29,v117
v_exp_f32 v114,v114
v_exp_f32 v115,v115
v_exp_f32 v116,v116
v_exp_f32 v117,v117
v_fma_f32 v114,v114,v105,v105
D 0,55,3072
v_fma_f32 v115,v115,v105,v105
v_fma_f32 v116,v116,v105,v105
v_fma_f32 v117,v117,v105,v105
v_rcp_f32 v114,v114
v_rcp_f32 v115,v115
v_rcp_f32 v116,v116
v_rcp_f32 v117,v117
s_add_u32 m0,s57,4096
s_nop 0
D 0,56,0
v_mul_f32 v26,v26,v114
v_mul_f32 v27,v27,v115
v_mul_f32 v28,v28,v116
v_mul_f32 v29,v29,v117
v_cvt_pk_bf16_f32 v122,v26,v27
v_cvt_pk_bf16_f32 v123,v28,v29
v_fma_f32 v30,-v102,v110,v30
D 0,56,1024
v_fma_f32 v31,-v102,v111,v31
v_fma_f32 v32,-v102,v112,v32
v_fma_f32 v33,-v102,v113,v33
v_mul_f32 v118,v30,v30
v_mul_f32 v119,v31,v31
v_mul_f32 v120,v32,v32
v_mul_f32 v121,v33,v33
D 0,56,2048
v_fma_f32 v118,v104,v118,v103
v_fma_f32 v119,v104,v119,v103
v_fma_f32 v120,v104,v120,v103
v_fma_f32 v121,v104,v121,v103
v_mul_f32 v118,v30,v118
v_mul_f32 v119,v31,v119
v_mul_f32 v120,v32,v120
D 0,56,3072
v_mul_f32 v121,v33,v121
v_exp_f32 v118,v118
v_exp_f32 v119,v119
v_exp_f32 v120,v120
v_exp_f32 v121,v121
v_fma_f32 v118,v118,v105,v105
v_fma_f32 v119,v119,v105,v105
s_add_u32 s55,s55,s60
s_add_u32 s57,s57,32768
s_and_b32 s57,s57,131071
s_add_u32 s54,s54,s46
v_fma_f32 v120,v120,v105,v105
v_fma_f32 v121,v121,v105,v105
v_rcp_f32 v118,v118
v_rcp_f32 v119,v119
v_rcp_f32 v120,v120
v_rcp_f32 v121,v121
v_mul_f32 v30,v30,v118
v_mul_f32 v31,v31,v119
v_mul_f32 v32,v32,v120
v_mul_f32 v33,v33,v121
v_cvt_pk_bf16_f32 v124,v30,v31
v_cvt_pk_bf16_f32 v125,v32,v33
buffer_store_dwordx4 v[122:125],v15,s[36:39],s67 offen offset:0 sc0 sc1
buffer_store_dwordx4 v[122:125],v16,s[36:39],s67 offen offset:0 sc0 sc1
s_mov_b32 s72,s69
s_mov_b32 s69,0x7fff0000
s_mov_b32 s67,0x7fff0000
s_mov_b32 s77,0x7fff0000
s_branch Lg1_gox_%=
Lg1_xn1_%=:
s_cmp_ge_u32 s54,256
s_cbranch_scc1 Lg1_fin1_%=
s_add_u32 m0,s59,4096
s_nop 0
buffer_load_dword v5,s[36:39],s58 offen offset:0 sc1 lds
buffer_store_dword v4,v3,s[36:39],s71 offen offset:0 sc0 sc1
s_add_u32 m0,s59,5120
s_nop 0
buffer_load_dword v5,s[36:39],s58 offen offset:0 sc1 lds
buffer_store_dword v4,v3,s[36:39],s71 offen offset:0 sc0 sc1
s_mov_b32 s83,0
v_fma_f32 v26,-v102,v106,v26
v_fma_f32 v27,-v102,v107,v27
v_fma_f32 v28,-v102,v108,v28
v_fma_f32 v29,-v102,v109,v29
v_mul_f32 v114,v26,v26
v_mul_f32 v115,v27,v27
v_mul_f32 v116,v28,v28
v_mul_f32 v117,v29,v29
v_fma_f32 v114,v104,v114,v103
v_fma_f32 v115,v104,v115,v103
v_fma_f32 v116,v104,v116,v103
v_fma_f32 v117,v104,v117,v103
v_mul_f32 v114,v26,v114
v_mul_f32 v115,v27,v115
v_mul_f32 v116,v28,v116
v_mul_f32 v117,v29,v117
v_exp_f32 v114,v114
v_exp_f32 v115,v115
v_exp_f32 v116,v116
v_exp_f32 v117,v117
v_fma_f32 v114,v114,v105,v105
v_fma_f32 v115,v115,v105,v105
v_fma_f32 v116,v116,v105,v105
v_fma_f32 v117,v117,v105,v105
v_rcp_f32 v114,v114
v_rcp_f32 v115,v115
v_rcp_f32 v116,v116
v_rcp_f32 v117,v117
v_mul_f32 v26,v26,v114
v_mul_f32 v27,v27,v115
v_mul_f32 v28,v28,v116
v_mul_f32 v29,v29,v117
v_cvt_pk_bf16_f32 v122,v26,v27
v_cvt_pk_bf16_f32 v123,v28,v29
v_fma_f32 v30,-v102,v110,v30
v_fma_f32 v31,-v102,v111,v31
v_fma_f32 v32,-v102,v112,v32
v_fma_f32 v33,-v102,v113,v33
v_mul_f32 v118,v30,v30
v_mul_f32 v119,v31,v31
v_mul_f32 v120,v32,v32
v_mul_f32 v121,v33,v33
v_fma_f32 v118,v104,v118,v103
v_fma_f32 v119,v104,v119,v103
v_fma_f32 v120,v104,v120,v103
v_fma_f32 v121,v104,v121,v103
v_mul_f32 v118,v30,v118
v_mul_f32 v119,v31,v119
v_mul_f32 v120,v32,v120
v_mul_f32 v121,v33,v121
v_exp_f32 v118,v118
v_exp_f32 v119,v119
v_exp_f32 v120,v120
v_exp_f32 v121,v121
v_fma_f32 v118,v118,v105,v105
v_fma_f32 v119,v119,v105,v105
v_fma_f32 v120,v120,v105,v105
v_fma_f32 v121,v121,v105,v105
v_rcp_f32 v118,v118
v_rcp_f32 v119,v119
v_rcp_f32 v120,v120
v_rcp_f32 v121,v121
v_mul_f32 v30,v30,v118
v_mul_f32 v31,v31,v119
v_mul_f32 v32,v32,v120
v_mul_f32 v33,v33,v121
v_cvt_pk_bf16_f32 v124,v30,v31
v_cvt_pk_bf16_f32 v125,v32,v33
buffer_store_dwordx4 v[122:125],v15,s[36:39],s67 offen offset:0 sc0 sc1
buffer_store_dwordx4 v[122:125],v16,s[36:39],s67 offen offset:0 sc0 sc1
s_mov_b32 s72,s69
s_mov_b32 s69,0x7fff0000
s_mov_b32 s67,0x7fff0000
s_mov_b32 s77,0x7fff0000
s_branch Lg1_poll_%=
Lg1_fin1_%=:
v_fma_f32 v26,-v102,v106,v26
v_fma_f32 v27,-v102,v107,v27
v_fma_f32 v28,-v102,v108,v28
v_fma_f32 v29,-v102,v109,v29
v_mul_f32 v114,v26,v26
v_mul_f32 v115,v27,v27
v_mul_f32 v116,v28,v28
v_mul_f32 v117,v29,v29
v_fma_f32 v114,v104,v114,v103
v_fma_f32 v115,v104,v115,v103
v_fma_f32 v116,v104,v116,v103
v_fma_f32 v117,v104,v117,v103
v_mul_f32 v114,v26,v114
v_mul_f32 v115,v27,v115
v_mul_f32 v116,v28,v116
v_mul_f32 v117,v29,v117
v_exp_f32 v114,v114
v_exp_f32 v115,v115
v_exp_f32 v116,v116
v_exp_f32 v117,v117
v_fma_f32 v114,v114,v105,v105
v_fma_f32 v115,v115,v105,v105
v_fma_f32 v116,v116,v105,v105
v_fma_f32 v117,v117,v105,v105
v_rcp_f32 v114,v114
v_rcp_f32 v115,v115
v_rcp_f32 v116,v116
v_rcp_f32 v117,v117
v_mul_f32 v26,v26,v114
v_mul_f32 v27,v27,v115
v_mul_f32 v28,v28,v116
v_mul_f32 v29,v29,v117
v_cvt_pk_bf16_f32 v122,v26,v27
v_cvt_pk_bf16_f32 v123,v28,v29
v_fma_f32 v30,-v102,v110,v30
v_fma_f32 v31,-v102,v111,v31
v_fma_f32 v32,-v102,v112,v32
v_fma_f32 v33,-v102,v113,v33
v_mul_f32 v118,v30,v30
v_mul_f32 v119,v31,v31
v_mul_f32 v120,v32,v32
v_mul_f32 v121,v33,v33
v_fma_f32 v118,v104,v118,v103
v_fma_f32 v119,v104,v119,v103
v_fma_f32 v120,v104,v120,v103
v_fma_f32 v121,v104,v121,v103
v_mul_f32 v118,v30,v118
v_mul_f32 v119,v31,v119
v_mul_f32 v120,v32,v120
v_mul_f32 v121,v33,v121
v_exp_f32 v118,v118
v_exp_f32 v119,v119
v_exp_f32 v120,v120
v_exp_f32 v121,v121
v_fma_f32 v118,v118,v105,v105
v_fma_f32 v119,v119,v105,v105
v_fma_f32 v120,v120,v105,v105
v_fma_f32 v121,v121,v105,v105
v_rcp_f32 v118,v118
v_rcp_f32 v119,v119
v_rcp_f32 v120,v120
v_rcp_f32 v121,v121
v_mul_f32 v30,v30,v118
v_mul_f32 v31,v31,v119
v_mul_f32 v32,v32,v120
v_mul_f32 v33,v33,v121
v_cvt_pk_bf16_f32 v124,v30,v31
v_cvt_pk_bf16_f32 v125,v32,v33
buffer_store_dwordx4 v[122:125],v15,s[36:39],s67 offen offset:0 sc0 sc1
buffer_store_dwordx4 v[122:125],v16,s[36:39],s67 offen offset:0 sc0 sc1
s_mov_b32 s72,s69
s_mov_b32 s69,0x7fff0000
s_mov_b32 s67,0x7fff0000
s_mov_b32 s77,0x7fff0000
s_branch Lg1_done_%=
Lg1_L10_%=:
v_mfma_f32_16x16x32_bf16 v[22:25],a[4:7],v[34:37],0
s_add_u32 s58,s58,s61
s_add_u32 m0,s59,0
R 62,1,7168
M 22,a[12:15],38
s_add_u32 s56,s55,4096
buffer_load_dword v5,s[36:39],s58 offen offset:0 sc1 lds
v_mfma_f32_16x16x32_bf16 v[18:21],a[0:3],v[34:37],0
s_mov_b32 m0,s57
R 66,1,8192
v_fma_f32 v26,-v102,v106,v26
M 18,a[8:11],38
L 5
buffer_load_dwordx4 v[98:101],v11,s[36:39],s73 offen offset:0 sc1
M 18,a[16:19],42
s_mov_b32 s66,s65
R 70,1,9216
v_fma_f32 v27,-v102,v107,v27
M 18,a[24:27],46
s_add_u32 s73,s73,s64
v_fma_f32 v28,-v102,v108,v28
v_fma_f32 v29,-v102,v109,v29
M 22,a[20:23],42
s_add_u32 s54,s54,s46
R 74,1,10240
v_mul_f32 v114,v26,v26
M 22,a[28:31],46
L 5
)"
R"(D 0,55,0
M 22,a[36:39],50
s_add_u32 s65,s65,s62
R 78,1,11264
v_mul_f32 v115,v27,v27
M 22,a[44:47],54
v_mul_f32 v116,v28,v28
v_mul_f32 v117,v29,v29
M 18,a[32:35],50
R 82,1,12288
v_fma_f32 v114,v104,v114,v103
M 18,a[40:43],54
L 5
D 0,55,1024
M 18,a[48:51],58
R 86,1,13312
v_fma_f32 v115,v104,v115,v103
M 18,a[56:59],62
v_fma_f32 v116,v104,v116,v103
v_fma_f32 v117,v104,v117,v103
M 22,a[52:55],58
R 90,1,14336
v_mul_f32 v114,v26,v114
M 22,a[60:63],62
L 5
D 0,55,2048
M 22,a[68:71],66
R 94,1,15360
v_mul_f32 v115,v27,v115
M 22,a[76:79],70
v_exp_f32 v114,v114
M 18,a[64:67],66
R 34,1,16384
v_mul_f32 v116,v28,v116
M 18,a[72:75],70
L 5
D 0,55,3072
M 18,a[80:83],74
s_add_u32 m0,s57,4096
R 38,1,17408
v_mul_f32 v117,v29,v117
M 18,a[88:91],78
s_add_u32 s57,s57,32768
v_exp_f32 v115,v115
M 22,a[84:87],74
s_add_u32 s55,s55,s60
R 42,1,18432
v_fma_f32 v114,v114,v105,v105
M 22,a[92:95],78
L 5
D 0,56,0
M 22,a[100:103],82
s_and_b32 s57,s57,131071
R 46,1,19456
v_fma_f32 v115,v115,v105,v105
M 22,a[108:111],86
v_exp_f32 v116,v116
M 18,a[96:99],82
R 50,1,20480
v_fma_f32 v116,v116,v105,v105
M 18,a[104:107],86
L 5
D 0,56,1024
M 18,a[112:115],90
R 54,1,21504
v_fma_f32 v30,-v102,v110,v30
M 18,a[120:123],94
v_exp_f32 v117,v117
M 22,a[116:119],90
R 58,1,22528
v_fma_f32 v117,v117,v105,v105
M 22,a[124:127],94
L 5
D 0,56,2048
M 22,a[132:135],34
R 62,1,23552
v_fma_f32 v31,-v102,v111,v31
M 22,a[140:143],38
v_rcp_f32 v114,v114
M 18,a[128:131],34
R 66,1,24576
v_mul_f32 v26,v26,v114
M 18,a[136:139],38
L 5
D 0,56,3072
M 18,a[144:147],42
R 70,1,25600
v_fma_f32 v32,-v102,v112,v32
M 18,a[152:155],46
v_rcp_f32 v115,v115
M 22,a[148:151],42
R 74,1,26624
v_mul_f32 v27,v27,v115
M 22,a[156:159],46
L 5
v_rcp_f32 v116,v116
M 22,a[164:167],50
R 78,1,27648
v_mul_f32 v28,v28,v116
M 22,a[172:175],54
v_rcp_f32 v117,v117
M 18,a[160:163],50
R 82,1,28672
v_mul_f32 v29,v29,v117
M 18,a[168:171],54
L 5
v_cvt_pk_bf16_f32 v122,v26,v27
v_cvt_pk_bf16_f32 v123,v28,v29
M 18,a[176:179],58
R 86,1,29696
v_fma_f32 v33,-v102,v113,v33
M 18,a[184:187],62
v_mul_f32 v118,v30,v30
v_mul_f32 v119,v31,v31
M 22,a[180:183],58
R 90,1,30720
v_mul_f32 v120,v32,v32
M 22,a[188:191],62
L 5
v_mul_f32 v121,v33,v33
v_fma_f32 v118,v104,v118,v103
M 22,a[196:199],66
s_waitcnt vmcnt(0)
R 94,1,31744
v_fma_f32 v119,v104,v119,v103
M 22,a[204:207],70
s_barrier
buffer_store_dword v4,v3,s[36:39],s70 offen offset:0 sc0 sc1
M 18,a[192:195],66
s_mov_b32 s70,s69
R 34,2,0
ds_read_b32 v7,v6 offset:0
M 18,a[200:203],70
L 6
v_fma_f32 v120,v104,v120,v103
v_fma_f32 v121,v104,v121,v103
M 18,a[208:211],74
s_mov_b32 s69,s68
R 38,2,1024
v_mul_f32 v118,v30,v118
M 18,a[216:219],78
s_add_u32 s68,s68,s63
v_mul_f32 v119,v31,v119
v_mul_f32 v120,v32,v120
M 22,a[212:215],74
R 42,2,2048
v_mul_f32 v121,v33,v121
v_exp_f32 v118,v118
v_exp_f32 v119,v119
v_exp_f32 v120,v120
v_exp_f32 v121,v121
M 22,a[220:223],78
L 6
v_fma_f32 v118,v118,v105,v105
v_fma_f32 v119,v119,v105,v105
v_fma_f32 v120,v120,v105,v105
v_fma_f32 v121,v121,v105,v105
v_rcp_f32 v118,v118
v_rcp_f32 v119,v119
v_rcp_f32 v120,v120
v_rcp_f32 v121,v121
M 22,a[228:231],82
L 2
R 46,2,3072
v_mul_f32 v30,v30,v118
v_mul_f32 v31,v31,v119
v_mul_f32 v32,v32,v120
v_mul_f32 v33,v33,v121
v_cvt_pk_bf16_f32 v124,v30,v31
v_cvt_pk_bf16_f32 v125,v32,v33
M 22,a[236:239],86
buffer_store_dwordx4 v[122:125],v15,s[36:39],s67 offen offset:0 sc0 sc1
v_xor_b32 v1,65536,v1
M 18,a[224:227],82
R 50,2,4096
v_cmp_eq_u32 vcc,s53,v7
M 18,a[232:235],86
s_nop 0
s_cmp_eq_u64 vcc,-1
M 18,a[240:243],90
R 54,2,5120
M 18,a[248:251],94
buffer_store_dwordx4 v[122:125],v16,s[36:39],s67 offen offset:0 sc0 sc1
M 22,a[244:247],90
R 58,2,6144
M 22,a[252:255],94
L 5
s_cbranch_scc1 Lg1_L21_%=
s_cmp_eq_u32 vcc_lo,-1
s_cbranch_scc1 Lg1_L11_%=
s_branch Lg1_X1_%=
Lg1_L11_%=:
v_mfma_f32_16x16x32_bf16 v[30:33],a[4:7],v[34:37],0
s_add_u32 s58,s58,s61
s_add_u32 m0,s59,1024
R 62,2,7168
M 30,a[12:15],38
s_add_u32 s56,s55,4096
buffer_load_dword v5,s[36:39],s58 offen offset:0 sc1 lds
v_mfma_f32_16x16x32_bf16 v[26:29],a[0:3],v[34:37],0
s_mov_b32 m0,s57
R 66,2,8192
v_fma_f32 v18,-v98,v106,v18
M 26,a[8:11],38
L 5
buffer_load_dwordx4 v[102:105],v11,s[36:39],s73 offen offset:0 sc1
M 26,a[16:19],42
s_mov_b32 s67,s65
R 70,2,9216
v_fma_f32 v19,-v98,v107,v19
M 26,a[24:27],46
s_add_u32 s73,s73,s64
v_fma_f32 v20,-v98,v108,v20
v_fma_f32 v21,-v98,v109,v21
M 30,a[20:23],42
s_add_u32 s54,s54,s46
R 74,2,10240
v_mul_f32 v114,v18,v18
M 30,a[28:31],46
L 5
D 0,55,0
M 30,a[36:39],50
s_add_u32 s65,s65,s62
R 78,2,11264
v_mul_f32 v115,v19,v19
M 30,a[44:47],54
v_mul_f32 v116,v20,v20
v_mul_f32 v117,v21,v21
M 26,a[32:35],50
R 82,2,12288
v_fma_f32 v114,v100,v114,v99
M 26,a[40:43],54
L 5
D 0,55,1024
M 26,a[48:51],58
R 86,2,13312
v_fma_f32 v115,v100,v115,v99
M 26,a[56:59],62
v_fma_f32 v116,v100,v116,v99
v_fma_f32 v117,v100,v117,v99
M 30,a[52:55],58
R 90,2,14336
v_mul_f32 v114,v18,v114
M 30,a[60:63],62
L 5
D 0,55,2048
M 30,a[68:71],66
R 94,2,15360
v_mul_f32 v115,v19,v115
M 30,a[76:79],70
v_exp_f32 v114,v114
M 26,a[64:67],66
R 34,2,16384
v_mul_f32 v116,v20,v116
M 26,a[72:75],70
L 5
D 0,55,3072
M 26,a[80:83],74
s_add_u32 m0,s57,4096
R 38,2,17408
v_mul_f32 v117,v21,v117
M 26,a[88:91],78
s_add_u32 s57,s57,32768
v_exp_f32 v115,v115
M 30,a[84:87],74
s_add_u32 s55,s55,s60
R 42,2,18432
v_fma_f32 v114,v114,v101,v101
M 30,a[92:95],78
L 5
D 0,56,0
M 30,a[100:103],82
s_and_b32 s57,s57,131071
R 46,2,19456
v_fma_f32 v115,v115,v101,v101
M 30,a[108:111],86
v_exp_f32 v116,v116
M 26,a[96:99],82
R 50,2,20480
v_fma_f32 v116,v116,v101,v101
M 26,a[104:107],86
L 5
D 0,56,1024
M 26,a[112:115],90
R 54,2,21504
v_fma_f32 v22,-v98,v110,v22
M 26,a[120:123],94
v_exp_f32 v117,v117
M 30,a[116:119],90
R 58,2,22528
v_fma_f32 v117,v117,v101,v101
M 30,a[124:127],94
L 5
D 0,56,2048
M 30,a[132:135],34
R 62,2,23552
v_fma_f32 v23,-v98,v111,v23
M 30,a[140:143],38
v_rcp_f32 v114,v114
M 26,a[128:131],34
R 66,2,24576
v_mul_f32 v18,v18,v114
M 26,a[136:139],38
L 5
D 0,56,3072
M 26,a[144:147],42
R 70,2,25600
v_fma_f32 v24,-v98,v112,v24
M 26,a[152:155],46
v_rcp_f32 v115,v115
M 30,a[148:151],42
R 74,2,26624
v_mul_f32 v19,v19,v115
M 30,a[156:159],46
L 5
v_rcp_f32 v116,v116
M 30,a[164:167],50
R 78,2,27648
v_mul_f32 v20,v20,v116
M 30,a[172:175],54
v_rcp_f32 v117,v117
M 26,a[160:163],50
R 82,2,28672
v_mul_f32 v21,v21,v117
M 26,a[168:171],54
L 5
v_cvt_pk_bf16_f32 v122,v18,v19
v_cvt_pk_bf16_f32 v123,v20,v21
M 26,a[176:179],58
R 86,2,29696
v_fma_f32 v25,-v98,v113,v25
M 26,a[184:187],62
v_mul_f32 v118,v22,v22
v_mul_f32 v119,v23,v23
M 30,a[180:183],58
R 90,2,30720
v_mul_f32 v120,v24,v24
M 30,a[188:191],62
L 5
v_mul_f32 v121,v25,v25
v_fma_f32 v118,v100,v118,v99
M 30,a[196:199],66
s_waitcnt vmcnt(0)
R 94,2,31744
v_fma_f32 v119,v100,v119,v99
M 30,a[204:207],70
s_barrier
buffer_store_dword v4,v3,s[36:39],s70 offen offset:0 sc0 sc1
M 26,a[192:195],66
s_mov_b32 s70,s69
R 34,1,0
ds_read_b32 v7,v6 offset:1024
M 26,a[200:203],70
L 6
v_fma_f32 v120,v100,v120,v99
v_fma_f32 v121,v100,v121,v99
M 26,a[208:211],74
s_mov_b32 s69,s68
R 38,1,1024
v_mul_f32 v118,v22,v118
M 26,a[216:219],78
s_add_u32 s68,s68,s63
v_mul_f32 v119,v23,v119
v_mul_f32 v120,v24,v120
M 30,a[212:215],74
R 42,1,2048
v_mul_f32 v121,v25,v121
v_exp_f32 v118,v118
v_exp_f32 v119,v119
v_exp_f32 v120,v120
v_exp_f32 v121,v121
M 30,a[220:223],78
L 6
v_fma_f32 v118,v118,v101,v101
v_fma_f32 v119,v119,v101,v101
v_fma_f32 v120,v120,v101,v101
v_fma_f32 v121,v121,v101,v101
v_rcp_f32 v118,v118
v_rcp_f32 v119,v119
v_rcp_f32 v120,v120
v_rcp_f32 v121,v121
M 30,a[228:231],82
L 2
R 46,1,3072
v_mul_f32 v22,v22,v118
v_mul_f32 v23,v23,v119
v_mul_f32 v24,v24,v120
v_mul_f32 v25,v25,v121
v_cvt_pk_bf16_f32 v124,v22,v23
v_cvt_pk_bf16_f32 v125,v24,v25
M 30,a[236:239],86
buffer_store_dwordx4 v[122:125],v15,s[36:39],s66 offen offset:0 sc0 sc1
v_xor_b32 v2,65536,v2
M 26,a[224:227],82
R 50,1,4096
v_cmp_eq_u32 vcc,s53,v7
M 26,a[232:235],86
s_nop 0
s_cmp_eq_u64 vcc,-1
M 26,a[240:243],90
R 54,1,5120
M 26,a[248:251],94
buffer_store_dwordx4 v[122:125],v16,s[36:39],s66 offen offset:0 sc0 sc1
M 30,a[244:247],90
R 58,1,6144
M 30,a[252:255],94
L 5
s_cbranch_scc1 Lg1_L20_%=
s_cmp_eq_u32 vcc_lo,-1
s_cbranch_scc1 Lg1_L10_%=
s_branch Lg1_X0_%=
Lg1_done_%=:
s_waitcnt vmcnt(2)
s_barrier
buffer_store_dword v4,v3,s[36:39],s70 offen offset:0 sc0 sc1
s_waitcnt vmcnt(1)
s_barrier
buffer_store_dword v4,v3,s[36:39],s72 offen offset:0 sc0 sc1
s_waitcnt vmcnt(0) lgkmcnt(0)
s_barrier
s_branch Lg2_end_%=
Lg2_begin_%=:
Lcode_g2_b:
s_mov_b64 s[36:37],%1
s_mov_b32 s38,111243392
s_mov_b32 s39,0x00020000
s_mov_b32 s44,%2
s_mov_b32 s45,%3
s_mov_b32 s46,%4
s_mov_b32 s47,%5
s_mov_b32 s48,%6
s_mov_b32 s49,%7
s_mov_b32 s50,%8
s_mov_b32 s51,%9
s_mov_b32 s93,%10
v_mov_b32 v0,%11
v_mov_b32 v3,%12
v_mov_b32 v5,%13
v_mov_b32 v6,%14
v_mov_b32 v18,%15
v_mov_b32 v19,%16
v_mov_b32 v20,%17
v_mov_b32 v21,%18
v_mov_b32 v22,%19
v_mov_b32 v10,%20
v_mov_b32 v11,%21
v_mov_b32 v12,%22
v_mov_b32 v13,%23
v_mov_b32 v14,%24
v_mov_b32 v15,%25
v_mov_b32 v16,%26
s_mov_b64 s[40:41],%27
s_mov_b32 s42,2097152
s_mov_b32 s43,0x00020000
s_lshl_b32 s57,s44,11
s_mov_b32 s79,111243264
buffer_load_dword v4,off,s[36:39],s79 sc1
s_add_u32 s55,s50,0
s_add_u32 m0,s57,0
s_nop 0
buffer_load_dwordx4 v18,s[40:43],s55 offen offset:0  lds
s_add_u32 m0,s57,1024
s_nop 0
buffer_load_dwordx4 v19,s[40:43],s55 offen offset:0  lds
s_add_u32 m0,s57,8064
s_nop 0
buffer_load_dwordx4 v18,s[40:43],s55 offen offset:128  lds
s_add_u32 m0,s57,9088
s_nop 0
buffer_load_dwordx4 v19,s[40:43],s55 offen offset:128  lds
s_add_u32 s55,s50,131072
s_add_u32 m0,s57,16384
s_nop 0
buffer_load_dwordx4 v18,s[40:43],s55 offen offset:0  lds
s_add_u32 m0,s57,17408
s_nop 0
buffer_load_dwordx4 v19,s[40:43],s55 offen offset:0  lds
s_add_u32 m0,s57,24448
s_nop 0
buffer_load_dwordx4 v18,s[40:43],s55 offen offset:128  lds
s_add_u32 m0,s57,25472
s_nop 0
buffer_load_dwordx4 v19,s[40:43],s55 offen offset:128  lds
s_add_u32 s55,s50,262144
s_add_u32 m0,s57,32768
s_nop 0
buffer_load_dwordx4 v18,s[40:43],s55 offen offset:0  lds
s_add_u32 m0,s57,33792
s_nop 0
buffer_load_dwordx4 v19,s[40:43],s55 offen offset:0  lds
s_add_u32 m0,s57,40832
s_nop 0
buffer_load_dwordx4 v18,s[40:43],s55 offen offset:128  lds
s_add_u32 m0,s57,41856
s_nop 0
buffer_load_dwordx4 v19,s[40:43],s55 offen offset:128  lds
s_add_u32 s55,s50,393216
s_add_u32 m0,s57,49152
s_nop 0
buffer_load_dwordx4 v18,s[40:43],s55 offen offset:0  lds
)"
R"(s_add_u32 m0,s57,50176
s_nop 0
buffer_load_dwordx4 v19,s[40:43],s55 offen offset:0  lds
s_add_u32 m0,s57,57216
s_nop 0
buffer_load_dwordx4 v18,s[40:43],s55 offen offset:128  lds
s_add_u32 m0,s57,58240
s_nop 0
buffer_load_dwordx4 v19,s[40:43],s55 offen offset:128  lds
s_add_u32 s55,s50,524288
s_add_u32 m0,s57,65536
s_nop 0
buffer_load_dwordx4 v18,s[40:43],s55 offen offset:0  lds
s_add_u32 m0,s57,66560
s_nop 0
buffer_load_dwordx4 v19,s[40:43],s55 offen offset:0  lds
s_add_u32 m0,s57,73600
s_nop 0
buffer_load_dwordx4 v18,s[40:43],s55 offen offset:128  lds
s_add_u32 m0,s57,74624
s_nop 0
buffer_load_dwordx4 v19,s[40:43],s55 offen offset:128  lds
s_add_u32 s55,s50,655360
s_add_u32 m0,s57,81920
s_nop 0
buffer_load_dwordx4 v18,s[40:43],s55 offen offset:0  lds
s_add_u32 m0,s57,82944
s_nop 0
buffer_load_dwordx4 v19,s[40:43],s55 offen offset:0  lds
s_add_u32 m0,s57,89984
s_nop 0
buffer_load_dwordx4 v18,s[40:43],s55 offen offset:128  lds
s_add_u32 m0,s57,91008
s_nop 0
buffer_load_dwordx4 v19,s[40:43],s55 offen offset:128  lds
s_waitcnt vmcnt(24)
s_barrier
v_readfirstlane_b32 s53,v4
s_nop 3
s_add_u32 s53,s53,1
s_cmp_eq_u32 s53,0
s_cselect_b32 s53,1,s53
v_mov_b32 v4,s53
s_waitcnt vmcnt(20)
L 0
s_barrier
s_add_u32 s55,s50,786432
s_add_u32 m0,s57,98304
s_nop 0
buffer_load_dwordx4 v18,s[40:43],s55 offen offset:0  lds
s_add_u32 m0,s57,99328
s_nop 0
buffer_load_dwordx4 v19,s[40:43],s55 offen offset:0  lds
s_add_u32 m0,s57,106368
s_nop 0
buffer_load_dwordx4 v18,s[40:43],s55 offen offset:128  lds
s_add_u32 m0,s57,107392
s_nop 0
buffer_load_dwordx4 v19,s[40:43],s55 offen offset:128  lds
v_add_u32 v26,0,v20
v_add_u32 v27,0,v21
T a[0:1],26,0
T a[2:3],26,512
T a[4:5],27,0
T a[6:7],27,512
T a[8:9],26,4096
T a[10:11],26,4608
T a[12:13],27,4096
T a[14:15],27,4608
s_waitcnt vmcnt(20)
L 0
s_barrier
s_add_u32 s55,s50,917504
s_add_u32 m0,s57,0
s_nop 0
buffer_load_dwordx4 v18,s[40:43],s55 offen offset:0  lds
s_add_u32 m0,s57,1024
s_nop 0
buffer_load_dwordx4 v19,s[40:43],s55 offen offset:0  lds
s_add_u32 m0,s57,8064
s_nop 0
buffer_load_dwordx4 v18,s[40:43],s55 offen offset:128  lds
s_add_u32 m0,s57,9088
s_nop 0
buffer_load_dwordx4 v19,s[40:43],s55 offen offset:128  lds
v_add_u32 v26,16384,v20
v_add_u32 v27,16384,v21
T a[16:17],26,0
T a[18:19],26,512
T a[20:21],27,0
T a[22:23],27,512
T a[24:25],26,4096
T a[26:27],26,4608
T a[28:29],27,4096
T a[30:31],27,4608
s_waitcnt vmcnt(20)
L 0
s_barrier
s_add_u32 s55,s50,1048576
s_add_u32 m0,s57,16384
s_nop 0
buffer_load_dwordx4 v18,s[40:43],s55 offen offset:0  lds
s_add_u32 m0,s57,17408
s_nop 0
buffer_load_dwordx4 v19,s[40:43],s55 offen offset:0  lds
s_add_u32 m0,s57,24448
s_nop 0
buffer_load_dwordx4 v18,s[40:43],s55 offen offset:128  lds
s_add_u32 m0,s57,25472
s_nop 0
buffer_load_dwordx4 v19,s[40:43],s55 offen offset:128  lds
v_add_u32 v26,32768,v20
v_add_u32 v27,32768,v21
T a[32:33],26,0
T a[34:35],26,512
T a[36:37],27,0
T a[38:39],27,512
T a[40:41],26,4096
T a[42:43],26,4608
T a[44:45],27,4096
T a[46:47],27,4608
s_waitcnt vmcnt(20)
L 0
s_barrier
s_add_u32 s55,s50,1179648
s_add_u32 m0,s57,32768
s_nop 0
buffer_load_dwordx4 v18,s[40:43],s55 offen offset:0  lds
s_add_u32 m0,s57,33792
s_nop 0
buffer_load_dwordx4 v19,s[40:43],s55 offen offset:0  lds
s_add_u32 m0,s57,40832
s_nop 0
buffer_load_dwordx4 v18,s[40:43],s55 offen offset:128  lds
s_add_u32 m0,s57,41856
s_nop 0
buffer_load_dwordx4 v19,s[40:43],s55 offen offset:128  lds
v_add_u32 v26,49152,v20
v_add_u32 v27,49152,v21
T a[48:49],26,0
T a[50:51],26,512
T a[52:53],27,0
T a[54:55],27,512
T a[56:57],26,4096
T a[58:59],26,4608
T a[60:61],27,4096
T a[62:63],27,4608
s_waitcnt vmcnt(20)
L 0
s_barrier
s_add_u32 s55,s50,1310720
s_add_u32 m0,s57,49152
s_nop 0
buffer_load_dwordx4 v18,s[40:43],s55 offen offset:0  lds
s_add_u32 m0,s57,50176
s_nop 0
buffer_load_dwordx4 v19,s[40:43],s55 offen offset:0  lds
s_add_u32 m0,s57,57216
s_nop 0
buffer_load_dwordx4 v18,s[40:43],s55 offen offset:128  lds
s_add_u32 m0,s57,58240
s_nop 0
buffer_load_dwordx4 v19,s[40:43],s55 offen offset:128  lds
v_add_u32 v26,65536,v20
v_add_u32 v27,65536,v21
T a[64:65],26,0
T a[66:67],26,512
T a[68:69],27,0
T a[70:71],27,512
T a[72:73],26,4096
T a[74:75],26,4608
T a[76:77],27,4096
T a[78:79],27,4608
s_waitcnt vmcnt(20)
L 0
s_barrier
s_add_u32 s55,s50,1441792
s_add_u32 m0,s57,65536
s_nop 0
buffer_load_dwordx4 v18,s[40:43],s55 offen offset:0  lds
s_add_u32 m0,s57,66560
s_nop 0
buffer_load_dwordx4 v19,s[40:43],s55 offen offset:0  lds
s_add_u32 m0,s57,73600
s_nop 0
buffer_load_dwordx4 v18,s[40:43],s55 offen offset:128  lds
s_add_u32 m0,s57,74624
s_nop 0
buffer_load_dwordx4 v19,s[40:43],s55 offen offset:128  lds
v_add_u32 v26,81920,v20
v_add_u32 v27,81920,v21
T a[80:81],26,0
T a[82:83],26,512
T a[84:85],27,0
T a[86:87],27,512
T a[88:89],26,4096
T a[90:91],26,4608
T a[92:93],27,4096
T a[94:95],27,4608
s_waitcnt vmcnt(20)
L 0
s_barrier
s_add_u32 s55,s50,1572864
s_add_u32 m0,s57,81920
s_nop 0
buffer_load_dwordx4 v18,s[40:43],s55 offen offset:0  lds
s_add_u32 m0,s57,82944
s_nop 0
buffer_load_dwordx4 v19,s[40:43],s55 offen offset:0  lds
s_add_u32 m0,s57,89984
s_nop 0
buffer_load_dwordx4 v18,s[40:43],s55 offen offset:128  lds
s_add_u32 m0,s57,91008
s_nop 0
buffer_load_dwordx4 v19,s[40:43],s55 offen offset:128  lds
v_add_u32 v26,98304,v20
v_add_u32 v27,98304,v21
T a[96:97],26,0
T a[98:99],26,512
T a[100:101],27,0
T a[102:103],27,512
T a[104:105],26,4096
T a[106:107],26,4608
T a[108:109],27,4096
T a[110:111],27,4608
s_waitcnt vmcnt(20)
L 0
s_barrier
s_add_u32 s55,s50,1703936
s_add_u32 m0,s57,98304
s_nop 0
buffer_load_dwordx4 v18,s[40:43],s55 offen offset:0  lds
s_add_u32 m0,s57,99328
s_nop 0
buffer_load_dwordx4 v19,s[40:43],s55 offen offset:0  lds
s_add_u32 m0,s57,106368
s_nop 0
buffer_load_dwordx4 v18,s[40:43],s55 offen offset:128  lds
s_add_u32 m0,s57,107392
s_nop 0
buffer_load_dwordx4 v19,s[40:43],s55 offen offset:128  lds
v_add_u32 v26,0,v20
v_add_u32 v27,0,v21
T a[112:113],26,0
T a[114:115],26,512
T a[116:117],27,0
T a[118:119],27,512
T a[120:121],26,4096
T a[122:123],26,4608
T a[124:125],27,4096
T a[126:127],27,4608
s_waitcnt vmcnt(20)
L 0
s_barrier
s_add_u32 s55,s50,1835008
s_add_u32 m0,s57,0
s_nop 0
buffer_load_dwordx4 v18,s[40:43],s55 offen offset:0  lds
s_add_u32 m0,s57,1024
s_nop 0
buffer_load_dwordx4 v19,s[40:43],s55 offen offset:0  lds
s_add_u32 m0,s57,8064
s_nop 0
buffer_load_dwordx4 v18,s[40:43],s55 offen offset:128  lds
s_add_u32 m0,s57,9088
s_nop 0
buffer_load_dwordx4 v19,s[40:43],s55 offen offset:128  lds
v_add_u32 v26,16384,v20
v_add_u32 v27,16384,v21
T a[128:129],26,0
T a[130:131],26,512
T a[132:133],27,0
T a[134:135],27,512
T a[136:137],26,4096
T a[138:139],26,4608
T a[140:141],27,4096
T a[142:143],27,4608
s_waitcnt vmcnt(20)
L 0
s_barrier
s_add_u32 s55,s50,1966080
s_add_u32 m0,s57,16384
s_nop 0
buffer_load_dwordx4 v18,s[40:43],s55 offen offset:0  lds
s_add_u32 m0,s57,17408
s_nop 0
buffer_load_dwordx4 v19,s[40:43],s55 offen offset:0  lds
s_add_u32 m0,s57,24448
s_nop 0
buffer_load_dwordx4 v18,s[40:43],s55 offen offset:128  lds
s_add_u32 m0,s57,25472
s_nop 0
buffer_load_dwordx4 v19,s[40:43],s55 offen offset:128  lds
v_add_u32 v26,32768,v20
v_add_u32 v27,32768,v21
T a[144:145],26,0
T a[146:147],26,512
T a[148:149],27,0
T a[150:151],27,512
T a[152:153],26,4096
T a[154:155],26,4608
T a[156:157],27,4096
T a[158:159],27,4608
s_waitcnt vmcnt(20)
L 0
s_barrier
v_add_u32 v26,49152,v20
v_add_u32 v27,49152,v21
T a[160:161],26,0
T a[162:163],26,512
T a[164:165],27,0
T a[166:167],27,512
T a[168:169],26,4096
T a[170:171],26,4608
T a[172:173],27,4096
T a[174:175],27,4608
s_waitcnt vmcnt(16)
L 0
s_barrier
v_add_u32 v26,65536,v20
v_add_u32 v27,65536,v21
T a[176:177],26,0
T a[178:179],26,512
T a[180:181],27,0
T a[182:183],27,512
T a[184:185],26,4096
T a[186:187],26,4608
T a[188:189],27,4096
T a[190:191],27,4608
s_waitcnt vmcnt(12)
L 0
s_barrier
v_add_u32 v26,81920,v20
v_add_u32 v27,81920,v21
T a[192:193],26,0
T a[194:195],26,512
T a[196:197],27,0
T a[198:199],27,512
T a[200:201],26,4096
T a[202:203],26,4608
T a[204:205],27,4096
T a[206:207],27,4608
s_waitcnt vmcnt(8)
L 0
s_barrier
v_add_u32 v26,98304,v20
v_add_u32 v27,98304,v21
T a[208:209],26,0
T a[210:211],26,512
T a[212:213],27,0
T a[214:215],27,512
T a[216:217],26,4096
T a[218:219],26,4608
T a[220:221],27,4096
T a[222:223],27,4608
s_waitcnt vmcnt(4)
L 0
s_barrier
v_add_u32 v26,0,v20
v_add_u32 v27,0,v21
T a[224:225],26,0
T a[226:227],26,512
T a[228:229],27,0
T a[230:231],27,512
T a[232:233],26,4096
T a[234:235],26,4608
T a[236:237],27,4096
T a[238:239],27,4608
s_waitcnt vmcnt(0)
L 0
s_barrier
v_add_u32 v26,16384,v20
v_add_u32 v27,16384,v21
T a[240:241],26,0
T a[242:243],26,512
T a[244:245],27,0
T a[246:247],27,512
T a[248:249],26,4096
T a[250:251],26,4608
T a[252:253],27,4096
T a[254:255],27,4608
L 0
s_barrier
v_mov_b32 v4,s53
s_mov_b32 s82,0
s_mov_b32 s80,0
s_mov_b32 s54,s45
s_mul_i32 s57,s44,10240
s_mul_i32 s55,s45,40960
s_add_u32 s55,s55,s57
s_add_u32 s55,s55,17825792
s_mul_i32 s57,s44,8192
s_mul_i32 s60,s46,40960
s_mul_i32 s61,s46,1024
s_mul_i32 s62,s46,32768
s_mul_i32 s63,s46,1024
s_mul_i32 s64,s46,512
s_lshl_b32 s59,s44,8
s_add_u32 s59,s59,131072
s_mul_i32 s58,s45,1024
s_add_u32 s58,s58,109333504
s_mul_i32 s65,s45,32768
s_add_u32 s65,s65,s47
s_mul_i32 s68,s45,1024
s_add_u32 s68,s68,s48
s_mul_i32 s73,s45,512
s_add_u32 s73,s73,106954752
s_mul_i32 s74,s45,32768
s_add_u32 s74,s74,s49
s_mul_i32 s75,s45,1024
s_add_u32 s75,s75,107479040
s_mov_b32 s66,0x7fff0000
s_mov_b32 s67,0x7fff0000
s_mov_b32 s69,0x7fff0000
s_mov_b32 s76,0x7fff0000
s_mov_b32 s77,0x7fff0000
s_mov_b32 s70,0x7fff0000
s_mov_b32 s71,0x7fff0000
s_mov_b32 s72,0x7fff0000
v_mov_b32 v1,v0
v_add_u32 v2,32768,v0
s_mov_b32 s84,32768
s_mov_b32 s86,0x3a800000
s_mov_b32 s87,0x38a7c5ac
s_mov_b32 s88,0xc0135761
s_mov_b32 s89,0x3d372713
s_and_b32 s85,s44,1
s_mul_i32 s85,s85,127
v_lshlrev_b32 v8,22,v0
v_and_b32 v8,0x80000000,v8
v_or_b32 v8,v8,v5
s_getpc_b64 s[40:41]
Lg2_pc_%=:
s_lshl_b32 s78,s44,13
v_lshlrev_b32 v106,3,v0
v_add_u32 v106,s78,v106
v_min_u32 v106,Lcode_g12_e-Lg2_pc_%=-4,v106
s_mov_b32 m0,163584
s_nop 0
global_load_lds_dword v106,s[40:41]
s_and_b32 s79,s54,127
s_cmp_eq_u32 s79,0
s_cselect_b32 s94,0x3300,0
s_cmp_eq_u32 s79,127
s_cselect_b32 s94,0xcc00,s94
s_add_u32 s79,s54,s46
s_and_b32 s79,s79,127
s_cmp_eq_u32 s79,0
s_cselect_b32 s95,0x3300,0
s_cmp_eq_u32 s79,127
s_cselect_b32 s95,0xcc00,s95
s_add_u32 m0,s59,4096
s_nop 0
buffer_load_dword v5,s[36:39],s58 offen offset:0 sc1 lds
buffer_store_dword v4,v3,s[36:39],s71 offen offset:0 sc0 sc1
s_add_u32 m0,s59,5120
s_nop 0
buffer_load_dword v5,s[36:39],s58 offen offset:0 sc1 lds
buffer_store_dword v4,v3,s[36:39],s71 offen offset:0 sc0 sc1
s_mov_b32 s83,0
Lg2_poll_%=:
s_add_u32 s78,s83,2
s_and_b32 s78,s78,3
s_lshl_b32 s78,s78,10
s_add_u32 s78,s78,4096
s_add_u32 m0,s59,s78
s_nop 0
buffer_load_dword v5,s[36:39],s58 offen offset:0 sc1 lds
s_and_b32 s78,s83,3
s_lshl_b32 s78,s78,10
v_add_u32 v106,s78,v6
s_waitcnt vmcnt(4)
s_barrier
ds_read_b32 v7,v106 offset:4096
buffer_store_dword v4,v3,s[36:39],s70 offen offset:0 sc0 sc1
s_mov_b32 s70,s71
s_mov_b32 s71,s72
s_mov_b32 s72,0x7fff0000
s_add_u32 s83,s83,1
s_add_u32 s82,s82,1
L 0
v_cmp_eq_u32 vcc,s53,v7
s_nop 0
s_or_b64 vcc,vcc,s[94:95]
s_cmp_eq_u32 vcc_lo,-1
s_cbranch_scc1 Lg2_go_%=
s_cmp_lt_u32 s82,20000
s_cbranch_scc1 Lg2_poll_%=
Lg2_go_%=:
s_and_b32 s78,s54,127
s_cmp_eq_u32 s78,s85
s_cselect_b64 s[40:41],-1,0
v_cndmask_b32 v17,v15,v16,s[40:41]
s_add_u32 m0,s57,0
s_add_u32 s56,s55,0
D 17,56,0
s_add_u32 s56,s55,256
D 17,56,1024
s_add_u32 s56,s55,512
D 17,56,2048
)"
R"(s_add_u32 s56,s55,768
D 17,56,3072
s_add_u32 m0,s57,4096
s_add_u32 s56,s55,5120
D 17,56,0
s_add_u32 s56,s55,5376
D 17,56,1024
s_add_u32 s56,s55,5632
D 17,56,2048
s_add_u32 s56,s55,5888
D 17,56,3072
s_add_u32 s55,s55,s60
s_add_u32 s57,s57,32768
s_and_b32 s57,s57,131071
s_add_u32 s54,s54,s46
Lg2_gox_%=:
s_add_u32 s58,s58,s61
s_cmp_eq_u32 vcc_hi,-1
s_cbranch_scc1 Lg2_go2_%=
s_add_u32 m0,s59,2048
s_nop 0
buffer_load_dword v5,s[36:39],s58 offen offset:0 sc1 lds
s_and_b32 s79,s54,127
s_cmp_eq_u32 s79,0
s_cselect_b32 s94,0x3300,0
s_cmp_eq_u32 s79,127
s_cselect_b32 s94,0xcc00,s94
s_add_u32 s79,s54,s46
s_and_b32 s79,s79,127
s_cmp_eq_u32 s79,0
s_cselect_b32 s95,0x3300,0
s_cmp_eq_u32 s79,127
s_cselect_b32 s95,0xcc00,s95
s_waitcnt vmcnt(0)
s_barrier
buffer_store_dword v4,v3,s[36:39],s70 offen offset:0 sc0 sc1
s_min_u32 s71,s71,s72
buffer_store_dword v4,v3,s[36:39],s71 offen offset:0 sc0 sc1
s_mov_b32 s70,0x7fff0000
s_mov_b32 s71,0x7fff0000
s_mov_b32 s72,0x7fff0000
s_cmp_eq_u32 s80,1
s_cbranch_scc1 Lg2_st11_%=
Lg2_st10_%=:
ds_read_b32 v7,v6 offset:2048
R 34,1,0
R 38,1,1024
R 42,1,2048
R 46,1,3072
R 50,1,4096
R 54,1,5120
R 58,1,6144
L 7
v_cmp_eq_u32 vcc,s53,v7
s_nop 0
s_or_b64 vcc,vcc,s[94:95]
L 5
s_cmp_eq_u64 vcc,-1
s_cbranch_scc1 Lg2_L20_%=
s_cmp_eq_u32 vcc_lo,-1
s_cbranch_scc1 Lg2_L10_%=
s_branch Lg2_X0_%=
Lg2_st11_%=:
ds_read_b32 v7,v6 offset:2048
R 34,2,0
R 38,2,1024
R 42,2,2048
R 46,2,3072
R 50,2,4096
R 54,2,5120
R 58,2,6144
L 7
v_cmp_eq_u32 vcc,s53,v7
s_nop 0
s_or_b64 vcc,vcc,s[94:95]
L 5
s_cmp_eq_u64 vcc,-1
s_cbranch_scc1 Lg2_L21_%=
s_cmp_eq_u32 vcc_lo,-1
s_cbranch_scc1 Lg2_L11_%=
s_branch Lg2_X1_%=
Lg2_go2_%=:
s_add_u32 s58,s58,s61
s_add_u32 m0,s59,2048
s_nop 0
buffer_load_dword v5,s[36:39],s58 offen offset:0 sc1 lds
s_and_b32 s78,s54,127
s_cmp_eq_u32 s78,s85
s_cselect_b64 s[40:41],-1,0
v_cndmask_b32 v17,v15,v16,s[40:41]
s_add_u32 m0,s57,0
s_add_u32 s56,s55,0
D 17,56,0
s_add_u32 s56,s55,256
D 17,56,1024
s_add_u32 s56,s55,512
D 17,56,2048
s_add_u32 s56,s55,768
D 17,56,3072
s_add_u32 m0,s57,4096
s_add_u32 s56,s55,5120
D 17,56,0
s_add_u32 s56,s55,5376
D 17,56,1024
s_add_u32 s56,s55,5632
D 17,56,2048
s_add_u32 s56,s55,5888
D 17,56,3072
s_add_u32 s55,s55,s60
s_add_u32 s57,s57,32768
s_and_b32 s57,s57,131071
s_add_u32 s54,s54,s46
s_and_b32 s79,s54,127
s_cmp_eq_u32 s79,0
s_cselect_b32 s94,0x3300,0
s_cmp_eq_u32 s79,127
s_cselect_b32 s94,0xcc00,s94
s_add_u32 s79,s54,s46
s_and_b32 s79,s79,127
s_cmp_eq_u32 s79,0
s_cselect_b32 s95,0x3300,0
s_cmp_eq_u32 s79,127
s_cselect_b32 s95,0xcc00,s95
s_waitcnt vmcnt(8)
s_barrier
buffer_store_dword v4,v3,s[36:39],s70 offen offset:0 sc0 sc1
s_min_u32 s71,s71,s72
buffer_store_dword v4,v3,s[36:39],s71 offen offset:0 sc0 sc1
s_mov_b32 s70,0x7fff0000
s_mov_b32 s71,0x7fff0000
s_mov_b32 s72,0x7fff0000
s_cmp_eq_u32 s80,1
s_cbranch_scc1 Lg2_st21_%=
Lg2_st20_%=:
ds_read_b32 v7,v6 offset:2048
R 34,1,0
R 38,1,1024
R 42,1,2048
R 46,1,3072
R 50,1,4096
R 54,1,5120
R 58,1,6144
L 7
v_cmp_eq_u32 vcc,s53,v7
s_nop 0
s_or_b64 vcc,vcc,s[94:95]
L 5
s_cmp_eq_u32 vcc_lo,-1
s_cbranch_scc1 Lg2_S0_%=
s_branch Lg2_N0_%=
Lg2_st21_%=:
ds_read_b32 v7,v6 offset:2048
R 34,2,0
R 38,2,1024
R 42,2,2048
R 46,2,3072
R 50,2,4096
R 54,2,5120
R 58,2,6144
L 7
v_cmp_eq_u32 vcc,s53,v7
s_nop 0
s_or_b64 vcc,vcc,s[94:95]
L 5
s_cmp_eq_u32 vcc_lo,-1
s_cbranch_scc1 Lg2_S1_%=
s_branch Lg2_N1_%=
Lg2_L20_%=:
v_mfma_f32_16x16x32_bf16 v[22:25],a[4:7],v[34:37],0
s_add_u32 s58,s58,s61
s_and_b32 s78,s54,127
s_add_u32 s58,s58,s61
R 62,1,7168
M 22,a[12:15],38
s_add_u32 m0,s59,0
s_cmp_eq_u32 s78,s85
s_cselect_b64 s[40:41],-1,0
buffer_load_dwordx4 v[122:125],v0,s[36:39],s74 offen offset:0 sc1
v_mfma_f32_16x16x32_bf16 v[18:21],a[0:3],v[34:37],0
s_add_u32 s56,s55,0
R 66,1,8192
v_cndmask_b32 v17,v15,v16,s[40:41]
M 18,a[8:11],38
L 5
buffer_load_dword v8,s[36:39],s58 offen offset:0 sc1 lds
M 18,a[16:19],42
s_add_u32 m0,s57,0
R 70,1,9216
v_lshlrev_b32 v106,16,v126
M 18,a[24:27],46
s_add_u32 s54,s54,s46
buffer_load_dwordx2 v[98:99],v11,s[36:39],s73 offen offset:256 sc1
M 22,a[20:23],42
s_and_b32 s78,s54,127
R 74,1,10240
v_and_b32 v107,0xffff0000,v126
M 22,a[28:31],46
L 5
D 17,56,0
M 22,a[36:39],50
s_add_u32 s56,s55,256
R 78,1,11264
v_lshlrev_b32 v108,16,v127
M 22,a[44:47],54
s_cmp_eq_u32 s78,s85
v_and_b32 v109,0xffff0000,v127
v_lshlrev_b32 v110,16,v128
M 18,a[32:35],50
s_cselect_b64 s[40:41],-1,0
R 82,1,12288
v_and_b32 v111,0xffff0000,v128
M 18,a[40:43],54
L 5
D 17,56,1024
M 18,a[48:51],58
s_add_u32 s56,s55,512
R 86,1,13312
v_lshlrev_b32 v112,16,v129
M 18,a[56:59],62
s_add_u32 s54,s54,s46
v_and_b32 v113,0xffff0000,v129
v_fma_f32 v106,v106,v103,v102
M 22,a[52:55],58
s_and_b32 s79,s54,127
R 90,1,14336
v_fma_f32 v107,v107,v103,v102
M 22,a[60:63],62
L 5
D 17,56,2048
M 22,a[68:71],66
s_add_u32 s56,s55,768
R 94,1,15360
v_fma_f32 v108,v108,v103,v102
M 22,a[76:79],70
s_cmp_eq_u32 s79,0
v_fma_f32 v109,v109,v103,v102
v_fma_f32 v110,v110,v103,v102
M 18,a[64:67],66
s_cselect_b32 s94,0x3300,0
R 34,1,16384
v_fma_f32 v111,v111,v103,v102
M 18,a[72:75],70
L 5
D 17,56,3072
M 18,a[80:83],74
s_add_u32 m0,s57,4096
s_add_u32 s56,s55,5120
R 38,1,17408
M 18,a[88:91],78
s_add_u32 s57,s57,32768
v_fma_f32 v112,v112,v103,v102
v_fma_f32 v113,v113,v103,v102
M 22,a[84:87],74
s_and_b32 s57,s57,131071
R 42,1,18432
v_add_f32 v26,v26,v106
M 22,a[92:95],78
L 5
D 17,56,0
M 22,a[100:103],82
s_add_u32 s56,s55,5376
R 46,1,19456
v_add_f32 v27,v27,v107
M 22,a[108:111],86
s_cmp_eq_u32 s79,127
v_add_f32 v28,v28,v108
v_add_f32 v29,v29,v109
M 18,a[96:99],82
s_cselect_b32 s94,0xcc00,s94
R 50,1,20480
v_fma_f32 v107,v26,v26,s87
M 18,a[104:107],86
L 5
D 17,56,1024
M 18,a[112:115],90
s_add_u32 s56,s55,5632
R 54,1,21504
v_add_f32 v106,v26,v27
M 18,a[120:123],94
s_add_u32 s79,s54,s46
v_fma_f32 v107,v27,v27,v107
v_add_f32 v106,v106,v28
M 22,a[116:119],90
s_and_b32 s79,s79,127
R 58,1,22528
v_fma_f32 v107,v28,v28,v107
M 22,a[124:127],94
L 5
D 17,56,2048
M 22,a[132:135],34
s_add_u32 s56,s55,5888
s_add_u32 s55,s55,s60
R 62,1,23552
M 22,a[140:143],38
s_cmp_eq_u32 s79,0
v_add_f32 v30,v30,v110
v_add_f32 v106,v106,v29
M 18,a[128:131],34
s_cselect_b32 s95,0x3300,0
R 66,1,24576
v_fma_f32 v107,v29,v29,v107
M 18,a[136:139],38
L 5
D 17,56,3072
M 18,a[144:147],42
s_add_u32 m0,s57,0
s_add_u32 s56,s55,0
R 70,1,25600
M 18,a[152:155],46
s_cmp_eq_u32 s79,127
v_add_f32 v31,v31,v111
v_add_f32 v32,v32,v112
v_add_f32 v33,v33,v113
v_add_f32 v106,v106,v30
v_fma_f32 v107,v30,v30,v107
v_add_f32 v106,v106,v31
v_fma_f32 v107,v31,v31,v107
v_add_f32 v106,v106,v32
v_fma_f32 v107,v32,v32,v107
v_add_f32 v106,v106,v33
v_fma_f32 v107,v33,v33,v107
M 22,a[148:151],42
s_cselect_b32 s95,0xcc00,s95
R 74,1,26624
v_cndmask_b32 v17,v15,v16,s[40:41]
M 22,a[156:159],46
L 5
v_permlane16_swap_b32 v106,v107
v_add_f32 v106,v106,v107
v_mov_b32 v107,v106
M 22,a[164:167],50
s_mov_b32 s66,s65
R 78,1,27648
v_cvt_pk_bf16_f32 v114,v26,v27
M 22,a[172:175],54
s_mov_b32 s76,s75
v_permlane32_swap_b32 v106,v107
v_add_f32 v106,v106,v107
M 18,a[160:163],50
s_add_u32 s73,s73,s64
R 82,1,28672
ds_write_b32 v12,v106 offset:2048
M 18,a[168:171],54
L 6
D 17,56,0
M 18,a[176:179],58
s_add_u32 s56,s55,256
R 86,1,29696
v_cvt_pk_bf16_f32 v115,v28,v29
M 18,a[184:187],62
s_add_u32 s74,s74,s62
D 17,56,1024
M 22,a[180:183],58
s_add_u32 s56,s55,512
R 90,1,30720
v_cvt_pk_bf16_f32 v116,v30,v31
M 22,a[188:191],62
L 6
L 2
D 17,56,2048
M 22,a[196:199],66
s_waitcnt vmcnt(3)
s_add_u32 s56,s55,768
R 94,1,31744
M 22,a[204:207],70
s_barrier
D 17,56,3072
M 18,a[192:195],66
s_add_u32 m0,s57,4096
s_add_u32 s56,s55,5120
R 34,2,0
M 18,a[200:203],70
s_nop 0
buffer_store_dword v4,v3,s[36:39],s70 offen offset:0 sc0 sc1
M 18,a[208:211],74
s_mov_b32 s70,s69
R 38,2,1024
R 118,13,2048
M 18,a[216:219],78
s_add_u32 s57,s57,32768
D 17,56,0
M 22,a[212:215],74
s_add_u32 s56,s55,5376
R 42,2,2048
ds_read_b32 v7,v6 offset:0
M 22,a[220:223],78
L 7
D 17,56,1024
M 22,a[228:231],82
s_add_u32 s56,s55,5632
R 46,2,3072
v_cvt_pk_bf16_f32 v117,v32,v33
M 22,a[236:239],86
s_mov_b32 s69,s68
D 17,56,2048
M 18,a[224:227],82
s_add_u32 s56,s55,5888
R 50,2,4096
v_xor_b32 v1,65536,v1
M 18,a[232:235],86
L 7
L 4
buffer_store_dwordx4 v[114:117],v0,s[36:39],s67 offen offset:0 sc0 sc1
M 18,a[240:243],90
L 2
R 54,2,5120
v_add_f32 v118,v118,v119
M 18,a[248:251],94
s_add_u32 s55,s55,s60
v_cmp_eq_u32 vcc,s53,v7
v_add_f32 v120,v120,v121
M 22,a[244:247],90
s_and_b32 s57,s57,131071
s_add_u32 s65,s65,s62
s_add_u32 s75,s75,s63
s_add_u32 s68,s68,s63
s_or_b64 vcc,vcc,s[94:95]
s_cmp_eq_u32 vcc_lo,-1
R 58,2,6144
v_add_f32 v118,v118,v120
v_mul_f32 v118,s86,v118
M 22,a[252:255],94
s_nop 0
D 17,56,3072
buffer_store_dword v118,v14,s[36:39],s77 offen offset:0 sc0 sc1
s_cbranch_scc0 Lg2_N1_%=
Lg2_S1_%=:
v_mfma_f32_16x16x32_bf16 v[30:33],a[4:7],v[34:37],0
s_add_u32 s58,s58,s61
R 62,2,7168
v_lshlrev_b32 v106,16,v122
M 30,a[12:15],38
s_add_u32 m0,s59,1024
buffer_load_dwordx4 v[126:129],v0,s[36:39],s74 offen offset:0 sc1
v_mfma_f32_16x16x32_bf16 v[26:29],a[0:3],v[34:37],0
s_and_b32 s78,s54,127
R 66,2,8192
v_and_b32 v107,0xffff0000,v122
M 26,a[8:11],38
L 5
buffer_load_dword v8,s[36:39],s58 offen offset:0 sc1 lds
M 26,a[16:19],42
s_cmp_eq_u32 s78,s85
R 70,2,9216
v_lshlrev_b32 v108,16,v123
M 26,a[24:27],46
s_cselect_b64 s[40:41],-1,0
buffer_load_dwordx2 v[102:103],v11,s[36:39],s73 offen offset:256 sc1
M 30,a[20:23],42
s_add_u32 m0,s57,0
R 74,2,10240
v_cndmask_b32 v17,v15,v16,s[40:41]
M 30,a[28:31],46
L 5
s_add_u32 s56,s55,0
D 17,56,0
M 30,a[36:39],50
s_add_u32 s56,s55,256
R 78,2,11264
v_and_b32 v109,0xffff0000,v123
M 30,a[44:47],54
s_add_u32 s54,s54,s46
v_lshlrev_b32 v110,16,v124
v_and_b32 v111,0xffff0000,v124
M 26,a[32:35],50
s_and_b32 s79,s54,127
R 82,2,12288
v_lshlrev_b32 v112,16,v125
M 26,a[40:43],54
L 5
v_and_b32 v113,0xffff0000,v125
v_fma_f32 v106,v106,v99,v98
M 26,a[48:51],58
s_cmp_eq_u32 s79,0
R 86,2,13312
v_fma_f32 v107,v107,v99,v98
M 26,a[56:59],62
s_cselect_b32 s94,0x3300,0
D 17,56,1024
M 30,a[52:55],58
s_add_u32 s56,s55,512
R 90,2,14336
v_fma_f32 v108,v108,v99,v98
M 30,a[60:63],62
L 5
v_fma_f32 v109,v109,v99,v98
v_fma_f32 v110,v110,v99,v98
M 30,a[68:71],66
s_cmp_eq_u32 s79,127
R 94,2,15360
v_fma_f32 v111,v111,v99,v98
M 30,a[76:79],70
s_cselect_b32 s94,0xcc00,s94
v_fma_f32 v112,v112,v99,v98
v_fma_f32 v113,v113,v99,v98
M 26,a[64:67],66
s_add_u32 s79,s54,s46
R 34,2,16384
v_add_f32 v18,v18,v106
M 26,a[72:75],70
)"
R"(L 5
v_add_f32 v19,v19,v107
v_add_f32 v20,v20,v108
M 26,a[80:83],74
s_and_b32 s79,s79,127
R 38,2,17408
v_add_f32 v21,v21,v109
M 26,a[88:91],78
s_cmp_eq_u32 s79,0
D 17,56,2048
M 30,a[84:87],74
s_cselect_b32 s95,0x3300,0
R 42,2,18432
v_add_f32 v22,v22,v110
M 30,a[92:95],78
L 5
v_add_f32 v23,v23,v111
v_add_f32 v24,v24,v112
M 30,a[100:103],82
s_add_u32 s56,s55,768
R 46,2,19456
v_add_f32 v25,v25,v113
M 30,a[108:111],86
s_cmp_eq_u32 s79,127
v_cvt_pk_bf16_f32 v114,v18,v19
v_cvt_pk_bf16_f32 v115,v20,v21
M 26,a[96:99],82
s_cselect_b32 s95,0xcc00,s95
R 50,2,20480
v_fma_f32 v107,v18,v18,s87
M 26,a[104:107],86
L 5
D 17,56,3072
M 26,a[112:115],90
s_add_u32 m0,s57,4096
R 54,2,21504
v_add_f32 v106,v18,v19
M 26,a[120:123],94
s_add_u32 s56,s55,5120
v_fma_f32 v107,v19,v19,v107
v_add_f32 v106,v106,v20
M 30,a[116:119],90
s_add_u32 s57,s57,32768
R 58,2,22528
v_fma_f32 v107,v20,v20,v107
M 30,a[124:127],94
L 5
v_add_f32 v106,v106,v21
v_fma_f32 v107,v21,v21,v107
M 30,a[132:135],34
s_mov_b32 s67,s65
R 62,2,23552
v_add_f32 v106,v106,v22
M 30,a[140:143],38
s_mov_b32 s77,s75
v_fma_f32 v107,v22,v22,v107
v_add_f32 v106,v106,v23
M 26,a[128:131],34
s_add_u32 s73,s73,s64
R 66,2,24576
v_fma_f32 v107,v23,v23,v107
M 26,a[136:139],38
L 5
D 17,56,0
M 26,a[144:147],42
s_add_u32 s56,s55,5376
R 70,2,25600
v_add_f32 v106,v106,v24
M 26,a[152:155],46
s_add_u32 s74,s74,s62
v_fma_f32 v107,v24,v24,v107
v_add_f32 v106,v106,v25
v_fma_f32 v107,v25,v25,v107
M 30,a[148:151],42
s_and_b32 s57,s57,131071
R 74,2,26624
v_cvt_pk_bf16_f32 v116,v22,v23
M 30,a[156:159],46
L 5
v_permlane16_swap_b32 v106,v107
v_add_f32 v106,v106,v107
v_mov_b32 v107,v106
M 30,a[164:167],50
s_add_u32 s65,s65,s62
R 78,2,27648
v_cvt_pk_bf16_f32 v117,v24,v25
M 30,a[172:175],54
s_add_u32 s75,s75,s63
v_permlane32_swap_b32 v106,v107
v_add_f32 v106,v106,v107
M 26,a[160:163],50
R 82,2,28672
ds_write_b32 v12,v106 offset:0
M 26,a[168:171],54
L 6
D 17,56,1024
M 26,a[176:179],58
s_add_u32 s56,s55,5632
R 86,2,29696
M 26,a[184:187],62
buffer_store_dwordx4 v[114:117],v0,s[36:39],s66 offen offset:0 sc0 sc1
M 30,a[180:183],58
R 90,2,30720
M 30,a[188:191],62
L 6
L 2
M 30,a[196:199],66
s_waitcnt vmcnt(7)
R 94,2,31744
v_xor_b32 v2,65536,v2
M 30,a[204:207],70
s_barrier
D 17,56,2048
M 26,a[192:195],66
s_add_u32 s56,s55,5888
R 34,1,0
R 118,13,0
M 26,a[200:203],70
s_nop 0
buffer_store_dword v4,v3,s[36:39],s70 offen offset:0 sc0 sc1
M 26,a[208:211],74
s_mov_b32 s70,s69
R 38,1,1024
ds_read_b32 v7,v6 offset:1024
M 26,a[216:219],78
s_mov_b32 s69,s68
s_add_u32 s55,s55,s60
s_add_u32 s68,s68,s63
M 30,a[212:215],74
R 42,1,2048
M 30,a[220:223],78
L 7
D 17,56,3072
M 30,a[228:231],82
L 3
R 46,1,3072
v_add_f32 v118,v118,v119
M 30,a[236:239],86
v_add_f32 v120,v120,v121
v_add_f32 v118,v118,v120
M 26,a[224:227],82
L 2
R 50,1,4096
v_mul_f32 v118,s86,v118
M 26,a[232:235],86
s_nop 0
buffer_store_dword v118,v14,s[36:39],s76 offen offset:0 sc0 sc1
M 26,a[240:243],90
R 54,1,5120
v_cmp_eq_u32 vcc,s53,v7
M 26,a[248:251],94
s_or_b64 vcc,vcc,s[94:95]
s_cmp_eq_u32 vcc_lo,-1
M 30,a[244:247],90
R 58,1,6144
M 30,a[252:255],94
s_nop 0
s_cbranch_scc0 Lg2_N0_%=
Lg2_S0_%=:
v_mfma_f32_16x16x32_bf16 v[22:25],a[4:7],v[34:37],0
s_add_u32 s58,s58,s61
R 62,1,7168
v_lshlrev_b32 v106,16,v126
M 22,a[12:15],38
s_add_u32 m0,s59,0
buffer_load_dwordx4 v[122:125],v0,s[36:39],s74 offen offset:0 sc1
v_mfma_f32_16x16x32_bf16 v[18:21],a[0:3],v[34:37],0
s_and_b32 s78,s54,127
R 66,1,8192
v_and_b32 v107,0xffff0000,v126
M 18,a[8:11],38
L 5
buffer_load_dword v8,s[36:39],s58 offen offset:0 sc1 lds
M 18,a[16:19],42
s_cmp_eq_u32 s78,s85
R 70,1,9216
v_lshlrev_b32 v108,16,v127
M 18,a[24:27],46
s_cselect_b64 s[40:41],-1,0
buffer_load_dwordx2 v[98:99],v11,s[36:39],s73 offen offset:256 sc1
M 22,a[20:23],42
s_add_u32 m0,s57,0
R 74,1,10240
v_cndmask_b32 v17,v15,v16,s[40:41]
M 22,a[28:31],46
L 5
s_add_u32 s56,s55,0
D 17,56,0
M 22,a[36:39],50
s_add_u32 s56,s55,256
R 78,1,11264
v_and_b32 v109,0xffff0000,v127
M 22,a[44:47],54
s_add_u32 s54,s54,s46
v_lshlrev_b32 v110,16,v128
v_and_b32 v111,0xffff0000,v128
M 18,a[32:35],50
s_and_b32 s79,s54,127
R 82,1,12288
v_lshlrev_b32 v112,16,v129
M 18,a[40:43],54
L 5
v_and_b32 v113,0xffff0000,v129
v_fma_f32 v106,v106,v103,v102
M 18,a[48:51],58
s_cmp_eq_u32 s79,0
R 86,1,13312
v_fma_f32 v107,v107,v103,v102
M 18,a[56:59],62
s_cselect_b32 s94,0x3300,0
D 17,56,1024
M 22,a[52:55],58
s_add_u32 s56,s55,512
R 90,1,14336
v_fma_f32 v108,v108,v103,v102
M 22,a[60:63],62
L 5
v_fma_f32 v109,v109,v103,v102
v_fma_f32 v110,v110,v103,v102
M 22,a[68:71],66
s_cmp_eq_u32 s79,127
R 94,1,15360
v_fma_f32 v111,v111,v103,v102
M 22,a[76:79],70
s_cselect_b32 s94,0xcc00,s94
v_fma_f32 v112,v112,v103,v102
v_fma_f32 v113,v113,v103,v102
M 18,a[64:67],66
s_add_u32 s79,s54,s46
R 34,1,16384
v_add_f32 v26,v26,v106
M 18,a[72:75],70
L 5
v_add_f32 v27,v27,v107
v_add_f32 v28,v28,v108
M 18,a[80:83],74
s_and_b32 s79,s79,127
R 38,1,17408
v_add_f32 v29,v29,v109
M 18,a[88:91],78
s_cmp_eq_u32 s79,0
D 17,56,2048
M 22,a[84:87],74
s_cselect_b32 s95,0x3300,0
R 42,1,18432
v_add_f32 v30,v30,v110
M 22,a[92:95],78
L 5
v_add_f32 v31,v31,v111
v_add_f32 v32,v32,v112
M 22,a[100:103],82
s_add_u32 s56,s55,768
R 46,1,19456
v_add_f32 v33,v33,v113
M 22,a[108:111],86
s_cmp_eq_u32 s79,127
v_cvt_pk_bf16_f32 v114,v26,v27
v_cvt_pk_bf16_f32 v115,v28,v29
M 18,a[96:99],82
s_cselect_b32 s95,0xcc00,s95
R 50,1,20480
v_fma_f32 v107,v26,v26,s87
M 18,a[104:107],86
L 5
D 17,56,3072
M 18,a[112:115],90
s_add_u32 m0,s57,4096
R 54,1,21504
v_add_f32 v106,v26,v27
M 18,a[120:123],94
s_add_u32 s56,s55,5120
v_fma_f32 v107,v27,v27,v107
v_add_f32 v106,v106,v28
M 22,a[116:119],90
s_add_u32 s57,s57,32768
R 58,1,22528
v_fma_f32 v107,v28,v28,v107
M 22,a[124:127],94
L 5
v_add_f32 v106,v106,v29
v_fma_f32 v107,v29,v29,v107
M 22,a[132:135],34
s_mov_b32 s66,s65
R 62,1,23552
v_add_f32 v106,v106,v30
M 22,a[140:143],38
s_mov_b32 s76,s75
v_fma_f32 v107,v30,v30,v107
v_add_f32 v106,v106,v31
M 18,a[128:131],34
s_add_u32 s73,s73,s64
R 66,1,24576
v_fma_f32 v107,v31,v31,v107
M 18,a[136:139],38
L 5
D 17,56,0
M 18,a[144:147],42
s_add_u32 s56,s55,5376
R 70,1,25600
v_add_f32 v106,v106,v32
M 18,a[152:155],46
s_add_u32 s74,s74,s62
v_fma_f32 v107,v32,v32,v107
v_add_f32 v106,v106,v33
v_fma_f32 v107,v33,v33,v107
M 22,a[148:151],42
s_and_b32 s57,s57,131071
R 74,1,26624
v_cvt_pk_bf16_f32 v116,v30,v31
M 22,a[156:159],46
L 5
v_permlane16_swap_b32 v106,v107
v_add_f32 v106,v106,v107
v_mov_b32 v107,v106
M 22,a[164:167],50
s_add_u32 s65,s65,s62
R 78,1,27648
v_cvt_pk_bf16_f32 v117,v32,v33
M 22,a[172:175],54
s_add_u32 s75,s75,s63
v_permlane32_swap_b32 v106,v107
v_add_f32 v106,v106,v107
M 18,a[160:163],50
R 82,1,28672
ds_write_b32 v12,v106 offset:2048
M 18,a[168:171],54
L 6
D 17,56,1024
M 18,a[176:179],58
s_add_u32 s56,s55,5632
R 86,1,29696
M 18,a[184:187],62
buffer_store_dwordx4 v[114:117],v0,s[36:39],s67 offen offset:0 sc0 sc1
M 22,a[180:183],58
R 90,1,30720
M 22,a[188:191],62
L 6
L 2
M 22,a[196:199],66
s_waitcnt vmcnt(7)
R 94,1,31744
v_xor_b32 v1,65536,v1
M 22,a[204:207],70
s_barrier
D 17,56,2048
M 18,a[192:195],66
s_add_u32 s56,s55,5888
R 34,2,0
R 118,13,2048
M 18,a[200:203],70
s_nop 0
buffer_store_dword v4,v3,s[36:39],s70 offen offset:0 sc0 sc1
M 18,a[208:211],74
s_mov_b32 s70,s69
R 38,2,1024
ds_read_b32 v7,v6 offset:0
M 18,a[216:219],78
s_mov_b32 s69,s68
s_add_u32 s55,s55,s60
s_add_u32 s68,s68,s63
M 22,a[212:215],74
R 42,2,2048
M 22,a[220:223],78
L 7
D 17,56,3072
M 22,a[228:231],82
L 3
R 46,2,3072
v_add_f32 v118,v118,v119
M 22,a[236:239],86
v_add_f32 v120,v120,v121
v_add_f32 v118,v118,v120
M 18,a[224:227],82
L 2
R 50,2,4096
v_mul_f32 v118,s86,v118
M 18,a[232:235],86
s_nop 0
buffer_store_dword v118,v14,s[36:39],s77 offen offset:0 sc0 sc1
M 18,a[240:243],90
R 54,2,5120
v_cmp_eq_u32 vcc,s53,v7
M 18,a[248:251],94
s_or_b64 vcc,vcc,s[94:95]
s_cmp_eq_u32 vcc_lo,-1
M 22,a[244:247],90
R 58,2,6144
M 22,a[252:255],94
s_nop 0
s_cbranch_scc1 Lg2_S1_%=
Lg2_N1_%=:
v_mfma_f32_16x16x32_bf16 v[30:33],a[4:7],v[34:37],0
s_add_u32 m0,s59,1024
R 62,2,7168
v_lshlrev_b32 v106,16,v122
M 30,a[12:15],38
s_and_b32 s79,s54,127
buffer_load_dword v5,s[36:39],s58 offen offset:0 sc1 lds
v_mfma_f32_16x16x32_bf16 v[26:29],a[0:3],v[34:37],0
s_cmp_eq_u32 s79,0
R 66,2,8192
v_and_b32 v107,0xffff0000,v122
M 26,a[8:11],38
L 5
buffer_load_dwordx4 v[126:129],v0,s[36:39],s74 offen offset:0 sc1
M 26,a[16:19],42
s_cselect_b32 s94,0x3300,0
R 70,2,9216
v_lshlrev_b32 v108,16,v123
M 26,a[24:27],46
s_cmp_eq_u32 s79,127
buffer_load_dwordx2 v[102:103],v11,s[36:39],s73 offen offset:256 sc1
M 30,a[20:23],42
s_cselect_b32 s94,0xcc00,s94
R 74,2,10240
v_and_b32 v109,0xffff0000,v123
M 30,a[28:31],46
L 5
v_lshlrev_b32 v110,16,v124
v_and_b32 v111,0xffff0000,v124
M 30,a[36:39],50
s_add_u32 s79,s54,s46
R 78,2,11264
v_lshlrev_b32 v112,16,v125
M 30,a[44:47],54
s_and_b32 s79,s79,127
v_and_b32 v113,0xffff0000,v125
v_fma_f32 v106,v106,v99,v98
M 26,a[32:35],50
s_cmp_eq_u32 s79,0
R 82,2,12288
v_fma_f32 v107,v107,v99,v98
M 26,a[40:43],54
L 5
v_fma_f32 v108,v108,v99,v98
v_fma_f32 v109,v109,v99,v98
M 26,a[48:51],58
s_cselect_b32 s95,0x3300,0
R 86,2,13312
v_fma_f32 v110,v110,v99,v98
M 26,a[56:59],62
s_cmp_eq_u32 s79,127
v_fma_f32 v111,v111,v99,v98
v_fma_f32 v112,v112,v99,v98
M 30,a[52:55],58
s_cselect_b32 s95,0xcc00,s95
R 90,2,14336
v_fma_f32 v113,v113,v99,v98
M 30,a[60:63],62
L 5
v_add_f32 v18,v18,v106
v_add_f32 v19,v19,v107
M 30,a[68:71],66
s_mov_b32 s67,s65
R 94,2,15360
v_add_f32 v20,v20,v108
M 30,a[76:79],70
s_mov_b32 s77,s75
v_add_f32 v21,v21,v109
v_add_f32 v22,v22,v110
M 26,a[64:67],66
s_add_u32 s73,s73,s64
R 34,2,16384
v_add_f32 v23,v23,v111
M 26,a[72:75],70
L 5
v_add_f32 v24,v24,v112
v_add_f32 v25,v25,v113
M 26,a[80:83],74
s_add_u32 s74,s74,s62
R 38,2,17408
v_cvt_pk_bf16_f32 v114,v18,v19
M 26,a[88:91],78
s_add_u32 s65,s65,s62
v_cvt_pk_bf16_f32 v115,v20,v21
v_cvt_pk_bf16_f32 v116,v22,v23
M 30,a[84:87],74
s_add_u32 s75,s75,s63
R 42,2,18432
v_cvt_pk_bf16_f32 v117,v24,v25
M 30,a[92:95],78
L 5
buffer_store_dwordx4 v[114:117],v0,s[36:39],s66 offen offset:0 sc0 sc1
v_add_f32 v106,v18,v19
M 30,a[100:103],82
R 46,2,19456
v_fma_f32 v107,v18,v18,s87
M 30,a[108:111],86
v_fma_f32 v107,v19,v19,v107
v_add_f32 v106,v106,v20
M 26,a[96:99],82
R 50,2,20480
v_fma_f32 v107,v20,v20,v107
M 26,a[104:107],86
L 5
v_add_f32 v106,v106,v21
v_fma_f32 v107,v21,v21,v107
M 26,a[112:115],90
R 54,2,21504
v_add_f32 v106,v106,v22
)"
R"(M 26,a[120:123],94
v_fma_f32 v107,v22,v22,v107
v_add_f32 v106,v106,v23
M 30,a[116:119],90
R 58,2,22528
v_fma_f32 v107,v23,v23,v107
M 30,a[124:127],94
L 5
v_add_f32 v106,v106,v24
v_fma_f32 v107,v24,v24,v107
M 30,a[132:135],34
R 62,2,23552
v_add_f32 v106,v106,v25
M 30,a[140:143],38
v_fma_f32 v107,v25,v25,v107
M 26,a[128:131],34
R 66,2,24576
M 26,a[136:139],38
L 5
v_permlane16_swap_b32 v106,v107
M 26,a[144:147],42
R 70,2,25600
v_add_f32 v106,v106,v107
M 26,a[152:155],46
v_mov_b32 v107,v106
M 30,a[148:151],42
R 74,2,26624
M 30,a[156:159],46
L 5
v_permlane32_swap_b32 v106,v107
M 30,a[164:167],50
R 78,2,27648
v_add_f32 v106,v106,v107
M 30,a[172:175],54
ds_write_b32 v12,v106 offset:0
M 26,a[160:163],50
R 82,2,28672
M 26,a[168:171],54
L 6
M 26,a[176:179],58
R 86,2,29696
M 26,a[184:187],62
M 30,a[180:183],58
R 90,2,30720
M 30,a[188:191],62
L 6
L 3
M 30,a[196:199],66
s_waitcnt vmcnt(1)
R 94,2,31744
v_xor_b32 v2,65536,v2
M 30,a[204:207],70
s_barrier
buffer_store_dword v4,v3,s[36:39],s70 offen offset:0 sc0 sc1
M 26,a[192:195],66
s_mov_b32 s70,s69
R 34,1,0
R 118,13,0
M 26,a[200:203],70
s_nop 0
s_mov_b32 s69,s68
ds_read_b32 v7,v6 offset:1024
M 26,a[208:211],74
s_add_u32 s68,s68,s63
R 38,1,1024
M 26,a[216:219],78
M 30,a[212:215],74
R 42,1,2048
M 30,a[220:223],78
L 7
M 30,a[228:231],82
L 3
R 46,1,3072
v_add_f32 v118,v118,v119
M 30,a[236:239],86
L 3
v_add_f32 v120,v120,v121
v_add_f32 v118,v118,v120
M 26,a[224:227],82
R 50,1,4096
v_mul_f32 v118,s86,v118
M 26,a[232:235],86
s_nop 0
buffer_store_dword v118,v14,s[36:39],s76 offen offset:0 sc0 sc1
M 26,a[240:243],90
R 54,1,5120
v_cmp_eq_u32 vcc,s53,v7
M 26,a[248:251],94
s_or_b64 vcc,vcc,s[94:95]
s_cmp_eq_u64 vcc,-1
M 30,a[244:247],90
R 58,1,6144
M 30,a[252:255],94
L 5
s_cbranch_scc1 Lg2_L20_%=
s_cmp_eq_u32 vcc_lo,-1
s_cbranch_scc1 Lg2_L10_%=
Lg2_X0_%=:
v_mfma_f32_16x16x32_bf16 v[22:25],a[4:7],v[34:37],0
s_add_u32 m0,s59,0
R 62,1,7168
v_lshlrev_b32 v106,16,v126
M 22,a[12:15],38
s_and_b32 s79,s54,127
buffer_load_dword v5,s[36:39],s58 offen offset:0 sc1 lds
v_mfma_f32_16x16x32_bf16 v[18:21],a[0:3],v[34:37],0
s_cmp_eq_u32 s79,0
R 66,1,8192
v_and_b32 v107,0xffff0000,v126
M 18,a[8:11],38
L 5
buffer_load_dwordx4 v[122:125],v0,s[36:39],s74 offen offset:0 sc1
M 18,a[16:19],42
s_cselect_b32 s94,0x3300,0
R 70,1,9216
v_lshlrev_b32 v108,16,v127
M 18,a[24:27],46
s_cmp_eq_u32 s79,127
buffer_load_dwordx2 v[98:99],v11,s[36:39],s73 offen offset:256 sc1
M 22,a[20:23],42
s_cselect_b32 s94,0xcc00,s94
R 74,1,10240
v_and_b32 v109,0xffff0000,v127
M 22,a[28:31],46
L 5
v_lshlrev_b32 v110,16,v128
v_and_b32 v111,0xffff0000,v128
M 22,a[36:39],50
s_add_u32 s79,s54,s46
R 78,1,11264
v_lshlrev_b32 v112,16,v129
M 22,a[44:47],54
s_and_b32 s79,s79,127
v_and_b32 v113,0xffff0000,v129
v_fma_f32 v106,v106,v103,v102
M 18,a[32:35],50
s_cmp_eq_u32 s79,0
R 82,1,12288
v_fma_f32 v107,v107,v103,v102
M 18,a[40:43],54
L 5
v_fma_f32 v108,v108,v103,v102
v_fma_f32 v109,v109,v103,v102
M 18,a[48:51],58
s_cselect_b32 s95,0x3300,0
R 86,1,13312
v_fma_f32 v110,v110,v103,v102
M 18,a[56:59],62
s_cmp_eq_u32 s79,127
v_fma_f32 v111,v111,v103,v102
v_fma_f32 v112,v112,v103,v102
M 22,a[52:55],58
s_cselect_b32 s95,0xcc00,s95
R 90,1,14336
v_fma_f32 v113,v113,v103,v102
M 22,a[60:63],62
L 5
v_add_f32 v26,v26,v106
v_add_f32 v27,v27,v107
M 22,a[68:71],66
s_mov_b32 s66,s65
R 94,1,15360
v_add_f32 v28,v28,v108
M 22,a[76:79],70
s_mov_b32 s76,s75
v_add_f32 v29,v29,v109
v_add_f32 v30,v30,v110
M 18,a[64:67],66
s_add_u32 s73,s73,s64
R 34,1,16384
v_add_f32 v31,v31,v111
M 18,a[72:75],70
L 5
v_add_f32 v32,v32,v112
v_add_f32 v33,v33,v113
M 18,a[80:83],74
s_add_u32 s74,s74,s62
R 38,1,17408
v_cvt_pk_bf16_f32 v114,v26,v27
M 18,a[88:91],78
s_add_u32 s65,s65,s62
v_cvt_pk_bf16_f32 v115,v28,v29
v_cvt_pk_bf16_f32 v116,v30,v31
M 22,a[84:87],74
s_add_u32 s75,s75,s63
R 42,1,18432
v_cvt_pk_bf16_f32 v117,v32,v33
M 22,a[92:95],78
L 5
buffer_store_dwordx4 v[114:117],v0,s[36:39],s67 offen offset:0 sc0 sc1
v_add_f32 v106,v26,v27
M 22,a[100:103],82
R 46,1,19456
v_fma_f32 v107,v26,v26,s87
M 22,a[108:111],86
v_fma_f32 v107,v27,v27,v107
v_add_f32 v106,v106,v28
M 18,a[96:99],82
R 50,1,20480
v_fma_f32 v107,v28,v28,v107
M 18,a[104:107],86
L 5
v_add_f32 v106,v106,v29
v_fma_f32 v107,v29,v29,v107
M 18,a[112:115],90
R 54,1,21504
v_add_f32 v106,v106,v30
M 18,a[120:123],94
v_fma_f32 v107,v30,v30,v107
v_add_f32 v106,v106,v31
M 22,a[116:119],90
R 58,1,22528
v_fma_f32 v107,v31,v31,v107
M 22,a[124:127],94
L 5
v_add_f32 v106,v106,v32
v_fma_f32 v107,v32,v32,v107
M 22,a[132:135],34
R 62,1,23552
v_add_f32 v106,v106,v33
M 22,a[140:143],38
v_fma_f32 v107,v33,v33,v107
M 18,a[128:131],34
R 66,1,24576
M 18,a[136:139],38
L 5
v_permlane16_swap_b32 v106,v107
M 18,a[144:147],42
R 70,1,25600
v_add_f32 v106,v106,v107
M 18,a[152:155],46
v_mov_b32 v107,v106
M 22,a[148:151],42
R 74,1,26624
M 22,a[156:159],46
L 5
v_permlane32_swap_b32 v106,v107
M 22,a[164:167],50
R 78,1,27648
v_add_f32 v106,v106,v107
M 22,a[172:175],54
ds_write_b32 v12,v106 offset:2048
M 18,a[160:163],50
R 82,1,28672
M 18,a[168:171],54
L 6
M 18,a[176:179],58
R 86,1,29696
M 18,a[184:187],62
M 22,a[180:183],58
R 90,1,30720
M 22,a[188:191],62
L 6
L 3
M 22,a[196:199],66
s_waitcnt vmcnt(1)
R 94,1,31744
v_xor_b32 v1,65536,v1
M 22,a[204:207],70
s_barrier
buffer_store_dword v4,v3,s[36:39],s70 offen offset:0 sc0 sc1
M 18,a[192:195],66
s_mov_b32 s70,s69
R 118,13,2048
ds_read_b32 v7,v6 offset:0
M 18,a[200:203],70
s_nop 0
s_mov_b32 s69,s68
s_add_u32 s68,s68,s63
M 18,a[208:211],74
M 18,a[216:219],78
M 22,a[212:215],74
M 22,a[220:223],78
L 4
M 22,a[228:231],82
L 1
v_add_f32 v118,v118,v119
v_add_f32 v120,v120,v121
M 22,a[236:239],86
L 0
v_add_f32 v118,v118,v120
v_mul_f32 v118,s86,v118
M 18,a[224:227],82
buffer_store_dword v118,v14,s[36:39],s77 offen offset:0 sc0 sc1
M 18,a[232:235],86
s_nop 0
v_cmp_eq_u32 vcc,s53,v7
M 18,a[240:243],90
s_or_b64 vcc,vcc,s[94:95]
M 18,a[248:251],94
M 22,a[244:247],90
M 22,a[252:255],94
s_mov_b32 s80,1
s_cmp_eq_u32 vcc_lo,-1
s_cbranch_scc0 Lg2_xn0_%=
s_and_b32 s78,s54,127
s_cmp_eq_u32 s78,s85
s_cselect_b64 s[40:41],-1,0
v_cndmask_b32 v17,v15,v16,s[40:41]
s_add_u32 m0,s57,0
s_add_u32 s56,s55,0
D 17,56,0
v_lshlrev_b32 v106,16,v122
v_and_b32 v107,0xffff0000,v122
v_lshlrev_b32 v108,16,v123
v_and_b32 v109,0xffff0000,v123
v_lshlrev_b32 v110,16,v124
s_add_u32 s56,s55,256
D 17,56,1024
v_and_b32 v111,0xffff0000,v124
v_lshlrev_b32 v112,16,v125
v_and_b32 v113,0xffff0000,v125
v_fma_f32 v106,v106,v99,v98
v_fma_f32 v107,v107,v99,v98
s_add_u32 s56,s55,512
D 17,56,2048
v_fma_f32 v108,v108,v99,v98
v_fma_f32 v109,v109,v99,v98
v_fma_f32 v110,v110,v99,v98
v_fma_f32 v111,v111,v99,v98
v_fma_f32 v112,v112,v99,v98
s_add_u32 s56,s55,768
D 17,56,3072
v_fma_f32 v113,v113,v99,v98
v_add_f32 v18,v18,v106
v_add_f32 v19,v19,v107
v_add_f32 v20,v20,v108
v_add_f32 v21,v21,v109
s_add_u32 m0,s57,4096
s_add_u32 s56,s55,5120
D 17,56,0
v_add_f32 v22,v22,v110
v_add_f32 v23,v23,v111
v_add_f32 v24,v24,v112
v_add_f32 v25,v25,v113
v_cvt_pk_bf16_f32 v114,v18,v19
s_add_u32 s56,s55,5376
D 17,56,1024
v_cvt_pk_bf16_f32 v115,v20,v21
v_cvt_pk_bf16_f32 v116,v22,v23
v_cvt_pk_bf16_f32 v117,v24,v25
s_add_u32 s56,s55,5632
D 17,56,2048
s_add_u32 s56,s55,5888
D 17,56,3072
buffer_store_dwordx4 v[114:117],v0,s[36:39],s66 offen offset:0 sc0 sc1
v_add_f32 v106,v18,v19
v_fma_f32 v107,v18,v18,s87
s_add_u32 s55,s55,s60
s_add_u32 s57,s57,32768
s_and_b32 s57,s57,131071
s_add_u32 s54,s54,s46
v_fma_f32 v107,v19,v19,v107
v_add_f32 v106,v106,v20
v_fma_f32 v107,v20,v20,v107
v_add_f32 v106,v106,v21
v_fma_f32 v107,v21,v21,v107
v_add_f32 v106,v106,v22
v_fma_f32 v107,v22,v22,v107
v_add_f32 v106,v106,v23
v_fma_f32 v107,v23,v23,v107
v_add_f32 v106,v106,v24
v_fma_f32 v107,v24,v24,v107
v_add_f32 v106,v106,v25
v_fma_f32 v107,v25,v25,v107
s_nop 1
v_permlane16_swap_b32 v106,v107
v_add_f32 v106,v106,v107
v_mov_b32 v107,v106
s_nop 1
v_permlane32_swap_b32 v106,v107
v_add_f32 v106,v106,v107
ds_write_b32 v12,v106 offset:0
L 0
s_barrier
R 118,13,0
L 0
v_add_f32 v118,v118,v119
v_add_f32 v120,v120,v121
v_add_f32 v118,v118,v120
v_mul_f32 v118,s86,v118
buffer_store_dword v118,v14,s[36:39],s76 offen offset:0 sc0 sc1
s_mov_b32 s72,s69
s_mov_b32 s69,0x7fff0000
s_mov_b32 s66,0x7fff0000
s_mov_b32 s76,0x7fff0000
s_branch Lg2_gox_%=
Lg2_xn0_%=:
s_cmp_ge_u32 s54,256
s_cbranch_scc1 Lg2_fin0_%=
s_add_u32 m0,s59,4096
s_nop 0
buffer_load_dword v5,s[36:39],s58 offen offset:0 sc1 lds
buffer_store_dword v4,v3,s[36:39],s71 offen offset:0 sc0 sc1
s_add_u32 m0,s59,5120
s_nop 0
buffer_load_dword v5,s[36:39],s58 offen offset:0 sc1 lds
buffer_store_dword v4,v3,s[36:39],s71 offen offset:0 sc0 sc1
s_mov_b32 s83,0
v_lshlrev_b32 v106,16,v122
v_and_b32 v107,0xffff0000,v122
v_lshlrev_b32 v108,16,v123
v_and_b32 v109,0xffff0000,v123
v_lshlrev_b32 v110,16,v124
v_and_b32 v111,0xffff0000,v124
v_lshlrev_b32 v112,16,v125
v_and_b32 v113,0xffff0000,v125
v_fma_f32 v106,v106,v99,v98
v_fma_f32 v107,v107,v99,v98
v_fma_f32 v108,v108,v99,v98
v_fma_f32 v109,v109,v99,v98
v_fma_f32 v110,v110,v99,v98
v_fma_f32 v111,v111,v99,v98
v_fma_f32 v112,v112,v99,v98
v_fma_f32 v113,v113,v99,v98
v_add_f32 v18,v18,v106
v_add_f32 v19,v19,v107
v_add_f32 v20,v20,v108
v_add_f32 v21,v21,v109
v_add_f32 v22,v22,v110
v_add_f32 v23,v23,v111
v_add_f32 v24,v24,v112
v_add_f32 v25,v25,v113
v_cvt_pk_bf16_f32 v114,v18,v19
v_cvt_pk_bf16_f32 v115,v20,v21
v_cvt_pk_bf16_f32 v116,v22,v23
v_cvt_pk_bf16_f32 v117,v24,v25
buffer_store_dwordx4 v[114:117],v0,s[36:39],s66 offen offset:0 sc0 sc1
v_add_f32 v106,v18,v19
v_fma_f32 v107,v18,v18,s87
v_fma_f32 v107,v19,v19,v107
v_add_f32 v106,v106,v20
v_fma_f32 v107,v20,v20,v107
v_add_f32 v106,v106,v21
v_fma_f32 v107,v21,v21,v107
v_add_f32 v106,v106,v22
v_fma_f32 v107,v22,v22,v107
v_add_f32 v106,v106,v23
v_fma_f32 v107,v23,v23,v107
v_add_f32 v106,v106,v24
v_fma_f32 v107,v24,v24,v107
v_add_f32 v106,v106,v25
v_fma_f32 v107,v25,v25,v107
s_nop 1
v_permlane16_swap_b32 v106,v107
v_add_f32 v106,v106,v107
v_mov_b32 v107,v106
s_nop 1
v_permlane32_swap_b32 v106,v107
v_add_f32 v106,v106,v107
ds_write_b32 v12,v106 offset:0
L 0
s_barrier
R 118,13,0
L 0
v_add_f32 v118,v118,v119
v_add_f32 v120,v120,v121
v_add_f32 v118,v118,v120
v_mul_f32 v118,s86,v118
buffer_store_dword v118,v14,s[36:39],s76 offen offset:0 sc0 sc1
s_mov_b32 s72,s69
s_mov_b32 s69,0x7fff0000
s_mov_b32 s66,0x7fff0000
s_mov_b32 s76,0x7fff0000
s_branch Lg2_poll_%=
Lg2_fin0_%=:
v_lshlrev_b32 v106,16,v122
v_and_b32 v107,0xffff0000,v122
v_lshlrev_b32 v108,16,v123
v_and_b32 v109,0xffff0000,v123
v_lshlrev_b32 v110,16,v124
v_and_b32 v111,0xffff0000,v124
v_lshlrev_b32 v112,16,v125
v_and_b32 v113,0xffff0000,v125
v_fma_f32 v106,v106,v99,v98
v_fma_f32 v107,v107,v99,v98
v_fma_f32 v108,v108,v99,v98
v_fma_f32 v109,v109,v99,v98
v_fma_f32 v110,v110,v99,v98
v_fma_f32 v111,v111,v99,v98
v_fma_f32 v112,v112,v99,v98
v_fma_f32 v113,v113,v99,v98
v_add_f32 v18,v18,v106
v_add_f32 v19,v19,v107
v_add_f32 v20,v20,v108
v_add_f32 v21,v21,v109
v_add_f32 v22,v22,v110
v_add_f32 v23,v23,v111
v_add_f32 v24,v24,v112
v_add_f32 v25,v25,v113
v_cvt_pk_bf16_f32 v114,v18,v19
v_cvt_pk_bf16_f32 v115,v20,v21
v_cvt_pk_bf16_f32 v116,v22,v23
v_cvt_pk_bf16_f32 v117,v24,v25
buffer_store_dwordx4 v[114:117],v0,s[36:39],s66 offen offset:0 sc0 sc1
v_add_f32 v106,v18,v19
v_fma_f32 v107,v18,v18,s87
v_fma_f32 v107,v19,v19,v107
v_add_f32 v106,v106,v20
v_fma_f32 v107,v20,v20,v107
)"
R"(v_add_f32 v106,v106,v21
v_fma_f32 v107,v21,v21,v107
v_add_f32 v106,v106,v22
v_fma_f32 v107,v22,v22,v107
v_add_f32 v106,v106,v23
v_fma_f32 v107,v23,v23,v107
v_add_f32 v106,v106,v24
v_fma_f32 v107,v24,v24,v107
v_add_f32 v106,v106,v25
v_fma_f32 v107,v25,v25,v107
s_nop 1
v_permlane16_swap_b32 v106,v107
v_add_f32 v106,v106,v107
v_mov_b32 v107,v106
s_nop 1
v_permlane32_swap_b32 v106,v107
v_add_f32 v106,v106,v107
ds_write_b32 v12,v106 offset:0
L 0
s_barrier
R 118,13,0
L 0
v_add_f32 v118,v118,v119
v_add_f32 v120,v120,v121
v_add_f32 v118,v118,v120
v_mul_f32 v118,s86,v118
buffer_store_dword v118,v14,s[36:39],s76 offen offset:0 sc0 sc1
s_mov_b32 s72,s69
s_mov_b32 s69,0x7fff0000
s_mov_b32 s66,0x7fff0000
s_mov_b32 s76,0x7fff0000
s_branch Lg2_done_%=
Lg2_L21_%=:
v_mfma_f32_16x16x32_bf16 v[30:33],a[4:7],v[34:37],0
s_add_u32 s58,s58,s61
s_and_b32 s78,s54,127
s_add_u32 s58,s58,s61
R 62,2,7168
M 30,a[12:15],38
s_add_u32 m0,s59,1024
s_cmp_eq_u32 s78,s85
s_cselect_b64 s[40:41],-1,0
buffer_load_dwordx4 v[126:129],v0,s[36:39],s74 offen offset:0 sc1
v_mfma_f32_16x16x32_bf16 v[26:29],a[0:3],v[34:37],0
s_add_u32 s56,s55,0
R 66,2,8192
v_cndmask_b32 v17,v15,v16,s[40:41]
M 26,a[8:11],38
L 5
buffer_load_dword v8,s[36:39],s58 offen offset:0 sc1 lds
M 26,a[16:19],42
s_add_u32 m0,s57,0
R 70,2,9216
v_lshlrev_b32 v106,16,v122
M 26,a[24:27],46
s_add_u32 s54,s54,s46
buffer_load_dwordx2 v[102:103],v11,s[36:39],s73 offen offset:256 sc1
M 30,a[20:23],42
s_and_b32 s78,s54,127
R 74,2,10240
v_and_b32 v107,0xffff0000,v122
M 30,a[28:31],46
L 5
D 17,56,0
M 30,a[36:39],50
s_add_u32 s56,s55,256
R 78,2,11264
v_lshlrev_b32 v108,16,v123
M 30,a[44:47],54
s_cmp_eq_u32 s78,s85
v_and_b32 v109,0xffff0000,v123
v_lshlrev_b32 v110,16,v124
M 26,a[32:35],50
s_cselect_b64 s[40:41],-1,0
R 82,2,12288
v_and_b32 v111,0xffff0000,v124
M 26,a[40:43],54
L 5
D 17,56,1024
M 26,a[48:51],58
s_add_u32 s56,s55,512
R 86,2,13312
v_lshlrev_b32 v112,16,v125
M 26,a[56:59],62
s_add_u32 s54,s54,s46
v_and_b32 v113,0xffff0000,v125
v_fma_f32 v106,v106,v99,v98
M 30,a[52:55],58
s_and_b32 s79,s54,127
R 90,2,14336
v_fma_f32 v107,v107,v99,v98
M 30,a[60:63],62
L 5
D 17,56,2048
M 30,a[68:71],66
s_add_u32 s56,s55,768
R 94,2,15360
v_fma_f32 v108,v108,v99,v98
M 30,a[76:79],70
s_cmp_eq_u32 s79,0
v_fma_f32 v109,v109,v99,v98
v_fma_f32 v110,v110,v99,v98
M 26,a[64:67],66
s_cselect_b32 s94,0x3300,0
R 34,2,16384
v_fma_f32 v111,v111,v99,v98
M 26,a[72:75],70
L 5
D 17,56,3072
M 26,a[80:83],74
s_add_u32 m0,s57,4096
s_add_u32 s56,s55,5120
R 38,2,17408
M 26,a[88:91],78
s_add_u32 s57,s57,32768
v_fma_f32 v112,v112,v99,v98
v_fma_f32 v113,v113,v99,v98
M 30,a[84:87],74
s_and_b32 s57,s57,131071
R 42,2,18432
v_add_f32 v18,v18,v106
M 30,a[92:95],78
L 5
D 17,56,0
M 30,a[100:103],82
s_add_u32 s56,s55,5376
R 46,2,19456
v_add_f32 v19,v19,v107
M 30,a[108:111],86
s_cmp_eq_u32 s79,127
v_add_f32 v20,v20,v108
v_add_f32 v21,v21,v109
M 26,a[96:99],82
s_cselect_b32 s94,0xcc00,s94
R 50,2,20480
v_fma_f32 v107,v18,v18,s87
M 26,a[104:107],86
L 5
D 17,56,1024
M 26,a[112:115],90
s_add_u32 s56,s55,5632
R 54,2,21504
v_add_f32 v106,v18,v19
M 26,a[120:123],94
s_add_u32 s79,s54,s46
v_fma_f32 v107,v19,v19,v107
v_add_f32 v106,v106,v20
M 30,a[116:119],90
s_and_b32 s79,s79,127
R 58,2,22528
v_fma_f32 v107,v20,v20,v107
M 30,a[124:127],94
L 5
D 17,56,2048
M 30,a[132:135],34
s_add_u32 s56,s55,5888
s_add_u32 s55,s55,s60
R 62,2,23552
M 30,a[140:143],38
s_cmp_eq_u32 s79,0
v_add_f32 v22,v22,v110
v_add_f32 v106,v106,v21
M 26,a[128:131],34
s_cselect_b32 s95,0x3300,0
R 66,2,24576
v_fma_f32 v107,v21,v21,v107
M 26,a[136:139],38
L 5
D 17,56,3072
M 26,a[144:147],42
s_add_u32 m0,s57,0
s_add_u32 s56,s55,0
R 70,2,25600
M 26,a[152:155],46
s_cmp_eq_u32 s79,127
v_add_f32 v23,v23,v111
v_add_f32 v24,v24,v112
v_add_f32 v25,v25,v113
v_add_f32 v106,v106,v22
v_fma_f32 v107,v22,v22,v107
v_add_f32 v106,v106,v23
v_fma_f32 v107,v23,v23,v107
v_add_f32 v106,v106,v24
v_fma_f32 v107,v24,v24,v107
v_add_f32 v106,v106,v25
v_fma_f32 v107,v25,v25,v107
M 30,a[148:151],42
s_cselect_b32 s95,0xcc00,s95
R 74,2,26624
v_cndmask_b32 v17,v15,v16,s[40:41]
M 30,a[156:159],46
L 5
v_permlane16_swap_b32 v106,v107
v_add_f32 v106,v106,v107
v_mov_b32 v107,v106
M 30,a[164:167],50
s_mov_b32 s67,s65
R 78,2,27648
v_cvt_pk_bf16_f32 v114,v18,v19
M 30,a[172:175],54
s_mov_b32 s77,s75
v_permlane32_swap_b32 v106,v107
v_add_f32 v106,v106,v107
M 26,a[160:163],50
s_add_u32 s73,s73,s64
R 82,2,28672
ds_write_b32 v12,v106 offset:0
M 26,a[168:171],54
L 6
D 17,56,0
M 26,a[176:179],58
s_add_u32 s56,s55,256
R 86,2,29696
v_cvt_pk_bf16_f32 v115,v20,v21
M 26,a[184:187],62
s_add_u32 s74,s74,s62
D 17,56,1024
M 30,a[180:183],58
s_add_u32 s56,s55,512
R 90,2,30720
v_cvt_pk_bf16_f32 v116,v22,v23
M 30,a[188:191],62
L 6
L 2
D 17,56,2048
M 30,a[196:199],66
s_waitcnt vmcnt(3)
s_add_u32 s56,s55,768
R 94,2,31744
M 30,a[204:207],70
s_barrier
D 17,56,3072
M 26,a[192:195],66
s_add_u32 m0,s57,4096
s_add_u32 s56,s55,5120
R 34,1,0
M 26,a[200:203],70
s_nop 0
buffer_store_dword v4,v3,s[36:39],s70 offen offset:0 sc0 sc1
M 26,a[208:211],74
s_mov_b32 s70,s69
R 38,1,1024
R 118,13,0
M 26,a[216:219],78
s_add_u32 s57,s57,32768
D 17,56,0
M 30,a[212:215],74
s_add_u32 s56,s55,5376
R 42,1,2048
ds_read_b32 v7,v6 offset:1024
M 30,a[220:223],78
L 7
D 17,56,1024
M 30,a[228:231],82
s_add_u32 s56,s55,5632
R 46,1,3072
v_cvt_pk_bf16_f32 v117,v24,v25
M 30,a[236:239],86
s_mov_b32 s69,s68
D 17,56,2048
M 26,a[224:227],82
s_add_u32 s56,s55,5888
R 50,1,4096
v_xor_b32 v2,65536,v2
M 26,a[232:235],86
L 7
L 4
buffer_store_dwordx4 v[114:117],v0,s[36:39],s66 offen offset:0 sc0 sc1
M 26,a[240:243],90
L 2
R 54,1,5120
v_add_f32 v118,v118,v119
M 26,a[248:251],94
s_add_u32 s55,s55,s60
v_cmp_eq_u32 vcc,s53,v7
v_add_f32 v120,v120,v121
M 30,a[244:247],90
s_and_b32 s57,s57,131071
s_add_u32 s65,s65,s62
s_add_u32 s75,s75,s63
s_add_u32 s68,s68,s63
s_or_b64 vcc,vcc,s[94:95]
s_cmp_eq_u32 vcc_lo,-1
R 58,1,6144
v_add_f32 v118,v118,v120
v_mul_f32 v118,s86,v118
M 30,a[252:255],94
s_nop 0
D 17,56,3072
buffer_store_dword v118,v14,s[36:39],s76 offen offset:0 sc0 sc1
s_cbranch_scc1 Lg2_S0_%=
Lg2_N0_%=:
v_mfma_f32_16x16x32_bf16 v[22:25],a[4:7],v[34:37],0
s_add_u32 m0,s59,0
R 62,1,7168
v_lshlrev_b32 v106,16,v126
M 22,a[12:15],38
s_and_b32 s79,s54,127
buffer_load_dword v5,s[36:39],s58 offen offset:0 sc1 lds
v_mfma_f32_16x16x32_bf16 v[18:21],a[0:3],v[34:37],0
s_cmp_eq_u32 s79,0
R 66,1,8192
v_and_b32 v107,0xffff0000,v126
M 18,a[8:11],38
L 5
buffer_load_dwordx4 v[122:125],v0,s[36:39],s74 offen offset:0 sc1
M 18,a[16:19],42
s_cselect_b32 s94,0x3300,0
R 70,1,9216
v_lshlrev_b32 v108,16,v127
M 18,a[24:27],46
s_cmp_eq_u32 s79,127
buffer_load_dwordx2 v[98:99],v11,s[36:39],s73 offen offset:256 sc1
M 22,a[20:23],42
s_cselect_b32 s94,0xcc00,s94
R 74,1,10240
v_and_b32 v109,0xffff0000,v127
M 22,a[28:31],46
L 5
v_lshlrev_b32 v110,16,v128
v_and_b32 v111,0xffff0000,v128
M 22,a[36:39],50
s_add_u32 s79,s54,s46
R 78,1,11264
v_lshlrev_b32 v112,16,v129
M 22,a[44:47],54
s_and_b32 s79,s79,127
v_and_b32 v113,0xffff0000,v129
v_fma_f32 v106,v106,v103,v102
M 18,a[32:35],50
s_cmp_eq_u32 s79,0
R 82,1,12288
v_fma_f32 v107,v107,v103,v102
M 18,a[40:43],54
L 5
v_fma_f32 v108,v108,v103,v102
v_fma_f32 v109,v109,v103,v102
M 18,a[48:51],58
s_cselect_b32 s95,0x3300,0
R 86,1,13312
v_fma_f32 v110,v110,v103,v102
M 18,a[56:59],62
s_cmp_eq_u32 s79,127
v_fma_f32 v111,v111,v103,v102
v_fma_f32 v112,v112,v103,v102
M 22,a[52:55],58
s_cselect_b32 s95,0xcc00,s95
R 90,1,14336
v_fma_f32 v113,v113,v103,v102
M 22,a[60:63],62
L 5
v_add_f32 v26,v26,v106
v_add_f32 v27,v27,v107
M 22,a[68:71],66
s_mov_b32 s66,s65
R 94,1,15360
v_add_f32 v28,v28,v108
M 22,a[76:79],70
s_mov_b32 s76,s75
v_add_f32 v29,v29,v109
v_add_f32 v30,v30,v110
M 18,a[64:67],66
s_add_u32 s73,s73,s64
R 34,1,16384
v_add_f32 v31,v31,v111
M 18,a[72:75],70
L 5
v_add_f32 v32,v32,v112
v_add_f32 v33,v33,v113
M 18,a[80:83],74
s_add_u32 s74,s74,s62
R 38,1,17408
v_cvt_pk_bf16_f32 v114,v26,v27
M 18,a[88:91],78
s_add_u32 s65,s65,s62
v_cvt_pk_bf16_f32 v115,v28,v29
v_cvt_pk_bf16_f32 v116,v30,v31
M 22,a[84:87],74
s_add_u32 s75,s75,s63
R 42,1,18432
v_cvt_pk_bf16_f32 v117,v32,v33
M 22,a[92:95],78
L 5
buffer_store_dwordx4 v[114:117],v0,s[36:39],s67 offen offset:0 sc0 sc1
v_add_f32 v106,v26,v27
M 22,a[100:103],82
R 46,1,19456
v_fma_f32 v107,v26,v26,s87
M 22,a[108:111],86
v_fma_f32 v107,v27,v27,v107
v_add_f32 v106,v106,v28
M 18,a[96:99],82
R 50,1,20480
v_fma_f32 v107,v28,v28,v107
M 18,a[104:107],86
L 5
v_add_f32 v106,v106,v29
v_fma_f32 v107,v29,v29,v107
M 18,a[112:115],90
R 54,1,21504
v_add_f32 v106,v106,v30
M 18,a[120:123],94
v_fma_f32 v107,v30,v30,v107
v_add_f32 v106,v106,v31
M 22,a[116:119],90
R 58,1,22528
v_fma_f32 v107,v31,v31,v107
M 22,a[124:127],94
L 5
v_add_f32 v106,v106,v32
v_fma_f32 v107,v32,v32,v107
M 22,a[132:135],34
R 62,1,23552
v_add_f32 v106,v106,v33
M 22,a[140:143],38
v_fma_f32 v107,v33,v33,v107
M 18,a[128:131],34
R 66,1,24576
M 18,a[136:139],38
L 5
v_permlane16_swap_b32 v106,v107
M 18,a[144:147],42
R 70,1,25600
v_add_f32 v106,v106,v107
M 18,a[152:155],46
v_mov_b32 v107,v106
M 22,a[148:151],42
R 74,1,26624
M 22,a[156:159],46
L 5
v_permlane32_swap_b32 v106,v107
M 22,a[164:167],50
R 78,1,27648
v_add_f32 v106,v106,v107
M 22,a[172:175],54
ds_write_b32 v12,v106 offset:2048
M 18,a[160:163],50
R 82,1,28672
M 18,a[168:171],54
L 6
M 18,a[176:179],58
R 86,1,29696
M 18,a[184:187],62
M 22,a[180:183],58
R 90,1,30720
M 22,a[188:191],62
L 6
L 3
M 22,a[196:199],66
s_waitcnt vmcnt(1)
R 94,1,31744
v_xor_b32 v1,65536,v1
M 22,a[204:207],70
s_barrier
buffer_store_dword v4,v3,s[36:39],s70 offen offset:0 sc0 sc1
M 18,a[192:195],66
s_mov_b32 s70,s69
R 34,2,0
R 118,13,2048
M 18,a[200:203],70
s_nop 0
s_mov_b32 s69,s68
ds_read_b32 v7,v6 offset:0
M 18,a[208:211],74
s_add_u32 s68,s68,s63
R 38,2,1024
M 18,a[216:219],78
M 22,a[212:215],74
R 42,2,2048
M 22,a[220:223],78
L 7
M 22,a[228:231],82
L 3
R 46,2,3072
v_add_f32 v118,v118,v119
M 22,a[236:239],86
L 3
v_add_f32 v120,v120,v121
v_add_f32 v118,v118,v120
M 18,a[224:227],82
R 50,2,4096
v_mul_f32 v118,s86,v118
M 18,a[232:235],86
s_nop 0
buffer_store_dword v118,v14,s[36:39],s77 offen offset:0 sc0 sc1
M 18,a[240:243],90
R 54,2,5120
v_cmp_eq_u32 vcc,s53,v7
M 18,a[248:251],94
s_or_b64 vcc,vcc,s[94:95]
s_cmp_eq_u64 vcc,-1
M 22,a[244:247],90
R 58,2,6144
M 22,a[252:255],94
L 5
s_cbranch_scc1 Lg2_L21_%=
s_cmp_eq_u32 vcc_lo,-1
s_cbranch_scc1 Lg2_L11_%=
Lg2_X1_%=:
v_mfma_f32_16x16x32_bf16 v[30:33],a[4:7],v[34:37],0
)"
R"(s_add_u32 m0,s59,1024
R 62,2,7168
v_lshlrev_b32 v106,16,v122
M 30,a[12:15],38
s_and_b32 s79,s54,127
buffer_load_dword v5,s[36:39],s58 offen offset:0 sc1 lds
v_mfma_f32_16x16x32_bf16 v[26:29],a[0:3],v[34:37],0
s_cmp_eq_u32 s79,0
R 66,2,8192
v_and_b32 v107,0xffff0000,v122
M 26,a[8:11],38
L 5
buffer_load_dwordx4 v[126:129],v0,s[36:39],s74 offen offset:0 sc1
M 26,a[16:19],42
s_cselect_b32 s94,0x3300,0
R 70,2,9216
v_lshlrev_b32 v108,16,v123
M 26,a[24:27],46
s_cmp_eq_u32 s79,127
buffer_load_dwordx2 v[102:103],v11,s[36:39],s73 offen offset:256 sc1
M 30,a[20:23],42
s_cselect_b32 s94,0xcc00,s94
R 74,2,10240
v_and_b32 v109,0xffff0000,v123
M 30,a[28:31],46
L 5
v_lshlrev_b32 v110,16,v124
v_and_b32 v111,0xffff0000,v124
M 30,a[36:39],50
s_add_u32 s79,s54,s46
R 78,2,11264
v_lshlrev_b32 v112,16,v125
M 30,a[44:47],54
s_and_b32 s79,s79,127
v_and_b32 v113,0xffff0000,v125
v_fma_f32 v106,v106,v99,v98
M 26,a[32:35],50
s_cmp_eq_u32 s79,0
R 82,2,12288
v_fma_f32 v107,v107,v99,v98
M 26,a[40:43],54
L 5
v_fma_f32 v108,v108,v99,v98
v_fma_f32 v109,v109,v99,v98
M 26,a[48:51],58
s_cselect_b32 s95,0x3300,0
R 86,2,13312
v_fma_f32 v110,v110,v99,v98
M 26,a[56:59],62
s_cmp_eq_u32 s79,127
v_fma_f32 v111,v111,v99,v98
v_fma_f32 v112,v112,v99,v98
M 30,a[52:55],58
s_cselect_b32 s95,0xcc00,s95
R 90,2,14336
v_fma_f32 v113,v113,v99,v98
M 30,a[60:63],62
L 5
v_add_f32 v18,v18,v106
v_add_f32 v19,v19,v107
M 30,a[68:71],66
s_mov_b32 s67,s65
R 94,2,15360
v_add_f32 v20,v20,v108
M 30,a[76:79],70
s_mov_b32 s77,s75
v_add_f32 v21,v21,v109
v_add_f32 v22,v22,v110
M 26,a[64:67],66
s_add_u32 s73,s73,s64
R 34,2,16384
v_add_f32 v23,v23,v111
M 26,a[72:75],70
L 5
v_add_f32 v24,v24,v112
v_add_f32 v25,v25,v113
M 26,a[80:83],74
s_add_u32 s74,s74,s62
R 38,2,17408
v_cvt_pk_bf16_f32 v114,v18,v19
M 26,a[88:91],78
s_add_u32 s65,s65,s62
v_cvt_pk_bf16_f32 v115,v20,v21
v_cvt_pk_bf16_f32 v116,v22,v23
M 30,a[84:87],74
s_add_u32 s75,s75,s63
R 42,2,18432
v_cvt_pk_bf16_f32 v117,v24,v25
M 30,a[92:95],78
L 5
buffer_store_dwordx4 v[114:117],v0,s[36:39],s66 offen offset:0 sc0 sc1
v_add_f32 v106,v18,v19
M 30,a[100:103],82
R 46,2,19456
v_fma_f32 v107,v18,v18,s87
M 30,a[108:111],86
v_fma_f32 v107,v19,v19,v107
v_add_f32 v106,v106,v20
M 26,a[96:99],82
R 50,2,20480
v_fma_f32 v107,v20,v20,v107
M 26,a[104:107],86
L 5
v_add_f32 v106,v106,v21
v_fma_f32 v107,v21,v21,v107
M 26,a[112:115],90
R 54,2,21504
v_add_f32 v106,v106,v22
M 26,a[120:123],94
v_fma_f32 v107,v22,v22,v107
v_add_f32 v106,v106,v23
M 30,a[116:119],90
R 58,2,22528
v_fma_f32 v107,v23,v23,v107
M 30,a[124:127],94
L 5
v_add_f32 v106,v106,v24
v_fma_f32 v107,v24,v24,v107
M 30,a[132:135],34
R 62,2,23552
v_add_f32 v106,v106,v25
M 30,a[140:143],38
v_fma_f32 v107,v25,v25,v107
M 26,a[128:131],34
R 66,2,24576
M 26,a[136:139],38
L 5
v_permlane16_swap_b32 v106,v107
M 26,a[144:147],42
R 70,2,25600
v_add_f32 v106,v106,v107
M 26,a[152:155],46
v_mov_b32 v107,v106
M 30,a[148:151],42
R 74,2,26624
M 30,a[156:159],46
L 5
v_permlane32_swap_b32 v106,v107
M 30,a[164:167],50
R 78,2,27648
v_add_f32 v106,v106,v107
M 30,a[172:175],54
ds_write_b32 v12,v106 offset:0
M 26,a[160:163],50
R 82,2,28672
M 26,a[168:171],54
L 6
M 26,a[176:179],58
R 86,2,29696
M 26,a[184:187],62
M 30,a[180:183],58
R 90,2,30720
M 30,a[188:191],62
L 6
L 3
M 30,a[196:199],66
s_waitcnt vmcnt(1)
R 94,2,31744
v_xor_b32 v2,65536,v2
M 30,a[204:207],70
s_barrier
buffer_store_dword v4,v3,s[36:39],s70 offen offset:0 sc0 sc1
M 26,a[192:195],66
s_mov_b32 s70,s69
R 118,13,0
ds_read_b32 v7,v6 offset:1024
M 26,a[200:203],70
s_nop 0
s_mov_b32 s69,s68
s_add_u32 s68,s68,s63
M 26,a[208:211],74
M 26,a[216:219],78
M 30,a[212:215],74
M 30,a[220:223],78
L 4
M 30,a[228:231],82
L 1
v_add_f32 v118,v118,v119
v_add_f32 v120,v120,v121
M 30,a[236:239],86
L 0
v_add_f32 v118,v118,v120
v_mul_f32 v118,s86,v118
M 26,a[224:227],82
buffer_store_dword v118,v14,s[36:39],s76 offen offset:0 sc0 sc1
M 26,a[232:235],86
s_nop 0
v_cmp_eq_u32 vcc,s53,v7
M 26,a[240:243],90
s_or_b64 vcc,vcc,s[94:95]
M 26,a[248:251],94
M 30,a[244:247],90
M 30,a[252:255],94
s_mov_b32 s80,0
s_cmp_eq_u32 vcc_lo,-1
s_cbranch_scc0 Lg2_xn1_%=
s_and_b32 s78,s54,127
s_cmp_eq_u32 s78,s85
s_cselect_b64 s[40:41],-1,0
v_cndmask_b32 v17,v15,v16,s[40:41]
s_add_u32 m0,s57,0
s_add_u32 s56,s55,0
D 17,56,0
v_lshlrev_b32 v106,16,v126
v_and_b32 v107,0xffff0000,v126
v_lshlrev_b32 v108,16,v127
v_and_b32 v109,0xffff0000,v127
v_lshlrev_b32 v110,16,v128
s_add_u32 s56,s55,256
D 17,56,1024
v_and_b32 v111,0xffff0000,v128
v_lshlrev_b32 v112,16,v129
v_and_b32 v113,0xffff0000,v129
v_fma_f32 v106,v106,v103,v102
v_fma_f32 v107,v107,v103,v102
s_add_u32 s56,s55,512
D 17,56,2048
v_fma_f32 v108,v108,v103,v102
v_fma_f32 v109,v109,v103,v102
v_fma_f32 v110,v110,v103,v102
v_fma_f32 v111,v111,v103,v102
v_fma_f32 v112,v112,v103,v102
s_add_u32 s56,s55,768
D 17,56,3072
v_fma_f32 v113,v113,v103,v102
v_add_f32 v26,v26,v106
v_add_f32 v27,v27,v107
v_add_f32 v28,v28,v108
v_add_f32 v29,v29,v109
s_add_u32 m0,s57,4096
s_add_u32 s56,s55,5120
D 17,56,0
v_add_f32 v30,v30,v110
v_add_f32 v31,v31,v111
v_add_f32 v32,v32,v112
v_add_f32 v33,v33,v113
v_cvt_pk_bf16_f32 v114,v26,v27
s_add_u32 s56,s55,5376
D 17,56,1024
v_cvt_pk_bf16_f32 v115,v28,v29
v_cvt_pk_bf16_f32 v116,v30,v31
v_cvt_pk_bf16_f32 v117,v32,v33
s_add_u32 s56,s55,5632
D 17,56,2048
s_add_u32 s56,s55,5888
D 17,56,3072
buffer_store_dwordx4 v[114:117],v0,s[36:39],s67 offen offset:0 sc0 sc1
v_add_f32 v106,v26,v27
v_fma_f32 v107,v26,v26,s87
s_add_u32 s55,s55,s60
s_add_u32 s57,s57,32768
s_and_b32 s57,s57,131071
s_add_u32 s54,s54,s46
v_fma_f32 v107,v27,v27,v107
v_add_f32 v106,v106,v28
v_fma_f32 v107,v28,v28,v107
v_add_f32 v106,v106,v29
v_fma_f32 v107,v29,v29,v107
v_add_f32 v106,v106,v30
v_fma_f32 v107,v30,v30,v107
v_add_f32 v106,v106,v31
v_fma_f32 v107,v31,v31,v107
v_add_f32 v106,v106,v32
v_fma_f32 v107,v32,v32,v107
v_add_f32 v106,v106,v33
v_fma_f32 v107,v33,v33,v107
s_nop 1
v_permlane16_swap_b32 v106,v107
v_add_f32 v106,v106,v107
v_mov_b32 v107,v106
s_nop 1
v_permlane32_swap_b32 v106,v107
v_add_f32 v106,v106,v107
ds_write_b32 v12,v106 offset:2048
L 0
s_barrier
R 118,13,2048
L 0
v_add_f32 v118,v118,v119
v_add_f32 v120,v120,v121
v_add_f32 v118,v118,v120
v_mul_f32 v118,s86,v118
buffer_store_dword v118,v14,s[36:39],s77 offen offset:0 sc0 sc1
s_mov_b32 s72,s69
s_mov_b32 s69,0x7fff0000
s_mov_b32 s67,0x7fff0000
s_mov_b32 s77,0x7fff0000
s_branch Lg2_gox_%=
Lg2_xn1_%=:
s_cmp_ge_u32 s54,256
s_cbranch_scc1 Lg2_fin1_%=
s_add_u32 m0,s59,4096
s_nop 0
buffer_load_dword v5,s[36:39],s58 offen offset:0 sc1 lds
buffer_store_dword v4,v3,s[36:39],s71 offen offset:0 sc0 sc1
s_add_u32 m0,s59,5120
s_nop 0
buffer_load_dword v5,s[36:39],s58 offen offset:0 sc1 lds
buffer_store_dword v4,v3,s[36:39],s71 offen offset:0 sc0 sc1
s_mov_b32 s83,0
v_lshlrev_b32 v106,16,v126
v_and_b32 v107,0xffff0000,v126
v_lshlrev_b32 v108,16,v127
v_and_b32 v109,0xffff0000,v127
v_lshlrev_b32 v110,16,v128
v_and_b32 v111,0xffff0000,v128
v_lshlrev_b32 v112,16,v129
v_and_b32 v113,0xffff0000,v129
v_fma_f32 v106,v106,v103,v102
v_fma_f32 v107,v107,v103,v102
v_fma_f32 v108,v108,v103,v102
v_fma_f32 v109,v109,v103,v102
v_fma_f32 v110,v110,v103,v102
v_fma_f32 v111,v111,v103,v102
v_fma_f32 v112,v112,v103,v102
v_fma_f32 v113,v113,v103,v102
v_add_f32 v26,v26,v106
v_add_f32 v27,v27,v107
v_add_f32 v28,v28,v108
v_add_f32 v29,v29,v109
v_add_f32 v30,v30,v110
v_add_f32 v31,v31,v111
v_add_f32 v32,v32,v112
v_add_f32 v33,v33,v113
v_cvt_pk_bf16_f32 v114,v26,v27
v_cvt_pk_bf16_f32 v115,v28,v29
v_cvt_pk_bf16_f32 v116,v30,v31
v_cvt_pk_bf16_f32 v117,v32,v33
buffer_store_dwordx4 v[114:117],v0,s[36:39],s67 offen offset:0 sc0 sc1
v_add_f32 v106,v26,v27
v_fma_f32 v107,v26,v26,s87
v_fma_f32 v107,v27,v27,v107
v_add_f32 v106,v106,v28
v_fma_f32 v107,v28,v28,v107
v_add_f32 v106,v106,v29
v_fma_f32 v107,v29,v29,v107
v_add_f32 v106,v106,v30
v_fma_f32 v107,v30,v30,v107
v_add_f32 v106,v106,v31
v_fma_f32 v107,v31,v31,v107
v_add_f32 v106,v106,v32
v_fma_f32 v107,v32,v32,v107
v_add_f32 v106,v106,v33
v_fma_f32 v107,v33,v33,v107
s_nop 1
v_permlane16_swap_b32 v106,v107
v_add_f32 v106,v106,v107
v_mov_b32 v107,v106
s_nop 1
v_permlane32_swap_b32 v106,v107
v_add_f32 v106,v106,v107
ds_write_b32 v12,v106 offset:2048
L 0
s_barrier
R 118,13,2048
L 0
v_add_f32 v118,v118,v119
v_add_f32 v120,v120,v121
v_add_f32 v118,v118,v120
v_mul_f32 v118,s86,v118
buffer_store_dword v118,v14,s[36:39],s77 offen offset:0 sc0 sc1
s_mov_b32 s72,s69
s_mov_b32 s69,0x7fff0000
s_mov_b32 s67,0x7fff0000
s_mov_b32 s77,0x7fff0000
s_branch Lg2_poll_%=
Lg2_fin1_%=:
v_lshlrev_b32 v106,16,v126
v_and_b32 v107,0xffff0000,v126
v_lshlrev_b32 v108,16,v127
v_and_b32 v109,0xffff0000,v127
v_lshlrev_b32 v110,16,v128
v_and_b32 v111,0xffff0000,v128
v_lshlrev_b32 v112,16,v129
v_and_b32 v113,0xffff0000,v129
v_fma_f32 v106,v106,v103,v102
v_fma_f32 v107,v107,v103,v102
v_fma_f32 v108,v108,v103,v102
v_fma_f32 v109,v109,v103,v102
v_fma_f32 v110,v110,v103,v102
v_fma_f32 v111,v111,v103,v102
v_fma_f32 v112,v112,v103,v102
v_fma_f32 v113,v113,v103,v102
v_add_f32 v26,v26,v106
v_add_f32 v27,v27,v107
v_add_f32 v28,v28,v108
v_add_f32 v29,v29,v109
v_add_f32 v30,v30,v110
v_add_f32 v31,v31,v111
v_add_f32 v32,v32,v112
v_add_f32 v33,v33,v113
v_cvt_pk_bf16_f32 v114,v26,v27
v_cvt_pk_bf16_f32 v115,v28,v29
v_cvt_pk_bf16_f32 v116,v30,v31
v_cvt_pk_bf16_f32 v117,v32,v33
buffer_store_dwordx4 v[114:117],v0,s[36:39],s67 offen offset:0 sc0 sc1
v_add_f32 v106,v26,v27
v_fma_f32 v107,v26,v26,s87
v_fma_f32 v107,v27,v27,v107
v_add_f32 v106,v106,v28
v_fma_f32 v107,v28,v28,v107
v_add_f32 v106,v106,v29
v_fma_f32 v107,v29,v29,v107
v_add_f32 v106,v106,v30
v_fma_f32 v107,v30,v30,v107
v_add_f32 v106,v106,v31
v_fma_f32 v107,v31,v31,v107
v_add_f32 v106,v106,v32
v_fma_f32 v107,v32,v32,v107
v_add_f32 v106,v106,v33
v_fma_f32 v107,v33,v33,v107
s_nop 1
v_permlane16_swap_b32 v106,v107
v_add_f32 v106,v106,v107
v_mov_b32 v107,v106
s_nop 1
v_permlane32_swap_b32 v106,v107
v_add_f32 v106,v106,v107
ds_write_b32 v12,v106 offset:2048
L 0
s_barrier
R 118,13,2048
L 0
v_add_f32 v118,v118,v119
v_add_f32 v120,v120,v121
v_add_f32 v118,v118,v120
v_mul_f32 v118,s86,v118
buffer_store_dword v118,v14,s[36:39],s77 offen offset:0 sc0 sc1
s_mov_b32 s72,s69
s_mov_b32 s69,0x7fff0000
s_mov_b32 s67,0x7fff0000
s_mov_b32 s77,0x7fff0000
s_branch Lg2_done_%=
Lg2_L10_%=:
v_mfma_f32_16x16x32_bf16 v[22:25],a[4:7],v[34:37],0
s_and_b32 s78,s54,127
s_add_u32 s58,s58,s61
s_add_u32 m0,s59,0
R 62,1,7168
M 22,a[12:15],38
s_cmp_eq_u32 s78,s85
s_cselect_b64 s[40:41],-1,0
s_add_u32 s56,s55,0
buffer_load_dword v5,s[36:39],s58 offen offset:0 sc1 lds
v_mfma_f32_16x16x32_bf16 v[18:21],a[0:3],v[34:37],0
s_add_u32 m0,s57,0
R 66,1,8192
v_cndmask_b32 v17,v15,v16,s[40:41]
M 18,a[8:11],38
L 5
buffer_load_dwordx4 v[122:125],v0,s[36:39],s74 offen offset:0 sc1
M 18,a[16:19],42
s_add_u32 s54,s54,s46
R 70,1,9216
v_lshlrev_b32 v106,16,v126
M 18,a[24:27],46
s_and_b32 s79,s54,127
buffer_load_dwordx2 v[98:99],v11,s[36:39],s73 offen offset:256 sc1
M 22,a[20:23],42
s_cmp_eq_u32 s79,0
R 74,1,10240
v_and_b32 v107,0xffff0000,v126
M 22,a[28:31],46
L 5
D 17,56,0
M 22,a[36:39],50
s_cselect_b32 s94,0x3300,0
s_add_u32 s56,s55,256
R 78,1,11264
M 22,a[44:47],54
s_cmp_eq_u32 s79,127
v_lshlrev_b32 v108,16,v127
v_and_b32 v109,0xffff0000,v127
M 18,a[32:35],50
s_cselect_b32 s94,0xcc00,s94
R 82,1,12288
v_lshlrev_b32 v110,16,v128
M 18,a[40:43],54
L 5
D 17,56,1024
M 18,a[48:51],58
s_add_u32 s56,s55,512
R 86,1,13312
v_and_b32 v111,0xffff0000,v128
M 18,a[56:59],62
s_add_u32 s79,s54,s46
v_lshlrev_b32 v112,16,v129
v_and_b32 v113,0xffff0000,v129
M 22,a[52:55],58
s_and_b32 s79,s79,127
R 90,1,14336
v_fma_f32 v106,v106,v103,v102
M 22,a[60:63],62
L 5
D 17,56,2048
M 22,a[68:71],66
s_add_u32 s56,s55,768
R 94,1,15360
v_fma_f32 v107,v107,v103,v102
M 22,a[76:79],70
)"
R"(s_cmp_eq_u32 s79,0
v_fma_f32 v108,v108,v103,v102
v_fma_f32 v109,v109,v103,v102
M 18,a[64:67],66
s_cselect_b32 s95,0x3300,0
R 34,1,16384
v_fma_f32 v110,v110,v103,v102
M 18,a[72:75],70
L 5
D 17,56,3072
M 18,a[80:83],74
s_add_u32 m0,s57,4096
s_add_u32 s56,s55,5120
R 38,1,17408
M 18,a[88:91],78
s_cmp_eq_u32 s79,127
v_fma_f32 v111,v111,v103,v102
v_fma_f32 v112,v112,v103,v102
M 22,a[84:87],74
s_cselect_b32 s95,0xcc00,s95
R 42,1,18432
v_fma_f32 v113,v113,v103,v102
M 22,a[92:95],78
L 5
D 17,56,0
M 22,a[100:103],82
s_add_u32 s56,s55,5376
R 46,1,19456
v_add_f32 v26,v26,v106
M 22,a[108:111],86
s_add_u32 s57,s57,32768
v_add_f32 v27,v27,v107
v_add_f32 v28,v28,v108
M 18,a[96:99],82
s_mov_b32 s66,s65
R 50,1,20480
v_fma_f32 v107,v26,v26,s87
M 18,a[104:107],86
L 5
D 17,56,1024
M 18,a[112:115],90
s_add_u32 s56,s55,5632
R 54,1,21504
v_add_f32 v106,v26,v27
M 18,a[120:123],94
s_mov_b32 s76,s75
v_fma_f32 v107,v27,v27,v107
v_add_f32 v29,v29,v109
M 22,a[116:119],90
s_add_u32 s73,s73,s64
R 58,1,22528
v_add_f32 v106,v106,v28
M 22,a[124:127],94
L 5
D 17,56,2048
M 22,a[132:135],34
s_add_u32 s56,s55,5888
R 62,1,23552
v_fma_f32 v107,v28,v28,v107
M 22,a[140:143],38
s_add_u32 s74,s74,s62
v_add_f32 v30,v30,v110
v_add_f32 v106,v106,v29
M 18,a[128:131],34
s_add_u32 s55,s55,s60
R 66,1,24576
v_fma_f32 v107,v29,v29,v107
M 18,a[136:139],38
L 5
D 17,56,3072
M 18,a[144:147],42
s_and_b32 s57,s57,131071
R 70,1,25600
v_add_f32 v31,v31,v111
M 18,a[152:155],46
s_add_u32 s65,s65,s62
v_add_f32 v32,v32,v112
v_add_f32 v33,v33,v113
v_add_f32 v106,v106,v30
v_fma_f32 v107,v30,v30,v107
v_add_f32 v106,v106,v31
v_fma_f32 v107,v31,v31,v107
v_add_f32 v106,v106,v32
v_fma_f32 v107,v32,v32,v107
v_add_f32 v106,v106,v33
v_fma_f32 v107,v33,v33,v107
M 22,a[148:151],42
s_add_u32 s75,s75,s63
R 74,1,26624
v_cvt_pk_bf16_f32 v114,v26,v27
M 22,a[156:159],46
L 5
v_permlane16_swap_b32 v106,v107
v_add_f32 v106,v106,v107
v_mov_b32 v107,v106
M 22,a[164:167],50
R 78,1,27648
v_cvt_pk_bf16_f32 v115,v28,v29
M 22,a[172:175],54
v_permlane32_swap_b32 v106,v107
v_add_f32 v106,v106,v107
M 18,a[160:163],50
R 82,1,28672
ds_write_b32 v12,v106 offset:2048
M 18,a[168:171],54
L 6
v_cvt_pk_bf16_f32 v116,v30,v31
v_cvt_pk_bf16_f32 v117,v32,v33
M 18,a[176:179],58
R 86,1,29696
M 18,a[184:187],62
buffer_store_dwordx4 v[114:117],v0,s[36:39],s67 offen offset:0 sc0 sc1
M 22,a[180:183],58
R 90,1,30720
M 22,a[188:191],62
L 6
L 2
M 22,a[196:199],66
s_waitcnt vmcnt(1)
R 94,1,31744
v_xor_b32 v1,65536,v1
M 22,a[204:207],70
s_barrier
buffer_store_dword v4,v3,s[36:39],s70 offen offset:0 sc0 sc1
M 18,a[192:195],66
s_mov_b32 s70,s69
R 34,2,0
R 118,13,2048
M 18,a[200:203],70
s_nop 0
s_mov_b32 s69,s68
ds_read_b32 v7,v6 offset:0
M 18,a[208:211],74
s_add_u32 s68,s68,s63
R 38,2,1024
M 18,a[216:219],78
M 22,a[212:215],74
R 42,2,2048
M 22,a[220:223],78
L 7
M 22,a[228:231],82
L 3
R 46,2,3072
v_add_f32 v118,v118,v119
M 22,a[236:239],86
L 3
v_add_f32 v120,v120,v121
v_add_f32 v118,v118,v120
M 18,a[224:227],82
R 50,2,4096
v_mul_f32 v118,s86,v118
M 18,a[232:235],86
s_nop 0
buffer_store_dword v118,v14,s[36:39],s77 offen offset:0 sc0 sc1
M 18,a[240:243],90
R 54,2,5120
v_cmp_eq_u32 vcc,s53,v7
M 18,a[248:251],94
s_or_b64 vcc,vcc,s[94:95]
s_cmp_eq_u64 vcc,-1
M 22,a[244:247],90
R 58,2,6144
M 22,a[252:255],94
L 5
s_cbranch_scc1 Lg2_L21_%=
s_cmp_eq_u32 vcc_lo,-1
s_cbranch_scc1 Lg2_L11_%=
s_branch Lg2_X1_%=
Lg2_L11_%=:
v_mfma_f32_16x16x32_bf16 v[30:33],a[4:7],v[34:37],0
s_and_b32 s78,s54,127
s_add_u32 s58,s58,s61
s_add_u32 m0,s59,1024
R 62,2,7168
M 30,a[12:15],38
s_cmp_eq_u32 s78,s85
s_cselect_b64 s[40:41],-1,0
s_add_u32 s56,s55,0
buffer_load_dword v5,s[36:39],s58 offen offset:0 sc1 lds
v_mfma_f32_16x16x32_bf16 v[26:29],a[0:3],v[34:37],0
s_add_u32 m0,s57,0
R 66,2,8192
v_cndmask_b32 v17,v15,v16,s[40:41]
M 26,a[8:11],38
L 5
buffer_load_dwordx4 v[126:129],v0,s[36:39],s74 offen offset:0 sc1
M 26,a[16:19],42
s_add_u32 s54,s54,s46
R 70,2,9216
v_lshlrev_b32 v106,16,v122
M 26,a[24:27],46
s_and_b32 s79,s54,127
buffer_load_dwordx2 v[102:103],v11,s[36:39],s73 offen offset:256 sc1
M 30,a[20:23],42
s_cmp_eq_u32 s79,0
R 74,2,10240
v_and_b32 v107,0xffff0000,v122
M 30,a[28:31],46
L 5
D 17,56,0
M 30,a[36:39],50
s_cselect_b32 s94,0x3300,0
s_add_u32 s56,s55,256
R 78,2,11264
M 30,a[44:47],54
s_cmp_eq_u32 s79,127
v_lshlrev_b32 v108,16,v123
v_and_b32 v109,0xffff0000,v123
M 26,a[32:35],50
s_cselect_b32 s94,0xcc00,s94
R 82,2,12288
v_lshlrev_b32 v110,16,v124
M 26,a[40:43],54
L 5
D 17,56,1024
M 26,a[48:51],58
s_add_u32 s56,s55,512
R 86,2,13312
v_and_b32 v111,0xffff0000,v124
M 26,a[56:59],62
s_add_u32 s79,s54,s46
v_lshlrev_b32 v112,16,v125
v_and_b32 v113,0xffff0000,v125
M 30,a[52:55],58
s_and_b32 s79,s79,127
R 90,2,14336
v_fma_f32 v106,v106,v99,v98
M 30,a[60:63],62
L 5
D 17,56,2048
M 30,a[68:71],66
s_add_u32 s56,s55,768
R 94,2,15360
v_fma_f32 v107,v107,v99,v98
M 30,a[76:79],70
s_cmp_eq_u32 s79,0
v_fma_f32 v108,v108,v99,v98
v_fma_f32 v109,v109,v99,v98
M 26,a[64:67],66
s_cselect_b32 s95,0x3300,0
R 34,2,16384
v_fma_f32 v110,v110,v99,v98
M 26,a[72:75],70
L 5
D 17,56,3072
M 26,a[80:83],74
s_add_u32 m0,s57,4096
s_add_u32 s56,s55,5120
R 38,2,17408
M 26,a[88:91],78
s_cmp_eq_u32 s79,127
v_fma_f32 v111,v111,v99,v98
v_fma_f32 v112,v112,v99,v98
M 30,a[84:87],74
s_cselect_b32 s95,0xcc00,s95
R 42,2,18432
v_fma_f32 v113,v113,v99,v98
M 30,a[92:95],78
L 5
D 17,56,0
M 30,a[100:103],82
s_add_u32 s56,s55,5376
R 46,2,19456
v_add_f32 v18,v18,v106
M 30,a[108:111],86
s_add_u32 s57,s57,32768
v_add_f32 v19,v19,v107
v_add_f32 v20,v20,v108
M 26,a[96:99],82
s_mov_b32 s67,s65
R 50,2,20480
v_fma_f32 v107,v18,v18,s87
M 26,a[104:107],86
L 5
D 17,56,1024
M 26,a[112:115],90
s_add_u32 s56,s55,5632
R 54,2,21504
v_add_f32 v106,v18,v19
M 26,a[120:123],94
s_mov_b32 s77,s75
v_fma_f32 v107,v19,v19,v107
v_add_f32 v21,v21,v109
M 30,a[116:119],90
s_add_u32 s73,s73,s64
R 58,2,22528
v_add_f32 v106,v106,v20
M 30,a[124:127],94
L 5
D 17,56,2048
M 30,a[132:135],34
s_add_u32 s56,s55,5888
R 62,2,23552
v_fma_f32 v107,v20,v20,v107
M 30,a[140:143],38
s_add_u32 s74,s74,s62
v_add_f32 v22,v22,v110
v_add_f32 v106,v106,v21
M 26,a[128:131],34
s_add_u32 s55,s55,s60
R 66,2,24576
v_fma_f32 v107,v21,v21,v107
M 26,a[136:139],38
L 5
D 17,56,3072
M 26,a[144:147],42
s_and_b32 s57,s57,131071
R 70,2,25600
v_add_f32 v23,v23,v111
M 26,a[152:155],46
s_add_u32 s65,s65,s62
v_add_f32 v24,v24,v112
v_add_f32 v25,v25,v113
v_add_f32 v106,v106,v22
v_fma_f32 v107,v22,v22,v107
v_add_f32 v106,v106,v23
v_fma_f32 v107,v23,v23,v107
v_add_f32 v106,v106,v24
v_fma_f32 v107,v24,v24,v107
v_add_f32 v106,v106,v25
v_fma_f32 v107,v25,v25,v107
M 30,a[148:151],42
s_add_u32 s75,s75,s63
R 74,2,26624
v_cvt_pk_bf16_f32 v114,v18,v19
M 30,a[156:159],46
L 5
v_permlane16_swap_b32 v106,v107
v_add_f32 v106,v106,v107
v_mov_b32 v107,v106
M 30,a[164:167],50
R 78,2,27648
v_cvt_pk_bf16_f32 v115,v20,v21
M 30,a[172:175],54
v_permlane32_swap_b32 v106,v107
v_add_f32 v106,v106,v107
M 26,a[160:163],50
R 82,2,28672
ds_write_b32 v12,v106 offset:0
M 26,a[168:171],54
L 6
v_cvt_pk_bf16_f32 v116,v22,v23
v_cvt_pk_bf16_f32 v117,v24,v25
M 26,a[176:179],58
R 86,2,29696
M 26,a[184:187],62
buffer_store_dwordx4 v[114:117],v0,s[36:39],s66 offen offset:0 sc0 sc1
M 30,a[180:183],58
R 90,2,30720
M 30,a[188:191],62
L 6
L 2
M 30,a[196:199],66
s_waitcnt vmcnt(1)
R 94,2,31744
v_xor_b32 v2,65536,v2
M 30,a[204:207],70
s_barrier
buffer_store_dword v4,v3,s[36:39],s70 offen offset:0 sc0 sc1
M 26,a[192:195],66
s_mov_b32 s70,s69
R 34,1,0
R 118,13,0
M 26,a[200:203],70
s_nop 0
s_mov_b32 s69,s68
ds_read_b32 v7,v6 offset:1024
M 26,a[208:211],74
s_add_u32 s68,s68,s63
R 38,1,1024
M 26,a[216:219],78
M 30,a[212:215],74
R 42,1,2048
M 30,a[220:223],78
L 7
M 30,a[228:231],82
L 3
R 46,1,3072
v_add_f32 v118,v118,v119
M 30,a[236:239],86
L 3
v_add_f32 v120,v120,v121
v_add_f32 v118,v118,v120
M 26,a[224:227],82
R 50,1,4096
v_mul_f32 v118,s86,v118
M 26,a[232:235],86
s_nop 0
buffer_store_dword v118,v14,s[36:39],s76 offen offset:0 sc0 sc1
M 26,a[240:243],90
R 54,1,5120
v_cmp_eq_u32 vcc,s53,v7
M 26,a[248:251],94
s_or_b64 vcc,vcc,s[94:95]
s_cmp_eq_u64 vcc,-1
M 30,a[244:247],90
R 58,1,6144
M 30,a[252:255],94
L 5
s_cbranch_scc1 Lg2_L20_%=
s_cmp_eq_u32 vcc_lo,-1
s_cbranch_scc1 Lg2_L10_%=
s_branch Lg2_X0_%=
Lg2_done_%=:
s_waitcnt vmcnt(2)
s_barrier
buffer_store_dword v4,v3,s[36:39],s70 offen offset:0 sc0 sc1
s_waitcnt vmcnt(1)
s_barrier
buffer_store_dword v4,v3,s[36:39],s72 offen offset:0 sc0 sc1
s_waitcnt vmcnt(0) lgkmcnt(0)
s_barrier
Lg2_end_%=:
s_mov_b32 %0,s53
Lcode_g12_e:
.purgem M
.purgem R
.purgem T
.purgem D
.purgem L
)"
  : "=&s"(epOut) : "s"(ws), "s"(sWave), "s"(sG0), "s"(sStep), "s"(sOutOff), "s"(sFoutOff), "s"(sAuxOff), "s"(sWw), "s"(sBuf), "s"(sRk), "v"(vL16), "v"(vFlag), "v"(vSnapOff), "v"(vSnapRd), "v"(vWd0), "v"(vWd1), "v"(vWr0), "v"(vWr1), "v"(vWr2), "v"(vOutS), "v"(vAux0), "v"(vAux1), "v"(vAux2), "v"(vAux3), "v"(vAn), "v"(vAe), "s"(w12) : "v0", "v1", "v2", "v3", "v4", "v5", "v6", "v7", "v8", "v9", "v10", "v11", "v12", "v13", "v14", "v15", "v16", "v17", "v18", "v19", "v20", "v21", "v22", "v23", "v24", "v25", "v26", "v27", "v28", "v29", "v30", "v31", "v32", "v33", "v34", "v35", "v36", "v37", "v38", "v39", "v40", "v41", "v42", "v43", "v44", "v45", "v46", "v47", "v48", "v49", "v50", "v51", "v52", "v53", "v54", "v55", "v56", "v57", "v58", "v59", "v60", "v61", "v62", "v63", "v64", "v65", "v66", "v67", "v68", "v69", "v70", "v71", "v72", "v73", "v74", "v75", "v76", "v77", "v78", "v79", "v80", "v81", "v82", "v83", "v84", "v85", "v86", "v87", "v88", "v89", "v90", "v91", "v92", "v93", "v94", "v95", "v96", "v97", "v98", "v99", "v100", "v101", "v102", "v103", "v104", "v105", "v106", "v107", "v108", "v109", "v110", "v111", "v112", "v113", "v114", "v115", "v116", "v117", "v118", "v119", "v120", "v121", "v122", "v123", "v124", "v125", "v126", "v127", "v128", "v129", "v130", "v131", "v132", "v133", "v134", "v135", "v136", "v137", "v138", "v139", "v140", "v141", "v142", "v143", "v144", "v145", "v146", "v147", "v148", "v149", "v150", "v151", "v152", "v153", "v154", "v155", "v156", "v157", "v158", "v159", "v160", "v161", "v162", "v163", "v164", "v165", "v166", "v167", "v168", "v169", "v170", "v171", "v172", "v173", "v174", "v175", "v176", "v177", "v178", "v179", "v180", "v181", "v182", "v183", "v184", "v185", "v186", "v187", "v188", "v189", "v190", "v191", "v192", "v193", "v194", "v195", "v196", "v197", "v198", "v199", "v200", "v201", "v202", "v203", "v204", "v205", "v206", "v207", "v208", "v209", "v210", "v211", "v212", "v213", "v214", "v215", "v216", "v217", "v218", "v219", "v220", "v221", "v222", "v223", "v224", "v225", "v226", "v227", "v228", "v229", "v230", "v231", "a0", "a1", "a2", "a3", "a4", "a5", "a6", "a7", "a8", "a9", "a10", "a11", "a12", "a13", "a14", "a15", "a16", "a17", "a18", "a19", "a20", "a21", "a22", "a23", "a24", "a25", "a26", "a27", "a28", "a29", "a30", "a31", "a32", "a33", "a34", "a35", "a36", "a37", "a38", "a39", "a40", "a41", "a42", "a43", "a44", "a45", "a46", "a47", "a48", "a49", "a50", "a51", "a52", "a53", "a54", "a55", "a56", "a57", "a58", "a59", "a60", "a61", "a62", "a63", "a64", "a65", "a66", "a67", "a68", "a69", "a70", "a71", "a72", "a73", "a74", "a75", "a76", "a77", "a78", "a79", "a80", "a81", "a82", "a83", "a84", "a85", "a86", "a87", "a88", "a89", "a90", "a91", "a92", "a93", "a94", "a95", "a96", "a97", "a98", "a99", "a100", "a101", "a102", "a103", "a104", "a105", "a106", "a107", "a108", "a109", "a110", "a111", "a112", "a113", "a114", "a115", "a116", "a117", "a118", "a119", "a120", "a121", "a122", "a123", "a124", "a125", "a126", "a127", "a128", "a129", "a130", "a131", "a132", "a133", "a134", "a135", "a136", "a137", "a138", "a139", "a140", "a141", "a142", "a143", "a144", "a145", "a146", "a147", "a148", "a149", "a150", "a151", "a152", "a153", "a154", "a155", "a156", "a157", "a158", "a159", "a160", "a161", "a162", "a163", "a164", "a165", "a166", "a167", "a168", "a169", "a170", "a171", "a172", "a173", "a174", "a175", "a176", "a177", "a178", "a179", "a180", "a181", "a182", "a183", "a184", "a185", "a186", "a187", "a188", "a189", "a190", "a191", "a192", "a193", "a194", "a195", "a196", "a197", "a198", "a199", "a200", "a201", "a202", "a203", "a204", "a205", "a206", "a207", "a208", "a209", "a210", "a211", "a212", "a213", "a214", "a215", "a216", "a217", "a218", "a219", "a220", "a221", "a222", "a223", "a224", "a225", "a226", "a227", "a228", "a229", "a230", "a231", "a232", "a233", "a234", "a235", "a236", "a237", "a238", "a239", "a240", "a241", "a242", "a243", "a244", "a245", "a246", "a247", "a248", "a249", "a250", "a251", "a252", "a253", "a254", "a255", "s36", "s37", "s38", "s39", "s40", "s41", "s42", "s43", "s44", "s45", "s46", "s47", "s48", "s49", "s50", "s51", "s52", "s53", "s54", "s55", "s56", "s57", "s58", "s59", "s60", "s61", "s62", "s63", "s64", "s65", "s66", "s67", "s68", "s69", "s70", "s71", "s72", "s73", "s74", "s75", "s76", "s77", "s78", "s79", "s80", "s81", "s82", "s83", "s84", "s85", "s86", "s87", "s88", "s89", "s90", "s91", "s92", "s93", "s94", "s95", "s96", "s97", "vcc", "scc", "memory");
      ep = epOut;
    }
    LANE_ID(lane2);
    for (int t = kind == 6 ? 64 + slice : (kind - 1) * 32 + rep * 8 + slice; t < 256; t += 64 + NRW)
      row_last(ws, out, t, lane2, wave, ep);
    CTS(14);
    if (b == 1 && lane2 == 0 && wave == 0) __hip_atomic_store((gu32*)(ws + OFF_EP), ep, __ATOMIC_RELAXED, __HIP_MEMORY_SCOPE_AGENT);
    asm volatile("s_endpgm\nLcode_end:" ::: "memory");
    __builtin_unreachable();
  }
}

extern "C" void kernel_launch(void* const* d_in, const int* in_sizes, int n_in, void* d_out, int out_size,
                              void* d_ws, size_t ws_size, hipStream_t stream) {
  const u16* q = (const u16*)d_in[0];
  const u16* k = (const u16*)d_in[1];
  const u16* v = (const u16*)d_in[2];
  const u16* shift_k = (const u16*)d_in[3];
  const u16* ssm_k = (const u16*)d_in[4];
  const u16* ssm_D = (const u16*)d_in[5];
  const u16* w1 = (const u16*)d_in[7];
  const u16* w2 = (const u16*)d_in[8];
  const u16* w3 = (const u16*)d_in[9];
  const u16* w4 = (const u16*)d_in[10];
  flow_kernel<<<256, 256, 0, stream>>>(q, k, v, shift_k, ssm_k, ssm_D, w1, w2, w3, w4, (char*)d_ws, (u16*)d_out);
}
```

```cpp
#include <hip/hip_runtime.h>
#include <hip/hip_bf16.h>
#include <stdint.h>
#include <type_traits>

typedef short s16x8 __attribute__((ext_vector_type(8)));
typedef short s16x4 __attribute__((ext_vector_type(4)));
typedef float f32x4 __attribute__((ext_vector_type(4)));
typedef unsigned short u16;
typedef unsigned short u16x8 __attribute__((ext_vector_type(8)));
typedef unsigned short u16x4 __attribute__((ext_vector_type(4)));
typedef float f32x2 __attribute__((ext_vector_type(2)));
typedef __bf16 bf16x2 __attribute__((ext_vector_type(2)));
typedef unsigned u32x4v __attribute__((ext_vector_type(4)));
typedef __attribute__((address_space(1))) unsigned gu32;

constexpr int S_ = 2048, D_ = 1024, F_ = 3584;
#define PF 1

constexpr int WS_USED = 111243392, LAST_TASKS = 236;
constexpr int OFF_KVR = 4194304, OFF_XP = 8388608, OFF_HS = 17825792, OFF_X1 = 31457280, OFF_G = 40894464, OFF_P0 = 71303168, OFF_P1 = 89128960, OFF_Y = 98566144, OFF_S1 = 106954752, OFF_ST = 107479040, OFF_FL = 109051904, OFF_FXP = 109056000, OFF_FH = 109334528, OFF_FX1 = 109613056, OFF_FG = 109891584, OFF_FP = 110587904, OFF_FKV = 111144960, OFF_FY = 111177728, OFF_EP = 111243264, OFF_DBG = 115343360;

#define WS_DIAG 0

__device__ __forceinline__ float bf2f(u16 v) { return __uint_as_float(((unsigned)v) << 16); }
__device__ __forceinline__ u16 f2bf(float f) { return __builtin_bit_cast(u16, (__bf16)f); }
__device__ __forceinline__ float rbf(float f) { return bf2f(f2bf(f)); }
__device__ __forceinline__ void st16(void* p, u16x8 v) {
  asm volatile("global_store_dwordx4 %0, %1, off sc0 sc1\n\ts_nop 1" :: "v"(p), "v"(v) : "memory");
}
#define WAIT_VM(n) asm volatile("s_waitcnt vmcnt(%0)" ::"n"(n) : "memory")
#define WAIT_LGKM0() asm volatile("s_waitcnt lgkmcnt(0)" ::: "memory")

#define CODE_RANGE(B, E, p, n)                                                                                   \
  const char* p; unsigned n;                                                                                     \
  { unsigned lo_, hi_;                                                                                           \
    asm volatile("s_getpc_b64 s[98:99]\nLpc%=:\n\ts_add_u32 %0, s98, (" B "-Lpc%=)&4294967295\n\ts_addc_u32 %1, s99, (" B "-Lpc%=)>>32\n\t" \
                 "s_mov_b32 %2, " E "-" B : "=s"(lo_), "=s"(hi_), "=s"(n) :: "s98", "s99", "scc");               \
    p = (const char*)(((unsigned long long)hi_ << 32) | lo_); }
__device__ __forceinline__ unsigned touch(const char* p, unsigned n, unsigned off) {
  unsigned t;
  asm volatile("global_load_dword %0, %1, off" : "=v"(t) : "v"(p + (off < n ? off : 0u)) : "memory");
  return t;
}

__device__ __forceinline__ void touch_lds(const char* p, unsigned a) {
  asm volatile("s_mov_b32 m0, %1\n\ts_nop 0\n\tglobal_load_lds_dword %0, off" :: "v"(p), "s"(a) : "memory");
}
#define PFPOL ""
__device__ __forceinline__ void touch_pol(const char* p, unsigned a) {
  asm volatile("s_mov_b32 m0, %1\n\ts_nop 0\n\tglobal_load_lds_dword %0, off " PFPOL :: "v"(p), "s"(a) : "memory");
}
#define YSLEEP 4
#define KSLEEP 2
#define CTS(i)
__device__ __forceinline__ f32x2 up2(unsigned p) { return (f32x2){__uint_as_float(p << 16), __uint_as_float(p & 0xffff0000u)}; }
__device__ __forceinline__ unsigned pk2(f32x2 v) { return __builtin_bit_cast(unsigned, __builtin_convertvector(v, bf16x2)); }
__device__ __forceinline__ f32x2 rbf2(f32x2 v) { return up2(pk2(v)); }
template <int N, class F> __device__ __forceinline__ void sfor(F&& f) {
  if constexpr (N > 0) { sfor<N - 1>(f); f(std::integral_constant<int, N - 1>{}); }
}
__device__ __forceinline__ void set_flag(char* p, int i, unsigned ep) {
  if (i < 32) __hip_atomic_store((gu32*)p + i, ep, __ATOMIC_RELAXED, __HIP_MEMORY_SCOPE_AGENT);
}
template <int NL>
__device__ __forceinline__ void poll_flags(const char* p, int n, int lane, unsigned ep) {
  for (int it = 0; it < 20000; ++it) {
    bool ok = true;
#pragma unroll
    for (int i = 0; i < NL; ++i) {
      const int f = lane + 64 * i;
      ok &= __hip_atomic_load((gu32*)(p + (f < n ? f : 0) * 128), __ATOMIC_RELAXED, __HIP_MEMORY_SCOPE_AGENT) == ep;
    }
    if (__all(ok)) break;
    __builtin_amdgcn_s_sleep(YSLEEP);
  }
  asm volatile("" ::: "memory");
}

constexpr int CV_HP = (2048 + 32) * 2, CV_UR = (2048 + 256) * 2, CV_CH = 2 * CV_HP + CV_UR;
constexpr int NCV = 174;
constexpr int CV_STG = 8 * CV_CH;
constexpr int YP = D_ * 16;

struct KvRegs { u32x4v k0, k1, k2, k3, k4, k5, k6, v0, v1, v2, v3, s0, s1, s2, s3; };
#define KV_LOADS                                                                                                                       \
  "v_lshrrev_b32 %[a], 3, %[tid]\n\tv_and_b32 %[c], 7, %[tid]\n\ts_lshr_b32 s90, %[tile], 4\n\ts_and_b32 s91, %[tile], 15\n\t"          \
  "v_lshlrev_b32 %[a], 13, %[a]\n\tv_lshlrev_b32 %[c], 4, %[c]\n\ts_lshl_b32 s90, s90, 18\n\ts_lshl_b32 s91, s91, 7\n\t"                 \
  "v_add_u32 %[a], s90, %[a]\n\tv_add_u32 %[c], s91, %[c]\n\tv_subrev_u32 %[h], 0x1000, %[a]\n\tv_max_i32 %[h], 0x800, %[h]\n\t"         \
  "v_add3_u32 %[a], %[a], %[c], s92\n\tv_add_u32 %[h], %[h], %[c]\n\tv_lshlrev_b32 %[c], 2, %[c]\n\ts_waitcnt lgkmcnt(0)\n\t"            \
  "global_load_dwordx4 %3, %[a], s[76:77] offset:-4096 nt\n\tglobal_load_dwordx4 %4, %[a], s[76:77] offset:-2048 nt\n\t"                \
  "global_load_dwordx4 %5, %[a], s[76:77] nt\n\tglobal_load_dwordx4 %6, %[a], s[76:77] offset:2048 nt\n\t"                              \
  "global_load_dwordx4 %7, %[a], s[78:79] offset:-4096 nt\n\tglobal_load_dwordx4 %8, %[a], s[78:79] offset:-2048 nt\n\t"                \
  "global_load_dwordx4 %9, %[a], s[78:79] nt\n\tglobal_load_dwordx4 %10, %[a], s[78:79] offset:2048 nt\n\t"                             \
  "global_load_dwordx4 %0, %[h], s[76:77] offset:-2048 nt\n\tglobal_load_dwordx4 %1, %[h], s[76:77] nt\n\t"                             \
  "global_load_dwordx4 %2, %[h], s[76:77] offset:2048 nt\n\t"                                                                           \
  "global_load_dwordx4 %11, %[c], s[80:81]\n\tglobal_load_dwordx4 %12, %[c], s[80:81] offset:16\n\t"                                    \
  "global_load_dwordx4 %13, %[c], s[80:81] offset:32\n\tglobal_load_dwordx4 %14, %[c], s[80:81] offset:48\n\t"
#define KV_OUTS(R) "=&v"(R.k0), "=&v"(R.k1), "=&v"(R.k2), "=&v"(R.k3), "=&v"(R.k4), "=&v"(R.k5), "=&v"(R.k6), "=&v"(R.v0), "=&v"(R.v1), "=&v"(R.v2), \
                   "=&v"(R.v3), "=&v"(R.s0), "=&v"(R.s1), "=&v"(R.s2), "=&v"(R.s3), [a] "=&v"(ta), [h] "=&v"(th), [c] "=&v"(tc)
#define KV_CLOB "s74", "s75", "s76", "s77", "s78", "s79", "s80", "s81", "s82", "s83", "s90", "s91", "s92", "scc", "vcc", "memory"
template <int N, int N2>
__device__ __forceinline__ void kv_wait(KvRegs& R, int tile) {
  asm volatile("s_cmp_ge_u32 %17, 192\n\ts_cbranch_scc1 Lkw%=\n\ts_waitcnt vmcnt(%15)\nLkw%=:\n\ts_waitcnt vmcnt(%16)"
               : "+v"(R.k0), "+v"(R.k1), "+v"(R.k2), "+v"(R.k3), "+v"(R.k4), "+v"(R.k5), "+v"(R.k6), "+v"(R.v0), "+v"(R.v1), "+v"(R.v2),
                 "+v"(R.v3), "+v"(R.s0), "+v"(R.s1), "+v"(R.s2), "+v"(R.s3) : "n"(N), "n"(N2), "s"(tile) : "scc", "memory");
}
__device__ __forceinline__ void kv_store(KvRegs& R, char* __restrict__ ws, char* T, int b, int tid) {
  const int t0 = (b >> 4) * 128, c0 = (b & 15) * 64, ch8 = tid & 7, rg = tid >> 3;
  if (t0 + 4 * rg < 3) R.k0 = R.k1 = R.k2 = (u32x4v){0u, 0u, 0u, 0u};
  typedef unsigned u32x2 __attribute__((ext_vector_type(2)));
  const u32x4v kr[7] = {R.k0, R.k1, R.k2, R.k3, R.k4, R.k5, R.k6}, vr[4] = {R.v0, R.v1, R.v2, R.v3}, sk[4] = {R.s0, R.s1, R.s2, R.s3};
#pragma unroll
  for (int j2 = 0; j2 < 4; ++j2) {
    const f32x2 sa = up2(sk[j2][0]), sb = up2(sk[j2][1]), sc = up2(sk[j2][2]), sd = up2(sk[j2][3]);
    const f32x2 s0 = {sa[0], sc[0]}, s1 = {sa[1], sc[1]}, s2 = {sb[0], sd[0]}, s3 = {sb[1], sd[1]};
    f32x2 kf[7], o[4];
#pragma unroll
    for (int r = 0; r < 7; ++r) kf[r] = up2(kr[r][j2]);
#pragma unroll
    for (int r = 0; r < 4; ++r)
      o[r] = __builtin_elementwise_fma(kf[r + 3], s0, __builtin_elementwise_fma(kf[r + 2], s1, __builtin_elementwise_fma(kf[r + 1], s2, kf[r] * s3))) * up2(vr[r][j2]);
#pragma unroll
    for (int e = 0; e < 2; ++e)
      *(u32x2*)(T + (8 * ch8 + 2 * j2 + e) * 264 + (124 - 4 * rg) * 2) = (u32x2){pk2((f32x2){o[3][e], o[2][e]}), pk2((f32x2){o[1][e], o[0][e]})};
  }
  __syncthreads();
  u16* kvr = (u16*)(ws + OFF_KVR);
#pragma unroll
  for (int i = 0; i < 4; ++i) {
    const int idx = tid + 256 * i, ch = idx >> 4, cb = idx & 15;
    const u16x4 lo = *(const u16x4*)(T + ch * 264 + cb * 16), hi = *(const u16x4*)(T + ch * 264 + cb * 16 + 8);
    st16(kvr + (size_t)(c0 + ch) * S_ + (1920 - t0) + cb * 8, __builtin_shufflevector(lo, hi, 0, 1, 2, 3, 4, 5, 6, 7));
  }
}

#define LASTSLEEP 4
__device__ __forceinline__ void wait_flags(const char* p, unsigned ep) {
  gu32* f = (gu32*)p;
  for (int it = 0; it < 20000; ++it) {
    if (__all(__hip_atomic_load(f, __ATOMIC_RELAXED, __HIP_MEMORY_SCOPE_AGENT) == ep)) break;
    __builtin_amdgcn_s_sleep(LASTSLEEP);
  }
  asm volatile("" ::: "memory");
}
__device__ __forceinline__ void row_last(char* __restrict__ ws, u16* __restrict__ out, int t, int lane, int wave, unsigned ep) {
  const __amdgpu_buffer_rsrc_t rs = __builtin_amdgcn_make_buffer_rsrc(ws, 0, WS_USED, 0x00020000);
  const int r8 = lane & 7, seg = lane >> 3, un = 128 + (t >> 1);
  const unsigned fo = un * 32768 + (wave * 8 + (seg >> 2)) * 1024 + ((seg & 3) * 16 + (t & 1) * 8 + r8) * 16;
  wait_flags(ws + OFF_FP + un * 2048 + ((lane & 2) * 4 + wave * 2 + (lane & 1)) * 128, ep);
  u32x4v p0[4], p1[4];
#pragma unroll
  for (int i = 0; i < 4; ++i) {
    p0[i] = __builtin_bit_cast(u32x4v, __builtin_amdgcn_raw_buffer_load_b128(rs, fo + i * 2048, OFF_P0, 16));
    p1[i] = __builtin_bit_cast(u32x4v, __builtin_amdgcn_raw_buffer_load_b128(rs, fo + i * 2048, OFF_P1, 16));
  }
#pragma unroll
  for (int i = 0; i < 4; ++i) {
    u32x4v o;
#pragma unroll
    for (int e = 0; e < 4; ++e) o[e] = pk2(up2(p0[i][e]) + up2(p1[i][e]));
    st16(out + (size_t)(t * 8 + r8) * D_ + wave * 256 + i * 64 + seg * 8, __builtin_bit_cast(u16x8, o));
  }
}

__device__ __forceinline__ int swzB(int r) { return ((((r >> 1) & 1) | (((r >> 3) & 1) << 1)) << 1); }
#define TCH 1
#define LANE_ID(x) int x; asm volatile("v_mbcnt_lo_u32_b32 %0, -1, 0\n\tv_mbcnt_hi_u32_b32 %0, -1, %0" : "=v"(x))
#define T2X 5
#define W12 3
#define HL 0
#define W_B1 (b == 1)
#define W_B7 (b == 7)
#define W_I15 (idx == 15)
#define W_I31 (idx == 31)
#define W_B249 (b == 249)
#define QTB 1
constexpr int QT0 = 34 * 128;
#define CODE_MAX(x, y) "(((" x ">" y ")&" x ")|((" x "<=" y ")&" y "))"
constexpr int HU = 40960;
#define G3OR 5
constexpr int NRW = 22 - G3OR;
__global__ __launch_bounds__(256, 1) void flow_kernel(const u16* __restrict__ q, const u16* __restrict__ kin, const u16* __restrict__ vin,
                                                      const u16* __restrict__ shift_k, const u16* __restrict__ ssm_k, const u16* __restrict__ ssm_D,
                                                      const u16* __restrict__ w1, const u16* __restrict__ w2,
                                                      const u16* __restrict__ w3, const u16* __restrict__ w4, char* __restrict__ ws,
                                                      u16* __restrict__ out) {
  __shared__ __attribute__((aligned(16))) char lds[163840];
  KvRegs KR, KR2;
  unsigned ew, ta, th, tc;
  int b, tile;
  asm volatile("Lcode_b:\n\ts_load_dwordx4 s[76:79], %[ka], 0x8\n\ts_load_dwordx2 s[82:83], %[ka], 0x50\n\t"
               "s_getpc_b64 s[74:75]\n\ts_lshr_b32 s90, %[bi], 3\n\ts_mov_b32 m0, 0x27f00\n\tv_lshlrev_b32 %[a], 5, %[tid]\n\tv_add_lshl_u32 %[a], %[a], s90, 7\n\t"
               "v_min_u32 %[a], " CODE_MAX(CODE_MAX(CODE_MAX("(Lcode_g3_e-Lcode_b)", "(Lcode_g3o_e-Lcode_b)"), CODE_MAX("(Lcode_g4_e-Lcode_b)", "(Lcode_g12_e-Lcode_b)")), "(Lcode_end-Lcode_b)") "-64, %[a]\n\t"
               "global_load_lds_dword %[a], s[74:75]\n\ts_load_dwordx2 s[80:81], %[ka], 0x18\n\t"
               "s_mov_b32 %[b], %[bi]\n\ts_bfe_u32 s91, %[bi], 0x20001\n\ts_lshl2_add_u32 %[tile], s90, s91\n\ts_movk_i32 s92, 0x1000\n\t"
               "s_bitcmp1_b32 %[bi], 0\n\ts_cbranch_scc0 Lk1%=\n\ts_add_u32 %[tile], %[tile], 64\n\ts_cmp_lt_u32 s90, 8\n\ts_cbranch_scc1 Lk2%=\n\t"
               "s_lshl3_add_u32 s91, s91, s90\n\ts_add_u32 s91, s91, 184\n\ts_cmp_lt_u32 s90, 16\n\ts_cselect_b32 %[tile], s91, %[tile]\nLk1%=:\n\t"
               KV_LOADS "v_mov_b32 %[a], %[eo]\n\tglobal_load_dword %[ew], %[a], s[82:83] sc1\nLk2%=:\n\ts_waitcnt lgkmcnt(0)"
               : KV_OUTS(KR), [ew] "=&v"(ew), [b] "=&s"(b), [tile] "=&s"(tile)
               : [ka] "s"(__builtin_amdgcn_kernarg_segment_ptr()), [bi] "s"(blockIdx.x), [tid] "v"(threadIdx.x), [eo] "n"(OFF_EP) : KV_CLOB);
  const unsigned t_start = 0;
  const int tid = threadIdx.x, lane = tid & 63, wave = __builtin_amdgcn_readfirstlane(tid >> 6);
  const int xcd = b & 7, idx = b >> 3, tile2 = (tile + 32) ^ T2X;
  asm volatile("s_cmp_lt_u32 %[tile], 224\n\ts_cbranch_scc1 Lk3%=\n\ts_load_dwordx4 s[76:79], %[ka], 0x8\n\ts_load_dwordx2 s[80:81], %[ka], 0x18\n\ts_movk_i32 s92, 0x1000\n\t"
               KV_LOADS "\nLk3%=:"
               : KV_OUTS(KR2) : [ka] "s"(__builtin_amdgcn_kernarg_segment_ptr()), [tile] "s"(tile2), [tid] "v"(threadIdx.x) : KV_CLOB);
  const unsigned sLds = (unsigned)(size_t)lds;
  const int o8 = idx & 7;
  int kind, rep, slice, kh = 0;
  int j3 = 0, j4 = 0;
  const int xp = xcd >> 1, pr = xp & 1;
  if (!(xcd & 1)) {
    if (idx < (xp < 2 ? 24 : 16)) { kind = 4; rep = (xp < 2 ? 0 : 3) + (idx >> 3); kh = pr; slice = o8; j4 = rep * 16 + kh * 8 + o8; }
    else { kind = 3; rep = xp < 2 ? 4 : pr * 2 + ((idx >> 3) & 1); slice = xp < 2 ? pr * 8 + o8 : o8; j3 = rep * 18 + slice; }
  } else if (idx < 16) { kind = 1 + (idx >> 3); rep = xp; slice = o8; }
  else {
    const int k = idx - 16, n3 = xp < 2 ? 10 : 11, no = xp == 2 ? 2 : 1;
    if (k < n3) { kind = 3; rep = k < 10 ? pr * 2 + (k >= 5) : 4; slice = k < 10 ? (xp < 2 ? 8 : 13) + k - 5 * (k >= 5) : 16 + pr; j3 = rep * 18 + slice; }
    else if (k < n3 + no) { kind = 5; rep = k > n3 ? 4 : pr * 2 + (xp >> 1); slice = 18; j3 = 90 + rep; }
    else { kind = 6; rep = 0; slice = (xp < 3 ? xp * 5 : 13) + k - n3 - no; }
  }
  CTS(0);
  const unsigned sScr = sLds + 163584;
  unsigned ep = 0;
  auto w_touch = [&](const u16* w) {
#pragma unroll
    for (int i = 0; i < 2; ++i) touch_lds((const char*)w + (size_t)(rep * 256 + i * 128 + (tid >> 1)) * 2048 + slice * 256 + (tid & 1) * 128, sScr);
  };
  if (kind == 1) { if (W12 & 1) w_touch(w1); }
  else {
  const bool isconv = kind >= 3 && kind != 6 && j3 < 94;
  const int nw = (kind == 4 || j3 >= 92) ? 2 : 1, c0 = kind == 4 ? 8 * j4 : j3 < 92 ? 640 + 4 * j3 : j3 < 94 ? 272 + 8 * j3 : 0, cw = c0 + nw * wave;
  const unsigned hd = isconv ? sLds + 2 * wave * CV_CH + 32 : 0x30000u;
  auto h_dma = [&](bool first) {
#pragma unroll
    for (int j = 0; j < 2; ++j) {
      const u16* hp = ssm_k + (size_t)cw * S_ + (isconv && j < nw ? j * S_ + lane * 8 : 0);
      if (first) asm volatile("s_mov_b32 m0, %1\n\ts_nop 0\n\tglobal_load_lds_dwordx4 %0, off nt" :: "v"(hp), "s"(hd + j * CV_CH) : "memory");
      else asm volatile("s_mov_b32 m0, %1\n\ts_nop 0\n\tglobal_load_lds_dwordx4 %0, off offset:1024 nt\n\t"
                        "global_load_lds_dwordx4 %0, off offset:2048 nt\n\tglobal_load_lds_dwordx4 %0, off offset:3072 nt" :: "v"(hp), "s"(hd + j * CV_CH) : "memory");
    }
  };
  h_dma(true);
  if (HL == 0) h_dma(false);
  *(u32x4v*)(lds + (2 * wave + (lane >> 5)) * CV_CH + 2 * CV_HP + 4096 + (lane & 31) * 16) = (u32x4v){0u, 0u, 0u, 0u};
  if (lane < 8) *(u32x4v*)(lds + (2 * wave + (lane >> 2)) * CV_CH + ((lane >> 1) & 1) * CV_HP + (lane & 1) * 16) = (u32x4v){0u, 0u, 0u, 0u};
  kv_wait<HL ? 3 : 9, HL ? 18 : 24>(KR, tile);
  CTS(12);
  kv_store(KR, ws, lds + CV_STG, tile, tid);
  kv_wait<63, HL ? 6 : 12>(KR2, tile);
  if (kind == 2) kv_store(KR2, ws, lds + CV_STG + 17408, tile2, tid);
  CTS(1);
  if (HL == 1) h_dma(false);
  const unsigned dw = ((const unsigned*)ssm_D)[cw >> 1];
  asm volatile("s_waitcnt vmcnt(%1)" : "+v"(ew) : "n"(HL == 1 ? 6 : 0) : "memory");
  ep = __builtin_amdgcn_readfirstlane(ew) + 1u;
  if (ep == 0u) ep = 1u;
  __builtin_amdgcn_s_barrier();
  set_flag(ws + OFF_FKV + ((wave & 1) ? tile2 : tile) * 128, wave < (kind == 2 ? 2 : 1) ? lane : 64, ep);
  CTS(2);
  if (HL == 2) h_dma(false);
  if (kind == 2 && (W12 & 2) != 0) w_touch(w2);
  if (isconv) {
    LANE_ID(lane);
    const int tid = wave * 64 + lane;
    const unsigned sC0 = (cw & 1) ? dw & 0xffff0000u : dw << 16, sC1 = dw & 0xffff0000u;
    const int l15 = lane & 15, lg = lane >> 4, cp = (l15 + 1) & 1, iw = kind == 4 ? j4 : 80 + j3, own = 2 * wave * CV_CH;
    const int ch = tid & (4 * nw - 1), tb = nw == 2 ? tid >> 3 : tid >> 2, slot = nw == 2 ? ch : 2 * ch;
    const unsigned sWave = wave, sBuf = nw, sAuxOff = OFF_KVR + cw * 4096, sFoutOff = OFF_FY + iw * 128, sExp = __builtin_amdgcn_readfirstlane(ep), sDbg = 110u * 1048576u + (b * 4 + wave) * 8192;
    const unsigned vL16 = lane * 16, vPA = own + cp * CV_HP + 2 * (8 * lg + l15 + 1 - cp), vPB = own + 2 * CV_HP + 2 * (2032 - 16 * l15 + 8 * lg) - 512 * 7;
    const unsigned vPU = own + 2 * CV_HP + 2 * (2044 - 16 * l15 - 4 * lg) - 512 * 7, vPW = 32 * l15 + 8 * lg;
    const unsigned vOR = slot * CV_CH + tb * 16, vORA = CV_STG + slot * 1024 + tb * 16, vOG = tb * YP + (c0 + ch) * 16;
    const unsigned vFA = ((lane & 3) * 16 + (c0 >> 6)) * 128, vFB = ((4 + lane % 12) * 16 + (c0 >> 6)) * 128, vFlag = tid < 32 ? tid * 4 : 0x80000000u;
    const unsigned vTW = lane < 42 ? (iw * 168 + wave * 42 + lane) * 128 : 0x80000000u;
    const unsigned vTX = lane < 44 ? ((kind == 4 ? j4 : j3 - 12) * 176 + wave * 44 + lane) * 128 : 0x80000000u;
    (void)sDbg;
    asm volatile(
R"(.macro M d,w,x
v_mfma_f32_16x16x32_bf16 v[\d:\d+3], \w, v[\x:\x+3], v[\d:\d+3]
.endm
.macro R d,a,o
ds_read_b128 v[\d:\d+3], v\a offset:\o
.endm
.macro T d,a,o
ds_read_b64_tr_b16 \d, v\a offset:\o
.endm
.macro D a,s,o
buffer_load_dwordx4 v\a, s[36:39], s\s offen offset:\o sc1 lds
.endm
.macro L c
s_waitcnt lgkmcnt(\c)
.endm
Lcode_cv_b:
s_mov_b64 s[36:37],%0
s_mov_b32 s38,111243392
s_mov_b32 s39,0x00020000
s_mov_b32 s44,%1
s_mov_b32 s51,%2
s_mov_b32 s49,%3
s_mov_b32 s48,%4
s_mov_b32 s86,%5
s_mov_b32 s87,%6
s_mov_b32 s53,%7
v_mov_b32 v0,%8
v_mov_b32 v1,%9
v_mov_b32 v2,%10
v_mov_b32 v3,%11
v_mov_b32 v4,%12
v_mov_b32 v5,%13
v_mov_b32 v6,%14
v_mov_b32 v7,%15
v_mov_b32 v8,%16
v_mov_b32 v9,%17
v_mov_b32 v10,%18
v_mov_b32 v11,%19
v_mov_b32 v12,%20
s_mov_b32 s58,111144960
buffer_load_dword v13,v8,s[36:39],s58 offen offset:0 sc1
s_mul_i32 s57,s44,25856
v_add_u32 v18,s57,v0
v_add_u32 v18,32,v18
v_mov_b32 v19,s57
R 88,18,0
ds_read_b32 v92,v18 offset:16
R 152,18,12928
ds_read_b32 v156,v18 offset:12944
ds_read_b32 v24,v19 offset:32
ds_read_b32 v25,v19 offset:12960
s_mov_b64 s[40:41],%21
s_mov_b32 s42,7340032
s_mov_b32 s43,0x00020000
s_mov_b64 s[60:61],%22
s_mov_b32 s62,7340032
s_mov_b32 s63,0x00020000
v_mov_b32 v23,s53
s_mov_b32 s82,0
s_add_u32 s55,s49,4096
s_cmp_eq_u32 s51,2
s_cselect_b32 s55,s55,0x7fff0000
s_cselect_b32 s56,0,0x70000000
s_add_u32 s57,s57,8320
v_add_u32 v14,1024,v1
v_add_u32 v15,2048,v1
v_add_u32 v16,3072,v1
v_add_u32 v17,12928,v1
L 4
v_alignbyte_b32 v96,v89,v88,2
v_alignbyte_b32 v97,v90,v89,2
v_alignbyte_b32 v98,v91,v90,2
v_alignbyte_b32 v99,v92,v91,2
ds_write_b128 v18,v[96:99] offset:4160
L 3
v_alignbyte_b32 v160,v153,v152,2
v_alignbyte_b32 v161,v154,v153,2
v_alignbyte_b32 v162,v155,v154,2
v_alignbyte_b32 v163,v156,v155,2
ds_write_b128 v18,v[160:163] offset:17088
L 2
v_lshlrev_b32 v24,16,v24
v_lshlrev_b32 v25,16,v25
s_mov_b64 exec,1
ds_write_b32 v19,v24 offset:4188
ds_write_b32 v19,v25 offset:17116
s_mov_b64 exec,-1
ds_read2_b32 a[0:1],v1 offset0:0 offset1:1
ds_read2_b32 a[2:3],v1 offset0:2 offset1:3
ds_read2_b32 a[4:5],v1 offset0:128 offset1:129
ds_read2_b32 a[6:7],v1 offset0:130 offset1:131
ds_read2_b32 a[8:9],v1 offset0:16 offset1:17
ds_read2_b32 a[10:11],v1 offset0:18 offset1:19
ds_read2_b32 a[12:13],v1 offset0:144 offset1:145
ds_read2_b32 a[14:15],v1 offset0:146 offset1:147
ds_read2_b32 a[16:17],v1 offset0:32 offset1:33
ds_read2_b32 a[18:19],v1 offset0:34 offset1:35
ds_read2_b32 a[20:21],v1 offset0:160 offset1:161
ds_read2_b32 a[22:23],v1 offset0:162 offset1:163
ds_read2_b32 a[24:25],v1 offset0:48 offset1:49
ds_read2_b32 a[26:27],v1 offset0:50 offset1:51
ds_read2_b32 a[28:29],v1 offset0:176 offset1:177
ds_read2_b32 a[30:31],v1 offset0:178 offset1:179
ds_read2_b32 a[32:33],v1 offset0:64 offset1:65
ds_read2_b32 a[34:35],v1 offset0:66 offset1:67
ds_read2_b32 a[36:37],v1 offset0:192 offset1:193
ds_read2_b32 a[38:39],v1 offset0:194 offset1:195
ds_read2_b32 a[40:41],v1 offset0:80 offset1:81
ds_read2_b32 a[42:43],v1 offset0:82 offset1:83
ds_read2_b32 a[44:45],v1 offset0:208 offset1:209
ds_read2_b32 a[46:47],v1 offset0:210 offset1:211
ds_read2_b32 a[48:49],v1 offset0:96 offset1:97
ds_read2_b32 a[50:51],v1 offset0:98 offset1:99
ds_read2_b32 a[52:53],v1 offset0:224 offset1:225
ds_read2_b32 a[54:55],v1 offset0:226 offset1:227
ds_read2_b32 a[56:57],v1 offset0:112 offset1:113
ds_read2_b32 a[58:59],v1 offset0:114 offset1:115
ds_read2_b32 a[60:61],v1 offset0:240 offset1:241
ds_read2_b32 a[62:63],v1 offset0:242 offset1:243
ds_read2_b32 a[64:65],v17 offset0:0 offset1:1
ds_read2_b32 a[66:67],v17 offset0:2 offset1:3
ds_read2_b32 a[68:69],v17 offset0:128 offset1:129
ds_read2_b32 a[70:71],v17 offset0:130 offset1:131
ds_read2_b32 a[72:73],v17 offset0:16 offset1:17
ds_read2_b32 a[74:75],v17 offset0:18 offset1:19
ds_read2_b32 a[76:77],v17 offset0:144 offset1:145
ds_read2_b32 a[78:79],v17 offset0:146 offset1:147
ds_read2_b32 a[80:81],v17 offset0:32 offset1:33
ds_read2_b32 a[82:83],v17 offset0:34 offset1:35
ds_read2_b32 a[84:85],v17 offset0:160 offset1:161
ds_read2_b32 a[86:87],v17 offset0:162 offset1:163
ds_read2_b32 a[88:89],v17 offset0:48 offset1:49
ds_read2_b32 a[90:91],v17 offset0:50 offset1:51
ds_read2_b32 a[92:93],v17 offset0:176 offset1:177
ds_read2_b32 a[94:95],v17 offset0:178 offset1:179
ds_read2_b32 a[96:97],v17 offset0:64 offset1:65
ds_read2_b32 a[98:99],v17 offset0:66 offset1:67
ds_read2_b32 a[100:101],v17 offset0:192 offset1:193
ds_read2_b32 a[102:103],v17 offset0:194 offset1:195
ds_read2_b32 a[104:105],v17 offset0:80 offset1:81
ds_read2_b32 a[106:107],v17 offset0:82 offset1:83
ds_read2_b32 a[108:109],v17 offset0:208 offset1:209
ds_read2_b32 a[110:111],v17 offset0:210 offset1:211
ds_read2_b32 a[112:113],v17 offset0:96 offset1:97
ds_read2_b32 a[114:115],v17 offset0:98 offset1:99
ds_read2_b32 a[116:117],v17 offset0:224 offset1:225
ds_read2_b32 a[118:119],v17 offset0:226 offset1:227
ds_read2_b32 a[120:121],v17 offset0:112 offset1:113
ds_read2_b32 a[122:123],v17 offset0:114 offset1:115
ds_read2_b32 a[124:125],v17 offset0:240 offset1:241
ds_read2_b32 a[126:127],v17 offset0:242 offset1:243
Lcv_bpa_%=:
s_waitcnt vmcnt(0)
v_cmp_eq_u32 vcc,s53,v13
s_add_u32 s82,s82,1
s_cmp_eq_u64 vcc,-1
s_cbranch_scc1 Lcv_bqa_%=
s_sleep 1
buffer_load_dword v13,v8,s[36:39],s58 offen offset:0 sc1
s_cmp_lt_u32 s82,20000
s_cbranch_scc1 Lcv_bpa_%=
s_waitcnt vmcnt(0)
Lcv_bqa_%=:
s_add_u32 m0,s57,0
s_nop 0
D 0,49,3072
s_add_u32 m0,s57,12928
s_nop 0
D 0,55,3072
buffer_load_dword v13,v9,s[36:39],s58 offen offset:0 sc1
R 88,18,0
ds_read_b32 v92,v18 offset:16
R 104,18,1024
ds_read_b32 v108,v18 offset:1040
R 120,18,2048
ds_read_b32 v124,v18 offset:2064
R 136,18,3072
ds_read_b32 v140,v18 offset:3088
L 6
v_alignbyte_b32 v96,v89,v88,2
v_alignbyte_b32 v97,v90,v89,2
v_alignbyte_b32 v98,v91,v90,2
v_alignbyte_b32 v99,v92,v91,2
ds_write_b128 v18,v[96:99] offset:4160
L 5
v_alignbyte_b32 v112,v105,v104,2
v_alignbyte_b32 v113,v106,v105,2
v_alignbyte_b32 v114,v107,v106,2
v_alignbyte_b32 v115,v108,v107,2
ds_write_b128 v18,v[112:115] offset:5184
L 4
v_alignbyte_b32 v128,v121,v120,2
v_alignbyte_b32 v129,v122,v121,2
v_alignbyte_b32 v130,v123,v122,2
v_alignbyte_b32 v131,v124,v123,2
ds_write_b128 v18,v[128:131] offset:6208
L 3
v_alignbyte_b32 v144,v137,v136,2
v_alignbyte_b32 v145,v138,v137,2
v_alignbyte_b32 v146,v139,v138,2
v_alignbyte_b32 v147,v140,v139,2
ds_write_b128 v18,v[144:147] offset:7232
R 152,18,12928
ds_read_b32 v156,v18 offset:12944
R 168,18,13952
ds_read_b32 v172,v18 offset:13968
R 184,18,14976
ds_read_b32 v188,v18 offset:14992
R 200,18,16000
ds_read_b32 v204,v18 offset:16016
L 6
v_alignbyte_b32 v160,v153,v152,2
v_alignbyte_b32 v161,v154,v153,2
v_alignbyte_b32 v162,v155,v154,2
v_alignbyte_b32 v163,v156,v155,2
ds_write_b128 v18,v[160:163] offset:17088
L 5
v_alignbyte_b32 v176,v169,v168,2
v_alignbyte_b32 v177,v170,v169,2
v_alignbyte_b32 v178,v171,v170,2
v_alignbyte_b32 v179,v172,v171,2
ds_write_b128 v18,v[176:179] offset:18112
L 4
v_alignbyte_b32 v192,v185,v184,2
v_alignbyte_b32 v193,v186,v185,2
v_alignbyte_b32 v194,v187,v186,2
v_alignbyte_b32 v195,v188,v187,2
ds_write_b128 v18,v[192:195] offset:19136
L 3
v_alignbyte_b32 v208,v201,v200,2
v_alignbyte_b32 v209,v202,v201,2
v_alignbyte_b32 v210,v203,v202,2
v_alignbyte_b32 v211,v204,v203,2
ds_write_b128 v18,v[208:211] offset:20160
s_lshl_b32 s78,s44,11
s_add_u32 s78,s78,103424
v_add_u32 v18,s78,v4
v_add_u32 v4,v19,v4
s_waitcnt vmcnt(0)
v_cmp_eq_u32 vcc,s53,v13
s_mov_b32 s83,0
s_cmp_eq_u64 vcc,-1
s_cbranch_scc0 Lcv_a_%=
s_mov_b32 s83,1
s_add_u32 m0,s57,0
s_nop 0
D 0,49,2048
D 0,49,1024
D 0,49,0
s_add_u32 m0,s57,12928
s_nop 0
D 0,55,2048
D 0,55,1024
D 0,55,0
Lcv_a_%=:
s_cmp_eq_u32 s51,2
s_cbranch_scc1 Lcv_a2_%=
L 0
R 88,2,3584
R 92,2,3072
R 96,2,3648
R 100,2,3136
R 104,2,3712
R 108,2,3200
R 112,2,3776
R 116,2,3264
L 6
v_mfma_f32_16x16x32_bf16 v[40:43],a[0:3],v[88:91],0
R 120,2,3840
R 124,2,3328
v_mfma_f32_16x16x32_bf16 v[44:47],a[0:3],v[92:95],0
M 44,a[4:7],88
L 6
M 40,a[8:11],96
R 128,2,3904
R 132,2,3392
M 44,a[8:11],100
M 44,a[12:15],96
L 6
M 40,a[16:19],104
R 136,2,3968
R 140,2,3456
M 44,a[16:19],108
M 44,a[20:23],104
L 6
M 40,a[24:27],112
R 144,2,4032
R 148,2,3520
M 44,a[24:27],116
M 44,a[28:31],112
L 6
M 40,a[32:35],120
M 44,a[32:35],124
M 44,a[36:39],120
L 4
M 40,a[40:43],128
M 44,a[40:43],132
M 44,a[44:47],128
L 2
M 40,a[48:51],136
ds_read_b64 v[24:25],v3 offset:3584
ds_read_b64 v[26:27],v3 offset:3072
M 44,a[48:51],140
M 44,a[52:55],136
L 2
M 40,a[56:59],144
M 44,a[56:59],148
M 44,a[60:63],144
L 0
v_and_b32 v36,0xffff0000,v25
v_lshlrev_b32 v37,16,v25
v_and_b32 v38,0xffff0000,v24
v_lshlrev_b32 v39,16,v24
s_nop 0
v_fma_f32 v40,v36,s86,v40
v_fma_f32 v41,v37,s86,v41
v_fma_f32 v42,v38,s86,v42
v_fma_f32 v43,v39,s86,v43
v_cvt_pk_bf16_f32 v40,v40,v41
v_cvt_pk_bf16_f32 v41,v42,v43
ds_write_b64 v18,v[40:41] offset:0
v_and_b32 v36,0xffff0000,v27
v_lshlrev_b32 v37,16,v27
v_and_b32 v38,0xffff0000,v26
v_lshlrev_b32 v39,16,v26
v_fma_f32 v44,v36,s86,v44
v_fma_f32 v45,v37,s86,v45
v_fma_f32 v46,v38,s86,v46
v_fma_f32 v47,v39,s86,v47
v_cvt_pk_bf16_f32 v44,v44,v45
v_cvt_pk_bf16_f32 v45,v46,v47
ds_write_b64 v18,v[44:45] offset:512
s_branch Lcv_a3_%=
Lcv_a2_%=:
L 0
R 88,2,3584
R 92,2,3072
R 96,2,3648
R 100,2,3136
R 104,2,3712
R 108,2,3200
R 112,2,3776
R 116,2,3264
L 6
v_mfma_f32_16x16x32_bf16 v[40:43],a[0:3],v[88:91],0
R 120,2,3840
R 124,2,3328
v_mfma_f32_16x16x32_bf16 v[44:47],a[0:3],v[92:95],0
M 44,a[4:7],88
L 6
M 40,a[8:11],96
R 128,2,3904
R 132,2,3392
M 44,a[8:11],100
M 44,a[12:15],96
L 6
M 40,a[16:19],104
R 136,2,3968
R 140,2,3456
M 44,a[16:19],108
M 44,a[20:23],104
L 6
M 40,a[24:27],112
R 144,2,4032
R 148,2,3520
M 44,a[24:27],116
M 44,a[28:31],112
L 6
M 40,a[32:35],120
R 152,2,16512
R 156,2,16000
M 44,a[32:35],124
M 44,a[36:39],120
L 6
M 40,a[40:43],128
R 160,2,16576
R 164,2,16064
M 44,a[40:43],132
M 44,a[44:47],128
L 6
M 40,a[48:51],136
R 168,2,16640
R 172,2,16128
M 44,a[48:51],140
ds_read_b64 v[24:25],v3 offset:3584
ds_read_b64 v[26:27],v3 offset:3072
M 44,a[52:55],136
L 8
M 40,a[56:59],144
R 176,2,16704
R 180,2,16192
M 44,a[56:59],148
M 44,a[60:63],144
L 8
v_mfma_f32_16x16x32_bf16 v[48:51],a[64:67],v[152:155],0
R 184,2,16768
R 188,2,16256
v_mfma_f32_16x16x32_bf16 v[52:55],a[64:67],v[156:159],0
M 52,a[68:71],152
L 8
M 48,a[72:75],160
R 192,2,16832
R 196,2,16320
M 52,a[72:75],164
L 6
v_and_b32 v36,0xffff0000,v25
M 52,a[76:79],160
v_lshlrev_b32 v37,16,v25
v_and_b32 v38,0xffff0000,v24
v_lshlrev_b32 v39,16,v24
s_nop 0
M 48,a[80:83],168
R 200,2,16896
R 204,2,16384
M 52,a[80:83],172
v_fma_f32 v40,v36,s86,v40
v_fma_f32 v41,v37,s86,v41
M 52,a[84:87],168
v_fma_f32 v42,v38,s86,v42
v_fma_f32 v43,v39,s86,v43
v_cvt_pk_bf16_f32 v40,v40,v41
L 6
M 48,a[88:91],176
R 208,2,16960
R 212,2,16448
M 52,a[88:91],180
v_cvt_pk_bf16_f32 v41,v42,v43
ds_write_b64 v18,v[40:41] offset:0
M 52,a[92:95],176
v_and_b32 v36,0xffff0000,v27
v_lshlrev_b32 v37,16,v27
v_and_b32 v38,0xffff0000,v26
L 7
M 48,a[96:99],184
M 52,a[96:99],188
v_lshlrev_b32 v39,16,v26
v_fma_f32 v44,v36,s86,v44
M 52,a[100:103],184
v_fma_f32 v45,v37,s86,v45
v_fma_f32 v46,v38,s86,v46
v_fma_f32 v47,v39,s86,v47
L 5
M 48,a[104:107],192
M 52,a[104:107],196
v_cvt_pk_bf16_f32 v44,v44,v45
v_cvt_pk_bf16_f32 v45,v46,v47
M 52,a[108:111],192
ds_write_b64 v18,v[44:45] offset:512
L 4
M 48,a[112:115],200
ds_read_b64 v[24:25],v3 offset:16512
ds_read_b64 v[26:27],v3 offset:16000
M 52,a[112:115],204
M 52,a[116:119],200
L 4
M 48,a[120:123],208
M 52,a[120:123],212
M 52,a[124:127],208
L 0
v_and_b32 v36,0xffff0000,v25
v_lshlrev_b32 v37,16,v25
v_and_b32 v38,0xffff0000,v24
v_lshlrev_b32 v39,16,v24
s_nop 0
v_fma_f32 v48,v36,s87,v48
v_fma_f32 v49,v37,s87,v49
v_fma_f32 v50,v38,s87,v50
v_fma_f32 v51,v39,s87,v51
v_cvt_pk_bf16_f32 v48,v48,v49
v_cvt_pk_bf16_f32 v49,v50,v51
ds_write_b64 v18,v[48:49] offset:1024
v_and_b32 v36,0xffff0000,v27
v_lshlrev_b32 v37,16,v27
v_and_b32 v38,0xffff0000,v26
v_lshlrev_b32 v39,16,v26
v_fma_f32 v52,v36,s87,v52
v_fma_f32 v53,v37,s87,v53
v_fma_f32 v54,v38,s87,v54
v_fma_f32 v55,v39,s87,v55
v_cvt_pk_bf16_f32 v52,v52,v53
v_cvt_pk_bf16_f32 v53,v54,v55
ds_write_b64 v18,v[52:53] offset:1536
Lcv_a3_%=:
L 0
s_barrier
R 152,6,0
R 156,6,512
s_mov_b32 s78,98566144
L 1
buffer_store_dwordx4 v[152:155],v7,s[36:39],s78 offen offset:0 sc0 sc1
s_add_u32 s78,s56,99090432
L 0
buffer_store_dwordx4 v[156:159],v7,s[36:39],s78 offen offset:0 sc0 sc1
s_cmp_eq_u32 s83,1
s_cbranch_scc1 Lcv_b_%=
s_waitcnt vmcnt(0)
s_barrier
buffer_store_dword v23,v10,s[36:39],s48 offen offset:0 sc0 sc1
buffer_load_dword v13,v9,s[36:39],s58 offen offset:0 sc1
Lcv_bpb_%=:
)"
R"(s_waitcnt vmcnt(0)
v_cmp_eq_u32 vcc,s53,v13
s_add_u32 s82,s82,1
s_cmp_eq_u64 vcc,-1
s_cbranch_scc1 Lcv_bqb_%=
s_sleep 1
buffer_load_dword v13,v9,s[36:39],s58 offen offset:0 sc1
s_cmp_lt_u32 s82,20000
s_cbranch_scc1 Lcv_bpb_%=
s_waitcnt vmcnt(0)
Lcv_bqb_%=:
s_add_u32 m0,s57,0
s_nop 0
D 0,49,2048
D 0,49,1024
D 0,49,0
s_add_u32 m0,s57,12928
s_nop 0
D 0,55,2048
D 0,55,1024
D 0,55,0
s_waitcnt vmcnt(0)
Lcv_b_%=:
s_waitcnt vmcnt(2)
ds_read2_b32 v[88:89],v1 offset0:0 offset1:1
ds_read2_b32 v[90:91],v1 offset0:2 offset1:3
R 120,2,3584
ds_read2_b32 v[92:93],v1 offset0:128 offset1:129
ds_read2_b32 v[94:95],v1 offset0:130 offset1:131
R 124,2,3072
ds_read2_b32 v[96:97],v14 offset0:0 offset1:1
ds_read2_b32 v[98:99],v14 offset0:2 offset1:3
R 128,2,2560
ds_read2_b32 v[100:101],v14 offset0:128 offset1:129
ds_read2_b32 v[102:103],v14 offset0:130 offset1:131
R 132,2,2048
ds_read2_b32 v[104:105],v15 offset0:0 offset1:1
ds_read2_b32 v[106:107],v15 offset0:2 offset1:3
R 136,2,1536
ds_read2_b32 v[108:109],v15 offset0:128 offset1:129
ds_read2_b32 v[110:111],v15 offset0:130 offset1:131
R 140,2,1024
ds_read2_b32 v[112:113],v16 offset0:0 offset1:1
ds_read2_b32 v[114:115],v16 offset0:2 offset1:3
R 144,2,512
ds_read2_b32 v[116:117],v16 offset0:128 offset1:129
ds_read2_b32 v[118:119],v16 offset0:130 offset1:131
R 148,2,0
L 15
v_mfma_f32_16x16x32_bf16 v[40:43],v[88:91],v[128:131],0
ds_read2_b32 v[152:153],v1 offset0:16 offset1:17
M 40,v[92:95],124
ds_read2_b32 v[154:155],v1 offset0:18 offset1:19
M 40,v[96:99],120
L 14
R 184,2,3648
v_mfma_f32_16x16x32_bf16 v[44:47],v[88:91],v[132:135],0
ds_read2_b32 v[156:157],v1 offset0:144 offset1:145
M 44,v[92:95],128
M 44,v[96:99],124
ds_read2_b32 v[158:159],v1 offset0:146 offset1:147
M 44,v[100:103],120
L 14
R 188,2,3136
v_mfma_f32_16x16x32_bf16 v[48:51],v[88:91],v[136:139],0
ds_read2_b32 v[160:161],v14 offset0:16 offset1:17
M 48,v[92:95],132
ds_read2_b32 v[162:163],v14 offset0:18 offset1:19
M 48,v[96:99],128
M 48,v[100:103],124
R 192,2,2624
M 48,v[104:107],120
L 15
ds_read2_b32 v[164:165],v14 offset0:144 offset1:145
v_mfma_f32_16x16x32_bf16 v[52:55],v[88:91],v[140:143],0
ds_read2_b32 v[166:167],v14 offset0:146 offset1:147
M 52,v[92:95],136
R 196,2,2112
M 52,v[96:99],132
M 52,v[100:103],128
ds_read2_b32 v[168:169],v15 offset0:16 offset1:17
M 52,v[104:107],124
ds_read2_b32 v[170:171],v15 offset0:18 offset1:19
M 52,v[108:111],120
L 15
R 200,2,1600
v_mfma_f32_16x16x32_bf16 v[56:59],v[88:91],v[144:147],0
ds_read2_b32 v[172:173],v15 offset0:144 offset1:145
M 56,v[92:95],140
M 56,v[96:99],136
ds_read2_b32 v[174:175],v15 offset0:146 offset1:147
M 56,v[100:103],132
R 204,2,1088
M 56,v[104:107],128
ds_read2_b32 v[176:177],v16 offset0:16 offset1:17
M 56,v[108:111],124
ds_read2_b32 v[178:179],v16 offset0:18 offset1:19
M 56,v[112:115],120
L 15
v_mfma_f32_16x16x32_bf16 v[60:63],v[88:91],v[148:151],0
R 208,2,576
M 60,v[92:95],144
ds_read2_b32 v[180:181],v16 offset0:144 offset1:145
M 60,v[96:99],140
ds_read2_b32 v[182:183],v16 offset0:146 offset1:147
M 60,v[100:103],136
R 212,2,64
M 60,v[104:107],132
M 60,v[108:111],128
M 60,v[112:115],124
M 60,v[116:119],120
L 15
M 40,v[152:155],192
ds_read2_b32 v[88:89],v1 offset0:32 offset1:33
M 40,v[156:159],188
ds_read2_b32 v[90:91],v1 offset0:34 offset1:35
M 40,v[160:163],184
L 14
R 120,2,3712
M 44,v[152:155],196
ds_read2_b32 v[92:93],v1 offset0:160 offset1:161
M 44,v[156:159],192
M 44,v[160:163],188
ds_read2_b32 v[94:95],v1 offset0:162 offset1:163
M 44,v[164:167],184
L 14
R 124,2,3200
M 48,v[152:155],200
ds_read2_b32 v[96:97],v14 offset0:32 offset1:33
M 48,v[156:159],196
ds_read2_b32 v[98:99],v14 offset0:34 offset1:35
M 48,v[160:163],192
M 48,v[164:167],188
R 128,2,2688
M 48,v[168:171],184
L 15
ds_read2_b32 v[100:101],v14 offset0:160 offset1:161
M 52,v[152:155],204
ds_read2_b32 v[102:103],v14 offset0:162 offset1:163
M 52,v[156:159],200
R 132,2,2176
M 52,v[160:163],196
M 52,v[164:167],192
ds_read2_b32 v[104:105],v15 offset0:32 offset1:33
M 52,v[168:171],188
ds_read2_b32 v[106:107],v15 offset0:34 offset1:35
M 52,v[172:175],184
L 15
R 136,2,1664
M 56,v[152:155],208
ds_read2_b32 v[108:109],v15 offset0:160 offset1:161
M 56,v[156:159],204
M 56,v[160:163],200
ds_read2_b32 v[110:111],v15 offset0:162 offset1:163
M 56,v[164:167],196
R 140,2,1152
M 56,v[168:171],192
ds_read2_b32 v[112:113],v16 offset0:32 offset1:33
M 56,v[172:175],188
ds_read2_b32 v[114:115],v16 offset0:34 offset1:35
M 56,v[176:179],184
L 15
M 60,v[152:155],212
R 144,2,640
M 60,v[156:159],208
ds_read2_b32 v[116:117],v16 offset0:160 offset1:161
s_cmp_eq_u32 s83,0
s_cbranch_scc1 Lcv_c_%=
s_waitcnt vmcnt(0)
s_barrier
buffer_store_dword v23,v10,s[36:39],s48 offen offset:0 sc0 sc1
Lcv_c_%=:
M 60,v[160:163],204
ds_read2_b32 v[118:119],v16 offset0:162 offset1:163
M 60,v[164:167],200
R 148,2,128
M 60,v[168:171],196
M 60,v[172:175],192
M 60,v[176:179],188
M 60,v[180:183],184
L 15
M 40,v[88:91],128
ds_read2_b32 v[152:153],v1 offset0:48 offset1:49
M 40,v[92:95],124
ds_read2_b32 v[154:155],v1 offset0:50 offset1:51
M 40,v[96:99],120
L 14
R 184,2,3776
M 44,v[88:91],132
ds_read2_b32 v[156:157],v1 offset0:176 offset1:177
M 44,v[92:95],128
M 44,v[96:99],124
ds_read2_b32 v[158:159],v1 offset0:178 offset1:179
M 44,v[100:103],120
L 14
R 188,2,3264
M 48,v[88:91],136
ds_read2_b32 v[160:161],v14 offset0:48 offset1:49
M 48,v[92:95],132
ds_read2_b32 v[162:163],v14 offset0:50 offset1:51
M 48,v[96:99],128
M 48,v[100:103],124
R 192,2,2752
M 48,v[104:107],120
L 15
ds_read2_b32 v[164:165],v14 offset0:176 offset1:177
M 52,v[88:91],140
s_mov_b32 m0,163584
s_mov_b32 s78,0
ds_read2_b32 v[166:167],v14 offset0:178 offset1:179
M 52,v[92:95],136
R 196,2,2240
M 52,v[96:99],132
buffer_load_dword v11,s[40:43],s78 offen offset:0  lds
M 52,v[100:103],128
ds_read2_b32 v[168:169],v15 offset0:48 offset1:49
M 52,v[104:107],124
ds_read2_b32 v[170:171],v15 offset0:50 offset1:51
M 52,v[108:111],120
L 15
R 200,2,1728
M 56,v[88:91],144
ds_read2_b32 v[172:173],v15 offset0:176 offset1:177
M 56,v[92:95],140
M 56,v[96:99],136
ds_read2_b32 v[174:175],v15 offset0:178 offset1:179
M 56,v[100:103],132
R 204,2,1216
M 56,v[104:107],128
ds_read2_b32 v[176:177],v16 offset0:48 offset1:49
M 56,v[108:111],124
ds_read2_b32 v[178:179],v16 offset0:50 offset1:51
M 56,v[112:115],120
L 15
M 60,v[88:91],148
R 208,2,704
M 60,v[92:95],144
ds_read2_b32 v[180:181],v16 offset0:176 offset1:177
M 60,v[96:99],140
ds_read2_b32 v[182:183],v16 offset0:178 offset1:179
M 60,v[100:103],136
R 212,2,192
M 60,v[104:107],132
M 60,v[108:111],128
M 60,v[112:115],124
M 60,v[116:119],120
L 15
M 40,v[152:155],192
ds_read2_b32 v[88:89],v1 offset0:64 offset1:65
M 40,v[156:159],188
ds_read2_b32 v[90:91],v1 offset0:66 offset1:67
M 40,v[160:163],184
L 14
R 120,2,3840
M 44,v[152:155],196
ds_read2_b32 v[92:93],v1 offset0:192 offset1:193
M 44,v[156:159],192
M 44,v[160:163],188
ds_read2_b32 v[94:95],v1 offset0:194 offset1:195
M 44,v[164:167],184
L 14
R 124,2,3328
M 48,v[152:155],200
ds_read2_b32 v[96:97],v14 offset0:64 offset1:65
M 48,v[156:159],196
ds_read2_b32 v[98:99],v14 offset0:66 offset1:67
M 48,v[160:163],192
M 48,v[164:167],188
R 128,2,2816
M 48,v[168:171],184
L 15
ds_read2_b32 v[100:101],v14 offset0:192 offset1:193
M 52,v[152:155],204
ds_read2_b32 v[102:103],v14 offset0:194 offset1:195
M 52,v[156:159],200
R 132,2,2304
M 52,v[160:163],196
M 52,v[164:167],192
ds_read2_b32 v[104:105],v15 offset0:64 offset1:65
M 52,v[168:171],188
ds_read2_b32 v[106:107],v15 offset0:66 offset1:67
M 52,v[172:175],184
L 15
R 136,2,1792
M 56,v[152:155],208
ds_read2_b32 v[108:109],v15 offset0:192 offset1:193
M 56,v[156:159],204
M 56,v[160:163],200
ds_read2_b32 v[110:111],v15 offset0:194 offset1:195
M 56,v[164:167],196
R 140,2,1280
M 56,v[168:171],192
ds_read2_b32 v[112:113],v16 offset0:64 offset1:65
M 56,v[172:175],188
ds_read2_b32 v[114:115],v16 offset0:66 offset1:67
M 56,v[176:179],184
L 15
M 60,v[152:155],212
R 144,2,768
M 60,v[156:159],208
ds_read2_b32 v[116:117],v16 offset0:192 offset1:193
M 60,v[160:163],204
ds_read2_b32 v[118:119],v16 offset0:194 offset1:195
M 60,v[164:167],200
R 148,2,256
M 60,v[168:171],196
M 60,v[172:175],192
M 60,v[176:179],188
M 60,v[180:183],184
L 15
M 40,v[88:91],128
ds_read2_b32 v[152:153],v1 offset0:80 offset1:81
M 40,v[92:95],124
ds_read2_b32 v[154:155],v1 offset0:82 offset1:83
M 40,v[96:99],120
L 14
R 184,2,3904
M 44,v[88:91],132
ds_read2_b32 v[156:157],v1 offset0:208 offset1:209
M 44,v[92:95],128
M 44,v[96:99],124
ds_read2_b32 v[158:159],v1 offset0:210 offset1:211
M 44,v[100:103],120
L 14
R 188,2,3392
M 48,v[88:91],136
ds_read2_b32 v[160:161],v14 offset0:80 offset1:81
M 48,v[92:95],132
ds_read2_b32 v[162:163],v14 offset0:82 offset1:83
M 48,v[96:99],128
M 48,v[100:103],124
R 192,2,2880
M 48,v[104:107],120
L 15
ds_read2_b32 v[164:165],v14 offset0:208 offset1:209
M 52,v[88:91],140
ds_read2_b32 v[166:167],v14 offset0:210 offset1:211
M 52,v[92:95],136
R 196,2,2368
M 52,v[96:99],132
M 52,v[100:103],128
ds_read2_b32 v[168:169],v15 offset0:80 offset1:81
M 52,v[104:107],124
ds_read2_b32 v[170:171],v15 offset0:82 offset1:83
M 52,v[108:111],120
L 15
R 200,2,1856
M 56,v[88:91],144
ds_read2_b32 v[172:173],v15 offset0:208 offset1:209
M 56,v[92:95],140
M 56,v[96:99],136
ds_read2_b32 v[174:175],v15 offset0:210 offset1:211
M 56,v[100:103],132
R 204,2,1344
M 56,v[104:107],128
ds_read2_b32 v[176:177],v16 offset0:80 offset1:81
M 56,v[108:111],124
ds_read2_b32 v[178:179],v16 offset0:82 offset1:83
M 56,v[112:115],120
L 15
M 60,v[88:91],148
R 208,2,832
M 60,v[92:95],144
ds_read2_b32 v[180:181],v16 offset0:208 offset1:209
M 60,v[96:99],140
ds_read2_b32 v[182:183],v16 offset0:210 offset1:211
M 60,v[100:103],136
R 212,2,320
M 60,v[104:107],132
M 60,v[108:111],128
M 60,v[112:115],124
M 60,v[116:119],120
L 15
M 40,v[152:155],192
ds_read2_b32 v[88:89],v1 offset0:96 offset1:97
M 40,v[156:159],188
ds_read2_b32 v[90:91],v1 offset0:98 offset1:99
M 40,v[160:163],184
L 14
R 120,2,3968
M 44,v[152:155],196
s_mov_b32 m0,163584
s_mov_b32 s78,3741696
ds_read2_b32 v[92:93],v1 offset0:224 offset1:225
M 44,v[156:159],192
M 44,v[160:163],188
ds_read2_b32 v[94:95],v1 offset0:226 offset1:227
M 44,v[164:167],184
L 14
R 124,2,3456
M 48,v[152:155],200
ds_read2_b32 v[96:97],v14 offset0:96 offset1:97
M 48,v[156:159],196
ds_read2_b32 v[98:99],v14 offset0:98 offset1:99
M 48,v[160:163],192
buffer_load_dword v11,s[40:43],s78 offen offset:0  lds
M 48,v[164:167],188
R 128,2,2944
M 48,v[168:171],184
L 15
ds_read2_b32 v[100:101],v14 offset0:224 offset1:225
M 52,v[152:155],204
ds_read2_b32 v[102:103],v14 offset0:226 offset1:227
M 52,v[156:159],200
R 132,2,2432
M 52,v[160:163],196
M 52,v[164:167],192
ds_read2_b32 v[104:105],v15 offset0:96 offset1:97
M 52,v[168:171],188
ds_read2_b32 v[106:107],v15 offset0:98 offset1:99
M 52,v[172:175],184
L 15
R 136,2,1920
M 56,v[152:155],208
ds_read2_b32 v[108:109],v15 offset0:224 offset1:225
M 56,v[156:159],204
M 56,v[160:163],200
ds_read2_b32 v[110:111],v15 offset0:226 offset1:227
M 56,v[164:167],196
R 140,2,1408
M 56,v[168:171],192
ds_read2_b32 v[112:113],v16 offset0:96 offset1:97
M 56,v[172:175],188
ds_read2_b32 v[114:115],v16 offset0:98 offset1:99
M 56,v[176:179],184
L 15
M 60,v[152:155],212
R 144,2,896
M 60,v[156:159],208
ds_read2_b32 v[116:117],v16 offset0:224 offset1:225
M 60,v[160:163],204
ds_read2_b32 v[118:119],v16 offset0:226 offset1:227
M 60,v[164:167],200
R 148,2,384
M 60,v[168:171],196
M 60,v[172:175],192
M 60,v[176:179],188
M 60,v[180:183],184
L 15
M 40,v[88:91],128
ds_read2_b32 v[152:153],v1 offset0:112 offset1:113
M 40,v[92:95],124
ds_read2_b32 v[154:155],v1 offset0:114 offset1:115
M 40,v[96:99],120
L 14
R 184,2,4032
M 44,v[88:91],132
ds_read2_b32 v[156:157],v1 offset0:240 offset1:241
M 44,v[92:95],128
M 44,v[96:99],124
ds_read2_b32 v[158:159],v1 offset0:242 offset1:243
v_add_u32 v1,12928,v1
M 44,v[100:103],120
L 14
R 188,2,3520
M 48,v[88:91],136
ds_read2_b32 v[160:161],v14 offset0:112 offset1:113
M 48,v[92:95],132
ds_read2_b32 v[162:163],v14 offset0:114 offset1:115
M 48,v[96:99],128
M 48,v[100:103],124
R 192,2,3008
M 48,v[104:107],120
L 15
ds_read2_b32 v[164:165],v14 offset0:240 offset1:241
M 52,v[88:91],140
ds_read2_b32 v[166:167],v14 offset0:242 offset1:243
v_add_u32 v14,12928,v14
M 52,v[92:95],136
R 196,2,2496
M 52,v[96:99],132
M 52,v[100:103],128
ds_read2_b32 v[168:169],v15 offset0:112 offset1:113
M 52,v[104:107],124
ds_read2_b32 v[170:171],v15 offset0:114 offset1:115
M 52,v[108:111],120
L 15
R 200,2,1984
M 56,v[88:91],144
ds_read2_b32 v[172:173],v15 offset0:240 offset1:241
M 56,v[92:95],140
M 56,v[96:99],136
ds_read2_b32 v[174:175],v15 offset0:242 offset1:243
v_add_u32 v15,12928,v15
M 56,v[100:103],132
R 204,2,1472
M 56,v[104:107],128
ds_read2_b32 v[176:177],v16 offset0:112 offset1:113
M 56,v[108:111],124
ds_read2_b32 v[178:179],v16 offset0:114 offset1:115
M 56,v[112:115],120
L 15
M 60,v[88:91],148
R 208,2,960
M 60,v[92:95],144
ds_read2_b32 v[180:181],v16 offset0:240 offset1:241
M 60,v[96:99],140
ds_read2_b32 v[182:183],v16 offset0:242 offset1:243
v_add_u32 v16,12928,v16
M 60,v[100:103],136
)"
R"(R 212,2,448
M 60,v[104:107],132
M 60,v[108:111],128
M 60,v[112:115],124
M 60,v[116:119],120
L 15
M 40,v[152:155],192
ds_read2_b32 v[88:89],v1 offset0:0 offset1:1
M 40,v[156:159],188
ds_read2_b32 v[90:91],v1 offset0:2 offset1:3
M 40,v[160:163],184
L 14
R 120,2,16512
M 44,v[152:155],196
ds_read2_b32 v[92:93],v1 offset0:128 offset1:129
M 44,v[156:159],192
M 44,v[160:163],188
ds_read2_b32 v[94:95],v1 offset0:130 offset1:131
M 44,v[164:167],184
L 14
R 124,2,16000
M 48,v[152:155],200
ds_read2_b32 v[96:97],v14 offset0:0 offset1:1
M 48,v[156:159],196
ds_read2_b32 v[98:99],v14 offset0:2 offset1:3
M 48,v[160:163],192
M 48,v[164:167],188
R 128,2,15488
M 48,v[168:171],184
L 15
ds_read2_b32 v[100:101],v14 offset0:128 offset1:129
M 52,v[152:155],204
ds_read2_b32 v[102:103],v14 offset0:130 offset1:131
M 52,v[156:159],200
R 132,2,14976
M 52,v[160:163],196
M 52,v[164:167],192
ds_read2_b32 v[104:105],v15 offset0:0 offset1:1
M 52,v[168:171],188
ds_read2_b32 v[106:107],v15 offset0:2 offset1:3
M 52,v[172:175],184
L 15
R 136,2,14464
M 56,v[152:155],208
ds_read2_b32 v[108:109],v15 offset0:128 offset1:129
M 56,v[156:159],204
M 56,v[160:163],200
ds_read2_b32 v[110:111],v15 offset0:130 offset1:131
M 56,v[164:167],196
R 140,2,13952
M 56,v[168:171],192
ds_read2_b32 v[112:113],v16 offset0:0 offset1:1
M 56,v[172:175],188
ds_read2_b32 v[114:115],v16 offset0:2 offset1:3
M 56,v[176:179],184
L 15
M 60,v[152:155],212
R 144,2,13440
M 60,v[156:159],208
ds_read2_b32 v[116:117],v16 offset0:128 offset1:129
M 60,v[160:163],204
ds_read2_b32 v[118:119],v16 offset0:130 offset1:131
M 60,v[164:167],200
R 148,2,12928
M 60,v[168:171],196
M 60,v[172:175],192
M 60,v[176:179],188
M 60,v[180:183],184
s_cmp_eq_u32 s51,2
s_cbranch_scc1 Lcv_d_%=
ds_read_b64 v[24:25],v3 offset:2560
ds_read_b64 v[26:27],v3 offset:2048
ds_read_b64 v[28:29],v3 offset:1536
ds_read_b64 v[30:31],v3 offset:1024
ds_read_b64 v[32:33],v3 offset:512
ds_read_b64 v[34:35],v3 offset:0
L 0
v_and_b32 v36,0xffff0000,v25
v_lshlrev_b32 v37,16,v25
v_and_b32 v38,0xffff0000,v24
v_lshlrev_b32 v39,16,v24
v_fma_f32 v40,v36,s86,v40
v_fma_f32 v41,v37,s86,v41
v_fma_f32 v42,v38,s86,v42
v_fma_f32 v43,v39,s86,v43
v_cvt_pk_bf16_f32 v40,v40,v41
v_cvt_pk_bf16_f32 v41,v42,v43
ds_write_b64 v4,v[40:41] offset:0
v_and_b32 v36,0xffff0000,v27
v_lshlrev_b32 v37,16,v27
v_and_b32 v38,0xffff0000,v26
v_lshlrev_b32 v39,16,v26
v_fma_f32 v44,v36,s86,v44
v_fma_f32 v45,v37,s86,v45
v_fma_f32 v46,v38,s86,v46
v_fma_f32 v47,v39,s86,v47
v_cvt_pk_bf16_f32 v44,v44,v45
v_cvt_pk_bf16_f32 v45,v46,v47
ds_write_b64 v4,v[44:45] offset:512
v_and_b32 v36,0xffff0000,v29
v_lshlrev_b32 v37,16,v29
v_and_b32 v38,0xffff0000,v28
v_lshlrev_b32 v39,16,v28
v_fma_f32 v48,v36,s86,v48
v_fma_f32 v49,v37,s86,v49
v_fma_f32 v50,v38,s86,v50
v_fma_f32 v51,v39,s86,v51
v_cvt_pk_bf16_f32 v48,v48,v49
v_cvt_pk_bf16_f32 v49,v50,v51
ds_write_b64 v4,v[48:49] offset:1024
v_and_b32 v36,0xffff0000,v31
v_lshlrev_b32 v37,16,v31
v_and_b32 v38,0xffff0000,v30
v_lshlrev_b32 v39,16,v30
v_fma_f32 v52,v36,s86,v52
v_fma_f32 v53,v37,s86,v53
v_fma_f32 v54,v38,s86,v54
v_fma_f32 v55,v39,s86,v55
v_cvt_pk_bf16_f32 v52,v52,v53
v_cvt_pk_bf16_f32 v53,v54,v55
ds_write_b64 v4,v[52:53] offset:1536
v_and_b32 v36,0xffff0000,v33
v_lshlrev_b32 v37,16,v33
v_and_b32 v38,0xffff0000,v32
v_lshlrev_b32 v39,16,v32
v_fma_f32 v56,v36,s86,v56
v_fma_f32 v57,v37,s86,v57
v_fma_f32 v58,v38,s86,v58
v_fma_f32 v59,v39,s86,v59
v_cvt_pk_bf16_f32 v56,v56,v57
v_cvt_pk_bf16_f32 v57,v58,v59
ds_write_b64 v4,v[56:57] offset:2048
v_and_b32 v36,0xffff0000,v35
v_lshlrev_b32 v37,16,v35
v_and_b32 v38,0xffff0000,v34
v_lshlrev_b32 v39,16,v34
v_fma_f32 v60,v36,s86,v60
v_fma_f32 v61,v37,s86,v61
v_fma_f32 v62,v38,s86,v62
v_fma_f32 v63,v39,s86,v63
v_cvt_pk_bf16_f32 v60,v60,v61
v_cvt_pk_bf16_f32 v61,v62,v63
ds_write_b64 v4,v[60:61] offset:2560
s_branch Lcv_e_%=
Lcv_d_%=:
L 15
v_mfma_f32_16x16x32_bf16 v[64:67],v[88:91],v[128:131],0
ds_read2_b32 v[152:153],v1 offset0:16 offset1:17
ds_read_b64 v[24:25],v3 offset:2560
M 64,v[92:95],124
ds_read2_b32 v[154:155],v1 offset0:18 offset1:19
ds_read_b64 v[26:27],v3 offset:2048
M 64,v[96:99],120
L 15
R 184,2,16576
ds_read_b64 v[28:29],v3 offset:1536
v_mfma_f32_16x16x32_bf16 v[68:71],v[88:91],v[132:135],0
ds_read2_b32 v[156:157],v1 offset0:144 offset1:145
ds_read_b64 v[30:31],v3 offset:1024
M 68,v[92:95],128
ds_read_b64 v[32:33],v3 offset:512
ds_read_b64 v[34:35],v3 offset:0
M 68,v[96:99],124
ds_read2_b32 v[158:159],v1 offset0:146 offset1:147
M 68,v[100:103],120
L 15
R 188,2,16064
v_mfma_f32_16x16x32_bf16 v[72:75],v[88:91],v[136:139],0
ds_read2_b32 v[160:161],v14 offset0:16 offset1:17
M 72,v[92:95],132
s_mov_b32 m0,163584
s_mov_b32 s78,0
ds_read2_b32 v[162:163],v14 offset0:18 offset1:19
M 72,v[96:99],128
M 72,v[100:103],124
L 4
R 192,2,15552
v_and_b32 v36,0xffff0000,v25
M 72,v[104:107],120
s_nop 0
ds_read2_b32 v[164:165],v14 offset0:144 offset1:145
v_lshlrev_b32 v37,16,v25
v_mfma_f32_16x16x32_bf16 v[76:79],v[88:91],v[140:143],0
ds_read2_b32 v[166:167],v14 offset0:146 offset1:147
v_and_b32 v38,0xffff0000,v24
M 76,v[92:95],136
R 196,2,15040
v_lshlrev_b32 v39,16,v24
M 76,v[96:99],132
buffer_load_dword v12,s[60:63],s78 offen offset:0  lds
M 76,v[100:103],128
ds_read2_b32 v[168:169],v15 offset0:16 offset1:17
v_fma_f32 v40,v36,s86,v40
M 76,v[104:107],124
ds_read2_b32 v[170:171],v15 offset0:18 offset1:19
v_fma_f32 v41,v37,s86,v41
M 76,v[108:111],120
s_nop 0
R 200,2,14528
v_fma_f32 v42,v38,s86,v42
v_mfma_f32_16x16x32_bf16 v[80:83],v[88:91],v[144:147],0
ds_read2_b32 v[172:173],v15 offset0:144 offset1:145
v_fma_f32 v43,v39,s86,v43
M 80,v[92:95],140
v_cvt_pk_bf16_f32 v40,v40,v41
v_cvt_pk_bf16_f32 v41,v42,v43
M 80,v[96:99],136
ds_read2_b32 v[174:175],v15 offset0:146 offset1:147
v_and_b32 v36,0xffff0000,v27
M 80,v[100:103],132
R 204,2,14016
v_lshlrev_b32 v37,16,v27
M 80,v[104:107],128
ds_read2_b32 v[176:177],v16 offset0:16 offset1:17
v_and_b32 v38,0xffff0000,v26
M 80,v[108:111],124
ds_read2_b32 v[178:179],v16 offset0:18 offset1:19
v_lshlrev_b32 v39,16,v26
M 80,v[112:115],120
s_nop 0
ds_write_b64 v4,v[40:41] offset:0
v_mfma_f32_16x16x32_bf16 v[84:87],v[88:91],v[148:151],0
R 208,2,13504
v_fma_f32 v44,v36,s86,v44
M 84,v[92:95],144
ds_read2_b32 v[180:181],v16 offset0:144 offset1:145
v_fma_f32 v45,v37,s86,v45
M 84,v[96:99],140
ds_read2_b32 v[182:183],v16 offset0:146 offset1:147
v_fma_f32 v46,v38,s86,v46
M 84,v[100:103],136
R 212,2,12992
v_fma_f32 v47,v39,s86,v47
M 84,v[104:107],132
v_cvt_pk_bf16_f32 v44,v44,v45
v_cvt_pk_bf16_f32 v45,v46,v47
M 84,v[108:111],128
ds_write_b64 v4,v[44:45] offset:512
M 84,v[112:115],124
v_and_b32 v36,0xffff0000,v29
v_lshlrev_b32 v37,16,v29
M 84,v[116:119],120
L 15
v_and_b32 v38,0xffff0000,v28
v_lshlrev_b32 v39,16,v28
M 64,v[152:155],192
ds_read2_b32 v[88:89],v1 offset0:32 offset1:33
v_fma_f32 v48,v36,s86,v48
M 64,v[156:159],188
ds_read2_b32 v[90:91],v1 offset0:34 offset1:35
v_fma_f32 v49,v37,s86,v49
M 64,v[160:163],184
L 15
R 120,2,16640
v_fma_f32 v50,v38,s86,v50
M 68,v[152:155],196
ds_read2_b32 v[92:93],v1 offset0:160 offset1:161
v_fma_f32 v51,v39,s86,v51
M 68,v[156:159],192
v_cvt_pk_bf16_f32 v48,v48,v49
v_cvt_pk_bf16_f32 v49,v50,v51
M 68,v[160:163],188
ds_read2_b32 v[94:95],v1 offset0:162 offset1:163
v_and_b32 v36,0xffff0000,v31
M 68,v[164:167],184
L 15
R 124,2,16128
v_lshlrev_b32 v37,16,v31
M 72,v[152:155],200
ds_read2_b32 v[96:97],v14 offset0:32 offset1:33
v_and_b32 v38,0xffff0000,v30
M 72,v[156:159],196
ds_read2_b32 v[98:99],v14 offset0:34 offset1:35
v_lshlrev_b32 v39,16,v30
M 72,v[160:163],192
ds_write_b64 v4,v[48:49] offset:1024
M 72,v[164:167],188
R 128,2,15616
v_fma_f32 v52,v36,s86,v52
M 72,v[168:171],184
L 15
ds_read2_b32 v[100:101],v14 offset0:160 offset1:161
v_fma_f32 v53,v37,s86,v53
M 76,v[152:155],204
ds_read2_b32 v[102:103],v14 offset0:162 offset1:163
v_fma_f32 v54,v38,s86,v54
M 76,v[156:159],200
R 132,2,15104
v_fma_f32 v55,v39,s86,v55
M 76,v[160:163],196
v_cvt_pk_bf16_f32 v52,v52,v53
v_cvt_pk_bf16_f32 v53,v54,v55
M 76,v[164:167],192
ds_read2_b32 v[104:105],v15 offset0:32 offset1:33
v_and_b32 v36,0xffff0000,v33
M 76,v[168:171],188
ds_read2_b32 v[106:107],v15 offset0:34 offset1:35
v_lshlrev_b32 v37,16,v33
M 76,v[172:175],184
L 15
R 136,2,14592
v_and_b32 v38,0xffff0000,v32
M 80,v[152:155],208
ds_read2_b32 v[108:109],v15 offset0:160 offset1:161
v_lshlrev_b32 v39,16,v32
M 80,v[156:159],204
ds_write_b64 v4,v[52:53] offset:1536
M 80,v[160:163],200
ds_read2_b32 v[110:111],v15 offset0:162 offset1:163
v_fma_f32 v56,v36,s86,v56
M 80,v[164:167],196
R 140,2,14080
v_fma_f32 v57,v37,s86,v57
M 80,v[168:171],192
ds_read2_b32 v[112:113],v16 offset0:32 offset1:33
v_fma_f32 v58,v38,s86,v58
M 80,v[172:175],188
ds_read2_b32 v[114:115],v16 offset0:34 offset1:35
v_fma_f32 v59,v39,s86,v59
M 80,v[176:179],184
L 15
v_cvt_pk_bf16_f32 v56,v56,v57
v_cvt_pk_bf16_f32 v57,v58,v59
M 84,v[152:155],212
R 144,2,13568
v_and_b32 v36,0xffff0000,v35
M 84,v[156:159],208
ds_read2_b32 v[116:117],v16 offset0:160 offset1:161
v_lshlrev_b32 v37,16,v35
M 84,v[160:163],204
ds_read2_b32 v[118:119],v16 offset0:162 offset1:163
v_and_b32 v38,0xffff0000,v34
M 84,v[164:167],200
R 148,2,13056
v_lshlrev_b32 v39,16,v34
M 84,v[168:171],196
ds_write_b64 v4,v[56:57] offset:2048
M 84,v[172:175],192
v_fma_f32 v60,v36,s86,v60
v_fma_f32 v61,v37,s86,v61
M 84,v[176:179],188
v_fma_f32 v62,v38,s86,v62
v_fma_f32 v63,v39,s86,v63
M 84,v[180:183],184
L 15
v_cvt_pk_bf16_f32 v60,v60,v61
v_cvt_pk_bf16_f32 v61,v62,v63
M 64,v[88:91],128
ds_read2_b32 v[152:153],v1 offset0:48 offset1:49
M 64,v[92:95],124
ds_read2_b32 v[154:155],v1 offset0:50 offset1:51
M 64,v[96:99],120
L 15
R 184,2,16704
M 68,v[88:91],132
ds_read2_b32 v[156:157],v1 offset0:176 offset1:177
M 68,v[92:95],128
ds_write_b64 v4,v[60:61] offset:2560
M 68,v[96:99],124
ds_read2_b32 v[158:159],v1 offset0:178 offset1:179
M 68,v[100:103],120
L 15
R 188,2,16192
M 72,v[88:91],136
ds_read2_b32 v[160:161],v14 offset0:48 offset1:49
M 72,v[92:95],132
ds_read2_b32 v[162:163],v14 offset0:50 offset1:51
M 72,v[96:99],128
M 72,v[100:103],124
R 192,2,15680
M 72,v[104:107],120
L 15
ds_read2_b32 v[164:165],v14 offset0:176 offset1:177
M 76,v[88:91],140
ds_read2_b32 v[166:167],v14 offset0:178 offset1:179
M 76,v[92:95],136
R 196,2,15168
M 76,v[96:99],132
M 76,v[100:103],128
ds_read2_b32 v[168:169],v15 offset0:48 offset1:49
M 76,v[104:107],124
ds_read2_b32 v[170:171],v15 offset0:50 offset1:51
M 76,v[108:111],120
L 15
R 200,2,14656
M 80,v[88:91],144
ds_read2_b32 v[172:173],v15 offset0:176 offset1:177
M 80,v[92:95],140
M 80,v[96:99],136
ds_read2_b32 v[174:175],v15 offset0:178 offset1:179
M 80,v[100:103],132
R 204,2,14144
M 80,v[104:107],128
s_mov_b32 m0,163584
s_mov_b32 s78,1847296
ds_read2_b32 v[176:177],v16 offset0:48 offset1:49
M 80,v[108:111],124
ds_read2_b32 v[178:179],v16 offset0:50 offset1:51
M 80,v[112:115],120
L 15
buffer_load_dword v12,s[60:63],s78 offen offset:0  lds
M 84,v[88:91],148
R 208,2,13632
M 84,v[92:95],144
ds_read2_b32 v[180:181],v16 offset0:176 offset1:177
M 84,v[96:99],140
ds_read2_b32 v[182:183],v16 offset0:178 offset1:179
M 84,v[100:103],136
R 212,2,13120
M 84,v[104:107],132
M 84,v[108:111],128
M 84,v[112:115],124
M 84,v[116:119],120
L 15
M 64,v[152:155],192
ds_read2_b32 v[88:89],v1 offset0:64 offset1:65
M 64,v[156:159],188
ds_read2_b32 v[90:91],v1 offset0:66 offset1:67
M 64,v[160:163],184
L 14
R 120,2,16768
M 68,v[152:155],196
ds_read2_b32 v[92:93],v1 offset0:192 offset1:193
M 68,v[156:159],192
M 68,v[160:163],188
ds_read2_b32 v[94:95],v1 offset0:194 offset1:195
M 68,v[164:167],184
L 14
R 124,2,16256
M 72,v[152:155],200
ds_read2_b32 v[96:97],v14 offset0:64 offset1:65
M 72,v[156:159],196
ds_read2_b32 v[98:99],v14 offset0:66 offset1:67
M 72,v[160:163],192
M 72,v[164:167],188
R 128,2,15744
M 72,v[168:171],184
L 15
ds_read2_b32 v[100:101],v14 offset0:192 offset1:193
M 76,v[152:155],204
ds_read2_b32 v[102:103],v14 offset0:194 offset1:195
M 76,v[156:159],200
R 132,2,15232
M 76,v[160:163],196
M 76,v[164:167],192
ds_read2_b32 v[104:105],v15 offset0:64 offset1:65
M 76,v[168:171],188
ds_read2_b32 v[106:107],v15 offset0:66 offset1:67
M 76,v[172:175],184
L 15
R 136,2,14720
M 80,v[152:155],208
ds_read2_b32 v[108:109],v15 offset0:192 offset1:193
M 80,v[156:159],204
M 80,v[160:163],200
ds_read2_b32 v[110:111],v15 offset0:194 offset1:195
M 80,v[164:167],196
R 140,2,14208
M 80,v[168:171],192
ds_read2_b32 v[112:113],v16 offset0:64 offset1:65
M 80,v[172:175],188
ds_read2_b32 v[114:115],v16 offset0:66 offset1:67
M 80,v[176:179],184
L 15
M 84,v[152:155],212
R 144,2,13696
M 84,v[156:159],208
ds_read2_b32 v[116:117],v16 offset0:192 offset1:193
M 84,v[160:163],204
ds_read2_b32 v[118:119],v16 offset0:194 offset1:195
M 84,v[164:167],200
R 148,2,13184
M 84,v[168:171],196
M 84,v[172:175],192
M 84,v[176:179],188
M 84,v[180:183],184
L 15
M 64,v[88:91],128
ds_read2_b32 v[152:153],v1 offset0:80 offset1:81
M 64,v[92:95],124
ds_read2_b32 v[154:155],v1 offset0:82 offset1:83
M 64,v[96:99],120
L 14
R 184,2,16832
M 68,v[88:91],132
ds_read2_b32 v[156:157],v1 offset0:208 offset1:209
M 68,v[92:95],128
M 68,v[96:99],124
ds_read2_b32 v[158:159],v1 offset0:210 offset1:211
M 68,v[100:103],120
L 14
R 188,2,16320
M 72,v[88:91],136
ds_read2_b32 v[160:161],v14 offset0:80 offset1:81
M 72,v[92:95],132
ds_read2_b32 v[162:163],v14 offset0:82 offset1:83
M 72,v[96:99],128
M 72,v[100:103],124
R 192,2,15808
M 72,v[104:107],120
)"
R"(L 15
ds_read2_b32 v[164:165],v14 offset0:208 offset1:209
M 76,v[88:91],140
ds_read2_b32 v[166:167],v14 offset0:210 offset1:211
M 76,v[92:95],136
R 196,2,15296
M 76,v[96:99],132
M 76,v[100:103],128
ds_read2_b32 v[168:169],v15 offset0:80 offset1:81
M 76,v[104:107],124
ds_read2_b32 v[170:171],v15 offset0:82 offset1:83
M 76,v[108:111],120
L 15
R 200,2,14784
M 80,v[88:91],144
ds_read2_b32 v[172:173],v15 offset0:208 offset1:209
M 80,v[92:95],140
M 80,v[96:99],136
ds_read2_b32 v[174:175],v15 offset0:210 offset1:211
M 80,v[100:103],132
R 204,2,14272
M 80,v[104:107],128
ds_read2_b32 v[176:177],v16 offset0:80 offset1:81
M 80,v[108:111],124
ds_read2_b32 v[178:179],v16 offset0:82 offset1:83
M 80,v[112:115],120
L 15
M 84,v[88:91],148
R 208,2,13760
M 84,v[92:95],144
ds_read2_b32 v[180:181],v16 offset0:208 offset1:209
M 84,v[96:99],140
ds_read2_b32 v[182:183],v16 offset0:210 offset1:211
M 84,v[100:103],136
R 212,2,13248
M 84,v[104:107],132
M 84,v[108:111],128
M 84,v[112:115],124
M 84,v[116:119],120
L 15
M 64,v[152:155],192
ds_read2_b32 v[88:89],v1 offset0:96 offset1:97
M 64,v[156:159],188
ds_read2_b32 v[90:91],v1 offset0:98 offset1:99
M 64,v[160:163],184
L 14
R 120,2,16896
M 68,v[152:155],196
ds_read2_b32 v[92:93],v1 offset0:224 offset1:225
M 68,v[156:159],192
M 68,v[160:163],188
ds_read2_b32 v[94:95],v1 offset0:226 offset1:227
M 68,v[164:167],184
L 14
R 124,2,16384
M 72,v[152:155],200
ds_read2_b32 v[96:97],v14 offset0:96 offset1:97
M 72,v[156:159],196
ds_read2_b32 v[98:99],v14 offset0:98 offset1:99
M 72,v[160:163],192
M 72,v[164:167],188
R 128,2,15872
M 72,v[168:171],184
L 15
ds_read2_b32 v[100:101],v14 offset0:224 offset1:225
M 76,v[152:155],204
ds_read2_b32 v[102:103],v14 offset0:226 offset1:227
M 76,v[156:159],200
R 132,2,15360
M 76,v[160:163],196
M 76,v[164:167],192
ds_read2_b32 v[104:105],v15 offset0:96 offset1:97
M 76,v[168:171],188
ds_read2_b32 v[106:107],v15 offset0:98 offset1:99
M 76,v[172:175],184
L 15
R 136,2,14848
M 80,v[152:155],208
ds_read2_b32 v[108:109],v15 offset0:224 offset1:225
M 80,v[156:159],204
M 80,v[160:163],200
ds_read2_b32 v[110:111],v15 offset0:226 offset1:227
M 80,v[164:167],196
R 140,2,14336
M 80,v[168:171],192
ds_read2_b32 v[112:113],v16 offset0:96 offset1:97
M 80,v[172:175],188
ds_read2_b32 v[114:115],v16 offset0:98 offset1:99
M 80,v[176:179],184
L 15
M 84,v[152:155],212
R 144,2,13824
M 84,v[156:159],208
ds_read2_b32 v[116:117],v16 offset0:224 offset1:225
M 84,v[160:163],204
ds_read2_b32 v[118:119],v16 offset0:226 offset1:227
M 84,v[164:167],200
R 148,2,13312
M 84,v[168:171],196
M 84,v[172:175],192
M 84,v[176:179],188
M 84,v[180:183],184
L 15
M 64,v[88:91],128
ds_read2_b32 v[152:153],v1 offset0:112 offset1:113
M 64,v[92:95],124
ds_read2_b32 v[154:155],v1 offset0:114 offset1:115
M 64,v[96:99],120
L 14
R 184,2,16960
M 68,v[88:91],132
ds_read2_b32 v[156:157],v1 offset0:240 offset1:241
M 68,v[92:95],128
M 68,v[96:99],124
ds_read2_b32 v[158:159],v1 offset0:242 offset1:243
M 68,v[100:103],120
L 14
R 188,2,16448
M 72,v[88:91],136
ds_read2_b32 v[160:161],v14 offset0:112 offset1:113
M 72,v[92:95],132
ds_read2_b32 v[162:163],v14 offset0:114 offset1:115
M 72,v[96:99],128
M 72,v[100:103],124
R 192,2,15936
M 72,v[104:107],120
L 15
ds_read2_b32 v[164:165],v14 offset0:240 offset1:241
M 76,v[88:91],140
ds_read2_b32 v[166:167],v14 offset0:242 offset1:243
M 76,v[92:95],136
R 196,2,15424
M 76,v[96:99],132
M 76,v[100:103],128
ds_read2_b32 v[168:169],v15 offset0:112 offset1:113
M 76,v[104:107],124
ds_read2_b32 v[170:171],v15 offset0:114 offset1:115
M 76,v[108:111],120
L 15
R 200,2,14912
M 80,v[88:91],144
ds_read2_b32 v[172:173],v15 offset0:240 offset1:241
M 80,v[92:95],140
M 80,v[96:99],136
ds_read2_b32 v[174:175],v15 offset0:242 offset1:243
M 80,v[100:103],132
R 204,2,14400
M 80,v[104:107],128
ds_read2_b32 v[176:177],v16 offset0:112 offset1:113
M 80,v[108:111],124
ds_read2_b32 v[178:179],v16 offset0:114 offset1:115
M 80,v[112:115],120
L 15
M 84,v[88:91],148
R 208,2,13888
M 84,v[92:95],144
ds_read2_b32 v[180:181],v16 offset0:240 offset1:241
M 84,v[96:99],140
ds_read2_b32 v[182:183],v16 offset0:242 offset1:243
M 84,v[100:103],136
R 212,2,13376
M 84,v[104:107],132
M 84,v[108:111],128
M 84,v[112:115],124
M 84,v[116:119],120
L 15
M 64,v[152:155],192
M 64,v[156:159],188
M 64,v[160:163],184
L 12
M 68,v[152:155],196
M 68,v[156:159],192
M 68,v[160:163],188
M 68,v[164:167],184
L 9
M 72,v[152:155],200
M 72,v[156:159],196
M 72,v[160:163],192
ds_read_b64 v[24:25],v3 offset:15488
ds_read_b64 v[26:27],v3 offset:14976
M 72,v[164:167],188
ds_read_b64 v[28:29],v3 offset:14464
ds_read_b64 v[30:31],v3 offset:13952
M 72,v[168:171],184
L 10
ds_read_b64 v[32:33],v3 offset:13440
ds_read_b64 v[34:35],v3 offset:12928
M 76,v[152:155],204
M 76,v[156:159],200
M 76,v[160:163],196
M 76,v[164:167],192
M 76,v[168:171],188
M 76,v[172:175],184
L 9
M 80,v[152:155],208
M 80,v[156:159],204
M 80,v[160:163],200
M 80,v[164:167],196
M 80,v[168:171],192
M 80,v[172:175],188
M 80,v[176:179],184
L 6
M 84,v[152:155],212
M 84,v[156:159],208
M 84,v[160:163],204
M 84,v[164:167],200
M 84,v[168:171],196
M 84,v[172:175],192
M 84,v[176:179],188
M 84,v[180:183],184
L 0
v_and_b32 v36,0xffff0000,v25
v_lshlrev_b32 v37,16,v25
v_and_b32 v38,0xffff0000,v24
v_lshlrev_b32 v39,16,v24
v_fma_f32 v64,v36,s87,v64
v_fma_f32 v65,v37,s87,v65
v_fma_f32 v66,v38,s87,v66
v_fma_f32 v67,v39,s87,v67
v_cvt_pk_bf16_f32 v64,v64,v65
v_cvt_pk_bf16_f32 v65,v66,v67
ds_write_b64 v4,v[64:65] offset:12928
v_and_b32 v36,0xffff0000,v27
v_lshlrev_b32 v37,16,v27
v_and_b32 v38,0xffff0000,v26
v_lshlrev_b32 v39,16,v26
v_fma_f32 v68,v36,s87,v68
v_fma_f32 v69,v37,s87,v69
v_fma_f32 v70,v38,s87,v70
v_fma_f32 v71,v39,s87,v71
v_cvt_pk_bf16_f32 v68,v68,v69
v_cvt_pk_bf16_f32 v69,v70,v71
ds_write_b64 v4,v[68:69] offset:13440
v_and_b32 v36,0xffff0000,v29
v_lshlrev_b32 v37,16,v29
v_and_b32 v38,0xffff0000,v28
v_lshlrev_b32 v39,16,v28
v_fma_f32 v72,v36,s87,v72
v_fma_f32 v73,v37,s87,v73
v_fma_f32 v74,v38,s87,v74
v_fma_f32 v75,v39,s87,v75
v_cvt_pk_bf16_f32 v72,v72,v73
v_cvt_pk_bf16_f32 v73,v74,v75
ds_write_b64 v4,v[72:73] offset:13952
v_and_b32 v36,0xffff0000,v31
v_lshlrev_b32 v37,16,v31
v_and_b32 v38,0xffff0000,v30
v_lshlrev_b32 v39,16,v30
v_fma_f32 v76,v36,s87,v76
v_fma_f32 v77,v37,s87,v77
v_fma_f32 v78,v38,s87,v78
v_fma_f32 v79,v39,s87,v79
v_cvt_pk_bf16_f32 v76,v76,v77
v_cvt_pk_bf16_f32 v77,v78,v79
ds_write_b64 v4,v[76:77] offset:14464
v_and_b32 v36,0xffff0000,v33
v_lshlrev_b32 v37,16,v33
v_and_b32 v38,0xffff0000,v32
v_lshlrev_b32 v39,16,v32
v_fma_f32 v80,v36,s87,v80
v_fma_f32 v81,v37,s87,v81
v_fma_f32 v82,v38,s87,v82
v_fma_f32 v83,v39,s87,v83
v_cvt_pk_bf16_f32 v80,v80,v81
v_cvt_pk_bf16_f32 v81,v82,v83
ds_write_b64 v4,v[80:81] offset:14976
v_and_b32 v36,0xffff0000,v35
v_lshlrev_b32 v37,16,v35
v_and_b32 v38,0xffff0000,v34
v_lshlrev_b32 v39,16,v34
v_fma_f32 v84,v36,s87,v84
v_fma_f32 v85,v37,s87,v85
v_fma_f32 v86,v38,s87,v86
v_fma_f32 v87,v39,s87,v87
v_cvt_pk_bf16_f32 v84,v84,v85
v_cvt_pk_bf16_f32 v85,v86,v87
ds_write_b64 v4,v[84:85] offset:15488
Lcv_e_%=:
L 0
s_barrier
R 152,5,0
R 156,5,512
R 160,5,1024
R 164,5,1536
R 168,5,2048
R 172,5,2560
s_mov_b32 s78,99614720
L 5
buffer_store_dwordx4 v[152:155],v7,s[36:39],s78 offen offset:0 sc0 sc1
s_add_u32 s78,s56,100139008
L 4
buffer_store_dwordx4 v[156:159],v7,s[36:39],s78 offen offset:0 sc0 sc1
s_mov_b32 s78,100663296
L 3
buffer_store_dwordx4 v[160:163],v7,s[36:39],s78 offen offset:0 sc0 sc1
s_add_u32 s78,s56,101187584
L 2
buffer_store_dwordx4 v[164:167],v7,s[36:39],s78 offen offset:0 sc0 sc1
s_mov_b32 s78,101711872
L 1
buffer_store_dwordx4 v[168:171],v7,s[36:39],s78 offen offset:0 sc0 sc1
s_add_u32 s78,s56,102236160
L 0
buffer_store_dwordx4 v[172:175],v7,s[36:39],s78 offen offset:0 sc0 sc1
s_waitcnt vmcnt(0)
s_barrier
s_add_u32 s48,s48,32768
buffer_store_dword v23,v10,s[36:39],s48 offen offset:0 sc0 sc1
s_cmp_eq_u32 s51,2
s_cbranch_scc0 Lcv_f_%=
s_mov_b32 m0,163584
s_mov_b32 s78,3694592
buffer_load_dword v12,s[60:63],s78 offen offset:0  lds
s_mov_b32 m0,163584
s_mov_b32 s78,5541888
buffer_load_dword v12,s[60:63],s78 offen offset:0  lds
Lcv_f_%=:
Lcode_cv_e:
.purgem M
.purgem R
.purgem T
.purgem D
.purgem L
)"
  :: "s"(ws), "s"(sWave), "s"(sBuf), "s"(sAuxOff), "s"(sFoutOff), "s"(sC0), "s"(sC1), "s"(sExp), "v"(vL16), "v"(vPA), "v"(vPB), "v"(vPU), "v"(vPW), "v"(vOR), "v"(vORA), "v"(vOG), "v"(vFA), "v"(vFB), "v"(vFlag), "v"(vTW), "v"(vTX), "s"(w3), "s"(w4) : "v0", "v1", "v2", "v3", "v4", "v5", "v6", "v7", "v8", "v9", "v10", "v11", "v12", "v13", "v14", "v15", "v16", "v17", "v18", "v19", "v20", "v21", "v22", "v23", "v24", "v25", "v26", "v27", "v28", "v29", "v30", "v31", "v32", "v33", "v34", "v35", "v36", "v37", "v38", "v39", "v40", "v41", "v42", "v43", "v44", "v45", "v46", "v47", "v48", "v49", "v50", "v51", "v52", "v53", "v54", "v55", "v56", "v57", "v58", "v59", "v60", "v61", "v62", "v63", "v64", "v65", "v66", "v67", "v68", "v69", "v70", "v71", "v72", "v73", "v74", "v75", "v76", "v77", "v78", "v79", "v80", "v81", "v82", "v83", "v84", "v85", "v86", "v87", "v88", "v89", "v90", "v91", "v92", "v93", "v94", "v95", "v96", "v97", "v98", "v99", "v100", "v101", "v102", "v103", "v104", "v105", "v106", "v107", "v108", "v109", "v110", "v111", "v112", "v113", "v114", "v115", "v116", "v117", "v118", "v119", "v120", "v121", "v122", "v123", "v124", "v125", "v126", "v127", "v128", "v129", "v130", "v131", "v132", "v133", "v134", "v135", "v136", "v137", "v138", "v139", "v140", "v141", "v142", "v143", "v144", "v145", "v146", "v147", "v148", "v149", "v150", "v151", "v152", "v153", "v154", "v155", "v156", "v157", "v158", "v159", "v160", "v161", "v162", "v163", "v164", "v165", "v166", "v167", "v168", "v169", "v170", "v171", "v172", "v173", "v174", "v175", "v176", "v177", "v178", "v179", "v180", "v181", "v182", "v183", "v184", "v185", "v186", "v187", "v188", "v189", "v190", "v191", "v192", "v193", "v194", "v195", "v196", "v197", "v198", "v199", "v200", "v201", "v202", "v203", "v204", "v205", "v206", "v207", "v208", "v209", "v210", "v211", "v212", "v213", "v214", "v215", "v216", "v217", "v218", "v219", "v220", "v221", "v222", "v223", "v224", "v225", "v226", "v227", "v228", "v229", "v230", "v231", "a0", "a1", "a2", "a3", "a4", "a5", "a6", "a7", "a8", "a9", "a10", "a11", "a12", "a13", "a14", "a15", "a16", "a17", "a18", "a19", "a20", "a21", "a22", "a23", "a24", "a25", "a26", "a27", "a28", "a29", "a30", "a31", "a32", "a33", "a34", "a35", "a36", "a37", "a38", "a39", "a40", "a41", "a42", "a43", "a44", "a45", "a46", "a47", "a48", "a49", "a50", "a51", "a52", "a53", "a54", "a55", "a56", "a57", "a58", "a59", "a60", "a61", "a62", "a63", "a64", "a65", "a66", "a67", "a68", "a69", "a70", "a71", "a72", "a73", "a74", "a75", "a76", "a77", "a78", "a79", "a80", "a81", "a82", "a83", "a84", "a85", "a86", "a87", "a88", "a89", "a90", "a91", "a92", "a93", "a94", "a95", "a96", "a97", "a98", "a99", "a100", "a101", "a102", "a103", "a104", "a105", "a106", "a107", "a108", "a109", "a110", "a111", "a112", "a113", "a114", "a115", "a116", "a117", "a118", "a119", "a120", "a121", "a122", "a123", "a124", "a125", "a126", "a127", "a128", "a129", "a130", "a131", "a132", "a133", "a134", "a135", "a136", "a137", "a138", "a139", "a140", "a141", "a142", "a143", "a144", "a145", "a146", "a147", "a148", "a149", "a150", "a151", "a152", "a153", "a154", "a155", "a156", "a157", "a158", "a159", "a160", "a161", "a162", "a163", "a164", "a165", "a166", "a167", "a168", "a169", "a170", "a171", "a172", "a173", "a174", "a175", "a176", "a177", "a178", "a179", "a180", "a181", "a182", "a183", "a184", "a185", "a186", "a187", "a188", "a189", "a190", "a191", "a192", "a193", "a194", "a195", "a196", "a197", "a198", "a199", "a200", "a201", "a202", "a203", "a204", "a205", "a206", "a207", "a208", "a209", "a210", "a211", "a212", "a213", "a214", "a215", "a216", "a217", "a218", "a219", "a220", "a221", "a222", "a223", "a224", "a225", "a226", "a227", "a228", "a229", "a230", "a231", "a232", "a233", "a234", "a235", "a236", "a237", "a238", "a239", "a240", "a241", "a242", "a243", "a244", "a245", "a246", "a247", "a248", "a249", "a250", "a251", "a252", "a253", "a254", "a255", "s36", "s37", "s38", "s39", "s40", "s41", "s42", "s43", "s44", "s45", "s46", "s47", "s48", "s49", "s50", "s51", "s52", "s53", "s54", "s55", "s56", "s57", "s58", "s59", "s60", "s61", "s62", "s63", "s64", "s65", "s66", "s67", "s68", "s69", "s70", "s71", "s72", "s73", "s74", "s75", "s76", "s77", "s78", "s79", "s80", "s81", "s82", "s83", "s84", "s85", "s86", "s87", "s88", "s89", "s90", "s91", "s92", "s93", "s94", "s95", "s96", "s97", "vcc", "scc", "memory");
    LANE_ID(lq_);
    const int g = QT0 + (NCV - 1 - iw) * 164 + min(wave * 64 + lq_, 163);
    if (QTB) touch_lds((const char*)q + (size_t)(g < 32768 ? g : 0) * 128, sScr);
  }
  WAIT_LGKM0();
  __builtin_amdgcn_s_barrier();
  CTS(10);
  }
  const unsigned sDbg0 = 110u * 1048576u + (b * 4 + wave) * 8192;
  constexpr unsigned OOB = 0x80000000u;
#define HLP 0
  if (kind == 6 || (kind == 4 && j4 < HLP)) {
    LANE_ID(lr);
    const int r8 = lr & 7, seg = lr >> 3, j = lr & 15, g = lr >> 4;
    const unsigned sWave = wave, sExp = __builtin_amdgcn_readfirstlane(ep), sG0 = kind == 6 ? slice : 64 + j4, sStep = NRW, sOutOff = kind == 6 ? 256 : 65 + j4, sAuxOff = HLP, sBuf = kind & 2, sRk = slice;
    const unsigned sDbg = sDbg0 + (kind == 6 ? 0 : 1024);
    const unsigned vRF = (wave * 8 + (seg >> 2)) * 1024 + ((seg & 3) * 16 + r8) * 16;
    const unsigned vRwr = seg == 0 ? r8 * 64 + wave * 4 : 512 + lr * 4, vRrd = r8 * 64;
    const unsigned vRP = g == (j >> 2) ? r8 * 64 + 16 + wave * 8 + (seg & 1) * 4 : 768 + lr * 4;
    const unsigned vRS = (wave == 0 && seg < 2) ? r8 * 16 + seg * 256 : OOB, vFlag = lr < 32 ? lr * 4 : OOB;
    const unsigned vL4 = ((lr & 2) * 4 + wave * 2 + (lr & 1)) * 128, vLY = min((wave < 3 ? wave * 32 : 112) + lr, wave < 2 ? wave * 32 + 31 : wave == 2 ? 111 : NCV - 1) * 128;
    const unsigned vQ = r8 * 2048 + wave * 512 + seg * 16;
    const unsigned vY0 = wave * 4096 + lr * 16;
    const unsigned vTr = 4096 + wave * 4096 + ((2 * g + ((j & 3) >> 1)) * 8 + (j >> 2)) * 16 + (j & 1) * 8;
    const unsigned one = g == (j >> 2) ? 0x3f80u << (16 * (j & 1)) : 0u, vSa = (j & 2) ? 0u : one, vSb = (j & 2) ? one : 0u;
    (void)sDbg;
    asm volatile(
R"(.macro M d,w,x
v_mfma_f32_16x16x32_bf16 v[\d:\d+3], \w, v[\x:\x+3], v[\d:\d+3]
.endm
.macro R d,a,o
ds_read_b128 v[\d:\d+3], v\a offset:\o
.endm
.macro T d,a,o
ds_read_b64_tr_b16 \d, v\a offset:\o
.endm
.macro D a,s,o
buffer_load_dwordx4 v\a, s[36:39], s\s offen offset:\o sc1 lds
.endm
.macro L c
s_waitcnt lgkmcnt(\c)
.endm
Lcode_r_b:
s_mov_b64 s[36:37],%0
s_mov_b32 s38,111243392
s_mov_b32 s39,0x00020000
s_mov_b32 s44,%1
s_mov_b32 s45,%2
s_mov_b32 s46,%3
s_mov_b32 s47,%4
s_mov_b32 s49,%5
s_mov_b32 s51,%6
s_mov_b32 s93,%7
s_mov_b32 s53,%8
v_mov_b32 v0,%9
v_mov_b32 v1,%10
v_mov_b32 v2,%11
v_mov_b32 v3,%12
v_mov_b32 v4,%13
v_mov_b32 v5,%14
v_mov_b32 v6,%15
v_mov_b32 v7,%16
v_mov_b32 v8,%17
v_mov_b32 v9,%18
v_mov_b32 v10,%19
v_mov_b32 v11,%20
v_mov_b32 v12,%21
s_mov_b64 s[84:85],%22
s_mov_b32 s86,4194304
s_mov_b32 s87,0x00020000
s_lshl_b32 s55,s45,14
buffer_load_dwordx4 v[164:167],v8,s[84:87],s55 offen offset:0 nt
buffer_load_dwordx4 v[168:171],v8,s[84:87],s55 offen offset:128 nt
buffer_load_dwordx4 v[172:175],v8,s[84:87],s55 offen offset:256 nt
buffer_load_dwordx4 v[176:179],v8,s[84:87],s55 offen offset:384 nt
v_mov_b32 v13,s53
v_mbcnt_lo_u32_b32 v15,-1,0
v_mbcnt_hi_u32_b32 v15,-1,v15
v_lshlrev_b32 v15,4,v15
s_mov_b32 s82,0
s_mov_b32 s63,0x7fff0000
s_mov_b32 s64,0
s_mov_b32 s67,0
s_lshl_b32 s65,s44,12
s_add_u32 s65,s65,4096
s_lshl_b32 s66,s44,7
s_add_u32 s66,s66,109056000
s_mov_b32 s70,0xaaaaaaaa
s_mov_b32 s71,0xaaaaaaaa
s_mov_b32 s72,0xcccccccc
s_mov_b32 s73,0xcccccccc
s_mov_b32 s74,0xff00ff00
s_mov_b32 s75,0xff00ff00
s_mov_b32 s68,0x3a800000
s_mov_b32 s69,0x3727c5ac
s_mov_b32 s76,0xc0135761
s_mov_b32 s77,0x3d372713
v_mov_b32 v32,0x3f803f80
v_mov_b32 v33,0x3f803f80
v_mov_b32 v34,0x3f803f80
v_mov_b32 v35,0x3f803f80
v_mov_b32 v24,0
v_mov_b32 v25,0
v_mov_b32 v26,v11
v_mov_b32 v27,v12
v_mov_b32 v28,0
v_mov_b32 v29,0
s_waitcnt vmcnt(0)
Lr_a0_%=:
s_waitcnt vmcnt(10)
v_mfma_f32_16x16x32_bf16 v[36:39],v[32:35],v[164:167],0
s_add_u32 s79,s45,s46
s_cmp_lt_u32 s45,64
s_cselect_b32 s78,s49,0
v_mfma_f32_16x16x32_bf16 v[40:43],v[164:167],v[164:167],0
s_cmp_ge_u32 s79,64
s_cselect_b32 s78,s78,0
s_add_u32 s62,s79,s78
M 36,v[32:35],168
s_lshr_b32 s78,s45,1
s_and_b32 s79,s45,1
s_lshl_b32 s79,s79,7
M 40,v[168:171],168
s_lshl_b32 s57,s78,15
s_lshl_b32 s55,s62,14
s_cmp_lt_u32 s62,s47
M 36,v[32:35],172
s_cselect_b32 s55,s55,0x7fff0000
buffer_load_dwordx4 v[196:199],v8,s[84:87],s55 offen offset:0 nt
M 40,v[172:175],172
s_add_u32 s57,s57,s79
buffer_load_dwordx4 v[200:203],v8,s[84:87],s55 offen offset:128 nt
M 36,v[32:35],176
s_lshl_b32 s59,s78,9
buffer_load_dwordx4 v[204:207],v8,s[84:87],s55 offen offset:256 nt
M 40,v[176:179],176
s_add_u32 s57,s57,8388608
buffer_load_dwordx4 v[208:211],v8,s[84:87],s55 offen offset:384 nt
v_mfma_f32_16x16x32_bf16 v[116:119],v[26:29],v[164:167],0
s_add_u32 s59,s59,s79
s_lshl_b32 s60,s45,9
s_add_u32 s58,s57,4096
v_mfma_f32_16x16x32_bf16 v[120:123],v[24:27],v[164:167],0
s_add_u32 s59,s59,106954752
s_add_u32 s60,s60,s66
v_mfma_f32_16x16x32_bf16 v[124:127],v[26:29],v[168:171],0
v_mfma_f32_16x16x32_bf16 v[128:131],v[24:27],v[168:171],0
v_add_f32_dpp v44,v36,v36 row_ror:8 row_mask:0xf bank_mask:0xf
v_cndmask_b32 v45,v40,v41,s[70:71]
v_mfma_f32_16x16x32_bf16 v[132:135],v[26:29],v[172:175],0
ds_write_b32 v2,v44 offset:0
v_mfma_f32_16x16x32_bf16 v[136:139],v[24:27],v[172:175],0
v_cndmask_b32 v46,v42,v43,s[70:71]
v_cndmask_b32 v45,v45,v46,s[72:73]
v_mfma_f32_16x16x32_bf16 v[140:143],v[26:29],v[176:179],0
ds_write_b32 v0,v45 offset:0
v_mfma_f32_16x16x32_bf16 v[144:147],v[24:27],v[176:179],0
L 0
s_barrier
R 52,3,0
R 56,3,16
R 60,3,32
s_cmp_lg_u32 s67,0
s_cbranch_scc1 Lr_b0_%=
s_waitcnt vmcnt(4)
buffer_store_dword v13,v5,s[36:39],s63 offen offset:0 sc0 sc1
s_mov_b32 s63,0x7fff0000
s_cmp_lt_u32 s45,64
s_cselect_b32 s64,-192,0
s_add_u32 s64,s64,256
s_cmp_lt_u32 s45,64
s_cselect_b32 s61,0,32768
s_add_u32 s61,s61,111177728
s_mul_i32 s56,s45,16384
s_add_u32 s56,s56,98566144
Lr_bpy0_%=:
buffer_load_dword v14,v7,s[36:39],s61 offen offset:0 sc1
s_add_u32 s82,s82,1
s_waitcnt vmcnt(0)
v_cmp_eq_u32 vcc,s53,v14
s_nop 0
s_cmp_eq_u64 vcc,-1
s_cbranch_scc1 Lr_bqy0_%=
s_sleep 2
s_cmp_lt_u32 s82,20000
s_cbranch_scc1 Lr_bpy0_%=
Lr_bqy0_%=:
s_add_u32 m0,s65,0
s_nop 0
D 9,56,0
D 9,56,1024
D 9,56,2048
D 9,56,3072
s_waitcnt vmcnt(0)
Lr_b0_%=:
s_waitcnt vmcnt(10)
T v[68:69],10,0
T v[70:71],10,64
T v[72:73],10,1024
T v[74:75],10,1088
T v[76:77],10,2048
T v[78:79],10,2112
T v[80:81],10,3072
T v[82:83],10,3136
s_mov_b32 s45,s62
s_min_u32 s78,s64,s47
s_mul_i32 s56,s45,16384
s_add_u32 s56,s56,98566144
s_cmp_lt_u32 s45,s78
s_cselect_b32 s67,1,0
s_cselect_b32 s56,s56,0x7fff0000
s_add_u32 m0,s65,16384
s_nop 0
D 9,56,0
D 9,56,1024
D 9,56,2048
D 9,56,3072
L 8
v_pk_add_f32 v[52:53],v[52:53],v[54:55]
v_pk_add_f32 v[56:57],v[56:57],v[58:59]
v_pk_add_f32 v[60:61],v[60:61],v[62:63]
v_add_f32 v52,v52,v53
v_pk_add_f32 v[56:57],v[56:57],v[60:61]
v_mul_f32 v52,s68,v52
v_add_f32 v53,v56,v57
v_mul_f32 v53,s68,v53
v_fma_f32 v53,-v52,v52,v53
v_max_f32 v53,0,v53
L 6
v_lshlrev_b32 v84,16,v68
v_and_b32 v85,0xffff0000,v68
v_lshlrev_b32 v86,16,v69
v_and_b32 v87,0xffff0000,v69
v_lshlrev_b32 v88,16,v70
v_and_b32 v89,0xffff0000,v70
v_lshlrev_b32 v90,16,v71
v_and_b32 v91,0xffff0000,v71
L 4
v_lshlrev_b32 v92,16,v72
v_and_b32 v93,0xffff0000,v72
v_lshlrev_b32 v94,16,v73
v_and_b32 v95,0xffff0000,v73
v_lshlrev_b32 v96,16,v74
v_and_b32 v97,0xffff0000,v74
v_lshlrev_b32 v98,16,v75
v_and_b32 v99,0xffff0000,v75
L 2
v_lshlrev_b32 v100,16,v76
v_and_b32 v101,0xffff0000,v76
v_lshlrev_b32 v102,16,v77
v_and_b32 v103,0xffff0000,v77
v_lshlrev_b32 v104,16,v78
v_and_b32 v105,0xffff0000,v78
v_lshlrev_b32 v106,16,v79
v_and_b32 v107,0xffff0000,v79
L 0
v_lshlrev_b32 v108,16,v80
v_and_b32 v109,0xffff0000,v80
v_lshlrev_b32 v110,16,v81
v_and_b32 v111,0xffff0000,v81
v_lshlrev_b32 v112,16,v82
v_and_b32 v113,0xffff0000,v82
v_lshlrev_b32 v114,16,v83
v_and_b32 v115,0xffff0000,v83
v_add_f32 v53,s69,v53
v_rsq_f32 v48,v53
s_nop 0
v_mul_f32_e64 v50,-v52,v48
v_mov_b32 v49,v48
v_mov_b32 v51,v50
v_pk_fma_f32 v[52:53],v[116:117],v[48:49],v[50:51]
v_pk_fma_f32 v[54:55],v[118:119],v[48:49],v[50:51]
v_pk_fma_f32 v[56:57],v[120:121],v[48:49],v[50:51]
v_pk_fma_f32 v[58:59],v[122:123],v[48:49],v[50:51]
v_pk_fma_f32 v[116:117],v[52:53],v[84:85],v[116:117]
v_pk_fma_f32 v[118:119],v[54:55],v[86:87],v[118:119]
v_pk_fma_f32 v[120:121],v[56:57],v[88:89],v[120:121]
v_pk_fma_f32 v[122:123],v[58:59],v[90:91],v[122:123]
v_cvt_pk_bf16_f32 v148,v116,v117
v_cvt_pk_bf16_f32 v149,v118,v119
v_cvt_pk_bf16_f32 v150,v120,v121
v_cvt_pk_bf16_f32 v151,v122,v123
v_pk_fma_f32 v[52:53],v[124:125],v[48:49],v[50:51]
v_pk_fma_f32 v[54:55],v[126:127],v[48:49],v[50:51]
v_pk_fma_f32 v[56:57],v[128:129],v[48:49],v[50:51]
v_pk_fma_f32 v[58:59],v[130:131],v[48:49],v[50:51]
v_pk_fma_f32 v[124:125],v[52:53],v[92:93],v[124:125]
v_pk_fma_f32 v[126:127],v[54:55],v[94:95],v[126:127]
v_pk_fma_f32 v[128:129],v[56:57],v[96:97],v[128:129]
v_pk_fma_f32 v[130:131],v[58:59],v[98:99],v[130:131]
v_cvt_pk_bf16_f32 v152,v124,v125
v_cvt_pk_bf16_f32 v153,v126,v127
v_cvt_pk_bf16_f32 v154,v128,v129
v_cvt_pk_bf16_f32 v155,v130,v131
v_pk_fma_f32 v[52:53],v[132:133],v[48:49],v[50:51]
v_pk_fma_f32 v[54:55],v[134:135],v[48:49],v[50:51]
v_pk_fma_f32 v[56:57],v[136:137],v[48:49],v[50:51]
v_pk_fma_f32 v[58:59],v[138:139],v[48:49],v[50:51]
v_pk_fma_f32 v[132:133],v[52:53],v[100:101],v[132:133]
v_pk_fma_f32 v[134:135],v[54:55],v[102:103],v[134:135]
v_pk_fma_f32 v[136:137],v[56:57],v[104:105],v[136:137]
v_pk_fma_f32 v[138:139],v[58:59],v[106:107],v[138:139]
v_cvt_pk_bf16_f32 v156,v132,v133
v_cvt_pk_bf16_f32 v157,v134,v135
v_cvt_pk_bf16_f32 v158,v136,v137
v_cvt_pk_bf16_f32 v159,v138,v139
v_pk_fma_f32 v[52:53],v[140:141],v[48:49],v[50:51]
v_pk_fma_f32 v[54:55],v[142:143],v[48:49],v[50:51]
v_pk_fma_f32 v[56:57],v[144:145],v[48:49],v[50:51]
v_pk_fma_f32 v[58:59],v[146:147],v[48:49],v[50:51]
v_pk_fma_f32 v[140:141],v[52:53],v[108:109],v[140:141]
v_pk_fma_f32 v[142:143],v[54:55],v[110:111],v[142:143]
v_pk_fma_f32 v[144:145],v[56:57],v[112:113],v[144:145]
v_pk_fma_f32 v[146:147],v[58:59],v[114:115],v[146:147]
v_cvt_pk_bf16_f32 v160,v140,v141
v_cvt_pk_bf16_f32 v161,v142,v143
v_cvt_pk_bf16_f32 v162,v144,v145
v_cvt_pk_bf16_f32 v163,v146,v147
s_waitcnt vmcnt(8)
buffer_store_dword v13,v5,s[36:39],s63 offen offset:0 sc0 sc1
s_mov_b32 s63,0x7fff0000
v_mfma_f32_16x16x32_bf16 v[36:39],v[32:35],v[148:151],0
buffer_store_dwordx4 v[148:151],v1,s[36:39],s57 offen offset:0 sc0 sc1
v_mfma_f32_16x16x32_bf16 v[40:43],v[148:151],v[148:151],0
M 36,v[32:35],152
buffer_store_dwordx4 v[152:155],v1,s[36:39],s57 offen offset:2048 sc0 sc1
M 40,v[152:155],152
M 36,v[32:35],156
buffer_store_dwordx4 v[156:159],v1,s[36:39],s58 offen offset:0 sc0 sc1
M 40,v[156:159],156
M 36,v[32:35],160
buffer_store_dwordx4 v[160:163],v1,s[36:39],s58 offen offset:2048 sc0 sc1
M 40,v[160:163],160
s_nop 5
v_add_f32_dpp v44,v36,v36 row_ror:8 row_mask:0xf bank_mask:0xf
s_nop 0
v_cndmask_b32 v45,v40,v41,s[70:71]
v_cndmask_b32 v46,v42,v43,s[70:71]
ds_write_b32 v2,v44 offset:1024
v_cndmask_b32 v45,v45,v46,s[72:73]
ds_write_b32 v0,v45 offset:1024
L 0
s_barrier
R 52,3,1024
R 56,3,1040
R 60,3,1056
L 0
v_pk_add_f32 v[52:53],v[52:53],v[54:55]
v_pk_add_f32 v[56:57],v[56:57],v[58:59]
v_pk_add_f32 v[60:61],v[60:61],v[62:63]
v_add_f32 v52,v52,v53
v_pk_add_f32 v[56:57],v[56:57],v[60:61]
v_mul_f32 v52,s68,v52
v_add_f32 v53,v56,v57
v_mul_f32 v53,s68,v53
v_fma_f32 v53,-v52,v52,v53
v_max_f32 v53,0,v53
v_add_f32 v54,s69,v53
v_fma_f32 v55,s69,v54,v53
v_rsq_f32 v65,v54
v_rsq_f32 v54,v55
v_mul_f32_e64 v64,-v52,v65
v_mul_f32 v63,v55,v54
v_mul_f32 v61,s76,v54
v_mul_f32 v55,v54,v54
v_mul_f32 v62,s77,v55
v_mul_f32 v62,v62,v61
v_cndmask_b32 v60,v52,v64,s[74:75]
v_cndmask_b32 v61,v61,v65,s[74:75]
buffer_store_dwordx4 v[60:63],v4,s[36:39],s59 offen offset:0 sc0 sc1
s_mov_b32 s63,s60
s_cmp_lt_u32 s45,s47
s_cbranch_scc0 Lr_mid_%=
Lr_a1_%=:
s_waitcnt vmcnt(10)
v_mfma_f32_16x16x32_bf16 v[36:39],v[32:35],v[196:199],0
s_add_u32 s79,s45,s46
s_cmp_lt_u32 s45,64
s_cselect_b32 s78,s49,0
v_mfma_f32_16x16x32_bf16 v[40:43],v[196:199],v[196:199],0
s_cmp_ge_u32 s79,64
s_cselect_b32 s78,s78,0
s_add_u32 s62,s79,s78
M 36,v[32:35],200
s_lshr_b32 s78,s45,1
s_and_b32 s79,s45,1
s_lshl_b32 s79,s79,7
M 40,v[200:203],200
s_lshl_b32 s57,s78,15
s_lshl_b32 s55,s62,14
s_cmp_lt_u32 s62,s47
M 36,v[32:35],204
s_cselect_b32 s55,s55,0x7fff0000
buffer_load_dwordx4 v[164:167],v8,s[84:87],s55 offen offset:0 nt
M 40,v[204:207],204
s_add_u32 s57,s57,s79
buffer_load_dwordx4 v[168:171],v8,s[84:87],s55 offen offset:128 nt
M 36,v[32:35],208
s_lshl_b32 s59,s78,9
buffer_load_dwordx4 v[172:175],v8,s[84:87],s55 offen offset:256 nt
M 40,v[208:211],208
s_add_u32 s57,s57,8388608
buffer_load_dwordx4 v[176:179],v8,s[84:87],s55 offen offset:384 nt
v_mfma_f32_16x16x32_bf16 v[116:119],v[26:29],v[196:199],0
s_add_u32 s59,s59,s79
s_lshl_b32 s60,s45,9
s_add_u32 s58,s57,4096
v_mfma_f32_16x16x32_bf16 v[120:123],v[24:27],v[196:199],0
s_add_u32 s59,s59,106954752
s_add_u32 s60,s60,s66
v_mfma_f32_16x16x32_bf16 v[124:127],v[26:29],v[200:203],0
v_mfma_f32_16x16x32_bf16 v[128:131],v[24:27],v[200:203],0
v_add_f32_dpp v44,v36,v36 row_ror:8 row_mask:0xf bank_mask:0xf
v_cndmask_b32 v45,v40,v41,s[70:71]
v_mfma_f32_16x16x32_bf16 v[132:135],v[26:29],v[204:207],0
ds_write_b32 v2,v44 offset:0
v_mfma_f32_16x16x32_bf16 v[136:139],v[24:27],v[204:207],0
v_cndmask_b32 v46,v42,v43,s[70:71]
v_cndmask_b32 v45,v45,v46,s[72:73]
v_mfma_f32_16x16x32_bf16 v[140:143],v[26:29],v[208:211],0
ds_write_b32 v0,v45 offset:0
v_mfma_f32_16x16x32_bf16 v[144:147],v[24:27],v[208:211],0
L 0
s_barrier
R 52,3,0
R 56,3,16
R 60,3,32
s_cmp_lg_u32 s67,0
s_cbranch_scc1 Lr_b1_%=
s_waitcnt vmcnt(4)
buffer_store_dword v13,v5,s[36:39],s63 offen offset:0 sc0 sc1
s_mov_b32 s63,0x7fff0000
s_cmp_lt_u32 s45,64
s_cselect_b32 s64,-192,0
s_add_u32 s64,s64,256
s_cmp_lt_u32 s45,64
s_cselect_b32 s61,0,32768
s_add_u32 s61,s61,111177728
s_mul_i32 s56,s45,16384
s_add_u32 s56,s56,98566144
Lr_bpy1_%=:
buffer_load_dword v14,v7,s[36:39],s61 offen offset:0 sc1
s_add_u32 s82,s82,1
s_waitcnt vmcnt(0)
v_cmp_eq_u32 vcc,s53,v14
s_nop 0
s_cmp_eq_u64 vcc,-1
s_cbranch_scc1 Lr_bqy1_%=
s_sleep 2
s_cmp_lt_u32 s82,20000
s_cbranch_scc1 Lr_bpy1_%=
Lr_bqy1_%=:
s_add_u32 m0,s65,16384
s_nop 0
D 9,56,0
D 9,56,1024
D 9,56,2048
D 9,56,3072
s_waitcnt vmcnt(0)
Lr_b1_%=:
s_waitcnt vmcnt(10)
T v[68:69],10,16384
T v[70:71],10,16448
T v[72:73],10,17408
T v[74:75],10,17472
T v[76:77],10,18432
T v[78:79],10,18496
T v[80:81],10,19456
T v[82:83],10,19520
s_mov_b32 s45,s62
s_min_u32 s78,s64,s47
s_mul_i32 s56,s45,16384
s_add_u32 s56,s56,98566144
s_cmp_lt_u32 s45,s78
s_cselect_b32 s67,1,0
s_cselect_b32 s56,s56,0x7fff0000
s_add_u32 m0,s65,0
s_nop 0
D 9,56,0
D 9,56,1024
D 9,56,2048
D 9,56,3072
L 8
v_pk_add_f32 v[52:53],v[52:53],v[54:55]
v_pk_add_f32 v[56:57],v[56:57],v[58:59]
v_pk_add_f32 v[60:61],v[60:61],v[62:63]
v_add_f32 v52,v52,v53
v_pk_add_f32 v[56:57],v[56:57],v[60:61]
v_mul_f32 v52,s68,v52
v_add_f32 v53,v56,v57
v_mul_f32 v53,s68,v53
v_fma_f32 v53,-v52,v52,v53
v_max_f32 v53,0,v53
L 6
v_lshlrev_b32 v84,16,v68
v_and_b32 v85,0xffff0000,v68
v_lshlrev_b32 v86,16,v69
v_and_b32 v87,0xffff0000,v69
v_lshlrev_b32 v88,16,v70
v_and_b32 v89,0xffff0000,v70
v_lshlrev_b32 v90,16,v71
v_and_b32 v91,0xffff0000,v71
L 4
v_lshlrev_b32 v92,16,v72
v_and_b32 v93,0xffff0000,v72
v_lshlrev_b32 v94,16,v73
v_and_b32 v95,0xffff0000,v73
v_lshlrev_b32 v96,16,v74
v_and_b32 v97,0xffff0000,v74
v_lshlrev_b32 v98,16,v75
v_and_b32 v99,0xffff0000,v75
L 2
v_lshlrev_b32 v100,16,v76
v_and_b32 v101,0xffff0000,v76
v_lshlrev_b32 v102,16,v77
v_and_b32 v103,0xffff0000,v77
v_lshlrev_b32 v104,16,v78
v_and_b32 v105,0xffff0000,v78
v_lshlrev_b32 v106,16,v79
v_and_b32 v107,0xffff0000,v79
L 0
v_lshlrev_b32 v108,16,v80
v_and_b32 v109,0xffff0000,v80
v_lshlrev_b32 v110,16,v81
v_and_b32 v111,0xffff0000,v81
v_lshlrev_b32 v112,16,v82
v_and_b32 v113,0xffff0000,v82
)"
R"(v_lshlrev_b32 v114,16,v83
v_and_b32 v115,0xffff0000,v83
v_add_f32 v53,s69,v53
v_rsq_f32 v48,v53
s_nop 0
v_mul_f32_e64 v50,-v52,v48
v_mov_b32 v49,v48
v_mov_b32 v51,v50
v_pk_fma_f32 v[52:53],v[116:117],v[48:49],v[50:51]
v_pk_fma_f32 v[54:55],v[118:119],v[48:49],v[50:51]
v_pk_fma_f32 v[56:57],v[120:121],v[48:49],v[50:51]
v_pk_fma_f32 v[58:59],v[122:123],v[48:49],v[50:51]
v_pk_fma_f32 v[116:117],v[52:53],v[84:85],v[116:117]
v_pk_fma_f32 v[118:119],v[54:55],v[86:87],v[118:119]
v_pk_fma_f32 v[120:121],v[56:57],v[88:89],v[120:121]
v_pk_fma_f32 v[122:123],v[58:59],v[90:91],v[122:123]
v_cvt_pk_bf16_f32 v148,v116,v117
v_cvt_pk_bf16_f32 v149,v118,v119
v_cvt_pk_bf16_f32 v150,v120,v121
v_cvt_pk_bf16_f32 v151,v122,v123
v_pk_fma_f32 v[52:53],v[124:125],v[48:49],v[50:51]
v_pk_fma_f32 v[54:55],v[126:127],v[48:49],v[50:51]
v_pk_fma_f32 v[56:57],v[128:129],v[48:49],v[50:51]
v_pk_fma_f32 v[58:59],v[130:131],v[48:49],v[50:51]
v_pk_fma_f32 v[124:125],v[52:53],v[92:93],v[124:125]
v_pk_fma_f32 v[126:127],v[54:55],v[94:95],v[126:127]
v_pk_fma_f32 v[128:129],v[56:57],v[96:97],v[128:129]
v_pk_fma_f32 v[130:131],v[58:59],v[98:99],v[130:131]
v_cvt_pk_bf16_f32 v152,v124,v125
v_cvt_pk_bf16_f32 v153,v126,v127
v_cvt_pk_bf16_f32 v154,v128,v129
v_cvt_pk_bf16_f32 v155,v130,v131
v_pk_fma_f32 v[52:53],v[132:133],v[48:49],v[50:51]
v_pk_fma_f32 v[54:55],v[134:135],v[48:49],v[50:51]
v_pk_fma_f32 v[56:57],v[136:137],v[48:49],v[50:51]
v_pk_fma_f32 v[58:59],v[138:139],v[48:49],v[50:51]
v_pk_fma_f32 v[132:133],v[52:53],v[100:101],v[132:133]
v_pk_fma_f32 v[134:135],v[54:55],v[102:103],v[134:135]
v_pk_fma_f32 v[136:137],v[56:57],v[104:105],v[136:137]
v_pk_fma_f32 v[138:139],v[58:59],v[106:107],v[138:139]
v_cvt_pk_bf16_f32 v156,v132,v133
v_cvt_pk_bf16_f32 v157,v134,v135
v_cvt_pk_bf16_f32 v158,v136,v137
v_cvt_pk_bf16_f32 v159,v138,v139
v_pk_fma_f32 v[52:53],v[140:141],v[48:49],v[50:51]
v_pk_fma_f32 v[54:55],v[142:143],v[48:49],v[50:51]
v_pk_fma_f32 v[56:57],v[144:145],v[48:49],v[50:51]
v_pk_fma_f32 v[58:59],v[146:147],v[48:49],v[50:51]
v_pk_fma_f32 v[140:141],v[52:53],v[108:109],v[140:141]
v_pk_fma_f32 v[142:143],v[54:55],v[110:111],v[142:143]
v_pk_fma_f32 v[144:145],v[56:57],v[112:113],v[144:145]
v_pk_fma_f32 v[146:147],v[58:59],v[114:115],v[146:147]
v_cvt_pk_bf16_f32 v160,v140,v141
v_cvt_pk_bf16_f32 v161,v142,v143
v_cvt_pk_bf16_f32 v162,v144,v145
v_cvt_pk_bf16_f32 v163,v146,v147
s_waitcnt vmcnt(8)
buffer_store_dword v13,v5,s[36:39],s63 offen offset:0 sc0 sc1
s_mov_b32 s63,0x7fff0000
v_mfma_f32_16x16x32_bf16 v[36:39],v[32:35],v[148:151],0
buffer_store_dwordx4 v[148:151],v1,s[36:39],s57 offen offset:0 sc0 sc1
v_mfma_f32_16x16x32_bf16 v[40:43],v[148:151],v[148:151],0
M 36,v[32:35],152
buffer_store_dwordx4 v[152:155],v1,s[36:39],s57 offen offset:2048 sc0 sc1
M 40,v[152:155],152
M 36,v[32:35],156
buffer_store_dwordx4 v[156:159],v1,s[36:39],s58 offen offset:0 sc0 sc1
M 40,v[156:159],156
M 36,v[32:35],160
buffer_store_dwordx4 v[160:163],v1,s[36:39],s58 offen offset:2048 sc0 sc1
M 40,v[160:163],160
s_nop 5
v_add_f32_dpp v44,v36,v36 row_ror:8 row_mask:0xf bank_mask:0xf
s_nop 0
v_cndmask_b32 v45,v40,v41,s[70:71]
v_cndmask_b32 v46,v42,v43,s[70:71]
ds_write_b32 v2,v44 offset:1024
v_cndmask_b32 v45,v45,v46,s[72:73]
ds_write_b32 v0,v45 offset:1024
L 0
s_barrier
R 52,3,1024
R 56,3,1040
R 60,3,1056
L 0
v_pk_add_f32 v[52:53],v[52:53],v[54:55]
v_pk_add_f32 v[56:57],v[56:57],v[58:59]
v_pk_add_f32 v[60:61],v[60:61],v[62:63]
v_add_f32 v52,v52,v53
v_pk_add_f32 v[56:57],v[56:57],v[60:61]
v_mul_f32 v52,s68,v52
v_add_f32 v53,v56,v57
v_mul_f32 v53,s68,v53
v_fma_f32 v53,-v52,v52,v53
v_max_f32 v53,0,v53
v_add_f32 v54,s69,v53
v_fma_f32 v55,s69,v54,v53
v_rsq_f32 v65,v54
v_rsq_f32 v54,v55
v_mul_f32_e64 v64,-v52,v65
v_mul_f32 v63,v55,v54
v_mul_f32 v61,s76,v54
v_mul_f32 v55,v54,v54
v_mul_f32 v62,s77,v55
v_mul_f32 v62,v62,v61
v_cndmask_b32 v60,v52,v64,s[74:75]
v_cndmask_b32 v61,v61,v65,s[74:75]
buffer_store_dwordx4 v[60:63],v4,s[36:39],s59 offen offset:0 sc0 sc1
s_mov_b32 s63,s60
s_cmp_lt_u32 s45,s47
s_cbranch_scc1 Lr_a0_%=
Lr_mid_%=:
s_waitcnt vmcnt(0)
buffer_store_dword v13,v5,s[36:39],s63 offen offset:0 sc0 sc1
s_mov_b32 s63,0x7fff0000
s_cmp_eq_u32 s51,0
s_cbranch_scc1 Lr_done_%=
s_mov_b32 s45,s93
s_lshr_b32 s78,s45,1
s_and_b32 s79,s45,1
s_lshl_b32 s79,s79,7
s_lshl_b32 s55,s78,15
s_add_u32 s55,s55,s79
s_mul_i32 s56,s45,16384
s_add_u32 s56,s56,98566144
s_lshl_b32 s61,s78,11
s_add_u32 s61,s61,110587904
s_add_u32 s78,s45,s46
s_lshr_b32 s78,s78,1
s_lshl_b32 s62,s78,11
s_add_u32 s62,s62,110587904
Lr_bps_%=:
buffer_load_dword v14,v6,s[36:39],s61 offen offset:0 sc1
s_add_u32 s82,s82,1
s_waitcnt vmcnt(0)
v_cmp_eq_u32 vcc,s53,v14
s_nop 0
s_cmp_eq_u64 vcc,-1
s_cbranch_scc1 Lr_bqs_%=
s_sleep 2
s_cmp_lt_u32 s82,20000
s_cbranch_scc1 Lr_bps_%=
Lr_bqs_%=:
s_add_u32 s78,s55,71303168
buffer_load_dwordx4 v[164:167],v1,s[36:39],s78 offen offset:0 sc1
buffer_load_dwordx4 v[168:171],v1,s[36:39],s78 offen offset:2048 sc1
s_add_u32 s78,s55,71307264
buffer_load_dwordx4 v[172:175],v1,s[36:39],s78 offen offset:0 sc1
buffer_load_dwordx4 v[176:179],v1,s[36:39],s78 offen offset:2048 sc1
s_add_u32 s78,s55,89128960
buffer_load_dwordx4 v[180:183],v1,s[36:39],s78 offen offset:0 sc1
buffer_load_dwordx4 v[184:187],v1,s[36:39],s78 offen offset:2048 sc1
s_add_u32 s78,s55,89133056
buffer_load_dwordx4 v[188:191],v1,s[36:39],s78 offen offset:0 sc1
buffer_load_dwordx4 v[192:195],v1,s[36:39],s78 offen offset:2048 sc1
s_add_u32 m0,s65,0
s_nop 0
D 9,56,0
D 9,56,1024
D 9,56,2048
D 9,56,3072
buffer_load_dword v14,v6,s[36:39],s62 offen offset:0 sc1
s_lshr_b32 s78,s45,1
s_and_b32 s79,s45,1
s_lshl_b32 s79,s79,7
s_lshl_b32 s57,s78,15
s_add_u32 s57,s57,s79
s_add_u32 s57,s57,12582912
s_add_u32 s58,s57,4096
s_lshl_b32 s59,s78,9
s_add_u32 s59,s59,s79
s_add_u32 s59,s59,107020288
s_lshl_b32 s60,s45,9
s_add_u32 s60,s60,s66
s_add_u32 s60,s60,131072
s_add_u32 s45,s45,s46
s_lshr_b32 s78,s45,1
s_and_b32 s79,s45,1
s_lshl_b32 s79,s79,7
s_lshl_b32 s55,s78,15
s_add_u32 s55,s55,s79
s_mul_i32 s56,s45,16384
s_add_u32 s56,s56,98566144
s_lshl_b32 s61,s78,11
s_add_u32 s61,s61,110587904
s_add_u32 s78,s45,s46
s_lshr_b32 s78,s78,1
s_lshl_b32 s62,s78,11
s_add_u32 s62,s62,110587904
s_waitcnt vmcnt(0)
Lr_it0_%=:
T v[68:69],10,0
T v[70:71],10,64
T v[72:73],10,1024
T v[74:75],10,1088
T v[76:77],10,2048
T v[78:79],10,2112
T v[80:81],10,3072
T v[82:83],10,3136
s_nop 0
v_cmp_eq_u32 vcc,s53,v14
s_nop 0
s_cmp_eq_u64 vcc,-1
s_cselect_b32 s78,s45,256
s_cmp_lt_u32 s78,256
s_cbranch_scc1 Lr_ta0_%=
buffer_load_dword v14,v6,s[36:39],s61 offen offset:0 sc1
v_mfma_f32_16x16x32_bf16 v[116:119],v[26:29],v[164:167],0
L 6
v_lshlrev_b32 v84,16,v68
v_and_b32 v85,0xffff0000,v68
v_mfma_f32_16x16x32_bf16 v[120:123],v[24:27],v[164:167],0
L 4
v_lshlrev_b32 v86,16,v69
v_and_b32 v87,0xffff0000,v69
M 116,v[26:29],180
L 2
v_lshlrev_b32 v88,16,v70
v_and_b32 v89,0xffff0000,v70
M 120,v[24:27],180
L 0
v_lshlrev_b32 v90,16,v71
v_and_b32 v91,0xffff0000,v71
v_mfma_f32_16x16x32_bf16 v[124:127],v[26:29],v[168:171],0
v_lshlrev_b32 v92,16,v72
v_and_b32 v93,0xffff0000,v72
v_mfma_f32_16x16x32_bf16 v[128:131],v[24:27],v[168:171],0
v_lshlrev_b32 v94,16,v73
v_and_b32 v95,0xffff0000,v73
M 124,v[26:29],184
v_cvt_pk_bf16_f32 v148,v116,v117
v_cvt_pk_bf16_f32 v149,v118,v119
M 128,v[24:27],184
v_cvt_pk_bf16_f32 v150,v120,v121
v_cvt_pk_bf16_f32 v151,v122,v123
v_mfma_f32_16x16x32_bf16 v[132:135],v[26:29],v[172:175],0
v_lshlrev_b32 v96,16,v74
v_and_b32 v97,0xffff0000,v74
v_mfma_f32_16x16x32_bf16 v[136:139],v[24:27],v[172:175],0
v_lshlrev_b32 v98,16,v75
v_and_b32 v99,0xffff0000,v75
M 132,v[26:29],188
v_cvt_pk_bf16_f32 v152,v124,v125
v_cvt_pk_bf16_f32 v153,v126,v127
M 136,v[24:27],188
v_cvt_pk_bf16_f32 v154,v128,v129
v_cvt_pk_bf16_f32 v155,v130,v131
v_mfma_f32_16x16x32_bf16 v[140:143],v[26:29],v[176:179],0
v_lshlrev_b32 v100,16,v76
v_and_b32 v101,0xffff0000,v76
v_mfma_f32_16x16x32_bf16 v[144:147],v[24:27],v[176:179],0
v_lshlrev_b32 v102,16,v77
v_and_b32 v103,0xffff0000,v77
M 140,v[26:29],192
v_cvt_pk_bf16_f32 v156,v132,v133
v_cvt_pk_bf16_f32 v157,v134,v135
M 144,v[24:27],192
v_cvt_pk_bf16_f32 v158,v136,v137
v_cvt_pk_bf16_f32 v159,v138,v139
v_mfma_f32_16x16x32_bf16 v[36:39],v[32:35],v[148:151],0
v_lshlrev_b32 v104,16,v78
v_and_b32 v105,0xffff0000,v78
v_mfma_f32_16x16x32_bf16 v[40:43],v[148:151],v[148:151],0
v_lshlrev_b32 v106,16,v79
v_and_b32 v107,0xffff0000,v79
M 36,v[32:35],152
v_cvt_pk_bf16_f32 v160,v140,v141
v_cvt_pk_bf16_f32 v161,v142,v143
M 40,v[152:155],152
v_cvt_pk_bf16_f32 v162,v144,v145
v_cvt_pk_bf16_f32 v163,v146,v147
M 36,v[32:35],156
v_lshlrev_b32 v108,16,v80
v_and_b32 v109,0xffff0000,v80
M 40,v[156:159],156
v_lshlrev_b32 v110,16,v81
v_and_b32 v111,0xffff0000,v81
M 36,v[32:35],160
v_lshlrev_b32 v112,16,v82
v_and_b32 v113,0xffff0000,v82
M 40,v[160:163],160
v_lshlrev_b32 v114,16,v83
v_and_b32 v115,0xffff0000,v83
s_nop 2
v_add_f32_dpp v44,v36,v36 row_ror:8 row_mask:0xf bank_mask:0xf
s_nop 1
v_cndmask_b32 v45,v40,v41,s[70:71]
v_cndmask_b32 v46,v42,v43,s[70:71]
ds_write_b32 v2,v44 offset:0
v_cndmask_b32 v45,v45,v46,s[72:73]
ds_write_b32 v0,v45 offset:0
L 0
s_barrier
R 52,3,0
R 56,3,16
R 60,3,32
L 0
v_pk_add_f32 v[52:53],v[52:53],v[54:55]
v_pk_add_f32 v[56:57],v[56:57],v[58:59]
v_pk_add_f32 v[60:61],v[60:61],v[62:63]
v_add_f32 v52,v52,v53
v_pk_add_f32 v[56:57],v[56:57],v[60:61]
v_mul_f32 v52,s68,v52
v_add_f32 v53,v56,v57
v_mul_f32 v53,s68,v53
v_fma_f32 v53,-v52,v52,v53
v_max_f32 v53,0,v53
s_waitcnt vmcnt(1)
buffer_store_dword v13,v5,s[36:39],s63 offen offset:0 sc0 sc1
s_mov_b32 s63,0x7fff0000
s_waitcnt vmcnt(1)
v_cmp_eq_u32 vcc,s53,v14
s_nop 0
s_cmp_eq_u64 vcc,-1
s_cselect_b32 s78,s45,256
s_cmp_lt_u32 s78,256
s_cbranch_scc1 Lr_tb0_%=
buffer_load_dword v14,v6,s[36:39],s61 offen offset:0 sc1
v_add_f32 v53,s69,v53
v_rsq_f32 v48,v53
s_nop 0
v_mul_f32_e64 v50,-v52,v48
v_mov_b32 v49,v48
v_mov_b32 v51,v50
v_pk_fma_f32 v[52:53],v[116:117],v[48:49],v[50:51]
v_pk_fma_f32 v[54:55],v[118:119],v[48:49],v[50:51]
v_pk_fma_f32 v[56:57],v[120:121],v[48:49],v[50:51]
v_pk_fma_f32 v[58:59],v[122:123],v[48:49],v[50:51]
v_pk_fma_f32 v[116:117],v[52:53],v[84:85],v[116:117]
v_pk_fma_f32 v[118:119],v[54:55],v[86:87],v[118:119]
v_pk_fma_f32 v[120:121],v[56:57],v[88:89],v[120:121]
v_pk_fma_f32 v[122:123],v[58:59],v[90:91],v[122:123]
v_cvt_pk_bf16_f32 v148,v116,v117
v_cvt_pk_bf16_f32 v149,v118,v119
v_cvt_pk_bf16_f32 v150,v120,v121
v_cvt_pk_bf16_f32 v151,v122,v123
v_pk_fma_f32 v[52:53],v[124:125],v[48:49],v[50:51]
v_pk_fma_f32 v[54:55],v[126:127],v[48:49],v[50:51]
v_pk_fma_f32 v[56:57],v[128:129],v[48:49],v[50:51]
v_pk_fma_f32 v[58:59],v[130:131],v[48:49],v[50:51]
v_pk_fma_f32 v[124:125],v[52:53],v[92:93],v[124:125]
v_pk_fma_f32 v[126:127],v[54:55],v[94:95],v[126:127]
v_pk_fma_f32 v[128:129],v[56:57],v[96:97],v[128:129]
v_pk_fma_f32 v[130:131],v[58:59],v[98:99],v[130:131]
v_cvt_pk_bf16_f32 v152,v124,v125
v_cvt_pk_bf16_f32 v153,v126,v127
v_cvt_pk_bf16_f32 v154,v128,v129
v_cvt_pk_bf16_f32 v155,v130,v131
v_pk_fma_f32 v[52:53],v[132:133],v[48:49],v[50:51]
v_pk_fma_f32 v[54:55],v[134:135],v[48:49],v[50:51]
v_pk_fma_f32 v[56:57],v[136:137],v[48:49],v[50:51]
v_pk_fma_f32 v[58:59],v[138:139],v[48:49],v[50:51]
v_pk_fma_f32 v[132:133],v[52:53],v[100:101],v[132:133]
v_pk_fma_f32 v[134:135],v[54:55],v[102:103],v[134:135]
v_pk_fma_f32 v[136:137],v[56:57],v[104:105],v[136:137]
v_pk_fma_f32 v[138:139],v[58:59],v[106:107],v[138:139]
v_cvt_pk_bf16_f32 v156,v132,v133
v_cvt_pk_bf16_f32 v157,v134,v135
v_cvt_pk_bf16_f32 v158,v136,v137
v_cvt_pk_bf16_f32 v159,v138,v139
v_pk_fma_f32 v[52:53],v[140:141],v[48:49],v[50:51]
v_pk_fma_f32 v[54:55],v[142:143],v[48:49],v[50:51]
v_pk_fma_f32 v[56:57],v[144:145],v[48:49],v[50:51]
v_pk_fma_f32 v[58:59],v[146:147],v[48:49],v[50:51]
v_pk_fma_f32 v[140:141],v[52:53],v[108:109],v[140:141]
v_pk_fma_f32 v[142:143],v[54:55],v[110:111],v[142:143]
v_pk_fma_f32 v[144:145],v[56:57],v[112:113],v[144:145]
v_pk_fma_f32 v[146:147],v[58:59],v[114:115],v[146:147]
v_cvt_pk_bf16_f32 v160,v140,v141
v_cvt_pk_bf16_f32 v161,v142,v143
v_cvt_pk_bf16_f32 v162,v144,v145
v_cvt_pk_bf16_f32 v163,v146,v147
v_mfma_f32_16x16x32_bf16 v[36:39],v[32:35],v[148:151],0
buffer_store_dwordx4 v[148:151],v1,s[36:39],s57 offen offset:0 sc0 sc1
v_mfma_f32_16x16x32_bf16 v[40:43],v[148:151],v[148:151],0
M 36,v[32:35],152
buffer_store_dwordx4 v[152:155],v1,s[36:39],s57 offen offset:2048 sc0 sc1
M 40,v[152:155],152
M 36,v[32:35],156
buffer_store_dwordx4 v[156:159],v1,s[36:39],s58 offen offset:0 sc0 sc1
M 40,v[156:159],156
M 36,v[32:35],160
buffer_store_dwordx4 v[160:163],v1,s[36:39],s58 offen offset:2048 sc0 sc1
M 40,v[160:163],160
s_nop 5
v_add_f32_dpp v44,v36,v36 row_ror:8 row_mask:0xf bank_mask:0xf
s_nop 0
v_cndmask_b32 v45,v40,v41,s[70:71]
v_cndmask_b32 v46,v42,v43,s[70:71]
ds_write_b32 v2,v44 offset:1024
v_cndmask_b32 v45,v45,v46,s[72:73]
ds_write_b32 v0,v45 offset:1024
L 0
s_barrier
R 52,3,1024
R 56,3,1040
R 60,3,1056
L 0
v_pk_add_f32 v[52:53],v[52:53],v[54:55]
v_pk_add_f32 v[56:57],v[56:57],v[58:59]
v_pk_add_f32 v[60:61],v[60:61],v[62:63]
v_add_f32 v52,v52,v53
v_pk_add_f32 v[56:57],v[56:57],v[60:61]
v_mul_f32 v52,s68,v52
v_add_f32 v53,v56,v57
v_mul_f32 v53,s68,v53
v_fma_f32 v53,-v52,v52,v53
v_max_f32 v53,0,v53
v_add_f32 v54,s69,v53
v_fma_f32 v55,s69,v54,v53
v_rsq_f32 v65,v54
v_rsq_f32 v54,v55
v_mul_f32_e64 v64,-v52,v65
v_mul_f32 v63,v55,v54
v_mul_f32 v61,s76,v54
v_mul_f32 v55,v54,v54
v_mul_f32 v62,s77,v55
v_mul_f32 v62,v62,v61
v_cndmask_b32 v60,v52,v64,s[74:75]
v_cndmask_b32 v61,v61,v65,s[74:75]
buffer_store_dwordx4 v[60:63],v4,s[36:39],s59 offen offset:0 sc0 sc1
s_mov_b32 s63,s60
s_waitcnt vmcnt(5)
v_cmp_eq_u32 vcc,s53,v14
s_nop 0
s_cmp_eq_u64 vcc,-1
s_cselect_b32 s78,s45,256
s_cmp_lt_u32 s78,256
s_cbranch_scc1 Lr_tc0_%=
s_waitcnt vmcnt(0)
buffer_store_dword v13,v5,s[36:39],s63 offen offset:0 sc0 sc1
s_mov_b32 s63,0x7fff0000
s_cmp_ge_u32 s45,256
s_cbranch_scc1 Lr_done_%=
Lr_bp0_%=:
buffer_load_dword v14,v6,s[36:39],s61 offen offset:0 sc1
s_add_u32 s82,s82,1
s_waitcnt vmcnt(0)
v_cmp_eq_u32 vcc,s53,v14
s_nop 0
s_cmp_eq_u64 vcc,-1
s_cbranch_scc1 Lr_bq0_%=
s_sleep 2
s_cmp_lt_u32 s82,20000
s_cbranch_scc1 Lr_bp0_%=
Lr_bq0_%=:
s_add_u32 s78,s55,71303168
buffer_load_dwordx4 v[196:199],v1,s[36:39],s78 offen offset:0 sc1
buffer_load_dwordx4 v[200:203],v1,s[36:39],s78 offen offset:2048 sc1
s_add_u32 s78,s55,71307264
buffer_load_dwordx4 v[204:207],v1,s[36:39],s78 offen offset:0 sc1
buffer_load_dwordx4 v[208:211],v1,s[36:39],s78 offen offset:2048 sc1
s_add_u32 s78,s55,89128960
buffer_load_dwordx4 v[212:215],v1,s[36:39],s78 offen offset:0 sc1
buffer_load_dwordx4 v[216:219],v1,s[36:39],s78 offen offset:2048 sc1
s_add_u32 s78,s55,89133056
buffer_load_dwordx4 v[220:223],v1,s[36:39],s78 offen offset:0 sc1
buffer_load_dwordx4 v[224:227],v1,s[36:39],s78 offen offset:2048 sc1
s_add_u32 m0,s65,16384
s_nop 0
D 9,56,0
D 9,56,1024
D 9,56,2048
D 9,56,3072
buffer_load_dword v14,v6,s[36:39],s62 offen offset:0 sc1
s_lshr_b32 s78,s45,1
s_and_b32 s79,s45,1
s_lshl_b32 s79,s79,7
s_lshl_b32 s57,s78,15
s_add_u32 s57,s57,s79
s_add_u32 s57,s57,12582912
s_add_u32 s58,s57,4096
s_lshl_b32 s59,s78,9
s_add_u32 s59,s59,s79
s_add_u32 s59,s59,107020288
s_lshl_b32 s60,s45,9
s_add_u32 s60,s60,s66
s_add_u32 s60,s60,131072
s_add_u32 s45,s45,s46
s_lshr_b32 s78,s45,1
s_and_b32 s79,s45,1
s_lshl_b32 s79,s79,7
s_lshl_b32 s55,s78,15
s_add_u32 s55,s55,s79
s_mul_i32 s56,s45,16384
s_add_u32 s56,s56,98566144
s_lshl_b32 s61,s78,11
s_add_u32 s61,s61,110587904
s_add_u32 s78,s45,s46
s_lshr_b32 s78,s78,1
s_lshl_b32 s62,s78,11
s_add_u32 s62,s62,110587904
s_waitcnt vmcnt(0)
s_branch Lr_it1_%=
Lr_ta0_%=:
v_mfma_f32_16x16x32_bf16 v[116:119],v[26:29],v[164:167],0
s_add_u32 s78,s55,71303168
buffer_load_dwordx4 v[196:199],v1,s[36:39],s78 offen offset:0 sc1
v_mfma_f32_16x16x32_bf16 v[120:123],v[24:27],v[164:167],0
L 6
)"
R"(buffer_load_dwordx4 v[200:203],v1,s[36:39],s78 offen offset:2048 sc1
M 116,v[26:29],180
s_add_u32 s78,s55,71307264
buffer_load_dwordx4 v[204:207],v1,s[36:39],s78 offen offset:0 sc1
M 120,v[24:27],180
L 4
buffer_load_dwordx4 v[208:211],v1,s[36:39],s78 offen offset:2048 sc1
v_mfma_f32_16x16x32_bf16 v[124:127],v[26:29],v[168:171],0
s_add_u32 s78,s55,89128960
buffer_load_dwordx4 v[212:215],v1,s[36:39],s78 offen offset:0 sc1
v_mfma_f32_16x16x32_bf16 v[128:131],v[24:27],v[168:171],0
L 2
buffer_load_dwordx4 v[216:219],v1,s[36:39],s78 offen offset:2048 sc1
M 124,v[26:29],184
L 0
v_cvt_pk_bf16_f32 v148,v116,v117
v_cvt_pk_bf16_f32 v149,v118,v119
M 128,v[24:27],184
s_add_u32 s78,s55,89133056
buffer_load_dwordx4 v[220:223],v1,s[36:39],s78 offen offset:0 sc1
v_mfma_f32_16x16x32_bf16 v[132:135],v[26:29],v[172:175],0
s_add_u32 m0,s65,16384
buffer_load_dwordx4 v[224:227],v1,s[36:39],s78 offen offset:2048 sc1
v_mfma_f32_16x16x32_bf16 v[136:139],v[24:27],v[172:175],0
D 9,56,0
M 132,v[26:29],188
D 9,56,1024
M 136,v[24:27],188
D 9,56,2048
v_mfma_f32_16x16x32_bf16 v[140:143],v[26:29],v[176:179],0
D 9,56,3072
v_mfma_f32_16x16x32_bf16 v[144:147],v[24:27],v[176:179],0
v_cvt_pk_bf16_f32 v150,v120,v121
v_cvt_pk_bf16_f32 v151,v122,v123
M 140,v[26:29],192
v_cvt_pk_bf16_f32 v152,v124,v125
v_cvt_pk_bf16_f32 v153,v126,v127
M 144,v[24:27],192
v_cvt_pk_bf16_f32 v154,v128,v129
v_cvt_pk_bf16_f32 v155,v130,v131
v_mfma_f32_16x16x32_bf16 v[36:39],v[32:35],v[148:151],0
v_cvt_pk_bf16_f32 v156,v132,v133
v_cvt_pk_bf16_f32 v157,v134,v135
v_mfma_f32_16x16x32_bf16 v[40:43],v[148:151],v[148:151],0
v_cvt_pk_bf16_f32 v158,v136,v137
v_cvt_pk_bf16_f32 v159,v138,v139
M 36,v[32:35],152
v_cvt_pk_bf16_f32 v160,v140,v141
v_cvt_pk_bf16_f32 v161,v142,v143
M 40,v[152:155],152
v_cvt_pk_bf16_f32 v162,v144,v145
v_cvt_pk_bf16_f32 v163,v146,v147
M 36,v[32:35],156
v_lshlrev_b32 v84,16,v68
v_and_b32 v85,0xffff0000,v68
M 40,v[156:159],156
v_lshlrev_b32 v86,16,v69
v_and_b32 v87,0xffff0000,v69
M 36,v[32:35],160
v_lshlrev_b32 v88,16,v70
v_and_b32 v89,0xffff0000,v70
M 40,v[160:163],160
v_lshlrev_b32 v90,16,v71
v_and_b32 v91,0xffff0000,v71
v_lshlrev_b32 v92,16,v72
v_and_b32 v93,0xffff0000,v72
v_lshlrev_b32 v94,16,v73
v_and_b32 v95,0xffff0000,v73
v_lshlrev_b32 v96,16,v74
v_and_b32 v97,0xffff0000,v74
v_lshlrev_b32 v98,16,v75
v_and_b32 v99,0xffff0000,v75
v_lshlrev_b32 v100,16,v76
v_and_b32 v101,0xffff0000,v76
v_lshlrev_b32 v102,16,v77
v_and_b32 v103,0xffff0000,v77
v_lshlrev_b32 v104,16,v78
v_and_b32 v105,0xffff0000,v78
v_lshlrev_b32 v106,16,v79
v_and_b32 v107,0xffff0000,v79
v_lshlrev_b32 v108,16,v80
v_and_b32 v109,0xffff0000,v80
v_lshlrev_b32 v110,16,v81
v_and_b32 v111,0xffff0000,v81
v_lshlrev_b32 v112,16,v82
v_and_b32 v113,0xffff0000,v82
v_lshlrev_b32 v114,16,v83
v_and_b32 v115,0xffff0000,v83
v_add_f32_dpp v44,v36,v36 row_ror:8 row_mask:0xf bank_mask:0xf
v_cndmask_b32 v45,v40,v41,s[70:71]
v_cndmask_b32 v46,v42,v43,s[70:71]
ds_write_b32 v2,v44 offset:0
v_cndmask_b32 v45,v45,v46,s[72:73]
ds_write_b32 v0,v45 offset:0
L 0
s_barrier
R 52,3,0
R 56,3,16
R 60,3,32
L 0
v_pk_add_f32 v[52:53],v[52:53],v[54:55]
v_pk_add_f32 v[56:57],v[56:57],v[58:59]
v_pk_add_f32 v[60:61],v[60:61],v[62:63]
v_add_f32 v52,v52,v53
v_pk_add_f32 v[56:57],v[56:57],v[60:61]
v_mul_f32 v52,s68,v52
v_add_f32 v53,v56,v57
v_mul_f32 v53,s68,v53
v_fma_f32 v53,-v52,v52,v53
v_max_f32 v53,0,v53
buffer_load_dword v14,v6,s[36:39],s62 offen offset:0 sc1
v_add_f32 v53,s69,v53
v_rsq_f32 v48,v53
s_nop 0
v_mul_f32_e64 v50,-v52,v48
v_mov_b32 v49,v48
v_mov_b32 v51,v50
v_pk_fma_f32 v[52:53],v[116:117],v[48:49],v[50:51]
v_pk_fma_f32 v[54:55],v[118:119],v[48:49],v[50:51]
v_pk_fma_f32 v[56:57],v[120:121],v[48:49],v[50:51]
v_pk_fma_f32 v[58:59],v[122:123],v[48:49],v[50:51]
v_pk_fma_f32 v[116:117],v[52:53],v[84:85],v[116:117]
v_pk_fma_f32 v[118:119],v[54:55],v[86:87],v[118:119]
v_pk_fma_f32 v[120:121],v[56:57],v[88:89],v[120:121]
v_pk_fma_f32 v[122:123],v[58:59],v[90:91],v[122:123]
v_cvt_pk_bf16_f32 v148,v116,v117
v_cvt_pk_bf16_f32 v149,v118,v119
v_cvt_pk_bf16_f32 v150,v120,v121
v_cvt_pk_bf16_f32 v151,v122,v123
v_pk_fma_f32 v[52:53],v[124:125],v[48:49],v[50:51]
v_pk_fma_f32 v[54:55],v[126:127],v[48:49],v[50:51]
v_pk_fma_f32 v[56:57],v[128:129],v[48:49],v[50:51]
v_pk_fma_f32 v[58:59],v[130:131],v[48:49],v[50:51]
v_pk_fma_f32 v[124:125],v[52:53],v[92:93],v[124:125]
v_pk_fma_f32 v[126:127],v[54:55],v[94:95],v[126:127]
v_pk_fma_f32 v[128:129],v[56:57],v[96:97],v[128:129]
v_pk_fma_f32 v[130:131],v[58:59],v[98:99],v[130:131]
v_cvt_pk_bf16_f32 v152,v124,v125
v_cvt_pk_bf16_f32 v153,v126,v127
v_cvt_pk_bf16_f32 v154,v128,v129
v_cvt_pk_bf16_f32 v155,v130,v131
v_pk_fma_f32 v[52:53],v[132:133],v[48:49],v[50:51]
v_pk_fma_f32 v[54:55],v[134:135],v[48:49],v[50:51]
v_pk_fma_f32 v[56:57],v[136:137],v[48:49],v[50:51]
v_pk_fma_f32 v[58:59],v[138:139],v[48:49],v[50:51]
v_pk_fma_f32 v[132:133],v[52:53],v[100:101],v[132:133]
v_pk_fma_f32 v[134:135],v[54:55],v[102:103],v[134:135]
v_pk_fma_f32 v[136:137],v[56:57],v[104:105],v[136:137]
v_pk_fma_f32 v[138:139],v[58:59],v[106:107],v[138:139]
v_cvt_pk_bf16_f32 v156,v132,v133
v_cvt_pk_bf16_f32 v157,v134,v135
v_cvt_pk_bf16_f32 v158,v136,v137
v_cvt_pk_bf16_f32 v159,v138,v139
v_pk_fma_f32 v[52:53],v[140:141],v[48:49],v[50:51]
v_pk_fma_f32 v[54:55],v[142:143],v[48:49],v[50:51]
v_pk_fma_f32 v[56:57],v[144:145],v[48:49],v[50:51]
v_pk_fma_f32 v[58:59],v[146:147],v[48:49],v[50:51]
v_pk_fma_f32 v[140:141],v[52:53],v[108:109],v[140:141]
v_pk_fma_f32 v[142:143],v[54:55],v[110:111],v[142:143]
v_pk_fma_f32 v[144:145],v[56:57],v[112:113],v[144:145]
v_pk_fma_f32 v[146:147],v[58:59],v[114:115],v[146:147]
v_cvt_pk_bf16_f32 v160,v140,v141
v_cvt_pk_bf16_f32 v161,v142,v143
v_cvt_pk_bf16_f32 v162,v144,v145
v_cvt_pk_bf16_f32 v163,v146,v147
s_waitcnt vmcnt(13)
buffer_store_dword v13,v5,s[36:39],s63 offen offset:0 sc0 sc1
s_mov_b32 s63,0x7fff0000
v_mfma_f32_16x16x32_bf16 v[36:39],v[32:35],v[148:151],0
buffer_store_dwordx4 v[148:151],v1,s[36:39],s57 offen offset:0 sc0 sc1
v_mfma_f32_16x16x32_bf16 v[40:43],v[148:151],v[148:151],0
M 36,v[32:35],152
buffer_store_dwordx4 v[152:155],v1,s[36:39],s57 offen offset:2048 sc0 sc1
M 40,v[152:155],152
M 36,v[32:35],156
buffer_store_dwordx4 v[156:159],v1,s[36:39],s58 offen offset:0 sc0 sc1
M 40,v[156:159],156
M 36,v[32:35],160
buffer_store_dwordx4 v[160:163],v1,s[36:39],s58 offen offset:2048 sc0 sc1
M 40,v[160:163],160
s_nop 5
v_add_f32_dpp v44,v36,v36 row_ror:8 row_mask:0xf bank_mask:0xf
s_nop 0
v_cndmask_b32 v45,v40,v41,s[70:71]
v_cndmask_b32 v46,v42,v43,s[70:71]
ds_write_b32 v2,v44 offset:1024
v_cndmask_b32 v45,v45,v46,s[72:73]
ds_write_b32 v0,v45 offset:1024
L 0
s_barrier
R 52,3,1024
R 56,3,1040
R 60,3,1056
L 0
v_pk_add_f32 v[52:53],v[52:53],v[54:55]
v_pk_add_f32 v[56:57],v[56:57],v[58:59]
v_pk_add_f32 v[60:61],v[60:61],v[62:63]
v_add_f32 v52,v52,v53
v_pk_add_f32 v[56:57],v[56:57],v[60:61]
v_mul_f32 v52,s68,v52
v_add_f32 v53,v56,v57
v_mul_f32 v53,s68,v53
v_fma_f32 v53,-v52,v52,v53
v_max_f32 v53,0,v53
v_add_f32 v54,s69,v53
v_fma_f32 v55,s69,v54,v53
v_rsq_f32 v65,v54
v_rsq_f32 v54,v55
v_mul_f32_e64 v64,-v52,v65
v_mul_f32 v63,v55,v54
v_mul_f32 v61,s76,v54
v_mul_f32 v55,v54,v54
v_mul_f32 v62,s77,v55
v_mul_f32 v62,v62,v61
v_cndmask_b32 v60,v52,v64,s[74:75]
v_cndmask_b32 v61,v61,v65,s[74:75]
buffer_store_dwordx4 v[60:63],v4,s[36:39],s59 offen offset:0 sc0 sc1
s_mov_b32 s63,s60
s_lshr_b32 s78,s45,1
s_and_b32 s79,s45,1
s_lshl_b32 s79,s79,7
s_lshl_b32 s57,s78,15
s_add_u32 s57,s57,s79
s_add_u32 s57,s57,12582912
s_add_u32 s58,s57,4096
s_lshl_b32 s59,s78,9
s_add_u32 s59,s59,s79
s_add_u32 s59,s59,107020288
s_lshl_b32 s60,s45,9
s_add_u32 s60,s60,s66
s_add_u32 s60,s60,131072
s_add_u32 s45,s45,s46
s_lshr_b32 s78,s45,1
s_and_b32 s79,s45,1
s_lshl_b32 s79,s79,7
s_lshl_b32 s55,s78,15
s_add_u32 s55,s55,s79
s_mul_i32 s56,s45,16384
s_add_u32 s56,s56,98566144
s_lshl_b32 s61,s78,11
s_add_u32 s61,s61,110587904
s_add_u32 s78,s45,s46
s_lshr_b32 s78,s78,1
s_lshl_b32 s62,s78,11
s_add_u32 s62,s62,110587904
s_waitcnt vmcnt(6)
s_branch Lr_it1_%=
Lr_tb0_%=:
v_add_f32 v53,s69,v53
v_rsq_f32 v48,v53
s_nop 0
v_mul_f32_e64 v50,-v52,v48
v_mov_b32 v49,v48
v_mov_b32 v51,v50
v_pk_fma_f32 v[52:53],v[116:117],v[48:49],v[50:51]
v_pk_fma_f32 v[54:55],v[118:119],v[48:49],v[50:51]
v_pk_fma_f32 v[56:57],v[120:121],v[48:49],v[50:51]
v_pk_fma_f32 v[58:59],v[122:123],v[48:49],v[50:51]
s_add_u32 s78,s55,71303168
buffer_load_dwordx4 v[196:199],v1,s[36:39],s78 offen offset:0 sc1
buffer_load_dwordx4 v[200:203],v1,s[36:39],s78 offen offset:2048 sc1
s_add_u32 s78,s55,71307264
buffer_load_dwordx4 v[204:207],v1,s[36:39],s78 offen offset:0 sc1
buffer_load_dwordx4 v[208:211],v1,s[36:39],s78 offen offset:2048 sc1
v_pk_fma_f32 v[116:117],v[52:53],v[84:85],v[116:117]
v_pk_fma_f32 v[118:119],v[54:55],v[86:87],v[118:119]
v_pk_fma_f32 v[120:121],v[56:57],v[88:89],v[120:121]
v_pk_fma_f32 v[122:123],v[58:59],v[90:91],v[122:123]
v_cvt_pk_bf16_f32 v148,v116,v117
v_cvt_pk_bf16_f32 v149,v118,v119
v_cvt_pk_bf16_f32 v150,v120,v121
v_cvt_pk_bf16_f32 v151,v122,v123
v_pk_fma_f32 v[52:53],v[124:125],v[48:49],v[50:51]
v_pk_fma_f32 v[54:55],v[126:127],v[48:49],v[50:51]
v_pk_fma_f32 v[56:57],v[128:129],v[48:49],v[50:51]
v_pk_fma_f32 v[58:59],v[130:131],v[48:49],v[50:51]
s_add_u32 s78,s55,89128960
buffer_load_dwordx4 v[212:215],v1,s[36:39],s78 offen offset:0 sc1
buffer_load_dwordx4 v[216:219],v1,s[36:39],s78 offen offset:2048 sc1
s_add_u32 s78,s55,89133056
buffer_load_dwordx4 v[220:223],v1,s[36:39],s78 offen offset:0 sc1
buffer_load_dwordx4 v[224:227],v1,s[36:39],s78 offen offset:2048 sc1
v_pk_fma_f32 v[124:125],v[52:53],v[92:93],v[124:125]
v_pk_fma_f32 v[126:127],v[54:55],v[94:95],v[126:127]
v_pk_fma_f32 v[128:129],v[56:57],v[96:97],v[128:129]
v_pk_fma_f32 v[130:131],v[58:59],v[98:99],v[130:131]
v_cvt_pk_bf16_f32 v152,v124,v125
v_cvt_pk_bf16_f32 v153,v126,v127
v_cvt_pk_bf16_f32 v154,v128,v129
v_cvt_pk_bf16_f32 v155,v130,v131
v_pk_fma_f32 v[52:53],v[132:133],v[48:49],v[50:51]
v_pk_fma_f32 v[54:55],v[134:135],v[48:49],v[50:51]
v_pk_fma_f32 v[56:57],v[136:137],v[48:49],v[50:51]
v_pk_fma_f32 v[58:59],v[138:139],v[48:49],v[50:51]
s_add_u32 m0,s65,16384
s_nop 0
D 9,56,0
D 9,56,1024
D 9,56,2048
D 9,56,3072
v_pk_fma_f32 v[132:133],v[52:53],v[100:101],v[132:133]
v_pk_fma_f32 v[134:135],v[54:55],v[102:103],v[134:135]
v_pk_fma_f32 v[136:137],v[56:57],v[104:105],v[136:137]
v_pk_fma_f32 v[138:139],v[58:59],v[106:107],v[138:139]
v_cvt_pk_bf16_f32 v156,v132,v133
v_cvt_pk_bf16_f32 v157,v134,v135
v_cvt_pk_bf16_f32 v158,v136,v137
v_cvt_pk_bf16_f32 v159,v138,v139
v_pk_fma_f32 v[52:53],v[140:141],v[48:49],v[50:51]
v_pk_fma_f32 v[54:55],v[142:143],v[48:49],v[50:51]
v_pk_fma_f32 v[56:57],v[144:145],v[48:49],v[50:51]
v_pk_fma_f32 v[58:59],v[146:147],v[48:49],v[50:51]
v_pk_fma_f32 v[140:141],v[52:53],v[108:109],v[140:141]
v_pk_fma_f32 v[142:143],v[54:55],v[110:111],v[142:143]
v_pk_fma_f32 v[144:145],v[56:57],v[112:113],v[144:145]
v_pk_fma_f32 v[146:147],v[58:59],v[114:115],v[146:147]
v_cvt_pk_bf16_f32 v160,v140,v141
v_cvt_pk_bf16_f32 v161,v142,v143
v_cvt_pk_bf16_f32 v162,v144,v145
v_cvt_pk_bf16_f32 v163,v146,v147
v_mfma_f32_16x16x32_bf16 v[36:39],v[32:35],v[148:151],0
buffer_load_dword v14,v6,s[36:39],s62 offen offset:0 sc1
buffer_store_dwordx4 v[148:151],v1,s[36:39],s57 offen offset:0 sc0 sc1
v_mfma_f32_16x16x32_bf16 v[40:43],v[148:151],v[148:151],0
M 36,v[32:35],152
buffer_store_dwordx4 v[152:155],v1,s[36:39],s57 offen offset:2048 sc0 sc1
M 40,v[152:155],152
M 36,v[32:35],156
buffer_store_dwordx4 v[156:159],v1,s[36:39],s58 offen offset:0 sc0 sc1
M 40,v[156:159],156
M 36,v[32:35],160
buffer_store_dwordx4 v[160:163],v1,s[36:39],s58 offen offset:2048 sc0 sc1
M 40,v[160:163],160
s_nop 5
v_add_f32_dpp v44,v36,v36 row_ror:8 row_mask:0xf bank_mask:0xf
s_nop 0
v_cndmask_b32 v45,v40,v41,s[70:71]
v_cndmask_b32 v46,v42,v43,s[70:71]
ds_write_b32 v2,v44 offset:1024
v_cndmask_b32 v45,v45,v46,s[72:73]
ds_write_b32 v0,v45 offset:1024
L 0
s_barrier
R 52,3,1024
R 56,3,1040
R 60,3,1056
L 0
v_pk_add_f32 v[52:53],v[52:53],v[54:55]
v_pk_add_f32 v[56:57],v[56:57],v[58:59]
v_pk_add_f32 v[60:61],v[60:61],v[62:63]
v_add_f32 v52,v52,v53
v_pk_add_f32 v[56:57],v[56:57],v[60:61]
v_mul_f32 v52,s68,v52
v_add_f32 v53,v56,v57
v_mul_f32 v53,s68,v53
v_fma_f32 v53,-v52,v52,v53
v_max_f32 v53,0,v53
v_add_f32 v54,s69,v53
v_fma_f32 v55,s69,v54,v53
v_rsq_f32 v65,v54
v_rsq_f32 v54,v55
v_mul_f32_e64 v64,-v52,v65
v_mul_f32 v63,v55,v54
v_mul_f32 v61,s76,v54
v_mul_f32 v55,v54,v54
v_mul_f32 v62,s77,v55
v_mul_f32 v62,v62,v61
v_cndmask_b32 v60,v52,v64,s[74:75]
v_cndmask_b32 v61,v61,v65,s[74:75]
buffer_store_dwordx4 v[60:63],v4,s[36:39],s59 offen offset:0 sc0 sc1
s_mov_b32 s63,s60
s_lshr_b32 s78,s45,1
s_and_b32 s79,s45,1
s_lshl_b32 s79,s79,7
s_lshl_b32 s57,s78,15
s_add_u32 s57,s57,s79
s_add_u32 s57,s57,12582912
s_add_u32 s58,s57,4096
s_lshl_b32 s59,s78,9
s_add_u32 s59,s59,s79
s_add_u32 s59,s59,107020288
s_lshl_b32 s60,s45,9
s_add_u32 s60,s60,s66
s_add_u32 s60,s60,131072
s_add_u32 s45,s45,s46
s_lshr_b32 s78,s45,1
s_and_b32 s79,s45,1
s_lshl_b32 s79,s79,7
s_lshl_b32 s55,s78,15
s_add_u32 s55,s55,s79
s_mul_i32 s56,s45,16384
s_add_u32 s56,s56,98566144
s_lshl_b32 s61,s78,11
s_add_u32 s61,s61,110587904
s_add_u32 s78,s45,s46
s_lshr_b32 s78,s78,1
s_lshl_b32 s62,s78,11
s_add_u32 s62,s62,110587904
s_waitcnt vmcnt(5)
s_branch Lr_it1_%=
Lr_tc0_%=:
s_add_u32 s78,s55,71303168
buffer_load_dwordx4 v[196:199],v1,s[36:39],s78 offen offset:0 sc1
buffer_load_dwordx4 v[200:203],v1,s[36:39],s78 offen offset:2048 sc1
s_add_u32 s78,s55,71307264
buffer_load_dwordx4 v[204:207],v1,s[36:39],s78 offen offset:0 sc1
buffer_load_dwordx4 v[208:211],v1,s[36:39],s78 offen offset:2048 sc1
s_add_u32 s78,s55,89128960
buffer_load_dwordx4 v[212:215],v1,s[36:39],s78 offen offset:0 sc1
buffer_load_dwordx4 v[216:219],v1,s[36:39],s78 offen offset:2048 sc1
s_add_u32 s78,s55,89133056
buffer_load_dwordx4 v[220:223],v1,s[36:39],s78 offen offset:0 sc1
buffer_load_dwordx4 v[224:227],v1,s[36:39],s78 offen offset:2048 sc1
s_add_u32 m0,s65,16384
s_nop 0
D 9,56,0
D 9,56,1024
D 9,56,2048
D 9,56,3072
buffer_load_dword v14,v6,s[36:39],s62 offen offset:0 sc1
s_waitcnt vmcnt(13)
buffer_store_dword v13,v5,s[36:39],s63 offen offset:0 sc0 sc1
s_mov_b32 s63,0x7fff0000
s_lshr_b32 s78,s45,1
s_and_b32 s79,s45,1
s_lshl_b32 s79,s79,7
s_lshl_b32 s57,s78,15
s_add_u32 s57,s57,s79
s_add_u32 s57,s57,12582912
s_add_u32 s58,s57,4096
s_lshl_b32 s59,s78,9
s_add_u32 s59,s59,s79
s_add_u32 s59,s59,107020288
s_lshl_b32 s60,s45,9
s_add_u32 s60,s60,s66
s_add_u32 s60,s60,131072
s_add_u32 s45,s45,s46
s_lshr_b32 s78,s45,1
s_and_b32 s79,s45,1
s_lshl_b32 s79,s79,7
s_lshl_b32 s55,s78,15
s_add_u32 s55,s55,s79
s_mul_i32 s56,s45,16384
s_add_u32 s56,s56,98566144
s_lshl_b32 s61,s78,11
s_add_u32 s61,s61,110587904
s_add_u32 s78,s45,s46
s_lshr_b32 s78,s78,1
s_lshl_b32 s62,s78,11
s_add_u32 s62,s62,110587904
s_waitcnt vmcnt(1)
s_branch Lr_it1_%=
Lr_it1_%=:
T v[68:69],10,16384
T v[70:71],10,16448
T v[72:73],10,17408
T v[74:75],10,17472
T v[76:77],10,18432
T v[78:79],10,18496
T v[80:81],10,19456
T v[82:83],10,19520
s_nop 0
v_cmp_eq_u32 vcc,s53,v14
s_nop 0
s_cmp_eq_u64 vcc,-1
s_cselect_b32 s78,s45,256
s_cmp_lt_u32 s78,256
s_cbranch_scc1 Lr_ta1_%=
buffer_load_dword v14,v6,s[36:39],s61 offen offset:0 sc1
v_mfma_f32_16x16x32_bf16 v[116:119],v[26:29],v[196:199],0
L 6
v_lshlrev_b32 v84,16,v68
v_and_b32 v85,0xffff0000,v68
v_mfma_f32_16x16x32_bf16 v[120:123],v[24:27],v[196:199],0
L 4
v_lshlrev_b32 v86,16,v69
v_and_b32 v87,0xffff0000,v69
M 116,v[26:29],212
L 2
v_lshlrev_b32 v88,16,v70
v_and_b32 v89,0xffff0000,v70
M 120,v[24:27],212
L 0
v_lshlrev_b32 v90,16,v71
v_and_b32 v91,0xffff0000,v71
v_mfma_f32_16x16x32_bf16 v[124:127],v[26:29],v[200:203],0
v_lshlrev_b32 v92,16,v72
v_and_b32 v93,0xffff0000,v72
v_mfma_f32_16x16x32_bf16 v[128:131],v[24:27],v[200:203],0
v_lshlrev_b32 v94,16,v73
v_and_b32 v95,0xffff0000,v73
M 124,v[26:29],216
v_cvt_pk_bf16_f32 v148,v116,v117
v_cvt_pk_bf16_f32 v149,v118,v119
M 128,v[24:27],216
v_cvt_pk_bf16_f32 v150,v120,v121
v_cvt_pk_bf16_f32 v151,v122,v123
v_mfma_f32_16x16x32_bf16 v[132:135],v[26:29],v[204:207],0
v_lshlrev_b32 v96,16,v74
v_and_b32 v97,0xffff0000,v74
)"
R"(v_mfma_f32_16x16x32_bf16 v[136:139],v[24:27],v[204:207],0
v_lshlrev_b32 v98,16,v75
v_and_b32 v99,0xffff0000,v75
M 132,v[26:29],220
v_cvt_pk_bf16_f32 v152,v124,v125
v_cvt_pk_bf16_f32 v153,v126,v127
M 136,v[24:27],220
v_cvt_pk_bf16_f32 v154,v128,v129
v_cvt_pk_bf16_f32 v155,v130,v131
v_mfma_f32_16x16x32_bf16 v[140:143],v[26:29],v[208:211],0
v_lshlrev_b32 v100,16,v76
v_and_b32 v101,0xffff0000,v76
v_mfma_f32_16x16x32_bf16 v[144:147],v[24:27],v[208:211],0
v_lshlrev_b32 v102,16,v77
v_and_b32 v103,0xffff0000,v77
M 140,v[26:29],224
v_cvt_pk_bf16_f32 v156,v132,v133
v_cvt_pk_bf16_f32 v157,v134,v135
M 144,v[24:27],224
v_cvt_pk_bf16_f32 v158,v136,v137
v_cvt_pk_bf16_f32 v159,v138,v139
v_mfma_f32_16x16x32_bf16 v[36:39],v[32:35],v[148:151],0
v_lshlrev_b32 v104,16,v78
v_and_b32 v105,0xffff0000,v78
v_mfma_f32_16x16x32_bf16 v[40:43],v[148:151],v[148:151],0
v_lshlrev_b32 v106,16,v79
v_and_b32 v107,0xffff0000,v79
M 36,v[32:35],152
v_cvt_pk_bf16_f32 v160,v140,v141
v_cvt_pk_bf16_f32 v161,v142,v143
M 40,v[152:155],152
v_cvt_pk_bf16_f32 v162,v144,v145
v_cvt_pk_bf16_f32 v163,v146,v147
M 36,v[32:35],156
v_lshlrev_b32 v108,16,v80
v_and_b32 v109,0xffff0000,v80
M 40,v[156:159],156
v_lshlrev_b32 v110,16,v81
v_and_b32 v111,0xffff0000,v81
M 36,v[32:35],160
v_lshlrev_b32 v112,16,v82
v_and_b32 v113,0xffff0000,v82
M 40,v[160:163],160
v_lshlrev_b32 v114,16,v83
v_and_b32 v115,0xffff0000,v83
s_nop 2
v_add_f32_dpp v44,v36,v36 row_ror:8 row_mask:0xf bank_mask:0xf
s_nop 1
v_cndmask_b32 v45,v40,v41,s[70:71]
v_cndmask_b32 v46,v42,v43,s[70:71]
ds_write_b32 v2,v44 offset:0
v_cndmask_b32 v45,v45,v46,s[72:73]
ds_write_b32 v0,v45 offset:0
L 0
s_barrier
R 52,3,0
R 56,3,16
R 60,3,32
L 0
v_pk_add_f32 v[52:53],v[52:53],v[54:55]
v_pk_add_f32 v[56:57],v[56:57],v[58:59]
v_pk_add_f32 v[60:61],v[60:61],v[62:63]
v_add_f32 v52,v52,v53
v_pk_add_f32 v[56:57],v[56:57],v[60:61]
v_mul_f32 v52,s68,v52
v_add_f32 v53,v56,v57
v_mul_f32 v53,s68,v53
v_fma_f32 v53,-v52,v52,v53
v_max_f32 v53,0,v53
s_waitcnt vmcnt(1)
buffer_store_dword v13,v5,s[36:39],s63 offen offset:0 sc0 sc1
s_mov_b32 s63,0x7fff0000
s_waitcnt vmcnt(1)
v_cmp_eq_u32 vcc,s53,v14
s_nop 0
s_cmp_eq_u64 vcc,-1
s_cselect_b32 s78,s45,256
s_cmp_lt_u32 s78,256
s_cbranch_scc1 Lr_tb1_%=
buffer_load_dword v14,v6,s[36:39],s61 offen offset:0 sc1
v_add_f32 v53,s69,v53
v_rsq_f32 v48,v53
s_nop 0
v_mul_f32_e64 v50,-v52,v48
v_mov_b32 v49,v48
v_mov_b32 v51,v50
v_pk_fma_f32 v[52:53],v[116:117],v[48:49],v[50:51]
v_pk_fma_f32 v[54:55],v[118:119],v[48:49],v[50:51]
v_pk_fma_f32 v[56:57],v[120:121],v[48:49],v[50:51]
v_pk_fma_f32 v[58:59],v[122:123],v[48:49],v[50:51]
v_pk_fma_f32 v[116:117],v[52:53],v[84:85],v[116:117]
v_pk_fma_f32 v[118:119],v[54:55],v[86:87],v[118:119]
v_pk_fma_f32 v[120:121],v[56:57],v[88:89],v[120:121]
v_pk_fma_f32 v[122:123],v[58:59],v[90:91],v[122:123]
v_cvt_pk_bf16_f32 v148,v116,v117
v_cvt_pk_bf16_f32 v149,v118,v119
v_cvt_pk_bf16_f32 v150,v120,v121
v_cvt_pk_bf16_f32 v151,v122,v123
v_pk_fma_f32 v[52:53],v[124:125],v[48:49],v[50:51]
v_pk_fma_f32 v[54:55],v[126:127],v[48:49],v[50:51]
v_pk_fma_f32 v[56:57],v[128:129],v[48:49],v[50:51]
v_pk_fma_f32 v[58:59],v[130:131],v[48:49],v[50:51]
v_pk_fma_f32 v[124:125],v[52:53],v[92:93],v[124:125]
v_pk_fma_f32 v[126:127],v[54:55],v[94:95],v[126:127]
v_pk_fma_f32 v[128:129],v[56:57],v[96:97],v[128:129]
v_pk_fma_f32 v[130:131],v[58:59],v[98:99],v[130:131]
v_cvt_pk_bf16_f32 v152,v124,v125
v_cvt_pk_bf16_f32 v153,v126,v127
v_cvt_pk_bf16_f32 v154,v128,v129
v_cvt_pk_bf16_f32 v155,v130,v131
v_pk_fma_f32 v[52:53],v[132:133],v[48:49],v[50:51]
v_pk_fma_f32 v[54:55],v[134:135],v[48:49],v[50:51]
v_pk_fma_f32 v[56:57],v[136:137],v[48:49],v[50:51]
v_pk_fma_f32 v[58:59],v[138:139],v[48:49],v[50:51]
v_pk_fma_f32 v[132:133],v[52:53],v[100:101],v[132:133]
v_pk_fma_f32 v[134:135],v[54:55],v[102:103],v[134:135]
v_pk_fma_f32 v[136:137],v[56:57],v[104:105],v[136:137]
v_pk_fma_f32 v[138:139],v[58:59],v[106:107],v[138:139]
v_cvt_pk_bf16_f32 v156,v132,v133
v_cvt_pk_bf16_f32 v157,v134,v135
v_cvt_pk_bf16_f32 v158,v136,v137
v_cvt_pk_bf16_f32 v159,v138,v139
v_pk_fma_f32 v[52:53],v[140:141],v[48:49],v[50:51]
v_pk_fma_f32 v[54:55],v[142:143],v[48:49],v[50:51]
v_pk_fma_f32 v[56:57],v[144:145],v[48:49],v[50:51]
v_pk_fma_f32 v[58:59],v[146:147],v[48:49],v[50:51]
v_pk_fma_f32 v[140:141],v[52:53],v[108:109],v[140:141]
v_pk_fma_f32 v[142:143],v[54:55],v[110:111],v[142:143]
v_pk_fma_f32 v[144:145],v[56:57],v[112:113],v[144:145]
v_pk_fma_f32 v[146:147],v[58:59],v[114:115],v[146:147]
v_cvt_pk_bf16_f32 v160,v140,v141
v_cvt_pk_bf16_f32 v161,v142,v143
v_cvt_pk_bf16_f32 v162,v144,v145
v_cvt_pk_bf16_f32 v163,v146,v147
v_mfma_f32_16x16x32_bf16 v[36:39],v[32:35],v[148:151],0
buffer_store_dwordx4 v[148:151],v1,s[36:39],s57 offen offset:0 sc0 sc1
v_mfma_f32_16x16x32_bf16 v[40:43],v[148:151],v[148:151],0
M 36,v[32:35],152
buffer_store_dwordx4 v[152:155],v1,s[36:39],s57 offen offset:2048 sc0 sc1
M 40,v[152:155],152
M 36,v[32:35],156
buffer_store_dwordx4 v[156:159],v1,s[36:39],s58 offen offset:0 sc0 sc1
M 40,v[156:159],156
M 36,v[32:35],160
buffer_store_dwordx4 v[160:163],v1,s[36:39],s58 offen offset:2048 sc0 sc1
M 40,v[160:163],160
s_nop 5
v_add_f32_dpp v44,v36,v36 row_ror:8 row_mask:0xf bank_mask:0xf
s_nop 0
v_cndmask_b32 v45,v40,v41,s[70:71]
v_cndmask_b32 v46,v42,v43,s[70:71]
ds_write_b32 v2,v44 offset:1024
v_cndmask_b32 v45,v45,v46,s[72:73]
ds_write_b32 v0,v45 offset:1024
L 0
s_barrier
R 52,3,1024
R 56,3,1040
R 60,3,1056
L 0
v_pk_add_f32 v[52:53],v[52:53],v[54:55]
v_pk_add_f32 v[56:57],v[56:57],v[58:59]
v_pk_add_f32 v[60:61],v[60:61],v[62:63]
v_add_f32 v52,v52,v53
v_pk_add_f32 v[56:57],v[56:57],v[60:61]
v_mul_f32 v52,s68,v52
v_add_f32 v53,v56,v57
v_mul_f32 v53,s68,v53
v_fma_f32 v53,-v52,v52,v53
v_max_f32 v53,0,v53
v_add_f32 v54,s69,v53
v_fma_f32 v55,s69,v54,v53
v_rsq_f32 v65,v54
v_rsq_f32 v54,v55
v_mul_f32_e64 v64,-v52,v65
v_mul_f32 v63,v55,v54
v_mul_f32 v61,s76,v54
v_mul_f32 v55,v54,v54
v_mul_f32 v62,s77,v55
v_mul_f32 v62,v62,v61
v_cndmask_b32 v60,v52,v64,s[74:75]
v_cndmask_b32 v61,v61,v65,s[74:75]
buffer_store_dwordx4 v[60:63],v4,s[36:39],s59 offen offset:0 sc0 sc1
s_mov_b32 s63,s60
s_waitcnt vmcnt(5)
v_cmp_eq_u32 vcc,s53,v14
s_nop 0
s_cmp_eq_u64 vcc,-1
s_cselect_b32 s78,s45,256
s_cmp_lt_u32 s78,256
s_cbranch_scc1 Lr_tc1_%=
s_waitcnt vmcnt(0)
buffer_store_dword v13,v5,s[36:39],s63 offen offset:0 sc0 sc1
s_mov_b32 s63,0x7fff0000
s_cmp_ge_u32 s45,256
s_cbranch_scc1 Lr_done_%=
Lr_bp1_%=:
buffer_load_dword v14,v6,s[36:39],s61 offen offset:0 sc1
s_add_u32 s82,s82,1
s_waitcnt vmcnt(0)
v_cmp_eq_u32 vcc,s53,v14
s_nop 0
s_cmp_eq_u64 vcc,-1
s_cbranch_scc1 Lr_bq1_%=
s_sleep 2
s_cmp_lt_u32 s82,20000
s_cbranch_scc1 Lr_bp1_%=
Lr_bq1_%=:
s_add_u32 s78,s55,71303168
buffer_load_dwordx4 v[164:167],v1,s[36:39],s78 offen offset:0 sc1
buffer_load_dwordx4 v[168:171],v1,s[36:39],s78 offen offset:2048 sc1
s_add_u32 s78,s55,71307264
buffer_load_dwordx4 v[172:175],v1,s[36:39],s78 offen offset:0 sc1
buffer_load_dwordx4 v[176:179],v1,s[36:39],s78 offen offset:2048 sc1
s_add_u32 s78,s55,89128960
buffer_load_dwordx4 v[180:183],v1,s[36:39],s78 offen offset:0 sc1
buffer_load_dwordx4 v[184:187],v1,s[36:39],s78 offen offset:2048 sc1
s_add_u32 s78,s55,89133056
buffer_load_dwordx4 v[188:191],v1,s[36:39],s78 offen offset:0 sc1
buffer_load_dwordx4 v[192:195],v1,s[36:39],s78 offen offset:2048 sc1
s_add_u32 m0,s65,0
s_nop 0
D 9,56,0
D 9,56,1024
D 9,56,2048
D 9,56,3072
buffer_load_dword v14,v6,s[36:39],s62 offen offset:0 sc1
s_lshr_b32 s78,s45,1
s_and_b32 s79,s45,1
s_lshl_b32 s79,s79,7
s_lshl_b32 s57,s78,15
s_add_u32 s57,s57,s79
s_add_u32 s57,s57,12582912
s_add_u32 s58,s57,4096
s_lshl_b32 s59,s78,9
s_add_u32 s59,s59,s79
s_add_u32 s59,s59,107020288
s_lshl_b32 s60,s45,9
s_add_u32 s60,s60,s66
s_add_u32 s60,s60,131072
s_add_u32 s45,s45,s46
s_lshr_b32 s78,s45,1
s_and_b32 s79,s45,1
s_lshl_b32 s79,s79,7
s_lshl_b32 s55,s78,15
s_add_u32 s55,s55,s79
s_mul_i32 s56,s45,16384
s_add_u32 s56,s56,98566144
s_lshl_b32 s61,s78,11
s_add_u32 s61,s61,110587904
s_add_u32 s78,s45,s46
s_lshr_b32 s78,s78,1
s_lshl_b32 s62,s78,11
s_add_u32 s62,s62,110587904
s_waitcnt vmcnt(0)
s_branch Lr_it0_%=
Lr_ta1_%=:
v_mfma_f32_16x16x32_bf16 v[116:119],v[26:29],v[196:199],0
s_add_u32 s78,s55,71303168
buffer_load_dwordx4 v[164:167],v1,s[36:39],s78 offen offset:0 sc1
v_mfma_f32_16x16x32_bf16 v[120:123],v[24:27],v[196:199],0
L 6
buffer_load_dwordx4 v[168:171],v1,s[36:39],s78 offen offset:2048 sc1
M 116,v[26:29],212
s_add_u32 s78,s55,71307264
buffer_load_dwordx4 v[172:175],v1,s[36:39],s78 offen offset:0 sc1
M 120,v[24:27],212
L 4
buffer_load_dwordx4 v[176:179],v1,s[36:39],s78 offen offset:2048 sc1
v_mfma_f32_16x16x32_bf16 v[124:127],v[26:29],v[200:203],0
s_add_u32 s78,s55,89128960
buffer_load_dwordx4 v[180:183],v1,s[36:39],s78 offen offset:0 sc1
v_mfma_f32_16x16x32_bf16 v[128:131],v[24:27],v[200:203],0
L 2
buffer_load_dwordx4 v[184:187],v1,s[36:39],s78 offen offset:2048 sc1
M 124,v[26:29],216
L 0
v_cvt_pk_bf16_f32 v148,v116,v117
v_cvt_pk_bf16_f32 v149,v118,v119
M 128,v[24:27],216
s_add_u32 s78,s55,89133056
buffer_load_dwordx4 v[188:191],v1,s[36:39],s78 offen offset:0 sc1
v_mfma_f32_16x16x32_bf16 v[132:135],v[26:29],v[204:207],0
s_add_u32 m0,s65,0
buffer_load_dwordx4 v[192:195],v1,s[36:39],s78 offen offset:2048 sc1
v_mfma_f32_16x16x32_bf16 v[136:139],v[24:27],v[204:207],0
D 9,56,0
M 132,v[26:29],220
D 9,56,1024
M 136,v[24:27],220
D 9,56,2048
v_mfma_f32_16x16x32_bf16 v[140:143],v[26:29],v[208:211],0
D 9,56,3072
v_mfma_f32_16x16x32_bf16 v[144:147],v[24:27],v[208:211],0
v_cvt_pk_bf16_f32 v150,v120,v121
v_cvt_pk_bf16_f32 v151,v122,v123
M 140,v[26:29],224
v_cvt_pk_bf16_f32 v152,v124,v125
v_cvt_pk_bf16_f32 v153,v126,v127
M 144,v[24:27],224
v_cvt_pk_bf16_f32 v154,v128,v129
v_cvt_pk_bf16_f32 v155,v130,v131
v_mfma_f32_16x16x32_bf16 v[36:39],v[32:35],v[148:151],0
v_cvt_pk_bf16_f32 v156,v132,v133
v_cvt_pk_bf16_f32 v157,v134,v135
v_mfma_f32_16x16x32_bf16 v[40:43],v[148:151],v[148:151],0
v_cvt_pk_bf16_f32 v158,v136,v137
v_cvt_pk_bf16_f32 v159,v138,v139
M 36,v[32:35],152
v_cvt_pk_bf16_f32 v160,v140,v141
v_cvt_pk_bf16_f32 v161,v142,v143
M 40,v[152:155],152
v_cvt_pk_bf16_f32 v162,v144,v145
v_cvt_pk_bf16_f32 v163,v146,v147
M 36,v[32:35],156
v_lshlrev_b32 v84,16,v68
v_and_b32 v85,0xffff0000,v68
M 40,v[156:159],156
v_lshlrev_b32 v86,16,v69
v_and_b32 v87,0xffff0000,v69
M 36,v[32:35],160
v_lshlrev_b32 v88,16,v70
v_and_b32 v89,0xffff0000,v70
M 40,v[160:163],160
v_lshlrev_b32 v90,16,v71
v_and_b32 v91,0xffff0000,v71
v_lshlrev_b32 v92,16,v72
v_and_b32 v93,0xffff0000,v72
v_lshlrev_b32 v94,16,v73
v_and_b32 v95,0xffff0000,v73
v_lshlrev_b32 v96,16,v74
v_and_b32 v97,0xffff0000,v74
v_lshlrev_b32 v98,16,v75
v_and_b32 v99,0xffff0000,v75
v_lshlrev_b32 v100,16,v76
v_and_b32 v101,0xffff0000,v76
v_lshlrev_b32 v102,16,v77
v_and_b32 v103,0xffff0000,v77
v_lshlrev_b32 v104,16,v78
v_and_b32 v105,0xffff0000,v78
v_lshlrev_b32 v106,16,v79
v_and_b32 v107,0xffff0000,v79
v_lshlrev_b32 v108,16,v80
v_and_b32 v109,0xffff0000,v80
v_lshlrev_b32 v110,16,v81
v_and_b32 v111,0xffff0000,v81
v_lshlrev_b32 v112,16,v82
v_and_b32 v113,0xffff0000,v82
v_lshlrev_b32 v114,16,v83
v_and_b32 v115,0xffff0000,v83
v_add_f32_dpp v44,v36,v36 row_ror:8 row_mask:0xf bank_mask:0xf
v_cndmask_b32 v45,v40,v41,s[70:71]
v_cndmask_b32 v46,v42,v43,s[70:71]
ds_write_b32 v2,v44 offset:0
v_cndmask_b32 v45,v45,v46,s[72:73]
ds_write_b32 v0,v45 offset:0
L 0
s_barrier
R 52,3,0
R 56,3,16
R 60,3,32
L 0
v_pk_add_f32 v[52:53],v[52:53],v[54:55]
v_pk_add_f32 v[56:57],v[56:57],v[58:59]
v_pk_add_f32 v[60:61],v[60:61],v[62:63]
v_add_f32 v52,v52,v53
v_pk_add_f32 v[56:57],v[56:57],v[60:61]
v_mul_f32 v52,s68,v52
v_add_f32 v53,v56,v57
v_mul_f32 v53,s68,v53
v_fma_f32 v53,-v52,v52,v53
v_max_f32 v53,0,v53
buffer_load_dword v14,v6,s[36:39],s62 offen offset:0 sc1
v_add_f32 v53,s69,v53
v_rsq_f32 v48,v53
s_nop 0
v_mul_f32_e64 v50,-v52,v48
v_mov_b32 v49,v48
v_mov_b32 v51,v50
v_pk_fma_f32 v[52:53],v[116:117],v[48:49],v[50:51]
v_pk_fma_f32 v[54:55],v[118:119],v[48:49],v[50:51]
v_pk_fma_f32 v[56:57],v[120:121],v[48:49],v[50:51]
v_pk_fma_f32 v[58:59],v[122:123],v[48:49],v[50:51]
v_pk_fma_f32 v[116:117],v[52:53],v[84:85],v[116:117]
v_pk_fma_f32 v[118:119],v[54:55],v[86:87],v[118:119]
v_pk_fma_f32 v[120:121],v[56:57],v[88:89],v[120:121]
v_pk_fma_f32 v[122:123],v[58:59],v[90:91],v[122:123]
v_cvt_pk_bf16_f32 v148,v116,v117
v_cvt_pk_bf16_f32 v149,v118,v119
v_cvt_pk_bf16_f32 v150,v120,v121
v_cvt_pk_bf16_f32 v151,v122,v123
v_pk_fma_f32 v[52:53],v[124:125],v[48:49],v[50:51]
v_pk_fma_f32 v[54:55],v[126:127],v[48:49],v[50:51]
v_pk_fma_f32 v[56:57],v[128:129],v[48:49],v[50:51]
v_pk_fma_f32 v[58:59],v[130:131],v[48:49],v[50:51]
v_pk_fma_f32 v[124:125],v[52:53],v[92:93],v[124:125]
v_pk_fma_f32 v[126:127],v[54:55],v[94:95],v[126:127]
v_pk_fma_f32 v[128:129],v[56:57],v[96:97],v[128:129]
v_pk_fma_f32 v[130:131],v[58:59],v[98:99],v[130:131]
v_cvt_pk_bf16_f32 v152,v124,v125
v_cvt_pk_bf16_f32 v153,v126,v127
v_cvt_pk_bf16_f32 v154,v128,v129
v_cvt_pk_bf16_f32 v155,v130,v131
v_pk_fma_f32 v[52:53],v[132:133],v[48:49],v[50:51]
v_pk_fma_f32 v[54:55],v[134:135],v[48:49],v[50:51]
v_pk_fma_f32 v[56:57],v[136:137],v[48:49],v[50:51]
v_pk_fma_f32 v[58:59],v[138:139],v[48:49],v[50:51]
v_pk_fma_f32 v[132:133],v[52:53],v[100:101],v[132:133]
v_pk_fma_f32 v[134:135],v[54:55],v[102:103],v[134:135]
v_pk_fma_f32 v[136:137],v[56:57],v[104:105],v[136:137]
v_pk_fma_f32 v[138:139],v[58:59],v[106:107],v[138:139]
v_cvt_pk_bf16_f32 v156,v132,v133
v_cvt_pk_bf16_f32 v157,v134,v135
v_cvt_pk_bf16_f32 v158,v136,v137
v_cvt_pk_bf16_f32 v159,v138,v139
v_pk_fma_f32 v[52:53],v[140:141],v[48:49],v[50:51]
v_pk_fma_f32 v[54:55],v[142:143],v[48:49],v[50:51]
v_pk_fma_f32 v[56:57],v[144:145],v[48:49],v[50:51]
v_pk_fma_f32 v[58:59],v[146:147],v[48:49],v[50:51]
v_pk_fma_f32 v[140:141],v[52:53],v[108:109],v[140:141]
v_pk_fma_f32 v[142:143],v[54:55],v[110:111],v[142:143]
v_pk_fma_f32 v[144:145],v[56:57],v[112:113],v[144:145]
v_pk_fma_f32 v[146:147],v[58:59],v[114:115],v[146:147]
v_cvt_pk_bf16_f32 v160,v140,v141
v_cvt_pk_bf16_f32 v161,v142,v143
v_cvt_pk_bf16_f32 v162,v144,v145
v_cvt_pk_bf16_f32 v163,v146,v147
s_waitcnt vmcnt(13)
buffer_store_dword v13,v5,s[36:39],s63 offen offset:0 sc0 sc1
s_mov_b32 s63,0x7fff0000
v_mfma_f32_16x16x32_bf16 v[36:39],v[32:35],v[148:151],0
buffer_store_dwordx4 v[148:151],v1,s[36:39],s57 offen offset:0 sc0 sc1
v_mfma_f32_16x16x32_bf16 v[40:43],v[148:151],v[148:151],0
M 36,v[32:35],152
buffer_store_dwordx4 v[152:155],v1,s[36:39],s57 offen offset:2048 sc0 sc1
M 40,v[152:155],152
M 36,v[32:35],156
buffer_store_dwordx4 v[156:159],v1,s[36:39],s58 offen offset:0 sc0 sc1
M 40,v[156:159],156
M 36,v[32:35],160
buffer_store_dwordx4 v[160:163],v1,s[36:39],s58 offen offset:2048 sc0 sc1
M 40,v[160:163],160
s_nop 5
v_add_f32_dpp v44,v36,v36 row_ror:8 row_mask:0xf bank_mask:0xf
s_nop 0
v_cndmask_b32 v45,v40,v41,s[70:71]
v_cndmask_b32 v46,v42,v43,s[70:71]
ds_write_b32 v2,v44 offset:1024
v_cndmask_b32 v45,v45,v46,s[72:73]
ds_write_b32 v0,v45 offset:1024
L 0
s_barrier
R 52,3,1024
R 56,3,1040
R 60,3,1056
L 0
v_pk_add_f32 v[52:53],v[52:53],v[54:55]
v_pk_add_f32 v[56:57],v[56:57],v[58:59]
v_pk_add_f32 v[60:61],v[60:61],v[62:63]
v_add_f32 v52,v52,v53
v_pk_add_f32 v[56:57],v[56:57],v[60:61]
v_mul_f32 v52,s68,v52
v_add_f32 v53,v56,v57
v_mul_f32 v53,s68,v53
v_fma_f32 v53,-v52,v52,v53
v_max_f32 v53,0,v53
v_add_f32 v54,s69,v53
v_fma_f32 v55,s69,v54,v53
v_rsq_f32 v65,v54
v_rsq_f32 v54,v55
v_mul_f32_e64 v64,-v52,v65
v_mul_f32 v63,v55,v54
v_mul_f32 v61,s76,v54
v_mul_f32 v55,v54,v54
v_mul_f32 v62,s77,v55
v_mul_f32 v62,v62,v61
v_cndmask_b32 v60,v52,v64,s[74:75]
v_cndmask_b32 v61,v61,v65,s[74:75]
buffer_store_dwordx4 v[60:63],v4,s[36:39],s59 offen offset:0 sc0 sc1
s_mov_b32 s63,s60
s_lshr_b32 s78,s45,1
s_and_b32 s79,s45,1
s_lshl_b32 s79,s79,7
s_lshl_b32 s57,s78,15
s_add_u32 s57,s57,s79
s_add_u32 s57,s57,12582912
s_add_u32 s58,s57,4096
s_lshl_b32 s59,s78,9
s_add_u32 s59,s59,s79
s_add_u32 s59,s59,107020288
s_lshl_b32 s60,s45,9
s_add_u32 s60,s60,s66
s_add_u32 s60,s60,131072
s_add_u32 s45,s45,s46
s_lshr_b32 s78,s45,1
s_and_b32 s79,s45,1
s_lshl_b32 s79,s79,7
s_lshl_b32 s55,s78,15
s_add_u32 s55,s55,s79
s_mul_i32 s56,s45,16384
)"
R"(s_add_u32 s56,s56,98566144
s_lshl_b32 s61,s78,11
s_add_u32 s61,s61,110587904
s_add_u32 s78,s45,s46
s_lshr_b32 s78,s78,1
s_lshl_b32 s62,s78,11
s_add_u32 s62,s62,110587904
s_waitcnt vmcnt(6)
s_branch Lr_it0_%=
Lr_tb1_%=:
v_add_f32 v53,s69,v53
v_rsq_f32 v48,v53
s_nop 0
v_mul_f32_e64 v50,-v52,v48
v_mov_b32 v49,v48
v_mov_b32 v51,v50
v_pk_fma_f32 v[52:53],v[116:117],v[48:49],v[50:51]
v_pk_fma_f32 v[54:55],v[118:119],v[48:49],v[50:51]
v_pk_fma_f32 v[56:57],v[120:121],v[48:49],v[50:51]
v_pk_fma_f32 v[58:59],v[122:123],v[48:49],v[50:51]
s_add_u32 s78,s55,71303168
buffer_load_dwordx4 v[164:167],v1,s[36:39],s78 offen offset:0 sc1
buffer_load_dwordx4 v[168:171],v1,s[36:39],s78 offen offset:2048 sc1
s_add_u32 s78,s55,71307264
buffer_load_dwordx4 v[172:175],v1,s[36:39],s78 offen offset:0 sc1
buffer_load_dwordx4 v[176:179],v1,s[36:39],s78 offen offset:2048 sc1
v_pk_fma_f32 v[116:117],v[52:53],v[84:85],v[116:117]
v_pk_fma_f32 v[118:119],v[54:55],v[86:87],v[118:119]
v_pk_fma_f32 v[120:121],v[56:57],v[88:89],v[120:121]
v_pk_fma_f32 v[122:123],v[58:59],v[90:91],v[122:123]
v_cvt_pk_bf16_f32 v148,v116,v117
v_cvt_pk_bf16_f32 v149,v118,v119
v_cvt_pk_bf16_f32 v150,v120,v121
v_cvt_pk_bf16_f32 v151,v122,v123
v_pk_fma_f32 v[52:53],v[124:125],v[48:49],v[50:51]
v_pk_fma_f32 v[54:55],v[126:127],v[48:49],v[50:51]
v_pk_fma_f32 v[56:57],v[128:129],v[48:49],v[50:51]
v_pk_fma_f32 v[58:59],v[130:131],v[48:49],v[50:51]
s_add_u32 s78,s55,89128960
buffer_load_dwordx4 v[180:183],v1,s[36:39],s78 offen offset:0 sc1
buffer_load_dwordx4 v[184:187],v1,s[36:39],s78 offen offset:2048 sc1
s_add_u32 s78,s55,89133056
buffer_load_dwordx4 v[188:191],v1,s[36:39],s78 offen offset:0 sc1
buffer_load_dwordx4 v[192:195],v1,s[36:39],s78 offen offset:2048 sc1
v_pk_fma_f32 v[124:125],v[52:53],v[92:93],v[124:125]
v_pk_fma_f32 v[126:127],v[54:55],v[94:95],v[126:127]
v_pk_fma_f32 v[128:129],v[56:57],v[96:97],v[128:129]
v_pk_fma_f32 v[130:131],v[58:59],v[98:99],v[130:131]
v_cvt_pk_bf16_f32 v152,v124,v125
v_cvt_pk_bf16_f32 v153,v126,v127
v_cvt_pk_bf16_f32 v154,v128,v129
v_cvt_pk_bf16_f32 v155,v130,v131
v_pk_fma_f32 v[52:53],v[132:133],v[48:49],v[50:51]
v_pk_fma_f32 v[54:55],v[134:135],v[48:49],v[50:51]
v_pk_fma_f32 v[56:57],v[136:137],v[48:49],v[50:51]
v_pk_fma_f32 v[58:59],v[138:139],v[48:49],v[50:51]
s_add_u32 m0,s65,0
s_nop 0
D 9,56,0
D 9,56,1024
D 9,56,2048
D 9,56,3072
v_pk_fma_f32 v[132:133],v[52:53],v[100:101],v[132:133]
v_pk_fma_f32 v[134:135],v[54:55],v[102:103],v[134:135]
v_pk_fma_f32 v[136:137],v[56:57],v[104:105],v[136:137]
v_pk_fma_f32 v[138:139],v[58:59],v[106:107],v[138:139]
v_cvt_pk_bf16_f32 v156,v132,v133
v_cvt_pk_bf16_f32 v157,v134,v135
v_cvt_pk_bf16_f32 v158,v136,v137
v_cvt_pk_bf16_f32 v159,v138,v139
v_pk_fma_f32 v[52:53],v[140:141],v[48:49],v[50:51]
v_pk_fma_f32 v[54:55],v[142:143],v[48:49],v[50:51]
v_pk_fma_f32 v[56:57],v[144:145],v[48:49],v[50:51]
v_pk_fma_f32 v[58:59],v[146:147],v[48:49],v[50:51]
v_pk_fma_f32 v[140:141],v[52:53],v[108:109],v[140:141]
v_pk_fma_f32 v[142:143],v[54:55],v[110:111],v[142:143]
v_pk_fma_f32 v[144:145],v[56:57],v[112:113],v[144:145]
v_pk_fma_f32 v[146:147],v[58:59],v[114:115],v[146:147]
v_cvt_pk_bf16_f32 v160,v140,v141
v_cvt_pk_bf16_f32 v161,v142,v143
v_cvt_pk_bf16_f32 v162,v144,v145
v_cvt_pk_bf16_f32 v163,v146,v147
v_mfma_f32_16x16x32_bf16 v[36:39],v[32:35],v[148:151],0
buffer_load_dword v14,v6,s[36:39],s62 offen offset:0 sc1
buffer_store_dwordx4 v[148:151],v1,s[36:39],s57 offen offset:0 sc0 sc1
v_mfma_f32_16x16x32_bf16 v[40:43],v[148:151],v[148:151],0
M 36,v[32:35],152
buffer_store_dwordx4 v[152:155],v1,s[36:39],s57 offen offset:2048 sc0 sc1
M 40,v[152:155],152
M 36,v[32:35],156
buffer_store_dwordx4 v[156:159],v1,s[36:39],s58 offen offset:0 sc0 sc1
M 40,v[156:159],156
M 36,v[32:35],160
buffer_store_dwordx4 v[160:163],v1,s[36:39],s58 offen offset:2048 sc0 sc1
M 40,v[160:163],160
s_nop 5
v_add_f32_dpp v44,v36,v36 row_ror:8 row_mask:0xf bank_mask:0xf
s_nop 0
v_cndmask_b32 v45,v40,v41,s[70:71]
v_cndmask_b32 v46,v42,v43,s[70:71]
ds_write_b32 v2,v44 offset:1024
v_cndmask_b32 v45,v45,v46,s[72:73]
ds_write_b32 v0,v45 offset:1024
L 0
s_barrier
R 52,3,1024
R 56,3,1040
R 60,3,1056
L 0
v_pk_add_f32 v[52:53],v[52:53],v[54:55]
v_pk_add_f32 v[56:57],v[56:57],v[58:59]
v_pk_add_f32 v[60:61],v[60:61],v[62:63]
v_add_f32 v52,v52,v53
v_pk_add_f32 v[56:57],v[56:57],v[60:61]
v_mul_f32 v52,s68,v52
v_add_f32 v53,v56,v57
v_mul_f32 v53,s68,v53
v_fma_f32 v53,-v52,v52,v53
v_max_f32 v53,0,v53
v_add_f32 v54,s69,v53
v_fma_f32 v55,s69,v54,v53
v_rsq_f32 v65,v54
v_rsq_f32 v54,v55
v_mul_f32_e64 v64,-v52,v65
v_mul_f32 v63,v55,v54
v_mul_f32 v61,s76,v54
v_mul_f32 v55,v54,v54
v_mul_f32 v62,s77,v55
v_mul_f32 v62,v62,v61
v_cndmask_b32 v60,v52,v64,s[74:75]
v_cndmask_b32 v61,v61,v65,s[74:75]
buffer_store_dwordx4 v[60:63],v4,s[36:39],s59 offen offset:0 sc0 sc1
s_mov_b32 s63,s60
s_lshr_b32 s78,s45,1
s_and_b32 s79,s45,1
s_lshl_b32 s79,s79,7
s_lshl_b32 s57,s78,15
s_add_u32 s57,s57,s79
s_add_u32 s57,s57,12582912
s_add_u32 s58,s57,4096
s_lshl_b32 s59,s78,9
s_add_u32 s59,s59,s79
s_add_u32 s59,s59,107020288
s_lshl_b32 s60,s45,9
s_add_u32 s60,s60,s66
s_add_u32 s60,s60,131072
s_add_u32 s45,s45,s46
s_lshr_b32 s78,s45,1
s_and_b32 s79,s45,1
s_lshl_b32 s79,s79,7
s_lshl_b32 s55,s78,15
s_add_u32 s55,s55,s79
s_mul_i32 s56,s45,16384
s_add_u32 s56,s56,98566144
s_lshl_b32 s61,s78,11
s_add_u32 s61,s61,110587904
s_add_u32 s78,s45,s46
s_lshr_b32 s78,s78,1
s_lshl_b32 s62,s78,11
s_add_u32 s62,s62,110587904
s_waitcnt vmcnt(5)
s_branch Lr_it0_%=
Lr_tc1_%=:
s_add_u32 s78,s55,71303168
buffer_load_dwordx4 v[164:167],v1,s[36:39],s78 offen offset:0 sc1
buffer_load_dwordx4 v[168:171],v1,s[36:39],s78 offen offset:2048 sc1
s_add_u32 s78,s55,71307264
buffer_load_dwordx4 v[172:175],v1,s[36:39],s78 offen offset:0 sc1
buffer_load_dwordx4 v[176:179],v1,s[36:39],s78 offen offset:2048 sc1
s_add_u32 s78,s55,89128960
buffer_load_dwordx4 v[180:183],v1,s[36:39],s78 offen offset:0 sc1
buffer_load_dwordx4 v[184:187],v1,s[36:39],s78 offen offset:2048 sc1
s_add_u32 s78,s55,89133056
buffer_load_dwordx4 v[188:191],v1,s[36:39],s78 offen offset:0 sc1
buffer_load_dwordx4 v[192:195],v1,s[36:39],s78 offen offset:2048 sc1
s_add_u32 m0,s65,0
s_nop 0
D 9,56,0
D 9,56,1024
D 9,56,2048
D 9,56,3072
buffer_load_dword v14,v6,s[36:39],s62 offen offset:0 sc1
s_waitcnt vmcnt(13)
buffer_store_dword v13,v5,s[36:39],s63 offen offset:0 sc0 sc1
s_mov_b32 s63,0x7fff0000
s_lshr_b32 s78,s45,1
s_and_b32 s79,s45,1
s_lshl_b32 s79,s79,7
s_lshl_b32 s57,s78,15
s_add_u32 s57,s57,s79
s_add_u32 s57,s57,12582912
s_add_u32 s58,s57,4096
s_lshl_b32 s59,s78,9
s_add_u32 s59,s59,s79
s_add_u32 s59,s59,107020288
s_lshl_b32 s60,s45,9
s_add_u32 s60,s60,s66
s_add_u32 s60,s60,131072
s_add_u32 s45,s45,s46
s_lshr_b32 s78,s45,1
s_and_b32 s79,s45,1
s_lshl_b32 s79,s79,7
s_lshl_b32 s55,s78,15
s_add_u32 s55,s55,s79
s_mul_i32 s56,s45,16384
s_add_u32 s56,s56,98566144
s_lshl_b32 s61,s78,11
s_add_u32 s61,s61,110587904
s_add_u32 s78,s45,s46
s_lshr_b32 s78,s78,1
s_lshl_b32 s62,s78,11
s_add_u32 s62,s62,110587904
s_waitcnt vmcnt(1)
s_branch Lr_it0_%=
Lr_done_%=:
s_waitcnt vmcnt(0) lgkmcnt(0)
s_barrier
Lcode_r_e:
.purgem M
.purgem R
.purgem T
.purgem D
.purgem L
)"
  :: "s"(ws), "s"(sWave), "s"(sG0), "s"(sStep), "s"(sOutOff), "s"(sAuxOff), "s"(sBuf), "s"(sRk), "s"(sExp), "v"(vRP), "v"(vRF), "v"(vRwr), "v"(vRrd), "v"(vRS), "v"(vFlag), "v"(vL4), "v"(vLY), "v"(vQ), "v"(vY0), "v"(vTr), "v"(vSa), "v"(vSb), "s"(q) : "v0", "v1", "v2", "v3", "v4", "v5", "v6", "v7", "v8", "v9", "v10", "v11", "v12", "v13", "v14", "v15", "v16", "v17", "v18", "v19", "v20", "v21", "v22", "v23", "v24", "v25", "v26", "v27", "v28", "v29", "v30", "v31", "v32", "v33", "v34", "v35", "v36", "v37", "v38", "v39", "v40", "v41", "v42", "v43", "v44", "v45", "v46", "v47", "v48", "v49", "v50", "v51", "v52", "v53", "v54", "v55", "v56", "v57", "v58", "v59", "v60", "v61", "v62", "v63", "v64", "v65", "v66", "v67", "v68", "v69", "v70", "v71", "v72", "v73", "v74", "v75", "v76", "v77", "v78", "v79", "v80", "v81", "v82", "v83", "v84", "v85", "v86", "v87", "v88", "v89", "v90", "v91", "v92", "v93", "v94", "v95", "v96", "v97", "v98", "v99", "v100", "v101", "v102", "v103", "v104", "v105", "v106", "v107", "v108", "v109", "v110", "v111", "v112", "v113", "v114", "v115", "v116", "v117", "v118", "v119", "v120", "v121", "v122", "v123", "v124", "v125", "v126", "v127", "v128", "v129", "v130", "v131", "v132", "v133", "v134", "v135", "v136", "v137", "v138", "v139", "v140", "v141", "v142", "v143", "v144", "v145", "v146", "v147", "v148", "v149", "v150", "v151", "v152", "v153", "v154", "v155", "v156", "v157", "v158", "v159", "v160", "v161", "v162", "v163", "v164", "v165", "v166", "v167", "v168", "v169", "v170", "v171", "v172", "v173", "v174", "v175", "v176", "v177", "v178", "v179", "v180", "v181", "v182", "v183", "v184", "v185", "v186", "v187", "v188", "v189", "v190", "v191", "v192", "v193", "v194", "v195", "v196", "v197", "v198", "v199", "v200", "v201", "v202", "v203", "v204", "v205", "v206", "v207", "v208", "v209", "v210", "v211", "v212", "v213", "v214", "v215", "v216", "v217", "v218", "v219", "v220", "v221", "v222", "v223", "v224", "v225", "v226", "v227", "v228", "v229", "v230", "v231", "s36", "s37", "s38", "s39", "s40", "s41", "s42", "s43", "s44", "s45", "s46", "s47", "s48", "s49", "s50", "s51", "s52", "s53", "s54", "s55", "s56", "s57", "s58", "s59", "s60", "s61", "s62", "s63", "s64", "s65", "s66", "s67", "s68", "s69", "s70", "s71", "s72", "s73", "s74", "s75", "s76", "s77", "s78", "s79", "s80", "s81", "s82", "s83", "s84", "s85", "s86", "s87", "s88", "s89", "s90", "s91", "s92", "s93", "s94", "s95", "s96", "s97", "vcc", "scc", "memory");
  }
  CTS(9);
#define COMMON(NF, FOFF, FSTR, SNAP, LDW, NL, TOFF)                                                                                            \
  LANE_ID(ln);                                                                                                                       \
  const int l15 = ln & 15, lg = ln >> 4, lq = l15 >> 2, lp = ln & 3, rr = 8 * lg + lq, fj = ln & 31;                                 \
  const unsigned vL16 = ln * 16, vFlag = (ln < 32 && wave == 0) ? ln * 4 : OOB;                                                      \
  const unsigned vSnapOff = (wave == 0 && fj < (NF)) ? (FOFF) + (ln >> 5) * sStep * (FSTR) : OOB;                                    \
  const unsigned vSnapRd = (SNAP) + (fj < (NF) ? ln : ln & 32) * 4;                                                                  \
  const unsigned vWd0 = (ln >> 3) * (LDW) + ((ln & 7) ^ swzB(ln >> 3)) * 16, vWd1 = ((ln >> 3) + 8) * (LDW) + ((ln & 7) ^ swzB(8 + (ln >> 3))) * 16; \
  const unsigned vWr0 = (wave >> 1) * 8192 + rr * 128 + (((wave & 1) * 4 + lp) ^ swzB(rr)) * 16, vWr1 = vWr0 + 8;                    \
  const unsigned vWr2 = 16384 + rr * 128 + ((2 * wave + (lp >> 1)) ^ swzB(rr)) * 16 + (lp & 1) * 8;                                  \
  const unsigned sWave = wave, sRk = t_start, sG0 = rep, sDbg = sDbg0
  if (kind == 3 || kind == 5) {
    const unsigned sStep = kind == 3 ? 5 : G3OR;
    COMMON(8, fj * 128, 1024, 131072, 7168, 256, 0);
    const unsigned vOutS = (lg & 1) ? OOB : (((wave & 1) * 2 + (lg >> 1)) * 16 + l15) * 16 + (4 + (wave >> 1) - wave) * 1024;
    const unsigned vAux0 = (ln & 7) * 128 + (4 * wave + lg) * 8 + ((ln >> 3) & 1) * 4, vAux1 = 139264 + (l15 == 0 ? (4 * wave + lg) * 16 : 512 + ln * 16);
    const unsigned vAux2 = 139264 + l15 * 16, vAux3 = 0, vAn = 0, vAe = 0;
    const unsigned sOutOff = OFF_G + (slice * 6 + wave) * 1024, sFoutOff = OFF_FG + slice * 128, sAuxOff = 0, sWw = 16 * wave * 7168 + slice * 384, sBuf = 0;
    if (kind == 3) { asm volatile(
R"(.macro M d,w,x
v_mfma_f32_16x16x32_bf16 v[\d:\d+3], \w, v[\x:\x+3], v[\d:\d+3]
.endm
.macro R d,a,o
ds_read_b128 v[\d:\d+3], v\a offset:\o
.endm
.macro T d,a,o
ds_read_b64_tr_b16 \d, v\a offset:\o
.endm
.macro D a,s,o
buffer_load_dwordx4 v\a, s[36:39], s\s offen offset:\o sc1 lds
.endm
.macro L c
s_waitcnt lgkmcnt(\c)
.endm
Lcode_g3_b:
s_mov_b64 s[36:37],%0
s_mov_b32 s38,111243392
s_mov_b32 s39,0x00020000
s_mov_b32 s44,%1
s_mov_b32 s45,%2
s_mov_b32 s46,%3
s_mov_b32 s47,%4
s_mov_b32 s48,%5
s_mov_b32 s49,%6
s_mov_b32 s50,%7
s_mov_b32 s51,%8
s_mov_b32 s93,%9
v_mov_b32 v0,%10
v_mov_b32 v3,%11
v_mov_b32 v5,%12
v_mov_b32 v6,%13
v_mov_b32 v18,%14
v_mov_b32 v19,%15
v_mov_b32 v20,%16
v_mov_b32 v21,%17
v_mov_b32 v22,%18
v_mov_b32 v10,%19
v_mov_b32 v11,%20
v_mov_b32 v12,%21
v_mov_b32 v13,%22
v_mov_b32 v14,%23
v_mov_b32 v15,%24
v_mov_b32 v16,%25
s_mov_b64 s[40:41],%26
s_mov_b32 s42,7340032
s_mov_b32 s43,0x00020000
s_lshl_b32 s57,s44,11
s_mov_b32 s79,111243264
buffer_load_dword v4,off,s[36:39],s79 sc1
s_add_u32 s55,s50,0
s_add_u32 m0,s57,0
s_nop 0
buffer_load_dwordx4 v18,s[40:43],s55 offen offset:0  lds
s_add_u32 m0,s57,1024
s_nop 0
buffer_load_dwordx4 v19,s[40:43],s55 offen offset:0  lds
s_add_u32 m0,s57,8064
s_nop 0
buffer_load_dwordx4 v18,s[40:43],s55 offen offset:128  lds
s_add_u32 m0,s57,9088
s_nop 0
buffer_load_dwordx4 v19,s[40:43],s55 offen offset:128  lds
s_add_u32 m0,s57,16128
s_nop 0
buffer_load_dwordx4 v18,s[40:43],s55 offen offset:256  lds
s_add_u32 m0,s57,17152
s_nop 0
buffer_load_dwordx4 v19,s[40:43],s55 offen offset:256  lds
s_add_u32 s55,s50,458752
s_add_u32 m0,s57,24576
s_nop 0
buffer_load_dwordx4 v18,s[40:43],s55 offen offset:0  lds
s_add_u32 m0,s57,25600
s_nop 0
buffer_load_dwordx4 v19,s[40:43],s55 offen offset:0  lds
s_add_u32 m0,s57,32640
s_nop 0
buffer_load_dwordx4 v18,s[40:43],s55 offen offset:128  lds
s_add_u32 m0,s57,33664
s_nop 0
buffer_load_dwordx4 v19,s[40:43],s55 offen offset:128  lds
s_add_u32 m0,s57,40704
s_nop 0
buffer_load_dwordx4 v18,s[40:43],s55 offen offset:256  lds
s_add_u32 m0,s57,41728
s_nop 0
buffer_load_dwordx4 v19,s[40:43],s55 offen offset:256  lds
s_add_u32 s55,s50,917504
s_add_u32 m0,s57,49152
s_nop 0
buffer_load_dwordx4 v18,s[40:43],s55 offen offset:0  lds
s_add_u32 m0,s57,50176
s_nop 0
buffer_load_dwordx4 v19,s[40:43],s55 offen offset:0  lds
s_add_u32 m0,s57,57216
s_nop 0
buffer_load_dwordx4 v18,s[40:43],s55 offen offset:128  lds
s_add_u32 m0,s57,58240
s_nop 0
buffer_load_dwordx4 v19,s[40:43],s55 offen offset:128  lds
s_add_u32 m0,s57,65280
s_nop 0
buffer_load_dwordx4 v18,s[40:43],s55 offen offset:256  lds
s_add_u32 m0,s57,66304
s_nop 0
buffer_load_dwordx4 v19,s[40:43],s55 offen offset:256  lds
s_add_u32 s55,s50,1376256
s_add_u32 m0,s57,73728
s_nop 0
buffer_load_dwordx4 v18,s[40:43],s55 offen offset:0  lds
s_add_u32 m0,s57,74752
s_nop 0
buffer_load_dwordx4 v19,s[40:43],s55 offen offset:0  lds
s_add_u32 m0,s57,81792
s_nop 0
buffer_load_dwordx4 v18,s[40:43],s55 offen offset:128  lds
s_add_u32 m0,s57,82816
s_nop 0
buffer_load_dwordx4 v19,s[40:43],s55 offen offset:128  lds
s_add_u32 m0,s57,89856
s_nop 0
buffer_load_dwordx4 v18,s[40:43],s55 offen offset:256  lds
s_add_u32 m0,s57,90880
s_nop 0
buffer_load_dwordx4 v19,s[40:43],s55 offen offset:256  lds
s_add_u32 s55,s50,1835008
s_add_u32 m0,s57,98304
s_nop 0
buffer_load_dwordx4 v18,s[40:43],s55 offen offset:0  lds
s_add_u32 m0,s57,99328
s_nop 0
buffer_load_dwordx4 v19,s[40:43],s55 offen offset:0  lds
s_add_u32 m0,s57,106368
s_nop 0
buffer_load_dwordx4 v18,s[40:43],s55 offen offset:128  lds
s_add_u32 m0,s57,107392
s_nop 0
buffer_load_dwordx4 v19,s[40:43],s55 offen offset:128  lds
s_add_u32 m0,s57,114432
s_nop 0
buffer_load_dwordx4 v18,s[40:43],s55 offen offset:256  lds
s_add_u32 m0,s57,115456
s_nop 0
buffer_load_dwordx4 v19,s[40:43],s55 offen offset:256  lds
s_waitcnt vmcnt(30)
s_barrier
v_readfirstlane_b32 s53,v4
s_nop 3
s_add_u32 s53,s53,1
s_cmp_eq_u32 s53,0
s_cselect_b32 s53,1,s53
v_mov_b32 v4,s53
s_waitcnt vmcnt(24) lgkmcnt(0)
s_barrier
s_add_u32 s55,s50,2293760
s_add_u32 m0,s57,122880
s_nop 0
buffer_load_dwordx4 v18,s[40:43],s55 offen offset:0  lds
s_add_u32 m0,s57,123904
s_nop 0
buffer_load_dwordx4 v19,s[40:43],s55 offen offset:0  lds
s_add_u32 m0,s57,130944
s_nop 0
buffer_load_dwordx4 v18,s[40:43],s55 offen offset:128  lds
s_add_u32 m0,s57,131968
s_nop 0
buffer_load_dwordx4 v19,s[40:43],s55 offen offset:128  lds
s_add_u32 m0,s57,139008
s_nop 0
buffer_load_dwordx4 v18,s[40:43],s55 offen offset:256  lds
s_add_u32 m0,s57,140032
s_nop 0
buffer_load_dwordx4 v19,s[40:43],s55 offen offset:256  lds
v_add_u32 v26,0,v20
v_add_u32 v27,0,v21
v_add_u32 v28,0,v22
T a[0:1],26,0
T a[2:3],26,512
T a[4:5],27,0
T a[6:7],27,512
T a[8:9],28,0
T a[10:11],28,512
T a[12:13],26,4096
T a[14:15],26,4608
T a[16:17],27,4096
T a[18:19],27,4608
T a[20:21],28,4096
T a[22:23],28,4608
s_waitcnt vmcnt(24) lgkmcnt(0)
s_barrier
s_add_u32 s55,s50,2752512
s_add_u32 m0,s57,0
s_nop 0
buffer_load_dwordx4 v18,s[40:43],s55 offen offset:0  lds
s_add_u32 m0,s57,1024
s_nop 0
buffer_load_dwordx4 v19,s[40:43],s55 offen offset:0  lds
s_add_u32 m0,s57,8064
s_nop 0
buffer_load_dwordx4 v18,s[40:43],s55 offen offset:128  lds
s_add_u32 m0,s57,9088
s_nop 0
buffer_load_dwordx4 v19,s[40:43],s55 offen offset:128  lds
s_add_u32 m0,s57,16128
s_nop 0
buffer_load_dwordx4 v18,s[40:43],s55 offen offset:256  lds
s_add_u32 m0,s57,17152
s_nop 0
buffer_load_dwordx4 v19,s[40:43],s55 offen offset:256  lds
v_add_u32 v26,24576,v20
v_add_u32 v27,24576,v21
v_add_u32 v28,24576,v22
T a[24:25],26,0
T a[26:27],26,512
T a[28:29],27,0
T a[30:31],27,512
T a[32:33],28,0
T a[34:35],28,512
T a[36:37],26,4096
T a[38:39],26,4608
T a[40:41],27,4096
T a[42:43],27,4608
T a[44:45],28,4096
T a[46:47],28,4608
s_waitcnt vmcnt(24) lgkmcnt(0)
s_barrier
s_add_u32 s55,s50,3211264
s_add_u32 m0,s57,24576
s_nop 0
buffer_load_dwordx4 v18,s[40:43],s55 offen offset:0  lds
s_add_u32 m0,s57,25600
s_nop 0
buffer_load_dwordx4 v19,s[40:43],s55 offen offset:0  lds
s_add_u32 m0,s57,32640
s_nop 0
buffer_load_dwordx4 v18,s[40:43],s55 offen offset:128  lds
s_add_u32 m0,s57,33664
s_nop 0
buffer_load_dwordx4 v19,s[40:43],s55 offen offset:128  lds
s_add_u32 m0,s57,40704
s_nop 0
buffer_load_dwordx4 v18,s[40:43],s55 offen offset:256  lds
s_add_u32 m0,s57,41728
s_nop 0
buffer_load_dwordx4 v19,s[40:43],s55 offen offset:256  lds
v_add_u32 v26,49152,v20
v_add_u32 v27,49152,v21
v_add_u32 v28,49152,v22
T a[48:49],26,0
T a[50:51],26,512
T a[52:53],27,0
T a[54:55],27,512
T a[56:57],28,0
T a[58:59],28,512
T a[60:61],26,4096
T a[62:63],26,4608
T a[64:65],27,4096
T a[66:67],27,4608
T a[68:69],28,4096
T a[70:71],28,4608
s_waitcnt vmcnt(24) lgkmcnt(0)
s_barrier
s_add_u32 s55,s50,3670016
s_add_u32 m0,s57,49152
s_nop 0
buffer_load_dwordx4 v18,s[40:43],s55 offen offset:0  lds
s_add_u32 m0,s57,50176
s_nop 0
buffer_load_dwordx4 v19,s[40:43],s55 offen offset:0  lds
s_add_u32 m0,s57,57216
s_nop 0
buffer_load_dwordx4 v18,s[40:43],s55 offen offset:128  lds
s_add_u32 m0,s57,58240
s_nop 0
buffer_load_dwordx4 v19,s[40:43],s55 offen offset:128  lds
s_add_u32 m0,s57,65280
s_nop 0
buffer_load_dwordx4 v18,s[40:43],s55 offen offset:256  lds
s_add_u32 m0,s57,66304
s_nop 0
buffer_load_dwordx4 v19,s[40:43],s55 offen offset:256  lds
v_add_u32 v26,73728,v20
v_add_u32 v27,73728,v21
v_add_u32 v28,73728,v22
T a[72:73],26,0
T a[74:75],26,512
T a[76:77],27,0
T a[78:79],27,512
T a[80:81],28,0
T a[82:83],28,512
T a[84:85],26,4096
T a[86:87],26,4608
T a[88:89],27,4096
T a[90:91],27,4608
T a[92:93],28,4096
T a[94:95],28,4608
s_waitcnt vmcnt(24) lgkmcnt(0)
s_barrier
s_add_u32 s55,s50,4128768
s_add_u32 m0,s57,73728
s_nop 0
buffer_load_dwordx4 v18,s[40:43],s55 offen offset:0  lds
s_add_u32 m0,s57,74752
s_nop 0
buffer_load_dwordx4 v19,s[40:43],s55 offen offset:0  lds
s_add_u32 m0,s57,81792
s_nop 0
buffer_load_dwordx4 v18,s[40:43],s55 offen offset:128  lds
s_add_u32 m0,s57,82816
s_nop 0
buffer_load_dwordx4 v19,s[40:43],s55 offen offset:128  lds
s_add_u32 m0,s57,89856
s_nop 0
buffer_load_dwordx4 v18,s[40:43],s55 offen offset:256  lds
s_add_u32 m0,s57,90880
s_nop 0
buffer_load_dwordx4 v19,s[40:43],s55 offen offset:256  lds
v_add_u32 v26,98304,v20
v_add_u32 v27,98304,v21
v_add_u32 v28,98304,v22
T a[96:97],26,0
T a[98:99],26,512
T a[100:101],27,0
T a[102:103],27,512
T a[104:105],28,0
T a[106:107],28,512
T a[108:109],26,4096
T a[110:111],26,4608
T a[112:113],27,4096
T a[114:115],27,4608
T a[116:117],28,4096
T a[118:119],28,4608
s_waitcnt vmcnt(24) lgkmcnt(0)
s_barrier
s_add_u32 s55,s50,4587520
s_add_u32 m0,s57,98304
s_nop 0
buffer_load_dwordx4 v18,s[40:43],s55 offen offset:0  lds
s_add_u32 m0,s57,99328
s_nop 0
buffer_load_dwordx4 v19,s[40:43],s55 offen offset:0  lds
s_add_u32 m0,s57,106368
s_nop 0
buffer_load_dwordx4 v18,s[40:43],s55 offen offset:128  lds
s_add_u32 m0,s57,107392
s_nop 0
buffer_load_dwordx4 v19,s[40:43],s55 offen offset:128  lds
s_add_u32 m0,s57,114432
s_nop 0
buffer_load_dwordx4 v18,s[40:43],s55 offen offset:256  lds
s_add_u32 m0,s57,115456
s_nop 0
buffer_load_dwordx4 v19,s[40:43],s55 offen offset:256  lds
v_add_u32 v26,122880,v20
v_add_u32 v27,122880,v21
v_add_u32 v28,122880,v22
T a[120:121],26,0
T a[122:123],26,512
T a[124:125],27,0
T a[126:127],27,512
T a[128:129],28,0
T a[130:131],28,512
T a[132:133],26,4096
T a[134:135],26,4608
T a[136:137],27,4096
T a[138:139],27,4608
T a[140:141],28,4096
T a[142:143],28,4608
s_waitcnt vmcnt(24) lgkmcnt(0)
s_barrier
s_add_u32 s55,s50,5046272
s_add_u32 m0,s57,122880
s_nop 0
buffer_load_dwordx4 v18,s[40:43],s55 offen offset:0  lds
s_add_u32 m0,s57,123904
s_nop 0
buffer_load_dwordx4 v19,s[40:43],s55 offen offset:0  lds
s_add_u32 m0,s57,130944
s_nop 0
buffer_load_dwordx4 v18,s[40:43],s55 offen offset:128  lds
s_add_u32 m0,s57,131968
s_nop 0
buffer_load_dwordx4 v19,s[40:43],s55 offen offset:128  lds
s_add_u32 m0,s57,139008
s_nop 0
buffer_load_dwordx4 v18,s[40:43],s55 offen offset:256  lds
s_add_u32 m0,s57,140032
s_nop 0
buffer_load_dwordx4 v19,s[40:43],s55 offen offset:256  lds
v_add_u32 v26,0,v20
v_add_u32 v27,0,v21
v_add_u32 v28,0,v22
T a[144:145],26,0
T a[146:147],26,512
T a[148:149],27,0
T a[150:151],27,512
T a[152:153],28,0
T a[154:155],28,512
T a[156:157],26,4096
T a[158:159],26,4608
T a[160:161],27,4096
T a[162:163],27,4608
T a[164:165],28,4096
T a[166:167],28,4608
s_waitcnt vmcnt(24) lgkmcnt(0)
s_barrier
s_add_u32 s55,s50,5505024
s_add_u32 m0,s57,0
s_nop 0
buffer_load_dwordx4 v18,s[40:43],s55 offen offset:0  lds
s_add_u32 m0,s57,1024
s_nop 0
buffer_load_dwordx4 v19,s[40:43],s55 offen offset:0  lds
s_add_u32 m0,s57,8064
s_nop 0
buffer_load_dwordx4 v18,s[40:43],s55 offen offset:128  lds
s_add_u32 m0,s57,9088
s_nop 0
buffer_load_dwordx4 v19,s[40:43],s55 offen offset:128  lds
s_add_u32 m0,s57,16128
s_nop 0
buffer_load_dwordx4 v18,s[40:43],s55 offen offset:256  lds
s_add_u32 m0,s57,17152
s_nop 0
buffer_load_dwordx4 v19,s[40:43],s55 offen offset:256  lds
v_add_u32 v26,24576,v20
v_add_u32 v27,24576,v21
v_add_u32 v28,24576,v22
T a[168:169],26,0
T a[170:171],26,512
T a[172:173],27,0
T a[174:175],27,512
T a[176:177],28,0
T a[178:179],28,512
T a[180:181],26,4096
T a[182:183],26,4608
T a[184:185],27,4096
T a[186:187],27,4608
T a[188:189],28,4096
T a[190:191],28,4608
s_waitcnt vmcnt(24) lgkmcnt(0)
s_barrier
s_add_u32 s55,s50,5963776
s_add_u32 m0,s57,24576
s_nop 0
buffer_load_dwordx4 v18,s[40:43],s55 offen offset:0  lds
s_add_u32 m0,s57,25600
s_nop 0
buffer_load_dwordx4 v19,s[40:43],s55 offen offset:0  lds
s_add_u32 m0,s57,32640
s_nop 0
buffer_load_dwordx4 v18,s[40:43],s55 offen offset:128  lds
s_add_u32 m0,s57,33664
s_nop 0
buffer_load_dwordx4 v19,s[40:43],s55 offen offset:128  lds
s_add_u32 m0,s57,40704
s_nop 0
buffer_load_dwordx4 v18,s[40:43],s55 offen offset:256  lds
s_add_u32 m0,s57,41728
s_nop 0
buffer_load_dwordx4 v19,s[40:43],s55 offen offset:256  lds
v_add_u32 v26,49152,v20
v_add_u32 v27,49152,v21
v_add_u32 v28,49152,v22
T a[192:193],26,0
T a[194:195],26,512
T a[196:197],27,0
T a[198:199],27,512
T a[200:201],28,0
T a[202:203],28,512
T a[204:205],26,4096
T a[206:207],26,4608
T a[208:209],27,4096
T a[210:211],27,4608
T a[212:213],28,4096
T a[214:215],28,4608
s_waitcnt vmcnt(24) lgkmcnt(0)
s_barrier
s_add_u32 s55,s50,6422528
s_add_u32 m0,s57,49152
s_nop 0
buffer_load_dwordx4 v18,s[40:43],s55 offen offset:0  lds
s_add_u32 m0,s57,50176
s_nop 0
buffer_load_dwordx4 v19,s[40:43],s55 offen offset:0  lds
s_add_u32 m0,s57,57216
s_nop 0
buffer_load_dwordx4 v18,s[40:43],s55 offen offset:128  lds
s_add_u32 m0,s57,58240
s_nop 0
buffer_load_dwordx4 v19,s[40:43],s55 offen offset:128  lds
s_add_u32 m0,s57,65280
s_nop 0
buffer_load_dwordx4 v18,s[40:43],s55 offen offset:256  lds
s_add_u32 m0,s57,66304
s_nop 0
buffer_load_dwordx4 v19,s[40:43],s55 offen offset:256  lds
v_add_u32 v26,73728,v20
v_add_u32 v27,73728,v21
)"
R"(v_add_u32 v28,73728,v22
T a[216:217],26,0
T a[218:219],26,512
T a[220:221],27,0
T a[222:223],27,512
T a[224:225],28,0
T a[226:227],28,512
T a[228:229],26,4096
T a[230:231],26,4608
T a[232:233],27,4096
T a[234:235],27,4608
T a[236:237],28,4096
T a[238:239],28,4608
s_waitcnt vmcnt(24) lgkmcnt(0)
s_barrier
s_add_u32 s55,s50,6881280
s_add_u32 m0,s57,73728
s_nop 0
buffer_load_dwordx4 v18,s[40:43],s55 offen offset:0  lds
s_add_u32 m0,s57,74752
s_nop 0
buffer_load_dwordx4 v19,s[40:43],s55 offen offset:0  lds
s_add_u32 m0,s57,81792
s_nop 0
buffer_load_dwordx4 v18,s[40:43],s55 offen offset:128  lds
s_add_u32 m0,s57,82816
s_nop 0
buffer_load_dwordx4 v19,s[40:43],s55 offen offset:128  lds
s_add_u32 m0,s57,89856
s_nop 0
buffer_load_dwordx4 v18,s[40:43],s55 offen offset:256  lds
s_add_u32 m0,s57,90880
s_nop 0
buffer_load_dwordx4 v19,s[40:43],s55 offen offset:256  lds
v_add_u32 v26,98304,v20
v_add_u32 v27,98304,v21
v_add_u32 v28,98304,v22
T a[240:241],26,0
T a[242:243],26,512
T a[244:245],27,0
T a[246:247],27,512
T a[248:249],28,0
T a[250:251],28,512
T a[252:253],26,4096
T a[254:255],26,4608
T v[128:129],27,4096
T v[130:131],27,4608
T v[132:133],28,4096
T v[134:135],28,4608
s_waitcnt vmcnt(24) lgkmcnt(0)
s_barrier
v_add_u32 v26,122880,v20
v_add_u32 v27,122880,v21
v_add_u32 v28,122880,v22
T v[136:137],26,0
T v[138:139],26,512
T v[140:141],27,0
T v[142:143],27,512
T v[144:145],28,0
T v[146:147],28,512
T v[148:149],26,4096
T v[150:151],26,4608
T v[152:153],27,4096
T v[154:155],27,4608
T v[156:157],28,4096
T v[158:159],28,4608
s_waitcnt vmcnt(18) lgkmcnt(0)
s_barrier
v_add_u32 v26,0,v20
v_add_u32 v27,0,v21
v_add_u32 v28,0,v22
T v[160:161],26,0
T v[162:163],26,512
T v[164:165],27,0
T v[166:167],27,512
T v[168:169],28,0
T v[170:171],28,512
T v[172:173],26,4096
T v[174:175],26,4608
T v[176:177],27,4096
T v[178:179],27,4608
T v[180:181],28,4096
T v[182:183],28,4608
s_waitcnt vmcnt(12) lgkmcnt(0)
s_barrier
v_add_u32 v26,24576,v20
v_add_u32 v27,24576,v21
v_add_u32 v28,24576,v22
T v[184:185],26,0
T v[186:187],26,512
T v[188:189],27,0
T v[190:191],27,512
T v[192:193],28,0
T v[194:195],28,512
T v[196:197],26,4096
T v[198:199],26,4608
T v[200:201],27,4096
T v[202:203],27,4608
T v[204:205],28,4096
T v[206:207],28,4608
s_waitcnt vmcnt(6) lgkmcnt(0)
s_barrier
v_add_u32 v26,49152,v20
v_add_u32 v27,49152,v21
v_add_u32 v28,49152,v22
T v[208:209],26,0
T v[210:211],26,512
T v[212:213],27,0
T v[214:215],27,512
T v[216:217],28,0
T v[218:219],28,512
T v[220:221],26,4096
T v[222:223],26,4608
T v[224:225],27,4096
T v[226:227],27,4608
T v[228:229],28,4096
T v[230:231],28,4608
s_waitcnt vmcnt(0) lgkmcnt(0)
s_barrier
v_add_u32 v26,73728,v20
v_add_u32 v27,73728,v21
v_add_u32 v28,73728,v22
T v[232:233],26,0
T v[234:235],26,512
T v[236:237],27,0
T v[238:239],27,512
T v[240:241],28,0
T v[242:243],28,512
T v[244:245],26,4096
T v[246:247],26,4608
T v[248:249],27,4096
T v[250:251],27,4608
T v[252:253],28,4096
T v[254:255],28,4608
L 0
s_barrier
v_mov_b32 v42,0x3f803f80
v_mov_b32 v43,0x3f803f80
v_mov_b32 v44,0x3f803f80
v_mov_b32 v45,0x3f803f80
s_nop 1
v_mfma_f32_16x16x32_bf16 v[82:85],a[0:3],v[42:45],0
v_mfma_f32_16x16x32_bf16 v[86:89],a[4:7],v[42:45],0
v_mfma_f32_16x16x32_bf16 v[90:93],a[8:11],v[42:45],0
M 82,a[12:15],42
M 86,a[16:19],42
M 90,a[20:23],42
M 82,a[24:27],42
M 86,a[28:31],42
M 90,a[32:35],42
M 82,a[36:39],42
M 86,a[40:43],42
M 90,a[44:47],42
M 82,a[48:51],42
M 86,a[52:55],42
M 90,a[56:59],42
M 82,a[60:63],42
M 86,a[64:67],42
M 90,a[68:71],42
M 82,a[72:75],42
M 86,a[76:79],42
M 90,a[80:83],42
M 82,a[84:87],42
M 86,a[88:91],42
M 90,a[92:95],42
M 82,a[96:99],42
M 86,a[100:103],42
M 90,a[104:107],42
M 82,a[108:111],42
M 86,a[112:115],42
M 90,a[116:119],42
M 82,a[120:123],42
M 86,a[124:127],42
M 90,a[128:131],42
M 82,a[132:135],42
M 86,a[136:139],42
M 90,a[140:143],42
M 82,a[144:147],42
M 86,a[148:151],42
M 90,a[152:155],42
M 82,a[156:159],42
M 86,a[160:163],42
M 90,a[164:167],42
M 82,a[168:171],42
M 86,a[172:175],42
M 90,a[176:179],42
M 82,a[180:183],42
M 86,a[184:187],42
M 90,a[188:191],42
M 82,a[192:195],42
M 86,a[196:199],42
M 90,a[200:203],42
M 82,a[204:207],42
M 86,a[208:211],42
M 90,a[212:215],42
M 82,a[216:219],42
M 86,a[220:223],42
M 90,a[224:227],42
M 82,a[228:231],42
M 86,a[232:235],42
M 90,a[236:239],42
M 82,a[240:243],42
M 86,a[244:247],42
M 90,a[248:251],42
M 82,a[252:255],42
M 86,v[128:131],42
M 90,v[132:135],42
M 82,v[136:139],42
M 86,v[140:143],42
M 90,v[144:147],42
M 82,v[148:151],42
M 86,v[152:155],42
M 90,v[156:159],42
M 82,v[160:163],42
M 86,v[164:167],42
M 90,v[168:171],42
M 82,v[172:175],42
M 86,v[176:179],42
M 90,v[180:183],42
M 82,v[184:187],42
M 86,v[188:191],42
M 90,v[192:195],42
M 82,v[196:199],42
M 86,v[200:203],42
M 90,v[204:207],42
M 82,v[208:211],42
M 86,v[212:215],42
M 90,v[216:219],42
M 82,v[220:223],42
M 86,v[224:227],42
M 90,v[228:231],42
M 82,v[232:235],42
M 86,v[236:239],42
M 90,v[240:243],42
M 82,v[244:247],42
M 86,v[248:251],42
M 90,v[252:255],42
v_mov_b32 v4,s53
s_mov_b32 s82,0
s_mov_b32 s80,0
s_mov_b32 s54,s45
s_mul_i32 s57,s44,4096
s_mul_i32 s55,s45,32768
s_add_u32 s55,s55,s57
s_add_u32 s55,s55,31457280
s_mul_i32 s57,s44,4096
s_mul_i32 s60,s46,32768
s_mul_i32 s61,s46,1024
s_mul_i32 s62,s46,114688
s_mul_i32 s63,s46,2560
s_mul_i32 s64,s46,1024
s_lshl_b32 s59,s44,8
s_add_u32 s59,s59,131072
s_mul_i32 s58,s45,1024
s_add_u32 s58,s58,109613056
s_mul_i32 s65,s45,114688
s_add_u32 s65,s65,s47
s_mul_i32 s68,s45,2560
s_add_u32 s68,s68,s48
s_mul_i32 s73,s45,1024
s_add_u32 s73,s73,107479040
s_mul_i32 s74,s45,32768
s_add_u32 s74,s74,s49
s_mul_i32 s75,s45,1024
s_add_u32 s75,s75,107479040
s_mov_b32 s66,0x7fff0000
s_mov_b32 s67,0x7fff0000
s_mov_b32 s69,0x7fff0000
s_mov_b32 s76,0x7fff0000
s_mov_b32 s77,0x7fff0000
s_mov_b32 s70,0x7fff0000
s_mov_b32 s71,0x7fff0000
s_mov_b32 s72,0x7fff0000
v_mov_b32 v1,v0
v_add_u32 v2,16384,v0
s_mov_b32 s84,16384
s_mov_b32 s86,0x3a800000
s_mov_b32 s87,0x3727c5ac
s_mov_b32 s88,0xc0135761
s_mov_b32 s89,0x3d372713
s_and_b32 s85,s44,1
s_mul_i32 s85,s85,127
v_lshlrev_b32 v8,22,v0
v_and_b32 v8,0x80000000,v8
v_or_b32 v8,v8,v5
v_add_u32 v2,32768,v0
s_lshl_b32 s84,s57,1
s_add_u32 s84,s84,32768
s_getpc_b64 s[40:41]
Lg3_pc_%=:
s_lshl_b32 s78,s44,13
v_lshlrev_b32 v94,3,v0
v_add_u32 v94,s78,v94
v_min_u32 v94,Lcode_g3_e-Lg3_pc_%=-4,v94
s_mov_b32 m0,163584
s_nop 0
global_load_lds_dword v94,s[40:41]
s_add_u32 m0,s59,4096
s_nop 0
buffer_load_dword v8,s[36:39],s58 offen offset:0 sc1 lds
buffer_store_dword v4,v3,s[36:39],s71 offen offset:0 sc0 sc1
s_add_u32 m0,s59,5120
s_nop 0
buffer_load_dword v8,s[36:39],s58 offen offset:0 sc1 lds
buffer_store_dword v4,v3,s[36:39],s71 offen offset:0 sc0 sc1
s_mov_b32 s83,0
Lg3_poll_%=:
s_add_u32 s78,s83,2
s_and_b32 s78,s78,3
s_lshl_b32 s78,s78,10
s_add_u32 s78,s78,4096
s_add_u32 m0,s59,s78
s_nop 0
buffer_load_dword v8,s[36:39],s58 offen offset:0 sc1 lds
s_and_b32 s78,s83,3
s_lshl_b32 s78,s78,10
v_add_u32 v94,s78,v6
s_waitcnt vmcnt(4)
s_barrier
ds_read_b32 v7,v94 offset:4096
buffer_store_dword v4,v3,s[36:39],s70 offen offset:0 sc0 sc1
s_mov_b32 s70,s71
s_mov_b32 s71,s72
s_mov_b32 s72,0x7fff0000
s_add_u32 s83,s83,1
s_add_u32 s82,s82,1
L 0
v_cmp_eq_u32 vcc,s53,v7
s_nop 0
s_cmp_eq_u32 vcc_lo,-1
s_cbranch_scc1 Lg3_go_%=
s_cmp_lt_u32 s82,20000
s_cbranch_scc1 Lg3_poll_%=
Lg3_go_%=:
buffer_load_dword v114,v11,s[36:39],s73 offen offset:0 sc1
s_add_u32 s73,s73,s64
s_add_u32 m0,s57,0
s_add_u32 s56,s55,0
D 0,56,0
D 0,56,1024
D 0,56,2048
D 0,56,3072
s_add_u32 s58,s58,s61
s_add_u32 m0,s59,2048
s_nop 0
buffer_load_dword v8,s[36:39],s58 offen offset:0 sc1 lds
s_add_u32 m0,s57,16384
s_add_u32 s56,s55,16384
D 0,56,0
D 0,56,1024
D 0,56,2048
D 0,56,3072
s_add_u32 s55,s55,s60
s_sub_u32 s57,s84,s57
s_add_u32 s54,s54,s46
s_waitcnt vmcnt(4)
s_barrier
buffer_store_dword v4,v3,s[36:39],s70 offen offset:0 sc0 sc1
s_min_u32 s71,s71,s72
buffer_store_dword v4,v3,s[36:39],s71 offen offset:0 sc0 sc1
s_mov_b32 s70,0x7fff0000
s_mov_b32 s71,0x7fff0000
s_mov_b32 s72,0x7fff0000
s_cmp_eq_u32 s80,1
s_cbranch_scc1 Lg3_st1_%=
s_branch Lg3_st0_%=
Lg3_gox_%=:
s_add_u32 s58,s58,s61
s_add_u32 m0,s59,2048
s_nop 0
buffer_load_dword v8,s[36:39],s58 offen offset:0 sc1 lds
s_waitcnt vmcnt(0)
s_barrier
buffer_store_dword v4,v3,s[36:39],s70 offen offset:0 sc0 sc1
s_min_u32 s71,s71,s72
buffer_store_dword v4,v3,s[36:39],s71 offen offset:0 sc0 sc1
s_mov_b32 s70,0x7fff0000
s_mov_b32 s71,0x7fff0000
s_mov_b32 s72,0x7fff0000
s_cmp_eq_u32 s80,1
s_cbranch_scc1 Lg3_st1_%=
Lg3_st0_%=:
ds_read_b32 v7,v6 offset:2048
R 42,1,0
R 46,1,1024
R 50,1,2048
R 54,1,3072
R 58,1,4096
L 5
v_cmp_eq_u32 vcc,s53,v7
L 3
s_cmp_eq_u32 vcc_lo,-1
s_cbranch_scc1 Lg3_F0_%=
s_branch Lg3_L0_%=
Lg3_st1_%=:
ds_read_b32 v7,v6 offset:2048
R 42,2,0
R 46,2,1024
R 50,2,2048
R 54,2,3072
R 58,2,4096
L 5
v_cmp_eq_u32 vcc,s53,v7
L 3
s_cmp_eq_u32 vcc_lo,-1
s_cbranch_scc1 Lg3_F1_%=
s_branch Lg3_L1_%=
Lg3_F0_%=:
v_mfma_f32_16x16x32_bf16 v[26:29],a[8:11],v[42:45],0
s_add_u32 m0,s57,0
R 62,1,5120
v_add_f32_dpp v115,v114,v114 quad_perm:[1,0,3,2] row_mask:0xf bank_mask:0xf
M 26,a[20:23],46
s_add_u32 s56,s55,0
buffer_load_dword v114,v11,s[36:39],s73 offen offset:0 sc1
v_mfma_f32_16x16x32_bf16 v[22:25],a[4:7],v[42:45],0
s_add_u32 s58,s58,s61
D 0,56,0
M 22,a[16:19],46
s_mov_b32 s66,s65
R 66,1,6144
v_add_f32_dpp v115,v115,v115 quad_perm:[2,3,0,1] row_mask:0xf bank_mask:0xf
v_mfma_f32_16x16x32_bf16 v[18:21],a[0:3],v[42:45],0
s_add_u32 s65,s65,s62
v_fma_f32 v30,-v78,v82,v30
v_fma_f32 v31,-v78,v83,v31
M 18,a[12:15],46
L 3
v_add_f32_dpp v118,v115,v115 row_half_mirror row_mask:0xf bank_mask:0xf
v_fma_f32 v32,-v78,v84,v32
M 18,a[24:27],50
s_add_u32 s54,s54,s46
R 70,1,7168
v_fma_f32 v33,-v78,v85,v33
v_mul_f32 v94,v30,v30
M 18,a[36:39],54
s_add_u32 s73,s73,s64
v_mov_b32_dpp v115,v118 row_ror:8 row_mask:0xf bank_mask:0xf
v_fma_f32 v115,-v118,v118,v115
M 22,a[28:31],50
v_max_f32 v115,s87,v115
v_mul_f32 v95,v31,v31
M 22,a[40:43],54
R 42,1,8192
v_mul_f32 v96,v32,v32
M 26,a[32:35],50
v_rsq_f32 v116,v115
M 26,a[44:47],54
L 3
D 0,56,1024
M 26,a[56:59],58
R 46,1,9216
v_mul_f32 v121,v115,v116
M 26,a[68:71],62
v_mul_f32 v119,s88,v116
v_mul_f32 v115,s89,v116
v_mul_f32 v120,v119,v116
M 22,a[52:55],58
v_mul_f32 v120,v120,v115
v_mul_f32 v97,v33,v33
M 22,a[64:67],62
R 50,1,10240
v_fma_f32 v94,v80,v94,v79
M 18,a[48:51],58
ds_write_b128 v12,v[118:121] offset:0
M 18,a[60:63],62
L 4
v_fma_f32 v95,v80,v95,v79
v_fma_f32 v96,v80,v96,v79
M 18,a[72:75],66
R 54,1,11264
v_fma_f32 v97,v80,v97,v79
M 18,a[84:87],70
v_mul_f32 v94,v30,v94
v_exp_f32 v94,v94
M 22,a[76:79],66
v_mul_f32 v95,v31,v95
v_mul_f32 v96,v32,v96
M 22,a[88:91],70
R 58,1,12288
v_mul_f32 v97,v33,v97
M 26,a[80:83],66
D 0,56,2048
M 26,a[92:95],70
L 4
v_exp_f32 v95,v95
M 26,a[104:107],42
R 62,1,13312
v_fma_f32 v94,v94,v81,v81
M 26,a[116:119],46
v_exp_f32 v96,v96
M 22,a[100:103],42
v_exp_f32 v97,v97
v_fma_f32 v95,v95,v81,v81
M 22,a[112:115],46
R 66,1,14336
v_fma_f32 v96,v96,v81,v81
M 18,a[96:99],42
v_rcp_f32 v94,v94
M 18,a[108:111],46
)"
R"(L 3
v_rcp_f32 v95,v95
M 18,a[120:123],50
R 70,1,15360
v_fma_f32 v97,v97,v81,v81
M 18,a[132:135],54
s_waitcnt vmcnt(6)
D 0,56,3072
M 22,a[124:127],50
s_barrier
v_rcp_f32 v96,v96
M 22,a[136:139],54
s_add_u32 m0,s59,0
R 42,1,16384
v_rcp_f32 v97,v97
M 26,a[128:131],50
s_add_u32 s56,s55,16384
v_mul_f32 v30,v30,v94
v_mul_f32 v31,v31,v95
M 26,a[140:143],54
L 3
v_mul_f32 v32,v32,v96
v_mul_f32 v33,v33,v97
M 26,a[152:155],58
s_add_u32 s55,s55,s60
R 46,1,17408
v_cvt_pk_bf16_f32 v106,v30,v31
M 26,a[164:167],62
v_cvt_pk_bf16_f32 v107,v32,v33
v_fma_f32 v34,-v78,v86,v34
M 22,a[148:151],58
v_fma_f32 v35,-v78,v87,v35
v_fma_f32 v36,-v78,v88,v36
M 22,a[160:163],62
R 50,1,18432
v_fma_f32 v37,-v78,v89,v37
v_mul_f32 v98,v34,v34
M 18,a[144:147],58
buffer_load_dword v8,s[36:39],s58 offen offset:0 sc1 lds
M 18,a[156:159],62
L 3
v_mul_f32 v99,v35,v35
v_mul_f32 v100,v36,v36
M 18,a[168:171],66
s_add_u32 m0,s57,16384
R 54,1,19456
v_mul_f32 v101,v37,v37
M 18,a[180:183],70
s_sub_u32 s57,s84,s57
v_fma_f32 v98,v80,v98,v79
v_fma_f32 v99,v80,v99,v79
M 22,a[172:175],66
v_fma_f32 v100,v80,v100,v79
v_fma_f32 v101,v80,v101,v79
M 22,a[184:187],70
R 58,1,20480
v_mul_f32 v98,v34,v98
M 26,a[176:179],66
D 0,56,0
v_mul_f32 v99,v35,v99
M 26,a[188:191],70
L 3
v_exp_f32 v98,v98
M 26,a[200:203],42
R 62,1,21504
v_mul_f32 v100,v36,v100
M 26,a[212:215],46
v_exp_f32 v99,v99
M 22,a[196:199],42
v_exp_f32 v100,v100
M 22,a[208:211],46
R 66,1,22528
v_mul_f32 v101,v37,v101
M 18,a[192:195],42
v_exp_f32 v101,v101
M 18,a[204:207],46
L 3
v_fma_f32 v98,v98,v81,v81
v_rcp_f32 v98,v98
M 18,a[216:219],50
R 70,1,23552
v_fma_f32 v99,v99,v81,v81
M 18,a[228:231],54
D 0,56,1024
M 22,a[220:223],50
v_rcp_f32 v99,v99
M 22,a[232:235],54
R 42,1,24576
v_fma_f32 v100,v100,v81,v81
M 26,a[224:227],50
v_rcp_f32 v100,v100
M 26,a[236:239],54
L 3
v_fma_f32 v101,v101,v81,v81
v_rcp_f32 v101,v101
M 26,a[248:251],58
R 46,1,25600
v_mul_f32 v34,v34,v98
M 26,v[132:135],62
v_mul_f32 v35,v35,v99
v_mul_f32 v36,v36,v100
M 22,a[244:247],58
v_mul_f32 v37,v37,v101
v_cvt_pk_bf16_f32 v108,v34,v35
M 22,v[128:131],62
R 50,1,26624
v_cvt_pk_bf16_f32 v109,v36,v37
M 18,a[240:243],58
buffer_store_dwordx4 v[106:109],v0,s[36:39],s67 offen offset:0 sc0 sc1
v_fma_f32 v38,-v78,v90,v38
M 18,a[252:255],62
L 3
v_fma_f32 v39,-v78,v91,v39
v_fma_f32 v40,-v78,v92,v40
M 18,v[136:139],66
R 54,1,27648
v_fma_f32 v41,-v78,v93,v41
v_mul_f32 v102,v38,v38
M 18,v[148:151],70
D 0,56,2048
M 22,v[140:143],66
v_mul_f32 v103,v39,v39
v_mul_f32 v104,v40,v40
M 22,v[152:155],70
R 58,1,28672
v_mul_f32 v105,v41,v41
M 26,v[144:147],66
v_fma_f32 v102,v80,v102,v79
v_fma_f32 v103,v80,v103,v79
M 26,v[156:159],70
L 3
v_fma_f32 v104,v80,v104,v79
v_fma_f32 v105,v80,v105,v79
M 26,v[168:171],42
R 62,1,29696
v_mul_f32 v102,v38,v102
M 26,v[180:183],46
v_exp_f32 v102,v102
v_mul_f32 v103,v39,v103
M 22,v[164:167],42
v_exp_f32 v103,v103
M 22,v[176:179],46
R 66,1,30720
v_mul_f32 v104,v40,v104
M 18,v[160:163],42
v_exp_f32 v104,v104
M 18,v[172:175],46
L 3
D 0,56,3072
M 18,v[184:187],50
s_nop 0
R 70,1,31744
v_mul_f32 v105,v41,v105
M 18,v[196:199],54
s_waitcnt vmcnt(5)
v_exp_f32 v105,v105
v_fma_f32 v102,v102,v81,v81
M 22,v[188:191],50
s_barrier
buffer_store_dword v4,v3,s[36:39],s70 offen offset:0 sc0 sc1
M 22,v[200:203],54
s_mov_b32 s70,s69
R 42,2,0
ds_read_b32 v7,v6 offset:0
M 26,v[192:195],50
s_mov_b32 s69,s68
R 74,13,0
v_fma_f32 v103,v103,v81,v81
M 26,v[204:207],54
L 5
v_rcp_f32 v102,v102
M 26,v[216:219],58
s_add_u32 s68,s68,s63
R 46,2,1024
v_fma_f32 v104,v104,v81,v81
M 26,v[228:231],62
v_rcp_f32 v103,v103
M 22,v[212:215],58
v_rcp_f32 v104,v104
v_fma_f32 v105,v105,v81,v81
M 22,v[224:227],62
L 2
R 50,2,2048
v_mul_f32 v38,v38,v102
M 18,v[208:211],58
v_rcp_f32 v105,v105
M 18,v[220:223],62
s_nop 0
v_mul_f32 v39,v39,v103
v_mul_f32 v40,v40,v104
M 18,v[232:235],66
R 54,2,3072
v_mul_f32 v41,v41,v105
M 18,v[244:247],70
v_cvt_pk_bf16_f32 v110,v38,v39
v_cvt_pk_bf16_f32 v111,v40,v41
M 22,v[236:239],66
v_cmp_eq_u32 vcc,s53,v7
M 22,v[248:251],70
s_cmp_eq_u32 vcc_lo,-1
R 58,2,4096
v_permlane16_swap_b32 v110,v112
v_permlane16_swap_b32 v111,v113
M 26,v[240:243],66
buffer_store_dwordx4 v[110:113],v10,s[36:39],s67 offen offset:0 sc0 sc1
M 26,v[252:255],70
L 3
s_cbranch_scc0 Lg3_L1_%=
Lg3_F1_%=:
v_mfma_f32_16x16x32_bf16 v[38:41],a[8:11],v[42:45],0
s_add_u32 m0,s57,0
R 62,2,5120
v_add_f32_dpp v115,v114,v114 quad_perm:[1,0,3,2] row_mask:0xf bank_mask:0xf
M 38,a[20:23],46
s_add_u32 s56,s55,0
buffer_load_dword v114,v11,s[36:39],s73 offen offset:0 sc1
v_mfma_f32_16x16x32_bf16 v[34:37],a[4:7],v[42:45],0
s_add_u32 s58,s58,s61
D 0,56,0
M 34,a[16:19],46
s_mov_b32 s67,s65
R 66,2,6144
v_add_f32_dpp v115,v115,v115 quad_perm:[2,3,0,1] row_mask:0xf bank_mask:0xf
v_mfma_f32_16x16x32_bf16 v[30:33],a[0:3],v[42:45],0
s_add_u32 s65,s65,s62
v_fma_f32 v18,-v74,v82,v18
v_fma_f32 v19,-v74,v83,v19
M 30,a[12:15],46
L 3
v_add_f32_dpp v118,v115,v115 row_half_mirror row_mask:0xf bank_mask:0xf
v_fma_f32 v20,-v74,v84,v20
M 30,a[24:27],50
s_add_u32 s54,s54,s46
R 70,2,7168
v_fma_f32 v21,-v74,v85,v21
v_mul_f32 v94,v18,v18
M 30,a[36:39],54
s_add_u32 s73,s73,s64
v_mov_b32_dpp v115,v118 row_ror:8 row_mask:0xf bank_mask:0xf
v_fma_f32 v115,-v118,v118,v115
M 34,a[28:31],50
v_max_f32 v115,s87,v115
v_mul_f32 v95,v19,v19
M 34,a[40:43],54
R 42,2,8192
v_mul_f32 v96,v20,v20
M 38,a[32:35],50
v_rsq_f32 v116,v115
M 38,a[44:47],54
L 3
D 0,56,1024
M 38,a[56:59],58
R 46,2,9216
v_mul_f32 v121,v115,v116
M 38,a[68:71],62
v_mul_f32 v119,s88,v116
v_mul_f32 v115,s89,v116
v_mul_f32 v120,v119,v116
M 34,a[52:55],58
v_mul_f32 v120,v120,v115
v_mul_f32 v97,v21,v21
M 34,a[64:67],62
R 50,2,10240
v_fma_f32 v94,v76,v94,v75
M 30,a[48:51],58
ds_write_b128 v12,v[118:121] offset:2048
M 30,a[60:63],62
L 4
v_fma_f32 v95,v76,v95,v75
v_fma_f32 v96,v76,v96,v75
M 30,a[72:75],66
R 54,2,11264
v_fma_f32 v97,v76,v97,v75
M 30,a[84:87],70
v_mul_f32 v94,v18,v94
v_exp_f32 v94,v94
M 34,a[76:79],66
v_mul_f32 v95,v19,v95
v_mul_f32 v96,v20,v96
M 34,a[88:91],70
R 58,2,12288
v_mul_f32 v97,v21,v97
M 38,a[80:83],66
D 0,56,2048
M 38,a[92:95],70
L 4
v_exp_f32 v95,v95
M 38,a[104:107],42
R 62,2,13312
v_fma_f32 v94,v94,v77,v77
M 38,a[116:119],46
v_exp_f32 v96,v96
M 34,a[100:103],42
v_exp_f32 v97,v97
v_fma_f32 v95,v95,v77,v77
M 34,a[112:115],46
R 66,2,14336
v_fma_f32 v96,v96,v77,v77
M 30,a[96:99],42
v_rcp_f32 v94,v94
M 30,a[108:111],46
L 3
v_rcp_f32 v95,v95
M 30,a[120:123],50
R 70,2,15360
v_fma_f32 v97,v97,v77,v77
M 30,a[132:135],54
s_waitcnt vmcnt(6)
D 0,56,3072
M 34,a[124:127],50
s_barrier
v_rcp_f32 v96,v96
M 34,a[136:139],54
s_add_u32 m0,s59,1024
R 42,2,16384
v_rcp_f32 v97,v97
M 38,a[128:131],50
s_add_u32 s56,s55,16384
v_mul_f32 v18,v18,v94
v_mul_f32 v19,v19,v95
M 38,a[140:143],54
L 3
v_mul_f32 v20,v20,v96
v_mul_f32 v21,v21,v97
M 38,a[152:155],58
s_add_u32 s55,s55,s60
R 46,2,17408
v_cvt_pk_bf16_f32 v106,v18,v19
M 38,a[164:167],62
v_cvt_pk_bf16_f32 v107,v20,v21
v_fma_f32 v22,-v74,v86,v22
M 34,a[148:151],58
v_fma_f32 v23,-v74,v87,v23
v_fma_f32 v24,-v74,v88,v24
M 34,a[160:163],62
R 50,2,18432
v_fma_f32 v25,-v74,v89,v25
v_mul_f32 v98,v22,v22
M 30,a[144:147],58
buffer_load_dword v8,s[36:39],s58 offen offset:0 sc1 lds
M 30,a[156:159],62
L 3
v_mul_f32 v99,v23,v23
v_mul_f32 v100,v24,v24
M 30,a[168:171],66
s_add_u32 m0,s57,16384
R 54,2,19456
v_mul_f32 v101,v25,v25
M 30,a[180:183],70
s_sub_u32 s57,s84,s57
v_fma_f32 v98,v76,v98,v75
v_fma_f32 v99,v76,v99,v75
M 34,a[172:175],66
v_fma_f32 v100,v76,v100,v75
v_fma_f32 v101,v76,v101,v75
M 34,a[184:187],70
R 58,2,20480
v_mul_f32 v98,v22,v98
M 38,a[176:179],66
D 0,56,0
v_mul_f32 v99,v23,v99
M 38,a[188:191],70
L 3
v_exp_f32 v98,v98
M 38,a[200:203],42
R 62,2,21504
v_mul_f32 v100,v24,v100
M 38,a[212:215],46
v_exp_f32 v99,v99
M 34,a[196:199],42
v_exp_f32 v100,v100
M 34,a[208:211],46
R 66,2,22528
v_mul_f32 v101,v25,v101
M 30,a[192:195],42
v_exp_f32 v101,v101
M 30,a[204:207],46
L 3
v_fma_f32 v98,v98,v77,v77
v_rcp_f32 v98,v98
M 30,a[216:219],50
R 70,2,23552
v_fma_f32 v99,v99,v77,v77
M 30,a[228:231],54
D 0,56,1024
M 34,a[220:223],50
v_rcp_f32 v99,v99
M 34,a[232:235],54
R 42,2,24576
v_fma_f32 v100,v100,v77,v77
M 38,a[224:227],50
v_rcp_f32 v100,v100
M 38,a[236:239],54
L 3
v_fma_f32 v101,v101,v77,v77
v_rcp_f32 v101,v101
M 38,a[248:251],58
R 46,2,25600
v_mul_f32 v22,v22,v98
M 38,v[132:135],62
v_mul_f32 v23,v23,v99
v_mul_f32 v24,v24,v100
M 34,a[244:247],58
v_mul_f32 v25,v25,v101
v_cvt_pk_bf16_f32 v108,v22,v23
M 34,v[128:131],62
R 50,2,26624
v_cvt_pk_bf16_f32 v109,v24,v25
M 30,a[240:243],58
buffer_store_dwordx4 v[106:109],v0,s[36:39],s66 offen offset:0 sc0 sc1
v_fma_f32 v26,-v74,v90,v26
M 30,a[252:255],62
L 3
v_fma_f32 v27,-v74,v91,v27
v_fma_f32 v28,-v74,v92,v28
M 30,v[136:139],66
R 54,2,27648
v_fma_f32 v29,-v74,v93,v29
v_mul_f32 v102,v26,v26
M 30,v[148:151],70
D 0,56,2048
M 34,v[140:143],66
v_mul_f32 v103,v27,v27
v_mul_f32 v104,v28,v28
M 34,v[152:155],70
R 58,2,28672
v_mul_f32 v105,v29,v29
M 38,v[144:147],66
v_fma_f32 v102,v76,v102,v75
v_fma_f32 v103,v76,v103,v75
M 38,v[156:159],70
L 3
v_fma_f32 v104,v76,v104,v75
v_fma_f32 v105,v76,v105,v75
M 38,v[168:171],42
R 62,2,29696
v_mul_f32 v102,v26,v102
M 38,v[180:183],46
v_exp_f32 v102,v102
v_mul_f32 v103,v27,v103
M 34,v[164:167],42
v_exp_f32 v103,v103
M 34,v[176:179],46
R 66,2,30720
v_mul_f32 v104,v28,v104
M 30,v[160:163],42
v_exp_f32 v104,v104
M 30,v[172:175],46
L 3
D 0,56,3072
M 30,v[184:187],50
s_nop 0
R 70,2,31744
v_mul_f32 v105,v29,v105
M 30,v[196:199],54
s_waitcnt vmcnt(5)
v_exp_f32 v105,v105
v_fma_f32 v102,v102,v77,v77
M 34,v[188:191],50
s_barrier
buffer_store_dword v4,v3,s[36:39],s70 offen offset:0 sc0 sc1
M 34,v[200:203],54
s_mov_b32 s70,s69
R 42,1,0
ds_read_b32 v7,v6 offset:1024
M 38,v[192:195],50
s_mov_b32 s69,s68
R 78,13,2048
v_fma_f32 v103,v103,v77,v77
M 38,v[204:207],54
L 5
v_rcp_f32 v102,v102
M 38,v[216:219],58
s_add_u32 s68,s68,s63
R 46,1,1024
v_fma_f32 v104,v104,v77,v77
M 38,v[228:231],62
v_rcp_f32 v103,v103
M 34,v[212:215],58
v_rcp_f32 v104,v104
v_fma_f32 v105,v105,v77,v77
M 34,v[224:227],62
L 2
R 50,1,2048
v_mul_f32 v26,v26,v102
M 30,v[208:211],58
v_rcp_f32 v105,v105
M 30,v[220:223],62
s_nop 0
v_mul_f32 v27,v27,v103
v_mul_f32 v28,v28,v104
M 30,v[232:235],66
R 54,1,3072
v_mul_f32 v29,v29,v105
M 30,v[244:247],70
v_cvt_pk_bf16_f32 v110,v26,v27
v_cvt_pk_bf16_f32 v111,v28,v29
M 34,v[236:239],66
v_cmp_eq_u32 vcc,s53,v7
M 34,v[248:251],70
)"
R"(s_cmp_eq_u32 vcc_lo,-1
R 58,1,4096
v_permlane16_swap_b32 v110,v112
v_permlane16_swap_b32 v111,v113
M 38,v[240:243],66
buffer_store_dwordx4 v[110:113],v10,s[36:39],s66 offen offset:0 sc0 sc1
M 38,v[252:255],70
L 3
s_cbranch_scc1 Lg3_F0_%=
Lg3_L0_%=:
v_mfma_f32_16x16x32_bf16 v[26:29],a[8:11],v[42:45],0
s_add_u32 m0,s59,3072
R 62,1,5120
v_add_f32_dpp v115,v114,v114 quad_perm:[1,0,3,2] row_mask:0xf bank_mask:0xf
M 26,a[20:23],46
s_mov_b32 s66,s65
buffer_load_dword v8,s[36:39],s58 offen offset:0 sc1 lds
v_mfma_f32_16x16x32_bf16 v[22:25],a[4:7],v[42:45],0
s_add_u32 s65,s65,s62
v_add_f32_dpp v115,v115,v115 quad_perm:[2,3,0,1] row_mask:0xf bank_mask:0xf
v_fma_f32 v30,-v78,v82,v30
M 22,a[16:19],46
R 66,1,6144
v_fma_f32 v31,-v78,v83,v31
v_mfma_f32_16x16x32_bf16 v[18:21],a[0:3],v[42:45],0
v_add_f32_dpp v118,v115,v115 row_half_mirror row_mask:0xf bank_mask:0xf
v_fma_f32 v32,-v78,v84,v32
v_fma_f32 v33,-v78,v85,v33
M 18,a[12:15],46
L 3
v_mul_f32 v94,v30,v30
v_mul_f32 v95,v31,v31
M 18,a[24:27],50
R 70,1,7168
v_mov_b32_dpp v115,v118 row_ror:8 row_mask:0xf bank_mask:0xf
M 18,a[36:39],54
v_fma_f32 v115,-v118,v118,v115
v_max_f32 v115,s87,v115
M 22,a[28:31],50
v_rsq_f32 v116,v115
v_mul_f32 v96,v32,v32
M 22,a[40:43],54
R 42,1,8192
v_mul_f32 v121,v115,v116
M 26,a[32:35],50
v_mul_f32 v119,s88,v116
v_mul_f32 v115,s89,v116
M 26,a[44:47],54
L 3
v_mul_f32 v120,v119,v116
v_mul_f32 v120,v120,v115
M 26,a[56:59],58
R 46,1,9216
v_mul_f32 v97,v33,v33
M 26,a[68:71],62
ds_write_b128 v12,v[118:121] offset:0
M 22,a[52:55],58
v_fma_f32 v94,v80,v94,v79
v_fma_f32 v95,v80,v95,v79
M 22,a[64:67],62
R 50,1,10240
v_fma_f32 v96,v80,v96,v79
M 18,a[48:51],58
v_fma_f32 v97,v80,v97,v79
v_mul_f32 v94,v30,v94
M 18,a[60:63],62
L 4
v_exp_f32 v94,v94
v_mul_f32 v95,v31,v95
M 18,a[72:75],66
R 54,1,11264
v_mul_f32 v96,v32,v96
M 18,a[84:87],70
v_exp_f32 v95,v95
M 22,a[76:79],66
v_exp_f32 v96,v96
M 22,a[88:91],70
R 58,1,12288
v_mul_f32 v97,v33,v97
M 26,a[80:83],66
v_exp_f32 v97,v97
v_fma_f32 v94,v94,v81,v81
M 26,a[92:95],70
L 4
v_rcp_f32 v94,v94
M 26,a[104:107],42
R 62,1,13312
v_fma_f32 v95,v95,v81,v81
M 26,a[116:119],46
v_rcp_f32 v95,v95
M 22,a[100:103],42
v_fma_f32 v96,v96,v81,v81
v_rcp_f32 v96,v96
M 22,a[112:115],46
R 66,1,14336
v_fma_f32 v97,v97,v81,v81
M 18,a[96:99],42
v_rcp_f32 v97,v97
M 18,a[108:111],46
L 3
v_mul_f32 v30,v30,v94
v_mul_f32 v31,v31,v95
M 18,a[120:123],50
R 70,1,15360
v_mul_f32 v32,v32,v96
M 18,a[132:135],54
s_waitcnt vmcnt(0)
v_mul_f32 v33,v33,v97
v_cvt_pk_bf16_f32 v106,v30,v31
v_cvt_pk_bf16_f32 v107,v32,v33
M 22,a[124:127],50
s_barrier
ds_read_b32 v7,v6 offset:3072
v_fma_f32 v34,-v78,v86,v34
M 22,a[136:139],54
R 42,1,16384
v_fma_f32 v35,-v78,v87,v35
M 26,a[128:131],50
buffer_store_dword v4,v3,s[36:39],s70 offen offset:0 sc0 sc1
M 26,a[140:143],54
L 4
v_fma_f32 v36,-v78,v88,v36
v_fma_f32 v37,-v78,v89,v37
v_mul_f32 v98,v34,v34
M 26,a[152:155],58
s_mov_b32 s70,0x7fff0000
R 46,1,17408
v_mul_f32 v99,v35,v35
M 26,a[164:167],62
v_mul_f32 v100,v36,v36
v_mul_f32 v101,v37,v37
M 22,a[148:151],58
L 2
v_cmp_eq_u32 vcc,s53,v7
v_fma_f32 v98,v80,v98,v79
M 22,a[160:163],62
R 50,1,18432
v_fma_f32 v99,v80,v99,v79
M 18,a[144:147],58
s_cmp_eq_u32 vcc_lo,-1
v_fma_f32 v100,v80,v100,v79
v_fma_f32 v101,v80,v101,v79
v_mul_f32 v98,v34,v98
s_cbranch_scc0 Lg3_X0_%=
M 18,a[156:159],62
s_nop 0
s_add_u32 m0,s57,0
s_add_u32 s56,s55,0
buffer_load_dword v114,v11,s[36:39],s73 offen offset:0 sc1
M 18,a[168:171],66
s_add_u32 s58,s58,s61
R 54,1,19456
v_mul_f32 v99,v35,v99
M 18,a[180:183],70
s_add_u32 s54,s54,s46
D 0,56,0
M 22,a[172:175],66
s_add_u32 s73,s73,s64
v_exp_f32 v98,v98
v_mul_f32 v100,v36,v100
M 22,a[184:187],70
R 58,1,20480
v_mul_f32 v101,v37,v101
M 26,a[176:179],66
v_exp_f32 v99,v99
M 26,a[188:191],70
L 3
D 0,56,1024
M 26,a[200:203],42
R 62,1,21504
v_exp_f32 v100,v100
M 26,a[212:215],46
v_exp_f32 v101,v101
M 22,a[196:199],42
v_fma_f32 v98,v98,v81,v81
v_fma_f32 v99,v99,v81,v81
M 22,a[208:211],46
R 66,1,22528
v_fma_f32 v100,v100,v81,v81
M 18,a[192:195],42
D 0,56,2048
M 18,a[204:207],46
L 3
v_rcp_f32 v98,v98
v_fma_f32 v101,v101,v81,v81
M 18,a[216:219],50
R 70,1,23552
v_mul_f32 v34,v34,v98
M 18,a[228:231],54
D 0,56,3072
M 22,a[220:223],50
s_add_u32 m0,s59,0
v_rcp_f32 v99,v99
M 22,a[232:235],54
s_add_u32 s56,s55,16384
R 42,1,24576
v_rcp_f32 v100,v100
M 26,a[224:227],50
s_add_u32 s55,s55,s60
buffer_load_dword v8,s[36:39],s58 offen offset:0 sc1 lds
M 26,a[236:239],54
L 3
v_rcp_f32 v101,v101
M 26,a[248:251],58
s_add_u32 m0,s57,16384
R 46,1,25600
v_mul_f32 v35,v35,v99
M 26,v[132:135],62
s_sub_u32 s57,s84,s57
D 0,56,0
M 22,a[244:247],58
v_mul_f32 v36,v36,v100
v_mul_f32 v37,v37,v101
v_cvt_pk_bf16_f32 v108,v34,v35
M 22,v[128:131],62
R 50,1,26624
v_cvt_pk_bf16_f32 v109,v36,v37
M 18,a[240:243],58
buffer_store_dwordx4 v[106:109],v0,s[36:39],s67 offen offset:0 sc0 sc1
v_fma_f32 v38,-v78,v90,v38
M 18,a[252:255],62
L 3
v_fma_f32 v39,-v78,v91,v39
v_fma_f32 v40,-v78,v92,v40
M 18,v[136:139],66
R 54,1,27648
v_fma_f32 v41,-v78,v93,v41
v_mul_f32 v102,v38,v38
M 18,v[148:151],70
D 0,56,1024
M 22,v[140:143],66
v_mul_f32 v103,v39,v39
v_mul_f32 v104,v40,v40
M 22,v[152:155],70
R 58,1,28672
v_mul_f32 v105,v41,v41
M 26,v[144:147],66
v_fma_f32 v102,v80,v102,v79
v_fma_f32 v103,v80,v103,v79
M 26,v[156:159],70
L 3
D 0,56,2048
v_fma_f32 v104,v80,v104,v79
M 26,v[168:171],42
R 62,1,29696
v_fma_f32 v105,v80,v105,v79
M 26,v[180:183],46
v_mul_f32 v102,v38,v102
v_mul_f32 v103,v39,v103
M 22,v[164:167],42
v_exp_f32 v102,v102
M 22,v[176:179],46
R 66,1,30720
v_exp_f32 v103,v103
M 18,v[160:163],42
D 0,56,3072
M 18,v[172:175],46
L 3
v_mul_f32 v104,v40,v104
v_mul_f32 v105,v41,v105
M 18,v[184:187],50
s_nop 0
R 70,1,31744
v_fma_f32 v102,v102,v81,v81
M 18,v[196:199],54
s_waitcnt vmcnt(5)
v_exp_f32 v104,v104
M 22,v[188:191],50
s_barrier
buffer_store_dword v4,v3,s[36:39],s70 offen offset:0 sc0 sc1
v_fma_f32 v103,v103,v81,v81
M 22,v[200:203],54
s_mov_b32 s70,s69
R 42,2,0
ds_read_b32 v7,v6 offset:0
M 26,v[192:195],50
s_mov_b32 s69,s68
R 74,13,0
v_fma_f32 v104,v104,v81,v81
M 26,v[204:207],54
L 5
v_exp_f32 v105,v105
M 26,v[216:219],58
s_add_u32 s68,s68,s63
R 46,2,1024
v_rcp_f32 v102,v102
M 26,v[228:231],62
v_rcp_f32 v103,v103
M 22,v[212:215],58
v_rcp_f32 v104,v104
M 22,v[224:227],62
L 2
R 50,2,2048
v_fma_f32 v105,v105,v81,v81
M 18,v[208:211],58
v_rcp_f32 v105,v105
v_mul_f32 v38,v38,v102
M 18,v[220:223],62
s_nop 0
v_mul_f32 v39,v39,v103
v_mul_f32 v40,v40,v104
M 18,v[232:235],66
R 54,2,3072
v_mul_f32 v41,v41,v105
M 18,v[244:247],70
v_cvt_pk_bf16_f32 v110,v38,v39
v_cvt_pk_bf16_f32 v111,v40,v41
M 22,v[236:239],66
v_cmp_eq_u32 vcc,s53,v7
M 22,v[248:251],70
s_cmp_eq_u32 vcc_lo,-1
R 58,2,4096
v_permlane16_swap_b32 v110,v112
v_permlane16_swap_b32 v111,v113
M 26,v[240:243],66
buffer_store_dwordx4 v[110:113],v10,s[36:39],s67 offen offset:0 sc0 sc1
M 26,v[252:255],70
L 3
s_cbranch_scc1 Lg3_F1_%=
s_branch Lg3_L1_%=
Lg3_X0_%=:
M 18,a[156:159],62
s_nop 0
s_add_u32 m0,s59,0
v_mul_f32 v99,v35,v99
M 18,a[168:171],66
R 54,1,19456
v_mul_f32 v100,v36,v100
M 18,a[180:183],70
buffer_load_dword v8,s[36:39],s58 offen offset:0 sc1 lds
M 22,a[172:175],66
v_exp_f32 v98,v98
M 22,a[184:187],70
R 58,1,20480
v_mul_f32 v101,v37,v101
M 26,a[176:179],66
v_exp_f32 v99,v99
M 26,a[188:191],70
L 3
v_exp_f32 v100,v100
M 26,a[200:203],42
R 62,1,21504
v_fma_f32 v98,v98,v81,v81
M 26,a[212:215],46
v_exp_f32 v101,v101
M 22,a[196:199],42
v_rcp_f32 v98,v98
M 22,a[208:211],46
R 66,1,22528
v_fma_f32 v99,v99,v81,v81
M 18,a[192:195],42
v_rcp_f32 v99,v99
M 18,a[204:207],46
L 3
v_fma_f32 v100,v100,v81,v81
v_fma_f32 v101,v101,v81,v81
M 18,a[216:219],50
R 70,1,23552
v_mul_f32 v34,v34,v98
M 18,a[228:231],54
v_rcp_f32 v100,v100
M 22,a[220:223],50
v_rcp_f32 v101,v101
M 22,a[232:235],54
R 42,1,24576
v_mul_f32 v35,v35,v99
M 26,a[224:227],50
v_mul_f32 v36,v36,v100
v_mul_f32 v37,v37,v101
M 26,a[236:239],54
L 3
v_cvt_pk_bf16_f32 v108,v34,v35
v_cvt_pk_bf16_f32 v109,v36,v37
M 26,a[248:251],58
R 46,1,25600
v_fma_f32 v38,-v78,v90,v38
M 26,v[132:135],62
buffer_store_dwordx4 v[106:109],v0,s[36:39],s67 offen offset:0 sc0 sc1
v_fma_f32 v39,-v78,v91,v39
M 22,a[244:247],58
v_fma_f32 v40,-v78,v92,v40
v_fma_f32 v41,-v78,v93,v41
M 22,v[128:131],62
R 50,1,26624
v_mul_f32 v102,v38,v38
M 18,a[240:243],58
v_mul_f32 v103,v39,v39
v_mul_f32 v104,v40,v40
M 18,a[252:255],62
L 3
v_mul_f32 v105,v41,v41
v_fma_f32 v102,v80,v102,v79
M 18,v[136:139],66
R 54,1,27648
v_fma_f32 v103,v80,v103,v79
M 18,v[148:151],70
v_fma_f32 v104,v80,v104,v79
v_fma_f32 v105,v80,v105,v79
M 22,v[140:143],66
v_mul_f32 v102,v38,v102
v_mul_f32 v103,v39,v103
M 22,v[152:155],70
R 58,1,28672
v_mul_f32 v104,v40,v104
M 26,v[144:147],66
v_exp_f32 v102,v102
M 26,v[156:159],70
L 3
v_exp_f32 v103,v103
M 26,v[168:171],42
R 62,1,29696
v_mul_f32 v105,v41,v105
M 26,v[180:183],46
v_exp_f32 v104,v104
M 22,v[164:167],42
v_exp_f32 v105,v105
M 22,v[176:179],46
R 66,1,30720
v_fma_f32 v102,v102,v81,v81
M 18,v[160:163],42
v_rcp_f32 v102,v102
M 18,v[172:175],46
L 3
v_fma_f32 v103,v103,v81,v81
v_fma_f32 v104,v104,v81,v81
M 18,v[184:187],50
s_nop 0
R 70,1,31744
v_fma_f32 v105,v105,v81,v81
M 18,v[196:199],54
s_waitcnt vmcnt(1)
v_rcp_f32 v103,v103
M 22,v[188:191],50
s_barrier
buffer_store_dword v4,v3,s[36:39],s70 offen offset:0 sc0 sc1
M 22,v[200:203],54
s_mov_b32 s70,s69
ds_read_b32 v7,v6 offset:0
R 74,13,0
M 26,v[192:195],50
s_mov_b32 s69,s68
v_rcp_f32 v104,v104
M 26,v[204:207],54
L 4
v_rcp_f32 v105,v105
M 26,v[216:219],58
s_add_u32 s68,s68,s63
v_mul_f32 v38,v38,v102
v_mul_f32 v39,v39,v103
M 26,v[228:231],62
v_mul_f32 v40,v40,v104
v_mul_f32 v41,v41,v105
M 22,v[212:215],58
v_cvt_pk_bf16_f32 v110,v38,v39
v_cvt_pk_bf16_f32 v111,v40,v41
M 22,v[224:227],62
L 1
v_cmp_eq_u32 vcc,s53,v7
M 18,v[208:211],58
v_permlane16_swap_b32 v110,v112
M 18,v[220:223],62
s_nop 0
v_permlane16_swap_b32 v111,v113
M 18,v[232:235],66
buffer_store_dwordx4 v[110:113],v10,s[36:39],s67 offen offset:0 sc0 sc1
M 18,v[244:247],70
M 22,v[236:239],66
M 22,v[248:251],70
M 26,v[240:243],66
M 26,v[252:255],70
s_mov_b32 s80,1
s_cmp_eq_u32 vcc_lo,-1
s_cbranch_scc0 Lg3_xn0_%=
buffer_load_dword v114,v11,s[36:39],s73 offen offset:0 sc1
L 0
v_fma_f32 v18,-v74,v82,v18
v_fma_f32 v19,-v74,v83,v19
v_fma_f32 v20,-v74,v84,v20
v_fma_f32 v21,-v74,v85,v21
v_mul_f32 v94,v18,v18
v_mul_f32 v95,v19,v19
v_mul_f32 v96,v20,v20
v_mul_f32 v97,v21,v21
s_add_u32 s73,s73,s64
s_add_u32 m0,s57,0
s_add_u32 s56,s55,0
D 0,56,0
v_fma_f32 v94,v76,v94,v75
v_fma_f32 v95,v76,v95,v75
v_fma_f32 v96,v76,v96,v75
v_fma_f32 v97,v76,v97,v75
v_mul_f32 v94,v18,v94
v_mul_f32 v95,v19,v95
v_mul_f32 v96,v20,v96
v_mul_f32 v97,v21,v97
D 0,56,1024
v_exp_f32 v94,v94
v_exp_f32 v95,v95
v_exp_f32 v96,v96
v_exp_f32 v97,v97
)"
R"(v_fma_f32 v94,v94,v77,v77
v_fma_f32 v95,v95,v77,v77
v_fma_f32 v96,v96,v77,v77
v_fma_f32 v97,v97,v77,v77
D 0,56,2048
v_rcp_f32 v94,v94
v_rcp_f32 v95,v95
v_rcp_f32 v96,v96
v_rcp_f32 v97,v97
v_mul_f32 v18,v18,v94
v_mul_f32 v19,v19,v95
v_mul_f32 v20,v20,v96
v_mul_f32 v21,v21,v97
D 0,56,3072
v_cvt_pk_bf16_f32 v106,v18,v19
v_cvt_pk_bf16_f32 v107,v20,v21
v_fma_f32 v22,-v74,v86,v22
v_fma_f32 v23,-v74,v87,v23
v_fma_f32 v24,-v74,v88,v24
v_fma_f32 v25,-v74,v89,v25
v_mul_f32 v98,v22,v22
v_mul_f32 v99,v23,v23
s_add_u32 m0,s57,16384
s_add_u32 s56,s55,16384
D 0,56,0
v_mul_f32 v100,v24,v24
v_mul_f32 v101,v25,v25
v_fma_f32 v98,v76,v98,v75
v_fma_f32 v99,v76,v99,v75
v_fma_f32 v100,v76,v100,v75
v_fma_f32 v101,v76,v101,v75
v_mul_f32 v98,v22,v98
v_mul_f32 v99,v23,v99
D 0,56,1024
v_mul_f32 v100,v24,v100
v_mul_f32 v101,v25,v101
v_exp_f32 v98,v98
v_exp_f32 v99,v99
v_exp_f32 v100,v100
v_exp_f32 v101,v101
v_fma_f32 v98,v98,v77,v77
v_fma_f32 v99,v99,v77,v77
D 0,56,2048
v_fma_f32 v100,v100,v77,v77
v_fma_f32 v101,v101,v77,v77
v_rcp_f32 v98,v98
v_rcp_f32 v99,v99
v_rcp_f32 v100,v100
v_rcp_f32 v101,v101
v_mul_f32 v22,v22,v98
v_mul_f32 v23,v23,v99
D 0,56,3072
v_mul_f32 v24,v24,v100
v_mul_f32 v25,v25,v101
v_cvt_pk_bf16_f32 v108,v22,v23
v_cvt_pk_bf16_f32 v109,v24,v25
buffer_store_dwordx4 v[106:109],v0,s[36:39],s66 offen offset:0 sc0 sc1
v_fma_f32 v26,-v74,v90,v26
v_fma_f32 v27,-v74,v91,v27
v_fma_f32 v28,-v74,v92,v28
v_fma_f32 v29,-v74,v93,v29
s_add_u32 s55,s55,s60
s_sub_u32 s57,s84,s57
s_add_u32 s54,s54,s46
v_mul_f32 v102,v26,v26
v_mul_f32 v103,v27,v27
v_mul_f32 v104,v28,v28
v_mul_f32 v105,v29,v29
v_fma_f32 v102,v76,v102,v75
v_fma_f32 v103,v76,v103,v75
v_fma_f32 v104,v76,v104,v75
v_fma_f32 v105,v76,v105,v75
v_mul_f32 v102,v26,v102
v_mul_f32 v103,v27,v103
v_mul_f32 v104,v28,v104
v_mul_f32 v105,v29,v105
v_exp_f32 v102,v102
v_exp_f32 v103,v103
v_exp_f32 v104,v104
v_exp_f32 v105,v105
v_fma_f32 v102,v102,v77,v77
v_fma_f32 v103,v103,v77,v77
v_fma_f32 v104,v104,v77,v77
v_fma_f32 v105,v105,v77,v77
v_rcp_f32 v102,v102
v_rcp_f32 v103,v103
v_rcp_f32 v104,v104
v_rcp_f32 v105,v105
v_mul_f32 v26,v26,v102
v_mul_f32 v27,v27,v103
v_mul_f32 v28,v28,v104
v_mul_f32 v29,v29,v105
v_cvt_pk_bf16_f32 v110,v26,v27
v_cvt_pk_bf16_f32 v111,v28,v29
s_nop 0
v_permlane16_swap_b32 v110,v112
v_permlane16_swap_b32 v111,v113
buffer_store_dwordx4 v[110:113],v10,s[36:39],s66 offen offset:0 sc0 sc1
s_mov_b32 s72,s69
s_mov_b32 s69,0x7fff0000
s_mov_b32 s66,0x7fff0000
s_mov_b32 s76,0x7fff0000
s_branch Lg3_gox_%=
Lg3_xn0_%=:
s_cmp_ge_u32 s54,256
s_cbranch_scc1 Lg3_fin0_%=
s_add_u32 m0,s59,4096
s_nop 0
buffer_load_dword v8,s[36:39],s58 offen offset:0 sc1 lds
buffer_store_dword v4,v3,s[36:39],s71 offen offset:0 sc0 sc1
s_add_u32 m0,s59,5120
s_nop 0
buffer_load_dword v8,s[36:39],s58 offen offset:0 sc1 lds
buffer_store_dword v4,v3,s[36:39],s71 offen offset:0 sc0 sc1
s_mov_b32 s83,0
L 0
v_fma_f32 v18,-v74,v82,v18
v_fma_f32 v19,-v74,v83,v19
v_fma_f32 v20,-v74,v84,v20
v_fma_f32 v21,-v74,v85,v21
v_mul_f32 v94,v18,v18
v_mul_f32 v95,v19,v19
v_mul_f32 v96,v20,v20
v_mul_f32 v97,v21,v21
v_fma_f32 v94,v76,v94,v75
v_fma_f32 v95,v76,v95,v75
v_fma_f32 v96,v76,v96,v75
v_fma_f32 v97,v76,v97,v75
v_mul_f32 v94,v18,v94
v_mul_f32 v95,v19,v95
v_mul_f32 v96,v20,v96
v_mul_f32 v97,v21,v97
v_exp_f32 v94,v94
v_exp_f32 v95,v95
v_exp_f32 v96,v96
v_exp_f32 v97,v97
v_fma_f32 v94,v94,v77,v77
v_fma_f32 v95,v95,v77,v77
v_fma_f32 v96,v96,v77,v77
v_fma_f32 v97,v97,v77,v77
v_rcp_f32 v94,v94
v_rcp_f32 v95,v95
v_rcp_f32 v96,v96
v_rcp_f32 v97,v97
v_mul_f32 v18,v18,v94
v_mul_f32 v19,v19,v95
v_mul_f32 v20,v20,v96
v_mul_f32 v21,v21,v97
v_cvt_pk_bf16_f32 v106,v18,v19
v_cvt_pk_bf16_f32 v107,v20,v21
v_fma_f32 v22,-v74,v86,v22
v_fma_f32 v23,-v74,v87,v23
v_fma_f32 v24,-v74,v88,v24
v_fma_f32 v25,-v74,v89,v25
v_mul_f32 v98,v22,v22
v_mul_f32 v99,v23,v23
v_mul_f32 v100,v24,v24
v_mul_f32 v101,v25,v25
v_fma_f32 v98,v76,v98,v75
v_fma_f32 v99,v76,v99,v75
v_fma_f32 v100,v76,v100,v75
v_fma_f32 v101,v76,v101,v75
v_mul_f32 v98,v22,v98
v_mul_f32 v99,v23,v99
v_mul_f32 v100,v24,v100
v_mul_f32 v101,v25,v101
v_exp_f32 v98,v98
v_exp_f32 v99,v99
v_exp_f32 v100,v100
v_exp_f32 v101,v101
v_fma_f32 v98,v98,v77,v77
v_fma_f32 v99,v99,v77,v77
v_fma_f32 v100,v100,v77,v77
v_fma_f32 v101,v101,v77,v77
v_rcp_f32 v98,v98
v_rcp_f32 v99,v99
v_rcp_f32 v100,v100
v_rcp_f32 v101,v101
v_mul_f32 v22,v22,v98
v_mul_f32 v23,v23,v99
v_mul_f32 v24,v24,v100
v_mul_f32 v25,v25,v101
v_cvt_pk_bf16_f32 v108,v22,v23
v_cvt_pk_bf16_f32 v109,v24,v25
buffer_store_dwordx4 v[106:109],v0,s[36:39],s66 offen offset:0 sc0 sc1
v_fma_f32 v26,-v74,v90,v26
v_fma_f32 v27,-v74,v91,v27
v_fma_f32 v28,-v74,v92,v28
v_fma_f32 v29,-v74,v93,v29
v_mul_f32 v102,v26,v26
v_mul_f32 v103,v27,v27
v_mul_f32 v104,v28,v28
v_mul_f32 v105,v29,v29
v_fma_f32 v102,v76,v102,v75
v_fma_f32 v103,v76,v103,v75
v_fma_f32 v104,v76,v104,v75
v_fma_f32 v105,v76,v105,v75
v_mul_f32 v102,v26,v102
v_mul_f32 v103,v27,v103
v_mul_f32 v104,v28,v104
v_mul_f32 v105,v29,v105
v_exp_f32 v102,v102
v_exp_f32 v103,v103
v_exp_f32 v104,v104
v_exp_f32 v105,v105
v_fma_f32 v102,v102,v77,v77
v_fma_f32 v103,v103,v77,v77
v_fma_f32 v104,v104,v77,v77
v_fma_f32 v105,v105,v77,v77
v_rcp_f32 v102,v102
v_rcp_f32 v103,v103
v_rcp_f32 v104,v104
v_rcp_f32 v105,v105
v_mul_f32 v26,v26,v102
v_mul_f32 v27,v27,v103
v_mul_f32 v28,v28,v104
v_mul_f32 v29,v29,v105
v_cvt_pk_bf16_f32 v110,v26,v27
v_cvt_pk_bf16_f32 v111,v28,v29
s_nop 0
v_permlane16_swap_b32 v110,v112
v_permlane16_swap_b32 v111,v113
buffer_store_dwordx4 v[110:113],v10,s[36:39],s66 offen offset:0 sc0 sc1
s_mov_b32 s72,s69
s_mov_b32 s69,0x7fff0000
s_mov_b32 s66,0x7fff0000
s_mov_b32 s76,0x7fff0000
s_branch Lg3_poll_%=
Lg3_fin0_%=:
L 0
v_fma_f32 v18,-v74,v82,v18
v_fma_f32 v19,-v74,v83,v19
v_fma_f32 v20,-v74,v84,v20
v_fma_f32 v21,-v74,v85,v21
v_mul_f32 v94,v18,v18
v_mul_f32 v95,v19,v19
v_mul_f32 v96,v20,v20
v_mul_f32 v97,v21,v21
v_fma_f32 v94,v76,v94,v75
v_fma_f32 v95,v76,v95,v75
v_fma_f32 v96,v76,v96,v75
v_fma_f32 v97,v76,v97,v75
v_mul_f32 v94,v18,v94
v_mul_f32 v95,v19,v95
v_mul_f32 v96,v20,v96
v_mul_f32 v97,v21,v97
v_exp_f32 v94,v94
v_exp_f32 v95,v95
v_exp_f32 v96,v96
v_exp_f32 v97,v97
v_fma_f32 v94,v94,v77,v77
v_fma_f32 v95,v95,v77,v77
v_fma_f32 v96,v96,v77,v77
v_fma_f32 v97,v97,v77,v77
v_rcp_f32 v94,v94
v_rcp_f32 v95,v95
v_rcp_f32 v96,v96
v_rcp_f32 v97,v97
v_mul_f32 v18,v18,v94
v_mul_f32 v19,v19,v95
v_mul_f32 v20,v20,v96
v_mul_f32 v21,v21,v97
v_cvt_pk_bf16_f32 v106,v18,v19
v_cvt_pk_bf16_f32 v107,v20,v21
v_fma_f32 v22,-v74,v86,v22
v_fma_f32 v23,-v74,v87,v23
v_fma_f32 v24,-v74,v88,v24
v_fma_f32 v25,-v74,v89,v25
v_mul_f32 v98,v22,v22
v_mul_f32 v99,v23,v23
v_mul_f32 v100,v24,v24
v_mul_f32 v101,v25,v25
v_fma_f32 v98,v76,v98,v75
v_fma_f32 v99,v76,v99,v75
v_fma_f32 v100,v76,v100,v75
v_fma_f32 v101,v76,v101,v75
v_mul_f32 v98,v22,v98
v_mul_f32 v99,v23,v99
v_mul_f32 v100,v24,v100
v_mul_f32 v101,v25,v101
v_exp_f32 v98,v98
v_exp_f32 v99,v99
v_exp_f32 v100,v100
v_exp_f32 v101,v101
v_fma_f32 v98,v98,v77,v77
v_fma_f32 v99,v99,v77,v77
v_fma_f32 v100,v100,v77,v77
v_fma_f32 v101,v101,v77,v77
v_rcp_f32 v98,v98
v_rcp_f32 v99,v99
v_rcp_f32 v100,v100
v_rcp_f32 v101,v101
v_mul_f32 v22,v22,v98
v_mul_f32 v23,v23,v99
v_mul_f32 v24,v24,v100
v_mul_f32 v25,v25,v101
v_cvt_pk_bf16_f32 v108,v22,v23
v_cvt_pk_bf16_f32 v109,v24,v25
buffer_store_dwordx4 v[106:109],v0,s[36:39],s66 offen offset:0 sc0 sc1
v_fma_f32 v26,-v74,v90,v26
v_fma_f32 v27,-v74,v91,v27
v_fma_f32 v28,-v74,v92,v28
v_fma_f32 v29,-v74,v93,v29
v_mul_f32 v102,v26,v26
v_mul_f32 v103,v27,v27
v_mul_f32 v104,v28,v28
v_mul_f32 v105,v29,v29
v_fma_f32 v102,v76,v102,v75
v_fma_f32 v103,v76,v103,v75
v_fma_f32 v104,v76,v104,v75
v_fma_f32 v105,v76,v105,v75
v_mul_f32 v102,v26,v102
v_mul_f32 v103,v27,v103
v_mul_f32 v104,v28,v104
v_mul_f32 v105,v29,v105
v_exp_f32 v102,v102
v_exp_f32 v103,v103
v_exp_f32 v104,v104
v_exp_f32 v105,v105
v_fma_f32 v102,v102,v77,v77
v_fma_f32 v103,v103,v77,v77
v_fma_f32 v104,v104,v77,v77
v_fma_f32 v105,v105,v77,v77
v_rcp_f32 v102,v102
v_rcp_f32 v103,v103
v_rcp_f32 v104,v104
v_rcp_f32 v105,v105
v_mul_f32 v26,v26,v102
v_mul_f32 v27,v27,v103
v_mul_f32 v28,v28,v104
v_mul_f32 v29,v29,v105
v_cvt_pk_bf16_f32 v110,v26,v27
v_cvt_pk_bf16_f32 v111,v28,v29
s_nop 0
v_permlane16_swap_b32 v110,v112
v_permlane16_swap_b32 v111,v113
buffer_store_dwordx4 v[110:113],v10,s[36:39],s66 offen offset:0 sc0 sc1
s_mov_b32 s72,s69
s_mov_b32 s69,0x7fff0000
s_mov_b32 s66,0x7fff0000
s_mov_b32 s76,0x7fff0000
s_branch Lg3_done_%=
Lg3_L1_%=:
v_mfma_f32_16x16x32_bf16 v[38:41],a[8:11],v[42:45],0
s_add_u32 m0,s59,3072
R 62,2,5120
v_add_f32_dpp v115,v114,v114 quad_perm:[1,0,3,2] row_mask:0xf bank_mask:0xf
M 38,a[20:23],46
s_mov_b32 s67,s65
buffer_load_dword v8,s[36:39],s58 offen offset:0 sc1 lds
v_mfma_f32_16x16x32_bf16 v[34:37],a[4:7],v[42:45],0
s_add_u32 s65,s65,s62
v_add_f32_dpp v115,v115,v115 quad_perm:[2,3,0,1] row_mask:0xf bank_mask:0xf
v_fma_f32 v18,-v74,v82,v18
M 34,a[16:19],46
R 66,2,6144
v_fma_f32 v19,-v74,v83,v19
v_mfma_f32_16x16x32_bf16 v[30:33],a[0:3],v[42:45],0
v_add_f32_dpp v118,v115,v115 row_half_mirror row_mask:0xf bank_mask:0xf
v_fma_f32 v20,-v74,v84,v20
v_fma_f32 v21,-v74,v85,v21
M 30,a[12:15],46
L 3
v_mul_f32 v94,v18,v18
v_mul_f32 v95,v19,v19
M 30,a[24:27],50
R 70,2,7168
v_mov_b32_dpp v115,v118 row_ror:8 row_mask:0xf bank_mask:0xf
M 30,a[36:39],54
v_fma_f32 v115,-v118,v118,v115
v_max_f32 v115,s87,v115
M 34,a[28:31],50
v_rsq_f32 v116,v115
v_mul_f32 v96,v20,v20
M 34,a[40:43],54
R 42,2,8192
v_mul_f32 v121,v115,v116
M 38,a[32:35],50
v_mul_f32 v119,s88,v116
v_mul_f32 v115,s89,v116
M 38,a[44:47],54
L 3
v_mul_f32 v120,v119,v116
v_mul_f32 v120,v120,v115
M 38,a[56:59],58
R 46,2,9216
v_mul_f32 v97,v21,v21
M 38,a[68:71],62
ds_write_b128 v12,v[118:121] offset:2048
M 34,a[52:55],58
v_fma_f32 v94,v76,v94,v75
v_fma_f32 v95,v76,v95,v75
M 34,a[64:67],62
R 50,2,10240
v_fma_f32 v96,v76,v96,v75
M 30,a[48:51],58
v_fma_f32 v97,v76,v97,v75
v_mul_f32 v94,v18,v94
M 30,a[60:63],62
L 4
v_exp_f32 v94,v94
v_mul_f32 v95,v19,v95
M 30,a[72:75],66
R 54,2,11264
v_mul_f32 v96,v20,v96
M 30,a[84:87],70
v_exp_f32 v95,v95
M 34,a[76:79],66
v_exp_f32 v96,v96
M 34,a[88:91],70
R 58,2,12288
v_mul_f32 v97,v21,v97
M 38,a[80:83],66
v_exp_f32 v97,v97
v_fma_f32 v94,v94,v77,v77
M 38,a[92:95],70
L 4
v_rcp_f32 v94,v94
M 38,a[104:107],42
R 62,2,13312
v_fma_f32 v95,v95,v77,v77
M 38,a[116:119],46
v_rcp_f32 v95,v95
M 34,a[100:103],42
v_fma_f32 v96,v96,v77,v77
v_rcp_f32 v96,v96
M 34,a[112:115],46
R 66,2,14336
v_fma_f32 v97,v97,v77,v77
M 30,a[96:99],42
v_rcp_f32 v97,v97
M 30,a[108:111],46
L 3
v_mul_f32 v18,v18,v94
v_mul_f32 v19,v19,v95
M 30,a[120:123],50
R 70,2,15360
v_mul_f32 v20,v20,v96
M 30,a[132:135],54
s_waitcnt vmcnt(0)
v_mul_f32 v21,v21,v97
v_cvt_pk_bf16_f32 v106,v18,v19
v_cvt_pk_bf16_f32 v107,v20,v21
M 34,a[124:127],50
s_barrier
ds_read_b32 v7,v6 offset:3072
v_fma_f32 v22,-v74,v86,v22
M 34,a[136:139],54
R 42,2,16384
v_fma_f32 v23,-v74,v87,v23
M 38,a[128:131],50
buffer_store_dword v4,v3,s[36:39],s70 offen offset:0 sc0 sc1
M 38,a[140:143],54
L 4
v_fma_f32 v24,-v74,v88,v24
v_fma_f32 v25,-v74,v89,v25
v_mul_f32 v98,v22,v22
M 38,a[152:155],58
s_mov_b32 s70,0x7fff0000
R 46,2,17408
v_mul_f32 v99,v23,v23
M 38,a[164:167],62
v_mul_f32 v100,v24,v24
v_mul_f32 v101,v25,v25
M 34,a[148:151],58
L 2
v_cmp_eq_u32 vcc,s53,v7
v_fma_f32 v98,v76,v98,v75
M 34,a[160:163],62
R 50,2,18432
v_fma_f32 v99,v76,v99,v75
M 30,a[144:147],58
s_cmp_eq_u32 vcc_lo,-1
v_fma_f32 v100,v76,v100,v75
v_fma_f32 v101,v76,v101,v75
v_mul_f32 v98,v22,v98
s_cbranch_scc0 Lg3_X1_%=
M 30,a[156:159],62
s_nop 0
s_add_u32 m0,s57,0
s_add_u32 s56,s55,0
buffer_load_dword v114,v11,s[36:39],s73 offen offset:0 sc1
M 30,a[168:171],66
s_add_u32 s58,s58,s61
R 54,2,19456
v_mul_f32 v99,v23,v99
M 30,a[180:183],70
s_add_u32 s54,s54,s46
D 0,56,0
M 34,a[172:175],66
s_add_u32 s73,s73,s64
v_exp_f32 v98,v98
v_mul_f32 v100,v24,v100
M 34,a[184:187],70
R 58,2,20480
v_mul_f32 v101,v25,v101
M 38,a[176:179],66
v_exp_f32 v99,v99
M 38,a[188:191],70
L 3
)"
R"(D 0,56,1024
M 38,a[200:203],42
R 62,2,21504
v_exp_f32 v100,v100
M 38,a[212:215],46
v_exp_f32 v101,v101
M 34,a[196:199],42
v_fma_f32 v98,v98,v77,v77
v_fma_f32 v99,v99,v77,v77
M 34,a[208:211],46
R 66,2,22528
v_fma_f32 v100,v100,v77,v77
M 30,a[192:195],42
D 0,56,2048
M 30,a[204:207],46
L 3
v_rcp_f32 v98,v98
v_fma_f32 v101,v101,v77,v77
M 30,a[216:219],50
R 70,2,23552
v_mul_f32 v22,v22,v98
M 30,a[228:231],54
D 0,56,3072
M 34,a[220:223],50
s_add_u32 m0,s59,1024
v_rcp_f32 v99,v99
M 34,a[232:235],54
s_add_u32 s56,s55,16384
R 42,2,24576
v_rcp_f32 v100,v100
M 38,a[224:227],50
s_add_u32 s55,s55,s60
buffer_load_dword v8,s[36:39],s58 offen offset:0 sc1 lds
M 38,a[236:239],54
L 3
v_rcp_f32 v101,v101
M 38,a[248:251],58
s_add_u32 m0,s57,16384
R 46,2,25600
v_mul_f32 v23,v23,v99
M 38,v[132:135],62
s_sub_u32 s57,s84,s57
D 0,56,0
M 34,a[244:247],58
v_mul_f32 v24,v24,v100
v_mul_f32 v25,v25,v101
v_cvt_pk_bf16_f32 v108,v22,v23
M 34,v[128:131],62
R 50,2,26624
v_cvt_pk_bf16_f32 v109,v24,v25
M 30,a[240:243],58
buffer_store_dwordx4 v[106:109],v0,s[36:39],s66 offen offset:0 sc0 sc1
v_fma_f32 v26,-v74,v90,v26
M 30,a[252:255],62
L 3
v_fma_f32 v27,-v74,v91,v27
v_fma_f32 v28,-v74,v92,v28
M 30,v[136:139],66
R 54,2,27648
v_fma_f32 v29,-v74,v93,v29
v_mul_f32 v102,v26,v26
M 30,v[148:151],70
D 0,56,1024
M 34,v[140:143],66
v_mul_f32 v103,v27,v27
v_mul_f32 v104,v28,v28
M 34,v[152:155],70
R 58,2,28672
v_mul_f32 v105,v29,v29
M 38,v[144:147],66
v_fma_f32 v102,v76,v102,v75
v_fma_f32 v103,v76,v103,v75
M 38,v[156:159],70
L 3
D 0,56,2048
v_fma_f32 v104,v76,v104,v75
M 38,v[168:171],42
R 62,2,29696
v_fma_f32 v105,v76,v105,v75
M 38,v[180:183],46
v_mul_f32 v102,v26,v102
v_mul_f32 v103,v27,v103
M 34,v[164:167],42
v_exp_f32 v102,v102
M 34,v[176:179],46
R 66,2,30720
v_exp_f32 v103,v103
M 30,v[160:163],42
D 0,56,3072
M 30,v[172:175],46
L 3
v_mul_f32 v104,v28,v104
v_mul_f32 v105,v29,v105
M 30,v[184:187],50
s_nop 0
R 70,2,31744
v_fma_f32 v102,v102,v77,v77
M 30,v[196:199],54
s_waitcnt vmcnt(5)
v_exp_f32 v104,v104
M 34,v[188:191],50
s_barrier
buffer_store_dword v4,v3,s[36:39],s70 offen offset:0 sc0 sc1
v_fma_f32 v103,v103,v77,v77
M 34,v[200:203],54
s_mov_b32 s70,s69
R 42,1,0
ds_read_b32 v7,v6 offset:1024
M 38,v[192:195],50
s_mov_b32 s69,s68
R 78,13,2048
v_fma_f32 v104,v104,v77,v77
M 38,v[204:207],54
L 5
v_exp_f32 v105,v105
M 38,v[216:219],58
s_add_u32 s68,s68,s63
R 46,1,1024
v_rcp_f32 v102,v102
M 38,v[228:231],62
v_rcp_f32 v103,v103
M 34,v[212:215],58
v_rcp_f32 v104,v104
M 34,v[224:227],62
L 2
R 50,1,2048
v_fma_f32 v105,v105,v77,v77
M 30,v[208:211],58
v_rcp_f32 v105,v105
v_mul_f32 v26,v26,v102
M 30,v[220:223],62
s_nop 0
v_mul_f32 v27,v27,v103
v_mul_f32 v28,v28,v104
M 30,v[232:235],66
R 54,1,3072
v_mul_f32 v29,v29,v105
M 30,v[244:247],70
v_cvt_pk_bf16_f32 v110,v26,v27
v_cvt_pk_bf16_f32 v111,v28,v29
M 34,v[236:239],66
v_cmp_eq_u32 vcc,s53,v7
M 34,v[248:251],70
s_cmp_eq_u32 vcc_lo,-1
R 58,1,4096
v_permlane16_swap_b32 v110,v112
v_permlane16_swap_b32 v111,v113
M 38,v[240:243],66
buffer_store_dwordx4 v[110:113],v10,s[36:39],s66 offen offset:0 sc0 sc1
M 38,v[252:255],70
L 3
s_cbranch_scc1 Lg3_F0_%=
s_branch Lg3_L0_%=
Lg3_X1_%=:
M 30,a[156:159],62
s_nop 0
s_add_u32 m0,s59,1024
v_mul_f32 v99,v23,v99
M 30,a[168:171],66
R 54,2,19456
v_mul_f32 v100,v24,v100
M 30,a[180:183],70
buffer_load_dword v8,s[36:39],s58 offen offset:0 sc1 lds
M 34,a[172:175],66
v_exp_f32 v98,v98
M 34,a[184:187],70
R 58,2,20480
v_mul_f32 v101,v25,v101
M 38,a[176:179],66
v_exp_f32 v99,v99
M 38,a[188:191],70
L 3
v_exp_f32 v100,v100
M 38,a[200:203],42
R 62,2,21504
v_fma_f32 v98,v98,v77,v77
M 38,a[212:215],46
v_exp_f32 v101,v101
M 34,a[196:199],42
v_rcp_f32 v98,v98
M 34,a[208:211],46
R 66,2,22528
v_fma_f32 v99,v99,v77,v77
M 30,a[192:195],42
v_rcp_f32 v99,v99
M 30,a[204:207],46
L 3
v_fma_f32 v100,v100,v77,v77
v_fma_f32 v101,v101,v77,v77
M 30,a[216:219],50
R 70,2,23552
v_mul_f32 v22,v22,v98
M 30,a[228:231],54
v_rcp_f32 v100,v100
M 34,a[220:223],50
v_rcp_f32 v101,v101
M 34,a[232:235],54
R 42,2,24576
v_mul_f32 v23,v23,v99
M 38,a[224:227],50
v_mul_f32 v24,v24,v100
v_mul_f32 v25,v25,v101
M 38,a[236:239],54
L 3
v_cvt_pk_bf16_f32 v108,v22,v23
v_cvt_pk_bf16_f32 v109,v24,v25
M 38,a[248:251],58
R 46,2,25600
v_fma_f32 v26,-v74,v90,v26
M 38,v[132:135],62
buffer_store_dwordx4 v[106:109],v0,s[36:39],s66 offen offset:0 sc0 sc1
v_fma_f32 v27,-v74,v91,v27
M 34,a[244:247],58
v_fma_f32 v28,-v74,v92,v28
v_fma_f32 v29,-v74,v93,v29
M 34,v[128:131],62
R 50,2,26624
v_mul_f32 v102,v26,v26
M 30,a[240:243],58
v_mul_f32 v103,v27,v27
v_mul_f32 v104,v28,v28
M 30,a[252:255],62
L 3
v_mul_f32 v105,v29,v29
v_fma_f32 v102,v76,v102,v75
M 30,v[136:139],66
R 54,2,27648
v_fma_f32 v103,v76,v103,v75
M 30,v[148:151],70
v_fma_f32 v104,v76,v104,v75
v_fma_f32 v105,v76,v105,v75
M 34,v[140:143],66
v_mul_f32 v102,v26,v102
v_mul_f32 v103,v27,v103
M 34,v[152:155],70
R 58,2,28672
v_mul_f32 v104,v28,v104
M 38,v[144:147],66
v_exp_f32 v102,v102
M 38,v[156:159],70
L 3
v_exp_f32 v103,v103
M 38,v[168:171],42
R 62,2,29696
v_mul_f32 v105,v29,v105
M 38,v[180:183],46
v_exp_f32 v104,v104
M 34,v[164:167],42
v_exp_f32 v105,v105
M 34,v[176:179],46
R 66,2,30720
v_fma_f32 v102,v102,v77,v77
M 30,v[160:163],42
v_rcp_f32 v102,v102
M 30,v[172:175],46
L 3
v_fma_f32 v103,v103,v77,v77
v_fma_f32 v104,v104,v77,v77
M 30,v[184:187],50
s_nop 0
R 70,2,31744
v_fma_f32 v105,v105,v77,v77
M 30,v[196:199],54
s_waitcnt vmcnt(1)
v_rcp_f32 v103,v103
M 34,v[188:191],50
s_barrier
buffer_store_dword v4,v3,s[36:39],s70 offen offset:0 sc0 sc1
M 34,v[200:203],54
s_mov_b32 s70,s69
ds_read_b32 v7,v6 offset:1024
R 78,13,2048
M 38,v[192:195],50
s_mov_b32 s69,s68
v_rcp_f32 v104,v104
M 38,v[204:207],54
L 4
v_rcp_f32 v105,v105
M 38,v[216:219],58
s_add_u32 s68,s68,s63
v_mul_f32 v26,v26,v102
v_mul_f32 v27,v27,v103
M 38,v[228:231],62
v_mul_f32 v28,v28,v104
v_mul_f32 v29,v29,v105
M 34,v[212:215],58
v_cvt_pk_bf16_f32 v110,v26,v27
v_cvt_pk_bf16_f32 v111,v28,v29
M 34,v[224:227],62
L 1
v_cmp_eq_u32 vcc,s53,v7
M 30,v[208:211],58
v_permlane16_swap_b32 v110,v112
M 30,v[220:223],62
s_nop 0
v_permlane16_swap_b32 v111,v113
M 30,v[232:235],66
buffer_store_dwordx4 v[110:113],v10,s[36:39],s66 offen offset:0 sc0 sc1
M 30,v[244:247],70
M 34,v[236:239],66
M 34,v[248:251],70
M 38,v[240:243],66
M 38,v[252:255],70
s_mov_b32 s80,0
s_cmp_eq_u32 vcc_lo,-1
s_cbranch_scc0 Lg3_xn1_%=
buffer_load_dword v114,v11,s[36:39],s73 offen offset:0 sc1
L 0
v_fma_f32 v30,-v78,v82,v30
v_fma_f32 v31,-v78,v83,v31
v_fma_f32 v32,-v78,v84,v32
v_fma_f32 v33,-v78,v85,v33
v_mul_f32 v94,v30,v30
v_mul_f32 v95,v31,v31
v_mul_f32 v96,v32,v32
v_mul_f32 v97,v33,v33
s_add_u32 s73,s73,s64
s_add_u32 m0,s57,0
s_add_u32 s56,s55,0
D 0,56,0
v_fma_f32 v94,v80,v94,v79
v_fma_f32 v95,v80,v95,v79
v_fma_f32 v96,v80,v96,v79
v_fma_f32 v97,v80,v97,v79
v_mul_f32 v94,v30,v94
v_mul_f32 v95,v31,v95
v_mul_f32 v96,v32,v96
v_mul_f32 v97,v33,v97
D 0,56,1024
v_exp_f32 v94,v94
v_exp_f32 v95,v95
v_exp_f32 v96,v96
v_exp_f32 v97,v97
v_fma_f32 v94,v94,v81,v81
v_fma_f32 v95,v95,v81,v81
v_fma_f32 v96,v96,v81,v81
v_fma_f32 v97,v97,v81,v81
D 0,56,2048
v_rcp_f32 v94,v94
v_rcp_f32 v95,v95
v_rcp_f32 v96,v96
v_rcp_f32 v97,v97
v_mul_f32 v30,v30,v94
v_mul_f32 v31,v31,v95
v_mul_f32 v32,v32,v96
v_mul_f32 v33,v33,v97
D 0,56,3072
v_cvt_pk_bf16_f32 v106,v30,v31
v_cvt_pk_bf16_f32 v107,v32,v33
v_fma_f32 v34,-v78,v86,v34
v_fma_f32 v35,-v78,v87,v35
v_fma_f32 v36,-v78,v88,v36
v_fma_f32 v37,-v78,v89,v37
v_mul_f32 v98,v34,v34
v_mul_f32 v99,v35,v35
s_add_u32 m0,s57,16384
s_add_u32 s56,s55,16384
D 0,56,0
v_mul_f32 v100,v36,v36
v_mul_f32 v101,v37,v37
v_fma_f32 v98,v80,v98,v79
v_fma_f32 v99,v80,v99,v79
v_fma_f32 v100,v80,v100,v79
v_fma_f32 v101,v80,v101,v79
v_mul_f32 v98,v34,v98
v_mul_f32 v99,v35,v99
D 0,56,1024
v_mul_f32 v100,v36,v100
v_mul_f32 v101,v37,v101
v_exp_f32 v98,v98
v_exp_f32 v99,v99
v_exp_f32 v100,v100
v_exp_f32 v101,v101
v_fma_f32 v98,v98,v81,v81
v_fma_f32 v99,v99,v81,v81
D 0,56,2048
v_fma_f32 v100,v100,v81,v81
v_fma_f32 v101,v101,v81,v81
v_rcp_f32 v98,v98
v_rcp_f32 v99,v99
v_rcp_f32 v100,v100
v_rcp_f32 v101,v101
v_mul_f32 v34,v34,v98
v_mul_f32 v35,v35,v99
D 0,56,3072
v_mul_f32 v36,v36,v100
v_mul_f32 v37,v37,v101
v_cvt_pk_bf16_f32 v108,v34,v35
v_cvt_pk_bf16_f32 v109,v36,v37
buffer_store_dwordx4 v[106:109],v0,s[36:39],s67 offen offset:0 sc0 sc1
v_fma_f32 v38,-v78,v90,v38
v_fma_f32 v39,-v78,v91,v39
v_fma_f32 v40,-v78,v92,v40
v_fma_f32 v41,-v78,v93,v41
s_add_u32 s55,s55,s60
s_sub_u32 s57,s84,s57
s_add_u32 s54,s54,s46
v_mul_f32 v102,v38,v38
v_mul_f32 v103,v39,v39
v_mul_f32 v104,v40,v40
v_mul_f32 v105,v41,v41
v_fma_f32 v102,v80,v102,v79
v_fma_f32 v103,v80,v103,v79
v_fma_f32 v104,v80,v104,v79
v_fma_f32 v105,v80,v105,v79
v_mul_f32 v102,v38,v102
v_mul_f32 v103,v39,v103
v_mul_f32 v104,v40,v104
v_mul_f32 v105,v41,v105
v_exp_f32 v102,v102
v_exp_f32 v103,v103
v_exp_f32 v104,v104
v_exp_f32 v105,v105
v_fma_f32 v102,v102,v81,v81
v_fma_f32 v103,v103,v81,v81
v_fma_f32 v104,v104,v81,v81
v_fma_f32 v105,v105,v81,v81
v_rcp_f32 v102,v102
v_rcp_f32 v103,v103
v_rcp_f32 v104,v104
v_rcp_f32 v105,v105
v_mul_f32 v38,v38,v102
v_mul_f32 v39,v39,v103
v_mul_f32 v40,v40,v104
v_mul_f32 v41,v41,v105
v_cvt_pk_bf16_f32 v110,v38,v39
v_cvt_pk_bf16_f32 v111,v40,v41
s_nop 0
v_permlane16_swap_b32 v110,v112
v_permlane16_swap_b32 v111,v113
buffer_store_dwordx4 v[110:113],v10,s[36:39],s67 offen offset:0 sc0 sc1
s_mov_b32 s72,s69
s_mov_b32 s69,0x7fff0000
s_mov_b32 s67,0x7fff0000
s_mov_b32 s77,0x7fff0000
s_branch Lg3_gox_%=
Lg3_xn1_%=:
s_cmp_ge_u32 s54,256
s_cbranch_scc1 Lg3_fin1_%=
s_add_u32 m0,s59,4096
s_nop 0
buffer_load_dword v8,s[36:39],s58 offen offset:0 sc1 lds
buffer_store_dword v4,v3,s[36:39],s71 offen offset:0 sc0 sc1
s_add_u32 m0,s59,5120
s_nop 0
buffer_load_dword v8,s[36:39],s58 offen offset:0 sc1 lds
buffer_store_dword v4,v3,s[36:39],s71 offen offset:0 sc0 sc1
s_mov_b32 s83,0
L 0
v_fma_f32 v30,-v78,v82,v30
v_fma_f32 v31,-v78,v83,v31
v_fma_f32 v32,-v78,v84,v32
v_fma_f32 v33,-v78,v85,v33
v_mul_f32 v94,v30,v30
v_mul_f32 v95,v31,v31
v_mul_f32 v96,v32,v32
v_mul_f32 v97,v33,v33
v_fma_f32 v94,v80,v94,v79
v_fma_f32 v95,v80,v95,v79
v_fma_f32 v96,v80,v96,v79
v_fma_f32 v97,v80,v97,v79
v_mul_f32 v94,v30,v94
v_mul_f32 v95,v31,v95
v_mul_f32 v96,v32,v96
v_mul_f32 v97,v33,v97
v_exp_f32 v94,v94
v_exp_f32 v95,v95
v_exp_f32 v96,v96
v_exp_f32 v97,v97
v_fma_f32 v94,v94,v81,v81
v_fma_f32 v95,v95,v81,v81
v_fma_f32 v96,v96,v81,v81
v_fma_f32 v97,v97,v81,v81
v_rcp_f32 v94,v94
v_rcp_f32 v95,v95
v_rcp_f32 v96,v96
v_rcp_f32 v97,v97
v_mul_f32 v30,v30,v94
v_mul_f32 v31,v31,v95
v_mul_f32 v32,v32,v96
v_mul_f32 v33,v33,v97
v_cvt_pk_bf16_f32 v106,v30,v31
v_cvt_pk_bf16_f32 v107,v32,v33
v_fma_f32 v34,-v78,v86,v34
v_fma_f32 v35,-v78,v87,v35
v_fma_f32 v36,-v78,v88,v36
v_fma_f32 v37,-v78,v89,v37
v_mul_f32 v98,v34,v34
v_mul_f32 v99,v35,v35
v_mul_f32 v100,v36,v36
v_mul_f32 v101,v37,v37
v_fma_f32 v98,v80,v98,v79
v_fma_f32 v99,v80,v99,v79
v_fma_f32 v100,v80,v100,v79
v_fma_f32 v101,v80,v101,v79
v_mul_f32 v98,v34,v98
v_mul_f32 v99,v35,v99
v_mul_f32 v100,v36,v100
v_mul_f32 v101,v37,v101
v_exp_f32 v98,v98
)"
R"(v_exp_f32 v99,v99
v_exp_f32 v100,v100
v_exp_f32 v101,v101
v_fma_f32 v98,v98,v81,v81
v_fma_f32 v99,v99,v81,v81
v_fma_f32 v100,v100,v81,v81
v_fma_f32 v101,v101,v81,v81
v_rcp_f32 v98,v98
v_rcp_f32 v99,v99
v_rcp_f32 v100,v100
v_rcp_f32 v101,v101
v_mul_f32 v34,v34,v98
v_mul_f32 v35,v35,v99
v_mul_f32 v36,v36,v100
v_mul_f32 v37,v37,v101
v_cvt_pk_bf16_f32 v108,v34,v35
v_cvt_pk_bf16_f32 v109,v36,v37
buffer_store_dwordx4 v[106:109],v0,s[36:39],s67 offen offset:0 sc0 sc1
v_fma_f32 v38,-v78,v90,v38
v_fma_f32 v39,-v78,v91,v39
v_fma_f32 v40,-v78,v92,v40
v_fma_f32 v41,-v78,v93,v41
v_mul_f32 v102,v38,v38
v_mul_f32 v103,v39,v39
v_mul_f32 v104,v40,v40
v_mul_f32 v105,v41,v41
v_fma_f32 v102,v80,v102,v79
v_fma_f32 v103,v80,v103,v79
v_fma_f32 v104,v80,v104,v79
v_fma_f32 v105,v80,v105,v79
v_mul_f32 v102,v38,v102
v_mul_f32 v103,v39,v103
v_mul_f32 v104,v40,v104
v_mul_f32 v105,v41,v105
v_exp_f32 v102,v102
v_exp_f32 v103,v103
v_exp_f32 v104,v104
v_exp_f32 v105,v105
v_fma_f32 v102,v102,v81,v81
v_fma_f32 v103,v103,v81,v81
v_fma_f32 v104,v104,v81,v81
v_fma_f32 v105,v105,v81,v81
v_rcp_f32 v102,v102
v_rcp_f32 v103,v103
v_rcp_f32 v104,v104
v_rcp_f32 v105,v105
v_mul_f32 v38,v38,v102
v_mul_f32 v39,v39,v103
v_mul_f32 v40,v40,v104
v_mul_f32 v41,v41,v105
v_cvt_pk_bf16_f32 v110,v38,v39
v_cvt_pk_bf16_f32 v111,v40,v41
s_nop 0
v_permlane16_swap_b32 v110,v112
v_permlane16_swap_b32 v111,v113
buffer_store_dwordx4 v[110:113],v10,s[36:39],s67 offen offset:0 sc0 sc1
s_mov_b32 s72,s69
s_mov_b32 s69,0x7fff0000
s_mov_b32 s67,0x7fff0000
s_mov_b32 s77,0x7fff0000
s_branch Lg3_poll_%=
Lg3_fin1_%=:
L 0
v_fma_f32 v30,-v78,v82,v30
v_fma_f32 v31,-v78,v83,v31
v_fma_f32 v32,-v78,v84,v32
v_fma_f32 v33,-v78,v85,v33
v_mul_f32 v94,v30,v30
v_mul_f32 v95,v31,v31
v_mul_f32 v96,v32,v32
v_mul_f32 v97,v33,v33
v_fma_f32 v94,v80,v94,v79
v_fma_f32 v95,v80,v95,v79
v_fma_f32 v96,v80,v96,v79
v_fma_f32 v97,v80,v97,v79
v_mul_f32 v94,v30,v94
v_mul_f32 v95,v31,v95
v_mul_f32 v96,v32,v96
v_mul_f32 v97,v33,v97
v_exp_f32 v94,v94
v_exp_f32 v95,v95
v_exp_f32 v96,v96
v_exp_f32 v97,v97
v_fma_f32 v94,v94,v81,v81
v_fma_f32 v95,v95,v81,v81
v_fma_f32 v96,v96,v81,v81
v_fma_f32 v97,v97,v81,v81
v_rcp_f32 v94,v94
v_rcp_f32 v95,v95
v_rcp_f32 v96,v96
v_rcp_f32 v97,v97
v_mul_f32 v30,v30,v94
v_mul_f32 v31,v31,v95
v_mul_f32 v32,v32,v96
v_mul_f32 v33,v33,v97
v_cvt_pk_bf16_f32 v106,v30,v31
v_cvt_pk_bf16_f32 v107,v32,v33
v_fma_f32 v34,-v78,v86,v34
v_fma_f32 v35,-v78,v87,v35
v_fma_f32 v36,-v78,v88,v36
v_fma_f32 v37,-v78,v89,v37
v_mul_f32 v98,v34,v34
v_mul_f32 v99,v35,v35
v_mul_f32 v100,v36,v36
v_mul_f32 v101,v37,v37
v_fma_f32 v98,v80,v98,v79
v_fma_f32 v99,v80,v99,v79
v_fma_f32 v100,v80,v100,v79
v_fma_f32 v101,v80,v101,v79
v_mul_f32 v98,v34,v98
v_mul_f32 v99,v35,v99
v_mul_f32 v100,v36,v100
v_mul_f32 v101,v37,v101
v_exp_f32 v98,v98
v_exp_f32 v99,v99
v_exp_f32 v100,v100
v_exp_f32 v101,v101
v_fma_f32 v98,v98,v81,v81
v_fma_f32 v99,v99,v81,v81
v_fma_f32 v100,v100,v81,v81
v_fma_f32 v101,v101,v81,v81
v_rcp_f32 v98,v98
v_rcp_f32 v99,v99
v_rcp_f32 v100,v100
v_rcp_f32 v101,v101
v_mul_f32 v34,v34,v98
v_mul_f32 v35,v35,v99
v_mul_f32 v36,v36,v100
v_mul_f32 v37,v37,v101
v_cvt_pk_bf16_f32 v108,v34,v35
v_cvt_pk_bf16_f32 v109,v36,v37
buffer_store_dwordx4 v[106:109],v0,s[36:39],s67 offen offset:0 sc0 sc1
v_fma_f32 v38,-v78,v90,v38
v_fma_f32 v39,-v78,v91,v39
v_fma_f32 v40,-v78,v92,v40
v_fma_f32 v41,-v78,v93,v41
v_mul_f32 v102,v38,v38
v_mul_f32 v103,v39,v39
v_mul_f32 v104,v40,v40
v_mul_f32 v105,v41,v41
v_fma_f32 v102,v80,v102,v79
v_fma_f32 v103,v80,v103,v79
v_fma_f32 v104,v80,v104,v79
v_fma_f32 v105,v80,v105,v79
v_mul_f32 v102,v38,v102
v_mul_f32 v103,v39,v103
v_mul_f32 v104,v40,v104
v_mul_f32 v105,v41,v105
v_exp_f32 v102,v102
v_exp_f32 v103,v103
v_exp_f32 v104,v104
v_exp_f32 v105,v105
v_fma_f32 v102,v102,v81,v81
v_fma_f32 v103,v103,v81,v81
v_fma_f32 v104,v104,v81,v81
v_fma_f32 v105,v105,v81,v81
v_rcp_f32 v102,v102
v_rcp_f32 v103,v103
v_rcp_f32 v104,v104
v_rcp_f32 v105,v105
v_mul_f32 v38,v38,v102
v_mul_f32 v39,v39,v103
v_mul_f32 v40,v40,v104
v_mul_f32 v41,v41,v105
v_cvt_pk_bf16_f32 v110,v38,v39
v_cvt_pk_bf16_f32 v111,v40,v41
s_nop 0
v_permlane16_swap_b32 v110,v112
v_permlane16_swap_b32 v111,v113
buffer_store_dwordx4 v[110:113],v10,s[36:39],s67 offen offset:0 sc0 sc1
s_mov_b32 s72,s69
s_mov_b32 s69,0x7fff0000
s_mov_b32 s67,0x7fff0000
s_mov_b32 s77,0x7fff0000
s_branch Lg3_done_%=
Lg3_done_%=:
s_waitcnt vmcnt(2)
s_barrier
buffer_store_dword v4,v3,s[36:39],s70 offen offset:0 sc0 sc1
s_waitcnt vmcnt(1)
s_barrier
buffer_store_dword v4,v3,s[36:39],s72 offen offset:0 sc0 sc1
s_waitcnt vmcnt(0) lgkmcnt(0)
s_barrier
s_endpgm
Lcode_g3_e:
.purgem M
.purgem R
.purgem T
.purgem D
.purgem L
)"
  :: "s"(ws), "s"(sWave), "s"(sG0), "s"(sStep), "s"(sOutOff), "s"(sFoutOff), "s"(sAuxOff), "s"(sWw), "s"(sBuf), "s"(sRk), "v"(vL16), "v"(vFlag), "v"(vSnapOff), "v"(vSnapRd), "v"(vWd0), "v"(vWd1), "v"(vWr0), "v"(vWr1), "v"(vWr2), "v"(vOutS), "v"(vAux0), "v"(vAux1), "v"(vAux2), "v"(vAux3), "v"(vAn), "v"(vAe), "s"(w3), "s"(out) : "v0", "v1", "v2", "v3", "v4", "v5", "v6", "v7", "v8", "v9", "v10", "v11", "v12", "v13", "v14", "v15", "v16", "v17", "v18", "v19", "v20", "v21", "v22", "v23", "v24", "v25", "v26", "v27", "v28", "v29", "v30", "v31", "v32", "v33", "v34", "v35", "v36", "v37", "v38", "v39", "v40", "v41", "v42", "v43", "v44", "v45", "v46", "v47", "v48", "v49", "v50", "v51", "v52", "v53", "v54", "v55", "v56", "v57", "v58", "v59", "v60", "v61", "v62", "v63", "v64", "v65", "v66", "v67", "v68", "v69", "v70", "v71", "v72", "v73", "v74", "v75", "v76", "v77", "v78", "v79", "v80", "v81", "v82", "v83", "v84", "v85", "v86", "v87", "v88", "v89", "v90", "v91", "v92", "v93", "v94", "v95", "v96", "v97", "v98", "v99", "v100", "v101", "v102", "v103", "v104", "v105", "v106", "v107", "v108", "v109", "v110", "v111", "v112", "v113", "v114", "v115", "v116", "v117", "v118", "v119", "v120", "v121", "v122", "v123", "v124", "v125", "v126", "v127", "v128", "v129", "v130", "v131", "v132", "v133", "v134", "v135", "v136", "v137", "v138", "v139", "v140", "v141", "v142", "v143", "v144", "v145", "v146", "v147", "v148", "v149", "v150", "v151", "v152", "v153", "v154", "v155", "v156", "v157", "v158", "v159", "v160", "v161", "v162", "v163", "v164", "v165", "v166", "v167", "v168", "v169", "v170", "v171", "v172", "v173", "v174", "v175", "v176", "v177", "v178", "v179", "v180", "v181", "v182", "v183", "v184", "v185", "v186", "v187", "v188", "v189", "v190", "v191", "v192", "v193", "v194", "v195", "v196", "v197", "v198", "v199", "v200", "v201", "v202", "v203", "v204", "v205", "v206", "v207", "v208", "v209", "v210", "v211", "v212", "v213", "v214", "v215", "v216", "v217", "v218", "v219", "v220", "v221", "v222", "v223", "v224", "v225", "v226", "v227", "v228", "v229", "v230", "v231", "v232", "v233", "v234", "v235", "v236", "v237", "v238", "v239", "a0", "a1", "a2", "a3", "a4", "a5", "a6", "a7", "a8", "a9", "a10", "a11", "a12", "a13", "a14", "a15", "a16", "a17", "a18", "a19", "a20", "a21", "a22", "a23", "a24", "a25", "a26", "a27", "a28", "a29", "a30", "a31", "a32", "a33", "a34", "a35", "a36", "a37", "a38", "a39", "a40", "a41", "a42", "a43", "a44", "a45", "a46", "a47", "a48", "a49", "a50", "a51", "a52", "a53", "a54", "a55", "a56", "a57", "a58", "a59", "a60", "a61", "a62", "a63", "a64", "a65", "a66", "a67", "a68", "a69", "a70", "a71", "a72", "a73", "a74", "a75", "a76", "a77", "a78", "a79", "a80", "a81", "a82", "a83", "a84", "a85", "a86", "a87", "a88", "a89", "a90", "a91", "a92", "a93", "a94", "a95", "a96", "a97", "a98", "a99", "a100", "a101", "a102", "a103", "a104", "a105", "a106", "a107", "a108", "a109", "a110", "a111", "a112", "a113", "a114", "a115", "a116", "a117", "a118", "a119", "a120", "a121", "a122", "a123", "a124", "a125", "a126", "a127", "a128", "a129", "a130", "a131", "a132", "a133", "a134", "a135", "a136", "a137", "a138", "a139", "a140", "a141", "a142", "a143", "a144", "a145", "a146", "a147", "a148", "a149", "a150", "a151", "a152", "a153", "a154", "a155", "a156", "a157", "a158", "a159", "a160", "a161", "a162", "a163", "a164", "a165", "a166", "a167", "a168", "a169", "a170", "a171", "a172", "a173", "a174", "a175", "a176", "a177", "a178", "a179", "a180", "a181", "a182", "a183", "a184", "a185", "a186", "a187", "a188", "a189", "a190", "a191", "a192", "a193", "a194", "a195", "a196", "a197", "a198", "a199", "a200", "a201", "a202", "a203", "a204", "a205", "a206", "a207", "a208", "a209", "a210", "a211", "a212", "a213", "a214", "a215", "a216", "a217", "a218", "a219", "a220", "a221", "a222", "a223", "a224", "a225", "a226", "a227", "a228", "a229", "a230", "a231", "a232", "a233", "a234", "a235", "a236", "a237", "a238", "a239", "a240", "a241", "a242", "a243", "a244", "a245", "a246", "a247", "a248", "a249", "a250", "a251", "a252", "a253", "a254", "a255", "s36", "s37", "s38", "s39", "s40", "s41", "s42", "s43", "s44", "s45", "s46", "s47", "s48", "s49", "s50", "s51", "s52", "s53", "s54", "s55", "s56", "s57", "s58", "s59", "s60", "s61", "s62", "s63", "s64", "s65", "s66", "s67", "s68", "s69", "s70", "s71", "s72", "s73", "s74", "s75", "s76", "s77", "s78", "s79", "s80", "s81", "s82", "s83", "s84", "s85", "s86", "s87", "s88", "s89", "s90", "s91", "s92", "s93", "s94", "s95", "s96", "s97", "vcc", "scc", "memory"); } else { asm volatile(
R"(.macro M d,w,x
v_mfma_f32_16x16x32_bf16 v[\d:\d+3], \w, v[\x:\x+3], v[\d:\d+3]
.endm
.macro R d,a,o
ds_read_b128 v[\d:\d+3], v\a offset:\o
.endm
.macro T d,a,o
ds_read_b64_tr_b16 \d, v\a offset:\o
.endm
.macro D a,s,o
buffer_load_dwordx4 v\a, s[36:39], s\s offen offset:\o sc1 lds
.endm
.macro L c
s_waitcnt lgkmcnt(\c)
.endm
Lcode_g3o_b:
s_mov_b64 s[36:37],%0
s_mov_b32 s38,111243392
s_mov_b32 s39,0x00020000
s_mov_b32 s44,%1
s_mov_b32 s45,%2
s_mov_b32 s46,%3
s_mov_b32 s47,%4
s_mov_b32 s48,%5
s_mov_b32 s49,%6
s_mov_b32 s50,%7
s_mov_b32 s51,%8
s_mov_b32 s93,%9
v_mov_b32 v0,%10
v_mov_b32 v3,%11
v_mov_b32 v5,%12
v_mov_b32 v6,%13
v_mov_b32 v18,%14
v_mov_b32 v19,%15
v_mov_b32 v20,%16
v_mov_b32 v21,%17
v_mov_b32 v22,%18
v_mov_b32 v10,%19
v_mov_b32 v11,%20
v_mov_b32 v12,%21
v_mov_b32 v13,%22
v_mov_b32 v14,%23
v_mov_b32 v15,%24
v_mov_b32 v16,%25
s_mov_b64 s[40:41],%26
s_mov_b32 s42,7340032
s_mov_b32 s43,0x00020000
s_lshl_b32 s57,s44,11
s_mov_b32 s79,111243264
buffer_load_dword v4,off,s[36:39],s79 sc1
s_add_u32 s55,s50,0
s_add_u32 m0,s57,0
s_nop 0
buffer_load_dwordx4 v18,s[40:43],s55 offen offset:0  lds
s_add_u32 m0,s57,1024
s_nop 0
buffer_load_dwordx4 v19,s[40:43],s55 offen offset:0  lds
s_add_u32 m0,s57,8064
s_nop 0
buffer_load_dwordx4 v18,s[40:43],s55 offen offset:128  lds
s_add_u32 m0,s57,9088
s_nop 0
buffer_load_dwordx4 v19,s[40:43],s55 offen offset:128  lds
s_add_u32 s55,s50,458752
s_add_u32 m0,s57,16384
s_nop 0
buffer_load_dwordx4 v18,s[40:43],s55 offen offset:0  lds
s_add_u32 m0,s57,17408
s_nop 0
buffer_load_dwordx4 v19,s[40:43],s55 offen offset:0  lds
s_add_u32 m0,s57,24448
s_nop 0
buffer_load_dwordx4 v18,s[40:43],s55 offen offset:128  lds
s_add_u32 m0,s57,25472
s_nop 0
buffer_load_dwordx4 v19,s[40:43],s55 offen offset:128  lds
s_add_u32 s55,s50,917504
s_add_u32 m0,s57,32768
s_nop 0
buffer_load_dwordx4 v18,s[40:43],s55 offen offset:0  lds
s_add_u32 m0,s57,33792
s_nop 0
buffer_load_dwordx4 v19,s[40:43],s55 offen offset:0  lds
s_add_u32 m0,s57,40832
s_nop 0
buffer_load_dwordx4 v18,s[40:43],s55 offen offset:128  lds
s_add_u32 m0,s57,41856
s_nop 0
buffer_load_dwordx4 v19,s[40:43],s55 offen offset:128  lds
s_add_u32 s55,s50,1376256
s_add_u32 m0,s57,49152
s_nop 0
buffer_load_dwordx4 v18,s[40:43],s55 offen offset:0  lds
s_add_u32 m0,s57,50176
s_nop 0
buffer_load_dwordx4 v19,s[40:43],s55 offen offset:0  lds
s_add_u32 m0,s57,57216
s_nop 0
buffer_load_dwordx4 v18,s[40:43],s55 offen offset:128  lds
s_add_u32 m0,s57,58240
s_nop 0
buffer_load_dwordx4 v19,s[40:43],s55 offen offset:128  lds
s_add_u32 s55,s50,1835008
s_add_u32 m0,s57,65536
s_nop 0
buffer_load_dwordx4 v18,s[40:43],s55 offen offset:0  lds
s_add_u32 m0,s57,66560
s_nop 0
buffer_load_dwordx4 v19,s[40:43],s55 offen offset:0  lds
s_add_u32 m0,s57,73600
s_nop 0
buffer_load_dwordx4 v18,s[40:43],s55 offen offset:128  lds
s_add_u32 m0,s57,74624
s_nop 0
buffer_load_dwordx4 v19,s[40:43],s55 offen offset:128  lds
s_add_u32 s55,s50,2293760
s_add_u32 m0,s57,81920
s_nop 0
buffer_load_dwordx4 v18,s[40:43],s55 offen offset:0  lds
s_add_u32 m0,s57,82944
s_nop 0
buffer_load_dwordx4 v19,s[40:43],s55 offen offset:0  lds
s_add_u32 m0,s57,89984
s_nop 0
buffer_load_dwordx4 v18,s[40:43],s55 offen offset:128  lds
s_add_u32 m0,s57,91008
s_nop 0
buffer_load_dwordx4 v19,s[40:43],s55 offen offset:128  lds
s_waitcnt vmcnt(24)
s_barrier
v_readfirstlane_b32 s53,v4
s_nop 3
s_add_u32 s53,s53,1
s_cmp_eq_u32 s53,0
s_cselect_b32 s53,1,s53
v_mov_b32 v4,s53
s_waitcnt vmcnt(20) lgkmcnt(0)
s_barrier
s_add_u32 s55,s50,2752512
s_add_u32 m0,s57,98304
s_nop 0
buffer_load_dwordx4 v18,s[40:43],s55 offen offset:0  lds
s_add_u32 m0,s57,99328
s_nop 0
buffer_load_dwordx4 v19,s[40:43],s55 offen offset:0  lds
s_add_u32 m0,s57,106368
s_nop 0
buffer_load_dwordx4 v18,s[40:43],s55 offen offset:128  lds
s_add_u32 m0,s57,107392
s_nop 0
buffer_load_dwordx4 v19,s[40:43],s55 offen offset:128  lds
v_add_u32 v26,0,v20
v_add_u32 v27,0,v21
T a[0:1],26,0
T a[2:3],26,512
T a[4:5],27,0
T a[6:7],27,512
T a[8:9],26,4096
T a[10:11],26,4608
T a[12:13],27,4096
T a[14:15],27,4608
s_waitcnt vmcnt(20) lgkmcnt(0)
s_barrier
s_add_u32 s55,s50,3211264
s_add_u32 m0,s57,0
s_nop 0
buffer_load_dwordx4 v18,s[40:43],s55 offen offset:0  lds
s_add_u32 m0,s57,1024
s_nop 0
buffer_load_dwordx4 v19,s[40:43],s55 offen offset:0  lds
s_add_u32 m0,s57,8064
s_nop 0
buffer_load_dwordx4 v18,s[40:43],s55 offen offset:128  lds
s_add_u32 m0,s57,9088
s_nop 0
buffer_load_dwordx4 v19,s[40:43],s55 offen offset:128  lds
v_add_u32 v26,16384,v20
v_add_u32 v27,16384,v21
T a[16:17],26,0
T a[18:19],26,512
T a[20:21],27,0
T a[22:23],27,512
T a[24:25],26,4096
T a[26:27],26,4608
T a[28:29],27,4096
T a[30:31],27,4608
s_waitcnt vmcnt(20) lgkmcnt(0)
s_barrier
s_add_u32 s55,s50,3670016
s_add_u32 m0,s57,16384
s_nop 0
buffer_load_dwordx4 v18,s[40:43],s55 offen offset:0  lds
s_add_u32 m0,s57,17408
s_nop 0
buffer_load_dwordx4 v19,s[40:43],s55 offen offset:0  lds
s_add_u32 m0,s57,24448
s_nop 0
buffer_load_dwordx4 v18,s[40:43],s55 offen offset:128  lds
s_add_u32 m0,s57,25472
s_nop 0
buffer_load_dwordx4 v19,s[40:43],s55 offen offset:128  lds
v_add_u32 v26,32768,v20
v_add_u32 v27,32768,v21
T a[32:33],26,0
T a[34:35],26,512
T a[36:37],27,0
T a[38:39],27,512
T a[40:41],26,4096
T a[42:43],26,4608
T a[44:45],27,4096
T a[46:47],27,4608
s_waitcnt vmcnt(20) lgkmcnt(0)
s_barrier
s_add_u32 s55,s50,4128768
s_add_u32 m0,s57,32768
s_nop 0
buffer_load_dwordx4 v18,s[40:43],s55 offen offset:0  lds
s_add_u32 m0,s57,33792
s_nop 0
buffer_load_dwordx4 v19,s[40:43],s55 offen offset:0  lds
s_add_u32 m0,s57,40832
s_nop 0
buffer_load_dwordx4 v18,s[40:43],s55 offen offset:128  lds
s_add_u32 m0,s57,41856
s_nop 0
buffer_load_dwordx4 v19,s[40:43],s55 offen offset:128  lds
v_add_u32 v26,49152,v20
v_add_u32 v27,49152,v21
T a[48:49],26,0
T a[50:51],26,512
T a[52:53],27,0
T a[54:55],27,512
T a[56:57],26,4096
T a[58:59],26,4608
T a[60:61],27,4096
T a[62:63],27,4608
s_waitcnt vmcnt(20) lgkmcnt(0)
s_barrier
s_add_u32 s55,s50,4587520
s_add_u32 m0,s57,49152
s_nop 0
buffer_load_dwordx4 v18,s[40:43],s55 offen offset:0  lds
s_add_u32 m0,s57,50176
s_nop 0
buffer_load_dwordx4 v19,s[40:43],s55 offen offset:0  lds
s_add_u32 m0,s57,57216
s_nop 0
buffer_load_dwordx4 v18,s[40:43],s55 offen offset:128  lds
s_add_u32 m0,s57,58240
s_nop 0
buffer_load_dwordx4 v19,s[40:43],s55 offen offset:128  lds
v_add_u32 v26,65536,v20
v_add_u32 v27,65536,v21
T a[64:65],26,0
T a[66:67],26,512
T a[68:69],27,0
T a[70:71],27,512
T a[72:73],26,4096
T a[74:75],26,4608
T a[76:77],27,4096
T a[78:79],27,4608
s_waitcnt vmcnt(20) lgkmcnt(0)
s_barrier
s_add_u32 s55,s50,5046272
s_add_u32 m0,s57,65536
s_nop 0
buffer_load_dwordx4 v18,s[40:43],s55 offen offset:0  lds
s_add_u32 m0,s57,66560
s_nop 0
buffer_load_dwordx4 v19,s[40:43],s55 offen offset:0  lds
s_add_u32 m0,s57,73600
s_nop 0
buffer_load_dwordx4 v18,s[40:43],s55 offen offset:128  lds
s_add_u32 m0,s57,74624
s_nop 0
buffer_load_dwordx4 v19,s[40:43],s55 offen offset:128  lds
v_add_u32 v26,81920,v20
v_add_u32 v27,81920,v21
T a[80:81],26,0
T a[82:83],26,512
T a[84:85],27,0
T a[86:87],27,512
T a[88:89],26,4096
T a[90:91],26,4608
T a[92:93],27,4096
T a[94:95],27,4608
s_waitcnt vmcnt(20) lgkmcnt(0)
s_barrier
s_add_u32 s55,s50,5505024
s_add_u32 m0,s57,81920
s_nop 0
buffer_load_dwordx4 v18,s[40:43],s55 offen offset:0  lds
s_add_u32 m0,s57,82944
s_nop 0
buffer_load_dwordx4 v19,s[40:43],s55 offen offset:0  lds
s_add_u32 m0,s57,89984
s_nop 0
buffer_load_dwordx4 v18,s[40:43],s55 offen offset:128  lds
s_add_u32 m0,s57,91008
s_nop 0
buffer_load_dwordx4 v19,s[40:43],s55 offen offset:128  lds
v_add_u32 v26,98304,v20
v_add_u32 v27,98304,v21
T a[96:97],26,0
T a[98:99],26,512
T a[100:101],27,0
T a[102:103],27,512
T a[104:105],26,4096
T a[106:107],26,4608
T a[108:109],27,4096
T a[110:111],27,4608
s_waitcnt vmcnt(20) lgkmcnt(0)
s_barrier
s_add_u32 s55,s50,5963776
s_add_u32 m0,s57,98304
s_nop 0
buffer_load_dwordx4 v18,s[40:43],s55 offen offset:0  lds
s_add_u32 m0,s57,99328
s_nop 0
buffer_load_dwordx4 v19,s[40:43],s55 offen offset:0  lds
s_add_u32 m0,s57,106368
s_nop 0
buffer_load_dwordx4 v18,s[40:43],s55 offen offset:128  lds
s_add_u32 m0,s57,107392
s_nop 0
buffer_load_dwordx4 v19,s[40:43],s55 offen offset:128  lds
v_add_u32 v26,0,v20
v_add_u32 v27,0,v21
T a[112:113],26,0
T a[114:115],26,512
T a[116:117],27,0
T a[118:119],27,512
T a[120:121],26,4096
T a[122:123],26,4608
T a[124:125],27,4096
T a[126:127],27,4608
s_waitcnt vmcnt(20) lgkmcnt(0)
s_barrier
s_add_u32 s55,s50,6422528
s_add_u32 m0,s57,0
s_nop 0
buffer_load_dwordx4 v18,s[40:43],s55 offen offset:0  lds
s_add_u32 m0,s57,1024
s_nop 0
buffer_load_dwordx4 v19,s[40:43],s55 offen offset:0  lds
s_add_u32 m0,s57,8064
s_nop 0
buffer_load_dwordx4 v18,s[40:43],s55 offen offset:128  lds
s_add_u32 m0,s57,9088
s_nop 0
buffer_load_dwordx4 v19,s[40:43],s55 offen offset:128  lds
v_add_u32 v26,16384,v20
v_add_u32 v27,16384,v21
T a[128:129],26,0
T a[130:131],26,512
T a[132:133],27,0
T a[134:135],27,512
T a[136:137],26,4096
T a[138:139],26,4608
T a[140:141],27,4096
T a[142:143],27,4608
s_waitcnt vmcnt(20) lgkmcnt(0)
s_barrier
s_add_u32 s55,s50,6881280
s_add_u32 m0,s57,16384
s_nop 0
buffer_load_dwordx4 v18,s[40:43],s55 offen offset:0  lds
s_add_u32 m0,s57,17408
s_nop 0
buffer_load_dwordx4 v19,s[40:43],s55 offen offset:0  lds
s_add_u32 m0,s57,24448
s_nop 0
buffer_load_dwordx4 v18,s[40:43],s55 offen offset:128  lds
s_add_u32 m0,s57,25472
s_nop 0
buffer_load_dwordx4 v19,s[40:43],s55 offen offset:128  lds
v_add_u32 v26,32768,v20
v_add_u32 v27,32768,v21
T a[144:145],26,0
T a[146:147],26,512
T a[148:149],27,0
T a[150:151],27,512
T a[152:153],26,4096
T a[154:155],26,4608
T a[156:157],27,4096
T a[158:159],27,4608
s_waitcnt vmcnt(20) lgkmcnt(0)
s_barrier
v_add_u32 v26,49152,v20
v_add_u32 v27,49152,v21
T a[160:161],26,0
T a[162:163],26,512
T a[164:165],27,0
T a[166:167],27,512
T a[168:169],26,4096
T a[170:171],26,4608
T a[172:173],27,4096
T a[174:175],27,4608
s_waitcnt vmcnt(16) lgkmcnt(0)
s_barrier
v_add_u32 v26,65536,v20
v_add_u32 v27,65536,v21
T a[176:177],26,0
T a[178:179],26,512
T a[180:181],27,0
T a[182:183],27,512
T a[184:185],26,4096
T a[186:187],26,4608
T a[188:189],27,4096
T a[190:191],27,4608
s_waitcnt vmcnt(12) lgkmcnt(0)
s_barrier
v_add_u32 v26,81920,v20
v_add_u32 v27,81920,v21
T a[192:193],26,0
T a[194:195],26,512
T a[196:197],27,0
T a[198:199],27,512
T a[200:201],26,4096
T a[202:203],26,4608
T a[204:205],27,4096
T a[206:207],27,4608
s_waitcnt vmcnt(8) lgkmcnt(0)
s_barrier
v_add_u32 v26,98304,v20
v_add_u32 v27,98304,v21
T a[208:209],26,0
T a[210:211],26,512
T a[212:213],27,0
T a[214:215],27,512
T a[216:217],26,4096
T a[218:219],26,4608
T a[220:221],27,4096
T a[222:223],27,4608
s_waitcnt vmcnt(4) lgkmcnt(0)
s_barrier
v_add_u32 v26,0,v20
v_add_u32 v27,0,v21
T a[224:225],26,0
T a[226:227],26,512
T a[228:229],27,0
T a[230:231],27,512
T a[232:233],26,4096
T a[234:235],26,4608
T a[236:237],27,4096
T a[238:239],27,4608
s_waitcnt vmcnt(0) lgkmcnt(0)
s_barrier
v_add_u32 v26,16384,v20
v_add_u32 v27,16384,v21
T a[240:241],26,0
T a[242:243],26,512
T a[244:245],27,0
T a[246:247],27,512
T a[248:249],26,4096
T a[250:251],26,4608
T a[252:253],27,4096
T a[254:255],27,4608
L 0
s_barrier
v_mov_b32 v34,0x3f803f80
v_mov_b32 v35,0x3f803f80
v_mov_b32 v36,0x3f803f80
v_mov_b32 v37,0x3f803f80
s_nop 1
v_mfma_f32_16x16x32_bf16 v[106:109],a[0:3],v[34:37],0
v_mfma_f32_16x16x32_bf16 v[110:113],a[4:7],v[34:37],0
M 106,a[8:11],34
M 110,a[12:15],34
M 106,a[16:19],34
M 110,a[20:23],34
M 106,a[24:27],34
M 110,a[28:31],34
M 106,a[32:35],34
M 110,a[36:39],34
M 106,a[40:43],34
M 110,a[44:47],34
M 106,a[48:51],34
M 110,a[52:55],34
M 106,a[56:59],34
M 110,a[60:63],34
M 106,a[64:67],34
M 110,a[68:71],34
M 106,a[72:75],34
M 110,a[76:79],34
M 106,a[80:83],34
M 110,a[84:87],34
M 106,a[88:91],34
M 110,a[92:95],34
M 106,a[96:99],34
M 110,a[100:103],34
M 106,a[104:107],34
M 110,a[108:111],34
M 106,a[112:115],34
M 110,a[116:119],34
M 106,a[120:123],34
M 110,a[124:127],34
M 106,a[128:131],34
M 110,a[132:135],34
M 106,a[136:139],34
)"
R"(M 110,a[140:143],34
M 106,a[144:147],34
M 110,a[148:151],34
M 106,a[152:155],34
M 110,a[156:159],34
M 106,a[160:163],34
M 110,a[164:167],34
M 106,a[168:171],34
M 110,a[172:175],34
M 106,a[176:179],34
M 110,a[180:183],34
M 106,a[184:187],34
M 110,a[188:191],34
M 106,a[192:195],34
M 110,a[196:199],34
M 106,a[200:203],34
M 110,a[204:207],34
M 106,a[208:211],34
M 110,a[212:215],34
M 106,a[216:219],34
M 110,a[220:223],34
M 106,a[224:227],34
M 110,a[228:231],34
M 106,a[232:235],34
M 110,a[236:239],34
M 106,a[240:243],34
M 110,a[244:247],34
M 106,a[248:251],34
M 110,a[252:255],34
v_mov_b32 v4,s53
s_mov_b32 s82,0
s_mov_b32 s80,0
s_mov_b32 s54,s45
s_mul_i32 s57,s44,4096
s_mul_i32 s55,s45,32768
s_add_u32 s55,s55,s57
s_add_u32 s55,s55,31457280
s_mul_i32 s57,s44,4096
s_mul_i32 s60,s46,32768
s_mul_i32 s61,s46,1024
s_mul_i32 s62,s46,114688
s_mul_i32 s63,s46,2560
s_mul_i32 s64,s46,1024
s_lshl_b32 s59,s44,8
s_add_u32 s59,s59,131072
s_mul_i32 s58,s45,1024
s_add_u32 s58,s58,109613056
s_mul_i32 s65,s45,114688
s_add_u32 s65,s65,s47
s_mul_i32 s68,s45,2560
s_add_u32 s68,s68,s48
s_mul_i32 s73,s45,1024
s_add_u32 s73,s73,107479040
s_mul_i32 s74,s45,32768
s_add_u32 s74,s74,s49
s_mul_i32 s75,s45,1024
s_add_u32 s75,s75,107479040
s_mov_b32 s66,0x7fff0000
s_mov_b32 s67,0x7fff0000
s_mov_b32 s69,0x7fff0000
s_mov_b32 s76,0x7fff0000
s_mov_b32 s77,0x7fff0000
s_mov_b32 s70,0x7fff0000
s_mov_b32 s71,0x7fff0000
s_mov_b32 s72,0x7fff0000
v_mov_b32 v1,v0
v_add_u32 v2,16384,v0
s_mov_b32 s84,16384
s_mov_b32 s86,0x3a800000
s_mov_b32 s87,0x3727c5ac
s_mov_b32 s88,0xc0135761
s_mov_b32 s89,0x3d372713
s_and_b32 s85,s44,1
s_mul_i32 s85,s85,127
v_lshlrev_b32 v8,22,v0
v_and_b32 v8,0x80000000,v8
v_or_b32 v8,v8,v5
v_add_u32 v2,32768,v0
s_lshl_b32 s84,s57,1
s_add_u32 s84,s84,32768
s_getpc_b64 s[40:41]
Lg3o_pc_%=:
s_lshl_b32 s78,s44,13
v_lshlrev_b32 v114,3,v0
v_add_u32 v114,s78,v114
v_min_u32 v114,Lcode_g3o_e-Lg3o_pc_%=-4,v114
s_mov_b32 m0,163584
s_nop 0
global_load_lds_dword v114,s[40:41]
s_add_u32 m0,s59,4096
s_nop 0
buffer_load_dword v8,s[36:39],s58 offen offset:0 sc1 lds
buffer_store_dword v4,v3,s[36:39],s71 offen offset:0 sc0 sc1
s_add_u32 m0,s59,5120
s_nop 0
buffer_load_dword v8,s[36:39],s58 offen offset:0 sc1 lds
buffer_store_dword v4,v3,s[36:39],s71 offen offset:0 sc0 sc1
s_mov_b32 s83,0
Lg3o_poll_%=:
s_add_u32 s78,s83,2
s_and_b32 s78,s78,3
s_lshl_b32 s78,s78,10
s_add_u32 s78,s78,4096
s_add_u32 m0,s59,s78
s_nop 0
buffer_load_dword v8,s[36:39],s58 offen offset:0 sc1 lds
s_and_b32 s78,s83,3
s_lshl_b32 s78,s78,10
v_add_u32 v114,s78,v6
s_waitcnt vmcnt(4)
s_barrier
ds_read_b32 v7,v114 offset:4096
buffer_store_dword v4,v3,s[36:39],s70 offen offset:0 sc0 sc1
s_mov_b32 s70,s71
s_mov_b32 s71,s72
s_mov_b32 s72,0x7fff0000
s_add_u32 s83,s83,1
s_add_u32 s82,s82,1
L 0
v_cmp_eq_u32 vcc,s53,v7
s_nop 0
s_cmp_eq_u32 vcc_lo,-1
s_cbranch_scc1 Lg3o_go_%=
s_cmp_lt_u32 s82,20000
s_cbranch_scc1 Lg3o_poll_%=
Lg3o_go_%=:
buffer_load_dword v130,v11,s[36:39],s73 offen offset:0 sc1
s_add_u32 s73,s73,s64
s_add_u32 m0,s57,0
s_add_u32 s56,s55,0
D 0,56,0
D 0,56,1024
D 0,56,2048
D 0,56,3072
s_add_u32 s58,s58,s61
s_add_u32 m0,s59,2048
s_nop 0
buffer_load_dword v8,s[36:39],s58 offen offset:0 sc1 lds
s_add_u32 m0,s57,16384
s_add_u32 s56,s55,16384
D 0,56,0
D 0,56,1024
D 0,56,2048
D 0,56,3072
s_add_u32 s55,s55,s60
s_sub_u32 s57,s84,s57
s_add_u32 s54,s54,s46
s_waitcnt vmcnt(4)
s_barrier
buffer_store_dword v4,v3,s[36:39],s70 offen offset:0 sc0 sc1
s_min_u32 s71,s71,s72
buffer_store_dword v4,v3,s[36:39],s71 offen offset:0 sc0 sc1
s_mov_b32 s70,0x7fff0000
s_mov_b32 s71,0x7fff0000
s_mov_b32 s72,0x7fff0000
s_cmp_eq_u32 s80,1
s_cbranch_scc1 Lg3o_st1_%=
s_branch Lg3o_st0_%=
Lg3o_gox_%=:
s_add_u32 s58,s58,s61
s_add_u32 m0,s59,2048
s_nop 0
buffer_load_dword v8,s[36:39],s58 offen offset:0 sc1 lds
s_waitcnt vmcnt(0)
s_barrier
buffer_store_dword v4,v3,s[36:39],s70 offen offset:0 sc0 sc1
s_min_u32 s71,s71,s72
buffer_store_dword v4,v3,s[36:39],s71 offen offset:0 sc0 sc1
s_mov_b32 s70,0x7fff0000
s_mov_b32 s71,0x7fff0000
s_mov_b32 s72,0x7fff0000
s_cmp_eq_u32 s80,1
s_cbranch_scc1 Lg3o_st1_%=
Lg3o_st0_%=:
ds_read_b32 v7,v6 offset:2048
R 34,1,0
R 38,1,1024
R 42,1,2048
R 46,1,3072
R 50,1,4096
R 54,1,5120
R 58,1,6144
L 7
v_cmp_eq_u32 vcc,s53,v7
L 5
s_cmp_eq_u32 vcc_lo,-1
s_cbranch_scc1 Lg3o_F0_%=
s_branch Lg3o_L0_%=
Lg3o_st1_%=:
ds_read_b32 v7,v6 offset:2048
R 34,2,0
R 38,2,1024
R 42,2,2048
R 46,2,3072
R 50,2,4096
R 54,2,5120
R 58,2,6144
L 7
v_cmp_eq_u32 vcc,s53,v7
L 5
s_cmp_eq_u32 vcc_lo,-1
s_cbranch_scc1 Lg3o_F1_%=
s_branch Lg3o_L1_%=
Lg3o_F0_%=:
v_mfma_f32_16x16x32_bf16 v[22:25],a[4:7],v[34:37],0
s_add_u32 m0,s57,0
R 62,1,7168
v_add_f32_dpp v131,v130,v130 quad_perm:[1,0,3,2] row_mask:0xf bank_mask:0xf
M 22,a[12:15],38
s_add_u32 s56,s55,0
buffer_load_dword v130,v11,s[36:39],s73 offen offset:0 sc1
v_mfma_f32_16x16x32_bf16 v[18:21],a[0:3],v[34:37],0
s_add_u32 s58,s58,s61
R 66,1,8192
v_add_f32_dpp v131,v131,v131 quad_perm:[2,3,0,1] row_mask:0xf bank_mask:0xf
M 18,a[8:11],38
L 5
D 0,56,0
M 18,a[16:19],42
s_mov_b32 s66,s65
R 70,1,9216
v_add_f32_dpp v134,v131,v131 row_half_mirror row_mask:0xf bank_mask:0xf
M 18,a[24:27],46
s_add_u32 s65,s65,s62
v_fma_f32 v26,-v102,v106,v26
v_fma_f32 v27,-v102,v107,v27
M 22,a[20:23],42
s_add_u32 s54,s54,s46
R 74,1,10240
v_mov_b32_dpp v131,v134 row_ror:8 row_mask:0xf bank_mask:0xf
M 22,a[28:31],46
L 5
v_fma_f32 v131,-v134,v134,v131
v_max_f32 v131,s87,v131
M 22,a[36:39],50
s_add_u32 s73,s73,s64
R 78,1,11264
v_fma_f32 v28,-v102,v108,v28
M 22,a[44:47],54
D 0,56,1024
M 18,a[32:35],50
R 82,1,12288
v_fma_f32 v29,-v102,v109,v29
M 18,a[40:43],54
L 5
v_rsq_f32 v132,v131
M 18,a[48:51],58
R 86,1,13312
v_mul_f32 v137,v131,v132
M 18,a[56:59],62
v_mul_f32 v135,s88,v132
v_mul_f32 v131,s89,v132
M 22,a[52:55],58
R 90,1,14336
v_mul_f32 v136,v135,v132
M 22,a[60:63],62
L 5
v_mul_f32 v136,v136,v131
v_mul_f32 v114,v26,v26
M 22,a[68:71],66
s_waitcnt vmcnt(4)
R 94,1,15360
v_mul_f32 v115,v27,v27
M 22,a[76:79],70
s_barrier
D 0,56,2048
M 18,a[64:67],66
R 34,1,16384
v_mul_f32 v116,v28,v28
M 18,a[72:75],70
L 5
ds_write_b128 v12,v[134:137] offset:0
M 18,a[80:83],74
R 38,1,17408
v_mul_f32 v117,v29,v29
M 18,a[88:91],78
v_fma_f32 v114,v104,v114,v103
v_fma_f32 v115,v104,v115,v103
M 22,a[84:87],74
R 42,1,18432
v_fma_f32 v116,v104,v116,v103
M 22,a[92:95],78
L 6
D 0,56,3072
M 22,a[100:103],82
s_add_u32 m0,s59,0
R 46,1,19456
v_fma_f32 v117,v104,v117,v103
M 22,a[108:111],86
s_add_u32 s56,s55,16384
v_mul_f32 v114,v26,v114
v_mul_f32 v115,v27,v115
M 18,a[96:99],82
s_add_u32 s55,s55,s60
R 50,1,20480
v_mul_f32 v116,v28,v116
M 18,a[104:107],86
L 6
v_exp_f32 v114,v114
M 18,a[112:115],90
R 54,1,21504
v_mul_f32 v117,v29,v117
M 18,a[120:123],94
v_exp_f32 v115,v115
M 22,a[116:119],90
R 58,1,22528
v_fma_f32 v114,v114,v105,v105
M 22,a[124:127],94
L 5
buffer_load_dword v8,s[36:39],s58 offen offset:0 sc1 lds
M 22,a[132:135],34
s_add_u32 m0,s57,16384
R 62,1,23552
v_fma_f32 v115,v115,v105,v105
M 22,a[140:143],38
s_sub_u32 s57,s84,s57
D 0,56,0
M 18,a[128:131],34
R 66,1,24576
v_fma_f32 v30,-v102,v110,v30
M 18,a[136:139],38
L 5
v_exp_f32 v116,v116
M 18,a[144:147],42
R 70,1,25600
v_fma_f32 v116,v116,v105,v105
M 18,a[152:155],46
v_exp_f32 v117,v117
M 22,a[148:151],42
R 74,1,26624
v_fma_f32 v117,v117,v105,v105
M 22,a[156:159],46
L 5
v_rcp_f32 v114,v114
M 22,a[164:167],50
R 78,1,27648
v_mul_f32 v26,v26,v114
M 22,a[172:175],54
D 0,56,1024
M 18,a[160:163],50
R 82,1,28672
v_fma_f32 v31,-v102,v111,v31
M 18,a[168:171],54
L 5
v_rcp_f32 v115,v115
M 18,a[176:179],58
R 86,1,29696
v_mul_f32 v27,v27,v115
M 18,a[184:187],62
v_rcp_f32 v116,v116
M 22,a[180:183],58
R 90,1,30720
v_mul_f32 v28,v28,v116
M 22,a[188:191],62
L 5
s_nop 0
D 0,56,2048
M 22,a[196:199],66
s_waitcnt vmcnt(3)
R 94,1,31744
v_cvt_pk_bf16_f32 v122,v26,v27
M 22,a[204:207],70
s_barrier
buffer_store_dword v4,v3,s[36:39],s70 offen offset:0 sc0 sc1
M 18,a[192:195],66
s_mov_b32 s70,s69
R 34,2,0
ds_read_b32 v7,v6 offset:0
M 18,a[200:203],70
L 6
R 98,13,0
v_fma_f32 v32,-v102,v112,v32
M 18,a[208:211],74
s_mov_b32 s69,s68
R 38,2,1024
v_fma_f32 v33,-v102,v113,v33
M 18,a[216:219],78
s_add_u32 s68,s68,s63
v_mul_f32 v118,v30,v30
v_mul_f32 v119,v31,v31
M 22,a[212:215],74
R 42,2,2048
v_mul_f32 v120,v32,v32
M 22,a[220:223],78
L 7
D 0,56,3072
M 22,a[228:231],82
L 3
R 46,2,3072
v_mul_f32 v121,v33,v33
M 22,a[236:239],86
v_fma_f32 v118,v104,v118,v103
v_fma_f32 v119,v104,v119,v103
M 18,a[224:227],82
R 50,2,4096
v_fma_f32 v120,v104,v120,v103
M 18,a[232:235],86
s_nop 0
v_fma_f32 v121,v104,v121,v103
v_mul_f32 v118,v30,v118
v_mul_f32 v119,v31,v119
v_mul_f32 v120,v32,v120
v_mul_f32 v121,v33,v121
v_exp_f32 v118,v118
v_exp_f32 v119,v119
v_exp_f32 v120,v120
v_exp_f32 v121,v121
M 18,a[240:243],90
R 54,2,5120
v_rcp_f32 v117,v117
v_fma_f32 v118,v118,v105,v105
v_fma_f32 v119,v119,v105,v105
v_fma_f32 v120,v120,v105,v105
v_fma_f32 v121,v121,v105,v105
v_rcp_f32 v118,v118
v_rcp_f32 v119,v119
v_rcp_f32 v120,v120
v_rcp_f32 v121,v121
M 18,a[248:251],94
v_mul_f32 v29,v29,v117
v_cvt_pk_bf16_f32 v123,v28,v29
v_mul_f32 v30,v30,v118
v_mul_f32 v31,v31,v119
v_mul_f32 v32,v32,v120
v_mul_f32 v33,v33,v121
v_cvt_pk_bf16_f32 v124,v30,v31
v_cvt_pk_bf16_f32 v125,v32,v33
v_cmp_eq_u32 vcc,s53,v7
M 22,a[244:247],90
s_cmp_eq_u32 vcc_lo,-1
R 58,2,6144
buffer_store_dwordx4 v[122:125],v0,s[36:39],s67 offen offset:0 sc0 sc1
M 22,a[252:255],94
L 5
s_cbranch_scc0 Lg3o_L1_%=
Lg3o_F1_%=:
v_mfma_f32_16x16x32_bf16 v[30:33],a[4:7],v[34:37],0
s_add_u32 m0,s57,0
R 62,2,7168
v_add_f32_dpp v131,v130,v130 quad_perm:[1,0,3,2] row_mask:0xf bank_mask:0xf
M 30,a[12:15],38
s_add_u32 s56,s55,0
buffer_load_dword v130,v11,s[36:39],s73 offen offset:0 sc1
v_mfma_f32_16x16x32_bf16 v[26:29],a[0:3],v[34:37],0
s_add_u32 s58,s58,s61
R 66,2,8192
v_add_f32_dpp v131,v131,v131 quad_perm:[2,3,0,1] row_mask:0xf bank_mask:0xf
M 26,a[8:11],38
L 5
D 0,56,0
M 26,a[16:19],42
s_mov_b32 s67,s65
R 70,2,9216
v_add_f32_dpp v134,v131,v131 row_half_mirror row_mask:0xf bank_mask:0xf
M 26,a[24:27],46
s_add_u32 s65,s65,s62
v_fma_f32 v18,-v98,v106,v18
v_fma_f32 v19,-v98,v107,v19
M 30,a[20:23],42
s_add_u32 s54,s54,s46
R 74,2,10240
v_mov_b32_dpp v131,v134 row_ror:8 row_mask:0xf bank_mask:0xf
M 30,a[28:31],46
L 5
v_fma_f32 v131,-v134,v134,v131
v_max_f32 v131,s87,v131
M 30,a[36:39],50
s_add_u32 s73,s73,s64
R 78,2,11264
v_fma_f32 v20,-v98,v108,v20
M 30,a[44:47],54
D 0,56,1024
M 26,a[32:35],50
R 82,2,12288
v_fma_f32 v21,-v98,v109,v21
M 26,a[40:43],54
L 5
v_rsq_f32 v132,v131
M 26,a[48:51],58
R 86,2,13312
v_mul_f32 v137,v131,v132
M 26,a[56:59],62
v_mul_f32 v135,s88,v132
v_mul_f32 v131,s89,v132
M 30,a[52:55],58
R 90,2,14336
v_mul_f32 v136,v135,v132
M 30,a[60:63],62
L 5
v_mul_f32 v136,v136,v131
v_mul_f32 v114,v18,v18
M 30,a[68:71],66
s_waitcnt vmcnt(4)
R 94,2,15360
v_mul_f32 v115,v19,v19
M 30,a[76:79],70
s_barrier
D 0,56,2048
M 26,a[64:67],66
R 34,2,16384
)"
R"(v_mul_f32 v116,v20,v20
M 26,a[72:75],70
L 5
ds_write_b128 v12,v[134:137] offset:2048
M 26,a[80:83],74
R 38,2,17408
v_mul_f32 v117,v21,v21
M 26,a[88:91],78
v_fma_f32 v114,v100,v114,v99
v_fma_f32 v115,v100,v115,v99
M 30,a[84:87],74
R 42,2,18432
v_fma_f32 v116,v100,v116,v99
M 30,a[92:95],78
L 6
D 0,56,3072
M 30,a[100:103],82
s_add_u32 m0,s59,1024
R 46,2,19456
v_fma_f32 v117,v100,v117,v99
M 30,a[108:111],86
s_add_u32 s56,s55,16384
v_mul_f32 v114,v18,v114
v_mul_f32 v115,v19,v115
M 26,a[96:99],82
s_add_u32 s55,s55,s60
R 50,2,20480
v_mul_f32 v116,v20,v116
M 26,a[104:107],86
L 6
v_exp_f32 v114,v114
M 26,a[112:115],90
R 54,2,21504
v_mul_f32 v117,v21,v117
M 26,a[120:123],94
v_exp_f32 v115,v115
M 30,a[116:119],90
R 58,2,22528
v_fma_f32 v114,v114,v101,v101
M 30,a[124:127],94
L 5
buffer_load_dword v8,s[36:39],s58 offen offset:0 sc1 lds
M 30,a[132:135],34
s_add_u32 m0,s57,16384
R 62,2,23552
v_fma_f32 v115,v115,v101,v101
M 30,a[140:143],38
s_sub_u32 s57,s84,s57
D 0,56,0
M 26,a[128:131],34
R 66,2,24576
v_fma_f32 v22,-v98,v110,v22
M 26,a[136:139],38
L 5
v_exp_f32 v116,v116
M 26,a[144:147],42
R 70,2,25600
v_fma_f32 v116,v116,v101,v101
M 26,a[152:155],46
v_exp_f32 v117,v117
M 30,a[148:151],42
R 74,2,26624
v_fma_f32 v117,v117,v101,v101
M 30,a[156:159],46
L 5
v_rcp_f32 v114,v114
M 30,a[164:167],50
R 78,2,27648
v_mul_f32 v18,v18,v114
M 30,a[172:175],54
D 0,56,1024
M 26,a[160:163],50
R 82,2,28672
v_fma_f32 v23,-v98,v111,v23
M 26,a[168:171],54
L 5
v_rcp_f32 v115,v115
M 26,a[176:179],58
R 86,2,29696
v_mul_f32 v19,v19,v115
M 26,a[184:187],62
v_rcp_f32 v116,v116
M 30,a[180:183],58
R 90,2,30720
v_mul_f32 v20,v20,v116
M 30,a[188:191],62
L 5
s_nop 0
D 0,56,2048
M 30,a[196:199],66
s_waitcnt vmcnt(3)
R 94,2,31744
v_cvt_pk_bf16_f32 v122,v18,v19
M 30,a[204:207],70
s_barrier
buffer_store_dword v4,v3,s[36:39],s70 offen offset:0 sc0 sc1
M 26,a[192:195],66
s_mov_b32 s70,s69
R 34,1,0
ds_read_b32 v7,v6 offset:1024
M 26,a[200:203],70
L 6
R 102,13,2048
v_fma_f32 v24,-v98,v112,v24
M 26,a[208:211],74
s_mov_b32 s69,s68
R 38,1,1024
v_fma_f32 v25,-v98,v113,v25
M 26,a[216:219],78
s_add_u32 s68,s68,s63
v_mul_f32 v118,v22,v22
v_mul_f32 v119,v23,v23
M 30,a[212:215],74
R 42,1,2048
v_mul_f32 v120,v24,v24
M 30,a[220:223],78
L 7
D 0,56,3072
M 30,a[228:231],82
L 3
R 46,1,3072
v_mul_f32 v121,v25,v25
M 30,a[236:239],86
v_fma_f32 v118,v100,v118,v99
v_fma_f32 v119,v100,v119,v99
M 26,a[224:227],82
R 50,1,4096
v_fma_f32 v120,v100,v120,v99
M 26,a[232:235],86
s_nop 0
v_fma_f32 v121,v100,v121,v99
v_mul_f32 v118,v22,v118
v_mul_f32 v119,v23,v119
v_mul_f32 v120,v24,v120
v_mul_f32 v121,v25,v121
v_exp_f32 v118,v118
v_exp_f32 v119,v119
v_exp_f32 v120,v120
v_exp_f32 v121,v121
M 26,a[240:243],90
R 54,1,5120
v_rcp_f32 v117,v117
v_fma_f32 v118,v118,v101,v101
v_fma_f32 v119,v119,v101,v101
v_fma_f32 v120,v120,v101,v101
v_fma_f32 v121,v121,v101,v101
v_rcp_f32 v118,v118
v_rcp_f32 v119,v119
v_rcp_f32 v120,v120
v_rcp_f32 v121,v121
M 26,a[248:251],94
v_mul_f32 v21,v21,v117
v_cvt_pk_bf16_f32 v123,v20,v21
v_mul_f32 v22,v22,v118
v_mul_f32 v23,v23,v119
v_mul_f32 v24,v24,v120
v_mul_f32 v25,v25,v121
v_cvt_pk_bf16_f32 v124,v22,v23
v_cvt_pk_bf16_f32 v125,v24,v25
v_cmp_eq_u32 vcc,s53,v7
M 30,a[244:247],90
s_cmp_eq_u32 vcc_lo,-1
R 58,1,6144
buffer_store_dwordx4 v[122:125],v0,s[36:39],s66 offen offset:0 sc0 sc1
M 30,a[252:255],94
L 5
s_cbranch_scc1 Lg3o_F0_%=
Lg3o_L0_%=:
v_mfma_f32_16x16x32_bf16 v[22:25],a[4:7],v[34:37],0
s_add_u32 m0,s59,3072
R 62,1,7168
v_add_f32_dpp v131,v130,v130 quad_perm:[1,0,3,2] row_mask:0xf bank_mask:0xf
M 22,a[12:15],38
s_mov_b32 s66,s65
buffer_load_dword v8,s[36:39],s58 offen offset:0 sc1 lds
v_mfma_f32_16x16x32_bf16 v[18:21],a[0:3],v[34:37],0
s_add_u32 s65,s65,s62
R 66,1,8192
v_add_f32_dpp v131,v131,v131 quad_perm:[2,3,0,1] row_mask:0xf bank_mask:0xf
M 18,a[8:11],38
L 5
v_fma_f32 v26,-v102,v106,v26
v_fma_f32 v27,-v102,v107,v27
M 18,a[16:19],42
R 70,1,9216
v_add_f32_dpp v134,v131,v131 row_half_mirror row_mask:0xf bank_mask:0xf
M 18,a[24:27],46
v_fma_f32 v28,-v102,v108,v28
v_fma_f32 v29,-v102,v109,v29
M 22,a[20:23],42
R 74,1,10240
v_mov_b32_dpp v131,v134 row_ror:8 row_mask:0xf bank_mask:0xf
M 22,a[28:31],46
L 5
v_fma_f32 v131,-v134,v134,v131
v_max_f32 v131,s87,v131
M 22,a[36:39],50
R 78,1,11264
v_mul_f32 v114,v26,v26
M 22,a[44:47],54
v_rsq_f32 v132,v131
M 18,a[32:35],50
R 82,1,12288
v_mul_f32 v137,v131,v132
M 18,a[40:43],54
L 5
v_mul_f32 v135,s88,v132
v_mul_f32 v131,s89,v132
M 18,a[48:51],58
R 86,1,13312
v_mul_f32 v136,v135,v132
M 18,a[56:59],62
v_mul_f32 v136,v136,v131
v_mul_f32 v115,v27,v27
M 22,a[52:55],58
R 90,1,14336
v_mul_f32 v116,v28,v28
M 22,a[60:63],62
L 5
ds_write_b128 v12,v[134:137] offset:0
M 22,a[68:71],66
s_waitcnt vmcnt(0)
R 94,1,15360
v_mul_f32 v117,v29,v29
M 22,a[76:79],70
s_barrier
ds_read_b32 v7,v6 offset:3072
v_fma_f32 v114,v104,v114,v103
M 18,a[64:67],66
R 34,1,16384
v_fma_f32 v115,v104,v115,v103
M 18,a[72:75],70
L 7
buffer_store_dword v4,v3,s[36:39],s70 offen offset:0 sc0 sc1
M 18,a[80:83],74
s_mov_b32 s70,0x7fff0000
R 38,1,17408
v_fma_f32 v116,v104,v116,v103
M 18,a[88:91],78
v_fma_f32 v117,v104,v117,v103
v_mul_f32 v114,v26,v114
M 22,a[84:87],74
R 42,1,18432
v_mul_f32 v115,v27,v115
M 22,a[92:95],78
L 7
L 3
v_cmp_eq_u32 vcc,s53,v7
M 22,a[100:103],82
R 46,1,19456
v_mul_f32 v116,v28,v116
M 22,a[108:111],86
s_cmp_eq_u32 vcc_lo,-1
v_exp_f32 v114,v114
s_cbranch_scc0 Lg3o_X0_%=
M 18,a[96:99],82
s_add_u32 m0,s57,0
s_add_u32 s56,s55,0
s_add_u32 s58,s58,s61
R 50,1,20480
M 18,a[104:107],86
s_nop 0
buffer_load_dword v130,v11,s[36:39],s73 offen offset:0 sc1
M 18,a[112:115],90
s_add_u32 s54,s54,s46
R 54,1,21504
v_mul_f32 v117,v29,v117
M 18,a[120:123],94
s_add_u32 s73,s73,s64
D 0,56,0
M 22,a[116:119],90
R 58,1,22528
v_fma_f32 v114,v114,v105,v105
M 22,a[124:127],94
L 5
v_exp_f32 v115,v115
M 22,a[132:135],34
R 62,1,23552
v_fma_f32 v115,v115,v105,v105
M 22,a[140:143],38
D 0,56,1024
M 18,a[128:131],34
R 66,1,24576
v_fma_f32 v30,-v102,v110,v30
M 18,a[136:139],38
L 5
v_exp_f32 v116,v116
M 18,a[144:147],42
R 70,1,25600
v_fma_f32 v116,v116,v105,v105
M 18,a[152:155],46
D 0,56,2048
M 22,a[148:151],42
R 74,1,26624
v_fma_f32 v31,-v102,v111,v31
M 22,a[156:159],46
L 5
v_exp_f32 v117,v117
M 22,a[164:167],50
R 78,1,27648
v_fma_f32 v117,v117,v105,v105
M 22,a[172:175],54
D 0,56,3072
M 18,a[160:163],50
s_add_u32 m0,s59,0
R 82,1,28672
v_fma_f32 v32,-v102,v112,v32
M 18,a[168:171],54
L 5
buffer_load_dword v8,s[36:39],s58 offen offset:0 sc1 lds
M 18,a[176:179],58
s_add_u32 m0,s57,16384
R 86,1,29696
v_fma_f32 v33,-v102,v113,v33
M 18,a[184:187],62
s_add_u32 s56,s55,16384
v_rcp_f32 v114,v114
M 22,a[180:183],58
s_add_u32 s55,s55,s60
R 90,1,30720
v_mul_f32 v26,v26,v114
M 22,a[188:191],62
L 5
s_nop 0
D 0,56,0
M 22,a[196:199],66
s_waitcnt vmcnt(1)
R 94,1,31744
v_mul_f32 v118,v30,v30
M 22,a[204:207],70
s_barrier
buffer_store_dword v4,v3,s[36:39],s70 offen offset:0 sc0 sc1
M 18,a[192:195],66
s_mov_b32 s70,s69
R 34,2,0
ds_read_b32 v7,v6 offset:0
M 18,a[200:203],70
L 6
D 0,56,1024
M 18,a[208:211],74
s_mov_b32 s69,s68
R 38,2,1024
R 98,13,0
M 18,a[216:219],78
s_sub_u32 s57,s84,s57
v_mul_f32 v119,v31,v31
v_mul_f32 v120,v32,v32
M 22,a[212:215],74
s_add_u32 s68,s68,s63
R 42,2,2048
v_mul_f32 v121,v33,v33
M 22,a[220:223],78
L 7
D 0,56,2048
M 22,a[228:231],82
L 3
R 46,2,3072
v_fma_f32 v118,v104,v118,v103
M 22,a[236:239],86
v_fma_f32 v119,v104,v119,v103
v_fma_f32 v120,v104,v120,v103
M 18,a[224:227],82
R 50,2,4096
v_fma_f32 v121,v104,v121,v103
M 18,a[232:235],86
s_nop 0
v_mul_f32 v118,v30,v118
v_mul_f32 v119,v31,v119
v_mul_f32 v120,v32,v120
v_mul_f32 v121,v33,v121
v_exp_f32 v118,v118
v_exp_f32 v119,v119
v_exp_f32 v120,v120
v_exp_f32 v121,v121
M 18,a[240:243],90
R 54,2,5120
v_rcp_f32 v115,v115
v_rcp_f32 v116,v116
v_rcp_f32 v117,v117
v_fma_f32 v118,v118,v105,v105
v_fma_f32 v119,v119,v105,v105
v_fma_f32 v120,v120,v105,v105
v_fma_f32 v121,v121,v105,v105
v_rcp_f32 v118,v118
v_rcp_f32 v119,v119
v_rcp_f32 v120,v120
v_rcp_f32 v121,v121
M 18,a[248:251],94
v_mul_f32 v27,v27,v115
v_mul_f32 v28,v28,v116
v_mul_f32 v29,v29,v117
v_cvt_pk_bf16_f32 v122,v26,v27
v_cvt_pk_bf16_f32 v123,v28,v29
v_mul_f32 v30,v30,v118
v_mul_f32 v31,v31,v119
v_mul_f32 v32,v32,v120
v_mul_f32 v33,v33,v121
v_cvt_pk_bf16_f32 v124,v30,v31
v_cvt_pk_bf16_f32 v125,v32,v33
v_cmp_eq_u32 vcc,s53,v7
M 22,a[244:247],90
s_cmp_eq_u32 vcc_lo,-1
R 58,2,6144
D 0,56,3072
buffer_store_dwordx4 v[122:125],v0,s[36:39],s67 offen offset:0 sc0 sc1
M 22,a[252:255],94
L 6
s_cbranch_scc1 Lg3o_F1_%=
s_branch Lg3o_L1_%=
Lg3o_X0_%=:
M 18,a[96:99],82
s_add_u32 m0,s59,0
R 50,1,20480
v_mul_f32 v117,v29,v117
M 18,a[104:107],86
s_nop 0
buffer_load_dword v8,s[36:39],s58 offen offset:0 sc1 lds
M 18,a[112:115],90
R 54,1,21504
v_fma_f32 v114,v114,v105,v105
M 18,a[120:123],94
v_exp_f32 v115,v115
M 22,a[116:119],90
R 58,1,22528
v_fma_f32 v115,v115,v105,v105
M 22,a[124:127],94
L 5
v_exp_f32 v116,v116
M 22,a[132:135],34
R 62,1,23552
v_fma_f32 v116,v116,v105,v105
M 22,a[140:143],38
v_exp_f32 v117,v117
M 18,a[128:131],34
R 66,1,24576
v_fma_f32 v117,v117,v105,v105
M 18,a[136:139],38
L 5
v_rcp_f32 v114,v114
M 18,a[144:147],42
R 70,1,25600
v_mul_f32 v26,v26,v114
M 18,a[152:155],46
v_rcp_f32 v115,v115
M 22,a[148:151],42
R 74,1,26624
v_mul_f32 v27,v27,v115
M 22,a[156:159],46
L 5
v_rcp_f32 v116,v116
M 22,a[164:167],50
R 78,1,27648
v_mul_f32 v28,v28,v116
M 22,a[172:175],54
v_rcp_f32 v117,v117
M 18,a[160:163],50
R 82,1,28672
v_mul_f32 v29,v29,v117
M 18,a[168:171],54
L 5
v_cvt_pk_bf16_f32 v122,v26,v27
v_cvt_pk_bf16_f32 v123,v28,v29
M 18,a[176:179],58
R 86,1,29696
v_fma_f32 v30,-v102,v110,v30
M 18,a[184:187],62
v_fma_f32 v31,-v102,v111,v31
v_fma_f32 v32,-v102,v112,v32
M 22,a[180:183],58
R 90,1,30720
v_fma_f32 v33,-v102,v113,v33
M 22,a[188:191],62
L 5
s_nop 0
v_mul_f32 v118,v30,v30
M 22,a[196:199],66
s_waitcnt vmcnt(0)
R 94,1,31744
v_mul_f32 v119,v31,v31
M 22,a[204:207],70
s_barrier
buffer_store_dword v4,v3,s[36:39],s70 offen offset:0 sc0 sc1
M 18,a[192:195],66
s_mov_b32 s70,s69
ds_read_b32 v7,v6 offset:0
R 98,13,0
M 18,a[200:203],70
L 6
v_mul_f32 v120,v32,v32
v_mul_f32 v121,v33,v33
M 18,a[208:211],74
s_mov_b32 s69,s68
v_fma_f32 v118,v104,v118,v103
v_fma_f32 v119,v104,v119,v103
M 18,a[216:219],78
s_add_u32 s68,s68,s63
v_fma_f32 v120,v104,v120,v103
v_fma_f32 v121,v104,v121,v103
M 22,a[212:215],74
v_mul_f32 v118,v30,v118
)"
R"(v_mul_f32 v119,v31,v119
M 22,a[220:223],78
L 4
v_mul_f32 v120,v32,v120
v_mul_f32 v121,v33,v121
M 22,a[228:231],82
L 1
v_exp_f32 v118,v118
M 22,a[236:239],86
v_exp_f32 v119,v119
M 18,a[224:227],82
v_exp_f32 v120,v120
M 18,a[232:235],86
s_nop 0
v_exp_f32 v121,v121
M 18,a[240:243],90
v_fma_f32 v118,v118,v105,v105
v_fma_f32 v119,v119,v105,v105
v_fma_f32 v120,v120,v105,v105
v_fma_f32 v121,v121,v105,v105
v_rcp_f32 v118,v118
v_rcp_f32 v119,v119
v_rcp_f32 v120,v120
v_rcp_f32 v121,v121
M 18,a[248:251],94
v_mul_f32 v30,v30,v118
v_mul_f32 v31,v31,v119
v_mul_f32 v32,v32,v120
v_mul_f32 v33,v33,v121
v_cvt_pk_bf16_f32 v124,v30,v31
v_cvt_pk_bf16_f32 v125,v32,v33
M 22,a[244:247],90
buffer_store_dwordx4 v[122:125],v0,s[36:39],s67 offen offset:0 sc0 sc1
v_cmp_eq_u32 vcc,s53,v7
M 22,a[252:255],94
s_mov_b32 s80,1
s_cmp_eq_u32 vcc_lo,-1
s_cbranch_scc0 Lg3o_xn0_%=
buffer_load_dword v130,v11,s[36:39],s73 offen offset:0 sc1
L 0
v_fma_f32 v18,-v98,v106,v18
v_fma_f32 v19,-v98,v107,v19
v_fma_f32 v20,-v98,v108,v20
v_fma_f32 v21,-v98,v109,v21
v_mul_f32 v114,v18,v18
v_mul_f32 v115,v19,v19
s_add_u32 s73,s73,s64
s_add_u32 m0,s57,0
s_add_u32 s56,s55,0
D 0,56,0
v_mul_f32 v116,v20,v20
v_mul_f32 v117,v21,v21
v_fma_f32 v114,v100,v114,v99
v_fma_f32 v115,v100,v115,v99
v_fma_f32 v116,v100,v116,v99
v_fma_f32 v117,v100,v117,v99
D 0,56,1024
v_mul_f32 v114,v18,v114
v_mul_f32 v115,v19,v115
v_mul_f32 v116,v20,v116
v_mul_f32 v117,v21,v117
v_exp_f32 v114,v114
v_exp_f32 v115,v115
D 0,56,2048
v_exp_f32 v116,v116
v_exp_f32 v117,v117
v_fma_f32 v114,v114,v101,v101
v_fma_f32 v115,v115,v101,v101
v_fma_f32 v116,v116,v101,v101
v_fma_f32 v117,v117,v101,v101
D 0,56,3072
v_rcp_f32 v114,v114
v_rcp_f32 v115,v115
v_rcp_f32 v116,v116
v_rcp_f32 v117,v117
v_mul_f32 v18,v18,v114
v_mul_f32 v19,v19,v115
s_add_u32 m0,s57,16384
s_add_u32 s56,s55,16384
D 0,56,0
v_mul_f32 v20,v20,v116
v_mul_f32 v21,v21,v117
v_cvt_pk_bf16_f32 v122,v18,v19
v_cvt_pk_bf16_f32 v123,v20,v21
v_fma_f32 v22,-v98,v110,v22
v_fma_f32 v23,-v98,v111,v23
D 0,56,1024
v_fma_f32 v24,-v98,v112,v24
v_fma_f32 v25,-v98,v113,v25
v_mul_f32 v118,v22,v22
v_mul_f32 v119,v23,v23
v_mul_f32 v120,v24,v24
v_mul_f32 v121,v25,v25
D 0,56,2048
v_fma_f32 v118,v100,v118,v99
v_fma_f32 v119,v100,v119,v99
v_fma_f32 v120,v100,v120,v99
v_fma_f32 v121,v100,v121,v99
v_mul_f32 v118,v22,v118
v_mul_f32 v119,v23,v119
D 0,56,3072
v_mul_f32 v120,v24,v120
v_mul_f32 v121,v25,v121
v_exp_f32 v118,v118
v_exp_f32 v119,v119
v_exp_f32 v120,v120
v_exp_f32 v121,v121
s_add_u32 s55,s55,s60
s_sub_u32 s57,s84,s57
s_add_u32 s54,s54,s46
v_fma_f32 v118,v118,v101,v101
v_fma_f32 v119,v119,v101,v101
v_fma_f32 v120,v120,v101,v101
v_fma_f32 v121,v121,v101,v101
v_rcp_f32 v118,v118
v_rcp_f32 v119,v119
v_rcp_f32 v120,v120
v_rcp_f32 v121,v121
v_mul_f32 v22,v22,v118
v_mul_f32 v23,v23,v119
v_mul_f32 v24,v24,v120
v_mul_f32 v25,v25,v121
v_cvt_pk_bf16_f32 v124,v22,v23
v_cvt_pk_bf16_f32 v125,v24,v25
buffer_store_dwordx4 v[122:125],v0,s[36:39],s66 offen offset:0 sc0 sc1
s_mov_b32 s72,s69
s_mov_b32 s69,0x7fff0000
s_mov_b32 s66,0x7fff0000
s_mov_b32 s76,0x7fff0000
s_branch Lg3o_gox_%=
Lg3o_xn0_%=:
s_cmp_ge_u32 s54,256
s_cbranch_scc1 Lg3o_fin0_%=
s_add_u32 m0,s59,4096
s_nop 0
buffer_load_dword v8,s[36:39],s58 offen offset:0 sc1 lds
buffer_store_dword v4,v3,s[36:39],s71 offen offset:0 sc0 sc1
s_add_u32 m0,s59,5120
s_nop 0
buffer_load_dword v8,s[36:39],s58 offen offset:0 sc1 lds
buffer_store_dword v4,v3,s[36:39],s71 offen offset:0 sc0 sc1
s_mov_b32 s83,0
L 0
v_fma_f32 v18,-v98,v106,v18
v_fma_f32 v19,-v98,v107,v19
v_fma_f32 v20,-v98,v108,v20
v_fma_f32 v21,-v98,v109,v21
v_mul_f32 v114,v18,v18
v_mul_f32 v115,v19,v19
v_mul_f32 v116,v20,v20
v_mul_f32 v117,v21,v21
v_fma_f32 v114,v100,v114,v99
v_fma_f32 v115,v100,v115,v99
v_fma_f32 v116,v100,v116,v99
v_fma_f32 v117,v100,v117,v99
v_mul_f32 v114,v18,v114
v_mul_f32 v115,v19,v115
v_mul_f32 v116,v20,v116
v_mul_f32 v117,v21,v117
v_exp_f32 v114,v114
v_exp_f32 v115,v115
v_exp_f32 v116,v116
v_exp_f32 v117,v117
v_fma_f32 v114,v114,v101,v101
v_fma_f32 v115,v115,v101,v101
v_fma_f32 v116,v116,v101,v101
v_fma_f32 v117,v117,v101,v101
v_rcp_f32 v114,v114
v_rcp_f32 v115,v115
v_rcp_f32 v116,v116
v_rcp_f32 v117,v117
v_mul_f32 v18,v18,v114
v_mul_f32 v19,v19,v115
v_mul_f32 v20,v20,v116
v_mul_f32 v21,v21,v117
v_cvt_pk_bf16_f32 v122,v18,v19
v_cvt_pk_bf16_f32 v123,v20,v21
v_fma_f32 v22,-v98,v110,v22
v_fma_f32 v23,-v98,v111,v23
v_fma_f32 v24,-v98,v112,v24
v_fma_f32 v25,-v98,v113,v25
v_mul_f32 v118,v22,v22
v_mul_f32 v119,v23,v23
v_mul_f32 v120,v24,v24
v_mul_f32 v121,v25,v25
v_fma_f32 v118,v100,v118,v99
v_fma_f32 v119,v100,v119,v99
v_fma_f32 v120,v100,v120,v99
v_fma_f32 v121,v100,v121,v99
v_mul_f32 v118,v22,v118
v_mul_f32 v119,v23,v119
v_mul_f32 v120,v24,v120
v_mul_f32 v121,v25,v121
v_exp_f32 v118,v118
v_exp_f32 v119,v119
v_exp_f32 v120,v120
v_exp_f32 v121,v121
v_fma_f32 v118,v118,v101,v101
v_fma_f32 v119,v119,v101,v101
v_fma_f32 v120,v120,v101,v101
v_fma_f32 v121,v121,v101,v101
v_rcp_f32 v118,v118
v_rcp_f32 v119,v119
v_rcp_f32 v120,v120
v_rcp_f32 v121,v121
v_mul_f32 v22,v22,v118
v_mul_f32 v23,v23,v119
v_mul_f32 v24,v24,v120
v_mul_f32 v25,v25,v121
v_cvt_pk_bf16_f32 v124,v22,v23
v_cvt_pk_bf16_f32 v125,v24,v25
buffer_store_dwordx4 v[122:125],v0,s[36:39],s66 offen offset:0 sc0 sc1
s_mov_b32 s72,s69
s_mov_b32 s69,0x7fff0000
s_mov_b32 s66,0x7fff0000
s_mov_b32 s76,0x7fff0000
s_branch Lg3o_poll_%=
Lg3o_fin0_%=:
L 0
v_fma_f32 v18,-v98,v106,v18
v_fma_f32 v19,-v98,v107,v19
v_fma_f32 v20,-v98,v108,v20
v_fma_f32 v21,-v98,v109,v21
v_mul_f32 v114,v18,v18
v_mul_f32 v115,v19,v19
v_mul_f32 v116,v20,v20
v_mul_f32 v117,v21,v21
v_fma_f32 v114,v100,v114,v99
v_fma_f32 v115,v100,v115,v99
v_fma_f32 v116,v100,v116,v99
v_fma_f32 v117,v100,v117,v99
v_mul_f32 v114,v18,v114
v_mul_f32 v115,v19,v115
v_mul_f32 v116,v20,v116
v_mul_f32 v117,v21,v117
v_exp_f32 v114,v114
v_exp_f32 v115,v115
v_exp_f32 v116,v116
v_exp_f32 v117,v117
v_fma_f32 v114,v114,v101,v101
v_fma_f32 v115,v115,v101,v101
v_fma_f32 v116,v116,v101,v101
v_fma_f32 v117,v117,v101,v101
v_rcp_f32 v114,v114
v_rcp_f32 v115,v115
v_rcp_f32 v116,v116
v_rcp_f32 v117,v117
v_mul_f32 v18,v18,v114
v_mul_f32 v19,v19,v115
v_mul_f32 v20,v20,v116
v_mul_f32 v21,v21,v117
v_cvt_pk_bf16_f32 v122,v18,v19
v_cvt_pk_bf16_f32 v123,v20,v21
v_fma_f32 v22,-v98,v110,v22
v_fma_f32 v23,-v98,v111,v23
v_fma_f32 v24,-v98,v112,v24
v_fma_f32 v25,-v98,v113,v25
v_mul_f32 v118,v22,v22
v_mul_f32 v119,v23,v23
v_mul_f32 v120,v24,v24
v_mul_f32 v121,v25,v25
v_fma_f32 v118,v100,v118,v99
v_fma_f32 v119,v100,v119,v99
v_fma_f32 v120,v100,v120,v99
v_fma_f32 v121,v100,v121,v99
v_mul_f32 v118,v22,v118
v_mul_f32 v119,v23,v119
v_mul_f32 v120,v24,v120
v_mul_f32 v121,v25,v121
v_exp_f32 v118,v118
v_exp_f32 v119,v119
v_exp_f32 v120,v120
v_exp_f32 v121,v121
v_fma_f32 v118,v118,v101,v101
v_fma_f32 v119,v119,v101,v101
v_fma_f32 v120,v120,v101,v101
v_fma_f32 v121,v121,v101,v101
v_rcp_f32 v118,v118
v_rcp_f32 v119,v119
v_rcp_f32 v120,v120
v_rcp_f32 v121,v121
v_mul_f32 v22,v22,v118
v_mul_f32 v23,v23,v119
v_mul_f32 v24,v24,v120
v_mul_f32 v25,v25,v121
v_cvt_pk_bf16_f32 v124,v22,v23
v_cvt_pk_bf16_f32 v125,v24,v25
buffer_store_dwordx4 v[122:125],v0,s[36:39],s66 offen offset:0 sc0 sc1
s_mov_b32 s72,s69
s_mov_b32 s69,0x7fff0000
s_mov_b32 s66,0x7fff0000
s_mov_b32 s76,0x7fff0000
s_branch Lg3o_done_%=
Lg3o_L1_%=:
v_mfma_f32_16x16x32_bf16 v[30:33],a[4:7],v[34:37],0
s_add_u32 m0,s59,3072
R 62,2,7168
v_add_f32_dpp v131,v130,v130 quad_perm:[1,0,3,2] row_mask:0xf bank_mask:0xf
M 30,a[12:15],38
s_mov_b32 s67,s65
buffer_load_dword v8,s[36:39],s58 offen offset:0 sc1 lds
v_mfma_f32_16x16x32_bf16 v[26:29],a[0:3],v[34:37],0
s_add_u32 s65,s65,s62
R 66,2,8192
v_add_f32_dpp v131,v131,v131 quad_perm:[2,3,0,1] row_mask:0xf bank_mask:0xf
M 26,a[8:11],38
L 5
v_fma_f32 v18,-v98,v106,v18
v_fma_f32 v19,-v98,v107,v19
M 26,a[16:19],42
R 70,2,9216
v_add_f32_dpp v134,v131,v131 row_half_mirror row_mask:0xf bank_mask:0xf
M 26,a[24:27],46
v_fma_f32 v20,-v98,v108,v20
v_fma_f32 v21,-v98,v109,v21
M 30,a[20:23],42
R 74,2,10240
v_mov_b32_dpp v131,v134 row_ror:8 row_mask:0xf bank_mask:0xf
M 30,a[28:31],46
L 5
v_fma_f32 v131,-v134,v134,v131
v_max_f32 v131,s87,v131
M 30,a[36:39],50
R 78,2,11264
v_mul_f32 v114,v18,v18
M 30,a[44:47],54
v_rsq_f32 v132,v131
M 26,a[32:35],50
R 82,2,12288
v_mul_f32 v137,v131,v132
M 26,a[40:43],54
L 5
v_mul_f32 v135,s88,v132
v_mul_f32 v131,s89,v132
M 26,a[48:51],58
R 86,2,13312
v_mul_f32 v136,v135,v132
M 26,a[56:59],62
v_mul_f32 v136,v136,v131
v_mul_f32 v115,v19,v19
M 30,a[52:55],58
R 90,2,14336
v_mul_f32 v116,v20,v20
M 30,a[60:63],62
L 5
ds_write_b128 v12,v[134:137] offset:2048
M 30,a[68:71],66
s_waitcnt vmcnt(0)
R 94,2,15360
v_mul_f32 v117,v21,v21
M 30,a[76:79],70
s_barrier
ds_read_b32 v7,v6 offset:3072
v_fma_f32 v114,v100,v114,v99
M 26,a[64:67],66
R 34,2,16384
v_fma_f32 v115,v100,v115,v99
M 26,a[72:75],70
L 7
buffer_store_dword v4,v3,s[36:39],s70 offen offset:0 sc0 sc1
M 26,a[80:83],74
s_mov_b32 s70,0x7fff0000
R 38,2,17408
v_fma_f32 v116,v100,v116,v99
M 26,a[88:91],78
v_fma_f32 v117,v100,v117,v99
v_mul_f32 v114,v18,v114
M 30,a[84:87],74
R 42,2,18432
v_mul_f32 v115,v19,v115
M 30,a[92:95],78
L 7
L 3
v_cmp_eq_u32 vcc,s53,v7
M 30,a[100:103],82
R 46,2,19456
v_mul_f32 v116,v20,v116
M 30,a[108:111],86
s_cmp_eq_u32 vcc_lo,-1
v_exp_f32 v114,v114
s_cbranch_scc0 Lg3o_X1_%=
M 26,a[96:99],82
s_add_u32 m0,s57,0
s_add_u32 s56,s55,0
s_add_u32 s58,s58,s61
R 50,2,20480
M 26,a[104:107],86
s_nop 0
buffer_load_dword v130,v11,s[36:39],s73 offen offset:0 sc1
M 26,a[112:115],90
s_add_u32 s54,s54,s46
R 54,2,21504
v_mul_f32 v117,v21,v117
M 26,a[120:123],94
s_add_u32 s73,s73,s64
D 0,56,0
M 30,a[116:119],90
R 58,2,22528
v_fma_f32 v114,v114,v101,v101
M 30,a[124:127],94
L 5
v_exp_f32 v115,v115
M 30,a[132:135],34
R 62,2,23552
v_fma_f32 v115,v115,v101,v101
M 30,a[140:143],38
D 0,56,1024
M 26,a[128:131],34
R 66,2,24576
v_fma_f32 v22,-v98,v110,v22
M 26,a[136:139],38
L 5
v_exp_f32 v116,v116
M 26,a[144:147],42
R 70,2,25600
v_fma_f32 v116,v116,v101,v101
M 26,a[152:155],46
D 0,56,2048
M 30,a[148:151],42
R 74,2,26624
v_fma_f32 v23,-v98,v111,v23
M 30,a[156:159],46
L 5
v_exp_f32 v117,v117
M 30,a[164:167],50
R 78,2,27648
v_fma_f32 v117,v117,v101,v101
M 30,a[172:175],54
D 0,56,3072
M 26,a[160:163],50
s_add_u32 m0,s59,1024
R 82,2,28672
v_fma_f32 v24,-v98,v112,v24
M 26,a[168:171],54
L 5
buffer_load_dword v8,s[36:39],s58 offen offset:0 sc1 lds
M 26,a[176:179],58
s_add_u32 m0,s57,16384
R 86,2,29696
v_fma_f32 v25,-v98,v113,v25
M 26,a[184:187],62
s_add_u32 s56,s55,16384
v_rcp_f32 v114,v114
M 30,a[180:183],58
s_add_u32 s55,s55,s60
R 90,2,30720
v_mul_f32 v18,v18,v114
M 30,a[188:191],62
L 5
s_nop 0
D 0,56,0
M 30,a[196:199],66
s_waitcnt vmcnt(1)
R 94,2,31744
v_mul_f32 v118,v22,v22
M 30,a[204:207],70
s_barrier
buffer_store_dword v4,v3,s[36:39],s70 offen offset:0 sc0 sc1
M 26,a[192:195],66
s_mov_b32 s70,s69
R 34,1,0
ds_read_b32 v7,v6 offset:1024
M 26,a[200:203],70
L 6
D 0,56,1024
M 26,a[208:211],74
s_mov_b32 s69,s68
R 38,1,1024
R 102,13,2048
M 26,a[216:219],78
s_sub_u32 s57,s84,s57
v_mul_f32 v119,v23,v23
v_mul_f32 v120,v24,v24
M 30,a[212:215],74
s_add_u32 s68,s68,s63
R 42,1,2048
v_mul_f32 v121,v25,v25
M 30,a[220:223],78
L 7
D 0,56,2048
M 30,a[228:231],82
L 3
R 46,1,3072
v_fma_f32 v118,v100,v118,v99
M 30,a[236:239],86
v_fma_f32 v119,v100,v119,v99
v_fma_f32 v120,v100,v120,v99
M 26,a[224:227],82
R 50,1,4096
v_fma_f32 v121,v100,v121,v99
M 26,a[232:235],86
s_nop 0
v_mul_f32 v118,v22,v118
v_mul_f32 v119,v23,v119
v_mul_f32 v120,v24,v120
v_mul_f32 v121,v25,v121
v_exp_f32 v118,v118
v_exp_f32 v119,v119
v_exp_f32 v120,v120
v_exp_f32 v121,v121
)"
R"(M 26,a[240:243],90
R 54,1,5120
v_rcp_f32 v115,v115
v_rcp_f32 v116,v116
v_rcp_f32 v117,v117
v_fma_f32 v118,v118,v101,v101
v_fma_f32 v119,v119,v101,v101
v_fma_f32 v120,v120,v101,v101
v_fma_f32 v121,v121,v101,v101
v_rcp_f32 v118,v118
v_rcp_f32 v119,v119
v_rcp_f32 v120,v120
v_rcp_f32 v121,v121
M 26,a[248:251],94
v_mul_f32 v19,v19,v115
v_mul_f32 v20,v20,v116
v_mul_f32 v21,v21,v117
v_cvt_pk_bf16_f32 v122,v18,v19
v_cvt_pk_bf16_f32 v123,v20,v21
v_mul_f32 v22,v22,v118
v_mul_f32 v23,v23,v119
v_mul_f32 v24,v24,v120
v_mul_f32 v25,v25,v121
v_cvt_pk_bf16_f32 v124,v22,v23
v_cvt_pk_bf16_f32 v125,v24,v25
v_cmp_eq_u32 vcc,s53,v7
M 30,a[244:247],90
s_cmp_eq_u32 vcc_lo,-1
R 58,1,6144
D 0,56,3072
buffer_store_dwordx4 v[122:125],v0,s[36:39],s66 offen offset:0 sc0 sc1
M 30,a[252:255],94
L 6
s_cbranch_scc1 Lg3o_F0_%=
s_branch Lg3o_L0_%=
Lg3o_X1_%=:
M 26,a[96:99],82
s_add_u32 m0,s59,1024
R 50,2,20480
v_mul_f32 v117,v21,v117
M 26,a[104:107],86
s_nop 0
buffer_load_dword v8,s[36:39],s58 offen offset:0 sc1 lds
M 26,a[112:115],90
R 54,2,21504
v_fma_f32 v114,v114,v101,v101
M 26,a[120:123],94
v_exp_f32 v115,v115
M 30,a[116:119],90
R 58,2,22528
v_fma_f32 v115,v115,v101,v101
M 30,a[124:127],94
L 5
v_exp_f32 v116,v116
M 30,a[132:135],34
R 62,2,23552
v_fma_f32 v116,v116,v101,v101
M 30,a[140:143],38
v_exp_f32 v117,v117
M 26,a[128:131],34
R 66,2,24576
v_fma_f32 v117,v117,v101,v101
M 26,a[136:139],38
L 5
v_rcp_f32 v114,v114
M 26,a[144:147],42
R 70,2,25600
v_mul_f32 v18,v18,v114
M 26,a[152:155],46
v_rcp_f32 v115,v115
M 30,a[148:151],42
R 74,2,26624
v_mul_f32 v19,v19,v115
M 30,a[156:159],46
L 5
v_rcp_f32 v116,v116
M 30,a[164:167],50
R 78,2,27648
v_mul_f32 v20,v20,v116
M 30,a[172:175],54
v_rcp_f32 v117,v117
M 26,a[160:163],50
R 82,2,28672
v_mul_f32 v21,v21,v117
M 26,a[168:171],54
L 5
v_cvt_pk_bf16_f32 v122,v18,v19
v_cvt_pk_bf16_f32 v123,v20,v21
M 26,a[176:179],58
R 86,2,29696
v_fma_f32 v22,-v98,v110,v22
M 26,a[184:187],62
v_fma_f32 v23,-v98,v111,v23
v_fma_f32 v24,-v98,v112,v24
M 30,a[180:183],58
R 90,2,30720
v_fma_f32 v25,-v98,v113,v25
M 30,a[188:191],62
L 5
s_nop 0
v_mul_f32 v118,v22,v22
M 30,a[196:199],66
s_waitcnt vmcnt(0)
R 94,2,31744
v_mul_f32 v119,v23,v23
M 30,a[204:207],70
s_barrier
buffer_store_dword v4,v3,s[36:39],s70 offen offset:0 sc0 sc1
M 26,a[192:195],66
s_mov_b32 s70,s69
ds_read_b32 v7,v6 offset:1024
R 102,13,2048
M 26,a[200:203],70
L 6
v_mul_f32 v120,v24,v24
v_mul_f32 v121,v25,v25
M 26,a[208:211],74
s_mov_b32 s69,s68
v_fma_f32 v118,v100,v118,v99
v_fma_f32 v119,v100,v119,v99
M 26,a[216:219],78
s_add_u32 s68,s68,s63
v_fma_f32 v120,v100,v120,v99
v_fma_f32 v121,v100,v121,v99
M 30,a[212:215],74
v_mul_f32 v118,v22,v118
v_mul_f32 v119,v23,v119
M 30,a[220:223],78
L 4
v_mul_f32 v120,v24,v120
v_mul_f32 v121,v25,v121
M 30,a[228:231],82
L 1
v_exp_f32 v118,v118
M 30,a[236:239],86
v_exp_f32 v119,v119
M 26,a[224:227],82
v_exp_f32 v120,v120
M 26,a[232:235],86
s_nop 0
v_exp_f32 v121,v121
M 26,a[240:243],90
v_fma_f32 v118,v118,v101,v101
v_fma_f32 v119,v119,v101,v101
v_fma_f32 v120,v120,v101,v101
v_fma_f32 v121,v121,v101,v101
v_rcp_f32 v118,v118
v_rcp_f32 v119,v119
v_rcp_f32 v120,v120
v_rcp_f32 v121,v121
M 26,a[248:251],94
v_mul_f32 v22,v22,v118
v_mul_f32 v23,v23,v119
v_mul_f32 v24,v24,v120
v_mul_f32 v25,v25,v121
v_cvt_pk_bf16_f32 v124,v22,v23
v_cvt_pk_bf16_f32 v125,v24,v25
M 30,a[244:247],90
buffer_store_dwordx4 v[122:125],v0,s[36:39],s66 offen offset:0 sc0 sc1
v_cmp_eq_u32 vcc,s53,v7
M 30,a[252:255],94
s_mov_b32 s80,0
s_cmp_eq_u32 vcc_lo,-1
s_cbranch_scc0 Lg3o_xn1_%=
buffer_load_dword v130,v11,s[36:39],s73 offen offset:0 sc1
L 0
v_fma_f32 v26,-v102,v106,v26
v_fma_f32 v27,-v102,v107,v27
v_fma_f32 v28,-v102,v108,v28
v_fma_f32 v29,-v102,v109,v29
v_mul_f32 v114,v26,v26
v_mul_f32 v115,v27,v27
s_add_u32 s73,s73,s64
s_add_u32 m0,s57,0
s_add_u32 s56,s55,0
D 0,56,0
v_mul_f32 v116,v28,v28
v_mul_f32 v117,v29,v29
v_fma_f32 v114,v104,v114,v103
v_fma_f32 v115,v104,v115,v103
v_fma_f32 v116,v104,v116,v103
v_fma_f32 v117,v104,v117,v103
D 0,56,1024
v_mul_f32 v114,v26,v114
v_mul_f32 v115,v27,v115
v_mul_f32 v116,v28,v116
v_mul_f32 v117,v29,v117
v_exp_f32 v114,v114
v_exp_f32 v115,v115
D 0,56,2048
v_exp_f32 v116,v116
v_exp_f32 v117,v117
v_fma_f32 v114,v114,v105,v105
v_fma_f32 v115,v115,v105,v105
v_fma_f32 v116,v116,v105,v105
v_fma_f32 v117,v117,v105,v105
D 0,56,3072
v_rcp_f32 v114,v114
v_rcp_f32 v115,v115
v_rcp_f32 v116,v116
v_rcp_f32 v117,v117
v_mul_f32 v26,v26,v114
v_mul_f32 v27,v27,v115
s_add_u32 m0,s57,16384
s_add_u32 s56,s55,16384
D 0,56,0
v_mul_f32 v28,v28,v116
v_mul_f32 v29,v29,v117
v_cvt_pk_bf16_f32 v122,v26,v27
v_cvt_pk_bf16_f32 v123,v28,v29
v_fma_f32 v30,-v102,v110,v30
v_fma_f32 v31,-v102,v111,v31
D 0,56,1024
v_fma_f32 v32,-v102,v112,v32
v_fma_f32 v33,-v102,v113,v33
v_mul_f32 v118,v30,v30
v_mul_f32 v119,v31,v31
v_mul_f32 v120,v32,v32
v_mul_f32 v121,v33,v33
D 0,56,2048
v_fma_f32 v118,v104,v118,v103
v_fma_f32 v119,v104,v119,v103
v_fma_f32 v120,v104,v120,v103
v_fma_f32 v121,v104,v121,v103
v_mul_f32 v118,v30,v118
v_mul_f32 v119,v31,v119
D 0,56,3072
v_mul_f32 v120,v32,v120
v_mul_f32 v121,v33,v121
v_exp_f32 v118,v118
v_exp_f32 v119,v119
v_exp_f32 v120,v120
v_exp_f32 v121,v121
s_add_u32 s55,s55,s60
s_sub_u32 s57,s84,s57
s_add_u32 s54,s54,s46
v_fma_f32 v118,v118,v105,v105
v_fma_f32 v119,v119,v105,v105
v_fma_f32 v120,v120,v105,v105
v_fma_f32 v121,v121,v105,v105
v_rcp_f32 v118,v118
v_rcp_f32 v119,v119
v_rcp_f32 v120,v120
v_rcp_f32 v121,v121
v_mul_f32 v30,v30,v118
v_mul_f32 v31,v31,v119
v_mul_f32 v32,v32,v120
v_mul_f32 v33,v33,v121
v_cvt_pk_bf16_f32 v124,v30,v31
v_cvt_pk_bf16_f32 v125,v32,v33
buffer_store_dwordx4 v[122:125],v0,s[36:39],s67 offen offset:0 sc0 sc1
s_mov_b32 s72,s69
s_mov_b32 s69,0x7fff0000
s_mov_b32 s67,0x7fff0000
s_mov_b32 s77,0x7fff0000
s_branch Lg3o_gox_%=
Lg3o_xn1_%=:
s_cmp_ge_u32 s54,256
s_cbranch_scc1 Lg3o_fin1_%=
s_add_u32 m0,s59,4096
s_nop 0
buffer_load_dword v8,s[36:39],s58 offen offset:0 sc1 lds
buffer_store_dword v4,v3,s[36:39],s71 offen offset:0 sc0 sc1
s_add_u32 m0,s59,5120
s_nop 0
buffer_load_dword v8,s[36:39],s58 offen offset:0 sc1 lds
buffer_store_dword v4,v3,s[36:39],s71 offen offset:0 sc0 sc1
s_mov_b32 s83,0
L 0
v_fma_f32 v26,-v102,v106,v26
v_fma_f32 v27,-v102,v107,v27
v_fma_f32 v28,-v102,v108,v28
v_fma_f32 v29,-v102,v109,v29
v_mul_f32 v114,v26,v26
v_mul_f32 v115,v27,v27
v_mul_f32 v116,v28,v28
v_mul_f32 v117,v29,v29
v_fma_f32 v114,v104,v114,v103
v_fma_f32 v115,v104,v115,v103
v_fma_f32 v116,v104,v116,v103
v_fma_f32 v117,v104,v117,v103
v_mul_f32 v114,v26,v114
v_mul_f32 v115,v27,v115
v_mul_f32 v116,v28,v116
v_mul_f32 v117,v29,v117
v_exp_f32 v114,v114
v_exp_f32 v115,v115
v_exp_f32 v116,v116
v_exp_f32 v117,v117
v_fma_f32 v114,v114,v105,v105
v_fma_f32 v115,v115,v105,v105
v_fma_f32 v116,v116,v105,v105
v_fma_f32 v117,v117,v105,v105
v_rcp_f32 v114,v114
v_rcp_f32 v115,v115
v_rcp_f32 v116,v116
v_rcp_f32 v117,v117
v_mul_f32 v26,v26,v114
v_mul_f32 v27,v27,v115
v_mul_f32 v28,v28,v116
v_mul_f32 v29,v29,v117
v_cvt_pk_bf16_f32 v122,v26,v27
v_cvt_pk_bf16_f32 v123,v28,v29
v_fma_f32 v30,-v102,v110,v30
v_fma_f32 v31,-v102,v111,v31
v_fma_f32 v32,-v102,v112,v32
v_fma_f32 v33,-v102,v113,v33
v_mul_f32 v118,v30,v30
v_mul_f32 v119,v31,v31
v_mul_f32 v120,v32,v32
v_mul_f32 v121,v33,v33
v_fma_f32 v118,v104,v118,v103
v_fma_f32 v119,v104,v119,v103
v_fma_f32 v120,v104,v120,v103
v_fma_f32 v121,v104,v121,v103
v_mul_f32 v118,v30,v118
v_mul_f32 v119,v31,v119
v_mul_f32 v120,v32,v120
v_mul_f32 v121,v33,v121
v_exp_f32 v118,v118
v_exp_f32 v119,v119
v_exp_f32 v120,v120
v_exp_f32 v121,v121
v_fma_f32 v118,v118,v105,v105
v_fma_f32 v119,v119,v105,v105
v_fma_f32 v120,v120,v105,v105
v_fma_f32 v121,v121,v105,v105
v_rcp_f32 v118,v118
v_rcp_f32 v119,v119
v_rcp_f32 v120,v120
v_rcp_f32 v121,v121
v_mul_f32 v30,v30,v118
v_mul_f32 v31,v31,v119
v_mul_f32 v32,v32,v120
v_mul_f32 v33,v33,v121
v_cvt_pk_bf16_f32 v124,v30,v31
v_cvt_pk_bf16_f32 v125,v32,v33
buffer_store_dwordx4 v[122:125],v0,s[36:39],s67 offen offset:0 sc0 sc1
s_mov_b32 s72,s69
s_mov_b32 s69,0x7fff0000
s_mov_b32 s67,0x7fff0000
s_mov_b32 s77,0x7fff0000
s_branch Lg3o_poll_%=
Lg3o_fin1_%=:
L 0
v_fma_f32 v26,-v102,v106,v26
v_fma_f32 v27,-v102,v107,v27
v_fma_f32 v28,-v102,v108,v28
v_fma_f32 v29,-v102,v109,v29
v_mul_f32 v114,v26,v26
v_mul_f32 v115,v27,v27
v_mul_f32 v116,v28,v28
v_mul_f32 v117,v29,v29
v_fma_f32 v114,v104,v114,v103
v_fma_f32 v115,v104,v115,v103
v_fma_f32 v116,v104,v116,v103
v_fma_f32 v117,v104,v117,v103
v_mul_f32 v114,v26,v114
v_mul_f32 v115,v27,v115
v_mul_f32 v116,v28,v116
v_mul_f32 v117,v29,v117
v_exp_f32 v114,v114
v_exp_f32 v115,v115
v_exp_f32 v116,v116
v_exp_f32 v117,v117
v_fma_f32 v114,v114,v105,v105
v_fma_f32 v115,v115,v105,v105
v_fma_f32 v116,v116,v105,v105
v_fma_f32 v117,v117,v105,v105
v_rcp_f32 v114,v114
v_rcp_f32 v115,v115
v_rcp_f32 v116,v116
v_rcp_f32 v117,v117
v_mul_f32 v26,v26,v114
v_mul_f32 v27,v27,v115
v_mul_f32 v28,v28,v116
v_mul_f32 v29,v29,v117
v_cvt_pk_bf16_f32 v122,v26,v27
v_cvt_pk_bf16_f32 v123,v28,v29
v_fma_f32 v30,-v102,v110,v30
v_fma_f32 v31,-v102,v111,v31
v_fma_f32 v32,-v102,v112,v32
v_fma_f32 v33,-v102,v113,v33
v_mul_f32 v118,v30,v30
v_mul_f32 v119,v31,v31
v_mul_f32 v120,v32,v32
v_mul_f32 v121,v33,v33
v_fma_f32 v118,v104,v118,v103
v_fma_f32 v119,v104,v119,v103
v_fma_f32 v120,v104,v120,v103
v_fma_f32 v121,v104,v121,v103
v_mul_f32 v118,v30,v118
v_mul_f32 v119,v31,v119
v_mul_f32 v120,v32,v120
v_mul_f32 v121,v33,v121
v_exp_f32 v118,v118
v_exp_f32 v119,v119
v_exp_f32 v120,v120
v_exp_f32 v121,v121
v_fma_f32 v118,v118,v105,v105
v_fma_f32 v119,v119,v105,v105
v_fma_f32 v120,v120,v105,v105
v_fma_f32 v121,v121,v105,v105
v_rcp_f32 v118,v118
v_rcp_f32 v119,v119
v_rcp_f32 v120,v120
v_rcp_f32 v121,v121
v_mul_f32 v30,v30,v118
v_mul_f32 v31,v31,v119
v_mul_f32 v32,v32,v120
v_mul_f32 v33,v33,v121
v_cvt_pk_bf16_f32 v124,v30,v31
v_cvt_pk_bf16_f32 v125,v32,v33
buffer_store_dwordx4 v[122:125],v0,s[36:39],s67 offen offset:0 sc0 sc1
s_mov_b32 s72,s69
s_mov_b32 s69,0x7fff0000
s_mov_b32 s67,0x7fff0000
s_mov_b32 s77,0x7fff0000
s_branch Lg3o_done_%=
Lg3o_done_%=:
s_waitcnt vmcnt(1)
s_barrier
buffer_store_dword v4,v3,s[36:39],s70 offen offset:0 sc0 sc1
s_waitcnt vmcnt(1)
s_barrier
buffer_store_dword v4,v3,s[36:39],s72 offen offset:0 sc0 sc1
s_waitcnt vmcnt(0) lgkmcnt(0)
s_barrier
s_endpgm
Lcode_g3o_e:
.purgem M
.purgem R
.purgem T
.purgem D
.purgem L
)"
  :: "s"(ws), "s"(sWave), "s"(sG0), "s"(sStep), "s"(sOutOff), "s"(sFoutOff), "s"(sAuxOff), "s"(sWw), "s"(sBuf), "s"(sRk), "v"(vL16), "v"(vFlag), "v"(vSnapOff), "v"(vSnapRd), "v"(vWd0), "v"(vWd1), "v"(vWr0), "v"(vWr1), "v"(vWr2), "v"(vOutS), "v"(vAux0), "v"(vAux1), "v"(vAux2), "v"(vAux3), "v"(vAn), "v"(vAe), "s"(w3), "s"(out) : "v0", "v1", "v2", "v3", "v4", "v5", "v6", "v7", "v8", "v9", "v10", "v11", "v12", "v13", "v14", "v15", "v16", "v17", "v18", "v19", "v20", "v21", "v22", "v23", "v24", "v25", "v26", "v27", "v28", "v29", "v30", "v31", "v32", "v33", "v34", "v35", "v36", "v37", "v38", "v39", "v40", "v41", "v42", "v43", "v44", "v45", "v46", "v47", "v48", "v49", "v50", "v51", "v52", "v53", "v54", "v55", "v56", "v57", "v58", "v59", "v60", "v61", "v62", "v63", "v64", "v65", "v66", "v67", "v68", "v69", "v70", "v71", "v72", "v73", "v74", "v75", "v76", "v77", "v78", "v79", "v80", "v81", "v82", "v83", "v84", "v85", "v86", "v87", "v88", "v89", "v90", "v91", "v92", "v93", "v94", "v95", "v96", "v97", "v98", "v99", "v100", "v101", "v102", "v103", "v104", "v105", "v106", "v107", "v108", "v109", "v110", "v111", "v112", "v113", "v114", "v115", "v116", "v117", "v118", "v119", "v120", "v121", "v122", "v123", "v124", "v125", "v126", "v127", "v128", "v129", "v130", "v131", "v132", "v133", "v134", "v135", "v136", "v137", "v138", "v139", "v140", "v141", "v142", "v143", "v144", "v145", "v146", "v147", "v148", "v149", "v150", "v151", "v152", "v153", "v154", "v155", "v156", "v157", "v158", "v159", "v160", "v161", "v162", "v163", "v164", "v165", "v166", "v167", "v168", "v169", "v170", "v171", "v172", "v173", "v174", "v175", "v176", "v177", "v178", "v179", "v180", "v181", "v182", "v183", "v184", "v185", "v186", "v187", "v188", "v189", "v190", "v191", "v192", "v193", "v194", "v195", "v196", "v197", "v198", "v199", "v200", "v201", "v202", "v203", "v204", "v205", "v206", "v207", "v208", "v209", "v210", "v211", "v212", "v213", "v214", "v215", "v216", "v217", "v218", "v219", "v220", "v221", "v222", "v223", "v224", "v225", "v226", "v227", "v228", "v229", "v230", "v231", "v232", "v233", "v234", "v235", "v236", "v237", "v238", "v239", "a0", "a1", "a2", "a3", "a4", "a5", "a6", "a7", "a8", "a9", "a10", "a11", "a12", "a13", "a14", "a15", "a16", "a17", "a18", "a19", "a20", "a21", "a22", "a23", "a24", "a25", "a26", "a27", "a28", "a29", "a30", "a31", "a32", "a33", "a34", "a35", "a36", "a37", "a38", "a39", "a40", "a41", "a42", "a43", "a44", "a45", "a46", "a47", "a48", "a49", "a50", "a51", "a52", "a53", "a54", "a55", "a56", "a57", "a58", "a59", "a60", "a61", "a62", "a63", "a64", "a65", "a66", "a67", "a68", "a69", "a70", "a71", "a72", "a73", "a74", "a75", "a76", "a77", "a78", "a79", "a80", "a81", "a82", "a83", "a84", "a85", "a86", "a87", "a88", "a89", "a90", "a91", "a92", "a93", "a94", "a95", "a96", "a97", "a98", "a99", "a100", "a101", "a102", "a103", "a104", "a105", "a106", "a107", "a108", "a109", "a110", "a111", "a112", "a113", "a114", "a115", "a116", "a117", "a118", "a119", "a120", "a121", "a122", "a123", "a124", "a125", "a126", "a127", "a128", "a129", "a130", "a131", "a132", "a133", "a134", "a135", "a136", "a137", "a138", "a139", "a140", "a141", "a142", "a143", "a144", "a145", "a146", "a147", "a148", "a149", "a150", "a151", "a152", "a153", "a154", "a155", "a156", "a157", "a158", "a159", "a160", "a161", "a162", "a163", "a164", "a165", "a166", "a167", "a168", "a169", "a170", "a171", "a172", "a173", "a174", "a175", "a176", "a177", "a178", "a179", "a180", "a181", "a182", "a183", "a184", "a185", "a186", "a187", "a188", "a189", "a190", "a191", "a192", "a193", "a194", "a195", "a196", "a197", "a198", "a199", "a200", "a201", "a202", "a203", "a204", "a205", "a206", "a207", "a208", "a209", "a210", "a211", "a212", "a213", "a214", "a215", "a216", "a217", "a218", "a219", "a220", "a221", "a222", "a223", "a224", "a225", "a226", "a227", "a228", "a229", "a230", "a231", "a232", "a233", "a234", "a235", "a236", "a237", "a238", "a239", "a240", "a241", "a242", "a243", "a244", "a245", "a246", "a247", "a248", "a249", "a250", "a251", "a252", "a253", "a254", "a255", "s36", "s37", "s38", "s39", "s40", "s41", "s42", "s43", "s44", "s45", "s46", "s47", "s48", "s49", "s50", "s51", "s52", "s53", "s54", "s55", "s56", "s57", "s58", "s59", "s60", "s61", "s62", "s63", "s64", "s65", "s66", "s67", "s68", "s69", "s70", "s71", "s72", "s73", "s74", "s75", "s76", "s77", "s78", "s79", "s80", "s81", "s82", "s83", "s84", "s85", "s86", "s87", "s88", "s89", "s90", "s91", "s92", "s93", "s94", "s95", "s96", "s97", "vcc", "scc", "memory"); }
    __builtin_unreachable();
  } else if (kind == 4) {
    const unsigned sStep = 5;
    COMMON(10, (kh * 9 + fj) * 128, 2560, 143360, 2048, 448, 0);
    const int cs = slice;
    const unsigned sOutOff = (kh ? OFF_P1 : OFF_P0) + (cs * 4 + wave) * 1024, sFoutOff = OFF_FP + (kh * 8 + cs) * 128;
    const unsigned sAuxOff = (kh ? OFF_XP : OFF_X1) + (cs * 4 + wave) * 1024, sWw = (kh * 1792 + 16 * wave) * 2048 + cs * 256, sBuf = kh * 57344;
    const int zu = 118 + (rep + 2) % 5 + 5 * (wave >> 1);
    const unsigned vSp = lg * 2048 + l15 * 4, vSn = (zu * 16 + (wave & 1) * 8 + (ln >> 3)) * 2048 + (cs * 2 + kh) * 128 + (ln & 7) * 16;
    (void)vWr2;
    asm volatile(
R"(.macro M d,w,x
v_mfma_f32_16x16x32_bf16 v[\d:\d+3], \w, v[\x:\x+3], v[\d:\d+3]
.endm
.macro R d,a,o
ds_read_b128 v[\d:\d+3], v\a offset:\o
.endm
.macro T d,a,o
ds_read_b64_tr_b16 \d, v\a offset:\o
.endm
.macro D a,s,o
buffer_load_dwordx4 v\a, s[36:39], s\s offen offset:\o sc1 lds
.endm
.macro L c
s_waitcnt lgkmcnt(\c)
.endm
Lcode_g4_b:
s_mov_b64 s[36:37],%0
s_mov_b32 s38,111243392
s_mov_b32 s39,0x00020000
s_mov_b32 s44,%1
s_mov_b32 s45,%2
s_mov_b32 s46,%3
s_mov_b32 s47,%4
s_mov_b32 s48,%5
s_mov_b32 s49,%6
s_mov_b32 s50,%7
s_mov_b32 s51,%8
s_mov_b32 s93,%9
v_mov_b32 v0,%10
v_mov_b32 v3,%11
v_mov_b32 v5,%12
v_mov_b32 v6,%13
v_mov_b32 v10,%14
v_mov_b32 v11,%15
v_mov_b32 v12,%16
v_mov_b32 v13,%17
v_mov_b32 v9,%18
v_mov_b32 v7,%19
s_mov_b64 s[40:41],%20
s_mov_b32 s42,7340032
s_mov_b32 s43,0x00020000
s_lshl_b32 s57,s44,11
s_mov_b32 s79,111243264
buffer_load_dword v4,off,s[36:39],s79 sc1
s_add_u32 s55,s50,0
s_add_u32 m0,s57,0
s_nop 0
buffer_load_dwordx4 v10,s[40:43],s55 offen offset:0  lds
s_add_u32 m0,s57,1024
s_nop 0
buffer_load_dwordx4 v11,s[40:43],s55 offen offset:0  lds
s_add_u32 m0,s57,8064
s_nop 0
buffer_load_dwordx4 v10,s[40:43],s55 offen offset:128  lds
s_add_u32 m0,s57,9088
s_nop 0
buffer_load_dwordx4 v11,s[40:43],s55 offen offset:128  lds
s_add_u32 s55,s50,131072
s_add_u32 m0,s57,16384
s_nop 0
buffer_load_dwordx4 v10,s[40:43],s55 offen offset:0  lds
s_add_u32 m0,s57,17408
s_nop 0
buffer_load_dwordx4 v11,s[40:43],s55 offen offset:0  lds
s_add_u32 m0,s57,24448
s_nop 0
buffer_load_dwordx4 v10,s[40:43],s55 offen offset:128  lds
s_add_u32 m0,s57,25472
s_nop 0
buffer_load_dwordx4 v11,s[40:43],s55 offen offset:128  lds
s_add_u32 s55,s50,262144
s_add_u32 m0,s57,32768
s_nop 0
buffer_load_dwordx4 v10,s[40:43],s55 offen offset:0  lds
s_add_u32 m0,s57,33792
s_nop 0
buffer_load_dwordx4 v11,s[40:43],s55 offen offset:0  lds
s_add_u32 m0,s57,40832
s_nop 0
buffer_load_dwordx4 v10,s[40:43],s55 offen offset:128  lds
s_add_u32 m0,s57,41856
s_nop 0
buffer_load_dwordx4 v11,s[40:43],s55 offen offset:128  lds
s_add_u32 s55,s50,393216
s_add_u32 m0,s57,49152
s_nop 0
buffer_load_dwordx4 v10,s[40:43],s55 offen offset:0  lds
s_add_u32 m0,s57,50176
s_nop 0
buffer_load_dwordx4 v11,s[40:43],s55 offen offset:0  lds
s_add_u32 m0,s57,57216
s_nop 0
buffer_load_dwordx4 v10,s[40:43],s55 offen offset:128  lds
s_add_u32 m0,s57,58240
s_nop 0
buffer_load_dwordx4 v11,s[40:43],s55 offen offset:128  lds
s_add_u32 s55,s50,524288
s_add_u32 m0,s57,65536
s_nop 0
buffer_load_dwordx4 v10,s[40:43],s55 offen offset:0  lds
s_add_u32 m0,s57,66560
s_nop 0
buffer_load_dwordx4 v11,s[40:43],s55 offen offset:0  lds
s_add_u32 m0,s57,73600
s_nop 0
buffer_load_dwordx4 v10,s[40:43],s55 offen offset:128  lds
s_add_u32 m0,s57,74624
s_nop 0
buffer_load_dwordx4 v11,s[40:43],s55 offen offset:128  lds
s_add_u32 s55,s50,655360
s_add_u32 m0,s57,81920
s_nop 0
buffer_load_dwordx4 v10,s[40:43],s55 offen offset:0  lds
s_add_u32 m0,s57,82944
s_nop 0
buffer_load_dwordx4 v11,s[40:43],s55 offen offset:0  lds
s_add_u32 m0,s57,89984
s_nop 0
buffer_load_dwordx4 v10,s[40:43],s55 offen offset:128  lds
s_add_u32 m0,s57,91008
s_nop 0
buffer_load_dwordx4 v11,s[40:43],s55 offen offset:128  lds
s_waitcnt vmcnt(24)
s_barrier
v_readfirstlane_b32 s53,v4
s_nop 3
s_add_u32 s53,s53,1
s_cmp_eq_u32 s53,0
s_cselect_b32 s53,1,s53
v_mov_b32 v4,s53
s_waitcnt vmcnt(20) lgkmcnt(0)
s_barrier
s_add_u32 s55,s50,786432
s_add_u32 m0,s57,98304
s_nop 0
buffer_load_dwordx4 v10,s[40:43],s55 offen offset:0  lds
s_add_u32 m0,s57,99328
s_nop 0
buffer_load_dwordx4 v11,s[40:43],s55 offen offset:0  lds
s_add_u32 m0,s57,106368
s_nop 0
buffer_load_dwordx4 v10,s[40:43],s55 offen offset:128  lds
s_add_u32 m0,s57,107392
s_nop 0
buffer_load_dwordx4 v11,s[40:43],s55 offen offset:128  lds
v_add_u32 v18,0,v12
v_add_u32 v19,0,v13
T a[0:1],18,0
T a[2:3],18,512
T a[4:5],19,0
T a[6:7],19,512
T a[8:9],18,4096
T a[10:11],18,4608
T a[12:13],19,4096
T a[14:15],19,4608
s_waitcnt vmcnt(20) lgkmcnt(0)
s_barrier
s_add_u32 s55,s50,917504
s_add_u32 m0,s57,0
s_nop 0
buffer_load_dwordx4 v10,s[40:43],s55 offen offset:0  lds
s_add_u32 m0,s57,1024
s_nop 0
buffer_load_dwordx4 v11,s[40:43],s55 offen offset:0  lds
s_add_u32 m0,s57,8064
s_nop 0
buffer_load_dwordx4 v10,s[40:43],s55 offen offset:128  lds
s_add_u32 m0,s57,9088
s_nop 0
buffer_load_dwordx4 v11,s[40:43],s55 offen offset:128  lds
v_add_u32 v18,16384,v12
v_add_u32 v19,16384,v13
T a[16:17],18,0
T a[18:19],18,512
T a[20:21],19,0
T a[22:23],19,512
T a[24:25],18,4096
T a[26:27],18,4608
T a[28:29],19,4096
T a[30:31],19,4608
s_waitcnt vmcnt(20) lgkmcnt(0)
s_barrier
s_add_u32 s55,s50,1048576
s_add_u32 m0,s57,16384
s_nop 0
buffer_load_dwordx4 v10,s[40:43],s55 offen offset:0  lds
s_add_u32 m0,s57,17408
s_nop 0
buffer_load_dwordx4 v11,s[40:43],s55 offen offset:0  lds
s_add_u32 m0,s57,24448
s_nop 0
buffer_load_dwordx4 v10,s[40:43],s55 offen offset:128  lds
s_add_u32 m0,s57,25472
s_nop 0
buffer_load_dwordx4 v11,s[40:43],s55 offen offset:128  lds
v_add_u32 v18,32768,v12
v_add_u32 v19,32768,v13
T a[32:33],18,0
T a[34:35],18,512
T a[36:37],19,0
T a[38:39],19,512
T a[40:41],18,4096
T a[42:43],18,4608
T a[44:45],19,4096
T a[46:47],19,4608
s_waitcnt vmcnt(20) lgkmcnt(0)
s_barrier
s_add_u32 s55,s50,1179648
s_add_u32 m0,s57,32768
s_nop 0
buffer_load_dwordx4 v10,s[40:43],s55 offen offset:0  lds
s_add_u32 m0,s57,33792
s_nop 0
buffer_load_dwordx4 v11,s[40:43],s55 offen offset:0  lds
s_add_u32 m0,s57,40832
s_nop 0
buffer_load_dwordx4 v10,s[40:43],s55 offen offset:128  lds
s_add_u32 m0,s57,41856
s_nop 0
buffer_load_dwordx4 v11,s[40:43],s55 offen offset:128  lds
v_add_u32 v18,49152,v12
v_add_u32 v19,49152,v13
T a[48:49],18,0
T a[50:51],18,512
T a[52:53],19,0
T a[54:55],19,512
T a[56:57],18,4096
T a[58:59],18,4608
T a[60:61],19,4096
T a[62:63],19,4608
s_waitcnt vmcnt(20) lgkmcnt(0)
s_barrier
s_add_u32 s55,s50,1310720
s_add_u32 m0,s57,49152
s_nop 0
buffer_load_dwordx4 v10,s[40:43],s55 offen offset:0  lds
s_add_u32 m0,s57,50176
s_nop 0
buffer_load_dwordx4 v11,s[40:43],s55 offen offset:0  lds
s_add_u32 m0,s57,57216
s_nop 0
buffer_load_dwordx4 v10,s[40:43],s55 offen offset:128  lds
s_add_u32 m0,s57,58240
s_nop 0
buffer_load_dwordx4 v11,s[40:43],s55 offen offset:128  lds
v_add_u32 v18,65536,v12
v_add_u32 v19,65536,v13
T a[64:65],18,0
T a[66:67],18,512
T a[68:69],19,0
T a[70:71],19,512
T a[72:73],18,4096
T a[74:75],18,4608
T a[76:77],19,4096
T a[78:79],19,4608
s_waitcnt vmcnt(20) lgkmcnt(0)
s_barrier
s_add_u32 s55,s50,1441792
s_add_u32 m0,s57,65536
s_nop 0
buffer_load_dwordx4 v10,s[40:43],s55 offen offset:0  lds
s_add_u32 m0,s57,66560
s_nop 0
buffer_load_dwordx4 v11,s[40:43],s55 offen offset:0  lds
s_add_u32 m0,s57,73600
s_nop 0
buffer_load_dwordx4 v10,s[40:43],s55 offen offset:128  lds
s_add_u32 m0,s57,74624
s_nop 0
buffer_load_dwordx4 v11,s[40:43],s55 offen offset:128  lds
v_add_u32 v18,81920,v12
v_add_u32 v19,81920,v13
T a[80:81],18,0
T a[82:83],18,512
T a[84:85],19,0
T a[86:87],19,512
T a[88:89],18,4096
T a[90:91],18,4608
T a[92:93],19,4096
T a[94:95],19,4608
s_waitcnt vmcnt(20) lgkmcnt(0)
s_barrier
s_add_u32 s55,s50,1572864
s_add_u32 m0,s57,81920
s_nop 0
buffer_load_dwordx4 v10,s[40:43],s55 offen offset:0  lds
s_add_u32 m0,s57,82944
s_nop 0
buffer_load_dwordx4 v11,s[40:43],s55 offen offset:0  lds
s_add_u32 m0,s57,89984
s_nop 0
buffer_load_dwordx4 v10,s[40:43],s55 offen offset:128  lds
s_add_u32 m0,s57,91008
s_nop 0
buffer_load_dwordx4 v11,s[40:43],s55 offen offset:128  lds
v_add_u32 v18,98304,v12
v_add_u32 v19,98304,v13
T a[96:97],18,0
T a[98:99],18,512
T a[100:101],19,0
T a[102:103],19,512
T a[104:105],18,4096
T a[106:107],18,4608
T a[108:109],19,4096
T a[110:111],19,4608
s_waitcnt vmcnt(20) lgkmcnt(0)
s_barrier
s_add_u32 s55,s50,1703936
s_add_u32 m0,s57,98304
s_nop 0
buffer_load_dwordx4 v10,s[40:43],s55 offen offset:0  lds
s_add_u32 m0,s57,99328
s_nop 0
buffer_load_dwordx4 v11,s[40:43],s55 offen offset:0  lds
s_add_u32 m0,s57,106368
s_nop 0
buffer_load_dwordx4 v10,s[40:43],s55 offen offset:128  lds
s_add_u32 m0,s57,107392
s_nop 0
buffer_load_dwordx4 v11,s[40:43],s55 offen offset:128  lds
v_add_u32 v18,0,v12
v_add_u32 v19,0,v13
T a[112:113],18,0
T a[114:115],18,512
T a[116:117],19,0
T a[118:119],19,512
T a[120:121],18,4096
T a[122:123],18,4608
T a[124:125],19,4096
T a[126:127],19,4608
s_waitcnt vmcnt(20) lgkmcnt(0)
s_barrier
s_add_u32 s55,s50,1835008
s_add_u32 m0,s57,0
s_nop 0
buffer_load_dwordx4 v10,s[40:43],s55 offen offset:0  lds
s_add_u32 m0,s57,1024
s_nop 0
buffer_load_dwordx4 v11,s[40:43],s55 offen offset:0  lds
s_add_u32 m0,s57,8064
s_nop 0
buffer_load_dwordx4 v10,s[40:43],s55 offen offset:128  lds
s_add_u32 m0,s57,9088
s_nop 0
buffer_load_dwordx4 v11,s[40:43],s55 offen offset:128  lds
v_add_u32 v18,16384,v12
v_add_u32 v19,16384,v13
T a[128:129],18,0
T a[130:131],18,512
T a[132:133],19,0
T a[134:135],19,512
T a[136:137],18,4096
T a[138:139],18,4608
T a[140:141],19,4096
T a[142:143],19,4608
s_waitcnt vmcnt(20) lgkmcnt(0)
s_barrier
s_add_u32 s55,s50,1966080
s_add_u32 m0,s57,16384
s_nop 0
buffer_load_dwordx4 v10,s[40:43],s55 offen offset:0  lds
s_add_u32 m0,s57,17408
s_nop 0
buffer_load_dwordx4 v11,s[40:43],s55 offen offset:0  lds
s_add_u32 m0,s57,24448
s_nop 0
buffer_load_dwordx4 v10,s[40:43],s55 offen offset:128  lds
s_add_u32 m0,s57,25472
s_nop 0
buffer_load_dwordx4 v11,s[40:43],s55 offen offset:128  lds
v_add_u32 v18,32768,v12
v_add_u32 v19,32768,v13
T a[144:145],18,0
T a[146:147],18,512
T a[148:149],19,0
T a[150:151],19,512
T a[152:153],18,4096
T a[154:155],18,4608
T a[156:157],19,4096
T a[158:159],19,4608
s_waitcnt vmcnt(20) lgkmcnt(0)
s_barrier
s_add_u32 s55,s50,2097152
s_add_u32 m0,s57,32768
s_nop 0
buffer_load_dwordx4 v10,s[40:43],s55 offen offset:0  lds
s_add_u32 m0,s57,33792
s_nop 0
buffer_load_dwordx4 v11,s[40:43],s55 offen offset:0  lds
s_add_u32 m0,s57,40832
s_nop 0
buffer_load_dwordx4 v10,s[40:43],s55 offen offset:128  lds
s_add_u32 m0,s57,41856
s_nop 0
buffer_load_dwordx4 v11,s[40:43],s55 offen offset:128  lds
v_add_u32 v18,49152,v12
v_add_u32 v19,49152,v13
T a[160:161],18,0
T a[162:163],18,512
T a[164:165],19,0
T a[166:167],19,512
T a[168:169],18,4096
T a[170:171],18,4608
T a[172:173],19,4096
T a[174:175],19,4608
s_waitcnt vmcnt(20) lgkmcnt(0)
s_barrier
s_add_u32 s55,s50,2228224
s_add_u32 m0,s57,49152
s_nop 0
buffer_load_dwordx4 v10,s[40:43],s55 offen offset:0  lds
s_add_u32 m0,s57,50176
s_nop 0
buffer_load_dwordx4 v11,s[40:43],s55 offen offset:0  lds
s_add_u32 m0,s57,57216
s_nop 0
buffer_load_dwordx4 v10,s[40:43],s55 offen offset:128  lds
s_add_u32 m0,s57,58240
s_nop 0
buffer_load_dwordx4 v11,s[40:43],s55 offen offset:128  lds
v_add_u32 v18,65536,v12
v_add_u32 v19,65536,v13
T a[176:177],18,0
T a[178:179],18,512
T a[180:181],19,0
T a[182:183],19,512
T a[184:185],18,4096
T a[186:187],18,4608
T a[188:189],19,4096
T a[190:191],19,4608
s_waitcnt vmcnt(20) lgkmcnt(0)
s_barrier
s_add_u32 s55,s50,2359296
s_add_u32 m0,s57,65536
s_nop 0
buffer_load_dwordx4 v10,s[40:43],s55 offen offset:0  lds
s_add_u32 m0,s57,66560
s_nop 0
buffer_load_dwordx4 v11,s[40:43],s55 offen offset:0  lds
s_add_u32 m0,s57,73600
s_nop 0
buffer_load_dwordx4 v10,s[40:43],s55 offen offset:128  lds
s_add_u32 m0,s57,74624
s_nop 0
buffer_load_dwordx4 v11,s[40:43],s55 offen offset:128  lds
v_add_u32 v18,81920,v12
v_add_u32 v19,81920,v13
T a[192:193],18,0
T a[194:195],18,512
T a[196:197],19,0
T a[198:199],19,512
T a[200:201],18,4096
T a[202:203],18,4608
T a[204:205],19,4096
T a[206:207],19,4608
s_waitcnt vmcnt(20) lgkmcnt(0)
s_barrier
s_add_u32 s55,s50,2490368
s_add_u32 m0,s57,81920
s_nop 0
buffer_load_dwordx4 v10,s[40:43],s55 offen offset:0  lds
s_add_u32 m0,s57,82944
s_nop 0
buffer_load_dwordx4 v11,s[40:43],s55 offen offset:0  lds
s_add_u32 m0,s57,89984
s_nop 0
buffer_load_dwordx4 v10,s[40:43],s55 offen offset:128  lds
s_add_u32 m0,s57,91008
s_nop 0
buffer_load_dwordx4 v11,s[40:43],s55 offen offset:128  lds
v_add_u32 v18,98304,v12
v_add_u32 v19,98304,v13
T a[208:209],18,0
T a[210:211],18,512
T a[212:213],19,0
T a[214:215],19,512
T a[216:217],18,4096
T a[218:219],18,4608
T a[220:221],19,4096
T a[222:223],19,4608
s_waitcnt vmcnt(20) lgkmcnt(0)
s_barrier
s_add_u32 s55,s50,2621440
s_add_u32 m0,s57,98304
s_nop 0
buffer_load_dwordx4 v10,s[40:43],s55 offen offset:0  lds
s_add_u32 m0,s57,99328
s_nop 0
buffer_load_dwordx4 v11,s[40:43],s55 offen offset:0  lds
s_add_u32 m0,s57,106368
s_nop 0
buffer_load_dwordx4 v10,s[40:43],s55 offen offset:128  lds
s_add_u32 m0,s57,107392
s_nop 0
buffer_load_dwordx4 v11,s[40:43],s55 offen offset:128  lds
v_add_u32 v18,0,v12
v_add_u32 v19,0,v13
T a[224:225],18,0
T a[226:227],18,512
T a[228:229],19,0
)"
R"(T a[230:231],19,512
T a[232:233],18,4096
T a[234:235],18,4608
T a[236:237],19,4096
T a[238:239],19,4608
s_waitcnt vmcnt(20) lgkmcnt(0)
s_barrier
s_add_u32 s55,s50,2752512
s_add_u32 m0,s57,0
s_nop 0
buffer_load_dwordx4 v10,s[40:43],s55 offen offset:0  lds
s_add_u32 m0,s57,1024
s_nop 0
buffer_load_dwordx4 v11,s[40:43],s55 offen offset:0  lds
s_add_u32 m0,s57,8064
s_nop 0
buffer_load_dwordx4 v10,s[40:43],s55 offen offset:128  lds
s_add_u32 m0,s57,9088
s_nop 0
buffer_load_dwordx4 v11,s[40:43],s55 offen offset:128  lds
v_add_u32 v18,16384,v12
v_add_u32 v19,16384,v13
T a[240:241],18,0
T a[242:243],18,512
T a[244:245],19,0
T a[246:247],19,512
T a[248:249],18,4096
T a[250:251],18,4608
T a[252:253],19,4096
T a[254:255],19,4608
s_waitcnt vmcnt(20) lgkmcnt(0)
s_barrier
s_add_u32 s55,s50,2883584
s_add_u32 m0,s57,16384
s_nop 0
buffer_load_dwordx4 v10,s[40:43],s55 offen offset:0  lds
s_add_u32 m0,s57,17408
s_nop 0
buffer_load_dwordx4 v11,s[40:43],s55 offen offset:0  lds
s_add_u32 m0,s57,24448
s_nop 0
buffer_load_dwordx4 v10,s[40:43],s55 offen offset:128  lds
s_add_u32 m0,s57,25472
s_nop 0
buffer_load_dwordx4 v11,s[40:43],s55 offen offset:128  lds
v_add_u32 v18,32768,v12
v_add_u32 v19,32768,v13
T v[64:65],18,0
T v[66:67],18,512
T v[68:69],19,0
T v[70:71],19,512
T v[72:73],18,4096
T v[74:75],18,4608
T v[76:77],19,4096
T v[78:79],19,4608
s_waitcnt vmcnt(20) lgkmcnt(0)
s_barrier
s_add_u32 s55,s50,3014656
s_add_u32 m0,s57,32768
s_nop 0
buffer_load_dwordx4 v10,s[40:43],s55 offen offset:0  lds
s_add_u32 m0,s57,33792
s_nop 0
buffer_load_dwordx4 v11,s[40:43],s55 offen offset:0  lds
s_add_u32 m0,s57,40832
s_nop 0
buffer_load_dwordx4 v10,s[40:43],s55 offen offset:128  lds
s_add_u32 m0,s57,41856
s_nop 0
buffer_load_dwordx4 v11,s[40:43],s55 offen offset:128  lds
v_add_u32 v18,49152,v12
v_add_u32 v19,49152,v13
T v[80:81],18,0
T v[82:83],18,512
T v[84:85],19,0
T v[86:87],19,512
T v[88:89],18,4096
T v[90:91],18,4608
T v[92:93],19,4096
T v[94:95],19,4608
s_waitcnt vmcnt(20) lgkmcnt(0)
s_barrier
s_add_u32 s55,s50,3145728
s_add_u32 m0,s57,49152
s_nop 0
buffer_load_dwordx4 v10,s[40:43],s55 offen offset:0  lds
s_add_u32 m0,s57,50176
s_nop 0
buffer_load_dwordx4 v11,s[40:43],s55 offen offset:0  lds
s_add_u32 m0,s57,57216
s_nop 0
buffer_load_dwordx4 v10,s[40:43],s55 offen offset:128  lds
s_add_u32 m0,s57,58240
s_nop 0
buffer_load_dwordx4 v11,s[40:43],s55 offen offset:128  lds
v_add_u32 v18,65536,v12
v_add_u32 v19,65536,v13
T v[96:97],18,0
T v[98:99],18,512
T v[100:101],19,0
T v[102:103],19,512
T v[104:105],18,4096
T v[106:107],18,4608
T v[108:109],19,4096
T v[110:111],19,4608
s_waitcnt vmcnt(20) lgkmcnt(0)
s_barrier
s_add_u32 s55,s50,3276800
s_add_u32 m0,s57,65536
s_nop 0
buffer_load_dwordx4 v10,s[40:43],s55 offen offset:0  lds
s_add_u32 m0,s57,66560
s_nop 0
buffer_load_dwordx4 v11,s[40:43],s55 offen offset:0  lds
s_add_u32 m0,s57,73600
s_nop 0
buffer_load_dwordx4 v10,s[40:43],s55 offen offset:128  lds
s_add_u32 m0,s57,74624
s_nop 0
buffer_load_dwordx4 v11,s[40:43],s55 offen offset:128  lds
v_add_u32 v18,81920,v12
v_add_u32 v19,81920,v13
T v[112:113],18,0
T v[114:115],18,512
T v[116:117],19,0
T v[118:119],19,512
T v[120:121],18,4096
T v[122:123],18,4608
T v[124:125],19,4096
T v[126:127],19,4608
s_waitcnt vmcnt(20) lgkmcnt(0)
s_barrier
s_add_u32 s55,s50,3407872
s_add_u32 m0,s57,81920
s_nop 0
buffer_load_dwordx4 v10,s[40:43],s55 offen offset:0  lds
s_add_u32 m0,s57,82944
s_nop 0
buffer_load_dwordx4 v11,s[40:43],s55 offen offset:0  lds
s_add_u32 m0,s57,89984
s_nop 0
buffer_load_dwordx4 v10,s[40:43],s55 offen offset:128  lds
s_add_u32 m0,s57,91008
s_nop 0
buffer_load_dwordx4 v11,s[40:43],s55 offen offset:128  lds
v_add_u32 v18,98304,v12
v_add_u32 v19,98304,v13
T v[128:129],18,0
T v[130:131],18,512
T v[132:133],19,0
T v[134:135],19,512
T v[136:137],18,4096
T v[138:139],18,4608
T v[140:141],19,4096
T v[142:143],19,4608
s_waitcnt vmcnt(20) lgkmcnt(0)
s_barrier
s_add_u32 s55,s50,3538944
s_add_u32 m0,s57,98304
s_nop 0
buffer_load_dwordx4 v10,s[40:43],s55 offen offset:0  lds
s_add_u32 m0,s57,99328
s_nop 0
buffer_load_dwordx4 v11,s[40:43],s55 offen offset:0  lds
s_add_u32 m0,s57,106368
s_nop 0
buffer_load_dwordx4 v10,s[40:43],s55 offen offset:128  lds
s_add_u32 m0,s57,107392
s_nop 0
buffer_load_dwordx4 v11,s[40:43],s55 offen offset:128  lds
v_add_u32 v18,0,v12
v_add_u32 v19,0,v13
T v[144:145],18,0
T v[146:147],18,512
T v[148:149],19,0
T v[150:151],19,512
T v[152:153],18,4096
T v[154:155],18,4608
T v[156:157],19,4096
T v[158:159],19,4608
s_waitcnt vmcnt(20) lgkmcnt(0)
s_barrier
v_add_u32 v18,16384,v12
v_add_u32 v19,16384,v13
T v[160:161],18,0
T v[162:163],18,512
T v[164:165],19,0
T v[166:167],19,512
T v[168:169],18,4096
T v[170:171],18,4608
T v[172:173],19,4096
T v[174:175],19,4608
s_waitcnt vmcnt(16) lgkmcnt(0)
s_barrier
v_add_u32 v18,32768,v12
v_add_u32 v19,32768,v13
T v[176:177],18,0
T v[178:179],18,512
T v[180:181],19,0
T v[182:183],19,512
T v[184:185],18,4096
T v[186:187],18,4608
T v[188:189],19,4096
T v[190:191],19,4608
s_waitcnt vmcnt(12) lgkmcnt(0)
s_barrier
v_add_u32 v18,49152,v12
v_add_u32 v19,49152,v13
T v[192:193],18,0
T v[194:195],18,512
T v[196:197],19,0
T v[198:199],19,512
T v[200:201],18,4096
T v[202:203],18,4608
T v[204:205],19,4096
T v[206:207],19,4608
s_waitcnt vmcnt(8) lgkmcnt(0)
s_barrier
v_add_u32 v18,65536,v12
v_add_u32 v19,65536,v13
T v[208:209],18,0
T v[210:211],18,512
T v[212:213],19,0
T v[214:215],19,512
T v[216:217],18,4096
T v[218:219],18,4608
T v[220:221],19,4096
T v[222:223],19,4608
s_waitcnt vmcnt(4) lgkmcnt(0)
s_barrier
v_add_u32 v18,81920,v12
v_add_u32 v19,81920,v13
T v[224:225],18,0
T v[226:227],18,512
T v[228:229],19,0
T v[230:231],19,512
T v[232:233],18,4096
T v[234:235],18,4608
T v[236:237],19,4096
T v[238:239],19,4608
s_waitcnt vmcnt(0) lgkmcnt(0)
s_barrier
v_add_u32 v18,98304,v12
v_add_u32 v19,98304,v13
T v[240:241],18,0
T v[242:243],18,512
T v[244:245],19,0
T v[246:247],19,512
T v[248:249],18,4096
T v[250:251],18,4608
T v[252:253],19,4096
T v[254:255],19,4608
L 0
s_barrier
v_mov_b32 v4,s53
s_mov_b32 s82,0
s_mov_b32 s80,0
s_mov_b32 s54,s45
s_mul_i32 s57,s44,7168
s_mul_i32 s55,s45,114688
s_add_u32 s55,s55,s57
s_add_u32 s55,s55,40894464
s_mul_i32 s57,s44,7168
s_mul_i32 s60,s46,114688
s_mul_i32 s61,s46,2560
s_mul_i32 s62,s46,32768
s_mul_i32 s63,s46,2048
s_mul_i32 s64,s46,512
s_lshl_b32 s59,s44,8
s_add_u32 s59,s59,143360
s_mul_i32 s58,s45,2560
s_add_u32 s58,s58,109891584
s_mul_i32 s65,s45,32768
s_add_u32 s65,s65,s47
s_mul_i32 s68,s45,2048
s_add_u32 s68,s68,s48
s_mul_i32 s73,s45,512
s_add_u32 s73,s73,106954752
s_mul_i32 s74,s45,32768
s_add_u32 s74,s74,s49
s_mul_i32 s75,s45,1024
s_add_u32 s75,s75,107479040
s_mov_b32 s66,0x7fff0000
s_mov_b32 s67,0x7fff0000
s_mov_b32 s69,0x7fff0000
s_mov_b32 s76,0x7fff0000
s_mov_b32 s77,0x7fff0000
s_mov_b32 s70,0x7fff0000
s_mov_b32 s71,0x7fff0000
s_mov_b32 s72,0x7fff0000
v_mov_b32 v1,v0
v_add_u32 v2,28672,v0
s_mov_b32 s84,28672
s_mov_b32 s86,0x3a800000
s_mov_b32 s87,0x3727c5ac
s_mov_b32 s88,0xc0135761
s_mov_b32 s89,0x3d372713
s_and_b32 s85,s44,1
s_mul_i32 s85,s85,127
s_add_u32 s55,s55,s51
v_lshlrev_b32 v8,22,v0
v_and_b32 v8,0x80000000,v8
v_or_b32 v8,v8,v5
v_add_u32 v2,57344,v0
s_lshl_b32 s84,s57,1
s_add_u32 s84,s84,57344
s_mov_b64 s[86:87],%21
s_mov_b64 s[40:41],%21
s_mov_b32 s42,4194304
s_and_b32 s78,s47,65535
s_lshr_b32 s78,s78,4
s_sub_u32 s85,s47,s78
s_add_u32 s85,s85,4194304
s_mul_i32 s73,s44,1280
s_add_u32 s73,s73,114688
v_lshrrev_b32 v62,4,v9
v_and_b32 v62,3,v62
v_mul_u32_u24 v62,288,v62
v_lshrrev_b32 v63,7,v9
v_and_b32 v5,12,v9
v_add3_u32 v5,v5,v62,v63
v_add_u32 v5,s73,v5
v_mov_b32 v10,0
v_mov_b32 v11,0
v_mov_b32 v12,0
v_mov_b32 v13,0
buffer_store_dwordx4 v[10:13],v7,s[40:43],0 offen offset:0 sc0 sc1
s_getpc_b64 s[40:41]
Lg4_pc_%=:
s_lshl_b32 s78,s44,13
v_lshlrev_b32 v62,3,v0
v_add_u32 v62,s78,v62
v_min_u32 v62,Lcode_g4_e-Lg4_pc_%=-4,v62
s_mov_b32 m0,163584
s_nop 0
global_load_lds_dword v62,s[40:41]
s_add_u32 m0,s59,4096
s_nop 0
buffer_load_dword v8,s[36:39],s58 offen offset:0 sc1 lds
buffer_store_dword v4,v3,s[36:39],s71 offen offset:0 sc0 sc1
s_add_u32 m0,s59,5120
s_nop 0
buffer_load_dword v8,s[36:39],s58 offen offset:0 sc1 lds
buffer_store_dword v4,v3,s[36:39],s71 offen offset:0 sc0 sc1
s_mov_b32 s83,0
Lg4_poll_%=:
s_add_u32 s78,s83,2
s_and_b32 s78,s78,3
s_lshl_b32 s78,s78,10
s_add_u32 s78,s78,4096
s_add_u32 m0,s59,s78
s_nop 0
buffer_load_dword v8,s[36:39],s58 offen offset:0 sc1 lds
s_and_b32 s78,s83,3
s_lshl_b32 s78,s78,10
v_add_u32 v62,s78,v6
s_waitcnt vmcnt(4)
s_barrier
ds_read_b32 v7,v62 offset:4096
buffer_store_dword v4,v3,s[36:39],s70 offen offset:0 sc0 sc1
s_mov_b32 s70,s71
s_mov_b32 s71,s72
s_mov_b32 s72,0x7fff0000
s_add_u32 s83,s83,1
s_add_u32 s82,s82,1
L 0
v_cmp_eq_u32 vcc,s53,v7
s_nop 0
s_cmp_eq_u32 vcc_lo,-1
s_cbranch_scc1 Lg4_go_%=
s_cmp_lt_u32 s82,20000
s_cbranch_scc1 Lg4_poll_%=
Lg4_go_%=:
s_add_u32 m0,s57,0
s_add_u32 s56,s55,0
D 0,56,0
D 0,56,1024
D 0,56,2048
D 0,56,3072
s_add_u32 m0,s57,4096
s_add_u32 s56,s56,4096
D 0,56,0
D 0,56,1024
D 0,56,2048
s_add_u32 s58,s58,s61
s_add_u32 m0,s59,2048
s_nop 0
buffer_load_dword v8,s[36:39],s58 offen offset:0 sc1 lds
s_add_u32 m0,s57,28672
s_add_u32 s56,s55,28672
D 0,56,0
D 0,56,1024
D 0,56,2048
D 0,56,3072
s_add_u32 m0,s57,32768
s_add_u32 s56,s56,4096
D 0,56,0
D 0,56,1024
D 0,56,2048
s_add_u32 s55,s55,s60
s_sub_u32 s57,s84,s57
s_add_u32 s54,s54,s46
s_waitcnt vmcnt(7)
s_barrier
buffer_store_dword v4,v3,s[36:39],s70 offen offset:0 sc0 sc1
s_min_u32 s71,s71,s72
buffer_store_dword v4,v3,s[36:39],s71 offen offset:0 sc0 sc1
s_mov_b32 s70,0x7fff0000
s_mov_b32 s71,0x7fff0000
s_mov_b32 s72,0x7fff0000
s_cmp_eq_u32 s80,1
s_cbranch_scc1 Lg4_st1_%=
s_branch Lg4_st0_%=
Lg4_gox_%=:
s_add_u32 s58,s58,s61
s_add_u32 m0,s59,2048
s_nop 0
buffer_load_dword v8,s[36:39],s58 offen offset:0 sc1 lds
s_waitcnt vmcnt(0)
s_barrier
buffer_store_dword v4,v3,s[36:39],s70 offen offset:0 sc0 sc1
s_min_u32 s71,s71,s72
buffer_store_dword v4,v3,s[36:39],s71 offen offset:0 sc0 sc1
s_mov_b32 s70,0x7fff0000
s_mov_b32 s71,0x7fff0000
s_mov_b32 s72,0x7fff0000
s_cmp_eq_u32 s80,1
s_cbranch_scc1 Lg4_st1_%=
Lg4_st0_%=:
ds_read_b32 v7,v6 offset:2048
R 26,1,0
R 30,1,1024
R 34,1,2048
R 38,1,3072
R 42,1,4096
L 5
v_cmp_eq_u32 vcc,s53,v7
L 3
s_cmp_eq_u32 vcc_lo,-1
s_cbranch_scc1 Lg4_F0_%=
s_branch Lg4_L0_%=
Lg4_st1_%=:
ds_read_b32 v7,v6 offset:2048
R 26,2,0
R 30,2,1024
R 34,2,2048
R 38,2,3072
R 42,2,4096
L 5
v_cmp_eq_u32 vcc,s53,v7
L 3
s_cmp_eq_u32 vcc_lo,-1
s_cbranch_scc1 Lg4_F1_%=
s_branch Lg4_L1_%=
Lg4_F1_%=:
s_cmp_eq_u32 s44,1
s_cbranch_scc1 Lg4_F1w1_%=
s_cmp_eq_u32 s44,2
s_cbranch_scc1 Lg4_F1w2_%=
s_cmp_eq_u32 s44,3
s_cbranch_scc1 Lg4_F1w3_%=
s_branch Lg4_F1w0_%=
Lg4_F0_%=:
s_cmp_eq_u32 s44,1
s_cbranch_scc1 Lg4_F0w1_%=
s_cmp_eq_u32 s44,2
s_cbranch_scc1 Lg4_F0w2_%=
s_cmp_eq_u32 s44,3
s_cbranch_scc1 Lg4_F0w3_%=
Lg4_F0w0_%=:
v_mfma_f32_16x16x32_bf16 v[14:17],a[4:7],v[26:29],0
s_add_u32 m0,s57,0
R 46,1,5120
v_lshlrev_b32 v62,16,v58
M 14,a[12:15],30
s_add_u32 s56,s55,0
v_add_f32 v18,v18,v62
v_and_b32 v63,0xffff0000,v58
v_mfma_f32_16x16x32_bf16 v[10:13],a[0:3],v[26:29],0
s_add_u32 s58,s58,s61
R 50,1,6144
v_add_f32 v19,v19,v63
M 10,a[8:11],30
L 3
D 0,56,0
M 10,a[16:19],34
s_mov_b32 s66,s65
R 54,1,7168
v_lshlrev_b32 v62,16,v59
M 10,a[24:27],38
s_add_u32 s65,s65,s62
v_add_f32 v20,v20,v62
v_and_b32 v63,0xffff0000,v59
M 14,a[20:23],34
s_add_u32 s54,s54,s46
R 26,1,8192
v_add_f32 v21,v21,v63
M 14,a[28:31],38
L 3
v_lshlrev_b32 v62,16,v60
)"
R"(v_add_f32 v22,v22,v62
M 14,a[36:39],42
R 30,1,9216
v_and_b32 v63,0xffff0000,v60
M 14,a[44:47],46
D 0,56,1024
M 10,a[32:35],42
R 34,1,10240
v_add_f32 v23,v23,v63
M 10,a[40:43],46
L 3
v_lshlrev_b32 v62,16,v61
v_add_f32 v24,v24,v62
M 10,a[48:51],50
R 38,1,11264
v_and_b32 v63,0xffff0000,v61
M 10,a[56:59],54
buffer_load_dwordx4 v[58:61],v0,s[36:39],s74 offen offset:0 sc1
M 14,a[52:55],50
s_add_u32 s74,s74,s62
R 42,1,12288
v_add_f32 v25,v25,v63
M 14,a[60:63],54
L 3
v_cvt_pk_bf16_f32 v18,v18,v19
v_cvt_pk_bf16_f32 v19,v20,v21
M 14,a[68:71],26
R 46,1,13312
v_cvt_pk_bf16_f32 v20,v22,v23
M 14,a[76:79],30
D 0,56,2048
M 10,a[64:67],26
R 50,1,14336
v_cvt_pk_bf16_f32 v21,v24,v25
M 10,a[72:75],30
L 3
buffer_store_dwordx4 v[18:21],v0,s[36:39],s67 offen offset:0 sc0 sc1
M 10,a[80:83],34
R 54,1,15360
M 10,a[88:91],38
M 14,a[84:87],34
R 26,1,16384
M 14,a[92:95],38
L 3
M 14,a[100:103],42
R 30,1,17408
M 14,a[108:111],46
D 0,56,3072
M 10,a[96:99],42
s_add_u32 m0,s57,4096
s_add_u32 s56,s56,4096
R 34,1,18432
M 10,a[104:107],46
L 3
M 10,a[112:115],50
R 38,1,19456
M 10,a[120:123],54
M 14,a[116:119],50
R 42,1,20480
M 14,a[124:127],54
L 3
D 0,56,0
M 14,a[132:135],26
R 46,1,21504
M 14,a[140:143],30
M 10,a[128:131],26
R 50,1,22528
M 10,a[136:139],30
L 3
M 10,a[144:147],34
R 54,1,23552
M 10,a[152:155],38
M 14,a[148:151],34
R 26,1,24576
M 14,a[156:159],38
L 3
D 0,56,1024
M 14,a[164:167],42
R 30,1,25600
M 14,a[172:175],46
M 10,a[160:163],42
R 34,1,26624
M 10,a[168:171],46
L 3
M 10,a[176:179],50
s_waitcnt vmcnt(8)
R 38,1,27648
M 10,a[184:187],54
s_barrier
M 14,a[180:183],50
R 42,1,28672
M 14,a[188:191],54
L 3
D 0,56,2048
M 14,a[196:199],26
s_add_u32 m0,s59,0
s_add_u32 s56,s55,28672
R 46,1,29696
M 14,a[204:207],30
s_add_u32 s55,s55,s60
M 10,a[192:195],26
R 50,1,30720
M 10,a[200:203],30
L 3
M 10,a[208:211],34
R 54,1,31744
M 10,a[216:219],38
M 14,a[212:215],34
R 26,1,32768
M 14,a[220:223],38
L 3
buffer_load_dword v8,s[36:39],s58 offen offset:0 sc1 lds
M 14,a[228:231],42
s_add_u32 m0,s57,28672
R 30,1,33792
M 14,a[236:239],46
D 0,56,0
M 10,a[224:227],42
R 34,1,34816
M 10,a[232:235],46
L 3
M 10,a[240:243],50
R 38,1,35840
M 10,a[248:251],54
M 14,a[244:247],50
R 42,1,36864
M 14,a[252:255],54
L 3
M 14,v[68:71],26
R 46,1,37888
M 14,v[76:79],30
D 0,56,1024
M 10,v[64:67],26
R 50,1,38912
M 10,v[72:75],30
L 3
M 10,v[80:83],34
R 54,1,39936
M 10,v[88:91],38
M 14,v[84:87],34
R 26,1,40960
M 14,v[92:95],38
L 3
M 14,v[100:103],42
R 30,1,41984
M 14,v[108:111],46
D 0,56,2048
M 10,v[96:99],42
R 34,1,43008
M 10,v[104:107],46
L 3
M 10,v[112:115],50
R 38,1,44032
M 10,v[120:123],54
M 14,v[116:119],50
R 42,1,45056
M 14,v[124:127],54
L 3
D 0,56,3072
M 14,v[132:135],26
s_add_u32 m0,s57,32768
s_add_u32 s56,s56,4096
R 46,1,46080
M 14,v[140:143],30
s_sub_u32 s57,s84,s57
M 10,v[128:131],26
R 50,1,47104
M 10,v[136:139],30
L 3
M 10,v[144:147],34
R 54,1,48128
M 10,v[152:155],38
M 14,v[148:151],34
R 26,1,49152
M 14,v[156:159],38
L 3
D 0,56,0
M 14,v[164:167],42
R 30,1,50176
M 14,v[172:175],46
M 10,v[160:163],42
R 34,1,51200
M 10,v[168:171],46
L 3
M 10,v[176:179],50
R 38,1,52224
M 10,v[184:187],54
M 14,v[180:183],50
R 42,1,53248
M 14,v[188:191],54
L 3
D 0,56,1024
M 14,v[196:199],26
R 46,1,54272
M 14,v[204:207],30
M 10,v[192:195],26
R 50,1,55296
M 10,v[200:203],30
L 3
M 10,v[208:211],34
s_waitcnt vmcnt(6)
R 54,1,56320
M 10,v[216:219],38
s_barrier
ds_read_b32 v7,v6 offset:0
M 14,v[212:215],34
R 26,2,0
M 14,v[220:223],38
L 4
buffer_store_dword v4,v3,s[36:39],s69 offen offset:0 sc0 sc1
M 14,v[228:231],42
s_mov_b32 s69,s68
s_add_u32 s68,s68,s63
R 30,2,1024
M 14,v[236:239],46
D 0,56,2048
M 10,v[224:227],42
R 34,2,2048
M 10,v[232:235],46
L 4
L 3
M 10,v[240:243],50
R 38,2,3072
v_cmp_eq_u32 vcc,s53,v7
M 10,v[248:251],54
s_cmp_eq_u32 vcc_lo,-1
M 14,v[244:247],50
R 42,2,4096
M 14,v[252:255],54
L 3
s_cbranch_scc0 Lg4_L1_%=
Lg4_F1w0_%=:
v_mfma_f32_16x16x32_bf16 v[22:25],a[4:7],v[26:29],0
s_add_u32 m0,s57,0
R 46,2,5120
v_lshlrev_b32 v62,16,v58
M 22,a[12:15],30
s_add_u32 s56,s55,0
v_add_f32 v10,v10,v62
v_and_b32 v63,0xffff0000,v58
v_mfma_f32_16x16x32_bf16 v[18:21],a[0:3],v[26:29],0
s_add_u32 s58,s58,s61
R 50,2,6144
v_add_f32 v11,v11,v63
M 18,a[8:11],30
L 3
D 0,56,0
M 18,a[16:19],34
s_mov_b32 s67,s65
R 54,2,7168
v_lshlrev_b32 v62,16,v59
M 18,a[24:27],38
s_add_u32 s65,s65,s62
v_add_f32 v12,v12,v62
v_and_b32 v63,0xffff0000,v59
M 22,a[20:23],34
s_add_u32 s54,s54,s46
R 26,2,8192
v_add_f32 v13,v13,v63
M 22,a[28:31],38
L 3
v_lshlrev_b32 v62,16,v60
v_add_f32 v14,v14,v62
M 22,a[36:39],42
R 30,2,9216
v_and_b32 v63,0xffff0000,v60
M 22,a[44:47],46
D 0,56,1024
M 18,a[32:35],42
R 34,2,10240
v_add_f32 v15,v15,v63
M 18,a[40:43],46
L 3
v_lshlrev_b32 v62,16,v61
v_add_f32 v16,v16,v62
M 18,a[48:51],50
R 38,2,11264
v_and_b32 v63,0xffff0000,v61
M 18,a[56:59],54
buffer_load_dwordx4 v[58:61],v0,s[36:39],s74 offen offset:0 sc1
M 22,a[52:55],50
s_add_u32 s74,s74,s62
R 42,2,12288
v_add_f32 v17,v17,v63
M 22,a[60:63],54
L 3
v_cvt_pk_bf16_f32 v10,v10,v11
v_cvt_pk_bf16_f32 v11,v12,v13
M 22,a[68:71],26
R 46,2,13312
v_cvt_pk_bf16_f32 v12,v14,v15
M 22,a[76:79],30
D 0,56,2048
M 18,a[64:67],26
R 50,2,14336
v_cvt_pk_bf16_f32 v13,v16,v17
M 18,a[72:75],30
L 3
buffer_store_dwordx4 v[10:13],v0,s[36:39],s66 offen offset:0 sc0 sc1
M 18,a[80:83],34
R 54,2,15360
M 18,a[88:91],38
M 22,a[84:87],34
R 26,2,16384
M 22,a[92:95],38
L 3
M 22,a[100:103],42
R 30,2,17408
M 22,a[108:111],46
D 0,56,3072
M 18,a[96:99],42
s_add_u32 m0,s57,4096
s_add_u32 s56,s56,4096
R 34,2,18432
M 18,a[104:107],46
L 3
M 18,a[112:115],50
R 38,2,19456
M 18,a[120:123],54
M 22,a[116:119],50
R 42,2,20480
M 22,a[124:127],54
L 3
D 0,56,0
M 22,a[132:135],26
R 46,2,21504
M 22,a[140:143],30
M 18,a[128:131],26
R 50,2,22528
M 18,a[136:139],30
L 3
M 18,a[144:147],34
R 54,2,23552
M 18,a[152:155],38
M 22,a[148:151],34
R 26,2,24576
M 22,a[156:159],38
L 3
D 0,56,1024
M 22,a[164:167],42
R 30,2,25600
M 22,a[172:175],46
M 18,a[160:163],42
R 34,2,26624
M 18,a[168:171],46
L 3
M 18,a[176:179],50
s_waitcnt vmcnt(8)
R 38,2,27648
M 18,a[184:187],54
s_barrier
M 22,a[180:183],50
R 42,2,28672
M 22,a[188:191],54
L 3
D 0,56,2048
M 22,a[196:199],26
s_add_u32 m0,s59,1024
s_add_u32 s56,s55,28672
R 46,2,29696
M 22,a[204:207],30
s_add_u32 s55,s55,s60
M 18,a[192:195],26
R 50,2,30720
M 18,a[200:203],30
L 3
M 18,a[208:211],34
R 54,2,31744
M 18,a[216:219],38
M 22,a[212:215],34
R 26,2,32768
M 22,a[220:223],38
L 3
buffer_load_dword v8,s[36:39],s58 offen offset:0 sc1 lds
M 22,a[228:231],42
s_add_u32 m0,s57,28672
R 30,2,33792
M 22,a[236:239],46
D 0,56,0
M 18,a[224:227],42
R 34,2,34816
M 18,a[232:235],46
L 3
M 18,a[240:243],50
R 38,2,35840
M 18,a[248:251],54
M 22,a[244:247],50
R 42,2,36864
M 22,a[252:255],54
L 3
M 22,v[68:71],26
R 46,2,37888
M 22,v[76:79],30
D 0,56,1024
M 18,v[64:67],26
R 50,2,38912
M 18,v[72:75],30
L 3
M 18,v[80:83],34
R 54,2,39936
M 18,v[88:91],38
M 22,v[84:87],34
R 26,2,40960
M 22,v[92:95],38
L 3
M 22,v[100:103],42
R 30,2,41984
M 22,v[108:111],46
D 0,56,2048
M 18,v[96:99],42
R 34,2,43008
M 18,v[104:107],46
L 3
M 18,v[112:115],50
R 38,2,44032
M 18,v[120:123],54
M 22,v[116:119],50
R 42,2,45056
M 22,v[124:127],54
L 3
D 0,56,3072
M 22,v[132:135],26
s_add_u32 m0,s57,32768
s_add_u32 s56,s56,4096
R 46,2,46080
M 22,v[140:143],30
s_sub_u32 s57,s84,s57
M 18,v[128:131],26
R 50,2,47104
M 18,v[136:139],30
L 3
M 18,v[144:147],34
R 54,2,48128
M 18,v[152:155],38
M 22,v[148:151],34
R 26,2,49152
M 22,v[156:159],38
L 3
D 0,56,0
M 22,v[164:167],42
R 30,2,50176
M 22,v[172:175],46
M 18,v[160:163],42
R 34,2,51200
M 18,v[168:171],46
L 3
M 18,v[176:179],50
R 38,2,52224
M 18,v[184:187],54
M 22,v[180:183],50
R 42,2,53248
M 22,v[188:191],54
L 3
D 0,56,1024
M 22,v[196:199],26
R 46,2,54272
M 22,v[204:207],30
M 18,v[192:195],26
R 50,2,55296
M 18,v[200:203],30
L 3
M 18,v[208:211],34
s_waitcnt vmcnt(6)
R 54,2,56320
M 18,v[216:219],38
s_barrier
ds_read_b32 v7,v6 offset:1024
M 22,v[212:215],34
R 26,1,0
M 22,v[220:223],38
L 4
buffer_store_dword v4,v3,s[36:39],s69 offen offset:0 sc0 sc1
M 22,v[228:231],42
s_mov_b32 s69,s68
s_add_u32 s68,s68,s63
R 30,1,1024
M 22,v[236:239],46
D 0,56,2048
M 18,v[224:227],42
R 34,1,2048
M 18,v[232:235],46
L 4
L 3
M 18,v[240:243],50
R 38,1,3072
v_cmp_eq_u32 vcc,s53,v7
M 18,v[248:251],54
s_cmp_eq_u32 vcc_lo,-1
M 22,v[244:247],50
R 42,1,4096
M 22,v[252:255],54
L 3
s_cbranch_scc1 Lg4_F0w0_%=
s_branch Lg4_L0_%=
Lg4_F0w1_%=:
v_mfma_f32_16x16x32_bf16 v[14:17],a[4:7],v[26:29],0
s_add_u32 m0,s57,0
R 46,1,5120
v_lshlrev_b32 v62,16,v58
M 14,a[12:15],30
)"
R"(s_add_u32 s56,s55,0
v_add_f32 v18,v18,v62
v_and_b32 v63,0xffff0000,v58
v_mfma_f32_16x16x32_bf16 v[10:13],a[0:3],v[26:29],0
s_add_u32 s58,s58,s61
R 50,1,6144
v_add_f32 v19,v19,v63
M 10,a[8:11],30
L 3
D 0,56,0
M 10,a[16:19],34
s_mov_b32 s66,s65
R 54,1,7168
v_lshlrev_b32 v62,16,v59
M 10,a[24:27],38
s_add_u32 s65,s65,s62
v_add_f32 v20,v20,v62
v_and_b32 v63,0xffff0000,v59
M 14,a[20:23],34
s_add_u32 s54,s54,s46
R 26,1,8192
v_add_f32 v21,v21,v63
M 14,a[28:31],38
L 3
v_lshlrev_b32 v62,16,v60
v_add_f32 v22,v22,v62
M 14,a[36:39],42
R 30,1,9216
v_and_b32 v63,0xffff0000,v60
M 14,a[44:47],46
D 0,56,1024
M 10,a[32:35],42
R 34,1,10240
v_add_f32 v23,v23,v63
M 10,a[40:43],46
L 3
v_lshlrev_b32 v62,16,v61
v_add_f32 v24,v24,v62
M 10,a[48:51],50
R 38,1,11264
v_and_b32 v63,0xffff0000,v61
M 10,a[56:59],54
buffer_load_dwordx4 v[58:61],v0,s[36:39],s74 offen offset:0 sc1
M 14,a[52:55],50
s_add_u32 s74,s74,s62
R 42,1,12288
v_add_f32 v25,v25,v63
M 14,a[60:63],54
L 3
v_cvt_pk_bf16_f32 v18,v18,v19
v_cvt_pk_bf16_f32 v19,v20,v21
M 14,a[68:71],26
R 46,1,13312
v_cvt_pk_bf16_f32 v20,v22,v23
M 14,a[76:79],30
D 0,56,2048
M 10,a[64:67],26
R 50,1,14336
v_cvt_pk_bf16_f32 v21,v24,v25
M 10,a[72:75],30
L 3
buffer_store_dwordx4 v[18:21],v0,s[36:39],s67 offen offset:0 sc0 sc1
M 10,a[80:83],34
R 54,1,15360
M 10,a[88:91],38
M 14,a[84:87],34
R 26,1,16384
M 14,a[92:95],38
L 3
M 14,a[100:103],42
R 30,1,17408
M 14,a[108:111],46
D 0,56,3072
M 10,a[96:99],42
s_add_u32 m0,s57,4096
s_add_u32 s56,s56,4096
R 34,1,18432
M 10,a[104:107],46
L 3
M 10,a[112:115],50
R 38,1,19456
M 10,a[120:123],54
M 14,a[116:119],50
R 42,1,20480
M 14,a[124:127],54
L 3
D 0,56,0
M 14,a[132:135],26
R 46,1,21504
M 14,a[140:143],30
M 10,a[128:131],26
R 50,1,22528
M 10,a[136:139],30
L 3
M 10,a[144:147],34
R 54,1,23552
M 10,a[152:155],38
M 14,a[148:151],34
R 26,1,24576
M 14,a[156:159],38
L 3
D 0,56,1024
M 14,a[164:167],42
R 30,1,25600
M 14,a[172:175],46
M 10,a[160:163],42
R 34,1,26624
M 10,a[168:171],46
s_waitcnt vmcnt(8) lgkmcnt(3)
M 10,a[176:179],50
s_barrier
R 38,1,27648
M 10,a[184:187],54
M 14,a[180:183],50
R 42,1,28672
M 14,a[188:191],54
L 3
D 0,56,2048
M 14,a[196:199],26
s_add_u32 m0,s59,0
s_add_u32 s56,s55,28672
R 46,1,29696
M 14,a[204:207],30
s_add_u32 s55,s55,s60
M 10,a[192:195],26
R 50,1,30720
M 10,a[200:203],30
L 3
M 10,a[208:211],34
R 54,1,31744
M 10,a[216:219],38
M 14,a[212:215],34
R 26,1,32768
M 14,a[220:223],38
L 3
buffer_load_dword v8,s[36:39],s58 offen offset:0 sc1 lds
M 14,a[228:231],42
s_add_u32 m0,s57,28672
R 30,1,33792
M 14,a[236:239],46
D 0,56,0
M 10,a[224:227],42
R 34,1,34816
M 10,a[232:235],46
L 3
M 10,a[240:243],50
R 38,1,35840
M 10,a[248:251],54
M 14,a[244:247],50
R 42,1,36864
M 14,a[252:255],54
L 3
M 14,v[68:71],26
R 46,1,37888
M 14,v[76:79],30
D 0,56,1024
M 10,v[64:67],26
R 50,1,38912
M 10,v[72:75],30
L 3
M 10,v[80:83],34
R 54,1,39936
M 10,v[88:91],38
M 14,v[84:87],34
R 26,1,40960
M 14,v[92:95],38
L 3
M 14,v[100:103],42
R 30,1,41984
M 14,v[108:111],46
D 0,56,2048
M 10,v[96:99],42
R 34,1,43008
M 10,v[104:107],46
L 3
M 10,v[112:115],50
R 38,1,44032
M 10,v[120:123],54
M 14,v[116:119],50
R 42,1,45056
M 14,v[124:127],54
L 3
D 0,56,3072
M 14,v[132:135],26
s_add_u32 m0,s57,32768
s_add_u32 s56,s56,4096
R 46,1,46080
M 14,v[140:143],30
s_sub_u32 s57,s84,s57
M 10,v[128:131],26
R 50,1,47104
M 10,v[136:139],30
L 3
M 10,v[144:147],34
R 54,1,48128
M 10,v[152:155],38
M 14,v[148:151],34
R 26,1,49152
M 14,v[156:159],38
L 3
D 0,56,0
M 14,v[164:167],42
R 30,1,50176
M 14,v[172:175],46
M 10,v[160:163],42
R 34,1,51200
M 10,v[168:171],46
L 3
M 10,v[176:179],50
R 38,1,52224
M 10,v[184:187],54
M 14,v[180:183],50
R 42,1,53248
M 14,v[188:191],54
L 3
D 0,56,1024
M 14,v[196:199],26
R 46,1,54272
M 14,v[204:207],30
M 10,v[192:195],26
R 50,1,55296
M 10,v[200:203],30
s_waitcnt vmcnt(6) lgkmcnt(3)
M 10,v[208:211],34
s_barrier
R 54,1,56320
ds_read_b32 v7,v6 offset:0
M 10,v[216:219],38
buffer_store_dword v4,v3,s[36:39],s69 offen offset:0 sc0 sc1
M 14,v[212:215],34
s_mov_b32 s69,s68
s_add_u32 s68,s68,s63
R 26,2,0
M 14,v[220:223],38
L 4
D 0,56,2048
M 14,v[228:231],42
R 30,2,1024
M 14,v[236:239],46
M 10,v[224:227],42
L 2
R 34,2,2048
v_cmp_eq_u32 vcc,s53,v7
M 10,v[232:235],46
s_nop 0
s_cmp_eq_u32 vcc_lo,-1
M 10,v[240:243],50
R 38,2,3072
M 10,v[248:251],54
M 14,v[244:247],50
R 42,2,4096
M 14,v[252:255],54
L 3
s_cbranch_scc0 Lg4_L1_%=
Lg4_F1w1_%=:
v_mfma_f32_16x16x32_bf16 v[22:25],a[4:7],v[26:29],0
s_add_u32 m0,s57,0
R 46,2,5120
v_lshlrev_b32 v62,16,v58
M 22,a[12:15],30
s_add_u32 s56,s55,0
v_add_f32 v10,v10,v62
v_and_b32 v63,0xffff0000,v58
v_mfma_f32_16x16x32_bf16 v[18:21],a[0:3],v[26:29],0
s_add_u32 s58,s58,s61
R 50,2,6144
v_add_f32 v11,v11,v63
M 18,a[8:11],30
L 3
D 0,56,0
M 18,a[16:19],34
s_mov_b32 s67,s65
R 54,2,7168
v_lshlrev_b32 v62,16,v59
M 18,a[24:27],38
s_add_u32 s65,s65,s62
v_add_f32 v12,v12,v62
v_and_b32 v63,0xffff0000,v59
M 22,a[20:23],34
s_add_u32 s54,s54,s46
R 26,2,8192
v_add_f32 v13,v13,v63
M 22,a[28:31],38
L 3
v_lshlrev_b32 v62,16,v60
v_add_f32 v14,v14,v62
M 22,a[36:39],42
R 30,2,9216
v_and_b32 v63,0xffff0000,v60
M 22,a[44:47],46
D 0,56,1024
M 18,a[32:35],42
R 34,2,10240
v_add_f32 v15,v15,v63
M 18,a[40:43],46
L 3
v_lshlrev_b32 v62,16,v61
v_add_f32 v16,v16,v62
M 18,a[48:51],50
R 38,2,11264
v_and_b32 v63,0xffff0000,v61
M 18,a[56:59],54
buffer_load_dwordx4 v[58:61],v0,s[36:39],s74 offen offset:0 sc1
M 22,a[52:55],50
s_add_u32 s74,s74,s62
R 42,2,12288
v_add_f32 v17,v17,v63
M 22,a[60:63],54
L 3
v_cvt_pk_bf16_f32 v10,v10,v11
v_cvt_pk_bf16_f32 v11,v12,v13
M 22,a[68:71],26
R 46,2,13312
v_cvt_pk_bf16_f32 v12,v14,v15
M 22,a[76:79],30
D 0,56,2048
M 18,a[64:67],26
R 50,2,14336
v_cvt_pk_bf16_f32 v13,v16,v17
M 18,a[72:75],30
L 3
buffer_store_dwordx4 v[10:13],v0,s[36:39],s66 offen offset:0 sc0 sc1
M 18,a[80:83],34
R 54,2,15360
M 18,a[88:91],38
M 22,a[84:87],34
R 26,2,16384
M 22,a[92:95],38
L 3
M 22,a[100:103],42
R 30,2,17408
M 22,a[108:111],46
D 0,56,3072
M 18,a[96:99],42
s_add_u32 m0,s57,4096
s_add_u32 s56,s56,4096
R 34,2,18432
M 18,a[104:107],46
L 3
M 18,a[112:115],50
R 38,2,19456
M 18,a[120:123],54
M 22,a[116:119],50
R 42,2,20480
M 22,a[124:127],54
L 3
D 0,56,0
M 22,a[132:135],26
R 46,2,21504
M 22,a[140:143],30
M 18,a[128:131],26
R 50,2,22528
M 18,a[136:139],30
L 3
M 18,a[144:147],34
R 54,2,23552
M 18,a[152:155],38
M 22,a[148:151],34
R 26,2,24576
M 22,a[156:159],38
L 3
D 0,56,1024
M 22,a[164:167],42
R 30,2,25600
M 22,a[172:175],46
M 18,a[160:163],42
R 34,2,26624
M 18,a[168:171],46
s_waitcnt vmcnt(8) lgkmcnt(3)
M 18,a[176:179],50
s_barrier
R 38,2,27648
M 18,a[184:187],54
M 22,a[180:183],50
R 42,2,28672
M 22,a[188:191],54
L 3
D 0,56,2048
M 22,a[196:199],26
s_add_u32 m0,s59,1024
s_add_u32 s56,s55,28672
R 46,2,29696
M 22,a[204:207],30
s_add_u32 s55,s55,s60
M 18,a[192:195],26
R 50,2,30720
M 18,a[200:203],30
L 3
M 18,a[208:211],34
R 54,2,31744
M 18,a[216:219],38
M 22,a[212:215],34
R 26,2,32768
M 22,a[220:223],38
L 3
buffer_load_dword v8,s[36:39],s58 offen offset:0 sc1 lds
M 22,a[228:231],42
s_add_u32 m0,s57,28672
R 30,2,33792
M 22,a[236:239],46
D 0,56,0
M 18,a[224:227],42
R 34,2,34816
M 18,a[232:235],46
L 3
M 18,a[240:243],50
R 38,2,35840
M 18,a[248:251],54
M 22,a[244:247],50
R 42,2,36864
M 22,a[252:255],54
L 3
M 22,v[68:71],26
R 46,2,37888
M 22,v[76:79],30
D 0,56,1024
M 18,v[64:67],26
R 50,2,38912
M 18,v[72:75],30
L 3
M 18,v[80:83],34
R 54,2,39936
M 18,v[88:91],38
M 22,v[84:87],34
R 26,2,40960
M 22,v[92:95],38
L 3
M 22,v[100:103],42
R 30,2,41984
M 22,v[108:111],46
D 0,56,2048
M 18,v[96:99],42
R 34,2,43008
M 18,v[104:107],46
L 3
M 18,v[112:115],50
R 38,2,44032
M 18,v[120:123],54
M 22,v[116:119],50
R 42,2,45056
M 22,v[124:127],54
L 3
D 0,56,3072
M 22,v[132:135],26
s_add_u32 m0,s57,32768
s_add_u32 s56,s56,4096
R 46,2,46080
M 22,v[140:143],30
s_sub_u32 s57,s84,s57
M 18,v[128:131],26
R 50,2,47104
M 18,v[136:139],30
L 3
M 18,v[144:147],34
R 54,2,48128
M 18,v[152:155],38
M 22,v[148:151],34
R 26,2,49152
M 22,v[156:159],38
L 3
D 0,56,0
M 22,v[164:167],42
R 30,2,50176
M 22,v[172:175],46
M 18,v[160:163],42
R 34,2,51200
M 18,v[168:171],46
L 3
M 18,v[176:179],50
R 38,2,52224
M 18,v[184:187],54
M 22,v[180:183],50
R 42,2,53248
M 22,v[188:191],54
L 3
D 0,56,1024
M 22,v[196:199],26
R 46,2,54272
M 22,v[204:207],30
M 18,v[192:195],26
R 50,2,55296
M 18,v[200:203],30
s_waitcnt vmcnt(6) lgkmcnt(3)
M 18,v[208:211],34
s_barrier
R 54,2,56320
ds_read_b32 v7,v6 offset:1024
M 18,v[216:219],38
buffer_store_dword v4,v3,s[36:39],s69 offen offset:0 sc0 sc1
M 22,v[212:215],34
s_mov_b32 s69,s68
s_add_u32 s68,s68,s63
R 26,1,0
M 22,v[220:223],38
L 4
D 0,56,2048
M 22,v[228:231],42
R 30,1,1024
M 22,v[236:239],46
M 18,v[224:227],42
)"
R"(L 2
R 34,1,2048
v_cmp_eq_u32 vcc,s53,v7
M 18,v[232:235],46
s_nop 0
s_cmp_eq_u32 vcc_lo,-1
M 18,v[240:243],50
R 38,1,3072
M 18,v[248:251],54
M 22,v[244:247],50
R 42,1,4096
M 22,v[252:255],54
L 3
s_cbranch_scc1 Lg4_F0w1_%=
s_branch Lg4_L0_%=
Lg4_F0w2_%=:
v_mfma_f32_16x16x32_bf16 v[14:17],a[4:7],v[26:29],0
s_add_u32 m0,s57,0
R 46,1,5120
v_lshlrev_b32 v62,16,v58
M 14,a[12:15],30
s_add_u32 s56,s55,0
v_add_f32 v18,v18,v62
v_and_b32 v63,0xffff0000,v58
v_mfma_f32_16x16x32_bf16 v[10:13],a[0:3],v[26:29],0
s_add_u32 s58,s58,s61
R 50,1,6144
v_add_f32 v19,v19,v63
M 10,a[8:11],30
L 3
D 0,56,0
M 10,a[16:19],34
s_mov_b32 s66,s65
R 54,1,7168
v_lshlrev_b32 v62,16,v59
M 10,a[24:27],38
s_add_u32 s65,s65,s62
v_add_f32 v20,v20,v62
v_and_b32 v63,0xffff0000,v59
M 14,a[20:23],34
s_add_u32 s54,s54,s46
R 26,1,8192
v_add_f32 v21,v21,v63
M 14,a[28:31],38
L 3
v_lshlrev_b32 v62,16,v60
v_add_f32 v22,v22,v62
M 14,a[36:39],42
R 30,1,9216
v_and_b32 v63,0xffff0000,v60
M 14,a[44:47],46
D 0,56,1024
M 10,a[32:35],42
R 34,1,10240
v_add_f32 v23,v23,v63
M 10,a[40:43],46
L 3
v_lshlrev_b32 v62,16,v61
v_add_f32 v24,v24,v62
M 10,a[48:51],50
R 38,1,11264
v_and_b32 v63,0xffff0000,v61
M 10,a[56:59],54
buffer_load_dwordx4 v[58:61],v0,s[36:39],s74 offen offset:0 sc1
M 14,a[52:55],50
s_add_u32 s74,s74,s62
R 42,1,12288
v_add_f32 v25,v25,v63
M 14,a[60:63],54
L 3
v_cvt_pk_bf16_f32 v18,v18,v19
v_cvt_pk_bf16_f32 v19,v20,v21
M 14,a[68:71],26
R 46,1,13312
v_cvt_pk_bf16_f32 v20,v22,v23
M 14,a[76:79],30
D 0,56,2048
M 10,a[64:67],26
R 50,1,14336
v_cvt_pk_bf16_f32 v21,v24,v25
M 10,a[72:75],30
L 3
buffer_store_dwordx4 v[18:21],v0,s[36:39],s67 offen offset:0 sc0 sc1
M 10,a[80:83],34
R 54,1,15360
M 10,a[88:91],38
M 14,a[84:87],34
R 26,1,16384
M 14,a[92:95],38
L 3
M 14,a[100:103],42
R 30,1,17408
M 14,a[108:111],46
D 0,56,3072
M 10,a[96:99],42
s_add_u32 m0,s57,4096
s_add_u32 s56,s56,4096
R 34,1,18432
M 10,a[104:107],46
L 3
M 10,a[112:115],50
R 38,1,19456
M 10,a[120:123],54
M 14,a[116:119],50
R 42,1,20480
M 14,a[124:127],54
L 3
D 0,56,0
M 14,a[132:135],26
R 46,1,21504
M 14,a[140:143],30
M 10,a[128:131],26
R 50,1,22528
M 10,a[136:139],30
L 3
M 10,a[144:147],34
R 54,1,23552
M 10,a[152:155],38
M 14,a[148:151],34
R 26,1,24576
M 14,a[156:159],38
L 3
D 0,56,1024
M 14,a[164:167],42
R 30,1,25600
M 14,a[172:175],46
M 10,a[160:163],42
s_waitcnt vmcnt(8)
R 34,1,26624
M 10,a[168:171],46
s_barrier
L 3
M 10,a[176:179],50
R 38,1,27648
M 10,a[184:187],54
M 14,a[180:183],50
R 42,1,28672
M 14,a[188:191],54
L 3
D 0,56,2048
M 14,a[196:199],26
s_add_u32 m0,s59,0
s_add_u32 s56,s55,28672
R 46,1,29696
M 14,a[204:207],30
s_add_u32 s55,s55,s60
M 10,a[192:195],26
R 50,1,30720
M 10,a[200:203],30
L 3
M 10,a[208:211],34
R 54,1,31744
M 10,a[216:219],38
M 14,a[212:215],34
R 26,1,32768
M 14,a[220:223],38
L 3
buffer_load_dword v8,s[36:39],s58 offen offset:0 sc1 lds
M 14,a[228:231],42
s_add_u32 m0,s57,28672
R 30,1,33792
M 14,a[236:239],46
D 0,56,0
M 10,a[224:227],42
R 34,1,34816
M 10,a[232:235],46
L 3
M 10,a[240:243],50
R 38,1,35840
M 10,a[248:251],54
M 14,a[244:247],50
R 42,1,36864
M 14,a[252:255],54
L 3
M 14,v[68:71],26
R 46,1,37888
M 14,v[76:79],30
D 0,56,1024
M 10,v[64:67],26
R 50,1,38912
M 10,v[72:75],30
L 3
M 10,v[80:83],34
R 54,1,39936
M 10,v[88:91],38
M 14,v[84:87],34
R 26,1,40960
M 14,v[92:95],38
L 3
M 14,v[100:103],42
R 30,1,41984
M 14,v[108:111],46
D 0,56,2048
M 10,v[96:99],42
R 34,1,43008
M 10,v[104:107],46
L 3
M 10,v[112:115],50
R 38,1,44032
M 10,v[120:123],54
M 14,v[116:119],50
R 42,1,45056
M 14,v[124:127],54
L 3
D 0,56,3072
M 14,v[132:135],26
s_add_u32 m0,s57,32768
s_add_u32 s56,s56,4096
R 46,1,46080
M 14,v[140:143],30
s_sub_u32 s57,s84,s57
M 10,v[128:131],26
R 50,1,47104
M 10,v[136:139],30
L 3
M 10,v[144:147],34
R 54,1,48128
M 10,v[152:155],38
M 14,v[148:151],34
R 26,1,49152
M 14,v[156:159],38
L 3
D 0,56,0
M 14,v[164:167],42
R 30,1,50176
M 14,v[172:175],46
M 10,v[160:163],42
R 34,1,51200
M 10,v[168:171],46
L 3
M 10,v[176:179],50
R 38,1,52224
M 10,v[184:187],54
M 14,v[180:183],50
R 42,1,53248
M 14,v[188:191],54
L 3
D 0,56,1024
M 14,v[196:199],26
R 46,1,54272
M 14,v[204:207],30
M 10,v[192:195],26
s_waitcnt vmcnt(6)
R 50,1,55296
M 10,v[200:203],30
L 3
s_barrier
buffer_store_dword v4,v3,s[36:39],s69 offen offset:0 sc0 sc1
M 10,v[208:211],34
s_mov_b32 s69,s68
R 54,1,56320
ds_read_b32 v7,v6 offset:0
M 10,v[216:219],38
s_add_u32 s68,s68,s63
M 14,v[212:215],34
R 26,2,0
M 14,v[220:223],38
L 4
D 0,56,2048
M 14,v[228:231],42
R 30,2,1024
M 14,v[236:239],46
M 10,v[224:227],42
L 2
R 34,2,2048
v_cmp_eq_u32 vcc,s53,v7
M 10,v[232:235],46
s_nop 0
s_cmp_eq_u32 vcc_lo,-1
M 10,v[240:243],50
R 38,2,3072
M 10,v[248:251],54
M 14,v[244:247],50
R 42,2,4096
M 14,v[252:255],54
L 3
s_cbranch_scc0 Lg4_L1_%=
Lg4_F1w2_%=:
v_mfma_f32_16x16x32_bf16 v[22:25],a[4:7],v[26:29],0
s_add_u32 m0,s57,0
R 46,2,5120
v_lshlrev_b32 v62,16,v58
M 22,a[12:15],30
s_add_u32 s56,s55,0
v_add_f32 v10,v10,v62
v_and_b32 v63,0xffff0000,v58
v_mfma_f32_16x16x32_bf16 v[18:21],a[0:3],v[26:29],0
s_add_u32 s58,s58,s61
R 50,2,6144
v_add_f32 v11,v11,v63
M 18,a[8:11],30
L 3
D 0,56,0
M 18,a[16:19],34
s_mov_b32 s67,s65
R 54,2,7168
v_lshlrev_b32 v62,16,v59
M 18,a[24:27],38
s_add_u32 s65,s65,s62
v_add_f32 v12,v12,v62
v_and_b32 v63,0xffff0000,v59
M 22,a[20:23],34
s_add_u32 s54,s54,s46
R 26,2,8192
v_add_f32 v13,v13,v63
M 22,a[28:31],38
L 3
v_lshlrev_b32 v62,16,v60
v_add_f32 v14,v14,v62
M 22,a[36:39],42
R 30,2,9216
v_and_b32 v63,0xffff0000,v60
M 22,a[44:47],46
D 0,56,1024
M 18,a[32:35],42
R 34,2,10240
v_add_f32 v15,v15,v63
M 18,a[40:43],46
L 3
v_lshlrev_b32 v62,16,v61
v_add_f32 v16,v16,v62
M 18,a[48:51],50
R 38,2,11264
v_and_b32 v63,0xffff0000,v61
M 18,a[56:59],54
buffer_load_dwordx4 v[58:61],v0,s[36:39],s74 offen offset:0 sc1
M 22,a[52:55],50
s_add_u32 s74,s74,s62
R 42,2,12288
v_add_f32 v17,v17,v63
M 22,a[60:63],54
L 3
v_cvt_pk_bf16_f32 v10,v10,v11
v_cvt_pk_bf16_f32 v11,v12,v13
M 22,a[68:71],26
R 46,2,13312
v_cvt_pk_bf16_f32 v12,v14,v15
M 22,a[76:79],30
D 0,56,2048
M 18,a[64:67],26
R 50,2,14336
v_cvt_pk_bf16_f32 v13,v16,v17
M 18,a[72:75],30
L 3
buffer_store_dwordx4 v[10:13],v0,s[36:39],s66 offen offset:0 sc0 sc1
M 18,a[80:83],34
R 54,2,15360
M 18,a[88:91],38
M 22,a[84:87],34
R 26,2,16384
M 22,a[92:95],38
L 3
M 22,a[100:103],42
R 30,2,17408
M 22,a[108:111],46
D 0,56,3072
M 18,a[96:99],42
s_add_u32 m0,s57,4096
s_add_u32 s56,s56,4096
R 34,2,18432
M 18,a[104:107],46
L 3
M 18,a[112:115],50
R 38,2,19456
M 18,a[120:123],54
M 22,a[116:119],50
R 42,2,20480
M 22,a[124:127],54
L 3
D 0,56,0
M 22,a[132:135],26
R 46,2,21504
M 22,a[140:143],30
M 18,a[128:131],26
R 50,2,22528
M 18,a[136:139],30
L 3
M 18,a[144:147],34
R 54,2,23552
M 18,a[152:155],38
M 22,a[148:151],34
R 26,2,24576
M 22,a[156:159],38
L 3
D 0,56,1024
M 22,a[164:167],42
R 30,2,25600
M 22,a[172:175],46
M 18,a[160:163],42
s_waitcnt vmcnt(8)
R 34,2,26624
M 18,a[168:171],46
s_barrier
L 3
M 18,a[176:179],50
R 38,2,27648
M 18,a[184:187],54
M 22,a[180:183],50
R 42,2,28672
M 22,a[188:191],54
L 3
D 0,56,2048
M 22,a[196:199],26
s_add_u32 m0,s59,1024
s_add_u32 s56,s55,28672
R 46,2,29696
M 22,a[204:207],30
s_add_u32 s55,s55,s60
M 18,a[192:195],26
R 50,2,30720
M 18,a[200:203],30
L 3
M 18,a[208:211],34
R 54,2,31744
M 18,a[216:219],38
M 22,a[212:215],34
R 26,2,32768
M 22,a[220:223],38
L 3
buffer_load_dword v8,s[36:39],s58 offen offset:0 sc1 lds
M 22,a[228:231],42
s_add_u32 m0,s57,28672
R 30,2,33792
M 22,a[236:239],46
D 0,56,0
M 18,a[224:227],42
R 34,2,34816
M 18,a[232:235],46
L 3
M 18,a[240:243],50
R 38,2,35840
M 18,a[248:251],54
M 22,a[244:247],50
R 42,2,36864
M 22,a[252:255],54
L 3
M 22,v[68:71],26
R 46,2,37888
M 22,v[76:79],30
D 0,56,1024
M 18,v[64:67],26
R 50,2,38912
M 18,v[72:75],30
L 3
M 18,v[80:83],34
R 54,2,39936
M 18,v[88:91],38
M 22,v[84:87],34
R 26,2,40960
M 22,v[92:95],38
L 3
M 22,v[100:103],42
R 30,2,41984
M 22,v[108:111],46
D 0,56,2048
M 18,v[96:99],42
R 34,2,43008
M 18,v[104:107],46
L 3
M 18,v[112:115],50
R 38,2,44032
M 18,v[120:123],54
M 22,v[116:119],50
R 42,2,45056
M 22,v[124:127],54
L 3
D 0,56,3072
M 22,v[132:135],26
s_add_u32 m0,s57,32768
s_add_u32 s56,s56,4096
R 46,2,46080
M 22,v[140:143],30
s_sub_u32 s57,s84,s57
M 18,v[128:131],26
R 50,2,47104
M 18,v[136:139],30
L 3
M 18,v[144:147],34
R 54,2,48128
M 18,v[152:155],38
M 22,v[148:151],34
R 26,2,49152
M 22,v[156:159],38
L 3
D 0,56,0
M 22,v[164:167],42
R 30,2,50176
M 22,v[172:175],46
M 18,v[160:163],42
R 34,2,51200
M 18,v[168:171],46
L 3
M 18,v[176:179],50
R 38,2,52224
M 18,v[184:187],54
M 22,v[180:183],50
R 42,2,53248
M 22,v[188:191],54
L 3
D 0,56,1024
)"
R"(M 22,v[196:199],26
R 46,2,54272
M 22,v[204:207],30
M 18,v[192:195],26
s_waitcnt vmcnt(6)
R 50,2,55296
M 18,v[200:203],30
L 3
s_barrier
buffer_store_dword v4,v3,s[36:39],s69 offen offset:0 sc0 sc1
M 18,v[208:211],34
s_mov_b32 s69,s68
R 54,2,56320
ds_read_b32 v7,v6 offset:1024
M 18,v[216:219],38
s_add_u32 s68,s68,s63
M 22,v[212:215],34
R 26,1,0
M 22,v[220:223],38
L 4
D 0,56,2048
M 22,v[228:231],42
R 30,1,1024
M 22,v[236:239],46
M 18,v[224:227],42
L 2
R 34,1,2048
v_cmp_eq_u32 vcc,s53,v7
M 18,v[232:235],46
s_nop 0
s_cmp_eq_u32 vcc_lo,-1
M 18,v[240:243],50
R 38,1,3072
M 18,v[248:251],54
M 22,v[244:247],50
R 42,1,4096
M 22,v[252:255],54
L 3
s_cbranch_scc1 Lg4_F0w2_%=
s_branch Lg4_L0_%=
Lg4_F0w3_%=:
v_mfma_f32_16x16x32_bf16 v[14:17],a[4:7],v[26:29],0
s_add_u32 m0,s57,0
R 46,1,5120
v_lshlrev_b32 v62,16,v58
M 14,a[12:15],30
s_add_u32 s56,s55,0
v_add_f32 v18,v18,v62
v_and_b32 v63,0xffff0000,v58
v_mfma_f32_16x16x32_bf16 v[10:13],a[0:3],v[26:29],0
s_add_u32 s58,s58,s61
R 50,1,6144
v_add_f32 v19,v19,v63
M 10,a[8:11],30
L 3
D 0,56,0
M 10,a[16:19],34
s_mov_b32 s66,s65
R 54,1,7168
v_lshlrev_b32 v62,16,v59
M 10,a[24:27],38
s_add_u32 s65,s65,s62
v_add_f32 v20,v20,v62
v_and_b32 v63,0xffff0000,v59
M 14,a[20:23],34
s_add_u32 s54,s54,s46
R 26,1,8192
v_add_f32 v21,v21,v63
M 14,a[28:31],38
L 3
v_lshlrev_b32 v62,16,v60
v_add_f32 v22,v22,v62
M 14,a[36:39],42
R 30,1,9216
v_and_b32 v63,0xffff0000,v60
M 14,a[44:47],46
D 0,56,1024
M 10,a[32:35],42
R 34,1,10240
v_add_f32 v23,v23,v63
M 10,a[40:43],46
L 3
v_lshlrev_b32 v62,16,v61
v_add_f32 v24,v24,v62
M 10,a[48:51],50
R 38,1,11264
v_and_b32 v63,0xffff0000,v61
M 10,a[56:59],54
buffer_load_dwordx4 v[58:61],v0,s[36:39],s74 offen offset:0 sc1
M 14,a[52:55],50
s_add_u32 s74,s74,s62
R 42,1,12288
v_add_f32 v25,v25,v63
M 14,a[60:63],54
L 3
v_cvt_pk_bf16_f32 v18,v18,v19
v_cvt_pk_bf16_f32 v19,v20,v21
M 14,a[68:71],26
R 46,1,13312
v_cvt_pk_bf16_f32 v20,v22,v23
M 14,a[76:79],30
D 0,56,2048
M 10,a[64:67],26
R 50,1,14336
v_cvt_pk_bf16_f32 v21,v24,v25
M 10,a[72:75],30
L 3
buffer_store_dwordx4 v[18:21],v0,s[36:39],s67 offen offset:0 sc0 sc1
M 10,a[80:83],34
R 54,1,15360
M 10,a[88:91],38
M 14,a[84:87],34
R 26,1,16384
M 14,a[92:95],38
L 3
M 14,a[100:103],42
R 30,1,17408
M 14,a[108:111],46
D 0,56,3072
M 10,a[96:99],42
s_add_u32 m0,s57,4096
s_add_u32 s56,s56,4096
R 34,1,18432
M 10,a[104:107],46
L 3
M 10,a[112:115],50
R 38,1,19456
M 10,a[120:123],54
M 14,a[116:119],50
R 42,1,20480
M 14,a[124:127],54
L 3
D 0,56,0
M 14,a[132:135],26
R 46,1,21504
M 14,a[140:143],30
M 10,a[128:131],26
R 50,1,22528
M 10,a[136:139],30
L 3
M 10,a[144:147],34
R 54,1,23552
M 10,a[152:155],38
M 14,a[148:151],34
R 26,1,24576
M 14,a[156:159],38
L 3
D 0,56,1024
M 14,a[164:167],42
R 30,1,25600
M 14,a[172:175],46
s_waitcnt vmcnt(8)
M 10,a[160:163],42
s_barrier
R 34,1,26624
M 10,a[168:171],46
L 3
M 10,a[176:179],50
R 38,1,27648
M 10,a[184:187],54
M 14,a[180:183],50
R 42,1,28672
M 14,a[188:191],54
L 3
D 0,56,2048
M 14,a[196:199],26
s_add_u32 m0,s59,0
s_add_u32 s56,s55,28672
R 46,1,29696
M 14,a[204:207],30
s_add_u32 s55,s55,s60
M 10,a[192:195],26
R 50,1,30720
M 10,a[200:203],30
L 3
M 10,a[208:211],34
R 54,1,31744
M 10,a[216:219],38
M 14,a[212:215],34
R 26,1,32768
M 14,a[220:223],38
L 3
buffer_load_dword v8,s[36:39],s58 offen offset:0 sc1 lds
M 14,a[228:231],42
s_add_u32 m0,s57,28672
R 30,1,33792
M 14,a[236:239],46
D 0,56,0
M 10,a[224:227],42
R 34,1,34816
M 10,a[232:235],46
L 3
M 10,a[240:243],50
R 38,1,35840
M 10,a[248:251],54
M 14,a[244:247],50
R 42,1,36864
M 14,a[252:255],54
L 3
M 14,v[68:71],26
R 46,1,37888
M 14,v[76:79],30
D 0,56,1024
M 10,v[64:67],26
R 50,1,38912
M 10,v[72:75],30
L 3
M 10,v[80:83],34
R 54,1,39936
M 10,v[88:91],38
M 14,v[84:87],34
R 26,1,40960
M 14,v[92:95],38
L 3
M 14,v[100:103],42
R 30,1,41984
M 14,v[108:111],46
D 0,56,2048
M 10,v[96:99],42
R 34,1,43008
M 10,v[104:107],46
L 3
M 10,v[112:115],50
R 38,1,44032
M 10,v[120:123],54
M 14,v[116:119],50
R 42,1,45056
M 14,v[124:127],54
L 3
D 0,56,3072
M 14,v[132:135],26
s_add_u32 m0,s57,32768
s_add_u32 s56,s56,4096
R 46,1,46080
M 14,v[140:143],30
s_sub_u32 s57,s84,s57
M 10,v[128:131],26
R 50,1,47104
M 10,v[136:139],30
L 3
M 10,v[144:147],34
R 54,1,48128
M 10,v[152:155],38
M 14,v[148:151],34
R 26,1,49152
M 14,v[156:159],38
L 3
D 0,56,0
M 14,v[164:167],42
R 30,1,50176
M 14,v[172:175],46
M 10,v[160:163],42
R 34,1,51200
M 10,v[168:171],46
L 3
M 10,v[176:179],50
R 38,1,52224
M 10,v[184:187],54
M 14,v[180:183],50
R 42,1,53248
M 14,v[188:191],54
L 3
D 0,56,1024
M 14,v[196:199],26
R 46,1,54272
M 14,v[204:207],30
s_waitcnt vmcnt(6)
M 10,v[192:195],26
s_barrier
R 50,1,55296
ds_read_b32 v7,v6 offset:0
M 10,v[200:203],30
L 4
buffer_store_dword v4,v3,s[36:39],s69 offen offset:0 sc0 sc1
M 10,v[208:211],34
s_mov_b32 s69,s68
s_add_u32 s68,s68,s63
R 54,1,56320
M 10,v[216:219],38
M 14,v[212:215],34
R 26,2,0
M 14,v[220:223],38
L 4
D 0,56,2048
M 14,v[228:231],42
L 2
R 30,2,1024
v_cmp_eq_u32 vcc,s53,v7
M 14,v[236:239],46
s_cmp_eq_u32 vcc_lo,-1
M 10,v[224:227],42
R 34,2,2048
M 10,v[232:235],46
L 3
M 10,v[240:243],50
R 38,2,3072
M 10,v[248:251],54
M 14,v[244:247],50
R 42,2,4096
M 14,v[252:255],54
L 3
s_cbranch_scc0 Lg4_L1_%=
Lg4_F1w3_%=:
v_mfma_f32_16x16x32_bf16 v[22:25],a[4:7],v[26:29],0
s_add_u32 m0,s57,0
R 46,2,5120
v_lshlrev_b32 v62,16,v58
M 22,a[12:15],30
s_add_u32 s56,s55,0
v_add_f32 v10,v10,v62
v_and_b32 v63,0xffff0000,v58
v_mfma_f32_16x16x32_bf16 v[18:21],a[0:3],v[26:29],0
s_add_u32 s58,s58,s61
R 50,2,6144
v_add_f32 v11,v11,v63
M 18,a[8:11],30
L 3
D 0,56,0
M 18,a[16:19],34
s_mov_b32 s67,s65
R 54,2,7168
v_lshlrev_b32 v62,16,v59
M 18,a[24:27],38
s_add_u32 s65,s65,s62
v_add_f32 v12,v12,v62
v_and_b32 v63,0xffff0000,v59
M 22,a[20:23],34
s_add_u32 s54,s54,s46
R 26,2,8192
v_add_f32 v13,v13,v63
M 22,a[28:31],38
L 3
v_lshlrev_b32 v62,16,v60
v_add_f32 v14,v14,v62
M 22,a[36:39],42
R 30,2,9216
v_and_b32 v63,0xffff0000,v60
M 22,a[44:47],46
D 0,56,1024
M 18,a[32:35],42
R 34,2,10240
v_add_f32 v15,v15,v63
M 18,a[40:43],46
L 3
v_lshlrev_b32 v62,16,v61
v_add_f32 v16,v16,v62
M 18,a[48:51],50
R 38,2,11264
v_and_b32 v63,0xffff0000,v61
M 18,a[56:59],54
buffer_load_dwordx4 v[58:61],v0,s[36:39],s74 offen offset:0 sc1
M 22,a[52:55],50
s_add_u32 s74,s74,s62
R 42,2,12288
v_add_f32 v17,v17,v63
M 22,a[60:63],54
L 3
v_cvt_pk_bf16_f32 v10,v10,v11
v_cvt_pk_bf16_f32 v11,v12,v13
M 22,a[68:71],26
R 46,2,13312
v_cvt_pk_bf16_f32 v12,v14,v15
M 22,a[76:79],30
D 0,56,2048
M 18,a[64:67],26
R 50,2,14336
v_cvt_pk_bf16_f32 v13,v16,v17
M 18,a[72:75],30
L 3
buffer_store_dwordx4 v[10:13],v0,s[36:39],s66 offen offset:0 sc0 sc1
M 18,a[80:83],34
R 54,2,15360
M 18,a[88:91],38
M 22,a[84:87],34
R 26,2,16384
M 22,a[92:95],38
L 3
M 22,a[100:103],42
R 30,2,17408
M 22,a[108:111],46
D 0,56,3072
M 18,a[96:99],42
s_add_u32 m0,s57,4096
s_add_u32 s56,s56,4096
R 34,2,18432
M 18,a[104:107],46
L 3
M 18,a[112:115],50
R 38,2,19456
M 18,a[120:123],54
M 22,a[116:119],50
R 42,2,20480
M 22,a[124:127],54
L 3
D 0,56,0
M 22,a[132:135],26
R 46,2,21504
M 22,a[140:143],30
M 18,a[128:131],26
R 50,2,22528
M 18,a[136:139],30
L 3
M 18,a[144:147],34
R 54,2,23552
M 18,a[152:155],38
M 22,a[148:151],34
R 26,2,24576
M 22,a[156:159],38
L 3
D 0,56,1024
M 22,a[164:167],42
R 30,2,25600
M 22,a[172:175],46
s_waitcnt vmcnt(8)
M 18,a[160:163],42
s_barrier
R 34,2,26624
M 18,a[168:171],46
L 3
M 18,a[176:179],50
R 38,2,27648
M 18,a[184:187],54
M 22,a[180:183],50
R 42,2,28672
M 22,a[188:191],54
L 3
D 0,56,2048
M 22,a[196:199],26
s_add_u32 m0,s59,1024
s_add_u32 s56,s55,28672
R 46,2,29696
M 22,a[204:207],30
s_add_u32 s55,s55,s60
M 18,a[192:195],26
R 50,2,30720
M 18,a[200:203],30
L 3
M 18,a[208:211],34
R 54,2,31744
M 18,a[216:219],38
M 22,a[212:215],34
R 26,2,32768
M 22,a[220:223],38
L 3
buffer_load_dword v8,s[36:39],s58 offen offset:0 sc1 lds
M 22,a[228:231],42
s_add_u32 m0,s57,28672
R 30,2,33792
M 22,a[236:239],46
D 0,56,0
M 18,a[224:227],42
R 34,2,34816
M 18,a[232:235],46
L 3
M 18,a[240:243],50
R 38,2,35840
M 18,a[248:251],54
M 22,a[244:247],50
R 42,2,36864
M 22,a[252:255],54
L 3
M 22,v[68:71],26
R 46,2,37888
M 22,v[76:79],30
D 0,56,1024
M 18,v[64:67],26
R 50,2,38912
M 18,v[72:75],30
L 3
M 18,v[80:83],34
R 54,2,39936
M 18,v[88:91],38
M 22,v[84:87],34
R 26,2,40960
M 22,v[92:95],38
L 3
M 22,v[100:103],42
R 30,2,41984
M 22,v[108:111],46
D 0,56,2048
M 18,v[96:99],42
R 34,2,43008
M 18,v[104:107],46
L 3
M 18,v[112:115],50
R 38,2,44032
M 18,v[120:123],54
M 22,v[116:119],50
R 42,2,45056
M 22,v[124:127],54
L 3
D 0,56,3072
M 22,v[132:135],26
s_add_u32 m0,s57,32768
s_add_u32 s56,s56,4096
R 46,2,46080
M 22,v[140:143],30
s_sub_u32 s57,s84,s57
M 18,v[128:131],26
R 50,2,47104
)"
R"(M 18,v[136:139],30
L 3
M 18,v[144:147],34
R 54,2,48128
M 18,v[152:155],38
M 22,v[148:151],34
R 26,2,49152
M 22,v[156:159],38
L 3
D 0,56,0
M 22,v[164:167],42
R 30,2,50176
M 22,v[172:175],46
M 18,v[160:163],42
R 34,2,51200
M 18,v[168:171],46
L 3
M 18,v[176:179],50
R 38,2,52224
M 18,v[184:187],54
M 22,v[180:183],50
R 42,2,53248
M 22,v[188:191],54
L 3
D 0,56,1024
M 22,v[196:199],26
R 46,2,54272
M 22,v[204:207],30
s_waitcnt vmcnt(6)
M 18,v[192:195],26
s_barrier
R 50,2,55296
ds_read_b32 v7,v6 offset:1024
M 18,v[200:203],30
L 4
buffer_store_dword v4,v3,s[36:39],s69 offen offset:0 sc0 sc1
M 18,v[208:211],34
s_mov_b32 s69,s68
s_add_u32 s68,s68,s63
R 54,2,56320
M 18,v[216:219],38
M 22,v[212:215],34
R 26,1,0
M 22,v[220:223],38
L 4
D 0,56,2048
M 22,v[228:231],42
L 2
R 30,1,1024
v_cmp_eq_u32 vcc,s53,v7
M 22,v[236:239],46
s_cmp_eq_u32 vcc_lo,-1
M 18,v[224:227],42
R 34,1,2048
M 18,v[232:235],46
L 3
M 18,v[240:243],50
R 38,1,3072
M 18,v[248:251],54
M 22,v[244:247],50
R 42,1,4096
M 22,v[252:255],54
L 3
s_cbranch_scc1 Lg4_F0w3_%=
Lg4_L0_%=:
s_cmp_ge_u32 s54,256
s_cbranch_scc1 Lg4_Z0_%=
v_mfma_f32_16x16x32_bf16 v[14:17],a[4:7],v[26:29],0
s_add_u32 m0,s59,3072
R 46,1,5120
v_lshlrev_b32 v62,16,v58
M 14,a[12:15],30
s_mov_b32 s66,s65
buffer_load_dword v8,s[36:39],s58 offen offset:0 sc1 lds
v_mfma_f32_16x16x32_bf16 v[10:13],a[0:3],v[26:29],0
s_add_u32 s65,s65,s62
R 50,1,6144
v_add_f32 v18,v18,v62
M 10,a[8:11],30
L 3
v_and_b32 v63,0xffff0000,v58
v_add_f32 v19,v19,v63
M 10,a[16:19],34
R 54,1,7168
v_lshlrev_b32 v62,16,v59
M 10,a[24:27],38
v_add_f32 v20,v20,v62
v_and_b32 v63,0xffff0000,v59
M 14,a[20:23],34
R 26,1,8192
v_add_f32 v21,v21,v63
M 14,a[28:31],38
L 3
v_lshlrev_b32 v62,16,v60
v_add_f32 v22,v22,v62
M 14,a[36:39],42
R 30,1,9216
v_and_b32 v63,0xffff0000,v60
M 14,a[44:47],46
v_add_f32 v23,v23,v63
v_lshlrev_b32 v62,16,v61
M 10,a[32:35],42
R 34,1,10240
v_add_f32 v24,v24,v62
M 10,a[40:43],46
L 3
v_and_b32 v63,0xffff0000,v61
v_add_f32 v25,v25,v63
M 10,a[48:51],50
R 38,1,11264
v_cvt_pk_bf16_f32 v18,v18,v19
M 10,a[56:59],54
buffer_load_dwordx4 v[58:61],v0,s[36:39],s74 offen offset:0 sc1
M 14,a[52:55],50
s_add_u32 s74,s74,s62
R 42,1,12288
v_cvt_pk_bf16_f32 v19,v20,v21
M 14,a[60:63],54
L 3
v_cvt_pk_bf16_f32 v20,v22,v23
v_cvt_pk_bf16_f32 v21,v24,v25
M 14,a[68:71],26
R 46,1,13312
M 14,a[76:79],30
buffer_store_dwordx4 v[18:21],v0,s[36:39],s67 offen offset:0 sc0 sc1
M 10,a[64:67],26
R 50,1,14336
M 10,a[72:75],30
L 3
M 10,a[80:83],34
R 54,1,15360
M 10,a[88:91],38
M 14,a[84:87],34
R 26,1,16384
M 14,a[92:95],38
L 3
M 14,a[100:103],42
R 30,1,17408
M 14,a[108:111],46
M 10,a[96:99],42
R 34,1,18432
M 10,a[104:107],46
L 3
M 10,a[112:115],50
R 38,1,19456
M 10,a[120:123],54
M 14,a[116:119],50
R 42,1,20480
M 14,a[124:127],54
L 3
M 14,a[132:135],26
R 46,1,21504
M 14,a[140:143],30
M 10,a[128:131],26
R 50,1,22528
M 10,a[136:139],30
L 3
M 10,a[144:147],34
R 54,1,23552
M 10,a[152:155],38
M 14,a[148:151],34
R 26,1,24576
M 14,a[156:159],38
L 3
M 14,a[164:167],42
R 30,1,25600
M 14,a[172:175],46
M 10,a[160:163],42
R 34,1,26624
M 10,a[168:171],46
L 3
M 10,a[176:179],50
s_waitcnt vmcnt(2)
R 38,1,27648
M 10,a[184:187],54
s_barrier
ds_read_b32 v7,v6 offset:3072
M 14,a[180:183],50
R 42,1,28672
M 14,a[188:191],54
L 4
M 14,a[196:199],26
R 46,1,29696
M 14,a[204:207],30
M 10,a[192:195],26
R 50,1,30720
M 10,a[200:203],30
L 4
L 3
v_cmp_eq_u32 vcc,s53,v7
M 10,a[208:211],34
R 54,1,31744
M 10,a[216:219],38
s_cmp_eq_u32 vcc_lo,-1
s_cbranch_scc0 Lg4_X0_%=
M 14,a[212:215],34
s_add_u32 m0,s57,0
s_add_u32 s56,s55,0
s_add_u32 s58,s58,s61
R 26,1,32768
M 14,a[220:223],38
L 3
s_add_u32 s54,s54,s46
M 14,a[228:231],42
R 30,1,33792
M 14,a[236:239],46
D 0,56,0
M 10,a[224:227],42
R 34,1,34816
M 10,a[232:235],46
L 3
M 10,a[240:243],50
R 38,1,35840
M 10,a[248:251],54
D 0,56,1024
M 14,a[244:247],50
R 42,1,36864
M 14,a[252:255],54
L 3
M 14,v[68:71],26
R 46,1,37888
M 14,v[76:79],30
D 0,56,2048
M 10,v[64:67],26
R 50,1,38912
M 10,v[72:75],30
L 3
M 10,v[80:83],34
R 54,1,39936
M 10,v[88:91],38
D 0,56,3072
M 14,v[84:87],34
s_add_u32 m0,s57,4096
s_add_u32 s56,s56,4096
R 26,1,40960
M 14,v[92:95],38
L 3
M 14,v[100:103],42
R 30,1,41984
M 14,v[108:111],46
D 0,56,0
M 10,v[96:99],42
R 34,1,43008
M 10,v[104:107],46
L 3
M 10,v[112:115],50
R 38,1,44032
M 10,v[120:123],54
D 0,56,1024
M 14,v[116:119],50
R 42,1,45056
M 14,v[124:127],54
L 3
M 14,v[132:135],26
R 46,1,46080
M 14,v[140:143],30
D 0,56,2048
M 10,v[128:131],26
s_add_u32 m0,s59,0
s_add_u32 s56,s55,28672
R 50,1,47104
M 10,v[136:139],30
L 3
buffer_load_dword v8,s[36:39],s58 offen offset:0 sc1 lds
M 10,v[144:147],34
s_add_u32 m0,s57,28672
s_add_u32 s55,s55,s60
R 54,1,48128
M 10,v[152:155],38
M 14,v[148:151],34
R 26,1,49152
M 14,v[156:159],38
L 3
D 0,56,0
M 14,v[164:167],42
R 30,1,50176
M 14,v[172:175],46
D 0,56,1024
M 10,v[160:163],42
R 34,1,51200
M 10,v[168:171],46
L 3
M 10,v[176:179],50
R 38,1,52224
M 10,v[184:187],54
D 0,56,2048
M 14,v[180:183],50
R 42,1,53248
M 14,v[188:191],54
L 3
M 14,v[196:199],26
R 46,1,54272
M 14,v[204:207],30
D 0,56,3072
M 10,v[192:195],26
s_add_u32 m0,s57,32768
s_add_u32 s56,s56,4096
R 50,1,55296
M 10,v[200:203],30
L 3
s_sub_u32 s57,s84,s57
M 10,v[208:211],34
s_waitcnt vmcnt(4)
R 54,1,56320
M 10,v[216:219],38
s_barrier
ds_read_b32 v7,v6 offset:0
M 14,v[212:215],34
R 26,2,0
M 14,v[220:223],38
L 4
buffer_store_dword v4,v3,s[36:39],s69 offen offset:0 sc0 sc1
M 14,v[228:231],42
s_mov_b32 s69,s68
s_add_u32 s68,s68,s63
R 30,2,1024
M 14,v[236:239],46
D 0,56,0
M 10,v[224:227],42
R 34,2,2048
M 10,v[232:235],46
L 4
D 0,56,1024
M 10,v[240:243],50
L 3
R 38,2,3072
v_cmp_eq_u32 vcc,s53,v7
M 10,v[248:251],54
s_cmp_eq_u32 vcc_lo,-1
D 0,56,2048
M 14,v[244:247],50
R 42,2,4096
M 14,v[252:255],54
L 3
s_cbranch_scc1 Lg4_F1_%=
s_branch Lg4_L1_%=
Lg4_X0_%=:
M 14,a[212:215],34
s_add_u32 m0,s59,0
R 26,1,32768
M 14,a[220:223],38
L 3
buffer_load_dword v8,s[36:39],s58 offen offset:0 sc1 lds
M 14,a[228:231],42
R 30,1,33792
M 14,a[236:239],46
M 10,a[224:227],42
R 34,1,34816
M 10,a[232:235],46
L 3
M 10,a[240:243],50
R 38,1,35840
M 10,a[248:251],54
M 14,a[244:247],50
R 42,1,36864
M 14,a[252:255],54
L 3
M 14,v[68:71],26
R 46,1,37888
M 14,v[76:79],30
M 10,v[64:67],26
R 50,1,38912
M 10,v[72:75],30
L 3
M 10,v[80:83],34
R 54,1,39936
M 10,v[88:91],38
M 14,v[84:87],34
R 26,1,40960
M 14,v[92:95],38
L 3
M 14,v[100:103],42
R 30,1,41984
M 14,v[108:111],46
M 10,v[96:99],42
R 34,1,43008
M 10,v[104:107],46
L 3
M 10,v[112:115],50
R 38,1,44032
M 10,v[120:123],54
M 14,v[116:119],50
R 42,1,45056
M 14,v[124:127],54
L 3
M 14,v[132:135],26
R 46,1,46080
M 14,v[140:143],30
M 10,v[128:131],26
R 50,1,47104
M 10,v[136:139],30
L 3
M 10,v[144:147],34
R 54,1,48128
M 10,v[152:155],38
M 14,v[148:151],34
R 26,1,49152
M 14,v[156:159],38
L 3
M 14,v[164:167],42
R 30,1,50176
M 14,v[172:175],46
M 10,v[160:163],42
R 34,1,51200
M 10,v[168:171],46
L 3
M 10,v[176:179],50
R 38,1,52224
M 10,v[184:187],54
M 14,v[180:183],50
R 42,1,53248
M 14,v[188:191],54
L 3
M 14,v[196:199],26
R 46,1,54272
M 14,v[204:207],30
M 10,v[192:195],26
R 50,1,55296
M 10,v[200:203],30
L 3
M 10,v[208:211],34
s_waitcnt vmcnt(0)
R 54,1,56320
M 10,v[216:219],38
s_barrier
buffer_store_dword v4,v3,s[36:39],s69 offen offset:0 sc0 sc1
M 14,v[212:215],34
s_mov_b32 s69,s68
s_add_u32 s68,s68,s63
ds_read_b32 v7,v6 offset:0
M 14,v[220:223],38
L 3
M 14,v[228:231],42
M 14,v[236:239],46
M 10,v[224:227],42
M 10,v[232:235],46
L 1
M 10,v[240:243],50
L 0
v_cmp_eq_u32 vcc,s53,v7
M 10,v[248:251],54
M 14,v[244:247],50
M 14,v[252:255],54
s_cmp_lt_u32 s54,251
s_cbranch_scc1 Lg4_xa0_%=
s_sub_u32 s75,s66,s85
v_lshlrev_b32 v62,16,v58
s_nop 1
v_add_f32 v10,v10,v62
v_and_b32 v63,0xffff0000,v58
v_add_f32 v11,v11,v63
v_lshlrev_b32 v62,16,v59
v_add_f32 v12,v12,v62
v_and_b32 v63,0xffff0000,v59
v_add_f32 v13,v13,v63
v_lshlrev_b32 v62,16,v60
v_add_f32 v14,v14,v62
v_and_b32 v63,0xffff0000,v60
v_add_f32 v15,v15,v63
v_lshlrev_b32 v62,16,v61
v_add_f32 v16,v16,v62
v_and_b32 v63,0xffff0000,v61
v_add_f32 v17,v17,v63
v_cvt_pk_bf16_f32 v10,v10,v11
v_cvt_pk_bf16_f32 v11,v12,v13
v_cvt_pk_bf16_f32 v12,v14,v15
v_cvt_pk_bf16_f32 v13,v16,v17
v_lshrrev_b32 v62,8,v0
v_lshl_add_u32 v62,v62,5,v0
v_add_u32 v62,s73,v62
ds_write_b128 v62,v[10:13] offset:0
ds_read2_b32 v[14:15],v5 offset0:0 offset1:16
ds_read2_b32 v[16:17],v5 offset0:32 offset1:48
s_mov_b64 s[40:41],s[86:87]
s_mov_b32 s42,4194304
s_mov_b32 s43,0x00020000
L 0
s_add_u32 s64,s75,0
buffer_atomic_pk_add_bf16 v14,v9,s[40:43],s64 offen sc1
s_add_u32 s64,s75,8192
buffer_atomic_pk_add_bf16 v15,v9,s[40:43],s64 offen sc1
s_add_u32 s64,s75,16384
buffer_atomic_pk_add_bf16 v16,v9,s[40:43],s64 offen sc1
s_add_u32 s64,s75,24576
buffer_atomic_pk_add_bf16 v17,v9,s[40:43],s64 offen sc1
s_mov_b32 s66,0x7fff0000
s_mov_b32 s69,0x7fff0000
Lg4_xa0_%=:
s_mov_b32 s80,1
s_cmp_eq_u32 vcc_lo,-1
s_cbranch_scc0 Lg4_xn0_%=
s_add_u32 m0,s57,0
s_add_u32 s56,s55,0
D 0,56,0
v_lshlrev_b32 v62,16,v58
D 0,56,1024
v_add_f32 v10,v10,v62
D 0,56,2048
v_and_b32 v63,0xffff0000,v58
D 0,56,3072
)"
R"(v_add_f32 v11,v11,v63
s_add_u32 m0,s57,4096
s_add_u32 s56,s56,4096
D 0,56,0
v_lshlrev_b32 v62,16,v59
D 0,56,1024
v_add_f32 v12,v12,v62
D 0,56,2048
v_and_b32 v63,0xffff0000,v59
s_add_u32 m0,s57,28672
s_add_u32 s56,s55,28672
D 0,56,0
v_add_f32 v13,v13,v63
D 0,56,1024
v_lshlrev_b32 v62,16,v60
D 0,56,2048
v_add_f32 v14,v14,v62
D 0,56,3072
v_and_b32 v63,0xffff0000,v60
s_add_u32 m0,s57,32768
s_add_u32 s56,s56,4096
D 0,56,0
v_add_f32 v15,v15,v63
D 0,56,1024
v_lshlrev_b32 v62,16,v61
D 0,56,2048
v_add_f32 v16,v16,v62
s_add_u32 s55,s55,s60
s_sub_u32 s57,s84,s57
s_add_u32 s54,s54,s46
v_and_b32 v63,0xffff0000,v61
v_add_f32 v17,v17,v63
v_cvt_pk_bf16_f32 v10,v10,v11
v_cvt_pk_bf16_f32 v11,v12,v13
v_cvt_pk_bf16_f32 v12,v14,v15
v_cvt_pk_bf16_f32 v13,v16,v17
buffer_store_dwordx4 v[10:13],v0,s[36:39],s66 offen offset:0 sc0 sc1
s_mov_b32 s72,s69
s_mov_b32 s69,0x7fff0000
s_mov_b32 s66,0x7fff0000
s_mov_b32 s76,0x7fff0000
s_branch Lg4_gox_%=
Lg4_xn0_%=:
s_cmp_ge_u32 s54,256
s_cbranch_scc1 Lg4_fin0_%=
s_add_u32 m0,s59,4096
s_nop 0
buffer_load_dword v8,s[36:39],s58 offen offset:0 sc1 lds
buffer_store_dword v4,v3,s[36:39],s71 offen offset:0 sc0 sc1
s_add_u32 m0,s59,5120
s_nop 0
buffer_load_dword v8,s[36:39],s58 offen offset:0 sc1 lds
buffer_store_dword v4,v3,s[36:39],s71 offen offset:0 sc0 sc1
s_mov_b32 s83,0
v_lshlrev_b32 v62,16,v58
v_add_f32 v10,v10,v62
v_and_b32 v63,0xffff0000,v58
v_add_f32 v11,v11,v63
v_lshlrev_b32 v62,16,v59
v_add_f32 v12,v12,v62
v_and_b32 v63,0xffff0000,v59
v_add_f32 v13,v13,v63
v_lshlrev_b32 v62,16,v60
v_add_f32 v14,v14,v62
v_and_b32 v63,0xffff0000,v60
v_add_f32 v15,v15,v63
v_lshlrev_b32 v62,16,v61
v_add_f32 v16,v16,v62
v_and_b32 v63,0xffff0000,v61
v_add_f32 v17,v17,v63
v_cvt_pk_bf16_f32 v10,v10,v11
v_cvt_pk_bf16_f32 v11,v12,v13
v_cvt_pk_bf16_f32 v12,v14,v15
v_cvt_pk_bf16_f32 v13,v16,v17
buffer_store_dwordx4 v[10:13],v0,s[36:39],s66 offen offset:0 sc0 sc1
s_mov_b32 s72,s69
s_mov_b32 s69,0x7fff0000
s_mov_b32 s66,0x7fff0000
s_mov_b32 s76,0x7fff0000
s_branch Lg4_poll_%=
Lg4_fin0_%=:
v_lshlrev_b32 v62,16,v58
v_add_f32 v10,v10,v62
v_and_b32 v63,0xffff0000,v58
v_add_f32 v11,v11,v63
v_lshlrev_b32 v62,16,v59
v_add_f32 v12,v12,v62
v_and_b32 v63,0xffff0000,v59
v_add_f32 v13,v13,v63
v_lshlrev_b32 v62,16,v60
v_add_f32 v14,v14,v62
v_and_b32 v63,0xffff0000,v60
v_add_f32 v15,v15,v63
v_lshlrev_b32 v62,16,v61
v_add_f32 v16,v16,v62
v_and_b32 v63,0xffff0000,v61
v_add_f32 v17,v17,v63
v_cvt_pk_bf16_f32 v10,v10,v11
v_cvt_pk_bf16_f32 v11,v12,v13
v_cvt_pk_bf16_f32 v12,v14,v15
v_cvt_pk_bf16_f32 v13,v16,v17
buffer_store_dwordx4 v[10:13],v0,s[36:39],s66 offen offset:0 sc0 sc1
s_mov_b32 s72,s69
s_mov_b32 s69,0x7fff0000
s_mov_b32 s66,0x7fff0000
s_mov_b32 s76,0x7fff0000
s_branch Lg4_done_%=
Lg4_L1_%=:
s_cmp_ge_u32 s54,256
s_cbranch_scc1 Lg4_Z1_%=
v_mfma_f32_16x16x32_bf16 v[22:25],a[4:7],v[26:29],0
s_add_u32 m0,s59,3072
R 46,2,5120
v_lshlrev_b32 v62,16,v58
M 22,a[12:15],30
s_mov_b32 s67,s65
buffer_load_dword v8,s[36:39],s58 offen offset:0 sc1 lds
v_mfma_f32_16x16x32_bf16 v[18:21],a[0:3],v[26:29],0
s_add_u32 s65,s65,s62
R 50,2,6144
v_add_f32 v10,v10,v62
M 18,a[8:11],30
L 3
v_and_b32 v63,0xffff0000,v58
v_add_f32 v11,v11,v63
M 18,a[16:19],34
R 54,2,7168
v_lshlrev_b32 v62,16,v59
M 18,a[24:27],38
v_add_f32 v12,v12,v62
v_and_b32 v63,0xffff0000,v59
M 22,a[20:23],34
R 26,2,8192
v_add_f32 v13,v13,v63
M 22,a[28:31],38
L 3
v_lshlrev_b32 v62,16,v60
v_add_f32 v14,v14,v62
M 22,a[36:39],42
R 30,2,9216
v_and_b32 v63,0xffff0000,v60
M 22,a[44:47],46
v_add_f32 v15,v15,v63
v_lshlrev_b32 v62,16,v61
M 18,a[32:35],42
R 34,2,10240
v_add_f32 v16,v16,v62
M 18,a[40:43],46
L 3
v_and_b32 v63,0xffff0000,v61
v_add_f32 v17,v17,v63
M 18,a[48:51],50
R 38,2,11264
v_cvt_pk_bf16_f32 v10,v10,v11
M 18,a[56:59],54
buffer_load_dwordx4 v[58:61],v0,s[36:39],s74 offen offset:0 sc1
M 22,a[52:55],50
s_add_u32 s74,s74,s62
R 42,2,12288
v_cvt_pk_bf16_f32 v11,v12,v13
M 22,a[60:63],54
L 3
v_cvt_pk_bf16_f32 v12,v14,v15
v_cvt_pk_bf16_f32 v13,v16,v17
M 22,a[68:71],26
R 46,2,13312
M 22,a[76:79],30
buffer_store_dwordx4 v[10:13],v0,s[36:39],s66 offen offset:0 sc0 sc1
M 18,a[64:67],26
R 50,2,14336
M 18,a[72:75],30
L 3
M 18,a[80:83],34
R 54,2,15360
M 18,a[88:91],38
M 22,a[84:87],34
R 26,2,16384
M 22,a[92:95],38
L 3
M 22,a[100:103],42
R 30,2,17408
M 22,a[108:111],46
M 18,a[96:99],42
R 34,2,18432
M 18,a[104:107],46
L 3
M 18,a[112:115],50
R 38,2,19456
M 18,a[120:123],54
M 22,a[116:119],50
R 42,2,20480
M 22,a[124:127],54
L 3
M 22,a[132:135],26
R 46,2,21504
M 22,a[140:143],30
M 18,a[128:131],26
R 50,2,22528
M 18,a[136:139],30
L 3
M 18,a[144:147],34
R 54,2,23552
M 18,a[152:155],38
M 22,a[148:151],34
R 26,2,24576
M 22,a[156:159],38
L 3
M 22,a[164:167],42
R 30,2,25600
M 22,a[172:175],46
M 18,a[160:163],42
R 34,2,26624
M 18,a[168:171],46
L 3
M 18,a[176:179],50
s_waitcnt vmcnt(2)
R 38,2,27648
M 18,a[184:187],54
s_barrier
ds_read_b32 v7,v6 offset:3072
M 22,a[180:183],50
R 42,2,28672
M 22,a[188:191],54
L 4
M 22,a[196:199],26
R 46,2,29696
M 22,a[204:207],30
M 18,a[192:195],26
R 50,2,30720
M 18,a[200:203],30
L 4
L 3
v_cmp_eq_u32 vcc,s53,v7
M 18,a[208:211],34
R 54,2,31744
M 18,a[216:219],38
s_cmp_eq_u32 vcc_lo,-1
s_cbranch_scc0 Lg4_X1_%=
M 22,a[212:215],34
s_add_u32 m0,s57,0
s_add_u32 s56,s55,0
s_add_u32 s58,s58,s61
R 26,2,32768
M 22,a[220:223],38
L 3
s_add_u32 s54,s54,s46
M 22,a[228:231],42
R 30,2,33792
M 22,a[236:239],46
D 0,56,0
M 18,a[224:227],42
R 34,2,34816
M 18,a[232:235],46
L 3
M 18,a[240:243],50
R 38,2,35840
M 18,a[248:251],54
D 0,56,1024
M 22,a[244:247],50
R 42,2,36864
M 22,a[252:255],54
L 3
M 22,v[68:71],26
R 46,2,37888
M 22,v[76:79],30
D 0,56,2048
M 18,v[64:67],26
R 50,2,38912
M 18,v[72:75],30
L 3
M 18,v[80:83],34
R 54,2,39936
M 18,v[88:91],38
D 0,56,3072
M 22,v[84:87],34
s_add_u32 m0,s57,4096
s_add_u32 s56,s56,4096
R 26,2,40960
M 22,v[92:95],38
L 3
M 22,v[100:103],42
R 30,2,41984
M 22,v[108:111],46
D 0,56,0
M 18,v[96:99],42
R 34,2,43008
M 18,v[104:107],46
L 3
M 18,v[112:115],50
R 38,2,44032
M 18,v[120:123],54
D 0,56,1024
M 22,v[116:119],50
R 42,2,45056
M 22,v[124:127],54
L 3
M 22,v[132:135],26
R 46,2,46080
M 22,v[140:143],30
D 0,56,2048
M 18,v[128:131],26
s_add_u32 m0,s59,1024
s_add_u32 s56,s55,28672
R 50,2,47104
M 18,v[136:139],30
L 3
buffer_load_dword v8,s[36:39],s58 offen offset:0 sc1 lds
M 18,v[144:147],34
s_add_u32 m0,s57,28672
s_add_u32 s55,s55,s60
R 54,2,48128
M 18,v[152:155],38
M 22,v[148:151],34
R 26,2,49152
M 22,v[156:159],38
L 3
D 0,56,0
M 22,v[164:167],42
R 30,2,50176
M 22,v[172:175],46
D 0,56,1024
M 18,v[160:163],42
R 34,2,51200
M 18,v[168:171],46
L 3
M 18,v[176:179],50
R 38,2,52224
M 18,v[184:187],54
D 0,56,2048
M 22,v[180:183],50
R 42,2,53248
M 22,v[188:191],54
L 3
M 22,v[196:199],26
R 46,2,54272
M 22,v[204:207],30
D 0,56,3072
M 18,v[192:195],26
s_add_u32 m0,s57,32768
s_add_u32 s56,s56,4096
R 50,2,55296
M 18,v[200:203],30
L 3
s_sub_u32 s57,s84,s57
M 18,v[208:211],34
s_waitcnt vmcnt(4)
R 54,2,56320
M 18,v[216:219],38
s_barrier
ds_read_b32 v7,v6 offset:1024
M 22,v[212:215],34
R 26,1,0
M 22,v[220:223],38
L 4
buffer_store_dword v4,v3,s[36:39],s69 offen offset:0 sc0 sc1
M 22,v[228:231],42
s_mov_b32 s69,s68
s_add_u32 s68,s68,s63
R 30,1,1024
M 22,v[236:239],46
D 0,56,0
M 18,v[224:227],42
R 34,1,2048
M 18,v[232:235],46
L 4
D 0,56,1024
M 18,v[240:243],50
L 3
R 38,1,3072
v_cmp_eq_u32 vcc,s53,v7
M 18,v[248:251],54
s_cmp_eq_u32 vcc_lo,-1
D 0,56,2048
M 22,v[244:247],50
R 42,1,4096
M 22,v[252:255],54
L 3
s_cbranch_scc1 Lg4_F0_%=
s_branch Lg4_L0_%=
Lg4_X1_%=:
M 22,a[212:215],34
s_add_u32 m0,s59,1024
R 26,2,32768
M 22,a[220:223],38
L 3
buffer_load_dword v8,s[36:39],s58 offen offset:0 sc1 lds
M 22,a[228:231],42
R 30,2,33792
M 22,a[236:239],46
M 18,a[224:227],42
R 34,2,34816
M 18,a[232:235],46
L 3
M 18,a[240:243],50
R 38,2,35840
M 18,a[248:251],54
M 22,a[244:247],50
R 42,2,36864
M 22,a[252:255],54
L 3
M 22,v[68:71],26
R 46,2,37888
M 22,v[76:79],30
M 18,v[64:67],26
R 50,2,38912
M 18,v[72:75],30
L 3
M 18,v[80:83],34
R 54,2,39936
M 18,v[88:91],38
M 22,v[84:87],34
R 26,2,40960
M 22,v[92:95],38
L 3
M 22,v[100:103],42
R 30,2,41984
M 22,v[108:111],46
M 18,v[96:99],42
R 34,2,43008
M 18,v[104:107],46
L 3
M 18,v[112:115],50
R 38,2,44032
M 18,v[120:123],54
M 22,v[116:119],50
R 42,2,45056
M 22,v[124:127],54
L 3
M 22,v[132:135],26
R 46,2,46080
M 22,v[140:143],30
M 18,v[128:131],26
R 50,2,47104
M 18,v[136:139],30
L 3
M 18,v[144:147],34
R 54,2,48128
M 18,v[152:155],38
M 22,v[148:151],34
R 26,2,49152
M 22,v[156:159],38
L 3
M 22,v[164:167],42
R 30,2,50176
M 22,v[172:175],46
M 18,v[160:163],42
R 34,2,51200
M 18,v[168:171],46
L 3
M 18,v[176:179],50
R 38,2,52224
M 18,v[184:187],54
M 22,v[180:183],50
R 42,2,53248
M 22,v[188:191],54
L 3
M 22,v[196:199],26
R 46,2,54272
M 22,v[204:207],30
M 18,v[192:195],26
R 50,2,55296
M 18,v[200:203],30
L 3
M 18,v[208:211],34
s_waitcnt vmcnt(0)
R 54,2,56320
M 18,v[216:219],38
s_barrier
buffer_store_dword v4,v3,s[36:39],s69 offen offset:0 sc0 sc1
M 22,v[212:215],34
s_mov_b32 s69,s68
s_add_u32 s68,s68,s63
ds_read_b32 v7,v6 offset:1024
M 22,v[220:223],38
L 3
M 22,v[228:231],42
M 22,v[236:239],46
M 18,v[224:227],42
M 18,v[232:235],46
L 1
M 18,v[240:243],50
L 0
v_cmp_eq_u32 vcc,s53,v7
M 18,v[248:251],54
M 22,v[244:247],50
M 22,v[252:255],54
s_cmp_lt_u32 s54,251
s_cbranch_scc1 Lg4_xa1_%=
s_sub_u32 s75,s67,s85
v_lshlrev_b32 v62,16,v58
s_nop 1
v_add_f32 v18,v18,v62
v_and_b32 v63,0xffff0000,v58
v_add_f32 v19,v19,v63
v_lshlrev_b32 v62,16,v59
v_add_f32 v20,v20,v62
v_and_b32 v63,0xffff0000,v59
v_add_f32 v21,v21,v63
v_lshlrev_b32 v62,16,v60
v_add_f32 v22,v22,v62
)"
R"(v_and_b32 v63,0xffff0000,v60
v_add_f32 v23,v23,v63
v_lshlrev_b32 v62,16,v61
v_add_f32 v24,v24,v62
v_and_b32 v63,0xffff0000,v61
v_add_f32 v25,v25,v63
v_cvt_pk_bf16_f32 v18,v18,v19
v_cvt_pk_bf16_f32 v19,v20,v21
v_cvt_pk_bf16_f32 v20,v22,v23
v_cvt_pk_bf16_f32 v21,v24,v25
v_lshrrev_b32 v62,8,v0
v_lshl_add_u32 v62,v62,5,v0
v_add_u32 v62,s73,v62
ds_write_b128 v62,v[18:21] offset:0
ds_read2_b32 v[22:23],v5 offset0:0 offset1:16
ds_read2_b32 v[24:25],v5 offset0:32 offset1:48
s_mov_b64 s[40:41],s[86:87]
s_mov_b32 s42,4194304
s_mov_b32 s43,0x00020000
L 0
s_add_u32 s64,s75,0
buffer_atomic_pk_add_bf16 v22,v9,s[40:43],s64 offen sc1
s_add_u32 s64,s75,8192
buffer_atomic_pk_add_bf16 v23,v9,s[40:43],s64 offen sc1
s_add_u32 s64,s75,16384
buffer_atomic_pk_add_bf16 v24,v9,s[40:43],s64 offen sc1
s_add_u32 s64,s75,24576
buffer_atomic_pk_add_bf16 v25,v9,s[40:43],s64 offen sc1
s_mov_b32 s67,0x7fff0000
s_mov_b32 s69,0x7fff0000
Lg4_xa1_%=:
s_mov_b32 s80,0
s_cmp_eq_u32 vcc_lo,-1
s_cbranch_scc0 Lg4_xn1_%=
s_add_u32 m0,s57,0
s_add_u32 s56,s55,0
D 0,56,0
v_lshlrev_b32 v62,16,v58
D 0,56,1024
v_add_f32 v18,v18,v62
D 0,56,2048
v_and_b32 v63,0xffff0000,v58
D 0,56,3072
v_add_f32 v19,v19,v63
s_add_u32 m0,s57,4096
s_add_u32 s56,s56,4096
D 0,56,0
v_lshlrev_b32 v62,16,v59
D 0,56,1024
v_add_f32 v20,v20,v62
D 0,56,2048
v_and_b32 v63,0xffff0000,v59
s_add_u32 m0,s57,28672
s_add_u32 s56,s55,28672
D 0,56,0
v_add_f32 v21,v21,v63
D 0,56,1024
v_lshlrev_b32 v62,16,v60
D 0,56,2048
v_add_f32 v22,v22,v62
D 0,56,3072
v_and_b32 v63,0xffff0000,v60
s_add_u32 m0,s57,32768
s_add_u32 s56,s56,4096
D 0,56,0
v_add_f32 v23,v23,v63
D 0,56,1024
v_lshlrev_b32 v62,16,v61
D 0,56,2048
v_add_f32 v24,v24,v62
s_add_u32 s55,s55,s60
s_sub_u32 s57,s84,s57
s_add_u32 s54,s54,s46
v_and_b32 v63,0xffff0000,v61
v_add_f32 v25,v25,v63
v_cvt_pk_bf16_f32 v18,v18,v19
v_cvt_pk_bf16_f32 v19,v20,v21
v_cvt_pk_bf16_f32 v20,v22,v23
v_cvt_pk_bf16_f32 v21,v24,v25
buffer_store_dwordx4 v[18:21],v0,s[36:39],s67 offen offset:0 sc0 sc1
s_mov_b32 s72,s69
s_mov_b32 s69,0x7fff0000
s_mov_b32 s67,0x7fff0000
s_mov_b32 s77,0x7fff0000
s_branch Lg4_gox_%=
Lg4_xn1_%=:
s_cmp_ge_u32 s54,256
s_cbranch_scc1 Lg4_fin1_%=
s_add_u32 m0,s59,4096
s_nop 0
buffer_load_dword v8,s[36:39],s58 offen offset:0 sc1 lds
buffer_store_dword v4,v3,s[36:39],s71 offen offset:0 sc0 sc1
s_add_u32 m0,s59,5120
s_nop 0
buffer_load_dword v8,s[36:39],s58 offen offset:0 sc1 lds
buffer_store_dword v4,v3,s[36:39],s71 offen offset:0 sc0 sc1
s_mov_b32 s83,0
v_lshlrev_b32 v62,16,v58
v_add_f32 v18,v18,v62
v_and_b32 v63,0xffff0000,v58
v_add_f32 v19,v19,v63
v_lshlrev_b32 v62,16,v59
v_add_f32 v20,v20,v62
v_and_b32 v63,0xffff0000,v59
v_add_f32 v21,v21,v63
v_lshlrev_b32 v62,16,v60
v_add_f32 v22,v22,v62
v_and_b32 v63,0xffff0000,v60
v_add_f32 v23,v23,v63
v_lshlrev_b32 v62,16,v61
v_add_f32 v24,v24,v62
v_and_b32 v63,0xffff0000,v61
v_add_f32 v25,v25,v63
v_cvt_pk_bf16_f32 v18,v18,v19
v_cvt_pk_bf16_f32 v19,v20,v21
v_cvt_pk_bf16_f32 v20,v22,v23
v_cvt_pk_bf16_f32 v21,v24,v25
buffer_store_dwordx4 v[18:21],v0,s[36:39],s67 offen offset:0 sc0 sc1
s_mov_b32 s72,s69
s_mov_b32 s69,0x7fff0000
s_mov_b32 s67,0x7fff0000
s_mov_b32 s77,0x7fff0000
s_branch Lg4_poll_%=
Lg4_fin1_%=:
v_lshlrev_b32 v62,16,v58
v_add_f32 v18,v18,v62
v_and_b32 v63,0xffff0000,v58
v_add_f32 v19,v19,v63
v_lshlrev_b32 v62,16,v59
v_add_f32 v20,v20,v62
v_and_b32 v63,0xffff0000,v59
v_add_f32 v21,v21,v63
v_lshlrev_b32 v62,16,v60
v_add_f32 v22,v22,v62
v_and_b32 v63,0xffff0000,v60
v_add_f32 v23,v23,v63
v_lshlrev_b32 v62,16,v61
v_add_f32 v24,v24,v62
v_and_b32 v63,0xffff0000,v61
v_add_f32 v25,v25,v63
v_cvt_pk_bf16_f32 v18,v18,v19
v_cvt_pk_bf16_f32 v19,v20,v21
v_cvt_pk_bf16_f32 v20,v22,v23
v_cvt_pk_bf16_f32 v21,v24,v25
buffer_store_dwordx4 v[18:21],v0,s[36:39],s67 offen offset:0 sc0 sc1
s_mov_b32 s72,s69
s_mov_b32 s69,0x7fff0000
s_mov_b32 s67,0x7fff0000
s_mov_b32 s77,0x7fff0000
s_branch Lg4_done_%=
Lg4_Z0_%=:
v_mfma_f32_16x16x32_bf16 v[14:17],a[4:7],v[26:29],0
s_sub_u32 s75,s67,s85
R 46,1,5120
v_lshlrev_b32 v62,16,v58
M 14,a[12:15],30
s_mov_b64 s[40:41],s[86:87]
v_add_f32 v18,v18,v62
v_and_b32 v63,0xffff0000,v58
v_mfma_f32_16x16x32_bf16 v[10:13],a[0:3],v[26:29],0
s_mov_b32 s42,4194304
R 50,1,6144
v_add_f32 v19,v19,v63
M 10,a[8:11],30
L 3
v_lshlrev_b32 v62,16,v59
v_add_f32 v20,v20,v62
M 10,a[16:19],34
s_mov_b32 s43,0x00020000
R 54,1,7168
v_and_b32 v63,0xffff0000,v59
M 10,a[24:27],38
s_add_u32 s64,s75,0
v_add_f32 v21,v21,v63
v_lshlrev_b32 v62,16,v60
M 14,a[20:23],34
s_mov_b32 s66,s65
R 26,1,8192
v_add_f32 v22,v22,v62
M 14,a[28:31],38
L 3
v_and_b32 v63,0xffff0000,v60
v_add_f32 v23,v23,v63
M 14,a[36:39],42
R 30,1,9216
v_lshlrev_b32 v62,16,v61
M 14,a[44:47],46
v_add_f32 v24,v24,v62
v_and_b32 v63,0xffff0000,v61
M 10,a[32:35],42
R 34,1,10240
v_add_f32 v25,v25,v63
M 10,a[40:43],46
L 3
buffer_load_dwordx4 v[58:61],v0,s[36:39],s74 offen offset:0 sc1
M 10,a[48:51],50
s_add_u32 s74,s74,s62
R 38,1,11264
v_cvt_pk_bf16_f32 v18,v18,v19
M 10,a[56:59],54
v_cvt_pk_bf16_f32 v19,v20,v21
v_cvt_pk_bf16_f32 v20,v22,v23
M 14,a[52:55],50
R 42,1,12288
v_cvt_pk_bf16_f32 v21,v24,v25
M 14,a[60:63],54
L 3
v_lshrrev_b32 v62,8,v0
v_lshl_add_u32 v62,v62,5,v0
M 14,a[68:71],26
R 46,1,13312
v_add_u32 v62,s73,v62
M 14,a[76:79],30
ds_write_b128 v62,v[18:21] offset:0
M 10,a[64:67],26
R 50,1,14336
ds_read2_b32 v[22:23],v5 offset0:0 offset1:16
M 10,a[72:75],30
L 5
ds_read2_b32 v[24:25],v5 offset0:32 offset1:48
M 10,a[80:83],34
R 54,1,15360
M 10,a[88:91],38
M 14,a[84:87],34
R 26,1,16384
M 14,a[92:95],38
L 6
M 14,a[100:103],42
R 30,1,17408
M 14,a[108:111],46
L 3
buffer_atomic_pk_add_bf16 v22,v9,s[40:43],s64 offen sc1
M 10,a[96:99],42
s_add_u32 s64,s75,8192
R 34,1,18432
M 10,a[104:107],46
L 3
buffer_atomic_pk_add_bf16 v23,v9,s[40:43],s64 offen sc1
M 10,a[112:115],50
s_add_u32 s64,s75,16384
R 38,1,19456
M 10,a[120:123],54
buffer_atomic_pk_add_bf16 v24,v9,s[40:43],s64 offen sc1
M 14,a[116:119],50
s_add_u32 s64,s75,24576
R 42,1,20480
M 14,a[124:127],54
L 3
buffer_atomic_pk_add_bf16 v25,v9,s[40:43],s64 offen sc1
M 14,a[132:135],26
R 46,1,21504
M 14,a[140:143],30
M 10,a[128:131],26
R 50,1,22528
M 10,a[136:139],30
L 3
M 10,a[144:147],34
R 54,1,23552
M 10,a[152:155],38
M 14,a[148:151],34
R 26,1,24576
M 14,a[156:159],38
L 3
M 14,a[164:167],42
R 30,1,25600
M 14,a[172:175],46
M 10,a[160:163],42
R 34,1,26624
M 10,a[168:171],46
L 3
M 10,a[176:179],50
s_waitcnt vmcnt(5)
R 38,1,27648
M 10,a[184:187],54
s_barrier
M 14,a[180:183],50
R 42,1,28672
M 14,a[188:191],54
L 3
M 14,a[196:199],26
R 46,1,29696
M 14,a[204:207],30
M 10,a[192:195],26
R 50,1,30720
M 10,a[200:203],30
L 3
M 10,a[208:211],34
R 54,1,31744
M 10,a[216:219],38
M 14,a[212:215],34
R 26,1,32768
M 14,a[220:223],38
L 3
M 14,a[228:231],42
R 30,1,33792
M 14,a[236:239],46
M 10,a[224:227],42
R 34,1,34816
M 10,a[232:235],46
L 3
M 10,a[240:243],50
R 38,1,35840
M 10,a[248:251],54
M 14,a[244:247],50
R 42,1,36864
M 14,a[252:255],54
L 3
M 14,v[68:71],26
R 46,1,37888
M 14,v[76:79],30
M 10,v[64:67],26
R 50,1,38912
M 10,v[72:75],30
L 3
M 10,v[80:83],34
R 54,1,39936
M 10,v[88:91],38
M 14,v[84:87],34
R 26,1,40960
M 14,v[92:95],38
L 3
M 14,v[100:103],42
R 30,1,41984
M 14,v[108:111],46
M 10,v[96:99],42
R 34,1,43008
M 10,v[104:107],46
L 3
M 10,v[112:115],50
R 38,1,44032
M 10,v[120:123],54
M 14,v[116:119],50
R 42,1,45056
M 14,v[124:127],54
L 3
M 14,v[132:135],26
R 46,1,46080
M 14,v[140:143],30
M 10,v[128:131],26
R 50,1,47104
M 10,v[136:139],30
L 3
M 10,v[144:147],34
R 54,1,48128
M 10,v[152:155],38
M 14,v[148:151],34
R 26,1,49152
M 14,v[156:159],38
L 3
M 14,v[164:167],42
R 30,1,50176
M 14,v[172:175],46
M 10,v[160:163],42
R 34,1,51200
M 10,v[168:171],46
L 3
M 10,v[176:179],50
R 38,1,52224
M 10,v[184:187],54
M 14,v[180:183],50
R 42,1,53248
M 14,v[188:191],54
L 3
M 14,v[196:199],26
R 46,1,54272
M 14,v[204:207],30
M 10,v[192:195],26
R 50,1,55296
M 10,v[200:203],30
L 3
M 10,v[208:211],34
R 54,1,56320
M 10,v[216:219],38
M 14,v[212:215],34
M 14,v[220:223],38
L 2
M 14,v[228:231],42
M 14,v[236:239],46
M 10,v[224:227],42
M 10,v[232:235],46
L 0
M 10,v[240:243],50
M 10,v[248:251],54
M 14,v[244:247],50
M 14,v[252:255],54
s_waitcnt vmcnt(4)
s_sub_u32 s75,s66,s85
v_lshlrev_b32 v62,16,v58
s_nop 2
v_add_f32 v10,v10,v62
v_and_b32 v63,0xffff0000,v58
v_add_f32 v11,v11,v63
v_lshlrev_b32 v62,16,v59
v_add_f32 v12,v12,v62
v_and_b32 v63,0xffff0000,v59
v_add_f32 v13,v13,v63
v_lshlrev_b32 v62,16,v60
v_add_f32 v14,v14,v62
v_and_b32 v63,0xffff0000,v60
v_add_f32 v15,v15,v63
v_lshlrev_b32 v62,16,v61
v_add_f32 v16,v16,v62
v_and_b32 v63,0xffff0000,v61
v_add_f32 v17,v17,v63
v_cvt_pk_bf16_f32 v10,v10,v11
v_cvt_pk_bf16_f32 v11,v12,v13
v_cvt_pk_bf16_f32 v12,v14,v15
v_cvt_pk_bf16_f32 v13,v16,v17
v_lshrrev_b32 v62,8,v0
v_lshl_add_u32 v62,v62,5,v0
v_add_u32 v62,s73,v62
ds_write_b128 v62,v[10:13] offset:0
ds_read2_b32 v[14:15],v5 offset0:0 offset1:16
ds_read2_b32 v[16:17],v5 offset0:32 offset1:48
s_mov_b64 s[40:41],s[86:87]
s_mov_b32 s42,4194304
s_mov_b32 s43,0x00020000
L 0
s_add_u32 s64,s75,0
buffer_atomic_pk_add_bf16 v14,v9,s[40:43],s64 offen sc1
s_add_u32 s64,s75,8192
buffer_atomic_pk_add_bf16 v15,v9,s[40:43],s64 offen sc1
s_add_u32 s64,s75,16384
buffer_atomic_pk_add_bf16 v16,v9,s[40:43],s64 offen sc1
s_add_u32 s64,s75,24576
buffer_atomic_pk_add_bf16 v17,v9,s[40:43],s64 offen sc1
s_branch Lg4_done_%=
Lg4_Z1_%=:
v_mfma_f32_16x16x32_bf16 v[22:25],a[4:7],v[26:29],0
s_sub_u32 s75,s66,s85
R 46,2,5120
v_lshlrev_b32 v62,16,v58
M 22,a[12:15],30
s_mov_b64 s[40:41],s[86:87]
v_add_f32 v10,v10,v62
v_and_b32 v63,0xffff0000,v58
v_mfma_f32_16x16x32_bf16 v[18:21],a[0:3],v[26:29],0
s_mov_b32 s42,4194304
R 50,2,6144
v_add_f32 v11,v11,v63
M 18,a[8:11],30
L 3
v_lshlrev_b32 v62,16,v59
v_add_f32 v12,v12,v62
M 18,a[16:19],34
s_mov_b32 s43,0x00020000
R 54,2,7168
v_and_b32 v63,0xffff0000,v59
M 18,a[24:27],38
s_add_u32 s64,s75,0
v_add_f32 v13,v13,v63
v_lshlrev_b32 v62,16,v60
M 22,a[20:23],34
s_mov_b32 s67,s65
R 26,2,8192
v_add_f32 v14,v14,v62
M 22,a[28:31],38
L 3
v_and_b32 v63,0xffff0000,v60
v_add_f32 v15,v15,v63
M 22,a[36:39],42
R 30,2,9216
v_lshlrev_b32 v62,16,v61
M 22,a[44:47],46
v_add_f32 v16,v16,v62
v_and_b32 v63,0xffff0000,v61
M 18,a[32:35],42
R 34,2,10240
v_add_f32 v17,v17,v63
M 18,a[40:43],46
L 3
buffer_load_dwordx4 v[58:61],v0,s[36:39],s74 offen offset:0 sc1
M 18,a[48:51],50
s_add_u32 s74,s74,s62
R 38,2,11264
v_cvt_pk_bf16_f32 v10,v10,v11
M 18,a[56:59],54
v_cvt_pk_bf16_f32 v11,v12,v13
v_cvt_pk_bf16_f32 v12,v14,v15
M 22,a[52:55],50
R 42,2,12288
v_cvt_pk_bf16_f32 v13,v16,v17
M 22,a[60:63],54
L 3
v_lshrrev_b32 v62,8,v0
v_lshl_add_u32 v62,v62,5,v0
M 22,a[68:71],26
R 46,2,13312
v_add_u32 v62,s73,v62
M 22,a[76:79],30
ds_write_b128 v62,v[10:13] offset:0
M 18,a[64:67],26
R 50,2,14336
ds_read2_b32 v[14:15],v5 offset0:0 offset1:16
M 18,a[72:75],30
L 5
ds_read2_b32 v[16:17],v5 offset0:32 offset1:48
M 18,a[80:83],34
R 54,2,15360
M 18,a[88:91],38
)"
R"(M 22,a[84:87],34
R 26,2,16384
M 22,a[92:95],38
L 6
M 22,a[100:103],42
R 30,2,17408
M 22,a[108:111],46
L 3
buffer_atomic_pk_add_bf16 v14,v9,s[40:43],s64 offen sc1
M 18,a[96:99],42
s_add_u32 s64,s75,8192
R 34,2,18432
M 18,a[104:107],46
L 3
buffer_atomic_pk_add_bf16 v15,v9,s[40:43],s64 offen sc1
M 18,a[112:115],50
s_add_u32 s64,s75,16384
R 38,2,19456
M 18,a[120:123],54
buffer_atomic_pk_add_bf16 v16,v9,s[40:43],s64 offen sc1
M 22,a[116:119],50
s_add_u32 s64,s75,24576
R 42,2,20480
M 22,a[124:127],54
L 3
buffer_atomic_pk_add_bf16 v17,v9,s[40:43],s64 offen sc1
M 22,a[132:135],26
R 46,2,21504
M 22,a[140:143],30
M 18,a[128:131],26
R 50,2,22528
M 18,a[136:139],30
L 3
M 18,a[144:147],34
R 54,2,23552
M 18,a[152:155],38
M 22,a[148:151],34
R 26,2,24576
M 22,a[156:159],38
L 3
M 22,a[164:167],42
R 30,2,25600
M 22,a[172:175],46
M 18,a[160:163],42
R 34,2,26624
M 18,a[168:171],46
L 3
M 18,a[176:179],50
s_waitcnt vmcnt(5)
R 38,2,27648
M 18,a[184:187],54
s_barrier
M 22,a[180:183],50
R 42,2,28672
M 22,a[188:191],54
L 3
M 22,a[196:199],26
R 46,2,29696
M 22,a[204:207],30
M 18,a[192:195],26
R 50,2,30720
M 18,a[200:203],30
L 3
M 18,a[208:211],34
R 54,2,31744
M 18,a[216:219],38
M 22,a[212:215],34
R 26,2,32768
M 22,a[220:223],38
L 3
M 22,a[228:231],42
R 30,2,33792
M 22,a[236:239],46
M 18,a[224:227],42
R 34,2,34816
M 18,a[232:235],46
L 3
M 18,a[240:243],50
R 38,2,35840
M 18,a[248:251],54
M 22,a[244:247],50
R 42,2,36864
M 22,a[252:255],54
L 3
M 22,v[68:71],26
R 46,2,37888
M 22,v[76:79],30
M 18,v[64:67],26
R 50,2,38912
M 18,v[72:75],30
L 3
M 18,v[80:83],34
R 54,2,39936
M 18,v[88:91],38
M 22,v[84:87],34
R 26,2,40960
M 22,v[92:95],38
L 3
M 22,v[100:103],42
R 30,2,41984
M 22,v[108:111],46
M 18,v[96:99],42
R 34,2,43008
M 18,v[104:107],46
L 3
M 18,v[112:115],50
R 38,2,44032
M 18,v[120:123],54
M 22,v[116:119],50
R 42,2,45056
M 22,v[124:127],54
L 3
M 22,v[132:135],26
R 46,2,46080
M 22,v[140:143],30
M 18,v[128:131],26
R 50,2,47104
M 18,v[136:139],30
L 3
M 18,v[144:147],34
R 54,2,48128
M 18,v[152:155],38
M 22,v[148:151],34
R 26,2,49152
M 22,v[156:159],38
L 3
M 22,v[164:167],42
R 30,2,50176
M 22,v[172:175],46
M 18,v[160:163],42
R 34,2,51200
M 18,v[168:171],46
L 3
M 18,v[176:179],50
R 38,2,52224
M 18,v[184:187],54
M 22,v[180:183],50
R 42,2,53248
M 22,v[188:191],54
L 3
M 22,v[196:199],26
R 46,2,54272
M 22,v[204:207],30
M 18,v[192:195],26
R 50,2,55296
M 18,v[200:203],30
L 3
M 18,v[208:211],34
R 54,2,56320
M 18,v[216:219],38
M 22,v[212:215],34
M 22,v[220:223],38
L 2
M 22,v[228:231],42
M 22,v[236:239],46
M 18,v[224:227],42
M 18,v[232:235],46
L 0
M 18,v[240:243],50
M 18,v[248:251],54
M 22,v[244:247],50
M 22,v[252:255],54
s_waitcnt vmcnt(4)
s_sub_u32 s75,s67,s85
v_lshlrev_b32 v62,16,v58
s_nop 2
v_add_f32 v18,v18,v62
v_and_b32 v63,0xffff0000,v58
v_add_f32 v19,v19,v63
v_lshlrev_b32 v62,16,v59
v_add_f32 v20,v20,v62
v_and_b32 v63,0xffff0000,v59
v_add_f32 v21,v21,v63
v_lshlrev_b32 v62,16,v60
v_add_f32 v22,v22,v62
v_and_b32 v63,0xffff0000,v60
v_add_f32 v23,v23,v63
v_lshlrev_b32 v62,16,v61
v_add_f32 v24,v24,v62
v_and_b32 v63,0xffff0000,v61
v_add_f32 v25,v25,v63
v_cvt_pk_bf16_f32 v18,v18,v19
v_cvt_pk_bf16_f32 v19,v20,v21
v_cvt_pk_bf16_f32 v20,v22,v23
v_cvt_pk_bf16_f32 v21,v24,v25
v_lshrrev_b32 v62,8,v0
v_lshl_add_u32 v62,v62,5,v0
v_add_u32 v62,s73,v62
ds_write_b128 v62,v[18:21] offset:0
ds_read2_b32 v[22:23],v5 offset0:0 offset1:16
ds_read2_b32 v[24:25],v5 offset0:32 offset1:48
s_mov_b64 s[40:41],s[86:87]
s_mov_b32 s42,4194304
s_mov_b32 s43,0x00020000
L 0
s_add_u32 s64,s75,0
buffer_atomic_pk_add_bf16 v22,v9,s[40:43],s64 offen sc1
s_add_u32 s64,s75,8192
buffer_atomic_pk_add_bf16 v23,v9,s[40:43],s64 offen sc1
s_add_u32 s64,s75,16384
buffer_atomic_pk_add_bf16 v24,v9,s[40:43],s64 offen sc1
s_add_u32 s64,s75,24576
buffer_atomic_pk_add_bf16 v25,v9,s[40:43],s64 offen sc1
s_branch Lg4_done_%=
Lg4_done_%=:
s_nop 0
s_barrier
buffer_store_dword v4,v3,s[36:39],s70 offen offset:0 sc0 sc1
s_waitcnt vmcnt(1)
s_barrier
buffer_store_dword v4,v3,s[36:39],s72 offen offset:0 sc0 sc1
s_waitcnt vmcnt(0) lgkmcnt(0)
s_barrier
s_endpgm
Lcode_g4_e:
.purgem M
.purgem R
.purgem T
.purgem D
.purgem L
)"
  :: "s"(ws), "s"(sWave), "s"(sG0), "s"(sStep), "s"(sOutOff), "s"(sFoutOff), "s"(sAuxOff), "s"(sWw), "s"(sBuf), "s"(sRk), "v"(vL16), "v"(vFlag), "v"(vSnapOff), "v"(vSnapRd), "v"(vWd0), "v"(vWd1), "v"(vWr0), "v"(vWr1), "v"(vSp), "v"(vSn), "s"(w4), "s"(out) : "v0", "v1", "v2", "v3", "v4", "v5", "v6", "v7", "v8", "v9", "v10", "v11", "v12", "v13", "v14", "v15", "v16", "v17", "v18", "v19", "v20", "v21", "v22", "v23", "v24", "v25", "v26", "v27", "v28", "v29", "v30", "v31", "v32", "v33", "v34", "v35", "v36", "v37", "v38", "v39", "v40", "v41", "v42", "v43", "v44", "v45", "v46", "v47", "v48", "v49", "v50", "v51", "v52", "v53", "v54", "v55", "v56", "v57", "v58", "v59", "v60", "v61", "v62", "v63", "v64", "v65", "v66", "v67", "v68", "v69", "v70", "v71", "v72", "v73", "v74", "v75", "v76", "v77", "v78", "v79", "v80", "v81", "v82", "v83", "v84", "v85", "v86", "v87", "v88", "v89", "v90", "v91", "v92", "v93", "v94", "v95", "v96", "v97", "v98", "v99", "v100", "v101", "v102", "v103", "v104", "v105", "v106", "v107", "v108", "v109", "v110", "v111", "v112", "v113", "v114", "v115", "v116", "v117", "v118", "v119", "v120", "v121", "v122", "v123", "v124", "v125", "v126", "v127", "v128", "v129", "v130", "v131", "v132", "v133", "v134", "v135", "v136", "v137", "v138", "v139", "v140", "v141", "v142", "v143", "v144", "v145", "v146", "v147", "v148", "v149", "v150", "v151", "v152", "v153", "v154", "v155", "v156", "v157", "v158", "v159", "v160", "v161", "v162", "v163", "v164", "v165", "v166", "v167", "v168", "v169", "v170", "v171", "v172", "v173", "v174", "v175", "v176", "v177", "v178", "v179", "v180", "v181", "v182", "v183", "v184", "v185", "v186", "v187", "v188", "v189", "v190", "v191", "v192", "v193", "v194", "v195", "v196", "v197", "v198", "v199", "v200", "v201", "v202", "v203", "v204", "v205", "v206", "v207", "v208", "v209", "v210", "v211", "v212", "v213", "v214", "v215", "v216", "v217", "v218", "v219", "v220", "v221", "v222", "v223", "v224", "v225", "v226", "v227", "v228", "v229", "v230", "v231", "v232", "v233", "v234", "v235", "v236", "v237", "v238", "v239", "a0", "a1", "a2", "a3", "a4", "a5", "a6", "a7", "a8", "a9", "a10", "a11", "a12", "a13", "a14", "a15", "a16", "a17", "a18", "a19", "a20", "a21", "a22", "a23", "a24", "a25", "a26", "a27", "a28", "a29", "a30", "a31", "a32", "a33", "a34", "a35", "a36", "a37", "a38", "a39", "a40", "a41", "a42", "a43", "a44", "a45", "a46", "a47", "a48", "a49", "a50", "a51", "a52", "a53", "a54", "a55", "a56", "a57", "a58", "a59", "a60", "a61", "a62", "a63", "a64", "a65", "a66", "a67", "a68", "a69", "a70", "a71", "a72", "a73", "a74", "a75", "a76", "a77", "a78", "a79", "a80", "a81", "a82", "a83", "a84", "a85", "a86", "a87", "a88", "a89", "a90", "a91", "a92", "a93", "a94", "a95", "a96", "a97", "a98", "a99", "a100", "a101", "a102", "a103", "a104", "a105", "a106", "a107", "a108", "a109", "a110", "a111", "a112", "a113", "a114", "a115", "a116", "a117", "a118", "a119", "a120", "a121", "a122", "a123", "a124", "a125", "a126", "a127", "a128", "a129", "a130", "a131", "a132", "a133", "a134", "a135", "a136", "a137", "a138", "a139", "a140", "a141", "a142", "a143", "a144", "a145", "a146", "a147", "a148", "a149", "a150", "a151", "a152", "a153", "a154", "a155", "a156", "a157", "a158", "a159", "a160", "a161", "a162", "a163", "a164", "a165", "a166", "a167", "a168", "a169", "a170", "a171", "a172", "a173", "a174", "a175", "a176", "a177", "a178", "a179", "a180", "a181", "a182", "a183", "a184", "a185", "a186", "a187", "a188", "a189", "a190", "a191", "a192", "a193", "a194", "a195", "a196", "a197", "a198", "a199", "a200", "a201", "a202", "a203", "a204", "a205", "a206", "a207", "a208", "a209", "a210", "a211", "a212", "a213", "a214", "a215", "a216", "a217", "a218", "a219", "a220", "a221", "a222", "a223", "a224", "a225", "a226", "a227", "a228", "a229", "a230", "a231", "a232", "a233", "a234", "a235", "a236", "a237", "a238", "a239", "a240", "a241", "a242", "a243", "a244", "a245", "a246", "a247", "a248", "a249", "a250", "a251", "a252", "a253", "a254", "a255", "s36", "s37", "s38", "s39", "s40", "s41", "s42", "s43", "s44", "s45", "s46", "s47", "s48", "s49", "s50", "s51", "s52", "s53", "s54", "s55", "s56", "s57", "s58", "s59", "s60", "s61", "s62", "s63", "s64", "s65", "s66", "s67", "s68", "s69", "s70", "s71", "s72", "s73", "s74", "s75", "s76", "s77", "s78", "s79", "s80", "s81", "s82", "s83", "s84", "s85", "s86", "s87", "s88", "s89", "s90", "s91", "s92", "s93", "s94", "s95", "s96", "s97", "vcc", "scc", "memory");
    __builtin_unreachable();
  } else {
    if (kind != 6) {
      const unsigned sStep = 4;
      COMMON(kind == 1 ? 8 : 16, kind == 1 ? fj * 128 : fj < 8 ? 1024 + fj * 128 : (fj & 2) * 1024 + (fj - 8) * 128, 1024, 131072, 2048, 256, kind == 1 ? 0 : 32768);
      const int sr = l15 - ((wave & 1) ? -1 : 1) * (1 + (wave >> 1)), nx = (slice >> 1) & 1;
      const unsigned vAn = kind == 1 ? HU + vL16 : (sr & 16) ? 1024 + lg * 64 + (sr & 3) * 16 : (lg * 16 + sr) * 16;
      const unsigned vAe = kind == 1 ? ((l15 >> 2) == (nx ? 0 : 3) ? (nx ? 0 : 2 * HU) + 1024 + lg * 64 + (l15 & 3) * 16 : OOB) : (sr & 16) ? OOB : vAn;
      const unsigned vOutS = 0, vAux0 = l15 * 16, vAux1 = 139264 + (l15 * 2 + (lg & 1)) * 16 + wave * 4 + (lg >> 1) * 512, vAux2 = 139264 + (l15 * 2 + (lg & 1)) * 16;
      const unsigned vAux3 = (wave == 0 && lg < 2) ? slice * 128 + l15 * 8 + lg * 4 : OOB;
      const unsigned sOutOff = kind == 1 ? OFF_HS - HU + (slice * 4 + wave) * 1280 : OFF_X1 + (slice * 4 + wave) * 1024, sFoutOff = (kind == 1 ? OFF_FH : OFF_FX1) + slice * 128;
      const unsigned sAuxOff = OFF_XP + (slice * 4 + wave) * 1024, sBuf = kind, sWw = 16 * wave * 2048 + slice * 256;
      const u16* w12 = kind == 1 ? w1 : w2;
      unsigned epOut;
      asm volatile(
R"(.macro M d,w,x
v_mfma_f32_16x16x32_bf16 v[\d:\d+3], \w, v[\x:\x+3], v[\d:\d+3]
.endm
.macro R d,a,o
ds_read_b128 v[\d:\d+3], v\a offset:\o
.endm
.macro T d,a,o
ds_read_b64_tr_b16 \d, v\a offset:\o
.endm
.macro D a,s,o
buffer_load_dwordx4 v\a, s[36:39], s\s offen offset:\o sc1 lds
.endm
.macro L c
s_waitcnt lgkmcnt(\c)
.endm
Lcode_g12_b:
s_cmp_eq_u32 %9,2
s_cbranch_scc1 Lg2_begin_%=
s_mov_b64 s[36:37],%1
s_mov_b32 s38,111243392
s_mov_b32 s39,0x00020000
s_mov_b32 s44,%2
s_mov_b32 s45,%3
s_mov_b32 s46,%4
s_mov_b32 s47,%5
s_mov_b32 s48,%6
s_mov_b32 s49,%7
s_mov_b32 s50,%8
s_mov_b32 s51,%9
s_mov_b32 s93,%10
v_mov_b32 v0,%11
v_mov_b32 v3,%12
v_mov_b32 v5,%13
v_mov_b32 v6,%14
v_mov_b32 v18,%15
v_mov_b32 v19,%16
v_mov_b32 v20,%17
v_mov_b32 v21,%18
v_mov_b32 v22,%19
v_mov_b32 v10,%20
v_mov_b32 v11,%21
v_mov_b32 v12,%22
v_mov_b32 v13,%23
v_mov_b32 v14,%24
v_mov_b32 v15,%25
v_mov_b32 v16,%26
s_mov_b64 s[40:41],%27
s_mov_b32 s42,2097152
s_mov_b32 s43,0x00020000
s_lshl_b32 s57,s44,11
s_mov_b32 s79,111243264
buffer_load_dword v4,off,s[36:39],s79 sc1
s_add_u32 s55,s50,0
s_add_u32 m0,s57,0
s_nop 0
buffer_load_dwordx4 v18,s[40:43],s55 offen offset:0  lds
s_add_u32 m0,s57,1024
s_nop 0
buffer_load_dwordx4 v19,s[40:43],s55 offen offset:0  lds
s_add_u32 m0,s57,8064
s_nop 0
buffer_load_dwordx4 v18,s[40:43],s55 offen offset:128  lds
s_add_u32 m0,s57,9088
s_nop 0
buffer_load_dwordx4 v19,s[40:43],s55 offen offset:128  lds
s_add_u32 s55,s50,131072
s_add_u32 m0,s57,16384
s_nop 0
buffer_load_dwordx4 v18,s[40:43],s55 offen offset:0  lds
s_add_u32 m0,s57,17408
s_nop 0
buffer_load_dwordx4 v19,s[40:43],s55 offen offset:0  lds
s_add_u32 m0,s57,24448
s_nop 0
buffer_load_dwordx4 v18,s[40:43],s55 offen offset:128  lds
s_add_u32 m0,s57,25472
s_nop 0
buffer_load_dwordx4 v19,s[40:43],s55 offen offset:128  lds
s_add_u32 s55,s50,262144
s_add_u32 m0,s57,32768
s_nop 0
buffer_load_dwordx4 v18,s[40:43],s55 offen offset:0  lds
s_add_u32 m0,s57,33792
s_nop 0
buffer_load_dwordx4 v19,s[40:43],s55 offen offset:0  lds
s_add_u32 m0,s57,40832
s_nop 0
buffer_load_dwordx4 v18,s[40:43],s55 offen offset:128  lds
s_add_u32 m0,s57,41856
s_nop 0
buffer_load_dwordx4 v19,s[40:43],s55 offen offset:128  lds
s_add_u32 s55,s50,393216
s_add_u32 m0,s57,49152
s_nop 0
buffer_load_dwordx4 v18,s[40:43],s55 offen offset:0  lds
s_add_u32 m0,s57,50176
s_nop 0
buffer_load_dwordx4 v19,s[40:43],s55 offen offset:0  lds
s_add_u32 m0,s57,57216
s_nop 0
buffer_load_dwordx4 v18,s[40:43],s55 offen offset:128  lds
s_add_u32 m0,s57,58240
s_nop 0
buffer_load_dwordx4 v19,s[40:43],s55 offen offset:128  lds
s_add_u32 s55,s50,524288
s_add_u32 m0,s57,65536
s_nop 0
buffer_load_dwordx4 v18,s[40:43],s55 offen offset:0  lds
s_add_u32 m0,s57,66560
s_nop 0
buffer_load_dwordx4 v19,s[40:43],s55 offen offset:0  lds
s_add_u32 m0,s57,73600
s_nop 0
buffer_load_dwordx4 v18,s[40:43],s55 offen offset:128  lds
s_add_u32 m0,s57,74624
s_nop 0
buffer_load_dwordx4 v19,s[40:43],s55 offen offset:128  lds
s_add_u32 s55,s50,655360
s_add_u32 m0,s57,81920
s_nop 0
buffer_load_dwordx4 v18,s[40:43],s55 offen offset:0  lds
s_add_u32 m0,s57,82944
s_nop 0
buffer_load_dwordx4 v19,s[40:43],s55 offen offset:0  lds
s_add_u32 m0,s57,89984
s_nop 0
buffer_load_dwordx4 v18,s[40:43],s55 offen offset:128  lds
s_add_u32 m0,s57,91008
s_nop 0
buffer_load_dwordx4 v19,s[40:43],s55 offen offset:128  lds
s_waitcnt vmcnt(24)
s_barrier
v_readfirstlane_b32 s53,v4
s_nop 3
s_add_u32 s53,s53,1
s_cmp_eq_u32 s53,0
s_cselect_b32 s53,1,s53
v_mov_b32 v4,s53
s_waitcnt vmcnt(20) lgkmcnt(0)
s_barrier
s_add_u32 s55,s50,786432
s_add_u32 m0,s57,98304
s_nop 0
buffer_load_dwordx4 v18,s[40:43],s55 offen offset:0  lds
s_add_u32 m0,s57,99328
s_nop 0
buffer_load_dwordx4 v19,s[40:43],s55 offen offset:0  lds
s_add_u32 m0,s57,106368
s_nop 0
buffer_load_dwordx4 v18,s[40:43],s55 offen offset:128  lds
s_add_u32 m0,s57,107392
s_nop 0
buffer_load_dwordx4 v19,s[40:43],s55 offen offset:128  lds
v_add_u32 v26,0,v20
v_add_u32 v27,0,v21
T a[0:1],26,0
T a[2:3],26,512
T a[4:5],27,0
T a[6:7],27,512
T a[8:9],26,4096
T a[10:11],26,4608
T a[12:13],27,4096
T a[14:15],27,4608
s_waitcnt vmcnt(20) lgkmcnt(0)
s_barrier
s_add_u32 s55,s50,917504
s_add_u32 m0,s57,0
s_nop 0
buffer_load_dwordx4 v18,s[40:43],s55 offen offset:0  lds
s_add_u32 m0,s57,1024
s_nop 0
buffer_load_dwordx4 v19,s[40:43],s55 offen offset:0  lds
s_add_u32 m0,s57,8064
s_nop 0
buffer_load_dwordx4 v18,s[40:43],s55 offen offset:128  lds
s_add_u32 m0,s57,9088
s_nop 0
buffer_load_dwordx4 v19,s[40:43],s55 offen offset:128  lds
v_add_u32 v26,16384,v20
v_add_u32 v27,16384,v21
T a[16:17],26,0
T a[18:19],26,512
T a[20:21],27,0
T a[22:23],27,512
T a[24:25],26,4096
T a[26:27],26,4608
T a[28:29],27,4096
T a[30:31],27,4608
s_waitcnt vmcnt(20) lgkmcnt(0)
s_barrier
s_add_u32 s55,s50,1048576
s_add_u32 m0,s57,16384
s_nop 0
buffer_load_dwordx4 v18,s[40:43],s55 offen offset:0  lds
s_add_u32 m0,s57,17408
s_nop 0
buffer_load_dwordx4 v19,s[40:43],s55 offen offset:0  lds
s_add_u32 m0,s57,24448
s_nop 0
buffer_load_dwordx4 v18,s[40:43],s55 offen offset:128  lds
s_add_u32 m0,s57,25472
s_nop 0
buffer_load_dwordx4 v19,s[40:43],s55 offen offset:128  lds
v_add_u32 v26,32768,v20
v_add_u32 v27,32768,v21
T a[32:33],26,0
T a[34:35],26,512
T a[36:37],27,0
T a[38:39],27,512
T a[40:41],26,4096
T a[42:43],26,4608
T a[44:45],27,4096
T a[46:47],27,4608
s_waitcnt vmcnt(20) lgkmcnt(0)
s_barrier
s_add_u32 s55,s50,1179648
s_add_u32 m0,s57,32768
s_nop 0
buffer_load_dwordx4 v18,s[40:43],s55 offen offset:0  lds
s_add_u32 m0,s57,33792
s_nop 0
buffer_load_dwordx4 v19,s[40:43],s55 offen offset:0  lds
s_add_u32 m0,s57,40832
s_nop 0
buffer_load_dwordx4 v18,s[40:43],s55 offen offset:128  lds
s_add_u32 m0,s57,41856
s_nop 0
buffer_load_dwordx4 v19,s[40:43],s55 offen offset:128  lds
v_add_u32 v26,49152,v20
v_add_u32 v27,49152,v21
T a[48:49],26,0
T a[50:51],26,512
T a[52:53],27,0
T a[54:55],27,512
T a[56:57],26,4096
T a[58:59],26,4608
T a[60:61],27,4096
T a[62:63],27,4608
s_waitcnt vmcnt(20) lgkmcnt(0)
s_barrier
s_add_u32 s55,s50,1310720
s_add_u32 m0,s57,49152
s_nop 0
buffer_load_dwordx4 v18,s[40:43],s55 offen offset:0  lds
s_add_u32 m0,s57,50176
s_nop 0
buffer_load_dwordx4 v19,s[40:43],s55 offen offset:0  lds
s_add_u32 m0,s57,57216
s_nop 0
buffer_load_dwordx4 v18,s[40:43],s55 offen offset:128  lds
s_add_u32 m0,s57,58240
s_nop 0
buffer_load_dwordx4 v19,s[40:43],s55 offen offset:128  lds
v_add_u32 v26,65536,v20
v_add_u32 v27,65536,v21
T a[64:65],26,0
T a[66:67],26,512
T a[68:69],27,0
T a[70:71],27,512
T a[72:73],26,4096
T a[74:75],26,4608
T a[76:77],27,4096
T a[78:79],27,4608
s_waitcnt vmcnt(20) lgkmcnt(0)
s_barrier
s_add_u32 s55,s50,1441792
s_add_u32 m0,s57,65536
s_nop 0
buffer_load_dwordx4 v18,s[40:43],s55 offen offset:0  lds
s_add_u32 m0,s57,66560
s_nop 0
buffer_load_dwordx4 v19,s[40:43],s55 offen offset:0  lds
s_add_u32 m0,s57,73600
s_nop 0
buffer_load_dwordx4 v18,s[40:43],s55 offen offset:128  lds
s_add_u32 m0,s57,74624
s_nop 0
buffer_load_dwordx4 v19,s[40:43],s55 offen offset:128  lds
v_add_u32 v26,81920,v20
v_add_u32 v27,81920,v21
T a[80:81],26,0
T a[82:83],26,512
T a[84:85],27,0
T a[86:87],27,512
T a[88:89],26,4096
T a[90:91],26,4608
T a[92:93],27,4096
T a[94:95],27,4608
s_waitcnt vmcnt(20) lgkmcnt(0)
s_barrier
s_add_u32 s55,s50,1572864
s_add_u32 m0,s57,81920
s_nop 0
buffer_load_dwordx4 v18,s[40:43],s55 offen offset:0  lds
s_add_u32 m0,s57,82944
s_nop 0
buffer_load_dwordx4 v19,s[40:43],s55 offen offset:0  lds
s_add_u32 m0,s57,89984
s_nop 0
buffer_load_dwordx4 v18,s[40:43],s55 offen offset:128  lds
s_add_u32 m0,s57,91008
s_nop 0
buffer_load_dwordx4 v19,s[40:43],s55 offen offset:128  lds
v_add_u32 v26,98304,v20
v_add_u32 v27,98304,v21
T a[96:97],26,0
T a[98:99],26,512
T a[100:101],27,0
T a[102:103],27,512
T a[104:105],26,4096
T a[106:107],26,4608
T a[108:109],27,4096
T a[110:111],27,4608
s_waitcnt vmcnt(20) lgkmcnt(0)
s_barrier
s_add_u32 s55,s50,1703936
s_add_u32 m0,s57,98304
s_nop 0
buffer_load_dwordx4 v18,s[40:43],s55 offen offset:0  lds
s_add_u32 m0,s57,99328
s_nop 0
buffer_load_dwordx4 v19,s[40:43],s55 offen offset:0  lds
s_add_u32 m0,s57,106368
s_nop 0
buffer_load_dwordx4 v18,s[40:43],s55 offen offset:128  lds
s_add_u32 m0,s57,107392
s_nop 0
buffer_load_dwordx4 v19,s[40:43],s55 offen offset:128  lds
v_add_u32 v26,0,v20
v_add_u32 v27,0,v21
T a[112:113],26,0
T a[114:115],26,512
T a[116:117],27,0
T a[118:119],27,512
T a[120:121],26,4096
T a[122:123],26,4608
T a[124:125],27,4096
T a[126:127],27,4608
s_waitcnt vmcnt(20) lgkmcnt(0)
s_barrier
s_add_u32 s55,s50,1835008
s_add_u32 m0,s57,0
s_nop 0
buffer_load_dwordx4 v18,s[40:43],s55 offen offset:0  lds
s_add_u32 m0,s57,1024
s_nop 0
buffer_load_dwordx4 v19,s[40:43],s55 offen offset:0  lds
s_add_u32 m0,s57,8064
s_nop 0
buffer_load_dwordx4 v18,s[40:43],s55 offen offset:128  lds
s_add_u32 m0,s57,9088
s_nop 0
buffer_load_dwordx4 v19,s[40:43],s55 offen offset:128  lds
v_add_u32 v26,16384,v20
v_add_u32 v27,16384,v21
T a[128:129],26,0
T a[130:131],26,512
T a[132:133],27,0
T a[134:135],27,512
T a[136:137],26,4096
T a[138:139],26,4608
T a[140:141],27,4096
T a[142:143],27,4608
s_waitcnt vmcnt(20) lgkmcnt(0)
s_barrier
s_add_u32 s55,s50,1966080
s_add_u32 m0,s57,16384
s_nop 0
buffer_load_dwordx4 v18,s[40:43],s55 offen offset:0  lds
s_add_u32 m0,s57,17408
s_nop 0
buffer_load_dwordx4 v19,s[40:43],s55 offen offset:0  lds
s_add_u32 m0,s57,24448
s_nop 0
buffer_load_dwordx4 v18,s[40:43],s55 offen offset:128  lds
s_add_u32 m0,s57,25472
s_nop 0
buffer_load_dwordx4 v19,s[40:43],s55 offen offset:128  lds
v_add_u32 v26,32768,v20
v_add_u32 v27,32768,v21
T a[144:145],26,0
T a[146:147],26,512
T a[148:149],27,0
T a[150:151],27,512
T a[152:153],26,4096
T a[154:155],26,4608
T a[156:157],27,4096
T a[158:159],27,4608
s_waitcnt vmcnt(20) lgkmcnt(0)
s_barrier
v_add_u32 v26,49152,v20
v_add_u32 v27,49152,v21
T a[160:161],26,0
T a[162:163],26,512
T a[164:165],27,0
T a[166:167],27,512
T a[168:169],26,4096
T a[170:171],26,4608
T a[172:173],27,4096
T a[174:175],27,4608
s_waitcnt vmcnt(16) lgkmcnt(0)
s_barrier
v_add_u32 v26,65536,v20
v_add_u32 v27,65536,v21
T a[176:177],26,0
T a[178:179],26,512
T a[180:181],27,0
T a[182:183],27,512
T a[184:185],26,4096
T a[186:187],26,4608
T a[188:189],27,4096
T a[190:191],27,4608
s_waitcnt vmcnt(12) lgkmcnt(0)
s_barrier
v_add_u32 v26,81920,v20
v_add_u32 v27,81920,v21
T a[192:193],26,0
T a[194:195],26,512
T a[196:197],27,0
T a[198:199],27,512
T a[200:201],26,4096
T a[202:203],26,4608
T a[204:205],27,4096
T a[206:207],27,4608
s_waitcnt vmcnt(8) lgkmcnt(0)
s_barrier
v_add_u32 v26,98304,v20
v_add_u32 v27,98304,v21
T a[208:209],26,0
T a[210:211],26,512
T a[212:213],27,0
T a[214:215],27,512
T a[216:217],26,4096
T a[218:219],26,4608
T a[220:221],27,4096
T a[222:223],27,4608
s_waitcnt vmcnt(4) lgkmcnt(0)
s_barrier
v_add_u32 v26,0,v20
v_add_u32 v27,0,v21
T a[224:225],26,0
T a[226:227],26,512
T a[228:229],27,0
T a[230:231],27,512
T a[232:233],26,4096
T a[234:235],26,4608
T a[236:237],27,4096
T a[238:239],27,4608
s_waitcnt vmcnt(0) lgkmcnt(0)
s_barrier
v_add_u32 v26,16384,v20
v_add_u32 v27,16384,v21
T a[240:241],26,0
T a[242:243],26,512
T a[244:245],27,0
T a[246:247],27,512
T a[248:249],26,4096
T a[250:251],26,4608
T a[252:253],27,4096
T a[254:255],27,4608
L 0
s_barrier
v_mov_b32 v34,0x3f803f80
v_mov_b32 v35,0x3f803f80
v_mov_b32 v36,0x3f803f80
v_mov_b32 v37,0x3f803f80
s_nop 1
v_mfma_f32_16x16x32_bf16 v[106:109],a[0:3],v[34:37],0
v_mfma_f32_16x16x32_bf16 v[110:113],a[4:7],v[34:37],0
M 106,a[8:11],34
M 110,a[12:15],34
M 106,a[16:19],34
M 110,a[20:23],34
M 106,a[24:27],34
M 110,a[28:31],34
M 106,a[32:35],34
M 110,a[36:39],34
M 106,a[40:43],34
M 110,a[44:47],34
M 106,a[48:51],34
M 110,a[52:55],34
M 106,a[56:59],34
M 110,a[60:63],34
M 106,a[64:67],34
M 110,a[68:71],34
M 106,a[72:75],34
M 110,a[76:79],34
M 106,a[80:83],34
M 110,a[84:87],34
M 106,a[88:91],34
M 110,a[92:95],34
M 106,a[96:99],34
M 110,a[100:103],34
M 106,a[104:107],34
M 110,a[108:111],34
M 106,a[112:115],34
M 110,a[116:119],34
M 106,a[120:123],34
M 110,a[124:127],34
M 106,a[128:131],34
)"
R"(M 110,a[132:135],34
M 106,a[136:139],34
M 110,a[140:143],34
M 106,a[144:147],34
M 110,a[148:151],34
M 106,a[152:155],34
M 110,a[156:159],34
M 106,a[160:163],34
M 110,a[164:167],34
M 106,a[168:171],34
M 110,a[172:175],34
M 106,a[176:179],34
M 110,a[180:183],34
M 106,a[184:187],34
M 110,a[188:191],34
M 106,a[192:195],34
M 110,a[196:199],34
M 106,a[200:203],34
M 110,a[204:207],34
M 106,a[208:211],34
M 110,a[212:215],34
M 106,a[216:219],34
M 110,a[220:223],34
M 106,a[224:227],34
M 110,a[228:231],34
M 106,a[232:235],34
M 110,a[236:239],34
M 106,a[240:243],34
M 110,a[244:247],34
M 106,a[248:251],34
M 110,a[252:255],34
v_mov_b32 v4,s53
s_mov_b32 s82,0
s_mov_b32 s80,0
s_mov_b32 s54,s45
s_mul_i32 s57,s44,8192
s_mul_i32 s55,s45,32768
s_add_u32 s55,s55,s57
s_add_u32 s55,s55,8388608
s_mul_i32 s57,s44,8192
s_mul_i32 s60,s46,32768
s_mul_i32 s61,s46,1024
s_mul_i32 s62,s46,40960
s_mul_i32 s63,s46,1024
s_mul_i32 s64,s46,512
s_lshl_b32 s59,s44,8
s_add_u32 s59,s59,131072
s_mul_i32 s58,s45,1024
s_add_u32 s58,s58,109056000
s_mul_i32 s65,s45,40960
s_add_u32 s65,s65,s47
s_mul_i32 s68,s45,1024
s_add_u32 s68,s68,s48
s_mul_i32 s73,s45,512
s_add_u32 s73,s73,106954752
s_mul_i32 s74,s45,32768
s_add_u32 s74,s74,s49
s_mul_i32 s75,s45,1024
s_add_u32 s75,s75,107479040
s_mov_b32 s66,0x7fff0000
s_mov_b32 s67,0x7fff0000
s_mov_b32 s69,0x7fff0000
s_mov_b32 s76,0x7fff0000
s_mov_b32 s77,0x7fff0000
s_mov_b32 s70,0x7fff0000
s_mov_b32 s71,0x7fff0000
s_mov_b32 s72,0x7fff0000
v_mov_b32 v1,v0
v_add_u32 v2,32768,v0
s_mov_b32 s84,32768
s_mov_b32 s86,0x3a800000
s_mov_b32 s87,0x3727c5ac
s_mov_b32 s88,0xc0135761
s_mov_b32 s89,0x3d372713
s_and_b32 s85,s44,1
s_mul_i32 s85,s85,127
v_lshlrev_b32 v8,22,v0
v_and_b32 v8,0x80000000,v8
v_or_b32 v8,v8,v5
s_getpc_b64 s[40:41]
Lg1_pc_%=:
s_lshl_b32 s78,s44,13
v_lshlrev_b32 v114,3,v0
v_add_u32 v114,s78,v114
v_min_u32 v114,Lcode_g2_b-Lg1_pc_%=-4,v114
s_mov_b32 m0,163584
s_nop 0
global_load_lds_dword v114,s[40:41]
s_add_u32 m0,s59,4096
s_nop 0
buffer_load_dword v5,s[36:39],s58 offen offset:0 sc1 lds
buffer_store_dword v4,v3,s[36:39],s71 offen offset:0 sc0 sc1
s_add_u32 m0,s59,5120
s_nop 0
buffer_load_dword v5,s[36:39],s58 offen offset:0 sc1 lds
buffer_store_dword v4,v3,s[36:39],s71 offen offset:0 sc0 sc1
s_mov_b32 s83,0
Lg1_poll_%=:
s_add_u32 s78,s83,2
s_and_b32 s78,s78,3
s_lshl_b32 s78,s78,10
s_add_u32 s78,s78,4096
s_add_u32 m0,s59,s78
s_nop 0
buffer_load_dword v5,s[36:39],s58 offen offset:0 sc1 lds
s_and_b32 s78,s83,3
s_lshl_b32 s78,s78,10
v_add_u32 v114,s78,v6
s_waitcnt vmcnt(4)
s_barrier
ds_read_b32 v7,v114 offset:4096
buffer_store_dword v4,v3,s[36:39],s70 offen offset:0 sc0 sc1
s_mov_b32 s70,s71
s_mov_b32 s71,s72
s_mov_b32 s72,0x7fff0000
s_add_u32 s83,s83,1
s_add_u32 s82,s82,1
L 0
v_cmp_eq_u32 vcc,s53,v7
s_nop 0
s_cmp_eq_u32 vcc_lo,-1
s_cbranch_scc1 Lg1_go_%=
s_cmp_lt_u32 s82,20000
s_cbranch_scc1 Lg1_poll_%=
Lg1_go_%=:
s_mov_b32 m0,s57
s_add_u32 s56,s55,4096
D 0,55,0
D 0,55,1024
D 0,55,2048
D 0,55,3072
s_add_u32 m0,s57,4096
s_nop 0
D 0,56,0
D 0,56,1024
D 0,56,2048
D 0,56,3072
s_add_u32 s55,s55,s60
s_add_u32 s57,s57,32768
s_and_b32 s57,s57,131071
s_add_u32 s54,s54,s46
Lg1_gox_%=:
s_add_u32 s58,s58,s61
s_cmp_eq_u32 vcc_hi,-1
s_cbranch_scc1 Lg1_go2_%=
s_add_u32 m0,s59,2048
s_nop 0
buffer_load_dword v5,s[36:39],s58 offen offset:0 sc1 lds
s_waitcnt vmcnt(0)
s_barrier
buffer_store_dword v4,v3,s[36:39],s70 offen offset:0 sc0 sc1
s_min_u32 s71,s71,s72
buffer_store_dword v4,v3,s[36:39],s71 offen offset:0 sc0 sc1
s_mov_b32 s70,0x7fff0000
s_mov_b32 s71,0x7fff0000
s_mov_b32 s72,0x7fff0000
s_cmp_eq_u32 s80,1
s_cbranch_scc1 Lg1_st11_%=
Lg1_st10_%=:
ds_read_b32 v7,v6 offset:2048
R 34,1,0
R 38,1,1024
R 42,1,2048
R 46,1,3072
R 50,1,4096
R 54,1,5120
R 58,1,6144
L 7
v_cmp_eq_u32 vcc,s53,v7
L 5
s_cmp_eq_u64 vcc,-1
s_cbranch_scc1 Lg1_L20_%=
s_cmp_eq_u32 vcc_lo,-1
s_cbranch_scc1 Lg1_L10_%=
s_branch Lg1_X0_%=
Lg1_st11_%=:
ds_read_b32 v7,v6 offset:2048
R 34,2,0
R 38,2,1024
R 42,2,2048
R 46,2,3072
R 50,2,4096
R 54,2,5120
R 58,2,6144
L 7
v_cmp_eq_u32 vcc,s53,v7
L 5
s_cmp_eq_u64 vcc,-1
s_cbranch_scc1 Lg1_L21_%=
s_cmp_eq_u32 vcc_lo,-1
s_cbranch_scc1 Lg1_L11_%=
s_branch Lg1_X1_%=
Lg1_go2_%=:
s_add_u32 s58,s58,s61
s_add_u32 m0,s59,2048
s_nop 0
buffer_load_dword v5,s[36:39],s58 offen offset:0 sc1 lds
s_mov_b32 m0,s57
s_add_u32 s56,s55,4096
D 0,55,0
D 0,55,1024
D 0,55,2048
D 0,55,3072
s_add_u32 m0,s57,4096
s_nop 0
D 0,56,0
D 0,56,1024
D 0,56,2048
D 0,56,3072
s_add_u32 s55,s55,s60
s_add_u32 s57,s57,32768
s_and_b32 s57,s57,131071
s_add_u32 s54,s54,s46
s_waitcnt vmcnt(8)
s_barrier
buffer_store_dword v4,v3,s[36:39],s70 offen offset:0 sc0 sc1
s_min_u32 s71,s71,s72
buffer_store_dword v4,v3,s[36:39],s71 offen offset:0 sc0 sc1
s_mov_b32 s70,0x7fff0000
s_mov_b32 s71,0x7fff0000
s_mov_b32 s72,0x7fff0000
s_cmp_eq_u32 s80,1
s_cbranch_scc1 Lg1_st21_%=
Lg1_st20_%=:
ds_read_b32 v7,v6 offset:2048
R 34,1,0
R 38,1,1024
R 42,1,2048
R 46,1,3072
R 50,1,4096
R 54,1,5120
R 58,1,6144
L 7
v_cmp_eq_u32 vcc,s53,v7
L 5
s_cmp_eq_u32 vcc_lo,-1
s_cbranch_scc1 Lg1_S0_%=
s_branch Lg1_N0_%=
Lg1_st21_%=:
ds_read_b32 v7,v6 offset:2048
R 34,2,0
R 38,2,1024
R 42,2,2048
R 46,2,3072
R 50,2,4096
R 54,2,5120
R 58,2,6144
L 7
v_cmp_eq_u32 vcc,s53,v7
L 5
s_cmp_eq_u32 vcc_lo,-1
s_cbranch_scc1 Lg1_S1_%=
s_branch Lg1_N1_%=
Lg1_L20_%=:
v_mfma_f32_16x16x32_bf16 v[22:25],a[4:7],v[34:37],0
s_add_u32 s58,s58,s61
s_add_u32 s58,s58,s61
R 62,1,7168
M 22,a[12:15],38
s_add_u32 m0,s59,0
buffer_load_dwordx4 v[98:101],v11,s[36:39],s73 offen offset:0 sc1
v_mfma_f32_16x16x32_bf16 v[18:21],a[0:3],v[34:37],0
s_add_u32 s56,s55,4096
R 66,1,8192
v_fma_f32 v26,-v102,v106,v26
M 18,a[8:11],38
L 5
buffer_load_dword v8,s[36:39],s58 offen offset:0 sc1 lds
M 18,a[16:19],42
s_mov_b32 m0,s57
R 70,1,9216
v_fma_f32 v27,-v102,v107,v27
M 18,a[24:27],46
s_add_u32 s54,s54,s46
v_fma_f32 v28,-v102,v108,v28
v_fma_f32 v29,-v102,v109,v29
M 22,a[20:23],42
s_mov_b32 s66,s65
R 74,1,10240
v_mul_f32 v114,v26,v26
M 22,a[28:31],46
L 5
D 0,55,0
M 22,a[36:39],50
s_add_u32 s73,s73,s64
R 78,1,11264
v_mul_f32 v115,v27,v27
M 22,a[44:47],54
s_add_u32 s54,s54,s46
v_mul_f32 v116,v28,v28
v_mul_f32 v117,v29,v29
M 18,a[32:35],50
s_add_u32 s65,s65,s62
R 82,1,12288
v_fma_f32 v114,v104,v114,v103
M 18,a[40:43],54
L 5
D 0,55,1024
M 18,a[48:51],58
R 86,1,13312
v_fma_f32 v115,v104,v115,v103
M 18,a[56:59],62
v_fma_f32 v116,v104,v116,v103
v_fma_f32 v117,v104,v117,v103
M 22,a[52:55],58
R 90,1,14336
v_mul_f32 v114,v26,v114
M 22,a[60:63],62
L 5
D 0,55,2048
M 22,a[68:71],66
R 94,1,15360
v_mul_f32 v115,v27,v115
M 22,a[76:79],70
v_exp_f32 v114,v114
M 18,a[64:67],66
R 34,1,16384
v_mul_f32 v116,v28,v116
M 18,a[72:75],70
L 5
D 0,55,3072
M 18,a[80:83],74
s_add_u32 m0,s57,4096
R 38,1,17408
v_mul_f32 v117,v29,v117
M 18,a[88:91],78
s_add_u32 s57,s57,32768
v_exp_f32 v115,v115
M 22,a[84:87],74
s_and_b32 s57,s57,131071
R 42,1,18432
v_fma_f32 v114,v114,v105,v105
M 22,a[92:95],78
L 5
D 0,56,0
M 22,a[100:103],82
s_add_u32 s55,s55,s60
R 46,1,19456
v_fma_f32 v115,v115,v105,v105
M 22,a[108:111],86
v_exp_f32 v116,v116
M 18,a[96:99],82
R 50,1,20480
v_fma_f32 v116,v116,v105,v105
M 18,a[104:107],86
L 5
D 0,56,1024
M 18,a[112:115],90
R 54,1,21504
v_fma_f32 v30,-v102,v110,v30
M 18,a[120:123],94
v_exp_f32 v117,v117
M 22,a[116:119],90
R 58,1,22528
v_fma_f32 v117,v117,v105,v105
M 22,a[124:127],94
L 5
D 0,56,2048
M 22,a[132:135],34
R 62,1,23552
v_fma_f32 v31,-v102,v111,v31
M 22,a[140:143],38
v_rcp_f32 v114,v114
M 18,a[128:131],34
R 66,1,24576
v_mul_f32 v26,v26,v114
M 18,a[136:139],38
L 5
D 0,56,3072
M 18,a[144:147],42
s_mov_b32 m0,s57
R 70,1,25600
v_fma_f32 v32,-v102,v112,v32
M 18,a[152:155],46
s_add_u32 s56,s55,4096
v_rcp_f32 v115,v115
M 22,a[148:151],42
R 74,1,26624
v_mul_f32 v27,v27,v115
M 22,a[156:159],46
L 5
D 0,55,0
M 22,a[164:167],50
R 78,1,27648
v_cvt_pk_bf16_f32 v122,v26,v27
M 22,a[172:175],54
D 0,55,1024
M 18,a[160:163],50
R 82,1,28672
v_fma_f32 v33,-v102,v113,v33
M 18,a[168:171],54
L 5
v_rcp_f32 v116,v116
M 18,a[176:179],58
R 86,1,29696
v_mul_f32 v28,v28,v116
M 18,a[184:187],62
D 0,55,2048
M 22,a[180:183],58
R 90,1,30720
v_mul_f32 v118,v30,v30
M 22,a[188:191],62
L 5
D 0,55,3072
M 22,a[196:199],66
s_waitcnt vmcnt(4)
R 94,1,31744
v_mul_f32 v119,v31,v31
M 22,a[204:207],70
s_barrier
buffer_store_dword v4,v3,s[36:39],s70 offen offset:0 sc0 sc1
M 18,a[192:195],66
s_add_u32 m0,s57,4096
R 34,2,0
ds_read_b32 v7,v6 offset:0
M 18,a[200:203],70
L 6
D 0,56,0
M 18,a[208:211],74
s_mov_b32 s70,s69
R 38,2,1024
v_mul_f32 v120,v32,v32
M 18,a[216:219],78
s_add_u32 s57,s57,32768
D 0,56,1024
M 22,a[212:215],74
s_mov_b32 s69,s68
R 42,2,2048
v_mul_f32 v121,v33,v33
v_fma_f32 v118,v104,v118,v103
v_fma_f32 v119,v104,v119,v103
v_fma_f32 v120,v104,v120,v103
v_fma_f32 v121,v104,v121,v103
v_mul_f32 v118,v30,v118
v_mul_f32 v119,v31,v119
v_mul_f32 v120,v32,v120
v_mul_f32 v121,v33,v121
v_exp_f32 v118,v118
v_exp_f32 v119,v119
v_exp_f32 v120,v120
v_exp_f32 v121,v121
M 22,a[220:223],78
L 6
v_rcp_f32 v117,v117
v_fma_f32 v118,v118,v105,v105
v_fma_f32 v119,v119,v105,v105
v_fma_f32 v120,v120,v105,v105
v_fma_f32 v121,v121,v105,v105
v_rcp_f32 v118,v118
v_rcp_f32 v119,v119
v_rcp_f32 v120,v120
v_rcp_f32 v121,v121
M 22,a[228:231],82
L 2
R 46,2,3072
v_mul_f32 v29,v29,v117
v_cvt_pk_bf16_f32 v123,v28,v29
v_mul_f32 v30,v30,v118
v_mul_f32 v31,v31,v119
v_mul_f32 v32,v32,v120
v_mul_f32 v33,v33,v121
v_cvt_pk_bf16_f32 v124,v30,v31
v_cvt_pk_bf16_f32 v125,v32,v33
M 22,a[236:239],86
s_add_u32 s55,s55,s60
buffer_store_dwordx4 v[122:125],v15,s[36:39],s67 offen offset:0 sc0 sc1
v_xor_b32 v1,65536,v1
M 18,a[224:227],82
s_and_b32 s57,s57,131071
R 50,2,4096
v_cmp_eq_u32 vcc,s53,v7
M 18,a[232:235],86
s_nop 0
s_add_u32 s68,s68,s63
s_cmp_eq_u32 vcc_lo,-1
M 18,a[240:243],90
R 54,2,5120
M 18,a[248:251],94
buffer_store_dwordx4 v[122:125],v16,s[36:39],s67 offen offset:0 sc0 sc1
M 22,a[244:247],90
R 58,2,6144
M 22,a[252:255],94
L 5
D 0,56,2048
D 0,56,3072
s_cbranch_scc0 Lg1_N1_%=
Lg1_S1_%=:
v_mfma_f32_16x16x32_bf16 v[30:33],a[4:7],v[34:37],0
s_add_u32 s58,s58,s61
s_add_u32 m0,s59,1024
R 62,2,7168
M 30,a[12:15],38
s_add_u32 s56,s55,4096
buffer_load_dword v8,s[36:39],s58 offen offset:0 sc1 lds
v_mfma_f32_16x16x32_bf16 v[26:29],a[0:3],v[34:37],0
s_mov_b32 m0,s57
R 66,2,8192
v_fma_f32 v18,-v98,v106,v18
M 26,a[8:11],38
L 5
)"
R"(buffer_load_dwordx4 v[102:105],v11,s[36:39],s73 offen offset:0 sc1
M 26,a[16:19],42
s_mov_b32 s67,s65
R 70,2,9216
v_fma_f32 v19,-v98,v107,v19
M 26,a[24:27],46
s_add_u32 s73,s73,s64
v_fma_f32 v20,-v98,v108,v20
v_fma_f32 v21,-v98,v109,v21
M 30,a[20:23],42
s_add_u32 s54,s54,s46
R 74,2,10240
v_mul_f32 v114,v18,v18
M 30,a[28:31],46
L 5
D 0,55,0
M 30,a[36:39],50
s_add_u32 s65,s65,s62
R 78,2,11264
v_mul_f32 v115,v19,v19
M 30,a[44:47],54
v_mul_f32 v116,v20,v20
v_mul_f32 v117,v21,v21
M 26,a[32:35],50
R 82,2,12288
v_fma_f32 v114,v100,v114,v99
M 26,a[40:43],54
L 5
v_fma_f32 v115,v100,v115,v99
v_fma_f32 v116,v100,v116,v99
M 26,a[48:51],58
R 86,2,13312
v_fma_f32 v117,v100,v117,v99
M 26,a[56:59],62
D 0,55,1024
M 30,a[52:55],58
R 90,2,14336
v_mul_f32 v114,v18,v114
M 30,a[60:63],62
L 5
v_exp_f32 v114,v114
M 30,a[68:71],66
R 94,2,15360
v_mul_f32 v115,v19,v115
M 30,a[76:79],70
v_exp_f32 v115,v115
M 26,a[64:67],66
R 34,2,16384
v_mul_f32 v116,v20,v116
M 26,a[72:75],70
L 5
v_exp_f32 v116,v116
M 26,a[80:83],74
R 38,2,17408
v_mul_f32 v117,v21,v117
M 26,a[88:91],78
D 0,55,2048
M 30,a[84:87],74
R 42,2,18432
v_fma_f32 v114,v114,v101,v101
M 30,a[92:95],78
L 5
v_exp_f32 v117,v117
M 30,a[100:103],82
R 46,2,19456
v_fma_f32 v115,v115,v101,v101
M 30,a[108:111],86
v_rcp_f32 v114,v114
M 26,a[96:99],82
R 50,2,20480
v_fma_f32 v116,v116,v101,v101
M 26,a[104:107],86
L 5
D 0,55,3072
M 26,a[112:115],90
s_add_u32 m0,s57,4096
R 54,2,21504
v_fma_f32 v117,v117,v101,v101
M 26,a[120:123],94
s_add_u32 s57,s57,32768
v_rcp_f32 v115,v115
M 30,a[116:119],90
s_add_u32 s55,s55,s60
R 58,2,22528
v_mul_f32 v18,v18,v114
M 30,a[124:127],94
L 5
v_rcp_f32 v116,v116
M 30,a[132:135],34
s_and_b32 s57,s57,131071
R 62,2,23552
v_mul_f32 v19,v19,v115
M 30,a[140:143],38
v_rcp_f32 v117,v117
M 26,a[128:131],34
R 66,2,24576
v_mul_f32 v20,v20,v116
M 26,a[136:139],38
L 5
D 0,56,0
M 26,a[144:147],42
R 70,2,25600
v_mul_f32 v21,v21,v117
M 26,a[152:155],46
v_cvt_pk_bf16_f32 v122,v18,v19
v_cvt_pk_bf16_f32 v123,v20,v21
M 30,a[148:151],42
R 74,2,26624
v_fma_f32 v22,-v98,v110,v22
M 30,a[156:159],46
L 5
v_fma_f32 v23,-v98,v111,v23
v_fma_f32 v24,-v98,v112,v24
v_fma_f32 v25,-v98,v113,v25
M 30,a[164:167],50
R 78,2,27648
v_mul_f32 v118,v22,v22
M 30,a[172:175],54
D 0,56,1024
M 26,a[160:163],50
R 82,2,28672
v_mul_f32 v119,v23,v23
M 26,a[168:171],54
L 5
v_mul_f32 v120,v24,v24
v_mul_f32 v121,v25,v25
M 26,a[176:179],58
R 86,2,29696
v_fma_f32 v118,v100,v118,v99
M 26,a[184:187],62
v_fma_f32 v119,v100,v119,v99
v_fma_f32 v120,v100,v120,v99
M 30,a[180:183],58
R 90,2,30720
v_fma_f32 v121,v100,v121,v99
M 30,a[188:191],62
L 5
v_mul_f32 v118,v22,v118
v_mul_f32 v119,v23,v119
M 30,a[196:199],66
s_waitcnt vmcnt(6)
R 94,2,31744
v_mul_f32 v120,v24,v120
M 30,a[204:207],70
s_barrier
D 0,56,2048
M 26,a[192:195],66
R 34,1,0
ds_read_b32 v7,v6 offset:1024
M 26,a[200:203],70
L 6
buffer_store_dword v4,v3,s[36:39],s70 offen offset:0 sc0 sc1
M 26,a[208:211],74
s_mov_b32 s70,s69
R 38,1,1024
v_mul_f32 v121,v25,v121
M 26,a[216:219],78
s_mov_b32 s69,s68
v_exp_f32 v118,v118
M 30,a[212:215],74
s_add_u32 s68,s68,s63
R 42,1,2048
v_exp_f32 v119,v119
v_exp_f32 v120,v120
v_exp_f32 v121,v121
M 30,a[220:223],78
L 6
v_fma_f32 v118,v118,v101,v101
v_fma_f32 v119,v119,v101,v101
v_fma_f32 v120,v120,v101,v101
v_fma_f32 v121,v121,v101,v101
v_rcp_f32 v118,v118
v_rcp_f32 v119,v119
v_rcp_f32 v120,v120
v_rcp_f32 v121,v121
M 30,a[228:231],82
L 2
R 46,1,3072
v_mul_f32 v22,v22,v118
v_mul_f32 v23,v23,v119
v_mul_f32 v24,v24,v120
v_mul_f32 v25,v25,v121
v_cvt_pk_bf16_f32 v124,v22,v23
v_cvt_pk_bf16_f32 v125,v24,v25
M 30,a[236:239],86
buffer_store_dwordx4 v[122:125],v15,s[36:39],s66 offen offset:0 sc0 sc1
v_xor_b32 v2,65536,v2
M 26,a[224:227],82
R 50,1,4096
v_cmp_eq_u32 vcc,s53,v7
M 26,a[232:235],86
s_nop 0
s_cmp_eq_u32 vcc_lo,-1
M 26,a[240:243],90
R 54,1,5120
M 26,a[248:251],94
buffer_store_dwordx4 v[122:125],v16,s[36:39],s66 offen offset:0 sc0 sc1
M 30,a[244:247],90
R 58,1,6144
M 30,a[252:255],94
L 5
D 0,56,3072
s_cbranch_scc0 Lg1_N0_%=
Lg1_S0_%=:
v_mfma_f32_16x16x32_bf16 v[22:25],a[4:7],v[34:37],0
s_add_u32 s58,s58,s61
s_add_u32 m0,s59,0
R 62,1,7168
M 22,a[12:15],38
s_add_u32 s56,s55,4096
buffer_load_dword v8,s[36:39],s58 offen offset:0 sc1 lds
v_mfma_f32_16x16x32_bf16 v[18:21],a[0:3],v[34:37],0
s_mov_b32 m0,s57
R 66,1,8192
v_fma_f32 v26,-v102,v106,v26
M 18,a[8:11],38
L 5
buffer_load_dwordx4 v[98:101],v11,s[36:39],s73 offen offset:0 sc1
M 18,a[16:19],42
s_mov_b32 s66,s65
R 70,1,9216
v_fma_f32 v27,-v102,v107,v27
M 18,a[24:27],46
s_add_u32 s73,s73,s64
v_fma_f32 v28,-v102,v108,v28
v_fma_f32 v29,-v102,v109,v29
M 22,a[20:23],42
s_add_u32 s54,s54,s46
R 74,1,10240
v_mul_f32 v114,v26,v26
M 22,a[28:31],46
L 5
D 0,55,0
M 22,a[36:39],50
s_add_u32 s65,s65,s62
R 78,1,11264
v_mul_f32 v115,v27,v27
M 22,a[44:47],54
v_mul_f32 v116,v28,v28
v_mul_f32 v117,v29,v29
M 18,a[32:35],50
R 82,1,12288
v_fma_f32 v114,v104,v114,v103
M 18,a[40:43],54
L 5
v_fma_f32 v115,v104,v115,v103
v_fma_f32 v116,v104,v116,v103
M 18,a[48:51],58
R 86,1,13312
v_fma_f32 v117,v104,v117,v103
M 18,a[56:59],62
D 0,55,1024
M 22,a[52:55],58
R 90,1,14336
v_mul_f32 v114,v26,v114
M 22,a[60:63],62
L 5
v_exp_f32 v114,v114
M 22,a[68:71],66
R 94,1,15360
v_mul_f32 v115,v27,v115
M 22,a[76:79],70
v_exp_f32 v115,v115
M 18,a[64:67],66
R 34,1,16384
v_mul_f32 v116,v28,v116
M 18,a[72:75],70
L 5
v_exp_f32 v116,v116
M 18,a[80:83],74
R 38,1,17408
v_mul_f32 v117,v29,v117
M 18,a[88:91],78
D 0,55,2048
M 22,a[84:87],74
R 42,1,18432
v_fma_f32 v114,v114,v105,v105
M 22,a[92:95],78
L 5
v_exp_f32 v117,v117
M 22,a[100:103],82
R 46,1,19456
v_fma_f32 v115,v115,v105,v105
M 22,a[108:111],86
v_rcp_f32 v114,v114
M 18,a[96:99],82
R 50,1,20480
v_fma_f32 v116,v116,v105,v105
M 18,a[104:107],86
L 5
D 0,55,3072
M 18,a[112:115],90
s_add_u32 m0,s57,4096
R 54,1,21504
v_fma_f32 v117,v117,v105,v105
M 18,a[120:123],94
s_add_u32 s57,s57,32768
v_rcp_f32 v115,v115
M 22,a[116:119],90
s_add_u32 s55,s55,s60
R 58,1,22528
v_mul_f32 v26,v26,v114
M 22,a[124:127],94
L 5
v_rcp_f32 v116,v116
M 22,a[132:135],34
s_and_b32 s57,s57,131071
R 62,1,23552
v_mul_f32 v27,v27,v115
M 22,a[140:143],38
v_rcp_f32 v117,v117
M 18,a[128:131],34
R 66,1,24576
v_mul_f32 v28,v28,v116
M 18,a[136:139],38
L 5
D 0,56,0
M 18,a[144:147],42
R 70,1,25600
v_mul_f32 v29,v29,v117
M 18,a[152:155],46
v_cvt_pk_bf16_f32 v122,v26,v27
v_cvt_pk_bf16_f32 v123,v28,v29
M 22,a[148:151],42
R 74,1,26624
v_fma_f32 v30,-v102,v110,v30
M 22,a[156:159],46
L 5
v_fma_f32 v31,-v102,v111,v31
v_fma_f32 v32,-v102,v112,v32
v_fma_f32 v33,-v102,v113,v33
M 22,a[164:167],50
R 78,1,27648
v_mul_f32 v118,v30,v30
M 22,a[172:175],54
D 0,56,1024
M 18,a[160:163],50
R 82,1,28672
v_mul_f32 v119,v31,v31
M 18,a[168:171],54
L 5
v_mul_f32 v120,v32,v32
v_mul_f32 v121,v33,v33
M 18,a[176:179],58
R 86,1,29696
v_fma_f32 v118,v104,v118,v103
M 18,a[184:187],62
v_fma_f32 v119,v104,v119,v103
v_fma_f32 v120,v104,v120,v103
M 22,a[180:183],58
R 90,1,30720
v_fma_f32 v121,v104,v121,v103
M 22,a[188:191],62
L 5
v_mul_f32 v118,v30,v118
v_mul_f32 v119,v31,v119
M 22,a[196:199],66
s_waitcnt vmcnt(6)
R 94,1,31744
v_mul_f32 v120,v32,v120
M 22,a[204:207],70
s_barrier
D 0,56,2048
M 18,a[192:195],66
R 34,2,0
ds_read_b32 v7,v6 offset:0
M 18,a[200:203],70
L 6
buffer_store_dword v4,v3,s[36:39],s70 offen offset:0 sc0 sc1
M 18,a[208:211],74
s_mov_b32 s70,s69
R 38,2,1024
v_mul_f32 v121,v33,v121
M 18,a[216:219],78
s_mov_b32 s69,s68
v_exp_f32 v118,v118
M 22,a[212:215],74
s_add_u32 s68,s68,s63
R 42,2,2048
v_exp_f32 v119,v119
v_exp_f32 v120,v120
v_exp_f32 v121,v121
M 22,a[220:223],78
L 6
v_fma_f32 v118,v118,v105,v105
v_fma_f32 v119,v119,v105,v105
v_fma_f32 v120,v120,v105,v105
v_fma_f32 v121,v121,v105,v105
v_rcp_f32 v118,v118
v_rcp_f32 v119,v119
v_rcp_f32 v120,v120
v_rcp_f32 v121,v121
M 22,a[228:231],82
L 2
R 46,2,3072
v_mul_f32 v30,v30,v118
v_mul_f32 v31,v31,v119
v_mul_f32 v32,v32,v120
v_mul_f32 v33,v33,v121
v_cvt_pk_bf16_f32 v124,v30,v31
v_cvt_pk_bf16_f32 v125,v32,v33
M 22,a[236:239],86
buffer_store_dwordx4 v[122:125],v15,s[36:39],s67 offen offset:0 sc0 sc1
v_xor_b32 v1,65536,v1
M 18,a[224:227],82
R 50,2,4096
v_cmp_eq_u32 vcc,s53,v7
M 18,a[232:235],86
s_nop 0
s_cmp_eq_u32 vcc_lo,-1
M 18,a[240:243],90
R 54,2,5120
M 18,a[248:251],94
buffer_store_dwordx4 v[122:125],v16,s[36:39],s67 offen offset:0 sc0 sc1
M 22,a[244:247],90
R 58,2,6144
M 22,a[252:255],94
L 5
D 0,56,3072
s_cbranch_scc1 Lg1_S1_%=
Lg1_N1_%=:
v_mfma_f32_16x16x32_bf16 v[30:33],a[4:7],v[34:37],0
s_add_u32 m0,s59,1024
s_mov_b32 s67,s65
R 62,2,7168
M 30,a[12:15],38
s_add_u32 s65,s65,s62
buffer_load_dword v5,s[36:39],s58 offen offset:0 sc1 lds
v_mfma_f32_16x16x32_bf16 v[26:29],a[0:3],v[34:37],0
R 66,2,8192
v_fma_f32 v18,-v98,v106,v18
M 26,a[8:11],38
L 5
buffer_load_dwordx4 v[102:105],v11,s[36:39],s73 offen offset:0 sc1
M 26,a[16:19],42
s_add_u32 s73,s73,s64
R 70,2,9216
v_fma_f32 v19,-v98,v107,v19
M 26,a[24:27],46
v_fma_f32 v20,-v98,v108,v20
v_fma_f32 v21,-v98,v109,v21
M 30,a[20:23],42
R 74,2,10240
v_mul_f32 v114,v18,v18
M 30,a[28:31],46
L 5
v_mul_f32 v115,v19,v19
v_mul_f32 v116,v20,v20
M 30,a[36:39],50
R 78,2,11264
v_mul_f32 v117,v21,v21
M 30,a[44:47],54
v_fma_f32 v114,v100,v114,v99
v_fma_f32 v115,v100,v115,v99
M 26,a[32:35],50
R 82,2,12288
v_fma_f32 v116,v100,v116,v99
M 26,a[40:43],54
L 5
v_fma_f32 v117,v100,v117,v99
v_mul_f32 v114,v18,v114
M 26,a[48:51],58
R 86,2,13312
v_mul_f32 v115,v19,v115
M 26,a[56:59],62
v_exp_f32 v114,v114
M 30,a[52:55],58
R 90,2,14336
v_mul_f32 v116,v20,v116
M 30,a[60:63],62
L 5
v_exp_f32 v115,v115
M 30,a[68:71],66
R 94,2,15360
v_mul_f32 v117,v21,v117
M 30,a[76:79],70
v_exp_f32 v116,v116
M 26,a[64:67],66
R 34,2,16384
v_fma_f32 v114,v114,v101,v101
M 26,a[72:75],70
L 5
v_exp_f32 v117,v117
M 26,a[80:83],74
R 38,2,17408
v_fma_f32 v115,v115,v101,v101
M 26,a[88:91],78
v_rcp_f32 v114,v114
M 30,a[84:87],74
R 42,2,18432
v_fma_f32 v116,v116,v101,v101
M 30,a[92:95],78
L 5
v_rcp_f32 v115,v115
M 30,a[100:103],82
R 46,2,19456
v_fma_f32 v117,v117,v101,v101
M 30,a[108:111],86
v_rcp_f32 v116,v116
M 26,a[96:99],82
)"
R"(R 50,2,20480
v_mul_f32 v18,v18,v114
M 26,a[104:107],86
L 5
v_rcp_f32 v117,v117
M 26,a[112:115],90
R 54,2,21504
v_mul_f32 v19,v19,v115
M 26,a[120:123],94
v_mul_f32 v20,v20,v116
v_mul_f32 v21,v21,v117
M 30,a[116:119],90
R 58,2,22528
v_cvt_pk_bf16_f32 v122,v18,v19
M 30,a[124:127],94
L 5
v_cvt_pk_bf16_f32 v123,v20,v21
v_fma_f32 v22,-v98,v110,v22
M 30,a[132:135],34
R 62,2,23552
v_fma_f32 v23,-v98,v111,v23
M 30,a[140:143],38
v_fma_f32 v24,-v98,v112,v24
v_fma_f32 v25,-v98,v113,v25
M 26,a[128:131],34
R 66,2,24576
v_mul_f32 v118,v22,v22
M 26,a[136:139],38
L 5
v_mul_f32 v119,v23,v23
v_mul_f32 v120,v24,v24
M 26,a[144:147],42
R 70,2,25600
v_mul_f32 v121,v25,v25
M 26,a[152:155],46
v_fma_f32 v118,v100,v118,v99
v_fma_f32 v119,v100,v119,v99
M 30,a[148:151],42
R 74,2,26624
v_fma_f32 v120,v100,v120,v99
M 30,a[156:159],46
L 5
v_fma_f32 v121,v100,v121,v99
v_mul_f32 v118,v22,v118
v_mul_f32 v119,v23,v119
M 30,a[164:167],50
R 78,2,27648
v_mul_f32 v120,v24,v120
M 30,a[172:175],54
v_exp_f32 v118,v118
M 26,a[160:163],50
R 82,2,28672
v_mul_f32 v121,v25,v121
M 26,a[168:171],54
L 5
v_exp_f32 v119,v119
M 26,a[176:179],58
R 86,2,29696
v_fma_f32 v118,v118,v101,v101
M 26,a[184:187],62
v_exp_f32 v120,v120
M 30,a[180:183],58
R 90,2,30720
v_fma_f32 v119,v119,v101,v101
M 30,a[188:191],62
L 5
v_exp_f32 v121,v121
M 30,a[196:199],66
s_waitcnt vmcnt(0)
R 94,2,31744
v_fma_f32 v120,v120,v101,v101
M 30,a[204:207],70
s_barrier
buffer_store_dword v4,v3,s[36:39],s70 offen offset:0 sc0 sc1
M 26,a[192:195],66
s_mov_b32 s70,s69
R 34,1,0
ds_read_b32 v7,v6 offset:1024
M 26,a[200:203],70
L 6
v_rcp_f32 v118,v118
M 26,a[208:211],74
s_mov_b32 s69,s68
R 38,1,1024
v_fma_f32 v121,v121,v101,v101
M 26,a[216:219],78
s_add_u32 s68,s68,s63
v_rcp_f32 v119,v119
M 30,a[212:215],74
R 42,1,2048
v_mul_f32 v22,v22,v118
M 30,a[220:223],78
L 6
v_rcp_f32 v120,v120
v_rcp_f32 v121,v121
M 30,a[228:231],82
L 2
R 46,1,3072
v_mul_f32 v23,v23,v119
v_mul_f32 v24,v24,v120
v_mul_f32 v25,v25,v121
v_cvt_pk_bf16_f32 v124,v22,v23
v_cvt_pk_bf16_f32 v125,v24,v25
M 30,a[236:239],86
buffer_store_dwordx4 v[122:125],v15,s[36:39],s66 offen offset:0 sc0 sc1
v_xor_b32 v2,65536,v2
M 26,a[224:227],82
R 50,1,4096
v_cmp_eq_u32 vcc,s53,v7
M 26,a[232:235],86
s_nop 0
s_cmp_eq_u64 vcc,-1
M 26,a[240:243],90
R 54,1,5120
M 26,a[248:251],94
buffer_store_dwordx4 v[122:125],v16,s[36:39],s66 offen offset:0 sc0 sc1
M 30,a[244:247],90
R 58,1,6144
M 30,a[252:255],94
L 5
s_cbranch_scc1 Lg1_L20_%=
s_cmp_eq_u32 vcc_lo,-1
s_cbranch_scc1 Lg1_L10_%=
Lg1_X0_%=:
v_mfma_f32_16x16x32_bf16 v[22:25],a[4:7],v[34:37],0
s_add_u32 m0,s59,0
s_mov_b32 s66,s65
R 62,1,7168
M 22,a[12:15],38
s_add_u32 s65,s65,s62
buffer_load_dword v5,s[36:39],s58 offen offset:0 sc1 lds
v_mfma_f32_16x16x32_bf16 v[18:21],a[0:3],v[34:37],0
R 66,1,8192
v_fma_f32 v26,-v102,v106,v26
M 18,a[8:11],38
L 5
buffer_load_dwordx4 v[98:101],v11,s[36:39],s73 offen offset:0 sc1
M 18,a[16:19],42
s_add_u32 s73,s73,s64
R 70,1,9216
v_fma_f32 v27,-v102,v107,v27
M 18,a[24:27],46
v_fma_f32 v28,-v102,v108,v28
v_fma_f32 v29,-v102,v109,v29
M 22,a[20:23],42
R 74,1,10240
v_mul_f32 v114,v26,v26
M 22,a[28:31],46
L 5
v_mul_f32 v115,v27,v27
v_mul_f32 v116,v28,v28
M 22,a[36:39],50
R 78,1,11264
v_mul_f32 v117,v29,v29
M 22,a[44:47],54
v_fma_f32 v114,v104,v114,v103
v_fma_f32 v115,v104,v115,v103
M 18,a[32:35],50
R 82,1,12288
v_fma_f32 v116,v104,v116,v103
M 18,a[40:43],54
L 5
v_fma_f32 v117,v104,v117,v103
v_mul_f32 v114,v26,v114
M 18,a[48:51],58
R 86,1,13312
v_mul_f32 v115,v27,v115
M 18,a[56:59],62
v_exp_f32 v114,v114
M 22,a[52:55],58
R 90,1,14336
v_mul_f32 v116,v28,v116
M 22,a[60:63],62
L 5
v_exp_f32 v115,v115
M 22,a[68:71],66
R 94,1,15360
v_mul_f32 v117,v29,v117
M 22,a[76:79],70
v_exp_f32 v116,v116
M 18,a[64:67],66
R 34,1,16384
v_fma_f32 v114,v114,v105,v105
M 18,a[72:75],70
L 5
v_exp_f32 v117,v117
M 18,a[80:83],74
R 38,1,17408
v_fma_f32 v115,v115,v105,v105
M 18,a[88:91],78
v_rcp_f32 v114,v114
M 22,a[84:87],74
R 42,1,18432
v_fma_f32 v116,v116,v105,v105
M 22,a[92:95],78
L 5
v_rcp_f32 v115,v115
M 22,a[100:103],82
R 46,1,19456
v_fma_f32 v117,v117,v105,v105
M 22,a[108:111],86
v_rcp_f32 v116,v116
M 18,a[96:99],82
R 50,1,20480
v_mul_f32 v26,v26,v114
M 18,a[104:107],86
L 5
v_rcp_f32 v117,v117
M 18,a[112:115],90
R 54,1,21504
v_mul_f32 v27,v27,v115
M 18,a[120:123],94
v_mul_f32 v28,v28,v116
v_mul_f32 v29,v29,v117
M 22,a[116:119],90
R 58,1,22528
v_cvt_pk_bf16_f32 v122,v26,v27
M 22,a[124:127],94
L 5
v_cvt_pk_bf16_f32 v123,v28,v29
v_fma_f32 v30,-v102,v110,v30
M 22,a[132:135],34
R 62,1,23552
v_fma_f32 v31,-v102,v111,v31
M 22,a[140:143],38
v_fma_f32 v32,-v102,v112,v32
v_fma_f32 v33,-v102,v113,v33
M 18,a[128:131],34
R 66,1,24576
v_mul_f32 v118,v30,v30
M 18,a[136:139],38
L 5
v_mul_f32 v119,v31,v31
v_mul_f32 v120,v32,v32
M 18,a[144:147],42
R 70,1,25600
v_mul_f32 v121,v33,v33
M 18,a[152:155],46
v_fma_f32 v118,v104,v118,v103
v_fma_f32 v119,v104,v119,v103
M 22,a[148:151],42
R 74,1,26624
v_fma_f32 v120,v104,v120,v103
M 22,a[156:159],46
L 5
v_fma_f32 v121,v104,v121,v103
v_mul_f32 v118,v30,v118
M 22,a[164:167],50
R 78,1,27648
v_mul_f32 v119,v31,v119
M 22,a[172:175],54
v_exp_f32 v118,v118
M 18,a[160:163],50
R 82,1,28672
v_mul_f32 v120,v32,v120
M 18,a[168:171],54
L 5
v_exp_f32 v119,v119
M 18,a[176:179],58
R 86,1,29696
v_mul_f32 v121,v33,v121
M 18,a[184:187],62
v_exp_f32 v120,v120
M 22,a[180:183],58
R 90,1,30720
v_fma_f32 v118,v118,v105,v105
M 22,a[188:191],62
L 5
v_exp_f32 v121,v121
M 22,a[196:199],66
s_waitcnt vmcnt(0)
R 94,1,31744
v_fma_f32 v119,v119,v105,v105
M 22,a[204:207],70
s_barrier
buffer_store_dword v4,v3,s[36:39],s70 offen offset:0 sc0 sc1
M 18,a[192:195],66
s_mov_b32 s70,s69
ds_read_b32 v7,v6 offset:0
v_fma_f32 v120,v120,v105,v105
M 18,a[200:203],70
L 5
v_rcp_f32 v118,v118
M 18,a[208:211],74
s_mov_b32 s69,s68
v_fma_f32 v121,v121,v105,v105
v_mul_f32 v30,v30,v118
M 18,a[216:219],78
s_add_u32 s68,s68,s63
v_rcp_f32 v119,v119
M 22,a[212:215],74
v_rcp_f32 v120,v120
M 22,a[220:223],78
L 3
v_rcp_f32 v121,v121
M 22,a[228:231],82
L 0
v_mul_f32 v31,v31,v119
v_mul_f32 v32,v32,v120
v_mul_f32 v33,v33,v121
v_cvt_pk_bf16_f32 v124,v30,v31
v_cvt_pk_bf16_f32 v125,v32,v33
M 22,a[236:239],86
buffer_store_dwordx4 v[122:125],v15,s[36:39],s67 offen offset:0 sc0 sc1
v_xor_b32 v1,65536,v1
M 18,a[224:227],82
v_cmp_eq_u32 vcc,s53,v7
M 18,a[232:235],86
s_nop 0
M 18,a[240:243],90
buffer_store_dwordx4 v[122:125],v16,s[36:39],s67 offen offset:0 sc0 sc1
M 18,a[248:251],94
M 22,a[244:247],90
M 22,a[252:255],94
s_mov_b32 s80,1
s_cmp_eq_u32 vcc_lo,-1
s_cbranch_scc0 Lg1_xn0_%=
s_mov_b32 m0,s57
s_add_u32 s56,s55,4096
D 0,55,0
v_fma_f32 v18,-v98,v106,v18
v_fma_f32 v19,-v98,v107,v19
v_fma_f32 v20,-v98,v108,v20
v_fma_f32 v21,-v98,v109,v21
v_mul_f32 v114,v18,v18
v_mul_f32 v115,v19,v19
v_mul_f32 v116,v20,v20
D 0,55,1024
v_mul_f32 v117,v21,v21
v_fma_f32 v114,v100,v114,v99
v_fma_f32 v115,v100,v115,v99
v_fma_f32 v116,v100,v116,v99
v_fma_f32 v117,v100,v117,v99
v_mul_f32 v114,v18,v114
v_mul_f32 v115,v19,v115
D 0,55,2048
v_mul_f32 v116,v20,v116
v_mul_f32 v117,v21,v117
v_exp_f32 v114,v114
v_exp_f32 v115,v115
v_exp_f32 v116,v116
v_exp_f32 v117,v117
v_fma_f32 v114,v114,v101,v101
D 0,55,3072
v_fma_f32 v115,v115,v101,v101
v_fma_f32 v116,v116,v101,v101
v_fma_f32 v117,v117,v101,v101
v_rcp_f32 v114,v114
v_rcp_f32 v115,v115
v_rcp_f32 v116,v116
v_rcp_f32 v117,v117
s_add_u32 m0,s57,4096
s_nop 0
D 0,56,0
v_mul_f32 v18,v18,v114
v_mul_f32 v19,v19,v115
v_mul_f32 v20,v20,v116
v_mul_f32 v21,v21,v117
v_cvt_pk_bf16_f32 v122,v18,v19
v_cvt_pk_bf16_f32 v123,v20,v21
v_fma_f32 v22,-v98,v110,v22
D 0,56,1024
v_fma_f32 v23,-v98,v111,v23
v_fma_f32 v24,-v98,v112,v24
v_fma_f32 v25,-v98,v113,v25
v_mul_f32 v118,v22,v22
v_mul_f32 v119,v23,v23
v_mul_f32 v120,v24,v24
v_mul_f32 v121,v25,v25
D 0,56,2048
v_fma_f32 v118,v100,v118,v99
v_fma_f32 v119,v100,v119,v99
v_fma_f32 v120,v100,v120,v99
v_fma_f32 v121,v100,v121,v99
v_mul_f32 v118,v22,v118
v_mul_f32 v119,v23,v119
v_mul_f32 v120,v24,v120
D 0,56,3072
v_mul_f32 v121,v25,v121
v_exp_f32 v118,v118
v_exp_f32 v119,v119
v_exp_f32 v120,v120
v_exp_f32 v121,v121
v_fma_f32 v118,v118,v101,v101
v_fma_f32 v119,v119,v101,v101
s_add_u32 s55,s55,s60
s_add_u32 s57,s57,32768
s_and_b32 s57,s57,131071
s_add_u32 s54,s54,s46
v_fma_f32 v120,v120,v101,v101
v_fma_f32 v121,v121,v101,v101
v_rcp_f32 v118,v118
v_rcp_f32 v119,v119
v_rcp_f32 v120,v120
v_rcp_f32 v121,v121
v_mul_f32 v22,v22,v118
v_mul_f32 v23,v23,v119
v_mul_f32 v24,v24,v120
v_mul_f32 v25,v25,v121
v_cvt_pk_bf16_f32 v124,v22,v23
v_cvt_pk_bf16_f32 v125,v24,v25
buffer_store_dwordx4 v[122:125],v15,s[36:39],s66 offen offset:0 sc0 sc1
buffer_store_dwordx4 v[122:125],v16,s[36:39],s66 offen offset:0 sc0 sc1
s_mov_b32 s72,s69
s_mov_b32 s69,0x7fff0000
s_mov_b32 s66,0x7fff0000
s_mov_b32 s76,0x7fff0000
s_branch Lg1_gox_%=
Lg1_xn0_%=:
s_cmp_ge_u32 s54,256
s_cbranch_scc1 Lg1_fin0_%=
s_add_u32 m0,s59,4096
s_nop 0
buffer_load_dword v5,s[36:39],s58 offen offset:0 sc1 lds
buffer_store_dword v4,v3,s[36:39],s71 offen offset:0 sc0 sc1
s_add_u32 m0,s59,5120
s_nop 0
buffer_load_dword v5,s[36:39],s58 offen offset:0 sc1 lds
buffer_store_dword v4,v3,s[36:39],s71 offen offset:0 sc0 sc1
s_mov_b32 s83,0
v_fma_f32 v18,-v98,v106,v18
v_fma_f32 v19,-v98,v107,v19
v_fma_f32 v20,-v98,v108,v20
v_fma_f32 v21,-v98,v109,v21
v_mul_f32 v114,v18,v18
v_mul_f32 v115,v19,v19
v_mul_f32 v116,v20,v20
v_mul_f32 v117,v21,v21
v_fma_f32 v114,v100,v114,v99
v_fma_f32 v115,v100,v115,v99
v_fma_f32 v116,v100,v116,v99
v_fma_f32 v117,v100,v117,v99
v_mul_f32 v114,v18,v114
v_mul_f32 v115,v19,v115
v_mul_f32 v116,v20,v116
v_mul_f32 v117,v21,v117
v_exp_f32 v114,v114
v_exp_f32 v115,v115
v_exp_f32 v116,v116
v_exp_f32 v117,v117
v_fma_f32 v114,v114,v101,v101
v_fma_f32 v115,v115,v101,v101
v_fma_f32 v116,v116,v101,v101
v_fma_f32 v117,v117,v101,v101
v_rcp_f32 v114,v114
v_rcp_f32 v115,v115
v_rcp_f32 v116,v116
v_rcp_f32 v117,v117
v_mul_f32 v18,v18,v114
v_mul_f32 v19,v19,v115
v_mul_f32 v20,v20,v116
v_mul_f32 v21,v21,v117
v_cvt_pk_bf16_f32 v122,v18,v19
v_cvt_pk_bf16_f32 v123,v20,v21
v_fma_f32 v22,-v98,v110,v22
v_fma_f32 v23,-v98,v111,v23
v_fma_f32 v24,-v98,v112,v24
v_fma_f32 v25,-v98,v113,v25
v_mul_f32 v118,v22,v22
v_mul_f32 v119,v23,v23
v_mul_f32 v120,v24,v24
v_mul_f32 v121,v25,v25
v_fma_f32 v118,v100,v118,v99
v_fma_f32 v119,v100,v119,v99
v_fma_f32 v120,v100,v120,v99
v_fma_f32 v121,v100,v121,v99
v_mul_f32 v118,v22,v118
v_mul_f32 v119,v23,v119
v_mul_f32 v120,v24,v120
v_mul_f32 v121,v25,v121
v_exp_f32 v118,v118
v_exp_f32 v119,v119
v_exp_f32 v120,v120
v_exp_f32 v121,v121
v_fma_f32 v118,v118,v101,v101
v_fma_f32 v119,v119,v101,v101
v_fma_f32 v120,v120,v101,v101
v_fma_f32 v121,v121,v101,v101
v_rcp_f32 v118,v118
v_rcp_f32 v119,v119
v_rcp_f32 v120,v120
v_rcp_f32 v121,v121
v_mul_f32 v22,v22,v118
v_mul_f32 v23,v23,v119
v_mul_f32 v24,v24,v120
v_mul_f32 v25,v25,v121
v_cvt_pk_bf16_f32 v124,v22,v23
v_cvt_pk_bf16_f32 v125,v24,v25
buffer_store_dwordx4 v[122:125],v15,s[36:39],s66 offen offset:0 sc0 sc1
buffer_store_dwordx4 v[122:125],v16,s[36:39],s66 offen offset:0 sc0 sc1
s_mov_b32 s72,s69
s_mov_b32 s69,0x7fff0000
s_mov_b32 s66,0x7fff0000
s_mov_b32 s76,0x7fff0000
s_branch Lg1_poll_%=
Lg1_fin0_%=:
v_fma_f32 v18,-v98,v106,v18
v_fma_f32 v19,-v98,v107,v19
v_fma_f32 v20,-v98,v108,v20
v_fma_f32 v21,-v98,v109,v21
)"
R"(v_mul_f32 v114,v18,v18
v_mul_f32 v115,v19,v19
v_mul_f32 v116,v20,v20
v_mul_f32 v117,v21,v21
v_fma_f32 v114,v100,v114,v99
v_fma_f32 v115,v100,v115,v99
v_fma_f32 v116,v100,v116,v99
v_fma_f32 v117,v100,v117,v99
v_mul_f32 v114,v18,v114
v_mul_f32 v115,v19,v115
v_mul_f32 v116,v20,v116
v_mul_f32 v117,v21,v117
v_exp_f32 v114,v114
v_exp_f32 v115,v115
v_exp_f32 v116,v116
v_exp_f32 v117,v117
v_fma_f32 v114,v114,v101,v101
v_fma_f32 v115,v115,v101,v101
v_fma_f32 v116,v116,v101,v101
v_fma_f32 v117,v117,v101,v101
v_rcp_f32 v114,v114
v_rcp_f32 v115,v115
v_rcp_f32 v116,v116
v_rcp_f32 v117,v117
v_mul_f32 v18,v18,v114
v_mul_f32 v19,v19,v115
v_mul_f32 v20,v20,v116
v_mul_f32 v21,v21,v117
v_cvt_pk_bf16_f32 v122,v18,v19
v_cvt_pk_bf16_f32 v123,v20,v21
v_fma_f32 v22,-v98,v110,v22
v_fma_f32 v23,-v98,v111,v23
v_fma_f32 v24,-v98,v112,v24
v_fma_f32 v25,-v98,v113,v25
v_mul_f32 v118,v22,v22
v_mul_f32 v119,v23,v23
v_mul_f32 v120,v24,v24
v_mul_f32 v121,v25,v25
v_fma_f32 v118,v100,v118,v99
v_fma_f32 v119,v100,v119,v99
v_fma_f32 v120,v100,v120,v99
v_fma_f32 v121,v100,v121,v99
v_mul_f32 v118,v22,v118
v_mul_f32 v119,v23,v119
v_mul_f32 v120,v24,v120
v_mul_f32 v121,v25,v121
v_exp_f32 v118,v118
v_exp_f32 v119,v119
v_exp_f32 v120,v120
v_exp_f32 v121,v121
v_fma_f32 v118,v118,v101,v101
v_fma_f32 v119,v119,v101,v101
v_fma_f32 v120,v120,v101,v101
v_fma_f32 v121,v121,v101,v101
v_rcp_f32 v118,v118
v_rcp_f32 v119,v119
v_rcp_f32 v120,v120
v_rcp_f32 v121,v121
v_mul_f32 v22,v22,v118
v_mul_f32 v23,v23,v119
v_mul_f32 v24,v24,v120
v_mul_f32 v25,v25,v121
v_cvt_pk_bf16_f32 v124,v22,v23
v_cvt_pk_bf16_f32 v125,v24,v25
buffer_store_dwordx4 v[122:125],v15,s[36:39],s66 offen offset:0 sc0 sc1
buffer_store_dwordx4 v[122:125],v16,s[36:39],s66 offen offset:0 sc0 sc1
s_mov_b32 s72,s69
s_mov_b32 s69,0x7fff0000
s_mov_b32 s66,0x7fff0000
s_mov_b32 s76,0x7fff0000
s_branch Lg1_done_%=
Lg1_L21_%=:
v_mfma_f32_16x16x32_bf16 v[30:33],a[4:7],v[34:37],0
s_add_u32 s58,s58,s61
s_add_u32 s58,s58,s61
R 62,2,7168
M 30,a[12:15],38
s_add_u32 m0,s59,1024
buffer_load_dwordx4 v[102:105],v11,s[36:39],s73 offen offset:0 sc1
v_mfma_f32_16x16x32_bf16 v[26:29],a[0:3],v[34:37],0
s_add_u32 s56,s55,4096
R 66,2,8192
v_fma_f32 v18,-v98,v106,v18
M 26,a[8:11],38
L 5
buffer_load_dword v8,s[36:39],s58 offen offset:0 sc1 lds
M 26,a[16:19],42
s_mov_b32 m0,s57
R 70,2,9216
v_fma_f32 v19,-v98,v107,v19
M 26,a[24:27],46
s_add_u32 s54,s54,s46
v_fma_f32 v20,-v98,v108,v20
v_fma_f32 v21,-v98,v109,v21
M 30,a[20:23],42
s_mov_b32 s67,s65
R 74,2,10240
v_mul_f32 v114,v18,v18
M 30,a[28:31],46
L 5
D 0,55,0
M 30,a[36:39],50
s_add_u32 s73,s73,s64
R 78,2,11264
v_mul_f32 v115,v19,v19
M 30,a[44:47],54
s_add_u32 s54,s54,s46
v_mul_f32 v116,v20,v20
v_mul_f32 v117,v21,v21
M 26,a[32:35],50
s_add_u32 s65,s65,s62
R 82,2,12288
v_fma_f32 v114,v100,v114,v99
M 26,a[40:43],54
L 5
D 0,55,1024
M 26,a[48:51],58
R 86,2,13312
v_fma_f32 v115,v100,v115,v99
M 26,a[56:59],62
v_fma_f32 v116,v100,v116,v99
v_fma_f32 v117,v100,v117,v99
M 30,a[52:55],58
R 90,2,14336
v_mul_f32 v114,v18,v114
M 30,a[60:63],62
L 5
D 0,55,2048
M 30,a[68:71],66
R 94,2,15360
v_mul_f32 v115,v19,v115
M 30,a[76:79],70
v_exp_f32 v114,v114
M 26,a[64:67],66
R 34,2,16384
v_mul_f32 v116,v20,v116
M 26,a[72:75],70
L 5
D 0,55,3072
M 26,a[80:83],74
s_add_u32 m0,s57,4096
R 38,2,17408
v_mul_f32 v117,v21,v117
M 26,a[88:91],78
s_add_u32 s57,s57,32768
v_exp_f32 v115,v115
M 30,a[84:87],74
s_and_b32 s57,s57,131071
R 42,2,18432
v_fma_f32 v114,v114,v101,v101
M 30,a[92:95],78
L 5
D 0,56,0
M 30,a[100:103],82
s_add_u32 s55,s55,s60
R 46,2,19456
v_fma_f32 v115,v115,v101,v101
M 30,a[108:111],86
v_exp_f32 v116,v116
M 26,a[96:99],82
R 50,2,20480
v_fma_f32 v116,v116,v101,v101
M 26,a[104:107],86
L 5
D 0,56,1024
M 26,a[112:115],90
R 54,2,21504
v_fma_f32 v22,-v98,v110,v22
M 26,a[120:123],94
v_exp_f32 v117,v117
M 30,a[116:119],90
R 58,2,22528
v_fma_f32 v117,v117,v101,v101
M 30,a[124:127],94
L 5
D 0,56,2048
M 30,a[132:135],34
R 62,2,23552
v_fma_f32 v23,-v98,v111,v23
M 30,a[140:143],38
v_rcp_f32 v114,v114
M 26,a[128:131],34
R 66,2,24576
v_mul_f32 v18,v18,v114
M 26,a[136:139],38
L 5
D 0,56,3072
M 26,a[144:147],42
s_mov_b32 m0,s57
R 70,2,25600
v_fma_f32 v24,-v98,v112,v24
M 26,a[152:155],46
s_add_u32 s56,s55,4096
v_rcp_f32 v115,v115
M 30,a[148:151],42
R 74,2,26624
v_mul_f32 v19,v19,v115
M 30,a[156:159],46
L 5
D 0,55,0
M 30,a[164:167],50
R 78,2,27648
v_cvt_pk_bf16_f32 v122,v18,v19
M 30,a[172:175],54
D 0,55,1024
M 26,a[160:163],50
R 82,2,28672
v_fma_f32 v25,-v98,v113,v25
M 26,a[168:171],54
L 5
v_rcp_f32 v116,v116
M 26,a[176:179],58
R 86,2,29696
v_mul_f32 v20,v20,v116
M 26,a[184:187],62
D 0,55,2048
M 30,a[180:183],58
R 90,2,30720
v_mul_f32 v118,v22,v22
M 30,a[188:191],62
L 5
D 0,55,3072
M 30,a[196:199],66
s_waitcnt vmcnt(4)
R 94,2,31744
v_mul_f32 v119,v23,v23
M 30,a[204:207],70
s_barrier
buffer_store_dword v4,v3,s[36:39],s70 offen offset:0 sc0 sc1
M 26,a[192:195],66
s_add_u32 m0,s57,4096
R 34,1,0
ds_read_b32 v7,v6 offset:1024
M 26,a[200:203],70
L 6
D 0,56,0
M 26,a[208:211],74
s_mov_b32 s70,s69
R 38,1,1024
v_mul_f32 v120,v24,v24
M 26,a[216:219],78
s_add_u32 s57,s57,32768
D 0,56,1024
M 30,a[212:215],74
s_mov_b32 s69,s68
R 42,1,2048
v_mul_f32 v121,v25,v25
v_fma_f32 v118,v100,v118,v99
v_fma_f32 v119,v100,v119,v99
v_fma_f32 v120,v100,v120,v99
v_fma_f32 v121,v100,v121,v99
v_mul_f32 v118,v22,v118
v_mul_f32 v119,v23,v119
v_mul_f32 v120,v24,v120
v_mul_f32 v121,v25,v121
v_exp_f32 v118,v118
v_exp_f32 v119,v119
v_exp_f32 v120,v120
v_exp_f32 v121,v121
M 30,a[220:223],78
L 6
v_rcp_f32 v117,v117
v_fma_f32 v118,v118,v101,v101
v_fma_f32 v119,v119,v101,v101
v_fma_f32 v120,v120,v101,v101
v_fma_f32 v121,v121,v101,v101
v_rcp_f32 v118,v118
v_rcp_f32 v119,v119
v_rcp_f32 v120,v120
v_rcp_f32 v121,v121
M 30,a[228:231],82
L 2
R 46,1,3072
v_mul_f32 v21,v21,v117
v_cvt_pk_bf16_f32 v123,v20,v21
v_mul_f32 v22,v22,v118
v_mul_f32 v23,v23,v119
v_mul_f32 v24,v24,v120
v_mul_f32 v25,v25,v121
v_cvt_pk_bf16_f32 v124,v22,v23
v_cvt_pk_bf16_f32 v125,v24,v25
M 30,a[236:239],86
s_add_u32 s55,s55,s60
buffer_store_dwordx4 v[122:125],v15,s[36:39],s66 offen offset:0 sc0 sc1
v_xor_b32 v2,65536,v2
M 26,a[224:227],82
s_and_b32 s57,s57,131071
R 50,1,4096
v_cmp_eq_u32 vcc,s53,v7
M 26,a[232:235],86
s_nop 0
s_add_u32 s68,s68,s63
s_cmp_eq_u32 vcc_lo,-1
M 26,a[240:243],90
R 54,1,5120
M 26,a[248:251],94
buffer_store_dwordx4 v[122:125],v16,s[36:39],s66 offen offset:0 sc0 sc1
M 30,a[244:247],90
R 58,1,6144
M 30,a[252:255],94
L 5
D 0,56,2048
D 0,56,3072
s_cbranch_scc1 Lg1_S0_%=
Lg1_N0_%=:
v_mfma_f32_16x16x32_bf16 v[22:25],a[4:7],v[34:37],0
s_add_u32 m0,s59,0
s_mov_b32 s66,s65
R 62,1,7168
M 22,a[12:15],38
s_add_u32 s65,s65,s62
buffer_load_dword v5,s[36:39],s58 offen offset:0 sc1 lds
v_mfma_f32_16x16x32_bf16 v[18:21],a[0:3],v[34:37],0
R 66,1,8192
v_fma_f32 v26,-v102,v106,v26
M 18,a[8:11],38
L 5
buffer_load_dwordx4 v[98:101],v11,s[36:39],s73 offen offset:0 sc1
M 18,a[16:19],42
s_add_u32 s73,s73,s64
R 70,1,9216
v_fma_f32 v27,-v102,v107,v27
M 18,a[24:27],46
v_fma_f32 v28,-v102,v108,v28
v_fma_f32 v29,-v102,v109,v29
M 22,a[20:23],42
R 74,1,10240
v_mul_f32 v114,v26,v26
M 22,a[28:31],46
L 5
v_mul_f32 v115,v27,v27
v_mul_f32 v116,v28,v28
M 22,a[36:39],50
R 78,1,11264
v_mul_f32 v117,v29,v29
M 22,a[44:47],54
v_fma_f32 v114,v104,v114,v103
v_fma_f32 v115,v104,v115,v103
M 18,a[32:35],50
R 82,1,12288
v_fma_f32 v116,v104,v116,v103
M 18,a[40:43],54
L 5
v_fma_f32 v117,v104,v117,v103
v_mul_f32 v114,v26,v114
M 18,a[48:51],58
R 86,1,13312
v_mul_f32 v115,v27,v115
M 18,a[56:59],62
v_exp_f32 v114,v114
M 22,a[52:55],58
R 90,1,14336
v_mul_f32 v116,v28,v116
M 22,a[60:63],62
L 5
v_exp_f32 v115,v115
M 22,a[68:71],66
R 94,1,15360
v_mul_f32 v117,v29,v117
M 22,a[76:79],70
v_exp_f32 v116,v116
M 18,a[64:67],66
R 34,1,16384
v_fma_f32 v114,v114,v105,v105
M 18,a[72:75],70
L 5
v_exp_f32 v117,v117
M 18,a[80:83],74
R 38,1,17408
v_fma_f32 v115,v115,v105,v105
M 18,a[88:91],78
v_rcp_f32 v114,v114
M 22,a[84:87],74
R 42,1,18432
v_fma_f32 v116,v116,v105,v105
M 22,a[92:95],78
L 5
v_rcp_f32 v115,v115
M 22,a[100:103],82
R 46,1,19456
v_fma_f32 v117,v117,v105,v105
M 22,a[108:111],86
v_rcp_f32 v116,v116
M 18,a[96:99],82
R 50,1,20480
v_mul_f32 v26,v26,v114
M 18,a[104:107],86
L 5
v_rcp_f32 v117,v117
M 18,a[112:115],90
R 54,1,21504
v_mul_f32 v27,v27,v115
M 18,a[120:123],94
v_mul_f32 v28,v28,v116
v_mul_f32 v29,v29,v117
M 22,a[116:119],90
R 58,1,22528
v_cvt_pk_bf16_f32 v122,v26,v27
M 22,a[124:127],94
L 5
v_cvt_pk_bf16_f32 v123,v28,v29
v_fma_f32 v30,-v102,v110,v30
M 22,a[132:135],34
R 62,1,23552
v_fma_f32 v31,-v102,v111,v31
M 22,a[140:143],38
v_fma_f32 v32,-v102,v112,v32
v_fma_f32 v33,-v102,v113,v33
M 18,a[128:131],34
R 66,1,24576
v_mul_f32 v118,v30,v30
M 18,a[136:139],38
L 5
v_mul_f32 v119,v31,v31
v_mul_f32 v120,v32,v32
M 18,a[144:147],42
R 70,1,25600
v_mul_f32 v121,v33,v33
M 18,a[152:155],46
v_fma_f32 v118,v104,v118,v103
v_fma_f32 v119,v104,v119,v103
M 22,a[148:151],42
R 74,1,26624
v_fma_f32 v120,v104,v120,v103
M 22,a[156:159],46
L 5
v_fma_f32 v121,v104,v121,v103
v_mul_f32 v118,v30,v118
v_mul_f32 v119,v31,v119
M 22,a[164:167],50
R 78,1,27648
v_mul_f32 v120,v32,v120
M 22,a[172:175],54
v_exp_f32 v118,v118
M 18,a[160:163],50
R 82,1,28672
v_mul_f32 v121,v33,v121
M 18,a[168:171],54
L 5
v_exp_f32 v119,v119
M 18,a[176:179],58
R 86,1,29696
v_fma_f32 v118,v118,v105,v105
M 18,a[184:187],62
v_exp_f32 v120,v120
M 22,a[180:183],58
R 90,1,30720
v_fma_f32 v119,v119,v105,v105
M 22,a[188:191],62
L 5
v_exp_f32 v121,v121
M 22,a[196:199],66
s_waitcnt vmcnt(0)
R 94,1,31744
v_fma_f32 v120,v120,v105,v105
M 22,a[204:207],70
s_barrier
buffer_store_dword v4,v3,s[36:39],s70 offen offset:0 sc0 sc1
M 18,a[192:195],66
s_mov_b32 s70,s69
R 34,2,0
ds_read_b32 v7,v6 offset:0
M 18,a[200:203],70
L 6
v_rcp_f32 v118,v118
M 18,a[208:211],74
s_mov_b32 s69,s68
R 38,2,1024
v_fma_f32 v121,v121,v105,v105
M 18,a[216:219],78
s_add_u32 s68,s68,s63
v_rcp_f32 v119,v119
M 22,a[212:215],74
R 42,2,2048
v_mul_f32 v30,v30,v118
M 22,a[220:223],78
L 6
v_rcp_f32 v120,v120
v_rcp_f32 v121,v121
M 22,a[228:231],82
L 2
R 46,2,3072
v_mul_f32 v31,v31,v119
v_mul_f32 v32,v32,v120
v_mul_f32 v33,v33,v121
v_cvt_pk_bf16_f32 v124,v30,v31
v_cvt_pk_bf16_f32 v125,v32,v33
M 22,a[236:239],86
buffer_store_dwordx4 v[122:125],v15,s[36:39],s67 offen offset:0 sc0 sc1
v_xor_b32 v1,65536,v1
M 18,a[224:227],82
R 50,2,4096
v_cmp_eq_u32 vcc,s53,v7
M 18,a[232:235],86
s_nop 0
s_cmp_eq_u64 vcc,-1
M 18,a[240:243],90
R 54,2,5120
M 18,a[248:251],94
buffer_store_dwordx4 v[122:125],v16,s[36:39],s67 offen offset:0 sc0 sc1
)"
R"(M 22,a[244:247],90
R 58,2,6144
M 22,a[252:255],94
L 5
s_cbranch_scc1 Lg1_L21_%=
s_cmp_eq_u32 vcc_lo,-1
s_cbranch_scc1 Lg1_L11_%=
Lg1_X1_%=:
v_mfma_f32_16x16x32_bf16 v[30:33],a[4:7],v[34:37],0
s_add_u32 m0,s59,1024
s_mov_b32 s67,s65
R 62,2,7168
M 30,a[12:15],38
s_add_u32 s65,s65,s62
buffer_load_dword v5,s[36:39],s58 offen offset:0 sc1 lds
v_mfma_f32_16x16x32_bf16 v[26:29],a[0:3],v[34:37],0
R 66,2,8192
v_fma_f32 v18,-v98,v106,v18
M 26,a[8:11],38
L 5
buffer_load_dwordx4 v[102:105],v11,s[36:39],s73 offen offset:0 sc1
M 26,a[16:19],42
s_add_u32 s73,s73,s64
R 70,2,9216
v_fma_f32 v19,-v98,v107,v19
M 26,a[24:27],46
v_fma_f32 v20,-v98,v108,v20
v_fma_f32 v21,-v98,v109,v21
M 30,a[20:23],42
R 74,2,10240
v_mul_f32 v114,v18,v18
M 30,a[28:31],46
L 5
v_mul_f32 v115,v19,v19
v_mul_f32 v116,v20,v20
M 30,a[36:39],50
R 78,2,11264
v_mul_f32 v117,v21,v21
M 30,a[44:47],54
v_fma_f32 v114,v100,v114,v99
v_fma_f32 v115,v100,v115,v99
M 26,a[32:35],50
R 82,2,12288
v_fma_f32 v116,v100,v116,v99
M 26,a[40:43],54
L 5
v_fma_f32 v117,v100,v117,v99
v_mul_f32 v114,v18,v114
M 26,a[48:51],58
R 86,2,13312
v_mul_f32 v115,v19,v115
M 26,a[56:59],62
v_exp_f32 v114,v114
M 30,a[52:55],58
R 90,2,14336
v_mul_f32 v116,v20,v116
M 30,a[60:63],62
L 5
v_exp_f32 v115,v115
M 30,a[68:71],66
R 94,2,15360
v_mul_f32 v117,v21,v117
M 30,a[76:79],70
v_exp_f32 v116,v116
M 26,a[64:67],66
R 34,2,16384
v_fma_f32 v114,v114,v101,v101
M 26,a[72:75],70
L 5
v_exp_f32 v117,v117
M 26,a[80:83],74
R 38,2,17408
v_fma_f32 v115,v115,v101,v101
M 26,a[88:91],78
v_rcp_f32 v114,v114
M 30,a[84:87],74
R 42,2,18432
v_fma_f32 v116,v116,v101,v101
M 30,a[92:95],78
L 5
v_rcp_f32 v115,v115
M 30,a[100:103],82
R 46,2,19456
v_fma_f32 v117,v117,v101,v101
M 30,a[108:111],86
v_rcp_f32 v116,v116
M 26,a[96:99],82
R 50,2,20480
v_mul_f32 v18,v18,v114
M 26,a[104:107],86
L 5
v_rcp_f32 v117,v117
M 26,a[112:115],90
R 54,2,21504
v_mul_f32 v19,v19,v115
M 26,a[120:123],94
v_mul_f32 v20,v20,v116
v_mul_f32 v21,v21,v117
M 30,a[116:119],90
R 58,2,22528
v_cvt_pk_bf16_f32 v122,v18,v19
M 30,a[124:127],94
L 5
v_cvt_pk_bf16_f32 v123,v20,v21
v_fma_f32 v22,-v98,v110,v22
M 30,a[132:135],34
R 62,2,23552
v_fma_f32 v23,-v98,v111,v23
M 30,a[140:143],38
v_fma_f32 v24,-v98,v112,v24
v_fma_f32 v25,-v98,v113,v25
M 26,a[128:131],34
R 66,2,24576
v_mul_f32 v118,v22,v22
M 26,a[136:139],38
L 5
v_mul_f32 v119,v23,v23
v_mul_f32 v120,v24,v24
M 26,a[144:147],42
R 70,2,25600
v_mul_f32 v121,v25,v25
M 26,a[152:155],46
v_fma_f32 v118,v100,v118,v99
v_fma_f32 v119,v100,v119,v99
M 30,a[148:151],42
R 74,2,26624
v_fma_f32 v120,v100,v120,v99
M 30,a[156:159],46
L 5
v_fma_f32 v121,v100,v121,v99
v_mul_f32 v118,v22,v118
M 30,a[164:167],50
R 78,2,27648
v_mul_f32 v119,v23,v119
M 30,a[172:175],54
v_exp_f32 v118,v118
M 26,a[160:163],50
R 82,2,28672
v_mul_f32 v120,v24,v120
M 26,a[168:171],54
L 5
v_exp_f32 v119,v119
M 26,a[176:179],58
R 86,2,29696
v_mul_f32 v121,v25,v121
M 26,a[184:187],62
v_exp_f32 v120,v120
M 30,a[180:183],58
R 90,2,30720
v_fma_f32 v118,v118,v101,v101
M 30,a[188:191],62
L 5
v_exp_f32 v121,v121
M 30,a[196:199],66
s_waitcnt vmcnt(0)
R 94,2,31744
v_fma_f32 v119,v119,v101,v101
M 30,a[204:207],70
s_barrier
buffer_store_dword v4,v3,s[36:39],s70 offen offset:0 sc0 sc1
M 26,a[192:195],66
s_mov_b32 s70,s69
ds_read_b32 v7,v6 offset:1024
v_fma_f32 v120,v120,v101,v101
M 26,a[200:203],70
L 5
v_rcp_f32 v118,v118
M 26,a[208:211],74
s_mov_b32 s69,s68
v_fma_f32 v121,v121,v101,v101
v_mul_f32 v22,v22,v118
M 26,a[216:219],78
s_add_u32 s68,s68,s63
v_rcp_f32 v119,v119
M 30,a[212:215],74
v_rcp_f32 v120,v120
M 30,a[220:223],78
L 3
v_rcp_f32 v121,v121
M 30,a[228:231],82
L 0
v_mul_f32 v23,v23,v119
v_mul_f32 v24,v24,v120
v_mul_f32 v25,v25,v121
v_cvt_pk_bf16_f32 v124,v22,v23
v_cvt_pk_bf16_f32 v125,v24,v25
M 30,a[236:239],86
buffer_store_dwordx4 v[122:125],v15,s[36:39],s66 offen offset:0 sc0 sc1
v_xor_b32 v2,65536,v2
M 26,a[224:227],82
v_cmp_eq_u32 vcc,s53,v7
M 26,a[232:235],86
s_nop 0
M 26,a[240:243],90
buffer_store_dwordx4 v[122:125],v16,s[36:39],s66 offen offset:0 sc0 sc1
M 26,a[248:251],94
M 30,a[244:247],90
M 30,a[252:255],94
s_mov_b32 s80,0
s_cmp_eq_u32 vcc_lo,-1
s_cbranch_scc0 Lg1_xn1_%=
s_mov_b32 m0,s57
s_add_u32 s56,s55,4096
D 0,55,0
v_fma_f32 v26,-v102,v106,v26
v_fma_f32 v27,-v102,v107,v27
v_fma_f32 v28,-v102,v108,v28
v_fma_f32 v29,-v102,v109,v29
v_mul_f32 v114,v26,v26
v_mul_f32 v115,v27,v27
v_mul_f32 v116,v28,v28
D 0,55,1024
v_mul_f32 v117,v29,v29
v_fma_f32 v114,v104,v114,v103
v_fma_f32 v115,v104,v115,v103
v_fma_f32 v116,v104,v116,v103
v_fma_f32 v117,v104,v117,v103
v_mul_f32 v114,v26,v114
v_mul_f32 v115,v27,v115
D 0,55,2048
v_mul_f32 v116,v28,v116
v_mul_f32 v117,v29,v117
v_exp_f32 v114,v114
v_exp_f32 v115,v115
v_exp_f32 v116,v116
v_exp_f32 v117,v117
v_fma_f32 v114,v114,v105,v105
D 0,55,3072
v_fma_f32 v115,v115,v105,v105
v_fma_f32 v116,v116,v105,v105
v_fma_f32 v117,v117,v105,v105
v_rcp_f32 v114,v114
v_rcp_f32 v115,v115
v_rcp_f32 v116,v116
v_rcp_f32 v117,v117
s_add_u32 m0,s57,4096
s_nop 0
D 0,56,0
v_mul_f32 v26,v26,v114
v_mul_f32 v27,v27,v115
v_mul_f32 v28,v28,v116
v_mul_f32 v29,v29,v117
v_cvt_pk_bf16_f32 v122,v26,v27
v_cvt_pk_bf16_f32 v123,v28,v29
v_fma_f32 v30,-v102,v110,v30
D 0,56,1024
v_fma_f32 v31,-v102,v111,v31
v_fma_f32 v32,-v102,v112,v32
v_fma_f32 v33,-v102,v113,v33
v_mul_f32 v118,v30,v30
v_mul_f32 v119,v31,v31
v_mul_f32 v120,v32,v32
v_mul_f32 v121,v33,v33
D 0,56,2048
v_fma_f32 v118,v104,v118,v103
v_fma_f32 v119,v104,v119,v103
v_fma_f32 v120,v104,v120,v103
v_fma_f32 v121,v104,v121,v103
v_mul_f32 v118,v30,v118
v_mul_f32 v119,v31,v119
v_mul_f32 v120,v32,v120
D 0,56,3072
v_mul_f32 v121,v33,v121
v_exp_f32 v118,v118
v_exp_f32 v119,v119
v_exp_f32 v120,v120
v_exp_f32 v121,v121
v_fma_f32 v118,v118,v105,v105
v_fma_f32 v119,v119,v105,v105
s_add_u32 s55,s55,s60
s_add_u32 s57,s57,32768
s_and_b32 s57,s57,131071
s_add_u32 s54,s54,s46
v_fma_f32 v120,v120,v105,v105
v_fma_f32 v121,v121,v105,v105
v_rcp_f32 v118,v118
v_rcp_f32 v119,v119
v_rcp_f32 v120,v120
v_rcp_f32 v121,v121
v_mul_f32 v30,v30,v118
v_mul_f32 v31,v31,v119
v_mul_f32 v32,v32,v120
v_mul_f32 v33,v33,v121
v_cvt_pk_bf16_f32 v124,v30,v31
v_cvt_pk_bf16_f32 v125,v32,v33
buffer_store_dwordx4 v[122:125],v15,s[36:39],s67 offen offset:0 sc0 sc1
buffer_store_dwordx4 v[122:125],v16,s[36:39],s67 offen offset:0 sc0 sc1
s_mov_b32 s72,s69
s_mov_b32 s69,0x7fff0000
s_mov_b32 s67,0x7fff0000
s_mov_b32 s77,0x7fff0000
s_branch Lg1_gox_%=
Lg1_xn1_%=:
s_cmp_ge_u32 s54,256
s_cbranch_scc1 Lg1_fin1_%=
s_add_u32 m0,s59,4096
s_nop 0
buffer_load_dword v5,s[36:39],s58 offen offset:0 sc1 lds
buffer_store_dword v4,v3,s[36:39],s71 offen offset:0 sc0 sc1
s_add_u32 m0,s59,5120
s_nop 0
buffer_load_dword v5,s[36:39],s58 offen offset:0 sc1 lds
buffer_store_dword v4,v3,s[36:39],s71 offen offset:0 sc0 sc1
s_mov_b32 s83,0
v_fma_f32 v26,-v102,v106,v26
v_fma_f32 v27,-v102,v107,v27
v_fma_f32 v28,-v102,v108,v28
v_fma_f32 v29,-v102,v109,v29
v_mul_f32 v114,v26,v26
v_mul_f32 v115,v27,v27
v_mul_f32 v116,v28,v28
v_mul_f32 v117,v29,v29
v_fma_f32 v114,v104,v114,v103
v_fma_f32 v115,v104,v115,v103
v_fma_f32 v116,v104,v116,v103
v_fma_f32 v117,v104,v117,v103
v_mul_f32 v114,v26,v114
v_mul_f32 v115,v27,v115
v_mul_f32 v116,v28,v116
v_mul_f32 v117,v29,v117
v_exp_f32 v114,v114
v_exp_f32 v115,v115
v_exp_f32 v116,v116
v_exp_f32 v117,v117
v_fma_f32 v114,v114,v105,v105
v_fma_f32 v115,v115,v105,v105
v_fma_f32 v116,v116,v105,v105
v_fma_f32 v117,v117,v105,v105
v_rcp_f32 v114,v114
v_rcp_f32 v115,v115
v_rcp_f32 v116,v116
v_rcp_f32 v117,v117
v_mul_f32 v26,v26,v114
v_mul_f32 v27,v27,v115
v_mul_f32 v28,v28,v116
v_mul_f32 v29,v29,v117
v_cvt_pk_bf16_f32 v122,v26,v27
v_cvt_pk_bf16_f32 v123,v28,v29
v_fma_f32 v30,-v102,v110,v30
v_fma_f32 v31,-v102,v111,v31
v_fma_f32 v32,-v102,v112,v32
v_fma_f32 v33,-v102,v113,v33
v_mul_f32 v118,v30,v30
v_mul_f32 v119,v31,v31
v_mul_f32 v120,v32,v32
v_mul_f32 v121,v33,v33
v_fma_f32 v118,v104,v118,v103
v_fma_f32 v119,v104,v119,v103
v_fma_f32 v120,v104,v120,v103
v_fma_f32 v121,v104,v121,v103
v_mul_f32 v118,v30,v118
v_mul_f32 v119,v31,v119
v_mul_f32 v120,v32,v120
v_mul_f32 v121,v33,v121
v_exp_f32 v118,v118
v_exp_f32 v119,v119
v_exp_f32 v120,v120
v_exp_f32 v121,v121
v_fma_f32 v118,v118,v105,v105
v_fma_f32 v119,v119,v105,v105
v_fma_f32 v120,v120,v105,v105
v_fma_f32 v121,v121,v105,v105
v_rcp_f32 v118,v118
v_rcp_f32 v119,v119
v_rcp_f32 v120,v120
v_rcp_f32 v121,v121
v_mul_f32 v30,v30,v118
v_mul_f32 v31,v31,v119
v_mul_f32 v32,v32,v120
v_mul_f32 v33,v33,v121
v_cvt_pk_bf16_f32 v124,v30,v31
v_cvt_pk_bf16_f32 v125,v32,v33
buffer_store_dwordx4 v[122:125],v15,s[36:39],s67 offen offset:0 sc0 sc1
buffer_store_dwordx4 v[122:125],v16,s[36:39],s67 offen offset:0 sc0 sc1
s_mov_b32 s72,s69
s_mov_b32 s69,0x7fff0000
s_mov_b32 s67,0x7fff0000
s_mov_b32 s77,0x7fff0000
s_branch Lg1_poll_%=
Lg1_fin1_%=:
v_fma_f32 v26,-v102,v106,v26
v_fma_f32 v27,-v102,v107,v27
v_fma_f32 v28,-v102,v108,v28
v_fma_f32 v29,-v102,v109,v29
v_mul_f32 v114,v26,v26
v_mul_f32 v115,v27,v27
v_mul_f32 v116,v28,v28
v_mul_f32 v117,v29,v29
v_fma_f32 v114,v104,v114,v103
v_fma_f32 v115,v104,v115,v103
v_fma_f32 v116,v104,v116,v103
v_fma_f32 v117,v104,v117,v103
v_mul_f32 v114,v26,v114
v_mul_f32 v115,v27,v115
v_mul_f32 v116,v28,v116
v_mul_f32 v117,v29,v117
v_exp_f32 v114,v114
v_exp_f32 v115,v115
v_exp_f32 v116,v116
v_exp_f32 v117,v117
v_fma_f32 v114,v114,v105,v105
v_fma_f32 v115,v115,v105,v105
v_fma_f32 v116,v116,v105,v105
v_fma_f32 v117,v117,v105,v105
v_rcp_f32 v114,v114
v_rcp_f32 v115,v115
v_rcp_f32 v116,v116
v_rcp_f32 v117,v117
v_mul_f32 v26,v26,v114
v_mul_f32 v27,v27,v115
v_mul_f32 v28,v28,v116
v_mul_f32 v29,v29,v117
v_cvt_pk_bf16_f32 v122,v26,v27
v_cvt_pk_bf16_f32 v123,v28,v29
v_fma_f32 v30,-v102,v110,v30
v_fma_f32 v31,-v102,v111,v31
v_fma_f32 v32,-v102,v112,v32
v_fma_f32 v33,-v102,v113,v33
v_mul_f32 v118,v30,v30
v_mul_f32 v119,v31,v31
v_mul_f32 v120,v32,v32
v_mul_f32 v121,v33,v33
v_fma_f32 v118,v104,v118,v103
v_fma_f32 v119,v104,v119,v103
v_fma_f32 v120,v104,v120,v103
v_fma_f32 v121,v104,v121,v103
v_mul_f32 v118,v30,v118
v_mul_f32 v119,v31,v119
v_mul_f32 v120,v32,v120
v_mul_f32 v121,v33,v121
v_exp_f32 v118,v118
v_exp_f32 v119,v119
v_exp_f32 v120,v120
v_exp_f32 v121,v121
v_fma_f32 v118,v118,v105,v105
v_fma_f32 v119,v119,v105,v105
v_fma_f32 v120,v120,v105,v105
v_fma_f32 v121,v121,v105,v105
v_rcp_f32 v118,v118
v_rcp_f32 v119,v119
v_rcp_f32 v120,v120
v_rcp_f32 v121,v121
v_mul_f32 v30,v30,v118
v_mul_f32 v31,v31,v119
v_mul_f32 v32,v32,v120
v_mul_f32 v33,v33,v121
v_cvt_pk_bf16_f32 v124,v30,v31
v_cvt_pk_bf16_f32 v125,v32,v33
buffer_store_dwordx4 v[122:125],v15,s[36:39],s67 offen offset:0 sc0 sc1
buffer_store_dwordx4 v[122:125],v16,s[36:39],s67 offen offset:0 sc0 sc1
s_mov_b32 s72,s69
s_mov_b32 s69,0x7fff0000
s_mov_b32 s67,0x7fff0000
s_mov_b32 s77,0x7fff0000
s_branch Lg1_done_%=
Lg1_L10_%=:
v_mfma_f32_16x16x32_bf16 v[22:25],a[4:7],v[34:37],0
s_add_u32 s58,s58,s61
s_add_u32 m0,s59,0
R 62,1,7168
M 22,a[12:15],38
s_add_u32 s56,s55,4096
buffer_load_dword v5,s[36:39],s58 offen offset:0 sc1 lds
v_mfma_f32_16x16x32_bf16 v[18:21],a[0:3],v[34:37],0
s_mov_b32 m0,s57
R 66,1,8192
v_fma_f32 v26,-v102,v106,v26
M 18,a[8:11],38
L 5
buffer_load_dwordx4 v[98:101],v11,s[36:39],s73 offen offset:0 sc1
M 18,a[16:19],42
s_mov_b32 s66,s65
R 70,1,9216
v_fma_f32 v27,-v102,v107,v27
M 18,a[24:27],46
s_add_u32 s73,s73,s64
v_fma_f32 v28,-v102,v108,v28
v_fma_f32 v29,-v102,v109,v29
M 22,a[20:23],42
s_add_u32 s54,s54,s46
R 74,1,10240
v_mul_f32 v114,v26,v26
M 22,a[28:31],46
L 5
D 0,55,0
M 22,a[36:39],50
s_add_u32 s65,s65,s62
R 78,1,11264
v_mul_f32 v115,v27,v27
M 22,a[44:47],54
v_mul_f32 v116,v28,v28
v_mul_f32 v117,v29,v29
M 18,a[32:35],50
R 82,1,12288
v_fma_f32 v114,v104,v114,v103
M 18,a[40:43],54
L 5
D 0,55,1024
M 18,a[48:51],58
R 86,1,13312
)"
R"(v_fma_f32 v115,v104,v115,v103
M 18,a[56:59],62
v_fma_f32 v116,v104,v116,v103
v_fma_f32 v117,v104,v117,v103
M 22,a[52:55],58
R 90,1,14336
v_mul_f32 v114,v26,v114
M 22,a[60:63],62
L 5
D 0,55,2048
M 22,a[68:71],66
R 94,1,15360
v_mul_f32 v115,v27,v115
M 22,a[76:79],70
v_exp_f32 v114,v114
M 18,a[64:67],66
R 34,1,16384
v_mul_f32 v116,v28,v116
M 18,a[72:75],70
L 5
D 0,55,3072
M 18,a[80:83],74
s_add_u32 m0,s57,4096
R 38,1,17408
v_mul_f32 v117,v29,v117
M 18,a[88:91],78
s_add_u32 s57,s57,32768
v_exp_f32 v115,v115
M 22,a[84:87],74
s_add_u32 s55,s55,s60
R 42,1,18432
v_fma_f32 v114,v114,v105,v105
M 22,a[92:95],78
L 5
D 0,56,0
M 22,a[100:103],82
s_and_b32 s57,s57,131071
R 46,1,19456
v_fma_f32 v115,v115,v105,v105
M 22,a[108:111],86
v_exp_f32 v116,v116
M 18,a[96:99],82
R 50,1,20480
v_fma_f32 v116,v116,v105,v105
M 18,a[104:107],86
L 5
D 0,56,1024
M 18,a[112:115],90
R 54,1,21504
v_fma_f32 v30,-v102,v110,v30
M 18,a[120:123],94
v_exp_f32 v117,v117
M 22,a[116:119],90
R 58,1,22528
v_fma_f32 v117,v117,v105,v105
M 22,a[124:127],94
L 5
D 0,56,2048
M 22,a[132:135],34
R 62,1,23552
v_fma_f32 v31,-v102,v111,v31
M 22,a[140:143],38
v_rcp_f32 v114,v114
M 18,a[128:131],34
R 66,1,24576
v_mul_f32 v26,v26,v114
M 18,a[136:139],38
L 5
D 0,56,3072
M 18,a[144:147],42
R 70,1,25600
v_fma_f32 v32,-v102,v112,v32
M 18,a[152:155],46
v_rcp_f32 v115,v115
M 22,a[148:151],42
R 74,1,26624
v_mul_f32 v27,v27,v115
M 22,a[156:159],46
L 5
v_rcp_f32 v116,v116
M 22,a[164:167],50
R 78,1,27648
v_mul_f32 v28,v28,v116
M 22,a[172:175],54
v_rcp_f32 v117,v117
M 18,a[160:163],50
R 82,1,28672
v_mul_f32 v29,v29,v117
M 18,a[168:171],54
L 5
v_cvt_pk_bf16_f32 v122,v26,v27
v_cvt_pk_bf16_f32 v123,v28,v29
M 18,a[176:179],58
R 86,1,29696
v_fma_f32 v33,-v102,v113,v33
M 18,a[184:187],62
v_mul_f32 v118,v30,v30
v_mul_f32 v119,v31,v31
M 22,a[180:183],58
R 90,1,30720
v_mul_f32 v120,v32,v32
M 22,a[188:191],62
L 5
v_mul_f32 v121,v33,v33
v_fma_f32 v118,v104,v118,v103
M 22,a[196:199],66
s_waitcnt vmcnt(0)
R 94,1,31744
v_fma_f32 v119,v104,v119,v103
M 22,a[204:207],70
s_barrier
buffer_store_dword v4,v3,s[36:39],s70 offen offset:0 sc0 sc1
M 18,a[192:195],66
s_mov_b32 s70,s69
R 34,2,0
ds_read_b32 v7,v6 offset:0
M 18,a[200:203],70
L 6
v_fma_f32 v120,v104,v120,v103
v_fma_f32 v121,v104,v121,v103
M 18,a[208:211],74
s_mov_b32 s69,s68
R 38,2,1024
v_mul_f32 v118,v30,v118
M 18,a[216:219],78
s_add_u32 s68,s68,s63
v_mul_f32 v119,v31,v119
v_mul_f32 v120,v32,v120
M 22,a[212:215],74
R 42,2,2048
v_mul_f32 v121,v33,v121
v_exp_f32 v118,v118
v_exp_f32 v119,v119
v_exp_f32 v120,v120
v_exp_f32 v121,v121
M 22,a[220:223],78
L 6
v_fma_f32 v118,v118,v105,v105
v_fma_f32 v119,v119,v105,v105
v_fma_f32 v120,v120,v105,v105
v_fma_f32 v121,v121,v105,v105
v_rcp_f32 v118,v118
v_rcp_f32 v119,v119
v_rcp_f32 v120,v120
v_rcp_f32 v121,v121
M 22,a[228:231],82
L 2
R 46,2,3072
v_mul_f32 v30,v30,v118
v_mul_f32 v31,v31,v119
v_mul_f32 v32,v32,v120
v_mul_f32 v33,v33,v121
v_cvt_pk_bf16_f32 v124,v30,v31
v_cvt_pk_bf16_f32 v125,v32,v33
M 22,a[236:239],86
buffer_store_dwordx4 v[122:125],v15,s[36:39],s67 offen offset:0 sc0 sc1
v_xor_b32 v1,65536,v1
M 18,a[224:227],82
R 50,2,4096
v_cmp_eq_u32 vcc,s53,v7
M 18,a[232:235],86
s_nop 0
s_cmp_eq_u64 vcc,-1
M 18,a[240:243],90
R 54,2,5120
M 18,a[248:251],94
buffer_store_dwordx4 v[122:125],v16,s[36:39],s67 offen offset:0 sc0 sc1
M 22,a[244:247],90
R 58,2,6144
M 22,a[252:255],94
L 5
s_cbranch_scc1 Lg1_L21_%=
s_cmp_eq_u32 vcc_lo,-1
s_cbranch_scc1 Lg1_L11_%=
s_branch Lg1_X1_%=
Lg1_L11_%=:
v_mfma_f32_16x16x32_bf16 v[30:33],a[4:7],v[34:37],0
s_add_u32 s58,s58,s61
s_add_u32 m0,s59,1024
R 62,2,7168
M 30,a[12:15],38
s_add_u32 s56,s55,4096
buffer_load_dword v5,s[36:39],s58 offen offset:0 sc1 lds
v_mfma_f32_16x16x32_bf16 v[26:29],a[0:3],v[34:37],0
s_mov_b32 m0,s57
R 66,2,8192
v_fma_f32 v18,-v98,v106,v18
M 26,a[8:11],38
L 5
buffer_load_dwordx4 v[102:105],v11,s[36:39],s73 offen offset:0 sc1
M 26,a[16:19],42
s_mov_b32 s67,s65
R 70,2,9216
v_fma_f32 v19,-v98,v107,v19
M 26,a[24:27],46
s_add_u32 s73,s73,s64
v_fma_f32 v20,-v98,v108,v20
v_fma_f32 v21,-v98,v109,v21
M 30,a[20:23],42
s_add_u32 s54,s54,s46
R 74,2,10240
v_mul_f32 v114,v18,v18
M 30,a[28:31],46
L 5
D 0,55,0
M 30,a[36:39],50
s_add_u32 s65,s65,s62
R 78,2,11264
v_mul_f32 v115,v19,v19
M 30,a[44:47],54
v_mul_f32 v116,v20,v20
v_mul_f32 v117,v21,v21
M 26,a[32:35],50
R 82,2,12288
v_fma_f32 v114,v100,v114,v99
M 26,a[40:43],54
L 5
D 0,55,1024
M 26,a[48:51],58
R 86,2,13312
v_fma_f32 v115,v100,v115,v99
M 26,a[56:59],62
v_fma_f32 v116,v100,v116,v99
v_fma_f32 v117,v100,v117,v99
M 30,a[52:55],58
R 90,2,14336
v_mul_f32 v114,v18,v114
M 30,a[60:63],62
L 5
D 0,55,2048
M 30,a[68:71],66
R 94,2,15360
v_mul_f32 v115,v19,v115
M 30,a[76:79],70
v_exp_f32 v114,v114
M 26,a[64:67],66
R 34,2,16384
v_mul_f32 v116,v20,v116
M 26,a[72:75],70
L 5
D 0,55,3072
M 26,a[80:83],74
s_add_u32 m0,s57,4096
R 38,2,17408
v_mul_f32 v117,v21,v117
M 26,a[88:91],78
s_add_u32 s57,s57,32768
v_exp_f32 v115,v115
M 30,a[84:87],74
s_add_u32 s55,s55,s60
R 42,2,18432
v_fma_f32 v114,v114,v101,v101
M 30,a[92:95],78
L 5
D 0,56,0
M 30,a[100:103],82
s_and_b32 s57,s57,131071
R 46,2,19456
v_fma_f32 v115,v115,v101,v101
M 30,a[108:111],86
v_exp_f32 v116,v116
M 26,a[96:99],82
R 50,2,20480
v_fma_f32 v116,v116,v101,v101
M 26,a[104:107],86
L 5
D 0,56,1024
M 26,a[112:115],90
R 54,2,21504
v_fma_f32 v22,-v98,v110,v22
M 26,a[120:123],94
v_exp_f32 v117,v117
M 30,a[116:119],90
R 58,2,22528
v_fma_f32 v117,v117,v101,v101
M 30,a[124:127],94
L 5
D 0,56,2048
M 30,a[132:135],34
R 62,2,23552
v_fma_f32 v23,-v98,v111,v23
M 30,a[140:143],38
v_rcp_f32 v114,v114
M 26,a[128:131],34
R 66,2,24576
v_mul_f32 v18,v18,v114
M 26,a[136:139],38
L 5
D 0,56,3072
M 26,a[144:147],42
R 70,2,25600
v_fma_f32 v24,-v98,v112,v24
M 26,a[152:155],46
v_rcp_f32 v115,v115
M 30,a[148:151],42
R 74,2,26624
v_mul_f32 v19,v19,v115
M 30,a[156:159],46
L 5
v_rcp_f32 v116,v116
M 30,a[164:167],50
R 78,2,27648
v_mul_f32 v20,v20,v116
M 30,a[172:175],54
v_rcp_f32 v117,v117
M 26,a[160:163],50
R 82,2,28672
v_mul_f32 v21,v21,v117
M 26,a[168:171],54
L 5
v_cvt_pk_bf16_f32 v122,v18,v19
v_cvt_pk_bf16_f32 v123,v20,v21
M 26,a[176:179],58
R 86,2,29696
v_fma_f32 v25,-v98,v113,v25
M 26,a[184:187],62
v_mul_f32 v118,v22,v22
v_mul_f32 v119,v23,v23
M 30,a[180:183],58
R 90,2,30720
v_mul_f32 v120,v24,v24
M 30,a[188:191],62
L 5
v_mul_f32 v121,v25,v25
v_fma_f32 v118,v100,v118,v99
M 30,a[196:199],66
s_waitcnt vmcnt(0)
R 94,2,31744
v_fma_f32 v119,v100,v119,v99
M 30,a[204:207],70
s_barrier
buffer_store_dword v4,v3,s[36:39],s70 offen offset:0 sc0 sc1
M 26,a[192:195],66
s_mov_b32 s70,s69
R 34,1,0
ds_read_b32 v7,v6 offset:1024
M 26,a[200:203],70
L 6
v_fma_f32 v120,v100,v120,v99
v_fma_f32 v121,v100,v121,v99
M 26,a[208:211],74
s_mov_b32 s69,s68
R 38,1,1024
v_mul_f32 v118,v22,v118
M 26,a[216:219],78
s_add_u32 s68,s68,s63
v_mul_f32 v119,v23,v119
v_mul_f32 v120,v24,v120
M 30,a[212:215],74
R 42,1,2048
v_mul_f32 v121,v25,v121
v_exp_f32 v118,v118
v_exp_f32 v119,v119
v_exp_f32 v120,v120
v_exp_f32 v121,v121
M 30,a[220:223],78
L 6
v_fma_f32 v118,v118,v101,v101
v_fma_f32 v119,v119,v101,v101
v_fma_f32 v120,v120,v101,v101
v_fma_f32 v121,v121,v101,v101
v_rcp_f32 v118,v118
v_rcp_f32 v119,v119
v_rcp_f32 v120,v120
v_rcp_f32 v121,v121
M 30,a[228:231],82
L 2
R 46,1,3072
v_mul_f32 v22,v22,v118
v_mul_f32 v23,v23,v119
v_mul_f32 v24,v24,v120
v_mul_f32 v25,v25,v121
v_cvt_pk_bf16_f32 v124,v22,v23
v_cvt_pk_bf16_f32 v125,v24,v25
M 30,a[236:239],86
buffer_store_dwordx4 v[122:125],v15,s[36:39],s66 offen offset:0 sc0 sc1
v_xor_b32 v2,65536,v2
M 26,a[224:227],82
R 50,1,4096
v_cmp_eq_u32 vcc,s53,v7
M 26,a[232:235],86
s_nop 0
s_cmp_eq_u64 vcc,-1
M 26,a[240:243],90
R 54,1,5120
M 26,a[248:251],94
buffer_store_dwordx4 v[122:125],v16,s[36:39],s66 offen offset:0 sc0 sc1
M 30,a[244:247],90
R 58,1,6144
M 30,a[252:255],94
L 5
s_cbranch_scc1 Lg1_L20_%=
s_cmp_eq_u32 vcc_lo,-1
s_cbranch_scc1 Lg1_L10_%=
s_branch Lg1_X0_%=
Lg1_done_%=:
s_waitcnt vmcnt(2)
s_barrier
buffer_store_dword v4,v3,s[36:39],s70 offen offset:0 sc0 sc1
s_waitcnt vmcnt(1)
s_barrier
buffer_store_dword v4,v3,s[36:39],s72 offen offset:0 sc0 sc1
s_waitcnt vmcnt(0) lgkmcnt(0)
s_barrier
s_branch Lg2_end_%=
Lg2_begin_%=:
Lcode_g2_b:
s_mov_b64 s[36:37],%1
s_mov_b32 s38,111243392
s_mov_b32 s39,0x00020000
s_mov_b32 s44,%2
s_mov_b32 s45,%3
s_mov_b32 s46,%4
s_mov_b32 s47,%5
s_mov_b32 s48,%6
s_mov_b32 s49,%7
s_mov_b32 s50,%8
s_mov_b32 s51,%9
s_mov_b32 s93,%10
v_mov_b32 v0,%11
v_mov_b32 v3,%12
v_mov_b32 v5,%13
v_mov_b32 v6,%14
v_mov_b32 v18,%15
v_mov_b32 v19,%16
v_mov_b32 v20,%17
v_mov_b32 v21,%18
v_mov_b32 v22,%19
v_mov_b32 v10,%20
v_mov_b32 v11,%21
v_mov_b32 v12,%22
v_mov_b32 v13,%23
v_mov_b32 v14,%24
v_mov_b32 v15,%25
v_mov_b32 v16,%26
s_mov_b64 s[40:41],%27
s_mov_b32 s42,2097152
s_mov_b32 s43,0x00020000
s_lshl_b32 s57,s44,11
s_mov_b32 s79,111243264
buffer_load_dword v4,off,s[36:39],s79 sc1
s_add_u32 s55,s50,0
s_add_u32 m0,s57,0
s_nop 0
buffer_load_dwordx4 v18,s[40:43],s55 offen offset:0  lds
s_add_u32 m0,s57,1024
s_nop 0
buffer_load_dwordx4 v19,s[40:43],s55 offen offset:0  lds
s_add_u32 m0,s57,8064
s_nop 0
buffer_load_dwordx4 v18,s[40:43],s55 offen offset:128  lds
s_add_u32 m0,s57,9088
s_nop 0
buffer_load_dwordx4 v19,s[40:43],s55 offen offset:128  lds
s_add_u32 s55,s50,131072
s_add_u32 m0,s57,16384
s_nop 0
buffer_load_dwordx4 v18,s[40:43],s55 offen offset:0  lds
s_add_u32 m0,s57,17408
s_nop 0
buffer_load_dwordx4 v19,s[40:43],s55 offen offset:0  lds
s_add_u32 m0,s57,24448
s_nop 0
buffer_load_dwordx4 v18,s[40:43],s55 offen offset:128  lds
s_add_u32 m0,s57,25472
s_nop 0
buffer_load_dwordx4 v19,s[40:43],s55 offen offset:128  lds
s_add_u32 s55,s50,262144
s_add_u32 m0,s57,32768
s_nop 0
buffer_load_dwordx4 v18,s[40:43],s55 offen offset:0  lds
s_add_u32 m0,s57,33792
s_nop 0
buffer_load_dwordx4 v19,s[40:43],s55 offen offset:0  lds
s_add_u32 m0,s57,40832
s_nop 0
buffer_load_dwordx4 v18,s[40:43],s55 offen offset:128  lds
s_add_u32 m0,s57,41856
s_nop 0
buffer_load_dwordx4 v19,s[40:43],s55 offen offset:128  lds
s_add_u32 s55,s50,393216
s_add_u32 m0,s57,49152
s_nop 0
buffer_load_dwordx4 v18,s[40:43],s55 offen offset:0  lds
s_add_u32 m0,s57,50176
s_nop 0
buffer_load_dwordx4 v19,s[40:43],s55 offen offset:0  lds
s_add_u32 m0,s57,57216
s_nop 0
buffer_load_dwordx4 v18,s[40:43],s55 offen offset:128  lds
s_add_u32 m0,s57,58240
s_nop 0
buffer_load_dwordx4 v19,s[40:43],s55 offen offset:128  lds
s_add_u32 s55,s50,524288
s_add_u32 m0,s57,65536
s_nop 0
buffer_load_dwordx4 v18,s[40:43],s55 offen offset:0  lds
s_add_u32 m0,s57,66560
s_nop 0
buffer_load_dwordx4 v19,s[40:43],s55 offen offset:0  lds
)"
R"(s_add_u32 m0,s57,73600
s_nop 0
buffer_load_dwordx4 v18,s[40:43],s55 offen offset:128  lds
s_add_u32 m0,s57,74624
s_nop 0
buffer_load_dwordx4 v19,s[40:43],s55 offen offset:128  lds
s_add_u32 s55,s50,655360
s_add_u32 m0,s57,81920
s_nop 0
buffer_load_dwordx4 v18,s[40:43],s55 offen offset:0  lds
s_add_u32 m0,s57,82944
s_nop 0
buffer_load_dwordx4 v19,s[40:43],s55 offen offset:0  lds
s_add_u32 m0,s57,89984
s_nop 0
buffer_load_dwordx4 v18,s[40:43],s55 offen offset:128  lds
s_add_u32 m0,s57,91008
s_nop 0
buffer_load_dwordx4 v19,s[40:43],s55 offen offset:128  lds
s_waitcnt vmcnt(24)
s_barrier
v_readfirstlane_b32 s53,v4
s_nop 3
s_add_u32 s53,s53,1
s_cmp_eq_u32 s53,0
s_cselect_b32 s53,1,s53
v_mov_b32 v4,s53
s_waitcnt vmcnt(20) lgkmcnt(0)
s_barrier
s_add_u32 s55,s50,786432
s_add_u32 m0,s57,98304
s_nop 0
buffer_load_dwordx4 v18,s[40:43],s55 offen offset:0  lds
s_add_u32 m0,s57,99328
s_nop 0
buffer_load_dwordx4 v19,s[40:43],s55 offen offset:0  lds
s_add_u32 m0,s57,106368
s_nop 0
buffer_load_dwordx4 v18,s[40:43],s55 offen offset:128  lds
s_add_u32 m0,s57,107392
s_nop 0
buffer_load_dwordx4 v19,s[40:43],s55 offen offset:128  lds
v_add_u32 v26,0,v20
v_add_u32 v27,0,v21
T a[0:1],26,0
T a[2:3],26,512
T a[4:5],27,0
T a[6:7],27,512
T a[8:9],26,4096
T a[10:11],26,4608
T a[12:13],27,4096
T a[14:15],27,4608
s_waitcnt vmcnt(20) lgkmcnt(0)
s_barrier
s_add_u32 s55,s50,917504
s_add_u32 m0,s57,0
s_nop 0
buffer_load_dwordx4 v18,s[40:43],s55 offen offset:0  lds
s_add_u32 m0,s57,1024
s_nop 0
buffer_load_dwordx4 v19,s[40:43],s55 offen offset:0  lds
s_add_u32 m0,s57,8064
s_nop 0
buffer_load_dwordx4 v18,s[40:43],s55 offen offset:128  lds
s_add_u32 m0,s57,9088
s_nop 0
buffer_load_dwordx4 v19,s[40:43],s55 offen offset:128  lds
v_add_u32 v26,16384,v20
v_add_u32 v27,16384,v21
T a[16:17],26,0
T a[18:19],26,512
T a[20:21],27,0
T a[22:23],27,512
T a[24:25],26,4096
T a[26:27],26,4608
T a[28:29],27,4096
T a[30:31],27,4608
s_waitcnt vmcnt(20) lgkmcnt(0)
s_barrier
s_add_u32 s55,s50,1048576
s_add_u32 m0,s57,16384
s_nop 0
buffer_load_dwordx4 v18,s[40:43],s55 offen offset:0  lds
s_add_u32 m0,s57,17408
s_nop 0
buffer_load_dwordx4 v19,s[40:43],s55 offen offset:0  lds
s_add_u32 m0,s57,24448
s_nop 0
buffer_load_dwordx4 v18,s[40:43],s55 offen offset:128  lds
s_add_u32 m0,s57,25472
s_nop 0
buffer_load_dwordx4 v19,s[40:43],s55 offen offset:128  lds
v_add_u32 v26,32768,v20
v_add_u32 v27,32768,v21
T a[32:33],26,0
T a[34:35],26,512
T a[36:37],27,0
T a[38:39],27,512
T a[40:41],26,4096
T a[42:43],26,4608
T a[44:45],27,4096
T a[46:47],27,4608
s_waitcnt vmcnt(20) lgkmcnt(0)
s_barrier
s_add_u32 s55,s50,1179648
s_add_u32 m0,s57,32768
s_nop 0
buffer_load_dwordx4 v18,s[40:43],s55 offen offset:0  lds
s_add_u32 m0,s57,33792
s_nop 0
buffer_load_dwordx4 v19,s[40:43],s55 offen offset:0  lds
s_add_u32 m0,s57,40832
s_nop 0
buffer_load_dwordx4 v18,s[40:43],s55 offen offset:128  lds
s_add_u32 m0,s57,41856
s_nop 0
buffer_load_dwordx4 v19,s[40:43],s55 offen offset:128  lds
v_add_u32 v26,49152,v20
v_add_u32 v27,49152,v21
T a[48:49],26,0
T a[50:51],26,512
T a[52:53],27,0
T a[54:55],27,512
T a[56:57],26,4096
T a[58:59],26,4608
T a[60:61],27,4096
T a[62:63],27,4608
s_waitcnt vmcnt(20) lgkmcnt(0)
s_barrier
s_add_u32 s55,s50,1310720
s_add_u32 m0,s57,49152
s_nop 0
buffer_load_dwordx4 v18,s[40:43],s55 offen offset:0  lds
s_add_u32 m0,s57,50176
s_nop 0
buffer_load_dwordx4 v19,s[40:43],s55 offen offset:0  lds
s_add_u32 m0,s57,57216
s_nop 0
buffer_load_dwordx4 v18,s[40:43],s55 offen offset:128  lds
s_add_u32 m0,s57,58240
s_nop 0
buffer_load_dwordx4 v19,s[40:43],s55 offen offset:128  lds
v_add_u32 v26,65536,v20
v_add_u32 v27,65536,v21
T a[64:65],26,0
T a[66:67],26,512
T a[68:69],27,0
T a[70:71],27,512
T a[72:73],26,4096
T a[74:75],26,4608
T a[76:77],27,4096
T a[78:79],27,4608
s_waitcnt vmcnt(20) lgkmcnt(0)
s_barrier
s_add_u32 s55,s50,1441792
s_add_u32 m0,s57,65536
s_nop 0
buffer_load_dwordx4 v18,s[40:43],s55 offen offset:0  lds
s_add_u32 m0,s57,66560
s_nop 0
buffer_load_dwordx4 v19,s[40:43],s55 offen offset:0  lds
s_add_u32 m0,s57,73600
s_nop 0
buffer_load_dwordx4 v18,s[40:43],s55 offen offset:128  lds
s_add_u32 m0,s57,74624
s_nop 0
buffer_load_dwordx4 v19,s[40:43],s55 offen offset:128  lds
v_add_u32 v26,81920,v20
v_add_u32 v27,81920,v21
T a[80:81],26,0
T a[82:83],26,512
T a[84:85],27,0
T a[86:87],27,512
T a[88:89],26,4096
T a[90:91],26,4608
T a[92:93],27,4096
T a[94:95],27,4608
s_waitcnt vmcnt(20) lgkmcnt(0)
s_barrier
s_add_u32 s55,s50,1572864
s_add_u32 m0,s57,81920
s_nop 0
buffer_load_dwordx4 v18,s[40:43],s55 offen offset:0  lds
s_add_u32 m0,s57,82944
s_nop 0
buffer_load_dwordx4 v19,s[40:43],s55 offen offset:0  lds
s_add_u32 m0,s57,89984
s_nop 0
buffer_load_dwordx4 v18,s[40:43],s55 offen offset:128  lds
s_add_u32 m0,s57,91008
s_nop 0
buffer_load_dwordx4 v19,s[40:43],s55 offen offset:128  lds
v_add_u32 v26,98304,v20
v_add_u32 v27,98304,v21
T a[96:97],26,0
T a[98:99],26,512
T a[100:101],27,0
T a[102:103],27,512
T a[104:105],26,4096
T a[106:107],26,4608
T a[108:109],27,4096
T a[110:111],27,4608
s_waitcnt vmcnt(20) lgkmcnt(0)
s_barrier
s_add_u32 s55,s50,1703936
s_add_u32 m0,s57,98304
s_nop 0
buffer_load_dwordx4 v18,s[40:43],s55 offen offset:0  lds
s_add_u32 m0,s57,99328
s_nop 0
buffer_load_dwordx4 v19,s[40:43],s55 offen offset:0  lds
s_add_u32 m0,s57,106368
s_nop 0
buffer_load_dwordx4 v18,s[40:43],s55 offen offset:128  lds
s_add_u32 m0,s57,107392
s_nop 0
buffer_load_dwordx4 v19,s[40:43],s55 offen offset:128  lds
v_add_u32 v26,0,v20
v_add_u32 v27,0,v21
T a[112:113],26,0
T a[114:115],26,512
T a[116:117],27,0
T a[118:119],27,512
T a[120:121],26,4096
T a[122:123],26,4608
T a[124:125],27,4096
T a[126:127],27,4608
s_waitcnt vmcnt(20) lgkmcnt(0)
s_barrier
s_add_u32 s55,s50,1835008
s_add_u32 m0,s57,0
s_nop 0
buffer_load_dwordx4 v18,s[40:43],s55 offen offset:0  lds
s_add_u32 m0,s57,1024
s_nop 0
buffer_load_dwordx4 v19,s[40:43],s55 offen offset:0  lds
s_add_u32 m0,s57,8064
s_nop 0
buffer_load_dwordx4 v18,s[40:43],s55 offen offset:128  lds
s_add_u32 m0,s57,9088
s_nop 0
buffer_load_dwordx4 v19,s[40:43],s55 offen offset:128  lds
v_add_u32 v26,16384,v20
v_add_u32 v27,16384,v21
T a[128:129],26,0
T a[130:131],26,512
T a[132:133],27,0
T a[134:135],27,512
T a[136:137],26,4096
T a[138:139],26,4608
T a[140:141],27,4096
T a[142:143],27,4608
s_waitcnt vmcnt(20) lgkmcnt(0)
s_barrier
s_add_u32 s55,s50,1966080
s_add_u32 m0,s57,16384
s_nop 0
buffer_load_dwordx4 v18,s[40:43],s55 offen offset:0  lds
s_add_u32 m0,s57,17408
s_nop 0
buffer_load_dwordx4 v19,s[40:43],s55 offen offset:0  lds
s_add_u32 m0,s57,24448
s_nop 0
buffer_load_dwordx4 v18,s[40:43],s55 offen offset:128  lds
s_add_u32 m0,s57,25472
s_nop 0
buffer_load_dwordx4 v19,s[40:43],s55 offen offset:128  lds
v_add_u32 v26,32768,v20
v_add_u32 v27,32768,v21
T a[144:145],26,0
T a[146:147],26,512
T a[148:149],27,0
T a[150:151],27,512
T a[152:153],26,4096
T a[154:155],26,4608
T a[156:157],27,4096
T a[158:159],27,4608
s_waitcnt vmcnt(20) lgkmcnt(0)
s_barrier
v_add_u32 v26,49152,v20
v_add_u32 v27,49152,v21
T a[160:161],26,0
T a[162:163],26,512
T a[164:165],27,0
T a[166:167],27,512
T a[168:169],26,4096
T a[170:171],26,4608
T a[172:173],27,4096
T a[174:175],27,4608
s_waitcnt vmcnt(16) lgkmcnt(0)
s_barrier
v_add_u32 v26,65536,v20
v_add_u32 v27,65536,v21
T a[176:177],26,0
T a[178:179],26,512
T a[180:181],27,0
T a[182:183],27,512
T a[184:185],26,4096
T a[186:187],26,4608
T a[188:189],27,4096
T a[190:191],27,4608
s_waitcnt vmcnt(12) lgkmcnt(0)
s_barrier
v_add_u32 v26,81920,v20
v_add_u32 v27,81920,v21
T a[192:193],26,0
T a[194:195],26,512
T a[196:197],27,0
T a[198:199],27,512
T a[200:201],26,4096
T a[202:203],26,4608
T a[204:205],27,4096
T a[206:207],27,4608
s_waitcnt vmcnt(8) lgkmcnt(0)
s_barrier
v_add_u32 v26,98304,v20
v_add_u32 v27,98304,v21
T a[208:209],26,0
T a[210:211],26,512
T a[212:213],27,0
T a[214:215],27,512
T a[216:217],26,4096
T a[218:219],26,4608
T a[220:221],27,4096
T a[222:223],27,4608
s_waitcnt vmcnt(4) lgkmcnt(0)
s_barrier
v_add_u32 v26,0,v20
v_add_u32 v27,0,v21
T a[224:225],26,0
T a[226:227],26,512
T a[228:229],27,0
T a[230:231],27,512
T a[232:233],26,4096
T a[234:235],26,4608
T a[236:237],27,4096
T a[238:239],27,4608
s_waitcnt vmcnt(0) lgkmcnt(0)
s_barrier
v_add_u32 v26,16384,v20
v_add_u32 v27,16384,v21
T a[240:241],26,0
T a[242:243],26,512
T a[244:245],27,0
T a[246:247],27,512
T a[248:249],26,4096
T a[250:251],26,4608
T a[252:253],27,4096
T a[254:255],27,4608
L 0
s_barrier
v_mov_b32 v4,s53
s_mov_b32 s82,0
s_mov_b32 s80,0
s_mov_b32 s54,s45
s_mul_i32 s57,s44,10240
s_mul_i32 s55,s45,40960
s_add_u32 s55,s55,s57
s_add_u32 s55,s55,17825792
s_mul_i32 s57,s44,8192
s_mul_i32 s60,s46,40960
s_mul_i32 s61,s46,1024
s_mul_i32 s62,s46,32768
s_mul_i32 s63,s46,1024
s_mul_i32 s64,s46,512
s_lshl_b32 s59,s44,8
s_add_u32 s59,s59,131072
s_mul_i32 s58,s45,1024
s_add_u32 s58,s58,109333504
s_mul_i32 s65,s45,32768
s_add_u32 s65,s65,s47
s_mul_i32 s68,s45,1024
s_add_u32 s68,s68,s48
s_mul_i32 s73,s45,512
s_add_u32 s73,s73,106954752
s_mul_i32 s74,s45,32768
s_add_u32 s74,s74,s49
s_mul_i32 s75,s45,1024
s_add_u32 s75,s75,107479040
s_mov_b32 s66,0x7fff0000
s_mov_b32 s67,0x7fff0000
s_mov_b32 s69,0x7fff0000
s_mov_b32 s76,0x7fff0000
s_mov_b32 s77,0x7fff0000
s_mov_b32 s70,0x7fff0000
s_mov_b32 s71,0x7fff0000
s_mov_b32 s72,0x7fff0000
v_mov_b32 v1,v0
v_add_u32 v2,32768,v0
s_mov_b32 s84,32768
s_mov_b32 s86,0x3a800000
s_mov_b32 s87,0x38a7c5ac
s_mov_b32 s88,0xc0135761
s_mov_b32 s89,0x3d372713
s_and_b32 s85,s44,1
s_mul_i32 s85,s85,127
v_lshlrev_b32 v8,22,v0
v_and_b32 v8,0x80000000,v8
v_or_b32 v8,v8,v5
s_getpc_b64 s[40:41]
Lg2_pc_%=:
s_lshl_b32 s78,s44,13
v_lshlrev_b32 v106,3,v0
v_add_u32 v106,s78,v106
v_min_u32 v106,Lcode_g12_e-Lg2_pc_%=-4,v106
s_mov_b32 m0,163584
s_nop 0
global_load_lds_dword v106,s[40:41]
s_and_b32 s79,s54,127
s_cmp_eq_u32 s79,0
s_cselect_b32 s94,0x3300,0
s_cmp_eq_u32 s79,127
s_cselect_b32 s94,0xcc00,s94
s_add_u32 s79,s54,s46
s_and_b32 s79,s79,127
s_cmp_eq_u32 s79,0
s_cselect_b32 s95,0x3300,0
s_cmp_eq_u32 s79,127
s_cselect_b32 s95,0xcc00,s95
s_add_u32 m0,s59,4096
s_nop 0
buffer_load_dword v5,s[36:39],s58 offen offset:0 sc1 lds
buffer_store_dword v4,v3,s[36:39],s71 offen offset:0 sc0 sc1
s_add_u32 m0,s59,5120
s_nop 0
buffer_load_dword v5,s[36:39],s58 offen offset:0 sc1 lds
buffer_store_dword v4,v3,s[36:39],s71 offen offset:0 sc0 sc1
s_mov_b32 s83,0
Lg2_poll_%=:
s_add_u32 s78,s83,2
s_and_b32 s78,s78,3
s_lshl_b32 s78,s78,10
s_add_u32 s78,s78,4096
s_add_u32 m0,s59,s78
s_nop 0
buffer_load_dword v5,s[36:39],s58 offen offset:0 sc1 lds
s_and_b32 s78,s83,3
s_lshl_b32 s78,s78,10
v_add_u32 v106,s78,v6
s_waitcnt vmcnt(4)
s_barrier
ds_read_b32 v7,v106 offset:4096
buffer_store_dword v4,v3,s[36:39],s70 offen offset:0 sc0 sc1
s_mov_b32 s70,s71
s_mov_b32 s71,s72
s_mov_b32 s72,0x7fff0000
s_add_u32 s83,s83,1
s_add_u32 s82,s82,1
L 0
v_cmp_eq_u32 vcc,s53,v7
s_nop 0
s_or_b64 vcc,vcc,s[94:95]
s_cmp_eq_u32 vcc_lo,-1
s_cbranch_scc1 Lg2_go_%=
s_cmp_lt_u32 s82,20000
s_cbranch_scc1 Lg2_poll_%=
Lg2_go_%=:
s_and_b32 s78,s54,127
s_cmp_eq_u32 s78,s85
s_cselect_b64 s[40:41],-1,0
v_cndmask_b32 v17,v15,v16,s[40:41]
s_add_u32 m0,s57,0
s_add_u32 s56,s55,0
D 17,56,0
s_add_u32 s56,s55,256
D 17,56,1024
s_add_u32 s56,s55,512
D 17,56,2048
s_add_u32 s56,s55,768
D 17,56,3072
s_add_u32 m0,s57,4096
s_add_u32 s56,s55,5120
D 17,56,0
s_add_u32 s56,s55,5376
D 17,56,1024
s_add_u32 s56,s55,5632
D 17,56,2048
s_add_u32 s56,s55,5888
D 17,56,3072
s_add_u32 s55,s55,s60
s_add_u32 s57,s57,32768
s_and_b32 s57,s57,131071
s_add_u32 s54,s54,s46
Lg2_gox_%=:
s_add_u32 s58,s58,s61
s_cmp_eq_u32 vcc_hi,-1
s_cbranch_scc1 Lg2_go2_%=
s_add_u32 m0,s59,2048
s_nop 0
buffer_load_dword v5,s[36:39],s58 offen offset:0 sc1 lds
s_and_b32 s79,s54,127
s_cmp_eq_u32 s79,0
s_cselect_b32 s94,0x3300,0
s_cmp_eq_u32 s79,127
s_cselect_b32 s94,0xcc00,s94
s_add_u32 s79,s54,s46
s_and_b32 s79,s79,127
s_cmp_eq_u32 s79,0
s_cselect_b32 s95,0x3300,0
s_cmp_eq_u32 s79,127
)"
R"(s_cselect_b32 s95,0xcc00,s95
s_waitcnt vmcnt(0)
s_barrier
buffer_store_dword v4,v3,s[36:39],s70 offen offset:0 sc0 sc1
s_min_u32 s71,s71,s72
buffer_store_dword v4,v3,s[36:39],s71 offen offset:0 sc0 sc1
s_mov_b32 s70,0x7fff0000
s_mov_b32 s71,0x7fff0000
s_mov_b32 s72,0x7fff0000
s_cmp_eq_u32 s80,1
s_cbranch_scc1 Lg2_st11_%=
Lg2_st10_%=:
ds_read_b32 v7,v6 offset:2048
R 34,1,0
R 38,1,1024
R 42,1,2048
R 46,1,3072
R 50,1,4096
R 54,1,5120
R 58,1,6144
L 7
v_cmp_eq_u32 vcc,s53,v7
s_nop 0
s_or_b64 vcc,vcc,s[94:95]
L 5
s_cmp_eq_u64 vcc,-1
s_cbranch_scc1 Lg2_L20_%=
s_cmp_eq_u32 vcc_lo,-1
s_cbranch_scc1 Lg2_L10_%=
s_branch Lg2_X0_%=
Lg2_st11_%=:
ds_read_b32 v7,v6 offset:2048
R 34,2,0
R 38,2,1024
R 42,2,2048
R 46,2,3072
R 50,2,4096
R 54,2,5120
R 58,2,6144
L 7
v_cmp_eq_u32 vcc,s53,v7
s_nop 0
s_or_b64 vcc,vcc,s[94:95]
L 5
s_cmp_eq_u64 vcc,-1
s_cbranch_scc1 Lg2_L21_%=
s_cmp_eq_u32 vcc_lo,-1
s_cbranch_scc1 Lg2_L11_%=
s_branch Lg2_X1_%=
Lg2_go2_%=:
s_add_u32 s58,s58,s61
s_add_u32 m0,s59,2048
s_nop 0
buffer_load_dword v5,s[36:39],s58 offen offset:0 sc1 lds
s_and_b32 s78,s54,127
s_cmp_eq_u32 s78,s85
s_cselect_b64 s[40:41],-1,0
v_cndmask_b32 v17,v15,v16,s[40:41]
s_add_u32 m0,s57,0
s_add_u32 s56,s55,0
D 17,56,0
s_add_u32 s56,s55,256
D 17,56,1024
s_add_u32 s56,s55,512
D 17,56,2048
s_add_u32 s56,s55,768
D 17,56,3072
s_add_u32 m0,s57,4096
s_add_u32 s56,s55,5120
D 17,56,0
s_add_u32 s56,s55,5376
D 17,56,1024
s_add_u32 s56,s55,5632
D 17,56,2048
s_add_u32 s56,s55,5888
D 17,56,3072
s_add_u32 s55,s55,s60
s_add_u32 s57,s57,32768
s_and_b32 s57,s57,131071
s_add_u32 s54,s54,s46
s_and_b32 s79,s54,127
s_cmp_eq_u32 s79,0
s_cselect_b32 s94,0x3300,0
s_cmp_eq_u32 s79,127
s_cselect_b32 s94,0xcc00,s94
s_add_u32 s79,s54,s46
s_and_b32 s79,s79,127
s_cmp_eq_u32 s79,0
s_cselect_b32 s95,0x3300,0
s_cmp_eq_u32 s79,127
s_cselect_b32 s95,0xcc00,s95
s_waitcnt vmcnt(8)
s_barrier
buffer_store_dword v4,v3,s[36:39],s70 offen offset:0 sc0 sc1
s_min_u32 s71,s71,s72
buffer_store_dword v4,v3,s[36:39],s71 offen offset:0 sc0 sc1
s_mov_b32 s70,0x7fff0000
s_mov_b32 s71,0x7fff0000
s_mov_b32 s72,0x7fff0000
s_cmp_eq_u32 s80,1
s_cbranch_scc1 Lg2_st21_%=
Lg2_st20_%=:
ds_read_b32 v7,v6 offset:2048
R 34,1,0
R 38,1,1024
R 42,1,2048
R 46,1,3072
R 50,1,4096
R 54,1,5120
R 58,1,6144
L 7
v_cmp_eq_u32 vcc,s53,v7
s_nop 0
s_or_b64 vcc,vcc,s[94:95]
L 5
s_cmp_eq_u32 vcc_lo,-1
s_cbranch_scc1 Lg2_S0_%=
s_branch Lg2_N0_%=
Lg2_st21_%=:
ds_read_b32 v7,v6 offset:2048
R 34,2,0
R 38,2,1024
R 42,2,2048
R 46,2,3072
R 50,2,4096
R 54,2,5120
R 58,2,6144
L 7
v_cmp_eq_u32 vcc,s53,v7
s_nop 0
s_or_b64 vcc,vcc,s[94:95]
L 5
s_cmp_eq_u32 vcc_lo,-1
s_cbranch_scc1 Lg2_S1_%=
s_branch Lg2_N1_%=
Lg2_L20_%=:
v_mfma_f32_16x16x32_bf16 v[22:25],a[4:7],v[34:37],0
s_add_u32 s58,s58,s61
s_and_b32 s78,s54,127
s_add_u32 s58,s58,s61
R 62,1,7168
M 22,a[12:15],38
s_add_u32 m0,s59,0
s_cmp_eq_u32 s78,s85
s_cselect_b64 s[40:41],-1,0
buffer_load_dwordx4 v[122:125],v0,s[36:39],s74 offen offset:0 sc1
v_mfma_f32_16x16x32_bf16 v[18:21],a[0:3],v[34:37],0
s_add_u32 s56,s55,0
R 66,1,8192
v_cndmask_b32 v17,v15,v16,s[40:41]
M 18,a[8:11],38
L 5
buffer_load_dword v8,s[36:39],s58 offen offset:0 sc1 lds
M 18,a[16:19],42
s_add_u32 m0,s57,0
R 70,1,9216
v_lshlrev_b32 v106,16,v126
M 18,a[24:27],46
s_add_u32 s54,s54,s46
buffer_load_dwordx2 v[98:99],v11,s[36:39],s73 offen offset:256 sc1
M 22,a[20:23],42
s_and_b32 s78,s54,127
R 74,1,10240
v_and_b32 v107,0xffff0000,v126
M 22,a[28:31],46
L 5
D 17,56,0
M 22,a[36:39],50
s_add_u32 s56,s55,256
R 78,1,11264
v_lshlrev_b32 v108,16,v127
M 22,a[44:47],54
s_cmp_eq_u32 s78,s85
v_and_b32 v109,0xffff0000,v127
v_lshlrev_b32 v110,16,v128
M 18,a[32:35],50
s_cselect_b64 s[40:41],-1,0
R 82,1,12288
v_and_b32 v111,0xffff0000,v128
M 18,a[40:43],54
L 5
D 17,56,1024
M 18,a[48:51],58
s_add_u32 s56,s55,512
R 86,1,13312
v_lshlrev_b32 v112,16,v129
M 18,a[56:59],62
s_add_u32 s54,s54,s46
v_and_b32 v113,0xffff0000,v129
v_fma_f32 v106,v106,v103,v102
M 22,a[52:55],58
s_and_b32 s79,s54,127
R 90,1,14336
v_fma_f32 v107,v107,v103,v102
M 22,a[60:63],62
L 5
D 17,56,2048
M 22,a[68:71],66
s_add_u32 s56,s55,768
R 94,1,15360
v_fma_f32 v108,v108,v103,v102
M 22,a[76:79],70
s_cmp_eq_u32 s79,0
v_fma_f32 v109,v109,v103,v102
v_fma_f32 v110,v110,v103,v102
M 18,a[64:67],66
s_cselect_b32 s94,0x3300,0
R 34,1,16384
v_fma_f32 v111,v111,v103,v102
M 18,a[72:75],70
L 5
D 17,56,3072
M 18,a[80:83],74
s_add_u32 m0,s57,4096
s_add_u32 s56,s55,5120
R 38,1,17408
M 18,a[88:91],78
s_add_u32 s57,s57,32768
v_fma_f32 v112,v112,v103,v102
v_fma_f32 v113,v113,v103,v102
M 22,a[84:87],74
s_and_b32 s57,s57,131071
R 42,1,18432
v_add_f32 v26,v26,v106
M 22,a[92:95],78
L 5
D 17,56,0
M 22,a[100:103],82
s_add_u32 s56,s55,5376
R 46,1,19456
v_add_f32 v27,v27,v107
M 22,a[108:111],86
s_cmp_eq_u32 s79,127
v_add_f32 v28,v28,v108
v_add_f32 v29,v29,v109
M 18,a[96:99],82
s_cselect_b32 s94,0xcc00,s94
R 50,1,20480
v_fma_f32 v107,v26,v26,s87
M 18,a[104:107],86
L 5
D 17,56,1024
M 18,a[112:115],90
s_add_u32 s56,s55,5632
R 54,1,21504
v_add_f32 v106,v26,v27
M 18,a[120:123],94
s_add_u32 s79,s54,s46
v_fma_f32 v107,v27,v27,v107
v_add_f32 v106,v106,v28
M 22,a[116:119],90
s_and_b32 s79,s79,127
R 58,1,22528
v_fma_f32 v107,v28,v28,v107
M 22,a[124:127],94
L 5
D 17,56,2048
M 22,a[132:135],34
s_add_u32 s56,s55,5888
s_add_u32 s55,s55,s60
R 62,1,23552
M 22,a[140:143],38
s_cmp_eq_u32 s79,0
v_add_f32 v30,v30,v110
v_add_f32 v106,v106,v29
M 18,a[128:131],34
s_cselect_b32 s95,0x3300,0
R 66,1,24576
v_fma_f32 v107,v29,v29,v107
M 18,a[136:139],38
L 5
D 17,56,3072
M 18,a[144:147],42
s_add_u32 m0,s57,0
s_add_u32 s56,s55,0
R 70,1,25600
M 18,a[152:155],46
s_cmp_eq_u32 s79,127
v_add_f32 v31,v31,v111
v_add_f32 v32,v32,v112
v_add_f32 v33,v33,v113
v_add_f32 v106,v106,v30
v_fma_f32 v107,v30,v30,v107
v_add_f32 v106,v106,v31
v_fma_f32 v107,v31,v31,v107
v_add_f32 v106,v106,v32
v_fma_f32 v107,v32,v32,v107
v_add_f32 v106,v106,v33
v_fma_f32 v107,v33,v33,v107
M 22,a[148:151],42
s_cselect_b32 s95,0xcc00,s95
R 74,1,26624
v_cndmask_b32 v17,v15,v16,s[40:41]
M 22,a[156:159],46
L 5
v_permlane16_swap_b32 v106,v107
v_add_f32 v106,v106,v107
v_mov_b32 v107,v106
M 22,a[164:167],50
s_mov_b32 s66,s65
R 78,1,27648
v_cvt_pk_bf16_f32 v114,v26,v27
M 22,a[172:175],54
s_mov_b32 s76,s75
v_permlane32_swap_b32 v106,v107
v_add_f32 v106,v106,v107
M 18,a[160:163],50
s_add_u32 s73,s73,s64
R 82,1,28672
ds_write_b32 v12,v106 offset:2048
M 18,a[168:171],54
L 6
D 17,56,0
M 18,a[176:179],58
s_add_u32 s56,s55,256
R 86,1,29696
v_cvt_pk_bf16_f32 v115,v28,v29
M 18,a[184:187],62
s_add_u32 s74,s74,s62
D 17,56,1024
M 22,a[180:183],58
s_add_u32 s56,s55,512
R 90,1,30720
v_cvt_pk_bf16_f32 v116,v30,v31
M 22,a[188:191],62
L 6
L 2
D 17,56,2048
M 22,a[196:199],66
s_waitcnt vmcnt(3)
s_add_u32 s56,s55,768
R 94,1,31744
M 22,a[204:207],70
s_barrier
D 17,56,3072
M 18,a[192:195],66
s_add_u32 m0,s57,4096
s_add_u32 s56,s55,5120
R 34,2,0
M 18,a[200:203],70
s_nop 0
buffer_store_dword v4,v3,s[36:39],s70 offen offset:0 sc0 sc1
M 18,a[208:211],74
s_mov_b32 s70,s69
R 38,2,1024
R 118,13,2048
M 18,a[216:219],78
s_add_u32 s57,s57,32768
D 17,56,0
M 22,a[212:215],74
s_add_u32 s56,s55,5376
R 42,2,2048
ds_read_b32 v7,v6 offset:0
M 22,a[220:223],78
L 7
D 17,56,1024
M 22,a[228:231],82
s_add_u32 s56,s55,5632
R 46,2,3072
v_cvt_pk_bf16_f32 v117,v32,v33
M 22,a[236:239],86
s_mov_b32 s69,s68
D 17,56,2048
M 18,a[224:227],82
s_add_u32 s56,s55,5888
R 50,2,4096
v_xor_b32 v1,65536,v1
M 18,a[232:235],86
L 7
L 4
buffer_store_dwordx4 v[114:117],v0,s[36:39],s67 offen offset:0 sc0 sc1
M 18,a[240:243],90
L 2
R 54,2,5120
v_add_f32 v118,v118,v119
M 18,a[248:251],94
s_add_u32 s55,s55,s60
v_cmp_eq_u32 vcc,s53,v7
v_add_f32 v120,v120,v121
M 22,a[244:247],90
s_and_b32 s57,s57,131071
s_add_u32 s65,s65,s62
s_add_u32 s75,s75,s63
s_add_u32 s68,s68,s63
s_or_b64 vcc,vcc,s[94:95]
s_cmp_eq_u32 vcc_lo,-1
R 58,2,6144
v_add_f32 v118,v118,v120
v_mul_f32 v118,s86,v118
M 22,a[252:255],94
s_nop 0
D 17,56,3072
buffer_store_dword v118,v14,s[36:39],s77 offen offset:0 sc0 sc1
s_cbranch_scc0 Lg2_N1_%=
Lg2_S1_%=:
v_mfma_f32_16x16x32_bf16 v[30:33],a[4:7],v[34:37],0
s_add_u32 s58,s58,s61
R 62,2,7168
v_lshlrev_b32 v106,16,v122
M 30,a[12:15],38
s_add_u32 m0,s59,1024
buffer_load_dwordx4 v[126:129],v0,s[36:39],s74 offen offset:0 sc1
v_mfma_f32_16x16x32_bf16 v[26:29],a[0:3],v[34:37],0
s_and_b32 s78,s54,127
R 66,2,8192
v_and_b32 v107,0xffff0000,v122
M 26,a[8:11],38
L 5
buffer_load_dword v8,s[36:39],s58 offen offset:0 sc1 lds
M 26,a[16:19],42
s_cmp_eq_u32 s78,s85
R 70,2,9216
v_lshlrev_b32 v108,16,v123
M 26,a[24:27],46
s_cselect_b64 s[40:41],-1,0
buffer_load_dwordx2 v[102:103],v11,s[36:39],s73 offen offset:256 sc1
M 30,a[20:23],42
s_add_u32 m0,s57,0
R 74,2,10240
v_cndmask_b32 v17,v15,v16,s[40:41]
M 30,a[28:31],46
L 5
s_add_u32 s56,s55,0
D 17,56,0
M 30,a[36:39],50
s_add_u32 s56,s55,256
R 78,2,11264
v_and_b32 v109,0xffff0000,v123
M 30,a[44:47],54
s_add_u32 s54,s54,s46
v_lshlrev_b32 v110,16,v124
v_and_b32 v111,0xffff0000,v124
M 26,a[32:35],50
s_and_b32 s79,s54,127
R 82,2,12288
v_lshlrev_b32 v112,16,v125
M 26,a[40:43],54
L 5
v_and_b32 v113,0xffff0000,v125
v_fma_f32 v106,v106,v99,v98
M 26,a[48:51],58
s_cmp_eq_u32 s79,0
R 86,2,13312
v_fma_f32 v107,v107,v99,v98
M 26,a[56:59],62
s_cselect_b32 s94,0x3300,0
D 17,56,1024
M 30,a[52:55],58
s_add_u32 s56,s55,512
R 90,2,14336
v_fma_f32 v108,v108,v99,v98
M 30,a[60:63],62
L 5
v_fma_f32 v109,v109,v99,v98
v_fma_f32 v110,v110,v99,v98
M 30,a[68:71],66
s_cmp_eq_u32 s79,127
R 94,2,15360
v_fma_f32 v111,v111,v99,v98
M 30,a[76:79],70
s_cselect_b32 s94,0xcc00,s94
v_fma_f32 v112,v112,v99,v98
v_fma_f32 v113,v113,v99,v98
M 26,a[64:67],66
s_add_u32 s79,s54,s46
R 34,2,16384
v_add_f32 v18,v18,v106
M 26,a[72:75],70
L 5
v_add_f32 v19,v19,v107
v_add_f32 v20,v20,v108
M 26,a[80:83],74
s_and_b32 s79,s79,127
R 38,2,17408
v_add_f32 v21,v21,v109
M 26,a[88:91],78
s_cmp_eq_u32 s79,0
D 17,56,2048
M 30,a[84:87],74
s_cselect_b32 s95,0x3300,0
R 42,2,18432
v_add_f32 v22,v22,v110
M 30,a[92:95],78
L 5
v_add_f32 v23,v23,v111
v_add_f32 v24,v24,v112
M 30,a[100:103],82
s_add_u32 s56,s55,768
R 46,2,19456
v_add_f32 v25,v25,v113
M 30,a[108:111],86
s_cmp_eq_u32 s79,127
v_cvt_pk_bf16_f32 v114,v18,v19
v_cvt_pk_bf16_f32 v115,v20,v21
M 26,a[96:99],82
s_cselect_b32 s95,0xcc00,s95
R 50,2,20480
v_fma_f32 v107,v18,v18,s87
M 26,a[104:107],86
L 5
)"
R"(D 17,56,3072
M 26,a[112:115],90
s_add_u32 m0,s57,4096
R 54,2,21504
v_add_f32 v106,v18,v19
M 26,a[120:123],94
s_add_u32 s56,s55,5120
v_fma_f32 v107,v19,v19,v107
v_add_f32 v106,v106,v20
M 30,a[116:119],90
s_add_u32 s57,s57,32768
R 58,2,22528
v_fma_f32 v107,v20,v20,v107
M 30,a[124:127],94
L 5
v_add_f32 v106,v106,v21
v_fma_f32 v107,v21,v21,v107
M 30,a[132:135],34
s_mov_b32 s67,s65
R 62,2,23552
v_add_f32 v106,v106,v22
M 30,a[140:143],38
s_mov_b32 s77,s75
v_fma_f32 v107,v22,v22,v107
v_add_f32 v106,v106,v23
M 26,a[128:131],34
s_add_u32 s73,s73,s64
R 66,2,24576
v_fma_f32 v107,v23,v23,v107
M 26,a[136:139],38
L 5
D 17,56,0
M 26,a[144:147],42
s_add_u32 s56,s55,5376
R 70,2,25600
v_add_f32 v106,v106,v24
M 26,a[152:155],46
s_add_u32 s74,s74,s62
v_fma_f32 v107,v24,v24,v107
v_add_f32 v106,v106,v25
v_fma_f32 v107,v25,v25,v107
M 30,a[148:151],42
s_and_b32 s57,s57,131071
R 74,2,26624
v_cvt_pk_bf16_f32 v116,v22,v23
M 30,a[156:159],46
L 5
v_permlane16_swap_b32 v106,v107
v_add_f32 v106,v106,v107
v_mov_b32 v107,v106
M 30,a[164:167],50
s_add_u32 s65,s65,s62
R 78,2,27648
v_cvt_pk_bf16_f32 v117,v24,v25
M 30,a[172:175],54
s_add_u32 s75,s75,s63
v_permlane32_swap_b32 v106,v107
v_add_f32 v106,v106,v107
M 26,a[160:163],50
R 82,2,28672
ds_write_b32 v12,v106 offset:0
M 26,a[168:171],54
L 6
D 17,56,1024
M 26,a[176:179],58
s_add_u32 s56,s55,5632
R 86,2,29696
M 26,a[184:187],62
buffer_store_dwordx4 v[114:117],v0,s[36:39],s66 offen offset:0 sc0 sc1
M 30,a[180:183],58
R 90,2,30720
M 30,a[188:191],62
L 6
L 2
M 30,a[196:199],66
s_waitcnt vmcnt(7)
R 94,2,31744
v_xor_b32 v2,65536,v2
M 30,a[204:207],70
s_barrier
D 17,56,2048
M 26,a[192:195],66
s_add_u32 s56,s55,5888
R 34,1,0
R 118,13,0
M 26,a[200:203],70
s_nop 0
buffer_store_dword v4,v3,s[36:39],s70 offen offset:0 sc0 sc1
M 26,a[208:211],74
s_mov_b32 s70,s69
R 38,1,1024
ds_read_b32 v7,v6 offset:1024
M 26,a[216:219],78
s_mov_b32 s69,s68
s_add_u32 s55,s55,s60
s_add_u32 s68,s68,s63
M 30,a[212:215],74
R 42,1,2048
M 30,a[220:223],78
L 7
D 17,56,3072
M 30,a[228:231],82
L 3
R 46,1,3072
v_add_f32 v118,v118,v119
M 30,a[236:239],86
v_add_f32 v120,v120,v121
v_add_f32 v118,v118,v120
M 26,a[224:227],82
L 2
R 50,1,4096
v_mul_f32 v118,s86,v118
M 26,a[232:235],86
s_nop 0
buffer_store_dword v118,v14,s[36:39],s76 offen offset:0 sc0 sc1
M 26,a[240:243],90
R 54,1,5120
v_cmp_eq_u32 vcc,s53,v7
M 26,a[248:251],94
s_or_b64 vcc,vcc,s[94:95]
s_cmp_eq_u32 vcc_lo,-1
M 30,a[244:247],90
R 58,1,6144
M 30,a[252:255],94
s_nop 0
s_cbranch_scc0 Lg2_N0_%=
Lg2_S0_%=:
v_mfma_f32_16x16x32_bf16 v[22:25],a[4:7],v[34:37],0
s_add_u32 s58,s58,s61
R 62,1,7168
v_lshlrev_b32 v106,16,v126
M 22,a[12:15],38
s_add_u32 m0,s59,0
buffer_load_dwordx4 v[122:125],v0,s[36:39],s74 offen offset:0 sc1
v_mfma_f32_16x16x32_bf16 v[18:21],a[0:3],v[34:37],0
s_and_b32 s78,s54,127
R 66,1,8192
v_and_b32 v107,0xffff0000,v126
M 18,a[8:11],38
L 5
buffer_load_dword v8,s[36:39],s58 offen offset:0 sc1 lds
M 18,a[16:19],42
s_cmp_eq_u32 s78,s85
R 70,1,9216
v_lshlrev_b32 v108,16,v127
M 18,a[24:27],46
s_cselect_b64 s[40:41],-1,0
buffer_load_dwordx2 v[98:99],v11,s[36:39],s73 offen offset:256 sc1
M 22,a[20:23],42
s_add_u32 m0,s57,0
R 74,1,10240
v_cndmask_b32 v17,v15,v16,s[40:41]
M 22,a[28:31],46
L 5
s_add_u32 s56,s55,0
D 17,56,0
M 22,a[36:39],50
s_add_u32 s56,s55,256
R 78,1,11264
v_and_b32 v109,0xffff0000,v127
M 22,a[44:47],54
s_add_u32 s54,s54,s46
v_lshlrev_b32 v110,16,v128
v_and_b32 v111,0xffff0000,v128
M 18,a[32:35],50
s_and_b32 s79,s54,127
R 82,1,12288
v_lshlrev_b32 v112,16,v129
M 18,a[40:43],54
L 5
v_and_b32 v113,0xffff0000,v129
v_fma_f32 v106,v106,v103,v102
M 18,a[48:51],58
s_cmp_eq_u32 s79,0
R 86,1,13312
v_fma_f32 v107,v107,v103,v102
M 18,a[56:59],62
s_cselect_b32 s94,0x3300,0
D 17,56,1024
M 22,a[52:55],58
s_add_u32 s56,s55,512
R 90,1,14336
v_fma_f32 v108,v108,v103,v102
M 22,a[60:63],62
L 5
v_fma_f32 v109,v109,v103,v102
v_fma_f32 v110,v110,v103,v102
M 22,a[68:71],66
s_cmp_eq_u32 s79,127
R 94,1,15360
v_fma_f32 v111,v111,v103,v102
M 22,a[76:79],70
s_cselect_b32 s94,0xcc00,s94
v_fma_f32 v112,v112,v103,v102
v_fma_f32 v113,v113,v103,v102
M 18,a[64:67],66
s_add_u32 s79,s54,s46
R 34,1,16384
v_add_f32 v26,v26,v106
M 18,a[72:75],70
L 5
v_add_f32 v27,v27,v107
v_add_f32 v28,v28,v108
M 18,a[80:83],74
s_and_b32 s79,s79,127
R 38,1,17408
v_add_f32 v29,v29,v109
M 18,a[88:91],78
s_cmp_eq_u32 s79,0
D 17,56,2048
M 22,a[84:87],74
s_cselect_b32 s95,0x3300,0
R 42,1,18432
v_add_f32 v30,v30,v110
M 22,a[92:95],78
L 5
v_add_f32 v31,v31,v111
v_add_f32 v32,v32,v112
M 22,a[100:103],82
s_add_u32 s56,s55,768
R 46,1,19456
v_add_f32 v33,v33,v113
M 22,a[108:111],86
s_cmp_eq_u32 s79,127
v_cvt_pk_bf16_f32 v114,v26,v27
v_cvt_pk_bf16_f32 v115,v28,v29
M 18,a[96:99],82
s_cselect_b32 s95,0xcc00,s95
R 50,1,20480
v_fma_f32 v107,v26,v26,s87
M 18,a[104:107],86
L 5
D 17,56,3072
M 18,a[112:115],90
s_add_u32 m0,s57,4096
R 54,1,21504
v_add_f32 v106,v26,v27
M 18,a[120:123],94
s_add_u32 s56,s55,5120
v_fma_f32 v107,v27,v27,v107
v_add_f32 v106,v106,v28
M 22,a[116:119],90
s_add_u32 s57,s57,32768
R 58,1,22528
v_fma_f32 v107,v28,v28,v107
M 22,a[124:127],94
L 5
v_add_f32 v106,v106,v29
v_fma_f32 v107,v29,v29,v107
M 22,a[132:135],34
s_mov_b32 s66,s65
R 62,1,23552
v_add_f32 v106,v106,v30
M 22,a[140:143],38
s_mov_b32 s76,s75
v_fma_f32 v107,v30,v30,v107
v_add_f32 v106,v106,v31
M 18,a[128:131],34
s_add_u32 s73,s73,s64
R 66,1,24576
v_fma_f32 v107,v31,v31,v107
M 18,a[136:139],38
L 5
D 17,56,0
M 18,a[144:147],42
s_add_u32 s56,s55,5376
R 70,1,25600
v_add_f32 v106,v106,v32
M 18,a[152:155],46
s_add_u32 s74,s74,s62
v_fma_f32 v107,v32,v32,v107
v_add_f32 v106,v106,v33
v_fma_f32 v107,v33,v33,v107
M 22,a[148:151],42
s_and_b32 s57,s57,131071
R 74,1,26624
v_cvt_pk_bf16_f32 v116,v30,v31
M 22,a[156:159],46
L 5
v_permlane16_swap_b32 v106,v107
v_add_f32 v106,v106,v107
v_mov_b32 v107,v106
M 22,a[164:167],50
s_add_u32 s65,s65,s62
R 78,1,27648
v_cvt_pk_bf16_f32 v117,v32,v33
M 22,a[172:175],54
s_add_u32 s75,s75,s63
v_permlane32_swap_b32 v106,v107
v_add_f32 v106,v106,v107
M 18,a[160:163],50
R 82,1,28672
ds_write_b32 v12,v106 offset:2048
M 18,a[168:171],54
L 6
D 17,56,1024
M 18,a[176:179],58
s_add_u32 s56,s55,5632
R 86,1,29696
M 18,a[184:187],62
buffer_store_dwordx4 v[114:117],v0,s[36:39],s67 offen offset:0 sc0 sc1
M 22,a[180:183],58
R 90,1,30720
M 22,a[188:191],62
L 6
L 2
M 22,a[196:199],66
s_waitcnt vmcnt(7)
R 94,1,31744
v_xor_b32 v1,65536,v1
M 22,a[204:207],70
s_barrier
D 17,56,2048
M 18,a[192:195],66
s_add_u32 s56,s55,5888
R 34,2,0
R 118,13,2048
M 18,a[200:203],70
s_nop 0
buffer_store_dword v4,v3,s[36:39],s70 offen offset:0 sc0 sc1
M 18,a[208:211],74
s_mov_b32 s70,s69
R 38,2,1024
ds_read_b32 v7,v6 offset:0
M 18,a[216:219],78
s_mov_b32 s69,s68
s_add_u32 s55,s55,s60
s_add_u32 s68,s68,s63
M 22,a[212:215],74
R 42,2,2048
M 22,a[220:223],78
L 7
D 17,56,3072
M 22,a[228:231],82
L 3
R 46,2,3072
v_add_f32 v118,v118,v119
M 22,a[236:239],86
v_add_f32 v120,v120,v121
v_add_f32 v118,v118,v120
M 18,a[224:227],82
L 2
R 50,2,4096
v_mul_f32 v118,s86,v118
M 18,a[232:235],86
s_nop 0
buffer_store_dword v118,v14,s[36:39],s77 offen offset:0 sc0 sc1
M 18,a[240:243],90
R 54,2,5120
v_cmp_eq_u32 vcc,s53,v7
M 18,a[248:251],94
s_or_b64 vcc,vcc,s[94:95]
s_cmp_eq_u32 vcc_lo,-1
M 22,a[244:247],90
R 58,2,6144
M 22,a[252:255],94
s_nop 0
s_cbranch_scc1 Lg2_S1_%=
Lg2_N1_%=:
v_mfma_f32_16x16x32_bf16 v[30:33],a[4:7],v[34:37],0
s_add_u32 m0,s59,1024
R 62,2,7168
v_lshlrev_b32 v106,16,v122
M 30,a[12:15],38
s_and_b32 s79,s54,127
buffer_load_dword v5,s[36:39],s58 offen offset:0 sc1 lds
v_mfma_f32_16x16x32_bf16 v[26:29],a[0:3],v[34:37],0
s_cmp_eq_u32 s79,0
R 66,2,8192
v_and_b32 v107,0xffff0000,v122
M 26,a[8:11],38
L 5
buffer_load_dwordx4 v[126:129],v0,s[36:39],s74 offen offset:0 sc1
M 26,a[16:19],42
s_cselect_b32 s94,0x3300,0
R 70,2,9216
v_lshlrev_b32 v108,16,v123
M 26,a[24:27],46
s_cmp_eq_u32 s79,127
buffer_load_dwordx2 v[102:103],v11,s[36:39],s73 offen offset:256 sc1
M 30,a[20:23],42
s_cselect_b32 s94,0xcc00,s94
R 74,2,10240
v_and_b32 v109,0xffff0000,v123
M 30,a[28:31],46
L 5
v_lshlrev_b32 v110,16,v124
v_and_b32 v111,0xffff0000,v124
M 30,a[36:39],50
s_add_u32 s79,s54,s46
R 78,2,11264
v_lshlrev_b32 v112,16,v125
M 30,a[44:47],54
s_and_b32 s79,s79,127
v_and_b32 v113,0xffff0000,v125
v_fma_f32 v106,v106,v99,v98
M 26,a[32:35],50
s_cmp_eq_u32 s79,0
R 82,2,12288
v_fma_f32 v107,v107,v99,v98
M 26,a[40:43],54
L 5
v_fma_f32 v108,v108,v99,v98
v_fma_f32 v109,v109,v99,v98
M 26,a[48:51],58
s_cselect_b32 s95,0x3300,0
R 86,2,13312
v_fma_f32 v110,v110,v99,v98
M 26,a[56:59],62
s_cmp_eq_u32 s79,127
v_fma_f32 v111,v111,v99,v98
v_fma_f32 v112,v112,v99,v98
M 30,a[52:55],58
s_cselect_b32 s95,0xcc00,s95
R 90,2,14336
v_fma_f32 v113,v113,v99,v98
M 30,a[60:63],62
L 5
v_add_f32 v18,v18,v106
v_add_f32 v19,v19,v107
M 30,a[68:71],66
s_mov_b32 s67,s65
R 94,2,15360
v_add_f32 v20,v20,v108
M 30,a[76:79],70
s_mov_b32 s77,s75
v_add_f32 v21,v21,v109
v_add_f32 v22,v22,v110
M 26,a[64:67],66
s_add_u32 s73,s73,s64
R 34,2,16384
v_add_f32 v23,v23,v111
M 26,a[72:75],70
L 5
v_add_f32 v24,v24,v112
v_add_f32 v25,v25,v113
M 26,a[80:83],74
s_add_u32 s74,s74,s62
R 38,2,17408
v_cvt_pk_bf16_f32 v114,v18,v19
M 26,a[88:91],78
s_add_u32 s65,s65,s62
v_cvt_pk_bf16_f32 v115,v20,v21
v_cvt_pk_bf16_f32 v116,v22,v23
M 30,a[84:87],74
s_add_u32 s75,s75,s63
R 42,2,18432
v_cvt_pk_bf16_f32 v117,v24,v25
M 30,a[92:95],78
L 5
buffer_store_dwordx4 v[114:117],v0,s[36:39],s66 offen offset:0 sc0 sc1
v_add_f32 v106,v18,v19
M 30,a[100:103],82
R 46,2,19456
v_fma_f32 v107,v18,v18,s87
M 30,a[108:111],86
v_fma_f32 v107,v19,v19,v107
v_add_f32 v106,v106,v20
M 26,a[96:99],82
R 50,2,20480
v_fma_f32 v107,v20,v20,v107
M 26,a[104:107],86
L 5
v_add_f32 v106,v106,v21
v_fma_f32 v107,v21,v21,v107
M 26,a[112:115],90
R 54,2,21504
v_add_f32 v106,v106,v22
M 26,a[120:123],94
v_fma_f32 v107,v22,v22,v107
v_add_f32 v106,v106,v23
M 30,a[116:119],90
R 58,2,22528
v_fma_f32 v107,v23,v23,v107
M 30,a[124:127],94
L 5
v_add_f32 v106,v106,v24
v_fma_f32 v107,v24,v24,v107
M 30,a[132:135],34
R 62,2,23552
v_add_f32 v106,v106,v25
M 30,a[140:143],38
v_fma_f32 v107,v25,v25,v107
M 26,a[128:131],34
R 66,2,24576
M 26,a[136:139],38
L 5
v_permlane16_swap_b32 v106,v107
M 26,a[144:147],42
R 70,2,25600
v_add_f32 v106,v106,v107
M 26,a[152:155],46
v_mov_b32 v107,v106
M 30,a[148:151],42
R 74,2,26624
M 30,a[156:159],46
L 5
v_permlane32_swap_b32 v106,v107
M 30,a[164:167],50
R 78,2,27648
)"
R"(v_add_f32 v106,v106,v107
M 30,a[172:175],54
ds_write_b32 v12,v106 offset:0
M 26,a[160:163],50
R 82,2,28672
M 26,a[168:171],54
L 6
M 26,a[176:179],58
R 86,2,29696
M 26,a[184:187],62
M 30,a[180:183],58
R 90,2,30720
M 30,a[188:191],62
L 6
L 3
M 30,a[196:199],66
s_waitcnt vmcnt(1)
R 94,2,31744
v_xor_b32 v2,65536,v2
M 30,a[204:207],70
s_barrier
buffer_store_dword v4,v3,s[36:39],s70 offen offset:0 sc0 sc1
M 26,a[192:195],66
s_mov_b32 s70,s69
R 34,1,0
R 118,13,0
M 26,a[200:203],70
s_nop 0
s_mov_b32 s69,s68
ds_read_b32 v7,v6 offset:1024
M 26,a[208:211],74
s_add_u32 s68,s68,s63
R 38,1,1024
M 26,a[216:219],78
M 30,a[212:215],74
R 42,1,2048
M 30,a[220:223],78
L 7
M 30,a[228:231],82
L 3
R 46,1,3072
v_add_f32 v118,v118,v119
M 30,a[236:239],86
L 3
v_add_f32 v120,v120,v121
v_add_f32 v118,v118,v120
M 26,a[224:227],82
R 50,1,4096
v_mul_f32 v118,s86,v118
M 26,a[232:235],86
s_nop 0
buffer_store_dword v118,v14,s[36:39],s76 offen offset:0 sc0 sc1
M 26,a[240:243],90
R 54,1,5120
v_cmp_eq_u32 vcc,s53,v7
M 26,a[248:251],94
s_or_b64 vcc,vcc,s[94:95]
s_cmp_eq_u64 vcc,-1
M 30,a[244:247],90
R 58,1,6144
M 30,a[252:255],94
L 5
s_cbranch_scc1 Lg2_L20_%=
s_cmp_eq_u32 vcc_lo,-1
s_cbranch_scc1 Lg2_L10_%=
Lg2_X0_%=:
v_mfma_f32_16x16x32_bf16 v[22:25],a[4:7],v[34:37],0
s_add_u32 m0,s59,0
R 62,1,7168
v_lshlrev_b32 v106,16,v126
M 22,a[12:15],38
s_and_b32 s79,s54,127
buffer_load_dword v5,s[36:39],s58 offen offset:0 sc1 lds
v_mfma_f32_16x16x32_bf16 v[18:21],a[0:3],v[34:37],0
s_cmp_eq_u32 s79,0
R 66,1,8192
v_and_b32 v107,0xffff0000,v126
M 18,a[8:11],38
L 5
buffer_load_dwordx4 v[122:125],v0,s[36:39],s74 offen offset:0 sc1
M 18,a[16:19],42
s_cselect_b32 s94,0x3300,0
R 70,1,9216
v_lshlrev_b32 v108,16,v127
M 18,a[24:27],46
s_cmp_eq_u32 s79,127
buffer_load_dwordx2 v[98:99],v11,s[36:39],s73 offen offset:256 sc1
M 22,a[20:23],42
s_cselect_b32 s94,0xcc00,s94
R 74,1,10240
v_and_b32 v109,0xffff0000,v127
M 22,a[28:31],46
L 5
v_lshlrev_b32 v110,16,v128
v_and_b32 v111,0xffff0000,v128
M 22,a[36:39],50
s_add_u32 s79,s54,s46
R 78,1,11264
v_lshlrev_b32 v112,16,v129
M 22,a[44:47],54
s_and_b32 s79,s79,127
v_and_b32 v113,0xffff0000,v129
v_fma_f32 v106,v106,v103,v102
M 18,a[32:35],50
s_cmp_eq_u32 s79,0
R 82,1,12288
v_fma_f32 v107,v107,v103,v102
M 18,a[40:43],54
L 5
v_fma_f32 v108,v108,v103,v102
v_fma_f32 v109,v109,v103,v102
M 18,a[48:51],58
s_cselect_b32 s95,0x3300,0
R 86,1,13312
v_fma_f32 v110,v110,v103,v102
M 18,a[56:59],62
s_cmp_eq_u32 s79,127
v_fma_f32 v111,v111,v103,v102
v_fma_f32 v112,v112,v103,v102
M 22,a[52:55],58
s_cselect_b32 s95,0xcc00,s95
R 90,1,14336
v_fma_f32 v113,v113,v103,v102
M 22,a[60:63],62
L 5
v_add_f32 v26,v26,v106
v_add_f32 v27,v27,v107
M 22,a[68:71],66
s_mov_b32 s66,s65
R 94,1,15360
v_add_f32 v28,v28,v108
M 22,a[76:79],70
s_mov_b32 s76,s75
v_add_f32 v29,v29,v109
v_add_f32 v30,v30,v110
M 18,a[64:67],66
s_add_u32 s73,s73,s64
R 34,1,16384
v_add_f32 v31,v31,v111
M 18,a[72:75],70
L 5
v_add_f32 v32,v32,v112
v_add_f32 v33,v33,v113
M 18,a[80:83],74
s_add_u32 s74,s74,s62
R 38,1,17408
v_cvt_pk_bf16_f32 v114,v26,v27
M 18,a[88:91],78
s_add_u32 s65,s65,s62
v_cvt_pk_bf16_f32 v115,v28,v29
v_cvt_pk_bf16_f32 v116,v30,v31
M 22,a[84:87],74
s_add_u32 s75,s75,s63
R 42,1,18432
v_cvt_pk_bf16_f32 v117,v32,v33
M 22,a[92:95],78
L 5
buffer_store_dwordx4 v[114:117],v0,s[36:39],s67 offen offset:0 sc0 sc1
v_add_f32 v106,v26,v27
M 22,a[100:103],82
R 46,1,19456
v_fma_f32 v107,v26,v26,s87
M 22,a[108:111],86
v_fma_f32 v107,v27,v27,v107
v_add_f32 v106,v106,v28
M 18,a[96:99],82
R 50,1,20480
v_fma_f32 v107,v28,v28,v107
M 18,a[104:107],86
L 5
v_add_f32 v106,v106,v29
v_fma_f32 v107,v29,v29,v107
M 18,a[112:115],90
R 54,1,21504
v_add_f32 v106,v106,v30
M 18,a[120:123],94
v_fma_f32 v107,v30,v30,v107
v_add_f32 v106,v106,v31
M 22,a[116:119],90
R 58,1,22528
v_fma_f32 v107,v31,v31,v107
M 22,a[124:127],94
L 5
v_add_f32 v106,v106,v32
v_fma_f32 v107,v32,v32,v107
M 22,a[132:135],34
R 62,1,23552
v_add_f32 v106,v106,v33
M 22,a[140:143],38
v_fma_f32 v107,v33,v33,v107
M 18,a[128:131],34
R 66,1,24576
M 18,a[136:139],38
L 5
v_permlane16_swap_b32 v106,v107
M 18,a[144:147],42
R 70,1,25600
v_add_f32 v106,v106,v107
M 18,a[152:155],46
v_mov_b32 v107,v106
M 22,a[148:151],42
R 74,1,26624
M 22,a[156:159],46
L 5
v_permlane32_swap_b32 v106,v107
M 22,a[164:167],50
R 78,1,27648
v_add_f32 v106,v106,v107
M 22,a[172:175],54
ds_write_b32 v12,v106 offset:2048
M 18,a[160:163],50
R 82,1,28672
M 18,a[168:171],54
L 6
M 18,a[176:179],58
R 86,1,29696
M 18,a[184:187],62
M 22,a[180:183],58
R 90,1,30720
M 22,a[188:191],62
L 6
L 3
M 22,a[196:199],66
s_waitcnt vmcnt(1)
R 94,1,31744
v_xor_b32 v1,65536,v1
M 22,a[204:207],70
s_barrier
buffer_store_dword v4,v3,s[36:39],s70 offen offset:0 sc0 sc1
M 18,a[192:195],66
s_mov_b32 s70,s69
R 118,13,2048
ds_read_b32 v7,v6 offset:0
M 18,a[200:203],70
s_nop 0
s_mov_b32 s69,s68
s_add_u32 s68,s68,s63
M 18,a[208:211],74
M 18,a[216:219],78
M 22,a[212:215],74
M 22,a[220:223],78
L 4
M 22,a[228:231],82
L 1
v_add_f32 v118,v118,v119
v_add_f32 v120,v120,v121
M 22,a[236:239],86
L 0
v_add_f32 v118,v118,v120
v_mul_f32 v118,s86,v118
M 18,a[224:227],82
buffer_store_dword v118,v14,s[36:39],s77 offen offset:0 sc0 sc1
M 18,a[232:235],86
s_nop 0
v_cmp_eq_u32 vcc,s53,v7
M 18,a[240:243],90
s_or_b64 vcc,vcc,s[94:95]
M 18,a[248:251],94
M 22,a[244:247],90
M 22,a[252:255],94
s_mov_b32 s80,1
s_cmp_eq_u32 vcc_lo,-1
s_cbranch_scc0 Lg2_xn0_%=
s_and_b32 s78,s54,127
s_cmp_eq_u32 s78,s85
s_cselect_b64 s[40:41],-1,0
v_cndmask_b32 v17,v15,v16,s[40:41]
s_add_u32 m0,s57,0
s_add_u32 s56,s55,0
D 17,56,0
v_lshlrev_b32 v106,16,v122
v_and_b32 v107,0xffff0000,v122
v_lshlrev_b32 v108,16,v123
v_and_b32 v109,0xffff0000,v123
v_lshlrev_b32 v110,16,v124
s_add_u32 s56,s55,256
D 17,56,1024
v_and_b32 v111,0xffff0000,v124
v_lshlrev_b32 v112,16,v125
v_and_b32 v113,0xffff0000,v125
v_fma_f32 v106,v106,v99,v98
v_fma_f32 v107,v107,v99,v98
s_add_u32 s56,s55,512
D 17,56,2048
v_fma_f32 v108,v108,v99,v98
v_fma_f32 v109,v109,v99,v98
v_fma_f32 v110,v110,v99,v98
v_fma_f32 v111,v111,v99,v98
v_fma_f32 v112,v112,v99,v98
s_add_u32 s56,s55,768
D 17,56,3072
v_fma_f32 v113,v113,v99,v98
v_add_f32 v18,v18,v106
v_add_f32 v19,v19,v107
v_add_f32 v20,v20,v108
v_add_f32 v21,v21,v109
s_add_u32 m0,s57,4096
s_add_u32 s56,s55,5120
D 17,56,0
v_add_f32 v22,v22,v110
v_add_f32 v23,v23,v111
v_add_f32 v24,v24,v112
v_add_f32 v25,v25,v113
v_cvt_pk_bf16_f32 v114,v18,v19
s_add_u32 s56,s55,5376
D 17,56,1024
v_cvt_pk_bf16_f32 v115,v20,v21
v_cvt_pk_bf16_f32 v116,v22,v23
v_cvt_pk_bf16_f32 v117,v24,v25
s_add_u32 s56,s55,5632
D 17,56,2048
s_add_u32 s56,s55,5888
D 17,56,3072
buffer_store_dwordx4 v[114:117],v0,s[36:39],s66 offen offset:0 sc0 sc1
v_add_f32 v106,v18,v19
v_fma_f32 v107,v18,v18,s87
s_add_u32 s55,s55,s60
s_add_u32 s57,s57,32768
s_and_b32 s57,s57,131071
s_add_u32 s54,s54,s46
v_fma_f32 v107,v19,v19,v107
v_add_f32 v106,v106,v20
v_fma_f32 v107,v20,v20,v107
v_add_f32 v106,v106,v21
v_fma_f32 v107,v21,v21,v107
v_add_f32 v106,v106,v22
v_fma_f32 v107,v22,v22,v107
v_add_f32 v106,v106,v23
v_fma_f32 v107,v23,v23,v107
v_add_f32 v106,v106,v24
v_fma_f32 v107,v24,v24,v107
v_add_f32 v106,v106,v25
v_fma_f32 v107,v25,v25,v107
s_nop 1
v_permlane16_swap_b32 v106,v107
v_add_f32 v106,v106,v107
v_mov_b32 v107,v106
s_nop 1
v_permlane32_swap_b32 v106,v107
v_add_f32 v106,v106,v107
ds_write_b32 v12,v106 offset:0
L 0
s_barrier
R 118,13,0
L 0
v_add_f32 v118,v118,v119
v_add_f32 v120,v120,v121
v_add_f32 v118,v118,v120
v_mul_f32 v118,s86,v118
buffer_store_dword v118,v14,s[36:39],s76 offen offset:0 sc0 sc1
s_mov_b32 s72,s69
s_mov_b32 s69,0x7fff0000
s_mov_b32 s66,0x7fff0000
s_mov_b32 s76,0x7fff0000
s_branch Lg2_gox_%=
Lg2_xn0_%=:
s_cmp_ge_u32 s54,256
s_cbranch_scc1 Lg2_fin0_%=
s_add_u32 m0,s59,4096
s_nop 0
buffer_load_dword v5,s[36:39],s58 offen offset:0 sc1 lds
buffer_store_dword v4,v3,s[36:39],s71 offen offset:0 sc0 sc1
s_add_u32 m0,s59,5120
s_nop 0
buffer_load_dword v5,s[36:39],s58 offen offset:0 sc1 lds
buffer_store_dword v4,v3,s[36:39],s71 offen offset:0 sc0 sc1
s_mov_b32 s83,0
v_lshlrev_b32 v106,16,v122
v_and_b32 v107,0xffff0000,v122
v_lshlrev_b32 v108,16,v123
v_and_b32 v109,0xffff0000,v123
v_lshlrev_b32 v110,16,v124
v_and_b32 v111,0xffff0000,v124
v_lshlrev_b32 v112,16,v125
v_and_b32 v113,0xffff0000,v125
v_fma_f32 v106,v106,v99,v98
v_fma_f32 v107,v107,v99,v98
v_fma_f32 v108,v108,v99,v98
v_fma_f32 v109,v109,v99,v98
v_fma_f32 v110,v110,v99,v98
v_fma_f32 v111,v111,v99,v98
v_fma_f32 v112,v112,v99,v98
v_fma_f32 v113,v113,v99,v98
v_add_f32 v18,v18,v106
v_add_f32 v19,v19,v107
v_add_f32 v20,v20,v108
v_add_f32 v21,v21,v109
v_add_f32 v22,v22,v110
v_add_f32 v23,v23,v111
v_add_f32 v24,v24,v112
v_add_f32 v25,v25,v113
v_cvt_pk_bf16_f32 v114,v18,v19
v_cvt_pk_bf16_f32 v115,v20,v21
v_cvt_pk_bf16_f32 v116,v22,v23
v_cvt_pk_bf16_f32 v117,v24,v25
buffer_store_dwordx4 v[114:117],v0,s[36:39],s66 offen offset:0 sc0 sc1
v_add_f32 v106,v18,v19
v_fma_f32 v107,v18,v18,s87
v_fma_f32 v107,v19,v19,v107
v_add_f32 v106,v106,v20
v_fma_f32 v107,v20,v20,v107
v_add_f32 v106,v106,v21
v_fma_f32 v107,v21,v21,v107
v_add_f32 v106,v106,v22
v_fma_f32 v107,v22,v22,v107
v_add_f32 v106,v106,v23
v_fma_f32 v107,v23,v23,v107
v_add_f32 v106,v106,v24
v_fma_f32 v107,v24,v24,v107
v_add_f32 v106,v106,v25
v_fma_f32 v107,v25,v25,v107
s_nop 1
v_permlane16_swap_b32 v106,v107
v_add_f32 v106,v106,v107
v_mov_b32 v107,v106
s_nop 1
v_permlane32_swap_b32 v106,v107
v_add_f32 v106,v106,v107
ds_write_b32 v12,v106 offset:0
L 0
s_barrier
R 118,13,0
L 0
v_add_f32 v118,v118,v119
v_add_f32 v120,v120,v121
v_add_f32 v118,v118,v120
v_mul_f32 v118,s86,v118
buffer_store_dword v118,v14,s[36:39],s76 offen offset:0 sc0 sc1
s_mov_b32 s72,s69
s_mov_b32 s69,0x7fff0000
s_mov_b32 s66,0x7fff0000
s_mov_b32 s76,0x7fff0000
s_branch Lg2_poll_%=
Lg2_fin0_%=:
v_lshlrev_b32 v106,16,v122
v_and_b32 v107,0xffff0000,v122
v_lshlrev_b32 v108,16,v123
v_and_b32 v109,0xffff0000,v123
v_lshlrev_b32 v110,16,v124
v_and_b32 v111,0xffff0000,v124
v_lshlrev_b32 v112,16,v125
v_and_b32 v113,0xffff0000,v125
v_fma_f32 v106,v106,v99,v98
v_fma_f32 v107,v107,v99,v98
v_fma_f32 v108,v108,v99,v98
v_fma_f32 v109,v109,v99,v98
v_fma_f32 v110,v110,v99,v98
v_fma_f32 v111,v111,v99,v98
v_fma_f32 v112,v112,v99,v98
v_fma_f32 v113,v113,v99,v98
v_add_f32 v18,v18,v106
v_add_f32 v19,v19,v107
v_add_f32 v20,v20,v108
v_add_f32 v21,v21,v109
v_add_f32 v22,v22,v110
v_add_f32 v23,v23,v111
v_add_f32 v24,v24,v112
v_add_f32 v25,v25,v113
v_cvt_pk_bf16_f32 v114,v18,v19
v_cvt_pk_bf16_f32 v115,v20,v21
v_cvt_pk_bf16_f32 v116,v22,v23
v_cvt_pk_bf16_f32 v117,v24,v25
buffer_store_dwordx4 v[114:117],v0,s[36:39],s66 offen offset:0 sc0 sc1
v_add_f32 v106,v18,v19
v_fma_f32 v107,v18,v18,s87
v_fma_f32 v107,v19,v19,v107
v_add_f32 v106,v106,v20
v_fma_f32 v107,v20,v20,v107
v_add_f32 v106,v106,v21
v_fma_f32 v107,v21,v21,v107
v_add_f32 v106,v106,v22
v_fma_f32 v107,v22,v22,v107
v_add_f32 v106,v106,v23
v_fma_f32 v107,v23,v23,v107
v_add_f32 v106,v106,v24
v_fma_f32 v107,v24,v24,v107
v_add_f32 v106,v106,v25
v_fma_f32 v107,v25,v25,v107
s_nop 1
v_permlane16_swap_b32 v106,v107
v_add_f32 v106,v106,v107
v_mov_b32 v107,v106
s_nop 1
v_permlane32_swap_b32 v106,v107
v_add_f32 v106,v106,v107
ds_write_b32 v12,v106 offset:0
L 0
s_barrier
R 118,13,0
L 0
v_add_f32 v118,v118,v119
v_add_f32 v120,v120,v121
v_add_f32 v118,v118,v120
v_mul_f32 v118,s86,v118
buffer_store_dword v118,v14,s[36:39],s76 offen offset:0 sc0 sc1
s_mov_b32 s72,s69
s_mov_b32 s69,0x7fff0000
s_mov_b32 s66,0x7fff0000
s_mov_b32 s76,0x7fff0000
s_branch Lg2_done_%=
)"
R"(Lg2_L21_%=:
v_mfma_f32_16x16x32_bf16 v[30:33],a[4:7],v[34:37],0
s_add_u32 s58,s58,s61
s_and_b32 s78,s54,127
s_add_u32 s58,s58,s61
R 62,2,7168
M 30,a[12:15],38
s_add_u32 m0,s59,1024
s_cmp_eq_u32 s78,s85
s_cselect_b64 s[40:41],-1,0
buffer_load_dwordx4 v[126:129],v0,s[36:39],s74 offen offset:0 sc1
v_mfma_f32_16x16x32_bf16 v[26:29],a[0:3],v[34:37],0
s_add_u32 s56,s55,0
R 66,2,8192
v_cndmask_b32 v17,v15,v16,s[40:41]
M 26,a[8:11],38
L 5
buffer_load_dword v8,s[36:39],s58 offen offset:0 sc1 lds
M 26,a[16:19],42
s_add_u32 m0,s57,0
R 70,2,9216
v_lshlrev_b32 v106,16,v122
M 26,a[24:27],46
s_add_u32 s54,s54,s46
buffer_load_dwordx2 v[102:103],v11,s[36:39],s73 offen offset:256 sc1
M 30,a[20:23],42
s_and_b32 s78,s54,127
R 74,2,10240
v_and_b32 v107,0xffff0000,v122
M 30,a[28:31],46
L 5
D 17,56,0
M 30,a[36:39],50
s_add_u32 s56,s55,256
R 78,2,11264
v_lshlrev_b32 v108,16,v123
M 30,a[44:47],54
s_cmp_eq_u32 s78,s85
v_and_b32 v109,0xffff0000,v123
v_lshlrev_b32 v110,16,v124
M 26,a[32:35],50
s_cselect_b64 s[40:41],-1,0
R 82,2,12288
v_and_b32 v111,0xffff0000,v124
M 26,a[40:43],54
L 5
D 17,56,1024
M 26,a[48:51],58
s_add_u32 s56,s55,512
R 86,2,13312
v_lshlrev_b32 v112,16,v125
M 26,a[56:59],62
s_add_u32 s54,s54,s46
v_and_b32 v113,0xffff0000,v125
v_fma_f32 v106,v106,v99,v98
M 30,a[52:55],58
s_and_b32 s79,s54,127
R 90,2,14336
v_fma_f32 v107,v107,v99,v98
M 30,a[60:63],62
L 5
D 17,56,2048
M 30,a[68:71],66
s_add_u32 s56,s55,768
R 94,2,15360
v_fma_f32 v108,v108,v99,v98
M 30,a[76:79],70
s_cmp_eq_u32 s79,0
v_fma_f32 v109,v109,v99,v98
v_fma_f32 v110,v110,v99,v98
M 26,a[64:67],66
s_cselect_b32 s94,0x3300,0
R 34,2,16384
v_fma_f32 v111,v111,v99,v98
M 26,a[72:75],70
L 5
D 17,56,3072
M 26,a[80:83],74
s_add_u32 m0,s57,4096
s_add_u32 s56,s55,5120
R 38,2,17408
M 26,a[88:91],78
s_add_u32 s57,s57,32768
v_fma_f32 v112,v112,v99,v98
v_fma_f32 v113,v113,v99,v98
M 30,a[84:87],74
s_and_b32 s57,s57,131071
R 42,2,18432
v_add_f32 v18,v18,v106
M 30,a[92:95],78
L 5
D 17,56,0
M 30,a[100:103],82
s_add_u32 s56,s55,5376
R 46,2,19456
v_add_f32 v19,v19,v107
M 30,a[108:111],86
s_cmp_eq_u32 s79,127
v_add_f32 v20,v20,v108
v_add_f32 v21,v21,v109
M 26,a[96:99],82
s_cselect_b32 s94,0xcc00,s94
R 50,2,20480
v_fma_f32 v107,v18,v18,s87
M 26,a[104:107],86
L 5
D 17,56,1024
M 26,a[112:115],90
s_add_u32 s56,s55,5632
R 54,2,21504
v_add_f32 v106,v18,v19
M 26,a[120:123],94
s_add_u32 s79,s54,s46
v_fma_f32 v107,v19,v19,v107
v_add_f32 v106,v106,v20
M 30,a[116:119],90
s_and_b32 s79,s79,127
R 58,2,22528
v_fma_f32 v107,v20,v20,v107
M 30,a[124:127],94
L 5
D 17,56,2048
M 30,a[132:135],34
s_add_u32 s56,s55,5888
s_add_u32 s55,s55,s60
R 62,2,23552
M 30,a[140:143],38
s_cmp_eq_u32 s79,0
v_add_f32 v22,v22,v110
v_add_f32 v106,v106,v21
M 26,a[128:131],34
s_cselect_b32 s95,0x3300,0
R 66,2,24576
v_fma_f32 v107,v21,v21,v107
M 26,a[136:139],38
L 5
D 17,56,3072
M 26,a[144:147],42
s_add_u32 m0,s57,0
s_add_u32 s56,s55,0
R 70,2,25600
M 26,a[152:155],46
s_cmp_eq_u32 s79,127
v_add_f32 v23,v23,v111
v_add_f32 v24,v24,v112
v_add_f32 v25,v25,v113
v_add_f32 v106,v106,v22
v_fma_f32 v107,v22,v22,v107
v_add_f32 v106,v106,v23
v_fma_f32 v107,v23,v23,v107
v_add_f32 v106,v106,v24
v_fma_f32 v107,v24,v24,v107
v_add_f32 v106,v106,v25
v_fma_f32 v107,v25,v25,v107
M 30,a[148:151],42
s_cselect_b32 s95,0xcc00,s95
R 74,2,26624
v_cndmask_b32 v17,v15,v16,s[40:41]
M 30,a[156:159],46
L 5
v_permlane16_swap_b32 v106,v107
v_add_f32 v106,v106,v107
v_mov_b32 v107,v106
M 30,a[164:167],50
s_mov_b32 s67,s65
R 78,2,27648
v_cvt_pk_bf16_f32 v114,v18,v19
M 30,a[172:175],54
s_mov_b32 s77,s75
v_permlane32_swap_b32 v106,v107
v_add_f32 v106,v106,v107
M 26,a[160:163],50
s_add_u32 s73,s73,s64
R 82,2,28672
ds_write_b32 v12,v106 offset:0
M 26,a[168:171],54
L 6
D 17,56,0
M 26,a[176:179],58
s_add_u32 s56,s55,256
R 86,2,29696
v_cvt_pk_bf16_f32 v115,v20,v21
M 26,a[184:187],62
s_add_u32 s74,s74,s62
D 17,56,1024
M 30,a[180:183],58
s_add_u32 s56,s55,512
R 90,2,30720
v_cvt_pk_bf16_f32 v116,v22,v23
M 30,a[188:191],62
L 6
L 2
D 17,56,2048
M 30,a[196:199],66
s_waitcnt vmcnt(3)
s_add_u32 s56,s55,768
R 94,2,31744
M 30,a[204:207],70
s_barrier
D 17,56,3072
M 26,a[192:195],66
s_add_u32 m0,s57,4096
s_add_u32 s56,s55,5120
R 34,1,0
M 26,a[200:203],70
s_nop 0
buffer_store_dword v4,v3,s[36:39],s70 offen offset:0 sc0 sc1
M 26,a[208:211],74
s_mov_b32 s70,s69
R 38,1,1024
R 118,13,0
M 26,a[216:219],78
s_add_u32 s57,s57,32768
D 17,56,0
M 30,a[212:215],74
s_add_u32 s56,s55,5376
R 42,1,2048
ds_read_b32 v7,v6 offset:1024
M 30,a[220:223],78
L 7
D 17,56,1024
M 30,a[228:231],82
s_add_u32 s56,s55,5632
R 46,1,3072
v_cvt_pk_bf16_f32 v117,v24,v25
M 30,a[236:239],86
s_mov_b32 s69,s68
D 17,56,2048
M 26,a[224:227],82
s_add_u32 s56,s55,5888
R 50,1,4096
v_xor_b32 v2,65536,v2
M 26,a[232:235],86
L 7
L 4
buffer_store_dwordx4 v[114:117],v0,s[36:39],s66 offen offset:0 sc0 sc1
M 26,a[240:243],90
L 2
R 54,1,5120
v_add_f32 v118,v118,v119
M 26,a[248:251],94
s_add_u32 s55,s55,s60
v_cmp_eq_u32 vcc,s53,v7
v_add_f32 v120,v120,v121
M 30,a[244:247],90
s_and_b32 s57,s57,131071
s_add_u32 s65,s65,s62
s_add_u32 s75,s75,s63
s_add_u32 s68,s68,s63
s_or_b64 vcc,vcc,s[94:95]
s_cmp_eq_u32 vcc_lo,-1
R 58,1,6144
v_add_f32 v118,v118,v120
v_mul_f32 v118,s86,v118
M 30,a[252:255],94
s_nop 0
D 17,56,3072
buffer_store_dword v118,v14,s[36:39],s76 offen offset:0 sc0 sc1
s_cbranch_scc1 Lg2_S0_%=
Lg2_N0_%=:
v_mfma_f32_16x16x32_bf16 v[22:25],a[4:7],v[34:37],0
s_add_u32 m0,s59,0
R 62,1,7168
v_lshlrev_b32 v106,16,v126
M 22,a[12:15],38
s_and_b32 s79,s54,127
buffer_load_dword v5,s[36:39],s58 offen offset:0 sc1 lds
v_mfma_f32_16x16x32_bf16 v[18:21],a[0:3],v[34:37],0
s_cmp_eq_u32 s79,0
R 66,1,8192
v_and_b32 v107,0xffff0000,v126
M 18,a[8:11],38
L 5
buffer_load_dwordx4 v[122:125],v0,s[36:39],s74 offen offset:0 sc1
M 18,a[16:19],42
s_cselect_b32 s94,0x3300,0
R 70,1,9216
v_lshlrev_b32 v108,16,v127
M 18,a[24:27],46
s_cmp_eq_u32 s79,127
buffer_load_dwordx2 v[98:99],v11,s[36:39],s73 offen offset:256 sc1
M 22,a[20:23],42
s_cselect_b32 s94,0xcc00,s94
R 74,1,10240
v_and_b32 v109,0xffff0000,v127
M 22,a[28:31],46
L 5
v_lshlrev_b32 v110,16,v128
v_and_b32 v111,0xffff0000,v128
M 22,a[36:39],50
s_add_u32 s79,s54,s46
R 78,1,11264
v_lshlrev_b32 v112,16,v129
M 22,a[44:47],54
s_and_b32 s79,s79,127
v_and_b32 v113,0xffff0000,v129
v_fma_f32 v106,v106,v103,v102
M 18,a[32:35],50
s_cmp_eq_u32 s79,0
R 82,1,12288
v_fma_f32 v107,v107,v103,v102
M 18,a[40:43],54
L 5
v_fma_f32 v108,v108,v103,v102
v_fma_f32 v109,v109,v103,v102
M 18,a[48:51],58
s_cselect_b32 s95,0x3300,0
R 86,1,13312
v_fma_f32 v110,v110,v103,v102
M 18,a[56:59],62
s_cmp_eq_u32 s79,127
v_fma_f32 v111,v111,v103,v102
v_fma_f32 v112,v112,v103,v102
M 22,a[52:55],58
s_cselect_b32 s95,0xcc00,s95
R 90,1,14336
v_fma_f32 v113,v113,v103,v102
M 22,a[60:63],62
L 5
v_add_f32 v26,v26,v106
v_add_f32 v27,v27,v107
M 22,a[68:71],66
s_mov_b32 s66,s65
R 94,1,15360
v_add_f32 v28,v28,v108
M 22,a[76:79],70
s_mov_b32 s76,s75
v_add_f32 v29,v29,v109
v_add_f32 v30,v30,v110
M 18,a[64:67],66
s_add_u32 s73,s73,s64
R 34,1,16384
v_add_f32 v31,v31,v111
M 18,a[72:75],70
L 5
v_add_f32 v32,v32,v112
v_add_f32 v33,v33,v113
M 18,a[80:83],74
s_add_u32 s74,s74,s62
R 38,1,17408
v_cvt_pk_bf16_f32 v114,v26,v27
M 18,a[88:91],78
s_add_u32 s65,s65,s62
v_cvt_pk_bf16_f32 v115,v28,v29
v_cvt_pk_bf16_f32 v116,v30,v31
M 22,a[84:87],74
s_add_u32 s75,s75,s63
R 42,1,18432
v_cvt_pk_bf16_f32 v117,v32,v33
M 22,a[92:95],78
L 5
buffer_store_dwordx4 v[114:117],v0,s[36:39],s67 offen offset:0 sc0 sc1
v_add_f32 v106,v26,v27
M 22,a[100:103],82
R 46,1,19456
v_fma_f32 v107,v26,v26,s87
M 22,a[108:111],86
v_fma_f32 v107,v27,v27,v107
v_add_f32 v106,v106,v28
M 18,a[96:99],82
R 50,1,20480
v_fma_f32 v107,v28,v28,v107
M 18,a[104:107],86
L 5
v_add_f32 v106,v106,v29
v_fma_f32 v107,v29,v29,v107
M 18,a[112:115],90
R 54,1,21504
v_add_f32 v106,v106,v30
M 18,a[120:123],94
v_fma_f32 v107,v30,v30,v107
v_add_f32 v106,v106,v31
M 22,a[116:119],90
R 58,1,22528
v_fma_f32 v107,v31,v31,v107
M 22,a[124:127],94
L 5
v_add_f32 v106,v106,v32
v_fma_f32 v107,v32,v32,v107
M 22,a[132:135],34
R 62,1,23552
v_add_f32 v106,v106,v33
M 22,a[140:143],38
v_fma_f32 v107,v33,v33,v107
M 18,a[128:131],34
R 66,1,24576
M 18,a[136:139],38
L 5
v_permlane16_swap_b32 v106,v107
M 18,a[144:147],42
R 70,1,25600
v_add_f32 v106,v106,v107
M 18,a[152:155],46
v_mov_b32 v107,v106
M 22,a[148:151],42
R 74,1,26624
M 22,a[156:159],46
L 5
v_permlane32_swap_b32 v106,v107
M 22,a[164:167],50
R 78,1,27648
v_add_f32 v106,v106,v107
M 22,a[172:175],54
ds_write_b32 v12,v106 offset:2048
M 18,a[160:163],50
R 82,1,28672
M 18,a[168:171],54
L 6
M 18,a[176:179],58
R 86,1,29696
M 18,a[184:187],62
M 22,a[180:183],58
R 90,1,30720
M 22,a[188:191],62
L 6
L 3
M 22,a[196:199],66
s_waitcnt vmcnt(1)
R 94,1,31744
v_xor_b32 v1,65536,v1
M 22,a[204:207],70
s_barrier
buffer_store_dword v4,v3,s[36:39],s70 offen offset:0 sc0 sc1
M 18,a[192:195],66
s_mov_b32 s70,s69
R 34,2,0
R 118,13,2048
M 18,a[200:203],70
s_nop 0
s_mov_b32 s69,s68
ds_read_b32 v7,v6 offset:0
M 18,a[208:211],74
s_add_u32 s68,s68,s63
R 38,2,1024
M 18,a[216:219],78
M 22,a[212:215],74
R 42,2,2048
M 22,a[220:223],78
L 7
M 22,a[228:231],82
L 3
R 46,2,3072
v_add_f32 v118,v118,v119
M 22,a[236:239],86
L 3
v_add_f32 v120,v120,v121
v_add_f32 v118,v118,v120
M 18,a[224:227],82
R 50,2,4096
v_mul_f32 v118,s86,v118
M 18,a[232:235],86
s_nop 0
buffer_store_dword v118,v14,s[36:39],s77 offen offset:0 sc0 sc1
M 18,a[240:243],90
R 54,2,5120
v_cmp_eq_u32 vcc,s53,v7
M 18,a[248:251],94
s_or_b64 vcc,vcc,s[94:95]
s_cmp_eq_u64 vcc,-1
M 22,a[244:247],90
R 58,2,6144
M 22,a[252:255],94
L 5
s_cbranch_scc1 Lg2_L21_%=
s_cmp_eq_u32 vcc_lo,-1
s_cbranch_scc1 Lg2_L11_%=
Lg2_X1_%=:
v_mfma_f32_16x16x32_bf16 v[30:33],a[4:7],v[34:37],0
s_add_u32 m0,s59,1024
R 62,2,7168
v_lshlrev_b32 v106,16,v122
M 30,a[12:15],38
s_and_b32 s79,s54,127
buffer_load_dword v5,s[36:39],s58 offen offset:0 sc1 lds
v_mfma_f32_16x16x32_bf16 v[26:29],a[0:3],v[34:37],0
s_cmp_eq_u32 s79,0
R 66,2,8192
v_and_b32 v107,0xffff0000,v122
M 26,a[8:11],38
L 5
buffer_load_dwordx4 v[126:129],v0,s[36:39],s74 offen offset:0 sc1
M 26,a[16:19],42
s_cselect_b32 s94,0x3300,0
R 70,2,9216
v_lshlrev_b32 v108,16,v123
M 26,a[24:27],46
s_cmp_eq_u32 s79,127
buffer_load_dwordx2 v[102:103],v11,s[36:39],s73 offen offset:256 sc1
M 30,a[20:23],42
s_cselect_b32 s94,0xcc00,s94
R 74,2,10240
v_and_b32 v109,0xffff0000,v123
M 30,a[28:31],46
L 5
v_lshlrev_b32 v110,16,v124
v_and_b32 v111,0xffff0000,v124
M 30,a[36:39],50
s_add_u32 s79,s54,s46
R 78,2,11264
v_lshlrev_b32 v112,16,v125
)"
R"(M 30,a[44:47],54
s_and_b32 s79,s79,127
v_and_b32 v113,0xffff0000,v125
v_fma_f32 v106,v106,v99,v98
M 26,a[32:35],50
s_cmp_eq_u32 s79,0
R 82,2,12288
v_fma_f32 v107,v107,v99,v98
M 26,a[40:43],54
L 5
v_fma_f32 v108,v108,v99,v98
v_fma_f32 v109,v109,v99,v98
M 26,a[48:51],58
s_cselect_b32 s95,0x3300,0
R 86,2,13312
v_fma_f32 v110,v110,v99,v98
M 26,a[56:59],62
s_cmp_eq_u32 s79,127
v_fma_f32 v111,v111,v99,v98
v_fma_f32 v112,v112,v99,v98
M 30,a[52:55],58
s_cselect_b32 s95,0xcc00,s95
R 90,2,14336
v_fma_f32 v113,v113,v99,v98
M 30,a[60:63],62
L 5
v_add_f32 v18,v18,v106
v_add_f32 v19,v19,v107
M 30,a[68:71],66
s_mov_b32 s67,s65
R 94,2,15360
v_add_f32 v20,v20,v108
M 30,a[76:79],70
s_mov_b32 s77,s75
v_add_f32 v21,v21,v109
v_add_f32 v22,v22,v110
M 26,a[64:67],66
s_add_u32 s73,s73,s64
R 34,2,16384
v_add_f32 v23,v23,v111
M 26,a[72:75],70
L 5
v_add_f32 v24,v24,v112
v_add_f32 v25,v25,v113
M 26,a[80:83],74
s_add_u32 s74,s74,s62
R 38,2,17408
v_cvt_pk_bf16_f32 v114,v18,v19
M 26,a[88:91],78
s_add_u32 s65,s65,s62
v_cvt_pk_bf16_f32 v115,v20,v21
v_cvt_pk_bf16_f32 v116,v22,v23
M 30,a[84:87],74
s_add_u32 s75,s75,s63
R 42,2,18432
v_cvt_pk_bf16_f32 v117,v24,v25
M 30,a[92:95],78
L 5
buffer_store_dwordx4 v[114:117],v0,s[36:39],s66 offen offset:0 sc0 sc1
v_add_f32 v106,v18,v19
M 30,a[100:103],82
R 46,2,19456
v_fma_f32 v107,v18,v18,s87
M 30,a[108:111],86
v_fma_f32 v107,v19,v19,v107
v_add_f32 v106,v106,v20
M 26,a[96:99],82
R 50,2,20480
v_fma_f32 v107,v20,v20,v107
M 26,a[104:107],86
L 5
v_add_f32 v106,v106,v21
v_fma_f32 v107,v21,v21,v107
M 26,a[112:115],90
R 54,2,21504
v_add_f32 v106,v106,v22
M 26,a[120:123],94
v_fma_f32 v107,v22,v22,v107
v_add_f32 v106,v106,v23
M 30,a[116:119],90
R 58,2,22528
v_fma_f32 v107,v23,v23,v107
M 30,a[124:127],94
L 5
v_add_f32 v106,v106,v24
v_fma_f32 v107,v24,v24,v107
M 30,a[132:135],34
R 62,2,23552
v_add_f32 v106,v106,v25
M 30,a[140:143],38
v_fma_f32 v107,v25,v25,v107
M 26,a[128:131],34
R 66,2,24576
M 26,a[136:139],38
L 5
v_permlane16_swap_b32 v106,v107
M 26,a[144:147],42
R 70,2,25600
v_add_f32 v106,v106,v107
M 26,a[152:155],46
v_mov_b32 v107,v106
M 30,a[148:151],42
R 74,2,26624
M 30,a[156:159],46
L 5
v_permlane32_swap_b32 v106,v107
M 30,a[164:167],50
R 78,2,27648
v_add_f32 v106,v106,v107
M 30,a[172:175],54
ds_write_b32 v12,v106 offset:0
M 26,a[160:163],50
R 82,2,28672
M 26,a[168:171],54
L 6
M 26,a[176:179],58
R 86,2,29696
M 26,a[184:187],62
M 30,a[180:183],58
R 90,2,30720
M 30,a[188:191],62
L 6
L 3
M 30,a[196:199],66
s_waitcnt vmcnt(1)
R 94,2,31744
v_xor_b32 v2,65536,v2
M 30,a[204:207],70
s_barrier
buffer_store_dword v4,v3,s[36:39],s70 offen offset:0 sc0 sc1
M 26,a[192:195],66
s_mov_b32 s70,s69
R 118,13,0
ds_read_b32 v7,v6 offset:1024
M 26,a[200:203],70
s_nop 0
s_mov_b32 s69,s68
s_add_u32 s68,s68,s63
M 26,a[208:211],74
M 26,a[216:219],78
M 30,a[212:215],74
M 30,a[220:223],78
L 4
M 30,a[228:231],82
L 1
v_add_f32 v118,v118,v119
v_add_f32 v120,v120,v121
M 30,a[236:239],86
L 0
v_add_f32 v118,v118,v120
v_mul_f32 v118,s86,v118
M 26,a[224:227],82
buffer_store_dword v118,v14,s[36:39],s76 offen offset:0 sc0 sc1
M 26,a[232:235],86
s_nop 0
v_cmp_eq_u32 vcc,s53,v7
M 26,a[240:243],90
s_or_b64 vcc,vcc,s[94:95]
M 26,a[248:251],94
M 30,a[244:247],90
M 30,a[252:255],94
s_mov_b32 s80,0
s_cmp_eq_u32 vcc_lo,-1
s_cbranch_scc0 Lg2_xn1_%=
s_and_b32 s78,s54,127
s_cmp_eq_u32 s78,s85
s_cselect_b64 s[40:41],-1,0
v_cndmask_b32 v17,v15,v16,s[40:41]
s_add_u32 m0,s57,0
s_add_u32 s56,s55,0
D 17,56,0
v_lshlrev_b32 v106,16,v126
v_and_b32 v107,0xffff0000,v126
v_lshlrev_b32 v108,16,v127
v_and_b32 v109,0xffff0000,v127
v_lshlrev_b32 v110,16,v128
s_add_u32 s56,s55,256
D 17,56,1024
v_and_b32 v111,0xffff0000,v128
v_lshlrev_b32 v112,16,v129
v_and_b32 v113,0xffff0000,v129
v_fma_f32 v106,v106,v103,v102
v_fma_f32 v107,v107,v103,v102
s_add_u32 s56,s55,512
D 17,56,2048
v_fma_f32 v108,v108,v103,v102
v_fma_f32 v109,v109,v103,v102
v_fma_f32 v110,v110,v103,v102
v_fma_f32 v111,v111,v103,v102
v_fma_f32 v112,v112,v103,v102
s_add_u32 s56,s55,768
D 17,56,3072
v_fma_f32 v113,v113,v103,v102
v_add_f32 v26,v26,v106
v_add_f32 v27,v27,v107
v_add_f32 v28,v28,v108
v_add_f32 v29,v29,v109
s_add_u32 m0,s57,4096
s_add_u32 s56,s55,5120
D 17,56,0
v_add_f32 v30,v30,v110
v_add_f32 v31,v31,v111
v_add_f32 v32,v32,v112
v_add_f32 v33,v33,v113
v_cvt_pk_bf16_f32 v114,v26,v27
s_add_u32 s56,s55,5376
D 17,56,1024
v_cvt_pk_bf16_f32 v115,v28,v29
v_cvt_pk_bf16_f32 v116,v30,v31
v_cvt_pk_bf16_f32 v117,v32,v33
s_add_u32 s56,s55,5632
D 17,56,2048
s_add_u32 s56,s55,5888
D 17,56,3072
buffer_store_dwordx4 v[114:117],v0,s[36:39],s67 offen offset:0 sc0 sc1
v_add_f32 v106,v26,v27
v_fma_f32 v107,v26,v26,s87
s_add_u32 s55,s55,s60
s_add_u32 s57,s57,32768
s_and_b32 s57,s57,131071
s_add_u32 s54,s54,s46
v_fma_f32 v107,v27,v27,v107
v_add_f32 v106,v106,v28
v_fma_f32 v107,v28,v28,v107
v_add_f32 v106,v106,v29
v_fma_f32 v107,v29,v29,v107
v_add_f32 v106,v106,v30
v_fma_f32 v107,v30,v30,v107
v_add_f32 v106,v106,v31
v_fma_f32 v107,v31,v31,v107
v_add_f32 v106,v106,v32
v_fma_f32 v107,v32,v32,v107
v_add_f32 v106,v106,v33
v_fma_f32 v107,v33,v33,v107
s_nop 1
v_permlane16_swap_b32 v106,v107
v_add_f32 v106,v106,v107
v_mov_b32 v107,v106
s_nop 1
v_permlane32_swap_b32 v106,v107
v_add_f32 v106,v106,v107
ds_write_b32 v12,v106 offset:2048
L 0
s_barrier
R 118,13,2048
L 0
v_add_f32 v118,v118,v119
v_add_f32 v120,v120,v121
v_add_f32 v118,v118,v120
v_mul_f32 v118,s86,v118
buffer_store_dword v118,v14,s[36:39],s77 offen offset:0 sc0 sc1
s_mov_b32 s72,s69
s_mov_b32 s69,0x7fff0000
s_mov_b32 s67,0x7fff0000
s_mov_b32 s77,0x7fff0000
s_branch Lg2_gox_%=
Lg2_xn1_%=:
s_cmp_ge_u32 s54,256
s_cbranch_scc1 Lg2_fin1_%=
s_add_u32 m0,s59,4096
s_nop 0
buffer_load_dword v5,s[36:39],s58 offen offset:0 sc1 lds
buffer_store_dword v4,v3,s[36:39],s71 offen offset:0 sc0 sc1
s_add_u32 m0,s59,5120
s_nop 0
buffer_load_dword v5,s[36:39],s58 offen offset:0 sc1 lds
buffer_store_dword v4,v3,s[36:39],s71 offen offset:0 sc0 sc1
s_mov_b32 s83,0
v_lshlrev_b32 v106,16,v126
v_and_b32 v107,0xffff0000,v126
v_lshlrev_b32 v108,16,v127
v_and_b32 v109,0xffff0000,v127
v_lshlrev_b32 v110,16,v128
v_and_b32 v111,0xffff0000,v128
v_lshlrev_b32 v112,16,v129
v_and_b32 v113,0xffff0000,v129
v_fma_f32 v106,v106,v103,v102
v_fma_f32 v107,v107,v103,v102
v_fma_f32 v108,v108,v103,v102
v_fma_f32 v109,v109,v103,v102
v_fma_f32 v110,v110,v103,v102
v_fma_f32 v111,v111,v103,v102
v_fma_f32 v112,v112,v103,v102
v_fma_f32 v113,v113,v103,v102
v_add_f32 v26,v26,v106
v_add_f32 v27,v27,v107
v_add_f32 v28,v28,v108
v_add_f32 v29,v29,v109
v_add_f32 v30,v30,v110
v_add_f32 v31,v31,v111
v_add_f32 v32,v32,v112
v_add_f32 v33,v33,v113
v_cvt_pk_bf16_f32 v114,v26,v27
v_cvt_pk_bf16_f32 v115,v28,v29
v_cvt_pk_bf16_f32 v116,v30,v31
v_cvt_pk_bf16_f32 v117,v32,v33
buffer_store_dwordx4 v[114:117],v0,s[36:39],s67 offen offset:0 sc0 sc1
v_add_f32 v106,v26,v27
v_fma_f32 v107,v26,v26,s87
v_fma_f32 v107,v27,v27,v107
v_add_f32 v106,v106,v28
v_fma_f32 v107,v28,v28,v107
v_add_f32 v106,v106,v29
v_fma_f32 v107,v29,v29,v107
v_add_f32 v106,v106,v30
v_fma_f32 v107,v30,v30,v107
v_add_f32 v106,v106,v31
v_fma_f32 v107,v31,v31,v107
v_add_f32 v106,v106,v32
v_fma_f32 v107,v32,v32,v107
v_add_f32 v106,v106,v33
v_fma_f32 v107,v33,v33,v107
s_nop 1
v_permlane16_swap_b32 v106,v107
v_add_f32 v106,v106,v107
v_mov_b32 v107,v106
s_nop 1
v_permlane32_swap_b32 v106,v107
v_add_f32 v106,v106,v107
ds_write_b32 v12,v106 offset:2048
L 0
s_barrier
R 118,13,2048
L 0
v_add_f32 v118,v118,v119
v_add_f32 v120,v120,v121
v_add_f32 v118,v118,v120
v_mul_f32 v118,s86,v118
buffer_store_dword v118,v14,s[36:39],s77 offen offset:0 sc0 sc1
s_mov_b32 s72,s69
s_mov_b32 s69,0x7fff0000
s_mov_b32 s67,0x7fff0000
s_mov_b32 s77,0x7fff0000
s_branch Lg2_poll_%=
Lg2_fin1_%=:
v_lshlrev_b32 v106,16,v126
v_and_b32 v107,0xffff0000,v126
v_lshlrev_b32 v108,16,v127
v_and_b32 v109,0xffff0000,v127
v_lshlrev_b32 v110,16,v128
v_and_b32 v111,0xffff0000,v128
v_lshlrev_b32 v112,16,v129
v_and_b32 v113,0xffff0000,v129
v_fma_f32 v106,v106,v103,v102
v_fma_f32 v107,v107,v103,v102
v_fma_f32 v108,v108,v103,v102
v_fma_f32 v109,v109,v103,v102
v_fma_f32 v110,v110,v103,v102
v_fma_f32 v111,v111,v103,v102
v_fma_f32 v112,v112,v103,v102
v_fma_f32 v113,v113,v103,v102
v_add_f32 v26,v26,v106
v_add_f32 v27,v27,v107
v_add_f32 v28,v28,v108
v_add_f32 v29,v29,v109
v_add_f32 v30,v30,v110
v_add_f32 v31,v31,v111
v_add_f32 v32,v32,v112
v_add_f32 v33,v33,v113
v_cvt_pk_bf16_f32 v114,v26,v27
v_cvt_pk_bf16_f32 v115,v28,v29
v_cvt_pk_bf16_f32 v116,v30,v31
v_cvt_pk_bf16_f32 v117,v32,v33
buffer_store_dwordx4 v[114:117],v0,s[36:39],s67 offen offset:0 sc0 sc1
v_add_f32 v106,v26,v27
v_fma_f32 v107,v26,v26,s87
v_fma_f32 v107,v27,v27,v107
v_add_f32 v106,v106,v28
v_fma_f32 v107,v28,v28,v107
v_add_f32 v106,v106,v29
v_fma_f32 v107,v29,v29,v107
v_add_f32 v106,v106,v30
v_fma_f32 v107,v30,v30,v107
v_add_f32 v106,v106,v31
v_fma_f32 v107,v31,v31,v107
v_add_f32 v106,v106,v32
v_fma_f32 v107,v32,v32,v107
v_add_f32 v106,v106,v33
v_fma_f32 v107,v33,v33,v107
s_nop 1
v_permlane16_swap_b32 v106,v107
v_add_f32 v106,v106,v107
v_mov_b32 v107,v106
s_nop 1
v_permlane32_swap_b32 v106,v107
v_add_f32 v106,v106,v107
ds_write_b32 v12,v106 offset:2048
L 0
s_barrier
R 118,13,2048
L 0
v_add_f32 v118,v118,v119
v_add_f32 v120,v120,v121
v_add_f32 v118,v118,v120
v_mul_f32 v118,s86,v118
buffer_store_dword v118,v14,s[36:39],s77 offen offset:0 sc0 sc1
s_mov_b32 s72,s69
s_mov_b32 s69,0x7fff0000
s_mov_b32 s67,0x7fff0000
s_mov_b32 s77,0x7fff0000
s_branch Lg2_done_%=
Lg2_L10_%=:
v_mfma_f32_16x16x32_bf16 v[22:25],a[4:7],v[34:37],0
s_and_b32 s78,s54,127
s_add_u32 s58,s58,s61
s_add_u32 m0,s59,0
R 62,1,7168
M 22,a[12:15],38
s_cmp_eq_u32 s78,s85
s_cselect_b64 s[40:41],-1,0
s_add_u32 s56,s55,0
buffer_load_dword v5,s[36:39],s58 offen offset:0 sc1 lds
v_mfma_f32_16x16x32_bf16 v[18:21],a[0:3],v[34:37],0
s_add_u32 m0,s57,0
R 66,1,8192
v_cndmask_b32 v17,v15,v16,s[40:41]
M 18,a[8:11],38
L 5
buffer_load_dwordx4 v[122:125],v0,s[36:39],s74 offen offset:0 sc1
M 18,a[16:19],42
s_add_u32 s54,s54,s46
R 70,1,9216
v_lshlrev_b32 v106,16,v126
M 18,a[24:27],46
s_and_b32 s79,s54,127
buffer_load_dwordx2 v[98:99],v11,s[36:39],s73 offen offset:256 sc1
M 22,a[20:23],42
s_cmp_eq_u32 s79,0
R 74,1,10240
v_and_b32 v107,0xffff0000,v126
M 22,a[28:31],46
L 5
D 17,56,0
M 22,a[36:39],50
s_cselect_b32 s94,0x3300,0
s_add_u32 s56,s55,256
R 78,1,11264
M 22,a[44:47],54
s_cmp_eq_u32 s79,127
v_lshlrev_b32 v108,16,v127
v_and_b32 v109,0xffff0000,v127
M 18,a[32:35],50
s_cselect_b32 s94,0xcc00,s94
R 82,1,12288
v_lshlrev_b32 v110,16,v128
M 18,a[40:43],54
L 5
D 17,56,1024
M 18,a[48:51],58
s_add_u32 s56,s55,512
R 86,1,13312
v_and_b32 v111,0xffff0000,v128
M 18,a[56:59],62
s_add_u32 s79,s54,s46
v_lshlrev_b32 v112,16,v129
v_and_b32 v113,0xffff0000,v129
M 22,a[52:55],58
s_and_b32 s79,s79,127
R 90,1,14336
v_fma_f32 v106,v106,v103,v102
M 22,a[60:63],62
L 5
D 17,56,2048
M 22,a[68:71],66
s_add_u32 s56,s55,768
R 94,1,15360
v_fma_f32 v107,v107,v103,v102
M 22,a[76:79],70
s_cmp_eq_u32 s79,0
v_fma_f32 v108,v108,v103,v102
v_fma_f32 v109,v109,v103,v102
M 18,a[64:67],66
s_cselect_b32 s95,0x3300,0
R 34,1,16384
v_fma_f32 v110,v110,v103,v102
M 18,a[72:75],70
L 5
D 17,56,3072
M 18,a[80:83],74
s_add_u32 m0,s57,4096
s_add_u32 s56,s55,5120
R 38,1,17408
M 18,a[88:91],78
s_cmp_eq_u32 s79,127
v_fma_f32 v111,v111,v103,v102
v_fma_f32 v112,v112,v103,v102
M 22,a[84:87],74
s_cselect_b32 s95,0xcc00,s95
R 42,1,18432
v_fma_f32 v113,v113,v103,v102
M 22,a[92:95],78
L 5
D 17,56,0
M 22,a[100:103],82
s_add_u32 s56,s55,5376
R 46,1,19456
v_add_f32 v26,v26,v106
M 22,a[108:111],86
s_add_u32 s57,s57,32768
v_add_f32 v27,v27,v107
)"
R"(v_add_f32 v28,v28,v108
M 18,a[96:99],82
s_mov_b32 s66,s65
R 50,1,20480
v_fma_f32 v107,v26,v26,s87
M 18,a[104:107],86
L 5
D 17,56,1024
M 18,a[112:115],90
s_add_u32 s56,s55,5632
R 54,1,21504
v_add_f32 v106,v26,v27
M 18,a[120:123],94
s_mov_b32 s76,s75
v_fma_f32 v107,v27,v27,v107
v_add_f32 v29,v29,v109
M 22,a[116:119],90
s_add_u32 s73,s73,s64
R 58,1,22528
v_add_f32 v106,v106,v28
M 22,a[124:127],94
L 5
D 17,56,2048
M 22,a[132:135],34
s_add_u32 s56,s55,5888
R 62,1,23552
v_fma_f32 v107,v28,v28,v107
M 22,a[140:143],38
s_add_u32 s74,s74,s62
v_add_f32 v30,v30,v110
v_add_f32 v106,v106,v29
M 18,a[128:131],34
s_add_u32 s55,s55,s60
R 66,1,24576
v_fma_f32 v107,v29,v29,v107
M 18,a[136:139],38
L 5
D 17,56,3072
M 18,a[144:147],42
s_and_b32 s57,s57,131071
R 70,1,25600
v_add_f32 v31,v31,v111
M 18,a[152:155],46
s_add_u32 s65,s65,s62
v_add_f32 v32,v32,v112
v_add_f32 v33,v33,v113
v_add_f32 v106,v106,v30
v_fma_f32 v107,v30,v30,v107
v_add_f32 v106,v106,v31
v_fma_f32 v107,v31,v31,v107
v_add_f32 v106,v106,v32
v_fma_f32 v107,v32,v32,v107
v_add_f32 v106,v106,v33
v_fma_f32 v107,v33,v33,v107
M 22,a[148:151],42
s_add_u32 s75,s75,s63
R 74,1,26624
v_cvt_pk_bf16_f32 v114,v26,v27
M 22,a[156:159],46
L 5
v_permlane16_swap_b32 v106,v107
v_add_f32 v106,v106,v107
v_mov_b32 v107,v106
M 22,a[164:167],50
R 78,1,27648
v_cvt_pk_bf16_f32 v115,v28,v29
M 22,a[172:175],54
v_permlane32_swap_b32 v106,v107
v_add_f32 v106,v106,v107
M 18,a[160:163],50
R 82,1,28672
ds_write_b32 v12,v106 offset:2048
M 18,a[168:171],54
L 6
v_cvt_pk_bf16_f32 v116,v30,v31
v_cvt_pk_bf16_f32 v117,v32,v33
M 18,a[176:179],58
R 86,1,29696
M 18,a[184:187],62
buffer_store_dwordx4 v[114:117],v0,s[36:39],s67 offen offset:0 sc0 sc1
M 22,a[180:183],58
R 90,1,30720
M 22,a[188:191],62
L 6
L 2
M 22,a[196:199],66
s_waitcnt vmcnt(1)
R 94,1,31744
v_xor_b32 v1,65536,v1
M 22,a[204:207],70
s_barrier
buffer_store_dword v4,v3,s[36:39],s70 offen offset:0 sc0 sc1
M 18,a[192:195],66
s_mov_b32 s70,s69
R 34,2,0
R 118,13,2048
M 18,a[200:203],70
s_nop 0
s_mov_b32 s69,s68
ds_read_b32 v7,v6 offset:0
M 18,a[208:211],74
s_add_u32 s68,s68,s63
R 38,2,1024
M 18,a[216:219],78
M 22,a[212:215],74
R 42,2,2048
M 22,a[220:223],78
L 7
M 22,a[228:231],82
L 3
R 46,2,3072
v_add_f32 v118,v118,v119
M 22,a[236:239],86
L 3
v_add_f32 v120,v120,v121
v_add_f32 v118,v118,v120
M 18,a[224:227],82
R 50,2,4096
v_mul_f32 v118,s86,v118
M 18,a[232:235],86
s_nop 0
buffer_store_dword v118,v14,s[36:39],s77 offen offset:0 sc0 sc1
M 18,a[240:243],90
R 54,2,5120
v_cmp_eq_u32 vcc,s53,v7
M 18,a[248:251],94
s_or_b64 vcc,vcc,s[94:95]
s_cmp_eq_u64 vcc,-1
M 22,a[244:247],90
R 58,2,6144
M 22,a[252:255],94
L 5
s_cbranch_scc1 Lg2_L21_%=
s_cmp_eq_u32 vcc_lo,-1
s_cbranch_scc1 Lg2_L11_%=
s_branch Lg2_X1_%=
Lg2_L11_%=:
v_mfma_f32_16x16x32_bf16 v[30:33],a[4:7],v[34:37],0
s_and_b32 s78,s54,127
s_add_u32 s58,s58,s61
s_add_u32 m0,s59,1024
R 62,2,7168
M 30,a[12:15],38
s_cmp_eq_u32 s78,s85
s_cselect_b64 s[40:41],-1,0
s_add_u32 s56,s55,0
buffer_load_dword v5,s[36:39],s58 offen offset:0 sc1 lds
v_mfma_f32_16x16x32_bf16 v[26:29],a[0:3],v[34:37],0
s_add_u32 m0,s57,0
R 66,2,8192
v_cndmask_b32 v17,v15,v16,s[40:41]
M 26,a[8:11],38
L 5
buffer_load_dwordx4 v[126:129],v0,s[36:39],s74 offen offset:0 sc1
M 26,a[16:19],42
s_add_u32 s54,s54,s46
R 70,2,9216
v_lshlrev_b32 v106,16,v122
M 26,a[24:27],46
s_and_b32 s79,s54,127
buffer_load_dwordx2 v[102:103],v11,s[36:39],s73 offen offset:256 sc1
M 30,a[20:23],42
s_cmp_eq_u32 s79,0
R 74,2,10240
v_and_b32 v107,0xffff0000,v122
M 30,a[28:31],46
L 5
D 17,56,0
M 30,a[36:39],50
s_cselect_b32 s94,0x3300,0
s_add_u32 s56,s55,256
R 78,2,11264
M 30,a[44:47],54
s_cmp_eq_u32 s79,127
v_lshlrev_b32 v108,16,v123
v_and_b32 v109,0xffff0000,v123
M 26,a[32:35],50
s_cselect_b32 s94,0xcc00,s94
R 82,2,12288
v_lshlrev_b32 v110,16,v124
M 26,a[40:43],54
L 5
D 17,56,1024
M 26,a[48:51],58
s_add_u32 s56,s55,512
R 86,2,13312
v_and_b32 v111,0xffff0000,v124
M 26,a[56:59],62
s_add_u32 s79,s54,s46
v_lshlrev_b32 v112,16,v125
v_and_b32 v113,0xffff0000,v125
M 30,a[52:55],58
s_and_b32 s79,s79,127
R 90,2,14336
v_fma_f32 v106,v106,v99,v98
M 30,a[60:63],62
L 5
D 17,56,2048
M 30,a[68:71],66
s_add_u32 s56,s55,768
R 94,2,15360
v_fma_f32 v107,v107,v99,v98
M 30,a[76:79],70
s_cmp_eq_u32 s79,0
v_fma_f32 v108,v108,v99,v98
v_fma_f32 v109,v109,v99,v98
M 26,a[64:67],66
s_cselect_b32 s95,0x3300,0
R 34,2,16384
v_fma_f32 v110,v110,v99,v98
M 26,a[72:75],70
L 5
D 17,56,3072
M 26,a[80:83],74
s_add_u32 m0,s57,4096
s_add_u32 s56,s55,5120
R 38,2,17408
M 26,a[88:91],78
s_cmp_eq_u32 s79,127
v_fma_f32 v111,v111,v99,v98
v_fma_f32 v112,v112,v99,v98
M 30,a[84:87],74
s_cselect_b32 s95,0xcc00,s95
R 42,2,18432
v_fma_f32 v113,v113,v99,v98
M 30,a[92:95],78
L 5
D 17,56,0
M 30,a[100:103],82
s_add_u32 s56,s55,5376
R 46,2,19456
v_add_f32 v18,v18,v106
M 30,a[108:111],86
s_add_u32 s57,s57,32768
v_add_f32 v19,v19,v107
v_add_f32 v20,v20,v108
M 26,a[96:99],82
s_mov_b32 s67,s65
R 50,2,20480
v_fma_f32 v107,v18,v18,s87
M 26,a[104:107],86
L 5
D 17,56,1024
M 26,a[112:115],90
s_add_u32 s56,s55,5632
R 54,2,21504
v_add_f32 v106,v18,v19
M 26,a[120:123],94
s_mov_b32 s77,s75
v_fma_f32 v107,v19,v19,v107
v_add_f32 v21,v21,v109
M 30,a[116:119],90
s_add_u32 s73,s73,s64
R 58,2,22528
v_add_f32 v106,v106,v20
M 30,a[124:127],94
L 5
D 17,56,2048
M 30,a[132:135],34
s_add_u32 s56,s55,5888
R 62,2,23552
v_fma_f32 v107,v20,v20,v107
M 30,a[140:143],38
s_add_u32 s74,s74,s62
v_add_f32 v22,v22,v110
v_add_f32 v106,v106,v21
M 26,a[128:131],34
s_add_u32 s55,s55,s60
R 66,2,24576
v_fma_f32 v107,v21,v21,v107
M 26,a[136:139],38
L 5
D 17,56,3072
M 26,a[144:147],42
s_and_b32 s57,s57,131071
R 70,2,25600
v_add_f32 v23,v23,v111
M 26,a[152:155],46
s_add_u32 s65,s65,s62
v_add_f32 v24,v24,v112
v_add_f32 v25,v25,v113
v_add_f32 v106,v106,v22
v_fma_f32 v107,v22,v22,v107
v_add_f32 v106,v106,v23
v_fma_f32 v107,v23,v23,v107
v_add_f32 v106,v106,v24
v_fma_f32 v107,v24,v24,v107
v_add_f32 v106,v106,v25
v_fma_f32 v107,v25,v25,v107
M 30,a[148:151],42
s_add_u32 s75,s75,s63
R 74,2,26624
v_cvt_pk_bf16_f32 v114,v18,v19
M 30,a[156:159],46
L 5
v_permlane16_swap_b32 v106,v107
v_add_f32 v106,v106,v107
v_mov_b32 v107,v106
M 30,a[164:167],50
R 78,2,27648
v_cvt_pk_bf16_f32 v115,v20,v21
M 30,a[172:175],54
v_permlane32_swap_b32 v106,v107
v_add_f32 v106,v106,v107
M 26,a[160:163],50
R 82,2,28672
ds_write_b32 v12,v106 offset:0
M 26,a[168:171],54
L 6
v_cvt_pk_bf16_f32 v116,v22,v23
v_cvt_pk_bf16_f32 v117,v24,v25
M 26,a[176:179],58
R 86,2,29696
M 26,a[184:187],62
buffer_store_dwordx4 v[114:117],v0,s[36:39],s66 offen offset:0 sc0 sc1
M 30,a[180:183],58
R 90,2,30720
M 30,a[188:191],62
L 6
L 2
M 30,a[196:199],66
s_waitcnt vmcnt(1)
R 94,2,31744
v_xor_b32 v2,65536,v2
M 30,a[204:207],70
s_barrier
buffer_store_dword v4,v3,s[36:39],s70 offen offset:0 sc0 sc1
M 26,a[192:195],66
s_mov_b32 s70,s69
R 34,1,0
R 118,13,0
M 26,a[200:203],70
s_nop 0
s_mov_b32 s69,s68
ds_read_b32 v7,v6 offset:1024
M 26,a[208:211],74
s_add_u32 s68,s68,s63
R 38,1,1024
M 26,a[216:219],78
M 30,a[212:215],74
R 42,1,2048
M 30,a[220:223],78
L 7
M 30,a[228:231],82
L 3
R 46,1,3072
v_add_f32 v118,v118,v119
M 30,a[236:239],86
L 3
v_add_f32 v120,v120,v121
v_add_f32 v118,v118,v120
M 26,a[224:227],82
R 50,1,4096
v_mul_f32 v118,s86,v118
M 26,a[232:235],86
s_nop 0
buffer_store_dword v118,v14,s[36:39],s76 offen offset:0 sc0 sc1
M 26,a[240:243],90
R 54,1,5120
v_cmp_eq_u32 vcc,s53,v7
M 26,a[248:251],94
s_or_b64 vcc,vcc,s[94:95]
s_cmp_eq_u64 vcc,-1
M 30,a[244:247],90
R 58,1,6144
M 30,a[252:255],94
L 5
s_cbranch_scc1 Lg2_L20_%=
s_cmp_eq_u32 vcc_lo,-1
s_cbranch_scc1 Lg2_L10_%=
s_branch Lg2_X0_%=
Lg2_done_%=:
s_waitcnt vmcnt(2)
s_barrier
buffer_store_dword v4,v3,s[36:39],s70 offen offset:0 sc0 sc1
s_waitcnt vmcnt(1)
s_barrier
buffer_store_dword v4,v3,s[36:39],s72 offen offset:0 sc0 sc1
s_waitcnt vmcnt(0) lgkmcnt(0)
s_barrier
Lg2_end_%=:
s_mov_b32 %0,s53
Lcode_g12_e:
.purgem M
.purgem R
.purgem T
.purgem D
.purgem L
)"
  : "=&s"(epOut) : "s"(ws), "s"(sWave), "s"(sG0), "s"(sStep), "s"(sOutOff), "s"(sFoutOff), "s"(sAuxOff), "s"(sWw), "s"(sBuf), "s"(sRk), "v"(vL16), "v"(vFlag), "v"(vSnapOff), "v"(vSnapRd), "v"(vWd0), "v"(vWd1), "v"(vWr0), "v"(vWr1), "v"(vWr2), "v"(vOutS), "v"(vAux0), "v"(vAux1), "v"(vAux2), "v"(vAux3), "v"(vAn), "v"(vAe), "s"(w12) : "v0", "v1", "v2", "v3", "v4", "v5", "v6", "v7", "v8", "v9", "v10", "v11", "v12", "v13", "v14", "v15", "v16", "v17", "v18", "v19", "v20", "v21", "v22", "v23", "v24", "v25", "v26", "v27", "v28", "v29", "v30", "v31", "v32", "v33", "v34", "v35", "v36", "v37", "v38", "v39", "v40", "v41", "v42", "v43", "v44", "v45", "v46", "v47", "v48", "v49", "v50", "v51", "v52", "v53", "v54", "v55", "v56", "v57", "v58", "v59", "v60", "v61", "v62", "v63", "v64", "v65", "v66", "v67", "v68", "v69", "v70", "v71", "v72", "v73", "v74", "v75", "v76", "v77", "v78", "v79", "v80", "v81", "v82", "v83", "v84", "v85", "v86", "v87", "v88", "v89", "v90", "v91", "v92", "v93", "v94", "v95", "v96", "v97", "v98", "v99", "v100", "v101", "v102", "v103", "v104", "v105", "v106", "v107", "v108", "v109", "v110", "v111", "v112", "v113", "v114", "v115", "v116", "v117", "v118", "v119", "v120", "v121", "v122", "v123", "v124", "v125", "v126", "v127", "v128", "v129", "v130", "v131", "v132", "v133", "v134", "v135", "v136", "v137", "v138", "v139", "v140", "v141", "v142", "v143", "v144", "v145", "v146", "v147", "v148", "v149", "v150", "v151", "v152", "v153", "v154", "v155", "v156", "v157", "v158", "v159", "v160", "v161", "v162", "v163", "v164", "v165", "v166", "v167", "v168", "v169", "v170", "v171", "v172", "v173", "v174", "v175", "v176", "v177", "v178", "v179", "v180", "v181", "v182", "v183", "v184", "v185", "v186", "v187", "v188", "v189", "v190", "v191", "v192", "v193", "v194", "v195", "v196", "v197", "v198", "v199", "v200", "v201", "v202", "v203", "v204", "v205", "v206", "v207", "v208", "v209", "v210", "v211", "v212", "v213", "v214", "v215", "v216", "v217", "v218", "v219", "v220", "v221", "v222", "v223", "v224", "v225", "v226", "v227", "v228", "v229", "v230", "v231", "a0", "a1", "a2", "a3", "a4", "a5", "a6", "a7", "a8", "a9", "a10", "a11", "a12", "a13", "a14", "a15", "a16", "a17", "a18", "a19", "a20", "a21", "a22", "a23", "a24", "a25", "a26", "a27", "a28", "a29", "a30", "a31", "a32", "a33", "a34", "a35", "a36", "a37", "a38", "a39", "a40", "a41", "a42", "a43", "a44", "a45", "a46", "a47", "a48", "a49", "a50", "a51", "a52", "a53", "a54", "a55", "a56", "a57", "a58", "a59", "a60", "a61", "a62", "a63", "a64", "a65", "a66", "a67", "a68", "a69", "a70", "a71", "a72", "a73", "a74", "a75", "a76", "a77", "a78", "a79", "a80", "a81", "a82", "a83", "a84", "a85", "a86", "a87", "a88", "a89", "a90", "a91", "a92", "a93", "a94", "a95", "a96", "a97", "a98", "a99", "a100", "a101", "a102", "a103", "a104", "a105", "a106", "a107", "a108", "a109", "a110", "a111", "a112", "a113", "a114", "a115", "a116", "a117", "a118", "a119", "a120", "a121", "a122", "a123", "a124", "a125", "a126", "a127", "a128", "a129", "a130", "a131", "a132", "a133", "a134", "a135", "a136", "a137", "a138", "a139", "a140", "a141", "a142", "a143", "a144", "a145", "a146", "a147", "a148", "a149", "a150", "a151", "a152", "a153", "a154", "a155", "a156", "a157", "a158", "a159", "a160", "a161", "a162", "a163", "a164", "a165", "a166", "a167", "a168", "a169", "a170", "a171", "a172", "a173", "a174", "a175", "a176", "a177", "a178", "a179", "a180", "a181", "a182", "a183", "a184", "a185", "a186", "a187", "a188", "a189", "a190", "a191", "a192", "a193", "a194", "a195", "a196", "a197", "a198", "a199", "a200", "a201", "a202", "a203", "a204", "a205", "a206", "a207", "a208", "a209", "a210", "a211", "a212", "a213", "a214", "a215", "a216", "a217", "a218", "a219", "a220", "a221", "a222", "a223", "a224", "a225", "a226", "a227", "a228", "a229", "a230", "a231", "a232", "a233", "a234", "a235", "a236", "a237", "a238", "a239", "a240", "a241", "a242", "a243", "a244", "a245", "a246", "a247", "a248", "a249", "a250", "a251", "a252", "a253", "a254", "a255", "s36", "s37", "s38", "s39", "s40", "s41", "s42", "s43", "s44", "s45", "s46", "s47", "s48", "s49", "s50", "s51", "s52", "s53", "s54", "s55", "s56", "s57", "s58", "s59", "s60", "s61", "s62", "s63", "s64", "s65", "s66", "s67", "s68", "s69", "s70", "s71", "s72", "s73", "s74", "s75", "s76", "s77", "s78", "s79", "s80", "s81", "s82", "s83", "s84", "s85", "s86", "s87", "s88", "s89", "s90", "s91", "s92", "s93", "s94", "s95", "s96", "s97", "vcc", "scc", "memory");
      ep = epOut;
    }
    LANE_ID(lane2);
    for (int t = kind == 6 ? 64 + slice : (kind - 1) * 32 + rep * 8 + slice; t < LAST_TASKS; t += 64 + NRW)
      row_last(ws, out, t, lane2, wave, ep);
    if (b == 1 && wave == 0) set_flag(ws + OFF_EP, lane2, ep);
    CTS(14);
    asm volatile("s_endpgm\nLcode_end:" ::: "memory");
    __builtin_unreachable();
  }
}

extern "C" void kernel_launch(void* const* d_in, const int* in_sizes, int n_in, void* d_out, int out_size,
                              void* d_ws, size_t ws_size, hipStream_t stream) {
  const u16* q = (const u16*)d_in[0];
  const u16* k = (const u16*)d_in[1];
  const u16* v = (const u16*)d_in[2];
  const u16* shift_k = (const u16*)d_in[3];
  const u16* ssm_k = (const u16*)d_in[4];
  const u16* ssm_D = (const u16*)d_in[5];
  const u16* w1 = (const u16*)d_in[7];
  const u16* w2 = (const u16*)d_in[8];
  const u16* w3 = (const u16*)d_in[9];
  const u16* w4 = (const u16*)d_in[10];
  flow_kernel<<<256, 256, 0, stream>>>(q, k, v, shift_k, ssm_k, ssm_D, w1, w2, w3, w4, (char*)d_ws, (u16*)d_out);
}
```

```cpp
#include <hip/hip_runtime.h>
#include <hip/hip_bf16.h>
#include <stdint.h>
#include <type_traits>

typedef short s16x8 __attribute__((ext_vector_type(8)));
typedef short s16x4 __attribute__((ext_vector_type(4)));
typedef float f32x4 __attribute__((ext_vector_type(4)));
typedef unsigned short u16;
typedef unsigned short u16x8 __attribute__((ext_vector_type(8)));
typedef unsigned short u16x4 __attribute__((ext_vector_type(4)));
typedef float f32x2 __attribute__((ext_vector_type(2)));
typedef __bf16 bf16x2 __attribute__((ext_vector_type(2)));
typedef unsigned u32x4v __attribute__((ext_vector_type(4)));
typedef __attribute__((address_space(1))) unsigned gu32;

constexpr int S_ = 2048, D_ = 1024, F_ = 3584;
#define PF 1

constexpr int WS_USED = 111243392, LAST_TASKS = 236;
constexpr int OFF_KVR = 4194304, OFF_XP = 8388608, OFF_HS = 17825792, OFF_X1 = 31457280, OFF_G = 40894464, OFF_P0 = 71303168, OFF_P1 = 89128960, OFF_Y = 98566144, OFF_S1 = 106954752, OFF_ST = 107479040, OFF_FL = 109051904, OFF_FXP = 109056000, OFF_FH = 109334528, OFF_FX1 = 109613056, OFF_FG = 109891584, OFF_FP = 110587904, OFF_FKV = 111144960, OFF_FY = 111177728, OFF_EP = 111243264, OFF_DBG = 115343360;

#define WS_DIAG 0

__device__ __forceinline__ float bf2f(u16 v) { return __uint_as_float(((unsigned)v) << 16); }
__device__ __forceinline__ u16 f2bf(float f) { return __builtin_bit_cast(u16, (__bf16)f); }
__device__ __forceinline__ float rbf(float f) { return bf2f(f2bf(f)); }
__device__ __forceinline__ void st16(void* p, u16x8 v) {
  asm volatile("global_store_dwordx4 %0, %1, off sc0 sc1\n\ts_nop 1" :: "v"(p), "v"(v) : "memory");
}
#define WAIT_VM(n) asm volatile("s_waitcnt vmcnt(%0)" ::"n"(n) : "memory")
#define WAIT_LGKM0() asm volatile("s_waitcnt lgkmcnt(0)" ::: "memory")

#define CODE_RANGE(B, E, p, n)                                                                                   \
  const char* p; unsigned n;                                                                                     \
  { unsigned lo_, hi_;                                                                                           \
    asm volatile("s_getpc_b64 s[98:99]\nLpc%=:\n\ts_add_u32 %0, s98, (" B "-Lpc%=)&4294967295\n\ts_addc_u32 %1, s99, (" B "-Lpc%=)>>32\n\t" \
                 "s_mov_b32 %2, " E "-" B : "=s"(lo_), "=s"(hi_), "=s"(n) :: "s98", "s99", "scc");               \
    p = (const char*)(((unsigned long long)hi_ << 32) | lo_); }
__device__ __forceinline__ unsigned touch(const char* p, unsigned n, unsigned off) {
  unsigned t;
  asm volatile("global_load_dword %0, %1, off" : "=v"(t) : "v"(p + (off < n ? off : 0u)) : "memory");
  return t;
}

__device__ __forceinline__ void touch_lds(const char* p, unsigned a) {
  asm volatile("s_mov_b32 m0, %1\n\ts_nop 0\n\tglobal_load_lds_dword %0, off" :: "v"(p), "s"(a) : "memory");
}
#define PFPOL ""
__device__ __forceinline__ void touch_pol(const char* p, unsigned a) {
  asm volatile("s_mov_b32 m0, %1\n\ts_nop 0\n\tglobal_load_lds_dword %0, off " PFPOL :: "v"(p), "s"(a) : "memory");
}
#define YSLEEP 4
#define KSLEEP 2
#define CTS(i)
__device__ __forceinline__ f32x2 up2(unsigned p) { return (f32x2){__uint_as_float(p << 16), __uint_as_float(p & 0xffff0000u)}; }
__device__ __forceinline__ unsigned pk2(f32x2 v) { return __builtin_bit_cast(unsigned, __builtin_convertvector(v, bf16x2)); }
__device__ __forceinline__ f32x2 rbf2(f32x2 v) { return up2(pk2(v)); }
template <int N, class F> __device__ __forceinline__ void sfor(F&& f) {
  if constexpr (N > 0) { sfor<N - 1>(f); f(std::integral_constant<int, N - 1>{}); }
}
__device__ __forceinline__ void set_flag(char* p, int i, unsigned ep) {
  if (i < 32) __hip_atomic_store((gu32*)p + i, ep, __ATOMIC_RELAXED, __HIP_MEMORY_SCOPE_AGENT);
}
template <int NL>
__device__ __forceinline__ void poll_flags(const char* p, int n, int lane, unsigned ep) {
  for (int it = 0; it < 20000; ++it) {
    bool ok = true;
#pragma unroll
    for (int i = 0; i < NL; ++i) {
      const int f = lane + 64 * i;
      ok &= __hip_atomic_load((gu32*)(p + (f < n ? f : 0) * 128), __ATOMIC_RELAXED, __HIP_MEMORY_SCOPE_AGENT) == ep;
    }
    if (__all(ok)) break;
    __builtin_amdgcn_s_sleep(YSLEEP);
  }
  asm volatile("" ::: "memory");
}

constexpr int CV_HP = (2048 + 32) * 2, CV_UR = (2048 + 256) * 2, CV_CH = 2 * CV_HP + CV_UR;
constexpr int NCV = 174;
constexpr int CV_STG = 8 * CV_CH;
constexpr int YP = D_ * 16;

struct KvRegs { u32x4v k0, k1, k2, k3, k4, k5, k6, v0, v1, v2, v3, s0, s1, s2, s3; };
#define KV_LOADS                                                                                                                       \
  "v_lshrrev_b32 %[a], 3, %[tid]\n\tv_and_b32 %[c], 7, %[tid]\n\ts_lshr_b32 s90, %[tile], 4\n\ts_and_b32 s91, %[tile], 15\n\t"          \
  "v_lshlrev_b32 %[a], 13, %[a]\n\tv_lshlrev_b32 %[c], 4, %[c]\n\ts_lshl_b32 s90, s90, 18\n\ts_lshl_b32 s91, s91, 7\n\t"                 \
  "v_add_u32 %[a], s90, %[a]\n\tv_add_u32 %[c], s91, %[c]\n\tv_subrev_u32 %[h], 0x1000, %[a]\n\tv_max_i32 %[h], 0x800, %[h]\n\t"         \
  "v_add3_u32 %[a], %[a], %[c], s92\n\tv_add_u32 %[h], %[h], %[c]\n\tv_lshlrev_b32 %[c], 2, %[c]\n\ts_waitcnt lgkmcnt(0)\n\t"            \
  "global_load_dwordx4 %3, %[a], s[76:77] offset:-4096 nt\n\tglobal_load_dwordx4 %4, %[a], s[76:77] offset:-2048 nt\n\t"                \
  "global_load_dwordx4 %5, %[a], s[76:77] nt\n\tglobal_load_dwordx4 %6, %[a], s[76:77] offset:2048 nt\n\t"                              \
  "global_load_dwordx4 %7, %[a], s[78:79] offset:-4096 nt\n\tglobal_load_dwordx4 %8, %[a], s[78:79] offset:-2048 nt\n\t"                \
  "global_load_dwordx4 %9, %[a], s[78:79] nt\n\tglobal_load_dwordx4 %10, %[a], s[78:79] offset:2048 nt\n\t"                             \
  "global_load_dwordx4 %0, %[h], s[76:77] offset:-2048 nt\n\tglobal_load_dwordx4 %1, %[h], s[76:77] nt\n\t"                             \
  "global_load_dwordx4 %2, %[h], s[76:77] offset:2048 nt\n\t"                                                                           \
  "global_load_dwordx4 %11, %[c], s[80:81]\n\tglobal_load_dwordx4 %12, %[c], s[80:81] offset:16\n\t"                                    \
  "global_load_dwordx4 %13, %[c], s[80:81] offset:32\n\tglobal_load_dwordx4 %14, %[c], s[80:81] offset:48\n\t"
#define KV_OUTS(R) "=&v"(R.k0), "=&v"(R.k1), "=&v"(R.k2), "=&v"(R.k3), "=&v"(R.k4), "=&v"(R.k5), "=&v"(R.k6), "=&v"(R.v0), "=&v"(R.v1), "=&v"(R.v2), \
                   "=&v"(R.v3), "=&v"(R.s0), "=&v"(R.s1), "=&v"(R.s2), "=&v"(R.s3), [a] "=&v"(ta), [h] "=&v"(th), [c] "=&v"(tc)
#define KV_CLOB "s74", "s75", "s76", "s77", "s78", "s79", "s80", "s81", "s82", "s83", "s90", "s91", "s92", "scc", "vcc", "memory"
template <int N, int N2>
__device__ __forceinline__ void kv_wait(KvRegs& R, int tile) {
  asm volatile("s_cmp_ge_u32 %17, 192\n\ts_cbranch_scc1 Lkw%=\n\ts_waitcnt vmcnt(%15)\nLkw%=:\n\ts_waitcnt vmcnt(%16)"
               : "+v"(R.k0), "+v"(R.k1), "+v"(R.k2), "+v"(R.k3), "+v"(R.k4), "+v"(R.k5), "+v"(R.k6), "+v"(R.v0), "+v"(R.v1), "+v"(R.v2),
                 "+v"(R.v3), "+v"(R.s0), "+v"(R.s1), "+v"(R.s2), "+v"(R.s3) : "n"(N), "n"(N2), "s"(tile) : "scc", "memory");
}
__device__ __forceinline__ void kv_store(KvRegs& R, char* __restrict__ ws, char* T, int b, int tid) {
  const int t0 = (b >> 4) * 128, c0 = (b & 15) * 64, ch8 = tid & 7, rg = tid >> 3;
  if (t0 + 4 * rg < 3) R.k0 = R.k1 = R.k2 = (u32x4v){0u, 0u, 0u, 0u};
  typedef unsigned u32x2 __attribute__((ext_vector_type(2)));
  const u32x4v kr[7] = {R.k0, R.k1, R.k2, R.k3, R.k4, R.k5, R.k6}, vr[4] = {R.v0, R.v1, R.v2, R.v3}, sk[4] = {R.s0, R.s1, R.s2, R.s3};
#pragma unroll
  for (int j2 = 0; j2 < 4; ++j2) {
    const f32x2 sa = up2(sk[j2][0]), sb = up2(sk[j2][1]), sc = up2(sk[j2][2]), sd = up2(sk[j2][3]);
    const f32x2 s0 = {sa[0], sc[0]}, s1 = {sa[1], sc[1]}, s2 = {sb[0], sd[0]}, s3 = {sb[1], sd[1]};
    f32x2 kf[7], o[4];
#pragma unroll
    for (int r = 0; r < 7; ++r) kf[r] = up2(kr[r][j2]);
#pragma unroll
    for (int r = 0; r < 4; ++r)
      o[r] = __builtin_elementwise_fma(kf[r + 3], s0, __builtin_elementwise_fma(kf[r + 2], s1, __builtin_elementwise_fma(kf[r + 1], s2, kf[r] * s3))) * up2(vr[r][j2]);
#pragma unroll
    for (int e = 0; e < 2; ++e)
      *(u32x2*)(T + (8 * ch8 + 2 * j2 + e) * 264 + (124 - 4 * rg) * 2) = (u32x2){pk2((f32x2){o[3][e], o[2][e]}), pk2((f32x2){o[1][e], o[0][e]})};
  }
  __syncthreads();
  u16* kvr = (u16*)(ws + OFF_KVR);
#pragma unroll
  for (int i = 0; i < 4; ++i) {
    const int idx = tid + 256 * i, ch = idx >> 4, cb = idx & 15;
    const u16x4 lo = *(const u16x4*)(T + ch * 264 + cb * 16), hi = *(const u16x4*)(T + ch * 264 + cb * 16 + 8);
    st16(kvr + (size_t)(c0 + ch) * S_ + (1920 - t0) + cb * 8, __builtin_shufflevector(lo, hi, 0, 1, 2, 3, 4, 5, 6, 7));
  }
}

#define LASTSLEEP 4
__device__ __forceinline__ void wait_flags(const char* p, unsigned ep) {
  gu32* f = (gu32*)p;
  for (int it = 0; it < 20000; ++it) {
    if (__all(__hip_atomic_load(f, __ATOMIC_RELAXED, __HIP_MEMORY_SCOPE_AGENT) == ep)) break;
    __builtin_amdgcn_s_sleep(LASTSLEEP);
  }
  asm volatile("" ::: "memory");
}
__device__ __forceinline__ void row_last(char* __restrict__ ws, u16* __restrict__ out, int t, int lane, int wave, unsigned ep) {
  const __amdgpu_buffer_rsrc_t rs = __builtin_amdgcn_make_buffer_rsrc(ws, 0, WS_USED, 0x00020000);
  const int r8 = lane & 7, seg = lane >> 3, un = 128 + (t >> 1);
  const unsigned fo = un * 32768 + (wave * 8 + (seg >> 2)) * 1024 + ((seg & 3) * 16 + (t & 1) * 8 + r8) * 16;
  wait_flags(ws + OFF_FP + un * 2048 + ((lane & 2) * 4 + wave * 2 + (lane & 1)) * 128, ep);
  u32x4v p0[4], p1[4];
#pragma unroll
  for (int i = 0; i < 4; ++i) {
    p0[i] = __builtin_bit_cast(u32x4v, __builtin_amdgcn_raw_buffer_load_b128(rs, fo + i * 2048, OFF_P0, 16));
    p1[i] = __builtin_bit_cast(u32x4v, __builtin_amdgcn_raw_buffer_load_b128(rs, fo + i * 2048, OFF_P1, 16));
  }
#pragma unroll
  for (int i = 0; i < 4; ++i) {
    u32x4v o;
#pragma unroll
    for (int e = 0; e < 4; ++e) o[e] = pk2(up2(p0[i][e]) + up2(p1[i][e]));
    st16(out + (size_t)(t * 8 + r8) * D_ + wave * 256 + i * 64 + seg * 8, __builtin_bit_cast(u16x8, o));
  }
}

__device__ __forceinline__ int swzB(int r) { return ((((r >> 1) & 1) | (((r >> 3) & 1) << 1)) << 1); }
#define TCH 1
#define LANE_ID(x) int x; asm volatile("v_mbcnt_lo_u32_b32 %0, -1, 0\n\tv_mbcnt_hi_u32_b32 %0, -1, %0" : "=v"(x))
#define T2X 5
#define W12 3
#define HL 0
#define W_B1 (b == 1)
#define W_B7 (b == 7)
#define W_I15 (idx == 15)
#define W_I31 (idx == 31)
#define W_B249 (b == 249)
#define QTB 1
constexpr int QT0 = 34 * 128;
#define CODE_MAX(x, y) "(((" x ">" y ")&" x ")|((" x "<=" y ")&" y "))"
constexpr int HU = 40960;
#define G3OR 5
constexpr int NRW = 22 - G3OR;
__global__ __launch_bounds__(256, 1) void flow_kernel(const u16* __restrict__ q, const u16* __restrict__ kin, const u16* __restrict__ vin,
                                                      const u16* __restrict__ shift_k, const u16* __restrict__ ssm_k, const u16* __restrict__ ssm_D,
                                                      const u16* __restrict__ w1, const u16* __restrict__ w2,
                                                      const u16* __restrict__ w3, const u16* __restrict__ w4, char* __restrict__ ws,
                                                      u16* __restrict__ out) {
  __shared__ __attribute__((aligned(16))) char lds[163840];
  KvRegs KR, KR2;
  unsigned ew, ta, th, tc;
  int b, tile;
  asm volatile("Lcode_b:\n\ts_load_dwordx4 s[76:79], %[ka], 0x8\n\ts_load_dwordx2 s[82:83], %[ka], 0x50\n\t"
               "s_getpc_b64 s[74:75]\n\ts_lshr_b32 s90, %[bi], 3\n\ts_mov_b32 m0, 0x27f00\n\tv_lshlrev_b32 %[a], 5, %[tid]\n\tv_add_lshl_u32 %[a], %[a], s90, 7\n\t"
               "v_min_u32 %[a], " CODE_MAX(CODE_MAX(CODE_MAX("(Lcode_g3_e-Lcode_b)", "(Lcode_g3o_e-Lcode_b)"), CODE_MAX("(Lcode_g4_e-Lcode_b)", "(Lcode_g12_e-Lcode_b)")), "(Lcode_end-Lcode_b)") "-64, %[a]\n\t"
               "global_load_lds_dword %[a], s[74:75]\n\ts_load_dwordx2 s[80:81], %[ka], 0x18\n\t"
               "s_mov_b32 %[b], %[bi]\n\ts_bfe_u32 s91, %[bi], 0x20001\n\ts_lshl2_add_u32 %[tile], s90, s91\n\ts_movk_i32 s92, 0x1000\n\t"
               "s_bitcmp1_b32 %[bi], 0\n\ts_cbranch_scc0 Lk1%=\n\ts_add_u32 %[tile], %[tile], 64\n\ts_cmp_lt_u32 s90, 8\n\ts_cbranch_scc1 Lk2%=\n\t"
               "s_lshl3_add_u32 s91, s91, s90\n\ts_add_u32 s91, s91, 184\n\ts_cmp_lt_u32 s90, 16\n\ts_cselect_b32 %[tile], s91, %[tile]\nLk1%=:\n\t"
               KV_LOADS "v_mov_b32 %[a], %[eo]\n\tglobal_load_dword %[ew], %[a], s[82:83] sc1\nLk2%=:\n\ts_waitcnt lgkmcnt(0)"
               : KV_OUTS(KR), [ew] "=&v"(ew), [b] "=&s"(b), [tile] "=&s"(tile)
               : [ka] "s"(__builtin_amdgcn_kernarg_segment_ptr()), [bi] "s"(blockIdx.x), [tid] "v"(threadIdx.x), [eo] "n"(OFF_EP) : KV_CLOB);
  const unsigned t_start = 0;
  const int tid = threadIdx.x, lane = tid & 63, wave = __builtin_amdgcn_readfirstlane(tid >> 6);
  const int xcd = b & 7, idx = b >> 3, tile2 = (tile + 32) ^ T2X;
  asm volatile("s_cmp_lt_u32 %[tile], 224\n\ts_cbranch_scc1 Lk3%=\n\ts_load_dwordx4 s[76:79], %[ka], 0x8\n\ts_load_dwordx2 s[80:81], %[ka], 0x18\n\ts_movk_i32 s92, 0x1000\n\t"
               KV_LOADS "\nLk3%=:"
               : KV_OUTS(KR2) : [ka] "s"(__builtin_amdgcn_kernarg_segment_ptr()), [tile] "s"(tile2), [tid] "v"(threadIdx.x) : KV_CLOB);
  const unsigned sLds = (unsigned)(size_t)lds;
  const int o8 = idx & 7;
  int kind, rep, slice, kh = 0;
  int j3 = 0, j4 = 0;
  const int xp = xcd >> 1, pr = xp & 1;
  if (!(xcd & 1)) {
    if (idx < (xp < 2 ? 24 : 16)) { kind = 4; rep = (xp < 2 ? 0 : 3) + (idx >> 3); kh = pr; slice = o8; j4 = rep * 16 + kh * 8 + o8; }
    else { kind = 3; rep = xp < 2 ? 4 : pr * 2 + ((idx >> 3) & 1); slice = xp < 2 ? pr * 8 + o8 : o8; j3 = rep * 18 + slice; }
  } else if (idx < 16) { kind = 1 + (idx >> 3); rep = xp; slice = o8; }
  else {
    const int k = idx - 16, n3 = xp < 2 ? 10 : 11, no = xp == 2 ? 2 : 1;
    if (k < n3) { kind = 3; rep = k < 10 ? pr * 2 + (k >= 5) : 4; slice = k < 10 ? (xp < 2 ? 8 : 13) + k - 5 * (k >= 5) : 16 + pr; j3 = rep * 18 + slice; }
    else if (k < n3 + no) { kind = 5; rep = k > n3 ? 4 : pr * 2 + (xp >> 1); slice = 18; j3 = 90 + rep; }
    else { kind = 6; rep = 0; slice = (xp < 3 ? xp * 5 : 13) + k - n3 - no; }
  }
  CTS(0);
  const unsigned sScr = sLds + 163584;
  unsigned ep = 0;
  auto w_touch = [&](const u16* w) {
#pragma unroll
    for (int i = 0; i < 2; ++i) touch_lds((const char*)w + (size_t)(rep * 256 + i * 128 + (tid >> 1)) * 2048 + slice * 256 + (tid & 1) * 128, sScr);
  };
  if (kind == 1) { if (W12 & 1) w_touch(w1); }
  else {
  const bool isconv = kind >= 3 && kind != 6 && j3 < 94;
  const int nw = (kind == 4 || j3 >= 92) ? 2 : 1, c0 = kind == 4 ? 8 * j4 : j3 < 92 ? 640 + 4 * j3 : j3 < 94 ? 272 + 8 * j3 : 0, cw = c0 + nw * wave;
  const unsigned hd = isconv ? sLds + 2 * wave * CV_CH + 32 : 0x30000u;
  auto h_dma = [&](bool first) {
#pragma unroll
    for (int j = 0; j < 2; ++j) {
      const u16* hp = ssm_k + (size_t)cw * S_ + (isconv && j < nw ? j * S_ + lane * 8 : 0);
      if (first) asm volatile("s_mov_b32 m0, %1\n\ts_nop 0\n\tglobal_load_lds_dwordx4 %0, off nt" :: "v"(hp), "s"(hd + j * CV_CH) : "memory");
      else asm volatile("s_mov_b32 m0, %1\n\ts_nop 0\n\tglobal_load_lds_dwordx4 %0, off offset:1024 nt\n\t"
                        "global_load_lds_dwordx4 %0, off offset:2048 nt\n\tglobal_load_lds_dwordx4 %0, off offset:3072 nt" :: "v"(hp), "s"(hd + j * CV_CH) : "memory");
    }
  };
  h_dma(true);
  if (HL == 0) h_dma(false);
  *(u32x4v*)(lds + (2 * wave + (lane >> 5)) * CV_CH + 2 * CV_HP + 4096 + (lane & 31) * 16) = (u32x4v){0u, 0u, 0u, 0u};
  if (lane < 8) *(u32x4v*)(lds + (2 * wave + (lane >> 2)) * CV_CH + ((lane >> 1) & 1) * CV_HP + (lane & 1) * 16) = (u32x4v){0u, 0u, 0u, 0u};
  kv_wait<HL ? 3 : 9, HL ? 18 : 24>(KR, tile);
  CTS(12);
  kv_store(KR, ws, lds + CV_STG, tile, tid);
  kv_wait<63, HL ? 6 : 12>(KR2, tile);
  if (kind == 2) kv_store(KR2, ws, lds + CV_STG + 17408, tile2, tid);
  CTS(1);
  if (HL == 1) h_dma(false);
  const unsigned dw = ((const unsigned*)ssm_D)[cw >> 1];
  asm volatile("s_waitcnt vmcnt(%1)" : "+v"(ew) : "n"(HL == 1 ? 6 : 0) : "memory");
  ep = __builtin_amdgcn_readfirstlane(ew) + 1u;
  if (ep == 0u) ep = 1u;
  __builtin_amdgcn_s_barrier();
  set_flag(ws + OFF_FKV + ((wave & 1) ? tile2 : tile) * 128, wave < (kind == 2 ? 2 : 1) ? lane : 64, ep);
  CTS(2);
  if (HL == 2) h_dma(false);
  if (kind == 2 && (W12 & 2) != 0) w_touch(w2);
  if (isconv) {
    LANE_ID(lane);
    const int tid = wave * 64 + lane;
    const unsigned sC0 = (cw & 1) ? dw & 0xffff0000u : dw << 16, sC1 = dw & 0xffff0000u;
    const int l15 = lane & 15, lg = lane >> 4, cp = (l15 + 1) & 1, iw = kind == 4 ? j4 : 80 + j3, own = 2 * wave * CV_CH;
    const int ch = tid & (4 * nw - 1), tb = nw == 2 ? tid >> 3 : tid >> 2, slot = nw == 2 ? ch : 2 * ch;
    const unsigned sWave = wave, sBuf = nw, sAuxOff = OFF_KVR + cw * 4096, sFoutOff = OFF_FY + iw * 128, sExp = __builtin_amdgcn_readfirstlane(ep), sDbg = 110u * 1048576u + (b * 4 + wave) * 8192;
    const unsigned vL16 = lane * 16, vPA = own + cp * CV_HP + 2 * (8 * lg + l15 + 1 - cp), vPB = own + 2 * CV_HP + 2 * (2032 - 16 * l15 + 8 * lg) - 512 * 7;
    const unsigned vPU = own + 2 * CV_HP + 2 * (2044 - 16 * l15 - 4 * lg) - 512 * 7, vPW = 32 * l15 + 8 * lg;
    const unsigned vOR = slot * CV_CH + tb * 16, vORA = CV_STG + slot * 1024 + tb * 16, vOG = tb * YP + (c0 + ch) * 16;
    const unsigned vFA = ((lane & 3) * 16 + (c0 >> 6)) * 128, vFB = ((4 + lane % 12) * 16 + (c0 >> 6)) * 128, vFlag = tid < 32 ? tid * 4 : 0x80000000u;
    const unsigned vTW = lane < 42 ? (iw * 168 + wave * 42 + lane) * 128 : 0x80000000u;
    const unsigned vTX = lane < 44 ? ((kind == 4 ? j4 : j3 - 12) * 176 + wave * 44 + lane) * 128 : 0x80000000u;
    (void)sDbg;
    asm volatile(
R"(.macro M d,w,x
v_mfma_f32_16x16x32_bf16 v[\d:\d+3], \w, v[\x:\x+3], v[\d:\d+3]
.endm
.macro R d,a,o
ds_read_b128 v[\d:\d+3], v\a offset:\o
.endm
.macro T d,a,o
ds_read_b64_tr_b16 \d, v\a offset:\o
.endm
.macro D a,s,o
buffer_load_dwordx4 v\a, s[36:39], s\s offen offset:\o sc1 lds
.endm
.macro L c
s_waitcnt lgkmcnt(\c)
.endm
Lcode_cv_b:
s_mov_b64 s[36:37],%0
s_mov_b32 s38,111243392
s_mov_b32 s39,0x00020000
s_mov_b32 s44,%1
s_mov_b32 s51,%2
s_mov_b32 s49,%3
s_mov_b32 s48,%4
s_mov_b32 s86,%5
s_mov_b32 s87,%6
s_mov_b32 s53,%7
v_mov_b32 v0,%8
v_mov_b32 v1,%9
v_mov_b32 v2,%10
v_mov_b32 v3,%11
v_mov_b32 v4,%12
v_mov_b32 v5,%13
v_mov_b32 v6,%14
v_mov_b32 v7,%15
v_mov_b32 v8,%16
v_mov_b32 v9,%17
v_mov_b32 v10,%18
v_mov_b32 v11,%19
v_mov_b32 v12,%20
s_mov_b32 s58,111144960
buffer_load_dword v13,v8,s[36:39],s58 offen offset:0 sc1
s_mul_i32 s57,s44,25856
v_add_u32 v18,s57,v0
v_add_u32 v18,32,v18
v_mov_b32 v19,s57
R 88,18,0
ds_read_b32 v92,v18 offset:16
R 152,18,12928
ds_read_b32 v156,v18 offset:12944
ds_read_b32 v24,v19 offset:32
ds_read_b32 v25,v19 offset:12960
s_mov_b64 s[40:41],%21
s_mov_b32 s42,7340032
s_mov_b32 s43,0x00020000
s_mov_b64 s[60:61],%22
s_mov_b32 s62,7340032
s_mov_b32 s63,0x00020000
v_mov_b32 v23,s53
s_mov_b32 s82,0
s_add_u32 s55,s49,4096
s_cmp_eq_u32 s51,2
s_cselect_b32 s55,s55,0x7fff0000
s_cselect_b32 s56,0,0x70000000
s_add_u32 s57,s57,8320
v_add_u32 v14,1024,v1
v_add_u32 v15,2048,v1
v_add_u32 v16,3072,v1
v_add_u32 v17,12928,v1
L 4
v_alignbyte_b32 v96,v89,v88,2
v_alignbyte_b32 v97,v90,v89,2
v_alignbyte_b32 v98,v91,v90,2
v_alignbyte_b32 v99,v92,v91,2
ds_write_b128 v18,v[96:99] offset:4160
L 3
v_alignbyte_b32 v160,v153,v152,2
v_alignbyte_b32 v161,v154,v153,2
v_alignbyte_b32 v162,v155,v154,2
v_alignbyte_b32 v163,v156,v155,2
ds_write_b128 v18,v[160:163] offset:17088
L 2
v_lshlrev_b32 v24,16,v24
v_lshlrev_b32 v25,16,v25
s_mov_b64 exec,1
ds_write_b32 v19,v24 offset:4188
ds_write_b32 v19,v25 offset:17116
s_mov_b64 exec,-1
ds_read2_b32 a[0:1],v1 offset0:0 offset1:1
ds_read2_b32 a[2:3],v1 offset0:2 offset1:3
ds_read2_b32 a[4:5],v1 offset0:128 offset1:129
ds_read2_b32 a[6:7],v1 offset0:130 offset1:131
ds_read2_b32 a[8:9],v1 offset0:16 offset1:17
ds_read2_b32 a[10:11],v1 offset0:18 offset1:19
ds_read2_b32 a[12:13],v1 offset0:144 offset1:145
ds_read2_b32 a[14:15],v1 offset0:146 offset1:147
ds_read2_b32 a[16:17],v1 offset0:32 offset1:33
ds_read2_b32 a[18:19],v1 offset0:34 offset1:35
ds_read2_b32 a[20:21],v1 offset0:160 offset1:161
ds_read2_b32 a[22:23],v1 offset0:162 offset1:163
ds_read2_b32 a[24:25],v1 offset0:48 offset1:49
ds_read2_b32 a[26:27],v1 offset0:50 offset1:51
ds_read2_b32 a[28:29],v1 offset0:176 offset1:177
ds_read2_b32 a[30:31],v1 offset0:178 offset1:179
ds_read2_b32 a[32:33],v1 offset0:64 offset1:65
ds_read2_b32 a[34:35],v1 offset0:66 offset1:67
ds_read2_b32 a[36:37],v1 offset0:192 offset1:193
ds_read2_b32 a[38:39],v1 offset0:194 offset1:195
ds_read2_b32 a[40:41],v1 offset0:80 offset1:81
ds_read2_b32 a[42:43],v1 offset0:82 offset1:83
ds_read2_b32 a[44:45],v1 offset0:208 offset1:209
ds_read2_b32 a[46:47],v1 offset0:210 offset1:211
ds_read2_b32 a[48:49],v1 offset0:96 offset1:97
ds_read2_b32 a[50:51],v1 offset0:98 offset1:99
ds_read2_b32 a[52:53],v1 offset0:224 offset1:225
ds_read2_b32 a[54:55],v1 offset0:226 offset1:227
ds_read2_b32 a[56:57],v1 offset0:112 offset1:113
ds_read2_b32 a[58:59],v1 offset0:114 offset1:115
ds_read2_b32 a[60:61],v1 offset0:240 offset1:241
ds_read2_b32 a[62:63],v1 offset0:242 offset1:243
ds_read2_b32 a[64:65],v17 offset0:0 offset1:1
ds_read2_b32 a[66:67],v17 offset0:2 offset1:3
ds_read2_b32 a[68:69],v17 offset0:128 offset1:129
ds_read2_b32 a[70:71],v17 offset0:130 offset1:131
ds_read2_b32 a[72:73],v17 offset0:16 offset1:17
ds_read2_b32 a[74:75],v17 offset0:18 offset1:19
ds_read2_b32 a[76:77],v17 offset0:144 offset1:145
ds_read2_b32 a[78:79],v17 offset0:146 offset1:147
ds_read2_b32 a[80:81],v17 offset0:32 offset1:33
ds_read2_b32 a[82:83],v17 offset0:34 offset1:35
ds_read2_b32 a[84:85],v17 offset0:160 offset1:161
ds_read2_b32 a[86:87],v17 offset0:162 offset1:163
ds_read2_b32 a[88:89],v17 offset0:48 offset1:49
ds_read2_b32 a[90:91],v17 offset0:50 offset1:51
ds_read2_b32 a[92:93],v17 offset0:176 offset1:177
ds_read2_b32 a[94:95],v17 offset0:178 offset1:179
ds_read2_b32 a[96:97],v17 offset0:64 offset1:65
ds_read2_b32 a[98:99],v17 offset0:66 offset1:67
ds_read2_b32 a[100:101],v17 offset0:192 offset1:193
ds_read2_b32 a[102:103],v17 offset0:194 offset1:195
ds_read2_b32 a[104:105],v17 offset0:80 offset1:81
ds_read2_b32 a[106:107],v17 offset0:82 offset1:83
ds_read2_b32 a[108:109],v17 offset0:208 offset1:209
ds_read2_b32 a[110:111],v17 offset0:210 offset1:211
ds_read2_b32 a[112:113],v17 offset0:96 offset1:97
ds_read2_b32 a[114:115],v17 offset0:98 offset1:99
ds_read2_b32 a[116:117],v17 offset0:224 offset1:225
ds_read2_b32 a[118:119],v17 offset0:226 offset1:227
ds_read2_b32 a[120:121],v17 offset0:112 offset1:113
ds_read2_b32 a[122:123],v17 offset0:114 offset1:115
ds_read2_b32 a[124:125],v17 offset0:240 offset1:241
ds_read2_b32 a[126:127],v17 offset0:242 offset1:243
Lcv_bpa_%=:
s_waitcnt vmcnt(0)
v_cmp_eq_u32 vcc,s53,v13
s_add_u32 s82,s82,1
s_cmp_eq_u64 vcc,-1
s_cbranch_scc1 Lcv_bqa_%=
s_sleep 1
buffer_load_dword v13,v8,s[36:39],s58 offen offset:0 sc1
s_cmp_lt_u32 s82,20000
s_cbranch_scc1 Lcv_bpa_%=
s_waitcnt vmcnt(0)
Lcv_bqa_%=:
s_add_u32 m0,s57,0
s_nop 0
D 0,49,3072
s_add_u32 m0,s57,12928
s_nop 0
D 0,55,3072
buffer_load_dword v13,v9,s[36:39],s58 offen offset:0 sc1
R 88,18,0
ds_read_b32 v92,v18 offset:16
R 104,18,1024
ds_read_b32 v108,v18 offset:1040
R 120,18,2048
ds_read_b32 v124,v18 offset:2064
R 136,18,3072
ds_read_b32 v140,v18 offset:3088
L 6
v_alignbyte_b32 v96,v89,v88,2
v_alignbyte_b32 v97,v90,v89,2
v_alignbyte_b32 v98,v91,v90,2
v_alignbyte_b32 v99,v92,v91,2
ds_write_b128 v18,v[96:99] offset:4160
L 5
v_alignbyte_b32 v112,v105,v104,2
v_alignbyte_b32 v113,v106,v105,2
v_alignbyte_b32 v114,v107,v106,2
v_alignbyte_b32 v115,v108,v107,2
ds_write_b128 v18,v[112:115] offset:5184
L 4
v_alignbyte_b32 v128,v121,v120,2
v_alignbyte_b32 v129,v122,v121,2
v_alignbyte_b32 v130,v123,v122,2
v_alignbyte_b32 v131,v124,v123,2
ds_write_b128 v18,v[128:131] offset:6208
L 3
v_alignbyte_b32 v144,v137,v136,2
v_alignbyte_b32 v145,v138,v137,2
v_alignbyte_b32 v146,v139,v138,2
v_alignbyte_b32 v147,v140,v139,2
ds_write_b128 v18,v[144:147] offset:7232
R 152,18,12928
ds_read_b32 v156,v18 offset:12944
R 168,18,13952
ds_read_b32 v172,v18 offset:13968
R 184,18,14976
ds_read_b32 v188,v18 offset:14992
R 200,18,16000
ds_read_b32 v204,v18 offset:16016
L 6
v_alignbyte_b32 v160,v153,v152,2
v_alignbyte_b32 v161,v154,v153,2
v_alignbyte_b32 v162,v155,v154,2
v_alignbyte_b32 v163,v156,v155,2
ds_write_b128 v18,v[160:163] offset:17088
L 5
v_alignbyte_b32 v176,v169,v168,2
v_alignbyte_b32 v177,v170,v169,2
v_alignbyte_b32 v178,v171,v170,2
v_alignbyte_b32 v179,v172,v171,2
ds_write_b128 v18,v[176:179] offset:18112
L 4
v_alignbyte_b32 v192,v185,v184,2
v_alignbyte_b32 v193,v186,v185,2
v_alignbyte_b32 v194,v187,v186,2
v_alignbyte_b32 v195,v188,v187,2
ds_write_b128 v18,v[192:195] offset:19136
L 3
v_alignbyte_b32 v208,v201,v200,2
v_alignbyte_b32 v209,v202,v201,2
v_alignbyte_b32 v210,v203,v202,2
v_alignbyte_b32 v211,v204,v203,2
ds_write_b128 v18,v[208:211] offset:20160
s_lshl_b32 s78,s44,11
s_add_u32 s78,s78,103424
v_add_u32 v18,s78,v4
v_add_u32 v4,v19,v4
s_waitcnt vmcnt(0)
v_cmp_eq_u32 vcc,s53,v13
s_mov_b32 s83,0
s_cmp_eq_u64 vcc,-1
s_cbranch_scc0 Lcv_a_%=
s_mov_b32 s83,1
s_add_u32 m0,s57,0
s_nop 0
D 0,49,2048
D 0,49,1024
D 0,49,0
s_add_u32 m0,s57,12928
s_nop 0
D 0,55,2048
D 0,55,1024
D 0,55,0
Lcv_a_%=:
s_cmp_eq_u32 s51,2
s_cbranch_scc1 Lcv_a2_%=
L 0
R 88,2,3584
R 92,2,3072
R 96,2,3648
R 100,2,3136
R 104,2,3712
R 108,2,3200
R 112,2,3776
R 116,2,3264
L 6
v_mfma_f32_16x16x32_bf16 v[40:43],a[0:3],v[88:91],0
R 120,2,3840
R 124,2,3328
v_mfma_f32_16x16x32_bf16 v[44:47],a[0:3],v[92:95],0
M 44,a[4:7],88
L 6
M 40,a[8:11],96
R 128,2,3904
R 132,2,3392
M 44,a[8:11],100
M 44,a[12:15],96
L 6
M 40,a[16:19],104
R 136,2,3968
R 140,2,3456
M 44,a[16:19],108
M 44,a[20:23],104
L 6
M 40,a[24:27],112
R 144,2,4032
R 148,2,3520
M 44,a[24:27],116
M 44,a[28:31],112
L 6
M 40,a[32:35],120
M 44,a[32:35],124
M 44,a[36:39],120
L 4
M 40,a[40:43],128
M 44,a[40:43],132
M 44,a[44:47],128
L 2
M 40,a[48:51],136
ds_read_b64 v[24:25],v3 offset:3584
ds_read_b64 v[26:27],v3 offset:3072
M 44,a[48:51],140
M 44,a[52:55],136
L 2
M 40,a[56:59],144
M 44,a[56:59],148
M 44,a[60:63],144
L 0
v_and_b32 v36,0xffff0000,v25
v_lshlrev_b32 v37,16,v25
v_and_b32 v38,0xffff0000,v24
v_lshlrev_b32 v39,16,v24
s_nop 0
v_fma_f32 v40,v36,s86,v40
v_fma_f32 v41,v37,s86,v41
v_fma_f32 v42,v38,s86,v42
v_fma_f32 v43,v39,s86,v43
v_cvt_pk_bf16_f32 v40,v40,v41
v_cvt_pk_bf16_f32 v41,v42,v43
ds_write_b64 v18,v[40:41] offset:0
v_and_b32 v36,0xffff0000,v27
v_lshlrev_b32 v37,16,v27
v_and_b32 v38,0xffff0000,v26
v_lshlrev_b32 v39,16,v26
v_fma_f32 v44,v36,s86,v44
v_fma_f32 v45,v37,s86,v45
v_fma_f32 v46,v38,s86,v46
v_fma_f32 v47,v39,s86,v47
v_cvt_pk_bf16_f32 v44,v44,v45
v_cvt_pk_bf16_f32 v45,v46,v47
ds_write_b64 v18,v[44:45] offset:512
s_branch Lcv_a3_%=
Lcv_a2_%=:
L 0
R 88,2,3584
R 92,2,3072
R 96,2,3648
R 100,2,3136
R 104,2,3712
R 108,2,3200
R 112,2,3776
R 116,2,3264
L 6
v_mfma_f32_16x16x32_bf16 v[40:43],a[0:3],v[88:91],0
R 120,2,3840
R 124,2,3328
v_mfma_f32_16x16x32_bf16 v[44:47],a[0:3],v[92:95],0
M 44,a[4:7],88
L 6
M 40,a[8:11],96
R 128,2,3904
R 132,2,3392
M 44,a[8:11],100
M 44,a[12:15],96
L 6
M 40,a[16:19],104
R 136,2,3968
R 140,2,3456
M 44,a[16:19],108
M 44,a[20:23],104
L 6
M 40,a[24:27],112
R 144,2,4032
R 148,2,3520
M 44,a[24:27],116
M 44,a[28:31],112
L 6
M 40,a[32:35],120
R 152,2,16512
R 156,2,16000
M 44,a[32:35],124
M 44,a[36:39],120
L 6
M 40,a[40:43],128
R 160,2,16576
R 164,2,16064
M 44,a[40:43],132
M 44,a[44:47],128
L 6
M 40,a[48:51],136
R 168,2,16640
R 172,2,16128
M 44,a[48:51],140
ds_read_b64 v[24:25],v3 offset:3584
ds_read_b64 v[26:27],v3 offset:3072
M 44,a[52:55],136
L 8
M 40,a[56:59],144
R 176,2,16704
R 180,2,16192
M 44,a[56:59],148
M 44,a[60:63],144
L 8
v_mfma_f32_16x16x32_bf16 v[48:51],a[64:67],v[152:155],0
R 184,2,16768
R 188,2,16256
v_mfma_f32_16x16x32_bf16 v[52:55],a[64:67],v[156:159],0
M 52,a[68:71],152
L 8
M 48,a[72:75],160
R 192,2,16832
R 196,2,16320
M 52,a[72:75],164
L 6
v_and_b32 v36,0xffff0000,v25
M 52,a[76:79],160
v_lshlrev_b32 v37,16,v25
v_and_b32 v38,0xffff0000,v24
v_lshlrev_b32 v39,16,v24
s_nop 0
M 48,a[80:83],168
R 200,2,16896
R 204,2,16384
M 52,a[80:83],172
v_fma_f32 v40,v36,s86,v40
v_fma_f32 v41,v37,s86,v41
M 52,a[84:87],168
v_fma_f32 v42,v38,s86,v42
v_fma_f32 v43,v39,s86,v43
v_cvt_pk_bf16_f32 v40,v40,v41
L 6
M 48,a[88:91],176
R 208,2,16960
R 212,2,16448
M 52,a[88:91],180
v_cvt_pk_bf16_f32 v41,v42,v43
ds_write_b64 v18,v[40:41] offset:0
M 52,a[92:95],176
v_and_b32 v36,0xffff0000,v27
v_lshlrev_b32 v37,16,v27
v_and_b32 v38,0xffff0000,v26
L 7
M 48,a[96:99],184
M 52,a[96:99],188
v_lshlrev_b32 v39,16,v26
v_fma_f32 v44,v36,s86,v44
M 52,a[100:103],184
v_fma_f32 v45,v37,s86,v45
v_fma_f32 v46,v38,s86,v46
v_fma_f32 v47,v39,s86,v47
L 5
M 48,a[104:107],192
M 52,a[104:107],196
v_cvt_pk_bf16_f32 v44,v44,v45
v_cvt_pk_bf16_f32 v45,v46,v47
M 52,a[108:111],192
ds_write_b64 v18,v[44:45] offset:512
L 4
M 48,a[112:115],200
ds_read_b64 v[24:25],v3 offset:16512
ds_read_b64 v[26:27],v3 offset:16000
M 52,a[112:115],204
M 52,a[116:119],200
L 4
M 48,a[120:123],208
M 52,a[120:123],212
M 52,a[124:127],208
L 0
v_and_b32 v36,0xffff0000,v25
v_lshlrev_b32 v37,16,v25
v_and_b32 v38,0xffff0000,v24
v_lshlrev_b32 v39,16,v24
s_nop 0
v_fma_f32 v48,v36,s87,v48
v_fma_f32 v49,v37,s87,v49
v_fma_f32 v50,v38,s87,v50
v_fma_f32 v51,v39,s87,v51
v_cvt_pk_bf16_f32 v48,v48,v49
v_cvt_pk_bf16_f32 v49,v50,v51
ds_write_b64 v18,v[48:49] offset:1024
v_and_b32 v36,0xffff0000,v27
v_lshlrev_b32 v37,16,v27
v_and_b32 v38,0xffff0000,v26
v_lshlrev_b32 v39,16,v26
v_fma_f32 v52,v36,s87,v52
v_fma_f32 v53,v37,s87,v53
v_fma_f32 v54,v38,s87,v54
v_fma_f32 v55,v39,s87,v55
v_cvt_pk_bf16_f32 v52,v52,v53
v_cvt_pk_bf16_f32 v53,v54,v55
ds_write_b64 v18,v[52:53] offset:1536
Lcv_a3_%=:
L 0
s_barrier
R 152,6,0
R 156,6,512
s_mov_b32 s78,98566144
L 1
buffer_store_dwordx4 v[152:155],v7,s[36:39],s78 offen offset:0 sc0 sc1
s_add_u32 s78,s56,99090432
L 0
buffer_store_dwordx4 v[156:159],v7,s[36:39],s78 offen offset:0 sc0 sc1
s_cmp_eq_u32 s83,1
s_cbranch_scc1 Lcv_b_%=
s_waitcnt vmcnt(0)
s_barrier
buffer_store_dword v23,v10,s[36:39],s48 offen offset:0 sc0 sc1
buffer_load_dword v13,v9,s[36:39],s58 offen offset:0 sc1
Lcv_bpb_%=:
)"
R"(s_waitcnt vmcnt(0)
v_cmp_eq_u32 vcc,s53,v13
s_add_u32 s82,s82,1
s_cmp_eq_u64 vcc,-1
s_cbranch_scc1 Lcv_bqb_%=
s_sleep 1
buffer_load_dword v13,v9,s[36:39],s58 offen offset:0 sc1
s_cmp_lt_u32 s82,20000
s_cbranch_scc1 Lcv_bpb_%=
s_waitcnt vmcnt(0)
Lcv_bqb_%=:
s_add_u32 m0,s57,0
s_nop 0
D 0,49,2048
D 0,49,1024
D 0,49,0
s_add_u32 m0,s57,12928
s_nop 0
D 0,55,2048
D 0,55,1024
D 0,55,0
s_waitcnt vmcnt(0)
Lcv_b_%=:
s_waitcnt vmcnt(2)
ds_read2_b32 v[88:89],v1 offset0:0 offset1:1
ds_read2_b32 v[90:91],v1 offset0:2 offset1:3
R 120,2,3584
ds_read2_b32 v[92:93],v1 offset0:128 offset1:129
ds_read2_b32 v[94:95],v1 offset0:130 offset1:131
R 124,2,3072
ds_read2_b32 v[96:97],v14 offset0:0 offset1:1
ds_read2_b32 v[98:99],v14 offset0:2 offset1:3
R 128,2,2560
ds_read2_b32 v[100:101],v14 offset0:128 offset1:129
ds_read2_b32 v[102:103],v14 offset0:130 offset1:131
R 132,2,2048
ds_read2_b32 v[104:105],v15 offset0:0 offset1:1
ds_read2_b32 v[106:107],v15 offset0:2 offset1:3
R 136,2,1536
ds_read2_b32 v[108:109],v15 offset0:128 offset1:129
ds_read2_b32 v[110:111],v15 offset0:130 offset1:131
R 140,2,1024
ds_read2_b32 v[112:113],v16 offset0:0 offset1:1
ds_read2_b32 v[114:115],v16 offset0:2 offset1:3
R 144,2,512
ds_read2_b32 v[116:117],v16 offset0:128 offset1:129
ds_read2_b32 v[118:119],v16 offset0:130 offset1:131
R 148,2,0
L 15
v_mfma_f32_16x16x32_bf16 v[40:43],v[88:91],v[128:131],0
ds_read2_b32 v[152:153],v1 offset0:16 offset1:17
M 40,v[92:95],124
ds_read2_b32 v[154:155],v1 offset0:18 offset1:19
M 40,v[96:99],120
L 14
R 184,2,3648
v_mfma_f32_16x16x32_bf16 v[44:47],v[88:91],v[132:135],0
ds_read2_b32 v[156:157],v1 offset0:144 offset1:145
M 44,v[92:95],128
M 44,v[96:99],124
ds_read2_b32 v[158:159],v1 offset0:146 offset1:147
M 44,v[100:103],120
L 14
R 188,2,3136
v_mfma_f32_16x16x32_bf16 v[48:51],v[88:91],v[136:139],0
ds_read2_b32 v[160:161],v14 offset0:16 offset1:17
M 48,v[92:95],132
ds_read2_b32 v[162:163],v14 offset0:18 offset1:19
M 48,v[96:99],128
M 48,v[100:103],124
R 192,2,2624
M 48,v[104:107],120
L 15
ds_read2_b32 v[164:165],v14 offset0:144 offset1:145
v_mfma_f32_16x16x32_bf16 v[52:55],v[88:91],v[140:143],0
ds_read2_b32 v[166:167],v14 offset0:146 offset1:147
M 52,v[92:95],136
R 196,2,2112
M 52,v[96:99],132
M 52,v[100:103],128
ds_read2_b32 v[168:169],v15 offset0:16 offset1:17
M 52,v[104:107],124
ds_read2_b32 v[170:171],v15 offset0:18 offset1:19
M 52,v[108:111],120
L 15
R 200,2,1600
v_mfma_f32_16x16x32_bf16 v[56:59],v[88:91],v[144:147],0
ds_read2_b32 v[172:173],v15 offset0:144 offset1:145
M 56,v[92:95],140
M 56,v[96:99],136
ds_read2_b32 v[174:175],v15 offset0:146 offset1:147
M 56,v[100:103],132
R 204,2,1088
M 56,v[104:107],128
ds_read2_b32 v[176:177],v16 offset0:16 offset1:17
M 56,v[108:111],124
ds_read2_b32 v[178:179],v16 offset0:18 offset1:19
M 56,v[112:115],120
L 15
v_mfma_f32_16x16x32_bf16 v[60:63],v[88:91],v[148:151],0
R 208,2,576
M 60,v[92:95],144
ds_read2_b32 v[180:181],v16 offset0:144 offset1:145
M 60,v[96:99],140
ds_read2_b32 v[182:183],v16 offset0:146 offset1:147
M 60,v[100:103],136
R 212,2,64
M 60,v[104:107],132
M 60,v[108:111],128
M 60,v[112:115],124
M 60,v[116:119],120
L 15
M 40,v[152:155],192
ds_read2_b32 v[88:89],v1 offset0:32 offset1:33
M 40,v[156:159],188
ds_read2_b32 v[90:91],v1 offset0:34 offset1:35
M 40,v[160:163],184
L 14
R 120,2,3712
M 44,v[152:155],196
ds_read2_b32 v[92:93],v1 offset0:160 offset1:161
M 44,v[156:159],192
M 44,v[160:163],188
ds_read2_b32 v[94:95],v1 offset0:162 offset1:163
M 44,v[164:167],184
L 14
R 124,2,3200
M 48,v[152:155],200
ds_read2_b32 v[96:97],v14 offset0:32 offset1:33
M 48,v[156:159],196
ds_read2_b32 v[98:99],v14 offset0:34 offset1:35
M 48,v[160:163],192
M 48,v[164:167],188
R 128,2,2688
M 48,v[168:171],184
L 15
ds_read2_b32 v[100:101],v14 offset0:160 offset1:161
M 52,v[152:155],204
ds_read2_b32 v[102:103],v14 offset0:162 offset1:163
M 52,v[156:159],200
R 132,2,2176
M 52,v[160:163],196
M 52,v[164:167],192
ds_read2_b32 v[104:105],v15 offset0:32 offset1:33
M 52,v[168:171],188
ds_read2_b32 v[106:107],v15 offset0:34 offset1:35
M 52,v[172:175],184
L 15
R 136,2,1664
M 56,v[152:155],208
ds_read2_b32 v[108:109],v15 offset0:160 offset1:161
M 56,v[156:159],204
M 56,v[160:163],200
ds_read2_b32 v[110:111],v15 offset0:162 offset1:163
M 56,v[164:167],196
R 140,2,1152
M 56,v[168:171],192
ds_read2_b32 v[112:113],v16 offset0:32 offset1:33
M 56,v[172:175],188
ds_read2_b32 v[114:115],v16 offset0:34 offset1:35
M 56,v[176:179],184
L 15
M 60,v[152:155],212
R 144,2,640
M 60,v[156:159],208
ds_read2_b32 v[116:117],v16 offset0:160 offset1:161
s_cmp_eq_u32 s83,0
s_cbranch_scc1 Lcv_c_%=
s_waitcnt vmcnt(0)
s_barrier
buffer_store_dword v23,v10,s[36:39],s48 offen offset:0 sc0 sc1
Lcv_c_%=:
M 60,v[160:163],204
ds_read2_b32 v[118:119],v16 offset0:162 offset1:163
M 60,v[164:167],200
R 148,2,128
M 60,v[168:171],196
M 60,v[172:175],192
M 60,v[176:179],188
M 60,v[180:183],184
L 15
M 40,v[88:91],128
ds_read2_b32 v[152:153],v1 offset0:48 offset1:49
M 40,v[92:95],124
ds_read2_b32 v[154:155],v1 offset0:50 offset1:51
M 40,v[96:99],120
L 14
R 184,2,3776
M 44,v[88:91],132
ds_read2_b32 v[156:157],v1 offset0:176 offset1:177
M 44,v[92:95],128
M 44,v[96:99],124
ds_read2_b32 v[158:159],v1 offset0:178 offset1:179
M 44,v[100:103],120
L 14
R 188,2,3264
M 48,v[88:91],136
ds_read2_b32 v[160:161],v14 offset0:48 offset1:49
M 48,v[92:95],132
ds_read2_b32 v[162:163],v14 offset0:50 offset1:51
M 48,v[96:99],128
M 48,v[100:103],124
R 192,2,2752
M 48,v[104:107],120
L 15
ds_read2_b32 v[164:165],v14 offset0:176 offset1:177
M 52,v[88:91],140
s_mov_b32 m0,163584
s_mov_b32 s78,0
ds_read2_b32 v[166:167],v14 offset0:178 offset1:179
M 52,v[92:95],136
R 196,2,2240
M 52,v[96:99],132
buffer_load_dword v11,s[40:43],s78 offen offset:0  lds
M 52,v[100:103],128
ds_read2_b32 v[168:169],v15 offset0:48 offset1:49
M 52,v[104:107],124
ds_read2_b32 v[170:171],v15 offset0:50 offset1:51
M 52,v[108:111],120
L 15
R 200,2,1728
M 56,v[88:91],144
ds_read2_b32 v[172:173],v15 offset0:176 offset1:177
M 56,v[92:95],140
M 56,v[96:99],136
ds_read2_b32 v[174:175],v15 offset0:178 offset1:179
M 56,v[100:103],132
R 204,2,1216
M 56,v[104:107],128
ds_read2_b32 v[176:177],v16 offset0:48 offset1:49
M 56,v[108:111],124
ds_read2_b32 v[178:179],v16 offset0:50 offset1:51
M 56,v[112:115],120
L 15
M 60,v[88:91],148
R 208,2,704
M 60,v[92:95],144
ds_read2_b32 v[180:181],v16 offset0:176 offset1:177
M 60,v[96:99],140
ds_read2_b32 v[182:183],v16 offset0:178 offset1:179
M 60,v[100:103],136
R 212,2,192
M 60,v[104:107],132
M 60,v[108:111],128
M 60,v[112:115],124
M 60,v[116:119],120
L 15
M 40,v[152:155],192
ds_read2_b32 v[88:89],v1 offset0:64 offset1:65
M 40,v[156:159],188
ds_read2_b32 v[90:91],v1 offset0:66 offset1:67
M 40,v[160:163],184
L 14
R 120,2,3840
M 44,v[152:155],196
ds_read2_b32 v[92:93],v1 offset0:192 offset1:193
M 44,v[156:159],192
M 44,v[160:163],188
ds_read2_b32 v[94:95],v1 offset0:194 offset1:195
M 44,v[164:167],184
L 14
R 124,2,3328
M 48,v[152:155],200
ds_read2_b32 v[96:97],v14 offset0:64 offset1:65
M 48,v[156:159],196
ds_read2_b32 v[98:99],v14 offset0:66 offset1:67
M 48,v[160:163],192
M 48,v[164:167],188
R 128,2,2816
M 48,v[168:171],184
L 15
ds_read2_b32 v[100:101],v14 offset0:192 offset1:193
M 52,v[152:155],204
ds_read2_b32 v[102:103],v14 offset0:194 offset1:195
M 52,v[156:159],200
R 132,2,2304
M 52,v[160:163],196
M 52,v[164:167],192
ds_read2_b32 v[104:105],v15 offset0:64 offset1:65
M 52,v[168:171],188
ds_read2_b32 v[106:107],v15 offset0:66 offset1:67
M 52,v[172:175],184
L 15
R 136,2,1792
M 56,v[152:155],208
ds_read2_b32 v[108:109],v15 offset0:192 offset1:193
M 56,v[156:159],204
M 56,v[160:163],200
ds_read2_b32 v[110:111],v15 offset0:194 offset1:195
M 56,v[164:167],196
R 140,2,1280
M 56,v[168:171],192
ds_read2_b32 v[112:113],v16 offset0:64 offset1:65
M 56,v[172:175],188
ds_read2_b32 v[114:115],v16 offset0:66 offset1:67
M 56,v[176:179],184
L 15
M 60,v[152:155],212
R 144,2,768
M 60,v[156:159],208
ds_read2_b32 v[116:117],v16 offset0:192 offset1:193
M 60,v[160:163],204
ds_read2_b32 v[118:119],v16 offset0:194 offset1:195
M 60,v[164:167],200
R 148,2,256
M 60,v[168:171],196
M 60,v[172:175],192
M 60,v[176:179],188
M 60,v[180:183],184
L 15
M 40,v[88:91],128
ds_read2_b32 v[152:153],v1 offset0:80 offset1:81
M 40,v[92:95],124
ds_read2_b32 v[154:155],v1 offset0:82 offset1:83
M 40,v[96:99],120
L 14
R 184,2,3904
M 44,v[88:91],132
ds_read2_b32 v[156:157],v1 offset0:208 offset1:209
M 44,v[92:95],128
M 44,v[96:99],124
ds_read2_b32 v[158:159],v1 offset0:210 offset1:211
M 44,v[100:103],120
L 14
R 188,2,3392
M 48,v[88:91],136
ds_read2_b32 v[160:161],v14 offset0:80 offset1:81
M 48,v[92:95],132
ds_read2_b32 v[162:163],v14 offset0:82 offset1:83
M 48,v[96:99],128
M 48,v[100:103],124
R 192,2,2880
M 48,v[104:107],120
L 15
ds_read2_b32 v[164:165],v14 offset0:208 offset1:209
M 52,v[88:91],140
ds_read2_b32 v[166:167],v14 offset0:210 offset1:211
M 52,v[92:95],136
R 196,2,2368
M 52,v[96:99],132
M 52,v[100:103],128
ds_read2_b32 v[168:169],v15 offset0:80 offset1:81
M 52,v[104:107],124
ds_read2_b32 v[170:171],v15 offset0:82 offset1:83
M 52,v[108:111],120
L 15
R 200,2,1856
M 56,v[88:91],144
ds_read2_b32 v[172:173],v15 offset0:208 offset1:209
M 56,v[92:95],140
M 56,v[96:99],136
ds_read2_b32 v[174:175],v15 offset0:210 offset1:211
M 56,v[100:103],132
R 204,2,1344
M 56,v[104:107],128
ds_read2_b32 v[176:177],v16 offset0:80 offset1:81
M 56,v[108:111],124
ds_read2_b32 v[178:179],v16 offset0:82 offset1:83
M 56,v[112:115],120
L 15
M 60,v[88:91],148
R 208,2,832
M 60,v[92:95],144
ds_read2_b32 v[180:181],v16 offset0:208 offset1:209
M 60,v[96:99],140
ds_read2_b32 v[182:183],v16 offset0:210 offset1:211
M 60,v[100:103],136
R 212,2,320
M 60,v[104:107],132
M 60,v[108:111],128
M 60,v[112:115],124
M 60,v[116:119],120
L 15
M 40,v[152:155],192
ds_read2_b32 v[88:89],v1 offset0:96 offset1:97
M 40,v[156:159],188
ds_read2_b32 v[90:91],v1 offset0:98 offset1:99
M 40,v[160:163],184
L 14
R 120,2,3968
M 44,v[152:155],196
s_mov_b32 m0,163584
s_mov_b32 s78,3741696
ds_read2_b32 v[92:93],v1 offset0:224 offset1:225
M 44,v[156:159],192
M 44,v[160:163],188
ds_read2_b32 v[94:95],v1 offset0:226 offset1:227
M 44,v[164:167],184
L 14
R 124,2,3456
M 48,v[152:155],200
ds_read2_b32 v[96:97],v14 offset0:96 offset1:97
M 48,v[156:159],196
ds_read2_b32 v[98:99],v14 offset0:98 offset1:99
M 48,v[160:163],192
buffer_load_dword v11,s[40:43],s78 offen offset:0  lds
M 48,v[164:167],188
R 128,2,2944
M 48,v[168:171],184
L 15
ds_read2_b32 v[100:101],v14 offset0:224 offset1:225
M 52,v[152:155],204
ds_read2_b32 v[102:103],v14 offset0:226 offset1:227
M 52,v[156:159],200
R 132,2,2432
M 52,v[160:163],196
M 52,v[164:167],192
ds_read2_b32 v[104:105],v15 offset0:96 offset1:97
M 52,v[168:171],188
ds_read2_b32 v[106:107],v15 offset0:98 offset1:99
M 52,v[172:175],184
L 15
R 136,2,1920
M 56,v[152:155],208
ds_read2_b32 v[108:109],v15 offset0:224 offset1:225
M 56,v[156:159],204
M 56,v[160:163],200
ds_read2_b32 v[110:111],v15 offset0:226 offset1:227
M 56,v[164:167],196
R 140,2,1408
M 56,v[168:171],192
ds_read2_b32 v[112:113],v16 offset0:96 offset1:97
M 56,v[172:175],188
ds_read2_b32 v[114:115],v16 offset0:98 offset1:99
M 56,v[176:179],184
L 15
M 60,v[152:155],212
R 144,2,896
M 60,v[156:159],208
ds_read2_b32 v[116:117],v16 offset0:224 offset1:225
M 60,v[160:163],204
ds_read2_b32 v[118:119],v16 offset0:226 offset1:227
M 60,v[164:167],200
R 148,2,384
M 60,v[168:171],196
M 60,v[172:175],192
M 60,v[176:179],188
M 60,v[180:183],184
L 15
M 40,v[88:91],128
ds_read2_b32 v[152:153],v1 offset0:112 offset1:113
M 40,v[92:95],124
ds_read2_b32 v[154:155],v1 offset0:114 offset1:115
M 40,v[96:99],120
L 14
R 184,2,4032
M 44,v[88:91],132
ds_read2_b32 v[156:157],v1 offset0:240 offset1:241
M 44,v[92:95],128
M 44,v[96:99],124
ds_read2_b32 v[158:159],v1 offset0:242 offset1:243
v_add_u32 v1,12928,v1
M 44,v[100:103],120
L 14
R 188,2,3520
M 48,v[88:91],136
ds_read2_b32 v[160:161],v14 offset0:112 offset1:113
M 48,v[92:95],132
ds_read2_b32 v[162:163],v14 offset0:114 offset1:115
M 48,v[96:99],128
M 48,v[100:103],124
R 192,2,3008
M 48,v[104:107],120
L 15
ds_read2_b32 v[164:165],v14 offset0:240 offset1:241
M 52,v[88:91],140
ds_read2_b32 v[166:167],v14 offset0:242 offset1:243
v_add_u32 v14,12928,v14
M 52,v[92:95],136
R 196,2,2496
M 52,v[96:99],132
M 52,v[100:103],128
ds_read2_b32 v[168:169],v15 offset0:112 offset1:113
M 52,v[104:107],124
ds_read2_b32 v[170:171],v15 offset0:114 offset1:115
M 52,v[108:111],120
L 15
R 200,2,1984
M 56,v[88:91],144
ds_read2_b32 v[172:173],v15 offset0:240 offset1:241
M 56,v[92:95],140
M 56,v[96:99],136
ds_read2_b32 v[174:175],v15 offset0:242 offset1:243
v_add_u32 v15,12928,v15
M 56,v[100:103],132
R 204,2,1472
M 56,v[104:107],128
ds_read2_b32 v[176:177],v16 offset0:112 offset1:113
M 56,v[108:111],124
ds_read2_b32 v[178:179],v16 offset0:114 offset1:115
M 56,v[112:115],120
L 15
M 60,v[88:91],148
R 208,2,960
M 60,v[92:95],144
ds_read2_b32 v[180:181],v16 offset0:240 offset1:241
M 60,v[96:99],140
ds_read2_b32 v[182:183],v16 offset0:242 offset1:243
v_add_u32 v16,12928,v16
M 60,v[100:103],136
)"
R"(R 212,2,448
M 60,v[104:107],132
M 60,v[108:111],128
M 60,v[112:115],124
M 60,v[116:119],120
L 15
M 40,v[152:155],192
ds_read2_b32 v[88:89],v1 offset0:0 offset1:1
M 40,v[156:159],188
ds_read2_b32 v[90:91],v1 offset0:2 offset1:3
M 40,v[160:163],184
L 14
R 120,2,16512
M 44,v[152:155],196
ds_read2_b32 v[92:93],v1 offset0:128 offset1:129
M 44,v[156:159],192
M 44,v[160:163],188
ds_read2_b32 v[94:95],v1 offset0:130 offset1:131
M 44,v[164:167],184
L 14
R 124,2,16000
M 48,v[152:155],200
ds_read2_b32 v[96:97],v14 offset0:0 offset1:1
M 48,v[156:159],196
ds_read2_b32 v[98:99],v14 offset0:2 offset1:3
M 48,v[160:163],192
M 48,v[164:167],188
R 128,2,15488
M 48,v[168:171],184
L 15
ds_read2_b32 v[100:101],v14 offset0:128 offset1:129
M 52,v[152:155],204
ds_read2_b32 v[102:103],v14 offset0:130 offset1:131
M 52,v[156:159],200
R 132,2,14976
M 52,v[160:163],196
M 52,v[164:167],192
ds_read2_b32 v[104:105],v15 offset0:0 offset1:1
M 52,v[168:171],188
ds_read2_b32 v[106:107],v15 offset0:2 offset1:3
M 52,v[172:175],184
L 15
R 136,2,14464
M 56,v[152:155],208
ds_read2_b32 v[108:109],v15 offset0:128 offset1:129
M 56,v[156:159],204
M 56,v[160:163],200
ds_read2_b32 v[110:111],v15 offset0:130 offset1:131
M 56,v[164:167],196
R 140,2,13952
M 56,v[168:171],192
ds_read2_b32 v[112:113],v16 offset0:0 offset1:1
M 56,v[172:175],188
ds_read2_b32 v[114:115],v16 offset0:2 offset1:3
M 56,v[176:179],184
L 15
M 60,v[152:155],212
R 144,2,13440
M 60,v[156:159],208
ds_read2_b32 v[116:117],v16 offset0:128 offset1:129
M 60,v[160:163],204
ds_read2_b32 v[118:119],v16 offset0:130 offset1:131
M 60,v[164:167],200
R 148,2,12928
M 60,v[168:171],196
M 60,v[172:175],192
M 60,v[176:179],188
M 60,v[180:183],184
s_cmp_eq_u32 s51,2
s_cbranch_scc1 Lcv_d_%=
ds_read_b64 v[24:25],v3 offset:2560
ds_read_b64 v[26:27],v3 offset:2048
ds_read_b64 v[28:29],v3 offset:1536
ds_read_b64 v[30:31],v3 offset:1024
ds_read_b64 v[32:33],v3 offset:512
ds_read_b64 v[34:35],v3 offset:0
L 0
v_and_b32 v36,0xffff0000,v25
v_lshlrev_b32 v37,16,v25
v_and_b32 v38,0xffff0000,v24
v_lshlrev_b32 v39,16,v24
v_fma_f32 v40,v36,s86,v40
v_fma_f32 v41,v37,s86,v41
v_fma_f32 v42,v38,s86,v42
v_fma_f32 v43,v39,s86,v43
v_cvt_pk_bf16_f32 v40,v40,v41
v_cvt_pk_bf16_f32 v41,v42,v43
ds_write_b64 v4,v[40:41] offset:0
v_and_b32 v36,0xffff0000,v27
v_lshlrev_b32 v37,16,v27
v_and_b32 v38,0xffff0000,v26
v_lshlrev_b32 v39,16,v26
v_fma_f32 v44,v36,s86,v44
v_fma_f32 v45,v37,s86,v45
v_fma_f32 v46,v38,s86,v46
v_fma_f32 v47,v39,s86,v47
v_cvt_pk_bf16_f32 v44,v44,v45
v_cvt_pk_bf16_f32 v45,v46,v47
ds_write_b64 v4,v[44:45] offset:512
v_and_b32 v36,0xffff0000,v29
v_lshlrev_b32 v37,16,v29
v_and_b32 v38,0xffff0000,v28
v_lshlrev_b32 v39,16,v28
v_fma_f32 v48,v36,s86,v48
v_fma_f32 v49,v37,s86,v49
v_fma_f32 v50,v38,s86,v50
v_fma_f32 v51,v39,s86,v51
v_cvt_pk_bf16_f32 v48,v48,v49
v_cvt_pk_bf16_f32 v49,v50,v51
ds_write_b64 v4,v[48:49] offset:1024
v_and_b32 v36,0xffff0000,v31
v_lshlrev_b32 v37,16,v31
v_and_b32 v38,0xffff0000,v30
v_lshlrev_b32 v39,16,v30
v_fma_f32 v52,v36,s86,v52
v_fma_f32 v53,v37,s86,v53
v_fma_f32 v54,v38,s86,v54
v_fma_f32 v55,v39,s86,v55
v_cvt_pk_bf16_f32 v52,v52,v53
v_cvt_pk_bf16_f32 v53,v54,v55
ds_write_b64 v4,v[52:53] offset:1536
v_and_b32 v36,0xffff0000,v33
v_lshlrev_b32 v37,16,v33
v_and_b32 v38,0xffff0000,v32
v_lshlrev_b32 v39,16,v32
v_fma_f32 v56,v36,s86,v56
v_fma_f32 v57,v37,s86,v57
v_fma_f32 v58,v38,s86,v58
v_fma_f32 v59,v39,s86,v59
v_cvt_pk_bf16_f32 v56,v56,v57
v_cvt_pk_bf16_f32 v57,v58,v59
ds_write_b64 v4,v[56:57] offset:2048
v_and_b32 v36,0xffff0000,v35
v_lshlrev_b32 v37,16,v35
v_and_b32 v38,0xffff0000,v34
v_lshlrev_b32 v39,16,v34
v_fma_f32 v60,v36,s86,v60
v_fma_f32 v61,v37,s86,v61
v_fma_f32 v62,v38,s86,v62
v_fma_f32 v63,v39,s86,v63
v_cvt_pk_bf16_f32 v60,v60,v61
v_cvt_pk_bf16_f32 v61,v62,v63
ds_write_b64 v4,v[60:61] offset:2560
s_branch Lcv_e_%=
Lcv_d_%=:
L 15
v_mfma_f32_16x16x32_bf16 v[64:67],v[88:91],v[128:131],0
ds_read2_b32 v[152:153],v1 offset0:16 offset1:17
ds_read_b64 v[24:25],v3 offset:2560
M 64,v[92:95],124
ds_read2_b32 v[154:155],v1 offset0:18 offset1:19
ds_read_b64 v[26:27],v3 offset:2048
M 64,v[96:99],120
L 15
R 184,2,16576
ds_read_b64 v[28:29],v3 offset:1536
v_mfma_f32_16x16x32_bf16 v[68:71],v[88:91],v[132:135],0
ds_read2_b32 v[156:157],v1 offset0:144 offset1:145
ds_read_b64 v[30:31],v3 offset:1024
M 68,v[92:95],128
ds_read_b64 v[32:33],v3 offset:512
ds_read_b64 v[34:35],v3 offset:0
M 68,v[96:99],124
ds_read2_b32 v[158:159],v1 offset0:146 offset1:147
M 68,v[100:103],120
L 15
R 188,2,16064
v_mfma_f32_16x16x32_bf16 v[72:75],v[88:91],v[136:139],0
ds_read2_b32 v[160:161],v14 offset0:16 offset1:17
M 72,v[92:95],132
s_mov_b32 m0,163584
s_mov_b32 s78,0
ds_read2_b32 v[162:163],v14 offset0:18 offset1:19
M 72,v[96:99],128
M 72,v[100:103],124
L 4
R 192,2,15552
v_and_b32 v36,0xffff0000,v25
M 72,v[104:107],120
s_nop 0
ds_read2_b32 v[164:165],v14 offset0:144 offset1:145
v_lshlrev_b32 v37,16,v25
v_mfma_f32_16x16x32_bf16 v[76:79],v[88:91],v[140:143],0
ds_read2_b32 v[166:167],v14 offset0:146 offset1:147
v_and_b32 v38,0xffff0000,v24
M 76,v[92:95],136
R 196,2,15040
v_lshlrev_b32 v39,16,v24
M 76,v[96:99],132
buffer_load_dword v12,s[60:63],s78 offen offset:0  lds
M 76,v[100:103],128
ds_read2_b32 v[168:169],v15 offset0:16 offset1:17
v_fma_f32 v40,v36,s86,v40
M 76,v[104:107],124
ds_read2_b32 v[170:171],v15 offset0:18 offset1:19
v_fma_f32 v41,v37,s86,v41
M 76,v[108:111],120
s_nop 0
R 200,2,14528
v_fma_f32 v42,v38,s86,v42
v_mfma_f32_16x16x32_bf16 v[80:83],v[88:91],v[144:147],0
ds_read2_b32 v[172:173],v15 offset0:144 offset1:145
v_fma_f32 v43,v39,s86,v43
M 80,v[92:95],140
v_cvt_pk_bf16_f32 v40,v40,v41
v_cvt_pk_bf16_f32 v41,v42,v43
M 80,v[96:99],136
ds_read2_b32 v[174:175],v15 offset0:146 offset1:147
v_and_b32 v36,0xffff0000,v27
M 80,v[100:103],132
R 204,2,14016
v_lshlrev_b32 v37,16,v27
M 80,v[104:107],128
ds_read2_b32 v[176:177],v16 offset0:16 offset1:17
v_and_b32 v38,0xffff0000,v26
M 80,v[108:111],124
ds_read2_b32 v[178:179],v16 offset0:18 offset1:19
v_lshlrev_b32 v39,16,v26
M 80,v[112:115],120
s_nop 0
ds_write_b64 v4,v[40:41] offset:0
v_mfma_f32_16x16x32_bf16 v[84:87],v[88:91],v[148:151],0
R 208,2,13504
v_fma_f32 v44,v36,s86,v44
M 84,v[92:95],144
ds_read2_b32 v[180:181],v16 offset0:144 offset1:145
v_fma_f32 v45,v37,s86,v45
M 84,v[96:99],140
ds_read2_b32 v[182:183],v16 offset0:146 offset1:147
v_fma_f32 v46,v38,s86,v46
M 84,v[100:103],136
R 212,2,12992
v_fma_f32 v47,v39,s86,v47
M 84,v[104:107],132
v_cvt_pk_bf16_f32 v44,v44,v45
v_cvt_pk_bf16_f32 v45,v46,v47
M 84,v[108:111],128
ds_write_b64 v4,v[44:45] offset:512
M 84,v[112:115],124
v_and_b32 v36,0xffff0000,v29
v_lshlrev_b32 v37,16,v29
M 84,v[116:119],120
L 15
v_and_b32 v38,0xffff0000,v28
v_lshlrev_b32 v39,16,v28
M 64,v[152:155],192
ds_read2_b32 v[88:89],v1 offset0:32 offset1:33
v_fma_f32 v48,v36,s86,v48
M 64,v[156:159],188
ds_read2_b32 v[90:91],v1 offset0:34 offset1:35
v_fma_f32 v49,v37,s86,v49
M 64,v[160:163],184
L 15
R 120,2,16640
v_fma_f32 v50,v38,s86,v50
M 68,v[152:155],196
ds_read2_b32 v[92:93],v1 offset0:160 offset1:161
v_fma_f32 v51,v39,s86,v51
M 68,v[156:159],192
v_cvt_pk_bf16_f32 v48,v48,v49
v_cvt_pk_bf16_f32 v49,v50,v51
M 68,v[160:163],188
ds_read2_b32 v[94:95],v1 offset0:162 offset1:163
v_and_b32 v36,0xffff0000,v31
M 68,v[164:167],184
L 15
R 124,2,16128
v_lshlrev_b32 v37,16,v31
M 72,v[152:155],200
ds_read2_b32 v[96:97],v14 offset0:32 offset1:33
v_and_b32 v38,0xffff0000,v30
M 72,v[156:159],196
ds_read2_b32 v[98:99],v14 offset0:34 offset1:35
v_lshlrev_b32 v39,16,v30
M 72,v[160:163],192
ds_write_b64 v4,v[48:49] offset:1024
M 72,v[164:167],188
R 128,2,15616
v_fma_f32 v52,v36,s86,v52
M 72,v[168:171],184
L 15
ds_read2_b32 v[100:101],v14 offset0:160 offset1:161
v_fma_f32 v53,v37,s86,v53
M 76,v[152:155],204
ds_read2_b32 v[102:103],v14 offset0:162 offset1:163
v_fma_f32 v54,v38,s86,v54
M 76,v[156:159],200
R 132,2,15104
v_fma_f32 v55,v39,s86,v55
M 76,v[160:163],196
v_cvt_pk_bf16_f32 v52,v52,v53
v_cvt_pk_bf16_f32 v53,v54,v55
M 76,v[164:167],192
ds_read2_b32 v[104:105],v15 offset0:32 offset1:33
v_and_b32 v36,0xffff0000,v33
M 76,v[168:171],188
ds_read2_b32 v[106:107],v15 offset0:34 offset1:35
v_lshlrev_b32 v37,16,v33
M 76,v[172:175],184
L 15
R 136,2,14592
v_and_b32 v38,0xffff0000,v32
M 80,v[152:155],208
ds_read2_b32 v[108:109],v15 offset0:160 offset1:161
v_lshlrev_b32 v39,16,v32
M 80,v[156:159],204
ds_write_b64 v4,v[52:53] offset:1536
M 80,v[160:163],200
ds_read2_b32 v[110:111],v15 offset0:162 offset1:163
v_fma_f32 v56,v36,s86,v56
M 80,v[164:167],196
R 140,2,14080
v_fma_f32 v57,v37,s86,v57
M 80,v[168:171],192
ds_read2_b32 v[112:113],v16 offset0:32 offset1:33
v_fma_f32 v58,v38,s86,v58
M 80,v[172:175],188
ds_read2_b32 v[114:115],v16 offset0:34 offset1:35
v_fma_f32 v59,v39,s86,v59
M 80,v[176:179],184
L 15
v_cvt_pk_bf16_f32 v56,v56,v57
v_cvt_pk_bf16_f32 v57,v58,v59
M 84,v[152:155],212
R 144,2,13568
v_and_b32 v36,0xffff0000,v35
M 84,v[156:159],208
ds_read2_b32 v[116:117],v16 offset0:160 offset1:161
v_lshlrev_b32 v37,16,v35
M 84,v[160:163],204
ds_read2_b32 v[118:119],v16 offset0:162 offset1:163
v_and_b32 v38,0xffff0000,v34
M 84,v[164:167],200
R 148,2,13056
v_lshlrev_b32 v39,16,v34
M 84,v[168:171],196
ds_write_b64 v4,v[56:57] offset:2048
M 84,v[172:175],192
v_fma_f32 v60,v36,s86,v60
v_fma_f32 v61,v37,s86,v61
M 84,v[176:179],188
v_fma_f32 v62,v38,s86,v62
v_fma_f32 v63,v39,s86,v63
M 84,v[180:183],184
L 15
v_cvt_pk_bf16_f32 v60,v60,v61
v_cvt_pk_bf16_f32 v61,v62,v63
M 64,v[88:91],128
ds_read2_b32 v[152:153],v1 offset0:48 offset1:49
M 64,v[92:95],124
ds_read2_b32 v[154:155],v1 offset0:50 offset1:51
M 64,v[96:99],120
L 15
R 184,2,16704
M 68,v[88:91],132
ds_read2_b32 v[156:157],v1 offset0:176 offset1:177
M 68,v[92:95],128
ds_write_b64 v4,v[60:61] offset:2560
M 68,v[96:99],124
ds_read2_b32 v[158:159],v1 offset0:178 offset1:179
M 68,v[100:103],120
L 15
R 188,2,16192
M 72,v[88:91],136
ds_read2_b32 v[160:161],v14 offset0:48 offset1:49
M 72,v[92:95],132
ds_read2_b32 v[162:163],v14 offset0:50 offset1:51
M 72,v[96:99],128
M 72,v[100:103],124
R 192,2,15680
M 72,v[104:107],120
L 15
ds_read2_b32 v[164:165],v14 offset0:176 offset1:177
M 76,v[88:91],140
ds_read2_b32 v[166:167],v14 offset0:178 offset1:179
M 76,v[92:95],136
R 196,2,15168
M 76,v[96:99],132
M 76,v[100:103],128
ds_read2_b32 v[168:169],v15 offset0:48 offset1:49
M 76,v[104:107],124
ds_read2_b32 v[170:171],v15 offset0:50 offset1:51
M 76,v[108:111],120
L 15
R 200,2,14656
M 80,v[88:91],144
ds_read2_b32 v[172:173],v15 offset0:176 offset1:177
M 80,v[92:95],140
M 80,v[96:99],136
ds_read2_b32 v[174:175],v15 offset0:178 offset1:179
M 80,v[100:103],132
R 204,2,14144
M 80,v[104:107],128
s_mov_b32 m0,163584
s_mov_b32 s78,1847296
ds_read2_b32 v[176:177],v16 offset0:48 offset1:49
M 80,v[108:111],124
ds_read2_b32 v[178:179],v16 offset0:50 offset1:51
M 80,v[112:115],120
L 15
buffer_load_dword v12,s[60:63],s78 offen offset:0  lds
M 84,v[88:91],148
R 208,2,13632
M 84,v[92:95],144
ds_read2_b32 v[180:181],v16 offset0:176 offset1:177
M 84,v[96:99],140
ds_read2_b32 v[182:183],v16 offset0:178 offset1:179
M 84,v[100:103],136
R 212,2,13120
M 84,v[104:107],132
M 84,v[108:111],128
M 84,v[112:115],124
M 84,v[116:119],120
L 15
M 64,v[152:155],192
ds_read2_b32 v[88:89],v1 offset0:64 offset1:65
M 64,v[156:159],188
ds_read2_b32 v[90:91],v1 offset0:66 offset1:67
M 64,v[160:163],184
L 14
R 120,2,16768
M 68,v[152:155],196
ds_read2_b32 v[92:93],v1 offset0:192 offset1:193
M 68,v[156:159],192
M 68,v[160:163],188
ds_read2_b32 v[94:95],v1 offset0:194 offset1:195
M 68,v[164:167],184
L 14
R 124,2,16256
M 72,v[152:155],200
ds_read2_b32 v[96:97],v14 offset0:64 offset1:65
M 72,v[156:159],196
ds_read2_b32 v[98:99],v14 offset0:66 offset1:67
M 72,v[160:163],192
M 72,v[164:167],188
R 128,2,15744
M 72,v[168:171],184
L 15
ds_read2_b32 v[100:101],v14 offset0:192 offset1:193
M 76,v[152:155],204
ds_read2_b32 v[102:103],v14 offset0:194 offset1:195
M 76,v[156:159],200
R 132,2,15232
M 76,v[160:163],196
M 76,v[164:167],192
ds_read2_b32 v[104:105],v15 offset0:64 offset1:65
M 76,v[168:171],188
ds_read2_b32 v[106:107],v15 offset0:66 offset1:67
M 76,v[172:175],184
L 15
R 136,2,14720
M 80,v[152:155],208
ds_read2_b32 v[108:109],v15 offset0:192 offset1:193
M 80,v[156:159],204
M 80,v[160:163],200
ds_read2_b32 v[110:111],v15 offset0:194 offset1:195
M 80,v[164:167],196
R 140,2,14208
M 80,v[168:171],192
ds_read2_b32 v[112:113],v16 offset0:64 offset1:65
M 80,v[172:175],188
ds_read2_b32 v[114:115],v16 offset0:66 offset1:67
M 80,v[176:179],184
L 15
M 84,v[152:155],212
R 144,2,13696
M 84,v[156:159],208
ds_read2_b32 v[116:117],v16 offset0:192 offset1:193
M 84,v[160:163],204
ds_read2_b32 v[118:119],v16 offset0:194 offset1:195
M 84,v[164:167],200
R 148,2,13184
M 84,v[168:171],196
M 84,v[172:175],192
M 84,v[176:179],188
M 84,v[180:183],184
L 15
M 64,v[88:91],128
ds_read2_b32 v[152:153],v1 offset0:80 offset1:81
M 64,v[92:95],124
ds_read2_b32 v[154:155],v1 offset0:82 offset1:83
M 64,v[96:99],120
L 14
R 184,2,16832
M 68,v[88:91],132
ds_read2_b32 v[156:157],v1 offset0:208 offset1:209
M 68,v[92:95],128
M 68,v[96:99],124
ds_read2_b32 v[158:159],v1 offset0:210 offset1:211
M 68,v[100:103],120
L 14
R 188,2,16320
M 72,v[88:91],136
ds_read2_b32 v[160:161],v14 offset0:80 offset1:81
M 72,v[92:95],132
ds_read2_b32 v[162:163],v14 offset0:82 offset1:83
M 72,v[96:99],128
M 72,v[100:103],124
R 192,2,15808
M 72,v[104:107],120
)"
R"(L 15
ds_read2_b32 v[164:165],v14 offset0:208 offset1:209
M 76,v[88:91],140
ds_read2_b32 v[166:167],v14 offset0:210 offset1:211
M 76,v[92:95],136
R 196,2,15296
M 76,v[96:99],132
M 76,v[100:103],128
ds_read2_b32 v[168:169],v15 offset0:80 offset1:81
M 76,v[104:107],124
ds_read2_b32 v[170:171],v15 offset0:82 offset1:83
M 76,v[108:111],120
L 15
R 200,2,14784
M 80,v[88:91],144
ds_read2_b32 v[172:173],v15 offset0:208 offset1:209
M 80,v[92:95],140
M 80,v[96:99],136
ds_read2_b32 v[174:175],v15 offset0:210 offset1:211
M 80,v[100:103],132
R 204,2,14272
M 80,v[104:107],128
ds_read2_b32 v[176:177],v16 offset0:80 offset1:81
M 80,v[108:111],124
ds_read2_b32 v[178:179],v16 offset0:82 offset1:83
M 80,v[112:115],120
L 15
M 84,v[88:91],148
R 208,2,13760
M 84,v[92:95],144
ds_read2_b32 v[180:181],v16 offset0:208 offset1:209
M 84,v[96:99],140
ds_read2_b32 v[182:183],v16 offset0:210 offset1:211
M 84,v[100:103],136
R 212,2,13248
M 84,v[104:107],132
M 84,v[108:111],128
M 84,v[112:115],124
M 84,v[116:119],120
L 15
M 64,v[152:155],192
ds_read2_b32 v[88:89],v1 offset0:96 offset1:97
M 64,v[156:159],188
ds_read2_b32 v[90:91],v1 offset0:98 offset1:99
M 64,v[160:163],184
L 14
R 120,2,16896
M 68,v[152:155],196
ds_read2_b32 v[92:93],v1 offset0:224 offset1:225
M 68,v[156:159],192
M 68,v[160:163],188
ds_read2_b32 v[94:95],v1 offset0:226 offset1:227
M 68,v[164:167],184
L 14
R 124,2,16384
M 72,v[152:155],200
ds_read2_b32 v[96:97],v14 offset0:96 offset1:97
M 72,v[156:159],196
ds_read2_b32 v[98:99],v14 offset0:98 offset1:99
M 72,v[160:163],192
M 72,v[164:167],188
R 128,2,15872
M 72,v[168:171],184
L 15
ds_read2_b32 v[100:101],v14 offset0:224 offset1:225
M 76,v[152:155],204
ds_read2_b32 v[102:103],v14 offset0:226 offset1:227
M 76,v[156:159],200
R 132,2,15360
M 76,v[160:163],196
M 76,v[164:167],192
ds_read2_b32 v[104:105],v15 offset0:96 offset1:97
M 76,v[168:171],188
ds_read2_b32 v[106:107],v15 offset0:98 offset1:99
M 76,v[172:175],184
L 15
R 136,2,14848
M 80,v[152:155],208
ds_read2_b32 v[108:109],v15 offset0:224 offset1:225
M 80,v[156:159],204
M 80,v[160:163],200
ds_read2_b32 v[110:111],v15 offset0:226 offset1:227
M 80,v[164:167],196
R 140,2,14336
M 80,v[168:171],192
ds_read2_b32 v[112:113],v16 offset0:96 offset1:97
M 80,v[172:175],188
ds_read2_b32 v[114:115],v16 offset0:98 offset1:99
M 80,v[176:179],184
L 15
M 84,v[152:155],212
R 144,2,13824
M 84,v[156:159],208
ds_read2_b32 v[116:117],v16 offset0:224 offset1:225
M 84,v[160:163],204
ds_read2_b32 v[118:119],v16 offset0:226 offset1:227
M 84,v[164:167],200
R 148,2,13312
M 84,v[168:171],196
M 84,v[172:175],192
M 84,v[176:179],188
M 84,v[180:183],184
L 15
M 64,v[88:91],128
ds_read2_b32 v[152:153],v1 offset0:112 offset1:113
M 64,v[92:95],124
ds_read2_b32 v[154:155],v1 offset0:114 offset1:115
M 64,v[96:99],120
L 14
R 184,2,16960
M 68,v[88:91],132
ds_read2_b32 v[156:157],v1 offset0:240 offset1:241
M 68,v[92:95],128
M 68,v[96:99],124
ds_read2_b32 v[158:159],v1 offset0:242 offset1:243
M 68,v[100:103],120
L 14
R 188,2,16448
M 72,v[88:91],136
ds_read2_b32 v[160:161],v14 offset0:112 offset1:113
M 72,v[92:95],132
ds_read2_b32 v[162:163],v14 offset0:114 offset1:115
M 72,v[96:99],128
M 72,v[100:103],124
R 192,2,15936
M 72,v[104:107],120
L 15
ds_read2_b32 v[164:165],v14 offset0:240 offset1:241
M 76,v[88:91],140
ds_read2_b32 v[166:167],v14 offset0:242 offset1:243
M 76,v[92:95],136
R 196,2,15424
M 76,v[96:99],132
M 76,v[100:103],128
ds_read2_b32 v[168:169],v15 offset0:112 offset1:113
M 76,v[104:107],124
ds_read2_b32 v[170:171],v15 offset0:114 offset1:115
M 76,v[108:111],120
L 15
R 200,2,14912
M 80,v[88:91],144
ds_read2_b32 v[172:173],v15 offset0:240 offset1:241
M 80,v[92:95],140
M 80,v[96:99],136
ds_read2_b32 v[174:175],v15 offset0:242 offset1:243
M 80,v[100:103],132
R 204,2,14400
M 80,v[104:107],128
ds_read2_b32 v[176:177],v16 offset0:112 offset1:113
M 80,v[108:111],124
ds_read2_b32 v[178:179],v16 offset0:114 offset1:115
M 80,v[112:115],120
L 15
M 84,v[88:91],148
R 208,2,13888
M 84,v[92:95],144
ds_read2_b32 v[180:181],v16 offset0:240 offset1:241
M 84,v[96:99],140
ds_read2_b32 v[182:183],v16 offset0:242 offset1:243
M 84,v[100:103],136
R 212,2,13376
M 84,v[104:107],132
M 84,v[108:111],128
M 84,v[112:115],124
M 84,v[116:119],120
L 15
M 64,v[152:155],192
M 64,v[156:159],188
M 64,v[160:163],184
L 12
M 68,v[152:155],196
M 68,v[156:159],192
M 68,v[160:163],188
M 68,v[164:167],184
L 9
M 72,v[152:155],200
M 72,v[156:159],196
M 72,v[160:163],192
ds_read_b64 v[24:25],v3 offset:15488
ds_read_b64 v[26:27],v3 offset:14976
M 72,v[164:167],188
ds_read_b64 v[28:29],v3 offset:14464
ds_read_b64 v[30:31],v3 offset:13952
M 72,v[168:171],184
L 10
ds_read_b64 v[32:33],v3 offset:13440
ds_read_b64 v[34:35],v3 offset:12928
M 76,v[152:155],204
M 76,v[156:159],200
M 76,v[160:163],196
M 76,v[164:167],192
M 76,v[168:171],188
M 76,v[172:175],184
L 9
M 80,v[152:155],208
M 80,v[156:159],204
M 80,v[160:163],200
M 80,v[164:167],196
M 80,v[168:171],192
M 80,v[172:175],188
M 80,v[176:179],184
L 6
M 84,v[152:155],212
M 84,v[156:159],208
M 84,v[160:163],204
M 84,v[164:167],200
M 84,v[168:171],196
M 84,v[172:175],192
M 84,v[176:179],188
M 84,v[180:183],184
L 0
v_and_b32 v36,0xffff0000,v25
v_lshlrev_b32 v37,16,v25
v_and_b32 v38,0xffff0000,v24
v_lshlrev_b32 v39,16,v24
v_fma_f32 v64,v36,s87,v64
v_fma_f32 v65,v37,s87,v65
v_fma_f32 v66,v38,s87,v66
v_fma_f32 v67,v39,s87,v67
v_cvt_pk_bf16_f32 v64,v64,v65
v_cvt_pk_bf16_f32 v65,v66,v67
ds_write_b64 v4,v[64:65] offset:12928
v_and_b32 v36,0xffff0000,v27
v_lshlrev_b32 v37,16,v27
v_and_b32 v38,0xffff0000,v26
v_lshlrev_b32 v39,16,v26
v_fma_f32 v68,v36,s87,v68
v_fma_f32 v69,v37,s87,v69
v_fma_f32 v70,v38,s87,v70
v_fma_f32 v71,v39,s87,v71
v_cvt_pk_bf16_f32 v68,v68,v69
v_cvt_pk_bf16_f32 v69,v70,v71
ds_write_b64 v4,v[68:69] offset:13440
v_and_b32 v36,0xffff0000,v29
v_lshlrev_b32 v37,16,v29
v_and_b32 v38,0xffff0000,v28
v_lshlrev_b32 v39,16,v28
v_fma_f32 v72,v36,s87,v72
v_fma_f32 v73,v37,s87,v73
v_fma_f32 v74,v38,s87,v74
v_fma_f32 v75,v39,s87,v75
v_cvt_pk_bf16_f32 v72,v72,v73
v_cvt_pk_bf16_f32 v73,v74,v75
ds_write_b64 v4,v[72:73] offset:13952
v_and_b32 v36,0xffff0000,v31
v_lshlrev_b32 v37,16,v31
v_and_b32 v38,0xffff0000,v30
v_lshlrev_b32 v39,16,v30
v_fma_f32 v76,v36,s87,v76
v_fma_f32 v77,v37,s87,v77
v_fma_f32 v78,v38,s87,v78
v_fma_f32 v79,v39,s87,v79
v_cvt_pk_bf16_f32 v76,v76,v77
v_cvt_pk_bf16_f32 v77,v78,v79
ds_write_b64 v4,v[76:77] offset:14464
v_and_b32 v36,0xffff0000,v33
v_lshlrev_b32 v37,16,v33
v_and_b32 v38,0xffff0000,v32
v_lshlrev_b32 v39,16,v32
v_fma_f32 v80,v36,s87,v80
v_fma_f32 v81,v37,s87,v81
v_fma_f32 v82,v38,s87,v82
v_fma_f32 v83,v39,s87,v83
v_cvt_pk_bf16_f32 v80,v80,v81
v_cvt_pk_bf16_f32 v81,v82,v83
ds_write_b64 v4,v[80:81] offset:14976
v_and_b32 v36,0xffff0000,v35
v_lshlrev_b32 v37,16,v35
v_and_b32 v38,0xffff0000,v34
v_lshlrev_b32 v39,16,v34
v_fma_f32 v84,v36,s87,v84
v_fma_f32 v85,v37,s87,v85
v_fma_f32 v86,v38,s87,v86
v_fma_f32 v87,v39,s87,v87
v_cvt_pk_bf16_f32 v84,v84,v85
v_cvt_pk_bf16_f32 v85,v86,v87
ds_write_b64 v4,v[84:85] offset:15488
Lcv_e_%=:
L 0
s_barrier
R 152,5,0
R 156,5,512
R 160,5,1024
R 164,5,1536
R 168,5,2048
R 172,5,2560
s_mov_b32 s78,99614720
L 5
buffer_store_dwordx4 v[152:155],v7,s[36:39],s78 offen offset:0 sc0 sc1
s_add_u32 s78,s56,100139008
L 4
buffer_store_dwordx4 v[156:159],v7,s[36:39],s78 offen offset:0 sc0 sc1
s_mov_b32 s78,100663296
L 3
buffer_store_dwordx4 v[160:163],v7,s[36:39],s78 offen offset:0 sc0 sc1
s_add_u32 s78,s56,101187584
L 2
buffer_store_dwordx4 v[164:167],v7,s[36:39],s78 offen offset:0 sc0 sc1
s_mov_b32 s78,101711872
L 1
buffer_store_dwordx4 v[168:171],v7,s[36:39],s78 offen offset:0 sc0 sc1
s_add_u32 s78,s56,102236160
L 0
buffer_store_dwordx4 v[172:175],v7,s[36:39],s78 offen offset:0 sc0 sc1
s_waitcnt vmcnt(0)
s_barrier
s_add_u32 s48,s48,32768
buffer_store_dword v23,v10,s[36:39],s48 offen offset:0 sc0 sc1
s_cmp_eq_u32 s51,2
s_cbranch_scc0 Lcv_f_%=
s_mov_b32 m0,163584
s_mov_b32 s78,3694592
buffer_load_dword v12,s[60:63],s78 offen offset:0  lds
s_mov_b32 m0,163584
s_mov_b32 s78,5541888
buffer_load_dword v12,s[60:63],s78 offen offset:0  lds
Lcv_f_%=:
Lcode_cv_e:
.purgem M
.purgem R
.purgem T
.purgem D
.purgem L
)"
  :: "s"(ws), "s"(sWave), "s"(sBuf), "s"(sAuxOff), "s"(sFoutOff), "s"(sC0), "s"(sC1), "s"(sExp), "v"(vL16), "v"(vPA), "v"(vPB), "v"(vPU), "v"(vPW), "v"(vOR), "v"(vORA), "v"(vOG), "v"(vFA), "v"(vFB), "v"(vFlag), "v"(vTW), "v"(vTX), "s"(w3), "s"(w4) : "v0", "v1", "v2", "v3", "v4", "v5", "v6", "v7", "v8", "v9", "v10", "v11", "v12", "v13", "v14", "v15", "v16", "v17", "v18", "v19", "v20", "v21", "v22", "v23", "v24", "v25", "v26", "v27", "v28", "v29", "v30", "v31", "v32", "v33", "v34", "v35", "v36", "v37", "v38", "v39", "v40", "v41", "v42", "v43", "v44", "v45", "v46", "v47", "v48", "v49", "v50", "v51", "v52", "v53", "v54", "v55", "v56", "v57", "v58", "v59", "v60", "v61", "v62", "v63", "v64", "v65", "v66", "v67", "v68", "v69", "v70", "v71", "v72", "v73", "v74", "v75", "v76", "v77", "v78", "v79", "v80", "v81", "v82", "v83", "v84", "v85", "v86", "v87", "v88", "v89", "v90", "v91", "v92", "v93", "v94", "v95", "v96", "v97", "v98", "v99", "v100", "v101", "v102", "v103", "v104", "v105", "v106", "v107", "v108", "v109", "v110", "v111", "v112", "v113", "v114", "v115", "v116", "v117", "v118", "v119", "v120", "v121", "v122", "v123", "v124", "v125", "v126", "v127", "v128", "v129", "v130", "v131", "v132", "v133", "v134", "v135", "v136", "v137", "v138", "v139", "v140", "v141", "v142", "v143", "v144", "v145", "v146", "v147", "v148", "v149", "v150", "v151", "v152", "v153", "v154", "v155", "v156", "v157", "v158", "v159", "v160", "v161", "v162", "v163", "v164", "v165", "v166", "v167", "v168", "v169", "v170", "v171", "v172", "v173", "v174", "v175", "v176", "v177", "v178", "v179", "v180", "v181", "v182", "v183", "v184", "v185", "v186", "v187", "v188", "v189", "v190", "v191", "v192", "v193", "v194", "v195", "v196", "v197", "v198", "v199", "v200", "v201", "v202", "v203", "v204", "v205", "v206", "v207", "v208", "v209", "v210", "v211", "v212", "v213", "v214", "v215", "v216", "v217", "v218", "v219", "v220", "v221", "v222", "v223", "v224", "v225", "v226", "v227", "v228", "v229", "v230", "v231", "a0", "a1", "a2", "a3", "a4", "a5", "a6", "a7", "a8", "a9", "a10", "a11", "a12", "a13", "a14", "a15", "a16", "a17", "a18", "a19", "a20", "a21", "a22", "a23", "a24", "a25", "a26", "a27", "a28", "a29", "a30", "a31", "a32", "a33", "a34", "a35", "a36", "a37", "a38", "a39", "a40", "a41", "a42", "a43", "a44", "a45", "a46", "a47", "a48", "a49", "a50", "a51", "a52", "a53", "a54", "a55", "a56", "a57", "a58", "a59", "a60", "a61", "a62", "a63", "a64", "a65", "a66", "a67", "a68", "a69", "a70", "a71", "a72", "a73", "a74", "a75", "a76", "a77", "a78", "a79", "a80", "a81", "a82", "a83", "a84", "a85", "a86", "a87", "a88", "a89", "a90", "a91", "a92", "a93", "a94", "a95", "a96", "a97", "a98", "a99", "a100", "a101", "a102", "a103", "a104", "a105", "a106", "a107", "a108", "a109", "a110", "a111", "a112", "a113", "a114", "a115", "a116", "a117", "a118", "a119", "a120", "a121", "a122", "a123", "a124", "a125", "a126", "a127", "a128", "a129", "a130", "a131", "a132", "a133", "a134", "a135", "a136", "a137", "a138", "a139", "a140", "a141", "a142", "a143", "a144", "a145", "a146", "a147", "a148", "a149", "a150", "a151", "a152", "a153", "a154", "a155", "a156", "a157", "a158", "a159", "a160", "a161", "a162", "a163", "a164", "a165", "a166", "a167", "a168", "a169", "a170", "a171", "a172", "a173", "a174", "a175", "a176", "a177", "a178", "a179", "a180", "a181", "a182", "a183", "a184", "a185", "a186", "a187", "a188", "a189", "a190", "a191", "a192", "a193", "a194", "a195", "a196", "a197", "a198", "a199", "a200", "a201", "a202", "a203", "a204", "a205", "a206", "a207", "a208", "a209", "a210", "a211", "a212", "a213", "a214", "a215", "a216", "a217", "a218", "a219", "a220", "a221", "a222", "a223", "a224", "a225", "a226", "a227", "a228", "a229", "a230", "a231", "a232", "a233", "a234", "a235", "a236", "a237", "a238", "a239", "a240", "a241", "a242", "a243", "a244", "a245", "a246", "a247", "a248", "a249", "a250", "a251", "a252", "a253", "a254", "a255", "s36", "s37", "s38", "s39", "s40", "s41", "s42", "s43", "s44", "s45", "s46", "s47", "s48", "s49", "s50", "s51", "s52", "s53", "s54", "s55", "s56", "s57", "s58", "s59", "s60", "s61", "s62", "s63", "s64", "s65", "s66", "s67", "s68", "s69", "s70", "s71", "s72", "s73", "s74", "s75", "s76", "s77", "s78", "s79", "s80", "s81", "s82", "s83", "s84", "s85", "s86", "s87", "s88", "s89", "s90", "s91", "s92", "s93", "s94", "s95", "s96", "s97", "vcc", "scc", "memory");
    LANE_ID(lq_);
    const int g = QT0 + (NCV - 1 - iw) * 164 + min(wave * 64 + lq_, 163);
    if (QTB) touch_lds((const char*)q + (size_t)(g < 32768 ? g : 0) * 128, sScr);
  }
  WAIT_LGKM0();
  __builtin_amdgcn_s_barrier();
  CTS(10);
  }
  const unsigned sDbg0 = 110u * 1048576u + (b * 4 + wave) * 8192;
  constexpr unsigned OOB = 0x80000000u;
#define HLP 0
  if (kind == 6 || (kind == 4 && j4 < HLP)) {
    LANE_ID(lr);
    const int r8 = lr & 7, seg = lr >> 3, j = lr & 15, g = lr >> 4;
    const unsigned sWave = wave, sExp = __builtin_amdgcn_readfirstlane(ep), sG0 = kind == 6 ? slice : 64 + j4, sStep = NRW, sOutOff = kind == 6 ? 256 : 65 + j4, sAuxOff = HLP, sBuf = kind & 2, sRk = slice;
    const unsigned sDbg = sDbg0 + (kind == 6 ? 0 : 1024);
    const unsigned vRF = (wave * 8 + (seg >> 2)) * 1024 + ((seg & 3) * 16 + r8) * 16;
    const unsigned vRwr = seg == 0 ? r8 * 64 + wave * 4 : 512 + lr * 4, vRrd = r8 * 64;
    const unsigned vRP = g == (j >> 2) ? r8 * 64 + 16 + wave * 8 + (seg & 1) * 4 : 768 + lr * 4;
    const unsigned vRS = (wave == 0 && seg < 2) ? r8 * 16 + seg * 256 : OOB, vFlag = lr < 32 ? lr * 4 : OOB;
    const unsigned vL4 = ((lr & 2) * 4 + wave * 2 + (lr & 1)) * 128, vLY = min((wave < 3 ? wave * 32 : 112) + lr, wave < 2 ? wave * 32 + 31 : wave == 2 ? 111 : NCV - 1) * 128;
    const unsigned vQ = r8 * 2048 + wave * 512 + seg * 16;
    const unsigned vY0 = wave * 4096 + lr * 16;
    const unsigned vTr = 4096 + wave * 4096 + ((2 * g + ((j & 3) >> 1)) * 8 + (j >> 2)) * 16 + (j & 1) * 8;
    const unsigned one = g == (j >> 2) ? 0x3f80u << (16 * (j & 1)) : 0u, vSa = (j & 2) ? 0u : one, vSb = (j & 2) ? one : 0u;
    (void)sDbg;
    asm volatile(
R"(.macro M d,w,x
v_mfma_f32_16x16x32_bf16 v[\d:\d+3], \w, v[\x:\x+3], v[\d:\d+3]
.endm
.macro R d,a,o
ds_read_b128 v[\d:\d+3], v\a offset:\o
.endm
.macro T d,a,o
ds_read_b64_tr_b16 \d, v\a offset:\o
.endm
.macro D a,s,o
buffer_load_dwordx4 v\a, s[36:39], s\s offen offset:\o sc1 lds
.endm
.macro L c
s_waitcnt lgkmcnt(\c)
.endm
Lcode_r_b:
s_mov_b64 s[36:37],%0
s_mov_b32 s38,111243392
s_mov_b32 s39,0x00020000
s_mov_b32 s44,%1
s_mov_b32 s45,%2
s_mov_b32 s46,%3
s_mov_b32 s47,%4
s_mov_b32 s49,%5
s_mov_b32 s51,%6
s_mov_b32 s93,%7
s_mov_b32 s53,%8
v_mov_b32 v0,%9
v_mov_b32 v1,%10
v_mov_b32 v2,%11
v_mov_b32 v3,%12
v_mov_b32 v4,%13
v_mov_b32 v5,%14
v_mov_b32 v6,%15
v_mov_b32 v7,%16
v_mov_b32 v8,%17
v_mov_b32 v9,%18
v_mov_b32 v10,%19
v_mov_b32 v11,%20
v_mov_b32 v12,%21
s_mov_b64 s[84:85],%22
s_mov_b32 s86,4194304
s_mov_b32 s87,0x00020000
s_lshl_b32 s55,s45,14
buffer_load_dwordx4 v[164:167],v8,s[84:87],s55 offen offset:0 nt
buffer_load_dwordx4 v[168:171],v8,s[84:87],s55 offen offset:128 nt
buffer_load_dwordx4 v[172:175],v8,s[84:87],s55 offen offset:256 nt
buffer_load_dwordx4 v[176:179],v8,s[84:87],s55 offen offset:384 nt
v_mov_b32 v13,s53
v_mbcnt_lo_u32_b32 v15,-1,0
v_mbcnt_hi_u32_b32 v15,-1,v15
v_lshlrev_b32 v15,4,v15
s_mov_b32 s82,0
s_mov_b32 s63,0x7fff0000
s_mov_b32 s64,0
s_mov_b32 s67,0
s_lshl_b32 s65,s44,12
s_add_u32 s65,s65,4096
s_lshl_b32 s66,s44,7
s_add_u32 s66,s66,109056000
s_mov_b32 s70,0xaaaaaaaa
s_mov_b32 s71,0xaaaaaaaa
s_mov_b32 s72,0xcccccccc
s_mov_b32 s73,0xcccccccc
s_mov_b32 s74,0xff00ff00
s_mov_b32 s75,0xff00ff00
s_mov_b32 s68,0x3a800000
s_mov_b32 s69,0x3727c5ac
s_mov_b32 s76,0xc0135761
s_mov_b32 s77,0x3d372713
v_mov_b32 v32,0x3f803f80
v_mov_b32 v33,0x3f803f80
v_mov_b32 v34,0x3f803f80
v_mov_b32 v35,0x3f803f80
v_mov_b32 v24,0
v_mov_b32 v25,0
v_mov_b32 v26,v11
v_mov_b32 v27,v12
v_mov_b32 v28,0
v_mov_b32 v29,0
s_waitcnt vmcnt(0)
Lr_a0_%=:
s_waitcnt vmcnt(10)
v_mfma_f32_16x16x32_bf16 v[36:39],v[32:35],v[164:167],0
s_add_u32 s79,s45,s46
s_cmp_lt_u32 s45,64
s_cselect_b32 s78,s49,0
v_mfma_f32_16x16x32_bf16 v[40:43],v[164:167],v[164:167],0
s_cmp_ge_u32 s79,64
s_cselect_b32 s78,s78,0
s_add_u32 s62,s79,s78
M 36,v[32:35],168
s_lshr_b32 s78,s45,1
s_and_b32 s79,s45,1
s_lshl_b32 s79,s79,7
M 40,v[168:171],168
s_lshl_b32 s57,s78,15
s_lshl_b32 s55,s62,14
s_cmp_lt_u32 s62,s47
M 36,v[32:35],172
s_cselect_b32 s55,s55,0x7fff0000
buffer_load_dwordx4 v[196:199],v8,s[84:87],s55 offen offset:0 nt
M 40,v[172:175],172
s_add_u32 s57,s57,s79
buffer_load_dwordx4 v[200:203],v8,s[84:87],s55 offen offset:128 nt
M 36,v[32:35],176
s_lshl_b32 s59,s78,9
buffer_load_dwordx4 v[204:207],v8,s[84:87],s55 offen offset:256 nt
M 40,v[176:179],176
s_add_u32 s57,s57,8388608
buffer_load_dwordx4 v[208:211],v8,s[84:87],s55 offen offset:384 nt
v_mfma_f32_16x16x32_bf16 v[116:119],v[26:29],v[164:167],0
s_add_u32 s59,s59,s79
s_lshl_b32 s60,s45,9
s_add_u32 s58,s57,4096
v_mfma_f32_16x16x32_bf16 v[120:123],v[24:27],v[164:167],0
s_add_u32 s59,s59,106954752
s_add_u32 s60,s60,s66
v_mfma_f32_16x16x32_bf16 v[124:127],v[26:29],v[168:171],0
v_mfma_f32_16x16x32_bf16 v[128:131],v[24:27],v[168:171],0
v_add_f32_dpp v44,v36,v36 row_ror:8 row_mask:0xf bank_mask:0xf
v_cndmask_b32 v45,v40,v41,s[70:71]
v_mfma_f32_16x16x32_bf16 v[132:135],v[26:29],v[172:175],0
ds_write_b32 v2,v44 offset:0
v_mfma_f32_16x16x32_bf16 v[136:139],v[24:27],v[172:175],0
v_cndmask_b32 v46,v42,v43,s[70:71]
v_cndmask_b32 v45,v45,v46,s[72:73]
v_mfma_f32_16x16x32_bf16 v[140:143],v[26:29],v[176:179],0
ds_write_b32 v0,v45 offset:0
v_mfma_f32_16x16x32_bf16 v[144:147],v[24:27],v[176:179],0
L 0
s_barrier
R 52,3,0
R 56,3,16
R 60,3,32
s_cmp_lg_u32 s67,0
s_cbranch_scc1 Lr_b0_%=
s_waitcnt vmcnt(4)
buffer_store_dword v13,v5,s[36:39],s63 offen offset:0 sc0 sc1
s_mov_b32 s63,0x7fff0000
s_cmp_lt_u32 s45,64
s_cselect_b32 s64,-192,0
s_add_u32 s64,s64,256
s_cmp_lt_u32 s45,64
s_cselect_b32 s61,0,32768
s_add_u32 s61,s61,111177728
s_mul_i32 s56,s45,16384
s_add_u32 s56,s56,98566144
Lr_bpy0_%=:
buffer_load_dword v14,v7,s[36:39],s61 offen offset:0 sc1
s_add_u32 s82,s82,1
s_waitcnt vmcnt(0)
v_cmp_eq_u32 vcc,s53,v14
s_nop 0
s_cmp_eq_u64 vcc,-1
s_cbranch_scc1 Lr_bqy0_%=
s_sleep 2
s_cmp_lt_u32 s82,20000
s_cbranch_scc1 Lr_bpy0_%=
Lr_bqy0_%=:
s_add_u32 m0,s65,0
s_nop 0
D 9,56,0
D 9,56,1024
D 9,56,2048
D 9,56,3072
s_waitcnt vmcnt(0)
Lr_b0_%=:
s_waitcnt vmcnt(10)
T v[68:69],10,0
T v[70:71],10,64
T v[72:73],10,1024
T v[74:75],10,1088
T v[76:77],10,2048
T v[78:79],10,2112
T v[80:81],10,3072
T v[82:83],10,3136
s_mov_b32 s45,s62
s_min_u32 s78,s64,s47
s_mul_i32 s56,s45,16384
s_add_u32 s56,s56,98566144
s_cmp_lt_u32 s45,s78
s_cselect_b32 s67,1,0
s_cselect_b32 s56,s56,0x7fff0000
s_add_u32 m0,s65,16384
s_nop 0
D 9,56,0
D 9,56,1024
D 9,56,2048
D 9,56,3072
L 8
v_pk_add_f32 v[52:53],v[52:53],v[54:55]
v_pk_add_f32 v[56:57],v[56:57],v[58:59]
v_pk_add_f32 v[60:61],v[60:61],v[62:63]
v_add_f32 v52,v52,v53
v_pk_add_f32 v[56:57],v[56:57],v[60:61]
v_mul_f32 v52,s68,v52
v_add_f32 v53,v56,v57
v_mul_f32 v53,s68,v53
v_fma_f32 v53,-v52,v52,v53
v_max_f32 v53,0,v53
L 6
v_lshlrev_b32 v84,16,v68
v_and_b32 v85,0xffff0000,v68
v_lshlrev_b32 v86,16,v69
v_and_b32 v87,0xffff0000,v69
v_lshlrev_b32 v88,16,v70
v_and_b32 v89,0xffff0000,v70
v_lshlrev_b32 v90,16,v71
v_and_b32 v91,0xffff0000,v71
L 4
v_lshlrev_b32 v92,16,v72
v_and_b32 v93,0xffff0000,v72
v_lshlrev_b32 v94,16,v73
v_and_b32 v95,0xffff0000,v73
v_lshlrev_b32 v96,16,v74
v_and_b32 v97,0xffff0000,v74
v_lshlrev_b32 v98,16,v75
v_and_b32 v99,0xffff0000,v75
L 2
v_lshlrev_b32 v100,16,v76
v_and_b32 v101,0xffff0000,v76
v_lshlrev_b32 v102,16,v77
v_and_b32 v103,0xffff0000,v77
v_lshlrev_b32 v104,16,v78
v_and_b32 v105,0xffff0000,v78
v_lshlrev_b32 v106,16,v79
v_and_b32 v107,0xffff0000,v79
L 0
v_lshlrev_b32 v108,16,v80
v_and_b32 v109,0xffff0000,v80
v_lshlrev_b32 v110,16,v81
v_and_b32 v111,0xffff0000,v81
v_lshlrev_b32 v112,16,v82
v_and_b32 v113,0xffff0000,v82
v_lshlrev_b32 v114,16,v83
v_and_b32 v115,0xffff0000,v83
v_add_f32 v53,s69,v53
v_rsq_f32 v48,v53
s_nop 0
v_mul_f32_e64 v50,-v52,v48
v_mov_b32 v49,v48
v_mov_b32 v51,v50
v_pk_fma_f32 v[52:53],v[116:117],v[48:49],v[50:51]
v_pk_fma_f32 v[54:55],v[118:119],v[48:49],v[50:51]
v_pk_fma_f32 v[56:57],v[120:121],v[48:49],v[50:51]
v_pk_fma_f32 v[58:59],v[122:123],v[48:49],v[50:51]
v_pk_fma_f32 v[116:117],v[52:53],v[84:85],v[116:117]
v_pk_fma_f32 v[118:119],v[54:55],v[86:87],v[118:119]
v_pk_fma_f32 v[120:121],v[56:57],v[88:89],v[120:121]
v_pk_fma_f32 v[122:123],v[58:59],v[90:91],v[122:123]
v_cvt_pk_bf16_f32 v148,v116,v117
v_cvt_pk_bf16_f32 v149,v118,v119
v_cvt_pk_bf16_f32 v150,v120,v121
v_cvt_pk_bf16_f32 v151,v122,v123
v_pk_fma_f32 v[52:53],v[124:125],v[48:49],v[50:51]
v_pk_fma_f32 v[54:55],v[126:127],v[48:49],v[50:51]
v_pk_fma_f32 v[56:57],v[128:129],v[48:49],v[50:51]
v_pk_fma_f32 v[58:59],v[130:131],v[48:49],v[50:51]
v_pk_fma_f32 v[124:125],v[52:53],v[92:93],v[124:125]
v_pk_fma_f32 v[126:127],v[54:55],v[94:95],v[126:127]
v_pk_fma_f32 v[128:129],v[56:57],v[96:97],v[128:129]
v_pk_fma_f32 v[130:131],v[58:59],v[98:99],v[130:131]
v_cvt_pk_bf16_f32 v152,v124,v125
v_cvt_pk_bf16_f32 v153,v126,v127
v_cvt_pk_bf16_f32 v154,v128,v129
v_cvt_pk_bf16_f32 v155,v130,v131
v_pk_fma_f32 v[52:53],v[132:133],v[48:49],v[50:51]
v_pk_fma_f32 v[54:55],v[134:135],v[48:49],v[50:51]
v_pk_fma_f32 v[56:57],v[136:137],v[48:49],v[50:51]
v_pk_fma_f32 v[58:59],v[138:139],v[48:49],v[50:51]
v_pk_fma_f32 v[132:133],v[52:53],v[100:101],v[132:133]
v_pk_fma_f32 v[134:135],v[54:55],v[102:103],v[134:135]
v_pk_fma_f32 v[136:137],v[56:57],v[104:105],v[136:137]
v_pk_fma_f32 v[138:139],v[58:59],v[106:107],v[138:139]
v_cvt_pk_bf16_f32 v156,v132,v133
v_cvt_pk_bf16_f32 v157,v134,v135
v_cvt_pk_bf16_f32 v158,v136,v137
v_cvt_pk_bf16_f32 v159,v138,v139
v_pk_fma_f32 v[52:53],v[140:141],v[48:49],v[50:51]
v_pk_fma_f32 v[54:55],v[142:143],v[48:49],v[50:51]
v_pk_fma_f32 v[56:57],v[144:145],v[48:49],v[50:51]
v_pk_fma_f32 v[58:59],v[146:147],v[48:49],v[50:51]
v_pk_fma_f32 v[140:141],v[52:53],v[108:109],v[140:141]
v_pk_fma_f32 v[142:143],v[54:55],v[110:111],v[142:143]
v_pk_fma_f32 v[144:145],v[56:57],v[112:113],v[144:145]
v_pk_fma_f32 v[146:147],v[58:59],v[114:115],v[146:147]
v_cvt_pk_bf16_f32 v160,v140,v141
v_cvt_pk_bf16_f32 v161,v142,v143
v_cvt_pk_bf16_f32 v162,v144,v145
v_cvt_pk_bf16_f32 v163,v146,v147
s_waitcnt vmcnt(8)
buffer_store_dword v13,v5,s[36:39],s63 offen offset:0 sc0 sc1
s_mov_b32 s63,0x7fff0000
v_mfma_f32_16x16x32_bf16 v[36:39],v[32:35],v[148:151],0
buffer_store_dwordx4 v[148:151],v1,s[36:39],s57 offen offset:0 sc0 sc1
v_mfma_f32_16x16x32_bf16 v[40:43],v[148:151],v[148:151],0
M 36,v[32:35],152
buffer_store_dwordx4 v[152:155],v1,s[36:39],s57 offen offset:2048 sc0 sc1
M 40,v[152:155],152
M 36,v[32:35],156
buffer_store_dwordx4 v[156:159],v1,s[36:39],s58 offen offset:0 sc0 sc1
M 40,v[156:159],156
M 36,v[32:35],160
buffer_store_dwordx4 v[160:163],v1,s[36:39],s58 offen offset:2048 sc0 sc1
M 40,v[160:163],160
s_nop 5
v_add_f32_dpp v44,v36,v36 row_ror:8 row_mask:0xf bank_mask:0xf
s_nop 0
v_cndmask_b32 v45,v40,v41,s[70:71]
v_cndmask_b32 v46,v42,v43,s[70:71]
ds_write_b32 v2,v44 offset:1024
v_cndmask_b32 v45,v45,v46,s[72:73]
ds_write_b32 v0,v45 offset:1024
L 0
s_barrier
R 52,3,1024
R 56,3,1040
R 60,3,1056
L 0
v_pk_add_f32 v[52:53],v[52:53],v[54:55]
v_pk_add_f32 v[56:57],v[56:57],v[58:59]
v_pk_add_f32 v[60:61],v[60:61],v[62:63]
v_add_f32 v52,v52,v53
v_pk_add_f32 v[56:57],v[56:57],v[60:61]
v_mul_f32 v52,s68,v52
v_add_f32 v53,v56,v57
v_mul_f32 v53,s68,v53
v_fma_f32 v53,-v52,v52,v53
v_max_f32 v53,0,v53
v_add_f32 v54,s69,v53
v_fma_f32 v55,s69,v54,v53
v_rsq_f32 v65,v54
v_rsq_f32 v54,v55
v_mul_f32_e64 v64,-v52,v65
v_mul_f32 v63,v55,v54
v_mul_f32 v61,s76,v54
v_mul_f32 v55,v54,v54
v_mul_f32 v62,s77,v55
v_mul_f32 v62,v62,v61
v_cndmask_b32 v60,v52,v64,s[74:75]
v_cndmask_b32 v61,v61,v65,s[74:75]
buffer_store_dwordx4 v[60:63],v4,s[36:39],s59 offen offset:0 sc0 sc1
s_mov_b32 s63,s60
s_cmp_lt_u32 s45,s47
s_cbranch_scc0 Lr_mid_%=
Lr_a1_%=:
s_waitcnt vmcnt(10)
v_mfma_f32_16x16x32_bf16 v[36:39],v[32:35],v[196:199],0
s_add_u32 s79,s45,s46
s_cmp_lt_u32 s45,64
s_cselect_b32 s78,s49,0
v_mfma_f32_16x16x32_bf16 v[40:43],v[196:199],v[196:199],0
s_cmp_ge_u32 s79,64
s_cselect_b32 s78,s78,0
s_add_u32 s62,s79,s78
M 36,v[32:35],200
s_lshr_b32 s78,s45,1
s_and_b32 s79,s45,1
s_lshl_b32 s79,s79,7
M 40,v[200:203],200
s_lshl_b32 s57,s78,15
s_lshl_b32 s55,s62,14
s_cmp_lt_u32 s62,s47
M 36,v[32:35],204
s_cselect_b32 s55,s55,0x7fff0000
buffer_load_dwordx4 v[164:167],v8,s[84:87],s55 offen offset:0 nt
M 40,v[204:207],204
s_add_u32 s57,s57,s79
buffer_load_dwordx4 v[168:171],v8,s[84:87],s55 offen offset:128 nt
M 36,v[32:35],208
s_lshl_b32 s59,s78,9
buffer_load_dwordx4 v[172:175],v8,s[84:87],s55 offen offset:256 nt
M 40,v[208:211],208
s_add_u32 s57,s57,8388608
buffer_load_dwordx4 v[176:179],v8,s[84:87],s55 offen offset:384 nt
v_mfma_f32_16x16x32_bf16 v[116:119],v[26:29],v[196:199],0
s_add_u32 s59,s59,s79
s_lshl_b32 s60,s45,9
s_add_u32 s58,s57,4096
v_mfma_f32_16x16x32_bf16 v[120:123],v[24:27],v[196:199],0
s_add_u32 s59,s59,106954752
s_add_u32 s60,s60,s66
v_mfma_f32_16x16x32_bf16 v[124:127],v[26:29],v[200:203],0
v_mfma_f32_16x16x32_bf16 v[128:131],v[24:27],v[200:203],0
v_add_f32_dpp v44,v36,v36 row_ror:8 row_mask:0xf bank_mask:0xf
v_cndmask_b32 v45,v40,v41,s[70:71]
v_mfma_f32_16x16x32_bf16 v[132:135],v[26:29],v[204:207],0
ds_write_b32 v2,v44 offset:0
v_mfma_f32_16x16x32_bf16 v[136:139],v[24:27],v[204:207],0
v_cndmask_b32 v46,v42,v43,s[70:71]
v_cndmask_b32 v45,v45,v46,s[72:73]
v_mfma_f32_16x16x32_bf16 v[140:143],v[26:29],v[208:211],0
ds_write_b32 v0,v45 offset:0
v_mfma_f32_16x16x32_bf16 v[144:147],v[24:27],v[208:211],0
L 0
s_barrier
R 52,3,0
R 56,3,16
R 60,3,32
s_cmp_lg_u32 s67,0
s_cbranch_scc1 Lr_b1_%=
s_waitcnt vmcnt(4)
buffer_store_dword v13,v5,s[36:39],s63 offen offset:0 sc0 sc1
s_mov_b32 s63,0x7fff0000
s_cmp_lt_u32 s45,64
s_cselect_b32 s64,-192,0
s_add_u32 s64,s64,256
s_cmp_lt_u32 s45,64
s_cselect_b32 s61,0,32768
s_add_u32 s61,s61,111177728
s_mul_i32 s56,s45,16384
s_add_u32 s56,s56,98566144
Lr_bpy1_%=:
buffer_load_dword v14,v7,s[36:39],s61 offen offset:0 sc1
s_add_u32 s82,s82,1
s_waitcnt vmcnt(0)
v_cmp_eq_u32 vcc,s53,v14
s_nop 0
s_cmp_eq_u64 vcc,-1
s_cbranch_scc1 Lr_bqy1_%=
s_sleep 2
s_cmp_lt_u32 s82,20000
s_cbranch_scc1 Lr_bpy1_%=
Lr_bqy1_%=:
s_add_u32 m0,s65,16384
s_nop 0
D 9,56,0
D 9,56,1024
D 9,56,2048
D 9,56,3072
s_waitcnt vmcnt(0)
Lr_b1_%=:
s_waitcnt vmcnt(10)
T v[68:69],10,16384
T v[70:71],10,16448
T v[72:73],10,17408
T v[74:75],10,17472
T v[76:77],10,18432
T v[78:79],10,18496
T v[80:81],10,19456
T v[82:83],10,19520
s_mov_b32 s45,s62
s_min_u32 s78,s64,s47
s_mul_i32 s56,s45,16384
s_add_u32 s56,s56,98566144
s_cmp_lt_u32 s45,s78
s_cselect_b32 s67,1,0
s_cselect_b32 s56,s56,0x7fff0000
s_add_u32 m0,s65,0
s_nop 0
D 9,56,0
D 9,56,1024
D 9,56,2048
D 9,56,3072
L 8
v_pk_add_f32 v[52:53],v[52:53],v[54:55]
v_pk_add_f32 v[56:57],v[56:57],v[58:59]
v_pk_add_f32 v[60:61],v[60:61],v[62:63]
v_add_f32 v52,v52,v53
v_pk_add_f32 v[56:57],v[56:57],v[60:61]
v_mul_f32 v52,s68,v52
v_add_f32 v53,v56,v57
v_mul_f32 v53,s68,v53
v_fma_f32 v53,-v52,v52,v53
v_max_f32 v53,0,v53
L 6
v_lshlrev_b32 v84,16,v68
v_and_b32 v85,0xffff0000,v68
v_lshlrev_b32 v86,16,v69
v_and_b32 v87,0xffff0000,v69
v_lshlrev_b32 v88,16,v70
v_and_b32 v89,0xffff0000,v70
v_lshlrev_b32 v90,16,v71
v_and_b32 v91,0xffff0000,v71
L 4
v_lshlrev_b32 v92,16,v72
v_and_b32 v93,0xffff0000,v72
v_lshlrev_b32 v94,16,v73
v_and_b32 v95,0xffff0000,v73
v_lshlrev_b32 v96,16,v74
v_and_b32 v97,0xffff0000,v74
v_lshlrev_b32 v98,16,v75
v_and_b32 v99,0xffff0000,v75
L 2
v_lshlrev_b32 v100,16,v76
v_and_b32 v101,0xffff0000,v76
v_lshlrev_b32 v102,16,v77
v_and_b32 v103,0xffff0000,v77
v_lshlrev_b32 v104,16,v78
v_and_b32 v105,0xffff0000,v78
v_lshlrev_b32 v106,16,v79
v_and_b32 v107,0xffff0000,v79
L 0
v_lshlrev_b32 v108,16,v80
v_and_b32 v109,0xffff0000,v80
v_lshlrev_b32 v110,16,v81
v_and_b32 v111,0xffff0000,v81
v_lshlrev_b32 v112,16,v82
v_and_b32 v113,0xffff0000,v82
)"
R"(v_lshlrev_b32 v114,16,v83
v_and_b32 v115,0xffff0000,v83
v_add_f32 v53,s69,v53
v_rsq_f32 v48,v53
s_nop 0
v_mul_f32_e64 v50,-v52,v48
v_mov_b32 v49,v48
v_mov_b32 v51,v50
v_pk_fma_f32 v[52:53],v[116:117],v[48:49],v[50:51]
v_pk_fma_f32 v[54:55],v[118:119],v[48:49],v[50:51]
v_pk_fma_f32 v[56:57],v[120:121],v[48:49],v[50:51]
v_pk_fma_f32 v[58:59],v[122:123],v[48:49],v[50:51]
v_pk_fma_f32 v[116:117],v[52:53],v[84:85],v[116:117]
v_pk_fma_f32 v[118:119],v[54:55],v[86:87],v[118:119]
v_pk_fma_f32 v[120:121],v[56:57],v[88:89],v[120:121]
v_pk_fma_f32 v[122:123],v[58:59],v[90:91],v[122:123]
v_cvt_pk_bf16_f32 v148,v116,v117
v_cvt_pk_bf16_f32 v149,v118,v119
v_cvt_pk_bf16_f32 v150,v120,v121
v_cvt_pk_bf16_f32 v151,v122,v123
v_pk_fma_f32 v[52:53],v[124:125],v[48:49],v[50:51]
v_pk_fma_f32 v[54:55],v[126:127],v[48:49],v[50:51]
v_pk_fma_f32 v[56:57],v[128:129],v[48:49],v[50:51]
v_pk_fma_f32 v[58:59],v[130:131],v[48:49],v[50:51]
v_pk_fma_f32 v[124:125],v[52:53],v[92:93],v[124:125]
v_pk_fma_f32 v[126:127],v[54:55],v[94:95],v[126:127]
v_pk_fma_f32 v[128:129],v[56:57],v[96:97],v[128:129]
v_pk_fma_f32 v[130:131],v[58:59],v[98:99],v[130:131]
v_cvt_pk_bf16_f32 v152,v124,v125
v_cvt_pk_bf16_f32 v153,v126,v127
v_cvt_pk_bf16_f32 v154,v128,v129
v_cvt_pk_bf16_f32 v155,v130,v131
v_pk_fma_f32 v[52:53],v[132:133],v[48:49],v[50:51]
v_pk_fma_f32 v[54:55],v[134:135],v[48:49],v[50:51]
v_pk_fma_f32 v[56:57],v[136:137],v[48:49],v[50:51]
v_pk_fma_f32 v[58:59],v[138:139],v[48:49],v[50:51]
v_pk_fma_f32 v[132:133],v[52:53],v[100:101],v[132:133]
v_pk_fma_f32 v[134:135],v[54:55],v[102:103],v[134:135]
v_pk_fma_f32 v[136:137],v[56:57],v[104:105],v[136:137]
v_pk_fma_f32 v[138:139],v[58:59],v[106:107],v[138:139]
v_cvt_pk_bf16_f32 v156,v132,v133
v_cvt_pk_bf16_f32 v157,v134,v135
v_cvt_pk_bf16_f32 v158,v136,v137
v_cvt_pk_bf16_f32 v159,v138,v139
v_pk_fma_f32 v[52:53],v[140:141],v[48:49],v[50:51]
v_pk_fma_f32 v[54:55],v[142:143],v[48:49],v[50:51]
v_pk_fma_f32 v[56:57],v[144:145],v[48:49],v[50:51]
v_pk_fma_f32 v[58:59],v[146:147],v[48:49],v[50:51]
v_pk_fma_f32 v[140:141],v[52:53],v[108:109],v[140:141]
v_pk_fma_f32 v[142:143],v[54:55],v[110:111],v[142:143]
v_pk_fma_f32 v[144:145],v[56:57],v[112:113],v[144:145]
v_pk_fma_f32 v[146:147],v[58:59],v[114:115],v[146:147]
v_cvt_pk_bf16_f32 v160,v140,v141
v_cvt_pk_bf16_f32 v161,v142,v143
v_cvt_pk_bf16_f32 v162,v144,v145
v_cvt_pk_bf16_f32 v163,v146,v147
s_waitcnt vmcnt(8)
buffer_store_dword v13,v5,s[36:39],s63 offen offset:0 sc0 sc1
s_mov_b32 s63,0x7fff0000
v_mfma_f32_16x16x32_bf16 v[36:39],v[32:35],v[148:151],0
buffer_store_dwordx4 v[148:151],v1,s[36:39],s57 offen offset:0 sc0 sc1
v_mfma_f32_16x16x32_bf16 v[40:43],v[148:151],v[148:151],0
M 36,v[32:35],152
buffer_store_dwordx4 v[152:155],v1,s[36:39],s57 offen offset:2048 sc0 sc1
M 40,v[152:155],152
M 36,v[32:35],156
buffer_store_dwordx4 v[156:159],v1,s[36:39],s58 offen offset:0 sc0 sc1
M 40,v[156:159],156
M 36,v[32:35],160
buffer_store_dwordx4 v[160:163],v1,s[36:39],s58 offen offset:2048 sc0 sc1
M 40,v[160:163],160
s_nop 5
v_add_f32_dpp v44,v36,v36 row_ror:8 row_mask:0xf bank_mask:0xf
s_nop 0
v_cndmask_b32 v45,v40,v41,s[70:71]
v_cndmask_b32 v46,v42,v43,s[70:71]
ds_write_b32 v2,v44 offset:1024
v_cndmask_b32 v45,v45,v46,s[72:73]
ds_write_b32 v0,v45 offset:1024
L 0
s_barrier
R 52,3,1024
R 56,3,1040
R 60,3,1056
L 0
v_pk_add_f32 v[52:53],v[52:53],v[54:55]
v_pk_add_f32 v[56:57],v[56:57],v[58:59]
v_pk_add_f32 v[60:61],v[60:61],v[62:63]
v_add_f32 v52,v52,v53
v_pk_add_f32 v[56:57],v[56:57],v[60:61]
v_mul_f32 v52,s68,v52
v_add_f32 v53,v56,v57
v_mul_f32 v53,s68,v53
v_fma_f32 v53,-v52,v52,v53
v_max_f32 v53,0,v53
v_add_f32 v54,s69,v53
v_fma_f32 v55,s69,v54,v53
v_rsq_f32 v65,v54
v_rsq_f32 v54,v55
v_mul_f32_e64 v64,-v52,v65
v_mul_f32 v63,v55,v54
v_mul_f32 v61,s76,v54
v_mul_f32 v55,v54,v54
v_mul_f32 v62,s77,v55
v_mul_f32 v62,v62,v61
v_cndmask_b32 v60,v52,v64,s[74:75]
v_cndmask_b32 v61,v61,v65,s[74:75]
buffer_store_dwordx4 v[60:63],v4,s[36:39],s59 offen offset:0 sc0 sc1
s_mov_b32 s63,s60
s_cmp_lt_u32 s45,s47
s_cbranch_scc1 Lr_a0_%=
Lr_mid_%=:
s_waitcnt vmcnt(0)
buffer_store_dword v13,v5,s[36:39],s63 offen offset:0 sc0 sc1
s_mov_b32 s63,0x7fff0000
s_cmp_eq_u32 s51,0
s_cbranch_scc1 Lr_done_%=
s_mov_b32 s45,s93
s_lshr_b32 s78,s45,1
s_and_b32 s79,s45,1
s_lshl_b32 s79,s79,7
s_lshl_b32 s55,s78,15
s_add_u32 s55,s55,s79
s_mul_i32 s56,s45,16384
s_add_u32 s56,s56,98566144
s_lshl_b32 s61,s78,11
s_add_u32 s61,s61,110587904
s_add_u32 s78,s45,s46
s_lshr_b32 s78,s78,1
s_lshl_b32 s62,s78,11
s_add_u32 s62,s62,110587904
Lr_bps_%=:
buffer_load_dword v14,v6,s[36:39],s61 offen offset:0 sc1
s_add_u32 s82,s82,1
s_waitcnt vmcnt(0)
v_cmp_eq_u32 vcc,s53,v14
s_nop 0
s_cmp_eq_u64 vcc,-1
s_cbranch_scc1 Lr_bqs_%=
s_sleep 2
s_cmp_lt_u32 s82,20000
s_cbranch_scc1 Lr_bps_%=
Lr_bqs_%=:
s_add_u32 s78,s55,71303168
buffer_load_dwordx4 v[164:167],v1,s[36:39],s78 offen offset:0 sc1
buffer_load_dwordx4 v[168:171],v1,s[36:39],s78 offen offset:2048 sc1
s_add_u32 s78,s55,71307264
buffer_load_dwordx4 v[172:175],v1,s[36:39],s78 offen offset:0 sc1
buffer_load_dwordx4 v[176:179],v1,s[36:39],s78 offen offset:2048 sc1
s_add_u32 s78,s55,89128960
buffer_load_dwordx4 v[180:183],v1,s[36:39],s78 offen offset:0 sc1
buffer_load_dwordx4 v[184:187],v1,s[36:39],s78 offen offset:2048 sc1
s_add_u32 s78,s55,89133056
buffer_load_dwordx4 v[188:191],v1,s[36:39],s78 offen offset:0 sc1
buffer_load_dwordx4 v[192:195],v1,s[36:39],s78 offen offset:2048 sc1
s_add_u32 m0,s65,0
s_nop 0
D 9,56,0
D 9,56,1024
D 9,56,2048
D 9,56,3072
buffer_load_dword v14,v6,s[36:39],s62 offen offset:0 sc1
s_lshr_b32 s78,s45,1
s_and_b32 s79,s45,1
s_lshl_b32 s79,s79,7
s_lshl_b32 s57,s78,15
s_add_u32 s57,s57,s79
s_add_u32 s57,s57,12582912
s_add_u32 s58,s57,4096
s_lshl_b32 s59,s78,9
s_add_u32 s59,s59,s79
s_add_u32 s59,s59,107020288
s_lshl_b32 s60,s45,9
s_add_u32 s60,s60,s66
s_add_u32 s60,s60,131072
s_add_u32 s45,s45,s46
s_lshr_b32 s78,s45,1
s_and_b32 s79,s45,1
s_lshl_b32 s79,s79,7
s_lshl_b32 s55,s78,15
s_add_u32 s55,s55,s79
s_mul_i32 s56,s45,16384
s_add_u32 s56,s56,98566144
s_lshl_b32 s61,s78,11
s_add_u32 s61,s61,110587904
s_add_u32 s78,s45,s46
s_lshr_b32 s78,s78,1
s_lshl_b32 s62,s78,11
s_add_u32 s62,s62,110587904
s_waitcnt vmcnt(0)
Lr_it0_%=:
T v[68:69],10,0
T v[70:71],10,64
T v[72:73],10,1024
T v[74:75],10,1088
T v[76:77],10,2048
T v[78:79],10,2112
T v[80:81],10,3072
T v[82:83],10,3136
s_nop 0
v_cmp_eq_u32 vcc,s53,v14
s_nop 0
s_cmp_eq_u64 vcc,-1
s_cselect_b32 s78,s45,256
s_cmp_lt_u32 s78,256
s_cbranch_scc1 Lr_ta0_%=
buffer_load_dword v14,v6,s[36:39],s61 offen offset:0 sc1
v_mfma_f32_16x16x32_bf16 v[116:119],v[26:29],v[164:167],0
L 6
v_lshlrev_b32 v84,16,v68
v_and_b32 v85,0xffff0000,v68
v_mfma_f32_16x16x32_bf16 v[120:123],v[24:27],v[164:167],0
L 4
v_lshlrev_b32 v86,16,v69
v_and_b32 v87,0xffff0000,v69
M 116,v[26:29],180
L 2
v_lshlrev_b32 v88,16,v70
v_and_b32 v89,0xffff0000,v70
M 120,v[24:27],180
L 0
v_lshlrev_b32 v90,16,v71
v_and_b32 v91,0xffff0000,v71
v_mfma_f32_16x16x32_bf16 v[124:127],v[26:29],v[168:171],0
v_lshlrev_b32 v92,16,v72
v_and_b32 v93,0xffff0000,v72
v_mfma_f32_16x16x32_bf16 v[128:131],v[24:27],v[168:171],0
v_lshlrev_b32 v94,16,v73
v_and_b32 v95,0xffff0000,v73
M 124,v[26:29],184
v_cvt_pk_bf16_f32 v148,v116,v117
v_cvt_pk_bf16_f32 v149,v118,v119
M 128,v[24:27],184
v_cvt_pk_bf16_f32 v150,v120,v121
v_cvt_pk_bf16_f32 v151,v122,v123
v_mfma_f32_16x16x32_bf16 v[132:135],v[26:29],v[172:175],0
v_lshlrev_b32 v96,16,v74
v_and_b32 v97,0xffff0000,v74
v_mfma_f32_16x16x32_bf16 v[136:139],v[24:27],v[172:175],0
v_lshlrev_b32 v98,16,v75
v_and_b32 v99,0xffff0000,v75
M 132,v[26:29],188
v_cvt_pk_bf16_f32 v152,v124,v125
v_cvt_pk_bf16_f32 v153,v126,v127
M 136,v[24:27],188
v_cvt_pk_bf16_f32 v154,v128,v129
v_cvt_pk_bf16_f32 v155,v130,v131
v_mfma_f32_16x16x32_bf16 v[140:143],v[26:29],v[176:179],0
v_lshlrev_b32 v100,16,v76
v_and_b32 v101,0xffff0000,v76
v_mfma_f32_16x16x32_bf16 v[144:147],v[24:27],v[176:179],0
v_lshlrev_b32 v102,16,v77
v_and_b32 v103,0xffff0000,v77
M 140,v[26:29],192
v_cvt_pk_bf16_f32 v156,v132,v133
v_cvt_pk_bf16_f32 v157,v134,v135
M 144,v[24:27],192
v_cvt_pk_bf16_f32 v158,v136,v137
v_cvt_pk_bf16_f32 v159,v138,v139
v_mfma_f32_16x16x32_bf16 v[36:39],v[32:35],v[148:151],0
v_lshlrev_b32 v104,16,v78
v_and_b32 v105,0xffff0000,v78
v_mfma_f32_16x16x32_bf16 v[40:43],v[148:151],v[148:151],0
v_lshlrev_b32 v106,16,v79
v_and_b32 v107,0xffff0000,v79
M 36,v[32:35],152
v_cvt_pk_bf16_f32 v160,v140,v141
v_cvt_pk_bf16_f32 v161,v142,v143
M 40,v[152:155],152
v_cvt_pk_bf16_f32 v162,v144,v145
v_cvt_pk_bf16_f32 v163,v146,v147
M 36,v[32:35],156
v_lshlrev_b32 v108,16,v80
v_and_b32 v109,0xffff0000,v80
M 40,v[156:159],156
v_lshlrev_b32 v110,16,v81
v_and_b32 v111,0xffff0000,v81
M 36,v[32:35],160
v_lshlrev_b32 v112,16,v82
v_and_b32 v113,0xffff0000,v82
M 40,v[160:163],160
v_lshlrev_b32 v114,16,v83
v_and_b32 v115,0xffff0000,v83
s_nop 2
v_add_f32_dpp v44,v36,v36 row_ror:8 row_mask:0xf bank_mask:0xf
s_nop 1
v_cndmask_b32 v45,v40,v41,s[70:71]
v_cndmask_b32 v46,v42,v43,s[70:71]
ds_write_b32 v2,v44 offset:0
v_cndmask_b32 v45,v45,v46,s[72:73]
ds_write_b32 v0,v45 offset:0
L 0
s_barrier
R 52,3,0
R 56,3,16
R 60,3,32
L 0
v_pk_add_f32 v[52:53],v[52:53],v[54:55]
v_pk_add_f32 v[56:57],v[56:57],v[58:59]
v_pk_add_f32 v[60:61],v[60:61],v[62:63]
v_add_f32 v52,v52,v53
v_pk_add_f32 v[56:57],v[56:57],v[60:61]
v_mul_f32 v52,s68,v52
v_add_f32 v53,v56,v57
v_mul_f32 v53,s68,v53
v_fma_f32 v53,-v52,v52,v53
v_max_f32 v53,0,v53
s_waitcnt vmcnt(1)
buffer_store_dword v13,v5,s[36:39],s63 offen offset:0 sc0 sc1
s_mov_b32 s63,0x7fff0000
s_waitcnt vmcnt(1)
v_cmp_eq_u32 vcc,s53,v14
s_nop 0
s_cmp_eq_u64 vcc,-1
s_cselect_b32 s78,s45,256
s_cmp_lt_u32 s78,256
s_cbranch_scc1 Lr_tb0_%=
buffer_load_dword v14,v6,s[36:39],s61 offen offset:0 sc1
v_add_f32 v53,s69,v53
v_rsq_f32 v48,v53
s_nop 0
v_mul_f32_e64 v50,-v52,v48
v_mov_b32 v49,v48
v_mov_b32 v51,v50
v_pk_fma_f32 v[52:53],v[116:117],v[48:49],v[50:51]
v_pk_fma_f32 v[54:55],v[118:119],v[48:49],v[50:51]
v_pk_fma_f32 v[56:57],v[120:121],v[48:49],v[50:51]
v_pk_fma_f32 v[58:59],v[122:123],v[48:49],v[50:51]
v_pk_fma_f32 v[116:117],v[52:53],v[84:85],v[116:117]
v_pk_fma_f32 v[118:119],v[54:55],v[86:87],v[118:119]
v_pk_fma_f32 v[120:121],v[56:57],v[88:89],v[120:121]
v_pk_fma_f32 v[122:123],v[58:59],v[90:91],v[122:123]
v_cvt_pk_bf16_f32 v148,v116,v117
v_cvt_pk_bf16_f32 v149,v118,v119
v_cvt_pk_bf16_f32 v150,v120,v121
v_cvt_pk_bf16_f32 v151,v122,v123
v_pk_fma_f32 v[52:53],v[124:125],v[48:49],v[50:51]
v_pk_fma_f32 v[54:55],v[126:127],v[48:49],v[50:51]
v_pk_fma_f32 v[56:57],v[128:129],v[48:49],v[50:51]
v_pk_fma_f32 v[58:59],v[130:131],v[48:49],v[50:51]
v_pk_fma_f32 v[124:125],v[52:53],v[92:93],v[124:125]
v_pk_fma_f32 v[126:127],v[54:55],v[94:95],v[126:127]
v_pk_fma_f32 v[128:129],v[56:57],v[96:97],v[128:129]
v_pk_fma_f32 v[130:131],v[58:59],v[98:99],v[130:131]
v_cvt_pk_bf16_f32 v152,v124,v125
v_cvt_pk_bf16_f32 v153,v126,v127
v_cvt_pk_bf16_f32 v154,v128,v129
v_cvt_pk_bf16_f32 v155,v130,v131
v_pk_fma_f32 v[52:53],v[132:133],v[48:49],v[50:51]
v_pk_fma_f32 v[54:55],v[134:135],v[48:49],v[50:51]
v_pk_fma_f32 v[56:57],v[136:137],v[48:49],v[50:51]
v_pk_fma_f32 v[58:59],v[138:139],v[48:49],v[50:51]
v_pk_fma_f32 v[132:133],v[52:53],v[100:101],v[132:133]
v_pk_fma_f32 v[134:135],v[54:55],v[102:103],v[134:135]
v_pk_fma_f32 v[136:137],v[56:57],v[104:105],v[136:137]
v_pk_fma_f32 v[138:139],v[58:59],v[106:107],v[138:139]
v_cvt_pk_bf16_f32 v156,v132,v133
v_cvt_pk_bf16_f32 v157,v134,v135
v_cvt_pk_bf16_f32 v158,v136,v137
v_cvt_pk_bf16_f32 v159,v138,v139
v_pk_fma_f32 v[52:53],v[140:141],v[48:49],v[50:51]
v_pk_fma_f32 v[54:55],v[142:143],v[48:49],v[50:51]
v_pk_fma_f32 v[56:57],v[144:145],v[48:49],v[50:51]
v_pk_fma_f32 v[58:59],v[146:147],v[48:49],v[50:51]
v_pk_fma_f32 v[140:141],v[52:53],v[108:109],v[140:141]
v_pk_fma_f32 v[142:143],v[54:55],v[110:111],v[142:143]
v_pk_fma_f32 v[144:145],v[56:57],v[112:113],v[144:145]
v_pk_fma_f32 v[146:147],v[58:59],v[114:115],v[146:147]
v_cvt_pk_bf16_f32 v160,v140,v141
v_cvt_pk_bf16_f32 v161,v142,v143
v_cvt_pk_bf16_f32 v162,v144,v145
v_cvt_pk_bf16_f32 v163,v146,v147
v_mfma_f32_16x16x32_bf16 v[36:39],v[32:35],v[148:151],0
buffer_store_dwordx4 v[148:151],v1,s[36:39],s57 offen offset:0 sc0 sc1
v_mfma_f32_16x16x32_bf16 v[40:43],v[148:151],v[148:151],0
M 36,v[32:35],152
buffer_store_dwordx4 v[152:155],v1,s[36:39],s57 offen offset:2048 sc0 sc1
M 40,v[152:155],152
M 36,v[32:35],156
buffer_store_dwordx4 v[156:159],v1,s[36:39],s58 offen offset:0 sc0 sc1
M 40,v[156:159],156
M 36,v[32:35],160
buffer_store_dwordx4 v[160:163],v1,s[36:39],s58 offen offset:2048 sc0 sc1
M 40,v[160:163],160
s_nop 5
v_add_f32_dpp v44,v36,v36 row_ror:8 row_mask:0xf bank_mask:0xf
s_nop 0
v_cndmask_b32 v45,v40,v41,s[70:71]
v_cndmask_b32 v46,v42,v43,s[70:71]
ds_write_b32 v2,v44 offset:1024
v_cndmask_b32 v45,v45,v46,s[72:73]
ds_write_b32 v0,v45 offset:1024
L 0
s_barrier
R 52,3,1024
R 56,3,1040
R 60,3,1056
L 0
v_pk_add_f32 v[52:53],v[52:53],v[54:55]
v_pk_add_f32 v[56:57],v[56:57],v[58:59]
v_pk_add_f32 v[60:61],v[60:61],v[62:63]
v_add_f32 v52,v52,v53
v_pk_add_f32 v[56:57],v[56:57],v[60:61]
v_mul_f32 v52,s68,v52
v_add_f32 v53,v56,v57
v_mul_f32 v53,s68,v53
v_fma_f32 v53,-v52,v52,v53
v_max_f32 v53,0,v53
v_add_f32 v54,s69,v53
v_fma_f32 v55,s69,v54,v53
v_rsq_f32 v65,v54
v_rsq_f32 v54,v55
v_mul_f32_e64 v64,-v52,v65
v_mul_f32 v63,v55,v54
v_mul_f32 v61,s76,v54
v_mul_f32 v55,v54,v54
v_mul_f32 v62,s77,v55
v_mul_f32 v62,v62,v61
v_cndmask_b32 v60,v52,v64,s[74:75]
v_cndmask_b32 v61,v61,v65,s[74:75]
buffer_store_dwordx4 v[60:63],v4,s[36:39],s59 offen offset:0 sc0 sc1
s_mov_b32 s63,s60
s_waitcnt vmcnt(5)
v_cmp_eq_u32 vcc,s53,v14
s_nop 0
s_cmp_eq_u64 vcc,-1
s_cselect_b32 s78,s45,256
s_cmp_lt_u32 s78,256
s_cbranch_scc1 Lr_tc0_%=
s_waitcnt vmcnt(0)
buffer_store_dword v13,v5,s[36:39],s63 offen offset:0 sc0 sc1
s_mov_b32 s63,0x7fff0000
s_cmp_ge_u32 s45,256
s_cbranch_scc1 Lr_done_%=
Lr_bp0_%=:
buffer_load_dword v14,v6,s[36:39],s61 offen offset:0 sc1
s_add_u32 s82,s82,1
s_waitcnt vmcnt(0)
v_cmp_eq_u32 vcc,s53,v14
s_nop 0
s_cmp_eq_u64 vcc,-1
s_cbranch_scc1 Lr_bq0_%=
s_sleep 2
s_cmp_lt_u32 s82,20000
s_cbranch_scc1 Lr_bp0_%=
Lr_bq0_%=:
s_add_u32 s78,s55,71303168
buffer_load_dwordx4 v[196:199],v1,s[36:39],s78 offen offset:0 sc1
buffer_load_dwordx4 v[200:203],v1,s[36:39],s78 offen offset:2048 sc1
s_add_u32 s78,s55,71307264
buffer_load_dwordx4 v[204:207],v1,s[36:39],s78 offen offset:0 sc1
buffer_load_dwordx4 v[208:211],v1,s[36:39],s78 offen offset:2048 sc1
s_add_u32 s78,s55,89128960
buffer_load_dwordx4 v[212:215],v1,s[36:39],s78 offen offset:0 sc1
buffer_load_dwordx4 v[216:219],v1,s[36:39],s78 offen offset:2048 sc1
s_add_u32 s78,s55,89133056
buffer_load_dwordx4 v[220:223],v1,s[36:39],s78 offen offset:0 sc1
buffer_load_dwordx4 v[224:227],v1,s[36:39],s78 offen offset:2048 sc1
s_add_u32 m0,s65,16384
s_nop 0
D 9,56,0
D 9,56,1024
D 9,56,2048
D 9,56,3072
buffer_load_dword v14,v6,s[36:39],s62 offen offset:0 sc1
s_lshr_b32 s78,s45,1
s_and_b32 s79,s45,1
s_lshl_b32 s79,s79,7
s_lshl_b32 s57,s78,15
s_add_u32 s57,s57,s79
s_add_u32 s57,s57,12582912
s_add_u32 s58,s57,4096
s_lshl_b32 s59,s78,9
s_add_u32 s59,s59,s79
s_add_u32 s59,s59,107020288
s_lshl_b32 s60,s45,9
s_add_u32 s60,s60,s66
s_add_u32 s60,s60,131072
s_add_u32 s45,s45,s46
s_lshr_b32 s78,s45,1
s_and_b32 s79,s45,1
s_lshl_b32 s79,s79,7
s_lshl_b32 s55,s78,15
s_add_u32 s55,s55,s79
s_mul_i32 s56,s45,16384
s_add_u32 s56,s56,98566144
s_lshl_b32 s61,s78,11
s_add_u32 s61,s61,110587904
s_add_u32 s78,s45,s46
s_lshr_b32 s78,s78,1
s_lshl_b32 s62,s78,11
s_add_u32 s62,s62,110587904
s_waitcnt vmcnt(0)
s_branch Lr_it1_%=
Lr_ta0_%=:
v_mfma_f32_16x16x32_bf16 v[116:119],v[26:29],v[164:167],0
s_add_u32 s78,s55,71303168
buffer_load_dwordx4 v[196:199],v1,s[36:39],s78 offen offset:0 sc1
v_mfma_f32_16x16x32_bf16 v[120:123],v[24:27],v[164:167],0
L 6
)"
R"(buffer_load_dwordx4 v[200:203],v1,s[36:39],s78 offen offset:2048 sc1
M 116,v[26:29],180
s_add_u32 s78,s55,71307264
buffer_load_dwordx4 v[204:207],v1,s[36:39],s78 offen offset:0 sc1
M 120,v[24:27],180
L 4
buffer_load_dwordx4 v[208:211],v1,s[36:39],s78 offen offset:2048 sc1
v_mfma_f32_16x16x32_bf16 v[124:127],v[26:29],v[168:171],0
s_add_u32 s78,s55,89128960
buffer_load_dwordx4 v[212:215],v1,s[36:39],s78 offen offset:0 sc1
v_mfma_f32_16x16x32_bf16 v[128:131],v[24:27],v[168:171],0
L 2
buffer_load_dwordx4 v[216:219],v1,s[36:39],s78 offen offset:2048 sc1
M 124,v[26:29],184
L 0
v_cvt_pk_bf16_f32 v148,v116,v117
v_cvt_pk_bf16_f32 v149,v118,v119
M 128,v[24:27],184
s_add_u32 s78,s55,89133056
buffer_load_dwordx4 v[220:223],v1,s[36:39],s78 offen offset:0 sc1
v_mfma_f32_16x16x32_bf16 v[132:135],v[26:29],v[172:175],0
s_add_u32 m0,s65,16384
buffer_load_dwordx4 v[224:227],v1,s[36:39],s78 offen offset:2048 sc1
v_mfma_f32_16x16x32_bf16 v[136:139],v[24:27],v[172:175],0
D 9,56,0
M 132,v[26:29],188
D 9,56,1024
M 136,v[24:27],188
D 9,56,2048
v_mfma_f32_16x16x32_bf16 v[140:143],v[26:29],v[176:179],0
D 9,56,3072
v_mfma_f32_16x16x32_bf16 v[144:147],v[24:27],v[176:179],0
v_cvt_pk_bf16_f32 v150,v120,v121
v_cvt_pk_bf16_f32 v151,v122,v123
M 140,v[26:29],192
v_cvt_pk_bf16_f32 v152,v124,v125
v_cvt_pk_bf16_f32 v153,v126,v127
M 144,v[24:27],192
v_cvt_pk_bf16_f32 v154,v128,v129
v_cvt_pk_bf16_f32 v155,v130,v131
v_mfma_f32_16x16x32_bf16 v[36:39],v[32:35],v[148:151],0
v_cvt_pk_bf16_f32 v156,v132,v133
v_cvt_pk_bf16_f32 v157,v134,v135
v_mfma_f32_16x16x32_bf16 v[40:43],v[148:151],v[148:151],0
v_cvt_pk_bf16_f32 v158,v136,v137
v_cvt_pk_bf16_f32 v159,v138,v139
M 36,v[32:35],152
v_cvt_pk_bf16_f32 v160,v140,v141
v_cvt_pk_bf16_f32 v161,v142,v143
M 40,v[152:155],152
v_cvt_pk_bf16_f32 v162,v144,v145
v_cvt_pk_bf16_f32 v163,v146,v147
M 36,v[32:35],156
v_lshlrev_b32 v84,16,v68
v_and_b32 v85,0xffff0000,v68
M 40,v[156:159],156
v_lshlrev_b32 v86,16,v69
v_and_b32 v87,0xffff0000,v69
M 36,v[32:35],160
v_lshlrev_b32 v88,16,v70
v_and_b32 v89,0xffff0000,v70
M 40,v[160:163],160
v_lshlrev_b32 v90,16,v71
v_and_b32 v91,0xffff0000,v71
v_lshlrev_b32 v92,16,v72
v_and_b32 v93,0xffff0000,v72
v_lshlrev_b32 v94,16,v73
v_and_b32 v95,0xffff0000,v73
v_lshlrev_b32 v96,16,v74
v_and_b32 v97,0xffff0000,v74
v_lshlrev_b32 v98,16,v75
v_and_b32 v99,0xffff0000,v75
v_lshlrev_b32 v100,16,v76
v_and_b32 v101,0xffff0000,v76
v_lshlrev_b32 v102,16,v77
v_and_b32 v103,0xffff0000,v77
v_lshlrev_b32 v104,16,v78
v_and_b32 v105,0xffff0000,v78
v_lshlrev_b32 v106,16,v79
v_and_b32 v107,0xffff0000,v79
v_lshlrev_b32 v108,16,v80
v_and_b32 v109,0xffff0000,v80
v_lshlrev_b32 v110,16,v81
v_and_b32 v111,0xffff0000,v81
v_lshlrev_b32 v112,16,v82
v_and_b32 v113,0xffff0000,v82
v_lshlrev_b32 v114,16,v83
v_and_b32 v115,0xffff0000,v83
v_add_f32_dpp v44,v36,v36 row_ror:8 row_mask:0xf bank_mask:0xf
v_cndmask_b32 v45,v40,v41,s[70:71]
v_cndmask_b32 v46,v42,v43,s[70:71]
ds_write_b32 v2,v44 offset:0
v_cndmask_b32 v45,v45,v46,s[72:73]
ds_write_b32 v0,v45 offset:0
L 0
s_barrier
R 52,3,0
R 56,3,16
R 60,3,32
L 0
v_pk_add_f32 v[52:53],v[52:53],v[54:55]
v_pk_add_f32 v[56:57],v[56:57],v[58:59]
v_pk_add_f32 v[60:61],v[60:61],v[62:63]
v_add_f32 v52,v52,v53
v_pk_add_f32 v[56:57],v[56:57],v[60:61]
v_mul_f32 v52,s68,v52
v_add_f32 v53,v56,v57
v_mul_f32 v53,s68,v53
v_fma_f32 v53,-v52,v52,v53
v_max_f32 v53,0,v53
buffer_load_dword v14,v6,s[36:39],s62 offen offset:0 sc1
v_add_f32 v53,s69,v53
v_rsq_f32 v48,v53
s_nop 0
v_mul_f32_e64 v50,-v52,v48
v_mov_b32 v49,v48
v_mov_b32 v51,v50
v_pk_fma_f32 v[52:53],v[116:117],v[48:49],v[50:51]
v_pk_fma_f32 v[54:55],v[118:119],v[48:49],v[50:51]
v_pk_fma_f32 v[56:57],v[120:121],v[48:49],v[50:51]
v_pk_fma_f32 v[58:59],v[122:123],v[48:49],v[50:51]
v_pk_fma_f32 v[116:117],v[52:53],v[84:85],v[116:117]
v_pk_fma_f32 v[118:119],v[54:55],v[86:87],v[118:119]
v_pk_fma_f32 v[120:121],v[56:57],v[88:89],v[120:121]
v_pk_fma_f32 v[122:123],v[58:59],v[90:91],v[122:123]
v_cvt_pk_bf16_f32 v148,v116,v117
v_cvt_pk_bf16_f32 v149,v118,v119
v_cvt_pk_bf16_f32 v150,v120,v121
v_cvt_pk_bf16_f32 v151,v122,v123
v_pk_fma_f32 v[52:53],v[124:125],v[48:49],v[50:51]
v_pk_fma_f32 v[54:55],v[126:127],v[48:49],v[50:51]
v_pk_fma_f32 v[56:57],v[128:129],v[48:49],v[50:51]
v_pk_fma_f32 v[58:59],v[130:131],v[48:49],v[50:51]
v_pk_fma_f32 v[124:125],v[52:53],v[92:93],v[124:125]
v_pk_fma_f32 v[126:127],v[54:55],v[94:95],v[126:127]
v_pk_fma_f32 v[128:129],v[56:57],v[96:97],v[128:129]
v_pk_fma_f32 v[130:131],v[58:59],v[98:99],v[130:131]
v_cvt_pk_bf16_f32 v152,v124,v125
v_cvt_pk_bf16_f32 v153,v126,v127
v_cvt_pk_bf16_f32 v154,v128,v129
v_cvt_pk_bf16_f32 v155,v130,v131
v_pk_fma_f32 v[52:53],v[132:133],v[48:49],v[50:51]
v_pk_fma_f32 v[54:55],v[134:135],v[48:49],v[50:51]
v_pk_fma_f32 v[56:57],v[136:137],v[48:49],v[50:51]
v_pk_fma_f32 v[58:59],v[138:139],v[48:49],v[50:51]
v_pk_fma_f32 v[132:133],v[52:53],v[100:101],v[132:133]
v_pk_fma_f32 v[134:135],v[54:55],v[102:103],v[134:135]
v_pk_fma_f32 v[136:137],v[56:57],v[104:105],v[136:137]
v_pk_fma_f32 v[138:139],v[58:59],v[106:107],v[138:139]
v_cvt_pk_bf16_f32 v156,v132,v133
v_cvt_pk_bf16_f32 v157,v134,v135
v_cvt_pk_bf16_f32 v158,v136,v137
v_cvt_pk_bf16_f32 v159,v138,v139
v_pk_fma_f32 v[52:53],v[140:141],v[48:49],v[50:51]
v_pk_fma_f32 v[54:55],v[142:143],v[48:49],v[50:51]
v_pk_fma_f32 v[56:57],v[144:145],v[48:49],v[50:51]
v_pk_fma_f32 v[58:59],v[146:147],v[48:49],v[50:51]
v_pk_fma_f32 v[140:141],v[52:53],v[108:109],v[140:141]
v_pk_fma_f32 v[142:143],v[54:55],v[110:111],v[142:143]
v_pk_fma_f32 v[144:145],v[56:57],v[112:113],v[144:145]
v_pk_fma_f32 v[146:147],v[58:59],v[114:115],v[146:147]
v_cvt_pk_bf16_f32 v160,v140,v141
v_cvt_pk_bf16_f32 v161,v142,v143
v_cvt_pk_bf16_f32 v162,v144,v145
v_cvt_pk_bf16_f32 v163,v146,v147
s_waitcnt vmcnt(13)
buffer_store_dword v13,v5,s[36:39],s63 offen offset:0 sc0 sc1
s_mov_b32 s63,0x7fff0000
v_mfma_f32_16x16x32_bf16 v[36:39],v[32:35],v[148:151],0
buffer_store_dwordx4 v[148:151],v1,s[36:39],s57 offen offset:0 sc0 sc1
v_mfma_f32_16x16x32_bf16 v[40:43],v[148:151],v[148:151],0
M 36,v[32:35],152
buffer_store_dwordx4 v[152:155],v1,s[36:39],s57 offen offset:2048 sc0 sc1
M 40,v[152:155],152
M 36,v[32:35],156
buffer_store_dwordx4 v[156:159],v1,s[36:39],s58 offen offset:0 sc0 sc1
M 40,v[156:159],156
M 36,v[32:35],160
buffer_store_dwordx4 v[160:163],v1,s[36:39],s58 offen offset:2048 sc0 sc1
M 40,v[160:163],160
s_nop 5
v_add_f32_dpp v44,v36,v36 row_ror:8 row_mask:0xf bank_mask:0xf
s_nop 0
v_cndmask_b32 v45,v40,v41,s[70:71]
v_cndmask_b32 v46,v42,v43,s[70:71]
ds_write_b32 v2,v44 offset:1024
v_cndmask_b32 v45,v45,v46,s[72:73]
ds_write_b32 v0,v45 offset:1024
L 0
s_barrier
R 52,3,1024
R 56,3,1040
R 60,3,1056
L 0
v_pk_add_f32 v[52:53],v[52:53],v[54:55]
v_pk_add_f32 v[56:57],v[56:57],v[58:59]
v_pk_add_f32 v[60:61],v[60:61],v[62:63]
v_add_f32 v52,v52,v53
v_pk_add_f32 v[56:57],v[56:57],v[60:61]
v_mul_f32 v52,s68,v52
v_add_f32 v53,v56,v57
v_mul_f32 v53,s68,v53
v_fma_f32 v53,-v52,v52,v53
v_max_f32 v53,0,v53
v_add_f32 v54,s69,v53
v_fma_f32 v55,s69,v54,v53
v_rsq_f32 v65,v54
v_rsq_f32 v54,v55
v_mul_f32_e64 v64,-v52,v65
v_mul_f32 v63,v55,v54
v_mul_f32 v61,s76,v54
v_mul_f32 v55,v54,v54
v_mul_f32 v62,s77,v55
v_mul_f32 v62,v62,v61
v_cndmask_b32 v60,v52,v64,s[74:75]
v_cndmask_b32 v61,v61,v65,s[74:75]
buffer_store_dwordx4 v[60:63],v4,s[36:39],s59 offen offset:0 sc0 sc1
s_mov_b32 s63,s60
s_lshr_b32 s78,s45,1
s_and_b32 s79,s45,1
s_lshl_b32 s79,s79,7
s_lshl_b32 s57,s78,15
s_add_u32 s57,s57,s79
s_add_u32 s57,s57,12582912
s_add_u32 s58,s57,4096
s_lshl_b32 s59,s78,9
s_add_u32 s59,s59,s79
s_add_u32 s59,s59,107020288
s_lshl_b32 s60,s45,9
s_add_u32 s60,s60,s66
s_add_u32 s60,s60,131072
s_add_u32 s45,s45,s46
s_lshr_b32 s78,s45,1
s_and_b32 s79,s45,1
s_lshl_b32 s79,s79,7
s_lshl_b32 s55,s78,15
s_add_u32 s55,s55,s79
s_mul_i32 s56,s45,16384
s_add_u32 s56,s56,98566144
s_lshl_b32 s61,s78,11
s_add_u32 s61,s61,110587904
s_add_u32 s78,s45,s46
s_lshr_b32 s78,s78,1
s_lshl_b32 s62,s78,11
s_add_u32 s62,s62,110587904
s_waitcnt vmcnt(6)
s_branch Lr_it1_%=
Lr_tb0_%=:
v_add_f32 v53,s69,v53
v_rsq_f32 v48,v53
s_nop 0
v_mul_f32_e64 v50,-v52,v48
v_mov_b32 v49,v48
v_mov_b32 v51,v50
v_pk_fma_f32 v[52:53],v[116:117],v[48:49],v[50:51]
v_pk_fma_f32 v[54:55],v[118:119],v[48:49],v[50:51]
v_pk_fma_f32 v[56:57],v[120:121],v[48:49],v[50:51]
v_pk_fma_f32 v[58:59],v[122:123],v[48:49],v[50:51]
s_add_u32 s78,s55,71303168
buffer_load_dwordx4 v[196:199],v1,s[36:39],s78 offen offset:0 sc1
buffer_load_dwordx4 v[200:203],v1,s[36:39],s78 offen offset:2048 sc1
s_add_u32 s78,s55,71307264
buffer_load_dwordx4 v[204:207],v1,s[36:39],s78 offen offset:0 sc1
buffer_load_dwordx4 v[208:211],v1,s[36:39],s78 offen offset:2048 sc1
v_pk_fma_f32 v[116:117],v[52:53],v[84:85],v[116:117]
v_pk_fma_f32 v[118:119],v[54:55],v[86:87],v[118:119]
v_pk_fma_f32 v[120:121],v[56:57],v[88:89],v[120:121]
v_pk_fma_f32 v[122:123],v[58:59],v[90:91],v[122:123]
v_cvt_pk_bf16_f32 v148,v116,v117
v_cvt_pk_bf16_f32 v149,v118,v119
v_cvt_pk_bf16_f32 v150,v120,v121
v_cvt_pk_bf16_f32 v151,v122,v123
v_pk_fma_f32 v[52:53],v[124:125],v[48:49],v[50:51]
v_pk_fma_f32 v[54:55],v[126:127],v[48:49],v[50:51]
v_pk_fma_f32 v[56:57],v[128:129],v[48:49],v[50:51]
v_pk_fma_f32 v[58:59],v[130:131],v[48:49],v[50:51]
s_add_u32 s78,s55,89128960
buffer_load_dwordx4 v[212:215],v1,s[36:39],s78 offen offset:0 sc1
buffer_load_dwordx4 v[216:219],v1,s[36:39],s78 offen offset:2048 sc1
s_add_u32 s78,s55,89133056
buffer_load_dwordx4 v[220:223],v1,s[36:39],s78 offen offset:0 sc1
buffer_load_dwordx4 v[224:227],v1,s[36:39],s78 offen offset:2048 sc1
v_pk_fma_f32 v[124:125],v[52:53],v[92:93],v[124:125]
v_pk_fma_f32 v[126:127],v[54:55],v[94:95],v[126:127]
v_pk_fma_f32 v[128:129],v[56:57],v[96:97],v[128:129]
v_pk_fma_f32 v[130:131],v[58:59],v[98:99],v[130:131]
v_cvt_pk_bf16_f32 v152,v124,v125
v_cvt_pk_bf16_f32 v153,v126,v127
v_cvt_pk_bf16_f32 v154,v128,v129
v_cvt_pk_bf16_f32 v155,v130,v131
v_pk_fma_f32 v[52:53],v[132:133],v[48:49],v[50:51]
v_pk_fma_f32 v[54:55],v[134:135],v[48:49],v[50:51]
v_pk_fma_f32 v[56:57],v[136:137],v[48:49],v[50:51]
v_pk_fma_f32 v[58:59],v[138:139],v[48:49],v[50:51]
s_add_u32 m0,s65,16384
s_nop 0
D 9,56,0
D 9,56,1024
D 9,56,2048
D 9,56,3072
v_pk_fma_f32 v[132:133],v[52:53],v[100:101],v[132:133]
v_pk_fma_f32 v[134:135],v[54:55],v[102:103],v[134:135]
v_pk_fma_f32 v[136:137],v[56:57],v[104:105],v[136:137]
v_pk_fma_f32 v[138:139],v[58:59],v[106:107],v[138:139]
v_cvt_pk_bf16_f32 v156,v132,v133
v_cvt_pk_bf16_f32 v157,v134,v135
v_cvt_pk_bf16_f32 v158,v136,v137
v_cvt_pk_bf16_f32 v159,v138,v139
v_pk_fma_f32 v[52:53],v[140:141],v[48:49],v[50:51]
v_pk_fma_f32 v[54:55],v[142:143],v[48:49],v[50:51]
v_pk_fma_f32 v[56:57],v[144:145],v[48:49],v[50:51]
v_pk_fma_f32 v[58:59],v[146:147],v[48:49],v[50:51]
v_pk_fma_f32 v[140:141],v[52:53],v[108:109],v[140:141]
v_pk_fma_f32 v[142:143],v[54:55],v[110:111],v[142:143]
v_pk_fma_f32 v[144:145],v[56:57],v[112:113],v[144:145]
v_pk_fma_f32 v[146:147],v[58:59],v[114:115],v[146:147]
v_cvt_pk_bf16_f32 v160,v140,v141
v_cvt_pk_bf16_f32 v161,v142,v143
v_cvt_pk_bf16_f32 v162,v144,v145
v_cvt_pk_bf16_f32 v163,v146,v147
v_mfma_f32_16x16x32_bf16 v[36:39],v[32:35],v[148:151],0
buffer_load_dword v14,v6,s[36:39],s62 offen offset:0 sc1
buffer_store_dwordx4 v[148:151],v1,s[36:39],s57 offen offset:0 sc0 sc1
v_mfma_f32_16x16x32_bf16 v[40:43],v[148:151],v[148:151],0
M 36,v[32:35],152
buffer_store_dwordx4 v[152:155],v1,s[36:39],s57 offen offset:2048 sc0 sc1
M 40,v[152:155],152
M 36,v[32:35],156
buffer_store_dwordx4 v[156:159],v1,s[36:39],s58 offen offset:0 sc0 sc1
M 40,v[156:159],156
M 36,v[32:35],160
buffer_store_dwordx4 v[160:163],v1,s[36:39],s58 offen offset:2048 sc0 sc1
M 40,v[160:163],160
s_nop 5
v_add_f32_dpp v44,v36,v36 row_ror:8 row_mask:0xf bank_mask:0xf
s_nop 0
v_cndmask_b32 v45,v40,v41,s[70:71]
v_cndmask_b32 v46,v42,v43,s[70:71]
ds_write_b32 v2,v44 offset:1024
v_cndmask_b32 v45,v45,v46,s[72:73]
ds_write_b32 v0,v45 offset:1024
L 0
s_barrier
R 52,3,1024
R 56,3,1040
R 60,3,1056
L 0
v_pk_add_f32 v[52:53],v[52:53],v[54:55]
v_pk_add_f32 v[56:57],v[56:57],v[58:59]
v_pk_add_f32 v[60:61],v[60:61],v[62:63]
v_add_f32 v52,v52,v53
v_pk_add_f32 v[56:57],v[56:57],v[60:61]
v_mul_f32 v52,s68,v52
v_add_f32 v53,v56,v57
v_mul_f32 v53,s68,v53
v_fma_f32 v53,-v52,v52,v53
v_max_f32 v53,0,v53
v_add_f32 v54,s69,v53
v_fma_f32 v55,s69,v54,v53
v_rsq_f32 v65,v54
v_rsq_f32 v54,v55
v_mul_f32_e64 v64,-v52,v65
v_mul_f32 v63,v55,v54
v_mul_f32 v61,s76,v54
v_mul_f32 v55,v54,v54
v_mul_f32 v62,s77,v55
v_mul_f32 v62,v62,v61
v_cndmask_b32 v60,v52,v64,s[74:75]
v_cndmask_b32 v61,v61,v65,s[74:75]
buffer_store_dwordx4 v[60:63],v4,s[36:39],s59 offen offset:0 sc0 sc1
s_mov_b32 s63,s60
s_lshr_b32 s78,s45,1
s_and_b32 s79,s45,1
s_lshl_b32 s79,s79,7
s_lshl_b32 s57,s78,15
s_add_u32 s57,s57,s79
s_add_u32 s57,s57,12582912
s_add_u32 s58,s57,4096
s_lshl_b32 s59,s78,9
s_add_u32 s59,s59,s79
s_add_u32 s59,s59,107020288
s_lshl_b32 s60,s45,9
s_add_u32 s60,s60,s66
s_add_u32 s60,s60,131072
s_add_u32 s45,s45,s46
s_lshr_b32 s78,s45,1
s_and_b32 s79,s45,1
s_lshl_b32 s79,s79,7
s_lshl_b32 s55,s78,15
s_add_u32 s55,s55,s79
s_mul_i32 s56,s45,16384
s_add_u32 s56,s56,98566144
s_lshl_b32 s61,s78,11
s_add_u32 s61,s61,110587904
s_add_u32 s78,s45,s46
s_lshr_b32 s78,s78,1
s_lshl_b32 s62,s78,11
s_add_u32 s62,s62,110587904
s_waitcnt vmcnt(5)
s_branch Lr_it1_%=
Lr_tc0_%=:
s_add_u32 s78,s55,71303168
buffer_load_dwordx4 v[196:199],v1,s[36:39],s78 offen offset:0 sc1
buffer_load_dwordx4 v[200:203],v1,s[36:39],s78 offen offset:2048 sc1
s_add_u32 s78,s55,71307264
buffer_load_dwordx4 v[204:207],v1,s[36:39],s78 offen offset:0 sc1
buffer_load_dwordx4 v[208:211],v1,s[36:39],s78 offen offset:2048 sc1
s_add_u32 s78,s55,89128960
buffer_load_dwordx4 v[212:215],v1,s[36:39],s78 offen offset:0 sc1
buffer_load_dwordx4 v[216:219],v1,s[36:39],s78 offen offset:2048 sc1
s_add_u32 s78,s55,89133056
buffer_load_dwordx4 v[220:223],v1,s[36:39],s78 offen offset:0 sc1
buffer_load_dwordx4 v[224:227],v1,s[36:39],s78 offen offset:2048 sc1
s_add_u32 m0,s65,16384
s_nop 0
D 9,56,0
D 9,56,1024
D 9,56,2048
D 9,56,3072
buffer_load_dword v14,v6,s[36:39],s62 offen offset:0 sc1
s_waitcnt vmcnt(13)
buffer_store_dword v13,v5,s[36:39],s63 offen offset:0 sc0 sc1
s_mov_b32 s63,0x7fff0000
s_lshr_b32 s78,s45,1
s_and_b32 s79,s45,1
s_lshl_b32 s79,s79,7
s_lshl_b32 s57,s78,15
s_add_u32 s57,s57,s79
s_add_u32 s57,s57,12582912
s_add_u32 s58,s57,4096
s_lshl_b32 s59,s78,9
s_add_u32 s59,s59,s79
s_add_u32 s59,s59,107020288
s_lshl_b32 s60,s45,9
s_add_u32 s60,s60,s66
s_add_u32 s60,s60,131072
s_add_u32 s45,s45,s46
s_lshr_b32 s78,s45,1
s_and_b32 s79,s45,1
s_lshl_b32 s79,s79,7
s_lshl_b32 s55,s78,15
s_add_u32 s55,s55,s79
s_mul_i32 s56,s45,16384
s_add_u32 s56,s56,98566144
s_lshl_b32 s61,s78,11
s_add_u32 s61,s61,110587904
s_add_u32 s78,s45,s46
s_lshr_b32 s78,s78,1
s_lshl_b32 s62,s78,11
s_add_u32 s62,s62,110587904
s_waitcnt vmcnt(1)
s_branch Lr_it1_%=
Lr_it1_%=:
T v[68:69],10,16384
T v[70:71],10,16448
T v[72:73],10,17408
T v[74:75],10,17472
T v[76:77],10,18432
T v[78:79],10,18496
T v[80:81],10,19456
T v[82:83],10,19520
s_nop 0
v_cmp_eq_u32 vcc,s53,v14
s_nop 0
s_cmp_eq_u64 vcc,-1
s_cselect_b32 s78,s45,256
s_cmp_lt_u32 s78,256
s_cbranch_scc1 Lr_ta1_%=
buffer_load_dword v14,v6,s[36:39],s61 offen offset:0 sc1
v_mfma_f32_16x16x32_bf16 v[116:119],v[26:29],v[196:199],0
L 6
v_lshlrev_b32 v84,16,v68
v_and_b32 v85,0xffff0000,v68
v_mfma_f32_16x16x32_bf16 v[120:123],v[24:27],v[196:199],0
L 4
v_lshlrev_b32 v86,16,v69
v_and_b32 v87,0xffff0000,v69
M 116,v[26:29],212
L 2
v_lshlrev_b32 v88,16,v70
v_and_b32 v89,0xffff0000,v70
M 120,v[24:27],212
L 0
v_lshlrev_b32 v90,16,v71
v_and_b32 v91,0xffff0000,v71
v_mfma_f32_16x16x32_bf16 v[124:127],v[26:29],v[200:203],0
v_lshlrev_b32 v92,16,v72
v_and_b32 v93,0xffff0000,v72
v_mfma_f32_16x16x32_bf16 v[128:131],v[24:27],v[200:203],0
v_lshlrev_b32 v94,16,v73
v_and_b32 v95,0xffff0000,v73
M 124,v[26:29],216
v_cvt_pk_bf16_f32 v148,v116,v117
v_cvt_pk_bf16_f32 v149,v118,v119
M 128,v[24:27],216
v_cvt_pk_bf16_f32 v150,v120,v121
v_cvt_pk_bf16_f32 v151,v122,v123
v_mfma_f32_16x16x32_bf16 v[132:135],v[26:29],v[204:207],0
v_lshlrev_b32 v96,16,v74
v_and_b32 v97,0xffff0000,v74
)"
R"(v_mfma_f32_16x16x32_bf16 v[136:139],v[24:27],v[204:207],0
v_lshlrev_b32 v98,16,v75
v_and_b32 v99,0xffff0000,v75
M 132,v[26:29],220
v_cvt_pk_bf16_f32 v152,v124,v125
v_cvt_pk_bf16_f32 v153,v126,v127
M 136,v[24:27],220
v_cvt_pk_bf16_f32 v154,v128,v129
v_cvt_pk_bf16_f32 v155,v130,v131
v_mfma_f32_16x16x32_bf16 v[140:143],v[26:29],v[208:211],0
v_lshlrev_b32 v100,16,v76
v_and_b32 v101,0xffff0000,v76
v_mfma_f32_16x16x32_bf16 v[144:147],v[24:27],v[208:211],0
v_lshlrev_b32 v102,16,v77
v_and_b32 v103,0xffff0000,v77
M 140,v[26:29],224
v_cvt_pk_bf16_f32 v156,v132,v133
v_cvt_pk_bf16_f32 v157,v134,v135
M 144,v[24:27],224
v_cvt_pk_bf16_f32 v158,v136,v137
v_cvt_pk_bf16_f32 v159,v138,v139
v_mfma_f32_16x16x32_bf16 v[36:39],v[32:35],v[148:151],0
v_lshlrev_b32 v104,16,v78
v_and_b32 v105,0xffff0000,v78
v_mfma_f32_16x16x32_bf16 v[40:43],v[148:151],v[148:151],0
v_lshlrev_b32 v106,16,v79
v_and_b32 v107,0xffff0000,v79
M 36,v[32:35],152
v_cvt_pk_bf16_f32 v160,v140,v141
v_cvt_pk_bf16_f32 v161,v142,v143
M 40,v[152:155],152
v_cvt_pk_bf16_f32 v162,v144,v145
v_cvt_pk_bf16_f32 v163,v146,v147
M 36,v[32:35],156
v_lshlrev_b32 v108,16,v80
v_and_b32 v109,0xffff0000,v80
M 40,v[156:159],156
v_lshlrev_b32 v110,16,v81
v_and_b32 v111,0xffff0000,v81
M 36,v[32:35],160
v_lshlrev_b32 v112,16,v82
v_and_b32 v113,0xffff0000,v82
M 40,v[160:163],160
v_lshlrev_b32 v114,16,v83
v_and_b32 v115,0xffff0000,v83
s_nop 2
v_add_f32_dpp v44,v36,v36 row_ror:8 row_mask:0xf bank_mask:0xf
s_nop 1
v_cndmask_b32 v45,v40,v41,s[70:71]
v_cndmask_b32 v46,v42,v43,s[70:71]
ds_write_b32 v2,v44 offset:0
v_cndmask_b32 v45,v45,v46,s[72:73]
ds_write_b32 v0,v45 offset:0
L 0
s_barrier
R 52,3,0
R 56,3,16
R 60,3,32
L 0
v_pk_add_f32 v[52:53],v[52:53],v[54:55]
v_pk_add_f32 v[56:57],v[56:57],v[58:59]
v_pk_add_f32 v[60:61],v[60:61],v[62:63]
v_add_f32 v52,v52,v53
v_pk_add_f32 v[56:57],v[56:57],v[60:61]
v_mul_f32 v52,s68,v52
v_add_f32 v53,v56,v57
v_mul_f32 v53,s68,v53
v_fma_f32 v53,-v52,v52,v53
v_max_f32 v53,0,v53
s_waitcnt vmcnt(1)
buffer_store_dword v13,v5,s[36:39],s63 offen offset:0 sc0 sc1
s_mov_b32 s63,0x7fff0000
s_waitcnt vmcnt(1)
v_cmp_eq_u32 vcc,s53,v14
s_nop 0
s_cmp_eq_u64 vcc,-1
s_cselect_b32 s78,s45,256
s_cmp_lt_u32 s78,256
s_cbranch_scc1 Lr_tb1_%=
buffer_load_dword v14,v6,s[36:39],s61 offen offset:0 sc1
v_add_f32 v53,s69,v53
v_rsq_f32 v48,v53
s_nop 0
v_mul_f32_e64 v50,-v52,v48
v_mov_b32 v49,v48
v_mov_b32 v51,v50
v_pk_fma_f32 v[52:53],v[116:117],v[48:49],v[50:51]
v_pk_fma_f32 v[54:55],v[118:119],v[48:49],v[50:51]
v_pk_fma_f32 v[56:57],v[120:121],v[48:49],v[50:51]
v_pk_fma_f32 v[58:59],v[122:123],v[48:49],v[50:51]
v_pk_fma_f32 v[116:117],v[52:53],v[84:85],v[116:117]
v_pk_fma_f32 v[118:119],v[54:55],v[86:87],v[118:119]
v_pk_fma_f32 v[120:121],v[56:57],v[88:89],v[120:121]
v_pk_fma_f32 v[122:123],v[58:59],v[90:91],v[122:123]
v_cvt_pk_bf16_f32 v148,v116,v117
v_cvt_pk_bf16_f32 v149,v118,v119
v_cvt_pk_bf16_f32 v150,v120,v121
v_cvt_pk_bf16_f32 v151,v122,v123
v_pk_fma_f32 v[52:53],v[124:125],v[48:49],v[50:51]
v_pk_fma_f32 v[54:55],v[126:127],v[48:49],v[50:51]
v_pk_fma_f32 v[56:57],v[128:129],v[48:49],v[50:51]
v_pk_fma_f32 v[58:59],v[130:131],v[48:49],v[50:51]
v_pk_fma_f32 v[124:125],v[52:53],v[92:93],v[124:125]
v_pk_fma_f32 v[126:127],v[54:55],v[94:95],v[126:127]
v_pk_fma_f32 v[128:129],v[56:57],v[96:97],v[128:129]
v_pk_fma_f32 v[130:131],v[58:59],v[98:99],v[130:131]
v_cvt_pk_bf16_f32 v152,v124,v125
v_cvt_pk_bf16_f32 v153,v126,v127
v_cvt_pk_bf16_f32 v154,v128,v129
v_cvt_pk_bf16_f32 v155,v130,v131
v_pk_fma_f32 v[52:53],v[132:133],v[48:49],v[50:51]
v_pk_fma_f32 v[54:55],v[134:135],v[48:49],v[50:51]
v_pk_fma_f32 v[56:57],v[136:137],v[48:49],v[50:51]
v_pk_fma_f32 v[58:59],v[138:139],v[48:49],v[50:51]
v_pk_fma_f32 v[132:133],v[52:53],v[100:101],v[132:133]
v_pk_fma_f32 v[134:135],v[54:55],v[102:103],v[134:135]
v_pk_fma_f32 v[136:137],v[56:57],v[104:105],v[136:137]
v_pk_fma_f32 v[138:139],v[58:59],v[106:107],v[138:139]
v_cvt_pk_bf16_f32 v156,v132,v133
v_cvt_pk_bf16_f32 v157,v134,v135
v_cvt_pk_bf16_f32 v158,v136,v137
v_cvt_pk_bf16_f32 v159,v138,v139
v_pk_fma_f32 v[52:53],v[140:141],v[48:49],v[50:51]
v_pk_fma_f32 v[54:55],v[142:143],v[48:49],v[50:51]
v_pk_fma_f32 v[56:57],v[144:145],v[48:49],v[50:51]
v_pk_fma_f32 v[58:59],v[146:147],v[48:49],v[50:51]
v_pk_fma_f32 v[140:141],v[52:53],v[108:109],v[140:141]
v_pk_fma_f32 v[142:143],v[54:55],v[110:111],v[142:143]
v_pk_fma_f32 v[144:145],v[56:57],v[112:113],v[144:145]
v_pk_fma_f32 v[146:147],v[58:59],v[114:115],v[146:147]
v_cvt_pk_bf16_f32 v160,v140,v141
v_cvt_pk_bf16_f32 v161,v142,v143
v_cvt_pk_bf16_f32 v162,v144,v145
v_cvt_pk_bf16_f32 v163,v146,v147
v_mfma_f32_16x16x32_bf16 v[36:39],v[32:35],v[148:151],0
buffer_store_dwordx4 v[148:151],v1,s[36:39],s57 offen offset:0 sc0 sc1
v_mfma_f32_16x16x32_bf16 v[40:43],v[148:151],v[148:151],0
M 36,v[32:35],152
buffer_store_dwordx4 v[152:155],v1,s[36:39],s57 offen offset:2048 sc0 sc1
M 40,v[152:155],152
M 36,v[32:35],156
buffer_store_dwordx4 v[156:159],v1,s[36:39],s58 offen offset:0 sc0 sc1
M 40,v[156:159],156
M 36,v[32:35],160
buffer_store_dwordx4 v[160:163],v1,s[36:39],s58 offen offset:2048 sc0 sc1
M 40,v[160:163],160
s_nop 5
v_add_f32_dpp v44,v36,v36 row_ror:8 row_mask:0xf bank_mask:0xf
s_nop 0
v_cndmask_b32 v45,v40,v41,s[70:71]
v_cndmask_b32 v46,v42,v43,s[70:71]
ds_write_b32 v2,v44 offset:1024
v_cndmask_b32 v45,v45,v46,s[72:73]
ds_write_b32 v0,v45 offset:1024
L 0
s_barrier
R 52,3,1024
R 56,3,1040
R 60,3,1056
L 0
v_pk_add_f32 v[52:53],v[52:53],v[54:55]
v_pk_add_f32 v[56:57],v[56:57],v[58:59]
v_pk_add_f32 v[60:61],v[60:61],v[62:63]
v_add_f32 v52,v52,v53
v_pk_add_f32 v[56:57],v[56:57],v[60:61]
v_mul_f32 v52,s68,v52
v_add_f32 v53,v56,v57
v_mul_f32 v53,s68,v53
v_fma_f32 v53,-v52,v52,v53
v_max_f32 v53,0,v53
v_add_f32 v54,s69,v53
v_fma_f32 v55,s69,v54,v53
v_rsq_f32 v65,v54
v_rsq_f32 v54,v55
v_mul_f32_e64 v64,-v52,v65
v_mul_f32 v63,v55,v54
v_mul_f32 v61,s76,v54
v_mul_f32 v55,v54,v54
v_mul_f32 v62,s77,v55
v_mul_f32 v62,v62,v61
v_cndmask_b32 v60,v52,v64,s[74:75]
v_cndmask_b32 v61,v61,v65,s[74:75]
buffer_store_dwordx4 v[60:63],v4,s[36:39],s59 offen offset:0 sc0 sc1
s_mov_b32 s63,s60
s_waitcnt vmcnt(5)
v_cmp_eq_u32 vcc,s53,v14
s_nop 0
s_cmp_eq_u64 vcc,-1
s_cselect_b32 s78,s45,256
s_cmp_lt_u32 s78,256
s_cbranch_scc1 Lr_tc1_%=
s_waitcnt vmcnt(0)
buffer_store_dword v13,v5,s[36:39],s63 offen offset:0 sc0 sc1
s_mov_b32 s63,0x7fff0000
s_cmp_ge_u32 s45,256
s_cbranch_scc1 Lr_done_%=
Lr_bp1_%=:
buffer_load_dword v14,v6,s[36:39],s61 offen offset:0 sc1
s_add_u32 s82,s82,1
s_waitcnt vmcnt(0)
v_cmp_eq_u32 vcc,s53,v14
s_nop 0
s_cmp_eq_u64 vcc,-1
s_cbranch_scc1 Lr_bq1_%=
s_sleep 2
s_cmp_lt_u32 s82,20000
s_cbranch_scc1 Lr_bp1_%=
Lr_bq1_%=:
s_add_u32 s78,s55,71303168
buffer_load_dwordx4 v[164:167],v1,s[36:39],s78 offen offset:0 sc1
buffer_load_dwordx4 v[168:171],v1,s[36:39],s78 offen offset:2048 sc1
s_add_u32 s78,s55,71307264
buffer_load_dwordx4 v[172:175],v1,s[36:39],s78 offen offset:0 sc1
buffer_load_dwordx4 v[176:179],v1,s[36:39],s78 offen offset:2048 sc1
s_add_u32 s78,s55,89128960
buffer_load_dwordx4 v[180:183],v1,s[36:39],s78 offen offset:0 sc1
buffer_load_dwordx4 v[184:187],v1,s[36:39],s78 offen offset:2048 sc1
s_add_u32 s78,s55,89133056
buffer_load_dwordx4 v[188:191],v1,s[36:39],s78 offen offset:0 sc1
buffer_load_dwordx4 v[192:195],v1,s[36:39],s78 offen offset:2048 sc1
s_add_u32 m0,s65,0
s_nop 0
D 9,56,0
D 9,56,1024
D 9,56,2048
D 9,56,3072
buffer_load_dword v14,v6,s[36:39],s62 offen offset:0 sc1
s_lshr_b32 s78,s45,1
s_and_b32 s79,s45,1
s_lshl_b32 s79,s79,7
s_lshl_b32 s57,s78,15
s_add_u32 s57,s57,s79
s_add_u32 s57,s57,12582912
s_add_u32 s58,s57,4096
s_lshl_b32 s59,s78,9
s_add_u32 s59,s59,s79
s_add_u32 s59,s59,107020288
s_lshl_b32 s60,s45,9
s_add_u32 s60,s60,s66
s_add_u32 s60,s60,131072
s_add_u32 s45,s45,s46
s_lshr_b32 s78,s45,1
s_and_b32 s79,s45,1
s_lshl_b32 s79,s79,7
s_lshl_b32 s55,s78,15
s_add_u32 s55,s55,s79
s_mul_i32 s56,s45,16384
s_add_u32 s56,s56,98566144
s_lshl_b32 s61,s78,11
s_add_u32 s61,s61,110587904
s_add_u32 s78,s45,s46
s_lshr_b32 s78,s78,1
s_lshl_b32 s62,s78,11
s_add_u32 s62,s62,110587904
s_waitcnt vmcnt(0)
s_branch Lr_it0_%=
Lr_ta1_%=:
v_mfma_f32_16x16x32_bf16 v[116:119],v[26:29],v[196:199],0
s_add_u32 s78,s55,71303168
buffer_load_dwordx4 v[164:167],v1,s[36:39],s78 offen offset:0 sc1
v_mfma_f32_16x16x32_bf16 v[120:123],v[24:27],v[196:199],0
L 6
buffer_load_dwordx4 v[168:171],v1,s[36:39],s78 offen offset:2048 sc1
M 116,v[26:29],212
s_add_u32 s78,s55,71307264
buffer_load_dwordx4 v[172:175],v1,s[36:39],s78 offen offset:0 sc1
M 120,v[24:27],212
L 4
buffer_load_dwordx4 v[176:179],v1,s[36:39],s78 offen offset:2048 sc1
v_mfma_f32_16x16x32_bf16 v[124:127],v[26:29],v[200:203],0
s_add_u32 s78,s55,89128960
buffer_load_dwordx4 v[180:183],v1,s[36:39],s78 offen offset:0 sc1
v_mfma_f32_16x16x32_bf16 v[128:131],v[24:27],v[200:203],0
L 2
buffer_load_dwordx4 v[184:187],v1,s[36:39],s78 offen offset:2048 sc1
M 124,v[26:29],216
L 0
v_cvt_pk_bf16_f32 v148,v116,v117
v_cvt_pk_bf16_f32 v149,v118,v119
M 128,v[24:27],216
s_add_u32 s78,s55,89133056
buffer_load_dwordx4 v[188:191],v1,s[36:39],s78 offen offset:0 sc1
v_mfma_f32_16x16x32_bf16 v[132:135],v[26:29],v[204:207],0
s_add_u32 m0,s65,0
buffer_load_dwordx4 v[192:195],v1,s[36:39],s78 offen offset:2048 sc1
v_mfma_f32_16x16x32_bf16 v[136:139],v[24:27],v[204:207],0
D 9,56,0
M 132,v[26:29],220
D 9,56,1024
M 136,v[24:27],220
D 9,56,2048
v_mfma_f32_16x16x32_bf16 v[140:143],v[26:29],v[208:211],0
D 9,56,3072
v_mfma_f32_16x16x32_bf16 v[144:147],v[24:27],v[208:211],0
v_cvt_pk_bf16_f32 v150,v120,v121
v_cvt_pk_bf16_f32 v151,v122,v123
M 140,v[26:29],224
v_cvt_pk_bf16_f32 v152,v124,v125
v_cvt_pk_bf16_f32 v153,v126,v127
M 144,v[24:27],224
v_cvt_pk_bf16_f32 v154,v128,v129
v_cvt_pk_bf16_f32 v155,v130,v131
v_mfma_f32_16x16x32_bf16 v[36:39],v[32:35],v[148:151],0
v_cvt_pk_bf16_f32 v156,v132,v133
v_cvt_pk_bf16_f32 v157,v134,v135
v_mfma_f32_16x16x32_bf16 v[40:43],v[148:151],v[148:151],0
v_cvt_pk_bf16_f32 v158,v136,v137
v_cvt_pk_bf16_f32 v159,v138,v139
M 36,v[32:35],152
v_cvt_pk_bf16_f32 v160,v140,v141
v_cvt_pk_bf16_f32 v161,v142,v143
M 40,v[152:155],152
v_cvt_pk_bf16_f32 v162,v144,v145
v_cvt_pk_bf16_f32 v163,v146,v147
M 36,v[32:35],156
v_lshlrev_b32 v84,16,v68
v_and_b32 v85,0xffff0000,v68
M 40,v[156:159],156
v_lshlrev_b32 v86,16,v69
v_and_b32 v87,0xffff0000,v69
M 36,v[32:35],160
v_lshlrev_b32 v88,16,v70
v_and_b32 v89,0xffff0000,v70
M 40,v[160:163],160
v_lshlrev_b32 v90,16,v71
v_and_b32 v91,0xffff0000,v71
v_lshlrev_b32 v92,16,v72
v_and_b32 v93,0xffff0000,v72
v_lshlrev_b32 v94,16,v73
v_and_b32 v95,0xffff0000,v73
v_lshlrev_b32 v96,16,v74
v_and_b32 v97,0xffff0000,v74
v_lshlrev_b32 v98,16,v75
v_and_b32 v99,0xffff0000,v75
v_lshlrev_b32 v100,16,v76
v_and_b32 v101,0xffff0000,v76
v_lshlrev_b32 v102,16,v77
v_and_b32 v103,0xffff0000,v77
v_lshlrev_b32 v104,16,v78
v_and_b32 v105,0xffff0000,v78
v_lshlrev_b32 v106,16,v79
v_and_b32 v107,0xffff0000,v79
v_lshlrev_b32 v108,16,v80
v_and_b32 v109,0xffff0000,v80
v_lshlrev_b32 v110,16,v81
v_and_b32 v111,0xffff0000,v81
v_lshlrev_b32 v112,16,v82
v_and_b32 v113,0xffff0000,v82
v_lshlrev_b32 v114,16,v83
v_and_b32 v115,0xffff0000,v83
v_add_f32_dpp v44,v36,v36 row_ror:8 row_mask:0xf bank_mask:0xf
v_cndmask_b32 v45,v40,v41,s[70:71]
v_cndmask_b32 v46,v42,v43,s[70:71]
ds_write_b32 v2,v44 offset:0
v_cndmask_b32 v45,v45,v46,s[72:73]
ds_write_b32 v0,v45 offset:0
L 0
s_barrier
R 52,3,0
R 56,3,16
R 60,3,32
L 0
v_pk_add_f32 v[52:53],v[52:53],v[54:55]
v_pk_add_f32 v[56:57],v[56:57],v[58:59]
v_pk_add_f32 v[60:61],v[60:61],v[62:63]
v_add_f32 v52,v52,v53
v_pk_add_f32 v[56:57],v[56:57],v[60:61]
v_mul_f32 v52,s68,v52
v_add_f32 v53,v56,v57
v_mul_f32 v53,s68,v53
v_fma_f32 v53,-v52,v52,v53
v_max_f32 v53,0,v53
buffer_load_dword v14,v6,s[36:39],s62 offen offset:0 sc1
v_add_f32 v53,s69,v53
v_rsq_f32 v48,v53
s_nop 0
v_mul_f32_e64 v50,-v52,v48
v_mov_b32 v49,v48
v_mov_b32 v51,v50
v_pk_fma_f32 v[52:53],v[116:117],v[48:49],v[50:51]
v_pk_fma_f32 v[54:55],v[118:119],v[48:49],v[50:51]
v_pk_fma_f32 v[56:57],v[120:121],v[48:49],v[50:51]
v_pk_fma_f32 v[58:59],v[122:123],v[48:49],v[50:51]
v_pk_fma_f32 v[116:117],v[52:53],v[84:85],v[116:117]
v_pk_fma_f32 v[118:119],v[54:55],v[86:87],v[118:119]
v_pk_fma_f32 v[120:121],v[56:57],v[88:89],v[120:121]
v_pk_fma_f32 v[122:123],v[58:59],v[90:91],v[122:123]
v_cvt_pk_bf16_f32 v148,v116,v117
v_cvt_pk_bf16_f32 v149,v118,v119
v_cvt_pk_bf16_f32 v150,v120,v121
v_cvt_pk_bf16_f32 v151,v122,v123
v_pk_fma_f32 v[52:53],v[124:125],v[48:49],v[50:51]
v_pk_fma_f32 v[54:55],v[126:127],v[48:49],v[50:51]
v_pk_fma_f32 v[56:57],v[128:129],v[48:49],v[50:51]
v_pk_fma_f32 v[58:59],v[130:131],v[48:49],v[50:51]
v_pk_fma_f32 v[124:125],v[52:53],v[92:93],v[124:125]
v_pk_fma_f32 v[126:127],v[54:55],v[94:95],v[126:127]
v_pk_fma_f32 v[128:129],v[56:57],v[96:97],v[128:129]
v_pk_fma_f32 v[130:131],v[58:59],v[98:99],v[130:131]
v_cvt_pk_bf16_f32 v152,v124,v125
v_cvt_pk_bf16_f32 v153,v126,v127
v_cvt_pk_bf16_f32 v154,v128,v129
v_cvt_pk_bf16_f32 v155,v130,v131
v_pk_fma_f32 v[52:53],v[132:133],v[48:49],v[50:51]
v_pk_fma_f32 v[54:55],v[134:135],v[48:49],v[50:51]
v_pk_fma_f32 v[56:57],v[136:137],v[48:49],v[50:51]
v_pk_fma_f32 v[58:59],v[138:139],v[48:49],v[50:51]
v_pk_fma_f32 v[132:133],v[52:53],v[100:101],v[132:133]
v_pk_fma_f32 v[134:135],v[54:55],v[102:103],v[134:135]
v_pk_fma_f32 v[136:137],v[56:57],v[104:105],v[136:137]
v_pk_fma_f32 v[138:139],v[58:59],v[106:107],v[138:139]
v_cvt_pk_bf16_f32 v156,v132,v133
v_cvt_pk_bf16_f32 v157,v134,v135
v_cvt_pk_bf16_f32 v158,v136,v137
v_cvt_pk_bf16_f32 v159,v138,v139
v_pk_fma_f32 v[52:53],v[140:141],v[48:49],v[50:51]
v_pk_fma_f32 v[54:55],v[142:143],v[48:49],v[50:51]
v_pk_fma_f32 v[56:57],v[144:145],v[48:49],v[50:51]
v_pk_fma_f32 v[58:59],v[146:147],v[48:49],v[50:51]
v_pk_fma_f32 v[140:141],v[52:53],v[108:109],v[140:141]
v_pk_fma_f32 v[142:143],v[54:55],v[110:111],v[142:143]
v_pk_fma_f32 v[144:145],v[56:57],v[112:113],v[144:145]
v_pk_fma_f32 v[146:147],v[58:59],v[114:115],v[146:147]
v_cvt_pk_bf16_f32 v160,v140,v141
v_cvt_pk_bf16_f32 v161,v142,v143
v_cvt_pk_bf16_f32 v162,v144,v145
v_cvt_pk_bf16_f32 v163,v146,v147
s_waitcnt vmcnt(13)
buffer_store_dword v13,v5,s[36:39],s63 offen offset:0 sc0 sc1
s_mov_b32 s63,0x7fff0000
v_mfma_f32_16x16x32_bf16 v[36:39],v[32:35],v[148:151],0
buffer_store_dwordx4 v[148:151],v1,s[36:39],s57 offen offset:0 sc0 sc1
v_mfma_f32_16x16x32_bf16 v[40:43],v[148:151],v[148:151],0
M 36,v[32:35],152
buffer_store_dwordx4 v[152:155],v1,s[36:39],s57 offen offset:2048 sc0 sc1
M 40,v[152:155],152
M 36,v[32:35],156
buffer_store_dwordx4 v[156:159],v1,s[36:39],s58 offen offset:0 sc0 sc1
M 40,v[156:159],156
M 36,v[32:35],160
buffer_store_dwordx4 v[160:163],v1,s[36:39],s58 offen offset:2048 sc0 sc1
M 40,v[160:163],160
s_nop 5
v_add_f32_dpp v44,v36,v36 row_ror:8 row_mask:0xf bank_mask:0xf
s_nop 0
v_cndmask_b32 v45,v40,v41,s[70:71]
v_cndmask_b32 v46,v42,v43,s[70:71]
ds_write_b32 v2,v44 offset:1024
v_cndmask_b32 v45,v45,v46,s[72:73]
ds_write_b32 v0,v45 offset:1024
L 0
s_barrier
R 52,3,1024
R 56,3,1040
R 60,3,1056
L 0
v_pk_add_f32 v[52:53],v[52:53],v[54:55]
v_pk_add_f32 v[56:57],v[56:57],v[58:59]
v_pk_add_f32 v[60:61],v[60:61],v[62:63]
v_add_f32 v52,v52,v53
v_pk_add_f32 v[56:57],v[56:57],v[60:61]
v_mul_f32 v52,s68,v52
v_add_f32 v53,v56,v57
v_mul_f32 v53,s68,v53
v_fma_f32 v53,-v52,v52,v53
v_max_f32 v53,0,v53
v_add_f32 v54,s69,v53
v_fma_f32 v55,s69,v54,v53
v_rsq_f32 v65,v54
v_rsq_f32 v54,v55
v_mul_f32_e64 v64,-v52,v65
v_mul_f32 v63,v55,v54
v_mul_f32 v61,s76,v54
v_mul_f32 v55,v54,v54
v_mul_f32 v62,s77,v55
v_mul_f32 v62,v62,v61
v_cndmask_b32 v60,v52,v64,s[74:75]
v_cndmask_b32 v61,v61,v65,s[74:75]
buffer_store_dwordx4 v[60:63],v4,s[36:39],s59 offen offset:0 sc0 sc1
s_mov_b32 s63,s60
s_lshr_b32 s78,s45,1
s_and_b32 s79,s45,1
s_lshl_b32 s79,s79,7
s_lshl_b32 s57,s78,15
s_add_u32 s57,s57,s79
s_add_u32 s57,s57,12582912
s_add_u32 s58,s57,4096
s_lshl_b32 s59,s78,9
s_add_u32 s59,s59,s79
s_add_u32 s59,s59,107020288
s_lshl_b32 s60,s45,9
s_add_u32 s60,s60,s66
s_add_u32 s60,s60,131072
s_add_u32 s45,s45,s46
s_lshr_b32 s78,s45,1
s_and_b32 s79,s45,1
s_lshl_b32 s79,s79,7
s_lshl_b32 s55,s78,15
s_add_u32 s55,s55,s79
s_mul_i32 s56,s45,16384
)"
R"(s_add_u32 s56,s56,98566144
s_lshl_b32 s61,s78,11
s_add_u32 s61,s61,110587904
s_add_u32 s78,s45,s46
s_lshr_b32 s78,s78,1
s_lshl_b32 s62,s78,11
s_add_u32 s62,s62,110587904
s_waitcnt vmcnt(6)
s_branch Lr_it0_%=
Lr_tb1_%=:
v_add_f32 v53,s69,v53
v_rsq_f32 v48,v53
s_nop 0
v_mul_f32_e64 v50,-v52,v48
v_mov_b32 v49,v48
v_mov_b32 v51,v50
v_pk_fma_f32 v[52:53],v[116:117],v[48:49],v[50:51]
v_pk_fma_f32 v[54:55],v[118:119],v[48:49],v[50:51]
v_pk_fma_f32 v[56:57],v[120:121],v[48:49],v[50:51]
v_pk_fma_f32 v[58:59],v[122:123],v[48:49],v[50:51]
s_add_u32 s78,s55,71303168
buffer_load_dwordx4 v[164:167],v1,s[36:39],s78 offen offset:0 sc1
buffer_load_dwordx4 v[168:171],v1,s[36:39],s78 offen offset:2048 sc1
s_add_u32 s78,s55,71307264
buffer_load_dwordx4 v[172:175],v1,s[36:39],s78 offen offset:0 sc1
buffer_load_dwordx4 v[176:179],v1,s[36:39],s78 offen offset:2048 sc1
v_pk_fma_f32 v[116:117],v[52:53],v[84:85],v[116:117]
v_pk_fma_f32 v[118:119],v[54:55],v[86:87],v[118:119]
v_pk_fma_f32 v[120:121],v[56:57],v[88:89],v[120:121]
v_pk_fma_f32 v[122:123],v[58:59],v[90:91],v[122:123]
v_cvt_pk_bf16_f32 v148,v116,v117
v_cvt_pk_bf16_f32 v149,v118,v119
v_cvt_pk_bf16_f32 v150,v120,v121
v_cvt_pk_bf16_f32 v151,v122,v123
v_pk_fma_f32 v[52:53],v[124:125],v[48:49],v[50:51]
v_pk_fma_f32 v[54:55],v[126:127],v[48:49],v[50:51]
v_pk_fma_f32 v[56:57],v[128:129],v[48:49],v[50:51]
v_pk_fma_f32 v[58:59],v[130:131],v[48:49],v[50:51]
s_add_u32 s78,s55,89128960
buffer_load_dwordx4 v[180:183],v1,s[36:39],s78 offen offset:0 sc1
buffer_load_dwordx4 v[184:187],v1,s[36:39],s78 offen offset:2048 sc1
s_add_u32 s78,s55,89133056
buffer_load_dwordx4 v[188:191],v1,s[36:39],s78 offen offset:0 sc1
buffer_load_dwordx4 v[192:195],v1,s[36:39],s78 offen offset:2048 sc1
v_pk_fma_f32 v[124:125],v[52:53],v[92:93],v[124:125]
v_pk_fma_f32 v[126:127],v[54:55],v[94:95],v[126:127]
v_pk_fma_f32 v[128:129],v[56:57],v[96:97],v[128:129]
v_pk_fma_f32 v[130:131],v[58:59],v[98:99],v[130:131]
v_cvt_pk_bf16_f32 v152,v124,v125
v_cvt_pk_bf16_f32 v153,v126,v127
v_cvt_pk_bf16_f32 v154,v128,v129
v_cvt_pk_bf16_f32 v155,v130,v131
v_pk_fma_f32 v[52:53],v[132:133],v[48:49],v[50:51]
v_pk_fma_f32 v[54:55],v[134:135],v[48:49],v[50:51]
v_pk_fma_f32 v[56:57],v[136:137],v[48:49],v[50:51]
v_pk_fma_f32 v[58:59],v[138:139],v[48:49],v[50:51]
s_add_u32 m0,s65,0
s_nop 0
D 9,56,0
D 9,56,1024
D 9,56,2048
D 9,56,3072
v_pk_fma_f32 v[132:133],v[52:53],v[100:101],v[132:133]
v_pk_fma_f32 v[134:135],v[54:55],v[102:103],v[134:135]
v_pk_fma_f32 v[136:137],v[56:57],v[104:105],v[136:137]
v_pk_fma_f32 v[138:139],v[58:59],v[106:107],v[138:139]
v_cvt_pk_bf16_f32 v156,v132,v133
v_cvt_pk_bf16_f32 v157,v134,v135
v_cvt_pk_bf16_f32 v158,v136,v137
v_cvt_pk_bf16_f32 v159,v138,v139
v_pk_fma_f32 v[52:53],v[140:141],v[48:49],v[50:51]
v_pk_fma_f32 v[54:55],v[142:143],v[48:49],v[50:51]
v_pk_fma_f32 v[56:57],v[144:145],v[48:49],v[50:51]
v_pk_fma_f32 v[58:59],v[146:147],v[48:49],v[50:51]
v_pk_fma_f32 v[140:141],v[52:53],v[108:109],v[140:141]
v_pk_fma_f32 v[142:143],v[54:55],v[110:111],v[142:143]
v_pk_fma_f32 v[144:145],v[56:57],v[112:113],v[144:145]
v_pk_fma_f32 v[146:147],v[58:59],v[114:115],v[146:147]
v_cvt_pk_bf16_f32 v160,v140,v141
v_cvt_pk_bf16_f32 v161,v142,v143
v_cvt_pk_bf16_f32 v162,v144,v145
v_cvt_pk_bf16_f32 v163,v146,v147
v_mfma_f32_16x16x32_bf16 v[36:39],v[32:35],v[148:151],0
buffer_load_dword v14,v6,s[36:39],s62 offen offset:0 sc1
buffer_store_dwordx4 v[148:151],v1,s[36:39],s57 offen offset:0 sc0 sc1
v_mfma_f32_16x16x32_bf16 v[40:43],v[148:151],v[148:151],0
M 36,v[32:35],152
buffer_store_dwordx4 v[152:155],v1,s[36:39],s57 offen offset:2048 sc0 sc1
M 40,v[152:155],152
M 36,v[32:35],156
buffer_store_dwordx4 v[156:159],v1,s[36:39],s58 offen offset:0 sc0 sc1
M 40,v[156:159],156
M 36,v[32:35],160
buffer_store_dwordx4 v[160:163],v1,s[36:39],s58 offen offset:2048 sc0 sc1
M 40,v[160:163],160
s_nop 5
v_add_f32_dpp v44,v36,v36 row_ror:8 row_mask:0xf bank_mask:0xf
s_nop 0
v_cndmask_b32 v45,v40,v41,s[70:71]
v_cndmask_b32 v46,v42,v43,s[70:71]
ds_write_b32 v2,v44 offset:1024
v_cndmask_b32 v45,v45,v46,s[72:73]
ds_write_b32 v0,v45 offset:1024
L 0
s_barrier
R 52,3,1024
R 56,3,1040
R 60,3,1056
L 0
v_pk_add_f32 v[52:53],v[52:53],v[54:55]
v_pk_add_f32 v[56:57],v[56:57],v[58:59]
v_pk_add_f32 v[60:61],v[60:61],v[62:63]
v_add_f32 v52,v52,v53
v_pk_add_f32 v[56:57],v[56:57],v[60:61]
v_mul_f32 v52,s68,v52
v_add_f32 v53,v56,v57
v_mul_f32 v53,s68,v53
v_fma_f32 v53,-v52,v52,v53
v_max_f32 v53,0,v53
v_add_f32 v54,s69,v53
v_fma_f32 v55,s69,v54,v53
v_rsq_f32 v65,v54
v_rsq_f32 v54,v55
v_mul_f32_e64 v64,-v52,v65
v_mul_f32 v63,v55,v54
v_mul_f32 v61,s76,v54
v_mul_f32 v55,v54,v54
v_mul_f32 v62,s77,v55
v_mul_f32 v62,v62,v61
v_cndmask_b32 v60,v52,v64,s[74:75]
v_cndmask_b32 v61,v61,v65,s[74:75]
buffer_store_dwordx4 v[60:63],v4,s[36:39],s59 offen offset:0 sc0 sc1
s_mov_b32 s63,s60
s_lshr_b32 s78,s45,1
s_and_b32 s79,s45,1
s_lshl_b32 s79,s79,7
s_lshl_b32 s57,s78,15
s_add_u32 s57,s57,s79
s_add_u32 s57,s57,12582912
s_add_u32 s58,s57,4096
s_lshl_b32 s59,s78,9
s_add_u32 s59,s59,s79
s_add_u32 s59,s59,107020288
s_lshl_b32 s60,s45,9
s_add_u32 s60,s60,s66
s_add_u32 s60,s60,131072
s_add_u32 s45,s45,s46
s_lshr_b32 s78,s45,1
s_and_b32 s79,s45,1
s_lshl_b32 s79,s79,7
s_lshl_b32 s55,s78,15
s_add_u32 s55,s55,s79
s_mul_i32 s56,s45,16384
s_add_u32 s56,s56,98566144
s_lshl_b32 s61,s78,11
s_add_u32 s61,s61,110587904
s_add_u32 s78,s45,s46
s_lshr_b32 s78,s78,1
s_lshl_b32 s62,s78,11
s_add_u32 s62,s62,110587904
s_waitcnt vmcnt(5)
s_branch Lr_it0_%=
Lr_tc1_%=:
s_add_u32 s78,s55,71303168
buffer_load_dwordx4 v[164:167],v1,s[36:39],s78 offen offset:0 sc1
buffer_load_dwordx4 v[168:171],v1,s[36:39],s78 offen offset:2048 sc1
s_add_u32 s78,s55,71307264
buffer_load_dwordx4 v[172:175],v1,s[36:39],s78 offen offset:0 sc1
buffer_load_dwordx4 v[176:179],v1,s[36:39],s78 offen offset:2048 sc1
s_add_u32 s78,s55,89128960
buffer_load_dwordx4 v[180:183],v1,s[36:39],s78 offen offset:0 sc1
buffer_load_dwordx4 v[184:187],v1,s[36:39],s78 offen offset:2048 sc1
s_add_u32 s78,s55,89133056
buffer_load_dwordx4 v[188:191],v1,s[36:39],s78 offen offset:0 sc1
buffer_load_dwordx4 v[192:195],v1,s[36:39],s78 offen offset:2048 sc1
s_add_u32 m0,s65,0
s_nop 0
D 9,56,0
D 9,56,1024
D 9,56,2048
D 9,56,3072
buffer_load_dword v14,v6,s[36:39],s62 offen offset:0 sc1
s_waitcnt vmcnt(13)
buffer_store_dword v13,v5,s[36:39],s63 offen offset:0 sc0 sc1
s_mov_b32 s63,0x7fff0000
s_lshr_b32 s78,s45,1
s_and_b32 s79,s45,1
s_lshl_b32 s79,s79,7
s_lshl_b32 s57,s78,15
s_add_u32 s57,s57,s79
s_add_u32 s57,s57,12582912
s_add_u32 s58,s57,4096
s_lshl_b32 s59,s78,9
s_add_u32 s59,s59,s79
s_add_u32 s59,s59,107020288
s_lshl_b32 s60,s45,9
s_add_u32 s60,s60,s66
s_add_u32 s60,s60,131072
s_add_u32 s45,s45,s46
s_lshr_b32 s78,s45,1
s_and_b32 s79,s45,1
s_lshl_b32 s79,s79,7
s_lshl_b32 s55,s78,15
s_add_u32 s55,s55,s79
s_mul_i32 s56,s45,16384
s_add_u32 s56,s56,98566144
s_lshl_b32 s61,s78,11
s_add_u32 s61,s61,110587904
s_add_u32 s78,s45,s46
s_lshr_b32 s78,s78,1
s_lshl_b32 s62,s78,11
s_add_u32 s62,s62,110587904
s_waitcnt vmcnt(1)
s_branch Lr_it0_%=
Lr_done_%=:
s_waitcnt vmcnt(0) lgkmcnt(0)
s_barrier
Lcode_r_e:
.purgem M
.purgem R
.purgem T
.purgem D
.purgem L
)"
  :: "s"(ws), "s"(sWave), "s"(sG0), "s"(sStep), "s"(sOutOff), "s"(sAuxOff), "s"(sBuf), "s"(sRk), "s"(sExp), "v"(vRP), "v"(vRF), "v"(vRwr), "v"(vRrd), "v"(vRS), "v"(vFlag), "v"(vL4), "v"(vLY), "v"(vQ), "v"(vY0), "v"(vTr), "v"(vSa), "v"(vSb), "s"(q) : "v0", "v1", "v2", "v3", "v4", "v5", "v6", "v7", "v8", "v9", "v10", "v11", "v12", "v13", "v14", "v15", "v16", "v17", "v18", "v19", "v20", "v21", "v22", "v23", "v24", "v25", "v26", "v27", "v28", "v29", "v30", "v31", "v32", "v33", "v34", "v35", "v36", "v37", "v38", "v39", "v40", "v41", "v42", "v43", "v44", "v45", "v46", "v47", "v48", "v49", "v50", "v51", "v52", "v53", "v54", "v55", "v56", "v57", "v58", "v59", "v60", "v61", "v62", "v63", "v64", "v65", "v66", "v67", "v68", "v69", "v70", "v71", "v72", "v73", "v74", "v75", "v76", "v77", "v78", "v79", "v80", "v81", "v82", "v83", "v84", "v85", "v86", "v87", "v88", "v89", "v90", "v91", "v92", "v93", "v94", "v95", "v96", "v97", "v98", "v99", "v100", "v101", "v102", "v103", "v104", "v105", "v106", "v107", "v108", "v109", "v110", "v111", "v112", "v113", "v114", "v115", "v116", "v117", "v118", "v119", "v120", "v121", "v122", "v123", "v124", "v125", "v126", "v127", "v128", "v129", "v130", "v131", "v132", "v133", "v134", "v135", "v136", "v137", "v138", "v139", "v140", "v141", "v142", "v143", "v144", "v145", "v146", "v147", "v148", "v149", "v150", "v151", "v152", "v153", "v154", "v155", "v156", "v157", "v158", "v159", "v160", "v161", "v162", "v163", "v164", "v165", "v166", "v167", "v168", "v169", "v170", "v171", "v172", "v173", "v174", "v175", "v176", "v177", "v178", "v179", "v180", "v181", "v182", "v183", "v184", "v185", "v186", "v187", "v188", "v189", "v190", "v191", "v192", "v193", "v194", "v195", "v196", "v197", "v198", "v199", "v200", "v201", "v202", "v203", "v204", "v205", "v206", "v207", "v208", "v209", "v210", "v211", "v212", "v213", "v214", "v215", "v216", "v217", "v218", "v219", "v220", "v221", "v222", "v223", "v224", "v225", "v226", "v227", "v228", "v229", "v230", "v231", "s36", "s37", "s38", "s39", "s40", "s41", "s42", "s43", "s44", "s45", "s46", "s47", "s48", "s49", "s50", "s51", "s52", "s53", "s54", "s55", "s56", "s57", "s58", "s59", "s60", "s61", "s62", "s63", "s64", "s65", "s66", "s67", "s68", "s69", "s70", "s71", "s72", "s73", "s74", "s75", "s76", "s77", "s78", "s79", "s80", "s81", "s82", "s83", "s84", "s85", "s86", "s87", "s88", "s89", "s90", "s91", "s92", "s93", "s94", "s95", "s96", "s97", "vcc", "scc", "memory");
  }
  CTS(9);
#define COMMON(NF, FOFF, FSTR, SNAP, LDW, NL, TOFF)                                                                                            \
  LANE_ID(ln);                                                                                                                       \
  const int l15 = ln & 15, lg = ln >> 4, lq = l15 >> 2, lp = ln & 3, rr = 8 * lg + lq, fj = ln & 31;                                 \
  const unsigned vL16 = ln * 16, vFlag = (ln < 32 && wave == 0) ? ln * 4 : OOB;                                                      \
  const unsigned vSnapOff = (wave == 0 && fj < (NF)) ? (FOFF) + (ln >> 5) * sStep * (FSTR) : OOB;                                    \
  const unsigned vSnapRd = (SNAP) + (fj < (NF) ? ln : ln & 32) * 4;                                                                  \
  const unsigned vWd0 = (ln >> 3) * (LDW) + ((ln & 7) ^ swzB(ln >> 3)) * 16, vWd1 = ((ln >> 3) + 8) * (LDW) + ((ln & 7) ^ swzB(8 + (ln >> 3))) * 16; \
  const unsigned vWr0 = (wave >> 1) * 8192 + rr * 128 + (((wave & 1) * 4 + lp) ^ swzB(rr)) * 16, vWr1 = vWr0 + 8;                    \
  const unsigned vWr2 = 16384 + rr * 128 + ((2 * wave + (lp >> 1)) ^ swzB(rr)) * 16 + (lp & 1) * 8;                                  \
  const unsigned sWave = wave, sRk = t_start, sG0 = rep, sDbg = sDbg0
  if (kind == 3 || kind == 5) {
    const unsigned sStep = kind == 3 ? 5 : G3OR;
    COMMON(8, fj * 128, 1024, 131072, 7168, 256, 0);
    const unsigned vOutS = (lg & 1) ? OOB : (((wave & 1) * 2 + (lg >> 1)) * 16 + l15) * 16 + (4 + (wave >> 1) - wave) * 1024;
    const unsigned vAux0 = (ln & 7) * 128 + (4 * wave + lg) * 8 + ((ln >> 3) & 1) * 4, vAux1 = 139264 + (l15 == 0 ? (4 * wave + lg) * 16 : 512 + ln * 16);
    const unsigned vAux2 = 139264 + l15 * 16, vAux3 = 0, vAn = 0, vAe = 0;
    const unsigned sOutOff = OFF_G + (slice * 6 + wave) * 1024, sFoutOff = OFF_FG + slice * 128, sAuxOff = 0, sWw = 16 * wave * 7168 + slice * 384, sBuf = 0;
    if (kind == 3) { asm volatile(
R"(.macro M d,w,x
v_mfma_f32_16x16x32_bf16 v[\d:\d+3], \w, v[\x:\x+3], v[\d:\d+3]
.endm
.macro R d,a,o
ds_read_b128 v[\d:\d+3], v\a offset:\o
.endm
.macro T d,a,o
ds_read_b64_tr_b16 \d, v\a offset:\o
.endm
.macro D a,s,o
buffer_load_dwordx4 v\a, s[36:39], s\s offen offset:\o sc1 lds
.endm
.macro L c
s_waitcnt lgkmcnt(\c)
.endm
Lcode_g3_b:
s_mov_b64 s[36:37],%0
s_mov_b32 s38,111243392
s_mov_b32 s39,0x00020000
s_mov_b32 s44,%1
s_mov_b32 s45,%2
s_mov_b32 s46,%3
s_mov_b32 s47,%4
s_mov_b32 s48,%5
s_mov_b32 s49,%6
s_mov_b32 s50,%7
s_mov_b32 s51,%8
s_mov_b32 s93,%9
v_mov_b32 v0,%10
v_mov_b32 v3,%11
v_mov_b32 v5,%12
v_mov_b32 v6,%13
v_mov_b32 v18,%14
v_mov_b32 v19,%15
v_mov_b32 v20,%16
v_mov_b32 v21,%17
v_mov_b32 v22,%18
v_mov_b32 v10,%19
v_mov_b32 v11,%20
v_mov_b32 v12,%21
v_mov_b32 v13,%22
v_mov_b32 v14,%23
v_mov_b32 v15,%24
v_mov_b32 v16,%25
s_mov_b64 s[40:41],%26
s_mov_b32 s42,7340032
s_mov_b32 s43,0x00020000
s_lshl_b32 s57,s44,11
s_mov_b32 s79,111243264
buffer_load_dword v4,off,s[36:39],s79 sc1
s_add_u32 s55,s50,0
s_add_u32 m0,s57,0
s_nop 0
buffer_load_dwordx4 v18,s[40:43],s55 offen offset:0  lds
s_add_u32 m0,s57,1024
s_nop 0
buffer_load_dwordx4 v19,s[40:43],s55 offen offset:0  lds
s_add_u32 m0,s57,8064
s_nop 0
buffer_load_dwordx4 v18,s[40:43],s55 offen offset:128  lds
s_add_u32 m0,s57,9088
s_nop 0
buffer_load_dwordx4 v19,s[40:43],s55 offen offset:128  lds
s_add_u32 m0,s57,16128
s_nop 0
buffer_load_dwordx4 v18,s[40:43],s55 offen offset:256  lds
s_add_u32 m0,s57,17152
s_nop 0
buffer_load_dwordx4 v19,s[40:43],s55 offen offset:256  lds
s_add_u32 s55,s50,458752
s_add_u32 m0,s57,24576
s_nop 0
buffer_load_dwordx4 v18,s[40:43],s55 offen offset:0  lds
s_add_u32 m0,s57,25600
s_nop 0
buffer_load_dwordx4 v19,s[40:43],s55 offen offset:0  lds
s_add_u32 m0,s57,32640
s_nop 0
buffer_load_dwordx4 v18,s[40:43],s55 offen offset:128  lds
s_add_u32 m0,s57,33664
s_nop 0
buffer_load_dwordx4 v19,s[40:43],s55 offen offset:128  lds
s_add_u32 m0,s57,40704
s_nop 0
buffer_load_dwordx4 v18,s[40:43],s55 offen offset:256  lds
s_add_u32 m0,s57,41728
s_nop 0
buffer_load_dwordx4 v19,s[40:43],s55 offen offset:256  lds
s_add_u32 s55,s50,917504
s_add_u32 m0,s57,49152
s_nop 0
buffer_load_dwordx4 v18,s[40:43],s55 offen offset:0  lds
s_add_u32 m0,s57,50176
s_nop 0
buffer_load_dwordx4 v19,s[40:43],s55 offen offset:0  lds
s_add_u32 m0,s57,57216
s_nop 0
buffer_load_dwordx4 v18,s[40:43],s55 offen offset:128  lds
s_add_u32 m0,s57,58240
s_nop 0
buffer_load_dwordx4 v19,s[40:43],s55 offen offset:128  lds
s_add_u32 m0,s57,65280
s_nop 0
buffer_load_dwordx4 v18,s[40:43],s55 offen offset:256  lds
s_add_u32 m0,s57,66304
s_nop 0
buffer_load_dwordx4 v19,s[40:43],s55 offen offset:256  lds
s_add_u32 s55,s50,1376256
s_add_u32 m0,s57,73728
s_nop 0
buffer_load_dwordx4 v18,s[40:43],s55 offen offset:0  lds
s_add_u32 m0,s57,74752
s_nop 0
buffer_load_dwordx4 v19,s[40:43],s55 offen offset:0  lds
s_add_u32 m0,s57,81792
s_nop 0
buffer_load_dwordx4 v18,s[40:43],s55 offen offset:128  lds
s_add_u32 m0,s57,82816
s_nop 0
buffer_load_dwordx4 v19,s[40:43],s55 offen offset:128  lds
s_add_u32 m0,s57,89856
s_nop 0
buffer_load_dwordx4 v18,s[40:43],s55 offen offset:256  lds
s_add_u32 m0,s57,90880
s_nop 0
buffer_load_dwordx4 v19,s[40:43],s55 offen offset:256  lds
s_add_u32 s55,s50,1835008
s_add_u32 m0,s57,98304
s_nop 0
buffer_load_dwordx4 v18,s[40:43],s55 offen offset:0  lds
s_add_u32 m0,s57,99328
s_nop 0
buffer_load_dwordx4 v19,s[40:43],s55 offen offset:0  lds
s_add_u32 m0,s57,106368
s_nop 0
buffer_load_dwordx4 v18,s[40:43],s55 offen offset:128  lds
s_add_u32 m0,s57,107392
s_nop 0
buffer_load_dwordx4 v19,s[40:43],s55 offen offset:128  lds
s_add_u32 m0,s57,114432
s_nop 0
buffer_load_dwordx4 v18,s[40:43],s55 offen offset:256  lds
s_add_u32 m0,s57,115456
s_nop 0
buffer_load_dwordx4 v19,s[40:43],s55 offen offset:256  lds
s_waitcnt vmcnt(30)
s_barrier
v_readfirstlane_b32 s53,v4
s_nop 3
s_add_u32 s53,s53,1
s_cmp_eq_u32 s53,0
s_cselect_b32 s53,1,s53
v_mov_b32 v4,s53
s_waitcnt vmcnt(24) lgkmcnt(0)
s_barrier
s_add_u32 s55,s50,2293760
s_add_u32 m0,s57,122880
s_nop 0
buffer_load_dwordx4 v18,s[40:43],s55 offen offset:0  lds
s_add_u32 m0,s57,123904
s_nop 0
buffer_load_dwordx4 v19,s[40:43],s55 offen offset:0  lds
s_add_u32 m0,s57,130944
s_nop 0
buffer_load_dwordx4 v18,s[40:43],s55 offen offset:128  lds
s_add_u32 m0,s57,131968
s_nop 0
buffer_load_dwordx4 v19,s[40:43],s55 offen offset:128  lds
s_add_u32 m0,s57,139008
s_nop 0
buffer_load_dwordx4 v18,s[40:43],s55 offen offset:256  lds
s_add_u32 m0,s57,140032
s_nop 0
buffer_load_dwordx4 v19,s[40:43],s55 offen offset:256  lds
v_add_u32 v26,0,v20
v_add_u32 v27,0,v21
v_add_u32 v28,0,v22
T a[0:1],26,0
T a[2:3],26,512
T a[4:5],27,0
T a[6:7],27,512
T a[8:9],28,0
T a[10:11],28,512
T a[12:13],26,4096
T a[14:15],26,4608
T a[16:17],27,4096
T a[18:19],27,4608
T a[20:21],28,4096
T a[22:23],28,4608
s_waitcnt vmcnt(24) lgkmcnt(0)
s_barrier
s_add_u32 s55,s50,2752512
s_add_u32 m0,s57,0
s_nop 0
buffer_load_dwordx4 v18,s[40:43],s55 offen offset:0  lds
s_add_u32 m0,s57,1024
s_nop 0
buffer_load_dwordx4 v19,s[40:43],s55 offen offset:0  lds
s_add_u32 m0,s57,8064
s_nop 0
buffer_load_dwordx4 v18,s[40:43],s55 offen offset:128  lds
s_add_u32 m0,s57,9088
s_nop 0
buffer_load_dwordx4 v19,s[40:43],s55 offen offset:128  lds
s_add_u32 m0,s57,16128
s_nop 0
buffer_load_dwordx4 v18,s[40:43],s55 offen offset:256  lds
s_add_u32 m0,s57,17152
s_nop 0
buffer_load_dwordx4 v19,s[40:43],s55 offen offset:256  lds
v_add_u32 v26,24576,v20
v_add_u32 v27,24576,v21
v_add_u32 v28,24576,v22
T a[24:25],26,0
T a[26:27],26,512
T a[28:29],27,0
T a[30:31],27,512
T a[32:33],28,0
T a[34:35],28,512
T a[36:37],26,4096
T a[38:39],26,4608
T a[40:41],27,4096
T a[42:43],27,4608
T a[44:45],28,4096
T a[46:47],28,4608
s_waitcnt vmcnt(24) lgkmcnt(0)
s_barrier
s_add_u32 s55,s50,3211264
s_add_u32 m0,s57,24576
s_nop 0
buffer_load_dwordx4 v18,s[40:43],s55 offen offset:0  lds
s_add_u32 m0,s57,25600
s_nop 0
buffer_load_dwordx4 v19,s[40:43],s55 offen offset:0  lds
s_add_u32 m0,s57,32640
s_nop 0
buffer_load_dwordx4 v18,s[40:43],s55 offen offset:128  lds
s_add_u32 m0,s57,33664
s_nop 0
buffer_load_dwordx4 v19,s[40:43],s55 offen offset:128  lds
s_add_u32 m0,s57,40704
s_nop 0
buffer_load_dwordx4 v18,s[40:43],s55 offen offset:256  lds
s_add_u32 m0,s57,41728
s_nop 0
buffer_load_dwordx4 v19,s[40:43],s55 offen offset:256  lds
v_add_u32 v26,49152,v20
v_add_u32 v27,49152,v21
v_add_u32 v28,49152,v22
T a[48:49],26,0
T a[50:51],26,512
T a[52:53],27,0
T a[54:55],27,512
T a[56:57],28,0
T a[58:59],28,512
T a[60:61],26,4096
T a[62:63],26,4608
T a[64:65],27,4096
T a[66:67],27,4608
T a[68:69],28,4096
T a[70:71],28,4608
s_waitcnt vmcnt(24) lgkmcnt(0)
s_barrier
s_add_u32 s55,s50,3670016
s_add_u32 m0,s57,49152
s_nop 0
buffer_load_dwordx4 v18,s[40:43],s55 offen offset:0  lds
s_add_u32 m0,s57,50176
s_nop 0
buffer_load_dwordx4 v19,s[40:43],s55 offen offset:0  lds
s_add_u32 m0,s57,57216
s_nop 0
buffer_load_dwordx4 v18,s[40:43],s55 offen offset:128  lds
s_add_u32 m0,s57,58240
s_nop 0
buffer_load_dwordx4 v19,s[40:43],s55 offen offset:128  lds
s_add_u32 m0,s57,65280
s_nop 0
buffer_load_dwordx4 v18,s[40:43],s55 offen offset:256  lds
s_add_u32 m0,s57,66304
s_nop 0
buffer_load_dwordx4 v19,s[40:43],s55 offen offset:256  lds
v_add_u32 v26,73728,v20
v_add_u32 v27,73728,v21
v_add_u32 v28,73728,v22
T a[72:73],26,0
T a[74:75],26,512
T a[76:77],27,0
T a[78:79],27,512
T a[80:81],28,0
T a[82:83],28,512
T a[84:85],26,4096
T a[86:87],26,4608
T a[88:89],27,4096
T a[90:91],27,4608
T a[92:93],28,4096
T a[94:95],28,4608
s_waitcnt vmcnt(24) lgkmcnt(0)
s_barrier
s_add_u32 s55,s50,4128768
s_add_u32 m0,s57,73728
s_nop 0
buffer_load_dwordx4 v18,s[40:43],s55 offen offset:0  lds
s_add_u32 m0,s57,74752
s_nop 0
buffer_load_dwordx4 v19,s[40:43],s55 offen offset:0  lds
s_add_u32 m0,s57,81792
s_nop 0
buffer_load_dwordx4 v18,s[40:43],s55 offen offset:128  lds
s_add_u32 m0,s57,82816
s_nop 0
buffer_load_dwordx4 v19,s[40:43],s55 offen offset:128  lds
s_add_u32 m0,s57,89856
s_nop 0
buffer_load_dwordx4 v18,s[40:43],s55 offen offset:256  lds
s_add_u32 m0,s57,90880
s_nop 0
buffer_load_dwordx4 v19,s[40:43],s55 offen offset:256  lds
v_add_u32 v26,98304,v20
v_add_u32 v27,98304,v21
v_add_u32 v28,98304,v22
T a[96:97],26,0
T a[98:99],26,512
T a[100:101],27,0
T a[102:103],27,512
T a[104:105],28,0
T a[106:107],28,512
T a[108:109],26,4096
T a[110:111],26,4608
T a[112:113],27,4096
T a[114:115],27,4608
T a[116:117],28,4096
T a[118:119],28,4608
s_waitcnt vmcnt(24) lgkmcnt(0)
s_barrier
s_add_u32 s55,s50,4587520
s_add_u32 m0,s57,98304
s_nop 0
buffer_load_dwordx4 v18,s[40:43],s55 offen offset:0  lds
s_add_u32 m0,s57,99328
s_nop 0
buffer_load_dwordx4 v19,s[40:43],s55 offen offset:0  lds
s_add_u32 m0,s57,106368
s_nop 0
buffer_load_dwordx4 v18,s[40:43],s55 offen offset:128  lds
s_add_u32 m0,s57,107392
s_nop 0
buffer_load_dwordx4 v19,s[40:43],s55 offen offset:128  lds
s_add_u32 m0,s57,114432
s_nop 0
buffer_load_dwordx4 v18,s[40:43],s55 offen offset:256  lds
s_add_u32 m0,s57,115456
s_nop 0
buffer_load_dwordx4 v19,s[40:43],s55 offen offset:256  lds
v_add_u32 v26,122880,v20
v_add_u32 v27,122880,v21
v_add_u32 v28,122880,v22
T a[120:121],26,0
T a[122:123],26,512
T a[124:125],27,0
T a[126:127],27,512
T a[128:129],28,0
T a[130:131],28,512
T a[132:133],26,4096
T a[134:135],26,4608
T a[136:137],27,4096
T a[138:139],27,4608
T a[140:141],28,4096
T a[142:143],28,4608
s_waitcnt vmcnt(24) lgkmcnt(0)
s_barrier
s_add_u32 s55,s50,5046272
s_add_u32 m0,s57,122880
s_nop 0
buffer_load_dwordx4 v18,s[40:43],s55 offen offset:0  lds
s_add_u32 m0,s57,123904
s_nop 0
buffer_load_dwordx4 v19,s[40:43],s55 offen offset:0  lds
s_add_u32 m0,s57,130944
s_nop 0
buffer_load_dwordx4 v18,s[40:43],s55 offen offset:128  lds
s_add_u32 m0,s57,131968
s_nop 0
buffer_load_dwordx4 v19,s[40:43],s55 offen offset:128  lds
s_add_u32 m0,s57,139008
s_nop 0
buffer_load_dwordx4 v18,s[40:43],s55 offen offset:256  lds
s_add_u32 m0,s57,140032
s_nop 0
buffer_load_dwordx4 v19,s[40:43],s55 offen offset:256  lds
v_add_u32 v26,0,v20
v_add_u32 v27,0,v21
v_add_u32 v28,0,v22
T a[144:145],26,0
T a[146:147],26,512
T a[148:149],27,0
T a[150:151],27,512
T a[152:153],28,0
T a[154:155],28,512
T a[156:157],26,4096
T a[158:159],26,4608
T a[160:161],27,4096
T a[162:163],27,4608
T a[164:165],28,4096
T a[166:167],28,4608
s_waitcnt vmcnt(24) lgkmcnt(0)
s_barrier
s_add_u32 s55,s50,5505024
s_add_u32 m0,s57,0
s_nop 0
buffer_load_dwordx4 v18,s[40:43],s55 offen offset:0  lds
s_add_u32 m0,s57,1024
s_nop 0
buffer_load_dwordx4 v19,s[40:43],s55 offen offset:0  lds
s_add_u32 m0,s57,8064
s_nop 0
buffer_load_dwordx4 v18,s[40:43],s55 offen offset:128  lds
s_add_u32 m0,s57,9088
s_nop 0
buffer_load_dwordx4 v19,s[40:43],s55 offen offset:128  lds
s_add_u32 m0,s57,16128
s_nop 0
buffer_load_dwordx4 v18,s[40:43],s55 offen offset:256  lds
s_add_u32 m0,s57,17152
s_nop 0
buffer_load_dwordx4 v19,s[40:43],s55 offen offset:256  lds
v_add_u32 v26,24576,v20
v_add_u32 v27,24576,v21
v_add_u32 v28,24576,v22
T a[168:169],26,0
T a[170:171],26,512
T a[172:173],27,0
T a[174:175],27,512
T a[176:177],28,0
T a[178:179],28,512
T a[180:181],26,4096
T a[182:183],26,4608
T a[184:185],27,4096
T a[186:187],27,4608
T a[188:189],28,4096
T a[190:191],28,4608
s_waitcnt vmcnt(24) lgkmcnt(0)
s_barrier
s_add_u32 s55,s50,5963776
s_add_u32 m0,s57,24576
s_nop 0
buffer_load_dwordx4 v18,s[40:43],s55 offen offset:0  lds
s_add_u32 m0,s57,25600
s_nop 0
buffer_load_dwordx4 v19,s[40:43],s55 offen offset:0  lds
s_add_u32 m0,s57,32640
s_nop 0
buffer_load_dwordx4 v18,s[40:43],s55 offen offset:128  lds
s_add_u32 m0,s57,33664
s_nop 0
buffer_load_dwordx4 v19,s[40:43],s55 offen offset:128  lds
s_add_u32 m0,s57,40704
s_nop 0
buffer_load_dwordx4 v18,s[40:43],s55 offen offset:256  lds
s_add_u32 m0,s57,41728
s_nop 0
buffer_load_dwordx4 v19,s[40:43],s55 offen offset:256  lds
v_add_u32 v26,49152,v20
v_add_u32 v27,49152,v21
v_add_u32 v28,49152,v22
T a[192:193],26,0
T a[194:195],26,512
T a[196:197],27,0
T a[198:199],27,512
T a[200:201],28,0
T a[202:203],28,512
T a[204:205],26,4096
T a[206:207],26,4608
T a[208:209],27,4096
T a[210:211],27,4608
T a[212:213],28,4096
T a[214:215],28,4608
s_waitcnt vmcnt(24) lgkmcnt(0)
s_barrier
s_add_u32 s55,s50,6422528
s_add_u32 m0,s57,49152
s_nop 0
buffer_load_dwordx4 v18,s[40:43],s55 offen offset:0  lds
s_add_u32 m0,s57,50176
s_nop 0
buffer_load_dwordx4 v19,s[40:43],s55 offen offset:0  lds
s_add_u32 m0,s57,57216
s_nop 0
buffer_load_dwordx4 v18,s[40:43],s55 offen offset:128  lds
s_add_u32 m0,s57,58240
s_nop 0
buffer_load_dwordx4 v19,s[40:43],s55 offen offset:128  lds
s_add_u32 m0,s57,65280
s_nop 0
buffer_load_dwordx4 v18,s[40:43],s55 offen offset:256  lds
s_add_u32 m0,s57,66304
s_nop 0
buffer_load_dwordx4 v19,s[40:43],s55 offen offset:256  lds
v_add_u32 v26,73728,v20
v_add_u32 v27,73728,v21
)"
R"(v_add_u32 v28,73728,v22
T a[216:217],26,0
T a[218:219],26,512
T a[220:221],27,0
T a[222:223],27,512
T a[224:225],28,0
T a[226:227],28,512
T a[228:229],26,4096
T a[230:231],26,4608
T a[232:233],27,4096
T a[234:235],27,4608
T a[236:237],28,4096
T a[238:239],28,4608
s_waitcnt vmcnt(24) lgkmcnt(0)
s_barrier
s_add_u32 s55,s50,6881280
s_add_u32 m0,s57,73728
s_nop 0
buffer_load_dwordx4 v18,s[40:43],s55 offen offset:0  lds
s_add_u32 m0,s57,74752
s_nop 0
buffer_load_dwordx4 v19,s[40:43],s55 offen offset:0  lds
s_add_u32 m0,s57,81792
s_nop 0
buffer_load_dwordx4 v18,s[40:43],s55 offen offset:128  lds
s_add_u32 m0,s57,82816
s_nop 0
buffer_load_dwordx4 v19,s[40:43],s55 offen offset:128  lds
s_add_u32 m0,s57,89856
s_nop 0
buffer_load_dwordx4 v18,s[40:43],s55 offen offset:256  lds
s_add_u32 m0,s57,90880
s_nop 0
buffer_load_dwordx4 v19,s[40:43],s55 offen offset:256  lds
v_add_u32 v26,98304,v20
v_add_u32 v27,98304,v21
v_add_u32 v28,98304,v22
T a[240:241],26,0
T a[242:243],26,512
T a[244:245],27,0
T a[246:247],27,512
T a[248:249],28,0
T a[250:251],28,512
T a[252:253],26,4096
T a[254:255],26,4608
T v[128:129],27,4096
T v[130:131],27,4608
T v[132:133],28,4096
T v[134:135],28,4608
s_waitcnt vmcnt(24) lgkmcnt(0)
s_barrier
v_add_u32 v26,122880,v20
v_add_u32 v27,122880,v21
v_add_u32 v28,122880,v22
T v[136:137],26,0
T v[138:139],26,512
T v[140:141],27,0
T v[142:143],27,512
T v[144:145],28,0
T v[146:147],28,512
T v[148:149],26,4096
T v[150:151],26,4608
T v[152:153],27,4096
T v[154:155],27,4608
T v[156:157],28,4096
T v[158:159],28,4608
s_waitcnt vmcnt(18) lgkmcnt(0)
s_barrier
v_add_u32 v26,0,v20
v_add_u32 v27,0,v21
v_add_u32 v28,0,v22
T v[160:161],26,0
T v[162:163],26,512
T v[164:165],27,0
T v[166:167],27,512
T v[168:169],28,0
T v[170:171],28,512
T v[172:173],26,4096
T v[174:175],26,4608
T v[176:177],27,4096
T v[178:179],27,4608
T v[180:181],28,4096
T v[182:183],28,4608
s_waitcnt vmcnt(12) lgkmcnt(0)
s_barrier
v_add_u32 v26,24576,v20
v_add_u32 v27,24576,v21
v_add_u32 v28,24576,v22
T v[184:185],26,0
T v[186:187],26,512
T v[188:189],27,0
T v[190:191],27,512
T v[192:193],28,0
T v[194:195],28,512
T v[196:197],26,4096
T v[198:199],26,4608
T v[200:201],27,4096
T v[202:203],27,4608
T v[204:205],28,4096
T v[206:207],28,4608
s_waitcnt vmcnt(6) lgkmcnt(0)
s_barrier
v_add_u32 v26,49152,v20
v_add_u32 v27,49152,v21
v_add_u32 v28,49152,v22
T v[208:209],26,0
T v[210:211],26,512
T v[212:213],27,0
T v[214:215],27,512
T v[216:217],28,0
T v[218:219],28,512
T v[220:221],26,4096
T v[222:223],26,4608
T v[224:225],27,4096
T v[226:227],27,4608
T v[228:229],28,4096
T v[230:231],28,4608
s_waitcnt vmcnt(0) lgkmcnt(0)
s_barrier
v_add_u32 v26,73728,v20
v_add_u32 v27,73728,v21
v_add_u32 v28,73728,v22
T v[232:233],26,0
T v[234:235],26,512
T v[236:237],27,0
T v[238:239],27,512
T v[240:241],28,0
T v[242:243],28,512
T v[244:245],26,4096
T v[246:247],26,4608
T v[248:249],27,4096
T v[250:251],27,4608
T v[252:253],28,4096
T v[254:255],28,4608
L 0
s_barrier
v_mov_b32 v42,0x3f803f80
v_mov_b32 v43,0x3f803f80
v_mov_b32 v44,0x3f803f80
v_mov_b32 v45,0x3f803f80
s_nop 1
v_mfma_f32_16x16x32_bf16 v[82:85],a[0:3],v[42:45],0
v_mfma_f32_16x16x32_bf16 v[86:89],a[4:7],v[42:45],0
v_mfma_f32_16x16x32_bf16 v[90:93],a[8:11],v[42:45],0
M 82,a[12:15],42
M 86,a[16:19],42
M 90,a[20:23],42
M 82,a[24:27],42
M 86,a[28:31],42
M 90,a[32:35],42
M 82,a[36:39],42
M 86,a[40:43],42
M 90,a[44:47],42
M 82,a[48:51],42
M 86,a[52:55],42
M 90,a[56:59],42
M 82,a[60:63],42
M 86,a[64:67],42
M 90,a[68:71],42
M 82,a[72:75],42
M 86,a[76:79],42
M 90,a[80:83],42
M 82,a[84:87],42
M 86,a[88:91],42
M 90,a[92:95],42
M 82,a[96:99],42
M 86,a[100:103],42
M 90,a[104:107],42
M 82,a[108:111],42
M 86,a[112:115],42
M 90,a[116:119],42
M 82,a[120:123],42
M 86,a[124:127],42
M 90,a[128:131],42
M 82,a[132:135],42
M 86,a[136:139],42
M 90,a[140:143],42
M 82,a[144:147],42
M 86,a[148:151],42
M 90,a[152:155],42
M 82,a[156:159],42
M 86,a[160:163],42
M 90,a[164:167],42
M 82,a[168:171],42
M 86,a[172:175],42
M 90,a[176:179],42
M 82,a[180:183],42
M 86,a[184:187],42
M 90,a[188:191],42
M 82,a[192:195],42
M 86,a[196:199],42
M 90,a[200:203],42
M 82,a[204:207],42
M 86,a[208:211],42
M 90,a[212:215],42
M 82,a[216:219],42
M 86,a[220:223],42
M 90,a[224:227],42
M 82,a[228:231],42
M 86,a[232:235],42
M 90,a[236:239],42
M 82,a[240:243],42
M 86,a[244:247],42
M 90,a[248:251],42
M 82,a[252:255],42
M 86,v[128:131],42
M 90,v[132:135],42
M 82,v[136:139],42
M 86,v[140:143],42
M 90,v[144:147],42
M 82,v[148:151],42
M 86,v[152:155],42
M 90,v[156:159],42
M 82,v[160:163],42
M 86,v[164:167],42
M 90,v[168:171],42
M 82,v[172:175],42
M 86,v[176:179],42
M 90,v[180:183],42
M 82,v[184:187],42
M 86,v[188:191],42
M 90,v[192:195],42
M 82,v[196:199],42
M 86,v[200:203],42
M 90,v[204:207],42
M 82,v[208:211],42
M 86,v[212:215],42
M 90,v[216:219],42
M 82,v[220:223],42
M 86,v[224:227],42
M 90,v[228:231],42
M 82,v[232:235],42
M 86,v[236:239],42
M 90,v[240:243],42
M 82,v[244:247],42
M 86,v[248:251],42
M 90,v[252:255],42
v_mov_b32 v4,s53
s_mov_b32 s82,0
s_mov_b32 s80,0
s_mov_b32 s54,s45
s_mul_i32 s57,s44,4096
s_mul_i32 s55,s45,32768
s_add_u32 s55,s55,s57
s_add_u32 s55,s55,31457280
s_mul_i32 s57,s44,4096
s_mul_i32 s60,s46,32768
s_mul_i32 s61,s46,1024
s_mul_i32 s62,s46,114688
s_mul_i32 s63,s46,2560
s_mul_i32 s64,s46,1024
s_lshl_b32 s59,s44,8
s_add_u32 s59,s59,131072
s_mul_i32 s58,s45,1024
s_add_u32 s58,s58,109613056
s_mul_i32 s65,s45,114688
s_add_u32 s65,s65,s47
s_mul_i32 s68,s45,2560
s_add_u32 s68,s68,s48
s_mul_i32 s73,s45,1024
s_add_u32 s73,s73,107479040
s_mul_i32 s74,s45,32768
s_add_u32 s74,s74,s49
s_mul_i32 s75,s45,1024
s_add_u32 s75,s75,107479040
s_mov_b32 s66,0x7fff0000
s_mov_b32 s67,0x7fff0000
s_mov_b32 s69,0x7fff0000
s_mov_b32 s76,0x7fff0000
s_mov_b32 s77,0x7fff0000
s_mov_b32 s70,0x7fff0000
s_mov_b32 s71,0x7fff0000
s_mov_b32 s72,0x7fff0000
v_mov_b32 v1,v0
v_add_u32 v2,16384,v0
s_mov_b32 s84,16384
s_mov_b32 s86,0x3a800000
s_mov_b32 s87,0x3727c5ac
s_mov_b32 s88,0xc0135761
s_mov_b32 s89,0x3d372713
s_lshr_b32 s85,s44,1
s_mul_i32 s85,s85,127
v_lshlrev_b32 v8,22,v0
v_and_b32 v8,0x80000000,v8
v_or_b32 v8,v8,v5
v_add_u32 v2,32768,v0
s_lshl_b32 s84,s57,1
s_add_u32 s84,s84,32768
s_getpc_b64 s[40:41]
Lg3_pc_%=:
s_lshl_b32 s78,s44,13
v_lshlrev_b32 v94,3,v0
v_add_u32 v94,s78,v94
v_min_u32 v94,Lcode_g3_e-Lg3_pc_%=-4,v94
s_mov_b32 m0,163584
s_nop 0
global_load_lds_dword v94,s[40:41]
s_add_u32 m0,s59,4096
s_nop 0
buffer_load_dword v8,s[36:39],s58 offen offset:0 sc1 lds
buffer_store_dword v4,v3,s[36:39],s71 offen offset:0 sc0 sc1
s_add_u32 m0,s59,5120
s_nop 0
buffer_load_dword v8,s[36:39],s58 offen offset:0 sc1 lds
buffer_store_dword v4,v3,s[36:39],s71 offen offset:0 sc0 sc1
s_mov_b32 s83,0
Lg3_poll_%=:
s_add_u32 s78,s83,2
s_and_b32 s78,s78,3
s_lshl_b32 s78,s78,10
s_add_u32 s78,s78,4096
s_add_u32 m0,s59,s78
s_nop 0
buffer_load_dword v8,s[36:39],s58 offen offset:0 sc1 lds
s_and_b32 s78,s83,3
s_lshl_b32 s78,s78,10
v_add_u32 v94,s78,v6
s_waitcnt vmcnt(4)
s_barrier
ds_read_b32 v7,v94 offset:4096
buffer_store_dword v4,v3,s[36:39],s70 offen offset:0 sc0 sc1
s_mov_b32 s70,s71
s_mov_b32 s71,s72
s_mov_b32 s72,0x7fff0000
s_add_u32 s83,s83,1
s_add_u32 s82,s82,1
L 0
v_cmp_eq_u32 vcc,s53,v7
s_nop 0
s_cmp_eq_u32 vcc_lo,-1
s_cbranch_scc1 Lg3_go_%=
s_cmp_lt_u32 s82,20000
s_cbranch_scc1 Lg3_poll_%=
Lg3_go_%=:
buffer_load_dword v114,v11,s[36:39],s73 offen offset:0 sc1
s_add_u32 s73,s73,s64
s_add_u32 m0,s57,0
s_add_u32 s56,s55,0
D 0,56,0
D 0,56,1024
D 0,56,2048
D 0,56,3072
s_add_u32 s58,s58,s61
s_add_u32 m0,s59,2048
s_nop 0
buffer_load_dword v8,s[36:39],s58 offen offset:0 sc1 lds
s_add_u32 m0,s57,16384
s_add_u32 s56,s55,16384
D 0,56,0
D 0,56,1024
D 0,56,2048
D 0,56,3072
s_add_u32 s55,s55,s60
s_sub_u32 s57,s84,s57
s_add_u32 s54,s54,s46
s_waitcnt vmcnt(4)
s_barrier
buffer_store_dword v4,v3,s[36:39],s70 offen offset:0 sc0 sc1
s_min_u32 s71,s71,s72
buffer_store_dword v4,v3,s[36:39],s71 offen offset:0 sc0 sc1
s_mov_b32 s70,0x7fff0000
s_mov_b32 s71,0x7fff0000
s_mov_b32 s72,0x7fff0000
s_cmp_eq_u32 s80,1
s_cbranch_scc1 Lg3_st1_%=
s_branch Lg3_st0_%=
Lg3_gox_%=:
s_add_u32 s58,s58,s61
s_add_u32 m0,s59,2048
s_nop 0
buffer_load_dword v8,s[36:39],s58 offen offset:0 sc1 lds
s_waitcnt vmcnt(0)
s_barrier
buffer_store_dword v4,v3,s[36:39],s70 offen offset:0 sc0 sc1
s_min_u32 s71,s71,s72
buffer_store_dword v4,v3,s[36:39],s71 offen offset:0 sc0 sc1
s_mov_b32 s70,0x7fff0000
s_mov_b32 s71,0x7fff0000
s_mov_b32 s72,0x7fff0000
s_cmp_eq_u32 s80,1
s_cbranch_scc1 Lg3_st1_%=
Lg3_st0_%=:
ds_read_b32 v7,v6 offset:2048
R 42,1,0
R 46,1,1024
R 50,1,2048
R 54,1,3072
R 58,1,4096
L 5
v_cmp_eq_u32 vcc,s53,v7
L 3
s_cmp_eq_u32 vcc_lo,-1
s_cbranch_scc1 Lg3_F0_%=
s_branch Lg3_L0_%=
Lg3_st1_%=:
ds_read_b32 v7,v6 offset:2048
R 42,2,0
R 46,2,1024
R 50,2,2048
R 54,2,3072
R 58,2,4096
L 5
v_cmp_eq_u32 vcc,s53,v7
L 3
s_cmp_eq_u32 vcc_lo,-1
s_cbranch_scc1 Lg3_F1_%=
s_branch Lg3_L1_%=
Lg3_F0_%=:
v_mfma_f32_16x16x32_bf16 v[26:29],a[8:11],v[42:45],0
s_add_u32 m0,s57,0
R 62,1,5120
v_add_f32_dpp v115,v114,v114 quad_perm:[1,0,3,2] row_mask:0xf bank_mask:0xf
M 26,a[20:23],46
s_add_u32 s56,s55,0
buffer_load_dword v114,v11,s[36:39],s73 offen offset:0 sc1
v_mfma_f32_16x16x32_bf16 v[22:25],a[4:7],v[42:45],0
s_add_u32 s58,s58,s61
D 0,56,0
M 22,a[16:19],46
s_mov_b32 s66,s65
R 66,1,6144
v_add_f32_dpp v115,v115,v115 quad_perm:[2,3,0,1] row_mask:0xf bank_mask:0xf
v_mfma_f32_16x16x32_bf16 v[18:21],a[0:3],v[42:45],0
s_add_u32 s65,s65,s62
v_fma_f32 v30,-v78,v82,v30
v_fma_f32 v31,-v78,v83,v31
M 18,a[12:15],46
L 3
v_add_f32_dpp v118,v115,v115 row_half_mirror row_mask:0xf bank_mask:0xf
v_fma_f32 v32,-v78,v84,v32
M 18,a[24:27],50
s_add_u32 s54,s54,s46
R 70,1,7168
v_fma_f32 v33,-v78,v85,v33
v_mul_f32 v94,v30,v30
M 18,a[36:39],54
s_add_u32 s73,s73,s64
v_mov_b32_dpp v115,v118 row_ror:8 row_mask:0xf bank_mask:0xf
v_fma_f32 v115,-v118,v118,v115
M 22,a[28:31],50
v_max_f32 v115,s87,v115
v_mul_f32 v95,v31,v31
M 22,a[40:43],54
R 42,1,8192
v_mul_f32 v96,v32,v32
M 26,a[32:35],50
v_rsq_f32 v116,v115
M 26,a[44:47],54
L 3
D 0,56,1024
M 26,a[56:59],58
R 46,1,9216
v_mul_f32 v121,v115,v116
M 26,a[68:71],62
v_mul_f32 v119,s88,v116
v_mul_f32 v115,s89,v116
v_mul_f32 v120,v119,v116
M 22,a[52:55],58
v_mul_f32 v120,v120,v115
v_mul_f32 v97,v33,v33
M 22,a[64:67],62
R 50,1,10240
v_fma_f32 v94,v80,v94,v79
M 18,a[48:51],58
ds_write_b128 v12,v[118:121] offset:0
M 18,a[60:63],62
L 4
v_fma_f32 v95,v80,v95,v79
v_fma_f32 v96,v80,v96,v79
M 18,a[72:75],66
R 54,1,11264
v_fma_f32 v97,v80,v97,v79
M 18,a[84:87],70
v_mul_f32 v94,v30,v94
v_exp_f32 v94,v94
M 22,a[76:79],66
v_mul_f32 v95,v31,v95
v_mul_f32 v96,v32,v96
M 22,a[88:91],70
R 58,1,12288
v_mul_f32 v97,v33,v97
M 26,a[80:83],66
D 0,56,2048
M 26,a[92:95],70
L 4
v_exp_f32 v95,v95
M 26,a[104:107],42
R 62,1,13312
v_fma_f32 v94,v94,v81,v81
M 26,a[116:119],46
v_exp_f32 v96,v96
M 22,a[100:103],42
v_exp_f32 v97,v97
v_fma_f32 v95,v95,v81,v81
M 22,a[112:115],46
R 66,1,14336
v_fma_f32 v96,v96,v81,v81
M 18,a[96:99],42
v_rcp_f32 v94,v94
M 18,a[108:111],46
)"
R"(L 3
v_rcp_f32 v95,v95
M 18,a[120:123],50
R 70,1,15360
v_fma_f32 v97,v97,v81,v81
M 18,a[132:135],54
s_waitcnt vmcnt(6)
D 0,56,3072
M 22,a[124:127],50
s_barrier
v_rcp_f32 v96,v96
M 22,a[136:139],54
s_add_u32 m0,s59,0
R 42,1,16384
v_rcp_f32 v97,v97
M 26,a[128:131],50
s_add_u32 s56,s55,16384
v_mul_f32 v30,v30,v94
v_mul_f32 v31,v31,v95
M 26,a[140:143],54
L 3
v_mul_f32 v32,v32,v96
v_mul_f32 v33,v33,v97
M 26,a[152:155],58
s_add_u32 s55,s55,s60
R 46,1,17408
v_cvt_pk_bf16_f32 v106,v30,v31
M 26,a[164:167],62
v_cvt_pk_bf16_f32 v107,v32,v33
v_fma_f32 v34,-v78,v86,v34
M 22,a[148:151],58
v_fma_f32 v35,-v78,v87,v35
v_fma_f32 v36,-v78,v88,v36
M 22,a[160:163],62
R 50,1,18432
v_fma_f32 v37,-v78,v89,v37
v_mul_f32 v98,v34,v34
M 18,a[144:147],58
buffer_load_dword v8,s[36:39],s58 offen offset:0 sc1 lds
M 18,a[156:159],62
L 3
v_mul_f32 v99,v35,v35
v_mul_f32 v100,v36,v36
M 18,a[168:171],66
s_add_u32 m0,s57,16384
R 54,1,19456
v_mul_f32 v101,v37,v37
M 18,a[180:183],70
s_sub_u32 s57,s84,s57
v_fma_f32 v98,v80,v98,v79
v_fma_f32 v99,v80,v99,v79
M 22,a[172:175],66
v_fma_f32 v100,v80,v100,v79
v_fma_f32 v101,v80,v101,v79
M 22,a[184:187],70
R 58,1,20480
v_mul_f32 v98,v34,v98
M 26,a[176:179],66
D 0,56,0
v_mul_f32 v99,v35,v99
M 26,a[188:191],70
L 3
v_exp_f32 v98,v98
M 26,a[200:203],42
R 62,1,21504
v_mul_f32 v100,v36,v100
M 26,a[212:215],46
v_exp_f32 v99,v99
M 22,a[196:199],42
v_exp_f32 v100,v100
M 22,a[208:211],46
R 66,1,22528
v_mul_f32 v101,v37,v101
M 18,a[192:195],42
v_exp_f32 v101,v101
M 18,a[204:207],46
L 3
v_fma_f32 v98,v98,v81,v81
v_rcp_f32 v98,v98
M 18,a[216:219],50
R 70,1,23552
v_fma_f32 v99,v99,v81,v81
M 18,a[228:231],54
D 0,56,1024
M 22,a[220:223],50
v_rcp_f32 v99,v99
M 22,a[232:235],54
R 42,1,24576
v_fma_f32 v100,v100,v81,v81
M 26,a[224:227],50
v_rcp_f32 v100,v100
M 26,a[236:239],54
L 3
v_fma_f32 v101,v101,v81,v81
v_rcp_f32 v101,v101
M 26,a[248:251],58
R 46,1,25600
v_mul_f32 v34,v34,v98
M 26,v[132:135],62
v_mul_f32 v35,v35,v99
v_mul_f32 v36,v36,v100
M 22,a[244:247],58
v_mul_f32 v37,v37,v101
v_cvt_pk_bf16_f32 v108,v34,v35
M 22,v[128:131],62
R 50,1,26624
v_cvt_pk_bf16_f32 v109,v36,v37
M 18,a[240:243],58
buffer_store_dwordx4 v[106:109],v0,s[36:39],s67 offen offset:0 sc0 sc1
v_fma_f32 v38,-v78,v90,v38
M 18,a[252:255],62
L 3
v_fma_f32 v39,-v78,v91,v39
v_fma_f32 v40,-v78,v92,v40
M 18,v[136:139],66
R 54,1,27648
v_fma_f32 v41,-v78,v93,v41
v_mul_f32 v102,v38,v38
M 18,v[148:151],70
D 0,56,2048
M 22,v[140:143],66
v_mul_f32 v103,v39,v39
v_mul_f32 v104,v40,v40
M 22,v[152:155],70
R 58,1,28672
v_mul_f32 v105,v41,v41
M 26,v[144:147],66
v_fma_f32 v102,v80,v102,v79
v_fma_f32 v103,v80,v103,v79
M 26,v[156:159],70
L 3
v_fma_f32 v104,v80,v104,v79
v_fma_f32 v105,v80,v105,v79
M 26,v[168:171],42
R 62,1,29696
v_mul_f32 v102,v38,v102
M 26,v[180:183],46
v_exp_f32 v102,v102
v_mul_f32 v103,v39,v103
M 22,v[164:167],42
v_exp_f32 v103,v103
M 22,v[176:179],46
R 66,1,30720
v_mul_f32 v104,v40,v104
M 18,v[160:163],42
v_exp_f32 v104,v104
M 18,v[172:175],46
L 3
D 0,56,3072
M 18,v[184:187],50
s_nop 0
R 70,1,31744
v_mul_f32 v105,v41,v105
M 18,v[196:199],54
s_waitcnt vmcnt(5)
v_exp_f32 v105,v105
v_fma_f32 v102,v102,v81,v81
M 22,v[188:191],50
s_barrier
buffer_store_dword v4,v3,s[36:39],s70 offen offset:0 sc0 sc1
M 22,v[200:203],54
s_mov_b32 s70,s69
R 42,2,0
ds_read_b32 v7,v6 offset:0
M 26,v[192:195],50
s_mov_b32 s69,s68
R 74,13,0
v_fma_f32 v103,v103,v81,v81
M 26,v[204:207],54
L 5
v_rcp_f32 v102,v102
M 26,v[216:219],58
s_add_u32 s68,s68,s63
R 46,2,1024
v_fma_f32 v104,v104,v81,v81
M 26,v[228:231],62
v_rcp_f32 v103,v103
M 22,v[212:215],58
v_rcp_f32 v104,v104
v_fma_f32 v105,v105,v81,v81
M 22,v[224:227],62
L 2
R 50,2,2048
v_mul_f32 v38,v38,v102
M 18,v[208:211],58
v_rcp_f32 v105,v105
M 18,v[220:223],62
s_nop 0
v_mul_f32 v39,v39,v103
v_mul_f32 v40,v40,v104
M 18,v[232:235],66
R 54,2,3072
v_mul_f32 v41,v41,v105
M 18,v[244:247],70
v_cvt_pk_bf16_f32 v110,v38,v39
v_cvt_pk_bf16_f32 v111,v40,v41
M 22,v[236:239],66
v_cmp_eq_u32 vcc,s53,v7
M 22,v[248:251],70
s_cmp_eq_u32 vcc_lo,-1
R 58,2,4096
v_permlane16_swap_b32 v110,v112
v_permlane16_swap_b32 v111,v113
M 26,v[240:243],66
buffer_store_dwordx4 v[110:113],v10,s[36:39],s67 offen offset:0 sc0 sc1
M 26,v[252:255],70
L 3
s_cbranch_scc0 Lg3_L1_%=
Lg3_F1_%=:
v_mfma_f32_16x16x32_bf16 v[38:41],a[8:11],v[42:45],0
s_add_u32 m0,s57,0
R 62,2,5120
v_add_f32_dpp v115,v114,v114 quad_perm:[1,0,3,2] row_mask:0xf bank_mask:0xf
M 38,a[20:23],46
s_add_u32 s56,s55,0
buffer_load_dword v114,v11,s[36:39],s73 offen offset:0 sc1
v_mfma_f32_16x16x32_bf16 v[34:37],a[4:7],v[42:45],0
s_add_u32 s58,s58,s61
D 0,56,0
M 34,a[16:19],46
s_mov_b32 s67,s65
R 66,2,6144
v_add_f32_dpp v115,v115,v115 quad_perm:[2,3,0,1] row_mask:0xf bank_mask:0xf
v_mfma_f32_16x16x32_bf16 v[30:33],a[0:3],v[42:45],0
s_add_u32 s65,s65,s62
v_fma_f32 v18,-v74,v82,v18
v_fma_f32 v19,-v74,v83,v19
M 30,a[12:15],46
L 3
v_add_f32_dpp v118,v115,v115 row_half_mirror row_mask:0xf bank_mask:0xf
v_fma_f32 v20,-v74,v84,v20
M 30,a[24:27],50
s_add_u32 s54,s54,s46
R 70,2,7168
v_fma_f32 v21,-v74,v85,v21
v_mul_f32 v94,v18,v18
M 30,a[36:39],54
s_add_u32 s73,s73,s64
v_mov_b32_dpp v115,v118 row_ror:8 row_mask:0xf bank_mask:0xf
v_fma_f32 v115,-v118,v118,v115
M 34,a[28:31],50
v_max_f32 v115,s87,v115
v_mul_f32 v95,v19,v19
M 34,a[40:43],54
R 42,2,8192
v_mul_f32 v96,v20,v20
M 38,a[32:35],50
v_rsq_f32 v116,v115
M 38,a[44:47],54
L 3
D 0,56,1024
M 38,a[56:59],58
R 46,2,9216
v_mul_f32 v121,v115,v116
M 38,a[68:71],62
v_mul_f32 v119,s88,v116
v_mul_f32 v115,s89,v116
v_mul_f32 v120,v119,v116
M 34,a[52:55],58
v_mul_f32 v120,v120,v115
v_mul_f32 v97,v21,v21
M 34,a[64:67],62
R 50,2,10240
v_fma_f32 v94,v76,v94,v75
M 30,a[48:51],58
ds_write_b128 v12,v[118:121] offset:2048
M 30,a[60:63],62
L 4
v_fma_f32 v95,v76,v95,v75
v_fma_f32 v96,v76,v96,v75
M 30,a[72:75],66
R 54,2,11264
v_fma_f32 v97,v76,v97,v75
M 30,a[84:87],70
v_mul_f32 v94,v18,v94
v_exp_f32 v94,v94
M 34,a[76:79],66
v_mul_f32 v95,v19,v95
v_mul_f32 v96,v20,v96
M 34,a[88:91],70
R 58,2,12288
v_mul_f32 v97,v21,v97
M 38,a[80:83],66
D 0,56,2048
M 38,a[92:95],70
L 4
v_exp_f32 v95,v95
M 38,a[104:107],42
R 62,2,13312
v_fma_f32 v94,v94,v77,v77
M 38,a[116:119],46
v_exp_f32 v96,v96
M 34,a[100:103],42
v_exp_f32 v97,v97
v_fma_f32 v95,v95,v77,v77
M 34,a[112:115],46
R 66,2,14336
v_fma_f32 v96,v96,v77,v77
M 30,a[96:99],42
v_rcp_f32 v94,v94
M 30,a[108:111],46
L 3
v_rcp_f32 v95,v95
M 30,a[120:123],50
R 70,2,15360
v_fma_f32 v97,v97,v77,v77
M 30,a[132:135],54
s_waitcnt vmcnt(6)
D 0,56,3072
M 34,a[124:127],50
s_barrier
v_rcp_f32 v96,v96
M 34,a[136:139],54
s_add_u32 m0,s59,1024
R 42,2,16384
v_rcp_f32 v97,v97
M 38,a[128:131],50
s_add_u32 s56,s55,16384
v_mul_f32 v18,v18,v94
v_mul_f32 v19,v19,v95
M 38,a[140:143],54
L 3
v_mul_f32 v20,v20,v96
v_mul_f32 v21,v21,v97
M 38,a[152:155],58
s_add_u32 s55,s55,s60
R 46,2,17408
v_cvt_pk_bf16_f32 v106,v18,v19
M 38,a[164:167],62
v_cvt_pk_bf16_f32 v107,v20,v21
v_fma_f32 v22,-v74,v86,v22
M 34,a[148:151],58
v_fma_f32 v23,-v74,v87,v23
v_fma_f32 v24,-v74,v88,v24
M 34,a[160:163],62
R 50,2,18432
v_fma_f32 v25,-v74,v89,v25
v_mul_f32 v98,v22,v22
M 30,a[144:147],58
buffer_load_dword v8,s[36:39],s58 offen offset:0 sc1 lds
M 30,a[156:159],62
L 3
v_mul_f32 v99,v23,v23
v_mul_f32 v100,v24,v24
M 30,a[168:171],66
s_add_u32 m0,s57,16384
R 54,2,19456
v_mul_f32 v101,v25,v25
M 30,a[180:183],70
s_sub_u32 s57,s84,s57
v_fma_f32 v98,v76,v98,v75
v_fma_f32 v99,v76,v99,v75
M 34,a[172:175],66
v_fma_f32 v100,v76,v100,v75
v_fma_f32 v101,v76,v101,v75
M 34,a[184:187],70
R 58,2,20480
v_mul_f32 v98,v22,v98
M 38,a[176:179],66
D 0,56,0
v_mul_f32 v99,v23,v99
M 38,a[188:191],70
L 3
v_exp_f32 v98,v98
M 38,a[200:203],42
R 62,2,21504
v_mul_f32 v100,v24,v100
M 38,a[212:215],46
v_exp_f32 v99,v99
M 34,a[196:199],42
v_exp_f32 v100,v100
M 34,a[208:211],46
R 66,2,22528
v_mul_f32 v101,v25,v101
M 30,a[192:195],42
v_exp_f32 v101,v101
M 30,a[204:207],46
L 3
v_fma_f32 v98,v98,v77,v77
v_rcp_f32 v98,v98
M 30,a[216:219],50
R 70,2,23552
v_fma_f32 v99,v99,v77,v77
M 30,a[228:231],54
D 0,56,1024
M 34,a[220:223],50
v_rcp_f32 v99,v99
M 34,a[232:235],54
R 42,2,24576
v_fma_f32 v100,v100,v77,v77
M 38,a[224:227],50
v_rcp_f32 v100,v100
M 38,a[236:239],54
L 3
v_fma_f32 v101,v101,v77,v77
v_rcp_f32 v101,v101
M 38,a[248:251],58
R 46,2,25600
v_mul_f32 v22,v22,v98
M 38,v[132:135],62
v_mul_f32 v23,v23,v99
v_mul_f32 v24,v24,v100
M 34,a[244:247],58
v_mul_f32 v25,v25,v101
v_cvt_pk_bf16_f32 v108,v22,v23
M 34,v[128:131],62
R 50,2,26624
v_cvt_pk_bf16_f32 v109,v24,v25
M 30,a[240:243],58
buffer_store_dwordx4 v[106:109],v0,s[36:39],s66 offen offset:0 sc0 sc1
v_fma_f32 v26,-v74,v90,v26
M 30,a[252:255],62
L 3
v_fma_f32 v27,-v74,v91,v27
v_fma_f32 v28,-v74,v92,v28
M 30,v[136:139],66
R 54,2,27648
v_fma_f32 v29,-v74,v93,v29
v_mul_f32 v102,v26,v26
M 30,v[148:151],70
D 0,56,2048
M 34,v[140:143],66
v_mul_f32 v103,v27,v27
v_mul_f32 v104,v28,v28
M 34,v[152:155],70
R 58,2,28672
v_mul_f32 v105,v29,v29
M 38,v[144:147],66
v_fma_f32 v102,v76,v102,v75
v_fma_f32 v103,v76,v103,v75
M 38,v[156:159],70
L 3
v_fma_f32 v104,v76,v104,v75
v_fma_f32 v105,v76,v105,v75
M 38,v[168:171],42
R 62,2,29696
v_mul_f32 v102,v26,v102
M 38,v[180:183],46
v_exp_f32 v102,v102
v_mul_f32 v103,v27,v103
M 34,v[164:167],42
v_exp_f32 v103,v103
M 34,v[176:179],46
R 66,2,30720
v_mul_f32 v104,v28,v104
M 30,v[160:163],42
v_exp_f32 v104,v104
M 30,v[172:175],46
L 3
D 0,56,3072
M 30,v[184:187],50
s_nop 0
R 70,2,31744
v_mul_f32 v105,v29,v105
M 30,v[196:199],54
s_waitcnt vmcnt(5)
v_exp_f32 v105,v105
v_fma_f32 v102,v102,v77,v77
M 34,v[188:191],50
s_barrier
buffer_store_dword v4,v3,s[36:39],s70 offen offset:0 sc0 sc1
M 34,v[200:203],54
s_mov_b32 s70,s69
R 42,1,0
ds_read_b32 v7,v6 offset:1024
M 38,v[192:195],50
s_mov_b32 s69,s68
R 78,13,2048
v_fma_f32 v103,v103,v77,v77
M 38,v[204:207],54
L 5
v_rcp_f32 v102,v102
M 38,v[216:219],58
s_add_u32 s68,s68,s63
R 46,1,1024
v_fma_f32 v104,v104,v77,v77
M 38,v[228:231],62
v_rcp_f32 v103,v103
M 34,v[212:215],58
v_rcp_f32 v104,v104
v_fma_f32 v105,v105,v77,v77
M 34,v[224:227],62
L 2
R 50,1,2048
v_mul_f32 v26,v26,v102
M 30,v[208:211],58
v_rcp_f32 v105,v105
M 30,v[220:223],62
s_nop 0
v_mul_f32 v27,v27,v103
v_mul_f32 v28,v28,v104
M 30,v[232:235],66
R 54,1,3072
v_mul_f32 v29,v29,v105
M 30,v[244:247],70
v_cvt_pk_bf16_f32 v110,v26,v27
v_cvt_pk_bf16_f32 v111,v28,v29
M 34,v[236:239],66
v_cmp_eq_u32 vcc,s53,v7
M 34,v[248:251],70
)"
R"(s_cmp_eq_u32 vcc_lo,-1
R 58,1,4096
v_permlane16_swap_b32 v110,v112
v_permlane16_swap_b32 v111,v113
M 38,v[240:243],66
buffer_store_dwordx4 v[110:113],v10,s[36:39],s66 offen offset:0 sc0 sc1
M 38,v[252:255],70
L 3
s_cbranch_scc1 Lg3_F0_%=
Lg3_L0_%=:
v_mfma_f32_16x16x32_bf16 v[26:29],a[8:11],v[42:45],0
s_add_u32 m0,s59,3072
R 62,1,5120
v_add_f32_dpp v115,v114,v114 quad_perm:[1,0,3,2] row_mask:0xf bank_mask:0xf
M 26,a[20:23],46
s_mov_b32 s66,s65
buffer_load_dword v8,s[36:39],s58 offen offset:0 sc1 lds
v_mfma_f32_16x16x32_bf16 v[22:25],a[4:7],v[42:45],0
s_add_u32 s65,s65,s62
v_add_f32_dpp v115,v115,v115 quad_perm:[2,3,0,1] row_mask:0xf bank_mask:0xf
v_fma_f32 v30,-v78,v82,v30
M 22,a[16:19],46
R 66,1,6144
v_fma_f32 v31,-v78,v83,v31
v_mfma_f32_16x16x32_bf16 v[18:21],a[0:3],v[42:45],0
v_add_f32_dpp v118,v115,v115 row_half_mirror row_mask:0xf bank_mask:0xf
v_fma_f32 v32,-v78,v84,v32
v_fma_f32 v33,-v78,v85,v33
M 18,a[12:15],46
L 3
v_mul_f32 v94,v30,v30
v_mul_f32 v95,v31,v31
M 18,a[24:27],50
R 70,1,7168
v_mov_b32_dpp v115,v118 row_ror:8 row_mask:0xf bank_mask:0xf
M 18,a[36:39],54
v_fma_f32 v115,-v118,v118,v115
v_max_f32 v115,s87,v115
M 22,a[28:31],50
v_rsq_f32 v116,v115
v_mul_f32 v96,v32,v32
M 22,a[40:43],54
R 42,1,8192
v_mul_f32 v121,v115,v116
M 26,a[32:35],50
v_mul_f32 v119,s88,v116
v_mul_f32 v115,s89,v116
M 26,a[44:47],54
L 3
v_mul_f32 v120,v119,v116
v_mul_f32 v120,v120,v115
M 26,a[56:59],58
R 46,1,9216
v_mul_f32 v97,v33,v33
M 26,a[68:71],62
ds_write_b128 v12,v[118:121] offset:0
M 22,a[52:55],58
v_fma_f32 v94,v80,v94,v79
v_fma_f32 v95,v80,v95,v79
M 22,a[64:67],62
R 50,1,10240
v_fma_f32 v96,v80,v96,v79
M 18,a[48:51],58
v_fma_f32 v97,v80,v97,v79
v_mul_f32 v94,v30,v94
M 18,a[60:63],62
L 4
v_exp_f32 v94,v94
v_mul_f32 v95,v31,v95
M 18,a[72:75],66
R 54,1,11264
v_mul_f32 v96,v32,v96
M 18,a[84:87],70
v_exp_f32 v95,v95
M 22,a[76:79],66
v_exp_f32 v96,v96
M 22,a[88:91],70
R 58,1,12288
v_mul_f32 v97,v33,v97
M 26,a[80:83],66
v_exp_f32 v97,v97
v_fma_f32 v94,v94,v81,v81
M 26,a[92:95],70
L 4
v_rcp_f32 v94,v94
M 26,a[104:107],42
R 62,1,13312
v_fma_f32 v95,v95,v81,v81
M 26,a[116:119],46
v_rcp_f32 v95,v95
M 22,a[100:103],42
v_fma_f32 v96,v96,v81,v81
v_rcp_f32 v96,v96
M 22,a[112:115],46
R 66,1,14336
v_fma_f32 v97,v97,v81,v81
M 18,a[96:99],42
v_rcp_f32 v97,v97
M 18,a[108:111],46
L 3
v_mul_f32 v30,v30,v94
v_mul_f32 v31,v31,v95
M 18,a[120:123],50
R 70,1,15360
v_mul_f32 v32,v32,v96
M 18,a[132:135],54
s_waitcnt vmcnt(0)
v_mul_f32 v33,v33,v97
v_cvt_pk_bf16_f32 v106,v30,v31
v_cvt_pk_bf16_f32 v107,v32,v33
M 22,a[124:127],50
s_barrier
ds_read_b32 v7,v6 offset:3072
v_fma_f32 v34,-v78,v86,v34
M 22,a[136:139],54
R 42,1,16384
v_fma_f32 v35,-v78,v87,v35
M 26,a[128:131],50
buffer_store_dword v4,v3,s[36:39],s70 offen offset:0 sc0 sc1
M 26,a[140:143],54
L 4
v_fma_f32 v36,-v78,v88,v36
v_fma_f32 v37,-v78,v89,v37
v_mul_f32 v98,v34,v34
M 26,a[152:155],58
s_mov_b32 s70,0x7fff0000
R 46,1,17408
v_mul_f32 v99,v35,v35
M 26,a[164:167],62
v_mul_f32 v100,v36,v36
v_mul_f32 v101,v37,v37
M 22,a[148:151],58
L 2
v_cmp_eq_u32 vcc,s53,v7
v_fma_f32 v98,v80,v98,v79
M 22,a[160:163],62
R 50,1,18432
v_fma_f32 v99,v80,v99,v79
M 18,a[144:147],58
s_cmp_eq_u32 vcc_lo,-1
v_fma_f32 v100,v80,v100,v79
v_fma_f32 v101,v80,v101,v79
v_mul_f32 v98,v34,v98
s_cbranch_scc0 Lg3_X0_%=
M 18,a[156:159],62
s_nop 0
s_add_u32 m0,s57,0
s_add_u32 s56,s55,0
buffer_load_dword v114,v11,s[36:39],s73 offen offset:0 sc1
M 18,a[168:171],66
s_add_u32 s58,s58,s61
R 54,1,19456
v_mul_f32 v99,v35,v99
M 18,a[180:183],70
s_add_u32 s54,s54,s46
D 0,56,0
M 22,a[172:175],66
s_add_u32 s73,s73,s64
v_exp_f32 v98,v98
v_mul_f32 v100,v36,v100
M 22,a[184:187],70
R 58,1,20480
v_mul_f32 v101,v37,v101
M 26,a[176:179],66
v_exp_f32 v99,v99
M 26,a[188:191],70
L 3
D 0,56,1024
M 26,a[200:203],42
R 62,1,21504
v_exp_f32 v100,v100
M 26,a[212:215],46
v_exp_f32 v101,v101
M 22,a[196:199],42
v_fma_f32 v98,v98,v81,v81
v_fma_f32 v99,v99,v81,v81
M 22,a[208:211],46
R 66,1,22528
v_fma_f32 v100,v100,v81,v81
M 18,a[192:195],42
D 0,56,2048
M 18,a[204:207],46
L 3
v_rcp_f32 v98,v98
v_fma_f32 v101,v101,v81,v81
M 18,a[216:219],50
R 70,1,23552
v_mul_f32 v34,v34,v98
M 18,a[228:231],54
D 0,56,3072
M 22,a[220:223],50
s_add_u32 m0,s59,0
v_rcp_f32 v99,v99
M 22,a[232:235],54
s_add_u32 s56,s55,16384
R 42,1,24576
v_rcp_f32 v100,v100
M 26,a[224:227],50
s_add_u32 s55,s55,s60
buffer_load_dword v8,s[36:39],s58 offen offset:0 sc1 lds
M 26,a[236:239],54
L 3
v_rcp_f32 v101,v101
M 26,a[248:251],58
s_add_u32 m0,s57,16384
R 46,1,25600
v_mul_f32 v35,v35,v99
M 26,v[132:135],62
s_sub_u32 s57,s84,s57
D 0,56,0
M 22,a[244:247],58
v_mul_f32 v36,v36,v100
v_mul_f32 v37,v37,v101
v_cvt_pk_bf16_f32 v108,v34,v35
M 22,v[128:131],62
R 50,1,26624
v_cvt_pk_bf16_f32 v109,v36,v37
M 18,a[240:243],58
buffer_store_dwordx4 v[106:109],v0,s[36:39],s67 offen offset:0 sc0 sc1
v_fma_f32 v38,-v78,v90,v38
M 18,a[252:255],62
L 3
v_fma_f32 v39,-v78,v91,v39
v_fma_f32 v40,-v78,v92,v40
M 18,v[136:139],66
R 54,1,27648
v_fma_f32 v41,-v78,v93,v41
v_mul_f32 v102,v38,v38
M 18,v[148:151],70
D 0,56,1024
M 22,v[140:143],66
v_mul_f32 v103,v39,v39
v_mul_f32 v104,v40,v40
M 22,v[152:155],70
R 58,1,28672
v_mul_f32 v105,v41,v41
M 26,v[144:147],66
v_fma_f32 v102,v80,v102,v79
v_fma_f32 v103,v80,v103,v79
M 26,v[156:159],70
L 3
D 0,56,2048
v_fma_f32 v104,v80,v104,v79
M 26,v[168:171],42
R 62,1,29696
v_fma_f32 v105,v80,v105,v79
M 26,v[180:183],46
v_mul_f32 v102,v38,v102
v_mul_f32 v103,v39,v103
M 22,v[164:167],42
v_exp_f32 v102,v102
M 22,v[176:179],46
R 66,1,30720
v_exp_f32 v103,v103
M 18,v[160:163],42
D 0,56,3072
M 18,v[172:175],46
L 3
v_mul_f32 v104,v40,v104
v_mul_f32 v105,v41,v105
M 18,v[184:187],50
s_nop 0
R 70,1,31744
v_fma_f32 v102,v102,v81,v81
M 18,v[196:199],54
s_waitcnt vmcnt(5)
v_exp_f32 v104,v104
M 22,v[188:191],50
s_barrier
buffer_store_dword v4,v3,s[36:39],s70 offen offset:0 sc0 sc1
v_fma_f32 v103,v103,v81,v81
M 22,v[200:203],54
s_mov_b32 s70,s69
R 42,2,0
ds_read_b32 v7,v6 offset:0
M 26,v[192:195],50
s_mov_b32 s69,s68
R 74,13,0
v_fma_f32 v104,v104,v81,v81
M 26,v[204:207],54
L 5
v_exp_f32 v105,v105
M 26,v[216:219],58
s_add_u32 s68,s68,s63
R 46,2,1024
v_rcp_f32 v102,v102
M 26,v[228:231],62
v_rcp_f32 v103,v103
M 22,v[212:215],58
v_rcp_f32 v104,v104
M 22,v[224:227],62
L 2
R 50,2,2048
v_fma_f32 v105,v105,v81,v81
M 18,v[208:211],58
v_rcp_f32 v105,v105
v_mul_f32 v38,v38,v102
M 18,v[220:223],62
s_nop 0
v_mul_f32 v39,v39,v103
v_mul_f32 v40,v40,v104
M 18,v[232:235],66
R 54,2,3072
v_mul_f32 v41,v41,v105
M 18,v[244:247],70
v_cvt_pk_bf16_f32 v110,v38,v39
v_cvt_pk_bf16_f32 v111,v40,v41
M 22,v[236:239],66
v_cmp_eq_u32 vcc,s53,v7
M 22,v[248:251],70
s_cmp_eq_u32 vcc_lo,-1
R 58,2,4096
v_permlane16_swap_b32 v110,v112
v_permlane16_swap_b32 v111,v113
M 26,v[240:243],66
buffer_store_dwordx4 v[110:113],v10,s[36:39],s67 offen offset:0 sc0 sc1
M 26,v[252:255],70
L 3
s_cbranch_scc1 Lg3_F1_%=
s_branch Lg3_L1_%=
Lg3_X0_%=:
M 18,a[156:159],62
s_nop 0
s_add_u32 m0,s59,0
v_mul_f32 v99,v35,v99
M 18,a[168:171],66
R 54,1,19456
v_mul_f32 v100,v36,v100
M 18,a[180:183],70
buffer_load_dword v8,s[36:39],s58 offen offset:0 sc1 lds
M 22,a[172:175],66
v_exp_f32 v98,v98
M 22,a[184:187],70
R 58,1,20480
v_mul_f32 v101,v37,v101
M 26,a[176:179],66
v_exp_f32 v99,v99
M 26,a[188:191],70
L 3
v_exp_f32 v100,v100
M 26,a[200:203],42
R 62,1,21504
v_fma_f32 v98,v98,v81,v81
M 26,a[212:215],46
v_exp_f32 v101,v101
M 22,a[196:199],42
v_rcp_f32 v98,v98
M 22,a[208:211],46
R 66,1,22528
v_fma_f32 v99,v99,v81,v81
M 18,a[192:195],42
v_rcp_f32 v99,v99
M 18,a[204:207],46
L 3
v_fma_f32 v100,v100,v81,v81
v_fma_f32 v101,v101,v81,v81
M 18,a[216:219],50
R 70,1,23552
v_mul_f32 v34,v34,v98
M 18,a[228:231],54
v_rcp_f32 v100,v100
M 22,a[220:223],50
v_rcp_f32 v101,v101
M 22,a[232:235],54
R 42,1,24576
v_mul_f32 v35,v35,v99
M 26,a[224:227],50
v_mul_f32 v36,v36,v100
v_mul_f32 v37,v37,v101
M 26,a[236:239],54
L 3
v_cvt_pk_bf16_f32 v108,v34,v35
v_cvt_pk_bf16_f32 v109,v36,v37
M 26,a[248:251],58
R 46,1,25600
v_fma_f32 v38,-v78,v90,v38
M 26,v[132:135],62
buffer_store_dwordx4 v[106:109],v0,s[36:39],s67 offen offset:0 sc0 sc1
v_fma_f32 v39,-v78,v91,v39
M 22,a[244:247],58
v_fma_f32 v40,-v78,v92,v40
v_fma_f32 v41,-v78,v93,v41
M 22,v[128:131],62
R 50,1,26624
v_mul_f32 v102,v38,v38
M 18,a[240:243],58
v_mul_f32 v103,v39,v39
v_mul_f32 v104,v40,v40
M 18,a[252:255],62
L 3
v_mul_f32 v105,v41,v41
v_fma_f32 v102,v80,v102,v79
M 18,v[136:139],66
R 54,1,27648
v_fma_f32 v103,v80,v103,v79
M 18,v[148:151],70
v_fma_f32 v104,v80,v104,v79
v_fma_f32 v105,v80,v105,v79
M 22,v[140:143],66
v_mul_f32 v102,v38,v102
v_mul_f32 v103,v39,v103
M 22,v[152:155],70
R 58,1,28672
v_mul_f32 v104,v40,v104
M 26,v[144:147],66
v_exp_f32 v102,v102
M 26,v[156:159],70
L 3
v_exp_f32 v103,v103
M 26,v[168:171],42
R 62,1,29696
v_mul_f32 v105,v41,v105
M 26,v[180:183],46
v_exp_f32 v104,v104
M 22,v[164:167],42
v_exp_f32 v105,v105
M 22,v[176:179],46
R 66,1,30720
v_fma_f32 v102,v102,v81,v81
M 18,v[160:163],42
v_rcp_f32 v102,v102
M 18,v[172:175],46
L 3
v_fma_f32 v103,v103,v81,v81
v_fma_f32 v104,v104,v81,v81
M 18,v[184:187],50
s_nop 0
R 70,1,31744
v_fma_f32 v105,v105,v81,v81
M 18,v[196:199],54
s_waitcnt vmcnt(1)
v_rcp_f32 v103,v103
M 22,v[188:191],50
s_barrier
buffer_store_dword v4,v3,s[36:39],s70 offen offset:0 sc0 sc1
M 22,v[200:203],54
s_mov_b32 s70,s69
ds_read_b32 v7,v6 offset:0
R 74,13,0
M 26,v[192:195],50
s_mov_b32 s69,s68
v_rcp_f32 v104,v104
M 26,v[204:207],54
L 4
v_rcp_f32 v105,v105
M 26,v[216:219],58
s_add_u32 s68,s68,s63
v_mul_f32 v38,v38,v102
v_mul_f32 v39,v39,v103
M 26,v[228:231],62
v_mul_f32 v40,v40,v104
v_mul_f32 v41,v41,v105
M 22,v[212:215],58
v_cvt_pk_bf16_f32 v110,v38,v39
v_cvt_pk_bf16_f32 v111,v40,v41
M 22,v[224:227],62
L 1
v_cmp_eq_u32 vcc,s53,v7
M 18,v[208:211],58
v_permlane16_swap_b32 v110,v112
M 18,v[220:223],62
s_nop 0
v_permlane16_swap_b32 v111,v113
M 18,v[232:235],66
buffer_store_dwordx4 v[110:113],v10,s[36:39],s67 offen offset:0 sc0 sc1
M 18,v[244:247],70
M 22,v[236:239],66
M 22,v[248:251],70
M 26,v[240:243],66
M 26,v[252:255],70
s_mov_b32 s80,1
s_cmp_eq_u32 vcc_lo,-1
s_cbranch_scc0 Lg3_xn0_%=
buffer_load_dword v114,v11,s[36:39],s73 offen offset:0 sc1
L 0
v_fma_f32 v18,-v74,v82,v18
v_fma_f32 v19,-v74,v83,v19
v_fma_f32 v20,-v74,v84,v20
v_fma_f32 v21,-v74,v85,v21
v_mul_f32 v94,v18,v18
v_mul_f32 v95,v19,v19
v_mul_f32 v96,v20,v20
v_mul_f32 v97,v21,v21
s_add_u32 s73,s73,s64
s_add_u32 m0,s57,0
s_add_u32 s56,s55,0
D 0,56,0
v_fma_f32 v94,v76,v94,v75
v_fma_f32 v95,v76,v95,v75
v_fma_f32 v96,v76,v96,v75
v_fma_f32 v97,v76,v97,v75
v_mul_f32 v94,v18,v94
v_mul_f32 v95,v19,v95
v_mul_f32 v96,v20,v96
v_mul_f32 v97,v21,v97
D 0,56,1024
v_exp_f32 v94,v94
v_exp_f32 v95,v95
v_exp_f32 v96,v96
v_exp_f32 v97,v97
)"
R"(v_fma_f32 v94,v94,v77,v77
v_fma_f32 v95,v95,v77,v77
v_fma_f32 v96,v96,v77,v77
v_fma_f32 v97,v97,v77,v77
D 0,56,2048
v_rcp_f32 v94,v94
v_rcp_f32 v95,v95
v_rcp_f32 v96,v96
v_rcp_f32 v97,v97
v_mul_f32 v18,v18,v94
v_mul_f32 v19,v19,v95
v_mul_f32 v20,v20,v96
v_mul_f32 v21,v21,v97
D 0,56,3072
v_cvt_pk_bf16_f32 v106,v18,v19
v_cvt_pk_bf16_f32 v107,v20,v21
v_fma_f32 v22,-v74,v86,v22
v_fma_f32 v23,-v74,v87,v23
v_fma_f32 v24,-v74,v88,v24
v_fma_f32 v25,-v74,v89,v25
v_mul_f32 v98,v22,v22
v_mul_f32 v99,v23,v23
s_add_u32 m0,s57,16384
s_add_u32 s56,s55,16384
D 0,56,0
v_mul_f32 v100,v24,v24
v_mul_f32 v101,v25,v25
v_fma_f32 v98,v76,v98,v75
v_fma_f32 v99,v76,v99,v75
v_fma_f32 v100,v76,v100,v75
v_fma_f32 v101,v76,v101,v75
v_mul_f32 v98,v22,v98
v_mul_f32 v99,v23,v99
D 0,56,1024
v_mul_f32 v100,v24,v100
v_mul_f32 v101,v25,v101
v_exp_f32 v98,v98
v_exp_f32 v99,v99
v_exp_f32 v100,v100
v_exp_f32 v101,v101
v_fma_f32 v98,v98,v77,v77
v_fma_f32 v99,v99,v77,v77
D 0,56,2048
v_fma_f32 v100,v100,v77,v77
v_fma_f32 v101,v101,v77,v77
v_rcp_f32 v98,v98
v_rcp_f32 v99,v99
v_rcp_f32 v100,v100
v_rcp_f32 v101,v101
v_mul_f32 v22,v22,v98
v_mul_f32 v23,v23,v99
D 0,56,3072
v_mul_f32 v24,v24,v100
v_mul_f32 v25,v25,v101
v_cvt_pk_bf16_f32 v108,v22,v23
v_cvt_pk_bf16_f32 v109,v24,v25
buffer_store_dwordx4 v[106:109],v0,s[36:39],s66 offen offset:0 sc0 sc1
v_fma_f32 v26,-v74,v90,v26
v_fma_f32 v27,-v74,v91,v27
v_fma_f32 v28,-v74,v92,v28
v_fma_f32 v29,-v74,v93,v29
s_add_u32 s55,s55,s60
s_sub_u32 s57,s84,s57
s_add_u32 s54,s54,s46
v_mul_f32 v102,v26,v26
v_mul_f32 v103,v27,v27
v_mul_f32 v104,v28,v28
v_mul_f32 v105,v29,v29
v_fma_f32 v102,v76,v102,v75
v_fma_f32 v103,v76,v103,v75
v_fma_f32 v104,v76,v104,v75
v_fma_f32 v105,v76,v105,v75
v_mul_f32 v102,v26,v102
v_mul_f32 v103,v27,v103
v_mul_f32 v104,v28,v104
v_mul_f32 v105,v29,v105
v_exp_f32 v102,v102
v_exp_f32 v103,v103
v_exp_f32 v104,v104
v_exp_f32 v105,v105
v_fma_f32 v102,v102,v77,v77
v_fma_f32 v103,v103,v77,v77
v_fma_f32 v104,v104,v77,v77
v_fma_f32 v105,v105,v77,v77
v_rcp_f32 v102,v102
v_rcp_f32 v103,v103
v_rcp_f32 v104,v104
v_rcp_f32 v105,v105
v_mul_f32 v26,v26,v102
v_mul_f32 v27,v27,v103
v_mul_f32 v28,v28,v104
v_mul_f32 v29,v29,v105
v_cvt_pk_bf16_f32 v110,v26,v27
v_cvt_pk_bf16_f32 v111,v28,v29
s_nop 0
v_permlane16_swap_b32 v110,v112
v_permlane16_swap_b32 v111,v113
buffer_store_dwordx4 v[110:113],v10,s[36:39],s66 offen offset:0 sc0 sc1
s_mov_b32 s72,s69
s_mov_b32 s69,0x7fff0000
s_mov_b32 s66,0x7fff0000
s_mov_b32 s76,0x7fff0000
s_branch Lg3_gox_%=
Lg3_xn0_%=:
s_cmp_ge_u32 s54,256
s_cbranch_scc1 Lg3_fin0_%=
s_add_u32 m0,s59,4096
s_nop 0
buffer_load_dword v8,s[36:39],s58 offen offset:0 sc1 lds
buffer_store_dword v4,v3,s[36:39],s71 offen offset:0 sc0 sc1
s_add_u32 m0,s59,5120
s_nop 0
buffer_load_dword v8,s[36:39],s58 offen offset:0 sc1 lds
buffer_store_dword v4,v3,s[36:39],s71 offen offset:0 sc0 sc1
s_mov_b32 s83,0
L 0
v_fma_f32 v18,-v74,v82,v18
v_fma_f32 v19,-v74,v83,v19
v_fma_f32 v20,-v74,v84,v20
v_fma_f32 v21,-v74,v85,v21
v_mul_f32 v94,v18,v18
v_mul_f32 v95,v19,v19
v_mul_f32 v96,v20,v20
v_mul_f32 v97,v21,v21
v_fma_f32 v94,v76,v94,v75
v_fma_f32 v95,v76,v95,v75
v_fma_f32 v96,v76,v96,v75
v_fma_f32 v97,v76,v97,v75
v_mul_f32 v94,v18,v94
v_mul_f32 v95,v19,v95
v_mul_f32 v96,v20,v96
v_mul_f32 v97,v21,v97
v_exp_f32 v94,v94
v_exp_f32 v95,v95
v_exp_f32 v96,v96
v_exp_f32 v97,v97
v_fma_f32 v94,v94,v77,v77
v_fma_f32 v95,v95,v77,v77
v_fma_f32 v96,v96,v77,v77
v_fma_f32 v97,v97,v77,v77
v_rcp_f32 v94,v94
v_rcp_f32 v95,v95
v_rcp_f32 v96,v96
v_rcp_f32 v97,v97
v_mul_f32 v18,v18,v94
v_mul_f32 v19,v19,v95
v_mul_f32 v20,v20,v96
v_mul_f32 v21,v21,v97
v_cvt_pk_bf16_f32 v106,v18,v19
v_cvt_pk_bf16_f32 v107,v20,v21
v_fma_f32 v22,-v74,v86,v22
v_fma_f32 v23,-v74,v87,v23
v_fma_f32 v24,-v74,v88,v24
v_fma_f32 v25,-v74,v89,v25
v_mul_f32 v98,v22,v22
v_mul_f32 v99,v23,v23
v_mul_f32 v100,v24,v24
v_mul_f32 v101,v25,v25
v_fma_f32 v98,v76,v98,v75
v_fma_f32 v99,v76,v99,v75
v_fma_f32 v100,v76,v100,v75
v_fma_f32 v101,v76,v101,v75
v_mul_f32 v98,v22,v98
v_mul_f32 v99,v23,v99
v_mul_f32 v100,v24,v100
v_mul_f32 v101,v25,v101
v_exp_f32 v98,v98
v_exp_f32 v99,v99
v_exp_f32 v100,v100
v_exp_f32 v101,v101
v_fma_f32 v98,v98,v77,v77
v_fma_f32 v99,v99,v77,v77
v_fma_f32 v100,v100,v77,v77
v_fma_f32 v101,v101,v77,v77
v_rcp_f32 v98,v98
v_rcp_f32 v99,v99
v_rcp_f32 v100,v100
v_rcp_f32 v101,v101
v_mul_f32 v22,v22,v98
v_mul_f32 v23,v23,v99
v_mul_f32 v24,v24,v100
v_mul_f32 v25,v25,v101
v_cvt_pk_bf16_f32 v108,v22,v23
v_cvt_pk_bf16_f32 v109,v24,v25
buffer_store_dwordx4 v[106:109],v0,s[36:39],s66 offen offset:0 sc0 sc1
v_fma_f32 v26,-v74,v90,v26
v_fma_f32 v27,-v74,v91,v27
v_fma_f32 v28,-v74,v92,v28
v_fma_f32 v29,-v74,v93,v29
v_mul_f32 v102,v26,v26
v_mul_f32 v103,v27,v27
v_mul_f32 v104,v28,v28
v_mul_f32 v105,v29,v29
v_fma_f32 v102,v76,v102,v75
v_fma_f32 v103,v76,v103,v75
v_fma_f32 v104,v76,v104,v75
v_fma_f32 v105,v76,v105,v75
v_mul_f32 v102,v26,v102
v_mul_f32 v103,v27,v103
v_mul_f32 v104,v28,v104
v_mul_f32 v105,v29,v105
v_exp_f32 v102,v102
v_exp_f32 v103,v103
v_exp_f32 v104,v104
v_exp_f32 v105,v105
v_fma_f32 v102,v102,v77,v77
v_fma_f32 v103,v103,v77,v77
v_fma_f32 v104,v104,v77,v77
v_fma_f32 v105,v105,v77,v77
v_rcp_f32 v102,v102
v_rcp_f32 v103,v103
v_rcp_f32 v104,v104
v_rcp_f32 v105,v105
v_mul_f32 v26,v26,v102
v_mul_f32 v27,v27,v103
v_mul_f32 v28,v28,v104
v_mul_f32 v29,v29,v105
v_cvt_pk_bf16_f32 v110,v26,v27
v_cvt_pk_bf16_f32 v111,v28,v29
s_nop 0
v_permlane16_swap_b32 v110,v112
v_permlane16_swap_b32 v111,v113
buffer_store_dwordx4 v[110:113],v10,s[36:39],s66 offen offset:0 sc0 sc1
s_mov_b32 s72,s69
s_mov_b32 s69,0x7fff0000
s_mov_b32 s66,0x7fff0000
s_mov_b32 s76,0x7fff0000
s_branch Lg3_poll_%=
Lg3_fin0_%=:
L 0
v_fma_f32 v18,-v74,v82,v18
v_fma_f32 v19,-v74,v83,v19
v_fma_f32 v20,-v74,v84,v20
v_fma_f32 v21,-v74,v85,v21
v_mul_f32 v94,v18,v18
v_mul_f32 v95,v19,v19
v_mul_f32 v96,v20,v20
v_mul_f32 v97,v21,v21
v_fma_f32 v94,v76,v94,v75
v_fma_f32 v95,v76,v95,v75
v_fma_f32 v96,v76,v96,v75
v_fma_f32 v97,v76,v97,v75
v_mul_f32 v94,v18,v94
v_mul_f32 v95,v19,v95
v_mul_f32 v96,v20,v96
v_mul_f32 v97,v21,v97
v_exp_f32 v94,v94
v_exp_f32 v95,v95
v_exp_f32 v96,v96
v_exp_f32 v97,v97
v_fma_f32 v94,v94,v77,v77
v_fma_f32 v95,v95,v77,v77
v_fma_f32 v96,v96,v77,v77
v_fma_f32 v97,v97,v77,v77
v_rcp_f32 v94,v94
v_rcp_f32 v95,v95
v_rcp_f32 v96,v96
v_rcp_f32 v97,v97
v_mul_f32 v18,v18,v94
v_mul_f32 v19,v19,v95
v_mul_f32 v20,v20,v96
v_mul_f32 v21,v21,v97
v_cvt_pk_bf16_f32 v106,v18,v19
v_cvt_pk_bf16_f32 v107,v20,v21
v_fma_f32 v22,-v74,v86,v22
v_fma_f32 v23,-v74,v87,v23
v_fma_f32 v24,-v74,v88,v24
v_fma_f32 v25,-v74,v89,v25
v_mul_f32 v98,v22,v22
v_mul_f32 v99,v23,v23
v_mul_f32 v100,v24,v24
v_mul_f32 v101,v25,v25
v_fma_f32 v98,v76,v98,v75
v_fma_f32 v99,v76,v99,v75
v_fma_f32 v100,v76,v100,v75
v_fma_f32 v101,v76,v101,v75
v_mul_f32 v98,v22,v98
v_mul_f32 v99,v23,v99
v_mul_f32 v100,v24,v100
v_mul_f32 v101,v25,v101
v_exp_f32 v98,v98
v_exp_f32 v99,v99
v_exp_f32 v100,v100
v_exp_f32 v101,v101
v_fma_f32 v98,v98,v77,v77
v_fma_f32 v99,v99,v77,v77
v_fma_f32 v100,v100,v77,v77
v_fma_f32 v101,v101,v77,v77
v_rcp_f32 v98,v98
v_rcp_f32 v99,v99
v_rcp_f32 v100,v100
v_rcp_f32 v101,v101
v_mul_f32 v22,v22,v98
v_mul_f32 v23,v23,v99
v_mul_f32 v24,v24,v100
v_mul_f32 v25,v25,v101
v_cvt_pk_bf16_f32 v108,v22,v23
v_cvt_pk_bf16_f32 v109,v24,v25
buffer_store_dwordx4 v[106:109],v0,s[36:39],s66 offen offset:0 sc0 sc1
v_fma_f32 v26,-v74,v90,v26
v_fma_f32 v27,-v74,v91,v27
v_fma_f32 v28,-v74,v92,v28
v_fma_f32 v29,-v74,v93,v29
v_mul_f32 v102,v26,v26
v_mul_f32 v103,v27,v27
v_mul_f32 v104,v28,v28
v_mul_f32 v105,v29,v29
v_fma_f32 v102,v76,v102,v75
v_fma_f32 v103,v76,v103,v75
v_fma_f32 v104,v76,v104,v75
v_fma_f32 v105,v76,v105,v75
v_mul_f32 v102,v26,v102
v_mul_f32 v103,v27,v103
v_mul_f32 v104,v28,v104
v_mul_f32 v105,v29,v105
v_exp_f32 v102,v102
v_exp_f32 v103,v103
v_exp_f32 v104,v104
v_exp_f32 v105,v105
v_fma_f32 v102,v102,v77,v77
v_fma_f32 v103,v103,v77,v77
v_fma_f32 v104,v104,v77,v77
v_fma_f32 v105,v105,v77,v77
v_rcp_f32 v102,v102
v_rcp_f32 v103,v103
v_rcp_f32 v104,v104
v_rcp_f32 v105,v105
v_mul_f32 v26,v26,v102
v_mul_f32 v27,v27,v103
v_mul_f32 v28,v28,v104
v_mul_f32 v29,v29,v105
v_cvt_pk_bf16_f32 v110,v26,v27
v_cvt_pk_bf16_f32 v111,v28,v29
s_nop 0
v_permlane16_swap_b32 v110,v112
v_permlane16_swap_b32 v111,v113
buffer_store_dwordx4 v[110:113],v10,s[36:39],s66 offen offset:0 sc0 sc1
s_mov_b32 s72,s69
s_mov_b32 s69,0x7fff0000
s_mov_b32 s66,0x7fff0000
s_mov_b32 s76,0x7fff0000
s_branch Lg3_done_%=
Lg3_L1_%=:
v_mfma_f32_16x16x32_bf16 v[38:41],a[8:11],v[42:45],0
s_add_u32 m0,s59,3072
R 62,2,5120
v_add_f32_dpp v115,v114,v114 quad_perm:[1,0,3,2] row_mask:0xf bank_mask:0xf
M 38,a[20:23],46
s_mov_b32 s67,s65
buffer_load_dword v8,s[36:39],s58 offen offset:0 sc1 lds
v_mfma_f32_16x16x32_bf16 v[34:37],a[4:7],v[42:45],0
s_add_u32 s65,s65,s62
v_add_f32_dpp v115,v115,v115 quad_perm:[2,3,0,1] row_mask:0xf bank_mask:0xf
v_fma_f32 v18,-v74,v82,v18
M 34,a[16:19],46
R 66,2,6144
v_fma_f32 v19,-v74,v83,v19
v_mfma_f32_16x16x32_bf16 v[30:33],a[0:3],v[42:45],0
v_add_f32_dpp v118,v115,v115 row_half_mirror row_mask:0xf bank_mask:0xf
v_fma_f32 v20,-v74,v84,v20
v_fma_f32 v21,-v74,v85,v21
M 30,a[12:15],46
L 3
v_mul_f32 v94,v18,v18
v_mul_f32 v95,v19,v19
M 30,a[24:27],50
R 70,2,7168
v_mov_b32_dpp v115,v118 row_ror:8 row_mask:0xf bank_mask:0xf
M 30,a[36:39],54
v_fma_f32 v115,-v118,v118,v115
v_max_f32 v115,s87,v115
M 34,a[28:31],50
v_rsq_f32 v116,v115
v_mul_f32 v96,v20,v20
M 34,a[40:43],54
R 42,2,8192
v_mul_f32 v121,v115,v116
M 38,a[32:35],50
v_mul_f32 v119,s88,v116
v_mul_f32 v115,s89,v116
M 38,a[44:47],54
L 3
v_mul_f32 v120,v119,v116
v_mul_f32 v120,v120,v115
M 38,a[56:59],58
R 46,2,9216
v_mul_f32 v97,v21,v21
M 38,a[68:71],62
ds_write_b128 v12,v[118:121] offset:2048
M 34,a[52:55],58
v_fma_f32 v94,v76,v94,v75
v_fma_f32 v95,v76,v95,v75
M 34,a[64:67],62
R 50,2,10240
v_fma_f32 v96,v76,v96,v75
M 30,a[48:51],58
v_fma_f32 v97,v76,v97,v75
v_mul_f32 v94,v18,v94
M 30,a[60:63],62
L 4
v_exp_f32 v94,v94
v_mul_f32 v95,v19,v95
M 30,a[72:75],66
R 54,2,11264
v_mul_f32 v96,v20,v96
M 30,a[84:87],70
v_exp_f32 v95,v95
M 34,a[76:79],66
v_exp_f32 v96,v96
M 34,a[88:91],70
R 58,2,12288
v_mul_f32 v97,v21,v97
M 38,a[80:83],66
v_exp_f32 v97,v97
v_fma_f32 v94,v94,v77,v77
M 38,a[92:95],70
L 4
v_rcp_f32 v94,v94
M 38,a[104:107],42
R 62,2,13312
v_fma_f32 v95,v95,v77,v77
M 38,a[116:119],46
v_rcp_f32 v95,v95
M 34,a[100:103],42
v_fma_f32 v96,v96,v77,v77
v_rcp_f32 v96,v96
M 34,a[112:115],46
R 66,2,14336
v_fma_f32 v97,v97,v77,v77
M 30,a[96:99],42
v_rcp_f32 v97,v97
M 30,a[108:111],46
L 3
v_mul_f32 v18,v18,v94
v_mul_f32 v19,v19,v95
M 30,a[120:123],50
R 70,2,15360
v_mul_f32 v20,v20,v96
M 30,a[132:135],54
s_waitcnt vmcnt(0)
v_mul_f32 v21,v21,v97
v_cvt_pk_bf16_f32 v106,v18,v19
v_cvt_pk_bf16_f32 v107,v20,v21
M 34,a[124:127],50
s_barrier
ds_read_b32 v7,v6 offset:3072
v_fma_f32 v22,-v74,v86,v22
M 34,a[136:139],54
R 42,2,16384
v_fma_f32 v23,-v74,v87,v23
M 38,a[128:131],50
buffer_store_dword v4,v3,s[36:39],s70 offen offset:0 sc0 sc1
M 38,a[140:143],54
L 4
v_fma_f32 v24,-v74,v88,v24
v_fma_f32 v25,-v74,v89,v25
v_mul_f32 v98,v22,v22
M 38,a[152:155],58
s_mov_b32 s70,0x7fff0000
R 46,2,17408
v_mul_f32 v99,v23,v23
M 38,a[164:167],62
v_mul_f32 v100,v24,v24
v_mul_f32 v101,v25,v25
M 34,a[148:151],58
L 2
v_cmp_eq_u32 vcc,s53,v7
v_fma_f32 v98,v76,v98,v75
M 34,a[160:163],62
R 50,2,18432
v_fma_f32 v99,v76,v99,v75
M 30,a[144:147],58
s_cmp_eq_u32 vcc_lo,-1
v_fma_f32 v100,v76,v100,v75
v_fma_f32 v101,v76,v101,v75
v_mul_f32 v98,v22,v98
s_cbranch_scc0 Lg3_X1_%=
M 30,a[156:159],62
s_nop 0
s_add_u32 m0,s57,0
s_add_u32 s56,s55,0
buffer_load_dword v114,v11,s[36:39],s73 offen offset:0 sc1
M 30,a[168:171],66
s_add_u32 s58,s58,s61
R 54,2,19456
v_mul_f32 v99,v23,v99
M 30,a[180:183],70
s_add_u32 s54,s54,s46
D 0,56,0
M 34,a[172:175],66
s_add_u32 s73,s73,s64
v_exp_f32 v98,v98
v_mul_f32 v100,v24,v100
M 34,a[184:187],70
R 58,2,20480
v_mul_f32 v101,v25,v101
M 38,a[176:179],66
v_exp_f32 v99,v99
M 38,a[188:191],70
L 3
)"
R"(D 0,56,1024
M 38,a[200:203],42
R 62,2,21504
v_exp_f32 v100,v100
M 38,a[212:215],46
v_exp_f32 v101,v101
M 34,a[196:199],42
v_fma_f32 v98,v98,v77,v77
v_fma_f32 v99,v99,v77,v77
M 34,a[208:211],46
R 66,2,22528
v_fma_f32 v100,v100,v77,v77
M 30,a[192:195],42
D 0,56,2048
M 30,a[204:207],46
L 3
v_rcp_f32 v98,v98
v_fma_f32 v101,v101,v77,v77
M 30,a[216:219],50
R 70,2,23552
v_mul_f32 v22,v22,v98
M 30,a[228:231],54
D 0,56,3072
M 34,a[220:223],50
s_add_u32 m0,s59,1024
v_rcp_f32 v99,v99
M 34,a[232:235],54
s_add_u32 s56,s55,16384
R 42,2,24576
v_rcp_f32 v100,v100
M 38,a[224:227],50
s_add_u32 s55,s55,s60
buffer_load_dword v8,s[36:39],s58 offen offset:0 sc1 lds
M 38,a[236:239],54
L 3
v_rcp_f32 v101,v101
M 38,a[248:251],58
s_add_u32 m0,s57,16384
R 46,2,25600
v_mul_f32 v23,v23,v99
M 38,v[132:135],62
s_sub_u32 s57,s84,s57
D 0,56,0
M 34,a[244:247],58
v_mul_f32 v24,v24,v100
v_mul_f32 v25,v25,v101
v_cvt_pk_bf16_f32 v108,v22,v23
M 34,v[128:131],62
R 50,2,26624
v_cvt_pk_bf16_f32 v109,v24,v25
M 30,a[240:243],58
buffer_store_dwordx4 v[106:109],v0,s[36:39],s66 offen offset:0 sc0 sc1
v_fma_f32 v26,-v74,v90,v26
M 30,a[252:255],62
L 3
v_fma_f32 v27,-v74,v91,v27
v_fma_f32 v28,-v74,v92,v28
M 30,v[136:139],66
R 54,2,27648
v_fma_f32 v29,-v74,v93,v29
v_mul_f32 v102,v26,v26
M 30,v[148:151],70
D 0,56,1024
M 34,v[140:143],66
v_mul_f32 v103,v27,v27
v_mul_f32 v104,v28,v28
M 34,v[152:155],70
R 58,2,28672
v_mul_f32 v105,v29,v29
M 38,v[144:147],66
v_fma_f32 v102,v76,v102,v75
v_fma_f32 v103,v76,v103,v75
M 38,v[156:159],70
L 3
D 0,56,2048
v_fma_f32 v104,v76,v104,v75
M 38,v[168:171],42
R 62,2,29696
v_fma_f32 v105,v76,v105,v75
M 38,v[180:183],46
v_mul_f32 v102,v26,v102
v_mul_f32 v103,v27,v103
M 34,v[164:167],42
v_exp_f32 v102,v102
M 34,v[176:179],46
R 66,2,30720
v_exp_f32 v103,v103
M 30,v[160:163],42
D 0,56,3072
M 30,v[172:175],46
L 3
v_mul_f32 v104,v28,v104
v_mul_f32 v105,v29,v105
M 30,v[184:187],50
s_nop 0
R 70,2,31744
v_fma_f32 v102,v102,v77,v77
M 30,v[196:199],54
s_waitcnt vmcnt(5)
v_exp_f32 v104,v104
M 34,v[188:191],50
s_barrier
buffer_store_dword v4,v3,s[36:39],s70 offen offset:0 sc0 sc1
v_fma_f32 v103,v103,v77,v77
M 34,v[200:203],54
s_mov_b32 s70,s69
R 42,1,0
ds_read_b32 v7,v6 offset:1024
M 38,v[192:195],50
s_mov_b32 s69,s68
R 78,13,2048
v_fma_f32 v104,v104,v77,v77
M 38,v[204:207],54
L 5
v_exp_f32 v105,v105
M 38,v[216:219],58
s_add_u32 s68,s68,s63
R 46,1,1024
v_rcp_f32 v102,v102
M 38,v[228:231],62
v_rcp_f32 v103,v103
M 34,v[212:215],58
v_rcp_f32 v104,v104
M 34,v[224:227],62
L 2
R 50,1,2048
v_fma_f32 v105,v105,v77,v77
M 30,v[208:211],58
v_rcp_f32 v105,v105
v_mul_f32 v26,v26,v102
M 30,v[220:223],62
s_nop 0
v_mul_f32 v27,v27,v103
v_mul_f32 v28,v28,v104
M 30,v[232:235],66
R 54,1,3072
v_mul_f32 v29,v29,v105
M 30,v[244:247],70
v_cvt_pk_bf16_f32 v110,v26,v27
v_cvt_pk_bf16_f32 v111,v28,v29
M 34,v[236:239],66
v_cmp_eq_u32 vcc,s53,v7
M 34,v[248:251],70
s_cmp_eq_u32 vcc_lo,-1
R 58,1,4096
v_permlane16_swap_b32 v110,v112
v_permlane16_swap_b32 v111,v113
M 38,v[240:243],66
buffer_store_dwordx4 v[110:113],v10,s[36:39],s66 offen offset:0 sc0 sc1
M 38,v[252:255],70
L 3
s_cbranch_scc1 Lg3_F0_%=
s_branch Lg3_L0_%=
Lg3_X1_%=:
M 30,a[156:159],62
s_nop 0
s_add_u32 m0,s59,1024
v_mul_f32 v99,v23,v99
M 30,a[168:171],66
R 54,2,19456
v_mul_f32 v100,v24,v100
M 30,a[180:183],70
buffer_load_dword v8,s[36:39],s58 offen offset:0 sc1 lds
M 34,a[172:175],66
v_exp_f32 v98,v98
M 34,a[184:187],70
R 58,2,20480
v_mul_f32 v101,v25,v101
M 38,a[176:179],66
v_exp_f32 v99,v99
M 38,a[188:191],70
L 3
v_exp_f32 v100,v100
M 38,a[200:203],42
R 62,2,21504
v_fma_f32 v98,v98,v77,v77
M 38,a[212:215],46
v_exp_f32 v101,v101
M 34,a[196:199],42
v_rcp_f32 v98,v98
M 34,a[208:211],46
R 66,2,22528
v_fma_f32 v99,v99,v77,v77
M 30,a[192:195],42
v_rcp_f32 v99,v99
M 30,a[204:207],46
L 3
v_fma_f32 v100,v100,v77,v77
v_fma_f32 v101,v101,v77,v77
M 30,a[216:219],50
R 70,2,23552
v_mul_f32 v22,v22,v98
M 30,a[228:231],54
v_rcp_f32 v100,v100
M 34,a[220:223],50
v_rcp_f32 v101,v101
M 34,a[232:235],54
R 42,2,24576
v_mul_f32 v23,v23,v99
M 38,a[224:227],50
v_mul_f32 v24,v24,v100
v_mul_f32 v25,v25,v101
M 38,a[236:239],54
L 3
v_cvt_pk_bf16_f32 v108,v22,v23
v_cvt_pk_bf16_f32 v109,v24,v25
M 38,a[248:251],58
R 46,2,25600
v_fma_f32 v26,-v74,v90,v26
M 38,v[132:135],62
buffer_store_dwordx4 v[106:109],v0,s[36:39],s66 offen offset:0 sc0 sc1
v_fma_f32 v27,-v74,v91,v27
M 34,a[244:247],58
v_fma_f32 v28,-v74,v92,v28
v_fma_f32 v29,-v74,v93,v29
M 34,v[128:131],62
R 50,2,26624
v_mul_f32 v102,v26,v26
M 30,a[240:243],58
v_mul_f32 v103,v27,v27
v_mul_f32 v104,v28,v28
M 30,a[252:255],62
L 3
v_mul_f32 v105,v29,v29
v_fma_f32 v102,v76,v102,v75
M 30,v[136:139],66
R 54,2,27648
v_fma_f32 v103,v76,v103,v75
M 30,v[148:151],70
v_fma_f32 v104,v76,v104,v75
v_fma_f32 v105,v76,v105,v75
M 34,v[140:143],66
v_mul_f32 v102,v26,v102
v_mul_f32 v103,v27,v103
M 34,v[152:155],70
R 58,2,28672
v_mul_f32 v104,v28,v104
M 38,v[144:147],66
v_exp_f32 v102,v102
M 38,v[156:159],70
L 3
v_exp_f32 v103,v103
M 38,v[168:171],42
R 62,2,29696
v_mul_f32 v105,v29,v105
M 38,v[180:183],46
v_exp_f32 v104,v104
M 34,v[164:167],42
v_exp_f32 v105,v105
M 34,v[176:179],46
R 66,2,30720
v_fma_f32 v102,v102,v77,v77
M 30,v[160:163],42
v_rcp_f32 v102,v102
M 30,v[172:175],46
L 3
v_fma_f32 v103,v103,v77,v77
v_fma_f32 v104,v104,v77,v77
M 30,v[184:187],50
s_nop 0
R 70,2,31744
v_fma_f32 v105,v105,v77,v77
M 30,v[196:199],54
s_waitcnt vmcnt(1)
v_rcp_f32 v103,v103
M 34,v[188:191],50
s_barrier
buffer_store_dword v4,v3,s[36:39],s70 offen offset:0 sc0 sc1
M 34,v[200:203],54
s_mov_b32 s70,s69
ds_read_b32 v7,v6 offset:1024
R 78,13,2048
M 38,v[192:195],50
s_mov_b32 s69,s68
v_rcp_f32 v104,v104
M 38,v[204:207],54
L 4
v_rcp_f32 v105,v105
M 38,v[216:219],58
s_add_u32 s68,s68,s63
v_mul_f32 v26,v26,v102
v_mul_f32 v27,v27,v103
M 38,v[228:231],62
v_mul_f32 v28,v28,v104
v_mul_f32 v29,v29,v105
M 34,v[212:215],58
v_cvt_pk_bf16_f32 v110,v26,v27
v_cvt_pk_bf16_f32 v111,v28,v29
M 34,v[224:227],62
L 1
v_cmp_eq_u32 vcc,s53,v7
M 30,v[208:211],58
v_permlane16_swap_b32 v110,v112
M 30,v[220:223],62
s_nop 0
v_permlane16_swap_b32 v111,v113
M 30,v[232:235],66
buffer_store_dwordx4 v[110:113],v10,s[36:39],s66 offen offset:0 sc0 sc1
M 30,v[244:247],70
M 34,v[236:239],66
M 34,v[248:251],70
M 38,v[240:243],66
M 38,v[252:255],70
s_mov_b32 s80,0
s_cmp_eq_u32 vcc_lo,-1
s_cbranch_scc0 Lg3_xn1_%=
buffer_load_dword v114,v11,s[36:39],s73 offen offset:0 sc1
L 0
v_fma_f32 v30,-v78,v82,v30
v_fma_f32 v31,-v78,v83,v31
v_fma_f32 v32,-v78,v84,v32
v_fma_f32 v33,-v78,v85,v33
v_mul_f32 v94,v30,v30
v_mul_f32 v95,v31,v31
v_mul_f32 v96,v32,v32
v_mul_f32 v97,v33,v33
s_add_u32 s73,s73,s64
s_add_u32 m0,s57,0
s_add_u32 s56,s55,0
D 0,56,0
v_fma_f32 v94,v80,v94,v79
v_fma_f32 v95,v80,v95,v79
v_fma_f32 v96,v80,v96,v79
v_fma_f32 v97,v80,v97,v79
v_mul_f32 v94,v30,v94
v_mul_f32 v95,v31,v95
v_mul_f32 v96,v32,v96
v_mul_f32 v97,v33,v97
D 0,56,1024
v_exp_f32 v94,v94
v_exp_f32 v95,v95
v_exp_f32 v96,v96
v_exp_f32 v97,v97
v_fma_f32 v94,v94,v81,v81
v_fma_f32 v95,v95,v81,v81
v_fma_f32 v96,v96,v81,v81
v_fma_f32 v97,v97,v81,v81
D 0,56,2048
v_rcp_f32 v94,v94
v_rcp_f32 v95,v95
v_rcp_f32 v96,v96
v_rcp_f32 v97,v97
v_mul_f32 v30,v30,v94
v_mul_f32 v31,v31,v95
v_mul_f32 v32,v32,v96
v_mul_f32 v33,v33,v97
D 0,56,3072
v_cvt_pk_bf16_f32 v106,v30,v31
v_cvt_pk_bf16_f32 v107,v32,v33
v_fma_f32 v34,-v78,v86,v34
v_fma_f32 v35,-v78,v87,v35
v_fma_f32 v36,-v78,v88,v36
v_fma_f32 v37,-v78,v89,v37
v_mul_f32 v98,v34,v34
v_mul_f32 v99,v35,v35
s_add_u32 m0,s57,16384
s_add_u32 s56,s55,16384
D 0,56,0
v_mul_f32 v100,v36,v36
v_mul_f32 v101,v37,v37
v_fma_f32 v98,v80,v98,v79
v_fma_f32 v99,v80,v99,v79
v_fma_f32 v100,v80,v100,v79
v_fma_f32 v101,v80,v101,v79
v_mul_f32 v98,v34,v98
v_mul_f32 v99,v35,v99
D 0,56,1024
v_mul_f32 v100,v36,v100
v_mul_f32 v101,v37,v101
v_exp_f32 v98,v98
v_exp_f32 v99,v99
v_exp_f32 v100,v100
v_exp_f32 v101,v101
v_fma_f32 v98,v98,v81,v81
v_fma_f32 v99,v99,v81,v81
D 0,56,2048
v_fma_f32 v100,v100,v81,v81
v_fma_f32 v101,v101,v81,v81
v_rcp_f32 v98,v98
v_rcp_f32 v99,v99
v_rcp_f32 v100,v100
v_rcp_f32 v101,v101
v_mul_f32 v34,v34,v98
v_mul_f32 v35,v35,v99
D 0,56,3072
v_mul_f32 v36,v36,v100
v_mul_f32 v37,v37,v101
v_cvt_pk_bf16_f32 v108,v34,v35
v_cvt_pk_bf16_f32 v109,v36,v37
buffer_store_dwordx4 v[106:109],v0,s[36:39],s67 offen offset:0 sc0 sc1
v_fma_f32 v38,-v78,v90,v38
v_fma_f32 v39,-v78,v91,v39
v_fma_f32 v40,-v78,v92,v40
v_fma_f32 v41,-v78,v93,v41
s_add_u32 s55,s55,s60
s_sub_u32 s57,s84,s57
s_add_u32 s54,s54,s46
v_mul_f32 v102,v38,v38
v_mul_f32 v103,v39,v39
v_mul_f32 v104,v40,v40
v_mul_f32 v105,v41,v41
v_fma_f32 v102,v80,v102,v79
v_fma_f32 v103,v80,v103,v79
v_fma_f32 v104,v80,v104,v79
v_fma_f32 v105,v80,v105,v79
v_mul_f32 v102,v38,v102
v_mul_f32 v103,v39,v103
v_mul_f32 v104,v40,v104
v_mul_f32 v105,v41,v105
v_exp_f32 v102,v102
v_exp_f32 v103,v103
v_exp_f32 v104,v104
v_exp_f32 v105,v105
v_fma_f32 v102,v102,v81,v81
v_fma_f32 v103,v103,v81,v81
v_fma_f32 v104,v104,v81,v81
v_fma_f32 v105,v105,v81,v81
v_rcp_f32 v102,v102
v_rcp_f32 v103,v103
v_rcp_f32 v104,v104
v_rcp_f32 v105,v105
v_mul_f32 v38,v38,v102
v_mul_f32 v39,v39,v103
v_mul_f32 v40,v40,v104
v_mul_f32 v41,v41,v105
v_cvt_pk_bf16_f32 v110,v38,v39
v_cvt_pk_bf16_f32 v111,v40,v41
s_nop 0
v_permlane16_swap_b32 v110,v112
v_permlane16_swap_b32 v111,v113
buffer_store_dwordx4 v[110:113],v10,s[36:39],s67 offen offset:0 sc0 sc1
s_mov_b32 s72,s69
s_mov_b32 s69,0x7fff0000
s_mov_b32 s67,0x7fff0000
s_mov_b32 s77,0x7fff0000
s_branch Lg3_gox_%=
Lg3_xn1_%=:
s_cmp_ge_u32 s54,256
s_cbranch_scc1 Lg3_fin1_%=
s_add_u32 m0,s59,4096
s_nop 0
buffer_load_dword v8,s[36:39],s58 offen offset:0 sc1 lds
buffer_store_dword v4,v3,s[36:39],s71 offen offset:0 sc0 sc1
s_add_u32 m0,s59,5120
s_nop 0
buffer_load_dword v8,s[36:39],s58 offen offset:0 sc1 lds
buffer_store_dword v4,v3,s[36:39],s71 offen offset:0 sc0 sc1
s_mov_b32 s83,0
L 0
v_fma_f32 v30,-v78,v82,v30
v_fma_f32 v31,-v78,v83,v31
v_fma_f32 v32,-v78,v84,v32
v_fma_f32 v33,-v78,v85,v33
v_mul_f32 v94,v30,v30
v_mul_f32 v95,v31,v31
v_mul_f32 v96,v32,v32
v_mul_f32 v97,v33,v33
v_fma_f32 v94,v80,v94,v79
v_fma_f32 v95,v80,v95,v79
v_fma_f32 v96,v80,v96,v79
v_fma_f32 v97,v80,v97,v79
v_mul_f32 v94,v30,v94
v_mul_f32 v95,v31,v95
v_mul_f32 v96,v32,v96
v_mul_f32 v97,v33,v97
v_exp_f32 v94,v94
v_exp_f32 v95,v95
v_exp_f32 v96,v96
v_exp_f32 v97,v97
v_fma_f32 v94,v94,v81,v81
v_fma_f32 v95,v95,v81,v81
v_fma_f32 v96,v96,v81,v81
v_fma_f32 v97,v97,v81,v81
v_rcp_f32 v94,v94
v_rcp_f32 v95,v95
v_rcp_f32 v96,v96
v_rcp_f32 v97,v97
v_mul_f32 v30,v30,v94
v_mul_f32 v31,v31,v95
v_mul_f32 v32,v32,v96
v_mul_f32 v33,v33,v97
v_cvt_pk_bf16_f32 v106,v30,v31
v_cvt_pk_bf16_f32 v107,v32,v33
v_fma_f32 v34,-v78,v86,v34
v_fma_f32 v35,-v78,v87,v35
v_fma_f32 v36,-v78,v88,v36
v_fma_f32 v37,-v78,v89,v37
v_mul_f32 v98,v34,v34
v_mul_f32 v99,v35,v35
v_mul_f32 v100,v36,v36
v_mul_f32 v101,v37,v37
v_fma_f32 v98,v80,v98,v79
v_fma_f32 v99,v80,v99,v79
v_fma_f32 v100,v80,v100,v79
v_fma_f32 v101,v80,v101,v79
v_mul_f32 v98,v34,v98
v_mul_f32 v99,v35,v99
v_mul_f32 v100,v36,v100
v_mul_f32 v101,v37,v101
v_exp_f32 v98,v98
)"
R"(v_exp_f32 v99,v99
v_exp_f32 v100,v100
v_exp_f32 v101,v101
v_fma_f32 v98,v98,v81,v81
v_fma_f32 v99,v99,v81,v81
v_fma_f32 v100,v100,v81,v81
v_fma_f32 v101,v101,v81,v81
v_rcp_f32 v98,v98
v_rcp_f32 v99,v99
v_rcp_f32 v100,v100
v_rcp_f32 v101,v101
v_mul_f32 v34,v34,v98
v_mul_f32 v35,v35,v99
v_mul_f32 v36,v36,v100
v_mul_f32 v37,v37,v101
v_cvt_pk_bf16_f32 v108,v34,v35
v_cvt_pk_bf16_f32 v109,v36,v37
buffer_store_dwordx4 v[106:109],v0,s[36:39],s67 offen offset:0 sc0 sc1
v_fma_f32 v38,-v78,v90,v38
v_fma_f32 v39,-v78,v91,v39
v_fma_f32 v40,-v78,v92,v40
v_fma_f32 v41,-v78,v93,v41
v_mul_f32 v102,v38,v38
v_mul_f32 v103,v39,v39
v_mul_f32 v104,v40,v40
v_mul_f32 v105,v41,v41
v_fma_f32 v102,v80,v102,v79
v_fma_f32 v103,v80,v103,v79
v_fma_f32 v104,v80,v104,v79
v_fma_f32 v105,v80,v105,v79
v_mul_f32 v102,v38,v102
v_mul_f32 v103,v39,v103
v_mul_f32 v104,v40,v104
v_mul_f32 v105,v41,v105
v_exp_f32 v102,v102
v_exp_f32 v103,v103
v_exp_f32 v104,v104
v_exp_f32 v105,v105
v_fma_f32 v102,v102,v81,v81
v_fma_f32 v103,v103,v81,v81
v_fma_f32 v104,v104,v81,v81
v_fma_f32 v105,v105,v81,v81
v_rcp_f32 v102,v102
v_rcp_f32 v103,v103
v_rcp_f32 v104,v104
v_rcp_f32 v105,v105
v_mul_f32 v38,v38,v102
v_mul_f32 v39,v39,v103
v_mul_f32 v40,v40,v104
v_mul_f32 v41,v41,v105
v_cvt_pk_bf16_f32 v110,v38,v39
v_cvt_pk_bf16_f32 v111,v40,v41
s_nop 0
v_permlane16_swap_b32 v110,v112
v_permlane16_swap_b32 v111,v113
buffer_store_dwordx4 v[110:113],v10,s[36:39],s67 offen offset:0 sc0 sc1
s_mov_b32 s72,s69
s_mov_b32 s69,0x7fff0000
s_mov_b32 s67,0x7fff0000
s_mov_b32 s77,0x7fff0000
s_branch Lg3_poll_%=
Lg3_fin1_%=:
L 0
v_fma_f32 v30,-v78,v82,v30
v_fma_f32 v31,-v78,v83,v31
v_fma_f32 v32,-v78,v84,v32
v_fma_f32 v33,-v78,v85,v33
v_mul_f32 v94,v30,v30
v_mul_f32 v95,v31,v31
v_mul_f32 v96,v32,v32
v_mul_f32 v97,v33,v33
v_fma_f32 v94,v80,v94,v79
v_fma_f32 v95,v80,v95,v79
v_fma_f32 v96,v80,v96,v79
v_fma_f32 v97,v80,v97,v79
v_mul_f32 v94,v30,v94
v_mul_f32 v95,v31,v95
v_mul_f32 v96,v32,v96
v_mul_f32 v97,v33,v97
v_exp_f32 v94,v94
v_exp_f32 v95,v95
v_exp_f32 v96,v96
v_exp_f32 v97,v97
v_fma_f32 v94,v94,v81,v81
v_fma_f32 v95,v95,v81,v81
v_fma_f32 v96,v96,v81,v81
v_fma_f32 v97,v97,v81,v81
v_rcp_f32 v94,v94
v_rcp_f32 v95,v95
v_rcp_f32 v96,v96
v_rcp_f32 v97,v97
v_mul_f32 v30,v30,v94
v_mul_f32 v31,v31,v95
v_mul_f32 v32,v32,v96
v_mul_f32 v33,v33,v97
v_cvt_pk_bf16_f32 v106,v30,v31
v_cvt_pk_bf16_f32 v107,v32,v33
v_fma_f32 v34,-v78,v86,v34
v_fma_f32 v35,-v78,v87,v35
v_fma_f32 v36,-v78,v88,v36
v_fma_f32 v37,-v78,v89,v37
v_mul_f32 v98,v34,v34
v_mul_f32 v99,v35,v35
v_mul_f32 v100,v36,v36
v_mul_f32 v101,v37,v37
v_fma_f32 v98,v80,v98,v79
v_fma_f32 v99,v80,v99,v79
v_fma_f32 v100,v80,v100,v79
v_fma_f32 v101,v80,v101,v79
v_mul_f32 v98,v34,v98
v_mul_f32 v99,v35,v99
v_mul_f32 v100,v36,v100
v_mul_f32 v101,v37,v101
v_exp_f32 v98,v98
v_exp_f32 v99,v99
v_exp_f32 v100,v100
v_exp_f32 v101,v101
v_fma_f32 v98,v98,v81,v81
v_fma_f32 v99,v99,v81,v81
v_fma_f32 v100,v100,v81,v81
v_fma_f32 v101,v101,v81,v81
v_rcp_f32 v98,v98
v_rcp_f32 v99,v99
v_rcp_f32 v100,v100
v_rcp_f32 v101,v101
v_mul_f32 v34,v34,v98
v_mul_f32 v35,v35,v99
v_mul_f32 v36,v36,v100
v_mul_f32 v37,v37,v101
v_cvt_pk_bf16_f32 v108,v34,v35
v_cvt_pk_bf16_f32 v109,v36,v37
buffer_store_dwordx4 v[106:109],v0,s[36:39],s67 offen offset:0 sc0 sc1
v_fma_f32 v38,-v78,v90,v38
v_fma_f32 v39,-v78,v91,v39
v_fma_f32 v40,-v78,v92,v40
v_fma_f32 v41,-v78,v93,v41
v_mul_f32 v102,v38,v38
v_mul_f32 v103,v39,v39
v_mul_f32 v104,v40,v40
v_mul_f32 v105,v41,v41
v_fma_f32 v102,v80,v102,v79
v_fma_f32 v103,v80,v103,v79
v_fma_f32 v104,v80,v104,v79
v_fma_f32 v105,v80,v105,v79
v_mul_f32 v102,v38,v102
v_mul_f32 v103,v39,v103
v_mul_f32 v104,v40,v104
v_mul_f32 v105,v41,v105
v_exp_f32 v102,v102
v_exp_f32 v103,v103
v_exp_f32 v104,v104
v_exp_f32 v105,v105
v_fma_f32 v102,v102,v81,v81
v_fma_f32 v103,v103,v81,v81
v_fma_f32 v104,v104,v81,v81
v_fma_f32 v105,v105,v81,v81
v_rcp_f32 v102,v102
v_rcp_f32 v103,v103
v_rcp_f32 v104,v104
v_rcp_f32 v105,v105
v_mul_f32 v38,v38,v102
v_mul_f32 v39,v39,v103
v_mul_f32 v40,v40,v104
v_mul_f32 v41,v41,v105
v_cvt_pk_bf16_f32 v110,v38,v39
v_cvt_pk_bf16_f32 v111,v40,v41
s_nop 0
v_permlane16_swap_b32 v110,v112
v_permlane16_swap_b32 v111,v113
buffer_store_dwordx4 v[110:113],v10,s[36:39],s67 offen offset:0 sc0 sc1
s_mov_b32 s72,s69
s_mov_b32 s69,0x7fff0000
s_mov_b32 s67,0x7fff0000
s_mov_b32 s77,0x7fff0000
s_branch Lg3_done_%=
Lg3_done_%=:
s_waitcnt vmcnt(2)
s_barrier
buffer_store_dword v4,v3,s[36:39],s70 offen offset:0 sc0 sc1
s_waitcnt vmcnt(1)
s_barrier
buffer_store_dword v4,v3,s[36:39],s72 offen offset:0 sc0 sc1
s_waitcnt vmcnt(0) lgkmcnt(0)
s_barrier
s_endpgm
Lcode_g3_e:
.purgem M
.purgem R
.purgem T
.purgem D
.purgem L
)"
  :: "s"(ws), "s"(sWave), "s"(sG0), "s"(sStep), "s"(sOutOff), "s"(sFoutOff), "s"(sAuxOff), "s"(sWw), "s"(sBuf), "s"(sRk), "v"(vL16), "v"(vFlag), "v"(vSnapOff), "v"(vSnapRd), "v"(vWd0), "v"(vWd1), "v"(vWr0), "v"(vWr1), "v"(vWr2), "v"(vOutS), "v"(vAux0), "v"(vAux1), "v"(vAux2), "v"(vAux3), "v"(vAn), "v"(vAe), "s"(w3), "s"(out) : "v0", "v1", "v2", "v3", "v4", "v5", "v6", "v7", "v8", "v9", "v10", "v11", "v12", "v13", "v14", "v15", "v16", "v17", "v18", "v19", "v20", "v21", "v22", "v23", "v24", "v25", "v26", "v27", "v28", "v29", "v30", "v31", "v32", "v33", "v34", "v35", "v36", "v37", "v38", "v39", "v40", "v41", "v42", "v43", "v44", "v45", "v46", "v47", "v48", "v49", "v50", "v51", "v52", "v53", "v54", "v55", "v56", "v57", "v58", "v59", "v60", "v61", "v62", "v63", "v64", "v65", "v66", "v67", "v68", "v69", "v70", "v71", "v72", "v73", "v74", "v75", "v76", "v77", "v78", "v79", "v80", "v81", "v82", "v83", "v84", "v85", "v86", "v87", "v88", "v89", "v90", "v91", "v92", "v93", "v94", "v95", "v96", "v97", "v98", "v99", "v100", "v101", "v102", "v103", "v104", "v105", "v106", "v107", "v108", "v109", "v110", "v111", "v112", "v113", "v114", "v115", "v116", "v117", "v118", "v119", "v120", "v121", "v122", "v123", "v124", "v125", "v126", "v127", "v128", "v129", "v130", "v131", "v132", "v133", "v134", "v135", "v136", "v137", "v138", "v139", "v140", "v141", "v142", "v143", "v144", "v145", "v146", "v147", "v148", "v149", "v150", "v151", "v152", "v153", "v154", "v155", "v156", "v157", "v158", "v159", "v160", "v161", "v162", "v163", "v164", "v165", "v166", "v167", "v168", "v169", "v170", "v171", "v172", "v173", "v174", "v175", "v176", "v177", "v178", "v179", "v180", "v181", "v182", "v183", "v184", "v185", "v186", "v187", "v188", "v189", "v190", "v191", "v192", "v193", "v194", "v195", "v196", "v197", "v198", "v199", "v200", "v201", "v202", "v203", "v204", "v205", "v206", "v207", "v208", "v209", "v210", "v211", "v212", "v213", "v214", "v215", "v216", "v217", "v218", "v219", "v220", "v221", "v222", "v223", "v224", "v225", "v226", "v227", "v228", "v229", "v230", "v231", "v232", "v233", "v234", "v235", "v236", "v237", "v238", "v239", "a0", "a1", "a2", "a3", "a4", "a5", "a6", "a7", "a8", "a9", "a10", "a11", "a12", "a13", "a14", "a15", "a16", "a17", "a18", "a19", "a20", "a21", "a22", "a23", "a24", "a25", "a26", "a27", "a28", "a29", "a30", "a31", "a32", "a33", "a34", "a35", "a36", "a37", "a38", "a39", "a40", "a41", "a42", "a43", "a44", "a45", "a46", "a47", "a48", "a49", "a50", "a51", "a52", "a53", "a54", "a55", "a56", "a57", "a58", "a59", "a60", "a61", "a62", "a63", "a64", "a65", "a66", "a67", "a68", "a69", "a70", "a71", "a72", "a73", "a74", "a75", "a76", "a77", "a78", "a79", "a80", "a81", "a82", "a83", "a84", "a85", "a86", "a87", "a88", "a89", "a90", "a91", "a92", "a93", "a94", "a95", "a96", "a97", "a98", "a99", "a100", "a101", "a102", "a103", "a104", "a105", "a106", "a107", "a108", "a109", "a110", "a111", "a112", "a113", "a114", "a115", "a116", "a117", "a118", "a119", "a120", "a121", "a122", "a123", "a124", "a125", "a126", "a127", "a128", "a129", "a130", "a131", "a132", "a133", "a134", "a135", "a136", "a137", "a138", "a139", "a140", "a141", "a142", "a143", "a144", "a145", "a146", "a147", "a148", "a149", "a150", "a151", "a152", "a153", "a154", "a155", "a156", "a157", "a158", "a159", "a160", "a161", "a162", "a163", "a164", "a165", "a166", "a167", "a168", "a169", "a170", "a171", "a172", "a173", "a174", "a175", "a176", "a177", "a178", "a179", "a180", "a181", "a182", "a183", "a184", "a185", "a186", "a187", "a188", "a189", "a190", "a191", "a192", "a193", "a194", "a195", "a196", "a197", "a198", "a199", "a200", "a201", "a202", "a203", "a204", "a205", "a206", "a207", "a208", "a209", "a210", "a211", "a212", "a213", "a214", "a215", "a216", "a217", "a218", "a219", "a220", "a221", "a222", "a223", "a224", "a225", "a226", "a227", "a228", "a229", "a230", "a231", "a232", "a233", "a234", "a235", "a236", "a237", "a238", "a239", "a240", "a241", "a242", "a243", "a244", "a245", "a246", "a247", "a248", "a249", "a250", "a251", "a252", "a253", "a254", "a255", "s36", "s37", "s38", "s39", "s40", "s41", "s42", "s43", "s44", "s45", "s46", "s47", "s48", "s49", "s50", "s51", "s52", "s53", "s54", "s55", "s56", "s57", "s58", "s59", "s60", "s61", "s62", "s63", "s64", "s65", "s66", "s67", "s68", "s69", "s70", "s71", "s72", "s73", "s74", "s75", "s76", "s77", "s78", "s79", "s80", "s81", "s82", "s83", "s84", "s85", "s86", "s87", "s88", "s89", "s90", "s91", "s92", "s93", "s94", "s95", "s96", "s97", "vcc", "scc", "memory"); } else { asm volatile(
R"(.macro M d,w,x
v_mfma_f32_16x16x32_bf16 v[\d:\d+3], \w, v[\x:\x+3], v[\d:\d+3]
.endm
.macro R d,a,o
ds_read_b128 v[\d:\d+3], v\a offset:\o
.endm
.macro T d,a,o
ds_read_b64_tr_b16 \d, v\a offset:\o
.endm
.macro D a,s,o
buffer_load_dwordx4 v\a, s[36:39], s\s offen offset:\o sc1 lds
.endm
.macro L c
s_waitcnt lgkmcnt(\c)
.endm
Lcode_g3o_b:
s_mov_b64 s[36:37],%0
s_mov_b32 s38,111243392
s_mov_b32 s39,0x00020000
s_mov_b32 s44,%1
s_mov_b32 s45,%2
s_mov_b32 s46,%3
s_mov_b32 s47,%4
s_mov_b32 s48,%5
s_mov_b32 s49,%6
s_mov_b32 s50,%7
s_mov_b32 s51,%8
s_mov_b32 s93,%9
v_mov_b32 v0,%10
v_mov_b32 v3,%11
v_mov_b32 v5,%12
v_mov_b32 v6,%13
v_mov_b32 v18,%14
v_mov_b32 v19,%15
v_mov_b32 v20,%16
v_mov_b32 v21,%17
v_mov_b32 v22,%18
v_mov_b32 v10,%19
v_mov_b32 v11,%20
v_mov_b32 v12,%21
v_mov_b32 v13,%22
v_mov_b32 v14,%23
v_mov_b32 v15,%24
v_mov_b32 v16,%25
s_mov_b64 s[40:41],%26
s_mov_b32 s42,7340032
s_mov_b32 s43,0x00020000
s_lshl_b32 s57,s44,11
s_mov_b32 s79,111243264
buffer_load_dword v4,off,s[36:39],s79 sc1
s_add_u32 s55,s50,0
s_add_u32 m0,s57,0
s_nop 0
buffer_load_dwordx4 v18,s[40:43],s55 offen offset:0  lds
s_add_u32 m0,s57,1024
s_nop 0
buffer_load_dwordx4 v19,s[40:43],s55 offen offset:0  lds
s_add_u32 m0,s57,8064
s_nop 0
buffer_load_dwordx4 v18,s[40:43],s55 offen offset:128  lds
s_add_u32 m0,s57,9088
s_nop 0
buffer_load_dwordx4 v19,s[40:43],s55 offen offset:128  lds
s_add_u32 s55,s50,458752
s_add_u32 m0,s57,16384
s_nop 0
buffer_load_dwordx4 v18,s[40:43],s55 offen offset:0  lds
s_add_u32 m0,s57,17408
s_nop 0
buffer_load_dwordx4 v19,s[40:43],s55 offen offset:0  lds
s_add_u32 m0,s57,24448
s_nop 0
buffer_load_dwordx4 v18,s[40:43],s55 offen offset:128  lds
s_add_u32 m0,s57,25472
s_nop 0
buffer_load_dwordx4 v19,s[40:43],s55 offen offset:128  lds
s_add_u32 s55,s50,917504
s_add_u32 m0,s57,32768
s_nop 0
buffer_load_dwordx4 v18,s[40:43],s55 offen offset:0  lds
s_add_u32 m0,s57,33792
s_nop 0
buffer_load_dwordx4 v19,s[40:43],s55 offen offset:0  lds
s_add_u32 m0,s57,40832
s_nop 0
buffer_load_dwordx4 v18,s[40:43],s55 offen offset:128  lds
s_add_u32 m0,s57,41856
s_nop 0
buffer_load_dwordx4 v19,s[40:43],s55 offen offset:128  lds
s_add_u32 s55,s50,1376256
s_add_u32 m0,s57,49152
s_nop 0
buffer_load_dwordx4 v18,s[40:43],s55 offen offset:0  lds
s_add_u32 m0,s57,50176
s_nop 0
buffer_load_dwordx4 v19,s[40:43],s55 offen offset:0  lds
s_add_u32 m0,s57,57216
s_nop 0
buffer_load_dwordx4 v18,s[40:43],s55 offen offset:128  lds
s_add_u32 m0,s57,58240
s_nop 0
buffer_load_dwordx4 v19,s[40:43],s55 offen offset:128  lds
s_add_u32 s55,s50,1835008
s_add_u32 m0,s57,65536
s_nop 0
buffer_load_dwordx4 v18,s[40:43],s55 offen offset:0  lds
s_add_u32 m0,s57,66560
s_nop 0
buffer_load_dwordx4 v19,s[40:43],s55 offen offset:0  lds
s_add_u32 m0,s57,73600
s_nop 0
buffer_load_dwordx4 v18,s[40:43],s55 offen offset:128  lds
s_add_u32 m0,s57,74624
s_nop 0
buffer_load_dwordx4 v19,s[40:43],s55 offen offset:128  lds
s_add_u32 s55,s50,2293760
s_add_u32 m0,s57,81920
s_nop 0
buffer_load_dwordx4 v18,s[40:43],s55 offen offset:0  lds
s_add_u32 m0,s57,82944
s_nop 0
buffer_load_dwordx4 v19,s[40:43],s55 offen offset:0  lds
s_add_u32 m0,s57,89984
s_nop 0
buffer_load_dwordx4 v18,s[40:43],s55 offen offset:128  lds
s_add_u32 m0,s57,91008
s_nop 0
buffer_load_dwordx4 v19,s[40:43],s55 offen offset:128  lds
s_waitcnt vmcnt(24)
s_barrier
v_readfirstlane_b32 s53,v4
s_nop 3
s_add_u32 s53,s53,1
s_cmp_eq_u32 s53,0
s_cselect_b32 s53,1,s53
v_mov_b32 v4,s53
s_waitcnt vmcnt(20) lgkmcnt(0)
s_barrier
s_add_u32 s55,s50,2752512
s_add_u32 m0,s57,98304
s_nop 0
buffer_load_dwordx4 v18,s[40:43],s55 offen offset:0  lds
s_add_u32 m0,s57,99328
s_nop 0
buffer_load_dwordx4 v19,s[40:43],s55 offen offset:0  lds
s_add_u32 m0,s57,106368
s_nop 0
buffer_load_dwordx4 v18,s[40:43],s55 offen offset:128  lds
s_add_u32 m0,s57,107392
s_nop 0
buffer_load_dwordx4 v19,s[40:43],s55 offen offset:128  lds
v_add_u32 v26,0,v20
v_add_u32 v27,0,v21
T a[0:1],26,0
T a[2:3],26,512
T a[4:5],27,0
T a[6:7],27,512
T a[8:9],26,4096
T a[10:11],26,4608
T a[12:13],27,4096
T a[14:15],27,4608
s_waitcnt vmcnt(20) lgkmcnt(0)
s_barrier
s_add_u32 s55,s50,3211264
s_add_u32 m0,s57,0
s_nop 0
buffer_load_dwordx4 v18,s[40:43],s55 offen offset:0  lds
s_add_u32 m0,s57,1024
s_nop 0
buffer_load_dwordx4 v19,s[40:43],s55 offen offset:0  lds
s_add_u32 m0,s57,8064
s_nop 0
buffer_load_dwordx4 v18,s[40:43],s55 offen offset:128  lds
s_add_u32 m0,s57,9088
s_nop 0
buffer_load_dwordx4 v19,s[40:43],s55 offen offset:128  lds
v_add_u32 v26,16384,v20
v_add_u32 v27,16384,v21
T a[16:17],26,0
T a[18:19],26,512
T a[20:21],27,0
T a[22:23],27,512
T a[24:25],26,4096
T a[26:27],26,4608
T a[28:29],27,4096
T a[30:31],27,4608
s_waitcnt vmcnt(20) lgkmcnt(0)
s_barrier
s_add_u32 s55,s50,3670016
s_add_u32 m0,s57,16384
s_nop 0
buffer_load_dwordx4 v18,s[40:43],s55 offen offset:0  lds
s_add_u32 m0,s57,17408
s_nop 0
buffer_load_dwordx4 v19,s[40:43],s55 offen offset:0  lds
s_add_u32 m0,s57,24448
s_nop 0
buffer_load_dwordx4 v18,s[40:43],s55 offen offset:128  lds
s_add_u32 m0,s57,25472
s_nop 0
buffer_load_dwordx4 v19,s[40:43],s55 offen offset:128  lds
v_add_u32 v26,32768,v20
v_add_u32 v27,32768,v21
T a[32:33],26,0
T a[34:35],26,512
T a[36:37],27,0
T a[38:39],27,512
T a[40:41],26,4096
T a[42:43],26,4608
T a[44:45],27,4096
T a[46:47],27,4608
s_waitcnt vmcnt(20) lgkmcnt(0)
s_barrier
s_add_u32 s55,s50,4128768
s_add_u32 m0,s57,32768
s_nop 0
buffer_load_dwordx4 v18,s[40:43],s55 offen offset:0  lds
s_add_u32 m0,s57,33792
s_nop 0
buffer_load_dwordx4 v19,s[40:43],s55 offen offset:0  lds
s_add_u32 m0,s57,40832
s_nop 0
buffer_load_dwordx4 v18,s[40:43],s55 offen offset:128  lds
s_add_u32 m0,s57,41856
s_nop 0
buffer_load_dwordx4 v19,s[40:43],s55 offen offset:128  lds
v_add_u32 v26,49152,v20
v_add_u32 v27,49152,v21
T a[48:49],26,0
T a[50:51],26,512
T a[52:53],27,0
T a[54:55],27,512
T a[56:57],26,4096
T a[58:59],26,4608
T a[60:61],27,4096
T a[62:63],27,4608
s_waitcnt vmcnt(20) lgkmcnt(0)
s_barrier
s_add_u32 s55,s50,4587520
s_add_u32 m0,s57,49152
s_nop 0
buffer_load_dwordx4 v18,s[40:43],s55 offen offset:0  lds
s_add_u32 m0,s57,50176
s_nop 0
buffer_load_dwordx4 v19,s[40:43],s55 offen offset:0  lds
s_add_u32 m0,s57,57216
s_nop 0
buffer_load_dwordx4 v18,s[40:43],s55 offen offset:128  lds
s_add_u32 m0,s57,58240
s_nop 0
buffer_load_dwordx4 v19,s[40:43],s55 offen offset:128  lds
v_add_u32 v26,65536,v20
v_add_u32 v27,65536,v21
T a[64:65],26,0
T a[66:67],26,512
T a[68:69],27,0
T a[70:71],27,512
T a[72:73],26,4096
T a[74:75],26,4608
T a[76:77],27,4096
T a[78:79],27,4608
s_waitcnt vmcnt(20) lgkmcnt(0)
s_barrier
s_add_u32 s55,s50,5046272
s_add_u32 m0,s57,65536
s_nop 0
buffer_load_dwordx4 v18,s[40:43],s55 offen offset:0  lds
s_add_u32 m0,s57,66560
s_nop 0
buffer_load_dwordx4 v19,s[40:43],s55 offen offset:0  lds
s_add_u32 m0,s57,73600
s_nop 0
buffer_load_dwordx4 v18,s[40:43],s55 offen offset:128  lds
s_add_u32 m0,s57,74624
s_nop 0
buffer_load_dwordx4 v19,s[40:43],s55 offen offset:128  lds
v_add_u32 v26,81920,v20
v_add_u32 v27,81920,v21
T a[80:81],26,0
T a[82:83],26,512
T a[84:85],27,0
T a[86:87],27,512
T a[88:89],26,4096
T a[90:91],26,4608
T a[92:93],27,4096
T a[94:95],27,4608
s_waitcnt vmcnt(20) lgkmcnt(0)
s_barrier
s_add_u32 s55,s50,5505024
s_add_u32 m0,s57,81920
s_nop 0
buffer_load_dwordx4 v18,s[40:43],s55 offen offset:0  lds
s_add_u32 m0,s57,82944
s_nop 0
buffer_load_dwordx4 v19,s[40:43],s55 offen offset:0  lds
s_add_u32 m0,s57,89984
s_nop 0
buffer_load_dwordx4 v18,s[40:43],s55 offen offset:128  lds
s_add_u32 m0,s57,91008
s_nop 0
buffer_load_dwordx4 v19,s[40:43],s55 offen offset:128  lds
v_add_u32 v26,98304,v20
v_add_u32 v27,98304,v21
T a[96:97],26,0
T a[98:99],26,512
T a[100:101],27,0
T a[102:103],27,512
T a[104:105],26,4096
T a[106:107],26,4608
T a[108:109],27,4096
T a[110:111],27,4608
s_waitcnt vmcnt(20) lgkmcnt(0)
s_barrier
s_add_u32 s55,s50,5963776
s_add_u32 m0,s57,98304
s_nop 0
buffer_load_dwordx4 v18,s[40:43],s55 offen offset:0  lds
s_add_u32 m0,s57,99328
s_nop 0
buffer_load_dwordx4 v19,s[40:43],s55 offen offset:0  lds
s_add_u32 m0,s57,106368
s_nop 0
buffer_load_dwordx4 v18,s[40:43],s55 offen offset:128  lds
s_add_u32 m0,s57,107392
s_nop 0
buffer_load_dwordx4 v19,s[40:43],s55 offen offset:128  lds
v_add_u32 v26,0,v20
v_add_u32 v27,0,v21
T a[112:113],26,0
T a[114:115],26,512
T a[116:117],27,0
T a[118:119],27,512
T a[120:121],26,4096
T a[122:123],26,4608
T a[124:125],27,4096
T a[126:127],27,4608
s_waitcnt vmcnt(20) lgkmcnt(0)
s_barrier
s_add_u32 s55,s50,6422528
s_add_u32 m0,s57,0
s_nop 0
buffer_load_dwordx4 v18,s[40:43],s55 offen offset:0  lds
s_add_u32 m0,s57,1024
s_nop 0
buffer_load_dwordx4 v19,s[40:43],s55 offen offset:0  lds
s_add_u32 m0,s57,8064
s_nop 0
buffer_load_dwordx4 v18,s[40:43],s55 offen offset:128  lds
s_add_u32 m0,s57,9088
s_nop 0
buffer_load_dwordx4 v19,s[40:43],s55 offen offset:128  lds
v_add_u32 v26,16384,v20
v_add_u32 v27,16384,v21
T a[128:129],26,0
T a[130:131],26,512
T a[132:133],27,0
T a[134:135],27,512
T a[136:137],26,4096
T a[138:139],26,4608
T a[140:141],27,4096
T a[142:143],27,4608
s_waitcnt vmcnt(20) lgkmcnt(0)
s_barrier
s_add_u32 s55,s50,6881280
s_add_u32 m0,s57,16384
s_nop 0
buffer_load_dwordx4 v18,s[40:43],s55 offen offset:0  lds
s_add_u32 m0,s57,17408
s_nop 0
buffer_load_dwordx4 v19,s[40:43],s55 offen offset:0  lds
s_add_u32 m0,s57,24448
s_nop 0
buffer_load_dwordx4 v18,s[40:43],s55 offen offset:128  lds
s_add_u32 m0,s57,25472
s_nop 0
buffer_load_dwordx4 v19,s[40:43],s55 offen offset:128  lds
v_add_u32 v26,32768,v20
v_add_u32 v27,32768,v21
T a[144:145],26,0
T a[146:147],26,512
T a[148:149],27,0
T a[150:151],27,512
T a[152:153],26,4096
T a[154:155],26,4608
T a[156:157],27,4096
T a[158:159],27,4608
s_waitcnt vmcnt(20) lgkmcnt(0)
s_barrier
v_add_u32 v26,49152,v20
v_add_u32 v27,49152,v21
T a[160:161],26,0
T a[162:163],26,512
T a[164:165],27,0
T a[166:167],27,512
T a[168:169],26,4096
T a[170:171],26,4608
T a[172:173],27,4096
T a[174:175],27,4608
s_waitcnt vmcnt(16) lgkmcnt(0)
s_barrier
v_add_u32 v26,65536,v20
v_add_u32 v27,65536,v21
T a[176:177],26,0
T a[178:179],26,512
T a[180:181],27,0
T a[182:183],27,512
T a[184:185],26,4096
T a[186:187],26,4608
T a[188:189],27,4096
T a[190:191],27,4608
s_waitcnt vmcnt(12) lgkmcnt(0)
s_barrier
v_add_u32 v26,81920,v20
v_add_u32 v27,81920,v21
T a[192:193],26,0
T a[194:195],26,512
T a[196:197],27,0
T a[198:199],27,512
T a[200:201],26,4096
T a[202:203],26,4608
T a[204:205],27,4096
T a[206:207],27,4608
s_waitcnt vmcnt(8) lgkmcnt(0)
s_barrier
v_add_u32 v26,98304,v20
v_add_u32 v27,98304,v21
T a[208:209],26,0
T a[210:211],26,512
T a[212:213],27,0
T a[214:215],27,512
T a[216:217],26,4096
T a[218:219],26,4608
T a[220:221],27,4096
T a[222:223],27,4608
s_waitcnt vmcnt(4) lgkmcnt(0)
s_barrier
v_add_u32 v26,0,v20
v_add_u32 v27,0,v21
T a[224:225],26,0
T a[226:227],26,512
T a[228:229],27,0
T a[230:231],27,512
T a[232:233],26,4096
T a[234:235],26,4608
T a[236:237],27,4096
T a[238:239],27,4608
s_waitcnt vmcnt(0) lgkmcnt(0)
s_barrier
v_add_u32 v26,16384,v20
v_add_u32 v27,16384,v21
T a[240:241],26,0
T a[242:243],26,512
T a[244:245],27,0
T a[246:247],27,512
T a[248:249],26,4096
T a[250:251],26,4608
T a[252:253],27,4096
T a[254:255],27,4608
L 0
s_barrier
v_mov_b32 v34,0x3f803f80
v_mov_b32 v35,0x3f803f80
v_mov_b32 v36,0x3f803f80
v_mov_b32 v37,0x3f803f80
s_nop 1
v_mfma_f32_16x16x32_bf16 v[106:109],a[0:3],v[34:37],0
v_mfma_f32_16x16x32_bf16 v[110:113],a[4:7],v[34:37],0
M 106,a[8:11],34
M 110,a[12:15],34
M 106,a[16:19],34
M 110,a[20:23],34
M 106,a[24:27],34
M 110,a[28:31],34
M 106,a[32:35],34
M 110,a[36:39],34
M 106,a[40:43],34
M 110,a[44:47],34
M 106,a[48:51],34
M 110,a[52:55],34
M 106,a[56:59],34
M 110,a[60:63],34
M 106,a[64:67],34
M 110,a[68:71],34
M 106,a[72:75],34
M 110,a[76:79],34
M 106,a[80:83],34
M 110,a[84:87],34
M 106,a[88:91],34
M 110,a[92:95],34
M 106,a[96:99],34
M 110,a[100:103],34
M 106,a[104:107],34
M 110,a[108:111],34
M 106,a[112:115],34
M 110,a[116:119],34
M 106,a[120:123],34
M 110,a[124:127],34
M 106,a[128:131],34
M 110,a[132:135],34
M 106,a[136:139],34
)"
R"(M 110,a[140:143],34
M 106,a[144:147],34
M 110,a[148:151],34
M 106,a[152:155],34
M 110,a[156:159],34
M 106,a[160:163],34
M 110,a[164:167],34
M 106,a[168:171],34
M 110,a[172:175],34
M 106,a[176:179],34
M 110,a[180:183],34
M 106,a[184:187],34
M 110,a[188:191],34
M 106,a[192:195],34
M 110,a[196:199],34
M 106,a[200:203],34
M 110,a[204:207],34
M 106,a[208:211],34
M 110,a[212:215],34
M 106,a[216:219],34
M 110,a[220:223],34
M 106,a[224:227],34
M 110,a[228:231],34
M 106,a[232:235],34
M 110,a[236:239],34
M 106,a[240:243],34
M 110,a[244:247],34
M 106,a[248:251],34
M 110,a[252:255],34
v_mov_b32 v4,s53
s_mov_b32 s82,0
s_mov_b32 s80,0
s_mov_b32 s54,s45
s_mul_i32 s57,s44,4096
s_mul_i32 s55,s45,32768
s_add_u32 s55,s55,s57
s_add_u32 s55,s55,31457280
s_mul_i32 s57,s44,4096
s_mul_i32 s60,s46,32768
s_mul_i32 s61,s46,1024
s_mul_i32 s62,s46,114688
s_mul_i32 s63,s46,2560
s_mul_i32 s64,s46,1024
s_lshl_b32 s59,s44,8
s_add_u32 s59,s59,131072
s_mul_i32 s58,s45,1024
s_add_u32 s58,s58,109613056
s_mul_i32 s65,s45,114688
s_add_u32 s65,s65,s47
s_mul_i32 s68,s45,2560
s_add_u32 s68,s68,s48
s_mul_i32 s73,s45,1024
s_add_u32 s73,s73,107479040
s_mul_i32 s74,s45,32768
s_add_u32 s74,s74,s49
s_mul_i32 s75,s45,1024
s_add_u32 s75,s75,107479040
s_mov_b32 s66,0x7fff0000
s_mov_b32 s67,0x7fff0000
s_mov_b32 s69,0x7fff0000
s_mov_b32 s76,0x7fff0000
s_mov_b32 s77,0x7fff0000
s_mov_b32 s70,0x7fff0000
s_mov_b32 s71,0x7fff0000
s_mov_b32 s72,0x7fff0000
v_mov_b32 v1,v0
v_add_u32 v2,16384,v0
s_mov_b32 s84,16384
s_mov_b32 s86,0x3a800000
s_mov_b32 s87,0x3727c5ac
s_mov_b32 s88,0xc0135761
s_mov_b32 s89,0x3d372713
s_lshr_b32 s85,s44,1
s_mul_i32 s85,s85,127
v_lshlrev_b32 v8,22,v0
v_and_b32 v8,0x80000000,v8
v_or_b32 v8,v8,v5
v_add_u32 v2,32768,v0
s_lshl_b32 s84,s57,1
s_add_u32 s84,s84,32768
s_getpc_b64 s[40:41]
Lg3o_pc_%=:
s_lshl_b32 s78,s44,13
v_lshlrev_b32 v114,3,v0
v_add_u32 v114,s78,v114
v_min_u32 v114,Lcode_g3o_e-Lg3o_pc_%=-4,v114
s_mov_b32 m0,163584
s_nop 0
global_load_lds_dword v114,s[40:41]
s_add_u32 m0,s59,4096
s_nop 0
buffer_load_dword v8,s[36:39],s58 offen offset:0 sc1 lds
buffer_store_dword v4,v3,s[36:39],s71 offen offset:0 sc0 sc1
s_add_u32 m0,s59,5120
s_nop 0
buffer_load_dword v8,s[36:39],s58 offen offset:0 sc1 lds
buffer_store_dword v4,v3,s[36:39],s71 offen offset:0 sc0 sc1
s_mov_b32 s83,0
Lg3o_poll_%=:
s_add_u32 s78,s83,2
s_and_b32 s78,s78,3
s_lshl_b32 s78,s78,10
s_add_u32 s78,s78,4096
s_add_u32 m0,s59,s78
s_nop 0
buffer_load_dword v8,s[36:39],s58 offen offset:0 sc1 lds
s_and_b32 s78,s83,3
s_lshl_b32 s78,s78,10
v_add_u32 v114,s78,v6
s_waitcnt vmcnt(4)
s_barrier
ds_read_b32 v7,v114 offset:4096
buffer_store_dword v4,v3,s[36:39],s70 offen offset:0 sc0 sc1
s_mov_b32 s70,s71
s_mov_b32 s71,s72
s_mov_b32 s72,0x7fff0000
s_add_u32 s83,s83,1
s_add_u32 s82,s82,1
L 0
v_cmp_eq_u32 vcc,s53,v7
s_nop 0
s_cmp_eq_u32 vcc_lo,-1
s_cbranch_scc1 Lg3o_go_%=
s_cmp_lt_u32 s82,20000
s_cbranch_scc1 Lg3o_poll_%=
Lg3o_go_%=:
buffer_load_dword v130,v11,s[36:39],s73 offen offset:0 sc1
s_add_u32 s73,s73,s64
s_add_u32 m0,s57,0
s_add_u32 s56,s55,0
D 0,56,0
D 0,56,1024
D 0,56,2048
D 0,56,3072
s_add_u32 s58,s58,s61
s_add_u32 m0,s59,2048
s_nop 0
buffer_load_dword v8,s[36:39],s58 offen offset:0 sc1 lds
s_add_u32 m0,s57,16384
s_add_u32 s56,s55,16384
D 0,56,0
D 0,56,1024
D 0,56,2048
D 0,56,3072
s_add_u32 s55,s55,s60
s_sub_u32 s57,s84,s57
s_add_u32 s54,s54,s46
s_waitcnt vmcnt(4)
s_barrier
buffer_store_dword v4,v3,s[36:39],s70 offen offset:0 sc0 sc1
s_min_u32 s71,s71,s72
buffer_store_dword v4,v3,s[36:39],s71 offen offset:0 sc0 sc1
s_mov_b32 s70,0x7fff0000
s_mov_b32 s71,0x7fff0000
s_mov_b32 s72,0x7fff0000
s_cmp_eq_u32 s80,1
s_cbranch_scc1 Lg3o_st1_%=
s_branch Lg3o_st0_%=
Lg3o_gox_%=:
s_add_u32 s58,s58,s61
s_add_u32 m0,s59,2048
s_nop 0
buffer_load_dword v8,s[36:39],s58 offen offset:0 sc1 lds
s_waitcnt vmcnt(0)
s_barrier
buffer_store_dword v4,v3,s[36:39],s70 offen offset:0 sc0 sc1
s_min_u32 s71,s71,s72
buffer_store_dword v4,v3,s[36:39],s71 offen offset:0 sc0 sc1
s_mov_b32 s70,0x7fff0000
s_mov_b32 s71,0x7fff0000
s_mov_b32 s72,0x7fff0000
s_cmp_eq_u32 s80,1
s_cbranch_scc1 Lg3o_st1_%=
Lg3o_st0_%=:
ds_read_b32 v7,v6 offset:2048
R 34,1,0
R 38,1,1024
R 42,1,2048
R 46,1,3072
R 50,1,4096
L 5
v_cmp_eq_u32 vcc,s53,v7
L 3
s_cmp_eq_u32 vcc_lo,-1
s_cbranch_scc1 Lg3o_F0_%=
s_branch Lg3o_L0_%=
Lg3o_st1_%=:
ds_read_b32 v7,v6 offset:2048
R 34,2,0
R 38,2,1024
R 42,2,2048
R 46,2,3072
R 50,2,4096
L 5
v_cmp_eq_u32 vcc,s53,v7
L 3
s_cmp_eq_u32 vcc_lo,-1
s_cbranch_scc1 Lg3o_F1_%=
s_branch Lg3o_L1_%=
Lg3o_F0_%=:
v_mfma_f32_16x16x32_bf16 v[22:25],a[4:7],v[34:37],0
s_add_u32 s58,s58,s61
s_add_u32 m0,s59,0
R 54,1,5120
M 22,a[12:15],38
s_add_u32 s56,s55,0
buffer_load_dword v8,s[36:39],s58 offen offset:0 sc1 lds
v_mfma_f32_16x16x32_bf16 v[18:21],a[0:3],v[34:37],0
s_add_u32 m0,s57,0
R 58,1,6144
v_add_f32_dpp v131,v130,v130 quad_perm:[1,0,3,2] row_mask:0xf bank_mask:0xf
M 18,a[8:11],38
L 3
D 0,56,0
M 18,a[16:19],42
s_mov_b32 s66,s65
R 62,1,7168
v_add_f32_dpp v131,v131,v131 quad_perm:[2,3,0,1] row_mask:0xf bank_mask:0xf
M 18,a[24:27],46
s_add_u32 s65,s65,s62
buffer_load_dword v130,v11,s[36:39],s73 offen offset:0 sc1
M 22,a[20:23],42
s_add_u32 s54,s54,s46
R 66,1,8192
v_add_f32_dpp v134,v131,v131 row_half_mirror row_mask:0xf bank_mask:0xf
M 22,a[28:31],46
L 3
D 0,56,1024
M 22,a[36:39],50
s_add_u32 s73,s73,s64
R 70,1,9216
v_mov_b32_dpp v131,v134 row_ror:8 row_mask:0xf bank_mask:0xf
M 22,a[44:47],54
v_fma_f32 v131,-v134,v134,v131
v_max_f32 v131,s87,v131
M 18,a[32:35],50
R 74,1,10240
v_fma_f32 v26,-v102,v106,v26
M 18,a[40:43],54
L 3
D 0,56,2048
M 18,a[48:51],58
R 78,1,11264
v_fma_f32 v27,-v102,v107,v27
M 18,a[56:59],62
D 0,56,3072
M 22,a[52:55],58
s_add_u32 m0,s57,16384
R 82,1,12288
v_fma_f32 v28,-v102,v108,v28
M 22,a[60:63],62
L 3
v_rsq_f32 v132,v131
M 22,a[68:71],66
s_add_u32 s56,s55,16384
R 86,1,13312
v_mul_f32 v137,v131,v132
M 22,a[76:79],70
s_add_u32 s55,s55,s60
v_mul_f32 v135,s88,v132
v_mul_f32 v131,s89,v132
M 18,a[64:67],66
s_sub_u32 s57,s84,s57
R 90,1,14336
v_mul_f32 v136,v135,v132
M 18,a[72:75],70
L 3
v_mul_f32 v136,v136,v131
v_fma_f32 v29,-v102,v109,v29
M 18,a[80:83],74
s_waitcnt vmcnt(8)
R 94,1,15360
v_mul_f32 v114,v26,v26
M 18,a[88:91],78
s_barrier
ds_write_b128 v12,v[134:137] offset:0
M 22,a[84:87],74
R 34,1,16384
v_mul_f32 v115,v27,v27
M 22,a[92:95],78
L 4
v_mul_f32 v116,v28,v28
v_mul_f32 v117,v29,v29
M 22,a[100:103],82
R 38,1,17408
v_fma_f32 v114,v104,v114,v103
M 22,a[108:111],86
D 0,56,0
M 18,a[96:99],82
R 42,1,18432
v_fma_f32 v115,v104,v115,v103
M 18,a[104:107],86
L 4
v_fma_f32 v116,v104,v116,v103
v_fma_f32 v117,v104,v117,v103
M 18,a[112:115],90
R 46,1,19456
v_mul_f32 v114,v26,v114
M 18,a[120:123],94
v_exp_f32 v114,v114
M 22,a[116:119],90
R 50,1,20480
v_mul_f32 v115,v27,v115
M 22,a[124:127],94
L 3
v_exp_f32 v115,v115
M 22,a[132:135],34
R 54,1,21504
v_mul_f32 v116,v28,v116
M 22,a[140:143],38
D 0,56,1024
M 18,a[128:131],34
R 58,1,22528
v_mul_f32 v117,v29,v117
M 18,a[136:139],38
L 3
v_exp_f32 v116,v116
M 18,a[144:147],42
R 62,1,23552
v_fma_f32 v114,v114,v105,v105
M 18,a[152:155],46
v_exp_f32 v117,v117
M 22,a[148:151],42
R 66,1,24576
v_fma_f32 v115,v115,v105,v105
M 22,a[156:159],46
L 3
D 0,56,2048
M 22,a[164:167],50
R 70,1,25600
v_fma_f32 v116,v116,v105,v105
M 22,a[172:175],54
v_rcp_f32 v114,v114
M 18,a[160:163],50
R 74,1,26624
v_fma_f32 v117,v117,v105,v105
M 18,a[168:171],54
L 3
v_rcp_f32 v115,v115
M 18,a[176:179],58
R 78,1,27648
v_mul_f32 v26,v26,v114
M 18,a[184:187],62
v_rcp_f32 v116,v116
M 22,a[180:183],58
R 82,1,28672
v_mul_f32 v27,v27,v115
M 22,a[188:191],62
L 3
D 0,56,3072
M 22,a[196:199],66
R 86,1,29696
v_mul_f32 v28,v28,v116
M 22,a[204:207],70
v_rcp_f32 v117,v117
M 18,a[192:195],66
R 90,1,30720
v_mul_f32 v29,v29,v117
M 18,a[200:203],70
L 3
s_nop 0
v_cvt_pk_bf16_f32 v122,v26,v27
M 18,a[208:211],74
s_waitcnt vmcnt(4)
R 94,1,31744
v_fma_f32 v30,-v102,v110,v30
M 18,a[216:219],78
s_barrier
ds_read_b32 v7,v6 offset:0
R 98,13,0
M 22,a[212:215],74
R 34,2,0
v_fma_f32 v31,-v102,v111,v31
M 22,a[220:223],78
L 5
buffer_store_dword v4,v3,s[36:39],s70 offen offset:0 sc0 sc1
M 22,a[228:231],82
s_mov_b32 s70,s69
R 38,2,1024
v_fma_f32 v32,-v102,v112,v32
M 22,a[236:239],86
s_mov_b32 s69,s68
v_fma_f32 v33,-v102,v113,v33
v_mul_f32 v118,v30,v30
M 18,a[224:227],82
s_add_u32 s68,s68,s63
R 42,2,2048
v_mul_f32 v119,v31,v31
M 18,a[232:235],86
L 5
v_mul_f32 v120,v32,v32
v_mul_f32 v121,v33,v33
v_fma_f32 v118,v104,v118,v103
v_fma_f32 v119,v104,v119,v103
v_fma_f32 v120,v104,v120,v103
v_fma_f32 v121,v104,v121,v103
v_mul_f32 v118,v30,v118
v_mul_f32 v119,v31,v119
v_mul_f32 v120,v32,v120
v_mul_f32 v121,v33,v121
v_exp_f32 v118,v118
v_exp_f32 v119,v119
v_exp_f32 v120,v120
v_exp_f32 v121,v121
M 18,a[240:243],90
L 4
R 46,2,3072
v_fma_f32 v118,v118,v105,v105
v_fma_f32 v119,v119,v105,v105
v_fma_f32 v120,v120,v105,v105
v_fma_f32 v121,v121,v105,v105
v_rcp_f32 v118,v118
v_rcp_f32 v119,v119
v_rcp_f32 v120,v120
v_rcp_f32 v121,v121
M 18,a[248:251],94
v_cvt_pk_bf16_f32 v123,v28,v29
v_mul_f32 v30,v30,v118
v_mul_f32 v31,v31,v119
v_mul_f32 v32,v32,v120
v_mul_f32 v33,v33,v121
v_cvt_pk_bf16_f32 v124,v30,v31
v_cvt_pk_bf16_f32 v125,v32,v33
v_cmp_eq_u32 vcc,s53,v7
M 22,a[244:247],90
s_cmp_eq_u32 vcc_lo,-1
R 50,2,4096
buffer_store_dwordx4 v[122:125],v0,s[36:39],s67 offen offset:0 sc0 sc1
M 22,a[252:255],94
L 3
s_cbranch_scc0 Lg3o_L1_%=
Lg3o_F1_%=:
v_mfma_f32_16x16x32_bf16 v[30:33],a[4:7],v[34:37],0
s_add_u32 s58,s58,s61
s_add_u32 m0,s59,1024
R 54,2,5120
M 30,a[12:15],38
s_add_u32 s56,s55,0
buffer_load_dword v8,s[36:39],s58 offen offset:0 sc1 lds
v_mfma_f32_16x16x32_bf16 v[26:29],a[0:3],v[34:37],0
s_add_u32 m0,s57,0
R 58,2,6144
v_add_f32_dpp v131,v130,v130 quad_perm:[1,0,3,2] row_mask:0xf bank_mask:0xf
M 26,a[8:11],38
L 3
D 0,56,0
M 26,a[16:19],42
s_mov_b32 s67,s65
R 62,2,7168
v_add_f32_dpp v131,v131,v131 quad_perm:[2,3,0,1] row_mask:0xf bank_mask:0xf
M 26,a[24:27],46
s_add_u32 s65,s65,s62
buffer_load_dword v130,v11,s[36:39],s73 offen offset:0 sc1
M 30,a[20:23],42
s_add_u32 s54,s54,s46
R 66,2,8192
v_add_f32_dpp v134,v131,v131 row_half_mirror row_mask:0xf bank_mask:0xf
M 30,a[28:31],46
L 3
D 0,56,1024
M 30,a[36:39],50
s_add_u32 s73,s73,s64
R 70,2,9216
v_mov_b32_dpp v131,v134 row_ror:8 row_mask:0xf bank_mask:0xf
M 30,a[44:47],54
v_fma_f32 v131,-v134,v134,v131
v_max_f32 v131,s87,v131
M 26,a[32:35],50
R 74,2,10240
v_fma_f32 v18,-v98,v106,v18
M 26,a[40:43],54
L 3
D 0,56,2048
M 26,a[48:51],58
R 78,2,11264
v_fma_f32 v19,-v98,v107,v19
M 26,a[56:59],62
D 0,56,3072
M 30,a[52:55],58
s_add_u32 m0,s57,16384
R 82,2,12288
v_fma_f32 v20,-v98,v108,v20
M 30,a[60:63],62
L 3
v_rsq_f32 v132,v131
M 30,a[68:71],66
s_add_u32 s56,s55,16384
R 86,2,13312
v_mul_f32 v137,v131,v132
M 30,a[76:79],70
s_add_u32 s55,s55,s60
v_mul_f32 v135,s88,v132
v_mul_f32 v131,s89,v132
M 26,a[64:67],66
s_sub_u32 s57,s84,s57
R 90,2,14336
v_mul_f32 v136,v135,v132
M 26,a[72:75],70
L 3
v_mul_f32 v136,v136,v131
)"
R"(v_fma_f32 v21,-v98,v109,v21
M 26,a[80:83],74
s_waitcnt vmcnt(8)
R 94,2,15360
v_mul_f32 v114,v18,v18
M 26,a[88:91],78
s_barrier
ds_write_b128 v12,v[134:137] offset:2048
M 30,a[84:87],74
R 34,2,16384
v_mul_f32 v115,v19,v19
M 30,a[92:95],78
L 4
v_mul_f32 v116,v20,v20
v_mul_f32 v117,v21,v21
M 30,a[100:103],82
R 38,2,17408
v_fma_f32 v114,v100,v114,v99
M 30,a[108:111],86
D 0,56,0
M 26,a[96:99],82
R 42,2,18432
v_fma_f32 v115,v100,v115,v99
M 26,a[104:107],86
L 4
v_fma_f32 v116,v100,v116,v99
v_fma_f32 v117,v100,v117,v99
M 26,a[112:115],90
R 46,2,19456
v_mul_f32 v114,v18,v114
M 26,a[120:123],94
v_exp_f32 v114,v114
M 30,a[116:119],90
R 50,2,20480
v_mul_f32 v115,v19,v115
M 30,a[124:127],94
L 3
v_exp_f32 v115,v115
M 30,a[132:135],34
R 54,2,21504
v_mul_f32 v116,v20,v116
M 30,a[140:143],38
D 0,56,1024
M 26,a[128:131],34
R 58,2,22528
v_mul_f32 v117,v21,v117
M 26,a[136:139],38
L 3
v_exp_f32 v116,v116
M 26,a[144:147],42
R 62,2,23552
v_fma_f32 v114,v114,v101,v101
M 26,a[152:155],46
v_exp_f32 v117,v117
M 30,a[148:151],42
R 66,2,24576
v_fma_f32 v115,v115,v101,v101
M 30,a[156:159],46
L 3
D 0,56,2048
M 30,a[164:167],50
R 70,2,25600
v_fma_f32 v116,v116,v101,v101
M 30,a[172:175],54
v_rcp_f32 v114,v114
M 26,a[160:163],50
R 74,2,26624
v_fma_f32 v117,v117,v101,v101
M 26,a[168:171],54
L 3
v_rcp_f32 v115,v115
M 26,a[176:179],58
R 78,2,27648
v_mul_f32 v18,v18,v114
M 26,a[184:187],62
v_rcp_f32 v116,v116
M 30,a[180:183],58
R 82,2,28672
v_mul_f32 v19,v19,v115
M 30,a[188:191],62
L 3
D 0,56,3072
M 30,a[196:199],66
R 86,2,29696
v_mul_f32 v20,v20,v116
M 30,a[204:207],70
v_rcp_f32 v117,v117
M 26,a[192:195],66
R 90,2,30720
v_mul_f32 v21,v21,v117
M 26,a[200:203],70
L 3
s_nop 0
v_cvt_pk_bf16_f32 v122,v18,v19
M 26,a[208:211],74
s_waitcnt vmcnt(4)
R 94,2,31744
v_fma_f32 v22,-v98,v110,v22
M 26,a[216:219],78
s_barrier
ds_read_b32 v7,v6 offset:1024
R 102,13,2048
M 30,a[212:215],74
R 34,1,0
v_fma_f32 v23,-v98,v111,v23
M 30,a[220:223],78
L 5
buffer_store_dword v4,v3,s[36:39],s70 offen offset:0 sc0 sc1
M 30,a[228:231],82
s_mov_b32 s70,s69
R 38,1,1024
v_fma_f32 v24,-v98,v112,v24
M 30,a[236:239],86
s_mov_b32 s69,s68
v_fma_f32 v25,-v98,v113,v25
v_mul_f32 v118,v22,v22
M 26,a[224:227],82
s_add_u32 s68,s68,s63
R 42,1,2048
v_mul_f32 v119,v23,v23
M 26,a[232:235],86
L 5
v_mul_f32 v120,v24,v24
v_mul_f32 v121,v25,v25
v_fma_f32 v118,v100,v118,v99
v_fma_f32 v119,v100,v119,v99
v_fma_f32 v120,v100,v120,v99
v_fma_f32 v121,v100,v121,v99
v_mul_f32 v118,v22,v118
v_mul_f32 v119,v23,v119
v_mul_f32 v120,v24,v120
v_mul_f32 v121,v25,v121
v_exp_f32 v118,v118
v_exp_f32 v119,v119
v_exp_f32 v120,v120
v_exp_f32 v121,v121
M 26,a[240:243],90
L 4
R 46,1,3072
v_fma_f32 v118,v118,v101,v101
v_fma_f32 v119,v119,v101,v101
v_fma_f32 v120,v120,v101,v101
v_fma_f32 v121,v121,v101,v101
v_rcp_f32 v118,v118
v_rcp_f32 v119,v119
v_rcp_f32 v120,v120
v_rcp_f32 v121,v121
M 26,a[248:251],94
v_cvt_pk_bf16_f32 v123,v20,v21
v_mul_f32 v22,v22,v118
v_mul_f32 v23,v23,v119
v_mul_f32 v24,v24,v120
v_mul_f32 v25,v25,v121
v_cvt_pk_bf16_f32 v124,v22,v23
v_cvt_pk_bf16_f32 v125,v24,v25
v_cmp_eq_u32 vcc,s53,v7
M 30,a[244:247],90
s_cmp_eq_u32 vcc_lo,-1
R 50,1,4096
buffer_store_dwordx4 v[122:125],v0,s[36:39],s66 offen offset:0 sc0 sc1
M 30,a[252:255],94
L 3
s_cbranch_scc1 Lg3o_F0_%=
Lg3o_L0_%=:
v_mfma_f32_16x16x32_bf16 v[22:25],a[4:7],v[34:37],0
s_add_u32 m0,s59,3072
R 54,1,5120
v_add_f32_dpp v131,v130,v130 quad_perm:[1,0,3,2] row_mask:0xf bank_mask:0xf
M 22,a[12:15],38
s_mov_b32 s66,s65
buffer_load_dword v8,s[36:39],s58 offen offset:0 sc1 lds
v_mfma_f32_16x16x32_bf16 v[18:21],a[0:3],v[34:37],0
s_add_u32 s65,s65,s62
R 58,1,6144
v_add_f32_dpp v131,v131,v131 quad_perm:[2,3,0,1] row_mask:0xf bank_mask:0xf
M 18,a[8:11],38
L 3
v_fma_f32 v26,-v102,v106,v26
v_fma_f32 v27,-v102,v107,v27
M 18,a[16:19],42
R 62,1,7168
v_add_f32_dpp v134,v131,v131 row_half_mirror row_mask:0xf bank_mask:0xf
M 18,a[24:27],46
v_fma_f32 v28,-v102,v108,v28
v_fma_f32 v29,-v102,v109,v29
M 22,a[20:23],42
R 66,1,8192
v_mov_b32_dpp v131,v134 row_ror:8 row_mask:0xf bank_mask:0xf
M 22,a[28:31],46
L 3
v_fma_f32 v131,-v134,v134,v131
v_max_f32 v131,s87,v131
M 22,a[36:39],50
R 70,1,9216
v_mul_f32 v114,v26,v26
M 22,a[44:47],54
v_rsq_f32 v132,v131
M 18,a[32:35],50
R 74,1,10240
v_mul_f32 v137,v131,v132
M 18,a[40:43],54
L 3
v_mul_f32 v135,s88,v132
v_mul_f32 v131,s89,v132
M 18,a[48:51],58
R 78,1,11264
v_mul_f32 v136,v135,v132
M 18,a[56:59],62
v_mul_f32 v136,v136,v131
v_mul_f32 v115,v27,v27
M 22,a[52:55],58
R 82,1,12288
v_mul_f32 v116,v28,v28
M 22,a[60:63],62
L 3
ds_write_b128 v12,v[134:137] offset:0
M 22,a[68:71],66
R 86,1,13312
v_mul_f32 v117,v29,v29
M 22,a[76:79],70
v_fma_f32 v114,v104,v114,v103
v_fma_f32 v115,v104,v115,v103
M 18,a[64:67],66
R 90,1,14336
v_fma_f32 v116,v104,v116,v103
M 18,a[72:75],70
L 4
v_fma_f32 v117,v104,v117,v103
v_mul_f32 v114,v26,v114
M 18,a[80:83],74
s_waitcnt vmcnt(0)
R 94,1,15360
v_mul_f32 v115,v27,v115
M 18,a[88:91],78
s_barrier
ds_read_b32 v7,v6 offset:3072
v_mul_f32 v116,v28,v116
M 22,a[84:87],74
R 34,1,16384
v_mul_f32 v117,v29,v117
M 22,a[92:95],78
L 4
buffer_store_dword v4,v3,s[36:39],s70 offen offset:0 sc0 sc1
M 22,a[100:103],82
s_mov_b32 s70,0x7fff0000
R 38,1,17408
v_fma_f32 v30,-v102,v110,v30
M 22,a[108:111],86
v_exp_f32 v114,v114
M 18,a[96:99],82
R 42,1,18432
v_fma_f32 v114,v114,v105,v105
M 18,a[104:107],86
L 4
L 3
v_cmp_eq_u32 vcc,s53,v7
M 18,a[112:115],90
R 46,1,19456
v_fma_f32 v31,-v102,v111,v31
M 18,a[120:123],94
s_cmp_eq_u32 vcc_lo,-1
v_exp_f32 v115,v115
s_cbranch_scc0 Lg3o_X0_%=
M 22,a[116:119],90
s_add_u32 m0,s57,0
s_add_u32 s56,s55,0
s_add_u32 s58,s58,s61
R 50,1,20480
M 22,a[124:127],94
L 3
buffer_load_dword v130,v11,s[36:39],s73 offen offset:0 sc1
M 22,a[132:135],34
s_add_u32 s54,s54,s46
R 54,1,21504
v_fma_f32 v115,v115,v105,v105
M 22,a[140:143],38
s_add_u32 s73,s73,s64
D 0,56,0
M 18,a[128:131],34
R 58,1,22528
v_fma_f32 v32,-v102,v112,v32
M 18,a[136:139],38
L 3
v_exp_f32 v116,v116
M 18,a[144:147],42
R 62,1,23552
v_fma_f32 v116,v116,v105,v105
M 18,a[152:155],46
D 0,56,1024
M 22,a[148:151],42
R 66,1,24576
v_fma_f32 v33,-v102,v113,v33
M 22,a[156:159],46
L 3
v_exp_f32 v117,v117
M 22,a[164:167],50
R 70,1,25600
v_fma_f32 v117,v117,v105,v105
M 22,a[172:175],54
D 0,56,2048
M 18,a[160:163],50
R 74,1,26624
v_mul_f32 v118,v30,v30
M 18,a[168:171],54
L 3
v_rcp_f32 v114,v114
M 18,a[176:179],58
R 78,1,27648
v_mul_f32 v26,v26,v114
M 18,a[184:187],62
D 0,56,3072
M 22,a[180:183],58
s_add_u32 m0,s59,0
R 82,1,28672
v_mul_f32 v119,v31,v31
M 22,a[188:191],62
L 3
buffer_load_dword v8,s[36:39],s58 offen offset:0 sc1 lds
M 22,a[196:199],66
s_add_u32 m0,s57,16384
R 86,1,29696
v_mul_f32 v120,v32,v32
M 22,a[204:207],70
s_add_u32 s56,s55,16384
v_rcp_f32 v115,v115
M 18,a[192:195],66
s_add_u32 s55,s55,s60
R 90,1,30720
v_mul_f32 v27,v27,v115
M 18,a[200:203],70
L 3
s_nop 0
D 0,56,0
M 18,a[208:211],74
s_waitcnt vmcnt(1)
R 94,1,31744
v_cvt_pk_bf16_f32 v122,v26,v27
M 18,a[216:219],78
s_barrier
ds_read_b32 v7,v6 offset:0
R 98,13,0
M 22,a[212:215],74
s_sub_u32 s57,s84,s57
R 34,2,0
v_mul_f32 v121,v33,v33
M 22,a[220:223],78
L 5
buffer_store_dword v4,v3,s[36:39],s70 offen offset:0 sc0 sc1
M 22,a[228:231],82
s_mov_b32 s70,s69
R 38,2,1024
v_fma_f32 v118,v104,v118,v103
M 22,a[236:239],86
s_mov_b32 s69,s68
D 0,56,1024
M 18,a[224:227],82
s_add_u32 s68,s68,s63
R 42,2,2048
v_fma_f32 v119,v104,v119,v103
M 18,a[232:235],86
L 5
v_fma_f32 v120,v104,v120,v103
v_fma_f32 v121,v104,v121,v103
v_mul_f32 v118,v30,v118
v_mul_f32 v119,v31,v119
v_mul_f32 v120,v32,v120
v_mul_f32 v121,v33,v121
v_exp_f32 v118,v118
v_exp_f32 v119,v119
v_exp_f32 v120,v120
v_exp_f32 v121,v121
M 18,a[240:243],90
L 4
R 46,2,3072
v_rcp_f32 v116,v116
v_rcp_f32 v117,v117
v_fma_f32 v118,v118,v105,v105
v_fma_f32 v119,v119,v105,v105
v_fma_f32 v120,v120,v105,v105
v_fma_f32 v121,v121,v105,v105
v_rcp_f32 v118,v118
v_rcp_f32 v119,v119
v_rcp_f32 v120,v120
v_rcp_f32 v121,v121
M 18,a[248:251],94
v_mul_f32 v28,v28,v116
v_mul_f32 v29,v29,v117
v_cvt_pk_bf16_f32 v123,v28,v29
v_mul_f32 v30,v30,v118
v_mul_f32 v31,v31,v119
v_mul_f32 v32,v32,v120
v_mul_f32 v33,v33,v121
v_cvt_pk_bf16_f32 v124,v30,v31
v_cvt_pk_bf16_f32 v125,v32,v33
v_cmp_eq_u32 vcc,s53,v7
M 22,a[244:247],90
s_cmp_eq_u32 vcc_lo,-1
R 50,2,4096
D 0,56,3072
buffer_store_dwordx4 v[122:125],v0,s[36:39],s67 offen offset:0 sc0 sc1
M 22,a[252:255],94
L 3
D 0,56,2048
buffer_store_dword v4,v3,s[36:39],s71 offen offset:0 sc0 sc1
buffer_store_dword v4,v3,s[36:39],s71 offen offset:0 sc0 sc1
s_cbranch_scc1 Lg3o_F1_%=
s_branch Lg3o_L1_%=
Lg3o_X0_%=:
M 22,a[116:119],90
s_add_u32 m0,s59,0
R 50,1,20480
v_fma_f32 v115,v115,v105,v105
M 22,a[124:127],94
L 3
buffer_load_dword v8,s[36:39],s58 offen offset:0 sc1 lds
M 22,a[132:135],34
R 54,1,21504
v_fma_f32 v32,-v102,v112,v32
M 22,a[140:143],38
v_exp_f32 v116,v116
M 18,a[128:131],34
R 58,1,22528
v_fma_f32 v116,v116,v105,v105
M 18,a[136:139],38
L 3
v_exp_f32 v117,v117
M 18,a[144:147],42
R 62,1,23552
v_fma_f32 v117,v117,v105,v105
M 18,a[152:155],46
v_rcp_f32 v114,v114
M 22,a[148:151],42
R 66,1,24576
v_mul_f32 v26,v26,v114
M 22,a[156:159],46
L 3
v_rcp_f32 v115,v115
M 22,a[164:167],50
R 70,1,25600
v_mul_f32 v27,v27,v115
M 22,a[172:175],54
v_rcp_f32 v116,v116
M 18,a[160:163],50
R 74,1,26624
v_mul_f32 v28,v28,v116
M 18,a[168:171],54
L 3
v_rcp_f32 v117,v117
M 18,a[176:179],58
R 78,1,27648
v_mul_f32 v29,v29,v117
M 18,a[184:187],62
v_cvt_pk_bf16_f32 v122,v26,v27
v_cvt_pk_bf16_f32 v123,v28,v29
M 22,a[180:183],58
R 82,1,28672
v_fma_f32 v33,-v102,v113,v33
M 22,a[188:191],62
L 3
v_mul_f32 v118,v30,v30
v_mul_f32 v119,v31,v31
M 22,a[196:199],66
R 86,1,29696
v_mul_f32 v120,v32,v32
M 22,a[204:207],70
v_mul_f32 v121,v33,v33
v_fma_f32 v118,v104,v118,v103
M 18,a[192:195],66
R 90,1,30720
v_fma_f32 v119,v104,v119,v103
M 18,a[200:203],70
L 3
s_nop 0
v_fma_f32 v120,v104,v120,v103
M 18,a[208:211],74
s_waitcnt vmcnt(0)
R 94,1,31744
v_fma_f32 v121,v104,v121,v103
M 18,a[216:219],78
s_barrier
buffer_store_dword v4,v3,s[36:39],s70 offen offset:0 sc0 sc1
M 22,a[212:215],74
s_mov_b32 s70,s69
ds_read_b32 v7,v6 offset:0
R 98,13,0
M 22,a[220:223],78
L 4
v_mul_f32 v118,v30,v118
v_mul_f32 v119,v31,v119
M 22,a[228:231],82
s_mov_b32 s69,s68
v_mul_f32 v120,v32,v120
v_mul_f32 v121,v33,v121
M 22,a[236:239],86
s_add_u32 s68,s68,s63
v_exp_f32 v118,v118
M 18,a[224:227],82
v_exp_f32 v119,v119
M 18,a[232:235],86
L 2
)"
R"(v_exp_f32 v120,v120
v_exp_f32 v121,v121
M 18,a[240:243],90
L 1
v_fma_f32 v118,v118,v105,v105
v_fma_f32 v119,v119,v105,v105
v_fma_f32 v120,v120,v105,v105
v_fma_f32 v121,v121,v105,v105
v_rcp_f32 v118,v118
v_rcp_f32 v119,v119
v_rcp_f32 v120,v120
v_rcp_f32 v121,v121
M 18,a[248:251],94
v_mul_f32 v30,v30,v118
v_mul_f32 v31,v31,v119
v_mul_f32 v32,v32,v120
v_mul_f32 v33,v33,v121
v_cvt_pk_bf16_f32 v124,v30,v31
v_cvt_pk_bf16_f32 v125,v32,v33
M 22,a[244:247],90
buffer_store_dwordx4 v[122:125],v0,s[36:39],s67 offen offset:0 sc0 sc1
v_cmp_eq_u32 vcc,s53,v7
M 22,a[252:255],94
s_mov_b32 s80,1
s_cmp_eq_u32 vcc_lo,-1
s_cbranch_scc0 Lg3o_xn0_%=
buffer_load_dword v130,v11,s[36:39],s73 offen offset:0 sc1
L 0
v_fma_f32 v18,-v98,v106,v18
v_fma_f32 v19,-v98,v107,v19
v_fma_f32 v20,-v98,v108,v20
v_fma_f32 v21,-v98,v109,v21
v_mul_f32 v114,v18,v18
v_mul_f32 v115,v19,v19
s_add_u32 s73,s73,s64
s_add_u32 m0,s57,0
s_add_u32 s56,s55,0
D 0,56,0
v_mul_f32 v116,v20,v20
v_mul_f32 v117,v21,v21
v_fma_f32 v114,v100,v114,v99
v_fma_f32 v115,v100,v115,v99
v_fma_f32 v116,v100,v116,v99
v_fma_f32 v117,v100,v117,v99
D 0,56,1024
v_mul_f32 v114,v18,v114
v_mul_f32 v115,v19,v115
v_mul_f32 v116,v20,v116
v_mul_f32 v117,v21,v117
v_exp_f32 v114,v114
v_exp_f32 v115,v115
D 0,56,2048
v_exp_f32 v116,v116
v_exp_f32 v117,v117
v_fma_f32 v114,v114,v101,v101
v_fma_f32 v115,v115,v101,v101
v_fma_f32 v116,v116,v101,v101
v_fma_f32 v117,v117,v101,v101
D 0,56,3072
v_rcp_f32 v114,v114
v_rcp_f32 v115,v115
v_rcp_f32 v116,v116
v_rcp_f32 v117,v117
v_mul_f32 v18,v18,v114
v_mul_f32 v19,v19,v115
s_add_u32 m0,s57,16384
s_add_u32 s56,s55,16384
D 0,56,0
v_mul_f32 v20,v20,v116
v_mul_f32 v21,v21,v117
v_cvt_pk_bf16_f32 v122,v18,v19
v_cvt_pk_bf16_f32 v123,v20,v21
v_fma_f32 v22,-v98,v110,v22
v_fma_f32 v23,-v98,v111,v23
D 0,56,1024
v_fma_f32 v24,-v98,v112,v24
v_fma_f32 v25,-v98,v113,v25
v_mul_f32 v118,v22,v22
v_mul_f32 v119,v23,v23
v_mul_f32 v120,v24,v24
v_mul_f32 v121,v25,v25
D 0,56,2048
v_fma_f32 v118,v100,v118,v99
v_fma_f32 v119,v100,v119,v99
v_fma_f32 v120,v100,v120,v99
v_fma_f32 v121,v100,v121,v99
v_mul_f32 v118,v22,v118
v_mul_f32 v119,v23,v119
D 0,56,3072
v_mul_f32 v120,v24,v120
v_mul_f32 v121,v25,v121
v_exp_f32 v118,v118
v_exp_f32 v119,v119
v_exp_f32 v120,v120
v_exp_f32 v121,v121
s_add_u32 s55,s55,s60
s_sub_u32 s57,s84,s57
s_add_u32 s54,s54,s46
v_fma_f32 v118,v118,v101,v101
v_fma_f32 v119,v119,v101,v101
v_fma_f32 v120,v120,v101,v101
v_fma_f32 v121,v121,v101,v101
v_rcp_f32 v118,v118
v_rcp_f32 v119,v119
v_rcp_f32 v120,v120
v_rcp_f32 v121,v121
v_mul_f32 v22,v22,v118
v_mul_f32 v23,v23,v119
v_mul_f32 v24,v24,v120
v_mul_f32 v25,v25,v121
v_cvt_pk_bf16_f32 v124,v22,v23
v_cvt_pk_bf16_f32 v125,v24,v25
buffer_store_dwordx4 v[122:125],v0,s[36:39],s66 offen offset:0 sc0 sc1
s_mov_b32 s72,s69
s_mov_b32 s69,0x7fff0000
s_mov_b32 s66,0x7fff0000
s_mov_b32 s76,0x7fff0000
s_branch Lg3o_gox_%=
Lg3o_xn0_%=:
s_cmp_ge_u32 s54,256
s_cbranch_scc1 Lg3o_fin0_%=
s_add_u32 m0,s59,4096
s_nop 0
buffer_load_dword v8,s[36:39],s58 offen offset:0 sc1 lds
buffer_store_dword v4,v3,s[36:39],s71 offen offset:0 sc0 sc1
s_add_u32 m0,s59,5120
s_nop 0
buffer_load_dword v8,s[36:39],s58 offen offset:0 sc1 lds
buffer_store_dword v4,v3,s[36:39],s71 offen offset:0 sc0 sc1
s_mov_b32 s83,0
L 0
v_fma_f32 v18,-v98,v106,v18
v_fma_f32 v19,-v98,v107,v19
v_fma_f32 v20,-v98,v108,v20
v_fma_f32 v21,-v98,v109,v21
v_mul_f32 v114,v18,v18
v_mul_f32 v115,v19,v19
v_mul_f32 v116,v20,v20
v_mul_f32 v117,v21,v21
v_fma_f32 v114,v100,v114,v99
v_fma_f32 v115,v100,v115,v99
v_fma_f32 v116,v100,v116,v99
v_fma_f32 v117,v100,v117,v99
v_mul_f32 v114,v18,v114
v_mul_f32 v115,v19,v115
v_mul_f32 v116,v20,v116
v_mul_f32 v117,v21,v117
v_exp_f32 v114,v114
v_exp_f32 v115,v115
v_exp_f32 v116,v116
v_exp_f32 v117,v117
v_fma_f32 v114,v114,v101,v101
v_fma_f32 v115,v115,v101,v101
v_fma_f32 v116,v116,v101,v101
v_fma_f32 v117,v117,v101,v101
v_rcp_f32 v114,v114
v_rcp_f32 v115,v115
v_rcp_f32 v116,v116
v_rcp_f32 v117,v117
v_mul_f32 v18,v18,v114
v_mul_f32 v19,v19,v115
v_mul_f32 v20,v20,v116
v_mul_f32 v21,v21,v117
v_cvt_pk_bf16_f32 v122,v18,v19
v_cvt_pk_bf16_f32 v123,v20,v21
v_fma_f32 v22,-v98,v110,v22
v_fma_f32 v23,-v98,v111,v23
v_fma_f32 v24,-v98,v112,v24
v_fma_f32 v25,-v98,v113,v25
v_mul_f32 v118,v22,v22
v_mul_f32 v119,v23,v23
v_mul_f32 v120,v24,v24
v_mul_f32 v121,v25,v25
v_fma_f32 v118,v100,v118,v99
v_fma_f32 v119,v100,v119,v99
v_fma_f32 v120,v100,v120,v99
v_fma_f32 v121,v100,v121,v99
v_mul_f32 v118,v22,v118
v_mul_f32 v119,v23,v119
v_mul_f32 v120,v24,v120
v_mul_f32 v121,v25,v121
v_exp_f32 v118,v118
v_exp_f32 v119,v119
v_exp_f32 v120,v120
v_exp_f32 v121,v121
v_fma_f32 v118,v118,v101,v101
v_fma_f32 v119,v119,v101,v101
v_fma_f32 v120,v120,v101,v101
v_fma_f32 v121,v121,v101,v101
v_rcp_f32 v118,v118
v_rcp_f32 v119,v119
v_rcp_f32 v120,v120
v_rcp_f32 v121,v121
v_mul_f32 v22,v22,v118
v_mul_f32 v23,v23,v119
v_mul_f32 v24,v24,v120
v_mul_f32 v25,v25,v121
v_cvt_pk_bf16_f32 v124,v22,v23
v_cvt_pk_bf16_f32 v125,v24,v25
buffer_store_dwordx4 v[122:125],v0,s[36:39],s66 offen offset:0 sc0 sc1
s_mov_b32 s72,s69
s_mov_b32 s69,0x7fff0000
s_mov_b32 s66,0x7fff0000
s_mov_b32 s76,0x7fff0000
s_branch Lg3o_poll_%=
Lg3o_fin0_%=:
L 0
v_fma_f32 v18,-v98,v106,v18
v_fma_f32 v19,-v98,v107,v19
v_fma_f32 v20,-v98,v108,v20
v_fma_f32 v21,-v98,v109,v21
v_mul_f32 v114,v18,v18
v_mul_f32 v115,v19,v19
v_mul_f32 v116,v20,v20
v_mul_f32 v117,v21,v21
v_fma_f32 v114,v100,v114,v99
v_fma_f32 v115,v100,v115,v99
v_fma_f32 v116,v100,v116,v99
v_fma_f32 v117,v100,v117,v99
v_mul_f32 v114,v18,v114
v_mul_f32 v115,v19,v115
v_mul_f32 v116,v20,v116
v_mul_f32 v117,v21,v117
v_exp_f32 v114,v114
v_exp_f32 v115,v115
v_exp_f32 v116,v116
v_exp_f32 v117,v117
v_fma_f32 v114,v114,v101,v101
v_fma_f32 v115,v115,v101,v101
v_fma_f32 v116,v116,v101,v101
v_fma_f32 v117,v117,v101,v101
v_rcp_f32 v114,v114
v_rcp_f32 v115,v115
v_rcp_f32 v116,v116
v_rcp_f32 v117,v117
v_mul_f32 v18,v18,v114
v_mul_f32 v19,v19,v115
v_mul_f32 v20,v20,v116
v_mul_f32 v21,v21,v117
v_cvt_pk_bf16_f32 v122,v18,v19
v_cvt_pk_bf16_f32 v123,v20,v21
v_fma_f32 v22,-v98,v110,v22
v_fma_f32 v23,-v98,v111,v23
v_fma_f32 v24,-v98,v112,v24
v_fma_f32 v25,-v98,v113,v25
v_mul_f32 v118,v22,v22
v_mul_f32 v119,v23,v23
v_mul_f32 v120,v24,v24
v_mul_f32 v121,v25,v25
v_fma_f32 v118,v100,v118,v99
v_fma_f32 v119,v100,v119,v99
v_fma_f32 v120,v100,v120,v99
v_fma_f32 v121,v100,v121,v99
v_mul_f32 v118,v22,v118
v_mul_f32 v119,v23,v119
v_mul_f32 v120,v24,v120
v_mul_f32 v121,v25,v121
v_exp_f32 v118,v118
v_exp_f32 v119,v119
v_exp_f32 v120,v120
v_exp_f32 v121,v121
v_fma_f32 v118,v118,v101,v101
v_fma_f32 v119,v119,v101,v101
v_fma_f32 v120,v120,v101,v101
v_fma_f32 v121,v121,v101,v101
v_rcp_f32 v118,v118
v_rcp_f32 v119,v119
v_rcp_f32 v120,v120
v_rcp_f32 v121,v121
v_mul_f32 v22,v22,v118
v_mul_f32 v23,v23,v119
v_mul_f32 v24,v24,v120
v_mul_f32 v25,v25,v121
v_cvt_pk_bf16_f32 v124,v22,v23
v_cvt_pk_bf16_f32 v125,v24,v25
buffer_store_dwordx4 v[122:125],v0,s[36:39],s66 offen offset:0 sc0 sc1
s_mov_b32 s72,s69
s_mov_b32 s69,0x7fff0000
s_mov_b32 s66,0x7fff0000
s_mov_b32 s76,0x7fff0000
s_branch Lg3o_done_%=
Lg3o_L1_%=:
v_mfma_f32_16x16x32_bf16 v[30:33],a[4:7],v[34:37],0
s_add_u32 m0,s59,3072
R 54,2,5120
v_add_f32_dpp v131,v130,v130 quad_perm:[1,0,3,2] row_mask:0xf bank_mask:0xf
M 30,a[12:15],38
s_mov_b32 s67,s65
buffer_load_dword v8,s[36:39],s58 offen offset:0 sc1 lds
v_mfma_f32_16x16x32_bf16 v[26:29],a[0:3],v[34:37],0
s_add_u32 s65,s65,s62
R 58,2,6144
v_add_f32_dpp v131,v131,v131 quad_perm:[2,3,0,1] row_mask:0xf bank_mask:0xf
M 26,a[8:11],38
L 3
v_fma_f32 v18,-v98,v106,v18
v_fma_f32 v19,-v98,v107,v19
M 26,a[16:19],42
R 62,2,7168
v_add_f32_dpp v134,v131,v131 row_half_mirror row_mask:0xf bank_mask:0xf
M 26,a[24:27],46
v_fma_f32 v20,-v98,v108,v20
v_fma_f32 v21,-v98,v109,v21
M 30,a[20:23],42
R 66,2,8192
v_mov_b32_dpp v131,v134 row_ror:8 row_mask:0xf bank_mask:0xf
M 30,a[28:31],46
L 3
v_fma_f32 v131,-v134,v134,v131
v_max_f32 v131,s87,v131
M 30,a[36:39],50
R 70,2,9216
v_mul_f32 v114,v18,v18
M 30,a[44:47],54
v_rsq_f32 v132,v131
M 26,a[32:35],50
R 74,2,10240
v_mul_f32 v137,v131,v132
M 26,a[40:43],54
L 3
v_mul_f32 v135,s88,v132
v_mul_f32 v131,s89,v132
M 26,a[48:51],58
R 78,2,11264
v_mul_f32 v136,v135,v132
M 26,a[56:59],62
v_mul_f32 v136,v136,v131
v_mul_f32 v115,v19,v19
M 30,a[52:55],58
R 82,2,12288
v_mul_f32 v116,v20,v20
M 30,a[60:63],62
L 3
ds_write_b128 v12,v[134:137] offset:2048
M 30,a[68:71],66
R 86,2,13312
v_mul_f32 v117,v21,v21
M 30,a[76:79],70
v_fma_f32 v114,v100,v114,v99
v_fma_f32 v115,v100,v115,v99
M 26,a[64:67],66
R 90,2,14336
v_fma_f32 v116,v100,v116,v99
M 26,a[72:75],70
L 4
v_fma_f32 v117,v100,v117,v99
v_mul_f32 v114,v18,v114
M 26,a[80:83],74
s_waitcnt vmcnt(0)
R 94,2,15360
v_mul_f32 v115,v19,v115
M 26,a[88:91],78
s_barrier
ds_read_b32 v7,v6 offset:3072
v_mul_f32 v116,v20,v116
M 30,a[84:87],74
R 34,2,16384
v_mul_f32 v117,v21,v117
M 30,a[92:95],78
L 4
buffer_store_dword v4,v3,s[36:39],s70 offen offset:0 sc0 sc1
M 30,a[100:103],82
s_mov_b32 s70,0x7fff0000
R 38,2,17408
v_fma_f32 v22,-v98,v110,v22
M 30,a[108:111],86
v_exp_f32 v114,v114
M 26,a[96:99],82
R 42,2,18432
v_fma_f32 v114,v114,v101,v101
M 26,a[104:107],86
L 4
L 3
v_cmp_eq_u32 vcc,s53,v7
M 26,a[112:115],90
R 46,2,19456
v_fma_f32 v23,-v98,v111,v23
M 26,a[120:123],94
s_cmp_eq_u32 vcc_lo,-1
v_exp_f32 v115,v115
s_cbranch_scc0 Lg3o_X1_%=
M 30,a[116:119],90
s_add_u32 m0,s57,0
s_add_u32 s56,s55,0
s_add_u32 s58,s58,s61
R 50,2,20480
M 30,a[124:127],94
L 3
buffer_load_dword v130,v11,s[36:39],s73 offen offset:0 sc1
M 30,a[132:135],34
s_add_u32 s54,s54,s46
R 54,2,21504
v_fma_f32 v115,v115,v101,v101
M 30,a[140:143],38
s_add_u32 s73,s73,s64
D 0,56,0
M 26,a[128:131],34
R 58,2,22528
v_fma_f32 v24,-v98,v112,v24
M 26,a[136:139],38
L 3
v_exp_f32 v116,v116
M 26,a[144:147],42
R 62,2,23552
v_fma_f32 v116,v116,v101,v101
M 26,a[152:155],46
D 0,56,1024
M 30,a[148:151],42
R 66,2,24576
v_fma_f32 v25,-v98,v113,v25
M 30,a[156:159],46
L 3
v_exp_f32 v117,v117
M 30,a[164:167],50
R 70,2,25600
v_fma_f32 v117,v117,v101,v101
M 30,a[172:175],54
D 0,56,2048
M 26,a[160:163],50
R 74,2,26624
v_mul_f32 v118,v22,v22
M 26,a[168:171],54
L 3
v_rcp_f32 v114,v114
M 26,a[176:179],58
R 78,2,27648
v_mul_f32 v18,v18,v114
M 26,a[184:187],62
D 0,56,3072
M 30,a[180:183],58
s_add_u32 m0,s59,1024
R 82,2,28672
v_mul_f32 v119,v23,v23
M 30,a[188:191],62
L 3
buffer_load_dword v8,s[36:39],s58 offen offset:0 sc1 lds
M 30,a[196:199],66
s_add_u32 m0,s57,16384
R 86,2,29696
v_mul_f32 v120,v24,v24
M 30,a[204:207],70
s_add_u32 s56,s55,16384
v_rcp_f32 v115,v115
M 26,a[192:195],66
s_add_u32 s55,s55,s60
R 90,2,30720
v_mul_f32 v19,v19,v115
M 26,a[200:203],70
L 3
s_nop 0
D 0,56,0
M 26,a[208:211],74
s_waitcnt vmcnt(1)
R 94,2,31744
v_cvt_pk_bf16_f32 v122,v18,v19
M 26,a[216:219],78
s_barrier
ds_read_b32 v7,v6 offset:1024
R 102,13,2048
M 30,a[212:215],74
s_sub_u32 s57,s84,s57
R 34,1,0
v_mul_f32 v121,v25,v25
M 30,a[220:223],78
L 5
buffer_store_dword v4,v3,s[36:39],s70 offen offset:0 sc0 sc1
M 30,a[228:231],82
s_mov_b32 s70,s69
R 38,1,1024
v_fma_f32 v118,v100,v118,v99
M 30,a[236:239],86
s_mov_b32 s69,s68
D 0,56,1024
M 26,a[224:227],82
s_add_u32 s68,s68,s63
R 42,1,2048
v_fma_f32 v119,v100,v119,v99
M 26,a[232:235],86
L 5
v_fma_f32 v120,v100,v120,v99
v_fma_f32 v121,v100,v121,v99
v_mul_f32 v118,v22,v118
v_mul_f32 v119,v23,v119
v_mul_f32 v120,v24,v120
v_mul_f32 v121,v25,v121
v_exp_f32 v118,v118
v_exp_f32 v119,v119
v_exp_f32 v120,v120
v_exp_f32 v121,v121
M 26,a[240:243],90
L 4
R 46,1,3072
v_rcp_f32 v116,v116
v_rcp_f32 v117,v117
v_fma_f32 v118,v118,v101,v101
v_fma_f32 v119,v119,v101,v101
v_fma_f32 v120,v120,v101,v101
v_fma_f32 v121,v121,v101,v101
v_rcp_f32 v118,v118
v_rcp_f32 v119,v119
)"
R"(v_rcp_f32 v120,v120
v_rcp_f32 v121,v121
M 26,a[248:251],94
v_mul_f32 v20,v20,v116
v_mul_f32 v21,v21,v117
v_cvt_pk_bf16_f32 v123,v20,v21
v_mul_f32 v22,v22,v118
v_mul_f32 v23,v23,v119
v_mul_f32 v24,v24,v120
v_mul_f32 v25,v25,v121
v_cvt_pk_bf16_f32 v124,v22,v23
v_cvt_pk_bf16_f32 v125,v24,v25
v_cmp_eq_u32 vcc,s53,v7
M 30,a[244:247],90
s_cmp_eq_u32 vcc_lo,-1
R 50,1,4096
D 0,56,3072
buffer_store_dwordx4 v[122:125],v0,s[36:39],s66 offen offset:0 sc0 sc1
M 30,a[252:255],94
L 3
D 0,56,2048
buffer_store_dword v4,v3,s[36:39],s71 offen offset:0 sc0 sc1
buffer_store_dword v4,v3,s[36:39],s71 offen offset:0 sc0 sc1
s_cbranch_scc1 Lg3o_F0_%=
s_branch Lg3o_L0_%=
Lg3o_X1_%=:
M 30,a[116:119],90
s_add_u32 m0,s59,1024
R 50,2,20480
v_fma_f32 v115,v115,v101,v101
M 30,a[124:127],94
L 3
buffer_load_dword v8,s[36:39],s58 offen offset:0 sc1 lds
M 30,a[132:135],34
R 54,2,21504
v_fma_f32 v24,-v98,v112,v24
M 30,a[140:143],38
v_exp_f32 v116,v116
M 26,a[128:131],34
R 58,2,22528
v_fma_f32 v116,v116,v101,v101
M 26,a[136:139],38
L 3
v_exp_f32 v117,v117
M 26,a[144:147],42
R 62,2,23552
v_fma_f32 v117,v117,v101,v101
M 26,a[152:155],46
v_rcp_f32 v114,v114
M 30,a[148:151],42
R 66,2,24576
v_mul_f32 v18,v18,v114
M 30,a[156:159],46
L 3
v_rcp_f32 v115,v115
M 30,a[164:167],50
R 70,2,25600
v_mul_f32 v19,v19,v115
M 30,a[172:175],54
v_rcp_f32 v116,v116
M 26,a[160:163],50
R 74,2,26624
v_mul_f32 v20,v20,v116
M 26,a[168:171],54
L 3
v_rcp_f32 v117,v117
M 26,a[176:179],58
R 78,2,27648
v_mul_f32 v21,v21,v117
M 26,a[184:187],62
v_cvt_pk_bf16_f32 v122,v18,v19
v_cvt_pk_bf16_f32 v123,v20,v21
M 30,a[180:183],58
R 82,2,28672
v_fma_f32 v25,-v98,v113,v25
M 30,a[188:191],62
L 3
v_mul_f32 v118,v22,v22
v_mul_f32 v119,v23,v23
M 30,a[196:199],66
R 86,2,29696
v_mul_f32 v120,v24,v24
M 30,a[204:207],70
v_mul_f32 v121,v25,v25
v_fma_f32 v118,v100,v118,v99
M 26,a[192:195],66
R 90,2,30720
v_fma_f32 v119,v100,v119,v99
M 26,a[200:203],70
L 3
s_nop 0
v_fma_f32 v120,v100,v120,v99
M 26,a[208:211],74
s_waitcnt vmcnt(0)
R 94,2,31744
v_fma_f32 v121,v100,v121,v99
M 26,a[216:219],78
s_barrier
buffer_store_dword v4,v3,s[36:39],s70 offen offset:0 sc0 sc1
M 30,a[212:215],74
s_mov_b32 s70,s69
ds_read_b32 v7,v6 offset:1024
R 102,13,2048
M 30,a[220:223],78
L 4
v_mul_f32 v118,v22,v118
v_mul_f32 v119,v23,v119
M 30,a[228:231],82
s_mov_b32 s69,s68
v_mul_f32 v120,v24,v120
v_mul_f32 v121,v25,v121
M 30,a[236:239],86
s_add_u32 s68,s68,s63
v_exp_f32 v118,v118
M 26,a[224:227],82
v_exp_f32 v119,v119
M 26,a[232:235],86
L 2
v_exp_f32 v120,v120
v_exp_f32 v121,v121
M 26,a[240:243],90
L 1
v_fma_f32 v118,v118,v101,v101
v_fma_f32 v119,v119,v101,v101
v_fma_f32 v120,v120,v101,v101
v_fma_f32 v121,v121,v101,v101
v_rcp_f32 v118,v118
v_rcp_f32 v119,v119
v_rcp_f32 v120,v120
v_rcp_f32 v121,v121
M 26,a[248:251],94
v_mul_f32 v22,v22,v118
v_mul_f32 v23,v23,v119
v_mul_f32 v24,v24,v120
v_mul_f32 v25,v25,v121
v_cvt_pk_bf16_f32 v124,v22,v23
v_cvt_pk_bf16_f32 v125,v24,v25
M 30,a[244:247],90
buffer_store_dwordx4 v[122:125],v0,s[36:39],s66 offen offset:0 sc0 sc1
v_cmp_eq_u32 vcc,s53,v7
M 30,a[252:255],94
s_mov_b32 s80,0
s_cmp_eq_u32 vcc_lo,-1
s_cbranch_scc0 Lg3o_xn1_%=
buffer_load_dword v130,v11,s[36:39],s73 offen offset:0 sc1
L 0
v_fma_f32 v26,-v102,v106,v26
v_fma_f32 v27,-v102,v107,v27
v_fma_f32 v28,-v102,v108,v28
v_fma_f32 v29,-v102,v109,v29
v_mul_f32 v114,v26,v26
v_mul_f32 v115,v27,v27
s_add_u32 s73,s73,s64
s_add_u32 m0,s57,0
s_add_u32 s56,s55,0
D 0,56,0
v_mul_f32 v116,v28,v28
v_mul_f32 v117,v29,v29
v_fma_f32 v114,v104,v114,v103
v_fma_f32 v115,v104,v115,v103
v_fma_f32 v116,v104,v116,v103
v_fma_f32 v117,v104,v117,v103
D 0,56,1024
v_mul_f32 v114,v26,v114
v_mul_f32 v115,v27,v115
v_mul_f32 v116,v28,v116
v_mul_f32 v117,v29,v117
v_exp_f32 v114,v114
v_exp_f32 v115,v115
D 0,56,2048
v_exp_f32 v116,v116
v_exp_f32 v117,v117
v_fma_f32 v114,v114,v105,v105
v_fma_f32 v115,v115,v105,v105
v_fma_f32 v116,v116,v105,v105
v_fma_f32 v117,v117,v105,v105
D 0,56,3072
v_rcp_f32 v114,v114
v_rcp_f32 v115,v115
v_rcp_f32 v116,v116
v_rcp_f32 v117,v117
v_mul_f32 v26,v26,v114
v_mul_f32 v27,v27,v115
s_add_u32 m0,s57,16384
s_add_u32 s56,s55,16384
D 0,56,0
v_mul_f32 v28,v28,v116
v_mul_f32 v29,v29,v117
v_cvt_pk_bf16_f32 v122,v26,v27
v_cvt_pk_bf16_f32 v123,v28,v29
v_fma_f32 v30,-v102,v110,v30
v_fma_f32 v31,-v102,v111,v31
D 0,56,1024
v_fma_f32 v32,-v102,v112,v32
v_fma_f32 v33,-v102,v113,v33
v_mul_f32 v118,v30,v30
v_mul_f32 v119,v31,v31
v_mul_f32 v120,v32,v32
v_mul_f32 v121,v33,v33
D 0,56,2048
v_fma_f32 v118,v104,v118,v103
v_fma_f32 v119,v104,v119,v103
v_fma_f32 v120,v104,v120,v103
v_fma_f32 v121,v104,v121,v103
v_mul_f32 v118,v30,v118
v_mul_f32 v119,v31,v119
D 0,56,3072
v_mul_f32 v120,v32,v120
v_mul_f32 v121,v33,v121
v_exp_f32 v118,v118
v_exp_f32 v119,v119
v_exp_f32 v120,v120
v_exp_f32 v121,v121
s_add_u32 s55,s55,s60
s_sub_u32 s57,s84,s57
s_add_u32 s54,s54,s46
v_fma_f32 v118,v118,v105,v105
v_fma_f32 v119,v119,v105,v105
v_fma_f32 v120,v120,v105,v105
v_fma_f32 v121,v121,v105,v105
v_rcp_f32 v118,v118
v_rcp_f32 v119,v119
v_rcp_f32 v120,v120
v_rcp_f32 v121,v121
v_mul_f32 v30,v30,v118
v_mul_f32 v31,v31,v119
v_mul_f32 v32,v32,v120
v_mul_f32 v33,v33,v121
v_cvt_pk_bf16_f32 v124,v30,v31
v_cvt_pk_bf16_f32 v125,v32,v33
buffer_store_dwordx4 v[122:125],v0,s[36:39],s67 offen offset:0 sc0 sc1
s_mov_b32 s72,s69
s_mov_b32 s69,0x7fff0000
s_mov_b32 s67,0x7fff0000
s_mov_b32 s77,0x7fff0000
s_branch Lg3o_gox_%=
Lg3o_xn1_%=:
s_cmp_ge_u32 s54,256
s_cbranch_scc1 Lg3o_fin1_%=
s_add_u32 m0,s59,4096
s_nop 0
buffer_load_dword v8,s[36:39],s58 offen offset:0 sc1 lds
buffer_store_dword v4,v3,s[36:39],s71 offen offset:0 sc0 sc1
s_add_u32 m0,s59,5120
s_nop 0
buffer_load_dword v8,s[36:39],s58 offen offset:0 sc1 lds
buffer_store_dword v4,v3,s[36:39],s71 offen offset:0 sc0 sc1
s_mov_b32 s83,0
L 0
v_fma_f32 v26,-v102,v106,v26
v_fma_f32 v27,-v102,v107,v27
v_fma_f32 v28,-v102,v108,v28
v_fma_f32 v29,-v102,v109,v29
v_mul_f32 v114,v26,v26
v_mul_f32 v115,v27,v27
v_mul_f32 v116,v28,v28
v_mul_f32 v117,v29,v29
v_fma_f32 v114,v104,v114,v103
v_fma_f32 v115,v104,v115,v103
v_fma_f32 v116,v104,v116,v103
v_fma_f32 v117,v104,v117,v103
v_mul_f32 v114,v26,v114
v_mul_f32 v115,v27,v115
v_mul_f32 v116,v28,v116
v_mul_f32 v117,v29,v117
v_exp_f32 v114,v114
v_exp_f32 v115,v115
v_exp_f32 v116,v116
v_exp_f32 v117,v117
v_fma_f32 v114,v114,v105,v105
v_fma_f32 v115,v115,v105,v105
v_fma_f32 v116,v116,v105,v105
v_fma_f32 v117,v117,v105,v105
v_rcp_f32 v114,v114
v_rcp_f32 v115,v115
v_rcp_f32 v116,v116
v_rcp_f32 v117,v117
v_mul_f32 v26,v26,v114
v_mul_f32 v27,v27,v115
v_mul_f32 v28,v28,v116
v_mul_f32 v29,v29,v117
v_cvt_pk_bf16_f32 v122,v26,v27
v_cvt_pk_bf16_f32 v123,v28,v29
v_fma_f32 v30,-v102,v110,v30
v_fma_f32 v31,-v102,v111,v31
v_fma_f32 v32,-v102,v112,v32
v_fma_f32 v33,-v102,v113,v33
v_mul_f32 v118,v30,v30
v_mul_f32 v119,v31,v31
v_mul_f32 v120,v32,v32
v_mul_f32 v121,v33,v33
v_fma_f32 v118,v104,v118,v103
v_fma_f32 v119,v104,v119,v103
v_fma_f32 v120,v104,v120,v103
v_fma_f32 v121,v104,v121,v103
v_mul_f32 v118,v30,v118
v_mul_f32 v119,v31,v119
v_mul_f32 v120,v32,v120
v_mul_f32 v121,v33,v121
v_exp_f32 v118,v118
v_exp_f32 v119,v119
v_exp_f32 v120,v120
v_exp_f32 v121,v121
v_fma_f32 v118,v118,v105,v105
v_fma_f32 v119,v119,v105,v105
v_fma_f32 v120,v120,v105,v105
v_fma_f32 v121,v121,v105,v105
v_rcp_f32 v118,v118
v_rcp_f32 v119,v119
v_rcp_f32 v120,v120
v_rcp_f32 v121,v121
v_mul_f32 v30,v30,v118
v_mul_f32 v31,v31,v119
v_mul_f32 v32,v32,v120
v_mul_f32 v33,v33,v121
v_cvt_pk_bf16_f32 v124,v30,v31
v_cvt_pk_bf16_f32 v125,v32,v33
buffer_store_dwordx4 v[122:125],v0,s[36:39],s67 offen offset:0 sc0 sc1
s_mov_b32 s72,s69
s_mov_b32 s69,0x7fff0000
s_mov_b32 s67,0x7fff0000
s_mov_b32 s77,0x7fff0000
s_branch Lg3o_poll_%=
Lg3o_fin1_%=:
L 0
v_fma_f32 v26,-v102,v106,v26
v_fma_f32 v27,-v102,v107,v27
v_fma_f32 v28,-v102,v108,v28
v_fma_f32 v29,-v102,v109,v29
v_mul_f32 v114,v26,v26
v_mul_f32 v115,v27,v27
v_mul_f32 v116,v28,v28
v_mul_f32 v117,v29,v29
v_fma_f32 v114,v104,v114,v103
v_fma_f32 v115,v104,v115,v103
v_fma_f32 v116,v104,v116,v103
v_fma_f32 v117,v104,v117,v103
v_mul_f32 v114,v26,v114
v_mul_f32 v115,v27,v115
v_mul_f32 v116,v28,v116
v_mul_f32 v117,v29,v117
v_exp_f32 v114,v114
v_exp_f32 v115,v115
v_exp_f32 v116,v116
v_exp_f32 v117,v117
v_fma_f32 v114,v114,v105,v105
v_fma_f32 v115,v115,v105,v105
v_fma_f32 v116,v116,v105,v105
v_fma_f32 v117,v117,v105,v105
v_rcp_f32 v114,v114
v_rcp_f32 v115,v115
v_rcp_f32 v116,v116
v_rcp_f32 v117,v117
v_mul_f32 v26,v26,v114
v_mul_f32 v27,v27,v115
v_mul_f32 v28,v28,v116
v_mul_f32 v29,v29,v117
v_cvt_pk_bf16_f32 v122,v26,v27
v_cvt_pk_bf16_f32 v123,v28,v29
v_fma_f32 v30,-v102,v110,v30
v_fma_f32 v31,-v102,v111,v31
v_fma_f32 v32,-v102,v112,v32
v_fma_f32 v33,-v102,v113,v33
v_mul_f32 v118,v30,v30
v_mul_f32 v119,v31,v31
v_mul_f32 v120,v32,v32
v_mul_f32 v121,v33,v33
v_fma_f32 v118,v104,v118,v103
v_fma_f32 v119,v104,v119,v103
v_fma_f32 v120,v104,v120,v103
v_fma_f32 v121,v104,v121,v103
v_mul_f32 v118,v30,v118
v_mul_f32 v119,v31,v119
v_mul_f32 v120,v32,v120
v_mul_f32 v121,v33,v121
v_exp_f32 v118,v118
v_exp_f32 v119,v119
v_exp_f32 v120,v120
v_exp_f32 v121,v121
v_fma_f32 v118,v118,v105,v105
v_fma_f32 v119,v119,v105,v105
v_fma_f32 v120,v120,v105,v105
v_fma_f32 v121,v121,v105,v105
v_rcp_f32 v118,v118
v_rcp_f32 v119,v119
v_rcp_f32 v120,v120
v_rcp_f32 v121,v121
v_mul_f32 v30,v30,v118
v_mul_f32 v31,v31,v119
v_mul_f32 v32,v32,v120
v_mul_f32 v33,v33,v121
v_cvt_pk_bf16_f32 v124,v30,v31
v_cvt_pk_bf16_f32 v125,v32,v33
buffer_store_dwordx4 v[122:125],v0,s[36:39],s67 offen offset:0 sc0 sc1
s_mov_b32 s72,s69
s_mov_b32 s69,0x7fff0000
s_mov_b32 s67,0x7fff0000
s_mov_b32 s77,0x7fff0000
s_branch Lg3o_done_%=
Lg3o_done_%=:
s_waitcnt vmcnt(1)
s_barrier
buffer_store_dword v4,v3,s[36:39],s70 offen offset:0 sc0 sc1
s_waitcnt vmcnt(1)
s_barrier
buffer_store_dword v4,v3,s[36:39],s72 offen offset:0 sc0 sc1
s_waitcnt vmcnt(0) lgkmcnt(0)
s_barrier
s_endpgm
Lcode_g3o_e:
.purgem M
.purgem R
.purgem T
.purgem D
.purgem L
)"
  :: "s"(ws), "s"(sWave), "s"(sG0), "s"(sStep), "s"(sOutOff), "s"(sFoutOff), "s"(sAuxOff), "s"(sWw), "s"(sBuf), "s"(sRk), "v"(vL16), "v"(vFlag), "v"(vSnapOff), "v"(vSnapRd), "v"(vWd0), "v"(vWd1), "v"(vWr0), "v"(vWr1), "v"(vWr2), "v"(vOutS), "v"(vAux0), "v"(vAux1), "v"(vAux2), "v"(vAux3), "v"(vAn), "v"(vAe), "s"(w3), "s"(out) : "v0", "v1", "v2", "v3", "v4", "v5", "v6", "v7", "v8", "v9", "v10", "v11", "v12", "v13", "v14", "v15", "v16", "v17", "v18", "v19", "v20", "v21", "v22", "v23", "v24", "v25", "v26", "v27", "v28", "v29", "v30", "v31", "v32", "v33", "v34", "v35", "v36", "v37", "v38", "v39", "v40", "v41", "v42", "v43", "v44", "v45", "v46", "v47", "v48", "v49", "v50", "v51", "v52", "v53", "v54", "v55", "v56", "v57", "v58", "v59", "v60", "v61", "v62", "v63", "v64", "v65", "v66", "v67", "v68", "v69", "v70", "v71", "v72", "v73", "v74", "v75", "v76", "v77", "v78", "v79", "v80", "v81", "v82", "v83", "v84", "v85", "v86", "v87", "v88", "v89", "v90", "v91", "v92", "v93", "v94", "v95", "v96", "v97", "v98", "v99", "v100", "v101", "v102", "v103", "v104", "v105", "v106", "v107", "v108", "v109", "v110", "v111", "v112", "v113", "v114", "v115", "v116", "v117", "v118", "v119", "v120", "v121", "v122", "v123", "v124", "v125", "v126", "v127", "v128", "v129", "v130", "v131", "v132", "v133", "v134", "v135", "v136", "v137", "v138", "v139", "v140", "v141", "v142", "v143", "v144", "v145", "v146", "v147", "v148", "v149", "v150", "v151", "v152", "v153", "v154", "v155", "v156", "v157", "v158", "v159", "v160", "v161", "v162", "v163", "v164", "v165", "v166", "v167", "v168", "v169", "v170", "v171", "v172", "v173", "v174", "v175", "v176", "v177", "v178", "v179", "v180", "v181", "v182", "v183", "v184", "v185", "v186", "v187", "v188", "v189", "v190", "v191", "v192", "v193", "v194", "v195", "v196", "v197", "v198", "v199", "v200", "v201", "v202", "v203", "v204", "v205", "v206", "v207", "v208", "v209", "v210", "v211", "v212", "v213", "v214", "v215", "v216", "v217", "v218", "v219", "v220", "v221", "v222", "v223", "v224", "v225", "v226", "v227", "v228", "v229", "v230", "v231", "v232", "v233", "v234", "v235", "v236", "v237", "v238", "v239", "a0", "a1", "a2", "a3", "a4", "a5", "a6", "a7", "a8", "a9", "a10", "a11", "a12", "a13", "a14", "a15", "a16", "a17", "a18", "a19", "a20", "a21", "a22", "a23", "a24", "a25", "a26", "a27", "a28", "a29", "a30", "a31", "a32", "a33", "a34", "a35", "a36", "a37", "a38", "a39", "a40", "a41", "a42", "a43", "a44", "a45", "a46", "a47", "a48", "a49", "a50", "a51", "a52", "a53", "a54", "a55", "a56", "a57", "a58", "a59", "a60", "a61", "a62", "a63", "a64", "a65", "a66", "a67", "a68", "a69", "a70", "a71", "a72", "a73", "a74", "a75", "a76", "a77", "a78", "a79", "a80", "a81", "a82", "a83", "a84", "a85", "a86", "a87", "a88", "a89", "a90", "a91", "a92", "a93", "a94", "a95", "a96", "a97", "a98", "a99", "a100", "a101", "a102", "a103", "a104", "a105", "a106", "a107", "a108", "a109", "a110", "a111", "a112", "a113", "a114", "a115", "a116", "a117", "a118", "a119", "a120", "a121", "a122", "a123", "a124", "a125", "a126", "a127", "a128", "a129", "a130", "a131", "a132", "a133", "a134", "a135", "a136", "a137", "a138", "a139", "a140", "a141", "a142", "a143", "a144", "a145", "a146", "a147", "a148", "a149", "a150", "a151", "a152", "a153", "a154", "a155", "a156", "a157", "a158", "a159", "a160", "a161", "a162", "a163", "a164", "a165", "a166", "a167", "a168", "a169", "a170", "a171", "a172", "a173", "a174", "a175", "a176", "a177", "a178", "a179", "a180", "a181", "a182", "a183", "a184", "a185", "a186", "a187", "a188", "a189", "a190", "a191", "a192", "a193", "a194", "a195", "a196", "a197", "a198", "a199", "a200", "a201", "a202", "a203", "a204", "a205", "a206", "a207", "a208", "a209", "a210", "a211", "a212", "a213", "a214", "a215", "a216", "a217", "a218", "a219", "a220", "a221", "a222", "a223", "a224", "a225", "a226", "a227", "a228", "a229", "a230", "a231", "a232", "a233", "a234", "a235", "a236", "a237", "a238", "a239", "a240", "a241", "a242", "a243", "a244", "a245", "a246", "a247", "a248", "a249", "a250", "a251", "a252", "a253", "a254", "a255", "s36", "s37", "s38", "s39", "s40", "s41", "s42", "s43", "s44", "s45", "s46", "s47", "s48", "s49", "s50", "s51", "s52", "s53", "s54", "s55", "s56", "s57", "s58", "s59", "s60", "s61", "s62", "s63", "s64", "s65", "s66", "s67", "s68", "s69", "s70", "s71", "s72", "s73", "s74", "s75", "s76", "s77", "s78", "s79", "s80", "s81", "s82", "s83", "s84", "s85", "s86", "s87", "s88", "s89", "s90", "s91", "s92", "s93", "s94", "s95", "s96", "s97", "vcc", "scc", "memory"); }
    __builtin_unreachable();
  } else if (kind == 4) {
    const unsigned sStep = 5;
    COMMON(10, (kh * 9 + fj) * 128, 2560, 143360, 2048, 448, 0);
    const int cs = slice;
    const unsigned sOutOff = (kh ? OFF_P1 : OFF_P0) + (cs * 4 + wave) * 1024, sFoutOff = OFF_FP + (kh * 8 + cs) * 128;
    const unsigned sAuxOff = (kh ? OFF_XP : OFF_X1) + (cs * 4 + wave) * 1024, sWw = (kh * 1792 + 16 * wave) * 2048 + cs * 256, sBuf = kh * 57344;
    const int zu = 118 + (rep + 2) % 5 + 5 * (wave >> 1);
    const unsigned vSp = lg * 2048 + l15 * 4, vSn = (zu * 16 + (wave & 1) * 8 + (ln >> 3)) * 2048 + (cs * 2 + kh) * 128 + (ln & 7) * 16;
    (void)vWr2;
    asm volatile(
R"(.macro M d,w,x
v_mfma_f32_16x16x32_bf16 v[\d:\d+3], \w, v[\x:\x+3], v[\d:\d+3]
.endm
.macro R d,a,o
ds_read_b128 v[\d:\d+3], v\a offset:\o
.endm
.macro T d,a,o
ds_read_b64_tr_b16 \d, v\a offset:\o
.endm
.macro D a,s,o
buffer_load_dwordx4 v\a, s[36:39], s\s offen offset:\o sc1 lds
.endm
.macro L c
s_waitcnt lgkmcnt(\c)
.endm
Lcode_g4_b:
s_mov_b64 s[36:37],%0
s_mov_b32 s38,111243392
s_mov_b32 s39,0x00020000
s_mov_b32 s44,%1
s_mov_b32 s45,%2
s_mov_b32 s46,%3
s_mov_b32 s47,%4
s_mov_b32 s48,%5
s_mov_b32 s49,%6
s_mov_b32 s50,%7
s_mov_b32 s51,%8
s_mov_b32 s93,%9
v_mov_b32 v0,%10
v_mov_b32 v3,%11
v_mov_b32 v5,%12
v_mov_b32 v6,%13
v_mov_b32 v10,%14
v_mov_b32 v11,%15
v_mov_b32 v12,%16
v_mov_b32 v13,%17
v_mov_b32 v9,%18
v_mov_b32 v7,%19
s_mov_b64 s[40:41],%20
s_mov_b32 s42,7340032
s_mov_b32 s43,0x00020000
s_lshl_b32 s57,s44,11
s_mov_b32 s79,111243264
buffer_load_dword v4,off,s[36:39],s79 sc1
s_add_u32 s55,s50,0
s_add_u32 m0,s57,0
s_nop 0
buffer_load_dwordx4 v10,s[40:43],s55 offen offset:0  lds
s_add_u32 m0,s57,1024
s_nop 0
buffer_load_dwordx4 v11,s[40:43],s55 offen offset:0  lds
s_add_u32 m0,s57,8064
s_nop 0
buffer_load_dwordx4 v10,s[40:43],s55 offen offset:128  lds
s_add_u32 m0,s57,9088
s_nop 0
buffer_load_dwordx4 v11,s[40:43],s55 offen offset:128  lds
s_add_u32 s55,s50,131072
s_add_u32 m0,s57,16384
s_nop 0
buffer_load_dwordx4 v10,s[40:43],s55 offen offset:0  lds
s_add_u32 m0,s57,17408
s_nop 0
buffer_load_dwordx4 v11,s[40:43],s55 offen offset:0  lds
s_add_u32 m0,s57,24448
s_nop 0
buffer_load_dwordx4 v10,s[40:43],s55 offen offset:128  lds
s_add_u32 m0,s57,25472
s_nop 0
buffer_load_dwordx4 v11,s[40:43],s55 offen offset:128  lds
s_add_u32 s55,s50,262144
s_add_u32 m0,s57,32768
s_nop 0
buffer_load_dwordx4 v10,s[40:43],s55 offen offset:0  lds
s_add_u32 m0,s57,33792
s_nop 0
buffer_load_dwordx4 v11,s[40:43],s55 offen offset:0  lds
s_add_u32 m0,s57,40832
s_nop 0
buffer_load_dwordx4 v10,s[40:43],s55 offen offset:128  lds
s_add_u32 m0,s57,41856
s_nop 0
buffer_load_dwordx4 v11,s[40:43],s55 offen offset:128  lds
s_add_u32 s55,s50,393216
s_add_u32 m0,s57,49152
s_nop 0
buffer_load_dwordx4 v10,s[40:43],s55 offen offset:0  lds
s_add_u32 m0,s57,50176
s_nop 0
buffer_load_dwordx4 v11,s[40:43],s55 offen offset:0  lds
s_add_u32 m0,s57,57216
s_nop 0
buffer_load_dwordx4 v10,s[40:43],s55 offen offset:128  lds
s_add_u32 m0,s57,58240
s_nop 0
buffer_load_dwordx4 v11,s[40:43],s55 offen offset:128  lds
s_add_u32 s55,s50,524288
s_add_u32 m0,s57,65536
s_nop 0
buffer_load_dwordx4 v10,s[40:43],s55 offen offset:0  lds
s_add_u32 m0,s57,66560
s_nop 0
buffer_load_dwordx4 v11,s[40:43],s55 offen offset:0  lds
s_add_u32 m0,s57,73600
s_nop 0
buffer_load_dwordx4 v10,s[40:43],s55 offen offset:128  lds
s_add_u32 m0,s57,74624
s_nop 0
buffer_load_dwordx4 v11,s[40:43],s55 offen offset:128  lds
s_add_u32 s55,s50,655360
s_add_u32 m0,s57,81920
s_nop 0
buffer_load_dwordx4 v10,s[40:43],s55 offen offset:0  lds
s_add_u32 m0,s57,82944
s_nop 0
buffer_load_dwordx4 v11,s[40:43],s55 offen offset:0  lds
s_add_u32 m0,s57,89984
s_nop 0
buffer_load_dwordx4 v10,s[40:43],s55 offen offset:128  lds
s_add_u32 m0,s57,91008
s_nop 0
buffer_load_dwordx4 v11,s[40:43],s55 offen offset:128  lds
s_waitcnt vmcnt(24)
s_barrier
v_readfirstlane_b32 s53,v4
s_nop 3
s_add_u32 s53,s53,1
s_cmp_eq_u32 s53,0
s_cselect_b32 s53,1,s53
v_mov_b32 v4,s53
s_waitcnt vmcnt(20) lgkmcnt(0)
s_barrier
s_add_u32 s55,s50,786432
s_add_u32 m0,s57,98304
s_nop 0
buffer_load_dwordx4 v10,s[40:43],s55 offen offset:0  lds
s_add_u32 m0,s57,99328
s_nop 0
buffer_load_dwordx4 v11,s[40:43],s55 offen offset:0  lds
s_add_u32 m0,s57,106368
s_nop 0
buffer_load_dwordx4 v10,s[40:43],s55 offen offset:128  lds
s_add_u32 m0,s57,107392
s_nop 0
buffer_load_dwordx4 v11,s[40:43],s55 offen offset:128  lds
v_add_u32 v18,0,v12
v_add_u32 v19,0,v13
T a[0:1],18,0
T a[2:3],18,512
T a[4:5],19,0
T a[6:7],19,512
T a[8:9],18,4096
T a[10:11],18,4608
T a[12:13],19,4096
T a[14:15],19,4608
s_waitcnt vmcnt(20) lgkmcnt(0)
s_barrier
s_add_u32 s55,s50,917504
s_add_u32 m0,s57,0
s_nop 0
buffer_load_dwordx4 v10,s[40:43],s55 offen offset:0  lds
s_add_u32 m0,s57,1024
s_nop 0
buffer_load_dwordx4 v11,s[40:43],s55 offen offset:0  lds
s_add_u32 m0,s57,8064
s_nop 0
buffer_load_dwordx4 v10,s[40:43],s55 offen offset:128  lds
s_add_u32 m0,s57,9088
s_nop 0
buffer_load_dwordx4 v11,s[40:43],s55 offen offset:128  lds
v_add_u32 v18,16384,v12
v_add_u32 v19,16384,v13
T a[16:17],18,0
T a[18:19],18,512
T a[20:21],19,0
T a[22:23],19,512
T a[24:25],18,4096
T a[26:27],18,4608
T a[28:29],19,4096
T a[30:31],19,4608
s_waitcnt vmcnt(20) lgkmcnt(0)
s_barrier
s_add_u32 s55,s50,1048576
s_add_u32 m0,s57,16384
s_nop 0
buffer_load_dwordx4 v10,s[40:43],s55 offen offset:0  lds
s_add_u32 m0,s57,17408
s_nop 0
buffer_load_dwordx4 v11,s[40:43],s55 offen offset:0  lds
s_add_u32 m0,s57,24448
s_nop 0
buffer_load_dwordx4 v10,s[40:43],s55 offen offset:128  lds
s_add_u32 m0,s57,25472
s_nop 0
buffer_load_dwordx4 v11,s[40:43],s55 offen offset:128  lds
v_add_u32 v18,32768,v12
v_add_u32 v19,32768,v13
T a[32:33],18,0
T a[34:35],18,512
T a[36:37],19,0
T a[38:39],19,512
T a[40:41],18,4096
T a[42:43],18,4608
T a[44:45],19,4096
T a[46:47],19,4608
s_waitcnt vmcnt(20) lgkmcnt(0)
s_barrier
s_add_u32 s55,s50,1179648
s_add_u32 m0,s57,32768
s_nop 0
buffer_load_dwordx4 v10,s[40:43],s55 offen offset:0  lds
s_add_u32 m0,s57,33792
s_nop 0
buffer_load_dwordx4 v11,s[40:43],s55 offen offset:0  lds
s_add_u32 m0,s57,40832
s_nop 0
buffer_load_dwordx4 v10,s[40:43],s55 offen offset:128  lds
s_add_u32 m0,s57,41856
s_nop 0
buffer_load_dwordx4 v11,s[40:43],s55 offen offset:128  lds
v_add_u32 v18,49152,v12
v_add_u32 v19,49152,v13
T a[48:49],18,0
T a[50:51],18,512
T a[52:53],19,0
T a[54:55],19,512
T a[56:57],18,4096
T a[58:59],18,4608
T a[60:61],19,4096
T a[62:63],19,4608
s_waitcnt vmcnt(20) lgkmcnt(0)
s_barrier
s_add_u32 s55,s50,1310720
s_add_u32 m0,s57,49152
s_nop 0
buffer_load_dwordx4 v10,s[40:43],s55 offen offset:0  lds
s_add_u32 m0,s57,50176
s_nop 0
buffer_load_dwordx4 v11,s[40:43],s55 offen offset:0  lds
s_add_u32 m0,s57,57216
s_nop 0
buffer_load_dwordx4 v10,s[40:43],s55 offen offset:128  lds
s_add_u32 m0,s57,58240
s_nop 0
buffer_load_dwordx4 v11,s[40:43],s55 offen offset:128  lds
v_add_u32 v18,65536,v12
v_add_u32 v19,65536,v13
T a[64:65],18,0
T a[66:67],18,512
T a[68:69],19,0
T a[70:71],19,512
T a[72:73],18,4096
T a[74:75],18,4608
T a[76:77],19,4096
T a[78:79],19,4608
s_waitcnt vmcnt(20) lgkmcnt(0)
s_barrier
s_add_u32 s55,s50,1441792
s_add_u32 m0,s57,65536
s_nop 0
buffer_load_dwordx4 v10,s[40:43],s55 offen offset:0  lds
s_add_u32 m0,s57,66560
s_nop 0
buffer_load_dwordx4 v11,s[40:43],s55 offen offset:0  lds
s_add_u32 m0,s57,73600
s_nop 0
buffer_load_dwordx4 v10,s[40:43],s55 offen offset:128  lds
s_add_u32 m0,s57,74624
s_nop 0
buffer_load_dwordx4 v11,s[40:43],s55 offen offset:128  lds
v_add_u32 v18,81920,v12
v_add_u32 v19,81920,v13
T a[80:81],18,0
T a[82:83],18,512
T a[84:85],19,0
T a[86:87],19,512
T a[88:89],18,4096
T a[90:91],18,4608
T a[92:93],19,4096
T a[94:95],19,4608
s_waitcnt vmcnt(20) lgkmcnt(0)
s_barrier
s_add_u32 s55,s50,1572864
s_add_u32 m0,s57,81920
s_nop 0
buffer_load_dwordx4 v10,s[40:43],s55 offen offset:0  lds
s_add_u32 m0,s57,82944
s_nop 0
buffer_load_dwordx4 v11,s[40:43],s55 offen offset:0  lds
s_add_u32 m0,s57,89984
s_nop 0
buffer_load_dwordx4 v10,s[40:43],s55 offen offset:128  lds
s_add_u32 m0,s57,91008
s_nop 0
buffer_load_dwordx4 v11,s[40:43],s55 offen offset:128  lds
v_add_u32 v18,98304,v12
v_add_u32 v19,98304,v13
T a[96:97],18,0
T a[98:99],18,512
T a[100:101],19,0
T a[102:103],19,512
T a[104:105],18,4096
T a[106:107],18,4608
T a[108:109],19,4096
T a[110:111],19,4608
s_waitcnt vmcnt(20) lgkmcnt(0)
s_barrier
s_add_u32 s55,s50,1703936
s_add_u32 m0,s57,98304
s_nop 0
buffer_load_dwordx4 v10,s[40:43],s55 offen offset:0  lds
s_add_u32 m0,s57,99328
s_nop 0
buffer_load_dwordx4 v11,s[40:43],s55 offen offset:0  lds
s_add_u32 m0,s57,106368
s_nop 0
buffer_load_dwordx4 v10,s[40:43],s55 offen offset:128  lds
s_add_u32 m0,s57,107392
s_nop 0
buffer_load_dwordx4 v11,s[40:43],s55 offen offset:128  lds
v_add_u32 v18,0,v12
v_add_u32 v19,0,v13
T a[112:113],18,0
T a[114:115],18,512
T a[116:117],19,0
T a[118:119],19,512
T a[120:121],18,4096
T a[122:123],18,4608
T a[124:125],19,4096
T a[126:127],19,4608
s_waitcnt vmcnt(20) lgkmcnt(0)
s_barrier
s_add_u32 s55,s50,1835008
s_add_u32 m0,s57,0
s_nop 0
buffer_load_dwordx4 v10,s[40:43],s55 offen offset:0  lds
s_add_u32 m0,s57,1024
s_nop 0
buffer_load_dwordx4 v11,s[40:43],s55 offen offset:0  lds
s_add_u32 m0,s57,8064
s_nop 0
buffer_load_dwordx4 v10,s[40:43],s55 offen offset:128  lds
s_add_u32 m0,s57,9088
s_nop 0
buffer_load_dwordx4 v11,s[40:43],s55 offen offset:128  lds
v_add_u32 v18,16384,v12
v_add_u32 v19,16384,v13
T a[128:129],18,0
T a[130:131],18,512
T a[132:133],19,0
T a[134:135],19,512
T a[136:137],18,4096
T a[138:139],18,4608
T a[140:141],19,4096
T a[142:143],19,4608
s_waitcnt vmcnt(20) lgkmcnt(0)
s_barrier
s_add_u32 s55,s50,1966080
s_add_u32 m0,s57,16384
s_nop 0
buffer_load_dwordx4 v10,s[40:43],s55 offen offset:0  lds
s_add_u32 m0,s57,17408
s_nop 0
buffer_load_dwordx4 v11,s[40:43],s55 offen offset:0  lds
s_add_u32 m0,s57,24448
s_nop 0
buffer_load_dwordx4 v10,s[40:43],s55 offen offset:128  lds
s_add_u32 m0,s57,25472
s_nop 0
buffer_load_dwordx4 v11,s[40:43],s55 offen offset:128  lds
v_add_u32 v18,32768,v12
v_add_u32 v19,32768,v13
T a[144:145],18,0
T a[146:147],18,512
T a[148:149],19,0
T a[150:151],19,512
T a[152:153],18,4096
T a[154:155],18,4608
T a[156:157],19,4096
T a[158:159],19,4608
s_waitcnt vmcnt(20) lgkmcnt(0)
s_barrier
s_add_u32 s55,s50,2097152
s_add_u32 m0,s57,32768
s_nop 0
buffer_load_dwordx4 v10,s[40:43],s55 offen offset:0  lds
s_add_u32 m0,s57,33792
s_nop 0
buffer_load_dwordx4 v11,s[40:43],s55 offen offset:0  lds
s_add_u32 m0,s57,40832
s_nop 0
buffer_load_dwordx4 v10,s[40:43],s55 offen offset:128  lds
s_add_u32 m0,s57,41856
s_nop 0
buffer_load_dwordx4 v11,s[40:43],s55 offen offset:128  lds
v_add_u32 v18,49152,v12
v_add_u32 v19,49152,v13
T a[160:161],18,0
T a[162:163],18,512
T a[164:165],19,0
T a[166:167],19,512
T a[168:169],18,4096
T a[170:171],18,4608
T a[172:173],19,4096
T a[174:175],19,4608
s_waitcnt vmcnt(20) lgkmcnt(0)
s_barrier
s_add_u32 s55,s50,2228224
s_add_u32 m0,s57,49152
s_nop 0
buffer_load_dwordx4 v10,s[40:43],s55 offen offset:0  lds
s_add_u32 m0,s57,50176
s_nop 0
buffer_load_dwordx4 v11,s[40:43],s55 offen offset:0  lds
s_add_u32 m0,s57,57216
s_nop 0
buffer_load_dwordx4 v10,s[40:43],s55 offen offset:128  lds
s_add_u32 m0,s57,58240
s_nop 0
buffer_load_dwordx4 v11,s[40:43],s55 offen offset:128  lds
v_add_u32 v18,65536,v12
v_add_u32 v19,65536,v13
T a[176:177],18,0
T a[178:179],18,512
T a[180:181],19,0
T a[182:183],19,512
T a[184:185],18,4096
T a[186:187],18,4608
T a[188:189],19,4096
T a[190:191],19,4608
s_waitcnt vmcnt(20) lgkmcnt(0)
s_barrier
s_add_u32 s55,s50,2359296
s_add_u32 m0,s57,65536
s_nop 0
buffer_load_dwordx4 v10,s[40:43],s55 offen offset:0  lds
s_add_u32 m0,s57,66560
s_nop 0
buffer_load_dwordx4 v11,s[40:43],s55 offen offset:0  lds
s_add_u32 m0,s57,73600
s_nop 0
buffer_load_dwordx4 v10,s[40:43],s55 offen offset:128  lds
s_add_u32 m0,s57,74624
s_nop 0
buffer_load_dwordx4 v11,s[40:43],s55 offen offset:128  lds
v_add_u32 v18,81920,v12
v_add_u32 v19,81920,v13
T a[192:193],18,0
T a[194:195],18,512
T a[196:197],19,0
T a[198:199],19,512
T a[200:201],18,4096
T a[202:203],18,4608
T a[204:205],19,4096
T a[206:207],19,4608
s_waitcnt vmcnt(20) lgkmcnt(0)
s_barrier
s_add_u32 s55,s50,2490368
s_add_u32 m0,s57,81920
s_nop 0
buffer_load_dwordx4 v10,s[40:43],s55 offen offset:0  lds
s_add_u32 m0,s57,82944
s_nop 0
buffer_load_dwordx4 v11,s[40:43],s55 offen offset:0  lds
s_add_u32 m0,s57,89984
s_nop 0
buffer_load_dwordx4 v10,s[40:43],s55 offen offset:128  lds
s_add_u32 m0,s57,91008
s_nop 0
buffer_load_dwordx4 v11,s[40:43],s55 offen offset:128  lds
v_add_u32 v18,98304,v12
v_add_u32 v19,98304,v13
T a[208:209],18,0
T a[210:211],18,512
T a[212:213],19,0
T a[214:215],19,512
T a[216:217],18,4096
T a[218:219],18,4608
T a[220:221],19,4096
T a[222:223],19,4608
s_waitcnt vmcnt(20) lgkmcnt(0)
s_barrier
s_add_u32 s55,s50,2621440
s_add_u32 m0,s57,98304
s_nop 0
buffer_load_dwordx4 v10,s[40:43],s55 offen offset:0  lds
s_add_u32 m0,s57,99328
s_nop 0
buffer_load_dwordx4 v11,s[40:43],s55 offen offset:0  lds
s_add_u32 m0,s57,106368
s_nop 0
buffer_load_dwordx4 v10,s[40:43],s55 offen offset:128  lds
s_add_u32 m0,s57,107392
s_nop 0
buffer_load_dwordx4 v11,s[40:43],s55 offen offset:128  lds
v_add_u32 v18,0,v12
v_add_u32 v19,0,v13
T a[224:225],18,0
T a[226:227],18,512
T a[228:229],19,0
)"
R"(T a[230:231],19,512
T a[232:233],18,4096
T a[234:235],18,4608
T a[236:237],19,4096
T a[238:239],19,4608
s_waitcnt vmcnt(20) lgkmcnt(0)
s_barrier
s_add_u32 s55,s50,2752512
s_add_u32 m0,s57,0
s_nop 0
buffer_load_dwordx4 v10,s[40:43],s55 offen offset:0  lds
s_add_u32 m0,s57,1024
s_nop 0
buffer_load_dwordx4 v11,s[40:43],s55 offen offset:0  lds
s_add_u32 m0,s57,8064
s_nop 0
buffer_load_dwordx4 v10,s[40:43],s55 offen offset:128  lds
s_add_u32 m0,s57,9088
s_nop 0
buffer_load_dwordx4 v11,s[40:43],s55 offen offset:128  lds
v_add_u32 v18,16384,v12
v_add_u32 v19,16384,v13
T a[240:241],18,0
T a[242:243],18,512
T a[244:245],19,0
T a[246:247],19,512
T a[248:249],18,4096
T a[250:251],18,4608
T a[252:253],19,4096
T a[254:255],19,4608
s_waitcnt vmcnt(20) lgkmcnt(0)
s_barrier
s_add_u32 s55,s50,2883584
s_add_u32 m0,s57,16384
s_nop 0
buffer_load_dwordx4 v10,s[40:43],s55 offen offset:0  lds
s_add_u32 m0,s57,17408
s_nop 0
buffer_load_dwordx4 v11,s[40:43],s55 offen offset:0  lds
s_add_u32 m0,s57,24448
s_nop 0
buffer_load_dwordx4 v10,s[40:43],s55 offen offset:128  lds
s_add_u32 m0,s57,25472
s_nop 0
buffer_load_dwordx4 v11,s[40:43],s55 offen offset:128  lds
v_add_u32 v18,32768,v12
v_add_u32 v19,32768,v13
T v[64:65],18,0
T v[66:67],18,512
T v[68:69],19,0
T v[70:71],19,512
T v[72:73],18,4096
T v[74:75],18,4608
T v[76:77],19,4096
T v[78:79],19,4608
s_waitcnt vmcnt(20) lgkmcnt(0)
s_barrier
s_add_u32 s55,s50,3014656
s_add_u32 m0,s57,32768
s_nop 0
buffer_load_dwordx4 v10,s[40:43],s55 offen offset:0  lds
s_add_u32 m0,s57,33792
s_nop 0
buffer_load_dwordx4 v11,s[40:43],s55 offen offset:0  lds
s_add_u32 m0,s57,40832
s_nop 0
buffer_load_dwordx4 v10,s[40:43],s55 offen offset:128  lds
s_add_u32 m0,s57,41856
s_nop 0
buffer_load_dwordx4 v11,s[40:43],s55 offen offset:128  lds
v_add_u32 v18,49152,v12
v_add_u32 v19,49152,v13
T v[80:81],18,0
T v[82:83],18,512
T v[84:85],19,0
T v[86:87],19,512
T v[88:89],18,4096
T v[90:91],18,4608
T v[92:93],19,4096
T v[94:95],19,4608
s_waitcnt vmcnt(20) lgkmcnt(0)
s_barrier
s_add_u32 s55,s50,3145728
s_add_u32 m0,s57,49152
s_nop 0
buffer_load_dwordx4 v10,s[40:43],s55 offen offset:0  lds
s_add_u32 m0,s57,50176
s_nop 0
buffer_load_dwordx4 v11,s[40:43],s55 offen offset:0  lds
s_add_u32 m0,s57,57216
s_nop 0
buffer_load_dwordx4 v10,s[40:43],s55 offen offset:128  lds
s_add_u32 m0,s57,58240
s_nop 0
buffer_load_dwordx4 v11,s[40:43],s55 offen offset:128  lds
v_add_u32 v18,65536,v12
v_add_u32 v19,65536,v13
T v[96:97],18,0
T v[98:99],18,512
T v[100:101],19,0
T v[102:103],19,512
T v[104:105],18,4096
T v[106:107],18,4608
T v[108:109],19,4096
T v[110:111],19,4608
s_waitcnt vmcnt(20) lgkmcnt(0)
s_barrier
s_add_u32 s55,s50,3276800
s_add_u32 m0,s57,65536
s_nop 0
buffer_load_dwordx4 v10,s[40:43],s55 offen offset:0  lds
s_add_u32 m0,s57,66560
s_nop 0
buffer_load_dwordx4 v11,s[40:43],s55 offen offset:0  lds
s_add_u32 m0,s57,73600
s_nop 0
buffer_load_dwordx4 v10,s[40:43],s55 offen offset:128  lds
s_add_u32 m0,s57,74624
s_nop 0
buffer_load_dwordx4 v11,s[40:43],s55 offen offset:128  lds
v_add_u32 v18,81920,v12
v_add_u32 v19,81920,v13
T v[112:113],18,0
T v[114:115],18,512
T v[116:117],19,0
T v[118:119],19,512
T v[120:121],18,4096
T v[122:123],18,4608
T v[124:125],19,4096
T v[126:127],19,4608
s_waitcnt vmcnt(20) lgkmcnt(0)
s_barrier
s_add_u32 s55,s50,3407872
s_add_u32 m0,s57,81920
s_nop 0
buffer_load_dwordx4 v10,s[40:43],s55 offen offset:0  lds
s_add_u32 m0,s57,82944
s_nop 0
buffer_load_dwordx4 v11,s[40:43],s55 offen offset:0  lds
s_add_u32 m0,s57,89984
s_nop 0
buffer_load_dwordx4 v10,s[40:43],s55 offen offset:128  lds
s_add_u32 m0,s57,91008
s_nop 0
buffer_load_dwordx4 v11,s[40:43],s55 offen offset:128  lds
v_add_u32 v18,98304,v12
v_add_u32 v19,98304,v13
T v[128:129],18,0
T v[130:131],18,512
T v[132:133],19,0
T v[134:135],19,512
T v[136:137],18,4096
T v[138:139],18,4608
T v[140:141],19,4096
T v[142:143],19,4608
s_waitcnt vmcnt(20) lgkmcnt(0)
s_barrier
s_add_u32 s55,s50,3538944
s_add_u32 m0,s57,98304
s_nop 0
buffer_load_dwordx4 v10,s[40:43],s55 offen offset:0  lds
s_add_u32 m0,s57,99328
s_nop 0
buffer_load_dwordx4 v11,s[40:43],s55 offen offset:0  lds
s_add_u32 m0,s57,106368
s_nop 0
buffer_load_dwordx4 v10,s[40:43],s55 offen offset:128  lds
s_add_u32 m0,s57,107392
s_nop 0
buffer_load_dwordx4 v11,s[40:43],s55 offen offset:128  lds
v_add_u32 v18,0,v12
v_add_u32 v19,0,v13
T v[144:145],18,0
T v[146:147],18,512
T v[148:149],19,0
T v[150:151],19,512
T v[152:153],18,4096
T v[154:155],18,4608
T v[156:157],19,4096
T v[158:159],19,4608
s_waitcnt vmcnt(20) lgkmcnt(0)
s_barrier
v_add_u32 v18,16384,v12
v_add_u32 v19,16384,v13
T v[160:161],18,0
T v[162:163],18,512
T v[164:165],19,0
T v[166:167],19,512
T v[168:169],18,4096
T v[170:171],18,4608
T v[172:173],19,4096
T v[174:175],19,4608
s_waitcnt vmcnt(16) lgkmcnt(0)
s_barrier
v_add_u32 v18,32768,v12
v_add_u32 v19,32768,v13
T v[176:177],18,0
T v[178:179],18,512
T v[180:181],19,0
T v[182:183],19,512
T v[184:185],18,4096
T v[186:187],18,4608
T v[188:189],19,4096
T v[190:191],19,4608
s_waitcnt vmcnt(12) lgkmcnt(0)
s_barrier
v_add_u32 v18,49152,v12
v_add_u32 v19,49152,v13
T v[192:193],18,0
T v[194:195],18,512
T v[196:197],19,0
T v[198:199],19,512
T v[200:201],18,4096
T v[202:203],18,4608
T v[204:205],19,4096
T v[206:207],19,4608
s_waitcnt vmcnt(8) lgkmcnt(0)
s_barrier
v_add_u32 v18,65536,v12
v_add_u32 v19,65536,v13
T v[208:209],18,0
T v[210:211],18,512
T v[212:213],19,0
T v[214:215],19,512
T v[216:217],18,4096
T v[218:219],18,4608
T v[220:221],19,4096
T v[222:223],19,4608
s_waitcnt vmcnt(4) lgkmcnt(0)
s_barrier
v_add_u32 v18,81920,v12
v_add_u32 v19,81920,v13
T v[224:225],18,0
T v[226:227],18,512
T v[228:229],19,0
T v[230:231],19,512
T v[232:233],18,4096
T v[234:235],18,4608
T v[236:237],19,4096
T v[238:239],19,4608
s_waitcnt vmcnt(0) lgkmcnt(0)
s_barrier
v_add_u32 v18,98304,v12
v_add_u32 v19,98304,v13
T v[240:241],18,0
T v[242:243],18,512
T v[244:245],19,0
T v[246:247],19,512
T v[248:249],18,4096
T v[250:251],18,4608
T v[252:253],19,4096
T v[254:255],19,4608
L 0
s_barrier
v_mov_b32 v4,s53
s_mov_b32 s82,0
s_mov_b32 s80,0
s_mov_b32 s54,s45
s_mul_i32 s57,s44,7168
s_mul_i32 s55,s45,114688
s_add_u32 s55,s55,s57
s_add_u32 s55,s55,40894464
s_mul_i32 s57,s44,7168
s_mul_i32 s60,s46,114688
s_mul_i32 s61,s46,2560
s_mul_i32 s62,s46,32768
s_mul_i32 s63,s46,2048
s_mul_i32 s64,s46,512
s_lshl_b32 s59,s44,8
s_add_u32 s59,s59,143360
s_mul_i32 s58,s45,2560
s_add_u32 s58,s58,109891584
s_mul_i32 s65,s45,32768
s_add_u32 s65,s65,s47
s_mul_i32 s68,s45,2048
s_add_u32 s68,s68,s48
s_mul_i32 s73,s45,512
s_add_u32 s73,s73,106954752
s_mul_i32 s74,s45,32768
s_add_u32 s74,s74,s49
s_mul_i32 s75,s45,1024
s_add_u32 s75,s75,107479040
s_mov_b32 s66,0x7fff0000
s_mov_b32 s67,0x7fff0000
s_mov_b32 s69,0x7fff0000
s_mov_b32 s76,0x7fff0000
s_mov_b32 s77,0x7fff0000
s_mov_b32 s70,0x7fff0000
s_mov_b32 s71,0x7fff0000
s_mov_b32 s72,0x7fff0000
v_mov_b32 v1,v0
v_add_u32 v2,28672,v0
s_mov_b32 s84,28672
s_mov_b32 s86,0x3a800000
s_mov_b32 s87,0x3727c5ac
s_mov_b32 s88,0xc0135761
s_mov_b32 s89,0x3d372713
s_lshr_b32 s85,s44,1
s_mul_i32 s85,s85,127
s_add_u32 s55,s55,s51
v_lshlrev_b32 v8,22,v0
v_and_b32 v8,0x80000000,v8
v_or_b32 v8,v8,v5
v_add_u32 v2,57344,v0
s_lshl_b32 s84,s57,1
s_add_u32 s84,s84,57344
s_mov_b64 s[86:87],%21
s_mov_b64 s[40:41],%21
s_mov_b32 s42,4194304
s_and_b32 s78,s47,65535
s_lshr_b32 s78,s78,4
s_sub_u32 s85,s47,s78
s_add_u32 s85,s85,4194304
s_mul_i32 s73,s44,1280
s_add_u32 s73,s73,114688
v_lshrrev_b32 v62,4,v9
v_and_b32 v62,3,v62
v_mul_u32_u24 v62,288,v62
v_lshrrev_b32 v63,7,v9
v_and_b32 v5,12,v9
v_add3_u32 v5,v5,v62,v63
v_add_u32 v5,s73,v5
v_mov_b32 v10,0
v_mov_b32 v11,0
v_mov_b32 v12,0
v_mov_b32 v13,0
buffer_store_dwordx4 v[10:13],v7,s[40:43],0 offen offset:0 sc0 sc1
s_getpc_b64 s[40:41]
Lg4_pc_%=:
s_lshl_b32 s78,s44,13
v_lshlrev_b32 v62,3,v0
v_add_u32 v62,s78,v62
v_min_u32 v62,Lcode_g4_e-Lg4_pc_%=-4,v62
s_mov_b32 m0,163584
s_nop 0
global_load_lds_dword v62,s[40:41]
s_add_u32 m0,s59,4096
s_nop 0
buffer_load_dword v8,s[36:39],s58 offen offset:0 sc1 lds
buffer_store_dword v4,v3,s[36:39],s71 offen offset:0 sc0 sc1
s_add_u32 m0,s59,5120
s_nop 0
buffer_load_dword v8,s[36:39],s58 offen offset:0 sc1 lds
buffer_store_dword v4,v3,s[36:39],s71 offen offset:0 sc0 sc1
s_mov_b32 s83,0
Lg4_poll_%=:
s_add_u32 s78,s83,2
s_and_b32 s78,s78,3
s_lshl_b32 s78,s78,10
s_add_u32 s78,s78,4096
s_add_u32 m0,s59,s78
s_nop 0
buffer_load_dword v8,s[36:39],s58 offen offset:0 sc1 lds
s_and_b32 s78,s83,3
s_lshl_b32 s78,s78,10
v_add_u32 v62,s78,v6
s_waitcnt vmcnt(4)
s_barrier
ds_read_b32 v7,v62 offset:4096
buffer_store_dword v4,v3,s[36:39],s70 offen offset:0 sc0 sc1
s_mov_b32 s70,s71
s_mov_b32 s71,s72
s_mov_b32 s72,0x7fff0000
s_add_u32 s83,s83,1
s_add_u32 s82,s82,1
L 0
v_cmp_eq_u32 vcc,s53,v7
s_nop 0
s_cmp_eq_u32 vcc_lo,-1
s_cbranch_scc1 Lg4_go_%=
s_cmp_lt_u32 s82,20000
s_cbranch_scc1 Lg4_poll_%=
Lg4_go_%=:
s_add_u32 m0,s57,0
s_add_u32 s56,s55,0
D 0,56,0
D 0,56,1024
D 0,56,2048
D 0,56,3072
s_add_u32 m0,s57,4096
s_add_u32 s56,s56,4096
D 0,56,0
D 0,56,1024
D 0,56,2048
s_add_u32 s58,s58,s61
s_add_u32 m0,s59,2048
s_nop 0
buffer_load_dword v8,s[36:39],s58 offen offset:0 sc1 lds
s_add_u32 m0,s57,28672
s_add_u32 s56,s55,28672
D 0,56,0
D 0,56,1024
D 0,56,2048
D 0,56,3072
s_add_u32 m0,s57,32768
s_add_u32 s56,s56,4096
D 0,56,0
D 0,56,1024
D 0,56,2048
s_add_u32 s55,s55,s60
s_sub_u32 s57,s84,s57
s_add_u32 s54,s54,s46
s_waitcnt vmcnt(7)
s_barrier
buffer_store_dword v4,v3,s[36:39],s70 offen offset:0 sc0 sc1
s_min_u32 s71,s71,s72
buffer_store_dword v4,v3,s[36:39],s71 offen offset:0 sc0 sc1
s_mov_b32 s70,0x7fff0000
s_mov_b32 s71,0x7fff0000
s_mov_b32 s72,0x7fff0000
s_cmp_eq_u32 s80,1
s_cbranch_scc1 Lg4_st1_%=
s_branch Lg4_st0_%=
Lg4_gox_%=:
s_add_u32 s58,s58,s61
s_add_u32 m0,s59,2048
s_nop 0
buffer_load_dword v8,s[36:39],s58 offen offset:0 sc1 lds
s_waitcnt vmcnt(0)
s_barrier
buffer_store_dword v4,v3,s[36:39],s70 offen offset:0 sc0 sc1
s_min_u32 s71,s71,s72
buffer_store_dword v4,v3,s[36:39],s71 offen offset:0 sc0 sc1
s_mov_b32 s70,0x7fff0000
s_mov_b32 s71,0x7fff0000
s_mov_b32 s72,0x7fff0000
s_cmp_eq_u32 s80,1
s_cbranch_scc1 Lg4_st1_%=
Lg4_st0_%=:
ds_read_b32 v7,v6 offset:2048
R 26,1,0
R 30,1,1024
R 34,1,2048
R 38,1,3072
R 42,1,4096
L 5
v_cmp_eq_u32 vcc,s53,v7
L 3
s_cmp_eq_u32 vcc_lo,-1
s_cbranch_scc1 Lg4_F0_%=
s_branch Lg4_L0_%=
Lg4_st1_%=:
ds_read_b32 v7,v6 offset:2048
R 26,2,0
R 30,2,1024
R 34,2,2048
R 38,2,3072
R 42,2,4096
L 5
v_cmp_eq_u32 vcc,s53,v7
L 3
s_cmp_eq_u32 vcc_lo,-1
s_cbranch_scc1 Lg4_F1_%=
s_branch Lg4_L1_%=
Lg4_F1_%=:
s_cmp_eq_u32 s44,1
s_cbranch_scc1 Lg4_F1w1_%=
s_cmp_eq_u32 s44,2
s_cbranch_scc1 Lg4_F1w2_%=
s_cmp_eq_u32 s44,3
s_cbranch_scc1 Lg4_F1w3_%=
s_branch Lg4_F1w0_%=
Lg4_F0_%=:
s_cmp_eq_u32 s44,1
s_cbranch_scc1 Lg4_F0w1_%=
s_cmp_eq_u32 s44,2
s_cbranch_scc1 Lg4_F0w2_%=
s_cmp_eq_u32 s44,3
s_cbranch_scc1 Lg4_F0w3_%=
Lg4_F0w0_%=:
v_mfma_f32_16x16x32_bf16 v[14:17],a[4:7],v[26:29],0
s_add_u32 m0,s57,0
R 46,1,5120
v_lshlrev_b32 v62,16,v58
M 14,a[12:15],30
s_add_u32 s56,s55,0
v_add_f32 v18,v18,v62
v_and_b32 v63,0xffff0000,v58
v_mfma_f32_16x16x32_bf16 v[10:13],a[0:3],v[26:29],0
s_add_u32 s58,s58,s61
R 50,1,6144
v_add_f32 v19,v19,v63
M 10,a[8:11],30
L 3
D 0,56,0
M 10,a[16:19],34
s_mov_b32 s66,s65
R 54,1,7168
v_lshlrev_b32 v62,16,v59
M 10,a[24:27],38
s_add_u32 s65,s65,s62
v_add_f32 v20,v20,v62
v_and_b32 v63,0xffff0000,v59
M 14,a[20:23],34
s_add_u32 s54,s54,s46
R 26,1,8192
v_add_f32 v21,v21,v63
M 14,a[28:31],38
L 3
v_lshlrev_b32 v62,16,v60
)"
R"(v_add_f32 v22,v22,v62
M 14,a[36:39],42
R 30,1,9216
v_and_b32 v63,0xffff0000,v60
M 14,a[44:47],46
D 0,56,1024
M 10,a[32:35],42
R 34,1,10240
v_add_f32 v23,v23,v63
M 10,a[40:43],46
L 3
v_lshlrev_b32 v62,16,v61
v_add_f32 v24,v24,v62
M 10,a[48:51],50
R 38,1,11264
v_and_b32 v63,0xffff0000,v61
M 10,a[56:59],54
buffer_load_dwordx4 v[58:61],v0,s[36:39],s74 offen offset:0 sc1
M 14,a[52:55],50
s_add_u32 s74,s74,s62
R 42,1,12288
v_add_f32 v25,v25,v63
M 14,a[60:63],54
L 3
v_cvt_pk_bf16_f32 v18,v18,v19
v_cvt_pk_bf16_f32 v19,v20,v21
M 14,a[68:71],26
R 46,1,13312
v_cvt_pk_bf16_f32 v20,v22,v23
M 14,a[76:79],30
D 0,56,2048
M 10,a[64:67],26
R 50,1,14336
v_cvt_pk_bf16_f32 v21,v24,v25
M 10,a[72:75],30
L 3
buffer_store_dwordx4 v[18:21],v0,s[36:39],s67 offen offset:0 sc0 sc1
M 10,a[80:83],34
R 54,1,15360
M 10,a[88:91],38
M 14,a[84:87],34
R 26,1,16384
M 14,a[92:95],38
L 3
M 14,a[100:103],42
R 30,1,17408
M 14,a[108:111],46
D 0,56,3072
M 10,a[96:99],42
s_add_u32 m0,s57,4096
s_add_u32 s56,s56,4096
R 34,1,18432
M 10,a[104:107],46
L 3
M 10,a[112:115],50
R 38,1,19456
M 10,a[120:123],54
M 14,a[116:119],50
R 42,1,20480
M 14,a[124:127],54
L 3
D 0,56,0
M 14,a[132:135],26
R 46,1,21504
M 14,a[140:143],30
M 10,a[128:131],26
R 50,1,22528
M 10,a[136:139],30
L 3
M 10,a[144:147],34
R 54,1,23552
M 10,a[152:155],38
M 14,a[148:151],34
R 26,1,24576
M 14,a[156:159],38
L 3
D 0,56,1024
M 14,a[164:167],42
R 30,1,25600
M 14,a[172:175],46
M 10,a[160:163],42
R 34,1,26624
M 10,a[168:171],46
L 3
M 10,a[176:179],50
s_waitcnt vmcnt(8)
R 38,1,27648
M 10,a[184:187],54
s_barrier
M 14,a[180:183],50
R 42,1,28672
M 14,a[188:191],54
L 3
D 0,56,2048
M 14,a[196:199],26
s_add_u32 m0,s59,0
s_add_u32 s56,s55,28672
R 46,1,29696
M 14,a[204:207],30
s_add_u32 s55,s55,s60
M 10,a[192:195],26
R 50,1,30720
M 10,a[200:203],30
L 3
M 10,a[208:211],34
R 54,1,31744
M 10,a[216:219],38
M 14,a[212:215],34
R 26,1,32768
M 14,a[220:223],38
L 3
buffer_load_dword v8,s[36:39],s58 offen offset:0 sc1 lds
M 14,a[228:231],42
s_add_u32 m0,s57,28672
R 30,1,33792
M 14,a[236:239],46
D 0,56,0
M 10,a[224:227],42
R 34,1,34816
M 10,a[232:235],46
L 3
M 10,a[240:243],50
R 38,1,35840
M 10,a[248:251],54
M 14,a[244:247],50
R 42,1,36864
M 14,a[252:255],54
L 3
M 14,v[68:71],26
R 46,1,37888
M 14,v[76:79],30
D 0,56,1024
M 10,v[64:67],26
R 50,1,38912
M 10,v[72:75],30
L 3
M 10,v[80:83],34
R 54,1,39936
M 10,v[88:91],38
M 14,v[84:87],34
R 26,1,40960
M 14,v[92:95],38
L 3
M 14,v[100:103],42
R 30,1,41984
M 14,v[108:111],46
D 0,56,2048
M 10,v[96:99],42
R 34,1,43008
M 10,v[104:107],46
L 3
M 10,v[112:115],50
R 38,1,44032
M 10,v[120:123],54
M 14,v[116:119],50
R 42,1,45056
M 14,v[124:127],54
L 3
D 0,56,3072
M 14,v[132:135],26
s_add_u32 m0,s57,32768
s_add_u32 s56,s56,4096
R 46,1,46080
M 14,v[140:143],30
s_sub_u32 s57,s84,s57
M 10,v[128:131],26
R 50,1,47104
M 10,v[136:139],30
L 3
M 10,v[144:147],34
R 54,1,48128
M 10,v[152:155],38
M 14,v[148:151],34
R 26,1,49152
M 14,v[156:159],38
L 3
D 0,56,0
M 14,v[164:167],42
R 30,1,50176
M 14,v[172:175],46
M 10,v[160:163],42
R 34,1,51200
M 10,v[168:171],46
L 3
M 10,v[176:179],50
R 38,1,52224
M 10,v[184:187],54
M 14,v[180:183],50
R 42,1,53248
M 14,v[188:191],54
L 3
D 0,56,1024
M 14,v[196:199],26
R 46,1,54272
M 14,v[204:207],30
M 10,v[192:195],26
R 50,1,55296
M 10,v[200:203],30
L 3
M 10,v[208:211],34
s_waitcnt vmcnt(6)
R 54,1,56320
M 10,v[216:219],38
s_barrier
ds_read_b32 v7,v6 offset:0
M 14,v[212:215],34
R 26,2,0
M 14,v[220:223],38
L 4
buffer_store_dword v4,v3,s[36:39],s69 offen offset:0 sc0 sc1
M 14,v[228:231],42
s_mov_b32 s69,s68
s_add_u32 s68,s68,s63
R 30,2,1024
M 14,v[236:239],46
D 0,56,2048
M 10,v[224:227],42
R 34,2,2048
M 10,v[232:235],46
L 4
L 3
M 10,v[240:243],50
R 38,2,3072
v_cmp_eq_u32 vcc,s53,v7
M 10,v[248:251],54
s_cmp_eq_u32 vcc_lo,-1
M 14,v[244:247],50
R 42,2,4096
M 14,v[252:255],54
L 3
s_cbranch_scc0 Lg4_L1_%=
Lg4_F1w0_%=:
v_mfma_f32_16x16x32_bf16 v[22:25],a[4:7],v[26:29],0
s_add_u32 m0,s57,0
R 46,2,5120
v_lshlrev_b32 v62,16,v58
M 22,a[12:15],30
s_add_u32 s56,s55,0
v_add_f32 v10,v10,v62
v_and_b32 v63,0xffff0000,v58
v_mfma_f32_16x16x32_bf16 v[18:21],a[0:3],v[26:29],0
s_add_u32 s58,s58,s61
R 50,2,6144
v_add_f32 v11,v11,v63
M 18,a[8:11],30
L 3
D 0,56,0
M 18,a[16:19],34
s_mov_b32 s67,s65
R 54,2,7168
v_lshlrev_b32 v62,16,v59
M 18,a[24:27],38
s_add_u32 s65,s65,s62
v_add_f32 v12,v12,v62
v_and_b32 v63,0xffff0000,v59
M 22,a[20:23],34
s_add_u32 s54,s54,s46
R 26,2,8192
v_add_f32 v13,v13,v63
M 22,a[28:31],38
L 3
v_lshlrev_b32 v62,16,v60
v_add_f32 v14,v14,v62
M 22,a[36:39],42
R 30,2,9216
v_and_b32 v63,0xffff0000,v60
M 22,a[44:47],46
D 0,56,1024
M 18,a[32:35],42
R 34,2,10240
v_add_f32 v15,v15,v63
M 18,a[40:43],46
L 3
v_lshlrev_b32 v62,16,v61
v_add_f32 v16,v16,v62
M 18,a[48:51],50
R 38,2,11264
v_and_b32 v63,0xffff0000,v61
M 18,a[56:59],54
buffer_load_dwordx4 v[58:61],v0,s[36:39],s74 offen offset:0 sc1
M 22,a[52:55],50
s_add_u32 s74,s74,s62
R 42,2,12288
v_add_f32 v17,v17,v63
M 22,a[60:63],54
L 3
v_cvt_pk_bf16_f32 v10,v10,v11
v_cvt_pk_bf16_f32 v11,v12,v13
M 22,a[68:71],26
R 46,2,13312
v_cvt_pk_bf16_f32 v12,v14,v15
M 22,a[76:79],30
D 0,56,2048
M 18,a[64:67],26
R 50,2,14336
v_cvt_pk_bf16_f32 v13,v16,v17
M 18,a[72:75],30
L 3
buffer_store_dwordx4 v[10:13],v0,s[36:39],s66 offen offset:0 sc0 sc1
M 18,a[80:83],34
R 54,2,15360
M 18,a[88:91],38
M 22,a[84:87],34
R 26,2,16384
M 22,a[92:95],38
L 3
M 22,a[100:103],42
R 30,2,17408
M 22,a[108:111],46
D 0,56,3072
M 18,a[96:99],42
s_add_u32 m0,s57,4096
s_add_u32 s56,s56,4096
R 34,2,18432
M 18,a[104:107],46
L 3
M 18,a[112:115],50
R 38,2,19456
M 18,a[120:123],54
M 22,a[116:119],50
R 42,2,20480
M 22,a[124:127],54
L 3
D 0,56,0
M 22,a[132:135],26
R 46,2,21504
M 22,a[140:143],30
M 18,a[128:131],26
R 50,2,22528
M 18,a[136:139],30
L 3
M 18,a[144:147],34
R 54,2,23552
M 18,a[152:155],38
M 22,a[148:151],34
R 26,2,24576
M 22,a[156:159],38
L 3
D 0,56,1024
M 22,a[164:167],42
R 30,2,25600
M 22,a[172:175],46
M 18,a[160:163],42
R 34,2,26624
M 18,a[168:171],46
L 3
M 18,a[176:179],50
s_waitcnt vmcnt(8)
R 38,2,27648
M 18,a[184:187],54
s_barrier
M 22,a[180:183],50
R 42,2,28672
M 22,a[188:191],54
L 3
D 0,56,2048
M 22,a[196:199],26
s_add_u32 m0,s59,1024
s_add_u32 s56,s55,28672
R 46,2,29696
M 22,a[204:207],30
s_add_u32 s55,s55,s60
M 18,a[192:195],26
R 50,2,30720
M 18,a[200:203],30
L 3
M 18,a[208:211],34
R 54,2,31744
M 18,a[216:219],38
M 22,a[212:215],34
R 26,2,32768
M 22,a[220:223],38
L 3
buffer_load_dword v8,s[36:39],s58 offen offset:0 sc1 lds
M 22,a[228:231],42
s_add_u32 m0,s57,28672
R 30,2,33792
M 22,a[236:239],46
D 0,56,0
M 18,a[224:227],42
R 34,2,34816
M 18,a[232:235],46
L 3
M 18,a[240:243],50
R 38,2,35840
M 18,a[248:251],54
M 22,a[244:247],50
R 42,2,36864
M 22,a[252:255],54
L 3
M 22,v[68:71],26
R 46,2,37888
M 22,v[76:79],30
D 0,56,1024
M 18,v[64:67],26
R 50,2,38912
M 18,v[72:75],30
L 3
M 18,v[80:83],34
R 54,2,39936
M 18,v[88:91],38
M 22,v[84:87],34
R 26,2,40960
M 22,v[92:95],38
L 3
M 22,v[100:103],42
R 30,2,41984
M 22,v[108:111],46
D 0,56,2048
M 18,v[96:99],42
R 34,2,43008
M 18,v[104:107],46
L 3
M 18,v[112:115],50
R 38,2,44032
M 18,v[120:123],54
M 22,v[116:119],50
R 42,2,45056
M 22,v[124:127],54
L 3
D 0,56,3072
M 22,v[132:135],26
s_add_u32 m0,s57,32768
s_add_u32 s56,s56,4096
R 46,2,46080
M 22,v[140:143],30
s_sub_u32 s57,s84,s57
M 18,v[128:131],26
R 50,2,47104
M 18,v[136:139],30
L 3
M 18,v[144:147],34
R 54,2,48128
M 18,v[152:155],38
M 22,v[148:151],34
R 26,2,49152
M 22,v[156:159],38
L 3
D 0,56,0
M 22,v[164:167],42
R 30,2,50176
M 22,v[172:175],46
M 18,v[160:163],42
R 34,2,51200
M 18,v[168:171],46
L 3
M 18,v[176:179],50
R 38,2,52224
M 18,v[184:187],54
M 22,v[180:183],50
R 42,2,53248
M 22,v[188:191],54
L 3
D 0,56,1024
M 22,v[196:199],26
R 46,2,54272
M 22,v[204:207],30
M 18,v[192:195],26
R 50,2,55296
M 18,v[200:203],30
L 3
M 18,v[208:211],34
s_waitcnt vmcnt(6)
R 54,2,56320
M 18,v[216:219],38
s_barrier
ds_read_b32 v7,v6 offset:1024
M 22,v[212:215],34
R 26,1,0
M 22,v[220:223],38
L 4
buffer_store_dword v4,v3,s[36:39],s69 offen offset:0 sc0 sc1
M 22,v[228:231],42
s_mov_b32 s69,s68
s_add_u32 s68,s68,s63
R 30,1,1024
M 22,v[236:239],46
D 0,56,2048
M 18,v[224:227],42
R 34,1,2048
M 18,v[232:235],46
L 4
L 3
M 18,v[240:243],50
R 38,1,3072
v_cmp_eq_u32 vcc,s53,v7
M 18,v[248:251],54
s_cmp_eq_u32 vcc_lo,-1
M 22,v[244:247],50
R 42,1,4096
M 22,v[252:255],54
L 3
s_cbranch_scc1 Lg4_F0w0_%=
s_branch Lg4_L0_%=
Lg4_F0w1_%=:
v_mfma_f32_16x16x32_bf16 v[14:17],a[4:7],v[26:29],0
s_add_u32 m0,s57,0
R 46,1,5120
v_lshlrev_b32 v62,16,v58
M 14,a[12:15],30
)"
R"(s_add_u32 s56,s55,0
v_add_f32 v18,v18,v62
v_and_b32 v63,0xffff0000,v58
v_mfma_f32_16x16x32_bf16 v[10:13],a[0:3],v[26:29],0
s_add_u32 s58,s58,s61
R 50,1,6144
v_add_f32 v19,v19,v63
M 10,a[8:11],30
L 3
D 0,56,0
M 10,a[16:19],34
s_mov_b32 s66,s65
R 54,1,7168
v_lshlrev_b32 v62,16,v59
M 10,a[24:27],38
s_add_u32 s65,s65,s62
v_add_f32 v20,v20,v62
v_and_b32 v63,0xffff0000,v59
M 14,a[20:23],34
s_add_u32 s54,s54,s46
R 26,1,8192
v_add_f32 v21,v21,v63
M 14,a[28:31],38
L 3
v_lshlrev_b32 v62,16,v60
v_add_f32 v22,v22,v62
M 14,a[36:39],42
R 30,1,9216
v_and_b32 v63,0xffff0000,v60
M 14,a[44:47],46
D 0,56,1024
M 10,a[32:35],42
R 34,1,10240
v_add_f32 v23,v23,v63
M 10,a[40:43],46
L 3
v_lshlrev_b32 v62,16,v61
v_add_f32 v24,v24,v62
M 10,a[48:51],50
R 38,1,11264
v_and_b32 v63,0xffff0000,v61
M 10,a[56:59],54
buffer_load_dwordx4 v[58:61],v0,s[36:39],s74 offen offset:0 sc1
M 14,a[52:55],50
s_add_u32 s74,s74,s62
R 42,1,12288
v_add_f32 v25,v25,v63
M 14,a[60:63],54
L 3
v_cvt_pk_bf16_f32 v18,v18,v19
v_cvt_pk_bf16_f32 v19,v20,v21
M 14,a[68:71],26
R 46,1,13312
v_cvt_pk_bf16_f32 v20,v22,v23
M 14,a[76:79],30
D 0,56,2048
M 10,a[64:67],26
R 50,1,14336
v_cvt_pk_bf16_f32 v21,v24,v25
M 10,a[72:75],30
L 3
buffer_store_dwordx4 v[18:21],v0,s[36:39],s67 offen offset:0 sc0 sc1
M 10,a[80:83],34
R 54,1,15360
M 10,a[88:91],38
M 14,a[84:87],34
R 26,1,16384
M 14,a[92:95],38
L 3
M 14,a[100:103],42
R 30,1,17408
M 14,a[108:111],46
D 0,56,3072
M 10,a[96:99],42
s_add_u32 m0,s57,4096
s_add_u32 s56,s56,4096
R 34,1,18432
M 10,a[104:107],46
L 3
M 10,a[112:115],50
R 38,1,19456
M 10,a[120:123],54
M 14,a[116:119],50
R 42,1,20480
M 14,a[124:127],54
L 3
D 0,56,0
M 14,a[132:135],26
R 46,1,21504
M 14,a[140:143],30
M 10,a[128:131],26
R 50,1,22528
M 10,a[136:139],30
L 3
M 10,a[144:147],34
R 54,1,23552
M 10,a[152:155],38
M 14,a[148:151],34
R 26,1,24576
M 14,a[156:159],38
L 3
D 0,56,1024
M 14,a[164:167],42
R 30,1,25600
M 14,a[172:175],46
M 10,a[160:163],42
R 34,1,26624
M 10,a[168:171],46
s_waitcnt vmcnt(8) lgkmcnt(3)
M 10,a[176:179],50
s_barrier
R 38,1,27648
M 10,a[184:187],54
M 14,a[180:183],50
R 42,1,28672
M 14,a[188:191],54
L 3
D 0,56,2048
M 14,a[196:199],26
s_add_u32 m0,s59,0
s_add_u32 s56,s55,28672
R 46,1,29696
M 14,a[204:207],30
s_add_u32 s55,s55,s60
M 10,a[192:195],26
R 50,1,30720
M 10,a[200:203],30
L 3
M 10,a[208:211],34
R 54,1,31744
M 10,a[216:219],38
M 14,a[212:215],34
R 26,1,32768
M 14,a[220:223],38
L 3
buffer_load_dword v8,s[36:39],s58 offen offset:0 sc1 lds
M 14,a[228:231],42
s_add_u32 m0,s57,28672
R 30,1,33792
M 14,a[236:239],46
D 0,56,0
M 10,a[224:227],42
R 34,1,34816
M 10,a[232:235],46
L 3
M 10,a[240:243],50
R 38,1,35840
M 10,a[248:251],54
M 14,a[244:247],50
R 42,1,36864
M 14,a[252:255],54
L 3
M 14,v[68:71],26
R 46,1,37888
M 14,v[76:79],30
D 0,56,1024
M 10,v[64:67],26
R 50,1,38912
M 10,v[72:75],30
L 3
M 10,v[80:83],34
R 54,1,39936
M 10,v[88:91],38
M 14,v[84:87],34
R 26,1,40960
M 14,v[92:95],38
L 3
M 14,v[100:103],42
R 30,1,41984
M 14,v[108:111],46
D 0,56,2048
M 10,v[96:99],42
R 34,1,43008
M 10,v[104:107],46
L 3
M 10,v[112:115],50
R 38,1,44032
M 10,v[120:123],54
M 14,v[116:119],50
R 42,1,45056
M 14,v[124:127],54
L 3
D 0,56,3072
M 14,v[132:135],26
s_add_u32 m0,s57,32768
s_add_u32 s56,s56,4096
R 46,1,46080
M 14,v[140:143],30
s_sub_u32 s57,s84,s57
M 10,v[128:131],26
R 50,1,47104
M 10,v[136:139],30
L 3
M 10,v[144:147],34
R 54,1,48128
M 10,v[152:155],38
M 14,v[148:151],34
R 26,1,49152
M 14,v[156:159],38
L 3
D 0,56,0
M 14,v[164:167],42
R 30,1,50176
M 14,v[172:175],46
M 10,v[160:163],42
R 34,1,51200
M 10,v[168:171],46
L 3
M 10,v[176:179],50
R 38,1,52224
M 10,v[184:187],54
M 14,v[180:183],50
R 42,1,53248
M 14,v[188:191],54
L 3
D 0,56,1024
M 14,v[196:199],26
R 46,1,54272
M 14,v[204:207],30
M 10,v[192:195],26
R 50,1,55296
M 10,v[200:203],30
s_waitcnt vmcnt(6) lgkmcnt(3)
M 10,v[208:211],34
s_barrier
R 54,1,56320
ds_read_b32 v7,v6 offset:0
M 10,v[216:219],38
buffer_store_dword v4,v3,s[36:39],s69 offen offset:0 sc0 sc1
M 14,v[212:215],34
s_mov_b32 s69,s68
s_add_u32 s68,s68,s63
R 26,2,0
M 14,v[220:223],38
L 4
D 0,56,2048
M 14,v[228:231],42
R 30,2,1024
M 14,v[236:239],46
M 10,v[224:227],42
L 2
R 34,2,2048
v_cmp_eq_u32 vcc,s53,v7
M 10,v[232:235],46
s_nop 0
s_cmp_eq_u32 vcc_lo,-1
M 10,v[240:243],50
R 38,2,3072
M 10,v[248:251],54
M 14,v[244:247],50
R 42,2,4096
M 14,v[252:255],54
L 3
s_cbranch_scc0 Lg4_L1_%=
Lg4_F1w1_%=:
v_mfma_f32_16x16x32_bf16 v[22:25],a[4:7],v[26:29],0
s_add_u32 m0,s57,0
R 46,2,5120
v_lshlrev_b32 v62,16,v58
M 22,a[12:15],30
s_add_u32 s56,s55,0
v_add_f32 v10,v10,v62
v_and_b32 v63,0xffff0000,v58
v_mfma_f32_16x16x32_bf16 v[18:21],a[0:3],v[26:29],0
s_add_u32 s58,s58,s61
R 50,2,6144
v_add_f32 v11,v11,v63
M 18,a[8:11],30
L 3
D 0,56,0
M 18,a[16:19],34
s_mov_b32 s67,s65
R 54,2,7168
v_lshlrev_b32 v62,16,v59
M 18,a[24:27],38
s_add_u32 s65,s65,s62
v_add_f32 v12,v12,v62
v_and_b32 v63,0xffff0000,v59
M 22,a[20:23],34
s_add_u32 s54,s54,s46
R 26,2,8192
v_add_f32 v13,v13,v63
M 22,a[28:31],38
L 3
v_lshlrev_b32 v62,16,v60
v_add_f32 v14,v14,v62
M 22,a[36:39],42
R 30,2,9216
v_and_b32 v63,0xffff0000,v60
M 22,a[44:47],46
D 0,56,1024
M 18,a[32:35],42
R 34,2,10240
v_add_f32 v15,v15,v63
M 18,a[40:43],46
L 3
v_lshlrev_b32 v62,16,v61
v_add_f32 v16,v16,v62
M 18,a[48:51],50
R 38,2,11264
v_and_b32 v63,0xffff0000,v61
M 18,a[56:59],54
buffer_load_dwordx4 v[58:61],v0,s[36:39],s74 offen offset:0 sc1
M 22,a[52:55],50
s_add_u32 s74,s74,s62
R 42,2,12288
v_add_f32 v17,v17,v63
M 22,a[60:63],54
L 3
v_cvt_pk_bf16_f32 v10,v10,v11
v_cvt_pk_bf16_f32 v11,v12,v13
M 22,a[68:71],26
R 46,2,13312
v_cvt_pk_bf16_f32 v12,v14,v15
M 22,a[76:79],30
D 0,56,2048
M 18,a[64:67],26
R 50,2,14336
v_cvt_pk_bf16_f32 v13,v16,v17
M 18,a[72:75],30
L 3
buffer_store_dwordx4 v[10:13],v0,s[36:39],s66 offen offset:0 sc0 sc1
M 18,a[80:83],34
R 54,2,15360
M 18,a[88:91],38
M 22,a[84:87],34
R 26,2,16384
M 22,a[92:95],38
L 3
M 22,a[100:103],42
R 30,2,17408
M 22,a[108:111],46
D 0,56,3072
M 18,a[96:99],42
s_add_u32 m0,s57,4096
s_add_u32 s56,s56,4096
R 34,2,18432
M 18,a[104:107],46
L 3
M 18,a[112:115],50
R 38,2,19456
M 18,a[120:123],54
M 22,a[116:119],50
R 42,2,20480
M 22,a[124:127],54
L 3
D 0,56,0
M 22,a[132:135],26
R 46,2,21504
M 22,a[140:143],30
M 18,a[128:131],26
R 50,2,22528
M 18,a[136:139],30
L 3
M 18,a[144:147],34
R 54,2,23552
M 18,a[152:155],38
M 22,a[148:151],34
R 26,2,24576
M 22,a[156:159],38
L 3
D 0,56,1024
M 22,a[164:167],42
R 30,2,25600
M 22,a[172:175],46
M 18,a[160:163],42
R 34,2,26624
M 18,a[168:171],46
s_waitcnt vmcnt(8) lgkmcnt(3)
M 18,a[176:179],50
s_barrier
R 38,2,27648
M 18,a[184:187],54
M 22,a[180:183],50
R 42,2,28672
M 22,a[188:191],54
L 3
D 0,56,2048
M 22,a[196:199],26
s_add_u32 m0,s59,1024
s_add_u32 s56,s55,28672
R 46,2,29696
M 22,a[204:207],30
s_add_u32 s55,s55,s60
M 18,a[192:195],26
R 50,2,30720
M 18,a[200:203],30
L 3
M 18,a[208:211],34
R 54,2,31744
M 18,a[216:219],38
M 22,a[212:215],34
R 26,2,32768
M 22,a[220:223],38
L 3
buffer_load_dword v8,s[36:39],s58 offen offset:0 sc1 lds
M 22,a[228:231],42
s_add_u32 m0,s57,28672
R 30,2,33792
M 22,a[236:239],46
D 0,56,0
M 18,a[224:227],42
R 34,2,34816
M 18,a[232:235],46
L 3
M 18,a[240:243],50
R 38,2,35840
M 18,a[248:251],54
M 22,a[244:247],50
R 42,2,36864
M 22,a[252:255],54
L 3
M 22,v[68:71],26
R 46,2,37888
M 22,v[76:79],30
D 0,56,1024
M 18,v[64:67],26
R 50,2,38912
M 18,v[72:75],30
L 3
M 18,v[80:83],34
R 54,2,39936
M 18,v[88:91],38
M 22,v[84:87],34
R 26,2,40960
M 22,v[92:95],38
L 3
M 22,v[100:103],42
R 30,2,41984
M 22,v[108:111],46
D 0,56,2048
M 18,v[96:99],42
R 34,2,43008
M 18,v[104:107],46
L 3
M 18,v[112:115],50
R 38,2,44032
M 18,v[120:123],54
M 22,v[116:119],50
R 42,2,45056
M 22,v[124:127],54
L 3
D 0,56,3072
M 22,v[132:135],26
s_add_u32 m0,s57,32768
s_add_u32 s56,s56,4096
R 46,2,46080
M 22,v[140:143],30
s_sub_u32 s57,s84,s57
M 18,v[128:131],26
R 50,2,47104
M 18,v[136:139],30
L 3
M 18,v[144:147],34
R 54,2,48128
M 18,v[152:155],38
M 22,v[148:151],34
R 26,2,49152
M 22,v[156:159],38
L 3
D 0,56,0
M 22,v[164:167],42
R 30,2,50176
M 22,v[172:175],46
M 18,v[160:163],42
R 34,2,51200
M 18,v[168:171],46
L 3
M 18,v[176:179],50
R 38,2,52224
M 18,v[184:187],54
M 22,v[180:183],50
R 42,2,53248
M 22,v[188:191],54
L 3
D 0,56,1024
M 22,v[196:199],26
R 46,2,54272
M 22,v[204:207],30
M 18,v[192:195],26
R 50,2,55296
M 18,v[200:203],30
s_waitcnt vmcnt(6) lgkmcnt(3)
M 18,v[208:211],34
s_barrier
R 54,2,56320
ds_read_b32 v7,v6 offset:1024
M 18,v[216:219],38
buffer_store_dword v4,v3,s[36:39],s69 offen offset:0 sc0 sc1
M 22,v[212:215],34
s_mov_b32 s69,s68
s_add_u32 s68,s68,s63
R 26,1,0
M 22,v[220:223],38
L 4
D 0,56,2048
M 22,v[228:231],42
R 30,1,1024
M 22,v[236:239],46
M 18,v[224:227],42
)"
R"(L 2
R 34,1,2048
v_cmp_eq_u32 vcc,s53,v7
M 18,v[232:235],46
s_nop 0
s_cmp_eq_u32 vcc_lo,-1
M 18,v[240:243],50
R 38,1,3072
M 18,v[248:251],54
M 22,v[244:247],50
R 42,1,4096
M 22,v[252:255],54
L 3
s_cbranch_scc1 Lg4_F0w1_%=
s_branch Lg4_L0_%=
Lg4_F0w2_%=:
v_mfma_f32_16x16x32_bf16 v[14:17],a[4:7],v[26:29],0
s_add_u32 m0,s57,0
R 46,1,5120
v_lshlrev_b32 v62,16,v58
M 14,a[12:15],30
s_add_u32 s56,s55,0
v_add_f32 v18,v18,v62
v_and_b32 v63,0xffff0000,v58
v_mfma_f32_16x16x32_bf16 v[10:13],a[0:3],v[26:29],0
s_add_u32 s58,s58,s61
R 50,1,6144
v_add_f32 v19,v19,v63
M 10,a[8:11],30
L 3
D 0,56,0
M 10,a[16:19],34
s_mov_b32 s66,s65
R 54,1,7168
v_lshlrev_b32 v62,16,v59
M 10,a[24:27],38
s_add_u32 s65,s65,s62
v_add_f32 v20,v20,v62
v_and_b32 v63,0xffff0000,v59
M 14,a[20:23],34
s_add_u32 s54,s54,s46
R 26,1,8192
v_add_f32 v21,v21,v63
M 14,a[28:31],38
L 3
v_lshlrev_b32 v62,16,v60
v_add_f32 v22,v22,v62
M 14,a[36:39],42
R 30,1,9216
v_and_b32 v63,0xffff0000,v60
M 14,a[44:47],46
D 0,56,1024
M 10,a[32:35],42
R 34,1,10240
v_add_f32 v23,v23,v63
M 10,a[40:43],46
L 3
v_lshlrev_b32 v62,16,v61
v_add_f32 v24,v24,v62
M 10,a[48:51],50
R 38,1,11264
v_and_b32 v63,0xffff0000,v61
M 10,a[56:59],54
buffer_load_dwordx4 v[58:61],v0,s[36:39],s74 offen offset:0 sc1
M 14,a[52:55],50
s_add_u32 s74,s74,s62
R 42,1,12288
v_add_f32 v25,v25,v63
M 14,a[60:63],54
L 3
v_cvt_pk_bf16_f32 v18,v18,v19
v_cvt_pk_bf16_f32 v19,v20,v21
M 14,a[68:71],26
R 46,1,13312
v_cvt_pk_bf16_f32 v20,v22,v23
M 14,a[76:79],30
D 0,56,2048
M 10,a[64:67],26
R 50,1,14336
v_cvt_pk_bf16_f32 v21,v24,v25
M 10,a[72:75],30
L 3
buffer_store_dwordx4 v[18:21],v0,s[36:39],s67 offen offset:0 sc0 sc1
M 10,a[80:83],34
R 54,1,15360
M 10,a[88:91],38
M 14,a[84:87],34
R 26,1,16384
M 14,a[92:95],38
L 3
M 14,a[100:103],42
R 30,1,17408
M 14,a[108:111],46
D 0,56,3072
M 10,a[96:99],42
s_add_u32 m0,s57,4096
s_add_u32 s56,s56,4096
R 34,1,18432
M 10,a[104:107],46
L 3
M 10,a[112:115],50
R 38,1,19456
M 10,a[120:123],54
M 14,a[116:119],50
R 42,1,20480
M 14,a[124:127],54
L 3
D 0,56,0
M 14,a[132:135],26
R 46,1,21504
M 14,a[140:143],30
M 10,a[128:131],26
R 50,1,22528
M 10,a[136:139],30
L 3
M 10,a[144:147],34
R 54,1,23552
M 10,a[152:155],38
M 14,a[148:151],34
R 26,1,24576
M 14,a[156:159],38
L 3
D 0,56,1024
M 14,a[164:167],42
R 30,1,25600
M 14,a[172:175],46
M 10,a[160:163],42
s_waitcnt vmcnt(8)
R 34,1,26624
M 10,a[168:171],46
s_barrier
L 3
M 10,a[176:179],50
R 38,1,27648
M 10,a[184:187],54
M 14,a[180:183],50
R 42,1,28672
M 14,a[188:191],54
L 3
D 0,56,2048
M 14,a[196:199],26
s_add_u32 m0,s59,0
s_add_u32 s56,s55,28672
R 46,1,29696
M 14,a[204:207],30
s_add_u32 s55,s55,s60
M 10,a[192:195],26
R 50,1,30720
M 10,a[200:203],30
L 3
M 10,a[208:211],34
R 54,1,31744
M 10,a[216:219],38
M 14,a[212:215],34
R 26,1,32768
M 14,a[220:223],38
L 3
buffer_load_dword v8,s[36:39],s58 offen offset:0 sc1 lds
M 14,a[228:231],42
s_add_u32 m0,s57,28672
R 30,1,33792
M 14,a[236:239],46
D 0,56,0
M 10,a[224:227],42
R 34,1,34816
M 10,a[232:235],46
L 3
M 10,a[240:243],50
R 38,1,35840
M 10,a[248:251],54
M 14,a[244:247],50
R 42,1,36864
M 14,a[252:255],54
L 3
M 14,v[68:71],26
R 46,1,37888
M 14,v[76:79],30
D 0,56,1024
M 10,v[64:67],26
R 50,1,38912
M 10,v[72:75],30
L 3
M 10,v[80:83],34
R 54,1,39936
M 10,v[88:91],38
M 14,v[84:87],34
R 26,1,40960
M 14,v[92:95],38
L 3
M 14,v[100:103],42
R 30,1,41984
M 14,v[108:111],46
D 0,56,2048
M 10,v[96:99],42
R 34,1,43008
M 10,v[104:107],46
L 3
M 10,v[112:115],50
R 38,1,44032
M 10,v[120:123],54
M 14,v[116:119],50
R 42,1,45056
M 14,v[124:127],54
L 3
D 0,56,3072
M 14,v[132:135],26
s_add_u32 m0,s57,32768
s_add_u32 s56,s56,4096
R 46,1,46080
M 14,v[140:143],30
s_sub_u32 s57,s84,s57
M 10,v[128:131],26
R 50,1,47104
M 10,v[136:139],30
L 3
M 10,v[144:147],34
R 54,1,48128
M 10,v[152:155],38
M 14,v[148:151],34
R 26,1,49152
M 14,v[156:159],38
L 3
D 0,56,0
M 14,v[164:167],42
R 30,1,50176
M 14,v[172:175],46
M 10,v[160:163],42
R 34,1,51200
M 10,v[168:171],46
L 3
M 10,v[176:179],50
R 38,1,52224
M 10,v[184:187],54
M 14,v[180:183],50
R 42,1,53248
M 14,v[188:191],54
L 3
D 0,56,1024
M 14,v[196:199],26
R 46,1,54272
M 14,v[204:207],30
M 10,v[192:195],26
s_waitcnt vmcnt(6)
R 50,1,55296
M 10,v[200:203],30
L 3
s_barrier
buffer_store_dword v4,v3,s[36:39],s69 offen offset:0 sc0 sc1
M 10,v[208:211],34
s_mov_b32 s69,s68
R 54,1,56320
ds_read_b32 v7,v6 offset:0
M 10,v[216:219],38
s_add_u32 s68,s68,s63
M 14,v[212:215],34
R 26,2,0
M 14,v[220:223],38
L 4
D 0,56,2048
M 14,v[228:231],42
R 30,2,1024
M 14,v[236:239],46
M 10,v[224:227],42
L 2
R 34,2,2048
v_cmp_eq_u32 vcc,s53,v7
M 10,v[232:235],46
s_nop 0
s_cmp_eq_u32 vcc_lo,-1
M 10,v[240:243],50
R 38,2,3072
M 10,v[248:251],54
M 14,v[244:247],50
R 42,2,4096
M 14,v[252:255],54
L 3
s_cbranch_scc0 Lg4_L1_%=
Lg4_F1w2_%=:
v_mfma_f32_16x16x32_bf16 v[22:25],a[4:7],v[26:29],0
s_add_u32 m0,s57,0
R 46,2,5120
v_lshlrev_b32 v62,16,v58
M 22,a[12:15],30
s_add_u32 s56,s55,0
v_add_f32 v10,v10,v62
v_and_b32 v63,0xffff0000,v58
v_mfma_f32_16x16x32_bf16 v[18:21],a[0:3],v[26:29],0
s_add_u32 s58,s58,s61
R 50,2,6144
v_add_f32 v11,v11,v63
M 18,a[8:11],30
L 3
D 0,56,0
M 18,a[16:19],34
s_mov_b32 s67,s65
R 54,2,7168
v_lshlrev_b32 v62,16,v59
M 18,a[24:27],38
s_add_u32 s65,s65,s62
v_add_f32 v12,v12,v62
v_and_b32 v63,0xffff0000,v59
M 22,a[20:23],34
s_add_u32 s54,s54,s46
R 26,2,8192
v_add_f32 v13,v13,v63
M 22,a[28:31],38
L 3
v_lshlrev_b32 v62,16,v60
v_add_f32 v14,v14,v62
M 22,a[36:39],42
R 30,2,9216
v_and_b32 v63,0xffff0000,v60
M 22,a[44:47],46
D 0,56,1024
M 18,a[32:35],42
R 34,2,10240
v_add_f32 v15,v15,v63
M 18,a[40:43],46
L 3
v_lshlrev_b32 v62,16,v61
v_add_f32 v16,v16,v62
M 18,a[48:51],50
R 38,2,11264
v_and_b32 v63,0xffff0000,v61
M 18,a[56:59],54
buffer_load_dwordx4 v[58:61],v0,s[36:39],s74 offen offset:0 sc1
M 22,a[52:55],50
s_add_u32 s74,s74,s62
R 42,2,12288
v_add_f32 v17,v17,v63
M 22,a[60:63],54
L 3
v_cvt_pk_bf16_f32 v10,v10,v11
v_cvt_pk_bf16_f32 v11,v12,v13
M 22,a[68:71],26
R 46,2,13312
v_cvt_pk_bf16_f32 v12,v14,v15
M 22,a[76:79],30
D 0,56,2048
M 18,a[64:67],26
R 50,2,14336
v_cvt_pk_bf16_f32 v13,v16,v17
M 18,a[72:75],30
L 3
buffer_store_dwordx4 v[10:13],v0,s[36:39],s66 offen offset:0 sc0 sc1
M 18,a[80:83],34
R 54,2,15360
M 18,a[88:91],38
M 22,a[84:87],34
R 26,2,16384
M 22,a[92:95],38
L 3
M 22,a[100:103],42
R 30,2,17408
M 22,a[108:111],46
D 0,56,3072
M 18,a[96:99],42
s_add_u32 m0,s57,4096
s_add_u32 s56,s56,4096
R 34,2,18432
M 18,a[104:107],46
L 3
M 18,a[112:115],50
R 38,2,19456
M 18,a[120:123],54
M 22,a[116:119],50
R 42,2,20480
M 22,a[124:127],54
L 3
D 0,56,0
M 22,a[132:135],26
R 46,2,21504
M 22,a[140:143],30
M 18,a[128:131],26
R 50,2,22528
M 18,a[136:139],30
L 3
M 18,a[144:147],34
R 54,2,23552
M 18,a[152:155],38
M 22,a[148:151],34
R 26,2,24576
M 22,a[156:159],38
L 3
D 0,56,1024
M 22,a[164:167],42
R 30,2,25600
M 22,a[172:175],46
M 18,a[160:163],42
s_waitcnt vmcnt(8)
R 34,2,26624
M 18,a[168:171],46
s_barrier
L 3
M 18,a[176:179],50
R 38,2,27648
M 18,a[184:187],54
M 22,a[180:183],50
R 42,2,28672
M 22,a[188:191],54
L 3
D 0,56,2048
M 22,a[196:199],26
s_add_u32 m0,s59,1024
s_add_u32 s56,s55,28672
R 46,2,29696
M 22,a[204:207],30
s_add_u32 s55,s55,s60
M 18,a[192:195],26
R 50,2,30720
M 18,a[200:203],30
L 3
M 18,a[208:211],34
R 54,2,31744
M 18,a[216:219],38
M 22,a[212:215],34
R 26,2,32768
M 22,a[220:223],38
L 3
buffer_load_dword v8,s[36:39],s58 offen offset:0 sc1 lds
M 22,a[228:231],42
s_add_u32 m0,s57,28672
R 30,2,33792
M 22,a[236:239],46
D 0,56,0
M 18,a[224:227],42
R 34,2,34816
M 18,a[232:235],46
L 3
M 18,a[240:243],50
R 38,2,35840
M 18,a[248:251],54
M 22,a[244:247],50
R 42,2,36864
M 22,a[252:255],54
L 3
M 22,v[68:71],26
R 46,2,37888
M 22,v[76:79],30
D 0,56,1024
M 18,v[64:67],26
R 50,2,38912
M 18,v[72:75],30
L 3
M 18,v[80:83],34
R 54,2,39936
M 18,v[88:91],38
M 22,v[84:87],34
R 26,2,40960
M 22,v[92:95],38
L 3
M 22,v[100:103],42
R 30,2,41984
M 22,v[108:111],46
D 0,56,2048
M 18,v[96:99],42
R 34,2,43008
M 18,v[104:107],46
L 3
M 18,v[112:115],50
R 38,2,44032
M 18,v[120:123],54
M 22,v[116:119],50
R 42,2,45056
M 22,v[124:127],54
L 3
D 0,56,3072
M 22,v[132:135],26
s_add_u32 m0,s57,32768
s_add_u32 s56,s56,4096
R 46,2,46080
M 22,v[140:143],30
s_sub_u32 s57,s84,s57
M 18,v[128:131],26
R 50,2,47104
M 18,v[136:139],30
L 3
M 18,v[144:147],34
R 54,2,48128
M 18,v[152:155],38
M 22,v[148:151],34
R 26,2,49152
M 22,v[156:159],38
L 3
D 0,56,0
M 22,v[164:167],42
R 30,2,50176
M 22,v[172:175],46
M 18,v[160:163],42
R 34,2,51200
M 18,v[168:171],46
L 3
M 18,v[176:179],50
R 38,2,52224
M 18,v[184:187],54
M 22,v[180:183],50
R 42,2,53248
M 22,v[188:191],54
L 3
D 0,56,1024
)"
R"(M 22,v[196:199],26
R 46,2,54272
M 22,v[204:207],30
M 18,v[192:195],26
s_waitcnt vmcnt(6)
R 50,2,55296
M 18,v[200:203],30
L 3
s_barrier
buffer_store_dword v4,v3,s[36:39],s69 offen offset:0 sc0 sc1
M 18,v[208:211],34
s_mov_b32 s69,s68
R 54,2,56320
ds_read_b32 v7,v6 offset:1024
M 18,v[216:219],38
s_add_u32 s68,s68,s63
M 22,v[212:215],34
R 26,1,0
M 22,v[220:223],38
L 4
D 0,56,2048
M 22,v[228:231],42
R 30,1,1024
M 22,v[236:239],46
M 18,v[224:227],42
L 2
R 34,1,2048
v_cmp_eq_u32 vcc,s53,v7
M 18,v[232:235],46
s_nop 0
s_cmp_eq_u32 vcc_lo,-1
M 18,v[240:243],50
R 38,1,3072
M 18,v[248:251],54
M 22,v[244:247],50
R 42,1,4096
M 22,v[252:255],54
L 3
s_cbranch_scc1 Lg4_F0w2_%=
s_branch Lg4_L0_%=
Lg4_F0w3_%=:
v_mfma_f32_16x16x32_bf16 v[14:17],a[4:7],v[26:29],0
s_add_u32 m0,s57,0
R 46,1,5120
v_lshlrev_b32 v62,16,v58
M 14,a[12:15],30
s_add_u32 s56,s55,0
v_add_f32 v18,v18,v62
v_and_b32 v63,0xffff0000,v58
v_mfma_f32_16x16x32_bf16 v[10:13],a[0:3],v[26:29],0
s_add_u32 s58,s58,s61
R 50,1,6144
v_add_f32 v19,v19,v63
M 10,a[8:11],30
L 3
D 0,56,0
M 10,a[16:19],34
s_mov_b32 s66,s65
R 54,1,7168
v_lshlrev_b32 v62,16,v59
M 10,a[24:27],38
s_add_u32 s65,s65,s62
v_add_f32 v20,v20,v62
v_and_b32 v63,0xffff0000,v59
M 14,a[20:23],34
s_add_u32 s54,s54,s46
R 26,1,8192
v_add_f32 v21,v21,v63
M 14,a[28:31],38
L 3
v_lshlrev_b32 v62,16,v60
v_add_f32 v22,v22,v62
M 14,a[36:39],42
R 30,1,9216
v_and_b32 v63,0xffff0000,v60
M 14,a[44:47],46
D 0,56,1024
M 10,a[32:35],42
R 34,1,10240
v_add_f32 v23,v23,v63
M 10,a[40:43],46
L 3
v_lshlrev_b32 v62,16,v61
v_add_f32 v24,v24,v62
M 10,a[48:51],50
R 38,1,11264
v_and_b32 v63,0xffff0000,v61
M 10,a[56:59],54
buffer_load_dwordx4 v[58:61],v0,s[36:39],s74 offen offset:0 sc1
M 14,a[52:55],50
s_add_u32 s74,s74,s62
R 42,1,12288
v_add_f32 v25,v25,v63
M 14,a[60:63],54
L 3
v_cvt_pk_bf16_f32 v18,v18,v19
v_cvt_pk_bf16_f32 v19,v20,v21
M 14,a[68:71],26
R 46,1,13312
v_cvt_pk_bf16_f32 v20,v22,v23
M 14,a[76:79],30
D 0,56,2048
M 10,a[64:67],26
R 50,1,14336
v_cvt_pk_bf16_f32 v21,v24,v25
M 10,a[72:75],30
L 3
buffer_store_dwordx4 v[18:21],v0,s[36:39],s67 offen offset:0 sc0 sc1
M 10,a[80:83],34
R 54,1,15360
M 10,a[88:91],38
M 14,a[84:87],34
R 26,1,16384
M 14,a[92:95],38
L 3
M 14,a[100:103],42
R 30,1,17408
M 14,a[108:111],46
D 0,56,3072
M 10,a[96:99],42
s_add_u32 m0,s57,4096
s_add_u32 s56,s56,4096
R 34,1,18432
M 10,a[104:107],46
L 3
M 10,a[112:115],50
R 38,1,19456
M 10,a[120:123],54
M 14,a[116:119],50
R 42,1,20480
M 14,a[124:127],54
L 3
D 0,56,0
M 14,a[132:135],26
R 46,1,21504
M 14,a[140:143],30
M 10,a[128:131],26
R 50,1,22528
M 10,a[136:139],30
L 3
M 10,a[144:147],34
R 54,1,23552
M 10,a[152:155],38
M 14,a[148:151],34
R 26,1,24576
M 14,a[156:159],38
L 3
D 0,56,1024
M 14,a[164:167],42
R 30,1,25600
M 14,a[172:175],46
s_waitcnt vmcnt(8)
M 10,a[160:163],42
s_barrier
R 34,1,26624
M 10,a[168:171],46
L 3
M 10,a[176:179],50
R 38,1,27648
M 10,a[184:187],54
M 14,a[180:183],50
R 42,1,28672
M 14,a[188:191],54
L 3
D 0,56,2048
M 14,a[196:199],26
s_add_u32 m0,s59,0
s_add_u32 s56,s55,28672
R 46,1,29696
M 14,a[204:207],30
s_add_u32 s55,s55,s60
M 10,a[192:195],26
R 50,1,30720
M 10,a[200:203],30
L 3
M 10,a[208:211],34
R 54,1,31744
M 10,a[216:219],38
M 14,a[212:215],34
R 26,1,32768
M 14,a[220:223],38
L 3
buffer_load_dword v8,s[36:39],s58 offen offset:0 sc1 lds
M 14,a[228:231],42
s_add_u32 m0,s57,28672
R 30,1,33792
M 14,a[236:239],46
D 0,56,0
M 10,a[224:227],42
R 34,1,34816
M 10,a[232:235],46
L 3
M 10,a[240:243],50
R 38,1,35840
M 10,a[248:251],54
M 14,a[244:247],50
R 42,1,36864
M 14,a[252:255],54
L 3
M 14,v[68:71],26
R 46,1,37888
M 14,v[76:79],30
D 0,56,1024
M 10,v[64:67],26
R 50,1,38912
M 10,v[72:75],30
L 3
M 10,v[80:83],34
R 54,1,39936
M 10,v[88:91],38
M 14,v[84:87],34
R 26,1,40960
M 14,v[92:95],38
L 3
M 14,v[100:103],42
R 30,1,41984
M 14,v[108:111],46
D 0,56,2048
M 10,v[96:99],42
R 34,1,43008
M 10,v[104:107],46
L 3
M 10,v[112:115],50
R 38,1,44032
M 10,v[120:123],54
M 14,v[116:119],50
R 42,1,45056
M 14,v[124:127],54
L 3
D 0,56,3072
M 14,v[132:135],26
s_add_u32 m0,s57,32768
s_add_u32 s56,s56,4096
R 46,1,46080
M 14,v[140:143],30
s_sub_u32 s57,s84,s57
M 10,v[128:131],26
R 50,1,47104
M 10,v[136:139],30
L 3
M 10,v[144:147],34
R 54,1,48128
M 10,v[152:155],38
M 14,v[148:151],34
R 26,1,49152
M 14,v[156:159],38
L 3
D 0,56,0
M 14,v[164:167],42
R 30,1,50176
M 14,v[172:175],46
M 10,v[160:163],42
R 34,1,51200
M 10,v[168:171],46
L 3
M 10,v[176:179],50
R 38,1,52224
M 10,v[184:187],54
M 14,v[180:183],50
R 42,1,53248
M 14,v[188:191],54
L 3
D 0,56,1024
M 14,v[196:199],26
R 46,1,54272
M 14,v[204:207],30
s_waitcnt vmcnt(6)
M 10,v[192:195],26
s_barrier
R 50,1,55296
ds_read_b32 v7,v6 offset:0
M 10,v[200:203],30
L 4
buffer_store_dword v4,v3,s[36:39],s69 offen offset:0 sc0 sc1
M 10,v[208:211],34
s_mov_b32 s69,s68
s_add_u32 s68,s68,s63
R 54,1,56320
M 10,v[216:219],38
M 14,v[212:215],34
R 26,2,0
M 14,v[220:223],38
L 4
D 0,56,2048
M 14,v[228:231],42
L 2
R 30,2,1024
v_cmp_eq_u32 vcc,s53,v7
M 14,v[236:239],46
s_cmp_eq_u32 vcc_lo,-1
M 10,v[224:227],42
R 34,2,2048
M 10,v[232:235],46
L 3
M 10,v[240:243],50
R 38,2,3072
M 10,v[248:251],54
M 14,v[244:247],50
R 42,2,4096
M 14,v[252:255],54
L 3
s_cbranch_scc0 Lg4_L1_%=
Lg4_F1w3_%=:
v_mfma_f32_16x16x32_bf16 v[22:25],a[4:7],v[26:29],0
s_add_u32 m0,s57,0
R 46,2,5120
v_lshlrev_b32 v62,16,v58
M 22,a[12:15],30
s_add_u32 s56,s55,0
v_add_f32 v10,v10,v62
v_and_b32 v63,0xffff0000,v58
v_mfma_f32_16x16x32_bf16 v[18:21],a[0:3],v[26:29],0
s_add_u32 s58,s58,s61
R 50,2,6144
v_add_f32 v11,v11,v63
M 18,a[8:11],30
L 3
D 0,56,0
M 18,a[16:19],34
s_mov_b32 s67,s65
R 54,2,7168
v_lshlrev_b32 v62,16,v59
M 18,a[24:27],38
s_add_u32 s65,s65,s62
v_add_f32 v12,v12,v62
v_and_b32 v63,0xffff0000,v59
M 22,a[20:23],34
s_add_u32 s54,s54,s46
R 26,2,8192
v_add_f32 v13,v13,v63
M 22,a[28:31],38
L 3
v_lshlrev_b32 v62,16,v60
v_add_f32 v14,v14,v62
M 22,a[36:39],42
R 30,2,9216
v_and_b32 v63,0xffff0000,v60
M 22,a[44:47],46
D 0,56,1024
M 18,a[32:35],42
R 34,2,10240
v_add_f32 v15,v15,v63
M 18,a[40:43],46
L 3
v_lshlrev_b32 v62,16,v61
v_add_f32 v16,v16,v62
M 18,a[48:51],50
R 38,2,11264
v_and_b32 v63,0xffff0000,v61
M 18,a[56:59],54
buffer_load_dwordx4 v[58:61],v0,s[36:39],s74 offen offset:0 sc1
M 22,a[52:55],50
s_add_u32 s74,s74,s62
R 42,2,12288
v_add_f32 v17,v17,v63
M 22,a[60:63],54
L 3
v_cvt_pk_bf16_f32 v10,v10,v11
v_cvt_pk_bf16_f32 v11,v12,v13
M 22,a[68:71],26
R 46,2,13312
v_cvt_pk_bf16_f32 v12,v14,v15
M 22,a[76:79],30
D 0,56,2048
M 18,a[64:67],26
R 50,2,14336
v_cvt_pk_bf16_f32 v13,v16,v17
M 18,a[72:75],30
L 3
buffer_store_dwordx4 v[10:13],v0,s[36:39],s66 offen offset:0 sc0 sc1
M 18,a[80:83],34
R 54,2,15360
M 18,a[88:91],38
M 22,a[84:87],34
R 26,2,16384
M 22,a[92:95],38
L 3
M 22,a[100:103],42
R 30,2,17408
M 22,a[108:111],46
D 0,56,3072
M 18,a[96:99],42
s_add_u32 m0,s57,4096
s_add_u32 s56,s56,4096
R 34,2,18432
M 18,a[104:107],46
L 3
M 18,a[112:115],50
R 38,2,19456
M 18,a[120:123],54
M 22,a[116:119],50
R 42,2,20480
M 22,a[124:127],54
L 3
D 0,56,0
M 22,a[132:135],26
R 46,2,21504
M 22,a[140:143],30
M 18,a[128:131],26
R 50,2,22528
M 18,a[136:139],30
L 3
M 18,a[144:147],34
R 54,2,23552
M 18,a[152:155],38
M 22,a[148:151],34
R 26,2,24576
M 22,a[156:159],38
L 3
D 0,56,1024
M 22,a[164:167],42
R 30,2,25600
M 22,a[172:175],46
s_waitcnt vmcnt(8)
M 18,a[160:163],42
s_barrier
R 34,2,26624
M 18,a[168:171],46
L 3
M 18,a[176:179],50
R 38,2,27648
M 18,a[184:187],54
M 22,a[180:183],50
R 42,2,28672
M 22,a[188:191],54
L 3
D 0,56,2048
M 22,a[196:199],26
s_add_u32 m0,s59,1024
s_add_u32 s56,s55,28672
R 46,2,29696
M 22,a[204:207],30
s_add_u32 s55,s55,s60
M 18,a[192:195],26
R 50,2,30720
M 18,a[200:203],30
L 3
M 18,a[208:211],34
R 54,2,31744
M 18,a[216:219],38
M 22,a[212:215],34
R 26,2,32768
M 22,a[220:223],38
L 3
buffer_load_dword v8,s[36:39],s58 offen offset:0 sc1 lds
M 22,a[228:231],42
s_add_u32 m0,s57,28672
R 30,2,33792
M 22,a[236:239],46
D 0,56,0
M 18,a[224:227],42
R 34,2,34816
M 18,a[232:235],46
L 3
M 18,a[240:243],50
R 38,2,35840
M 18,a[248:251],54
M 22,a[244:247],50
R 42,2,36864
M 22,a[252:255],54
L 3
M 22,v[68:71],26
R 46,2,37888
M 22,v[76:79],30
D 0,56,1024
M 18,v[64:67],26
R 50,2,38912
M 18,v[72:75],30
L 3
M 18,v[80:83],34
R 54,2,39936
M 18,v[88:91],38
M 22,v[84:87],34
R 26,2,40960
M 22,v[92:95],38
L 3
M 22,v[100:103],42
R 30,2,41984
M 22,v[108:111],46
D 0,56,2048
M 18,v[96:99],42
R 34,2,43008
M 18,v[104:107],46
L 3
M 18,v[112:115],50
R 38,2,44032
M 18,v[120:123],54
M 22,v[116:119],50
R 42,2,45056
M 22,v[124:127],54
L 3
D 0,56,3072
M 22,v[132:135],26
s_add_u32 m0,s57,32768
s_add_u32 s56,s56,4096
R 46,2,46080
M 22,v[140:143],30
s_sub_u32 s57,s84,s57
M 18,v[128:131],26
R 50,2,47104
)"
R"(M 18,v[136:139],30
L 3
M 18,v[144:147],34
R 54,2,48128
M 18,v[152:155],38
M 22,v[148:151],34
R 26,2,49152
M 22,v[156:159],38
L 3
D 0,56,0
M 22,v[164:167],42
R 30,2,50176
M 22,v[172:175],46
M 18,v[160:163],42
R 34,2,51200
M 18,v[168:171],46
L 3
M 18,v[176:179],50
R 38,2,52224
M 18,v[184:187],54
M 22,v[180:183],50
R 42,2,53248
M 22,v[188:191],54
L 3
D 0,56,1024
M 22,v[196:199],26
R 46,2,54272
M 22,v[204:207],30
s_waitcnt vmcnt(6)
M 18,v[192:195],26
s_barrier
R 50,2,55296
ds_read_b32 v7,v6 offset:1024
M 18,v[200:203],30
L 4
buffer_store_dword v4,v3,s[36:39],s69 offen offset:0 sc0 sc1
M 18,v[208:211],34
s_mov_b32 s69,s68
s_add_u32 s68,s68,s63
R 54,2,56320
M 18,v[216:219],38
M 22,v[212:215],34
R 26,1,0
M 22,v[220:223],38
L 4
D 0,56,2048
M 22,v[228:231],42
L 2
R 30,1,1024
v_cmp_eq_u32 vcc,s53,v7
M 22,v[236:239],46
s_cmp_eq_u32 vcc_lo,-1
M 18,v[224:227],42
R 34,1,2048
M 18,v[232:235],46
L 3
M 18,v[240:243],50
R 38,1,3072
M 18,v[248:251],54
M 22,v[244:247],50
R 42,1,4096
M 22,v[252:255],54
L 3
s_cbranch_scc1 Lg4_F0w3_%=
Lg4_L0_%=:
s_cmp_ge_u32 s54,256
s_cbranch_scc1 Lg4_Z0_%=
v_mfma_f32_16x16x32_bf16 v[14:17],a[4:7],v[26:29],0
s_add_u32 m0,s59,3072
R 46,1,5120
v_lshlrev_b32 v62,16,v58
M 14,a[12:15],30
s_mov_b32 s66,s65
buffer_load_dword v8,s[36:39],s58 offen offset:0 sc1 lds
v_mfma_f32_16x16x32_bf16 v[10:13],a[0:3],v[26:29],0
s_add_u32 s65,s65,s62
R 50,1,6144
v_add_f32 v18,v18,v62
M 10,a[8:11],30
L 3
v_and_b32 v63,0xffff0000,v58
v_add_f32 v19,v19,v63
M 10,a[16:19],34
R 54,1,7168
v_lshlrev_b32 v62,16,v59
M 10,a[24:27],38
v_add_f32 v20,v20,v62
v_and_b32 v63,0xffff0000,v59
M 14,a[20:23],34
R 26,1,8192
v_add_f32 v21,v21,v63
M 14,a[28:31],38
L 3
v_lshlrev_b32 v62,16,v60
v_add_f32 v22,v22,v62
M 14,a[36:39],42
R 30,1,9216
v_and_b32 v63,0xffff0000,v60
M 14,a[44:47],46
v_add_f32 v23,v23,v63
v_lshlrev_b32 v62,16,v61
M 10,a[32:35],42
R 34,1,10240
v_add_f32 v24,v24,v62
M 10,a[40:43],46
L 3
v_and_b32 v63,0xffff0000,v61
v_add_f32 v25,v25,v63
M 10,a[48:51],50
R 38,1,11264
v_cvt_pk_bf16_f32 v18,v18,v19
M 10,a[56:59],54
buffer_load_dwordx4 v[58:61],v0,s[36:39],s74 offen offset:0 sc1
M 14,a[52:55],50
s_add_u32 s74,s74,s62
R 42,1,12288
v_cvt_pk_bf16_f32 v19,v20,v21
M 14,a[60:63],54
L 3
v_cvt_pk_bf16_f32 v20,v22,v23
v_cvt_pk_bf16_f32 v21,v24,v25
M 14,a[68:71],26
R 46,1,13312
M 14,a[76:79],30
buffer_store_dwordx4 v[18:21],v0,s[36:39],s67 offen offset:0 sc0 sc1
M 10,a[64:67],26
R 50,1,14336
M 10,a[72:75],30
L 3
M 10,a[80:83],34
R 54,1,15360
M 10,a[88:91],38
M 14,a[84:87],34
R 26,1,16384
M 14,a[92:95],38
L 3
M 14,a[100:103],42
R 30,1,17408
M 14,a[108:111],46
M 10,a[96:99],42
R 34,1,18432
M 10,a[104:107],46
L 3
M 10,a[112:115],50
R 38,1,19456
M 10,a[120:123],54
M 14,a[116:119],50
R 42,1,20480
M 14,a[124:127],54
L 3
M 14,a[132:135],26
R 46,1,21504
M 14,a[140:143],30
M 10,a[128:131],26
R 50,1,22528
M 10,a[136:139],30
L 3
M 10,a[144:147],34
R 54,1,23552
M 10,a[152:155],38
M 14,a[148:151],34
R 26,1,24576
M 14,a[156:159],38
L 3
M 14,a[164:167],42
R 30,1,25600
M 14,a[172:175],46
M 10,a[160:163],42
R 34,1,26624
M 10,a[168:171],46
L 3
M 10,a[176:179],50
s_waitcnt vmcnt(2)
R 38,1,27648
M 10,a[184:187],54
s_barrier
ds_read_b32 v7,v6 offset:3072
M 14,a[180:183],50
R 42,1,28672
M 14,a[188:191],54
L 4
M 14,a[196:199],26
R 46,1,29696
M 14,a[204:207],30
M 10,a[192:195],26
R 50,1,30720
M 10,a[200:203],30
L 4
L 3
v_cmp_eq_u32 vcc,s53,v7
M 10,a[208:211],34
R 54,1,31744
M 10,a[216:219],38
s_cmp_eq_u32 vcc_lo,-1
s_cbranch_scc0 Lg4_X0_%=
M 14,a[212:215],34
s_add_u32 m0,s57,0
s_add_u32 s56,s55,0
s_add_u32 s58,s58,s61
R 26,1,32768
M 14,a[220:223],38
L 3
s_add_u32 s54,s54,s46
M 14,a[228:231],42
R 30,1,33792
M 14,a[236:239],46
D 0,56,0
M 10,a[224:227],42
R 34,1,34816
M 10,a[232:235],46
L 3
M 10,a[240:243],50
R 38,1,35840
M 10,a[248:251],54
D 0,56,1024
M 14,a[244:247],50
R 42,1,36864
M 14,a[252:255],54
L 3
M 14,v[68:71],26
R 46,1,37888
M 14,v[76:79],30
D 0,56,2048
M 10,v[64:67],26
R 50,1,38912
M 10,v[72:75],30
L 3
M 10,v[80:83],34
R 54,1,39936
M 10,v[88:91],38
D 0,56,3072
M 14,v[84:87],34
s_add_u32 m0,s57,4096
s_add_u32 s56,s56,4096
R 26,1,40960
M 14,v[92:95],38
L 3
M 14,v[100:103],42
R 30,1,41984
M 14,v[108:111],46
D 0,56,0
M 10,v[96:99],42
R 34,1,43008
M 10,v[104:107],46
L 3
M 10,v[112:115],50
R 38,1,44032
M 10,v[120:123],54
D 0,56,1024
M 14,v[116:119],50
R 42,1,45056
M 14,v[124:127],54
L 3
M 14,v[132:135],26
R 46,1,46080
M 14,v[140:143],30
D 0,56,2048
M 10,v[128:131],26
s_add_u32 m0,s59,0
s_add_u32 s56,s55,28672
R 50,1,47104
M 10,v[136:139],30
L 3
buffer_load_dword v8,s[36:39],s58 offen offset:0 sc1 lds
M 10,v[144:147],34
s_add_u32 m0,s57,28672
s_add_u32 s55,s55,s60
R 54,1,48128
M 10,v[152:155],38
M 14,v[148:151],34
R 26,1,49152
M 14,v[156:159],38
L 3
D 0,56,0
M 14,v[164:167],42
R 30,1,50176
M 14,v[172:175],46
D 0,56,1024
M 10,v[160:163],42
R 34,1,51200
M 10,v[168:171],46
L 3
M 10,v[176:179],50
R 38,1,52224
M 10,v[184:187],54
D 0,56,2048
M 14,v[180:183],50
R 42,1,53248
M 14,v[188:191],54
L 3
M 14,v[196:199],26
R 46,1,54272
M 14,v[204:207],30
D 0,56,3072
M 10,v[192:195],26
s_add_u32 m0,s57,32768
s_add_u32 s56,s56,4096
R 50,1,55296
M 10,v[200:203],30
L 3
s_sub_u32 s57,s84,s57
M 10,v[208:211],34
s_waitcnt vmcnt(4)
R 54,1,56320
M 10,v[216:219],38
s_barrier
ds_read_b32 v7,v6 offset:0
M 14,v[212:215],34
R 26,2,0
M 14,v[220:223],38
L 4
buffer_store_dword v4,v3,s[36:39],s69 offen offset:0 sc0 sc1
M 14,v[228:231],42
s_mov_b32 s69,s68
s_add_u32 s68,s68,s63
R 30,2,1024
M 14,v[236:239],46
D 0,56,0
M 10,v[224:227],42
R 34,2,2048
M 10,v[232:235],46
L 4
D 0,56,1024
M 10,v[240:243],50
L 3
R 38,2,3072
v_cmp_eq_u32 vcc,s53,v7
M 10,v[248:251],54
s_cmp_eq_u32 vcc_lo,-1
D 0,56,2048
M 14,v[244:247],50
R 42,2,4096
M 14,v[252:255],54
L 3
s_cbranch_scc1 Lg4_F1_%=
s_branch Lg4_L1_%=
Lg4_X0_%=:
M 14,a[212:215],34
s_add_u32 m0,s59,0
R 26,1,32768
M 14,a[220:223],38
L 3
buffer_load_dword v8,s[36:39],s58 offen offset:0 sc1 lds
M 14,a[228:231],42
R 30,1,33792
M 14,a[236:239],46
M 10,a[224:227],42
R 34,1,34816
M 10,a[232:235],46
L 3
M 10,a[240:243],50
R 38,1,35840
M 10,a[248:251],54
M 14,a[244:247],50
R 42,1,36864
M 14,a[252:255],54
L 3
M 14,v[68:71],26
R 46,1,37888
M 14,v[76:79],30
M 10,v[64:67],26
R 50,1,38912
M 10,v[72:75],30
L 3
M 10,v[80:83],34
R 54,1,39936
M 10,v[88:91],38
M 14,v[84:87],34
R 26,1,40960
M 14,v[92:95],38
L 3
M 14,v[100:103],42
R 30,1,41984
M 14,v[108:111],46
M 10,v[96:99],42
R 34,1,43008
M 10,v[104:107],46
L 3
M 10,v[112:115],50
R 38,1,44032
M 10,v[120:123],54
M 14,v[116:119],50
R 42,1,45056
M 14,v[124:127],54
L 3
M 14,v[132:135],26
R 46,1,46080
M 14,v[140:143],30
M 10,v[128:131],26
R 50,1,47104
M 10,v[136:139],30
L 3
M 10,v[144:147],34
R 54,1,48128
M 10,v[152:155],38
M 14,v[148:151],34
R 26,1,49152
M 14,v[156:159],38
L 3
M 14,v[164:167],42
R 30,1,50176
M 14,v[172:175],46
M 10,v[160:163],42
R 34,1,51200
M 10,v[168:171],46
L 3
M 10,v[176:179],50
R 38,1,52224
M 10,v[184:187],54
M 14,v[180:183],50
R 42,1,53248
M 14,v[188:191],54
L 3
M 14,v[196:199],26
R 46,1,54272
M 14,v[204:207],30
M 10,v[192:195],26
R 50,1,55296
M 10,v[200:203],30
L 3
M 10,v[208:211],34
s_waitcnt vmcnt(0)
R 54,1,56320
M 10,v[216:219],38
s_barrier
buffer_store_dword v4,v3,s[36:39],s69 offen offset:0 sc0 sc1
M 14,v[212:215],34
s_mov_b32 s69,s68
s_add_u32 s68,s68,s63
ds_read_b32 v7,v6 offset:0
M 14,v[220:223],38
L 3
M 14,v[228:231],42
M 14,v[236:239],46
M 10,v[224:227],42
M 10,v[232:235],46
L 1
M 10,v[240:243],50
L 0
v_cmp_eq_u32 vcc,s53,v7
M 10,v[248:251],54
M 14,v[244:247],50
M 14,v[252:255],54
s_cmp_lt_u32 s54,251
s_cbranch_scc1 Lg4_xa0_%=
s_sub_u32 s75,s66,s85
v_lshlrev_b32 v62,16,v58
s_nop 1
v_add_f32 v10,v10,v62
v_and_b32 v63,0xffff0000,v58
v_add_f32 v11,v11,v63
v_lshlrev_b32 v62,16,v59
v_add_f32 v12,v12,v62
v_and_b32 v63,0xffff0000,v59
v_add_f32 v13,v13,v63
v_lshlrev_b32 v62,16,v60
v_add_f32 v14,v14,v62
v_and_b32 v63,0xffff0000,v60
v_add_f32 v15,v15,v63
v_lshlrev_b32 v62,16,v61
v_add_f32 v16,v16,v62
v_and_b32 v63,0xffff0000,v61
v_add_f32 v17,v17,v63
v_cvt_pk_bf16_f32 v10,v10,v11
v_cvt_pk_bf16_f32 v11,v12,v13
v_cvt_pk_bf16_f32 v12,v14,v15
v_cvt_pk_bf16_f32 v13,v16,v17
v_lshrrev_b32 v62,8,v0
v_lshl_add_u32 v62,v62,5,v0
v_add_u32 v62,s73,v62
ds_write_b128 v62,v[10:13] offset:0
ds_read2_b32 v[14:15],v5 offset0:0 offset1:16
ds_read2_b32 v[16:17],v5 offset0:32 offset1:48
s_mov_b64 s[40:41],s[86:87]
s_mov_b32 s42,4194304
s_mov_b32 s43,0x00020000
L 0
s_add_u32 s64,s75,0
buffer_atomic_pk_add_bf16 v14,v9,s[40:43],s64 offen sc1
s_add_u32 s64,s75,8192
buffer_atomic_pk_add_bf16 v15,v9,s[40:43],s64 offen sc1
s_add_u32 s64,s75,16384
buffer_atomic_pk_add_bf16 v16,v9,s[40:43],s64 offen sc1
s_add_u32 s64,s75,24576
buffer_atomic_pk_add_bf16 v17,v9,s[40:43],s64 offen sc1
s_mov_b32 s66,0x7fff0000
s_mov_b32 s69,0x7fff0000
Lg4_xa0_%=:
s_mov_b32 s80,1
s_cmp_eq_u32 vcc_lo,-1
s_cbranch_scc0 Lg4_xn0_%=
s_add_u32 m0,s57,0
s_add_u32 s56,s55,0
D 0,56,0
v_lshlrev_b32 v62,16,v58
D 0,56,1024
v_add_f32 v10,v10,v62
D 0,56,2048
v_and_b32 v63,0xffff0000,v58
D 0,56,3072
)"
R"(v_add_f32 v11,v11,v63
s_add_u32 m0,s57,4096
s_add_u32 s56,s56,4096
D 0,56,0
v_lshlrev_b32 v62,16,v59
D 0,56,1024
v_add_f32 v12,v12,v62
D 0,56,2048
v_and_b32 v63,0xffff0000,v59
s_add_u32 m0,s57,28672
s_add_u32 s56,s55,28672
D 0,56,0
v_add_f32 v13,v13,v63
D 0,56,1024
v_lshlrev_b32 v62,16,v60
D 0,56,2048
v_add_f32 v14,v14,v62
D 0,56,3072
v_and_b32 v63,0xffff0000,v60
s_add_u32 m0,s57,32768
s_add_u32 s56,s56,4096
D 0,56,0
v_add_f32 v15,v15,v63
D 0,56,1024
v_lshlrev_b32 v62,16,v61
D 0,56,2048
v_add_f32 v16,v16,v62
s_add_u32 s55,s55,s60
s_sub_u32 s57,s84,s57
s_add_u32 s54,s54,s46
v_and_b32 v63,0xffff0000,v61
v_add_f32 v17,v17,v63
v_cvt_pk_bf16_f32 v10,v10,v11
v_cvt_pk_bf16_f32 v11,v12,v13
v_cvt_pk_bf16_f32 v12,v14,v15
v_cvt_pk_bf16_f32 v13,v16,v17
buffer_store_dwordx4 v[10:13],v0,s[36:39],s66 offen offset:0 sc0 sc1
s_mov_b32 s72,s69
s_mov_b32 s69,0x7fff0000
s_mov_b32 s66,0x7fff0000
s_mov_b32 s76,0x7fff0000
s_branch Lg4_gox_%=
Lg4_xn0_%=:
s_cmp_ge_u32 s54,256
s_cbranch_scc1 Lg4_fin0_%=
s_add_u32 m0,s59,4096
s_nop 0
buffer_load_dword v8,s[36:39],s58 offen offset:0 sc1 lds
buffer_store_dword v4,v3,s[36:39],s71 offen offset:0 sc0 sc1
s_add_u32 m0,s59,5120
s_nop 0
buffer_load_dword v8,s[36:39],s58 offen offset:0 sc1 lds
buffer_store_dword v4,v3,s[36:39],s71 offen offset:0 sc0 sc1
s_mov_b32 s83,0
v_lshlrev_b32 v62,16,v58
v_add_f32 v10,v10,v62
v_and_b32 v63,0xffff0000,v58
v_add_f32 v11,v11,v63
v_lshlrev_b32 v62,16,v59
v_add_f32 v12,v12,v62
v_and_b32 v63,0xffff0000,v59
v_add_f32 v13,v13,v63
v_lshlrev_b32 v62,16,v60
v_add_f32 v14,v14,v62
v_and_b32 v63,0xffff0000,v60
v_add_f32 v15,v15,v63
v_lshlrev_b32 v62,16,v61
v_add_f32 v16,v16,v62
v_and_b32 v63,0xffff0000,v61
v_add_f32 v17,v17,v63
v_cvt_pk_bf16_f32 v10,v10,v11
v_cvt_pk_bf16_f32 v11,v12,v13
v_cvt_pk_bf16_f32 v12,v14,v15
v_cvt_pk_bf16_f32 v13,v16,v17
buffer_store_dwordx4 v[10:13],v0,s[36:39],s66 offen offset:0 sc0 sc1
s_mov_b32 s72,s69
s_mov_b32 s69,0x7fff0000
s_mov_b32 s66,0x7fff0000
s_mov_b32 s76,0x7fff0000
s_branch Lg4_poll_%=
Lg4_fin0_%=:
v_lshlrev_b32 v62,16,v58
v_add_f32 v10,v10,v62
v_and_b32 v63,0xffff0000,v58
v_add_f32 v11,v11,v63
v_lshlrev_b32 v62,16,v59
v_add_f32 v12,v12,v62
v_and_b32 v63,0xffff0000,v59
v_add_f32 v13,v13,v63
v_lshlrev_b32 v62,16,v60
v_add_f32 v14,v14,v62
v_and_b32 v63,0xffff0000,v60
v_add_f32 v15,v15,v63
v_lshlrev_b32 v62,16,v61
v_add_f32 v16,v16,v62
v_and_b32 v63,0xffff0000,v61
v_add_f32 v17,v17,v63
v_cvt_pk_bf16_f32 v10,v10,v11
v_cvt_pk_bf16_f32 v11,v12,v13
v_cvt_pk_bf16_f32 v12,v14,v15
v_cvt_pk_bf16_f32 v13,v16,v17
buffer_store_dwordx4 v[10:13],v0,s[36:39],s66 offen offset:0 sc0 sc1
s_mov_b32 s72,s69
s_mov_b32 s69,0x7fff0000
s_mov_b32 s66,0x7fff0000
s_mov_b32 s76,0x7fff0000
s_branch Lg4_done_%=
Lg4_L1_%=:
s_cmp_ge_u32 s54,256
s_cbranch_scc1 Lg4_Z1_%=
v_mfma_f32_16x16x32_bf16 v[22:25],a[4:7],v[26:29],0
s_add_u32 m0,s59,3072
R 46,2,5120
v_lshlrev_b32 v62,16,v58
M 22,a[12:15],30
s_mov_b32 s67,s65
buffer_load_dword v8,s[36:39],s58 offen offset:0 sc1 lds
v_mfma_f32_16x16x32_bf16 v[18:21],a[0:3],v[26:29],0
s_add_u32 s65,s65,s62
R 50,2,6144
v_add_f32 v10,v10,v62
M 18,a[8:11],30
L 3
v_and_b32 v63,0xffff0000,v58
v_add_f32 v11,v11,v63
M 18,a[16:19],34
R 54,2,7168
v_lshlrev_b32 v62,16,v59
M 18,a[24:27],38
v_add_f32 v12,v12,v62
v_and_b32 v63,0xffff0000,v59
M 22,a[20:23],34
R 26,2,8192
v_add_f32 v13,v13,v63
M 22,a[28:31],38
L 3
v_lshlrev_b32 v62,16,v60
v_add_f32 v14,v14,v62
M 22,a[36:39],42
R 30,2,9216
v_and_b32 v63,0xffff0000,v60
M 22,a[44:47],46
v_add_f32 v15,v15,v63
v_lshlrev_b32 v62,16,v61
M 18,a[32:35],42
R 34,2,10240
v_add_f32 v16,v16,v62
M 18,a[40:43],46
L 3
v_and_b32 v63,0xffff0000,v61
v_add_f32 v17,v17,v63
M 18,a[48:51],50
R 38,2,11264
v_cvt_pk_bf16_f32 v10,v10,v11
M 18,a[56:59],54
buffer_load_dwordx4 v[58:61],v0,s[36:39],s74 offen offset:0 sc1
M 22,a[52:55],50
s_add_u32 s74,s74,s62
R 42,2,12288
v_cvt_pk_bf16_f32 v11,v12,v13
M 22,a[60:63],54
L 3
v_cvt_pk_bf16_f32 v12,v14,v15
v_cvt_pk_bf16_f32 v13,v16,v17
M 22,a[68:71],26
R 46,2,13312
M 22,a[76:79],30
buffer_store_dwordx4 v[10:13],v0,s[36:39],s66 offen offset:0 sc0 sc1
M 18,a[64:67],26
R 50,2,14336
M 18,a[72:75],30
L 3
M 18,a[80:83],34
R 54,2,15360
M 18,a[88:91],38
M 22,a[84:87],34
R 26,2,16384
M 22,a[92:95],38
L 3
M 22,a[100:103],42
R 30,2,17408
M 22,a[108:111],46
M 18,a[96:99],42
R 34,2,18432
M 18,a[104:107],46
L 3
M 18,a[112:115],50
R 38,2,19456
M 18,a[120:123],54
M 22,a[116:119],50
R 42,2,20480
M 22,a[124:127],54
L 3
M 22,a[132:135],26
R 46,2,21504
M 22,a[140:143],30
M 18,a[128:131],26
R 50,2,22528
M 18,a[136:139],30
L 3
M 18,a[144:147],34
R 54,2,23552
M 18,a[152:155],38
M 22,a[148:151],34
R 26,2,24576
M 22,a[156:159],38
L 3
M 22,a[164:167],42
R 30,2,25600
M 22,a[172:175],46
M 18,a[160:163],42
R 34,2,26624
M 18,a[168:171],46
L 3
M 18,a[176:179],50
s_waitcnt vmcnt(2)
R 38,2,27648
M 18,a[184:187],54
s_barrier
ds_read_b32 v7,v6 offset:3072
M 22,a[180:183],50
R 42,2,28672
M 22,a[188:191],54
L 4
M 22,a[196:199],26
R 46,2,29696
M 22,a[204:207],30
M 18,a[192:195],26
R 50,2,30720
M 18,a[200:203],30
L 4
L 3
v_cmp_eq_u32 vcc,s53,v7
M 18,a[208:211],34
R 54,2,31744
M 18,a[216:219],38
s_cmp_eq_u32 vcc_lo,-1
s_cbranch_scc0 Lg4_X1_%=
M 22,a[212:215],34
s_add_u32 m0,s57,0
s_add_u32 s56,s55,0
s_add_u32 s58,s58,s61
R 26,2,32768
M 22,a[220:223],38
L 3
s_add_u32 s54,s54,s46
M 22,a[228:231],42
R 30,2,33792
M 22,a[236:239],46
D 0,56,0
M 18,a[224:227],42
R 34,2,34816
M 18,a[232:235],46
L 3
M 18,a[240:243],50
R 38,2,35840
M 18,a[248:251],54
D 0,56,1024
M 22,a[244:247],50
R 42,2,36864
M 22,a[252:255],54
L 3
M 22,v[68:71],26
R 46,2,37888
M 22,v[76:79],30
D 0,56,2048
M 18,v[64:67],26
R 50,2,38912
M 18,v[72:75],30
L 3
M 18,v[80:83],34
R 54,2,39936
M 18,v[88:91],38
D 0,56,3072
M 22,v[84:87],34
s_add_u32 m0,s57,4096
s_add_u32 s56,s56,4096
R 26,2,40960
M 22,v[92:95],38
L 3
M 22,v[100:103],42
R 30,2,41984
M 22,v[108:111],46
D 0,56,0
M 18,v[96:99],42
R 34,2,43008
M 18,v[104:107],46
L 3
M 18,v[112:115],50
R 38,2,44032
M 18,v[120:123],54
D 0,56,1024
M 22,v[116:119],50
R 42,2,45056
M 22,v[124:127],54
L 3
M 22,v[132:135],26
R 46,2,46080
M 22,v[140:143],30
D 0,56,2048
M 18,v[128:131],26
s_add_u32 m0,s59,1024
s_add_u32 s56,s55,28672
R 50,2,47104
M 18,v[136:139],30
L 3
buffer_load_dword v8,s[36:39],s58 offen offset:0 sc1 lds
M 18,v[144:147],34
s_add_u32 m0,s57,28672
s_add_u32 s55,s55,s60
R 54,2,48128
M 18,v[152:155],38
M 22,v[148:151],34
R 26,2,49152
M 22,v[156:159],38
L 3
D 0,56,0
M 22,v[164:167],42
R 30,2,50176
M 22,v[172:175],46
D 0,56,1024
M 18,v[160:163],42
R 34,2,51200
M 18,v[168:171],46
L 3
M 18,v[176:179],50
R 38,2,52224
M 18,v[184:187],54
D 0,56,2048
M 22,v[180:183],50
R 42,2,53248
M 22,v[188:191],54
L 3
M 22,v[196:199],26
R 46,2,54272
M 22,v[204:207],30
D 0,56,3072
M 18,v[192:195],26
s_add_u32 m0,s57,32768
s_add_u32 s56,s56,4096
R 50,2,55296
M 18,v[200:203],30
L 3
s_sub_u32 s57,s84,s57
M 18,v[208:211],34
s_waitcnt vmcnt(4)
R 54,2,56320
M 18,v[216:219],38
s_barrier
ds_read_b32 v7,v6 offset:1024
M 22,v[212:215],34
R 26,1,0
M 22,v[220:223],38
L 4
buffer_store_dword v4,v3,s[36:39],s69 offen offset:0 sc0 sc1
M 22,v[228:231],42
s_mov_b32 s69,s68
s_add_u32 s68,s68,s63
R 30,1,1024
M 22,v[236:239],46
D 0,56,0
M 18,v[224:227],42
R 34,1,2048
M 18,v[232:235],46
L 4
D 0,56,1024
M 18,v[240:243],50
L 3
R 38,1,3072
v_cmp_eq_u32 vcc,s53,v7
M 18,v[248:251],54
s_cmp_eq_u32 vcc_lo,-1
D 0,56,2048
M 22,v[244:247],50
R 42,1,4096
M 22,v[252:255],54
L 3
s_cbranch_scc1 Lg4_F0_%=
s_branch Lg4_L0_%=
Lg4_X1_%=:
M 22,a[212:215],34
s_add_u32 m0,s59,1024
R 26,2,32768
M 22,a[220:223],38
L 3
buffer_load_dword v8,s[36:39],s58 offen offset:0 sc1 lds
M 22,a[228:231],42
R 30,2,33792
M 22,a[236:239],46
M 18,a[224:227],42
R 34,2,34816
M 18,a[232:235],46
L 3
M 18,a[240:243],50
R 38,2,35840
M 18,a[248:251],54
M 22,a[244:247],50
R 42,2,36864
M 22,a[252:255],54
L 3
M 22,v[68:71],26
R 46,2,37888
M 22,v[76:79],30
M 18,v[64:67],26
R 50,2,38912
M 18,v[72:75],30
L 3
M 18,v[80:83],34
R 54,2,39936
M 18,v[88:91],38
M 22,v[84:87],34
R 26,2,40960
M 22,v[92:95],38
L 3
M 22,v[100:103],42
R 30,2,41984
M 22,v[108:111],46
M 18,v[96:99],42
R 34,2,43008
M 18,v[104:107],46
L 3
M 18,v[112:115],50
R 38,2,44032
M 18,v[120:123],54
M 22,v[116:119],50
R 42,2,45056
M 22,v[124:127],54
L 3
M 22,v[132:135],26
R 46,2,46080
M 22,v[140:143],30
M 18,v[128:131],26
R 50,2,47104
M 18,v[136:139],30
L 3
M 18,v[144:147],34
R 54,2,48128
M 18,v[152:155],38
M 22,v[148:151],34
R 26,2,49152
M 22,v[156:159],38
L 3
M 22,v[164:167],42
R 30,2,50176
M 22,v[172:175],46
M 18,v[160:163],42
R 34,2,51200
M 18,v[168:171],46
L 3
M 18,v[176:179],50
R 38,2,52224
M 18,v[184:187],54
M 22,v[180:183],50
R 42,2,53248
M 22,v[188:191],54
L 3
M 22,v[196:199],26
R 46,2,54272
M 22,v[204:207],30
M 18,v[192:195],26
R 50,2,55296
M 18,v[200:203],30
L 3
M 18,v[208:211],34
s_waitcnt vmcnt(0)
R 54,2,56320
M 18,v[216:219],38
s_barrier
buffer_store_dword v4,v3,s[36:39],s69 offen offset:0 sc0 sc1
M 22,v[212:215],34
s_mov_b32 s69,s68
s_add_u32 s68,s68,s63
ds_read_b32 v7,v6 offset:1024
M 22,v[220:223],38
L 3
M 22,v[228:231],42
M 22,v[236:239],46
M 18,v[224:227],42
M 18,v[232:235],46
L 1
M 18,v[240:243],50
L 0
v_cmp_eq_u32 vcc,s53,v7
M 18,v[248:251],54
M 22,v[244:247],50
M 22,v[252:255],54
s_cmp_lt_u32 s54,251
s_cbranch_scc1 Lg4_xa1_%=
s_sub_u32 s75,s67,s85
v_lshlrev_b32 v62,16,v58
s_nop 1
v_add_f32 v18,v18,v62
v_and_b32 v63,0xffff0000,v58
v_add_f32 v19,v19,v63
v_lshlrev_b32 v62,16,v59
v_add_f32 v20,v20,v62
v_and_b32 v63,0xffff0000,v59
v_add_f32 v21,v21,v63
v_lshlrev_b32 v62,16,v60
v_add_f32 v22,v22,v62
)"
R"(v_and_b32 v63,0xffff0000,v60
v_add_f32 v23,v23,v63
v_lshlrev_b32 v62,16,v61
v_add_f32 v24,v24,v62
v_and_b32 v63,0xffff0000,v61
v_add_f32 v25,v25,v63
v_cvt_pk_bf16_f32 v18,v18,v19
v_cvt_pk_bf16_f32 v19,v20,v21
v_cvt_pk_bf16_f32 v20,v22,v23
v_cvt_pk_bf16_f32 v21,v24,v25
v_lshrrev_b32 v62,8,v0
v_lshl_add_u32 v62,v62,5,v0
v_add_u32 v62,s73,v62
ds_write_b128 v62,v[18:21] offset:0
ds_read2_b32 v[22:23],v5 offset0:0 offset1:16
ds_read2_b32 v[24:25],v5 offset0:32 offset1:48
s_mov_b64 s[40:41],s[86:87]
s_mov_b32 s42,4194304
s_mov_b32 s43,0x00020000
L 0
s_add_u32 s64,s75,0
buffer_atomic_pk_add_bf16 v22,v9,s[40:43],s64 offen sc1
s_add_u32 s64,s75,8192
buffer_atomic_pk_add_bf16 v23,v9,s[40:43],s64 offen sc1
s_add_u32 s64,s75,16384
buffer_atomic_pk_add_bf16 v24,v9,s[40:43],s64 offen sc1
s_add_u32 s64,s75,24576
buffer_atomic_pk_add_bf16 v25,v9,s[40:43],s64 offen sc1
s_mov_b32 s67,0x7fff0000
s_mov_b32 s69,0x7fff0000
Lg4_xa1_%=:
s_mov_b32 s80,0
s_cmp_eq_u32 vcc_lo,-1
s_cbranch_scc0 Lg4_xn1_%=
s_add_u32 m0,s57,0
s_add_u32 s56,s55,0
D 0,56,0
v_lshlrev_b32 v62,16,v58
D 0,56,1024
v_add_f32 v18,v18,v62
D 0,56,2048
v_and_b32 v63,0xffff0000,v58
D 0,56,3072
v_add_f32 v19,v19,v63
s_add_u32 m0,s57,4096
s_add_u32 s56,s56,4096
D 0,56,0
v_lshlrev_b32 v62,16,v59
D 0,56,1024
v_add_f32 v20,v20,v62
D 0,56,2048
v_and_b32 v63,0xffff0000,v59
s_add_u32 m0,s57,28672
s_add_u32 s56,s55,28672
D 0,56,0
v_add_f32 v21,v21,v63
D 0,56,1024
v_lshlrev_b32 v62,16,v60
D 0,56,2048
v_add_f32 v22,v22,v62
D 0,56,3072
v_and_b32 v63,0xffff0000,v60
s_add_u32 m0,s57,32768
s_add_u32 s56,s56,4096
D 0,56,0
v_add_f32 v23,v23,v63
D 0,56,1024
v_lshlrev_b32 v62,16,v61
D 0,56,2048
v_add_f32 v24,v24,v62
s_add_u32 s55,s55,s60
s_sub_u32 s57,s84,s57
s_add_u32 s54,s54,s46
v_and_b32 v63,0xffff0000,v61
v_add_f32 v25,v25,v63
v_cvt_pk_bf16_f32 v18,v18,v19
v_cvt_pk_bf16_f32 v19,v20,v21
v_cvt_pk_bf16_f32 v20,v22,v23
v_cvt_pk_bf16_f32 v21,v24,v25
buffer_store_dwordx4 v[18:21],v0,s[36:39],s67 offen offset:0 sc0 sc1
s_mov_b32 s72,s69
s_mov_b32 s69,0x7fff0000
s_mov_b32 s67,0x7fff0000
s_mov_b32 s77,0x7fff0000
s_branch Lg4_gox_%=
Lg4_xn1_%=:
s_cmp_ge_u32 s54,256
s_cbranch_scc1 Lg4_fin1_%=
s_add_u32 m0,s59,4096
s_nop 0
buffer_load_dword v8,s[36:39],s58 offen offset:0 sc1 lds
buffer_store_dword v4,v3,s[36:39],s71 offen offset:0 sc0 sc1
s_add_u32 m0,s59,5120
s_nop 0
buffer_load_dword v8,s[36:39],s58 offen offset:0 sc1 lds
buffer_store_dword v4,v3,s[36:39],s71 offen offset:0 sc0 sc1
s_mov_b32 s83,0
v_lshlrev_b32 v62,16,v58
v_add_f32 v18,v18,v62
v_and_b32 v63,0xffff0000,v58
v_add_f32 v19,v19,v63
v_lshlrev_b32 v62,16,v59
v_add_f32 v20,v20,v62
v_and_b32 v63,0xffff0000,v59
v_add_f32 v21,v21,v63
v_lshlrev_b32 v62,16,v60
v_add_f32 v22,v22,v62
v_and_b32 v63,0xffff0000,v60
v_add_f32 v23,v23,v63
v_lshlrev_b32 v62,16,v61
v_add_f32 v24,v24,v62
v_and_b32 v63,0xffff0000,v61
v_add_f32 v25,v25,v63
v_cvt_pk_bf16_f32 v18,v18,v19
v_cvt_pk_bf16_f32 v19,v20,v21
v_cvt_pk_bf16_f32 v20,v22,v23
v_cvt_pk_bf16_f32 v21,v24,v25
buffer_store_dwordx4 v[18:21],v0,s[36:39],s67 offen offset:0 sc0 sc1
s_mov_b32 s72,s69
s_mov_b32 s69,0x7fff0000
s_mov_b32 s67,0x7fff0000
s_mov_b32 s77,0x7fff0000
s_branch Lg4_poll_%=
Lg4_fin1_%=:
v_lshlrev_b32 v62,16,v58
v_add_f32 v18,v18,v62
v_and_b32 v63,0xffff0000,v58
v_add_f32 v19,v19,v63
v_lshlrev_b32 v62,16,v59
v_add_f32 v20,v20,v62
v_and_b32 v63,0xffff0000,v59
v_add_f32 v21,v21,v63
v_lshlrev_b32 v62,16,v60
v_add_f32 v22,v22,v62
v_and_b32 v63,0xffff0000,v60
v_add_f32 v23,v23,v63
v_lshlrev_b32 v62,16,v61
v_add_f32 v24,v24,v62
v_and_b32 v63,0xffff0000,v61
v_add_f32 v25,v25,v63
v_cvt_pk_bf16_f32 v18,v18,v19
v_cvt_pk_bf16_f32 v19,v20,v21
v_cvt_pk_bf16_f32 v20,v22,v23
v_cvt_pk_bf16_f32 v21,v24,v25
buffer_store_dwordx4 v[18:21],v0,s[36:39],s67 offen offset:0 sc0 sc1
s_mov_b32 s72,s69
s_mov_b32 s69,0x7fff0000
s_mov_b32 s67,0x7fff0000
s_mov_b32 s77,0x7fff0000
s_branch Lg4_done_%=
Lg4_Z0_%=:
v_mfma_f32_16x16x32_bf16 v[14:17],a[4:7],v[26:29],0
s_sub_u32 s75,s67,s85
R 46,1,5120
v_lshlrev_b32 v62,16,v58
M 14,a[12:15],30
s_mov_b64 s[40:41],s[86:87]
v_add_f32 v18,v18,v62
v_and_b32 v63,0xffff0000,v58
v_mfma_f32_16x16x32_bf16 v[10:13],a[0:3],v[26:29],0
s_mov_b32 s42,4194304
R 50,1,6144
v_add_f32 v19,v19,v63
M 10,a[8:11],30
L 3
v_lshlrev_b32 v62,16,v59
v_add_f32 v20,v20,v62
M 10,a[16:19],34
s_mov_b32 s43,0x00020000
R 54,1,7168
v_and_b32 v63,0xffff0000,v59
M 10,a[24:27],38
s_add_u32 s64,s75,0
v_add_f32 v21,v21,v63
v_lshlrev_b32 v62,16,v60
M 14,a[20:23],34
s_mov_b32 s66,s65
R 26,1,8192
v_add_f32 v22,v22,v62
M 14,a[28:31],38
L 3
v_and_b32 v63,0xffff0000,v60
v_add_f32 v23,v23,v63
M 14,a[36:39],42
R 30,1,9216
v_lshlrev_b32 v62,16,v61
M 14,a[44:47],46
v_add_f32 v24,v24,v62
v_and_b32 v63,0xffff0000,v61
M 10,a[32:35],42
R 34,1,10240
v_add_f32 v25,v25,v63
M 10,a[40:43],46
L 3
buffer_load_dwordx4 v[58:61],v0,s[36:39],s74 offen offset:0 sc1
M 10,a[48:51],50
s_add_u32 s74,s74,s62
R 38,1,11264
v_cvt_pk_bf16_f32 v18,v18,v19
M 10,a[56:59],54
v_cvt_pk_bf16_f32 v19,v20,v21
v_cvt_pk_bf16_f32 v20,v22,v23
M 14,a[52:55],50
R 42,1,12288
v_cvt_pk_bf16_f32 v21,v24,v25
M 14,a[60:63],54
L 3
v_lshrrev_b32 v62,8,v0
v_lshl_add_u32 v62,v62,5,v0
M 14,a[68:71],26
R 46,1,13312
v_add_u32 v62,s73,v62
M 14,a[76:79],30
ds_write_b128 v62,v[18:21] offset:0
M 10,a[64:67],26
R 50,1,14336
ds_read2_b32 v[22:23],v5 offset0:0 offset1:16
M 10,a[72:75],30
L 5
ds_read2_b32 v[24:25],v5 offset0:32 offset1:48
M 10,a[80:83],34
R 54,1,15360
M 10,a[88:91],38
M 14,a[84:87],34
R 26,1,16384
M 14,a[92:95],38
L 6
M 14,a[100:103],42
R 30,1,17408
M 14,a[108:111],46
L 3
buffer_atomic_pk_add_bf16 v22,v9,s[40:43],s64 offen sc1
M 10,a[96:99],42
s_add_u32 s64,s75,8192
R 34,1,18432
M 10,a[104:107],46
L 3
buffer_atomic_pk_add_bf16 v23,v9,s[40:43],s64 offen sc1
M 10,a[112:115],50
s_add_u32 s64,s75,16384
R 38,1,19456
M 10,a[120:123],54
buffer_atomic_pk_add_bf16 v24,v9,s[40:43],s64 offen sc1
M 14,a[116:119],50
s_add_u32 s64,s75,24576
R 42,1,20480
M 14,a[124:127],54
L 3
buffer_atomic_pk_add_bf16 v25,v9,s[40:43],s64 offen sc1
M 14,a[132:135],26
R 46,1,21504
M 14,a[140:143],30
M 10,a[128:131],26
R 50,1,22528
M 10,a[136:139],30
L 3
M 10,a[144:147],34
R 54,1,23552
M 10,a[152:155],38
M 14,a[148:151],34
R 26,1,24576
M 14,a[156:159],38
L 3
M 14,a[164:167],42
R 30,1,25600
M 14,a[172:175],46
M 10,a[160:163],42
R 34,1,26624
M 10,a[168:171],46
L 3
M 10,a[176:179],50
s_waitcnt vmcnt(5)
R 38,1,27648
M 10,a[184:187],54
s_barrier
M 14,a[180:183],50
R 42,1,28672
M 14,a[188:191],54
L 3
M 14,a[196:199],26
R 46,1,29696
M 14,a[204:207],30
M 10,a[192:195],26
R 50,1,30720
M 10,a[200:203],30
L 3
M 10,a[208:211],34
R 54,1,31744
M 10,a[216:219],38
M 14,a[212:215],34
R 26,1,32768
M 14,a[220:223],38
L 3
M 14,a[228:231],42
R 30,1,33792
M 14,a[236:239],46
M 10,a[224:227],42
R 34,1,34816
M 10,a[232:235],46
L 3
M 10,a[240:243],50
R 38,1,35840
M 10,a[248:251],54
M 14,a[244:247],50
R 42,1,36864
M 14,a[252:255],54
L 3
M 14,v[68:71],26
R 46,1,37888
M 14,v[76:79],30
M 10,v[64:67],26
R 50,1,38912
M 10,v[72:75],30
L 3
M 10,v[80:83],34
R 54,1,39936
M 10,v[88:91],38
M 14,v[84:87],34
R 26,1,40960
M 14,v[92:95],38
L 3
M 14,v[100:103],42
R 30,1,41984
M 14,v[108:111],46
M 10,v[96:99],42
R 34,1,43008
M 10,v[104:107],46
L 3
M 10,v[112:115],50
R 38,1,44032
M 10,v[120:123],54
M 14,v[116:119],50
R 42,1,45056
M 14,v[124:127],54
L 3
M 14,v[132:135],26
R 46,1,46080
M 14,v[140:143],30
M 10,v[128:131],26
R 50,1,47104
M 10,v[136:139],30
L 3
M 10,v[144:147],34
R 54,1,48128
M 10,v[152:155],38
M 14,v[148:151],34
R 26,1,49152
M 14,v[156:159],38
L 3
M 14,v[164:167],42
R 30,1,50176
M 14,v[172:175],46
M 10,v[160:163],42
R 34,1,51200
M 10,v[168:171],46
L 3
M 10,v[176:179],50
R 38,1,52224
M 10,v[184:187],54
M 14,v[180:183],50
R 42,1,53248
M 14,v[188:191],54
L 3
M 14,v[196:199],26
R 46,1,54272
M 14,v[204:207],30
M 10,v[192:195],26
R 50,1,55296
M 10,v[200:203],30
L 3
M 10,v[208:211],34
R 54,1,56320
M 10,v[216:219],38
M 14,v[212:215],34
M 14,v[220:223],38
L 2
M 14,v[228:231],42
M 14,v[236:239],46
M 10,v[224:227],42
M 10,v[232:235],46
L 0
M 10,v[240:243],50
M 10,v[248:251],54
M 14,v[244:247],50
M 14,v[252:255],54
s_waitcnt vmcnt(4)
s_sub_u32 s75,s66,s85
v_lshlrev_b32 v62,16,v58
s_nop 2
v_add_f32 v10,v10,v62
v_and_b32 v63,0xffff0000,v58
v_add_f32 v11,v11,v63
v_lshlrev_b32 v62,16,v59
v_add_f32 v12,v12,v62
v_and_b32 v63,0xffff0000,v59
v_add_f32 v13,v13,v63
v_lshlrev_b32 v62,16,v60
v_add_f32 v14,v14,v62
v_and_b32 v63,0xffff0000,v60
v_add_f32 v15,v15,v63
v_lshlrev_b32 v62,16,v61
v_add_f32 v16,v16,v62
v_and_b32 v63,0xffff0000,v61
v_add_f32 v17,v17,v63
v_cvt_pk_bf16_f32 v10,v10,v11
v_cvt_pk_bf16_f32 v11,v12,v13
v_cvt_pk_bf16_f32 v12,v14,v15
v_cvt_pk_bf16_f32 v13,v16,v17
v_lshrrev_b32 v62,8,v0
v_lshl_add_u32 v62,v62,5,v0
v_add_u32 v62,s73,v62
ds_write_b128 v62,v[10:13] offset:0
ds_read2_b32 v[14:15],v5 offset0:0 offset1:16
ds_read2_b32 v[16:17],v5 offset0:32 offset1:48
s_mov_b64 s[40:41],s[86:87]
s_mov_b32 s42,4194304
s_mov_b32 s43,0x00020000
L 0
s_add_u32 s64,s75,0
buffer_atomic_pk_add_bf16 v14,v9,s[40:43],s64 offen sc1
s_add_u32 s64,s75,8192
buffer_atomic_pk_add_bf16 v15,v9,s[40:43],s64 offen sc1
s_add_u32 s64,s75,16384
buffer_atomic_pk_add_bf16 v16,v9,s[40:43],s64 offen sc1
s_add_u32 s64,s75,24576
buffer_atomic_pk_add_bf16 v17,v9,s[40:43],s64 offen sc1
s_branch Lg4_done_%=
Lg4_Z1_%=:
v_mfma_f32_16x16x32_bf16 v[22:25],a[4:7],v[26:29],0
s_sub_u32 s75,s66,s85
R 46,2,5120
v_lshlrev_b32 v62,16,v58
M 22,a[12:15],30
s_mov_b64 s[40:41],s[86:87]
v_add_f32 v10,v10,v62
v_and_b32 v63,0xffff0000,v58
v_mfma_f32_16x16x32_bf16 v[18:21],a[0:3],v[26:29],0
s_mov_b32 s42,4194304
R 50,2,6144
v_add_f32 v11,v11,v63
M 18,a[8:11],30
L 3
v_lshlrev_b32 v62,16,v59
v_add_f32 v12,v12,v62
M 18,a[16:19],34
s_mov_b32 s43,0x00020000
R 54,2,7168
v_and_b32 v63,0xffff0000,v59
M 18,a[24:27],38
s_add_u32 s64,s75,0
v_add_f32 v13,v13,v63
v_lshlrev_b32 v62,16,v60
M 22,a[20:23],34
s_mov_b32 s67,s65
R 26,2,8192
v_add_f32 v14,v14,v62
M 22,a[28:31],38
L 3
v_and_b32 v63,0xffff0000,v60
v_add_f32 v15,v15,v63
M 22,a[36:39],42
R 30,2,9216
v_lshlrev_b32 v62,16,v61
M 22,a[44:47],46
v_add_f32 v16,v16,v62
v_and_b32 v63,0xffff0000,v61
M 18,a[32:35],42
R 34,2,10240
v_add_f32 v17,v17,v63
M 18,a[40:43],46
L 3
buffer_load_dwordx4 v[58:61],v0,s[36:39],s74 offen offset:0 sc1
M 18,a[48:51],50
s_add_u32 s74,s74,s62
R 38,2,11264
v_cvt_pk_bf16_f32 v10,v10,v11
M 18,a[56:59],54
v_cvt_pk_bf16_f32 v11,v12,v13
v_cvt_pk_bf16_f32 v12,v14,v15
M 22,a[52:55],50
R 42,2,12288
v_cvt_pk_bf16_f32 v13,v16,v17
M 22,a[60:63],54
L 3
v_lshrrev_b32 v62,8,v0
v_lshl_add_u32 v62,v62,5,v0
M 22,a[68:71],26
R 46,2,13312
v_add_u32 v62,s73,v62
M 22,a[76:79],30
ds_write_b128 v62,v[10:13] offset:0
M 18,a[64:67],26
R 50,2,14336
ds_read2_b32 v[14:15],v5 offset0:0 offset1:16
M 18,a[72:75],30
L 5
ds_read2_b32 v[16:17],v5 offset0:32 offset1:48
M 18,a[80:83],34
R 54,2,15360
M 18,a[88:91],38
)"
R"(M 22,a[84:87],34
R 26,2,16384
M 22,a[92:95],38
L 6
M 22,a[100:103],42
R 30,2,17408
M 22,a[108:111],46
L 3
buffer_atomic_pk_add_bf16 v14,v9,s[40:43],s64 offen sc1
M 18,a[96:99],42
s_add_u32 s64,s75,8192
R 34,2,18432
M 18,a[104:107],46
L 3
buffer_atomic_pk_add_bf16 v15,v9,s[40:43],s64 offen sc1
M 18,a[112:115],50
s_add_u32 s64,s75,16384
R 38,2,19456
M 18,a[120:123],54
buffer_atomic_pk_add_bf16 v16,v9,s[40:43],s64 offen sc1
M 22,a[116:119],50
s_add_u32 s64,s75,24576
R 42,2,20480
M 22,a[124:127],54
L 3
buffer_atomic_pk_add_bf16 v17,v9,s[40:43],s64 offen sc1
M 22,a[132:135],26
R 46,2,21504
M 22,a[140:143],30
M 18,a[128:131],26
R 50,2,22528
M 18,a[136:139],30
L 3
M 18,a[144:147],34
R 54,2,23552
M 18,a[152:155],38
M 22,a[148:151],34
R 26,2,24576
M 22,a[156:159],38
L 3
M 22,a[164:167],42
R 30,2,25600
M 22,a[172:175],46
M 18,a[160:163],42
R 34,2,26624
M 18,a[168:171],46
L 3
M 18,a[176:179],50
s_waitcnt vmcnt(5)
R 38,2,27648
M 18,a[184:187],54
s_barrier
M 22,a[180:183],50
R 42,2,28672
M 22,a[188:191],54
L 3
M 22,a[196:199],26
R 46,2,29696
M 22,a[204:207],30
M 18,a[192:195],26
R 50,2,30720
M 18,a[200:203],30
L 3
M 18,a[208:211],34
R 54,2,31744
M 18,a[216:219],38
M 22,a[212:215],34
R 26,2,32768
M 22,a[220:223],38
L 3
M 22,a[228:231],42
R 30,2,33792
M 22,a[236:239],46
M 18,a[224:227],42
R 34,2,34816
M 18,a[232:235],46
L 3
M 18,a[240:243],50
R 38,2,35840
M 18,a[248:251],54
M 22,a[244:247],50
R 42,2,36864
M 22,a[252:255],54
L 3
M 22,v[68:71],26
R 46,2,37888
M 22,v[76:79],30
M 18,v[64:67],26
R 50,2,38912
M 18,v[72:75],30
L 3
M 18,v[80:83],34
R 54,2,39936
M 18,v[88:91],38
M 22,v[84:87],34
R 26,2,40960
M 22,v[92:95],38
L 3
M 22,v[100:103],42
R 30,2,41984
M 22,v[108:111],46
M 18,v[96:99],42
R 34,2,43008
M 18,v[104:107],46
L 3
M 18,v[112:115],50
R 38,2,44032
M 18,v[120:123],54
M 22,v[116:119],50
R 42,2,45056
M 22,v[124:127],54
L 3
M 22,v[132:135],26
R 46,2,46080
M 22,v[140:143],30
M 18,v[128:131],26
R 50,2,47104
M 18,v[136:139],30
L 3
M 18,v[144:147],34
R 54,2,48128
M 18,v[152:155],38
M 22,v[148:151],34
R 26,2,49152
M 22,v[156:159],38
L 3
M 22,v[164:167],42
R 30,2,50176
M 22,v[172:175],46
M 18,v[160:163],42
R 34,2,51200
M 18,v[168:171],46
L 3
M 18,v[176:179],50
R 38,2,52224
M 18,v[184:187],54
M 22,v[180:183],50
R 42,2,53248
M 22,v[188:191],54
L 3
M 22,v[196:199],26
R 46,2,54272
M 22,v[204:207],30
M 18,v[192:195],26
R 50,2,55296
M 18,v[200:203],30
L 3
M 18,v[208:211],34
R 54,2,56320
M 18,v[216:219],38
M 22,v[212:215],34
M 22,v[220:223],38
L 2
M 22,v[228:231],42
M 22,v[236:239],46
M 18,v[224:227],42
M 18,v[232:235],46
L 0
M 18,v[240:243],50
M 18,v[248:251],54
M 22,v[244:247],50
M 22,v[252:255],54
s_waitcnt vmcnt(4)
s_sub_u32 s75,s67,s85
v_lshlrev_b32 v62,16,v58
s_nop 2
v_add_f32 v18,v18,v62
v_and_b32 v63,0xffff0000,v58
v_add_f32 v19,v19,v63
v_lshlrev_b32 v62,16,v59
v_add_f32 v20,v20,v62
v_and_b32 v63,0xffff0000,v59
v_add_f32 v21,v21,v63
v_lshlrev_b32 v62,16,v60
v_add_f32 v22,v22,v62
v_and_b32 v63,0xffff0000,v60
v_add_f32 v23,v23,v63
v_lshlrev_b32 v62,16,v61
v_add_f32 v24,v24,v62
v_and_b32 v63,0xffff0000,v61
v_add_f32 v25,v25,v63
v_cvt_pk_bf16_f32 v18,v18,v19
v_cvt_pk_bf16_f32 v19,v20,v21
v_cvt_pk_bf16_f32 v20,v22,v23
v_cvt_pk_bf16_f32 v21,v24,v25
v_lshrrev_b32 v62,8,v0
v_lshl_add_u32 v62,v62,5,v0
v_add_u32 v62,s73,v62
ds_write_b128 v62,v[18:21] offset:0
ds_read2_b32 v[22:23],v5 offset0:0 offset1:16
ds_read2_b32 v[24:25],v5 offset0:32 offset1:48
s_mov_b64 s[40:41],s[86:87]
s_mov_b32 s42,4194304
s_mov_b32 s43,0x00020000
L 0
s_add_u32 s64,s75,0
buffer_atomic_pk_add_bf16 v22,v9,s[40:43],s64 offen sc1
s_add_u32 s64,s75,8192
buffer_atomic_pk_add_bf16 v23,v9,s[40:43],s64 offen sc1
s_add_u32 s64,s75,16384
buffer_atomic_pk_add_bf16 v24,v9,s[40:43],s64 offen sc1
s_add_u32 s64,s75,24576
buffer_atomic_pk_add_bf16 v25,v9,s[40:43],s64 offen sc1
s_branch Lg4_done_%=
Lg4_done_%=:
s_nop 0
s_barrier
buffer_store_dword v4,v3,s[36:39],s70 offen offset:0 sc0 sc1
s_waitcnt vmcnt(1)
s_barrier
buffer_store_dword v4,v3,s[36:39],s72 offen offset:0 sc0 sc1
s_waitcnt vmcnt(0) lgkmcnt(0)
s_barrier
s_endpgm
Lcode_g4_e:
.purgem M
.purgem R
.purgem T
.purgem D
.purgem L
)"
  :: "s"(ws), "s"(sWave), "s"(sG0), "s"(sStep), "s"(sOutOff), "s"(sFoutOff), "s"(sAuxOff), "s"(sWw), "s"(sBuf), "s"(sRk), "v"(vL16), "v"(vFlag), "v"(vSnapOff), "v"(vSnapRd), "v"(vWd0), "v"(vWd1), "v"(vWr0), "v"(vWr1), "v"(vSp), "v"(vSn), "s"(w4), "s"(out) : "v0", "v1", "v2", "v3", "v4", "v5", "v6", "v7", "v8", "v9", "v10", "v11", "v12", "v13", "v14", "v15", "v16", "v17", "v18", "v19", "v20", "v21", "v22", "v23", "v24", "v25", "v26", "v27", "v28", "v29", "v30", "v31", "v32", "v33", "v34", "v35", "v36", "v37", "v38", "v39", "v40", "v41", "v42", "v43", "v44", "v45", "v46", "v47", "v48", "v49", "v50", "v51", "v52", "v53", "v54", "v55", "v56", "v57", "v58", "v59", "v60", "v61", "v62", "v63", "v64", "v65", "v66", "v67", "v68", "v69", "v70", "v71", "v72", "v73", "v74", "v75", "v76", "v77", "v78", "v79", "v80", "v81", "v82", "v83", "v84", "v85", "v86", "v87", "v88", "v89", "v90", "v91", "v92", "v93", "v94", "v95", "v96", "v97", "v98", "v99", "v100", "v101", "v102", "v103", "v104", "v105", "v106", "v107", "v108", "v109", "v110", "v111", "v112", "v113", "v114", "v115", "v116", "v117", "v118", "v119", "v120", "v121", "v122", "v123", "v124", "v125", "v126", "v127", "v128", "v129", "v130", "v131", "v132", "v133", "v134", "v135", "v136", "v137", "v138", "v139", "v140", "v141", "v142", "v143", "v144", "v145", "v146", "v147", "v148", "v149", "v150", "v151", "v152", "v153", "v154", "v155", "v156", "v157", "v158", "v159", "v160", "v161", "v162", "v163", "v164", "v165", "v166", "v167", "v168", "v169", "v170", "v171", "v172", "v173", "v174", "v175", "v176", "v177", "v178", "v179", "v180", "v181", "v182", "v183", "v184", "v185", "v186", "v187", "v188", "v189", "v190", "v191", "v192", "v193", "v194", "v195", "v196", "v197", "v198", "v199", "v200", "v201", "v202", "v203", "v204", "v205", "v206", "v207", "v208", "v209", "v210", "v211", "v212", "v213", "v214", "v215", "v216", "v217", "v218", "v219", "v220", "v221", "v222", "v223", "v224", "v225", "v226", "v227", "v228", "v229", "v230", "v231", "v232", "v233", "v234", "v235", "v236", "v237", "v238", "v239", "a0", "a1", "a2", "a3", "a4", "a5", "a6", "a7", "a8", "a9", "a10", "a11", "a12", "a13", "a14", "a15", "a16", "a17", "a18", "a19", "a20", "a21", "a22", "a23", "a24", "a25", "a26", "a27", "a28", "a29", "a30", "a31", "a32", "a33", "a34", "a35", "a36", "a37", "a38", "a39", "a40", "a41", "a42", "a43", "a44", "a45", "a46", "a47", "a48", "a49", "a50", "a51", "a52", "a53", "a54", "a55", "a56", "a57", "a58", "a59", "a60", "a61", "a62", "a63", "a64", "a65", "a66", "a67", "a68", "a69", "a70", "a71", "a72", "a73", "a74", "a75", "a76", "a77", "a78", "a79", "a80", "a81", "a82", "a83", "a84", "a85", "a86", "a87", "a88", "a89", "a90", "a91", "a92", "a93", "a94", "a95", "a96", "a97", "a98", "a99", "a100", "a101", "a102", "a103", "a104", "a105", "a106", "a107", "a108", "a109", "a110", "a111", "a112", "a113", "a114", "a115", "a116", "a117", "a118", "a119", "a120", "a121", "a122", "a123", "a124", "a125", "a126", "a127", "a128", "a129", "a130", "a131", "a132", "a133", "a134", "a135", "a136", "a137", "a138", "a139", "a140", "a141", "a142", "a143", "a144", "a145", "a146", "a147", "a148", "a149", "a150", "a151", "a152", "a153", "a154", "a155", "a156", "a157", "a158", "a159", "a160", "a161", "a162", "a163", "a164", "a165", "a166", "a167", "a168", "a169", "a170", "a171", "a172", "a173", "a174", "a175", "a176", "a177", "a178", "a179", "a180", "a181", "a182", "a183", "a184", "a185", "a186", "a187", "a188", "a189", "a190", "a191", "a192", "a193", "a194", "a195", "a196", "a197", "a198", "a199", "a200", "a201", "a202", "a203", "a204", "a205", "a206", "a207", "a208", "a209", "a210", "a211", "a212", "a213", "a214", "a215", "a216", "a217", "a218", "a219", "a220", "a221", "a222", "a223", "a224", "a225", "a226", "a227", "a228", "a229", "a230", "a231", "a232", "a233", "a234", "a235", "a236", "a237", "a238", "a239", "a240", "a241", "a242", "a243", "a244", "a245", "a246", "a247", "a248", "a249", "a250", "a251", "a252", "a253", "a254", "a255", "s36", "s37", "s38", "s39", "s40", "s41", "s42", "s43", "s44", "s45", "s46", "s47", "s48", "s49", "s50", "s51", "s52", "s53", "s54", "s55", "s56", "s57", "s58", "s59", "s60", "s61", "s62", "s63", "s64", "s65", "s66", "s67", "s68", "s69", "s70", "s71", "s72", "s73", "s74", "s75", "s76", "s77", "s78", "s79", "s80", "s81", "s82", "s83", "s84", "s85", "s86", "s87", "s88", "s89", "s90", "s91", "s92", "s93", "s94", "s95", "s96", "s97", "vcc", "scc", "memory");
    __builtin_unreachable();
  } else {
    if (kind != 6) {
      const unsigned sStep = 4;
      COMMON(kind == 1 ? 8 : 16, kind == 1 ? fj * 128 : fj < 8 ? 1024 + fj * 128 : (fj & 2) * 1024 + (fj - 8) * 128, 1024, 131072, 2048, 256, kind == 1 ? 0 : 32768);
      const int sr = l15 - ((wave & 2) ? -1 : 1), sr2 = l15 - ((wave & 2) ? -2 : 2), nx = (slice >> 1) & 1;
      const unsigned vOutS = (sr2 & 16) ? 1024 + lg * 64 + (sr2 & 3) * 16 : (lg * 16 + sr2) * 16, vSp = (sr2 & 16) ? OOB : vOutS;
      (void)vWr2;
      const unsigned vAn = kind == 1 ? HU + vL16 : (sr & 16) ? 1024 + lg * 64 + (sr & 3) * 16 : (lg * 16 + sr) * 16;
      const unsigned vAe = kind == 1 ? ((l15 >> 2) == (nx ? 0 : 3) ? (nx ? 0 : 2 * HU) + 1024 + lg * 64 + (l15 & 3) * 16 : OOB) : (sr & 16) ? OOB : vAn;
      const unsigned vAux0 = l15 * 16, vAux1 = 139264 + (l15 * 2 + (lg & 1)) * 16 + wave * 4 + (lg >> 1) * 512, vAux2 = 139264 + (l15 * 2 + (lg & 1)) * 16;
      const unsigned vAux3 = (wave == 0 && lg < 2) ? slice * 128 + l15 * 8 + lg * 4 : OOB;
      const unsigned sOutOff = kind == 1 ? OFF_HS - HU + (slice * 4 + wave) * 1280 : OFF_X1 + (slice * 4 + wave) * 1024, sFoutOff = (kind == 1 ? OFF_FH : OFF_FX1) + slice * 128;
      const unsigned sAuxOff = OFF_XP + (slice * 4 + wave) * 1024, sBuf = kind, sWw = 16 * wave * 2048 + slice * 256;
      const u16* w12 = kind == 1 ? w1 : w2;
      unsigned epOut;
      asm volatile(
R"(.macro M d,w,x
v_mfma_f32_16x16x32_bf16 v[\d:\d+3], \w, v[\x:\x+3], v[\d:\d+3]
.endm
.macro R d,a,o
ds_read_b128 v[\d:\d+3], v\a offset:\o
.endm
.macro T d,a,o
ds_read_b64_tr_b16 \d, v\a offset:\o
.endm
.macro D a,s,o
buffer_load_dwordx4 v\a, s[36:39], s\s offen offset:\o sc1 lds
.endm
.macro L c
s_waitcnt lgkmcnt(\c)
.endm
Lcode_g12_b:
s_cmp_eq_u32 %9,2
s_cbranch_scc1 Lg2_begin_%=
s_mov_b64 s[36:37],%1
s_mov_b32 s38,111243392
s_mov_b32 s39,0x00020000
s_mov_b32 s44,%2
s_mov_b32 s45,%3
s_mov_b32 s46,%4
s_mov_b32 s47,%5
s_mov_b32 s48,%6
s_mov_b32 s49,%7
s_mov_b32 s50,%8
s_mov_b32 s51,%9
s_mov_b32 s93,%10
v_mov_b32 v0,%11
v_mov_b32 v3,%12
v_mov_b32 v5,%13
v_mov_b32 v6,%14
v_mov_b32 v18,%15
v_mov_b32 v19,%16
v_mov_b32 v20,%17
v_mov_b32 v21,%18
v_mov_b32 v9,%19
v_mov_b32 v10,%20
v_mov_b32 v11,%21
v_mov_b32 v12,%22
v_mov_b32 v13,%23
v_mov_b32 v14,%24
v_mov_b32 v15,%25
v_mov_b32 v16,%26
s_mov_b64 s[40:41],%27
s_mov_b32 s42,2097152
s_mov_b32 s43,0x00020000
s_lshl_b32 s57,s44,11
s_mov_b32 s79,111243264
buffer_load_dword v4,off,s[36:39],s79 sc1
s_add_u32 s55,s50,0
s_add_u32 m0,s57,0
s_nop 0
buffer_load_dwordx4 v18,s[40:43],s55 offen offset:0  lds
s_add_u32 m0,s57,1024
s_nop 0
buffer_load_dwordx4 v19,s[40:43],s55 offen offset:0  lds
s_add_u32 m0,s57,8064
s_nop 0
buffer_load_dwordx4 v18,s[40:43],s55 offen offset:128  lds
s_add_u32 m0,s57,9088
s_nop 0
buffer_load_dwordx4 v19,s[40:43],s55 offen offset:128  lds
s_add_u32 s55,s50,131072
s_add_u32 m0,s57,16384
s_nop 0
buffer_load_dwordx4 v18,s[40:43],s55 offen offset:0  lds
s_add_u32 m0,s57,17408
s_nop 0
buffer_load_dwordx4 v19,s[40:43],s55 offen offset:0  lds
s_add_u32 m0,s57,24448
s_nop 0
buffer_load_dwordx4 v18,s[40:43],s55 offen offset:128  lds
s_add_u32 m0,s57,25472
s_nop 0
buffer_load_dwordx4 v19,s[40:43],s55 offen offset:128  lds
s_add_u32 s55,s50,262144
s_add_u32 m0,s57,32768
s_nop 0
buffer_load_dwordx4 v18,s[40:43],s55 offen offset:0  lds
s_add_u32 m0,s57,33792
s_nop 0
buffer_load_dwordx4 v19,s[40:43],s55 offen offset:0  lds
s_add_u32 m0,s57,40832
s_nop 0
buffer_load_dwordx4 v18,s[40:43],s55 offen offset:128  lds
s_add_u32 m0,s57,41856
s_nop 0
buffer_load_dwordx4 v19,s[40:43],s55 offen offset:128  lds
s_add_u32 s55,s50,393216
s_add_u32 m0,s57,49152
s_nop 0
buffer_load_dwordx4 v18,s[40:43],s55 offen offset:0  lds
s_add_u32 m0,s57,50176
s_nop 0
buffer_load_dwordx4 v19,s[40:43],s55 offen offset:0  lds
s_add_u32 m0,s57,57216
s_nop 0
buffer_load_dwordx4 v18,s[40:43],s55 offen offset:128  lds
s_add_u32 m0,s57,58240
s_nop 0
buffer_load_dwordx4 v19,s[40:43],s55 offen offset:128  lds
s_add_u32 s55,s50,524288
s_add_u32 m0,s57,65536
s_nop 0
buffer_load_dwordx4 v18,s[40:43],s55 offen offset:0  lds
s_add_u32 m0,s57,66560
s_nop 0
buffer_load_dwordx4 v19,s[40:43],s55 offen offset:0  lds
s_add_u32 m0,s57,73600
s_nop 0
buffer_load_dwordx4 v18,s[40:43],s55 offen offset:128  lds
s_add_u32 m0,s57,74624
s_nop 0
buffer_load_dwordx4 v19,s[40:43],s55 offen offset:128  lds
s_add_u32 s55,s50,655360
s_add_u32 m0,s57,81920
s_nop 0
buffer_load_dwordx4 v18,s[40:43],s55 offen offset:0  lds
s_add_u32 m0,s57,82944
s_nop 0
buffer_load_dwordx4 v19,s[40:43],s55 offen offset:0  lds
s_add_u32 m0,s57,89984
s_nop 0
buffer_load_dwordx4 v18,s[40:43],s55 offen offset:128  lds
s_add_u32 m0,s57,91008
s_nop 0
buffer_load_dwordx4 v19,s[40:43],s55 offen offset:128  lds
s_waitcnt vmcnt(24)
s_barrier
v_readfirstlane_b32 s53,v4
s_nop 3
s_add_u32 s53,s53,1
s_cmp_eq_u32 s53,0
s_cselect_b32 s53,1,s53
v_mov_b32 v4,s53
s_waitcnt vmcnt(20) lgkmcnt(0)
s_barrier
s_add_u32 s55,s50,786432
s_add_u32 m0,s57,98304
s_nop 0
buffer_load_dwordx4 v18,s[40:43],s55 offen offset:0  lds
s_add_u32 m0,s57,99328
s_nop 0
buffer_load_dwordx4 v19,s[40:43],s55 offen offset:0  lds
s_add_u32 m0,s57,106368
s_nop 0
buffer_load_dwordx4 v18,s[40:43],s55 offen offset:128  lds
s_add_u32 m0,s57,107392
s_nop 0
buffer_load_dwordx4 v19,s[40:43],s55 offen offset:128  lds
v_add_u32 v26,0,v20
v_add_u32 v27,0,v21
T a[0:1],26,0
T a[2:3],26,512
T a[4:5],27,0
T a[6:7],27,512
T a[8:9],26,4096
T a[10:11],26,4608
T a[12:13],27,4096
T a[14:15],27,4608
s_waitcnt vmcnt(20) lgkmcnt(0)
s_barrier
s_add_u32 s55,s50,917504
s_add_u32 m0,s57,0
s_nop 0
buffer_load_dwordx4 v18,s[40:43],s55 offen offset:0  lds
s_add_u32 m0,s57,1024
s_nop 0
buffer_load_dwordx4 v19,s[40:43],s55 offen offset:0  lds
s_add_u32 m0,s57,8064
s_nop 0
buffer_load_dwordx4 v18,s[40:43],s55 offen offset:128  lds
s_add_u32 m0,s57,9088
s_nop 0
buffer_load_dwordx4 v19,s[40:43],s55 offen offset:128  lds
v_add_u32 v26,16384,v20
v_add_u32 v27,16384,v21
T a[16:17],26,0
T a[18:19],26,512
T a[20:21],27,0
T a[22:23],27,512
T a[24:25],26,4096
T a[26:27],26,4608
T a[28:29],27,4096
T a[30:31],27,4608
s_waitcnt vmcnt(20) lgkmcnt(0)
s_barrier
s_add_u32 s55,s50,1048576
s_add_u32 m0,s57,16384
s_nop 0
buffer_load_dwordx4 v18,s[40:43],s55 offen offset:0  lds
s_add_u32 m0,s57,17408
s_nop 0
buffer_load_dwordx4 v19,s[40:43],s55 offen offset:0  lds
s_add_u32 m0,s57,24448
s_nop 0
buffer_load_dwordx4 v18,s[40:43],s55 offen offset:128  lds
s_add_u32 m0,s57,25472
s_nop 0
buffer_load_dwordx4 v19,s[40:43],s55 offen offset:128  lds
v_add_u32 v26,32768,v20
v_add_u32 v27,32768,v21
T a[32:33],26,0
T a[34:35],26,512
T a[36:37],27,0
T a[38:39],27,512
T a[40:41],26,4096
T a[42:43],26,4608
T a[44:45],27,4096
T a[46:47],27,4608
s_waitcnt vmcnt(20) lgkmcnt(0)
s_barrier
s_add_u32 s55,s50,1179648
s_add_u32 m0,s57,32768
s_nop 0
buffer_load_dwordx4 v18,s[40:43],s55 offen offset:0  lds
s_add_u32 m0,s57,33792
s_nop 0
buffer_load_dwordx4 v19,s[40:43],s55 offen offset:0  lds
s_add_u32 m0,s57,40832
s_nop 0
buffer_load_dwordx4 v18,s[40:43],s55 offen offset:128  lds
s_add_u32 m0,s57,41856
s_nop 0
buffer_load_dwordx4 v19,s[40:43],s55 offen offset:128  lds
v_add_u32 v26,49152,v20
v_add_u32 v27,49152,v21
T a[48:49],26,0
T a[50:51],26,512
T a[52:53],27,0
T a[54:55],27,512
T a[56:57],26,4096
T a[58:59],26,4608
T a[60:61],27,4096
T a[62:63],27,4608
s_waitcnt vmcnt(20) lgkmcnt(0)
s_barrier
s_add_u32 s55,s50,1310720
s_add_u32 m0,s57,49152
s_nop 0
buffer_load_dwordx4 v18,s[40:43],s55 offen offset:0  lds
s_add_u32 m0,s57,50176
s_nop 0
buffer_load_dwordx4 v19,s[40:43],s55 offen offset:0  lds
s_add_u32 m0,s57,57216
s_nop 0
buffer_load_dwordx4 v18,s[40:43],s55 offen offset:128  lds
s_add_u32 m0,s57,58240
s_nop 0
buffer_load_dwordx4 v19,s[40:43],s55 offen offset:128  lds
v_add_u32 v26,65536,v20
v_add_u32 v27,65536,v21
T a[64:65],26,0
T a[66:67],26,512
T a[68:69],27,0
T a[70:71],27,512
T a[72:73],26,4096
T a[74:75],26,4608
T a[76:77],27,4096
T a[78:79],27,4608
s_waitcnt vmcnt(20) lgkmcnt(0)
s_barrier
s_add_u32 s55,s50,1441792
s_add_u32 m0,s57,65536
s_nop 0
buffer_load_dwordx4 v18,s[40:43],s55 offen offset:0  lds
s_add_u32 m0,s57,66560
s_nop 0
buffer_load_dwordx4 v19,s[40:43],s55 offen offset:0  lds
s_add_u32 m0,s57,73600
s_nop 0
buffer_load_dwordx4 v18,s[40:43],s55 offen offset:128  lds
s_add_u32 m0,s57,74624
s_nop 0
buffer_load_dwordx4 v19,s[40:43],s55 offen offset:128  lds
v_add_u32 v26,81920,v20
v_add_u32 v27,81920,v21
T a[80:81],26,0
T a[82:83],26,512
T a[84:85],27,0
T a[86:87],27,512
T a[88:89],26,4096
T a[90:91],26,4608
T a[92:93],27,4096
T a[94:95],27,4608
s_waitcnt vmcnt(20) lgkmcnt(0)
s_barrier
s_add_u32 s55,s50,1572864
s_add_u32 m0,s57,81920
s_nop 0
buffer_load_dwordx4 v18,s[40:43],s55 offen offset:0  lds
s_add_u32 m0,s57,82944
s_nop 0
buffer_load_dwordx4 v19,s[40:43],s55 offen offset:0  lds
s_add_u32 m0,s57,89984
s_nop 0
buffer_load_dwordx4 v18,s[40:43],s55 offen offset:128  lds
s_add_u32 m0,s57,91008
s_nop 0
buffer_load_dwordx4 v19,s[40:43],s55 offen offset:128  lds
v_add_u32 v26,98304,v20
v_add_u32 v27,98304,v21
T a[96:97],26,0
T a[98:99],26,512
T a[100:101],27,0
T a[102:103],27,512
T a[104:105],26,4096
T a[106:107],26,4608
T a[108:109],27,4096
T a[110:111],27,4608
s_waitcnt vmcnt(20) lgkmcnt(0)
s_barrier
s_add_u32 s55,s50,1703936
s_add_u32 m0,s57,98304
s_nop 0
buffer_load_dwordx4 v18,s[40:43],s55 offen offset:0  lds
s_add_u32 m0,s57,99328
s_nop 0
buffer_load_dwordx4 v19,s[40:43],s55 offen offset:0  lds
s_add_u32 m0,s57,106368
s_nop 0
buffer_load_dwordx4 v18,s[40:43],s55 offen offset:128  lds
s_add_u32 m0,s57,107392
s_nop 0
buffer_load_dwordx4 v19,s[40:43],s55 offen offset:128  lds
v_add_u32 v26,0,v20
v_add_u32 v27,0,v21
T a[112:113],26,0
T a[114:115],26,512
T a[116:117],27,0
T a[118:119],27,512
T a[120:121],26,4096
T a[122:123],26,4608
T a[124:125],27,4096
T a[126:127],27,4608
s_waitcnt vmcnt(20) lgkmcnt(0)
s_barrier
s_add_u32 s55,s50,1835008
s_add_u32 m0,s57,0
s_nop 0
buffer_load_dwordx4 v18,s[40:43],s55 offen offset:0  lds
s_add_u32 m0,s57,1024
s_nop 0
buffer_load_dwordx4 v19,s[40:43],s55 offen offset:0  lds
s_add_u32 m0,s57,8064
s_nop 0
buffer_load_dwordx4 v18,s[40:43],s55 offen offset:128  lds
s_add_u32 m0,s57,9088
s_nop 0
buffer_load_dwordx4 v19,s[40:43],s55 offen offset:128  lds
v_add_u32 v26,16384,v20
v_add_u32 v27,16384,v21
T a[128:129],26,0
T a[130:131],26,512
T a[132:133],27,0
T a[134:135],27,512
T a[136:137],26,4096
T a[138:139],26,4608
T a[140:141],27,4096
T a[142:143],27,4608
s_waitcnt vmcnt(20) lgkmcnt(0)
s_barrier
s_add_u32 s55,s50,1966080
s_add_u32 m0,s57,16384
s_nop 0
buffer_load_dwordx4 v18,s[40:43],s55 offen offset:0  lds
s_add_u32 m0,s57,17408
s_nop 0
buffer_load_dwordx4 v19,s[40:43],s55 offen offset:0  lds
s_add_u32 m0,s57,24448
s_nop 0
buffer_load_dwordx4 v18,s[40:43],s55 offen offset:128  lds
s_add_u32 m0,s57,25472
s_nop 0
buffer_load_dwordx4 v19,s[40:43],s55 offen offset:128  lds
v_add_u32 v26,32768,v20
v_add_u32 v27,32768,v21
T a[144:145],26,0
T a[146:147],26,512
T a[148:149],27,0
T a[150:151],27,512
T a[152:153],26,4096
T a[154:155],26,4608
T a[156:157],27,4096
T a[158:159],27,4608
s_waitcnt vmcnt(20) lgkmcnt(0)
s_barrier
v_add_u32 v26,49152,v20
v_add_u32 v27,49152,v21
T a[160:161],26,0
T a[162:163],26,512
T a[164:165],27,0
T a[166:167],27,512
T a[168:169],26,4096
T a[170:171],26,4608
T a[172:173],27,4096
T a[174:175],27,4608
s_waitcnt vmcnt(16) lgkmcnt(0)
s_barrier
v_add_u32 v26,65536,v20
v_add_u32 v27,65536,v21
T a[176:177],26,0
T a[178:179],26,512
T a[180:181],27,0
T a[182:183],27,512
T a[184:185],26,4096
T a[186:187],26,4608
T a[188:189],27,4096
T a[190:191],27,4608
s_waitcnt vmcnt(12) lgkmcnt(0)
s_barrier
v_add_u32 v26,81920,v20
v_add_u32 v27,81920,v21
T a[192:193],26,0
T a[194:195],26,512
T a[196:197],27,0
T a[198:199],27,512
T a[200:201],26,4096
T a[202:203],26,4608
T a[204:205],27,4096
T a[206:207],27,4608
s_waitcnt vmcnt(8) lgkmcnt(0)
s_barrier
v_add_u32 v26,98304,v20
v_add_u32 v27,98304,v21
T a[208:209],26,0
T a[210:211],26,512
T a[212:213],27,0
T a[214:215],27,512
T a[216:217],26,4096
T a[218:219],26,4608
T a[220:221],27,4096
T a[222:223],27,4608
s_waitcnt vmcnt(4) lgkmcnt(0)
s_barrier
v_add_u32 v26,0,v20
v_add_u32 v27,0,v21
T a[224:225],26,0
T a[226:227],26,512
T a[228:229],27,0
T a[230:231],27,512
T a[232:233],26,4096
T a[234:235],26,4608
T a[236:237],27,4096
T a[238:239],27,4608
s_waitcnt vmcnt(0) lgkmcnt(0)
s_barrier
v_add_u32 v26,16384,v20
v_add_u32 v27,16384,v21
T a[240:241],26,0
T a[242:243],26,512
T a[244:245],27,0
T a[246:247],27,512
T a[248:249],26,4096
T a[250:251],26,4608
T a[252:253],27,4096
T a[254:255],27,4608
L 0
s_barrier
v_mov_b32 v34,0x3f803f80
v_mov_b32 v35,0x3f803f80
v_mov_b32 v36,0x3f803f80
v_mov_b32 v37,0x3f803f80
s_nop 1
v_mfma_f32_16x16x32_bf16 v[106:109],a[0:3],v[34:37],0
v_mfma_f32_16x16x32_bf16 v[110:113],a[4:7],v[34:37],0
M 106,a[8:11],34
M 110,a[12:15],34
M 106,a[16:19],34
M 110,a[20:23],34
M 106,a[24:27],34
M 110,a[28:31],34
M 106,a[32:35],34
M 110,a[36:39],34
M 106,a[40:43],34
M 110,a[44:47],34
M 106,a[48:51],34
M 110,a[52:55],34
M 106,a[56:59],34
M 110,a[60:63],34
M 106,a[64:67],34
M 110,a[68:71],34
M 106,a[72:75],34
M 110,a[76:79],34
M 106,a[80:83],34
M 110,a[84:87],34
M 106,a[88:91],34
M 110,a[92:95],34
M 106,a[96:99],34
M 110,a[100:103],34
M 106,a[104:107],34
M 110,a[108:111],34
M 106,a[112:115],34
M 110,a[116:119],34
M 106,a[120:123],34
M 110,a[124:127],34
M 106,a[128:131],34
)"
R"(M 110,a[132:135],34
M 106,a[136:139],34
M 110,a[140:143],34
M 106,a[144:147],34
M 110,a[148:151],34
M 106,a[152:155],34
M 110,a[156:159],34
M 106,a[160:163],34
M 110,a[164:167],34
M 106,a[168:171],34
M 110,a[172:175],34
M 106,a[176:179],34
M 110,a[180:183],34
M 106,a[184:187],34
M 110,a[188:191],34
M 106,a[192:195],34
M 110,a[196:199],34
M 106,a[200:203],34
M 110,a[204:207],34
M 106,a[208:211],34
M 110,a[212:215],34
M 106,a[216:219],34
M 110,a[220:223],34
M 106,a[224:227],34
M 110,a[228:231],34
M 106,a[232:235],34
M 110,a[236:239],34
M 106,a[240:243],34
M 110,a[244:247],34
M 106,a[248:251],34
M 110,a[252:255],34
v_mov_b32 v4,s53
s_mov_b32 s82,0
s_mov_b32 s80,0
s_mov_b32 s54,s45
s_mul_i32 s57,s44,4096
s_mul_i32 s55,s45,32768
s_add_u32 s55,s55,s57
s_add_u32 s55,s55,8388608
s_mul_i32 s57,s44,4096
s_mul_i32 s60,s46,32768
s_mul_i32 s61,s46,1024
s_mul_i32 s62,s46,40960
s_mul_i32 s63,s46,1024
s_mul_i32 s64,s46,512
s_lshl_b32 s59,s44,8
s_add_u32 s59,s59,131072
s_mul_i32 s58,s45,1024
s_add_u32 s58,s58,109056000
s_mul_i32 s65,s45,40960
s_add_u32 s65,s65,s47
s_mul_i32 s68,s45,1024
s_add_u32 s68,s68,s48
s_mul_i32 s73,s45,512
s_add_u32 s73,s73,106954752
s_mul_i32 s74,s45,32768
s_add_u32 s74,s74,s49
s_mul_i32 s75,s45,1024
s_add_u32 s75,s75,107479040
s_mov_b32 s66,0x7fff0000
s_mov_b32 s67,0x7fff0000
s_mov_b32 s69,0x7fff0000
s_mov_b32 s76,0x7fff0000
s_mov_b32 s77,0x7fff0000
s_mov_b32 s70,0x7fff0000
s_mov_b32 s71,0x7fff0000
s_mov_b32 s72,0x7fff0000
v_mov_b32 v1,v0
v_add_u32 v2,16384,v0
s_mov_b32 s84,16384
s_mov_b32 s86,0x3a800000
s_mov_b32 s87,0x3727c5ac
s_mov_b32 s88,0xc0135761
s_mov_b32 s89,0x3d372713
s_lshr_b32 s85,s44,1
s_mul_i32 s85,s85,127
v_lshlrev_b32 v8,22,v0
v_and_b32 v8,0x80000000,v8
v_or_b32 v8,v8,v5
v_add_u32 v2,32768,v0
s_lshl_b32 s84,s57,1
s_add_u32 s84,s84,32768
s_getpc_b64 s[40:41]
Lg1_pc_%=:
s_lshl_b32 s78,s44,13
v_lshlrev_b32 v114,3,v0
v_add_u32 v114,s78,v114
v_min_u32 v114,Lcode_g2_b-Lg1_pc_%=-4,v114
s_mov_b32 m0,163584
s_nop 0
global_load_lds_dword v114,s[40:41]
s_add_u32 m0,s59,4096
s_nop 0
buffer_load_dword v8,s[36:39],s58 offen offset:0 sc1 lds
buffer_store_dword v4,v3,s[36:39],s71 offen offset:0 sc0 sc1
s_add_u32 m0,s59,5120
s_nop 0
buffer_load_dword v8,s[36:39],s58 offen offset:0 sc1 lds
buffer_store_dword v4,v3,s[36:39],s71 offen offset:0 sc0 sc1
s_mov_b32 s83,0
Lg1_poll_%=:
s_add_u32 s78,s83,2
s_and_b32 s78,s78,3
s_lshl_b32 s78,s78,10
s_add_u32 s78,s78,4096
s_add_u32 m0,s59,s78
s_nop 0
buffer_load_dword v8,s[36:39],s58 offen offset:0 sc1 lds
s_and_b32 s78,s83,3
s_lshl_b32 s78,s78,10
v_add_u32 v114,s78,v6
s_waitcnt vmcnt(4)
s_barrier
ds_read_b32 v7,v114 offset:4096
buffer_store_dword v4,v3,s[36:39],s70 offen offset:0 sc0 sc1
s_mov_b32 s70,s71
s_mov_b32 s71,s72
s_mov_b32 s72,0x7fff0000
s_add_u32 s83,s83,1
s_add_u32 s82,s82,1
L 0
v_cmp_eq_u32 vcc,s53,v7
s_nop 0
s_cmp_eq_u32 vcc_lo,-1
s_cbranch_scc1 Lg1_go_%=
s_cmp_lt_u32 s82,20000
s_cbranch_scc1 Lg1_poll_%=
Lg1_go_%=:
s_add_u32 m0,s57,0
s_add_u32 s56,s55,0
D 0,56,0
D 0,56,1024
D 0,56,2048
D 0,56,3072
s_add_u32 s58,s58,s61
s_add_u32 m0,s59,2048
s_nop 0
buffer_load_dword v8,s[36:39],s58 offen offset:0 sc1 lds
s_add_u32 m0,s57,16384
s_add_u32 s56,s55,16384
D 0,56,0
D 0,56,1024
D 0,56,2048
D 0,56,3072
s_add_u32 s55,s55,s60
s_sub_u32 s57,s84,s57
s_add_u32 s54,s54,s46
s_waitcnt vmcnt(4)
s_barrier
buffer_store_dword v4,v3,s[36:39],s70 offen offset:0 sc0 sc1
s_min_u32 s71,s71,s72
buffer_store_dword v4,v3,s[36:39],s71 offen offset:0 sc0 sc1
s_mov_b32 s70,0x7fff0000
s_mov_b32 s71,0x7fff0000
s_mov_b32 s72,0x7fff0000
s_cmp_eq_u32 s80,1
buffer_store_dword v4,v3,s[36:39],s71 offen offset:0 sc0 sc1
s_cbranch_scc1 Lg1_st1_%=
s_branch Lg1_st0_%=
Lg1_gox_%=:
s_add_u32 s58,s58,s61
s_add_u32 m0,s59,2048
s_nop 0
buffer_load_dword v8,s[36:39],s58 offen offset:0 sc1 lds
s_waitcnt vmcnt(0)
s_barrier
buffer_store_dword v4,v3,s[36:39],s70 offen offset:0 sc0 sc1
s_min_u32 s71,s71,s72
buffer_store_dword v4,v3,s[36:39],s71 offen offset:0 sc0 sc1
s_mov_b32 s70,0x7fff0000
s_mov_b32 s71,0x7fff0000
s_mov_b32 s72,0x7fff0000
s_cmp_eq_u32 s80,1
s_cbranch_scc1 Lg1_st1_%=
Lg1_st0_%=:
ds_read_b32 v7,v6 offset:2048
R 34,1,0
R 38,1,1024
R 42,1,2048
R 46,1,3072
R 50,1,4096
L 5
v_cmp_eq_u32 vcc,s53,v7
L 3
s_cmp_eq_u32 vcc_lo,-1
s_cbranch_scc1 Lg1_F0_%=
s_branch Lg1_L0_%=
Lg1_st1_%=:
ds_read_b32 v7,v6 offset:2048
R 34,2,0
R 38,2,1024
R 42,2,2048
R 46,2,3072
R 50,2,4096
L 5
v_cmp_eq_u32 vcc,s53,v7
L 3
s_cmp_eq_u32 vcc_lo,-1
s_cbranch_scc1 Lg1_F1_%=
s_branch Lg1_L1_%=
Lg1_F0_%=:
v_mfma_f32_16x16x32_bf16 v[22:25],a[4:7],v[34:37],0
s_add_u32 s58,s58,s61
s_add_u32 m0,s59,0
R 54,1,5120
M 22,a[12:15],38
s_add_u32 s56,s55,0
buffer_load_dword v8,s[36:39],s58 offen offset:0 sc1 lds
v_mfma_f32_16x16x32_bf16 v[18:21],a[0:3],v[34:37],0
s_add_u32 m0,s57,0
R 58,1,6144
v_fma_f32 v26,-v102,v106,v26
M 18,a[8:11],38
L 3
D 0,56,0
M 18,a[16:19],42
s_mov_b32 s66,s65
R 62,1,7168
v_fma_f32 v27,-v102,v107,v27
M 18,a[24:27],46
s_add_u32 s65,s65,s62
v_fma_f32 v28,-v102,v108,v28
v_fma_f32 v29,-v102,v109,v29
M 22,a[20:23],42
s_add_u32 s54,s54,s46
R 66,1,8192
v_mul_f32 v114,v26,v26
M 22,a[28:31],46
L 3
D 0,56,1024
M 22,a[36:39],50
R 70,1,9216
v_mul_f32 v115,v27,v27
M 22,a[44:47],54
v_mul_f32 v116,v28,v28
v_mul_f32 v117,v29,v29
M 18,a[32:35],50
R 74,1,10240
v_fma_f32 v114,v104,v114,v103
M 18,a[40:43],54
L 3
D 0,56,2048
M 18,a[48:51],58
R 78,1,11264
v_fma_f32 v115,v104,v115,v103
M 18,a[56:59],62
D 0,56,3072
M 22,a[52:55],58
s_add_u32 m0,s57,16384
R 82,1,12288
v_fma_f32 v116,v104,v116,v103
M 22,a[60:63],62
L 3
v_fma_f32 v117,v104,v117,v103
v_mul_f32 v114,v26,v114
M 22,a[68:71],66
s_add_u32 s56,s55,16384
R 86,1,13312
v_mul_f32 v115,v27,v115
M 22,a[76:79],70
s_add_u32 s55,s55,s60
buffer_load_dwordx4 v[98:101],v11,s[36:39],s73 offen offset:0 sc1
M 18,a[64:67],66
s_add_u32 s73,s73,s64
R 90,1,14336
v_mul_f32 v116,v28,v116
M 18,a[72:75],70
L 3
v_exp_f32 v114,v114
M 18,a[80:83],74
s_waitcnt vmcnt(9)
R 94,1,15360
v_mul_f32 v117,v29,v117
M 18,a[88:91],78
s_barrier
v_exp_f32 v115,v115
M 22,a[84:87],74
s_sub_u32 s57,s84,s57
R 34,1,16384
v_fma_f32 v114,v114,v105,v105
M 22,a[92:95],78
L 3
v_exp_f32 v116,v116
M 22,a[100:103],82
R 38,1,17408
v_fma_f32 v115,v115,v105,v105
M 22,a[108:111],86
D 0,56,0
M 18,a[96:99],82
R 42,1,18432
v_fma_f32 v116,v116,v105,v105
M 18,a[104:107],86
L 3
v_exp_f32 v117,v117
M 18,a[112:115],90
R 46,1,19456
v_fma_f32 v117,v117,v105,v105
M 18,a[120:123],94
v_rcp_f32 v114,v114
M 22,a[116:119],90
R 50,1,20480
v_mul_f32 v26,v26,v114
M 22,a[124:127],94
L 3
v_rcp_f32 v115,v115
M 22,a[132:135],34
R 54,1,21504
v_mul_f32 v27,v27,v115
M 22,a[140:143],38
D 0,56,1024
M 18,a[128:131],34
R 58,1,22528
v_cvt_pk_bf16_f32 v122,v26,v27
M 18,a[136:139],38
L 3
v_rcp_f32 v116,v116
M 18,a[144:147],42
R 62,1,23552
v_mul_f32 v28,v28,v116
M 18,a[152:155],46
v_rcp_f32 v117,v117
M 22,a[148:151],42
R 66,1,24576
v_mul_f32 v29,v29,v117
M 22,a[156:159],46
L 3
D 0,56,2048
M 22,a[164:167],50
R 70,1,25600
v_cvt_pk_bf16_f32 v123,v28,v29
M 22,a[172:175],54
v_fma_f32 v30,-v102,v110,v30
v_fma_f32 v31,-v102,v111,v31
M 18,a[160:163],50
R 74,1,26624
v_fma_f32 v32,-v102,v112,v32
M 18,a[168:171],54
L 3
v_fma_f32 v33,-v102,v113,v33
v_mul_f32 v118,v30,v30
M 18,a[176:179],58
R 78,1,27648
v_mul_f32 v119,v31,v31
M 18,a[184:187],62
v_mul_f32 v120,v32,v32
v_mul_f32 v121,v33,v33
M 22,a[180:183],58
R 82,1,28672
v_fma_f32 v118,v104,v118,v103
M 22,a[188:191],62
L 3
D 0,56,3072
M 22,a[196:199],66
R 86,1,29696
v_fma_f32 v119,v104,v119,v103
M 22,a[204:207],70
v_fma_f32 v120,v104,v120,v103
v_fma_f32 v121,v104,v121,v103
M 18,a[192:195],66
R 90,1,30720
v_mul_f32 v118,v30,v118
M 18,a[200:203],70
L 3
v_exp_f32 v118,v118
M 18,a[208:211],74
s_waitcnt vmcnt(5)
R 94,1,31744
v_mul_f32 v119,v31,v119
M 18,a[216:219],78
s_barrier
ds_read_b32 v7,v6 offset:0
v_mul_f32 v120,v32,v120
M 22,a[212:215],74
R 34,2,0
v_mul_f32 v121,v33,v121
M 22,a[220:223],78
L 4
buffer_store_dword v4,v3,s[36:39],s70 offen offset:0 sc0 sc1
M 22,a[228:231],82
s_mov_b32 s70,s69
R 38,2,1024
v_fma_f32 v118,v118,v105,v105
M 22,a[236:239],86
s_mov_b32 s69,s68
v_exp_f32 v119,v119
M 18,a[224:227],82
s_add_u32 s68,s68,s63
R 42,2,2048
v_fma_f32 v119,v119,v105,v105
M 18,a[232:235],86
L 4
v_exp_f32 v120,v120
v_exp_f32 v121,v121
M 18,a[240:243],90
L 3
R 46,2,3072
v_fma_f32 v120,v120,v105,v105
v_fma_f32 v121,v121,v105,v105
v_rcp_f32 v118,v118
v_rcp_f32 v119,v119
v_rcp_f32 v120,v120
v_rcp_f32 v121,v121
M 18,a[248:251],94
v_mul_f32 v30,v30,v118
v_mul_f32 v31,v31,v119
v_mul_f32 v32,v32,v120
v_mul_f32 v33,v33,v121
v_cvt_pk_bf16_f32 v124,v30,v31
v_cvt_pk_bf16_f32 v125,v32,v33
v_cmp_eq_u32 vcc,s53,v7
M 22,a[244:247],90
s_cmp_eq_u32 vcc_lo,-1
R 50,2,4096
buffer_store_dwordx4 v[122:125],v15,s[36:39],s67 offen offset:0 sc0 sc1
buffer_store_dwordx4 v[122:125],v16,s[36:39],s67 offen offset:0 sc0 sc1
M 22,a[252:255],94
s_waitcnt vmcnt(7) lgkmcnt(3)
s_cbranch_scc0 Lg1_L1_%=
Lg1_F1_%=:
v_mfma_f32_16x16x32_bf16 v[30:33],a[4:7],v[34:37],0
s_add_u32 s58,s58,s61
s_add_u32 m0,s59,1024
R 54,2,5120
M 30,a[12:15],38
s_add_u32 s56,s55,0
buffer_load_dword v8,s[36:39],s58 offen offset:0 sc1 lds
v_mfma_f32_16x16x32_bf16 v[26:29],a[0:3],v[34:37],0
s_add_u32 m0,s57,0
R 58,2,6144
v_fma_f32 v18,-v98,v106,v18
M 26,a[8:11],38
L 3
D 0,56,0
M 26,a[16:19],42
s_mov_b32 s67,s65
R 62,2,7168
v_fma_f32 v19,-v98,v107,v19
M 26,a[24:27],46
s_add_u32 s65,s65,s62
v_fma_f32 v20,-v98,v108,v20
v_fma_f32 v21,-v98,v109,v21
M 30,a[20:23],42
s_add_u32 s54,s54,s46
R 66,2,8192
v_mul_f32 v114,v18,v18
M 30,a[28:31],46
L 3
D 0,56,1024
M 30,a[36:39],50
R 70,2,9216
v_mul_f32 v115,v19,v19
M 30,a[44:47],54
v_mul_f32 v116,v20,v20
v_mul_f32 v117,v21,v21
M 26,a[32:35],50
R 74,2,10240
v_fma_f32 v114,v100,v114,v99
M 26,a[40:43],54
L 3
D 0,56,2048
M 26,a[48:51],58
R 78,2,11264
v_fma_f32 v115,v100,v115,v99
M 26,a[56:59],62
D 0,56,3072
M 30,a[52:55],58
s_add_u32 m0,s57,16384
R 82,2,12288
v_fma_f32 v116,v100,v116,v99
M 30,a[60:63],62
L 3
v_fma_f32 v117,v100,v117,v99
v_mul_f32 v114,v18,v114
M 30,a[68:71],66
s_add_u32 s56,s55,16384
R 86,2,13312
v_mul_f32 v115,v19,v115
M 30,a[76:79],70
s_add_u32 s55,s55,s60
buffer_load_dwordx4 v[102:105],v11,s[36:39],s73 offen offset:0 sc1
M 26,a[64:67],66
s_add_u32 s73,s73,s64
R 90,2,14336
v_mul_f32 v116,v20,v116
M 26,a[72:75],70
L 3
v_exp_f32 v114,v114
M 26,a[80:83],74
s_waitcnt vmcnt(9)
R 94,2,15360
v_mul_f32 v117,v21,v117
M 26,a[88:91],78
s_barrier
v_exp_f32 v115,v115
M 30,a[84:87],74
s_sub_u32 s57,s84,s57
R 34,2,16384
v_fma_f32 v114,v114,v101,v101
M 30,a[92:95],78
L 3
)"
R"(v_exp_f32 v116,v116
M 30,a[100:103],82
R 38,2,17408
v_fma_f32 v115,v115,v101,v101
M 30,a[108:111],86
D 0,56,0
M 26,a[96:99],82
R 42,2,18432
v_fma_f32 v116,v116,v101,v101
M 26,a[104:107],86
L 3
v_exp_f32 v117,v117
M 26,a[112:115],90
R 46,2,19456
v_fma_f32 v117,v117,v101,v101
M 26,a[120:123],94
v_rcp_f32 v114,v114
M 30,a[116:119],90
R 50,2,20480
v_mul_f32 v18,v18,v114
M 30,a[124:127],94
L 3
v_rcp_f32 v115,v115
M 30,a[132:135],34
R 54,2,21504
v_mul_f32 v19,v19,v115
M 30,a[140:143],38
D 0,56,1024
M 26,a[128:131],34
R 58,2,22528
v_cvt_pk_bf16_f32 v122,v18,v19
M 26,a[136:139],38
L 3
v_rcp_f32 v116,v116
M 26,a[144:147],42
R 62,2,23552
v_mul_f32 v20,v20,v116
M 26,a[152:155],46
v_rcp_f32 v117,v117
M 30,a[148:151],42
R 66,2,24576
v_mul_f32 v21,v21,v117
M 30,a[156:159],46
L 3
D 0,56,2048
M 30,a[164:167],50
R 70,2,25600
v_cvt_pk_bf16_f32 v123,v20,v21
M 30,a[172:175],54
v_fma_f32 v22,-v98,v110,v22
v_fma_f32 v23,-v98,v111,v23
M 26,a[160:163],50
R 74,2,26624
v_fma_f32 v24,-v98,v112,v24
M 26,a[168:171],54
L 3
v_fma_f32 v25,-v98,v113,v25
v_mul_f32 v118,v22,v22
M 26,a[176:179],58
R 78,2,27648
v_mul_f32 v119,v23,v23
M 26,a[184:187],62
v_mul_f32 v120,v24,v24
v_mul_f32 v121,v25,v25
M 30,a[180:183],58
R 82,2,28672
v_fma_f32 v118,v100,v118,v99
M 30,a[188:191],62
L 3
D 0,56,3072
M 30,a[196:199],66
R 86,2,29696
v_fma_f32 v119,v100,v119,v99
M 30,a[204:207],70
v_fma_f32 v120,v100,v120,v99
v_fma_f32 v121,v100,v121,v99
M 26,a[192:195],66
R 90,2,30720
v_mul_f32 v118,v22,v118
M 26,a[200:203],70
L 3
v_exp_f32 v118,v118
M 26,a[208:211],74
s_waitcnt vmcnt(5)
R 94,2,31744
v_mul_f32 v119,v23,v119
M 26,a[216:219],78
s_barrier
ds_read_b32 v7,v6 offset:1024
v_mul_f32 v120,v24,v120
M 30,a[212:215],74
R 34,1,0
v_mul_f32 v121,v25,v121
M 30,a[220:223],78
L 4
buffer_store_dword v4,v3,s[36:39],s70 offen offset:0 sc0 sc1
M 30,a[228:231],82
s_mov_b32 s70,s69
R 38,1,1024
v_fma_f32 v118,v118,v101,v101
M 30,a[236:239],86
s_mov_b32 s69,s68
v_exp_f32 v119,v119
M 26,a[224:227],82
s_add_u32 s68,s68,s63
R 42,1,2048
v_fma_f32 v119,v119,v101,v101
M 26,a[232:235],86
L 4
v_exp_f32 v120,v120
v_exp_f32 v121,v121
M 26,a[240:243],90
L 3
R 46,1,3072
v_fma_f32 v120,v120,v101,v101
v_fma_f32 v121,v121,v101,v101
v_rcp_f32 v118,v118
v_rcp_f32 v119,v119
v_rcp_f32 v120,v120
v_rcp_f32 v121,v121
M 26,a[248:251],94
v_mul_f32 v22,v22,v118
v_mul_f32 v23,v23,v119
v_mul_f32 v24,v24,v120
v_mul_f32 v25,v25,v121
v_cvt_pk_bf16_f32 v124,v22,v23
v_cvt_pk_bf16_f32 v125,v24,v25
v_cmp_eq_u32 vcc,s53,v7
M 30,a[244:247],90
s_cmp_eq_u32 vcc_lo,-1
R 50,1,4096
buffer_store_dwordx4 v[122:125],v15,s[36:39],s66 offen offset:0 sc0 sc1
buffer_store_dwordx4 v[122:125],v16,s[36:39],s66 offen offset:0 sc0 sc1
M 30,a[252:255],94
s_waitcnt vmcnt(7) lgkmcnt(3)
s_cbranch_scc1 Lg1_F0_%=
Lg1_L0_%=:
v_mfma_f32_16x16x32_bf16 v[22:25],a[4:7],v[34:37],0
s_add_u32 m0,s59,3072
s_mov_b32 s66,s65
R 54,1,5120
M 22,a[12:15],38
s_add_u32 s65,s65,s62
buffer_load_dword v8,s[36:39],s58 offen offset:0 sc1 lds
v_mfma_f32_16x16x32_bf16 v[18:21],a[0:3],v[34:37],0
R 58,1,6144
v_fma_f32 v26,-v102,v106,v26
M 18,a[8:11],38
L 3
v_fma_f32 v27,-v102,v107,v27
v_fma_f32 v28,-v102,v108,v28
M 18,a[16:19],42
R 62,1,7168
v_fma_f32 v29,-v102,v109,v29
M 18,a[24:27],46
v_mul_f32 v114,v26,v26
v_mul_f32 v115,v27,v27
M 22,a[20:23],42
R 66,1,8192
v_mul_f32 v116,v28,v28
M 22,a[28:31],46
L 3
v_mul_f32 v117,v29,v29
v_fma_f32 v114,v104,v114,v103
M 22,a[36:39],50
R 70,1,9216
v_fma_f32 v115,v104,v115,v103
M 22,a[44:47],54
v_fma_f32 v116,v104,v116,v103
v_fma_f32 v117,v104,v117,v103
M 18,a[32:35],50
R 74,1,10240
v_mul_f32 v114,v26,v114
M 18,a[40:43],54
L 3
v_exp_f32 v114,v114
M 18,a[48:51],58
R 78,1,11264
v_mul_f32 v115,v27,v115
M 18,a[56:59],62
v_exp_f32 v115,v115
v_mul_f32 v116,v28,v116
M 22,a[52:55],58
R 82,1,12288
v_mul_f32 v117,v29,v117
M 22,a[60:63],62
L 3
v_exp_f32 v116,v116
M 22,a[68:71],66
R 86,1,13312
v_fma_f32 v114,v114,v105,v105
M 22,a[76:79],70
buffer_load_dwordx4 v[98:101],v11,s[36:39],s73 offen offset:0 sc1
M 18,a[64:67],66
s_add_u32 s73,s73,s64
R 90,1,14336
v_fma_f32 v115,v115,v105,v105
M 18,a[72:75],70
L 3
v_exp_f32 v117,v117
M 18,a[80:83],74
s_waitcnt vmcnt(1)
R 94,1,15360
v_fma_f32 v116,v116,v105,v105
M 18,a[88:91],78
s_barrier
ds_read_b32 v7,v6 offset:3072
v_fma_f32 v117,v117,v105,v105
M 22,a[84:87],74
R 34,1,16384
v_fma_f32 v30,-v102,v110,v30
M 22,a[92:95],78
L 4
buffer_store_dword v4,v3,s[36:39],s70 offen offset:0 sc0 sc1
M 22,a[100:103],82
s_mov_b32 s70,0x7fff0000
R 38,1,17408
v_fma_f32 v31,-v102,v111,v31
M 22,a[108:111],86
v_rcp_f32 v114,v114
M 18,a[96:99],82
R 42,1,18432
v_rcp_f32 v115,v115
M 18,a[104:107],86
L 4
L 3
v_cmp_eq_u32 vcc,s53,v7
M 18,a[112:115],90
R 46,1,19456
v_mul_f32 v26,v26,v114
M 18,a[120:123],94
s_cmp_eq_u32 vcc_lo,-1
v_rcp_f32 v116,v116
s_cbranch_scc0 Lg1_X0_%=
M 22,a[116:119],90
s_add_u32 m0,s57,0
s_add_u32 s56,s55,0
s_add_u32 s58,s58,s61
R 50,1,20480
M 22,a[124:127],94
L 3
v_rcp_f32 v117,v117
M 22,a[132:135],34
s_add_u32 s54,s54,s46
R 54,1,21504
v_mul_f32 v27,v27,v115
M 22,a[140:143],38
D 0,56,0
M 18,a[128:131],34
R 58,1,22528
v_mul_f32 v28,v28,v116
M 18,a[136:139],38
L 3
v_mul_f32 v29,v29,v117
v_cvt_pk_bf16_f32 v122,v26,v27
M 18,a[144:147],42
R 62,1,23552
v_cvt_pk_bf16_f32 v123,v28,v29
M 18,a[152:155],46
D 0,56,1024
M 22,a[148:151],42
R 66,1,24576
v_fma_f32 v32,-v102,v112,v32
M 22,a[156:159],46
L 3
v_fma_f32 v33,-v102,v113,v33
v_mul_f32 v118,v30,v30
M 22,a[164:167],50
R 70,1,25600
v_mul_f32 v119,v31,v31
v_mul_f32 v120,v32,v32
M 22,a[172:175],54
D 0,56,2048
M 18,a[160:163],50
R 74,1,26624
v_mul_f32 v121,v33,v33
M 18,a[168:171],54
L 3
v_fma_f32 v118,v104,v118,v103
v_fma_f32 v119,v104,v119,v103
M 18,a[176:179],58
R 78,1,27648
v_fma_f32 v120,v104,v120,v103
M 18,a[184:187],62
D 0,56,3072
M 22,a[180:183],58
s_add_u32 m0,s59,0
R 82,1,28672
v_fma_f32 v121,v104,v121,v103
M 22,a[188:191],62
L 3
buffer_load_dword v8,s[36:39],s58 offen offset:0 sc1 lds
M 22,a[196:199],66
s_add_u32 m0,s57,16384
R 86,1,29696
v_mul_f32 v118,v30,v118
M 22,a[204:207],70
s_add_u32 s56,s55,16384
v_exp_f32 v118,v118
M 18,a[192:195],66
s_add_u32 s55,s55,s60
R 90,1,30720
v_mul_f32 v119,v31,v119
M 18,a[200:203],70
L 3
D 0,56,0
M 18,a[208:211],74
s_waitcnt vmcnt(1)
R 94,1,31744
v_mul_f32 v120,v32,v120
M 18,a[216:219],78
s_barrier
ds_read_b32 v7,v6 offset:0
v_exp_f32 v119,v119
M 22,a[212:215],74
s_sub_u32 s57,s84,s57
R 34,2,0
v_mul_f32 v121,v33,v121
M 22,a[220:223],78
L 4
buffer_store_dword v4,v3,s[36:39],s70 offen offset:0 sc0 sc1
M 22,a[228:231],82
s_mov_b32 s70,s69
R 38,2,1024
v_fma_f32 v118,v118,v105,v105
M 22,a[236:239],86
s_mov_b32 s69,s68
D 0,56,1024
M 18,a[224:227],82
s_add_u32 s68,s68,s63
R 42,2,2048
v_fma_f32 v119,v119,v105,v105
M 18,a[232:235],86
L 4
v_exp_f32 v120,v120
v_exp_f32 v121,v121
M 18,a[240:243],90
L 3
R 46,2,3072
v_fma_f32 v120,v120,v105,v105
v_fma_f32 v121,v121,v105,v105
v_rcp_f32 v118,v118
v_rcp_f32 v119,v119
v_rcp_f32 v120,v120
v_rcp_f32 v121,v121
M 18,a[248:251],94
v_mul_f32 v30,v30,v118
v_mul_f32 v31,v31,v119
v_mul_f32 v32,v32,v120
v_mul_f32 v33,v33,v121
v_cvt_pk_bf16_f32 v124,v30,v31
v_cvt_pk_bf16_f32 v125,v32,v33
v_cmp_eq_u32 vcc,s53,v7
M 22,a[244:247],90
s_cmp_eq_u32 vcc_lo,-1
R 50,2,4096
buffer_store_dwordx4 v[122:125],v15,s[36:39],s67 offen offset:0 sc0 sc1
D 0,56,3072
buffer_store_dwordx4 v[122:125],v16,s[36:39],s67 offen offset:0 sc0 sc1
M 22,a[252:255],94
L 3
D 0,56,2048
buffer_store_dword v4,v3,s[36:39],s71 offen offset:0 sc0 sc1
buffer_store_dword v4,v3,s[36:39],s71 offen offset:0 sc0 sc1
buffer_store_dword v4,v3,s[36:39],s71 offen offset:0 sc0 sc1
s_cbranch_scc1 Lg1_F1_%=
s_branch Lg1_L1_%=
Lg1_X0_%=:
M 22,a[116:119],90
s_add_u32 m0,s59,0
R 50,1,20480
v_mul_f32 v27,v27,v115
M 22,a[124:127],94
L 3
buffer_load_dword v8,s[36:39],s58 offen offset:0 sc1 lds
M 22,a[132:135],34
R 54,1,21504
v_mul_f32 v28,v28,v116
M 22,a[140:143],38
v_rcp_f32 v117,v117
M 18,a[128:131],34
R 58,1,22528
v_mul_f32 v29,v29,v117
M 18,a[136:139],38
L 3
v_cvt_pk_bf16_f32 v122,v26,v27
v_cvt_pk_bf16_f32 v123,v28,v29
M 18,a[144:147],42
R 62,1,23552
v_fma_f32 v32,-v102,v112,v32
M 18,a[152:155],46
v_fma_f32 v33,-v102,v113,v33
v_mul_f32 v118,v30,v30
M 22,a[148:151],42
R 66,1,24576
v_mul_f32 v119,v31,v31
M 22,a[156:159],46
L 3
v_mul_f32 v120,v32,v32
v_mul_f32 v121,v33,v33
M 22,a[164:167],50
R 70,1,25600
v_fma_f32 v118,v104,v118,v103
M 22,a[172:175],54
v_fma_f32 v119,v104,v119,v103
v_fma_f32 v120,v104,v120,v103
M 18,a[160:163],50
R 74,1,26624
v_fma_f32 v121,v104,v121,v103
M 18,a[168:171],54
L 3
v_mul_f32 v118,v30,v118
v_mul_f32 v119,v31,v119
M 18,a[176:179],58
R 78,1,27648
v_mul_f32 v120,v32,v120
M 18,a[184:187],62
v_exp_f32 v118,v118
M 22,a[180:183],58
R 82,1,28672
v_mul_f32 v121,v33,v121
M 22,a[188:191],62
L 3
v_exp_f32 v119,v119
M 22,a[196:199],66
R 86,1,29696
v_fma_f32 v118,v118,v105,v105
M 22,a[204:207],70
v_exp_f32 v120,v120
M 18,a[192:195],66
R 90,1,30720
v_fma_f32 v119,v119,v105,v105
M 18,a[200:203],70
L 3
v_exp_f32 v121,v121
M 18,a[208:211],74
s_waitcnt vmcnt(0)
R 94,1,31744
v_fma_f32 v120,v120,v105,v105
M 18,a[216:219],78
s_barrier
buffer_store_dword v4,v3,s[36:39],s70 offen offset:0 sc0 sc1
M 22,a[212:215],74
s_mov_b32 s70,s69
ds_read_b32 v7,v6 offset:0
v_fma_f32 v121,v121,v105,v105
M 22,a[220:223],78
L 3
v_rcp_f32 v118,v118
M 22,a[228:231],82
s_mov_b32 s69,s68
v_rcp_f32 v119,v119
M 22,a[236:239],86
s_add_u32 s68,s68,s63
v_rcp_f32 v120,v120
M 18,a[224:227],82
v_rcp_f32 v121,v121
M 18,a[232:235],86
L 1
v_mul_f32 v30,v30,v118
v_mul_f32 v31,v31,v119
M 18,a[240:243],90
L 0
v_mul_f32 v32,v32,v120
v_mul_f32 v33,v33,v121
M 18,a[248:251],94
v_cvt_pk_bf16_f32 v124,v30,v31
v_cvt_pk_bf16_f32 v125,v32,v33
M 22,a[244:247],90
buffer_store_dwordx4 v[122:125],v15,s[36:39],s67 offen offset:0 sc0 sc1
buffer_store_dwordx4 v[122:125],v16,s[36:39],s67 offen offset:0 sc0 sc1
M 22,a[252:255],94
s_nop 0
v_cmp_eq_u32 vcc,s53,v7
s_mov_b32 s80,1
s_cmp_eq_u32 vcc_lo,-1
s_cbranch_scc0 Lg1_xn0_%=
s_add_u32 m0,s57,0
s_add_u32 s56,s55,0
D 0,56,0
v_fma_f32 v18,-v98,v106,v18
v_fma_f32 v19,-v98,v107,v19
v_fma_f32 v20,-v98,v108,v20
v_fma_f32 v21,-v98,v109,v21
v_mul_f32 v114,v18,v18
v_mul_f32 v115,v19,v19
v_mul_f32 v116,v20,v20
D 0,56,1024
v_mul_f32 v117,v21,v21
v_fma_f32 v114,v100,v114,v99
v_fma_f32 v115,v100,v115,v99
v_fma_f32 v116,v100,v116,v99
v_fma_f32 v117,v100,v117,v99
v_mul_f32 v114,v18,v114
v_mul_f32 v115,v19,v115
D 0,56,2048
v_mul_f32 v116,v20,v116
v_mul_f32 v117,v21,v117
v_exp_f32 v114,v114
v_exp_f32 v115,v115
)"
R"(v_exp_f32 v116,v116
v_exp_f32 v117,v117
v_fma_f32 v114,v114,v101,v101
D 0,56,3072
v_fma_f32 v115,v115,v101,v101
v_fma_f32 v116,v116,v101,v101
v_fma_f32 v117,v117,v101,v101
v_rcp_f32 v114,v114
v_rcp_f32 v115,v115
v_rcp_f32 v116,v116
v_rcp_f32 v117,v117
s_add_u32 m0,s57,16384
s_add_u32 s56,s55,16384
D 0,56,0
v_mul_f32 v18,v18,v114
v_mul_f32 v19,v19,v115
v_mul_f32 v20,v20,v116
v_mul_f32 v21,v21,v117
v_cvt_pk_bf16_f32 v122,v18,v19
v_cvt_pk_bf16_f32 v123,v20,v21
v_fma_f32 v22,-v98,v110,v22
D 0,56,1024
v_fma_f32 v23,-v98,v111,v23
v_fma_f32 v24,-v98,v112,v24
v_fma_f32 v25,-v98,v113,v25
v_mul_f32 v118,v22,v22
v_mul_f32 v119,v23,v23
v_mul_f32 v120,v24,v24
v_mul_f32 v121,v25,v25
D 0,56,2048
v_fma_f32 v118,v100,v118,v99
v_fma_f32 v119,v100,v119,v99
v_fma_f32 v120,v100,v120,v99
v_fma_f32 v121,v100,v121,v99
v_mul_f32 v118,v22,v118
v_mul_f32 v119,v23,v119
v_mul_f32 v120,v24,v120
D 0,56,3072
v_mul_f32 v121,v25,v121
v_exp_f32 v118,v118
v_exp_f32 v119,v119
v_exp_f32 v120,v120
v_exp_f32 v121,v121
v_fma_f32 v118,v118,v101,v101
v_fma_f32 v119,v119,v101,v101
s_add_u32 s55,s55,s60
s_sub_u32 s57,s84,s57
s_add_u32 s54,s54,s46
v_fma_f32 v120,v120,v101,v101
v_fma_f32 v121,v121,v101,v101
v_rcp_f32 v118,v118
v_rcp_f32 v119,v119
v_rcp_f32 v120,v120
v_rcp_f32 v121,v121
v_mul_f32 v22,v22,v118
v_mul_f32 v23,v23,v119
v_mul_f32 v24,v24,v120
v_mul_f32 v25,v25,v121
v_cvt_pk_bf16_f32 v124,v22,v23
v_cvt_pk_bf16_f32 v125,v24,v25
buffer_store_dwordx4 v[122:125],v15,s[36:39],s66 offen offset:0 sc0 sc1
buffer_store_dwordx4 v[122:125],v16,s[36:39],s66 offen offset:0 sc0 sc1
s_mov_b32 s72,s69
s_mov_b32 s69,0x7fff0000
s_mov_b32 s66,0x7fff0000
s_mov_b32 s76,0x7fff0000
s_branch Lg1_gox_%=
Lg1_xn0_%=:
s_cmp_ge_u32 s54,256
s_cbranch_scc1 Lg1_fin0_%=
s_add_u32 m0,s59,4096
s_nop 0
buffer_load_dword v8,s[36:39],s58 offen offset:0 sc1 lds
buffer_store_dword v4,v3,s[36:39],s71 offen offset:0 sc0 sc1
s_add_u32 m0,s59,5120
s_nop 0
buffer_load_dword v8,s[36:39],s58 offen offset:0 sc1 lds
buffer_store_dword v4,v3,s[36:39],s71 offen offset:0 sc0 sc1
s_mov_b32 s83,0
v_fma_f32 v18,-v98,v106,v18
v_fma_f32 v19,-v98,v107,v19
v_fma_f32 v20,-v98,v108,v20
v_fma_f32 v21,-v98,v109,v21
v_mul_f32 v114,v18,v18
v_mul_f32 v115,v19,v19
v_mul_f32 v116,v20,v20
v_mul_f32 v117,v21,v21
v_fma_f32 v114,v100,v114,v99
v_fma_f32 v115,v100,v115,v99
v_fma_f32 v116,v100,v116,v99
v_fma_f32 v117,v100,v117,v99
v_mul_f32 v114,v18,v114
v_mul_f32 v115,v19,v115
v_mul_f32 v116,v20,v116
v_mul_f32 v117,v21,v117
v_exp_f32 v114,v114
v_exp_f32 v115,v115
v_exp_f32 v116,v116
v_exp_f32 v117,v117
v_fma_f32 v114,v114,v101,v101
v_fma_f32 v115,v115,v101,v101
v_fma_f32 v116,v116,v101,v101
v_fma_f32 v117,v117,v101,v101
v_rcp_f32 v114,v114
v_rcp_f32 v115,v115
v_rcp_f32 v116,v116
v_rcp_f32 v117,v117
v_mul_f32 v18,v18,v114
v_mul_f32 v19,v19,v115
v_mul_f32 v20,v20,v116
v_mul_f32 v21,v21,v117
v_cvt_pk_bf16_f32 v122,v18,v19
v_cvt_pk_bf16_f32 v123,v20,v21
v_fma_f32 v22,-v98,v110,v22
v_fma_f32 v23,-v98,v111,v23
v_fma_f32 v24,-v98,v112,v24
v_fma_f32 v25,-v98,v113,v25
v_mul_f32 v118,v22,v22
v_mul_f32 v119,v23,v23
v_mul_f32 v120,v24,v24
v_mul_f32 v121,v25,v25
v_fma_f32 v118,v100,v118,v99
v_fma_f32 v119,v100,v119,v99
v_fma_f32 v120,v100,v120,v99
v_fma_f32 v121,v100,v121,v99
v_mul_f32 v118,v22,v118
v_mul_f32 v119,v23,v119
v_mul_f32 v120,v24,v120
v_mul_f32 v121,v25,v121
v_exp_f32 v118,v118
v_exp_f32 v119,v119
v_exp_f32 v120,v120
v_exp_f32 v121,v121
v_fma_f32 v118,v118,v101,v101
v_fma_f32 v119,v119,v101,v101
v_fma_f32 v120,v120,v101,v101
v_fma_f32 v121,v121,v101,v101
v_rcp_f32 v118,v118
v_rcp_f32 v119,v119
v_rcp_f32 v120,v120
v_rcp_f32 v121,v121
v_mul_f32 v22,v22,v118
v_mul_f32 v23,v23,v119
v_mul_f32 v24,v24,v120
v_mul_f32 v25,v25,v121
v_cvt_pk_bf16_f32 v124,v22,v23
v_cvt_pk_bf16_f32 v125,v24,v25
buffer_store_dwordx4 v[122:125],v15,s[36:39],s66 offen offset:0 sc0 sc1
buffer_store_dwordx4 v[122:125],v16,s[36:39],s66 offen offset:0 sc0 sc1
s_mov_b32 s72,s69
s_mov_b32 s69,0x7fff0000
s_mov_b32 s66,0x7fff0000
s_mov_b32 s76,0x7fff0000
s_branch Lg1_poll_%=
Lg1_fin0_%=:
v_fma_f32 v18,-v98,v106,v18
v_fma_f32 v19,-v98,v107,v19
v_fma_f32 v20,-v98,v108,v20
v_fma_f32 v21,-v98,v109,v21
v_mul_f32 v114,v18,v18
v_mul_f32 v115,v19,v19
v_mul_f32 v116,v20,v20
v_mul_f32 v117,v21,v21
v_fma_f32 v114,v100,v114,v99
v_fma_f32 v115,v100,v115,v99
v_fma_f32 v116,v100,v116,v99
v_fma_f32 v117,v100,v117,v99
v_mul_f32 v114,v18,v114
v_mul_f32 v115,v19,v115
v_mul_f32 v116,v20,v116
v_mul_f32 v117,v21,v117
v_exp_f32 v114,v114
v_exp_f32 v115,v115
v_exp_f32 v116,v116
v_exp_f32 v117,v117
v_fma_f32 v114,v114,v101,v101
v_fma_f32 v115,v115,v101,v101
v_fma_f32 v116,v116,v101,v101
v_fma_f32 v117,v117,v101,v101
v_rcp_f32 v114,v114
v_rcp_f32 v115,v115
v_rcp_f32 v116,v116
v_rcp_f32 v117,v117
v_mul_f32 v18,v18,v114
v_mul_f32 v19,v19,v115
v_mul_f32 v20,v20,v116
v_mul_f32 v21,v21,v117
v_cvt_pk_bf16_f32 v122,v18,v19
v_cvt_pk_bf16_f32 v123,v20,v21
v_fma_f32 v22,-v98,v110,v22
v_fma_f32 v23,-v98,v111,v23
v_fma_f32 v24,-v98,v112,v24
v_fma_f32 v25,-v98,v113,v25
v_mul_f32 v118,v22,v22
v_mul_f32 v119,v23,v23
v_mul_f32 v120,v24,v24
v_mul_f32 v121,v25,v25
v_fma_f32 v118,v100,v118,v99
v_fma_f32 v119,v100,v119,v99
v_fma_f32 v120,v100,v120,v99
v_fma_f32 v121,v100,v121,v99
v_mul_f32 v118,v22,v118
v_mul_f32 v119,v23,v119
v_mul_f32 v120,v24,v120
v_mul_f32 v121,v25,v121
v_exp_f32 v118,v118
v_exp_f32 v119,v119
v_exp_f32 v120,v120
v_exp_f32 v121,v121
v_fma_f32 v118,v118,v101,v101
v_fma_f32 v119,v119,v101,v101
v_fma_f32 v120,v120,v101,v101
v_fma_f32 v121,v121,v101,v101
v_rcp_f32 v118,v118
v_rcp_f32 v119,v119
v_rcp_f32 v120,v120
v_rcp_f32 v121,v121
v_mul_f32 v22,v22,v118
v_mul_f32 v23,v23,v119
v_mul_f32 v24,v24,v120
v_mul_f32 v25,v25,v121
v_cvt_pk_bf16_f32 v124,v22,v23
v_cvt_pk_bf16_f32 v125,v24,v25
buffer_store_dwordx4 v[122:125],v15,s[36:39],s66 offen offset:0 sc0 sc1
buffer_store_dwordx4 v[122:125],v16,s[36:39],s66 offen offset:0 sc0 sc1
s_mov_b32 s72,s69
s_mov_b32 s69,0x7fff0000
s_mov_b32 s66,0x7fff0000
s_mov_b32 s76,0x7fff0000
s_branch Lg1_done_%=
Lg1_L1_%=:
v_mfma_f32_16x16x32_bf16 v[30:33],a[4:7],v[34:37],0
s_add_u32 m0,s59,3072
s_mov_b32 s67,s65
R 54,2,5120
M 30,a[12:15],38
s_add_u32 s65,s65,s62
buffer_load_dword v8,s[36:39],s58 offen offset:0 sc1 lds
v_mfma_f32_16x16x32_bf16 v[26:29],a[0:3],v[34:37],0
R 58,2,6144
v_fma_f32 v18,-v98,v106,v18
M 26,a[8:11],38
L 3
v_fma_f32 v19,-v98,v107,v19
v_fma_f32 v20,-v98,v108,v20
M 26,a[16:19],42
R 62,2,7168
v_fma_f32 v21,-v98,v109,v21
M 26,a[24:27],46
v_mul_f32 v114,v18,v18
v_mul_f32 v115,v19,v19
M 30,a[20:23],42
R 66,2,8192
v_mul_f32 v116,v20,v20
M 30,a[28:31],46
L 3
v_mul_f32 v117,v21,v21
v_fma_f32 v114,v100,v114,v99
M 30,a[36:39],50
R 70,2,9216
v_fma_f32 v115,v100,v115,v99
M 30,a[44:47],54
v_fma_f32 v116,v100,v116,v99
v_fma_f32 v117,v100,v117,v99
M 26,a[32:35],50
R 74,2,10240
v_mul_f32 v114,v18,v114
M 26,a[40:43],54
L 3
v_exp_f32 v114,v114
M 26,a[48:51],58
R 78,2,11264
v_mul_f32 v115,v19,v115
M 26,a[56:59],62
v_exp_f32 v115,v115
v_mul_f32 v116,v20,v116
M 30,a[52:55],58
R 82,2,12288
v_mul_f32 v117,v21,v117
M 30,a[60:63],62
L 3
v_exp_f32 v116,v116
M 30,a[68:71],66
R 86,2,13312
v_fma_f32 v114,v114,v101,v101
M 30,a[76:79],70
buffer_load_dwordx4 v[102:105],v11,s[36:39],s73 offen offset:0 sc1
M 26,a[64:67],66
s_add_u32 s73,s73,s64
R 90,2,14336
v_fma_f32 v115,v115,v101,v101
M 26,a[72:75],70
L 3
v_exp_f32 v117,v117
M 26,a[80:83],74
s_waitcnt vmcnt(1)
R 94,2,15360
v_fma_f32 v116,v116,v101,v101
M 26,a[88:91],78
s_barrier
ds_read_b32 v7,v6 offset:3072
v_fma_f32 v117,v117,v101,v101
M 30,a[84:87],74
R 34,2,16384
v_fma_f32 v22,-v98,v110,v22
M 30,a[92:95],78
L 4
buffer_store_dword v4,v3,s[36:39],s70 offen offset:0 sc0 sc1
M 30,a[100:103],82
s_mov_b32 s70,0x7fff0000
R 38,2,17408
v_fma_f32 v23,-v98,v111,v23
M 30,a[108:111],86
v_rcp_f32 v114,v114
M 26,a[96:99],82
R 42,2,18432
v_rcp_f32 v115,v115
M 26,a[104:107],86
L 4
L 3
v_cmp_eq_u32 vcc,s53,v7
M 26,a[112:115],90
R 46,2,19456
v_mul_f32 v18,v18,v114
M 26,a[120:123],94
s_cmp_eq_u32 vcc_lo,-1
v_rcp_f32 v116,v116
s_cbranch_scc0 Lg1_X1_%=
M 30,a[116:119],90
s_add_u32 m0,s57,0
s_add_u32 s56,s55,0
s_add_u32 s58,s58,s61
R 50,2,20480
M 30,a[124:127],94
L 3
v_rcp_f32 v117,v117
M 30,a[132:135],34
s_add_u32 s54,s54,s46
R 54,2,21504
v_mul_f32 v19,v19,v115
M 30,a[140:143],38
D 0,56,0
M 26,a[128:131],34
R 58,2,22528
v_mul_f32 v20,v20,v116
M 26,a[136:139],38
L 3
v_mul_f32 v21,v21,v117
v_cvt_pk_bf16_f32 v122,v18,v19
M 26,a[144:147],42
R 62,2,23552
v_cvt_pk_bf16_f32 v123,v20,v21
M 26,a[152:155],46
D 0,56,1024
M 30,a[148:151],42
R 66,2,24576
v_fma_f32 v24,-v98,v112,v24
M 30,a[156:159],46
L 3
v_fma_f32 v25,-v98,v113,v25
v_mul_f32 v118,v22,v22
M 30,a[164:167],50
R 70,2,25600
v_mul_f32 v119,v23,v23
v_mul_f32 v120,v24,v24
M 30,a[172:175],54
D 0,56,2048
M 26,a[160:163],50
R 74,2,26624
v_mul_f32 v121,v25,v25
M 26,a[168:171],54
L 3
v_fma_f32 v118,v100,v118,v99
v_fma_f32 v119,v100,v119,v99
M 26,a[176:179],58
R 78,2,27648
v_fma_f32 v120,v100,v120,v99
M 26,a[184:187],62
D 0,56,3072
M 30,a[180:183],58
s_add_u32 m0,s59,1024
R 82,2,28672
v_fma_f32 v121,v100,v121,v99
M 30,a[188:191],62
L 3
buffer_load_dword v8,s[36:39],s58 offen offset:0 sc1 lds
M 30,a[196:199],66
s_add_u32 m0,s57,16384
R 86,2,29696
v_mul_f32 v118,v22,v118
M 30,a[204:207],70
s_add_u32 s56,s55,16384
v_exp_f32 v118,v118
M 26,a[192:195],66
s_add_u32 s55,s55,s60
R 90,2,30720
v_mul_f32 v119,v23,v119
M 26,a[200:203],70
L 3
D 0,56,0
M 26,a[208:211],74
s_waitcnt vmcnt(1)
R 94,2,31744
v_mul_f32 v120,v24,v120
M 26,a[216:219],78
s_barrier
ds_read_b32 v7,v6 offset:1024
v_exp_f32 v119,v119
M 30,a[212:215],74
s_sub_u32 s57,s84,s57
R 34,1,0
v_mul_f32 v121,v25,v121
M 30,a[220:223],78
L 4
buffer_store_dword v4,v3,s[36:39],s70 offen offset:0 sc0 sc1
M 30,a[228:231],82
s_mov_b32 s70,s69
R 38,1,1024
v_fma_f32 v118,v118,v101,v101
M 30,a[236:239],86
s_mov_b32 s69,s68
D 0,56,1024
M 26,a[224:227],82
s_add_u32 s68,s68,s63
R 42,1,2048
v_fma_f32 v119,v119,v101,v101
M 26,a[232:235],86
L 4
v_exp_f32 v120,v120
v_exp_f32 v121,v121
M 26,a[240:243],90
L 3
R 46,1,3072
v_fma_f32 v120,v120,v101,v101
v_fma_f32 v121,v121,v101,v101
v_rcp_f32 v118,v118
v_rcp_f32 v119,v119
v_rcp_f32 v120,v120
v_rcp_f32 v121,v121
M 26,a[248:251],94
v_mul_f32 v22,v22,v118
v_mul_f32 v23,v23,v119
v_mul_f32 v24,v24,v120
v_mul_f32 v25,v25,v121
v_cvt_pk_bf16_f32 v124,v22,v23
v_cvt_pk_bf16_f32 v125,v24,v25
v_cmp_eq_u32 vcc,s53,v7
M 30,a[244:247],90
s_cmp_eq_u32 vcc_lo,-1
R 50,1,4096
buffer_store_dwordx4 v[122:125],v15,s[36:39],s66 offen offset:0 sc0 sc1
D 0,56,3072
buffer_store_dwordx4 v[122:125],v16,s[36:39],s66 offen offset:0 sc0 sc1
M 30,a[252:255],94
L 3
D 0,56,2048
buffer_store_dword v4,v3,s[36:39],s71 offen offset:0 sc0 sc1
buffer_store_dword v4,v3,s[36:39],s71 offen offset:0 sc0 sc1
buffer_store_dword v4,v3,s[36:39],s71 offen offset:0 sc0 sc1
s_cbranch_scc1 Lg1_F0_%=
s_branch Lg1_L0_%=
Lg1_X1_%=:
M 30,a[116:119],90
s_add_u32 m0,s59,1024
R 50,2,20480
v_mul_f32 v19,v19,v115
M 30,a[124:127],94
L 3
buffer_load_dword v8,s[36:39],s58 offen offset:0 sc1 lds
M 30,a[132:135],34
R 54,2,21504
v_mul_f32 v20,v20,v116
M 30,a[140:143],38
v_rcp_f32 v117,v117
M 26,a[128:131],34
R 58,2,22528
v_mul_f32 v21,v21,v117
M 26,a[136:139],38
L 3
v_cvt_pk_bf16_f32 v122,v18,v19
v_cvt_pk_bf16_f32 v123,v20,v21
M 26,a[144:147],42
R 62,2,23552
v_fma_f32 v24,-v98,v112,v24
M 26,a[152:155],46
v_fma_f32 v25,-v98,v113,v25
v_mul_f32 v118,v22,v22
M 30,a[148:151],42
R 66,2,24576
v_mul_f32 v119,v23,v23
M 30,a[156:159],46
L 3
v_mul_f32 v120,v24,v24
v_mul_f32 v121,v25,v25
M 30,a[164:167],50
R 70,2,25600
v_fma_f32 v118,v100,v118,v99
M 30,a[172:175],54
v_fma_f32 v119,v100,v119,v99
v_fma_f32 v120,v100,v120,v99
)"
R"(M 26,a[160:163],50
R 74,2,26624
v_fma_f32 v121,v100,v121,v99
M 26,a[168:171],54
L 3
v_mul_f32 v118,v22,v118
v_mul_f32 v119,v23,v119
M 26,a[176:179],58
R 78,2,27648
v_mul_f32 v120,v24,v120
M 26,a[184:187],62
v_exp_f32 v118,v118
M 30,a[180:183],58
R 82,2,28672
v_mul_f32 v121,v25,v121
M 30,a[188:191],62
L 3
v_exp_f32 v119,v119
M 30,a[196:199],66
R 86,2,29696
v_fma_f32 v118,v118,v101,v101
M 30,a[204:207],70
v_exp_f32 v120,v120
M 26,a[192:195],66
R 90,2,30720
v_fma_f32 v119,v119,v101,v101
M 26,a[200:203],70
L 3
v_exp_f32 v121,v121
M 26,a[208:211],74
s_waitcnt vmcnt(0)
R 94,2,31744
v_fma_f32 v120,v120,v101,v101
M 26,a[216:219],78
s_barrier
buffer_store_dword v4,v3,s[36:39],s70 offen offset:0 sc0 sc1
M 30,a[212:215],74
s_mov_b32 s70,s69
ds_read_b32 v7,v6 offset:1024
v_fma_f32 v121,v121,v101,v101
M 30,a[220:223],78
L 3
v_rcp_f32 v118,v118
M 30,a[228:231],82
s_mov_b32 s69,s68
v_rcp_f32 v119,v119
M 30,a[236:239],86
s_add_u32 s68,s68,s63
v_rcp_f32 v120,v120
M 26,a[224:227],82
v_rcp_f32 v121,v121
M 26,a[232:235],86
L 1
v_mul_f32 v22,v22,v118
v_mul_f32 v23,v23,v119
M 26,a[240:243],90
L 0
v_mul_f32 v24,v24,v120
v_mul_f32 v25,v25,v121
M 26,a[248:251],94
v_cvt_pk_bf16_f32 v124,v22,v23
v_cvt_pk_bf16_f32 v125,v24,v25
M 30,a[244:247],90
buffer_store_dwordx4 v[122:125],v15,s[36:39],s66 offen offset:0 sc0 sc1
buffer_store_dwordx4 v[122:125],v16,s[36:39],s66 offen offset:0 sc0 sc1
M 30,a[252:255],94
s_nop 0
v_cmp_eq_u32 vcc,s53,v7
s_mov_b32 s80,0
s_cmp_eq_u32 vcc_lo,-1
s_cbranch_scc0 Lg1_xn1_%=
s_add_u32 m0,s57,0
s_add_u32 s56,s55,0
D 0,56,0
v_fma_f32 v26,-v102,v106,v26
v_fma_f32 v27,-v102,v107,v27
v_fma_f32 v28,-v102,v108,v28
v_fma_f32 v29,-v102,v109,v29
v_mul_f32 v114,v26,v26
v_mul_f32 v115,v27,v27
v_mul_f32 v116,v28,v28
D 0,56,1024
v_mul_f32 v117,v29,v29
v_fma_f32 v114,v104,v114,v103
v_fma_f32 v115,v104,v115,v103
v_fma_f32 v116,v104,v116,v103
v_fma_f32 v117,v104,v117,v103
v_mul_f32 v114,v26,v114
v_mul_f32 v115,v27,v115
D 0,56,2048
v_mul_f32 v116,v28,v116
v_mul_f32 v117,v29,v117
v_exp_f32 v114,v114
v_exp_f32 v115,v115
v_exp_f32 v116,v116
v_exp_f32 v117,v117
v_fma_f32 v114,v114,v105,v105
D 0,56,3072
v_fma_f32 v115,v115,v105,v105
v_fma_f32 v116,v116,v105,v105
v_fma_f32 v117,v117,v105,v105
v_rcp_f32 v114,v114
v_rcp_f32 v115,v115
v_rcp_f32 v116,v116
v_rcp_f32 v117,v117
s_add_u32 m0,s57,16384
s_add_u32 s56,s55,16384
D 0,56,0
v_mul_f32 v26,v26,v114
v_mul_f32 v27,v27,v115
v_mul_f32 v28,v28,v116
v_mul_f32 v29,v29,v117
v_cvt_pk_bf16_f32 v122,v26,v27
v_cvt_pk_bf16_f32 v123,v28,v29
v_fma_f32 v30,-v102,v110,v30
D 0,56,1024
v_fma_f32 v31,-v102,v111,v31
v_fma_f32 v32,-v102,v112,v32
v_fma_f32 v33,-v102,v113,v33
v_mul_f32 v118,v30,v30
v_mul_f32 v119,v31,v31
v_mul_f32 v120,v32,v32
v_mul_f32 v121,v33,v33
D 0,56,2048
v_fma_f32 v118,v104,v118,v103
v_fma_f32 v119,v104,v119,v103
v_fma_f32 v120,v104,v120,v103
v_fma_f32 v121,v104,v121,v103
v_mul_f32 v118,v30,v118
v_mul_f32 v119,v31,v119
v_mul_f32 v120,v32,v120
D 0,56,3072
v_mul_f32 v121,v33,v121
v_exp_f32 v118,v118
v_exp_f32 v119,v119
v_exp_f32 v120,v120
v_exp_f32 v121,v121
v_fma_f32 v118,v118,v105,v105
v_fma_f32 v119,v119,v105,v105
s_add_u32 s55,s55,s60
s_sub_u32 s57,s84,s57
s_add_u32 s54,s54,s46
v_fma_f32 v120,v120,v105,v105
v_fma_f32 v121,v121,v105,v105
v_rcp_f32 v118,v118
v_rcp_f32 v119,v119
v_rcp_f32 v120,v120
v_rcp_f32 v121,v121
v_mul_f32 v30,v30,v118
v_mul_f32 v31,v31,v119
v_mul_f32 v32,v32,v120
v_mul_f32 v33,v33,v121
v_cvt_pk_bf16_f32 v124,v30,v31
v_cvt_pk_bf16_f32 v125,v32,v33
buffer_store_dwordx4 v[122:125],v15,s[36:39],s67 offen offset:0 sc0 sc1
buffer_store_dwordx4 v[122:125],v16,s[36:39],s67 offen offset:0 sc0 sc1
s_mov_b32 s72,s69
s_mov_b32 s69,0x7fff0000
s_mov_b32 s67,0x7fff0000
s_mov_b32 s77,0x7fff0000
s_branch Lg1_gox_%=
Lg1_xn1_%=:
s_cmp_ge_u32 s54,256
s_cbranch_scc1 Lg1_fin1_%=
s_add_u32 m0,s59,4096
s_nop 0
buffer_load_dword v8,s[36:39],s58 offen offset:0 sc1 lds
buffer_store_dword v4,v3,s[36:39],s71 offen offset:0 sc0 sc1
s_add_u32 m0,s59,5120
s_nop 0
buffer_load_dword v8,s[36:39],s58 offen offset:0 sc1 lds
buffer_store_dword v4,v3,s[36:39],s71 offen offset:0 sc0 sc1
s_mov_b32 s83,0
v_fma_f32 v26,-v102,v106,v26
v_fma_f32 v27,-v102,v107,v27
v_fma_f32 v28,-v102,v108,v28
v_fma_f32 v29,-v102,v109,v29
v_mul_f32 v114,v26,v26
v_mul_f32 v115,v27,v27
v_mul_f32 v116,v28,v28
v_mul_f32 v117,v29,v29
v_fma_f32 v114,v104,v114,v103
v_fma_f32 v115,v104,v115,v103
v_fma_f32 v116,v104,v116,v103
v_fma_f32 v117,v104,v117,v103
v_mul_f32 v114,v26,v114
v_mul_f32 v115,v27,v115
v_mul_f32 v116,v28,v116
v_mul_f32 v117,v29,v117
v_exp_f32 v114,v114
v_exp_f32 v115,v115
v_exp_f32 v116,v116
v_exp_f32 v117,v117
v_fma_f32 v114,v114,v105,v105
v_fma_f32 v115,v115,v105,v105
v_fma_f32 v116,v116,v105,v105
v_fma_f32 v117,v117,v105,v105
v_rcp_f32 v114,v114
v_rcp_f32 v115,v115
v_rcp_f32 v116,v116
v_rcp_f32 v117,v117
v_mul_f32 v26,v26,v114
v_mul_f32 v27,v27,v115
v_mul_f32 v28,v28,v116
v_mul_f32 v29,v29,v117
v_cvt_pk_bf16_f32 v122,v26,v27
v_cvt_pk_bf16_f32 v123,v28,v29
v_fma_f32 v30,-v102,v110,v30
v_fma_f32 v31,-v102,v111,v31
v_fma_f32 v32,-v102,v112,v32
v_fma_f32 v33,-v102,v113,v33
v_mul_f32 v118,v30,v30
v_mul_f32 v119,v31,v31
v_mul_f32 v120,v32,v32
v_mul_f32 v121,v33,v33
v_fma_f32 v118,v104,v118,v103
v_fma_f32 v119,v104,v119,v103
v_fma_f32 v120,v104,v120,v103
v_fma_f32 v121,v104,v121,v103
v_mul_f32 v118,v30,v118
v_mul_f32 v119,v31,v119
v_mul_f32 v120,v32,v120
v_mul_f32 v121,v33,v121
v_exp_f32 v118,v118
v_exp_f32 v119,v119
v_exp_f32 v120,v120
v_exp_f32 v121,v121
v_fma_f32 v118,v118,v105,v105
v_fma_f32 v119,v119,v105,v105
v_fma_f32 v120,v120,v105,v105
v_fma_f32 v121,v121,v105,v105
v_rcp_f32 v118,v118
v_rcp_f32 v119,v119
v_rcp_f32 v120,v120
v_rcp_f32 v121,v121
v_mul_f32 v30,v30,v118
v_mul_f32 v31,v31,v119
v_mul_f32 v32,v32,v120
v_mul_f32 v33,v33,v121
v_cvt_pk_bf16_f32 v124,v30,v31
v_cvt_pk_bf16_f32 v125,v32,v33
buffer_store_dwordx4 v[122:125],v15,s[36:39],s67 offen offset:0 sc0 sc1
buffer_store_dwordx4 v[122:125],v16,s[36:39],s67 offen offset:0 sc0 sc1
s_mov_b32 s72,s69
s_mov_b32 s69,0x7fff0000
s_mov_b32 s67,0x7fff0000
s_mov_b32 s77,0x7fff0000
s_branch Lg1_poll_%=
Lg1_fin1_%=:
v_fma_f32 v26,-v102,v106,v26
v_fma_f32 v27,-v102,v107,v27
v_fma_f32 v28,-v102,v108,v28
v_fma_f32 v29,-v102,v109,v29
v_mul_f32 v114,v26,v26
v_mul_f32 v115,v27,v27
v_mul_f32 v116,v28,v28
v_mul_f32 v117,v29,v29
v_fma_f32 v114,v104,v114,v103
v_fma_f32 v115,v104,v115,v103
v_fma_f32 v116,v104,v116,v103
v_fma_f32 v117,v104,v117,v103
v_mul_f32 v114,v26,v114
v_mul_f32 v115,v27,v115
v_mul_f32 v116,v28,v116
v_mul_f32 v117,v29,v117
v_exp_f32 v114,v114
v_exp_f32 v115,v115
v_exp_f32 v116,v116
v_exp_f32 v117,v117
v_fma_f32 v114,v114,v105,v105
v_fma_f32 v115,v115,v105,v105
v_fma_f32 v116,v116,v105,v105
v_fma_f32 v117,v117,v105,v105
v_rcp_f32 v114,v114
v_rcp_f32 v115,v115
v_rcp_f32 v116,v116
v_rcp_f32 v117,v117
v_mul_f32 v26,v26,v114
v_mul_f32 v27,v27,v115
v_mul_f32 v28,v28,v116
v_mul_f32 v29,v29,v117
v_cvt_pk_bf16_f32 v122,v26,v27
v_cvt_pk_bf16_f32 v123,v28,v29
v_fma_f32 v30,-v102,v110,v30
v_fma_f32 v31,-v102,v111,v31
v_fma_f32 v32,-v102,v112,v32
v_fma_f32 v33,-v102,v113,v33
v_mul_f32 v118,v30,v30
v_mul_f32 v119,v31,v31
v_mul_f32 v120,v32,v32
v_mul_f32 v121,v33,v33
v_fma_f32 v118,v104,v118,v103
v_fma_f32 v119,v104,v119,v103
v_fma_f32 v120,v104,v120,v103
v_fma_f32 v121,v104,v121,v103
v_mul_f32 v118,v30,v118
v_mul_f32 v119,v31,v119
v_mul_f32 v120,v32,v120
v_mul_f32 v121,v33,v121
v_exp_f32 v118,v118
v_exp_f32 v119,v119
v_exp_f32 v120,v120
v_exp_f32 v121,v121
v_fma_f32 v118,v118,v105,v105
v_fma_f32 v119,v119,v105,v105
v_fma_f32 v120,v120,v105,v105
v_fma_f32 v121,v121,v105,v105
v_rcp_f32 v118,v118
v_rcp_f32 v119,v119
v_rcp_f32 v120,v120
v_rcp_f32 v121,v121
v_mul_f32 v30,v30,v118
v_mul_f32 v31,v31,v119
v_mul_f32 v32,v32,v120
v_mul_f32 v33,v33,v121
v_cvt_pk_bf16_f32 v124,v30,v31
v_cvt_pk_bf16_f32 v125,v32,v33
buffer_store_dwordx4 v[122:125],v15,s[36:39],s67 offen offset:0 sc0 sc1
buffer_store_dwordx4 v[122:125],v16,s[36:39],s67 offen offset:0 sc0 sc1
s_mov_b32 s72,s69
s_mov_b32 s69,0x7fff0000
s_mov_b32 s67,0x7fff0000
s_mov_b32 s77,0x7fff0000
s_branch Lg1_done_%=
Lg1_done_%=:
s_waitcnt vmcnt(2)
s_barrier
buffer_store_dword v4,v3,s[36:39],s70 offen offset:0 sc0 sc1
s_waitcnt vmcnt(1)
s_barrier
buffer_store_dword v4,v3,s[36:39],s72 offen offset:0 sc0 sc1
s_waitcnt vmcnt(0) lgkmcnt(0)
s_barrier
s_branch Lg2_end_%=
Lg2_begin_%=:
Lcode_g2_b:
s_mov_b64 s[36:37],%1
s_mov_b32 s38,111243392
s_mov_b32 s39,0x00020000
s_mov_b32 s44,%2
s_mov_b32 s45,%3
s_mov_b32 s46,%4
s_mov_b32 s47,%5
s_mov_b32 s48,%6
s_mov_b32 s49,%7
s_mov_b32 s50,%8
s_mov_b32 s51,%9
s_mov_b32 s93,%10
v_mov_b32 v0,%11
v_mov_b32 v3,%12
v_mov_b32 v5,%13
v_mov_b32 v6,%14
v_mov_b32 v18,%15
v_mov_b32 v19,%16
v_mov_b32 v20,%17
v_mov_b32 v21,%18
v_mov_b32 v9,%19
v_mov_b32 v10,%20
v_mov_b32 v11,%21
v_mov_b32 v12,%22
v_mov_b32 v13,%23
v_mov_b32 v14,%24
v_mov_b32 v15,%25
v_mov_b32 v16,%26
s_mov_b64 s[40:41],%27
s_mov_b32 s42,2097152
s_mov_b32 s43,0x00020000
s_lshl_b32 s57,s44,11
s_mov_b32 s79,111243264
buffer_load_dword v4,off,s[36:39],s79 sc1
s_add_u32 s55,s50,0
s_add_u32 m0,s57,0
s_nop 0
buffer_load_dwordx4 v18,s[40:43],s55 offen offset:0  lds
s_add_u32 m0,s57,1024
s_nop 0
buffer_load_dwordx4 v19,s[40:43],s55 offen offset:0  lds
s_add_u32 m0,s57,8064
s_nop 0
buffer_load_dwordx4 v18,s[40:43],s55 offen offset:128  lds
s_add_u32 m0,s57,9088
s_nop 0
buffer_load_dwordx4 v19,s[40:43],s55 offen offset:128  lds
s_add_u32 s55,s50,131072
s_add_u32 m0,s57,16384
s_nop 0
buffer_load_dwordx4 v18,s[40:43],s55 offen offset:0  lds
s_add_u32 m0,s57,17408
s_nop 0
buffer_load_dwordx4 v19,s[40:43],s55 offen offset:0  lds
s_add_u32 m0,s57,24448
s_nop 0
buffer_load_dwordx4 v18,s[40:43],s55 offen offset:128  lds
s_add_u32 m0,s57,25472
s_nop 0
buffer_load_dwordx4 v19,s[40:43],s55 offen offset:128  lds
s_add_u32 s55,s50,262144
s_add_u32 m0,s57,32768
s_nop 0
buffer_load_dwordx4 v18,s[40:43],s55 offen offset:0  lds
s_add_u32 m0,s57,33792
s_nop 0
buffer_load_dwordx4 v19,s[40:43],s55 offen offset:0  lds
s_add_u32 m0,s57,40832
s_nop 0
buffer_load_dwordx4 v18,s[40:43],s55 offen offset:128  lds
s_add_u32 m0,s57,41856
s_nop 0
buffer_load_dwordx4 v19,s[40:43],s55 offen offset:128  lds
s_add_u32 s55,s50,393216
s_add_u32 m0,s57,49152
s_nop 0
buffer_load_dwordx4 v18,s[40:43],s55 offen offset:0  lds
s_add_u32 m0,s57,50176
s_nop 0
buffer_load_dwordx4 v19,s[40:43],s55 offen offset:0  lds
s_add_u32 m0,s57,57216
s_nop 0
buffer_load_dwordx4 v18,s[40:43],s55 offen offset:128  lds
s_add_u32 m0,s57,58240
s_nop 0
buffer_load_dwordx4 v19,s[40:43],s55 offen offset:128  lds
s_add_u32 s55,s50,524288
s_add_u32 m0,s57,65536
s_nop 0
buffer_load_dwordx4 v18,s[40:43],s55 offen offset:0  lds
s_add_u32 m0,s57,66560
s_nop 0
buffer_load_dwordx4 v19,s[40:43],s55 offen offset:0  lds
s_add_u32 m0,s57,73600
s_nop 0
buffer_load_dwordx4 v18,s[40:43],s55 offen offset:128  lds
s_add_u32 m0,s57,74624
s_nop 0
buffer_load_dwordx4 v19,s[40:43],s55 offen offset:128  lds
s_add_u32 s55,s50,655360
s_add_u32 m0,s57,81920
s_nop 0
buffer_load_dwordx4 v18,s[40:43],s55 offen offset:0  lds
s_add_u32 m0,s57,82944
s_nop 0
buffer_load_dwordx4 v19,s[40:43],s55 offen offset:0  lds
s_add_u32 m0,s57,89984
s_nop 0
buffer_load_dwordx4 v18,s[40:43],s55 offen offset:128  lds
s_add_u32 m0,s57,91008
s_nop 0
buffer_load_dwordx4 v19,s[40:43],s55 offen offset:128  lds
s_waitcnt vmcnt(24)
s_barrier
v_readfirstlane_b32 s53,v4
s_nop 3
s_add_u32 s53,s53,1
s_cmp_eq_u32 s53,0
s_cselect_b32 s53,1,s53
v_mov_b32 v4,s53
s_waitcnt vmcnt(20) lgkmcnt(0)
s_barrier
s_add_u32 s55,s50,786432
s_add_u32 m0,s57,98304
s_nop 0
buffer_load_dwordx4 v18,s[40:43],s55 offen offset:0  lds
s_add_u32 m0,s57,99328
s_nop 0
buffer_load_dwordx4 v19,s[40:43],s55 offen offset:0  lds
s_add_u32 m0,s57,106368
s_nop 0
buffer_load_dwordx4 v18,s[40:43],s55 offen offset:128  lds
s_add_u32 m0,s57,107392
s_nop 0
buffer_load_dwordx4 v19,s[40:43],s55 offen offset:128  lds
v_add_u32 v26,0,v20
v_add_u32 v27,0,v21
T a[0:1],26,0
T a[2:3],26,512
T a[4:5],27,0
T a[6:7],27,512
T a[8:9],26,4096
T a[10:11],26,4608
T a[12:13],27,4096
T a[14:15],27,4608
s_waitcnt vmcnt(20) lgkmcnt(0)
s_barrier
s_add_u32 s55,s50,917504
s_add_u32 m0,s57,0
s_nop 0
buffer_load_dwordx4 v18,s[40:43],s55 offen offset:0  lds
s_add_u32 m0,s57,1024
s_nop 0
buffer_load_dwordx4 v19,s[40:43],s55 offen offset:0  lds
s_add_u32 m0,s57,8064
s_nop 0
buffer_load_dwordx4 v18,s[40:43],s55 offen offset:128  lds
s_add_u32 m0,s57,9088
s_nop 0
buffer_load_dwordx4 v19,s[40:43],s55 offen offset:128  lds
v_add_u32 v26,16384,v20
v_add_u32 v27,16384,v21
T a[16:17],26,0
T a[18:19],26,512
)"
R"(T a[20:21],27,0
T a[22:23],27,512
T a[24:25],26,4096
T a[26:27],26,4608
T a[28:29],27,4096
T a[30:31],27,4608
s_waitcnt vmcnt(20) lgkmcnt(0)
s_barrier
s_add_u32 s55,s50,1048576
s_add_u32 m0,s57,16384
s_nop 0
buffer_load_dwordx4 v18,s[40:43],s55 offen offset:0  lds
s_add_u32 m0,s57,17408
s_nop 0
buffer_load_dwordx4 v19,s[40:43],s55 offen offset:0  lds
s_add_u32 m0,s57,24448
s_nop 0
buffer_load_dwordx4 v18,s[40:43],s55 offen offset:128  lds
s_add_u32 m0,s57,25472
s_nop 0
buffer_load_dwordx4 v19,s[40:43],s55 offen offset:128  lds
v_add_u32 v26,32768,v20
v_add_u32 v27,32768,v21
T a[32:33],26,0
T a[34:35],26,512
T a[36:37],27,0
T a[38:39],27,512
T a[40:41],26,4096
T a[42:43],26,4608
T a[44:45],27,4096
T a[46:47],27,4608
s_waitcnt vmcnt(20) lgkmcnt(0)
s_barrier
s_add_u32 s55,s50,1179648
s_add_u32 m0,s57,32768
s_nop 0
buffer_load_dwordx4 v18,s[40:43],s55 offen offset:0  lds
s_add_u32 m0,s57,33792
s_nop 0
buffer_load_dwordx4 v19,s[40:43],s55 offen offset:0  lds
s_add_u32 m0,s57,40832
s_nop 0
buffer_load_dwordx4 v18,s[40:43],s55 offen offset:128  lds
s_add_u32 m0,s57,41856
s_nop 0
buffer_load_dwordx4 v19,s[40:43],s55 offen offset:128  lds
v_add_u32 v26,49152,v20
v_add_u32 v27,49152,v21
T a[48:49],26,0
T a[50:51],26,512
T a[52:53],27,0
T a[54:55],27,512
T a[56:57],26,4096
T a[58:59],26,4608
T a[60:61],27,4096
T a[62:63],27,4608
s_waitcnt vmcnt(20) lgkmcnt(0)
s_barrier
s_add_u32 s55,s50,1310720
s_add_u32 m0,s57,49152
s_nop 0
buffer_load_dwordx4 v18,s[40:43],s55 offen offset:0  lds
s_add_u32 m0,s57,50176
s_nop 0
buffer_load_dwordx4 v19,s[40:43],s55 offen offset:0  lds
s_add_u32 m0,s57,57216
s_nop 0
buffer_load_dwordx4 v18,s[40:43],s55 offen offset:128  lds
s_add_u32 m0,s57,58240
s_nop 0
buffer_load_dwordx4 v19,s[40:43],s55 offen offset:128  lds
v_add_u32 v26,65536,v20
v_add_u32 v27,65536,v21
T a[64:65],26,0
T a[66:67],26,512
T a[68:69],27,0
T a[70:71],27,512
T a[72:73],26,4096
T a[74:75],26,4608
T a[76:77],27,4096
T a[78:79],27,4608
s_waitcnt vmcnt(20) lgkmcnt(0)
s_barrier
s_add_u32 s55,s50,1441792
s_add_u32 m0,s57,65536
s_nop 0
buffer_load_dwordx4 v18,s[40:43],s55 offen offset:0  lds
s_add_u32 m0,s57,66560
s_nop 0
buffer_load_dwordx4 v19,s[40:43],s55 offen offset:0  lds
s_add_u32 m0,s57,73600
s_nop 0
buffer_load_dwordx4 v18,s[40:43],s55 offen offset:128  lds
s_add_u32 m0,s57,74624
s_nop 0
buffer_load_dwordx4 v19,s[40:43],s55 offen offset:128  lds
v_add_u32 v26,81920,v20
v_add_u32 v27,81920,v21
T a[80:81],26,0
T a[82:83],26,512
T a[84:85],27,0
T a[86:87],27,512
T a[88:89],26,4096
T a[90:91],26,4608
T a[92:93],27,4096
T a[94:95],27,4608
s_waitcnt vmcnt(20) lgkmcnt(0)
s_barrier
s_add_u32 s55,s50,1572864
s_add_u32 m0,s57,81920
s_nop 0
buffer_load_dwordx4 v18,s[40:43],s55 offen offset:0  lds
s_add_u32 m0,s57,82944
s_nop 0
buffer_load_dwordx4 v19,s[40:43],s55 offen offset:0  lds
s_add_u32 m0,s57,89984
s_nop 0
buffer_load_dwordx4 v18,s[40:43],s55 offen offset:128  lds
s_add_u32 m0,s57,91008
s_nop 0
buffer_load_dwordx4 v19,s[40:43],s55 offen offset:128  lds
v_add_u32 v26,98304,v20
v_add_u32 v27,98304,v21
T a[96:97],26,0
T a[98:99],26,512
T a[100:101],27,0
T a[102:103],27,512
T a[104:105],26,4096
T a[106:107],26,4608
T a[108:109],27,4096
T a[110:111],27,4608
s_waitcnt vmcnt(20) lgkmcnt(0)
s_barrier
s_add_u32 s55,s50,1703936
s_add_u32 m0,s57,98304
s_nop 0
buffer_load_dwordx4 v18,s[40:43],s55 offen offset:0  lds
s_add_u32 m0,s57,99328
s_nop 0
buffer_load_dwordx4 v19,s[40:43],s55 offen offset:0  lds
s_add_u32 m0,s57,106368
s_nop 0
buffer_load_dwordx4 v18,s[40:43],s55 offen offset:128  lds
s_add_u32 m0,s57,107392
s_nop 0
buffer_load_dwordx4 v19,s[40:43],s55 offen offset:128  lds
v_add_u32 v26,0,v20
v_add_u32 v27,0,v21
T a[112:113],26,0
T a[114:115],26,512
T a[116:117],27,0
T a[118:119],27,512
T a[120:121],26,4096
T a[122:123],26,4608
T a[124:125],27,4096
T a[126:127],27,4608
s_waitcnt vmcnt(20) lgkmcnt(0)
s_barrier
s_add_u32 s55,s50,1835008
s_add_u32 m0,s57,0
s_nop 0
buffer_load_dwordx4 v18,s[40:43],s55 offen offset:0  lds
s_add_u32 m0,s57,1024
s_nop 0
buffer_load_dwordx4 v19,s[40:43],s55 offen offset:0  lds
s_add_u32 m0,s57,8064
s_nop 0
buffer_load_dwordx4 v18,s[40:43],s55 offen offset:128  lds
s_add_u32 m0,s57,9088
s_nop 0
buffer_load_dwordx4 v19,s[40:43],s55 offen offset:128  lds
v_add_u32 v26,16384,v20
v_add_u32 v27,16384,v21
T a[128:129],26,0
T a[130:131],26,512
T a[132:133],27,0
T a[134:135],27,512
T a[136:137],26,4096
T a[138:139],26,4608
T a[140:141],27,4096
T a[142:143],27,4608
s_waitcnt vmcnt(20) lgkmcnt(0)
s_barrier
s_add_u32 s55,s50,1966080
s_add_u32 m0,s57,16384
s_nop 0
buffer_load_dwordx4 v18,s[40:43],s55 offen offset:0  lds
s_add_u32 m0,s57,17408
s_nop 0
buffer_load_dwordx4 v19,s[40:43],s55 offen offset:0  lds
s_add_u32 m0,s57,24448
s_nop 0
buffer_load_dwordx4 v18,s[40:43],s55 offen offset:128  lds
s_add_u32 m0,s57,25472
s_nop 0
buffer_load_dwordx4 v19,s[40:43],s55 offen offset:128  lds
v_add_u32 v26,32768,v20
v_add_u32 v27,32768,v21
T a[144:145],26,0
T a[146:147],26,512
T a[148:149],27,0
T a[150:151],27,512
T a[152:153],26,4096
T a[154:155],26,4608
T a[156:157],27,4096
T a[158:159],27,4608
s_waitcnt vmcnt(20) lgkmcnt(0)
s_barrier
v_add_u32 v26,49152,v20
v_add_u32 v27,49152,v21
T a[160:161],26,0
T a[162:163],26,512
T a[164:165],27,0
T a[166:167],27,512
T a[168:169],26,4096
T a[170:171],26,4608
T a[172:173],27,4096
T a[174:175],27,4608
s_waitcnt vmcnt(16) lgkmcnt(0)
s_barrier
v_add_u32 v26,65536,v20
v_add_u32 v27,65536,v21
T a[176:177],26,0
T a[178:179],26,512
T a[180:181],27,0
T a[182:183],27,512
T a[184:185],26,4096
T a[186:187],26,4608
T a[188:189],27,4096
T a[190:191],27,4608
s_waitcnt vmcnt(12) lgkmcnt(0)
s_barrier
v_add_u32 v26,81920,v20
v_add_u32 v27,81920,v21
T a[192:193],26,0
T a[194:195],26,512
T a[196:197],27,0
T a[198:199],27,512
T a[200:201],26,4096
T a[202:203],26,4608
T a[204:205],27,4096
T a[206:207],27,4608
s_waitcnt vmcnt(8) lgkmcnt(0)
s_barrier
v_add_u32 v26,98304,v20
v_add_u32 v27,98304,v21
T a[208:209],26,0
T a[210:211],26,512
T a[212:213],27,0
T a[214:215],27,512
T a[216:217],26,4096
T a[218:219],26,4608
T a[220:221],27,4096
T a[222:223],27,4608
s_waitcnt vmcnt(4) lgkmcnt(0)
s_barrier
v_add_u32 v26,0,v20
v_add_u32 v27,0,v21
T a[224:225],26,0
T a[226:227],26,512
T a[228:229],27,0
T a[230:231],27,512
T a[232:233],26,4096
T a[234:235],26,4608
T a[236:237],27,4096
T a[238:239],27,4608
s_waitcnt vmcnt(0) lgkmcnt(0)
s_barrier
v_add_u32 v26,16384,v20
v_add_u32 v27,16384,v21
T a[240:241],26,0
T a[242:243],26,512
T a[244:245],27,0
T a[246:247],27,512
T a[248:249],26,4096
T a[250:251],26,4608
T a[252:253],27,4096
T a[254:255],27,4608
L 0
s_barrier
v_mov_b32 v4,s53
s_mov_b32 s82,0
s_mov_b32 s80,0
s_mov_b32 s54,s45
s_mul_i32 s57,s44,5120
s_mul_i32 s55,s45,40960
s_add_u32 s55,s55,s57
s_add_u32 s55,s55,17825792
s_mul_i32 s57,s44,4096
s_mul_i32 s60,s46,40960
s_mul_i32 s61,s46,1024
s_mul_i32 s62,s46,32768
s_mul_i32 s63,s46,1024
s_mul_i32 s64,s46,512
s_lshl_b32 s59,s44,8
s_add_u32 s59,s59,131072
s_mul_i32 s58,s45,1024
s_add_u32 s58,s58,109333504
s_mul_i32 s65,s45,32768
s_add_u32 s65,s65,s47
s_mul_i32 s68,s45,1024
s_add_u32 s68,s68,s48
s_mul_i32 s73,s45,512
s_add_u32 s73,s73,106954752
s_mul_i32 s74,s45,32768
s_add_u32 s74,s74,s49
s_mul_i32 s75,s45,1024
s_add_u32 s75,s75,107479040
s_mov_b32 s66,0x7fff0000
s_mov_b32 s67,0x7fff0000
s_mov_b32 s69,0x7fff0000
s_mov_b32 s76,0x7fff0000
s_mov_b32 s77,0x7fff0000
s_mov_b32 s70,0x7fff0000
s_mov_b32 s71,0x7fff0000
s_mov_b32 s72,0x7fff0000
v_mov_b32 v1,v0
v_add_u32 v2,16384,v0
s_mov_b32 s84,16384
s_mov_b32 s86,0x3a800000
s_mov_b32 s87,0x38a7c5ac
s_mov_b32 s88,0xc0135761
s_mov_b32 s89,0x3d372713
s_lshr_b32 s85,s44,1
s_mul_i32 s85,s85,127
v_lshlrev_b32 v8,22,v0
v_and_b32 v8,0x80000000,v8
v_or_b32 v8,v8,v5
v_add_u32 v2,32768,v0
s_lshl_b32 s84,s57,1
s_add_u32 s84,s84,32768
s_getpc_b64 s[40:41]
Lg2_pc_%=:
s_lshl_b32 s78,s44,13
v_lshlrev_b32 v106,3,v0
v_add_u32 v106,s78,v106
v_min_u32 v106,Lcode_g12_e-Lg2_pc_%=-4,v106
s_mov_b32 m0,163584
s_nop 0
global_load_lds_dword v106,s[40:41]
s_and_b32 s79,s54,127
s_cmp_eq_u32 s79,0
s_cselect_b32 s94,0x3300,0
s_cmp_eq_u32 s79,127
s_cselect_b32 s94,0xcc00,s94
s_add_u32 m0,s59,4096
s_nop 0
buffer_load_dword v8,s[36:39],s58 offen offset:0 sc1 lds
buffer_store_dword v4,v3,s[36:39],s71 offen offset:0 sc0 sc1
s_add_u32 m0,s59,5120
s_nop 0
buffer_load_dword v8,s[36:39],s58 offen offset:0 sc1 lds
buffer_store_dword v4,v3,s[36:39],s71 offen offset:0 sc0 sc1
s_mov_b32 s83,0
Lg2_poll_%=:
s_add_u32 s78,s83,2
s_and_b32 s78,s78,3
s_lshl_b32 s78,s78,10
s_add_u32 s78,s78,4096
s_add_u32 m0,s59,s78
s_nop 0
buffer_load_dword v8,s[36:39],s58 offen offset:0 sc1 lds
s_and_b32 s78,s83,3
s_lshl_b32 s78,s78,10
v_add_u32 v106,s78,v6
s_waitcnt vmcnt(4)
s_barrier
ds_read_b32 v7,v106 offset:4096
buffer_store_dword v4,v3,s[36:39],s70 offen offset:0 sc0 sc1
s_mov_b32 s70,s71
s_mov_b32 s71,s72
s_mov_b32 s72,0x7fff0000
s_add_u32 s83,s83,1
s_add_u32 s82,s82,1
L 0
v_cmp_eq_u32 vcc,s53,v7
s_nop 0
s_or_b64 vcc,vcc,s[94:95]
s_cmp_eq_u32 vcc_lo,-1
s_cbranch_scc1 Lg2_go_%=
s_cmp_lt_u32 s82,20000
s_cbranch_scc1 Lg2_poll_%=
Lg2_go_%=:
s_and_b32 s78,s54,127
s_cmp_eq_u32 s78,s85
s_cselect_b64 s[40:41],-1,0
v_cndmask_b32 v17,v15,v16,s[40:41]
s_add_u32 m0,s57,0
s_add_u32 s56,s55,0
D 17,56,0
s_add_u32 s56,s55,256
D 17,56,1024
s_add_u32 s56,s55,512
D 17,56,2048
s_add_u32 s56,s55,768
D 17,56,3072
s_add_u32 s58,s58,s61
s_add_u32 m0,s59,2048
s_nop 0
buffer_load_dword v8,s[36:39],s58 offen offset:0 sc1 lds
v_cndmask_b32 v17,v10,v9,s[40:41]
s_add_u32 m0,s57,16384
s_add_u32 s56,s55,20480
D 17,56,0
s_add_u32 s56,s55,20736
D 17,56,1024
s_add_u32 s56,s55,20992
D 17,56,2048
s_add_u32 s56,s55,21248
D 17,56,3072
s_add_u32 s55,s55,s60
s_sub_u32 s57,s84,s57
s_add_u32 s54,s54,s46
s_and_b32 s79,s54,127
s_cmp_eq_u32 s79,0
s_cselect_b32 s94,0x3300,0
s_cmp_eq_u32 s79,127
s_cselect_b32 s94,0xcc00,s94
s_waitcnt vmcnt(4)
s_barrier
buffer_store_dword v4,v3,s[36:39],s70 offen offset:0 sc0 sc1
s_min_u32 s71,s71,s72
buffer_store_dword v4,v3,s[36:39],s71 offen offset:0 sc0 sc1
s_mov_b32 s70,0x7fff0000
s_mov_b32 s71,0x7fff0000
s_mov_b32 s72,0x7fff0000
s_cmp_eq_u32 s80,1
buffer_store_dword v4,v3,s[36:39],s71 offen offset:0 sc0 sc1
s_cbranch_scc1 Lg2_st1_%=
s_branch Lg2_st0_%=
Lg2_gox_%=:
s_add_u32 s58,s58,s61
s_add_u32 m0,s59,2048
s_nop 0
buffer_load_dword v8,s[36:39],s58 offen offset:0 sc1 lds
s_and_b32 s79,s54,127
s_cmp_eq_u32 s79,0
s_cselect_b32 s94,0x3300,0
s_cmp_eq_u32 s79,127
s_cselect_b32 s94,0xcc00,s94
s_waitcnt vmcnt(0)
s_barrier
buffer_store_dword v4,v3,s[36:39],s70 offen offset:0 sc0 sc1
s_min_u32 s71,s71,s72
buffer_store_dword v4,v3,s[36:39],s71 offen offset:0 sc0 sc1
s_mov_b32 s70,0x7fff0000
s_mov_b32 s71,0x7fff0000
s_mov_b32 s72,0x7fff0000
s_cmp_eq_u32 s80,1
s_cbranch_scc1 Lg2_st1_%=
Lg2_st0_%=:
ds_read_b32 v7,v6 offset:2048
R 34,1,0
R 38,1,1024
R 42,1,2048
R 46,1,3072
R 50,1,4096
L 5
v_cmp_eq_u32 vcc,s53,v7
s_nop 0
s_or_b64 vcc,vcc,s[94:95]
L 3
s_cmp_eq_u32 vcc_lo,-1
s_cbranch_scc1 Lg2_F0_%=
s_branch Lg2_L0_%=
Lg2_st1_%=:
ds_read_b32 v7,v6 offset:2048
R 34,2,0
R 38,2,1024
R 42,2,2048
R 46,2,3072
R 50,2,4096
L 5
v_cmp_eq_u32 vcc,s53,v7
s_nop 0
s_or_b64 vcc,vcc,s[94:95]
L 3
s_cmp_eq_u32 vcc_lo,-1
s_cbranch_scc1 Lg2_F1_%=
s_branch Lg2_L1_%=
Lg2_F0_%=:
v_mfma_f32_16x16x32_bf16 v[22:25],a[4:7],v[34:37],0
s_add_u32 s58,s58,s61
s_add_u32 m0,s59,0
s_and_b32 s78,s54,127
R 54,1,5120
M 22,a[12:15],38
s_cmp_eq_u32 s78,s85
s_cselect_b64 s[40:41],-1,0
s_add_u32 s56,s55,0
buffer_load_dword v8,s[36:39],s58 offen offset:0 sc1 lds
v_mfma_f32_16x16x32_bf16 v[18:21],a[0:3],v[34:37],0
s_add_u32 m0,s57,0
R 58,1,6144
v_cndmask_b32 v17,v15,v16,s[40:41]
M 18,a[8:11],38
L 3
D 17,56,0
M 18,a[16:19],42
s_add_u32 s56,s55,256
)"
R"(R 62,1,7168
v_lshlrev_b32 v106,16,v126
M 18,a[24:27],46
s_add_u32 s54,s54,s46
v_and_b32 v107,0xffff0000,v126
v_lshlrev_b32 v108,16,v127
M 22,a[20:23],42
s_and_b32 s79,s54,127
R 66,1,8192
v_and_b32 v109,0xffff0000,v127
M 22,a[28:31],46
L 3
D 17,56,1024
M 22,a[36:39],50
s_add_u32 s56,s55,512
R 70,1,9216
v_lshlrev_b32 v110,16,v128
M 22,a[44:47],54
s_cmp_eq_u32 s79,0
v_and_b32 v111,0xffff0000,v128
v_lshlrev_b32 v112,16,v129
M 18,a[32:35],50
s_cselect_b32 s94,0x3300,0
R 74,1,10240
v_and_b32 v113,0xffff0000,v129
M 18,a[40:43],54
L 3
D 17,56,2048
M 18,a[48:51],58
s_add_u32 s56,s55,768
R 78,1,11264
v_fma_f32 v106,v106,v103,v102
M 18,a[56:59],62
s_cmp_eq_u32 s79,127
D 17,56,3072
M 22,a[52:55],58
s_cselect_b32 s94,0xcc00,s94
R 82,1,12288
v_fma_f32 v107,v107,v103,v102
M 22,a[60:63],62
L 3
v_fma_f32 v108,v108,v103,v102
v_fma_f32 v109,v109,v103,v102
M 22,a[68:71],66
s_add_u32 m0,s57,16384
R 86,1,13312
v_fma_f32 v110,v110,v103,v102
M 22,a[76:79],70
s_add_u32 s56,s55,20480
buffer_load_dwordx4 v[122:125],v0,s[36:39],s74 offen offset:0 sc1
M 18,a[64:67],66
s_mov_b32 s66,s65
R 90,1,14336
v_fma_f32 v111,v111,v103,v102
M 18,a[72:75],70
L 3
buffer_load_dwordx2 v[98:99],v11,s[36:39],s73 offen offset:256 sc1
M 18,a[80:83],74
s_waitcnt vmcnt(10)
R 94,1,15360
v_fma_f32 v112,v112,v103,v102
M 18,a[88:91],78
s_barrier
v_fma_f32 v113,v113,v103,v102
v_add_f32 v26,v26,v106
M 22,a[84:87],74
s_mov_b32 s76,s75
R 34,1,16384
v_add_f32 v27,v27,v107
M 22,a[92:95],78
L 3
v_add_f32 v28,v28,v108
v_add_f32 v29,v29,v109
M 22,a[100:103],82
s_add_u32 s73,s73,s64
R 38,1,17408
v_add_f32 v30,v30,v110
M 22,a[108:111],86
s_add_u32 s74,s74,s62
v_cndmask_b32 v17,v10,v9,s[40:41]
v_add_f32 v31,v31,v111
M 18,a[96:99],82
s_add_u32 s65,s65,s62
R 42,1,18432
v_add_f32 v32,v32,v112
M 18,a[104:107],86
L 3
D 17,56,0
M 18,a[112:115],90
s_add_u32 s56,s55,20736
R 46,1,19456
v_add_f32 v33,v33,v113
M 18,a[120:123],94
s_add_u32 s75,s75,s63
v_cvt_pk_bf16_f32 v114,v26,v27
v_cvt_pk_bf16_f32 v115,v28,v29
M 22,a[116:119],90
s_sub_u32 s57,s84,s57
R 50,1,20480
v_fma_f32 v107,v26,v26,s87
M 22,a[124:127],94
L 3
v_add_f32 v106,v26,v27
v_fma_f32 v107,v27,v27,v107
M 22,a[132:135],34
R 54,1,21504
v_add_f32 v106,v106,v28
M 22,a[140:143],38
D 17,56,1024
M 18,a[128:131],34
s_add_u32 s56,s55,20992
R 58,1,22528
v_fma_f32 v107,v28,v28,v107
M 18,a[136:139],38
L 3
v_add_f32 v106,v106,v29
v_fma_f32 v107,v29,v29,v107
M 18,a[144:147],42
R 62,1,23552
v_add_f32 v106,v106,v30
M 18,a[152:155],46
v_fma_f32 v107,v30,v30,v107
v_add_f32 v106,v106,v31
M 22,a[148:151],42
R 66,1,24576
v_fma_f32 v107,v31,v31,v107
M 22,a[156:159],46
L 3
v_add_f32 v106,v106,v32
v_fma_f32 v107,v32,v32,v107
M 22,a[164:167],50
R 70,1,25600
v_add_f32 v106,v106,v33
M 22,a[172:175],54
v_fma_f32 v107,v33,v33,v107
v_cvt_pk_bf16_f32 v116,v30,v31
M 18,a[160:163],50
R 74,1,26624
v_cvt_pk_bf16_f32 v117,v32,v33
M 18,a[168:171],54
L 3
v_permlane16_swap_b32 v106,v107
v_add_f32 v106,v106,v107
v_mov_b32 v107,v106
M 18,a[176:179],58
R 78,1,27648
M 18,a[184:187],62
v_permlane32_swap_b32 v106,v107
v_add_f32 v106,v106,v107
M 22,a[180:183],58
R 82,1,28672
ds_write_b32 v12,v106 offset:2048
M 22,a[188:191],62
L 4
D 17,56,2048
M 22,a[196:199],66
s_add_u32 s56,s55,21248
s_add_u32 s55,s55,s60
R 86,1,29696
M 22,a[204:207],70
D 17,56,3072
M 18,a[192:195],66
R 90,1,30720
M 18,a[200:203],70
L 4
L 2
buffer_store_dwordx4 v[114:117],v0,s[36:39],s67 offen offset:0 sc0 sc1
M 18,a[208:211],74
s_waitcnt vmcnt(7)
R 94,1,31744
M 18,a[216:219],78
s_barrier
R 118,13,2048
ds_read_b32 v7,v6 offset:0
M 22,a[212:215],74
R 34,2,0
M 22,a[220:223],78
L 5
buffer_store_dword v4,v3,s[36:39],s70 offen offset:0 sc0 sc1
M 22,a[228:231],82
s_mov_b32 s70,s69
s_mov_b32 s69,s68
R 38,2,1024
M 22,a[236:239],86
s_add_u32 s68,s68,s63
M 18,a[224:227],82
R 42,2,2048
M 18,a[232:235],86
L 5
L 4
L 3
M 18,a[240:243],90
R 46,2,3072
v_add_f32 v118,v118,v119
M 18,a[248:251],94
v_cmp_eq_u32 vcc,s53,v7
v_add_f32 v120,v120,v121
M 22,a[244:247],90
s_or_b64 vcc,vcc,s[94:95]
s_cmp_eq_u32 vcc_lo,-1
R 50,2,4096
v_add_f32 v118,v118,v120
v_mul_f32 v118,s86,v118
M 22,a[252:255],94
s_waitcnt vmcnt(6) lgkmcnt(3)
buffer_store_dword v118,v14,s[36:39],s77 offen offset:0 sc0 sc1
s_cbranch_scc0 Lg2_L1_%=
Lg2_F1_%=:
v_mfma_f32_16x16x32_bf16 v[30:33],a[4:7],v[34:37],0
s_add_u32 s58,s58,s61
s_add_u32 m0,s59,1024
s_and_b32 s78,s54,127
R 54,2,5120
M 30,a[12:15],38
s_cmp_eq_u32 s78,s85
s_cselect_b64 s[40:41],-1,0
s_add_u32 s56,s55,0
buffer_load_dword v8,s[36:39],s58 offen offset:0 sc1 lds
v_mfma_f32_16x16x32_bf16 v[26:29],a[0:3],v[34:37],0
s_add_u32 m0,s57,0
R 58,2,6144
v_cndmask_b32 v17,v15,v16,s[40:41]
M 26,a[8:11],38
L 3
D 17,56,0
M 26,a[16:19],42
s_add_u32 s56,s55,256
R 62,2,7168
v_lshlrev_b32 v106,16,v122
M 26,a[24:27],46
s_add_u32 s54,s54,s46
v_and_b32 v107,0xffff0000,v122
v_lshlrev_b32 v108,16,v123
M 30,a[20:23],42
s_and_b32 s79,s54,127
R 66,2,8192
v_and_b32 v109,0xffff0000,v123
M 30,a[28:31],46
L 3
D 17,56,1024
M 30,a[36:39],50
s_add_u32 s56,s55,512
R 70,2,9216
v_lshlrev_b32 v110,16,v124
M 30,a[44:47],54
s_cmp_eq_u32 s79,0
v_and_b32 v111,0xffff0000,v124
v_lshlrev_b32 v112,16,v125
M 26,a[32:35],50
s_cselect_b32 s94,0x3300,0
R 74,2,10240
v_and_b32 v113,0xffff0000,v125
M 26,a[40:43],54
L 3
D 17,56,2048
M 26,a[48:51],58
s_add_u32 s56,s55,768
R 78,2,11264
v_fma_f32 v106,v106,v99,v98
M 26,a[56:59],62
s_cmp_eq_u32 s79,127
D 17,56,3072
M 30,a[52:55],58
s_cselect_b32 s94,0xcc00,s94
R 82,2,12288
v_fma_f32 v107,v107,v99,v98
M 30,a[60:63],62
L 3
v_fma_f32 v108,v108,v99,v98
v_fma_f32 v109,v109,v99,v98
M 30,a[68:71],66
s_add_u32 m0,s57,16384
R 86,2,13312
v_fma_f32 v110,v110,v99,v98
M 30,a[76:79],70
s_add_u32 s56,s55,20480
buffer_load_dwordx4 v[126:129],v0,s[36:39],s74 offen offset:0 sc1
M 26,a[64:67],66
s_mov_b32 s67,s65
R 90,2,14336
v_fma_f32 v111,v111,v99,v98
M 26,a[72:75],70
L 3
buffer_load_dwordx2 v[102:103],v11,s[36:39],s73 offen offset:256 sc1
M 26,a[80:83],74
s_waitcnt vmcnt(10)
R 94,2,15360
v_fma_f32 v112,v112,v99,v98
M 26,a[88:91],78
s_barrier
v_fma_f32 v113,v113,v99,v98
v_add_f32 v18,v18,v106
M 30,a[84:87],74
s_mov_b32 s77,s75
R 34,2,16384
v_add_f32 v19,v19,v107
M 30,a[92:95],78
L 3
v_add_f32 v20,v20,v108
v_add_f32 v21,v21,v109
M 30,a[100:103],82
s_add_u32 s73,s73,s64
R 38,2,17408
v_add_f32 v22,v22,v110
M 30,a[108:111],86
s_add_u32 s74,s74,s62
v_cndmask_b32 v17,v10,v9,s[40:41]
v_add_f32 v23,v23,v111
M 26,a[96:99],82
s_add_u32 s65,s65,s62
R 42,2,18432
v_add_f32 v24,v24,v112
M 26,a[104:107],86
L 3
D 17,56,0
M 26,a[112:115],90
s_add_u32 s56,s55,20736
R 46,2,19456
v_add_f32 v25,v25,v113
M 26,a[120:123],94
s_add_u32 s75,s75,s63
v_cvt_pk_bf16_f32 v114,v18,v19
v_cvt_pk_bf16_f32 v115,v20,v21
M 30,a[116:119],90
s_sub_u32 s57,s84,s57
R 50,2,20480
v_fma_f32 v107,v18,v18,s87
M 30,a[124:127],94
L 3
v_add_f32 v106,v18,v19
v_fma_f32 v107,v19,v19,v107
M 30,a[132:135],34
R 54,2,21504
v_add_f32 v106,v106,v20
M 30,a[140:143],38
D 17,56,1024
M 26,a[128:131],34
s_add_u32 s56,s55,20992
R 58,2,22528
v_fma_f32 v107,v20,v20,v107
M 26,a[136:139],38
L 3
v_add_f32 v106,v106,v21
v_fma_f32 v107,v21,v21,v107
M 26,a[144:147],42
R 62,2,23552
v_add_f32 v106,v106,v22
M 26,a[152:155],46
v_fma_f32 v107,v22,v22,v107
v_add_f32 v106,v106,v23
M 30,a[148:151],42
R 66,2,24576
v_fma_f32 v107,v23,v23,v107
M 30,a[156:159],46
L 3
v_add_f32 v106,v106,v24
v_fma_f32 v107,v24,v24,v107
M 30,a[164:167],50
R 70,2,25600
v_add_f32 v106,v106,v25
M 30,a[172:175],54
v_fma_f32 v107,v25,v25,v107
v_cvt_pk_bf16_f32 v116,v22,v23
M 26,a[160:163],50
R 74,2,26624
v_cvt_pk_bf16_f32 v117,v24,v25
M 26,a[168:171],54
L 3
v_permlane16_swap_b32 v106,v107
v_add_f32 v106,v106,v107
v_mov_b32 v107,v106
M 26,a[176:179],58
R 78,2,27648
M 26,a[184:187],62
v_permlane32_swap_b32 v106,v107
v_add_f32 v106,v106,v107
M 30,a[180:183],58
R 82,2,28672
ds_write_b32 v12,v106 offset:0
M 30,a[188:191],62
L 4
D 17,56,2048
M 30,a[196:199],66
s_add_u32 s56,s55,21248
s_add_u32 s55,s55,s60
R 86,2,29696
M 30,a[204:207],70
D 17,56,3072
M 26,a[192:195],66
R 90,2,30720
M 26,a[200:203],70
L 4
L 2
buffer_store_dwordx4 v[114:117],v0,s[36:39],s66 offen offset:0 sc0 sc1
M 26,a[208:211],74
s_waitcnt vmcnt(7)
R 94,2,31744
M 26,a[216:219],78
s_barrier
R 118,13,0
ds_read_b32 v7,v6 offset:1024
M 30,a[212:215],74
R 34,1,0
M 30,a[220:223],78
L 5
buffer_store_dword v4,v3,s[36:39],s70 offen offset:0 sc0 sc1
M 30,a[228:231],82
s_mov_b32 s70,s69
s_mov_b32 s69,s68
R 38,1,1024
M 30,a[236:239],86
s_add_u32 s68,s68,s63
M 26,a[224:227],82
R 42,1,2048
M 26,a[232:235],86
L 5
L 4
L 3
M 26,a[240:243],90
R 46,1,3072
v_add_f32 v118,v118,v119
M 26,a[248:251],94
v_cmp_eq_u32 vcc,s53,v7
v_add_f32 v120,v120,v121
M 30,a[244:247],90
s_or_b64 vcc,vcc,s[94:95]
s_cmp_eq_u32 vcc_lo,-1
R 50,1,4096
v_add_f32 v118,v118,v120
v_mul_f32 v118,s86,v118
M 30,a[252:255],94
s_waitcnt vmcnt(6) lgkmcnt(3)
buffer_store_dword v118,v14,s[36:39],s76 offen offset:0 sc0 sc1
s_cbranch_scc1 Lg2_F0_%=
Lg2_L0_%=:
v_mfma_f32_16x16x32_bf16 v[22:25],a[4:7],v[34:37],0
s_add_u32 m0,s59,3072
R 54,1,5120
v_lshlrev_b32 v106,16,v126
M 22,a[12:15],38
s_mov_b32 s66,s65
buffer_load_dword v8,s[36:39],s58 offen offset:0 sc1 lds
v_mfma_f32_16x16x32_bf16 v[18:21],a[0:3],v[34:37],0
s_mov_b32 s76,s75
R 58,1,6144
v_and_b32 v107,0xffff0000,v126
M 18,a[8:11],38
L 3
v_lshlrev_b32 v108,16,v127
v_and_b32 v109,0xffff0000,v127
M 18,a[16:19],42
s_add_u32 s65,s65,s62
R 62,1,7168
v_lshlrev_b32 v110,16,v128
M 18,a[24:27],46
s_add_u32 s75,s75,s63
v_and_b32 v111,0xffff0000,v128
v_lshlrev_b32 v112,16,v129
M 22,a[20:23],42
R 66,1,8192
v_and_b32 v113,0xffff0000,v129
M 22,a[28:31],46
L 3
v_fma_f32 v106,v106,v103,v102
v_fma_f32 v107,v107,v103,v102
M 22,a[36:39],50
R 70,1,9216
v_fma_f32 v108,v108,v103,v102
M 22,a[44:47],54
v_fma_f32 v109,v109,v103,v102
v_fma_f32 v110,v110,v103,v102
M 18,a[32:35],50
R 74,1,10240
v_fma_f32 v111,v111,v103,v102
M 18,a[40:43],54
L 3
v_fma_f32 v112,v112,v103,v102
v_fma_f32 v113,v113,v103,v102
M 18,a[48:51],58
R 78,1,11264
v_add_f32 v26,v26,v106
M 18,a[56:59],62
v_add_f32 v27,v27,v107
v_add_f32 v28,v28,v108
v_add_f32 v29,v29,v109
M 22,a[52:55],58
R 82,1,12288
v_add_f32 v30,v30,v110
M 22,a[60:63],62
L 3
v_add_f32 v31,v31,v111
v_add_f32 v32,v32,v112
M 22,a[68:71],66
R 86,1,13312
v_add_f32 v33,v33,v113
M 22,a[76:79],70
buffer_load_dwordx4 v[122:125],v0,s[36:39],s74 offen offset:0 sc1
M 18,a[64:67],66
s_add_u32 s74,s74,s62
R 90,1,14336
)"
R"(v_cvt_pk_bf16_f32 v114,v26,v27
M 18,a[72:75],70
L 3
buffer_load_dwordx2 v[98:99],v11,s[36:39],s73 offen offset:256 sc1
M 18,a[80:83],74
s_waitcnt vmcnt(2)
s_add_u32 s73,s73,s64
R 94,1,15360
M 18,a[88:91],78
s_barrier
ds_read_b32 v7,v6 offset:3072
v_cvt_pk_bf16_f32 v115,v28,v29
M 22,a[84:87],74
R 34,1,16384
v_cvt_pk_bf16_f32 v116,v30,v31
M 22,a[92:95],78
L 4
buffer_store_dword v4,v3,s[36:39],s70 offen offset:0 sc0 sc1
M 22,a[100:103],82
s_mov_b32 s70,0x7fff0000
R 38,1,17408
v_cvt_pk_bf16_f32 v117,v32,v33
M 22,a[108:111],86
v_add_f32 v106,v26,v27
v_fma_f32 v107,v26,v26,s87
M 18,a[96:99],82
R 42,1,18432
v_fma_f32 v107,v27,v27,v107
v_add_f32 v106,v106,v28
M 18,a[104:107],86
L 4
L 3
v_cmp_eq_u32 vcc,s53,v7
M 18,a[112:115],90
s_or_b64 vcc,vcc,s[94:95]
R 46,1,19456
v_fma_f32 v107,v28,v28,v107
M 18,a[120:123],94
v_add_f32 v106,v106,v29
v_fma_f32 v107,v29,v29,v107
M 22,a[116:119],90
s_cmp_eq_u32 vcc_lo,-1
R 50,1,20480
v_add_f32 v106,v106,v30
s_cbranch_scc0 Lg2_X0_%=
M 22,a[124:127],94
L 3
s_and_b32 s78,s54,127
s_cmp_eq_u32 s78,s85
M 22,a[132:135],34
s_cselect_b64 s[40:41],-1,0
s_add_u32 m0,s57,0
s_add_u32 s56,s55,0
R 54,1,21504
M 22,a[140:143],38
s_add_u32 s58,s58,s61
v_cndmask_b32 v17,v15,v16,s[40:41]
v_fma_f32 v107,v30,v30,v107
M 18,a[128:131],34
s_add_u32 s54,s54,s46
R 58,1,22528
v_add_f32 v106,v106,v31
M 18,a[136:139],38
L 3
D 17,56,0
M 18,a[144:147],42
s_add_u32 s56,s55,256
R 62,1,23552
v_fma_f32 v107,v31,v31,v107
M 18,a[152:155],46
s_and_b32 s79,s54,127
D 17,56,1024
M 22,a[148:151],42
s_add_u32 s56,s55,512
R 66,1,24576
v_add_f32 v106,v106,v32
M 22,a[156:159],46
L 3
v_fma_f32 v107,v32,v32,v107
v_add_f32 v106,v106,v33
M 22,a[164:167],50
s_cmp_eq_u32 s79,0
R 70,1,25600
v_fma_f32 v107,v33,v33,v107
M 22,a[172:175],54
s_cselect_b32 s94,0x3300,0
D 17,56,2048
M 18,a[160:163],50
s_add_u32 s56,s55,768
R 74,1,26624
v_permlane16_swap_b32 v106,v107
M 18,a[168:171],54
L 3
v_add_f32 v106,v106,v107
v_mov_b32 v107,v106
M 18,a[176:179],58
s_cmp_eq_u32 s79,127
s_cselect_b32 s94,0xcc00,s94
R 78,1,27648
M 18,a[184:187],62
v_permlane32_swap_b32 v106,v107
v_add_f32 v106,v106,v107
M 22,a[180:183],58
R 82,1,28672
ds_write_b32 v12,v106 offset:2048
M 22,a[188:191],62
L 4
D 17,56,3072
M 22,a[196:199],66
s_add_u32 m0,s59,0
s_add_u32 s56,s55,20480
R 86,1,29696
M 22,a[204:207],70
buffer_load_dword v8,s[36:39],s58 offen offset:0 sc1 lds
M 18,a[192:195],66
s_add_u32 m0,s57,16384
R 90,1,30720
v_cndmask_b32 v17,v10,v9,s[40:41]
M 18,a[200:203],70
L 4
L 2
D 17,56,0
M 18,a[208:211],74
s_waitcnt vmcnt(1)
s_add_u32 s56,s55,20736
R 94,1,31744
M 18,a[216:219],78
s_barrier
R 118,13,2048
ds_read_b32 v7,v6 offset:0
M 22,a[212:215],74
s_sub_u32 s57,s84,s57
R 34,2,0
M 22,a[220:223],78
L 5
buffer_store_dword v4,v3,s[36:39],s70 offen offset:0 sc0 sc1
M 22,a[228:231],82
s_mov_b32 s70,s69
s_mov_b32 s69,s68
R 38,2,1024
M 22,a[236:239],86
s_add_u32 s68,s68,s63
D 17,56,1024
M 18,a[224:227],82
s_add_u32 s56,s55,20992
R 42,2,2048
M 18,a[232:235],86
L 5
D 17,56,2048
M 18,a[240:243],90
s_add_u32 s56,s55,21248
L 4
L 3
R 46,2,3072
M 18,a[248:251],94
s_add_u32 s55,s55,s60
v_cmp_eq_u32 vcc,s53,v7
v_add_f32 v118,v118,v119
M 22,a[244:247],90
s_or_b64 vcc,vcc,s[94:95]
s_cmp_eq_u32 vcc_lo,-1
R 50,2,4096
D 17,56,3072
buffer_store_dwordx4 v[114:117],v0,s[36:39],s67 offen offset:0 sc0 sc1
v_add_f32 v120,v120,v121
v_add_f32 v118,v118,v120
v_mul_f32 v118,s86,v118
M 22,a[252:255],94
L 3
buffer_store_dword v118,v14,s[36:39],s77 offen offset:0 sc0 sc1
buffer_store_dword v4,v3,s[36:39],s71 offen offset:0 sc0 sc1
s_cbranch_scc1 Lg2_F1_%=
s_branch Lg2_L1_%=
Lg2_X0_%=:
M 22,a[124:127],94
L 3
s_add_u32 m0,s59,0
buffer_store_dwordx4 v[114:117],v0,s[36:39],s67 offen offset:0 sc0 sc1
M 22,a[132:135],34
R 54,1,21504
v_fma_f32 v107,v30,v30,v107
M 22,a[140:143],38
v_add_f32 v106,v106,v31
v_fma_f32 v107,v31,v31,v107
M 18,a[128:131],34
R 58,1,22528
v_add_f32 v106,v106,v32
M 18,a[136:139],38
L 3
buffer_load_dword v8,s[36:39],s58 offen offset:0 sc1 lds
M 18,a[144:147],42
R 62,1,23552
v_fma_f32 v107,v32,v32,v107
M 18,a[152:155],46
v_add_f32 v106,v106,v33
v_fma_f32 v107,v33,v33,v107
M 22,a[148:151],42
R 66,1,24576
M 22,a[156:159],46
L 3
v_permlane16_swap_b32 v106,v107
M 22,a[164:167],50
R 70,1,25600
v_add_f32 v106,v106,v107
M 22,a[172:175],54
v_mov_b32 v107,v106
M 18,a[160:163],50
R 74,1,26624
M 18,a[168:171],54
L 3
v_permlane32_swap_b32 v106,v107
M 18,a[176:179],58
R 78,1,27648
v_add_f32 v106,v106,v107
M 18,a[184:187],62
ds_write_b32 v12,v106 offset:2048
M 22,a[180:183],58
R 82,1,28672
M 22,a[188:191],62
L 4
M 22,a[196:199],66
R 86,1,29696
M 22,a[204:207],70
M 18,a[192:195],66
R 90,1,30720
M 18,a[200:203],70
L 4
L 3
M 18,a[208:211],74
s_waitcnt vmcnt(0)
R 94,1,31744
M 18,a[216:219],78
s_barrier
R 118,13,2048
ds_read_b32 v7,v6 offset:0
M 22,a[212:215],74
buffer_store_dword v4,v3,s[36:39],s70 offen offset:0 sc0 sc1
M 22,a[220:223],78
L 4
s_mov_b32 s70,s69
s_mov_b32 s69,s68
M 22,a[228:231],82
s_add_u32 s68,s68,s63
M 22,a[236:239],86
M 18,a[224:227],82
M 18,a[232:235],86
L 2
L 1
L 0
M 18,a[240:243],90
v_add_f32 v118,v118,v119
v_add_f32 v120,v120,v121
M 18,a[248:251],94
v_add_f32 v118,v118,v120
v_mul_f32 v118,s86,v118
M 22,a[244:247],90
v_cmp_eq_u32 vcc,s53,v7
M 22,a[252:255],94
s_nop 0
s_or_b64 vcc,vcc,s[94:95]
buffer_store_dword v118,v14,s[36:39],s77 offen offset:0 sc0 sc1
s_mov_b32 s80,1
s_cmp_eq_u32 vcc_lo,-1
s_cbranch_scc0 Lg2_xn0_%=
s_and_b32 s78,s54,127
s_cmp_eq_u32 s78,s85
s_cselect_b64 s[40:41],-1,0
v_cndmask_b32 v17,v15,v16,s[40:41]
s_add_u32 m0,s57,0
s_add_u32 s56,s55,0
D 17,56,0
v_lshlrev_b32 v106,16,v122
v_and_b32 v107,0xffff0000,v122
v_lshlrev_b32 v108,16,v123
v_and_b32 v109,0xffff0000,v123
v_lshlrev_b32 v110,16,v124
s_add_u32 s56,s55,256
D 17,56,1024
v_and_b32 v111,0xffff0000,v124
v_lshlrev_b32 v112,16,v125
v_and_b32 v113,0xffff0000,v125
v_fma_f32 v106,v106,v99,v98
v_fma_f32 v107,v107,v99,v98
s_add_u32 s56,s55,512
D 17,56,2048
v_fma_f32 v108,v108,v99,v98
v_fma_f32 v109,v109,v99,v98
v_fma_f32 v110,v110,v99,v98
v_fma_f32 v111,v111,v99,v98
v_fma_f32 v112,v112,v99,v98
s_add_u32 s56,s55,768
D 17,56,3072
v_fma_f32 v113,v113,v99,v98
v_add_f32 v18,v18,v106
v_add_f32 v19,v19,v107
v_add_f32 v20,v20,v108
v_add_f32 v21,v21,v109
v_cndmask_b32 v17,v10,v9,s[40:41]
s_add_u32 m0,s57,16384
s_add_u32 s56,s55,20480
D 17,56,0
v_add_f32 v22,v22,v110
v_add_f32 v23,v23,v111
v_add_f32 v24,v24,v112
v_add_f32 v25,v25,v113
v_cvt_pk_bf16_f32 v114,v18,v19
s_add_u32 s56,s55,20736
D 17,56,1024
v_cvt_pk_bf16_f32 v115,v20,v21
v_cvt_pk_bf16_f32 v116,v22,v23
v_cvt_pk_bf16_f32 v117,v24,v25
s_add_u32 s56,s55,20992
D 17,56,2048
s_add_u32 s56,s55,21248
D 17,56,3072
buffer_store_dwordx4 v[114:117],v0,s[36:39],s66 offen offset:0 sc0 sc1
v_add_f32 v106,v18,v19
v_fma_f32 v107,v18,v18,s87
s_add_u32 s55,s55,s60
s_sub_u32 s57,s84,s57
s_add_u32 s54,s54,s46
v_fma_f32 v107,v19,v19,v107
v_add_f32 v106,v106,v20
v_fma_f32 v107,v20,v20,v107
v_add_f32 v106,v106,v21
v_fma_f32 v107,v21,v21,v107
v_add_f32 v106,v106,v22
v_fma_f32 v107,v22,v22,v107
v_add_f32 v106,v106,v23
v_fma_f32 v107,v23,v23,v107
v_add_f32 v106,v106,v24
v_fma_f32 v107,v24,v24,v107
v_add_f32 v106,v106,v25
v_fma_f32 v107,v25,v25,v107
s_nop 1
v_permlane16_swap_b32 v106,v107
v_add_f32 v106,v106,v107
v_mov_b32 v107,v106
s_nop 1
v_permlane32_swap_b32 v106,v107
v_add_f32 v106,v106,v107
ds_write_b32 v12,v106 offset:0
L 0
s_barrier
R 118,13,0
L 0
v_add_f32 v118,v118,v119
v_add_f32 v120,v120,v121
v_add_f32 v118,v118,v120
v_mul_f32 v118,s86,v118
buffer_store_dword v118,v14,s[36:39],s76 offen offset:0 sc0 sc1
s_mov_b32 s72,s69
s_mov_b32 s69,0x7fff0000
s_mov_b32 s66,0x7fff0000
s_mov_b32 s76,0x7fff0000
s_branch Lg2_gox_%=
Lg2_xn0_%=:
s_cmp_ge_u32 s54,256
s_cbranch_scc1 Lg2_fin0_%=
s_add_u32 m0,s59,4096
s_nop 0
buffer_load_dword v8,s[36:39],s58 offen offset:0 sc1 lds
buffer_store_dword v4,v3,s[36:39],s71 offen offset:0 sc0 sc1
s_add_u32 m0,s59,5120
s_nop 0
buffer_load_dword v8,s[36:39],s58 offen offset:0 sc1 lds
buffer_store_dword v4,v3,s[36:39],s71 offen offset:0 sc0 sc1
s_mov_b32 s83,0
v_lshlrev_b32 v106,16,v122
v_and_b32 v107,0xffff0000,v122
v_lshlrev_b32 v108,16,v123
v_and_b32 v109,0xffff0000,v123
v_lshlrev_b32 v110,16,v124
v_and_b32 v111,0xffff0000,v124
v_lshlrev_b32 v112,16,v125
v_and_b32 v113,0xffff0000,v125
v_fma_f32 v106,v106,v99,v98
v_fma_f32 v107,v107,v99,v98
v_fma_f32 v108,v108,v99,v98
v_fma_f32 v109,v109,v99,v98
v_fma_f32 v110,v110,v99,v98
v_fma_f32 v111,v111,v99,v98
v_fma_f32 v112,v112,v99,v98
v_fma_f32 v113,v113,v99,v98
v_add_f32 v18,v18,v106
v_add_f32 v19,v19,v107
v_add_f32 v20,v20,v108
v_add_f32 v21,v21,v109
v_add_f32 v22,v22,v110
v_add_f32 v23,v23,v111
v_add_f32 v24,v24,v112
v_add_f32 v25,v25,v113
v_cvt_pk_bf16_f32 v114,v18,v19
v_cvt_pk_bf16_f32 v115,v20,v21
v_cvt_pk_bf16_f32 v116,v22,v23
v_cvt_pk_bf16_f32 v117,v24,v25
buffer_store_dwordx4 v[114:117],v0,s[36:39],s66 offen offset:0 sc0 sc1
v_add_f32 v106,v18,v19
v_fma_f32 v107,v18,v18,s87
v_fma_f32 v107,v19,v19,v107
v_add_f32 v106,v106,v20
v_fma_f32 v107,v20,v20,v107
v_add_f32 v106,v106,v21
v_fma_f32 v107,v21,v21,v107
v_add_f32 v106,v106,v22
v_fma_f32 v107,v22,v22,v107
v_add_f32 v106,v106,v23
v_fma_f32 v107,v23,v23,v107
v_add_f32 v106,v106,v24
v_fma_f32 v107,v24,v24,v107
v_add_f32 v106,v106,v25
v_fma_f32 v107,v25,v25,v107
s_nop 1
v_permlane16_swap_b32 v106,v107
v_add_f32 v106,v106,v107
v_mov_b32 v107,v106
s_nop 1
v_permlane32_swap_b32 v106,v107
v_add_f32 v106,v106,v107
ds_write_b32 v12,v106 offset:0
L 0
s_barrier
R 118,13,0
L 0
v_add_f32 v118,v118,v119
v_add_f32 v120,v120,v121
v_add_f32 v118,v118,v120
v_mul_f32 v118,s86,v118
buffer_store_dword v118,v14,s[36:39],s76 offen offset:0 sc0 sc1
s_mov_b32 s72,s69
s_mov_b32 s69,0x7fff0000
s_mov_b32 s66,0x7fff0000
s_mov_b32 s76,0x7fff0000
s_branch Lg2_poll_%=
Lg2_fin0_%=:
v_lshlrev_b32 v106,16,v122
v_and_b32 v107,0xffff0000,v122
v_lshlrev_b32 v108,16,v123
v_and_b32 v109,0xffff0000,v123
v_lshlrev_b32 v110,16,v124
v_and_b32 v111,0xffff0000,v124
v_lshlrev_b32 v112,16,v125
v_and_b32 v113,0xffff0000,v125
v_fma_f32 v106,v106,v99,v98
v_fma_f32 v107,v107,v99,v98
v_fma_f32 v108,v108,v99,v98
v_fma_f32 v109,v109,v99,v98
v_fma_f32 v110,v110,v99,v98
v_fma_f32 v111,v111,v99,v98
v_fma_f32 v112,v112,v99,v98
v_fma_f32 v113,v113,v99,v98
v_add_f32 v18,v18,v106
v_add_f32 v19,v19,v107
v_add_f32 v20,v20,v108
v_add_f32 v21,v21,v109
v_add_f32 v22,v22,v110
v_add_f32 v23,v23,v111
v_add_f32 v24,v24,v112
v_add_f32 v25,v25,v113
v_cvt_pk_bf16_f32 v114,v18,v19
v_cvt_pk_bf16_f32 v115,v20,v21
v_cvt_pk_bf16_f32 v116,v22,v23
v_cvt_pk_bf16_f32 v117,v24,v25
buffer_store_dwordx4 v[114:117],v0,s[36:39],s66 offen offset:0 sc0 sc1
v_add_f32 v106,v18,v19
v_fma_f32 v107,v18,v18,s87
v_fma_f32 v107,v19,v19,v107
v_add_f32 v106,v106,v20
v_fma_f32 v107,v20,v20,v107
v_add_f32 v106,v106,v21
v_fma_f32 v107,v21,v21,v107
v_add_f32 v106,v106,v22
v_fma_f32 v107,v22,v22,v107
v_add_f32 v106,v106,v23
v_fma_f32 v107,v23,v23,v107
v_add_f32 v106,v106,v24
v_fma_f32 v107,v24,v24,v107
v_add_f32 v106,v106,v25
v_fma_f32 v107,v25,v25,v107
s_nop 1
v_permlane16_swap_b32 v106,v107
v_add_f32 v106,v106,v107
v_mov_b32 v107,v106
s_nop 1
v_permlane32_swap_b32 v106,v107
v_add_f32 v106,v106,v107
ds_write_b32 v12,v106 offset:0
L 0
s_barrier
R 118,13,0
L 0
v_add_f32 v118,v118,v119
v_add_f32 v120,v120,v121
v_add_f32 v118,v118,v120
v_mul_f32 v118,s86,v118
buffer_store_dword v118,v14,s[36:39],s76 offen offset:0 sc0 sc1
s_mov_b32 s72,s69
s_mov_b32 s69,0x7fff0000
s_mov_b32 s66,0x7fff0000
s_mov_b32 s76,0x7fff0000
)"
R"(s_branch Lg2_done_%=
Lg2_L1_%=:
v_mfma_f32_16x16x32_bf16 v[30:33],a[4:7],v[34:37],0
s_add_u32 m0,s59,3072
R 54,2,5120
v_lshlrev_b32 v106,16,v122
M 30,a[12:15],38
s_mov_b32 s67,s65
buffer_load_dword v8,s[36:39],s58 offen offset:0 sc1 lds
v_mfma_f32_16x16x32_bf16 v[26:29],a[0:3],v[34:37],0
s_mov_b32 s77,s75
R 58,2,6144
v_and_b32 v107,0xffff0000,v122
M 26,a[8:11],38
L 3
v_lshlrev_b32 v108,16,v123
v_and_b32 v109,0xffff0000,v123
M 26,a[16:19],42
s_add_u32 s65,s65,s62
R 62,2,7168
v_lshlrev_b32 v110,16,v124
M 26,a[24:27],46
s_add_u32 s75,s75,s63
v_and_b32 v111,0xffff0000,v124
v_lshlrev_b32 v112,16,v125
M 30,a[20:23],42
R 66,2,8192
v_and_b32 v113,0xffff0000,v125
M 30,a[28:31],46
L 3
v_fma_f32 v106,v106,v99,v98
v_fma_f32 v107,v107,v99,v98
M 30,a[36:39],50
R 70,2,9216
v_fma_f32 v108,v108,v99,v98
M 30,a[44:47],54
v_fma_f32 v109,v109,v99,v98
v_fma_f32 v110,v110,v99,v98
M 26,a[32:35],50
R 74,2,10240
v_fma_f32 v111,v111,v99,v98
M 26,a[40:43],54
L 3
v_fma_f32 v112,v112,v99,v98
v_fma_f32 v113,v113,v99,v98
M 26,a[48:51],58
R 78,2,11264
v_add_f32 v18,v18,v106
M 26,a[56:59],62
v_add_f32 v19,v19,v107
v_add_f32 v20,v20,v108
v_add_f32 v21,v21,v109
M 30,a[52:55],58
R 82,2,12288
v_add_f32 v22,v22,v110
M 30,a[60:63],62
L 3
v_add_f32 v23,v23,v111
v_add_f32 v24,v24,v112
M 30,a[68:71],66
R 86,2,13312
v_add_f32 v25,v25,v113
M 30,a[76:79],70
buffer_load_dwordx4 v[126:129],v0,s[36:39],s74 offen offset:0 sc1
M 26,a[64:67],66
s_add_u32 s74,s74,s62
R 90,2,14336
v_cvt_pk_bf16_f32 v114,v18,v19
M 26,a[72:75],70
L 3
buffer_load_dwordx2 v[102:103],v11,s[36:39],s73 offen offset:256 sc1
M 26,a[80:83],74
s_waitcnt vmcnt(2)
s_add_u32 s73,s73,s64
R 94,2,15360
M 26,a[88:91],78
s_barrier
ds_read_b32 v7,v6 offset:3072
v_cvt_pk_bf16_f32 v115,v20,v21
M 30,a[84:87],74
R 34,2,16384
v_cvt_pk_bf16_f32 v116,v22,v23
M 30,a[92:95],78
L 4
buffer_store_dword v4,v3,s[36:39],s70 offen offset:0 sc0 sc1
M 30,a[100:103],82
s_mov_b32 s70,0x7fff0000
R 38,2,17408
v_cvt_pk_bf16_f32 v117,v24,v25
M 30,a[108:111],86
v_add_f32 v106,v18,v19
v_fma_f32 v107,v18,v18,s87
M 26,a[96:99],82
R 42,2,18432
v_fma_f32 v107,v19,v19,v107
v_add_f32 v106,v106,v20
M 26,a[104:107],86
L 4
L 3
v_cmp_eq_u32 vcc,s53,v7
M 26,a[112:115],90
s_or_b64 vcc,vcc,s[94:95]
R 46,2,19456
v_fma_f32 v107,v20,v20,v107
M 26,a[120:123],94
v_add_f32 v106,v106,v21
v_fma_f32 v107,v21,v21,v107
M 30,a[116:119],90
s_cmp_eq_u32 vcc_lo,-1
R 50,2,20480
v_add_f32 v106,v106,v22
s_cbranch_scc0 Lg2_X1_%=
M 30,a[124:127],94
L 3
s_and_b32 s78,s54,127
s_cmp_eq_u32 s78,s85
M 30,a[132:135],34
s_cselect_b64 s[40:41],-1,0
s_add_u32 m0,s57,0
s_add_u32 s56,s55,0
R 54,2,21504
M 30,a[140:143],38
s_add_u32 s58,s58,s61
v_cndmask_b32 v17,v15,v16,s[40:41]
v_fma_f32 v107,v22,v22,v107
M 26,a[128:131],34
s_add_u32 s54,s54,s46
R 58,2,22528
v_add_f32 v106,v106,v23
M 26,a[136:139],38
L 3
D 17,56,0
M 26,a[144:147],42
s_add_u32 s56,s55,256
R 62,2,23552
v_fma_f32 v107,v23,v23,v107
M 26,a[152:155],46
s_and_b32 s79,s54,127
D 17,56,1024
M 30,a[148:151],42
s_add_u32 s56,s55,512
R 66,2,24576
v_add_f32 v106,v106,v24
M 30,a[156:159],46
L 3
v_fma_f32 v107,v24,v24,v107
v_add_f32 v106,v106,v25
M 30,a[164:167],50
s_cmp_eq_u32 s79,0
R 70,2,25600
v_fma_f32 v107,v25,v25,v107
M 30,a[172:175],54
s_cselect_b32 s94,0x3300,0
D 17,56,2048
M 26,a[160:163],50
s_add_u32 s56,s55,768
R 74,2,26624
v_permlane16_swap_b32 v106,v107
M 26,a[168:171],54
L 3
v_add_f32 v106,v106,v107
v_mov_b32 v107,v106
M 26,a[176:179],58
s_cmp_eq_u32 s79,127
s_cselect_b32 s94,0xcc00,s94
R 78,2,27648
M 26,a[184:187],62
v_permlane32_swap_b32 v106,v107
v_add_f32 v106,v106,v107
M 30,a[180:183],58
R 82,2,28672
ds_write_b32 v12,v106 offset:0
M 30,a[188:191],62
L 4
D 17,56,3072
M 30,a[196:199],66
s_add_u32 m0,s59,1024
s_add_u32 s56,s55,20480
R 86,2,29696
M 30,a[204:207],70
buffer_load_dword v8,s[36:39],s58 offen offset:0 sc1 lds
M 26,a[192:195],66
s_add_u32 m0,s57,16384
R 90,2,30720
v_cndmask_b32 v17,v10,v9,s[40:41]
M 26,a[200:203],70
L 4
L 2
D 17,56,0
M 26,a[208:211],74
s_waitcnt vmcnt(1)
s_add_u32 s56,s55,20736
R 94,2,31744
M 26,a[216:219],78
s_barrier
R 118,13,0
ds_read_b32 v7,v6 offset:1024
M 30,a[212:215],74
s_sub_u32 s57,s84,s57
R 34,1,0
M 30,a[220:223],78
L 5
buffer_store_dword v4,v3,s[36:39],s70 offen offset:0 sc0 sc1
M 30,a[228:231],82
s_mov_b32 s70,s69
s_mov_b32 s69,s68
R 38,1,1024
M 30,a[236:239],86
s_add_u32 s68,s68,s63
D 17,56,1024
M 26,a[224:227],82
s_add_u32 s56,s55,20992
R 42,1,2048
M 26,a[232:235],86
L 5
D 17,56,2048
M 26,a[240:243],90
s_add_u32 s56,s55,21248
L 4
L 3
R 46,1,3072
M 26,a[248:251],94
s_add_u32 s55,s55,s60
v_cmp_eq_u32 vcc,s53,v7
v_add_f32 v118,v118,v119
M 30,a[244:247],90
s_or_b64 vcc,vcc,s[94:95]
s_cmp_eq_u32 vcc_lo,-1
R 50,1,4096
D 17,56,3072
buffer_store_dwordx4 v[114:117],v0,s[36:39],s66 offen offset:0 sc0 sc1
v_add_f32 v120,v120,v121
v_add_f32 v118,v118,v120
v_mul_f32 v118,s86,v118
M 30,a[252:255],94
L 3
buffer_store_dword v118,v14,s[36:39],s76 offen offset:0 sc0 sc1
buffer_store_dword v4,v3,s[36:39],s71 offen offset:0 sc0 sc1
s_cbranch_scc1 Lg2_F0_%=
s_branch Lg2_L0_%=
Lg2_X1_%=:
M 30,a[124:127],94
L 3
s_add_u32 m0,s59,1024
buffer_store_dwordx4 v[114:117],v0,s[36:39],s66 offen offset:0 sc0 sc1
M 30,a[132:135],34
R 54,2,21504
v_fma_f32 v107,v22,v22,v107
M 30,a[140:143],38
v_add_f32 v106,v106,v23
v_fma_f32 v107,v23,v23,v107
M 26,a[128:131],34
R 58,2,22528
v_add_f32 v106,v106,v24
M 26,a[136:139],38
L 3
buffer_load_dword v8,s[36:39],s58 offen offset:0 sc1 lds
M 26,a[144:147],42
R 62,2,23552
v_fma_f32 v107,v24,v24,v107
M 26,a[152:155],46
v_add_f32 v106,v106,v25
v_fma_f32 v107,v25,v25,v107
M 30,a[148:151],42
R 66,2,24576
M 30,a[156:159],46
L 3
v_permlane16_swap_b32 v106,v107
M 30,a[164:167],50
R 70,2,25600
v_add_f32 v106,v106,v107
M 30,a[172:175],54
v_mov_b32 v107,v106
M 26,a[160:163],50
R 74,2,26624
M 26,a[168:171],54
L 3
v_permlane32_swap_b32 v106,v107
M 26,a[176:179],58
R 78,2,27648
v_add_f32 v106,v106,v107
M 26,a[184:187],62
ds_write_b32 v12,v106 offset:0
M 30,a[180:183],58
R 82,2,28672
M 30,a[188:191],62
L 4
M 30,a[196:199],66
R 86,2,29696
M 30,a[204:207],70
M 26,a[192:195],66
R 90,2,30720
M 26,a[200:203],70
L 4
L 3
M 26,a[208:211],74
s_waitcnt vmcnt(0)
R 94,2,31744
M 26,a[216:219],78
s_barrier
R 118,13,0
ds_read_b32 v7,v6 offset:1024
M 30,a[212:215],74
buffer_store_dword v4,v3,s[36:39],s70 offen offset:0 sc0 sc1
M 30,a[220:223],78
L 4
s_mov_b32 s70,s69
s_mov_b32 s69,s68
M 30,a[228:231],82
s_add_u32 s68,s68,s63
M 30,a[236:239],86
M 26,a[224:227],82
M 26,a[232:235],86
L 2
L 1
L 0
M 26,a[240:243],90
v_add_f32 v118,v118,v119
v_add_f32 v120,v120,v121
M 26,a[248:251],94
v_add_f32 v118,v118,v120
v_mul_f32 v118,s86,v118
M 30,a[244:247],90
v_cmp_eq_u32 vcc,s53,v7
M 30,a[252:255],94
s_nop 0
s_or_b64 vcc,vcc,s[94:95]
buffer_store_dword v118,v14,s[36:39],s76 offen offset:0 sc0 sc1
s_mov_b32 s80,0
s_cmp_eq_u32 vcc_lo,-1
s_cbranch_scc0 Lg2_xn1_%=
s_and_b32 s78,s54,127
s_cmp_eq_u32 s78,s85
s_cselect_b64 s[40:41],-1,0
v_cndmask_b32 v17,v15,v16,s[40:41]
s_add_u32 m0,s57,0
s_add_u32 s56,s55,0
D 17,56,0
v_lshlrev_b32 v106,16,v126
v_and_b32 v107,0xffff0000,v126
v_lshlrev_b32 v108,16,v127
v_and_b32 v109,0xffff0000,v127
v_lshlrev_b32 v110,16,v128
s_add_u32 s56,s55,256
D 17,56,1024
v_and_b32 v111,0xffff0000,v128
v_lshlrev_b32 v112,16,v129
v_and_b32 v113,0xffff0000,v129
v_fma_f32 v106,v106,v103,v102
v_fma_f32 v107,v107,v103,v102
s_add_u32 s56,s55,512
D 17,56,2048
v_fma_f32 v108,v108,v103,v102
v_fma_f32 v109,v109,v103,v102
v_fma_f32 v110,v110,v103,v102
v_fma_f32 v111,v111,v103,v102
v_fma_f32 v112,v112,v103,v102
s_add_u32 s56,s55,768
D 17,56,3072
v_fma_f32 v113,v113,v103,v102
v_add_f32 v26,v26,v106
v_add_f32 v27,v27,v107
v_add_f32 v28,v28,v108
v_add_f32 v29,v29,v109
v_cndmask_b32 v17,v10,v9,s[40:41]
s_add_u32 m0,s57,16384
s_add_u32 s56,s55,20480
D 17,56,0
v_add_f32 v30,v30,v110
v_add_f32 v31,v31,v111
v_add_f32 v32,v32,v112
v_add_f32 v33,v33,v113
v_cvt_pk_bf16_f32 v114,v26,v27
s_add_u32 s56,s55,20736
D 17,56,1024
v_cvt_pk_bf16_f32 v115,v28,v29
v_cvt_pk_bf16_f32 v116,v30,v31
v_cvt_pk_bf16_f32 v117,v32,v33
s_add_u32 s56,s55,20992
D 17,56,2048
s_add_u32 s56,s55,21248
D 17,56,3072
buffer_store_dwordx4 v[114:117],v0,s[36:39],s67 offen offset:0 sc0 sc1
v_add_f32 v106,v26,v27
v_fma_f32 v107,v26,v26,s87
s_add_u32 s55,s55,s60
s_sub_u32 s57,s84,s57
s_add_u32 s54,s54,s46
v_fma_f32 v107,v27,v27,v107
v_add_f32 v106,v106,v28
v_fma_f32 v107,v28,v28,v107
v_add_f32 v106,v106,v29
v_fma_f32 v107,v29,v29,v107
v_add_f32 v106,v106,v30
v_fma_f32 v107,v30,v30,v107
v_add_f32 v106,v106,v31
v_fma_f32 v107,v31,v31,v107
v_add_f32 v106,v106,v32
v_fma_f32 v107,v32,v32,v107
v_add_f32 v106,v106,v33
v_fma_f32 v107,v33,v33,v107
s_nop 1
v_permlane16_swap_b32 v106,v107
v_add_f32 v106,v106,v107
v_mov_b32 v107,v106
s_nop 1
v_permlane32_swap_b32 v106,v107
v_add_f32 v106,v106,v107
ds_write_b32 v12,v106 offset:2048
L 0
s_barrier
R 118,13,2048
L 0
v_add_f32 v118,v118,v119
v_add_f32 v120,v120,v121
v_add_f32 v118,v118,v120
v_mul_f32 v118,s86,v118
buffer_store_dword v118,v14,s[36:39],s77 offen offset:0 sc0 sc1
s_mov_b32 s72,s69
s_mov_b32 s69,0x7fff0000
s_mov_b32 s67,0x7fff0000
s_mov_b32 s77,0x7fff0000
s_branch Lg2_gox_%=
Lg2_xn1_%=:
s_cmp_ge_u32 s54,256
s_cbranch_scc1 Lg2_fin1_%=
s_add_u32 m0,s59,4096
s_nop 0
buffer_load_dword v8,s[36:39],s58 offen offset:0 sc1 lds
buffer_store_dword v4,v3,s[36:39],s71 offen offset:0 sc0 sc1
s_add_u32 m0,s59,5120
s_nop 0
buffer_load_dword v8,s[36:39],s58 offen offset:0 sc1 lds
buffer_store_dword v4,v3,s[36:39],s71 offen offset:0 sc0 sc1
s_mov_b32 s83,0
v_lshlrev_b32 v106,16,v126
v_and_b32 v107,0xffff0000,v126
v_lshlrev_b32 v108,16,v127
v_and_b32 v109,0xffff0000,v127
v_lshlrev_b32 v110,16,v128
v_and_b32 v111,0xffff0000,v128
v_lshlrev_b32 v112,16,v129
v_and_b32 v113,0xffff0000,v129
v_fma_f32 v106,v106,v103,v102
v_fma_f32 v107,v107,v103,v102
v_fma_f32 v108,v108,v103,v102
v_fma_f32 v109,v109,v103,v102
v_fma_f32 v110,v110,v103,v102
v_fma_f32 v111,v111,v103,v102
v_fma_f32 v112,v112,v103,v102
v_fma_f32 v113,v113,v103,v102
v_add_f32 v26,v26,v106
v_add_f32 v27,v27,v107
v_add_f32 v28,v28,v108
v_add_f32 v29,v29,v109
v_add_f32 v30,v30,v110
v_add_f32 v31,v31,v111
v_add_f32 v32,v32,v112
v_add_f32 v33,v33,v113
v_cvt_pk_bf16_f32 v114,v26,v27
v_cvt_pk_bf16_f32 v115,v28,v29
v_cvt_pk_bf16_f32 v116,v30,v31
v_cvt_pk_bf16_f32 v117,v32,v33
buffer_store_dwordx4 v[114:117],v0,s[36:39],s67 offen offset:0 sc0 sc1
v_add_f32 v106,v26,v27
v_fma_f32 v107,v26,v26,s87
v_fma_f32 v107,v27,v27,v107
v_add_f32 v106,v106,v28
v_fma_f32 v107,v28,v28,v107
v_add_f32 v106,v106,v29
v_fma_f32 v107,v29,v29,v107
v_add_f32 v106,v106,v30
v_fma_f32 v107,v30,v30,v107
v_add_f32 v106,v106,v31
v_fma_f32 v107,v31,v31,v107
v_add_f32 v106,v106,v32
v_fma_f32 v107,v32,v32,v107
v_add_f32 v106,v106,v33
v_fma_f32 v107,v33,v33,v107
s_nop 1
v_permlane16_swap_b32 v106,v107
v_add_f32 v106,v106,v107
v_mov_b32 v107,v106
s_nop 1
v_permlane32_swap_b32 v106,v107
v_add_f32 v106,v106,v107
ds_write_b32 v12,v106 offset:2048
L 0
s_barrier
R 118,13,2048
L 0
v_add_f32 v118,v118,v119
v_add_f32 v120,v120,v121
v_add_f32 v118,v118,v120
v_mul_f32 v118,s86,v118
buffer_store_dword v118,v14,s[36:39],s77 offen offset:0 sc0 sc1
s_mov_b32 s72,s69
s_mov_b32 s69,0x7fff0000
s_mov_b32 s67,0x7fff0000
s_mov_b32 s77,0x7fff0000
)"
R"(s_branch Lg2_poll_%=
Lg2_fin1_%=:
v_lshlrev_b32 v106,16,v126
v_and_b32 v107,0xffff0000,v126
v_lshlrev_b32 v108,16,v127
v_and_b32 v109,0xffff0000,v127
v_lshlrev_b32 v110,16,v128
v_and_b32 v111,0xffff0000,v128
v_lshlrev_b32 v112,16,v129
v_and_b32 v113,0xffff0000,v129
v_fma_f32 v106,v106,v103,v102
v_fma_f32 v107,v107,v103,v102
v_fma_f32 v108,v108,v103,v102
v_fma_f32 v109,v109,v103,v102
v_fma_f32 v110,v110,v103,v102
v_fma_f32 v111,v111,v103,v102
v_fma_f32 v112,v112,v103,v102
v_fma_f32 v113,v113,v103,v102
v_add_f32 v26,v26,v106
v_add_f32 v27,v27,v107
v_add_f32 v28,v28,v108
v_add_f32 v29,v29,v109
v_add_f32 v30,v30,v110
v_add_f32 v31,v31,v111
v_add_f32 v32,v32,v112
v_add_f32 v33,v33,v113
v_cvt_pk_bf16_f32 v114,v26,v27
v_cvt_pk_bf16_f32 v115,v28,v29
v_cvt_pk_bf16_f32 v116,v30,v31
v_cvt_pk_bf16_f32 v117,v32,v33
buffer_store_dwordx4 v[114:117],v0,s[36:39],s67 offen offset:0 sc0 sc1
v_add_f32 v106,v26,v27
v_fma_f32 v107,v26,v26,s87
v_fma_f32 v107,v27,v27,v107
v_add_f32 v106,v106,v28
v_fma_f32 v107,v28,v28,v107
v_add_f32 v106,v106,v29
v_fma_f32 v107,v29,v29,v107
v_add_f32 v106,v106,v30
v_fma_f32 v107,v30,v30,v107
v_add_f32 v106,v106,v31
v_fma_f32 v107,v31,v31,v107
v_add_f32 v106,v106,v32
v_fma_f32 v107,v32,v32,v107
v_add_f32 v106,v106,v33
v_fma_f32 v107,v33,v33,v107
s_nop 1
v_permlane16_swap_b32 v106,v107
v_add_f32 v106,v106,v107
v_mov_b32 v107,v106
s_nop 1
v_permlane32_swap_b32 v106,v107
v_add_f32 v106,v106,v107
ds_write_b32 v12,v106 offset:2048
L 0
s_barrier
R 118,13,2048
L 0
v_add_f32 v118,v118,v119
v_add_f32 v120,v120,v121
v_add_f32 v118,v118,v120
v_mul_f32 v118,s86,v118
buffer_store_dword v118,v14,s[36:39],s77 offen offset:0 sc0 sc1
s_mov_b32 s72,s69
s_mov_b32 s69,0x7fff0000
s_mov_b32 s67,0x7fff0000
s_mov_b32 s77,0x7fff0000
s_branch Lg2_done_%=
Lg2_done_%=:
s_waitcnt vmcnt(2)
s_barrier
buffer_store_dword v4,v3,s[36:39],s70 offen offset:0 sc0 sc1
s_waitcnt vmcnt(1)
s_barrier
buffer_store_dword v4,v3,s[36:39],s72 offen offset:0 sc0 sc1
s_waitcnt vmcnt(0) lgkmcnt(0)
s_barrier
Lg2_end_%=:
s_mov_b32 %0,s53
Lcode_g12_e:
.purgem M
.purgem R
.purgem T
.purgem D
.purgem L
)"
  : "=&s"(epOut) : "s"(ws), "s"(sWave), "s"(sG0), "s"(sStep), "s"(sOutOff), "s"(sFoutOff), "s"(sAuxOff), "s"(sWw), "s"(sBuf), "s"(sRk), "v"(vL16), "v"(vFlag), "v"(vSnapOff), "v"(vSnapRd), "v"(vWd0), "v"(vWd1), "v"(vWr0), "v"(vWr1), "v"(vSp), "v"(vOutS), "v"(vAux0), "v"(vAux1), "v"(vAux2), "v"(vAux3), "v"(vAn), "v"(vAe), "s"(w12) : "v0", "v1", "v2", "v3", "v4", "v5", "v6", "v7", "v8", "v9", "v10", "v11", "v12", "v13", "v14", "v15", "v16", "v17", "v18", "v19", "v20", "v21", "v22", "v23", "v24", "v25", "v26", "v27", "v28", "v29", "v30", "v31", "v32", "v33", "v34", "v35", "v36", "v37", "v38", "v39", "v40", "v41", "v42", "v43", "v44", "v45", "v46", "v47", "v48", "v49", "v50", "v51", "v52", "v53", "v54", "v55", "v56", "v57", "v58", "v59", "v60", "v61", "v62", "v63", "v64", "v65", "v66", "v67", "v68", "v69", "v70", "v71", "v72", "v73", "v74", "v75", "v76", "v77", "v78", "v79", "v80", "v81", "v82", "v83", "v84", "v85", "v86", "v87", "v88", "v89", "v90", "v91", "v92", "v93", "v94", "v95", "v96", "v97", "v98", "v99", "v100", "v101", "v102", "v103", "v104", "v105", "v106", "v107", "v108", "v109", "v110", "v111", "v112", "v113", "v114", "v115", "v116", "v117", "v118", "v119", "v120", "v121", "v122", "v123", "v124", "v125", "v126", "v127", "v128", "v129", "v130", "v131", "v132", "v133", "v134", "v135", "v136", "v137", "v138", "v139", "v140", "v141", "v142", "v143", "v144", "v145", "v146", "v147", "v148", "v149", "v150", "v151", "v152", "v153", "v154", "v155", "v156", "v157", "v158", "v159", "v160", "v161", "v162", "v163", "v164", "v165", "v166", "v167", "v168", "v169", "v170", "v171", "v172", "v173", "v174", "v175", "v176", "v177", "v178", "v179", "v180", "v181", "v182", "v183", "v184", "v185", "v186", "v187", "v188", "v189", "v190", "v191", "v192", "v193", "v194", "v195", "v196", "v197", "v198", "v199", "v200", "v201", "v202", "v203", "v204", "v205", "v206", "v207", "v208", "v209", "v210", "v211", "v212", "v213", "v214", "v215", "v216", "v217", "v218", "v219", "v220", "v221", "v222", "v223", "v224", "v225", "v226", "v227", "v228", "v229", "v230", "v231", "a0", "a1", "a2", "a3", "a4", "a5", "a6", "a7", "a8", "a9", "a10", "a11", "a12", "a13", "a14", "a15", "a16", "a17", "a18", "a19", "a20", "a21", "a22", "a23", "a24", "a25", "a26", "a27", "a28", "a29", "a30", "a31", "a32", "a33", "a34", "a35", "a36", "a37", "a38", "a39", "a40", "a41", "a42", "a43", "a44", "a45", "a46", "a47", "a48", "a49", "a50", "a51", "a52", "a53", "a54", "a55", "a56", "a57", "a58", "a59", "a60", "a61", "a62", "a63", "a64", "a65", "a66", "a67", "a68", "a69", "a70", "a71", "a72", "a73", "a74", "a75", "a76", "a77", "a78", "a79", "a80", "a81", "a82", "a83", "a84", "a85", "a86", "a87", "a88", "a89", "a90", "a91", "a92", "a93", "a94", "a95", "a96", "a97", "a98", "a99", "a100", "a101", "a102", "a103", "a104", "a105", "a106", "a107", "a108", "a109", "a110", "a111", "a112", "a113", "a114", "a115", "a116", "a117", "a118", "a119", "a120", "a121", "a122", "a123", "a124", "a125", "a126", "a127", "a128", "a129", "a130", "a131", "a132", "a133", "a134", "a135", "a136", "a137", "a138", "a139", "a140", "a141", "a142", "a143", "a144", "a145", "a146", "a147", "a148", "a149", "a150", "a151", "a152", "a153", "a154", "a155", "a156", "a157", "a158", "a159", "a160", "a161", "a162", "a163", "a164", "a165", "a166", "a167", "a168", "a169", "a170", "a171", "a172", "a173", "a174", "a175", "a176", "a177", "a178", "a179", "a180", "a181", "a182", "a183", "a184", "a185", "a186", "a187", "a188", "a189", "a190", "a191", "a192", "a193", "a194", "a195", "a196", "a197", "a198", "a199", "a200", "a201", "a202", "a203", "a204", "a205", "a206", "a207", "a208", "a209", "a210", "a211", "a212", "a213", "a214", "a215", "a216", "a217", "a218", "a219", "a220", "a221", "a222", "a223", "a224", "a225", "a226", "a227", "a228", "a229", "a230", "a231", "a232", "a233", "a234", "a235", "a236", "a237", "a238", "a239", "a240", "a241", "a242", "a243", "a244", "a245", "a246", "a247", "a248", "a249", "a250", "a251", "a252", "a253", "a254", "a255", "s36", "s37", "s38", "s39", "s40", "s41", "s42", "s43", "s44", "s45", "s46", "s47", "s48", "s49", "s50", "s51", "s52", "s53", "s54", "s55", "s56", "s57", "s58", "s59", "s60", "s61", "s62", "s63", "s64", "s65", "s66", "s67", "s68", "s69", "s70", "s71", "s72", "s73", "s74", "s75", "s76", "s77", "s78", "s79", "s80", "s81", "s82", "s83", "s84", "s85", "s86", "s87", "s88", "s89", "s90", "s91", "s92", "s93", "s94", "s95", "s96", "s97", "vcc", "scc", "memory");
      ep = epOut;
    }
    LANE_ID(lane2);
    for (int t = kind == 6 ? 64 + slice : (kind - 1) * 32 + rep * 8 + slice; t < LAST_TASKS; t += 64 + NRW)
      row_last(ws, out, t, lane2, wave, ep);
    if (b == 1 && wave == 0) set_flag(ws + OFF_EP, lane2, ep);
    CTS(14);
    asm volatile("s_endpgm\nLcode_end:" ::: "memory");
    __builtin_unreachable();
  }
}

extern "C" void kernel_launch(void* const* d_in, const int* in_sizes, int n_in, void* d_out, int out_size,
                              void* d_ws, size_t ws_size, hipStream_t stream) {
  const u16* q = (const u16*)d_in[0];
  const u16* k = (const u16*)d_in[1];
  const u16* v = (const u16*)d_in[2];
  const u16* shift_k = (const u16*)d_in[3];
  const u16* ssm_k = (const u16*)d_in[4];
  const u16* ssm_D = (const u16*)d_in[5];
  const u16* w1 = (const u16*)d_in[7];
  const u16* w2 = (const u16*)d_in[8];
  const u16* w3 = (const u16*)d_in[9];
  const u16* w4 = (const u16*)d_in[10];
  flow_kernel<<<256, 256, 0, stream>>>(q, k, v, shift_k, ssm_k, ssm_D, w1, w2, w3, w4, (char*)d_ws, (u16*)d_out);
}
```

```cpp
#include <hip/hip_runtime.h>
#include <hip/hip_bf16.h>
#include <stdint.h>
#include <type_traits>

typedef short s16x8 __attribute__((ext_vector_type(8)));
typedef short s16x4 __attribute__((ext_vector_type(4)));
typedef float f32x4 __attribute__((ext_vector_type(4)));
typedef unsigned short u16;
typedef unsigned short u16x8 __attribute__((ext_vector_type(8)));
typedef unsigned short u16x4 __attribute__((ext_vector_type(4)));
typedef float f32x2 __attribute__((ext_vector_type(2)));
typedef __bf16 bf16x2 __attribute__((ext_vector_type(2)));
typedef unsigned u32x4v __attribute__((ext_vector_type(4)));
typedef __attribute__((address_space(1))) unsigned gu32;

constexpr int S_ = 2048, D_ = 1024, F_ = 3584;
#define PF 1

constexpr int WS_USED = 111243392, LAST_TASKS = 228, U0 = 214, NR4 = 7;
constexpr int OFF_KVR = 4194304, OFF_XP = 8388608, OFF_HS = 17825792, OFF_X1 = 31457280, OFF_G = 40894464, OFF_P0 = 71303168, OFF_P1 = 89128960, OFF_Y = 98566144, OFF_S1 = 106954752, OFF_ST = 107479040, OFF_FL = 109051904, OFF_FXP = 109056000, OFF_FH = 109334528, OFF_FX1 = 109613056, OFF_FG = 109891584, OFF_FP = 110587904, OFF_FKV = 111144960, OFF_FY = 111177728, OFF_EP = 111243264, OFF_DBG = 115343360;

#define WS_DIAG 0

__device__ __forceinline__ float bf2f(u16 v) { return __uint_as_float(((unsigned)v) << 16); }
__device__ __forceinline__ u16 f2bf(float f) { return __builtin_bit_cast(u16, (__bf16)f); }
__device__ __forceinline__ float rbf(float f) { return bf2f(f2bf(f)); }
__device__ __forceinline__ void st16(void* p, u16x8 v) {
  asm volatile("global_store_dwordx4 %0, %1, off sc0 sc1\n\ts_nop 1" :: "v"(p), "v"(v) : "memory");
}
#define WAIT_VM(n) asm volatile("s_waitcnt vmcnt(%0)" ::"n"(n) : "memory")
#define WAIT_LGKM0() asm volatile("s_waitcnt lgkmcnt(0)" ::: "memory")

#define CODE_RANGE(B, E, p, n)                                                                                   \
  const char* p; unsigned n;                                                                                     \
  { unsigned lo_, hi_;                                                                                           \
    asm volatile("s_getpc_b64 s[98:99]\nLpc%=:\n\ts_add_u32 %0, s98, (" B "-Lpc%=)&4294967295\n\ts_addc_u32 %1, s99, (" B "-Lpc%=)>>32\n\t" \
                 "s_mov_b32 %2, " E "-" B : "=s"(lo_), "=s"(hi_), "=s"(n) :: "s98", "s99", "scc");               \
    p = (const char*)(((unsigned long long)hi_ << 32) | lo_); }
__device__ __forceinline__ unsigned touch(const char* p, unsigned n, unsigned off) {
  unsigned t;
  asm volatile("global_load_dword %0, %1, off" : "=v"(t) : "v"(p + (off < n ? off : 0u)) : "memory");
  return t;
}

__device__ __forceinline__ void touch_lds(const char* p, unsigned a) {
  asm volatile("s_mov_b32 m0, %1\n\ts_nop 0\n\tglobal_load_lds_dword %0, off" :: "v"(p), "s"(a) : "memory");
}
#define PFPOL ""
__device__ __forceinline__ void touch_pol(const char* p, unsigned a) {
  asm volatile("s_mov_b32 m0, %1\n\ts_nop 0\n\tglobal_load_lds_dword %0, off " PFPOL :: "v"(p), "s"(a) : "memory");
}
#define YSLEEP 4
#define KSLEEP 2
#define CTS(i)
__device__ __forceinline__ f32x2 up2(unsigned p) { return (f32x2){__uint_as_float(p << 16), __uint_as_float(p & 0xffff0000u)}; }
__device__ __forceinline__ unsigned pk2(f32x2 v) { return __builtin_bit_cast(unsigned, __builtin_convertvector(v, bf16x2)); }
__device__ __forceinline__ f32x2 rbf2(f32x2 v) { return up2(pk2(v)); }
template <int N, class F> __device__ __forceinline__ void sfor(F&& f) {
  if constexpr (N > 0) { sfor<N - 1>(f); f(std::integral_constant<int, N - 1>{}); }
}
__device__ __forceinline__ void set_flag(char* p, int i, unsigned ep) {
  if (i < 32) __hip_atomic_store((gu32*)p + i, ep, __ATOMIC_RELAXED, __HIP_MEMORY_SCOPE_AGENT);
}
template <int NL>
__device__ __forceinline__ void poll_flags(const char* p, int n, int lane, unsigned ep) {
  for (int it = 0; it < 20000; ++it) {
    bool ok = true;
#pragma unroll
    for (int i = 0; i < NL; ++i) {
      const int f = lane + 64 * i;
      ok &= __hip_atomic_load((gu32*)(p + (f < n ? f : 0) * 128), __ATOMIC_RELAXED, __HIP_MEMORY_SCOPE_AGENT) == ep;
    }
    if (__all(ok)) break;
    __builtin_amdgcn_s_sleep(YSLEEP);
  }
  asm volatile("" ::: "memory");
}

constexpr int CV_HP = (2048 + 32) * 2, CV_UR = (2048 + 256) * 2, CV_CH = 2 * CV_HP + CV_UR;
constexpr int NCV = 174;
constexpr int CV_STG = 8 * CV_CH;
constexpr int YP = D_ * 16;

struct KvRegs { u32x4v k0, k1, k2, k3, k4, k5, k6, v0, v1, v2, v3, s0, s1, s2, s3; };
#define KV_LOADS                                                                                                                       \
  "v_lshrrev_b32 %[a], 3, %[tid]\n\tv_and_b32 %[c], 7, %[tid]\n\ts_lshr_b32 s90, %[tile], 4\n\ts_and_b32 s91, %[tile], 15\n\t"          \
  "v_lshlrev_b32 %[a], 13, %[a]\n\tv_lshlrev_b32 %[c], 4, %[c]\n\ts_lshl_b32 s90, s90, 18\n\ts_lshl_b32 s91, s91, 7\n\t"                 \
  "v_add_u32 %[a], s90, %[a]\n\tv_add_u32 %[c], s91, %[c]\n\tv_subrev_u32 %[h], 0x1000, %[a]\n\tv_max_i32 %[h], 0x800, %[h]\n\t"         \
  "v_add3_u32 %[a], %[a], %[c], s92\n\tv_add_u32 %[h], %[h], %[c]\n\tv_lshlrev_b32 %[c], 2, %[c]\n\ts_waitcnt lgkmcnt(0)\n\t"            \
  "global_load_dwordx4 %3, %[a], s[76:77] offset:-4096 nt\n\tglobal_load_dwordx4 %4, %[a], s[76:77] offset:-2048 nt\n\t"                \
  "global_load_dwordx4 %5, %[a], s[76:77] nt\n\tglobal_load_dwordx4 %6, %[a], s[76:77] offset:2048 nt\n\t"                              \
  "global_load_dwordx4 %7, %[a], s[78:79] offset:-4096 nt\n\tglobal_load_dwordx4 %8, %[a], s[78:79] offset:-2048 nt\n\t"                \
  "global_load_dwordx4 %9, %[a], s[78:79] nt\n\tglobal_load_dwordx4 %10, %[a], s[78:79] offset:2048 nt\n\t"                             \
  "global_load_dwordx4 %0, %[h], s[76:77] offset:-2048 nt\n\tglobal_load_dwordx4 %1, %[h], s[76:77] nt\n\t"                             \
  "global_load_dwordx4 %2, %[h], s[76:77] offset:2048 nt\n\t"                                                                           \
  "global_load_dwordx4 %11, %[c], s[80:81]\n\tglobal_load_dwordx4 %12, %[c], s[80:81] offset:16\n\t"                                    \
  "global_load_dwordx4 %13, %[c], s[80:81] offset:32\n\tglobal_load_dwordx4 %14, %[c], s[80:81] offset:48\n\t"
#define KV_OUTS(R) "=&v"(R.k0), "=&v"(R.k1), "=&v"(R.k2), "=&v"(R.k3), "=&v"(R.k4), "=&v"(R.k5), "=&v"(R.k6), "=&v"(R.v0), "=&v"(R.v1), "=&v"(R.v2), \
                   "=&v"(R.v3), "=&v"(R.s0), "=&v"(R.s1), "=&v"(R.s2), "=&v"(R.s3), [a] "=&v"(ta), [h] "=&v"(th), [c] "=&v"(tc)
#define KV_CLOB "s74", "s75", "s76", "s77", "s78", "s79", "s80", "s81", "s82", "s83", "s90", "s91", "s92", "scc", "vcc", "memory"
template <int N, int N2>
__device__ __forceinline__ void kv_wait(KvRegs& R, int tile) {
  asm volatile("s_cmp_ge_u32 %17, 192\n\ts_cbranch_scc1 Lkw%=\n\ts_waitcnt vmcnt(%15)\nLkw%=:\n\ts_waitcnt vmcnt(%16)"
               : "+v"(R.k0), "+v"(R.k1), "+v"(R.k2), "+v"(R.k3), "+v"(R.k4), "+v"(R.k5), "+v"(R.k6), "+v"(R.v0), "+v"(R.v1), "+v"(R.v2),
                 "+v"(R.v3), "+v"(R.s0), "+v"(R.s1), "+v"(R.s2), "+v"(R.s3) : "n"(N), "n"(N2), "s"(tile) : "scc", "memory");
}
__device__ __forceinline__ void kv_store(KvRegs& R, char* __restrict__ ws, char* T, int b, int tid) {
  const int t0 = (b >> 4) * 128, c0 = (b & 15) * 64, ch8 = tid & 7, rg = tid >> 3;
  if (t0 + 4 * rg < 3) R.k0 = R.k1 = R.k2 = (u32x4v){0u, 0u, 0u, 0u};
  typedef unsigned u32x2 __attribute__((ext_vector_type(2)));
  const u32x4v kr[7] = {R.k0, R.k1, R.k2, R.k3, R.k4, R.k5, R.k6}, vr[4] = {R.v0, R.v1, R.v2, R.v3}, sk[4] = {R.s0, R.s1, R.s2, R.s3};
#pragma unroll
  for (int j2 = 0; j2 < 4; ++j2) {
    const f32x2 sa = up2(sk[j2][0]), sb = up2(sk[j2][1]), sc = up2(sk[j2][2]), sd = up2(sk[j2][3]);
    const f32x2 s0 = {sa[0], sc[0]}, s1 = {sa[1], sc[1]}, s2 = {sb[0], sd[0]}, s3 = {sb[1], sd[1]};
    f32x2 kf[7], o[4];
#pragma unroll
    for (int r = 0; r < 7; ++r) kf[r] = up2(kr[r][j2]);
#pragma unroll
    for (int r = 0; r < 4; ++r)
      o[r] = __builtin_elementwise_fma(kf[r + 3], s0, __builtin_elementwise_fma(kf[r + 2], s1, __builtin_elementwise_fma(kf[r + 1], s2, kf[r] * s3))) * up2(vr[r][j2]);
#pragma unroll
    for (int e = 0; e < 2; ++e)
      *(u32x2*)(T + (8 * ch8 + 2 * j2 + e) * 264 + (124 - 4 * rg) * 2) = (u32x2){pk2((f32x2){o[3][e], o[2][e]}), pk2((f32x2){o[1][e], o[0][e]})};
  }
  __syncthreads();
  u16* kvr = (u16*)(ws + OFF_KVR);
#pragma unroll
  for (int i = 0; i < 4; ++i) {
    const int idx = tid + 256 * i, ch = idx >> 4, cb = idx & 15;
    const u16x4 lo = *(const u16x4*)(T + ch * 264 + cb * 16), hi = *(const u16x4*)(T + ch * 264 + cb * 16 + 8);
    st16(kvr + (size_t)(c0 + ch) * S_ + (1920 - t0) + cb * 8, __builtin_shufflevector(lo, hi, 0, 1, 2, 3, 4, 5, 6, 7));
  }
}

#define LASTSLEEP 4
__device__ __forceinline__ void wait_flags(const char* p, unsigned ep) {
  gu32* f = (gu32*)p;
  for (int it = 0; it < 20000; ++it) {
    if (__all(__hip_atomic_load(f, __ATOMIC_RELAXED, __HIP_MEMORY_SCOPE_AGENT) == ep)) break;
    __builtin_amdgcn_s_sleep(LASTSLEEP);
  }
  asm volatile("" ::: "memory");
}
__device__ __forceinline__ void row_last(char* __restrict__ ws, u16* __restrict__ out, int t, int lane, int wave, unsigned ep) {
  const __amdgpu_buffer_rsrc_t rs = __builtin_amdgcn_make_buffer_rsrc(ws, 0, WS_USED, 0x00020000);
  const int r8 = lane & 7, seg = lane >> 3, un = 128 + (t >> 1);
  const unsigned fo = un * 32768 + (wave * 8 + (seg >> 2)) * 1024 + ((seg & 3) * 16 + (t & 1) * 8 + r8) * 16;
  wait_flags(ws + OFF_FP + un * 2048 + ((lane & 2) * 4 + wave * 2 + (lane & 1)) * 128, ep);
  u32x4v p0[4], p1[4];
#pragma unroll
  for (int i = 0; i < 4; ++i) {
    p0[i] = __builtin_bit_cast(u32x4v, __builtin_amdgcn_raw_buffer_load_b128(rs, fo + i * 2048, OFF_P0, 16));
    p1[i] = __builtin_bit_cast(u32x4v, __builtin_amdgcn_raw_buffer_load_b128(rs, fo + i * 2048, OFF_P1, 16));
  }
#pragma unroll
  for (int i = 0; i < 4; ++i) {
    u32x4v o;
#pragma unroll
    for (int e = 0; e < 4; ++e) o[e] = pk2(up2(p0[i][e]) + up2(p1[i][e]));
    st16(out + (size_t)(t * 8 + r8) * D_ + wave * 256 + i * 64 + seg * 8, __builtin_bit_cast(u16x8, o));
  }
}

__device__ __forceinline__ int swzB(int r) { return ((((r >> 1) & 1) | (((r >> 3) & 1) << 1)) << 1); }
#define TCH 1
#define LANE_ID(x) int x; asm volatile("v_mbcnt_lo_u32_b32 %0, -1, 0\n\tv_mbcnt_hi_u32_b32 %0, -1, %0" : "=v"(x))
#define T2X 5
#define W12 3
#define HL 0
#define W_B1 (b == 1)
#define W_B7 (b == 7)
#define W_I15 (idx == 15)
#define W_I31 (idx == 31)
#define W_B249 (b == 249)
#define RLN 7
#define QTB 1
constexpr int QT0 = 34 * 128;
#define CODE_MAX(x, y) "(((" x ">" y ")&" x ")|((" x "<=" y ")&" y "))"
constexpr int HU = 40960;
#define G3OR 5
constexpr int NRW = 22 - G3OR;
__global__ __launch_bounds__(256, 1) void flow_kernel(const u16* __restrict__ q, const u16* __restrict__ kin, const u16* __restrict__ vin,
                                                      const u16* __restrict__ shift_k, const u16* __restrict__ ssm_k, const u16* __restrict__ ssm_D,
                                                      const u16* __restrict__ w1, const u16* __restrict__ w2,
                                                      const u16* __restrict__ w3, const u16* __restrict__ w4, char* __restrict__ ws,
                                                      u16* __restrict__ out) {
  __shared__ __attribute__((aligned(16))) char lds[163840];
  KvRegs KR, KR2;
  unsigned ew, ta, th, tc;
  int b, tile;
  asm volatile("Lcode_b:\n\ts_load_dwordx4 s[76:79], %[ka], 0x8\n\ts_load_dwordx2 s[82:83], %[ka], 0x50\n\t"
               "s_getpc_b64 s[74:75]\n\ts_lshr_b32 s90, %[bi], 3\n\ts_mov_b32 m0, 0x27f00\n\tv_lshlrev_b32 %[a], 5, %[tid]\n\tv_add_lshl_u32 %[a], %[a], s90, 7\n\t"
               "v_min_u32 %[a], " CODE_MAX(CODE_MAX(CODE_MAX("(Lcode_g3_e-Lcode_b)", "(Lcode_g3o_e-Lcode_b)"), CODE_MAX("(Lcode_g4_e-Lcode_b)", "(Lcode_g12_e-Lcode_b)")), "(Lcode_end-Lcode_b)") "-64, %[a]\n\t"
               "global_load_lds_dword %[a], s[74:75]\n\ts_load_dwordx2 s[80:81], %[ka], 0x18\n\t"
               "s_mov_b32 %[b], %[bi]\n\ts_bfe_u32 s91, %[bi], 0x20001\n\ts_lshl2_add_u32 %[tile], s90, s91\n\ts_movk_i32 s92, 0x1000\n\t"
               "s_bitcmp1_b32 %[bi], 0\n\ts_cbranch_scc0 Lk1%=\n\ts_add_u32 %[tile], %[tile], 64\n\ts_cmp_lt_u32 s90, 8\n\ts_cbranch_scc1 Lk2%=\n\t"
               "s_lshl3_add_u32 s91, s91, s90\n\ts_add_u32 s91, s91, 184\n\ts_cmp_lt_u32 s90, 16\n\ts_cselect_b32 %[tile], s91, %[tile]\nLk1%=:\n\t"
               KV_LOADS "v_mov_b32 %[a], %[eo]\n\tglobal_load_dword %[ew], %[a], s[82:83] sc1\nLk2%=:\n\ts_waitcnt lgkmcnt(0)"
               : KV_OUTS(KR), [ew] "=&v"(ew), [b] "=&s"(b), [tile] "=&s"(tile)
               : [ka] "s"(__builtin_amdgcn_kernarg_segment_ptr()), [bi] "s"(blockIdx.x), [tid] "v"(threadIdx.x), [eo] "n"(OFF_EP) : KV_CLOB);
  const unsigned t_start = 0;
  const int tid = threadIdx.x, lane = tid & 63, wave = __builtin_amdgcn_readfirstlane(tid >> 6);
  const int xcd = b & 7, idx = b >> 3, tile2 = (tile + 32) ^ T2X;
  asm volatile("s_cmp_lt_u32 %[tile], 224\n\ts_cbranch_scc1 Lk3%=\n\ts_load_dwordx4 s[76:79], %[ka], 0x8\n\ts_load_dwordx2 s[80:81], %[ka], 0x18\n\ts_movk_i32 s92, 0x1000\n\t"
               KV_LOADS "\nLk3%=:"
               : KV_OUTS(KR2) : [ka] "s"(__builtin_amdgcn_kernarg_segment_ptr()), [tile] "s"(tile2), [tid] "v"(threadIdx.x) : KV_CLOB);
  const unsigned sLds = (unsigned)(size_t)lds;
  const int o8 = idx & 7;
  int kind, rep, slice, kh = 0;
  int j3 = 0, j4 = 0;
  const int xp = xcd >> 1, pr = xp & 1;
  if (!(xcd & 1)) {
    if (idx < 20) { kind = 4; rep = idx < 16 ? (xp >> 1) * 3 + (idx >> 3) : 2; kh = pr; slice = idx < 16 ? o8 : (xp >> 1) * 4 + o8; j4 = rep * 16 + kh * 8 + slice; }
    else { kind = 3; rep = pr * 2 + (idx >= 26); slice = (xp >> 1) * 6 + idx - (idx >= 26 ? 26 : 20); j3 = rep * 18 + slice; }
  } else if (idx < 16) { kind = 1 + (idx >> 3); rep = xp; slice = o8; }
  else {
    const int k = idx - 16, n3 = xp < 2 ? 12 : 9, no = xp < 2 ? 0 : 5 - xp;
    if (k < n3) { kind = 3; rep = xp < 2 ? pr * 2 + (k >= 6) : 4; slice = xp < 2 ? 12 + k - 6 * (k >= 6) : pr * 9 + k; j3 = rep * 18 + slice; }
    else if (k < n3 + no) { kind = 5; rep = pr * 3 + k - n3; slice = 18; j3 = 90 + rep; }
    else { kind = 6; rep = 0; slice = xp * 4 + k - n3 - no; }
  }
  CTS(0);
  const unsigned sScr = sLds + 163584;
  unsigned ep = 0;
  auto w_touch = [&](const u16* w) {
#pragma unroll
    for (int i = 0; i < 2; ++i) touch_lds((const char*)w + (size_t)(rep * 256 + i * 128 + (tid >> 1)) * 2048 + slice * 256 + (tid & 1) * 128, sScr);
  };
  if (kind == 1) { if (W12 & 1) w_touch(w1); }
  else {
  const bool isconv = kind >= 3 && kind != 6 && j3 < 94;
  const int nw = (kind == 4 || j3 >= 92) ? 2 : 1, c0 = kind == 4 ? 8 * j4 : j3 < 92 ? 640 + 4 * j3 : j3 < 94 ? 272 + 8 * j3 : 0, cw = c0 + nw * wave;
  const unsigned hd = isconv ? sLds + 2 * wave * CV_CH + 32 : 0x30000u;
  auto h_dma = [&](bool first) {
#pragma unroll
    for (int j = 0; j < 2; ++j) {
      const u16* hp = ssm_k + (size_t)cw * S_ + (isconv && j < nw ? j * S_ + lane * 8 : 0);
      if (first) asm volatile("s_mov_b32 m0, %1\n\ts_nop 0\n\tglobal_load_lds_dwordx4 %0, off nt" :: "v"(hp), "s"(hd + j * CV_CH) : "memory");
      else asm volatile("s_mov_b32 m0, %1\n\ts_nop 0\n\tglobal_load_lds_dwordx4 %0, off offset:1024 nt\n\t"
                        "global_load_lds_dwordx4 %0, off offset:2048 nt\n\tglobal_load_lds_dwordx4 %0, off offset:3072 nt" :: "v"(hp), "s"(hd + j * CV_CH) : "memory");
    }
  };
  h_dma(true);
  if (HL == 0) h_dma(false);
  *(u32x4v*)(lds + (2 * wave + (lane >> 5)) * CV_CH + 2 * CV_HP + 4096 + (lane & 31) * 16) = (u32x4v){0u, 0u, 0u, 0u};
  if (lane < 8) *(u32x4v*)(lds + (2 * wave + (lane >> 2)) * CV_CH + ((lane >> 1) & 1) * CV_HP + (lane & 1) * 16) = (u32x4v){0u, 0u, 0u, 0u};
  kv_wait<HL ? 3 : 9, HL ? 18 : 24>(KR, tile);
  CTS(12);
  kv_store(KR, ws, lds + CV_STG, tile, tid);
  kv_wait<63, HL ? 6 : 12>(KR2, tile);
  if (kind == 2) kv_store(KR2, ws, lds + CV_STG + 17408, tile2, tid);
  CTS(1);
  if (HL == 1) h_dma(false);
  const unsigned dw = ((const unsigned*)ssm_D)[cw >> 1];
  asm volatile("s_waitcnt vmcnt(%1)" : "+v"(ew) : "n"(HL == 1 ? 6 : 0) : "memory");
  ep = __builtin_amdgcn_readfirstlane(ew) + 1u;
  if (ep == 0u) ep = 1u;
  __builtin_amdgcn_s_barrier();
  set_flag(ws + OFF_FKV + ((wave & 1) ? tile2 : tile) * 128, wave < (kind == 2 ? 2 : 1) ? lane : 64, ep);
  CTS(2);
  if (HL == 2) h_dma(false);
  if (kind == 2 && (W12 & 2) != 0) w_touch(w2);
  if (isconv) {
    LANE_ID(lane);
    const int tid = wave * 64 + lane;
    const unsigned sC0 = (cw & 1) ? dw & 0xffff0000u : dw << 16, sC1 = dw & 0xffff0000u;
    const int l15 = lane & 15, lg = lane >> 4, cp = (l15 + 1) & 1, iw = kind == 4 ? j4 : 80 + j3, own = 2 * wave * CV_CH;
    const int ch = tid & (4 * nw - 1), tb = nw == 2 ? tid >> 3 : tid >> 2, slot = nw == 2 ? ch : 2 * ch;
    const unsigned sWave = wave, sBuf = nw, sAuxOff = OFF_KVR + cw * 4096, sFoutOff = OFF_FY + iw * 128, sExp = __builtin_amdgcn_readfirstlane(ep), sDbg = 110u * 1048576u + (b * 4 + wave) * 8192;
    const unsigned vL16 = lane * 16, vPA = own + cp * CV_HP + 2 * (8 * lg + l15 + 1 - cp), vPB = own + 2 * CV_HP + 2 * (2032 - 16 * l15 + 8 * lg) - 512 * 7;
    const unsigned vPU = own + 2 * CV_HP + 2 * (2044 - 16 * l15 - 4 * lg) - 512 * 7, vPW = 32 * l15 + 8 * lg;
    const unsigned vOR = slot * CV_CH + tb * 16, vORA = CV_STG + slot * 1024 + tb * 16, vOG = tb * YP + (c0 + ch) * 16;
    const unsigned vFA = ((lane & 3) * 16 + (c0 >> 6)) * 128, vFB = ((4 + lane % 12) * 16 + (c0 >> 6)) * 128, vFlag = tid < 32 ? tid * 4 : 0x80000000u;
    const unsigned vTW = lane < 42 ? (iw * 168 + wave * 42 + lane) * 128 : 0x80000000u;
    const unsigned vTX = lane < 44 ? ((kind == 4 ? j4 : j3 - 12) * 176 + wave * 44 + lane) * 128 : 0x80000000u;
    (void)sDbg;
    asm volatile(
R"(.macro M d,w,x
v_mfma_f32_16x16x32_bf16 v[\d:\d+3], \w, v[\x:\x+3], v[\d:\d+3]
.endm
.macro R d,a,o
ds_read_b128 v[\d:\d+3], v\a offset:\o
.endm
.macro T d,a,o
ds_read_b64_tr_b16 \d, v\a offset:\o
.endm
.macro D a,s,o
buffer_load_dwordx4 v\a, s[36:39], s\s offen offset:\o sc1 lds
.endm
.macro L c
s_waitcnt lgkmcnt(\c)
.endm
Lcode_cv_b:
s_mov_b64 s[36:37],%0
s_mov_b32 s38,111243392
s_mov_b32 s39,0x00020000
s_mov_b32 s44,%1
s_mov_b32 s51,%2
s_mov_b32 s49,%3
s_mov_b32 s48,%4
s_mov_b32 s86,%5
s_mov_b32 s87,%6
s_mov_b32 s53,%7
v_mov_b32 v0,%8
v_mov_b32 v1,%9
v_mov_b32 v2,%10
v_mov_b32 v3,%11
v_mov_b32 v4,%12
v_mov_b32 v5,%13
v_mov_b32 v6,%14
v_mov_b32 v7,%15
v_mov_b32 v8,%16
v_mov_b32 v9,%17
v_mov_b32 v10,%18
v_mov_b32 v11,%19
v_mov_b32 v12,%20
s_mov_b32 s58,111144960
buffer_load_dword v13,v8,s[36:39],s58 offen offset:0 sc1
s_mul_i32 s57,s44,25856
v_add_u32 v18,s57,v0
v_add_u32 v18,32,v18
v_mov_b32 v19,s57
R 88,18,0
ds_read_b32 v92,v18 offset:16
R 152,18,12928
ds_read_b32 v156,v18 offset:12944
ds_read_b32 v24,v19 offset:32
ds_read_b32 v25,v19 offset:12960
s_mov_b64 s[40:41],%21
s_mov_b32 s42,7340032
s_mov_b32 s43,0x00020000
s_mov_b64 s[60:61],%22
s_mov_b32 s62,7340032
s_mov_b32 s63,0x00020000
v_mov_b32 v23,s53
s_mov_b32 s82,0
s_add_u32 s55,s49,4096
s_cmp_eq_u32 s51,2
s_cselect_b32 s55,s55,0x7fff0000
s_cselect_b32 s56,0,0x70000000
s_add_u32 s57,s57,8320
v_add_u32 v14,1024,v1
v_add_u32 v15,2048,v1
v_add_u32 v16,3072,v1
v_add_u32 v17,12928,v1
L 4
v_alignbyte_b32 v96,v89,v88,2
v_alignbyte_b32 v97,v90,v89,2
v_alignbyte_b32 v98,v91,v90,2
v_alignbyte_b32 v99,v92,v91,2
ds_write_b128 v18,v[96:99] offset:4160
L 3
v_alignbyte_b32 v160,v153,v152,2
v_alignbyte_b32 v161,v154,v153,2
v_alignbyte_b32 v162,v155,v154,2
v_alignbyte_b32 v163,v156,v155,2
ds_write_b128 v18,v[160:163] offset:17088
L 2
v_lshlrev_b32 v24,16,v24
v_lshlrev_b32 v25,16,v25
s_mov_b64 exec,1
ds_write_b32 v19,v24 offset:4188
ds_write_b32 v19,v25 offset:17116
s_mov_b64 exec,-1
ds_read2_b32 a[0:1],v1 offset0:0 offset1:1
ds_read2_b32 a[2:3],v1 offset0:2 offset1:3
ds_read2_b32 a[4:5],v1 offset0:128 offset1:129
ds_read2_b32 a[6:7],v1 offset0:130 offset1:131
ds_read2_b32 a[8:9],v1 offset0:16 offset1:17
ds_read2_b32 a[10:11],v1 offset0:18 offset1:19
ds_read2_b32 a[12:13],v1 offset0:144 offset1:145
ds_read2_b32 a[14:15],v1 offset0:146 offset1:147
ds_read2_b32 a[16:17],v1 offset0:32 offset1:33
ds_read2_b32 a[18:19],v1 offset0:34 offset1:35
ds_read2_b32 a[20:21],v1 offset0:160 offset1:161
ds_read2_b32 a[22:23],v1 offset0:162 offset1:163
ds_read2_b32 a[24:25],v1 offset0:48 offset1:49
ds_read2_b32 a[26:27],v1 offset0:50 offset1:51
ds_read2_b32 a[28:29],v1 offset0:176 offset1:177
ds_read2_b32 a[30:31],v1 offset0:178 offset1:179
ds_read2_b32 a[32:33],v1 offset0:64 offset1:65
ds_read2_b32 a[34:35],v1 offset0:66 offset1:67
ds_read2_b32 a[36:37],v1 offset0:192 offset1:193
ds_read2_b32 a[38:39],v1 offset0:194 offset1:195
ds_read2_b32 a[40:41],v1 offset0:80 offset1:81
ds_read2_b32 a[42:43],v1 offset0:82 offset1:83
ds_read2_b32 a[44:45],v1 offset0:208 offset1:209
ds_read2_b32 a[46:47],v1 offset0:210 offset1:211
ds_read2_b32 a[48:49],v1 offset0:96 offset1:97
ds_read2_b32 a[50:51],v1 offset0:98 offset1:99
ds_read2_b32 a[52:53],v1 offset0:224 offset1:225
ds_read2_b32 a[54:55],v1 offset0:226 offset1:227
ds_read2_b32 a[56:57],v1 offset0:112 offset1:113
ds_read2_b32 a[58:59],v1 offset0:114 offset1:115
ds_read2_b32 a[60:61],v1 offset0:240 offset1:241
ds_read2_b32 a[62:63],v1 offset0:242 offset1:243
ds_read2_b32 a[64:65],v17 offset0:0 offset1:1
ds_read2_b32 a[66:67],v17 offset0:2 offset1:3
ds_read2_b32 a[68:69],v17 offset0:128 offset1:129
ds_read2_b32 a[70:71],v17 offset0:130 offset1:131
ds_read2_b32 a[72:73],v17 offset0:16 offset1:17
ds_read2_b32 a[74:75],v17 offset0:18 offset1:19
ds_read2_b32 a[76:77],v17 offset0:144 offset1:145
ds_read2_b32 a[78:79],v17 offset0:146 offset1:147
ds_read2_b32 a[80:81],v17 offset0:32 offset1:33
ds_read2_b32 a[82:83],v17 offset0:34 offset1:35
ds_read2_b32 a[84:85],v17 offset0:160 offset1:161
ds_read2_b32 a[86:87],v17 offset0:162 offset1:163
ds_read2_b32 a[88:89],v17 offset0:48 offset1:49
ds_read2_b32 a[90:91],v17 offset0:50 offset1:51
ds_read2_b32 a[92:93],v17 offset0:176 offset1:177
ds_read2_b32 a[94:95],v17 offset0:178 offset1:179
ds_read2_b32 a[96:97],v17 offset0:64 offset1:65
ds_read2_b32 a[98:99],v17 offset0:66 offset1:67
ds_read2_b32 a[100:101],v17 offset0:192 offset1:193
ds_read2_b32 a[102:103],v17 offset0:194 offset1:195
ds_read2_b32 a[104:105],v17 offset0:80 offset1:81
ds_read2_b32 a[106:107],v17 offset0:82 offset1:83
ds_read2_b32 a[108:109],v17 offset0:208 offset1:209
ds_read2_b32 a[110:111],v17 offset0:210 offset1:211
ds_read2_b32 a[112:113],v17 offset0:96 offset1:97
ds_read2_b32 a[114:115],v17 offset0:98 offset1:99
ds_read2_b32 a[116:117],v17 offset0:224 offset1:225
ds_read2_b32 a[118:119],v17 offset0:226 offset1:227
ds_read2_b32 a[120:121],v17 offset0:112 offset1:113
ds_read2_b32 a[122:123],v17 offset0:114 offset1:115
ds_read2_b32 a[124:125],v17 offset0:240 offset1:241
ds_read2_b32 a[126:127],v17 offset0:242 offset1:243
Lcv_bpa_%=:
s_waitcnt vmcnt(0)
v_cmp_eq_u32 vcc,s53,v13
s_add_u32 s82,s82,1
s_cmp_eq_u64 vcc,-1
s_cbranch_scc1 Lcv_bqa_%=
s_sleep 1
buffer_load_dword v13,v8,s[36:39],s58 offen offset:0 sc1
s_cmp_lt_u32 s82,20000
s_cbranch_scc1 Lcv_bpa_%=
s_waitcnt vmcnt(0)
Lcv_bqa_%=:
s_add_u32 m0,s57,0
s_nop 0
D 0,49,3072
s_add_u32 m0,s57,12928
s_nop 0
D 0,55,3072
buffer_load_dword v13,v9,s[36:39],s58 offen offset:0 sc1
R 88,18,0
ds_read_b32 v92,v18 offset:16
R 104,18,1024
ds_read_b32 v108,v18 offset:1040
R 120,18,2048
ds_read_b32 v124,v18 offset:2064
R 136,18,3072
ds_read_b32 v140,v18 offset:3088
L 6
v_alignbyte_b32 v96,v89,v88,2
v_alignbyte_b32 v97,v90,v89,2
v_alignbyte_b32 v98,v91,v90,2
v_alignbyte_b32 v99,v92,v91,2
ds_write_b128 v18,v[96:99] offset:4160
L 5
v_alignbyte_b32 v112,v105,v104,2
v_alignbyte_b32 v113,v106,v105,2
v_alignbyte_b32 v114,v107,v106,2
v_alignbyte_b32 v115,v108,v107,2
ds_write_b128 v18,v[112:115] offset:5184
L 4
v_alignbyte_b32 v128,v121,v120,2
v_alignbyte_b32 v129,v122,v121,2
v_alignbyte_b32 v130,v123,v122,2
v_alignbyte_b32 v131,v124,v123,2
ds_write_b128 v18,v[128:131] offset:6208
L 3
v_alignbyte_b32 v144,v137,v136,2
v_alignbyte_b32 v145,v138,v137,2
v_alignbyte_b32 v146,v139,v138,2
v_alignbyte_b32 v147,v140,v139,2
ds_write_b128 v18,v[144:147] offset:7232
R 152,18,12928
ds_read_b32 v156,v18 offset:12944
R 168,18,13952
ds_read_b32 v172,v18 offset:13968
R 184,18,14976
ds_read_b32 v188,v18 offset:14992
R 200,18,16000
ds_read_b32 v204,v18 offset:16016
L 6
v_alignbyte_b32 v160,v153,v152,2
v_alignbyte_b32 v161,v154,v153,2
v_alignbyte_b32 v162,v155,v154,2
v_alignbyte_b32 v163,v156,v155,2
ds_write_b128 v18,v[160:163] offset:17088
L 5
v_alignbyte_b32 v176,v169,v168,2
v_alignbyte_b32 v177,v170,v169,2
v_alignbyte_b32 v178,v171,v170,2
v_alignbyte_b32 v179,v172,v171,2
ds_write_b128 v18,v[176:179] offset:18112
L 4
v_alignbyte_b32 v192,v185,v184,2
v_alignbyte_b32 v193,v186,v185,2
v_alignbyte_b32 v194,v187,v186,2
v_alignbyte_b32 v195,v188,v187,2
ds_write_b128 v18,v[192:195] offset:19136
L 3
v_alignbyte_b32 v208,v201,v200,2
v_alignbyte_b32 v209,v202,v201,2
v_alignbyte_b32 v210,v203,v202,2
v_alignbyte_b32 v211,v204,v203,2
ds_write_b128 v18,v[208:211] offset:20160
s_lshl_b32 s78,s44,11
s_add_u32 s78,s78,103424
v_add_u32 v18,s78,v4
v_add_u32 v4,v19,v4
s_waitcnt vmcnt(0)
v_cmp_eq_u32 vcc,s53,v13
s_mov_b32 s83,0
s_cmp_eq_u64 vcc,-1
s_cbranch_scc0 Lcv_a_%=
s_mov_b32 s83,1
s_add_u32 m0,s57,0
s_nop 0
D 0,49,2048
D 0,49,1024
D 0,49,0
s_add_u32 m0,s57,12928
s_nop 0
D 0,55,2048
D 0,55,1024
D 0,55,0
Lcv_a_%=:
s_cmp_eq_u32 s51,2
s_cbranch_scc1 Lcv_a2_%=
L 0
R 88,2,3584
R 92,2,3072
R 96,2,3648
R 100,2,3136
R 104,2,3712
R 108,2,3200
R 112,2,3776
R 116,2,3264
L 6
v_mfma_f32_16x16x32_bf16 v[40:43],a[0:3],v[88:91],0
R 120,2,3840
R 124,2,3328
v_mfma_f32_16x16x32_bf16 v[44:47],a[0:3],v[92:95],0
M 44,a[4:7],88
L 6
M 40,a[8:11],96
R 128,2,3904
R 132,2,3392
M 44,a[8:11],100
M 44,a[12:15],96
L 6
M 40,a[16:19],104
R 136,2,3968
R 140,2,3456
M 44,a[16:19],108
M 44,a[20:23],104
L 6
M 40,a[24:27],112
R 144,2,4032
R 148,2,3520
M 44,a[24:27],116
M 44,a[28:31],112
L 6
M 40,a[32:35],120
M 44,a[32:35],124
M 44,a[36:39],120
L 4
M 40,a[40:43],128
M 44,a[40:43],132
M 44,a[44:47],128
L 2
M 40,a[48:51],136
ds_read_b64 v[24:25],v3 offset:3584
ds_read_b64 v[26:27],v3 offset:3072
M 44,a[48:51],140
M 44,a[52:55],136
L 2
M 40,a[56:59],144
M 44,a[56:59],148
M 44,a[60:63],144
L 0
v_and_b32 v36,0xffff0000,v25
v_lshlrev_b32 v37,16,v25
v_and_b32 v38,0xffff0000,v24
v_lshlrev_b32 v39,16,v24
s_nop 0
v_fma_f32 v40,v36,s86,v40
v_fma_f32 v41,v37,s86,v41
v_fma_f32 v42,v38,s86,v42
v_fma_f32 v43,v39,s86,v43
v_cvt_pk_bf16_f32 v40,v40,v41
v_cvt_pk_bf16_f32 v41,v42,v43
ds_write_b64 v18,v[40:41] offset:0
v_and_b32 v36,0xffff0000,v27
v_lshlrev_b32 v37,16,v27
v_and_b32 v38,0xffff0000,v26
v_lshlrev_b32 v39,16,v26
v_fma_f32 v44,v36,s86,v44
v_fma_f32 v45,v37,s86,v45
v_fma_f32 v46,v38,s86,v46
v_fma_f32 v47,v39,s86,v47
v_cvt_pk_bf16_f32 v44,v44,v45
v_cvt_pk_bf16_f32 v45,v46,v47
ds_write_b64 v18,v[44:45] offset:512
s_branch Lcv_a3_%=
Lcv_a2_%=:
L 0
R 88,2,3584
R 92,2,3072
R 96,2,3648
R 100,2,3136
R 104,2,3712
R 108,2,3200
R 112,2,3776
R 116,2,3264
L 6
v_mfma_f32_16x16x32_bf16 v[40:43],a[0:3],v[88:91],0
R 120,2,3840
R 124,2,3328
v_mfma_f32_16x16x32_bf16 v[44:47],a[0:3],v[92:95],0
M 44,a[4:7],88
L 6
M 40,a[8:11],96
R 128,2,3904
R 132,2,3392
M 44,a[8:11],100
M 44,a[12:15],96
L 6
M 40,a[16:19],104
R 136,2,3968
R 140,2,3456
M 44,a[16:19],108
M 44,a[20:23],104
L 6
M 40,a[24:27],112
R 144,2,4032
R 148,2,3520
M 44,a[24:27],116
M 44,a[28:31],112
L 6
M 40,a[32:35],120
R 152,2,16512
R 156,2,16000
M 44,a[32:35],124
M 44,a[36:39],120
L 6
M 40,a[40:43],128
R 160,2,16576
R 164,2,16064
M 44,a[40:43],132
M 44,a[44:47],128
L 6
M 40,a[48:51],136
R 168,2,16640
R 172,2,16128
M 44,a[48:51],140
ds_read_b64 v[24:25],v3 offset:3584
ds_read_b64 v[26:27],v3 offset:3072
M 44,a[52:55],136
L 8
M 40,a[56:59],144
R 176,2,16704
R 180,2,16192
M 44,a[56:59],148
M 44,a[60:63],144
L 8
v_mfma_f32_16x16x32_bf16 v[48:51],a[64:67],v[152:155],0
R 184,2,16768
R 188,2,16256
v_mfma_f32_16x16x32_bf16 v[52:55],a[64:67],v[156:159],0
M 52,a[68:71],152
L 8
M 48,a[72:75],160
R 192,2,16832
R 196,2,16320
M 52,a[72:75],164
L 6
v_and_b32 v36,0xffff0000,v25
M 52,a[76:79],160
v_lshlrev_b32 v37,16,v25
v_and_b32 v38,0xffff0000,v24
v_lshlrev_b32 v39,16,v24
s_nop 0
M 48,a[80:83],168
R 200,2,16896
R 204,2,16384
M 52,a[80:83],172
v_fma_f32 v40,v36,s86,v40
v_fma_f32 v41,v37,s86,v41
M 52,a[84:87],168
v_fma_f32 v42,v38,s86,v42
v_fma_f32 v43,v39,s86,v43
v_cvt_pk_bf16_f32 v40,v40,v41
L 6
M 48,a[88:91],176
R 208,2,16960
R 212,2,16448
M 52,a[88:91],180
v_cvt_pk_bf16_f32 v41,v42,v43
ds_write_b64 v18,v[40:41] offset:0
M 52,a[92:95],176
v_and_b32 v36,0xffff0000,v27
v_lshlrev_b32 v37,16,v27
v_and_b32 v38,0xffff0000,v26
L 7
M 48,a[96:99],184
M 52,a[96:99],188
v_lshlrev_b32 v39,16,v26
v_fma_f32 v44,v36,s86,v44
M 52,a[100:103],184
v_fma_f32 v45,v37,s86,v45
v_fma_f32 v46,v38,s86,v46
v_fma_f32 v47,v39,s86,v47
L 5
M 48,a[104:107],192
M 52,a[104:107],196
v_cvt_pk_bf16_f32 v44,v44,v45
v_cvt_pk_bf16_f32 v45,v46,v47
M 52,a[108:111],192
ds_write_b64 v18,v[44:45] offset:512
L 4
M 48,a[112:115],200
ds_read_b64 v[24:25],v3 offset:16512
ds_read_b64 v[26:27],v3 offset:16000
M 52,a[112:115],204
M 52,a[116:119],200
L 4
M 48,a[120:123],208
M 52,a[120:123],212
M 52,a[124:127],208
L 0
v_and_b32 v36,0xffff0000,v25
v_lshlrev_b32 v37,16,v25
v_and_b32 v38,0xffff0000,v24
v_lshlrev_b32 v39,16,v24
s_nop 0
v_fma_f32 v48,v36,s87,v48
v_fma_f32 v49,v37,s87,v49
v_fma_f32 v50,v38,s87,v50
v_fma_f32 v51,v39,s87,v51
v_cvt_pk_bf16_f32 v48,v48,v49
v_cvt_pk_bf16_f32 v49,v50,v51
ds_write_b64 v18,v[48:49] offset:1024
v_and_b32 v36,0xffff0000,v27
v_lshlrev_b32 v37,16,v27
v_and_b32 v38,0xffff0000,v26
v_lshlrev_b32 v39,16,v26
v_fma_f32 v52,v36,s87,v52
v_fma_f32 v53,v37,s87,v53
v_fma_f32 v54,v38,s87,v54
v_fma_f32 v55,v39,s87,v55
v_cvt_pk_bf16_f32 v52,v52,v53
v_cvt_pk_bf16_f32 v53,v54,v55
ds_write_b64 v18,v[52:53] offset:1536
Lcv_a3_%=:
L 0
s_barrier
R 152,6,0
R 156,6,512
s_mov_b32 s78,98566144
L 1
buffer_store_dwordx4 v[152:155],v7,s[36:39],s78 offen offset:0 sc0 sc1
s_add_u32 s78,s56,99090432
L 0
buffer_store_dwordx4 v[156:159],v7,s[36:39],s78 offen offset:0 sc0 sc1
s_cmp_eq_u32 s83,1
s_cbranch_scc1 Lcv_b_%=
s_waitcnt vmcnt(0)
s_barrier
buffer_store_dword v23,v10,s[36:39],s48 offen offset:0 sc0 sc1
buffer_load_dword v13,v9,s[36:39],s58 offen offset:0 sc1
Lcv_bpb_%=:
)"
R"(s_waitcnt vmcnt(0)
v_cmp_eq_u32 vcc,s53,v13
s_add_u32 s82,s82,1
s_cmp_eq_u64 vcc,-1
s_cbranch_scc1 Lcv_bqb_%=
s_sleep 1
buffer_load_dword v13,v9,s[36:39],s58 offen offset:0 sc1
s_cmp_lt_u32 s82,20000
s_cbranch_scc1 Lcv_bpb_%=
s_waitcnt vmcnt(0)
Lcv_bqb_%=:
s_add_u32 m0,s57,0
s_nop 0
D 0,49,2048
D 0,49,1024
D 0,49,0
s_add_u32 m0,s57,12928
s_nop 0
D 0,55,2048
D 0,55,1024
D 0,55,0
s_waitcnt vmcnt(0)
Lcv_b_%=:
s_waitcnt vmcnt(2)
ds_read2_b32 v[88:89],v1 offset0:0 offset1:1
ds_read2_b32 v[90:91],v1 offset0:2 offset1:3
R 120,2,3584
ds_read2_b32 v[92:93],v1 offset0:128 offset1:129
ds_read2_b32 v[94:95],v1 offset0:130 offset1:131
R 124,2,3072
ds_read2_b32 v[96:97],v14 offset0:0 offset1:1
ds_read2_b32 v[98:99],v14 offset0:2 offset1:3
R 128,2,2560
ds_read2_b32 v[100:101],v14 offset0:128 offset1:129
ds_read2_b32 v[102:103],v14 offset0:130 offset1:131
R 132,2,2048
ds_read2_b32 v[104:105],v15 offset0:0 offset1:1
ds_read2_b32 v[106:107],v15 offset0:2 offset1:3
R 136,2,1536
ds_read2_b32 v[108:109],v15 offset0:128 offset1:129
ds_read2_b32 v[110:111],v15 offset0:130 offset1:131
R 140,2,1024
ds_read2_b32 v[112:113],v16 offset0:0 offset1:1
ds_read2_b32 v[114:115],v16 offset0:2 offset1:3
R 144,2,512
ds_read2_b32 v[116:117],v16 offset0:128 offset1:129
ds_read2_b32 v[118:119],v16 offset0:130 offset1:131
R 148,2,0
L 15
v_mfma_f32_16x16x32_bf16 v[40:43],v[88:91],v[128:131],0
ds_read2_b32 v[152:153],v1 offset0:16 offset1:17
M 40,v[92:95],124
ds_read2_b32 v[154:155],v1 offset0:18 offset1:19
M 40,v[96:99],120
L 14
R 184,2,3648
v_mfma_f32_16x16x32_bf16 v[44:47],v[88:91],v[132:135],0
ds_read2_b32 v[156:157],v1 offset0:144 offset1:145
M 44,v[92:95],128
M 44,v[96:99],124
ds_read2_b32 v[158:159],v1 offset0:146 offset1:147
M 44,v[100:103],120
L 14
R 188,2,3136
v_mfma_f32_16x16x32_bf16 v[48:51],v[88:91],v[136:139],0
ds_read2_b32 v[160:161],v14 offset0:16 offset1:17
M 48,v[92:95],132
ds_read2_b32 v[162:163],v14 offset0:18 offset1:19
M 48,v[96:99],128
M 48,v[100:103],124
R 192,2,2624
M 48,v[104:107],120
L 15
ds_read2_b32 v[164:165],v14 offset0:144 offset1:145
v_mfma_f32_16x16x32_bf16 v[52:55],v[88:91],v[140:143],0
ds_read2_b32 v[166:167],v14 offset0:146 offset1:147
M 52,v[92:95],136
R 196,2,2112
M 52,v[96:99],132
M 52,v[100:103],128
ds_read2_b32 v[168:169],v15 offset0:16 offset1:17
M 52,v[104:107],124
ds_read2_b32 v[170:171],v15 offset0:18 offset1:19
M 52,v[108:111],120
L 15
R 200,2,1600
v_mfma_f32_16x16x32_bf16 v[56:59],v[88:91],v[144:147],0
ds_read2_b32 v[172:173],v15 offset0:144 offset1:145
M 56,v[92:95],140
M 56,v[96:99],136
ds_read2_b32 v[174:175],v15 offset0:146 offset1:147
M 56,v[100:103],132
R 204,2,1088
M 56,v[104:107],128
ds_read2_b32 v[176:177],v16 offset0:16 offset1:17
M 56,v[108:111],124
ds_read2_b32 v[178:179],v16 offset0:18 offset1:19
M 56,v[112:115],120
L 15
v_mfma_f32_16x16x32_bf16 v[60:63],v[88:91],v[148:151],0
R 208,2,576
M 60,v[92:95],144
ds_read2_b32 v[180:181],v16 offset0:144 offset1:145
M 60,v[96:99],140
ds_read2_b32 v[182:183],v16 offset0:146 offset1:147
M 60,v[100:103],136
R 212,2,64
M 60,v[104:107],132
M 60,v[108:111],128
M 60,v[112:115],124
M 60,v[116:119],120
L 15
M 40,v[152:155],192
ds_read2_b32 v[88:89],v1 offset0:32 offset1:33
M 40,v[156:159],188
ds_read2_b32 v[90:91],v1 offset0:34 offset1:35
M 40,v[160:163],184
L 14
R 120,2,3712
M 44,v[152:155],196
ds_read2_b32 v[92:93],v1 offset0:160 offset1:161
M 44,v[156:159],192
M 44,v[160:163],188
ds_read2_b32 v[94:95],v1 offset0:162 offset1:163
M 44,v[164:167],184
L 14
R 124,2,3200
M 48,v[152:155],200
ds_read2_b32 v[96:97],v14 offset0:32 offset1:33
M 48,v[156:159],196
ds_read2_b32 v[98:99],v14 offset0:34 offset1:35
M 48,v[160:163],192
M 48,v[164:167],188
R 128,2,2688
M 48,v[168:171],184
L 15
ds_read2_b32 v[100:101],v14 offset0:160 offset1:161
M 52,v[152:155],204
ds_read2_b32 v[102:103],v14 offset0:162 offset1:163
M 52,v[156:159],200
R 132,2,2176
M 52,v[160:163],196
M 52,v[164:167],192
ds_read2_b32 v[104:105],v15 offset0:32 offset1:33
M 52,v[168:171],188
ds_read2_b32 v[106:107],v15 offset0:34 offset1:35
M 52,v[172:175],184
L 15
R 136,2,1664
M 56,v[152:155],208
ds_read2_b32 v[108:109],v15 offset0:160 offset1:161
M 56,v[156:159],204
M 56,v[160:163],200
ds_read2_b32 v[110:111],v15 offset0:162 offset1:163
M 56,v[164:167],196
R 140,2,1152
M 56,v[168:171],192
ds_read2_b32 v[112:113],v16 offset0:32 offset1:33
M 56,v[172:175],188
ds_read2_b32 v[114:115],v16 offset0:34 offset1:35
M 56,v[176:179],184
L 15
M 60,v[152:155],212
R 144,2,640
M 60,v[156:159],208
ds_read2_b32 v[116:117],v16 offset0:160 offset1:161
s_cmp_eq_u32 s83,0
s_cbranch_scc1 Lcv_c_%=
s_waitcnt vmcnt(0)
s_barrier
buffer_store_dword v23,v10,s[36:39],s48 offen offset:0 sc0 sc1
Lcv_c_%=:
M 60,v[160:163],204
ds_read2_b32 v[118:119],v16 offset0:162 offset1:163
M 60,v[164:167],200
R 148,2,128
M 60,v[168:171],196
M 60,v[172:175],192
M 60,v[176:179],188
M 60,v[180:183],184
L 15
M 40,v[88:91],128
ds_read2_b32 v[152:153],v1 offset0:48 offset1:49
M 40,v[92:95],124
ds_read2_b32 v[154:155],v1 offset0:50 offset1:51
M 40,v[96:99],120
L 14
R 184,2,3776
M 44,v[88:91],132
ds_read2_b32 v[156:157],v1 offset0:176 offset1:177
M 44,v[92:95],128
M 44,v[96:99],124
ds_read2_b32 v[158:159],v1 offset0:178 offset1:179
M 44,v[100:103],120
L 14
R 188,2,3264
M 48,v[88:91],136
ds_read2_b32 v[160:161],v14 offset0:48 offset1:49
M 48,v[92:95],132
ds_read2_b32 v[162:163],v14 offset0:50 offset1:51
M 48,v[96:99],128
M 48,v[100:103],124
R 192,2,2752
M 48,v[104:107],120
L 15
ds_read2_b32 v[164:165],v14 offset0:176 offset1:177
M 52,v[88:91],140
s_mov_b32 m0,163584
s_mov_b32 s78,0
ds_read2_b32 v[166:167],v14 offset0:178 offset1:179
M 52,v[92:95],136
R 196,2,2240
M 52,v[96:99],132
buffer_load_dword v11,s[40:43],s78 offen offset:0  lds
M 52,v[100:103],128
ds_read2_b32 v[168:169],v15 offset0:48 offset1:49
M 52,v[104:107],124
ds_read2_b32 v[170:171],v15 offset0:50 offset1:51
M 52,v[108:111],120
L 15
R 200,2,1728
M 56,v[88:91],144
ds_read2_b32 v[172:173],v15 offset0:176 offset1:177
M 56,v[92:95],140
M 56,v[96:99],136
ds_read2_b32 v[174:175],v15 offset0:178 offset1:179
M 56,v[100:103],132
R 204,2,1216
M 56,v[104:107],128
ds_read2_b32 v[176:177],v16 offset0:48 offset1:49
M 56,v[108:111],124
ds_read2_b32 v[178:179],v16 offset0:50 offset1:51
M 56,v[112:115],120
L 15
M 60,v[88:91],148
R 208,2,704
M 60,v[92:95],144
ds_read2_b32 v[180:181],v16 offset0:176 offset1:177
M 60,v[96:99],140
ds_read2_b32 v[182:183],v16 offset0:178 offset1:179
M 60,v[100:103],136
R 212,2,192
M 60,v[104:107],132
M 60,v[108:111],128
M 60,v[112:115],124
M 60,v[116:119],120
L 15
M 40,v[152:155],192
ds_read2_b32 v[88:89],v1 offset0:64 offset1:65
M 40,v[156:159],188
ds_read2_b32 v[90:91],v1 offset0:66 offset1:67
M 40,v[160:163],184
L 14
R 120,2,3840
M 44,v[152:155],196
ds_read2_b32 v[92:93],v1 offset0:192 offset1:193
M 44,v[156:159],192
M 44,v[160:163],188
ds_read2_b32 v[94:95],v1 offset0:194 offset1:195
M 44,v[164:167],184
L 14
R 124,2,3328
M 48,v[152:155],200
ds_read2_b32 v[96:97],v14 offset0:64 offset1:65
M 48,v[156:159],196
ds_read2_b32 v[98:99],v14 offset0:66 offset1:67
M 48,v[160:163],192
M 48,v[164:167],188
R 128,2,2816
M 48,v[168:171],184
L 15
ds_read2_b32 v[100:101],v14 offset0:192 offset1:193
M 52,v[152:155],204
ds_read2_b32 v[102:103],v14 offset0:194 offset1:195
M 52,v[156:159],200
R 132,2,2304
M 52,v[160:163],196
M 52,v[164:167],192
ds_read2_b32 v[104:105],v15 offset0:64 offset1:65
M 52,v[168:171],188
ds_read2_b32 v[106:107],v15 offset0:66 offset1:67
M 52,v[172:175],184
L 15
R 136,2,1792
M 56,v[152:155],208
ds_read2_b32 v[108:109],v15 offset0:192 offset1:193
M 56,v[156:159],204
M 56,v[160:163],200
ds_read2_b32 v[110:111],v15 offset0:194 offset1:195
M 56,v[164:167],196
R 140,2,1280
M 56,v[168:171],192
ds_read2_b32 v[112:113],v16 offset0:64 offset1:65
M 56,v[172:175],188
ds_read2_b32 v[114:115],v16 offset0:66 offset1:67
M 56,v[176:179],184
L 15
M 60,v[152:155],212
R 144,2,768
M 60,v[156:159],208
ds_read2_b32 v[116:117],v16 offset0:192 offset1:193
M 60,v[160:163],204
ds_read2_b32 v[118:119],v16 offset0:194 offset1:195
M 60,v[164:167],200
R 148,2,256
M 60,v[168:171],196
M 60,v[172:175],192
M 60,v[176:179],188
M 60,v[180:183],184
L 15
M 40,v[88:91],128
ds_read2_b32 v[152:153],v1 offset0:80 offset1:81
M 40,v[92:95],124
ds_read2_b32 v[154:155],v1 offset0:82 offset1:83
M 40,v[96:99],120
L 14
R 184,2,3904
M 44,v[88:91],132
ds_read2_b32 v[156:157],v1 offset0:208 offset1:209
M 44,v[92:95],128
M 44,v[96:99],124
ds_read2_b32 v[158:159],v1 offset0:210 offset1:211
M 44,v[100:103],120
L 14
R 188,2,3392
M 48,v[88:91],136
ds_read2_b32 v[160:161],v14 offset0:80 offset1:81
M 48,v[92:95],132
ds_read2_b32 v[162:163],v14 offset0:82 offset1:83
M 48,v[96:99],128
M 48,v[100:103],124
R 192,2,2880
M 48,v[104:107],120
L 15
ds_read2_b32 v[164:165],v14 offset0:208 offset1:209
M 52,v[88:91],140
ds_read2_b32 v[166:167],v14 offset0:210 offset1:211
M 52,v[92:95],136
R 196,2,2368
M 52,v[96:99],132
M 52,v[100:103],128
ds_read2_b32 v[168:169],v15 offset0:80 offset1:81
M 52,v[104:107],124
ds_read2_b32 v[170:171],v15 offset0:82 offset1:83
M 52,v[108:111],120
L 15
R 200,2,1856
M 56,v[88:91],144
ds_read2_b32 v[172:173],v15 offset0:208 offset1:209
M 56,v[92:95],140
M 56,v[96:99],136
ds_read2_b32 v[174:175],v15 offset0:210 offset1:211
M 56,v[100:103],132
R 204,2,1344
M 56,v[104:107],128
ds_read2_b32 v[176:177],v16 offset0:80 offset1:81
M 56,v[108:111],124
ds_read2_b32 v[178:179],v16 offset0:82 offset1:83
M 56,v[112:115],120
L 15
M 60,v[88:91],148
R 208,2,832
M 60,v[92:95],144
ds_read2_b32 v[180:181],v16 offset0:208 offset1:209
M 60,v[96:99],140
ds_read2_b32 v[182:183],v16 offset0:210 offset1:211
M 60,v[100:103],136
R 212,2,320
M 60,v[104:107],132
M 60,v[108:111],128
M 60,v[112:115],124
M 60,v[116:119],120
L 15
M 40,v[152:155],192
ds_read2_b32 v[88:89],v1 offset0:96 offset1:97
M 40,v[156:159],188
ds_read2_b32 v[90:91],v1 offset0:98 offset1:99
M 40,v[160:163],184
L 14
R 120,2,3968
M 44,v[152:155],196
s_mov_b32 m0,163584
s_mov_b32 s78,3741696
ds_read2_b32 v[92:93],v1 offset0:224 offset1:225
M 44,v[156:159],192
M 44,v[160:163],188
ds_read2_b32 v[94:95],v1 offset0:226 offset1:227
M 44,v[164:167],184
L 14
R 124,2,3456
M 48,v[152:155],200
ds_read2_b32 v[96:97],v14 offset0:96 offset1:97
M 48,v[156:159],196
ds_read2_b32 v[98:99],v14 offset0:98 offset1:99
M 48,v[160:163],192
buffer_load_dword v11,s[40:43],s78 offen offset:0  lds
M 48,v[164:167],188
R 128,2,2944
M 48,v[168:171],184
L 15
ds_read2_b32 v[100:101],v14 offset0:224 offset1:225
M 52,v[152:155],204
ds_read2_b32 v[102:103],v14 offset0:226 offset1:227
M 52,v[156:159],200
R 132,2,2432
M 52,v[160:163],196
M 52,v[164:167],192
ds_read2_b32 v[104:105],v15 offset0:96 offset1:97
M 52,v[168:171],188
ds_read2_b32 v[106:107],v15 offset0:98 offset1:99
M 52,v[172:175],184
L 15
R 136,2,1920
M 56,v[152:155],208
ds_read2_b32 v[108:109],v15 offset0:224 offset1:225
M 56,v[156:159],204
M 56,v[160:163],200
ds_read2_b32 v[110:111],v15 offset0:226 offset1:227
M 56,v[164:167],196
R 140,2,1408
M 56,v[168:171],192
ds_read2_b32 v[112:113],v16 offset0:96 offset1:97
M 56,v[172:175],188
ds_read2_b32 v[114:115],v16 offset0:98 offset1:99
M 56,v[176:179],184
L 15
M 60,v[152:155],212
R 144,2,896
M 60,v[156:159],208
ds_read2_b32 v[116:117],v16 offset0:224 offset1:225
M 60,v[160:163],204
ds_read2_b32 v[118:119],v16 offset0:226 offset1:227
M 60,v[164:167],200
R 148,2,384
M 60,v[168:171],196
M 60,v[172:175],192
M 60,v[176:179],188
M 60,v[180:183],184
L 15
M 40,v[88:91],128
ds_read2_b32 v[152:153],v1 offset0:112 offset1:113
M 40,v[92:95],124
ds_read2_b32 v[154:155],v1 offset0:114 offset1:115
M 40,v[96:99],120
L 14
R 184,2,4032
M 44,v[88:91],132
ds_read2_b32 v[156:157],v1 offset0:240 offset1:241
M 44,v[92:95],128
M 44,v[96:99],124
ds_read2_b32 v[158:159],v1 offset0:242 offset1:243
v_add_u32 v1,12928,v1
M 44,v[100:103],120
L 14
R 188,2,3520
M 48,v[88:91],136
ds_read2_b32 v[160:161],v14 offset0:112 offset1:113
M 48,v[92:95],132
ds_read2_b32 v[162:163],v14 offset0:114 offset1:115
M 48,v[96:99],128
M 48,v[100:103],124
R 192,2,3008
M 48,v[104:107],120
L 15
ds_read2_b32 v[164:165],v14 offset0:240 offset1:241
M 52,v[88:91],140
ds_read2_b32 v[166:167],v14 offset0:242 offset1:243
v_add_u32 v14,12928,v14
M 52,v[92:95],136
R 196,2,2496
M 52,v[96:99],132
M 52,v[100:103],128
ds_read2_b32 v[168:169],v15 offset0:112 offset1:113
M 52,v[104:107],124
ds_read2_b32 v[170:171],v15 offset0:114 offset1:115
M 52,v[108:111],120
L 15
R 200,2,1984
M 56,v[88:91],144
ds_read2_b32 v[172:173],v15 offset0:240 offset1:241
M 56,v[92:95],140
M 56,v[96:99],136
ds_read2_b32 v[174:175],v15 offset0:242 offset1:243
v_add_u32 v15,12928,v15
M 56,v[100:103],132
R 204,2,1472
M 56,v[104:107],128
ds_read2_b32 v[176:177],v16 offset0:112 offset1:113
M 56,v[108:111],124
ds_read2_b32 v[178:179],v16 offset0:114 offset1:115
M 56,v[112:115],120
L 15
M 60,v[88:91],148
R 208,2,960
M 60,v[92:95],144
ds_read2_b32 v[180:181],v16 offset0:240 offset1:241
M 60,v[96:99],140
ds_read2_b32 v[182:183],v16 offset0:242 offset1:243
v_add_u32 v16,12928,v16
M 60,v[100:103],136
)"
R"(R 212,2,448
M 60,v[104:107],132
M 60,v[108:111],128
M 60,v[112:115],124
M 60,v[116:119],120
L 15
M 40,v[152:155],192
ds_read2_b32 v[88:89],v1 offset0:0 offset1:1
M 40,v[156:159],188
ds_read2_b32 v[90:91],v1 offset0:2 offset1:3
M 40,v[160:163],184
L 14
R 120,2,16512
M 44,v[152:155],196
ds_read2_b32 v[92:93],v1 offset0:128 offset1:129
M 44,v[156:159],192
M 44,v[160:163],188
ds_read2_b32 v[94:95],v1 offset0:130 offset1:131
M 44,v[164:167],184
L 14
R 124,2,16000
M 48,v[152:155],200
ds_read2_b32 v[96:97],v14 offset0:0 offset1:1
M 48,v[156:159],196
ds_read2_b32 v[98:99],v14 offset0:2 offset1:3
M 48,v[160:163],192
M 48,v[164:167],188
R 128,2,15488
M 48,v[168:171],184
L 15
ds_read2_b32 v[100:101],v14 offset0:128 offset1:129
M 52,v[152:155],204
ds_read2_b32 v[102:103],v14 offset0:130 offset1:131
M 52,v[156:159],200
R 132,2,14976
M 52,v[160:163],196
M 52,v[164:167],192
ds_read2_b32 v[104:105],v15 offset0:0 offset1:1
M 52,v[168:171],188
ds_read2_b32 v[106:107],v15 offset0:2 offset1:3
M 52,v[172:175],184
L 15
R 136,2,14464
M 56,v[152:155],208
ds_read2_b32 v[108:109],v15 offset0:128 offset1:129
M 56,v[156:159],204
M 56,v[160:163],200
ds_read2_b32 v[110:111],v15 offset0:130 offset1:131
M 56,v[164:167],196
R 140,2,13952
M 56,v[168:171],192
ds_read2_b32 v[112:113],v16 offset0:0 offset1:1
M 56,v[172:175],188
ds_read2_b32 v[114:115],v16 offset0:2 offset1:3
M 56,v[176:179],184
L 15
M 60,v[152:155],212
R 144,2,13440
M 60,v[156:159],208
ds_read2_b32 v[116:117],v16 offset0:128 offset1:129
M 60,v[160:163],204
ds_read2_b32 v[118:119],v16 offset0:130 offset1:131
M 60,v[164:167],200
R 148,2,12928
M 60,v[168:171],196
M 60,v[172:175],192
M 60,v[176:179],188
M 60,v[180:183],184
s_cmp_eq_u32 s51,2
s_cbranch_scc1 Lcv_d_%=
ds_read_b64 v[24:25],v3 offset:2560
ds_read_b64 v[26:27],v3 offset:2048
ds_read_b64 v[28:29],v3 offset:1536
ds_read_b64 v[30:31],v3 offset:1024
ds_read_b64 v[32:33],v3 offset:512
ds_read_b64 v[34:35],v3 offset:0
L 0
v_and_b32 v36,0xffff0000,v25
v_lshlrev_b32 v37,16,v25
v_and_b32 v38,0xffff0000,v24
v_lshlrev_b32 v39,16,v24
v_fma_f32 v40,v36,s86,v40
v_fma_f32 v41,v37,s86,v41
v_fma_f32 v42,v38,s86,v42
v_fma_f32 v43,v39,s86,v43
v_cvt_pk_bf16_f32 v40,v40,v41
v_cvt_pk_bf16_f32 v41,v42,v43
ds_write_b64 v4,v[40:41] offset:0
v_and_b32 v36,0xffff0000,v27
v_lshlrev_b32 v37,16,v27
v_and_b32 v38,0xffff0000,v26
v_lshlrev_b32 v39,16,v26
v_fma_f32 v44,v36,s86,v44
v_fma_f32 v45,v37,s86,v45
v_fma_f32 v46,v38,s86,v46
v_fma_f32 v47,v39,s86,v47
v_cvt_pk_bf16_f32 v44,v44,v45
v_cvt_pk_bf16_f32 v45,v46,v47
ds_write_b64 v4,v[44:45] offset:512
v_and_b32 v36,0xffff0000,v29
v_lshlrev_b32 v37,16,v29
v_and_b32 v38,0xffff0000,v28
v_lshlrev_b32 v39,16,v28
v_fma_f32 v48,v36,s86,v48
v_fma_f32 v49,v37,s86,v49
v_fma_f32 v50,v38,s86,v50
v_fma_f32 v51,v39,s86,v51
v_cvt_pk_bf16_f32 v48,v48,v49
v_cvt_pk_bf16_f32 v49,v50,v51
ds_write_b64 v4,v[48:49] offset:1024
v_and_b32 v36,0xffff0000,v31
v_lshlrev_b32 v37,16,v31
v_and_b32 v38,0xffff0000,v30
v_lshlrev_b32 v39,16,v30
v_fma_f32 v52,v36,s86,v52
v_fma_f32 v53,v37,s86,v53
v_fma_f32 v54,v38,s86,v54
v_fma_f32 v55,v39,s86,v55
v_cvt_pk_bf16_f32 v52,v52,v53
v_cvt_pk_bf16_f32 v53,v54,v55
ds_write_b64 v4,v[52:53] offset:1536
v_and_b32 v36,0xffff0000,v33
v_lshlrev_b32 v37,16,v33
v_and_b32 v38,0xffff0000,v32
v_lshlrev_b32 v39,16,v32
v_fma_f32 v56,v36,s86,v56
v_fma_f32 v57,v37,s86,v57
v_fma_f32 v58,v38,s86,v58
v_fma_f32 v59,v39,s86,v59
v_cvt_pk_bf16_f32 v56,v56,v57
v_cvt_pk_bf16_f32 v57,v58,v59
ds_write_b64 v4,v[56:57] offset:2048
v_and_b32 v36,0xffff0000,v35
v_lshlrev_b32 v37,16,v35
v_and_b32 v38,0xffff0000,v34
v_lshlrev_b32 v39,16,v34
v_fma_f32 v60,v36,s86,v60
v_fma_f32 v61,v37,s86,v61
v_fma_f32 v62,v38,s86,v62
v_fma_f32 v63,v39,s86,v63
v_cvt_pk_bf16_f32 v60,v60,v61
v_cvt_pk_bf16_f32 v61,v62,v63
ds_write_b64 v4,v[60:61] offset:2560
s_branch Lcv_e_%=
Lcv_d_%=:
L 15
v_mfma_f32_16x16x32_bf16 v[64:67],v[88:91],v[128:131],0
ds_read2_b32 v[152:153],v1 offset0:16 offset1:17
ds_read_b64 v[24:25],v3 offset:2560
M 64,v[92:95],124
ds_read2_b32 v[154:155],v1 offset0:18 offset1:19
ds_read_b64 v[26:27],v3 offset:2048
M 64,v[96:99],120
L 15
R 184,2,16576
ds_read_b64 v[28:29],v3 offset:1536
v_mfma_f32_16x16x32_bf16 v[68:71],v[88:91],v[132:135],0
ds_read2_b32 v[156:157],v1 offset0:144 offset1:145
ds_read_b64 v[30:31],v3 offset:1024
M 68,v[92:95],128
ds_read_b64 v[32:33],v3 offset:512
ds_read_b64 v[34:35],v3 offset:0
M 68,v[96:99],124
ds_read2_b32 v[158:159],v1 offset0:146 offset1:147
M 68,v[100:103],120
L 15
R 188,2,16064
v_mfma_f32_16x16x32_bf16 v[72:75],v[88:91],v[136:139],0
ds_read2_b32 v[160:161],v14 offset0:16 offset1:17
M 72,v[92:95],132
s_mov_b32 m0,163584
s_mov_b32 s78,0
ds_read2_b32 v[162:163],v14 offset0:18 offset1:19
M 72,v[96:99],128
M 72,v[100:103],124
L 4
R 192,2,15552
v_and_b32 v36,0xffff0000,v25
M 72,v[104:107],120
s_nop 0
ds_read2_b32 v[164:165],v14 offset0:144 offset1:145
v_lshlrev_b32 v37,16,v25
v_mfma_f32_16x16x32_bf16 v[76:79],v[88:91],v[140:143],0
ds_read2_b32 v[166:167],v14 offset0:146 offset1:147
v_and_b32 v38,0xffff0000,v24
M 76,v[92:95],136
R 196,2,15040
v_lshlrev_b32 v39,16,v24
M 76,v[96:99],132
buffer_load_dword v12,s[60:63],s78 offen offset:0  lds
M 76,v[100:103],128
ds_read2_b32 v[168:169],v15 offset0:16 offset1:17
v_fma_f32 v40,v36,s86,v40
M 76,v[104:107],124
ds_read2_b32 v[170:171],v15 offset0:18 offset1:19
v_fma_f32 v41,v37,s86,v41
M 76,v[108:111],120
s_nop 0
R 200,2,14528
v_fma_f32 v42,v38,s86,v42
v_mfma_f32_16x16x32_bf16 v[80:83],v[88:91],v[144:147],0
ds_read2_b32 v[172:173],v15 offset0:144 offset1:145
v_fma_f32 v43,v39,s86,v43
M 80,v[92:95],140
v_cvt_pk_bf16_f32 v40,v40,v41
v_cvt_pk_bf16_f32 v41,v42,v43
M 80,v[96:99],136
ds_read2_b32 v[174:175],v15 offset0:146 offset1:147
v_and_b32 v36,0xffff0000,v27
M 80,v[100:103],132
R 204,2,14016
v_lshlrev_b32 v37,16,v27
M 80,v[104:107],128
ds_read2_b32 v[176:177],v16 offset0:16 offset1:17
v_and_b32 v38,0xffff0000,v26
M 80,v[108:111],124
ds_read2_b32 v[178:179],v16 offset0:18 offset1:19
v_lshlrev_b32 v39,16,v26
M 80,v[112:115],120
s_nop 0
ds_write_b64 v4,v[40:41] offset:0
v_mfma_f32_16x16x32_bf16 v[84:87],v[88:91],v[148:151],0
R 208,2,13504
v_fma_f32 v44,v36,s86,v44
M 84,v[92:95],144
ds_read2_b32 v[180:181],v16 offset0:144 offset1:145
v_fma_f32 v45,v37,s86,v45
M 84,v[96:99],140
ds_read2_b32 v[182:183],v16 offset0:146 offset1:147
v_fma_f32 v46,v38,s86,v46
M 84,v[100:103],136
R 212,2,12992
v_fma_f32 v47,v39,s86,v47
M 84,v[104:107],132
v_cvt_pk_bf16_f32 v44,v44,v45
v_cvt_pk_bf16_f32 v45,v46,v47
M 84,v[108:111],128
ds_write_b64 v4,v[44:45] offset:512
M 84,v[112:115],124
v_and_b32 v36,0xffff0000,v29
v_lshlrev_b32 v37,16,v29
M 84,v[116:119],120
L 15
v_and_b32 v38,0xffff0000,v28
v_lshlrev_b32 v39,16,v28
M 64,v[152:155],192
ds_read2_b32 v[88:89],v1 offset0:32 offset1:33
v_fma_f32 v48,v36,s86,v48
M 64,v[156:159],188
ds_read2_b32 v[90:91],v1 offset0:34 offset1:35
v_fma_f32 v49,v37,s86,v49
M 64,v[160:163],184
L 15
R 120,2,16640
v_fma_f32 v50,v38,s86,v50
M 68,v[152:155],196
ds_read2_b32 v[92:93],v1 offset0:160 offset1:161
v_fma_f32 v51,v39,s86,v51
M 68,v[156:159],192
v_cvt_pk_bf16_f32 v48,v48,v49
v_cvt_pk_bf16_f32 v49,v50,v51
M 68,v[160:163],188
ds_read2_b32 v[94:95],v1 offset0:162 offset1:163
v_and_b32 v36,0xffff0000,v31
M 68,v[164:167],184
L 15
R 124,2,16128
v_lshlrev_b32 v37,16,v31
M 72,v[152:155],200
ds_read2_b32 v[96:97],v14 offset0:32 offset1:33
v_and_b32 v38,0xffff0000,v30
M 72,v[156:159],196
ds_read2_b32 v[98:99],v14 offset0:34 offset1:35
v_lshlrev_b32 v39,16,v30
M 72,v[160:163],192
ds_write_b64 v4,v[48:49] offset:1024
M 72,v[164:167],188
R 128,2,15616
v_fma_f32 v52,v36,s86,v52
M 72,v[168:171],184
L 15
ds_read2_b32 v[100:101],v14 offset0:160 offset1:161
v_fma_f32 v53,v37,s86,v53
M 76,v[152:155],204
ds_read2_b32 v[102:103],v14 offset0:162 offset1:163
v_fma_f32 v54,v38,s86,v54
M 76,v[156:159],200
R 132,2,15104
v_fma_f32 v55,v39,s86,v55
M 76,v[160:163],196
v_cvt_pk_bf16_f32 v52,v52,v53
v_cvt_pk_bf16_f32 v53,v54,v55
M 76,v[164:167],192
ds_read2_b32 v[104:105],v15 offset0:32 offset1:33
v_and_b32 v36,0xffff0000,v33
M 76,v[168:171],188
ds_read2_b32 v[106:107],v15 offset0:34 offset1:35
v_lshlrev_b32 v37,16,v33
M 76,v[172:175],184
L 15
R 136,2,14592
v_and_b32 v38,0xffff0000,v32
M 80,v[152:155],208
ds_read2_b32 v[108:109],v15 offset0:160 offset1:161
v_lshlrev_b32 v39,16,v32
M 80,v[156:159],204
ds_write_b64 v4,v[52:53] offset:1536
M 80,v[160:163],200
ds_read2_b32 v[110:111],v15 offset0:162 offset1:163
v_fma_f32 v56,v36,s86,v56
M 80,v[164:167],196
R 140,2,14080
v_fma_f32 v57,v37,s86,v57
M 80,v[168:171],192
ds_read2_b32 v[112:113],v16 offset0:32 offset1:33
v_fma_f32 v58,v38,s86,v58
M 80,v[172:175],188
ds_read2_b32 v[114:115],v16 offset0:34 offset1:35
v_fma_f32 v59,v39,s86,v59
M 80,v[176:179],184
L 15
v_cvt_pk_bf16_f32 v56,v56,v57
v_cvt_pk_bf16_f32 v57,v58,v59
M 84,v[152:155],212
R 144,2,13568
v_and_b32 v36,0xffff0000,v35
M 84,v[156:159],208
ds_read2_b32 v[116:117],v16 offset0:160 offset1:161
v_lshlrev_b32 v37,16,v35
M 84,v[160:163],204
ds_read2_b32 v[118:119],v16 offset0:162 offset1:163
v_and_b32 v38,0xffff0000,v34
M 84,v[164:167],200
R 148,2,13056
v_lshlrev_b32 v39,16,v34
M 84,v[168:171],196
ds_write_b64 v4,v[56:57] offset:2048
M 84,v[172:175],192
v_fma_f32 v60,v36,s86,v60
v_fma_f32 v61,v37,s86,v61
M 84,v[176:179],188
v_fma_f32 v62,v38,s86,v62
v_fma_f32 v63,v39,s86,v63
M 84,v[180:183],184
L 15
v_cvt_pk_bf16_f32 v60,v60,v61
v_cvt_pk_bf16_f32 v61,v62,v63
M 64,v[88:91],128
ds_read2_b32 v[152:153],v1 offset0:48 offset1:49
M 64,v[92:95],124
ds_read2_b32 v[154:155],v1 offset0:50 offset1:51
M 64,v[96:99],120
L 15
R 184,2,16704
M 68,v[88:91],132
ds_read2_b32 v[156:157],v1 offset0:176 offset1:177
M 68,v[92:95],128
ds_write_b64 v4,v[60:61] offset:2560
M 68,v[96:99],124
ds_read2_b32 v[158:159],v1 offset0:178 offset1:179
M 68,v[100:103],120
L 15
R 188,2,16192
M 72,v[88:91],136
ds_read2_b32 v[160:161],v14 offset0:48 offset1:49
M 72,v[92:95],132
ds_read2_b32 v[162:163],v14 offset0:50 offset1:51
M 72,v[96:99],128
M 72,v[100:103],124
R 192,2,15680
M 72,v[104:107],120
L 15
ds_read2_b32 v[164:165],v14 offset0:176 offset1:177
M 76,v[88:91],140
ds_read2_b32 v[166:167],v14 offset0:178 offset1:179
M 76,v[92:95],136
R 196,2,15168
M 76,v[96:99],132
M 76,v[100:103],128
ds_read2_b32 v[168:169],v15 offset0:48 offset1:49
M 76,v[104:107],124
ds_read2_b32 v[170:171],v15 offset0:50 offset1:51
M 76,v[108:111],120
L 15
R 200,2,14656
M 80,v[88:91],144
ds_read2_b32 v[172:173],v15 offset0:176 offset1:177
M 80,v[92:95],140
M 80,v[96:99],136
ds_read2_b32 v[174:175],v15 offset0:178 offset1:179
M 80,v[100:103],132
R 204,2,14144
M 80,v[104:107],128
s_mov_b32 m0,163584
s_mov_b32 s78,1847296
ds_read2_b32 v[176:177],v16 offset0:48 offset1:49
M 80,v[108:111],124
ds_read2_b32 v[178:179],v16 offset0:50 offset1:51
M 80,v[112:115],120
L 15
buffer_load_dword v12,s[60:63],s78 offen offset:0  lds
M 84,v[88:91],148
R 208,2,13632
M 84,v[92:95],144
ds_read2_b32 v[180:181],v16 offset0:176 offset1:177
M 84,v[96:99],140
ds_read2_b32 v[182:183],v16 offset0:178 offset1:179
M 84,v[100:103],136
R 212,2,13120
M 84,v[104:107],132
M 84,v[108:111],128
M 84,v[112:115],124
M 84,v[116:119],120
L 15
M 64,v[152:155],192
ds_read2_b32 v[88:89],v1 offset0:64 offset1:65
M 64,v[156:159],188
ds_read2_b32 v[90:91],v1 offset0:66 offset1:67
M 64,v[160:163],184
L 14
R 120,2,16768
M 68,v[152:155],196
ds_read2_b32 v[92:93],v1 offset0:192 offset1:193
M 68,v[156:159],192
M 68,v[160:163],188
ds_read2_b32 v[94:95],v1 offset0:194 offset1:195
M 68,v[164:167],184
L 14
R 124,2,16256
M 72,v[152:155],200
ds_read2_b32 v[96:97],v14 offset0:64 offset1:65
M 72,v[156:159],196
ds_read2_b32 v[98:99],v14 offset0:66 offset1:67
M 72,v[160:163],192
M 72,v[164:167],188
R 128,2,15744
M 72,v[168:171],184
L 15
ds_read2_b32 v[100:101],v14 offset0:192 offset1:193
M 76,v[152:155],204
ds_read2_b32 v[102:103],v14 offset0:194 offset1:195
M 76,v[156:159],200
R 132,2,15232
M 76,v[160:163],196
M 76,v[164:167],192
ds_read2_b32 v[104:105],v15 offset0:64 offset1:65
M 76,v[168:171],188
ds_read2_b32 v[106:107],v15 offset0:66 offset1:67
M 76,v[172:175],184
L 15
R 136,2,14720
M 80,v[152:155],208
ds_read2_b32 v[108:109],v15 offset0:192 offset1:193
M 80,v[156:159],204
M 80,v[160:163],200
ds_read2_b32 v[110:111],v15 offset0:194 offset1:195
M 80,v[164:167],196
R 140,2,14208
M 80,v[168:171],192
ds_read2_b32 v[112:113],v16 offset0:64 offset1:65
M 80,v[172:175],188
ds_read2_b32 v[114:115],v16 offset0:66 offset1:67
M 80,v[176:179],184
L 15
M 84,v[152:155],212
R 144,2,13696
M 84,v[156:159],208
ds_read2_b32 v[116:117],v16 offset0:192 offset1:193
M 84,v[160:163],204
ds_read2_b32 v[118:119],v16 offset0:194 offset1:195
M 84,v[164:167],200
R 148,2,13184
M 84,v[168:171],196
M 84,v[172:175],192
M 84,v[176:179],188
M 84,v[180:183],184
L 15
M 64,v[88:91],128
ds_read2_b32 v[152:153],v1 offset0:80 offset1:81
M 64,v[92:95],124
ds_read2_b32 v[154:155],v1 offset0:82 offset1:83
M 64,v[96:99],120
L 14
R 184,2,16832
M 68,v[88:91],132
ds_read2_b32 v[156:157],v1 offset0:208 offset1:209
M 68,v[92:95],128
M 68,v[96:99],124
ds_read2_b32 v[158:159],v1 offset0:210 offset1:211
M 68,v[100:103],120
L 14
R 188,2,16320
M 72,v[88:91],136
ds_read2_b32 v[160:161],v14 offset0:80 offset1:81
M 72,v[92:95],132
ds_read2_b32 v[162:163],v14 offset0:82 offset1:83
M 72,v[96:99],128
M 72,v[100:103],124
R 192,2,15808
M 72,v[104:107],120
)"
R"(L 15
ds_read2_b32 v[164:165],v14 offset0:208 offset1:209
M 76,v[88:91],140
ds_read2_b32 v[166:167],v14 offset0:210 offset1:211
M 76,v[92:95],136
R 196,2,15296
M 76,v[96:99],132
M 76,v[100:103],128
ds_read2_b32 v[168:169],v15 offset0:80 offset1:81
M 76,v[104:107],124
ds_read2_b32 v[170:171],v15 offset0:82 offset1:83
M 76,v[108:111],120
L 15
R 200,2,14784
M 80,v[88:91],144
ds_read2_b32 v[172:173],v15 offset0:208 offset1:209
M 80,v[92:95],140
M 80,v[96:99],136
ds_read2_b32 v[174:175],v15 offset0:210 offset1:211
M 80,v[100:103],132
R 204,2,14272
M 80,v[104:107],128
ds_read2_b32 v[176:177],v16 offset0:80 offset1:81
M 80,v[108:111],124
ds_read2_b32 v[178:179],v16 offset0:82 offset1:83
M 80,v[112:115],120
L 15
M 84,v[88:91],148
R 208,2,13760
M 84,v[92:95],144
ds_read2_b32 v[180:181],v16 offset0:208 offset1:209
M 84,v[96:99],140
ds_read2_b32 v[182:183],v16 offset0:210 offset1:211
M 84,v[100:103],136
R 212,2,13248
M 84,v[104:107],132
M 84,v[108:111],128
M 84,v[112:115],124
M 84,v[116:119],120
L 15
M 64,v[152:155],192
ds_read2_b32 v[88:89],v1 offset0:96 offset1:97
M 64,v[156:159],188
ds_read2_b32 v[90:91],v1 offset0:98 offset1:99
M 64,v[160:163],184
L 14
R 120,2,16896
M 68,v[152:155],196
ds_read2_b32 v[92:93],v1 offset0:224 offset1:225
M 68,v[156:159],192
M 68,v[160:163],188
ds_read2_b32 v[94:95],v1 offset0:226 offset1:227
M 68,v[164:167],184
L 14
R 124,2,16384
M 72,v[152:155],200
ds_read2_b32 v[96:97],v14 offset0:96 offset1:97
M 72,v[156:159],196
ds_read2_b32 v[98:99],v14 offset0:98 offset1:99
M 72,v[160:163],192
M 72,v[164:167],188
R 128,2,15872
M 72,v[168:171],184
L 15
ds_read2_b32 v[100:101],v14 offset0:224 offset1:225
M 76,v[152:155],204
ds_read2_b32 v[102:103],v14 offset0:226 offset1:227
M 76,v[156:159],200
R 132,2,15360
M 76,v[160:163],196
M 76,v[164:167],192
ds_read2_b32 v[104:105],v15 offset0:96 offset1:97
M 76,v[168:171],188
ds_read2_b32 v[106:107],v15 offset0:98 offset1:99
M 76,v[172:175],184
L 15
R 136,2,14848
M 80,v[152:155],208
ds_read2_b32 v[108:109],v15 offset0:224 offset1:225
M 80,v[156:159],204
M 80,v[160:163],200
ds_read2_b32 v[110:111],v15 offset0:226 offset1:227
M 80,v[164:167],196
R 140,2,14336
M 80,v[168:171],192
ds_read2_b32 v[112:113],v16 offset0:96 offset1:97
M 80,v[172:175],188
ds_read2_b32 v[114:115],v16 offset0:98 offset1:99
M 80,v[176:179],184
L 15
M 84,v[152:155],212
R 144,2,13824
M 84,v[156:159],208
ds_read2_b32 v[116:117],v16 offset0:224 offset1:225
M 84,v[160:163],204
ds_read2_b32 v[118:119],v16 offset0:226 offset1:227
M 84,v[164:167],200
R 148,2,13312
M 84,v[168:171],196
M 84,v[172:175],192
M 84,v[176:179],188
M 84,v[180:183],184
L 15
M 64,v[88:91],128
ds_read2_b32 v[152:153],v1 offset0:112 offset1:113
M 64,v[92:95],124
ds_read2_b32 v[154:155],v1 offset0:114 offset1:115
M 64,v[96:99],120
L 14
R 184,2,16960
M 68,v[88:91],132
ds_read2_b32 v[156:157],v1 offset0:240 offset1:241
M 68,v[92:95],128
M 68,v[96:99],124
ds_read2_b32 v[158:159],v1 offset0:242 offset1:243
M 68,v[100:103],120
L 14
R 188,2,16448
M 72,v[88:91],136
ds_read2_b32 v[160:161],v14 offset0:112 offset1:113
M 72,v[92:95],132
ds_read2_b32 v[162:163],v14 offset0:114 offset1:115
M 72,v[96:99],128
M 72,v[100:103],124
R 192,2,15936
M 72,v[104:107],120
L 15
ds_read2_b32 v[164:165],v14 offset0:240 offset1:241
M 76,v[88:91],140
ds_read2_b32 v[166:167],v14 offset0:242 offset1:243
M 76,v[92:95],136
R 196,2,15424
M 76,v[96:99],132
M 76,v[100:103],128
ds_read2_b32 v[168:169],v15 offset0:112 offset1:113
M 76,v[104:107],124
ds_read2_b32 v[170:171],v15 offset0:114 offset1:115
M 76,v[108:111],120
L 15
R 200,2,14912
M 80,v[88:91],144
ds_read2_b32 v[172:173],v15 offset0:240 offset1:241
M 80,v[92:95],140
M 80,v[96:99],136
ds_read2_b32 v[174:175],v15 offset0:242 offset1:243
M 80,v[100:103],132
R 204,2,14400
M 80,v[104:107],128
ds_read2_b32 v[176:177],v16 offset0:112 offset1:113
M 80,v[108:111],124
ds_read2_b32 v[178:179],v16 offset0:114 offset1:115
M 80,v[112:115],120
L 15
M 84,v[88:91],148
R 208,2,13888
M 84,v[92:95],144
ds_read2_b32 v[180:181],v16 offset0:240 offset1:241
M 84,v[96:99],140
ds_read2_b32 v[182:183],v16 offset0:242 offset1:243
M 84,v[100:103],136
R 212,2,13376
M 84,v[104:107],132
M 84,v[108:111],128
M 84,v[112:115],124
M 84,v[116:119],120
L 15
M 64,v[152:155],192
M 64,v[156:159],188
M 64,v[160:163],184
L 12
M 68,v[152:155],196
M 68,v[156:159],192
M 68,v[160:163],188
M 68,v[164:167],184
L 9
M 72,v[152:155],200
M 72,v[156:159],196
M 72,v[160:163],192
ds_read_b64 v[24:25],v3 offset:15488
ds_read_b64 v[26:27],v3 offset:14976
M 72,v[164:167],188
ds_read_b64 v[28:29],v3 offset:14464
ds_read_b64 v[30:31],v3 offset:13952
M 72,v[168:171],184
L 10
ds_read_b64 v[32:33],v3 offset:13440
ds_read_b64 v[34:35],v3 offset:12928
M 76,v[152:155],204
M 76,v[156:159],200
M 76,v[160:163],196
M 76,v[164:167],192
M 76,v[168:171],188
M 76,v[172:175],184
L 9
M 80,v[152:155],208
M 80,v[156:159],204
M 80,v[160:163],200
M 80,v[164:167],196
M 80,v[168:171],192
M 80,v[172:175],188
M 80,v[176:179],184
L 6
M 84,v[152:155],212
M 84,v[156:159],208
M 84,v[160:163],204
M 84,v[164:167],200
M 84,v[168:171],196
M 84,v[172:175],192
M 84,v[176:179],188
M 84,v[180:183],184
L 0
v_and_b32 v36,0xffff0000,v25
v_lshlrev_b32 v37,16,v25
v_and_b32 v38,0xffff0000,v24
v_lshlrev_b32 v39,16,v24
v_fma_f32 v64,v36,s87,v64
v_fma_f32 v65,v37,s87,v65
v_fma_f32 v66,v38,s87,v66
v_fma_f32 v67,v39,s87,v67
v_cvt_pk_bf16_f32 v64,v64,v65
v_cvt_pk_bf16_f32 v65,v66,v67
ds_write_b64 v4,v[64:65] offset:12928
v_and_b32 v36,0xffff0000,v27
v_lshlrev_b32 v37,16,v27
v_and_b32 v38,0xffff0000,v26
v_lshlrev_b32 v39,16,v26
v_fma_f32 v68,v36,s87,v68
v_fma_f32 v69,v37,s87,v69
v_fma_f32 v70,v38,s87,v70
v_fma_f32 v71,v39,s87,v71
v_cvt_pk_bf16_f32 v68,v68,v69
v_cvt_pk_bf16_f32 v69,v70,v71
ds_write_b64 v4,v[68:69] offset:13440
v_and_b32 v36,0xffff0000,v29
v_lshlrev_b32 v37,16,v29
v_and_b32 v38,0xffff0000,v28
v_lshlrev_b32 v39,16,v28
v_fma_f32 v72,v36,s87,v72
v_fma_f32 v73,v37,s87,v73
v_fma_f32 v74,v38,s87,v74
v_fma_f32 v75,v39,s87,v75
v_cvt_pk_bf16_f32 v72,v72,v73
v_cvt_pk_bf16_f32 v73,v74,v75
ds_write_b64 v4,v[72:73] offset:13952
v_and_b32 v36,0xffff0000,v31
v_lshlrev_b32 v37,16,v31
v_and_b32 v38,0xffff0000,v30
v_lshlrev_b32 v39,16,v30
v_fma_f32 v76,v36,s87,v76
v_fma_f32 v77,v37,s87,v77
v_fma_f32 v78,v38,s87,v78
v_fma_f32 v79,v39,s87,v79
v_cvt_pk_bf16_f32 v76,v76,v77
v_cvt_pk_bf16_f32 v77,v78,v79
ds_write_b64 v4,v[76:77] offset:14464
v_and_b32 v36,0xffff0000,v33
v_lshlrev_b32 v37,16,v33
v_and_b32 v38,0xffff0000,v32
v_lshlrev_b32 v39,16,v32
v_fma_f32 v80,v36,s87,v80
v_fma_f32 v81,v37,s87,v81
v_fma_f32 v82,v38,s87,v82
v_fma_f32 v83,v39,s87,v83
v_cvt_pk_bf16_f32 v80,v80,v81
v_cvt_pk_bf16_f32 v81,v82,v83
ds_write_b64 v4,v[80:81] offset:14976
v_and_b32 v36,0xffff0000,v35
v_lshlrev_b32 v37,16,v35
v_and_b32 v38,0xffff0000,v34
v_lshlrev_b32 v39,16,v34
v_fma_f32 v84,v36,s87,v84
v_fma_f32 v85,v37,s87,v85
v_fma_f32 v86,v38,s87,v86
v_fma_f32 v87,v39,s87,v87
v_cvt_pk_bf16_f32 v84,v84,v85
v_cvt_pk_bf16_f32 v85,v86,v87
ds_write_b64 v4,v[84:85] offset:15488
Lcv_e_%=:
L 0
s_barrier
R 152,5,0
R 156,5,512
R 160,5,1024
R 164,5,1536
R 168,5,2048
R 172,5,2560
s_mov_b32 s78,99614720
L 5
buffer_store_dwordx4 v[152:155],v7,s[36:39],s78 offen offset:0 sc0 sc1
s_add_u32 s78,s56,100139008
L 4
buffer_store_dwordx4 v[156:159],v7,s[36:39],s78 offen offset:0 sc0 sc1
s_mov_b32 s78,100663296
L 3
buffer_store_dwordx4 v[160:163],v7,s[36:39],s78 offen offset:0 sc0 sc1
s_add_u32 s78,s56,101187584
L 2
buffer_store_dwordx4 v[164:167],v7,s[36:39],s78 offen offset:0 sc0 sc1
s_mov_b32 s78,101711872
L 1
buffer_store_dwordx4 v[168:171],v7,s[36:39],s78 offen offset:0 sc0 sc1
s_add_u32 s78,s56,102236160
L 0
buffer_store_dwordx4 v[172:175],v7,s[36:39],s78 offen offset:0 sc0 sc1
s_waitcnt vmcnt(0)
s_barrier
s_add_u32 s48,s48,32768
buffer_store_dword v23,v10,s[36:39],s48 offen offset:0 sc0 sc1
s_cmp_eq_u32 s51,2
s_cbranch_scc0 Lcv_f_%=
s_mov_b32 m0,163584
s_mov_b32 s78,3694592
buffer_load_dword v12,s[60:63],s78 offen offset:0  lds
s_mov_b32 m0,163584
s_mov_b32 s78,5541888
buffer_load_dword v12,s[60:63],s78 offen offset:0  lds
Lcv_f_%=:
Lcode_cv_e:
.purgem M
.purgem R
.purgem T
.purgem D
.purgem L
)"
  :: "s"(ws), "s"(sWave), "s"(sBuf), "s"(sAuxOff), "s"(sFoutOff), "s"(sC0), "s"(sC1), "s"(sExp), "v"(vL16), "v"(vPA), "v"(vPB), "v"(vPU), "v"(vPW), "v"(vOR), "v"(vORA), "v"(vOG), "v"(vFA), "v"(vFB), "v"(vFlag), "v"(vTW), "v"(vTX), "s"(w3), "s"(w4) : "v0", "v1", "v2", "v3", "v4", "v5", "v6", "v7", "v8", "v9", "v10", "v11", "v12", "v13", "v14", "v15", "v16", "v17", "v18", "v19", "v20", "v21", "v22", "v23", "v24", "v25", "v26", "v27", "v28", "v29", "v30", "v31", "v32", "v33", "v34", "v35", "v36", "v37", "v38", "v39", "v40", "v41", "v42", "v43", "v44", "v45", "v46", "v47", "v48", "v49", "v50", "v51", "v52", "v53", "v54", "v55", "v56", "v57", "v58", "v59", "v60", "v61", "v62", "v63", "v64", "v65", "v66", "v67", "v68", "v69", "v70", "v71", "v72", "v73", "v74", "v75", "v76", "v77", "v78", "v79", "v80", "v81", "v82", "v83", "v84", "v85", "v86", "v87", "v88", "v89", "v90", "v91", "v92", "v93", "v94", "v95", "v96", "v97", "v98", "v99", "v100", "v101", "v102", "v103", "v104", "v105", "v106", "v107", "v108", "v109", "v110", "v111", "v112", "v113", "v114", "v115", "v116", "v117", "v118", "v119", "v120", "v121", "v122", "v123", "v124", "v125", "v126", "v127", "v128", "v129", "v130", "v131", "v132", "v133", "v134", "v135", "v136", "v137", "v138", "v139", "v140", "v141", "v142", "v143", "v144", "v145", "v146", "v147", "v148", "v149", "v150", "v151", "v152", "v153", "v154", "v155", "v156", "v157", "v158", "v159", "v160", "v161", "v162", "v163", "v164", "v165", "v166", "v167", "v168", "v169", "v170", "v171", "v172", "v173", "v174", "v175", "v176", "v177", "v178", "v179", "v180", "v181", "v182", "v183", "v184", "v185", "v186", "v187", "v188", "v189", "v190", "v191", "v192", "v193", "v194", "v195", "v196", "v197", "v198", "v199", "v200", "v201", "v202", "v203", "v204", "v205", "v206", "v207", "v208", "v209", "v210", "v211", "v212", "v213", "v214", "v215", "v216", "v217", "v218", "v219", "v220", "v221", "v222", "v223", "v224", "v225", "v226", "v227", "v228", "v229", "v230", "v231", "a0", "a1", "a2", "a3", "a4", "a5", "a6", "a7", "a8", "a9", "a10", "a11", "a12", "a13", "a14", "a15", "a16", "a17", "a18", "a19", "a20", "a21", "a22", "a23", "a24", "a25", "a26", "a27", "a28", "a29", "a30", "a31", "a32", "a33", "a34", "a35", "a36", "a37", "a38", "a39", "a40", "a41", "a42", "a43", "a44", "a45", "a46", "a47", "a48", "a49", "a50", "a51", "a52", "a53", "a54", "a55", "a56", "a57", "a58", "a59", "a60", "a61", "a62", "a63", "a64", "a65", "a66", "a67", "a68", "a69", "a70", "a71", "a72", "a73", "a74", "a75", "a76", "a77", "a78", "a79", "a80", "a81", "a82", "a83", "a84", "a85", "a86", "a87", "a88", "a89", "a90", "a91", "a92", "a93", "a94", "a95", "a96", "a97", "a98", "a99", "a100", "a101", "a102", "a103", "a104", "a105", "a106", "a107", "a108", "a109", "a110", "a111", "a112", "a113", "a114", "a115", "a116", "a117", "a118", "a119", "a120", "a121", "a122", "a123", "a124", "a125", "a126", "a127", "a128", "a129", "a130", "a131", "a132", "a133", "a134", "a135", "a136", "a137", "a138", "a139", "a140", "a141", "a142", "a143", "a144", "a145", "a146", "a147", "a148", "a149", "a150", "a151", "a152", "a153", "a154", "a155", "a156", "a157", "a158", "a159", "a160", "a161", "a162", "a163", "a164", "a165", "a166", "a167", "a168", "a169", "a170", "a171", "a172", "a173", "a174", "a175", "a176", "a177", "a178", "a179", "a180", "a181", "a182", "a183", "a184", "a185", "a186", "a187", "a188", "a189", "a190", "a191", "a192", "a193", "a194", "a195", "a196", "a197", "a198", "a199", "a200", "a201", "a202", "a203", "a204", "a205", "a206", "a207", "a208", "a209", "a210", "a211", "a212", "a213", "a214", "a215", "a216", "a217", "a218", "a219", "a220", "a221", "a222", "a223", "a224", "a225", "a226", "a227", "a228", "a229", "a230", "a231", "a232", "a233", "a234", "a235", "a236", "a237", "a238", "a239", "a240", "a241", "a242", "a243", "a244", "a245", "a246", "a247", "a248", "a249", "a250", "a251", "a252", "a253", "a254", "a255", "s36", "s37", "s38", "s39", "s40", "s41", "s42", "s43", "s44", "s45", "s46", "s47", "s48", "s49", "s50", "s51", "s52", "s53", "s54", "s55", "s56", "s57", "s58", "s59", "s60", "s61", "s62", "s63", "s64", "s65", "s66", "s67", "s68", "s69", "s70", "s71", "s72", "s73", "s74", "s75", "s76", "s77", "s78", "s79", "s80", "s81", "s82", "s83", "s84", "s85", "s86", "s87", "s88", "s89", "s90", "s91", "s92", "s93", "s94", "s95", "s96", "s97", "vcc", "scc", "memory");
    LANE_ID(lq_);
    const int g = QT0 + (NCV - 1 - iw) * 164 + min(wave * 64 + lq_, 163);
    if (QTB) touch_lds((const char*)q + (size_t)(g < 32768 ? g : 0) * 128, sScr);
  }
  WAIT_LGKM0();
  __builtin_amdgcn_s_barrier();
  CTS(10);
  }
  const unsigned sDbg0 = 110u * 1048576u + (b * 4 + wave) * 8192;
  constexpr unsigned OOB = 0x80000000u;
#define HLP 0
  if (kind == 6 || (kind == 4 && j4 < HLP)) {
    LANE_ID(lr);
    const int r8 = lr & 7, seg = lr >> 3, j = lr & 15, g = lr >> 4;
    const unsigned sWave = wave, sExp = __builtin_amdgcn_readfirstlane(ep), sG0 = kind == 6 ? slice : 64 + j4, sStep = NRW, sOutOff = kind == 6 ? 256 : 65 + j4, sAuxOff = HLP, sBuf = kind & 2, sRk = slice;
    const unsigned sDbg = sDbg0 + (kind == 6 ? 0 : 1024);
    const unsigned vRF = (wave * 8 + (seg >> 2)) * 1024 + ((seg & 3) * 16 + r8) * 16;
    const unsigned vRwr = seg == 0 ? r8 * 64 + wave * 4 : 512 + lr * 4, vRrd = r8 * 64;
    const unsigned vRP = g == (j >> 2) ? r8 * 64 + 16 + wave * 8 + (seg & 1) * 4 : 768 + lr * 4;
    const unsigned vRS = (wave == 0 && seg < 2) ? r8 * 16 + seg * 256 : OOB, vFlag = lr < 32 ? lr * 4 : OOB;
    const unsigned vL4 = ((lr & 2) * 4 + wave * 2 + (lr & 1)) * 128, vLY = min((wave < 3 ? wave * 32 : 112) + lr, wave < 2 ? wave * 32 + 31 : wave == 2 ? 111 : NCV - 1) * 128;
    const unsigned vQ = r8 * 2048 + wave * 512 + seg * 16;
    const unsigned vY0 = wave * 4096 + lr * 16;
    const unsigned vTr = 4096 + wave * 4096 + ((2 * g + ((j & 3) >> 1)) * 8 + (j >> 2)) * 16 + (j & 1) * 8;
    const unsigned one = g == (j >> 2) ? 0x3f80u << (16 * (j & 1)) : 0u, vSa = (j & 2) ? 0u : one, vSb = (j & 2) ? one : 0u;
    (void)sDbg;
    asm volatile(
R"(.macro M d,w,x
v_mfma_f32_16x16x32_bf16 v[\d:\d+3], \w, v[\x:\x+3], v[\d:\d+3]
.endm
.macro R d,a,o
ds_read_b128 v[\d:\d+3], v\a offset:\o
.endm
.macro T d,a,o
ds_read_b64_tr_b16 \d, v\a offset:\o
.endm
.macro D a,s,o
buffer_load_dwordx4 v\a, s[36:39], s\s offen offset:\o sc1 lds
.endm
.macro L c
s_waitcnt lgkmcnt(\c)
.endm
Lcode_r_b:
s_mov_b64 s[36:37],%0
s_mov_b32 s38,111243392
s_mov_b32 s39,0x00020000
s_mov_b32 s44,%1
s_mov_b32 s45,%2
s_mov_b32 s46,%3
s_mov_b32 s47,%4
s_mov_b32 s49,%5
s_mov_b32 s51,%6
s_mov_b32 s93,%7
s_mov_b32 s53,%8
v_mov_b32 v0,%9
v_mov_b32 v1,%10
v_mov_b32 v2,%11
v_mov_b32 v3,%12
v_mov_b32 v4,%13
v_mov_b32 v5,%14
v_mov_b32 v6,%15
v_mov_b32 v7,%16
v_mov_b32 v8,%17
v_mov_b32 v9,%18
v_mov_b32 v10,%19
v_mov_b32 v11,%20
v_mov_b32 v12,%21
s_mov_b64 s[84:85],%22
s_mov_b32 s86,4194304
s_mov_b32 s87,0x00020000
s_lshl_b32 s55,s45,14
buffer_load_dwordx4 v[164:167],v8,s[84:87],s55 offen offset:0 nt
buffer_load_dwordx4 v[168:171],v8,s[84:87],s55 offen offset:128 nt
buffer_load_dwordx4 v[172:175],v8,s[84:87],s55 offen offset:256 nt
buffer_load_dwordx4 v[176:179],v8,s[84:87],s55 offen offset:384 nt
v_mov_b32 v13,s53
v_mbcnt_lo_u32_b32 v15,-1,0
v_mbcnt_hi_u32_b32 v15,-1,v15
v_lshlrev_b32 v15,4,v15
s_mov_b32 s82,0
s_mov_b32 s63,0x7fff0000
s_mov_b32 s64,0
s_mov_b32 s67,0
s_lshl_b32 s65,s44,12
s_add_u32 s65,s65,4096
s_lshl_b32 s66,s44,7
s_add_u32 s66,s66,109056000
s_mov_b32 s70,0xaaaaaaaa
s_mov_b32 s71,0xaaaaaaaa
s_mov_b32 s72,0xcccccccc
s_mov_b32 s73,0xcccccccc
s_mov_b32 s74,0xff00ff00
s_mov_b32 s75,0xff00ff00
s_mov_b32 s68,0x3a800000
s_mov_b32 s69,0x3727c5ac
s_mov_b32 s76,0xc0135761
s_mov_b32 s77,0x3d372713
v_mov_b32 v32,0x3f803f80
v_mov_b32 v33,0x3f803f80
v_mov_b32 v34,0x3f803f80
v_mov_b32 v35,0x3f803f80
v_mov_b32 v24,0
v_mov_b32 v25,0
v_mov_b32 v26,v11
v_mov_b32 v27,v12
v_mov_b32 v28,0
v_mov_b32 v29,0
s_waitcnt vmcnt(0)
Lr_a0_%=:
s_waitcnt vmcnt(10)
v_mfma_f32_16x16x32_bf16 v[36:39],v[32:35],v[164:167],0
s_add_u32 s79,s45,s46
s_cmp_lt_u32 s45,64
s_cselect_b32 s78,s49,0
v_mfma_f32_16x16x32_bf16 v[40:43],v[164:167],v[164:167],0
s_cmp_ge_u32 s79,64
s_cselect_b32 s78,s78,0
s_add_u32 s62,s79,s78
M 36,v[32:35],168
s_lshr_b32 s78,s45,1
s_and_b32 s79,s45,1
s_lshl_b32 s79,s79,7
M 40,v[168:171],168
s_lshl_b32 s57,s78,15
s_lshl_b32 s55,s62,14
s_cmp_lt_u32 s62,s47
M 36,v[32:35],172
s_cselect_b32 s55,s55,0x7fff0000
buffer_load_dwordx4 v[196:199],v8,s[84:87],s55 offen offset:0 nt
M 40,v[172:175],172
s_add_u32 s57,s57,s79
buffer_load_dwordx4 v[200:203],v8,s[84:87],s55 offen offset:128 nt
M 36,v[32:35],176
s_lshl_b32 s59,s78,9
buffer_load_dwordx4 v[204:207],v8,s[84:87],s55 offen offset:256 nt
M 40,v[176:179],176
s_add_u32 s57,s57,8388608
buffer_load_dwordx4 v[208:211],v8,s[84:87],s55 offen offset:384 nt
v_mfma_f32_16x16x32_bf16 v[116:119],v[26:29],v[164:167],0
s_add_u32 s59,s59,s79
s_lshl_b32 s60,s45,9
s_add_u32 s58,s57,4096
v_mfma_f32_16x16x32_bf16 v[120:123],v[24:27],v[164:167],0
s_add_u32 s59,s59,106954752
s_add_u32 s60,s60,s66
v_mfma_f32_16x16x32_bf16 v[124:127],v[26:29],v[168:171],0
v_mfma_f32_16x16x32_bf16 v[128:131],v[24:27],v[168:171],0
v_add_f32_dpp v44,v36,v36 row_ror:8 row_mask:0xf bank_mask:0xf
v_cndmask_b32 v45,v40,v41,s[70:71]
v_mfma_f32_16x16x32_bf16 v[132:135],v[26:29],v[172:175],0
ds_write_b32 v2,v44 offset:0
v_mfma_f32_16x16x32_bf16 v[136:139],v[24:27],v[172:175],0
v_cndmask_b32 v46,v42,v43,s[70:71]
v_cndmask_b32 v45,v45,v46,s[72:73]
v_mfma_f32_16x16x32_bf16 v[140:143],v[26:29],v[176:179],0
ds_write_b32 v0,v45 offset:0
v_mfma_f32_16x16x32_bf16 v[144:147],v[24:27],v[176:179],0
L 0
s_barrier
R 52,3,0
R 56,3,16
R 60,3,32
s_cmp_lg_u32 s67,0
s_cbranch_scc1 Lr_b0_%=
s_waitcnt vmcnt(4)
buffer_store_dword v13,v5,s[36:39],s63 offen offset:0 sc0 sc1
s_mov_b32 s63,0x7fff0000
s_cmp_lt_u32 s45,64
s_cselect_b32 s64,-192,0
s_add_u32 s64,s64,256
s_cmp_lt_u32 s45,64
s_cselect_b32 s61,0,32768
s_add_u32 s61,s61,111177728
s_mul_i32 s56,s45,16384
s_add_u32 s56,s56,98566144
Lr_bpy0_%=:
buffer_load_dword v14,v7,s[36:39],s61 offen offset:0 sc1
s_add_u32 s82,s82,1
s_waitcnt vmcnt(0)
v_cmp_eq_u32 vcc,s53,v14
s_nop 0
s_cmp_eq_u64 vcc,-1
s_cbranch_scc1 Lr_bqy0_%=
s_sleep 2
s_cmp_lt_u32 s82,20000
s_cbranch_scc1 Lr_bpy0_%=
Lr_bqy0_%=:
s_add_u32 m0,s65,0
s_nop 0
D 9,56,0
D 9,56,1024
D 9,56,2048
D 9,56,3072
s_waitcnt vmcnt(0)
Lr_b0_%=:
s_waitcnt vmcnt(10)
T v[68:69],10,0
T v[70:71],10,64
T v[72:73],10,1024
T v[74:75],10,1088
T v[76:77],10,2048
T v[78:79],10,2112
T v[80:81],10,3072
T v[82:83],10,3136
s_mov_b32 s45,s62
s_min_u32 s78,s64,s47
s_mul_i32 s56,s45,16384
s_add_u32 s56,s56,98566144
s_cmp_lt_u32 s45,s78
s_cselect_b32 s67,1,0
s_cselect_b32 s56,s56,0x7fff0000
s_add_u32 m0,s65,16384
s_nop 0
D 9,56,0
D 9,56,1024
D 9,56,2048
D 9,56,3072
L 8
v_pk_add_f32 v[52:53],v[52:53],v[54:55]
v_pk_add_f32 v[56:57],v[56:57],v[58:59]
v_pk_add_f32 v[60:61],v[60:61],v[62:63]
v_add_f32 v52,v52,v53
v_pk_add_f32 v[56:57],v[56:57],v[60:61]
v_mul_f32 v52,s68,v52
v_add_f32 v53,v56,v57
v_mul_f32 v53,s68,v53
v_fma_f32 v53,-v52,v52,v53
v_max_f32 v53,0,v53
L 6
v_lshlrev_b32 v84,16,v68
v_and_b32 v85,0xffff0000,v68
v_lshlrev_b32 v86,16,v69
v_and_b32 v87,0xffff0000,v69
v_lshlrev_b32 v88,16,v70
v_and_b32 v89,0xffff0000,v70
v_lshlrev_b32 v90,16,v71
v_and_b32 v91,0xffff0000,v71
L 4
v_lshlrev_b32 v92,16,v72
v_and_b32 v93,0xffff0000,v72
v_lshlrev_b32 v94,16,v73
v_and_b32 v95,0xffff0000,v73
v_lshlrev_b32 v96,16,v74
v_and_b32 v97,0xffff0000,v74
v_lshlrev_b32 v98,16,v75
v_and_b32 v99,0xffff0000,v75
L 2
v_lshlrev_b32 v100,16,v76
v_and_b32 v101,0xffff0000,v76
v_lshlrev_b32 v102,16,v77
v_and_b32 v103,0xffff0000,v77
v_lshlrev_b32 v104,16,v78
v_and_b32 v105,0xffff0000,v78
v_lshlrev_b32 v106,16,v79
v_and_b32 v107,0xffff0000,v79
L 0
v_lshlrev_b32 v108,16,v80
v_and_b32 v109,0xffff0000,v80
v_lshlrev_b32 v110,16,v81
v_and_b32 v111,0xffff0000,v81
v_lshlrev_b32 v112,16,v82
v_and_b32 v113,0xffff0000,v82
v_lshlrev_b32 v114,16,v83
v_and_b32 v115,0xffff0000,v83
v_add_f32 v53,s69,v53
v_rsq_f32 v48,v53
s_nop 0
v_mul_f32_e64 v50,-v52,v48
v_mov_b32 v49,v48
v_mov_b32 v51,v50
v_pk_fma_f32 v[52:53],v[116:117],v[48:49],v[50:51]
v_pk_fma_f32 v[54:55],v[118:119],v[48:49],v[50:51]
v_pk_fma_f32 v[56:57],v[120:121],v[48:49],v[50:51]
v_pk_fma_f32 v[58:59],v[122:123],v[48:49],v[50:51]
v_pk_fma_f32 v[116:117],v[52:53],v[84:85],v[116:117]
v_pk_fma_f32 v[118:119],v[54:55],v[86:87],v[118:119]
v_pk_fma_f32 v[120:121],v[56:57],v[88:89],v[120:121]
v_pk_fma_f32 v[122:123],v[58:59],v[90:91],v[122:123]
v_cvt_pk_bf16_f32 v148,v116,v117
v_cvt_pk_bf16_f32 v149,v118,v119
v_cvt_pk_bf16_f32 v150,v120,v121
v_cvt_pk_bf16_f32 v151,v122,v123
v_pk_fma_f32 v[52:53],v[124:125],v[48:49],v[50:51]
v_pk_fma_f32 v[54:55],v[126:127],v[48:49],v[50:51]
v_pk_fma_f32 v[56:57],v[128:129],v[48:49],v[50:51]
v_pk_fma_f32 v[58:59],v[130:131],v[48:49],v[50:51]
v_pk_fma_f32 v[124:125],v[52:53],v[92:93],v[124:125]
v_pk_fma_f32 v[126:127],v[54:55],v[94:95],v[126:127]
v_pk_fma_f32 v[128:129],v[56:57],v[96:97],v[128:129]
v_pk_fma_f32 v[130:131],v[58:59],v[98:99],v[130:131]
v_cvt_pk_bf16_f32 v152,v124,v125
v_cvt_pk_bf16_f32 v153,v126,v127
v_cvt_pk_bf16_f32 v154,v128,v129
v_cvt_pk_bf16_f32 v155,v130,v131
v_pk_fma_f32 v[52:53],v[132:133],v[48:49],v[50:51]
v_pk_fma_f32 v[54:55],v[134:135],v[48:49],v[50:51]
v_pk_fma_f32 v[56:57],v[136:137],v[48:49],v[50:51]
v_pk_fma_f32 v[58:59],v[138:139],v[48:49],v[50:51]
v_pk_fma_f32 v[132:133],v[52:53],v[100:101],v[132:133]
v_pk_fma_f32 v[134:135],v[54:55],v[102:103],v[134:135]
v_pk_fma_f32 v[136:137],v[56:57],v[104:105],v[136:137]
v_pk_fma_f32 v[138:139],v[58:59],v[106:107],v[138:139]
v_cvt_pk_bf16_f32 v156,v132,v133
v_cvt_pk_bf16_f32 v157,v134,v135
v_cvt_pk_bf16_f32 v158,v136,v137
v_cvt_pk_bf16_f32 v159,v138,v139
v_pk_fma_f32 v[52:53],v[140:141],v[48:49],v[50:51]
v_pk_fma_f32 v[54:55],v[142:143],v[48:49],v[50:51]
v_pk_fma_f32 v[56:57],v[144:145],v[48:49],v[50:51]
v_pk_fma_f32 v[58:59],v[146:147],v[48:49],v[50:51]
v_pk_fma_f32 v[140:141],v[52:53],v[108:109],v[140:141]
v_pk_fma_f32 v[142:143],v[54:55],v[110:111],v[142:143]
v_pk_fma_f32 v[144:145],v[56:57],v[112:113],v[144:145]
v_pk_fma_f32 v[146:147],v[58:59],v[114:115],v[146:147]
v_cvt_pk_bf16_f32 v160,v140,v141
v_cvt_pk_bf16_f32 v161,v142,v143
v_cvt_pk_bf16_f32 v162,v144,v145
v_cvt_pk_bf16_f32 v163,v146,v147
s_waitcnt vmcnt(8)
buffer_store_dword v13,v5,s[36:39],s63 offen offset:0 sc0 sc1
s_mov_b32 s63,0x7fff0000
v_mfma_f32_16x16x32_bf16 v[36:39],v[32:35],v[148:151],0
buffer_store_dwordx4 v[148:151],v1,s[36:39],s57 offen offset:0 sc0 sc1
v_mfma_f32_16x16x32_bf16 v[40:43],v[148:151],v[148:151],0
M 36,v[32:35],152
buffer_store_dwordx4 v[152:155],v1,s[36:39],s57 offen offset:2048 sc0 sc1
M 40,v[152:155],152
M 36,v[32:35],156
buffer_store_dwordx4 v[156:159],v1,s[36:39],s58 offen offset:0 sc0 sc1
M 40,v[156:159],156
M 36,v[32:35],160
buffer_store_dwordx4 v[160:163],v1,s[36:39],s58 offen offset:2048 sc0 sc1
M 40,v[160:163],160
s_nop 5
v_add_f32_dpp v44,v36,v36 row_ror:8 row_mask:0xf bank_mask:0xf
s_nop 0
v_cndmask_b32 v45,v40,v41,s[70:71]
v_cndmask_b32 v46,v42,v43,s[70:71]
ds_write_b32 v2,v44 offset:1024
v_cndmask_b32 v45,v45,v46,s[72:73]
ds_write_b32 v0,v45 offset:1024
L 0
s_barrier
R 52,3,1024
R 56,3,1040
R 60,3,1056
L 0
v_pk_add_f32 v[52:53],v[52:53],v[54:55]
v_pk_add_f32 v[56:57],v[56:57],v[58:59]
v_pk_add_f32 v[60:61],v[60:61],v[62:63]
v_add_f32 v52,v52,v53
v_pk_add_f32 v[56:57],v[56:57],v[60:61]
v_mul_f32 v52,s68,v52
v_add_f32 v53,v56,v57
v_mul_f32 v53,s68,v53
v_fma_f32 v53,-v52,v52,v53
v_max_f32 v53,0,v53
v_add_f32 v54,s69,v53
v_fma_f32 v55,s69,v54,v53
v_rsq_f32 v65,v54
v_rsq_f32 v54,v55
v_mul_f32_e64 v64,-v52,v65
v_mul_f32 v63,v55,v54
v_mul_f32 v61,s76,v54
v_mul_f32 v55,v54,v54
v_mul_f32 v62,s77,v55
v_mul_f32 v62,v62,v61
v_cndmask_b32 v60,v52,v64,s[74:75]
v_cndmask_b32 v61,v61,v65,s[74:75]
buffer_store_dwordx4 v[60:63],v4,s[36:39],s59 offen offset:0 sc0 sc1
s_mov_b32 s63,s60
s_cmp_lt_u32 s45,s47
s_cbranch_scc0 Lr_mid_%=
Lr_a1_%=:
s_waitcnt vmcnt(10)
v_mfma_f32_16x16x32_bf16 v[36:39],v[32:35],v[196:199],0
s_add_u32 s79,s45,s46
s_cmp_lt_u32 s45,64
s_cselect_b32 s78,s49,0
v_mfma_f32_16x16x32_bf16 v[40:43],v[196:199],v[196:199],0
s_cmp_ge_u32 s79,64
s_cselect_b32 s78,s78,0
s_add_u32 s62,s79,s78
M 36,v[32:35],200
s_lshr_b32 s78,s45,1
s_and_b32 s79,s45,1
s_lshl_b32 s79,s79,7
M 40,v[200:203],200
s_lshl_b32 s57,s78,15
s_lshl_b32 s55,s62,14
s_cmp_lt_u32 s62,s47
M 36,v[32:35],204
s_cselect_b32 s55,s55,0x7fff0000
buffer_load_dwordx4 v[164:167],v8,s[84:87],s55 offen offset:0 nt
M 40,v[204:207],204
s_add_u32 s57,s57,s79
buffer_load_dwordx4 v[168:171],v8,s[84:87],s55 offen offset:128 nt
M 36,v[32:35],208
s_lshl_b32 s59,s78,9
buffer_load_dwordx4 v[172:175],v8,s[84:87],s55 offen offset:256 nt
M 40,v[208:211],208
s_add_u32 s57,s57,8388608
buffer_load_dwordx4 v[176:179],v8,s[84:87],s55 offen offset:384 nt
v_mfma_f32_16x16x32_bf16 v[116:119],v[26:29],v[196:199],0
s_add_u32 s59,s59,s79
s_lshl_b32 s60,s45,9
s_add_u32 s58,s57,4096
v_mfma_f32_16x16x32_bf16 v[120:123],v[24:27],v[196:199],0
s_add_u32 s59,s59,106954752
s_add_u32 s60,s60,s66
v_mfma_f32_16x16x32_bf16 v[124:127],v[26:29],v[200:203],0
v_mfma_f32_16x16x32_bf16 v[128:131],v[24:27],v[200:203],0
v_add_f32_dpp v44,v36,v36 row_ror:8 row_mask:0xf bank_mask:0xf
v_cndmask_b32 v45,v40,v41,s[70:71]
v_mfma_f32_16x16x32_bf16 v[132:135],v[26:29],v[204:207],0
ds_write_b32 v2,v44 offset:0
v_mfma_f32_16x16x32_bf16 v[136:139],v[24:27],v[204:207],0
v_cndmask_b32 v46,v42,v43,s[70:71]
v_cndmask_b32 v45,v45,v46,s[72:73]
v_mfma_f32_16x16x32_bf16 v[140:143],v[26:29],v[208:211],0
ds_write_b32 v0,v45 offset:0
v_mfma_f32_16x16x32_bf16 v[144:147],v[24:27],v[208:211],0
L 0
s_barrier
R 52,3,0
R 56,3,16
R 60,3,32
s_cmp_lg_u32 s67,0
s_cbranch_scc1 Lr_b1_%=
s_waitcnt vmcnt(4)
buffer_store_dword v13,v5,s[36:39],s63 offen offset:0 sc0 sc1
s_mov_b32 s63,0x7fff0000
s_cmp_lt_u32 s45,64
s_cselect_b32 s64,-192,0
s_add_u32 s64,s64,256
s_cmp_lt_u32 s45,64
s_cselect_b32 s61,0,32768
s_add_u32 s61,s61,111177728
s_mul_i32 s56,s45,16384
s_add_u32 s56,s56,98566144
Lr_bpy1_%=:
buffer_load_dword v14,v7,s[36:39],s61 offen offset:0 sc1
s_add_u32 s82,s82,1
s_waitcnt vmcnt(0)
v_cmp_eq_u32 vcc,s53,v14
s_nop 0
s_cmp_eq_u64 vcc,-1
s_cbranch_scc1 Lr_bqy1_%=
s_sleep 2
s_cmp_lt_u32 s82,20000
s_cbranch_scc1 Lr_bpy1_%=
Lr_bqy1_%=:
s_add_u32 m0,s65,16384
s_nop 0
D 9,56,0
D 9,56,1024
D 9,56,2048
D 9,56,3072
s_waitcnt vmcnt(0)
Lr_b1_%=:
s_waitcnt vmcnt(10)
T v[68:69],10,16384
T v[70:71],10,16448
T v[72:73],10,17408
T v[74:75],10,17472
T v[76:77],10,18432
T v[78:79],10,18496
T v[80:81],10,19456
T v[82:83],10,19520
s_mov_b32 s45,s62
s_min_u32 s78,s64,s47
s_mul_i32 s56,s45,16384
s_add_u32 s56,s56,98566144
s_cmp_lt_u32 s45,s78
s_cselect_b32 s67,1,0
s_cselect_b32 s56,s56,0x7fff0000
s_add_u32 m0,s65,0
s_nop 0
D 9,56,0
D 9,56,1024
D 9,56,2048
D 9,56,3072
L 8
v_pk_add_f32 v[52:53],v[52:53],v[54:55]
v_pk_add_f32 v[56:57],v[56:57],v[58:59]
v_pk_add_f32 v[60:61],v[60:61],v[62:63]
v_add_f32 v52,v52,v53
v_pk_add_f32 v[56:57],v[56:57],v[60:61]
v_mul_f32 v52,s68,v52
v_add_f32 v53,v56,v57
v_mul_f32 v53,s68,v53
v_fma_f32 v53,-v52,v52,v53
v_max_f32 v53,0,v53
L 6
v_lshlrev_b32 v84,16,v68
v_and_b32 v85,0xffff0000,v68
v_lshlrev_b32 v86,16,v69
v_and_b32 v87,0xffff0000,v69
v_lshlrev_b32 v88,16,v70
v_and_b32 v89,0xffff0000,v70
v_lshlrev_b32 v90,16,v71
v_and_b32 v91,0xffff0000,v71
L 4
v_lshlrev_b32 v92,16,v72
v_and_b32 v93,0xffff0000,v72
v_lshlrev_b32 v94,16,v73
v_and_b32 v95,0xffff0000,v73
v_lshlrev_b32 v96,16,v74
v_and_b32 v97,0xffff0000,v74
v_lshlrev_b32 v98,16,v75
v_and_b32 v99,0xffff0000,v75
L 2
v_lshlrev_b32 v100,16,v76
v_and_b32 v101,0xffff0000,v76
v_lshlrev_b32 v102,16,v77
v_and_b32 v103,0xffff0000,v77
v_lshlrev_b32 v104,16,v78
v_and_b32 v105,0xffff0000,v78
v_lshlrev_b32 v106,16,v79
v_and_b32 v107,0xffff0000,v79
L 0
v_lshlrev_b32 v108,16,v80
v_and_b32 v109,0xffff0000,v80
v_lshlrev_b32 v110,16,v81
v_and_b32 v111,0xffff0000,v81
v_lshlrev_b32 v112,16,v82
v_and_b32 v113,0xffff0000,v82
)"
R"(v_lshlrev_b32 v114,16,v83
v_and_b32 v115,0xffff0000,v83
v_add_f32 v53,s69,v53
v_rsq_f32 v48,v53
s_nop 0
v_mul_f32_e64 v50,-v52,v48
v_mov_b32 v49,v48
v_mov_b32 v51,v50
v_pk_fma_f32 v[52:53],v[116:117],v[48:49],v[50:51]
v_pk_fma_f32 v[54:55],v[118:119],v[48:49],v[50:51]
v_pk_fma_f32 v[56:57],v[120:121],v[48:49],v[50:51]
v_pk_fma_f32 v[58:59],v[122:123],v[48:49],v[50:51]
v_pk_fma_f32 v[116:117],v[52:53],v[84:85],v[116:117]
v_pk_fma_f32 v[118:119],v[54:55],v[86:87],v[118:119]
v_pk_fma_f32 v[120:121],v[56:57],v[88:89],v[120:121]
v_pk_fma_f32 v[122:123],v[58:59],v[90:91],v[122:123]
v_cvt_pk_bf16_f32 v148,v116,v117
v_cvt_pk_bf16_f32 v149,v118,v119
v_cvt_pk_bf16_f32 v150,v120,v121
v_cvt_pk_bf16_f32 v151,v122,v123
v_pk_fma_f32 v[52:53],v[124:125],v[48:49],v[50:51]
v_pk_fma_f32 v[54:55],v[126:127],v[48:49],v[50:51]
v_pk_fma_f32 v[56:57],v[128:129],v[48:49],v[50:51]
v_pk_fma_f32 v[58:59],v[130:131],v[48:49],v[50:51]
v_pk_fma_f32 v[124:125],v[52:53],v[92:93],v[124:125]
v_pk_fma_f32 v[126:127],v[54:55],v[94:95],v[126:127]
v_pk_fma_f32 v[128:129],v[56:57],v[96:97],v[128:129]
v_pk_fma_f32 v[130:131],v[58:59],v[98:99],v[130:131]
v_cvt_pk_bf16_f32 v152,v124,v125
v_cvt_pk_bf16_f32 v153,v126,v127
v_cvt_pk_bf16_f32 v154,v128,v129
v_cvt_pk_bf16_f32 v155,v130,v131
v_pk_fma_f32 v[52:53],v[132:133],v[48:49],v[50:51]
v_pk_fma_f32 v[54:55],v[134:135],v[48:49],v[50:51]
v_pk_fma_f32 v[56:57],v[136:137],v[48:49],v[50:51]
v_pk_fma_f32 v[58:59],v[138:139],v[48:49],v[50:51]
v_pk_fma_f32 v[132:133],v[52:53],v[100:101],v[132:133]
v_pk_fma_f32 v[134:135],v[54:55],v[102:103],v[134:135]
v_pk_fma_f32 v[136:137],v[56:57],v[104:105],v[136:137]
v_pk_fma_f32 v[138:139],v[58:59],v[106:107],v[138:139]
v_cvt_pk_bf16_f32 v156,v132,v133
v_cvt_pk_bf16_f32 v157,v134,v135
v_cvt_pk_bf16_f32 v158,v136,v137
v_cvt_pk_bf16_f32 v159,v138,v139
v_pk_fma_f32 v[52:53],v[140:141],v[48:49],v[50:51]
v_pk_fma_f32 v[54:55],v[142:143],v[48:49],v[50:51]
v_pk_fma_f32 v[56:57],v[144:145],v[48:49],v[50:51]
v_pk_fma_f32 v[58:59],v[146:147],v[48:49],v[50:51]
v_pk_fma_f32 v[140:141],v[52:53],v[108:109],v[140:141]
v_pk_fma_f32 v[142:143],v[54:55],v[110:111],v[142:143]
v_pk_fma_f32 v[144:145],v[56:57],v[112:113],v[144:145]
v_pk_fma_f32 v[146:147],v[58:59],v[114:115],v[146:147]
v_cvt_pk_bf16_f32 v160,v140,v141
v_cvt_pk_bf16_f32 v161,v142,v143
v_cvt_pk_bf16_f32 v162,v144,v145
v_cvt_pk_bf16_f32 v163,v146,v147
s_waitcnt vmcnt(8)
buffer_store_dword v13,v5,s[36:39],s63 offen offset:0 sc0 sc1
s_mov_b32 s63,0x7fff0000
v_mfma_f32_16x16x32_bf16 v[36:39],v[32:35],v[148:151],0
buffer_store_dwordx4 v[148:151],v1,s[36:39],s57 offen offset:0 sc0 sc1
v_mfma_f32_16x16x32_bf16 v[40:43],v[148:151],v[148:151],0
M 36,v[32:35],152
buffer_store_dwordx4 v[152:155],v1,s[36:39],s57 offen offset:2048 sc0 sc1
M 40,v[152:155],152
M 36,v[32:35],156
buffer_store_dwordx4 v[156:159],v1,s[36:39],s58 offen offset:0 sc0 sc1
M 40,v[156:159],156
M 36,v[32:35],160
buffer_store_dwordx4 v[160:163],v1,s[36:39],s58 offen offset:2048 sc0 sc1
M 40,v[160:163],160
s_nop 5
v_add_f32_dpp v44,v36,v36 row_ror:8 row_mask:0xf bank_mask:0xf
s_nop 0
v_cndmask_b32 v45,v40,v41,s[70:71]
v_cndmask_b32 v46,v42,v43,s[70:71]
ds_write_b32 v2,v44 offset:1024
v_cndmask_b32 v45,v45,v46,s[72:73]
ds_write_b32 v0,v45 offset:1024
L 0
s_barrier
R 52,3,1024
R 56,3,1040
R 60,3,1056
L 0
v_pk_add_f32 v[52:53],v[52:53],v[54:55]
v_pk_add_f32 v[56:57],v[56:57],v[58:59]
v_pk_add_f32 v[60:61],v[60:61],v[62:63]
v_add_f32 v52,v52,v53
v_pk_add_f32 v[56:57],v[56:57],v[60:61]
v_mul_f32 v52,s68,v52
v_add_f32 v53,v56,v57
v_mul_f32 v53,s68,v53
v_fma_f32 v53,-v52,v52,v53
v_max_f32 v53,0,v53
v_add_f32 v54,s69,v53
v_fma_f32 v55,s69,v54,v53
v_rsq_f32 v65,v54
v_rsq_f32 v54,v55
v_mul_f32_e64 v64,-v52,v65
v_mul_f32 v63,v55,v54
v_mul_f32 v61,s76,v54
v_mul_f32 v55,v54,v54
v_mul_f32 v62,s77,v55
v_mul_f32 v62,v62,v61
v_cndmask_b32 v60,v52,v64,s[74:75]
v_cndmask_b32 v61,v61,v65,s[74:75]
buffer_store_dwordx4 v[60:63],v4,s[36:39],s59 offen offset:0 sc0 sc1
s_mov_b32 s63,s60
s_cmp_lt_u32 s45,s47
s_cbranch_scc1 Lr_a0_%=
Lr_mid_%=:
s_waitcnt vmcnt(0)
buffer_store_dword v13,v5,s[36:39],s63 offen offset:0 sc0 sc1
s_mov_b32 s63,0x7fff0000
s_cmp_eq_u32 s51,0
s_cbranch_scc1 Lr_done_%=
s_mov_b32 s45,s93
s_lshr_b32 s78,s45,1
s_and_b32 s79,s45,1
s_lshl_b32 s79,s79,7
s_lshl_b32 s55,s78,15
s_add_u32 s55,s55,s79
s_mul_i32 s56,s45,16384
s_add_u32 s56,s56,98566144
s_lshl_b32 s61,s78,11
s_add_u32 s61,s61,110587904
s_add_u32 s78,s45,s46
s_lshr_b32 s78,s78,1
s_lshl_b32 s62,s78,11
s_add_u32 s62,s62,110587904
Lr_bps_%=:
buffer_load_dword v14,v6,s[36:39],s61 offen offset:0 sc1
s_add_u32 s82,s82,1
s_waitcnt vmcnt(0)
v_cmp_eq_u32 vcc,s53,v14
s_nop 0
s_cmp_eq_u64 vcc,-1
s_cbranch_scc1 Lr_bqs_%=
s_sleep 2
s_cmp_lt_u32 s82,20000
s_cbranch_scc1 Lr_bps_%=
Lr_bqs_%=:
s_add_u32 s78,s55,71303168
buffer_load_dwordx4 v[164:167],v1,s[36:39],s78 offen offset:0 sc1
buffer_load_dwordx4 v[168:171],v1,s[36:39],s78 offen offset:2048 sc1
s_add_u32 s78,s55,71307264
buffer_load_dwordx4 v[172:175],v1,s[36:39],s78 offen offset:0 sc1
buffer_load_dwordx4 v[176:179],v1,s[36:39],s78 offen offset:2048 sc1
s_add_u32 s78,s55,89128960
buffer_load_dwordx4 v[180:183],v1,s[36:39],s78 offen offset:0 sc1
buffer_load_dwordx4 v[184:187],v1,s[36:39],s78 offen offset:2048 sc1
s_add_u32 s78,s55,89133056
buffer_load_dwordx4 v[188:191],v1,s[36:39],s78 offen offset:0 sc1
buffer_load_dwordx4 v[192:195],v1,s[36:39],s78 offen offset:2048 sc1
s_add_u32 m0,s65,0
s_nop 0
D 9,56,0
D 9,56,1024
D 9,56,2048
D 9,56,3072
buffer_load_dword v14,v6,s[36:39],s62 offen offset:0 sc1
s_lshr_b32 s78,s45,1
s_and_b32 s79,s45,1
s_lshl_b32 s79,s79,7
s_lshl_b32 s57,s78,15
s_add_u32 s57,s57,s79
s_add_u32 s57,s57,12582912
s_add_u32 s58,s57,4096
s_lshl_b32 s59,s78,9
s_add_u32 s59,s59,s79
s_add_u32 s59,s59,107020288
s_lshl_b32 s60,s45,9
s_add_u32 s60,s60,s66
s_add_u32 s60,s60,131072
s_add_u32 s45,s45,s46
s_lshr_b32 s78,s45,1
s_and_b32 s79,s45,1
s_lshl_b32 s79,s79,7
s_lshl_b32 s55,s78,15
s_add_u32 s55,s55,s79
s_mul_i32 s56,s45,16384
s_add_u32 s56,s56,98566144
s_lshl_b32 s61,s78,11
s_add_u32 s61,s61,110587904
s_add_u32 s78,s45,s46
s_lshr_b32 s78,s78,1
s_lshl_b32 s62,s78,11
s_add_u32 s62,s62,110587904
s_waitcnt vmcnt(0)
Lr_it0_%=:
T v[68:69],10,0
T v[70:71],10,64
T v[72:73],10,1024
T v[74:75],10,1088
T v[76:77],10,2048
T v[78:79],10,2112
T v[80:81],10,3072
T v[82:83],10,3136
s_nop 0
v_cmp_eq_u32 vcc,s53,v14
s_nop 0
s_cmp_eq_u64 vcc,-1
s_cselect_b32 s78,s45,256
s_cmp_lt_u32 s78,256
s_cbranch_scc1 Lr_ta0_%=
buffer_load_dword v14,v6,s[36:39],s61 offen offset:0 sc1
v_mfma_f32_16x16x32_bf16 v[116:119],v[26:29],v[164:167],0
L 6
v_lshlrev_b32 v84,16,v68
v_and_b32 v85,0xffff0000,v68
v_mfma_f32_16x16x32_bf16 v[120:123],v[24:27],v[164:167],0
L 4
v_lshlrev_b32 v86,16,v69
v_and_b32 v87,0xffff0000,v69
M 116,v[26:29],180
L 2
v_lshlrev_b32 v88,16,v70
v_and_b32 v89,0xffff0000,v70
M 120,v[24:27],180
L 0
v_lshlrev_b32 v90,16,v71
v_and_b32 v91,0xffff0000,v71
v_mfma_f32_16x16x32_bf16 v[124:127],v[26:29],v[168:171],0
v_lshlrev_b32 v92,16,v72
v_and_b32 v93,0xffff0000,v72
v_mfma_f32_16x16x32_bf16 v[128:131],v[24:27],v[168:171],0
v_lshlrev_b32 v94,16,v73
v_and_b32 v95,0xffff0000,v73
M 124,v[26:29],184
v_cvt_pk_bf16_f32 v148,v116,v117
v_cvt_pk_bf16_f32 v149,v118,v119
M 128,v[24:27],184
v_cvt_pk_bf16_f32 v150,v120,v121
v_cvt_pk_bf16_f32 v151,v122,v123
v_mfma_f32_16x16x32_bf16 v[132:135],v[26:29],v[172:175],0
v_lshlrev_b32 v96,16,v74
v_and_b32 v97,0xffff0000,v74
v_mfma_f32_16x16x32_bf16 v[136:139],v[24:27],v[172:175],0
v_lshlrev_b32 v98,16,v75
v_and_b32 v99,0xffff0000,v75
M 132,v[26:29],188
v_cvt_pk_bf16_f32 v152,v124,v125
v_cvt_pk_bf16_f32 v153,v126,v127
M 136,v[24:27],188
v_cvt_pk_bf16_f32 v154,v128,v129
v_cvt_pk_bf16_f32 v155,v130,v131
v_mfma_f32_16x16x32_bf16 v[140:143],v[26:29],v[176:179],0
v_lshlrev_b32 v100,16,v76
v_and_b32 v101,0xffff0000,v76
v_mfma_f32_16x16x32_bf16 v[144:147],v[24:27],v[176:179],0
v_lshlrev_b32 v102,16,v77
v_and_b32 v103,0xffff0000,v77
M 140,v[26:29],192
v_cvt_pk_bf16_f32 v156,v132,v133
v_cvt_pk_bf16_f32 v157,v134,v135
M 144,v[24:27],192
v_cvt_pk_bf16_f32 v158,v136,v137
v_cvt_pk_bf16_f32 v159,v138,v139
v_mfma_f32_16x16x32_bf16 v[36:39],v[32:35],v[148:151],0
v_lshlrev_b32 v104,16,v78
v_and_b32 v105,0xffff0000,v78
v_mfma_f32_16x16x32_bf16 v[40:43],v[148:151],v[148:151],0
v_lshlrev_b32 v106,16,v79
v_and_b32 v107,0xffff0000,v79
M 36,v[32:35],152
v_cvt_pk_bf16_f32 v160,v140,v141
v_cvt_pk_bf16_f32 v161,v142,v143
M 40,v[152:155],152
v_cvt_pk_bf16_f32 v162,v144,v145
v_cvt_pk_bf16_f32 v163,v146,v147
M 36,v[32:35],156
v_lshlrev_b32 v108,16,v80
v_and_b32 v109,0xffff0000,v80
M 40,v[156:159],156
v_lshlrev_b32 v110,16,v81
v_and_b32 v111,0xffff0000,v81
M 36,v[32:35],160
v_lshlrev_b32 v112,16,v82
v_and_b32 v113,0xffff0000,v82
M 40,v[160:163],160
v_lshlrev_b32 v114,16,v83
v_and_b32 v115,0xffff0000,v83
s_nop 2
v_add_f32_dpp v44,v36,v36 row_ror:8 row_mask:0xf bank_mask:0xf
s_nop 1
v_cndmask_b32 v45,v40,v41,s[70:71]
v_cndmask_b32 v46,v42,v43,s[70:71]
ds_write_b32 v2,v44 offset:0
v_cndmask_b32 v45,v45,v46,s[72:73]
ds_write_b32 v0,v45 offset:0
L 0
s_barrier
R 52,3,0
R 56,3,16
R 60,3,32
L 0
v_pk_add_f32 v[52:53],v[52:53],v[54:55]
v_pk_add_f32 v[56:57],v[56:57],v[58:59]
v_pk_add_f32 v[60:61],v[60:61],v[62:63]
v_add_f32 v52,v52,v53
v_pk_add_f32 v[56:57],v[56:57],v[60:61]
v_mul_f32 v52,s68,v52
v_add_f32 v53,v56,v57
v_mul_f32 v53,s68,v53
v_fma_f32 v53,-v52,v52,v53
v_max_f32 v53,0,v53
s_waitcnt vmcnt(1)
buffer_store_dword v13,v5,s[36:39],s63 offen offset:0 sc0 sc1
s_mov_b32 s63,0x7fff0000
s_waitcnt vmcnt(1)
v_cmp_eq_u32 vcc,s53,v14
s_nop 0
s_cmp_eq_u64 vcc,-1
s_cselect_b32 s78,s45,256
s_cmp_lt_u32 s78,256
s_cbranch_scc1 Lr_tb0_%=
buffer_load_dword v14,v6,s[36:39],s61 offen offset:0 sc1
v_add_f32 v53,s69,v53
v_rsq_f32 v48,v53
s_nop 0
v_mul_f32_e64 v50,-v52,v48
v_mov_b32 v49,v48
v_mov_b32 v51,v50
v_pk_fma_f32 v[52:53],v[116:117],v[48:49],v[50:51]
v_pk_fma_f32 v[54:55],v[118:119],v[48:49],v[50:51]
v_pk_fma_f32 v[56:57],v[120:121],v[48:49],v[50:51]
v_pk_fma_f32 v[58:59],v[122:123],v[48:49],v[50:51]
v_pk_fma_f32 v[116:117],v[52:53],v[84:85],v[116:117]
v_pk_fma_f32 v[118:119],v[54:55],v[86:87],v[118:119]
v_pk_fma_f32 v[120:121],v[56:57],v[88:89],v[120:121]
v_pk_fma_f32 v[122:123],v[58:59],v[90:91],v[122:123]
v_cvt_pk_bf16_f32 v148,v116,v117
v_cvt_pk_bf16_f32 v149,v118,v119
v_cvt_pk_bf16_f32 v150,v120,v121
v_cvt_pk_bf16_f32 v151,v122,v123
v_pk_fma_f32 v[52:53],v[124:125],v[48:49],v[50:51]
v_pk_fma_f32 v[54:55],v[126:127],v[48:49],v[50:51]
v_pk_fma_f32 v[56:57],v[128:129],v[48:49],v[50:51]
v_pk_fma_f32 v[58:59],v[130:131],v[48:49],v[50:51]
v_pk_fma_f32 v[124:125],v[52:53],v[92:93],v[124:125]
v_pk_fma_f32 v[126:127],v[54:55],v[94:95],v[126:127]
v_pk_fma_f32 v[128:129],v[56:57],v[96:97],v[128:129]
v_pk_fma_f32 v[130:131],v[58:59],v[98:99],v[130:131]
v_cvt_pk_bf16_f32 v152,v124,v125
v_cvt_pk_bf16_f32 v153,v126,v127
v_cvt_pk_bf16_f32 v154,v128,v129
v_cvt_pk_bf16_f32 v155,v130,v131
v_pk_fma_f32 v[52:53],v[132:133],v[48:49],v[50:51]
v_pk_fma_f32 v[54:55],v[134:135],v[48:49],v[50:51]
v_pk_fma_f32 v[56:57],v[136:137],v[48:49],v[50:51]
v_pk_fma_f32 v[58:59],v[138:139],v[48:49],v[50:51]
v_pk_fma_f32 v[132:133],v[52:53],v[100:101],v[132:133]
v_pk_fma_f32 v[134:135],v[54:55],v[102:103],v[134:135]
v_pk_fma_f32 v[136:137],v[56:57],v[104:105],v[136:137]
v_pk_fma_f32 v[138:139],v[58:59],v[106:107],v[138:139]
v_cvt_pk_bf16_f32 v156,v132,v133
v_cvt_pk_bf16_f32 v157,v134,v135
v_cvt_pk_bf16_f32 v158,v136,v137
v_cvt_pk_bf16_f32 v159,v138,v139
v_pk_fma_f32 v[52:53],v[140:141],v[48:49],v[50:51]
v_pk_fma_f32 v[54:55],v[142:143],v[48:49],v[50:51]
v_pk_fma_f32 v[56:57],v[144:145],v[48:49],v[50:51]
v_pk_fma_f32 v[58:59],v[146:147],v[48:49],v[50:51]
v_pk_fma_f32 v[140:141],v[52:53],v[108:109],v[140:141]
v_pk_fma_f32 v[142:143],v[54:55],v[110:111],v[142:143]
v_pk_fma_f32 v[144:145],v[56:57],v[112:113],v[144:145]
v_pk_fma_f32 v[146:147],v[58:59],v[114:115],v[146:147]
v_cvt_pk_bf16_f32 v160,v140,v141
v_cvt_pk_bf16_f32 v161,v142,v143
v_cvt_pk_bf16_f32 v162,v144,v145
v_cvt_pk_bf16_f32 v163,v146,v147
v_mfma_f32_16x16x32_bf16 v[36:39],v[32:35],v[148:151],0
buffer_store_dwordx4 v[148:151],v1,s[36:39],s57 offen offset:0 sc0 sc1
v_mfma_f32_16x16x32_bf16 v[40:43],v[148:151],v[148:151],0
M 36,v[32:35],152
buffer_store_dwordx4 v[152:155],v1,s[36:39],s57 offen offset:2048 sc0 sc1
M 40,v[152:155],152
M 36,v[32:35],156
buffer_store_dwordx4 v[156:159],v1,s[36:39],s58 offen offset:0 sc0 sc1
M 40,v[156:159],156
M 36,v[32:35],160
buffer_store_dwordx4 v[160:163],v1,s[36:39],s58 offen offset:2048 sc0 sc1
M 40,v[160:163],160
s_nop 5
v_add_f32_dpp v44,v36,v36 row_ror:8 row_mask:0xf bank_mask:0xf
s_nop 0
v_cndmask_b32 v45,v40,v41,s[70:71]
v_cndmask_b32 v46,v42,v43,s[70:71]
ds_write_b32 v2,v44 offset:1024
v_cndmask_b32 v45,v45,v46,s[72:73]
ds_write_b32 v0,v45 offset:1024
L 0
s_barrier
R 52,3,1024
R 56,3,1040
R 60,3,1056
L 0
v_pk_add_f32 v[52:53],v[52:53],v[54:55]
v_pk_add_f32 v[56:57],v[56:57],v[58:59]
v_pk_add_f32 v[60:61],v[60:61],v[62:63]
v_add_f32 v52,v52,v53
v_pk_add_f32 v[56:57],v[56:57],v[60:61]
v_mul_f32 v52,s68,v52
v_add_f32 v53,v56,v57
v_mul_f32 v53,s68,v53
v_fma_f32 v53,-v52,v52,v53
v_max_f32 v53,0,v53
v_add_f32 v54,s69,v53
v_fma_f32 v55,s69,v54,v53
v_rsq_f32 v65,v54
v_rsq_f32 v54,v55
v_mul_f32_e64 v64,-v52,v65
v_mul_f32 v63,v55,v54
v_mul_f32 v61,s76,v54
v_mul_f32 v55,v54,v54
v_mul_f32 v62,s77,v55
v_mul_f32 v62,v62,v61
v_cndmask_b32 v60,v52,v64,s[74:75]
v_cndmask_b32 v61,v61,v65,s[74:75]
buffer_store_dwordx4 v[60:63],v4,s[36:39],s59 offen offset:0 sc0 sc1
s_mov_b32 s63,s60
s_waitcnt vmcnt(5)
v_cmp_eq_u32 vcc,s53,v14
s_nop 0
s_cmp_eq_u64 vcc,-1
s_cselect_b32 s78,s45,256
s_cmp_lt_u32 s78,256
s_cbranch_scc1 Lr_tc0_%=
s_waitcnt vmcnt(0)
buffer_store_dword v13,v5,s[36:39],s63 offen offset:0 sc0 sc1
s_mov_b32 s63,0x7fff0000
s_cmp_ge_u32 s45,256
s_cbranch_scc1 Lr_done_%=
Lr_bp0_%=:
buffer_load_dword v14,v6,s[36:39],s61 offen offset:0 sc1
s_add_u32 s82,s82,1
s_waitcnt vmcnt(0)
v_cmp_eq_u32 vcc,s53,v14
s_nop 0
s_cmp_eq_u64 vcc,-1
s_cbranch_scc1 Lr_bq0_%=
s_sleep 2
s_cmp_lt_u32 s82,20000
s_cbranch_scc1 Lr_bp0_%=
Lr_bq0_%=:
s_add_u32 s78,s55,71303168
buffer_load_dwordx4 v[196:199],v1,s[36:39],s78 offen offset:0 sc1
buffer_load_dwordx4 v[200:203],v1,s[36:39],s78 offen offset:2048 sc1
s_add_u32 s78,s55,71307264
buffer_load_dwordx4 v[204:207],v1,s[36:39],s78 offen offset:0 sc1
buffer_load_dwordx4 v[208:211],v1,s[36:39],s78 offen offset:2048 sc1
s_add_u32 s78,s55,89128960
buffer_load_dwordx4 v[212:215],v1,s[36:39],s78 offen offset:0 sc1
buffer_load_dwordx4 v[216:219],v1,s[36:39],s78 offen offset:2048 sc1
s_add_u32 s78,s55,89133056
buffer_load_dwordx4 v[220:223],v1,s[36:39],s78 offen offset:0 sc1
buffer_load_dwordx4 v[224:227],v1,s[36:39],s78 offen offset:2048 sc1
s_add_u32 m0,s65,16384
s_nop 0
D 9,56,0
D 9,56,1024
D 9,56,2048
D 9,56,3072
buffer_load_dword v14,v6,s[36:39],s62 offen offset:0 sc1
s_lshr_b32 s78,s45,1
s_and_b32 s79,s45,1
s_lshl_b32 s79,s79,7
s_lshl_b32 s57,s78,15
s_add_u32 s57,s57,s79
s_add_u32 s57,s57,12582912
s_add_u32 s58,s57,4096
s_lshl_b32 s59,s78,9
s_add_u32 s59,s59,s79
s_add_u32 s59,s59,107020288
s_lshl_b32 s60,s45,9
s_add_u32 s60,s60,s66
s_add_u32 s60,s60,131072
s_add_u32 s45,s45,s46
s_lshr_b32 s78,s45,1
s_and_b32 s79,s45,1
s_lshl_b32 s79,s79,7
s_lshl_b32 s55,s78,15
s_add_u32 s55,s55,s79
s_mul_i32 s56,s45,16384
s_add_u32 s56,s56,98566144
s_lshl_b32 s61,s78,11
s_add_u32 s61,s61,110587904
s_add_u32 s78,s45,s46
s_lshr_b32 s78,s78,1
s_lshl_b32 s62,s78,11
s_add_u32 s62,s62,110587904
s_waitcnt vmcnt(0)
s_branch Lr_it1_%=
Lr_ta0_%=:
v_mfma_f32_16x16x32_bf16 v[116:119],v[26:29],v[164:167],0
s_add_u32 s78,s55,71303168
buffer_load_dwordx4 v[196:199],v1,s[36:39],s78 offen offset:0 sc1
v_mfma_f32_16x16x32_bf16 v[120:123],v[24:27],v[164:167],0
L 6
)"
R"(buffer_load_dwordx4 v[200:203],v1,s[36:39],s78 offen offset:2048 sc1
M 116,v[26:29],180
s_add_u32 s78,s55,71307264
buffer_load_dwordx4 v[204:207],v1,s[36:39],s78 offen offset:0 sc1
M 120,v[24:27],180
L 4
buffer_load_dwordx4 v[208:211],v1,s[36:39],s78 offen offset:2048 sc1
v_mfma_f32_16x16x32_bf16 v[124:127],v[26:29],v[168:171],0
s_add_u32 s78,s55,89128960
buffer_load_dwordx4 v[212:215],v1,s[36:39],s78 offen offset:0 sc1
v_mfma_f32_16x16x32_bf16 v[128:131],v[24:27],v[168:171],0
L 2
buffer_load_dwordx4 v[216:219],v1,s[36:39],s78 offen offset:2048 sc1
M 124,v[26:29],184
L 0
v_cvt_pk_bf16_f32 v148,v116,v117
v_cvt_pk_bf16_f32 v149,v118,v119
M 128,v[24:27],184
s_add_u32 s78,s55,89133056
buffer_load_dwordx4 v[220:223],v1,s[36:39],s78 offen offset:0 sc1
v_mfma_f32_16x16x32_bf16 v[132:135],v[26:29],v[172:175],0
s_add_u32 m0,s65,16384
buffer_load_dwordx4 v[224:227],v1,s[36:39],s78 offen offset:2048 sc1
v_mfma_f32_16x16x32_bf16 v[136:139],v[24:27],v[172:175],0
D 9,56,0
M 132,v[26:29],188
D 9,56,1024
M 136,v[24:27],188
D 9,56,2048
v_mfma_f32_16x16x32_bf16 v[140:143],v[26:29],v[176:179],0
D 9,56,3072
v_mfma_f32_16x16x32_bf16 v[144:147],v[24:27],v[176:179],0
v_cvt_pk_bf16_f32 v150,v120,v121
v_cvt_pk_bf16_f32 v151,v122,v123
M 140,v[26:29],192
v_cvt_pk_bf16_f32 v152,v124,v125
v_cvt_pk_bf16_f32 v153,v126,v127
M 144,v[24:27],192
v_cvt_pk_bf16_f32 v154,v128,v129
v_cvt_pk_bf16_f32 v155,v130,v131
v_mfma_f32_16x16x32_bf16 v[36:39],v[32:35],v[148:151],0
v_cvt_pk_bf16_f32 v156,v132,v133
v_cvt_pk_bf16_f32 v157,v134,v135
v_mfma_f32_16x16x32_bf16 v[40:43],v[148:151],v[148:151],0
v_cvt_pk_bf16_f32 v158,v136,v137
v_cvt_pk_bf16_f32 v159,v138,v139
M 36,v[32:35],152
v_cvt_pk_bf16_f32 v160,v140,v141
v_cvt_pk_bf16_f32 v161,v142,v143
M 40,v[152:155],152
v_cvt_pk_bf16_f32 v162,v144,v145
v_cvt_pk_bf16_f32 v163,v146,v147
M 36,v[32:35],156
v_lshlrev_b32 v84,16,v68
v_and_b32 v85,0xffff0000,v68
M 40,v[156:159],156
v_lshlrev_b32 v86,16,v69
v_and_b32 v87,0xffff0000,v69
M 36,v[32:35],160
v_lshlrev_b32 v88,16,v70
v_and_b32 v89,0xffff0000,v70
M 40,v[160:163],160
v_lshlrev_b32 v90,16,v71
v_and_b32 v91,0xffff0000,v71
v_lshlrev_b32 v92,16,v72
v_and_b32 v93,0xffff0000,v72
v_lshlrev_b32 v94,16,v73
v_and_b32 v95,0xffff0000,v73
v_lshlrev_b32 v96,16,v74
v_and_b32 v97,0xffff0000,v74
v_lshlrev_b32 v98,16,v75
v_and_b32 v99,0xffff0000,v75
v_lshlrev_b32 v100,16,v76
v_and_b32 v101,0xffff0000,v76
v_lshlrev_b32 v102,16,v77
v_and_b32 v103,0xffff0000,v77
v_lshlrev_b32 v104,16,v78
v_and_b32 v105,0xffff0000,v78
v_lshlrev_b32 v106,16,v79
v_and_b32 v107,0xffff0000,v79
v_lshlrev_b32 v108,16,v80
v_and_b32 v109,0xffff0000,v80
v_lshlrev_b32 v110,16,v81
v_and_b32 v111,0xffff0000,v81
v_lshlrev_b32 v112,16,v82
v_and_b32 v113,0xffff0000,v82
v_lshlrev_b32 v114,16,v83
v_and_b32 v115,0xffff0000,v83
v_add_f32_dpp v44,v36,v36 row_ror:8 row_mask:0xf bank_mask:0xf
v_cndmask_b32 v45,v40,v41,s[70:71]
v_cndmask_b32 v46,v42,v43,s[70:71]
ds_write_b32 v2,v44 offset:0
v_cndmask_b32 v45,v45,v46,s[72:73]
ds_write_b32 v0,v45 offset:0
L 0
s_barrier
R 52,3,0
R 56,3,16
R 60,3,32
L 0
v_pk_add_f32 v[52:53],v[52:53],v[54:55]
v_pk_add_f32 v[56:57],v[56:57],v[58:59]
v_pk_add_f32 v[60:61],v[60:61],v[62:63]
v_add_f32 v52,v52,v53
v_pk_add_f32 v[56:57],v[56:57],v[60:61]
v_mul_f32 v52,s68,v52
v_add_f32 v53,v56,v57
v_mul_f32 v53,s68,v53
v_fma_f32 v53,-v52,v52,v53
v_max_f32 v53,0,v53
buffer_load_dword v14,v6,s[36:39],s62 offen offset:0 sc1
v_add_f32 v53,s69,v53
v_rsq_f32 v48,v53
s_nop 0
v_mul_f32_e64 v50,-v52,v48
v_mov_b32 v49,v48
v_mov_b32 v51,v50
v_pk_fma_f32 v[52:53],v[116:117],v[48:49],v[50:51]
v_pk_fma_f32 v[54:55],v[118:119],v[48:49],v[50:51]
v_pk_fma_f32 v[56:57],v[120:121],v[48:49],v[50:51]
v_pk_fma_f32 v[58:59],v[122:123],v[48:49],v[50:51]
v_pk_fma_f32 v[116:117],v[52:53],v[84:85],v[116:117]
v_pk_fma_f32 v[118:119],v[54:55],v[86:87],v[118:119]
v_pk_fma_f32 v[120:121],v[56:57],v[88:89],v[120:121]
v_pk_fma_f32 v[122:123],v[58:59],v[90:91],v[122:123]
v_cvt_pk_bf16_f32 v148,v116,v117
v_cvt_pk_bf16_f32 v149,v118,v119
v_cvt_pk_bf16_f32 v150,v120,v121
v_cvt_pk_bf16_f32 v151,v122,v123
v_pk_fma_f32 v[52:53],v[124:125],v[48:49],v[50:51]
v_pk_fma_f32 v[54:55],v[126:127],v[48:49],v[50:51]
v_pk_fma_f32 v[56:57],v[128:129],v[48:49],v[50:51]
v_pk_fma_f32 v[58:59],v[130:131],v[48:49],v[50:51]
v_pk_fma_f32 v[124:125],v[52:53],v[92:93],v[124:125]
v_pk_fma_f32 v[126:127],v[54:55],v[94:95],v[126:127]
v_pk_fma_f32 v[128:129],v[56:57],v[96:97],v[128:129]
v_pk_fma_f32 v[130:131],v[58:59],v[98:99],v[130:131]
v_cvt_pk_bf16_f32 v152,v124,v125
v_cvt_pk_bf16_f32 v153,v126,v127
v_cvt_pk_bf16_f32 v154,v128,v129
v_cvt_pk_bf16_f32 v155,v130,v131
v_pk_fma_f32 v[52:53],v[132:133],v[48:49],v[50:51]
v_pk_fma_f32 v[54:55],v[134:135],v[48:49],v[50:51]
v_pk_fma_f32 v[56:57],v[136:137],v[48:49],v[50:51]
v_pk_fma_f32 v[58:59],v[138:139],v[48:49],v[50:51]
v_pk_fma_f32 v[132:133],v[52:53],v[100:101],v[132:133]
v_pk_fma_f32 v[134:135],v[54:55],v[102:103],v[134:135]
v_pk_fma_f32 v[136:137],v[56:57],v[104:105],v[136:137]
v_pk_fma_f32 v[138:139],v[58:59],v[106:107],v[138:139]
v_cvt_pk_bf16_f32 v156,v132,v133
v_cvt_pk_bf16_f32 v157,v134,v135
v_cvt_pk_bf16_f32 v158,v136,v137
v_cvt_pk_bf16_f32 v159,v138,v139
v_pk_fma_f32 v[52:53],v[140:141],v[48:49],v[50:51]
v_pk_fma_f32 v[54:55],v[142:143],v[48:49],v[50:51]
v_pk_fma_f32 v[56:57],v[144:145],v[48:49],v[50:51]
v_pk_fma_f32 v[58:59],v[146:147],v[48:49],v[50:51]
v_pk_fma_f32 v[140:141],v[52:53],v[108:109],v[140:141]
v_pk_fma_f32 v[142:143],v[54:55],v[110:111],v[142:143]
v_pk_fma_f32 v[144:145],v[56:57],v[112:113],v[144:145]
v_pk_fma_f32 v[146:147],v[58:59],v[114:115],v[146:147]
v_cvt_pk_bf16_f32 v160,v140,v141
v_cvt_pk_bf16_f32 v161,v142,v143
v_cvt_pk_bf16_f32 v162,v144,v145
v_cvt_pk_bf16_f32 v163,v146,v147
s_waitcnt vmcnt(13)
buffer_store_dword v13,v5,s[36:39],s63 offen offset:0 sc0 sc1
s_mov_b32 s63,0x7fff0000
v_mfma_f32_16x16x32_bf16 v[36:39],v[32:35],v[148:151],0
buffer_store_dwordx4 v[148:151],v1,s[36:39],s57 offen offset:0 sc0 sc1
v_mfma_f32_16x16x32_bf16 v[40:43],v[148:151],v[148:151],0
M 36,v[32:35],152
buffer_store_dwordx4 v[152:155],v1,s[36:39],s57 offen offset:2048 sc0 sc1
M 40,v[152:155],152
M 36,v[32:35],156
buffer_store_dwordx4 v[156:159],v1,s[36:39],s58 offen offset:0 sc0 sc1
M 40,v[156:159],156
M 36,v[32:35],160
buffer_store_dwordx4 v[160:163],v1,s[36:39],s58 offen offset:2048 sc0 sc1
M 40,v[160:163],160
s_nop 5
v_add_f32_dpp v44,v36,v36 row_ror:8 row_mask:0xf bank_mask:0xf
s_nop 0
v_cndmask_b32 v45,v40,v41,s[70:71]
v_cndmask_b32 v46,v42,v43,s[70:71]
ds_write_b32 v2,v44 offset:1024
v_cndmask_b32 v45,v45,v46,s[72:73]
ds_write_b32 v0,v45 offset:1024
L 0
s_barrier
R 52,3,1024
R 56,3,1040
R 60,3,1056
L 0
v_pk_add_f32 v[52:53],v[52:53],v[54:55]
v_pk_add_f32 v[56:57],v[56:57],v[58:59]
v_pk_add_f32 v[60:61],v[60:61],v[62:63]
v_add_f32 v52,v52,v53
v_pk_add_f32 v[56:57],v[56:57],v[60:61]
v_mul_f32 v52,s68,v52
v_add_f32 v53,v56,v57
v_mul_f32 v53,s68,v53
v_fma_f32 v53,-v52,v52,v53
v_max_f32 v53,0,v53
v_add_f32 v54,s69,v53
v_fma_f32 v55,s69,v54,v53
v_rsq_f32 v65,v54
v_rsq_f32 v54,v55
v_mul_f32_e64 v64,-v52,v65
v_mul_f32 v63,v55,v54
v_mul_f32 v61,s76,v54
v_mul_f32 v55,v54,v54
v_mul_f32 v62,s77,v55
v_mul_f32 v62,v62,v61
v_cndmask_b32 v60,v52,v64,s[74:75]
v_cndmask_b32 v61,v61,v65,s[74:75]
buffer_store_dwordx4 v[60:63],v4,s[36:39],s59 offen offset:0 sc0 sc1
s_mov_b32 s63,s60
s_lshr_b32 s78,s45,1
s_and_b32 s79,s45,1
s_lshl_b32 s79,s79,7
s_lshl_b32 s57,s78,15
s_add_u32 s57,s57,s79
s_add_u32 s57,s57,12582912
s_add_u32 s58,s57,4096
s_lshl_b32 s59,s78,9
s_add_u32 s59,s59,s79
s_add_u32 s59,s59,107020288
s_lshl_b32 s60,s45,9
s_add_u32 s60,s60,s66
s_add_u32 s60,s60,131072
s_add_u32 s45,s45,s46
s_lshr_b32 s78,s45,1
s_and_b32 s79,s45,1
s_lshl_b32 s79,s79,7
s_lshl_b32 s55,s78,15
s_add_u32 s55,s55,s79
s_mul_i32 s56,s45,16384
s_add_u32 s56,s56,98566144
s_lshl_b32 s61,s78,11
s_add_u32 s61,s61,110587904
s_add_u32 s78,s45,s46
s_lshr_b32 s78,s78,1
s_lshl_b32 s62,s78,11
s_add_u32 s62,s62,110587904
s_waitcnt vmcnt(6)
s_branch Lr_it1_%=
Lr_tb0_%=:
v_add_f32 v53,s69,v53
v_rsq_f32 v48,v53
s_nop 0
v_mul_f32_e64 v50,-v52,v48
v_mov_b32 v49,v48
v_mov_b32 v51,v50
v_pk_fma_f32 v[52:53],v[116:117],v[48:49],v[50:51]
v_pk_fma_f32 v[54:55],v[118:119],v[48:49],v[50:51]
v_pk_fma_f32 v[56:57],v[120:121],v[48:49],v[50:51]
v_pk_fma_f32 v[58:59],v[122:123],v[48:49],v[50:51]
s_add_u32 s78,s55,71303168
buffer_load_dwordx4 v[196:199],v1,s[36:39],s78 offen offset:0 sc1
buffer_load_dwordx4 v[200:203],v1,s[36:39],s78 offen offset:2048 sc1
s_add_u32 s78,s55,71307264
buffer_load_dwordx4 v[204:207],v1,s[36:39],s78 offen offset:0 sc1
buffer_load_dwordx4 v[208:211],v1,s[36:39],s78 offen offset:2048 sc1
v_pk_fma_f32 v[116:117],v[52:53],v[84:85],v[116:117]
v_pk_fma_f32 v[118:119],v[54:55],v[86:87],v[118:119]
v_pk_fma_f32 v[120:121],v[56:57],v[88:89],v[120:121]
v_pk_fma_f32 v[122:123],v[58:59],v[90:91],v[122:123]
v_cvt_pk_bf16_f32 v148,v116,v117
v_cvt_pk_bf16_f32 v149,v118,v119
v_cvt_pk_bf16_f32 v150,v120,v121
v_cvt_pk_bf16_f32 v151,v122,v123
v_pk_fma_f32 v[52:53],v[124:125],v[48:49],v[50:51]
v_pk_fma_f32 v[54:55],v[126:127],v[48:49],v[50:51]
v_pk_fma_f32 v[56:57],v[128:129],v[48:49],v[50:51]
v_pk_fma_f32 v[58:59],v[130:131],v[48:49],v[50:51]
s_add_u32 s78,s55,89128960
buffer_load_dwordx4 v[212:215],v1,s[36:39],s78 offen offset:0 sc1
buffer_load_dwordx4 v[216:219],v1,s[36:39],s78 offen offset:2048 sc1
s_add_u32 s78,s55,89133056
buffer_load_dwordx4 v[220:223],v1,s[36:39],s78 offen offset:0 sc1
buffer_load_dwordx4 v[224:227],v1,s[36:39],s78 offen offset:2048 sc1
v_pk_fma_f32 v[124:125],v[52:53],v[92:93],v[124:125]
v_pk_fma_f32 v[126:127],v[54:55],v[94:95],v[126:127]
v_pk_fma_f32 v[128:129],v[56:57],v[96:97],v[128:129]
v_pk_fma_f32 v[130:131],v[58:59],v[98:99],v[130:131]
v_cvt_pk_bf16_f32 v152,v124,v125
v_cvt_pk_bf16_f32 v153,v126,v127
v_cvt_pk_bf16_f32 v154,v128,v129
v_cvt_pk_bf16_f32 v155,v130,v131
v_pk_fma_f32 v[52:53],v[132:133],v[48:49],v[50:51]
v_pk_fma_f32 v[54:55],v[134:135],v[48:49],v[50:51]
v_pk_fma_f32 v[56:57],v[136:137],v[48:49],v[50:51]
v_pk_fma_f32 v[58:59],v[138:139],v[48:49],v[50:51]
s_add_u32 m0,s65,16384
s_nop 0
D 9,56,0
D 9,56,1024
D 9,56,2048
D 9,56,3072
v_pk_fma_f32 v[132:133],v[52:53],v[100:101],v[132:133]
v_pk_fma_f32 v[134:135],v[54:55],v[102:103],v[134:135]
v_pk_fma_f32 v[136:137],v[56:57],v[104:105],v[136:137]
v_pk_fma_f32 v[138:139],v[58:59],v[106:107],v[138:139]
v_cvt_pk_bf16_f32 v156,v132,v133
v_cvt_pk_bf16_f32 v157,v134,v135
v_cvt_pk_bf16_f32 v158,v136,v137
v_cvt_pk_bf16_f32 v159,v138,v139
v_pk_fma_f32 v[52:53],v[140:141],v[48:49],v[50:51]
v_pk_fma_f32 v[54:55],v[142:143],v[48:49],v[50:51]
v_pk_fma_f32 v[56:57],v[144:145],v[48:49],v[50:51]
v_pk_fma_f32 v[58:59],v[146:147],v[48:49],v[50:51]
v_pk_fma_f32 v[140:141],v[52:53],v[108:109],v[140:141]
v_pk_fma_f32 v[142:143],v[54:55],v[110:111],v[142:143]
v_pk_fma_f32 v[144:145],v[56:57],v[112:113],v[144:145]
v_pk_fma_f32 v[146:147],v[58:59],v[114:115],v[146:147]
v_cvt_pk_bf16_f32 v160,v140,v141
v_cvt_pk_bf16_f32 v161,v142,v143
v_cvt_pk_bf16_f32 v162,v144,v145
v_cvt_pk_bf16_f32 v163,v146,v147
v_mfma_f32_16x16x32_bf16 v[36:39],v[32:35],v[148:151],0
buffer_load_dword v14,v6,s[36:39],s62 offen offset:0 sc1
buffer_store_dwordx4 v[148:151],v1,s[36:39],s57 offen offset:0 sc0 sc1
v_mfma_f32_16x16x32_bf16 v[40:43],v[148:151],v[148:151],0
M 36,v[32:35],152
buffer_store_dwordx4 v[152:155],v1,s[36:39],s57 offen offset:2048 sc0 sc1
M 40,v[152:155],152
M 36,v[32:35],156
buffer_store_dwordx4 v[156:159],v1,s[36:39],s58 offen offset:0 sc0 sc1
M 40,v[156:159],156
M 36,v[32:35],160
buffer_store_dwordx4 v[160:163],v1,s[36:39],s58 offen offset:2048 sc0 sc1
M 40,v[160:163],160
s_nop 5
v_add_f32_dpp v44,v36,v36 row_ror:8 row_mask:0xf bank_mask:0xf
s_nop 0
v_cndmask_b32 v45,v40,v41,s[70:71]
v_cndmask_b32 v46,v42,v43,s[70:71]
ds_write_b32 v2,v44 offset:1024
v_cndmask_b32 v45,v45,v46,s[72:73]
ds_write_b32 v0,v45 offset:1024
L 0
s_barrier
R 52,3,1024
R 56,3,1040
R 60,3,1056
L 0
v_pk_add_f32 v[52:53],v[52:53],v[54:55]
v_pk_add_f32 v[56:57],v[56:57],v[58:59]
v_pk_add_f32 v[60:61],v[60:61],v[62:63]
v_add_f32 v52,v52,v53
v_pk_add_f32 v[56:57],v[56:57],v[60:61]
v_mul_f32 v52,s68,v52
v_add_f32 v53,v56,v57
v_mul_f32 v53,s68,v53
v_fma_f32 v53,-v52,v52,v53
v_max_f32 v53,0,v53
v_add_f32 v54,s69,v53
v_fma_f32 v55,s69,v54,v53
v_rsq_f32 v65,v54
v_rsq_f32 v54,v55
v_mul_f32_e64 v64,-v52,v65
v_mul_f32 v63,v55,v54
v_mul_f32 v61,s76,v54
v_mul_f32 v55,v54,v54
v_mul_f32 v62,s77,v55
v_mul_f32 v62,v62,v61
v_cndmask_b32 v60,v52,v64,s[74:75]
v_cndmask_b32 v61,v61,v65,s[74:75]
buffer_store_dwordx4 v[60:63],v4,s[36:39],s59 offen offset:0 sc0 sc1
s_mov_b32 s63,s60
s_lshr_b32 s78,s45,1
s_and_b32 s79,s45,1
s_lshl_b32 s79,s79,7
s_lshl_b32 s57,s78,15
s_add_u32 s57,s57,s79
s_add_u32 s57,s57,12582912
s_add_u32 s58,s57,4096
s_lshl_b32 s59,s78,9
s_add_u32 s59,s59,s79
s_add_u32 s59,s59,107020288
s_lshl_b32 s60,s45,9
s_add_u32 s60,s60,s66
s_add_u32 s60,s60,131072
s_add_u32 s45,s45,s46
s_lshr_b32 s78,s45,1
s_and_b32 s79,s45,1
s_lshl_b32 s79,s79,7
s_lshl_b32 s55,s78,15
s_add_u32 s55,s55,s79
s_mul_i32 s56,s45,16384
s_add_u32 s56,s56,98566144
s_lshl_b32 s61,s78,11
s_add_u32 s61,s61,110587904
s_add_u32 s78,s45,s46
s_lshr_b32 s78,s78,1
s_lshl_b32 s62,s78,11
s_add_u32 s62,s62,110587904
s_waitcnt vmcnt(5)
s_branch Lr_it1_%=
Lr_tc0_%=:
s_add_u32 s78,s55,71303168
buffer_load_dwordx4 v[196:199],v1,s[36:39],s78 offen offset:0 sc1
buffer_load_dwordx4 v[200:203],v1,s[36:39],s78 offen offset:2048 sc1
s_add_u32 s78,s55,71307264
buffer_load_dwordx4 v[204:207],v1,s[36:39],s78 offen offset:0 sc1
buffer_load_dwordx4 v[208:211],v1,s[36:39],s78 offen offset:2048 sc1
s_add_u32 s78,s55,89128960
buffer_load_dwordx4 v[212:215],v1,s[36:39],s78 offen offset:0 sc1
buffer_load_dwordx4 v[216:219],v1,s[36:39],s78 offen offset:2048 sc1
s_add_u32 s78,s55,89133056
buffer_load_dwordx4 v[220:223],v1,s[36:39],s78 offen offset:0 sc1
buffer_load_dwordx4 v[224:227],v1,s[36:39],s78 offen offset:2048 sc1
s_add_u32 m0,s65,16384
s_nop 0
D 9,56,0
D 9,56,1024
D 9,56,2048
D 9,56,3072
buffer_load_dword v14,v6,s[36:39],s62 offen offset:0 sc1
s_waitcnt vmcnt(13)
buffer_store_dword v13,v5,s[36:39],s63 offen offset:0 sc0 sc1
s_mov_b32 s63,0x7fff0000
s_lshr_b32 s78,s45,1
s_and_b32 s79,s45,1
s_lshl_b32 s79,s79,7
s_lshl_b32 s57,s78,15
s_add_u32 s57,s57,s79
s_add_u32 s57,s57,12582912
s_add_u32 s58,s57,4096
s_lshl_b32 s59,s78,9
s_add_u32 s59,s59,s79
s_add_u32 s59,s59,107020288
s_lshl_b32 s60,s45,9
s_add_u32 s60,s60,s66
s_add_u32 s60,s60,131072
s_add_u32 s45,s45,s46
s_lshr_b32 s78,s45,1
s_and_b32 s79,s45,1
s_lshl_b32 s79,s79,7
s_lshl_b32 s55,s78,15
s_add_u32 s55,s55,s79
s_mul_i32 s56,s45,16384
s_add_u32 s56,s56,98566144
s_lshl_b32 s61,s78,11
s_add_u32 s61,s61,110587904
s_add_u32 s78,s45,s46
s_lshr_b32 s78,s78,1
s_lshl_b32 s62,s78,11
s_add_u32 s62,s62,110587904
s_waitcnt vmcnt(1)
s_branch Lr_it1_%=
Lr_it1_%=:
T v[68:69],10,16384
T v[70:71],10,16448
T v[72:73],10,17408
T v[74:75],10,17472
T v[76:77],10,18432
T v[78:79],10,18496
T v[80:81],10,19456
T v[82:83],10,19520
s_nop 0
v_cmp_eq_u32 vcc,s53,v14
s_nop 0
s_cmp_eq_u64 vcc,-1
s_cselect_b32 s78,s45,256
s_cmp_lt_u32 s78,256
s_cbranch_scc1 Lr_ta1_%=
buffer_load_dword v14,v6,s[36:39],s61 offen offset:0 sc1
v_mfma_f32_16x16x32_bf16 v[116:119],v[26:29],v[196:199],0
L 6
v_lshlrev_b32 v84,16,v68
v_and_b32 v85,0xffff0000,v68
v_mfma_f32_16x16x32_bf16 v[120:123],v[24:27],v[196:199],0
L 4
v_lshlrev_b32 v86,16,v69
v_and_b32 v87,0xffff0000,v69
M 116,v[26:29],212
L 2
v_lshlrev_b32 v88,16,v70
v_and_b32 v89,0xffff0000,v70
M 120,v[24:27],212
L 0
v_lshlrev_b32 v90,16,v71
v_and_b32 v91,0xffff0000,v71
v_mfma_f32_16x16x32_bf16 v[124:127],v[26:29],v[200:203],0
v_lshlrev_b32 v92,16,v72
v_and_b32 v93,0xffff0000,v72
v_mfma_f32_16x16x32_bf16 v[128:131],v[24:27],v[200:203],0
v_lshlrev_b32 v94,16,v73
v_and_b32 v95,0xffff0000,v73
M 124,v[26:29],216
v_cvt_pk_bf16_f32 v148,v116,v117
v_cvt_pk_bf16_f32 v149,v118,v119
M 128,v[24:27],216
v_cvt_pk_bf16_f32 v150,v120,v121
v_cvt_pk_bf16_f32 v151,v122,v123
v_mfma_f32_16x16x32_bf16 v[132:135],v[26:29],v[204:207],0
v_lshlrev_b32 v96,16,v74
v_and_b32 v97,0xffff0000,v74
)"
R"(v_mfma_f32_16x16x32_bf16 v[136:139],v[24:27],v[204:207],0
v_lshlrev_b32 v98,16,v75
v_and_b32 v99,0xffff0000,v75
M 132,v[26:29],220
v_cvt_pk_bf16_f32 v152,v124,v125
v_cvt_pk_bf16_f32 v153,v126,v127
M 136,v[24:27],220
v_cvt_pk_bf16_f32 v154,v128,v129
v_cvt_pk_bf16_f32 v155,v130,v131
v_mfma_f32_16x16x32_bf16 v[140:143],v[26:29],v[208:211],0
v_lshlrev_b32 v100,16,v76
v_and_b32 v101,0xffff0000,v76
v_mfma_f32_16x16x32_bf16 v[144:147],v[24:27],v[208:211],0
v_lshlrev_b32 v102,16,v77
v_and_b32 v103,0xffff0000,v77
M 140,v[26:29],224
v_cvt_pk_bf16_f32 v156,v132,v133
v_cvt_pk_bf16_f32 v157,v134,v135
M 144,v[24:27],224
v_cvt_pk_bf16_f32 v158,v136,v137
v_cvt_pk_bf16_f32 v159,v138,v139
v_mfma_f32_16x16x32_bf16 v[36:39],v[32:35],v[148:151],0
v_lshlrev_b32 v104,16,v78
v_and_b32 v105,0xffff0000,v78
v_mfma_f32_16x16x32_bf16 v[40:43],v[148:151],v[148:151],0
v_lshlrev_b32 v106,16,v79
v_and_b32 v107,0xffff0000,v79
M 36,v[32:35],152
v_cvt_pk_bf16_f32 v160,v140,v141
v_cvt_pk_bf16_f32 v161,v142,v143
M 40,v[152:155],152
v_cvt_pk_bf16_f32 v162,v144,v145
v_cvt_pk_bf16_f32 v163,v146,v147
M 36,v[32:35],156
v_lshlrev_b32 v108,16,v80
v_and_b32 v109,0xffff0000,v80
M 40,v[156:159],156
v_lshlrev_b32 v110,16,v81
v_and_b32 v111,0xffff0000,v81
M 36,v[32:35],160
v_lshlrev_b32 v112,16,v82
v_and_b32 v113,0xffff0000,v82
M 40,v[160:163],160
v_lshlrev_b32 v114,16,v83
v_and_b32 v115,0xffff0000,v83
s_nop 2
v_add_f32_dpp v44,v36,v36 row_ror:8 row_mask:0xf bank_mask:0xf
s_nop 1
v_cndmask_b32 v45,v40,v41,s[70:71]
v_cndmask_b32 v46,v42,v43,s[70:71]
ds_write_b32 v2,v44 offset:0
v_cndmask_b32 v45,v45,v46,s[72:73]
ds_write_b32 v0,v45 offset:0
L 0
s_barrier
R 52,3,0
R 56,3,16
R 60,3,32
L 0
v_pk_add_f32 v[52:53],v[52:53],v[54:55]
v_pk_add_f32 v[56:57],v[56:57],v[58:59]
v_pk_add_f32 v[60:61],v[60:61],v[62:63]
v_add_f32 v52,v52,v53
v_pk_add_f32 v[56:57],v[56:57],v[60:61]
v_mul_f32 v52,s68,v52
v_add_f32 v53,v56,v57
v_mul_f32 v53,s68,v53
v_fma_f32 v53,-v52,v52,v53
v_max_f32 v53,0,v53
s_waitcnt vmcnt(1)
buffer_store_dword v13,v5,s[36:39],s63 offen offset:0 sc0 sc1
s_mov_b32 s63,0x7fff0000
s_waitcnt vmcnt(1)
v_cmp_eq_u32 vcc,s53,v14
s_nop 0
s_cmp_eq_u64 vcc,-1
s_cselect_b32 s78,s45,256
s_cmp_lt_u32 s78,256
s_cbranch_scc1 Lr_tb1_%=
buffer_load_dword v14,v6,s[36:39],s61 offen offset:0 sc1
v_add_f32 v53,s69,v53
v_rsq_f32 v48,v53
s_nop 0
v_mul_f32_e64 v50,-v52,v48
v_mov_b32 v49,v48
v_mov_b32 v51,v50
v_pk_fma_f32 v[52:53],v[116:117],v[48:49],v[50:51]
v_pk_fma_f32 v[54:55],v[118:119],v[48:49],v[50:51]
v_pk_fma_f32 v[56:57],v[120:121],v[48:49],v[50:51]
v_pk_fma_f32 v[58:59],v[122:123],v[48:49],v[50:51]
v_pk_fma_f32 v[116:117],v[52:53],v[84:85],v[116:117]
v_pk_fma_f32 v[118:119],v[54:55],v[86:87],v[118:119]
v_pk_fma_f32 v[120:121],v[56:57],v[88:89],v[120:121]
v_pk_fma_f32 v[122:123],v[58:59],v[90:91],v[122:123]
v_cvt_pk_bf16_f32 v148,v116,v117
v_cvt_pk_bf16_f32 v149,v118,v119
v_cvt_pk_bf16_f32 v150,v120,v121
v_cvt_pk_bf16_f32 v151,v122,v123
v_pk_fma_f32 v[52:53],v[124:125],v[48:49],v[50:51]
v_pk_fma_f32 v[54:55],v[126:127],v[48:49],v[50:51]
v_pk_fma_f32 v[56:57],v[128:129],v[48:49],v[50:51]
v_pk_fma_f32 v[58:59],v[130:131],v[48:49],v[50:51]
v_pk_fma_f32 v[124:125],v[52:53],v[92:93],v[124:125]
v_pk_fma_f32 v[126:127],v[54:55],v[94:95],v[126:127]
v_pk_fma_f32 v[128:129],v[56:57],v[96:97],v[128:129]
v_pk_fma_f32 v[130:131],v[58:59],v[98:99],v[130:131]
v_cvt_pk_bf16_f32 v152,v124,v125
v_cvt_pk_bf16_f32 v153,v126,v127
v_cvt_pk_bf16_f32 v154,v128,v129
v_cvt_pk_bf16_f32 v155,v130,v131
v_pk_fma_f32 v[52:53],v[132:133],v[48:49],v[50:51]
v_pk_fma_f32 v[54:55],v[134:135],v[48:49],v[50:51]
v_pk_fma_f32 v[56:57],v[136:137],v[48:49],v[50:51]
v_pk_fma_f32 v[58:59],v[138:139],v[48:49],v[50:51]
v_pk_fma_f32 v[132:133],v[52:53],v[100:101],v[132:133]
v_pk_fma_f32 v[134:135],v[54:55],v[102:103],v[134:135]
v_pk_fma_f32 v[136:137],v[56:57],v[104:105],v[136:137]
v_pk_fma_f32 v[138:139],v[58:59],v[106:107],v[138:139]
v_cvt_pk_bf16_f32 v156,v132,v133
v_cvt_pk_bf16_f32 v157,v134,v135
v_cvt_pk_bf16_f32 v158,v136,v137
v_cvt_pk_bf16_f32 v159,v138,v139
v_pk_fma_f32 v[52:53],v[140:141],v[48:49],v[50:51]
v_pk_fma_f32 v[54:55],v[142:143],v[48:49],v[50:51]
v_pk_fma_f32 v[56:57],v[144:145],v[48:49],v[50:51]
v_pk_fma_f32 v[58:59],v[146:147],v[48:49],v[50:51]
v_pk_fma_f32 v[140:141],v[52:53],v[108:109],v[140:141]
v_pk_fma_f32 v[142:143],v[54:55],v[110:111],v[142:143]
v_pk_fma_f32 v[144:145],v[56:57],v[112:113],v[144:145]
v_pk_fma_f32 v[146:147],v[58:59],v[114:115],v[146:147]
v_cvt_pk_bf16_f32 v160,v140,v141
v_cvt_pk_bf16_f32 v161,v142,v143
v_cvt_pk_bf16_f32 v162,v144,v145
v_cvt_pk_bf16_f32 v163,v146,v147
v_mfma_f32_16x16x32_bf16 v[36:39],v[32:35],v[148:151],0
buffer_store_dwordx4 v[148:151],v1,s[36:39],s57 offen offset:0 sc0 sc1
v_mfma_f32_16x16x32_bf16 v[40:43],v[148:151],v[148:151],0
M 36,v[32:35],152
buffer_store_dwordx4 v[152:155],v1,s[36:39],s57 offen offset:2048 sc0 sc1
M 40,v[152:155],152
M 36,v[32:35],156
buffer_store_dwordx4 v[156:159],v1,s[36:39],s58 offen offset:0 sc0 sc1
M 40,v[156:159],156
M 36,v[32:35],160
buffer_store_dwordx4 v[160:163],v1,s[36:39],s58 offen offset:2048 sc0 sc1
M 40,v[160:163],160
s_nop 5
v_add_f32_dpp v44,v36,v36 row_ror:8 row_mask:0xf bank_mask:0xf
s_nop 0
v_cndmask_b32 v45,v40,v41,s[70:71]
v_cndmask_b32 v46,v42,v43,s[70:71]
ds_write_b32 v2,v44 offset:1024
v_cndmask_b32 v45,v45,v46,s[72:73]
ds_write_b32 v0,v45 offset:1024
L 0
s_barrier
R 52,3,1024
R 56,3,1040
R 60,3,1056
L 0
v_pk_add_f32 v[52:53],v[52:53],v[54:55]
v_pk_add_f32 v[56:57],v[56:57],v[58:59]
v_pk_add_f32 v[60:61],v[60:61],v[62:63]
v_add_f32 v52,v52,v53
v_pk_add_f32 v[56:57],v[56:57],v[60:61]
v_mul_f32 v52,s68,v52
v_add_f32 v53,v56,v57
v_mul_f32 v53,s68,v53
v_fma_f32 v53,-v52,v52,v53
v_max_f32 v53,0,v53
v_add_f32 v54,s69,v53
v_fma_f32 v55,s69,v54,v53
v_rsq_f32 v65,v54
v_rsq_f32 v54,v55
v_mul_f32_e64 v64,-v52,v65
v_mul_f32 v63,v55,v54
v_mul_f32 v61,s76,v54
v_mul_f32 v55,v54,v54
v_mul_f32 v62,s77,v55
v_mul_f32 v62,v62,v61
v_cndmask_b32 v60,v52,v64,s[74:75]
v_cndmask_b32 v61,v61,v65,s[74:75]
buffer_store_dwordx4 v[60:63],v4,s[36:39],s59 offen offset:0 sc0 sc1
s_mov_b32 s63,s60
s_waitcnt vmcnt(5)
v_cmp_eq_u32 vcc,s53,v14
s_nop 0
s_cmp_eq_u64 vcc,-1
s_cselect_b32 s78,s45,256
s_cmp_lt_u32 s78,256
s_cbranch_scc1 Lr_tc1_%=
s_waitcnt vmcnt(0)
buffer_store_dword v13,v5,s[36:39],s63 offen offset:0 sc0 sc1
s_mov_b32 s63,0x7fff0000
s_cmp_ge_u32 s45,256
s_cbranch_scc1 Lr_done_%=
Lr_bp1_%=:
buffer_load_dword v14,v6,s[36:39],s61 offen offset:0 sc1
s_add_u32 s82,s82,1
s_waitcnt vmcnt(0)
v_cmp_eq_u32 vcc,s53,v14
s_nop 0
s_cmp_eq_u64 vcc,-1
s_cbranch_scc1 Lr_bq1_%=
s_sleep 2
s_cmp_lt_u32 s82,20000
s_cbranch_scc1 Lr_bp1_%=
Lr_bq1_%=:
s_add_u32 s78,s55,71303168
buffer_load_dwordx4 v[164:167],v1,s[36:39],s78 offen offset:0 sc1
buffer_load_dwordx4 v[168:171],v1,s[36:39],s78 offen offset:2048 sc1
s_add_u32 s78,s55,71307264
buffer_load_dwordx4 v[172:175],v1,s[36:39],s78 offen offset:0 sc1
buffer_load_dwordx4 v[176:179],v1,s[36:39],s78 offen offset:2048 sc1
s_add_u32 s78,s55,89128960
buffer_load_dwordx4 v[180:183],v1,s[36:39],s78 offen offset:0 sc1
buffer_load_dwordx4 v[184:187],v1,s[36:39],s78 offen offset:2048 sc1
s_add_u32 s78,s55,89133056
buffer_load_dwordx4 v[188:191],v1,s[36:39],s78 offen offset:0 sc1
buffer_load_dwordx4 v[192:195],v1,s[36:39],s78 offen offset:2048 sc1
s_add_u32 m0,s65,0
s_nop 0
D 9,56,0
D 9,56,1024
D 9,56,2048
D 9,56,3072
buffer_load_dword v14,v6,s[36:39],s62 offen offset:0 sc1
s_lshr_b32 s78,s45,1
s_and_b32 s79,s45,1
s_lshl_b32 s79,s79,7
s_lshl_b32 s57,s78,15
s_add_u32 s57,s57,s79
s_add_u32 s57,s57,12582912
s_add_u32 s58,s57,4096
s_lshl_b32 s59,s78,9
s_add_u32 s59,s59,s79
s_add_u32 s59,s59,107020288
s_lshl_b32 s60,s45,9
s_add_u32 s60,s60,s66
s_add_u32 s60,s60,131072
s_add_u32 s45,s45,s46
s_lshr_b32 s78,s45,1
s_and_b32 s79,s45,1
s_lshl_b32 s79,s79,7
s_lshl_b32 s55,s78,15
s_add_u32 s55,s55,s79
s_mul_i32 s56,s45,16384
s_add_u32 s56,s56,98566144
s_lshl_b32 s61,s78,11
s_add_u32 s61,s61,110587904
s_add_u32 s78,s45,s46
s_lshr_b32 s78,s78,1
s_lshl_b32 s62,s78,11
s_add_u32 s62,s62,110587904
s_waitcnt vmcnt(0)
s_branch Lr_it0_%=
Lr_ta1_%=:
v_mfma_f32_16x16x32_bf16 v[116:119],v[26:29],v[196:199],0
s_add_u32 s78,s55,71303168
buffer_load_dwordx4 v[164:167],v1,s[36:39],s78 offen offset:0 sc1
v_mfma_f32_16x16x32_bf16 v[120:123],v[24:27],v[196:199],0
L 6
buffer_load_dwordx4 v[168:171],v1,s[36:39],s78 offen offset:2048 sc1
M 116,v[26:29],212
s_add_u32 s78,s55,71307264
buffer_load_dwordx4 v[172:175],v1,s[36:39],s78 offen offset:0 sc1
M 120,v[24:27],212
L 4
buffer_load_dwordx4 v[176:179],v1,s[36:39],s78 offen offset:2048 sc1
v_mfma_f32_16x16x32_bf16 v[124:127],v[26:29],v[200:203],0
s_add_u32 s78,s55,89128960
buffer_load_dwordx4 v[180:183],v1,s[36:39],s78 offen offset:0 sc1
v_mfma_f32_16x16x32_bf16 v[128:131],v[24:27],v[200:203],0
L 2
buffer_load_dwordx4 v[184:187],v1,s[36:39],s78 offen offset:2048 sc1
M 124,v[26:29],216
L 0
v_cvt_pk_bf16_f32 v148,v116,v117
v_cvt_pk_bf16_f32 v149,v118,v119
M 128,v[24:27],216
s_add_u32 s78,s55,89133056
buffer_load_dwordx4 v[188:191],v1,s[36:39],s78 offen offset:0 sc1
v_mfma_f32_16x16x32_bf16 v[132:135],v[26:29],v[204:207],0
s_add_u32 m0,s65,0
buffer_load_dwordx4 v[192:195],v1,s[36:39],s78 offen offset:2048 sc1
v_mfma_f32_16x16x32_bf16 v[136:139],v[24:27],v[204:207],0
D 9,56,0
M 132,v[26:29],220
D 9,56,1024
M 136,v[24:27],220
D 9,56,2048
v_mfma_f32_16x16x32_bf16 v[140:143],v[26:29],v[208:211],0
D 9,56,3072
v_mfma_f32_16x16x32_bf16 v[144:147],v[24:27],v[208:211],0
v_cvt_pk_bf16_f32 v150,v120,v121
v_cvt_pk_bf16_f32 v151,v122,v123
M 140,v[26:29],224
v_cvt_pk_bf16_f32 v152,v124,v125
v_cvt_pk_bf16_f32 v153,v126,v127
M 144,v[24:27],224
v_cvt_pk_bf16_f32 v154,v128,v129
v_cvt_pk_bf16_f32 v155,v130,v131
v_mfma_f32_16x16x32_bf16 v[36:39],v[32:35],v[148:151],0
v_cvt_pk_bf16_f32 v156,v132,v133
v_cvt_pk_bf16_f32 v157,v134,v135
v_mfma_f32_16x16x32_bf16 v[40:43],v[148:151],v[148:151],0
v_cvt_pk_bf16_f32 v158,v136,v137
v_cvt_pk_bf16_f32 v159,v138,v139
M 36,v[32:35],152
v_cvt_pk_bf16_f32 v160,v140,v141
v_cvt_pk_bf16_f32 v161,v142,v143
M 40,v[152:155],152
v_cvt_pk_bf16_f32 v162,v144,v145
v_cvt_pk_bf16_f32 v163,v146,v147
M 36,v[32:35],156
v_lshlrev_b32 v84,16,v68
v_and_b32 v85,0xffff0000,v68
M 40,v[156:159],156
v_lshlrev_b32 v86,16,v69
v_and_b32 v87,0xffff0000,v69
M 36,v[32:35],160
v_lshlrev_b32 v88,16,v70
v_and_b32 v89,0xffff0000,v70
M 40,v[160:163],160
v_lshlrev_b32 v90,16,v71
v_and_b32 v91,0xffff0000,v71
v_lshlrev_b32 v92,16,v72
v_and_b32 v93,0xffff0000,v72
v_lshlrev_b32 v94,16,v73
v_and_b32 v95,0xffff0000,v73
v_lshlrev_b32 v96,16,v74
v_and_b32 v97,0xffff0000,v74
v_lshlrev_b32 v98,16,v75
v_and_b32 v99,0xffff0000,v75
v_lshlrev_b32 v100,16,v76
v_and_b32 v101,0xffff0000,v76
v_lshlrev_b32 v102,16,v77
v_and_b32 v103,0xffff0000,v77
v_lshlrev_b32 v104,16,v78
v_and_b32 v105,0xffff0000,v78
v_lshlrev_b32 v106,16,v79
v_and_b32 v107,0xffff0000,v79
v_lshlrev_b32 v108,16,v80
v_and_b32 v109,0xffff0000,v80
v_lshlrev_b32 v110,16,v81
v_and_b32 v111,0xffff0000,v81
v_lshlrev_b32 v112,16,v82
v_and_b32 v113,0xffff0000,v82
v_lshlrev_b32 v114,16,v83
v_and_b32 v115,0xffff0000,v83
v_add_f32_dpp v44,v36,v36 row_ror:8 row_mask:0xf bank_mask:0xf
v_cndmask_b32 v45,v40,v41,s[70:71]
v_cndmask_b32 v46,v42,v43,s[70:71]
ds_write_b32 v2,v44 offset:0
v_cndmask_b32 v45,v45,v46,s[72:73]
ds_write_b32 v0,v45 offset:0
L 0
s_barrier
R 52,3,0
R 56,3,16
R 60,3,32
L 0
v_pk_add_f32 v[52:53],v[52:53],v[54:55]
v_pk_add_f32 v[56:57],v[56:57],v[58:59]
v_pk_add_f32 v[60:61],v[60:61],v[62:63]
v_add_f32 v52,v52,v53
v_pk_add_f32 v[56:57],v[56:57],v[60:61]
v_mul_f32 v52,s68,v52
v_add_f32 v53,v56,v57
v_mul_f32 v53,s68,v53
v_fma_f32 v53,-v52,v52,v53
v_max_f32 v53,0,v53
buffer_load_dword v14,v6,s[36:39],s62 offen offset:0 sc1
v_add_f32 v53,s69,v53
v_rsq_f32 v48,v53
s_nop 0
v_mul_f32_e64 v50,-v52,v48
v_mov_b32 v49,v48
v_mov_b32 v51,v50
v_pk_fma_f32 v[52:53],v[116:117],v[48:49],v[50:51]
v_pk_fma_f32 v[54:55],v[118:119],v[48:49],v[50:51]
v_pk_fma_f32 v[56:57],v[120:121],v[48:49],v[50:51]
v_pk_fma_f32 v[58:59],v[122:123],v[48:49],v[50:51]
v_pk_fma_f32 v[116:117],v[52:53],v[84:85],v[116:117]
v_pk_fma_f32 v[118:119],v[54:55],v[86:87],v[118:119]
v_pk_fma_f32 v[120:121],v[56:57],v[88:89],v[120:121]
v_pk_fma_f32 v[122:123],v[58:59],v[90:91],v[122:123]
v_cvt_pk_bf16_f32 v148,v116,v117
v_cvt_pk_bf16_f32 v149,v118,v119
v_cvt_pk_bf16_f32 v150,v120,v121
v_cvt_pk_bf16_f32 v151,v122,v123
v_pk_fma_f32 v[52:53],v[124:125],v[48:49],v[50:51]
v_pk_fma_f32 v[54:55],v[126:127],v[48:49],v[50:51]
v_pk_fma_f32 v[56:57],v[128:129],v[48:49],v[50:51]
v_pk_fma_f32 v[58:59],v[130:131],v[48:49],v[50:51]
v_pk_fma_f32 v[124:125],v[52:53],v[92:93],v[124:125]
v_pk_fma_f32 v[126:127],v[54:55],v[94:95],v[126:127]
v_pk_fma_f32 v[128:129],v[56:57],v[96:97],v[128:129]
v_pk_fma_f32 v[130:131],v[58:59],v[98:99],v[130:131]
v_cvt_pk_bf16_f32 v152,v124,v125
v_cvt_pk_bf16_f32 v153,v126,v127
v_cvt_pk_bf16_f32 v154,v128,v129
v_cvt_pk_bf16_f32 v155,v130,v131
v_pk_fma_f32 v[52:53],v[132:133],v[48:49],v[50:51]
v_pk_fma_f32 v[54:55],v[134:135],v[48:49],v[50:51]
v_pk_fma_f32 v[56:57],v[136:137],v[48:49],v[50:51]
v_pk_fma_f32 v[58:59],v[138:139],v[48:49],v[50:51]
v_pk_fma_f32 v[132:133],v[52:53],v[100:101],v[132:133]
v_pk_fma_f32 v[134:135],v[54:55],v[102:103],v[134:135]
v_pk_fma_f32 v[136:137],v[56:57],v[104:105],v[136:137]
v_pk_fma_f32 v[138:139],v[58:59],v[106:107],v[138:139]
v_cvt_pk_bf16_f32 v156,v132,v133
v_cvt_pk_bf16_f32 v157,v134,v135
v_cvt_pk_bf16_f32 v158,v136,v137
v_cvt_pk_bf16_f32 v159,v138,v139
v_pk_fma_f32 v[52:53],v[140:141],v[48:49],v[50:51]
v_pk_fma_f32 v[54:55],v[142:143],v[48:49],v[50:51]
v_pk_fma_f32 v[56:57],v[144:145],v[48:49],v[50:51]
v_pk_fma_f32 v[58:59],v[146:147],v[48:49],v[50:51]
v_pk_fma_f32 v[140:141],v[52:53],v[108:109],v[140:141]
v_pk_fma_f32 v[142:143],v[54:55],v[110:111],v[142:143]
v_pk_fma_f32 v[144:145],v[56:57],v[112:113],v[144:145]
v_pk_fma_f32 v[146:147],v[58:59],v[114:115],v[146:147]
v_cvt_pk_bf16_f32 v160,v140,v141
v_cvt_pk_bf16_f32 v161,v142,v143
v_cvt_pk_bf16_f32 v162,v144,v145
v_cvt_pk_bf16_f32 v163,v146,v147
s_waitcnt vmcnt(13)
buffer_store_dword v13,v5,s[36:39],s63 offen offset:0 sc0 sc1
s_mov_b32 s63,0x7fff0000
v_mfma_f32_16x16x32_bf16 v[36:39],v[32:35],v[148:151],0
buffer_store_dwordx4 v[148:151],v1,s[36:39],s57 offen offset:0 sc0 sc1
v_mfma_f32_16x16x32_bf16 v[40:43],v[148:151],v[148:151],0
M 36,v[32:35],152
buffer_store_dwordx4 v[152:155],v1,s[36:39],s57 offen offset:2048 sc0 sc1
M 40,v[152:155],152
M 36,v[32:35],156
buffer_store_dwordx4 v[156:159],v1,s[36:39],s58 offen offset:0 sc0 sc1
M 40,v[156:159],156
M 36,v[32:35],160
buffer_store_dwordx4 v[160:163],v1,s[36:39],s58 offen offset:2048 sc0 sc1
M 40,v[160:163],160
s_nop 5
v_add_f32_dpp v44,v36,v36 row_ror:8 row_mask:0xf bank_mask:0xf
s_nop 0
v_cndmask_b32 v45,v40,v41,s[70:71]
v_cndmask_b32 v46,v42,v43,s[70:71]
ds_write_b32 v2,v44 offset:1024
v_cndmask_b32 v45,v45,v46,s[72:73]
ds_write_b32 v0,v45 offset:1024
L 0
s_barrier
R 52,3,1024
R 56,3,1040
R 60,3,1056
L 0
v_pk_add_f32 v[52:53],v[52:53],v[54:55]
v_pk_add_f32 v[56:57],v[56:57],v[58:59]
v_pk_add_f32 v[60:61],v[60:61],v[62:63]
v_add_f32 v52,v52,v53
v_pk_add_f32 v[56:57],v[56:57],v[60:61]
v_mul_f32 v52,s68,v52
v_add_f32 v53,v56,v57
v_mul_f32 v53,s68,v53
v_fma_f32 v53,-v52,v52,v53
v_max_f32 v53,0,v53
v_add_f32 v54,s69,v53
v_fma_f32 v55,s69,v54,v53
v_rsq_f32 v65,v54
v_rsq_f32 v54,v55
v_mul_f32_e64 v64,-v52,v65
v_mul_f32 v63,v55,v54
v_mul_f32 v61,s76,v54
v_mul_f32 v55,v54,v54
v_mul_f32 v62,s77,v55
v_mul_f32 v62,v62,v61
v_cndmask_b32 v60,v52,v64,s[74:75]
v_cndmask_b32 v61,v61,v65,s[74:75]
buffer_store_dwordx4 v[60:63],v4,s[36:39],s59 offen offset:0 sc0 sc1
s_mov_b32 s63,s60
s_lshr_b32 s78,s45,1
s_and_b32 s79,s45,1
s_lshl_b32 s79,s79,7
s_lshl_b32 s57,s78,15
s_add_u32 s57,s57,s79
s_add_u32 s57,s57,12582912
s_add_u32 s58,s57,4096
s_lshl_b32 s59,s78,9
s_add_u32 s59,s59,s79
s_add_u32 s59,s59,107020288
s_lshl_b32 s60,s45,9
s_add_u32 s60,s60,s66
s_add_u32 s60,s60,131072
s_add_u32 s45,s45,s46
s_lshr_b32 s78,s45,1
s_and_b32 s79,s45,1
s_lshl_b32 s79,s79,7
s_lshl_b32 s55,s78,15
s_add_u32 s55,s55,s79
s_mul_i32 s56,s45,16384
)"
R"(s_add_u32 s56,s56,98566144
s_lshl_b32 s61,s78,11
s_add_u32 s61,s61,110587904
s_add_u32 s78,s45,s46
s_lshr_b32 s78,s78,1
s_lshl_b32 s62,s78,11
s_add_u32 s62,s62,110587904
s_waitcnt vmcnt(6)
s_branch Lr_it0_%=
Lr_tb1_%=:
v_add_f32 v53,s69,v53
v_rsq_f32 v48,v53
s_nop 0
v_mul_f32_e64 v50,-v52,v48
v_mov_b32 v49,v48
v_mov_b32 v51,v50
v_pk_fma_f32 v[52:53],v[116:117],v[48:49],v[50:51]
v_pk_fma_f32 v[54:55],v[118:119],v[48:49],v[50:51]
v_pk_fma_f32 v[56:57],v[120:121],v[48:49],v[50:51]
v_pk_fma_f32 v[58:59],v[122:123],v[48:49],v[50:51]
s_add_u32 s78,s55,71303168
buffer_load_dwordx4 v[164:167],v1,s[36:39],s78 offen offset:0 sc1
buffer_load_dwordx4 v[168:171],v1,s[36:39],s78 offen offset:2048 sc1
s_add_u32 s78,s55,71307264
buffer_load_dwordx4 v[172:175],v1,s[36:39],s78 offen offset:0 sc1
buffer_load_dwordx4 v[176:179],v1,s[36:39],s78 offen offset:2048 sc1
v_pk_fma_f32 v[116:117],v[52:53],v[84:85],v[116:117]
v_pk_fma_f32 v[118:119],v[54:55],v[86:87],v[118:119]
v_pk_fma_f32 v[120:121],v[56:57],v[88:89],v[120:121]
v_pk_fma_f32 v[122:123],v[58:59],v[90:91],v[122:123]
v_cvt_pk_bf16_f32 v148,v116,v117
v_cvt_pk_bf16_f32 v149,v118,v119
v_cvt_pk_bf16_f32 v150,v120,v121
v_cvt_pk_bf16_f32 v151,v122,v123
v_pk_fma_f32 v[52:53],v[124:125],v[48:49],v[50:51]
v_pk_fma_f32 v[54:55],v[126:127],v[48:49],v[50:51]
v_pk_fma_f32 v[56:57],v[128:129],v[48:49],v[50:51]
v_pk_fma_f32 v[58:59],v[130:131],v[48:49],v[50:51]
s_add_u32 s78,s55,89128960
buffer_load_dwordx4 v[180:183],v1,s[36:39],s78 offen offset:0 sc1
buffer_load_dwordx4 v[184:187],v1,s[36:39],s78 offen offset:2048 sc1
s_add_u32 s78,s55,89133056
buffer_load_dwordx4 v[188:191],v1,s[36:39],s78 offen offset:0 sc1
buffer_load_dwordx4 v[192:195],v1,s[36:39],s78 offen offset:2048 sc1
v_pk_fma_f32 v[124:125],v[52:53],v[92:93],v[124:125]
v_pk_fma_f32 v[126:127],v[54:55],v[94:95],v[126:127]
v_pk_fma_f32 v[128:129],v[56:57],v[96:97],v[128:129]
v_pk_fma_f32 v[130:131],v[58:59],v[98:99],v[130:131]
v_cvt_pk_bf16_f32 v152,v124,v125
v_cvt_pk_bf16_f32 v153,v126,v127
v_cvt_pk_bf16_f32 v154,v128,v129
v_cvt_pk_bf16_f32 v155,v130,v131
v_pk_fma_f32 v[52:53],v[132:133],v[48:49],v[50:51]
v_pk_fma_f32 v[54:55],v[134:135],v[48:49],v[50:51]
v_pk_fma_f32 v[56:57],v[136:137],v[48:49],v[50:51]
v_pk_fma_f32 v[58:59],v[138:139],v[48:49],v[50:51]
s_add_u32 m0,s65,0
s_nop 0
D 9,56,0
D 9,56,1024
D 9,56,2048
D 9,56,3072
v_pk_fma_f32 v[132:133],v[52:53],v[100:101],v[132:133]
v_pk_fma_f32 v[134:135],v[54:55],v[102:103],v[134:135]
v_pk_fma_f32 v[136:137],v[56:57],v[104:105],v[136:137]
v_pk_fma_f32 v[138:139],v[58:59],v[106:107],v[138:139]
v_cvt_pk_bf16_f32 v156,v132,v133
v_cvt_pk_bf16_f32 v157,v134,v135
v_cvt_pk_bf16_f32 v158,v136,v137
v_cvt_pk_bf16_f32 v159,v138,v139
v_pk_fma_f32 v[52:53],v[140:141],v[48:49],v[50:51]
v_pk_fma_f32 v[54:55],v[142:143],v[48:49],v[50:51]
v_pk_fma_f32 v[56:57],v[144:145],v[48:49],v[50:51]
v_pk_fma_f32 v[58:59],v[146:147],v[48:49],v[50:51]
v_pk_fma_f32 v[140:141],v[52:53],v[108:109],v[140:141]
v_pk_fma_f32 v[142:143],v[54:55],v[110:111],v[142:143]
v_pk_fma_f32 v[144:145],v[56:57],v[112:113],v[144:145]
v_pk_fma_f32 v[146:147],v[58:59],v[114:115],v[146:147]
v_cvt_pk_bf16_f32 v160,v140,v141
v_cvt_pk_bf16_f32 v161,v142,v143
v_cvt_pk_bf16_f32 v162,v144,v145
v_cvt_pk_bf16_f32 v163,v146,v147
v_mfma_f32_16x16x32_bf16 v[36:39],v[32:35],v[148:151],0
buffer_load_dword v14,v6,s[36:39],s62 offen offset:0 sc1
buffer_store_dwordx4 v[148:151],v1,s[36:39],s57 offen offset:0 sc0 sc1
v_mfma_f32_16x16x32_bf16 v[40:43],v[148:151],v[148:151],0
M 36,v[32:35],152
buffer_store_dwordx4 v[152:155],v1,s[36:39],s57 offen offset:2048 sc0 sc1
M 40,v[152:155],152
M 36,v[32:35],156
buffer_store_dwordx4 v[156:159],v1,s[36:39],s58 offen offset:0 sc0 sc1
M 40,v[156:159],156
M 36,v[32:35],160
buffer_store_dwordx4 v[160:163],v1,s[36:39],s58 offen offset:2048 sc0 sc1
M 40,v[160:163],160
s_nop 5
v_add_f32_dpp v44,v36,v36 row_ror:8 row_mask:0xf bank_mask:0xf
s_nop 0
v_cndmask_b32 v45,v40,v41,s[70:71]
v_cndmask_b32 v46,v42,v43,s[70:71]
ds_write_b32 v2,v44 offset:1024
v_cndmask_b32 v45,v45,v46,s[72:73]
ds_write_b32 v0,v45 offset:1024
L 0
s_barrier
R 52,3,1024
R 56,3,1040
R 60,3,1056
L 0
v_pk_add_f32 v[52:53],v[52:53],v[54:55]
v_pk_add_f32 v[56:57],v[56:57],v[58:59]
v_pk_add_f32 v[60:61],v[60:61],v[62:63]
v_add_f32 v52,v52,v53
v_pk_add_f32 v[56:57],v[56:57],v[60:61]
v_mul_f32 v52,s68,v52
v_add_f32 v53,v56,v57
v_mul_f32 v53,s68,v53
v_fma_f32 v53,-v52,v52,v53
v_max_f32 v53,0,v53
v_add_f32 v54,s69,v53
v_fma_f32 v55,s69,v54,v53
v_rsq_f32 v65,v54
v_rsq_f32 v54,v55
v_mul_f32_e64 v64,-v52,v65
v_mul_f32 v63,v55,v54
v_mul_f32 v61,s76,v54
v_mul_f32 v55,v54,v54
v_mul_f32 v62,s77,v55
v_mul_f32 v62,v62,v61
v_cndmask_b32 v60,v52,v64,s[74:75]
v_cndmask_b32 v61,v61,v65,s[74:75]
buffer_store_dwordx4 v[60:63],v4,s[36:39],s59 offen offset:0 sc0 sc1
s_mov_b32 s63,s60
s_lshr_b32 s78,s45,1
s_and_b32 s79,s45,1
s_lshl_b32 s79,s79,7
s_lshl_b32 s57,s78,15
s_add_u32 s57,s57,s79
s_add_u32 s57,s57,12582912
s_add_u32 s58,s57,4096
s_lshl_b32 s59,s78,9
s_add_u32 s59,s59,s79
s_add_u32 s59,s59,107020288
s_lshl_b32 s60,s45,9
s_add_u32 s60,s60,s66
s_add_u32 s60,s60,131072
s_add_u32 s45,s45,s46
s_lshr_b32 s78,s45,1
s_and_b32 s79,s45,1
s_lshl_b32 s79,s79,7
s_lshl_b32 s55,s78,15
s_add_u32 s55,s55,s79
s_mul_i32 s56,s45,16384
s_add_u32 s56,s56,98566144
s_lshl_b32 s61,s78,11
s_add_u32 s61,s61,110587904
s_add_u32 s78,s45,s46
s_lshr_b32 s78,s78,1
s_lshl_b32 s62,s78,11
s_add_u32 s62,s62,110587904
s_waitcnt vmcnt(5)
s_branch Lr_it0_%=
Lr_tc1_%=:
s_add_u32 s78,s55,71303168
buffer_load_dwordx4 v[164:167],v1,s[36:39],s78 offen offset:0 sc1
buffer_load_dwordx4 v[168:171],v1,s[36:39],s78 offen offset:2048 sc1
s_add_u32 s78,s55,71307264
buffer_load_dwordx4 v[172:175],v1,s[36:39],s78 offen offset:0 sc1
buffer_load_dwordx4 v[176:179],v1,s[36:39],s78 offen offset:2048 sc1
s_add_u32 s78,s55,89128960
buffer_load_dwordx4 v[180:183],v1,s[36:39],s78 offen offset:0 sc1
buffer_load_dwordx4 v[184:187],v1,s[36:39],s78 offen offset:2048 sc1
s_add_u32 s78,s55,89133056
buffer_load_dwordx4 v[188:191],v1,s[36:39],s78 offen offset:0 sc1
buffer_load_dwordx4 v[192:195],v1,s[36:39],s78 offen offset:2048 sc1
s_add_u32 m0,s65,0
s_nop 0
D 9,56,0
D 9,56,1024
D 9,56,2048
D 9,56,3072
buffer_load_dword v14,v6,s[36:39],s62 offen offset:0 sc1
s_waitcnt vmcnt(13)
buffer_store_dword v13,v5,s[36:39],s63 offen offset:0 sc0 sc1
s_mov_b32 s63,0x7fff0000
s_lshr_b32 s78,s45,1
s_and_b32 s79,s45,1
s_lshl_b32 s79,s79,7
s_lshl_b32 s57,s78,15
s_add_u32 s57,s57,s79
s_add_u32 s57,s57,12582912
s_add_u32 s58,s57,4096
s_lshl_b32 s59,s78,9
s_add_u32 s59,s59,s79
s_add_u32 s59,s59,107020288
s_lshl_b32 s60,s45,9
s_add_u32 s60,s60,s66
s_add_u32 s60,s60,131072
s_add_u32 s45,s45,s46
s_lshr_b32 s78,s45,1
s_and_b32 s79,s45,1
s_lshl_b32 s79,s79,7
s_lshl_b32 s55,s78,15
s_add_u32 s55,s55,s79
s_mul_i32 s56,s45,16384
s_add_u32 s56,s56,98566144
s_lshl_b32 s61,s78,11
s_add_u32 s61,s61,110587904
s_add_u32 s78,s45,s46
s_lshr_b32 s78,s78,1
s_lshl_b32 s62,s78,11
s_add_u32 s62,s62,110587904
s_waitcnt vmcnt(1)
s_branch Lr_it0_%=
Lr_done_%=:
s_waitcnt vmcnt(0) lgkmcnt(0)
s_barrier
Lcode_r_e:
.purgem M
.purgem R
.purgem T
.purgem D
.purgem L
)"
  :: "s"(ws), "s"(sWave), "s"(sG0), "s"(sStep), "s"(sOutOff), "s"(sAuxOff), "s"(sBuf), "s"(sRk), "s"(sExp), "v"(vRP), "v"(vRF), "v"(vRwr), "v"(vRrd), "v"(vRS), "v"(vFlag), "v"(vL4), "v"(vLY), "v"(vQ), "v"(vY0), "v"(vTr), "v"(vSa), "v"(vSb), "s"(q) : "v0", "v1", "v2", "v3", "v4", "v5", "v6", "v7", "v8", "v9", "v10", "v11", "v12", "v13", "v14", "v15", "v16", "v17", "v18", "v19", "v20", "v21", "v22", "v23", "v24", "v25", "v26", "v27", "v28", "v29", "v30", "v31", "v32", "v33", "v34", "v35", "v36", "v37", "v38", "v39", "v40", "v41", "v42", "v43", "v44", "v45", "v46", "v47", "v48", "v49", "v50", "v51", "v52", "v53", "v54", "v55", "v56", "v57", "v58", "v59", "v60", "v61", "v62", "v63", "v64", "v65", "v66", "v67", "v68", "v69", "v70", "v71", "v72", "v73", "v74", "v75", "v76", "v77", "v78", "v79", "v80", "v81", "v82", "v83", "v84", "v85", "v86", "v87", "v88", "v89", "v90", "v91", "v92", "v93", "v94", "v95", "v96", "v97", "v98", "v99", "v100", "v101", "v102", "v103", "v104", "v105", "v106", "v107", "v108", "v109", "v110", "v111", "v112", "v113", "v114", "v115", "v116", "v117", "v118", "v119", "v120", "v121", "v122", "v123", "v124", "v125", "v126", "v127", "v128", "v129", "v130", "v131", "v132", "v133", "v134", "v135", "v136", "v137", "v138", "v139", "v140", "v141", "v142", "v143", "v144", "v145", "v146", "v147", "v148", "v149", "v150", "v151", "v152", "v153", "v154", "v155", "v156", "v157", "v158", "v159", "v160", "v161", "v162", "v163", "v164", "v165", "v166", "v167", "v168", "v169", "v170", "v171", "v172", "v173", "v174", "v175", "v176", "v177", "v178", "v179", "v180", "v181", "v182", "v183", "v184", "v185", "v186", "v187", "v188", "v189", "v190", "v191", "v192", "v193", "v194", "v195", "v196", "v197", "v198", "v199", "v200", "v201", "v202", "v203", "v204", "v205", "v206", "v207", "v208", "v209", "v210", "v211", "v212", "v213", "v214", "v215", "v216", "v217", "v218", "v219", "v220", "v221", "v222", "v223", "v224", "v225", "v226", "v227", "v228", "v229", "v230", "v231", "s36", "s37", "s38", "s39", "s40", "s41", "s42", "s43", "s44", "s45", "s46", "s47", "s48", "s49", "s50", "s51", "s52", "s53", "s54", "s55", "s56", "s57", "s58", "s59", "s60", "s61", "s62", "s63", "s64", "s65", "s66", "s67", "s68", "s69", "s70", "s71", "s72", "s73", "s74", "s75", "s76", "s77", "s78", "s79", "s80", "s81", "s82", "s83", "s84", "s85", "s86", "s87", "s88", "s89", "s90", "s91", "s92", "s93", "s94", "s95", "s96", "s97", "vcc", "scc", "memory");
  }
  CTS(9);
#define COMMON(NF, FOFF, FSTR, SNAP, LDW, NL, TOFF)                                                                                            \
  LANE_ID(ln);                                                                                                                       \
  const int l15 = ln & 15, lg = ln >> 4, lq = l15 >> 2, lp = ln & 3, rr = 8 * lg + lq, fj = ln & 31;                                 \
  const unsigned vL16 = ln * 16, vFlag = (ln < 32 && wave == 0) ? ln * 4 : OOB;                                                      \
  const unsigned vSnapOff = (wave == 0 && fj < (NF)) ? (FOFF) + (ln >> 5) * sStep * (FSTR) : OOB;                                    \
  const unsigned vSnapRd = (SNAP) + (fj < (NF) ? ln : ln & 32) * 4;                                                                  \
  const unsigned vWd0 = (ln >> 3) * (LDW) + ((ln & 7) ^ swzB(ln >> 3)) * 16, vWd1 = ((ln >> 3) + 8) * (LDW) + ((ln & 7) ^ swzB(8 + (ln >> 3))) * 16; \
  const unsigned vWr0 = (wave >> 1) * 8192 + rr * 128 + (((wave & 1) * 4 + lp) ^ swzB(rr)) * 16, vWr1 = vWr0 + 8;                    \
  const unsigned vWr2 = 16384 + rr * 128 + ((2 * wave + (lp >> 1)) ^ swzB(rr)) * 16 + (lp & 1) * 8;                                  \
  const unsigned sWave = wave, sRk = kind == 4 ? U0 + rep : t_start, sG0 = rep < 5 ? rep : U0 + rep, sDbg = sDbg0 + (rep < 5 ? 0 : 2048)
  if (kind == 3 || kind == 5) {
    const unsigned sStep = kind == 3 ? 5 : G3OR;
    COMMON(8, fj * 128, 1024, 131072, 7168, 256, 0);
    const unsigned vOutS = (lg & 1) ? OOB : (((wave & 1) * 2 + (lg >> 1)) * 16 + l15) * 16 + (4 + (wave >> 1) - wave) * 1024;
    const unsigned vAux0 = (ln & 7) * 128 + (4 * wave + lg) * 8 + ((ln >> 3) & 1) * 4, vAux1 = 139264 + (l15 == 0 ? (4 * wave + lg) * 16 : 512 + ln * 16);
    const unsigned vAux2 = 139264 + l15 * 16, vAux3 = 0, vAn = 0, vAe = 0;
    const unsigned sOutOff = OFF_G + (slice * 6 + wave) * 1024, sFoutOff = OFF_FG + slice * 128, sAuxOff = 0, sWw = 16 * wave * 7168 + slice * 384, sBuf = 0;
    const unsigned sExp = __builtin_amdgcn_readfirstlane(ep);
    if (kind == 3) { asm volatile(
R"(.macro M d,w,x
v_mfma_f32_16x16x32_bf16 v[\d:\d+3], \w, v[\x:\x+3], v[\d:\d+3]
.endm
.macro R d,a,o
ds_read_b128 v[\d:\d+3], v\a offset:\o
.endm
.macro T d,a,o
ds_read_b64_tr_b16 \d, v\a offset:\o
.endm
.macro D a,s,o
buffer_load_dwordx4 v\a, s[36:39], s\s offen offset:\o sc1 lds
.endm
.macro L c
s_waitcnt lgkmcnt(\c)
.endm
Lcode_g3_b:
s_mov_b64 s[36:37],%0
s_mov_b32 s38,111243392
s_mov_b32 s39,0x00020000
s_mov_b32 s44,%1
s_mov_b32 s45,%2
s_mov_b32 s46,%3
s_mov_b32 s47,%4
s_mov_b32 s48,%5
s_mov_b32 s49,%6
s_mov_b32 s50,%7
s_mov_b32 s51,%8
s_mov_b32 s93,%9
s_mov_b32 s53,%10
v_mov_b32 v0,%11
v_mov_b32 v3,%12
v_mov_b32 v5,%13
v_mov_b32 v6,%14
v_mov_b32 v18,%15
v_mov_b32 v19,%16
v_mov_b32 v20,%17
v_mov_b32 v21,%18
v_mov_b32 v22,%19
v_mov_b32 v10,%20
v_mov_b32 v11,%21
v_mov_b32 v12,%22
v_mov_b32 v13,%23
v_mov_b32 v14,%24
v_mov_b32 v15,%25
v_mov_b32 v16,%26
s_mov_b64 s[40:41],%27
s_mov_b32 s42,7340032
s_mov_b32 s43,0x00020000
s_lshl_b32 s57,s44,11
s_add_u32 s55,s50,0
s_add_u32 m0,s57,0
s_nop 0
buffer_load_dwordx4 v18,s[40:43],s55 offen offset:0  lds
s_add_u32 m0,s57,1024
s_nop 0
buffer_load_dwordx4 v19,s[40:43],s55 offen offset:0  lds
s_add_u32 m0,s57,8064
s_nop 0
buffer_load_dwordx4 v18,s[40:43],s55 offen offset:128  lds
s_add_u32 m0,s57,9088
s_nop 0
buffer_load_dwordx4 v19,s[40:43],s55 offen offset:128  lds
s_add_u32 m0,s57,16128
s_nop 0
buffer_load_dwordx4 v18,s[40:43],s55 offen offset:256  lds
s_add_u32 m0,s57,17152
s_nop 0
buffer_load_dwordx4 v19,s[40:43],s55 offen offset:256  lds
s_add_u32 s55,s50,458752
s_add_u32 m0,s57,24576
s_nop 0
buffer_load_dwordx4 v18,s[40:43],s55 offen offset:0  lds
s_add_u32 m0,s57,25600
s_nop 0
buffer_load_dwordx4 v19,s[40:43],s55 offen offset:0  lds
s_add_u32 m0,s57,32640
s_nop 0
buffer_load_dwordx4 v18,s[40:43],s55 offen offset:128  lds
s_add_u32 m0,s57,33664
s_nop 0
buffer_load_dwordx4 v19,s[40:43],s55 offen offset:128  lds
s_add_u32 m0,s57,40704
s_nop 0
buffer_load_dwordx4 v18,s[40:43],s55 offen offset:256  lds
s_add_u32 m0,s57,41728
s_nop 0
buffer_load_dwordx4 v19,s[40:43],s55 offen offset:256  lds
s_add_u32 s55,s50,917504
s_add_u32 m0,s57,49152
s_nop 0
buffer_load_dwordx4 v18,s[40:43],s55 offen offset:0  lds
s_add_u32 m0,s57,50176
s_nop 0
buffer_load_dwordx4 v19,s[40:43],s55 offen offset:0  lds
s_add_u32 m0,s57,57216
s_nop 0
buffer_load_dwordx4 v18,s[40:43],s55 offen offset:128  lds
s_add_u32 m0,s57,58240
s_nop 0
buffer_load_dwordx4 v19,s[40:43],s55 offen offset:128  lds
s_add_u32 m0,s57,65280
s_nop 0
buffer_load_dwordx4 v18,s[40:43],s55 offen offset:256  lds
s_add_u32 m0,s57,66304
s_nop 0
buffer_load_dwordx4 v19,s[40:43],s55 offen offset:256  lds
s_add_u32 s55,s50,1376256
s_add_u32 m0,s57,73728
s_nop 0
buffer_load_dwordx4 v18,s[40:43],s55 offen offset:0  lds
s_add_u32 m0,s57,74752
s_nop 0
buffer_load_dwordx4 v19,s[40:43],s55 offen offset:0  lds
s_add_u32 m0,s57,81792
s_nop 0
buffer_load_dwordx4 v18,s[40:43],s55 offen offset:128  lds
s_add_u32 m0,s57,82816
s_nop 0
buffer_load_dwordx4 v19,s[40:43],s55 offen offset:128  lds
s_add_u32 m0,s57,89856
s_nop 0
buffer_load_dwordx4 v18,s[40:43],s55 offen offset:256  lds
s_add_u32 m0,s57,90880
s_nop 0
buffer_load_dwordx4 v19,s[40:43],s55 offen offset:256  lds
s_add_u32 s55,s50,1835008
s_add_u32 m0,s57,98304
s_nop 0
buffer_load_dwordx4 v18,s[40:43],s55 offen offset:0  lds
s_add_u32 m0,s57,99328
s_nop 0
buffer_load_dwordx4 v19,s[40:43],s55 offen offset:0  lds
s_add_u32 m0,s57,106368
s_nop 0
buffer_load_dwordx4 v18,s[40:43],s55 offen offset:128  lds
s_add_u32 m0,s57,107392
s_nop 0
buffer_load_dwordx4 v19,s[40:43],s55 offen offset:128  lds
s_add_u32 m0,s57,114432
s_nop 0
buffer_load_dwordx4 v18,s[40:43],s55 offen offset:256  lds
s_add_u32 m0,s57,115456
s_nop 0
buffer_load_dwordx4 v19,s[40:43],s55 offen offset:256  lds
s_waitcnt vmcnt(24) lgkmcnt(0)
s_barrier
s_add_u32 s55,s50,2293760
s_add_u32 m0,s57,122880
s_nop 0
buffer_load_dwordx4 v18,s[40:43],s55 offen offset:0  lds
s_add_u32 m0,s57,123904
s_nop 0
buffer_load_dwordx4 v19,s[40:43],s55 offen offset:0  lds
s_add_u32 m0,s57,130944
s_nop 0
buffer_load_dwordx4 v18,s[40:43],s55 offen offset:128  lds
s_add_u32 m0,s57,131968
s_nop 0
buffer_load_dwordx4 v19,s[40:43],s55 offen offset:128  lds
s_add_u32 m0,s57,139008
s_nop 0
buffer_load_dwordx4 v18,s[40:43],s55 offen offset:256  lds
s_add_u32 m0,s57,140032
s_nop 0
buffer_load_dwordx4 v19,s[40:43],s55 offen offset:256  lds
v_add_u32 v26,0,v20
v_add_u32 v27,0,v21
v_add_u32 v28,0,v22
T a[0:1],26,0
T a[2:3],26,512
T a[4:5],27,0
T a[6:7],27,512
T a[8:9],28,0
T a[10:11],28,512
T a[12:13],26,4096
T a[14:15],26,4608
T a[16:17],27,4096
T a[18:19],27,4608
T a[20:21],28,4096
T a[22:23],28,4608
s_waitcnt vmcnt(24) lgkmcnt(0)
s_barrier
s_add_u32 s55,s50,2752512
s_add_u32 m0,s57,0
s_nop 0
buffer_load_dwordx4 v18,s[40:43],s55 offen offset:0  lds
s_add_u32 m0,s57,1024
s_nop 0
buffer_load_dwordx4 v19,s[40:43],s55 offen offset:0  lds
s_add_u32 m0,s57,8064
s_nop 0
buffer_load_dwordx4 v18,s[40:43],s55 offen offset:128  lds
s_add_u32 m0,s57,9088
s_nop 0
buffer_load_dwordx4 v19,s[40:43],s55 offen offset:128  lds
s_add_u32 m0,s57,16128
s_nop 0
buffer_load_dwordx4 v18,s[40:43],s55 offen offset:256  lds
s_add_u32 m0,s57,17152
s_nop 0
buffer_load_dwordx4 v19,s[40:43],s55 offen offset:256  lds
v_add_u32 v26,24576,v20
v_add_u32 v27,24576,v21
v_add_u32 v28,24576,v22
T a[24:25],26,0
T a[26:27],26,512
T a[28:29],27,0
T a[30:31],27,512
T a[32:33],28,0
T a[34:35],28,512
T a[36:37],26,4096
T a[38:39],26,4608
T a[40:41],27,4096
T a[42:43],27,4608
T a[44:45],28,4096
T a[46:47],28,4608
s_waitcnt vmcnt(24) lgkmcnt(0)
s_barrier
s_add_u32 s55,s50,3211264
s_add_u32 m0,s57,24576
s_nop 0
buffer_load_dwordx4 v18,s[40:43],s55 offen offset:0  lds
s_add_u32 m0,s57,25600
s_nop 0
buffer_load_dwordx4 v19,s[40:43],s55 offen offset:0  lds
s_add_u32 m0,s57,32640
s_nop 0
buffer_load_dwordx4 v18,s[40:43],s55 offen offset:128  lds
s_add_u32 m0,s57,33664
s_nop 0
buffer_load_dwordx4 v19,s[40:43],s55 offen offset:128  lds
s_add_u32 m0,s57,40704
s_nop 0
buffer_load_dwordx4 v18,s[40:43],s55 offen offset:256  lds
s_add_u32 m0,s57,41728
s_nop 0
buffer_load_dwordx4 v19,s[40:43],s55 offen offset:256  lds
v_add_u32 v26,49152,v20
v_add_u32 v27,49152,v21
v_add_u32 v28,49152,v22
T a[48:49],26,0
T a[50:51],26,512
T a[52:53],27,0
T a[54:55],27,512
T a[56:57],28,0
T a[58:59],28,512
T a[60:61],26,4096
T a[62:63],26,4608
T a[64:65],27,4096
T a[66:67],27,4608
T a[68:69],28,4096
T a[70:71],28,4608
s_waitcnt vmcnt(24) lgkmcnt(0)
s_barrier
s_add_u32 s55,s50,3670016
s_add_u32 m0,s57,49152
s_nop 0
buffer_load_dwordx4 v18,s[40:43],s55 offen offset:0  lds
s_add_u32 m0,s57,50176
s_nop 0
buffer_load_dwordx4 v19,s[40:43],s55 offen offset:0  lds
s_add_u32 m0,s57,57216
s_nop 0
buffer_load_dwordx4 v18,s[40:43],s55 offen offset:128  lds
s_add_u32 m0,s57,58240
s_nop 0
buffer_load_dwordx4 v19,s[40:43],s55 offen offset:128  lds
s_add_u32 m0,s57,65280
s_nop 0
buffer_load_dwordx4 v18,s[40:43],s55 offen offset:256  lds
s_add_u32 m0,s57,66304
s_nop 0
buffer_load_dwordx4 v19,s[40:43],s55 offen offset:256  lds
v_add_u32 v26,73728,v20
v_add_u32 v27,73728,v21
v_add_u32 v28,73728,v22
T a[72:73],26,0
T a[74:75],26,512
T a[76:77],27,0
T a[78:79],27,512
T a[80:81],28,0
T a[82:83],28,512
T a[84:85],26,4096
T a[86:87],26,4608
T a[88:89],27,4096
T a[90:91],27,4608
T a[92:93],28,4096
T a[94:95],28,4608
s_waitcnt vmcnt(24) lgkmcnt(0)
s_barrier
s_add_u32 s55,s50,4128768
s_add_u32 m0,s57,73728
s_nop 0
buffer_load_dwordx4 v18,s[40:43],s55 offen offset:0  lds
s_add_u32 m0,s57,74752
s_nop 0
buffer_load_dwordx4 v19,s[40:43],s55 offen offset:0  lds
s_add_u32 m0,s57,81792
s_nop 0
buffer_load_dwordx4 v18,s[40:43],s55 offen offset:128  lds
s_add_u32 m0,s57,82816
s_nop 0
buffer_load_dwordx4 v19,s[40:43],s55 offen offset:128  lds
s_add_u32 m0,s57,89856
s_nop 0
buffer_load_dwordx4 v18,s[40:43],s55 offen offset:256  lds
s_add_u32 m0,s57,90880
s_nop 0
buffer_load_dwordx4 v19,s[40:43],s55 offen offset:256  lds
v_add_u32 v26,98304,v20
v_add_u32 v27,98304,v21
v_add_u32 v28,98304,v22
T a[96:97],26,0
T a[98:99],26,512
T a[100:101],27,0
T a[102:103],27,512
T a[104:105],28,0
T a[106:107],28,512
T a[108:109],26,4096
T a[110:111],26,4608
T a[112:113],27,4096
T a[114:115],27,4608
T a[116:117],28,4096
T a[118:119],28,4608
s_waitcnt vmcnt(24) lgkmcnt(0)
s_barrier
s_add_u32 s55,s50,4587520
s_add_u32 m0,s57,98304
s_nop 0
buffer_load_dwordx4 v18,s[40:43],s55 offen offset:0  lds
s_add_u32 m0,s57,99328
s_nop 0
buffer_load_dwordx4 v19,s[40:43],s55 offen offset:0  lds
s_add_u32 m0,s57,106368
s_nop 0
buffer_load_dwordx4 v18,s[40:43],s55 offen offset:128  lds
s_add_u32 m0,s57,107392
s_nop 0
buffer_load_dwordx4 v19,s[40:43],s55 offen offset:128  lds
s_add_u32 m0,s57,114432
s_nop 0
buffer_load_dwordx4 v18,s[40:43],s55 offen offset:256  lds
s_add_u32 m0,s57,115456
s_nop 0
buffer_load_dwordx4 v19,s[40:43],s55 offen offset:256  lds
v_add_u32 v26,122880,v20
v_add_u32 v27,122880,v21
v_add_u32 v28,122880,v22
T a[120:121],26,0
T a[122:123],26,512
T a[124:125],27,0
T a[126:127],27,512
T a[128:129],28,0
T a[130:131],28,512
T a[132:133],26,4096
T a[134:135],26,4608
T a[136:137],27,4096
T a[138:139],27,4608
T a[140:141],28,4096
T a[142:143],28,4608
s_waitcnt vmcnt(24) lgkmcnt(0)
s_barrier
s_add_u32 s55,s50,5046272
s_add_u32 m0,s57,122880
s_nop 0
buffer_load_dwordx4 v18,s[40:43],s55 offen offset:0  lds
s_add_u32 m0,s57,123904
s_nop 0
buffer_load_dwordx4 v19,s[40:43],s55 offen offset:0  lds
s_add_u32 m0,s57,130944
s_nop 0
buffer_load_dwordx4 v18,s[40:43],s55 offen offset:128  lds
s_add_u32 m0,s57,131968
s_nop 0
buffer_load_dwordx4 v19,s[40:43],s55 offen offset:128  lds
s_add_u32 m0,s57,139008
s_nop 0
buffer_load_dwordx4 v18,s[40:43],s55 offen offset:256  lds
s_add_u32 m0,s57,140032
s_nop 0
buffer_load_dwordx4 v19,s[40:43],s55 offen offset:256  lds
v_add_u32 v26,0,v20
v_add_u32 v27,0,v21
v_add_u32 v28,0,v22
T a[144:145],26,0
T a[146:147],26,512
T a[148:149],27,0
T a[150:151],27,512
T a[152:153],28,0
T a[154:155],28,512
T a[156:157],26,4096
T a[158:159],26,4608
T a[160:161],27,4096
T a[162:163],27,4608
T a[164:165],28,4096
T a[166:167],28,4608
s_waitcnt vmcnt(24) lgkmcnt(0)
s_barrier
s_add_u32 s55,s50,5505024
s_add_u32 m0,s57,0
s_nop 0
buffer_load_dwordx4 v18,s[40:43],s55 offen offset:0  lds
s_add_u32 m0,s57,1024
s_nop 0
buffer_load_dwordx4 v19,s[40:43],s55 offen offset:0  lds
s_add_u32 m0,s57,8064
s_nop 0
buffer_load_dwordx4 v18,s[40:43],s55 offen offset:128  lds
s_add_u32 m0,s57,9088
s_nop 0
buffer_load_dwordx4 v19,s[40:43],s55 offen offset:128  lds
s_add_u32 m0,s57,16128
s_nop 0
buffer_load_dwordx4 v18,s[40:43],s55 offen offset:256  lds
s_add_u32 m0,s57,17152
s_nop 0
buffer_load_dwordx4 v19,s[40:43],s55 offen offset:256  lds
v_add_u32 v26,24576,v20
v_add_u32 v27,24576,v21
v_add_u32 v28,24576,v22
T a[168:169],26,0
T a[170:171],26,512
T a[172:173],27,0
T a[174:175],27,512
T a[176:177],28,0
T a[178:179],28,512
T a[180:181],26,4096
T a[182:183],26,4608
T a[184:185],27,4096
T a[186:187],27,4608
T a[188:189],28,4096
T a[190:191],28,4608
s_waitcnt vmcnt(24) lgkmcnt(0)
s_barrier
s_add_u32 s55,s50,5963776
s_add_u32 m0,s57,24576
s_nop 0
buffer_load_dwordx4 v18,s[40:43],s55 offen offset:0  lds
s_add_u32 m0,s57,25600
s_nop 0
buffer_load_dwordx4 v19,s[40:43],s55 offen offset:0  lds
s_add_u32 m0,s57,32640
s_nop 0
buffer_load_dwordx4 v18,s[40:43],s55 offen offset:128  lds
s_add_u32 m0,s57,33664
s_nop 0
buffer_load_dwordx4 v19,s[40:43],s55 offen offset:128  lds
s_add_u32 m0,s57,40704
s_nop 0
buffer_load_dwordx4 v18,s[40:43],s55 offen offset:256  lds
s_add_u32 m0,s57,41728
s_nop 0
buffer_load_dwordx4 v19,s[40:43],s55 offen offset:256  lds
v_add_u32 v26,49152,v20
v_add_u32 v27,49152,v21
v_add_u32 v28,49152,v22
T a[192:193],26,0
T a[194:195],26,512
T a[196:197],27,0
T a[198:199],27,512
T a[200:201],28,0
T a[202:203],28,512
T a[204:205],26,4096
T a[206:207],26,4608
T a[208:209],27,4096
T a[210:211],27,4608
T a[212:213],28,4096
T a[214:215],28,4608
s_waitcnt vmcnt(24) lgkmcnt(0)
s_barrier
s_add_u32 s55,s50,6422528
s_add_u32 m0,s57,49152
s_nop 0
buffer_load_dwordx4 v18,s[40:43],s55 offen offset:0  lds
s_add_u32 m0,s57,50176
s_nop 0
buffer_load_dwordx4 v19,s[40:43],s55 offen offset:0  lds
s_add_u32 m0,s57,57216
s_nop 0
buffer_load_dwordx4 v18,s[40:43],s55 offen offset:128  lds
s_add_u32 m0,s57,58240
s_nop 0
buffer_load_dwordx4 v19,s[40:43],s55 offen offset:128  lds
s_add_u32 m0,s57,65280
s_nop 0
buffer_load_dwordx4 v18,s[40:43],s55 offen offset:256  lds
s_add_u32 m0,s57,66304
s_nop 0
buffer_load_dwordx4 v19,s[40:43],s55 offen offset:256  lds
v_add_u32 v26,73728,v20
v_add_u32 v27,73728,v21
v_add_u32 v28,73728,v22
T a[216:217],26,0
T a[218:219],26,512
T a[220:221],27,0
T a[222:223],27,512
T a[224:225],28,0
T a[226:227],28,512
T a[228:229],26,4096
T a[230:231],26,4608
)"
R"(T a[232:233],27,4096
T a[234:235],27,4608
T a[236:237],28,4096
T a[238:239],28,4608
s_waitcnt vmcnt(24) lgkmcnt(0)
s_barrier
s_add_u32 s55,s50,6881280
s_add_u32 m0,s57,73728
s_nop 0
buffer_load_dwordx4 v18,s[40:43],s55 offen offset:0  lds
s_add_u32 m0,s57,74752
s_nop 0
buffer_load_dwordx4 v19,s[40:43],s55 offen offset:0  lds
s_add_u32 m0,s57,81792
s_nop 0
buffer_load_dwordx4 v18,s[40:43],s55 offen offset:128  lds
s_add_u32 m0,s57,82816
s_nop 0
buffer_load_dwordx4 v19,s[40:43],s55 offen offset:128  lds
s_add_u32 m0,s57,89856
s_nop 0
buffer_load_dwordx4 v18,s[40:43],s55 offen offset:256  lds
s_add_u32 m0,s57,90880
s_nop 0
buffer_load_dwordx4 v19,s[40:43],s55 offen offset:256  lds
v_add_u32 v26,98304,v20
v_add_u32 v27,98304,v21
v_add_u32 v28,98304,v22
T a[240:241],26,0
T a[242:243],26,512
T a[244:245],27,0
T a[246:247],27,512
T a[248:249],28,0
T a[250:251],28,512
T a[252:253],26,4096
T a[254:255],26,4608
T v[128:129],27,4096
T v[130:131],27,4608
T v[132:133],28,4096
T v[134:135],28,4608
s_waitcnt vmcnt(24) lgkmcnt(0)
s_barrier
v_add_u32 v26,122880,v20
v_add_u32 v27,122880,v21
v_add_u32 v28,122880,v22
T v[136:137],26,0
T v[138:139],26,512
T v[140:141],27,0
T v[142:143],27,512
T v[144:145],28,0
T v[146:147],28,512
T v[148:149],26,4096
T v[150:151],26,4608
T v[152:153],27,4096
T v[154:155],27,4608
T v[156:157],28,4096
T v[158:159],28,4608
s_waitcnt vmcnt(18) lgkmcnt(0)
s_barrier
v_add_u32 v26,0,v20
v_add_u32 v27,0,v21
v_add_u32 v28,0,v22
T v[160:161],26,0
T v[162:163],26,512
T v[164:165],27,0
T v[166:167],27,512
T v[168:169],28,0
T v[170:171],28,512
T v[172:173],26,4096
T v[174:175],26,4608
T v[176:177],27,4096
T v[178:179],27,4608
T v[180:181],28,4096
T v[182:183],28,4608
s_waitcnt vmcnt(12) lgkmcnt(0)
s_barrier
v_add_u32 v26,24576,v20
v_add_u32 v27,24576,v21
v_add_u32 v28,24576,v22
T v[184:185],26,0
T v[186:187],26,512
T v[188:189],27,0
T v[190:191],27,512
T v[192:193],28,0
T v[194:195],28,512
T v[196:197],26,4096
T v[198:199],26,4608
T v[200:201],27,4096
T v[202:203],27,4608
T v[204:205],28,4096
T v[206:207],28,4608
s_waitcnt vmcnt(6) lgkmcnt(0)
s_barrier
v_add_u32 v26,49152,v20
v_add_u32 v27,49152,v21
v_add_u32 v28,49152,v22
T v[208:209],26,0
T v[210:211],26,512
T v[212:213],27,0
T v[214:215],27,512
T v[216:217],28,0
T v[218:219],28,512
T v[220:221],26,4096
T v[222:223],26,4608
T v[224:225],27,4096
T v[226:227],27,4608
T v[228:229],28,4096
T v[230:231],28,4608
s_waitcnt vmcnt(0) lgkmcnt(0)
s_barrier
v_add_u32 v26,73728,v20
v_add_u32 v27,73728,v21
v_add_u32 v28,73728,v22
T v[232:233],26,0
T v[234:235],26,512
T v[236:237],27,0
T v[238:239],27,512
T v[240:241],28,0
T v[242:243],28,512
T v[244:245],26,4096
T v[246:247],26,4608
T v[248:249],27,4096
T v[250:251],27,4608
T v[252:253],28,4096
T v[254:255],28,4608
L 0
s_barrier
v_mov_b32 v42,0x3f803f80
v_mov_b32 v43,0x3f803f80
v_mov_b32 v44,0x3f803f80
v_mov_b32 v45,0x3f803f80
s_nop 1
v_mfma_f32_16x16x32_bf16 v[82:85],a[0:3],v[42:45],0
v_mfma_f32_16x16x32_bf16 v[86:89],a[4:7],v[42:45],0
v_mfma_f32_16x16x32_bf16 v[90:93],a[8:11],v[42:45],0
M 82,a[12:15],42
M 86,a[16:19],42
M 90,a[20:23],42
M 82,a[24:27],42
M 86,a[28:31],42
M 90,a[32:35],42
M 82,a[36:39],42
M 86,a[40:43],42
M 90,a[44:47],42
M 82,a[48:51],42
M 86,a[52:55],42
M 90,a[56:59],42
M 82,a[60:63],42
M 86,a[64:67],42
M 90,a[68:71],42
M 82,a[72:75],42
M 86,a[76:79],42
M 90,a[80:83],42
M 82,a[84:87],42
M 86,a[88:91],42
M 90,a[92:95],42
M 82,a[96:99],42
M 86,a[100:103],42
M 90,a[104:107],42
M 82,a[108:111],42
M 86,a[112:115],42
M 90,a[116:119],42
M 82,a[120:123],42
M 86,a[124:127],42
M 90,a[128:131],42
M 82,a[132:135],42
M 86,a[136:139],42
M 90,a[140:143],42
M 82,a[144:147],42
M 86,a[148:151],42
M 90,a[152:155],42
M 82,a[156:159],42
M 86,a[160:163],42
M 90,a[164:167],42
M 82,a[168:171],42
M 86,a[172:175],42
M 90,a[176:179],42
M 82,a[180:183],42
M 86,a[184:187],42
M 90,a[188:191],42
M 82,a[192:195],42
M 86,a[196:199],42
M 90,a[200:203],42
M 82,a[204:207],42
M 86,a[208:211],42
M 90,a[212:215],42
M 82,a[216:219],42
M 86,a[220:223],42
M 90,a[224:227],42
M 82,a[228:231],42
M 86,a[232:235],42
M 90,a[236:239],42
M 82,a[240:243],42
M 86,a[244:247],42
M 90,a[248:251],42
M 82,a[252:255],42
M 86,v[128:131],42
M 90,v[132:135],42
M 82,v[136:139],42
M 86,v[140:143],42
M 90,v[144:147],42
M 82,v[148:151],42
M 86,v[152:155],42
M 90,v[156:159],42
M 82,v[160:163],42
M 86,v[164:167],42
M 90,v[168:171],42
M 82,v[172:175],42
M 86,v[176:179],42
M 90,v[180:183],42
M 82,v[184:187],42
M 86,v[188:191],42
M 90,v[192:195],42
M 82,v[196:199],42
M 86,v[200:203],42
M 90,v[204:207],42
M 82,v[208:211],42
M 86,v[212:215],42
M 90,v[216:219],42
M 82,v[220:223],42
M 86,v[224:227],42
M 90,v[228:231],42
M 82,v[232:235],42
M 86,v[236:239],42
M 90,v[240:243],42
M 82,v[244:247],42
M 86,v[248:251],42
M 90,v[252:255],42
v_mov_b32 v4,s53
s_mov_b32 s82,0
s_mov_b32 s80,0
s_mov_b32 s54,s45
s_mul_i32 s57,s44,4096
s_mul_i32 s55,s45,32768
s_add_u32 s55,s55,s57
s_add_u32 s55,s55,31457280
s_mul_i32 s57,s44,4096
s_mul_i32 s60,s46,32768
s_mul_i32 s61,s46,1024
s_mul_i32 s62,s46,114688
s_mul_i32 s63,s46,2560
s_mul_i32 s64,s46,1024
s_lshl_b32 s59,s44,8
s_add_u32 s59,s59,131072
s_mul_i32 s58,s45,1024
s_add_u32 s58,s58,109613056
s_mul_i32 s65,s45,114688
s_add_u32 s65,s65,s47
s_mul_i32 s68,s45,2560
s_add_u32 s68,s68,s48
s_mul_i32 s73,s45,1024
s_add_u32 s73,s73,107479040
s_mul_i32 s74,s45,32768
s_add_u32 s74,s74,s49
s_mul_i32 s75,s45,1024
s_add_u32 s75,s75,107479040
s_mov_b32 s66,0x7fff0000
s_mov_b32 s67,0x7fff0000
s_mov_b32 s69,0x7fff0000
s_mov_b32 s76,0x7fff0000
s_mov_b32 s77,0x7fff0000
s_mov_b32 s70,0x7fff0000
s_mov_b32 s71,0x7fff0000
s_mov_b32 s72,0x7fff0000
v_mov_b32 v1,v0
v_add_u32 v2,16384,v0
s_mov_b32 s84,16384
s_mov_b32 s86,0x3a800000
s_mov_b32 s87,0x3727c5ac
s_mov_b32 s88,0xc0135761
s_mov_b32 s89,0x3d372713
s_lshr_b32 s85,s44,1
s_mul_i32 s85,s85,127
v_lshlrev_b32 v8,22,v0
v_and_b32 v8,0x80000000,v8
v_or_b32 v8,v8,v5
v_add_u32 v2,32768,v0
s_lshl_b32 s84,s57,1
s_add_u32 s84,s84,32768
s_getpc_b64 s[40:41]
Lg3_pc_%=:
s_lshl_b32 s78,s44,13
v_lshlrev_b32 v94,3,v0
v_add_u32 v94,s78,v94
v_min_u32 v94,Lcode_g3_e-Lg3_pc_%=-4,v94
s_mov_b32 m0,163584
s_nop 0
global_load_lds_dword v94,s[40:41]
s_add_u32 m0,s59,4096
s_nop 0
buffer_load_dword v8,s[36:39],s58 offen offset:0 sc1 lds
buffer_store_dword v4,v3,s[36:39],s71 offen offset:0 sc0 sc1
s_add_u32 m0,s59,5120
s_nop 0
buffer_load_dword v8,s[36:39],s58 offen offset:0 sc1 lds
buffer_store_dword v4,v3,s[36:39],s71 offen offset:0 sc0 sc1
s_mov_b32 s83,0
Lg3_poll_%=:
s_add_u32 s78,s83,2
s_and_b32 s78,s78,3
s_lshl_b32 s78,s78,10
s_add_u32 s78,s78,4096
s_add_u32 m0,s59,s78
s_nop 0
buffer_load_dword v8,s[36:39],s58 offen offset:0 sc1 lds
s_and_b32 s78,s83,3
s_lshl_b32 s78,s78,10
v_add_u32 v94,s78,v6
s_waitcnt vmcnt(4)
s_barrier
ds_read_b32 v7,v94 offset:4096
buffer_store_dword v4,v3,s[36:39],s70 offen offset:0 sc0 sc1
s_mov_b32 s70,s71
s_mov_b32 s71,s72
s_mov_b32 s72,0x7fff0000
s_add_u32 s83,s83,1
s_add_u32 s82,s82,1
L 0
v_cmp_eq_u32 vcc,s53,v7
s_nop 0
s_cmp_eq_u32 vcc_lo,-1
s_cbranch_scc1 Lg3_go_%=
s_cmp_lt_u32 s82,20000
s_cbranch_scc1 Lg3_poll_%=
Lg3_go_%=:
buffer_load_dword v114,v11,s[36:39],s73 offen offset:0 sc1
s_add_u32 s73,s73,s64
s_add_u32 m0,s57,0
s_add_u32 s56,s55,0
D 0,56,0
D 0,56,1024
D 0,56,2048
D 0,56,3072
s_add_u32 s58,s58,s61
s_add_u32 m0,s59,2048
s_nop 0
buffer_load_dword v8,s[36:39],s58 offen offset:0 sc1 lds
s_add_u32 m0,s57,16384
s_add_u32 s56,s55,16384
D 0,56,0
D 0,56,1024
D 0,56,2048
D 0,56,3072
s_add_u32 s55,s55,s60
s_sub_u32 s57,s84,s57
s_add_u32 s54,s54,s46
s_waitcnt vmcnt(4)
s_barrier
buffer_store_dword v4,v3,s[36:39],s70 offen offset:0 sc0 sc1
s_min_u32 s71,s71,s72
buffer_store_dword v4,v3,s[36:39],s71 offen offset:0 sc0 sc1
s_mov_b32 s70,0x7fff0000
s_mov_b32 s71,0x7fff0000
s_mov_b32 s72,0x7fff0000
s_cmp_eq_u32 s80,1
s_cbranch_scc1 Lg3_st1_%=
s_branch Lg3_st0_%=
Lg3_gox_%=:
s_add_u32 m0,s59,2048
s_nop 0
buffer_load_dword v8,s[36:39],s58 offen offset:0 sc1 lds
s_waitcnt vmcnt(0)
s_barrier
buffer_store_dword v4,v3,s[36:39],s70 offen offset:0 sc0 sc1
s_min_u32 s71,s71,s72
buffer_store_dword v4,v3,s[36:39],s71 offen offset:0 sc0 sc1
s_mov_b32 s70,0x7fff0000
s_mov_b32 s71,0x7fff0000
s_mov_b32 s72,0x7fff0000
s_cmp_eq_u32 s80,1
s_cbranch_scc1 Lg3_st1_%=
Lg3_st0_%=:
ds_read_b32 v7,v6 offset:2048
R 42,1,0
R 46,1,1024
R 50,1,2048
R 54,1,3072
R 58,1,4096
L 5
v_cmp_eq_u32 vcc,s53,v7
L 3
s_cmp_eq_u32 vcc_lo,-1
s_cbranch_scc1 Lg3_F0_%=
s_branch Lg3_L0_%=
Lg3_st1_%=:
ds_read_b32 v7,v6 offset:2048
R 42,2,0
R 46,2,1024
R 50,2,2048
R 54,2,3072
R 58,2,4096
L 5
v_cmp_eq_u32 vcc,s53,v7
L 3
s_cmp_eq_u32 vcc_lo,-1
s_cbranch_scc1 Lg3_F1_%=
s_branch Lg3_L1_%=
Lg3_F1_%=:
s_cmp_eq_u32 s44,1
s_cbranch_scc1 Lg3_F1w1_%=
s_cmp_eq_u32 s44,2
s_cbranch_scc1 Lg3_F1w2_%=
s_cmp_eq_u32 s44,3
s_cbranch_scc1 Lg3_F1w3_%=
s_branch Lg3_F1w0_%=
Lg3_F0_%=:
s_cmp_eq_u32 s44,1
s_cbranch_scc1 Lg3_F0w1_%=
s_cmp_eq_u32 s44,2
s_cbranch_scc1 Lg3_F0w2_%=
s_cmp_eq_u32 s44,3
s_cbranch_scc1 Lg3_F0w3_%=
Lg3_F0w0_%=:
v_mfma_f32_16x16x32_bf16 v[26:29],a[8:11],v[42:45],0
s_add_u32 m0,s57,0
R 62,1,5120
v_add_f32_dpp v115,v114,v114 quad_perm:[1,0,3,2] row_mask:0xf bank_mask:0xf
M 26,a[20:23],46
s_add_u32 s56,s55,0
buffer_load_dword v114,v11,s[36:39],s73 offen offset:0 sc1
v_mfma_f32_16x16x32_bf16 v[22:25],a[4:7],v[42:45],0
s_add_u32 s58,s58,s61
D 0,56,0
M 22,a[16:19],46
s_mov_b32 s66,s65
R 66,1,6144
v_add_f32_dpp v115,v115,v115 quad_perm:[2,3,0,1] row_mask:0xf bank_mask:0xf
v_mfma_f32_16x16x32_bf16 v[18:21],a[0:3],v[42:45],0
s_add_u32 s65,s65,s62
v_fma_f32 v30,-v78,v82,v30
v_fma_f32 v31,-v78,v83,v31
M 18,a[12:15],46
L 3
v_add_f32_dpp v118,v115,v115 row_half_mirror row_mask:0xf bank_mask:0xf
v_fma_f32 v32,-v78,v84,v32
M 18,a[24:27],50
s_add_u32 s54,s54,s46
R 70,1,7168
v_fma_f32 v33,-v78,v85,v33
v_mul_f32 v94,v30,v30
M 18,a[36:39],54
s_add_u32 s73,s73,s64
v_mov_b32_dpp v115,v118 row_ror:8 row_mask:0xf bank_mask:0xf
v_fma_f32 v115,-v118,v118,v115
M 22,a[28:31],50
v_max_f32 v115,s87,v115
v_mul_f32 v95,v31,v31
M 22,a[40:43],54
R 42,1,8192
v_mul_f32 v96,v32,v32
M 26,a[32:35],50
v_rsq_f32 v116,v115
M 26,a[44:47],54
L 3
D 0,56,1024
M 26,a[56:59],58
R 46,1,9216
v_mul_f32 v121,v115,v116
M 26,a[68:71],62
v_mul_f32 v119,s88,v116
v_mul_f32 v115,s89,v116
v_mul_f32 v120,v119,v116
M 22,a[52:55],58
v_mul_f32 v120,v120,v115
v_mul_f32 v97,v33,v33
M 22,a[64:67],62
R 50,1,10240
v_fma_f32 v94,v80,v94,v79
M 18,a[48:51],58
ds_write_b128 v12,v[118:121] offset:0
M 18,a[60:63],62
L 4
v_fma_f32 v95,v80,v95,v79
v_fma_f32 v96,v80,v96,v79
M 18,a[72:75],66
R 54,1,11264
v_fma_f32 v97,v80,v97,v79
M 18,a[84:87],70
v_mul_f32 v94,v30,v94
v_exp_f32 v94,v94
M 22,a[76:79],66
v_mul_f32 v95,v31,v95
v_mul_f32 v96,v32,v96
M 22,a[88:91],70
R 58,1,12288
v_mul_f32 v97,v33,v97
M 26,a[80:83],66
D 0,56,2048
M 26,a[92:95],70
L 4
v_exp_f32 v95,v95
M 26,a[104:107],42
R 62,1,13312
v_fma_f32 v94,v94,v81,v81
M 26,a[116:119],46
v_exp_f32 v96,v96
M 22,a[100:103],42
v_exp_f32 v97,v97
v_fma_f32 v95,v95,v81,v81
M 22,a[112:115],46
)"
R"(R 66,1,14336
v_fma_f32 v96,v96,v81,v81
M 18,a[96:99],42
v_rcp_f32 v94,v94
M 18,a[108:111],46
L 3
v_rcp_f32 v95,v95
M 18,a[120:123],50
R 70,1,15360
v_fma_f32 v97,v97,v81,v81
M 18,a[132:135],54
s_waitcnt vmcnt(6)
D 0,56,3072
M 22,a[124:127],50
s_barrier
v_rcp_f32 v96,v96
M 22,a[136:139],54
s_add_u32 m0,s59,0
R 42,1,16384
v_rcp_f32 v97,v97
M 26,a[128:131],50
s_add_u32 s56,s55,16384
v_mul_f32 v30,v30,v94
v_mul_f32 v31,v31,v95
M 26,a[140:143],54
L 3
v_mul_f32 v32,v32,v96
v_mul_f32 v33,v33,v97
M 26,a[152:155],58
s_add_u32 s55,s55,s60
R 46,1,17408
v_cvt_pk_bf16_f32 v106,v30,v31
M 26,a[164:167],62
v_cvt_pk_bf16_f32 v107,v32,v33
v_fma_f32 v34,-v78,v86,v34
M 22,a[148:151],58
v_fma_f32 v35,-v78,v87,v35
v_fma_f32 v36,-v78,v88,v36
M 22,a[160:163],62
R 50,1,18432
v_fma_f32 v37,-v78,v89,v37
v_mul_f32 v98,v34,v34
M 18,a[144:147],58
buffer_load_dword v8,s[36:39],s58 offen offset:0 sc1 lds
M 18,a[156:159],62
L 3
v_mul_f32 v99,v35,v35
v_mul_f32 v100,v36,v36
M 18,a[168:171],66
s_add_u32 m0,s57,16384
R 54,1,19456
v_mul_f32 v101,v37,v37
M 18,a[180:183],70
s_sub_u32 s57,s84,s57
v_fma_f32 v98,v80,v98,v79
v_fma_f32 v99,v80,v99,v79
M 22,a[172:175],66
v_fma_f32 v100,v80,v100,v79
v_fma_f32 v101,v80,v101,v79
M 22,a[184:187],70
R 58,1,20480
v_mul_f32 v98,v34,v98
M 26,a[176:179],66
D 0,56,0
v_mul_f32 v99,v35,v99
M 26,a[188:191],70
L 3
v_exp_f32 v98,v98
M 26,a[200:203],42
R 62,1,21504
v_mul_f32 v100,v36,v100
M 26,a[212:215],46
v_exp_f32 v99,v99
M 22,a[196:199],42
v_exp_f32 v100,v100
M 22,a[208:211],46
R 66,1,22528
v_mul_f32 v101,v37,v101
M 18,a[192:195],42
v_exp_f32 v101,v101
M 18,a[204:207],46
L 3
v_fma_f32 v98,v98,v81,v81
v_rcp_f32 v98,v98
M 18,a[216:219],50
R 70,1,23552
v_fma_f32 v99,v99,v81,v81
M 18,a[228:231],54
D 0,56,1024
M 22,a[220:223],50
v_rcp_f32 v99,v99
M 22,a[232:235],54
R 42,1,24576
v_fma_f32 v100,v100,v81,v81
M 26,a[224:227],50
v_rcp_f32 v100,v100
M 26,a[236:239],54
L 3
v_fma_f32 v101,v101,v81,v81
v_rcp_f32 v101,v101
M 26,a[248:251],58
R 46,1,25600
v_mul_f32 v34,v34,v98
M 26,v[132:135],62
v_mul_f32 v35,v35,v99
v_mul_f32 v36,v36,v100
M 22,a[244:247],58
v_mul_f32 v37,v37,v101
v_cvt_pk_bf16_f32 v108,v34,v35
M 22,v[128:131],62
R 50,1,26624
v_cvt_pk_bf16_f32 v109,v36,v37
M 18,a[240:243],58
buffer_store_dwordx4 v[106:109],v0,s[36:39],s67 offen offset:0 sc0 sc1
v_fma_f32 v38,-v78,v90,v38
M 18,a[252:255],62
L 3
v_fma_f32 v39,-v78,v91,v39
v_fma_f32 v40,-v78,v92,v40
M 18,v[136:139],66
R 54,1,27648
v_fma_f32 v41,-v78,v93,v41
v_mul_f32 v102,v38,v38
M 18,v[148:151],70
D 0,56,2048
M 22,v[140:143],66
v_mul_f32 v103,v39,v39
v_mul_f32 v104,v40,v40
M 22,v[152:155],70
R 58,1,28672
v_mul_f32 v105,v41,v41
M 26,v[144:147],66
v_fma_f32 v102,v80,v102,v79
v_fma_f32 v103,v80,v103,v79
M 26,v[156:159],70
L 3
v_fma_f32 v104,v80,v104,v79
v_fma_f32 v105,v80,v105,v79
M 26,v[168:171],42
R 62,1,29696
v_mul_f32 v102,v38,v102
M 26,v[180:183],46
v_exp_f32 v102,v102
v_mul_f32 v103,v39,v103
M 22,v[164:167],42
v_exp_f32 v103,v103
M 22,v[176:179],46
R 66,1,30720
v_mul_f32 v104,v40,v104
M 18,v[160:163],42
v_exp_f32 v104,v104
M 18,v[172:175],46
L 3
D 0,56,3072
M 18,v[184:187],50
s_nop 0
R 70,1,31744
v_mul_f32 v105,v41,v105
M 18,v[196:199],54
s_waitcnt vmcnt(5)
v_exp_f32 v105,v105
v_fma_f32 v102,v102,v81,v81
M 22,v[188:191],50
s_barrier
buffer_store_dword v4,v3,s[36:39],s70 offen offset:0 sc0 sc1
M 22,v[200:203],54
s_mov_b32 s70,s69
R 42,2,0
ds_read_b32 v7,v6 offset:0
M 26,v[192:195],50
s_mov_b32 s69,s68
R 74,13,0
v_fma_f32 v103,v103,v81,v81
M 26,v[204:207],54
L 5
v_rcp_f32 v102,v102
M 26,v[216:219],58
s_add_u32 s68,s68,s63
R 46,2,1024
v_fma_f32 v104,v104,v81,v81
M 26,v[228:231],62
v_rcp_f32 v103,v103
M 22,v[212:215],58
v_rcp_f32 v104,v104
v_fma_f32 v105,v105,v81,v81
M 22,v[224:227],62
L 2
R 50,2,2048
v_mul_f32 v38,v38,v102
M 18,v[208:211],58
v_rcp_f32 v105,v105
M 18,v[220:223],62
s_nop 0
v_mul_f32 v39,v39,v103
v_mul_f32 v40,v40,v104
M 18,v[232:235],66
R 54,2,3072
v_mul_f32 v41,v41,v105
M 18,v[244:247],70
v_cvt_pk_bf16_f32 v110,v38,v39
v_cvt_pk_bf16_f32 v111,v40,v41
M 22,v[236:239],66
v_cmp_eq_u32 vcc,s53,v7
M 22,v[248:251],70
s_cmp_eq_u32 vcc_lo,-1
R 58,2,4096
v_permlane16_swap_b32 v110,v112
v_permlane16_swap_b32 v111,v113
M 26,v[240:243],66
buffer_store_dwordx4 v[110:113],v10,s[36:39],s67 offen offset:0 sc0 sc1
M 26,v[252:255],70
L 3
s_cbranch_scc0 Lg3_L1_%=
Lg3_F1w0_%=:
v_mfma_f32_16x16x32_bf16 v[38:41],a[8:11],v[42:45],0
s_add_u32 m0,s57,0
R 62,2,5120
v_add_f32_dpp v115,v114,v114 quad_perm:[1,0,3,2] row_mask:0xf bank_mask:0xf
M 38,a[20:23],46
s_add_u32 s56,s55,0
buffer_load_dword v114,v11,s[36:39],s73 offen offset:0 sc1
v_mfma_f32_16x16x32_bf16 v[34:37],a[4:7],v[42:45],0
s_add_u32 s58,s58,s61
D 0,56,0
M 34,a[16:19],46
s_mov_b32 s67,s65
R 66,2,6144
v_add_f32_dpp v115,v115,v115 quad_perm:[2,3,0,1] row_mask:0xf bank_mask:0xf
v_mfma_f32_16x16x32_bf16 v[30:33],a[0:3],v[42:45],0
s_add_u32 s65,s65,s62
v_fma_f32 v18,-v74,v82,v18
v_fma_f32 v19,-v74,v83,v19
M 30,a[12:15],46
L 3
v_add_f32_dpp v118,v115,v115 row_half_mirror row_mask:0xf bank_mask:0xf
v_fma_f32 v20,-v74,v84,v20
M 30,a[24:27],50
s_add_u32 s54,s54,s46
R 70,2,7168
v_fma_f32 v21,-v74,v85,v21
v_mul_f32 v94,v18,v18
M 30,a[36:39],54
s_add_u32 s73,s73,s64
v_mov_b32_dpp v115,v118 row_ror:8 row_mask:0xf bank_mask:0xf
v_fma_f32 v115,-v118,v118,v115
M 34,a[28:31],50
v_max_f32 v115,s87,v115
v_mul_f32 v95,v19,v19
M 34,a[40:43],54
R 42,2,8192
v_mul_f32 v96,v20,v20
M 38,a[32:35],50
v_rsq_f32 v116,v115
M 38,a[44:47],54
L 3
D 0,56,1024
M 38,a[56:59],58
R 46,2,9216
v_mul_f32 v121,v115,v116
M 38,a[68:71],62
v_mul_f32 v119,s88,v116
v_mul_f32 v115,s89,v116
v_mul_f32 v120,v119,v116
M 34,a[52:55],58
v_mul_f32 v120,v120,v115
v_mul_f32 v97,v21,v21
M 34,a[64:67],62
R 50,2,10240
v_fma_f32 v94,v76,v94,v75
M 30,a[48:51],58
ds_write_b128 v12,v[118:121] offset:2048
M 30,a[60:63],62
L 4
v_fma_f32 v95,v76,v95,v75
v_fma_f32 v96,v76,v96,v75
M 30,a[72:75],66
R 54,2,11264
v_fma_f32 v97,v76,v97,v75
M 30,a[84:87],70
v_mul_f32 v94,v18,v94
v_exp_f32 v94,v94
M 34,a[76:79],66
v_mul_f32 v95,v19,v95
v_mul_f32 v96,v20,v96
M 34,a[88:91],70
R 58,2,12288
v_mul_f32 v97,v21,v97
M 38,a[80:83],66
D 0,56,2048
M 38,a[92:95],70
L 4
v_exp_f32 v95,v95
M 38,a[104:107],42
R 62,2,13312
v_fma_f32 v94,v94,v77,v77
M 38,a[116:119],46
v_exp_f32 v96,v96
M 34,a[100:103],42
v_exp_f32 v97,v97
v_fma_f32 v95,v95,v77,v77
M 34,a[112:115],46
R 66,2,14336
v_fma_f32 v96,v96,v77,v77
M 30,a[96:99],42
v_rcp_f32 v94,v94
M 30,a[108:111],46
L 3
v_rcp_f32 v95,v95
M 30,a[120:123],50
R 70,2,15360
v_fma_f32 v97,v97,v77,v77
M 30,a[132:135],54
s_waitcnt vmcnt(6)
D 0,56,3072
M 34,a[124:127],50
s_barrier
v_rcp_f32 v96,v96
M 34,a[136:139],54
s_add_u32 m0,s59,1024
R 42,2,16384
v_rcp_f32 v97,v97
M 38,a[128:131],50
s_add_u32 s56,s55,16384
v_mul_f32 v18,v18,v94
v_mul_f32 v19,v19,v95
M 38,a[140:143],54
L 3
v_mul_f32 v20,v20,v96
v_mul_f32 v21,v21,v97
M 38,a[152:155],58
s_add_u32 s55,s55,s60
R 46,2,17408
v_cvt_pk_bf16_f32 v106,v18,v19
M 38,a[164:167],62
v_cvt_pk_bf16_f32 v107,v20,v21
v_fma_f32 v22,-v74,v86,v22
M 34,a[148:151],58
v_fma_f32 v23,-v74,v87,v23
v_fma_f32 v24,-v74,v88,v24
M 34,a[160:163],62
R 50,2,18432
v_fma_f32 v25,-v74,v89,v25
v_mul_f32 v98,v22,v22
M 30,a[144:147],58
buffer_load_dword v8,s[36:39],s58 offen offset:0 sc1 lds
M 30,a[156:159],62
L 3
v_mul_f32 v99,v23,v23
v_mul_f32 v100,v24,v24
M 30,a[168:171],66
s_add_u32 m0,s57,16384
R 54,2,19456
v_mul_f32 v101,v25,v25
M 30,a[180:183],70
s_sub_u32 s57,s84,s57
v_fma_f32 v98,v76,v98,v75
v_fma_f32 v99,v76,v99,v75
M 34,a[172:175],66
v_fma_f32 v100,v76,v100,v75
v_fma_f32 v101,v76,v101,v75
M 34,a[184:187],70
R 58,2,20480
v_mul_f32 v98,v22,v98
M 38,a[176:179],66
D 0,56,0
v_mul_f32 v99,v23,v99
M 38,a[188:191],70
L 3
v_exp_f32 v98,v98
M 38,a[200:203],42
R 62,2,21504
v_mul_f32 v100,v24,v100
M 38,a[212:215],46
v_exp_f32 v99,v99
M 34,a[196:199],42
v_exp_f32 v100,v100
M 34,a[208:211],46
R 66,2,22528
v_mul_f32 v101,v25,v101
M 30,a[192:195],42
v_exp_f32 v101,v101
M 30,a[204:207],46
L 3
v_fma_f32 v98,v98,v77,v77
v_rcp_f32 v98,v98
M 30,a[216:219],50
R 70,2,23552
v_fma_f32 v99,v99,v77,v77
M 30,a[228:231],54
D 0,56,1024
M 34,a[220:223],50
v_rcp_f32 v99,v99
M 34,a[232:235],54
R 42,2,24576
v_fma_f32 v100,v100,v77,v77
M 38,a[224:227],50
v_rcp_f32 v100,v100
M 38,a[236:239],54
L 3
v_fma_f32 v101,v101,v77,v77
v_rcp_f32 v101,v101
M 38,a[248:251],58
R 46,2,25600
v_mul_f32 v22,v22,v98
M 38,v[132:135],62
v_mul_f32 v23,v23,v99
v_mul_f32 v24,v24,v100
M 34,a[244:247],58
v_mul_f32 v25,v25,v101
v_cvt_pk_bf16_f32 v108,v22,v23
M 34,v[128:131],62
R 50,2,26624
v_cvt_pk_bf16_f32 v109,v24,v25
M 30,a[240:243],58
buffer_store_dwordx4 v[106:109],v0,s[36:39],s66 offen offset:0 sc0 sc1
v_fma_f32 v26,-v74,v90,v26
M 30,a[252:255],62
L 3
v_fma_f32 v27,-v74,v91,v27
v_fma_f32 v28,-v74,v92,v28
M 30,v[136:139],66
R 54,2,27648
v_fma_f32 v29,-v74,v93,v29
v_mul_f32 v102,v26,v26
M 30,v[148:151],70
D 0,56,2048
M 34,v[140:143],66
v_mul_f32 v103,v27,v27
v_mul_f32 v104,v28,v28
M 34,v[152:155],70
R 58,2,28672
v_mul_f32 v105,v29,v29
M 38,v[144:147],66
v_fma_f32 v102,v76,v102,v75
v_fma_f32 v103,v76,v103,v75
M 38,v[156:159],70
L 3
v_fma_f32 v104,v76,v104,v75
v_fma_f32 v105,v76,v105,v75
M 38,v[168:171],42
R 62,2,29696
v_mul_f32 v102,v26,v102
M 38,v[180:183],46
v_exp_f32 v102,v102
v_mul_f32 v103,v27,v103
M 34,v[164:167],42
v_exp_f32 v103,v103
M 34,v[176:179],46
R 66,2,30720
v_mul_f32 v104,v28,v104
M 30,v[160:163],42
v_exp_f32 v104,v104
M 30,v[172:175],46
L 3
D 0,56,3072
M 30,v[184:187],50
s_nop 0
R 70,2,31744
v_mul_f32 v105,v29,v105
M 30,v[196:199],54
s_waitcnt vmcnt(5)
v_exp_f32 v105,v105
v_fma_f32 v102,v102,v77,v77
M 34,v[188:191],50
s_barrier
buffer_store_dword v4,v3,s[36:39],s70 offen offset:0 sc0 sc1
M 34,v[200:203],54
s_mov_b32 s70,s69
R 42,1,0
ds_read_b32 v7,v6 offset:1024
M 38,v[192:195],50
s_mov_b32 s69,s68
R 78,13,2048
v_fma_f32 v103,v103,v77,v77
M 38,v[204:207],54
L 5
v_rcp_f32 v102,v102
M 38,v[216:219],58
s_add_u32 s68,s68,s63
R 46,1,1024
v_fma_f32 v104,v104,v77,v77
M 38,v[228:231],62
v_rcp_f32 v103,v103
M 34,v[212:215],58
v_rcp_f32 v104,v104
v_fma_f32 v105,v105,v77,v77
M 34,v[224:227],62
L 2
R 50,1,2048
v_mul_f32 v26,v26,v102
M 30,v[208:211],58
v_rcp_f32 v105,v105
M 30,v[220:223],62
s_nop 0
v_mul_f32 v27,v27,v103
v_mul_f32 v28,v28,v104
M 30,v[232:235],66
R 54,1,3072
v_mul_f32 v29,v29,v105
M 30,v[244:247],70
)"
R"(v_cvt_pk_bf16_f32 v110,v26,v27
v_cvt_pk_bf16_f32 v111,v28,v29
M 34,v[236:239],66
v_cmp_eq_u32 vcc,s53,v7
M 34,v[248:251],70
s_cmp_eq_u32 vcc_lo,-1
R 58,1,4096
v_permlane16_swap_b32 v110,v112
v_permlane16_swap_b32 v111,v113
M 38,v[240:243],66
buffer_store_dwordx4 v[110:113],v10,s[36:39],s66 offen offset:0 sc0 sc1
M 38,v[252:255],70
L 3
s_cbranch_scc1 Lg3_F0w0_%=
s_branch Lg3_L0_%=
Lg3_F0w1_%=:
v_mfma_f32_16x16x32_bf16 v[26:29],a[8:11],v[42:45],0
s_add_u32 m0,s57,0
R 62,1,5120
v_add_f32_dpp v115,v114,v114 quad_perm:[1,0,3,2] row_mask:0xf bank_mask:0xf
M 26,a[20:23],46
s_add_u32 s56,s55,0
buffer_load_dword v114,v11,s[36:39],s73 offen offset:0 sc1
v_mfma_f32_16x16x32_bf16 v[22:25],a[4:7],v[42:45],0
s_add_u32 s58,s58,s61
D 0,56,0
M 22,a[16:19],46
s_mov_b32 s66,s65
R 66,1,6144
v_add_f32_dpp v115,v115,v115 quad_perm:[2,3,0,1] row_mask:0xf bank_mask:0xf
v_mfma_f32_16x16x32_bf16 v[18:21],a[0:3],v[42:45],0
s_add_u32 s65,s65,s62
v_fma_f32 v30,-v78,v82,v30
v_fma_f32 v31,-v78,v83,v31
M 18,a[12:15],46
L 3
v_add_f32_dpp v118,v115,v115 row_half_mirror row_mask:0xf bank_mask:0xf
v_fma_f32 v32,-v78,v84,v32
M 18,a[24:27],50
s_add_u32 s54,s54,s46
R 70,1,7168
v_fma_f32 v33,-v78,v85,v33
v_mul_f32 v94,v30,v30
M 18,a[36:39],54
s_add_u32 s73,s73,s64
v_mov_b32_dpp v115,v118 row_ror:8 row_mask:0xf bank_mask:0xf
v_fma_f32 v115,-v118,v118,v115
M 22,a[28:31],50
v_max_f32 v115,s87,v115
v_mul_f32 v95,v31,v31
M 22,a[40:43],54
R 42,1,8192
v_mul_f32 v96,v32,v32
M 26,a[32:35],50
v_rsq_f32 v116,v115
M 26,a[44:47],54
L 3
D 0,56,1024
M 26,a[56:59],58
R 46,1,9216
v_mul_f32 v121,v115,v116
M 26,a[68:71],62
v_mul_f32 v119,s88,v116
v_mul_f32 v115,s89,v116
v_mul_f32 v120,v119,v116
M 22,a[52:55],58
v_mul_f32 v120,v120,v115
v_mul_f32 v97,v33,v33
M 22,a[64:67],62
R 50,1,10240
v_fma_f32 v94,v80,v94,v79
M 18,a[48:51],58
ds_write_b128 v12,v[118:121] offset:0
M 18,a[60:63],62
L 4
v_fma_f32 v95,v80,v95,v79
v_fma_f32 v96,v80,v96,v79
M 18,a[72:75],66
R 54,1,11264
v_fma_f32 v97,v80,v97,v79
M 18,a[84:87],70
v_mul_f32 v94,v30,v94
v_exp_f32 v94,v94
M 22,a[76:79],66
v_mul_f32 v95,v31,v95
v_mul_f32 v96,v32,v96
M 22,a[88:91],70
R 58,1,12288
v_mul_f32 v97,v33,v97
M 26,a[80:83],66
D 0,56,2048
M 26,a[92:95],70
L 4
v_exp_f32 v95,v95
M 26,a[104:107],42
R 62,1,13312
v_fma_f32 v94,v94,v81,v81
M 26,a[116:119],46
v_exp_f32 v96,v96
M 22,a[100:103],42
v_exp_f32 v97,v97
v_fma_f32 v95,v95,v81,v81
M 22,a[112:115],46
R 66,1,14336
v_fma_f32 v96,v96,v81,v81
M 18,a[96:99],42
v_rcp_f32 v94,v94
M 18,a[108:111],46
L 3
v_rcp_f32 v95,v95
M 18,a[120:123],50
s_waitcnt vmcnt(6)
R 70,1,15360
v_fma_f32 v97,v97,v81,v81
M 18,a[132:135],54
s_barrier
D 0,56,3072
M 22,a[124:127],50
s_add_u32 m0,s59,0
v_rcp_f32 v96,v96
M 22,a[136:139],54
s_add_u32 s56,s55,16384
R 42,1,16384
v_rcp_f32 v97,v97
M 26,a[128:131],50
s_add_u32 s55,s55,s60
v_mul_f32 v30,v30,v94
v_mul_f32 v31,v31,v95
M 26,a[140:143],54
L 3
v_mul_f32 v32,v32,v96
v_mul_f32 v33,v33,v97
M 26,a[152:155],58
R 46,1,17408
v_cvt_pk_bf16_f32 v106,v30,v31
M 26,a[164:167],62
v_cvt_pk_bf16_f32 v107,v32,v33
v_fma_f32 v34,-v78,v86,v34
M 22,a[148:151],58
v_fma_f32 v35,-v78,v87,v35
v_fma_f32 v36,-v78,v88,v36
M 22,a[160:163],62
R 50,1,18432
v_fma_f32 v37,-v78,v89,v37
v_mul_f32 v98,v34,v34
M 18,a[144:147],58
buffer_load_dword v8,s[36:39],s58 offen offset:0 sc1 lds
M 18,a[156:159],62
L 3
v_mul_f32 v99,v35,v35
v_mul_f32 v100,v36,v36
M 18,a[168:171],66
s_add_u32 m0,s57,16384
R 54,1,19456
v_mul_f32 v101,v37,v37
M 18,a[180:183],70
s_sub_u32 s57,s84,s57
v_fma_f32 v98,v80,v98,v79
v_fma_f32 v99,v80,v99,v79
M 22,a[172:175],66
v_fma_f32 v100,v80,v100,v79
v_fma_f32 v101,v80,v101,v79
M 22,a[184:187],70
R 58,1,20480
v_mul_f32 v98,v34,v98
M 26,a[176:179],66
D 0,56,0
v_mul_f32 v99,v35,v99
M 26,a[188:191],70
L 3
v_exp_f32 v98,v98
M 26,a[200:203],42
R 62,1,21504
v_mul_f32 v100,v36,v100
M 26,a[212:215],46
v_exp_f32 v99,v99
M 22,a[196:199],42
v_exp_f32 v100,v100
M 22,a[208:211],46
R 66,1,22528
v_mul_f32 v101,v37,v101
M 18,a[192:195],42
v_exp_f32 v101,v101
M 18,a[204:207],46
L 3
v_fma_f32 v98,v98,v81,v81
v_rcp_f32 v98,v98
M 18,a[216:219],50
R 70,1,23552
v_fma_f32 v99,v99,v81,v81
M 18,a[228:231],54
D 0,56,1024
M 22,a[220:223],50
v_rcp_f32 v99,v99
M 22,a[232:235],54
R 42,1,24576
v_fma_f32 v100,v100,v81,v81
M 26,a[224:227],50
v_rcp_f32 v100,v100
M 26,a[236:239],54
L 3
v_fma_f32 v101,v101,v81,v81
v_rcp_f32 v101,v101
M 26,a[248:251],58
R 46,1,25600
v_mul_f32 v34,v34,v98
M 26,v[132:135],62
v_mul_f32 v35,v35,v99
v_mul_f32 v36,v36,v100
M 22,a[244:247],58
v_mul_f32 v37,v37,v101
v_cvt_pk_bf16_f32 v108,v34,v35
M 22,v[128:131],62
R 50,1,26624
v_cvt_pk_bf16_f32 v109,v36,v37
M 18,a[240:243],58
buffer_store_dwordx4 v[106:109],v0,s[36:39],s67 offen offset:0 sc0 sc1
v_fma_f32 v38,-v78,v90,v38
M 18,a[252:255],62
L 3
v_fma_f32 v39,-v78,v91,v39
v_fma_f32 v40,-v78,v92,v40
M 18,v[136:139],66
R 54,1,27648
v_fma_f32 v41,-v78,v93,v41
v_mul_f32 v102,v38,v38
M 18,v[148:151],70
D 0,56,2048
M 22,v[140:143],66
v_mul_f32 v103,v39,v39
v_mul_f32 v104,v40,v40
M 22,v[152:155],70
R 58,1,28672
v_mul_f32 v105,v41,v41
M 26,v[144:147],66
v_fma_f32 v102,v80,v102,v79
v_fma_f32 v103,v80,v103,v79
M 26,v[156:159],70
L 3
v_fma_f32 v104,v80,v104,v79
v_fma_f32 v105,v80,v105,v79
M 26,v[168:171],42
R 62,1,29696
v_mul_f32 v102,v38,v102
M 26,v[180:183],46
v_exp_f32 v102,v102
v_mul_f32 v103,v39,v103
M 22,v[164:167],42
v_exp_f32 v103,v103
M 22,v[176:179],46
R 66,1,30720
v_mul_f32 v104,v40,v104
M 18,v[160:163],42
v_exp_f32 v104,v104
M 18,v[172:175],46
L 3
s_nop 0
D 0,56,3072
M 18,v[184:187],50
s_waitcnt vmcnt(5)
R 70,1,31744
v_mul_f32 v105,v41,v105
M 18,v[196:199],54
s_barrier
buffer_store_dword v4,v3,s[36:39],s70 offen offset:0 sc0 sc1
v_fma_f32 v102,v102,v81,v81
M 22,v[188:191],50
s_mov_b32 s70,s69
ds_read_b32 v7,v6 offset:0
R 74,13,0
M 22,v[200:203],54
s_mov_b32 s69,s68
R 42,2,0
v_fma_f32 v103,v103,v81,v81
M 26,v[192:195],50
s_add_u32 s68,s68,s63
v_exp_f32 v105,v105
M 26,v[204:207],54
L 5
v_rcp_f32 v102,v102
M 26,v[216:219],58
R 46,2,1024
v_fma_f32 v104,v104,v81,v81
M 26,v[228:231],62
v_rcp_f32 v103,v103
M 22,v[212:215],58
L 3
v_rcp_f32 v104,v104
v_fma_f32 v105,v105,v81,v81
M 22,v[224:227],62
R 50,2,2048
v_mul_f32 v38,v38,v102
M 18,v[208:211],58
v_rcp_f32 v105,v105
M 18,v[220:223],62
s_nop 0
v_mul_f32 v39,v39,v103
v_mul_f32 v40,v40,v104
M 18,v[232:235],66
R 54,2,3072
v_mul_f32 v41,v41,v105
M 18,v[244:247],70
v_cvt_pk_bf16_f32 v110,v38,v39
v_cvt_pk_bf16_f32 v111,v40,v41
M 22,v[236:239],66
v_cmp_eq_u32 vcc,s53,v7
M 22,v[248:251],70
s_cmp_eq_u32 vcc_lo,-1
R 58,2,4096
v_permlane16_swap_b32 v110,v112
v_permlane16_swap_b32 v111,v113
M 26,v[240:243],66
buffer_store_dwordx4 v[110:113],v10,s[36:39],s67 offen offset:0 sc0 sc1
M 26,v[252:255],70
L 3
s_cbranch_scc0 Lg3_L1_%=
Lg3_F1w1_%=:
v_mfma_f32_16x16x32_bf16 v[38:41],a[8:11],v[42:45],0
s_add_u32 m0,s57,0
R 62,2,5120
v_add_f32_dpp v115,v114,v114 quad_perm:[1,0,3,2] row_mask:0xf bank_mask:0xf
M 38,a[20:23],46
s_add_u32 s56,s55,0
buffer_load_dword v114,v11,s[36:39],s73 offen offset:0 sc1
v_mfma_f32_16x16x32_bf16 v[34:37],a[4:7],v[42:45],0
s_add_u32 s58,s58,s61
D 0,56,0
M 34,a[16:19],46
s_mov_b32 s67,s65
R 66,2,6144
v_add_f32_dpp v115,v115,v115 quad_perm:[2,3,0,1] row_mask:0xf bank_mask:0xf
v_mfma_f32_16x16x32_bf16 v[30:33],a[0:3],v[42:45],0
s_add_u32 s65,s65,s62
v_fma_f32 v18,-v74,v82,v18
v_fma_f32 v19,-v74,v83,v19
M 30,a[12:15],46
L 3
v_add_f32_dpp v118,v115,v115 row_half_mirror row_mask:0xf bank_mask:0xf
v_fma_f32 v20,-v74,v84,v20
M 30,a[24:27],50
s_add_u32 s54,s54,s46
R 70,2,7168
v_fma_f32 v21,-v74,v85,v21
v_mul_f32 v94,v18,v18
M 30,a[36:39],54
s_add_u32 s73,s73,s64
v_mov_b32_dpp v115,v118 row_ror:8 row_mask:0xf bank_mask:0xf
v_fma_f32 v115,-v118,v118,v115
M 34,a[28:31],50
v_max_f32 v115,s87,v115
v_mul_f32 v95,v19,v19
M 34,a[40:43],54
R 42,2,8192
v_mul_f32 v96,v20,v20
M 38,a[32:35],50
v_rsq_f32 v116,v115
M 38,a[44:47],54
L 3
D 0,56,1024
M 38,a[56:59],58
R 46,2,9216
v_mul_f32 v121,v115,v116
M 38,a[68:71],62
v_mul_f32 v119,s88,v116
v_mul_f32 v115,s89,v116
v_mul_f32 v120,v119,v116
M 34,a[52:55],58
v_mul_f32 v120,v120,v115
v_mul_f32 v97,v21,v21
M 34,a[64:67],62
R 50,2,10240
v_fma_f32 v94,v76,v94,v75
M 30,a[48:51],58
ds_write_b128 v12,v[118:121] offset:2048
M 30,a[60:63],62
L 4
v_fma_f32 v95,v76,v95,v75
v_fma_f32 v96,v76,v96,v75
M 30,a[72:75],66
R 54,2,11264
v_fma_f32 v97,v76,v97,v75
M 30,a[84:87],70
v_mul_f32 v94,v18,v94
v_exp_f32 v94,v94
M 34,a[76:79],66
v_mul_f32 v95,v19,v95
v_mul_f32 v96,v20,v96
M 34,a[88:91],70
R 58,2,12288
v_mul_f32 v97,v21,v97
M 38,a[80:83],66
D 0,56,2048
M 38,a[92:95],70
L 4
v_exp_f32 v95,v95
M 38,a[104:107],42
R 62,2,13312
v_fma_f32 v94,v94,v77,v77
M 38,a[116:119],46
v_exp_f32 v96,v96
M 34,a[100:103],42
v_exp_f32 v97,v97
v_fma_f32 v95,v95,v77,v77
M 34,a[112:115],46
R 66,2,14336
v_fma_f32 v96,v96,v77,v77
M 30,a[96:99],42
v_rcp_f32 v94,v94
M 30,a[108:111],46
L 3
v_rcp_f32 v95,v95
M 30,a[120:123],50
s_waitcnt vmcnt(6)
R 70,2,15360
v_fma_f32 v97,v97,v77,v77
M 30,a[132:135],54
s_barrier
D 0,56,3072
M 34,a[124:127],50
s_add_u32 m0,s59,1024
v_rcp_f32 v96,v96
M 34,a[136:139],54
s_add_u32 s56,s55,16384
R 42,2,16384
v_rcp_f32 v97,v97
M 38,a[128:131],50
s_add_u32 s55,s55,s60
v_mul_f32 v18,v18,v94
v_mul_f32 v19,v19,v95
M 38,a[140:143],54
L 3
v_mul_f32 v20,v20,v96
v_mul_f32 v21,v21,v97
M 38,a[152:155],58
R 46,2,17408
v_cvt_pk_bf16_f32 v106,v18,v19
M 38,a[164:167],62
v_cvt_pk_bf16_f32 v107,v20,v21
v_fma_f32 v22,-v74,v86,v22
M 34,a[148:151],58
v_fma_f32 v23,-v74,v87,v23
v_fma_f32 v24,-v74,v88,v24
M 34,a[160:163],62
R 50,2,18432
v_fma_f32 v25,-v74,v89,v25
v_mul_f32 v98,v22,v22
M 30,a[144:147],58
buffer_load_dword v8,s[36:39],s58 offen offset:0 sc1 lds
M 30,a[156:159],62
L 3
v_mul_f32 v99,v23,v23
v_mul_f32 v100,v24,v24
M 30,a[168:171],66
s_add_u32 m0,s57,16384
R 54,2,19456
v_mul_f32 v101,v25,v25
M 30,a[180:183],70
s_sub_u32 s57,s84,s57
v_fma_f32 v98,v76,v98,v75
v_fma_f32 v99,v76,v99,v75
M 34,a[172:175],66
v_fma_f32 v100,v76,v100,v75
v_fma_f32 v101,v76,v101,v75
M 34,a[184:187],70
R 58,2,20480
v_mul_f32 v98,v22,v98
M 38,a[176:179],66
D 0,56,0
v_mul_f32 v99,v23,v99
M 38,a[188:191],70
L 3
v_exp_f32 v98,v98
M 38,a[200:203],42
R 62,2,21504
v_mul_f32 v100,v24,v100
M 38,a[212:215],46
v_exp_f32 v99,v99
M 34,a[196:199],42
v_exp_f32 v100,v100
M 34,a[208:211],46
R 66,2,22528
v_mul_f32 v101,v25,v101
M 30,a[192:195],42
v_exp_f32 v101,v101
M 30,a[204:207],46
L 3
v_fma_f32 v98,v98,v77,v77
v_rcp_f32 v98,v98
M 30,a[216:219],50
R 70,2,23552
v_fma_f32 v99,v99,v77,v77
M 30,a[228:231],54
D 0,56,1024
M 34,a[220:223],50
v_rcp_f32 v99,v99
M 34,a[232:235],54
R 42,2,24576
v_fma_f32 v100,v100,v77,v77
M 38,a[224:227],50
v_rcp_f32 v100,v100
)"
R"(M 38,a[236:239],54
L 3
v_fma_f32 v101,v101,v77,v77
v_rcp_f32 v101,v101
M 38,a[248:251],58
R 46,2,25600
v_mul_f32 v22,v22,v98
M 38,v[132:135],62
v_mul_f32 v23,v23,v99
v_mul_f32 v24,v24,v100
M 34,a[244:247],58
v_mul_f32 v25,v25,v101
v_cvt_pk_bf16_f32 v108,v22,v23
M 34,v[128:131],62
R 50,2,26624
v_cvt_pk_bf16_f32 v109,v24,v25
M 30,a[240:243],58
buffer_store_dwordx4 v[106:109],v0,s[36:39],s66 offen offset:0 sc0 sc1
v_fma_f32 v26,-v74,v90,v26
M 30,a[252:255],62
L 3
v_fma_f32 v27,-v74,v91,v27
v_fma_f32 v28,-v74,v92,v28
M 30,v[136:139],66
R 54,2,27648
v_fma_f32 v29,-v74,v93,v29
v_mul_f32 v102,v26,v26
M 30,v[148:151],70
D 0,56,2048
M 34,v[140:143],66
v_mul_f32 v103,v27,v27
v_mul_f32 v104,v28,v28
M 34,v[152:155],70
R 58,2,28672
v_mul_f32 v105,v29,v29
M 38,v[144:147],66
v_fma_f32 v102,v76,v102,v75
v_fma_f32 v103,v76,v103,v75
M 38,v[156:159],70
L 3
v_fma_f32 v104,v76,v104,v75
v_fma_f32 v105,v76,v105,v75
M 38,v[168:171],42
R 62,2,29696
v_mul_f32 v102,v26,v102
M 38,v[180:183],46
v_exp_f32 v102,v102
v_mul_f32 v103,v27,v103
M 34,v[164:167],42
v_exp_f32 v103,v103
M 34,v[176:179],46
R 66,2,30720
v_mul_f32 v104,v28,v104
M 30,v[160:163],42
v_exp_f32 v104,v104
M 30,v[172:175],46
L 3
s_nop 0
D 0,56,3072
M 30,v[184:187],50
s_waitcnt vmcnt(5)
R 70,2,31744
v_mul_f32 v105,v29,v105
M 30,v[196:199],54
s_barrier
buffer_store_dword v4,v3,s[36:39],s70 offen offset:0 sc0 sc1
v_fma_f32 v102,v102,v77,v77
M 34,v[188:191],50
s_mov_b32 s70,s69
ds_read_b32 v7,v6 offset:1024
R 78,13,2048
M 34,v[200:203],54
s_mov_b32 s69,s68
R 42,1,0
v_fma_f32 v103,v103,v77,v77
M 38,v[192:195],50
s_add_u32 s68,s68,s63
v_exp_f32 v105,v105
M 38,v[204:207],54
L 5
v_rcp_f32 v102,v102
M 38,v[216:219],58
R 46,1,1024
v_fma_f32 v104,v104,v77,v77
M 38,v[228:231],62
v_rcp_f32 v103,v103
M 34,v[212:215],58
L 3
v_rcp_f32 v104,v104
v_fma_f32 v105,v105,v77,v77
M 34,v[224:227],62
R 50,1,2048
v_mul_f32 v26,v26,v102
M 30,v[208:211],58
v_rcp_f32 v105,v105
M 30,v[220:223],62
s_nop 0
v_mul_f32 v27,v27,v103
v_mul_f32 v28,v28,v104
M 30,v[232:235],66
R 54,1,3072
v_mul_f32 v29,v29,v105
M 30,v[244:247],70
v_cvt_pk_bf16_f32 v110,v26,v27
v_cvt_pk_bf16_f32 v111,v28,v29
M 34,v[236:239],66
v_cmp_eq_u32 vcc,s53,v7
M 34,v[248:251],70
s_cmp_eq_u32 vcc_lo,-1
R 58,1,4096
v_permlane16_swap_b32 v110,v112
v_permlane16_swap_b32 v111,v113
M 38,v[240:243],66
buffer_store_dwordx4 v[110:113],v10,s[36:39],s66 offen offset:0 sc0 sc1
M 38,v[252:255],70
L 3
s_cbranch_scc1 Lg3_F0w1_%=
s_branch Lg3_L0_%=
Lg3_F0w2_%=:
v_mfma_f32_16x16x32_bf16 v[26:29],a[8:11],v[42:45],0
s_add_u32 m0,s57,0
R 62,1,5120
v_add_f32_dpp v115,v114,v114 quad_perm:[1,0,3,2] row_mask:0xf bank_mask:0xf
M 26,a[20:23],46
s_add_u32 s56,s55,0
buffer_load_dword v114,v11,s[36:39],s73 offen offset:0 sc1
v_mfma_f32_16x16x32_bf16 v[22:25],a[4:7],v[42:45],0
s_add_u32 s58,s58,s61
D 0,56,0
M 22,a[16:19],46
s_mov_b32 s66,s65
R 66,1,6144
v_add_f32_dpp v115,v115,v115 quad_perm:[2,3,0,1] row_mask:0xf bank_mask:0xf
v_mfma_f32_16x16x32_bf16 v[18:21],a[0:3],v[42:45],0
s_add_u32 s65,s65,s62
v_fma_f32 v30,-v78,v82,v30
v_fma_f32 v31,-v78,v83,v31
M 18,a[12:15],46
L 3
v_add_f32_dpp v118,v115,v115 row_half_mirror row_mask:0xf bank_mask:0xf
v_fma_f32 v32,-v78,v84,v32
M 18,a[24:27],50
s_add_u32 s54,s54,s46
R 70,1,7168
v_fma_f32 v33,-v78,v85,v33
v_mul_f32 v94,v30,v30
M 18,a[36:39],54
s_add_u32 s73,s73,s64
v_mov_b32_dpp v115,v118 row_ror:8 row_mask:0xf bank_mask:0xf
v_fma_f32 v115,-v118,v118,v115
M 22,a[28:31],50
v_max_f32 v115,s87,v115
v_mul_f32 v95,v31,v31
M 22,a[40:43],54
R 42,1,8192
v_mul_f32 v96,v32,v32
M 26,a[32:35],50
v_rsq_f32 v116,v115
M 26,a[44:47],54
L 3
D 0,56,1024
M 26,a[56:59],58
R 46,1,9216
v_mul_f32 v121,v115,v116
M 26,a[68:71],62
v_mul_f32 v119,s88,v116
v_mul_f32 v115,s89,v116
v_mul_f32 v120,v119,v116
M 22,a[52:55],58
v_mul_f32 v120,v120,v115
v_mul_f32 v97,v33,v33
M 22,a[64:67],62
R 50,1,10240
v_fma_f32 v94,v80,v94,v79
M 18,a[48:51],58
ds_write_b128 v12,v[118:121] offset:0
M 18,a[60:63],62
L 4
v_fma_f32 v95,v80,v95,v79
v_fma_f32 v96,v80,v96,v79
M 18,a[72:75],66
R 54,1,11264
v_fma_f32 v97,v80,v97,v79
M 18,a[84:87],70
v_mul_f32 v94,v30,v94
v_exp_f32 v94,v94
M 22,a[76:79],66
v_mul_f32 v95,v31,v95
v_mul_f32 v96,v32,v96
M 22,a[88:91],70
R 58,1,12288
v_mul_f32 v97,v33,v97
M 26,a[80:83],66
D 0,56,2048
M 26,a[92:95],70
L 4
v_exp_f32 v95,v95
M 26,a[104:107],42
R 62,1,13312
v_fma_f32 v94,v94,v81,v81
M 26,a[116:119],46
v_exp_f32 v96,v96
M 22,a[100:103],42
v_exp_f32 v97,v97
v_fma_f32 v95,v95,v81,v81
M 22,a[112:115],46
R 66,1,14336
v_fma_f32 v96,v96,v81,v81
M 18,a[96:99],42
v_rcp_f32 v94,v94
M 18,a[108:111],46
s_waitcnt vmcnt(6) lgkmcnt(3)
v_rcp_f32 v95,v95
M 18,a[120:123],50
s_barrier
R 70,1,15360
v_fma_f32 v97,v97,v81,v81
M 18,a[132:135],54
D 0,56,3072
M 22,a[124:127],50
s_add_u32 m0,s59,0
v_rcp_f32 v96,v96
M 22,a[136:139],54
s_add_u32 s56,s55,16384
R 42,1,16384
v_rcp_f32 v97,v97
M 26,a[128:131],50
s_add_u32 s55,s55,s60
v_mul_f32 v30,v30,v94
v_mul_f32 v31,v31,v95
M 26,a[140:143],54
L 3
v_mul_f32 v32,v32,v96
v_mul_f32 v33,v33,v97
M 26,a[152:155],58
R 46,1,17408
v_cvt_pk_bf16_f32 v106,v30,v31
M 26,a[164:167],62
v_cvt_pk_bf16_f32 v107,v32,v33
v_fma_f32 v34,-v78,v86,v34
M 22,a[148:151],58
v_fma_f32 v35,-v78,v87,v35
v_fma_f32 v36,-v78,v88,v36
M 22,a[160:163],62
R 50,1,18432
v_fma_f32 v37,-v78,v89,v37
v_mul_f32 v98,v34,v34
M 18,a[144:147],58
buffer_load_dword v8,s[36:39],s58 offen offset:0 sc1 lds
M 18,a[156:159],62
L 3
v_mul_f32 v99,v35,v35
v_mul_f32 v100,v36,v36
M 18,a[168:171],66
s_add_u32 m0,s57,16384
R 54,1,19456
v_mul_f32 v101,v37,v37
M 18,a[180:183],70
s_sub_u32 s57,s84,s57
v_fma_f32 v98,v80,v98,v79
v_fma_f32 v99,v80,v99,v79
M 22,a[172:175],66
v_fma_f32 v100,v80,v100,v79
v_fma_f32 v101,v80,v101,v79
M 22,a[184:187],70
R 58,1,20480
v_mul_f32 v98,v34,v98
M 26,a[176:179],66
D 0,56,0
v_mul_f32 v99,v35,v99
M 26,a[188:191],70
L 3
v_exp_f32 v98,v98
M 26,a[200:203],42
R 62,1,21504
v_mul_f32 v100,v36,v100
M 26,a[212:215],46
v_exp_f32 v99,v99
M 22,a[196:199],42
v_exp_f32 v100,v100
M 22,a[208:211],46
R 66,1,22528
v_mul_f32 v101,v37,v101
M 18,a[192:195],42
v_exp_f32 v101,v101
M 18,a[204:207],46
L 3
v_fma_f32 v98,v98,v81,v81
v_rcp_f32 v98,v98
M 18,a[216:219],50
R 70,1,23552
v_fma_f32 v99,v99,v81,v81
M 18,a[228:231],54
D 0,56,1024
M 22,a[220:223],50
v_rcp_f32 v99,v99
M 22,a[232:235],54
R 42,1,24576
v_fma_f32 v100,v100,v81,v81
M 26,a[224:227],50
v_rcp_f32 v100,v100
M 26,a[236:239],54
L 3
v_fma_f32 v101,v101,v81,v81
v_rcp_f32 v101,v101
M 26,a[248:251],58
R 46,1,25600
v_mul_f32 v34,v34,v98
M 26,v[132:135],62
v_mul_f32 v35,v35,v99
v_mul_f32 v36,v36,v100
M 22,a[244:247],58
v_mul_f32 v37,v37,v101
v_cvt_pk_bf16_f32 v108,v34,v35
M 22,v[128:131],62
R 50,1,26624
v_cvt_pk_bf16_f32 v109,v36,v37
M 18,a[240:243],58
buffer_store_dwordx4 v[106:109],v0,s[36:39],s67 offen offset:0 sc0 sc1
v_fma_f32 v38,-v78,v90,v38
M 18,a[252:255],62
L 3
v_fma_f32 v39,-v78,v91,v39
v_fma_f32 v40,-v78,v92,v40
M 18,v[136:139],66
R 54,1,27648
v_fma_f32 v41,-v78,v93,v41
v_mul_f32 v102,v38,v38
M 18,v[148:151],70
D 0,56,2048
M 22,v[140:143],66
v_mul_f32 v103,v39,v39
v_mul_f32 v104,v40,v40
M 22,v[152:155],70
R 58,1,28672
v_mul_f32 v105,v41,v41
M 26,v[144:147],66
v_fma_f32 v102,v80,v102,v79
v_fma_f32 v103,v80,v103,v79
M 26,v[156:159],70
L 3
v_fma_f32 v104,v80,v104,v79
v_fma_f32 v105,v80,v105,v79
M 26,v[168:171],42
R 62,1,29696
v_mul_f32 v102,v38,v102
M 26,v[180:183],46
v_exp_f32 v102,v102
v_mul_f32 v103,v39,v103
M 22,v[164:167],42
v_exp_f32 v103,v103
M 22,v[176:179],46
R 66,1,30720
v_mul_f32 v104,v40,v104
M 18,v[160:163],42
s_nop 0
v_exp_f32 v104,v104
M 18,v[172:175],46
s_waitcnt vmcnt(4) lgkmcnt(3)
D 0,56,3072
M 18,v[184:187],50
s_barrier
R 70,1,31744
ds_read_b32 v7,v6 offset:0
M 18,v[196:199],54
buffer_store_dword v4,v3,s[36:39],s70 offen offset:0 sc0 sc1
v_mul_f32 v105,v41,v105
M 22,v[188:191],50
s_mov_b32 s70,s69
R 74,13,0
v_fma_f32 v102,v102,v81,v81
M 22,v[200:203],54
s_mov_b32 s69,s68
R 42,2,0
v_fma_f32 v103,v103,v81,v81
M 26,v[192:195],50
s_add_u32 s68,s68,s63
v_exp_f32 v105,v105
M 26,v[204:207],54
L 5
v_rcp_f32 v102,v102
M 26,v[216:219],58
L 2
R 46,2,1024
v_fma_f32 v104,v104,v81,v81
M 26,v[228:231],62
v_rcp_f32 v103,v103
M 22,v[212:215],58
v_rcp_f32 v104,v104
v_fma_f32 v105,v105,v81,v81
M 22,v[224:227],62
R 50,2,2048
v_mul_f32 v38,v38,v102
M 18,v[208:211],58
v_rcp_f32 v105,v105
M 18,v[220:223],62
s_nop 0
v_mul_f32 v39,v39,v103
v_mul_f32 v40,v40,v104
M 18,v[232:235],66
R 54,2,3072
v_mul_f32 v41,v41,v105
M 18,v[244:247],70
v_cvt_pk_bf16_f32 v110,v38,v39
v_cvt_pk_bf16_f32 v111,v40,v41
M 22,v[236:239],66
v_cmp_eq_u32 vcc,s53,v7
M 22,v[248:251],70
s_cmp_eq_u32 vcc_lo,-1
R 58,2,4096
v_permlane16_swap_b32 v110,v112
v_permlane16_swap_b32 v111,v113
M 26,v[240:243],66
buffer_store_dwordx4 v[110:113],v10,s[36:39],s67 offen offset:0 sc0 sc1
M 26,v[252:255],70
L 3
s_cbranch_scc0 Lg3_L1_%=
Lg3_F1w2_%=:
v_mfma_f32_16x16x32_bf16 v[38:41],a[8:11],v[42:45],0
s_add_u32 m0,s57,0
R 62,2,5120
v_add_f32_dpp v115,v114,v114 quad_perm:[1,0,3,2] row_mask:0xf bank_mask:0xf
M 38,a[20:23],46
s_add_u32 s56,s55,0
buffer_load_dword v114,v11,s[36:39],s73 offen offset:0 sc1
v_mfma_f32_16x16x32_bf16 v[34:37],a[4:7],v[42:45],0
s_add_u32 s58,s58,s61
D 0,56,0
M 34,a[16:19],46
s_mov_b32 s67,s65
R 66,2,6144
v_add_f32_dpp v115,v115,v115 quad_perm:[2,3,0,1] row_mask:0xf bank_mask:0xf
v_mfma_f32_16x16x32_bf16 v[30:33],a[0:3],v[42:45],0
s_add_u32 s65,s65,s62
v_fma_f32 v18,-v74,v82,v18
v_fma_f32 v19,-v74,v83,v19
M 30,a[12:15],46
L 3
v_add_f32_dpp v118,v115,v115 row_half_mirror row_mask:0xf bank_mask:0xf
v_fma_f32 v20,-v74,v84,v20
M 30,a[24:27],50
s_add_u32 s54,s54,s46
R 70,2,7168
v_fma_f32 v21,-v74,v85,v21
v_mul_f32 v94,v18,v18
M 30,a[36:39],54
s_add_u32 s73,s73,s64
v_mov_b32_dpp v115,v118 row_ror:8 row_mask:0xf bank_mask:0xf
v_fma_f32 v115,-v118,v118,v115
M 34,a[28:31],50
v_max_f32 v115,s87,v115
v_mul_f32 v95,v19,v19
M 34,a[40:43],54
R 42,2,8192
v_mul_f32 v96,v20,v20
M 38,a[32:35],50
v_rsq_f32 v116,v115
M 38,a[44:47],54
L 3
D 0,56,1024
M 38,a[56:59],58
R 46,2,9216
v_mul_f32 v121,v115,v116
M 38,a[68:71],62
v_mul_f32 v119,s88,v116
v_mul_f32 v115,s89,v116
v_mul_f32 v120,v119,v116
M 34,a[52:55],58
v_mul_f32 v120,v120,v115
v_mul_f32 v97,v21,v21
M 34,a[64:67],62
R 50,2,10240
v_fma_f32 v94,v76,v94,v75
M 30,a[48:51],58
ds_write_b128 v12,v[118:121] offset:2048
M 30,a[60:63],62
L 4
v_fma_f32 v95,v76,v95,v75
v_fma_f32 v96,v76,v96,v75
M 30,a[72:75],66
R 54,2,11264
v_fma_f32 v97,v76,v97,v75
M 30,a[84:87],70
v_mul_f32 v94,v18,v94
v_exp_f32 v94,v94
M 34,a[76:79],66
v_mul_f32 v95,v19,v95
v_mul_f32 v96,v20,v96
M 34,a[88:91],70
R 58,2,12288
v_mul_f32 v97,v21,v97
M 38,a[80:83],66
D 0,56,2048
M 38,a[92:95],70
L 4
v_exp_f32 v95,v95
M 38,a[104:107],42
R 62,2,13312
v_fma_f32 v94,v94,v77,v77
M 38,a[116:119],46
)"
R"(v_exp_f32 v96,v96
M 34,a[100:103],42
v_exp_f32 v97,v97
v_fma_f32 v95,v95,v77,v77
M 34,a[112:115],46
R 66,2,14336
v_fma_f32 v96,v96,v77,v77
M 30,a[96:99],42
v_rcp_f32 v94,v94
M 30,a[108:111],46
s_waitcnt vmcnt(6) lgkmcnt(3)
v_rcp_f32 v95,v95
M 30,a[120:123],50
s_barrier
R 70,2,15360
v_fma_f32 v97,v97,v77,v77
M 30,a[132:135],54
D 0,56,3072
M 34,a[124:127],50
s_add_u32 m0,s59,1024
v_rcp_f32 v96,v96
M 34,a[136:139],54
s_add_u32 s56,s55,16384
R 42,2,16384
v_rcp_f32 v97,v97
M 38,a[128:131],50
s_add_u32 s55,s55,s60
v_mul_f32 v18,v18,v94
v_mul_f32 v19,v19,v95
M 38,a[140:143],54
L 3
v_mul_f32 v20,v20,v96
v_mul_f32 v21,v21,v97
M 38,a[152:155],58
R 46,2,17408
v_cvt_pk_bf16_f32 v106,v18,v19
M 38,a[164:167],62
v_cvt_pk_bf16_f32 v107,v20,v21
v_fma_f32 v22,-v74,v86,v22
M 34,a[148:151],58
v_fma_f32 v23,-v74,v87,v23
v_fma_f32 v24,-v74,v88,v24
M 34,a[160:163],62
R 50,2,18432
v_fma_f32 v25,-v74,v89,v25
v_mul_f32 v98,v22,v22
M 30,a[144:147],58
buffer_load_dword v8,s[36:39],s58 offen offset:0 sc1 lds
M 30,a[156:159],62
L 3
v_mul_f32 v99,v23,v23
v_mul_f32 v100,v24,v24
M 30,a[168:171],66
s_add_u32 m0,s57,16384
R 54,2,19456
v_mul_f32 v101,v25,v25
M 30,a[180:183],70
s_sub_u32 s57,s84,s57
v_fma_f32 v98,v76,v98,v75
v_fma_f32 v99,v76,v99,v75
M 34,a[172:175],66
v_fma_f32 v100,v76,v100,v75
v_fma_f32 v101,v76,v101,v75
M 34,a[184:187],70
R 58,2,20480
v_mul_f32 v98,v22,v98
M 38,a[176:179],66
D 0,56,0
v_mul_f32 v99,v23,v99
M 38,a[188:191],70
L 3
v_exp_f32 v98,v98
M 38,a[200:203],42
R 62,2,21504
v_mul_f32 v100,v24,v100
M 38,a[212:215],46
v_exp_f32 v99,v99
M 34,a[196:199],42
v_exp_f32 v100,v100
M 34,a[208:211],46
R 66,2,22528
v_mul_f32 v101,v25,v101
M 30,a[192:195],42
v_exp_f32 v101,v101
M 30,a[204:207],46
L 3
v_fma_f32 v98,v98,v77,v77
v_rcp_f32 v98,v98
M 30,a[216:219],50
R 70,2,23552
v_fma_f32 v99,v99,v77,v77
M 30,a[228:231],54
D 0,56,1024
M 34,a[220:223],50
v_rcp_f32 v99,v99
M 34,a[232:235],54
R 42,2,24576
v_fma_f32 v100,v100,v77,v77
M 38,a[224:227],50
v_rcp_f32 v100,v100
M 38,a[236:239],54
L 3
v_fma_f32 v101,v101,v77,v77
v_rcp_f32 v101,v101
M 38,a[248:251],58
R 46,2,25600
v_mul_f32 v22,v22,v98
M 38,v[132:135],62
v_mul_f32 v23,v23,v99
v_mul_f32 v24,v24,v100
M 34,a[244:247],58
v_mul_f32 v25,v25,v101
v_cvt_pk_bf16_f32 v108,v22,v23
M 34,v[128:131],62
R 50,2,26624
v_cvt_pk_bf16_f32 v109,v24,v25
M 30,a[240:243],58
buffer_store_dwordx4 v[106:109],v0,s[36:39],s66 offen offset:0 sc0 sc1
v_fma_f32 v26,-v74,v90,v26
M 30,a[252:255],62
L 3
v_fma_f32 v27,-v74,v91,v27
v_fma_f32 v28,-v74,v92,v28
M 30,v[136:139],66
R 54,2,27648
v_fma_f32 v29,-v74,v93,v29
v_mul_f32 v102,v26,v26
M 30,v[148:151],70
D 0,56,2048
M 34,v[140:143],66
v_mul_f32 v103,v27,v27
v_mul_f32 v104,v28,v28
M 34,v[152:155],70
R 58,2,28672
v_mul_f32 v105,v29,v29
M 38,v[144:147],66
v_fma_f32 v102,v76,v102,v75
v_fma_f32 v103,v76,v103,v75
M 38,v[156:159],70
L 3
v_fma_f32 v104,v76,v104,v75
v_fma_f32 v105,v76,v105,v75
M 38,v[168:171],42
R 62,2,29696
v_mul_f32 v102,v26,v102
M 38,v[180:183],46
v_exp_f32 v102,v102
v_mul_f32 v103,v27,v103
M 34,v[164:167],42
v_exp_f32 v103,v103
M 34,v[176:179],46
R 66,2,30720
v_mul_f32 v104,v28,v104
M 30,v[160:163],42
s_nop 0
v_exp_f32 v104,v104
M 30,v[172:175],46
s_waitcnt vmcnt(4) lgkmcnt(3)
D 0,56,3072
M 30,v[184:187],50
s_barrier
R 70,2,31744
ds_read_b32 v7,v6 offset:1024
M 30,v[196:199],54
buffer_store_dword v4,v3,s[36:39],s70 offen offset:0 sc0 sc1
v_mul_f32 v105,v29,v105
M 34,v[188:191],50
s_mov_b32 s70,s69
R 78,13,2048
v_fma_f32 v102,v102,v77,v77
M 34,v[200:203],54
s_mov_b32 s69,s68
R 42,1,0
v_fma_f32 v103,v103,v77,v77
M 38,v[192:195],50
s_add_u32 s68,s68,s63
v_exp_f32 v105,v105
M 38,v[204:207],54
L 5
v_rcp_f32 v102,v102
M 38,v[216:219],58
L 2
R 46,1,1024
v_fma_f32 v104,v104,v77,v77
M 38,v[228:231],62
v_rcp_f32 v103,v103
M 34,v[212:215],58
v_rcp_f32 v104,v104
v_fma_f32 v105,v105,v77,v77
M 34,v[224:227],62
R 50,1,2048
v_mul_f32 v26,v26,v102
M 30,v[208:211],58
v_rcp_f32 v105,v105
M 30,v[220:223],62
s_nop 0
v_mul_f32 v27,v27,v103
v_mul_f32 v28,v28,v104
M 30,v[232:235],66
R 54,1,3072
v_mul_f32 v29,v29,v105
M 30,v[244:247],70
v_cvt_pk_bf16_f32 v110,v26,v27
v_cvt_pk_bf16_f32 v111,v28,v29
M 34,v[236:239],66
v_cmp_eq_u32 vcc,s53,v7
M 34,v[248:251],70
s_cmp_eq_u32 vcc_lo,-1
R 58,1,4096
v_permlane16_swap_b32 v110,v112
v_permlane16_swap_b32 v111,v113
M 38,v[240:243],66
buffer_store_dwordx4 v[110:113],v10,s[36:39],s66 offen offset:0 sc0 sc1
M 38,v[252:255],70
L 3
s_cbranch_scc1 Lg3_F0w2_%=
s_branch Lg3_L0_%=
Lg3_F0w3_%=:
v_mfma_f32_16x16x32_bf16 v[26:29],a[8:11],v[42:45],0
s_add_u32 m0,s57,0
R 62,1,5120
v_add_f32_dpp v115,v114,v114 quad_perm:[1,0,3,2] row_mask:0xf bank_mask:0xf
M 26,a[20:23],46
s_add_u32 s56,s55,0
buffer_load_dword v114,v11,s[36:39],s73 offen offset:0 sc1
v_mfma_f32_16x16x32_bf16 v[22:25],a[4:7],v[42:45],0
s_add_u32 s58,s58,s61
D 0,56,0
M 22,a[16:19],46
s_mov_b32 s66,s65
R 66,1,6144
v_add_f32_dpp v115,v115,v115 quad_perm:[2,3,0,1] row_mask:0xf bank_mask:0xf
v_mfma_f32_16x16x32_bf16 v[18:21],a[0:3],v[42:45],0
s_add_u32 s65,s65,s62
v_fma_f32 v30,-v78,v82,v30
v_fma_f32 v31,-v78,v83,v31
M 18,a[12:15],46
L 3
v_add_f32_dpp v118,v115,v115 row_half_mirror row_mask:0xf bank_mask:0xf
v_fma_f32 v32,-v78,v84,v32
v_fma_f32 v33,-v78,v85,v33
M 18,a[24:27],50
s_add_u32 s54,s54,s46
R 70,1,7168
v_mul_f32 v94,v30,v30
M 18,a[36:39],54
s_add_u32 s73,s73,s64
v_mov_b32_dpp v115,v118 row_ror:8 row_mask:0xf bank_mask:0xf
v_fma_f32 v115,-v118,v118,v115
M 22,a[28:31],50
v_max_f32 v115,s87,v115
v_mul_f32 v95,v31,v31
M 22,a[40:43],54
R 42,1,8192
v_mul_f32 v96,v32,v32
M 26,a[32:35],50
v_rsq_f32 v116,v115
M 26,a[44:47],54
L 3
D 0,56,1024
v_mul_f32 v121,v115,v116
M 26,a[56:59],58
R 46,1,9216
v_mul_f32 v119,s88,v116
M 26,a[68:71],62
v_mul_f32 v115,s89,v116
v_mul_f32 v120,v119,v116
M 22,a[52:55],58
v_mul_f32 v120,v120,v115
v_mul_f32 v97,v33,v33
M 22,a[64:67],62
R 50,1,10240
v_fma_f32 v94,v80,v94,v79
M 18,a[48:51],58
ds_write_b128 v12,v[118:121] offset:0
M 18,a[60:63],62
L 4
v_fma_f32 v95,v80,v95,v79
v_fma_f32 v96,v80,v96,v79
v_fma_f32 v97,v80,v97,v79
M 18,a[72:75],66
R 54,1,11264
v_mul_f32 v94,v30,v94
M 18,a[84:87],70
v_exp_f32 v94,v94
M 22,a[76:79],66
v_mul_f32 v95,v31,v95
v_mul_f32 v96,v32,v96
M 22,a[88:91],70
R 58,1,12288
v_mul_f32 v97,v33,v97
M 26,a[80:83],66
D 0,56,2048
v_fma_f32 v94,v94,v81,v81
M 26,a[92:95],70
L 4
v_exp_f32 v95,v95
M 26,a[104:107],42
R 62,1,13312
v_fma_f32 v95,v95,v81,v81
M 26,a[116:119],46
v_exp_f32 v96,v96
M 22,a[100:103],42
v_exp_f32 v97,v97
M 22,a[112:115],46
R 66,1,14336
v_fma_f32 v96,v96,v81,v81
M 18,a[96:99],42
s_waitcnt vmcnt(5)
v_rcp_f32 v94,v94
v_fma_f32 v97,v97,v81,v81
M 18,a[108:111],46
s_barrier
L 3
v_mul_f32 v30,v30,v94
M 18,a[120:123],50
R 70,1,15360
v_fma_f32 v34,-v78,v86,v34
M 18,a[132:135],54
D 0,56,3072
M 22,a[124:127],50
s_add_u32 m0,s59,0
v_rcp_f32 v95,v95
M 22,a[136:139],54
s_add_u32 s56,s55,16384
R 42,1,16384
v_mul_f32 v31,v31,v95
M 26,a[128:131],50
s_add_u32 s55,s55,s60
v_rcp_f32 v96,v96
v_cvt_pk_bf16_f32 v106,v30,v31
M 26,a[140:143],54
L 3
v_rcp_f32 v97,v97
M 26,a[152:155],58
R 46,1,17408
v_mul_f32 v32,v32,v96
M 26,a[164:167],62
v_mul_f32 v33,v33,v97
v_cvt_pk_bf16_f32 v107,v32,v33
M 22,a[148:151],58
v_fma_f32 v35,-v78,v87,v35
v_fma_f32 v36,-v78,v88,v36
M 22,a[160:163],62
R 50,1,18432
v_fma_f32 v37,-v78,v89,v37
v_mul_f32 v98,v34,v34
M 18,a[144:147],58
buffer_load_dword v8,s[36:39],s58 offen offset:0 sc1 lds
M 18,a[156:159],62
L 3
v_mul_f32 v99,v35,v35
v_mul_f32 v100,v36,v36
M 18,a[168:171],66
s_add_u32 m0,s57,16384
R 54,1,19456
v_mul_f32 v101,v37,v37
M 18,a[180:183],70
s_sub_u32 s57,s84,s57
v_fma_f32 v98,v80,v98,v79
v_fma_f32 v99,v80,v99,v79
M 22,a[172:175],66
v_fma_f32 v100,v80,v100,v79
v_fma_f32 v101,v80,v101,v79
M 22,a[184:187],70
R 58,1,20480
v_mul_f32 v98,v34,v98
v_mul_f32 v99,v35,v99
M 26,a[176:179],66
D 0,56,0
M 26,a[188:191],70
L 3
v_exp_f32 v98,v98
M 26,a[200:203],42
R 62,1,21504
v_mul_f32 v100,v36,v100
M 26,a[212:215],46
v_exp_f32 v99,v99
M 22,a[196:199],42
v_exp_f32 v100,v100
M 22,a[208:211],46
R 66,1,22528
v_mul_f32 v101,v37,v101
v_fma_f32 v98,v98,v81,v81
M 18,a[192:195],42
v_exp_f32 v101,v101
M 18,a[204:207],46
L 3
v_rcp_f32 v98,v98
M 18,a[216:219],50
R 70,1,23552
v_fma_f32 v99,v99,v81,v81
M 18,a[228:231],54
D 0,56,1024
M 22,a[220:223],50
v_rcp_f32 v99,v99
M 22,a[232:235],54
R 42,1,24576
v_fma_f32 v100,v100,v81,v81
v_fma_f32 v101,v101,v81,v81
M 26,a[224:227],50
v_rcp_f32 v100,v100
M 26,a[236:239],54
L 3
v_rcp_f32 v101,v101
M 26,a[248:251],58
R 46,1,25600
v_mul_f32 v34,v34,v98
M 26,v[132:135],62
v_mul_f32 v35,v35,v99
v_mul_f32 v36,v36,v100
M 22,a[244:247],58
v_mul_f32 v37,v37,v101
v_cvt_pk_bf16_f32 v108,v34,v35
v_cvt_pk_bf16_f32 v109,v36,v37
M 22,v[128:131],62
R 50,1,26624
v_fma_f32 v38,-v78,v90,v38
M 18,a[240:243],58
buffer_store_dwordx4 v[106:109],v0,s[36:39],s67 offen offset:0 sc0 sc1
v_fma_f32 v39,-v78,v91,v39
M 18,a[252:255],62
L 3
v_fma_f32 v40,-v78,v92,v40
v_fma_f32 v41,-v78,v93,v41
M 18,v[136:139],66
R 54,1,27648
v_mul_f32 v102,v38,v38
M 18,v[148:151],70
D 0,56,2048
M 22,v[140:143],66
v_mul_f32 v103,v39,v39
v_mul_f32 v104,v40,v40
v_mul_f32 v105,v41,v41
M 22,v[152:155],70
R 58,1,28672
v_fma_f32 v102,v80,v102,v79
M 26,v[144:147],66
v_fma_f32 v103,v80,v103,v79
v_fma_f32 v104,v80,v104,v79
M 26,v[156:159],70
L 3
v_fma_f32 v105,v80,v105,v79
v_mul_f32 v102,v38,v102
M 26,v[168:171],42
R 62,1,29696
v_mul_f32 v103,v39,v103
M 26,v[180:183],46
v_exp_f32 v102,v102
M 22,v[164:167],42
v_exp_f32 v103,v103
v_mul_f32 v104,v40,v104
M 22,v[176:179],46
s_nop 0
R 66,1,30720
v_mul_f32 v105,v41,v105
M 18,v[160:163],42
s_waitcnt vmcnt(4)
v_exp_f32 v104,v104
M 18,v[172:175],46
L 3
s_barrier
buffer_store_dword v4,v3,s[36:39],s70 offen offset:0 sc0 sc1
M 18,v[184:187],50
s_mov_b32 s70,s69
R 70,1,31744
ds_read_b32 v7,v6 offset:0
M 18,v[196:199],54
s_mov_b32 s69,s68
D 0,56,3072
v_fma_f32 v102,v102,v81,v81
M 22,v[188:191],50
s_add_u32 s68,s68,s63
R 74,13,0
v_fma_f32 v103,v103,v81,v81
M 22,v[200:203],54
R 42,2,0
v_fma_f32 v104,v104,v81,v81
M 26,v[192:195],50
v_exp_f32 v105,v105
M 26,v[204:207],54
L 5
v_rcp_f32 v102,v102
M 26,v[216:219],58
L 2
R 46,2,1024
v_fma_f32 v105,v105,v81,v81
M 26,v[228:231],62
v_rcp_f32 v103,v103
v_mul_f32 v38,v38,v102
M 22,v[212:215],58
v_rcp_f32 v104,v104
M 22,v[224:227],62
R 50,2,2048
v_mul_f32 v39,v39,v103
M 18,v[208:211],58
v_rcp_f32 v105,v105
M 18,v[220:223],62
s_nop 0
v_mul_f32 v40,v40,v104
)"
R"(v_mul_f32 v41,v41,v105
M 18,v[232:235],66
R 54,2,3072
v_cvt_pk_bf16_f32 v110,v38,v39
M 18,v[244:247],70
v_cvt_pk_bf16_f32 v111,v40,v41
v_cmp_eq_u32 vcc,s53,v7
M 22,v[236:239],66
s_cmp_eq_u32 vcc_lo,-1
v_permlane16_swap_b32 v110,v112
M 22,v[248:251],70
R 58,2,4096
v_permlane16_swap_b32 v111,v113
M 26,v[240:243],66
buffer_store_dwordx4 v[110:113],v10,s[36:39],s67 offen offset:0 sc0 sc1
M 26,v[252:255],70
L 3
s_cbranch_scc0 Lg3_L1_%=
Lg3_F1w3_%=:
v_mfma_f32_16x16x32_bf16 v[38:41],a[8:11],v[42:45],0
s_add_u32 m0,s57,0
R 62,2,5120
v_add_f32_dpp v115,v114,v114 quad_perm:[1,0,3,2] row_mask:0xf bank_mask:0xf
M 38,a[20:23],46
s_add_u32 s56,s55,0
buffer_load_dword v114,v11,s[36:39],s73 offen offset:0 sc1
v_mfma_f32_16x16x32_bf16 v[34:37],a[4:7],v[42:45],0
s_add_u32 s58,s58,s61
D 0,56,0
M 34,a[16:19],46
s_mov_b32 s67,s65
R 66,2,6144
v_add_f32_dpp v115,v115,v115 quad_perm:[2,3,0,1] row_mask:0xf bank_mask:0xf
v_mfma_f32_16x16x32_bf16 v[30:33],a[0:3],v[42:45],0
s_add_u32 s65,s65,s62
v_fma_f32 v18,-v74,v82,v18
v_fma_f32 v19,-v74,v83,v19
M 30,a[12:15],46
L 3
v_add_f32_dpp v118,v115,v115 row_half_mirror row_mask:0xf bank_mask:0xf
v_fma_f32 v20,-v74,v84,v20
v_fma_f32 v21,-v74,v85,v21
M 30,a[24:27],50
s_add_u32 s54,s54,s46
R 70,2,7168
v_mul_f32 v94,v18,v18
M 30,a[36:39],54
s_add_u32 s73,s73,s64
v_mov_b32_dpp v115,v118 row_ror:8 row_mask:0xf bank_mask:0xf
v_fma_f32 v115,-v118,v118,v115
M 34,a[28:31],50
v_max_f32 v115,s87,v115
v_mul_f32 v95,v19,v19
M 34,a[40:43],54
R 42,2,8192
v_mul_f32 v96,v20,v20
M 38,a[32:35],50
v_rsq_f32 v116,v115
M 38,a[44:47],54
L 3
D 0,56,1024
v_mul_f32 v121,v115,v116
M 38,a[56:59],58
R 46,2,9216
v_mul_f32 v119,s88,v116
M 38,a[68:71],62
v_mul_f32 v115,s89,v116
v_mul_f32 v120,v119,v116
M 34,a[52:55],58
v_mul_f32 v120,v120,v115
v_mul_f32 v97,v21,v21
M 34,a[64:67],62
R 50,2,10240
v_fma_f32 v94,v76,v94,v75
M 30,a[48:51],58
ds_write_b128 v12,v[118:121] offset:2048
M 30,a[60:63],62
L 4
v_fma_f32 v95,v76,v95,v75
v_fma_f32 v96,v76,v96,v75
v_fma_f32 v97,v76,v97,v75
M 30,a[72:75],66
R 54,2,11264
v_mul_f32 v94,v18,v94
M 30,a[84:87],70
v_exp_f32 v94,v94
M 34,a[76:79],66
v_mul_f32 v95,v19,v95
v_mul_f32 v96,v20,v96
M 34,a[88:91],70
R 58,2,12288
v_mul_f32 v97,v21,v97
M 38,a[80:83],66
D 0,56,2048
v_fma_f32 v94,v94,v77,v77
M 38,a[92:95],70
L 4
v_exp_f32 v95,v95
M 38,a[104:107],42
R 62,2,13312
v_fma_f32 v95,v95,v77,v77
M 38,a[116:119],46
v_exp_f32 v96,v96
M 34,a[100:103],42
v_exp_f32 v97,v97
M 34,a[112:115],46
R 66,2,14336
v_fma_f32 v96,v96,v77,v77
M 30,a[96:99],42
s_waitcnt vmcnt(5)
v_rcp_f32 v94,v94
v_fma_f32 v97,v97,v77,v77
M 30,a[108:111],46
s_barrier
L 3
v_mul_f32 v18,v18,v94
M 30,a[120:123],50
R 70,2,15360
v_fma_f32 v22,-v74,v86,v22
M 30,a[132:135],54
D 0,56,3072
M 34,a[124:127],50
s_add_u32 m0,s59,1024
v_rcp_f32 v95,v95
M 34,a[136:139],54
s_add_u32 s56,s55,16384
R 42,2,16384
v_mul_f32 v19,v19,v95
M 38,a[128:131],50
s_add_u32 s55,s55,s60
v_rcp_f32 v96,v96
v_cvt_pk_bf16_f32 v106,v18,v19
M 38,a[140:143],54
L 3
v_rcp_f32 v97,v97
M 38,a[152:155],58
R 46,2,17408
v_mul_f32 v20,v20,v96
M 38,a[164:167],62
v_mul_f32 v21,v21,v97
v_cvt_pk_bf16_f32 v107,v20,v21
M 34,a[148:151],58
v_fma_f32 v23,-v74,v87,v23
v_fma_f32 v24,-v74,v88,v24
M 34,a[160:163],62
R 50,2,18432
v_fma_f32 v25,-v74,v89,v25
v_mul_f32 v98,v22,v22
M 30,a[144:147],58
buffer_load_dword v8,s[36:39],s58 offen offset:0 sc1 lds
M 30,a[156:159],62
L 3
v_mul_f32 v99,v23,v23
v_mul_f32 v100,v24,v24
M 30,a[168:171],66
s_add_u32 m0,s57,16384
R 54,2,19456
v_mul_f32 v101,v25,v25
M 30,a[180:183],70
s_sub_u32 s57,s84,s57
v_fma_f32 v98,v76,v98,v75
v_fma_f32 v99,v76,v99,v75
M 34,a[172:175],66
v_fma_f32 v100,v76,v100,v75
v_fma_f32 v101,v76,v101,v75
M 34,a[184:187],70
R 58,2,20480
v_mul_f32 v98,v22,v98
v_mul_f32 v99,v23,v99
M 38,a[176:179],66
D 0,56,0
M 38,a[188:191],70
L 3
v_exp_f32 v98,v98
M 38,a[200:203],42
R 62,2,21504
v_mul_f32 v100,v24,v100
M 38,a[212:215],46
v_exp_f32 v99,v99
M 34,a[196:199],42
v_exp_f32 v100,v100
M 34,a[208:211],46
R 66,2,22528
v_mul_f32 v101,v25,v101
v_fma_f32 v98,v98,v77,v77
M 30,a[192:195],42
v_exp_f32 v101,v101
M 30,a[204:207],46
L 3
v_rcp_f32 v98,v98
M 30,a[216:219],50
R 70,2,23552
v_fma_f32 v99,v99,v77,v77
M 30,a[228:231],54
D 0,56,1024
M 34,a[220:223],50
v_rcp_f32 v99,v99
M 34,a[232:235],54
R 42,2,24576
v_fma_f32 v100,v100,v77,v77
v_fma_f32 v101,v101,v77,v77
M 38,a[224:227],50
v_rcp_f32 v100,v100
M 38,a[236:239],54
L 3
v_rcp_f32 v101,v101
M 38,a[248:251],58
R 46,2,25600
v_mul_f32 v22,v22,v98
M 38,v[132:135],62
v_mul_f32 v23,v23,v99
v_mul_f32 v24,v24,v100
M 34,a[244:247],58
v_mul_f32 v25,v25,v101
v_cvt_pk_bf16_f32 v108,v22,v23
v_cvt_pk_bf16_f32 v109,v24,v25
M 34,v[128:131],62
R 50,2,26624
v_fma_f32 v26,-v74,v90,v26
M 30,a[240:243],58
buffer_store_dwordx4 v[106:109],v0,s[36:39],s66 offen offset:0 sc0 sc1
v_fma_f32 v27,-v74,v91,v27
M 30,a[252:255],62
L 3
v_fma_f32 v28,-v74,v92,v28
v_fma_f32 v29,-v74,v93,v29
M 30,v[136:139],66
R 54,2,27648
v_mul_f32 v102,v26,v26
M 30,v[148:151],70
D 0,56,2048
M 34,v[140:143],66
v_mul_f32 v103,v27,v27
v_mul_f32 v104,v28,v28
v_mul_f32 v105,v29,v29
M 34,v[152:155],70
R 58,2,28672
v_fma_f32 v102,v76,v102,v75
M 38,v[144:147],66
v_fma_f32 v103,v76,v103,v75
v_fma_f32 v104,v76,v104,v75
M 38,v[156:159],70
L 3
v_fma_f32 v105,v76,v105,v75
v_mul_f32 v102,v26,v102
M 38,v[168:171],42
R 62,2,29696
v_mul_f32 v103,v27,v103
M 38,v[180:183],46
v_exp_f32 v102,v102
M 34,v[164:167],42
v_exp_f32 v103,v103
v_mul_f32 v104,v28,v104
M 34,v[176:179],46
s_nop 0
R 66,2,30720
v_mul_f32 v105,v29,v105
M 30,v[160:163],42
s_waitcnt vmcnt(4)
v_exp_f32 v104,v104
M 30,v[172:175],46
L 3
s_barrier
buffer_store_dword v4,v3,s[36:39],s70 offen offset:0 sc0 sc1
M 30,v[184:187],50
s_mov_b32 s70,s69
R 70,2,31744
ds_read_b32 v7,v6 offset:1024
M 30,v[196:199],54
s_mov_b32 s69,s68
D 0,56,3072
v_fma_f32 v102,v102,v77,v77
M 34,v[188:191],50
s_add_u32 s68,s68,s63
R 78,13,2048
v_fma_f32 v103,v103,v77,v77
M 34,v[200:203],54
R 42,1,0
v_fma_f32 v104,v104,v77,v77
M 38,v[192:195],50
v_exp_f32 v105,v105
M 38,v[204:207],54
L 5
v_rcp_f32 v102,v102
M 38,v[216:219],58
L 2
R 46,1,1024
v_fma_f32 v105,v105,v77,v77
M 38,v[228:231],62
v_rcp_f32 v103,v103
v_mul_f32 v26,v26,v102
M 34,v[212:215],58
v_rcp_f32 v104,v104
M 34,v[224:227],62
R 50,1,2048
v_mul_f32 v27,v27,v103
M 30,v[208:211],58
v_rcp_f32 v105,v105
M 30,v[220:223],62
s_nop 0
v_mul_f32 v28,v28,v104
v_mul_f32 v29,v29,v105
M 30,v[232:235],66
R 54,1,3072
v_cvt_pk_bf16_f32 v110,v26,v27
M 30,v[244:247],70
v_cvt_pk_bf16_f32 v111,v28,v29
v_cmp_eq_u32 vcc,s53,v7
M 34,v[236:239],66
s_cmp_eq_u32 vcc_lo,-1
v_permlane16_swap_b32 v110,v112
M 34,v[248:251],70
R 58,1,4096
v_permlane16_swap_b32 v111,v113
M 38,v[240:243],66
buffer_store_dwordx4 v[110:113],v10,s[36:39],s66 offen offset:0 sc0 sc1
M 38,v[252:255],70
L 3
s_cbranch_scc1 Lg3_F0w3_%=
Lg3_L0_%=:
v_mfma_f32_16x16x32_bf16 v[26:29],a[8:11],v[42:45],0
s_add_u32 m0,s59,3072
R 62,1,5120
v_add_f32_dpp v115,v114,v114 quad_perm:[1,0,3,2] row_mask:0xf bank_mask:0xf
M 26,a[20:23],46
s_mov_b32 s66,s65
buffer_load_dword v8,s[36:39],s58 offen offset:0 sc1 lds
v_mfma_f32_16x16x32_bf16 v[22:25],a[4:7],v[42:45],0
s_add_u32 s65,s65,s62
v_add_f32_dpp v115,v115,v115 quad_perm:[2,3,0,1] row_mask:0xf bank_mask:0xf
v_fma_f32 v30,-v78,v82,v30
M 22,a[16:19],46
R 66,1,6144
v_fma_f32 v31,-v78,v83,v31
v_mfma_f32_16x16x32_bf16 v[18:21],a[0:3],v[42:45],0
v_add_f32_dpp v118,v115,v115 row_half_mirror row_mask:0xf bank_mask:0xf
v_fma_f32 v32,-v78,v84,v32
v_fma_f32 v33,-v78,v85,v33
M 18,a[12:15],46
L 3
v_mul_f32 v94,v30,v30
v_mul_f32 v95,v31,v31
M 18,a[24:27],50
R 70,1,7168
v_mov_b32_dpp v115,v118 row_ror:8 row_mask:0xf bank_mask:0xf
M 18,a[36:39],54
v_fma_f32 v115,-v118,v118,v115
v_max_f32 v115,s87,v115
M 22,a[28:31],50
v_rsq_f32 v116,v115
v_mul_f32 v96,v32,v32
M 22,a[40:43],54
R 42,1,8192
v_mul_f32 v121,v115,v116
M 26,a[32:35],50
v_mul_f32 v119,s88,v116
v_mul_f32 v115,s89,v116
M 26,a[44:47],54
L 3
v_mul_f32 v120,v119,v116
v_mul_f32 v120,v120,v115
M 26,a[56:59],58
R 46,1,9216
v_mul_f32 v97,v33,v33
M 26,a[68:71],62
ds_write_b128 v12,v[118:121] offset:0
M 22,a[52:55],58
v_fma_f32 v94,v80,v94,v79
v_fma_f32 v95,v80,v95,v79
M 22,a[64:67],62
R 50,1,10240
v_fma_f32 v96,v80,v96,v79
M 18,a[48:51],58
v_fma_f32 v97,v80,v97,v79
v_mul_f32 v94,v30,v94
M 18,a[60:63],62
L 4
v_exp_f32 v94,v94
v_mul_f32 v95,v31,v95
M 18,a[72:75],66
R 54,1,11264
v_mul_f32 v96,v32,v96
M 18,a[84:87],70
v_exp_f32 v95,v95
M 22,a[76:79],66
v_exp_f32 v96,v96
M 22,a[88:91],70
R 58,1,12288
v_mul_f32 v97,v33,v97
M 26,a[80:83],66
v_exp_f32 v97,v97
v_fma_f32 v94,v94,v81,v81
M 26,a[92:95],70
L 4
v_rcp_f32 v94,v94
M 26,a[104:107],42
R 62,1,13312
v_fma_f32 v95,v95,v81,v81
M 26,a[116:119],46
v_rcp_f32 v95,v95
M 22,a[100:103],42
v_fma_f32 v96,v96,v81,v81
v_rcp_f32 v96,v96
M 22,a[112:115],46
R 66,1,14336
v_fma_f32 v97,v97,v81,v81
M 18,a[96:99],42
v_rcp_f32 v97,v97
M 18,a[108:111],46
L 3
v_mul_f32 v30,v30,v94
v_mul_f32 v31,v31,v95
M 18,a[120:123],50
R 70,1,15360
v_mul_f32 v32,v32,v96
M 18,a[132:135],54
s_waitcnt vmcnt(0)
v_mul_f32 v33,v33,v97
v_cvt_pk_bf16_f32 v106,v30,v31
v_cvt_pk_bf16_f32 v107,v32,v33
M 22,a[124:127],50
s_barrier
ds_read_b32 v7,v6 offset:3072
v_fma_f32 v34,-v78,v86,v34
M 22,a[136:139],54
R 42,1,16384
v_fma_f32 v35,-v78,v87,v35
M 26,a[128:131],50
buffer_store_dword v4,v3,s[36:39],s70 offen offset:0 sc0 sc1
M 26,a[140:143],54
L 4
v_fma_f32 v36,-v78,v88,v36
v_fma_f32 v37,-v78,v89,v37
v_mul_f32 v98,v34,v34
M 26,a[152:155],58
s_mov_b32 s70,0x7fff0000
R 46,1,17408
v_mul_f32 v99,v35,v35
M 26,a[164:167],62
v_mul_f32 v100,v36,v36
v_mul_f32 v101,v37,v37
M 22,a[148:151],58
L 2
v_cmp_eq_u32 vcc,s53,v7
v_fma_f32 v98,v80,v98,v79
M 22,a[160:163],62
R 50,1,18432
v_fma_f32 v99,v80,v99,v79
M 18,a[144:147],58
s_cmp_eq_u32 vcc_lo,-1
v_fma_f32 v100,v80,v100,v79
v_fma_f32 v101,v80,v101,v79
v_mul_f32 v98,v34,v98
s_cbranch_scc0 Lg3_X0_%=
M 18,a[156:159],62
s_nop 0
s_add_u32 m0,s57,0
s_add_u32 s56,s55,0
buffer_load_dword v114,v11,s[36:39],s73 offen offset:0 sc1
M 18,a[168:171],66
s_add_u32 s58,s58,s61
R 54,1,19456
v_mul_f32 v99,v35,v99
M 18,a[180:183],70
s_add_u32 s54,s54,s46
D 0,56,0
M 22,a[172:175],66
s_add_u32 s73,s73,s64
v_exp_f32 v98,v98
v_mul_f32 v100,v36,v100
M 22,a[184:187],70
R 58,1,20480
v_mul_f32 v101,v37,v101
M 26,a[176:179],66
v_exp_f32 v99,v99
M 26,a[188:191],70
L 3
D 0,56,1024
M 26,a[200:203],42
R 62,1,21504
v_exp_f32 v100,v100
M 26,a[212:215],46
v_exp_f32 v101,v101
M 22,a[196:199],42
v_fma_f32 v98,v98,v81,v81
v_fma_f32 v99,v99,v81,v81
M 22,a[208:211],46
R 66,1,22528
v_fma_f32 v100,v100,v81,v81
M 18,a[192:195],42
D 0,56,2048
M 18,a[204:207],46
L 3
v_rcp_f32 v98,v98
v_fma_f32 v101,v101,v81,v81
M 18,a[216:219],50
R 70,1,23552
v_mul_f32 v34,v34,v98
M 18,a[228:231],54
D 0,56,3072
)"
R"(M 22,a[220:223],50
s_add_u32 m0,s59,0
v_rcp_f32 v99,v99
M 22,a[232:235],54
s_add_u32 s56,s55,16384
R 42,1,24576
v_rcp_f32 v100,v100
M 26,a[224:227],50
s_add_u32 s55,s55,s60
buffer_load_dword v8,s[36:39],s58 offen offset:0 sc1 lds
M 26,a[236:239],54
L 3
v_rcp_f32 v101,v101
M 26,a[248:251],58
s_add_u32 m0,s57,16384
R 46,1,25600
v_mul_f32 v35,v35,v99
M 26,v[132:135],62
s_sub_u32 s57,s84,s57
D 0,56,0
M 22,a[244:247],58
v_mul_f32 v36,v36,v100
v_mul_f32 v37,v37,v101
v_cvt_pk_bf16_f32 v108,v34,v35
M 22,v[128:131],62
R 50,1,26624
v_cvt_pk_bf16_f32 v109,v36,v37
M 18,a[240:243],58
buffer_store_dwordx4 v[106:109],v0,s[36:39],s67 offen offset:0 sc0 sc1
v_fma_f32 v38,-v78,v90,v38
M 18,a[252:255],62
L 3
v_fma_f32 v39,-v78,v91,v39
v_fma_f32 v40,-v78,v92,v40
M 18,v[136:139],66
R 54,1,27648
v_fma_f32 v41,-v78,v93,v41
v_mul_f32 v102,v38,v38
M 18,v[148:151],70
D 0,56,1024
M 22,v[140:143],66
v_mul_f32 v103,v39,v39
v_mul_f32 v104,v40,v40
M 22,v[152:155],70
R 58,1,28672
v_mul_f32 v105,v41,v41
M 26,v[144:147],66
v_fma_f32 v102,v80,v102,v79
v_fma_f32 v103,v80,v103,v79
M 26,v[156:159],70
L 3
D 0,56,2048
v_fma_f32 v104,v80,v104,v79
M 26,v[168:171],42
R 62,1,29696
v_fma_f32 v105,v80,v105,v79
M 26,v[180:183],46
v_mul_f32 v102,v38,v102
v_mul_f32 v103,v39,v103
M 22,v[164:167],42
v_exp_f32 v102,v102
M 22,v[176:179],46
R 66,1,30720
v_exp_f32 v103,v103
M 18,v[160:163],42
D 0,56,3072
M 18,v[172:175],46
L 3
v_mul_f32 v104,v40,v104
v_mul_f32 v105,v41,v105
M 18,v[184:187],50
s_nop 0
R 70,1,31744
v_fma_f32 v102,v102,v81,v81
M 18,v[196:199],54
s_waitcnt vmcnt(5)
v_exp_f32 v104,v104
M 22,v[188:191],50
s_barrier
buffer_store_dword v4,v3,s[36:39],s70 offen offset:0 sc0 sc1
v_fma_f32 v103,v103,v81,v81
M 22,v[200:203],54
s_mov_b32 s70,s69
R 42,2,0
ds_read_b32 v7,v6 offset:0
M 26,v[192:195],50
s_mov_b32 s69,s68
R 74,13,0
v_fma_f32 v104,v104,v81,v81
M 26,v[204:207],54
L 5
v_exp_f32 v105,v105
M 26,v[216:219],58
s_add_u32 s68,s68,s63
R 46,2,1024
v_rcp_f32 v102,v102
M 26,v[228:231],62
v_rcp_f32 v103,v103
M 22,v[212:215],58
v_rcp_f32 v104,v104
M 22,v[224:227],62
L 2
R 50,2,2048
v_fma_f32 v105,v105,v81,v81
M 18,v[208:211],58
v_rcp_f32 v105,v105
v_mul_f32 v38,v38,v102
M 18,v[220:223],62
s_nop 0
v_mul_f32 v39,v39,v103
v_mul_f32 v40,v40,v104
M 18,v[232:235],66
R 54,2,3072
v_mul_f32 v41,v41,v105
M 18,v[244:247],70
v_cvt_pk_bf16_f32 v110,v38,v39
v_cvt_pk_bf16_f32 v111,v40,v41
M 22,v[236:239],66
v_cmp_eq_u32 vcc,s53,v7
M 22,v[248:251],70
s_cmp_eq_u32 vcc_lo,-1
R 58,2,4096
v_permlane16_swap_b32 v110,v112
v_permlane16_swap_b32 v111,v113
M 26,v[240:243],66
buffer_store_dwordx4 v[110:113],v10,s[36:39],s67 offen offset:0 sc0 sc1
M 26,v[252:255],70
L 3
s_cbranch_scc1 Lg3_F1_%=
s_branch Lg3_L1_%=
Lg3_X0_%=:
M 18,a[156:159],62
s_nop 0
s_add_u32 m0,s59,0
v_mul_f32 v99,v35,v99
M 18,a[168:171],66
R 54,1,19456
v_mul_f32 v100,v36,v100
M 18,a[180:183],70
buffer_load_dword v8,s[36:39],s58 offen offset:0 sc1 lds
M 22,a[172:175],66
v_exp_f32 v98,v98
M 22,a[184:187],70
R 58,1,20480
v_mul_f32 v101,v37,v101
M 26,a[176:179],66
v_exp_f32 v99,v99
M 26,a[188:191],70
L 3
v_exp_f32 v100,v100
M 26,a[200:203],42
R 62,1,21504
v_fma_f32 v98,v98,v81,v81
M 26,a[212:215],46
v_exp_f32 v101,v101
M 22,a[196:199],42
v_rcp_f32 v98,v98
M 22,a[208:211],46
R 66,1,22528
v_fma_f32 v99,v99,v81,v81
M 18,a[192:195],42
v_rcp_f32 v99,v99
M 18,a[204:207],46
L 3
v_fma_f32 v100,v100,v81,v81
v_fma_f32 v101,v101,v81,v81
M 18,a[216:219],50
R 70,1,23552
v_mul_f32 v34,v34,v98
M 18,a[228:231],54
v_rcp_f32 v100,v100
M 22,a[220:223],50
v_rcp_f32 v101,v101
M 22,a[232:235],54
R 42,1,24576
v_mul_f32 v35,v35,v99
M 26,a[224:227],50
v_mul_f32 v36,v36,v100
v_mul_f32 v37,v37,v101
M 26,a[236:239],54
L 3
v_cvt_pk_bf16_f32 v108,v34,v35
v_cvt_pk_bf16_f32 v109,v36,v37
M 26,a[248:251],58
R 46,1,25600
v_fma_f32 v38,-v78,v90,v38
M 26,v[132:135],62
buffer_store_dwordx4 v[106:109],v0,s[36:39],s67 offen offset:0 sc0 sc1
v_fma_f32 v39,-v78,v91,v39
M 22,a[244:247],58
v_fma_f32 v40,-v78,v92,v40
v_fma_f32 v41,-v78,v93,v41
M 22,v[128:131],62
R 50,1,26624
v_mul_f32 v102,v38,v38
M 18,a[240:243],58
v_mul_f32 v103,v39,v39
v_mul_f32 v104,v40,v40
M 18,a[252:255],62
L 3
v_mul_f32 v105,v41,v41
v_fma_f32 v102,v80,v102,v79
M 18,v[136:139],66
R 54,1,27648
v_fma_f32 v103,v80,v103,v79
M 18,v[148:151],70
v_fma_f32 v104,v80,v104,v79
v_fma_f32 v105,v80,v105,v79
M 22,v[140:143],66
v_mul_f32 v102,v38,v102
v_mul_f32 v103,v39,v103
M 22,v[152:155],70
R 58,1,28672
v_mul_f32 v104,v40,v104
M 26,v[144:147],66
v_exp_f32 v102,v102
M 26,v[156:159],70
L 3
v_exp_f32 v103,v103
M 26,v[168:171],42
R 62,1,29696
v_mul_f32 v105,v41,v105
M 26,v[180:183],46
v_exp_f32 v104,v104
M 22,v[164:167],42
v_exp_f32 v105,v105
M 22,v[176:179],46
R 66,1,30720
v_fma_f32 v102,v102,v81,v81
M 18,v[160:163],42
v_rcp_f32 v102,v102
M 18,v[172:175],46
L 3
v_fma_f32 v103,v103,v81,v81
v_fma_f32 v104,v104,v81,v81
M 18,v[184:187],50
s_nop 0
R 70,1,31744
v_fma_f32 v105,v105,v81,v81
M 18,v[196:199],54
s_waitcnt vmcnt(1)
v_rcp_f32 v103,v103
M 22,v[188:191],50
s_barrier
buffer_store_dword v4,v3,s[36:39],s70 offen offset:0 sc0 sc1
M 22,v[200:203],54
s_mov_b32 s70,s69
ds_read_b32 v7,v6 offset:0
R 74,13,0
M 26,v[192:195],50
s_mov_b32 s69,s68
v_rcp_f32 v104,v104
M 26,v[204:207],54
L 4
v_rcp_f32 v105,v105
M 26,v[216:219],58
s_add_u32 s68,s68,s63
v_mul_f32 v38,v38,v102
v_mul_f32 v39,v39,v103
M 26,v[228:231],62
v_mul_f32 v40,v40,v104
v_mul_f32 v41,v41,v105
M 22,v[212:215],58
v_cvt_pk_bf16_f32 v110,v38,v39
v_cvt_pk_bf16_f32 v111,v40,v41
M 22,v[224:227],62
L 1
v_cmp_eq_u32 vcc,s53,v7
M 18,v[208:211],58
v_permlane16_swap_b32 v110,v112
M 18,v[220:223],62
s_nop 0
v_permlane16_swap_b32 v111,v113
M 18,v[232:235],66
buffer_store_dwordx4 v[110:113],v10,s[36:39],s67 offen offset:0 sc0 sc1
M 18,v[244:247],70
M 22,v[236:239],66
M 22,v[248:251],70
M 26,v[240:243],66
M 26,v[252:255],70
s_mov_b32 s80,1
s_cmp_eq_u32 vcc_lo,-1
s_cbranch_scc0 Lg3_xn0_%=
buffer_load_dword v114,v11,s[36:39],s73 offen offset:0 sc1
L 0
v_fma_f32 v18,-v74,v82,v18
v_fma_f32 v19,-v74,v83,v19
v_fma_f32 v20,-v74,v84,v20
v_fma_f32 v21,-v74,v85,v21
v_mul_f32 v94,v18,v18
v_mul_f32 v95,v19,v19
v_mul_f32 v96,v20,v20
v_mul_f32 v97,v21,v21
s_add_u32 s73,s73,s64
s_add_u32 m0,s57,0
s_add_u32 s56,s55,0
D 0,56,0
v_fma_f32 v94,v76,v94,v75
v_fma_f32 v95,v76,v95,v75
v_fma_f32 v96,v76,v96,v75
v_fma_f32 v97,v76,v97,v75
v_mul_f32 v94,v18,v94
v_mul_f32 v95,v19,v95
v_mul_f32 v96,v20,v96
v_mul_f32 v97,v21,v97
D 0,56,1024
v_exp_f32 v94,v94
v_exp_f32 v95,v95
v_exp_f32 v96,v96
v_exp_f32 v97,v97
v_fma_f32 v94,v94,v77,v77
v_fma_f32 v95,v95,v77,v77
v_fma_f32 v96,v96,v77,v77
v_fma_f32 v97,v97,v77,v77
D 0,56,2048
v_rcp_f32 v94,v94
v_rcp_f32 v95,v95
v_rcp_f32 v96,v96
v_rcp_f32 v97,v97
v_mul_f32 v18,v18,v94
v_mul_f32 v19,v19,v95
v_mul_f32 v20,v20,v96
v_mul_f32 v21,v21,v97
D 0,56,3072
v_cvt_pk_bf16_f32 v106,v18,v19
v_cvt_pk_bf16_f32 v107,v20,v21
v_fma_f32 v22,-v74,v86,v22
v_fma_f32 v23,-v74,v87,v23
v_fma_f32 v24,-v74,v88,v24
v_fma_f32 v25,-v74,v89,v25
v_mul_f32 v98,v22,v22
v_mul_f32 v99,v23,v23
s_add_u32 s58,s58,s61
s_add_u32 m0,s57,16384
s_add_u32 s56,s55,16384
D 0,56,0
v_mul_f32 v100,v24,v24
v_mul_f32 v101,v25,v25
v_fma_f32 v98,v76,v98,v75
v_fma_f32 v99,v76,v99,v75
v_fma_f32 v100,v76,v100,v75
v_fma_f32 v101,v76,v101,v75
v_mul_f32 v98,v22,v98
v_mul_f32 v99,v23,v99
D 0,56,1024
v_mul_f32 v100,v24,v100
v_mul_f32 v101,v25,v101
v_exp_f32 v98,v98
v_exp_f32 v99,v99
v_exp_f32 v100,v100
v_exp_f32 v101,v101
v_fma_f32 v98,v98,v77,v77
v_fma_f32 v99,v99,v77,v77
D 0,56,2048
v_fma_f32 v100,v100,v77,v77
v_fma_f32 v101,v101,v77,v77
v_rcp_f32 v98,v98
v_rcp_f32 v99,v99
v_rcp_f32 v100,v100
v_rcp_f32 v101,v101
v_mul_f32 v22,v22,v98
v_mul_f32 v23,v23,v99
D 0,56,3072
v_mul_f32 v24,v24,v100
v_mul_f32 v25,v25,v101
v_cvt_pk_bf16_f32 v108,v22,v23
v_cvt_pk_bf16_f32 v109,v24,v25
buffer_store_dwordx4 v[106:109],v0,s[36:39],s66 offen offset:0 sc0 sc1
v_fma_f32 v26,-v74,v90,v26
v_fma_f32 v27,-v74,v91,v27
v_fma_f32 v28,-v74,v92,v28
v_fma_f32 v29,-v74,v93,v29
s_add_u32 s55,s55,s60
s_sub_u32 s57,s84,s57
s_add_u32 s54,s54,s46
v_mul_f32 v102,v26,v26
v_mul_f32 v103,v27,v27
v_mul_f32 v104,v28,v28
v_mul_f32 v105,v29,v29
v_fma_f32 v102,v76,v102,v75
v_fma_f32 v103,v76,v103,v75
v_fma_f32 v104,v76,v104,v75
v_fma_f32 v105,v76,v105,v75
v_mul_f32 v102,v26,v102
v_mul_f32 v103,v27,v103
v_mul_f32 v104,v28,v104
v_mul_f32 v105,v29,v105
v_exp_f32 v102,v102
v_exp_f32 v103,v103
v_exp_f32 v104,v104
v_exp_f32 v105,v105
v_fma_f32 v102,v102,v77,v77
v_fma_f32 v103,v103,v77,v77
v_fma_f32 v104,v104,v77,v77
v_fma_f32 v105,v105,v77,v77
v_rcp_f32 v102,v102
v_rcp_f32 v103,v103
v_rcp_f32 v104,v104
v_rcp_f32 v105,v105
v_mul_f32 v26,v26,v102
v_mul_f32 v27,v27,v103
v_mul_f32 v28,v28,v104
v_mul_f32 v29,v29,v105
v_cvt_pk_bf16_f32 v110,v26,v27
v_cvt_pk_bf16_f32 v111,v28,v29
s_nop 0
v_permlane16_swap_b32 v110,v112
v_permlane16_swap_b32 v111,v113
buffer_store_dwordx4 v[110:113],v10,s[36:39],s66 offen offset:0 sc0 sc1
s_mov_b32 s72,s69
s_mov_b32 s69,0x7fff0000
s_mov_b32 s66,0x7fff0000
s_mov_b32 s76,0x7fff0000
s_branch Lg3_gox_%=
Lg3_xn0_%=:
s_cmp_ge_u32 s54,256
s_cbranch_scc1 Lg3_fin0_%=
s_add_u32 m0,s59,4096
s_nop 0
buffer_load_dword v8,s[36:39],s58 offen offset:0 sc1 lds
buffer_store_dword v4,v3,s[36:39],s71 offen offset:0 sc0 sc1
s_add_u32 m0,s59,5120
s_nop 0
buffer_load_dword v8,s[36:39],s58 offen offset:0 sc1 lds
buffer_store_dword v4,v3,s[36:39],s71 offen offset:0 sc0 sc1
s_mov_b32 s83,0
L 0
v_fma_f32 v18,-v74,v82,v18
v_fma_f32 v19,-v74,v83,v19
v_fma_f32 v20,-v74,v84,v20
v_fma_f32 v21,-v74,v85,v21
v_mul_f32 v94,v18,v18
v_mul_f32 v95,v19,v19
v_mul_f32 v96,v20,v20
v_mul_f32 v97,v21,v21
v_fma_f32 v94,v76,v94,v75
v_fma_f32 v95,v76,v95,v75
v_fma_f32 v96,v76,v96,v75
v_fma_f32 v97,v76,v97,v75
v_mul_f32 v94,v18,v94
v_mul_f32 v95,v19,v95
v_mul_f32 v96,v20,v96
v_mul_f32 v97,v21,v97
v_exp_f32 v94,v94
v_exp_f32 v95,v95
v_exp_f32 v96,v96
v_exp_f32 v97,v97
v_fma_f32 v94,v94,v77,v77
v_fma_f32 v95,v95,v77,v77
v_fma_f32 v96,v96,v77,v77
v_fma_f32 v97,v97,v77,v77
v_rcp_f32 v94,v94
v_rcp_f32 v95,v95
v_rcp_f32 v96,v96
v_rcp_f32 v97,v97
v_mul_f32 v18,v18,v94
v_mul_f32 v19,v19,v95
v_mul_f32 v20,v20,v96
v_mul_f32 v21,v21,v97
v_cvt_pk_bf16_f32 v106,v18,v19
v_cvt_pk_bf16_f32 v107,v20,v21
v_fma_f32 v22,-v74,v86,v22
v_fma_f32 v23,-v74,v87,v23
v_fma_f32 v24,-v74,v88,v24
v_fma_f32 v25,-v74,v89,v25
v_mul_f32 v98,v22,v22
v_mul_f32 v99,v23,v23
v_mul_f32 v100,v24,v24
v_mul_f32 v101,v25,v25
v_fma_f32 v98,v76,v98,v75
v_fma_f32 v99,v76,v99,v75
v_fma_f32 v100,v76,v100,v75
v_fma_f32 v101,v76,v101,v75
v_mul_f32 v98,v22,v98
v_mul_f32 v99,v23,v99
v_mul_f32 v100,v24,v100
v_mul_f32 v101,v25,v101
v_exp_f32 v98,v98
v_exp_f32 v99,v99
v_exp_f32 v100,v100
v_exp_f32 v101,v101
v_fma_f32 v98,v98,v77,v77
v_fma_f32 v99,v99,v77,v77
v_fma_f32 v100,v100,v77,v77
v_fma_f32 v101,v101,v77,v77
v_rcp_f32 v98,v98
v_rcp_f32 v99,v99
v_rcp_f32 v100,v100
v_rcp_f32 v101,v101
v_mul_f32 v22,v22,v98
v_mul_f32 v23,v23,v99
v_mul_f32 v24,v24,v100
v_mul_f32 v25,v25,v101
v_cvt_pk_bf16_f32 v108,v22,v23
v_cvt_pk_bf16_f32 v109,v24,v25
buffer_store_dwordx4 v[106:109],v0,s[36:39],s66 offen offset:0 sc0 sc1
v_fma_f32 v26,-v74,v90,v26
v_fma_f32 v27,-v74,v91,v27
v_fma_f32 v28,-v74,v92,v28
v_fma_f32 v29,-v74,v93,v29
)"
R"(v_mul_f32 v102,v26,v26
v_mul_f32 v103,v27,v27
v_mul_f32 v104,v28,v28
v_mul_f32 v105,v29,v29
v_fma_f32 v102,v76,v102,v75
v_fma_f32 v103,v76,v103,v75
v_fma_f32 v104,v76,v104,v75
v_fma_f32 v105,v76,v105,v75
v_mul_f32 v102,v26,v102
v_mul_f32 v103,v27,v103
v_mul_f32 v104,v28,v104
v_mul_f32 v105,v29,v105
v_exp_f32 v102,v102
v_exp_f32 v103,v103
v_exp_f32 v104,v104
v_exp_f32 v105,v105
v_fma_f32 v102,v102,v77,v77
v_fma_f32 v103,v103,v77,v77
v_fma_f32 v104,v104,v77,v77
v_fma_f32 v105,v105,v77,v77
v_rcp_f32 v102,v102
v_rcp_f32 v103,v103
v_rcp_f32 v104,v104
v_rcp_f32 v105,v105
v_mul_f32 v26,v26,v102
v_mul_f32 v27,v27,v103
v_mul_f32 v28,v28,v104
v_mul_f32 v29,v29,v105
v_cvt_pk_bf16_f32 v110,v26,v27
v_cvt_pk_bf16_f32 v111,v28,v29
s_nop 0
v_permlane16_swap_b32 v110,v112
v_permlane16_swap_b32 v111,v113
buffer_store_dwordx4 v[110:113],v10,s[36:39],s66 offen offset:0 sc0 sc1
s_mov_b32 s72,s69
s_mov_b32 s69,0x7fff0000
s_mov_b32 s66,0x7fff0000
s_mov_b32 s76,0x7fff0000
s_branch Lg3_poll_%=
Lg3_fin0_%=:
L 0
v_fma_f32 v18,-v74,v82,v18
v_fma_f32 v19,-v74,v83,v19
v_fma_f32 v20,-v74,v84,v20
v_fma_f32 v21,-v74,v85,v21
v_mul_f32 v94,v18,v18
v_mul_f32 v95,v19,v19
v_mul_f32 v96,v20,v20
v_mul_f32 v97,v21,v21
v_fma_f32 v94,v76,v94,v75
v_fma_f32 v95,v76,v95,v75
v_fma_f32 v96,v76,v96,v75
v_fma_f32 v97,v76,v97,v75
v_mul_f32 v94,v18,v94
v_mul_f32 v95,v19,v95
v_mul_f32 v96,v20,v96
v_mul_f32 v97,v21,v97
v_exp_f32 v94,v94
v_exp_f32 v95,v95
v_exp_f32 v96,v96
v_exp_f32 v97,v97
v_fma_f32 v94,v94,v77,v77
v_fma_f32 v95,v95,v77,v77
v_fma_f32 v96,v96,v77,v77
v_fma_f32 v97,v97,v77,v77
v_rcp_f32 v94,v94
v_rcp_f32 v95,v95
v_rcp_f32 v96,v96
v_rcp_f32 v97,v97
v_mul_f32 v18,v18,v94
v_mul_f32 v19,v19,v95
v_mul_f32 v20,v20,v96
v_mul_f32 v21,v21,v97
v_cvt_pk_bf16_f32 v106,v18,v19
v_cvt_pk_bf16_f32 v107,v20,v21
v_fma_f32 v22,-v74,v86,v22
v_fma_f32 v23,-v74,v87,v23
v_fma_f32 v24,-v74,v88,v24
v_fma_f32 v25,-v74,v89,v25
v_mul_f32 v98,v22,v22
v_mul_f32 v99,v23,v23
v_mul_f32 v100,v24,v24
v_mul_f32 v101,v25,v25
v_fma_f32 v98,v76,v98,v75
v_fma_f32 v99,v76,v99,v75
v_fma_f32 v100,v76,v100,v75
v_fma_f32 v101,v76,v101,v75
v_mul_f32 v98,v22,v98
v_mul_f32 v99,v23,v99
v_mul_f32 v100,v24,v100
v_mul_f32 v101,v25,v101
v_exp_f32 v98,v98
v_exp_f32 v99,v99
v_exp_f32 v100,v100
v_exp_f32 v101,v101
v_fma_f32 v98,v98,v77,v77
v_fma_f32 v99,v99,v77,v77
v_fma_f32 v100,v100,v77,v77
v_fma_f32 v101,v101,v77,v77
v_rcp_f32 v98,v98
v_rcp_f32 v99,v99
v_rcp_f32 v100,v100
v_rcp_f32 v101,v101
v_mul_f32 v22,v22,v98
v_mul_f32 v23,v23,v99
v_mul_f32 v24,v24,v100
v_mul_f32 v25,v25,v101
v_cvt_pk_bf16_f32 v108,v22,v23
v_cvt_pk_bf16_f32 v109,v24,v25
buffer_store_dwordx4 v[106:109],v0,s[36:39],s66 offen offset:0 sc0 sc1
v_fma_f32 v26,-v74,v90,v26
v_fma_f32 v27,-v74,v91,v27
v_fma_f32 v28,-v74,v92,v28
v_fma_f32 v29,-v74,v93,v29
v_mul_f32 v102,v26,v26
v_mul_f32 v103,v27,v27
v_mul_f32 v104,v28,v28
v_mul_f32 v105,v29,v29
v_fma_f32 v102,v76,v102,v75
v_fma_f32 v103,v76,v103,v75
v_fma_f32 v104,v76,v104,v75
v_fma_f32 v105,v76,v105,v75
v_mul_f32 v102,v26,v102
v_mul_f32 v103,v27,v103
v_mul_f32 v104,v28,v104
v_mul_f32 v105,v29,v105
v_exp_f32 v102,v102
v_exp_f32 v103,v103
v_exp_f32 v104,v104
v_exp_f32 v105,v105
v_fma_f32 v102,v102,v77,v77
v_fma_f32 v103,v103,v77,v77
v_fma_f32 v104,v104,v77,v77
v_fma_f32 v105,v105,v77,v77
v_rcp_f32 v102,v102
v_rcp_f32 v103,v103
v_rcp_f32 v104,v104
v_rcp_f32 v105,v105
v_mul_f32 v26,v26,v102
v_mul_f32 v27,v27,v103
v_mul_f32 v28,v28,v104
v_mul_f32 v29,v29,v105
v_cvt_pk_bf16_f32 v110,v26,v27
v_cvt_pk_bf16_f32 v111,v28,v29
s_nop 0
v_permlane16_swap_b32 v110,v112
v_permlane16_swap_b32 v111,v113
buffer_store_dwordx4 v[110:113],v10,s[36:39],s66 offen offset:0 sc0 sc1
s_mov_b32 s72,s69
s_mov_b32 s69,0x7fff0000
s_mov_b32 s66,0x7fff0000
s_mov_b32 s76,0x7fff0000
s_branch Lg3_done_%=
Lg3_L1_%=:
v_mfma_f32_16x16x32_bf16 v[38:41],a[8:11],v[42:45],0
s_add_u32 m0,s59,3072
R 62,2,5120
v_add_f32_dpp v115,v114,v114 quad_perm:[1,0,3,2] row_mask:0xf bank_mask:0xf
M 38,a[20:23],46
s_mov_b32 s67,s65
buffer_load_dword v8,s[36:39],s58 offen offset:0 sc1 lds
v_mfma_f32_16x16x32_bf16 v[34:37],a[4:7],v[42:45],0
s_add_u32 s65,s65,s62
v_add_f32_dpp v115,v115,v115 quad_perm:[2,3,0,1] row_mask:0xf bank_mask:0xf
v_fma_f32 v18,-v74,v82,v18
M 34,a[16:19],46
R 66,2,6144
v_fma_f32 v19,-v74,v83,v19
v_mfma_f32_16x16x32_bf16 v[30:33],a[0:3],v[42:45],0
v_add_f32_dpp v118,v115,v115 row_half_mirror row_mask:0xf bank_mask:0xf
v_fma_f32 v20,-v74,v84,v20
v_fma_f32 v21,-v74,v85,v21
M 30,a[12:15],46
L 3
v_mul_f32 v94,v18,v18
v_mul_f32 v95,v19,v19
M 30,a[24:27],50
R 70,2,7168
v_mov_b32_dpp v115,v118 row_ror:8 row_mask:0xf bank_mask:0xf
M 30,a[36:39],54
v_fma_f32 v115,-v118,v118,v115
v_max_f32 v115,s87,v115
M 34,a[28:31],50
v_rsq_f32 v116,v115
v_mul_f32 v96,v20,v20
M 34,a[40:43],54
R 42,2,8192
v_mul_f32 v121,v115,v116
M 38,a[32:35],50
v_mul_f32 v119,s88,v116
v_mul_f32 v115,s89,v116
M 38,a[44:47],54
L 3
v_mul_f32 v120,v119,v116
v_mul_f32 v120,v120,v115
M 38,a[56:59],58
R 46,2,9216
v_mul_f32 v97,v21,v21
M 38,a[68:71],62
ds_write_b128 v12,v[118:121] offset:2048
M 34,a[52:55],58
v_fma_f32 v94,v76,v94,v75
v_fma_f32 v95,v76,v95,v75
M 34,a[64:67],62
R 50,2,10240
v_fma_f32 v96,v76,v96,v75
M 30,a[48:51],58
v_fma_f32 v97,v76,v97,v75
v_mul_f32 v94,v18,v94
M 30,a[60:63],62
L 4
v_exp_f32 v94,v94
v_mul_f32 v95,v19,v95
M 30,a[72:75],66
R 54,2,11264
v_mul_f32 v96,v20,v96
M 30,a[84:87],70
v_exp_f32 v95,v95
M 34,a[76:79],66
v_exp_f32 v96,v96
M 34,a[88:91],70
R 58,2,12288
v_mul_f32 v97,v21,v97
M 38,a[80:83],66
v_exp_f32 v97,v97
v_fma_f32 v94,v94,v77,v77
M 38,a[92:95],70
L 4
v_rcp_f32 v94,v94
M 38,a[104:107],42
R 62,2,13312
v_fma_f32 v95,v95,v77,v77
M 38,a[116:119],46
v_rcp_f32 v95,v95
M 34,a[100:103],42
v_fma_f32 v96,v96,v77,v77
v_rcp_f32 v96,v96
M 34,a[112:115],46
R 66,2,14336
v_fma_f32 v97,v97,v77,v77
M 30,a[96:99],42
v_rcp_f32 v97,v97
M 30,a[108:111],46
L 3
v_mul_f32 v18,v18,v94
v_mul_f32 v19,v19,v95
M 30,a[120:123],50
R 70,2,15360
v_mul_f32 v20,v20,v96
M 30,a[132:135],54
s_waitcnt vmcnt(0)
v_mul_f32 v21,v21,v97
v_cvt_pk_bf16_f32 v106,v18,v19
v_cvt_pk_bf16_f32 v107,v20,v21
M 34,a[124:127],50
s_barrier
ds_read_b32 v7,v6 offset:3072
v_fma_f32 v22,-v74,v86,v22
M 34,a[136:139],54
R 42,2,16384
v_fma_f32 v23,-v74,v87,v23
M 38,a[128:131],50
buffer_store_dword v4,v3,s[36:39],s70 offen offset:0 sc0 sc1
M 38,a[140:143],54
L 4
v_fma_f32 v24,-v74,v88,v24
v_fma_f32 v25,-v74,v89,v25
v_mul_f32 v98,v22,v22
M 38,a[152:155],58
s_mov_b32 s70,0x7fff0000
R 46,2,17408
v_mul_f32 v99,v23,v23
M 38,a[164:167],62
v_mul_f32 v100,v24,v24
v_mul_f32 v101,v25,v25
M 34,a[148:151],58
L 2
v_cmp_eq_u32 vcc,s53,v7
v_fma_f32 v98,v76,v98,v75
M 34,a[160:163],62
R 50,2,18432
v_fma_f32 v99,v76,v99,v75
M 30,a[144:147],58
s_cmp_eq_u32 vcc_lo,-1
v_fma_f32 v100,v76,v100,v75
v_fma_f32 v101,v76,v101,v75
v_mul_f32 v98,v22,v98
s_cbranch_scc0 Lg3_X1_%=
M 30,a[156:159],62
s_nop 0
s_add_u32 m0,s57,0
s_add_u32 s56,s55,0
buffer_load_dword v114,v11,s[36:39],s73 offen offset:0 sc1
M 30,a[168:171],66
s_add_u32 s58,s58,s61
R 54,2,19456
v_mul_f32 v99,v23,v99
M 30,a[180:183],70
s_add_u32 s54,s54,s46
D 0,56,0
M 34,a[172:175],66
s_add_u32 s73,s73,s64
v_exp_f32 v98,v98
v_mul_f32 v100,v24,v100
M 34,a[184:187],70
R 58,2,20480
v_mul_f32 v101,v25,v101
M 38,a[176:179],66
v_exp_f32 v99,v99
M 38,a[188:191],70
L 3
D 0,56,1024
M 38,a[200:203],42
R 62,2,21504
v_exp_f32 v100,v100
M 38,a[212:215],46
v_exp_f32 v101,v101
M 34,a[196:199],42
v_fma_f32 v98,v98,v77,v77
v_fma_f32 v99,v99,v77,v77
M 34,a[208:211],46
R 66,2,22528
v_fma_f32 v100,v100,v77,v77
M 30,a[192:195],42
D 0,56,2048
M 30,a[204:207],46
L 3
v_rcp_f32 v98,v98
v_fma_f32 v101,v101,v77,v77
M 30,a[216:219],50
R 70,2,23552
v_mul_f32 v22,v22,v98
M 30,a[228:231],54
D 0,56,3072
M 34,a[220:223],50
s_add_u32 m0,s59,1024
v_rcp_f32 v99,v99
M 34,a[232:235],54
s_add_u32 s56,s55,16384
R 42,2,24576
v_rcp_f32 v100,v100
M 38,a[224:227],50
s_add_u32 s55,s55,s60
buffer_load_dword v8,s[36:39],s58 offen offset:0 sc1 lds
M 38,a[236:239],54
L 3
v_rcp_f32 v101,v101
M 38,a[248:251],58
s_add_u32 m0,s57,16384
R 46,2,25600
v_mul_f32 v23,v23,v99
M 38,v[132:135],62
s_sub_u32 s57,s84,s57
D 0,56,0
M 34,a[244:247],58
v_mul_f32 v24,v24,v100
v_mul_f32 v25,v25,v101
v_cvt_pk_bf16_f32 v108,v22,v23
M 34,v[128:131],62
R 50,2,26624
v_cvt_pk_bf16_f32 v109,v24,v25
M 30,a[240:243],58
buffer_store_dwordx4 v[106:109],v0,s[36:39],s66 offen offset:0 sc0 sc1
v_fma_f32 v26,-v74,v90,v26
M 30,a[252:255],62
L 3
v_fma_f32 v27,-v74,v91,v27
v_fma_f32 v28,-v74,v92,v28
M 30,v[136:139],66
R 54,2,27648
v_fma_f32 v29,-v74,v93,v29
v_mul_f32 v102,v26,v26
M 30,v[148:151],70
D 0,56,1024
M 34,v[140:143],66
v_mul_f32 v103,v27,v27
v_mul_f32 v104,v28,v28
M 34,v[152:155],70
R 58,2,28672
v_mul_f32 v105,v29,v29
M 38,v[144:147],66
v_fma_f32 v102,v76,v102,v75
v_fma_f32 v103,v76,v103,v75
M 38,v[156:159],70
L 3
D 0,56,2048
v_fma_f32 v104,v76,v104,v75
M 38,v[168:171],42
R 62,2,29696
v_fma_f32 v105,v76,v105,v75
M 38,v[180:183],46
v_mul_f32 v102,v26,v102
v_mul_f32 v103,v27,v103
M 34,v[164:167],42
v_exp_f32 v102,v102
M 34,v[176:179],46
R 66,2,30720
v_exp_f32 v103,v103
M 30,v[160:163],42
D 0,56,3072
M 30,v[172:175],46
L 3
v_mul_f32 v104,v28,v104
v_mul_f32 v105,v29,v105
M 30,v[184:187],50
s_nop 0
R 70,2,31744
v_fma_f32 v102,v102,v77,v77
M 30,v[196:199],54
s_waitcnt vmcnt(5)
v_exp_f32 v104,v104
M 34,v[188:191],50
s_barrier
buffer_store_dword v4,v3,s[36:39],s70 offen offset:0 sc0 sc1
v_fma_f32 v103,v103,v77,v77
M 34,v[200:203],54
s_mov_b32 s70,s69
R 42,1,0
ds_read_b32 v7,v6 offset:1024
M 38,v[192:195],50
s_mov_b32 s69,s68
R 78,13,2048
v_fma_f32 v104,v104,v77,v77
M 38,v[204:207],54
L 5
v_exp_f32 v105,v105
M 38,v[216:219],58
s_add_u32 s68,s68,s63
R 46,1,1024
v_rcp_f32 v102,v102
M 38,v[228:231],62
v_rcp_f32 v103,v103
M 34,v[212:215],58
v_rcp_f32 v104,v104
M 34,v[224:227],62
L 2
R 50,1,2048
v_fma_f32 v105,v105,v77,v77
M 30,v[208:211],58
v_rcp_f32 v105,v105
v_mul_f32 v26,v26,v102
M 30,v[220:223],62
s_nop 0
v_mul_f32 v27,v27,v103
v_mul_f32 v28,v28,v104
M 30,v[232:235],66
R 54,1,3072
v_mul_f32 v29,v29,v105
M 30,v[244:247],70
v_cvt_pk_bf16_f32 v110,v26,v27
v_cvt_pk_bf16_f32 v111,v28,v29
M 34,v[236:239],66
v_cmp_eq_u32 vcc,s53,v7
M 34,v[248:251],70
s_cmp_eq_u32 vcc_lo,-1
R 58,1,4096
v_permlane16_swap_b32 v110,v112
v_permlane16_swap_b32 v111,v113
M 38,v[240:243],66
buffer_store_dwordx4 v[110:113],v10,s[36:39],s66 offen offset:0 sc0 sc1
M 38,v[252:255],70
L 3
s_cbranch_scc1 Lg3_F0_%=
s_branch Lg3_L0_%=
Lg3_X1_%=:
M 30,a[156:159],62
s_nop 0
s_add_u32 m0,s59,1024
v_mul_f32 v99,v23,v99
M 30,a[168:171],66
R 54,2,19456
v_mul_f32 v100,v24,v100
M 30,a[180:183],70
buffer_load_dword v8,s[36:39],s58 offen offset:0 sc1 lds
M 34,a[172:175],66
v_exp_f32 v98,v98
M 34,a[184:187],70
R 58,2,20480
v_mul_f32 v101,v25,v101
M 38,a[176:179],66
v_exp_f32 v99,v99
M 38,a[188:191],70
L 3
v_exp_f32 v100,v100
M 38,a[200:203],42
R 62,2,21504
v_fma_f32 v98,v98,v77,v77
M 38,a[212:215],46
v_exp_f32 v101,v101
M 34,a[196:199],42
v_rcp_f32 v98,v98
M 34,a[208:211],46
R 66,2,22528
v_fma_f32 v99,v99,v77,v77
M 30,a[192:195],42
v_rcp_f32 v99,v99
M 30,a[204:207],46
L 3
v_fma_f32 v100,v100,v77,v77
v_fma_f32 v101,v101,v77,v77
)"
R"(M 30,a[216:219],50
R 70,2,23552
v_mul_f32 v22,v22,v98
M 30,a[228:231],54
v_rcp_f32 v100,v100
M 34,a[220:223],50
v_rcp_f32 v101,v101
M 34,a[232:235],54
R 42,2,24576
v_mul_f32 v23,v23,v99
M 38,a[224:227],50
v_mul_f32 v24,v24,v100
v_mul_f32 v25,v25,v101
M 38,a[236:239],54
L 3
v_cvt_pk_bf16_f32 v108,v22,v23
v_cvt_pk_bf16_f32 v109,v24,v25
M 38,a[248:251],58
R 46,2,25600
v_fma_f32 v26,-v74,v90,v26
M 38,v[132:135],62
buffer_store_dwordx4 v[106:109],v0,s[36:39],s66 offen offset:0 sc0 sc1
v_fma_f32 v27,-v74,v91,v27
M 34,a[244:247],58
v_fma_f32 v28,-v74,v92,v28
v_fma_f32 v29,-v74,v93,v29
M 34,v[128:131],62
R 50,2,26624
v_mul_f32 v102,v26,v26
M 30,a[240:243],58
v_mul_f32 v103,v27,v27
v_mul_f32 v104,v28,v28
M 30,a[252:255],62
L 3
v_mul_f32 v105,v29,v29
v_fma_f32 v102,v76,v102,v75
M 30,v[136:139],66
R 54,2,27648
v_fma_f32 v103,v76,v103,v75
M 30,v[148:151],70
v_fma_f32 v104,v76,v104,v75
v_fma_f32 v105,v76,v105,v75
M 34,v[140:143],66
v_mul_f32 v102,v26,v102
v_mul_f32 v103,v27,v103
M 34,v[152:155],70
R 58,2,28672
v_mul_f32 v104,v28,v104
M 38,v[144:147],66
v_exp_f32 v102,v102
M 38,v[156:159],70
L 3
v_exp_f32 v103,v103
M 38,v[168:171],42
R 62,2,29696
v_mul_f32 v105,v29,v105
M 38,v[180:183],46
v_exp_f32 v104,v104
M 34,v[164:167],42
v_exp_f32 v105,v105
M 34,v[176:179],46
R 66,2,30720
v_fma_f32 v102,v102,v77,v77
M 30,v[160:163],42
v_rcp_f32 v102,v102
M 30,v[172:175],46
L 3
v_fma_f32 v103,v103,v77,v77
v_fma_f32 v104,v104,v77,v77
M 30,v[184:187],50
s_nop 0
R 70,2,31744
v_fma_f32 v105,v105,v77,v77
M 30,v[196:199],54
s_waitcnt vmcnt(1)
v_rcp_f32 v103,v103
M 34,v[188:191],50
s_barrier
buffer_store_dword v4,v3,s[36:39],s70 offen offset:0 sc0 sc1
M 34,v[200:203],54
s_mov_b32 s70,s69
ds_read_b32 v7,v6 offset:1024
R 78,13,2048
M 38,v[192:195],50
s_mov_b32 s69,s68
v_rcp_f32 v104,v104
M 38,v[204:207],54
L 4
v_rcp_f32 v105,v105
M 38,v[216:219],58
s_add_u32 s68,s68,s63
v_mul_f32 v26,v26,v102
v_mul_f32 v27,v27,v103
M 38,v[228:231],62
v_mul_f32 v28,v28,v104
v_mul_f32 v29,v29,v105
M 34,v[212:215],58
v_cvt_pk_bf16_f32 v110,v26,v27
v_cvt_pk_bf16_f32 v111,v28,v29
M 34,v[224:227],62
L 1
v_cmp_eq_u32 vcc,s53,v7
M 30,v[208:211],58
v_permlane16_swap_b32 v110,v112
M 30,v[220:223],62
s_nop 0
v_permlane16_swap_b32 v111,v113
M 30,v[232:235],66
buffer_store_dwordx4 v[110:113],v10,s[36:39],s66 offen offset:0 sc0 sc1
M 30,v[244:247],70
M 34,v[236:239],66
M 34,v[248:251],70
M 38,v[240:243],66
M 38,v[252:255],70
s_mov_b32 s80,0
s_cmp_eq_u32 vcc_lo,-1
s_cbranch_scc0 Lg3_xn1_%=
buffer_load_dword v114,v11,s[36:39],s73 offen offset:0 sc1
L 0
v_fma_f32 v30,-v78,v82,v30
v_fma_f32 v31,-v78,v83,v31
v_fma_f32 v32,-v78,v84,v32
v_fma_f32 v33,-v78,v85,v33
v_mul_f32 v94,v30,v30
v_mul_f32 v95,v31,v31
v_mul_f32 v96,v32,v32
v_mul_f32 v97,v33,v33
s_add_u32 s73,s73,s64
s_add_u32 m0,s57,0
s_add_u32 s56,s55,0
D 0,56,0
v_fma_f32 v94,v80,v94,v79
v_fma_f32 v95,v80,v95,v79
v_fma_f32 v96,v80,v96,v79
v_fma_f32 v97,v80,v97,v79
v_mul_f32 v94,v30,v94
v_mul_f32 v95,v31,v95
v_mul_f32 v96,v32,v96
v_mul_f32 v97,v33,v97
D 0,56,1024
v_exp_f32 v94,v94
v_exp_f32 v95,v95
v_exp_f32 v96,v96
v_exp_f32 v97,v97
v_fma_f32 v94,v94,v81,v81
v_fma_f32 v95,v95,v81,v81
v_fma_f32 v96,v96,v81,v81
v_fma_f32 v97,v97,v81,v81
D 0,56,2048
v_rcp_f32 v94,v94
v_rcp_f32 v95,v95
v_rcp_f32 v96,v96
v_rcp_f32 v97,v97
v_mul_f32 v30,v30,v94
v_mul_f32 v31,v31,v95
v_mul_f32 v32,v32,v96
v_mul_f32 v33,v33,v97
D 0,56,3072
v_cvt_pk_bf16_f32 v106,v30,v31
v_cvt_pk_bf16_f32 v107,v32,v33
v_fma_f32 v34,-v78,v86,v34
v_fma_f32 v35,-v78,v87,v35
v_fma_f32 v36,-v78,v88,v36
v_fma_f32 v37,-v78,v89,v37
v_mul_f32 v98,v34,v34
v_mul_f32 v99,v35,v35
s_add_u32 s58,s58,s61
s_add_u32 m0,s57,16384
s_add_u32 s56,s55,16384
D 0,56,0
v_mul_f32 v100,v36,v36
v_mul_f32 v101,v37,v37
v_fma_f32 v98,v80,v98,v79
v_fma_f32 v99,v80,v99,v79
v_fma_f32 v100,v80,v100,v79
v_fma_f32 v101,v80,v101,v79
v_mul_f32 v98,v34,v98
v_mul_f32 v99,v35,v99
D 0,56,1024
v_mul_f32 v100,v36,v100
v_mul_f32 v101,v37,v101
v_exp_f32 v98,v98
v_exp_f32 v99,v99
v_exp_f32 v100,v100
v_exp_f32 v101,v101
v_fma_f32 v98,v98,v81,v81
v_fma_f32 v99,v99,v81,v81
D 0,56,2048
v_fma_f32 v100,v100,v81,v81
v_fma_f32 v101,v101,v81,v81
v_rcp_f32 v98,v98
v_rcp_f32 v99,v99
v_rcp_f32 v100,v100
v_rcp_f32 v101,v101
v_mul_f32 v34,v34,v98
v_mul_f32 v35,v35,v99
D 0,56,3072
v_mul_f32 v36,v36,v100
v_mul_f32 v37,v37,v101
v_cvt_pk_bf16_f32 v108,v34,v35
v_cvt_pk_bf16_f32 v109,v36,v37
buffer_store_dwordx4 v[106:109],v0,s[36:39],s67 offen offset:0 sc0 sc1
v_fma_f32 v38,-v78,v90,v38
v_fma_f32 v39,-v78,v91,v39
v_fma_f32 v40,-v78,v92,v40
v_fma_f32 v41,-v78,v93,v41
s_add_u32 s55,s55,s60
s_sub_u32 s57,s84,s57
s_add_u32 s54,s54,s46
v_mul_f32 v102,v38,v38
v_mul_f32 v103,v39,v39
v_mul_f32 v104,v40,v40
v_mul_f32 v105,v41,v41
v_fma_f32 v102,v80,v102,v79
v_fma_f32 v103,v80,v103,v79
v_fma_f32 v104,v80,v104,v79
v_fma_f32 v105,v80,v105,v79
v_mul_f32 v102,v38,v102
v_mul_f32 v103,v39,v103
v_mul_f32 v104,v40,v104
v_mul_f32 v105,v41,v105
v_exp_f32 v102,v102
v_exp_f32 v103,v103
v_exp_f32 v104,v104
v_exp_f32 v105,v105
v_fma_f32 v102,v102,v81,v81
v_fma_f32 v103,v103,v81,v81
v_fma_f32 v104,v104,v81,v81
v_fma_f32 v105,v105,v81,v81
v_rcp_f32 v102,v102
v_rcp_f32 v103,v103
v_rcp_f32 v104,v104
v_rcp_f32 v105,v105
v_mul_f32 v38,v38,v102
v_mul_f32 v39,v39,v103
v_mul_f32 v40,v40,v104
v_mul_f32 v41,v41,v105
v_cvt_pk_bf16_f32 v110,v38,v39
v_cvt_pk_bf16_f32 v111,v40,v41
s_nop 0
v_permlane16_swap_b32 v110,v112
v_permlane16_swap_b32 v111,v113
buffer_store_dwordx4 v[110:113],v10,s[36:39],s67 offen offset:0 sc0 sc1
s_mov_b32 s72,s69
s_mov_b32 s69,0x7fff0000
s_mov_b32 s67,0x7fff0000
s_mov_b32 s77,0x7fff0000
s_branch Lg3_gox_%=
Lg3_xn1_%=:
s_cmp_ge_u32 s54,256
s_cbranch_scc1 Lg3_fin1_%=
s_add_u32 m0,s59,4096
s_nop 0
buffer_load_dword v8,s[36:39],s58 offen offset:0 sc1 lds
buffer_store_dword v4,v3,s[36:39],s71 offen offset:0 sc0 sc1
s_add_u32 m0,s59,5120
s_nop 0
buffer_load_dword v8,s[36:39],s58 offen offset:0 sc1 lds
buffer_store_dword v4,v3,s[36:39],s71 offen offset:0 sc0 sc1
s_mov_b32 s83,0
L 0
v_fma_f32 v30,-v78,v82,v30
v_fma_f32 v31,-v78,v83,v31
v_fma_f32 v32,-v78,v84,v32
v_fma_f32 v33,-v78,v85,v33
v_mul_f32 v94,v30,v30
v_mul_f32 v95,v31,v31
v_mul_f32 v96,v32,v32
v_mul_f32 v97,v33,v33
v_fma_f32 v94,v80,v94,v79
v_fma_f32 v95,v80,v95,v79
v_fma_f32 v96,v80,v96,v79
v_fma_f32 v97,v80,v97,v79
v_mul_f32 v94,v30,v94
v_mul_f32 v95,v31,v95
v_mul_f32 v96,v32,v96
v_mul_f32 v97,v33,v97
v_exp_f32 v94,v94
v_exp_f32 v95,v95
v_exp_f32 v96,v96
v_exp_f32 v97,v97
v_fma_f32 v94,v94,v81,v81
v_fma_f32 v95,v95,v81,v81
v_fma_f32 v96,v96,v81,v81
v_fma_f32 v97,v97,v81,v81
v_rcp_f32 v94,v94
v_rcp_f32 v95,v95
v_rcp_f32 v96,v96
v_rcp_f32 v97,v97
v_mul_f32 v30,v30,v94
v_mul_f32 v31,v31,v95
v_mul_f32 v32,v32,v96
v_mul_f32 v33,v33,v97
v_cvt_pk_bf16_f32 v106,v30,v31
v_cvt_pk_bf16_f32 v107,v32,v33
v_fma_f32 v34,-v78,v86,v34
v_fma_f32 v35,-v78,v87,v35
v_fma_f32 v36,-v78,v88,v36
v_fma_f32 v37,-v78,v89,v37
v_mul_f32 v98,v34,v34
v_mul_f32 v99,v35,v35
v_mul_f32 v100,v36,v36
v_mul_f32 v101,v37,v37
v_fma_f32 v98,v80,v98,v79
v_fma_f32 v99,v80,v99,v79
v_fma_f32 v100,v80,v100,v79
v_fma_f32 v101,v80,v101,v79
v_mul_f32 v98,v34,v98
v_mul_f32 v99,v35,v99
v_mul_f32 v100,v36,v100
v_mul_f32 v101,v37,v101
v_exp_f32 v98,v98
v_exp_f32 v99,v99
v_exp_f32 v100,v100
v_exp_f32 v101,v101
v_fma_f32 v98,v98,v81,v81
v_fma_f32 v99,v99,v81,v81
v_fma_f32 v100,v100,v81,v81
v_fma_f32 v101,v101,v81,v81
v_rcp_f32 v98,v98
v_rcp_f32 v99,v99
v_rcp_f32 v100,v100
v_rcp_f32 v101,v101
v_mul_f32 v34,v34,v98
v_mul_f32 v35,v35,v99
v_mul_f32 v36,v36,v100
v_mul_f32 v37,v37,v101
v_cvt_pk_bf16_f32 v108,v34,v35
v_cvt_pk_bf16_f32 v109,v36,v37
buffer_store_dwordx4 v[106:109],v0,s[36:39],s67 offen offset:0 sc0 sc1
v_fma_f32 v38,-v78,v90,v38
v_fma_f32 v39,-v78,v91,v39
v_fma_f32 v40,-v78,v92,v40
v_fma_f32 v41,-v78,v93,v41
v_mul_f32 v102,v38,v38
v_mul_f32 v103,v39,v39
v_mul_f32 v104,v40,v40
v_mul_f32 v105,v41,v41
v_fma_f32 v102,v80,v102,v79
v_fma_f32 v103,v80,v103,v79
v_fma_f32 v104,v80,v104,v79
v_fma_f32 v105,v80,v105,v79
v_mul_f32 v102,v38,v102
v_mul_f32 v103,v39,v103
v_mul_f32 v104,v40,v104
v_mul_f32 v105,v41,v105
v_exp_f32 v102,v102
v_exp_f32 v103,v103
v_exp_f32 v104,v104
v_exp_f32 v105,v105
v_fma_f32 v102,v102,v81,v81
v_fma_f32 v103,v103,v81,v81
v_fma_f32 v104,v104,v81,v81
v_fma_f32 v105,v105,v81,v81
v_rcp_f32 v102,v102
v_rcp_f32 v103,v103
v_rcp_f32 v104,v104
v_rcp_f32 v105,v105
v_mul_f32 v38,v38,v102
v_mul_f32 v39,v39,v103
v_mul_f32 v40,v40,v104
v_mul_f32 v41,v41,v105
v_cvt_pk_bf16_f32 v110,v38,v39
v_cvt_pk_bf16_f32 v111,v40,v41
s_nop 0
v_permlane16_swap_b32 v110,v112
v_permlane16_swap_b32 v111,v113
buffer_store_dwordx4 v[110:113],v10,s[36:39],s67 offen offset:0 sc0 sc1
s_mov_b32 s72,s69
s_mov_b32 s69,0x7fff0000
s_mov_b32 s67,0x7fff0000
s_mov_b32 s77,0x7fff0000
s_branch Lg3_poll_%=
Lg3_fin1_%=:
L 0
v_fma_f32 v30,-v78,v82,v30
v_fma_f32 v31,-v78,v83,v31
v_fma_f32 v32,-v78,v84,v32
v_fma_f32 v33,-v78,v85,v33
v_mul_f32 v94,v30,v30
v_mul_f32 v95,v31,v31
v_mul_f32 v96,v32,v32
v_mul_f32 v97,v33,v33
v_fma_f32 v94,v80,v94,v79
v_fma_f32 v95,v80,v95,v79
v_fma_f32 v96,v80,v96,v79
v_fma_f32 v97,v80,v97,v79
v_mul_f32 v94,v30,v94
v_mul_f32 v95,v31,v95
v_mul_f32 v96,v32,v96
v_mul_f32 v97,v33,v97
v_exp_f32 v94,v94
v_exp_f32 v95,v95
v_exp_f32 v96,v96
v_exp_f32 v97,v97
v_fma_f32 v94,v94,v81,v81
v_fma_f32 v95,v95,v81,v81
v_fma_f32 v96,v96,v81,v81
v_fma_f32 v97,v97,v81,v81
v_rcp_f32 v94,v94
v_rcp_f32 v95,v95
v_rcp_f32 v96,v96
v_rcp_f32 v97,v97
v_mul_f32 v30,v30,v94
v_mul_f32 v31,v31,v95
v_mul_f32 v32,v32,v96
v_mul_f32 v33,v33,v97
v_cvt_pk_bf16_f32 v106,v30,v31
v_cvt_pk_bf16_f32 v107,v32,v33
v_fma_f32 v34,-v78,v86,v34
v_fma_f32 v35,-v78,v87,v35
v_fma_f32 v36,-v78,v88,v36
v_fma_f32 v37,-v78,v89,v37
v_mul_f32 v98,v34,v34
v_mul_f32 v99,v35,v35
v_mul_f32 v100,v36,v36
v_mul_f32 v101,v37,v37
v_fma_f32 v98,v80,v98,v79
v_fma_f32 v99,v80,v99,v79
v_fma_f32 v100,v80,v100,v79
v_fma_f32 v101,v80,v101,v79
v_mul_f32 v98,v34,v98
v_mul_f32 v99,v35,v99
v_mul_f32 v100,v36,v100
v_mul_f32 v101,v37,v101
v_exp_f32 v98,v98
v_exp_f32 v99,v99
v_exp_f32 v100,v100
v_exp_f32 v101,v101
v_fma_f32 v98,v98,v81,v81
v_fma_f32 v99,v99,v81,v81
v_fma_f32 v100,v100,v81,v81
v_fma_f32 v101,v101,v81,v81
v_rcp_f32 v98,v98
v_rcp_f32 v99,v99
v_rcp_f32 v100,v100
v_rcp_f32 v101,v101
v_mul_f32 v34,v34,v98
v_mul_f32 v35,v35,v99
v_mul_f32 v36,v36,v100
v_mul_f32 v37,v37,v101
v_cvt_pk_bf16_f32 v108,v34,v35
v_cvt_pk_bf16_f32 v109,v36,v37
buffer_store_dwordx4 v[106:109],v0,s[36:39],s67 offen offset:0 sc0 sc1
v_fma_f32 v38,-v78,v90,v38
v_fma_f32 v39,-v78,v91,v39
v_fma_f32 v40,-v78,v92,v40
v_fma_f32 v41,-v78,v93,v41
v_mul_f32 v102,v38,v38
v_mul_f32 v103,v39,v39
v_mul_f32 v104,v40,v40
v_mul_f32 v105,v41,v41
v_fma_f32 v102,v80,v102,v79
v_fma_f32 v103,v80,v103,v79
v_fma_f32 v104,v80,v104,v79
v_fma_f32 v105,v80,v105,v79
v_mul_f32 v102,v38,v102
v_mul_f32 v103,v39,v103
v_mul_f32 v104,v40,v104
v_mul_f32 v105,v41,v105
v_exp_f32 v102,v102
v_exp_f32 v103,v103
v_exp_f32 v104,v104
v_exp_f32 v105,v105
v_fma_f32 v102,v102,v81,v81
v_fma_f32 v103,v103,v81,v81
v_fma_f32 v104,v104,v81,v81
v_fma_f32 v105,v105,v81,v81
v_rcp_f32 v102,v102
v_rcp_f32 v103,v103
v_rcp_f32 v104,v104
v_rcp_f32 v105,v105
v_mul_f32 v38,v38,v102
v_mul_f32 v39,v39,v103
v_mul_f32 v40,v40,v104
v_mul_f32 v41,v41,v105
v_cvt_pk_bf16_f32 v110,v38,v39
v_cvt_pk_bf16_f32 v111,v40,v41
s_nop 0
v_permlane16_swap_b32 v110,v112
v_permlane16_swap_b32 v111,v113
buffer_store_dwordx4 v[110:113],v10,s[36:39],s67 offen offset:0 sc0 sc1
s_mov_b32 s72,s69
s_mov_b32 s69,0x7fff0000
s_mov_b32 s67,0x7fff0000
s_mov_b32 s77,0x7fff0000
s_branch Lg3_done_%=
Lg3_done_%=:
s_waitcnt vmcnt(2)
s_barrier
buffer_store_dword v4,v3,s[36:39],s70 offen offset:0 sc0 sc1
s_waitcnt vmcnt(1)
s_barrier
buffer_store_dword v4,v3,s[36:39],s72 offen offset:0 sc0 sc1
s_waitcnt vmcnt(0) lgkmcnt(0)
s_barrier
s_endpgm
Lcode_g3_e:
.purgem M
.purgem R
)"
R"(.purgem T
.purgem D
.purgem L
)"
  :: "s"(ws), "s"(sWave), "s"(sG0), "s"(sStep), "s"(sOutOff), "s"(sFoutOff), "s"(sAuxOff), "s"(sWw), "s"(sBuf), "s"(sRk), "s"(sExp), "v"(vL16), "v"(vFlag), "v"(vSnapOff), "v"(vSnapRd), "v"(vWd0), "v"(vWd1), "v"(vWr0), "v"(vWr1), "v"(vWr2), "v"(vOutS), "v"(vAux0), "v"(vAux1), "v"(vAux2), "v"(vAux3), "v"(vAn), "v"(vAe), "s"(w3), "s"(out) : "v0", "v1", "v2", "v3", "v4", "v5", "v6", "v7", "v8", "v9", "v10", "v11", "v12", "v13", "v14", "v15", "v16", "v17", "v18", "v19", "v20", "v21", "v22", "v23", "v24", "v25", "v26", "v27", "v28", "v29", "v30", "v31", "v32", "v33", "v34", "v35", "v36", "v37", "v38", "v39", "v40", "v41", "v42", "v43", "v44", "v45", "v46", "v47", "v48", "v49", "v50", "v51", "v52", "v53", "v54", "v55", "v56", "v57", "v58", "v59", "v60", "v61", "v62", "v63", "v64", "v65", "v66", "v67", "v68", "v69", "v70", "v71", "v72", "v73", "v74", "v75", "v76", "v77", "v78", "v79", "v80", "v81", "v82", "v83", "v84", "v85", "v86", "v87", "v88", "v89", "v90", "v91", "v92", "v93", "v94", "v95", "v96", "v97", "v98", "v99", "v100", "v101", "v102", "v103", "v104", "v105", "v106", "v107", "v108", "v109", "v110", "v111", "v112", "v113", "v114", "v115", "v116", "v117", "v118", "v119", "v120", "v121", "v122", "v123", "v124", "v125", "v126", "v127", "v128", "v129", "v130", "v131", "v132", "v133", "v134", "v135", "v136", "v137", "v138", "v139", "v140", "v141", "v142", "v143", "v144", "v145", "v146", "v147", "v148", "v149", "v150", "v151", "v152", "v153", "v154", "v155", "v156", "v157", "v158", "v159", "v160", "v161", "v162", "v163", "v164", "v165", "v166", "v167", "v168", "v169", "v170", "v171", "v172", "v173", "v174", "v175", "v176", "v177", "v178", "v179", "v180", "v181", "v182", "v183", "v184", "v185", "v186", "v187", "v188", "v189", "v190", "v191", "v192", "v193", "v194", "v195", "v196", "v197", "v198", "v199", "v200", "v201", "v202", "v203", "v204", "v205", "v206", "v207", "v208", "v209", "v210", "v211", "v212", "v213", "v214", "v215", "v216", "v217", "v218", "v219", "v220", "v221", "v222", "v223", "v224", "v225", "v226", "v227", "v228", "v229", "v230", "v231", "v232", "v233", "v234", "v235", "v236", "v237", "v238", "v239", "a0", "a1", "a2", "a3", "a4", "a5", "a6", "a7", "a8", "a9", "a10", "a11", "a12", "a13", "a14", "a15", "a16", "a17", "a18", "a19", "a20", "a21", "a22", "a23", "a24", "a25", "a26", "a27", "a28", "a29", "a30", "a31", "a32", "a33", "a34", "a35", "a36", "a37", "a38", "a39", "a40", "a41", "a42", "a43", "a44", "a45", "a46", "a47", "a48", "a49", "a50", "a51", "a52", "a53", "a54", "a55", "a56", "a57", "a58", "a59", "a60", "a61", "a62", "a63", "a64", "a65", "a66", "a67", "a68", "a69", "a70", "a71", "a72", "a73", "a74", "a75", "a76", "a77", "a78", "a79", "a80", "a81", "a82", "a83", "a84", "a85", "a86", "a87", "a88", "a89", "a90", "a91", "a92", "a93", "a94", "a95", "a96", "a97", "a98", "a99", "a100", "a101", "a102", "a103", "a104", "a105", "a106", "a107", "a108", "a109", "a110", "a111", "a112", "a113", "a114", "a115", "a116", "a117", "a118", "a119", "a120", "a121", "a122", "a123", "a124", "a125", "a126", "a127", "a128", "a129", "a130", "a131", "a132", "a133", "a134", "a135", "a136", "a137", "a138", "a139", "a140", "a141", "a142", "a143", "a144", "a145", "a146", "a147", "a148", "a149", "a150", "a151", "a152", "a153", "a154", "a155", "a156", "a157", "a158", "a159", "a160", "a161", "a162", "a163", "a164", "a165", "a166", "a167", "a168", "a169", "a170", "a171", "a172", "a173", "a174", "a175", "a176", "a177", "a178", "a179", "a180", "a181", "a182", "a183", "a184", "a185", "a186", "a187", "a188", "a189", "a190", "a191", "a192", "a193", "a194", "a195", "a196", "a197", "a198", "a199", "a200", "a201", "a202", "a203", "a204", "a205", "a206", "a207", "a208", "a209", "a210", "a211", "a212", "a213", "a214", "a215", "a216", "a217", "a218", "a219", "a220", "a221", "a222", "a223", "a224", "a225", "a226", "a227", "a228", "a229", "a230", "a231", "a232", "a233", "a234", "a235", "a236", "a237", "a238", "a239", "a240", "a241", "a242", "a243", "a244", "a245", "a246", "a247", "a248", "a249", "a250", "a251", "a252", "a253", "a254", "a255", "s36", "s37", "s38", "s39", "s40", "s41", "s42", "s43", "s44", "s45", "s46", "s47", "s48", "s49", "s50", "s51", "s52", "s53", "s54", "s55", "s56", "s57", "s58", "s59", "s60", "s61", "s62", "s63", "s64", "s65", "s66", "s67", "s68", "s69", "s70", "s71", "s72", "s73", "s74", "s75", "s76", "s77", "s78", "s79", "s80", "s81", "s82", "s83", "s84", "s85", "s86", "s87", "s88", "s89", "s90", "s91", "s92", "s93", "s94", "s95", "s96", "s97", "vcc", "scc", "memory"); } else { asm volatile(
R"(.macro M d,w,x
v_mfma_f32_16x16x32_bf16 v[\d:\d+3], \w, v[\x:\x+3], v[\d:\d+3]
.endm
.macro R d,a,o
ds_read_b128 v[\d:\d+3], v\a offset:\o
.endm
.macro T d,a,o
ds_read_b64_tr_b16 \d, v\a offset:\o
.endm
.macro D a,s,o
buffer_load_dwordx4 v\a, s[36:39], s\s offen offset:\o sc1 lds
.endm
.macro L c
s_waitcnt lgkmcnt(\c)
.endm
Lcode_g3o_b:
s_mov_b64 s[36:37],%0
s_mov_b32 s38,111243392
s_mov_b32 s39,0x00020000
s_mov_b32 s44,%1
s_mov_b32 s45,%2
s_mov_b32 s46,%3
s_mov_b32 s47,%4
s_mov_b32 s48,%5
s_mov_b32 s49,%6
s_mov_b32 s50,%7
s_mov_b32 s51,%8
s_mov_b32 s93,%9
s_mov_b32 s53,%10
v_mov_b32 v0,%11
v_mov_b32 v3,%12
v_mov_b32 v5,%13
v_mov_b32 v6,%14
v_mov_b32 v18,%15
v_mov_b32 v19,%16
v_mov_b32 v20,%17
v_mov_b32 v21,%18
v_mov_b32 v22,%19
v_mov_b32 v10,%20
v_mov_b32 v11,%21
v_mov_b32 v12,%22
v_mov_b32 v13,%23
v_mov_b32 v14,%24
v_mov_b32 v15,%25
v_mov_b32 v16,%26
s_mov_b64 s[40:41],%27
s_mov_b32 s42,7340032
s_mov_b32 s43,0x00020000
s_lshl_b32 s57,s44,11
s_add_u32 s55,s50,0
s_add_u32 m0,s57,0
s_nop 0
buffer_load_dwordx4 v18,s[40:43],s55 offen offset:0  lds
s_add_u32 m0,s57,1024
s_nop 0
buffer_load_dwordx4 v19,s[40:43],s55 offen offset:0  lds
s_add_u32 m0,s57,8064
s_nop 0
buffer_load_dwordx4 v18,s[40:43],s55 offen offset:128  lds
s_add_u32 m0,s57,9088
s_nop 0
buffer_load_dwordx4 v19,s[40:43],s55 offen offset:128  lds
s_add_u32 s55,s50,458752
s_add_u32 m0,s57,16384
s_nop 0
buffer_load_dwordx4 v18,s[40:43],s55 offen offset:0  lds
s_add_u32 m0,s57,17408
s_nop 0
buffer_load_dwordx4 v19,s[40:43],s55 offen offset:0  lds
s_add_u32 m0,s57,24448
s_nop 0
buffer_load_dwordx4 v18,s[40:43],s55 offen offset:128  lds
s_add_u32 m0,s57,25472
s_nop 0
buffer_load_dwordx4 v19,s[40:43],s55 offen offset:128  lds
s_add_u32 s55,s50,917504
s_add_u32 m0,s57,32768
s_nop 0
buffer_load_dwordx4 v18,s[40:43],s55 offen offset:0  lds
s_add_u32 m0,s57,33792
s_nop 0
buffer_load_dwordx4 v19,s[40:43],s55 offen offset:0  lds
s_add_u32 m0,s57,40832
s_nop 0
buffer_load_dwordx4 v18,s[40:43],s55 offen offset:128  lds
s_add_u32 m0,s57,41856
s_nop 0
buffer_load_dwordx4 v19,s[40:43],s55 offen offset:128  lds
s_add_u32 s55,s50,1376256
s_add_u32 m0,s57,49152
s_nop 0
buffer_load_dwordx4 v18,s[40:43],s55 offen offset:0  lds
s_add_u32 m0,s57,50176
s_nop 0
buffer_load_dwordx4 v19,s[40:43],s55 offen offset:0  lds
s_add_u32 m0,s57,57216
s_nop 0
buffer_load_dwordx4 v18,s[40:43],s55 offen offset:128  lds
s_add_u32 m0,s57,58240
s_nop 0
buffer_load_dwordx4 v19,s[40:43],s55 offen offset:128  lds
s_add_u32 s55,s50,1835008
s_add_u32 m0,s57,65536
s_nop 0
buffer_load_dwordx4 v18,s[40:43],s55 offen offset:0  lds
s_add_u32 m0,s57,66560
s_nop 0
buffer_load_dwordx4 v19,s[40:43],s55 offen offset:0  lds
s_add_u32 m0,s57,73600
s_nop 0
buffer_load_dwordx4 v18,s[40:43],s55 offen offset:128  lds
s_add_u32 m0,s57,74624
s_nop 0
buffer_load_dwordx4 v19,s[40:43],s55 offen offset:128  lds
s_add_u32 s55,s50,2293760
s_add_u32 m0,s57,81920
s_nop 0
buffer_load_dwordx4 v18,s[40:43],s55 offen offset:0  lds
s_add_u32 m0,s57,82944
s_nop 0
buffer_load_dwordx4 v19,s[40:43],s55 offen offset:0  lds
s_add_u32 m0,s57,89984
s_nop 0
buffer_load_dwordx4 v18,s[40:43],s55 offen offset:128  lds
s_add_u32 m0,s57,91008
s_nop 0
buffer_load_dwordx4 v19,s[40:43],s55 offen offset:128  lds
s_waitcnt vmcnt(20) lgkmcnt(0)
s_barrier
s_add_u32 s55,s50,2752512
s_add_u32 m0,s57,98304
s_nop 0
buffer_load_dwordx4 v18,s[40:43],s55 offen offset:0  lds
s_add_u32 m0,s57,99328
s_nop 0
buffer_load_dwordx4 v19,s[40:43],s55 offen offset:0  lds
s_add_u32 m0,s57,106368
s_nop 0
buffer_load_dwordx4 v18,s[40:43],s55 offen offset:128  lds
s_add_u32 m0,s57,107392
s_nop 0
buffer_load_dwordx4 v19,s[40:43],s55 offen offset:128  lds
v_add_u32 v26,0,v20
v_add_u32 v27,0,v21
T a[0:1],26,0
T a[2:3],26,512
T a[4:5],27,0
T a[6:7],27,512
T a[8:9],26,4096
T a[10:11],26,4608
T a[12:13],27,4096
T a[14:15],27,4608
s_waitcnt vmcnt(20) lgkmcnt(0)
s_barrier
s_add_u32 s55,s50,3211264
s_add_u32 m0,s57,0
s_nop 0
buffer_load_dwordx4 v18,s[40:43],s55 offen offset:0  lds
s_add_u32 m0,s57,1024
s_nop 0
buffer_load_dwordx4 v19,s[40:43],s55 offen offset:0  lds
s_add_u32 m0,s57,8064
s_nop 0
buffer_load_dwordx4 v18,s[40:43],s55 offen offset:128  lds
s_add_u32 m0,s57,9088
s_nop 0
buffer_load_dwordx4 v19,s[40:43],s55 offen offset:128  lds
v_add_u32 v26,16384,v20
v_add_u32 v27,16384,v21
T a[16:17],26,0
T a[18:19],26,512
T a[20:21],27,0
T a[22:23],27,512
T a[24:25],26,4096
T a[26:27],26,4608
T a[28:29],27,4096
T a[30:31],27,4608
s_waitcnt vmcnt(20) lgkmcnt(0)
s_barrier
s_add_u32 s55,s50,3670016
s_add_u32 m0,s57,16384
s_nop 0
buffer_load_dwordx4 v18,s[40:43],s55 offen offset:0  lds
s_add_u32 m0,s57,17408
s_nop 0
buffer_load_dwordx4 v19,s[40:43],s55 offen offset:0  lds
s_add_u32 m0,s57,24448
s_nop 0
buffer_load_dwordx4 v18,s[40:43],s55 offen offset:128  lds
s_add_u32 m0,s57,25472
s_nop 0
buffer_load_dwordx4 v19,s[40:43],s55 offen offset:128  lds
v_add_u32 v26,32768,v20
v_add_u32 v27,32768,v21
T a[32:33],26,0
T a[34:35],26,512
T a[36:37],27,0
T a[38:39],27,512
T a[40:41],26,4096
T a[42:43],26,4608
T a[44:45],27,4096
T a[46:47],27,4608
s_waitcnt vmcnt(20) lgkmcnt(0)
s_barrier
s_add_u32 s55,s50,4128768
s_add_u32 m0,s57,32768
s_nop 0
buffer_load_dwordx4 v18,s[40:43],s55 offen offset:0  lds
s_add_u32 m0,s57,33792
s_nop 0
buffer_load_dwordx4 v19,s[40:43],s55 offen offset:0  lds
s_add_u32 m0,s57,40832
s_nop 0
buffer_load_dwordx4 v18,s[40:43],s55 offen offset:128  lds
s_add_u32 m0,s57,41856
s_nop 0
buffer_load_dwordx4 v19,s[40:43],s55 offen offset:128  lds
v_add_u32 v26,49152,v20
v_add_u32 v27,49152,v21
T a[48:49],26,0
T a[50:51],26,512
T a[52:53],27,0
T a[54:55],27,512
T a[56:57],26,4096
T a[58:59],26,4608
T a[60:61],27,4096
T a[62:63],27,4608
s_waitcnt vmcnt(20) lgkmcnt(0)
s_barrier
s_add_u32 s55,s50,4587520
s_add_u32 m0,s57,49152
s_nop 0
buffer_load_dwordx4 v18,s[40:43],s55 offen offset:0  lds
s_add_u32 m0,s57,50176
s_nop 0
buffer_load_dwordx4 v19,s[40:43],s55 offen offset:0  lds
s_add_u32 m0,s57,57216
s_nop 0
buffer_load_dwordx4 v18,s[40:43],s55 offen offset:128  lds
s_add_u32 m0,s57,58240
s_nop 0
buffer_load_dwordx4 v19,s[40:43],s55 offen offset:128  lds
v_add_u32 v26,65536,v20
v_add_u32 v27,65536,v21
T a[64:65],26,0
T a[66:67],26,512
T a[68:69],27,0
T a[70:71],27,512
T a[72:73],26,4096
T a[74:75],26,4608
T a[76:77],27,4096
T a[78:79],27,4608
s_waitcnt vmcnt(20) lgkmcnt(0)
s_barrier
s_add_u32 s55,s50,5046272
s_add_u32 m0,s57,65536
s_nop 0
buffer_load_dwordx4 v18,s[40:43],s55 offen offset:0  lds
s_add_u32 m0,s57,66560
s_nop 0
buffer_load_dwordx4 v19,s[40:43],s55 offen offset:0  lds
s_add_u32 m0,s57,73600
s_nop 0
buffer_load_dwordx4 v18,s[40:43],s55 offen offset:128  lds
s_add_u32 m0,s57,74624
s_nop 0
buffer_load_dwordx4 v19,s[40:43],s55 offen offset:128  lds
v_add_u32 v26,81920,v20
v_add_u32 v27,81920,v21
T a[80:81],26,0
T a[82:83],26,512
T a[84:85],27,0
T a[86:87],27,512
T a[88:89],26,4096
T a[90:91],26,4608
T a[92:93],27,4096
T a[94:95],27,4608
s_waitcnt vmcnt(20) lgkmcnt(0)
s_barrier
s_add_u32 s55,s50,5505024
s_add_u32 m0,s57,81920
s_nop 0
buffer_load_dwordx4 v18,s[40:43],s55 offen offset:0  lds
s_add_u32 m0,s57,82944
s_nop 0
buffer_load_dwordx4 v19,s[40:43],s55 offen offset:0  lds
s_add_u32 m0,s57,89984
s_nop 0
buffer_load_dwordx4 v18,s[40:43],s55 offen offset:128  lds
s_add_u32 m0,s57,91008
s_nop 0
buffer_load_dwordx4 v19,s[40:43],s55 offen offset:128  lds
v_add_u32 v26,98304,v20
v_add_u32 v27,98304,v21
T a[96:97],26,0
T a[98:99],26,512
T a[100:101],27,0
T a[102:103],27,512
T a[104:105],26,4096
T a[106:107],26,4608
T a[108:109],27,4096
T a[110:111],27,4608
s_waitcnt vmcnt(20) lgkmcnt(0)
s_barrier
s_add_u32 s55,s50,5963776
s_add_u32 m0,s57,98304
s_nop 0
buffer_load_dwordx4 v18,s[40:43],s55 offen offset:0  lds
s_add_u32 m0,s57,99328
s_nop 0
buffer_load_dwordx4 v19,s[40:43],s55 offen offset:0  lds
s_add_u32 m0,s57,106368
s_nop 0
buffer_load_dwordx4 v18,s[40:43],s55 offen offset:128  lds
s_add_u32 m0,s57,107392
s_nop 0
buffer_load_dwordx4 v19,s[40:43],s55 offen offset:128  lds
v_add_u32 v26,0,v20
v_add_u32 v27,0,v21
T a[112:113],26,0
T a[114:115],26,512
T a[116:117],27,0
T a[118:119],27,512
T a[120:121],26,4096
T a[122:123],26,4608
T a[124:125],27,4096
T a[126:127],27,4608
s_waitcnt vmcnt(20) lgkmcnt(0)
s_barrier
s_add_u32 s55,s50,6422528
s_add_u32 m0,s57,0
s_nop 0
buffer_load_dwordx4 v18,s[40:43],s55 offen offset:0  lds
s_add_u32 m0,s57,1024
s_nop 0
buffer_load_dwordx4 v19,s[40:43],s55 offen offset:0  lds
s_add_u32 m0,s57,8064
s_nop 0
buffer_load_dwordx4 v18,s[40:43],s55 offen offset:128  lds
s_add_u32 m0,s57,9088
s_nop 0
buffer_load_dwordx4 v19,s[40:43],s55 offen offset:128  lds
v_add_u32 v26,16384,v20
v_add_u32 v27,16384,v21
T a[128:129],26,0
T a[130:131],26,512
T a[132:133],27,0
T a[134:135],27,512
T a[136:137],26,4096
T a[138:139],26,4608
T a[140:141],27,4096
T a[142:143],27,4608
s_waitcnt vmcnt(20) lgkmcnt(0)
s_barrier
s_add_u32 s55,s50,6881280
s_add_u32 m0,s57,16384
s_nop 0
buffer_load_dwordx4 v18,s[40:43],s55 offen offset:0  lds
s_add_u32 m0,s57,17408
s_nop 0
buffer_load_dwordx4 v19,s[40:43],s55 offen offset:0  lds
s_add_u32 m0,s57,24448
s_nop 0
buffer_load_dwordx4 v18,s[40:43],s55 offen offset:128  lds
s_add_u32 m0,s57,25472
s_nop 0
buffer_load_dwordx4 v19,s[40:43],s55 offen offset:128  lds
v_add_u32 v26,32768,v20
v_add_u32 v27,32768,v21
T a[144:145],26,0
T a[146:147],26,512
T a[148:149],27,0
T a[150:151],27,512
T a[152:153],26,4096
T a[154:155],26,4608
T a[156:157],27,4096
T a[158:159],27,4608
s_waitcnt vmcnt(20) lgkmcnt(0)
s_barrier
v_add_u32 v26,49152,v20
v_add_u32 v27,49152,v21
T a[160:161],26,0
T a[162:163],26,512
T a[164:165],27,0
T a[166:167],27,512
T a[168:169],26,4096
T a[170:171],26,4608
T a[172:173],27,4096
T a[174:175],27,4608
s_waitcnt vmcnt(16) lgkmcnt(0)
s_barrier
v_add_u32 v26,65536,v20
v_add_u32 v27,65536,v21
T a[176:177],26,0
T a[178:179],26,512
T a[180:181],27,0
T a[182:183],27,512
T a[184:185],26,4096
T a[186:187],26,4608
T a[188:189],27,4096
T a[190:191],27,4608
s_waitcnt vmcnt(12) lgkmcnt(0)
s_barrier
v_add_u32 v26,81920,v20
v_add_u32 v27,81920,v21
T a[192:193],26,0
T a[194:195],26,512
T a[196:197],27,0
T a[198:199],27,512
T a[200:201],26,4096
T a[202:203],26,4608
T a[204:205],27,4096
T a[206:207],27,4608
s_waitcnt vmcnt(8) lgkmcnt(0)
s_barrier
v_add_u32 v26,98304,v20
v_add_u32 v27,98304,v21
T a[208:209],26,0
T a[210:211],26,512
T a[212:213],27,0
T a[214:215],27,512
T a[216:217],26,4096
T a[218:219],26,4608
T a[220:221],27,4096
T a[222:223],27,4608
s_waitcnt vmcnt(4) lgkmcnt(0)
s_barrier
v_add_u32 v26,0,v20
v_add_u32 v27,0,v21
T a[224:225],26,0
T a[226:227],26,512
T a[228:229],27,0
T a[230:231],27,512
T a[232:233],26,4096
T a[234:235],26,4608
T a[236:237],27,4096
T a[238:239],27,4608
s_waitcnt vmcnt(0) lgkmcnt(0)
s_barrier
v_add_u32 v26,16384,v20
v_add_u32 v27,16384,v21
T a[240:241],26,0
T a[242:243],26,512
T a[244:245],27,0
T a[246:247],27,512
T a[248:249],26,4096
T a[250:251],26,4608
T a[252:253],27,4096
T a[254:255],27,4608
L 0
s_barrier
v_mov_b32 v34,0x3f803f80
v_mov_b32 v35,0x3f803f80
v_mov_b32 v36,0x3f803f80
v_mov_b32 v37,0x3f803f80
s_nop 1
v_mfma_f32_16x16x32_bf16 v[106:109],a[0:3],v[34:37],0
v_mfma_f32_16x16x32_bf16 v[110:113],a[4:7],v[34:37],0
M 106,a[8:11],34
M 110,a[12:15],34
M 106,a[16:19],34
M 110,a[20:23],34
M 106,a[24:27],34
M 110,a[28:31],34
M 106,a[32:35],34
M 110,a[36:39],34
M 106,a[40:43],34
M 110,a[44:47],34
M 106,a[48:51],34
M 110,a[52:55],34
M 106,a[56:59],34
M 110,a[60:63],34
M 106,a[64:67],34
M 110,a[68:71],34
M 106,a[72:75],34
M 110,a[76:79],34
M 106,a[80:83],34
M 110,a[84:87],34
M 106,a[88:91],34
M 110,a[92:95],34
M 106,a[96:99],34
M 110,a[100:103],34
M 106,a[104:107],34
M 110,a[108:111],34
M 106,a[112:115],34
M 110,a[116:119],34
M 106,a[120:123],34
M 110,a[124:127],34
M 106,a[128:131],34
M 110,a[132:135],34
M 106,a[136:139],34
M 110,a[140:143],34
M 106,a[144:147],34
M 110,a[148:151],34
M 106,a[152:155],34
M 110,a[156:159],34
M 106,a[160:163],34
M 110,a[164:167],34
M 106,a[168:171],34
M 110,a[172:175],34
)"
R"(M 106,a[176:179],34
M 110,a[180:183],34
M 106,a[184:187],34
M 110,a[188:191],34
M 106,a[192:195],34
M 110,a[196:199],34
M 106,a[200:203],34
M 110,a[204:207],34
M 106,a[208:211],34
M 110,a[212:215],34
M 106,a[216:219],34
M 110,a[220:223],34
M 106,a[224:227],34
M 110,a[228:231],34
M 106,a[232:235],34
M 110,a[236:239],34
M 106,a[240:243],34
M 110,a[244:247],34
M 106,a[248:251],34
M 110,a[252:255],34
v_mov_b32 v4,s53
s_mov_b32 s82,0
s_mov_b32 s80,0
s_mov_b32 s54,s45
s_mul_i32 s57,s44,4096
s_mul_i32 s55,s45,32768
s_add_u32 s55,s55,s57
s_add_u32 s55,s55,31457280
s_mul_i32 s57,s44,4096
s_mul_i32 s60,s46,32768
s_mul_i32 s61,s46,1024
s_mul_i32 s62,s46,114688
s_mul_i32 s63,s46,2560
s_mul_i32 s64,s46,1024
s_lshl_b32 s59,s44,8
s_add_u32 s59,s59,131072
s_mul_i32 s58,s45,1024
s_add_u32 s58,s58,109613056
s_mul_i32 s65,s45,114688
s_add_u32 s65,s65,s47
s_mul_i32 s68,s45,2560
s_add_u32 s68,s68,s48
s_mul_i32 s73,s45,1024
s_add_u32 s73,s73,107479040
s_mul_i32 s74,s45,32768
s_add_u32 s74,s74,s49
s_mul_i32 s75,s45,1024
s_add_u32 s75,s75,107479040
s_mov_b32 s66,0x7fff0000
s_mov_b32 s67,0x7fff0000
s_mov_b32 s69,0x7fff0000
s_mov_b32 s76,0x7fff0000
s_mov_b32 s77,0x7fff0000
s_mov_b32 s70,0x7fff0000
s_mov_b32 s71,0x7fff0000
s_mov_b32 s72,0x7fff0000
v_mov_b32 v1,v0
v_add_u32 v2,16384,v0
s_mov_b32 s84,16384
s_mov_b32 s86,0x3a800000
s_mov_b32 s87,0x3727c5ac
s_mov_b32 s88,0xc0135761
s_mov_b32 s89,0x3d372713
s_lshr_b32 s85,s44,1
s_mul_i32 s85,s85,127
v_lshlrev_b32 v8,22,v0
v_and_b32 v8,0x80000000,v8
v_or_b32 v8,v8,v5
v_add_u32 v2,32768,v0
s_lshl_b32 s84,s57,1
s_add_u32 s84,s84,32768
s_getpc_b64 s[40:41]
Lg3o_pc_%=:
s_lshl_b32 s78,s44,13
v_lshlrev_b32 v114,3,v0
v_add_u32 v114,s78,v114
v_min_u32 v114,Lcode_g3o_e-Lg3o_pc_%=-4,v114
s_mov_b32 m0,163584
s_nop 0
global_load_lds_dword v114,s[40:41]
s_add_u32 m0,s59,4096
s_nop 0
buffer_load_dword v8,s[36:39],s58 offen offset:0 sc1 lds
buffer_store_dword v4,v3,s[36:39],s71 offen offset:0 sc0 sc1
s_add_u32 m0,s59,5120
s_nop 0
buffer_load_dword v8,s[36:39],s58 offen offset:0 sc1 lds
buffer_store_dword v4,v3,s[36:39],s71 offen offset:0 sc0 sc1
s_mov_b32 s83,0
Lg3o_poll_%=:
s_add_u32 s78,s83,2
s_and_b32 s78,s78,3
s_lshl_b32 s78,s78,10
s_add_u32 s78,s78,4096
s_add_u32 m0,s59,s78
s_nop 0
buffer_load_dword v8,s[36:39],s58 offen offset:0 sc1 lds
s_and_b32 s78,s83,3
s_lshl_b32 s78,s78,10
v_add_u32 v114,s78,v6
s_waitcnt vmcnt(4)
s_barrier
ds_read_b32 v7,v114 offset:4096
buffer_store_dword v4,v3,s[36:39],s70 offen offset:0 sc0 sc1
s_mov_b32 s70,s71
s_mov_b32 s71,s72
s_mov_b32 s72,0x7fff0000
s_add_u32 s83,s83,1
s_add_u32 s82,s82,1
L 0
v_cmp_eq_u32 vcc,s53,v7
s_nop 0
s_cmp_eq_u32 vcc_lo,-1
s_cbranch_scc1 Lg3o_go_%=
s_cmp_lt_u32 s82,20000
s_cbranch_scc1 Lg3o_poll_%=
Lg3o_go_%=:
buffer_load_dword v130,v11,s[36:39],s73 offen offset:0 sc1
s_add_u32 s73,s73,s64
s_add_u32 m0,s57,0
s_add_u32 s56,s55,0
D 0,56,0
D 0,56,1024
D 0,56,2048
D 0,56,3072
s_add_u32 s58,s58,s61
s_add_u32 m0,s59,2048
s_nop 0
buffer_load_dword v8,s[36:39],s58 offen offset:0 sc1 lds
s_add_u32 m0,s57,16384
s_add_u32 s56,s55,16384
D 0,56,0
D 0,56,1024
D 0,56,2048
D 0,56,3072
s_add_u32 s55,s55,s60
s_sub_u32 s57,s84,s57
s_add_u32 s54,s54,s46
s_waitcnt vmcnt(4)
s_barrier
buffer_store_dword v4,v3,s[36:39],s70 offen offset:0 sc0 sc1
s_min_u32 s71,s71,s72
buffer_store_dword v4,v3,s[36:39],s71 offen offset:0 sc0 sc1
s_mov_b32 s70,0x7fff0000
s_mov_b32 s71,0x7fff0000
s_mov_b32 s72,0x7fff0000
s_cmp_eq_u32 s80,1
s_cbranch_scc1 Lg3o_st1_%=
s_branch Lg3o_st0_%=
Lg3o_gox_%=:
s_add_u32 m0,s59,2048
s_nop 0
buffer_load_dword v8,s[36:39],s58 offen offset:0 sc1 lds
s_waitcnt vmcnt(0)
s_barrier
buffer_store_dword v4,v3,s[36:39],s70 offen offset:0 sc0 sc1
s_min_u32 s71,s71,s72
buffer_store_dword v4,v3,s[36:39],s71 offen offset:0 sc0 sc1
s_mov_b32 s70,0x7fff0000
s_mov_b32 s71,0x7fff0000
s_mov_b32 s72,0x7fff0000
s_cmp_eq_u32 s80,1
s_cbranch_scc1 Lg3o_st1_%=
Lg3o_st0_%=:
ds_read_b32 v7,v6 offset:2048
R 34,1,0
R 38,1,1024
R 42,1,2048
R 46,1,3072
R 50,1,4096
L 5
v_cmp_eq_u32 vcc,s53,v7
L 3
s_cmp_eq_u32 vcc_lo,-1
s_cbranch_scc1 Lg3o_F0_%=
s_branch Lg3o_L0_%=
Lg3o_st1_%=:
ds_read_b32 v7,v6 offset:2048
R 34,2,0
R 38,2,1024
R 42,2,2048
R 46,2,3072
R 50,2,4096
L 5
v_cmp_eq_u32 vcc,s53,v7
L 3
s_cmp_eq_u32 vcc_lo,-1
s_cbranch_scc1 Lg3o_F1_%=
s_branch Lg3o_L1_%=
Lg3o_F0_%=:
v_mfma_f32_16x16x32_bf16 v[22:25],a[4:7],v[34:37],0
s_add_u32 s58,s58,s61
s_add_u32 m0,s59,0
R 54,1,5120
M 22,a[12:15],38
s_add_u32 s56,s55,0
buffer_load_dword v8,s[36:39],s58 offen offset:0 sc1 lds
v_mfma_f32_16x16x32_bf16 v[18:21],a[0:3],v[34:37],0
s_add_u32 m0,s57,0
R 58,1,6144
v_add_f32_dpp v131,v130,v130 quad_perm:[1,0,3,2] row_mask:0xf bank_mask:0xf
M 18,a[8:11],38
L 3
D 0,56,0
M 18,a[16:19],42
s_mov_b32 s66,s65
R 62,1,7168
v_add_f32_dpp v131,v131,v131 quad_perm:[2,3,0,1] row_mask:0xf bank_mask:0xf
M 18,a[24:27],46
s_add_u32 s65,s65,s62
buffer_load_dword v130,v11,s[36:39],s73 offen offset:0 sc1
M 22,a[20:23],42
s_add_u32 s54,s54,s46
R 66,1,8192
v_add_f32_dpp v134,v131,v131 row_half_mirror row_mask:0xf bank_mask:0xf
M 22,a[28:31],46
L 3
D 0,56,1024
M 22,a[36:39],50
s_add_u32 s73,s73,s64
R 70,1,9216
v_mov_b32_dpp v131,v134 row_ror:8 row_mask:0xf bank_mask:0xf
M 22,a[44:47],54
v_fma_f32 v131,-v134,v134,v131
v_max_f32 v131,s87,v131
M 18,a[32:35],50
R 74,1,10240
v_fma_f32 v26,-v102,v106,v26
M 18,a[40:43],54
L 3
D 0,56,2048
M 18,a[48:51],58
R 78,1,11264
v_fma_f32 v27,-v102,v107,v27
M 18,a[56:59],62
D 0,56,3072
M 22,a[52:55],58
s_add_u32 m0,s57,16384
R 82,1,12288
v_fma_f32 v28,-v102,v108,v28
M 22,a[60:63],62
L 3
v_rsq_f32 v132,v131
M 22,a[68:71],66
s_add_u32 s56,s55,16384
R 86,1,13312
v_mul_f32 v137,v131,v132
M 22,a[76:79],70
s_add_u32 s55,s55,s60
v_mul_f32 v135,s88,v132
v_mul_f32 v131,s89,v132
M 18,a[64:67],66
s_sub_u32 s57,s84,s57
R 90,1,14336
v_mul_f32 v136,v135,v132
M 18,a[72:75],70
L 3
v_mul_f32 v136,v136,v131
v_fma_f32 v29,-v102,v109,v29
M 18,a[80:83],74
s_waitcnt vmcnt(8)
R 94,1,15360
v_mul_f32 v114,v26,v26
M 18,a[88:91],78
s_barrier
ds_write_b128 v12,v[134:137] offset:0
M 22,a[84:87],74
R 34,1,16384
v_mul_f32 v115,v27,v27
M 22,a[92:95],78
L 4
v_mul_f32 v116,v28,v28
v_mul_f32 v117,v29,v29
M 22,a[100:103],82
R 38,1,17408
v_fma_f32 v114,v104,v114,v103
M 22,a[108:111],86
D 0,56,0
M 18,a[96:99],82
R 42,1,18432
v_fma_f32 v115,v104,v115,v103
M 18,a[104:107],86
L 4
v_fma_f32 v116,v104,v116,v103
v_fma_f32 v117,v104,v117,v103
M 18,a[112:115],90
R 46,1,19456
v_mul_f32 v114,v26,v114
M 18,a[120:123],94
v_exp_f32 v114,v114
M 22,a[116:119],90
R 50,1,20480
v_mul_f32 v115,v27,v115
M 22,a[124:127],94
L 3
v_exp_f32 v115,v115
M 22,a[132:135],34
R 54,1,21504
v_mul_f32 v116,v28,v116
M 22,a[140:143],38
D 0,56,1024
M 18,a[128:131],34
R 58,1,22528
v_mul_f32 v117,v29,v117
M 18,a[136:139],38
L 3
v_exp_f32 v116,v116
M 18,a[144:147],42
R 62,1,23552
v_fma_f32 v114,v114,v105,v105
M 18,a[152:155],46
v_exp_f32 v117,v117
M 22,a[148:151],42
R 66,1,24576
v_fma_f32 v115,v115,v105,v105
M 22,a[156:159],46
L 3
D 0,56,2048
M 22,a[164:167],50
R 70,1,25600
v_fma_f32 v116,v116,v105,v105
M 22,a[172:175],54
v_rcp_f32 v114,v114
M 18,a[160:163],50
R 74,1,26624
v_fma_f32 v117,v117,v105,v105
M 18,a[168:171],54
L 3
v_rcp_f32 v115,v115
M 18,a[176:179],58
R 78,1,27648
v_mul_f32 v26,v26,v114
M 18,a[184:187],62
v_rcp_f32 v116,v116
M 22,a[180:183],58
R 82,1,28672
v_mul_f32 v27,v27,v115
M 22,a[188:191],62
L 3
D 0,56,3072
M 22,a[196:199],66
R 86,1,29696
v_mul_f32 v28,v28,v116
M 22,a[204:207],70
v_rcp_f32 v117,v117
M 18,a[192:195],66
R 90,1,30720
v_mul_f32 v29,v29,v117
M 18,a[200:203],70
L 3
s_nop 0
v_cvt_pk_bf16_f32 v122,v26,v27
M 18,a[208:211],74
s_waitcnt vmcnt(4)
R 94,1,31744
v_fma_f32 v30,-v102,v110,v30
M 18,a[216:219],78
s_barrier
ds_read_b32 v7,v6 offset:0
R 98,13,0
M 22,a[212:215],74
R 34,2,0
v_fma_f32 v31,-v102,v111,v31
M 22,a[220:223],78
L 5
buffer_store_dword v4,v3,s[36:39],s70 offen offset:0 sc0 sc1
M 22,a[228:231],82
s_mov_b32 s70,s69
R 38,2,1024
v_fma_f32 v32,-v102,v112,v32
M 22,a[236:239],86
s_mov_b32 s69,s68
v_fma_f32 v33,-v102,v113,v33
v_mul_f32 v118,v30,v30
M 18,a[224:227],82
s_add_u32 s68,s68,s63
R 42,2,2048
v_mul_f32 v119,v31,v31
M 18,a[232:235],86
L 5
v_mul_f32 v120,v32,v32
v_mul_f32 v121,v33,v33
v_fma_f32 v118,v104,v118,v103
v_fma_f32 v119,v104,v119,v103
v_fma_f32 v120,v104,v120,v103
v_fma_f32 v121,v104,v121,v103
v_mul_f32 v118,v30,v118
v_mul_f32 v119,v31,v119
v_mul_f32 v120,v32,v120
v_mul_f32 v121,v33,v121
v_exp_f32 v118,v118
v_exp_f32 v119,v119
v_exp_f32 v120,v120
v_exp_f32 v121,v121
M 18,a[240:243],90
L 4
R 46,2,3072
v_fma_f32 v118,v118,v105,v105
v_fma_f32 v119,v119,v105,v105
v_fma_f32 v120,v120,v105,v105
v_fma_f32 v121,v121,v105,v105
v_rcp_f32 v118,v118
v_rcp_f32 v119,v119
v_rcp_f32 v120,v120
v_rcp_f32 v121,v121
M 18,a[248:251],94
v_cvt_pk_bf16_f32 v123,v28,v29
v_mul_f32 v30,v30,v118
v_mul_f32 v31,v31,v119
v_mul_f32 v32,v32,v120
v_mul_f32 v33,v33,v121
v_cvt_pk_bf16_f32 v124,v30,v31
v_cvt_pk_bf16_f32 v125,v32,v33
v_cmp_eq_u32 vcc,s53,v7
M 22,a[244:247],90
s_cmp_eq_u32 vcc_lo,-1
R 50,2,4096
buffer_store_dwordx4 v[122:125],v0,s[36:39],s67 offen offset:0 sc0 sc1
M 22,a[252:255],94
L 3
s_cbranch_scc0 Lg3o_L1_%=
Lg3o_F1_%=:
v_mfma_f32_16x16x32_bf16 v[30:33],a[4:7],v[34:37],0
s_add_u32 s58,s58,s61
s_add_u32 m0,s59,1024
R 54,2,5120
M 30,a[12:15],38
s_add_u32 s56,s55,0
buffer_load_dword v8,s[36:39],s58 offen offset:0 sc1 lds
v_mfma_f32_16x16x32_bf16 v[26:29],a[0:3],v[34:37],0
s_add_u32 m0,s57,0
R 58,2,6144
v_add_f32_dpp v131,v130,v130 quad_perm:[1,0,3,2] row_mask:0xf bank_mask:0xf
M 26,a[8:11],38
L 3
D 0,56,0
M 26,a[16:19],42
s_mov_b32 s67,s65
R 62,2,7168
v_add_f32_dpp v131,v131,v131 quad_perm:[2,3,0,1] row_mask:0xf bank_mask:0xf
M 26,a[24:27],46
s_add_u32 s65,s65,s62
buffer_load_dword v130,v11,s[36:39],s73 offen offset:0 sc1
M 30,a[20:23],42
s_add_u32 s54,s54,s46
R 66,2,8192
v_add_f32_dpp v134,v131,v131 row_half_mirror row_mask:0xf bank_mask:0xf
M 30,a[28:31],46
L 3
D 0,56,1024
M 30,a[36:39],50
s_add_u32 s73,s73,s64
R 70,2,9216
v_mov_b32_dpp v131,v134 row_ror:8 row_mask:0xf bank_mask:0xf
M 30,a[44:47],54
v_fma_f32 v131,-v134,v134,v131
v_max_f32 v131,s87,v131
M 26,a[32:35],50
R 74,2,10240
v_fma_f32 v18,-v98,v106,v18
M 26,a[40:43],54
L 3
D 0,56,2048
M 26,a[48:51],58
R 78,2,11264
v_fma_f32 v19,-v98,v107,v19
M 26,a[56:59],62
D 0,56,3072
M 30,a[52:55],58
s_add_u32 m0,s57,16384
R 82,2,12288
v_fma_f32 v20,-v98,v108,v20
M 30,a[60:63],62
L 3
v_rsq_f32 v132,v131
M 30,a[68:71],66
s_add_u32 s56,s55,16384
R 86,2,13312
v_mul_f32 v137,v131,v132
M 30,a[76:79],70
s_add_u32 s55,s55,s60
v_mul_f32 v135,s88,v132
v_mul_f32 v131,s89,v132
M 26,a[64:67],66
s_sub_u32 s57,s84,s57
R 90,2,14336
v_mul_f32 v136,v135,v132
M 26,a[72:75],70
L 3
v_mul_f32 v136,v136,v131
v_fma_f32 v21,-v98,v109,v21
M 26,a[80:83],74
s_waitcnt vmcnt(8)
R 94,2,15360
v_mul_f32 v114,v18,v18
M 26,a[88:91],78
s_barrier
ds_write_b128 v12,v[134:137] offset:2048
M 30,a[84:87],74
R 34,2,16384
)"
R"(v_mul_f32 v115,v19,v19
M 30,a[92:95],78
L 4
v_mul_f32 v116,v20,v20
v_mul_f32 v117,v21,v21
M 30,a[100:103],82
R 38,2,17408
v_fma_f32 v114,v100,v114,v99
M 30,a[108:111],86
D 0,56,0
M 26,a[96:99],82
R 42,2,18432
v_fma_f32 v115,v100,v115,v99
M 26,a[104:107],86
L 4
v_fma_f32 v116,v100,v116,v99
v_fma_f32 v117,v100,v117,v99
M 26,a[112:115],90
R 46,2,19456
v_mul_f32 v114,v18,v114
M 26,a[120:123],94
v_exp_f32 v114,v114
M 30,a[116:119],90
R 50,2,20480
v_mul_f32 v115,v19,v115
M 30,a[124:127],94
L 3
v_exp_f32 v115,v115
M 30,a[132:135],34
R 54,2,21504
v_mul_f32 v116,v20,v116
M 30,a[140:143],38
D 0,56,1024
M 26,a[128:131],34
R 58,2,22528
v_mul_f32 v117,v21,v117
M 26,a[136:139],38
L 3
v_exp_f32 v116,v116
M 26,a[144:147],42
R 62,2,23552
v_fma_f32 v114,v114,v101,v101
M 26,a[152:155],46
v_exp_f32 v117,v117
M 30,a[148:151],42
R 66,2,24576
v_fma_f32 v115,v115,v101,v101
M 30,a[156:159],46
L 3
D 0,56,2048
M 30,a[164:167],50
R 70,2,25600
v_fma_f32 v116,v116,v101,v101
M 30,a[172:175],54
v_rcp_f32 v114,v114
M 26,a[160:163],50
R 74,2,26624
v_fma_f32 v117,v117,v101,v101
M 26,a[168:171],54
L 3
v_rcp_f32 v115,v115
M 26,a[176:179],58
R 78,2,27648
v_mul_f32 v18,v18,v114
M 26,a[184:187],62
v_rcp_f32 v116,v116
M 30,a[180:183],58
R 82,2,28672
v_mul_f32 v19,v19,v115
M 30,a[188:191],62
L 3
D 0,56,3072
M 30,a[196:199],66
R 86,2,29696
v_mul_f32 v20,v20,v116
M 30,a[204:207],70
v_rcp_f32 v117,v117
M 26,a[192:195],66
R 90,2,30720
v_mul_f32 v21,v21,v117
M 26,a[200:203],70
L 3
s_nop 0
v_cvt_pk_bf16_f32 v122,v18,v19
M 26,a[208:211],74
s_waitcnt vmcnt(4)
R 94,2,31744
v_fma_f32 v22,-v98,v110,v22
M 26,a[216:219],78
s_barrier
ds_read_b32 v7,v6 offset:1024
R 102,13,2048
M 30,a[212:215],74
R 34,1,0
v_fma_f32 v23,-v98,v111,v23
M 30,a[220:223],78
L 5
buffer_store_dword v4,v3,s[36:39],s70 offen offset:0 sc0 sc1
M 30,a[228:231],82
s_mov_b32 s70,s69
R 38,1,1024
v_fma_f32 v24,-v98,v112,v24
M 30,a[236:239],86
s_mov_b32 s69,s68
v_fma_f32 v25,-v98,v113,v25
v_mul_f32 v118,v22,v22
M 26,a[224:227],82
s_add_u32 s68,s68,s63
R 42,1,2048
v_mul_f32 v119,v23,v23
M 26,a[232:235],86
L 5
v_mul_f32 v120,v24,v24
v_mul_f32 v121,v25,v25
v_fma_f32 v118,v100,v118,v99
v_fma_f32 v119,v100,v119,v99
v_fma_f32 v120,v100,v120,v99
v_fma_f32 v121,v100,v121,v99
v_mul_f32 v118,v22,v118
v_mul_f32 v119,v23,v119
v_mul_f32 v120,v24,v120
v_mul_f32 v121,v25,v121
v_exp_f32 v118,v118
v_exp_f32 v119,v119
v_exp_f32 v120,v120
v_exp_f32 v121,v121
M 26,a[240:243],90
L 4
R 46,1,3072
v_fma_f32 v118,v118,v101,v101
v_fma_f32 v119,v119,v101,v101
v_fma_f32 v120,v120,v101,v101
v_fma_f32 v121,v121,v101,v101
v_rcp_f32 v118,v118
v_rcp_f32 v119,v119
v_rcp_f32 v120,v120
v_rcp_f32 v121,v121
M 26,a[248:251],94
v_cvt_pk_bf16_f32 v123,v20,v21
v_mul_f32 v22,v22,v118
v_mul_f32 v23,v23,v119
v_mul_f32 v24,v24,v120
v_mul_f32 v25,v25,v121
v_cvt_pk_bf16_f32 v124,v22,v23
v_cvt_pk_bf16_f32 v125,v24,v25
v_cmp_eq_u32 vcc,s53,v7
M 30,a[244:247],90
s_cmp_eq_u32 vcc_lo,-1
R 50,1,4096
buffer_store_dwordx4 v[122:125],v0,s[36:39],s66 offen offset:0 sc0 sc1
M 30,a[252:255],94
L 3
s_cbranch_scc1 Lg3o_F0_%=
Lg3o_L0_%=:
v_mfma_f32_16x16x32_bf16 v[22:25],a[4:7],v[34:37],0
s_add_u32 m0,s59,3072
R 54,1,5120
v_add_f32_dpp v131,v130,v130 quad_perm:[1,0,3,2] row_mask:0xf bank_mask:0xf
M 22,a[12:15],38
s_mov_b32 s66,s65
buffer_load_dword v8,s[36:39],s58 offen offset:0 sc1 lds
v_mfma_f32_16x16x32_bf16 v[18:21],a[0:3],v[34:37],0
s_add_u32 s65,s65,s62
R 58,1,6144
v_add_f32_dpp v131,v131,v131 quad_perm:[2,3,0,1] row_mask:0xf bank_mask:0xf
M 18,a[8:11],38
L 3
v_fma_f32 v26,-v102,v106,v26
v_fma_f32 v27,-v102,v107,v27
M 18,a[16:19],42
R 62,1,7168
v_add_f32_dpp v134,v131,v131 row_half_mirror row_mask:0xf bank_mask:0xf
M 18,a[24:27],46
v_fma_f32 v28,-v102,v108,v28
v_fma_f32 v29,-v102,v109,v29
M 22,a[20:23],42
R 66,1,8192
v_mov_b32_dpp v131,v134 row_ror:8 row_mask:0xf bank_mask:0xf
M 22,a[28:31],46
L 3
v_fma_f32 v131,-v134,v134,v131
v_max_f32 v131,s87,v131
M 22,a[36:39],50
R 70,1,9216
v_mul_f32 v114,v26,v26
M 22,a[44:47],54
v_rsq_f32 v132,v131
M 18,a[32:35],50
R 74,1,10240
v_mul_f32 v137,v131,v132
M 18,a[40:43],54
L 3
v_mul_f32 v135,s88,v132
v_mul_f32 v131,s89,v132
M 18,a[48:51],58
R 78,1,11264
v_mul_f32 v136,v135,v132
M 18,a[56:59],62
v_mul_f32 v136,v136,v131
v_mul_f32 v115,v27,v27
M 22,a[52:55],58
R 82,1,12288
v_mul_f32 v116,v28,v28
M 22,a[60:63],62
L 3
ds_write_b128 v12,v[134:137] offset:0
M 22,a[68:71],66
R 86,1,13312
v_mul_f32 v117,v29,v29
M 22,a[76:79],70
v_fma_f32 v114,v104,v114,v103
v_fma_f32 v115,v104,v115,v103
M 18,a[64:67],66
R 90,1,14336
v_fma_f32 v116,v104,v116,v103
M 18,a[72:75],70
L 4
v_fma_f32 v117,v104,v117,v103
v_mul_f32 v114,v26,v114
M 18,a[80:83],74
s_waitcnt vmcnt(0)
R 94,1,15360
v_mul_f32 v115,v27,v115
M 18,a[88:91],78
s_barrier
ds_read_b32 v7,v6 offset:3072
v_mul_f32 v116,v28,v116
M 22,a[84:87],74
R 34,1,16384
v_mul_f32 v117,v29,v117
M 22,a[92:95],78
L 4
buffer_store_dword v4,v3,s[36:39],s70 offen offset:0 sc0 sc1
M 22,a[100:103],82
s_mov_b32 s70,0x7fff0000
R 38,1,17408
v_fma_f32 v30,-v102,v110,v30
M 22,a[108:111],86
v_exp_f32 v114,v114
M 18,a[96:99],82
R 42,1,18432
v_fma_f32 v114,v114,v105,v105
M 18,a[104:107],86
L 4
L 3
v_cmp_eq_u32 vcc,s53,v7
M 18,a[112:115],90
R 46,1,19456
v_fma_f32 v31,-v102,v111,v31
M 18,a[120:123],94
s_cmp_eq_u32 vcc_lo,-1
v_exp_f32 v115,v115
s_cbranch_scc0 Lg3o_X0_%=
M 22,a[116:119],90
s_add_u32 m0,s57,0
s_add_u32 s56,s55,0
s_add_u32 s58,s58,s61
R 50,1,20480
M 22,a[124:127],94
L 3
buffer_load_dword v130,v11,s[36:39],s73 offen offset:0 sc1
M 22,a[132:135],34
s_add_u32 s54,s54,s46
R 54,1,21504
v_fma_f32 v115,v115,v105,v105
M 22,a[140:143],38
s_add_u32 s73,s73,s64
D 0,56,0
M 18,a[128:131],34
R 58,1,22528
v_fma_f32 v32,-v102,v112,v32
M 18,a[136:139],38
L 3
v_exp_f32 v116,v116
M 18,a[144:147],42
R 62,1,23552
v_fma_f32 v116,v116,v105,v105
M 18,a[152:155],46
D 0,56,1024
M 22,a[148:151],42
R 66,1,24576
v_fma_f32 v33,-v102,v113,v33
M 22,a[156:159],46
L 3
v_exp_f32 v117,v117
M 22,a[164:167],50
R 70,1,25600
v_fma_f32 v117,v117,v105,v105
M 22,a[172:175],54
D 0,56,2048
M 18,a[160:163],50
R 74,1,26624
v_mul_f32 v118,v30,v30
M 18,a[168:171],54
L 3
v_rcp_f32 v114,v114
M 18,a[176:179],58
R 78,1,27648
v_mul_f32 v26,v26,v114
M 18,a[184:187],62
D 0,56,3072
M 22,a[180:183],58
s_add_u32 m0,s59,0
R 82,1,28672
v_mul_f32 v119,v31,v31
M 22,a[188:191],62
L 3
buffer_load_dword v8,s[36:39],s58 offen offset:0 sc1 lds
M 22,a[196:199],66
s_add_u32 m0,s57,16384
R 86,1,29696
v_mul_f32 v120,v32,v32
M 22,a[204:207],70
s_add_u32 s56,s55,16384
v_rcp_f32 v115,v115
M 18,a[192:195],66
s_add_u32 s55,s55,s60
R 90,1,30720
v_mul_f32 v27,v27,v115
M 18,a[200:203],70
L 3
s_nop 0
D 0,56,0
M 18,a[208:211],74
s_waitcnt vmcnt(1)
R 94,1,31744
v_cvt_pk_bf16_f32 v122,v26,v27
M 18,a[216:219],78
s_barrier
ds_read_b32 v7,v6 offset:0
R 98,13,0
M 22,a[212:215],74
s_sub_u32 s57,s84,s57
R 34,2,0
v_mul_f32 v121,v33,v33
M 22,a[220:223],78
L 5
buffer_store_dword v4,v3,s[36:39],s70 offen offset:0 sc0 sc1
M 22,a[228:231],82
s_mov_b32 s70,s69
R 38,2,1024
v_fma_f32 v118,v104,v118,v103
M 22,a[236:239],86
s_mov_b32 s69,s68
D 0,56,1024
M 18,a[224:227],82
s_add_u32 s68,s68,s63
R 42,2,2048
v_fma_f32 v119,v104,v119,v103
M 18,a[232:235],86
L 5
v_fma_f32 v120,v104,v120,v103
v_fma_f32 v121,v104,v121,v103
v_mul_f32 v118,v30,v118
v_mul_f32 v119,v31,v119
v_mul_f32 v120,v32,v120
v_mul_f32 v121,v33,v121
v_exp_f32 v118,v118
v_exp_f32 v119,v119
v_exp_f32 v120,v120
v_exp_f32 v121,v121
M 18,a[240:243],90
L 4
R 46,2,3072
v_rcp_f32 v116,v116
v_rcp_f32 v117,v117
v_fma_f32 v118,v118,v105,v105
v_fma_f32 v119,v119,v105,v105
v_fma_f32 v120,v120,v105,v105
v_fma_f32 v121,v121,v105,v105
v_rcp_f32 v118,v118
v_rcp_f32 v119,v119
v_rcp_f32 v120,v120
v_rcp_f32 v121,v121
M 18,a[248:251],94
v_mul_f32 v28,v28,v116
v_mul_f32 v29,v29,v117
v_cvt_pk_bf16_f32 v123,v28,v29
v_mul_f32 v30,v30,v118
v_mul_f32 v31,v31,v119
v_mul_f32 v32,v32,v120
v_mul_f32 v33,v33,v121
v_cvt_pk_bf16_f32 v124,v30,v31
v_cvt_pk_bf16_f32 v125,v32,v33
v_cmp_eq_u32 vcc,s53,v7
M 22,a[244:247],90
s_cmp_eq_u32 vcc_lo,-1
R 50,2,4096
buffer_store_dwordx4 v[122:125],v0,s[36:39],s67 offen offset:0 sc0 sc1
M 22,a[252:255],94
L 3
D 0,56,2048
D 0,56,3072
buffer_store_dword v4,v3,s[36:39],s71 offen offset:0 sc0 sc1
buffer_store_dword v4,v3,s[36:39],s71 offen offset:0 sc0 sc1
s_cbranch_scc1 Lg3o_F1_%=
s_branch Lg3o_L1_%=
Lg3o_X0_%=:
M 22,a[116:119],90
s_add_u32 m0,s59,0
R 50,1,20480
v_fma_f32 v115,v115,v105,v105
M 22,a[124:127],94
L 3
buffer_load_dword v8,s[36:39],s58 offen offset:0 sc1 lds
M 22,a[132:135],34
R 54,1,21504
v_fma_f32 v32,-v102,v112,v32
M 22,a[140:143],38
v_exp_f32 v116,v116
M 18,a[128:131],34
R 58,1,22528
v_fma_f32 v116,v116,v105,v105
M 18,a[136:139],38
L 3
v_exp_f32 v117,v117
M 18,a[144:147],42
R 62,1,23552
v_fma_f32 v117,v117,v105,v105
M 18,a[152:155],46
v_rcp_f32 v114,v114
M 22,a[148:151],42
R 66,1,24576
v_mul_f32 v26,v26,v114
M 22,a[156:159],46
L 3
v_rcp_f32 v115,v115
M 22,a[164:167],50
R 70,1,25600
v_mul_f32 v27,v27,v115
M 22,a[172:175],54
v_rcp_f32 v116,v116
M 18,a[160:163],50
R 74,1,26624
v_mul_f32 v28,v28,v116
M 18,a[168:171],54
L 3
v_rcp_f32 v117,v117
M 18,a[176:179],58
R 78,1,27648
v_mul_f32 v29,v29,v117
M 18,a[184:187],62
v_cvt_pk_bf16_f32 v122,v26,v27
v_cvt_pk_bf16_f32 v123,v28,v29
M 22,a[180:183],58
R 82,1,28672
v_fma_f32 v33,-v102,v113,v33
M 22,a[188:191],62
L 3
v_mul_f32 v118,v30,v30
v_mul_f32 v119,v31,v31
M 22,a[196:199],66
R 86,1,29696
v_mul_f32 v120,v32,v32
M 22,a[204:207],70
v_mul_f32 v121,v33,v33
v_fma_f32 v118,v104,v118,v103
M 18,a[192:195],66
R 90,1,30720
v_fma_f32 v119,v104,v119,v103
M 18,a[200:203],70
L 3
s_nop 0
v_fma_f32 v120,v104,v120,v103
M 18,a[208:211],74
s_waitcnt vmcnt(0)
R 94,1,31744
v_fma_f32 v121,v104,v121,v103
M 18,a[216:219],78
s_barrier
buffer_store_dword v4,v3,s[36:39],s70 offen offset:0 sc0 sc1
M 22,a[212:215],74
s_mov_b32 s70,s69
ds_read_b32 v7,v6 offset:0
R 98,13,0
M 22,a[220:223],78
L 4
v_mul_f32 v118,v30,v118
v_mul_f32 v119,v31,v119
M 22,a[228:231],82
s_mov_b32 s69,s68
v_mul_f32 v120,v32,v120
v_mul_f32 v121,v33,v121
M 22,a[236:239],86
s_add_u32 s68,s68,s63
v_exp_f32 v118,v118
M 18,a[224:227],82
v_exp_f32 v119,v119
M 18,a[232:235],86
L 2
v_exp_f32 v120,v120
v_exp_f32 v121,v121
M 18,a[240:243],90
L 1
v_fma_f32 v118,v118,v105,v105
v_fma_f32 v119,v119,v105,v105
v_fma_f32 v120,v120,v105,v105
v_fma_f32 v121,v121,v105,v105
v_rcp_f32 v118,v118
v_rcp_f32 v119,v119
)"
R"(v_rcp_f32 v120,v120
v_rcp_f32 v121,v121
M 18,a[248:251],94
v_mul_f32 v30,v30,v118
v_mul_f32 v31,v31,v119
v_mul_f32 v32,v32,v120
v_mul_f32 v33,v33,v121
v_cvt_pk_bf16_f32 v124,v30,v31
v_cvt_pk_bf16_f32 v125,v32,v33
M 22,a[244:247],90
buffer_store_dwordx4 v[122:125],v0,s[36:39],s67 offen offset:0 sc0 sc1
v_cmp_eq_u32 vcc,s53,v7
M 22,a[252:255],94
s_mov_b32 s80,1
s_cmp_eq_u32 vcc_lo,-1
s_cbranch_scc0 Lg3o_xn0_%=
buffer_load_dword v130,v11,s[36:39],s73 offen offset:0 sc1
L 0
v_fma_f32 v18,-v98,v106,v18
v_fma_f32 v19,-v98,v107,v19
v_fma_f32 v20,-v98,v108,v20
v_fma_f32 v21,-v98,v109,v21
v_mul_f32 v114,v18,v18
v_mul_f32 v115,v19,v19
s_add_u32 s73,s73,s64
s_add_u32 m0,s57,0
s_add_u32 s56,s55,0
D 0,56,0
v_mul_f32 v116,v20,v20
v_mul_f32 v117,v21,v21
v_fma_f32 v114,v100,v114,v99
v_fma_f32 v115,v100,v115,v99
v_fma_f32 v116,v100,v116,v99
v_fma_f32 v117,v100,v117,v99
D 0,56,1024
v_mul_f32 v114,v18,v114
v_mul_f32 v115,v19,v115
v_mul_f32 v116,v20,v116
v_mul_f32 v117,v21,v117
v_exp_f32 v114,v114
v_exp_f32 v115,v115
D 0,56,2048
v_exp_f32 v116,v116
v_exp_f32 v117,v117
v_fma_f32 v114,v114,v101,v101
v_fma_f32 v115,v115,v101,v101
v_fma_f32 v116,v116,v101,v101
v_fma_f32 v117,v117,v101,v101
D 0,56,3072
v_rcp_f32 v114,v114
v_rcp_f32 v115,v115
v_rcp_f32 v116,v116
v_rcp_f32 v117,v117
v_mul_f32 v18,v18,v114
v_mul_f32 v19,v19,v115
s_add_u32 s58,s58,s61
s_add_u32 m0,s57,16384
s_add_u32 s56,s55,16384
D 0,56,0
v_mul_f32 v20,v20,v116
v_mul_f32 v21,v21,v117
v_cvt_pk_bf16_f32 v122,v18,v19
v_cvt_pk_bf16_f32 v123,v20,v21
v_fma_f32 v22,-v98,v110,v22
v_fma_f32 v23,-v98,v111,v23
D 0,56,1024
v_fma_f32 v24,-v98,v112,v24
v_fma_f32 v25,-v98,v113,v25
v_mul_f32 v118,v22,v22
v_mul_f32 v119,v23,v23
v_mul_f32 v120,v24,v24
v_mul_f32 v121,v25,v25
D 0,56,2048
v_fma_f32 v118,v100,v118,v99
v_fma_f32 v119,v100,v119,v99
v_fma_f32 v120,v100,v120,v99
v_fma_f32 v121,v100,v121,v99
v_mul_f32 v118,v22,v118
v_mul_f32 v119,v23,v119
D 0,56,3072
v_mul_f32 v120,v24,v120
v_mul_f32 v121,v25,v121
v_exp_f32 v118,v118
v_exp_f32 v119,v119
v_exp_f32 v120,v120
v_exp_f32 v121,v121
s_add_u32 s55,s55,s60
s_sub_u32 s57,s84,s57
s_add_u32 s54,s54,s46
v_fma_f32 v118,v118,v101,v101
v_fma_f32 v119,v119,v101,v101
v_fma_f32 v120,v120,v101,v101
v_fma_f32 v121,v121,v101,v101
v_rcp_f32 v118,v118
v_rcp_f32 v119,v119
v_rcp_f32 v120,v120
v_rcp_f32 v121,v121
v_mul_f32 v22,v22,v118
v_mul_f32 v23,v23,v119
v_mul_f32 v24,v24,v120
v_mul_f32 v25,v25,v121
v_cvt_pk_bf16_f32 v124,v22,v23
v_cvt_pk_bf16_f32 v125,v24,v25
buffer_store_dwordx4 v[122:125],v0,s[36:39],s66 offen offset:0 sc0 sc1
s_mov_b32 s72,s69
s_mov_b32 s69,0x7fff0000
s_mov_b32 s66,0x7fff0000
s_mov_b32 s76,0x7fff0000
s_branch Lg3o_gox_%=
Lg3o_xn0_%=:
s_cmp_ge_u32 s54,256
s_cbranch_scc1 Lg3o_fin0_%=
s_add_u32 m0,s59,4096
s_nop 0
buffer_load_dword v8,s[36:39],s58 offen offset:0 sc1 lds
buffer_store_dword v4,v3,s[36:39],s71 offen offset:0 sc0 sc1
s_add_u32 m0,s59,5120
s_nop 0
buffer_load_dword v8,s[36:39],s58 offen offset:0 sc1 lds
buffer_store_dword v4,v3,s[36:39],s71 offen offset:0 sc0 sc1
s_mov_b32 s83,0
L 0
v_fma_f32 v18,-v98,v106,v18
v_fma_f32 v19,-v98,v107,v19
v_fma_f32 v20,-v98,v108,v20
v_fma_f32 v21,-v98,v109,v21
v_mul_f32 v114,v18,v18
v_mul_f32 v115,v19,v19
v_mul_f32 v116,v20,v20
v_mul_f32 v117,v21,v21
v_fma_f32 v114,v100,v114,v99
v_fma_f32 v115,v100,v115,v99
v_fma_f32 v116,v100,v116,v99
v_fma_f32 v117,v100,v117,v99
v_mul_f32 v114,v18,v114
v_mul_f32 v115,v19,v115
v_mul_f32 v116,v20,v116
v_mul_f32 v117,v21,v117
v_exp_f32 v114,v114
v_exp_f32 v115,v115
v_exp_f32 v116,v116
v_exp_f32 v117,v117
v_fma_f32 v114,v114,v101,v101
v_fma_f32 v115,v115,v101,v101
v_fma_f32 v116,v116,v101,v101
v_fma_f32 v117,v117,v101,v101
v_rcp_f32 v114,v114
v_rcp_f32 v115,v115
v_rcp_f32 v116,v116
v_rcp_f32 v117,v117
v_mul_f32 v18,v18,v114
v_mul_f32 v19,v19,v115
v_mul_f32 v20,v20,v116
v_mul_f32 v21,v21,v117
v_cvt_pk_bf16_f32 v122,v18,v19
v_cvt_pk_bf16_f32 v123,v20,v21
v_fma_f32 v22,-v98,v110,v22
v_fma_f32 v23,-v98,v111,v23
v_fma_f32 v24,-v98,v112,v24
v_fma_f32 v25,-v98,v113,v25
v_mul_f32 v118,v22,v22
v_mul_f32 v119,v23,v23
v_mul_f32 v120,v24,v24
v_mul_f32 v121,v25,v25
v_fma_f32 v118,v100,v118,v99
v_fma_f32 v119,v100,v119,v99
v_fma_f32 v120,v100,v120,v99
v_fma_f32 v121,v100,v121,v99
v_mul_f32 v118,v22,v118
v_mul_f32 v119,v23,v119
v_mul_f32 v120,v24,v120
v_mul_f32 v121,v25,v121
v_exp_f32 v118,v118
v_exp_f32 v119,v119
v_exp_f32 v120,v120
v_exp_f32 v121,v121
v_fma_f32 v118,v118,v101,v101
v_fma_f32 v119,v119,v101,v101
v_fma_f32 v120,v120,v101,v101
v_fma_f32 v121,v121,v101,v101
v_rcp_f32 v118,v118
v_rcp_f32 v119,v119
v_rcp_f32 v120,v120
v_rcp_f32 v121,v121
v_mul_f32 v22,v22,v118
v_mul_f32 v23,v23,v119
v_mul_f32 v24,v24,v120
v_mul_f32 v25,v25,v121
v_cvt_pk_bf16_f32 v124,v22,v23
v_cvt_pk_bf16_f32 v125,v24,v25
buffer_store_dwordx4 v[122:125],v0,s[36:39],s66 offen offset:0 sc0 sc1
s_mov_b32 s72,s69
s_mov_b32 s69,0x7fff0000
s_mov_b32 s66,0x7fff0000
s_mov_b32 s76,0x7fff0000
s_branch Lg3o_poll_%=
Lg3o_fin0_%=:
L 0
v_fma_f32 v18,-v98,v106,v18
v_fma_f32 v19,-v98,v107,v19
v_fma_f32 v20,-v98,v108,v20
v_fma_f32 v21,-v98,v109,v21
v_mul_f32 v114,v18,v18
v_mul_f32 v115,v19,v19
v_mul_f32 v116,v20,v20
v_mul_f32 v117,v21,v21
v_fma_f32 v114,v100,v114,v99
v_fma_f32 v115,v100,v115,v99
v_fma_f32 v116,v100,v116,v99
v_fma_f32 v117,v100,v117,v99
v_mul_f32 v114,v18,v114
v_mul_f32 v115,v19,v115
v_mul_f32 v116,v20,v116
v_mul_f32 v117,v21,v117
v_exp_f32 v114,v114
v_exp_f32 v115,v115
v_exp_f32 v116,v116
v_exp_f32 v117,v117
v_fma_f32 v114,v114,v101,v101
v_fma_f32 v115,v115,v101,v101
v_fma_f32 v116,v116,v101,v101
v_fma_f32 v117,v117,v101,v101
v_rcp_f32 v114,v114
v_rcp_f32 v115,v115
v_rcp_f32 v116,v116
v_rcp_f32 v117,v117
v_mul_f32 v18,v18,v114
v_mul_f32 v19,v19,v115
v_mul_f32 v20,v20,v116
v_mul_f32 v21,v21,v117
v_cvt_pk_bf16_f32 v122,v18,v19
v_cvt_pk_bf16_f32 v123,v20,v21
v_fma_f32 v22,-v98,v110,v22
v_fma_f32 v23,-v98,v111,v23
v_fma_f32 v24,-v98,v112,v24
v_fma_f32 v25,-v98,v113,v25
v_mul_f32 v118,v22,v22
v_mul_f32 v119,v23,v23
v_mul_f32 v120,v24,v24
v_mul_f32 v121,v25,v25
v_fma_f32 v118,v100,v118,v99
v_fma_f32 v119,v100,v119,v99
v_fma_f32 v120,v100,v120,v99
v_fma_f32 v121,v100,v121,v99
v_mul_f32 v118,v22,v118
v_mul_f32 v119,v23,v119
v_mul_f32 v120,v24,v120
v_mul_f32 v121,v25,v121
v_exp_f32 v118,v118
v_exp_f32 v119,v119
v_exp_f32 v120,v120
v_exp_f32 v121,v121
v_fma_f32 v118,v118,v101,v101
v_fma_f32 v119,v119,v101,v101
v_fma_f32 v120,v120,v101,v101
v_fma_f32 v121,v121,v101,v101
v_rcp_f32 v118,v118
v_rcp_f32 v119,v119
v_rcp_f32 v120,v120
v_rcp_f32 v121,v121
v_mul_f32 v22,v22,v118
v_mul_f32 v23,v23,v119
v_mul_f32 v24,v24,v120
v_mul_f32 v25,v25,v121
v_cvt_pk_bf16_f32 v124,v22,v23
v_cvt_pk_bf16_f32 v125,v24,v25
buffer_store_dwordx4 v[122:125],v0,s[36:39],s66 offen offset:0 sc0 sc1
s_mov_b32 s72,s69
s_mov_b32 s69,0x7fff0000
s_mov_b32 s66,0x7fff0000
s_mov_b32 s76,0x7fff0000
s_branch Lg3o_done_%=
Lg3o_L1_%=:
v_mfma_f32_16x16x32_bf16 v[30:33],a[4:7],v[34:37],0
s_add_u32 m0,s59,3072
R 54,2,5120
v_add_f32_dpp v131,v130,v130 quad_perm:[1,0,3,2] row_mask:0xf bank_mask:0xf
M 30,a[12:15],38
s_mov_b32 s67,s65
buffer_load_dword v8,s[36:39],s58 offen offset:0 sc1 lds
v_mfma_f32_16x16x32_bf16 v[26:29],a[0:3],v[34:37],0
s_add_u32 s65,s65,s62
R 58,2,6144
v_add_f32_dpp v131,v131,v131 quad_perm:[2,3,0,1] row_mask:0xf bank_mask:0xf
M 26,a[8:11],38
L 3
v_fma_f32 v18,-v98,v106,v18
v_fma_f32 v19,-v98,v107,v19
M 26,a[16:19],42
R 62,2,7168
v_add_f32_dpp v134,v131,v131 row_half_mirror row_mask:0xf bank_mask:0xf
M 26,a[24:27],46
v_fma_f32 v20,-v98,v108,v20
v_fma_f32 v21,-v98,v109,v21
M 30,a[20:23],42
R 66,2,8192
v_mov_b32_dpp v131,v134 row_ror:8 row_mask:0xf bank_mask:0xf
M 30,a[28:31],46
L 3
v_fma_f32 v131,-v134,v134,v131
v_max_f32 v131,s87,v131
M 30,a[36:39],50
R 70,2,9216
v_mul_f32 v114,v18,v18
M 30,a[44:47],54
v_rsq_f32 v132,v131
M 26,a[32:35],50
R 74,2,10240
v_mul_f32 v137,v131,v132
M 26,a[40:43],54
L 3
v_mul_f32 v135,s88,v132
v_mul_f32 v131,s89,v132
M 26,a[48:51],58
R 78,2,11264
v_mul_f32 v136,v135,v132
M 26,a[56:59],62
v_mul_f32 v136,v136,v131
v_mul_f32 v115,v19,v19
M 30,a[52:55],58
R 82,2,12288
v_mul_f32 v116,v20,v20
M 30,a[60:63],62
L 3
ds_write_b128 v12,v[134:137] offset:2048
M 30,a[68:71],66
R 86,2,13312
v_mul_f32 v117,v21,v21
M 30,a[76:79],70
v_fma_f32 v114,v100,v114,v99
v_fma_f32 v115,v100,v115,v99
M 26,a[64:67],66
R 90,2,14336
v_fma_f32 v116,v100,v116,v99
M 26,a[72:75],70
L 4
v_fma_f32 v117,v100,v117,v99
v_mul_f32 v114,v18,v114
M 26,a[80:83],74
s_waitcnt vmcnt(0)
R 94,2,15360
v_mul_f32 v115,v19,v115
M 26,a[88:91],78
s_barrier
ds_read_b32 v7,v6 offset:3072
v_mul_f32 v116,v20,v116
M 30,a[84:87],74
R 34,2,16384
v_mul_f32 v117,v21,v117
M 30,a[92:95],78
L 4
buffer_store_dword v4,v3,s[36:39],s70 offen offset:0 sc0 sc1
M 30,a[100:103],82
s_mov_b32 s70,0x7fff0000
R 38,2,17408
v_fma_f32 v22,-v98,v110,v22
M 30,a[108:111],86
v_exp_f32 v114,v114
M 26,a[96:99],82
R 42,2,18432
v_fma_f32 v114,v114,v101,v101
M 26,a[104:107],86
L 4
L 3
v_cmp_eq_u32 vcc,s53,v7
M 26,a[112:115],90
R 46,2,19456
v_fma_f32 v23,-v98,v111,v23
M 26,a[120:123],94
s_cmp_eq_u32 vcc_lo,-1
v_exp_f32 v115,v115
s_cbranch_scc0 Lg3o_X1_%=
M 30,a[116:119],90
s_add_u32 m0,s57,0
s_add_u32 s56,s55,0
s_add_u32 s58,s58,s61
R 50,2,20480
M 30,a[124:127],94
L 3
buffer_load_dword v130,v11,s[36:39],s73 offen offset:0 sc1
M 30,a[132:135],34
s_add_u32 s54,s54,s46
R 54,2,21504
v_fma_f32 v115,v115,v101,v101
M 30,a[140:143],38
s_add_u32 s73,s73,s64
D 0,56,0
M 26,a[128:131],34
R 58,2,22528
v_fma_f32 v24,-v98,v112,v24
M 26,a[136:139],38
L 3
v_exp_f32 v116,v116
M 26,a[144:147],42
R 62,2,23552
v_fma_f32 v116,v116,v101,v101
M 26,a[152:155],46
D 0,56,1024
M 30,a[148:151],42
R 66,2,24576
v_fma_f32 v25,-v98,v113,v25
M 30,a[156:159],46
L 3
v_exp_f32 v117,v117
M 30,a[164:167],50
R 70,2,25600
v_fma_f32 v117,v117,v101,v101
M 30,a[172:175],54
D 0,56,2048
M 26,a[160:163],50
R 74,2,26624
v_mul_f32 v118,v22,v22
M 26,a[168:171],54
L 3
v_rcp_f32 v114,v114
M 26,a[176:179],58
R 78,2,27648
v_mul_f32 v18,v18,v114
M 26,a[184:187],62
D 0,56,3072
M 30,a[180:183],58
s_add_u32 m0,s59,1024
R 82,2,28672
v_mul_f32 v119,v23,v23
M 30,a[188:191],62
L 3
buffer_load_dword v8,s[36:39],s58 offen offset:0 sc1 lds
M 30,a[196:199],66
s_add_u32 m0,s57,16384
R 86,2,29696
v_mul_f32 v120,v24,v24
M 30,a[204:207],70
s_add_u32 s56,s55,16384
v_rcp_f32 v115,v115
M 26,a[192:195],66
s_add_u32 s55,s55,s60
R 90,2,30720
v_mul_f32 v19,v19,v115
M 26,a[200:203],70
L 3
s_nop 0
D 0,56,0
M 26,a[208:211],74
s_waitcnt vmcnt(1)
R 94,2,31744
v_cvt_pk_bf16_f32 v122,v18,v19
M 26,a[216:219],78
s_barrier
ds_read_b32 v7,v6 offset:1024
R 102,13,2048
M 30,a[212:215],74
s_sub_u32 s57,s84,s57
R 34,1,0
v_mul_f32 v121,v25,v25
M 30,a[220:223],78
L 5
buffer_store_dword v4,v3,s[36:39],s70 offen offset:0 sc0 sc1
M 30,a[228:231],82
s_mov_b32 s70,s69
R 38,1,1024
v_fma_f32 v118,v100,v118,v99
M 30,a[236:239],86
s_mov_b32 s69,s68
D 0,56,1024
M 26,a[224:227],82
s_add_u32 s68,s68,s63
R 42,1,2048
v_fma_f32 v119,v100,v119,v99
M 26,a[232:235],86
L 5
v_fma_f32 v120,v100,v120,v99
v_fma_f32 v121,v100,v121,v99
v_mul_f32 v118,v22,v118
v_mul_f32 v119,v23,v119
v_mul_f32 v120,v24,v120
v_mul_f32 v121,v25,v121
v_exp_f32 v118,v118
v_exp_f32 v119,v119
v_exp_f32 v120,v120
v_exp_f32 v121,v121
M 26,a[240:243],90
L 4
R 46,1,3072
v_rcp_f32 v116,v116
v_rcp_f32 v117,v117
v_fma_f32 v118,v118,v101,v101
v_fma_f32 v119,v119,v101,v101
v_fma_f32 v120,v120,v101,v101
v_fma_f32 v121,v121,v101,v101
v_rcp_f32 v118,v118
v_rcp_f32 v119,v119
v_rcp_f32 v120,v120
v_rcp_f32 v121,v121
M 26,a[248:251],94
v_mul_f32 v20,v20,v116
v_mul_f32 v21,v21,v117
v_cvt_pk_bf16_f32 v123,v20,v21
v_mul_f32 v22,v22,v118
v_mul_f32 v23,v23,v119
v_mul_f32 v24,v24,v120
)"
R"(v_mul_f32 v25,v25,v121
v_cvt_pk_bf16_f32 v124,v22,v23
v_cvt_pk_bf16_f32 v125,v24,v25
v_cmp_eq_u32 vcc,s53,v7
M 30,a[244:247],90
s_cmp_eq_u32 vcc_lo,-1
R 50,1,4096
buffer_store_dwordx4 v[122:125],v0,s[36:39],s66 offen offset:0 sc0 sc1
M 30,a[252:255],94
L 3
D 0,56,2048
D 0,56,3072
buffer_store_dword v4,v3,s[36:39],s71 offen offset:0 sc0 sc1
buffer_store_dword v4,v3,s[36:39],s71 offen offset:0 sc0 sc1
s_cbranch_scc1 Lg3o_F0_%=
s_branch Lg3o_L0_%=
Lg3o_X1_%=:
M 30,a[116:119],90
s_add_u32 m0,s59,1024
R 50,2,20480
v_fma_f32 v115,v115,v101,v101
M 30,a[124:127],94
L 3
buffer_load_dword v8,s[36:39],s58 offen offset:0 sc1 lds
M 30,a[132:135],34
R 54,2,21504
v_fma_f32 v24,-v98,v112,v24
M 30,a[140:143],38
v_exp_f32 v116,v116
M 26,a[128:131],34
R 58,2,22528
v_fma_f32 v116,v116,v101,v101
M 26,a[136:139],38
L 3
v_exp_f32 v117,v117
M 26,a[144:147],42
R 62,2,23552
v_fma_f32 v117,v117,v101,v101
M 26,a[152:155],46
v_rcp_f32 v114,v114
M 30,a[148:151],42
R 66,2,24576
v_mul_f32 v18,v18,v114
M 30,a[156:159],46
L 3
v_rcp_f32 v115,v115
M 30,a[164:167],50
R 70,2,25600
v_mul_f32 v19,v19,v115
M 30,a[172:175],54
v_rcp_f32 v116,v116
M 26,a[160:163],50
R 74,2,26624
v_mul_f32 v20,v20,v116
M 26,a[168:171],54
L 3
v_rcp_f32 v117,v117
M 26,a[176:179],58
R 78,2,27648
v_mul_f32 v21,v21,v117
M 26,a[184:187],62
v_cvt_pk_bf16_f32 v122,v18,v19
v_cvt_pk_bf16_f32 v123,v20,v21
M 30,a[180:183],58
R 82,2,28672
v_fma_f32 v25,-v98,v113,v25
M 30,a[188:191],62
L 3
v_mul_f32 v118,v22,v22
v_mul_f32 v119,v23,v23
M 30,a[196:199],66
R 86,2,29696
v_mul_f32 v120,v24,v24
M 30,a[204:207],70
v_mul_f32 v121,v25,v25
v_fma_f32 v118,v100,v118,v99
M 26,a[192:195],66
R 90,2,30720
v_fma_f32 v119,v100,v119,v99
M 26,a[200:203],70
L 3
s_nop 0
v_fma_f32 v120,v100,v120,v99
M 26,a[208:211],74
s_waitcnt vmcnt(0)
R 94,2,31744
v_fma_f32 v121,v100,v121,v99
M 26,a[216:219],78
s_barrier
buffer_store_dword v4,v3,s[36:39],s70 offen offset:0 sc0 sc1
M 30,a[212:215],74
s_mov_b32 s70,s69
ds_read_b32 v7,v6 offset:1024
R 102,13,2048
M 30,a[220:223],78
L 4
v_mul_f32 v118,v22,v118
v_mul_f32 v119,v23,v119
M 30,a[228:231],82
s_mov_b32 s69,s68
v_mul_f32 v120,v24,v120
v_mul_f32 v121,v25,v121
M 30,a[236:239],86
s_add_u32 s68,s68,s63
v_exp_f32 v118,v118
M 26,a[224:227],82
v_exp_f32 v119,v119
M 26,a[232:235],86
L 2
v_exp_f32 v120,v120
v_exp_f32 v121,v121
M 26,a[240:243],90
L 1
v_fma_f32 v118,v118,v101,v101
v_fma_f32 v119,v119,v101,v101
v_fma_f32 v120,v120,v101,v101
v_fma_f32 v121,v121,v101,v101
v_rcp_f32 v118,v118
v_rcp_f32 v119,v119
v_rcp_f32 v120,v120
v_rcp_f32 v121,v121
M 26,a[248:251],94
v_mul_f32 v22,v22,v118
v_mul_f32 v23,v23,v119
v_mul_f32 v24,v24,v120
v_mul_f32 v25,v25,v121
v_cvt_pk_bf16_f32 v124,v22,v23
v_cvt_pk_bf16_f32 v125,v24,v25
M 30,a[244:247],90
buffer_store_dwordx4 v[122:125],v0,s[36:39],s66 offen offset:0 sc0 sc1
v_cmp_eq_u32 vcc,s53,v7
M 30,a[252:255],94
s_mov_b32 s80,0
s_cmp_eq_u32 vcc_lo,-1
s_cbranch_scc0 Lg3o_xn1_%=
buffer_load_dword v130,v11,s[36:39],s73 offen offset:0 sc1
L 0
v_fma_f32 v26,-v102,v106,v26
v_fma_f32 v27,-v102,v107,v27
v_fma_f32 v28,-v102,v108,v28
v_fma_f32 v29,-v102,v109,v29
v_mul_f32 v114,v26,v26
v_mul_f32 v115,v27,v27
s_add_u32 s73,s73,s64
s_add_u32 m0,s57,0
s_add_u32 s56,s55,0
D 0,56,0
v_mul_f32 v116,v28,v28
v_mul_f32 v117,v29,v29
v_fma_f32 v114,v104,v114,v103
v_fma_f32 v115,v104,v115,v103
v_fma_f32 v116,v104,v116,v103
v_fma_f32 v117,v104,v117,v103
D 0,56,1024
v_mul_f32 v114,v26,v114
v_mul_f32 v115,v27,v115
v_mul_f32 v116,v28,v116
v_mul_f32 v117,v29,v117
v_exp_f32 v114,v114
v_exp_f32 v115,v115
D 0,56,2048
v_exp_f32 v116,v116
v_exp_f32 v117,v117
v_fma_f32 v114,v114,v105,v105
v_fma_f32 v115,v115,v105,v105
v_fma_f32 v116,v116,v105,v105
v_fma_f32 v117,v117,v105,v105
D 0,56,3072
v_rcp_f32 v114,v114
v_rcp_f32 v115,v115
v_rcp_f32 v116,v116
v_rcp_f32 v117,v117
v_mul_f32 v26,v26,v114
v_mul_f32 v27,v27,v115
s_add_u32 s58,s58,s61
s_add_u32 m0,s57,16384
s_add_u32 s56,s55,16384
D 0,56,0
v_mul_f32 v28,v28,v116
v_mul_f32 v29,v29,v117
v_cvt_pk_bf16_f32 v122,v26,v27
v_cvt_pk_bf16_f32 v123,v28,v29
v_fma_f32 v30,-v102,v110,v30
v_fma_f32 v31,-v102,v111,v31
D 0,56,1024
v_fma_f32 v32,-v102,v112,v32
v_fma_f32 v33,-v102,v113,v33
v_mul_f32 v118,v30,v30
v_mul_f32 v119,v31,v31
v_mul_f32 v120,v32,v32
v_mul_f32 v121,v33,v33
D 0,56,2048
v_fma_f32 v118,v104,v118,v103
v_fma_f32 v119,v104,v119,v103
v_fma_f32 v120,v104,v120,v103
v_fma_f32 v121,v104,v121,v103
v_mul_f32 v118,v30,v118
v_mul_f32 v119,v31,v119
D 0,56,3072
v_mul_f32 v120,v32,v120
v_mul_f32 v121,v33,v121
v_exp_f32 v118,v118
v_exp_f32 v119,v119
v_exp_f32 v120,v120
v_exp_f32 v121,v121
s_add_u32 s55,s55,s60
s_sub_u32 s57,s84,s57
s_add_u32 s54,s54,s46
v_fma_f32 v118,v118,v105,v105
v_fma_f32 v119,v119,v105,v105
v_fma_f32 v120,v120,v105,v105
v_fma_f32 v121,v121,v105,v105
v_rcp_f32 v118,v118
v_rcp_f32 v119,v119
v_rcp_f32 v120,v120
v_rcp_f32 v121,v121
v_mul_f32 v30,v30,v118
v_mul_f32 v31,v31,v119
v_mul_f32 v32,v32,v120
v_mul_f32 v33,v33,v121
v_cvt_pk_bf16_f32 v124,v30,v31
v_cvt_pk_bf16_f32 v125,v32,v33
buffer_store_dwordx4 v[122:125],v0,s[36:39],s67 offen offset:0 sc0 sc1
s_mov_b32 s72,s69
s_mov_b32 s69,0x7fff0000
s_mov_b32 s67,0x7fff0000
s_mov_b32 s77,0x7fff0000
s_branch Lg3o_gox_%=
Lg3o_xn1_%=:
s_cmp_ge_u32 s54,256
s_cbranch_scc1 Lg3o_fin1_%=
s_add_u32 m0,s59,4096
s_nop 0
buffer_load_dword v8,s[36:39],s58 offen offset:0 sc1 lds
buffer_store_dword v4,v3,s[36:39],s71 offen offset:0 sc0 sc1
s_add_u32 m0,s59,5120
s_nop 0
buffer_load_dword v8,s[36:39],s58 offen offset:0 sc1 lds
buffer_store_dword v4,v3,s[36:39],s71 offen offset:0 sc0 sc1
s_mov_b32 s83,0
L 0
v_fma_f32 v26,-v102,v106,v26
v_fma_f32 v27,-v102,v107,v27
v_fma_f32 v28,-v102,v108,v28
v_fma_f32 v29,-v102,v109,v29
v_mul_f32 v114,v26,v26
v_mul_f32 v115,v27,v27
v_mul_f32 v116,v28,v28
v_mul_f32 v117,v29,v29
v_fma_f32 v114,v104,v114,v103
v_fma_f32 v115,v104,v115,v103
v_fma_f32 v116,v104,v116,v103
v_fma_f32 v117,v104,v117,v103
v_mul_f32 v114,v26,v114
v_mul_f32 v115,v27,v115
v_mul_f32 v116,v28,v116
v_mul_f32 v117,v29,v117
v_exp_f32 v114,v114
v_exp_f32 v115,v115
v_exp_f32 v116,v116
v_exp_f32 v117,v117
v_fma_f32 v114,v114,v105,v105
v_fma_f32 v115,v115,v105,v105
v_fma_f32 v116,v116,v105,v105
v_fma_f32 v117,v117,v105,v105
v_rcp_f32 v114,v114
v_rcp_f32 v115,v115
v_rcp_f32 v116,v116
v_rcp_f32 v117,v117
v_mul_f32 v26,v26,v114
v_mul_f32 v27,v27,v115
v_mul_f32 v28,v28,v116
v_mul_f32 v29,v29,v117
v_cvt_pk_bf16_f32 v122,v26,v27
v_cvt_pk_bf16_f32 v123,v28,v29
v_fma_f32 v30,-v102,v110,v30
v_fma_f32 v31,-v102,v111,v31
v_fma_f32 v32,-v102,v112,v32
v_fma_f32 v33,-v102,v113,v33
v_mul_f32 v118,v30,v30
v_mul_f32 v119,v31,v31
v_mul_f32 v120,v32,v32
v_mul_f32 v121,v33,v33
v_fma_f32 v118,v104,v118,v103
v_fma_f32 v119,v104,v119,v103
v_fma_f32 v120,v104,v120,v103
v_fma_f32 v121,v104,v121,v103
v_mul_f32 v118,v30,v118
v_mul_f32 v119,v31,v119
v_mul_f32 v120,v32,v120
v_mul_f32 v121,v33,v121
v_exp_f32 v118,v118
v_exp_f32 v119,v119
v_exp_f32 v120,v120
v_exp_f32 v121,v121
v_fma_f32 v118,v118,v105,v105
v_fma_f32 v119,v119,v105,v105
v_fma_f32 v120,v120,v105,v105
v_fma_f32 v121,v121,v105,v105
v_rcp_f32 v118,v118
v_rcp_f32 v119,v119
v_rcp_f32 v120,v120
v_rcp_f32 v121,v121
v_mul_f32 v30,v30,v118
v_mul_f32 v31,v31,v119
v_mul_f32 v32,v32,v120
v_mul_f32 v33,v33,v121
v_cvt_pk_bf16_f32 v124,v30,v31
v_cvt_pk_bf16_f32 v125,v32,v33
buffer_store_dwordx4 v[122:125],v0,s[36:39],s67 offen offset:0 sc0 sc1
s_mov_b32 s72,s69
s_mov_b32 s69,0x7fff0000
s_mov_b32 s67,0x7fff0000
s_mov_b32 s77,0x7fff0000
s_branch Lg3o_poll_%=
Lg3o_fin1_%=:
L 0
v_fma_f32 v26,-v102,v106,v26
v_fma_f32 v27,-v102,v107,v27
v_fma_f32 v28,-v102,v108,v28
v_fma_f32 v29,-v102,v109,v29
v_mul_f32 v114,v26,v26
v_mul_f32 v115,v27,v27
v_mul_f32 v116,v28,v28
v_mul_f32 v117,v29,v29
v_fma_f32 v114,v104,v114,v103
v_fma_f32 v115,v104,v115,v103
v_fma_f32 v116,v104,v116,v103
v_fma_f32 v117,v104,v117,v103
v_mul_f32 v114,v26,v114
v_mul_f32 v115,v27,v115
v_mul_f32 v116,v28,v116
v_mul_f32 v117,v29,v117
v_exp_f32 v114,v114
v_exp_f32 v115,v115
v_exp_f32 v116,v116
v_exp_f32 v117,v117
v_fma_f32 v114,v114,v105,v105
v_fma_f32 v115,v115,v105,v105
v_fma_f32 v116,v116,v105,v105
v_fma_f32 v117,v117,v105,v105
v_rcp_f32 v114,v114
v_rcp_f32 v115,v115
v_rcp_f32 v116,v116
v_rcp_f32 v117,v117
v_mul_f32 v26,v26,v114
v_mul_f32 v27,v27,v115
v_mul_f32 v28,v28,v116
v_mul_f32 v29,v29,v117
v_cvt_pk_bf16_f32 v122,v26,v27
v_cvt_pk_bf16_f32 v123,v28,v29
v_fma_f32 v30,-v102,v110,v30
v_fma_f32 v31,-v102,v111,v31
v_fma_f32 v32,-v102,v112,v32
v_fma_f32 v33,-v102,v113,v33
v_mul_f32 v118,v30,v30
v_mul_f32 v119,v31,v31
v_mul_f32 v120,v32,v32
v_mul_f32 v121,v33,v33
v_fma_f32 v118,v104,v118,v103
v_fma_f32 v119,v104,v119,v103
v_fma_f32 v120,v104,v120,v103
v_fma_f32 v121,v104,v121,v103
v_mul_f32 v118,v30,v118
v_mul_f32 v119,v31,v119
v_mul_f32 v120,v32,v120
v_mul_f32 v121,v33,v121
v_exp_f32 v118,v118
v_exp_f32 v119,v119
v_exp_f32 v120,v120
v_exp_f32 v121,v121
v_fma_f32 v118,v118,v105,v105
v_fma_f32 v119,v119,v105,v105
v_fma_f32 v120,v120,v105,v105
v_fma_f32 v121,v121,v105,v105
v_rcp_f32 v118,v118
v_rcp_f32 v119,v119
v_rcp_f32 v120,v120
v_rcp_f32 v121,v121
v_mul_f32 v30,v30,v118
v_mul_f32 v31,v31,v119
v_mul_f32 v32,v32,v120
v_mul_f32 v33,v33,v121
v_cvt_pk_bf16_f32 v124,v30,v31
v_cvt_pk_bf16_f32 v125,v32,v33
buffer_store_dwordx4 v[122:125],v0,s[36:39],s67 offen offset:0 sc0 sc1
s_mov_b32 s72,s69
s_mov_b32 s69,0x7fff0000
s_mov_b32 s67,0x7fff0000
s_mov_b32 s77,0x7fff0000
s_branch Lg3o_done_%=
Lg3o_done_%=:
s_waitcnt vmcnt(1)
s_barrier
buffer_store_dword v4,v3,s[36:39],s70 offen offset:0 sc0 sc1
s_waitcnt vmcnt(1)
s_barrier
buffer_store_dword v4,v3,s[36:39],s72 offen offset:0 sc0 sc1
s_waitcnt vmcnt(0) lgkmcnt(0)
s_barrier
s_endpgm
Lcode_g3o_e:
.purgem M
.purgem R
.purgem T
.purgem D
.purgem L
)"
  :: "s"(ws), "s"(sWave), "s"(sG0), "s"(sStep), "s"(sOutOff), "s"(sFoutOff), "s"(sAuxOff), "s"(sWw), "s"(sBuf), "s"(sRk), "s"(sExp), "v"(vL16), "v"(vFlag), "v"(vSnapOff), "v"(vSnapRd), "v"(vWd0), "v"(vWd1), "v"(vWr0), "v"(vWr1), "v"(vWr2), "v"(vOutS), "v"(vAux0), "v"(vAux1), "v"(vAux2), "v"(vAux3), "v"(vAn), "v"(vAe), "s"(w3), "s"(out) : "v0", "v1", "v2", "v3", "v4", "v5", "v6", "v7", "v8", "v9", "v10", "v11", "v12", "v13", "v14", "v15", "v16", "v17", "v18", "v19", "v20", "v21", "v22", "v23", "v24", "v25", "v26", "v27", "v28", "v29", "v30", "v31", "v32", "v33", "v34", "v35", "v36", "v37", "v38", "v39", "v40", "v41", "v42", "v43", "v44", "v45", "v46", "v47", "v48", "v49", "v50", "v51", "v52", "v53", "v54", "v55", "v56", "v57", "v58", "v59", "v60", "v61", "v62", "v63", "v64", "v65", "v66", "v67", "v68", "v69", "v70", "v71", "v72", "v73", "v74", "v75", "v76", "v77", "v78", "v79", "v80", "v81", "v82", "v83", "v84", "v85", "v86", "v87", "v88", "v89", "v90", "v91", "v92", "v93", "v94", "v95", "v96", "v97", "v98", "v99", "v100", "v101", "v102", "v103", "v104", "v105", "v106", "v107", "v108", "v109", "v110", "v111", "v112", "v113", "v114", "v115", "v116", "v117", "v118", "v119", "v120", "v121", "v122", "v123", "v124", "v125", "v126", "v127", "v128", "v129", "v130", "v131", "v132", "v133", "v134", "v135", "v136", "v137", "v138", "v139", "v140", "v141", "v142", "v143", "v144", "v145", "v146", "v147", "v148", "v149", "v150", "v151", "v152", "v153", "v154", "v155", "v156", "v157", "v158", "v159", "v160", "v161", "v162", "v163", "v164", "v165", "v166", "v167", "v168", "v169", "v170", "v171", "v172", "v173", "v174", "v175", "v176", "v177", "v178", "v179", "v180", "v181", "v182", "v183", "v184", "v185", "v186", "v187", "v188", "v189", "v190", "v191", "v192", "v193", "v194", "v195", "v196", "v197", "v198", "v199", "v200", "v201", "v202", "v203", "v204", "v205", "v206", "v207", "v208", "v209", "v210", "v211", "v212", "v213", "v214", "v215", "v216", "v217", "v218", "v219", "v220", "v221", "v222", "v223", "v224", "v225", "v226", "v227", "v228", "v229", "v230", "v231", "v232", "v233", "v234", "v235", "v236", "v237", "v238", "v239", "a0", "a1", "a2", "a3", "a4", "a5", "a6", "a7", "a8", "a9", "a10", "a11", "a12", "a13", "a14", "a15", "a16", "a17", "a18", "a19", "a20", "a21", "a22", "a23", "a24", "a25", "a26", "a27", "a28", "a29", "a30", "a31", "a32", "a33", "a34", "a35", "a36", "a37", "a38", "a39", "a40", "a41", "a42", "a43", "a44", "a45", "a46", "a47", "a48", "a49", "a50", "a51", "a52", "a53", "a54", "a55", "a56", "a57", "a58", "a59", "a60", "a61", "a62", "a63", "a64", "a65", "a66", "a67", "a68", "a69", "a70", "a71", "a72", "a73", "a74", "a75", "a76", "a77", "a78", "a79", "a80", "a81", "a82", "a83", "a84", "a85", "a86", "a87", "a88", "a89", "a90", "a91", "a92", "a93", "a94", "a95", "a96", "a97", "a98", "a99", "a100", "a101", "a102", "a103", "a104", "a105", "a106", "a107", "a108", "a109", "a110", "a111", "a112", "a113", "a114", "a115", "a116", "a117", "a118", "a119", "a120", "a121", "a122", "a123", "a124", "a125", "a126", "a127", "a128", "a129", "a130", "a131", "a132", "a133", "a134", "a135", "a136", "a137", "a138", "a139", "a140", "a141", "a142", "a143", "a144", "a145", "a146", "a147", "a148", "a149", "a150", "a151", "a152", "a153", "a154", "a155", "a156", "a157", "a158", "a159", "a160", "a161", "a162", "a163", "a164", "a165", "a166", "a167", "a168", "a169", "a170", "a171", "a172", "a173", "a174", "a175", "a176", "a177", "a178", "a179", "a180", "a181", "a182", "a183", "a184", "a185", "a186", "a187", "a188", "a189", "a190", "a191", "a192", "a193", "a194", "a195", "a196", "a197", "a198", "a199", "a200", "a201", "a202", "a203", "a204", "a205", "a206", "a207", "a208", "a209", "a210", "a211", "a212", "a213", "a214", "a215", "a216", "a217", "a218", "a219", "a220", "a221", "a222", "a223", "a224", "a225", "a226", "a227", "a228", "a229", "a230", "a231", "a232", "a233", "a234", "a235", "a236", "a237", "a238", "a239", "a240", "a241", "a242", "a243", "a244", "a245", "a246", "a247", "a248", "a249", "a250", "a251", "a252", "a253", "a254", "a255", "s36", "s37", "s38", "s39", "s40", "s41", "s42", "s43", "s44", "s45", "s46", "s47", "s48", "s49", "s50", "s51", "s52", "s53", "s54", "s55", "s56", "s57", "s58", "s59", "s60", "s61", "s62", "s63", "s64", "s65", "s66", "s67", "s68", "s69", "s70", "s71", "s72", "s73", "s74", "s75", "s76", "s77", "s78", "s79", "s80", "s81", "s82", "s83", "s84", "s85", "s86", "s87", "s88", "s89", "s90", "s91", "s92", "s93", "s94", "s95", "s96", "s97", "vcc", "scc", "memory"); }
    __builtin_unreachable();
  }
  if (kind < 3) {
    const unsigned sStep = 4;
    COMMON(kind == 1 ? 8 : 16, kind == 1 ? fj * 128 : fj < 8 ? 1024 + fj * 128 : (fj & 2) * 1024 + (fj - 8) * 128, 1024, 131072, 2048, 256, kind == 1 ? 0 : 32768);
    const int sr = l15 - ((wave & 2) ? -1 : 1), sr2 = l15 - ((wave & 2) ? -2 : 2), nx = (slice >> 1) & 1;
    const unsigned vOutS = (sr2 & 16) ? 1024 + lg * 64 + (sr2 & 3) * 16 : (lg * 16 + sr2) * 16, vSp = (sr2 & 16) ? OOB : vOutS;
    (void)vWr2;
    const unsigned vAn = kind == 1 ? HU + vL16 : (sr & 16) ? 1024 + lg * 64 + (sr & 3) * 16 : (lg * 16 + sr) * 16;
    const unsigned vAe = kind == 1 ? ((l15 >> 2) == (nx ? 0 : 3) ? (nx ? 0 : 2 * HU) + 1024 + lg * 64 + (l15 & 3) * 16 : OOB) : (sr & 16) ? OOB : vAn;
    const unsigned vAux0 = l15 * 16, vAux1 = 139264 + (l15 * 2 + (lg & 1)) * 16 + wave * 4 + (lg >> 1) * 512, vAux2 = 139264 + (l15 * 2 + (lg & 1)) * 16;
    const unsigned vAux3 = (wave == 0 && lg < 2) ? slice * 128 + l15 * 8 + lg * 4 : OOB;
    const unsigned sOutOff = kind == 1 ? OFF_HS - HU + (slice * 4 + wave) * 1280 : OFF_X1 + (slice * 4 + wave) * 1024, sFoutOff = (kind == 1 ? OFF_FH : OFF_FX1) + slice * 128;
    const unsigned sAuxOff = OFF_XP + (slice * 4 + wave) * 1024, sBuf = kind, sWw = 16 * wave * 2048 + slice * 256;
    const u16* w12 = kind == 1 ? w1 : w2;
    unsigned epOut;
    asm volatile(
R"(.macro M d,w,x
v_mfma_f32_16x16x32_bf16 v[\d:\d+3], \w, v[\x:\x+3], v[\d:\d+3]
.endm
.macro R d,a,o
ds_read_b128 v[\d:\d+3], v\a offset:\o
.endm
.macro T d,a,o
ds_read_b64_tr_b16 \d, v\a offset:\o
.endm
.macro D a,s,o
buffer_load_dwordx4 v\a, s[36:39], s\s offen offset:\o sc1 lds
.endm
.macro L c
s_waitcnt lgkmcnt(\c)
.endm
Lcode_g12_b:
s_cmp_eq_u32 %9,2
s_cbranch_scc1 Lg2_begin_%=
s_mov_b64 s[36:37],%1
s_mov_b32 s38,111243392
s_mov_b32 s39,0x00020000
s_mov_b32 s44,%2
s_mov_b32 s45,%3
s_mov_b32 s46,%4
s_mov_b32 s47,%5
s_mov_b32 s48,%6
s_mov_b32 s49,%7
s_mov_b32 s50,%8
s_mov_b32 s51,%9
s_mov_b32 s93,%10
v_mov_b32 v0,%11
v_mov_b32 v3,%12
v_mov_b32 v5,%13
v_mov_b32 v6,%14
v_mov_b32 v18,%15
v_mov_b32 v19,%16
v_mov_b32 v20,%17
v_mov_b32 v21,%18
v_mov_b32 v9,%19
v_mov_b32 v10,%20
v_mov_b32 v11,%21
v_mov_b32 v12,%22
v_mov_b32 v13,%23
v_mov_b32 v14,%24
v_mov_b32 v15,%25
v_mov_b32 v16,%26
s_mov_b64 s[40:41],%27
s_mov_b32 s42,2097152
s_mov_b32 s43,0x00020000
s_lshl_b32 s57,s44,11
s_mov_b32 s79,111243264
buffer_load_dword v4,off,s[36:39],s79 sc1
s_add_u32 s55,s50,0
s_add_u32 m0,s57,0
s_nop 0
buffer_load_dwordx4 v18,s[40:43],s55 offen offset:0  lds
s_add_u32 m0,s57,1024
s_nop 0
buffer_load_dwordx4 v19,s[40:43],s55 offen offset:0  lds
s_add_u32 m0,s57,8064
s_nop 0
buffer_load_dwordx4 v18,s[40:43],s55 offen offset:128  lds
s_add_u32 m0,s57,9088
s_nop 0
buffer_load_dwordx4 v19,s[40:43],s55 offen offset:128  lds
s_add_u32 s55,s50,131072
s_add_u32 m0,s57,16384
s_nop 0
buffer_load_dwordx4 v18,s[40:43],s55 offen offset:0  lds
s_add_u32 m0,s57,17408
s_nop 0
buffer_load_dwordx4 v19,s[40:43],s55 offen offset:0  lds
s_add_u32 m0,s57,24448
s_nop 0
buffer_load_dwordx4 v18,s[40:43],s55 offen offset:128  lds
s_add_u32 m0,s57,25472
s_nop 0
buffer_load_dwordx4 v19,s[40:43],s55 offen offset:128  lds
s_add_u32 s55,s50,262144
s_add_u32 m0,s57,32768
s_nop 0
buffer_load_dwordx4 v18,s[40:43],s55 offen offset:0  lds
s_add_u32 m0,s57,33792
s_nop 0
buffer_load_dwordx4 v19,s[40:43],s55 offen offset:0  lds
s_add_u32 m0,s57,40832
s_nop 0
buffer_load_dwordx4 v18,s[40:43],s55 offen offset:128  lds
s_add_u32 m0,s57,41856
s_nop 0
buffer_load_dwordx4 v19,s[40:43],s55 offen offset:128  lds
s_add_u32 s55,s50,393216
s_add_u32 m0,s57,49152
s_nop 0
buffer_load_dwordx4 v18,s[40:43],s55 offen offset:0  lds
s_add_u32 m0,s57,50176
s_nop 0
buffer_load_dwordx4 v19,s[40:43],s55 offen offset:0  lds
s_add_u32 m0,s57,57216
s_nop 0
buffer_load_dwordx4 v18,s[40:43],s55 offen offset:128  lds
s_add_u32 m0,s57,58240
s_nop 0
buffer_load_dwordx4 v19,s[40:43],s55 offen offset:128  lds
s_add_u32 s55,s50,524288
s_add_u32 m0,s57,65536
s_nop 0
buffer_load_dwordx4 v18,s[40:43],s55 offen offset:0  lds
s_add_u32 m0,s57,66560
s_nop 0
buffer_load_dwordx4 v19,s[40:43],s55 offen offset:0  lds
s_add_u32 m0,s57,73600
s_nop 0
buffer_load_dwordx4 v18,s[40:43],s55 offen offset:128  lds
s_add_u32 m0,s57,74624
s_nop 0
buffer_load_dwordx4 v19,s[40:43],s55 offen offset:128  lds
s_add_u32 s55,s50,655360
s_add_u32 m0,s57,81920
s_nop 0
buffer_load_dwordx4 v18,s[40:43],s55 offen offset:0  lds
s_add_u32 m0,s57,82944
s_nop 0
buffer_load_dwordx4 v19,s[40:43],s55 offen offset:0  lds
s_add_u32 m0,s57,89984
s_nop 0
buffer_load_dwordx4 v18,s[40:43],s55 offen offset:128  lds
s_add_u32 m0,s57,91008
s_nop 0
buffer_load_dwordx4 v19,s[40:43],s55 offen offset:128  lds
s_waitcnt vmcnt(24)
v_readfirstlane_b32 s53,v4
s_nop 3
s_add_u32 s53,s53,1
s_cmp_eq_u32 s53,0
s_cselect_b32 s53,1,s53
v_mov_b32 v4,s53
s_waitcnt vmcnt(20) lgkmcnt(0)
s_barrier
s_add_u32 s55,s50,786432
s_add_u32 m0,s57,98304
s_nop 0
buffer_load_dwordx4 v18,s[40:43],s55 offen offset:0  lds
s_add_u32 m0,s57,99328
s_nop 0
buffer_load_dwordx4 v19,s[40:43],s55 offen offset:0  lds
s_add_u32 m0,s57,106368
s_nop 0
buffer_load_dwordx4 v18,s[40:43],s55 offen offset:128  lds
s_add_u32 m0,s57,107392
s_nop 0
buffer_load_dwordx4 v19,s[40:43],s55 offen offset:128  lds
v_add_u32 v26,0,v20
v_add_u32 v27,0,v21
T a[0:1],26,0
T a[2:3],26,512
T a[4:5],27,0
T a[6:7],27,512
T a[8:9],26,4096
T a[10:11],26,4608
T a[12:13],27,4096
T a[14:15],27,4608
s_waitcnt vmcnt(20) lgkmcnt(0)
s_barrier
s_add_u32 s55,s50,917504
s_add_u32 m0,s57,0
s_nop 0
buffer_load_dwordx4 v18,s[40:43],s55 offen offset:0  lds
s_add_u32 m0,s57,1024
s_nop 0
buffer_load_dwordx4 v19,s[40:43],s55 offen offset:0  lds
s_add_u32 m0,s57,8064
s_nop 0
buffer_load_dwordx4 v18,s[40:43],s55 offen offset:128  lds
s_add_u32 m0,s57,9088
s_nop 0
buffer_load_dwordx4 v19,s[40:43],s55 offen offset:128  lds
v_add_u32 v26,16384,v20
v_add_u32 v27,16384,v21
T a[16:17],26,0
T a[18:19],26,512
T a[20:21],27,0
T a[22:23],27,512
T a[24:25],26,4096
T a[26:27],26,4608
T a[28:29],27,4096
T a[30:31],27,4608
s_waitcnt vmcnt(20) lgkmcnt(0)
s_barrier
s_add_u32 s55,s50,1048576
s_add_u32 m0,s57,16384
s_nop 0
buffer_load_dwordx4 v18,s[40:43],s55 offen offset:0  lds
s_add_u32 m0,s57,17408
s_nop 0
buffer_load_dwordx4 v19,s[40:43],s55 offen offset:0  lds
s_add_u32 m0,s57,24448
s_nop 0
buffer_load_dwordx4 v18,s[40:43],s55 offen offset:128  lds
s_add_u32 m0,s57,25472
s_nop 0
buffer_load_dwordx4 v19,s[40:43],s55 offen offset:128  lds
v_add_u32 v26,32768,v20
v_add_u32 v27,32768,v21
T a[32:33],26,0
T a[34:35],26,512
T a[36:37],27,0
T a[38:39],27,512
T a[40:41],26,4096
T a[42:43],26,4608
T a[44:45],27,4096
T a[46:47],27,4608
s_waitcnt vmcnt(20) lgkmcnt(0)
s_barrier
s_add_u32 s55,s50,1179648
s_add_u32 m0,s57,32768
s_nop 0
buffer_load_dwordx4 v18,s[40:43],s55 offen offset:0  lds
s_add_u32 m0,s57,33792
s_nop 0
buffer_load_dwordx4 v19,s[40:43],s55 offen offset:0  lds
s_add_u32 m0,s57,40832
s_nop 0
buffer_load_dwordx4 v18,s[40:43],s55 offen offset:128  lds
s_add_u32 m0,s57,41856
s_nop 0
buffer_load_dwordx4 v19,s[40:43],s55 offen offset:128  lds
v_add_u32 v26,49152,v20
v_add_u32 v27,49152,v21
T a[48:49],26,0
T a[50:51],26,512
T a[52:53],27,0
T a[54:55],27,512
T a[56:57],26,4096
T a[58:59],26,4608
T a[60:61],27,4096
T a[62:63],27,4608
s_waitcnt vmcnt(20) lgkmcnt(0)
s_barrier
s_add_u32 s55,s50,1310720
s_add_u32 m0,s57,49152
s_nop 0
buffer_load_dwordx4 v18,s[40:43],s55 offen offset:0  lds
s_add_u32 m0,s57,50176
s_nop 0
buffer_load_dwordx4 v19,s[40:43],s55 offen offset:0  lds
s_add_u32 m0,s57,57216
s_nop 0
buffer_load_dwordx4 v18,s[40:43],s55 offen offset:128  lds
s_add_u32 m0,s57,58240
s_nop 0
buffer_load_dwordx4 v19,s[40:43],s55 offen offset:128  lds
v_add_u32 v26,65536,v20
v_add_u32 v27,65536,v21
T a[64:65],26,0
T a[66:67],26,512
T a[68:69],27,0
T a[70:71],27,512
T a[72:73],26,4096
T a[74:75],26,4608
T a[76:77],27,4096
T a[78:79],27,4608
s_waitcnt vmcnt(20) lgkmcnt(0)
s_barrier
s_add_u32 s55,s50,1441792
s_add_u32 m0,s57,65536
s_nop 0
buffer_load_dwordx4 v18,s[40:43],s55 offen offset:0  lds
s_add_u32 m0,s57,66560
s_nop 0
buffer_load_dwordx4 v19,s[40:43],s55 offen offset:0  lds
s_add_u32 m0,s57,73600
s_nop 0
buffer_load_dwordx4 v18,s[40:43],s55 offen offset:128  lds
s_add_u32 m0,s57,74624
s_nop 0
buffer_load_dwordx4 v19,s[40:43],s55 offen offset:128  lds
v_add_u32 v26,81920,v20
v_add_u32 v27,81920,v21
T a[80:81],26,0
T a[82:83],26,512
T a[84:85],27,0
T a[86:87],27,512
T a[88:89],26,4096
T a[90:91],26,4608
T a[92:93],27,4096
T a[94:95],27,4608
s_waitcnt vmcnt(20) lgkmcnt(0)
s_barrier
s_add_u32 s55,s50,1572864
s_add_u32 m0,s57,81920
s_nop 0
buffer_load_dwordx4 v18,s[40:43],s55 offen offset:0  lds
s_add_u32 m0,s57,82944
s_nop 0
buffer_load_dwordx4 v19,s[40:43],s55 offen offset:0  lds
s_add_u32 m0,s57,89984
s_nop 0
buffer_load_dwordx4 v18,s[40:43],s55 offen offset:128  lds
s_add_u32 m0,s57,91008
s_nop 0
buffer_load_dwordx4 v19,s[40:43],s55 offen offset:128  lds
v_add_u32 v26,98304,v20
v_add_u32 v27,98304,v21
T a[96:97],26,0
T a[98:99],26,512
T a[100:101],27,0
T a[102:103],27,512
T a[104:105],26,4096
T a[106:107],26,4608
T a[108:109],27,4096
T a[110:111],27,4608
s_waitcnt vmcnt(20) lgkmcnt(0)
s_barrier
s_add_u32 s55,s50,1703936
s_add_u32 m0,s57,98304
s_nop 0
buffer_load_dwordx4 v18,s[40:43],s55 offen offset:0  lds
s_add_u32 m0,s57,99328
s_nop 0
buffer_load_dwordx4 v19,s[40:43],s55 offen offset:0  lds
s_add_u32 m0,s57,106368
s_nop 0
buffer_load_dwordx4 v18,s[40:43],s55 offen offset:128  lds
s_add_u32 m0,s57,107392
s_nop 0
buffer_load_dwordx4 v19,s[40:43],s55 offen offset:128  lds
v_add_u32 v26,0,v20
v_add_u32 v27,0,v21
T a[112:113],26,0
T a[114:115],26,512
T a[116:117],27,0
T a[118:119],27,512
T a[120:121],26,4096
T a[122:123],26,4608
T a[124:125],27,4096
T a[126:127],27,4608
s_waitcnt vmcnt(20) lgkmcnt(0)
s_barrier
s_add_u32 s55,s50,1835008
s_add_u32 m0,s57,0
s_nop 0
buffer_load_dwordx4 v18,s[40:43],s55 offen offset:0  lds
s_add_u32 m0,s57,1024
s_nop 0
buffer_load_dwordx4 v19,s[40:43],s55 offen offset:0  lds
s_add_u32 m0,s57,8064
s_nop 0
buffer_load_dwordx4 v18,s[40:43],s55 offen offset:128  lds
s_add_u32 m0,s57,9088
s_nop 0
buffer_load_dwordx4 v19,s[40:43],s55 offen offset:128  lds
v_add_u32 v26,16384,v20
v_add_u32 v27,16384,v21
T a[128:129],26,0
T a[130:131],26,512
T a[132:133],27,0
T a[134:135],27,512
T a[136:137],26,4096
T a[138:139],26,4608
T a[140:141],27,4096
T a[142:143],27,4608
s_waitcnt vmcnt(20) lgkmcnt(0)
s_barrier
s_add_u32 s55,s50,1966080
s_add_u32 m0,s57,16384
s_nop 0
buffer_load_dwordx4 v18,s[40:43],s55 offen offset:0  lds
s_add_u32 m0,s57,17408
s_nop 0
buffer_load_dwordx4 v19,s[40:43],s55 offen offset:0  lds
s_add_u32 m0,s57,24448
s_nop 0
buffer_load_dwordx4 v18,s[40:43],s55 offen offset:128  lds
s_add_u32 m0,s57,25472
s_nop 0
buffer_load_dwordx4 v19,s[40:43],s55 offen offset:128  lds
v_add_u32 v26,32768,v20
v_add_u32 v27,32768,v21
T a[144:145],26,0
T a[146:147],26,512
T a[148:149],27,0
T a[150:151],27,512
T a[152:153],26,4096
T a[154:155],26,4608
T a[156:157],27,4096
T a[158:159],27,4608
s_waitcnt vmcnt(20) lgkmcnt(0)
s_barrier
v_add_u32 v26,49152,v20
v_add_u32 v27,49152,v21
T a[160:161],26,0
T a[162:163],26,512
T a[164:165],27,0
T a[166:167],27,512
T a[168:169],26,4096
T a[170:171],26,4608
T a[172:173],27,4096
T a[174:175],27,4608
s_waitcnt vmcnt(16) lgkmcnt(0)
s_barrier
v_add_u32 v26,65536,v20
v_add_u32 v27,65536,v21
T a[176:177],26,0
T a[178:179],26,512
T a[180:181],27,0
T a[182:183],27,512
T a[184:185],26,4096
T a[186:187],26,4608
T a[188:189],27,4096
T a[190:191],27,4608
s_waitcnt vmcnt(12) lgkmcnt(0)
s_barrier
v_add_u32 v26,81920,v20
v_add_u32 v27,81920,v21
T a[192:193],26,0
T a[194:195],26,512
T a[196:197],27,0
T a[198:199],27,512
T a[200:201],26,4096
T a[202:203],26,4608
T a[204:205],27,4096
T a[206:207],27,4608
s_waitcnt vmcnt(8) lgkmcnt(0)
s_barrier
v_add_u32 v26,98304,v20
v_add_u32 v27,98304,v21
T a[208:209],26,0
T a[210:211],26,512
T a[212:213],27,0
T a[214:215],27,512
T a[216:217],26,4096
T a[218:219],26,4608
T a[220:221],27,4096
T a[222:223],27,4608
s_waitcnt vmcnt(4) lgkmcnt(0)
s_barrier
v_add_u32 v26,0,v20
v_add_u32 v27,0,v21
T a[224:225],26,0
T a[226:227],26,512
T a[228:229],27,0
T a[230:231],27,512
T a[232:233],26,4096
T a[234:235],26,4608
T a[236:237],27,4096
T a[238:239],27,4608
s_waitcnt vmcnt(0) lgkmcnt(0)
s_barrier
v_add_u32 v26,16384,v20
v_add_u32 v27,16384,v21
T a[240:241],26,0
T a[242:243],26,512
T a[244:245],27,0
T a[246:247],27,512
T a[248:249],26,4096
T a[250:251],26,4608
T a[252:253],27,4096
T a[254:255],27,4608
L 0
s_barrier
v_mov_b32 v34,0x3f803f80
v_mov_b32 v35,0x3f803f80
v_mov_b32 v36,0x3f803f80
v_mov_b32 v37,0x3f803f80
s_nop 1
v_mfma_f32_16x16x32_bf16 v[106:109],a[0:3],v[34:37],0
v_mfma_f32_16x16x32_bf16 v[110:113],a[4:7],v[34:37],0
M 106,a[8:11],34
M 110,a[12:15],34
M 106,a[16:19],34
M 110,a[20:23],34
M 106,a[24:27],34
M 110,a[28:31],34
M 106,a[32:35],34
M 110,a[36:39],34
M 106,a[40:43],34
M 110,a[44:47],34
M 106,a[48:51],34
M 110,a[52:55],34
M 106,a[56:59],34
M 110,a[60:63],34
M 106,a[64:67],34
M 110,a[68:71],34
M 106,a[72:75],34
M 110,a[76:79],34
M 106,a[80:83],34
M 110,a[84:87],34
M 106,a[88:91],34
M 110,a[92:95],34
M 106,a[96:99],34
M 110,a[100:103],34
M 106,a[104:107],34
M 110,a[108:111],34
M 106,a[112:115],34
M 110,a[116:119],34
M 106,a[120:123],34
M 110,a[124:127],34
M 106,a[128:131],34
M 110,a[132:135],34
)"
R"(M 106,a[136:139],34
M 110,a[140:143],34
M 106,a[144:147],34
M 110,a[148:151],34
M 106,a[152:155],34
M 110,a[156:159],34
M 106,a[160:163],34
M 110,a[164:167],34
M 106,a[168:171],34
M 110,a[172:175],34
M 106,a[176:179],34
M 110,a[180:183],34
M 106,a[184:187],34
M 110,a[188:191],34
M 106,a[192:195],34
M 110,a[196:199],34
M 106,a[200:203],34
M 110,a[204:207],34
M 106,a[208:211],34
M 110,a[212:215],34
M 106,a[216:219],34
M 110,a[220:223],34
M 106,a[224:227],34
M 110,a[228:231],34
M 106,a[232:235],34
M 110,a[236:239],34
M 106,a[240:243],34
M 110,a[244:247],34
M 106,a[248:251],34
M 110,a[252:255],34
v_mov_b32 v4,s53
s_mov_b32 s82,0
s_mov_b32 s80,0
s_mov_b32 s54,s45
s_mul_i32 s57,s44,4096
s_mul_i32 s55,s45,32768
s_add_u32 s55,s55,s57
s_add_u32 s55,s55,8388608
s_mul_i32 s57,s44,4096
s_mul_i32 s60,s46,32768
s_mul_i32 s61,s46,1024
s_mul_i32 s62,s46,40960
s_mul_i32 s63,s46,1024
s_mul_i32 s64,s46,512
s_lshl_b32 s59,s44,8
s_add_u32 s59,s59,131072
s_mul_i32 s58,s45,1024
s_add_u32 s58,s58,109056000
s_mul_i32 s65,s45,40960
s_add_u32 s65,s65,s47
s_mul_i32 s68,s45,1024
s_add_u32 s68,s68,s48
s_mul_i32 s73,s45,512
s_add_u32 s73,s73,106954752
s_mul_i32 s74,s45,32768
s_add_u32 s74,s74,s49
s_mul_i32 s75,s45,1024
s_add_u32 s75,s75,107479040
s_mov_b32 s66,0x7fff0000
s_mov_b32 s67,0x7fff0000
s_mov_b32 s69,0x7fff0000
s_mov_b32 s76,0x7fff0000
s_mov_b32 s77,0x7fff0000
s_mov_b32 s70,0x7fff0000
s_mov_b32 s71,0x7fff0000
s_mov_b32 s72,0x7fff0000
v_mov_b32 v1,v0
v_add_u32 v2,16384,v0
s_mov_b32 s84,16384
s_mov_b32 s86,0x3a800000
s_mov_b32 s87,0x3727c5ac
s_mov_b32 s88,0xc0135761
s_mov_b32 s89,0x3d372713
s_lshr_b32 s85,s44,1
s_mul_i32 s85,s85,127
v_lshlrev_b32 v8,22,v0
v_and_b32 v8,0x80000000,v8
v_or_b32 v8,v8,v5
v_add_u32 v2,32768,v0
s_lshl_b32 s84,s57,1
s_add_u32 s84,s84,32768
s_getpc_b64 s[40:41]
Lg1_pc_%=:
s_lshl_b32 s78,s44,13
v_lshlrev_b32 v114,3,v0
v_add_u32 v114,s78,v114
v_min_u32 v114,Lcode_g2_b-Lg1_pc_%=-4,v114
s_mov_b32 m0,163584
s_nop 0
global_load_lds_dword v114,s[40:41]
s_add_u32 m0,s59,4096
s_nop 0
buffer_load_dword v8,s[36:39],s58 offen offset:0 sc1 lds
buffer_store_dword v4,v3,s[36:39],s71 offen offset:0 sc0 sc1
s_add_u32 m0,s59,5120
s_nop 0
buffer_load_dword v8,s[36:39],s58 offen offset:0 sc1 lds
buffer_store_dword v4,v3,s[36:39],s71 offen offset:0 sc0 sc1
s_mov_b32 s83,0
Lg1_poll_%=:
s_add_u32 s78,s83,2
s_and_b32 s78,s78,3
s_lshl_b32 s78,s78,10
s_add_u32 s78,s78,4096
s_add_u32 m0,s59,s78
s_nop 0
buffer_load_dword v8,s[36:39],s58 offen offset:0 sc1 lds
s_and_b32 s78,s83,3
s_lshl_b32 s78,s78,10
v_add_u32 v114,s78,v6
s_waitcnt vmcnt(4)
s_barrier
ds_read_b32 v7,v114 offset:4096
buffer_store_dword v4,v3,s[36:39],s70 offen offset:0 sc0 sc1
s_mov_b32 s70,s71
s_mov_b32 s71,s72
s_mov_b32 s72,0x7fff0000
s_add_u32 s83,s83,1
s_add_u32 s82,s82,1
L 0
v_cmp_eq_u32 vcc,s53,v7
s_nop 0
s_cmp_eq_u32 vcc_lo,-1
s_cbranch_scc1 Lg1_go_%=
s_cmp_lt_u32 s82,20000
s_cbranch_scc1 Lg1_poll_%=
Lg1_go_%=:
s_add_u32 m0,s57,0
s_add_u32 s56,s55,0
D 0,56,0
D 0,56,1024
D 0,56,2048
D 0,56,3072
s_add_u32 s58,s58,s61
s_add_u32 m0,s59,2048
s_nop 0
buffer_load_dword v8,s[36:39],s58 offen offset:0 sc1 lds
s_add_u32 m0,s57,16384
s_add_u32 s56,s55,16384
D 0,56,0
D 0,56,1024
D 0,56,2048
D 0,56,3072
s_add_u32 s55,s55,s60
s_sub_u32 s57,s84,s57
s_add_u32 s54,s54,s46
s_waitcnt vmcnt(4)
s_barrier
buffer_store_dword v4,v3,s[36:39],s70 offen offset:0 sc0 sc1
s_min_u32 s71,s71,s72
buffer_store_dword v4,v3,s[36:39],s71 offen offset:0 sc0 sc1
s_mov_b32 s70,0x7fff0000
s_mov_b32 s71,0x7fff0000
s_mov_b32 s72,0x7fff0000
s_cmp_eq_u32 s80,1
buffer_store_dword v4,v3,s[36:39],s71 offen offset:0 sc0 sc1
s_cbranch_scc1 Lg1_st1_%=
s_branch Lg1_st0_%=
Lg1_gox_%=:
s_add_u32 m0,s59,2048
s_nop 0
buffer_load_dword v8,s[36:39],s58 offen offset:0 sc1 lds
s_waitcnt vmcnt(0)
s_barrier
buffer_store_dword v4,v3,s[36:39],s70 offen offset:0 sc0 sc1
s_min_u32 s71,s71,s72
buffer_store_dword v4,v3,s[36:39],s71 offen offset:0 sc0 sc1
s_mov_b32 s70,0x7fff0000
s_mov_b32 s71,0x7fff0000
s_mov_b32 s72,0x7fff0000
s_cmp_eq_u32 s80,1
s_cbranch_scc1 Lg1_st1_%=
Lg1_st0_%=:
ds_read_b32 v7,v6 offset:2048
R 34,1,0
R 38,1,1024
R 42,1,2048
R 46,1,3072
R 50,1,4096
L 5
v_cmp_eq_u32 vcc,s53,v7
L 3
s_cmp_eq_u32 vcc_lo,-1
s_cbranch_scc1 Lg1_F0_%=
s_branch Lg1_L0_%=
Lg1_st1_%=:
ds_read_b32 v7,v6 offset:2048
R 34,2,0
R 38,2,1024
R 42,2,2048
R 46,2,3072
R 50,2,4096
L 5
v_cmp_eq_u32 vcc,s53,v7
L 3
s_cmp_eq_u32 vcc_lo,-1
s_cbranch_scc1 Lg1_F1_%=
s_branch Lg1_L1_%=
Lg1_F0_%=:
v_mfma_f32_16x16x32_bf16 v[22:25],a[4:7],v[34:37],0
s_add_u32 s58,s58,s61
s_add_u32 m0,s59,0
R 54,1,5120
M 22,a[12:15],38
s_add_u32 s56,s55,0
buffer_load_dword v8,s[36:39],s58 offen offset:0 sc1 lds
v_mfma_f32_16x16x32_bf16 v[18:21],a[0:3],v[34:37],0
s_add_u32 m0,s57,0
R 58,1,6144
v_fma_f32 v26,-v102,v106,v26
M 18,a[8:11],38
L 3
D 0,56,0
M 18,a[16:19],42
s_mov_b32 s66,s65
R 62,1,7168
v_fma_f32 v27,-v102,v107,v27
M 18,a[24:27],46
s_add_u32 s65,s65,s62
v_fma_f32 v28,-v102,v108,v28
v_fma_f32 v29,-v102,v109,v29
M 22,a[20:23],42
s_add_u32 s54,s54,s46
R 66,1,8192
v_mul_f32 v114,v26,v26
M 22,a[28:31],46
L 3
D 0,56,1024
M 22,a[36:39],50
R 70,1,9216
v_mul_f32 v115,v27,v27
M 22,a[44:47],54
v_mul_f32 v116,v28,v28
v_mul_f32 v117,v29,v29
M 18,a[32:35],50
R 74,1,10240
v_fma_f32 v114,v104,v114,v103
M 18,a[40:43],54
L 3
D 0,56,2048
M 18,a[48:51],58
R 78,1,11264
v_fma_f32 v115,v104,v115,v103
M 18,a[56:59],62
D 0,56,3072
M 22,a[52:55],58
s_add_u32 m0,s57,16384
R 82,1,12288
v_fma_f32 v116,v104,v116,v103
M 22,a[60:63],62
L 3
v_fma_f32 v117,v104,v117,v103
v_mul_f32 v114,v26,v114
M 22,a[68:71],66
s_add_u32 s56,s55,16384
R 86,1,13312
v_mul_f32 v115,v27,v115
M 22,a[76:79],70
s_add_u32 s55,s55,s60
buffer_load_dwordx4 v[98:101],v11,s[36:39],s73 offen offset:0 sc1
M 18,a[64:67],66
s_add_u32 s73,s73,s64
R 90,1,14336
v_mul_f32 v116,v28,v116
M 18,a[72:75],70
L 3
v_exp_f32 v114,v114
M 18,a[80:83],74
s_waitcnt vmcnt(9)
R 94,1,15360
v_mul_f32 v117,v29,v117
M 18,a[88:91],78
s_barrier
v_exp_f32 v115,v115
M 22,a[84:87],74
s_sub_u32 s57,s84,s57
R 34,1,16384
v_fma_f32 v114,v114,v105,v105
M 22,a[92:95],78
L 3
v_exp_f32 v116,v116
M 22,a[100:103],82
R 38,1,17408
v_fma_f32 v115,v115,v105,v105
M 22,a[108:111],86
D 0,56,0
M 18,a[96:99],82
R 42,1,18432
v_fma_f32 v116,v116,v105,v105
M 18,a[104:107],86
L 3
v_exp_f32 v117,v117
M 18,a[112:115],90
R 46,1,19456
v_fma_f32 v117,v117,v105,v105
M 18,a[120:123],94
v_rcp_f32 v114,v114
M 22,a[116:119],90
R 50,1,20480
v_mul_f32 v26,v26,v114
M 22,a[124:127],94
L 3
v_rcp_f32 v115,v115
M 22,a[132:135],34
R 54,1,21504
v_mul_f32 v27,v27,v115
M 22,a[140:143],38
D 0,56,1024
M 18,a[128:131],34
R 58,1,22528
v_cvt_pk_bf16_f32 v122,v26,v27
M 18,a[136:139],38
L 3
v_rcp_f32 v116,v116
M 18,a[144:147],42
R 62,1,23552
v_mul_f32 v28,v28,v116
M 18,a[152:155],46
v_rcp_f32 v117,v117
M 22,a[148:151],42
R 66,1,24576
v_mul_f32 v29,v29,v117
M 22,a[156:159],46
L 3
D 0,56,2048
M 22,a[164:167],50
R 70,1,25600
v_cvt_pk_bf16_f32 v123,v28,v29
M 22,a[172:175],54
v_fma_f32 v30,-v102,v110,v30
v_fma_f32 v31,-v102,v111,v31
M 18,a[160:163],50
R 74,1,26624
v_fma_f32 v32,-v102,v112,v32
M 18,a[168:171],54
L 3
v_fma_f32 v33,-v102,v113,v33
v_mul_f32 v118,v30,v30
M 18,a[176:179],58
R 78,1,27648
v_mul_f32 v119,v31,v31
M 18,a[184:187],62
v_mul_f32 v120,v32,v32
v_mul_f32 v121,v33,v33
M 22,a[180:183],58
R 82,1,28672
v_fma_f32 v118,v104,v118,v103
M 22,a[188:191],62
L 3
D 0,56,3072
M 22,a[196:199],66
R 86,1,29696
v_fma_f32 v119,v104,v119,v103
v_fma_f32 v120,v104,v120,v103
v_fma_f32 v121,v104,v121,v103
v_mul_f32 v118,v30,v118
v_mul_f32 v119,v31,v119
v_mul_f32 v120,v32,v120
v_mul_f32 v121,v33,v121
v_exp_f32 v118,v118
v_exp_f32 v119,v119
v_exp_f32 v120,v120
v_exp_f32 v121,v121
M 22,a[204:207],70
v_fma_f32 v118,v118,v105,v105
v_fma_f32 v119,v119,v105,v105
v_fma_f32 v120,v120,v105,v105
v_fma_f32 v121,v121,v105,v105
v_rcp_f32 v118,v118
v_rcp_f32 v119,v119
v_rcp_f32 v120,v120
v_rcp_f32 v121,v121
M 18,a[192:195],66
R 90,1,30720
v_mul_f32 v30,v30,v118
v_mul_f32 v31,v31,v119
v_mul_f32 v32,v32,v120
v_mul_f32 v33,v33,v121
v_cvt_pk_bf16_f32 v124,v30,v31
v_cvt_pk_bf16_f32 v125,v32,v33
M 18,a[200:203],70
L 3
buffer_store_dwordx4 v[122:125],v15,s[36:39],s67 offen offset:0 sc0 sc1
M 18,a[208:211],74
s_waitcnt vmcnt(6)
R 94,1,31744
M 18,a[216:219],78
s_barrier
ds_read_b32 v7,v6 offset:0
M 22,a[212:215],74
R 34,2,0
M 22,a[220:223],78
L 4
buffer_store_dwordx4 v[122:125],v16,s[36:39],s67 offen offset:0 sc0 sc1
buffer_store_dword v4,v3,s[36:39],s70 offen offset:0 sc0 sc1
M 22,a[228:231],82
s_mov_b32 s70,s69
s_mov_b32 s69,s68
R 38,2,1024
M 22,a[236:239],86
s_add_u32 s68,s68,s63
M 18,a[224:227],82
R 42,2,2048
M 18,a[232:235],86
L 4
L 3
M 18,a[240:243],90
R 46,2,3072
v_cmp_eq_u32 vcc,s53,v7
M 18,a[248:251],94
s_cmp_eq_u32 vcc_lo,-1
M 22,a[244:247],90
R 50,2,4096
M 22,a[252:255],94
s_waitcnt vmcnt(7) lgkmcnt(3)
s_cbranch_scc0 Lg1_L1_%=
Lg1_F1_%=:
v_mfma_f32_16x16x32_bf16 v[30:33],a[4:7],v[34:37],0
s_add_u32 s58,s58,s61
s_add_u32 m0,s59,1024
R 54,2,5120
M 30,a[12:15],38
s_add_u32 s56,s55,0
buffer_load_dword v8,s[36:39],s58 offen offset:0 sc1 lds
v_mfma_f32_16x16x32_bf16 v[26:29],a[0:3],v[34:37],0
s_add_u32 m0,s57,0
R 58,2,6144
v_fma_f32 v18,-v98,v106,v18
M 26,a[8:11],38
L 3
D 0,56,0
M 26,a[16:19],42
s_mov_b32 s67,s65
R 62,2,7168
v_fma_f32 v19,-v98,v107,v19
M 26,a[24:27],46
s_add_u32 s65,s65,s62
v_fma_f32 v20,-v98,v108,v20
v_fma_f32 v21,-v98,v109,v21
M 30,a[20:23],42
s_add_u32 s54,s54,s46
R 66,2,8192
v_mul_f32 v114,v18,v18
M 30,a[28:31],46
L 3
D 0,56,1024
M 30,a[36:39],50
R 70,2,9216
v_mul_f32 v115,v19,v19
M 30,a[44:47],54
v_mul_f32 v116,v20,v20
v_mul_f32 v117,v21,v21
M 26,a[32:35],50
R 74,2,10240
v_fma_f32 v114,v100,v114,v99
M 26,a[40:43],54
L 3
D 0,56,2048
M 26,a[48:51],58
R 78,2,11264
v_fma_f32 v115,v100,v115,v99
M 26,a[56:59],62
D 0,56,3072
M 30,a[52:55],58
s_add_u32 m0,s57,16384
R 82,2,12288
v_fma_f32 v116,v100,v116,v99
M 30,a[60:63],62
L 3
v_fma_f32 v117,v100,v117,v99
v_mul_f32 v114,v18,v114
M 30,a[68:71],66
s_add_u32 s56,s55,16384
R 86,2,13312
v_mul_f32 v115,v19,v115
M 30,a[76:79],70
s_add_u32 s55,s55,s60
buffer_load_dwordx4 v[102:105],v11,s[36:39],s73 offen offset:0 sc1
M 26,a[64:67],66
s_add_u32 s73,s73,s64
R 90,2,14336
v_mul_f32 v116,v20,v116
M 26,a[72:75],70
L 3
v_exp_f32 v114,v114
M 26,a[80:83],74
s_waitcnt vmcnt(9)
R 94,2,15360
v_mul_f32 v117,v21,v117
M 26,a[88:91],78
s_barrier
v_exp_f32 v115,v115
M 30,a[84:87],74
s_sub_u32 s57,s84,s57
R 34,2,16384
v_fma_f32 v114,v114,v101,v101
M 30,a[92:95],78
L 3
v_exp_f32 v116,v116
M 30,a[100:103],82
)"
R"(R 38,2,17408
v_fma_f32 v115,v115,v101,v101
M 30,a[108:111],86
D 0,56,0
M 26,a[96:99],82
R 42,2,18432
v_fma_f32 v116,v116,v101,v101
M 26,a[104:107],86
L 3
v_exp_f32 v117,v117
M 26,a[112:115],90
R 46,2,19456
v_fma_f32 v117,v117,v101,v101
M 26,a[120:123],94
v_rcp_f32 v114,v114
M 30,a[116:119],90
R 50,2,20480
v_mul_f32 v18,v18,v114
M 30,a[124:127],94
L 3
v_rcp_f32 v115,v115
M 30,a[132:135],34
R 54,2,21504
v_mul_f32 v19,v19,v115
M 30,a[140:143],38
D 0,56,1024
M 26,a[128:131],34
R 58,2,22528
v_cvt_pk_bf16_f32 v122,v18,v19
M 26,a[136:139],38
L 3
v_rcp_f32 v116,v116
M 26,a[144:147],42
R 62,2,23552
v_mul_f32 v20,v20,v116
M 26,a[152:155],46
v_rcp_f32 v117,v117
M 30,a[148:151],42
R 66,2,24576
v_mul_f32 v21,v21,v117
M 30,a[156:159],46
L 3
D 0,56,2048
M 30,a[164:167],50
R 70,2,25600
v_cvt_pk_bf16_f32 v123,v20,v21
M 30,a[172:175],54
v_fma_f32 v22,-v98,v110,v22
v_fma_f32 v23,-v98,v111,v23
M 26,a[160:163],50
R 74,2,26624
v_fma_f32 v24,-v98,v112,v24
M 26,a[168:171],54
L 3
v_fma_f32 v25,-v98,v113,v25
v_mul_f32 v118,v22,v22
M 26,a[176:179],58
R 78,2,27648
v_mul_f32 v119,v23,v23
M 26,a[184:187],62
v_mul_f32 v120,v24,v24
v_mul_f32 v121,v25,v25
M 30,a[180:183],58
R 82,2,28672
v_fma_f32 v118,v100,v118,v99
M 30,a[188:191],62
L 3
D 0,56,3072
M 30,a[196:199],66
R 86,2,29696
v_fma_f32 v119,v100,v119,v99
v_fma_f32 v120,v100,v120,v99
v_fma_f32 v121,v100,v121,v99
v_mul_f32 v118,v22,v118
v_mul_f32 v119,v23,v119
v_mul_f32 v120,v24,v120
v_mul_f32 v121,v25,v121
v_exp_f32 v118,v118
v_exp_f32 v119,v119
v_exp_f32 v120,v120
v_exp_f32 v121,v121
M 30,a[204:207],70
v_fma_f32 v118,v118,v101,v101
v_fma_f32 v119,v119,v101,v101
v_fma_f32 v120,v120,v101,v101
v_fma_f32 v121,v121,v101,v101
v_rcp_f32 v118,v118
v_rcp_f32 v119,v119
v_rcp_f32 v120,v120
v_rcp_f32 v121,v121
M 26,a[192:195],66
R 90,2,30720
v_mul_f32 v22,v22,v118
v_mul_f32 v23,v23,v119
v_mul_f32 v24,v24,v120
v_mul_f32 v25,v25,v121
v_cvt_pk_bf16_f32 v124,v22,v23
v_cvt_pk_bf16_f32 v125,v24,v25
M 26,a[200:203],70
L 3
buffer_store_dwordx4 v[122:125],v15,s[36:39],s66 offen offset:0 sc0 sc1
M 26,a[208:211],74
s_waitcnt vmcnt(6)
R 94,2,31744
M 26,a[216:219],78
s_barrier
ds_read_b32 v7,v6 offset:1024
M 30,a[212:215],74
R 34,1,0
M 30,a[220:223],78
L 4
buffer_store_dwordx4 v[122:125],v16,s[36:39],s66 offen offset:0 sc0 sc1
buffer_store_dword v4,v3,s[36:39],s70 offen offset:0 sc0 sc1
M 30,a[228:231],82
s_mov_b32 s70,s69
s_mov_b32 s69,s68
R 38,1,1024
M 30,a[236:239],86
s_add_u32 s68,s68,s63
M 26,a[224:227],82
R 42,1,2048
M 26,a[232:235],86
L 4
L 3
M 26,a[240:243],90
R 46,1,3072
v_cmp_eq_u32 vcc,s53,v7
M 26,a[248:251],94
s_cmp_eq_u32 vcc_lo,-1
M 30,a[244:247],90
R 50,1,4096
M 30,a[252:255],94
s_waitcnt vmcnt(7) lgkmcnt(3)
s_cbranch_scc1 Lg1_F0_%=
Lg1_L0_%=:
v_mfma_f32_16x16x32_bf16 v[22:25],a[4:7],v[34:37],0
s_add_u32 m0,s59,3072
s_mov_b32 s66,s65
R 54,1,5120
M 22,a[12:15],38
s_add_u32 s65,s65,s62
buffer_load_dword v8,s[36:39],s58 offen offset:0 sc1 lds
v_mfma_f32_16x16x32_bf16 v[18:21],a[0:3],v[34:37],0
R 58,1,6144
v_fma_f32 v26,-v102,v106,v26
M 18,a[8:11],38
L 3
v_fma_f32 v27,-v102,v107,v27
v_fma_f32 v28,-v102,v108,v28
M 18,a[16:19],42
R 62,1,7168
v_fma_f32 v29,-v102,v109,v29
M 18,a[24:27],46
v_mul_f32 v114,v26,v26
v_mul_f32 v115,v27,v27
M 22,a[20:23],42
R 66,1,8192
v_mul_f32 v116,v28,v28
M 22,a[28:31],46
L 3
v_mul_f32 v117,v29,v29
v_fma_f32 v114,v104,v114,v103
M 22,a[36:39],50
R 70,1,9216
v_fma_f32 v115,v104,v115,v103
M 22,a[44:47],54
v_fma_f32 v116,v104,v116,v103
v_fma_f32 v117,v104,v117,v103
M 18,a[32:35],50
R 74,1,10240
v_mul_f32 v114,v26,v114
M 18,a[40:43],54
L 3
v_exp_f32 v114,v114
M 18,a[48:51],58
R 78,1,11264
v_mul_f32 v115,v27,v115
M 18,a[56:59],62
v_exp_f32 v115,v115
M 22,a[52:55],58
R 82,1,12288
v_mul_f32 v116,v28,v116
M 22,a[60:63],62
L 3
v_exp_f32 v116,v116
M 22,a[68:71],66
R 86,1,13312
v_mul_f32 v117,v29,v117
M 22,a[76:79],70
buffer_load_dwordx4 v[98:101],v11,s[36:39],s73 offen offset:0 sc1
M 18,a[64:67],66
s_add_u32 s73,s73,s64
R 90,1,14336
v_fma_f32 v114,v114,v105,v105
M 18,a[72:75],70
L 3
v_exp_f32 v117,v117
M 18,a[80:83],74
s_waitcnt vmcnt(1)
R 94,1,15360
v_fma_f32 v115,v115,v105,v105
M 18,a[88:91],78
s_barrier
ds_read_b32 v7,v6 offset:3072
v_fma_f32 v116,v116,v105,v105
M 22,a[84:87],74
R 34,1,16384
v_fma_f32 v117,v117,v105,v105
M 22,a[92:95],78
L 4
buffer_store_dword v4,v3,s[36:39],s70 offen offset:0 sc0 sc1
M 22,a[100:103],82
s_mov_b32 s70,0x7fff0000
R 38,1,17408
v_fma_f32 v30,-v102,v110,v30
M 22,a[108:111],86
v_rcp_f32 v114,v114
M 18,a[96:99],82
R 42,1,18432
v_mul_f32 v26,v26,v114
M 18,a[104:107],86
L 4
L 3
v_cmp_eq_u32 vcc,s53,v7
M 18,a[112:115],90
R 46,1,19456
v_fma_f32 v31,-v102,v111,v31
M 18,a[120:123],94
s_cmp_eq_u32 vcc_lo,-1
v_rcp_f32 v115,v115
s_cbranch_scc0 Lg1_X0_%=
M 22,a[116:119],90
s_add_u32 m0,s57,0
s_add_u32 s56,s55,0
s_add_u32 s58,s58,s61
R 50,1,20480
M 22,a[124:127],94
L 3
v_rcp_f32 v116,v116
M 22,a[132:135],34
s_add_u32 s54,s54,s46
R 54,1,21504
v_mul_f32 v27,v27,v115
M 22,a[140:143],38
D 0,56,0
M 18,a[128:131],34
R 58,1,22528
v_mul_f32 v28,v28,v116
M 18,a[136:139],38
L 3
v_rcp_f32 v117,v117
M 18,a[144:147],42
R 62,1,23552
v_mul_f32 v29,v29,v117
M 18,a[152:155],46
D 0,56,1024
M 22,a[148:151],42
R 66,1,24576
v_cvt_pk_bf16_f32 v122,v26,v27
M 22,a[156:159],46
L 3
v_cvt_pk_bf16_f32 v123,v28,v29
v_fma_f32 v32,-v102,v112,v32
M 22,a[164:167],50
R 70,1,25600
v_fma_f32 v33,-v102,v113,v33
M 22,a[172:175],54
D 0,56,2048
M 18,a[160:163],50
R 74,1,26624
v_mul_f32 v118,v30,v30
M 18,a[168:171],54
L 3
v_mul_f32 v119,v31,v31
v_mul_f32 v120,v32,v32
M 18,a[176:179],58
R 78,1,27648
v_mul_f32 v121,v33,v33
M 18,a[184:187],62
D 0,56,3072
M 22,a[180:183],58
s_add_u32 m0,s59,0
R 82,1,28672
v_fma_f32 v118,v104,v118,v103
M 22,a[188:191],62
L 3
buffer_load_dword v8,s[36:39],s58 offen offset:0 sc1 lds
M 22,a[196:199],66
s_add_u32 m0,s57,16384
R 86,1,29696
v_fma_f32 v119,v104,v119,v103
v_fma_f32 v120,v104,v120,v103
v_fma_f32 v121,v104,v121,v103
v_mul_f32 v118,v30,v118
v_mul_f32 v119,v31,v119
v_mul_f32 v120,v32,v120
v_mul_f32 v121,v33,v121
v_exp_f32 v118,v118
v_exp_f32 v119,v119
v_exp_f32 v120,v120
v_exp_f32 v121,v121
M 22,a[204:207],70
s_add_u32 s56,s55,16384
v_fma_f32 v118,v118,v105,v105
v_fma_f32 v119,v119,v105,v105
v_fma_f32 v120,v120,v105,v105
v_fma_f32 v121,v121,v105,v105
v_rcp_f32 v118,v118
v_rcp_f32 v119,v119
v_rcp_f32 v120,v120
v_rcp_f32 v121,v121
M 18,a[192:195],66
s_add_u32 s55,s55,s60
R 90,1,30720
v_mul_f32 v30,v30,v118
v_mul_f32 v31,v31,v119
v_mul_f32 v32,v32,v120
v_mul_f32 v33,v33,v121
v_cvt_pk_bf16_f32 v124,v30,v31
v_cvt_pk_bf16_f32 v125,v32,v33
M 18,a[200:203],70
L 3
s_sub_u32 s57,s84,s57
buffer_store_dwordx4 v[122:125],v15,s[36:39],s67 offen offset:0 sc0 sc1
M 18,a[208:211],74
s_waitcnt vmcnt(1)
R 94,1,31744
M 18,a[216:219],78
s_barrier
ds_read_b32 v7,v6 offset:0
M 22,a[212:215],74
R 34,2,0
M 22,a[220:223],78
L 4
buffer_store_dwordx4 v[122:125],v16,s[36:39],s67 offen offset:0 sc0 sc1
buffer_store_dword v4,v3,s[36:39],s70 offen offset:0 sc0 sc1
M 22,a[228:231],82
s_mov_b32 s70,s69
s_mov_b32 s69,s68
R 38,2,1024
M 22,a[236:239],86
s_add_u32 s68,s68,s63
M 18,a[224:227],82
R 42,2,2048
M 18,a[232:235],86
L 4
D 0,56,0
M 18,a[240:243],90
L 3
R 46,2,3072
v_cmp_eq_u32 vcc,s53,v7
M 18,a[248:251],94
s_cmp_eq_u32 vcc_lo,-1
D 0,56,1024
M 22,a[244:247],90
R 50,2,4096
M 22,a[252:255],94
L 3
D 0,56,2048
D 0,56,3072
buffer_store_dword v4,v3,s[36:39],s71 offen offset:0 sc0 sc1
buffer_store_dword v4,v3,s[36:39],s71 offen offset:0 sc0 sc1
buffer_store_dword v4,v3,s[36:39],s71 offen offset:0 sc0 sc1
s_cbranch_scc1 Lg1_F1_%=
s_branch Lg1_L1_%=
Lg1_X0_%=:
M 22,a[116:119],90
s_add_u32 m0,s59,0
R 50,1,20480
v_mul_f32 v27,v27,v115
M 22,a[124:127],94
L 3
buffer_load_dword v8,s[36:39],s58 offen offset:0 sc1 lds
M 22,a[132:135],34
R 54,1,21504
v_cvt_pk_bf16_f32 v122,v26,v27
M 22,a[140:143],38
v_rcp_f32 v116,v116
M 18,a[128:131],34
R 58,1,22528
v_mul_f32 v28,v28,v116
M 18,a[136:139],38
L 3
v_rcp_f32 v117,v117
M 18,a[144:147],42
R 62,1,23552
v_mul_f32 v29,v29,v117
M 18,a[152:155],46
v_cvt_pk_bf16_f32 v123,v28,v29
v_fma_f32 v32,-v102,v112,v32
M 22,a[148:151],42
R 66,1,24576
v_fma_f32 v33,-v102,v113,v33
M 22,a[156:159],46
L 3
v_mul_f32 v118,v30,v30
v_mul_f32 v119,v31,v31
M 22,a[164:167],50
R 70,1,25600
v_mul_f32 v120,v32,v32
M 22,a[172:175],54
v_mul_f32 v121,v33,v33
v_fma_f32 v118,v104,v118,v103
M 18,a[160:163],50
R 74,1,26624
v_fma_f32 v119,v104,v119,v103
M 18,a[168:171],54
L 3
v_fma_f32 v120,v104,v120,v103
v_fma_f32 v121,v104,v121,v103
M 18,a[176:179],58
R 78,1,27648
v_mul_f32 v118,v30,v118
M 18,a[184:187],62
v_mul_f32 v119,v31,v119
v_mul_f32 v120,v32,v120
M 22,a[180:183],58
R 82,1,28672
v_mul_f32 v121,v33,v121
M 22,a[188:191],62
L 3
v_exp_f32 v118,v118
M 22,a[196:199],66
R 86,1,29696
v_exp_f32 v119,v119
v_exp_f32 v120,v120
v_exp_f32 v121,v121
M 22,a[204:207],70
v_fma_f32 v118,v118,v105,v105
v_fma_f32 v119,v119,v105,v105
v_fma_f32 v120,v120,v105,v105
v_fma_f32 v121,v121,v105,v105
v_rcp_f32 v118,v118
v_rcp_f32 v119,v119
v_rcp_f32 v120,v120
v_rcp_f32 v121,v121
M 18,a[192:195],66
R 90,1,30720
v_mul_f32 v30,v30,v118
v_mul_f32 v31,v31,v119
v_mul_f32 v32,v32,v120
v_mul_f32 v33,v33,v121
v_cvt_pk_bf16_f32 v124,v30,v31
v_cvt_pk_bf16_f32 v125,v32,v33
M 18,a[200:203],70
L 3
buffer_store_dwordx4 v[122:125],v15,s[36:39],s67 offen offset:0 sc0 sc1
M 18,a[208:211],74
s_waitcnt vmcnt(1)
R 94,1,31744
M 18,a[216:219],78
s_barrier
ds_read_b32 v7,v6 offset:0
M 22,a[212:215],74
buffer_store_dwordx4 v[122:125],v16,s[36:39],s67 offen offset:0 sc0 sc1
M 22,a[220:223],78
L 3
buffer_store_dword v4,v3,s[36:39],s70 offen offset:0 sc0 sc1
M 22,a[228:231],82
s_mov_b32 s70,s69
s_mov_b32 s69,s68
s_add_u32 s68,s68,s63
M 22,a[236:239],86
M 18,a[224:227],82
M 18,a[232:235],86
L 1
L 0
M 18,a[240:243],90
v_cmp_eq_u32 vcc,s53,v7
M 18,a[248:251],94
M 22,a[244:247],90
M 22,a[252:255],94
s_nop 0
s_mov_b32 s80,1
s_cmp_eq_u32 vcc_lo,-1
s_cbranch_scc0 Lg1_xn0_%=
s_add_u32 m0,s57,0
s_add_u32 s56,s55,0
D 0,56,0
v_fma_f32 v18,-v98,v106,v18
v_fma_f32 v19,-v98,v107,v19
v_fma_f32 v20,-v98,v108,v20
v_fma_f32 v21,-v98,v109,v21
v_mul_f32 v114,v18,v18
v_mul_f32 v115,v19,v19
v_mul_f32 v116,v20,v20
D 0,56,1024
v_mul_f32 v117,v21,v21
v_fma_f32 v114,v100,v114,v99
v_fma_f32 v115,v100,v115,v99
v_fma_f32 v116,v100,v116,v99
v_fma_f32 v117,v100,v117,v99
v_mul_f32 v114,v18,v114
v_mul_f32 v115,v19,v115
D 0,56,2048
v_mul_f32 v116,v20,v116
v_mul_f32 v117,v21,v117
v_exp_f32 v114,v114
v_exp_f32 v115,v115
v_exp_f32 v116,v116
)"
R"(v_exp_f32 v117,v117
v_fma_f32 v114,v114,v101,v101
D 0,56,3072
v_fma_f32 v115,v115,v101,v101
v_fma_f32 v116,v116,v101,v101
v_fma_f32 v117,v117,v101,v101
v_rcp_f32 v114,v114
v_rcp_f32 v115,v115
v_rcp_f32 v116,v116
v_rcp_f32 v117,v117
s_add_u32 s58,s58,s61
s_add_u32 m0,s57,16384
s_add_u32 s56,s55,16384
D 0,56,0
v_mul_f32 v18,v18,v114
v_mul_f32 v19,v19,v115
v_mul_f32 v20,v20,v116
v_mul_f32 v21,v21,v117
v_cvt_pk_bf16_f32 v122,v18,v19
v_cvt_pk_bf16_f32 v123,v20,v21
v_fma_f32 v22,-v98,v110,v22
D 0,56,1024
v_fma_f32 v23,-v98,v111,v23
v_fma_f32 v24,-v98,v112,v24
v_fma_f32 v25,-v98,v113,v25
v_mul_f32 v118,v22,v22
v_mul_f32 v119,v23,v23
v_mul_f32 v120,v24,v24
v_mul_f32 v121,v25,v25
D 0,56,2048
v_fma_f32 v118,v100,v118,v99
v_fma_f32 v119,v100,v119,v99
v_fma_f32 v120,v100,v120,v99
v_fma_f32 v121,v100,v121,v99
v_mul_f32 v118,v22,v118
v_mul_f32 v119,v23,v119
v_mul_f32 v120,v24,v120
D 0,56,3072
v_mul_f32 v121,v25,v121
v_exp_f32 v118,v118
v_exp_f32 v119,v119
v_exp_f32 v120,v120
v_exp_f32 v121,v121
v_fma_f32 v118,v118,v101,v101
v_fma_f32 v119,v119,v101,v101
s_add_u32 s55,s55,s60
s_sub_u32 s57,s84,s57
s_add_u32 s54,s54,s46
v_fma_f32 v120,v120,v101,v101
v_fma_f32 v121,v121,v101,v101
v_rcp_f32 v118,v118
v_rcp_f32 v119,v119
v_rcp_f32 v120,v120
v_rcp_f32 v121,v121
v_mul_f32 v22,v22,v118
v_mul_f32 v23,v23,v119
v_mul_f32 v24,v24,v120
v_mul_f32 v25,v25,v121
v_cvt_pk_bf16_f32 v124,v22,v23
v_cvt_pk_bf16_f32 v125,v24,v25
buffer_store_dwordx4 v[122:125],v15,s[36:39],s66 offen offset:0 sc0 sc1
buffer_store_dwordx4 v[122:125],v16,s[36:39],s66 offen offset:0 sc0 sc1
s_mov_b32 s72,s69
s_mov_b32 s69,0x7fff0000
s_mov_b32 s66,0x7fff0000
s_mov_b32 s76,0x7fff0000
s_branch Lg1_gox_%=
Lg1_xn0_%=:
s_cmp_ge_u32 s54,256
s_cbranch_scc1 Lg1_fin0_%=
s_add_u32 m0,s59,4096
s_nop 0
buffer_load_dword v8,s[36:39],s58 offen offset:0 sc1 lds
buffer_store_dword v4,v3,s[36:39],s71 offen offset:0 sc0 sc1
s_add_u32 m0,s59,5120
s_nop 0
buffer_load_dword v8,s[36:39],s58 offen offset:0 sc1 lds
buffer_store_dword v4,v3,s[36:39],s71 offen offset:0 sc0 sc1
s_mov_b32 s83,0
v_fma_f32 v18,-v98,v106,v18
v_fma_f32 v19,-v98,v107,v19
v_fma_f32 v20,-v98,v108,v20
v_fma_f32 v21,-v98,v109,v21
v_mul_f32 v114,v18,v18
v_mul_f32 v115,v19,v19
v_mul_f32 v116,v20,v20
v_mul_f32 v117,v21,v21
v_fma_f32 v114,v100,v114,v99
v_fma_f32 v115,v100,v115,v99
v_fma_f32 v116,v100,v116,v99
v_fma_f32 v117,v100,v117,v99
v_mul_f32 v114,v18,v114
v_mul_f32 v115,v19,v115
v_mul_f32 v116,v20,v116
v_mul_f32 v117,v21,v117
v_exp_f32 v114,v114
v_exp_f32 v115,v115
v_exp_f32 v116,v116
v_exp_f32 v117,v117
v_fma_f32 v114,v114,v101,v101
v_fma_f32 v115,v115,v101,v101
v_fma_f32 v116,v116,v101,v101
v_fma_f32 v117,v117,v101,v101
v_rcp_f32 v114,v114
v_rcp_f32 v115,v115
v_rcp_f32 v116,v116
v_rcp_f32 v117,v117
v_mul_f32 v18,v18,v114
v_mul_f32 v19,v19,v115
v_mul_f32 v20,v20,v116
v_mul_f32 v21,v21,v117
v_cvt_pk_bf16_f32 v122,v18,v19
v_cvt_pk_bf16_f32 v123,v20,v21
v_fma_f32 v22,-v98,v110,v22
v_fma_f32 v23,-v98,v111,v23
v_fma_f32 v24,-v98,v112,v24
v_fma_f32 v25,-v98,v113,v25
v_mul_f32 v118,v22,v22
v_mul_f32 v119,v23,v23
v_mul_f32 v120,v24,v24
v_mul_f32 v121,v25,v25
v_fma_f32 v118,v100,v118,v99
v_fma_f32 v119,v100,v119,v99
v_fma_f32 v120,v100,v120,v99
v_fma_f32 v121,v100,v121,v99
v_mul_f32 v118,v22,v118
v_mul_f32 v119,v23,v119
v_mul_f32 v120,v24,v120
v_mul_f32 v121,v25,v121
v_exp_f32 v118,v118
v_exp_f32 v119,v119
v_exp_f32 v120,v120
v_exp_f32 v121,v121
v_fma_f32 v118,v118,v101,v101
v_fma_f32 v119,v119,v101,v101
v_fma_f32 v120,v120,v101,v101
v_fma_f32 v121,v121,v101,v101
v_rcp_f32 v118,v118
v_rcp_f32 v119,v119
v_rcp_f32 v120,v120
v_rcp_f32 v121,v121
v_mul_f32 v22,v22,v118
v_mul_f32 v23,v23,v119
v_mul_f32 v24,v24,v120
v_mul_f32 v25,v25,v121
v_cvt_pk_bf16_f32 v124,v22,v23
v_cvt_pk_bf16_f32 v125,v24,v25
buffer_store_dwordx4 v[122:125],v15,s[36:39],s66 offen offset:0 sc0 sc1
buffer_store_dwordx4 v[122:125],v16,s[36:39],s66 offen offset:0 sc0 sc1
s_mov_b32 s72,s69
s_mov_b32 s69,0x7fff0000
s_mov_b32 s66,0x7fff0000
s_mov_b32 s76,0x7fff0000
s_branch Lg1_poll_%=
Lg1_fin0_%=:
v_fma_f32 v18,-v98,v106,v18
v_fma_f32 v19,-v98,v107,v19
v_fma_f32 v20,-v98,v108,v20
v_fma_f32 v21,-v98,v109,v21
v_mul_f32 v114,v18,v18
v_mul_f32 v115,v19,v19
v_mul_f32 v116,v20,v20
v_mul_f32 v117,v21,v21
v_fma_f32 v114,v100,v114,v99
v_fma_f32 v115,v100,v115,v99
v_fma_f32 v116,v100,v116,v99
v_fma_f32 v117,v100,v117,v99
v_mul_f32 v114,v18,v114
v_mul_f32 v115,v19,v115
v_mul_f32 v116,v20,v116
v_mul_f32 v117,v21,v117
v_exp_f32 v114,v114
v_exp_f32 v115,v115
v_exp_f32 v116,v116
v_exp_f32 v117,v117
v_fma_f32 v114,v114,v101,v101
v_fma_f32 v115,v115,v101,v101
v_fma_f32 v116,v116,v101,v101
v_fma_f32 v117,v117,v101,v101
v_rcp_f32 v114,v114
v_rcp_f32 v115,v115
v_rcp_f32 v116,v116
v_rcp_f32 v117,v117
v_mul_f32 v18,v18,v114
v_mul_f32 v19,v19,v115
v_mul_f32 v20,v20,v116
v_mul_f32 v21,v21,v117
v_cvt_pk_bf16_f32 v122,v18,v19
v_cvt_pk_bf16_f32 v123,v20,v21
v_fma_f32 v22,-v98,v110,v22
v_fma_f32 v23,-v98,v111,v23
v_fma_f32 v24,-v98,v112,v24
v_fma_f32 v25,-v98,v113,v25
v_mul_f32 v118,v22,v22
v_mul_f32 v119,v23,v23
v_mul_f32 v120,v24,v24
v_mul_f32 v121,v25,v25
v_fma_f32 v118,v100,v118,v99
v_fma_f32 v119,v100,v119,v99
v_fma_f32 v120,v100,v120,v99
v_fma_f32 v121,v100,v121,v99
v_mul_f32 v118,v22,v118
v_mul_f32 v119,v23,v119
v_mul_f32 v120,v24,v120
v_mul_f32 v121,v25,v121
v_exp_f32 v118,v118
v_exp_f32 v119,v119
v_exp_f32 v120,v120
v_exp_f32 v121,v121
v_fma_f32 v118,v118,v101,v101
v_fma_f32 v119,v119,v101,v101
v_fma_f32 v120,v120,v101,v101
v_fma_f32 v121,v121,v101,v101
v_rcp_f32 v118,v118
v_rcp_f32 v119,v119
v_rcp_f32 v120,v120
v_rcp_f32 v121,v121
v_mul_f32 v22,v22,v118
v_mul_f32 v23,v23,v119
v_mul_f32 v24,v24,v120
v_mul_f32 v25,v25,v121
v_cvt_pk_bf16_f32 v124,v22,v23
v_cvt_pk_bf16_f32 v125,v24,v25
buffer_store_dwordx4 v[122:125],v15,s[36:39],s66 offen offset:0 sc0 sc1
buffer_store_dwordx4 v[122:125],v16,s[36:39],s66 offen offset:0 sc0 sc1
s_mov_b32 s72,s69
s_mov_b32 s69,0x7fff0000
s_mov_b32 s66,0x7fff0000
s_mov_b32 s76,0x7fff0000
s_branch Lg1_done_%=
Lg1_L1_%=:
v_mfma_f32_16x16x32_bf16 v[30:33],a[4:7],v[34:37],0
s_add_u32 m0,s59,3072
s_mov_b32 s67,s65
R 54,2,5120
M 30,a[12:15],38
s_add_u32 s65,s65,s62
buffer_load_dword v8,s[36:39],s58 offen offset:0 sc1 lds
v_mfma_f32_16x16x32_bf16 v[26:29],a[0:3],v[34:37],0
R 58,2,6144
v_fma_f32 v18,-v98,v106,v18
M 26,a[8:11],38
L 3
v_fma_f32 v19,-v98,v107,v19
v_fma_f32 v20,-v98,v108,v20
M 26,a[16:19],42
R 62,2,7168
v_fma_f32 v21,-v98,v109,v21
M 26,a[24:27],46
v_mul_f32 v114,v18,v18
v_mul_f32 v115,v19,v19
M 30,a[20:23],42
R 66,2,8192
v_mul_f32 v116,v20,v20
M 30,a[28:31],46
L 3
v_mul_f32 v117,v21,v21
v_fma_f32 v114,v100,v114,v99
M 30,a[36:39],50
R 70,2,9216
v_fma_f32 v115,v100,v115,v99
M 30,a[44:47],54
v_fma_f32 v116,v100,v116,v99
v_fma_f32 v117,v100,v117,v99
M 26,a[32:35],50
R 74,2,10240
v_mul_f32 v114,v18,v114
M 26,a[40:43],54
L 3
v_exp_f32 v114,v114
M 26,a[48:51],58
R 78,2,11264
v_mul_f32 v115,v19,v115
M 26,a[56:59],62
v_exp_f32 v115,v115
M 30,a[52:55],58
R 82,2,12288
v_mul_f32 v116,v20,v116
M 30,a[60:63],62
L 3
v_exp_f32 v116,v116
M 30,a[68:71],66
R 86,2,13312
v_mul_f32 v117,v21,v117
M 30,a[76:79],70
buffer_load_dwordx4 v[102:105],v11,s[36:39],s73 offen offset:0 sc1
M 26,a[64:67],66
s_add_u32 s73,s73,s64
R 90,2,14336
v_fma_f32 v114,v114,v101,v101
M 26,a[72:75],70
L 3
v_exp_f32 v117,v117
M 26,a[80:83],74
s_waitcnt vmcnt(1)
R 94,2,15360
v_fma_f32 v115,v115,v101,v101
M 26,a[88:91],78
s_barrier
ds_read_b32 v7,v6 offset:3072
v_fma_f32 v116,v116,v101,v101
M 30,a[84:87],74
R 34,2,16384
v_fma_f32 v117,v117,v101,v101
M 30,a[92:95],78
L 4
buffer_store_dword v4,v3,s[36:39],s70 offen offset:0 sc0 sc1
M 30,a[100:103],82
s_mov_b32 s70,0x7fff0000
R 38,2,17408
v_fma_f32 v22,-v98,v110,v22
M 30,a[108:111],86
v_rcp_f32 v114,v114
M 26,a[96:99],82
R 42,2,18432
v_mul_f32 v18,v18,v114
M 26,a[104:107],86
L 4
L 3
v_cmp_eq_u32 vcc,s53,v7
M 26,a[112:115],90
R 46,2,19456
v_fma_f32 v23,-v98,v111,v23
M 26,a[120:123],94
s_cmp_eq_u32 vcc_lo,-1
v_rcp_f32 v115,v115
s_cbranch_scc0 Lg1_X1_%=
M 30,a[116:119],90
s_add_u32 m0,s57,0
s_add_u32 s56,s55,0
s_add_u32 s58,s58,s61
R 50,2,20480
M 30,a[124:127],94
L 3
v_rcp_f32 v116,v116
M 30,a[132:135],34
s_add_u32 s54,s54,s46
R 54,2,21504
v_mul_f32 v19,v19,v115
M 30,a[140:143],38
D 0,56,0
M 26,a[128:131],34
R 58,2,22528
v_mul_f32 v20,v20,v116
M 26,a[136:139],38
L 3
v_rcp_f32 v117,v117
M 26,a[144:147],42
R 62,2,23552
v_mul_f32 v21,v21,v117
M 26,a[152:155],46
D 0,56,1024
M 30,a[148:151],42
R 66,2,24576
v_cvt_pk_bf16_f32 v122,v18,v19
M 30,a[156:159],46
L 3
v_cvt_pk_bf16_f32 v123,v20,v21
v_fma_f32 v24,-v98,v112,v24
M 30,a[164:167],50
R 70,2,25600
v_fma_f32 v25,-v98,v113,v25
M 30,a[172:175],54
D 0,56,2048
M 26,a[160:163],50
R 74,2,26624
v_mul_f32 v118,v22,v22
M 26,a[168:171],54
L 3
v_mul_f32 v119,v23,v23
v_mul_f32 v120,v24,v24
M 26,a[176:179],58
R 78,2,27648
v_mul_f32 v121,v25,v25
M 26,a[184:187],62
D 0,56,3072
M 30,a[180:183],58
s_add_u32 m0,s59,1024
R 82,2,28672
v_fma_f32 v118,v100,v118,v99
M 30,a[188:191],62
L 3
buffer_load_dword v8,s[36:39],s58 offen offset:0 sc1 lds
M 30,a[196:199],66
s_add_u32 m0,s57,16384
R 86,2,29696
v_fma_f32 v119,v100,v119,v99
v_fma_f32 v120,v100,v120,v99
v_fma_f32 v121,v100,v121,v99
v_mul_f32 v118,v22,v118
v_mul_f32 v119,v23,v119
v_mul_f32 v120,v24,v120
v_mul_f32 v121,v25,v121
v_exp_f32 v118,v118
v_exp_f32 v119,v119
v_exp_f32 v120,v120
v_exp_f32 v121,v121
M 30,a[204:207],70
s_add_u32 s56,s55,16384
v_fma_f32 v118,v118,v101,v101
v_fma_f32 v119,v119,v101,v101
v_fma_f32 v120,v120,v101,v101
v_fma_f32 v121,v121,v101,v101
v_rcp_f32 v118,v118
v_rcp_f32 v119,v119
v_rcp_f32 v120,v120
v_rcp_f32 v121,v121
M 26,a[192:195],66
s_add_u32 s55,s55,s60
R 90,2,30720
v_mul_f32 v22,v22,v118
v_mul_f32 v23,v23,v119
v_mul_f32 v24,v24,v120
v_mul_f32 v25,v25,v121
v_cvt_pk_bf16_f32 v124,v22,v23
v_cvt_pk_bf16_f32 v125,v24,v25
M 26,a[200:203],70
L 3
s_sub_u32 s57,s84,s57
buffer_store_dwordx4 v[122:125],v15,s[36:39],s66 offen offset:0 sc0 sc1
M 26,a[208:211],74
s_waitcnt vmcnt(1)
R 94,2,31744
M 26,a[216:219],78
s_barrier
ds_read_b32 v7,v6 offset:1024
M 30,a[212:215],74
R 34,1,0
M 30,a[220:223],78
L 4
buffer_store_dwordx4 v[122:125],v16,s[36:39],s66 offen offset:0 sc0 sc1
buffer_store_dword v4,v3,s[36:39],s70 offen offset:0 sc0 sc1
M 30,a[228:231],82
s_mov_b32 s70,s69
s_mov_b32 s69,s68
R 38,1,1024
M 30,a[236:239],86
s_add_u32 s68,s68,s63
M 26,a[224:227],82
R 42,1,2048
M 26,a[232:235],86
L 4
D 0,56,0
M 26,a[240:243],90
L 3
R 46,1,3072
v_cmp_eq_u32 vcc,s53,v7
M 26,a[248:251],94
s_cmp_eq_u32 vcc_lo,-1
D 0,56,1024
M 30,a[244:247],90
R 50,1,4096
M 30,a[252:255],94
L 3
D 0,56,2048
D 0,56,3072
buffer_store_dword v4,v3,s[36:39],s71 offen offset:0 sc0 sc1
buffer_store_dword v4,v3,s[36:39],s71 offen offset:0 sc0 sc1
buffer_store_dword v4,v3,s[36:39],s71 offen offset:0 sc0 sc1
s_cbranch_scc1 Lg1_F0_%=
s_branch Lg1_L0_%=
Lg1_X1_%=:
M 30,a[116:119],90
s_add_u32 m0,s59,1024
R 50,2,20480
v_mul_f32 v19,v19,v115
M 30,a[124:127],94
L 3
buffer_load_dword v8,s[36:39],s58 offen offset:0 sc1 lds
M 30,a[132:135],34
R 54,2,21504
v_cvt_pk_bf16_f32 v122,v18,v19
M 30,a[140:143],38
v_rcp_f32 v116,v116
M 26,a[128:131],34
R 58,2,22528
v_mul_f32 v20,v20,v116
M 26,a[136:139],38
L 3
v_rcp_f32 v117,v117
M 26,a[144:147],42
R 62,2,23552
v_mul_f32 v21,v21,v117
M 26,a[152:155],46
v_cvt_pk_bf16_f32 v123,v20,v21
v_fma_f32 v24,-v98,v112,v24
M 30,a[148:151],42
R 66,2,24576
v_fma_f32 v25,-v98,v113,v25
M 30,a[156:159],46
L 3
v_mul_f32 v118,v22,v22
v_mul_f32 v119,v23,v23
M 30,a[164:167],50
R 70,2,25600
v_mul_f32 v120,v24,v24
M 30,a[172:175],54
v_mul_f32 v121,v25,v25
v_fma_f32 v118,v100,v118,v99
M 26,a[160:163],50
)"
R"(R 74,2,26624
v_fma_f32 v119,v100,v119,v99
M 26,a[168:171],54
L 3
v_fma_f32 v120,v100,v120,v99
v_fma_f32 v121,v100,v121,v99
M 26,a[176:179],58
R 78,2,27648
v_mul_f32 v118,v22,v118
M 26,a[184:187],62
v_mul_f32 v119,v23,v119
v_mul_f32 v120,v24,v120
M 30,a[180:183],58
R 82,2,28672
v_mul_f32 v121,v25,v121
M 30,a[188:191],62
L 3
v_exp_f32 v118,v118
M 30,a[196:199],66
R 86,2,29696
v_exp_f32 v119,v119
v_exp_f32 v120,v120
v_exp_f32 v121,v121
M 30,a[204:207],70
v_fma_f32 v118,v118,v101,v101
v_fma_f32 v119,v119,v101,v101
v_fma_f32 v120,v120,v101,v101
v_fma_f32 v121,v121,v101,v101
v_rcp_f32 v118,v118
v_rcp_f32 v119,v119
v_rcp_f32 v120,v120
v_rcp_f32 v121,v121
M 26,a[192:195],66
R 90,2,30720
v_mul_f32 v22,v22,v118
v_mul_f32 v23,v23,v119
v_mul_f32 v24,v24,v120
v_mul_f32 v25,v25,v121
v_cvt_pk_bf16_f32 v124,v22,v23
v_cvt_pk_bf16_f32 v125,v24,v25
M 26,a[200:203],70
L 3
buffer_store_dwordx4 v[122:125],v15,s[36:39],s66 offen offset:0 sc0 sc1
M 26,a[208:211],74
s_waitcnt vmcnt(1)
R 94,2,31744
M 26,a[216:219],78
s_barrier
ds_read_b32 v7,v6 offset:1024
M 30,a[212:215],74
buffer_store_dwordx4 v[122:125],v16,s[36:39],s66 offen offset:0 sc0 sc1
M 30,a[220:223],78
L 3
buffer_store_dword v4,v3,s[36:39],s70 offen offset:0 sc0 sc1
M 30,a[228:231],82
s_mov_b32 s70,s69
s_mov_b32 s69,s68
s_add_u32 s68,s68,s63
M 30,a[236:239],86
M 26,a[224:227],82
M 26,a[232:235],86
L 1
L 0
M 26,a[240:243],90
v_cmp_eq_u32 vcc,s53,v7
M 26,a[248:251],94
M 30,a[244:247],90
M 30,a[252:255],94
s_nop 0
s_mov_b32 s80,0
s_cmp_eq_u32 vcc_lo,-1
s_cbranch_scc0 Lg1_xn1_%=
s_add_u32 m0,s57,0
s_add_u32 s56,s55,0
D 0,56,0
v_fma_f32 v26,-v102,v106,v26
v_fma_f32 v27,-v102,v107,v27
v_fma_f32 v28,-v102,v108,v28
v_fma_f32 v29,-v102,v109,v29
v_mul_f32 v114,v26,v26
v_mul_f32 v115,v27,v27
v_mul_f32 v116,v28,v28
D 0,56,1024
v_mul_f32 v117,v29,v29
v_fma_f32 v114,v104,v114,v103
v_fma_f32 v115,v104,v115,v103
v_fma_f32 v116,v104,v116,v103
v_fma_f32 v117,v104,v117,v103
v_mul_f32 v114,v26,v114
v_mul_f32 v115,v27,v115
D 0,56,2048
v_mul_f32 v116,v28,v116
v_mul_f32 v117,v29,v117
v_exp_f32 v114,v114
v_exp_f32 v115,v115
v_exp_f32 v116,v116
v_exp_f32 v117,v117
v_fma_f32 v114,v114,v105,v105
D 0,56,3072
v_fma_f32 v115,v115,v105,v105
v_fma_f32 v116,v116,v105,v105
v_fma_f32 v117,v117,v105,v105
v_rcp_f32 v114,v114
v_rcp_f32 v115,v115
v_rcp_f32 v116,v116
v_rcp_f32 v117,v117
s_add_u32 s58,s58,s61
s_add_u32 m0,s57,16384
s_add_u32 s56,s55,16384
D 0,56,0
v_mul_f32 v26,v26,v114
v_mul_f32 v27,v27,v115
v_mul_f32 v28,v28,v116
v_mul_f32 v29,v29,v117
v_cvt_pk_bf16_f32 v122,v26,v27
v_cvt_pk_bf16_f32 v123,v28,v29
v_fma_f32 v30,-v102,v110,v30
D 0,56,1024
v_fma_f32 v31,-v102,v111,v31
v_fma_f32 v32,-v102,v112,v32
v_fma_f32 v33,-v102,v113,v33
v_mul_f32 v118,v30,v30
v_mul_f32 v119,v31,v31
v_mul_f32 v120,v32,v32
v_mul_f32 v121,v33,v33
D 0,56,2048
v_fma_f32 v118,v104,v118,v103
v_fma_f32 v119,v104,v119,v103
v_fma_f32 v120,v104,v120,v103
v_fma_f32 v121,v104,v121,v103
v_mul_f32 v118,v30,v118
v_mul_f32 v119,v31,v119
v_mul_f32 v120,v32,v120
D 0,56,3072
v_mul_f32 v121,v33,v121
v_exp_f32 v118,v118
v_exp_f32 v119,v119
v_exp_f32 v120,v120
v_exp_f32 v121,v121
v_fma_f32 v118,v118,v105,v105
v_fma_f32 v119,v119,v105,v105
s_add_u32 s55,s55,s60
s_sub_u32 s57,s84,s57
s_add_u32 s54,s54,s46
v_fma_f32 v120,v120,v105,v105
v_fma_f32 v121,v121,v105,v105
v_rcp_f32 v118,v118
v_rcp_f32 v119,v119
v_rcp_f32 v120,v120
v_rcp_f32 v121,v121
v_mul_f32 v30,v30,v118
v_mul_f32 v31,v31,v119
v_mul_f32 v32,v32,v120
v_mul_f32 v33,v33,v121
v_cvt_pk_bf16_f32 v124,v30,v31
v_cvt_pk_bf16_f32 v125,v32,v33
buffer_store_dwordx4 v[122:125],v15,s[36:39],s67 offen offset:0 sc0 sc1
buffer_store_dwordx4 v[122:125],v16,s[36:39],s67 offen offset:0 sc0 sc1
s_mov_b32 s72,s69
s_mov_b32 s69,0x7fff0000
s_mov_b32 s67,0x7fff0000
s_mov_b32 s77,0x7fff0000
s_branch Lg1_gox_%=
Lg1_xn1_%=:
s_cmp_ge_u32 s54,256
s_cbranch_scc1 Lg1_fin1_%=
s_add_u32 m0,s59,4096
s_nop 0
buffer_load_dword v8,s[36:39],s58 offen offset:0 sc1 lds
buffer_store_dword v4,v3,s[36:39],s71 offen offset:0 sc0 sc1
s_add_u32 m0,s59,5120
s_nop 0
buffer_load_dword v8,s[36:39],s58 offen offset:0 sc1 lds
buffer_store_dword v4,v3,s[36:39],s71 offen offset:0 sc0 sc1
s_mov_b32 s83,0
v_fma_f32 v26,-v102,v106,v26
v_fma_f32 v27,-v102,v107,v27
v_fma_f32 v28,-v102,v108,v28
v_fma_f32 v29,-v102,v109,v29
v_mul_f32 v114,v26,v26
v_mul_f32 v115,v27,v27
v_mul_f32 v116,v28,v28
v_mul_f32 v117,v29,v29
v_fma_f32 v114,v104,v114,v103
v_fma_f32 v115,v104,v115,v103
v_fma_f32 v116,v104,v116,v103
v_fma_f32 v117,v104,v117,v103
v_mul_f32 v114,v26,v114
v_mul_f32 v115,v27,v115
v_mul_f32 v116,v28,v116
v_mul_f32 v117,v29,v117
v_exp_f32 v114,v114
v_exp_f32 v115,v115
v_exp_f32 v116,v116
v_exp_f32 v117,v117
v_fma_f32 v114,v114,v105,v105
v_fma_f32 v115,v115,v105,v105
v_fma_f32 v116,v116,v105,v105
v_fma_f32 v117,v117,v105,v105
v_rcp_f32 v114,v114
v_rcp_f32 v115,v115
v_rcp_f32 v116,v116
v_rcp_f32 v117,v117
v_mul_f32 v26,v26,v114
v_mul_f32 v27,v27,v115
v_mul_f32 v28,v28,v116
v_mul_f32 v29,v29,v117
v_cvt_pk_bf16_f32 v122,v26,v27
v_cvt_pk_bf16_f32 v123,v28,v29
v_fma_f32 v30,-v102,v110,v30
v_fma_f32 v31,-v102,v111,v31
v_fma_f32 v32,-v102,v112,v32
v_fma_f32 v33,-v102,v113,v33
v_mul_f32 v118,v30,v30
v_mul_f32 v119,v31,v31
v_mul_f32 v120,v32,v32
v_mul_f32 v121,v33,v33
v_fma_f32 v118,v104,v118,v103
v_fma_f32 v119,v104,v119,v103
v_fma_f32 v120,v104,v120,v103
v_fma_f32 v121,v104,v121,v103
v_mul_f32 v118,v30,v118
v_mul_f32 v119,v31,v119
v_mul_f32 v120,v32,v120
v_mul_f32 v121,v33,v121
v_exp_f32 v118,v118
v_exp_f32 v119,v119
v_exp_f32 v120,v120
v_exp_f32 v121,v121
v_fma_f32 v118,v118,v105,v105
v_fma_f32 v119,v119,v105,v105
v_fma_f32 v120,v120,v105,v105
v_fma_f32 v121,v121,v105,v105
v_rcp_f32 v118,v118
v_rcp_f32 v119,v119
v_rcp_f32 v120,v120
v_rcp_f32 v121,v121
v_mul_f32 v30,v30,v118
v_mul_f32 v31,v31,v119
v_mul_f32 v32,v32,v120
v_mul_f32 v33,v33,v121
v_cvt_pk_bf16_f32 v124,v30,v31
v_cvt_pk_bf16_f32 v125,v32,v33
buffer_store_dwordx4 v[122:125],v15,s[36:39],s67 offen offset:0 sc0 sc1
buffer_store_dwordx4 v[122:125],v16,s[36:39],s67 offen offset:0 sc0 sc1
s_mov_b32 s72,s69
s_mov_b32 s69,0x7fff0000
s_mov_b32 s67,0x7fff0000
s_mov_b32 s77,0x7fff0000
s_branch Lg1_poll_%=
Lg1_fin1_%=:
v_fma_f32 v26,-v102,v106,v26
v_fma_f32 v27,-v102,v107,v27
v_fma_f32 v28,-v102,v108,v28
v_fma_f32 v29,-v102,v109,v29
v_mul_f32 v114,v26,v26
v_mul_f32 v115,v27,v27
v_mul_f32 v116,v28,v28
v_mul_f32 v117,v29,v29
v_fma_f32 v114,v104,v114,v103
v_fma_f32 v115,v104,v115,v103
v_fma_f32 v116,v104,v116,v103
v_fma_f32 v117,v104,v117,v103
v_mul_f32 v114,v26,v114
v_mul_f32 v115,v27,v115
v_mul_f32 v116,v28,v116
v_mul_f32 v117,v29,v117
v_exp_f32 v114,v114
v_exp_f32 v115,v115
v_exp_f32 v116,v116
v_exp_f32 v117,v117
v_fma_f32 v114,v114,v105,v105
v_fma_f32 v115,v115,v105,v105
v_fma_f32 v116,v116,v105,v105
v_fma_f32 v117,v117,v105,v105
v_rcp_f32 v114,v114
v_rcp_f32 v115,v115
v_rcp_f32 v116,v116
v_rcp_f32 v117,v117
v_mul_f32 v26,v26,v114
v_mul_f32 v27,v27,v115
v_mul_f32 v28,v28,v116
v_mul_f32 v29,v29,v117
v_cvt_pk_bf16_f32 v122,v26,v27
v_cvt_pk_bf16_f32 v123,v28,v29
v_fma_f32 v30,-v102,v110,v30
v_fma_f32 v31,-v102,v111,v31
v_fma_f32 v32,-v102,v112,v32
v_fma_f32 v33,-v102,v113,v33
v_mul_f32 v118,v30,v30
v_mul_f32 v119,v31,v31
v_mul_f32 v120,v32,v32
v_mul_f32 v121,v33,v33
v_fma_f32 v118,v104,v118,v103
v_fma_f32 v119,v104,v119,v103
v_fma_f32 v120,v104,v120,v103
v_fma_f32 v121,v104,v121,v103
v_mul_f32 v118,v30,v118
v_mul_f32 v119,v31,v119
v_mul_f32 v120,v32,v120
v_mul_f32 v121,v33,v121
v_exp_f32 v118,v118
v_exp_f32 v119,v119
v_exp_f32 v120,v120
v_exp_f32 v121,v121
v_fma_f32 v118,v118,v105,v105
v_fma_f32 v119,v119,v105,v105
v_fma_f32 v120,v120,v105,v105
v_fma_f32 v121,v121,v105,v105
v_rcp_f32 v118,v118
v_rcp_f32 v119,v119
v_rcp_f32 v120,v120
v_rcp_f32 v121,v121
v_mul_f32 v30,v30,v118
v_mul_f32 v31,v31,v119
v_mul_f32 v32,v32,v120
v_mul_f32 v33,v33,v121
v_cvt_pk_bf16_f32 v124,v30,v31
v_cvt_pk_bf16_f32 v125,v32,v33
buffer_store_dwordx4 v[122:125],v15,s[36:39],s67 offen offset:0 sc0 sc1
buffer_store_dwordx4 v[122:125],v16,s[36:39],s67 offen offset:0 sc0 sc1
s_mov_b32 s72,s69
s_mov_b32 s69,0x7fff0000
s_mov_b32 s67,0x7fff0000
s_mov_b32 s77,0x7fff0000
s_branch Lg1_done_%=
Lg1_done_%=:
s_waitcnt vmcnt(3)
s_barrier
buffer_store_dword v4,v3,s[36:39],s70 offen offset:0 sc0 sc1
s_waitcnt vmcnt(1)
s_barrier
buffer_store_dword v4,v3,s[36:39],s72 offen offset:0 sc0 sc1
s_waitcnt vmcnt(0) lgkmcnt(0)
s_barrier
s_branch Lg2_end_%=
Lg2_begin_%=:
Lcode_g2_b:
s_mov_b64 s[36:37],%1
s_mov_b32 s38,111243392
s_mov_b32 s39,0x00020000
s_mov_b32 s44,%2
s_mov_b32 s45,%3
s_mov_b32 s46,%4
s_mov_b32 s47,%5
s_mov_b32 s48,%6
s_mov_b32 s49,%7
s_mov_b32 s50,%8
s_mov_b32 s51,%9
s_mov_b32 s93,%10
v_mov_b32 v0,%11
v_mov_b32 v3,%12
v_mov_b32 v5,%13
v_mov_b32 v6,%14
v_mov_b32 v18,%15
v_mov_b32 v19,%16
v_mov_b32 v20,%17
v_mov_b32 v21,%18
v_mov_b32 v9,%19
v_mov_b32 v10,%20
v_mov_b32 v11,%21
v_mov_b32 v12,%22
v_mov_b32 v13,%23
v_mov_b32 v14,%24
v_mov_b32 v15,%25
v_mov_b32 v16,%26
s_mov_b64 s[40:41],%27
s_mov_b32 s42,2097152
s_mov_b32 s43,0x00020000
s_lshl_b32 s57,s44,11
s_mov_b32 s79,111243264
buffer_load_dword v4,off,s[36:39],s79 sc1
s_add_u32 s55,s50,0
s_add_u32 m0,s57,0
s_nop 0
buffer_load_dwordx4 v18,s[40:43],s55 offen offset:0  lds
s_add_u32 m0,s57,1024
s_nop 0
buffer_load_dwordx4 v19,s[40:43],s55 offen offset:0  lds
s_add_u32 m0,s57,8064
s_nop 0
buffer_load_dwordx4 v18,s[40:43],s55 offen offset:128  lds
s_add_u32 m0,s57,9088
s_nop 0
buffer_load_dwordx4 v19,s[40:43],s55 offen offset:128  lds
s_add_u32 s55,s50,131072
s_add_u32 m0,s57,16384
s_nop 0
buffer_load_dwordx4 v18,s[40:43],s55 offen offset:0  lds
s_add_u32 m0,s57,17408
s_nop 0
buffer_load_dwordx4 v19,s[40:43],s55 offen offset:0  lds
s_add_u32 m0,s57,24448
s_nop 0
buffer_load_dwordx4 v18,s[40:43],s55 offen offset:128  lds
s_add_u32 m0,s57,25472
s_nop 0
buffer_load_dwordx4 v19,s[40:43],s55 offen offset:128  lds
s_add_u32 s55,s50,262144
s_add_u32 m0,s57,32768
s_nop 0
buffer_load_dwordx4 v18,s[40:43],s55 offen offset:0  lds
s_add_u32 m0,s57,33792
s_nop 0
buffer_load_dwordx4 v19,s[40:43],s55 offen offset:0  lds
s_add_u32 m0,s57,40832
s_nop 0
buffer_load_dwordx4 v18,s[40:43],s55 offen offset:128  lds
s_add_u32 m0,s57,41856
s_nop 0
buffer_load_dwordx4 v19,s[40:43],s55 offen offset:128  lds
s_add_u32 s55,s50,393216
s_add_u32 m0,s57,49152
s_nop 0
buffer_load_dwordx4 v18,s[40:43],s55 offen offset:0  lds
s_add_u32 m0,s57,50176
s_nop 0
buffer_load_dwordx4 v19,s[40:43],s55 offen offset:0  lds
s_add_u32 m0,s57,57216
s_nop 0
buffer_load_dwordx4 v18,s[40:43],s55 offen offset:128  lds
s_add_u32 m0,s57,58240
s_nop 0
buffer_load_dwordx4 v19,s[40:43],s55 offen offset:128  lds
s_add_u32 s55,s50,524288
s_add_u32 m0,s57,65536
s_nop 0
buffer_load_dwordx4 v18,s[40:43],s55 offen offset:0  lds
s_add_u32 m0,s57,66560
s_nop 0
buffer_load_dwordx4 v19,s[40:43],s55 offen offset:0  lds
s_add_u32 m0,s57,73600
s_nop 0
buffer_load_dwordx4 v18,s[40:43],s55 offen offset:128  lds
s_add_u32 m0,s57,74624
s_nop 0
buffer_load_dwordx4 v19,s[40:43],s55 offen offset:128  lds
s_add_u32 s55,s50,655360
s_add_u32 m0,s57,81920
s_nop 0
buffer_load_dwordx4 v18,s[40:43],s55 offen offset:0  lds
s_add_u32 m0,s57,82944
s_nop 0
buffer_load_dwordx4 v19,s[40:43],s55 offen offset:0  lds
s_add_u32 m0,s57,89984
s_nop 0
buffer_load_dwordx4 v18,s[40:43],s55 offen offset:128  lds
s_add_u32 m0,s57,91008
s_nop 0
buffer_load_dwordx4 v19,s[40:43],s55 offen offset:128  lds
s_waitcnt vmcnt(24)
v_readfirstlane_b32 s53,v4
s_nop 3
s_add_u32 s53,s53,1
s_cmp_eq_u32 s53,0
s_cselect_b32 s53,1,s53
v_mov_b32 v4,s53
s_waitcnt vmcnt(20) lgkmcnt(0)
s_barrier
s_add_u32 s55,s50,786432
s_add_u32 m0,s57,98304
s_nop 0
buffer_load_dwordx4 v18,s[40:43],s55 offen offset:0  lds
s_add_u32 m0,s57,99328
s_nop 0
buffer_load_dwordx4 v19,s[40:43],s55 offen offset:0  lds
s_add_u32 m0,s57,106368
s_nop 0
buffer_load_dwordx4 v18,s[40:43],s55 offen offset:128  lds
s_add_u32 m0,s57,107392
s_nop 0
buffer_load_dwordx4 v19,s[40:43],s55 offen offset:128  lds
v_add_u32 v26,0,v20
v_add_u32 v27,0,v21
T a[0:1],26,0
T a[2:3],26,512
T a[4:5],27,0
T a[6:7],27,512
T a[8:9],26,4096
T a[10:11],26,4608
T a[12:13],27,4096
T a[14:15],27,4608
s_waitcnt vmcnt(20) lgkmcnt(0)
s_barrier
s_add_u32 s55,s50,917504
s_add_u32 m0,s57,0
s_nop 0
buffer_load_dwordx4 v18,s[40:43],s55 offen offset:0  lds
s_add_u32 m0,s57,1024
s_nop 0
buffer_load_dwordx4 v19,s[40:43],s55 offen offset:0  lds
s_add_u32 m0,s57,8064
s_nop 0
buffer_load_dwordx4 v18,s[40:43],s55 offen offset:128  lds
s_add_u32 m0,s57,9088
s_nop 0
buffer_load_dwordx4 v19,s[40:43],s55 offen offset:128  lds
v_add_u32 v26,16384,v20
v_add_u32 v27,16384,v21
T a[16:17],26,0
)"
R"(T a[18:19],26,512
T a[20:21],27,0
T a[22:23],27,512
T a[24:25],26,4096
T a[26:27],26,4608
T a[28:29],27,4096
T a[30:31],27,4608
s_waitcnt vmcnt(20) lgkmcnt(0)
s_barrier
s_add_u32 s55,s50,1048576
s_add_u32 m0,s57,16384
s_nop 0
buffer_load_dwordx4 v18,s[40:43],s55 offen offset:0  lds
s_add_u32 m0,s57,17408
s_nop 0
buffer_load_dwordx4 v19,s[40:43],s55 offen offset:0  lds
s_add_u32 m0,s57,24448
s_nop 0
buffer_load_dwordx4 v18,s[40:43],s55 offen offset:128  lds
s_add_u32 m0,s57,25472
s_nop 0
buffer_load_dwordx4 v19,s[40:43],s55 offen offset:128  lds
v_add_u32 v26,32768,v20
v_add_u32 v27,32768,v21
T a[32:33],26,0
T a[34:35],26,512
T a[36:37],27,0
T a[38:39],27,512
T a[40:41],26,4096
T a[42:43],26,4608
T a[44:45],27,4096
T a[46:47],27,4608
s_waitcnt vmcnt(20) lgkmcnt(0)
s_barrier
s_add_u32 s55,s50,1179648
s_add_u32 m0,s57,32768
s_nop 0
buffer_load_dwordx4 v18,s[40:43],s55 offen offset:0  lds
s_add_u32 m0,s57,33792
s_nop 0
buffer_load_dwordx4 v19,s[40:43],s55 offen offset:0  lds
s_add_u32 m0,s57,40832
s_nop 0
buffer_load_dwordx4 v18,s[40:43],s55 offen offset:128  lds
s_add_u32 m0,s57,41856
s_nop 0
buffer_load_dwordx4 v19,s[40:43],s55 offen offset:128  lds
v_add_u32 v26,49152,v20
v_add_u32 v27,49152,v21
T a[48:49],26,0
T a[50:51],26,512
T a[52:53],27,0
T a[54:55],27,512
T a[56:57],26,4096
T a[58:59],26,4608
T a[60:61],27,4096
T a[62:63],27,4608
s_waitcnt vmcnt(20) lgkmcnt(0)
s_barrier
s_add_u32 s55,s50,1310720
s_add_u32 m0,s57,49152
s_nop 0
buffer_load_dwordx4 v18,s[40:43],s55 offen offset:0  lds
s_add_u32 m0,s57,50176
s_nop 0
buffer_load_dwordx4 v19,s[40:43],s55 offen offset:0  lds
s_add_u32 m0,s57,57216
s_nop 0
buffer_load_dwordx4 v18,s[40:43],s55 offen offset:128  lds
s_add_u32 m0,s57,58240
s_nop 0
buffer_load_dwordx4 v19,s[40:43],s55 offen offset:128  lds
v_add_u32 v26,65536,v20
v_add_u32 v27,65536,v21
T a[64:65],26,0
T a[66:67],26,512
T a[68:69],27,0
T a[70:71],27,512
T a[72:73],26,4096
T a[74:75],26,4608
T a[76:77],27,4096
T a[78:79],27,4608
s_waitcnt vmcnt(20) lgkmcnt(0)
s_barrier
s_add_u32 s55,s50,1441792
s_add_u32 m0,s57,65536
s_nop 0
buffer_load_dwordx4 v18,s[40:43],s55 offen offset:0  lds
s_add_u32 m0,s57,66560
s_nop 0
buffer_load_dwordx4 v19,s[40:43],s55 offen offset:0  lds
s_add_u32 m0,s57,73600
s_nop 0
buffer_load_dwordx4 v18,s[40:43],s55 offen offset:128  lds
s_add_u32 m0,s57,74624
s_nop 0
buffer_load_dwordx4 v19,s[40:43],s55 offen offset:128  lds
v_add_u32 v26,81920,v20
v_add_u32 v27,81920,v21
T a[80:81],26,0
T a[82:83],26,512
T a[84:85],27,0
T a[86:87],27,512
T a[88:89],26,4096
T a[90:91],26,4608
T a[92:93],27,4096
T a[94:95],27,4608
s_waitcnt vmcnt(20) lgkmcnt(0)
s_barrier
s_add_u32 s55,s50,1572864
s_add_u32 m0,s57,81920
s_nop 0
buffer_load_dwordx4 v18,s[40:43],s55 offen offset:0  lds
s_add_u32 m0,s57,82944
s_nop 0
buffer_load_dwordx4 v19,s[40:43],s55 offen offset:0  lds
s_add_u32 m0,s57,89984
s_nop 0
buffer_load_dwordx4 v18,s[40:43],s55 offen offset:128  lds
s_add_u32 m0,s57,91008
s_nop 0
buffer_load_dwordx4 v19,s[40:43],s55 offen offset:128  lds
v_add_u32 v26,98304,v20
v_add_u32 v27,98304,v21
T a[96:97],26,0
T a[98:99],26,512
T a[100:101],27,0
T a[102:103],27,512
T a[104:105],26,4096
T a[106:107],26,4608
T a[108:109],27,4096
T a[110:111],27,4608
s_waitcnt vmcnt(20) lgkmcnt(0)
s_barrier
s_add_u32 s55,s50,1703936
s_add_u32 m0,s57,98304
s_nop 0
buffer_load_dwordx4 v18,s[40:43],s55 offen offset:0  lds
s_add_u32 m0,s57,99328
s_nop 0
buffer_load_dwordx4 v19,s[40:43],s55 offen offset:0  lds
s_add_u32 m0,s57,106368
s_nop 0
buffer_load_dwordx4 v18,s[40:43],s55 offen offset:128  lds
s_add_u32 m0,s57,107392
s_nop 0
buffer_load_dwordx4 v19,s[40:43],s55 offen offset:128  lds
v_add_u32 v26,0,v20
v_add_u32 v27,0,v21
T a[112:113],26,0
T a[114:115],26,512
T a[116:117],27,0
T a[118:119],27,512
T a[120:121],26,4096
T a[122:123],26,4608
T a[124:125],27,4096
T a[126:127],27,4608
s_waitcnt vmcnt(20) lgkmcnt(0)
s_barrier
s_add_u32 s55,s50,1835008
s_add_u32 m0,s57,0
s_nop 0
buffer_load_dwordx4 v18,s[40:43],s55 offen offset:0  lds
s_add_u32 m0,s57,1024
s_nop 0
buffer_load_dwordx4 v19,s[40:43],s55 offen offset:0  lds
s_add_u32 m0,s57,8064
s_nop 0
buffer_load_dwordx4 v18,s[40:43],s55 offen offset:128  lds
s_add_u32 m0,s57,9088
s_nop 0
buffer_load_dwordx4 v19,s[40:43],s55 offen offset:128  lds
v_add_u32 v26,16384,v20
v_add_u32 v27,16384,v21
T a[128:129],26,0
T a[130:131],26,512
T a[132:133],27,0
T a[134:135],27,512
T a[136:137],26,4096
T a[138:139],26,4608
T a[140:141],27,4096
T a[142:143],27,4608
s_waitcnt vmcnt(20) lgkmcnt(0)
s_barrier
s_add_u32 s55,s50,1966080
s_add_u32 m0,s57,16384
s_nop 0
buffer_load_dwordx4 v18,s[40:43],s55 offen offset:0  lds
s_add_u32 m0,s57,17408
s_nop 0
buffer_load_dwordx4 v19,s[40:43],s55 offen offset:0  lds
s_add_u32 m0,s57,24448
s_nop 0
buffer_load_dwordx4 v18,s[40:43],s55 offen offset:128  lds
s_add_u32 m0,s57,25472
s_nop 0
buffer_load_dwordx4 v19,s[40:43],s55 offen offset:128  lds
v_add_u32 v26,32768,v20
v_add_u32 v27,32768,v21
T a[144:145],26,0
T a[146:147],26,512
T a[148:149],27,0
T a[150:151],27,512
T a[152:153],26,4096
T a[154:155],26,4608
T a[156:157],27,4096
T a[158:159],27,4608
s_waitcnt vmcnt(20) lgkmcnt(0)
s_barrier
v_add_u32 v26,49152,v20
v_add_u32 v27,49152,v21
T a[160:161],26,0
T a[162:163],26,512
T a[164:165],27,0
T a[166:167],27,512
T a[168:169],26,4096
T a[170:171],26,4608
T a[172:173],27,4096
T a[174:175],27,4608
s_waitcnt vmcnt(16) lgkmcnt(0)
s_barrier
v_add_u32 v26,65536,v20
v_add_u32 v27,65536,v21
T a[176:177],26,0
T a[178:179],26,512
T a[180:181],27,0
T a[182:183],27,512
T a[184:185],26,4096
T a[186:187],26,4608
T a[188:189],27,4096
T a[190:191],27,4608
s_waitcnt vmcnt(12) lgkmcnt(0)
s_barrier
v_add_u32 v26,81920,v20
v_add_u32 v27,81920,v21
T a[192:193],26,0
T a[194:195],26,512
T a[196:197],27,0
T a[198:199],27,512
T a[200:201],26,4096
T a[202:203],26,4608
T a[204:205],27,4096
T a[206:207],27,4608
s_waitcnt vmcnt(8) lgkmcnt(0)
s_barrier
v_add_u32 v26,98304,v20
v_add_u32 v27,98304,v21
T a[208:209],26,0
T a[210:211],26,512
T a[212:213],27,0
T a[214:215],27,512
T a[216:217],26,4096
T a[218:219],26,4608
T a[220:221],27,4096
T a[222:223],27,4608
s_waitcnt vmcnt(4) lgkmcnt(0)
s_barrier
v_add_u32 v26,0,v20
v_add_u32 v27,0,v21
T a[224:225],26,0
T a[226:227],26,512
T a[228:229],27,0
T a[230:231],27,512
T a[232:233],26,4096
T a[234:235],26,4608
T a[236:237],27,4096
T a[238:239],27,4608
s_waitcnt vmcnt(0) lgkmcnt(0)
s_barrier
v_add_u32 v26,16384,v20
v_add_u32 v27,16384,v21
T a[240:241],26,0
T a[242:243],26,512
T a[244:245],27,0
T a[246:247],27,512
T a[248:249],26,4096
T a[250:251],26,4608
T a[252:253],27,4096
T a[254:255],27,4608
L 0
s_barrier
v_mov_b32 v4,s53
s_mov_b32 s82,0
s_mov_b32 s80,0
s_mov_b32 s54,s45
s_mul_i32 s57,s44,5120
s_mul_i32 s55,s45,40960
s_add_u32 s55,s55,s57
s_add_u32 s55,s55,17825792
s_mul_i32 s57,s44,4096
s_mul_i32 s60,s46,40960
s_mul_i32 s61,s46,1024
s_mul_i32 s62,s46,32768
s_mul_i32 s63,s46,1024
s_mul_i32 s64,s46,512
s_lshl_b32 s59,s44,8
s_add_u32 s59,s59,131072
s_mul_i32 s58,s45,1024
s_add_u32 s58,s58,109333504
s_mul_i32 s65,s45,32768
s_add_u32 s65,s65,s47
s_mul_i32 s68,s45,1024
s_add_u32 s68,s68,s48
s_mul_i32 s73,s45,512
s_add_u32 s73,s73,106954752
s_mul_i32 s74,s45,32768
s_add_u32 s74,s74,s49
s_mul_i32 s75,s45,1024
s_add_u32 s75,s75,107479040
s_mov_b32 s66,0x7fff0000
s_mov_b32 s67,0x7fff0000
s_mov_b32 s69,0x7fff0000
s_mov_b32 s76,0x7fff0000
s_mov_b32 s77,0x7fff0000
s_mov_b32 s70,0x7fff0000
s_mov_b32 s71,0x7fff0000
s_mov_b32 s72,0x7fff0000
v_mov_b32 v1,v0
v_add_u32 v2,16384,v0
s_mov_b32 s84,16384
s_mov_b32 s86,0x3a800000
s_mov_b32 s87,0x38a7c5ac
s_mov_b32 s88,0xc0135761
s_mov_b32 s89,0x3d372713
s_lshr_b32 s85,s44,1
s_mul_i32 s85,s85,127
v_lshlrev_b32 v8,22,v0
v_and_b32 v8,0x80000000,v8
v_or_b32 v8,v8,v5
v_add_u32 v2,32768,v0
s_lshl_b32 s84,s57,1
s_add_u32 s84,s84,32768
s_getpc_b64 s[40:41]
Lg2_pc_%=:
s_lshl_b32 s78,s44,13
v_lshlrev_b32 v106,3,v0
v_add_u32 v106,s78,v106
v_min_u32 v106,Lcode_g12_e-Lg2_pc_%=-4,v106
s_mov_b32 m0,163584
s_nop 0
global_load_lds_dword v106,s[40:41]
s_and_b32 s79,s54,127
s_cmp_eq_u32 s79,0
s_cselect_b32 s94,0x3300,0
s_cmp_eq_u32 s79,127
s_cselect_b32 s94,0xcc00,s94
s_add_u32 m0,s59,4096
s_nop 0
buffer_load_dword v8,s[36:39],s58 offen offset:0 sc1 lds
buffer_store_dword v4,v3,s[36:39],s71 offen offset:0 sc0 sc1
s_add_u32 m0,s59,5120
s_nop 0
buffer_load_dword v8,s[36:39],s58 offen offset:0 sc1 lds
buffer_store_dword v4,v3,s[36:39],s71 offen offset:0 sc0 sc1
s_mov_b32 s83,0
Lg2_poll_%=:
s_add_u32 s78,s83,2
s_and_b32 s78,s78,3
s_lshl_b32 s78,s78,10
s_add_u32 s78,s78,4096
s_add_u32 m0,s59,s78
s_nop 0
buffer_load_dword v8,s[36:39],s58 offen offset:0 sc1 lds
s_and_b32 s78,s83,3
s_lshl_b32 s78,s78,10
v_add_u32 v106,s78,v6
s_waitcnt vmcnt(4)
s_barrier
ds_read_b32 v7,v106 offset:4096
buffer_store_dword v4,v3,s[36:39],s70 offen offset:0 sc0 sc1
s_mov_b32 s70,s71
s_mov_b32 s71,s72
s_mov_b32 s72,0x7fff0000
s_add_u32 s83,s83,1
s_add_u32 s82,s82,1
L 0
v_cmp_eq_u32 vcc,s53,v7
s_nop 0
s_or_b64 vcc,vcc,s[94:95]
s_cmp_eq_u32 vcc_lo,-1
s_cbranch_scc1 Lg2_go_%=
s_cmp_lt_u32 s82,20000
s_cbranch_scc1 Lg2_poll_%=
Lg2_go_%=:
s_and_b32 s78,s54,127
s_cmp_eq_u32 s78,s85
s_cselect_b64 s[40:41],-1,0
v_cndmask_b32 v17,v15,v16,s[40:41]
s_add_u32 m0,s57,0
s_add_u32 s56,s55,0
D 17,56,0
s_add_u32 s56,s55,256
D 17,56,1024
s_add_u32 s56,s55,512
D 17,56,2048
s_add_u32 s56,s55,768
D 17,56,3072
s_add_u32 s58,s58,s61
s_add_u32 m0,s59,2048
s_nop 0
buffer_load_dword v8,s[36:39],s58 offen offset:0 sc1 lds
v_cndmask_b32 v17,v10,v9,s[40:41]
s_add_u32 m0,s57,16384
s_add_u32 s56,s55,20480
D 17,56,0
s_add_u32 s56,s55,20736
D 17,56,1024
s_add_u32 s56,s55,20992
D 17,56,2048
s_add_u32 s56,s55,21248
D 17,56,3072
s_add_u32 s55,s55,s60
s_sub_u32 s57,s84,s57
s_add_u32 s54,s54,s46
s_and_b32 s79,s54,127
s_cmp_eq_u32 s79,0
s_cselect_b32 s94,0x3300,0
s_cmp_eq_u32 s79,127
s_cselect_b32 s94,0xcc00,s94
s_waitcnt vmcnt(4)
s_barrier
buffer_store_dword v4,v3,s[36:39],s70 offen offset:0 sc0 sc1
s_min_u32 s71,s71,s72
buffer_store_dword v4,v3,s[36:39],s71 offen offset:0 sc0 sc1
s_mov_b32 s70,0x7fff0000
s_mov_b32 s71,0x7fff0000
s_mov_b32 s72,0x7fff0000
s_cmp_eq_u32 s80,1
buffer_store_dword v4,v3,s[36:39],s71 offen offset:0 sc0 sc1
s_cbranch_scc1 Lg2_st1_%=
s_branch Lg2_st0_%=
Lg2_gox_%=:
s_add_u32 m0,s59,2048
s_nop 0
buffer_load_dword v8,s[36:39],s58 offen offset:0 sc1 lds
s_and_b32 s79,s54,127
s_cmp_eq_u32 s79,0
s_cselect_b32 s94,0x3300,0
s_cmp_eq_u32 s79,127
s_cselect_b32 s94,0xcc00,s94
s_waitcnt vmcnt(0)
s_barrier
buffer_store_dword v4,v3,s[36:39],s70 offen offset:0 sc0 sc1
s_min_u32 s71,s71,s72
buffer_store_dword v4,v3,s[36:39],s71 offen offset:0 sc0 sc1
s_mov_b32 s70,0x7fff0000
s_mov_b32 s71,0x7fff0000
s_mov_b32 s72,0x7fff0000
s_cmp_eq_u32 s80,1
s_cbranch_scc1 Lg2_st1_%=
Lg2_st0_%=:
ds_read_b32 v7,v6 offset:2048
R 34,1,0
R 38,1,1024
R 42,1,2048
R 46,1,3072
R 50,1,4096
L 5
v_cmp_eq_u32 vcc,s53,v7
s_nop 0
s_or_b64 vcc,vcc,s[94:95]
L 3
s_cmp_eq_u32 vcc_lo,-1
s_cbranch_scc1 Lg2_F0_%=
s_branch Lg2_L0_%=
Lg2_st1_%=:
ds_read_b32 v7,v6 offset:2048
R 34,2,0
R 38,2,1024
R 42,2,2048
R 46,2,3072
R 50,2,4096
L 5
v_cmp_eq_u32 vcc,s53,v7
s_nop 0
s_or_b64 vcc,vcc,s[94:95]
L 3
s_cmp_eq_u32 vcc_lo,-1
s_cbranch_scc1 Lg2_F1_%=
s_branch Lg2_L1_%=
Lg2_F0_%=:
v_mfma_f32_16x16x32_bf16 v[22:25],a[4:7],v[34:37],0
s_add_u32 s58,s58,s61
s_add_u32 m0,s59,0
s_and_b32 s78,s54,127
R 54,1,5120
M 22,a[12:15],38
s_cmp_eq_u32 s78,s85
s_cselect_b64 s[40:41],-1,0
s_add_u32 s56,s55,0
buffer_load_dword v8,s[36:39],s58 offen offset:0 sc1 lds
v_mfma_f32_16x16x32_bf16 v[18:21],a[0:3],v[34:37],0
s_add_u32 m0,s57,0
R 58,1,6144
v_cndmask_b32 v17,v15,v16,s[40:41]
M 18,a[8:11],38
L 3
D 17,56,0
M 18,a[16:19],42
s_add_u32 s56,s55,256
)"
R"(R 62,1,7168
v_lshlrev_b32 v106,16,v126
M 18,a[24:27],46
s_add_u32 s54,s54,s46
v_and_b32 v107,0xffff0000,v126
v_lshlrev_b32 v108,16,v127
M 22,a[20:23],42
s_and_b32 s79,s54,127
R 66,1,8192
v_and_b32 v109,0xffff0000,v127
M 22,a[28:31],46
L 3
D 17,56,1024
M 22,a[36:39],50
s_add_u32 s56,s55,512
R 70,1,9216
v_lshlrev_b32 v110,16,v128
M 22,a[44:47],54
s_cmp_eq_u32 s79,0
v_and_b32 v111,0xffff0000,v128
v_lshlrev_b32 v112,16,v129
M 18,a[32:35],50
s_cselect_b32 s94,0x3300,0
R 74,1,10240
v_and_b32 v113,0xffff0000,v129
M 18,a[40:43],54
L 3
D 17,56,2048
M 18,a[48:51],58
s_add_u32 s56,s55,768
R 78,1,11264
v_fma_f32 v106,v106,v103,v102
M 18,a[56:59],62
s_cmp_eq_u32 s79,127
D 17,56,3072
M 22,a[52:55],58
s_cselect_b32 s94,0xcc00,s94
R 82,1,12288
v_fma_f32 v107,v107,v103,v102
M 22,a[60:63],62
L 3
v_fma_f32 v108,v108,v103,v102
v_fma_f32 v109,v109,v103,v102
M 22,a[68:71],66
s_add_u32 m0,s57,16384
R 86,1,13312
v_fma_f32 v110,v110,v103,v102
M 22,a[76:79],70
s_add_u32 s56,s55,20480
buffer_load_dwordx4 v[122:125],v0,s[36:39],s74 offen offset:0 sc1
M 18,a[64:67],66
s_mov_b32 s66,s65
R 90,1,14336
v_fma_f32 v111,v111,v103,v102
M 18,a[72:75],70
L 3
buffer_load_dwordx2 v[98:99],v11,s[36:39],s73 offen offset:256 sc1
M 18,a[80:83],74
s_waitcnt vmcnt(10)
R 94,1,15360
v_fma_f32 v112,v112,v103,v102
M 18,a[88:91],78
s_barrier
v_fma_f32 v113,v113,v103,v102
v_add_f32 v26,v26,v106
M 22,a[84:87],74
s_mov_b32 s76,s75
R 34,1,16384
v_add_f32 v27,v27,v107
M 22,a[92:95],78
L 3
v_add_f32 v28,v28,v108
v_add_f32 v29,v29,v109
M 22,a[100:103],82
s_add_u32 s73,s73,s64
R 38,1,17408
v_add_f32 v30,v30,v110
M 22,a[108:111],86
s_add_u32 s74,s74,s62
v_cndmask_b32 v17,v10,v9,s[40:41]
v_add_f32 v31,v31,v111
M 18,a[96:99],82
s_add_u32 s65,s65,s62
R 42,1,18432
v_add_f32 v32,v32,v112
M 18,a[104:107],86
L 3
D 17,56,0
M 18,a[112:115],90
s_add_u32 s56,s55,20736
R 46,1,19456
v_add_f32 v33,v33,v113
M 18,a[120:123],94
s_add_u32 s75,s75,s63
v_cvt_pk_bf16_f32 v114,v26,v27
v_cvt_pk_bf16_f32 v115,v28,v29
M 22,a[116:119],90
s_sub_u32 s57,s84,s57
R 50,1,20480
v_fma_f32 v107,v26,v26,s87
M 22,a[124:127],94
L 3
v_add_f32 v106,v26,v27
v_fma_f32 v107,v27,v27,v107
M 22,a[132:135],34
R 54,1,21504
v_add_f32 v106,v106,v28
M 22,a[140:143],38
D 17,56,1024
M 18,a[128:131],34
s_add_u32 s56,s55,20992
R 58,1,22528
v_fma_f32 v107,v28,v28,v107
M 18,a[136:139],38
L 3
v_add_f32 v106,v106,v29
v_fma_f32 v107,v29,v29,v107
M 18,a[144:147],42
R 62,1,23552
v_add_f32 v106,v106,v30
M 18,a[152:155],46
v_fma_f32 v107,v30,v30,v107
v_add_f32 v106,v106,v31
M 22,a[148:151],42
R 66,1,24576
v_fma_f32 v107,v31,v31,v107
M 22,a[156:159],46
L 3
v_add_f32 v106,v106,v32
v_fma_f32 v107,v32,v32,v107
M 22,a[164:167],50
R 70,1,25600
v_add_f32 v106,v106,v33
M 22,a[172:175],54
v_fma_f32 v107,v33,v33,v107
v_cvt_pk_bf16_f32 v116,v30,v31
M 18,a[160:163],50
R 74,1,26624
v_cvt_pk_bf16_f32 v117,v32,v33
M 18,a[168:171],54
L 3
v_permlane16_swap_b32 v106,v107
v_add_f32 v106,v106,v107
v_mov_b32 v107,v106
M 18,a[176:179],58
R 78,1,27648
M 18,a[184:187],62
v_permlane32_swap_b32 v106,v107
v_add_f32 v106,v106,v107
M 22,a[180:183],58
R 82,1,28672
ds_write_b32 v12,v106 offset:2048
M 22,a[188:191],62
L 4
D 17,56,2048
M 22,a[196:199],66
s_add_u32 s56,s55,21248
s_add_u32 s55,s55,s60
R 86,1,29696
M 22,a[204:207],70
D 17,56,3072
M 18,a[192:195],66
R 90,1,30720
M 18,a[200:203],70
L 4
L 2
buffer_store_dwordx4 v[114:117],v0,s[36:39],s67 offen offset:0 sc0 sc1
M 18,a[208:211],74
s_waitcnt vmcnt(7)
R 94,1,31744
M 18,a[216:219],78
s_barrier
R 118,13,2048
ds_read_b32 v7,v6 offset:0
M 22,a[212:215],74
R 34,2,0
M 22,a[220:223],78
L 5
buffer_store_dword v4,v3,s[36:39],s70 offen offset:0 sc0 sc1
M 22,a[228:231],82
s_mov_b32 s70,s69
s_mov_b32 s69,s68
R 38,2,1024
M 22,a[236:239],86
s_add_u32 s68,s68,s63
M 18,a[224:227],82
R 42,2,2048
M 18,a[232:235],86
L 5
L 4
L 3
M 18,a[240:243],90
R 46,2,3072
v_add_f32 v118,v118,v119
M 18,a[248:251],94
v_cmp_eq_u32 vcc,s53,v7
v_add_f32 v120,v120,v121
M 22,a[244:247],90
s_or_b64 vcc,vcc,s[94:95]
s_cmp_eq_u32 vcc_lo,-1
R 50,2,4096
v_add_f32 v118,v118,v120
v_mul_f32 v118,s86,v118
M 22,a[252:255],94
s_waitcnt vmcnt(6) lgkmcnt(3)
buffer_store_dword v118,v14,s[36:39],s77 offen offset:0 sc0 sc1
s_cbranch_scc0 Lg2_L1_%=
Lg2_F1_%=:
v_mfma_f32_16x16x32_bf16 v[30:33],a[4:7],v[34:37],0
s_add_u32 s58,s58,s61
s_add_u32 m0,s59,1024
s_and_b32 s78,s54,127
R 54,2,5120
M 30,a[12:15],38
s_cmp_eq_u32 s78,s85
s_cselect_b64 s[40:41],-1,0
s_add_u32 s56,s55,0
buffer_load_dword v8,s[36:39],s58 offen offset:0 sc1 lds
v_mfma_f32_16x16x32_bf16 v[26:29],a[0:3],v[34:37],0
s_add_u32 m0,s57,0
R 58,2,6144
v_cndmask_b32 v17,v15,v16,s[40:41]
M 26,a[8:11],38
L 3
D 17,56,0
M 26,a[16:19],42
s_add_u32 s56,s55,256
R 62,2,7168
v_lshlrev_b32 v106,16,v122
M 26,a[24:27],46
s_add_u32 s54,s54,s46
v_and_b32 v107,0xffff0000,v122
v_lshlrev_b32 v108,16,v123
M 30,a[20:23],42
s_and_b32 s79,s54,127
R 66,2,8192
v_and_b32 v109,0xffff0000,v123
M 30,a[28:31],46
L 3
D 17,56,1024
M 30,a[36:39],50
s_add_u32 s56,s55,512
R 70,2,9216
v_lshlrev_b32 v110,16,v124
M 30,a[44:47],54
s_cmp_eq_u32 s79,0
v_and_b32 v111,0xffff0000,v124
v_lshlrev_b32 v112,16,v125
M 26,a[32:35],50
s_cselect_b32 s94,0x3300,0
R 74,2,10240
v_and_b32 v113,0xffff0000,v125
M 26,a[40:43],54
L 3
D 17,56,2048
M 26,a[48:51],58
s_add_u32 s56,s55,768
R 78,2,11264
v_fma_f32 v106,v106,v99,v98
M 26,a[56:59],62
s_cmp_eq_u32 s79,127
D 17,56,3072
M 30,a[52:55],58
s_cselect_b32 s94,0xcc00,s94
R 82,2,12288
v_fma_f32 v107,v107,v99,v98
M 30,a[60:63],62
L 3
v_fma_f32 v108,v108,v99,v98
v_fma_f32 v109,v109,v99,v98
M 30,a[68:71],66
s_add_u32 m0,s57,16384
R 86,2,13312
v_fma_f32 v110,v110,v99,v98
M 30,a[76:79],70
s_add_u32 s56,s55,20480
buffer_load_dwordx4 v[126:129],v0,s[36:39],s74 offen offset:0 sc1
M 26,a[64:67],66
s_mov_b32 s67,s65
R 90,2,14336
v_fma_f32 v111,v111,v99,v98
M 26,a[72:75],70
L 3
buffer_load_dwordx2 v[102:103],v11,s[36:39],s73 offen offset:256 sc1
M 26,a[80:83],74
s_waitcnt vmcnt(10)
R 94,2,15360
v_fma_f32 v112,v112,v99,v98
M 26,a[88:91],78
s_barrier
v_fma_f32 v113,v113,v99,v98
v_add_f32 v18,v18,v106
M 30,a[84:87],74
s_mov_b32 s77,s75
R 34,2,16384
v_add_f32 v19,v19,v107
M 30,a[92:95],78
L 3
v_add_f32 v20,v20,v108
v_add_f32 v21,v21,v109
M 30,a[100:103],82
s_add_u32 s73,s73,s64
R 38,2,17408
v_add_f32 v22,v22,v110
M 30,a[108:111],86
s_add_u32 s74,s74,s62
v_cndmask_b32 v17,v10,v9,s[40:41]
v_add_f32 v23,v23,v111
M 26,a[96:99],82
s_add_u32 s65,s65,s62
R 42,2,18432
v_add_f32 v24,v24,v112
M 26,a[104:107],86
L 3
D 17,56,0
M 26,a[112:115],90
s_add_u32 s56,s55,20736
R 46,2,19456
v_add_f32 v25,v25,v113
M 26,a[120:123],94
s_add_u32 s75,s75,s63
v_cvt_pk_bf16_f32 v114,v18,v19
v_cvt_pk_bf16_f32 v115,v20,v21
M 30,a[116:119],90
s_sub_u32 s57,s84,s57
R 50,2,20480
v_fma_f32 v107,v18,v18,s87
M 30,a[124:127],94
L 3
v_add_f32 v106,v18,v19
v_fma_f32 v107,v19,v19,v107
M 30,a[132:135],34
R 54,2,21504
v_add_f32 v106,v106,v20
M 30,a[140:143],38
D 17,56,1024
M 26,a[128:131],34
s_add_u32 s56,s55,20992
R 58,2,22528
v_fma_f32 v107,v20,v20,v107
M 26,a[136:139],38
L 3
v_add_f32 v106,v106,v21
v_fma_f32 v107,v21,v21,v107
M 26,a[144:147],42
R 62,2,23552
v_add_f32 v106,v106,v22
M 26,a[152:155],46
v_fma_f32 v107,v22,v22,v107
v_add_f32 v106,v106,v23
M 30,a[148:151],42
R 66,2,24576
v_fma_f32 v107,v23,v23,v107
M 30,a[156:159],46
L 3
v_add_f32 v106,v106,v24
v_fma_f32 v107,v24,v24,v107
M 30,a[164:167],50
R 70,2,25600
v_add_f32 v106,v106,v25
M 30,a[172:175],54
v_fma_f32 v107,v25,v25,v107
v_cvt_pk_bf16_f32 v116,v22,v23
M 26,a[160:163],50
R 74,2,26624
v_cvt_pk_bf16_f32 v117,v24,v25
M 26,a[168:171],54
L 3
v_permlane16_swap_b32 v106,v107
v_add_f32 v106,v106,v107
v_mov_b32 v107,v106
M 26,a[176:179],58
R 78,2,27648
M 26,a[184:187],62
v_permlane32_swap_b32 v106,v107
v_add_f32 v106,v106,v107
M 30,a[180:183],58
R 82,2,28672
ds_write_b32 v12,v106 offset:0
M 30,a[188:191],62
L 4
D 17,56,2048
M 30,a[196:199],66
s_add_u32 s56,s55,21248
s_add_u32 s55,s55,s60
R 86,2,29696
M 30,a[204:207],70
D 17,56,3072
M 26,a[192:195],66
R 90,2,30720
M 26,a[200:203],70
L 4
L 2
buffer_store_dwordx4 v[114:117],v0,s[36:39],s66 offen offset:0 sc0 sc1
M 26,a[208:211],74
s_waitcnt vmcnt(7)
R 94,2,31744
M 26,a[216:219],78
s_barrier
R 118,13,0
ds_read_b32 v7,v6 offset:1024
M 30,a[212:215],74
R 34,1,0
M 30,a[220:223],78
L 5
buffer_store_dword v4,v3,s[36:39],s70 offen offset:0 sc0 sc1
M 30,a[228:231],82
s_mov_b32 s70,s69
s_mov_b32 s69,s68
R 38,1,1024
M 30,a[236:239],86
s_add_u32 s68,s68,s63
M 26,a[224:227],82
R 42,1,2048
M 26,a[232:235],86
L 5
L 4
L 3
M 26,a[240:243],90
R 46,1,3072
v_add_f32 v118,v118,v119
M 26,a[248:251],94
v_cmp_eq_u32 vcc,s53,v7
v_add_f32 v120,v120,v121
M 30,a[244:247],90
s_or_b64 vcc,vcc,s[94:95]
s_cmp_eq_u32 vcc_lo,-1
R 50,1,4096
v_add_f32 v118,v118,v120
v_mul_f32 v118,s86,v118
M 30,a[252:255],94
s_waitcnt vmcnt(6) lgkmcnt(3)
buffer_store_dword v118,v14,s[36:39],s76 offen offset:0 sc0 sc1
s_cbranch_scc1 Lg2_F0_%=
Lg2_L0_%=:
v_mfma_f32_16x16x32_bf16 v[22:25],a[4:7],v[34:37],0
s_add_u32 m0,s59,3072
R 54,1,5120
v_lshlrev_b32 v106,16,v126
M 22,a[12:15],38
s_mov_b32 s66,s65
buffer_load_dword v8,s[36:39],s58 offen offset:0 sc1 lds
v_mfma_f32_16x16x32_bf16 v[18:21],a[0:3],v[34:37],0
s_mov_b32 s76,s75
R 58,1,6144
v_and_b32 v107,0xffff0000,v126
M 18,a[8:11],38
L 3
v_lshlrev_b32 v108,16,v127
v_and_b32 v109,0xffff0000,v127
M 18,a[16:19],42
s_add_u32 s65,s65,s62
R 62,1,7168
v_lshlrev_b32 v110,16,v128
M 18,a[24:27],46
s_add_u32 s75,s75,s63
v_and_b32 v111,0xffff0000,v128
v_lshlrev_b32 v112,16,v129
M 22,a[20:23],42
R 66,1,8192
v_and_b32 v113,0xffff0000,v129
M 22,a[28:31],46
L 3
v_fma_f32 v106,v106,v103,v102
v_fma_f32 v107,v107,v103,v102
M 22,a[36:39],50
R 70,1,9216
v_fma_f32 v108,v108,v103,v102
M 22,a[44:47],54
v_fma_f32 v109,v109,v103,v102
v_fma_f32 v110,v110,v103,v102
M 18,a[32:35],50
R 74,1,10240
v_fma_f32 v111,v111,v103,v102
v_fma_f32 v112,v112,v103,v102
M 18,a[40:43],54
L 3
v_fma_f32 v113,v113,v103,v102
v_add_f32 v26,v26,v106
M 18,a[48:51],58
R 78,1,11264
v_add_f32 v27,v27,v107
M 18,a[56:59],62
v_add_f32 v28,v28,v108
v_add_f32 v29,v29,v109
M 22,a[52:55],58
R 82,1,12288
v_add_f32 v30,v30,v110
M 22,a[60:63],62
L 3
v_add_f32 v31,v31,v111
v_add_f32 v32,v32,v112
M 22,a[68:71],66
R 86,1,13312
v_add_f32 v33,v33,v113
M 22,a[76:79],70
buffer_load_dwordx4 v[122:125],v0,s[36:39],s74 offen offset:0 sc1
M 18,a[64:67],66
s_add_u32 s74,s74,s62
R 90,1,14336
)"
R"(v_cvt_pk_bf16_f32 v114,v26,v27
M 18,a[72:75],70
L 3
buffer_load_dwordx2 v[98:99],v11,s[36:39],s73 offen offset:256 sc1
M 18,a[80:83],74
s_waitcnt vmcnt(2)
s_add_u32 s73,s73,s64
R 94,1,15360
v_cvt_pk_bf16_f32 v115,v28,v29
M 18,a[88:91],78
s_barrier
ds_read_b32 v7,v6 offset:3072
v_cvt_pk_bf16_f32 v116,v30,v31
M 22,a[84:87],74
R 34,1,16384
v_cvt_pk_bf16_f32 v117,v32,v33
M 22,a[92:95],78
L 4
buffer_store_dword v4,v3,s[36:39],s70 offen offset:0 sc0 sc1
M 22,a[100:103],82
s_mov_b32 s70,0x7fff0000
R 38,1,17408
v_add_f32 v106,v26,v27
M 22,a[108:111],86
buffer_store_dwordx4 v[114:117],v0,s[36:39],s67 offen offset:0 sc0 sc1
v_fma_f32 v107,v26,v26,s87
M 18,a[96:99],82
R 42,1,18432
v_fma_f32 v107,v27,v27,v107
M 18,a[104:107],86
L 4
L 3
v_cmp_eq_u32 vcc,s53,v7
M 18,a[112:115],90
s_or_b64 vcc,vcc,s[94:95]
R 46,1,19456
v_add_f32 v106,v106,v28
M 18,a[120:123],94
v_fma_f32 v107,v28,v28,v107
v_add_f32 v106,v106,v29
M 22,a[116:119],90
s_cmp_eq_u32 vcc_lo,-1
R 50,1,20480
v_fma_f32 v107,v29,v29,v107
v_add_f32 v106,v106,v30
s_cbranch_scc0 Lg2_X0_%=
M 22,a[124:127],94
L 3
s_and_b32 s78,s54,127
s_cmp_eq_u32 s78,s85
M 22,a[132:135],34
s_cselect_b64 s[40:41],-1,0
s_add_u32 m0,s57,0
s_add_u32 s56,s55,0
R 54,1,21504
M 22,a[140:143],38
s_add_u32 s58,s58,s61
v_cndmask_b32 v17,v15,v16,s[40:41]
v_fma_f32 v107,v30,v30,v107
M 18,a[128:131],34
s_add_u32 s54,s54,s46
R 58,1,22528
v_add_f32 v106,v106,v31
M 18,a[136:139],38
L 3
D 17,56,0
M 18,a[144:147],42
s_add_u32 s56,s55,256
R 62,1,23552
v_fma_f32 v107,v31,v31,v107
M 18,a[152:155],46
s_and_b32 s79,s54,127
D 17,56,1024
M 22,a[148:151],42
s_add_u32 s56,s55,512
R 66,1,24576
v_add_f32 v106,v106,v32
M 22,a[156:159],46
L 3
v_fma_f32 v107,v32,v32,v107
v_add_f32 v106,v106,v33
M 22,a[164:167],50
s_cmp_eq_u32 s79,0
R 70,1,25600
v_fma_f32 v107,v33,v33,v107
M 22,a[172:175],54
s_cselect_b32 s94,0x3300,0
D 17,56,2048
M 18,a[160:163],50
s_add_u32 s56,s55,768
R 74,1,26624
v_permlane16_swap_b32 v106,v107
M 18,a[168:171],54
L 3
v_add_f32 v106,v106,v107
v_mov_b32 v107,v106
M 18,a[176:179],58
s_cmp_eq_u32 s79,127
s_cselect_b32 s94,0xcc00,s94
R 78,1,27648
M 18,a[184:187],62
v_permlane32_swap_b32 v106,v107
v_add_f32 v106,v106,v107
M 22,a[180:183],58
R 82,1,28672
ds_write_b32 v12,v106 offset:2048
M 22,a[188:191],62
L 4
D 17,56,3072
M 22,a[196:199],66
s_add_u32 m0,s59,0
s_add_u32 s56,s55,20480
R 86,1,29696
M 22,a[204:207],70
buffer_load_dword v8,s[36:39],s58 offen offset:0 sc1 lds
M 18,a[192:195],66
s_add_u32 m0,s57,16384
R 90,1,30720
v_cndmask_b32 v17,v10,v9,s[40:41]
M 18,a[200:203],70
L 4
L 2
D 17,56,0
M 18,a[208:211],74
s_waitcnt vmcnt(1)
s_add_u32 s56,s55,20736
R 94,1,31744
M 18,a[216:219],78
s_barrier
R 118,13,2048
ds_read_b32 v7,v6 offset:0
M 22,a[212:215],74
s_sub_u32 s57,s84,s57
R 34,2,0
M 22,a[220:223],78
L 5
buffer_store_dword v4,v3,s[36:39],s70 offen offset:0 sc0 sc1
M 22,a[228:231],82
s_mov_b32 s70,s69
s_mov_b32 s69,s68
R 38,2,1024
M 22,a[236:239],86
s_add_u32 s68,s68,s63
D 17,56,1024
M 18,a[224:227],82
s_add_u32 s56,s55,20992
R 42,2,2048
M 18,a[232:235],86
L 5
D 17,56,2048
M 18,a[240:243],90
s_add_u32 s56,s55,21248
L 4
L 3
R 46,2,3072
M 18,a[248:251],94
s_add_u32 s55,s55,s60
v_cmp_eq_u32 vcc,s53,v7
v_add_f32 v118,v118,v119
M 22,a[244:247],90
s_or_b64 vcc,vcc,s[94:95]
s_cmp_eq_u32 vcc_lo,-1
R 50,2,4096
v_add_f32 v120,v120,v121
v_add_f32 v118,v118,v120
v_mul_f32 v118,s86,v118
M 22,a[252:255],94
L 3
D 17,56,3072
buffer_store_dword v118,v14,s[36:39],s77 offen offset:0 sc0 sc1
buffer_store_dword v4,v3,s[36:39],s71 offen offset:0 sc0 sc1
buffer_store_dword v4,v3,s[36:39],s71 offen offset:0 sc0 sc1
s_cbranch_scc1 Lg2_F1_%=
s_branch Lg2_L1_%=
Lg2_X0_%=:
M 22,a[124:127],94
L 3
s_add_u32 m0,s59,0
v_fma_f32 v107,v30,v30,v107
M 22,a[132:135],34
R 54,1,21504
v_add_f32 v106,v106,v31
M 22,a[140:143],38
buffer_load_dword v8,s[36:39],s58 offen offset:0 sc1 lds
M 18,a[128:131],34
R 58,1,22528
v_fma_f32 v107,v31,v31,v107
M 18,a[136:139],38
L 3
v_add_f32 v106,v106,v32
v_fma_f32 v107,v32,v32,v107
M 18,a[144:147],42
R 62,1,23552
v_add_f32 v106,v106,v33
M 18,a[152:155],46
v_fma_f32 v107,v33,v33,v107
M 22,a[148:151],42
R 66,1,24576
M 22,a[156:159],46
L 3
v_permlane16_swap_b32 v106,v107
M 22,a[164:167],50
R 70,1,25600
v_add_f32 v106,v106,v107
M 22,a[172:175],54
v_mov_b32 v107,v106
M 18,a[160:163],50
R 74,1,26624
M 18,a[168:171],54
L 3
v_permlane32_swap_b32 v106,v107
M 18,a[176:179],58
R 78,1,27648
v_add_f32 v106,v106,v107
M 18,a[184:187],62
ds_write_b32 v12,v106 offset:2048
M 22,a[180:183],58
R 82,1,28672
M 22,a[188:191],62
L 4
M 22,a[196:199],66
R 86,1,29696
M 22,a[204:207],70
M 18,a[192:195],66
R 90,1,30720
M 18,a[200:203],70
L 4
L 3
M 18,a[208:211],74
s_waitcnt vmcnt(0)
R 94,1,31744
M 18,a[216:219],78
s_barrier
R 118,13,2048
ds_read_b32 v7,v6 offset:0
M 22,a[212:215],74
buffer_store_dword v4,v3,s[36:39],s70 offen offset:0 sc0 sc1
M 22,a[220:223],78
L 4
s_mov_b32 s70,s69
s_mov_b32 s69,s68
M 22,a[228:231],82
s_add_u32 s68,s68,s63
M 22,a[236:239],86
M 18,a[224:227],82
M 18,a[232:235],86
L 2
L 1
L 0
M 18,a[240:243],90
v_add_f32 v118,v118,v119
v_add_f32 v120,v120,v121
M 18,a[248:251],94
v_add_f32 v118,v118,v120
v_mul_f32 v118,s86,v118
M 22,a[244:247],90
v_cmp_eq_u32 vcc,s53,v7
M 22,a[252:255],94
s_nop 0
s_or_b64 vcc,vcc,s[94:95]
buffer_store_dword v118,v14,s[36:39],s77 offen offset:0 sc0 sc1
s_mov_b32 s80,1
s_cmp_eq_u32 vcc_lo,-1
s_cbranch_scc0 Lg2_xn0_%=
s_and_b32 s78,s54,127
s_cmp_eq_u32 s78,s85
s_cselect_b64 s[40:41],-1,0
v_cndmask_b32 v17,v15,v16,s[40:41]
s_add_u32 m0,s57,0
s_add_u32 s56,s55,0
D 17,56,0
v_lshlrev_b32 v106,16,v122
v_and_b32 v107,0xffff0000,v122
v_lshlrev_b32 v108,16,v123
v_and_b32 v109,0xffff0000,v123
v_lshlrev_b32 v110,16,v124
s_add_u32 s56,s55,256
D 17,56,1024
v_and_b32 v111,0xffff0000,v124
v_lshlrev_b32 v112,16,v125
v_and_b32 v113,0xffff0000,v125
v_fma_f32 v106,v106,v99,v98
v_fma_f32 v107,v107,v99,v98
s_add_u32 s56,s55,512
D 17,56,2048
v_fma_f32 v108,v108,v99,v98
v_fma_f32 v109,v109,v99,v98
v_fma_f32 v110,v110,v99,v98
v_fma_f32 v111,v111,v99,v98
v_fma_f32 v112,v112,v99,v98
s_add_u32 s56,s55,768
D 17,56,3072
v_fma_f32 v113,v113,v99,v98
v_add_f32 v18,v18,v106
v_add_f32 v19,v19,v107
v_add_f32 v20,v20,v108
v_add_f32 v21,v21,v109
s_add_u32 s58,s58,s61
v_cndmask_b32 v17,v10,v9,s[40:41]
s_add_u32 m0,s57,16384
s_add_u32 s56,s55,20480
D 17,56,0
v_add_f32 v22,v22,v110
v_add_f32 v23,v23,v111
v_add_f32 v24,v24,v112
v_add_f32 v25,v25,v113
v_cvt_pk_bf16_f32 v114,v18,v19
s_add_u32 s56,s55,20736
D 17,56,1024
v_cvt_pk_bf16_f32 v115,v20,v21
v_cvt_pk_bf16_f32 v116,v22,v23
v_cvt_pk_bf16_f32 v117,v24,v25
s_add_u32 s56,s55,20992
D 17,56,2048
s_add_u32 s56,s55,21248
D 17,56,3072
buffer_store_dwordx4 v[114:117],v0,s[36:39],s66 offen offset:0 sc0 sc1
v_add_f32 v106,v18,v19
v_fma_f32 v107,v18,v18,s87
s_add_u32 s55,s55,s60
s_sub_u32 s57,s84,s57
s_add_u32 s54,s54,s46
v_fma_f32 v107,v19,v19,v107
v_add_f32 v106,v106,v20
v_fma_f32 v107,v20,v20,v107
v_add_f32 v106,v106,v21
v_fma_f32 v107,v21,v21,v107
v_add_f32 v106,v106,v22
v_fma_f32 v107,v22,v22,v107
v_add_f32 v106,v106,v23
v_fma_f32 v107,v23,v23,v107
v_add_f32 v106,v106,v24
v_fma_f32 v107,v24,v24,v107
v_add_f32 v106,v106,v25
v_fma_f32 v107,v25,v25,v107
s_nop 1
v_permlane16_swap_b32 v106,v107
v_add_f32 v106,v106,v107
v_mov_b32 v107,v106
s_nop 1
v_permlane32_swap_b32 v106,v107
v_add_f32 v106,v106,v107
ds_write_b32 v12,v106 offset:0
L 0
s_barrier
R 118,13,0
L 0
v_add_f32 v118,v118,v119
v_add_f32 v120,v120,v121
v_add_f32 v118,v118,v120
v_mul_f32 v118,s86,v118
buffer_store_dword v118,v14,s[36:39],s76 offen offset:0 sc0 sc1
s_mov_b32 s72,s69
s_mov_b32 s69,0x7fff0000
s_mov_b32 s66,0x7fff0000
s_mov_b32 s76,0x7fff0000
s_branch Lg2_gox_%=
Lg2_xn0_%=:
s_cmp_ge_u32 s54,256
s_cbranch_scc1 Lg2_fin0_%=
s_add_u32 m0,s59,4096
s_nop 0
buffer_load_dword v8,s[36:39],s58 offen offset:0 sc1 lds
buffer_store_dword v4,v3,s[36:39],s71 offen offset:0 sc0 sc1
s_add_u32 m0,s59,5120
s_nop 0
buffer_load_dword v8,s[36:39],s58 offen offset:0 sc1 lds
buffer_store_dword v4,v3,s[36:39],s71 offen offset:0 sc0 sc1
s_mov_b32 s83,0
v_lshlrev_b32 v106,16,v122
v_and_b32 v107,0xffff0000,v122
v_lshlrev_b32 v108,16,v123
v_and_b32 v109,0xffff0000,v123
v_lshlrev_b32 v110,16,v124
v_and_b32 v111,0xffff0000,v124
v_lshlrev_b32 v112,16,v125
v_and_b32 v113,0xffff0000,v125
v_fma_f32 v106,v106,v99,v98
v_fma_f32 v107,v107,v99,v98
v_fma_f32 v108,v108,v99,v98
v_fma_f32 v109,v109,v99,v98
v_fma_f32 v110,v110,v99,v98
v_fma_f32 v111,v111,v99,v98
v_fma_f32 v112,v112,v99,v98
v_fma_f32 v113,v113,v99,v98
v_add_f32 v18,v18,v106
v_add_f32 v19,v19,v107
v_add_f32 v20,v20,v108
v_add_f32 v21,v21,v109
v_add_f32 v22,v22,v110
v_add_f32 v23,v23,v111
v_add_f32 v24,v24,v112
v_add_f32 v25,v25,v113
v_cvt_pk_bf16_f32 v114,v18,v19
v_cvt_pk_bf16_f32 v115,v20,v21
v_cvt_pk_bf16_f32 v116,v22,v23
v_cvt_pk_bf16_f32 v117,v24,v25
buffer_store_dwordx4 v[114:117],v0,s[36:39],s66 offen offset:0 sc0 sc1
v_add_f32 v106,v18,v19
v_fma_f32 v107,v18,v18,s87
v_fma_f32 v107,v19,v19,v107
v_add_f32 v106,v106,v20
v_fma_f32 v107,v20,v20,v107
v_add_f32 v106,v106,v21
v_fma_f32 v107,v21,v21,v107
v_add_f32 v106,v106,v22
v_fma_f32 v107,v22,v22,v107
v_add_f32 v106,v106,v23
v_fma_f32 v107,v23,v23,v107
v_add_f32 v106,v106,v24
v_fma_f32 v107,v24,v24,v107
v_add_f32 v106,v106,v25
v_fma_f32 v107,v25,v25,v107
s_nop 1
v_permlane16_swap_b32 v106,v107
v_add_f32 v106,v106,v107
v_mov_b32 v107,v106
s_nop 1
v_permlane32_swap_b32 v106,v107
v_add_f32 v106,v106,v107
ds_write_b32 v12,v106 offset:0
L 0
s_barrier
R 118,13,0
L 0
v_add_f32 v118,v118,v119
v_add_f32 v120,v120,v121
v_add_f32 v118,v118,v120
v_mul_f32 v118,s86,v118
buffer_store_dword v118,v14,s[36:39],s76 offen offset:0 sc0 sc1
s_mov_b32 s72,s69
s_mov_b32 s69,0x7fff0000
s_mov_b32 s66,0x7fff0000
s_mov_b32 s76,0x7fff0000
s_branch Lg2_poll_%=
Lg2_fin0_%=:
v_lshlrev_b32 v106,16,v122
v_and_b32 v107,0xffff0000,v122
v_lshlrev_b32 v108,16,v123
v_and_b32 v109,0xffff0000,v123
v_lshlrev_b32 v110,16,v124
v_and_b32 v111,0xffff0000,v124
v_lshlrev_b32 v112,16,v125
v_and_b32 v113,0xffff0000,v125
v_fma_f32 v106,v106,v99,v98
v_fma_f32 v107,v107,v99,v98
v_fma_f32 v108,v108,v99,v98
v_fma_f32 v109,v109,v99,v98
v_fma_f32 v110,v110,v99,v98
v_fma_f32 v111,v111,v99,v98
v_fma_f32 v112,v112,v99,v98
v_fma_f32 v113,v113,v99,v98
v_add_f32 v18,v18,v106
v_add_f32 v19,v19,v107
v_add_f32 v20,v20,v108
v_add_f32 v21,v21,v109
v_add_f32 v22,v22,v110
v_add_f32 v23,v23,v111
v_add_f32 v24,v24,v112
v_add_f32 v25,v25,v113
v_cvt_pk_bf16_f32 v114,v18,v19
v_cvt_pk_bf16_f32 v115,v20,v21
v_cvt_pk_bf16_f32 v116,v22,v23
v_cvt_pk_bf16_f32 v117,v24,v25
buffer_store_dwordx4 v[114:117],v0,s[36:39],s66 offen offset:0 sc0 sc1
v_add_f32 v106,v18,v19
v_fma_f32 v107,v18,v18,s87
v_fma_f32 v107,v19,v19,v107
v_add_f32 v106,v106,v20
v_fma_f32 v107,v20,v20,v107
v_add_f32 v106,v106,v21
v_fma_f32 v107,v21,v21,v107
v_add_f32 v106,v106,v22
v_fma_f32 v107,v22,v22,v107
v_add_f32 v106,v106,v23
v_fma_f32 v107,v23,v23,v107
v_add_f32 v106,v106,v24
v_fma_f32 v107,v24,v24,v107
v_add_f32 v106,v106,v25
v_fma_f32 v107,v25,v25,v107
s_nop 1
v_permlane16_swap_b32 v106,v107
v_add_f32 v106,v106,v107
v_mov_b32 v107,v106
s_nop 1
v_permlane32_swap_b32 v106,v107
v_add_f32 v106,v106,v107
ds_write_b32 v12,v106 offset:0
L 0
s_barrier
R 118,13,0
L 0
v_add_f32 v118,v118,v119
v_add_f32 v120,v120,v121
v_add_f32 v118,v118,v120
v_mul_f32 v118,s86,v118
buffer_store_dword v118,v14,s[36:39],s76 offen offset:0 sc0 sc1
s_mov_b32 s72,s69
s_mov_b32 s69,0x7fff0000
s_mov_b32 s66,0x7fff0000
)"
R"(s_mov_b32 s76,0x7fff0000
s_branch Lg2_done_%=
Lg2_L1_%=:
v_mfma_f32_16x16x32_bf16 v[30:33],a[4:7],v[34:37],0
s_add_u32 m0,s59,3072
R 54,2,5120
v_lshlrev_b32 v106,16,v122
M 30,a[12:15],38
s_mov_b32 s67,s65
buffer_load_dword v8,s[36:39],s58 offen offset:0 sc1 lds
v_mfma_f32_16x16x32_bf16 v[26:29],a[0:3],v[34:37],0
s_mov_b32 s77,s75
R 58,2,6144
v_and_b32 v107,0xffff0000,v122
M 26,a[8:11],38
L 3
v_lshlrev_b32 v108,16,v123
v_and_b32 v109,0xffff0000,v123
M 26,a[16:19],42
s_add_u32 s65,s65,s62
R 62,2,7168
v_lshlrev_b32 v110,16,v124
M 26,a[24:27],46
s_add_u32 s75,s75,s63
v_and_b32 v111,0xffff0000,v124
v_lshlrev_b32 v112,16,v125
M 30,a[20:23],42
R 66,2,8192
v_and_b32 v113,0xffff0000,v125
M 30,a[28:31],46
L 3
v_fma_f32 v106,v106,v99,v98
v_fma_f32 v107,v107,v99,v98
M 30,a[36:39],50
R 70,2,9216
v_fma_f32 v108,v108,v99,v98
M 30,a[44:47],54
v_fma_f32 v109,v109,v99,v98
v_fma_f32 v110,v110,v99,v98
M 26,a[32:35],50
R 74,2,10240
v_fma_f32 v111,v111,v99,v98
v_fma_f32 v112,v112,v99,v98
M 26,a[40:43],54
L 3
v_fma_f32 v113,v113,v99,v98
v_add_f32 v18,v18,v106
M 26,a[48:51],58
R 78,2,11264
v_add_f32 v19,v19,v107
M 26,a[56:59],62
v_add_f32 v20,v20,v108
v_add_f32 v21,v21,v109
M 30,a[52:55],58
R 82,2,12288
v_add_f32 v22,v22,v110
M 30,a[60:63],62
L 3
v_add_f32 v23,v23,v111
v_add_f32 v24,v24,v112
M 30,a[68:71],66
R 86,2,13312
v_add_f32 v25,v25,v113
M 30,a[76:79],70
buffer_load_dwordx4 v[126:129],v0,s[36:39],s74 offen offset:0 sc1
M 26,a[64:67],66
s_add_u32 s74,s74,s62
R 90,2,14336
v_cvt_pk_bf16_f32 v114,v18,v19
M 26,a[72:75],70
L 3
buffer_load_dwordx2 v[102:103],v11,s[36:39],s73 offen offset:256 sc1
M 26,a[80:83],74
s_waitcnt vmcnt(2)
s_add_u32 s73,s73,s64
R 94,2,15360
v_cvt_pk_bf16_f32 v115,v20,v21
M 26,a[88:91],78
s_barrier
ds_read_b32 v7,v6 offset:3072
v_cvt_pk_bf16_f32 v116,v22,v23
M 30,a[84:87],74
R 34,2,16384
v_cvt_pk_bf16_f32 v117,v24,v25
M 30,a[92:95],78
L 4
buffer_store_dword v4,v3,s[36:39],s70 offen offset:0 sc0 sc1
M 30,a[100:103],82
s_mov_b32 s70,0x7fff0000
R 38,2,17408
v_add_f32 v106,v18,v19
M 30,a[108:111],86
buffer_store_dwordx4 v[114:117],v0,s[36:39],s66 offen offset:0 sc0 sc1
v_fma_f32 v107,v18,v18,s87
M 26,a[96:99],82
R 42,2,18432
v_fma_f32 v107,v19,v19,v107
M 26,a[104:107],86
L 4
L 3
v_cmp_eq_u32 vcc,s53,v7
M 26,a[112:115],90
s_or_b64 vcc,vcc,s[94:95]
R 46,2,19456
v_add_f32 v106,v106,v20
M 26,a[120:123],94
v_fma_f32 v107,v20,v20,v107
v_add_f32 v106,v106,v21
M 30,a[116:119],90
s_cmp_eq_u32 vcc_lo,-1
R 50,2,20480
v_fma_f32 v107,v21,v21,v107
v_add_f32 v106,v106,v22
s_cbranch_scc0 Lg2_X1_%=
M 30,a[124:127],94
L 3
s_and_b32 s78,s54,127
s_cmp_eq_u32 s78,s85
M 30,a[132:135],34
s_cselect_b64 s[40:41],-1,0
s_add_u32 m0,s57,0
s_add_u32 s56,s55,0
R 54,2,21504
M 30,a[140:143],38
s_add_u32 s58,s58,s61
v_cndmask_b32 v17,v15,v16,s[40:41]
v_fma_f32 v107,v22,v22,v107
M 26,a[128:131],34
s_add_u32 s54,s54,s46
R 58,2,22528
v_add_f32 v106,v106,v23
M 26,a[136:139],38
L 3
D 17,56,0
M 26,a[144:147],42
s_add_u32 s56,s55,256
R 62,2,23552
v_fma_f32 v107,v23,v23,v107
M 26,a[152:155],46
s_and_b32 s79,s54,127
D 17,56,1024
M 30,a[148:151],42
s_add_u32 s56,s55,512
R 66,2,24576
v_add_f32 v106,v106,v24
M 30,a[156:159],46
L 3
v_fma_f32 v107,v24,v24,v107
v_add_f32 v106,v106,v25
M 30,a[164:167],50
s_cmp_eq_u32 s79,0
R 70,2,25600
v_fma_f32 v107,v25,v25,v107
M 30,a[172:175],54
s_cselect_b32 s94,0x3300,0
D 17,56,2048
M 26,a[160:163],50
s_add_u32 s56,s55,768
R 74,2,26624
v_permlane16_swap_b32 v106,v107
M 26,a[168:171],54
L 3
v_add_f32 v106,v106,v107
v_mov_b32 v107,v106
M 26,a[176:179],58
s_cmp_eq_u32 s79,127
s_cselect_b32 s94,0xcc00,s94
R 78,2,27648
M 26,a[184:187],62
v_permlane32_swap_b32 v106,v107
v_add_f32 v106,v106,v107
M 30,a[180:183],58
R 82,2,28672
ds_write_b32 v12,v106 offset:0
M 30,a[188:191],62
L 4
D 17,56,3072
M 30,a[196:199],66
s_add_u32 m0,s59,1024
s_add_u32 s56,s55,20480
R 86,2,29696
M 30,a[204:207],70
buffer_load_dword v8,s[36:39],s58 offen offset:0 sc1 lds
M 26,a[192:195],66
s_add_u32 m0,s57,16384
R 90,2,30720
v_cndmask_b32 v17,v10,v9,s[40:41]
M 26,a[200:203],70
L 4
L 2
D 17,56,0
M 26,a[208:211],74
s_waitcnt vmcnt(1)
s_add_u32 s56,s55,20736
R 94,2,31744
M 26,a[216:219],78
s_barrier
R 118,13,0
ds_read_b32 v7,v6 offset:1024
M 30,a[212:215],74
s_sub_u32 s57,s84,s57
R 34,1,0
M 30,a[220:223],78
L 5
buffer_store_dword v4,v3,s[36:39],s70 offen offset:0 sc0 sc1
M 30,a[228:231],82
s_mov_b32 s70,s69
s_mov_b32 s69,s68
R 38,1,1024
M 30,a[236:239],86
s_add_u32 s68,s68,s63
D 17,56,1024
M 26,a[224:227],82
s_add_u32 s56,s55,20992
R 42,1,2048
M 26,a[232:235],86
L 5
D 17,56,2048
M 26,a[240:243],90
s_add_u32 s56,s55,21248
L 4
L 3
R 46,1,3072
M 26,a[248:251],94
s_add_u32 s55,s55,s60
v_cmp_eq_u32 vcc,s53,v7
v_add_f32 v118,v118,v119
M 30,a[244:247],90
s_or_b64 vcc,vcc,s[94:95]
s_cmp_eq_u32 vcc_lo,-1
R 50,1,4096
v_add_f32 v120,v120,v121
v_add_f32 v118,v118,v120
v_mul_f32 v118,s86,v118
M 30,a[252:255],94
L 3
D 17,56,3072
buffer_store_dword v118,v14,s[36:39],s76 offen offset:0 sc0 sc1
buffer_store_dword v4,v3,s[36:39],s71 offen offset:0 sc0 sc1
buffer_store_dword v4,v3,s[36:39],s71 offen offset:0 sc0 sc1
s_cbranch_scc1 Lg2_F0_%=
s_branch Lg2_L0_%=
Lg2_X1_%=:
M 30,a[124:127],94
L 3
s_add_u32 m0,s59,1024
v_fma_f32 v107,v22,v22,v107
M 30,a[132:135],34
R 54,2,21504
v_add_f32 v106,v106,v23
M 30,a[140:143],38
buffer_load_dword v8,s[36:39],s58 offen offset:0 sc1 lds
M 26,a[128:131],34
R 58,2,22528
v_fma_f32 v107,v23,v23,v107
M 26,a[136:139],38
L 3
v_add_f32 v106,v106,v24
v_fma_f32 v107,v24,v24,v107
M 26,a[144:147],42
R 62,2,23552
v_add_f32 v106,v106,v25
M 26,a[152:155],46
v_fma_f32 v107,v25,v25,v107
M 30,a[148:151],42
R 66,2,24576
M 30,a[156:159],46
L 3
v_permlane16_swap_b32 v106,v107
M 30,a[164:167],50
R 70,2,25600
v_add_f32 v106,v106,v107
M 30,a[172:175],54
v_mov_b32 v107,v106
M 26,a[160:163],50
R 74,2,26624
M 26,a[168:171],54
L 3
v_permlane32_swap_b32 v106,v107
M 26,a[176:179],58
R 78,2,27648
v_add_f32 v106,v106,v107
M 26,a[184:187],62
ds_write_b32 v12,v106 offset:0
M 30,a[180:183],58
R 82,2,28672
M 30,a[188:191],62
L 4
M 30,a[196:199],66
R 86,2,29696
M 30,a[204:207],70
M 26,a[192:195],66
R 90,2,30720
M 26,a[200:203],70
L 4
L 3
M 26,a[208:211],74
s_waitcnt vmcnt(0)
R 94,2,31744
M 26,a[216:219],78
s_barrier
R 118,13,0
ds_read_b32 v7,v6 offset:1024
M 30,a[212:215],74
buffer_store_dword v4,v3,s[36:39],s70 offen offset:0 sc0 sc1
M 30,a[220:223],78
L 4
s_mov_b32 s70,s69
s_mov_b32 s69,s68
M 30,a[228:231],82
s_add_u32 s68,s68,s63
M 30,a[236:239],86
M 26,a[224:227],82
M 26,a[232:235],86
L 2
L 1
L 0
M 26,a[240:243],90
v_add_f32 v118,v118,v119
v_add_f32 v120,v120,v121
M 26,a[248:251],94
v_add_f32 v118,v118,v120
v_mul_f32 v118,s86,v118
M 30,a[244:247],90
v_cmp_eq_u32 vcc,s53,v7
M 30,a[252:255],94
s_nop 0
s_or_b64 vcc,vcc,s[94:95]
buffer_store_dword v118,v14,s[36:39],s76 offen offset:0 sc0 sc1
s_mov_b32 s80,0
s_cmp_eq_u32 vcc_lo,-1
s_cbranch_scc0 Lg2_xn1_%=
s_and_b32 s78,s54,127
s_cmp_eq_u32 s78,s85
s_cselect_b64 s[40:41],-1,0
v_cndmask_b32 v17,v15,v16,s[40:41]
s_add_u32 m0,s57,0
s_add_u32 s56,s55,0
D 17,56,0
v_lshlrev_b32 v106,16,v126
v_and_b32 v107,0xffff0000,v126
v_lshlrev_b32 v108,16,v127
v_and_b32 v109,0xffff0000,v127
v_lshlrev_b32 v110,16,v128
s_add_u32 s56,s55,256
D 17,56,1024
v_and_b32 v111,0xffff0000,v128
v_lshlrev_b32 v112,16,v129
v_and_b32 v113,0xffff0000,v129
v_fma_f32 v106,v106,v103,v102
v_fma_f32 v107,v107,v103,v102
s_add_u32 s56,s55,512
D 17,56,2048
v_fma_f32 v108,v108,v103,v102
v_fma_f32 v109,v109,v103,v102
v_fma_f32 v110,v110,v103,v102
v_fma_f32 v111,v111,v103,v102
v_fma_f32 v112,v112,v103,v102
s_add_u32 s56,s55,768
D 17,56,3072
v_fma_f32 v113,v113,v103,v102
v_add_f32 v26,v26,v106
v_add_f32 v27,v27,v107
v_add_f32 v28,v28,v108
v_add_f32 v29,v29,v109
s_add_u32 s58,s58,s61
v_cndmask_b32 v17,v10,v9,s[40:41]
s_add_u32 m0,s57,16384
s_add_u32 s56,s55,20480
D 17,56,0
v_add_f32 v30,v30,v110
v_add_f32 v31,v31,v111
v_add_f32 v32,v32,v112
v_add_f32 v33,v33,v113
v_cvt_pk_bf16_f32 v114,v26,v27
s_add_u32 s56,s55,20736
D 17,56,1024
v_cvt_pk_bf16_f32 v115,v28,v29
v_cvt_pk_bf16_f32 v116,v30,v31
v_cvt_pk_bf16_f32 v117,v32,v33
s_add_u32 s56,s55,20992
D 17,56,2048
s_add_u32 s56,s55,21248
D 17,56,3072
buffer_store_dwordx4 v[114:117],v0,s[36:39],s67 offen offset:0 sc0 sc1
v_add_f32 v106,v26,v27
v_fma_f32 v107,v26,v26,s87
s_add_u32 s55,s55,s60
s_sub_u32 s57,s84,s57
s_add_u32 s54,s54,s46
v_fma_f32 v107,v27,v27,v107
v_add_f32 v106,v106,v28
v_fma_f32 v107,v28,v28,v107
v_add_f32 v106,v106,v29
v_fma_f32 v107,v29,v29,v107
v_add_f32 v106,v106,v30
v_fma_f32 v107,v30,v30,v107
v_add_f32 v106,v106,v31
v_fma_f32 v107,v31,v31,v107
v_add_f32 v106,v106,v32
v_fma_f32 v107,v32,v32,v107
v_add_f32 v106,v106,v33
v_fma_f32 v107,v33,v33,v107
s_nop 1
v_permlane16_swap_b32 v106,v107
v_add_f32 v106,v106,v107
v_mov_b32 v107,v106
s_nop 1
v_permlane32_swap_b32 v106,v107
v_add_f32 v106,v106,v107
ds_write_b32 v12,v106 offset:2048
L 0
s_barrier
R 118,13,2048
L 0
v_add_f32 v118,v118,v119
v_add_f32 v120,v120,v121
v_add_f32 v118,v118,v120
v_mul_f32 v118,s86,v118
buffer_store_dword v118,v14,s[36:39],s77 offen offset:0 sc0 sc1
s_mov_b32 s72,s69
s_mov_b32 s69,0x7fff0000
s_mov_b32 s67,0x7fff0000
s_mov_b32 s77,0x7fff0000
s_branch Lg2_gox_%=
Lg2_xn1_%=:
s_cmp_ge_u32 s54,256
s_cbranch_scc1 Lg2_fin1_%=
s_add_u32 m0,s59,4096
s_nop 0
buffer_load_dword v8,s[36:39],s58 offen offset:0 sc1 lds
buffer_store_dword v4,v3,s[36:39],s71 offen offset:0 sc0 sc1
s_add_u32 m0,s59,5120
s_nop 0
buffer_load_dword v8,s[36:39],s58 offen offset:0 sc1 lds
buffer_store_dword v4,v3,s[36:39],s71 offen offset:0 sc0 sc1
s_mov_b32 s83,0
v_lshlrev_b32 v106,16,v126
v_and_b32 v107,0xffff0000,v126
v_lshlrev_b32 v108,16,v127
v_and_b32 v109,0xffff0000,v127
v_lshlrev_b32 v110,16,v128
v_and_b32 v111,0xffff0000,v128
v_lshlrev_b32 v112,16,v129
v_and_b32 v113,0xffff0000,v129
v_fma_f32 v106,v106,v103,v102
v_fma_f32 v107,v107,v103,v102
v_fma_f32 v108,v108,v103,v102
v_fma_f32 v109,v109,v103,v102
v_fma_f32 v110,v110,v103,v102
v_fma_f32 v111,v111,v103,v102
v_fma_f32 v112,v112,v103,v102
v_fma_f32 v113,v113,v103,v102
v_add_f32 v26,v26,v106
v_add_f32 v27,v27,v107
v_add_f32 v28,v28,v108
v_add_f32 v29,v29,v109
v_add_f32 v30,v30,v110
v_add_f32 v31,v31,v111
v_add_f32 v32,v32,v112
v_add_f32 v33,v33,v113
v_cvt_pk_bf16_f32 v114,v26,v27
v_cvt_pk_bf16_f32 v115,v28,v29
v_cvt_pk_bf16_f32 v116,v30,v31
v_cvt_pk_bf16_f32 v117,v32,v33
buffer_store_dwordx4 v[114:117],v0,s[36:39],s67 offen offset:0 sc0 sc1
v_add_f32 v106,v26,v27
v_fma_f32 v107,v26,v26,s87
v_fma_f32 v107,v27,v27,v107
v_add_f32 v106,v106,v28
v_fma_f32 v107,v28,v28,v107
v_add_f32 v106,v106,v29
v_fma_f32 v107,v29,v29,v107
v_add_f32 v106,v106,v30
v_fma_f32 v107,v30,v30,v107
v_add_f32 v106,v106,v31
v_fma_f32 v107,v31,v31,v107
v_add_f32 v106,v106,v32
v_fma_f32 v107,v32,v32,v107
v_add_f32 v106,v106,v33
v_fma_f32 v107,v33,v33,v107
s_nop 1
v_permlane16_swap_b32 v106,v107
v_add_f32 v106,v106,v107
v_mov_b32 v107,v106
s_nop 1
v_permlane32_swap_b32 v106,v107
v_add_f32 v106,v106,v107
ds_write_b32 v12,v106 offset:2048
L 0
s_barrier
R 118,13,2048
L 0
v_add_f32 v118,v118,v119
v_add_f32 v120,v120,v121
v_add_f32 v118,v118,v120
v_mul_f32 v118,s86,v118
buffer_store_dword v118,v14,s[36:39],s77 offen offset:0 sc0 sc1
s_mov_b32 s72,s69
s_mov_b32 s69,0x7fff0000
)"
R"(s_mov_b32 s67,0x7fff0000
s_mov_b32 s77,0x7fff0000
s_branch Lg2_poll_%=
Lg2_fin1_%=:
v_lshlrev_b32 v106,16,v126
v_and_b32 v107,0xffff0000,v126
v_lshlrev_b32 v108,16,v127
v_and_b32 v109,0xffff0000,v127
v_lshlrev_b32 v110,16,v128
v_and_b32 v111,0xffff0000,v128
v_lshlrev_b32 v112,16,v129
v_and_b32 v113,0xffff0000,v129
v_fma_f32 v106,v106,v103,v102
v_fma_f32 v107,v107,v103,v102
v_fma_f32 v108,v108,v103,v102
v_fma_f32 v109,v109,v103,v102
v_fma_f32 v110,v110,v103,v102
v_fma_f32 v111,v111,v103,v102
v_fma_f32 v112,v112,v103,v102
v_fma_f32 v113,v113,v103,v102
v_add_f32 v26,v26,v106
v_add_f32 v27,v27,v107
v_add_f32 v28,v28,v108
v_add_f32 v29,v29,v109
v_add_f32 v30,v30,v110
v_add_f32 v31,v31,v111
v_add_f32 v32,v32,v112
v_add_f32 v33,v33,v113
v_cvt_pk_bf16_f32 v114,v26,v27
v_cvt_pk_bf16_f32 v115,v28,v29
v_cvt_pk_bf16_f32 v116,v30,v31
v_cvt_pk_bf16_f32 v117,v32,v33
buffer_store_dwordx4 v[114:117],v0,s[36:39],s67 offen offset:0 sc0 sc1
v_add_f32 v106,v26,v27
v_fma_f32 v107,v26,v26,s87
v_fma_f32 v107,v27,v27,v107
v_add_f32 v106,v106,v28
v_fma_f32 v107,v28,v28,v107
v_add_f32 v106,v106,v29
v_fma_f32 v107,v29,v29,v107
v_add_f32 v106,v106,v30
v_fma_f32 v107,v30,v30,v107
v_add_f32 v106,v106,v31
v_fma_f32 v107,v31,v31,v107
v_add_f32 v106,v106,v32
v_fma_f32 v107,v32,v32,v107
v_add_f32 v106,v106,v33
v_fma_f32 v107,v33,v33,v107
s_nop 1
v_permlane16_swap_b32 v106,v107
v_add_f32 v106,v106,v107
v_mov_b32 v107,v106
s_nop 1
v_permlane32_swap_b32 v106,v107
v_add_f32 v106,v106,v107
ds_write_b32 v12,v106 offset:2048
L 0
s_barrier
R 118,13,2048
L 0
v_add_f32 v118,v118,v119
v_add_f32 v120,v120,v121
v_add_f32 v118,v118,v120
v_mul_f32 v118,s86,v118
buffer_store_dword v118,v14,s[36:39],s77 offen offset:0 sc0 sc1
s_mov_b32 s72,s69
s_mov_b32 s69,0x7fff0000
s_mov_b32 s67,0x7fff0000
s_mov_b32 s77,0x7fff0000
s_branch Lg2_done_%=
Lg2_done_%=:
s_waitcnt vmcnt(2)
s_barrier
buffer_store_dword v4,v3,s[36:39],s70 offen offset:0 sc0 sc1
s_waitcnt vmcnt(1)
s_barrier
buffer_store_dword v4,v3,s[36:39],s72 offen offset:0 sc0 sc1
s_waitcnt vmcnt(0) lgkmcnt(0)
s_barrier
Lg2_end_%=:
s_mov_b32 %0,s53
Lcode_g12_e:
.purgem M
.purgem R
.purgem T
.purgem D
.purgem L
)"
  : "=&s"(epOut) : "s"(ws), "s"(sWave), "s"(sG0), "s"(sStep), "s"(sOutOff), "s"(sFoutOff), "s"(sAuxOff), "s"(sWw), "s"(sBuf), "s"(sRk), "v"(vL16), "v"(vFlag), "v"(vSnapOff), "v"(vSnapRd), "v"(vWd0), "v"(vWd1), "v"(vWr0), "v"(vWr1), "v"(vSp), "v"(vOutS), "v"(vAux0), "v"(vAux1), "v"(vAux2), "v"(vAux3), "v"(vAn), "v"(vAe), "s"(w12) : "v0", "v1", "v2", "v3", "v4", "v5", "v6", "v7", "v8", "v9", "v10", "v11", "v12", "v13", "v14", "v15", "v16", "v17", "v18", "v19", "v20", "v21", "v22", "v23", "v24", "v25", "v26", "v27", "v28", "v29", "v30", "v31", "v32", "v33", "v34", "v35", "v36", "v37", "v38", "v39", "v40", "v41", "v42", "v43", "v44", "v45", "v46", "v47", "v48", "v49", "v50", "v51", "v52", "v53", "v54", "v55", "v56", "v57", "v58", "v59", "v60", "v61", "v62", "v63", "v64", "v65", "v66", "v67", "v68", "v69", "v70", "v71", "v72", "v73", "v74", "v75", "v76", "v77", "v78", "v79", "v80", "v81", "v82", "v83", "v84", "v85", "v86", "v87", "v88", "v89", "v90", "v91", "v92", "v93", "v94", "v95", "v96", "v97", "v98", "v99", "v100", "v101", "v102", "v103", "v104", "v105", "v106", "v107", "v108", "v109", "v110", "v111", "v112", "v113", "v114", "v115", "v116", "v117", "v118", "v119", "v120", "v121", "v122", "v123", "v124", "v125", "v126", "v127", "v128", "v129", "v130", "v131", "v132", "v133", "v134", "v135", "v136", "v137", "v138", "v139", "v140", "v141", "v142", "v143", "v144", "v145", "v146", "v147", "v148", "v149", "v150", "v151", "v152", "v153", "v154", "v155", "v156", "v157", "v158", "v159", "v160", "v161", "v162", "v163", "v164", "v165", "v166", "v167", "v168", "v169", "v170", "v171", "v172", "v173", "v174", "v175", "v176", "v177", "v178", "v179", "v180", "v181", "v182", "v183", "v184", "v185", "v186", "v187", "v188", "v189", "v190", "v191", "v192", "v193", "v194", "v195", "v196", "v197", "v198", "v199", "v200", "v201", "v202", "v203", "v204", "v205", "v206", "v207", "v208", "v209", "v210", "v211", "v212", "v213", "v214", "v215", "v216", "v217", "v218", "v219", "v220", "v221", "v222", "v223", "v224", "v225", "v226", "v227", "v228", "v229", "v230", "v231", "a0", "a1", "a2", "a3", "a4", "a5", "a6", "a7", "a8", "a9", "a10", "a11", "a12", "a13", "a14", "a15", "a16", "a17", "a18", "a19", "a20", "a21", "a22", "a23", "a24", "a25", "a26", "a27", "a28", "a29", "a30", "a31", "a32", "a33", "a34", "a35", "a36", "a37", "a38", "a39", "a40", "a41", "a42", "a43", "a44", "a45", "a46", "a47", "a48", "a49", "a50", "a51", "a52", "a53", "a54", "a55", "a56", "a57", "a58", "a59", "a60", "a61", "a62", "a63", "a64", "a65", "a66", "a67", "a68", "a69", "a70", "a71", "a72", "a73", "a74", "a75", "a76", "a77", "a78", "a79", "a80", "a81", "a82", "a83", "a84", "a85", "a86", "a87", "a88", "a89", "a90", "a91", "a92", "a93", "a94", "a95", "a96", "a97", "a98", "a99", "a100", "a101", "a102", "a103", "a104", "a105", "a106", "a107", "a108", "a109", "a110", "a111", "a112", "a113", "a114", "a115", "a116", "a117", "a118", "a119", "a120", "a121", "a122", "a123", "a124", "a125", "a126", "a127", "a128", "a129", "a130", "a131", "a132", "a133", "a134", "a135", "a136", "a137", "a138", "a139", "a140", "a141", "a142", "a143", "a144", "a145", "a146", "a147", "a148", "a149", "a150", "a151", "a152", "a153", "a154", "a155", "a156", "a157", "a158", "a159", "a160", "a161", "a162", "a163", "a164", "a165", "a166", "a167", "a168", "a169", "a170", "a171", "a172", "a173", "a174", "a175", "a176", "a177", "a178", "a179", "a180", "a181", "a182", "a183", "a184", "a185", "a186", "a187", "a188", "a189", "a190", "a191", "a192", "a193", "a194", "a195", "a196", "a197", "a198", "a199", "a200", "a201", "a202", "a203", "a204", "a205", "a206", "a207", "a208", "a209", "a210", "a211", "a212", "a213", "a214", "a215", "a216", "a217", "a218", "a219", "a220", "a221", "a222", "a223", "a224", "a225", "a226", "a227", "a228", "a229", "a230", "a231", "a232", "a233", "a234", "a235", "a236", "a237", "a238", "a239", "a240", "a241", "a242", "a243", "a244", "a245", "a246", "a247", "a248", "a249", "a250", "a251", "a252", "a253", "a254", "a255", "s36", "s37", "s38", "s39", "s40", "s41", "s42", "s43", "s44", "s45", "s46", "s47", "s48", "s49", "s50", "s51", "s52", "s53", "s54", "s55", "s56", "s57", "s58", "s59", "s60", "s61", "s62", "s63", "s64", "s65", "s66", "s67", "s68", "s69", "s70", "s71", "s72", "s73", "s74", "s75", "s76", "s77", "s78", "s79", "s80", "s81", "s82", "s83", "s84", "s85", "s86", "s87", "s88", "s89", "s90", "s91", "s92", "s93", "s94", "s95", "s96", "s97", "vcc", "scc", "memory");
    ep = epOut;
    if (kind == 1 && 5 + (rep >> 1) < NR4) { kind = 4; kh = rep & 1; rep = 5 + (rep >> 1); }
  }
  if (kind == 4) {
    const unsigned sStep = rep < 5 ? 5 : NR4;
    COMMON(10, (kh * 9 + fj) * 128, 2560, 143360, 2048, 448, 0);
    const int cs = slice;
    const unsigned sOutOff = (kh ? OFF_P1 : OFF_P0) + (cs * 4 + wave) * 1024, sFoutOff = OFF_FP + (kh * 8 + cs) * 128;
    const unsigned sAuxOff = (kh ? OFF_XP : OFF_X1) + (cs * 4 + wave) * 1024, sWw = (kh * 1792 + 16 * wave) * 2048 + cs * 256, sBuf = kh * 57344;
    const int zu = 113 + (rep + 2) % 5;
    const unsigned vSp = lg * 2048 + l15 * 4, vSn = rep < 5 ? (zu * 16 + wave * 4 + lg) * 2048 + (cs * 2 + kh) * 128 + l15 * 8 : OOB;
    (void)vWr2;
    const unsigned sExp = __builtin_amdgcn_readfirstlane(ep);
    asm volatile(
R"(.macro M d,w,x
v_mfma_f32_16x16x32_bf16 v[\d:\d+3], \w, v[\x:\x+3], v[\d:\d+3]
.endm
.macro R d,a,o
ds_read_b128 v[\d:\d+3], v\a offset:\o
.endm
.macro T d,a,o
ds_read_b64_tr_b16 \d, v\a offset:\o
.endm
.macro D a,s,o
buffer_load_dwordx4 v\a, s[36:39], s\s offen offset:\o sc1 lds
.endm
.macro L c
s_waitcnt lgkmcnt(\c)
.endm
Lcode_g4_b:
s_mov_b64 s[36:37],%0
s_mov_b32 s38,111243392
s_mov_b32 s39,0x00020000
s_mov_b32 s44,%1
s_mov_b32 s45,%2
s_mov_b32 s46,%3
s_mov_b32 s47,%4
s_mov_b32 s48,%5
s_mov_b32 s49,%6
s_mov_b32 s50,%7
s_mov_b32 s51,%8
s_mov_b32 s93,%9
s_mov_b32 s53,%10
v_mov_b32 v0,%11
v_mov_b32 v3,%12
v_mov_b32 v5,%13
v_mov_b32 v6,%14
v_mov_b32 v10,%15
v_mov_b32 v11,%16
v_mov_b32 v12,%17
v_mov_b32 v13,%18
v_mov_b32 v9,%19
v_mov_b32 v7,%20
s_mov_b64 s[40:41],%21
s_mov_b32 s42,7340032
s_mov_b32 s43,0x00020000
s_lshl_b32 s57,s44,11
s_add_u32 s55,s50,0
s_add_u32 m0,s57,0
s_nop 0
buffer_load_dwordx4 v10,s[40:43],s55 offen offset:0  lds
s_add_u32 m0,s57,1024
s_nop 0
buffer_load_dwordx4 v11,s[40:43],s55 offen offset:0  lds
s_add_u32 m0,s57,8064
s_nop 0
buffer_load_dwordx4 v10,s[40:43],s55 offen offset:128  lds
s_add_u32 m0,s57,9088
s_nop 0
buffer_load_dwordx4 v11,s[40:43],s55 offen offset:128  lds
s_add_u32 s55,s50,131072
s_add_u32 m0,s57,16384
s_nop 0
buffer_load_dwordx4 v10,s[40:43],s55 offen offset:0  lds
s_add_u32 m0,s57,17408
s_nop 0
buffer_load_dwordx4 v11,s[40:43],s55 offen offset:0  lds
s_add_u32 m0,s57,24448
s_nop 0
buffer_load_dwordx4 v10,s[40:43],s55 offen offset:128  lds
s_add_u32 m0,s57,25472
s_nop 0
buffer_load_dwordx4 v11,s[40:43],s55 offen offset:128  lds
s_add_u32 s55,s50,262144
s_add_u32 m0,s57,32768
s_nop 0
buffer_load_dwordx4 v10,s[40:43],s55 offen offset:0  lds
s_add_u32 m0,s57,33792
s_nop 0
buffer_load_dwordx4 v11,s[40:43],s55 offen offset:0  lds
s_add_u32 m0,s57,40832
s_nop 0
buffer_load_dwordx4 v10,s[40:43],s55 offen offset:128  lds
s_add_u32 m0,s57,41856
s_nop 0
buffer_load_dwordx4 v11,s[40:43],s55 offen offset:128  lds
s_add_u32 s55,s50,393216
s_add_u32 m0,s57,49152
s_nop 0
buffer_load_dwordx4 v10,s[40:43],s55 offen offset:0  lds
s_add_u32 m0,s57,50176
s_nop 0
buffer_load_dwordx4 v11,s[40:43],s55 offen offset:0  lds
s_add_u32 m0,s57,57216
s_nop 0
buffer_load_dwordx4 v10,s[40:43],s55 offen offset:128  lds
s_add_u32 m0,s57,58240
s_nop 0
buffer_load_dwordx4 v11,s[40:43],s55 offen offset:128  lds
s_add_u32 s55,s50,524288
s_add_u32 m0,s57,65536
s_nop 0
buffer_load_dwordx4 v10,s[40:43],s55 offen offset:0  lds
s_add_u32 m0,s57,66560
s_nop 0
buffer_load_dwordx4 v11,s[40:43],s55 offen offset:0  lds
s_add_u32 m0,s57,73600
s_nop 0
buffer_load_dwordx4 v10,s[40:43],s55 offen offset:128  lds
s_add_u32 m0,s57,74624
s_nop 0
buffer_load_dwordx4 v11,s[40:43],s55 offen offset:128  lds
s_add_u32 s55,s50,655360
s_add_u32 m0,s57,81920
s_nop 0
buffer_load_dwordx4 v10,s[40:43],s55 offen offset:0  lds
s_add_u32 m0,s57,82944
s_nop 0
buffer_load_dwordx4 v11,s[40:43],s55 offen offset:0  lds
s_add_u32 m0,s57,89984
s_nop 0
buffer_load_dwordx4 v10,s[40:43],s55 offen offset:128  lds
s_add_u32 m0,s57,91008
s_nop 0
buffer_load_dwordx4 v11,s[40:43],s55 offen offset:128  lds
s_waitcnt vmcnt(20) lgkmcnt(0)
s_barrier
s_add_u32 s55,s50,786432
s_add_u32 m0,s57,98304
s_nop 0
buffer_load_dwordx4 v10,s[40:43],s55 offen offset:0  lds
s_add_u32 m0,s57,99328
s_nop 0
buffer_load_dwordx4 v11,s[40:43],s55 offen offset:0  lds
s_add_u32 m0,s57,106368
s_nop 0
buffer_load_dwordx4 v10,s[40:43],s55 offen offset:128  lds
s_add_u32 m0,s57,107392
s_nop 0
buffer_load_dwordx4 v11,s[40:43],s55 offen offset:128  lds
v_add_u32 v18,0,v12
v_add_u32 v19,0,v13
T a[0:1],18,0
T a[2:3],18,512
T a[4:5],19,0
T a[6:7],19,512
T a[8:9],18,4096
T a[10:11],18,4608
T a[12:13],19,4096
T a[14:15],19,4608
s_waitcnt vmcnt(20) lgkmcnt(0)
s_barrier
s_add_u32 s55,s50,917504
s_add_u32 m0,s57,0
s_nop 0
buffer_load_dwordx4 v10,s[40:43],s55 offen offset:0  lds
s_add_u32 m0,s57,1024
s_nop 0
buffer_load_dwordx4 v11,s[40:43],s55 offen offset:0  lds
s_add_u32 m0,s57,8064
s_nop 0
buffer_load_dwordx4 v10,s[40:43],s55 offen offset:128  lds
s_add_u32 m0,s57,9088
s_nop 0
buffer_load_dwordx4 v11,s[40:43],s55 offen offset:128  lds
v_add_u32 v18,16384,v12
v_add_u32 v19,16384,v13
T a[16:17],18,0
T a[18:19],18,512
T a[20:21],19,0
T a[22:23],19,512
T a[24:25],18,4096
T a[26:27],18,4608
T a[28:29],19,4096
T a[30:31],19,4608
s_waitcnt vmcnt(20) lgkmcnt(0)
s_barrier
s_add_u32 s55,s50,1048576
s_add_u32 m0,s57,16384
s_nop 0
buffer_load_dwordx4 v10,s[40:43],s55 offen offset:0  lds
s_add_u32 m0,s57,17408
s_nop 0
buffer_load_dwordx4 v11,s[40:43],s55 offen offset:0  lds
s_add_u32 m0,s57,24448
s_nop 0
buffer_load_dwordx4 v10,s[40:43],s55 offen offset:128  lds
s_add_u32 m0,s57,25472
s_nop 0
buffer_load_dwordx4 v11,s[40:43],s55 offen offset:128  lds
v_add_u32 v18,32768,v12
v_add_u32 v19,32768,v13
T a[32:33],18,0
T a[34:35],18,512
T a[36:37],19,0
T a[38:39],19,512
T a[40:41],18,4096
T a[42:43],18,4608
T a[44:45],19,4096
T a[46:47],19,4608
s_waitcnt vmcnt(20) lgkmcnt(0)
s_barrier
s_add_u32 s55,s50,1179648
s_add_u32 m0,s57,32768
s_nop 0
buffer_load_dwordx4 v10,s[40:43],s55 offen offset:0  lds
s_add_u32 m0,s57,33792
s_nop 0
buffer_load_dwordx4 v11,s[40:43],s55 offen offset:0  lds
s_add_u32 m0,s57,40832
s_nop 0
buffer_load_dwordx4 v10,s[40:43],s55 offen offset:128  lds
s_add_u32 m0,s57,41856
s_nop 0
buffer_load_dwordx4 v11,s[40:43],s55 offen offset:128  lds
v_add_u32 v18,49152,v12
v_add_u32 v19,49152,v13
T a[48:49],18,0
T a[50:51],18,512
T a[52:53],19,0
T a[54:55],19,512
T a[56:57],18,4096
T a[58:59],18,4608
T a[60:61],19,4096
T a[62:63],19,4608
s_waitcnt vmcnt(20) lgkmcnt(0)
s_barrier
s_add_u32 s55,s50,1310720
s_add_u32 m0,s57,49152
s_nop 0
buffer_load_dwordx4 v10,s[40:43],s55 offen offset:0  lds
s_add_u32 m0,s57,50176
s_nop 0
buffer_load_dwordx4 v11,s[40:43],s55 offen offset:0  lds
s_add_u32 m0,s57,57216
s_nop 0
buffer_load_dwordx4 v10,s[40:43],s55 offen offset:128  lds
s_add_u32 m0,s57,58240
s_nop 0
buffer_load_dwordx4 v11,s[40:43],s55 offen offset:128  lds
v_add_u32 v18,65536,v12
v_add_u32 v19,65536,v13
T a[64:65],18,0
T a[66:67],18,512
T a[68:69],19,0
T a[70:71],19,512
T a[72:73],18,4096
T a[74:75],18,4608
T a[76:77],19,4096
T a[78:79],19,4608
s_waitcnt vmcnt(20) lgkmcnt(0)
s_barrier
s_add_u32 s55,s50,1441792
s_add_u32 m0,s57,65536
s_nop 0
buffer_load_dwordx4 v10,s[40:43],s55 offen offset:0  lds
s_add_u32 m0,s57,66560
s_nop 0
buffer_load_dwordx4 v11,s[40:43],s55 offen offset:0  lds
s_add_u32 m0,s57,73600
s_nop 0
buffer_load_dwordx4 v10,s[40:43],s55 offen offset:128  lds
s_add_u32 m0,s57,74624
s_nop 0
buffer_load_dwordx4 v11,s[40:43],s55 offen offset:128  lds
v_add_u32 v18,81920,v12
v_add_u32 v19,81920,v13
T a[80:81],18,0
T a[82:83],18,512
T a[84:85],19,0
T a[86:87],19,512
T a[88:89],18,4096
T a[90:91],18,4608
T a[92:93],19,4096
T a[94:95],19,4608
s_waitcnt vmcnt(20) lgkmcnt(0)
s_barrier
s_add_u32 s55,s50,1572864
s_add_u32 m0,s57,81920
s_nop 0
buffer_load_dwordx4 v10,s[40:43],s55 offen offset:0  lds
s_add_u32 m0,s57,82944
s_nop 0
buffer_load_dwordx4 v11,s[40:43],s55 offen offset:0  lds
s_add_u32 m0,s57,89984
s_nop 0
buffer_load_dwordx4 v10,s[40:43],s55 offen offset:128  lds
s_add_u32 m0,s57,91008
s_nop 0
buffer_load_dwordx4 v11,s[40:43],s55 offen offset:128  lds
v_add_u32 v18,98304,v12
v_add_u32 v19,98304,v13
T a[96:97],18,0
T a[98:99],18,512
T a[100:101],19,0
T a[102:103],19,512
T a[104:105],18,4096
T a[106:107],18,4608
T a[108:109],19,4096
T a[110:111],19,4608
s_waitcnt vmcnt(20) lgkmcnt(0)
s_barrier
s_add_u32 s55,s50,1703936
s_add_u32 m0,s57,98304
s_nop 0
buffer_load_dwordx4 v10,s[40:43],s55 offen offset:0  lds
s_add_u32 m0,s57,99328
s_nop 0
buffer_load_dwordx4 v11,s[40:43],s55 offen offset:0  lds
s_add_u32 m0,s57,106368
s_nop 0
buffer_load_dwordx4 v10,s[40:43],s55 offen offset:128  lds
s_add_u32 m0,s57,107392
s_nop 0
buffer_load_dwordx4 v11,s[40:43],s55 offen offset:128  lds
v_add_u32 v18,0,v12
v_add_u32 v19,0,v13
T a[112:113],18,0
T a[114:115],18,512
T a[116:117],19,0
T a[118:119],19,512
T a[120:121],18,4096
T a[122:123],18,4608
T a[124:125],19,4096
T a[126:127],19,4608
s_waitcnt vmcnt(20) lgkmcnt(0)
s_barrier
s_add_u32 s55,s50,1835008
s_add_u32 m0,s57,0
s_nop 0
buffer_load_dwordx4 v10,s[40:43],s55 offen offset:0  lds
s_add_u32 m0,s57,1024
s_nop 0
buffer_load_dwordx4 v11,s[40:43],s55 offen offset:0  lds
s_add_u32 m0,s57,8064
s_nop 0
buffer_load_dwordx4 v10,s[40:43],s55 offen offset:128  lds
s_add_u32 m0,s57,9088
s_nop 0
buffer_load_dwordx4 v11,s[40:43],s55 offen offset:128  lds
v_add_u32 v18,16384,v12
v_add_u32 v19,16384,v13
T a[128:129],18,0
T a[130:131],18,512
T a[132:133],19,0
T a[134:135],19,512
T a[136:137],18,4096
T a[138:139],18,4608
T a[140:141],19,4096
T a[142:143],19,4608
s_waitcnt vmcnt(20) lgkmcnt(0)
s_barrier
s_add_u32 s55,s50,1966080
s_add_u32 m0,s57,16384
s_nop 0
buffer_load_dwordx4 v10,s[40:43],s55 offen offset:0  lds
s_add_u32 m0,s57,17408
s_nop 0
buffer_load_dwordx4 v11,s[40:43],s55 offen offset:0  lds
s_add_u32 m0,s57,24448
s_nop 0
buffer_load_dwordx4 v10,s[40:43],s55 offen offset:128  lds
s_add_u32 m0,s57,25472
s_nop 0
buffer_load_dwordx4 v11,s[40:43],s55 offen offset:128  lds
v_add_u32 v18,32768,v12
v_add_u32 v19,32768,v13
T a[144:145],18,0
T a[146:147],18,512
T a[148:149],19,0
T a[150:151],19,512
T a[152:153],18,4096
T a[154:155],18,4608
T a[156:157],19,4096
T a[158:159],19,4608
s_waitcnt vmcnt(20) lgkmcnt(0)
s_barrier
s_add_u32 s55,s50,2097152
s_add_u32 m0,s57,32768
s_nop 0
buffer_load_dwordx4 v10,s[40:43],s55 offen offset:0  lds
s_add_u32 m0,s57,33792
s_nop 0
buffer_load_dwordx4 v11,s[40:43],s55 offen offset:0  lds
s_add_u32 m0,s57,40832
s_nop 0
buffer_load_dwordx4 v10,s[40:43],s55 offen offset:128  lds
s_add_u32 m0,s57,41856
s_nop 0
buffer_load_dwordx4 v11,s[40:43],s55 offen offset:128  lds
v_add_u32 v18,49152,v12
v_add_u32 v19,49152,v13
T a[160:161],18,0
T a[162:163],18,512
T a[164:165],19,0
T a[166:167],19,512
T a[168:169],18,4096
T a[170:171],18,4608
T a[172:173],19,4096
T a[174:175],19,4608
s_waitcnt vmcnt(20) lgkmcnt(0)
s_barrier
s_add_u32 s55,s50,2228224
s_add_u32 m0,s57,49152
s_nop 0
buffer_load_dwordx4 v10,s[40:43],s55 offen offset:0  lds
s_add_u32 m0,s57,50176
s_nop 0
buffer_load_dwordx4 v11,s[40:43],s55 offen offset:0  lds
s_add_u32 m0,s57,57216
s_nop 0
buffer_load_dwordx4 v10,s[40:43],s55 offen offset:128  lds
s_add_u32 m0,s57,58240
s_nop 0
buffer_load_dwordx4 v11,s[40:43],s55 offen offset:128  lds
v_add_u32 v18,65536,v12
v_add_u32 v19,65536,v13
T a[176:177],18,0
T a[178:179],18,512
T a[180:181],19,0
T a[182:183],19,512
T a[184:185],18,4096
T a[186:187],18,4608
T a[188:189],19,4096
T a[190:191],19,4608
s_waitcnt vmcnt(20) lgkmcnt(0)
s_barrier
s_add_u32 s55,s50,2359296
s_add_u32 m0,s57,65536
s_nop 0
buffer_load_dwordx4 v10,s[40:43],s55 offen offset:0  lds
s_add_u32 m0,s57,66560
s_nop 0
buffer_load_dwordx4 v11,s[40:43],s55 offen offset:0  lds
s_add_u32 m0,s57,73600
s_nop 0
buffer_load_dwordx4 v10,s[40:43],s55 offen offset:128  lds
s_add_u32 m0,s57,74624
s_nop 0
buffer_load_dwordx4 v11,s[40:43],s55 offen offset:128  lds
v_add_u32 v18,81920,v12
v_add_u32 v19,81920,v13
T a[192:193],18,0
T a[194:195],18,512
T a[196:197],19,0
T a[198:199],19,512
T a[200:201],18,4096
T a[202:203],18,4608
T a[204:205],19,4096
T a[206:207],19,4608
s_waitcnt vmcnt(20) lgkmcnt(0)
s_barrier
s_add_u32 s55,s50,2490368
s_add_u32 m0,s57,81920
s_nop 0
buffer_load_dwordx4 v10,s[40:43],s55 offen offset:0  lds
s_add_u32 m0,s57,82944
s_nop 0
buffer_load_dwordx4 v11,s[40:43],s55 offen offset:0  lds
s_add_u32 m0,s57,89984
s_nop 0
buffer_load_dwordx4 v10,s[40:43],s55 offen offset:128  lds
s_add_u32 m0,s57,91008
s_nop 0
buffer_load_dwordx4 v11,s[40:43],s55 offen offset:128  lds
v_add_u32 v18,98304,v12
v_add_u32 v19,98304,v13
T a[208:209],18,0
T a[210:211],18,512
T a[212:213],19,0
T a[214:215],19,512
T a[216:217],18,4096
T a[218:219],18,4608
T a[220:221],19,4096
T a[222:223],19,4608
s_waitcnt vmcnt(20) lgkmcnt(0)
s_barrier
s_add_u32 s55,s50,2621440
s_add_u32 m0,s57,98304
s_nop 0
buffer_load_dwordx4 v10,s[40:43],s55 offen offset:0  lds
s_add_u32 m0,s57,99328
s_nop 0
buffer_load_dwordx4 v11,s[40:43],s55 offen offset:0  lds
s_add_u32 m0,s57,106368
s_nop 0
buffer_load_dwordx4 v10,s[40:43],s55 offen offset:128  lds
s_add_u32 m0,s57,107392
s_nop 0
buffer_load_dwordx4 v11,s[40:43],s55 offen offset:128  lds
v_add_u32 v18,0,v12
v_add_u32 v19,0,v13
T a[224:225],18,0
T a[226:227],18,512
T a[228:229],19,0
T a[230:231],19,512
T a[232:233],18,4096
T a[234:235],18,4608
T a[236:237],19,4096
T a[238:239],19,4608
s_waitcnt vmcnt(20) lgkmcnt(0)
s_barrier
s_add_u32 s55,s50,2752512
s_add_u32 m0,s57,0
)"
R"(s_nop 0
buffer_load_dwordx4 v10,s[40:43],s55 offen offset:0  lds
s_add_u32 m0,s57,1024
s_nop 0
buffer_load_dwordx4 v11,s[40:43],s55 offen offset:0  lds
s_add_u32 m0,s57,8064
s_nop 0
buffer_load_dwordx4 v10,s[40:43],s55 offen offset:128  lds
s_add_u32 m0,s57,9088
s_nop 0
buffer_load_dwordx4 v11,s[40:43],s55 offen offset:128  lds
v_add_u32 v18,16384,v12
v_add_u32 v19,16384,v13
T a[240:241],18,0
T a[242:243],18,512
T a[244:245],19,0
T a[246:247],19,512
T a[248:249],18,4096
T a[250:251],18,4608
T a[252:253],19,4096
T a[254:255],19,4608
s_waitcnt vmcnt(20) lgkmcnt(0)
s_barrier
s_add_u32 s55,s50,2883584
s_add_u32 m0,s57,16384
s_nop 0
buffer_load_dwordx4 v10,s[40:43],s55 offen offset:0  lds
s_add_u32 m0,s57,17408
s_nop 0
buffer_load_dwordx4 v11,s[40:43],s55 offen offset:0  lds
s_add_u32 m0,s57,24448
s_nop 0
buffer_load_dwordx4 v10,s[40:43],s55 offen offset:128  lds
s_add_u32 m0,s57,25472
s_nop 0
buffer_load_dwordx4 v11,s[40:43],s55 offen offset:128  lds
v_add_u32 v18,32768,v12
v_add_u32 v19,32768,v13
T v[64:65],18,0
T v[66:67],18,512
T v[68:69],19,0
T v[70:71],19,512
T v[72:73],18,4096
T v[74:75],18,4608
T v[76:77],19,4096
T v[78:79],19,4608
s_waitcnt vmcnt(20) lgkmcnt(0)
s_barrier
s_add_u32 s55,s50,3014656
s_add_u32 m0,s57,32768
s_nop 0
buffer_load_dwordx4 v10,s[40:43],s55 offen offset:0  lds
s_add_u32 m0,s57,33792
s_nop 0
buffer_load_dwordx4 v11,s[40:43],s55 offen offset:0  lds
s_add_u32 m0,s57,40832
s_nop 0
buffer_load_dwordx4 v10,s[40:43],s55 offen offset:128  lds
s_add_u32 m0,s57,41856
s_nop 0
buffer_load_dwordx4 v11,s[40:43],s55 offen offset:128  lds
v_add_u32 v18,49152,v12
v_add_u32 v19,49152,v13
T v[80:81],18,0
T v[82:83],18,512
T v[84:85],19,0
T v[86:87],19,512
T v[88:89],18,4096
T v[90:91],18,4608
T v[92:93],19,4096
T v[94:95],19,4608
s_waitcnt vmcnt(20) lgkmcnt(0)
s_barrier
s_add_u32 s55,s50,3145728
s_add_u32 m0,s57,49152
s_nop 0
buffer_load_dwordx4 v10,s[40:43],s55 offen offset:0  lds
s_add_u32 m0,s57,50176
s_nop 0
buffer_load_dwordx4 v11,s[40:43],s55 offen offset:0  lds
s_add_u32 m0,s57,57216
s_nop 0
buffer_load_dwordx4 v10,s[40:43],s55 offen offset:128  lds
s_add_u32 m0,s57,58240
s_nop 0
buffer_load_dwordx4 v11,s[40:43],s55 offen offset:128  lds
v_add_u32 v18,65536,v12
v_add_u32 v19,65536,v13
T v[96:97],18,0
T v[98:99],18,512
T v[100:101],19,0
T v[102:103],19,512
T v[104:105],18,4096
T v[106:107],18,4608
T v[108:109],19,4096
T v[110:111],19,4608
s_waitcnt vmcnt(20) lgkmcnt(0)
s_barrier
s_add_u32 s55,s50,3276800
s_add_u32 m0,s57,65536
s_nop 0
buffer_load_dwordx4 v10,s[40:43],s55 offen offset:0  lds
s_add_u32 m0,s57,66560
s_nop 0
buffer_load_dwordx4 v11,s[40:43],s55 offen offset:0  lds
s_add_u32 m0,s57,73600
s_nop 0
buffer_load_dwordx4 v10,s[40:43],s55 offen offset:128  lds
s_add_u32 m0,s57,74624
s_nop 0
buffer_load_dwordx4 v11,s[40:43],s55 offen offset:128  lds
v_add_u32 v18,81920,v12
v_add_u32 v19,81920,v13
T v[112:113],18,0
T v[114:115],18,512
T v[116:117],19,0
T v[118:119],19,512
T v[120:121],18,4096
T v[122:123],18,4608
T v[124:125],19,4096
T v[126:127],19,4608
s_waitcnt vmcnt(20) lgkmcnt(0)
s_barrier
s_add_u32 s55,s50,3407872
s_add_u32 m0,s57,81920
s_nop 0
buffer_load_dwordx4 v10,s[40:43],s55 offen offset:0  lds
s_add_u32 m0,s57,82944
s_nop 0
buffer_load_dwordx4 v11,s[40:43],s55 offen offset:0  lds
s_add_u32 m0,s57,89984
s_nop 0
buffer_load_dwordx4 v10,s[40:43],s55 offen offset:128  lds
s_add_u32 m0,s57,91008
s_nop 0
buffer_load_dwordx4 v11,s[40:43],s55 offen offset:128  lds
v_add_u32 v18,98304,v12
v_add_u32 v19,98304,v13
T v[128:129],18,0
T v[130:131],18,512
T v[132:133],19,0
T v[134:135],19,512
T v[136:137],18,4096
T v[138:139],18,4608
T v[140:141],19,4096
T v[142:143],19,4608
s_waitcnt vmcnt(20) lgkmcnt(0)
s_barrier
s_add_u32 s55,s50,3538944
s_add_u32 m0,s57,98304
s_nop 0
buffer_load_dwordx4 v10,s[40:43],s55 offen offset:0  lds
s_add_u32 m0,s57,99328
s_nop 0
buffer_load_dwordx4 v11,s[40:43],s55 offen offset:0  lds
s_add_u32 m0,s57,106368
s_nop 0
buffer_load_dwordx4 v10,s[40:43],s55 offen offset:128  lds
s_add_u32 m0,s57,107392
s_nop 0
buffer_load_dwordx4 v11,s[40:43],s55 offen offset:128  lds
v_add_u32 v18,0,v12
v_add_u32 v19,0,v13
T v[144:145],18,0
T v[146:147],18,512
T v[148:149],19,0
T v[150:151],19,512
T v[152:153],18,4096
T v[154:155],18,4608
T v[156:157],19,4096
T v[158:159],19,4608
s_waitcnt vmcnt(20) lgkmcnt(0)
s_barrier
v_add_u32 v18,16384,v12
v_add_u32 v19,16384,v13
T v[160:161],18,0
T v[162:163],18,512
T v[164:165],19,0
T v[166:167],19,512
T v[168:169],18,4096
T v[170:171],18,4608
T v[172:173],19,4096
T v[174:175],19,4608
s_waitcnt vmcnt(16) lgkmcnt(0)
s_barrier
v_add_u32 v18,32768,v12
v_add_u32 v19,32768,v13
T v[176:177],18,0
T v[178:179],18,512
T v[180:181],19,0
T v[182:183],19,512
T v[184:185],18,4096
T v[186:187],18,4608
T v[188:189],19,4096
T v[190:191],19,4608
s_waitcnt vmcnt(12) lgkmcnt(0)
s_barrier
v_add_u32 v18,49152,v12
v_add_u32 v19,49152,v13
T v[192:193],18,0
T v[194:195],18,512
T v[196:197],19,0
T v[198:199],19,512
T v[200:201],18,4096
T v[202:203],18,4608
T v[204:205],19,4096
T v[206:207],19,4608
s_waitcnt vmcnt(8) lgkmcnt(0)
s_barrier
v_add_u32 v18,65536,v12
v_add_u32 v19,65536,v13
T v[208:209],18,0
T v[210:211],18,512
T v[212:213],19,0
T v[214:215],19,512
T v[216:217],18,4096
T v[218:219],18,4608
T v[220:221],19,4096
T v[222:223],19,4608
s_waitcnt vmcnt(4) lgkmcnt(0)
s_barrier
v_add_u32 v18,81920,v12
v_add_u32 v19,81920,v13
T v[224:225],18,0
T v[226:227],18,512
T v[228:229],19,0
T v[230:231],19,512
T v[232:233],18,4096
T v[234:235],18,4608
T v[236:237],19,4096
T v[238:239],19,4608
s_waitcnt vmcnt(0) lgkmcnt(0)
s_barrier
v_add_u32 v18,98304,v12
v_add_u32 v19,98304,v13
T v[240:241],18,0
T v[242:243],18,512
T v[244:245],19,0
T v[246:247],19,512
T v[248:249],18,4096
T v[250:251],18,4608
T v[252:253],19,4096
T v[254:255],19,4608
L 0
s_barrier
v_mov_b32 v4,s53
s_mov_b32 s82,0
s_mov_b32 s80,0
s_mov_b32 s54,s45
s_mul_i32 s57,s44,7168
s_mul_i32 s55,s45,114688
s_add_u32 s55,s55,s57
s_add_u32 s55,s55,40894464
s_mul_i32 s57,s44,7168
s_mul_i32 s60,s46,114688
s_mul_i32 s61,s46,2560
s_mul_i32 s62,s46,32768
s_mul_i32 s63,s46,2048
s_mul_i32 s64,s46,512
s_lshl_b32 s59,s44,8
s_add_u32 s59,s59,143360
s_mul_i32 s58,s45,2560
s_add_u32 s58,s58,109891584
s_mul_i32 s65,s45,32768
s_add_u32 s65,s65,s47
s_mul_i32 s68,s45,2048
s_add_u32 s68,s68,s48
s_mul_i32 s73,s45,512
s_add_u32 s73,s73,106954752
s_mul_i32 s74,s45,32768
s_add_u32 s74,s74,s49
s_mul_i32 s75,s45,1024
s_add_u32 s75,s75,107479040
s_mov_b32 s66,0x7fff0000
s_mov_b32 s67,0x7fff0000
s_mov_b32 s69,0x7fff0000
s_mov_b32 s76,0x7fff0000
s_mov_b32 s77,0x7fff0000
s_mov_b32 s70,0x7fff0000
s_mov_b32 s71,0x7fff0000
s_mov_b32 s72,0x7fff0000
v_mov_b32 v1,v0
v_add_u32 v2,28672,v0
s_mov_b32 s84,28672
s_mov_b32 s86,0x3a800000
s_mov_b32 s87,0x3727c5ac
s_mov_b32 s88,0xc0135761
s_mov_b32 s89,0x3d372713
s_lshr_b32 s85,s44,1
s_mul_i32 s85,s85,127
s_add_u32 s55,s55,s51
s_mul_i32 s78,s45,114688
s_sub_u32 s60,s55,s78
s_cmp_lt_u32 s45,214
s_cselect_b32 s63,214,-1
v_lshlrev_b32 v8,22,v0
v_and_b32 v8,0x80000000,v8
v_or_b32 v8,v8,v5
v_add_u32 v2,57344,v0
s_lshl_b32 s84,s57,1
s_add_u32 s84,s84,57344
s_mov_b64 s[86:87],%22
s_mov_b64 s[40:41],%22
s_mov_b32 s42,4194304
s_and_b32 s78,s47,65535
s_lshr_b32 s78,s78,4
s_sub_u32 s85,s47,s78
s_add_u32 s85,s85,4194304
s_mul_i32 s73,s44,1280
s_add_u32 s73,s73,114688
v_lshrrev_b32 v62,4,v9
v_and_b32 v62,3,v62
v_mul_u32_u24 v62,288,v62
v_lshrrev_b32 v63,7,v9
v_and_b32 v5,12,v9
v_add3_u32 v5,v5,v62,v63
v_add_u32 v5,s73,v5
v_mov_b32 v10,0
v_mov_b32 v11,0
v_mov_b32 v12,0
v_mov_b32 v13,0
s_mov_b32 s79,0
buffer_store_dwordx2 v[10:11],v7,s[40:43],s79 offen offset:0 sc0 sc1
s_mov_b32 s79,163840
buffer_store_dwordx2 v[10:11],v7,s[40:43],s79 offen offset:0 sc0 sc1
s_mov_b32 s79,327680
buffer_store_dwordx2 v[10:11],v7,s[40:43],s79 offen offset:0 sc0 sc1
s_getpc_b64 s[40:41]
Lg4_pc_%=:
s_lshl_b32 s78,s44,13
v_lshlrev_b32 v62,3,v0
v_add_u32 v62,s78,v62
v_min_u32 v62,Lcode_g4_e-Lg4_pc_%=-4,v62
s_mov_b32 m0,163584
s_nop 0
global_load_lds_dword v62,s[40:41]
s_add_u32 m0,s59,4096
s_nop 0
buffer_load_dword v8,s[36:39],s58 offen offset:0 sc1 lds
buffer_store_dword v4,v3,s[36:39],s71 offen offset:0 sc0 sc1
s_add_u32 m0,s59,5120
s_nop 0
buffer_load_dword v8,s[36:39],s58 offen offset:0 sc1 lds
buffer_store_dword v4,v3,s[36:39],s71 offen offset:0 sc0 sc1
s_mov_b32 s83,0
Lg4_poll_%=:
s_add_u32 s78,s83,2
s_and_b32 s78,s78,3
s_lshl_b32 s78,s78,10
s_add_u32 s78,s78,4096
s_add_u32 m0,s59,s78
s_nop 0
buffer_load_dword v8,s[36:39],s58 offen offset:0 sc1 lds
s_and_b32 s78,s83,3
s_lshl_b32 s78,s78,10
v_add_u32 v62,s78,v6
s_waitcnt vmcnt(4)
s_barrier
ds_read_b32 v7,v62 offset:4096
buffer_store_dword v4,v3,s[36:39],s70 offen offset:0 sc0 sc1
s_mov_b32 s70,s71
s_mov_b32 s71,s72
s_mov_b32 s72,0x7fff0000
s_add_u32 s83,s83,1
s_add_u32 s82,s82,1
L 0
v_cmp_eq_u32 vcc,s53,v7
s_nop 0
s_cmp_eq_u32 vcc_lo,-1
s_cbranch_scc1 Lg4_go_%=
s_cmp_lt_u32 s82,20000
s_cbranch_scc1 Lg4_poll_%=
Lg4_go_%=:
s_add_u32 m0,s57,0
s_add_u32 s56,s55,0
D 0,56,0
D 0,56,1024
D 0,56,2048
D 0,56,3072
s_add_u32 m0,s57,4096
s_add_u32 s56,s56,4096
D 0,56,0
D 0,56,1024
D 0,56,2048
s_add_u32 s61,s54,s46
s_cmp_ge_u32 s61,s63
s_cselect_b32 s61,s93,s61
s_cselect_b32 s46,7,s46
s_cselect_b32 s63,-1,s63
s_mul_i32 s58,s61,2560
s_add_u32 s58,s58,109891584
s_add_u32 m0,s59,2048
s_nop 0
buffer_load_dword v8,s[36:39],s58 offen offset:0 sc1 lds
s_add_u32 m0,s57,28672
s_add_u32 s56,s55,28672
D 0,56,0
D 0,56,1024
D 0,56,2048
D 0,56,3072
s_add_u32 m0,s57,32768
s_add_u32 s56,s56,4096
D 0,56,0
D 0,56,1024
D 0,56,2048
s_mul_i32 s55,s61,114688
s_add_u32 s55,s55,s60
s_sub_u32 s57,s84,s57
s_mov_b32 s54,s61
s_waitcnt vmcnt(7)
s_barrier
buffer_store_dword v4,v3,s[36:39],s70 offen offset:0 sc0 sc1
s_min_u32 s71,s71,s72
buffer_store_dword v4,v3,s[36:39],s71 offen offset:0 sc0 sc1
s_mov_b32 s70,0x7fff0000
s_mov_b32 s71,0x7fff0000
s_mov_b32 s72,0x7fff0000
s_cmp_eq_u32 s80,1
s_cbranch_scc1 Lg4_st1_%=
s_branch Lg4_st0_%=
Lg4_gox_%=:
s_add_u32 m0,s59,2048
s_nop 0
buffer_load_dword v8,s[36:39],s58 offen offset:0 sc1 lds
s_waitcnt vmcnt(0)
s_barrier
buffer_store_dword v4,v3,s[36:39],s70 offen offset:0 sc0 sc1
s_min_u32 s71,s71,s72
buffer_store_dword v4,v3,s[36:39],s71 offen offset:0 sc0 sc1
s_mov_b32 s70,0x7fff0000
s_mov_b32 s71,0x7fff0000
s_mov_b32 s72,0x7fff0000
s_cmp_eq_u32 s80,1
s_cbranch_scc1 Lg4_st1_%=
Lg4_st0_%=:
ds_read_b32 v7,v6 offset:2048
R 26,1,0
R 30,1,1024
R 34,1,2048
R 38,1,3072
R 42,1,4096
L 5
v_cmp_eq_u32 vcc,s53,v7
L 3
s_cmp_eq_u32 vcc_lo,-1
s_cbranch_scc1 Lg4_F0_%=
s_branch Lg4_L0_%=
Lg4_st1_%=:
ds_read_b32 v7,v6 offset:2048
R 26,2,0
R 30,2,1024
R 34,2,2048
R 38,2,3072
R 42,2,4096
L 5
v_cmp_eq_u32 vcc,s53,v7
L 3
s_cmp_eq_u32 vcc_lo,-1
s_cbranch_scc1 Lg4_F1_%=
s_branch Lg4_L1_%=
Lg4_F1_%=:
s_cmp_eq_u32 s44,1
s_cbranch_scc1 Lg4_F1w1_%=
s_cmp_eq_u32 s44,2
s_cbranch_scc1 Lg4_F1w2_%=
s_cmp_eq_u32 s44,3
s_cbranch_scc1 Lg4_F1w3_%=
s_branch Lg4_F1w0_%=
Lg4_F0_%=:
s_cmp_eq_u32 s44,1
s_cbranch_scc1 Lg4_F0w1_%=
s_cmp_eq_u32 s44,2
s_cbranch_scc1 Lg4_F0w2_%=
s_cmp_eq_u32 s44,3
s_cbranch_scc1 Lg4_F0w3_%=
Lg4_F0w0_%=:
v_mfma_f32_16x16x32_bf16 v[14:17],a[4:7],v[26:29],0
s_add_u32 m0,s57,0
R 46,1,5120
v_lshlrev_b32 v62,16,v58
M 14,a[12:15],30
s_add_u32 s56,s55,0
v_add_f32 v18,v18,v62
v_and_b32 v63,0xffff0000,v58
v_mfma_f32_16x16x32_bf16 v[10:13],a[0:3],v[26:29],0
s_add_u32 s61,s54,s46
R 50,1,6144
v_add_f32 v19,v19,v63
M 10,a[8:11],30
L 3
D 0,56,0
M 10,a[16:19],34
s_cmp_ge_u32 s61,s63
R 54,1,7168
v_lshlrev_b32 v62,16,v59
M 10,a[24:27],38
s_cselect_b32 s61,s93,s61
v_add_f32 v20,v20,v62
v_and_b32 v63,0xffff0000,v59
M 14,a[20:23],34
)"
R"(s_cselect_b32 s46,7,s46
R 26,1,8192
v_add_f32 v21,v21,v63
M 14,a[28:31],38
L 3
v_lshlrev_b32 v62,16,v60
v_add_f32 v22,v22,v62
M 14,a[36:39],42
s_cselect_b32 s63,-1,s63
R 30,1,9216
v_and_b32 v63,0xffff0000,v60
M 14,a[44:47],46
s_mul_i32 s58,s61,2560
D 0,56,1024
M 10,a[32:35],42
s_add_u32 s58,s58,109891584
R 34,1,10240
v_add_f32 v23,v23,v63
M 10,a[40:43],46
L 3
v_lshlrev_b32 v62,16,v61
v_add_f32 v24,v24,v62
M 10,a[48:51],50
s_lshl_b32 s62,s54,15
R 38,1,11264
v_and_b32 v63,0xffff0000,v61
M 10,a[56:59],54
s_mov_b32 s66,s65
buffer_load_dwordx4 v[58:61],v0,s[36:39],s74 offen offset:0 sc1
M 14,a[52:55],50
s_add_u32 s74,s49,s62
R 42,1,12288
v_add_f32 v25,v25,v63
M 14,a[60:63],54
L 3
v_cvt_pk_bf16_f32 v18,v18,v19
v_cvt_pk_bf16_f32 v19,v20,v21
M 14,a[68:71],26
s_add_u32 s65,s47,s62
R 46,1,13312
v_cvt_pk_bf16_f32 v20,v22,v23
M 14,a[76:79],30
s_mov_b32 s54,s61
D 0,56,2048
M 10,a[64:67],26
R 50,1,14336
v_cvt_pk_bf16_f32 v21,v24,v25
M 10,a[72:75],30
L 3
buffer_store_dwordx4 v[18:21],v0,s[36:39],s67 offen offset:0 sc0 sc1
M 10,a[80:83],34
R 54,1,15360
M 10,a[88:91],38
M 14,a[84:87],34
R 26,1,16384
M 14,a[92:95],38
L 3
M 14,a[100:103],42
R 30,1,17408
M 14,a[108:111],46
D 0,56,3072
M 10,a[96:99],42
s_add_u32 m0,s57,4096
s_add_u32 s56,s56,4096
R 34,1,18432
M 10,a[104:107],46
L 3
M 10,a[112:115],50
R 38,1,19456
M 10,a[120:123],54
M 14,a[116:119],50
R 42,1,20480
M 14,a[124:127],54
L 3
D 0,56,0
M 14,a[132:135],26
R 46,1,21504
M 14,a[140:143],30
M 10,a[128:131],26
R 50,1,22528
M 10,a[136:139],30
L 3
M 10,a[144:147],34
R 54,1,23552
M 10,a[152:155],38
M 14,a[148:151],34
R 26,1,24576
M 14,a[156:159],38
L 3
D 0,56,1024
M 14,a[164:167],42
R 30,1,25600
M 14,a[172:175],46
M 10,a[160:163],42
R 34,1,26624
M 10,a[168:171],46
L 3
M 10,a[176:179],50
s_waitcnt vmcnt(8)
R 38,1,27648
M 10,a[184:187],54
s_barrier
M 14,a[180:183],50
R 42,1,28672
M 14,a[188:191],54
L 3
D 0,56,2048
M 14,a[196:199],26
s_add_u32 m0,s59,0
s_add_u32 s56,s55,28672
R 46,1,29696
M 14,a[204:207],30
s_mul_i32 s55,s61,114688
s_add_u32 s55,s55,s60
M 10,a[192:195],26
R 50,1,30720
M 10,a[200:203],30
L 3
M 10,a[208:211],34
R 54,1,31744
M 10,a[216:219],38
M 14,a[212:215],34
R 26,1,32768
M 14,a[220:223],38
L 3
buffer_load_dword v8,s[36:39],s58 offen offset:0 sc1 lds
M 14,a[228:231],42
s_add_u32 m0,s57,28672
R 30,1,33792
M 14,a[236:239],46
D 0,56,0
M 10,a[224:227],42
R 34,1,34816
M 10,a[232:235],46
L 3
M 10,a[240:243],50
R 38,1,35840
M 10,a[248:251],54
M 14,a[244:247],50
R 42,1,36864
M 14,a[252:255],54
L 3
M 14,v[68:71],26
R 46,1,37888
M 14,v[76:79],30
D 0,56,1024
M 10,v[64:67],26
R 50,1,38912
M 10,v[72:75],30
L 3
M 10,v[80:83],34
R 54,1,39936
M 10,v[88:91],38
M 14,v[84:87],34
R 26,1,40960
M 14,v[92:95],38
L 3
M 14,v[100:103],42
R 30,1,41984
M 14,v[108:111],46
D 0,56,2048
M 10,v[96:99],42
R 34,1,43008
M 10,v[104:107],46
L 3
M 10,v[112:115],50
R 38,1,44032
M 10,v[120:123],54
M 14,v[116:119],50
R 42,1,45056
M 14,v[124:127],54
L 3
D 0,56,3072
M 14,v[132:135],26
s_add_u32 m0,s57,32768
s_add_u32 s56,s56,4096
R 46,1,46080
M 14,v[140:143],30
s_sub_u32 s57,s84,s57
M 10,v[128:131],26
R 50,1,47104
M 10,v[136:139],30
L 3
M 10,v[144:147],34
R 54,1,48128
M 10,v[152:155],38
M 14,v[148:151],34
R 26,1,49152
M 14,v[156:159],38
L 3
D 0,56,0
M 14,v[164:167],42
R 30,1,50176
M 14,v[172:175],46
M 10,v[160:163],42
R 34,1,51200
M 10,v[168:171],46
L 3
M 10,v[176:179],50
R 38,1,52224
M 10,v[184:187],54
M 14,v[180:183],50
R 42,1,53248
M 14,v[188:191],54
L 3
D 0,56,1024
M 14,v[196:199],26
R 46,1,54272
M 14,v[204:207],30
M 10,v[192:195],26
R 50,1,55296
M 10,v[200:203],30
L 3
M 10,v[208:211],34
s_waitcnt vmcnt(6)
R 54,1,56320
M 10,v[216:219],38
s_barrier
ds_read_b32 v7,v6 offset:0
M 14,v[212:215],34
R 26,2,0
M 14,v[220:223],38
L 4
buffer_store_dword v4,v3,s[36:39],s69 offen offset:0 sc0 sc1
M 14,v[228:231],42
s_mov_b32 s69,s68
s_lshr_b32 s68,s62,4
R 30,2,1024
M 14,v[236:239],46
s_add_u32 s68,s68,s48
D 0,56,2048
M 10,v[224:227],42
R 34,2,2048
M 10,v[232:235],46
L 4
L 3
M 10,v[240:243],50
R 38,2,3072
v_cmp_eq_u32 vcc,s53,v7
M 10,v[248:251],54
s_cmp_eq_u32 vcc_lo,-1
M 14,v[244:247],50
R 42,2,4096
M 14,v[252:255],54
L 3
s_cbranch_scc0 Lg4_L1_%=
Lg4_F1w0_%=:
v_mfma_f32_16x16x32_bf16 v[22:25],a[4:7],v[26:29],0
s_add_u32 m0,s57,0
R 46,2,5120
v_lshlrev_b32 v62,16,v58
M 22,a[12:15],30
s_add_u32 s56,s55,0
v_add_f32 v10,v10,v62
v_and_b32 v63,0xffff0000,v58
v_mfma_f32_16x16x32_bf16 v[18:21],a[0:3],v[26:29],0
s_add_u32 s61,s54,s46
R 50,2,6144
v_add_f32 v11,v11,v63
M 18,a[8:11],30
L 3
D 0,56,0
M 18,a[16:19],34
s_cmp_ge_u32 s61,s63
R 54,2,7168
v_lshlrev_b32 v62,16,v59
M 18,a[24:27],38
s_cselect_b32 s61,s93,s61
v_add_f32 v12,v12,v62
v_and_b32 v63,0xffff0000,v59
M 22,a[20:23],34
s_cselect_b32 s46,7,s46
R 26,2,8192
v_add_f32 v13,v13,v63
M 22,a[28:31],38
L 3
v_lshlrev_b32 v62,16,v60
v_add_f32 v14,v14,v62
M 22,a[36:39],42
s_cselect_b32 s63,-1,s63
R 30,2,9216
v_and_b32 v63,0xffff0000,v60
M 22,a[44:47],46
s_mul_i32 s58,s61,2560
D 0,56,1024
M 18,a[32:35],42
s_add_u32 s58,s58,109891584
R 34,2,10240
v_add_f32 v15,v15,v63
M 18,a[40:43],46
L 3
v_lshlrev_b32 v62,16,v61
v_add_f32 v16,v16,v62
M 18,a[48:51],50
s_lshl_b32 s62,s54,15
R 38,2,11264
v_and_b32 v63,0xffff0000,v61
M 18,a[56:59],54
s_mov_b32 s67,s65
buffer_load_dwordx4 v[58:61],v0,s[36:39],s74 offen offset:0 sc1
M 22,a[52:55],50
s_add_u32 s74,s49,s62
R 42,2,12288
v_add_f32 v17,v17,v63
M 22,a[60:63],54
L 3
v_cvt_pk_bf16_f32 v10,v10,v11
v_cvt_pk_bf16_f32 v11,v12,v13
M 22,a[68:71],26
s_add_u32 s65,s47,s62
R 46,2,13312
v_cvt_pk_bf16_f32 v12,v14,v15
M 22,a[76:79],30
s_mov_b32 s54,s61
D 0,56,2048
M 18,a[64:67],26
R 50,2,14336
v_cvt_pk_bf16_f32 v13,v16,v17
M 18,a[72:75],30
L 3
buffer_store_dwordx4 v[10:13],v0,s[36:39],s66 offen offset:0 sc0 sc1
M 18,a[80:83],34
R 54,2,15360
M 18,a[88:91],38
M 22,a[84:87],34
R 26,2,16384
M 22,a[92:95],38
L 3
M 22,a[100:103],42
R 30,2,17408
M 22,a[108:111],46
D 0,56,3072
M 18,a[96:99],42
s_add_u32 m0,s57,4096
s_add_u32 s56,s56,4096
R 34,2,18432
M 18,a[104:107],46
L 3
M 18,a[112:115],50
R 38,2,19456
M 18,a[120:123],54
M 22,a[116:119],50
R 42,2,20480
M 22,a[124:127],54
L 3
D 0,56,0
M 22,a[132:135],26
R 46,2,21504
M 22,a[140:143],30
M 18,a[128:131],26
R 50,2,22528
M 18,a[136:139],30
L 3
M 18,a[144:147],34
R 54,2,23552
M 18,a[152:155],38
M 22,a[148:151],34
R 26,2,24576
M 22,a[156:159],38
L 3
D 0,56,1024
M 22,a[164:167],42
R 30,2,25600
M 22,a[172:175],46
M 18,a[160:163],42
R 34,2,26624
M 18,a[168:171],46
L 3
M 18,a[176:179],50
s_waitcnt vmcnt(8)
R 38,2,27648
M 18,a[184:187],54
s_barrier
M 22,a[180:183],50
R 42,2,28672
M 22,a[188:191],54
L 3
D 0,56,2048
M 22,a[196:199],26
s_add_u32 m0,s59,1024
s_add_u32 s56,s55,28672
R 46,2,29696
M 22,a[204:207],30
s_mul_i32 s55,s61,114688
s_add_u32 s55,s55,s60
M 18,a[192:195],26
R 50,2,30720
M 18,a[200:203],30
L 3
M 18,a[208:211],34
R 54,2,31744
M 18,a[216:219],38
M 22,a[212:215],34
R 26,2,32768
M 22,a[220:223],38
L 3
buffer_load_dword v8,s[36:39],s58 offen offset:0 sc1 lds
M 22,a[228:231],42
s_add_u32 m0,s57,28672
R 30,2,33792
M 22,a[236:239],46
D 0,56,0
M 18,a[224:227],42
R 34,2,34816
M 18,a[232:235],46
L 3
M 18,a[240:243],50
R 38,2,35840
M 18,a[248:251],54
M 22,a[244:247],50
R 42,2,36864
M 22,a[252:255],54
L 3
M 22,v[68:71],26
R 46,2,37888
M 22,v[76:79],30
D 0,56,1024
M 18,v[64:67],26
R 50,2,38912
M 18,v[72:75],30
L 3
M 18,v[80:83],34
R 54,2,39936
M 18,v[88:91],38
M 22,v[84:87],34
R 26,2,40960
M 22,v[92:95],38
L 3
M 22,v[100:103],42
R 30,2,41984
M 22,v[108:111],46
D 0,56,2048
M 18,v[96:99],42
R 34,2,43008
M 18,v[104:107],46
L 3
M 18,v[112:115],50
R 38,2,44032
M 18,v[120:123],54
M 22,v[116:119],50
R 42,2,45056
M 22,v[124:127],54
L 3
D 0,56,3072
M 22,v[132:135],26
s_add_u32 m0,s57,32768
s_add_u32 s56,s56,4096
R 46,2,46080
M 22,v[140:143],30
s_sub_u32 s57,s84,s57
M 18,v[128:131],26
R 50,2,47104
M 18,v[136:139],30
L 3
M 18,v[144:147],34
R 54,2,48128
M 18,v[152:155],38
M 22,v[148:151],34
R 26,2,49152
M 22,v[156:159],38
L 3
D 0,56,0
M 22,v[164:167],42
R 30,2,50176
M 22,v[172:175],46
M 18,v[160:163],42
R 34,2,51200
M 18,v[168:171],46
L 3
M 18,v[176:179],50
R 38,2,52224
M 18,v[184:187],54
M 22,v[180:183],50
R 42,2,53248
M 22,v[188:191],54
L 3
D 0,56,1024
M 22,v[196:199],26
R 46,2,54272
M 22,v[204:207],30
M 18,v[192:195],26
R 50,2,55296
M 18,v[200:203],30
L 3
M 18,v[208:211],34
s_waitcnt vmcnt(6)
R 54,2,56320
M 18,v[216:219],38
s_barrier
ds_read_b32 v7,v6 offset:1024
M 22,v[212:215],34
R 26,1,0
M 22,v[220:223],38
L 4
buffer_store_dword v4,v3,s[36:39],s69 offen offset:0 sc0 sc1
M 22,v[228:231],42
s_mov_b32 s69,s68
s_lshr_b32 s68,s62,4
R 30,1,1024
M 22,v[236:239],46
)"
R"(s_add_u32 s68,s68,s48
D 0,56,2048
M 18,v[224:227],42
R 34,1,2048
M 18,v[232:235],46
L 4
L 3
M 18,v[240:243],50
R 38,1,3072
v_cmp_eq_u32 vcc,s53,v7
M 18,v[248:251],54
s_cmp_eq_u32 vcc_lo,-1
M 22,v[244:247],50
R 42,1,4096
M 22,v[252:255],54
L 3
s_cbranch_scc1 Lg4_F0w0_%=
s_branch Lg4_L0_%=
Lg4_F0w1_%=:
v_mfma_f32_16x16x32_bf16 v[14:17],a[4:7],v[26:29],0
s_add_u32 m0,s57,0
R 46,1,5120
v_lshlrev_b32 v62,16,v58
M 14,a[12:15],30
s_add_u32 s56,s55,0
v_add_f32 v18,v18,v62
v_and_b32 v63,0xffff0000,v58
v_mfma_f32_16x16x32_bf16 v[10:13],a[0:3],v[26:29],0
s_add_u32 s61,s54,s46
R 50,1,6144
v_add_f32 v19,v19,v63
M 10,a[8:11],30
L 3
D 0,56,0
M 10,a[16:19],34
s_cmp_ge_u32 s61,s63
R 54,1,7168
v_lshlrev_b32 v62,16,v59
M 10,a[24:27],38
s_cselect_b32 s61,s93,s61
v_add_f32 v20,v20,v62
v_and_b32 v63,0xffff0000,v59
M 14,a[20:23],34
s_cselect_b32 s46,7,s46
R 26,1,8192
v_add_f32 v21,v21,v63
M 14,a[28:31],38
L 3
v_lshlrev_b32 v62,16,v60
v_add_f32 v22,v22,v62
M 14,a[36:39],42
s_cselect_b32 s63,-1,s63
R 30,1,9216
v_and_b32 v63,0xffff0000,v60
M 14,a[44:47],46
s_mul_i32 s58,s61,2560
D 0,56,1024
M 10,a[32:35],42
s_add_u32 s58,s58,109891584
R 34,1,10240
v_add_f32 v23,v23,v63
M 10,a[40:43],46
L 3
v_lshlrev_b32 v62,16,v61
v_add_f32 v24,v24,v62
M 10,a[48:51],50
s_lshl_b32 s62,s54,15
R 38,1,11264
v_and_b32 v63,0xffff0000,v61
M 10,a[56:59],54
s_mov_b32 s66,s65
buffer_load_dwordx4 v[58:61],v0,s[36:39],s74 offen offset:0 sc1
M 14,a[52:55],50
s_add_u32 s74,s49,s62
R 42,1,12288
v_add_f32 v25,v25,v63
M 14,a[60:63],54
L 3
v_cvt_pk_bf16_f32 v18,v18,v19
v_cvt_pk_bf16_f32 v19,v20,v21
M 14,a[68:71],26
s_add_u32 s65,s47,s62
R 46,1,13312
v_cvt_pk_bf16_f32 v20,v22,v23
M 14,a[76:79],30
s_mov_b32 s54,s61
D 0,56,2048
M 10,a[64:67],26
R 50,1,14336
v_cvt_pk_bf16_f32 v21,v24,v25
M 10,a[72:75],30
L 3
buffer_store_dwordx4 v[18:21],v0,s[36:39],s67 offen offset:0 sc0 sc1
M 10,a[80:83],34
R 54,1,15360
M 10,a[88:91],38
M 14,a[84:87],34
R 26,1,16384
M 14,a[92:95],38
L 3
M 14,a[100:103],42
R 30,1,17408
M 14,a[108:111],46
D 0,56,3072
M 10,a[96:99],42
s_add_u32 m0,s57,4096
s_add_u32 s56,s56,4096
R 34,1,18432
M 10,a[104:107],46
L 3
M 10,a[112:115],50
R 38,1,19456
M 10,a[120:123],54
M 14,a[116:119],50
R 42,1,20480
M 14,a[124:127],54
L 3
D 0,56,0
M 14,a[132:135],26
R 46,1,21504
M 14,a[140:143],30
M 10,a[128:131],26
R 50,1,22528
M 10,a[136:139],30
L 3
M 10,a[144:147],34
R 54,1,23552
M 10,a[152:155],38
M 14,a[148:151],34
R 26,1,24576
M 14,a[156:159],38
L 3
D 0,56,1024
M 14,a[164:167],42
R 30,1,25600
M 14,a[172:175],46
M 10,a[160:163],42
R 34,1,26624
M 10,a[168:171],46
s_waitcnt vmcnt(8) lgkmcnt(3)
M 10,a[176:179],50
s_barrier
R 38,1,27648
M 10,a[184:187],54
M 14,a[180:183],50
R 42,1,28672
M 14,a[188:191],54
L 3
D 0,56,2048
M 14,a[196:199],26
s_add_u32 m0,s59,0
s_add_u32 s56,s55,28672
R 46,1,29696
M 14,a[204:207],30
s_mul_i32 s55,s61,114688
s_add_u32 s55,s55,s60
M 10,a[192:195],26
R 50,1,30720
M 10,a[200:203],30
L 3
M 10,a[208:211],34
R 54,1,31744
M 10,a[216:219],38
M 14,a[212:215],34
R 26,1,32768
M 14,a[220:223],38
L 3
buffer_load_dword v8,s[36:39],s58 offen offset:0 sc1 lds
M 14,a[228:231],42
s_add_u32 m0,s57,28672
R 30,1,33792
M 14,a[236:239],46
D 0,56,0
M 10,a[224:227],42
R 34,1,34816
M 10,a[232:235],46
L 3
M 10,a[240:243],50
R 38,1,35840
M 10,a[248:251],54
M 14,a[244:247],50
R 42,1,36864
M 14,a[252:255],54
L 3
M 14,v[68:71],26
R 46,1,37888
M 14,v[76:79],30
D 0,56,1024
M 10,v[64:67],26
R 50,1,38912
M 10,v[72:75],30
L 3
M 10,v[80:83],34
R 54,1,39936
M 10,v[88:91],38
M 14,v[84:87],34
R 26,1,40960
M 14,v[92:95],38
L 3
M 14,v[100:103],42
R 30,1,41984
M 14,v[108:111],46
D 0,56,2048
M 10,v[96:99],42
R 34,1,43008
M 10,v[104:107],46
L 3
M 10,v[112:115],50
R 38,1,44032
M 10,v[120:123],54
M 14,v[116:119],50
R 42,1,45056
M 14,v[124:127],54
L 3
D 0,56,3072
M 14,v[132:135],26
s_add_u32 m0,s57,32768
s_add_u32 s56,s56,4096
R 46,1,46080
M 14,v[140:143],30
s_sub_u32 s57,s84,s57
M 10,v[128:131],26
R 50,1,47104
M 10,v[136:139],30
L 3
M 10,v[144:147],34
R 54,1,48128
M 10,v[152:155],38
M 14,v[148:151],34
R 26,1,49152
M 14,v[156:159],38
L 3
D 0,56,0
M 14,v[164:167],42
R 30,1,50176
M 14,v[172:175],46
M 10,v[160:163],42
R 34,1,51200
M 10,v[168:171],46
L 3
M 10,v[176:179],50
R 38,1,52224
M 10,v[184:187],54
M 14,v[180:183],50
R 42,1,53248
M 14,v[188:191],54
L 3
D 0,56,1024
M 14,v[196:199],26
R 46,1,54272
M 14,v[204:207],30
M 10,v[192:195],26
R 50,1,55296
M 10,v[200:203],30
s_waitcnt vmcnt(6) lgkmcnt(3)
M 10,v[208:211],34
s_barrier
R 54,1,56320
ds_read_b32 v7,v6 offset:0
M 10,v[216:219],38
buffer_store_dword v4,v3,s[36:39],s69 offen offset:0 sc0 sc1
M 14,v[212:215],34
s_mov_b32 s69,s68
s_lshr_b32 s68,s62,4
R 26,2,0
M 14,v[220:223],38
L 4
D 0,56,2048
M 14,v[228:231],42
s_add_u32 s68,s68,s48
R 30,2,1024
M 14,v[236:239],46
M 10,v[224:227],42
L 2
R 34,2,2048
v_cmp_eq_u32 vcc,s53,v7
M 10,v[232:235],46
s_nop 0
s_cmp_eq_u32 vcc_lo,-1
M 10,v[240:243],50
R 38,2,3072
M 10,v[248:251],54
M 14,v[244:247],50
R 42,2,4096
M 14,v[252:255],54
L 3
s_cbranch_scc0 Lg4_L1_%=
Lg4_F1w1_%=:
v_mfma_f32_16x16x32_bf16 v[22:25],a[4:7],v[26:29],0
s_add_u32 m0,s57,0
R 46,2,5120
v_lshlrev_b32 v62,16,v58
M 22,a[12:15],30
s_add_u32 s56,s55,0
v_add_f32 v10,v10,v62
v_and_b32 v63,0xffff0000,v58
v_mfma_f32_16x16x32_bf16 v[18:21],a[0:3],v[26:29],0
s_add_u32 s61,s54,s46
R 50,2,6144
v_add_f32 v11,v11,v63
M 18,a[8:11],30
L 3
D 0,56,0
M 18,a[16:19],34
s_cmp_ge_u32 s61,s63
R 54,2,7168
v_lshlrev_b32 v62,16,v59
M 18,a[24:27],38
s_cselect_b32 s61,s93,s61
v_add_f32 v12,v12,v62
v_and_b32 v63,0xffff0000,v59
M 22,a[20:23],34
s_cselect_b32 s46,7,s46
R 26,2,8192
v_add_f32 v13,v13,v63
M 22,a[28:31],38
L 3
v_lshlrev_b32 v62,16,v60
v_add_f32 v14,v14,v62
M 22,a[36:39],42
s_cselect_b32 s63,-1,s63
R 30,2,9216
v_and_b32 v63,0xffff0000,v60
M 22,a[44:47],46
s_mul_i32 s58,s61,2560
D 0,56,1024
M 18,a[32:35],42
s_add_u32 s58,s58,109891584
R 34,2,10240
v_add_f32 v15,v15,v63
M 18,a[40:43],46
L 3
v_lshlrev_b32 v62,16,v61
v_add_f32 v16,v16,v62
M 18,a[48:51],50
s_lshl_b32 s62,s54,15
R 38,2,11264
v_and_b32 v63,0xffff0000,v61
M 18,a[56:59],54
s_mov_b32 s67,s65
buffer_load_dwordx4 v[58:61],v0,s[36:39],s74 offen offset:0 sc1
M 22,a[52:55],50
s_add_u32 s74,s49,s62
R 42,2,12288
v_add_f32 v17,v17,v63
M 22,a[60:63],54
L 3
v_cvt_pk_bf16_f32 v10,v10,v11
v_cvt_pk_bf16_f32 v11,v12,v13
M 22,a[68:71],26
s_add_u32 s65,s47,s62
R 46,2,13312
v_cvt_pk_bf16_f32 v12,v14,v15
M 22,a[76:79],30
s_mov_b32 s54,s61
D 0,56,2048
M 18,a[64:67],26
R 50,2,14336
v_cvt_pk_bf16_f32 v13,v16,v17
M 18,a[72:75],30
L 3
buffer_store_dwordx4 v[10:13],v0,s[36:39],s66 offen offset:0 sc0 sc1
M 18,a[80:83],34
R 54,2,15360
M 18,a[88:91],38
M 22,a[84:87],34
R 26,2,16384
M 22,a[92:95],38
L 3
M 22,a[100:103],42
R 30,2,17408
M 22,a[108:111],46
D 0,56,3072
M 18,a[96:99],42
s_add_u32 m0,s57,4096
s_add_u32 s56,s56,4096
R 34,2,18432
M 18,a[104:107],46
L 3
M 18,a[112:115],50
R 38,2,19456
M 18,a[120:123],54
M 22,a[116:119],50
R 42,2,20480
M 22,a[124:127],54
L 3
D 0,56,0
M 22,a[132:135],26
R 46,2,21504
M 22,a[140:143],30
M 18,a[128:131],26
R 50,2,22528
M 18,a[136:139],30
L 3
M 18,a[144:147],34
R 54,2,23552
M 18,a[152:155],38
M 22,a[148:151],34
R 26,2,24576
M 22,a[156:159],38
L 3
D 0,56,1024
M 22,a[164:167],42
R 30,2,25600
M 22,a[172:175],46
M 18,a[160:163],42
R 34,2,26624
M 18,a[168:171],46
s_waitcnt vmcnt(8) lgkmcnt(3)
M 18,a[176:179],50
s_barrier
R 38,2,27648
M 18,a[184:187],54
M 22,a[180:183],50
R 42,2,28672
M 22,a[188:191],54
L 3
D 0,56,2048
M 22,a[196:199],26
s_add_u32 m0,s59,1024
s_add_u32 s56,s55,28672
R 46,2,29696
M 22,a[204:207],30
s_mul_i32 s55,s61,114688
s_add_u32 s55,s55,s60
M 18,a[192:195],26
R 50,2,30720
M 18,a[200:203],30
L 3
M 18,a[208:211],34
R 54,2,31744
M 18,a[216:219],38
M 22,a[212:215],34
R 26,2,32768
M 22,a[220:223],38
L 3
buffer_load_dword v8,s[36:39],s58 offen offset:0 sc1 lds
M 22,a[228:231],42
s_add_u32 m0,s57,28672
R 30,2,33792
M 22,a[236:239],46
D 0,56,0
M 18,a[224:227],42
R 34,2,34816
M 18,a[232:235],46
L 3
M 18,a[240:243],50
R 38,2,35840
M 18,a[248:251],54
M 22,a[244:247],50
R 42,2,36864
M 22,a[252:255],54
L 3
M 22,v[68:71],26
R 46,2,37888
M 22,v[76:79],30
D 0,56,1024
M 18,v[64:67],26
R 50,2,38912
M 18,v[72:75],30
L 3
M 18,v[80:83],34
R 54,2,39936
M 18,v[88:91],38
M 22,v[84:87],34
R 26,2,40960
M 22,v[92:95],38
L 3
M 22,v[100:103],42
R 30,2,41984
M 22,v[108:111],46
D 0,56,2048
M 18,v[96:99],42
R 34,2,43008
M 18,v[104:107],46
L 3
M 18,v[112:115],50
R 38,2,44032
M 18,v[120:123],54
M 22,v[116:119],50
R 42,2,45056
M 22,v[124:127],54
L 3
D 0,56,3072
M 22,v[132:135],26
s_add_u32 m0,s57,32768
s_add_u32 s56,s56,4096
R 46,2,46080
M 22,v[140:143],30
s_sub_u32 s57,s84,s57
M 18,v[128:131],26
R 50,2,47104
M 18,v[136:139],30
L 3
M 18,v[144:147],34
R 54,2,48128
M 18,v[152:155],38
M 22,v[148:151],34
R 26,2,49152
M 22,v[156:159],38
)"
R"(L 3
D 0,56,0
M 22,v[164:167],42
R 30,2,50176
M 22,v[172:175],46
M 18,v[160:163],42
R 34,2,51200
M 18,v[168:171],46
L 3
M 18,v[176:179],50
R 38,2,52224
M 18,v[184:187],54
M 22,v[180:183],50
R 42,2,53248
M 22,v[188:191],54
L 3
D 0,56,1024
M 22,v[196:199],26
R 46,2,54272
M 22,v[204:207],30
M 18,v[192:195],26
R 50,2,55296
M 18,v[200:203],30
s_waitcnt vmcnt(6) lgkmcnt(3)
M 18,v[208:211],34
s_barrier
R 54,2,56320
ds_read_b32 v7,v6 offset:1024
M 18,v[216:219],38
buffer_store_dword v4,v3,s[36:39],s69 offen offset:0 sc0 sc1
M 22,v[212:215],34
s_mov_b32 s69,s68
s_lshr_b32 s68,s62,4
R 26,1,0
M 22,v[220:223],38
L 4
D 0,56,2048
M 22,v[228:231],42
s_add_u32 s68,s68,s48
R 30,1,1024
M 22,v[236:239],46
M 18,v[224:227],42
L 2
R 34,1,2048
v_cmp_eq_u32 vcc,s53,v7
M 18,v[232:235],46
s_nop 0
s_cmp_eq_u32 vcc_lo,-1
M 18,v[240:243],50
R 38,1,3072
M 18,v[248:251],54
M 22,v[244:247],50
R 42,1,4096
M 22,v[252:255],54
L 3
s_cbranch_scc1 Lg4_F0w1_%=
s_branch Lg4_L0_%=
Lg4_F0w2_%=:
v_mfma_f32_16x16x32_bf16 v[14:17],a[4:7],v[26:29],0
s_add_u32 m0,s57,0
R 46,1,5120
v_lshlrev_b32 v62,16,v58
M 14,a[12:15],30
s_add_u32 s56,s55,0
v_add_f32 v18,v18,v62
v_and_b32 v63,0xffff0000,v58
v_mfma_f32_16x16x32_bf16 v[10:13],a[0:3],v[26:29],0
s_add_u32 s61,s54,s46
R 50,1,6144
v_add_f32 v19,v19,v63
M 10,a[8:11],30
L 3
D 0,56,0
M 10,a[16:19],34
s_cmp_ge_u32 s61,s63
R 54,1,7168
v_lshlrev_b32 v62,16,v59
M 10,a[24:27],38
s_cselect_b32 s61,s93,s61
v_add_f32 v20,v20,v62
v_and_b32 v63,0xffff0000,v59
M 14,a[20:23],34
s_cselect_b32 s46,7,s46
R 26,1,8192
v_add_f32 v21,v21,v63
M 14,a[28:31],38
L 3
v_lshlrev_b32 v62,16,v60
v_add_f32 v22,v22,v62
M 14,a[36:39],42
s_cselect_b32 s63,-1,s63
R 30,1,9216
v_and_b32 v63,0xffff0000,v60
M 14,a[44:47],46
s_mul_i32 s58,s61,2560
D 0,56,1024
M 10,a[32:35],42
s_add_u32 s58,s58,109891584
R 34,1,10240
v_add_f32 v23,v23,v63
M 10,a[40:43],46
L 3
v_lshlrev_b32 v62,16,v61
v_add_f32 v24,v24,v62
M 10,a[48:51],50
s_lshl_b32 s62,s54,15
R 38,1,11264
v_and_b32 v63,0xffff0000,v61
M 10,a[56:59],54
s_mov_b32 s66,s65
buffer_load_dwordx4 v[58:61],v0,s[36:39],s74 offen offset:0 sc1
M 14,a[52:55],50
s_add_u32 s74,s49,s62
R 42,1,12288
v_add_f32 v25,v25,v63
M 14,a[60:63],54
L 3
v_cvt_pk_bf16_f32 v18,v18,v19
v_cvt_pk_bf16_f32 v19,v20,v21
M 14,a[68:71],26
s_add_u32 s65,s47,s62
R 46,1,13312
v_cvt_pk_bf16_f32 v20,v22,v23
M 14,a[76:79],30
s_mov_b32 s54,s61
D 0,56,2048
M 10,a[64:67],26
R 50,1,14336
v_cvt_pk_bf16_f32 v21,v24,v25
M 10,a[72:75],30
L 3
buffer_store_dwordx4 v[18:21],v0,s[36:39],s67 offen offset:0 sc0 sc1
M 10,a[80:83],34
R 54,1,15360
M 10,a[88:91],38
M 14,a[84:87],34
R 26,1,16384
M 14,a[92:95],38
L 3
M 14,a[100:103],42
R 30,1,17408
M 14,a[108:111],46
D 0,56,3072
M 10,a[96:99],42
s_add_u32 m0,s57,4096
s_add_u32 s56,s56,4096
R 34,1,18432
M 10,a[104:107],46
L 3
M 10,a[112:115],50
R 38,1,19456
M 10,a[120:123],54
M 14,a[116:119],50
R 42,1,20480
M 14,a[124:127],54
L 3
D 0,56,0
M 14,a[132:135],26
R 46,1,21504
M 14,a[140:143],30
M 10,a[128:131],26
R 50,1,22528
M 10,a[136:139],30
L 3
M 10,a[144:147],34
R 54,1,23552
M 10,a[152:155],38
M 14,a[148:151],34
R 26,1,24576
M 14,a[156:159],38
L 3
D 0,56,1024
M 14,a[164:167],42
R 30,1,25600
M 14,a[172:175],46
M 10,a[160:163],42
s_waitcnt vmcnt(8)
R 34,1,26624
M 10,a[168:171],46
s_barrier
L 3
M 10,a[176:179],50
R 38,1,27648
M 10,a[184:187],54
M 14,a[180:183],50
R 42,1,28672
M 14,a[188:191],54
L 3
D 0,56,2048
M 14,a[196:199],26
s_add_u32 m0,s59,0
s_add_u32 s56,s55,28672
R 46,1,29696
M 14,a[204:207],30
s_mul_i32 s55,s61,114688
s_add_u32 s55,s55,s60
M 10,a[192:195],26
R 50,1,30720
M 10,a[200:203],30
L 3
M 10,a[208:211],34
R 54,1,31744
M 10,a[216:219],38
M 14,a[212:215],34
R 26,1,32768
M 14,a[220:223],38
L 3
buffer_load_dword v8,s[36:39],s58 offen offset:0 sc1 lds
M 14,a[228:231],42
s_add_u32 m0,s57,28672
R 30,1,33792
M 14,a[236:239],46
D 0,56,0
M 10,a[224:227],42
R 34,1,34816
M 10,a[232:235],46
L 3
M 10,a[240:243],50
R 38,1,35840
M 10,a[248:251],54
M 14,a[244:247],50
R 42,1,36864
M 14,a[252:255],54
L 3
M 14,v[68:71],26
R 46,1,37888
M 14,v[76:79],30
D 0,56,1024
M 10,v[64:67],26
R 50,1,38912
M 10,v[72:75],30
L 3
M 10,v[80:83],34
R 54,1,39936
M 10,v[88:91],38
M 14,v[84:87],34
R 26,1,40960
M 14,v[92:95],38
L 3
M 14,v[100:103],42
R 30,1,41984
M 14,v[108:111],46
D 0,56,2048
M 10,v[96:99],42
R 34,1,43008
M 10,v[104:107],46
L 3
M 10,v[112:115],50
R 38,1,44032
M 10,v[120:123],54
M 14,v[116:119],50
R 42,1,45056
M 14,v[124:127],54
L 3
D 0,56,3072
M 14,v[132:135],26
s_add_u32 m0,s57,32768
s_add_u32 s56,s56,4096
R 46,1,46080
M 14,v[140:143],30
s_sub_u32 s57,s84,s57
M 10,v[128:131],26
R 50,1,47104
M 10,v[136:139],30
L 3
M 10,v[144:147],34
R 54,1,48128
M 10,v[152:155],38
M 14,v[148:151],34
R 26,1,49152
M 14,v[156:159],38
L 3
D 0,56,0
M 14,v[164:167],42
R 30,1,50176
M 14,v[172:175],46
M 10,v[160:163],42
R 34,1,51200
M 10,v[168:171],46
L 3
M 10,v[176:179],50
R 38,1,52224
M 10,v[184:187],54
M 14,v[180:183],50
R 42,1,53248
M 14,v[188:191],54
L 3
D 0,56,1024
M 14,v[196:199],26
R 46,1,54272
M 14,v[204:207],30
M 10,v[192:195],26
s_waitcnt vmcnt(6)
R 50,1,55296
M 10,v[200:203],30
L 3
s_barrier
buffer_store_dword v4,v3,s[36:39],s69 offen offset:0 sc0 sc1
M 10,v[208:211],34
s_mov_b32 s69,s68
R 54,1,56320
ds_read_b32 v7,v6 offset:0
M 10,v[216:219],38
s_lshr_b32 s68,s62,4
s_add_u32 s68,s68,s48
M 14,v[212:215],34
R 26,2,0
M 14,v[220:223],38
L 4
D 0,56,2048
M 14,v[228:231],42
R 30,2,1024
M 14,v[236:239],46
M 10,v[224:227],42
L 2
R 34,2,2048
v_cmp_eq_u32 vcc,s53,v7
M 10,v[232:235],46
s_nop 0
s_cmp_eq_u32 vcc_lo,-1
M 10,v[240:243],50
R 38,2,3072
M 10,v[248:251],54
M 14,v[244:247],50
R 42,2,4096
M 14,v[252:255],54
L 3
s_cbranch_scc0 Lg4_L1_%=
Lg4_F1w2_%=:
v_mfma_f32_16x16x32_bf16 v[22:25],a[4:7],v[26:29],0
s_add_u32 m0,s57,0
R 46,2,5120
v_lshlrev_b32 v62,16,v58
M 22,a[12:15],30
s_add_u32 s56,s55,0
v_add_f32 v10,v10,v62
v_and_b32 v63,0xffff0000,v58
v_mfma_f32_16x16x32_bf16 v[18:21],a[0:3],v[26:29],0
s_add_u32 s61,s54,s46
R 50,2,6144
v_add_f32 v11,v11,v63
M 18,a[8:11],30
L 3
D 0,56,0
M 18,a[16:19],34
s_cmp_ge_u32 s61,s63
R 54,2,7168
v_lshlrev_b32 v62,16,v59
M 18,a[24:27],38
s_cselect_b32 s61,s93,s61
v_add_f32 v12,v12,v62
v_and_b32 v63,0xffff0000,v59
M 22,a[20:23],34
s_cselect_b32 s46,7,s46
R 26,2,8192
v_add_f32 v13,v13,v63
M 22,a[28:31],38
L 3
v_lshlrev_b32 v62,16,v60
v_add_f32 v14,v14,v62
M 22,a[36:39],42
s_cselect_b32 s63,-1,s63
R 30,2,9216
v_and_b32 v63,0xffff0000,v60
M 22,a[44:47],46
s_mul_i32 s58,s61,2560
D 0,56,1024
M 18,a[32:35],42
s_add_u32 s58,s58,109891584
R 34,2,10240
v_add_f32 v15,v15,v63
M 18,a[40:43],46
L 3
v_lshlrev_b32 v62,16,v61
v_add_f32 v16,v16,v62
M 18,a[48:51],50
s_lshl_b32 s62,s54,15
R 38,2,11264
v_and_b32 v63,0xffff0000,v61
M 18,a[56:59],54
s_mov_b32 s67,s65
buffer_load_dwordx4 v[58:61],v0,s[36:39],s74 offen offset:0 sc1
M 22,a[52:55],50
s_add_u32 s74,s49,s62
R 42,2,12288
v_add_f32 v17,v17,v63
M 22,a[60:63],54
L 3
v_cvt_pk_bf16_f32 v10,v10,v11
v_cvt_pk_bf16_f32 v11,v12,v13
M 22,a[68:71],26
s_add_u32 s65,s47,s62
R 46,2,13312
v_cvt_pk_bf16_f32 v12,v14,v15
M 22,a[76:79],30
s_mov_b32 s54,s61
D 0,56,2048
M 18,a[64:67],26
R 50,2,14336
v_cvt_pk_bf16_f32 v13,v16,v17
M 18,a[72:75],30
L 3
buffer_store_dwordx4 v[10:13],v0,s[36:39],s66 offen offset:0 sc0 sc1
M 18,a[80:83],34
R 54,2,15360
M 18,a[88:91],38
M 22,a[84:87],34
R 26,2,16384
M 22,a[92:95],38
L 3
M 22,a[100:103],42
R 30,2,17408
M 22,a[108:111],46
D 0,56,3072
M 18,a[96:99],42
s_add_u32 m0,s57,4096
s_add_u32 s56,s56,4096
R 34,2,18432
M 18,a[104:107],46
L 3
M 18,a[112:115],50
R 38,2,19456
M 18,a[120:123],54
M 22,a[116:119],50
R 42,2,20480
M 22,a[124:127],54
L 3
D 0,56,0
M 22,a[132:135],26
R 46,2,21504
M 22,a[140:143],30
M 18,a[128:131],26
R 50,2,22528
M 18,a[136:139],30
L 3
M 18,a[144:147],34
R 54,2,23552
M 18,a[152:155],38
M 22,a[148:151],34
R 26,2,24576
M 22,a[156:159],38
L 3
D 0,56,1024
M 22,a[164:167],42
R 30,2,25600
M 22,a[172:175],46
M 18,a[160:163],42
s_waitcnt vmcnt(8)
R 34,2,26624
M 18,a[168:171],46
s_barrier
L 3
M 18,a[176:179],50
R 38,2,27648
M 18,a[184:187],54
M 22,a[180:183],50
R 42,2,28672
M 22,a[188:191],54
L 3
D 0,56,2048
M 22,a[196:199],26
s_add_u32 m0,s59,1024
s_add_u32 s56,s55,28672
R 46,2,29696
M 22,a[204:207],30
s_mul_i32 s55,s61,114688
s_add_u32 s55,s55,s60
M 18,a[192:195],26
R 50,2,30720
M 18,a[200:203],30
L 3
M 18,a[208:211],34
R 54,2,31744
M 18,a[216:219],38
M 22,a[212:215],34
R 26,2,32768
M 22,a[220:223],38
L 3
buffer_load_dword v8,s[36:39],s58 offen offset:0 sc1 lds
M 22,a[228:231],42
s_add_u32 m0,s57,28672
R 30,2,33792
M 22,a[236:239],46
D 0,56,0
M 18,a[224:227],42
R 34,2,34816
M 18,a[232:235],46
L 3
M 18,a[240:243],50
R 38,2,35840
M 18,a[248:251],54
M 22,a[244:247],50
R 42,2,36864
M 22,a[252:255],54
L 3
M 22,v[68:71],26
R 46,2,37888
M 22,v[76:79],30
D 0,56,1024
M 18,v[64:67],26
)"
R"(R 50,2,38912
M 18,v[72:75],30
L 3
M 18,v[80:83],34
R 54,2,39936
M 18,v[88:91],38
M 22,v[84:87],34
R 26,2,40960
M 22,v[92:95],38
L 3
M 22,v[100:103],42
R 30,2,41984
M 22,v[108:111],46
D 0,56,2048
M 18,v[96:99],42
R 34,2,43008
M 18,v[104:107],46
L 3
M 18,v[112:115],50
R 38,2,44032
M 18,v[120:123],54
M 22,v[116:119],50
R 42,2,45056
M 22,v[124:127],54
L 3
D 0,56,3072
M 22,v[132:135],26
s_add_u32 m0,s57,32768
s_add_u32 s56,s56,4096
R 46,2,46080
M 22,v[140:143],30
s_sub_u32 s57,s84,s57
M 18,v[128:131],26
R 50,2,47104
M 18,v[136:139],30
L 3
M 18,v[144:147],34
R 54,2,48128
M 18,v[152:155],38
M 22,v[148:151],34
R 26,2,49152
M 22,v[156:159],38
L 3
D 0,56,0
M 22,v[164:167],42
R 30,2,50176
M 22,v[172:175],46
M 18,v[160:163],42
R 34,2,51200
M 18,v[168:171],46
L 3
M 18,v[176:179],50
R 38,2,52224
M 18,v[184:187],54
M 22,v[180:183],50
R 42,2,53248
M 22,v[188:191],54
L 3
D 0,56,1024
M 22,v[196:199],26
R 46,2,54272
M 22,v[204:207],30
M 18,v[192:195],26
s_waitcnt vmcnt(6)
R 50,2,55296
M 18,v[200:203],30
L 3
s_barrier
buffer_store_dword v4,v3,s[36:39],s69 offen offset:0 sc0 sc1
M 18,v[208:211],34
s_mov_b32 s69,s68
R 54,2,56320
ds_read_b32 v7,v6 offset:1024
M 18,v[216:219],38
s_lshr_b32 s68,s62,4
s_add_u32 s68,s68,s48
M 22,v[212:215],34
R 26,1,0
M 22,v[220:223],38
L 4
D 0,56,2048
M 22,v[228:231],42
R 30,1,1024
M 22,v[236:239],46
M 18,v[224:227],42
L 2
R 34,1,2048
v_cmp_eq_u32 vcc,s53,v7
M 18,v[232:235],46
s_nop 0
s_cmp_eq_u32 vcc_lo,-1
M 18,v[240:243],50
R 38,1,3072
M 18,v[248:251],54
M 22,v[244:247],50
R 42,1,4096
M 22,v[252:255],54
L 3
s_cbranch_scc1 Lg4_F0w2_%=
s_branch Lg4_L0_%=
Lg4_F0w3_%=:
v_mfma_f32_16x16x32_bf16 v[14:17],a[4:7],v[26:29],0
s_add_u32 m0,s57,0
R 46,1,5120
v_lshlrev_b32 v62,16,v58
M 14,a[12:15],30
s_add_u32 s56,s55,0
v_add_f32 v18,v18,v62
v_and_b32 v63,0xffff0000,v58
v_mfma_f32_16x16x32_bf16 v[10:13],a[0:3],v[26:29],0
s_add_u32 s61,s54,s46
R 50,1,6144
v_add_f32 v19,v19,v63
M 10,a[8:11],30
L 3
D 0,56,0
M 10,a[16:19],34
s_cmp_ge_u32 s61,s63
R 54,1,7168
v_lshlrev_b32 v62,16,v59
M 10,a[24:27],38
s_cselect_b32 s61,s93,s61
v_add_f32 v20,v20,v62
v_and_b32 v63,0xffff0000,v59
M 14,a[20:23],34
s_cselect_b32 s46,7,s46
R 26,1,8192
v_add_f32 v21,v21,v63
M 14,a[28:31],38
L 3
v_lshlrev_b32 v62,16,v60
v_add_f32 v22,v22,v62
M 14,a[36:39],42
s_cselect_b32 s63,-1,s63
R 30,1,9216
v_and_b32 v63,0xffff0000,v60
M 14,a[44:47],46
s_mul_i32 s58,s61,2560
D 0,56,1024
M 10,a[32:35],42
s_add_u32 s58,s58,109891584
R 34,1,10240
v_add_f32 v23,v23,v63
M 10,a[40:43],46
L 3
v_lshlrev_b32 v62,16,v61
v_add_f32 v24,v24,v62
M 10,a[48:51],50
s_lshl_b32 s62,s54,15
R 38,1,11264
v_and_b32 v63,0xffff0000,v61
M 10,a[56:59],54
s_mov_b32 s66,s65
buffer_load_dwordx4 v[58:61],v0,s[36:39],s74 offen offset:0 sc1
M 14,a[52:55],50
s_add_u32 s74,s49,s62
R 42,1,12288
v_add_f32 v25,v25,v63
M 14,a[60:63],54
L 3
v_cvt_pk_bf16_f32 v18,v18,v19
v_cvt_pk_bf16_f32 v19,v20,v21
M 14,a[68:71],26
s_add_u32 s65,s47,s62
R 46,1,13312
v_cvt_pk_bf16_f32 v20,v22,v23
M 14,a[76:79],30
s_mov_b32 s54,s61
D 0,56,2048
M 10,a[64:67],26
R 50,1,14336
v_cvt_pk_bf16_f32 v21,v24,v25
M 10,a[72:75],30
L 3
buffer_store_dwordx4 v[18:21],v0,s[36:39],s67 offen offset:0 sc0 sc1
M 10,a[80:83],34
R 54,1,15360
M 10,a[88:91],38
M 14,a[84:87],34
R 26,1,16384
M 14,a[92:95],38
L 3
M 14,a[100:103],42
R 30,1,17408
M 14,a[108:111],46
D 0,56,3072
M 10,a[96:99],42
s_add_u32 m0,s57,4096
s_add_u32 s56,s56,4096
R 34,1,18432
M 10,a[104:107],46
L 3
M 10,a[112:115],50
R 38,1,19456
M 10,a[120:123],54
M 14,a[116:119],50
R 42,1,20480
M 14,a[124:127],54
L 3
D 0,56,0
M 14,a[132:135],26
R 46,1,21504
M 14,a[140:143],30
M 10,a[128:131],26
R 50,1,22528
M 10,a[136:139],30
L 3
M 10,a[144:147],34
R 54,1,23552
M 10,a[152:155],38
M 14,a[148:151],34
R 26,1,24576
M 14,a[156:159],38
L 3
D 0,56,1024
M 14,a[164:167],42
R 30,1,25600
M 14,a[172:175],46
s_waitcnt vmcnt(8)
M 10,a[160:163],42
s_barrier
R 34,1,26624
M 10,a[168:171],46
L 3
M 10,a[176:179],50
R 38,1,27648
M 10,a[184:187],54
M 14,a[180:183],50
R 42,1,28672
M 14,a[188:191],54
L 3
D 0,56,2048
M 14,a[196:199],26
s_add_u32 m0,s59,0
s_add_u32 s56,s55,28672
R 46,1,29696
M 14,a[204:207],30
s_mul_i32 s55,s61,114688
s_add_u32 s55,s55,s60
M 10,a[192:195],26
R 50,1,30720
M 10,a[200:203],30
L 3
M 10,a[208:211],34
R 54,1,31744
M 10,a[216:219],38
M 14,a[212:215],34
R 26,1,32768
M 14,a[220:223],38
L 3
buffer_load_dword v8,s[36:39],s58 offen offset:0 sc1 lds
M 14,a[228:231],42
s_add_u32 m0,s57,28672
R 30,1,33792
M 14,a[236:239],46
D 0,56,0
M 10,a[224:227],42
R 34,1,34816
M 10,a[232:235],46
L 3
M 10,a[240:243],50
R 38,1,35840
M 10,a[248:251],54
M 14,a[244:247],50
R 42,1,36864
M 14,a[252:255],54
L 3
M 14,v[68:71],26
R 46,1,37888
M 14,v[76:79],30
D 0,56,1024
M 10,v[64:67],26
R 50,1,38912
M 10,v[72:75],30
L 3
M 10,v[80:83],34
R 54,1,39936
M 10,v[88:91],38
M 14,v[84:87],34
R 26,1,40960
M 14,v[92:95],38
L 3
M 14,v[100:103],42
R 30,1,41984
M 14,v[108:111],46
D 0,56,2048
M 10,v[96:99],42
R 34,1,43008
M 10,v[104:107],46
L 3
M 10,v[112:115],50
R 38,1,44032
M 10,v[120:123],54
M 14,v[116:119],50
R 42,1,45056
M 14,v[124:127],54
L 3
D 0,56,3072
M 14,v[132:135],26
s_add_u32 m0,s57,32768
s_add_u32 s56,s56,4096
R 46,1,46080
M 14,v[140:143],30
s_sub_u32 s57,s84,s57
M 10,v[128:131],26
R 50,1,47104
M 10,v[136:139],30
L 3
M 10,v[144:147],34
R 54,1,48128
M 10,v[152:155],38
M 14,v[148:151],34
R 26,1,49152
M 14,v[156:159],38
L 3
D 0,56,0
M 14,v[164:167],42
R 30,1,50176
M 14,v[172:175],46
M 10,v[160:163],42
R 34,1,51200
M 10,v[168:171],46
L 3
M 10,v[176:179],50
R 38,1,52224
M 10,v[184:187],54
M 14,v[180:183],50
R 42,1,53248
M 14,v[188:191],54
L 3
D 0,56,1024
M 14,v[196:199],26
R 46,1,54272
M 14,v[204:207],30
s_waitcnt vmcnt(6)
M 10,v[192:195],26
s_barrier
R 50,1,55296
ds_read_b32 v7,v6 offset:0
M 10,v[200:203],30
L 4
buffer_store_dword v4,v3,s[36:39],s69 offen offset:0 sc0 sc1
M 10,v[208:211],34
s_mov_b32 s69,s68
s_lshr_b32 s68,s62,4
R 54,1,56320
M 10,v[216:219],38
s_add_u32 s68,s68,s48
M 14,v[212:215],34
R 26,2,0
M 14,v[220:223],38
L 4
D 0,56,2048
M 14,v[228:231],42
L 2
R 30,2,1024
v_cmp_eq_u32 vcc,s53,v7
M 14,v[236:239],46
s_cmp_eq_u32 vcc_lo,-1
M 10,v[224:227],42
R 34,2,2048
M 10,v[232:235],46
L 3
M 10,v[240:243],50
R 38,2,3072
M 10,v[248:251],54
M 14,v[244:247],50
R 42,2,4096
M 14,v[252:255],54
L 3
s_cbranch_scc0 Lg4_L1_%=
Lg4_F1w3_%=:
v_mfma_f32_16x16x32_bf16 v[22:25],a[4:7],v[26:29],0
s_add_u32 m0,s57,0
R 46,2,5120
v_lshlrev_b32 v62,16,v58
M 22,a[12:15],30
s_add_u32 s56,s55,0
v_add_f32 v10,v10,v62
v_and_b32 v63,0xffff0000,v58
v_mfma_f32_16x16x32_bf16 v[18:21],a[0:3],v[26:29],0
s_add_u32 s61,s54,s46
R 50,2,6144
v_add_f32 v11,v11,v63
M 18,a[8:11],30
L 3
D 0,56,0
M 18,a[16:19],34
s_cmp_ge_u32 s61,s63
R 54,2,7168
v_lshlrev_b32 v62,16,v59
M 18,a[24:27],38
s_cselect_b32 s61,s93,s61
v_add_f32 v12,v12,v62
v_and_b32 v63,0xffff0000,v59
M 22,a[20:23],34
s_cselect_b32 s46,7,s46
R 26,2,8192
v_add_f32 v13,v13,v63
M 22,a[28:31],38
L 3
v_lshlrev_b32 v62,16,v60
v_add_f32 v14,v14,v62
M 22,a[36:39],42
s_cselect_b32 s63,-1,s63
R 30,2,9216
v_and_b32 v63,0xffff0000,v60
M 22,a[44:47],46
s_mul_i32 s58,s61,2560
D 0,56,1024
M 18,a[32:35],42
s_add_u32 s58,s58,109891584
R 34,2,10240
v_add_f32 v15,v15,v63
M 18,a[40:43],46
L 3
v_lshlrev_b32 v62,16,v61
v_add_f32 v16,v16,v62
M 18,a[48:51],50
s_lshl_b32 s62,s54,15
R 38,2,11264
v_and_b32 v63,0xffff0000,v61
M 18,a[56:59],54
s_mov_b32 s67,s65
buffer_load_dwordx4 v[58:61],v0,s[36:39],s74 offen offset:0 sc1
M 22,a[52:55],50
s_add_u32 s74,s49,s62
R 42,2,12288
v_add_f32 v17,v17,v63
M 22,a[60:63],54
L 3
v_cvt_pk_bf16_f32 v10,v10,v11
v_cvt_pk_bf16_f32 v11,v12,v13
M 22,a[68:71],26
s_add_u32 s65,s47,s62
R 46,2,13312
v_cvt_pk_bf16_f32 v12,v14,v15
M 22,a[76:79],30
s_mov_b32 s54,s61
D 0,56,2048
M 18,a[64:67],26
R 50,2,14336
v_cvt_pk_bf16_f32 v13,v16,v17
M 18,a[72:75],30
L 3
buffer_store_dwordx4 v[10:13],v0,s[36:39],s66 offen offset:0 sc0 sc1
M 18,a[80:83],34
R 54,2,15360
M 18,a[88:91],38
M 22,a[84:87],34
R 26,2,16384
M 22,a[92:95],38
L 3
M 22,a[100:103],42
R 30,2,17408
M 22,a[108:111],46
D 0,56,3072
M 18,a[96:99],42
s_add_u32 m0,s57,4096
s_add_u32 s56,s56,4096
R 34,2,18432
M 18,a[104:107],46
L 3
M 18,a[112:115],50
R 38,2,19456
M 18,a[120:123],54
M 22,a[116:119],50
R 42,2,20480
M 22,a[124:127],54
L 3
D 0,56,0
M 22,a[132:135],26
R 46,2,21504
M 22,a[140:143],30
M 18,a[128:131],26
R 50,2,22528
M 18,a[136:139],30
L 3
M 18,a[144:147],34
R 54,2,23552
M 18,a[152:155],38
M 22,a[148:151],34
R 26,2,24576
M 22,a[156:159],38
L 3
D 0,56,1024
M 22,a[164:167],42
R 30,2,25600
M 22,a[172:175],46
s_waitcnt vmcnt(8)
M 18,a[160:163],42
s_barrier
R 34,2,26624
M 18,a[168:171],46
L 3
M 18,a[176:179],50
R 38,2,27648
M 18,a[184:187],54
M 22,a[180:183],50
R 42,2,28672
)"
R"(M 22,a[188:191],54
L 3
D 0,56,2048
M 22,a[196:199],26
s_add_u32 m0,s59,1024
s_add_u32 s56,s55,28672
R 46,2,29696
M 22,a[204:207],30
s_mul_i32 s55,s61,114688
s_add_u32 s55,s55,s60
M 18,a[192:195],26
R 50,2,30720
M 18,a[200:203],30
L 3
M 18,a[208:211],34
R 54,2,31744
M 18,a[216:219],38
M 22,a[212:215],34
R 26,2,32768
M 22,a[220:223],38
L 3
buffer_load_dword v8,s[36:39],s58 offen offset:0 sc1 lds
M 22,a[228:231],42
s_add_u32 m0,s57,28672
R 30,2,33792
M 22,a[236:239],46
D 0,56,0
M 18,a[224:227],42
R 34,2,34816
M 18,a[232:235],46
L 3
M 18,a[240:243],50
R 38,2,35840
M 18,a[248:251],54
M 22,a[244:247],50
R 42,2,36864
M 22,a[252:255],54
L 3
M 22,v[68:71],26
R 46,2,37888
M 22,v[76:79],30
D 0,56,1024
M 18,v[64:67],26
R 50,2,38912
M 18,v[72:75],30
L 3
M 18,v[80:83],34
R 54,2,39936
M 18,v[88:91],38
M 22,v[84:87],34
R 26,2,40960
M 22,v[92:95],38
L 3
M 22,v[100:103],42
R 30,2,41984
M 22,v[108:111],46
D 0,56,2048
M 18,v[96:99],42
R 34,2,43008
M 18,v[104:107],46
L 3
M 18,v[112:115],50
R 38,2,44032
M 18,v[120:123],54
M 22,v[116:119],50
R 42,2,45056
M 22,v[124:127],54
L 3
D 0,56,3072
M 22,v[132:135],26
s_add_u32 m0,s57,32768
s_add_u32 s56,s56,4096
R 46,2,46080
M 22,v[140:143],30
s_sub_u32 s57,s84,s57
M 18,v[128:131],26
R 50,2,47104
M 18,v[136:139],30
L 3
M 18,v[144:147],34
R 54,2,48128
M 18,v[152:155],38
M 22,v[148:151],34
R 26,2,49152
M 22,v[156:159],38
L 3
D 0,56,0
M 22,v[164:167],42
R 30,2,50176
M 22,v[172:175],46
M 18,v[160:163],42
R 34,2,51200
M 18,v[168:171],46
L 3
M 18,v[176:179],50
R 38,2,52224
M 18,v[184:187],54
M 22,v[180:183],50
R 42,2,53248
M 22,v[188:191],54
L 3
D 0,56,1024
M 22,v[196:199],26
R 46,2,54272
M 22,v[204:207],30
s_waitcnt vmcnt(6)
M 18,v[192:195],26
s_barrier
R 50,2,55296
ds_read_b32 v7,v6 offset:1024
M 18,v[200:203],30
L 4
buffer_store_dword v4,v3,s[36:39],s69 offen offset:0 sc0 sc1
M 18,v[208:211],34
s_mov_b32 s69,s68
s_lshr_b32 s68,s62,4
R 54,2,56320
M 18,v[216:219],38
s_add_u32 s68,s68,s48
M 22,v[212:215],34
R 26,1,0
M 22,v[220:223],38
L 4
D 0,56,2048
M 22,v[228:231],42
L 2
R 30,1,1024
v_cmp_eq_u32 vcc,s53,v7
M 22,v[236:239],46
s_cmp_eq_u32 vcc_lo,-1
M 18,v[224:227],42
R 34,1,2048
M 18,v[232:235],46
L 3
M 18,v[240:243],50
R 38,1,3072
M 18,v[248:251],54
M 22,v[244:247],50
R 42,1,4096
M 22,v[252:255],54
L 3
s_cbranch_scc1 Lg4_F0w3_%=
Lg4_L0_%=:
s_cmp_ge_u32 s54,256
s_cbranch_scc1 Lg4_Z0_%=
v_mfma_f32_16x16x32_bf16 v[14:17],a[4:7],v[26:29],0
s_add_u32 m0,s59,3072
R 46,1,5120
v_lshlrev_b32 v62,16,v58
M 14,a[12:15],30
s_lshl_b32 s62,s54,15
buffer_load_dword v8,s[36:39],s58 offen offset:0 sc1 lds
v_mfma_f32_16x16x32_bf16 v[10:13],a[0:3],v[26:29],0
s_mov_b32 s66,s65
R 50,1,6144
v_add_f32 v18,v18,v62
M 10,a[8:11],30
L 3
v_and_b32 v63,0xffff0000,v58
v_add_f32 v19,v19,v63
M 10,a[16:19],34
s_add_u32 s65,s47,s62
R 54,1,7168
v_lshlrev_b32 v62,16,v59
M 10,a[24:27],38
v_add_f32 v20,v20,v62
v_and_b32 v63,0xffff0000,v59
M 14,a[20:23],34
R 26,1,8192
v_add_f32 v21,v21,v63
M 14,a[28:31],38
L 3
v_lshlrev_b32 v62,16,v60
v_add_f32 v22,v22,v62
M 14,a[36:39],42
R 30,1,9216
v_and_b32 v63,0xffff0000,v60
M 14,a[44:47],46
v_add_f32 v23,v23,v63
v_lshlrev_b32 v62,16,v61
M 10,a[32:35],42
R 34,1,10240
v_add_f32 v24,v24,v62
M 10,a[40:43],46
L 3
v_and_b32 v63,0xffff0000,v61
v_add_f32 v25,v25,v63
M 10,a[48:51],50
R 38,1,11264
v_cvt_pk_bf16_f32 v18,v18,v19
M 10,a[56:59],54
buffer_load_dwordx4 v[58:61],v0,s[36:39],s74 offen offset:0 sc1
M 14,a[52:55],50
s_add_u32 s74,s49,s62
R 42,1,12288
v_cvt_pk_bf16_f32 v19,v20,v21
M 14,a[60:63],54
L 3
v_cvt_pk_bf16_f32 v20,v22,v23
v_cvt_pk_bf16_f32 v21,v24,v25
M 14,a[68:71],26
R 46,1,13312
M 14,a[76:79],30
buffer_store_dwordx4 v[18:21],v0,s[36:39],s67 offen offset:0 sc0 sc1
M 10,a[64:67],26
R 50,1,14336
M 10,a[72:75],30
L 3
M 10,a[80:83],34
R 54,1,15360
M 10,a[88:91],38
M 14,a[84:87],34
R 26,1,16384
M 14,a[92:95],38
L 3
M 14,a[100:103],42
R 30,1,17408
M 14,a[108:111],46
M 10,a[96:99],42
R 34,1,18432
M 10,a[104:107],46
L 3
M 10,a[112:115],50
R 38,1,19456
M 10,a[120:123],54
M 14,a[116:119],50
R 42,1,20480
M 14,a[124:127],54
L 3
M 14,a[132:135],26
R 46,1,21504
M 14,a[140:143],30
M 10,a[128:131],26
R 50,1,22528
M 10,a[136:139],30
L 3
M 10,a[144:147],34
R 54,1,23552
M 10,a[152:155],38
M 14,a[148:151],34
R 26,1,24576
M 14,a[156:159],38
L 3
M 14,a[164:167],42
R 30,1,25600
M 14,a[172:175],46
M 10,a[160:163],42
R 34,1,26624
M 10,a[168:171],46
L 3
M 10,a[176:179],50
s_waitcnt vmcnt(2)
R 38,1,27648
M 10,a[184:187],54
s_barrier
ds_read_b32 v7,v6 offset:3072
M 14,a[180:183],50
R 42,1,28672
M 14,a[188:191],54
L 4
M 14,a[196:199],26
R 46,1,29696
M 14,a[204:207],30
M 10,a[192:195],26
R 50,1,30720
M 10,a[200:203],30
L 4
L 3
v_cmp_eq_u32 vcc,s53,v7
M 10,a[208:211],34
R 54,1,31744
M 10,a[216:219],38
s_cmp_eq_u32 vcc_lo,-1
s_cbranch_scc0 Lg4_X0_%=
M 14,a[212:215],34
s_add_u32 m0,s57,0
s_add_u32 s56,s55,0
s_add_u32 s61,s54,s46
R 26,1,32768
M 14,a[220:223],38
L 3
s_cmp_ge_u32 s61,s63
s_cselect_b32 s61,s93,s61
M 14,a[228:231],42
s_cselect_b32 s46,7,s46
s_mul_i32 s58,s61,2560
R 30,1,33792
M 14,a[236:239],46
s_cselect_b32 s63,-1,s63
s_add_u32 s58,s58,109891584
D 0,56,0
M 10,a[224:227],42
s_mov_b32 s54,s61
R 34,1,34816
M 10,a[232:235],46
L 3
M 10,a[240:243],50
R 38,1,35840
M 10,a[248:251],54
D 0,56,1024
M 14,a[244:247],50
R 42,1,36864
M 14,a[252:255],54
L 3
M 14,v[68:71],26
R 46,1,37888
M 14,v[76:79],30
D 0,56,2048
M 10,v[64:67],26
R 50,1,38912
M 10,v[72:75],30
L 3
M 10,v[80:83],34
R 54,1,39936
M 10,v[88:91],38
D 0,56,3072
M 14,v[84:87],34
s_add_u32 m0,s57,4096
s_add_u32 s56,s56,4096
R 26,1,40960
M 14,v[92:95],38
L 3
M 14,v[100:103],42
R 30,1,41984
M 14,v[108:111],46
D 0,56,0
M 10,v[96:99],42
R 34,1,43008
M 10,v[104:107],46
L 3
M 10,v[112:115],50
R 38,1,44032
M 10,v[120:123],54
D 0,56,1024
M 14,v[116:119],50
R 42,1,45056
M 14,v[124:127],54
L 3
M 14,v[132:135],26
R 46,1,46080
M 14,v[140:143],30
D 0,56,2048
M 10,v[128:131],26
s_add_u32 m0,s59,0
s_add_u32 s56,s55,28672
R 50,1,47104
M 10,v[136:139],30
L 3
buffer_load_dword v8,s[36:39],s58 offen offset:0 sc1 lds
M 10,v[144:147],34
s_add_u32 m0,s57,28672
s_mul_i32 s55,s61,114688
R 54,1,48128
M 10,v[152:155],38
s_add_u32 s55,s55,s60
M 14,v[148:151],34
R 26,1,49152
M 14,v[156:159],38
L 3
D 0,56,0
M 14,v[164:167],42
R 30,1,50176
M 14,v[172:175],46
D 0,56,1024
M 10,v[160:163],42
R 34,1,51200
M 10,v[168:171],46
L 3
M 10,v[176:179],50
R 38,1,52224
M 10,v[184:187],54
D 0,56,2048
M 14,v[180:183],50
R 42,1,53248
M 14,v[188:191],54
L 3
M 14,v[196:199],26
R 46,1,54272
M 14,v[204:207],30
D 0,56,3072
M 10,v[192:195],26
s_add_u32 m0,s57,32768
s_add_u32 s56,s56,4096
R 50,1,55296
M 10,v[200:203],30
L 3
s_sub_u32 s57,s84,s57
M 10,v[208:211],34
s_waitcnt vmcnt(4)
R 54,1,56320
M 10,v[216:219],38
s_barrier
ds_read_b32 v7,v6 offset:0
M 14,v[212:215],34
R 26,2,0
M 14,v[220:223],38
L 4
buffer_store_dword v4,v3,s[36:39],s69 offen offset:0 sc0 sc1
M 14,v[228:231],42
s_mov_b32 s69,s68
s_lshr_b32 s68,s62,4
R 30,2,1024
M 14,v[236:239],46
s_add_u32 s68,s68,s48
D 0,56,0
M 10,v[224:227],42
R 34,2,2048
M 10,v[232:235],46
L 4
D 0,56,1024
M 10,v[240:243],50
L 3
R 38,2,3072
v_cmp_eq_u32 vcc,s53,v7
M 10,v[248:251],54
s_cmp_eq_u32 vcc_lo,-1
D 0,56,2048
M 14,v[244:247],50
R 42,2,4096
M 14,v[252:255],54
L 3
s_cbranch_scc1 Lg4_F1_%=
s_branch Lg4_L1_%=
Lg4_X0_%=:
M 14,a[212:215],34
s_add_u32 m0,s59,0
R 26,1,32768
M 14,a[220:223],38
L 3
buffer_load_dword v8,s[36:39],s58 offen offset:0 sc1 lds
M 14,a[228:231],42
R 30,1,33792
M 14,a[236:239],46
M 10,a[224:227],42
R 34,1,34816
M 10,a[232:235],46
L 3
M 10,a[240:243],50
R 38,1,35840
M 10,a[248:251],54
M 14,a[244:247],50
R 42,1,36864
M 14,a[252:255],54
L 3
M 14,v[68:71],26
R 46,1,37888
M 14,v[76:79],30
M 10,v[64:67],26
R 50,1,38912
M 10,v[72:75],30
L 3
M 10,v[80:83],34
R 54,1,39936
M 10,v[88:91],38
M 14,v[84:87],34
R 26,1,40960
M 14,v[92:95],38
L 3
M 14,v[100:103],42
R 30,1,41984
M 14,v[108:111],46
M 10,v[96:99],42
R 34,1,43008
M 10,v[104:107],46
L 3
M 10,v[112:115],50
R 38,1,44032
M 10,v[120:123],54
M 14,v[116:119],50
R 42,1,45056
M 14,v[124:127],54
L 3
M 14,v[132:135],26
R 46,1,46080
M 14,v[140:143],30
M 10,v[128:131],26
R 50,1,47104
M 10,v[136:139],30
L 3
M 10,v[144:147],34
R 54,1,48128
M 10,v[152:155],38
M 14,v[148:151],34
R 26,1,49152
M 14,v[156:159],38
L 3
M 14,v[164:167],42
R 30,1,50176
M 14,v[172:175],46
M 10,v[160:163],42
R 34,1,51200
M 10,v[168:171],46
L 3
M 10,v[176:179],50
R 38,1,52224
M 10,v[184:187],54
M 14,v[180:183],50
R 42,1,53248
M 14,v[188:191],54
L 3
)"
R"(M 14,v[196:199],26
R 46,1,54272
M 14,v[204:207],30
M 10,v[192:195],26
R 50,1,55296
M 10,v[200:203],30
L 3
M 10,v[208:211],34
s_waitcnt vmcnt(0)
R 54,1,56320
M 10,v[216:219],38
s_barrier
buffer_store_dword v4,v3,s[36:39],s69 offen offset:0 sc0 sc1
M 14,v[212:215],34
s_mov_b32 s69,s68
s_lshr_b32 s68,s62,4
ds_read_b32 v7,v6 offset:0
M 14,v[220:223],38
L 3
s_add_u32 s68,s68,s48
M 14,v[228:231],42
M 14,v[236:239],46
M 10,v[224:227],42
M 10,v[232:235],46
L 1
M 10,v[240:243],50
L 0
v_cmp_eq_u32 vcc,s53,v7
M 10,v[248:251],54
M 14,v[244:247],50
M 14,v[252:255],54
s_cmp_lt_u32 s54,249
s_cbranch_scc1 Lg4_xa0_%=
s_sub_u32 s75,s66,s85
v_lshlrev_b32 v62,16,v58
s_nop 1
v_add_f32 v10,v10,v62
v_and_b32 v63,0xffff0000,v58
v_add_f32 v11,v11,v63
v_lshlrev_b32 v62,16,v59
v_add_f32 v12,v12,v62
v_and_b32 v63,0xffff0000,v59
v_add_f32 v13,v13,v63
v_lshlrev_b32 v62,16,v60
v_add_f32 v14,v14,v62
v_and_b32 v63,0xffff0000,v60
v_add_f32 v15,v15,v63
v_lshlrev_b32 v62,16,v61
v_add_f32 v16,v16,v62
v_and_b32 v63,0xffff0000,v61
v_add_f32 v17,v17,v63
v_cvt_pk_bf16_f32 v10,v10,v11
v_cvt_pk_bf16_f32 v11,v12,v13
v_cvt_pk_bf16_f32 v12,v14,v15
v_cvt_pk_bf16_f32 v13,v16,v17
v_lshrrev_b32 v62,8,v0
v_lshl_add_u32 v62,v62,5,v0
v_add_u32 v62,s73,v62
ds_write_b128 v62,v[10:13] offset:0
ds_read2_b32 v[14:15],v5 offset0:0 offset1:16
ds_read2_b32 v[16:17],v5 offset0:32 offset1:48
s_mov_b64 s[40:41],s[86:87]
s_mov_b32 s42,4194304
s_mov_b32 s43,0x00020000
L 0
s_add_u32 s64,s75,0
buffer_atomic_pk_add_bf16 v14,v9,s[40:43],s64 offen sc1
s_add_u32 s64,s75,8192
buffer_atomic_pk_add_bf16 v15,v9,s[40:43],s64 offen sc1
s_add_u32 s64,s75,16384
buffer_atomic_pk_add_bf16 v16,v9,s[40:43],s64 offen sc1
s_add_u32 s64,s75,24576
buffer_atomic_pk_add_bf16 v17,v9,s[40:43],s64 offen sc1
s_mov_b32 s66,0x7fff0000
s_mov_b32 s69,0x7fff0000
Lg4_xa0_%=:
s_mov_b32 s80,1
s_cmp_eq_u32 vcc_lo,-1
s_cbranch_scc0 Lg4_xn0_%=
s_add_u32 m0,s57,0
s_add_u32 s56,s55,0
D 0,56,0
v_lshlrev_b32 v62,16,v58
D 0,56,1024
v_add_f32 v10,v10,v62
D 0,56,2048
v_and_b32 v63,0xffff0000,v58
D 0,56,3072
v_add_f32 v11,v11,v63
s_add_u32 m0,s57,4096
s_add_u32 s56,s56,4096
D 0,56,0
v_lshlrev_b32 v62,16,v59
D 0,56,1024
v_add_f32 v12,v12,v62
D 0,56,2048
v_and_b32 v63,0xffff0000,v59
s_add_u32 s61,s54,s46
s_cmp_ge_u32 s61,s63
s_cselect_b32 s61,s93,s61
s_cselect_b32 s46,7,s46
s_cselect_b32 s63,-1,s63
s_mul_i32 s58,s61,2560
s_add_u32 s58,s58,109891584
s_add_u32 m0,s57,28672
s_add_u32 s56,s55,28672
D 0,56,0
v_add_f32 v13,v13,v63
D 0,56,1024
v_lshlrev_b32 v62,16,v60
D 0,56,2048
v_add_f32 v14,v14,v62
D 0,56,3072
v_and_b32 v63,0xffff0000,v60
s_add_u32 m0,s57,32768
s_add_u32 s56,s56,4096
D 0,56,0
v_add_f32 v15,v15,v63
D 0,56,1024
v_lshlrev_b32 v62,16,v61
D 0,56,2048
v_add_f32 v16,v16,v62
s_mul_i32 s55,s61,114688
s_add_u32 s55,s55,s60
s_sub_u32 s57,s84,s57
s_mov_b32 s54,s61
v_and_b32 v63,0xffff0000,v61
v_add_f32 v17,v17,v63
v_cvt_pk_bf16_f32 v10,v10,v11
v_cvt_pk_bf16_f32 v11,v12,v13
v_cvt_pk_bf16_f32 v12,v14,v15
v_cvt_pk_bf16_f32 v13,v16,v17
buffer_store_dwordx4 v[10:13],v0,s[36:39],s66 offen offset:0 sc0 sc1
s_mov_b32 s72,s69
s_mov_b32 s69,0x7fff0000
s_mov_b32 s66,0x7fff0000
s_mov_b32 s76,0x7fff0000
s_branch Lg4_gox_%=
Lg4_xn0_%=:
s_cmp_ge_u32 s54,256
s_cbranch_scc1 Lg4_fin0_%=
s_add_u32 m0,s59,4096
s_nop 0
buffer_load_dword v8,s[36:39],s58 offen offset:0 sc1 lds
buffer_store_dword v4,v3,s[36:39],s71 offen offset:0 sc0 sc1
s_add_u32 m0,s59,5120
s_nop 0
buffer_load_dword v8,s[36:39],s58 offen offset:0 sc1 lds
buffer_store_dword v4,v3,s[36:39],s71 offen offset:0 sc0 sc1
s_mov_b32 s83,0
v_lshlrev_b32 v62,16,v58
v_add_f32 v10,v10,v62
v_and_b32 v63,0xffff0000,v58
v_add_f32 v11,v11,v63
v_lshlrev_b32 v62,16,v59
v_add_f32 v12,v12,v62
v_and_b32 v63,0xffff0000,v59
v_add_f32 v13,v13,v63
v_lshlrev_b32 v62,16,v60
v_add_f32 v14,v14,v62
v_and_b32 v63,0xffff0000,v60
v_add_f32 v15,v15,v63
v_lshlrev_b32 v62,16,v61
v_add_f32 v16,v16,v62
v_and_b32 v63,0xffff0000,v61
v_add_f32 v17,v17,v63
v_cvt_pk_bf16_f32 v10,v10,v11
v_cvt_pk_bf16_f32 v11,v12,v13
v_cvt_pk_bf16_f32 v12,v14,v15
v_cvt_pk_bf16_f32 v13,v16,v17
buffer_store_dwordx4 v[10:13],v0,s[36:39],s66 offen offset:0 sc0 sc1
s_mov_b32 s72,s69
s_mov_b32 s69,0x7fff0000
s_mov_b32 s66,0x7fff0000
s_mov_b32 s76,0x7fff0000
s_branch Lg4_poll_%=
Lg4_fin0_%=:
v_lshlrev_b32 v62,16,v58
v_add_f32 v10,v10,v62
v_and_b32 v63,0xffff0000,v58
v_add_f32 v11,v11,v63
v_lshlrev_b32 v62,16,v59
v_add_f32 v12,v12,v62
v_and_b32 v63,0xffff0000,v59
v_add_f32 v13,v13,v63
v_lshlrev_b32 v62,16,v60
v_add_f32 v14,v14,v62
v_and_b32 v63,0xffff0000,v60
v_add_f32 v15,v15,v63
v_lshlrev_b32 v62,16,v61
v_add_f32 v16,v16,v62
v_and_b32 v63,0xffff0000,v61
v_add_f32 v17,v17,v63
v_cvt_pk_bf16_f32 v10,v10,v11
v_cvt_pk_bf16_f32 v11,v12,v13
v_cvt_pk_bf16_f32 v12,v14,v15
v_cvt_pk_bf16_f32 v13,v16,v17
buffer_store_dwordx4 v[10:13],v0,s[36:39],s66 offen offset:0 sc0 sc1
s_mov_b32 s72,s69
s_mov_b32 s69,0x7fff0000
s_mov_b32 s66,0x7fff0000
s_mov_b32 s76,0x7fff0000
s_branch Lg4_done_%=
Lg4_L1_%=:
s_cmp_ge_u32 s54,256
s_cbranch_scc1 Lg4_Z1_%=
v_mfma_f32_16x16x32_bf16 v[22:25],a[4:7],v[26:29],0
s_add_u32 m0,s59,3072
R 46,2,5120
v_lshlrev_b32 v62,16,v58
M 22,a[12:15],30
s_lshl_b32 s62,s54,15
buffer_load_dword v8,s[36:39],s58 offen offset:0 sc1 lds
v_mfma_f32_16x16x32_bf16 v[18:21],a[0:3],v[26:29],0
s_mov_b32 s67,s65
R 50,2,6144
v_add_f32 v10,v10,v62
M 18,a[8:11],30
L 3
v_and_b32 v63,0xffff0000,v58
v_add_f32 v11,v11,v63
M 18,a[16:19],34
s_add_u32 s65,s47,s62
R 54,2,7168
v_lshlrev_b32 v62,16,v59
M 18,a[24:27],38
v_add_f32 v12,v12,v62
v_and_b32 v63,0xffff0000,v59
M 22,a[20:23],34
R 26,2,8192
v_add_f32 v13,v13,v63
M 22,a[28:31],38
L 3
v_lshlrev_b32 v62,16,v60
v_add_f32 v14,v14,v62
M 22,a[36:39],42
R 30,2,9216
v_and_b32 v63,0xffff0000,v60
M 22,a[44:47],46
v_add_f32 v15,v15,v63
v_lshlrev_b32 v62,16,v61
M 18,a[32:35],42
R 34,2,10240
v_add_f32 v16,v16,v62
M 18,a[40:43],46
L 3
v_and_b32 v63,0xffff0000,v61
v_add_f32 v17,v17,v63
M 18,a[48:51],50
R 38,2,11264
v_cvt_pk_bf16_f32 v10,v10,v11
M 18,a[56:59],54
buffer_load_dwordx4 v[58:61],v0,s[36:39],s74 offen offset:0 sc1
M 22,a[52:55],50
s_add_u32 s74,s49,s62
R 42,2,12288
v_cvt_pk_bf16_f32 v11,v12,v13
M 22,a[60:63],54
L 3
v_cvt_pk_bf16_f32 v12,v14,v15
v_cvt_pk_bf16_f32 v13,v16,v17
M 22,a[68:71],26
R 46,2,13312
M 22,a[76:79],30
buffer_store_dwordx4 v[10:13],v0,s[36:39],s66 offen offset:0 sc0 sc1
M 18,a[64:67],26
R 50,2,14336
M 18,a[72:75],30
L 3
M 18,a[80:83],34
R 54,2,15360
M 18,a[88:91],38
M 22,a[84:87],34
R 26,2,16384
M 22,a[92:95],38
L 3
M 22,a[100:103],42
R 30,2,17408
M 22,a[108:111],46
M 18,a[96:99],42
R 34,2,18432
M 18,a[104:107],46
L 3
M 18,a[112:115],50
R 38,2,19456
M 18,a[120:123],54
M 22,a[116:119],50
R 42,2,20480
M 22,a[124:127],54
L 3
M 22,a[132:135],26
R 46,2,21504
M 22,a[140:143],30
M 18,a[128:131],26
R 50,2,22528
M 18,a[136:139],30
L 3
M 18,a[144:147],34
R 54,2,23552
M 18,a[152:155],38
M 22,a[148:151],34
R 26,2,24576
M 22,a[156:159],38
L 3
M 22,a[164:167],42
R 30,2,25600
M 22,a[172:175],46
M 18,a[160:163],42
R 34,2,26624
M 18,a[168:171],46
L 3
M 18,a[176:179],50
s_waitcnt vmcnt(2)
R 38,2,27648
M 18,a[184:187],54
s_barrier
ds_read_b32 v7,v6 offset:3072
M 22,a[180:183],50
R 42,2,28672
M 22,a[188:191],54
L 4
M 22,a[196:199],26
R 46,2,29696
M 22,a[204:207],30
M 18,a[192:195],26
R 50,2,30720
M 18,a[200:203],30
L 4
L 3
v_cmp_eq_u32 vcc,s53,v7
M 18,a[208:211],34
R 54,2,31744
M 18,a[216:219],38
s_cmp_eq_u32 vcc_lo,-1
s_cbranch_scc0 Lg4_X1_%=
M 22,a[212:215],34
s_add_u32 m0,s57,0
s_add_u32 s56,s55,0
s_add_u32 s61,s54,s46
R 26,2,32768
M 22,a[220:223],38
L 3
s_cmp_ge_u32 s61,s63
s_cselect_b32 s61,s93,s61
M 22,a[228:231],42
s_cselect_b32 s46,7,s46
s_mul_i32 s58,s61,2560
R 30,2,33792
M 22,a[236:239],46
s_cselect_b32 s63,-1,s63
s_add_u32 s58,s58,109891584
D 0,56,0
M 18,a[224:227],42
s_mov_b32 s54,s61
R 34,2,34816
M 18,a[232:235],46
L 3
M 18,a[240:243],50
R 38,2,35840
M 18,a[248:251],54
D 0,56,1024
M 22,a[244:247],50
R 42,2,36864
M 22,a[252:255],54
L 3
M 22,v[68:71],26
R 46,2,37888
M 22,v[76:79],30
D 0,56,2048
M 18,v[64:67],26
R 50,2,38912
M 18,v[72:75],30
L 3
M 18,v[80:83],34
R 54,2,39936
M 18,v[88:91],38
D 0,56,3072
M 22,v[84:87],34
s_add_u32 m0,s57,4096
s_add_u32 s56,s56,4096
R 26,2,40960
M 22,v[92:95],38
L 3
M 22,v[100:103],42
R 30,2,41984
M 22,v[108:111],46
D 0,56,0
M 18,v[96:99],42
R 34,2,43008
M 18,v[104:107],46
L 3
M 18,v[112:115],50
R 38,2,44032
M 18,v[120:123],54
D 0,56,1024
M 22,v[116:119],50
R 42,2,45056
M 22,v[124:127],54
L 3
M 22,v[132:135],26
R 46,2,46080
M 22,v[140:143],30
D 0,56,2048
M 18,v[128:131],26
s_add_u32 m0,s59,1024
s_add_u32 s56,s55,28672
R 50,2,47104
M 18,v[136:139],30
L 3
buffer_load_dword v8,s[36:39],s58 offen offset:0 sc1 lds
M 18,v[144:147],34
s_add_u32 m0,s57,28672
s_mul_i32 s55,s61,114688
R 54,2,48128
M 18,v[152:155],38
s_add_u32 s55,s55,s60
M 22,v[148:151],34
R 26,2,49152
M 22,v[156:159],38
L 3
D 0,56,0
M 22,v[164:167],42
R 30,2,50176
M 22,v[172:175],46
D 0,56,1024
M 18,v[160:163],42
R 34,2,51200
M 18,v[168:171],46
L 3
M 18,v[176:179],50
R 38,2,52224
M 18,v[184:187],54
D 0,56,2048
M 22,v[180:183],50
R 42,2,53248
M 22,v[188:191],54
L 3
M 22,v[196:199],26
R 46,2,54272
M 22,v[204:207],30
D 0,56,3072
M 18,v[192:195],26
s_add_u32 m0,s57,32768
s_add_u32 s56,s56,4096
R 50,2,55296
M 18,v[200:203],30
L 3
s_sub_u32 s57,s84,s57
M 18,v[208:211],34
s_waitcnt vmcnt(4)
R 54,2,56320
M 18,v[216:219],38
s_barrier
ds_read_b32 v7,v6 offset:1024
M 22,v[212:215],34
R 26,1,0
M 22,v[220:223],38
L 4
buffer_store_dword v4,v3,s[36:39],s69 offen offset:0 sc0 sc1
M 22,v[228:231],42
s_mov_b32 s69,s68
s_lshr_b32 s68,s62,4
R 30,1,1024
M 22,v[236:239],46
s_add_u32 s68,s68,s48
D 0,56,0
M 18,v[224:227],42
R 34,1,2048
M 18,v[232:235],46
L 4
D 0,56,1024
M 18,v[240:243],50
L 3
R 38,1,3072
v_cmp_eq_u32 vcc,s53,v7
M 18,v[248:251],54
s_cmp_eq_u32 vcc_lo,-1
D 0,56,2048
M 22,v[244:247],50
R 42,1,4096
M 22,v[252:255],54
L 3
s_cbranch_scc1 Lg4_F0_%=
s_branch Lg4_L0_%=
Lg4_X1_%=:
M 22,a[212:215],34
s_add_u32 m0,s59,1024
R 26,2,32768
M 22,a[220:223],38
L 3
buffer_load_dword v8,s[36:39],s58 offen offset:0 sc1 lds
M 22,a[228:231],42
R 30,2,33792
M 22,a[236:239],46
M 18,a[224:227],42
R 34,2,34816
M 18,a[232:235],46
L 3
M 18,a[240:243],50
R 38,2,35840
)"
R"(M 18,a[248:251],54
M 22,a[244:247],50
R 42,2,36864
M 22,a[252:255],54
L 3
M 22,v[68:71],26
R 46,2,37888
M 22,v[76:79],30
M 18,v[64:67],26
R 50,2,38912
M 18,v[72:75],30
L 3
M 18,v[80:83],34
R 54,2,39936
M 18,v[88:91],38
M 22,v[84:87],34
R 26,2,40960
M 22,v[92:95],38
L 3
M 22,v[100:103],42
R 30,2,41984
M 22,v[108:111],46
M 18,v[96:99],42
R 34,2,43008
M 18,v[104:107],46
L 3
M 18,v[112:115],50
R 38,2,44032
M 18,v[120:123],54
M 22,v[116:119],50
R 42,2,45056
M 22,v[124:127],54
L 3
M 22,v[132:135],26
R 46,2,46080
M 22,v[140:143],30
M 18,v[128:131],26
R 50,2,47104
M 18,v[136:139],30
L 3
M 18,v[144:147],34
R 54,2,48128
M 18,v[152:155],38
M 22,v[148:151],34
R 26,2,49152
M 22,v[156:159],38
L 3
M 22,v[164:167],42
R 30,2,50176
M 22,v[172:175],46
M 18,v[160:163],42
R 34,2,51200
M 18,v[168:171],46
L 3
M 18,v[176:179],50
R 38,2,52224
M 18,v[184:187],54
M 22,v[180:183],50
R 42,2,53248
M 22,v[188:191],54
L 3
M 22,v[196:199],26
R 46,2,54272
M 22,v[204:207],30
M 18,v[192:195],26
R 50,2,55296
M 18,v[200:203],30
L 3
M 18,v[208:211],34
s_waitcnt vmcnt(0)
R 54,2,56320
M 18,v[216:219],38
s_barrier
buffer_store_dword v4,v3,s[36:39],s69 offen offset:0 sc0 sc1
M 22,v[212:215],34
s_mov_b32 s69,s68
s_lshr_b32 s68,s62,4
ds_read_b32 v7,v6 offset:1024
M 22,v[220:223],38
L 3
s_add_u32 s68,s68,s48
M 22,v[228:231],42
M 22,v[236:239],46
M 18,v[224:227],42
M 18,v[232:235],46
L 1
M 18,v[240:243],50
L 0
v_cmp_eq_u32 vcc,s53,v7
M 18,v[248:251],54
M 22,v[244:247],50
M 22,v[252:255],54
s_cmp_lt_u32 s54,249
s_cbranch_scc1 Lg4_xa1_%=
s_sub_u32 s75,s67,s85
v_lshlrev_b32 v62,16,v58
s_nop 1
v_add_f32 v18,v18,v62
v_and_b32 v63,0xffff0000,v58
v_add_f32 v19,v19,v63
v_lshlrev_b32 v62,16,v59
v_add_f32 v20,v20,v62
v_and_b32 v63,0xffff0000,v59
v_add_f32 v21,v21,v63
v_lshlrev_b32 v62,16,v60
v_add_f32 v22,v22,v62
v_and_b32 v63,0xffff0000,v60
v_add_f32 v23,v23,v63
v_lshlrev_b32 v62,16,v61
v_add_f32 v24,v24,v62
v_and_b32 v63,0xffff0000,v61
v_add_f32 v25,v25,v63
v_cvt_pk_bf16_f32 v18,v18,v19
v_cvt_pk_bf16_f32 v19,v20,v21
v_cvt_pk_bf16_f32 v20,v22,v23
v_cvt_pk_bf16_f32 v21,v24,v25
v_lshrrev_b32 v62,8,v0
v_lshl_add_u32 v62,v62,5,v0
v_add_u32 v62,s73,v62
ds_write_b128 v62,v[18:21] offset:0
ds_read2_b32 v[22:23],v5 offset0:0 offset1:16
ds_read2_b32 v[24:25],v5 offset0:32 offset1:48
s_mov_b64 s[40:41],s[86:87]
s_mov_b32 s42,4194304
s_mov_b32 s43,0x00020000
L 0
s_add_u32 s64,s75,0
buffer_atomic_pk_add_bf16 v22,v9,s[40:43],s64 offen sc1
s_add_u32 s64,s75,8192
buffer_atomic_pk_add_bf16 v23,v9,s[40:43],s64 offen sc1
s_add_u32 s64,s75,16384
buffer_atomic_pk_add_bf16 v24,v9,s[40:43],s64 offen sc1
s_add_u32 s64,s75,24576
buffer_atomic_pk_add_bf16 v25,v9,s[40:43],s64 offen sc1
s_mov_b32 s67,0x7fff0000
s_mov_b32 s69,0x7fff0000
Lg4_xa1_%=:
s_mov_b32 s80,0
s_cmp_eq_u32 vcc_lo,-1
s_cbranch_scc0 Lg4_xn1_%=
s_add_u32 m0,s57,0
s_add_u32 s56,s55,0
D 0,56,0
v_lshlrev_b32 v62,16,v58
D 0,56,1024
v_add_f32 v18,v18,v62
D 0,56,2048
v_and_b32 v63,0xffff0000,v58
D 0,56,3072
v_add_f32 v19,v19,v63
s_add_u32 m0,s57,4096
s_add_u32 s56,s56,4096
D 0,56,0
v_lshlrev_b32 v62,16,v59
D 0,56,1024
v_add_f32 v20,v20,v62
D 0,56,2048
v_and_b32 v63,0xffff0000,v59
s_add_u32 s61,s54,s46
s_cmp_ge_u32 s61,s63
s_cselect_b32 s61,s93,s61
s_cselect_b32 s46,7,s46
s_cselect_b32 s63,-1,s63
s_mul_i32 s58,s61,2560
s_add_u32 s58,s58,109891584
s_add_u32 m0,s57,28672
s_add_u32 s56,s55,28672
D 0,56,0
v_add_f32 v21,v21,v63
D 0,56,1024
v_lshlrev_b32 v62,16,v60
D 0,56,2048
v_add_f32 v22,v22,v62
D 0,56,3072
v_and_b32 v63,0xffff0000,v60
s_add_u32 m0,s57,32768
s_add_u32 s56,s56,4096
D 0,56,0
v_add_f32 v23,v23,v63
D 0,56,1024
v_lshlrev_b32 v62,16,v61
D 0,56,2048
v_add_f32 v24,v24,v62
s_mul_i32 s55,s61,114688
s_add_u32 s55,s55,s60
s_sub_u32 s57,s84,s57
s_mov_b32 s54,s61
v_and_b32 v63,0xffff0000,v61
v_add_f32 v25,v25,v63
v_cvt_pk_bf16_f32 v18,v18,v19
v_cvt_pk_bf16_f32 v19,v20,v21
v_cvt_pk_bf16_f32 v20,v22,v23
v_cvt_pk_bf16_f32 v21,v24,v25
buffer_store_dwordx4 v[18:21],v0,s[36:39],s67 offen offset:0 sc0 sc1
s_mov_b32 s72,s69
s_mov_b32 s69,0x7fff0000
s_mov_b32 s67,0x7fff0000
s_mov_b32 s77,0x7fff0000
s_branch Lg4_gox_%=
Lg4_xn1_%=:
s_cmp_ge_u32 s54,256
s_cbranch_scc1 Lg4_fin1_%=
s_add_u32 m0,s59,4096
s_nop 0
buffer_load_dword v8,s[36:39],s58 offen offset:0 sc1 lds
buffer_store_dword v4,v3,s[36:39],s71 offen offset:0 sc0 sc1
s_add_u32 m0,s59,5120
s_nop 0
buffer_load_dword v8,s[36:39],s58 offen offset:0 sc1 lds
buffer_store_dword v4,v3,s[36:39],s71 offen offset:0 sc0 sc1
s_mov_b32 s83,0
v_lshlrev_b32 v62,16,v58
v_add_f32 v18,v18,v62
v_and_b32 v63,0xffff0000,v58
v_add_f32 v19,v19,v63
v_lshlrev_b32 v62,16,v59
v_add_f32 v20,v20,v62
v_and_b32 v63,0xffff0000,v59
v_add_f32 v21,v21,v63
v_lshlrev_b32 v62,16,v60
v_add_f32 v22,v22,v62
v_and_b32 v63,0xffff0000,v60
v_add_f32 v23,v23,v63
v_lshlrev_b32 v62,16,v61
v_add_f32 v24,v24,v62
v_and_b32 v63,0xffff0000,v61
v_add_f32 v25,v25,v63
v_cvt_pk_bf16_f32 v18,v18,v19
v_cvt_pk_bf16_f32 v19,v20,v21
v_cvt_pk_bf16_f32 v20,v22,v23
v_cvt_pk_bf16_f32 v21,v24,v25
buffer_store_dwordx4 v[18:21],v0,s[36:39],s67 offen offset:0 sc0 sc1
s_mov_b32 s72,s69
s_mov_b32 s69,0x7fff0000
s_mov_b32 s67,0x7fff0000
s_mov_b32 s77,0x7fff0000
s_branch Lg4_poll_%=
Lg4_fin1_%=:
v_lshlrev_b32 v62,16,v58
v_add_f32 v18,v18,v62
v_and_b32 v63,0xffff0000,v58
v_add_f32 v19,v19,v63
v_lshlrev_b32 v62,16,v59
v_add_f32 v20,v20,v62
v_and_b32 v63,0xffff0000,v59
v_add_f32 v21,v21,v63
v_lshlrev_b32 v62,16,v60
v_add_f32 v22,v22,v62
v_and_b32 v63,0xffff0000,v60
v_add_f32 v23,v23,v63
v_lshlrev_b32 v62,16,v61
v_add_f32 v24,v24,v62
v_and_b32 v63,0xffff0000,v61
v_add_f32 v25,v25,v63
v_cvt_pk_bf16_f32 v18,v18,v19
v_cvt_pk_bf16_f32 v19,v20,v21
v_cvt_pk_bf16_f32 v20,v22,v23
v_cvt_pk_bf16_f32 v21,v24,v25
buffer_store_dwordx4 v[18:21],v0,s[36:39],s67 offen offset:0 sc0 sc1
s_mov_b32 s72,s69
s_mov_b32 s69,0x7fff0000
s_mov_b32 s67,0x7fff0000
s_mov_b32 s77,0x7fff0000
s_branch Lg4_done_%=
Lg4_Z0_%=:
v_mfma_f32_16x16x32_bf16 v[14:17],a[4:7],v[26:29],0
s_sub_u32 s75,s67,s85
R 46,1,5120
v_lshlrev_b32 v62,16,v58
M 14,a[12:15],30
s_mov_b64 s[40:41],s[86:87]
v_add_f32 v18,v18,v62
v_and_b32 v63,0xffff0000,v58
v_mfma_f32_16x16x32_bf16 v[10:13],a[0:3],v[26:29],0
s_mov_b32 s42,4194304
R 50,1,6144
v_add_f32 v19,v19,v63
M 10,a[8:11],30
L 3
v_lshlrev_b32 v62,16,v59
v_add_f32 v20,v20,v62
M 10,a[16:19],34
s_mov_b32 s43,0x00020000
R 54,1,7168
v_and_b32 v63,0xffff0000,v59
M 10,a[24:27],38
s_add_u32 s64,s75,0
v_add_f32 v21,v21,v63
v_lshlrev_b32 v62,16,v60
M 14,a[20:23],34
s_mov_b32 s66,s65
R 26,1,8192
v_add_f32 v22,v22,v62
M 14,a[28:31],38
L 3
v_and_b32 v63,0xffff0000,v60
v_add_f32 v23,v23,v63
M 14,a[36:39],42
R 30,1,9216
v_lshlrev_b32 v62,16,v61
M 14,a[44:47],46
v_add_f32 v24,v24,v62
v_and_b32 v63,0xffff0000,v61
M 10,a[32:35],42
R 34,1,10240
v_add_f32 v25,v25,v63
M 10,a[40:43],46
L 3
buffer_load_dwordx4 v[58:61],v0,s[36:39],s74 offen offset:0 sc1
M 10,a[48:51],50
s_add_u32 s74,s49,s62
R 38,1,11264
v_cvt_pk_bf16_f32 v18,v18,v19
M 10,a[56:59],54
v_cvt_pk_bf16_f32 v19,v20,v21
v_cvt_pk_bf16_f32 v20,v22,v23
M 14,a[52:55],50
R 42,1,12288
v_cvt_pk_bf16_f32 v21,v24,v25
M 14,a[60:63],54
L 3
v_lshrrev_b32 v62,8,v0
v_lshl_add_u32 v62,v62,5,v0
M 14,a[68:71],26
R 46,1,13312
v_add_u32 v62,s73,v62
M 14,a[76:79],30
ds_write_b128 v62,v[18:21] offset:0
M 10,a[64:67],26
R 50,1,14336
ds_read2_b32 v[22:23],v5 offset0:0 offset1:16
M 10,a[72:75],30
L 5
ds_read2_b32 v[24:25],v5 offset0:32 offset1:48
M 10,a[80:83],34
R 54,1,15360
M 10,a[88:91],38
M 14,a[84:87],34
R 26,1,16384
M 14,a[92:95],38
L 6
M 14,a[100:103],42
R 30,1,17408
M 14,a[108:111],46
L 3
buffer_atomic_pk_add_bf16 v22,v9,s[40:43],s64 offen sc1
M 10,a[96:99],42
s_add_u32 s64,s75,8192
R 34,1,18432
M 10,a[104:107],46
L 3
buffer_atomic_pk_add_bf16 v23,v9,s[40:43],s64 offen sc1
M 10,a[112:115],50
s_add_u32 s64,s75,16384
R 38,1,19456
M 10,a[120:123],54
buffer_atomic_pk_add_bf16 v24,v9,s[40:43],s64 offen sc1
M 14,a[116:119],50
s_add_u32 s64,s75,24576
R 42,1,20480
M 14,a[124:127],54
L 3
buffer_atomic_pk_add_bf16 v25,v9,s[40:43],s64 offen sc1
M 14,a[132:135],26
R 46,1,21504
M 14,a[140:143],30
M 10,a[128:131],26
R 50,1,22528
M 10,a[136:139],30
L 3
M 10,a[144:147],34
R 54,1,23552
M 10,a[152:155],38
M 14,a[148:151],34
R 26,1,24576
M 14,a[156:159],38
L 3
M 14,a[164:167],42
R 30,1,25600
M 14,a[172:175],46
M 10,a[160:163],42
R 34,1,26624
M 10,a[168:171],46
L 3
M 10,a[176:179],50
s_waitcnt vmcnt(5)
R 38,1,27648
M 10,a[184:187],54
s_barrier
M 14,a[180:183],50
R 42,1,28672
M 14,a[188:191],54
L 3
M 14,a[196:199],26
R 46,1,29696
M 14,a[204:207],30
M 10,a[192:195],26
R 50,1,30720
M 10,a[200:203],30
L 3
M 10,a[208:211],34
R 54,1,31744
M 10,a[216:219],38
M 14,a[212:215],34
R 26,1,32768
M 14,a[220:223],38
L 3
M 14,a[228:231],42
R 30,1,33792
M 14,a[236:239],46
M 10,a[224:227],42
R 34,1,34816
M 10,a[232:235],46
L 3
M 10,a[240:243],50
R 38,1,35840
M 10,a[248:251],54
M 14,a[244:247],50
R 42,1,36864
M 14,a[252:255],54
L 3
M 14,v[68:71],26
R 46,1,37888
M 14,v[76:79],30
M 10,v[64:67],26
R 50,1,38912
M 10,v[72:75],30
L 3
M 10,v[80:83],34
R 54,1,39936
M 10,v[88:91],38
M 14,v[84:87],34
R 26,1,40960
M 14,v[92:95],38
L 3
M 14,v[100:103],42
R 30,1,41984
M 14,v[108:111],46
M 10,v[96:99],42
R 34,1,43008
M 10,v[104:107],46
L 3
M 10,v[112:115],50
R 38,1,44032
M 10,v[120:123],54
M 14,v[116:119],50
R 42,1,45056
M 14,v[124:127],54
L 3
M 14,v[132:135],26
R 46,1,46080
M 14,v[140:143],30
M 10,v[128:131],26
R 50,1,47104
M 10,v[136:139],30
L 3
M 10,v[144:147],34
R 54,1,48128
M 10,v[152:155],38
M 14,v[148:151],34
R 26,1,49152
M 14,v[156:159],38
L 3
M 14,v[164:167],42
R 30,1,50176
M 14,v[172:175],46
M 10,v[160:163],42
R 34,1,51200
M 10,v[168:171],46
L 3
M 10,v[176:179],50
R 38,1,52224
M 10,v[184:187],54
M 14,v[180:183],50
R 42,1,53248
M 14,v[188:191],54
L 3
M 14,v[196:199],26
R 46,1,54272
M 14,v[204:207],30
M 10,v[192:195],26
R 50,1,55296
M 10,v[200:203],30
L 3
M 10,v[208:211],34
R 54,1,56320
M 10,v[216:219],38
M 14,v[212:215],34
M 14,v[220:223],38
L 2
M 14,v[228:231],42
M 14,v[236:239],46
M 10,v[224:227],42
M 10,v[232:235],46
L 0
M 10,v[240:243],50
M 10,v[248:251],54
M 14,v[244:247],50
M 14,v[252:255],54
s_waitcnt vmcnt(4)
)"
R"(s_sub_u32 s75,s66,s85
v_lshlrev_b32 v62,16,v58
s_nop 2
v_add_f32 v10,v10,v62
v_and_b32 v63,0xffff0000,v58
v_add_f32 v11,v11,v63
v_lshlrev_b32 v62,16,v59
v_add_f32 v12,v12,v62
v_and_b32 v63,0xffff0000,v59
v_add_f32 v13,v13,v63
v_lshlrev_b32 v62,16,v60
v_add_f32 v14,v14,v62
v_and_b32 v63,0xffff0000,v60
v_add_f32 v15,v15,v63
v_lshlrev_b32 v62,16,v61
v_add_f32 v16,v16,v62
v_and_b32 v63,0xffff0000,v61
v_add_f32 v17,v17,v63
v_cvt_pk_bf16_f32 v10,v10,v11
v_cvt_pk_bf16_f32 v11,v12,v13
v_cvt_pk_bf16_f32 v12,v14,v15
v_cvt_pk_bf16_f32 v13,v16,v17
v_lshrrev_b32 v62,8,v0
v_lshl_add_u32 v62,v62,5,v0
v_add_u32 v62,s73,v62
ds_write_b128 v62,v[10:13] offset:0
ds_read2_b32 v[14:15],v5 offset0:0 offset1:16
ds_read2_b32 v[16:17],v5 offset0:32 offset1:48
s_mov_b64 s[40:41],s[86:87]
s_mov_b32 s42,4194304
s_mov_b32 s43,0x00020000
L 0
s_add_u32 s64,s75,0
buffer_atomic_pk_add_bf16 v14,v9,s[40:43],s64 offen sc1
s_add_u32 s64,s75,8192
buffer_atomic_pk_add_bf16 v15,v9,s[40:43],s64 offen sc1
s_add_u32 s64,s75,16384
buffer_atomic_pk_add_bf16 v16,v9,s[40:43],s64 offen sc1
s_add_u32 s64,s75,24576
buffer_atomic_pk_add_bf16 v17,v9,s[40:43],s64 offen sc1
s_branch Lg4_done_%=
Lg4_Z1_%=:
v_mfma_f32_16x16x32_bf16 v[22:25],a[4:7],v[26:29],0
s_sub_u32 s75,s66,s85
R 46,2,5120
v_lshlrev_b32 v62,16,v58
M 22,a[12:15],30
s_mov_b64 s[40:41],s[86:87]
v_add_f32 v10,v10,v62
v_and_b32 v63,0xffff0000,v58
v_mfma_f32_16x16x32_bf16 v[18:21],a[0:3],v[26:29],0
s_mov_b32 s42,4194304
R 50,2,6144
v_add_f32 v11,v11,v63
M 18,a[8:11],30
L 3
v_lshlrev_b32 v62,16,v59
v_add_f32 v12,v12,v62
M 18,a[16:19],34
s_mov_b32 s43,0x00020000
R 54,2,7168
v_and_b32 v63,0xffff0000,v59
M 18,a[24:27],38
s_add_u32 s64,s75,0
v_add_f32 v13,v13,v63
v_lshlrev_b32 v62,16,v60
M 22,a[20:23],34
s_mov_b32 s67,s65
R 26,2,8192
v_add_f32 v14,v14,v62
M 22,a[28:31],38
L 3
v_and_b32 v63,0xffff0000,v60
v_add_f32 v15,v15,v63
M 22,a[36:39],42
R 30,2,9216
v_lshlrev_b32 v62,16,v61
M 22,a[44:47],46
v_add_f32 v16,v16,v62
v_and_b32 v63,0xffff0000,v61
M 18,a[32:35],42
R 34,2,10240
v_add_f32 v17,v17,v63
M 18,a[40:43],46
L 3
buffer_load_dwordx4 v[58:61],v0,s[36:39],s74 offen offset:0 sc1
M 18,a[48:51],50
s_add_u32 s74,s49,s62
R 38,2,11264
v_cvt_pk_bf16_f32 v10,v10,v11
M 18,a[56:59],54
v_cvt_pk_bf16_f32 v11,v12,v13
v_cvt_pk_bf16_f32 v12,v14,v15
M 22,a[52:55],50
R 42,2,12288
v_cvt_pk_bf16_f32 v13,v16,v17
M 22,a[60:63],54
L 3
v_lshrrev_b32 v62,8,v0
v_lshl_add_u32 v62,v62,5,v0
M 22,a[68:71],26
R 46,2,13312
v_add_u32 v62,s73,v62
M 22,a[76:79],30
ds_write_b128 v62,v[10:13] offset:0
M 18,a[64:67],26
R 50,2,14336
ds_read2_b32 v[14:15],v5 offset0:0 offset1:16
M 18,a[72:75],30
L 5
ds_read2_b32 v[16:17],v5 offset0:32 offset1:48
M 18,a[80:83],34
R 54,2,15360
M 18,a[88:91],38
M 22,a[84:87],34
R 26,2,16384
M 22,a[92:95],38
L 6
M 22,a[100:103],42
R 30,2,17408
M 22,a[108:111],46
L 3
buffer_atomic_pk_add_bf16 v14,v9,s[40:43],s64 offen sc1
M 18,a[96:99],42
s_add_u32 s64,s75,8192
R 34,2,18432
M 18,a[104:107],46
L 3
buffer_atomic_pk_add_bf16 v15,v9,s[40:43],s64 offen sc1
M 18,a[112:115],50
s_add_u32 s64,s75,16384
R 38,2,19456
M 18,a[120:123],54
buffer_atomic_pk_add_bf16 v16,v9,s[40:43],s64 offen sc1
M 22,a[116:119],50
s_add_u32 s64,s75,24576
R 42,2,20480
M 22,a[124:127],54
L 3
buffer_atomic_pk_add_bf16 v17,v9,s[40:43],s64 offen sc1
M 22,a[132:135],26
R 46,2,21504
M 22,a[140:143],30
M 18,a[128:131],26
R 50,2,22528
M 18,a[136:139],30
L 3
M 18,a[144:147],34
R 54,2,23552
M 18,a[152:155],38
M 22,a[148:151],34
R 26,2,24576
M 22,a[156:159],38
L 3
M 22,a[164:167],42
R 30,2,25600
M 22,a[172:175],46
M 18,a[160:163],42
R 34,2,26624
M 18,a[168:171],46
L 3
M 18,a[176:179],50
s_waitcnt vmcnt(5)
R 38,2,27648
M 18,a[184:187],54
s_barrier
M 22,a[180:183],50
R 42,2,28672
M 22,a[188:191],54
L 3
M 22,a[196:199],26
R 46,2,29696
M 22,a[204:207],30
M 18,a[192:195],26
R 50,2,30720
M 18,a[200:203],30
L 3
M 18,a[208:211],34
R 54,2,31744
M 18,a[216:219],38
M 22,a[212:215],34
R 26,2,32768
M 22,a[220:223],38
L 3
M 22,a[228:231],42
R 30,2,33792
M 22,a[236:239],46
M 18,a[224:227],42
R 34,2,34816
M 18,a[232:235],46
L 3
M 18,a[240:243],50
R 38,2,35840
M 18,a[248:251],54
M 22,a[244:247],50
R 42,2,36864
M 22,a[252:255],54
L 3
M 22,v[68:71],26
R 46,2,37888
M 22,v[76:79],30
M 18,v[64:67],26
R 50,2,38912
M 18,v[72:75],30
L 3
M 18,v[80:83],34
R 54,2,39936
M 18,v[88:91],38
M 22,v[84:87],34
R 26,2,40960
M 22,v[92:95],38
L 3
M 22,v[100:103],42
R 30,2,41984
M 22,v[108:111],46
M 18,v[96:99],42
R 34,2,43008
M 18,v[104:107],46
L 3
M 18,v[112:115],50
R 38,2,44032
M 18,v[120:123],54
M 22,v[116:119],50
R 42,2,45056
M 22,v[124:127],54
L 3
M 22,v[132:135],26
R 46,2,46080
M 22,v[140:143],30
M 18,v[128:131],26
R 50,2,47104
M 18,v[136:139],30
L 3
M 18,v[144:147],34
R 54,2,48128
M 18,v[152:155],38
M 22,v[148:151],34
R 26,2,49152
M 22,v[156:159],38
L 3
M 22,v[164:167],42
R 30,2,50176
M 22,v[172:175],46
M 18,v[160:163],42
R 34,2,51200
M 18,v[168:171],46
L 3
M 18,v[176:179],50
R 38,2,52224
M 18,v[184:187],54
M 22,v[180:183],50
R 42,2,53248
M 22,v[188:191],54
L 3
M 22,v[196:199],26
R 46,2,54272
M 22,v[204:207],30
M 18,v[192:195],26
R 50,2,55296
M 18,v[200:203],30
L 3
M 18,v[208:211],34
R 54,2,56320
M 18,v[216:219],38
M 22,v[212:215],34
M 22,v[220:223],38
L 2
M 22,v[228:231],42
M 22,v[236:239],46
M 18,v[224:227],42
M 18,v[232:235],46
L 0
M 18,v[240:243],50
M 18,v[248:251],54
M 22,v[244:247],50
M 22,v[252:255],54
s_waitcnt vmcnt(4)
s_sub_u32 s75,s67,s85
v_lshlrev_b32 v62,16,v58
s_nop 2
v_add_f32 v18,v18,v62
v_and_b32 v63,0xffff0000,v58
v_add_f32 v19,v19,v63
v_lshlrev_b32 v62,16,v59
v_add_f32 v20,v20,v62
v_and_b32 v63,0xffff0000,v59
v_add_f32 v21,v21,v63
v_lshlrev_b32 v62,16,v60
v_add_f32 v22,v22,v62
v_and_b32 v63,0xffff0000,v60
v_add_f32 v23,v23,v63
v_lshlrev_b32 v62,16,v61
v_add_f32 v24,v24,v62
v_and_b32 v63,0xffff0000,v61
v_add_f32 v25,v25,v63
v_cvt_pk_bf16_f32 v18,v18,v19
v_cvt_pk_bf16_f32 v19,v20,v21
v_cvt_pk_bf16_f32 v20,v22,v23
v_cvt_pk_bf16_f32 v21,v24,v25
v_lshrrev_b32 v62,8,v0
v_lshl_add_u32 v62,v62,5,v0
v_add_u32 v62,s73,v62
ds_write_b128 v62,v[18:21] offset:0
ds_read2_b32 v[22:23],v5 offset0:0 offset1:16
ds_read2_b32 v[24:25],v5 offset0:32 offset1:48
s_mov_b64 s[40:41],s[86:87]
s_mov_b32 s42,4194304
s_mov_b32 s43,0x00020000
L 0
s_add_u32 s64,s75,0
buffer_atomic_pk_add_bf16 v22,v9,s[40:43],s64 offen sc1
s_add_u32 s64,s75,8192
buffer_atomic_pk_add_bf16 v23,v9,s[40:43],s64 offen sc1
s_add_u32 s64,s75,16384
buffer_atomic_pk_add_bf16 v24,v9,s[40:43],s64 offen sc1
s_add_u32 s64,s75,24576
buffer_atomic_pk_add_bf16 v25,v9,s[40:43],s64 offen sc1
s_branch Lg4_done_%=
Lg4_done_%=:
s_nop 0
s_barrier
buffer_store_dword v4,v3,s[36:39],s70 offen offset:0 sc0 sc1
s_waitcnt vmcnt(1)
s_barrier
buffer_store_dword v4,v3,s[36:39],s72 offen offset:0 sc0 sc1
s_waitcnt vmcnt(0) lgkmcnt(0)
s_barrier
s_endpgm
Lcode_g4_e:
.purgem M
.purgem R
.purgem T
.purgem D
.purgem L
)"
  :: "s"(ws), "s"(sWave), "s"(sG0), "s"(sStep), "s"(sOutOff), "s"(sFoutOff), "s"(sAuxOff), "s"(sWw), "s"(sBuf), "s"(sRk), "s"(sExp), "v"(vL16), "v"(vFlag), "v"(vSnapOff), "v"(vSnapRd), "v"(vWd0), "v"(vWd1), "v"(vWr0), "v"(vWr1), "v"(vSp), "v"(vSn), "s"(w4), "s"(out) : "v0", "v1", "v2", "v3", "v4", "v5", "v6", "v7", "v8", "v9", "v10", "v11", "v12", "v13", "v14", "v15", "v16", "v17", "v18", "v19", "v20", "v21", "v22", "v23", "v24", "v25", "v26", "v27", "v28", "v29", "v30", "v31", "v32", "v33", "v34", "v35", "v36", "v37", "v38", "v39", "v40", "v41", "v42", "v43", "v44", "v45", "v46", "v47", "v48", "v49", "v50", "v51", "v52", "v53", "v54", "v55", "v56", "v57", "v58", "v59", "v60", "v61", "v62", "v63", "v64", "v65", "v66", "v67", "v68", "v69", "v70", "v71", "v72", "v73", "v74", "v75", "v76", "v77", "v78", "v79", "v80", "v81", "v82", "v83", "v84", "v85", "v86", "v87", "v88", "v89", "v90", "v91", "v92", "v93", "v94", "v95", "v96", "v97", "v98", "v99", "v100", "v101", "v102", "v103", "v104", "v105", "v106", "v107", "v108", "v109", "v110", "v111", "v112", "v113", "v114", "v115", "v116", "v117", "v118", "v119", "v120", "v121", "v122", "v123", "v124", "v125", "v126", "v127", "v128", "v129", "v130", "v131", "v132", "v133", "v134", "v135", "v136", "v137", "v138", "v139", "v140", "v141", "v142", "v143", "v144", "v145", "v146", "v147", "v148", "v149", "v150", "v151", "v152", "v153", "v154", "v155", "v156", "v157", "v158", "v159", "v160", "v161", "v162", "v163", "v164", "v165", "v166", "v167", "v168", "v169", "v170", "v171", "v172", "v173", "v174", "v175", "v176", "v177", "v178", "v179", "v180", "v181", "v182", "v183", "v184", "v185", "v186", "v187", "v188", "v189", "v190", "v191", "v192", "v193", "v194", "v195", "v196", "v197", "v198", "v199", "v200", "v201", "v202", "v203", "v204", "v205", "v206", "v207", "v208", "v209", "v210", "v211", "v212", "v213", "v214", "v215", "v216", "v217", "v218", "v219", "v220", "v221", "v222", "v223", "v224", "v225", "v226", "v227", "v228", "v229", "v230", "v231", "v232", "v233", "v234", "v235", "v236", "v237", "v238", "v239", "a0", "a1", "a2", "a3", "a4", "a5", "a6", "a7", "a8", "a9", "a10", "a11", "a12", "a13", "a14", "a15", "a16", "a17", "a18", "a19", "a20", "a21", "a22", "a23", "a24", "a25", "a26", "a27", "a28", "a29", "a30", "a31", "a32", "a33", "a34", "a35", "a36", "a37", "a38", "a39", "a40", "a41", "a42", "a43", "a44", "a45", "a46", "a47", "a48", "a49", "a50", "a51", "a52", "a53", "a54", "a55", "a56", "a57", "a58", "a59", "a60", "a61", "a62", "a63", "a64", "a65", "a66", "a67", "a68", "a69", "a70", "a71", "a72", "a73", "a74", "a75", "a76", "a77", "a78", "a79", "a80", "a81", "a82", "a83", "a84", "a85", "a86", "a87", "a88", "a89", "a90", "a91", "a92", "a93", "a94", "a95", "a96", "a97", "a98", "a99", "a100", "a101", "a102", "a103", "a104", "a105", "a106", "a107", "a108", "a109", "a110", "a111", "a112", "a113", "a114", "a115", "a116", "a117", "a118", "a119", "a120", "a121", "a122", "a123", "a124", "a125", "a126", "a127", "a128", "a129", "a130", "a131", "a132", "a133", "a134", "a135", "a136", "a137", "a138", "a139", "a140", "a141", "a142", "a143", "a144", "a145", "a146", "a147", "a148", "a149", "a150", "a151", "a152", "a153", "a154", "a155", "a156", "a157", "a158", "a159", "a160", "a161", "a162", "a163", "a164", "a165", "a166", "a167", "a168", "a169", "a170", "a171", "a172", "a173", "a174", "a175", "a176", "a177", "a178", "a179", "a180", "a181", "a182", "a183", "a184", "a185", "a186", "a187", "a188", "a189", "a190", "a191", "a192", "a193", "a194", "a195", "a196", "a197", "a198", "a199", "a200", "a201", "a202", "a203", "a204", "a205", "a206", "a207", "a208", "a209", "a210", "a211", "a212", "a213", "a214", "a215", "a216", "a217", "a218", "a219", "a220", "a221", "a222", "a223", "a224", "a225", "a226", "a227", "a228", "a229", "a230", "a231", "a232", "a233", "a234", "a235", "a236", "a237", "a238", "a239", "a240", "a241", "a242", "a243", "a244", "a245", "a246", "a247", "a248", "a249", "a250", "a251", "a252", "a253", "a254", "a255", "s36", "s37", "s38", "s39", "s40", "s41", "s42", "s43", "s44", "s45", "s46", "s47", "s48", "s49", "s50", "s51", "s52", "s53", "s54", "s55", "s56", "s57", "s58", "s59", "s60", "s61", "s62", "s63", "s64", "s65", "s66", "s67", "s68", "s69", "s70", "s71", "s72", "s73", "s74", "s75", "s76", "s77", "s78", "s79", "s80", "s81", "s82", "s83", "s84", "s85", "s86", "s87", "s88", "s89", "s90", "s91", "s92", "s93", "s94", "s95", "s96", "s97", "vcc", "scc", "memory");
    __builtin_unreachable();
  } else {
    LANE_ID(lane2);
    constexpr int NG = 32 + (7 - NR4) * 16, T1 = RLN * NRW;
    if (kind == 6) for (int t = slice; t < T1; t += NRW) row_last(ws, out, t, lane2, wave, ep);
    for (int t = T1 + (kind == 6 ? NG + slice : kind == 2 ? rep * 8 + slice : 32 + (3 - rep) * 8 + slice); t < LAST_TASKS; t += NG + NRW) row_last(ws, out, t, lane2, wave, ep);
    if (b == 65 && wave == 0) set_flag(ws + OFF_EP, lane2, ep);
    CTS(14);
    asm volatile("s_endpgm\nLcode_end:" ::: "memory");
    __builtin_unreachable();
  }
}

extern "C" void kernel_launch(void* const* d_in, const int* in_sizes, int n_in, void* d_out, int out_size,
                              void* d_ws, size_t ws_size, hipStream_t stream) {
  const u16* q = (const u16*)d_in[0];
  const u16* k = (const u16*)d_in[1];
  const u16* v = (const u16*)d_in[2];
  const u16* shift_k = (const u16*)d_in[3];
  const u16* ssm_k = (const u16*)d_in[4];
  const u16* ssm_D = (const u16*)d_in[5];
  const u16* w1 = (const u16*)d_in[7];
  const u16* w2 = (const u16*)d_in[8];
  const u16* w3 = (const u16*)d_in[9];
  const u16* w4 = (const u16*)d_in[10];
  flow_kernel<<<256, 256, 0, stream>>>(q, k, v, shift_k, ssm_k, ssm_D, w1, w2, w3, w4, (char*)d_ws, (u16*)d_out);
}
```

```cpp
#include <hip/hip_runtime.h>
#include <hip/hip_bf16.h>
#include <stdint.h>
#include <type_traits>

typedef short s16x8 __attribute__((ext_vector_type(8)));
typedef short s16x4 __attribute__((ext_vector_type(4)));
typedef float f32x4 __attribute__((ext_vector_type(4)));
typedef unsigned short u16;
typedef unsigned short u16x8 __attribute__((ext_vector_type(8)));
typedef unsigned short u16x4 __attribute__((ext_vector_type(4)));
typedef float f32x2 __attribute__((ext_vector_type(2)));
typedef __bf16 bf16x2 __attribute__((ext_vector_type(2)));
typedef unsigned u32x4v __attribute__((ext_vector_type(4)));
typedef __attribute__((address_space(1))) unsigned gu32;

constexpr int S_ = 2048, D_ = 1024, F_ = 3584;
#define PF 1

constexpr int WS_USED = 111243392, LAST_TASKS = 228, U0 = 214, NR4 = 7;
constexpr int OFF_KVR = 4194304, OFF_XP = 8388608, OFF_HS = 17825792, OFF_X1 = 31457280, OFF_G = 40894464, OFF_P0 = 71303168, OFF_P1 = 89128960, OFF_Y = 98566144, OFF_S1 = 106954752, OFF_ST = 107479040, OFF_FL = 109051904, OFF_FXP = 109056000, OFF_FH = 109334528, OFF_FX1 = 109613056, OFF_FG = 109891584, OFF_FP = 110587904, OFF_FKV = 111144960, OFF_FY = 111177728, OFF_EP = 111243264, OFF_DBG = 115343360;

#define WS_DIAG 0

__device__ __forceinline__ float bf2f(u16 v) { return __uint_as_float(((unsigned)v) << 16); }
__device__ __forceinline__ u16 f2bf(float f) { return __builtin_bit_cast(u16, (__bf16)f); }
__device__ __forceinline__ float rbf(float f) { return bf2f(f2bf(f)); }
__device__ __forceinline__ void st16(void* p, u16x8 v) {
  asm volatile("global_store_dwordx4 %0, %1, off sc0 sc1\n\ts_nop 1" :: "v"(p), "v"(v) : "memory");
}
#define WAIT_VM(n) asm volatile("s_waitcnt vmcnt(%0)" ::"n"(n) : "memory")
#define WAIT_LGKM0() asm volatile("s_waitcnt lgkmcnt(0)" ::: "memory")

#define CODE_RANGE(B, E, p, n)                                                                                   \
  const char* p; unsigned n;                                                                                     \
  { unsigned lo_, hi_;                                                                                           \
    asm volatile("s_getpc_b64 s[98:99]\nLpc%=:\n\ts_add_u32 %0, s98, (" B "-Lpc%=)&4294967295\n\ts_addc_u32 %1, s99, (" B "-Lpc%=)>>32\n\t" \
                 "s_mov_b32 %2, " E "-" B : "=s"(lo_), "=s"(hi_), "=s"(n) :: "s98", "s99", "scc");               \
    p = (const char*)(((unsigned long long)hi_ << 32) | lo_); }
__device__ __forceinline__ unsigned touch(const char* p, unsigned n, unsigned off) {
  unsigned t;
  asm volatile("global_load_dword %0, %1, off" : "=v"(t) : "v"(p + (off < n ? off : 0u)) : "memory");
  return t;
}

__device__ __forceinline__ void touch_lds(const char* p, unsigned a) {
  asm volatile("s_mov_b32 m0, %1\n\ts_nop 0\n\tglobal_load_lds_dword %0, off" :: "v"(p), "s"(a) : "memory");
}
#define PFPOL ""
__device__ __forceinline__ void touch_pol(const char* p, unsigned a) {
  asm volatile("s_mov_b32 m0, %1\n\ts_nop 0\n\tglobal_load_lds_dword %0, off " PFPOL :: "v"(p), "s"(a) : "memory");
}
#define YSLEEP 4
#define KSLEEP 2
#define CTS(i)
__device__ __forceinline__ f32x2 up2(unsigned p) { return (f32x2){__uint_as_float(p << 16), __uint_as_float(p & 0xffff0000u)}; }
__device__ __forceinline__ unsigned pk2(f32x2 v) { return __builtin_bit_cast(unsigned, __builtin_convertvector(v, bf16x2)); }
__device__ __forceinline__ f32x2 rbf2(f32x2 v) { return up2(pk2(v)); }
template <int N, class F> __device__ __forceinline__ void sfor(F&& f) {
  if constexpr (N > 0) { sfor<N - 1>(f); f(std::integral_constant<int, N - 1>{}); }
}
__device__ __forceinline__ void set_flag(char* p, int i, unsigned ep) {
  if (i < 32) __hip_atomic_store((gu32*)p + i, ep, __ATOMIC_RELAXED, __HIP_MEMORY_SCOPE_AGENT);
}
template <int NL>
__device__ __forceinline__ void poll_flags(const char* p, int n, int lane, unsigned ep) {
  for (int it = 0; it < 20000; ++it) {
    bool ok = true;
#pragma unroll
    for (int i = 0; i < NL; ++i) {
      const int f = lane + 64 * i;
      ok &= __hip_atomic_load((gu32*)(p + (f < n ? f : 0) * 128), __ATOMIC_RELAXED, __HIP_MEMORY_SCOPE_AGENT) == ep;
    }
    if (__all(ok)) break;
    __builtin_amdgcn_s_sleep(YSLEEP);
  }
  asm volatile("" ::: "memory");
}

constexpr int CV_HP = (2048 + 32) * 2, CV_UR = (2048 + 256) * 2, CV_CH = 2 * CV_HP + CV_UR;
constexpr int NCV = 174;
constexpr int CV_STG = 8 * CV_CH;
constexpr int YP = D_ * 16;

struct KvRegs { u32x4v k0, k1, k2, k3, k4, k5, k6, v0, v1, v2, v3, s0, s1, s2, s3; };
#define KV_LOADS                                                                                                                       \
  "v_lshrrev_b32 %[a], 3, %[tid]\n\tv_and_b32 %[c], 7, %[tid]\n\ts_lshr_b32 s90, %[tile], 4\n\ts_and_b32 s91, %[tile], 15\n\t"          \
  "v_lshlrev_b32 %[a], 13, %[a]\n\tv_lshlrev_b32 %[c], 4, %[c]\n\ts_lshl_b32 s90, s90, 18\n\ts_lshl_b32 s91, s91, 7\n\t"                 \
  "v_add_u32 %[a], s90, %[a]\n\tv_add_u32 %[c], s91, %[c]\n\tv_subrev_u32 %[h], 0x1000, %[a]\n\tv_max_i32 %[h], 0x800, %[h]\n\t"         \
  "v_add3_u32 %[a], %[a], %[c], s92\n\tv_add_u32 %[h], %[h], %[c]\n\tv_lshlrev_b32 %[c], 2, %[c]\n\ts_waitcnt lgkmcnt(0)\n\t"            \
  "global_load_dwordx4 %3, %[a], s[76:77] offset:-4096 nt\n\tglobal_load_dwordx4 %4, %[a], s[76:77] offset:-2048 nt\n\t"                \
  "global_load_dwordx4 %5, %[a], s[76:77] nt\n\tglobal_load_dwordx4 %6, %[a], s[76:77] offset:2048 nt\n\t"                              \
  "global_load_dwordx4 %7, %[a], s[78:79] offset:-4096 nt\n\tglobal_load_dwordx4 %8, %[a], s[78:79] offset:-2048 nt\n\t"                \
  "global_load_dwordx4 %9, %[a], s[78:79] nt\n\tglobal_load_dwordx4 %10, %[a], s[78:79] offset:2048 nt\n\t"                             \
  "global_load_dwordx4 %0, %[h], s[76:77] offset:-2048 nt\n\tglobal_load_dwordx4 %1, %[h], s[76:77] nt\n\t"                             \
  "global_load_dwordx4 %2, %[h], s[76:77] offset:2048 nt\n\t"                                                                           \
  "global_load_dwordx4 %11, %[c], s[80:81]\n\tglobal_load_dwordx4 %12, %[c], s[80:81] offset:16\n\t"                                    \
  "global_load_dwordx4 %13, %[c], s[80:81] offset:32\n\tglobal_load_dwordx4 %14, %[c], s[80:81] offset:48\n\t"
#define KV_OUTS(R) "=&v"(R.k0), "=&v"(R.k1), "=&v"(R.k2), "=&v"(R.k3), "=&v"(R.k4), "=&v"(R.k5), "=&v"(R.k6), "=&v"(R.v0), "=&v"(R.v1), "=&v"(R.v2), \
                   "=&v"(R.v3), "=&v"(R.s0), "=&v"(R.s1), "=&v"(R.s2), "=&v"(R.s3), [a] "=&v"(ta), [h] "=&v"(th), [c] "=&v"(tc)
#define KV_CLOB "s74", "s75", "s76", "s77", "s78", "s79", "s80", "s81", "s82", "s83", "s90", "s91", "s92", "scc", "vcc", "memory"
template <int N>
__device__ __forceinline__ void kv_wait(KvRegs& R) {
  asm volatile("s_waitcnt vmcnt(%15)" : "+v"(R.k0), "+v"(R.k1), "+v"(R.k2), "+v"(R.k3), "+v"(R.k4), "+v"(R.k5), "+v"(R.k6), "+v"(R.v0), "+v"(R.v1), "+v"(R.v2),
               "+v"(R.v3), "+v"(R.s0), "+v"(R.s1), "+v"(R.s2), "+v"(R.s3) : "n"(N) : "memory");
}
__device__ __forceinline__ void kv_store(KvRegs& R, char* __restrict__ ws, char* T, int b, int tid) {
  const int t0 = (b >> 4) * 128, c0 = (b & 15) * 64, ch8 = tid & 7, rg = tid >> 3;
  if (t0 + 4 * rg < 3) R.k0 = R.k1 = R.k2 = (u32x4v){0u, 0u, 0u, 0u};
  typedef unsigned u32x2 __attribute__((ext_vector_type(2)));
  const u32x4v kr[7] = {R.k0, R.k1, R.k2, R.k3, R.k4, R.k5, R.k6}, vr[4] = {R.v0, R.v1, R.v2, R.v3}, sk[4] = {R.s0, R.s1, R.s2, R.s3};
#pragma unroll
  for (int j2 = 0; j2 < 4; ++j2) {
    const f32x2 sa = up2(sk[j2][0]), sb = up2(sk[j2][1]), sc = up2(sk[j2][2]), sd = up2(sk[j2][3]);
    const f32x2 s0 = {sa[0], sc[0]}, s1 = {sa[1], sc[1]}, s2 = {sb[0], sd[0]}, s3 = {sb[1], sd[1]};
    f32x2 kf[7], o[4];
#pragma unroll
    for (int r = 0; r < 7; ++r) kf[r] = up2(kr[r][j2]);
#pragma unroll
    for (int r = 0; r < 4; ++r)
      o[r] = __builtin_elementwise_fma(kf[r + 3], s0, __builtin_elementwise_fma(kf[r + 2], s1, __builtin_elementwise_fma(kf[r + 1], s2, kf[r] * s3))) * up2(vr[r][j2]);
#pragma unroll
    for (int e = 0; e < 2; ++e)
      *(u32x2*)(T + (8 * ch8 + 2 * j2 + e) * 264 + (124 - 4 * rg) * 2) = (u32x2){pk2((f32x2){o[3][e], o[2][e]}), pk2((f32x2){o[1][e], o[0][e]})};
  }
  __syncthreads();
  u16* kvr = (u16*)(ws + OFF_KVR);
#pragma unroll
  for (int i = 0; i < 4; ++i) {
    const int idx = tid + 256 * i, ch = idx >> 4, cb = idx & 15;
    const u16x4 lo = *(const u16x4*)(T + ch * 264 + cb * 16), hi = *(const u16x4*)(T + ch * 264 + cb * 16 + 8);
    st16(kvr + (size_t)(c0 + ch) * S_ + (1920 - t0) + cb * 8, __builtin_shufflevector(lo, hi, 0, 1, 2, 3, 4, 5, 6, 7));
  }
}

#define LASTSLEEP 4
__device__ __forceinline__ void wait_flags(const char* p, unsigned ep) {
  gu32* f = (gu32*)p;
  for (int it = 0; it < 20000; ++it) {
    if (__all(__hip_atomic_load(f, __ATOMIC_RELAXED, __HIP_MEMORY_SCOPE_AGENT) == ep)) break;
    __builtin_amdgcn_s_sleep(LASTSLEEP);
  }
  asm volatile("" ::: "memory");
}
__device__ __forceinline__ void row_last(char* __restrict__ ws, u16* __restrict__ out, int t, int lane, int wave, unsigned ep) {
  const __amdgpu_buffer_rsrc_t rs = __builtin_amdgcn_make_buffer_rsrc(ws, 0, WS_USED, 0x00020000);
  const int r8 = lane & 7, seg = lane >> 3, un = 128 + (t >> 1);
  const unsigned fo = un * 32768 + (wave * 8 + (seg >> 2)) * 1024 + ((seg & 3) * 16 + (t & 1) * 8 + r8) * 16;
  wait_flags(ws + OFF_FP + un * 2048 + ((lane & 2) * 4 + wave * 2 + (lane & 1)) * 128, ep);
  u32x4v p0[4], p1[4];
#pragma unroll
  for (int i = 0; i < 4; ++i) {
    p0[i] = __builtin_bit_cast(u32x4v, __builtin_amdgcn_raw_buffer_load_b128(rs, fo + i * 2048, OFF_P0, 16));
    p1[i] = __builtin_bit_cast(u32x4v, __builtin_amdgcn_raw_buffer_load_b128(rs, fo + i * 2048, OFF_P1, 16));
  }
#pragma unroll
  for (int i = 0; i < 4; ++i) {
    u32x4v o;
#pragma unroll
    for (int e = 0; e < 4; ++e) o[e] = pk2(up2(p0[i][e]) + up2(p1[i][e]));
    st16(out + (size_t)(t * 8 + r8) * D_ + wave * 256 + i * 64 + seg * 8, __builtin_bit_cast(u16x8, o));
  }
}

__device__ __forceinline__ int swzB(int r) { return ((((r >> 1) & 1) | (((r >> 3) & 1) << 1)) << 1); }
#define TCH 1
#define LANE_ID(x) int x; asm volatile("v_mbcnt_lo_u32_b32 %0, -1, 0\n\tv_mbcnt_hi_u32_b32 %0, -1, %0" : "=v"(x))
#define W12 6
#define HL 0
#define W_B1 (b == 1)
#define W_B7 (b == 7)
#define W_I15 (idx == 15)
#define W_I31 (idx == 31)
#define W_B249 (b == 249)
#define RLN 7
#define QTB 1
constexpr int QT0 = 34 * 128;
#define CODE_MAX(x, y) "(((" x ">" y ")&" x ")|((" x "<=" y ")&" y "))"
constexpr int HU = 40960;
#define G3OR 5
constexpr int NRW = 22 - G3OR;
__global__ __launch_bounds__(256, 1) void flow_kernel(const u16* __restrict__ q, const u16* __restrict__ kin, const u16* __restrict__ vin,
                                                      const u16* __restrict__ shift_k, const u16* __restrict__ ssm_k, const u16* __restrict__ ssm_D,
                                                      const u16* __restrict__ w1, const u16* __restrict__ w2,
                                                      const u16* __restrict__ w3, const u16* __restrict__ w4, char* __restrict__ ws,
                                                      u16* __restrict__ out) {
  __shared__ __attribute__((aligned(16))) char lds[163840];
  KvRegs KR;
  unsigned ew, ta, th, tc;
  int b, tile;
  asm volatile("Lcode_b:\n\ts_load_dwordx4 s[76:79], %[ka], 0x8\n\ts_load_dwordx2 s[82:83], %[ka], 0x50\n\t"
               "s_getpc_b64 s[74:75]\n\ts_lshr_b32 s90, %[bi], 3\n\ts_mov_b32 m0, 0x27f00\n\tv_lshlrev_b32 %[a], 5, %[tid]\n\tv_add_lshl_u32 %[a], %[a], s90, 7\n\t"
               "v_min_u32 %[a], " CODE_MAX(CODE_MAX(CODE_MAX("(Lcode_g3_e-Lcode_b)", "(Lcode_g3o_e-Lcode_b)"), CODE_MAX("(Lcode_g4_e-Lcode_b)", "(Lcode_g12_e-Lcode_b)")), "(Lcode_end-Lcode_b)") "-64, %[a]\n\t"
               "global_load_lds_dword %[a], s[74:75]\n\ts_load_dwordx2 s[80:81], %[ka], 0x18\n\t"
               "s_mov_b32 %[b], %[bi]\n\ts_bfe_u32 s91, %[bi], 0x20001\n\ts_lshl2_add_u32 %[tile], s90, s91\n\ts_movk_i32 s92, 0x1000\n\t"
               "s_and_b32 s90, %[bi], 128\n\ts_bitcmp1_b32 %[bi], 0\n\ts_cselect_b32 s90, s90, 64\n\ts_add_u32 %[tile], %[tile], s90\n\t"
               KV_LOADS "v_mov_b32 %[a], %[eo]\n\tglobal_load_dword %[ew], %[a], s[82:83] sc1"
               : KV_OUTS(KR), [ew] "=&v"(ew), [b] "=&s"(b), [tile] "=&s"(tile)
               : [ka] "s"(__builtin_amdgcn_kernarg_segment_ptr()), [bi] "s"(blockIdx.x), [tid] "v"(threadIdx.x), [eo] "n"(OFF_EP) : KV_CLOB);
  const unsigned t_start = 0;
  const int tid = threadIdx.x, lane = tid & 63, wave = __builtin_amdgcn_readfirstlane(tid >> 6);
  const int xcd = b & 7, idx = b >> 3;
  const unsigned sLds = (unsigned)(size_t)lds;
  const int o8 = idx & 7;
  int kind, rep, slice, kh = 0;
  int j3 = 0, j4 = 0;
  const int xp = xcd >> 1, pr = xp & 1;
  if (!(xcd & 1)) {
    if (idx < 20) { kind = 4; rep = idx < 16 ? (xp >> 1) * 3 + (idx >> 3) : 2; kh = pr; slice = idx < 16 ? o8 : (xp >> 1) * 4 + o8; j4 = rep * 16 + kh * 8 + slice; }
    else { kind = 3; rep = pr * 2 + (idx >= 26); slice = (xp >> 1) * 6 + idx - (idx >= 26 ? 26 : 20); j3 = rep * 18 + slice; }
  } else if (idx < 16) { kind = 1 + (idx >> 3); rep = xp; slice = o8; }
  else {
    const int k = idx - 16, n3 = xp < 2 ? 12 : 9, no = xp < 2 ? 0 : 5 - xp;
    if (k < n3) { kind = 3; rep = xp < 2 ? pr * 2 + (k >= 6) : 4; slice = xp < 2 ? 12 + k - 6 * (k >= 6) : pr * 9 + k; j3 = rep * 18 + slice; }
    else if (k < n3 + no) { kind = 5; rep = pr * 3 + k - n3; slice = 18; j3 = 90 + rep; }
    else { kind = 6; rep = 0; slice = xp * 4 + k - n3 - no; }
  }
  CTS(0);
  const unsigned sScr = sLds + 163584;
  unsigned ep = 0;
  auto w_touch = [&](const u16* w) {
#pragma unroll
    for (int i = 0; i < 2; ++i) touch_lds((const char*)w + (size_t)(rep * 256 + i * 128 + (tid >> 1)) * 2048 + slice * 256 + (tid & 1) * 128, sScr);
  };
  {
  const bool isconv = kind >= 3 && kind != 6 && j3 < 94;
  const int nw = (kind == 4 || j3 >= 92) ? 2 : 1, c0 = kind == 4 ? 8 * j4 : j3 < 92 ? 640 + 4 * j3 : j3 < 94 ? 272 + 8 * j3 : 0, cw = c0 + nw * wave;
  const unsigned hd = isconv ? sLds + 2 * wave * CV_CH + 32 : 0x30000u;
  auto h_dma = [&](bool first) {
#pragma unroll
    for (int j = 0; j < 2; ++j) {
      const u16* hp = ssm_k + (size_t)cw * S_ + (isconv && j < nw ? j * S_ + lane * 8 : 0);
      if (first) asm volatile("s_mov_b32 m0, %1\n\ts_nop 0\n\tglobal_load_lds_dwordx4 %0, off nt" :: "v"(hp), "s"(hd + j * CV_CH) : "memory");
      else asm volatile("s_mov_b32 m0, %1\n\ts_nop 0\n\tglobal_load_lds_dwordx4 %0, off offset:1024 nt\n\t"
                        "global_load_lds_dwordx4 %0, off offset:2048 nt\n\tglobal_load_lds_dwordx4 %0, off offset:3072 nt" :: "v"(hp), "s"(hd + j * CV_CH) : "memory");
    }
  };
  h_dma(true);
  if (HL == 0) h_dma(false);
  *(u32x4v*)(lds + (2 * wave + (lane >> 5)) * CV_CH + 2 * CV_HP + 4096 + (lane & 31) * 16) = (u32x4v){0u, 0u, 0u, 0u};
  if (lane < 8) *(u32x4v*)(lds + (2 * wave + (lane >> 2)) * CV_CH + ((lane >> 1) & 1) * CV_HP + (lane & 1) * 16) = (u32x4v){0u, 0u, 0u, 0u};
  kv_wait<HL ? 3 : 9>(KR);
  CTS(12);
  kv_store(KR, ws, lds + CV_STG, tile, tid);
  CTS(1);
  if (HL == 1) h_dma(false);
  const unsigned dw = ((const unsigned*)ssm_D)[cw >> 1];
  asm volatile("s_waitcnt vmcnt(%1)" : "+v"(ew) : "n"(HL == 1 ? 6 : 0) : "memory");
  ep = __builtin_amdgcn_readfirstlane(ew) + 1u;
  if (ep == 0u) ep = 1u;
  __builtin_amdgcn_s_barrier();
  set_flag(ws + OFF_FKV + tile * 128, tid, ep);
  CTS(2);
  if (HL == 2) h_dma(false);
  if (kind < 3 && (W12 & kind) != 0) w_touch(kind == 1 ? w1 : w2);
  if (kind == 2 && (W12 & 4) != 0) w_touch(w1);
  if (isconv) {
    LANE_ID(lane);
    const int tid = wave * 64 + lane;
    const unsigned sC0 = (cw & 1) ? dw & 0xffff0000u : dw << 16, sC1 = dw & 0xffff0000u;
    const int l15 = lane & 15, lg = lane >> 4, cp = (l15 + 1) & 1, iw = kind == 4 ? j4 : 80 + j3, own = 2 * wave * CV_CH;
    const int ch = tid & (4 * nw - 1), tb = nw == 2 ? tid >> 3 : tid >> 2, slot = nw == 2 ? ch : 2 * ch;
    const unsigned sWave = wave, sBuf = nw, sAuxOff = OFF_KVR + cw * 4096, sFoutOff = OFF_FY + iw * 128, sExp = __builtin_amdgcn_readfirstlane(ep), sDbg = 110u * 1048576u + (b * 4 + wave) * 8192;
    const unsigned vL16 = lane * 16, vPA = own + cp * CV_HP + 2 * (8 * lg + l15 + 1 - cp), vPB = own + 2 * CV_HP + 2 * (2032 - 16 * l15 + 8 * lg) - 512 * 7;
    const unsigned vPU = own + 2 * CV_HP + 2 * (2044 - 16 * l15 - 4 * lg) - 512 * 7, vPW = 32 * l15 + 8 * lg;
    const unsigned vOR = slot * CV_CH + tb * 16, vORA = CV_STG + slot * 1024 + tb * 16, vOG = tb * YP + (c0 + ch) * 16;
    const unsigned vFA = ((lane & 3) * 16 + (c0 >> 6)) * 128, vFB = ((4 + lane % 12) * 16 + (c0 >> 6)) * 128, vFlag = tid < 32 ? tid * 4 : 0x80000000u;
    const unsigned vTW = lane < 42 ? (iw * 168 + wave * 42 + lane) * 128 : 0x80000000u;
    const unsigned vTX = lane < 44 ? ((kind == 4 ? j4 : j3 - 12) * 176 + wave * 44 + lane) * 128 : 0x80000000u;
    (void)sDbg;
    asm volatile(
R"(.macro M d,w,x
v_mfma_f32_16x16x32_bf16 v[\d:\d+3], \w, v[\x:\x+3], v[\d:\d+3]
.endm
.macro R d,a,o
ds_read_b128 v[\d:\d+3], v\a offset:\o
.endm
.macro T d,a,o
ds_read_b64_tr_b16 \d, v\a offset:\o
.endm
.macro D a,s,o
buffer_load_dwordx4 v\a, s[36:39], s\s offen offset:\o sc1 lds
.endm
.macro L c
s_waitcnt lgkmcnt(\c)
.endm
Lcode_cv_b:
s_mov_b64 s[36:37],%0
s_mov_b32 s38,111243392
s_mov_b32 s39,0x00020000
s_mov_b32 s44,%1
s_mov_b32 s51,%2
s_mov_b32 s49,%3
s_mov_b32 s48,%4
s_mov_b32 s86,%5
s_mov_b32 s87,%6
s_mov_b32 s53,%7
v_mov_b32 v0,%8
v_mov_b32 v1,%9
v_mov_b32 v2,%10
v_mov_b32 v3,%11
v_mov_b32 v4,%12
v_mov_b32 v5,%13
v_mov_b32 v6,%14
v_mov_b32 v7,%15
v_mov_b32 v8,%16
v_mov_b32 v9,%17
v_mov_b32 v10,%18
v_mov_b32 v11,%19
v_mov_b32 v12,%20
s_mov_b32 s58,111144960
buffer_load_dword v13,v8,s[36:39],s58 offen offset:0 sc1
s_mul_i32 s57,s44,25856
v_add_u32 v18,s57,v0
v_add_u32 v18,32,v18
v_mov_b32 v19,s57
R 88,18,0
ds_read_b32 v92,v18 offset:16
R 152,18,12928
ds_read_b32 v156,v18 offset:12944
ds_read_b32 v24,v19 offset:32
ds_read_b32 v25,v19 offset:12960
s_mov_b64 s[40:41],%21
s_mov_b32 s42,7340032
s_mov_b32 s43,0x00020000
s_mov_b64 s[60:61],%22
s_mov_b32 s62,7340032
s_mov_b32 s63,0x00020000
v_mov_b32 v23,s53
s_mov_b32 s82,0
s_add_u32 s55,s49,4096
s_cmp_eq_u32 s51,2
s_cselect_b32 s55,s55,0x7fff0000
s_cselect_b32 s56,0,0x70000000
s_add_u32 s57,s57,8320
v_add_u32 v14,1024,v1
v_add_u32 v15,2048,v1
v_add_u32 v16,3072,v1
v_add_u32 v17,12928,v1
L 4
v_alignbyte_b32 v96,v89,v88,2
v_alignbyte_b32 v97,v90,v89,2
v_alignbyte_b32 v98,v91,v90,2
v_alignbyte_b32 v99,v92,v91,2
ds_write_b128 v18,v[96:99] offset:4160
L 3
v_alignbyte_b32 v160,v153,v152,2
v_alignbyte_b32 v161,v154,v153,2
v_alignbyte_b32 v162,v155,v154,2
v_alignbyte_b32 v163,v156,v155,2
ds_write_b128 v18,v[160:163] offset:17088
L 2
v_lshlrev_b32 v24,16,v24
v_lshlrev_b32 v25,16,v25
s_mov_b64 exec,1
ds_write_b32 v19,v24 offset:4188
ds_write_b32 v19,v25 offset:17116
s_mov_b64 exec,-1
ds_read2_b32 a[0:1],v1 offset0:0 offset1:1
ds_read2_b32 a[2:3],v1 offset0:2 offset1:3
ds_read2_b32 a[4:5],v1 offset0:128 offset1:129
ds_read2_b32 a[6:7],v1 offset0:130 offset1:131
ds_read2_b32 a[8:9],v1 offset0:16 offset1:17
ds_read2_b32 a[10:11],v1 offset0:18 offset1:19
ds_read2_b32 a[12:13],v1 offset0:144 offset1:145
ds_read2_b32 a[14:15],v1 offset0:146 offset1:147
ds_read2_b32 a[16:17],v1 offset0:32 offset1:33
ds_read2_b32 a[18:19],v1 offset0:34 offset1:35
ds_read2_b32 a[20:21],v1 offset0:160 offset1:161
ds_read2_b32 a[22:23],v1 offset0:162 offset1:163
ds_read2_b32 a[24:25],v1 offset0:48 offset1:49
ds_read2_b32 a[26:27],v1 offset0:50 offset1:51
ds_read2_b32 a[28:29],v1 offset0:176 offset1:177
ds_read2_b32 a[30:31],v1 offset0:178 offset1:179
ds_read2_b32 a[32:33],v1 offset0:64 offset1:65
ds_read2_b32 a[34:35],v1 offset0:66 offset1:67
ds_read2_b32 a[36:37],v1 offset0:192 offset1:193
ds_read2_b32 a[38:39],v1 offset0:194 offset1:195
ds_read2_b32 a[40:41],v1 offset0:80 offset1:81
ds_read2_b32 a[42:43],v1 offset0:82 offset1:83
ds_read2_b32 a[44:45],v1 offset0:208 offset1:209
ds_read2_b32 a[46:47],v1 offset0:210 offset1:211
ds_read2_b32 a[48:49],v1 offset0:96 offset1:97
ds_read2_b32 a[50:51],v1 offset0:98 offset1:99
ds_read2_b32 a[52:53],v1 offset0:224 offset1:225
ds_read2_b32 a[54:55],v1 offset0:226 offset1:227
ds_read2_b32 a[56:57],v1 offset0:112 offset1:113
ds_read2_b32 a[58:59],v1 offset0:114 offset1:115
ds_read2_b32 a[60:61],v1 offset0:240 offset1:241
ds_read2_b32 a[62:63],v1 offset0:242 offset1:243
ds_read2_b32 a[64:65],v17 offset0:0 offset1:1
ds_read2_b32 a[66:67],v17 offset0:2 offset1:3
ds_read2_b32 a[68:69],v17 offset0:128 offset1:129
ds_read2_b32 a[70:71],v17 offset0:130 offset1:131
ds_read2_b32 a[72:73],v17 offset0:16 offset1:17
ds_read2_b32 a[74:75],v17 offset0:18 offset1:19
ds_read2_b32 a[76:77],v17 offset0:144 offset1:145
ds_read2_b32 a[78:79],v17 offset0:146 offset1:147
ds_read2_b32 a[80:81],v17 offset0:32 offset1:33
ds_read2_b32 a[82:83],v17 offset0:34 offset1:35
ds_read2_b32 a[84:85],v17 offset0:160 offset1:161
ds_read2_b32 a[86:87],v17 offset0:162 offset1:163
ds_read2_b32 a[88:89],v17 offset0:48 offset1:49
ds_read2_b32 a[90:91],v17 offset0:50 offset1:51
ds_read2_b32 a[92:93],v17 offset0:176 offset1:177
ds_read2_b32 a[94:95],v17 offset0:178 offset1:179
ds_read2_b32 a[96:97],v17 offset0:64 offset1:65
ds_read2_b32 a[98:99],v17 offset0:66 offset1:67
ds_read2_b32 a[100:101],v17 offset0:192 offset1:193
ds_read2_b32 a[102:103],v17 offset0:194 offset1:195
ds_read2_b32 a[104:105],v17 offset0:80 offset1:81
ds_read2_b32 a[106:107],v17 offset0:82 offset1:83
ds_read2_b32 a[108:109],v17 offset0:208 offset1:209
ds_read2_b32 a[110:111],v17 offset0:210 offset1:211
ds_read2_b32 a[112:113],v17 offset0:96 offset1:97
ds_read2_b32 a[114:115],v17 offset0:98 offset1:99
ds_read2_b32 a[116:117],v17 offset0:224 offset1:225
ds_read2_b32 a[118:119],v17 offset0:226 offset1:227
ds_read2_b32 a[120:121],v17 offset0:112 offset1:113
ds_read2_b32 a[122:123],v17 offset0:114 offset1:115
ds_read2_b32 a[124:125],v17 offset0:240 offset1:241
ds_read2_b32 a[126:127],v17 offset0:242 offset1:243
Lcv_bpa_%=:
s_waitcnt vmcnt(0)
v_cmp_eq_u32 vcc,s53,v13
s_add_u32 s82,s82,1
s_cmp_eq_u64 vcc,-1
s_cbranch_scc1 Lcv_bqa_%=
s_sleep 1
buffer_load_dword v13,v8,s[36:39],s58 offen offset:0 sc1
s_cmp_lt_u32 s82,20000
s_cbranch_scc1 Lcv_bpa_%=
s_waitcnt vmcnt(0)
Lcv_bqa_%=:
s_add_u32 m0,s57,0
s_nop 0
D 0,49,3072
s_add_u32 m0,s57,12928
s_nop 0
D 0,55,3072
buffer_load_dword v13,v9,s[36:39],s58 offen offset:0 sc1
R 88,18,0
ds_read_b32 v92,v18 offset:16
R 104,18,1024
ds_read_b32 v108,v18 offset:1040
R 120,18,2048
ds_read_b32 v124,v18 offset:2064
R 136,18,3072
ds_read_b32 v140,v18 offset:3088
L 6
v_alignbyte_b32 v96,v89,v88,2
v_alignbyte_b32 v97,v90,v89,2
v_alignbyte_b32 v98,v91,v90,2
v_alignbyte_b32 v99,v92,v91,2
ds_write_b128 v18,v[96:99] offset:4160
L 5
v_alignbyte_b32 v112,v105,v104,2
v_alignbyte_b32 v113,v106,v105,2
v_alignbyte_b32 v114,v107,v106,2
v_alignbyte_b32 v115,v108,v107,2
ds_write_b128 v18,v[112:115] offset:5184
L 4
v_alignbyte_b32 v128,v121,v120,2
v_alignbyte_b32 v129,v122,v121,2
v_alignbyte_b32 v130,v123,v122,2
v_alignbyte_b32 v131,v124,v123,2
ds_write_b128 v18,v[128:131] offset:6208
L 3
v_alignbyte_b32 v144,v137,v136,2
v_alignbyte_b32 v145,v138,v137,2
v_alignbyte_b32 v146,v139,v138,2
v_alignbyte_b32 v147,v140,v139,2
ds_write_b128 v18,v[144:147] offset:7232
R 152,18,12928
ds_read_b32 v156,v18 offset:12944
R 168,18,13952
ds_read_b32 v172,v18 offset:13968
R 184,18,14976
ds_read_b32 v188,v18 offset:14992
R 200,18,16000
ds_read_b32 v204,v18 offset:16016
L 6
v_alignbyte_b32 v160,v153,v152,2
v_alignbyte_b32 v161,v154,v153,2
v_alignbyte_b32 v162,v155,v154,2
v_alignbyte_b32 v163,v156,v155,2
ds_write_b128 v18,v[160:163] offset:17088
L 5
v_alignbyte_b32 v176,v169,v168,2
v_alignbyte_b32 v177,v170,v169,2
v_alignbyte_b32 v178,v171,v170,2
v_alignbyte_b32 v179,v172,v171,2
ds_write_b128 v18,v[176:179] offset:18112
L 4
v_alignbyte_b32 v192,v185,v184,2
v_alignbyte_b32 v193,v186,v185,2
v_alignbyte_b32 v194,v187,v186,2
v_alignbyte_b32 v195,v188,v187,2
ds_write_b128 v18,v[192:195] offset:19136
L 3
v_alignbyte_b32 v208,v201,v200,2
v_alignbyte_b32 v209,v202,v201,2
v_alignbyte_b32 v210,v203,v202,2
v_alignbyte_b32 v211,v204,v203,2
ds_write_b128 v18,v[208:211] offset:20160
s_lshl_b32 s78,s44,11
s_add_u32 s78,s78,103424
v_add_u32 v18,s78,v4
v_add_u32 v4,v19,v4
s_waitcnt vmcnt(0)
v_cmp_eq_u32 vcc,s53,v13
s_mov_b32 s83,0
s_cmp_eq_u64 vcc,-1
s_cbranch_scc0 Lcv_a_%=
s_mov_b32 s83,1
s_add_u32 m0,s57,0
s_nop 0
D 0,49,2048
D 0,49,1024
D 0,49,0
s_add_u32 m0,s57,12928
s_nop 0
D 0,55,2048
D 0,55,1024
D 0,55,0
Lcv_a_%=:
s_cmp_eq_u32 s51,2
s_cbranch_scc1 Lcv_a2_%=
L 0
R 88,2,3584
R 92,2,3072
R 96,2,3648
R 100,2,3136
R 104,2,3712
R 108,2,3200
R 112,2,3776
R 116,2,3264
L 6
v_mfma_f32_16x16x32_bf16 v[40:43],a[0:3],v[88:91],0
R 120,2,3840
R 124,2,3328
v_mfma_f32_16x16x32_bf16 v[44:47],a[0:3],v[92:95],0
M 44,a[4:7],88
L 6
M 40,a[8:11],96
R 128,2,3904
R 132,2,3392
M 44,a[8:11],100
M 44,a[12:15],96
L 6
M 40,a[16:19],104
R 136,2,3968
R 140,2,3456
M 44,a[16:19],108
M 44,a[20:23],104
L 6
M 40,a[24:27],112
R 144,2,4032
R 148,2,3520
M 44,a[24:27],116
M 44,a[28:31],112
L 6
M 40,a[32:35],120
M 44,a[32:35],124
M 44,a[36:39],120
L 4
M 40,a[40:43],128
M 44,a[40:43],132
M 44,a[44:47],128
L 2
M 40,a[48:51],136
ds_read_b64 v[24:25],v3 offset:3584
ds_read_b64 v[26:27],v3 offset:3072
M 44,a[48:51],140
M 44,a[52:55],136
L 2
M 40,a[56:59],144
M 44,a[56:59],148
M 44,a[60:63],144
L 0
v_and_b32 v36,0xffff0000,v25
v_lshlrev_b32 v37,16,v25
v_and_b32 v38,0xffff0000,v24
v_lshlrev_b32 v39,16,v24
s_nop 0
v_fma_f32 v40,v36,s86,v40
v_fma_f32 v41,v37,s86,v41
v_fma_f32 v42,v38,s86,v42
v_fma_f32 v43,v39,s86,v43
v_cvt_pk_bf16_f32 v40,v40,v41
v_cvt_pk_bf16_f32 v41,v42,v43
ds_write_b64 v18,v[40:41] offset:0
v_and_b32 v36,0xffff0000,v27
v_lshlrev_b32 v37,16,v27
v_and_b32 v38,0xffff0000,v26
v_lshlrev_b32 v39,16,v26
v_fma_f32 v44,v36,s86,v44
v_fma_f32 v45,v37,s86,v45
v_fma_f32 v46,v38,s86,v46
v_fma_f32 v47,v39,s86,v47
v_cvt_pk_bf16_f32 v44,v44,v45
v_cvt_pk_bf16_f32 v45,v46,v47
ds_write_b64 v18,v[44:45] offset:512
s_branch Lcv_a3_%=
Lcv_a2_%=:
L 0
R 88,2,3584
R 92,2,3072
R 96,2,3648
R 100,2,3136
R 104,2,3712
R 108,2,3200
R 112,2,3776
R 116,2,3264
L 6
v_mfma_f32_16x16x32_bf16 v[40:43],a[0:3],v[88:91],0
R 120,2,3840
R 124,2,3328
v_mfma_f32_16x16x32_bf16 v[44:47],a[0:3],v[92:95],0
M 44,a[4:7],88
L 6
M 40,a[8:11],96
R 128,2,3904
R 132,2,3392
M 44,a[8:11],100
M 44,a[12:15],96
L 6
M 40,a[16:19],104
R 136,2,3968
R 140,2,3456
M 44,a[16:19],108
M 44,a[20:23],104
L 6
M 40,a[24:27],112
R 144,2,4032
R 148,2,3520
M 44,a[24:27],116
M 44,a[28:31],112
L 6
M 40,a[32:35],120
R 152,2,16512
R 156,2,16000
M 44,a[32:35],124
M 44,a[36:39],120
L 6
M 40,a[40:43],128
R 160,2,16576
R 164,2,16064
M 44,a[40:43],132
M 44,a[44:47],128
L 6
M 40,a[48:51],136
R 168,2,16640
R 172,2,16128
M 44,a[48:51],140
ds_read_b64 v[24:25],v3 offset:3584
ds_read_b64 v[26:27],v3 offset:3072
M 44,a[52:55],136
L 8
M 40,a[56:59],144
R 176,2,16704
R 180,2,16192
M 44,a[56:59],148
M 44,a[60:63],144
L 8
v_mfma_f32_16x16x32_bf16 v[48:51],a[64:67],v[152:155],0
R 184,2,16768
R 188,2,16256
v_mfma_f32_16x16x32_bf16 v[52:55],a[64:67],v[156:159],0
M 52,a[68:71],152
L 8
M 48,a[72:75],160
R 192,2,16832
R 196,2,16320
M 52,a[72:75],164
L 6
v_and_b32 v36,0xffff0000,v25
M 52,a[76:79],160
v_lshlrev_b32 v37,16,v25
v_and_b32 v38,0xffff0000,v24
v_lshlrev_b32 v39,16,v24
s_nop 0
M 48,a[80:83],168
R 200,2,16896
R 204,2,16384
M 52,a[80:83],172
v_fma_f32 v40,v36,s86,v40
v_fma_f32 v41,v37,s86,v41
M 52,a[84:87],168
v_fma_f32 v42,v38,s86,v42
v_fma_f32 v43,v39,s86,v43
v_cvt_pk_bf16_f32 v40,v40,v41
L 6
M 48,a[88:91],176
R 208,2,16960
R 212,2,16448
M 52,a[88:91],180
v_cvt_pk_bf16_f32 v41,v42,v43
ds_write_b64 v18,v[40:41] offset:0
M 52,a[92:95],176
v_and_b32 v36,0xffff0000,v27
v_lshlrev_b32 v37,16,v27
v_and_b32 v38,0xffff0000,v26
L 7
M 48,a[96:99],184
M 52,a[96:99],188
v_lshlrev_b32 v39,16,v26
v_fma_f32 v44,v36,s86,v44
M 52,a[100:103],184
v_fma_f32 v45,v37,s86,v45
v_fma_f32 v46,v38,s86,v46
v_fma_f32 v47,v39,s86,v47
L 5
M 48,a[104:107],192
M 52,a[104:107],196
v_cvt_pk_bf16_f32 v44,v44,v45
v_cvt_pk_bf16_f32 v45,v46,v47
M 52,a[108:111],192
ds_write_b64 v18,v[44:45] offset:512
L 4
M 48,a[112:115],200
ds_read_b64 v[24:25],v3 offset:16512
ds_read_b64 v[26:27],v3 offset:16000
M 52,a[112:115],204
M 52,a[116:119],200
L 4
M 48,a[120:123],208
M 52,a[120:123],212
M 52,a[124:127],208
L 0
v_and_b32 v36,0xffff0000,v25
v_lshlrev_b32 v37,16,v25
v_and_b32 v38,0xffff0000,v24
v_lshlrev_b32 v39,16,v24
s_nop 0
v_fma_f32 v48,v36,s87,v48
v_fma_f32 v49,v37,s87,v49
v_fma_f32 v50,v38,s87,v50
v_fma_f32 v51,v39,s87,v51
v_cvt_pk_bf16_f32 v48,v48,v49
v_cvt_pk_bf16_f32 v49,v50,v51
ds_write_b64 v18,v[48:49] offset:1024
v_and_b32 v36,0xffff0000,v27
v_lshlrev_b32 v37,16,v27
v_and_b32 v38,0xffff0000,v26
v_lshlrev_b32 v39,16,v26
v_fma_f32 v52,v36,s87,v52
v_fma_f32 v53,v37,s87,v53
v_fma_f32 v54,v38,s87,v54
v_fma_f32 v55,v39,s87,v55
v_cvt_pk_bf16_f32 v52,v52,v53
v_cvt_pk_bf16_f32 v53,v54,v55
ds_write_b64 v18,v[52:53] offset:1536
Lcv_a3_%=:
L 0
s_barrier
R 152,6,0
R 156,6,512
s_mov_b32 s78,98566144
L 1
buffer_store_dwordx4 v[152:155],v7,s[36:39],s78 offen offset:0 sc0 sc1
s_add_u32 s78,s56,99090432
L 0
buffer_store_dwordx4 v[156:159],v7,s[36:39],s78 offen offset:0 sc0 sc1
s_cmp_eq_u32 s83,1
s_cbranch_scc1 Lcv_b_%=
s_waitcnt vmcnt(0)
s_barrier
buffer_store_dword v23,v10,s[36:39],s48 offen offset:0 sc0 sc1
buffer_load_dword v13,v9,s[36:39],s58 offen offset:0 sc1
Lcv_bpb_%=:
)"
R"(s_waitcnt vmcnt(0)
v_cmp_eq_u32 vcc,s53,v13
s_add_u32 s82,s82,1
s_cmp_eq_u64 vcc,-1
s_cbranch_scc1 Lcv_bqb_%=
s_sleep 1
buffer_load_dword v13,v9,s[36:39],s58 offen offset:0 sc1
s_cmp_lt_u32 s82,20000
s_cbranch_scc1 Lcv_bpb_%=
s_waitcnt vmcnt(0)
Lcv_bqb_%=:
s_add_u32 m0,s57,0
s_nop 0
D 0,49,2048
D 0,49,1024
D 0,49,0
s_add_u32 m0,s57,12928
s_nop 0
D 0,55,2048
D 0,55,1024
D 0,55,0
s_waitcnt vmcnt(0)
Lcv_b_%=:
s_waitcnt vmcnt(2)
ds_read2_b32 v[88:89],v1 offset0:0 offset1:1
ds_read2_b32 v[90:91],v1 offset0:2 offset1:3
R 120,2,3584
ds_read2_b32 v[92:93],v1 offset0:128 offset1:129
ds_read2_b32 v[94:95],v1 offset0:130 offset1:131
R 124,2,3072
ds_read2_b32 v[96:97],v14 offset0:0 offset1:1
ds_read2_b32 v[98:99],v14 offset0:2 offset1:3
R 128,2,2560
ds_read2_b32 v[100:101],v14 offset0:128 offset1:129
ds_read2_b32 v[102:103],v14 offset0:130 offset1:131
R 132,2,2048
ds_read2_b32 v[104:105],v15 offset0:0 offset1:1
ds_read2_b32 v[106:107],v15 offset0:2 offset1:3
R 136,2,1536
ds_read2_b32 v[108:109],v15 offset0:128 offset1:129
ds_read2_b32 v[110:111],v15 offset0:130 offset1:131
R 140,2,1024
ds_read2_b32 v[112:113],v16 offset0:0 offset1:1
ds_read2_b32 v[114:115],v16 offset0:2 offset1:3
R 144,2,512
ds_read2_b32 v[116:117],v16 offset0:128 offset1:129
ds_read2_b32 v[118:119],v16 offset0:130 offset1:131
R 148,2,0
L 15
v_mfma_f32_16x16x32_bf16 v[40:43],v[88:91],v[128:131],0
ds_read2_b32 v[152:153],v1 offset0:16 offset1:17
M 40,v[92:95],124
ds_read2_b32 v[154:155],v1 offset0:18 offset1:19
M 40,v[96:99],120
L 14
R 184,2,3648
v_mfma_f32_16x16x32_bf16 v[44:47],v[88:91],v[132:135],0
ds_read2_b32 v[156:157],v1 offset0:144 offset1:145
M 44,v[92:95],128
M 44,v[96:99],124
ds_read2_b32 v[158:159],v1 offset0:146 offset1:147
M 44,v[100:103],120
L 14
R 188,2,3136
v_mfma_f32_16x16x32_bf16 v[48:51],v[88:91],v[136:139],0
ds_read2_b32 v[160:161],v14 offset0:16 offset1:17
M 48,v[92:95],132
ds_read2_b32 v[162:163],v14 offset0:18 offset1:19
M 48,v[96:99],128
M 48,v[100:103],124
R 192,2,2624
M 48,v[104:107],120
L 15
ds_read2_b32 v[164:165],v14 offset0:144 offset1:145
v_mfma_f32_16x16x32_bf16 v[52:55],v[88:91],v[140:143],0
ds_read2_b32 v[166:167],v14 offset0:146 offset1:147
M 52,v[92:95],136
R 196,2,2112
M 52,v[96:99],132
M 52,v[100:103],128
ds_read2_b32 v[168:169],v15 offset0:16 offset1:17
M 52,v[104:107],124
ds_read2_b32 v[170:171],v15 offset0:18 offset1:19
M 52,v[108:111],120
L 15
R 200,2,1600
v_mfma_f32_16x16x32_bf16 v[56:59],v[88:91],v[144:147],0
ds_read2_b32 v[172:173],v15 offset0:144 offset1:145
M 56,v[92:95],140
M 56,v[96:99],136
ds_read2_b32 v[174:175],v15 offset0:146 offset1:147
M 56,v[100:103],132
R 204,2,1088
M 56,v[104:107],128
ds_read2_b32 v[176:177],v16 offset0:16 offset1:17
M 56,v[108:111],124
ds_read2_b32 v[178:179],v16 offset0:18 offset1:19
M 56,v[112:115],120
L 15
v_mfma_f32_16x16x32_bf16 v[60:63],v[88:91],v[148:151],0
R 208,2,576
M 60,v[92:95],144
ds_read2_b32 v[180:181],v16 offset0:144 offset1:145
M 60,v[96:99],140
ds_read2_b32 v[182:183],v16 offset0:146 offset1:147
M 60,v[100:103],136
R 212,2,64
M 60,v[104:107],132
s_cmp_eq_u32 s83,0
s_cbranch_scc1 Lcv_c_%=
s_waitcnt vmcnt(0)
s_barrier
buffer_store_dword v23,v10,s[36:39],s48 offen offset:0 sc0 sc1
Lcv_c_%=:
M 60,v[108:111],128
M 60,v[112:115],124
M 60,v[116:119],120
L 15
M 40,v[152:155],192
ds_read2_b32 v[88:89],v1 offset0:32 offset1:33
M 40,v[156:159],188
ds_read2_b32 v[90:91],v1 offset0:34 offset1:35
M 40,v[160:163],184
L 14
R 120,2,3712
M 44,v[152:155],196
ds_read2_b32 v[92:93],v1 offset0:160 offset1:161
M 44,v[156:159],192
M 44,v[160:163],188
ds_read2_b32 v[94:95],v1 offset0:162 offset1:163
M 44,v[164:167],184
L 14
R 124,2,3200
M 48,v[152:155],200
ds_read2_b32 v[96:97],v14 offset0:32 offset1:33
M 48,v[156:159],196
ds_read2_b32 v[98:99],v14 offset0:34 offset1:35
M 48,v[160:163],192
M 48,v[164:167],188
R 128,2,2688
M 48,v[168:171],184
L 15
ds_read2_b32 v[100:101],v14 offset0:160 offset1:161
M 52,v[152:155],204
ds_read2_b32 v[102:103],v14 offset0:162 offset1:163
M 52,v[156:159],200
R 132,2,2176
M 52,v[160:163],196
M 52,v[164:167],192
ds_read2_b32 v[104:105],v15 offset0:32 offset1:33
M 52,v[168:171],188
ds_read2_b32 v[106:107],v15 offset0:34 offset1:35
M 52,v[172:175],184
L 15
R 136,2,1664
M 56,v[152:155],208
ds_read2_b32 v[108:109],v15 offset0:160 offset1:161
M 56,v[156:159],204
M 56,v[160:163],200
ds_read2_b32 v[110:111],v15 offset0:162 offset1:163
M 56,v[164:167],196
R 140,2,1152
M 56,v[168:171],192
ds_read2_b32 v[112:113],v16 offset0:32 offset1:33
M 56,v[172:175],188
ds_read2_b32 v[114:115],v16 offset0:34 offset1:35
M 56,v[176:179],184
L 15
M 60,v[152:155],212
R 144,2,640
M 60,v[156:159],208
ds_read2_b32 v[116:117],v16 offset0:160 offset1:161
M 60,v[160:163],204
ds_read2_b32 v[118:119],v16 offset0:162 offset1:163
M 60,v[164:167],200
R 148,2,128
M 60,v[168:171],196
M 60,v[172:175],192
M 60,v[176:179],188
M 60,v[180:183],184
L 15
M 40,v[88:91],128
ds_read2_b32 v[152:153],v1 offset0:48 offset1:49
M 40,v[92:95],124
ds_read2_b32 v[154:155],v1 offset0:50 offset1:51
M 40,v[96:99],120
L 14
R 184,2,3776
M 44,v[88:91],132
ds_read2_b32 v[156:157],v1 offset0:176 offset1:177
M 44,v[92:95],128
M 44,v[96:99],124
ds_read2_b32 v[158:159],v1 offset0:178 offset1:179
M 44,v[100:103],120
L 14
R 188,2,3264
M 48,v[88:91],136
ds_read2_b32 v[160:161],v14 offset0:48 offset1:49
M 48,v[92:95],132
ds_read2_b32 v[162:163],v14 offset0:50 offset1:51
M 48,v[96:99],128
M 48,v[100:103],124
R 192,2,2752
M 48,v[104:107],120
L 15
ds_read2_b32 v[164:165],v14 offset0:176 offset1:177
M 52,v[88:91],140
s_mov_b32 m0,163584
s_mov_b32 s78,0
ds_read2_b32 v[166:167],v14 offset0:178 offset1:179
M 52,v[92:95],136
R 196,2,2240
M 52,v[96:99],132
buffer_load_dword v11,s[40:43],s78 offen offset:0  lds
M 52,v[100:103],128
ds_read2_b32 v[168:169],v15 offset0:48 offset1:49
M 52,v[104:107],124
ds_read2_b32 v[170:171],v15 offset0:50 offset1:51
M 52,v[108:111],120
L 15
R 200,2,1728
M 56,v[88:91],144
ds_read2_b32 v[172:173],v15 offset0:176 offset1:177
M 56,v[92:95],140
M 56,v[96:99],136
ds_read2_b32 v[174:175],v15 offset0:178 offset1:179
M 56,v[100:103],132
R 204,2,1216
M 56,v[104:107],128
ds_read2_b32 v[176:177],v16 offset0:48 offset1:49
M 56,v[108:111],124
ds_read2_b32 v[178:179],v16 offset0:50 offset1:51
M 56,v[112:115],120
L 15
M 60,v[88:91],148
R 208,2,704
M 60,v[92:95],144
ds_read2_b32 v[180:181],v16 offset0:176 offset1:177
M 60,v[96:99],140
ds_read2_b32 v[182:183],v16 offset0:178 offset1:179
M 60,v[100:103],136
R 212,2,192
M 60,v[104:107],132
M 60,v[108:111],128
M 60,v[112:115],124
M 60,v[116:119],120
L 15
M 40,v[152:155],192
ds_read2_b32 v[88:89],v1 offset0:64 offset1:65
M 40,v[156:159],188
ds_read2_b32 v[90:91],v1 offset0:66 offset1:67
M 40,v[160:163],184
L 14
R 120,2,3840
M 44,v[152:155],196
ds_read2_b32 v[92:93],v1 offset0:192 offset1:193
M 44,v[156:159],192
M 44,v[160:163],188
ds_read2_b32 v[94:95],v1 offset0:194 offset1:195
M 44,v[164:167],184
L 14
R 124,2,3328
M 48,v[152:155],200
ds_read2_b32 v[96:97],v14 offset0:64 offset1:65
M 48,v[156:159],196
ds_read2_b32 v[98:99],v14 offset0:66 offset1:67
M 48,v[160:163],192
M 48,v[164:167],188
R 128,2,2816
M 48,v[168:171],184
L 15
ds_read2_b32 v[100:101],v14 offset0:192 offset1:193
M 52,v[152:155],204
ds_read2_b32 v[102:103],v14 offset0:194 offset1:195
M 52,v[156:159],200
R 132,2,2304
M 52,v[160:163],196
M 52,v[164:167],192
ds_read2_b32 v[104:105],v15 offset0:64 offset1:65
M 52,v[168:171],188
ds_read2_b32 v[106:107],v15 offset0:66 offset1:67
M 52,v[172:175],184
L 15
R 136,2,1792
M 56,v[152:155],208
ds_read2_b32 v[108:109],v15 offset0:192 offset1:193
M 56,v[156:159],204
M 56,v[160:163],200
ds_read2_b32 v[110:111],v15 offset0:194 offset1:195
M 56,v[164:167],196
R 140,2,1280
M 56,v[168:171],192
ds_read2_b32 v[112:113],v16 offset0:64 offset1:65
M 56,v[172:175],188
ds_read2_b32 v[114:115],v16 offset0:66 offset1:67
M 56,v[176:179],184
L 15
M 60,v[152:155],212
R 144,2,768
M 60,v[156:159],208
ds_read2_b32 v[116:117],v16 offset0:192 offset1:193
M 60,v[160:163],204
ds_read2_b32 v[118:119],v16 offset0:194 offset1:195
M 60,v[164:167],200
R 148,2,256
M 60,v[168:171],196
M 60,v[172:175],192
M 60,v[176:179],188
M 60,v[180:183],184
L 15
M 40,v[88:91],128
ds_read2_b32 v[152:153],v1 offset0:80 offset1:81
M 40,v[92:95],124
ds_read2_b32 v[154:155],v1 offset0:82 offset1:83
M 40,v[96:99],120
L 14
R 184,2,3904
M 44,v[88:91],132
ds_read2_b32 v[156:157],v1 offset0:208 offset1:209
M 44,v[92:95],128
M 44,v[96:99],124
ds_read2_b32 v[158:159],v1 offset0:210 offset1:211
M 44,v[100:103],120
L 14
R 188,2,3392
M 48,v[88:91],136
ds_read2_b32 v[160:161],v14 offset0:80 offset1:81
M 48,v[92:95],132
ds_read2_b32 v[162:163],v14 offset0:82 offset1:83
M 48,v[96:99],128
M 48,v[100:103],124
R 192,2,2880
M 48,v[104:107],120
L 15
ds_read2_b32 v[164:165],v14 offset0:208 offset1:209
M 52,v[88:91],140
ds_read2_b32 v[166:167],v14 offset0:210 offset1:211
M 52,v[92:95],136
R 196,2,2368
M 52,v[96:99],132
M 52,v[100:103],128
ds_read2_b32 v[168:169],v15 offset0:80 offset1:81
M 52,v[104:107],124
ds_read2_b32 v[170:171],v15 offset0:82 offset1:83
M 52,v[108:111],120
L 15
R 200,2,1856
M 56,v[88:91],144
ds_read2_b32 v[172:173],v15 offset0:208 offset1:209
M 56,v[92:95],140
M 56,v[96:99],136
ds_read2_b32 v[174:175],v15 offset0:210 offset1:211
M 56,v[100:103],132
R 204,2,1344
M 56,v[104:107],128
ds_read2_b32 v[176:177],v16 offset0:80 offset1:81
M 56,v[108:111],124
ds_read2_b32 v[178:179],v16 offset0:82 offset1:83
M 56,v[112:115],120
L 15
M 60,v[88:91],148
R 208,2,832
M 60,v[92:95],144
ds_read2_b32 v[180:181],v16 offset0:208 offset1:209
M 60,v[96:99],140
ds_read2_b32 v[182:183],v16 offset0:210 offset1:211
M 60,v[100:103],136
R 212,2,320
M 60,v[104:107],132
M 60,v[108:111],128
M 60,v[112:115],124
M 60,v[116:119],120
L 15
M 40,v[152:155],192
ds_read2_b32 v[88:89],v1 offset0:96 offset1:97
M 40,v[156:159],188
ds_read2_b32 v[90:91],v1 offset0:98 offset1:99
M 40,v[160:163],184
L 14
R 120,2,3968
M 44,v[152:155],196
s_mov_b32 m0,163584
s_mov_b32 s78,3741696
ds_read2_b32 v[92:93],v1 offset0:224 offset1:225
M 44,v[156:159],192
M 44,v[160:163],188
ds_read2_b32 v[94:95],v1 offset0:226 offset1:227
M 44,v[164:167],184
L 14
R 124,2,3456
M 48,v[152:155],200
ds_read2_b32 v[96:97],v14 offset0:96 offset1:97
M 48,v[156:159],196
ds_read2_b32 v[98:99],v14 offset0:98 offset1:99
M 48,v[160:163],192
buffer_load_dword v11,s[40:43],s78 offen offset:0  lds
M 48,v[164:167],188
R 128,2,2944
M 48,v[168:171],184
L 15
ds_read2_b32 v[100:101],v14 offset0:224 offset1:225
M 52,v[152:155],204
ds_read2_b32 v[102:103],v14 offset0:226 offset1:227
M 52,v[156:159],200
R 132,2,2432
M 52,v[160:163],196
M 52,v[164:167],192
ds_read2_b32 v[104:105],v15 offset0:96 offset1:97
M 52,v[168:171],188
ds_read2_b32 v[106:107],v15 offset0:98 offset1:99
M 52,v[172:175],184
L 15
R 136,2,1920
M 56,v[152:155],208
ds_read2_b32 v[108:109],v15 offset0:224 offset1:225
M 56,v[156:159],204
M 56,v[160:163],200
ds_read2_b32 v[110:111],v15 offset0:226 offset1:227
M 56,v[164:167],196
R 140,2,1408
M 56,v[168:171],192
ds_read2_b32 v[112:113],v16 offset0:96 offset1:97
M 56,v[172:175],188
ds_read2_b32 v[114:115],v16 offset0:98 offset1:99
M 56,v[176:179],184
L 15
M 60,v[152:155],212
R 144,2,896
M 60,v[156:159],208
ds_read2_b32 v[116:117],v16 offset0:224 offset1:225
M 60,v[160:163],204
ds_read2_b32 v[118:119],v16 offset0:226 offset1:227
M 60,v[164:167],200
R 148,2,384
M 60,v[168:171],196
M 60,v[172:175],192
M 60,v[176:179],188
M 60,v[180:183],184
L 15
M 40,v[88:91],128
ds_read2_b32 v[152:153],v1 offset0:112 offset1:113
M 40,v[92:95],124
ds_read2_b32 v[154:155],v1 offset0:114 offset1:115
M 40,v[96:99],120
L 14
R 184,2,4032
M 44,v[88:91],132
ds_read2_b32 v[156:157],v1 offset0:240 offset1:241
M 44,v[92:95],128
M 44,v[96:99],124
ds_read2_b32 v[158:159],v1 offset0:242 offset1:243
v_add_u32 v1,12928,v1
M 44,v[100:103],120
L 14
R 188,2,3520
M 48,v[88:91],136
ds_read2_b32 v[160:161],v14 offset0:112 offset1:113
M 48,v[92:95],132
ds_read2_b32 v[162:163],v14 offset0:114 offset1:115
M 48,v[96:99],128
M 48,v[100:103],124
R 192,2,3008
M 48,v[104:107],120
L 15
ds_read2_b32 v[164:165],v14 offset0:240 offset1:241
M 52,v[88:91],140
ds_read2_b32 v[166:167],v14 offset0:242 offset1:243
v_add_u32 v14,12928,v14
M 52,v[92:95],136
R 196,2,2496
M 52,v[96:99],132
M 52,v[100:103],128
ds_read2_b32 v[168:169],v15 offset0:112 offset1:113
M 52,v[104:107],124
ds_read2_b32 v[170:171],v15 offset0:114 offset1:115
M 52,v[108:111],120
L 15
R 200,2,1984
M 56,v[88:91],144
ds_read2_b32 v[172:173],v15 offset0:240 offset1:241
M 56,v[92:95],140
M 56,v[96:99],136
ds_read2_b32 v[174:175],v15 offset0:242 offset1:243
v_add_u32 v15,12928,v15
M 56,v[100:103],132
R 204,2,1472
M 56,v[104:107],128
ds_read2_b32 v[176:177],v16 offset0:112 offset1:113
M 56,v[108:111],124
ds_read2_b32 v[178:179],v16 offset0:114 offset1:115
M 56,v[112:115],120
L 15
M 60,v[88:91],148
R 208,2,960
M 60,v[92:95],144
ds_read2_b32 v[180:181],v16 offset0:240 offset1:241
M 60,v[96:99],140
ds_read2_b32 v[182:183],v16 offset0:242 offset1:243
v_add_u32 v16,12928,v16
M 60,v[100:103],136
)"
R"(R 212,2,448
M 60,v[104:107],132
M 60,v[108:111],128
M 60,v[112:115],124
M 60,v[116:119],120
L 15
M 40,v[152:155],192
ds_read2_b32 v[88:89],v1 offset0:0 offset1:1
M 40,v[156:159],188
ds_read2_b32 v[90:91],v1 offset0:2 offset1:3
M 40,v[160:163],184
L 14
R 120,2,16512
M 44,v[152:155],196
ds_read2_b32 v[92:93],v1 offset0:128 offset1:129
M 44,v[156:159],192
M 44,v[160:163],188
ds_read2_b32 v[94:95],v1 offset0:130 offset1:131
M 44,v[164:167],184
L 14
R 124,2,16000
M 48,v[152:155],200
ds_read2_b32 v[96:97],v14 offset0:0 offset1:1
M 48,v[156:159],196
ds_read2_b32 v[98:99],v14 offset0:2 offset1:3
M 48,v[160:163],192
M 48,v[164:167],188
R 128,2,15488
M 48,v[168:171],184
L 15
ds_read2_b32 v[100:101],v14 offset0:128 offset1:129
M 52,v[152:155],204
ds_read2_b32 v[102:103],v14 offset0:130 offset1:131
M 52,v[156:159],200
R 132,2,14976
M 52,v[160:163],196
M 52,v[164:167],192
ds_read2_b32 v[104:105],v15 offset0:0 offset1:1
M 52,v[168:171],188
ds_read2_b32 v[106:107],v15 offset0:2 offset1:3
M 52,v[172:175],184
L 15
R 136,2,14464
M 56,v[152:155],208
ds_read2_b32 v[108:109],v15 offset0:128 offset1:129
M 56,v[156:159],204
M 56,v[160:163],200
ds_read2_b32 v[110:111],v15 offset0:130 offset1:131
M 56,v[164:167],196
R 140,2,13952
M 56,v[168:171],192
ds_read2_b32 v[112:113],v16 offset0:0 offset1:1
M 56,v[172:175],188
ds_read2_b32 v[114:115],v16 offset0:2 offset1:3
M 56,v[176:179],184
L 15
M 60,v[152:155],212
R 144,2,13440
M 60,v[156:159],208
ds_read2_b32 v[116:117],v16 offset0:128 offset1:129
M 60,v[160:163],204
ds_read2_b32 v[118:119],v16 offset0:130 offset1:131
M 60,v[164:167],200
R 148,2,12928
M 60,v[168:171],196
M 60,v[172:175],192
M 60,v[176:179],188
M 60,v[180:183],184
s_cmp_eq_u32 s51,2
s_cbranch_scc1 Lcv_d_%=
ds_read_b64 v[24:25],v3 offset:2560
ds_read_b64 v[26:27],v3 offset:2048
ds_read_b64 v[28:29],v3 offset:1536
ds_read_b64 v[30:31],v3 offset:1024
ds_read_b64 v[32:33],v3 offset:512
ds_read_b64 v[34:35],v3 offset:0
L 0
v_and_b32 v36,0xffff0000,v25
v_lshlrev_b32 v37,16,v25
v_and_b32 v38,0xffff0000,v24
v_lshlrev_b32 v39,16,v24
v_fma_f32 v40,v36,s86,v40
v_fma_f32 v41,v37,s86,v41
v_fma_f32 v42,v38,s86,v42
v_fma_f32 v43,v39,s86,v43
v_cvt_pk_bf16_f32 v40,v40,v41
v_cvt_pk_bf16_f32 v41,v42,v43
ds_write_b64 v4,v[40:41] offset:0
v_and_b32 v36,0xffff0000,v27
v_lshlrev_b32 v37,16,v27
v_and_b32 v38,0xffff0000,v26
v_lshlrev_b32 v39,16,v26
v_fma_f32 v44,v36,s86,v44
v_fma_f32 v45,v37,s86,v45
v_fma_f32 v46,v38,s86,v46
v_fma_f32 v47,v39,s86,v47
v_cvt_pk_bf16_f32 v44,v44,v45
v_cvt_pk_bf16_f32 v45,v46,v47
ds_write_b64 v4,v[44:45] offset:512
v_and_b32 v36,0xffff0000,v29
v_lshlrev_b32 v37,16,v29
v_and_b32 v38,0xffff0000,v28
v_lshlrev_b32 v39,16,v28
v_fma_f32 v48,v36,s86,v48
v_fma_f32 v49,v37,s86,v49
v_fma_f32 v50,v38,s86,v50
v_fma_f32 v51,v39,s86,v51
v_cvt_pk_bf16_f32 v48,v48,v49
v_cvt_pk_bf16_f32 v49,v50,v51
ds_write_b64 v4,v[48:49] offset:1024
v_and_b32 v36,0xffff0000,v31
v_lshlrev_b32 v37,16,v31
v_and_b32 v38,0xffff0000,v30
v_lshlrev_b32 v39,16,v30
v_fma_f32 v52,v36,s86,v52
v_fma_f32 v53,v37,s86,v53
v_fma_f32 v54,v38,s86,v54
v_fma_f32 v55,v39,s86,v55
v_cvt_pk_bf16_f32 v52,v52,v53
v_cvt_pk_bf16_f32 v53,v54,v55
ds_write_b64 v4,v[52:53] offset:1536
v_and_b32 v36,0xffff0000,v33
v_lshlrev_b32 v37,16,v33
v_and_b32 v38,0xffff0000,v32
v_lshlrev_b32 v39,16,v32
v_fma_f32 v56,v36,s86,v56
v_fma_f32 v57,v37,s86,v57
v_fma_f32 v58,v38,s86,v58
v_fma_f32 v59,v39,s86,v59
v_cvt_pk_bf16_f32 v56,v56,v57
v_cvt_pk_bf16_f32 v57,v58,v59
ds_write_b64 v4,v[56:57] offset:2048
v_and_b32 v36,0xffff0000,v35
v_lshlrev_b32 v37,16,v35
v_and_b32 v38,0xffff0000,v34
v_lshlrev_b32 v39,16,v34
v_fma_f32 v60,v36,s86,v60
v_fma_f32 v61,v37,s86,v61
v_fma_f32 v62,v38,s86,v62
v_fma_f32 v63,v39,s86,v63
v_cvt_pk_bf16_f32 v60,v60,v61
v_cvt_pk_bf16_f32 v61,v62,v63
ds_write_b64 v4,v[60:61] offset:2560
s_branch Lcv_e_%=
Lcv_d_%=:
L 15
v_mfma_f32_16x16x32_bf16 v[64:67],v[88:91],v[128:131],0
ds_read2_b32 v[152:153],v1 offset0:16 offset1:17
ds_read_b64 v[24:25],v3 offset:2560
M 64,v[92:95],124
ds_read2_b32 v[154:155],v1 offset0:18 offset1:19
ds_read_b64 v[26:27],v3 offset:2048
M 64,v[96:99],120
L 15
R 184,2,16576
ds_read_b64 v[28:29],v3 offset:1536
v_mfma_f32_16x16x32_bf16 v[68:71],v[88:91],v[132:135],0
ds_read2_b32 v[156:157],v1 offset0:144 offset1:145
ds_read_b64 v[30:31],v3 offset:1024
M 68,v[92:95],128
ds_read_b64 v[32:33],v3 offset:512
ds_read_b64 v[34:35],v3 offset:0
M 68,v[96:99],124
ds_read2_b32 v[158:159],v1 offset0:146 offset1:147
M 68,v[100:103],120
L 15
R 188,2,16064
v_mfma_f32_16x16x32_bf16 v[72:75],v[88:91],v[136:139],0
ds_read2_b32 v[160:161],v14 offset0:16 offset1:17
M 72,v[92:95],132
s_mov_b32 m0,163584
s_mov_b32 s78,0
ds_read2_b32 v[162:163],v14 offset0:18 offset1:19
M 72,v[96:99],128
M 72,v[100:103],124
L 4
R 192,2,15552
v_and_b32 v36,0xffff0000,v25
M 72,v[104:107],120
s_nop 0
ds_read2_b32 v[164:165],v14 offset0:144 offset1:145
v_lshlrev_b32 v37,16,v25
v_mfma_f32_16x16x32_bf16 v[76:79],v[88:91],v[140:143],0
ds_read2_b32 v[166:167],v14 offset0:146 offset1:147
v_and_b32 v38,0xffff0000,v24
M 76,v[92:95],136
R 196,2,15040
v_lshlrev_b32 v39,16,v24
M 76,v[96:99],132
buffer_load_dword v12,s[60:63],s78 offen offset:0  lds
M 76,v[100:103],128
ds_read2_b32 v[168:169],v15 offset0:16 offset1:17
v_fma_f32 v40,v36,s86,v40
M 76,v[104:107],124
ds_read2_b32 v[170:171],v15 offset0:18 offset1:19
v_fma_f32 v41,v37,s86,v41
M 76,v[108:111],120
s_nop 0
R 200,2,14528
v_fma_f32 v42,v38,s86,v42
v_mfma_f32_16x16x32_bf16 v[80:83],v[88:91],v[144:147],0
ds_read2_b32 v[172:173],v15 offset0:144 offset1:145
v_fma_f32 v43,v39,s86,v43
M 80,v[92:95],140
v_cvt_pk_bf16_f32 v40,v40,v41
v_cvt_pk_bf16_f32 v41,v42,v43
M 80,v[96:99],136
ds_read2_b32 v[174:175],v15 offset0:146 offset1:147
v_and_b32 v36,0xffff0000,v27
M 80,v[100:103],132
R 204,2,14016
v_lshlrev_b32 v37,16,v27
M 80,v[104:107],128
ds_read2_b32 v[176:177],v16 offset0:16 offset1:17
v_and_b32 v38,0xffff0000,v26
M 80,v[108:111],124
ds_read2_b32 v[178:179],v16 offset0:18 offset1:19
v_lshlrev_b32 v39,16,v26
M 80,v[112:115],120
s_nop 0
ds_write_b64 v4,v[40:41] offset:0
v_mfma_f32_16x16x32_bf16 v[84:87],v[88:91],v[148:151],0
R 208,2,13504
v_fma_f32 v44,v36,s86,v44
M 84,v[92:95],144
ds_read2_b32 v[180:181],v16 offset0:144 offset1:145
v_fma_f32 v45,v37,s86,v45
M 84,v[96:99],140
ds_read2_b32 v[182:183],v16 offset0:146 offset1:147
v_fma_f32 v46,v38,s86,v46
M 84,v[100:103],136
R 212,2,12992
v_fma_f32 v47,v39,s86,v47
M 84,v[104:107],132
v_cvt_pk_bf16_f32 v44,v44,v45
v_cvt_pk_bf16_f32 v45,v46,v47
M 84,v[108:111],128
ds_write_b64 v4,v[44:45] offset:512
M 84,v[112:115],124
v_and_b32 v36,0xffff0000,v29
v_lshlrev_b32 v37,16,v29
M 84,v[116:119],120
L 15
v_and_b32 v38,0xffff0000,v28
v_lshlrev_b32 v39,16,v28
M 64,v[152:155],192
ds_read2_b32 v[88:89],v1 offset0:32 offset1:33
v_fma_f32 v48,v36,s86,v48
M 64,v[156:159],188
ds_read2_b32 v[90:91],v1 offset0:34 offset1:35
v_fma_f32 v49,v37,s86,v49
M 64,v[160:163],184
L 15
R 120,2,16640
v_fma_f32 v50,v38,s86,v50
M 68,v[152:155],196
ds_read2_b32 v[92:93],v1 offset0:160 offset1:161
v_fma_f32 v51,v39,s86,v51
M 68,v[156:159],192
v_cvt_pk_bf16_f32 v48,v48,v49
v_cvt_pk_bf16_f32 v49,v50,v51
M 68,v[160:163],188
ds_read2_b32 v[94:95],v1 offset0:162 offset1:163
v_and_b32 v36,0xffff0000,v31
M 68,v[164:167],184
L 15
R 124,2,16128
v_lshlrev_b32 v37,16,v31
M 72,v[152:155],200
ds_read2_b32 v[96:97],v14 offset0:32 offset1:33
v_and_b32 v38,0xffff0000,v30
M 72,v[156:159],196
ds_read2_b32 v[98:99],v14 offset0:34 offset1:35
v_lshlrev_b32 v39,16,v30
M 72,v[160:163],192
ds_write_b64 v4,v[48:49] offset:1024
M 72,v[164:167],188
R 128,2,15616
v_fma_f32 v52,v36,s86,v52
M 72,v[168:171],184
L 15
ds_read2_b32 v[100:101],v14 offset0:160 offset1:161
v_fma_f32 v53,v37,s86,v53
M 76,v[152:155],204
ds_read2_b32 v[102:103],v14 offset0:162 offset1:163
v_fma_f32 v54,v38,s86,v54
M 76,v[156:159],200
R 132,2,15104
v_fma_f32 v55,v39,s86,v55
M 76,v[160:163],196
v_cvt_pk_bf16_f32 v52,v52,v53
v_cvt_pk_bf16_f32 v53,v54,v55
M 76,v[164:167],192
ds_read2_b32 v[104:105],v15 offset0:32 offset1:33
v_and_b32 v36,0xffff0000,v33
M 76,v[168:171],188
ds_read2_b32 v[106:107],v15 offset0:34 offset1:35
v_lshlrev_b32 v37,16,v33
M 76,v[172:175],184
L 15
R 136,2,14592
v_and_b32 v38,0xffff0000,v32
M 80,v[152:155],208
ds_read2_b32 v[108:109],v15 offset0:160 offset1:161
v_lshlrev_b32 v39,16,v32
M 80,v[156:159],204
ds_write_b64 v4,v[52:53] offset:1536
M 80,v[160:163],200
ds_read2_b32 v[110:111],v15 offset0:162 offset1:163
v_fma_f32 v56,v36,s86,v56
M 80,v[164:167],196
R 140,2,14080
v_fma_f32 v57,v37,s86,v57
M 80,v[168:171],192
ds_read2_b32 v[112:113],v16 offset0:32 offset1:33
v_fma_f32 v58,v38,s86,v58
M 80,v[172:175],188
ds_read2_b32 v[114:115],v16 offset0:34 offset1:35
v_fma_f32 v59,v39,s86,v59
M 80,v[176:179],184
L 15
v_cvt_pk_bf16_f32 v56,v56,v57
v_cvt_pk_bf16_f32 v57,v58,v59
M 84,v[152:155],212
R 144,2,13568
v_and_b32 v36,0xffff0000,v35
M 84,v[156:159],208
ds_read2_b32 v[116:117],v16 offset0:160 offset1:161
v_lshlrev_b32 v37,16,v35
M 84,v[160:163],204
ds_read2_b32 v[118:119],v16 offset0:162 offset1:163
v_and_b32 v38,0xffff0000,v34
M 84,v[164:167],200
R 148,2,13056
v_lshlrev_b32 v39,16,v34
M 84,v[168:171],196
ds_write_b64 v4,v[56:57] offset:2048
M 84,v[172:175],192
v_fma_f32 v60,v36,s86,v60
v_fma_f32 v61,v37,s86,v61
M 84,v[176:179],188
v_fma_f32 v62,v38,s86,v62
v_fma_f32 v63,v39,s86,v63
M 84,v[180:183],184
L 15
v_cvt_pk_bf16_f32 v60,v60,v61
v_cvt_pk_bf16_f32 v61,v62,v63
M 64,v[88:91],128
ds_read2_b32 v[152:153],v1 offset0:48 offset1:49
M 64,v[92:95],124
ds_read2_b32 v[154:155],v1 offset0:50 offset1:51
M 64,v[96:99],120
L 15
R 184,2,16704
M 68,v[88:91],132
ds_read2_b32 v[156:157],v1 offset0:176 offset1:177
M 68,v[92:95],128
ds_write_b64 v4,v[60:61] offset:2560
M 68,v[96:99],124
ds_read2_b32 v[158:159],v1 offset0:178 offset1:179
M 68,v[100:103],120
L 15
R 188,2,16192
M 72,v[88:91],136
ds_read2_b32 v[160:161],v14 offset0:48 offset1:49
M 72,v[92:95],132
ds_read2_b32 v[162:163],v14 offset0:50 offset1:51
M 72,v[96:99],128
M 72,v[100:103],124
R 192,2,15680
M 72,v[104:107],120
L 15
ds_read2_b32 v[164:165],v14 offset0:176 offset1:177
M 76,v[88:91],140
ds_read2_b32 v[166:167],v14 offset0:178 offset1:179
M 76,v[92:95],136
R 196,2,15168
M 76,v[96:99],132
M 76,v[100:103],128
ds_read2_b32 v[168:169],v15 offset0:48 offset1:49
M 76,v[104:107],124
ds_read2_b32 v[170:171],v15 offset0:50 offset1:51
M 76,v[108:111],120
L 15
R 200,2,14656
M 80,v[88:91],144
ds_read2_b32 v[172:173],v15 offset0:176 offset1:177
M 80,v[92:95],140
M 80,v[96:99],136
ds_read2_b32 v[174:175],v15 offset0:178 offset1:179
M 80,v[100:103],132
R 204,2,14144
M 80,v[104:107],128
s_mov_b32 m0,163584
s_mov_b32 s78,1847296
ds_read2_b32 v[176:177],v16 offset0:48 offset1:49
M 80,v[108:111],124
ds_read2_b32 v[178:179],v16 offset0:50 offset1:51
M 80,v[112:115],120
L 15
buffer_load_dword v12,s[60:63],s78 offen offset:0  lds
M 84,v[88:91],148
R 208,2,13632
M 84,v[92:95],144
ds_read2_b32 v[180:181],v16 offset0:176 offset1:177
M 84,v[96:99],140
ds_read2_b32 v[182:183],v16 offset0:178 offset1:179
M 84,v[100:103],136
R 212,2,13120
M 84,v[104:107],132
M 84,v[108:111],128
M 84,v[112:115],124
M 84,v[116:119],120
L 15
M 64,v[152:155],192
ds_read2_b32 v[88:89],v1 offset0:64 offset1:65
M 64,v[156:159],188
ds_read2_b32 v[90:91],v1 offset0:66 offset1:67
M 64,v[160:163],184
L 14
R 120,2,16768
M 68,v[152:155],196
ds_read2_b32 v[92:93],v1 offset0:192 offset1:193
M 68,v[156:159],192
M 68,v[160:163],188
ds_read2_b32 v[94:95],v1 offset0:194 offset1:195
M 68,v[164:167],184
L 14
R 124,2,16256
M 72,v[152:155],200
ds_read2_b32 v[96:97],v14 offset0:64 offset1:65
M 72,v[156:159],196
ds_read2_b32 v[98:99],v14 offset0:66 offset1:67
M 72,v[160:163],192
M 72,v[164:167],188
R 128,2,15744
M 72,v[168:171],184
L 15
ds_read2_b32 v[100:101],v14 offset0:192 offset1:193
M 76,v[152:155],204
ds_read2_b32 v[102:103],v14 offset0:194 offset1:195
M 76,v[156:159],200
R 132,2,15232
M 76,v[160:163],196
M 76,v[164:167],192
ds_read2_b32 v[104:105],v15 offset0:64 offset1:65
M 76,v[168:171],188
ds_read2_b32 v[106:107],v15 offset0:66 offset1:67
M 76,v[172:175],184
L 15
R 136,2,14720
M 80,v[152:155],208
ds_read2_b32 v[108:109],v15 offset0:192 offset1:193
M 80,v[156:159],204
M 80,v[160:163],200
ds_read2_b32 v[110:111],v15 offset0:194 offset1:195
M 80,v[164:167],196
R 140,2,14208
M 80,v[168:171],192
ds_read2_b32 v[112:113],v16 offset0:64 offset1:65
M 80,v[172:175],188
ds_read2_b32 v[114:115],v16 offset0:66 offset1:67
M 80,v[176:179],184
L 15
M 84,v[152:155],212
R 144,2,13696
M 84,v[156:159],208
ds_read2_b32 v[116:117],v16 offset0:192 offset1:193
M 84,v[160:163],204
ds_read2_b32 v[118:119],v16 offset0:194 offset1:195
M 84,v[164:167],200
R 148,2,13184
M 84,v[168:171],196
M 84,v[172:175],192
M 84,v[176:179],188
M 84,v[180:183],184
L 15
M 64,v[88:91],128
ds_read2_b32 v[152:153],v1 offset0:80 offset1:81
M 64,v[92:95],124
ds_read2_b32 v[154:155],v1 offset0:82 offset1:83
M 64,v[96:99],120
L 14
R 184,2,16832
M 68,v[88:91],132
ds_read2_b32 v[156:157],v1 offset0:208 offset1:209
M 68,v[92:95],128
M 68,v[96:99],124
ds_read2_b32 v[158:159],v1 offset0:210 offset1:211
M 68,v[100:103],120
L 14
R 188,2,16320
M 72,v[88:91],136
ds_read2_b32 v[160:161],v14 offset0:80 offset1:81
M 72,v[92:95],132
ds_read2_b32 v[162:163],v14 offset0:82 offset1:83
M 72,v[96:99],128
M 72,v[100:103],124
R 192,2,15808
M 72,v[104:107],120
)"
R"(L 15
ds_read2_b32 v[164:165],v14 offset0:208 offset1:209
M 76,v[88:91],140
ds_read2_b32 v[166:167],v14 offset0:210 offset1:211
M 76,v[92:95],136
R 196,2,15296
M 76,v[96:99],132
M 76,v[100:103],128
ds_read2_b32 v[168:169],v15 offset0:80 offset1:81
M 76,v[104:107],124
ds_read2_b32 v[170:171],v15 offset0:82 offset1:83
M 76,v[108:111],120
L 15
R 200,2,14784
M 80,v[88:91],144
ds_read2_b32 v[172:173],v15 offset0:208 offset1:209
M 80,v[92:95],140
M 80,v[96:99],136
ds_read2_b32 v[174:175],v15 offset0:210 offset1:211
M 80,v[100:103],132
R 204,2,14272
M 80,v[104:107],128
ds_read2_b32 v[176:177],v16 offset0:80 offset1:81
M 80,v[108:111],124
ds_read2_b32 v[178:179],v16 offset0:82 offset1:83
M 80,v[112:115],120
L 15
M 84,v[88:91],148
R 208,2,13760
M 84,v[92:95],144
ds_read2_b32 v[180:181],v16 offset0:208 offset1:209
M 84,v[96:99],140
ds_read2_b32 v[182:183],v16 offset0:210 offset1:211
M 84,v[100:103],136
R 212,2,13248
M 84,v[104:107],132
M 84,v[108:111],128
M 84,v[112:115],124
M 84,v[116:119],120
L 15
M 64,v[152:155],192
ds_read2_b32 v[88:89],v1 offset0:96 offset1:97
M 64,v[156:159],188
ds_read2_b32 v[90:91],v1 offset0:98 offset1:99
M 64,v[160:163],184
L 14
R 120,2,16896
M 68,v[152:155],196
ds_read2_b32 v[92:93],v1 offset0:224 offset1:225
M 68,v[156:159],192
M 68,v[160:163],188
ds_read2_b32 v[94:95],v1 offset0:226 offset1:227
M 68,v[164:167],184
L 14
R 124,2,16384
M 72,v[152:155],200
ds_read2_b32 v[96:97],v14 offset0:96 offset1:97
M 72,v[156:159],196
ds_read2_b32 v[98:99],v14 offset0:98 offset1:99
M 72,v[160:163],192
M 72,v[164:167],188
R 128,2,15872
M 72,v[168:171],184
L 15
ds_read2_b32 v[100:101],v14 offset0:224 offset1:225
M 76,v[152:155],204
ds_read2_b32 v[102:103],v14 offset0:226 offset1:227
M 76,v[156:159],200
R 132,2,15360
M 76,v[160:163],196
M 76,v[164:167],192
ds_read2_b32 v[104:105],v15 offset0:96 offset1:97
M 76,v[168:171],188
ds_read2_b32 v[106:107],v15 offset0:98 offset1:99
M 76,v[172:175],184
L 15
R 136,2,14848
M 80,v[152:155],208
ds_read2_b32 v[108:109],v15 offset0:224 offset1:225
M 80,v[156:159],204
M 80,v[160:163],200
ds_read2_b32 v[110:111],v15 offset0:226 offset1:227
M 80,v[164:167],196
R 140,2,14336
M 80,v[168:171],192
ds_read2_b32 v[112:113],v16 offset0:96 offset1:97
M 80,v[172:175],188
ds_read2_b32 v[114:115],v16 offset0:98 offset1:99
M 80,v[176:179],184
L 15
M 84,v[152:155],212
R 144,2,13824
M 84,v[156:159],208
ds_read2_b32 v[116:117],v16 offset0:224 offset1:225
M 84,v[160:163],204
ds_read2_b32 v[118:119],v16 offset0:226 offset1:227
M 84,v[164:167],200
R 148,2,13312
M 84,v[168:171],196
M 84,v[172:175],192
M 84,v[176:179],188
M 84,v[180:183],184
L 15
M 64,v[88:91],128
ds_read2_b32 v[152:153],v1 offset0:112 offset1:113
M 64,v[92:95],124
ds_read2_b32 v[154:155],v1 offset0:114 offset1:115
M 64,v[96:99],120
L 14
R 184,2,16960
M 68,v[88:91],132
ds_read2_b32 v[156:157],v1 offset0:240 offset1:241
M 68,v[92:95],128
M 68,v[96:99],124
ds_read2_b32 v[158:159],v1 offset0:242 offset1:243
M 68,v[100:103],120
L 14
R 188,2,16448
M 72,v[88:91],136
ds_read2_b32 v[160:161],v14 offset0:112 offset1:113
M 72,v[92:95],132
ds_read2_b32 v[162:163],v14 offset0:114 offset1:115
M 72,v[96:99],128
M 72,v[100:103],124
R 192,2,15936
M 72,v[104:107],120
L 15
ds_read2_b32 v[164:165],v14 offset0:240 offset1:241
M 76,v[88:91],140
ds_read2_b32 v[166:167],v14 offset0:242 offset1:243
M 76,v[92:95],136
R 196,2,15424
M 76,v[96:99],132
M 76,v[100:103],128
ds_read2_b32 v[168:169],v15 offset0:112 offset1:113
M 76,v[104:107],124
ds_read2_b32 v[170:171],v15 offset0:114 offset1:115
M 76,v[108:111],120
L 15
R 200,2,14912
M 80,v[88:91],144
ds_read2_b32 v[172:173],v15 offset0:240 offset1:241
M 80,v[92:95],140
M 80,v[96:99],136
ds_read2_b32 v[174:175],v15 offset0:242 offset1:243
M 80,v[100:103],132
R 204,2,14400
M 80,v[104:107],128
ds_read2_b32 v[176:177],v16 offset0:112 offset1:113
M 80,v[108:111],124
ds_read2_b32 v[178:179],v16 offset0:114 offset1:115
M 80,v[112:115],120
L 15
M 84,v[88:91],148
R 208,2,13888
M 84,v[92:95],144
ds_read2_b32 v[180:181],v16 offset0:240 offset1:241
M 84,v[96:99],140
ds_read2_b32 v[182:183],v16 offset0:242 offset1:243
M 84,v[100:103],136
R 212,2,13376
M 84,v[104:107],132
M 84,v[108:111],128
M 84,v[112:115],124
M 84,v[116:119],120
L 15
M 64,v[152:155],192
M 64,v[156:159],188
M 64,v[160:163],184
L 12
M 68,v[152:155],196
M 68,v[156:159],192
M 68,v[160:163],188
M 68,v[164:167],184
L 9
M 72,v[152:155],200
M 72,v[156:159],196
M 72,v[160:163],192
ds_read_b64 v[24:25],v3 offset:15488
ds_read_b64 v[26:27],v3 offset:14976
M 72,v[164:167],188
ds_read_b64 v[28:29],v3 offset:14464
ds_read_b64 v[30:31],v3 offset:13952
M 72,v[168:171],184
L 10
ds_read_b64 v[32:33],v3 offset:13440
ds_read_b64 v[34:35],v3 offset:12928
M 76,v[152:155],204
M 76,v[156:159],200
M 76,v[160:163],196
M 76,v[164:167],192
M 76,v[168:171],188
M 76,v[172:175],184
L 9
M 80,v[152:155],208
M 80,v[156:159],204
M 80,v[160:163],200
M 80,v[164:167],196
M 80,v[168:171],192
M 80,v[172:175],188
M 80,v[176:179],184
L 6
M 84,v[152:155],212
M 84,v[156:159],208
M 84,v[160:163],204
M 84,v[164:167],200
M 84,v[168:171],196
M 84,v[172:175],192
M 84,v[176:179],188
M 84,v[180:183],184
L 0
v_and_b32 v36,0xffff0000,v25
v_lshlrev_b32 v37,16,v25
v_and_b32 v38,0xffff0000,v24
v_lshlrev_b32 v39,16,v24
v_fma_f32 v64,v36,s87,v64
v_fma_f32 v65,v37,s87,v65
v_fma_f32 v66,v38,s87,v66
v_fma_f32 v67,v39,s87,v67
v_cvt_pk_bf16_f32 v64,v64,v65
v_cvt_pk_bf16_f32 v65,v66,v67
ds_write_b64 v4,v[64:65] offset:12928
v_and_b32 v36,0xffff0000,v27
v_lshlrev_b32 v37,16,v27
v_and_b32 v38,0xffff0000,v26
v_lshlrev_b32 v39,16,v26
v_fma_f32 v68,v36,s87,v68
v_fma_f32 v69,v37,s87,v69
v_fma_f32 v70,v38,s87,v70
v_fma_f32 v71,v39,s87,v71
v_cvt_pk_bf16_f32 v68,v68,v69
v_cvt_pk_bf16_f32 v69,v70,v71
ds_write_b64 v4,v[68:69] offset:13440
v_and_b32 v36,0xffff0000,v29
v_lshlrev_b32 v37,16,v29
v_and_b32 v38,0xffff0000,v28
v_lshlrev_b32 v39,16,v28
v_fma_f32 v72,v36,s87,v72
v_fma_f32 v73,v37,s87,v73
v_fma_f32 v74,v38,s87,v74
v_fma_f32 v75,v39,s87,v75
v_cvt_pk_bf16_f32 v72,v72,v73
v_cvt_pk_bf16_f32 v73,v74,v75
ds_write_b64 v4,v[72:73] offset:13952
v_and_b32 v36,0xffff0000,v31
v_lshlrev_b32 v37,16,v31
v_and_b32 v38,0xffff0000,v30
v_lshlrev_b32 v39,16,v30
v_fma_f32 v76,v36,s87,v76
v_fma_f32 v77,v37,s87,v77
v_fma_f32 v78,v38,s87,v78
v_fma_f32 v79,v39,s87,v79
v_cvt_pk_bf16_f32 v76,v76,v77
v_cvt_pk_bf16_f32 v77,v78,v79
ds_write_b64 v4,v[76:77] offset:14464
v_and_b32 v36,0xffff0000,v33
v_lshlrev_b32 v37,16,v33
v_and_b32 v38,0xffff0000,v32
v_lshlrev_b32 v39,16,v32
v_fma_f32 v80,v36,s87,v80
v_fma_f32 v81,v37,s87,v81
v_fma_f32 v82,v38,s87,v82
v_fma_f32 v83,v39,s87,v83
v_cvt_pk_bf16_f32 v80,v80,v81
v_cvt_pk_bf16_f32 v81,v82,v83
ds_write_b64 v4,v[80:81] offset:14976
v_and_b32 v36,0xffff0000,v35
v_lshlrev_b32 v37,16,v35
v_and_b32 v38,0xffff0000,v34
v_lshlrev_b32 v39,16,v34
v_fma_f32 v84,v36,s87,v84
v_fma_f32 v85,v37,s87,v85
v_fma_f32 v86,v38,s87,v86
v_fma_f32 v87,v39,s87,v87
v_cvt_pk_bf16_f32 v84,v84,v85
v_cvt_pk_bf16_f32 v85,v86,v87
ds_write_b64 v4,v[84:85] offset:15488
Lcv_e_%=:
L 0
s_barrier
R 152,5,0
R 156,5,512
R 160,5,1024
R 164,5,1536
R 168,5,2048
R 172,5,2560
s_mov_b32 s78,99614720
L 5
buffer_store_dwordx4 v[152:155],v7,s[36:39],s78 offen offset:0 sc0 sc1
s_add_u32 s78,s56,100139008
L 4
buffer_store_dwordx4 v[156:159],v7,s[36:39],s78 offen offset:0 sc0 sc1
s_mov_b32 s78,100663296
L 3
buffer_store_dwordx4 v[160:163],v7,s[36:39],s78 offen offset:0 sc0 sc1
s_add_u32 s78,s56,101187584
L 2
buffer_store_dwordx4 v[164:167],v7,s[36:39],s78 offen offset:0 sc0 sc1
s_mov_b32 s78,101711872
L 1
buffer_store_dwordx4 v[168:171],v7,s[36:39],s78 offen offset:0 sc0 sc1
s_add_u32 s78,s56,102236160
L 0
buffer_store_dwordx4 v[172:175],v7,s[36:39],s78 offen offset:0 sc0 sc1
s_waitcnt vmcnt(0)
s_barrier
s_add_u32 s48,s48,32768
buffer_store_dword v23,v10,s[36:39],s48 offen offset:0 sc0 sc1
s_cmp_eq_u32 s51,2
s_cbranch_scc0 Lcv_f_%=
s_mov_b32 m0,163584
s_mov_b32 s78,3694592
buffer_load_dword v12,s[60:63],s78 offen offset:0  lds
s_mov_b32 m0,163584
s_mov_b32 s78,5541888
buffer_load_dword v12,s[60:63],s78 offen offset:0  lds
Lcv_f_%=:
Lcode_cv_e:
.purgem M
.purgem R
.purgem T
.purgem D
.purgem L
)"
  :: "s"(ws), "s"(sWave), "s"(sBuf), "s"(sAuxOff), "s"(sFoutOff), "s"(sC0), "s"(sC1), "s"(sExp), "v"(vL16), "v"(vPA), "v"(vPB), "v"(vPU), "v"(vPW), "v"(vOR), "v"(vORA), "v"(vOG), "v"(vFA), "v"(vFB), "v"(vFlag), "v"(vTW), "v"(vTX), "s"(w3), "s"(w4) : "v0", "v1", "v2", "v3", "v4", "v5", "v6", "v7", "v8", "v9", "v10", "v11", "v12", "v13", "v14", "v15", "v16", "v17", "v18", "v19", "v20", "v21", "v22", "v23", "v24", "v25", "v26", "v27", "v28", "v29", "v30", "v31", "v32", "v33", "v34", "v35", "v36", "v37", "v38", "v39", "v40", "v41", "v42", "v43", "v44", "v45", "v46", "v47", "v48", "v49", "v50", "v51", "v52", "v53", "v54", "v55", "v56", "v57", "v58", "v59", "v60", "v61", "v62", "v63", "v64", "v65", "v66", "v67", "v68", "v69", "v70", "v71", "v72", "v73", "v74", "v75", "v76", "v77", "v78", "v79", "v80", "v81", "v82", "v83", "v84", "v85", "v86", "v87", "v88", "v89", "v90", "v91", "v92", "v93", "v94", "v95", "v96", "v97", "v98", "v99", "v100", "v101", "v102", "v103", "v104", "v105", "v106", "v107", "v108", "v109", "v110", "v111", "v112", "v113", "v114", "v115", "v116", "v117", "v118", "v119", "v120", "v121", "v122", "v123", "v124", "v125", "v126", "v127", "v128", "v129", "v130", "v131", "v132", "v133", "v134", "v135", "v136", "v137", "v138", "v139", "v140", "v141", "v142", "v143", "v144", "v145", "v146", "v147", "v148", "v149", "v150", "v151", "v152", "v153", "v154", "v155", "v156", "v157", "v158", "v159", "v160", "v161", "v162", "v163", "v164", "v165", "v166", "v167", "v168", "v169", "v170", "v171", "v172", "v173", "v174", "v175", "v176", "v177", "v178", "v179", "v180", "v181", "v182", "v183", "v184", "v185", "v186", "v187", "v188", "v189", "v190", "v191", "v192", "v193", "v194", "v195", "v196", "v197", "v198", "v199", "v200", "v201", "v202", "v203", "v204", "v205", "v206", "v207", "v208", "v209", "v210", "v211", "v212", "v213", "v214", "v215", "v216", "v217", "v218", "v219", "v220", "v221", "v222", "v223", "v224", "v225", "v226", "v227", "v228", "v229", "v230", "v231", "a0", "a1", "a2", "a3", "a4", "a5", "a6", "a7", "a8", "a9", "a10", "a11", "a12", "a13", "a14", "a15", "a16", "a17", "a18", "a19", "a20", "a21", "a22", "a23", "a24", "a25", "a26", "a27", "a28", "a29", "a30", "a31", "a32", "a33", "a34", "a35", "a36", "a37", "a38", "a39", "a40", "a41", "a42", "a43", "a44", "a45", "a46", "a47", "a48", "a49", "a50", "a51", "a52", "a53", "a54", "a55", "a56", "a57", "a58", "a59", "a60", "a61", "a62", "a63", "a64", "a65", "a66", "a67", "a68", "a69", "a70", "a71", "a72", "a73", "a74", "a75", "a76", "a77", "a78", "a79", "a80", "a81", "a82", "a83", "a84", "a85", "a86", "a87", "a88", "a89", "a90", "a91", "a92", "a93", "a94", "a95", "a96", "a97", "a98", "a99", "a100", "a101", "a102", "a103", "a104", "a105", "a106", "a107", "a108", "a109", "a110", "a111", "a112", "a113", "a114", "a115", "a116", "a117", "a118", "a119", "a120", "a121", "a122", "a123", "a124", "a125", "a126", "a127", "a128", "a129", "a130", "a131", "a132", "a133", "a134", "a135", "a136", "a137", "a138", "a139", "a140", "a141", "a142", "a143", "a144", "a145", "a146", "a147", "a148", "a149", "a150", "a151", "a152", "a153", "a154", "a155", "a156", "a157", "a158", "a159", "a160", "a161", "a162", "a163", "a164", "a165", "a166", "a167", "a168", "a169", "a170", "a171", "a172", "a173", "a174", "a175", "a176", "a177", "a178", "a179", "a180", "a181", "a182", "a183", "a184", "a185", "a186", "a187", "a188", "a189", "a190", "a191", "a192", "a193", "a194", "a195", "a196", "a197", "a198", "a199", "a200", "a201", "a202", "a203", "a204", "a205", "a206", "a207", "a208", "a209", "a210", "a211", "a212", "a213", "a214", "a215", "a216", "a217", "a218", "a219", "a220", "a221", "a222", "a223", "a224", "a225", "a226", "a227", "a228", "a229", "a230", "a231", "a232", "a233", "a234", "a235", "a236", "a237", "a238", "a239", "a240", "a241", "a242", "a243", "a244", "a245", "a246", "a247", "a248", "a249", "a250", "a251", "a252", "a253", "a254", "a255", "s36", "s37", "s38", "s39", "s40", "s41", "s42", "s43", "s44", "s45", "s46", "s47", "s48", "s49", "s50", "s51", "s52", "s53", "s54", "s55", "s56", "s57", "s58", "s59", "s60", "s61", "s62", "s63", "s64", "s65", "s66", "s67", "s68", "s69", "s70", "s71", "s72", "s73", "s74", "s75", "s76", "s77", "s78", "s79", "s80", "s81", "s82", "s83", "s84", "s85", "s86", "s87", "s88", "s89", "s90", "s91", "s92", "s93", "s94", "s95", "s96", "s97", "vcc", "scc", "memory");
    LANE_ID(lq_);
    const int g = QT0 + (NCV - 1 - iw) * 164 + min(wave * 64 + lq_, 163);
    if (QTB) touch_lds((const char*)q + (size_t)(g < 32768 ? g : 0) * 128, sScr);
  }
  WAIT_LGKM0();
  __builtin_amdgcn_s_barrier();
  CTS(10);
  }
  const unsigned sDbg0 = 110u * 1048576u + (b * 4 + wave) * 8192;
  constexpr unsigned OOB = 0x80000000u;
#define HLP 0
  if (kind == 6 || (kind == 4 && j4 < HLP)) {
    LANE_ID(lr);
    const int r8 = lr & 7, seg = lr >> 3, j = lr & 15, g = lr >> 4;
    const unsigned sWave = wave, sExp = __builtin_amdgcn_readfirstlane(ep), sG0 = kind == 6 ? slice : 64 + j4, sStep = NRW, sOutOff = kind == 6 ? 256 : 65 + j4, sAuxOff = HLP, sBuf = kind & 2, sRk = slice;
    const unsigned sDbg = sDbg0 + (kind == 6 ? 0 : 1024);
    const unsigned vRF = (wave * 8 + (seg >> 2)) * 1024 + ((seg & 3) * 16 + r8) * 16;
    const unsigned vRwr = seg == 0 ? r8 * 64 + wave * 4 : 512 + lr * 4, vRrd = r8 * 64;
    const unsigned vRP = g == (j >> 2) ? r8 * 64 + 16 + wave * 8 + (seg & 1) * 4 : 768 + lr * 4;
    const unsigned vRS = (wave == 0 && seg < 2) ? r8 * 16 + seg * 256 : OOB, vFlag = lr < 32 ? lr * 4 : OOB;
    const unsigned vL4 = ((lr & 2) * 4 + wave * 2 + (lr & 1)) * 128, vLY = min((wave < 3 ? wave * 32 : 112) + lr, wave < 2 ? wave * 32 + 31 : wave == 2 ? 111 : NCV - 1) * 128;
    const unsigned vQ = r8 * 2048 + wave * 512 + seg * 16;
    const unsigned vY0 = wave * 4096 + lr * 16;
    const unsigned vTr = 4096 + wave * 4096 + ((2 * g + ((j & 3) >> 1)) * 8 + (j >> 2)) * 16 + (j & 1) * 8;
    const unsigned one = g == (j >> 2) ? 0x3f80u << (16 * (j & 1)) : 0u, vSa = (j & 2) ? 0u : one, vSb = (j & 2) ? one : 0u;
    (void)sDbg;
    asm volatile(
R"(.macro M d,w,x
v_mfma_f32_16x16x32_bf16 v[\d:\d+3], \w, v[\x:\x+3], v[\d:\d+3]
.endm
.macro R d,a,o
ds_read_b128 v[\d:\d+3], v\a offset:\o
.endm
.macro T d,a,o
ds_read_b64_tr_b16 \d, v\a offset:\o
.endm
.macro D a,s,o
buffer_load_dwordx4 v\a, s[36:39], s\s offen offset:\o sc1 lds
.endm
.macro L c
s_waitcnt lgkmcnt(\c)
.endm
Lcode_r_b:
s_mov_b64 s[36:37],%0
s_mov_b32 s38,111243392
s_mov_b32 s39,0x00020000
s_mov_b32 s44,%1
s_mov_b32 s45,%2
s_mov_b32 s46,%3
s_mov_b32 s47,%4
s_mov_b32 s49,%5
s_mov_b32 s51,%6
s_mov_b32 s93,%7
s_mov_b32 s53,%8
v_mov_b32 v0,%9
v_mov_b32 v1,%10
v_mov_b32 v2,%11
v_mov_b32 v3,%12
v_mov_b32 v4,%13
v_mov_b32 v5,%14
v_mov_b32 v6,%15
v_mov_b32 v7,%16
v_mov_b32 v8,%17
v_mov_b32 v9,%18
v_mov_b32 v10,%19
v_mov_b32 v11,%20
v_mov_b32 v12,%21
s_mov_b64 s[84:85],%22
s_mov_b32 s86,4194304
s_mov_b32 s87,0x00020000
s_lshl_b32 s55,s45,14
buffer_load_dwordx4 v[164:167],v8,s[84:87],s55 offen offset:0 nt
buffer_load_dwordx4 v[168:171],v8,s[84:87],s55 offen offset:128 nt
buffer_load_dwordx4 v[172:175],v8,s[84:87],s55 offen offset:256 nt
buffer_load_dwordx4 v[176:179],v8,s[84:87],s55 offen offset:384 nt
v_mov_b32 v13,s53
v_mbcnt_lo_u32_b32 v15,-1,0
v_mbcnt_hi_u32_b32 v15,-1,v15
v_lshlrev_b32 v15,4,v15
s_mov_b32 s82,0
s_mov_b32 s63,0x7fff0000
s_mov_b32 s64,0
s_mov_b32 s67,0
s_lshl_b32 s65,s44,12
s_add_u32 s65,s65,4096
s_lshl_b32 s66,s44,7
s_add_u32 s66,s66,109056000
s_mov_b32 s70,0xaaaaaaaa
s_mov_b32 s71,0xaaaaaaaa
s_mov_b32 s72,0xcccccccc
s_mov_b32 s73,0xcccccccc
s_mov_b32 s74,0xff00ff00
s_mov_b32 s75,0xff00ff00
s_mov_b32 s68,0x3a800000
s_mov_b32 s69,0x3727c5ac
s_mov_b32 s76,0xc0135761
s_mov_b32 s77,0x3d372713
v_mov_b32 v32,0x3f803f80
v_mov_b32 v33,0x3f803f80
v_mov_b32 v34,0x3f803f80
v_mov_b32 v35,0x3f803f80
v_mov_b32 v24,0
v_mov_b32 v25,0
v_mov_b32 v26,v11
v_mov_b32 v27,v12
v_mov_b32 v28,0
v_mov_b32 v29,0
s_waitcnt vmcnt(0)
Lr_a0_%=:
s_cmp_ge_u32 s45,34
s_cbranch_scc1 Lr_fp0_%=
s_waitcnt vmcnt(0)
buffer_store_dword v13,v5,s[36:39],s63 offen offset:0 sc0 sc1
s_mov_b32 s63,0x7fff0000
Lr_fp0_%=:
s_waitcnt vmcnt(10)
v_mfma_f32_16x16x32_bf16 v[36:39],v[32:35],v[164:167],0
s_add_u32 s79,s45,s46
s_cmp_lt_u32 s45,64
s_cselect_b32 s78,s49,0
v_mfma_f32_16x16x32_bf16 v[40:43],v[164:167],v[164:167],0
s_cmp_ge_u32 s79,64
s_cselect_b32 s78,s78,0
s_add_u32 s62,s79,s78
M 36,v[32:35],168
s_lshr_b32 s78,s45,1
s_and_b32 s79,s45,1
s_lshl_b32 s79,s79,7
M 40,v[168:171],168
s_lshl_b32 s57,s78,15
s_lshl_b32 s55,s62,14
s_cmp_lt_u32 s62,s47
M 36,v[32:35],172
s_cselect_b32 s55,s55,0x7fff0000
s_add_u32 s57,s57,s79
s_lshl_b32 s59,s78,9
M 40,v[172:175],172
buffer_load_dwordx4 v[196:199],v8,s[84:87],s55 offen offset:0 nt
M 36,v[32:35],176
buffer_load_dwordx4 v[200:203],v8,s[84:87],s55 offen offset:128 nt
M 40,v[176:179],176
buffer_load_dwordx4 v[204:207],v8,s[84:87],s55 offen offset:256 nt
v_mfma_f32_16x16x32_bf16 v[116:119],v[26:29],v[164:167],0
buffer_load_dwordx4 v[208:211],v8,s[84:87],s55 offen offset:384 nt
v_mfma_f32_16x16x32_bf16 v[120:123],v[24:27],v[164:167],0
s_add_u32 s57,s57,8388608
s_add_u32 s59,s59,s79
s_lshl_b32 s60,s45,9
v_mfma_f32_16x16x32_bf16 v[124:127],v[26:29],v[168:171],0
s_add_u32 s58,s57,4096
s_add_u32 s59,s59,106954752
s_add_u32 s60,s60,s66
v_mfma_f32_16x16x32_bf16 v[128:131],v[24:27],v[168:171],0
v_add_f32_dpp v44,v36,v36 row_ror:8 row_mask:0xf bank_mask:0xf
v_cndmask_b32 v45,v40,v41,s[70:71]
v_mfma_f32_16x16x32_bf16 v[132:135],v[26:29],v[172:175],0
ds_write_b32 v2,v44 offset:0
v_mfma_f32_16x16x32_bf16 v[136:139],v[24:27],v[172:175],0
v_cndmask_b32 v46,v42,v43,s[70:71]
v_cndmask_b32 v45,v45,v46,s[72:73]
v_mfma_f32_16x16x32_bf16 v[140:143],v[26:29],v[176:179],0
ds_write_b32 v0,v45 offset:0
v_mfma_f32_16x16x32_bf16 v[144:147],v[24:27],v[176:179],0
L 0
s_barrier
R 52,3,0
R 56,3,16
R 60,3,32
s_cmp_lg_u32 s67,0
s_cbranch_scc1 Lr_b0_%=
s_waitcnt vmcnt(4)
buffer_store_dword v13,v5,s[36:39],s63 offen offset:0 sc0 sc1
s_mov_b32 s63,0x7fff0000
s_mov_b32 s38,111243392
s_cmp_lt_u32 s45,64
s_cselect_b32 s64,-192,0
s_add_u32 s64,s64,256
s_cmp_lt_u32 s45,64
s_cselect_b32 s61,0,32768
s_add_u32 s61,s61,111177728
s_mul_i32 s56,s45,16384
s_add_u32 s56,s56,98566144
Lr_bpy0_%=:
buffer_load_dword v14,v7,s[36:39],s61 offen offset:0 sc1
s_add_u32 s82,s82,1
s_waitcnt vmcnt(0)
v_cmp_eq_u32 vcc,s53,v14
s_nop 0
s_cmp_eq_u64 vcc,-1
s_cbranch_scc1 Lr_bqy0_%=
s_sleep 2
s_cmp_lt_u32 s82,20000
s_cbranch_scc1 Lr_bpy0_%=
Lr_bqy0_%=:
s_add_u32 m0,s65,0
s_nop 0
D 9,56,0
D 9,56,1024
D 9,56,2048
D 9,56,3072
s_waitcnt vmcnt(0)
Lr_b0_%=:
s_waitcnt vmcnt(10)
T v[68:69],10,0
T v[70:71],10,64
T v[72:73],10,1024
T v[74:75],10,1088
T v[76:77],10,2048
T v[78:79],10,2112
T v[80:81],10,3072
T v[82:83],10,3136
s_mov_b32 s45,s62
s_min_u32 s78,s64,s47
s_mul_i32 s56,s45,16384
s_add_u32 s56,s56,98566144
s_cmp_lt_i32 s45,s78
s_cselect_b32 s67,1,0
s_cselect_b32 s56,s56,0x7fff0000
s_add_u32 m0,s65,16384
s_nop 0
D 9,56,0
D 9,56,1024
D 9,56,2048
D 9,56,3072
L 8
v_pk_add_f32 v[52:53],v[52:53],v[54:55]
v_pk_add_f32 v[56:57],v[56:57],v[58:59]
v_pk_add_f32 v[60:61],v[60:61],v[62:63]
v_add_f32 v52,v52,v53
v_pk_add_f32 v[56:57],v[56:57],v[60:61]
v_mul_f32 v52,s68,v52
v_add_f32 v53,v56,v57
v_mul_f32 v53,s68,v53
v_fma_f32 v53,-v52,v52,v53
v_max_f32 v53,0,v53
L 6
v_lshlrev_b32 v84,16,v68
v_and_b32 v85,0xffff0000,v68
v_lshlrev_b32 v86,16,v69
v_and_b32 v87,0xffff0000,v69
v_lshlrev_b32 v88,16,v70
v_and_b32 v89,0xffff0000,v70
v_lshlrev_b32 v90,16,v71
v_and_b32 v91,0xffff0000,v71
L 4
v_lshlrev_b32 v92,16,v72
v_and_b32 v93,0xffff0000,v72
v_lshlrev_b32 v94,16,v73
v_and_b32 v95,0xffff0000,v73
v_lshlrev_b32 v96,16,v74
v_and_b32 v97,0xffff0000,v74
v_lshlrev_b32 v98,16,v75
v_and_b32 v99,0xffff0000,v75
L 2
v_lshlrev_b32 v100,16,v76
v_and_b32 v101,0xffff0000,v76
v_lshlrev_b32 v102,16,v77
v_and_b32 v103,0xffff0000,v77
v_lshlrev_b32 v104,16,v78
v_and_b32 v105,0xffff0000,v78
v_lshlrev_b32 v106,16,v79
v_and_b32 v107,0xffff0000,v79
L 0
v_lshlrev_b32 v108,16,v80
v_and_b32 v109,0xffff0000,v80
v_lshlrev_b32 v110,16,v81
v_and_b32 v111,0xffff0000,v81
v_lshlrev_b32 v112,16,v82
v_and_b32 v113,0xffff0000,v82
v_lshlrev_b32 v114,16,v83
v_and_b32 v115,0xffff0000,v83
v_add_f32 v53,s69,v53
v_rsq_f32 v48,v53
s_nop 0
v_mul_f32_e64 v50,-v52,v48
v_mov_b32 v49,v48
v_mov_b32 v51,v50
v_pk_fma_f32 v[52:53],v[116:117],v[48:49],v[50:51]
v_pk_fma_f32 v[54:55],v[118:119],v[48:49],v[50:51]
v_pk_fma_f32 v[56:57],v[120:121],v[48:49],v[50:51]
v_pk_fma_f32 v[58:59],v[122:123],v[48:49],v[50:51]
v_pk_fma_f32 v[116:117],v[52:53],v[84:85],v[116:117]
v_pk_fma_f32 v[118:119],v[54:55],v[86:87],v[118:119]
v_pk_fma_f32 v[120:121],v[56:57],v[88:89],v[120:121]
v_pk_fma_f32 v[122:123],v[58:59],v[90:91],v[122:123]
v_cvt_pk_bf16_f32 v148,v116,v117
v_cvt_pk_bf16_f32 v149,v118,v119
v_cvt_pk_bf16_f32 v150,v120,v121
v_cvt_pk_bf16_f32 v151,v122,v123
v_pk_fma_f32 v[52:53],v[124:125],v[48:49],v[50:51]
v_pk_fma_f32 v[54:55],v[126:127],v[48:49],v[50:51]
v_pk_fma_f32 v[56:57],v[128:129],v[48:49],v[50:51]
v_pk_fma_f32 v[58:59],v[130:131],v[48:49],v[50:51]
v_pk_fma_f32 v[124:125],v[52:53],v[92:93],v[124:125]
v_pk_fma_f32 v[126:127],v[54:55],v[94:95],v[126:127]
v_pk_fma_f32 v[128:129],v[56:57],v[96:97],v[128:129]
v_pk_fma_f32 v[130:131],v[58:59],v[98:99],v[130:131]
v_cvt_pk_bf16_f32 v152,v124,v125
v_cvt_pk_bf16_f32 v153,v126,v127
v_cvt_pk_bf16_f32 v154,v128,v129
v_cvt_pk_bf16_f32 v155,v130,v131
v_pk_fma_f32 v[52:53],v[132:133],v[48:49],v[50:51]
v_pk_fma_f32 v[54:55],v[134:135],v[48:49],v[50:51]
v_pk_fma_f32 v[56:57],v[136:137],v[48:49],v[50:51]
v_pk_fma_f32 v[58:59],v[138:139],v[48:49],v[50:51]
v_pk_fma_f32 v[132:133],v[52:53],v[100:101],v[132:133]
v_pk_fma_f32 v[134:135],v[54:55],v[102:103],v[134:135]
v_pk_fma_f32 v[136:137],v[56:57],v[104:105],v[136:137]
v_pk_fma_f32 v[138:139],v[58:59],v[106:107],v[138:139]
v_cvt_pk_bf16_f32 v156,v132,v133
v_cvt_pk_bf16_f32 v157,v134,v135
v_cvt_pk_bf16_f32 v158,v136,v137
v_cvt_pk_bf16_f32 v159,v138,v139
v_pk_fma_f32 v[52:53],v[140:141],v[48:49],v[50:51]
v_pk_fma_f32 v[54:55],v[142:143],v[48:49],v[50:51]
v_pk_fma_f32 v[56:57],v[144:145],v[48:49],v[50:51]
v_pk_fma_f32 v[58:59],v[146:147],v[48:49],v[50:51]
v_pk_fma_f32 v[140:141],v[52:53],v[108:109],v[140:141]
v_pk_fma_f32 v[142:143],v[54:55],v[110:111],v[142:143]
v_pk_fma_f32 v[144:145],v[56:57],v[112:113],v[144:145]
v_pk_fma_f32 v[146:147],v[58:59],v[114:115],v[146:147]
v_cvt_pk_bf16_f32 v160,v140,v141
v_cvt_pk_bf16_f32 v161,v142,v143
v_cvt_pk_bf16_f32 v162,v144,v145
v_cvt_pk_bf16_f32 v163,v146,v147
s_waitcnt vmcnt(8)
buffer_store_dword v13,v5,s[36:39],s63 offen offset:0 sc0 sc1
s_mov_b32 s63,0x7fff0000
v_mfma_f32_16x16x32_bf16 v[36:39],v[32:35],v[148:151],0
buffer_store_dwordx4 v[148:151],v1,s[36:39],s57 offen offset:0 sc0 sc1
v_mfma_f32_16x16x32_bf16 v[40:43],v[148:151],v[148:151],0
M 36,v[32:35],152
buffer_store_dwordx4 v[152:155],v1,s[36:39],s57 offen offset:2048 sc0 sc1
M 40,v[152:155],152
M 36,v[32:35],156
buffer_store_dwordx4 v[156:159],v1,s[36:39],s58 offen offset:0 sc0 sc1
M 40,v[156:159],156
M 36,v[32:35],160
buffer_store_dwordx4 v[160:163],v1,s[36:39],s58 offen offset:2048 sc0 sc1
M 40,v[160:163],160
s_nop 5
v_add_f32_dpp v44,v36,v36 row_ror:8 row_mask:0xf bank_mask:0xf
s_nop 0
v_cndmask_b32 v45,v40,v41,s[70:71]
v_cndmask_b32 v46,v42,v43,s[70:71]
ds_write_b32 v2,v44 offset:1024
v_cndmask_b32 v45,v45,v46,s[72:73]
ds_write_b32 v0,v45 offset:1024
L 0
s_barrier
R 52,3,1024
R 56,3,1040
R 60,3,1056
L 0
v_pk_add_f32 v[52:53],v[52:53],v[54:55]
v_pk_add_f32 v[56:57],v[56:57],v[58:59]
v_pk_add_f32 v[60:61],v[60:61],v[62:63]
v_add_f32 v52,v52,v53
v_pk_add_f32 v[56:57],v[56:57],v[60:61]
v_mul_f32 v52,s68,v52
v_add_f32 v53,v56,v57
v_mul_f32 v53,s68,v53
v_fma_f32 v53,-v52,v52,v53
v_max_f32 v53,0,v53
v_add_f32 v54,s69,v53
v_fma_f32 v55,s69,v54,v53
v_rsq_f32 v65,v54
v_rsq_f32 v54,v55
v_mul_f32_e64 v64,-v52,v65
v_mul_f32 v63,v55,v54
v_mul_f32 v61,s76,v54
v_mul_f32 v55,v54,v54
v_mul_f32 v62,s77,v55
v_mul_f32 v62,v62,v61
v_cndmask_b32 v60,v52,v64,s[74:75]
v_cndmask_b32 v61,v61,v65,s[74:75]
buffer_store_dwordx4 v[60:63],v4,s[36:39],s59 offen offset:0 sc0 sc1
s_mov_b32 s63,s60
s_cmp_lt_i32 s45,s47
s_cbranch_scc0 Lr_mid_%=
Lr_a1_%=:
s_cmp_ge_u32 s45,34
s_cbranch_scc1 Lr_fp1_%=
s_waitcnt vmcnt(0)
buffer_store_dword v13,v5,s[36:39],s63 offen offset:0 sc0 sc1
s_mov_b32 s63,0x7fff0000
Lr_fp1_%=:
s_waitcnt vmcnt(10)
v_mfma_f32_16x16x32_bf16 v[36:39],v[32:35],v[196:199],0
s_add_u32 s79,s45,s46
s_cmp_lt_u32 s45,64
s_cselect_b32 s78,s49,0
v_mfma_f32_16x16x32_bf16 v[40:43],v[196:199],v[196:199],0
s_cmp_ge_u32 s79,64
s_cselect_b32 s78,s78,0
s_add_u32 s62,s79,s78
M 36,v[32:35],200
s_lshr_b32 s78,s45,1
s_and_b32 s79,s45,1
s_lshl_b32 s79,s79,7
M 40,v[200:203],200
s_lshl_b32 s57,s78,15
s_lshl_b32 s55,s62,14
s_cmp_lt_u32 s62,s47
M 36,v[32:35],204
s_cselect_b32 s55,s55,0x7fff0000
s_add_u32 s57,s57,s79
s_lshl_b32 s59,s78,9
M 40,v[204:207],204
buffer_load_dwordx4 v[164:167],v8,s[84:87],s55 offen offset:0 nt
M 36,v[32:35],208
buffer_load_dwordx4 v[168:171],v8,s[84:87],s55 offen offset:128 nt
M 40,v[208:211],208
buffer_load_dwordx4 v[172:175],v8,s[84:87],s55 offen offset:256 nt
v_mfma_f32_16x16x32_bf16 v[116:119],v[26:29],v[196:199],0
buffer_load_dwordx4 v[176:179],v8,s[84:87],s55 offen offset:384 nt
v_mfma_f32_16x16x32_bf16 v[120:123],v[24:27],v[196:199],0
s_add_u32 s57,s57,8388608
s_add_u32 s59,s59,s79
s_lshl_b32 s60,s45,9
v_mfma_f32_16x16x32_bf16 v[124:127],v[26:29],v[200:203],0
s_add_u32 s58,s57,4096
s_add_u32 s59,s59,106954752
s_add_u32 s60,s60,s66
v_mfma_f32_16x16x32_bf16 v[128:131],v[24:27],v[200:203],0
v_add_f32_dpp v44,v36,v36 row_ror:8 row_mask:0xf bank_mask:0xf
v_cndmask_b32 v45,v40,v41,s[70:71]
v_mfma_f32_16x16x32_bf16 v[132:135],v[26:29],v[204:207],0
ds_write_b32 v2,v44 offset:0
v_mfma_f32_16x16x32_bf16 v[136:139],v[24:27],v[204:207],0
v_cndmask_b32 v46,v42,v43,s[70:71]
v_cndmask_b32 v45,v45,v46,s[72:73]
v_mfma_f32_16x16x32_bf16 v[140:143],v[26:29],v[208:211],0
ds_write_b32 v0,v45 offset:0
v_mfma_f32_16x16x32_bf16 v[144:147],v[24:27],v[208:211],0
L 0
s_barrier
R 52,3,0
R 56,3,16
R 60,3,32
s_cmp_lg_u32 s67,0
s_cbranch_scc1 Lr_b1_%=
s_waitcnt vmcnt(4)
buffer_store_dword v13,v5,s[36:39],s63 offen offset:0 sc0 sc1
s_mov_b32 s63,0x7fff0000
s_mov_b32 s38,111243392
s_cmp_lt_u32 s45,64
s_cselect_b32 s64,-192,0
s_add_u32 s64,s64,256
s_cmp_lt_u32 s45,64
s_cselect_b32 s61,0,32768
s_add_u32 s61,s61,111177728
s_mul_i32 s56,s45,16384
s_add_u32 s56,s56,98566144
Lr_bpy1_%=:
buffer_load_dword v14,v7,s[36:39],s61 offen offset:0 sc1
s_add_u32 s82,s82,1
s_waitcnt vmcnt(0)
v_cmp_eq_u32 vcc,s53,v14
s_nop 0
s_cmp_eq_u64 vcc,-1
s_cbranch_scc1 Lr_bqy1_%=
s_sleep 2
s_cmp_lt_u32 s82,20000
s_cbranch_scc1 Lr_bpy1_%=
Lr_bqy1_%=:
s_add_u32 m0,s65,16384
s_nop 0
D 9,56,0
D 9,56,1024
D 9,56,2048
D 9,56,3072
s_waitcnt vmcnt(0)
Lr_b1_%=:
s_waitcnt vmcnt(10)
T v[68:69],10,16384
T v[70:71],10,16448
T v[72:73],10,17408
T v[74:75],10,17472
T v[76:77],10,18432
T v[78:79],10,18496
T v[80:81],10,19456
T v[82:83],10,19520
s_mov_b32 s45,s62
s_min_u32 s78,s64,s47
s_mul_i32 s56,s45,16384
s_add_u32 s56,s56,98566144
s_cmp_lt_i32 s45,s78
s_cselect_b32 s67,1,0
s_cselect_b32 s56,s56,0x7fff0000
s_add_u32 m0,s65,0
s_nop 0
D 9,56,0
D 9,56,1024
D 9,56,2048
D 9,56,3072
L 8
v_pk_add_f32 v[52:53],v[52:53],v[54:55]
v_pk_add_f32 v[56:57],v[56:57],v[58:59]
v_pk_add_f32 v[60:61],v[60:61],v[62:63]
v_add_f32 v52,v52,v53
v_pk_add_f32 v[56:57],v[56:57],v[60:61]
v_mul_f32 v52,s68,v52
v_add_f32 v53,v56,v57
v_mul_f32 v53,s68,v53
v_fma_f32 v53,-v52,v52,v53
v_max_f32 v53,0,v53
L 6
v_lshlrev_b32 v84,16,v68
v_and_b32 v85,0xffff0000,v68
v_lshlrev_b32 v86,16,v69
v_and_b32 v87,0xffff0000,v69
v_lshlrev_b32 v88,16,v70
v_and_b32 v89,0xffff0000,v70
v_lshlrev_b32 v90,16,v71
v_and_b32 v91,0xffff0000,v71
L 4
v_lshlrev_b32 v92,16,v72
v_and_b32 v93,0xffff0000,v72
v_lshlrev_b32 v94,16,v73
v_and_b32 v95,0xffff0000,v73
v_lshlrev_b32 v96,16,v74
v_and_b32 v97,0xffff0000,v74
v_lshlrev_b32 v98,16,v75
v_and_b32 v99,0xffff0000,v75
L 2
v_lshlrev_b32 v100,16,v76
)"
R"(v_and_b32 v101,0xffff0000,v76
v_lshlrev_b32 v102,16,v77
v_and_b32 v103,0xffff0000,v77
v_lshlrev_b32 v104,16,v78
v_and_b32 v105,0xffff0000,v78
v_lshlrev_b32 v106,16,v79
v_and_b32 v107,0xffff0000,v79
L 0
v_lshlrev_b32 v108,16,v80
v_and_b32 v109,0xffff0000,v80
v_lshlrev_b32 v110,16,v81
v_and_b32 v111,0xffff0000,v81
v_lshlrev_b32 v112,16,v82
v_and_b32 v113,0xffff0000,v82
v_lshlrev_b32 v114,16,v83
v_and_b32 v115,0xffff0000,v83
v_add_f32 v53,s69,v53
v_rsq_f32 v48,v53
s_nop 0
v_mul_f32_e64 v50,-v52,v48
v_mov_b32 v49,v48
v_mov_b32 v51,v50
v_pk_fma_f32 v[52:53],v[116:117],v[48:49],v[50:51]
v_pk_fma_f32 v[54:55],v[118:119],v[48:49],v[50:51]
v_pk_fma_f32 v[56:57],v[120:121],v[48:49],v[50:51]
v_pk_fma_f32 v[58:59],v[122:123],v[48:49],v[50:51]
v_pk_fma_f32 v[116:117],v[52:53],v[84:85],v[116:117]
v_pk_fma_f32 v[118:119],v[54:55],v[86:87],v[118:119]
v_pk_fma_f32 v[120:121],v[56:57],v[88:89],v[120:121]
v_pk_fma_f32 v[122:123],v[58:59],v[90:91],v[122:123]
v_cvt_pk_bf16_f32 v148,v116,v117
v_cvt_pk_bf16_f32 v149,v118,v119
v_cvt_pk_bf16_f32 v150,v120,v121
v_cvt_pk_bf16_f32 v151,v122,v123
v_pk_fma_f32 v[52:53],v[124:125],v[48:49],v[50:51]
v_pk_fma_f32 v[54:55],v[126:127],v[48:49],v[50:51]
v_pk_fma_f32 v[56:57],v[128:129],v[48:49],v[50:51]
v_pk_fma_f32 v[58:59],v[130:131],v[48:49],v[50:51]
v_pk_fma_f32 v[124:125],v[52:53],v[92:93],v[124:125]
v_pk_fma_f32 v[126:127],v[54:55],v[94:95],v[126:127]
v_pk_fma_f32 v[128:129],v[56:57],v[96:97],v[128:129]
v_pk_fma_f32 v[130:131],v[58:59],v[98:99],v[130:131]
v_cvt_pk_bf16_f32 v152,v124,v125
v_cvt_pk_bf16_f32 v153,v126,v127
v_cvt_pk_bf16_f32 v154,v128,v129
v_cvt_pk_bf16_f32 v155,v130,v131
v_pk_fma_f32 v[52:53],v[132:133],v[48:49],v[50:51]
v_pk_fma_f32 v[54:55],v[134:135],v[48:49],v[50:51]
v_pk_fma_f32 v[56:57],v[136:137],v[48:49],v[50:51]
v_pk_fma_f32 v[58:59],v[138:139],v[48:49],v[50:51]
v_pk_fma_f32 v[132:133],v[52:53],v[100:101],v[132:133]
v_pk_fma_f32 v[134:135],v[54:55],v[102:103],v[134:135]
v_pk_fma_f32 v[136:137],v[56:57],v[104:105],v[136:137]
v_pk_fma_f32 v[138:139],v[58:59],v[106:107],v[138:139]
v_cvt_pk_bf16_f32 v156,v132,v133
v_cvt_pk_bf16_f32 v157,v134,v135
v_cvt_pk_bf16_f32 v158,v136,v137
v_cvt_pk_bf16_f32 v159,v138,v139
v_pk_fma_f32 v[52:53],v[140:141],v[48:49],v[50:51]
v_pk_fma_f32 v[54:55],v[142:143],v[48:49],v[50:51]
v_pk_fma_f32 v[56:57],v[144:145],v[48:49],v[50:51]
v_pk_fma_f32 v[58:59],v[146:147],v[48:49],v[50:51]
v_pk_fma_f32 v[140:141],v[52:53],v[108:109],v[140:141]
v_pk_fma_f32 v[142:143],v[54:55],v[110:111],v[142:143]
v_pk_fma_f32 v[144:145],v[56:57],v[112:113],v[144:145]
v_pk_fma_f32 v[146:147],v[58:59],v[114:115],v[146:147]
v_cvt_pk_bf16_f32 v160,v140,v141
v_cvt_pk_bf16_f32 v161,v142,v143
v_cvt_pk_bf16_f32 v162,v144,v145
v_cvt_pk_bf16_f32 v163,v146,v147
s_waitcnt vmcnt(8)
buffer_store_dword v13,v5,s[36:39],s63 offen offset:0 sc0 sc1
s_mov_b32 s63,0x7fff0000
v_mfma_f32_16x16x32_bf16 v[36:39],v[32:35],v[148:151],0
buffer_store_dwordx4 v[148:151],v1,s[36:39],s57 offen offset:0 sc0 sc1
v_mfma_f32_16x16x32_bf16 v[40:43],v[148:151],v[148:151],0
M 36,v[32:35],152
buffer_store_dwordx4 v[152:155],v1,s[36:39],s57 offen offset:2048 sc0 sc1
M 40,v[152:155],152
M 36,v[32:35],156
buffer_store_dwordx4 v[156:159],v1,s[36:39],s58 offen offset:0 sc0 sc1
M 40,v[156:159],156
M 36,v[32:35],160
buffer_store_dwordx4 v[160:163],v1,s[36:39],s58 offen offset:2048 sc0 sc1
M 40,v[160:163],160
s_nop 5
v_add_f32_dpp v44,v36,v36 row_ror:8 row_mask:0xf bank_mask:0xf
s_nop 0
v_cndmask_b32 v45,v40,v41,s[70:71]
v_cndmask_b32 v46,v42,v43,s[70:71]
ds_write_b32 v2,v44 offset:1024
v_cndmask_b32 v45,v45,v46,s[72:73]
ds_write_b32 v0,v45 offset:1024
L 0
s_barrier
R 52,3,1024
R 56,3,1040
R 60,3,1056
L 0
v_pk_add_f32 v[52:53],v[52:53],v[54:55]
v_pk_add_f32 v[56:57],v[56:57],v[58:59]
v_pk_add_f32 v[60:61],v[60:61],v[62:63]
v_add_f32 v52,v52,v53
v_pk_add_f32 v[56:57],v[56:57],v[60:61]
v_mul_f32 v52,s68,v52
v_add_f32 v53,v56,v57
v_mul_f32 v53,s68,v53
v_fma_f32 v53,-v52,v52,v53
v_max_f32 v53,0,v53
v_add_f32 v54,s69,v53
v_fma_f32 v55,s69,v54,v53
v_rsq_f32 v65,v54
v_rsq_f32 v54,v55
v_mul_f32_e64 v64,-v52,v65
v_mul_f32 v63,v55,v54
v_mul_f32 v61,s76,v54
v_mul_f32 v55,v54,v54
v_mul_f32 v62,s77,v55
v_mul_f32 v62,v62,v61
v_cndmask_b32 v60,v52,v64,s[74:75]
v_cndmask_b32 v61,v61,v65,s[74:75]
buffer_store_dwordx4 v[60:63],v4,s[36:39],s59 offen offset:0 sc0 sc1
s_mov_b32 s63,s60
s_cmp_lt_i32 s45,s47
s_cbranch_scc1 Lr_a0_%=
Lr_mid_%=:
s_waitcnt vmcnt(0)
buffer_store_dword v13,v5,s[36:39],s63 offen offset:0 sc0 sc1
s_mov_b32 s63,0x7fff0000
s_cmp_eq_u32 s51,0
s_cbranch_scc1 Lr_done_%=
s_mov_b32 s45,s93
s_lshr_b32 s78,s45,1
s_and_b32 s79,s45,1
s_lshl_b32 s79,s79,7
s_lshl_b32 s55,s78,15
s_add_u32 s55,s55,s79
s_mul_i32 s56,s45,16384
s_add_u32 s56,s56,98566144
s_lshl_b32 s61,s78,11
s_add_u32 s61,s61,110587904
s_add_u32 s78,s45,s46
s_lshr_b32 s78,s78,1
s_lshl_b32 s62,s78,11
s_add_u32 s62,s62,110587904
Lr_bps_%=:
buffer_load_dword v14,v6,s[36:39],s61 offen offset:0 sc1
s_add_u32 s82,s82,1
s_waitcnt vmcnt(0)
v_cmp_eq_u32 vcc,s53,v14
s_nop 0
s_cmp_eq_u64 vcc,-1
s_cbranch_scc1 Lr_bqs_%=
s_sleep 2
s_cmp_lt_u32 s82,20000
s_cbranch_scc1 Lr_bps_%=
Lr_bqs_%=:
s_add_u32 s78,s55,71303168
buffer_load_dwordx4 v[164:167],v1,s[36:39],s78 offen offset:0 sc1
buffer_load_dwordx4 v[168:171],v1,s[36:39],s78 offen offset:2048 sc1
s_add_u32 s78,s55,71307264
buffer_load_dwordx4 v[172:175],v1,s[36:39],s78 offen offset:0 sc1
buffer_load_dwordx4 v[176:179],v1,s[36:39],s78 offen offset:2048 sc1
s_add_u32 s78,s55,89128960
buffer_load_dwordx4 v[180:183],v1,s[36:39],s78 offen offset:0 sc1
buffer_load_dwordx4 v[184:187],v1,s[36:39],s78 offen offset:2048 sc1
s_add_u32 s78,s55,89133056
buffer_load_dwordx4 v[188:191],v1,s[36:39],s78 offen offset:0 sc1
buffer_load_dwordx4 v[192:195],v1,s[36:39],s78 offen offset:2048 sc1
s_add_u32 m0,s65,0
s_nop 0
D 9,56,0
D 9,56,1024
D 9,56,2048
D 9,56,3072
buffer_load_dword v14,v6,s[36:39],s62 offen offset:0 sc1
s_lshr_b32 s78,s45,1
s_and_b32 s79,s45,1
s_lshl_b32 s79,s79,7
s_lshl_b32 s57,s78,15
s_add_u32 s57,s57,s79
s_add_u32 s57,s57,12582912
s_add_u32 s58,s57,4096
s_lshl_b32 s59,s78,9
s_add_u32 s59,s59,s79
s_add_u32 s59,s59,107020288
s_lshl_b32 s60,s45,9
s_add_u32 s60,s60,s66
s_add_u32 s60,s60,131072
s_add_u32 s45,s45,s46
s_lshr_b32 s78,s45,1
s_and_b32 s79,s45,1
s_lshl_b32 s79,s79,7
s_lshl_b32 s55,s78,15
s_add_u32 s55,s55,s79
s_mul_i32 s56,s45,16384
s_add_u32 s56,s56,98566144
s_lshl_b32 s61,s78,11
s_add_u32 s61,s61,110587904
s_add_u32 s78,s45,s46
s_lshr_b32 s78,s78,1
s_lshl_b32 s62,s78,11
s_add_u32 s62,s62,110587904
s_waitcnt vmcnt(0)
Lr_it0_%=:
T v[68:69],10,0
T v[70:71],10,64
T v[72:73],10,1024
T v[74:75],10,1088
T v[76:77],10,2048
T v[78:79],10,2112
T v[80:81],10,3072
T v[82:83],10,3136
s_nop 0
v_cmp_eq_u32 vcc,s53,v14
s_nop 0
s_cmp_eq_u64 vcc,-1
s_cselect_b32 s78,s45,256
s_cmp_lt_u32 s78,256
s_cbranch_scc1 Lr_ta0_%=
buffer_load_dword v14,v6,s[36:39],s61 offen offset:0 sc1
v_mfma_f32_16x16x32_bf16 v[116:119],v[26:29],v[164:167],0
L 6
v_lshlrev_b32 v84,16,v68
v_and_b32 v85,0xffff0000,v68
v_mfma_f32_16x16x32_bf16 v[120:123],v[24:27],v[164:167],0
L 4
v_lshlrev_b32 v86,16,v69
v_and_b32 v87,0xffff0000,v69
M 116,v[26:29],180
L 2
v_lshlrev_b32 v88,16,v70
v_and_b32 v89,0xffff0000,v70
M 120,v[24:27],180
L 0
v_lshlrev_b32 v90,16,v71
v_and_b32 v91,0xffff0000,v71
v_mfma_f32_16x16x32_bf16 v[124:127],v[26:29],v[168:171],0
v_lshlrev_b32 v92,16,v72
v_and_b32 v93,0xffff0000,v72
v_mfma_f32_16x16x32_bf16 v[128:131],v[24:27],v[168:171],0
v_lshlrev_b32 v94,16,v73
v_and_b32 v95,0xffff0000,v73
M 124,v[26:29],184
v_cvt_pk_bf16_f32 v148,v116,v117
v_cvt_pk_bf16_f32 v149,v118,v119
M 128,v[24:27],184
v_cvt_pk_bf16_f32 v150,v120,v121
v_cvt_pk_bf16_f32 v151,v122,v123
v_mfma_f32_16x16x32_bf16 v[132:135],v[26:29],v[172:175],0
v_lshlrev_b32 v96,16,v74
v_and_b32 v97,0xffff0000,v74
v_mfma_f32_16x16x32_bf16 v[136:139],v[24:27],v[172:175],0
v_lshlrev_b32 v98,16,v75
v_and_b32 v99,0xffff0000,v75
M 132,v[26:29],188
v_cvt_pk_bf16_f32 v152,v124,v125
v_cvt_pk_bf16_f32 v153,v126,v127
M 136,v[24:27],188
v_cvt_pk_bf16_f32 v154,v128,v129
v_cvt_pk_bf16_f32 v155,v130,v131
v_mfma_f32_16x16x32_bf16 v[140:143],v[26:29],v[176:179],0
v_lshlrev_b32 v100,16,v76
v_and_b32 v101,0xffff0000,v76
v_mfma_f32_16x16x32_bf16 v[144:147],v[24:27],v[176:179],0
v_lshlrev_b32 v102,16,v77
v_and_b32 v103,0xffff0000,v77
M 140,v[26:29],192
v_cvt_pk_bf16_f32 v156,v132,v133
v_cvt_pk_bf16_f32 v157,v134,v135
M 144,v[24:27],192
v_cvt_pk_bf16_f32 v158,v136,v137
v_cvt_pk_bf16_f32 v159,v138,v139
v_mfma_f32_16x16x32_bf16 v[36:39],v[32:35],v[148:151],0
v_lshlrev_b32 v104,16,v78
v_and_b32 v105,0xffff0000,v78
v_mfma_f32_16x16x32_bf16 v[40:43],v[148:151],v[148:151],0
v_lshlrev_b32 v106,16,v79
v_and_b32 v107,0xffff0000,v79
M 36,v[32:35],152
v_cvt_pk_bf16_f32 v160,v140,v141
v_cvt_pk_bf16_f32 v161,v142,v143
M 40,v[152:155],152
v_cvt_pk_bf16_f32 v162,v144,v145
v_cvt_pk_bf16_f32 v163,v146,v147
M 36,v[32:35],156
v_lshlrev_b32 v108,16,v80
v_and_b32 v109,0xffff0000,v80
M 40,v[156:159],156
v_lshlrev_b32 v110,16,v81
v_and_b32 v111,0xffff0000,v81
M 36,v[32:35],160
v_lshlrev_b32 v112,16,v82
v_and_b32 v113,0xffff0000,v82
M 40,v[160:163],160
v_lshlrev_b32 v114,16,v83
v_and_b32 v115,0xffff0000,v83
s_nop 2
v_add_f32_dpp v44,v36,v36 row_ror:8 row_mask:0xf bank_mask:0xf
s_nop 1
v_cndmask_b32 v45,v40,v41,s[70:71]
v_cndmask_b32 v46,v42,v43,s[70:71]
ds_write_b32 v2,v44 offset:0
v_cndmask_b32 v45,v45,v46,s[72:73]
ds_write_b32 v0,v45 offset:0
L 0
s_barrier
R 52,3,0
R 56,3,16
R 60,3,32
L 0
v_pk_add_f32 v[52:53],v[52:53],v[54:55]
v_pk_add_f32 v[56:57],v[56:57],v[58:59]
v_pk_add_f32 v[60:61],v[60:61],v[62:63]
v_add_f32 v52,v52,v53
v_pk_add_f32 v[56:57],v[56:57],v[60:61]
v_mul_f32 v52,s68,v52
v_add_f32 v53,v56,v57
v_mul_f32 v53,s68,v53
v_fma_f32 v53,-v52,v52,v53
v_max_f32 v53,0,v53
s_waitcnt vmcnt(1)
buffer_store_dword v13,v5,s[36:39],s63 offen offset:0 sc0 sc1
s_mov_b32 s63,0x7fff0000
s_waitcnt vmcnt(1)
v_cmp_eq_u32 vcc,s53,v14
s_nop 0
s_cmp_eq_u64 vcc,-1
s_cselect_b32 s78,s45,256
s_cmp_lt_u32 s78,256
s_cbranch_scc1 Lr_tb0_%=
buffer_load_dword v14,v6,s[36:39],s61 offen offset:0 sc1
v_add_f32 v53,s69,v53
v_rsq_f32 v48,v53
s_nop 0
v_mul_f32_e64 v50,-v52,v48
v_mov_b32 v49,v48
v_mov_b32 v51,v50
v_pk_fma_f32 v[52:53],v[116:117],v[48:49],v[50:51]
v_pk_fma_f32 v[54:55],v[118:119],v[48:49],v[50:51]
v_pk_fma_f32 v[56:57],v[120:121],v[48:49],v[50:51]
v_pk_fma_f32 v[58:59],v[122:123],v[48:49],v[50:51]
v_pk_fma_f32 v[116:117],v[52:53],v[84:85],v[116:117]
v_pk_fma_f32 v[118:119],v[54:55],v[86:87],v[118:119]
v_pk_fma_f32 v[120:121],v[56:57],v[88:89],v[120:121]
v_pk_fma_f32 v[122:123],v[58:59],v[90:91],v[122:123]
v_cvt_pk_bf16_f32 v148,v116,v117
v_cvt_pk_bf16_f32 v149,v118,v119
v_cvt_pk_bf16_f32 v150,v120,v121
v_cvt_pk_bf16_f32 v151,v122,v123
v_pk_fma_f32 v[52:53],v[124:125],v[48:49],v[50:51]
v_pk_fma_f32 v[54:55],v[126:127],v[48:49],v[50:51]
v_pk_fma_f32 v[56:57],v[128:129],v[48:49],v[50:51]
v_pk_fma_f32 v[58:59],v[130:131],v[48:49],v[50:51]
v_pk_fma_f32 v[124:125],v[52:53],v[92:93],v[124:125]
v_pk_fma_f32 v[126:127],v[54:55],v[94:95],v[126:127]
v_pk_fma_f32 v[128:129],v[56:57],v[96:97],v[128:129]
v_pk_fma_f32 v[130:131],v[58:59],v[98:99],v[130:131]
v_cvt_pk_bf16_f32 v152,v124,v125
v_cvt_pk_bf16_f32 v153,v126,v127
v_cvt_pk_bf16_f32 v154,v128,v129
v_cvt_pk_bf16_f32 v155,v130,v131
v_pk_fma_f32 v[52:53],v[132:133],v[48:49],v[50:51]
v_pk_fma_f32 v[54:55],v[134:135],v[48:49],v[50:51]
v_pk_fma_f32 v[56:57],v[136:137],v[48:49],v[50:51]
v_pk_fma_f32 v[58:59],v[138:139],v[48:49],v[50:51]
v_pk_fma_f32 v[132:133],v[52:53],v[100:101],v[132:133]
v_pk_fma_f32 v[134:135],v[54:55],v[102:103],v[134:135]
v_pk_fma_f32 v[136:137],v[56:57],v[104:105],v[136:137]
v_pk_fma_f32 v[138:139],v[58:59],v[106:107],v[138:139]
v_cvt_pk_bf16_f32 v156,v132,v133
v_cvt_pk_bf16_f32 v157,v134,v135
v_cvt_pk_bf16_f32 v158,v136,v137
v_cvt_pk_bf16_f32 v159,v138,v139
v_pk_fma_f32 v[52:53],v[140:141],v[48:49],v[50:51]
v_pk_fma_f32 v[54:55],v[142:143],v[48:49],v[50:51]
v_pk_fma_f32 v[56:57],v[144:145],v[48:49],v[50:51]
v_pk_fma_f32 v[58:59],v[146:147],v[48:49],v[50:51]
v_pk_fma_f32 v[140:141],v[52:53],v[108:109],v[140:141]
v_pk_fma_f32 v[142:143],v[54:55],v[110:111],v[142:143]
v_pk_fma_f32 v[144:145],v[56:57],v[112:113],v[144:145]
v_pk_fma_f32 v[146:147],v[58:59],v[114:115],v[146:147]
v_cvt_pk_bf16_f32 v160,v140,v141
v_cvt_pk_bf16_f32 v161,v142,v143
v_cvt_pk_bf16_f32 v162,v144,v145
v_cvt_pk_bf16_f32 v163,v146,v147
v_mfma_f32_16x16x32_bf16 v[36:39],v[32:35],v[148:151],0
buffer_store_dwordx4 v[148:151],v1,s[36:39],s57 offen offset:0 sc0 sc1
v_mfma_f32_16x16x32_bf16 v[40:43],v[148:151],v[148:151],0
M 36,v[32:35],152
buffer_store_dwordx4 v[152:155],v1,s[36:39],s57 offen offset:2048 sc0 sc1
M 40,v[152:155],152
M 36,v[32:35],156
buffer_store_dwordx4 v[156:159],v1,s[36:39],s58 offen offset:0 sc0 sc1
M 40,v[156:159],156
M 36,v[32:35],160
buffer_store_dwordx4 v[160:163],v1,s[36:39],s58 offen offset:2048 sc0 sc1
M 40,v[160:163],160
s_nop 5
v_add_f32_dpp v44,v36,v36 row_ror:8 row_mask:0xf bank_mask:0xf
s_nop 0
v_cndmask_b32 v45,v40,v41,s[70:71]
v_cndmask_b32 v46,v42,v43,s[70:71]
ds_write_b32 v2,v44 offset:1024
v_cndmask_b32 v45,v45,v46,s[72:73]
ds_write_b32 v0,v45 offset:1024
L 0
s_barrier
R 52,3,1024
R 56,3,1040
R 60,3,1056
L 0
v_pk_add_f32 v[52:53],v[52:53],v[54:55]
v_pk_add_f32 v[56:57],v[56:57],v[58:59]
v_pk_add_f32 v[60:61],v[60:61],v[62:63]
v_add_f32 v52,v52,v53
v_pk_add_f32 v[56:57],v[56:57],v[60:61]
v_mul_f32 v52,s68,v52
v_add_f32 v53,v56,v57
v_mul_f32 v53,s68,v53
v_fma_f32 v53,-v52,v52,v53
v_max_f32 v53,0,v53
v_add_f32 v54,s69,v53
v_fma_f32 v55,s69,v54,v53
v_rsq_f32 v65,v54
v_rsq_f32 v54,v55
v_mul_f32_e64 v64,-v52,v65
v_mul_f32 v63,v55,v54
v_mul_f32 v61,s76,v54
v_mul_f32 v55,v54,v54
v_mul_f32 v62,s77,v55
v_mul_f32 v62,v62,v61
v_cndmask_b32 v60,v52,v64,s[74:75]
v_cndmask_b32 v61,v61,v65,s[74:75]
buffer_store_dwordx4 v[60:63],v4,s[36:39],s59 offen offset:0 sc0 sc1
s_mov_b32 s63,s60
s_waitcnt vmcnt(5)
v_cmp_eq_u32 vcc,s53,v14
s_nop 0
s_cmp_eq_u64 vcc,-1
s_cselect_b32 s78,s45,256
s_cmp_lt_u32 s78,256
s_cbranch_scc1 Lr_tc0_%=
s_waitcnt vmcnt(0)
buffer_store_dword v13,v5,s[36:39],s63 offen offset:0 sc0 sc1
s_mov_b32 s63,0x7fff0000
s_cmp_ge_u32 s45,256
s_cbranch_scc1 Lr_done_%=
Lr_bp0_%=:
buffer_load_dword v14,v6,s[36:39],s61 offen offset:0 sc1
s_add_u32 s82,s82,1
s_waitcnt vmcnt(0)
v_cmp_eq_u32 vcc,s53,v14
s_nop 0
s_cmp_eq_u64 vcc,-1
s_cbranch_scc1 Lr_bq0_%=
s_sleep 2
s_cmp_lt_u32 s82,20000
s_cbranch_scc1 Lr_bp0_%=
Lr_bq0_%=:
s_add_u32 s78,s55,71303168
buffer_load_dwordx4 v[196:199],v1,s[36:39],s78 offen offset:0 sc1
buffer_load_dwordx4 v[200:203],v1,s[36:39],s78 offen offset:2048 sc1
s_add_u32 s78,s55,71307264
buffer_load_dwordx4 v[204:207],v1,s[36:39],s78 offen offset:0 sc1
buffer_load_dwordx4 v[208:211],v1,s[36:39],s78 offen offset:2048 sc1
s_add_u32 s78,s55,89128960
buffer_load_dwordx4 v[212:215],v1,s[36:39],s78 offen offset:0 sc1
buffer_load_dwordx4 v[216:219],v1,s[36:39],s78 offen offset:2048 sc1
s_add_u32 s78,s55,89133056
buffer_load_dwordx4 v[220:223],v1,s[36:39],s78 offen offset:0 sc1
buffer_load_dwordx4 v[224:227],v1,s[36:39],s78 offen offset:2048 sc1
s_add_u32 m0,s65,16384
s_nop 0
D 9,56,0
D 9,56,1024
D 9,56,2048
D 9,56,3072
buffer_load_dword v14,v6,s[36:39],s62 offen offset:0 sc1
s_lshr_b32 s78,s45,1
s_and_b32 s79,s45,1
s_lshl_b32 s79,s79,7
s_lshl_b32 s57,s78,15
s_add_u32 s57,s57,s79
s_add_u32 s57,s57,12582912
s_add_u32 s58,s57,4096
s_lshl_b32 s59,s78,9
s_add_u32 s59,s59,s79
s_add_u32 s59,s59,107020288
s_lshl_b32 s60,s45,9
s_add_u32 s60,s60,s66
s_add_u32 s60,s60,131072
s_add_u32 s45,s45,s46
s_lshr_b32 s78,s45,1
s_and_b32 s79,s45,1
s_lshl_b32 s79,s79,7
s_lshl_b32 s55,s78,15
s_add_u32 s55,s55,s79
s_mul_i32 s56,s45,16384
s_add_u32 s56,s56,98566144
)"
R"(s_lshl_b32 s61,s78,11
s_add_u32 s61,s61,110587904
s_add_u32 s78,s45,s46
s_lshr_b32 s78,s78,1
s_lshl_b32 s62,s78,11
s_add_u32 s62,s62,110587904
s_waitcnt vmcnt(0)
s_branch Lr_it1_%=
Lr_ta0_%=:
v_mfma_f32_16x16x32_bf16 v[116:119],v[26:29],v[164:167],0
s_add_u32 s78,s55,71303168
L 6
L 4
v_mfma_f32_16x16x32_bf16 v[120:123],v[24:27],v[164:167],0
L 2
buffer_load_dwordx4 v[196:199],v1,s[36:39],s78 offen offset:0 sc1
M 116,v[26:29],180
L 0
buffer_load_dwordx4 v[200:203],v1,s[36:39],s78 offen offset:2048 sc1
M 120,v[24:27],180
s_add_u32 s78,s55,71307264
v_lshlrev_b32 v84,16,v68
v_and_b32 v85,0xffff0000,v68
v_mfma_f32_16x16x32_bf16 v[124:127],v[26:29],v[168:171],0
buffer_load_dwordx4 v[204:207],v1,s[36:39],s78 offen offset:0 sc1
v_mfma_f32_16x16x32_bf16 v[128:131],v[24:27],v[168:171],0
buffer_load_dwordx4 v[208:211],v1,s[36:39],s78 offen offset:2048 sc1
M 124,v[26:29],184
s_add_u32 s78,s55,89128960
v_cvt_pk_bf16_f32 v148,v116,v117
v_cvt_pk_bf16_f32 v149,v118,v119
M 128,v[24:27],184
buffer_load_dwordx4 v[212:215],v1,s[36:39],s78 offen offset:0 sc1
v_mfma_f32_16x16x32_bf16 v[132:135],v[26:29],v[172:175],0
buffer_load_dwordx4 v[216:219],v1,s[36:39],s78 offen offset:2048 sc1
v_mfma_f32_16x16x32_bf16 v[136:139],v[24:27],v[172:175],0
s_add_u32 s78,s55,89133056
v_cvt_pk_bf16_f32 v150,v120,v121
v_cvt_pk_bf16_f32 v151,v122,v123
M 132,v[26:29],188
buffer_load_dwordx4 v[220:223],v1,s[36:39],s78 offen offset:0 sc1
M 136,v[24:27],188
buffer_load_dwordx4 v[224:227],v1,s[36:39],s78 offen offset:2048 sc1
v_mfma_f32_16x16x32_bf16 v[140:143],v[26:29],v[176:179],0
s_add_u32 m0,s65,16384
v_cvt_pk_bf16_f32 v152,v124,v125
v_cvt_pk_bf16_f32 v153,v126,v127
v_mfma_f32_16x16x32_bf16 v[144:147],v[24:27],v[176:179],0
D 9,56,0
M 140,v[26:29],192
D 9,56,1024
M 144,v[24:27],192
D 9,56,2048
v_mfma_f32_16x16x32_bf16 v[36:39],v[32:35],v[148:151],0
v_cvt_pk_bf16_f32 v154,v128,v129
v_cvt_pk_bf16_f32 v155,v130,v131
v_mfma_f32_16x16x32_bf16 v[40:43],v[148:151],v[148:151],0
D 9,56,3072
M 36,v[32:35],152
v_cvt_pk_bf16_f32 v156,v132,v133
v_cvt_pk_bf16_f32 v157,v134,v135
v_cvt_pk_bf16_f32 v158,v136,v137
v_cvt_pk_bf16_f32 v159,v138,v139
M 40,v[152:155],152
v_cvt_pk_bf16_f32 v160,v140,v141
v_cvt_pk_bf16_f32 v161,v142,v143
M 36,v[32:35],156
v_cvt_pk_bf16_f32 v162,v144,v145
v_cvt_pk_bf16_f32 v163,v146,v147
M 40,v[156:159],156
v_lshlrev_b32 v86,16,v69
v_and_b32 v87,0xffff0000,v69
M 36,v[32:35],160
v_lshlrev_b32 v88,16,v70
v_and_b32 v89,0xffff0000,v70
M 40,v[160:163],160
v_lshlrev_b32 v90,16,v71
v_and_b32 v91,0xffff0000,v71
v_lshlrev_b32 v92,16,v72
v_and_b32 v93,0xffff0000,v72
v_lshlrev_b32 v94,16,v73
v_and_b32 v95,0xffff0000,v73
v_lshlrev_b32 v96,16,v74
v_and_b32 v97,0xffff0000,v74
v_lshlrev_b32 v98,16,v75
v_and_b32 v99,0xffff0000,v75
v_lshlrev_b32 v100,16,v76
v_and_b32 v101,0xffff0000,v76
v_lshlrev_b32 v102,16,v77
v_and_b32 v103,0xffff0000,v77
v_lshlrev_b32 v104,16,v78
v_and_b32 v105,0xffff0000,v78
v_lshlrev_b32 v106,16,v79
v_and_b32 v107,0xffff0000,v79
v_lshlrev_b32 v108,16,v80
v_and_b32 v109,0xffff0000,v80
v_lshlrev_b32 v110,16,v81
v_and_b32 v111,0xffff0000,v81
v_lshlrev_b32 v112,16,v82
v_and_b32 v113,0xffff0000,v82
v_lshlrev_b32 v114,16,v83
v_and_b32 v115,0xffff0000,v83
v_add_f32_dpp v44,v36,v36 row_ror:8 row_mask:0xf bank_mask:0xf
v_cndmask_b32 v45,v40,v41,s[70:71]
v_cndmask_b32 v46,v42,v43,s[70:71]
ds_write_b32 v2,v44 offset:0
v_cndmask_b32 v45,v45,v46,s[72:73]
ds_write_b32 v0,v45 offset:0
L 0
s_barrier
R 52,3,0
R 56,3,16
R 60,3,32
L 0
v_pk_add_f32 v[52:53],v[52:53],v[54:55]
v_pk_add_f32 v[56:57],v[56:57],v[58:59]
v_pk_add_f32 v[60:61],v[60:61],v[62:63]
v_add_f32 v52,v52,v53
v_pk_add_f32 v[56:57],v[56:57],v[60:61]
v_mul_f32 v52,s68,v52
v_add_f32 v53,v56,v57
v_mul_f32 v53,s68,v53
v_fma_f32 v53,-v52,v52,v53
v_max_f32 v53,0,v53
buffer_load_dword v14,v6,s[36:39],s62 offen offset:0 sc1
v_add_f32 v53,s69,v53
v_rsq_f32 v48,v53
s_nop 0
v_mul_f32_e64 v50,-v52,v48
v_mov_b32 v49,v48
v_mov_b32 v51,v50
v_pk_fma_f32 v[52:53],v[116:117],v[48:49],v[50:51]
v_pk_fma_f32 v[54:55],v[118:119],v[48:49],v[50:51]
v_pk_fma_f32 v[56:57],v[120:121],v[48:49],v[50:51]
v_pk_fma_f32 v[58:59],v[122:123],v[48:49],v[50:51]
v_pk_fma_f32 v[116:117],v[52:53],v[84:85],v[116:117]
v_pk_fma_f32 v[118:119],v[54:55],v[86:87],v[118:119]
v_pk_fma_f32 v[120:121],v[56:57],v[88:89],v[120:121]
v_pk_fma_f32 v[122:123],v[58:59],v[90:91],v[122:123]
v_cvt_pk_bf16_f32 v148,v116,v117
v_cvt_pk_bf16_f32 v149,v118,v119
v_cvt_pk_bf16_f32 v150,v120,v121
v_cvt_pk_bf16_f32 v151,v122,v123
v_pk_fma_f32 v[52:53],v[124:125],v[48:49],v[50:51]
v_pk_fma_f32 v[54:55],v[126:127],v[48:49],v[50:51]
v_pk_fma_f32 v[56:57],v[128:129],v[48:49],v[50:51]
v_pk_fma_f32 v[58:59],v[130:131],v[48:49],v[50:51]
v_pk_fma_f32 v[124:125],v[52:53],v[92:93],v[124:125]
v_pk_fma_f32 v[126:127],v[54:55],v[94:95],v[126:127]
v_pk_fma_f32 v[128:129],v[56:57],v[96:97],v[128:129]
v_pk_fma_f32 v[130:131],v[58:59],v[98:99],v[130:131]
v_cvt_pk_bf16_f32 v152,v124,v125
v_cvt_pk_bf16_f32 v153,v126,v127
v_cvt_pk_bf16_f32 v154,v128,v129
v_cvt_pk_bf16_f32 v155,v130,v131
v_pk_fma_f32 v[52:53],v[132:133],v[48:49],v[50:51]
v_pk_fma_f32 v[54:55],v[134:135],v[48:49],v[50:51]
v_pk_fma_f32 v[56:57],v[136:137],v[48:49],v[50:51]
v_pk_fma_f32 v[58:59],v[138:139],v[48:49],v[50:51]
v_pk_fma_f32 v[132:133],v[52:53],v[100:101],v[132:133]
v_pk_fma_f32 v[134:135],v[54:55],v[102:103],v[134:135]
v_pk_fma_f32 v[136:137],v[56:57],v[104:105],v[136:137]
v_pk_fma_f32 v[138:139],v[58:59],v[106:107],v[138:139]
v_cvt_pk_bf16_f32 v156,v132,v133
v_cvt_pk_bf16_f32 v157,v134,v135
v_cvt_pk_bf16_f32 v158,v136,v137
v_cvt_pk_bf16_f32 v159,v138,v139
v_pk_fma_f32 v[52:53],v[140:141],v[48:49],v[50:51]
v_pk_fma_f32 v[54:55],v[142:143],v[48:49],v[50:51]
v_pk_fma_f32 v[56:57],v[144:145],v[48:49],v[50:51]
v_pk_fma_f32 v[58:59],v[146:147],v[48:49],v[50:51]
v_pk_fma_f32 v[140:141],v[52:53],v[108:109],v[140:141]
v_pk_fma_f32 v[142:143],v[54:55],v[110:111],v[142:143]
v_pk_fma_f32 v[144:145],v[56:57],v[112:113],v[144:145]
v_pk_fma_f32 v[146:147],v[58:59],v[114:115],v[146:147]
v_cvt_pk_bf16_f32 v160,v140,v141
v_cvt_pk_bf16_f32 v161,v142,v143
v_cvt_pk_bf16_f32 v162,v144,v145
v_cvt_pk_bf16_f32 v163,v146,v147
s_waitcnt vmcnt(13)
buffer_store_dword v13,v5,s[36:39],s63 offen offset:0 sc0 sc1
s_mov_b32 s63,0x7fff0000
v_mfma_f32_16x16x32_bf16 v[36:39],v[32:35],v[148:151],0
buffer_store_dwordx4 v[148:151],v1,s[36:39],s57 offen offset:0 sc0 sc1
v_mfma_f32_16x16x32_bf16 v[40:43],v[148:151],v[148:151],0
M 36,v[32:35],152
buffer_store_dwordx4 v[152:155],v1,s[36:39],s57 offen offset:2048 sc0 sc1
M 40,v[152:155],152
M 36,v[32:35],156
buffer_store_dwordx4 v[156:159],v1,s[36:39],s58 offen offset:0 sc0 sc1
M 40,v[156:159],156
M 36,v[32:35],160
buffer_store_dwordx4 v[160:163],v1,s[36:39],s58 offen offset:2048 sc0 sc1
M 40,v[160:163],160
s_nop 5
v_add_f32_dpp v44,v36,v36 row_ror:8 row_mask:0xf bank_mask:0xf
s_nop 0
v_cndmask_b32 v45,v40,v41,s[70:71]
v_cndmask_b32 v46,v42,v43,s[70:71]
ds_write_b32 v2,v44 offset:1024
v_cndmask_b32 v45,v45,v46,s[72:73]
ds_write_b32 v0,v45 offset:1024
L 0
s_barrier
R 52,3,1024
R 56,3,1040
R 60,3,1056
L 0
v_pk_add_f32 v[52:53],v[52:53],v[54:55]
v_pk_add_f32 v[56:57],v[56:57],v[58:59]
v_pk_add_f32 v[60:61],v[60:61],v[62:63]
v_add_f32 v52,v52,v53
v_pk_add_f32 v[56:57],v[56:57],v[60:61]
v_mul_f32 v52,s68,v52
v_add_f32 v53,v56,v57
v_mul_f32 v53,s68,v53
v_fma_f32 v53,-v52,v52,v53
v_max_f32 v53,0,v53
v_add_f32 v54,s69,v53
v_fma_f32 v55,s69,v54,v53
v_rsq_f32 v65,v54
v_rsq_f32 v54,v55
v_mul_f32_e64 v64,-v52,v65
v_mul_f32 v63,v55,v54
v_mul_f32 v61,s76,v54
v_mul_f32 v55,v54,v54
v_mul_f32 v62,s77,v55
v_mul_f32 v62,v62,v61
v_cndmask_b32 v60,v52,v64,s[74:75]
v_cndmask_b32 v61,v61,v65,s[74:75]
buffer_store_dwordx4 v[60:63],v4,s[36:39],s59 offen offset:0 sc0 sc1
s_mov_b32 s63,s60
s_lshr_b32 s78,s45,1
s_and_b32 s79,s45,1
s_lshl_b32 s79,s79,7
s_lshl_b32 s57,s78,15
s_add_u32 s57,s57,s79
s_add_u32 s57,s57,12582912
s_add_u32 s58,s57,4096
s_lshl_b32 s59,s78,9
s_add_u32 s59,s59,s79
s_add_u32 s59,s59,107020288
s_lshl_b32 s60,s45,9
s_add_u32 s60,s60,s66
s_add_u32 s60,s60,131072
s_add_u32 s45,s45,s46
s_lshr_b32 s78,s45,1
s_and_b32 s79,s45,1
s_lshl_b32 s79,s79,7
s_lshl_b32 s55,s78,15
s_add_u32 s55,s55,s79
s_mul_i32 s56,s45,16384
s_add_u32 s56,s56,98566144
s_lshl_b32 s61,s78,11
s_add_u32 s61,s61,110587904
s_add_u32 s78,s45,s46
s_lshr_b32 s78,s78,1
s_lshl_b32 s62,s78,11
s_add_u32 s62,s62,110587904
s_waitcnt vmcnt(6)
s_branch Lr_it1_%=
Lr_tb0_%=:
v_add_f32 v53,s69,v53
v_rsq_f32 v48,v53
s_nop 0
v_mul_f32_e64 v50,-v52,v48
v_mov_b32 v49,v48
v_mov_b32 v51,v50
v_pk_fma_f32 v[52:53],v[116:117],v[48:49],v[50:51]
v_pk_fma_f32 v[54:55],v[118:119],v[48:49],v[50:51]
v_pk_fma_f32 v[56:57],v[120:121],v[48:49],v[50:51]
v_pk_fma_f32 v[58:59],v[122:123],v[48:49],v[50:51]
s_add_u32 s78,s55,71303168
buffer_load_dwordx4 v[196:199],v1,s[36:39],s78 offen offset:0 sc1
buffer_load_dwordx4 v[200:203],v1,s[36:39],s78 offen offset:2048 sc1
s_add_u32 s78,s55,71307264
buffer_load_dwordx4 v[204:207],v1,s[36:39],s78 offen offset:0 sc1
buffer_load_dwordx4 v[208:211],v1,s[36:39],s78 offen offset:2048 sc1
v_pk_fma_f32 v[116:117],v[52:53],v[84:85],v[116:117]
v_pk_fma_f32 v[118:119],v[54:55],v[86:87],v[118:119]
v_pk_fma_f32 v[120:121],v[56:57],v[88:89],v[120:121]
v_pk_fma_f32 v[122:123],v[58:59],v[90:91],v[122:123]
v_cvt_pk_bf16_f32 v148,v116,v117
v_cvt_pk_bf16_f32 v149,v118,v119
v_cvt_pk_bf16_f32 v150,v120,v121
v_cvt_pk_bf16_f32 v151,v122,v123
v_pk_fma_f32 v[52:53],v[124:125],v[48:49],v[50:51]
v_pk_fma_f32 v[54:55],v[126:127],v[48:49],v[50:51]
v_pk_fma_f32 v[56:57],v[128:129],v[48:49],v[50:51]
v_pk_fma_f32 v[58:59],v[130:131],v[48:49],v[50:51]
s_add_u32 s78,s55,89128960
buffer_load_dwordx4 v[212:215],v1,s[36:39],s78 offen offset:0 sc1
buffer_load_dwordx4 v[216:219],v1,s[36:39],s78 offen offset:2048 sc1
s_add_u32 s78,s55,89133056
buffer_load_dwordx4 v[220:223],v1,s[36:39],s78 offen offset:0 sc1
buffer_load_dwordx4 v[224:227],v1,s[36:39],s78 offen offset:2048 sc1
v_pk_fma_f32 v[124:125],v[52:53],v[92:93],v[124:125]
v_pk_fma_f32 v[126:127],v[54:55],v[94:95],v[126:127]
v_pk_fma_f32 v[128:129],v[56:57],v[96:97],v[128:129]
v_pk_fma_f32 v[130:131],v[58:59],v[98:99],v[130:131]
v_cvt_pk_bf16_f32 v152,v124,v125
v_cvt_pk_bf16_f32 v153,v126,v127
v_cvt_pk_bf16_f32 v154,v128,v129
v_cvt_pk_bf16_f32 v155,v130,v131
v_pk_fma_f32 v[52:53],v[132:133],v[48:49],v[50:51]
v_pk_fma_f32 v[54:55],v[134:135],v[48:49],v[50:51]
v_pk_fma_f32 v[56:57],v[136:137],v[48:49],v[50:51]
v_pk_fma_f32 v[58:59],v[138:139],v[48:49],v[50:51]
s_add_u32 m0,s65,16384
s_nop 0
D 9,56,0
D 9,56,1024
D 9,56,2048
D 9,56,3072
v_pk_fma_f32 v[132:133],v[52:53],v[100:101],v[132:133]
v_pk_fma_f32 v[134:135],v[54:55],v[102:103],v[134:135]
v_pk_fma_f32 v[136:137],v[56:57],v[104:105],v[136:137]
v_pk_fma_f32 v[138:139],v[58:59],v[106:107],v[138:139]
v_cvt_pk_bf16_f32 v156,v132,v133
v_cvt_pk_bf16_f32 v157,v134,v135
v_cvt_pk_bf16_f32 v158,v136,v137
v_cvt_pk_bf16_f32 v159,v138,v139
v_pk_fma_f32 v[52:53],v[140:141],v[48:49],v[50:51]
v_pk_fma_f32 v[54:55],v[142:143],v[48:49],v[50:51]
v_pk_fma_f32 v[56:57],v[144:145],v[48:49],v[50:51]
v_pk_fma_f32 v[58:59],v[146:147],v[48:49],v[50:51]
v_pk_fma_f32 v[140:141],v[52:53],v[108:109],v[140:141]
v_pk_fma_f32 v[142:143],v[54:55],v[110:111],v[142:143]
v_pk_fma_f32 v[144:145],v[56:57],v[112:113],v[144:145]
v_pk_fma_f32 v[146:147],v[58:59],v[114:115],v[146:147]
v_cvt_pk_bf16_f32 v160,v140,v141
v_cvt_pk_bf16_f32 v161,v142,v143
v_cvt_pk_bf16_f32 v162,v144,v145
v_cvt_pk_bf16_f32 v163,v146,v147
v_mfma_f32_16x16x32_bf16 v[36:39],v[32:35],v[148:151],0
buffer_load_dword v14,v6,s[36:39],s62 offen offset:0 sc1
buffer_store_dwordx4 v[148:151],v1,s[36:39],s57 offen offset:0 sc0 sc1
v_mfma_f32_16x16x32_bf16 v[40:43],v[148:151],v[148:151],0
M 36,v[32:35],152
buffer_store_dwordx4 v[152:155],v1,s[36:39],s57 offen offset:2048 sc0 sc1
M 40,v[152:155],152
M 36,v[32:35],156
buffer_store_dwordx4 v[156:159],v1,s[36:39],s58 offen offset:0 sc0 sc1
M 40,v[156:159],156
M 36,v[32:35],160
buffer_store_dwordx4 v[160:163],v1,s[36:39],s58 offen offset:2048 sc0 sc1
M 40,v[160:163],160
s_nop 5
v_add_f32_dpp v44,v36,v36 row_ror:8 row_mask:0xf bank_mask:0xf
s_nop 0
v_cndmask_b32 v45,v40,v41,s[70:71]
v_cndmask_b32 v46,v42,v43,s[70:71]
ds_write_b32 v2,v44 offset:1024
v_cndmask_b32 v45,v45,v46,s[72:73]
ds_write_b32 v0,v45 offset:1024
L 0
s_barrier
R 52,3,1024
R 56,3,1040
R 60,3,1056
L 0
v_pk_add_f32 v[52:53],v[52:53],v[54:55]
v_pk_add_f32 v[56:57],v[56:57],v[58:59]
v_pk_add_f32 v[60:61],v[60:61],v[62:63]
v_add_f32 v52,v52,v53
v_pk_add_f32 v[56:57],v[56:57],v[60:61]
v_mul_f32 v52,s68,v52
v_add_f32 v53,v56,v57
v_mul_f32 v53,s68,v53
v_fma_f32 v53,-v52,v52,v53
v_max_f32 v53,0,v53
v_add_f32 v54,s69,v53
v_fma_f32 v55,s69,v54,v53
v_rsq_f32 v65,v54
v_rsq_f32 v54,v55
v_mul_f32_e64 v64,-v52,v65
v_mul_f32 v63,v55,v54
v_mul_f32 v61,s76,v54
v_mul_f32 v55,v54,v54
v_mul_f32 v62,s77,v55
v_mul_f32 v62,v62,v61
v_cndmask_b32 v60,v52,v64,s[74:75]
v_cndmask_b32 v61,v61,v65,s[74:75]
buffer_store_dwordx4 v[60:63],v4,s[36:39],s59 offen offset:0 sc0 sc1
s_mov_b32 s63,s60
s_lshr_b32 s78,s45,1
s_and_b32 s79,s45,1
s_lshl_b32 s79,s79,7
s_lshl_b32 s57,s78,15
s_add_u32 s57,s57,s79
s_add_u32 s57,s57,12582912
s_add_u32 s58,s57,4096
s_lshl_b32 s59,s78,9
s_add_u32 s59,s59,s79
s_add_u32 s59,s59,107020288
s_lshl_b32 s60,s45,9
s_add_u32 s60,s60,s66
s_add_u32 s60,s60,131072
s_add_u32 s45,s45,s46
s_lshr_b32 s78,s45,1
s_and_b32 s79,s45,1
s_lshl_b32 s79,s79,7
s_lshl_b32 s55,s78,15
s_add_u32 s55,s55,s79
s_mul_i32 s56,s45,16384
s_add_u32 s56,s56,98566144
s_lshl_b32 s61,s78,11
s_add_u32 s61,s61,110587904
s_add_u32 s78,s45,s46
s_lshr_b32 s78,s78,1
s_lshl_b32 s62,s78,11
s_add_u32 s62,s62,110587904
s_waitcnt vmcnt(5)
s_branch Lr_it1_%=
Lr_tc0_%=:
s_add_u32 s78,s55,71303168
buffer_load_dwordx4 v[196:199],v1,s[36:39],s78 offen offset:0 sc1
buffer_load_dwordx4 v[200:203],v1,s[36:39],s78 offen offset:2048 sc1
s_add_u32 s78,s55,71307264
buffer_load_dwordx4 v[204:207],v1,s[36:39],s78 offen offset:0 sc1
buffer_load_dwordx4 v[208:211],v1,s[36:39],s78 offen offset:2048 sc1
s_add_u32 s78,s55,89128960
buffer_load_dwordx4 v[212:215],v1,s[36:39],s78 offen offset:0 sc1
buffer_load_dwordx4 v[216:219],v1,s[36:39],s78 offen offset:2048 sc1
s_add_u32 s78,s55,89133056
buffer_load_dwordx4 v[220:223],v1,s[36:39],s78 offen offset:0 sc1
buffer_load_dwordx4 v[224:227],v1,s[36:39],s78 offen offset:2048 sc1
s_add_u32 m0,s65,16384
s_nop 0
D 9,56,0
D 9,56,1024
D 9,56,2048
D 9,56,3072
buffer_load_dword v14,v6,s[36:39],s62 offen offset:0 sc1
s_waitcnt vmcnt(13)
buffer_store_dword v13,v5,s[36:39],s63 offen offset:0 sc0 sc1
s_mov_b32 s63,0x7fff0000
s_lshr_b32 s78,s45,1
s_and_b32 s79,s45,1
s_lshl_b32 s79,s79,7
s_lshl_b32 s57,s78,15
s_add_u32 s57,s57,s79
s_add_u32 s57,s57,12582912
s_add_u32 s58,s57,4096
s_lshl_b32 s59,s78,9
s_add_u32 s59,s59,s79
s_add_u32 s59,s59,107020288
s_lshl_b32 s60,s45,9
s_add_u32 s60,s60,s66
s_add_u32 s60,s60,131072
s_add_u32 s45,s45,s46
s_lshr_b32 s78,s45,1
s_and_b32 s79,s45,1
s_lshl_b32 s79,s79,7
s_lshl_b32 s55,s78,15
s_add_u32 s55,s55,s79
s_mul_i32 s56,s45,16384
s_add_u32 s56,s56,98566144
s_lshl_b32 s61,s78,11
s_add_u32 s61,s61,110587904
s_add_u32 s78,s45,s46
s_lshr_b32 s78,s78,1
s_lshl_b32 s62,s78,11
s_add_u32 s62,s62,110587904
s_waitcnt vmcnt(1)
s_branch Lr_it1_%=
Lr_it1_%=:
T v[68:69],10,16384
T v[70:71],10,16448
T v[72:73],10,17408
T v[74:75],10,17472
T v[76:77],10,18432
T v[78:79],10,18496
T v[80:81],10,19456
T v[82:83],10,19520
s_nop 0
v_cmp_eq_u32 vcc,s53,v14
s_nop 0
s_cmp_eq_u64 vcc,-1
s_cselect_b32 s78,s45,256
s_cmp_lt_u32 s78,256
s_cbranch_scc1 Lr_ta1_%=
buffer_load_dword v14,v6,s[36:39],s61 offen offset:0 sc1
v_mfma_f32_16x16x32_bf16 v[116:119],v[26:29],v[196:199],0
L 6
v_lshlrev_b32 v84,16,v68
v_and_b32 v85,0xffff0000,v68
v_mfma_f32_16x16x32_bf16 v[120:123],v[24:27],v[196:199],0
L 4
v_lshlrev_b32 v86,16,v69
v_and_b32 v87,0xffff0000,v69
M 116,v[26:29],212
L 2
v_lshlrev_b32 v88,16,v70
v_and_b32 v89,0xffff0000,v70
M 120,v[24:27],212
L 0
v_lshlrev_b32 v90,16,v71
v_and_b32 v91,0xffff0000,v71
v_mfma_f32_16x16x32_bf16 v[124:127],v[26:29],v[200:203],0
)"
R"(v_lshlrev_b32 v92,16,v72
v_and_b32 v93,0xffff0000,v72
v_mfma_f32_16x16x32_bf16 v[128:131],v[24:27],v[200:203],0
v_lshlrev_b32 v94,16,v73
v_and_b32 v95,0xffff0000,v73
M 124,v[26:29],216
v_cvt_pk_bf16_f32 v148,v116,v117
v_cvt_pk_bf16_f32 v149,v118,v119
M 128,v[24:27],216
v_cvt_pk_bf16_f32 v150,v120,v121
v_cvt_pk_bf16_f32 v151,v122,v123
v_mfma_f32_16x16x32_bf16 v[132:135],v[26:29],v[204:207],0
v_lshlrev_b32 v96,16,v74
v_and_b32 v97,0xffff0000,v74
v_mfma_f32_16x16x32_bf16 v[136:139],v[24:27],v[204:207],0
v_lshlrev_b32 v98,16,v75
v_and_b32 v99,0xffff0000,v75
M 132,v[26:29],220
v_cvt_pk_bf16_f32 v152,v124,v125
v_cvt_pk_bf16_f32 v153,v126,v127
M 136,v[24:27],220
v_cvt_pk_bf16_f32 v154,v128,v129
v_cvt_pk_bf16_f32 v155,v130,v131
v_mfma_f32_16x16x32_bf16 v[140:143],v[26:29],v[208:211],0
v_lshlrev_b32 v100,16,v76
v_and_b32 v101,0xffff0000,v76
v_mfma_f32_16x16x32_bf16 v[144:147],v[24:27],v[208:211],0
v_lshlrev_b32 v102,16,v77
v_and_b32 v103,0xffff0000,v77
M 140,v[26:29],224
v_cvt_pk_bf16_f32 v156,v132,v133
v_cvt_pk_bf16_f32 v157,v134,v135
M 144,v[24:27],224
v_cvt_pk_bf16_f32 v158,v136,v137
v_cvt_pk_bf16_f32 v159,v138,v139
v_mfma_f32_16x16x32_bf16 v[36:39],v[32:35],v[148:151],0
v_lshlrev_b32 v104,16,v78
v_and_b32 v105,0xffff0000,v78
v_mfma_f32_16x16x32_bf16 v[40:43],v[148:151],v[148:151],0
v_lshlrev_b32 v106,16,v79
v_and_b32 v107,0xffff0000,v79
M 36,v[32:35],152
v_cvt_pk_bf16_f32 v160,v140,v141
v_cvt_pk_bf16_f32 v161,v142,v143
M 40,v[152:155],152
v_cvt_pk_bf16_f32 v162,v144,v145
v_cvt_pk_bf16_f32 v163,v146,v147
M 36,v[32:35],156
v_lshlrev_b32 v108,16,v80
v_and_b32 v109,0xffff0000,v80
M 40,v[156:159],156
v_lshlrev_b32 v110,16,v81
v_and_b32 v111,0xffff0000,v81
M 36,v[32:35],160
v_lshlrev_b32 v112,16,v82
v_and_b32 v113,0xffff0000,v82
M 40,v[160:163],160
v_lshlrev_b32 v114,16,v83
v_and_b32 v115,0xffff0000,v83
s_nop 2
v_add_f32_dpp v44,v36,v36 row_ror:8 row_mask:0xf bank_mask:0xf
s_nop 1
v_cndmask_b32 v45,v40,v41,s[70:71]
v_cndmask_b32 v46,v42,v43,s[70:71]
ds_write_b32 v2,v44 offset:0
v_cndmask_b32 v45,v45,v46,s[72:73]
ds_write_b32 v0,v45 offset:0
L 0
s_barrier
R 52,3,0
R 56,3,16
R 60,3,32
L 0
v_pk_add_f32 v[52:53],v[52:53],v[54:55]
v_pk_add_f32 v[56:57],v[56:57],v[58:59]
v_pk_add_f32 v[60:61],v[60:61],v[62:63]
v_add_f32 v52,v52,v53
v_pk_add_f32 v[56:57],v[56:57],v[60:61]
v_mul_f32 v52,s68,v52
v_add_f32 v53,v56,v57
v_mul_f32 v53,s68,v53
v_fma_f32 v53,-v52,v52,v53
v_max_f32 v53,0,v53
s_waitcnt vmcnt(1)
buffer_store_dword v13,v5,s[36:39],s63 offen offset:0 sc0 sc1
s_mov_b32 s63,0x7fff0000
s_waitcnt vmcnt(1)
v_cmp_eq_u32 vcc,s53,v14
s_nop 0
s_cmp_eq_u64 vcc,-1
s_cselect_b32 s78,s45,256
s_cmp_lt_u32 s78,256
s_cbranch_scc1 Lr_tb1_%=
buffer_load_dword v14,v6,s[36:39],s61 offen offset:0 sc1
v_add_f32 v53,s69,v53
v_rsq_f32 v48,v53
s_nop 0
v_mul_f32_e64 v50,-v52,v48
v_mov_b32 v49,v48
v_mov_b32 v51,v50
v_pk_fma_f32 v[52:53],v[116:117],v[48:49],v[50:51]
v_pk_fma_f32 v[54:55],v[118:119],v[48:49],v[50:51]
v_pk_fma_f32 v[56:57],v[120:121],v[48:49],v[50:51]
v_pk_fma_f32 v[58:59],v[122:123],v[48:49],v[50:51]
v_pk_fma_f32 v[116:117],v[52:53],v[84:85],v[116:117]
v_pk_fma_f32 v[118:119],v[54:55],v[86:87],v[118:119]
v_pk_fma_f32 v[120:121],v[56:57],v[88:89],v[120:121]
v_pk_fma_f32 v[122:123],v[58:59],v[90:91],v[122:123]
v_cvt_pk_bf16_f32 v148,v116,v117
v_cvt_pk_bf16_f32 v149,v118,v119
v_cvt_pk_bf16_f32 v150,v120,v121
v_cvt_pk_bf16_f32 v151,v122,v123
v_pk_fma_f32 v[52:53],v[124:125],v[48:49],v[50:51]
v_pk_fma_f32 v[54:55],v[126:127],v[48:49],v[50:51]
v_pk_fma_f32 v[56:57],v[128:129],v[48:49],v[50:51]
v_pk_fma_f32 v[58:59],v[130:131],v[48:49],v[50:51]
v_pk_fma_f32 v[124:125],v[52:53],v[92:93],v[124:125]
v_pk_fma_f32 v[126:127],v[54:55],v[94:95],v[126:127]
v_pk_fma_f32 v[128:129],v[56:57],v[96:97],v[128:129]
v_pk_fma_f32 v[130:131],v[58:59],v[98:99],v[130:131]
v_cvt_pk_bf16_f32 v152,v124,v125
v_cvt_pk_bf16_f32 v153,v126,v127
v_cvt_pk_bf16_f32 v154,v128,v129
v_cvt_pk_bf16_f32 v155,v130,v131
v_pk_fma_f32 v[52:53],v[132:133],v[48:49],v[50:51]
v_pk_fma_f32 v[54:55],v[134:135],v[48:49],v[50:51]
v_pk_fma_f32 v[56:57],v[136:137],v[48:49],v[50:51]
v_pk_fma_f32 v[58:59],v[138:139],v[48:49],v[50:51]
v_pk_fma_f32 v[132:133],v[52:53],v[100:101],v[132:133]
v_pk_fma_f32 v[134:135],v[54:55],v[102:103],v[134:135]
v_pk_fma_f32 v[136:137],v[56:57],v[104:105],v[136:137]
v_pk_fma_f32 v[138:139],v[58:59],v[106:107],v[138:139]
v_cvt_pk_bf16_f32 v156,v132,v133
v_cvt_pk_bf16_f32 v157,v134,v135
v_cvt_pk_bf16_f32 v158,v136,v137
v_cvt_pk_bf16_f32 v159,v138,v139
v_pk_fma_f32 v[52:53],v[140:141],v[48:49],v[50:51]
v_pk_fma_f32 v[54:55],v[142:143],v[48:49],v[50:51]
v_pk_fma_f32 v[56:57],v[144:145],v[48:49],v[50:51]
v_pk_fma_f32 v[58:59],v[146:147],v[48:49],v[50:51]
v_pk_fma_f32 v[140:141],v[52:53],v[108:109],v[140:141]
v_pk_fma_f32 v[142:143],v[54:55],v[110:111],v[142:143]
v_pk_fma_f32 v[144:145],v[56:57],v[112:113],v[144:145]
v_pk_fma_f32 v[146:147],v[58:59],v[114:115],v[146:147]
v_cvt_pk_bf16_f32 v160,v140,v141
v_cvt_pk_bf16_f32 v161,v142,v143
v_cvt_pk_bf16_f32 v162,v144,v145
v_cvt_pk_bf16_f32 v163,v146,v147
v_mfma_f32_16x16x32_bf16 v[36:39],v[32:35],v[148:151],0
buffer_store_dwordx4 v[148:151],v1,s[36:39],s57 offen offset:0 sc0 sc1
v_mfma_f32_16x16x32_bf16 v[40:43],v[148:151],v[148:151],0
M 36,v[32:35],152
buffer_store_dwordx4 v[152:155],v1,s[36:39],s57 offen offset:2048 sc0 sc1
M 40,v[152:155],152
M 36,v[32:35],156
buffer_store_dwordx4 v[156:159],v1,s[36:39],s58 offen offset:0 sc0 sc1
M 40,v[156:159],156
M 36,v[32:35],160
buffer_store_dwordx4 v[160:163],v1,s[36:39],s58 offen offset:2048 sc0 sc1
M 40,v[160:163],160
s_nop 5
v_add_f32_dpp v44,v36,v36 row_ror:8 row_mask:0xf bank_mask:0xf
s_nop 0
v_cndmask_b32 v45,v40,v41,s[70:71]
v_cndmask_b32 v46,v42,v43,s[70:71]
ds_write_b32 v2,v44 offset:1024
v_cndmask_b32 v45,v45,v46,s[72:73]
ds_write_b32 v0,v45 offset:1024
L 0
s_barrier
R 52,3,1024
R 56,3,1040
R 60,3,1056
L 0
v_pk_add_f32 v[52:53],v[52:53],v[54:55]
v_pk_add_f32 v[56:57],v[56:57],v[58:59]
v_pk_add_f32 v[60:61],v[60:61],v[62:63]
v_add_f32 v52,v52,v53
v_pk_add_f32 v[56:57],v[56:57],v[60:61]
v_mul_f32 v52,s68,v52
v_add_f32 v53,v56,v57
v_mul_f32 v53,s68,v53
v_fma_f32 v53,-v52,v52,v53
v_max_f32 v53,0,v53
v_add_f32 v54,s69,v53
v_fma_f32 v55,s69,v54,v53
v_rsq_f32 v65,v54
v_rsq_f32 v54,v55
v_mul_f32_e64 v64,-v52,v65
v_mul_f32 v63,v55,v54
v_mul_f32 v61,s76,v54
v_mul_f32 v55,v54,v54
v_mul_f32 v62,s77,v55
v_mul_f32 v62,v62,v61
v_cndmask_b32 v60,v52,v64,s[74:75]
v_cndmask_b32 v61,v61,v65,s[74:75]
buffer_store_dwordx4 v[60:63],v4,s[36:39],s59 offen offset:0 sc0 sc1
s_mov_b32 s63,s60
s_waitcnt vmcnt(5)
v_cmp_eq_u32 vcc,s53,v14
s_nop 0
s_cmp_eq_u64 vcc,-1
s_cselect_b32 s78,s45,256
s_cmp_lt_u32 s78,256
s_cbranch_scc1 Lr_tc1_%=
s_waitcnt vmcnt(0)
buffer_store_dword v13,v5,s[36:39],s63 offen offset:0 sc0 sc1
s_mov_b32 s63,0x7fff0000
s_cmp_ge_u32 s45,256
s_cbranch_scc1 Lr_done_%=
Lr_bp1_%=:
buffer_load_dword v14,v6,s[36:39],s61 offen offset:0 sc1
s_add_u32 s82,s82,1
s_waitcnt vmcnt(0)
v_cmp_eq_u32 vcc,s53,v14
s_nop 0
s_cmp_eq_u64 vcc,-1
s_cbranch_scc1 Lr_bq1_%=
s_sleep 2
s_cmp_lt_u32 s82,20000
s_cbranch_scc1 Lr_bp1_%=
Lr_bq1_%=:
s_add_u32 s78,s55,71303168
buffer_load_dwordx4 v[164:167],v1,s[36:39],s78 offen offset:0 sc1
buffer_load_dwordx4 v[168:171],v1,s[36:39],s78 offen offset:2048 sc1
s_add_u32 s78,s55,71307264
buffer_load_dwordx4 v[172:175],v1,s[36:39],s78 offen offset:0 sc1
buffer_load_dwordx4 v[176:179],v1,s[36:39],s78 offen offset:2048 sc1
s_add_u32 s78,s55,89128960
buffer_load_dwordx4 v[180:183],v1,s[36:39],s78 offen offset:0 sc1
buffer_load_dwordx4 v[184:187],v1,s[36:39],s78 offen offset:2048 sc1
s_add_u32 s78,s55,89133056
buffer_load_dwordx4 v[188:191],v1,s[36:39],s78 offen offset:0 sc1
buffer_load_dwordx4 v[192:195],v1,s[36:39],s78 offen offset:2048 sc1
s_add_u32 m0,s65,0
s_nop 0
D 9,56,0
D 9,56,1024
D 9,56,2048
D 9,56,3072
buffer_load_dword v14,v6,s[36:39],s62 offen offset:0 sc1
s_lshr_b32 s78,s45,1
s_and_b32 s79,s45,1
s_lshl_b32 s79,s79,7
s_lshl_b32 s57,s78,15
s_add_u32 s57,s57,s79
s_add_u32 s57,s57,12582912
s_add_u32 s58,s57,4096
s_lshl_b32 s59,s78,9
s_add_u32 s59,s59,s79
s_add_u32 s59,s59,107020288
s_lshl_b32 s60,s45,9
s_add_u32 s60,s60,s66
s_add_u32 s60,s60,131072
s_add_u32 s45,s45,s46
s_lshr_b32 s78,s45,1
s_and_b32 s79,s45,1
s_lshl_b32 s79,s79,7
s_lshl_b32 s55,s78,15
s_add_u32 s55,s55,s79
s_mul_i32 s56,s45,16384
s_add_u32 s56,s56,98566144
s_lshl_b32 s61,s78,11
s_add_u32 s61,s61,110587904
s_add_u32 s78,s45,s46
s_lshr_b32 s78,s78,1
s_lshl_b32 s62,s78,11
s_add_u32 s62,s62,110587904
s_waitcnt vmcnt(0)
s_branch Lr_it0_%=
Lr_ta1_%=:
v_mfma_f32_16x16x32_bf16 v[116:119],v[26:29],v[196:199],0
s_add_u32 s78,s55,71303168
L 6
L 4
v_mfma_f32_16x16x32_bf16 v[120:123],v[24:27],v[196:199],0
L 2
buffer_load_dwordx4 v[164:167],v1,s[36:39],s78 offen offset:0 sc1
M 116,v[26:29],212
L 0
buffer_load_dwordx4 v[168:171],v1,s[36:39],s78 offen offset:2048 sc1
M 120,v[24:27],212
s_add_u32 s78,s55,71307264
v_lshlrev_b32 v84,16,v68
v_and_b32 v85,0xffff0000,v68
v_mfma_f32_16x16x32_bf16 v[124:127],v[26:29],v[200:203],0
buffer_load_dwordx4 v[172:175],v1,s[36:39],s78 offen offset:0 sc1
v_mfma_f32_16x16x32_bf16 v[128:131],v[24:27],v[200:203],0
buffer_load_dwordx4 v[176:179],v1,s[36:39],s78 offen offset:2048 sc1
M 124,v[26:29],216
s_add_u32 s78,s55,89128960
v_cvt_pk_bf16_f32 v148,v116,v117
v_cvt_pk_bf16_f32 v149,v118,v119
M 128,v[24:27],216
buffer_load_dwordx4 v[180:183],v1,s[36:39],s78 offen offset:0 sc1
v_mfma_f32_16x16x32_bf16 v[132:135],v[26:29],v[204:207],0
buffer_load_dwordx4 v[184:187],v1,s[36:39],s78 offen offset:2048 sc1
v_mfma_f32_16x16x32_bf16 v[136:139],v[24:27],v[204:207],0
s_add_u32 s78,s55,89133056
v_cvt_pk_bf16_f32 v150,v120,v121
v_cvt_pk_bf16_f32 v151,v122,v123
M 132,v[26:29],220
buffer_load_dwordx4 v[188:191],v1,s[36:39],s78 offen offset:0 sc1
M 136,v[24:27],220
buffer_load_dwordx4 v[192:195],v1,s[36:39],s78 offen offset:2048 sc1
v_mfma_f32_16x16x32_bf16 v[140:143],v[26:29],v[208:211],0
s_add_u32 m0,s65,0
v_cvt_pk_bf16_f32 v152,v124,v125
v_cvt_pk_bf16_f32 v153,v126,v127
v_mfma_f32_16x16x32_bf16 v[144:147],v[24:27],v[208:211],0
D 9,56,0
M 140,v[26:29],224
D 9,56,1024
M 144,v[24:27],224
D 9,56,2048
v_mfma_f32_16x16x32_bf16 v[36:39],v[32:35],v[148:151],0
v_cvt_pk_bf16_f32 v154,v128,v129
v_cvt_pk_bf16_f32 v155,v130,v131
v_mfma_f32_16x16x32_bf16 v[40:43],v[148:151],v[148:151],0
D 9,56,3072
M 36,v[32:35],152
v_cvt_pk_bf16_f32 v156,v132,v133
v_cvt_pk_bf16_f32 v157,v134,v135
v_cvt_pk_bf16_f32 v158,v136,v137
v_cvt_pk_bf16_f32 v159,v138,v139
M 40,v[152:155],152
v_cvt_pk_bf16_f32 v160,v140,v141
v_cvt_pk_bf16_f32 v161,v142,v143
M 36,v[32:35],156
v_cvt_pk_bf16_f32 v162,v144,v145
v_cvt_pk_bf16_f32 v163,v146,v147
M 40,v[156:159],156
v_lshlrev_b32 v86,16,v69
v_and_b32 v87,0xffff0000,v69
M 36,v[32:35],160
v_lshlrev_b32 v88,16,v70
v_and_b32 v89,0xffff0000,v70
M 40,v[160:163],160
v_lshlrev_b32 v90,16,v71
v_and_b32 v91,0xffff0000,v71
v_lshlrev_b32 v92,16,v72
v_and_b32 v93,0xffff0000,v72
v_lshlrev_b32 v94,16,v73
v_and_b32 v95,0xffff0000,v73
v_lshlrev_b32 v96,16,v74
v_and_b32 v97,0xffff0000,v74
v_lshlrev_b32 v98,16,v75
v_and_b32 v99,0xffff0000,v75
v_lshlrev_b32 v100,16,v76
v_and_b32 v101,0xffff0000,v76
v_lshlrev_b32 v102,16,v77
v_and_b32 v103,0xffff0000,v77
v_lshlrev_b32 v104,16,v78
v_and_b32 v105,0xffff0000,v78
v_lshlrev_b32 v106,16,v79
v_and_b32 v107,0xffff0000,v79
v_lshlrev_b32 v108,16,v80
v_and_b32 v109,0xffff0000,v80
v_lshlrev_b32 v110,16,v81
v_and_b32 v111,0xffff0000,v81
v_lshlrev_b32 v112,16,v82
v_and_b32 v113,0xffff0000,v82
v_lshlrev_b32 v114,16,v83
v_and_b32 v115,0xffff0000,v83
v_add_f32_dpp v44,v36,v36 row_ror:8 row_mask:0xf bank_mask:0xf
v_cndmask_b32 v45,v40,v41,s[70:71]
v_cndmask_b32 v46,v42,v43,s[70:71]
ds_write_b32 v2,v44 offset:0
v_cndmask_b32 v45,v45,v46,s[72:73]
ds_write_b32 v0,v45 offset:0
L 0
s_barrier
R 52,3,0
R 56,3,16
R 60,3,32
L 0
v_pk_add_f32 v[52:53],v[52:53],v[54:55]
v_pk_add_f32 v[56:57],v[56:57],v[58:59]
v_pk_add_f32 v[60:61],v[60:61],v[62:63]
v_add_f32 v52,v52,v53
v_pk_add_f32 v[56:57],v[56:57],v[60:61]
v_mul_f32 v52,s68,v52
v_add_f32 v53,v56,v57
v_mul_f32 v53,s68,v53
v_fma_f32 v53,-v52,v52,v53
v_max_f32 v53,0,v53
buffer_load_dword v14,v6,s[36:39],s62 offen offset:0 sc1
v_add_f32 v53,s69,v53
v_rsq_f32 v48,v53
s_nop 0
v_mul_f32_e64 v50,-v52,v48
v_mov_b32 v49,v48
v_mov_b32 v51,v50
v_pk_fma_f32 v[52:53],v[116:117],v[48:49],v[50:51]
v_pk_fma_f32 v[54:55],v[118:119],v[48:49],v[50:51]
v_pk_fma_f32 v[56:57],v[120:121],v[48:49],v[50:51]
v_pk_fma_f32 v[58:59],v[122:123],v[48:49],v[50:51]
v_pk_fma_f32 v[116:117],v[52:53],v[84:85],v[116:117]
v_pk_fma_f32 v[118:119],v[54:55],v[86:87],v[118:119]
v_pk_fma_f32 v[120:121],v[56:57],v[88:89],v[120:121]
v_pk_fma_f32 v[122:123],v[58:59],v[90:91],v[122:123]
v_cvt_pk_bf16_f32 v148,v116,v117
v_cvt_pk_bf16_f32 v149,v118,v119
v_cvt_pk_bf16_f32 v150,v120,v121
v_cvt_pk_bf16_f32 v151,v122,v123
v_pk_fma_f32 v[52:53],v[124:125],v[48:49],v[50:51]
v_pk_fma_f32 v[54:55],v[126:127],v[48:49],v[50:51]
v_pk_fma_f32 v[56:57],v[128:129],v[48:49],v[50:51]
v_pk_fma_f32 v[58:59],v[130:131],v[48:49],v[50:51]
v_pk_fma_f32 v[124:125],v[52:53],v[92:93],v[124:125]
v_pk_fma_f32 v[126:127],v[54:55],v[94:95],v[126:127]
v_pk_fma_f32 v[128:129],v[56:57],v[96:97],v[128:129]
v_pk_fma_f32 v[130:131],v[58:59],v[98:99],v[130:131]
v_cvt_pk_bf16_f32 v152,v124,v125
v_cvt_pk_bf16_f32 v153,v126,v127
v_cvt_pk_bf16_f32 v154,v128,v129
v_cvt_pk_bf16_f32 v155,v130,v131
v_pk_fma_f32 v[52:53],v[132:133],v[48:49],v[50:51]
v_pk_fma_f32 v[54:55],v[134:135],v[48:49],v[50:51]
v_pk_fma_f32 v[56:57],v[136:137],v[48:49],v[50:51]
v_pk_fma_f32 v[58:59],v[138:139],v[48:49],v[50:51]
v_pk_fma_f32 v[132:133],v[52:53],v[100:101],v[132:133]
v_pk_fma_f32 v[134:135],v[54:55],v[102:103],v[134:135]
v_pk_fma_f32 v[136:137],v[56:57],v[104:105],v[136:137]
v_pk_fma_f32 v[138:139],v[58:59],v[106:107],v[138:139]
v_cvt_pk_bf16_f32 v156,v132,v133
v_cvt_pk_bf16_f32 v157,v134,v135
v_cvt_pk_bf16_f32 v158,v136,v137
v_cvt_pk_bf16_f32 v159,v138,v139
v_pk_fma_f32 v[52:53],v[140:141],v[48:49],v[50:51]
v_pk_fma_f32 v[54:55],v[142:143],v[48:49],v[50:51]
v_pk_fma_f32 v[56:57],v[144:145],v[48:49],v[50:51]
v_pk_fma_f32 v[58:59],v[146:147],v[48:49],v[50:51]
v_pk_fma_f32 v[140:141],v[52:53],v[108:109],v[140:141]
v_pk_fma_f32 v[142:143],v[54:55],v[110:111],v[142:143]
v_pk_fma_f32 v[144:145],v[56:57],v[112:113],v[144:145]
v_pk_fma_f32 v[146:147],v[58:59],v[114:115],v[146:147]
v_cvt_pk_bf16_f32 v160,v140,v141
v_cvt_pk_bf16_f32 v161,v142,v143
v_cvt_pk_bf16_f32 v162,v144,v145
v_cvt_pk_bf16_f32 v163,v146,v147
s_waitcnt vmcnt(13)
buffer_store_dword v13,v5,s[36:39],s63 offen offset:0 sc0 sc1
s_mov_b32 s63,0x7fff0000
v_mfma_f32_16x16x32_bf16 v[36:39],v[32:35],v[148:151],0
buffer_store_dwordx4 v[148:151],v1,s[36:39],s57 offen offset:0 sc0 sc1
v_mfma_f32_16x16x32_bf16 v[40:43],v[148:151],v[148:151],0
M 36,v[32:35],152
buffer_store_dwordx4 v[152:155],v1,s[36:39],s57 offen offset:2048 sc0 sc1
M 40,v[152:155],152
M 36,v[32:35],156
buffer_store_dwordx4 v[156:159],v1,s[36:39],s58 offen offset:0 sc0 sc1
M 40,v[156:159],156
M 36,v[32:35],160
buffer_store_dwordx4 v[160:163],v1,s[36:39],s58 offen offset:2048 sc0 sc1
M 40,v[160:163],160
s_nop 5
v_add_f32_dpp v44,v36,v36 row_ror:8 row_mask:0xf bank_mask:0xf
s_nop 0
v_cndmask_b32 v45,v40,v41,s[70:71]
v_cndmask_b32 v46,v42,v43,s[70:71]
ds_write_b32 v2,v44 offset:1024
v_cndmask_b32 v45,v45,v46,s[72:73]
ds_write_b32 v0,v45 offset:1024
L 0
s_barrier
R 52,3,1024
R 56,3,1040
R 60,3,1056
L 0
v_pk_add_f32 v[52:53],v[52:53],v[54:55]
v_pk_add_f32 v[56:57],v[56:57],v[58:59]
v_pk_add_f32 v[60:61],v[60:61],v[62:63]
v_add_f32 v52,v52,v53
v_pk_add_f32 v[56:57],v[56:57],v[60:61]
v_mul_f32 v52,s68,v52
v_add_f32 v53,v56,v57
v_mul_f32 v53,s68,v53
v_fma_f32 v53,-v52,v52,v53
v_max_f32 v53,0,v53
v_add_f32 v54,s69,v53
v_fma_f32 v55,s69,v54,v53
v_rsq_f32 v65,v54
v_rsq_f32 v54,v55
v_mul_f32_e64 v64,-v52,v65
v_mul_f32 v63,v55,v54
v_mul_f32 v61,s76,v54
v_mul_f32 v55,v54,v54
v_mul_f32 v62,s77,v55
v_mul_f32 v62,v62,v61
v_cndmask_b32 v60,v52,v64,s[74:75]
v_cndmask_b32 v61,v61,v65,s[74:75]
buffer_store_dwordx4 v[60:63],v4,s[36:39],s59 offen offset:0 sc0 sc1
s_mov_b32 s63,s60
s_lshr_b32 s78,s45,1
s_and_b32 s79,s45,1
s_lshl_b32 s79,s79,7
s_lshl_b32 s57,s78,15
s_add_u32 s57,s57,s79
s_add_u32 s57,s57,12582912
)"
R"(s_add_u32 s58,s57,4096
s_lshl_b32 s59,s78,9
s_add_u32 s59,s59,s79
s_add_u32 s59,s59,107020288
s_lshl_b32 s60,s45,9
s_add_u32 s60,s60,s66
s_add_u32 s60,s60,131072
s_add_u32 s45,s45,s46
s_lshr_b32 s78,s45,1
s_and_b32 s79,s45,1
s_lshl_b32 s79,s79,7
s_lshl_b32 s55,s78,15
s_add_u32 s55,s55,s79
s_mul_i32 s56,s45,16384
s_add_u32 s56,s56,98566144
s_lshl_b32 s61,s78,11
s_add_u32 s61,s61,110587904
s_add_u32 s78,s45,s46
s_lshr_b32 s78,s78,1
s_lshl_b32 s62,s78,11
s_add_u32 s62,s62,110587904
s_waitcnt vmcnt(6)
s_branch Lr_it0_%=
Lr_tb1_%=:
v_add_f32 v53,s69,v53
v_rsq_f32 v48,v53
s_nop 0
v_mul_f32_e64 v50,-v52,v48
v_mov_b32 v49,v48
v_mov_b32 v51,v50
v_pk_fma_f32 v[52:53],v[116:117],v[48:49],v[50:51]
v_pk_fma_f32 v[54:55],v[118:119],v[48:49],v[50:51]
v_pk_fma_f32 v[56:57],v[120:121],v[48:49],v[50:51]
v_pk_fma_f32 v[58:59],v[122:123],v[48:49],v[50:51]
s_add_u32 s78,s55,71303168
buffer_load_dwordx4 v[164:167],v1,s[36:39],s78 offen offset:0 sc1
buffer_load_dwordx4 v[168:171],v1,s[36:39],s78 offen offset:2048 sc1
s_add_u32 s78,s55,71307264
buffer_load_dwordx4 v[172:175],v1,s[36:39],s78 offen offset:0 sc1
buffer_load_dwordx4 v[176:179],v1,s[36:39],s78 offen offset:2048 sc1
v_pk_fma_f32 v[116:117],v[52:53],v[84:85],v[116:117]
v_pk_fma_f32 v[118:119],v[54:55],v[86:87],v[118:119]
v_pk_fma_f32 v[120:121],v[56:57],v[88:89],v[120:121]
v_pk_fma_f32 v[122:123],v[58:59],v[90:91],v[122:123]
v_cvt_pk_bf16_f32 v148,v116,v117
v_cvt_pk_bf16_f32 v149,v118,v119
v_cvt_pk_bf16_f32 v150,v120,v121
v_cvt_pk_bf16_f32 v151,v122,v123
v_pk_fma_f32 v[52:53],v[124:125],v[48:49],v[50:51]
v_pk_fma_f32 v[54:55],v[126:127],v[48:49],v[50:51]
v_pk_fma_f32 v[56:57],v[128:129],v[48:49],v[50:51]
v_pk_fma_f32 v[58:59],v[130:131],v[48:49],v[50:51]
s_add_u32 s78,s55,89128960
buffer_load_dwordx4 v[180:183],v1,s[36:39],s78 offen offset:0 sc1
buffer_load_dwordx4 v[184:187],v1,s[36:39],s78 offen offset:2048 sc1
s_add_u32 s78,s55,89133056
buffer_load_dwordx4 v[188:191],v1,s[36:39],s78 offen offset:0 sc1
buffer_load_dwordx4 v[192:195],v1,s[36:39],s78 offen offset:2048 sc1
v_pk_fma_f32 v[124:125],v[52:53],v[92:93],v[124:125]
v_pk_fma_f32 v[126:127],v[54:55],v[94:95],v[126:127]
v_pk_fma_f32 v[128:129],v[56:57],v[96:97],v[128:129]
v_pk_fma_f32 v[130:131],v[58:59],v[98:99],v[130:131]
v_cvt_pk_bf16_f32 v152,v124,v125
v_cvt_pk_bf16_f32 v153,v126,v127
v_cvt_pk_bf16_f32 v154,v128,v129
v_cvt_pk_bf16_f32 v155,v130,v131
v_pk_fma_f32 v[52:53],v[132:133],v[48:49],v[50:51]
v_pk_fma_f32 v[54:55],v[134:135],v[48:49],v[50:51]
v_pk_fma_f32 v[56:57],v[136:137],v[48:49],v[50:51]
v_pk_fma_f32 v[58:59],v[138:139],v[48:49],v[50:51]
s_add_u32 m0,s65,0
s_nop 0
D 9,56,0
D 9,56,1024
D 9,56,2048
D 9,56,3072
v_pk_fma_f32 v[132:133],v[52:53],v[100:101],v[132:133]
v_pk_fma_f32 v[134:135],v[54:55],v[102:103],v[134:135]
v_pk_fma_f32 v[136:137],v[56:57],v[104:105],v[136:137]
v_pk_fma_f32 v[138:139],v[58:59],v[106:107],v[138:139]
v_cvt_pk_bf16_f32 v156,v132,v133
v_cvt_pk_bf16_f32 v157,v134,v135
v_cvt_pk_bf16_f32 v158,v136,v137
v_cvt_pk_bf16_f32 v159,v138,v139
v_pk_fma_f32 v[52:53],v[140:141],v[48:49],v[50:51]
v_pk_fma_f32 v[54:55],v[142:143],v[48:49],v[50:51]
v_pk_fma_f32 v[56:57],v[144:145],v[48:49],v[50:51]
v_pk_fma_f32 v[58:59],v[146:147],v[48:49],v[50:51]
v_pk_fma_f32 v[140:141],v[52:53],v[108:109],v[140:141]
v_pk_fma_f32 v[142:143],v[54:55],v[110:111],v[142:143]
v_pk_fma_f32 v[144:145],v[56:57],v[112:113],v[144:145]
v_pk_fma_f32 v[146:147],v[58:59],v[114:115],v[146:147]
v_cvt_pk_bf16_f32 v160,v140,v141
v_cvt_pk_bf16_f32 v161,v142,v143
v_cvt_pk_bf16_f32 v162,v144,v145
v_cvt_pk_bf16_f32 v163,v146,v147
v_mfma_f32_16x16x32_bf16 v[36:39],v[32:35],v[148:151],0
buffer_load_dword v14,v6,s[36:39],s62 offen offset:0 sc1
buffer_store_dwordx4 v[148:151],v1,s[36:39],s57 offen offset:0 sc0 sc1
v_mfma_f32_16x16x32_bf16 v[40:43],v[148:151],v[148:151],0
M 36,v[32:35],152
buffer_store_dwordx4 v[152:155],v1,s[36:39],s57 offen offset:2048 sc0 sc1
M 40,v[152:155],152
M 36,v[32:35],156
buffer_store_dwordx4 v[156:159],v1,s[36:39],s58 offen offset:0 sc0 sc1
M 40,v[156:159],156
M 36,v[32:35],160
buffer_store_dwordx4 v[160:163],v1,s[36:39],s58 offen offset:2048 sc0 sc1
M 40,v[160:163],160
s_nop 5
v_add_f32_dpp v44,v36,v36 row_ror:8 row_mask:0xf bank_mask:0xf
s_nop 0
v_cndmask_b32 v45,v40,v41,s[70:71]
v_cndmask_b32 v46,v42,v43,s[70:71]
ds_write_b32 v2,v44 offset:1024
v_cndmask_b32 v45,v45,v46,s[72:73]
ds_write_b32 v0,v45 offset:1024
L 0
s_barrier
R 52,3,1024
R 56,3,1040
R 60,3,1056
L 0
v_pk_add_f32 v[52:53],v[52:53],v[54:55]
v_pk_add_f32 v[56:57],v[56:57],v[58:59]
v_pk_add_f32 v[60:61],v[60:61],v[62:63]
v_add_f32 v52,v52,v53
v_pk_add_f32 v[56:57],v[56:57],v[60:61]
v_mul_f32 v52,s68,v52
v_add_f32 v53,v56,v57
v_mul_f32 v53,s68,v53
v_fma_f32 v53,-v52,v52,v53
v_max_f32 v53,0,v53
v_add_f32 v54,s69,v53
v_fma_f32 v55,s69,v54,v53
v_rsq_f32 v65,v54
v_rsq_f32 v54,v55
v_mul_f32_e64 v64,-v52,v65
v_mul_f32 v63,v55,v54
v_mul_f32 v61,s76,v54
v_mul_f32 v55,v54,v54
v_mul_f32 v62,s77,v55
v_mul_f32 v62,v62,v61
v_cndmask_b32 v60,v52,v64,s[74:75]
v_cndmask_b32 v61,v61,v65,s[74:75]
buffer_store_dwordx4 v[60:63],v4,s[36:39],s59 offen offset:0 sc0 sc1
s_mov_b32 s63,s60
s_lshr_b32 s78,s45,1
s_and_b32 s79,s45,1
s_lshl_b32 s79,s79,7
s_lshl_b32 s57,s78,15
s_add_u32 s57,s57,s79
s_add_u32 s57,s57,12582912
s_add_u32 s58,s57,4096
s_lshl_b32 s59,s78,9
s_add_u32 s59,s59,s79
s_add_u32 s59,s59,107020288
s_lshl_b32 s60,s45,9
s_add_u32 s60,s60,s66
s_add_u32 s60,s60,131072
s_add_u32 s45,s45,s46
s_lshr_b32 s78,s45,1
s_and_b32 s79,s45,1
s_lshl_b32 s79,s79,7
s_lshl_b32 s55,s78,15
s_add_u32 s55,s55,s79
s_mul_i32 s56,s45,16384
s_add_u32 s56,s56,98566144
s_lshl_b32 s61,s78,11
s_add_u32 s61,s61,110587904
s_add_u32 s78,s45,s46
s_lshr_b32 s78,s78,1
s_lshl_b32 s62,s78,11
s_add_u32 s62,s62,110587904
s_waitcnt vmcnt(5)
s_branch Lr_it0_%=
Lr_tc1_%=:
s_add_u32 s78,s55,71303168
buffer_load_dwordx4 v[164:167],v1,s[36:39],s78 offen offset:0 sc1
buffer_load_dwordx4 v[168:171],v1,s[36:39],s78 offen offset:2048 sc1
s_add_u32 s78,s55,71307264
buffer_load_dwordx4 v[172:175],v1,s[36:39],s78 offen offset:0 sc1
buffer_load_dwordx4 v[176:179],v1,s[36:39],s78 offen offset:2048 sc1
s_add_u32 s78,s55,89128960
buffer_load_dwordx4 v[180:183],v1,s[36:39],s78 offen offset:0 sc1
buffer_load_dwordx4 v[184:187],v1,s[36:39],s78 offen offset:2048 sc1
s_add_u32 s78,s55,89133056
buffer_load_dwordx4 v[188:191],v1,s[36:39],s78 offen offset:0 sc1
buffer_load_dwordx4 v[192:195],v1,s[36:39],s78 offen offset:2048 sc1
s_add_u32 m0,s65,0
s_nop 0
D 9,56,0
D 9,56,1024
D 9,56,2048
D 9,56,3072
buffer_load_dword v14,v6,s[36:39],s62 offen offset:0 sc1
s_waitcnt vmcnt(13)
buffer_store_dword v13,v5,s[36:39],s63 offen offset:0 sc0 sc1
s_mov_b32 s63,0x7fff0000
s_lshr_b32 s78,s45,1
s_and_b32 s79,s45,1
s_lshl_b32 s79,s79,7
s_lshl_b32 s57,s78,15
s_add_u32 s57,s57,s79
s_add_u32 s57,s57,12582912
s_add_u32 s58,s57,4096
s_lshl_b32 s59,s78,9
s_add_u32 s59,s59,s79
s_add_u32 s59,s59,107020288
s_lshl_b32 s60,s45,9
s_add_u32 s60,s60,s66
s_add_u32 s60,s60,131072
s_add_u32 s45,s45,s46
s_lshr_b32 s78,s45,1
s_and_b32 s79,s45,1
s_lshl_b32 s79,s79,7
s_lshl_b32 s55,s78,15
s_add_u32 s55,s55,s79
s_mul_i32 s56,s45,16384
s_add_u32 s56,s56,98566144
s_lshl_b32 s61,s78,11
s_add_u32 s61,s61,110587904
s_add_u32 s78,s45,s46
s_lshr_b32 s78,s78,1
s_lshl_b32 s62,s78,11
s_add_u32 s62,s62,110587904
s_waitcnt vmcnt(1)
s_branch Lr_it0_%=
Lr_done_%=:
s_waitcnt vmcnt(0) lgkmcnt(0)
s_barrier
Lcode_r_e:
.purgem M
.purgem R
.purgem T
.purgem D
.purgem L
)"
  :: "s"(ws), "s"(sWave), "s"(sG0), "s"(sStep), "s"(sOutOff), "s"(sAuxOff), "s"(sBuf), "s"(sRk), "s"(sExp), "v"(vRP), "v"(vRF), "v"(vRwr), "v"(vRrd), "v"(vRS), "v"(vFlag), "v"(vL4), "v"(vLY), "v"(vQ), "v"(vY0), "v"(vTr), "v"(vSa), "v"(vSb), "s"(q) : "v0", "v1", "v2", "v3", "v4", "v5", "v6", "v7", "v8", "v9", "v10", "v11", "v12", "v13", "v14", "v15", "v16", "v17", "v18", "v19", "v20", "v21", "v22", "v23", "v24", "v25", "v26", "v27", "v28", "v29", "v30", "v31", "v32", "v33", "v34", "v35", "v36", "v37", "v38", "v39", "v40", "v41", "v42", "v43", "v44", "v45", "v46", "v47", "v48", "v49", "v50", "v51", "v52", "v53", "v54", "v55", "v56", "v57", "v58", "v59", "v60", "v61", "v62", "v63", "v64", "v65", "v66", "v67", "v68", "v69", "v70", "v71", "v72", "v73", "v74", "v75", "v76", "v77", "v78", "v79", "v80", "v81", "v82", "v83", "v84", "v85", "v86", "v87", "v88", "v89", "v90", "v91", "v92", "v93", "v94", "v95", "v96", "v97", "v98", "v99", "v100", "v101", "v102", "v103", "v104", "v105", "v106", "v107", "v108", "v109", "v110", "v111", "v112", "v113", "v114", "v115", "v116", "v117", "v118", "v119", "v120", "v121", "v122", "v123", "v124", "v125", "v126", "v127", "v128", "v129", "v130", "v131", "v132", "v133", "v134", "v135", "v136", "v137", "v138", "v139", "v140", "v141", "v142", "v143", "v144", "v145", "v146", "v147", "v148", "v149", "v150", "v151", "v152", "v153", "v154", "v155", "v156", "v157", "v158", "v159", "v160", "v161", "v162", "v163", "v164", "v165", "v166", "v167", "v168", "v169", "v170", "v171", "v172", "v173", "v174", "v175", "v176", "v177", "v178", "v179", "v180", "v181", "v182", "v183", "v184", "v185", "v186", "v187", "v188", "v189", "v190", "v191", "v192", "v193", "v194", "v195", "v196", "v197", "v198", "v199", "v200", "v201", "v202", "v203", "v204", "v205", "v206", "v207", "v208", "v209", "v210", "v211", "v212", "v213", "v214", "v215", "v216", "v217", "v218", "v219", "v220", "v221", "v222", "v223", "v224", "v225", "v226", "v227", "v228", "v229", "v230", "v231", "s36", "s37", "s38", "s39", "s40", "s41", "s42", "s43", "s44", "s45", "s46", "s47", "s48", "s49", "s50", "s51", "s52", "s53", "s54", "s55", "s56", "s57", "s58", "s59", "s60", "s61", "s62", "s63", "s64", "s65", "s66", "s67", "s68", "s69", "s70", "s71", "s72", "s73", "s74", "s75", "s76", "s77", "s78", "s79", "s80", "s81", "s82", "s83", "s84", "s85", "s86", "s87", "s88", "s89", "s90", "s91", "s92", "s93", "s94", "s95", "s96", "s97", "vcc", "scc", "memory");
  }
  CTS(9);
#define COMMON(NF, FOFF, FSTR, SNAP, LDW, NL, TOFF)                                                                                            \
  LANE_ID(ln);                                                                                                                       \
  const int l15 = ln & 15, lg = ln >> 4, lq = l15 >> 2, lp = ln & 3, rr = 8 * lg + lq, fj = ln & 31;                                 \
  const unsigned vL16 = ln * 16, vFlag = (ln < 32 && wave == 0) ? ln * 4 : OOB;                                                      \
  const unsigned vSnapOff = (wave == 0 && fj < (NF)) ? (FOFF) + (ln >> 5) * sStep * (FSTR) : OOB;                                    \
  const unsigned vSnapRd = (SNAP) + (fj < (NF) ? ln : ln & 32) * 4;                                                                  \
  const unsigned vWd0 = (ln >> 3) * (LDW) + ((ln & 7) ^ swzB(ln >> 3)) * 16, vWd1 = ((ln >> 3) + 8) * (LDW) + ((ln & 7) ^ swzB(8 + (ln >> 3))) * 16; \
  const unsigned vWr0 = (wave >> 1) * 8192 + rr * 128 + (((wave & 1) * 4 + lp) ^ swzB(rr)) * 16, vWr1 = vWr0 + 8;                    \
  const unsigned vWr2 = 16384 + rr * 128 + ((2 * wave + (lp >> 1)) ^ swzB(rr)) * 16 + (lp & 1) * 8;                                  \
  const unsigned sWave = wave, sRk = kind == 4 ? U0 + rep : t_start, sG0 = rep < 5 ? rep : U0 + rep, sDbg = sDbg0 + (rep < 5 ? 0 : 2048)
  if (kind == 3 || kind == 5) {
    const unsigned sStep = kind == 3 ? 5 : G3OR;
    COMMON(8, fj * 128, 1024, 131072, 7168, 256, 0);
    const unsigned vOutS = (lg & 1) * 8 + (((wave & 1) * 2 + (lg >> 1)) * 16 + l15) * 16 + (4 + (wave >> 1) - wave) * 1024;
    const unsigned vAux0 = (ln & 7) * 128 + (4 * wave + lg) * 8 + ((ln >> 3) & 1) * 4;
    const float k1 = -2.0f * 0.7978845608028654f * 1.4426950408889634f;
    const unsigned vAux1 = 139264 + (l15 < 4 ? (4 * wave + lg) * 16 + l15 * 4 : 512 + ln * 4), vAux2 = 139264 + l15 * 16;
    const unsigned vAux3 = l15 == 2 ? __float_as_uint(k1 * 0.044715f) : 0u, vAn = l15 == 3 ? 0x3f800000u : 0u, vAe = l15 == 1 ? __float_as_uint(k1) : 0u;
    const unsigned sOutOff = OFF_G + (slice * 6 + wave) * 1024, sFoutOff = OFF_FG + slice * 128, sAuxOff = 0, sWw = 16 * wave * 7168 + slice * 384, sBuf = 0;
    const unsigned sExp = __builtin_amdgcn_readfirstlane(ep);
    if (kind == 3) { asm volatile(
R"(.macro M d,w,x
v_mfma_f32_16x16x32_bf16 v[\d:\d+3], \w, v[\x:\x+3], v[\d:\d+3]
.endm
.macro R d,a,o
ds_read_b128 v[\d:\d+3], v\a offset:\o
.endm
.macro T d,a,o
ds_read_b64_tr_b16 \d, v\a offset:\o
.endm
.macro D a,s,o
buffer_load_dwordx4 v\a, s[36:39], s\s offen offset:\o sc1 lds
.endm
.macro L c
s_waitcnt lgkmcnt(\c)
.endm
Lcode_g3_b:
s_mov_b64 s[36:37],%0
s_mov_b32 s38,111243392
s_mov_b32 s39,0x00020000
s_mov_b32 s44,%1
s_mov_b32 s45,%2
s_mov_b32 s46,%3
s_mov_b32 s47,%4
s_mov_b32 s48,%5
s_mov_b32 s49,%6
s_mov_b32 s50,%7
s_mov_b32 s51,%8
s_mov_b32 s53,%9
v_mov_b32 v0,%10
v_mov_b32 v3,%11
v_mov_b32 v5,%12
v_mov_b32 v6,%13
v_mov_b32 v18,%14
v_mov_b32 v19,%15
v_mov_b32 v20,%16
v_mov_b32 v21,%17
v_mov_b32 v22,%18
v_mov_b32 v10,%19
v_mov_b32 v11,%20
v_mov_b32 v12,%21
v_mov_b32 v13,%22
v_mov_b32 v14,%23
v_mov_b32 v15,%24
v_mov_b32 v16,%25
s_mov_b64 s[40:41],%26
s_mov_b32 s42,7340032
s_mov_b32 s43,0x00020000
s_lshl_b32 s57,s44,11
s_cmp_ge_u32 s45,8
s_cbranch_scc1 Lg3_dly_%=
s_sleep 16
s_sleep 16
s_sleep 16
Lg3_dly_%=:
s_add_u32 s55,s50,0
s_add_u32 m0,s57,0
s_nop 0
buffer_load_dwordx4 v18,s[40:43],s55 offen offset:0  lds
s_add_u32 m0,s57,1024
s_nop 0
buffer_load_dwordx4 v19,s[40:43],s55 offen offset:0  lds
s_add_u32 m0,s57,8064
s_nop 0
buffer_load_dwordx4 v18,s[40:43],s55 offen offset:128  lds
s_add_u32 m0,s57,9088
s_nop 0
buffer_load_dwordx4 v19,s[40:43],s55 offen offset:128  lds
s_add_u32 m0,s57,16128
s_nop 0
buffer_load_dwordx4 v18,s[40:43],s55 offen offset:256  lds
s_add_u32 m0,s57,17152
s_nop 0
buffer_load_dwordx4 v19,s[40:43],s55 offen offset:256  lds
s_add_u32 s55,s50,458752
s_add_u32 m0,s57,24576
s_nop 0
buffer_load_dwordx4 v18,s[40:43],s55 offen offset:0  lds
s_add_u32 m0,s57,25600
s_nop 0
buffer_load_dwordx4 v19,s[40:43],s55 offen offset:0  lds
s_add_u32 m0,s57,32640
s_nop 0
buffer_load_dwordx4 v18,s[40:43],s55 offen offset:128  lds
s_add_u32 m0,s57,33664
s_nop 0
buffer_load_dwordx4 v19,s[40:43],s55 offen offset:128  lds
s_add_u32 m0,s57,40704
s_nop 0
buffer_load_dwordx4 v18,s[40:43],s55 offen offset:256  lds
s_add_u32 m0,s57,41728
s_nop 0
buffer_load_dwordx4 v19,s[40:43],s55 offen offset:256  lds
s_add_u32 s55,s50,917504
s_add_u32 m0,s57,49152
s_nop 0
buffer_load_dwordx4 v18,s[40:43],s55 offen offset:0  lds
s_add_u32 m0,s57,50176
s_nop 0
buffer_load_dwordx4 v19,s[40:43],s55 offen offset:0  lds
s_add_u32 m0,s57,57216
s_nop 0
buffer_load_dwordx4 v18,s[40:43],s55 offen offset:128  lds
s_add_u32 m0,s57,58240
s_nop 0
buffer_load_dwordx4 v19,s[40:43],s55 offen offset:128  lds
s_add_u32 m0,s57,65280
s_nop 0
buffer_load_dwordx4 v18,s[40:43],s55 offen offset:256  lds
s_add_u32 m0,s57,66304
s_nop 0
buffer_load_dwordx4 v19,s[40:43],s55 offen offset:256  lds
s_add_u32 s55,s50,1376256
s_add_u32 m0,s57,73728
s_nop 0
buffer_load_dwordx4 v18,s[40:43],s55 offen offset:0  lds
s_add_u32 m0,s57,74752
s_nop 0
buffer_load_dwordx4 v19,s[40:43],s55 offen offset:0  lds
s_add_u32 m0,s57,81792
s_nop 0
buffer_load_dwordx4 v18,s[40:43],s55 offen offset:128  lds
s_add_u32 m0,s57,82816
s_nop 0
buffer_load_dwordx4 v19,s[40:43],s55 offen offset:128  lds
s_add_u32 m0,s57,89856
s_nop 0
buffer_load_dwordx4 v18,s[40:43],s55 offen offset:256  lds
s_add_u32 m0,s57,90880
s_nop 0
buffer_load_dwordx4 v19,s[40:43],s55 offen offset:256  lds
s_add_u32 s55,s50,1835008
s_add_u32 m0,s57,98304
s_nop 0
buffer_load_dwordx4 v18,s[40:43],s55 offen offset:0  lds
s_add_u32 m0,s57,99328
s_nop 0
buffer_load_dwordx4 v19,s[40:43],s55 offen offset:0  lds
s_add_u32 m0,s57,106368
s_nop 0
buffer_load_dwordx4 v18,s[40:43],s55 offen offset:128  lds
s_add_u32 m0,s57,107392
s_nop 0
buffer_load_dwordx4 v19,s[40:43],s55 offen offset:128  lds
s_add_u32 m0,s57,114432
s_nop 0
buffer_load_dwordx4 v18,s[40:43],s55 offen offset:256  lds
s_add_u32 m0,s57,115456
s_nop 0
buffer_load_dwordx4 v19,s[40:43],s55 offen offset:256  lds
s_waitcnt vmcnt(24) lgkmcnt(0)
s_barrier
s_add_u32 s55,s50,2293760
s_add_u32 m0,s57,122880
s_nop 0
buffer_load_dwordx4 v18,s[40:43],s55 offen offset:0  lds
s_add_u32 m0,s57,123904
s_nop 0
buffer_load_dwordx4 v19,s[40:43],s55 offen offset:0  lds
s_add_u32 m0,s57,130944
s_nop 0
buffer_load_dwordx4 v18,s[40:43],s55 offen offset:128  lds
s_add_u32 m0,s57,131968
s_nop 0
buffer_load_dwordx4 v19,s[40:43],s55 offen offset:128  lds
s_add_u32 m0,s57,139008
s_nop 0
buffer_load_dwordx4 v18,s[40:43],s55 offen offset:256  lds
s_add_u32 m0,s57,140032
s_nop 0
buffer_load_dwordx4 v19,s[40:43],s55 offen offset:256  lds
v_add_u32 v26,0,v20
v_add_u32 v27,0,v21
v_add_u32 v28,0,v22
T a[0:1],26,0
T a[2:3],26,512
T a[4:5],27,0
T a[6:7],27,512
T a[8:9],28,0
T a[10:11],28,512
T a[12:13],26,4096
T a[14:15],26,4608
T a[16:17],27,4096
T a[18:19],27,4608
T a[20:21],28,4096
T a[22:23],28,4608
s_waitcnt vmcnt(24) lgkmcnt(0)
s_barrier
s_add_u32 s55,s50,2752512
s_add_u32 m0,s57,0
s_nop 0
buffer_load_dwordx4 v18,s[40:43],s55 offen offset:0  lds
s_add_u32 m0,s57,1024
s_nop 0
buffer_load_dwordx4 v19,s[40:43],s55 offen offset:0  lds
s_add_u32 m0,s57,8064
s_nop 0
buffer_load_dwordx4 v18,s[40:43],s55 offen offset:128  lds
s_add_u32 m0,s57,9088
s_nop 0
buffer_load_dwordx4 v19,s[40:43],s55 offen offset:128  lds
s_add_u32 m0,s57,16128
s_nop 0
buffer_load_dwordx4 v18,s[40:43],s55 offen offset:256  lds
s_add_u32 m0,s57,17152
s_nop 0
buffer_load_dwordx4 v19,s[40:43],s55 offen offset:256  lds
v_add_u32 v26,24576,v20
v_add_u32 v27,24576,v21
v_add_u32 v28,24576,v22
T a[24:25],26,0
T a[26:27],26,512
T a[28:29],27,0
T a[30:31],27,512
T a[32:33],28,0
T a[34:35],28,512
T a[36:37],26,4096
T a[38:39],26,4608
T a[40:41],27,4096
T a[42:43],27,4608
T a[44:45],28,4096
T a[46:47],28,4608
s_waitcnt vmcnt(24) lgkmcnt(0)
s_barrier
s_add_u32 s55,s50,3211264
s_add_u32 m0,s57,24576
s_nop 0
buffer_load_dwordx4 v18,s[40:43],s55 offen offset:0  lds
s_add_u32 m0,s57,25600
s_nop 0
buffer_load_dwordx4 v19,s[40:43],s55 offen offset:0  lds
s_add_u32 m0,s57,32640
s_nop 0
buffer_load_dwordx4 v18,s[40:43],s55 offen offset:128  lds
s_add_u32 m0,s57,33664
s_nop 0
buffer_load_dwordx4 v19,s[40:43],s55 offen offset:128  lds
s_add_u32 m0,s57,40704
s_nop 0
buffer_load_dwordx4 v18,s[40:43],s55 offen offset:256  lds
s_add_u32 m0,s57,41728
s_nop 0
buffer_load_dwordx4 v19,s[40:43],s55 offen offset:256  lds
v_add_u32 v26,49152,v20
v_add_u32 v27,49152,v21
v_add_u32 v28,49152,v22
T a[48:49],26,0
T a[50:51],26,512
T a[52:53],27,0
T a[54:55],27,512
T a[56:57],28,0
T a[58:59],28,512
T a[60:61],26,4096
T a[62:63],26,4608
T a[64:65],27,4096
T a[66:67],27,4608
T a[68:69],28,4096
T a[70:71],28,4608
s_waitcnt vmcnt(24) lgkmcnt(0)
s_barrier
s_add_u32 s55,s50,3670016
s_add_u32 m0,s57,49152
s_nop 0
buffer_load_dwordx4 v18,s[40:43],s55 offen offset:0  lds
s_add_u32 m0,s57,50176
s_nop 0
buffer_load_dwordx4 v19,s[40:43],s55 offen offset:0  lds
s_add_u32 m0,s57,57216
s_nop 0
buffer_load_dwordx4 v18,s[40:43],s55 offen offset:128  lds
s_add_u32 m0,s57,58240
s_nop 0
buffer_load_dwordx4 v19,s[40:43],s55 offen offset:128  lds
s_add_u32 m0,s57,65280
s_nop 0
buffer_load_dwordx4 v18,s[40:43],s55 offen offset:256  lds
s_add_u32 m0,s57,66304
s_nop 0
buffer_load_dwordx4 v19,s[40:43],s55 offen offset:256  lds
v_add_u32 v26,73728,v20
v_add_u32 v27,73728,v21
v_add_u32 v28,73728,v22
T a[72:73],26,0
T a[74:75],26,512
T a[76:77],27,0
T a[78:79],27,512
T a[80:81],28,0
T a[82:83],28,512
T a[84:85],26,4096
T a[86:87],26,4608
T a[88:89],27,4096
T a[90:91],27,4608
T a[92:93],28,4096
T a[94:95],28,4608
s_waitcnt vmcnt(24) lgkmcnt(0)
s_barrier
s_add_u32 s55,s50,4128768
s_add_u32 m0,s57,73728
s_nop 0
buffer_load_dwordx4 v18,s[40:43],s55 offen offset:0  lds
s_add_u32 m0,s57,74752
s_nop 0
buffer_load_dwordx4 v19,s[40:43],s55 offen offset:0  lds
s_add_u32 m0,s57,81792
s_nop 0
buffer_load_dwordx4 v18,s[40:43],s55 offen offset:128  lds
s_add_u32 m0,s57,82816
s_nop 0
buffer_load_dwordx4 v19,s[40:43],s55 offen offset:128  lds
s_add_u32 m0,s57,89856
s_nop 0
buffer_load_dwordx4 v18,s[40:43],s55 offen offset:256  lds
s_add_u32 m0,s57,90880
s_nop 0
buffer_load_dwordx4 v19,s[40:43],s55 offen offset:256  lds
v_add_u32 v26,98304,v20
v_add_u32 v27,98304,v21
v_add_u32 v28,98304,v22
T a[96:97],26,0
T a[98:99],26,512
T a[100:101],27,0
T a[102:103],27,512
T a[104:105],28,0
T a[106:107],28,512
T a[108:109],26,4096
T a[110:111],26,4608
T a[112:113],27,4096
T a[114:115],27,4608
T a[116:117],28,4096
T a[118:119],28,4608
s_waitcnt vmcnt(24) lgkmcnt(0)
s_barrier
s_add_u32 s55,s50,4587520
s_add_u32 m0,s57,98304
s_nop 0
buffer_load_dwordx4 v18,s[40:43],s55 offen offset:0  lds
s_add_u32 m0,s57,99328
s_nop 0
buffer_load_dwordx4 v19,s[40:43],s55 offen offset:0  lds
s_add_u32 m0,s57,106368
s_nop 0
buffer_load_dwordx4 v18,s[40:43],s55 offen offset:128  lds
s_add_u32 m0,s57,107392
s_nop 0
buffer_load_dwordx4 v19,s[40:43],s55 offen offset:128  lds
s_add_u32 m0,s57,114432
s_nop 0
buffer_load_dwordx4 v18,s[40:43],s55 offen offset:256  lds
s_add_u32 m0,s57,115456
s_nop 0
buffer_load_dwordx4 v19,s[40:43],s55 offen offset:256  lds
v_add_u32 v26,122880,v20
v_add_u32 v27,122880,v21
v_add_u32 v28,122880,v22
T a[120:121],26,0
T a[122:123],26,512
T a[124:125],27,0
T a[126:127],27,512
T a[128:129],28,0
T a[130:131],28,512
T a[132:133],26,4096
T a[134:135],26,4608
T a[136:137],27,4096
T a[138:139],27,4608
T a[140:141],28,4096
T a[142:143],28,4608
s_waitcnt vmcnt(24) lgkmcnt(0)
s_barrier
s_add_u32 s55,s50,5046272
s_add_u32 m0,s57,122880
s_nop 0
buffer_load_dwordx4 v18,s[40:43],s55 offen offset:0  lds
s_add_u32 m0,s57,123904
s_nop 0
buffer_load_dwordx4 v19,s[40:43],s55 offen offset:0  lds
s_add_u32 m0,s57,130944
s_nop 0
buffer_load_dwordx4 v18,s[40:43],s55 offen offset:128  lds
s_add_u32 m0,s57,131968
s_nop 0
buffer_load_dwordx4 v19,s[40:43],s55 offen offset:128  lds
s_add_u32 m0,s57,139008
s_nop 0
buffer_load_dwordx4 v18,s[40:43],s55 offen offset:256  lds
s_add_u32 m0,s57,140032
s_nop 0
buffer_load_dwordx4 v19,s[40:43],s55 offen offset:256  lds
v_add_u32 v26,0,v20
v_add_u32 v27,0,v21
v_add_u32 v28,0,v22
T a[144:145],26,0
T a[146:147],26,512
T a[148:149],27,0
T a[150:151],27,512
T a[152:153],28,0
T a[154:155],28,512
T a[156:157],26,4096
T a[158:159],26,4608
T a[160:161],27,4096
T a[162:163],27,4608
T a[164:165],28,4096
T a[166:167],28,4608
s_waitcnt vmcnt(24) lgkmcnt(0)
s_barrier
s_add_u32 s55,s50,5505024
s_add_u32 m0,s57,0
s_nop 0
buffer_load_dwordx4 v18,s[40:43],s55 offen offset:0  lds
s_add_u32 m0,s57,1024
s_nop 0
buffer_load_dwordx4 v19,s[40:43],s55 offen offset:0  lds
s_add_u32 m0,s57,8064
s_nop 0
buffer_load_dwordx4 v18,s[40:43],s55 offen offset:128  lds
s_add_u32 m0,s57,9088
s_nop 0
buffer_load_dwordx4 v19,s[40:43],s55 offen offset:128  lds
s_add_u32 m0,s57,16128
s_nop 0
buffer_load_dwordx4 v18,s[40:43],s55 offen offset:256  lds
s_add_u32 m0,s57,17152
s_nop 0
buffer_load_dwordx4 v19,s[40:43],s55 offen offset:256  lds
v_add_u32 v26,24576,v20
v_add_u32 v27,24576,v21
v_add_u32 v28,24576,v22
T a[168:169],26,0
T a[170:171],26,512
T a[172:173],27,0
T a[174:175],27,512
T a[176:177],28,0
T a[178:179],28,512
T a[180:181],26,4096
T a[182:183],26,4608
T a[184:185],27,4096
T a[186:187],27,4608
T a[188:189],28,4096
T a[190:191],28,4608
s_waitcnt vmcnt(24) lgkmcnt(0)
s_barrier
s_add_u32 s55,s50,5963776
s_add_u32 m0,s57,24576
s_nop 0
buffer_load_dwordx4 v18,s[40:43],s55 offen offset:0  lds
s_add_u32 m0,s57,25600
s_nop 0
buffer_load_dwordx4 v19,s[40:43],s55 offen offset:0  lds
s_add_u32 m0,s57,32640
s_nop 0
buffer_load_dwordx4 v18,s[40:43],s55 offen offset:128  lds
s_add_u32 m0,s57,33664
s_nop 0
buffer_load_dwordx4 v19,s[40:43],s55 offen offset:128  lds
s_add_u32 m0,s57,40704
s_nop 0
buffer_load_dwordx4 v18,s[40:43],s55 offen offset:256  lds
s_add_u32 m0,s57,41728
s_nop 0
buffer_load_dwordx4 v19,s[40:43],s55 offen offset:256  lds
v_add_u32 v26,49152,v20
v_add_u32 v27,49152,v21
v_add_u32 v28,49152,v22
T a[192:193],26,0
T a[194:195],26,512
T a[196:197],27,0
T a[198:199],27,512
T a[200:201],28,0
T a[202:203],28,512
T a[204:205],26,4096
T a[206:207],26,4608
T a[208:209],27,4096
T a[210:211],27,4608
T a[212:213],28,4096
T a[214:215],28,4608
s_waitcnt vmcnt(24) lgkmcnt(0)
s_barrier
s_add_u32 s55,s50,6422528
s_add_u32 m0,s57,49152
s_nop 0
buffer_load_dwordx4 v18,s[40:43],s55 offen offset:0  lds
s_add_u32 m0,s57,50176
s_nop 0
buffer_load_dwordx4 v19,s[40:43],s55 offen offset:0  lds
s_add_u32 m0,s57,57216
s_nop 0
buffer_load_dwordx4 v18,s[40:43],s55 offen offset:128  lds
s_add_u32 m0,s57,58240
s_nop 0
buffer_load_dwordx4 v19,s[40:43],s55 offen offset:128  lds
s_add_u32 m0,s57,65280
s_nop 0
buffer_load_dwordx4 v18,s[40:43],s55 offen offset:256  lds
s_add_u32 m0,s57,66304
s_nop 0
buffer_load_dwordx4 v19,s[40:43],s55 offen offset:256  lds
v_add_u32 v26,73728,v20
v_add_u32 v27,73728,v21
v_add_u32 v28,73728,v22
T a[216:217],26,0
T a[218:219],26,512
T a[220:221],27,0
)"
R"(T a[222:223],27,512
T a[224:225],28,0
T a[226:227],28,512
T a[228:229],26,4096
T a[230:231],26,4608
T a[232:233],27,4096
T a[234:235],27,4608
T a[236:237],28,4096
T a[238:239],28,4608
s_waitcnt vmcnt(24) lgkmcnt(0)
s_barrier
s_add_u32 s55,s50,6881280
s_add_u32 m0,s57,73728
s_nop 0
buffer_load_dwordx4 v18,s[40:43],s55 offen offset:0  lds
s_add_u32 m0,s57,74752
s_nop 0
buffer_load_dwordx4 v19,s[40:43],s55 offen offset:0  lds
s_add_u32 m0,s57,81792
s_nop 0
buffer_load_dwordx4 v18,s[40:43],s55 offen offset:128  lds
s_add_u32 m0,s57,82816
s_nop 0
buffer_load_dwordx4 v19,s[40:43],s55 offen offset:128  lds
s_add_u32 m0,s57,89856
s_nop 0
buffer_load_dwordx4 v18,s[40:43],s55 offen offset:256  lds
s_add_u32 m0,s57,90880
s_nop 0
buffer_load_dwordx4 v19,s[40:43],s55 offen offset:256  lds
v_add_u32 v26,98304,v20
v_add_u32 v27,98304,v21
v_add_u32 v28,98304,v22
T a[240:241],26,0
T a[242:243],26,512
T a[244:245],27,0
T a[246:247],27,512
T a[248:249],28,0
T a[250:251],28,512
T a[252:253],26,4096
T a[254:255],26,4608
T v[128:129],27,4096
T v[130:131],27,4608
T v[132:133],28,4096
T v[134:135],28,4608
s_waitcnt vmcnt(24) lgkmcnt(0)
s_barrier
v_add_u32 v26,122880,v20
v_add_u32 v27,122880,v21
v_add_u32 v28,122880,v22
T v[136:137],26,0
T v[138:139],26,512
T v[140:141],27,0
T v[142:143],27,512
T v[144:145],28,0
T v[146:147],28,512
T v[148:149],26,4096
T v[150:151],26,4608
T v[152:153],27,4096
T v[154:155],27,4608
T v[156:157],28,4096
T v[158:159],28,4608
s_waitcnt vmcnt(18) lgkmcnt(0)
s_barrier
v_add_u32 v26,0,v20
v_add_u32 v27,0,v21
v_add_u32 v28,0,v22
T v[160:161],26,0
T v[162:163],26,512
T v[164:165],27,0
T v[166:167],27,512
T v[168:169],28,0
T v[170:171],28,512
T v[172:173],26,4096
T v[174:175],26,4608
T v[176:177],27,4096
T v[178:179],27,4608
T v[180:181],28,4096
T v[182:183],28,4608
s_waitcnt vmcnt(12) lgkmcnt(0)
s_barrier
v_add_u32 v26,24576,v20
v_add_u32 v27,24576,v21
v_add_u32 v28,24576,v22
T v[184:185],26,0
T v[186:187],26,512
T v[188:189],27,0
T v[190:191],27,512
T v[192:193],28,0
T v[194:195],28,512
T v[196:197],26,4096
T v[198:199],26,4608
T v[200:201],27,4096
T v[202:203],27,4608
T v[204:205],28,4096
T v[206:207],28,4608
s_waitcnt vmcnt(6) lgkmcnt(0)
s_barrier
v_add_u32 v26,49152,v20
v_add_u32 v27,49152,v21
v_add_u32 v28,49152,v22
T v[208:209],26,0
T v[210:211],26,512
T v[212:213],27,0
T v[214:215],27,512
T v[216:217],28,0
T v[218:219],28,512
T v[220:221],26,4096
T v[222:223],26,4608
T v[224:225],27,4096
T v[226:227],27,4608
T v[228:229],28,4096
T v[230:231],28,4608
s_waitcnt vmcnt(0) lgkmcnt(0)
s_barrier
v_add_u32 v26,73728,v20
v_add_u32 v27,73728,v21
v_add_u32 v28,73728,v22
T v[232:233],26,0
T v[234:235],26,512
T v[236:237],27,0
T v[238:239],27,512
T v[240:241],28,0
T v[242:243],28,512
T v[244:245],26,4096
T v[246:247],26,4608
T v[248:249],27,4096
T v[250:251],27,4608
T v[252:253],28,4096
T v[254:255],28,4608
L 0
s_barrier
v_mov_b32 v42,0x3f803f80
v_mov_b32 v43,0x3f803f80
v_mov_b32 v44,0x3f803f80
v_mov_b32 v45,0x3f803f80
s_nop 1
v_mfma_f32_16x16x32_bf16 v[82:85],a[0:3],v[42:45],0
v_mfma_f32_16x16x32_bf16 v[86:89],a[4:7],v[42:45],0
v_mfma_f32_16x16x32_bf16 v[90:93],a[8:11],v[42:45],0
M 82,a[12:15],42
M 86,a[16:19],42
M 90,a[20:23],42
M 82,a[24:27],42
M 86,a[28:31],42
M 90,a[32:35],42
M 82,a[36:39],42
M 86,a[40:43],42
M 90,a[44:47],42
M 82,a[48:51],42
M 86,a[52:55],42
M 90,a[56:59],42
M 82,a[60:63],42
M 86,a[64:67],42
M 90,a[68:71],42
M 82,a[72:75],42
M 86,a[76:79],42
M 90,a[80:83],42
M 82,a[84:87],42
M 86,a[88:91],42
M 90,a[92:95],42
M 82,a[96:99],42
M 86,a[100:103],42
M 90,a[104:107],42
M 82,a[108:111],42
M 86,a[112:115],42
M 90,a[116:119],42
M 82,a[120:123],42
M 86,a[124:127],42
M 90,a[128:131],42
M 82,a[132:135],42
M 86,a[136:139],42
M 90,a[140:143],42
M 82,a[144:147],42
M 86,a[148:151],42
M 90,a[152:155],42
M 82,a[156:159],42
M 86,a[160:163],42
M 90,a[164:167],42
M 82,a[168:171],42
M 86,a[172:175],42
M 90,a[176:179],42
M 82,a[180:183],42
M 86,a[184:187],42
M 90,a[188:191],42
M 82,a[192:195],42
M 86,a[196:199],42
M 90,a[200:203],42
M 82,a[204:207],42
M 86,a[208:211],42
M 90,a[212:215],42
M 82,a[216:219],42
M 86,a[220:223],42
M 90,a[224:227],42
M 82,a[228:231],42
M 86,a[232:235],42
M 90,a[236:239],42
M 82,a[240:243],42
M 86,a[244:247],42
M 90,a[248:251],42
M 82,a[252:255],42
M 86,v[128:131],42
M 90,v[132:135],42
M 82,v[136:139],42
M 86,v[140:143],42
M 90,v[144:147],42
M 82,v[148:151],42
M 86,v[152:155],42
M 90,v[156:159],42
M 82,v[160:163],42
M 86,v[164:167],42
M 90,v[168:171],42
M 82,v[172:175],42
M 86,v[176:179],42
M 90,v[180:183],42
M 82,v[184:187],42
M 86,v[188:191],42
M 90,v[192:195],42
M 82,v[196:199],42
M 86,v[200:203],42
M 90,v[204:207],42
M 82,v[208:211],42
M 86,v[212:215],42
M 90,v[216:219],42
M 82,v[220:223],42
M 86,v[224:227],42
M 90,v[228:231],42
M 82,v[232:235],42
M 86,v[236:239],42
M 90,v[240:243],42
M 82,v[244:247],42
M 86,v[248:251],42
M 90,v[252:255],42
v_mov_b32 v4,s53
s_mov_b32 s82,0
s_mov_b32 s80,0
s_mov_b32 s54,s45
s_mul_i32 s57,s44,4096
s_mul_i32 s55,s45,32768
s_add_u32 s55,s55,s57
s_add_u32 s55,s55,31457280
s_mul_i32 s57,s44,4096
s_mul_i32 s60,s46,32768
s_mul_i32 s61,s46,1024
s_mul_i32 s62,s46,114688
s_mul_i32 s63,s46,2560
s_mul_i32 s64,s46,1024
s_lshl_b32 s59,s44,8
s_add_u32 s59,s59,131072
s_mul_i32 s58,s45,1024
s_add_u32 s58,s58,109613056
s_mul_i32 s65,s45,114688
s_add_u32 s65,s65,s47
s_mul_i32 s68,s45,2560
s_add_u32 s68,s68,s48
s_mul_i32 s73,s45,1024
s_add_u32 s73,s73,107479040
s_mul_i32 s74,s45,32768
s_add_u32 s74,s74,s49
s_mul_i32 s75,s45,1024
s_add_u32 s75,s75,107479040
s_mov_b32 s66,0x7fff0000
s_mov_b32 s67,0x7fff0000
s_mov_b32 s69,0x7fff0000
s_mov_b32 s76,0x7fff0000
s_mov_b32 s77,0x7fff0000
s_mov_b32 s70,0x7fff0000
s_mov_b32 s71,0x7fff0000
s_mov_b32 s72,0x7fff0000
v_mov_b32 v1,v0
v_add_u32 v2,16384,v0
s_mov_b32 s84,16384
s_mov_b32 s86,0x3a800000
s_mov_b32 s87,0x3727c5ac
s_mov_b32 s88,0xc0135761
s_mov_b32 s89,0x3d372713
s_lshr_b32 s85,s44,1
s_mul_i32 s85,s85,127
v_lshlrev_b32 v8,22,v0
v_and_b32 v8,0x80000000,v8
v_or_b32 v8,v8,v5
v_add_u32 v2,32768,v0
s_lshl_b32 s84,s57,1
s_add_u32 s84,s84,32768
v_and_b32 v94,240,v0
v_cmp_eq_u32 vcc,0,v94
s_nop 1
v_cndmask_b32 v17,0,1.0,vcc
s_getpc_b64 s[40:41]
Lg3_pc_%=:
s_lshl_b32 s78,s44,13
v_lshlrev_b32 v94,3,v0
v_add_u32 v94,s78,v94
v_min_u32 v94,Lcode_g3_e-Lg3_pc_%=-4,v94
s_mov_b32 m0,163584
s_nop 0
global_load_lds_dword v94,s[40:41]
s_add_u32 m0,s59,4096
s_nop 0
buffer_load_dword v8,s[36:39],s58 offen offset:0 sc1 lds
buffer_store_dword v4,v3,s[36:39],s71 offen offset:0 sc0 sc1
s_add_u32 m0,s59,5120
s_nop 0
buffer_load_dword v8,s[36:39],s58 offen offset:0 sc1 lds
buffer_store_dword v4,v3,s[36:39],s71 offen offset:0 sc0 sc1
s_mov_b32 s83,0
Lg3_poll_%=:
s_add_u32 s78,s83,2
s_and_b32 s78,s78,3
s_lshl_b32 s78,s78,10
s_add_u32 s78,s78,4096
s_add_u32 m0,s59,s78
s_nop 0
buffer_load_dword v8,s[36:39],s58 offen offset:0 sc1 lds
s_and_b32 s78,s83,3
s_lshl_b32 s78,s78,10
v_add_u32 v94,s78,v6
s_waitcnt vmcnt(4)
s_barrier
ds_read_b32 v7,v94 offset:4096
buffer_store_dword v4,v3,s[36:39],s70 offen offset:0 sc0 sc1
s_mov_b32 s70,s71
s_mov_b32 s71,s72
s_mov_b32 s72,0x7fff0000
s_add_u32 s83,s83,1
s_add_u32 s82,s82,1
L 0
v_cmp_eq_u32 vcc,s53,v7
s_nop 0
s_cmp_eq_u32 vcc_lo,-1
s_cbranch_scc1 Lg3_go_%=
s_cmp_lt_u32 s82,20000
s_cbranch_scc1 Lg3_poll_%=
Lg3_go_%=:
buffer_load_dword v114,v11,s[36:39],s73 offen offset:0 sc1
s_add_u32 s73,s73,s64
s_add_u32 m0,s57,0
s_add_u32 s56,s55,0
D 0,56,0
D 0,56,1024
D 0,56,2048
D 0,56,3072
s_add_u32 s58,s58,s61
s_add_u32 m0,s59,2048
s_nop 0
buffer_load_dword v8,s[36:39],s58 offen offset:0 sc1 lds
s_add_u32 m0,s57,16384
s_add_u32 s56,s55,16384
D 0,56,0
D 0,56,1024
D 0,56,2048
D 0,56,3072
s_add_u32 s55,s55,s60
s_sub_u32 s57,s84,s57
s_add_u32 s54,s54,s46
s_waitcnt vmcnt(4)
s_barrier
buffer_store_dword v4,v3,s[36:39],s70 offen offset:0 sc0 sc1
s_min_u32 s71,s71,s72
buffer_store_dword v4,v3,s[36:39],s71 offen offset:0 sc0 sc1
s_mov_b32 s70,0x7fff0000
s_mov_b32 s71,0x7fff0000
s_mov_b32 s72,0x7fff0000
s_cmp_eq_u32 s80,1
buffer_store_dword v4,v3,s[36:39],s71 offen offset:0 sc0 sc1
s_cbranch_scc1 Lg3_st1_%=
s_branch Lg3_st0_%=
Lg3_gox_%=:
s_add_u32 m0,s59,2048
s_nop 0
buffer_load_dword v8,s[36:39],s58 offen offset:0 sc1 lds
s_waitcnt vmcnt(0)
s_barrier
buffer_store_dword v4,v3,s[36:39],s70 offen offset:0 sc0 sc1
s_min_u32 s71,s71,s72
buffer_store_dword v4,v3,s[36:39],s71 offen offset:0 sc0 sc1
s_mov_b32 s70,0x7fff0000
s_mov_b32 s71,0x7fff0000
s_mov_b32 s72,0x7fff0000
s_cmp_eq_u32 s80,1
s_cbranch_scc1 Lg3_st1_%=
Lg3_st0_%=:
ds_read_b32 v7,v6 offset:2048
R 42,1,0
R 46,1,1024
R 50,1,2048
R 54,1,3072
R 58,1,4096
L 5
v_cmp_eq_u32 vcc,s53,v7
L 3
s_cmp_eq_u32 vcc_lo,-1
s_cbranch_scc1 Lg3_F0_%=
s_branch Lg3_L0_%=
Lg3_st1_%=:
ds_read_b32 v7,v6 offset:2048
R 42,2,0
R 46,2,1024
R 50,2,2048
R 54,2,3072
R 58,2,4096
L 5
v_cmp_eq_u32 vcc,s53,v7
L 3
s_cmp_eq_u32 vcc_lo,-1
s_cbranch_scc1 Lg3_F1_%=
s_branch Lg3_L1_%=
Lg3_F0_%=:
v_mfma_f32_16x16x32_bf16 v[26:29],a[8:11],v[42:45],0
s_add_u32 m0,s57,0
R 62,1,5120
v_add_f32_dpp v115,v114,v114 quad_perm:[1,0,3,2] row_mask:0xf bank_mask:0xf
M 26,a[20:23],46
buffer_load_dword v114,v11,s[36:39],s73 offen offset:0 sc1
v_mfma_f32_16x16x32_bf16 v[22:25],a[4:7],v[42:45],0
s_add_u32 s56,s55,0
v_add_f32_dpp v115,v115,v115 quad_perm:[2,3,0,1] row_mask:0xf bank_mask:0xf
v_fma_f32 v30,-v78,v82,v30
M 22,a[16:19],46
s_add_u32 s58,s58,s61
R 66,1,6144
v_fma_f32 v31,-v78,v83,v31
v_mfma_f32_16x16x32_bf16 v[18:21],a[0:3],v[42:45],0
D 0,56,0
M 18,a[12:15],46
L 3
v_add_f32_dpp v118,v115,v115 row_half_mirror row_mask:0xf bank_mask:0xf
v_fma_f32 v32,-v78,v84,v32
M 18,a[24:27],50
s_mov_b32 s66,s65
R 70,1,7168
v_fma_f32 v33,-v78,v85,v33
M 18,a[36:39],54
s_add_u32 s65,s65,s62
v_mov_b32_dpp v115,v118 row_ror:8 row_mask:0xf bank_mask:0xf
v_fma_f32 v115,-v118,v118,v115
M 22,a[28:31],50
s_add_u32 s54,s54,s46
v_max_f32 v115,s87,v115
v_mul_f32 v118,v17,v118
M 22,a[40:43],54
s_add_u32 s73,s73,s64
R 42,1,8192
v_mul_f32 v94,v30,v30
M 26,a[32:35],50
v_rsq_f32 v116,v115
M 26,a[44:47],54
L 3
D 0,56,1024
M 26,a[56:59],58
R 46,1,9216
v_fma_f32 v115,v15,v115,v16
M 26,a[68:71],62
v_mul_f32 v119,v116,v116
v_fma_f32 v115,v14,v119,v115
M 22,a[52:55],58
v_fma_f32 v118,v116,v115,v118
v_mul_f32 v95,v31,v31
M 22,a[64:67],62
R 50,1,10240
v_mul_f32 v96,v32,v32
M 18,a[48:51],58
ds_write_b32 v12,v118 offset:0
M 18,a[60:63],62
L 4
v_mul_f32 v97,v33,v33
v_fma_f32 v94,v80,v94,v79
M 18,a[72:75],66
R 54,1,11264
v_fma_f32 v95,v80,v95,v79
M 18,a[84:87],70
v_fma_f32 v96,v80,v96,v79
v_fma_f32 v97,v80,v97,v79
M 22,a[76:79],66
v_mul_f32 v94,v30,v94
v_mul_f32 v95,v31,v95
M 22,a[88:91],70
R 58,1,12288
v_mul_f32 v96,v32,v96
M 26,a[80:83],66
D 0,56,2048
M 26,a[92:95],70
L 4
v_exp_f32 v94,v94
M 26,a[104:107],42
R 62,1,13312
v_mul_f32 v97,v33,v97
M 26,a[116:119],46
v_exp_f32 v95,v95
M 22,a[100:103],42
v_exp_f32 v96,v96
M 22,a[112:115],46
R 66,1,14336
v_fma_f32 v94,v94,v81,v81
M 18,a[96:99],42
v_exp_f32 v97,v97
M 18,a[108:111],46
L 3
v_rcp_f32 v94,v94
M 18,a[120:123],50
)"
R"(R 70,1,15360
v_fma_f32 v95,v95,v81,v81
M 18,a[132:135],54
s_waitcnt vmcnt(7)
D 0,56,3072
M 22,a[124:127],50
s_barrier
v_rcp_f32 v95,v95
M 22,a[136:139],54
s_add_u32 m0,s59,0
R 42,1,16384
v_fma_f32 v96,v96,v81,v81
M 26,a[128:131],50
s_add_u32 s56,s55,16384
v_rcp_f32 v96,v96
M 26,a[140:143],54
L 3
v_fma_f32 v97,v97,v81,v81
v_mul_f32 v30,v30,v94
M 26,a[152:155],58
s_add_u32 s55,s55,s60
R 46,1,17408
v_mul_f32 v31,v31,v95
M 26,a[164:167],62
v_rcp_f32 v97,v97
M 22,a[148:151],58
v_mul_f32 v32,v32,v96
v_mul_f32 v33,v33,v97
M 22,a[160:163],62
R 50,1,18432
v_cvt_pk_bf16_f32 v106,v30,v31
M 18,a[144:147],58
buffer_load_dword v8,s[36:39],s58 offen offset:0 sc1 lds
M 18,a[156:159],62
L 3
v_cvt_pk_bf16_f32 v107,v32,v33
v_fma_f32 v34,-v78,v86,v34
M 18,a[168:171],66
s_add_u32 m0,s57,16384
R 54,1,19456
v_fma_f32 v35,-v78,v87,v35
M 18,a[180:183],70
s_sub_u32 s57,s84,s57
v_fma_f32 v36,-v78,v88,v36
v_fma_f32 v37,-v78,v89,v37
M 22,a[172:175],66
v_mul_f32 v98,v34,v34
v_mul_f32 v99,v35,v35
M 22,a[184:187],70
R 58,1,20480
v_mul_f32 v100,v36,v36
M 26,a[176:179],66
D 0,56,0
M 26,a[188:191],70
L 3
v_mul_f32 v101,v37,v37
v_fma_f32 v98,v80,v98,v79
M 26,a[200:203],42
R 62,1,21504
v_fma_f32 v99,v80,v99,v79
M 26,a[212:215],46
v_fma_f32 v100,v80,v100,v79
v_fma_f32 v101,v80,v101,v79
M 22,a[196:199],42
v_mul_f32 v98,v34,v98
v_mul_f32 v99,v35,v99
M 22,a[208:211],46
R 66,1,22528
v_mul_f32 v100,v36,v100
M 18,a[192:195],42
v_exp_f32 v98,v98
M 18,a[204:207],46
L 3
v_exp_f32 v99,v99
M 18,a[216:219],50
R 70,1,23552
v_mul_f32 v101,v37,v101
M 18,a[228:231],54
D 0,56,1024
M 22,a[220:223],50
v_exp_f32 v100,v100
M 22,a[232:235],54
R 42,1,24576
v_fma_f32 v98,v98,v81,v81
M 26,a[224:227],50
v_exp_f32 v101,v101
M 26,a[236:239],54
L 3
v_rcp_f32 v98,v98
M 26,a[248:251],58
R 46,1,25600
v_fma_f32 v99,v99,v81,v81
M 26,v[132:135],62
v_rcp_f32 v99,v99
M 22,a[244:247],58
v_fma_f32 v100,v100,v81,v81
v_fma_f32 v101,v101,v81,v81
M 22,v[128:131],62
R 50,1,26624
v_mul_f32 v34,v34,v98
M 18,a[240:243],58
v_rcp_f32 v100,v100
M 18,a[252:255],62
L 3
v_rcp_f32 v101,v101
M 18,v[136:139],66
R 54,1,27648
v_mul_f32 v35,v35,v99
M 18,v[148:151],70
D 0,56,2048
M 22,v[140:143],66
v_mul_f32 v36,v36,v100
v_mul_f32 v37,v37,v101
M 22,v[152:155],70
R 58,1,28672
v_cvt_pk_bf16_f32 v108,v34,v35
M 26,v[144:147],66
v_cvt_pk_bf16_f32 v109,v36,v37
v_fma_f32 v38,-v78,v90,v38
M 26,v[156:159],70
L 3
v_fma_f32 v39,-v78,v91,v39
v_fma_f32 v40,-v78,v92,v40
M 26,v[168:171],42
R 62,1,29696
v_fma_f32 v41,-v78,v93,v41
M 26,v[180:183],46
v_mul_f32 v102,v38,v38
v_mul_f32 v103,v39,v39
M 22,v[164:167],42
v_mul_f32 v104,v40,v40
v_mul_f32 v105,v41,v41
M 22,v[176:179],46
R 66,1,30720
v_fma_f32 v102,v80,v102,v79
M 18,v[160:163],42
v_fma_f32 v103,v80,v103,v79
v_fma_f32 v104,v80,v104,v79
M 18,v[172:175],46
L 3
D 0,56,3072
M 18,v[184:187],50
s_nop 0
R 70,1,31744
v_fma_f32 v105,v80,v105,v79
M 18,v[196:199],54
s_waitcnt vmcnt(4)
v_mul_f32 v102,v38,v102
v_mul_f32 v103,v39,v103
M 22,v[188:191],50
s_barrier
buffer_store_dword v4,v3,s[36:39],s70 offen offset:0 sc0 sc1
M 22,v[200:203],54
s_mov_b32 s70,s69
R 42,2,0
ds_read_b32 v7,v6 offset:0
M 26,v[192:195],50
s_mov_b32 s69,s68
R 74,13,0
v_mul_f32 v104,v40,v104
M 26,v[204:207],54
L 5
v_exp_f32 v102,v102
M 26,v[216:219],58
s_add_u32 s68,s68,s63
R 46,2,1024
v_mul_f32 v105,v41,v105
M 26,v[228:231],62
v_exp_f32 v103,v103
M 22,v[212:215],58
buffer_store_dwordx4 v[106:109],v0,s[36:39],s67 offen offset:0 sc0 sc1
v_fma_f32 v102,v102,v81,v81
M 22,v[224:227],62
L 2
R 50,2,2048
v_fma_f32 v103,v103,v81,v81
M 18,v[208:211],58
v_exp_f32 v104,v104
M 18,v[220:223],62
s_nop 0
v_exp_f32 v105,v105
M 18,v[232:235],66
R 54,2,3072
v_fma_f32 v104,v104,v81,v81
M 18,v[244:247],70
v_fma_f32 v105,v105,v81,v81
v_cmp_eq_u32 vcc,s53,v7
M 22,v[236:239],66
v_rcp_f32 v102,v102
v_rcp_f32 v103,v103
v_rcp_f32 v104,v104
v_rcp_f32 v105,v105
M 22,v[248:251],70
s_cmp_eq_u32 vcc_lo,-1
R 58,2,4096
v_mul_f32 v38,v38,v102
v_mul_f32 v39,v39,v103
v_mul_f32 v40,v40,v104
v_mul_f32 v41,v41,v105
v_cvt_pk_bf16_f32 v110,v38,v39
v_cvt_pk_bf16_f32 v111,v40,v41
M 26,v[240:243],66
L 3
buffer_store_dwordx2 v[110:111],v10,s[36:39],s67 offen offset:0 sc0 sc1
M 26,v[252:255],70
s_cbranch_scc0 Lg3_L1_%=
Lg3_F1_%=:
v_mfma_f32_16x16x32_bf16 v[38:41],a[8:11],v[42:45],0
s_add_u32 m0,s57,0
R 62,2,5120
v_add_f32_dpp v115,v114,v114 quad_perm:[1,0,3,2] row_mask:0xf bank_mask:0xf
M 38,a[20:23],46
buffer_load_dword v114,v11,s[36:39],s73 offen offset:0 sc1
v_mfma_f32_16x16x32_bf16 v[34:37],a[4:7],v[42:45],0
s_add_u32 s56,s55,0
v_add_f32_dpp v115,v115,v115 quad_perm:[2,3,0,1] row_mask:0xf bank_mask:0xf
v_fma_f32 v18,-v74,v82,v18
M 34,a[16:19],46
s_add_u32 s58,s58,s61
R 66,2,6144
v_fma_f32 v19,-v74,v83,v19
v_mfma_f32_16x16x32_bf16 v[30:33],a[0:3],v[42:45],0
D 0,56,0
M 30,a[12:15],46
L 3
v_add_f32_dpp v118,v115,v115 row_half_mirror row_mask:0xf bank_mask:0xf
v_fma_f32 v20,-v74,v84,v20
M 30,a[24:27],50
s_mov_b32 s67,s65
R 70,2,7168
v_fma_f32 v21,-v74,v85,v21
M 30,a[36:39],54
s_add_u32 s65,s65,s62
v_mov_b32_dpp v115,v118 row_ror:8 row_mask:0xf bank_mask:0xf
v_fma_f32 v115,-v118,v118,v115
M 34,a[28:31],50
s_add_u32 s54,s54,s46
v_max_f32 v115,s87,v115
v_mul_f32 v118,v17,v118
M 34,a[40:43],54
s_add_u32 s73,s73,s64
R 42,2,8192
v_mul_f32 v94,v18,v18
M 38,a[32:35],50
v_rsq_f32 v116,v115
M 38,a[44:47],54
L 3
D 0,56,1024
M 38,a[56:59],58
R 46,2,9216
v_fma_f32 v115,v15,v115,v16
M 38,a[68:71],62
v_mul_f32 v119,v116,v116
v_fma_f32 v115,v14,v119,v115
M 34,a[52:55],58
v_fma_f32 v118,v116,v115,v118
v_mul_f32 v95,v19,v19
M 34,a[64:67],62
R 50,2,10240
v_mul_f32 v96,v20,v20
M 30,a[48:51],58
ds_write_b32 v12,v118 offset:2048
M 30,a[60:63],62
L 4
v_mul_f32 v97,v21,v21
v_fma_f32 v94,v76,v94,v75
M 30,a[72:75],66
R 54,2,11264
v_fma_f32 v95,v76,v95,v75
M 30,a[84:87],70
v_fma_f32 v96,v76,v96,v75
v_fma_f32 v97,v76,v97,v75
M 34,a[76:79],66
v_mul_f32 v94,v18,v94
v_mul_f32 v95,v19,v95
M 34,a[88:91],70
R 58,2,12288
v_mul_f32 v96,v20,v96
M 38,a[80:83],66
D 0,56,2048
M 38,a[92:95],70
L 4
v_exp_f32 v94,v94
M 38,a[104:107],42
R 62,2,13312
v_mul_f32 v97,v21,v97
M 38,a[116:119],46
v_exp_f32 v95,v95
M 34,a[100:103],42
v_exp_f32 v96,v96
M 34,a[112:115],46
R 66,2,14336
v_fma_f32 v94,v94,v77,v77
M 30,a[96:99],42
v_exp_f32 v97,v97
M 30,a[108:111],46
L 3
v_rcp_f32 v94,v94
M 30,a[120:123],50
R 70,2,15360
v_fma_f32 v95,v95,v77,v77
M 30,a[132:135],54
s_waitcnt vmcnt(7)
D 0,56,3072
M 34,a[124:127],50
s_barrier
v_rcp_f32 v95,v95
M 34,a[136:139],54
s_add_u32 m0,s59,1024
R 42,2,16384
v_fma_f32 v96,v96,v77,v77
M 38,a[128:131],50
s_add_u32 s56,s55,16384
v_rcp_f32 v96,v96
M 38,a[140:143],54
L 3
v_fma_f32 v97,v97,v77,v77
v_mul_f32 v18,v18,v94
M 38,a[152:155],58
s_add_u32 s55,s55,s60
R 46,2,17408
v_mul_f32 v19,v19,v95
M 38,a[164:167],62
v_rcp_f32 v97,v97
M 34,a[148:151],58
v_mul_f32 v20,v20,v96
v_mul_f32 v21,v21,v97
M 34,a[160:163],62
R 50,2,18432
v_cvt_pk_bf16_f32 v106,v18,v19
M 30,a[144:147],58
buffer_load_dword v8,s[36:39],s58 offen offset:0 sc1 lds
M 30,a[156:159],62
L 3
v_cvt_pk_bf16_f32 v107,v20,v21
v_fma_f32 v22,-v74,v86,v22
M 30,a[168:171],66
s_add_u32 m0,s57,16384
R 54,2,19456
v_fma_f32 v23,-v74,v87,v23
M 30,a[180:183],70
s_sub_u32 s57,s84,s57
v_fma_f32 v24,-v74,v88,v24
v_fma_f32 v25,-v74,v89,v25
M 34,a[172:175],66
v_mul_f32 v98,v22,v22
v_mul_f32 v99,v23,v23
M 34,a[184:187],70
R 58,2,20480
v_mul_f32 v100,v24,v24
M 38,a[176:179],66
D 0,56,0
M 38,a[188:191],70
L 3
v_mul_f32 v101,v25,v25
v_fma_f32 v98,v76,v98,v75
M 38,a[200:203],42
R 62,2,21504
v_fma_f32 v99,v76,v99,v75
M 38,a[212:215],46
v_fma_f32 v100,v76,v100,v75
v_fma_f32 v101,v76,v101,v75
M 34,a[196:199],42
v_mul_f32 v98,v22,v98
v_mul_f32 v99,v23,v99
M 34,a[208:211],46
R 66,2,22528
v_mul_f32 v100,v24,v100
M 30,a[192:195],42
v_exp_f32 v98,v98
M 30,a[204:207],46
L 3
v_exp_f32 v99,v99
M 30,a[216:219],50
R 70,2,23552
v_mul_f32 v101,v25,v101
M 30,a[228:231],54
D 0,56,1024
M 34,a[220:223],50
v_exp_f32 v100,v100
M 34,a[232:235],54
R 42,2,24576
v_fma_f32 v98,v98,v77,v77
M 38,a[224:227],50
v_exp_f32 v101,v101
M 38,a[236:239],54
L 3
v_rcp_f32 v98,v98
M 38,a[248:251],58
R 46,2,25600
v_fma_f32 v99,v99,v77,v77
M 38,v[132:135],62
v_rcp_f32 v99,v99
M 34,a[244:247],58
v_fma_f32 v100,v100,v77,v77
v_fma_f32 v101,v101,v77,v77
M 34,v[128:131],62
R 50,2,26624
v_mul_f32 v22,v22,v98
M 30,a[240:243],58
v_rcp_f32 v100,v100
M 30,a[252:255],62
L 3
v_rcp_f32 v101,v101
M 30,v[136:139],66
R 54,2,27648
v_mul_f32 v23,v23,v99
M 30,v[148:151],70
D 0,56,2048
M 34,v[140:143],66
v_mul_f32 v24,v24,v100
v_mul_f32 v25,v25,v101
M 34,v[152:155],70
R 58,2,28672
v_cvt_pk_bf16_f32 v108,v22,v23
M 38,v[144:147],66
v_cvt_pk_bf16_f32 v109,v24,v25
v_fma_f32 v26,-v74,v90,v26
M 38,v[156:159],70
L 3
v_fma_f32 v27,-v74,v91,v27
v_fma_f32 v28,-v74,v92,v28
M 38,v[168:171],42
R 62,2,29696
v_fma_f32 v29,-v74,v93,v29
M 38,v[180:183],46
v_mul_f32 v102,v26,v26
v_mul_f32 v103,v27,v27
M 34,v[164:167],42
v_mul_f32 v104,v28,v28
v_mul_f32 v105,v29,v29
M 34,v[176:179],46
R 66,2,30720
v_fma_f32 v102,v76,v102,v75
M 30,v[160:163],42
v_fma_f32 v103,v76,v103,v75
v_fma_f32 v104,v76,v104,v75
M 30,v[172:175],46
L 3
D 0,56,3072
M 30,v[184:187],50
s_nop 0
R 70,2,31744
v_fma_f32 v105,v76,v105,v75
M 30,v[196:199],54
s_waitcnt vmcnt(4)
v_mul_f32 v102,v26,v102
v_mul_f32 v103,v27,v103
M 34,v[188:191],50
s_barrier
buffer_store_dword v4,v3,s[36:39],s70 offen offset:0 sc0 sc1
M 34,v[200:203],54
s_mov_b32 s70,s69
R 42,1,0
ds_read_b32 v7,v6 offset:1024
M 38,v[192:195],50
s_mov_b32 s69,s68
R 78,13,2048
v_mul_f32 v104,v28,v104
M 38,v[204:207],54
L 5
v_exp_f32 v102,v102
M 38,v[216:219],58
s_add_u32 s68,s68,s63
R 46,1,1024
v_mul_f32 v105,v29,v105
M 38,v[228:231],62
v_exp_f32 v103,v103
M 34,v[212:215],58
buffer_store_dwordx4 v[106:109],v0,s[36:39],s66 offen offset:0 sc0 sc1
v_fma_f32 v102,v102,v77,v77
M 34,v[224:227],62
L 2
R 50,1,2048
v_fma_f32 v103,v103,v77,v77
M 30,v[208:211],58
v_exp_f32 v104,v104
M 30,v[220:223],62
s_nop 0
v_exp_f32 v105,v105
M 30,v[232:235],66
R 54,1,3072
v_fma_f32 v104,v104,v77,v77
M 30,v[244:247],70
v_fma_f32 v105,v105,v77,v77
v_cmp_eq_u32 vcc,s53,v7
M 34,v[236:239],66
v_rcp_f32 v102,v102
v_rcp_f32 v103,v103
v_rcp_f32 v104,v104
v_rcp_f32 v105,v105
M 34,v[248:251],70
s_cmp_eq_u32 vcc_lo,-1
R 58,1,4096
v_mul_f32 v26,v26,v102
v_mul_f32 v27,v27,v103
v_mul_f32 v28,v28,v104
v_mul_f32 v29,v29,v105
v_cvt_pk_bf16_f32 v110,v26,v27
v_cvt_pk_bf16_f32 v111,v28,v29
)"
R"(M 38,v[240:243],66
L 3
buffer_store_dwordx2 v[110:111],v10,s[36:39],s66 offen offset:0 sc0 sc1
M 38,v[252:255],70
s_cbranch_scc1 Lg3_F0_%=
Lg3_L0_%=:
v_mfma_f32_16x16x32_bf16 v[26:29],a[8:11],v[42:45],0
s_add_u32 m0,s59,3072
R 62,1,5120
v_add_f32_dpp v115,v114,v114 quad_perm:[1,0,3,2] row_mask:0xf bank_mask:0xf
M 26,a[20:23],46
buffer_load_dword v8,s[36:39],s58 offen offset:0 sc1 lds
v_mfma_f32_16x16x32_bf16 v[22:25],a[4:7],v[42:45],0
s_mov_b32 s66,s65
v_add_f32_dpp v115,v115,v115 quad_perm:[2,3,0,1] row_mask:0xf bank_mask:0xf
v_fma_f32 v30,-v78,v82,v30
M 22,a[16:19],46
s_add_u32 s65,s65,s62
R 66,1,6144
v_fma_f32 v31,-v78,v83,v31
v_mfma_f32_16x16x32_bf16 v[18:21],a[0:3],v[42:45],0
v_add_f32_dpp v118,v115,v115 row_half_mirror row_mask:0xf bank_mask:0xf
v_fma_f32 v32,-v78,v84,v32
v_fma_f32 v33,-v78,v85,v33
M 18,a[12:15],46
L 3
v_mul_f32 v94,v30,v30
v_mul_f32 v95,v31,v31
M 18,a[24:27],50
R 70,1,7168
v_mov_b32_dpp v115,v118 row_ror:8 row_mask:0xf bank_mask:0xf
M 18,a[36:39],54
v_fma_f32 v115,-v118,v118,v115
v_max_f32 v115,s87,v115
M 22,a[28:31],50
v_rsq_f32 v116,v115
M 22,a[40:43],54
R 42,1,8192
v_fma_f32 v115,v15,v115,v16
v_mul_f32 v118,v17,v118
M 26,a[32:35],50
v_mul_f32 v119,v116,v116
v_fma_f32 v115,v14,v119,v115
M 26,a[44:47],54
L 3
v_fma_f32 v118,v116,v115,v118
v_mul_f32 v96,v32,v32
M 26,a[56:59],58
R 46,1,9216
v_mul_f32 v97,v33,v33
M 26,a[68:71],62
ds_write_b32 v12,v118 offset:0
M 22,a[52:55],58
v_fma_f32 v94,v80,v94,v79
v_fma_f32 v95,v80,v95,v79
v_fma_f32 v96,v80,v96,v79
M 22,a[64:67],62
R 50,1,10240
v_fma_f32 v97,v80,v97,v79
M 18,a[48:51],58
v_mul_f32 v94,v30,v94
v_mul_f32 v95,v31,v95
M 18,a[60:63],62
L 4
v_exp_f32 v94,v94
M 18,a[72:75],66
R 54,1,11264
v_mul_f32 v96,v32,v96
M 18,a[84:87],70
v_exp_f32 v95,v95
v_mul_f32 v97,v33,v97
M 22,a[76:79],66
v_exp_f32 v96,v96
M 22,a[88:91],70
R 58,1,12288
v_fma_f32 v94,v94,v81,v81
M 26,a[80:83],66
v_exp_f32 v97,v97
M 26,a[92:95],70
L 4
v_rcp_f32 v94,v94
M 26,a[104:107],42
R 62,1,13312
v_fma_f32 v95,v95,v81,v81
v_fma_f32 v96,v96,v81,v81
M 26,a[116:119],46
v_rcp_f32 v95,v95
M 22,a[100:103],42
v_rcp_f32 v96,v96
M 22,a[112:115],46
R 66,1,14336
v_fma_f32 v97,v97,v81,v81
M 18,a[96:99],42
v_rcp_f32 v97,v97
M 18,a[108:111],46
L 3
v_mul_f32 v30,v30,v94
v_mul_f32 v31,v31,v95
v_mul_f32 v32,v32,v96
M 18,a[120:123],50
R 70,1,15360
v_mul_f32 v33,v33,v97
M 18,a[132:135],54
s_waitcnt vmcnt(0)
v_cvt_pk_bf16_f32 v106,v30,v31
v_cvt_pk_bf16_f32 v107,v32,v33
M 22,a[124:127],50
s_barrier
ds_read_b32 v7,v6 offset:3072
v_fma_f32 v34,-v78,v86,v34
M 22,a[136:139],54
R 42,1,16384
v_fma_f32 v35,-v78,v87,v35
M 26,a[128:131],50
buffer_store_dword v4,v3,s[36:39],s70 offen offset:0 sc0 sc1
v_fma_f32 v36,-v78,v88,v36
M 26,a[140:143],54
L 4
v_fma_f32 v37,-v78,v89,v37
v_mul_f32 v98,v34,v34
M 26,a[152:155],58
s_mov_b32 s70,0x7fff0000
R 46,1,17408
v_mul_f32 v99,v35,v35
M 26,a[164:167],62
v_mul_f32 v100,v36,v36
v_mul_f32 v101,v37,v37
M 22,a[148:151],58
L 2
v_cmp_eq_u32 vcc,s53,v7
v_fma_f32 v98,v80,v98,v79
M 22,a[160:163],62
R 50,1,18432
v_fma_f32 v99,v80,v99,v79
v_fma_f32 v100,v80,v100,v79
M 18,a[144:147],58
v_fma_f32 v101,v80,v101,v79
v_mul_f32 v98,v34,v98
M 18,a[156:159],62
s_cmp_eq_u32 vcc_lo,-1
s_nop 0
v_mul_f32 v99,v35,v99
s_cbranch_scc0 Lg3_X0_%=
M 18,a[168:171],66
s_add_u32 m0,s57,0
s_add_u32 s56,s55,0
s_add_u32 s58,s58,s61
R 54,1,19456
M 18,a[180:183],70
buffer_load_dword v114,v11,s[36:39],s73 offen offset:0 sc1
M 22,a[172:175],66
D 0,56,0
v_mul_f32 v100,v36,v100
M 22,a[184:187],70
s_add_u32 s54,s54,s46
R 58,1,20480
v_mul_f32 v101,v37,v101
M 26,a[176:179],66
s_add_u32 s73,s73,s64
v_exp_f32 v98,v98
M 26,a[188:191],70
L 3
v_exp_f32 v99,v99
M 26,a[200:203],42
R 62,1,21504
v_fma_f32 v98,v98,v81,v81
M 26,a[212:215],46
D 0,56,1024
v_fma_f32 v99,v99,v81,v81
M 22,a[196:199],42
v_exp_f32 v100,v100
M 22,a[208:211],46
R 66,1,22528
v_fma_f32 v100,v100,v81,v81
M 18,a[192:195],42
D 0,56,2048
M 18,a[204:207],46
L 3
v_exp_f32 v101,v101
M 18,a[216:219],50
R 70,1,23552
v_rcp_f32 v98,v98
M 18,a[228:231],54
v_rcp_f32 v99,v99
M 22,a[220:223],50
D 0,56,3072
M 22,a[232:235],54
s_add_u32 m0,s59,0
R 42,1,24576
v_fma_f32 v101,v101,v81,v81
M 26,a[224:227],50
buffer_load_dword v8,s[36:39],s58 offen offset:0 sc1 lds
M 26,a[236:239],54
L 3
v_rcp_f32 v100,v100
v_mul_f32 v34,v34,v98
M 26,a[248:251],58
s_add_u32 m0,s57,16384
R 46,1,25600
v_mul_f32 v35,v35,v99
M 26,v[132:135],62
s_add_u32 s56,s55,16384
v_rcp_f32 v101,v101
M 22,a[244:247],58
D 0,56,0
M 22,v[128:131],62
s_add_u32 s55,s55,s60
R 50,1,26624
v_mul_f32 v36,v36,v100
M 18,a[240:243],58
s_sub_u32 s57,s84,s57
v_mul_f32 v37,v37,v101
v_cvt_pk_bf16_f32 v108,v34,v35
v_cvt_pk_bf16_f32 v109,v36,v37
M 18,a[252:255],62
L 3
buffer_store_dwordx4 v[106:109],v0,s[36:39],s67 offen offset:0 sc0 sc1
v_fma_f32 v38,-v78,v90,v38
M 18,v[136:139],66
R 54,1,27648
v_fma_f32 v39,-v78,v91,v39
M 18,v[148:151],70
v_fma_f32 v40,-v78,v92,v40
v_fma_f32 v41,-v78,v93,v41
M 22,v[140:143],66
D 0,56,1024
M 22,v[152:155],70
R 58,1,28672
v_mul_f32 v102,v38,v38
v_mul_f32 v103,v39,v39
M 26,v[144:147],66
v_mul_f32 v104,v40,v40
v_mul_f32 v105,v41,v41
M 26,v[156:159],70
L 3
v_fma_f32 v102,v80,v102,v79
v_fma_f32 v103,v80,v103,v79
M 26,v[168:171],42
R 62,1,29696
v_fma_f32 v104,v80,v104,v79
M 26,v[180:183],46
D 0,56,2048
M 22,v[164:167],42
v_fma_f32 v105,v80,v105,v79
v_mul_f32 v102,v38,v102
v_mul_f32 v103,v39,v103
M 22,v[176:179],46
R 66,1,30720
v_mul_f32 v104,v40,v104
M 18,v[160:163],42
v_exp_f32 v102,v102
M 18,v[172:175],46
L 3
D 0,56,3072
M 18,v[184:187],50
s_nop 0
R 70,1,31744
v_mul_f32 v105,v41,v105
M 18,v[196:199],54
s_waitcnt vmcnt(5)
v_exp_f32 v103,v103
v_fma_f32 v102,v102,v81,v81
M 22,v[188:191],50
s_barrier
buffer_store_dword v4,v3,s[36:39],s70 offen offset:0 sc0 sc1
M 22,v[200:203],54
s_mov_b32 s70,s69
R 42,2,0
ds_read_b32 v7,v6 offset:0
M 26,v[192:195],50
s_mov_b32 s69,s68
R 74,13,0
v_fma_f32 v103,v103,v81,v81
M 26,v[204:207],54
L 5
v_exp_f32 v104,v104
M 26,v[216:219],58
s_add_u32 s68,s68,s63
R 46,2,1024
v_exp_f32 v105,v105
M 26,v[228:231],62
v_rcp_f32 v102,v102
M 22,v[212:215],58
v_rcp_f32 v103,v103
M 22,v[224:227],62
L 2
R 50,2,2048
v_fma_f32 v104,v104,v81,v81
M 18,v[208:211],58
v_rcp_f32 v104,v104
M 18,v[220:223],62
s_nop 0
v_fma_f32 v105,v105,v81,v81
v_rcp_f32 v105,v105
M 18,v[232:235],66
R 54,2,3072
v_mul_f32 v38,v38,v102
M 18,v[244:247],70
v_mul_f32 v39,v39,v103
v_mul_f32 v40,v40,v104
M 22,v[236:239],66
v_cmp_eq_u32 vcc,s53,v7
v_mul_f32 v41,v41,v105
M 22,v[248:251],70
R 58,2,4096
v_cvt_pk_bf16_f32 v110,v38,v39
v_cvt_pk_bf16_f32 v111,v40,v41
M 26,v[240:243],66
L 3
s_cmp_eq_u32 vcc_lo,-1
buffer_store_dwordx2 v[110:111],v10,s[36:39],s67 offen offset:0 sc0 sc1
M 26,v[252:255],70
buffer_store_dword v4,v3,s[36:39],s71 offen offset:0 sc0 sc1
s_cbranch_scc1 Lg3_F1_%=
s_branch Lg3_L1_%=
Lg3_X0_%=:
M 18,a[168:171],66
s_add_u32 m0,s59,0
R 54,1,19456
v_mul_f32 v100,v36,v100
M 18,a[180:183],70
buffer_load_dword v8,s[36:39],s58 offen offset:0 sc1 lds
M 22,a[172:175],66
v_exp_f32 v98,v98
M 22,a[184:187],70
R 58,1,20480
v_mul_f32 v101,v37,v101
M 26,a[176:179],66
v_exp_f32 v99,v99
M 26,a[188:191],70
L 3
v_exp_f32 v100,v100
M 26,a[200:203],42
R 62,1,21504
v_fma_f32 v98,v98,v81,v81
M 26,a[212:215],46
v_exp_f32 v101,v101
M 22,a[196:199],42
v_rcp_f32 v98,v98
M 22,a[208:211],46
R 66,1,22528
v_fma_f32 v99,v99,v81,v81
M 18,a[192:195],42
v_rcp_f32 v99,v99
M 18,a[204:207],46
L 3
v_fma_f32 v100,v100,v81,v81
v_fma_f32 v101,v101,v81,v81
M 18,a[216:219],50
R 70,1,23552
v_mul_f32 v34,v34,v98
M 18,a[228:231],54
v_rcp_f32 v100,v100
M 22,a[220:223],50
v_rcp_f32 v101,v101
M 22,a[232:235],54
R 42,1,24576
v_mul_f32 v35,v35,v99
M 26,a[224:227],50
v_mul_f32 v36,v36,v100
v_mul_f32 v37,v37,v101
M 26,a[236:239],54
L 3
v_cvt_pk_bf16_f32 v108,v34,v35
v_cvt_pk_bf16_f32 v109,v36,v37
M 26,a[248:251],58
R 46,1,25600
v_fma_f32 v38,-v78,v90,v38
M 26,v[132:135],62
buffer_store_dwordx4 v[106:109],v0,s[36:39],s67 offen offset:0 sc0 sc1
v_fma_f32 v39,-v78,v91,v39
M 22,a[244:247],58
v_fma_f32 v40,-v78,v92,v40
v_fma_f32 v41,-v78,v93,v41
M 22,v[128:131],62
R 50,1,26624
v_mul_f32 v102,v38,v38
M 18,a[240:243],58
v_mul_f32 v103,v39,v39
v_mul_f32 v104,v40,v40
M 18,a[252:255],62
L 3
v_mul_f32 v105,v41,v41
v_fma_f32 v102,v80,v102,v79
M 18,v[136:139],66
R 54,1,27648
v_fma_f32 v103,v80,v103,v79
M 18,v[148:151],70
v_fma_f32 v104,v80,v104,v79
v_fma_f32 v105,v80,v105,v79
M 22,v[140:143],66
v_mul_f32 v102,v38,v102
v_mul_f32 v103,v39,v103
M 22,v[152:155],70
R 58,1,28672
v_mul_f32 v104,v40,v104
M 26,v[144:147],66
v_exp_f32 v102,v102
M 26,v[156:159],70
L 3
v_exp_f32 v103,v103
M 26,v[168:171],42
R 62,1,29696
v_mul_f32 v105,v41,v105
M 26,v[180:183],46
v_exp_f32 v104,v104
M 22,v[164:167],42
v_exp_f32 v105,v105
M 22,v[176:179],46
R 66,1,30720
v_fma_f32 v102,v102,v81,v81
M 18,v[160:163],42
v_rcp_f32 v102,v102
M 18,v[172:175],46
L 3
v_fma_f32 v103,v103,v81,v81
v_fma_f32 v104,v104,v81,v81
M 18,v[184:187],50
s_nop 0
R 70,1,31744
v_fma_f32 v105,v105,v81,v81
M 18,v[196:199],54
s_waitcnt vmcnt(1)
v_rcp_f32 v103,v103
M 22,v[188:191],50
s_barrier
buffer_store_dword v4,v3,s[36:39],s70 offen offset:0 sc0 sc1
M 22,v[200:203],54
s_mov_b32 s70,s69
ds_read_b32 v7,v6 offset:0
R 74,13,0
M 26,v[192:195],50
s_mov_b32 s69,s68
v_rcp_f32 v104,v104
M 26,v[204:207],54
L 4
v_rcp_f32 v105,v105
M 26,v[216:219],58
s_add_u32 s68,s68,s63
v_mul_f32 v38,v38,v102
v_mul_f32 v39,v39,v103
M 26,v[228:231],62
v_mul_f32 v40,v40,v104
v_mul_f32 v41,v41,v105
M 22,v[212:215],58
v_cvt_pk_bf16_f32 v110,v38,v39
v_cvt_pk_bf16_f32 v111,v40,v41
M 22,v[224:227],62
L 1
buffer_store_dwordx2 v[110:111],v10,s[36:39],s67 offen offset:0 sc0 sc1
M 18,v[208:211],58
v_cmp_eq_u32 vcc,s53,v7
M 18,v[220:223],62
s_nop 0
M 18,v[232:235],66
M 18,v[244:247],70
M 22,v[236:239],66
M 22,v[248:251],70
M 26,v[240:243],66
M 26,v[252:255],70
s_mov_b32 s80,1
s_cmp_eq_u32 vcc_lo,-1
s_cbranch_scc0 Lg3_xn0_%=
buffer_load_dword v114,v11,s[36:39],s73 offen offset:0 sc1
L 0
v_fma_f32 v18,-v74,v82,v18
v_fma_f32 v19,-v74,v83,v19
v_fma_f32 v20,-v74,v84,v20
v_fma_f32 v21,-v74,v85,v21
v_mul_f32 v94,v18,v18
v_mul_f32 v95,v19,v19
v_mul_f32 v96,v20,v20
v_mul_f32 v97,v21,v21
s_add_u32 s73,s73,s64
s_add_u32 m0,s57,0
s_add_u32 s56,s55,0
D 0,56,0
v_fma_f32 v94,v76,v94,v75
v_fma_f32 v95,v76,v95,v75
v_fma_f32 v96,v76,v96,v75
v_fma_f32 v97,v76,v97,v75
v_mul_f32 v94,v18,v94
v_mul_f32 v95,v19,v95
v_mul_f32 v96,v20,v96
v_mul_f32 v97,v21,v97
D 0,56,1024
v_exp_f32 v94,v94
v_exp_f32 v95,v95
v_exp_f32 v96,v96
v_exp_f32 v97,v97
v_fma_f32 v94,v94,v77,v77
v_fma_f32 v95,v95,v77,v77
v_fma_f32 v96,v96,v77,v77
v_fma_f32 v97,v97,v77,v77
D 0,56,2048
v_rcp_f32 v94,v94
v_rcp_f32 v95,v95
v_rcp_f32 v96,v96
v_rcp_f32 v97,v97
v_mul_f32 v18,v18,v94
)"
R"(v_mul_f32 v19,v19,v95
v_mul_f32 v20,v20,v96
v_mul_f32 v21,v21,v97
D 0,56,3072
v_cvt_pk_bf16_f32 v106,v18,v19
v_cvt_pk_bf16_f32 v107,v20,v21
v_fma_f32 v22,-v74,v86,v22
v_fma_f32 v23,-v74,v87,v23
v_fma_f32 v24,-v74,v88,v24
v_fma_f32 v25,-v74,v89,v25
v_mul_f32 v98,v22,v22
v_mul_f32 v99,v23,v23
s_add_u32 s58,s58,s61
s_add_u32 m0,s57,16384
s_add_u32 s56,s55,16384
D 0,56,0
v_mul_f32 v100,v24,v24
v_mul_f32 v101,v25,v25
v_fma_f32 v98,v76,v98,v75
v_fma_f32 v99,v76,v99,v75
v_fma_f32 v100,v76,v100,v75
v_fma_f32 v101,v76,v101,v75
v_mul_f32 v98,v22,v98
v_mul_f32 v99,v23,v99
D 0,56,1024
v_mul_f32 v100,v24,v100
v_mul_f32 v101,v25,v101
v_exp_f32 v98,v98
v_exp_f32 v99,v99
v_exp_f32 v100,v100
v_exp_f32 v101,v101
v_fma_f32 v98,v98,v77,v77
v_fma_f32 v99,v99,v77,v77
D 0,56,2048
v_fma_f32 v100,v100,v77,v77
v_fma_f32 v101,v101,v77,v77
v_rcp_f32 v98,v98
v_rcp_f32 v99,v99
v_rcp_f32 v100,v100
v_rcp_f32 v101,v101
v_mul_f32 v22,v22,v98
v_mul_f32 v23,v23,v99
D 0,56,3072
v_mul_f32 v24,v24,v100
v_mul_f32 v25,v25,v101
v_cvt_pk_bf16_f32 v108,v22,v23
v_cvt_pk_bf16_f32 v109,v24,v25
buffer_store_dwordx4 v[106:109],v0,s[36:39],s66 offen offset:0 sc0 sc1
v_fma_f32 v26,-v74,v90,v26
v_fma_f32 v27,-v74,v91,v27
v_fma_f32 v28,-v74,v92,v28
v_fma_f32 v29,-v74,v93,v29
s_add_u32 s55,s55,s60
s_sub_u32 s57,s84,s57
s_add_u32 s54,s54,s46
v_mul_f32 v102,v26,v26
v_mul_f32 v103,v27,v27
v_mul_f32 v104,v28,v28
v_mul_f32 v105,v29,v29
v_fma_f32 v102,v76,v102,v75
v_fma_f32 v103,v76,v103,v75
v_fma_f32 v104,v76,v104,v75
v_fma_f32 v105,v76,v105,v75
v_mul_f32 v102,v26,v102
v_mul_f32 v103,v27,v103
v_mul_f32 v104,v28,v104
v_mul_f32 v105,v29,v105
v_exp_f32 v102,v102
v_exp_f32 v103,v103
v_exp_f32 v104,v104
v_exp_f32 v105,v105
v_fma_f32 v102,v102,v77,v77
v_fma_f32 v103,v103,v77,v77
v_fma_f32 v104,v104,v77,v77
v_fma_f32 v105,v105,v77,v77
v_rcp_f32 v102,v102
v_rcp_f32 v103,v103
v_rcp_f32 v104,v104
v_rcp_f32 v105,v105
v_mul_f32 v26,v26,v102
v_mul_f32 v27,v27,v103
v_mul_f32 v28,v28,v104
v_mul_f32 v29,v29,v105
v_cvt_pk_bf16_f32 v110,v26,v27
v_cvt_pk_bf16_f32 v111,v28,v29
buffer_store_dwordx2 v[110:111],v10,s[36:39],s66 offen offset:0 sc0 sc1
s_mov_b32 s72,s69
s_mov_b32 s69,0x7fff0000
s_mov_b32 s66,0x7fff0000
s_mov_b32 s76,0x7fff0000
s_branch Lg3_gox_%=
Lg3_xn0_%=:
s_cmp_ge_u32 s54,256
s_cbranch_scc1 Lg3_fin0_%=
s_add_u32 m0,s59,4096
s_nop 0
buffer_load_dword v8,s[36:39],s58 offen offset:0 sc1 lds
buffer_store_dword v4,v3,s[36:39],s71 offen offset:0 sc0 sc1
s_add_u32 m0,s59,5120
s_nop 0
buffer_load_dword v8,s[36:39],s58 offen offset:0 sc1 lds
buffer_store_dword v4,v3,s[36:39],s71 offen offset:0 sc0 sc1
s_mov_b32 s83,0
L 0
v_fma_f32 v18,-v74,v82,v18
v_fma_f32 v19,-v74,v83,v19
v_fma_f32 v20,-v74,v84,v20
v_fma_f32 v21,-v74,v85,v21
v_mul_f32 v94,v18,v18
v_mul_f32 v95,v19,v19
v_mul_f32 v96,v20,v20
v_mul_f32 v97,v21,v21
v_fma_f32 v94,v76,v94,v75
v_fma_f32 v95,v76,v95,v75
v_fma_f32 v96,v76,v96,v75
v_fma_f32 v97,v76,v97,v75
v_mul_f32 v94,v18,v94
v_mul_f32 v95,v19,v95
v_mul_f32 v96,v20,v96
v_mul_f32 v97,v21,v97
v_exp_f32 v94,v94
v_exp_f32 v95,v95
v_exp_f32 v96,v96
v_exp_f32 v97,v97
v_fma_f32 v94,v94,v77,v77
v_fma_f32 v95,v95,v77,v77
v_fma_f32 v96,v96,v77,v77
v_fma_f32 v97,v97,v77,v77
v_rcp_f32 v94,v94
v_rcp_f32 v95,v95
v_rcp_f32 v96,v96
v_rcp_f32 v97,v97
v_mul_f32 v18,v18,v94
v_mul_f32 v19,v19,v95
v_mul_f32 v20,v20,v96
v_mul_f32 v21,v21,v97
v_cvt_pk_bf16_f32 v106,v18,v19
v_cvt_pk_bf16_f32 v107,v20,v21
v_fma_f32 v22,-v74,v86,v22
v_fma_f32 v23,-v74,v87,v23
v_fma_f32 v24,-v74,v88,v24
v_fma_f32 v25,-v74,v89,v25
v_mul_f32 v98,v22,v22
v_mul_f32 v99,v23,v23
v_mul_f32 v100,v24,v24
v_mul_f32 v101,v25,v25
v_fma_f32 v98,v76,v98,v75
v_fma_f32 v99,v76,v99,v75
v_fma_f32 v100,v76,v100,v75
v_fma_f32 v101,v76,v101,v75
v_mul_f32 v98,v22,v98
v_mul_f32 v99,v23,v99
v_mul_f32 v100,v24,v100
v_mul_f32 v101,v25,v101
v_exp_f32 v98,v98
v_exp_f32 v99,v99
v_exp_f32 v100,v100
v_exp_f32 v101,v101
v_fma_f32 v98,v98,v77,v77
v_fma_f32 v99,v99,v77,v77
v_fma_f32 v100,v100,v77,v77
v_fma_f32 v101,v101,v77,v77
v_rcp_f32 v98,v98
v_rcp_f32 v99,v99
v_rcp_f32 v100,v100
v_rcp_f32 v101,v101
v_mul_f32 v22,v22,v98
v_mul_f32 v23,v23,v99
v_mul_f32 v24,v24,v100
v_mul_f32 v25,v25,v101
v_cvt_pk_bf16_f32 v108,v22,v23
v_cvt_pk_bf16_f32 v109,v24,v25
buffer_store_dwordx4 v[106:109],v0,s[36:39],s66 offen offset:0 sc0 sc1
v_fma_f32 v26,-v74,v90,v26
v_fma_f32 v27,-v74,v91,v27
v_fma_f32 v28,-v74,v92,v28
v_fma_f32 v29,-v74,v93,v29
v_mul_f32 v102,v26,v26
v_mul_f32 v103,v27,v27
v_mul_f32 v104,v28,v28
v_mul_f32 v105,v29,v29
v_fma_f32 v102,v76,v102,v75
v_fma_f32 v103,v76,v103,v75
v_fma_f32 v104,v76,v104,v75
v_fma_f32 v105,v76,v105,v75
v_mul_f32 v102,v26,v102
v_mul_f32 v103,v27,v103
v_mul_f32 v104,v28,v104
v_mul_f32 v105,v29,v105
v_exp_f32 v102,v102
v_exp_f32 v103,v103
v_exp_f32 v104,v104
v_exp_f32 v105,v105
v_fma_f32 v102,v102,v77,v77
v_fma_f32 v103,v103,v77,v77
v_fma_f32 v104,v104,v77,v77
v_fma_f32 v105,v105,v77,v77
v_rcp_f32 v102,v102
v_rcp_f32 v103,v103
v_rcp_f32 v104,v104
v_rcp_f32 v105,v105
v_mul_f32 v26,v26,v102
v_mul_f32 v27,v27,v103
v_mul_f32 v28,v28,v104
v_mul_f32 v29,v29,v105
v_cvt_pk_bf16_f32 v110,v26,v27
v_cvt_pk_bf16_f32 v111,v28,v29
buffer_store_dwordx2 v[110:111],v10,s[36:39],s66 offen offset:0 sc0 sc1
s_mov_b32 s72,s69
s_mov_b32 s69,0x7fff0000
s_mov_b32 s66,0x7fff0000
s_mov_b32 s76,0x7fff0000
s_branch Lg3_poll_%=
Lg3_fin0_%=:
L 0
v_fma_f32 v18,-v74,v82,v18
v_fma_f32 v19,-v74,v83,v19
v_fma_f32 v20,-v74,v84,v20
v_fma_f32 v21,-v74,v85,v21
v_mul_f32 v94,v18,v18
v_mul_f32 v95,v19,v19
v_mul_f32 v96,v20,v20
v_mul_f32 v97,v21,v21
v_fma_f32 v94,v76,v94,v75
v_fma_f32 v95,v76,v95,v75
v_fma_f32 v96,v76,v96,v75
v_fma_f32 v97,v76,v97,v75
v_mul_f32 v94,v18,v94
v_mul_f32 v95,v19,v95
v_mul_f32 v96,v20,v96
v_mul_f32 v97,v21,v97
v_exp_f32 v94,v94
v_exp_f32 v95,v95
v_exp_f32 v96,v96
v_exp_f32 v97,v97
v_fma_f32 v94,v94,v77,v77
v_fma_f32 v95,v95,v77,v77
v_fma_f32 v96,v96,v77,v77
v_fma_f32 v97,v97,v77,v77
v_rcp_f32 v94,v94
v_rcp_f32 v95,v95
v_rcp_f32 v96,v96
v_rcp_f32 v97,v97
v_mul_f32 v18,v18,v94
v_mul_f32 v19,v19,v95
v_mul_f32 v20,v20,v96
v_mul_f32 v21,v21,v97
v_cvt_pk_bf16_f32 v106,v18,v19
v_cvt_pk_bf16_f32 v107,v20,v21
v_fma_f32 v22,-v74,v86,v22
v_fma_f32 v23,-v74,v87,v23
v_fma_f32 v24,-v74,v88,v24
v_fma_f32 v25,-v74,v89,v25
v_mul_f32 v98,v22,v22
v_mul_f32 v99,v23,v23
v_mul_f32 v100,v24,v24
v_mul_f32 v101,v25,v25
v_fma_f32 v98,v76,v98,v75
v_fma_f32 v99,v76,v99,v75
v_fma_f32 v100,v76,v100,v75
v_fma_f32 v101,v76,v101,v75
v_mul_f32 v98,v22,v98
v_mul_f32 v99,v23,v99
v_mul_f32 v100,v24,v100
v_mul_f32 v101,v25,v101
v_exp_f32 v98,v98
v_exp_f32 v99,v99
v_exp_f32 v100,v100
v_exp_f32 v101,v101
v_fma_f32 v98,v98,v77,v77
v_fma_f32 v99,v99,v77,v77
v_fma_f32 v100,v100,v77,v77
v_fma_f32 v101,v101,v77,v77
v_rcp_f32 v98,v98
v_rcp_f32 v99,v99
v_rcp_f32 v100,v100
v_rcp_f32 v101,v101
v_mul_f32 v22,v22,v98
v_mul_f32 v23,v23,v99
v_mul_f32 v24,v24,v100
v_mul_f32 v25,v25,v101
v_cvt_pk_bf16_f32 v108,v22,v23
v_cvt_pk_bf16_f32 v109,v24,v25
buffer_store_dwordx4 v[106:109],v0,s[36:39],s66 offen offset:0 sc0 sc1
v_fma_f32 v26,-v74,v90,v26
v_fma_f32 v27,-v74,v91,v27
v_fma_f32 v28,-v74,v92,v28
v_fma_f32 v29,-v74,v93,v29
v_mul_f32 v102,v26,v26
v_mul_f32 v103,v27,v27
v_mul_f32 v104,v28,v28
v_mul_f32 v105,v29,v29
v_fma_f32 v102,v76,v102,v75
v_fma_f32 v103,v76,v103,v75
v_fma_f32 v104,v76,v104,v75
v_fma_f32 v105,v76,v105,v75
v_mul_f32 v102,v26,v102
v_mul_f32 v103,v27,v103
v_mul_f32 v104,v28,v104
v_mul_f32 v105,v29,v105
v_exp_f32 v102,v102
v_exp_f32 v103,v103
v_exp_f32 v104,v104
v_exp_f32 v105,v105
v_fma_f32 v102,v102,v77,v77
v_fma_f32 v103,v103,v77,v77
v_fma_f32 v104,v104,v77,v77
v_fma_f32 v105,v105,v77,v77
v_rcp_f32 v102,v102
v_rcp_f32 v103,v103
v_rcp_f32 v104,v104
v_rcp_f32 v105,v105
v_mul_f32 v26,v26,v102
v_mul_f32 v27,v27,v103
v_mul_f32 v28,v28,v104
v_mul_f32 v29,v29,v105
v_cvt_pk_bf16_f32 v110,v26,v27
v_cvt_pk_bf16_f32 v111,v28,v29
buffer_store_dwordx2 v[110:111],v10,s[36:39],s66 offen offset:0 sc0 sc1
s_mov_b32 s72,s69
s_mov_b32 s69,0x7fff0000
s_mov_b32 s66,0x7fff0000
s_mov_b32 s76,0x7fff0000
s_branch Lg3_done_%=
Lg3_L1_%=:
v_mfma_f32_16x16x32_bf16 v[38:41],a[8:11],v[42:45],0
s_add_u32 m0,s59,3072
R 62,2,5120
v_add_f32_dpp v115,v114,v114 quad_perm:[1,0,3,2] row_mask:0xf bank_mask:0xf
M 38,a[20:23],46
buffer_load_dword v8,s[36:39],s58 offen offset:0 sc1 lds
v_mfma_f32_16x16x32_bf16 v[34:37],a[4:7],v[42:45],0
s_mov_b32 s67,s65
v_add_f32_dpp v115,v115,v115 quad_perm:[2,3,0,1] row_mask:0xf bank_mask:0xf
v_fma_f32 v18,-v74,v82,v18
M 34,a[16:19],46
s_add_u32 s65,s65,s62
R 66,2,6144
v_fma_f32 v19,-v74,v83,v19
v_mfma_f32_16x16x32_bf16 v[30:33],a[0:3],v[42:45],0
v_add_f32_dpp v118,v115,v115 row_half_mirror row_mask:0xf bank_mask:0xf
v_fma_f32 v20,-v74,v84,v20
v_fma_f32 v21,-v74,v85,v21
M 30,a[12:15],46
L 3
v_mul_f32 v94,v18,v18
v_mul_f32 v95,v19,v19
M 30,a[24:27],50
R 70,2,7168
v_mov_b32_dpp v115,v118 row_ror:8 row_mask:0xf bank_mask:0xf
M 30,a[36:39],54
v_fma_f32 v115,-v118,v118,v115
v_max_f32 v115,s87,v115
M 34,a[28:31],50
v_rsq_f32 v116,v115
M 34,a[40:43],54
R 42,2,8192
v_fma_f32 v115,v15,v115,v16
v_mul_f32 v118,v17,v118
M 38,a[32:35],50
v_mul_f32 v119,v116,v116
v_fma_f32 v115,v14,v119,v115
M 38,a[44:47],54
L 3
v_fma_f32 v118,v116,v115,v118
v_mul_f32 v96,v20,v20
M 38,a[56:59],58
R 46,2,9216
v_mul_f32 v97,v21,v21
M 38,a[68:71],62
ds_write_b32 v12,v118 offset:2048
M 34,a[52:55],58
v_fma_f32 v94,v76,v94,v75
v_fma_f32 v95,v76,v95,v75
v_fma_f32 v96,v76,v96,v75
M 34,a[64:67],62
R 50,2,10240
v_fma_f32 v97,v76,v97,v75
M 30,a[48:51],58
v_mul_f32 v94,v18,v94
v_mul_f32 v95,v19,v95
M 30,a[60:63],62
L 4
v_exp_f32 v94,v94
M 30,a[72:75],66
R 54,2,11264
v_mul_f32 v96,v20,v96
M 30,a[84:87],70
v_exp_f32 v95,v95
v_mul_f32 v97,v21,v97
M 34,a[76:79],66
v_exp_f32 v96,v96
M 34,a[88:91],70
R 58,2,12288
v_fma_f32 v94,v94,v77,v77
M 38,a[80:83],66
v_exp_f32 v97,v97
M 38,a[92:95],70
L 4
v_rcp_f32 v94,v94
M 38,a[104:107],42
R 62,2,13312
v_fma_f32 v95,v95,v77,v77
v_fma_f32 v96,v96,v77,v77
M 38,a[116:119],46
v_rcp_f32 v95,v95
M 34,a[100:103],42
v_rcp_f32 v96,v96
M 34,a[112:115],46
R 66,2,14336
v_fma_f32 v97,v97,v77,v77
M 30,a[96:99],42
v_rcp_f32 v97,v97
M 30,a[108:111],46
L 3
v_mul_f32 v18,v18,v94
v_mul_f32 v19,v19,v95
v_mul_f32 v20,v20,v96
M 30,a[120:123],50
R 70,2,15360
v_mul_f32 v21,v21,v97
M 30,a[132:135],54
s_waitcnt vmcnt(0)
v_cvt_pk_bf16_f32 v106,v18,v19
v_cvt_pk_bf16_f32 v107,v20,v21
M 34,a[124:127],50
s_barrier
ds_read_b32 v7,v6 offset:3072
v_fma_f32 v22,-v74,v86,v22
M 34,a[136:139],54
R 42,2,16384
v_fma_f32 v23,-v74,v87,v23
M 38,a[128:131],50
buffer_store_dword v4,v3,s[36:39],s70 offen offset:0 sc0 sc1
v_fma_f32 v24,-v74,v88,v24
M 38,a[140:143],54
L 4
v_fma_f32 v25,-v74,v89,v25
v_mul_f32 v98,v22,v22
M 38,a[152:155],58
s_mov_b32 s70,0x7fff0000
R 46,2,17408
v_mul_f32 v99,v23,v23
M 38,a[164:167],62
v_mul_f32 v100,v24,v24
v_mul_f32 v101,v25,v25
M 34,a[148:151],58
L 2
v_cmp_eq_u32 vcc,s53,v7
v_fma_f32 v98,v76,v98,v75
M 34,a[160:163],62
R 50,2,18432
v_fma_f32 v99,v76,v99,v75
v_fma_f32 v100,v76,v100,v75
M 30,a[144:147],58
v_fma_f32 v101,v76,v101,v75
v_mul_f32 v98,v22,v98
M 30,a[156:159],62
s_cmp_eq_u32 vcc_lo,-1
s_nop 0
v_mul_f32 v99,v23,v99
s_cbranch_scc0 Lg3_X1_%=
M 30,a[168:171],66
s_add_u32 m0,s57,0
s_add_u32 s56,s55,0
s_add_u32 s58,s58,s61
R 54,2,19456
M 30,a[180:183],70
buffer_load_dword v114,v11,s[36:39],s73 offen offset:0 sc1
M 34,a[172:175],66
D 0,56,0
v_mul_f32 v100,v24,v100
M 34,a[184:187],70
s_add_u32 s54,s54,s46
R 58,2,20480
v_mul_f32 v101,v25,v101
M 38,a[176:179],66
s_add_u32 s73,s73,s64
v_exp_f32 v98,v98
M 38,a[188:191],70
L 3
v_exp_f32 v99,v99
M 38,a[200:203],42
R 62,2,21504
v_fma_f32 v98,v98,v77,v77
M 38,a[212:215],46
D 0,56,1024
v_fma_f32 v99,v99,v77,v77
M 34,a[196:199],42
v_exp_f32 v100,v100
M 34,a[208:211],46
R 66,2,22528
v_fma_f32 v100,v100,v77,v77
M 30,a[192:195],42
D 0,56,2048
M 30,a[204:207],46
L 3
v_exp_f32 v101,v101
M 30,a[216:219],50
R 70,2,23552
)"
R"(v_rcp_f32 v98,v98
M 30,a[228:231],54
v_rcp_f32 v99,v99
M 34,a[220:223],50
D 0,56,3072
M 34,a[232:235],54
s_add_u32 m0,s59,1024
R 42,2,24576
v_fma_f32 v101,v101,v77,v77
M 38,a[224:227],50
buffer_load_dword v8,s[36:39],s58 offen offset:0 sc1 lds
M 38,a[236:239],54
L 3
v_rcp_f32 v100,v100
v_mul_f32 v22,v22,v98
M 38,a[248:251],58
s_add_u32 m0,s57,16384
R 46,2,25600
v_mul_f32 v23,v23,v99
M 38,v[132:135],62
s_add_u32 s56,s55,16384
v_rcp_f32 v101,v101
M 34,a[244:247],58
D 0,56,0
M 34,v[128:131],62
s_add_u32 s55,s55,s60
R 50,2,26624
v_mul_f32 v24,v24,v100
M 30,a[240:243],58
s_sub_u32 s57,s84,s57
v_mul_f32 v25,v25,v101
v_cvt_pk_bf16_f32 v108,v22,v23
v_cvt_pk_bf16_f32 v109,v24,v25
M 30,a[252:255],62
L 3
buffer_store_dwordx4 v[106:109],v0,s[36:39],s66 offen offset:0 sc0 sc1
v_fma_f32 v26,-v74,v90,v26
M 30,v[136:139],66
R 54,2,27648
v_fma_f32 v27,-v74,v91,v27
M 30,v[148:151],70
v_fma_f32 v28,-v74,v92,v28
v_fma_f32 v29,-v74,v93,v29
M 34,v[140:143],66
D 0,56,1024
M 34,v[152:155],70
R 58,2,28672
v_mul_f32 v102,v26,v26
v_mul_f32 v103,v27,v27
M 38,v[144:147],66
v_mul_f32 v104,v28,v28
v_mul_f32 v105,v29,v29
M 38,v[156:159],70
L 3
v_fma_f32 v102,v76,v102,v75
v_fma_f32 v103,v76,v103,v75
M 38,v[168:171],42
R 62,2,29696
v_fma_f32 v104,v76,v104,v75
M 38,v[180:183],46
D 0,56,2048
M 34,v[164:167],42
v_fma_f32 v105,v76,v105,v75
v_mul_f32 v102,v26,v102
v_mul_f32 v103,v27,v103
M 34,v[176:179],46
R 66,2,30720
v_mul_f32 v104,v28,v104
M 30,v[160:163],42
v_exp_f32 v102,v102
M 30,v[172:175],46
L 3
D 0,56,3072
M 30,v[184:187],50
s_nop 0
R 70,2,31744
v_mul_f32 v105,v29,v105
M 30,v[196:199],54
s_waitcnt vmcnt(5)
v_exp_f32 v103,v103
v_fma_f32 v102,v102,v77,v77
M 34,v[188:191],50
s_barrier
buffer_store_dword v4,v3,s[36:39],s70 offen offset:0 sc0 sc1
M 34,v[200:203],54
s_mov_b32 s70,s69
R 42,1,0
ds_read_b32 v7,v6 offset:1024
M 38,v[192:195],50
s_mov_b32 s69,s68
R 78,13,2048
v_fma_f32 v103,v103,v77,v77
M 38,v[204:207],54
L 5
v_exp_f32 v104,v104
M 38,v[216:219],58
s_add_u32 s68,s68,s63
R 46,1,1024
v_exp_f32 v105,v105
M 38,v[228:231],62
v_rcp_f32 v102,v102
M 34,v[212:215],58
v_rcp_f32 v103,v103
M 34,v[224:227],62
L 2
R 50,1,2048
v_fma_f32 v104,v104,v77,v77
M 30,v[208:211],58
v_rcp_f32 v104,v104
M 30,v[220:223],62
s_nop 0
v_fma_f32 v105,v105,v77,v77
v_rcp_f32 v105,v105
M 30,v[232:235],66
R 54,1,3072
v_mul_f32 v26,v26,v102
M 30,v[244:247],70
v_mul_f32 v27,v27,v103
v_mul_f32 v28,v28,v104
M 34,v[236:239],66
v_cmp_eq_u32 vcc,s53,v7
v_mul_f32 v29,v29,v105
M 34,v[248:251],70
R 58,1,4096
v_cvt_pk_bf16_f32 v110,v26,v27
v_cvt_pk_bf16_f32 v111,v28,v29
M 38,v[240:243],66
L 3
s_cmp_eq_u32 vcc_lo,-1
buffer_store_dwordx2 v[110:111],v10,s[36:39],s66 offen offset:0 sc0 sc1
M 38,v[252:255],70
buffer_store_dword v4,v3,s[36:39],s71 offen offset:0 sc0 sc1
s_cbranch_scc1 Lg3_F0_%=
s_branch Lg3_L0_%=
Lg3_X1_%=:
M 30,a[168:171],66
s_add_u32 m0,s59,1024
R 54,2,19456
v_mul_f32 v100,v24,v100
M 30,a[180:183],70
buffer_load_dword v8,s[36:39],s58 offen offset:0 sc1 lds
M 34,a[172:175],66
v_exp_f32 v98,v98
M 34,a[184:187],70
R 58,2,20480
v_mul_f32 v101,v25,v101
M 38,a[176:179],66
v_exp_f32 v99,v99
M 38,a[188:191],70
L 3
v_exp_f32 v100,v100
M 38,a[200:203],42
R 62,2,21504
v_fma_f32 v98,v98,v77,v77
M 38,a[212:215],46
v_exp_f32 v101,v101
M 34,a[196:199],42
v_rcp_f32 v98,v98
M 34,a[208:211],46
R 66,2,22528
v_fma_f32 v99,v99,v77,v77
M 30,a[192:195],42
v_rcp_f32 v99,v99
M 30,a[204:207],46
L 3
v_fma_f32 v100,v100,v77,v77
v_fma_f32 v101,v101,v77,v77
M 30,a[216:219],50
R 70,2,23552
v_mul_f32 v22,v22,v98
M 30,a[228:231],54
v_rcp_f32 v100,v100
M 34,a[220:223],50
v_rcp_f32 v101,v101
M 34,a[232:235],54
R 42,2,24576
v_mul_f32 v23,v23,v99
M 38,a[224:227],50
v_mul_f32 v24,v24,v100
v_mul_f32 v25,v25,v101
M 38,a[236:239],54
L 3
v_cvt_pk_bf16_f32 v108,v22,v23
v_cvt_pk_bf16_f32 v109,v24,v25
M 38,a[248:251],58
R 46,2,25600
v_fma_f32 v26,-v74,v90,v26
M 38,v[132:135],62
buffer_store_dwordx4 v[106:109],v0,s[36:39],s66 offen offset:0 sc0 sc1
v_fma_f32 v27,-v74,v91,v27
M 34,a[244:247],58
v_fma_f32 v28,-v74,v92,v28
v_fma_f32 v29,-v74,v93,v29
M 34,v[128:131],62
R 50,2,26624
v_mul_f32 v102,v26,v26
M 30,a[240:243],58
v_mul_f32 v103,v27,v27
v_mul_f32 v104,v28,v28
M 30,a[252:255],62
L 3
v_mul_f32 v105,v29,v29
v_fma_f32 v102,v76,v102,v75
M 30,v[136:139],66
R 54,2,27648
v_fma_f32 v103,v76,v103,v75
M 30,v[148:151],70
v_fma_f32 v104,v76,v104,v75
v_fma_f32 v105,v76,v105,v75
M 34,v[140:143],66
v_mul_f32 v102,v26,v102
v_mul_f32 v103,v27,v103
M 34,v[152:155],70
R 58,2,28672
v_mul_f32 v104,v28,v104
M 38,v[144:147],66
v_exp_f32 v102,v102
M 38,v[156:159],70
L 3
v_exp_f32 v103,v103
M 38,v[168:171],42
R 62,2,29696
v_mul_f32 v105,v29,v105
M 38,v[180:183],46
v_exp_f32 v104,v104
M 34,v[164:167],42
v_exp_f32 v105,v105
M 34,v[176:179],46
R 66,2,30720
v_fma_f32 v102,v102,v77,v77
M 30,v[160:163],42
v_rcp_f32 v102,v102
M 30,v[172:175],46
L 3
v_fma_f32 v103,v103,v77,v77
v_fma_f32 v104,v104,v77,v77
M 30,v[184:187],50
s_nop 0
R 70,2,31744
v_fma_f32 v105,v105,v77,v77
M 30,v[196:199],54
s_waitcnt vmcnt(1)
v_rcp_f32 v103,v103
M 34,v[188:191],50
s_barrier
buffer_store_dword v4,v3,s[36:39],s70 offen offset:0 sc0 sc1
M 34,v[200:203],54
s_mov_b32 s70,s69
ds_read_b32 v7,v6 offset:1024
R 78,13,2048
M 38,v[192:195],50
s_mov_b32 s69,s68
v_rcp_f32 v104,v104
M 38,v[204:207],54
L 4
v_rcp_f32 v105,v105
M 38,v[216:219],58
s_add_u32 s68,s68,s63
v_mul_f32 v26,v26,v102
v_mul_f32 v27,v27,v103
M 38,v[228:231],62
v_mul_f32 v28,v28,v104
v_mul_f32 v29,v29,v105
M 34,v[212:215],58
v_cvt_pk_bf16_f32 v110,v26,v27
v_cvt_pk_bf16_f32 v111,v28,v29
M 34,v[224:227],62
L 1
buffer_store_dwordx2 v[110:111],v10,s[36:39],s66 offen offset:0 sc0 sc1
M 30,v[208:211],58
v_cmp_eq_u32 vcc,s53,v7
M 30,v[220:223],62
s_nop 0
M 30,v[232:235],66
M 30,v[244:247],70
M 34,v[236:239],66
M 34,v[248:251],70
M 38,v[240:243],66
M 38,v[252:255],70
s_mov_b32 s80,0
s_cmp_eq_u32 vcc_lo,-1
s_cbranch_scc0 Lg3_xn1_%=
buffer_load_dword v114,v11,s[36:39],s73 offen offset:0 sc1
L 0
v_fma_f32 v30,-v78,v82,v30
v_fma_f32 v31,-v78,v83,v31
v_fma_f32 v32,-v78,v84,v32
v_fma_f32 v33,-v78,v85,v33
v_mul_f32 v94,v30,v30
v_mul_f32 v95,v31,v31
v_mul_f32 v96,v32,v32
v_mul_f32 v97,v33,v33
s_add_u32 s73,s73,s64
s_add_u32 m0,s57,0
s_add_u32 s56,s55,0
D 0,56,0
v_fma_f32 v94,v80,v94,v79
v_fma_f32 v95,v80,v95,v79
v_fma_f32 v96,v80,v96,v79
v_fma_f32 v97,v80,v97,v79
v_mul_f32 v94,v30,v94
v_mul_f32 v95,v31,v95
v_mul_f32 v96,v32,v96
v_mul_f32 v97,v33,v97
D 0,56,1024
v_exp_f32 v94,v94
v_exp_f32 v95,v95
v_exp_f32 v96,v96
v_exp_f32 v97,v97
v_fma_f32 v94,v94,v81,v81
v_fma_f32 v95,v95,v81,v81
v_fma_f32 v96,v96,v81,v81
v_fma_f32 v97,v97,v81,v81
D 0,56,2048
v_rcp_f32 v94,v94
v_rcp_f32 v95,v95
v_rcp_f32 v96,v96
v_rcp_f32 v97,v97
v_mul_f32 v30,v30,v94
v_mul_f32 v31,v31,v95
v_mul_f32 v32,v32,v96
v_mul_f32 v33,v33,v97
D 0,56,3072
v_cvt_pk_bf16_f32 v106,v30,v31
v_cvt_pk_bf16_f32 v107,v32,v33
v_fma_f32 v34,-v78,v86,v34
v_fma_f32 v35,-v78,v87,v35
v_fma_f32 v36,-v78,v88,v36
v_fma_f32 v37,-v78,v89,v37
v_mul_f32 v98,v34,v34
v_mul_f32 v99,v35,v35
s_add_u32 s58,s58,s61
s_add_u32 m0,s57,16384
s_add_u32 s56,s55,16384
D 0,56,0
v_mul_f32 v100,v36,v36
v_mul_f32 v101,v37,v37
v_fma_f32 v98,v80,v98,v79
v_fma_f32 v99,v80,v99,v79
v_fma_f32 v100,v80,v100,v79
v_fma_f32 v101,v80,v101,v79
v_mul_f32 v98,v34,v98
v_mul_f32 v99,v35,v99
D 0,56,1024
v_mul_f32 v100,v36,v100
v_mul_f32 v101,v37,v101
v_exp_f32 v98,v98
v_exp_f32 v99,v99
v_exp_f32 v100,v100
v_exp_f32 v101,v101
v_fma_f32 v98,v98,v81,v81
v_fma_f32 v99,v99,v81,v81
D 0,56,2048
v_fma_f32 v100,v100,v81,v81
v_fma_f32 v101,v101,v81,v81
v_rcp_f32 v98,v98
v_rcp_f32 v99,v99
v_rcp_f32 v100,v100
v_rcp_f32 v101,v101
v_mul_f32 v34,v34,v98
v_mul_f32 v35,v35,v99
D 0,56,3072
v_mul_f32 v36,v36,v100
v_mul_f32 v37,v37,v101
v_cvt_pk_bf16_f32 v108,v34,v35
v_cvt_pk_bf16_f32 v109,v36,v37
buffer_store_dwordx4 v[106:109],v0,s[36:39],s67 offen offset:0 sc0 sc1
v_fma_f32 v38,-v78,v90,v38
v_fma_f32 v39,-v78,v91,v39
v_fma_f32 v40,-v78,v92,v40
v_fma_f32 v41,-v78,v93,v41
s_add_u32 s55,s55,s60
s_sub_u32 s57,s84,s57
s_add_u32 s54,s54,s46
v_mul_f32 v102,v38,v38
v_mul_f32 v103,v39,v39
v_mul_f32 v104,v40,v40
v_mul_f32 v105,v41,v41
v_fma_f32 v102,v80,v102,v79
v_fma_f32 v103,v80,v103,v79
v_fma_f32 v104,v80,v104,v79
v_fma_f32 v105,v80,v105,v79
v_mul_f32 v102,v38,v102
v_mul_f32 v103,v39,v103
v_mul_f32 v104,v40,v104
v_mul_f32 v105,v41,v105
v_exp_f32 v102,v102
v_exp_f32 v103,v103
v_exp_f32 v104,v104
v_exp_f32 v105,v105
v_fma_f32 v102,v102,v81,v81
v_fma_f32 v103,v103,v81,v81
v_fma_f32 v104,v104,v81,v81
v_fma_f32 v105,v105,v81,v81
v_rcp_f32 v102,v102
v_rcp_f32 v103,v103
v_rcp_f32 v104,v104
v_rcp_f32 v105,v105
v_mul_f32 v38,v38,v102
v_mul_f32 v39,v39,v103
v_mul_f32 v40,v40,v104
v_mul_f32 v41,v41,v105
v_cvt_pk_bf16_f32 v110,v38,v39
v_cvt_pk_bf16_f32 v111,v40,v41
buffer_store_dwordx2 v[110:111],v10,s[36:39],s67 offen offset:0 sc0 sc1
s_mov_b32 s72,s69
s_mov_b32 s69,0x7fff0000
s_mov_b32 s67,0x7fff0000
s_mov_b32 s77,0x7fff0000
s_branch Lg3_gox_%=
Lg3_xn1_%=:
s_cmp_ge_u32 s54,256
s_cbranch_scc1 Lg3_fin1_%=
s_add_u32 m0,s59,4096
s_nop 0
buffer_load_dword v8,s[36:39],s58 offen offset:0 sc1 lds
buffer_store_dword v4,v3,s[36:39],s71 offen offset:0 sc0 sc1
s_add_u32 m0,s59,5120
s_nop 0
buffer_load_dword v8,s[36:39],s58 offen offset:0 sc1 lds
buffer_store_dword v4,v3,s[36:39],s71 offen offset:0 sc0 sc1
s_mov_b32 s83,0
L 0
v_fma_f32 v30,-v78,v82,v30
v_fma_f32 v31,-v78,v83,v31
v_fma_f32 v32,-v78,v84,v32
v_fma_f32 v33,-v78,v85,v33
v_mul_f32 v94,v30,v30
v_mul_f32 v95,v31,v31
v_mul_f32 v96,v32,v32
v_mul_f32 v97,v33,v33
v_fma_f32 v94,v80,v94,v79
v_fma_f32 v95,v80,v95,v79
v_fma_f32 v96,v80,v96,v79
v_fma_f32 v97,v80,v97,v79
v_mul_f32 v94,v30,v94
v_mul_f32 v95,v31,v95
v_mul_f32 v96,v32,v96
v_mul_f32 v97,v33,v97
v_exp_f32 v94,v94
v_exp_f32 v95,v95
v_exp_f32 v96,v96
v_exp_f32 v97,v97
v_fma_f32 v94,v94,v81,v81
v_fma_f32 v95,v95,v81,v81
v_fma_f32 v96,v96,v81,v81
v_fma_f32 v97,v97,v81,v81
v_rcp_f32 v94,v94
v_rcp_f32 v95,v95
v_rcp_f32 v96,v96
v_rcp_f32 v97,v97
v_mul_f32 v30,v30,v94
v_mul_f32 v31,v31,v95
v_mul_f32 v32,v32,v96
v_mul_f32 v33,v33,v97
v_cvt_pk_bf16_f32 v106,v30,v31
v_cvt_pk_bf16_f32 v107,v32,v33
v_fma_f32 v34,-v78,v86,v34
v_fma_f32 v35,-v78,v87,v35
v_fma_f32 v36,-v78,v88,v36
v_fma_f32 v37,-v78,v89,v37
v_mul_f32 v98,v34,v34
v_mul_f32 v99,v35,v35
v_mul_f32 v100,v36,v36
v_mul_f32 v101,v37,v37
v_fma_f32 v98,v80,v98,v79
v_fma_f32 v99,v80,v99,v79
v_fma_f32 v100,v80,v100,v79
v_fma_f32 v101,v80,v101,v79
v_mul_f32 v98,v34,v98
v_mul_f32 v99,v35,v99
v_mul_f32 v100,v36,v100
v_mul_f32 v101,v37,v101
v_exp_f32 v98,v98
v_exp_f32 v99,v99
v_exp_f32 v100,v100
v_exp_f32 v101,v101
v_fma_f32 v98,v98,v81,v81
v_fma_f32 v99,v99,v81,v81
v_fma_f32 v100,v100,v81,v81
v_fma_f32 v101,v101,v81,v81
v_rcp_f32 v98,v98
v_rcp_f32 v99,v99
v_rcp_f32 v100,v100
v_rcp_f32 v101,v101
v_mul_f32 v34,v34,v98
v_mul_f32 v35,v35,v99
v_mul_f32 v36,v36,v100
v_mul_f32 v37,v37,v101
v_cvt_pk_bf16_f32 v108,v34,v35
v_cvt_pk_bf16_f32 v109,v36,v37
buffer_store_dwordx4 v[106:109],v0,s[36:39],s67 offen offset:0 sc0 sc1
v_fma_f32 v38,-v78,v90,v38
v_fma_f32 v39,-v78,v91,v39
v_fma_f32 v40,-v78,v92,v40
v_fma_f32 v41,-v78,v93,v41
v_mul_f32 v102,v38,v38
v_mul_f32 v103,v39,v39
v_mul_f32 v104,v40,v40
v_mul_f32 v105,v41,v41
)"
R"(v_fma_f32 v102,v80,v102,v79
v_fma_f32 v103,v80,v103,v79
v_fma_f32 v104,v80,v104,v79
v_fma_f32 v105,v80,v105,v79
v_mul_f32 v102,v38,v102
v_mul_f32 v103,v39,v103
v_mul_f32 v104,v40,v104
v_mul_f32 v105,v41,v105
v_exp_f32 v102,v102
v_exp_f32 v103,v103
v_exp_f32 v104,v104
v_exp_f32 v105,v105
v_fma_f32 v102,v102,v81,v81
v_fma_f32 v103,v103,v81,v81
v_fma_f32 v104,v104,v81,v81
v_fma_f32 v105,v105,v81,v81
v_rcp_f32 v102,v102
v_rcp_f32 v103,v103
v_rcp_f32 v104,v104
v_rcp_f32 v105,v105
v_mul_f32 v38,v38,v102
v_mul_f32 v39,v39,v103
v_mul_f32 v40,v40,v104
v_mul_f32 v41,v41,v105
v_cvt_pk_bf16_f32 v110,v38,v39
v_cvt_pk_bf16_f32 v111,v40,v41
buffer_store_dwordx2 v[110:111],v10,s[36:39],s67 offen offset:0 sc0 sc1
s_mov_b32 s72,s69
s_mov_b32 s69,0x7fff0000
s_mov_b32 s67,0x7fff0000
s_mov_b32 s77,0x7fff0000
s_branch Lg3_poll_%=
Lg3_fin1_%=:
L 0
v_fma_f32 v30,-v78,v82,v30
v_fma_f32 v31,-v78,v83,v31
v_fma_f32 v32,-v78,v84,v32
v_fma_f32 v33,-v78,v85,v33
v_mul_f32 v94,v30,v30
v_mul_f32 v95,v31,v31
v_mul_f32 v96,v32,v32
v_mul_f32 v97,v33,v33
v_fma_f32 v94,v80,v94,v79
v_fma_f32 v95,v80,v95,v79
v_fma_f32 v96,v80,v96,v79
v_fma_f32 v97,v80,v97,v79
v_mul_f32 v94,v30,v94
v_mul_f32 v95,v31,v95
v_mul_f32 v96,v32,v96
v_mul_f32 v97,v33,v97
v_exp_f32 v94,v94
v_exp_f32 v95,v95
v_exp_f32 v96,v96
v_exp_f32 v97,v97
v_fma_f32 v94,v94,v81,v81
v_fma_f32 v95,v95,v81,v81
v_fma_f32 v96,v96,v81,v81
v_fma_f32 v97,v97,v81,v81
v_rcp_f32 v94,v94
v_rcp_f32 v95,v95
v_rcp_f32 v96,v96
v_rcp_f32 v97,v97
v_mul_f32 v30,v30,v94
v_mul_f32 v31,v31,v95
v_mul_f32 v32,v32,v96
v_mul_f32 v33,v33,v97
v_cvt_pk_bf16_f32 v106,v30,v31
v_cvt_pk_bf16_f32 v107,v32,v33
v_fma_f32 v34,-v78,v86,v34
v_fma_f32 v35,-v78,v87,v35
v_fma_f32 v36,-v78,v88,v36
v_fma_f32 v37,-v78,v89,v37
v_mul_f32 v98,v34,v34
v_mul_f32 v99,v35,v35
v_mul_f32 v100,v36,v36
v_mul_f32 v101,v37,v37
v_fma_f32 v98,v80,v98,v79
v_fma_f32 v99,v80,v99,v79
v_fma_f32 v100,v80,v100,v79
v_fma_f32 v101,v80,v101,v79
v_mul_f32 v98,v34,v98
v_mul_f32 v99,v35,v99
v_mul_f32 v100,v36,v100
v_mul_f32 v101,v37,v101
v_exp_f32 v98,v98
v_exp_f32 v99,v99
v_exp_f32 v100,v100
v_exp_f32 v101,v101
v_fma_f32 v98,v98,v81,v81
v_fma_f32 v99,v99,v81,v81
v_fma_f32 v100,v100,v81,v81
v_fma_f32 v101,v101,v81,v81
v_rcp_f32 v98,v98
v_rcp_f32 v99,v99
v_rcp_f32 v100,v100
v_rcp_f32 v101,v101
v_mul_f32 v34,v34,v98
v_mul_f32 v35,v35,v99
v_mul_f32 v36,v36,v100
v_mul_f32 v37,v37,v101
v_cvt_pk_bf16_f32 v108,v34,v35
v_cvt_pk_bf16_f32 v109,v36,v37
buffer_store_dwordx4 v[106:109],v0,s[36:39],s67 offen offset:0 sc0 sc1
v_fma_f32 v38,-v78,v90,v38
v_fma_f32 v39,-v78,v91,v39
v_fma_f32 v40,-v78,v92,v40
v_fma_f32 v41,-v78,v93,v41
v_mul_f32 v102,v38,v38
v_mul_f32 v103,v39,v39
v_mul_f32 v104,v40,v40
v_mul_f32 v105,v41,v41
v_fma_f32 v102,v80,v102,v79
v_fma_f32 v103,v80,v103,v79
v_fma_f32 v104,v80,v104,v79
v_fma_f32 v105,v80,v105,v79
v_mul_f32 v102,v38,v102
v_mul_f32 v103,v39,v103
v_mul_f32 v104,v40,v104
v_mul_f32 v105,v41,v105
v_exp_f32 v102,v102
v_exp_f32 v103,v103
v_exp_f32 v104,v104
v_exp_f32 v105,v105
v_fma_f32 v102,v102,v81,v81
v_fma_f32 v103,v103,v81,v81
v_fma_f32 v104,v104,v81,v81
v_fma_f32 v105,v105,v81,v81
v_rcp_f32 v102,v102
v_rcp_f32 v103,v103
v_rcp_f32 v104,v104
v_rcp_f32 v105,v105
v_mul_f32 v38,v38,v102
v_mul_f32 v39,v39,v103
v_mul_f32 v40,v40,v104
v_mul_f32 v41,v41,v105
v_cvt_pk_bf16_f32 v110,v38,v39
v_cvt_pk_bf16_f32 v111,v40,v41
buffer_store_dwordx2 v[110:111],v10,s[36:39],s67 offen offset:0 sc0 sc1
s_mov_b32 s72,s69
s_mov_b32 s69,0x7fff0000
s_mov_b32 s67,0x7fff0000
s_mov_b32 s77,0x7fff0000
s_branch Lg3_done_%=
Lg3_done_%=:
s_waitcnt vmcnt(2)
s_barrier
buffer_store_dword v4,v3,s[36:39],s70 offen offset:0 sc0 sc1
s_waitcnt vmcnt(1)
s_barrier
buffer_store_dword v4,v3,s[36:39],s72 offen offset:0 sc0 sc1
s_waitcnt vmcnt(0) lgkmcnt(0)
s_barrier
s_endpgm
Lcode_g3_e:
.purgem M
.purgem R
.purgem T
.purgem D
.purgem L
)"
  :: "s"(ws), "s"(sWave), "s"(sG0), "s"(sStep), "s"(sOutOff), "s"(sFoutOff), "s"(sAuxOff), "s"(sWw), "s"(sBuf), "s"(sExp), "v"(vL16), "v"(vFlag), "v"(vSnapOff), "v"(vSnapRd), "v"(vWd0), "v"(vWd1), "v"(vWr0), "v"(vWr1), "v"(vWr2), "v"(vOutS), "v"(vAux0), "v"(vAux1), "v"(vAux2), "v"(vAux3), "v"(vAn), "v"(vAe), "s"(w3), "s"(out) : "v255", "v0", "v1", "v2", "v3", "v4", "v5", "v6", "v7", "v8", "v9", "v10", "v11", "v12", "v13", "v14", "v15", "v16", "v17", "v18", "v19", "v20", "v21", "v22", "v23", "v24", "v25", "v26", "v27", "v28", "v29", "v30", "v31", "v32", "v33", "v34", "v35", "v36", "v37", "v38", "v39", "v40", "v41", "v42", "v43", "v44", "v45", "v46", "v47", "v48", "v49", "v50", "v51", "v52", "v53", "v54", "v55", "v56", "v57", "v58", "v59", "v60", "v61", "v62", "v63", "v64", "v65", "v66", "v67", "v68", "v69", "v70", "v71", "v72", "v73", "v74", "v75", "v76", "v77", "v78", "v79", "v80", "v81", "v82", "v83", "v84", "v85", "v86", "v87", "v88", "v89", "v90", "v91", "v92", "v93", "v94", "v95", "v96", "v97", "v98", "v99", "v100", "v101", "v102", "v103", "v104", "v105", "v106", "v107", "v108", "v109", "v110", "v111", "v112", "v113", "v114", "v115", "v116", "v117", "v118", "v119", "v120", "v121", "v122", "v123", "v124", "v125", "v126", "v127", "v128", "v129", "v130", "v131", "v132", "v133", "v134", "v135", "v136", "v137", "v138", "v139", "v140", "v141", "v142", "v143", "v144", "v145", "v146", "v147", "v148", "v149", "v150", "v151", "v152", "v153", "v154", "v155", "v156", "v157", "v158", "v159", "v160", "v161", "v162", "v163", "v164", "v165", "v166", "v167", "v168", "v169", "v170", "v171", "v172", "v173", "v174", "v175", "v176", "v177", "v178", "v179", "v180", "v181", "v182", "v183", "v184", "v185", "v186", "v187", "v188", "v189", "v190", "v191", "v192", "v193", "v194", "v195", "v196", "v197", "v198", "v199", "v200", "v201", "v202", "v203", "v204", "v205", "v206", "v207", "v208", "v209", "v210", "v211", "v212", "v213", "v214", "v215", "v216", "v217", "v218", "v219", "v220", "v221", "v222", "v223", "v224", "v225", "v226", "v227", "v228", "v229", "v230", "v231", "a0", "a1", "a2", "a3", "a4", "a5", "a6", "a7", "a8", "a9", "a10", "a11", "a12", "a13", "a14", "a15", "a16", "a17", "a18", "a19", "a20", "a21", "a22", "a23", "a24", "a25", "a26", "a27", "a28", "a29", "a30", "a31", "a32", "a33", "a34", "a35", "a36", "a37", "a38", "a39", "a40", "a41", "a42", "a43", "a44", "a45", "a46", "a47", "a48", "a49", "a50", "a51", "a52", "a53", "a54", "a55", "a56", "a57", "a58", "a59", "a60", "a61", "a62", "a63", "a64", "a65", "a66", "a67", "a68", "a69", "a70", "a71", "a72", "a73", "a74", "a75", "a76", "a77", "a78", "a79", "a80", "a81", "a82", "a83", "a84", "a85", "a86", "a87", "a88", "a89", "a90", "a91", "a92", "a93", "a94", "a95", "a96", "a97", "a98", "a99", "a100", "a101", "a102", "a103", "a104", "a105", "a106", "a107", "a108", "a109", "a110", "a111", "a112", "a113", "a114", "a115", "a116", "a117", "a118", "a119", "a120", "a121", "a122", "a123", "a124", "a125", "a126", "a127", "a128", "a129", "a130", "a131", "a132", "a133", "a134", "a135", "a136", "a137", "a138", "a139", "a140", "a141", "a142", "a143", "a144", "a145", "a146", "a147", "a148", "a149", "a150", "a151", "a152", "a153", "a154", "a155", "a156", "a157", "a158", "a159", "a160", "a161", "a162", "a163", "a164", "a165", "a166", "a167", "a168", "a169", "a170", "a171", "a172", "a173", "a174", "a175", "a176", "a177", "a178", "a179", "a180", "a181", "a182", "a183", "a184", "a185", "a186", "a187", "a188", "a189", "a190", "a191", "a192", "a193", "a194", "a195", "a196", "a197", "a198", "a199", "a200", "a201", "a202", "a203", "a204", "a205", "a206", "a207", "a208", "a209", "a210", "a211", "a212", "a213", "a214", "a215", "a216", "a217", "a218", "a219", "a220", "a221", "a222", "a223", "a224", "a225", "a226", "a227", "a228", "a229", "a230", "a231", "a232", "a233", "a234", "a235", "a236", "a237", "a238", "a239", "a240", "a241", "a242", "a243", "a244", "a245", "a246", "a247", "a248", "a249", "a250", "a251", "a252", "a253", "a254", "a255", "s36", "s37", "s38", "s39", "s40", "s41", "s42", "s43", "s44", "s45", "s46", "s47", "s48", "s49", "s50", "s51", "s52", "s53", "s54", "s55", "s56", "s57", "s58", "s59", "s60", "s61", "s62", "s63", "s64", "s65", "s66", "s67", "s68", "s69", "s70", "s71", "s72", "s73", "s74", "s75", "s76", "s77", "s78", "s79", "s80", "s81", "s82", "s83", "s84", "s85", "s86", "s87", "s88", "s89", "s90", "s91", "s92", "s93", "s94", "s95", "s96", "s97", "vcc", "scc", "memory"); } else { asm volatile(
R"(.macro M d,w,x
v_mfma_f32_16x16x32_bf16 v[\d:\d+3], \w, v[\x:\x+3], v[\d:\d+3]
.endm
.macro R d,a,o
ds_read_b128 v[\d:\d+3], v\a offset:\o
.endm
.macro T d,a,o
ds_read_b64_tr_b16 \d, v\a offset:\o
.endm
.macro D a,s,o
buffer_load_dwordx4 v\a, s[36:39], s\s offen offset:\o sc1 lds
.endm
.macro L c
s_waitcnt lgkmcnt(\c)
.endm
Lcode_g3o_b:
s_mov_b64 s[36:37],%0
s_mov_b32 s38,111243392
s_mov_b32 s39,0x00020000
s_mov_b32 s44,%1
s_mov_b32 s45,%2
s_mov_b32 s46,%3
s_mov_b32 s47,%4
s_mov_b32 s48,%5
s_mov_b32 s49,%6
s_mov_b32 s50,%7
s_mov_b32 s51,%8
s_mov_b32 s53,%9
v_mov_b32 v0,%10
v_mov_b32 v3,%11
v_mov_b32 v5,%12
v_mov_b32 v6,%13
v_mov_b32 v18,%14
v_mov_b32 v19,%15
v_mov_b32 v20,%16
v_mov_b32 v21,%17
v_mov_b32 v22,%18
v_mov_b32 v10,%19
v_mov_b32 v11,%20
v_mov_b32 v12,%21
v_mov_b32 v13,%22
v_mov_b32 v14,%23
v_mov_b32 v15,%24
v_mov_b32 v16,%25
s_mov_b64 s[40:41],%26
s_mov_b32 s42,7340032
s_mov_b32 s43,0x00020000
s_lshl_b32 s57,s44,11
s_add_u32 s55,s50,0
s_add_u32 m0,s57,0
s_nop 0
buffer_load_dwordx4 v18,s[40:43],s55 offen offset:0  lds
s_add_u32 m0,s57,1024
s_nop 0
buffer_load_dwordx4 v19,s[40:43],s55 offen offset:0  lds
s_add_u32 m0,s57,8064
s_nop 0
buffer_load_dwordx4 v18,s[40:43],s55 offen offset:128  lds
s_add_u32 m0,s57,9088
s_nop 0
buffer_load_dwordx4 v19,s[40:43],s55 offen offset:128  lds
s_add_u32 s55,s50,458752
s_add_u32 m0,s57,16384
s_nop 0
buffer_load_dwordx4 v18,s[40:43],s55 offen offset:0  lds
s_add_u32 m0,s57,17408
s_nop 0
buffer_load_dwordx4 v19,s[40:43],s55 offen offset:0  lds
s_add_u32 m0,s57,24448
s_nop 0
buffer_load_dwordx4 v18,s[40:43],s55 offen offset:128  lds
s_add_u32 m0,s57,25472
s_nop 0
buffer_load_dwordx4 v19,s[40:43],s55 offen offset:128  lds
s_add_u32 s55,s50,917504
s_add_u32 m0,s57,32768
s_nop 0
buffer_load_dwordx4 v18,s[40:43],s55 offen offset:0  lds
s_add_u32 m0,s57,33792
s_nop 0
buffer_load_dwordx4 v19,s[40:43],s55 offen offset:0  lds
s_add_u32 m0,s57,40832
s_nop 0
buffer_load_dwordx4 v18,s[40:43],s55 offen offset:128  lds
s_add_u32 m0,s57,41856
s_nop 0
buffer_load_dwordx4 v19,s[40:43],s55 offen offset:128  lds
s_add_u32 s55,s50,1376256
s_add_u32 m0,s57,49152
s_nop 0
buffer_load_dwordx4 v18,s[40:43],s55 offen offset:0  lds
s_add_u32 m0,s57,50176
s_nop 0
buffer_load_dwordx4 v19,s[40:43],s55 offen offset:0  lds
s_add_u32 m0,s57,57216
s_nop 0
buffer_load_dwordx4 v18,s[40:43],s55 offen offset:128  lds
s_add_u32 m0,s57,58240
s_nop 0
buffer_load_dwordx4 v19,s[40:43],s55 offen offset:128  lds
s_add_u32 s55,s50,1835008
s_add_u32 m0,s57,65536
s_nop 0
buffer_load_dwordx4 v18,s[40:43],s55 offen offset:0  lds
s_add_u32 m0,s57,66560
s_nop 0
buffer_load_dwordx4 v19,s[40:43],s55 offen offset:0  lds
s_add_u32 m0,s57,73600
s_nop 0
buffer_load_dwordx4 v18,s[40:43],s55 offen offset:128  lds
s_add_u32 m0,s57,74624
s_nop 0
buffer_load_dwordx4 v19,s[40:43],s55 offen offset:128  lds
s_add_u32 s55,s50,2293760
s_add_u32 m0,s57,81920
s_nop 0
buffer_load_dwordx4 v18,s[40:43],s55 offen offset:0  lds
s_add_u32 m0,s57,82944
s_nop 0
buffer_load_dwordx4 v19,s[40:43],s55 offen offset:0  lds
s_add_u32 m0,s57,89984
s_nop 0
buffer_load_dwordx4 v18,s[40:43],s55 offen offset:128  lds
s_add_u32 m0,s57,91008
s_nop 0
buffer_load_dwordx4 v19,s[40:43],s55 offen offset:128  lds
s_waitcnt vmcnt(20) lgkmcnt(0)
s_barrier
s_add_u32 s55,s50,2752512
s_add_u32 m0,s57,98304
s_nop 0
buffer_load_dwordx4 v18,s[40:43],s55 offen offset:0  lds
s_add_u32 m0,s57,99328
s_nop 0
buffer_load_dwordx4 v19,s[40:43],s55 offen offset:0  lds
s_add_u32 m0,s57,106368
s_nop 0
buffer_load_dwordx4 v18,s[40:43],s55 offen offset:128  lds
s_add_u32 m0,s57,107392
s_nop 0
buffer_load_dwordx4 v19,s[40:43],s55 offen offset:128  lds
v_add_u32 v26,0,v20
v_add_u32 v27,0,v21
T a[0:1],26,0
T a[2:3],26,512
T a[4:5],27,0
T a[6:7],27,512
T a[8:9],26,4096
T a[10:11],26,4608
T a[12:13],27,4096
T a[14:15],27,4608
s_waitcnt vmcnt(20) lgkmcnt(0)
s_barrier
s_add_u32 s55,s50,3211264
s_add_u32 m0,s57,0
s_nop 0
buffer_load_dwordx4 v18,s[40:43],s55 offen offset:0  lds
s_add_u32 m0,s57,1024
s_nop 0
buffer_load_dwordx4 v19,s[40:43],s55 offen offset:0  lds
s_add_u32 m0,s57,8064
s_nop 0
buffer_load_dwordx4 v18,s[40:43],s55 offen offset:128  lds
s_add_u32 m0,s57,9088
s_nop 0
buffer_load_dwordx4 v19,s[40:43],s55 offen offset:128  lds
v_add_u32 v26,16384,v20
v_add_u32 v27,16384,v21
T a[16:17],26,0
T a[18:19],26,512
T a[20:21],27,0
T a[22:23],27,512
T a[24:25],26,4096
T a[26:27],26,4608
T a[28:29],27,4096
T a[30:31],27,4608
s_waitcnt vmcnt(20) lgkmcnt(0)
s_barrier
s_add_u32 s55,s50,3670016
s_add_u32 m0,s57,16384
s_nop 0
buffer_load_dwordx4 v18,s[40:43],s55 offen offset:0  lds
s_add_u32 m0,s57,17408
s_nop 0
buffer_load_dwordx4 v19,s[40:43],s55 offen offset:0  lds
s_add_u32 m0,s57,24448
s_nop 0
buffer_load_dwordx4 v18,s[40:43],s55 offen offset:128  lds
s_add_u32 m0,s57,25472
s_nop 0
buffer_load_dwordx4 v19,s[40:43],s55 offen offset:128  lds
v_add_u32 v26,32768,v20
v_add_u32 v27,32768,v21
T a[32:33],26,0
T a[34:35],26,512
T a[36:37],27,0
T a[38:39],27,512
T a[40:41],26,4096
T a[42:43],26,4608
T a[44:45],27,4096
T a[46:47],27,4608
s_waitcnt vmcnt(20) lgkmcnt(0)
s_barrier
s_add_u32 s55,s50,4128768
s_add_u32 m0,s57,32768
s_nop 0
buffer_load_dwordx4 v18,s[40:43],s55 offen offset:0  lds
s_add_u32 m0,s57,33792
s_nop 0
buffer_load_dwordx4 v19,s[40:43],s55 offen offset:0  lds
s_add_u32 m0,s57,40832
s_nop 0
buffer_load_dwordx4 v18,s[40:43],s55 offen offset:128  lds
s_add_u32 m0,s57,41856
s_nop 0
buffer_load_dwordx4 v19,s[40:43],s55 offen offset:128  lds
v_add_u32 v26,49152,v20
v_add_u32 v27,49152,v21
T a[48:49],26,0
T a[50:51],26,512
T a[52:53],27,0
T a[54:55],27,512
T a[56:57],26,4096
T a[58:59],26,4608
T a[60:61],27,4096
T a[62:63],27,4608
s_waitcnt vmcnt(20) lgkmcnt(0)
s_barrier
s_add_u32 s55,s50,4587520
s_add_u32 m0,s57,49152
s_nop 0
buffer_load_dwordx4 v18,s[40:43],s55 offen offset:0  lds
s_add_u32 m0,s57,50176
s_nop 0
buffer_load_dwordx4 v19,s[40:43],s55 offen offset:0  lds
s_add_u32 m0,s57,57216
s_nop 0
buffer_load_dwordx4 v18,s[40:43],s55 offen offset:128  lds
s_add_u32 m0,s57,58240
s_nop 0
buffer_load_dwordx4 v19,s[40:43],s55 offen offset:128  lds
v_add_u32 v26,65536,v20
v_add_u32 v27,65536,v21
T a[64:65],26,0
T a[66:67],26,512
T a[68:69],27,0
T a[70:71],27,512
T a[72:73],26,4096
T a[74:75],26,4608
T a[76:77],27,4096
T a[78:79],27,4608
s_waitcnt vmcnt(20) lgkmcnt(0)
s_barrier
s_add_u32 s55,s50,5046272
s_add_u32 m0,s57,65536
s_nop 0
buffer_load_dwordx4 v18,s[40:43],s55 offen offset:0  lds
s_add_u32 m0,s57,66560
s_nop 0
buffer_load_dwordx4 v19,s[40:43],s55 offen offset:0  lds
s_add_u32 m0,s57,73600
s_nop 0
buffer_load_dwordx4 v18,s[40:43],s55 offen offset:128  lds
s_add_u32 m0,s57,74624
s_nop 0
buffer_load_dwordx4 v19,s[40:43],s55 offen offset:128  lds
v_add_u32 v26,81920,v20
v_add_u32 v27,81920,v21
T a[80:81],26,0
T a[82:83],26,512
T a[84:85],27,0
T a[86:87],27,512
T a[88:89],26,4096
T a[90:91],26,4608
T a[92:93],27,4096
T a[94:95],27,4608
s_waitcnt vmcnt(20) lgkmcnt(0)
s_barrier
s_add_u32 s55,s50,5505024
s_add_u32 m0,s57,81920
s_nop 0
buffer_load_dwordx4 v18,s[40:43],s55 offen offset:0  lds
s_add_u32 m0,s57,82944
s_nop 0
buffer_load_dwordx4 v19,s[40:43],s55 offen offset:0  lds
s_add_u32 m0,s57,89984
s_nop 0
buffer_load_dwordx4 v18,s[40:43],s55 offen offset:128  lds
s_add_u32 m0,s57,91008
s_nop 0
buffer_load_dwordx4 v19,s[40:43],s55 offen offset:128  lds
v_add_u32 v26,98304,v20
v_add_u32 v27,98304,v21
T a[96:97],26,0
T a[98:99],26,512
T a[100:101],27,0
T a[102:103],27,512
T a[104:105],26,4096
T a[106:107],26,4608
T a[108:109],27,4096
T a[110:111],27,4608
s_waitcnt vmcnt(20) lgkmcnt(0)
s_barrier
s_add_u32 s55,s50,5963776
s_add_u32 m0,s57,98304
s_nop 0
buffer_load_dwordx4 v18,s[40:43],s55 offen offset:0  lds
s_add_u32 m0,s57,99328
s_nop 0
buffer_load_dwordx4 v19,s[40:43],s55 offen offset:0  lds
s_add_u32 m0,s57,106368
s_nop 0
buffer_load_dwordx4 v18,s[40:43],s55 offen offset:128  lds
s_add_u32 m0,s57,107392
s_nop 0
buffer_load_dwordx4 v19,s[40:43],s55 offen offset:128  lds
v_add_u32 v26,0,v20
v_add_u32 v27,0,v21
T a[112:113],26,0
T a[114:115],26,512
T a[116:117],27,0
T a[118:119],27,512
T a[120:121],26,4096
T a[122:123],26,4608
T a[124:125],27,4096
T a[126:127],27,4608
s_waitcnt vmcnt(20) lgkmcnt(0)
s_barrier
s_add_u32 s55,s50,6422528
s_add_u32 m0,s57,0
s_nop 0
buffer_load_dwordx4 v18,s[40:43],s55 offen offset:0  lds
s_add_u32 m0,s57,1024
s_nop 0
buffer_load_dwordx4 v19,s[40:43],s55 offen offset:0  lds
s_add_u32 m0,s57,8064
s_nop 0
buffer_load_dwordx4 v18,s[40:43],s55 offen offset:128  lds
s_add_u32 m0,s57,9088
s_nop 0
buffer_load_dwordx4 v19,s[40:43],s55 offen offset:128  lds
v_add_u32 v26,16384,v20
v_add_u32 v27,16384,v21
T a[128:129],26,0
T a[130:131],26,512
T a[132:133],27,0
T a[134:135],27,512
T a[136:137],26,4096
T a[138:139],26,4608
T a[140:141],27,4096
T a[142:143],27,4608
s_waitcnt vmcnt(20) lgkmcnt(0)
s_barrier
s_add_u32 s55,s50,6881280
s_add_u32 m0,s57,16384
s_nop 0
buffer_load_dwordx4 v18,s[40:43],s55 offen offset:0  lds
s_add_u32 m0,s57,17408
s_nop 0
buffer_load_dwordx4 v19,s[40:43],s55 offen offset:0  lds
s_add_u32 m0,s57,24448
s_nop 0
buffer_load_dwordx4 v18,s[40:43],s55 offen offset:128  lds
s_add_u32 m0,s57,25472
s_nop 0
buffer_load_dwordx4 v19,s[40:43],s55 offen offset:128  lds
v_add_u32 v26,32768,v20
v_add_u32 v27,32768,v21
T a[144:145],26,0
T a[146:147],26,512
T a[148:149],27,0
T a[150:151],27,512
T a[152:153],26,4096
T a[154:155],26,4608
T a[156:157],27,4096
T a[158:159],27,4608
s_waitcnt vmcnt(20) lgkmcnt(0)
s_barrier
v_add_u32 v26,49152,v20
v_add_u32 v27,49152,v21
T a[160:161],26,0
T a[162:163],26,512
T a[164:165],27,0
T a[166:167],27,512
T a[168:169],26,4096
T a[170:171],26,4608
T a[172:173],27,4096
T a[174:175],27,4608
s_waitcnt vmcnt(16) lgkmcnt(0)
s_barrier
v_add_u32 v26,65536,v20
v_add_u32 v27,65536,v21
T a[176:177],26,0
T a[178:179],26,512
T a[180:181],27,0
T a[182:183],27,512
T a[184:185],26,4096
T a[186:187],26,4608
T a[188:189],27,4096
T a[190:191],27,4608
s_waitcnt vmcnt(12) lgkmcnt(0)
s_barrier
v_add_u32 v26,81920,v20
v_add_u32 v27,81920,v21
T a[192:193],26,0
T a[194:195],26,512
T a[196:197],27,0
T a[198:199],27,512
T a[200:201],26,4096
T a[202:203],26,4608
T a[204:205],27,4096
T a[206:207],27,4608
s_waitcnt vmcnt(8) lgkmcnt(0)
s_barrier
v_add_u32 v26,98304,v20
v_add_u32 v27,98304,v21
T a[208:209],26,0
T a[210:211],26,512
T a[212:213],27,0
T a[214:215],27,512
T a[216:217],26,4096
T a[218:219],26,4608
T a[220:221],27,4096
T a[222:223],27,4608
s_waitcnt vmcnt(4) lgkmcnt(0)
s_barrier
v_add_u32 v26,0,v20
v_add_u32 v27,0,v21
T a[224:225],26,0
T a[226:227],26,512
T a[228:229],27,0
T a[230:231],27,512
T a[232:233],26,4096
T a[234:235],26,4608
T a[236:237],27,4096
T a[238:239],27,4608
s_waitcnt vmcnt(0) lgkmcnt(0)
s_barrier
v_add_u32 v26,16384,v20
v_add_u32 v27,16384,v21
T a[240:241],26,0
T a[242:243],26,512
T a[244:245],27,0
T a[246:247],27,512
T a[248:249],26,4096
T a[250:251],26,4608
T a[252:253],27,4096
T a[254:255],27,4608
L 0
s_barrier
v_mov_b32 v34,0x3f803f80
v_mov_b32 v35,0x3f803f80
v_mov_b32 v36,0x3f803f80
v_mov_b32 v37,0x3f803f80
s_nop 1
v_mfma_f32_16x16x32_bf16 v[106:109],a[0:3],v[34:37],0
v_mfma_f32_16x16x32_bf16 v[110:113],a[4:7],v[34:37],0
M 106,a[8:11],34
M 110,a[12:15],34
M 106,a[16:19],34
M 110,a[20:23],34
M 106,a[24:27],34
M 110,a[28:31],34
M 106,a[32:35],34
M 110,a[36:39],34
M 106,a[40:43],34
M 110,a[44:47],34
M 106,a[48:51],34
M 110,a[52:55],34
M 106,a[56:59],34
M 110,a[60:63],34
M 106,a[64:67],34
M 110,a[68:71],34
M 106,a[72:75],34
M 110,a[76:79],34
M 106,a[80:83],34
M 110,a[84:87],34
M 106,a[88:91],34
M 110,a[92:95],34
M 106,a[96:99],34
M 110,a[100:103],34
M 106,a[104:107],34
M 110,a[108:111],34
M 106,a[112:115],34
M 110,a[116:119],34
M 106,a[120:123],34
M 110,a[124:127],34
M 106,a[128:131],34
M 110,a[132:135],34
M 106,a[136:139],34
M 110,a[140:143],34
M 106,a[144:147],34
M 110,a[148:151],34
M 106,a[152:155],34
M 110,a[156:159],34
M 106,a[160:163],34
M 110,a[164:167],34
M 106,a[168:171],34
M 110,a[172:175],34
M 106,a[176:179],34
)"
R"(M 110,a[180:183],34
M 106,a[184:187],34
M 110,a[188:191],34
M 106,a[192:195],34
M 110,a[196:199],34
M 106,a[200:203],34
M 110,a[204:207],34
M 106,a[208:211],34
M 110,a[212:215],34
M 106,a[216:219],34
M 110,a[220:223],34
M 106,a[224:227],34
M 110,a[228:231],34
M 106,a[232:235],34
M 110,a[236:239],34
M 106,a[240:243],34
M 110,a[244:247],34
M 106,a[248:251],34
M 110,a[252:255],34
v_mov_b32 v4,s53
s_mov_b32 s82,0
s_mov_b32 s80,0
s_mov_b32 s54,s45
s_mul_i32 s57,s44,4096
s_mul_i32 s55,s45,32768
s_add_u32 s55,s55,s57
s_add_u32 s55,s55,31457280
s_mul_i32 s57,s44,4096
s_mul_i32 s60,s46,32768
s_mul_i32 s61,s46,1024
s_mul_i32 s62,s46,114688
s_mul_i32 s63,s46,2560
s_mul_i32 s64,s46,1024
s_lshl_b32 s59,s44,8
s_add_u32 s59,s59,131072
s_mul_i32 s58,s45,1024
s_add_u32 s58,s58,109613056
s_mul_i32 s65,s45,114688
s_add_u32 s65,s65,s47
s_mul_i32 s68,s45,2560
s_add_u32 s68,s68,s48
s_mul_i32 s73,s45,1024
s_add_u32 s73,s73,107479040
s_mul_i32 s74,s45,32768
s_add_u32 s74,s74,s49
s_mul_i32 s75,s45,1024
s_add_u32 s75,s75,107479040
s_mov_b32 s66,0x7fff0000
s_mov_b32 s67,0x7fff0000
s_mov_b32 s69,0x7fff0000
s_mov_b32 s76,0x7fff0000
s_mov_b32 s77,0x7fff0000
s_mov_b32 s70,0x7fff0000
s_mov_b32 s71,0x7fff0000
s_mov_b32 s72,0x7fff0000
v_mov_b32 v1,v0
v_add_u32 v2,16384,v0
s_mov_b32 s84,16384
s_mov_b32 s86,0x3a800000
s_mov_b32 s87,0x3727c5ac
s_mov_b32 s88,0xc0135761
s_mov_b32 s89,0x3d372713
s_lshr_b32 s85,s44,1
s_mul_i32 s85,s85,127
v_lshlrev_b32 v8,22,v0
v_and_b32 v8,0x80000000,v8
v_or_b32 v8,v8,v5
v_add_u32 v2,32768,v0
s_lshl_b32 s84,s57,1
s_add_u32 s84,s84,32768
v_and_b32 v114,240,v0
v_cmp_eq_u32 vcc,0,v114
s_nop 1
v_cndmask_b32 v17,0,1.0,vcc
s_getpc_b64 s[40:41]
Lg3o_pc_%=:
s_lshl_b32 s78,s44,13
v_lshlrev_b32 v114,3,v0
v_add_u32 v114,s78,v114
v_min_u32 v114,Lcode_g3o_e-Lg3o_pc_%=-4,v114
s_mov_b32 m0,163584
s_nop 0
global_load_lds_dword v114,s[40:41]
s_add_u32 m0,s59,4096
s_nop 0
buffer_load_dword v8,s[36:39],s58 offen offset:0 sc1 lds
buffer_store_dword v4,v3,s[36:39],s71 offen offset:0 sc0 sc1
s_add_u32 m0,s59,5120
s_nop 0
buffer_load_dword v8,s[36:39],s58 offen offset:0 sc1 lds
buffer_store_dword v4,v3,s[36:39],s71 offen offset:0 sc0 sc1
s_mov_b32 s83,0
Lg3o_poll_%=:
s_add_u32 s78,s83,2
s_and_b32 s78,s78,3
s_lshl_b32 s78,s78,10
s_add_u32 s78,s78,4096
s_add_u32 m0,s59,s78
s_nop 0
buffer_load_dword v8,s[36:39],s58 offen offset:0 sc1 lds
s_and_b32 s78,s83,3
s_lshl_b32 s78,s78,10
v_add_u32 v114,s78,v6
s_waitcnt vmcnt(4)
s_barrier
ds_read_b32 v7,v114 offset:4096
buffer_store_dword v4,v3,s[36:39],s70 offen offset:0 sc0 sc1
s_mov_b32 s70,s71
s_mov_b32 s71,s72
s_mov_b32 s72,0x7fff0000
s_add_u32 s83,s83,1
s_add_u32 s82,s82,1
L 0
v_cmp_eq_u32 vcc,s53,v7
s_nop 0
s_cmp_eq_u32 vcc_lo,-1
s_cbranch_scc1 Lg3o_go_%=
s_cmp_lt_u32 s82,20000
s_cbranch_scc1 Lg3o_poll_%=
Lg3o_go_%=:
buffer_load_dword v130,v11,s[36:39],s73 offen offset:0 sc1
s_add_u32 s73,s73,s64
s_add_u32 m0,s57,0
s_add_u32 s56,s55,0
D 0,56,0
D 0,56,1024
D 0,56,2048
D 0,56,3072
s_add_u32 s58,s58,s61
s_add_u32 m0,s59,2048
s_nop 0
buffer_load_dword v8,s[36:39],s58 offen offset:0 sc1 lds
s_add_u32 m0,s57,16384
s_add_u32 s56,s55,16384
D 0,56,0
D 0,56,1024
D 0,56,2048
D 0,56,3072
s_add_u32 s55,s55,s60
s_sub_u32 s57,s84,s57
s_add_u32 s54,s54,s46
s_waitcnt vmcnt(4)
s_barrier
buffer_store_dword v4,v3,s[36:39],s70 offen offset:0 sc0 sc1
s_min_u32 s71,s71,s72
buffer_store_dword v4,v3,s[36:39],s71 offen offset:0 sc0 sc1
s_mov_b32 s70,0x7fff0000
s_mov_b32 s71,0x7fff0000
s_mov_b32 s72,0x7fff0000
s_cmp_eq_u32 s80,1
s_cbranch_scc1 Lg3o_st1_%=
s_branch Lg3o_st0_%=
Lg3o_gox_%=:
s_add_u32 m0,s59,2048
s_nop 0
buffer_load_dword v8,s[36:39],s58 offen offset:0 sc1 lds
s_waitcnt vmcnt(0)
s_barrier
buffer_store_dword v4,v3,s[36:39],s70 offen offset:0 sc0 sc1
s_min_u32 s71,s71,s72
buffer_store_dword v4,v3,s[36:39],s71 offen offset:0 sc0 sc1
s_mov_b32 s70,0x7fff0000
s_mov_b32 s71,0x7fff0000
s_mov_b32 s72,0x7fff0000
s_cmp_eq_u32 s80,1
s_cbranch_scc1 Lg3o_st1_%=
Lg3o_st0_%=:
ds_read_b32 v7,v6 offset:2048
R 34,1,0
R 38,1,1024
R 42,1,2048
R 46,1,3072
R 50,1,4096
L 5
v_cmp_eq_u32 vcc,s53,v7
L 3
s_cmp_eq_u32 vcc_lo,-1
s_cbranch_scc1 Lg3o_F0_%=
s_branch Lg3o_L0_%=
Lg3o_st1_%=:
ds_read_b32 v7,v6 offset:2048
R 34,2,0
R 38,2,1024
R 42,2,2048
R 46,2,3072
R 50,2,4096
L 5
v_cmp_eq_u32 vcc,s53,v7
L 3
s_cmp_eq_u32 vcc_lo,-1
s_cbranch_scc1 Lg3o_F1_%=
s_branch Lg3o_L1_%=
Lg3o_F0_%=:
v_mfma_f32_16x16x32_bf16 v[22:25],a[4:7],v[34:37],0
s_add_u32 s58,s58,s61
s_add_u32 m0,s59,0
R 54,1,5120
M 22,a[12:15],38
buffer_load_dword v8,s[36:39],s58 offen offset:0 sc1 lds
v_mfma_f32_16x16x32_bf16 v[18:21],a[0:3],v[34:37],0
s_add_u32 m0,s57,0
R 58,1,6144
v_add_f32_dpp v131,v130,v130 quad_perm:[1,0,3,2] row_mask:0xf bank_mask:0xf
M 18,a[8:11],38
L 3
s_add_u32 s56,s55,0
buffer_load_dword v130,v11,s[36:39],s73 offen offset:0 sc1
M 18,a[16:19],42
s_mov_b32 s66,s65
R 62,1,7168
v_add_f32_dpp v131,v131,v131 quad_perm:[2,3,0,1] row_mask:0xf bank_mask:0xf
M 18,a[24:27],46
D 0,56,0
M 22,a[20:23],42
s_add_u32 s65,s65,s62
R 66,1,8192
v_add_f32_dpp v134,v131,v131 row_half_mirror row_mask:0xf bank_mask:0xf
M 22,a[28:31],46
L 3
D 0,56,1024
M 22,a[36:39],50
s_add_u32 s54,s54,s46
R 70,1,9216
v_mov_b32_dpp v131,v134 row_ror:8 row_mask:0xf bank_mask:0xf
M 22,a[44:47],54
s_add_u32 s73,s73,s64
v_fma_f32 v131,-v134,v134,v131
v_max_f32 v131,s87,v131
M 18,a[32:35],50
R 74,1,10240
v_mul_f32 v134,v17,v134
M 18,a[40:43],54
L 3
D 0,56,2048
M 18,a[48:51],58
R 78,1,11264
v_fma_f32 v26,-v102,v106,v26
M 18,a[56:59],62
D 0,56,3072
M 22,a[52:55],58
s_add_u32 m0,s57,16384
R 82,1,12288
v_fma_f32 v27,-v102,v107,v27
M 22,a[60:63],62
L 3
v_rsq_f32 v132,v131
M 22,a[68:71],66
s_add_u32 s56,s55,16384
R 86,1,13312
v_fma_f32 v131,v15,v131,v16
M 22,a[76:79],70
s_add_u32 s55,s55,s60
v_mul_f32 v135,v132,v132
v_fma_f32 v131,v14,v135,v131
M 18,a[64:67],66
s_sub_u32 s57,s84,s57
R 90,1,14336
v_fma_f32 v134,v132,v131,v134
M 18,a[72:75],70
L 3
ds_write_b32 v12,v134 offset:0
M 18,a[80:83],74
s_waitcnt vmcnt(8)
R 94,1,15360
v_fma_f32 v28,-v102,v108,v28
M 18,a[88:91],78
s_barrier
v_fma_f32 v29,-v102,v109,v29
v_mul_f32 v114,v26,v26
M 22,a[84:87],74
R 34,1,16384
v_mul_f32 v115,v27,v27
M 22,a[92:95],78
L 4
v_mul_f32 v116,v28,v28
v_mul_f32 v117,v29,v29
M 22,a[100:103],82
R 38,1,17408
v_fma_f32 v114,v104,v114,v103
M 22,a[108:111],86
D 0,56,0
M 18,a[96:99],82
R 42,1,18432
v_fma_f32 v115,v104,v115,v103
M 18,a[104:107],86
L 3
v_fma_f32 v116,v104,v116,v103
v_fma_f32 v117,v104,v117,v103
M 18,a[112:115],90
R 46,1,19456
v_mul_f32 v114,v26,v114
M 18,a[120:123],94
v_exp_f32 v114,v114
M 22,a[116:119],90
R 50,1,20480
v_mul_f32 v115,v27,v115
M 22,a[124:127],94
L 3
v_exp_f32 v115,v115
M 22,a[132:135],34
R 54,1,21504
v_mul_f32 v116,v28,v116
M 22,a[140:143],38
D 0,56,1024
M 18,a[128:131],34
R 58,1,22528
v_mul_f32 v117,v29,v117
M 18,a[136:139],38
L 3
v_exp_f32 v116,v116
M 18,a[144:147],42
R 62,1,23552
v_fma_f32 v114,v114,v105,v105
M 18,a[152:155],46
v_exp_f32 v117,v117
M 22,a[148:151],42
R 66,1,24576
v_fma_f32 v115,v115,v105,v105
M 22,a[156:159],46
L 3
D 0,56,2048
M 22,a[164:167],50
R 70,1,25600
v_fma_f32 v116,v116,v105,v105
M 22,a[172:175],54
v_rcp_f32 v114,v114
M 18,a[160:163],50
R 74,1,26624
v_fma_f32 v117,v117,v105,v105
M 18,a[168:171],54
L 3
v_rcp_f32 v115,v115
M 18,a[176:179],58
R 78,1,27648
v_mul_f32 v26,v26,v114
M 18,a[184:187],62
v_rcp_f32 v116,v116
M 22,a[180:183],58
R 82,1,28672
v_mul_f32 v27,v27,v115
M 22,a[188:191],62
L 3
D 0,56,3072
M 22,a[196:199],66
R 86,1,29696
v_mul_f32 v28,v28,v116
M 22,a[204:207],70
v_rcp_f32 v117,v117
M 18,a[192:195],66
R 90,1,30720
v_mul_f32 v29,v29,v117
M 18,a[200:203],70
L 3
s_nop 0
v_cvt_pk_bf16_f32 v122,v26,v27
M 18,a[208:211],74
s_waitcnt vmcnt(4)
R 94,1,31744
v_fma_f32 v30,-v102,v110,v30
M 18,a[216:219],78
s_barrier
ds_read_b32 v7,v6 offset:0
R 98,13,0
M 22,a[212:215],74
R 34,2,0
v_fma_f32 v31,-v102,v111,v31
M 22,a[220:223],78
L 5
buffer_store_dword v4,v3,s[36:39],s70 offen offset:0 sc0 sc1
M 22,a[228:231],82
s_mov_b32 s70,s69
R 38,2,1024
v_fma_f32 v32,-v102,v112,v32
M 22,a[236:239],86
s_mov_b32 s69,s68
v_fma_f32 v33,-v102,v113,v33
v_mul_f32 v118,v30,v30
M 18,a[224:227],82
s_add_u32 s68,s68,s63
R 42,2,2048
v_mul_f32 v119,v31,v31
M 18,a[232:235],86
L 5
v_mul_f32 v120,v32,v32
v_mul_f32 v121,v33,v33
v_fma_f32 v118,v104,v118,v103
v_fma_f32 v119,v104,v119,v103
v_fma_f32 v120,v104,v120,v103
v_fma_f32 v121,v104,v121,v103
v_mul_f32 v118,v30,v118
v_mul_f32 v119,v31,v119
v_mul_f32 v120,v32,v120
v_mul_f32 v121,v33,v121
v_exp_f32 v118,v118
v_exp_f32 v119,v119
v_exp_f32 v120,v120
v_exp_f32 v121,v121
M 18,a[240:243],90
L 4
R 46,2,3072
v_fma_f32 v118,v118,v105,v105
v_fma_f32 v119,v119,v105,v105
v_fma_f32 v120,v120,v105,v105
v_fma_f32 v121,v121,v105,v105
v_rcp_f32 v118,v118
v_rcp_f32 v119,v119
v_rcp_f32 v120,v120
v_rcp_f32 v121,v121
v_cmp_eq_u32 vcc,s53,v7
M 18,a[248:251],94
v_cvt_pk_bf16_f32 v123,v28,v29
v_mul_f32 v30,v30,v118
v_mul_f32 v31,v31,v119
v_mul_f32 v32,v32,v120
v_mul_f32 v33,v33,v121
v_cvt_pk_bf16_f32 v124,v30,v31
v_cvt_pk_bf16_f32 v125,v32,v33
M 22,a[244:247],90
L 2
s_cmp_eq_u32 vcc_lo,-1
R 50,2,4096
buffer_store_dwordx4 v[122:125],v0,s[36:39],s67 offen offset:0 sc0 sc1
M 22,a[252:255],94
s_cbranch_scc0 Lg3o_L1_%=
Lg3o_F1_%=:
v_mfma_f32_16x16x32_bf16 v[30:33],a[4:7],v[34:37],0
s_add_u32 s58,s58,s61
s_add_u32 m0,s59,1024
R 54,2,5120
M 30,a[12:15],38
buffer_load_dword v8,s[36:39],s58 offen offset:0 sc1 lds
v_mfma_f32_16x16x32_bf16 v[26:29],a[0:3],v[34:37],0
s_add_u32 m0,s57,0
R 58,2,6144
v_add_f32_dpp v131,v130,v130 quad_perm:[1,0,3,2] row_mask:0xf bank_mask:0xf
M 26,a[8:11],38
L 3
s_add_u32 s56,s55,0
buffer_load_dword v130,v11,s[36:39],s73 offen offset:0 sc1
M 26,a[16:19],42
s_mov_b32 s67,s65
R 62,2,7168
v_add_f32_dpp v131,v131,v131 quad_perm:[2,3,0,1] row_mask:0xf bank_mask:0xf
M 26,a[24:27],46
D 0,56,0
M 30,a[20:23],42
s_add_u32 s65,s65,s62
R 66,2,8192
v_add_f32_dpp v134,v131,v131 row_half_mirror row_mask:0xf bank_mask:0xf
M 30,a[28:31],46
L 3
D 0,56,1024
M 30,a[36:39],50
s_add_u32 s54,s54,s46
R 70,2,9216
v_mov_b32_dpp v131,v134 row_ror:8 row_mask:0xf bank_mask:0xf
M 30,a[44:47],54
s_add_u32 s73,s73,s64
v_fma_f32 v131,-v134,v134,v131
v_max_f32 v131,s87,v131
M 26,a[32:35],50
R 74,2,10240
v_mul_f32 v134,v17,v134
M 26,a[40:43],54
L 3
D 0,56,2048
M 26,a[48:51],58
R 78,2,11264
v_fma_f32 v18,-v98,v106,v18
M 26,a[56:59],62
D 0,56,3072
M 30,a[52:55],58
s_add_u32 m0,s57,16384
R 82,2,12288
v_fma_f32 v19,-v98,v107,v19
M 30,a[60:63],62
L 3
v_rsq_f32 v132,v131
M 30,a[68:71],66
s_add_u32 s56,s55,16384
R 86,2,13312
v_fma_f32 v131,v15,v131,v16
M 30,a[76:79],70
s_add_u32 s55,s55,s60
v_mul_f32 v135,v132,v132
v_fma_f32 v131,v14,v135,v131
M 26,a[64:67],66
s_sub_u32 s57,s84,s57
R 90,2,14336
v_fma_f32 v134,v132,v131,v134
M 26,a[72:75],70
L 3
ds_write_b32 v12,v134 offset:2048
M 26,a[80:83],74
s_waitcnt vmcnt(8)
R 94,2,15360
v_fma_f32 v20,-v98,v108,v20
M 26,a[88:91],78
s_barrier
v_fma_f32 v21,-v98,v109,v21
)"
R"(v_mul_f32 v114,v18,v18
M 30,a[84:87],74
R 34,2,16384
v_mul_f32 v115,v19,v19
M 30,a[92:95],78
L 4
v_mul_f32 v116,v20,v20
v_mul_f32 v117,v21,v21
M 30,a[100:103],82
R 38,2,17408
v_fma_f32 v114,v100,v114,v99
M 30,a[108:111],86
D 0,56,0
M 26,a[96:99],82
R 42,2,18432
v_fma_f32 v115,v100,v115,v99
M 26,a[104:107],86
L 3
v_fma_f32 v116,v100,v116,v99
v_fma_f32 v117,v100,v117,v99
M 26,a[112:115],90
R 46,2,19456
v_mul_f32 v114,v18,v114
M 26,a[120:123],94
v_exp_f32 v114,v114
M 30,a[116:119],90
R 50,2,20480
v_mul_f32 v115,v19,v115
M 30,a[124:127],94
L 3
v_exp_f32 v115,v115
M 30,a[132:135],34
R 54,2,21504
v_mul_f32 v116,v20,v116
M 30,a[140:143],38
D 0,56,1024
M 26,a[128:131],34
R 58,2,22528
v_mul_f32 v117,v21,v117
M 26,a[136:139],38
L 3
v_exp_f32 v116,v116
M 26,a[144:147],42
R 62,2,23552
v_fma_f32 v114,v114,v101,v101
M 26,a[152:155],46
v_exp_f32 v117,v117
M 30,a[148:151],42
R 66,2,24576
v_fma_f32 v115,v115,v101,v101
M 30,a[156:159],46
L 3
D 0,56,2048
M 30,a[164:167],50
R 70,2,25600
v_fma_f32 v116,v116,v101,v101
M 30,a[172:175],54
v_rcp_f32 v114,v114
M 26,a[160:163],50
R 74,2,26624
v_fma_f32 v117,v117,v101,v101
M 26,a[168:171],54
L 3
v_rcp_f32 v115,v115
M 26,a[176:179],58
R 78,2,27648
v_mul_f32 v18,v18,v114
M 26,a[184:187],62
v_rcp_f32 v116,v116
M 30,a[180:183],58
R 82,2,28672
v_mul_f32 v19,v19,v115
M 30,a[188:191],62
L 3
D 0,56,3072
M 30,a[196:199],66
R 86,2,29696
v_mul_f32 v20,v20,v116
M 30,a[204:207],70
v_rcp_f32 v117,v117
M 26,a[192:195],66
R 90,2,30720
v_mul_f32 v21,v21,v117
M 26,a[200:203],70
L 3
s_nop 0
v_cvt_pk_bf16_f32 v122,v18,v19
M 26,a[208:211],74
s_waitcnt vmcnt(4)
R 94,2,31744
v_fma_f32 v22,-v98,v110,v22
M 26,a[216:219],78
s_barrier
ds_read_b32 v7,v6 offset:1024
R 102,13,2048
M 30,a[212:215],74
R 34,1,0
v_fma_f32 v23,-v98,v111,v23
M 30,a[220:223],78
L 5
buffer_store_dword v4,v3,s[36:39],s70 offen offset:0 sc0 sc1
M 30,a[228:231],82
s_mov_b32 s70,s69
R 38,1,1024
v_fma_f32 v24,-v98,v112,v24
M 30,a[236:239],86
s_mov_b32 s69,s68
v_fma_f32 v25,-v98,v113,v25
v_mul_f32 v118,v22,v22
M 26,a[224:227],82
s_add_u32 s68,s68,s63
R 42,1,2048
v_mul_f32 v119,v23,v23
M 26,a[232:235],86
L 5
v_mul_f32 v120,v24,v24
v_mul_f32 v121,v25,v25
v_fma_f32 v118,v100,v118,v99
v_fma_f32 v119,v100,v119,v99
v_fma_f32 v120,v100,v120,v99
v_fma_f32 v121,v100,v121,v99
v_mul_f32 v118,v22,v118
v_mul_f32 v119,v23,v119
v_mul_f32 v120,v24,v120
v_mul_f32 v121,v25,v121
v_exp_f32 v118,v118
v_exp_f32 v119,v119
v_exp_f32 v120,v120
v_exp_f32 v121,v121
M 26,a[240:243],90
L 4
R 46,1,3072
v_fma_f32 v118,v118,v101,v101
v_fma_f32 v119,v119,v101,v101
v_fma_f32 v120,v120,v101,v101
v_fma_f32 v121,v121,v101,v101
v_rcp_f32 v118,v118
v_rcp_f32 v119,v119
v_rcp_f32 v120,v120
v_rcp_f32 v121,v121
v_cmp_eq_u32 vcc,s53,v7
M 26,a[248:251],94
v_cvt_pk_bf16_f32 v123,v20,v21
v_mul_f32 v22,v22,v118
v_mul_f32 v23,v23,v119
v_mul_f32 v24,v24,v120
v_mul_f32 v25,v25,v121
v_cvt_pk_bf16_f32 v124,v22,v23
v_cvt_pk_bf16_f32 v125,v24,v25
M 30,a[244:247],90
L 2
s_cmp_eq_u32 vcc_lo,-1
R 50,1,4096
buffer_store_dwordx4 v[122:125],v0,s[36:39],s66 offen offset:0 sc0 sc1
M 30,a[252:255],94
s_cbranch_scc1 Lg3o_F0_%=
Lg3o_L0_%=:
v_mfma_f32_16x16x32_bf16 v[22:25],a[4:7],v[34:37],0
s_add_u32 m0,s59,3072
R 54,1,5120
v_add_f32_dpp v131,v130,v130 quad_perm:[1,0,3,2] row_mask:0xf bank_mask:0xf
M 22,a[12:15],38
buffer_load_dword v8,s[36:39],s58 offen offset:0 sc1 lds
v_mfma_f32_16x16x32_bf16 v[18:21],a[0:3],v[34:37],0
s_mov_b32 s66,s65
R 58,1,6144
v_add_f32_dpp v131,v131,v131 quad_perm:[2,3,0,1] row_mask:0xf bank_mask:0xf
M 18,a[8:11],38
L 3
v_fma_f32 v26,-v102,v106,v26
v_fma_f32 v27,-v102,v107,v27
M 18,a[16:19],42
s_add_u32 s65,s65,s62
R 62,1,7168
v_add_f32_dpp v134,v131,v131 row_half_mirror row_mask:0xf bank_mask:0xf
M 18,a[24:27],46
v_fma_f32 v28,-v102,v108,v28
v_fma_f32 v29,-v102,v109,v29
M 22,a[20:23],42
R 66,1,8192
v_mov_b32_dpp v131,v134 row_ror:8 row_mask:0xf bank_mask:0xf
M 22,a[28:31],46
L 3
v_fma_f32 v131,-v134,v134,v131
v_max_f32 v131,s87,v131
M 22,a[36:39],50
R 70,1,9216
v_mul_f32 v134,v17,v134
M 22,a[44:47],54
v_rsq_f32 v132,v131
M 18,a[32:35],50
R 74,1,10240
v_fma_f32 v131,v15,v131,v16
M 18,a[40:43],54
L 3
v_mul_f32 v135,v132,v132
v_fma_f32 v131,v14,v135,v131
M 18,a[48:51],58
R 78,1,11264
v_fma_f32 v134,v132,v131,v134
M 18,a[56:59],62
ds_write_b32 v12,v134 offset:0
M 22,a[52:55],58
R 82,1,12288
v_mul_f32 v114,v26,v26
M 22,a[60:63],62
L 4
v_mul_f32 v115,v27,v27
v_mul_f32 v116,v28,v28
M 22,a[68:71],66
R 86,1,13312
v_mul_f32 v117,v29,v29
M 22,a[76:79],70
v_fma_f32 v114,v104,v114,v103
v_fma_f32 v115,v104,v115,v103
M 18,a[64:67],66
R 90,1,14336
v_fma_f32 v116,v104,v116,v103
M 18,a[72:75],70
L 4
v_fma_f32 v117,v104,v117,v103
v_mul_f32 v114,v26,v114
M 18,a[80:83],74
s_waitcnt vmcnt(0)
R 94,1,15360
v_mul_f32 v115,v27,v115
M 18,a[88:91],78
s_barrier
ds_read_b32 v7,v6 offset:3072
v_mul_f32 v116,v28,v116
M 22,a[84:87],74
R 34,1,16384
v_mul_f32 v117,v29,v117
M 22,a[92:95],78
L 4
buffer_store_dword v4,v3,s[36:39],s70 offen offset:0 sc0 sc1
M 22,a[100:103],82
s_mov_b32 s70,0x7fff0000
R 38,1,17408
v_fma_f32 v30,-v102,v110,v30
M 22,a[108:111],86
v_exp_f32 v114,v114
M 18,a[96:99],82
R 42,1,18432
v_fma_f32 v114,v114,v105,v105
M 18,a[104:107],86
L 4
L 3
v_cmp_eq_u32 vcc,s53,v7
M 18,a[112:115],90
R 46,1,19456
v_fma_f32 v31,-v102,v111,v31
M 18,a[120:123],94
v_exp_f32 v115,v115
M 22,a[116:119],90
s_cmp_eq_u32 vcc_lo,-1
R 50,1,20480
v_fma_f32 v115,v115,v105,v105
s_cbranch_scc0 Lg3o_X0_%=
M 22,a[124:127],94
L 3
s_add_u32 m0,s57,0
s_add_u32 s56,s55,0
buffer_load_dword v130,v11,s[36:39],s73 offen offset:0 sc1
M 22,a[132:135],34
s_add_u32 s58,s58,s61
R 54,1,21504
v_fma_f32 v32,-v102,v112,v32
M 22,a[140:143],38
D 0,56,0
M 18,a[128:131],34
s_add_u32 s54,s54,s46
R 58,1,22528
v_fma_f32 v33,-v102,v113,v33
M 18,a[136:139],38
L 3
v_exp_f32 v116,v116
M 18,a[144:147],42
s_add_u32 s73,s73,s64
R 62,1,23552
v_fma_f32 v116,v116,v105,v105
M 18,a[152:155],46
D 0,56,1024
M 22,a[148:151],42
R 66,1,24576
v_mul_f32 v118,v30,v30
M 22,a[156:159],46
L 3
v_exp_f32 v117,v117
M 22,a[164:167],50
R 70,1,25600
v_fma_f32 v117,v117,v105,v105
M 22,a[172:175],54
D 0,56,2048
M 18,a[160:163],50
R 74,1,26624
v_mul_f32 v119,v31,v31
M 18,a[168:171],54
L 3
v_rcp_f32 v114,v114
M 18,a[176:179],58
R 78,1,27648
v_mul_f32 v26,v26,v114
M 18,a[184:187],62
D 0,56,3072
M 22,a[180:183],58
s_add_u32 m0,s59,0
R 82,1,28672
v_mul_f32 v120,v32,v32
M 22,a[188:191],62
L 3
buffer_load_dword v8,s[36:39],s58 offen offset:0 sc1 lds
M 22,a[196:199],66
s_add_u32 m0,s57,16384
R 86,1,29696
v_mul_f32 v121,v33,v33
M 22,a[204:207],70
s_add_u32 s56,s55,16384
v_rcp_f32 v115,v115
M 18,a[192:195],66
s_add_u32 s55,s55,s60
R 90,1,30720
v_mul_f32 v27,v27,v115
M 18,a[200:203],70
L 3
s_nop 0
D 0,56,0
M 18,a[208:211],74
s_waitcnt vmcnt(1)
R 94,1,31744
v_cvt_pk_bf16_f32 v122,v26,v27
M 18,a[216:219],78
s_barrier
ds_read_b32 v7,v6 offset:0
R 98,13,0
M 22,a[212:215],74
s_sub_u32 s57,s84,s57
R 34,2,0
v_fma_f32 v118,v104,v118,v103
M 22,a[220:223],78
L 5
buffer_store_dword v4,v3,s[36:39],s70 offen offset:0 sc0 sc1
M 22,a[228:231],82
s_mov_b32 s70,s69
R 38,2,1024
v_fma_f32 v119,v104,v119,v103
M 22,a[236:239],86
D 0,56,1024
M 18,a[224:227],82
s_mov_b32 s69,s68
R 42,2,2048
v_fma_f32 v120,v104,v120,v103
M 18,a[232:235],86
L 5
v_fma_f32 v121,v104,v121,v103
v_mul_f32 v118,v30,v118
v_mul_f32 v119,v31,v119
v_mul_f32 v120,v32,v120
v_mul_f32 v121,v33,v121
v_exp_f32 v118,v118
v_exp_f32 v119,v119
v_exp_f32 v120,v120
v_exp_f32 v121,v121
M 18,a[240:243],90
L 4
R 46,2,3072
v_rcp_f32 v116,v116
v_rcp_f32 v117,v117
v_fma_f32 v118,v118,v105,v105
v_fma_f32 v119,v119,v105,v105
v_fma_f32 v120,v120,v105,v105
v_fma_f32 v121,v121,v105,v105
v_rcp_f32 v118,v118
v_rcp_f32 v119,v119
v_rcp_f32 v120,v120
v_rcp_f32 v121,v121
v_cmp_eq_u32 vcc,s53,v7
M 18,a[248:251],94
s_add_u32 s68,s68,s63
v_mul_f32 v28,v28,v116
v_mul_f32 v29,v29,v117
v_cvt_pk_bf16_f32 v123,v28,v29
v_mul_f32 v30,v30,v118
v_mul_f32 v31,v31,v119
v_mul_f32 v32,v32,v120
v_mul_f32 v33,v33,v121
v_cvt_pk_bf16_f32 v124,v30,v31
v_cvt_pk_bf16_f32 v125,v32,v33
M 22,a[244:247],90
L 2
s_cmp_eq_u32 vcc_lo,-1
R 50,2,4096
buffer_store_dwordx4 v[122:125],v0,s[36:39],s67 offen offset:0 sc0 sc1
M 22,a[252:255],94
D 0,56,2048
D 0,56,3072
buffer_store_dword v4,v3,s[36:39],s71 offen offset:0 sc0 sc1
buffer_store_dword v4,v3,s[36:39],s71 offen offset:0 sc0 sc1
s_cbranch_scc1 Lg3o_F1_%=
s_branch Lg3o_L1_%=
Lg3o_X0_%=:
M 22,a[124:127],94
L 3
s_add_u32 m0,s59,0
v_fma_f32 v32,-v102,v112,v32
M 22,a[132:135],34
R 54,1,21504
v_fma_f32 v33,-v102,v113,v33
M 22,a[140:143],38
buffer_load_dword v8,s[36:39],s58 offen offset:0 sc1 lds
M 18,a[128:131],34
R 58,1,22528
v_mul_f32 v118,v30,v30
M 18,a[136:139],38
L 3
v_exp_f32 v116,v116
M 18,a[144:147],42
R 62,1,23552
v_fma_f32 v116,v116,v105,v105
M 18,a[152:155],46
v_exp_f32 v117,v117
M 22,a[148:151],42
R 66,1,24576
v_fma_f32 v117,v117,v105,v105
M 22,a[156:159],46
L 3
v_rcp_f32 v114,v114
v_mul_f32 v119,v31,v31
M 22,a[164:167],50
R 70,1,25600
v_mul_f32 v26,v26,v114
M 22,a[172:175],54
v_rcp_f32 v115,v115
M 18,a[160:163],50
R 74,1,26624
v_mul_f32 v27,v27,v115
M 18,a[168:171],54
L 3
v_rcp_f32 v116,v116
M 18,a[176:179],58
R 78,1,27648
v_mul_f32 v28,v28,v116
M 18,a[184:187],62
v_rcp_f32 v117,v117
M 22,a[180:183],58
R 82,1,28672
v_mul_f32 v29,v29,v117
M 22,a[188:191],62
L 3
v_cvt_pk_bf16_f32 v122,v26,v27
v_cvt_pk_bf16_f32 v123,v28,v29
M 22,a[196:199],66
R 86,1,29696
v_mul_f32 v120,v32,v32
M 22,a[204:207],70
v_mul_f32 v121,v33,v33
v_fma_f32 v118,v104,v118,v103
M 18,a[192:195],66
R 90,1,30720
v_fma_f32 v119,v104,v119,v103
M 18,a[200:203],70
L 3
s_nop 0
v_fma_f32 v120,v104,v120,v103
M 18,a[208:211],74
s_waitcnt vmcnt(0)
R 94,1,31744
v_fma_f32 v121,v104,v121,v103
M 18,a[216:219],78
s_barrier
buffer_store_dword v4,v3,s[36:39],s70 offen offset:0 sc0 sc1
M 22,a[212:215],74
s_mov_b32 s70,s69
ds_read_b32 v7,v6 offset:0
R 98,13,0
M 22,a[220:223],78
L 4
v_mul_f32 v118,v30,v118
v_mul_f32 v119,v31,v119
M 22,a[228:231],82
s_mov_b32 s69,s68
v_mul_f32 v120,v32,v120
v_mul_f32 v121,v33,v121
M 22,a[236:239],86
s_add_u32 s68,s68,s63
v_exp_f32 v118,v118
M 18,a[224:227],82
v_exp_f32 v119,v119
M 18,a[232:235],86
L 2
v_exp_f32 v120,v120
v_exp_f32 v121,v121
M 18,a[240:243],90
L 1
v_fma_f32 v118,v118,v105,v105
v_fma_f32 v119,v119,v105,v105
v_fma_f32 v120,v120,v105,v105
v_fma_f32 v121,v121,v105,v105
v_rcp_f32 v118,v118
v_rcp_f32 v119,v119
)"
R"(v_rcp_f32 v120,v120
v_rcp_f32 v121,v121
M 18,a[248:251],94
v_mul_f32 v30,v30,v118
v_mul_f32 v31,v31,v119
v_mul_f32 v32,v32,v120
v_mul_f32 v33,v33,v121
v_cvt_pk_bf16_f32 v124,v30,v31
v_cvt_pk_bf16_f32 v125,v32,v33
M 22,a[244:247],90
buffer_store_dwordx4 v[122:125],v0,s[36:39],s67 offen offset:0 sc0 sc1
v_cmp_eq_u32 vcc,s53,v7
M 22,a[252:255],94
s_mov_b32 s80,1
s_cmp_eq_u32 vcc_lo,-1
s_cbranch_scc0 Lg3o_xn0_%=
buffer_load_dword v130,v11,s[36:39],s73 offen offset:0 sc1
L 0
v_fma_f32 v18,-v98,v106,v18
v_fma_f32 v19,-v98,v107,v19
v_fma_f32 v20,-v98,v108,v20
v_fma_f32 v21,-v98,v109,v21
v_mul_f32 v114,v18,v18
v_mul_f32 v115,v19,v19
s_add_u32 s73,s73,s64
s_add_u32 m0,s57,0
s_add_u32 s56,s55,0
D 0,56,0
v_mul_f32 v116,v20,v20
v_mul_f32 v117,v21,v21
v_fma_f32 v114,v100,v114,v99
v_fma_f32 v115,v100,v115,v99
v_fma_f32 v116,v100,v116,v99
v_fma_f32 v117,v100,v117,v99
D 0,56,1024
v_mul_f32 v114,v18,v114
v_mul_f32 v115,v19,v115
v_mul_f32 v116,v20,v116
v_mul_f32 v117,v21,v117
v_exp_f32 v114,v114
v_exp_f32 v115,v115
D 0,56,2048
v_exp_f32 v116,v116
v_exp_f32 v117,v117
v_fma_f32 v114,v114,v101,v101
v_fma_f32 v115,v115,v101,v101
v_fma_f32 v116,v116,v101,v101
v_fma_f32 v117,v117,v101,v101
D 0,56,3072
v_rcp_f32 v114,v114
v_rcp_f32 v115,v115
v_rcp_f32 v116,v116
v_rcp_f32 v117,v117
v_mul_f32 v18,v18,v114
v_mul_f32 v19,v19,v115
s_add_u32 s58,s58,s61
s_add_u32 m0,s57,16384
s_add_u32 s56,s55,16384
D 0,56,0
v_mul_f32 v20,v20,v116
v_mul_f32 v21,v21,v117
v_cvt_pk_bf16_f32 v122,v18,v19
v_cvt_pk_bf16_f32 v123,v20,v21
v_fma_f32 v22,-v98,v110,v22
v_fma_f32 v23,-v98,v111,v23
D 0,56,1024
v_fma_f32 v24,-v98,v112,v24
v_fma_f32 v25,-v98,v113,v25
v_mul_f32 v118,v22,v22
v_mul_f32 v119,v23,v23
v_mul_f32 v120,v24,v24
v_mul_f32 v121,v25,v25
D 0,56,2048
v_fma_f32 v118,v100,v118,v99
v_fma_f32 v119,v100,v119,v99
v_fma_f32 v120,v100,v120,v99
v_fma_f32 v121,v100,v121,v99
v_mul_f32 v118,v22,v118
v_mul_f32 v119,v23,v119
D 0,56,3072
v_mul_f32 v120,v24,v120
v_mul_f32 v121,v25,v121
v_exp_f32 v118,v118
v_exp_f32 v119,v119
v_exp_f32 v120,v120
v_exp_f32 v121,v121
s_add_u32 s55,s55,s60
s_sub_u32 s57,s84,s57
s_add_u32 s54,s54,s46
v_fma_f32 v118,v118,v101,v101
v_fma_f32 v119,v119,v101,v101
v_fma_f32 v120,v120,v101,v101
v_fma_f32 v121,v121,v101,v101
v_rcp_f32 v118,v118
v_rcp_f32 v119,v119
v_rcp_f32 v120,v120
v_rcp_f32 v121,v121
v_mul_f32 v22,v22,v118
v_mul_f32 v23,v23,v119
v_mul_f32 v24,v24,v120
v_mul_f32 v25,v25,v121
v_cvt_pk_bf16_f32 v124,v22,v23
v_cvt_pk_bf16_f32 v125,v24,v25
buffer_store_dwordx4 v[122:125],v0,s[36:39],s66 offen offset:0 sc0 sc1
s_mov_b32 s72,s69
s_mov_b32 s69,0x7fff0000
s_mov_b32 s66,0x7fff0000
s_mov_b32 s76,0x7fff0000
s_branch Lg3o_gox_%=
Lg3o_xn0_%=:
s_cmp_ge_u32 s54,256
s_cbranch_scc1 Lg3o_fin0_%=
s_add_u32 m0,s59,4096
s_nop 0
buffer_load_dword v8,s[36:39],s58 offen offset:0 sc1 lds
buffer_store_dword v4,v3,s[36:39],s71 offen offset:0 sc0 sc1
s_add_u32 m0,s59,5120
s_nop 0
buffer_load_dword v8,s[36:39],s58 offen offset:0 sc1 lds
buffer_store_dword v4,v3,s[36:39],s71 offen offset:0 sc0 sc1
s_mov_b32 s83,0
L 0
v_fma_f32 v18,-v98,v106,v18
v_fma_f32 v19,-v98,v107,v19
v_fma_f32 v20,-v98,v108,v20
v_fma_f32 v21,-v98,v109,v21
v_mul_f32 v114,v18,v18
v_mul_f32 v115,v19,v19
v_mul_f32 v116,v20,v20
v_mul_f32 v117,v21,v21
v_fma_f32 v114,v100,v114,v99
v_fma_f32 v115,v100,v115,v99
v_fma_f32 v116,v100,v116,v99
v_fma_f32 v117,v100,v117,v99
v_mul_f32 v114,v18,v114
v_mul_f32 v115,v19,v115
v_mul_f32 v116,v20,v116
v_mul_f32 v117,v21,v117
v_exp_f32 v114,v114
v_exp_f32 v115,v115
v_exp_f32 v116,v116
v_exp_f32 v117,v117
v_fma_f32 v114,v114,v101,v101
v_fma_f32 v115,v115,v101,v101
v_fma_f32 v116,v116,v101,v101
v_fma_f32 v117,v117,v101,v101
v_rcp_f32 v114,v114
v_rcp_f32 v115,v115
v_rcp_f32 v116,v116
v_rcp_f32 v117,v117
v_mul_f32 v18,v18,v114
v_mul_f32 v19,v19,v115
v_mul_f32 v20,v20,v116
v_mul_f32 v21,v21,v117
v_cvt_pk_bf16_f32 v122,v18,v19
v_cvt_pk_bf16_f32 v123,v20,v21
v_fma_f32 v22,-v98,v110,v22
v_fma_f32 v23,-v98,v111,v23
v_fma_f32 v24,-v98,v112,v24
v_fma_f32 v25,-v98,v113,v25
v_mul_f32 v118,v22,v22
v_mul_f32 v119,v23,v23
v_mul_f32 v120,v24,v24
v_mul_f32 v121,v25,v25
v_fma_f32 v118,v100,v118,v99
v_fma_f32 v119,v100,v119,v99
v_fma_f32 v120,v100,v120,v99
v_fma_f32 v121,v100,v121,v99
v_mul_f32 v118,v22,v118
v_mul_f32 v119,v23,v119
v_mul_f32 v120,v24,v120
v_mul_f32 v121,v25,v121
v_exp_f32 v118,v118
v_exp_f32 v119,v119
v_exp_f32 v120,v120
v_exp_f32 v121,v121
v_fma_f32 v118,v118,v101,v101
v_fma_f32 v119,v119,v101,v101
v_fma_f32 v120,v120,v101,v101
v_fma_f32 v121,v121,v101,v101
v_rcp_f32 v118,v118
v_rcp_f32 v119,v119
v_rcp_f32 v120,v120
v_rcp_f32 v121,v121
v_mul_f32 v22,v22,v118
v_mul_f32 v23,v23,v119
v_mul_f32 v24,v24,v120
v_mul_f32 v25,v25,v121
v_cvt_pk_bf16_f32 v124,v22,v23
v_cvt_pk_bf16_f32 v125,v24,v25
buffer_store_dwordx4 v[122:125],v0,s[36:39],s66 offen offset:0 sc0 sc1
s_mov_b32 s72,s69
s_mov_b32 s69,0x7fff0000
s_mov_b32 s66,0x7fff0000
s_mov_b32 s76,0x7fff0000
s_branch Lg3o_poll_%=
Lg3o_fin0_%=:
L 0
v_fma_f32 v18,-v98,v106,v18
v_fma_f32 v19,-v98,v107,v19
v_fma_f32 v20,-v98,v108,v20
v_fma_f32 v21,-v98,v109,v21
v_mul_f32 v114,v18,v18
v_mul_f32 v115,v19,v19
v_mul_f32 v116,v20,v20
v_mul_f32 v117,v21,v21
v_fma_f32 v114,v100,v114,v99
v_fma_f32 v115,v100,v115,v99
v_fma_f32 v116,v100,v116,v99
v_fma_f32 v117,v100,v117,v99
v_mul_f32 v114,v18,v114
v_mul_f32 v115,v19,v115
v_mul_f32 v116,v20,v116
v_mul_f32 v117,v21,v117
v_exp_f32 v114,v114
v_exp_f32 v115,v115
v_exp_f32 v116,v116
v_exp_f32 v117,v117
v_fma_f32 v114,v114,v101,v101
v_fma_f32 v115,v115,v101,v101
v_fma_f32 v116,v116,v101,v101
v_fma_f32 v117,v117,v101,v101
v_rcp_f32 v114,v114
v_rcp_f32 v115,v115
v_rcp_f32 v116,v116
v_rcp_f32 v117,v117
v_mul_f32 v18,v18,v114
v_mul_f32 v19,v19,v115
v_mul_f32 v20,v20,v116
v_mul_f32 v21,v21,v117
v_cvt_pk_bf16_f32 v122,v18,v19
v_cvt_pk_bf16_f32 v123,v20,v21
v_fma_f32 v22,-v98,v110,v22
v_fma_f32 v23,-v98,v111,v23
v_fma_f32 v24,-v98,v112,v24
v_fma_f32 v25,-v98,v113,v25
v_mul_f32 v118,v22,v22
v_mul_f32 v119,v23,v23
v_mul_f32 v120,v24,v24
v_mul_f32 v121,v25,v25
v_fma_f32 v118,v100,v118,v99
v_fma_f32 v119,v100,v119,v99
v_fma_f32 v120,v100,v120,v99
v_fma_f32 v121,v100,v121,v99
v_mul_f32 v118,v22,v118
v_mul_f32 v119,v23,v119
v_mul_f32 v120,v24,v120
v_mul_f32 v121,v25,v121
v_exp_f32 v118,v118
v_exp_f32 v119,v119
v_exp_f32 v120,v120
v_exp_f32 v121,v121
v_fma_f32 v118,v118,v101,v101
v_fma_f32 v119,v119,v101,v101
v_fma_f32 v120,v120,v101,v101
v_fma_f32 v121,v121,v101,v101
v_rcp_f32 v118,v118
v_rcp_f32 v119,v119
v_rcp_f32 v120,v120
v_rcp_f32 v121,v121
v_mul_f32 v22,v22,v118
v_mul_f32 v23,v23,v119
v_mul_f32 v24,v24,v120
v_mul_f32 v25,v25,v121
v_cvt_pk_bf16_f32 v124,v22,v23
v_cvt_pk_bf16_f32 v125,v24,v25
buffer_store_dwordx4 v[122:125],v0,s[36:39],s66 offen offset:0 sc0 sc1
s_mov_b32 s72,s69
s_mov_b32 s69,0x7fff0000
s_mov_b32 s66,0x7fff0000
s_mov_b32 s76,0x7fff0000
s_branch Lg3o_done_%=
Lg3o_L1_%=:
v_mfma_f32_16x16x32_bf16 v[30:33],a[4:7],v[34:37],0
s_add_u32 m0,s59,3072
R 54,2,5120
v_add_f32_dpp v131,v130,v130 quad_perm:[1,0,3,2] row_mask:0xf bank_mask:0xf
M 30,a[12:15],38
buffer_load_dword v8,s[36:39],s58 offen offset:0 sc1 lds
v_mfma_f32_16x16x32_bf16 v[26:29],a[0:3],v[34:37],0
s_mov_b32 s67,s65
R 58,2,6144
v_add_f32_dpp v131,v131,v131 quad_perm:[2,3,0,1] row_mask:0xf bank_mask:0xf
M 26,a[8:11],38
L 3
v_fma_f32 v18,-v98,v106,v18
v_fma_f32 v19,-v98,v107,v19
M 26,a[16:19],42
s_add_u32 s65,s65,s62
R 62,2,7168
v_add_f32_dpp v134,v131,v131 row_half_mirror row_mask:0xf bank_mask:0xf
M 26,a[24:27],46
v_fma_f32 v20,-v98,v108,v20
v_fma_f32 v21,-v98,v109,v21
M 30,a[20:23],42
R 66,2,8192
v_mov_b32_dpp v131,v134 row_ror:8 row_mask:0xf bank_mask:0xf
M 30,a[28:31],46
L 3
v_fma_f32 v131,-v134,v134,v131
v_max_f32 v131,s87,v131
M 30,a[36:39],50
R 70,2,9216
v_mul_f32 v134,v17,v134
M 30,a[44:47],54
v_rsq_f32 v132,v131
M 26,a[32:35],50
R 74,2,10240
v_fma_f32 v131,v15,v131,v16
M 26,a[40:43],54
L 3
v_mul_f32 v135,v132,v132
v_fma_f32 v131,v14,v135,v131
M 26,a[48:51],58
R 78,2,11264
v_fma_f32 v134,v132,v131,v134
M 26,a[56:59],62
ds_write_b32 v12,v134 offset:2048
M 30,a[52:55],58
R 82,2,12288
v_mul_f32 v114,v18,v18
M 30,a[60:63],62
L 4
v_mul_f32 v115,v19,v19
v_mul_f32 v116,v20,v20
M 30,a[68:71],66
R 86,2,13312
v_mul_f32 v117,v21,v21
M 30,a[76:79],70
v_fma_f32 v114,v100,v114,v99
v_fma_f32 v115,v100,v115,v99
M 26,a[64:67],66
R 90,2,14336
v_fma_f32 v116,v100,v116,v99
M 26,a[72:75],70
L 4
v_fma_f32 v117,v100,v117,v99
v_mul_f32 v114,v18,v114
M 26,a[80:83],74
s_waitcnt vmcnt(0)
R 94,2,15360
v_mul_f32 v115,v19,v115
M 26,a[88:91],78
s_barrier
ds_read_b32 v7,v6 offset:3072
v_mul_f32 v116,v20,v116
M 30,a[84:87],74
R 34,2,16384
v_mul_f32 v117,v21,v117
M 30,a[92:95],78
L 4
buffer_store_dword v4,v3,s[36:39],s70 offen offset:0 sc0 sc1
M 30,a[100:103],82
s_mov_b32 s70,0x7fff0000
R 38,2,17408
v_fma_f32 v22,-v98,v110,v22
M 30,a[108:111],86
v_exp_f32 v114,v114
M 26,a[96:99],82
R 42,2,18432
v_fma_f32 v114,v114,v101,v101
M 26,a[104:107],86
L 4
L 3
v_cmp_eq_u32 vcc,s53,v7
M 26,a[112:115],90
R 46,2,19456
v_fma_f32 v23,-v98,v111,v23
M 26,a[120:123],94
v_exp_f32 v115,v115
M 30,a[116:119],90
s_cmp_eq_u32 vcc_lo,-1
R 50,2,20480
v_fma_f32 v115,v115,v101,v101
s_cbranch_scc0 Lg3o_X1_%=
M 30,a[124:127],94
L 3
s_add_u32 m0,s57,0
s_add_u32 s56,s55,0
buffer_load_dword v130,v11,s[36:39],s73 offen offset:0 sc1
M 30,a[132:135],34
s_add_u32 s58,s58,s61
R 54,2,21504
v_fma_f32 v24,-v98,v112,v24
M 30,a[140:143],38
D 0,56,0
M 26,a[128:131],34
s_add_u32 s54,s54,s46
R 58,2,22528
v_fma_f32 v25,-v98,v113,v25
M 26,a[136:139],38
L 3
v_exp_f32 v116,v116
M 26,a[144:147],42
s_add_u32 s73,s73,s64
R 62,2,23552
v_fma_f32 v116,v116,v101,v101
M 26,a[152:155],46
D 0,56,1024
M 30,a[148:151],42
R 66,2,24576
v_mul_f32 v118,v22,v22
M 30,a[156:159],46
L 3
v_exp_f32 v117,v117
M 30,a[164:167],50
R 70,2,25600
v_fma_f32 v117,v117,v101,v101
M 30,a[172:175],54
D 0,56,2048
M 26,a[160:163],50
R 74,2,26624
v_mul_f32 v119,v23,v23
M 26,a[168:171],54
L 3
v_rcp_f32 v114,v114
M 26,a[176:179],58
R 78,2,27648
v_mul_f32 v18,v18,v114
M 26,a[184:187],62
D 0,56,3072
M 30,a[180:183],58
s_add_u32 m0,s59,1024
R 82,2,28672
v_mul_f32 v120,v24,v24
M 30,a[188:191],62
L 3
buffer_load_dword v8,s[36:39],s58 offen offset:0 sc1 lds
M 30,a[196:199],66
s_add_u32 m0,s57,16384
R 86,2,29696
v_mul_f32 v121,v25,v25
M 30,a[204:207],70
s_add_u32 s56,s55,16384
v_rcp_f32 v115,v115
M 26,a[192:195],66
s_add_u32 s55,s55,s60
R 90,2,30720
v_mul_f32 v19,v19,v115
M 26,a[200:203],70
L 3
s_nop 0
D 0,56,0
M 26,a[208:211],74
s_waitcnt vmcnt(1)
R 94,2,31744
v_cvt_pk_bf16_f32 v122,v18,v19
M 26,a[216:219],78
s_barrier
ds_read_b32 v7,v6 offset:1024
R 102,13,2048
M 30,a[212:215],74
s_sub_u32 s57,s84,s57
R 34,1,0
v_fma_f32 v118,v100,v118,v99
M 30,a[220:223],78
L 5
buffer_store_dword v4,v3,s[36:39],s70 offen offset:0 sc0 sc1
M 30,a[228:231],82
s_mov_b32 s70,s69
R 38,1,1024
v_fma_f32 v119,v100,v119,v99
M 30,a[236:239],86
D 0,56,1024
M 26,a[224:227],82
s_mov_b32 s69,s68
R 42,1,2048
v_fma_f32 v120,v100,v120,v99
M 26,a[232:235],86
L 5
v_fma_f32 v121,v100,v121,v99
v_mul_f32 v118,v22,v118
v_mul_f32 v119,v23,v119
v_mul_f32 v120,v24,v120
v_mul_f32 v121,v25,v121
v_exp_f32 v118,v118
v_exp_f32 v119,v119
v_exp_f32 v120,v120
v_exp_f32 v121,v121
M 26,a[240:243],90
L 4
R 46,1,3072
v_rcp_f32 v116,v116
v_rcp_f32 v117,v117
v_fma_f32 v118,v118,v101,v101
v_fma_f32 v119,v119,v101,v101
v_fma_f32 v120,v120,v101,v101
v_fma_f32 v121,v121,v101,v101
v_rcp_f32 v118,v118
v_rcp_f32 v119,v119
v_rcp_f32 v120,v120
v_rcp_f32 v121,v121
v_cmp_eq_u32 vcc,s53,v7
M 26,a[248:251],94
s_add_u32 s68,s68,s63
v_mul_f32 v20,v20,v116
v_mul_f32 v21,v21,v117
v_cvt_pk_bf16_f32 v123,v20,v21
v_mul_f32 v22,v22,v118
v_mul_f32 v23,v23,v119
)"
R"(v_mul_f32 v24,v24,v120
v_mul_f32 v25,v25,v121
v_cvt_pk_bf16_f32 v124,v22,v23
v_cvt_pk_bf16_f32 v125,v24,v25
M 30,a[244:247],90
L 2
s_cmp_eq_u32 vcc_lo,-1
R 50,1,4096
buffer_store_dwordx4 v[122:125],v0,s[36:39],s66 offen offset:0 sc0 sc1
M 30,a[252:255],94
D 0,56,2048
D 0,56,3072
buffer_store_dword v4,v3,s[36:39],s71 offen offset:0 sc0 sc1
buffer_store_dword v4,v3,s[36:39],s71 offen offset:0 sc0 sc1
s_cbranch_scc1 Lg3o_F0_%=
s_branch Lg3o_L0_%=
Lg3o_X1_%=:
M 30,a[124:127],94
L 3
s_add_u32 m0,s59,1024
v_fma_f32 v24,-v98,v112,v24
M 30,a[132:135],34
R 54,2,21504
v_fma_f32 v25,-v98,v113,v25
M 30,a[140:143],38
buffer_load_dword v8,s[36:39],s58 offen offset:0 sc1 lds
M 26,a[128:131],34
R 58,2,22528
v_mul_f32 v118,v22,v22
M 26,a[136:139],38
L 3
v_exp_f32 v116,v116
M 26,a[144:147],42
R 62,2,23552
v_fma_f32 v116,v116,v101,v101
M 26,a[152:155],46
v_exp_f32 v117,v117
M 30,a[148:151],42
R 66,2,24576
v_fma_f32 v117,v117,v101,v101
M 30,a[156:159],46
L 3
v_rcp_f32 v114,v114
v_mul_f32 v119,v23,v23
M 30,a[164:167],50
R 70,2,25600
v_mul_f32 v18,v18,v114
M 30,a[172:175],54
v_rcp_f32 v115,v115
M 26,a[160:163],50
R 74,2,26624
v_mul_f32 v19,v19,v115
M 26,a[168:171],54
L 3
v_rcp_f32 v116,v116
M 26,a[176:179],58
R 78,2,27648
v_mul_f32 v20,v20,v116
M 26,a[184:187],62
v_rcp_f32 v117,v117
M 30,a[180:183],58
R 82,2,28672
v_mul_f32 v21,v21,v117
M 30,a[188:191],62
L 3
v_cvt_pk_bf16_f32 v122,v18,v19
v_cvt_pk_bf16_f32 v123,v20,v21
M 30,a[196:199],66
R 86,2,29696
v_mul_f32 v120,v24,v24
M 30,a[204:207],70
v_mul_f32 v121,v25,v25
v_fma_f32 v118,v100,v118,v99
M 26,a[192:195],66
R 90,2,30720
v_fma_f32 v119,v100,v119,v99
M 26,a[200:203],70
L 3
s_nop 0
v_fma_f32 v120,v100,v120,v99
M 26,a[208:211],74
s_waitcnt vmcnt(0)
R 94,2,31744
v_fma_f32 v121,v100,v121,v99
M 26,a[216:219],78
s_barrier
buffer_store_dword v4,v3,s[36:39],s70 offen offset:0 sc0 sc1
M 30,a[212:215],74
s_mov_b32 s70,s69
ds_read_b32 v7,v6 offset:1024
R 102,13,2048
M 30,a[220:223],78
L 4
v_mul_f32 v118,v22,v118
v_mul_f32 v119,v23,v119
M 30,a[228:231],82
s_mov_b32 s69,s68
v_mul_f32 v120,v24,v120
v_mul_f32 v121,v25,v121
M 30,a[236:239],86
s_add_u32 s68,s68,s63
v_exp_f32 v118,v118
M 26,a[224:227],82
v_exp_f32 v119,v119
M 26,a[232:235],86
L 2
v_exp_f32 v120,v120
v_exp_f32 v121,v121
M 26,a[240:243],90
L 1
v_fma_f32 v118,v118,v101,v101
v_fma_f32 v119,v119,v101,v101
v_fma_f32 v120,v120,v101,v101
v_fma_f32 v121,v121,v101,v101
v_rcp_f32 v118,v118
v_rcp_f32 v119,v119
v_rcp_f32 v120,v120
v_rcp_f32 v121,v121
M 26,a[248:251],94
v_mul_f32 v22,v22,v118
v_mul_f32 v23,v23,v119
v_mul_f32 v24,v24,v120
v_mul_f32 v25,v25,v121
v_cvt_pk_bf16_f32 v124,v22,v23
v_cvt_pk_bf16_f32 v125,v24,v25
M 30,a[244:247],90
buffer_store_dwordx4 v[122:125],v0,s[36:39],s66 offen offset:0 sc0 sc1
v_cmp_eq_u32 vcc,s53,v7
M 30,a[252:255],94
s_mov_b32 s80,0
s_cmp_eq_u32 vcc_lo,-1
s_cbranch_scc0 Lg3o_xn1_%=
buffer_load_dword v130,v11,s[36:39],s73 offen offset:0 sc1
L 0
v_fma_f32 v26,-v102,v106,v26
v_fma_f32 v27,-v102,v107,v27
v_fma_f32 v28,-v102,v108,v28
v_fma_f32 v29,-v102,v109,v29
v_mul_f32 v114,v26,v26
v_mul_f32 v115,v27,v27
s_add_u32 s73,s73,s64
s_add_u32 m0,s57,0
s_add_u32 s56,s55,0
D 0,56,0
v_mul_f32 v116,v28,v28
v_mul_f32 v117,v29,v29
v_fma_f32 v114,v104,v114,v103
v_fma_f32 v115,v104,v115,v103
v_fma_f32 v116,v104,v116,v103
v_fma_f32 v117,v104,v117,v103
D 0,56,1024
v_mul_f32 v114,v26,v114
v_mul_f32 v115,v27,v115
v_mul_f32 v116,v28,v116
v_mul_f32 v117,v29,v117
v_exp_f32 v114,v114
v_exp_f32 v115,v115
D 0,56,2048
v_exp_f32 v116,v116
v_exp_f32 v117,v117
v_fma_f32 v114,v114,v105,v105
v_fma_f32 v115,v115,v105,v105
v_fma_f32 v116,v116,v105,v105
v_fma_f32 v117,v117,v105,v105
D 0,56,3072
v_rcp_f32 v114,v114
v_rcp_f32 v115,v115
v_rcp_f32 v116,v116
v_rcp_f32 v117,v117
v_mul_f32 v26,v26,v114
v_mul_f32 v27,v27,v115
s_add_u32 s58,s58,s61
s_add_u32 m0,s57,16384
s_add_u32 s56,s55,16384
D 0,56,0
v_mul_f32 v28,v28,v116
v_mul_f32 v29,v29,v117
v_cvt_pk_bf16_f32 v122,v26,v27
v_cvt_pk_bf16_f32 v123,v28,v29
v_fma_f32 v30,-v102,v110,v30
v_fma_f32 v31,-v102,v111,v31
D 0,56,1024
v_fma_f32 v32,-v102,v112,v32
v_fma_f32 v33,-v102,v113,v33
v_mul_f32 v118,v30,v30
v_mul_f32 v119,v31,v31
v_mul_f32 v120,v32,v32
v_mul_f32 v121,v33,v33
D 0,56,2048
v_fma_f32 v118,v104,v118,v103
v_fma_f32 v119,v104,v119,v103
v_fma_f32 v120,v104,v120,v103
v_fma_f32 v121,v104,v121,v103
v_mul_f32 v118,v30,v118
v_mul_f32 v119,v31,v119
D 0,56,3072
v_mul_f32 v120,v32,v120
v_mul_f32 v121,v33,v121
v_exp_f32 v118,v118
v_exp_f32 v119,v119
v_exp_f32 v120,v120
v_exp_f32 v121,v121
s_add_u32 s55,s55,s60
s_sub_u32 s57,s84,s57
s_add_u32 s54,s54,s46
v_fma_f32 v118,v118,v105,v105
v_fma_f32 v119,v119,v105,v105
v_fma_f32 v120,v120,v105,v105
v_fma_f32 v121,v121,v105,v105
v_rcp_f32 v118,v118
v_rcp_f32 v119,v119
v_rcp_f32 v120,v120
v_rcp_f32 v121,v121
v_mul_f32 v30,v30,v118
v_mul_f32 v31,v31,v119
v_mul_f32 v32,v32,v120
v_mul_f32 v33,v33,v121
v_cvt_pk_bf16_f32 v124,v30,v31
v_cvt_pk_bf16_f32 v125,v32,v33
buffer_store_dwordx4 v[122:125],v0,s[36:39],s67 offen offset:0 sc0 sc1
s_mov_b32 s72,s69
s_mov_b32 s69,0x7fff0000
s_mov_b32 s67,0x7fff0000
s_mov_b32 s77,0x7fff0000
s_branch Lg3o_gox_%=
Lg3o_xn1_%=:
s_cmp_ge_u32 s54,256
s_cbranch_scc1 Lg3o_fin1_%=
s_add_u32 m0,s59,4096
s_nop 0
buffer_load_dword v8,s[36:39],s58 offen offset:0 sc1 lds
buffer_store_dword v4,v3,s[36:39],s71 offen offset:0 sc0 sc1
s_add_u32 m0,s59,5120
s_nop 0
buffer_load_dword v8,s[36:39],s58 offen offset:0 sc1 lds
buffer_store_dword v4,v3,s[36:39],s71 offen offset:0 sc0 sc1
s_mov_b32 s83,0
L 0
v_fma_f32 v26,-v102,v106,v26
v_fma_f32 v27,-v102,v107,v27
v_fma_f32 v28,-v102,v108,v28
v_fma_f32 v29,-v102,v109,v29
v_mul_f32 v114,v26,v26
v_mul_f32 v115,v27,v27
v_mul_f32 v116,v28,v28
v_mul_f32 v117,v29,v29
v_fma_f32 v114,v104,v114,v103
v_fma_f32 v115,v104,v115,v103
v_fma_f32 v116,v104,v116,v103
v_fma_f32 v117,v104,v117,v103
v_mul_f32 v114,v26,v114
v_mul_f32 v115,v27,v115
v_mul_f32 v116,v28,v116
v_mul_f32 v117,v29,v117
v_exp_f32 v114,v114
v_exp_f32 v115,v115
v_exp_f32 v116,v116
v_exp_f32 v117,v117
v_fma_f32 v114,v114,v105,v105
v_fma_f32 v115,v115,v105,v105
v_fma_f32 v116,v116,v105,v105
v_fma_f32 v117,v117,v105,v105
v_rcp_f32 v114,v114
v_rcp_f32 v115,v115
v_rcp_f32 v116,v116
v_rcp_f32 v117,v117
v_mul_f32 v26,v26,v114
v_mul_f32 v27,v27,v115
v_mul_f32 v28,v28,v116
v_mul_f32 v29,v29,v117
v_cvt_pk_bf16_f32 v122,v26,v27
v_cvt_pk_bf16_f32 v123,v28,v29
v_fma_f32 v30,-v102,v110,v30
v_fma_f32 v31,-v102,v111,v31
v_fma_f32 v32,-v102,v112,v32
v_fma_f32 v33,-v102,v113,v33
v_mul_f32 v118,v30,v30
v_mul_f32 v119,v31,v31
v_mul_f32 v120,v32,v32
v_mul_f32 v121,v33,v33
v_fma_f32 v118,v104,v118,v103
v_fma_f32 v119,v104,v119,v103
v_fma_f32 v120,v104,v120,v103
v_fma_f32 v121,v104,v121,v103
v_mul_f32 v118,v30,v118
v_mul_f32 v119,v31,v119
v_mul_f32 v120,v32,v120
v_mul_f32 v121,v33,v121
v_exp_f32 v118,v118
v_exp_f32 v119,v119
v_exp_f32 v120,v120
v_exp_f32 v121,v121
v_fma_f32 v118,v118,v105,v105
v_fma_f32 v119,v119,v105,v105
v_fma_f32 v120,v120,v105,v105
v_fma_f32 v121,v121,v105,v105
v_rcp_f32 v118,v118
v_rcp_f32 v119,v119
v_rcp_f32 v120,v120
v_rcp_f32 v121,v121
v_mul_f32 v30,v30,v118
v_mul_f32 v31,v31,v119
v_mul_f32 v32,v32,v120
v_mul_f32 v33,v33,v121
v_cvt_pk_bf16_f32 v124,v30,v31
v_cvt_pk_bf16_f32 v125,v32,v33
buffer_store_dwordx4 v[122:125],v0,s[36:39],s67 offen offset:0 sc0 sc1
s_mov_b32 s72,s69
s_mov_b32 s69,0x7fff0000
s_mov_b32 s67,0x7fff0000
s_mov_b32 s77,0x7fff0000
s_branch Lg3o_poll_%=
Lg3o_fin1_%=:
L 0
v_fma_f32 v26,-v102,v106,v26
v_fma_f32 v27,-v102,v107,v27
v_fma_f32 v28,-v102,v108,v28
v_fma_f32 v29,-v102,v109,v29
v_mul_f32 v114,v26,v26
v_mul_f32 v115,v27,v27
v_mul_f32 v116,v28,v28
v_mul_f32 v117,v29,v29
v_fma_f32 v114,v104,v114,v103
v_fma_f32 v115,v104,v115,v103
v_fma_f32 v116,v104,v116,v103
v_fma_f32 v117,v104,v117,v103
v_mul_f32 v114,v26,v114
v_mul_f32 v115,v27,v115
v_mul_f32 v116,v28,v116
v_mul_f32 v117,v29,v117
v_exp_f32 v114,v114
v_exp_f32 v115,v115
v_exp_f32 v116,v116
v_exp_f32 v117,v117
v_fma_f32 v114,v114,v105,v105
v_fma_f32 v115,v115,v105,v105
v_fma_f32 v116,v116,v105,v105
v_fma_f32 v117,v117,v105,v105
v_rcp_f32 v114,v114
v_rcp_f32 v115,v115
v_rcp_f32 v116,v116
v_rcp_f32 v117,v117
v_mul_f32 v26,v26,v114
v_mul_f32 v27,v27,v115
v_mul_f32 v28,v28,v116
v_mul_f32 v29,v29,v117
v_cvt_pk_bf16_f32 v122,v26,v27
v_cvt_pk_bf16_f32 v123,v28,v29
v_fma_f32 v30,-v102,v110,v30
v_fma_f32 v31,-v102,v111,v31
v_fma_f32 v32,-v102,v112,v32
v_fma_f32 v33,-v102,v113,v33
v_mul_f32 v118,v30,v30
v_mul_f32 v119,v31,v31
v_mul_f32 v120,v32,v32
v_mul_f32 v121,v33,v33
v_fma_f32 v118,v104,v118,v103
v_fma_f32 v119,v104,v119,v103
v_fma_f32 v120,v104,v120,v103
v_fma_f32 v121,v104,v121,v103
v_mul_f32 v118,v30,v118
v_mul_f32 v119,v31,v119
v_mul_f32 v120,v32,v120
v_mul_f32 v121,v33,v121
v_exp_f32 v118,v118
v_exp_f32 v119,v119
v_exp_f32 v120,v120
v_exp_f32 v121,v121
v_fma_f32 v118,v118,v105,v105
v_fma_f32 v119,v119,v105,v105
v_fma_f32 v120,v120,v105,v105
v_fma_f32 v121,v121,v105,v105
v_rcp_f32 v118,v118
v_rcp_f32 v119,v119
v_rcp_f32 v120,v120
v_rcp_f32 v121,v121
v_mul_f32 v30,v30,v118
v_mul_f32 v31,v31,v119
v_mul_f32 v32,v32,v120
v_mul_f32 v33,v33,v121
v_cvt_pk_bf16_f32 v124,v30,v31
v_cvt_pk_bf16_f32 v125,v32,v33
buffer_store_dwordx4 v[122:125],v0,s[36:39],s67 offen offset:0 sc0 sc1
s_mov_b32 s72,s69
s_mov_b32 s69,0x7fff0000
s_mov_b32 s67,0x7fff0000
s_mov_b32 s77,0x7fff0000
s_branch Lg3o_done_%=
Lg3o_done_%=:
s_waitcnt vmcnt(1)
s_barrier
buffer_store_dword v4,v3,s[36:39],s70 offen offset:0 sc0 sc1
s_waitcnt vmcnt(1)
s_barrier
buffer_store_dword v4,v3,s[36:39],s72 offen offset:0 sc0 sc1
s_waitcnt vmcnt(0) lgkmcnt(0)
s_barrier
s_endpgm
Lcode_g3o_e:
.purgem M
.purgem R
.purgem T
.purgem D
.purgem L
)"
  :: "s"(ws), "s"(sWave), "s"(sG0), "s"(sStep), "s"(sOutOff), "s"(sFoutOff), "s"(sAuxOff), "s"(sWw), "s"(sBuf), "s"(sExp), "v"(vL16), "v"(vFlag), "v"(vSnapOff), "v"(vSnapRd), "v"(vWd0), "v"(vWd1), "v"(vWr0), "v"(vWr1), "v"(vWr2), "v"(vOutS), "v"(vAux0), "v"(vAux1), "v"(vAux2), "v"(vAux3), "v"(vAn), "v"(vAe), "s"(w3), "s"(out) : "v255", "v0", "v1", "v2", "v3", "v4", "v5", "v6", "v7", "v8", "v9", "v10", "v11", "v12", "v13", "v14", "v15", "v16", "v17", "v18", "v19", "v20", "v21", "v22", "v23", "v24", "v25", "v26", "v27", "v28", "v29", "v30", "v31", "v32", "v33", "v34", "v35", "v36", "v37", "v38", "v39", "v40", "v41", "v42", "v43", "v44", "v45", "v46", "v47", "v48", "v49", "v50", "v51", "v52", "v53", "v54", "v55", "v56", "v57", "v58", "v59", "v60", "v61", "v62", "v63", "v64", "v65", "v66", "v67", "v68", "v69", "v70", "v71", "v72", "v73", "v74", "v75", "v76", "v77", "v78", "v79", "v80", "v81", "v82", "v83", "v84", "v85", "v86", "v87", "v88", "v89", "v90", "v91", "v92", "v93", "v94", "v95", "v96", "v97", "v98", "v99", "v100", "v101", "v102", "v103", "v104", "v105", "v106", "v107", "v108", "v109", "v110", "v111", "v112", "v113", "v114", "v115", "v116", "v117", "v118", "v119", "v120", "v121", "v122", "v123", "v124", "v125", "v126", "v127", "v128", "v129", "v130", "v131", "v132", "v133", "v134", "v135", "v136", "v137", "v138", "v139", "v140", "v141", "v142", "v143", "v144", "v145", "v146", "v147", "v148", "v149", "v150", "v151", "v152", "v153", "v154", "v155", "v156", "v157", "v158", "v159", "v160", "v161", "v162", "v163", "v164", "v165", "v166", "v167", "v168", "v169", "v170", "v171", "v172", "v173", "v174", "v175", "v176", "v177", "v178", "v179", "v180", "v181", "v182", "v183", "v184", "v185", "v186", "v187", "v188", "v189", "v190", "v191", "v192", "v193", "v194", "v195", "v196", "v197", "v198", "v199", "v200", "v201", "v202", "v203", "v204", "v205", "v206", "v207", "v208", "v209", "v210", "v211", "v212", "v213", "v214", "v215", "v216", "v217", "v218", "v219", "v220", "v221", "v222", "v223", "v224", "v225", "v226", "v227", "v228", "v229", "v230", "v231", "a0", "a1", "a2", "a3", "a4", "a5", "a6", "a7", "a8", "a9", "a10", "a11", "a12", "a13", "a14", "a15", "a16", "a17", "a18", "a19", "a20", "a21", "a22", "a23", "a24", "a25", "a26", "a27", "a28", "a29", "a30", "a31", "a32", "a33", "a34", "a35", "a36", "a37", "a38", "a39", "a40", "a41", "a42", "a43", "a44", "a45", "a46", "a47", "a48", "a49", "a50", "a51", "a52", "a53", "a54", "a55", "a56", "a57", "a58", "a59", "a60", "a61", "a62", "a63", "a64", "a65", "a66", "a67", "a68", "a69", "a70", "a71", "a72", "a73", "a74", "a75", "a76", "a77", "a78", "a79", "a80", "a81", "a82", "a83", "a84", "a85", "a86", "a87", "a88", "a89", "a90", "a91", "a92", "a93", "a94", "a95", "a96", "a97", "a98", "a99", "a100", "a101", "a102", "a103", "a104", "a105", "a106", "a107", "a108", "a109", "a110", "a111", "a112", "a113", "a114", "a115", "a116", "a117", "a118", "a119", "a120", "a121", "a122", "a123", "a124", "a125", "a126", "a127", "a128", "a129", "a130", "a131", "a132", "a133", "a134", "a135", "a136", "a137", "a138", "a139", "a140", "a141", "a142", "a143", "a144", "a145", "a146", "a147", "a148", "a149", "a150", "a151", "a152", "a153", "a154", "a155", "a156", "a157", "a158", "a159", "a160", "a161", "a162", "a163", "a164", "a165", "a166", "a167", "a168", "a169", "a170", "a171", "a172", "a173", "a174", "a175", "a176", "a177", "a178", "a179", "a180", "a181", "a182", "a183", "a184", "a185", "a186", "a187", "a188", "a189", "a190", "a191", "a192", "a193", "a194", "a195", "a196", "a197", "a198", "a199", "a200", "a201", "a202", "a203", "a204", "a205", "a206", "a207", "a208", "a209", "a210", "a211", "a212", "a213", "a214", "a215", "a216", "a217", "a218", "a219", "a220", "a221", "a222", "a223", "a224", "a225", "a226", "a227", "a228", "a229", "a230", "a231", "a232", "a233", "a234", "a235", "a236", "a237", "a238", "a239", "a240", "a241", "a242", "a243", "a244", "a245", "a246", "a247", "a248", "a249", "a250", "a251", "a252", "a253", "a254", "a255", "s36", "s37", "s38", "s39", "s40", "s41", "s42", "s43", "s44", "s45", "s46", "s47", "s48", "s49", "s50", "s51", "s52", "s53", "s54", "s55", "s56", "s57", "s58", "s59", "s60", "s61", "s62", "s63", "s64", "s65", "s66", "s67", "s68", "s69", "s70", "s71", "s72", "s73", "s74", "s75", "s76", "s77", "s78", "s79", "s80", "s81", "s82", "s83", "s84", "s85", "s86", "s87", "s88", "s89", "s90", "s91", "s92", "s93", "s94", "s95", "s96", "s97", "vcc", "scc", "memory"); }
    __builtin_unreachable();
  }
  if (kind < 3) {
    const unsigned sStep = 4;
    COMMON(kind == 1 ? 8 : 16, kind == 1 ? fj * 128 : fj < 8 ? 1024 + fj * 128 : (fj & 2) * 1024 + (fj - 8) * 128, 1024, 131072, 2048, 256, kind == 1 ? 0 : 32768);
    const int sr = l15 - ((wave & 2) ? -1 : 1), sr2 = l15 - ((wave & 2) ? -2 : 2), nx = (slice >> 1) & 1;
    const unsigned vOutS = (sr2 & 16) ? 1024 + lg * 64 + (sr2 & 3) * 16 : (lg * 16 + sr2) * 16, vSp = (sr2 & 16) ? OOB : vOutS;
    (void)vWr2;
    const unsigned vAn = kind == 1 ? HU + vL16 : (sr & 16) ? 1024 + lg * 64 + (sr & 3) * 16 : (lg * 16 + sr) * 16;
    const unsigned vAe = kind == 1 ? ((l15 >> 2) == (nx ? 0 : 3) ? (nx ? 0 : 2 * HU) + 1024 + lg * 64 + (l15 & 3) * 16 : OOB) : (sr & 16) ? OOB : vAn;
    const unsigned vAux0 = l15 * 16, vAux1 = 139264 + (l15 * 2 + (lg & 1)) * 16 + wave * 4 + (lg >> 1) * 512, vAux2 = 139264 + (l15 * 2 + (lg & 1)) * 16;
    const unsigned vAux3 = (wave == 0 && lg < 2) ? slice * 128 + l15 * 8 + lg * 4 : OOB;
    const unsigned sOutOff = kind == 1 ? OFF_HS - HU + (slice * 4 + wave) * 1280 : OFF_X1 + (slice * 4 + wave) * 1024, sFoutOff = (kind == 1 ? OFF_FH : OFF_FX1) + slice * 128;
    const unsigned sAuxOff = OFF_XP + (slice * 4 + wave) * 1024, sBuf = kind, sWw = 16 * wave * 2048 + slice * 256;
    const u16* w12 = kind == 1 ? w1 : w2;
    const unsigned sExp = __builtin_amdgcn_readfirstlane(ep);
    unsigned epOut;
    asm volatile(
R"(.macro M d,w,x
v_mfma_f32_16x16x32_bf16 v[\d:\d+3], \w, v[\x:\x+3], v[\d:\d+3]
.endm
.macro R d,a,o
ds_read_b128 v[\d:\d+3], v\a offset:\o
.endm
.macro T d,a,o
ds_read_b64_tr_b16 \d, v\a offset:\o
.endm
.macro D a,s,o
buffer_load_dwordx4 v\a, s[36:39], s\s offen offset:\o sc1 lds
.endm
.macro L c
s_waitcnt lgkmcnt(\c)
.endm
Lcode_g12_b:
s_cmp_eq_u32 %9,2
s_cbranch_scc1 Lg2_begin_%=
s_mov_b64 s[36:37],%1
s_mov_b32 s38,111243392
s_mov_b32 s39,0x00020000
s_mov_b32 s44,%2
s_mov_b32 s45,%3
s_mov_b32 s46,%4
s_mov_b32 s47,%5
s_mov_b32 s48,%6
s_mov_b32 s49,%7
s_mov_b32 s50,%8
s_mov_b32 s51,%9
s_mov_b32 s53,%10
v_mov_b32 v0,%11
v_mov_b32 v3,%12
v_mov_b32 v5,%13
v_mov_b32 v6,%14
v_mov_b32 v18,%15
v_mov_b32 v19,%16
v_mov_b32 v20,%17
v_mov_b32 v21,%18
v_mov_b32 v9,%19
v_mov_b32 v10,%20
v_mov_b32 v11,%21
v_mov_b32 v12,%22
v_mov_b32 v13,%23
v_mov_b32 v14,%24
v_mov_b32 v15,%25
v_mov_b32 v16,%26
s_mov_b64 s[40:41],%27
s_mov_b32 s42,2097152
s_mov_b32 s43,0x00020000
s_lshl_b32 s57,s44,11
s_add_u32 s55,s50,0
s_add_u32 m0,s57,0
s_nop 0
buffer_load_dwordx4 v18,s[40:43],s55 offen offset:0  lds
s_add_u32 m0,s57,1024
s_nop 0
buffer_load_dwordx4 v19,s[40:43],s55 offen offset:0  lds
s_add_u32 m0,s57,8064
s_nop 0
buffer_load_dwordx4 v18,s[40:43],s55 offen offset:128  lds
s_add_u32 m0,s57,9088
s_nop 0
buffer_load_dwordx4 v19,s[40:43],s55 offen offset:128  lds
s_add_u32 s55,s50,131072
s_add_u32 m0,s57,16384
s_nop 0
buffer_load_dwordx4 v18,s[40:43],s55 offen offset:0  lds
s_add_u32 m0,s57,17408
s_nop 0
buffer_load_dwordx4 v19,s[40:43],s55 offen offset:0  lds
s_add_u32 m0,s57,24448
s_nop 0
buffer_load_dwordx4 v18,s[40:43],s55 offen offset:128  lds
s_add_u32 m0,s57,25472
s_nop 0
buffer_load_dwordx4 v19,s[40:43],s55 offen offset:128  lds
s_add_u32 s55,s50,262144
s_add_u32 m0,s57,32768
s_nop 0
buffer_load_dwordx4 v18,s[40:43],s55 offen offset:0  lds
s_add_u32 m0,s57,33792
s_nop 0
buffer_load_dwordx4 v19,s[40:43],s55 offen offset:0  lds
s_add_u32 m0,s57,40832
s_nop 0
buffer_load_dwordx4 v18,s[40:43],s55 offen offset:128  lds
s_add_u32 m0,s57,41856
s_nop 0
buffer_load_dwordx4 v19,s[40:43],s55 offen offset:128  lds
s_add_u32 s55,s50,393216
s_add_u32 m0,s57,49152
s_nop 0
buffer_load_dwordx4 v18,s[40:43],s55 offen offset:0  lds
s_add_u32 m0,s57,50176
s_nop 0
buffer_load_dwordx4 v19,s[40:43],s55 offen offset:0  lds
s_add_u32 m0,s57,57216
s_nop 0
buffer_load_dwordx4 v18,s[40:43],s55 offen offset:128  lds
s_add_u32 m0,s57,58240
s_nop 0
buffer_load_dwordx4 v19,s[40:43],s55 offen offset:128  lds
s_add_u32 s55,s50,524288
s_add_u32 m0,s57,65536
s_nop 0
buffer_load_dwordx4 v18,s[40:43],s55 offen offset:0  lds
s_add_u32 m0,s57,66560
s_nop 0
buffer_load_dwordx4 v19,s[40:43],s55 offen offset:0  lds
s_add_u32 m0,s57,73600
s_nop 0
buffer_load_dwordx4 v18,s[40:43],s55 offen offset:128  lds
s_add_u32 m0,s57,74624
s_nop 0
buffer_load_dwordx4 v19,s[40:43],s55 offen offset:128  lds
s_add_u32 s55,s50,655360
s_add_u32 m0,s57,81920
s_nop 0
buffer_load_dwordx4 v18,s[40:43],s55 offen offset:0  lds
s_add_u32 m0,s57,82944
s_nop 0
buffer_load_dwordx4 v19,s[40:43],s55 offen offset:0  lds
s_add_u32 m0,s57,89984
s_nop 0
buffer_load_dwordx4 v18,s[40:43],s55 offen offset:128  lds
s_add_u32 m0,s57,91008
s_nop 0
buffer_load_dwordx4 v19,s[40:43],s55 offen offset:128  lds
s_waitcnt vmcnt(20) lgkmcnt(0)
s_barrier
s_add_u32 s55,s50,786432
s_add_u32 m0,s57,98304
s_nop 0
buffer_load_dwordx4 v18,s[40:43],s55 offen offset:0  lds
s_add_u32 m0,s57,99328
s_nop 0
buffer_load_dwordx4 v19,s[40:43],s55 offen offset:0  lds
s_add_u32 m0,s57,106368
s_nop 0
buffer_load_dwordx4 v18,s[40:43],s55 offen offset:128  lds
s_add_u32 m0,s57,107392
s_nop 0
buffer_load_dwordx4 v19,s[40:43],s55 offen offset:128  lds
v_add_u32 v26,0,v20
v_add_u32 v27,0,v21
T a[0:1],26,0
T a[2:3],26,512
T a[4:5],27,0
T a[6:7],27,512
T a[8:9],26,4096
T a[10:11],26,4608
T a[12:13],27,4096
T a[14:15],27,4608
s_waitcnt vmcnt(20) lgkmcnt(0)
s_barrier
s_add_u32 s55,s50,917504
s_add_u32 m0,s57,0
s_nop 0
buffer_load_dwordx4 v18,s[40:43],s55 offen offset:0  lds
s_add_u32 m0,s57,1024
s_nop 0
buffer_load_dwordx4 v19,s[40:43],s55 offen offset:0  lds
s_add_u32 m0,s57,8064
s_nop 0
buffer_load_dwordx4 v18,s[40:43],s55 offen offset:128  lds
s_add_u32 m0,s57,9088
s_nop 0
buffer_load_dwordx4 v19,s[40:43],s55 offen offset:128  lds
v_add_u32 v26,16384,v20
v_add_u32 v27,16384,v21
T a[16:17],26,0
T a[18:19],26,512
T a[20:21],27,0
T a[22:23],27,512
T a[24:25],26,4096
T a[26:27],26,4608
T a[28:29],27,4096
T a[30:31],27,4608
s_waitcnt vmcnt(20) lgkmcnt(0)
s_barrier
s_add_u32 s55,s50,1048576
s_add_u32 m0,s57,16384
s_nop 0
buffer_load_dwordx4 v18,s[40:43],s55 offen offset:0  lds
s_add_u32 m0,s57,17408
s_nop 0
buffer_load_dwordx4 v19,s[40:43],s55 offen offset:0  lds
s_add_u32 m0,s57,24448
s_nop 0
buffer_load_dwordx4 v18,s[40:43],s55 offen offset:128  lds
s_add_u32 m0,s57,25472
s_nop 0
buffer_load_dwordx4 v19,s[40:43],s55 offen offset:128  lds
v_add_u32 v26,32768,v20
v_add_u32 v27,32768,v21
T a[32:33],26,0
T a[34:35],26,512
T a[36:37],27,0
T a[38:39],27,512
T a[40:41],26,4096
T a[42:43],26,4608
T a[44:45],27,4096
T a[46:47],27,4608
s_waitcnt vmcnt(20) lgkmcnt(0)
s_barrier
s_add_u32 s55,s50,1179648
s_add_u32 m0,s57,32768
s_nop 0
buffer_load_dwordx4 v18,s[40:43],s55 offen offset:0  lds
s_add_u32 m0,s57,33792
s_nop 0
buffer_load_dwordx4 v19,s[40:43],s55 offen offset:0  lds
s_add_u32 m0,s57,40832
s_nop 0
buffer_load_dwordx4 v18,s[40:43],s55 offen offset:128  lds
s_add_u32 m0,s57,41856
s_nop 0
buffer_load_dwordx4 v19,s[40:43],s55 offen offset:128  lds
v_add_u32 v26,49152,v20
v_add_u32 v27,49152,v21
T a[48:49],26,0
T a[50:51],26,512
T a[52:53],27,0
T a[54:55],27,512
T a[56:57],26,4096
T a[58:59],26,4608
T a[60:61],27,4096
T a[62:63],27,4608
s_waitcnt vmcnt(20) lgkmcnt(0)
s_barrier
s_add_u32 s55,s50,1310720
s_add_u32 m0,s57,49152
s_nop 0
buffer_load_dwordx4 v18,s[40:43],s55 offen offset:0  lds
s_add_u32 m0,s57,50176
s_nop 0
buffer_load_dwordx4 v19,s[40:43],s55 offen offset:0  lds
s_add_u32 m0,s57,57216
s_nop 0
buffer_load_dwordx4 v18,s[40:43],s55 offen offset:128  lds
s_add_u32 m0,s57,58240
s_nop 0
buffer_load_dwordx4 v19,s[40:43],s55 offen offset:128  lds
v_add_u32 v26,65536,v20
v_add_u32 v27,65536,v21
T a[64:65],26,0
T a[66:67],26,512
T a[68:69],27,0
T a[70:71],27,512
T a[72:73],26,4096
T a[74:75],26,4608
T a[76:77],27,4096
T a[78:79],27,4608
s_waitcnt vmcnt(20) lgkmcnt(0)
s_barrier
s_add_u32 s55,s50,1441792
s_add_u32 m0,s57,65536
s_nop 0
buffer_load_dwordx4 v18,s[40:43],s55 offen offset:0  lds
s_add_u32 m0,s57,66560
s_nop 0
buffer_load_dwordx4 v19,s[40:43],s55 offen offset:0  lds
s_add_u32 m0,s57,73600
s_nop 0
buffer_load_dwordx4 v18,s[40:43],s55 offen offset:128  lds
s_add_u32 m0,s57,74624
s_nop 0
buffer_load_dwordx4 v19,s[40:43],s55 offen offset:128  lds
v_add_u32 v26,81920,v20
v_add_u32 v27,81920,v21
T a[80:81],26,0
T a[82:83],26,512
T a[84:85],27,0
T a[86:87],27,512
T a[88:89],26,4096
T a[90:91],26,4608
T a[92:93],27,4096
T a[94:95],27,4608
s_waitcnt vmcnt(20) lgkmcnt(0)
s_barrier
s_add_u32 s55,s50,1572864
s_add_u32 m0,s57,81920
s_nop 0
buffer_load_dwordx4 v18,s[40:43],s55 offen offset:0  lds
s_add_u32 m0,s57,82944
s_nop 0
buffer_load_dwordx4 v19,s[40:43],s55 offen offset:0  lds
s_add_u32 m0,s57,89984
s_nop 0
buffer_load_dwordx4 v18,s[40:43],s55 offen offset:128  lds
s_add_u32 m0,s57,91008
s_nop 0
buffer_load_dwordx4 v19,s[40:43],s55 offen offset:128  lds
v_add_u32 v26,98304,v20
v_add_u32 v27,98304,v21
T a[96:97],26,0
T a[98:99],26,512
T a[100:101],27,0
T a[102:103],27,512
T a[104:105],26,4096
T a[106:107],26,4608
T a[108:109],27,4096
T a[110:111],27,4608
s_waitcnt vmcnt(20) lgkmcnt(0)
s_barrier
s_add_u32 s55,s50,1703936
s_add_u32 m0,s57,98304
s_nop 0
buffer_load_dwordx4 v18,s[40:43],s55 offen offset:0  lds
s_add_u32 m0,s57,99328
s_nop 0
buffer_load_dwordx4 v19,s[40:43],s55 offen offset:0  lds
s_add_u32 m0,s57,106368
s_nop 0
buffer_load_dwordx4 v18,s[40:43],s55 offen offset:128  lds
s_add_u32 m0,s57,107392
s_nop 0
buffer_load_dwordx4 v19,s[40:43],s55 offen offset:128  lds
v_add_u32 v26,0,v20
v_add_u32 v27,0,v21
T a[112:113],26,0
T a[114:115],26,512
T a[116:117],27,0
T a[118:119],27,512
T a[120:121],26,4096
T a[122:123],26,4608
T a[124:125],27,4096
T a[126:127],27,4608
s_waitcnt vmcnt(20) lgkmcnt(0)
s_barrier
s_add_u32 s55,s50,1835008
s_add_u32 m0,s57,0
s_nop 0
buffer_load_dwordx4 v18,s[40:43],s55 offen offset:0  lds
s_add_u32 m0,s57,1024
s_nop 0
buffer_load_dwordx4 v19,s[40:43],s55 offen offset:0  lds
s_add_u32 m0,s57,8064
s_nop 0
buffer_load_dwordx4 v18,s[40:43],s55 offen offset:128  lds
s_add_u32 m0,s57,9088
s_nop 0
buffer_load_dwordx4 v19,s[40:43],s55 offen offset:128  lds
v_add_u32 v26,16384,v20
v_add_u32 v27,16384,v21
T a[128:129],26,0
T a[130:131],26,512
T a[132:133],27,0
T a[134:135],27,512
T a[136:137],26,4096
T a[138:139],26,4608
T a[140:141],27,4096
T a[142:143],27,4608
s_waitcnt vmcnt(20) lgkmcnt(0)
s_barrier
s_add_u32 s55,s50,1966080
s_add_u32 m0,s57,16384
s_nop 0
buffer_load_dwordx4 v18,s[40:43],s55 offen offset:0  lds
s_add_u32 m0,s57,17408
s_nop 0
buffer_load_dwordx4 v19,s[40:43],s55 offen offset:0  lds
s_add_u32 m0,s57,24448
s_nop 0
buffer_load_dwordx4 v18,s[40:43],s55 offen offset:128  lds
s_add_u32 m0,s57,25472
s_nop 0
buffer_load_dwordx4 v19,s[40:43],s55 offen offset:128  lds
v_add_u32 v26,32768,v20
v_add_u32 v27,32768,v21
T a[144:145],26,0
T a[146:147],26,512
T a[148:149],27,0
T a[150:151],27,512
T a[152:153],26,4096
T a[154:155],26,4608
T a[156:157],27,4096
T a[158:159],27,4608
s_waitcnt vmcnt(20) lgkmcnt(0)
s_barrier
v_add_u32 v26,49152,v20
v_add_u32 v27,49152,v21
T a[160:161],26,0
T a[162:163],26,512
T a[164:165],27,0
T a[166:167],27,512
T a[168:169],26,4096
T a[170:171],26,4608
T a[172:173],27,4096
T a[174:175],27,4608
s_waitcnt vmcnt(16) lgkmcnt(0)
s_barrier
v_add_u32 v26,65536,v20
v_add_u32 v27,65536,v21
T a[176:177],26,0
T a[178:179],26,512
T a[180:181],27,0
T a[182:183],27,512
T a[184:185],26,4096
T a[186:187],26,4608
T a[188:189],27,4096
T a[190:191],27,4608
s_waitcnt vmcnt(12) lgkmcnt(0)
s_barrier
v_add_u32 v26,81920,v20
v_add_u32 v27,81920,v21
T a[192:193],26,0
T a[194:195],26,512
T a[196:197],27,0
T a[198:199],27,512
T a[200:201],26,4096
T a[202:203],26,4608
T a[204:205],27,4096
T a[206:207],27,4608
s_waitcnt vmcnt(8) lgkmcnt(0)
s_barrier
v_add_u32 v26,98304,v20
v_add_u32 v27,98304,v21
T a[208:209],26,0
T a[210:211],26,512
T a[212:213],27,0
T a[214:215],27,512
T a[216:217],26,4096
T a[218:219],26,4608
T a[220:221],27,4096
T a[222:223],27,4608
s_waitcnt vmcnt(4) lgkmcnt(0)
s_barrier
v_add_u32 v26,0,v20
v_add_u32 v27,0,v21
T a[224:225],26,0
T a[226:227],26,512
T a[228:229],27,0
T a[230:231],27,512
T a[232:233],26,4096
T a[234:235],26,4608
T a[236:237],27,4096
T a[238:239],27,4608
s_waitcnt vmcnt(0) lgkmcnt(0)
s_barrier
v_add_u32 v26,16384,v20
v_add_u32 v27,16384,v21
T a[240:241],26,0
T a[242:243],26,512
T a[244:245],27,0
T a[246:247],27,512
T a[248:249],26,4096
T a[250:251],26,4608
T a[252:253],27,4096
T a[254:255],27,4608
L 0
s_barrier
v_mov_b32 v34,0x3f803f80
v_mov_b32 v35,0x3f803f80
v_mov_b32 v36,0x3f803f80
v_mov_b32 v37,0x3f803f80
s_nop 1
v_mfma_f32_16x16x32_bf16 v[106:109],a[0:3],v[34:37],0
v_mfma_f32_16x16x32_bf16 v[110:113],a[4:7],v[34:37],0
M 106,a[8:11],34
M 110,a[12:15],34
M 106,a[16:19],34
M 110,a[20:23],34
M 106,a[24:27],34
M 110,a[28:31],34
M 106,a[32:35],34
M 110,a[36:39],34
M 106,a[40:43],34
M 110,a[44:47],34
M 106,a[48:51],34
M 110,a[52:55],34
M 106,a[56:59],34
M 110,a[60:63],34
M 106,a[64:67],34
M 110,a[68:71],34
M 106,a[72:75],34
M 110,a[76:79],34
M 106,a[80:83],34
M 110,a[84:87],34
M 106,a[88:91],34
M 110,a[92:95],34
M 106,a[96:99],34
M 110,a[100:103],34
M 106,a[104:107],34
M 110,a[108:111],34
M 106,a[112:115],34
M 110,a[116:119],34
M 106,a[120:123],34
M 110,a[124:127],34
M 106,a[128:131],34
M 110,a[132:135],34
M 106,a[136:139],34
M 110,a[140:143],34
M 106,a[144:147],34
M 110,a[148:151],34
M 106,a[152:155],34
M 110,a[156:159],34
M 106,a[160:163],34
M 110,a[164:167],34
M 106,a[168:171],34
)"
R"(M 110,a[172:175],34
M 106,a[176:179],34
M 110,a[180:183],34
M 106,a[184:187],34
M 110,a[188:191],34
M 106,a[192:195],34
M 110,a[196:199],34
M 106,a[200:203],34
M 110,a[204:207],34
M 106,a[208:211],34
M 110,a[212:215],34
M 106,a[216:219],34
M 110,a[220:223],34
M 106,a[224:227],34
M 110,a[228:231],34
M 106,a[232:235],34
M 110,a[236:239],34
M 106,a[240:243],34
M 110,a[244:247],34
M 106,a[248:251],34
M 110,a[252:255],34
v_mov_b32 v4,s53
s_mov_b32 s82,0
s_mov_b32 s80,0
s_mov_b32 s54,s45
s_mul_i32 s57,s44,4096
s_mul_i32 s55,s45,32768
s_add_u32 s55,s55,s57
s_add_u32 s55,s55,8388608
s_mul_i32 s57,s44,4096
s_mul_i32 s60,s46,32768
s_mul_i32 s61,s46,1024
s_mul_i32 s62,s46,40960
s_mul_i32 s63,s46,1024
s_mul_i32 s64,s46,512
s_lshl_b32 s59,s44,8
s_add_u32 s59,s59,131072
s_mul_i32 s58,s45,1024
s_add_u32 s58,s58,109056000
s_mul_i32 s65,s45,40960
s_add_u32 s65,s65,s47
s_mul_i32 s68,s45,1024
s_add_u32 s68,s68,s48
s_mul_i32 s73,s45,512
s_add_u32 s73,s73,106954752
s_mul_i32 s74,s45,32768
s_add_u32 s74,s74,s49
s_mul_i32 s75,s45,1024
s_add_u32 s75,s75,107479040
s_mov_b32 s66,0x7fff0000
s_mov_b32 s67,0x7fff0000
s_mov_b32 s69,0x7fff0000
s_mov_b32 s76,0x7fff0000
s_mov_b32 s77,0x7fff0000
s_mov_b32 s70,0x7fff0000
s_mov_b32 s71,0x7fff0000
s_mov_b32 s72,0x7fff0000
v_mov_b32 v1,v0
v_add_u32 v2,16384,v0
s_mov_b32 s84,16384
s_mov_b32 s86,0x3a800000
s_mov_b32 s87,0x3727c5ac
s_mov_b32 s88,0xc0135761
s_mov_b32 s89,0x3d372713
s_lshr_b32 s85,s44,1
s_mul_i32 s85,s85,127
v_lshlrev_b32 v8,22,v0
v_and_b32 v8,0x80000000,v8
v_or_b32 v8,v8,v5
v_add_u32 v2,32768,v0
s_lshl_b32 s84,s57,1
s_add_u32 s84,s84,32768
s_getpc_b64 s[40:41]
Lg1_pc_%=:
s_lshl_b32 s78,s44,13
v_lshlrev_b32 v114,3,v0
v_add_u32 v114,s78,v114
v_min_u32 v114,Lcode_g2_b-Lg1_pc_%=-4,v114
s_mov_b32 m0,163584
s_nop 0
global_load_lds_dword v114,s[40:41]
s_add_u32 m0,s59,4096
s_nop 0
buffer_load_dword v8,s[36:39],s58 offen offset:0 sc1 lds
buffer_store_dword v4,v3,s[36:39],s71 offen offset:0 sc0 sc1
s_add_u32 m0,s59,5120
s_nop 0
buffer_load_dword v8,s[36:39],s58 offen offset:0 sc1 lds
buffer_store_dword v4,v3,s[36:39],s71 offen offset:0 sc0 sc1
s_mov_b32 s83,0
Lg1_poll_%=:
s_add_u32 s78,s83,2
s_and_b32 s78,s78,3
s_lshl_b32 s78,s78,10
s_add_u32 s78,s78,4096
s_add_u32 m0,s59,s78
s_nop 0
buffer_load_dword v8,s[36:39],s58 offen offset:0 sc1 lds
s_and_b32 s78,s83,3
s_lshl_b32 s78,s78,10
v_add_u32 v114,s78,v6
s_waitcnt vmcnt(4)
s_barrier
ds_read_b32 v7,v114 offset:4096
buffer_store_dword v4,v3,s[36:39],s70 offen offset:0 sc0 sc1
s_mov_b32 s70,s71
s_mov_b32 s71,s72
s_mov_b32 s72,0x7fff0000
s_add_u32 s83,s83,1
s_add_u32 s82,s82,1
L 0
v_cmp_eq_u32 vcc,s53,v7
s_nop 0
s_cmp_eq_u32 vcc_lo,-1
s_cbranch_scc1 Lg1_go_%=
s_cmp_lt_u32 s82,20000
s_cbranch_scc1 Lg1_poll_%=
Lg1_go_%=:
s_add_u32 m0,s57,0
s_add_u32 s56,s55,0
D 0,56,0
D 0,56,1024
D 0,56,2048
D 0,56,3072
s_add_u32 s58,s58,s61
s_add_u32 m0,s59,2048
s_nop 0
buffer_load_dword v8,s[36:39],s58 offen offset:0 sc1 lds
s_add_u32 m0,s57,16384
s_add_u32 s56,s55,16384
D 0,56,0
D 0,56,1024
D 0,56,2048
D 0,56,3072
s_add_u32 s55,s55,s60
s_sub_u32 s57,s84,s57
s_add_u32 s54,s54,s46
s_waitcnt vmcnt(4)
s_barrier
buffer_store_dword v4,v3,s[36:39],s70 offen offset:0 sc0 sc1
s_min_u32 s71,s71,s72
buffer_store_dword v4,v3,s[36:39],s71 offen offset:0 sc0 sc1
s_mov_b32 s70,0x7fff0000
s_mov_b32 s71,0x7fff0000
s_mov_b32 s72,0x7fff0000
s_cmp_eq_u32 s80,1
buffer_store_dword v4,v3,s[36:39],s71 offen offset:0 sc0 sc1
s_cbranch_scc1 Lg1_st1_%=
s_branch Lg1_st0_%=
Lg1_gox_%=:
s_add_u32 m0,s59,2048
s_nop 0
buffer_load_dword v8,s[36:39],s58 offen offset:0 sc1 lds
s_waitcnt vmcnt(0)
s_barrier
buffer_store_dword v4,v3,s[36:39],s70 offen offset:0 sc0 sc1
s_min_u32 s71,s71,s72
buffer_store_dword v4,v3,s[36:39],s71 offen offset:0 sc0 sc1
s_mov_b32 s70,0x7fff0000
s_mov_b32 s71,0x7fff0000
s_mov_b32 s72,0x7fff0000
s_cmp_eq_u32 s80,1
s_cbranch_scc1 Lg1_st1_%=
Lg1_st0_%=:
ds_read_b32 v7,v6 offset:2048
R 34,1,0
R 38,1,1024
R 42,1,2048
R 46,1,3072
R 50,1,4096
L 5
v_cmp_eq_u32 vcc,s53,v7
L 3
s_cmp_eq_u32 vcc_lo,-1
s_cbranch_scc1 Lg1_F0_%=
s_branch Lg1_L0_%=
Lg1_st1_%=:
ds_read_b32 v7,v6 offset:2048
R 34,2,0
R 38,2,1024
R 42,2,2048
R 46,2,3072
R 50,2,4096
L 5
v_cmp_eq_u32 vcc,s53,v7
L 3
s_cmp_eq_u32 vcc_lo,-1
s_cbranch_scc1 Lg1_F1_%=
s_branch Lg1_L1_%=
Lg1_F0_%=:
v_mfma_f32_16x16x32_bf16 v[22:25],a[4:7],v[34:37],0
s_add_u32 s58,s58,s61
s_add_u32 m0,s59,0
R 54,1,5120
M 22,a[12:15],38
buffer_load_dword v8,s[36:39],s58 offen offset:0 sc1 lds
v_mfma_f32_16x16x32_bf16 v[18:21],a[0:3],v[34:37],0
s_add_u32 m0,s57,0
R 58,1,6144
v_fma_f32 v26,-v102,v106,v26
M 18,a[8:11],38
L 3
s_add_u32 s56,s55,0
v_fma_f32 v27,-v102,v107,v27
M 18,a[16:19],42
s_mov_b32 s66,s65
R 62,1,7168
v_fma_f32 v28,-v102,v108,v28
M 18,a[24:27],46
D 0,56,0
M 22,a[20:23],42
s_add_u32 s65,s65,s62
R 66,1,8192
v_fma_f32 v29,-v102,v109,v29
v_mul_f32 v114,v26,v26
M 22,a[28:31],46
L 3
D 0,56,1024
M 22,a[36:39],50
s_add_u32 s54,s54,s46
R 70,1,9216
v_mul_f32 v115,v27,v27
M 22,a[44:47],54
v_mul_f32 v116,v28,v28
v_mul_f32 v117,v29,v29
M 18,a[32:35],50
R 74,1,10240
v_fma_f32 v114,v104,v114,v103
M 18,a[40:43],54
L 3
D 0,56,2048
M 18,a[48:51],58
R 78,1,11264
v_fma_f32 v115,v104,v115,v103
M 18,a[56:59],62
D 0,56,3072
v_fma_f32 v116,v104,v116,v103
M 22,a[52:55],58
s_add_u32 m0,s57,16384
R 82,1,12288
v_fma_f32 v117,v104,v117,v103
M 22,a[60:63],62
L 3
v_mul_f32 v114,v26,v114
v_mul_f32 v115,v27,v115
M 22,a[68:71],66
s_add_u32 s56,s55,16384
R 86,1,13312
v_mul_f32 v116,v28,v116
M 22,a[76:79],70
buffer_load_dwordx4 v[98:101],v11,s[36:39],s73 offen offset:0 sc1
M 18,a[64:67],66
s_add_u32 s73,s73,s64
R 90,1,14336
v_mul_f32 v117,v29,v117
M 18,a[72:75],70
L 3
v_exp_f32 v114,v114
v_fma_f32 v30,-v102,v110,v30
M 18,a[80:83],74
s_waitcnt vmcnt(9)
R 94,1,15360
v_fma_f32 v114,v114,v105,v105
M 18,a[88:91],78
s_barrier
v_exp_f32 v115,v115
M 22,a[84:87],74
s_add_u32 s55,s55,s60
R 34,1,16384
v_fma_f32 v115,v115,v105,v105
M 22,a[92:95],78
L 3
v_exp_f32 v116,v116
M 22,a[100:103],82
s_sub_u32 s57,s84,s57
R 38,1,17408
v_fma_f32 v116,v116,v105,v105
M 22,a[108:111],86
D 0,56,0
M 18,a[96:99],82
R 42,1,18432
v_exp_f32 v117,v117
M 18,a[104:107],86
L 3
v_rcp_f32 v114,v114
M 18,a[112:115],90
R 46,1,19456
v_fma_f32 v117,v117,v105,v105
M 18,a[120:123],94
v_rcp_f32 v115,v115
M 22,a[116:119],90
R 50,1,20480
v_mul_f32 v26,v26,v114
M 22,a[124:127],94
L 3
v_rcp_f32 v116,v116
M 22,a[132:135],34
R 54,1,21504
v_mul_f32 v27,v27,v115
M 22,a[140:143],38
D 0,56,1024
v_mul_f32 v28,v28,v116
M 18,a[128:131],34
R 58,1,22528
v_cvt_pk_bf16_f32 v122,v26,v27
M 18,a[136:139],38
L 3
v_rcp_f32 v117,v117
M 18,a[144:147],42
R 62,1,23552
v_mul_f32 v29,v29,v117
M 18,a[152:155],46
v_cvt_pk_bf16_f32 v123,v28,v29
v_fma_f32 v31,-v102,v111,v31
M 22,a[148:151],42
R 66,1,24576
v_fma_f32 v32,-v102,v112,v32
M 22,a[156:159],46
L 3
D 0,56,2048
v_fma_f32 v33,-v102,v113,v33
M 22,a[164:167],50
R 70,1,25600
v_mul_f32 v118,v30,v30
M 22,a[172:175],54
v_mul_f32 v119,v31,v31
v_mul_f32 v120,v32,v32
M 18,a[160:163],50
R 74,1,26624
v_mul_f32 v121,v33,v33
M 18,a[168:171],54
L 3
v_fma_f32 v118,v104,v118,v103
v_fma_f32 v119,v104,v119,v103
M 18,a[176:179],58
R 78,1,27648
v_fma_f32 v120,v104,v120,v103
M 18,a[184:187],62
v_fma_f32 v121,v104,v121,v103
v_mul_f32 v118,v30,v118
M 22,a[180:183],58
R 82,1,28672
v_exp_f32 v118,v118
M 22,a[188:191],62
L 3
D 0,56,3072
M 22,a[196:199],66
R 86,1,29696
v_mul_f32 v119,v31,v119
M 22,a[204:207],70
v_exp_f32 v119,v119
M 18,a[192:195],66
R 90,1,30720
v_mul_f32 v120,v32,v120
M 18,a[200:203],70
L 3
v_mul_f32 v121,v33,v121
v_fma_f32 v118,v118,v105,v105
M 18,a[208:211],74
s_waitcnt vmcnt(5)
R 94,1,31744
v_fma_f32 v119,v119,v105,v105
M 18,a[216:219],78
s_barrier
ds_read_b32 v7,v6 offset:0
v_exp_f32 v120,v120
M 22,a[212:215],74
R 34,2,0
v_fma_f32 v120,v120,v105,v105
M 22,a[220:223],78
L 4
buffer_store_dword v4,v3,s[36:39],s70 offen offset:0 sc0 sc1
v_exp_f32 v121,v121
M 22,a[228:231],82
s_mov_b32 s70,s69
R 38,2,1024
v_fma_f32 v121,v121,v105,v105
v_rcp_f32 v118,v118
v_rcp_f32 v119,v119
v_rcp_f32 v120,v120
v_rcp_f32 v121,v121
M 22,a[236:239],86
s_mov_b32 s69,s68
v_mul_f32 v30,v30,v118
v_mul_f32 v31,v31,v119
v_mul_f32 v32,v32,v120
v_mul_f32 v33,v33,v121
v_cvt_pk_bf16_f32 v124,v30,v31
v_cvt_pk_bf16_f32 v125,v32,v33
M 18,a[224:227],82
s_add_u32 s68,s68,s63
R 42,2,2048
buffer_store_dwordx4 v[122:125],v15,s[36:39],s67 offen offset:0 sc0 sc1
M 18,a[232:235],86
L 4
L 3
M 18,a[240:243],90
R 46,2,3072
v_cmp_eq_u32 vcc,s53,v7
M 18,a[248:251],94
buffer_store_dwordx4 v[122:125],v16,s[36:39],s67 offen offset:0 sc0 sc1
M 22,a[244:247],90
s_waitcnt vmcnt(7) lgkmcnt(2)
s_cmp_eq_u32 vcc_lo,-1
R 50,2,4096
M 22,a[252:255],94
s_cbranch_scc0 Lg1_L1_%=
Lg1_F1_%=:
v_mfma_f32_16x16x32_bf16 v[30:33],a[4:7],v[34:37],0
s_add_u32 s58,s58,s61
s_add_u32 m0,s59,1024
R 54,2,5120
M 30,a[12:15],38
buffer_load_dword v8,s[36:39],s58 offen offset:0 sc1 lds
v_mfma_f32_16x16x32_bf16 v[26:29],a[0:3],v[34:37],0
s_add_u32 m0,s57,0
R 58,2,6144
v_fma_f32 v18,-v98,v106,v18
M 26,a[8:11],38
L 3
s_add_u32 s56,s55,0
v_fma_f32 v19,-v98,v107,v19
M 26,a[16:19],42
s_mov_b32 s67,s65
R 62,2,7168
v_fma_f32 v20,-v98,v108,v20
M 26,a[24:27],46
D 0,56,0
M 30,a[20:23],42
s_add_u32 s65,s65,s62
R 66,2,8192
v_fma_f32 v21,-v98,v109,v21
v_mul_f32 v114,v18,v18
M 30,a[28:31],46
L 3
D 0,56,1024
M 30,a[36:39],50
s_add_u32 s54,s54,s46
R 70,2,9216
v_mul_f32 v115,v19,v19
M 30,a[44:47],54
v_mul_f32 v116,v20,v20
v_mul_f32 v117,v21,v21
M 26,a[32:35],50
R 74,2,10240
v_fma_f32 v114,v100,v114,v99
M 26,a[40:43],54
L 3
D 0,56,2048
M 26,a[48:51],58
R 78,2,11264
v_fma_f32 v115,v100,v115,v99
M 26,a[56:59],62
D 0,56,3072
v_fma_f32 v116,v100,v116,v99
M 30,a[52:55],58
s_add_u32 m0,s57,16384
R 82,2,12288
v_fma_f32 v117,v100,v117,v99
M 30,a[60:63],62
L 3
v_mul_f32 v114,v18,v114
v_mul_f32 v115,v19,v115
M 30,a[68:71],66
s_add_u32 s56,s55,16384
R 86,2,13312
v_mul_f32 v116,v20,v116
M 30,a[76:79],70
buffer_load_dwordx4 v[102:105],v11,s[36:39],s73 offen offset:0 sc1
M 26,a[64:67],66
s_add_u32 s73,s73,s64
R 90,2,14336
v_mul_f32 v117,v21,v117
M 26,a[72:75],70
L 3
v_exp_f32 v114,v114
v_fma_f32 v22,-v98,v110,v22
M 26,a[80:83],74
s_waitcnt vmcnt(9)
R 94,2,15360
v_fma_f32 v114,v114,v101,v101
M 26,a[88:91],78
s_barrier
v_exp_f32 v115,v115
M 30,a[84:87],74
s_add_u32 s55,s55,s60
R 34,2,16384
v_fma_f32 v115,v115,v101,v101
M 30,a[92:95],78
L 3
v_exp_f32 v116,v116
M 30,a[100:103],82
s_sub_u32 s57,s84,s57
R 38,2,17408
v_fma_f32 v116,v116,v101,v101
M 30,a[108:111],86
D 0,56,0
M 26,a[96:99],82
R 42,2,18432
v_exp_f32 v117,v117
)"
R"(M 26,a[104:107],86
L 3
v_rcp_f32 v114,v114
M 26,a[112:115],90
R 46,2,19456
v_fma_f32 v117,v117,v101,v101
M 26,a[120:123],94
v_rcp_f32 v115,v115
M 30,a[116:119],90
R 50,2,20480
v_mul_f32 v18,v18,v114
M 30,a[124:127],94
L 3
v_rcp_f32 v116,v116
M 30,a[132:135],34
R 54,2,21504
v_mul_f32 v19,v19,v115
M 30,a[140:143],38
D 0,56,1024
v_mul_f32 v20,v20,v116
M 26,a[128:131],34
R 58,2,22528
v_cvt_pk_bf16_f32 v122,v18,v19
M 26,a[136:139],38
L 3
v_rcp_f32 v117,v117
M 26,a[144:147],42
R 62,2,23552
v_mul_f32 v21,v21,v117
M 26,a[152:155],46
v_cvt_pk_bf16_f32 v123,v20,v21
v_fma_f32 v23,-v98,v111,v23
M 30,a[148:151],42
R 66,2,24576
v_fma_f32 v24,-v98,v112,v24
M 30,a[156:159],46
L 3
D 0,56,2048
v_fma_f32 v25,-v98,v113,v25
M 30,a[164:167],50
R 70,2,25600
v_mul_f32 v118,v22,v22
M 30,a[172:175],54
v_mul_f32 v119,v23,v23
v_mul_f32 v120,v24,v24
M 26,a[160:163],50
R 74,2,26624
v_mul_f32 v121,v25,v25
M 26,a[168:171],54
L 3
v_fma_f32 v118,v100,v118,v99
v_fma_f32 v119,v100,v119,v99
M 26,a[176:179],58
R 78,2,27648
v_fma_f32 v120,v100,v120,v99
M 26,a[184:187],62
v_fma_f32 v121,v100,v121,v99
v_mul_f32 v118,v22,v118
M 30,a[180:183],58
R 82,2,28672
v_exp_f32 v118,v118
M 30,a[188:191],62
L 3
D 0,56,3072
M 30,a[196:199],66
R 86,2,29696
v_mul_f32 v119,v23,v119
M 30,a[204:207],70
v_exp_f32 v119,v119
M 26,a[192:195],66
R 90,2,30720
v_mul_f32 v120,v24,v120
M 26,a[200:203],70
L 3
v_mul_f32 v121,v25,v121
v_fma_f32 v118,v118,v101,v101
M 26,a[208:211],74
s_waitcnt vmcnt(5)
R 94,2,31744
v_fma_f32 v119,v119,v101,v101
M 26,a[216:219],78
s_barrier
ds_read_b32 v7,v6 offset:1024
v_exp_f32 v120,v120
M 30,a[212:215],74
R 34,1,0
v_fma_f32 v120,v120,v101,v101
M 30,a[220:223],78
L 4
buffer_store_dword v4,v3,s[36:39],s70 offen offset:0 sc0 sc1
v_exp_f32 v121,v121
M 30,a[228:231],82
s_mov_b32 s70,s69
R 38,1,1024
v_fma_f32 v121,v121,v101,v101
v_rcp_f32 v118,v118
v_rcp_f32 v119,v119
v_rcp_f32 v120,v120
v_rcp_f32 v121,v121
M 30,a[236:239],86
s_mov_b32 s69,s68
v_mul_f32 v22,v22,v118
v_mul_f32 v23,v23,v119
v_mul_f32 v24,v24,v120
v_mul_f32 v25,v25,v121
v_cvt_pk_bf16_f32 v124,v22,v23
v_cvt_pk_bf16_f32 v125,v24,v25
M 26,a[224:227],82
s_add_u32 s68,s68,s63
R 42,1,2048
buffer_store_dwordx4 v[122:125],v15,s[36:39],s66 offen offset:0 sc0 sc1
M 26,a[232:235],86
L 4
L 3
M 26,a[240:243],90
R 46,1,3072
v_cmp_eq_u32 vcc,s53,v7
M 26,a[248:251],94
buffer_store_dwordx4 v[122:125],v16,s[36:39],s66 offen offset:0 sc0 sc1
M 30,a[244:247],90
s_waitcnt vmcnt(7) lgkmcnt(2)
s_cmp_eq_u32 vcc_lo,-1
R 50,1,4096
M 30,a[252:255],94
s_cbranch_scc1 Lg1_F0_%=
Lg1_L0_%=:
v_mfma_f32_16x16x32_bf16 v[22:25],a[4:7],v[34:37],0
s_add_u32 m0,s59,3072
s_mov_b32 s66,s65
R 54,1,5120
M 22,a[12:15],38
buffer_load_dword v8,s[36:39],s58 offen offset:0 sc1 lds
v_mfma_f32_16x16x32_bf16 v[18:21],a[0:3],v[34:37],0
s_add_u32 s65,s65,s62
R 58,1,6144
v_fma_f32 v26,-v102,v106,v26
M 18,a[8:11],38
L 3
v_fma_f32 v27,-v102,v107,v27
v_fma_f32 v28,-v102,v108,v28
M 18,a[16:19],42
R 62,1,7168
v_fma_f32 v29,-v102,v109,v29
M 18,a[24:27],46
v_mul_f32 v114,v26,v26
v_mul_f32 v115,v27,v27
M 22,a[20:23],42
R 66,1,8192
v_mul_f32 v116,v28,v28
M 22,a[28:31],46
L 3
v_mul_f32 v117,v29,v29
v_fma_f32 v114,v104,v114,v103
M 22,a[36:39],50
R 70,1,9216
v_fma_f32 v115,v104,v115,v103
M 22,a[44:47],54
v_fma_f32 v116,v104,v116,v103
v_fma_f32 v117,v104,v117,v103
M 18,a[32:35],50
R 74,1,10240
v_mul_f32 v114,v26,v114
M 18,a[40:43],54
L 3
v_exp_f32 v114,v114
M 18,a[48:51],58
R 78,1,11264
v_mul_f32 v115,v27,v115
M 18,a[56:59],62
v_exp_f32 v115,v115
M 22,a[52:55],58
R 82,1,12288
v_mul_f32 v116,v28,v116
M 22,a[60:63],62
L 3
v_exp_f32 v116,v116
M 22,a[68:71],66
R 86,1,13312
v_mul_f32 v117,v29,v117
M 22,a[76:79],70
buffer_load_dwordx4 v[98:101],v11,s[36:39],s73 offen offset:0 sc1
M 18,a[64:67],66
s_add_u32 s73,s73,s64
R 90,1,14336
v_fma_f32 v114,v114,v105,v105
M 18,a[72:75],70
L 3
v_exp_f32 v117,v117
M 18,a[80:83],74
s_waitcnt vmcnt(1)
R 94,1,15360
v_fma_f32 v115,v115,v105,v105
M 18,a[88:91],78
s_barrier
ds_read_b32 v7,v6 offset:3072
v_fma_f32 v116,v116,v105,v105
M 22,a[84:87],74
R 34,1,16384
v_fma_f32 v117,v117,v105,v105
M 22,a[92:95],78
L 4
buffer_store_dword v4,v3,s[36:39],s70 offen offset:0 sc0 sc1
M 22,a[100:103],82
s_mov_b32 s70,0x7fff0000
R 38,1,17408
v_fma_f32 v30,-v102,v110,v30
M 22,a[108:111],86
v_rcp_f32 v114,v114
M 18,a[96:99],82
R 42,1,18432
v_mul_f32 v26,v26,v114
M 18,a[104:107],86
L 4
L 3
v_cmp_eq_u32 vcc,s53,v7
M 18,a[112:115],90
R 46,1,19456
v_fma_f32 v31,-v102,v111,v31
M 18,a[120:123],94
v_rcp_f32 v115,v115
M 22,a[116:119],90
s_cmp_eq_u32 vcc_lo,-1
R 50,1,20480
v_mul_f32 v27,v27,v115
s_cbranch_scc0 Lg1_X0_%=
M 22,a[124:127],94
L 3
s_add_u32 m0,s57,0
s_add_u32 s56,s55,0
M 22,a[132:135],34
s_add_u32 s58,s58,s61
R 54,1,21504
v_cvt_pk_bf16_f32 v122,v26,v27
M 22,a[140:143],38
D 0,56,0
M 18,a[128:131],34
s_add_u32 s54,s54,s46
R 58,1,22528
v_fma_f32 v32,-v102,v112,v32
M 18,a[136:139],38
L 3
v_rcp_f32 v116,v116
M 18,a[144:147],42
R 62,1,23552
v_mul_f32 v28,v28,v116
M 18,a[152:155],46
D 0,56,1024
M 22,a[148:151],42
R 66,1,24576
v_fma_f32 v33,-v102,v113,v33
M 22,a[156:159],46
L 3
v_rcp_f32 v117,v117
M 22,a[164:167],50
R 70,1,25600
v_mul_f32 v29,v29,v117
M 22,a[172:175],54
D 0,56,2048
M 18,a[160:163],50
R 74,1,26624
v_cvt_pk_bf16_f32 v123,v28,v29
M 18,a[168:171],54
L 3
v_mul_f32 v118,v30,v30
v_mul_f32 v119,v31,v31
M 18,a[176:179],58
R 78,1,27648
v_mul_f32 v120,v32,v32
M 18,a[184:187],62
D 0,56,3072
M 22,a[180:183],58
s_add_u32 m0,s59,0
R 82,1,28672
v_mul_f32 v121,v33,v33
M 22,a[188:191],62
L 3
buffer_load_dword v8,s[36:39],s58 offen offset:0 sc1 lds
M 22,a[196:199],66
s_add_u32 m0,s57,16384
R 86,1,29696
v_fma_f32 v118,v104,v118,v103
M 22,a[204:207],70
s_add_u32 s56,s55,16384
v_fma_f32 v119,v104,v119,v103
v_fma_f32 v120,v104,v120,v103
M 18,a[192:195],66
s_add_u32 s55,s55,s60
R 90,1,30720
v_fma_f32 v121,v104,v121,v103
M 18,a[200:203],70
L 3
D 0,56,0
M 18,a[208:211],74
s_waitcnt vmcnt(1)
R 94,1,31744
v_mul_f32 v118,v30,v118
M 18,a[216:219],78
s_barrier
ds_read_b32 v7,v6 offset:0
v_mul_f32 v119,v31,v119
M 22,a[212:215],74
s_sub_u32 s57,s84,s57
R 34,2,0
v_mul_f32 v120,v32,v120
M 22,a[220:223],78
L 4
buffer_store_dword v4,v3,s[36:39],s70 offen offset:0 sc0 sc1
v_mul_f32 v121,v33,v121
v_exp_f32 v118,v118
v_exp_f32 v119,v119
v_exp_f32 v120,v120
v_exp_f32 v121,v121
M 22,a[228:231],82
s_mov_b32 s70,s69
R 38,2,1024
v_fma_f32 v118,v118,v105,v105
v_fma_f32 v119,v119,v105,v105
v_fma_f32 v120,v120,v105,v105
v_fma_f32 v121,v121,v105,v105
v_rcp_f32 v118,v118
v_rcp_f32 v119,v119
v_rcp_f32 v120,v120
v_rcp_f32 v121,v121
M 22,a[236:239],86
s_mov_b32 s69,s68
v_mul_f32 v30,v30,v118
v_mul_f32 v31,v31,v119
v_mul_f32 v32,v32,v120
v_mul_f32 v33,v33,v121
v_cvt_pk_bf16_f32 v124,v30,v31
v_cvt_pk_bf16_f32 v125,v32,v33
M 18,a[224:227],82
s_add_u32 s68,s68,s63
R 42,2,2048
buffer_store_dwordx4 v[122:125],v15,s[36:39],s67 offen offset:0 sc0 sc1
M 18,a[232:235],86
L 4
L 3
M 18,a[240:243],90
R 46,2,3072
v_cmp_eq_u32 vcc,s53,v7
M 18,a[248:251],94
D 0,56,1024
M 22,a[244:247],90
L 2
s_cmp_eq_u32 vcc_lo,-1
R 50,2,4096
buffer_store_dwordx4 v[122:125],v16,s[36:39],s67 offen offset:0 sc0 sc1
M 22,a[252:255],94
D 0,56,2048
D 0,56,3072
buffer_store_dword v4,v3,s[36:39],s71 offen offset:0 sc0 sc1
buffer_store_dword v4,v3,s[36:39],s71 offen offset:0 sc0 sc1
buffer_store_dword v4,v3,s[36:39],s71 offen offset:0 sc0 sc1
s_cbranch_scc1 Lg1_F1_%=
s_branch Lg1_L1_%=
Lg1_X0_%=:
M 22,a[124:127],94
L 3
s_add_u32 m0,s59,0
v_cvt_pk_bf16_f32 v122,v26,v27
M 22,a[132:135],34
R 54,1,21504
v_fma_f32 v32,-v102,v112,v32
M 22,a[140:143],38
buffer_load_dword v8,s[36:39],s58 offen offset:0 sc1 lds
M 18,a[128:131],34
R 58,1,22528
v_fma_f32 v33,-v102,v113,v33
M 18,a[136:139],38
L 3
v_rcp_f32 v116,v116
M 18,a[144:147],42
R 62,1,23552
v_mul_f32 v28,v28,v116
M 18,a[152:155],46
v_rcp_f32 v117,v117
M 22,a[148:151],42
R 66,1,24576
v_mul_f32 v29,v29,v117
M 22,a[156:159],46
L 3
v_cvt_pk_bf16_f32 v123,v28,v29
v_mul_f32 v118,v30,v30
M 22,a[164:167],50
R 70,1,25600
v_mul_f32 v119,v31,v31
M 22,a[172:175],54
v_mul_f32 v120,v32,v32
v_mul_f32 v121,v33,v33
M 18,a[160:163],50
R 74,1,26624
v_fma_f32 v118,v104,v118,v103
M 18,a[168:171],54
L 3
v_fma_f32 v119,v104,v119,v103
v_fma_f32 v120,v104,v120,v103
M 18,a[176:179],58
R 78,1,27648
v_fma_f32 v121,v104,v121,v103
M 18,a[184:187],62
v_mul_f32 v118,v30,v118
v_mul_f32 v119,v31,v119
M 22,a[180:183],58
R 82,1,28672
v_mul_f32 v120,v32,v120
M 22,a[188:191],62
L 3
v_exp_f32 v118,v118
M 22,a[196:199],66
R 86,1,29696
v_mul_f32 v121,v33,v121
M 22,a[204:207],70
v_exp_f32 v119,v119
M 18,a[192:195],66
R 90,1,30720
v_fma_f32 v118,v118,v105,v105
M 18,a[200:203],70
L 3
v_exp_f32 v120,v120
M 18,a[208:211],74
s_waitcnt vmcnt(0)
R 94,1,31744
v_fma_f32 v119,v119,v105,v105
M 18,a[216:219],78
s_barrier
buffer_store_dword v4,v3,s[36:39],s70 offen offset:0 sc0 sc1
M 22,a[212:215],74
s_mov_b32 s70,s69
ds_read_b32 v7,v6 offset:0
v_fma_f32 v120,v120,v105,v105
M 22,a[220:223],78
L 3
v_exp_f32 v121,v121
M 22,a[228:231],82
s_mov_b32 s69,s68
v_fma_f32 v121,v121,v105,v105
v_rcp_f32 v118,v118
v_rcp_f32 v119,v119
v_rcp_f32 v120,v120
v_rcp_f32 v121,v121
M 22,a[236:239],86
s_add_u32 s68,s68,s63
v_mul_f32 v30,v30,v118
v_mul_f32 v31,v31,v119
v_mul_f32 v32,v32,v120
v_mul_f32 v33,v33,v121
v_cvt_pk_bf16_f32 v124,v30,v31
v_cvt_pk_bf16_f32 v125,v32,v33
M 18,a[224:227],82
buffer_store_dwordx4 v[122:125],v15,s[36:39],s67 offen offset:0 sc0 sc1
M 18,a[232:235],86
L 1
M 18,a[240:243],90
L 0
v_cmp_eq_u32 vcc,s53,v7
M 18,a[248:251],94
buffer_store_dwordx4 v[122:125],v16,s[36:39],s67 offen offset:0 sc0 sc1
M 22,a[244:247],90
s_nop 0
M 22,a[252:255],94
s_mov_b32 s80,1
s_cmp_eq_u32 vcc_lo,-1
s_cbranch_scc0 Lg1_xn0_%=
s_add_u32 m0,s57,0
s_add_u32 s56,s55,0
D 0,56,0
v_fma_f32 v18,-v98,v106,v18
v_fma_f32 v19,-v98,v107,v19
v_fma_f32 v20,-v98,v108,v20
v_fma_f32 v21,-v98,v109,v21
v_mul_f32 v114,v18,v18
v_mul_f32 v115,v19,v19
v_mul_f32 v116,v20,v20
D 0,56,1024
v_mul_f32 v117,v21,v21
v_fma_f32 v114,v100,v114,v99
v_fma_f32 v115,v100,v115,v99
v_fma_f32 v116,v100,v116,v99
v_fma_f32 v117,v100,v117,v99
v_mul_f32 v114,v18,v114
v_mul_f32 v115,v19,v115
D 0,56,2048
v_mul_f32 v116,v20,v116
v_mul_f32 v117,v21,v117
v_exp_f32 v114,v114
v_exp_f32 v115,v115
v_exp_f32 v116,v116
v_exp_f32 v117,v117
v_fma_f32 v114,v114,v101,v101
D 0,56,3072
v_fma_f32 v115,v115,v101,v101
v_fma_f32 v116,v116,v101,v101
v_fma_f32 v117,v117,v101,v101
v_rcp_f32 v114,v114
v_rcp_f32 v115,v115
v_rcp_f32 v116,v116
v_rcp_f32 v117,v117
s_add_u32 s58,s58,s61
s_add_u32 m0,s57,16384
)"
R"(s_add_u32 s56,s55,16384
D 0,56,0
v_mul_f32 v18,v18,v114
v_mul_f32 v19,v19,v115
v_mul_f32 v20,v20,v116
v_mul_f32 v21,v21,v117
v_cvt_pk_bf16_f32 v122,v18,v19
v_cvt_pk_bf16_f32 v123,v20,v21
v_fma_f32 v22,-v98,v110,v22
D 0,56,1024
v_fma_f32 v23,-v98,v111,v23
v_fma_f32 v24,-v98,v112,v24
v_fma_f32 v25,-v98,v113,v25
v_mul_f32 v118,v22,v22
v_mul_f32 v119,v23,v23
v_mul_f32 v120,v24,v24
v_mul_f32 v121,v25,v25
D 0,56,2048
v_fma_f32 v118,v100,v118,v99
v_fma_f32 v119,v100,v119,v99
v_fma_f32 v120,v100,v120,v99
v_fma_f32 v121,v100,v121,v99
v_mul_f32 v118,v22,v118
v_mul_f32 v119,v23,v119
v_mul_f32 v120,v24,v120
D 0,56,3072
v_mul_f32 v121,v25,v121
v_exp_f32 v118,v118
v_exp_f32 v119,v119
v_exp_f32 v120,v120
v_exp_f32 v121,v121
v_fma_f32 v118,v118,v101,v101
v_fma_f32 v119,v119,v101,v101
s_add_u32 s55,s55,s60
s_sub_u32 s57,s84,s57
s_add_u32 s54,s54,s46
v_fma_f32 v120,v120,v101,v101
v_fma_f32 v121,v121,v101,v101
v_rcp_f32 v118,v118
v_rcp_f32 v119,v119
v_rcp_f32 v120,v120
v_rcp_f32 v121,v121
v_mul_f32 v22,v22,v118
v_mul_f32 v23,v23,v119
v_mul_f32 v24,v24,v120
v_mul_f32 v25,v25,v121
v_cvt_pk_bf16_f32 v124,v22,v23
v_cvt_pk_bf16_f32 v125,v24,v25
buffer_store_dwordx4 v[122:125],v15,s[36:39],s66 offen offset:0 sc0 sc1
buffer_store_dwordx4 v[122:125],v16,s[36:39],s66 offen offset:0 sc0 sc1
s_mov_b32 s72,s69
s_mov_b32 s69,0x7fff0000
s_mov_b32 s66,0x7fff0000
s_mov_b32 s76,0x7fff0000
s_branch Lg1_gox_%=
Lg1_xn0_%=:
s_cmp_ge_u32 s54,256
s_cbranch_scc1 Lg1_fin0_%=
s_add_u32 m0,s59,4096
s_nop 0
buffer_load_dword v8,s[36:39],s58 offen offset:0 sc1 lds
buffer_store_dword v4,v3,s[36:39],s71 offen offset:0 sc0 sc1
s_add_u32 m0,s59,5120
s_nop 0
buffer_load_dword v8,s[36:39],s58 offen offset:0 sc1 lds
buffer_store_dword v4,v3,s[36:39],s71 offen offset:0 sc0 sc1
s_mov_b32 s83,0
v_fma_f32 v18,-v98,v106,v18
v_fma_f32 v19,-v98,v107,v19
v_fma_f32 v20,-v98,v108,v20
v_fma_f32 v21,-v98,v109,v21
v_mul_f32 v114,v18,v18
v_mul_f32 v115,v19,v19
v_mul_f32 v116,v20,v20
v_mul_f32 v117,v21,v21
v_fma_f32 v114,v100,v114,v99
v_fma_f32 v115,v100,v115,v99
v_fma_f32 v116,v100,v116,v99
v_fma_f32 v117,v100,v117,v99
v_mul_f32 v114,v18,v114
v_mul_f32 v115,v19,v115
v_mul_f32 v116,v20,v116
v_mul_f32 v117,v21,v117
v_exp_f32 v114,v114
v_exp_f32 v115,v115
v_exp_f32 v116,v116
v_exp_f32 v117,v117
v_fma_f32 v114,v114,v101,v101
v_fma_f32 v115,v115,v101,v101
v_fma_f32 v116,v116,v101,v101
v_fma_f32 v117,v117,v101,v101
v_rcp_f32 v114,v114
v_rcp_f32 v115,v115
v_rcp_f32 v116,v116
v_rcp_f32 v117,v117
v_mul_f32 v18,v18,v114
v_mul_f32 v19,v19,v115
v_mul_f32 v20,v20,v116
v_mul_f32 v21,v21,v117
v_cvt_pk_bf16_f32 v122,v18,v19
v_cvt_pk_bf16_f32 v123,v20,v21
v_fma_f32 v22,-v98,v110,v22
v_fma_f32 v23,-v98,v111,v23
v_fma_f32 v24,-v98,v112,v24
v_fma_f32 v25,-v98,v113,v25
v_mul_f32 v118,v22,v22
v_mul_f32 v119,v23,v23
v_mul_f32 v120,v24,v24
v_mul_f32 v121,v25,v25
v_fma_f32 v118,v100,v118,v99
v_fma_f32 v119,v100,v119,v99
v_fma_f32 v120,v100,v120,v99
v_fma_f32 v121,v100,v121,v99
v_mul_f32 v118,v22,v118
v_mul_f32 v119,v23,v119
v_mul_f32 v120,v24,v120
v_mul_f32 v121,v25,v121
v_exp_f32 v118,v118
v_exp_f32 v119,v119
v_exp_f32 v120,v120
v_exp_f32 v121,v121
v_fma_f32 v118,v118,v101,v101
v_fma_f32 v119,v119,v101,v101
v_fma_f32 v120,v120,v101,v101
v_fma_f32 v121,v121,v101,v101
v_rcp_f32 v118,v118
v_rcp_f32 v119,v119
v_rcp_f32 v120,v120
v_rcp_f32 v121,v121
v_mul_f32 v22,v22,v118
v_mul_f32 v23,v23,v119
v_mul_f32 v24,v24,v120
v_mul_f32 v25,v25,v121
v_cvt_pk_bf16_f32 v124,v22,v23
v_cvt_pk_bf16_f32 v125,v24,v25
buffer_store_dwordx4 v[122:125],v15,s[36:39],s66 offen offset:0 sc0 sc1
buffer_store_dwordx4 v[122:125],v16,s[36:39],s66 offen offset:0 sc0 sc1
s_mov_b32 s72,s69
s_mov_b32 s69,0x7fff0000
s_mov_b32 s66,0x7fff0000
s_mov_b32 s76,0x7fff0000
s_branch Lg1_poll_%=
Lg1_fin0_%=:
v_fma_f32 v18,-v98,v106,v18
v_fma_f32 v19,-v98,v107,v19
v_fma_f32 v20,-v98,v108,v20
v_fma_f32 v21,-v98,v109,v21
v_mul_f32 v114,v18,v18
v_mul_f32 v115,v19,v19
v_mul_f32 v116,v20,v20
v_mul_f32 v117,v21,v21
v_fma_f32 v114,v100,v114,v99
v_fma_f32 v115,v100,v115,v99
v_fma_f32 v116,v100,v116,v99
v_fma_f32 v117,v100,v117,v99
v_mul_f32 v114,v18,v114
v_mul_f32 v115,v19,v115
v_mul_f32 v116,v20,v116
v_mul_f32 v117,v21,v117
v_exp_f32 v114,v114
v_exp_f32 v115,v115
v_exp_f32 v116,v116
v_exp_f32 v117,v117
v_fma_f32 v114,v114,v101,v101
v_fma_f32 v115,v115,v101,v101
v_fma_f32 v116,v116,v101,v101
v_fma_f32 v117,v117,v101,v101
v_rcp_f32 v114,v114
v_rcp_f32 v115,v115
v_rcp_f32 v116,v116
v_rcp_f32 v117,v117
v_mul_f32 v18,v18,v114
v_mul_f32 v19,v19,v115
v_mul_f32 v20,v20,v116
v_mul_f32 v21,v21,v117
v_cvt_pk_bf16_f32 v122,v18,v19
v_cvt_pk_bf16_f32 v123,v20,v21
v_fma_f32 v22,-v98,v110,v22
v_fma_f32 v23,-v98,v111,v23
v_fma_f32 v24,-v98,v112,v24
v_fma_f32 v25,-v98,v113,v25
v_mul_f32 v118,v22,v22
v_mul_f32 v119,v23,v23
v_mul_f32 v120,v24,v24
v_mul_f32 v121,v25,v25
v_fma_f32 v118,v100,v118,v99
v_fma_f32 v119,v100,v119,v99
v_fma_f32 v120,v100,v120,v99
v_fma_f32 v121,v100,v121,v99
v_mul_f32 v118,v22,v118
v_mul_f32 v119,v23,v119
v_mul_f32 v120,v24,v120
v_mul_f32 v121,v25,v121
v_exp_f32 v118,v118
v_exp_f32 v119,v119
v_exp_f32 v120,v120
v_exp_f32 v121,v121
v_fma_f32 v118,v118,v101,v101
v_fma_f32 v119,v119,v101,v101
v_fma_f32 v120,v120,v101,v101
v_fma_f32 v121,v121,v101,v101
v_rcp_f32 v118,v118
v_rcp_f32 v119,v119
v_rcp_f32 v120,v120
v_rcp_f32 v121,v121
v_mul_f32 v22,v22,v118
v_mul_f32 v23,v23,v119
v_mul_f32 v24,v24,v120
v_mul_f32 v25,v25,v121
v_cvt_pk_bf16_f32 v124,v22,v23
v_cvt_pk_bf16_f32 v125,v24,v25
buffer_store_dwordx4 v[122:125],v15,s[36:39],s66 offen offset:0 sc0 sc1
buffer_store_dwordx4 v[122:125],v16,s[36:39],s66 offen offset:0 sc0 sc1
s_mov_b32 s72,s69
s_mov_b32 s69,0x7fff0000
s_mov_b32 s66,0x7fff0000
s_mov_b32 s76,0x7fff0000
s_branch Lg1_done_%=
Lg1_L1_%=:
v_mfma_f32_16x16x32_bf16 v[30:33],a[4:7],v[34:37],0
s_add_u32 m0,s59,3072
s_mov_b32 s67,s65
R 54,2,5120
M 30,a[12:15],38
buffer_load_dword v8,s[36:39],s58 offen offset:0 sc1 lds
v_mfma_f32_16x16x32_bf16 v[26:29],a[0:3],v[34:37],0
s_add_u32 s65,s65,s62
R 58,2,6144
v_fma_f32 v18,-v98,v106,v18
M 26,a[8:11],38
L 3
v_fma_f32 v19,-v98,v107,v19
v_fma_f32 v20,-v98,v108,v20
M 26,a[16:19],42
R 62,2,7168
v_fma_f32 v21,-v98,v109,v21
M 26,a[24:27],46
v_mul_f32 v114,v18,v18
v_mul_f32 v115,v19,v19
M 30,a[20:23],42
R 66,2,8192
v_mul_f32 v116,v20,v20
M 30,a[28:31],46
L 3
v_mul_f32 v117,v21,v21
v_fma_f32 v114,v100,v114,v99
M 30,a[36:39],50
R 70,2,9216
v_fma_f32 v115,v100,v115,v99
M 30,a[44:47],54
v_fma_f32 v116,v100,v116,v99
v_fma_f32 v117,v100,v117,v99
M 26,a[32:35],50
R 74,2,10240
v_mul_f32 v114,v18,v114
M 26,a[40:43],54
L 3
v_exp_f32 v114,v114
M 26,a[48:51],58
R 78,2,11264
v_mul_f32 v115,v19,v115
M 26,a[56:59],62
v_exp_f32 v115,v115
M 30,a[52:55],58
R 82,2,12288
v_mul_f32 v116,v20,v116
M 30,a[60:63],62
L 3
v_exp_f32 v116,v116
M 30,a[68:71],66
R 86,2,13312
v_mul_f32 v117,v21,v117
M 30,a[76:79],70
buffer_load_dwordx4 v[102:105],v11,s[36:39],s73 offen offset:0 sc1
M 26,a[64:67],66
s_add_u32 s73,s73,s64
R 90,2,14336
v_fma_f32 v114,v114,v101,v101
M 26,a[72:75],70
L 3
v_exp_f32 v117,v117
M 26,a[80:83],74
s_waitcnt vmcnt(1)
R 94,2,15360
v_fma_f32 v115,v115,v101,v101
M 26,a[88:91],78
s_barrier
ds_read_b32 v7,v6 offset:3072
v_fma_f32 v116,v116,v101,v101
M 30,a[84:87],74
R 34,2,16384
v_fma_f32 v117,v117,v101,v101
M 30,a[92:95],78
L 4
buffer_store_dword v4,v3,s[36:39],s70 offen offset:0 sc0 sc1
M 30,a[100:103],82
s_mov_b32 s70,0x7fff0000
R 38,2,17408
v_fma_f32 v22,-v98,v110,v22
M 30,a[108:111],86
v_rcp_f32 v114,v114
M 26,a[96:99],82
R 42,2,18432
v_mul_f32 v18,v18,v114
M 26,a[104:107],86
L 4
L 3
v_cmp_eq_u32 vcc,s53,v7
M 26,a[112:115],90
R 46,2,19456
v_fma_f32 v23,-v98,v111,v23
M 26,a[120:123],94
v_rcp_f32 v115,v115
M 30,a[116:119],90
s_cmp_eq_u32 vcc_lo,-1
R 50,2,20480
v_mul_f32 v19,v19,v115
s_cbranch_scc0 Lg1_X1_%=
M 30,a[124:127],94
L 3
s_add_u32 m0,s57,0
s_add_u32 s56,s55,0
M 30,a[132:135],34
s_add_u32 s58,s58,s61
R 54,2,21504
v_cvt_pk_bf16_f32 v122,v18,v19
M 30,a[140:143],38
D 0,56,0
M 26,a[128:131],34
s_add_u32 s54,s54,s46
R 58,2,22528
v_fma_f32 v24,-v98,v112,v24
M 26,a[136:139],38
L 3
v_rcp_f32 v116,v116
M 26,a[144:147],42
R 62,2,23552
v_mul_f32 v20,v20,v116
M 26,a[152:155],46
D 0,56,1024
M 30,a[148:151],42
R 66,2,24576
v_fma_f32 v25,-v98,v113,v25
M 30,a[156:159],46
L 3
v_rcp_f32 v117,v117
M 30,a[164:167],50
R 70,2,25600
v_mul_f32 v21,v21,v117
M 30,a[172:175],54
D 0,56,2048
M 26,a[160:163],50
R 74,2,26624
v_cvt_pk_bf16_f32 v123,v20,v21
M 26,a[168:171],54
L 3
v_mul_f32 v118,v22,v22
v_mul_f32 v119,v23,v23
M 26,a[176:179],58
R 78,2,27648
v_mul_f32 v120,v24,v24
M 26,a[184:187],62
D 0,56,3072
M 30,a[180:183],58
s_add_u32 m0,s59,1024
R 82,2,28672
v_mul_f32 v121,v25,v25
M 30,a[188:191],62
L 3
buffer_load_dword v8,s[36:39],s58 offen offset:0 sc1 lds
M 30,a[196:199],66
s_add_u32 m0,s57,16384
R 86,2,29696
v_fma_f32 v118,v100,v118,v99
M 30,a[204:207],70
s_add_u32 s56,s55,16384
v_fma_f32 v119,v100,v119,v99
v_fma_f32 v120,v100,v120,v99
M 26,a[192:195],66
s_add_u32 s55,s55,s60
R 90,2,30720
v_fma_f32 v121,v100,v121,v99
M 26,a[200:203],70
L 3
D 0,56,0
M 26,a[208:211],74
s_waitcnt vmcnt(1)
R 94,2,31744
v_mul_f32 v118,v22,v118
M 26,a[216:219],78
s_barrier
ds_read_b32 v7,v6 offset:1024
v_mul_f32 v119,v23,v119
M 30,a[212:215],74
s_sub_u32 s57,s84,s57
R 34,1,0
v_mul_f32 v120,v24,v120
M 30,a[220:223],78
L 4
buffer_store_dword v4,v3,s[36:39],s70 offen offset:0 sc0 sc1
v_mul_f32 v121,v25,v121
v_exp_f32 v118,v118
v_exp_f32 v119,v119
v_exp_f32 v120,v120
v_exp_f32 v121,v121
M 30,a[228:231],82
s_mov_b32 s70,s69
R 38,1,1024
v_fma_f32 v118,v118,v101,v101
v_fma_f32 v119,v119,v101,v101
v_fma_f32 v120,v120,v101,v101
v_fma_f32 v121,v121,v101,v101
v_rcp_f32 v118,v118
v_rcp_f32 v119,v119
v_rcp_f32 v120,v120
v_rcp_f32 v121,v121
M 30,a[236:239],86
s_mov_b32 s69,s68
v_mul_f32 v22,v22,v118
v_mul_f32 v23,v23,v119
v_mul_f32 v24,v24,v120
v_mul_f32 v25,v25,v121
v_cvt_pk_bf16_f32 v124,v22,v23
v_cvt_pk_bf16_f32 v125,v24,v25
M 26,a[224:227],82
s_add_u32 s68,s68,s63
R 42,1,2048
buffer_store_dwordx4 v[122:125],v15,s[36:39],s66 offen offset:0 sc0 sc1
M 26,a[232:235],86
L 4
L 3
M 26,a[240:243],90
R 46,1,3072
v_cmp_eq_u32 vcc,s53,v7
M 26,a[248:251],94
D 0,56,1024
M 30,a[244:247],90
L 2
s_cmp_eq_u32 vcc_lo,-1
R 50,1,4096
buffer_store_dwordx4 v[122:125],v16,s[36:39],s66 offen offset:0 sc0 sc1
M 30,a[252:255],94
D 0,56,2048
D 0,56,3072
buffer_store_dword v4,v3,s[36:39],s71 offen offset:0 sc0 sc1
buffer_store_dword v4,v3,s[36:39],s71 offen offset:0 sc0 sc1
buffer_store_dword v4,v3,s[36:39],s71 offen offset:0 sc0 sc1
s_cbranch_scc1 Lg1_F0_%=
s_branch Lg1_L0_%=
Lg1_X1_%=:
M 30,a[124:127],94
L 3
s_add_u32 m0,s59,1024
v_cvt_pk_bf16_f32 v122,v18,v19
M 30,a[132:135],34
R 54,2,21504
v_fma_f32 v24,-v98,v112,v24
M 30,a[140:143],38
buffer_load_dword v8,s[36:39],s58 offen offset:0 sc1 lds
M 26,a[128:131],34
R 58,2,22528
v_fma_f32 v25,-v98,v113,v25
M 26,a[136:139],38
L 3
v_rcp_f32 v116,v116
M 26,a[144:147],42
R 62,2,23552
v_mul_f32 v20,v20,v116
M 26,a[152:155],46
v_rcp_f32 v117,v117
M 30,a[148:151],42
R 66,2,24576
v_mul_f32 v21,v21,v117
M 30,a[156:159],46
L 3
v_cvt_pk_bf16_f32 v123,v20,v21
v_mul_f32 v118,v22,v22
M 30,a[164:167],50
R 70,2,25600
v_mul_f32 v119,v23,v23
M 30,a[172:175],54
v_mul_f32 v120,v24,v24
v_mul_f32 v121,v25,v25
M 26,a[160:163],50
R 74,2,26624
v_fma_f32 v118,v100,v118,v99
M 26,a[168:171],54
L 3
v_fma_f32 v119,v100,v119,v99
v_fma_f32 v120,v100,v120,v99
M 26,a[176:179],58
R 78,2,27648
v_fma_f32 v121,v100,v121,v99
M 26,a[184:187],62
v_mul_f32 v118,v22,v118
v_mul_f32 v119,v23,v119
M 30,a[180:183],58
R 82,2,28672
v_mul_f32 v120,v24,v120
M 30,a[188:191],62
)"
R"(L 3
v_exp_f32 v118,v118
M 30,a[196:199],66
R 86,2,29696
v_mul_f32 v121,v25,v121
M 30,a[204:207],70
v_exp_f32 v119,v119
M 26,a[192:195],66
R 90,2,30720
v_fma_f32 v118,v118,v101,v101
M 26,a[200:203],70
L 3
v_exp_f32 v120,v120
M 26,a[208:211],74
s_waitcnt vmcnt(0)
R 94,2,31744
v_fma_f32 v119,v119,v101,v101
M 26,a[216:219],78
s_barrier
buffer_store_dword v4,v3,s[36:39],s70 offen offset:0 sc0 sc1
M 30,a[212:215],74
s_mov_b32 s70,s69
ds_read_b32 v7,v6 offset:1024
v_fma_f32 v120,v120,v101,v101
M 30,a[220:223],78
L 3
v_exp_f32 v121,v121
M 30,a[228:231],82
s_mov_b32 s69,s68
v_fma_f32 v121,v121,v101,v101
v_rcp_f32 v118,v118
v_rcp_f32 v119,v119
v_rcp_f32 v120,v120
v_rcp_f32 v121,v121
M 30,a[236:239],86
s_add_u32 s68,s68,s63
v_mul_f32 v22,v22,v118
v_mul_f32 v23,v23,v119
v_mul_f32 v24,v24,v120
v_mul_f32 v25,v25,v121
v_cvt_pk_bf16_f32 v124,v22,v23
v_cvt_pk_bf16_f32 v125,v24,v25
M 26,a[224:227],82
buffer_store_dwordx4 v[122:125],v15,s[36:39],s66 offen offset:0 sc0 sc1
M 26,a[232:235],86
L 1
M 26,a[240:243],90
L 0
v_cmp_eq_u32 vcc,s53,v7
M 26,a[248:251],94
buffer_store_dwordx4 v[122:125],v16,s[36:39],s66 offen offset:0 sc0 sc1
M 30,a[244:247],90
s_nop 0
M 30,a[252:255],94
s_mov_b32 s80,0
s_cmp_eq_u32 vcc_lo,-1
s_cbranch_scc0 Lg1_xn1_%=
s_add_u32 m0,s57,0
s_add_u32 s56,s55,0
D 0,56,0
v_fma_f32 v26,-v102,v106,v26
v_fma_f32 v27,-v102,v107,v27
v_fma_f32 v28,-v102,v108,v28
v_fma_f32 v29,-v102,v109,v29
v_mul_f32 v114,v26,v26
v_mul_f32 v115,v27,v27
v_mul_f32 v116,v28,v28
D 0,56,1024
v_mul_f32 v117,v29,v29
v_fma_f32 v114,v104,v114,v103
v_fma_f32 v115,v104,v115,v103
v_fma_f32 v116,v104,v116,v103
v_fma_f32 v117,v104,v117,v103
v_mul_f32 v114,v26,v114
v_mul_f32 v115,v27,v115
D 0,56,2048
v_mul_f32 v116,v28,v116
v_mul_f32 v117,v29,v117
v_exp_f32 v114,v114
v_exp_f32 v115,v115
v_exp_f32 v116,v116
v_exp_f32 v117,v117
v_fma_f32 v114,v114,v105,v105
D 0,56,3072
v_fma_f32 v115,v115,v105,v105
v_fma_f32 v116,v116,v105,v105
v_fma_f32 v117,v117,v105,v105
v_rcp_f32 v114,v114
v_rcp_f32 v115,v115
v_rcp_f32 v116,v116
v_rcp_f32 v117,v117
s_add_u32 s58,s58,s61
s_add_u32 m0,s57,16384
s_add_u32 s56,s55,16384
D 0,56,0
v_mul_f32 v26,v26,v114
v_mul_f32 v27,v27,v115
v_mul_f32 v28,v28,v116
v_mul_f32 v29,v29,v117
v_cvt_pk_bf16_f32 v122,v26,v27
v_cvt_pk_bf16_f32 v123,v28,v29
v_fma_f32 v30,-v102,v110,v30
D 0,56,1024
v_fma_f32 v31,-v102,v111,v31
v_fma_f32 v32,-v102,v112,v32
v_fma_f32 v33,-v102,v113,v33
v_mul_f32 v118,v30,v30
v_mul_f32 v119,v31,v31
v_mul_f32 v120,v32,v32
v_mul_f32 v121,v33,v33
D 0,56,2048
v_fma_f32 v118,v104,v118,v103
v_fma_f32 v119,v104,v119,v103
v_fma_f32 v120,v104,v120,v103
v_fma_f32 v121,v104,v121,v103
v_mul_f32 v118,v30,v118
v_mul_f32 v119,v31,v119
v_mul_f32 v120,v32,v120
D 0,56,3072
v_mul_f32 v121,v33,v121
v_exp_f32 v118,v118
v_exp_f32 v119,v119
v_exp_f32 v120,v120
v_exp_f32 v121,v121
v_fma_f32 v118,v118,v105,v105
v_fma_f32 v119,v119,v105,v105
s_add_u32 s55,s55,s60
s_sub_u32 s57,s84,s57
s_add_u32 s54,s54,s46
v_fma_f32 v120,v120,v105,v105
v_fma_f32 v121,v121,v105,v105
v_rcp_f32 v118,v118
v_rcp_f32 v119,v119
v_rcp_f32 v120,v120
v_rcp_f32 v121,v121
v_mul_f32 v30,v30,v118
v_mul_f32 v31,v31,v119
v_mul_f32 v32,v32,v120
v_mul_f32 v33,v33,v121
v_cvt_pk_bf16_f32 v124,v30,v31
v_cvt_pk_bf16_f32 v125,v32,v33
buffer_store_dwordx4 v[122:125],v15,s[36:39],s67 offen offset:0 sc0 sc1
buffer_store_dwordx4 v[122:125],v16,s[36:39],s67 offen offset:0 sc0 sc1
s_mov_b32 s72,s69
s_mov_b32 s69,0x7fff0000
s_mov_b32 s67,0x7fff0000
s_mov_b32 s77,0x7fff0000
s_branch Lg1_gox_%=
Lg1_xn1_%=:
s_cmp_ge_u32 s54,256
s_cbranch_scc1 Lg1_fin1_%=
s_add_u32 m0,s59,4096
s_nop 0
buffer_load_dword v8,s[36:39],s58 offen offset:0 sc1 lds
buffer_store_dword v4,v3,s[36:39],s71 offen offset:0 sc0 sc1
s_add_u32 m0,s59,5120
s_nop 0
buffer_load_dword v8,s[36:39],s58 offen offset:0 sc1 lds
buffer_store_dword v4,v3,s[36:39],s71 offen offset:0 sc0 sc1
s_mov_b32 s83,0
v_fma_f32 v26,-v102,v106,v26
v_fma_f32 v27,-v102,v107,v27
v_fma_f32 v28,-v102,v108,v28
v_fma_f32 v29,-v102,v109,v29
v_mul_f32 v114,v26,v26
v_mul_f32 v115,v27,v27
v_mul_f32 v116,v28,v28
v_mul_f32 v117,v29,v29
v_fma_f32 v114,v104,v114,v103
v_fma_f32 v115,v104,v115,v103
v_fma_f32 v116,v104,v116,v103
v_fma_f32 v117,v104,v117,v103
v_mul_f32 v114,v26,v114
v_mul_f32 v115,v27,v115
v_mul_f32 v116,v28,v116
v_mul_f32 v117,v29,v117
v_exp_f32 v114,v114
v_exp_f32 v115,v115
v_exp_f32 v116,v116
v_exp_f32 v117,v117
v_fma_f32 v114,v114,v105,v105
v_fma_f32 v115,v115,v105,v105
v_fma_f32 v116,v116,v105,v105
v_fma_f32 v117,v117,v105,v105
v_rcp_f32 v114,v114
v_rcp_f32 v115,v115
v_rcp_f32 v116,v116
v_rcp_f32 v117,v117
v_mul_f32 v26,v26,v114
v_mul_f32 v27,v27,v115
v_mul_f32 v28,v28,v116
v_mul_f32 v29,v29,v117
v_cvt_pk_bf16_f32 v122,v26,v27
v_cvt_pk_bf16_f32 v123,v28,v29
v_fma_f32 v30,-v102,v110,v30
v_fma_f32 v31,-v102,v111,v31
v_fma_f32 v32,-v102,v112,v32
v_fma_f32 v33,-v102,v113,v33
v_mul_f32 v118,v30,v30
v_mul_f32 v119,v31,v31
v_mul_f32 v120,v32,v32
v_mul_f32 v121,v33,v33
v_fma_f32 v118,v104,v118,v103
v_fma_f32 v119,v104,v119,v103
v_fma_f32 v120,v104,v120,v103
v_fma_f32 v121,v104,v121,v103
v_mul_f32 v118,v30,v118
v_mul_f32 v119,v31,v119
v_mul_f32 v120,v32,v120
v_mul_f32 v121,v33,v121
v_exp_f32 v118,v118
v_exp_f32 v119,v119
v_exp_f32 v120,v120
v_exp_f32 v121,v121
v_fma_f32 v118,v118,v105,v105
v_fma_f32 v119,v119,v105,v105
v_fma_f32 v120,v120,v105,v105
v_fma_f32 v121,v121,v105,v105
v_rcp_f32 v118,v118
v_rcp_f32 v119,v119
v_rcp_f32 v120,v120
v_rcp_f32 v121,v121
v_mul_f32 v30,v30,v118
v_mul_f32 v31,v31,v119
v_mul_f32 v32,v32,v120
v_mul_f32 v33,v33,v121
v_cvt_pk_bf16_f32 v124,v30,v31
v_cvt_pk_bf16_f32 v125,v32,v33
buffer_store_dwordx4 v[122:125],v15,s[36:39],s67 offen offset:0 sc0 sc1
buffer_store_dwordx4 v[122:125],v16,s[36:39],s67 offen offset:0 sc0 sc1
s_mov_b32 s72,s69
s_mov_b32 s69,0x7fff0000
s_mov_b32 s67,0x7fff0000
s_mov_b32 s77,0x7fff0000
s_branch Lg1_poll_%=
Lg1_fin1_%=:
v_fma_f32 v26,-v102,v106,v26
v_fma_f32 v27,-v102,v107,v27
v_fma_f32 v28,-v102,v108,v28
v_fma_f32 v29,-v102,v109,v29
v_mul_f32 v114,v26,v26
v_mul_f32 v115,v27,v27
v_mul_f32 v116,v28,v28
v_mul_f32 v117,v29,v29
v_fma_f32 v114,v104,v114,v103
v_fma_f32 v115,v104,v115,v103
v_fma_f32 v116,v104,v116,v103
v_fma_f32 v117,v104,v117,v103
v_mul_f32 v114,v26,v114
v_mul_f32 v115,v27,v115
v_mul_f32 v116,v28,v116
v_mul_f32 v117,v29,v117
v_exp_f32 v114,v114
v_exp_f32 v115,v115
v_exp_f32 v116,v116
v_exp_f32 v117,v117
v_fma_f32 v114,v114,v105,v105
v_fma_f32 v115,v115,v105,v105
v_fma_f32 v116,v116,v105,v105
v_fma_f32 v117,v117,v105,v105
v_rcp_f32 v114,v114
v_rcp_f32 v115,v115
v_rcp_f32 v116,v116
v_rcp_f32 v117,v117
v_mul_f32 v26,v26,v114
v_mul_f32 v27,v27,v115
v_mul_f32 v28,v28,v116
v_mul_f32 v29,v29,v117
v_cvt_pk_bf16_f32 v122,v26,v27
v_cvt_pk_bf16_f32 v123,v28,v29
v_fma_f32 v30,-v102,v110,v30
v_fma_f32 v31,-v102,v111,v31
v_fma_f32 v32,-v102,v112,v32
v_fma_f32 v33,-v102,v113,v33
v_mul_f32 v118,v30,v30
v_mul_f32 v119,v31,v31
v_mul_f32 v120,v32,v32
v_mul_f32 v121,v33,v33
v_fma_f32 v118,v104,v118,v103
v_fma_f32 v119,v104,v119,v103
v_fma_f32 v120,v104,v120,v103
v_fma_f32 v121,v104,v121,v103
v_mul_f32 v118,v30,v118
v_mul_f32 v119,v31,v119
v_mul_f32 v120,v32,v120
v_mul_f32 v121,v33,v121
v_exp_f32 v118,v118
v_exp_f32 v119,v119
v_exp_f32 v120,v120
v_exp_f32 v121,v121
v_fma_f32 v118,v118,v105,v105
v_fma_f32 v119,v119,v105,v105
v_fma_f32 v120,v120,v105,v105
v_fma_f32 v121,v121,v105,v105
v_rcp_f32 v118,v118
v_rcp_f32 v119,v119
v_rcp_f32 v120,v120
v_rcp_f32 v121,v121
v_mul_f32 v30,v30,v118
v_mul_f32 v31,v31,v119
v_mul_f32 v32,v32,v120
v_mul_f32 v33,v33,v121
v_cvt_pk_bf16_f32 v124,v30,v31
v_cvt_pk_bf16_f32 v125,v32,v33
buffer_store_dwordx4 v[122:125],v15,s[36:39],s67 offen offset:0 sc0 sc1
buffer_store_dwordx4 v[122:125],v16,s[36:39],s67 offen offset:0 sc0 sc1
s_mov_b32 s72,s69
s_mov_b32 s69,0x7fff0000
s_mov_b32 s67,0x7fff0000
s_mov_b32 s77,0x7fff0000
s_branch Lg1_done_%=
Lg1_done_%=:
s_waitcnt vmcnt(2)
s_barrier
buffer_store_dword v4,v3,s[36:39],s70 offen offset:0 sc0 sc1
s_waitcnt vmcnt(1)
s_barrier
buffer_store_dword v4,v3,s[36:39],s72 offen offset:0 sc0 sc1
s_waitcnt vmcnt(0) lgkmcnt(0)
s_barrier
s_branch Lg2_end_%=
Lg2_begin_%=:
Lcode_g2_b:
s_mov_b64 s[36:37],%1
s_mov_b32 s38,111243392
s_mov_b32 s39,0x00020000
s_mov_b32 s44,%2
s_mov_b32 s45,%3
s_mov_b32 s46,%4
s_mov_b32 s47,%5
s_mov_b32 s48,%6
s_mov_b32 s49,%7
s_mov_b32 s50,%8
s_mov_b32 s51,%9
s_mov_b32 s53,%10
v_mov_b32 v0,%11
v_mov_b32 v3,%12
v_mov_b32 v5,%13
v_mov_b32 v6,%14
v_mov_b32 v18,%15
v_mov_b32 v19,%16
v_mov_b32 v20,%17
v_mov_b32 v21,%18
v_mov_b32 v9,%19
v_mov_b32 v10,%20
v_mov_b32 v11,%21
v_mov_b32 v12,%22
v_mov_b32 v13,%23
v_mov_b32 v14,%24
v_mov_b32 v15,%25
v_mov_b32 v16,%26
s_mov_b64 s[40:41],%27
s_mov_b32 s42,2097152
s_mov_b32 s43,0x00020000
s_lshl_b32 s57,s44,11
s_cmp_ge_u32 s45,8
s_cbranch_scc1 Lg2_dly_%=
s_sleep 16
s_sleep 16
s_sleep 16
s_sleep 16
Lg2_dly_%=:
s_add_u32 s55,s50,0
s_add_u32 m0,s57,0
s_nop 0
buffer_load_dwordx4 v18,s[40:43],s55 offen offset:0  lds
s_add_u32 m0,s57,1024
s_nop 0
buffer_load_dwordx4 v19,s[40:43],s55 offen offset:0  lds
s_add_u32 m0,s57,8064
s_nop 0
buffer_load_dwordx4 v18,s[40:43],s55 offen offset:128  lds
s_add_u32 m0,s57,9088
s_nop 0
buffer_load_dwordx4 v19,s[40:43],s55 offen offset:128  lds
s_add_u32 s55,s50,131072
s_add_u32 m0,s57,16384
s_nop 0
buffer_load_dwordx4 v18,s[40:43],s55 offen offset:0  lds
s_add_u32 m0,s57,17408
s_nop 0
buffer_load_dwordx4 v19,s[40:43],s55 offen offset:0  lds
s_add_u32 m0,s57,24448
s_nop 0
buffer_load_dwordx4 v18,s[40:43],s55 offen offset:128  lds
s_add_u32 m0,s57,25472
s_nop 0
buffer_load_dwordx4 v19,s[40:43],s55 offen offset:128  lds
s_add_u32 s55,s50,262144
s_add_u32 m0,s57,32768
s_nop 0
buffer_load_dwordx4 v18,s[40:43],s55 offen offset:0  lds
s_add_u32 m0,s57,33792
s_nop 0
buffer_load_dwordx4 v19,s[40:43],s55 offen offset:0  lds
s_add_u32 m0,s57,40832
s_nop 0
buffer_load_dwordx4 v18,s[40:43],s55 offen offset:128  lds
s_add_u32 m0,s57,41856
s_nop 0
buffer_load_dwordx4 v19,s[40:43],s55 offen offset:128  lds
s_add_u32 s55,s50,393216
s_add_u32 m0,s57,49152
s_nop 0
buffer_load_dwordx4 v18,s[40:43],s55 offen offset:0  lds
s_add_u32 m0,s57,50176
s_nop 0
buffer_load_dwordx4 v19,s[40:43],s55 offen offset:0  lds
s_add_u32 m0,s57,57216
s_nop 0
buffer_load_dwordx4 v18,s[40:43],s55 offen offset:128  lds
s_add_u32 m0,s57,58240
s_nop 0
buffer_load_dwordx4 v19,s[40:43],s55 offen offset:128  lds
s_add_u32 s55,s50,524288
s_add_u32 m0,s57,65536
s_nop 0
buffer_load_dwordx4 v18,s[40:43],s55 offen offset:0  lds
s_add_u32 m0,s57,66560
s_nop 0
buffer_load_dwordx4 v19,s[40:43],s55 offen offset:0  lds
s_add_u32 m0,s57,73600
s_nop 0
buffer_load_dwordx4 v18,s[40:43],s55 offen offset:128  lds
s_add_u32 m0,s57,74624
s_nop 0
buffer_load_dwordx4 v19,s[40:43],s55 offen offset:128  lds
s_add_u32 s55,s50,655360
s_add_u32 m0,s57,81920
s_nop 0
buffer_load_dwordx4 v18,s[40:43],s55 offen offset:0  lds
s_add_u32 m0,s57,82944
s_nop 0
buffer_load_dwordx4 v19,s[40:43],s55 offen offset:0  lds
s_add_u32 m0,s57,89984
s_nop 0
buffer_load_dwordx4 v18,s[40:43],s55 offen offset:128  lds
s_add_u32 m0,s57,91008
s_nop 0
buffer_load_dwordx4 v19,s[40:43],s55 offen offset:128  lds
s_waitcnt vmcnt(20) lgkmcnt(0)
s_barrier
s_add_u32 s55,s50,786432
s_add_u32 m0,s57,98304
s_nop 0
buffer_load_dwordx4 v18,s[40:43],s55 offen offset:0  lds
s_add_u32 m0,s57,99328
s_nop 0
buffer_load_dwordx4 v19,s[40:43],s55 offen offset:0  lds
s_add_u32 m0,s57,106368
s_nop 0
buffer_load_dwordx4 v18,s[40:43],s55 offen offset:128  lds
s_add_u32 m0,s57,107392
s_nop 0
buffer_load_dwordx4 v19,s[40:43],s55 offen offset:128  lds
v_add_u32 v26,0,v20
v_add_u32 v27,0,v21
T a[0:1],26,0
T a[2:3],26,512
T a[4:5],27,0
T a[6:7],27,512
T a[8:9],26,4096
T a[10:11],26,4608
T a[12:13],27,4096
T a[14:15],27,4608
s_waitcnt vmcnt(20) lgkmcnt(0)
s_barrier
s_add_u32 s55,s50,917504
s_add_u32 m0,s57,0
s_nop 0
buffer_load_dwordx4 v18,s[40:43],s55 offen offset:0  lds
s_add_u32 m0,s57,1024
s_nop 0
buffer_load_dwordx4 v19,s[40:43],s55 offen offset:0  lds
s_add_u32 m0,s57,8064
s_nop 0
buffer_load_dwordx4 v18,s[40:43],s55 offen offset:128  lds
s_add_u32 m0,s57,9088
s_nop 0
buffer_load_dwordx4 v19,s[40:43],s55 offen offset:128  lds
v_add_u32 v26,16384,v20
v_add_u32 v27,16384,v21
T a[16:17],26,0
T a[18:19],26,512
T a[20:21],27,0
T a[22:23],27,512
T a[24:25],26,4096
T a[26:27],26,4608
T a[28:29],27,4096
T a[30:31],27,4608
s_waitcnt vmcnt(20) lgkmcnt(0)
s_barrier
s_add_u32 s55,s50,1048576
s_add_u32 m0,s57,16384
s_nop 0
buffer_load_dwordx4 v18,s[40:43],s55 offen offset:0  lds
s_add_u32 m0,s57,17408
s_nop 0
buffer_load_dwordx4 v19,s[40:43],s55 offen offset:0  lds
s_add_u32 m0,s57,24448
)"
R"(s_nop 0
buffer_load_dwordx4 v18,s[40:43],s55 offen offset:128  lds
s_add_u32 m0,s57,25472
s_nop 0
buffer_load_dwordx4 v19,s[40:43],s55 offen offset:128  lds
v_add_u32 v26,32768,v20
v_add_u32 v27,32768,v21
T a[32:33],26,0
T a[34:35],26,512
T a[36:37],27,0
T a[38:39],27,512
T a[40:41],26,4096
T a[42:43],26,4608
T a[44:45],27,4096
T a[46:47],27,4608
s_waitcnt vmcnt(20) lgkmcnt(0)
s_barrier
s_add_u32 s55,s50,1179648
s_add_u32 m0,s57,32768
s_nop 0
buffer_load_dwordx4 v18,s[40:43],s55 offen offset:0  lds
s_add_u32 m0,s57,33792
s_nop 0
buffer_load_dwordx4 v19,s[40:43],s55 offen offset:0  lds
s_add_u32 m0,s57,40832
s_nop 0
buffer_load_dwordx4 v18,s[40:43],s55 offen offset:128  lds
s_add_u32 m0,s57,41856
s_nop 0
buffer_load_dwordx4 v19,s[40:43],s55 offen offset:128  lds
v_add_u32 v26,49152,v20
v_add_u32 v27,49152,v21
T a[48:49],26,0
T a[50:51],26,512
T a[52:53],27,0
T a[54:55],27,512
T a[56:57],26,4096
T a[58:59],26,4608
T a[60:61],27,4096
T a[62:63],27,4608
s_waitcnt vmcnt(20) lgkmcnt(0)
s_barrier
s_add_u32 s55,s50,1310720
s_add_u32 m0,s57,49152
s_nop 0
buffer_load_dwordx4 v18,s[40:43],s55 offen offset:0  lds
s_add_u32 m0,s57,50176
s_nop 0
buffer_load_dwordx4 v19,s[40:43],s55 offen offset:0  lds
s_add_u32 m0,s57,57216
s_nop 0
buffer_load_dwordx4 v18,s[40:43],s55 offen offset:128  lds
s_add_u32 m0,s57,58240
s_nop 0
buffer_load_dwordx4 v19,s[40:43],s55 offen offset:128  lds
v_add_u32 v26,65536,v20
v_add_u32 v27,65536,v21
T a[64:65],26,0
T a[66:67],26,512
T a[68:69],27,0
T a[70:71],27,512
T a[72:73],26,4096
T a[74:75],26,4608
T a[76:77],27,4096
T a[78:79],27,4608
s_waitcnt vmcnt(20) lgkmcnt(0)
s_barrier
s_add_u32 s55,s50,1441792
s_add_u32 m0,s57,65536
s_nop 0
buffer_load_dwordx4 v18,s[40:43],s55 offen offset:0  lds
s_add_u32 m0,s57,66560
s_nop 0
buffer_load_dwordx4 v19,s[40:43],s55 offen offset:0  lds
s_add_u32 m0,s57,73600
s_nop 0
buffer_load_dwordx4 v18,s[40:43],s55 offen offset:128  lds
s_add_u32 m0,s57,74624
s_nop 0
buffer_load_dwordx4 v19,s[40:43],s55 offen offset:128  lds
v_add_u32 v26,81920,v20
v_add_u32 v27,81920,v21
T a[80:81],26,0
T a[82:83],26,512
T a[84:85],27,0
T a[86:87],27,512
T a[88:89],26,4096
T a[90:91],26,4608
T a[92:93],27,4096
T a[94:95],27,4608
s_waitcnt vmcnt(20) lgkmcnt(0)
s_barrier
s_add_u32 s55,s50,1572864
s_add_u32 m0,s57,81920
s_nop 0
buffer_load_dwordx4 v18,s[40:43],s55 offen offset:0  lds
s_add_u32 m0,s57,82944
s_nop 0
buffer_load_dwordx4 v19,s[40:43],s55 offen offset:0  lds
s_add_u32 m0,s57,89984
s_nop 0
buffer_load_dwordx4 v18,s[40:43],s55 offen offset:128  lds
s_add_u32 m0,s57,91008
s_nop 0
buffer_load_dwordx4 v19,s[40:43],s55 offen offset:128  lds
v_add_u32 v26,98304,v20
v_add_u32 v27,98304,v21
T a[96:97],26,0
T a[98:99],26,512
T a[100:101],27,0
T a[102:103],27,512
T a[104:105],26,4096
T a[106:107],26,4608
T a[108:109],27,4096
T a[110:111],27,4608
s_waitcnt vmcnt(20) lgkmcnt(0)
s_barrier
s_add_u32 s55,s50,1703936
s_add_u32 m0,s57,98304
s_nop 0
buffer_load_dwordx4 v18,s[40:43],s55 offen offset:0  lds
s_add_u32 m0,s57,99328
s_nop 0
buffer_load_dwordx4 v19,s[40:43],s55 offen offset:0  lds
s_add_u32 m0,s57,106368
s_nop 0
buffer_load_dwordx4 v18,s[40:43],s55 offen offset:128  lds
s_add_u32 m0,s57,107392
s_nop 0
buffer_load_dwordx4 v19,s[40:43],s55 offen offset:128  lds
v_add_u32 v26,0,v20
v_add_u32 v27,0,v21
T a[112:113],26,0
T a[114:115],26,512
T a[116:117],27,0
T a[118:119],27,512
T a[120:121],26,4096
T a[122:123],26,4608
T a[124:125],27,4096
T a[126:127],27,4608
s_waitcnt vmcnt(20) lgkmcnt(0)
s_barrier
s_add_u32 s55,s50,1835008
s_add_u32 m0,s57,0
s_nop 0
buffer_load_dwordx4 v18,s[40:43],s55 offen offset:0  lds
s_add_u32 m0,s57,1024
s_nop 0
buffer_load_dwordx4 v19,s[40:43],s55 offen offset:0  lds
s_add_u32 m0,s57,8064
s_nop 0
buffer_load_dwordx4 v18,s[40:43],s55 offen offset:128  lds
s_add_u32 m0,s57,9088
s_nop 0
buffer_load_dwordx4 v19,s[40:43],s55 offen offset:128  lds
v_add_u32 v26,16384,v20
v_add_u32 v27,16384,v21
T a[128:129],26,0
T a[130:131],26,512
T a[132:133],27,0
T a[134:135],27,512
T a[136:137],26,4096
T a[138:139],26,4608
T a[140:141],27,4096
T a[142:143],27,4608
s_waitcnt vmcnt(20) lgkmcnt(0)
s_barrier
s_add_u32 s55,s50,1966080
s_add_u32 m0,s57,16384
s_nop 0
buffer_load_dwordx4 v18,s[40:43],s55 offen offset:0  lds
s_add_u32 m0,s57,17408
s_nop 0
buffer_load_dwordx4 v19,s[40:43],s55 offen offset:0  lds
s_add_u32 m0,s57,24448
s_nop 0
buffer_load_dwordx4 v18,s[40:43],s55 offen offset:128  lds
s_add_u32 m0,s57,25472
s_nop 0
buffer_load_dwordx4 v19,s[40:43],s55 offen offset:128  lds
v_add_u32 v26,32768,v20
v_add_u32 v27,32768,v21
T a[144:145],26,0
T a[146:147],26,512
T a[148:149],27,0
T a[150:151],27,512
T a[152:153],26,4096
T a[154:155],26,4608
T a[156:157],27,4096
T a[158:159],27,4608
s_waitcnt vmcnt(20) lgkmcnt(0)
s_barrier
v_add_u32 v26,49152,v20
v_add_u32 v27,49152,v21
T a[160:161],26,0
T a[162:163],26,512
T a[164:165],27,0
T a[166:167],27,512
T a[168:169],26,4096
T a[170:171],26,4608
T a[172:173],27,4096
T a[174:175],27,4608
s_waitcnt vmcnt(16) lgkmcnt(0)
s_barrier
v_add_u32 v26,65536,v20
v_add_u32 v27,65536,v21
T a[176:177],26,0
T a[178:179],26,512
T a[180:181],27,0
T a[182:183],27,512
T a[184:185],26,4096
T a[186:187],26,4608
T a[188:189],27,4096
T a[190:191],27,4608
s_waitcnt vmcnt(12) lgkmcnt(0)
s_barrier
v_add_u32 v26,81920,v20
v_add_u32 v27,81920,v21
T a[192:193],26,0
T a[194:195],26,512
T a[196:197],27,0
T a[198:199],27,512
T a[200:201],26,4096
T a[202:203],26,4608
T a[204:205],27,4096
T a[206:207],27,4608
s_waitcnt vmcnt(8) lgkmcnt(0)
s_barrier
v_add_u32 v26,98304,v20
v_add_u32 v27,98304,v21
T a[208:209],26,0
T a[210:211],26,512
T a[212:213],27,0
T a[214:215],27,512
T a[216:217],26,4096
T a[218:219],26,4608
T a[220:221],27,4096
T a[222:223],27,4608
s_waitcnt vmcnt(4) lgkmcnt(0)
s_barrier
v_add_u32 v26,0,v20
v_add_u32 v27,0,v21
T a[224:225],26,0
T a[226:227],26,512
T a[228:229],27,0
T a[230:231],27,512
T a[232:233],26,4096
T a[234:235],26,4608
T a[236:237],27,4096
T a[238:239],27,4608
s_waitcnt vmcnt(0) lgkmcnt(0)
s_barrier
v_add_u32 v26,16384,v20
v_add_u32 v27,16384,v21
T a[240:241],26,0
T a[242:243],26,512
T a[244:245],27,0
T a[246:247],27,512
T a[248:249],26,4096
T a[250:251],26,4608
T a[252:253],27,4096
T a[254:255],27,4608
L 0
s_barrier
v_mov_b32 v4,s53
s_mov_b32 s82,0
s_mov_b32 s80,0
s_mov_b32 s54,s45
s_mul_i32 s57,s44,5120
s_mul_i32 s55,s45,40960
s_add_u32 s55,s55,s57
s_add_u32 s55,s55,17825792
s_mul_i32 s57,s44,4096
s_mul_i32 s60,s46,40960
s_mul_i32 s61,s46,1024
s_mul_i32 s62,s46,32768
s_mul_i32 s63,s46,1024
s_mul_i32 s64,s46,512
s_lshl_b32 s59,s44,8
s_add_u32 s59,s59,131072
s_mul_i32 s58,s45,1024
s_add_u32 s58,s58,109333504
s_mul_i32 s65,s45,32768
s_add_u32 s65,s65,s47
s_mul_i32 s68,s45,1024
s_add_u32 s68,s68,s48
s_mul_i32 s73,s45,512
s_add_u32 s73,s73,106954752
s_mul_i32 s74,s45,32768
s_add_u32 s74,s74,s49
s_mul_i32 s75,s45,1024
s_add_u32 s75,s75,107479040
s_mov_b32 s66,0x7fff0000
s_mov_b32 s67,0x7fff0000
s_mov_b32 s69,0x7fff0000
s_mov_b32 s76,0x7fff0000
s_mov_b32 s77,0x7fff0000
s_mov_b32 s70,0x7fff0000
s_mov_b32 s71,0x7fff0000
s_mov_b32 s72,0x7fff0000
v_mov_b32 v1,v0
v_add_u32 v2,16384,v0
s_mov_b32 s84,16384
s_mov_b32 s86,0x3a800000
s_mov_b32 s87,0x38a7c5ac
s_mov_b32 s88,0xc0135761
s_mov_b32 s89,0x3d372713
s_lshr_b32 s85,s44,1
s_mul_i32 s85,s85,127
v_lshlrev_b32 v8,22,v0
v_and_b32 v8,0x80000000,v8
v_or_b32 v8,v8,v5
v_add_u32 v2,32768,v0
s_lshl_b32 s84,s57,1
s_add_u32 s84,s84,32768
s_getpc_b64 s[40:41]
Lg2_pc_%=:
s_lshl_b32 s78,s44,13
v_lshlrev_b32 v106,3,v0
v_add_u32 v106,s78,v106
v_min_u32 v106,Lcode_g12_e-Lg2_pc_%=-4,v106
s_mov_b32 m0,163584
s_nop 0
global_load_lds_dword v106,s[40:41]
s_and_b32 s79,s54,127
s_cmp_eq_u32 s79,0
s_cselect_b32 s94,0x3300,0
s_cmp_eq_u32 s79,127
s_cselect_b32 s94,0xcc00,s94
s_add_u32 m0,s59,4096
s_nop 0
buffer_load_dword v8,s[36:39],s58 offen offset:0 sc1 lds
buffer_store_dword v4,v3,s[36:39],s71 offen offset:0 sc0 sc1
s_add_u32 m0,s59,5120
s_nop 0
buffer_load_dword v8,s[36:39],s58 offen offset:0 sc1 lds
buffer_store_dword v4,v3,s[36:39],s71 offen offset:0 sc0 sc1
s_mov_b32 s83,0
Lg2_poll_%=:
s_add_u32 s78,s83,2
s_and_b32 s78,s78,3
s_lshl_b32 s78,s78,10
s_add_u32 s78,s78,4096
s_add_u32 m0,s59,s78
s_nop 0
buffer_load_dword v8,s[36:39],s58 offen offset:0 sc1 lds
s_and_b32 s78,s83,3
s_lshl_b32 s78,s78,10
v_add_u32 v106,s78,v6
s_waitcnt vmcnt(4)
s_barrier
ds_read_b32 v7,v106 offset:4096
buffer_store_dword v4,v3,s[36:39],s70 offen offset:0 sc0 sc1
s_mov_b32 s70,s71
s_mov_b32 s71,s72
s_mov_b32 s72,0x7fff0000
s_add_u32 s83,s83,1
s_add_u32 s82,s82,1
L 0
v_cmp_eq_u32 vcc,s53,v7
s_nop 0
s_or_b64 vcc,vcc,s[94:95]
s_cmp_eq_u32 vcc_lo,-1
s_cbranch_scc1 Lg2_go_%=
s_cmp_lt_u32 s82,20000
s_cbranch_scc1 Lg2_poll_%=
Lg2_go_%=:
s_and_b32 s78,s54,127
s_cmp_eq_u32 s78,s85
s_cselect_b64 s[40:41],-1,0
v_cndmask_b32 v17,v15,v16,s[40:41]
s_add_u32 m0,s57,0
s_add_u32 s56,s55,0
D 17,56,0
s_add_u32 s56,s55,256
D 17,56,1024
s_add_u32 s56,s55,512
D 17,56,2048
s_add_u32 s56,s55,768
D 17,56,3072
s_add_u32 s58,s58,s61
s_add_u32 m0,s59,2048
s_nop 0
buffer_load_dword v8,s[36:39],s58 offen offset:0 sc1 lds
v_cndmask_b32 v17,v10,v9,s[40:41]
s_add_u32 m0,s57,16384
s_add_u32 s56,s55,20480
D 17,56,0
s_add_u32 s56,s55,20736
D 17,56,1024
s_add_u32 s56,s55,20992
D 17,56,2048
s_add_u32 s56,s55,21248
D 17,56,3072
s_add_u32 s55,s55,s60
s_sub_u32 s57,s84,s57
s_add_u32 s54,s54,s46
s_and_b32 s79,s54,127
s_cmp_eq_u32 s79,0
s_cselect_b32 s94,0x3300,0
s_cmp_eq_u32 s79,127
s_cselect_b32 s94,0xcc00,s94
s_waitcnt vmcnt(4)
s_barrier
buffer_store_dword v4,v3,s[36:39],s70 offen offset:0 sc0 sc1
s_min_u32 s71,s71,s72
buffer_store_dword v4,v3,s[36:39],s71 offen offset:0 sc0 sc1
s_mov_b32 s70,0x7fff0000
s_mov_b32 s71,0x7fff0000
s_mov_b32 s72,0x7fff0000
s_cmp_eq_u32 s80,1
buffer_store_dword v4,v3,s[36:39],s71 offen offset:0 sc0 sc1
s_cbranch_scc1 Lg2_st1_%=
s_branch Lg2_st0_%=
Lg2_gox_%=:
s_add_u32 m0,s59,2048
s_nop 0
buffer_load_dword v8,s[36:39],s58 offen offset:0 sc1 lds
s_and_b32 s79,s54,127
s_cmp_eq_u32 s79,0
s_cselect_b32 s94,0x3300,0
s_cmp_eq_u32 s79,127
s_cselect_b32 s94,0xcc00,s94
s_waitcnt vmcnt(0)
s_barrier
buffer_store_dword v4,v3,s[36:39],s70 offen offset:0 sc0 sc1
s_min_u32 s71,s71,s72
buffer_store_dword v4,v3,s[36:39],s71 offen offset:0 sc0 sc1
s_mov_b32 s70,0x7fff0000
s_mov_b32 s71,0x7fff0000
s_mov_b32 s72,0x7fff0000
s_cmp_eq_u32 s80,1
s_cbranch_scc1 Lg2_st1_%=
Lg2_st0_%=:
ds_read_b32 v7,v6 offset:2048
R 34,1,0
R 38,1,1024
R 42,1,2048
R 46,1,3072
R 50,1,4096
L 5
v_cmp_eq_u32 vcc,s53,v7
s_nop 0
s_or_b64 vcc,vcc,s[94:95]
L 3
s_cmp_eq_u32 vcc_lo,-1
s_cbranch_scc1 Lg2_F0_%=
s_branch Lg2_L0_%=
Lg2_st1_%=:
ds_read_b32 v7,v6 offset:2048
R 34,2,0
R 38,2,1024
R 42,2,2048
R 46,2,3072
R 50,2,4096
L 5
v_cmp_eq_u32 vcc,s53,v7
s_nop 0
s_or_b64 vcc,vcc,s[94:95]
L 3
s_cmp_eq_u32 vcc_lo,-1
s_cbranch_scc1 Lg2_F1_%=
s_branch Lg2_L1_%=
Lg2_F0_%=:
v_mfma_f32_16x16x32_bf16 v[22:25],a[4:7],v[34:37],0
s_add_u32 s58,s58,s61
s_add_u32 m0,s59,0
s_and_b32 s78,s54,127
R 54,1,5120
M 22,a[12:15],38
s_cmp_eq_u32 s78,s85
s_cselect_b64 s[40:41],-1,0
s_add_u32 s56,s55,0
buffer_load_dword v8,s[36:39],s58 offen offset:0 sc1 lds
v_mfma_f32_16x16x32_bf16 v[18:21],a[0:3],v[34:37],0
s_add_u32 m0,s57,0
R 58,1,6144
v_cndmask_b32 v17,v15,v16,s[40:41]
M 18,a[8:11],38
L 3
D 17,56,0
M 18,a[16:19],42
s_add_u32 s56,s55,256
R 62,1,7168
v_lshlrev_b32 v106,16,v126
M 18,a[24:27],46
s_add_u32 s54,s54,s46
v_and_b32 v107,0xffff0000,v126
v_lshlrev_b32 v108,16,v127
M 22,a[20:23],42
s_and_b32 s79,s54,127
R 66,1,8192
v_and_b32 v109,0xffff0000,v127
M 22,a[28:31],46
L 3
D 17,56,1024
M 22,a[36:39],50
s_add_u32 s56,s55,512
R 70,1,9216
v_lshlrev_b32 v110,16,v128
)"
R"(M 22,a[44:47],54
s_cmp_eq_u32 s79,0
v_and_b32 v111,0xffff0000,v128
v_lshlrev_b32 v112,16,v129
M 18,a[32:35],50
s_cselect_b32 s94,0x3300,0
R 74,1,10240
v_and_b32 v113,0xffff0000,v129
M 18,a[40:43],54
L 3
D 17,56,2048
M 18,a[48:51],58
s_add_u32 s56,s55,768
R 78,1,11264
v_fma_f32 v106,v106,v103,v102
M 18,a[56:59],62
D 17,56,3072
M 22,a[52:55],58
s_add_u32 m0,s57,16384
R 82,1,12288
v_fma_f32 v107,v107,v103,v102
M 22,a[60:63],62
L 3
v_fma_f32 v108,v108,v103,v102
v_fma_f32 v109,v109,v103,v102
M 22,a[68:71],66
s_add_u32 s56,s55,20480
R 86,1,13312
v_fma_f32 v110,v110,v103,v102
M 22,a[76:79],70
buffer_load_dwordx4 v[122:125],v0,s[36:39],s74 offen offset:0 sc1
M 18,a[64:67],66
s_cmp_eq_u32 s79,127
R 90,1,14336
v_fma_f32 v111,v111,v103,v102
M 18,a[72:75],70
L 3
buffer_load_dwordx2 v[98:99],v11,s[36:39],s73 offen offset:256 sc1
M 18,a[80:83],74
s_waitcnt vmcnt(10)
R 94,1,15360
v_fma_f32 v112,v112,v103,v102
M 18,a[88:91],78
s_barrier
v_fma_f32 v113,v113,v103,v102
v_add_f32 v26,v26,v106
M 22,a[84:87],74
s_cselect_b32 s94,0xcc00,s94
R 34,1,16384
v_add_f32 v27,v27,v107
M 22,a[92:95],78
L 3
v_add_f32 v28,v28,v108
v_add_f32 v29,v29,v109
M 22,a[100:103],82
s_mov_b32 s66,s65
R 38,1,17408
v_add_f32 v30,v30,v110
M 22,a[108:111],86
s_mov_b32 s76,s75
v_cndmask_b32 v17,v10,v9,s[40:41]
v_add_f32 v31,v31,v111
M 18,a[96:99],82
s_add_u32 s73,s73,s64
R 42,1,18432
v_add_f32 v32,v32,v112
M 18,a[104:107],86
L 3
D 17,56,0
M 18,a[112:115],90
s_add_u32 s56,s55,20736
R 46,1,19456
v_add_f32 v33,v33,v113
M 18,a[120:123],94
s_add_u32 s74,s74,s62
v_cvt_pk_bf16_f32 v114,v26,v27
v_cvt_pk_bf16_f32 v115,v28,v29
M 22,a[116:119],90
s_add_u32 s65,s65,s62
R 50,1,20480
v_fma_f32 v107,v26,v26,s87
M 22,a[124:127],94
L 3
v_add_f32 v106,v26,v27
v_fma_f32 v107,v27,v27,v107
M 22,a[132:135],34
s_add_u32 s75,s75,s63
R 54,1,21504
v_add_f32 v106,v106,v28
M 22,a[140:143],38
D 17,56,1024
M 18,a[128:131],34
s_add_u32 s56,s55,20992
R 58,1,22528
v_fma_f32 v107,v28,v28,v107
M 18,a[136:139],38
L 3
v_add_f32 v106,v106,v29
v_fma_f32 v107,v29,v29,v107
M 18,a[144:147],42
s_sub_u32 s57,s84,s57
R 62,1,23552
v_add_f32 v106,v106,v30
M 18,a[152:155],46
v_fma_f32 v107,v30,v30,v107
v_add_f32 v106,v106,v31
M 22,a[148:151],42
R 66,1,24576
v_fma_f32 v107,v31,v31,v107
M 22,a[156:159],46
L 3
v_add_f32 v106,v106,v32
v_fma_f32 v107,v32,v32,v107
M 22,a[164:167],50
R 70,1,25600
v_add_f32 v106,v106,v33
M 22,a[172:175],54
v_fma_f32 v107,v33,v33,v107
v_cvt_pk_bf16_f32 v116,v30,v31
M 18,a[160:163],50
R 74,1,26624
v_cvt_pk_bf16_f32 v117,v32,v33
M 18,a[168:171],54
L 3
v_permlane16_swap_b32 v106,v107
v_add_f32 v106,v106,v107
v_mov_b32 v107,v106
M 18,a[176:179],58
R 78,1,27648
M 18,a[184:187],62
v_permlane32_swap_b32 v106,v107
v_add_f32 v106,v106,v107
M 22,a[180:183],58
R 82,1,28672
ds_write_b32 v12,v106 offset:2048
M 22,a[188:191],62
L 4
D 17,56,2048
M 22,a[196:199],66
s_add_u32 s56,s55,21248
s_add_u32 s55,s55,s60
R 86,1,29696
M 22,a[204:207],70
D 17,56,3072
M 18,a[192:195],66
R 90,1,30720
M 18,a[200:203],70
L 4
L 2
buffer_store_dwordx4 v[114:117],v0,s[36:39],s67 offen offset:0 sc0 sc1
M 18,a[208:211],74
s_waitcnt vmcnt(7)
R 94,1,31744
M 18,a[216:219],78
s_barrier
ds_read_b32 v7,v6 offset:0
R 118,13,2048
M 22,a[212:215],74
R 34,2,0
M 22,a[220:223],78
L 5
buffer_store_dword v4,v3,s[36:39],s70 offen offset:0 sc0 sc1
M 22,a[228:231],82
s_mov_b32 s70,s69
s_mov_b32 s69,s68
R 38,2,1024
M 22,a[236:239],86
s_add_u32 s68,s68,s63
M 18,a[224:227],82
R 42,2,2048
M 18,a[232:235],86
L 5
L 4
v_cmp_eq_u32 vcc,s53,v7
M 18,a[240:243],90
L 3
R 46,2,3072
v_add_f32 v118,v118,v119
M 18,a[248:251],94
s_or_b64 vcc,vcc,s[94:95]
v_add_f32 v120,v120,v121
v_add_f32 v118,v118,v120
M 22,a[244:247],90
s_cmp_eq_u32 vcc_lo,-1
R 50,2,4096
v_mul_f32 v118,s86,v118
M 22,a[252:255],94
s_waitcnt vmcnt(6) lgkmcnt(3)
buffer_store_dword v118,v14,s[36:39],s77 offen offset:0 sc0 sc1
s_cbranch_scc0 Lg2_L1_%=
Lg2_F1_%=:
v_mfma_f32_16x16x32_bf16 v[30:33],a[4:7],v[34:37],0
s_add_u32 s58,s58,s61
s_add_u32 m0,s59,1024
s_and_b32 s78,s54,127
R 54,2,5120
M 30,a[12:15],38
s_cmp_eq_u32 s78,s85
s_cselect_b64 s[40:41],-1,0
s_add_u32 s56,s55,0
buffer_load_dword v8,s[36:39],s58 offen offset:0 sc1 lds
v_mfma_f32_16x16x32_bf16 v[26:29],a[0:3],v[34:37],0
s_add_u32 m0,s57,0
R 58,2,6144
v_cndmask_b32 v17,v15,v16,s[40:41]
M 26,a[8:11],38
L 3
D 17,56,0
M 26,a[16:19],42
s_add_u32 s56,s55,256
R 62,2,7168
v_lshlrev_b32 v106,16,v122
M 26,a[24:27],46
s_add_u32 s54,s54,s46
v_and_b32 v107,0xffff0000,v122
v_lshlrev_b32 v108,16,v123
M 30,a[20:23],42
s_and_b32 s79,s54,127
R 66,2,8192
v_and_b32 v109,0xffff0000,v123
M 30,a[28:31],46
L 3
D 17,56,1024
M 30,a[36:39],50
s_add_u32 s56,s55,512
R 70,2,9216
v_lshlrev_b32 v110,16,v124
M 30,a[44:47],54
s_cmp_eq_u32 s79,0
v_and_b32 v111,0xffff0000,v124
v_lshlrev_b32 v112,16,v125
M 26,a[32:35],50
s_cselect_b32 s94,0x3300,0
R 74,2,10240
v_and_b32 v113,0xffff0000,v125
M 26,a[40:43],54
L 3
D 17,56,2048
M 26,a[48:51],58
s_add_u32 s56,s55,768
R 78,2,11264
v_fma_f32 v106,v106,v99,v98
M 26,a[56:59],62
D 17,56,3072
M 30,a[52:55],58
s_add_u32 m0,s57,16384
R 82,2,12288
v_fma_f32 v107,v107,v99,v98
M 30,a[60:63],62
L 3
v_fma_f32 v108,v108,v99,v98
v_fma_f32 v109,v109,v99,v98
M 30,a[68:71],66
s_add_u32 s56,s55,20480
R 86,2,13312
v_fma_f32 v110,v110,v99,v98
M 30,a[76:79],70
buffer_load_dwordx4 v[126:129],v0,s[36:39],s74 offen offset:0 sc1
M 26,a[64:67],66
s_cmp_eq_u32 s79,127
R 90,2,14336
v_fma_f32 v111,v111,v99,v98
M 26,a[72:75],70
L 3
buffer_load_dwordx2 v[102:103],v11,s[36:39],s73 offen offset:256 sc1
M 26,a[80:83],74
s_waitcnt vmcnt(10)
R 94,2,15360
v_fma_f32 v112,v112,v99,v98
M 26,a[88:91],78
s_barrier
v_fma_f32 v113,v113,v99,v98
v_add_f32 v18,v18,v106
M 30,a[84:87],74
s_cselect_b32 s94,0xcc00,s94
R 34,2,16384
v_add_f32 v19,v19,v107
M 30,a[92:95],78
L 3
v_add_f32 v20,v20,v108
v_add_f32 v21,v21,v109
M 30,a[100:103],82
s_mov_b32 s67,s65
R 38,2,17408
v_add_f32 v22,v22,v110
M 30,a[108:111],86
s_mov_b32 s77,s75
v_cndmask_b32 v17,v10,v9,s[40:41]
v_add_f32 v23,v23,v111
M 26,a[96:99],82
s_add_u32 s73,s73,s64
R 42,2,18432
v_add_f32 v24,v24,v112
M 26,a[104:107],86
L 3
D 17,56,0
M 26,a[112:115],90
s_add_u32 s56,s55,20736
R 46,2,19456
v_add_f32 v25,v25,v113
M 26,a[120:123],94
s_add_u32 s74,s74,s62
v_cvt_pk_bf16_f32 v114,v18,v19
v_cvt_pk_bf16_f32 v115,v20,v21
M 30,a[116:119],90
s_add_u32 s65,s65,s62
R 50,2,20480
v_fma_f32 v107,v18,v18,s87
M 30,a[124:127],94
L 3
v_add_f32 v106,v18,v19
v_fma_f32 v107,v19,v19,v107
M 30,a[132:135],34
s_add_u32 s75,s75,s63
R 54,2,21504
v_add_f32 v106,v106,v20
M 30,a[140:143],38
D 17,56,1024
M 26,a[128:131],34
s_add_u32 s56,s55,20992
R 58,2,22528
v_fma_f32 v107,v20,v20,v107
M 26,a[136:139],38
L 3
v_add_f32 v106,v106,v21
v_fma_f32 v107,v21,v21,v107
M 26,a[144:147],42
s_sub_u32 s57,s84,s57
R 62,2,23552
v_add_f32 v106,v106,v22
M 26,a[152:155],46
v_fma_f32 v107,v22,v22,v107
v_add_f32 v106,v106,v23
M 30,a[148:151],42
R 66,2,24576
v_fma_f32 v107,v23,v23,v107
M 30,a[156:159],46
L 3
v_add_f32 v106,v106,v24
v_fma_f32 v107,v24,v24,v107
M 30,a[164:167],50
R 70,2,25600
v_add_f32 v106,v106,v25
M 30,a[172:175],54
v_fma_f32 v107,v25,v25,v107
v_cvt_pk_bf16_f32 v116,v22,v23
M 26,a[160:163],50
R 74,2,26624
v_cvt_pk_bf16_f32 v117,v24,v25
M 26,a[168:171],54
L 3
v_permlane16_swap_b32 v106,v107
v_add_f32 v106,v106,v107
v_mov_b32 v107,v106
M 26,a[176:179],58
R 78,2,27648
M 26,a[184:187],62
v_permlane32_swap_b32 v106,v107
v_add_f32 v106,v106,v107
M 30,a[180:183],58
R 82,2,28672
ds_write_b32 v12,v106 offset:0
M 30,a[188:191],62
L 4
D 17,56,2048
M 30,a[196:199],66
s_add_u32 s56,s55,21248
s_add_u32 s55,s55,s60
R 86,2,29696
M 30,a[204:207],70
D 17,56,3072
M 26,a[192:195],66
R 90,2,30720
M 26,a[200:203],70
L 4
L 2
buffer_store_dwordx4 v[114:117],v0,s[36:39],s66 offen offset:0 sc0 sc1
M 26,a[208:211],74
s_waitcnt vmcnt(7)
R 94,2,31744
M 26,a[216:219],78
s_barrier
ds_read_b32 v7,v6 offset:1024
R 118,13,0
M 30,a[212:215],74
R 34,1,0
M 30,a[220:223],78
L 5
buffer_store_dword v4,v3,s[36:39],s70 offen offset:0 sc0 sc1
M 30,a[228:231],82
s_mov_b32 s70,s69
s_mov_b32 s69,s68
R 38,1,1024
M 30,a[236:239],86
s_add_u32 s68,s68,s63
M 26,a[224:227],82
R 42,1,2048
M 26,a[232:235],86
L 5
L 4
v_cmp_eq_u32 vcc,s53,v7
M 26,a[240:243],90
L 3
R 46,1,3072
v_add_f32 v118,v118,v119
M 26,a[248:251],94
s_or_b64 vcc,vcc,s[94:95]
v_add_f32 v120,v120,v121
v_add_f32 v118,v118,v120
M 30,a[244:247],90
s_cmp_eq_u32 vcc_lo,-1
R 50,1,4096
v_mul_f32 v118,s86,v118
M 30,a[252:255],94
s_waitcnt vmcnt(6) lgkmcnt(3)
buffer_store_dword v118,v14,s[36:39],s76 offen offset:0 sc0 sc1
s_cbranch_scc1 Lg2_F0_%=
Lg2_L0_%=:
v_mfma_f32_16x16x32_bf16 v[22:25],a[4:7],v[34:37],0
s_add_u32 m0,s59,3072
R 54,1,5120
v_lshlrev_b32 v106,16,v126
M 22,a[12:15],38
buffer_load_dword v8,s[36:39],s58 offen offset:0 sc1 lds
v_mfma_f32_16x16x32_bf16 v[18:21],a[0:3],v[34:37],0
s_mov_b32 s66,s65
R 58,1,6144
v_and_b32 v107,0xffff0000,v126
M 18,a[8:11],38
L 3
v_lshlrev_b32 v108,16,v127
v_and_b32 v109,0xffff0000,v127
M 18,a[16:19],42
s_mov_b32 s76,s75
R 62,1,7168
v_lshlrev_b32 v110,16,v128
M 18,a[24:27],46
s_add_u32 s65,s65,s62
v_and_b32 v111,0xffff0000,v128
v_lshlrev_b32 v112,16,v129
M 22,a[20:23],42
s_add_u32 s75,s75,s63
R 66,1,8192
v_and_b32 v113,0xffff0000,v129
v_fma_f32 v106,v106,v103,v102
M 22,a[28:31],46
L 3
v_fma_f32 v107,v107,v103,v102
v_fma_f32 v108,v108,v103,v102
M 22,a[36:39],50
R 70,1,9216
v_fma_f32 v109,v109,v103,v102
M 22,a[44:47],54
v_fma_f32 v110,v110,v103,v102
v_fma_f32 v111,v111,v103,v102
M 18,a[32:35],50
R 74,1,10240
v_fma_f32 v112,v112,v103,v102
M 18,a[40:43],54
L 3
v_fma_f32 v113,v113,v103,v102
v_add_f32 v26,v26,v106
M 18,a[48:51],58
R 78,1,11264
v_add_f32 v27,v27,v107
M 18,a[56:59],62
v_add_f32 v28,v28,v108
v_add_f32 v29,v29,v109
v_add_f32 v30,v30,v110
M 22,a[52:55],58
R 82,1,12288
v_add_f32 v31,v31,v111
M 22,a[60:63],62
L 3
v_add_f32 v32,v32,v112
v_add_f32 v33,v33,v113
M 22,a[68:71],66
R 86,1,13312
v_cvt_pk_bf16_f32 v114,v26,v27
M 22,a[76:79],70
buffer_load_dwordx4 v[122:125],v0,s[36:39],s74 offen offset:0 sc1
M 18,a[64:67],66
s_add_u32 s74,s74,s62
R 90,1,14336
v_cvt_pk_bf16_f32 v115,v28,v29
M 18,a[72:75],70
L 3
buffer_load_dwordx2 v[98:99],v11,s[36:39],s73 offen offset:256 sc1
v_cvt_pk_bf16_f32 v116,v30,v31
M 18,a[80:83],74
s_waitcnt vmcnt(2)
s_add_u32 s73,s73,s64
R 94,1,15360
M 18,a[88:91],78
s_barrier
ds_read_b32 v7,v6 offset:3072
v_cvt_pk_bf16_f32 v117,v32,v33
M 22,a[84:87],74
R 34,1,16384
v_add_f32 v106,v26,v27
)"
R"(M 22,a[92:95],78
L 4
buffer_store_dword v4,v3,s[36:39],s70 offen offset:0 sc0 sc1
M 22,a[100:103],82
s_mov_b32 s70,0x7fff0000
R 38,1,17408
v_fma_f32 v107,v26,v26,s87
M 22,a[108:111],86
buffer_store_dwordx4 v[114:117],v0,s[36:39],s67 offen offset:0 sc0 sc1
v_fma_f32 v107,v27,v27,v107
M 18,a[96:99],82
R 42,1,18432
v_add_f32 v106,v106,v28
v_fma_f32 v107,v28,v28,v107
M 18,a[104:107],86
L 4
L 3
v_cmp_eq_u32 vcc,s53,v7
M 18,a[112:115],90
R 46,1,19456
v_add_f32 v106,v106,v29
M 18,a[120:123],94
s_or_b64 vcc,vcc,s[94:95]
v_fma_f32 v107,v29,v29,v107
v_add_f32 v106,v106,v30
M 22,a[116:119],90
R 50,1,20480
v_fma_f32 v107,v30,v30,v107
M 22,a[124:127],94
s_cmp_eq_u32 vcc_lo,-1
L 3
v_add_f32 v106,v106,v31
s_cbranch_scc0 Lg2_X0_%=
M 22,a[132:135],34
s_and_b32 s78,s54,127
s_cmp_eq_u32 s78,s85
s_cselect_b64 s[40:41],-1,0
R 54,1,21504
M 22,a[140:143],38
s_add_u32 m0,s57,0
s_add_u32 s56,s55,0
s_add_u32 s58,s58,s61
v_cndmask_b32 v17,v15,v16,s[40:41]
M 18,a[128:131],34
s_add_u32 s54,s54,s46
R 58,1,22528
v_fma_f32 v107,v31,v31,v107
M 18,a[136:139],38
L 3
D 17,56,0
M 18,a[144:147],42
s_add_u32 s56,s55,256
R 62,1,23552
v_add_f32 v106,v106,v32
M 18,a[152:155],46
s_and_b32 s79,s54,127
v_fma_f32 v107,v32,v32,v107
v_add_f32 v106,v106,v33
M 22,a[148:151],42
s_cmp_eq_u32 s79,0
R 66,1,24576
v_fma_f32 v107,v33,v33,v107
M 22,a[156:159],46
L 3
D 17,56,1024
M 22,a[164:167],50
s_cselect_b32 s94,0x3300,0
s_add_u32 s56,s55,512
R 70,1,25600
M 22,a[172:175],54
s_cmp_eq_u32 s79,127
v_permlane16_swap_b32 v106,v107
v_add_f32 v106,v106,v107
M 18,a[160:163],50
s_cselect_b32 s94,0xcc00,s94
R 74,1,26624
v_mov_b32 v107,v106
M 18,a[168:171],54
L 3
D 17,56,2048
M 18,a[176:179],58
s_add_u32 s56,s55,768
R 78,1,27648
M 18,a[184:187],62
v_permlane32_swap_b32 v106,v107
v_add_f32 v106,v106,v107
M 22,a[180:183],58
R 82,1,28672
ds_write_b32 v12,v106 offset:2048
M 22,a[188:191],62
L 4
D 17,56,3072
M 22,a[196:199],66
s_add_u32 m0,s59,0
s_add_u32 s56,s55,20480
R 86,1,29696
M 22,a[204:207],70
buffer_load_dword v8,s[36:39],s58 offen offset:0 sc1 lds
M 18,a[192:195],66
s_add_u32 m0,s57,16384
R 90,1,30720
v_cndmask_b32 v17,v10,v9,s[40:41]
M 18,a[200:203],70
L 4
L 2
s_sub_u32 s57,s84,s57
M 18,a[208:211],74
s_waitcnt vmcnt(0)
R 94,1,31744
M 18,a[216:219],78
s_barrier
D 17,56,0
M 22,a[212:215],74
s_add_u32 s56,s55,20736
R 34,2,0
ds_read_b32 v7,v6 offset:0
M 22,a[220:223],78
L 4
buffer_store_dword v4,v3,s[36:39],s70 offen offset:0 sc0 sc1
M 22,a[228:231],82
s_mov_b32 s70,s69
R 38,2,1024
R 118,13,2048
M 22,a[236:239],86
D 17,56,1024
M 18,a[224:227],82
s_add_u32 s56,s55,20992
s_mov_b32 s69,s68
R 42,2,2048
M 18,a[232:235],86
L 5
D 17,56,2048
M 18,a[240:243],90
L 3
s_add_u32 s56,s55,21248
R 46,2,3072
v_cmp_eq_u32 vcc,s53,v7
M 18,a[248:251],94
D 17,56,3072
M 22,a[244:247],90
s_add_u32 s55,s55,s60
s_add_u32 s68,s68,s63
L 2
s_or_b64 vcc,vcc,s[94:95]
s_cmp_eq_u32 vcc_lo,-1
R 50,2,4096
v_add_f32 v118,v118,v119
v_add_f32 v120,v120,v121
v_add_f32 v118,v118,v120
v_mul_f32 v118,s86,v118
M 22,a[252:255],94
s_nop 0
buffer_store_dword v118,v14,s[36:39],s77 offen offset:0 sc0 sc1
buffer_store_dword v4,v3,s[36:39],s71 offen offset:0 sc0 sc1
buffer_store_dword v4,v3,s[36:39],s71 offen offset:0 sc0 sc1
s_cbranch_scc1 Lg2_F1_%=
s_branch Lg2_L1_%=
Lg2_X0_%=:
M 22,a[132:135],34
s_add_u32 m0,s59,0
R 54,1,21504
v_fma_f32 v107,v31,v31,v107
M 22,a[140:143],38
buffer_load_dword v8,s[36:39],s58 offen offset:0 sc1 lds
M 18,a[128:131],34
R 58,1,22528
v_add_f32 v106,v106,v32
M 18,a[136:139],38
L 3
v_fma_f32 v107,v32,v32,v107
v_add_f32 v106,v106,v33
M 18,a[144:147],42
R 62,1,23552
v_fma_f32 v107,v33,v33,v107
M 18,a[152:155],46
M 22,a[148:151],42
R 66,1,24576
M 22,a[156:159],46
L 3
v_permlane16_swap_b32 v106,v107
v_add_f32 v106,v106,v107
M 22,a[164:167],50
R 70,1,25600
v_mov_b32 v107,v106
M 22,a[172:175],54
M 18,a[160:163],50
R 74,1,26624
M 18,a[168:171],54
L 3
v_permlane32_swap_b32 v106,v107
M 18,a[176:179],58
R 78,1,27648
v_add_f32 v106,v106,v107
M 18,a[184:187],62
ds_write_b32 v12,v106 offset:2048
M 22,a[180:183],58
R 82,1,28672
M 22,a[188:191],62
L 4
M 22,a[196:199],66
R 86,1,29696
M 22,a[204:207],70
M 18,a[192:195],66
R 90,1,30720
M 18,a[200:203],70
L 4
L 3
M 18,a[208:211],74
s_waitcnt vmcnt(0)
R 94,1,31744
M 18,a[216:219],78
s_barrier
R 118,13,2048
ds_read_b32 v7,v6 offset:0
M 22,a[212:215],74
buffer_store_dword v4,v3,s[36:39],s70 offen offset:0 sc0 sc1
M 22,a[220:223],78
L 4
s_mov_b32 s70,s69
s_mov_b32 s69,s68
M 22,a[228:231],82
s_add_u32 s68,s68,s63
M 22,a[236:239],86
M 18,a[224:227],82
M 18,a[232:235],86
L 2
L 1
L 0
M 18,a[240:243],90
v_add_f32 v118,v118,v119
v_add_f32 v120,v120,v121
v_add_f32 v118,v118,v120
M 18,a[248:251],94
v_cmp_eq_u32 vcc,s53,v7
v_mul_f32 v118,s86,v118
M 22,a[244:247],90
buffer_store_dword v118,v14,s[36:39],s77 offen offset:0 sc0 sc1
M 22,a[252:255],94
s_nop 0
s_or_b64 vcc,vcc,s[94:95]
s_mov_b32 s80,1
s_cmp_eq_u32 vcc_lo,-1
s_cbranch_scc0 Lg2_xn0_%=
s_and_b32 s78,s54,127
s_cmp_eq_u32 s78,s85
s_cselect_b64 s[40:41],-1,0
v_cndmask_b32 v17,v15,v16,s[40:41]
s_add_u32 m0,s57,0
s_add_u32 s56,s55,0
D 17,56,0
v_lshlrev_b32 v106,16,v122
v_and_b32 v107,0xffff0000,v122
v_lshlrev_b32 v108,16,v123
v_and_b32 v109,0xffff0000,v123
v_lshlrev_b32 v110,16,v124
s_add_u32 s56,s55,256
D 17,56,1024
v_and_b32 v111,0xffff0000,v124
v_lshlrev_b32 v112,16,v125
v_and_b32 v113,0xffff0000,v125
v_fma_f32 v106,v106,v99,v98
v_fma_f32 v107,v107,v99,v98
s_add_u32 s56,s55,512
D 17,56,2048
v_fma_f32 v108,v108,v99,v98
v_fma_f32 v109,v109,v99,v98
v_fma_f32 v110,v110,v99,v98
v_fma_f32 v111,v111,v99,v98
v_fma_f32 v112,v112,v99,v98
s_add_u32 s56,s55,768
D 17,56,3072
v_fma_f32 v113,v113,v99,v98
v_add_f32 v18,v18,v106
v_add_f32 v19,v19,v107
v_add_f32 v20,v20,v108
v_add_f32 v21,v21,v109
s_add_u32 s58,s58,s61
v_cndmask_b32 v17,v10,v9,s[40:41]
s_add_u32 m0,s57,16384
s_add_u32 s56,s55,20480
D 17,56,0
v_add_f32 v22,v22,v110
v_add_f32 v23,v23,v111
v_add_f32 v24,v24,v112
v_add_f32 v25,v25,v113
v_cvt_pk_bf16_f32 v114,v18,v19
s_add_u32 s56,s55,20736
D 17,56,1024
v_cvt_pk_bf16_f32 v115,v20,v21
v_cvt_pk_bf16_f32 v116,v22,v23
v_cvt_pk_bf16_f32 v117,v24,v25
s_add_u32 s56,s55,20992
D 17,56,2048
s_add_u32 s56,s55,21248
D 17,56,3072
buffer_store_dwordx4 v[114:117],v0,s[36:39],s66 offen offset:0 sc0 sc1
v_add_f32 v106,v18,v19
v_fma_f32 v107,v18,v18,s87
s_add_u32 s55,s55,s60
s_sub_u32 s57,s84,s57
s_add_u32 s54,s54,s46
v_fma_f32 v107,v19,v19,v107
v_add_f32 v106,v106,v20
v_fma_f32 v107,v20,v20,v107
v_add_f32 v106,v106,v21
v_fma_f32 v107,v21,v21,v107
v_add_f32 v106,v106,v22
v_fma_f32 v107,v22,v22,v107
v_add_f32 v106,v106,v23
v_fma_f32 v107,v23,v23,v107
v_add_f32 v106,v106,v24
v_fma_f32 v107,v24,v24,v107
v_add_f32 v106,v106,v25
v_fma_f32 v107,v25,v25,v107
s_nop 1
v_permlane16_swap_b32 v106,v107
v_add_f32 v106,v106,v107
v_mov_b32 v107,v106
s_nop 1
v_permlane32_swap_b32 v106,v107
v_add_f32 v106,v106,v107
ds_write_b32 v12,v106 offset:0
L 0
s_barrier
R 118,13,0
L 0
v_add_f32 v118,v118,v119
v_add_f32 v120,v120,v121
v_add_f32 v118,v118,v120
v_mul_f32 v118,s86,v118
buffer_store_dword v118,v14,s[36:39],s76 offen offset:0 sc0 sc1
s_mov_b32 s72,s69
s_mov_b32 s69,0x7fff0000
s_mov_b32 s66,0x7fff0000
s_mov_b32 s76,0x7fff0000
s_branch Lg2_gox_%=
Lg2_xn0_%=:
s_cmp_ge_u32 s54,256
s_cbranch_scc1 Lg2_fin0_%=
s_add_u32 m0,s59,4096
s_nop 0
buffer_load_dword v8,s[36:39],s58 offen offset:0 sc1 lds
buffer_store_dword v4,v3,s[36:39],s71 offen offset:0 sc0 sc1
s_add_u32 m0,s59,5120
s_nop 0
buffer_load_dword v8,s[36:39],s58 offen offset:0 sc1 lds
buffer_store_dword v4,v3,s[36:39],s71 offen offset:0 sc0 sc1
s_mov_b32 s83,0
v_lshlrev_b32 v106,16,v122
v_and_b32 v107,0xffff0000,v122
v_lshlrev_b32 v108,16,v123
v_and_b32 v109,0xffff0000,v123
v_lshlrev_b32 v110,16,v124
v_and_b32 v111,0xffff0000,v124
v_lshlrev_b32 v112,16,v125
v_and_b32 v113,0xffff0000,v125
v_fma_f32 v106,v106,v99,v98
v_fma_f32 v107,v107,v99,v98
v_fma_f32 v108,v108,v99,v98
v_fma_f32 v109,v109,v99,v98
v_fma_f32 v110,v110,v99,v98
v_fma_f32 v111,v111,v99,v98
v_fma_f32 v112,v112,v99,v98
v_fma_f32 v113,v113,v99,v98
v_add_f32 v18,v18,v106
v_add_f32 v19,v19,v107
v_add_f32 v20,v20,v108
v_add_f32 v21,v21,v109
v_add_f32 v22,v22,v110
v_add_f32 v23,v23,v111
v_add_f32 v24,v24,v112
v_add_f32 v25,v25,v113
v_cvt_pk_bf16_f32 v114,v18,v19
v_cvt_pk_bf16_f32 v115,v20,v21
v_cvt_pk_bf16_f32 v116,v22,v23
v_cvt_pk_bf16_f32 v117,v24,v25
buffer_store_dwordx4 v[114:117],v0,s[36:39],s66 offen offset:0 sc0 sc1
v_add_f32 v106,v18,v19
v_fma_f32 v107,v18,v18,s87
v_fma_f32 v107,v19,v19,v107
v_add_f32 v106,v106,v20
v_fma_f32 v107,v20,v20,v107
v_add_f32 v106,v106,v21
v_fma_f32 v107,v21,v21,v107
v_add_f32 v106,v106,v22
v_fma_f32 v107,v22,v22,v107
v_add_f32 v106,v106,v23
v_fma_f32 v107,v23,v23,v107
v_add_f32 v106,v106,v24
v_fma_f32 v107,v24,v24,v107
v_add_f32 v106,v106,v25
v_fma_f32 v107,v25,v25,v107
s_nop 1
v_permlane16_swap_b32 v106,v107
v_add_f32 v106,v106,v107
v_mov_b32 v107,v106
s_nop 1
v_permlane32_swap_b32 v106,v107
v_add_f32 v106,v106,v107
ds_write_b32 v12,v106 offset:0
L 0
s_barrier
R 118,13,0
L 0
v_add_f32 v118,v118,v119
v_add_f32 v120,v120,v121
v_add_f32 v118,v118,v120
v_mul_f32 v118,s86,v118
buffer_store_dword v118,v14,s[36:39],s76 offen offset:0 sc0 sc1
s_mov_b32 s72,s69
s_mov_b32 s69,0x7fff0000
s_mov_b32 s66,0x7fff0000
s_mov_b32 s76,0x7fff0000
s_branch Lg2_poll_%=
Lg2_fin0_%=:
v_lshlrev_b32 v106,16,v122
v_and_b32 v107,0xffff0000,v122
v_lshlrev_b32 v108,16,v123
v_and_b32 v109,0xffff0000,v123
v_lshlrev_b32 v110,16,v124
v_and_b32 v111,0xffff0000,v124
v_lshlrev_b32 v112,16,v125
v_and_b32 v113,0xffff0000,v125
v_fma_f32 v106,v106,v99,v98
v_fma_f32 v107,v107,v99,v98
v_fma_f32 v108,v108,v99,v98
v_fma_f32 v109,v109,v99,v98
v_fma_f32 v110,v110,v99,v98
v_fma_f32 v111,v111,v99,v98
v_fma_f32 v112,v112,v99,v98
v_fma_f32 v113,v113,v99,v98
v_add_f32 v18,v18,v106
v_add_f32 v19,v19,v107
v_add_f32 v20,v20,v108
v_add_f32 v21,v21,v109
v_add_f32 v22,v22,v110
v_add_f32 v23,v23,v111
v_add_f32 v24,v24,v112
v_add_f32 v25,v25,v113
v_cvt_pk_bf16_f32 v114,v18,v19
v_cvt_pk_bf16_f32 v115,v20,v21
v_cvt_pk_bf16_f32 v116,v22,v23
v_cvt_pk_bf16_f32 v117,v24,v25
buffer_store_dwordx4 v[114:117],v0,s[36:39],s66 offen offset:0 sc0 sc1
v_add_f32 v106,v18,v19
v_fma_f32 v107,v18,v18,s87
v_fma_f32 v107,v19,v19,v107
v_add_f32 v106,v106,v20
v_fma_f32 v107,v20,v20,v107
v_add_f32 v106,v106,v21
v_fma_f32 v107,v21,v21,v107
v_add_f32 v106,v106,v22
v_fma_f32 v107,v22,v22,v107
v_add_f32 v106,v106,v23
v_fma_f32 v107,v23,v23,v107
v_add_f32 v106,v106,v24
v_fma_f32 v107,v24,v24,v107
v_add_f32 v106,v106,v25
v_fma_f32 v107,v25,v25,v107
s_nop 1
v_permlane16_swap_b32 v106,v107
v_add_f32 v106,v106,v107
v_mov_b32 v107,v106
s_nop 1
v_permlane32_swap_b32 v106,v107
v_add_f32 v106,v106,v107
ds_write_b32 v12,v106 offset:0
L 0
s_barrier
R 118,13,0
L 0
v_add_f32 v118,v118,v119
v_add_f32 v120,v120,v121
v_add_f32 v118,v118,v120
v_mul_f32 v118,s86,v118
buffer_store_dword v118,v14,s[36:39],s76 offen offset:0 sc0 sc1
s_mov_b32 s72,s69
s_mov_b32 s69,0x7fff0000
s_mov_b32 s66,0x7fff0000
s_mov_b32 s76,0x7fff0000
s_branch Lg2_done_%=
Lg2_L1_%=:
v_mfma_f32_16x16x32_bf16 v[30:33],a[4:7],v[34:37],0
s_add_u32 m0,s59,3072
R 54,2,5120
v_lshlrev_b32 v106,16,v122
M 30,a[12:15],38
buffer_load_dword v8,s[36:39],s58 offen offset:0 sc1 lds
v_mfma_f32_16x16x32_bf16 v[26:29],a[0:3],v[34:37],0
s_mov_b32 s67,s65
R 58,2,6144
v_and_b32 v107,0xffff0000,v122
M 26,a[8:11],38
L 3
v_lshlrev_b32 v108,16,v123
v_and_b32 v109,0xffff0000,v123
M 26,a[16:19],42
s_mov_b32 s77,s75
R 62,2,7168
v_lshlrev_b32 v110,16,v124
)"
R"(M 26,a[24:27],46
s_add_u32 s65,s65,s62
v_and_b32 v111,0xffff0000,v124
v_lshlrev_b32 v112,16,v125
M 30,a[20:23],42
s_add_u32 s75,s75,s63
R 66,2,8192
v_and_b32 v113,0xffff0000,v125
v_fma_f32 v106,v106,v99,v98
M 30,a[28:31],46
L 3
v_fma_f32 v107,v107,v99,v98
v_fma_f32 v108,v108,v99,v98
M 30,a[36:39],50
R 70,2,9216
v_fma_f32 v109,v109,v99,v98
M 30,a[44:47],54
v_fma_f32 v110,v110,v99,v98
v_fma_f32 v111,v111,v99,v98
M 26,a[32:35],50
R 74,2,10240
v_fma_f32 v112,v112,v99,v98
M 26,a[40:43],54
L 3
v_fma_f32 v113,v113,v99,v98
v_add_f32 v18,v18,v106
M 26,a[48:51],58
R 78,2,11264
v_add_f32 v19,v19,v107
M 26,a[56:59],62
v_add_f32 v20,v20,v108
v_add_f32 v21,v21,v109
v_add_f32 v22,v22,v110
M 30,a[52:55],58
R 82,2,12288
v_add_f32 v23,v23,v111
M 30,a[60:63],62
L 3
v_add_f32 v24,v24,v112
v_add_f32 v25,v25,v113
M 30,a[68:71],66
R 86,2,13312
v_cvt_pk_bf16_f32 v114,v18,v19
M 30,a[76:79],70
buffer_load_dwordx4 v[126:129],v0,s[36:39],s74 offen offset:0 sc1
M 26,a[64:67],66
s_add_u32 s74,s74,s62
R 90,2,14336
v_cvt_pk_bf16_f32 v115,v20,v21
M 26,a[72:75],70
L 3
buffer_load_dwordx2 v[102:103],v11,s[36:39],s73 offen offset:256 sc1
v_cvt_pk_bf16_f32 v116,v22,v23
M 26,a[80:83],74
s_waitcnt vmcnt(2)
s_add_u32 s73,s73,s64
R 94,2,15360
M 26,a[88:91],78
s_barrier
ds_read_b32 v7,v6 offset:3072
v_cvt_pk_bf16_f32 v117,v24,v25
M 30,a[84:87],74
R 34,2,16384
v_add_f32 v106,v18,v19
M 30,a[92:95],78
L 4
buffer_store_dword v4,v3,s[36:39],s70 offen offset:0 sc0 sc1
M 30,a[100:103],82
s_mov_b32 s70,0x7fff0000
R 38,2,17408
v_fma_f32 v107,v18,v18,s87
M 30,a[108:111],86
buffer_store_dwordx4 v[114:117],v0,s[36:39],s66 offen offset:0 sc0 sc1
v_fma_f32 v107,v19,v19,v107
M 26,a[96:99],82
R 42,2,18432
v_add_f32 v106,v106,v20
v_fma_f32 v107,v20,v20,v107
M 26,a[104:107],86
L 4
L 3
v_cmp_eq_u32 vcc,s53,v7
M 26,a[112:115],90
R 46,2,19456
v_add_f32 v106,v106,v21
M 26,a[120:123],94
s_or_b64 vcc,vcc,s[94:95]
v_fma_f32 v107,v21,v21,v107
v_add_f32 v106,v106,v22
M 30,a[116:119],90
R 50,2,20480
v_fma_f32 v107,v22,v22,v107
M 30,a[124:127],94
s_cmp_eq_u32 vcc_lo,-1
L 3
v_add_f32 v106,v106,v23
s_cbranch_scc0 Lg2_X1_%=
M 30,a[132:135],34
s_and_b32 s78,s54,127
s_cmp_eq_u32 s78,s85
s_cselect_b64 s[40:41],-1,0
R 54,2,21504
M 30,a[140:143],38
s_add_u32 m0,s57,0
s_add_u32 s56,s55,0
s_add_u32 s58,s58,s61
v_cndmask_b32 v17,v15,v16,s[40:41]
M 26,a[128:131],34
s_add_u32 s54,s54,s46
R 58,2,22528
v_fma_f32 v107,v23,v23,v107
M 26,a[136:139],38
L 3
D 17,56,0
M 26,a[144:147],42
s_add_u32 s56,s55,256
R 62,2,23552
v_add_f32 v106,v106,v24
M 26,a[152:155],46
s_and_b32 s79,s54,127
v_fma_f32 v107,v24,v24,v107
v_add_f32 v106,v106,v25
M 30,a[148:151],42
s_cmp_eq_u32 s79,0
R 66,2,24576
v_fma_f32 v107,v25,v25,v107
M 30,a[156:159],46
L 3
D 17,56,1024
M 30,a[164:167],50
s_cselect_b32 s94,0x3300,0
s_add_u32 s56,s55,512
R 70,2,25600
M 30,a[172:175],54
s_cmp_eq_u32 s79,127
v_permlane16_swap_b32 v106,v107
v_add_f32 v106,v106,v107
M 26,a[160:163],50
s_cselect_b32 s94,0xcc00,s94
R 74,2,26624
v_mov_b32 v107,v106
M 26,a[168:171],54
L 3
D 17,56,2048
M 26,a[176:179],58
s_add_u32 s56,s55,768
R 78,2,27648
M 26,a[184:187],62
v_permlane32_swap_b32 v106,v107
v_add_f32 v106,v106,v107
M 30,a[180:183],58
R 82,2,28672
ds_write_b32 v12,v106 offset:0
M 30,a[188:191],62
L 4
D 17,56,3072
M 30,a[196:199],66
s_add_u32 m0,s59,1024
s_add_u32 s56,s55,20480
R 86,2,29696
M 30,a[204:207],70
buffer_load_dword v8,s[36:39],s58 offen offset:0 sc1 lds
M 26,a[192:195],66
s_add_u32 m0,s57,16384
R 90,2,30720
v_cndmask_b32 v17,v10,v9,s[40:41]
M 26,a[200:203],70
L 4
L 2
s_sub_u32 s57,s84,s57
M 26,a[208:211],74
s_waitcnt vmcnt(0)
R 94,2,31744
M 26,a[216:219],78
s_barrier
D 17,56,0
M 30,a[212:215],74
s_add_u32 s56,s55,20736
R 34,1,0
ds_read_b32 v7,v6 offset:1024
M 30,a[220:223],78
L 4
buffer_store_dword v4,v3,s[36:39],s70 offen offset:0 sc0 sc1
M 30,a[228:231],82
s_mov_b32 s70,s69
R 38,1,1024
R 118,13,0
M 30,a[236:239],86
D 17,56,1024
M 26,a[224:227],82
s_add_u32 s56,s55,20992
s_mov_b32 s69,s68
R 42,1,2048
M 26,a[232:235],86
L 5
D 17,56,2048
M 26,a[240:243],90
L 3
s_add_u32 s56,s55,21248
R 46,1,3072
v_cmp_eq_u32 vcc,s53,v7
M 26,a[248:251],94
D 17,56,3072
M 30,a[244:247],90
s_add_u32 s55,s55,s60
s_add_u32 s68,s68,s63
L 2
s_or_b64 vcc,vcc,s[94:95]
s_cmp_eq_u32 vcc_lo,-1
R 50,1,4096
v_add_f32 v118,v118,v119
v_add_f32 v120,v120,v121
v_add_f32 v118,v118,v120
v_mul_f32 v118,s86,v118
M 30,a[252:255],94
s_nop 0
buffer_store_dword v118,v14,s[36:39],s76 offen offset:0 sc0 sc1
buffer_store_dword v4,v3,s[36:39],s71 offen offset:0 sc0 sc1
buffer_store_dword v4,v3,s[36:39],s71 offen offset:0 sc0 sc1
s_cbranch_scc1 Lg2_F0_%=
s_branch Lg2_L0_%=
Lg2_X1_%=:
M 30,a[132:135],34
s_add_u32 m0,s59,1024
R 54,2,21504
v_fma_f32 v107,v23,v23,v107
M 30,a[140:143],38
buffer_load_dword v8,s[36:39],s58 offen offset:0 sc1 lds
M 26,a[128:131],34
R 58,2,22528
v_add_f32 v106,v106,v24
M 26,a[136:139],38
L 3
v_fma_f32 v107,v24,v24,v107
v_add_f32 v106,v106,v25
M 26,a[144:147],42
R 62,2,23552
v_fma_f32 v107,v25,v25,v107
M 26,a[152:155],46
M 30,a[148:151],42
R 66,2,24576
M 30,a[156:159],46
L 3
v_permlane16_swap_b32 v106,v107
v_add_f32 v106,v106,v107
M 30,a[164:167],50
R 70,2,25600
v_mov_b32 v107,v106
M 30,a[172:175],54
M 26,a[160:163],50
R 74,2,26624
M 26,a[168:171],54
L 3
v_permlane32_swap_b32 v106,v107
M 26,a[176:179],58
R 78,2,27648
v_add_f32 v106,v106,v107
M 26,a[184:187],62
ds_write_b32 v12,v106 offset:0
M 30,a[180:183],58
R 82,2,28672
M 30,a[188:191],62
L 4
M 30,a[196:199],66
R 86,2,29696
M 30,a[204:207],70
M 26,a[192:195],66
R 90,2,30720
M 26,a[200:203],70
L 4
L 3
M 26,a[208:211],74
s_waitcnt vmcnt(0)
R 94,2,31744
M 26,a[216:219],78
s_barrier
R 118,13,0
ds_read_b32 v7,v6 offset:1024
M 30,a[212:215],74
buffer_store_dword v4,v3,s[36:39],s70 offen offset:0 sc0 sc1
M 30,a[220:223],78
L 4
s_mov_b32 s70,s69
s_mov_b32 s69,s68
M 30,a[228:231],82
s_add_u32 s68,s68,s63
M 30,a[236:239],86
M 26,a[224:227],82
M 26,a[232:235],86
L 2
L 1
L 0
M 26,a[240:243],90
v_add_f32 v118,v118,v119
v_add_f32 v120,v120,v121
v_add_f32 v118,v118,v120
M 26,a[248:251],94
v_cmp_eq_u32 vcc,s53,v7
v_mul_f32 v118,s86,v118
M 30,a[244:247],90
buffer_store_dword v118,v14,s[36:39],s76 offen offset:0 sc0 sc1
M 30,a[252:255],94
s_nop 0
s_or_b64 vcc,vcc,s[94:95]
s_mov_b32 s80,0
s_cmp_eq_u32 vcc_lo,-1
s_cbranch_scc0 Lg2_xn1_%=
s_and_b32 s78,s54,127
s_cmp_eq_u32 s78,s85
s_cselect_b64 s[40:41],-1,0
v_cndmask_b32 v17,v15,v16,s[40:41]
s_add_u32 m0,s57,0
s_add_u32 s56,s55,0
D 17,56,0
v_lshlrev_b32 v106,16,v126
v_and_b32 v107,0xffff0000,v126
v_lshlrev_b32 v108,16,v127
v_and_b32 v109,0xffff0000,v127
v_lshlrev_b32 v110,16,v128
s_add_u32 s56,s55,256
D 17,56,1024
v_and_b32 v111,0xffff0000,v128
v_lshlrev_b32 v112,16,v129
v_and_b32 v113,0xffff0000,v129
v_fma_f32 v106,v106,v103,v102
v_fma_f32 v107,v107,v103,v102
s_add_u32 s56,s55,512
D 17,56,2048
v_fma_f32 v108,v108,v103,v102
v_fma_f32 v109,v109,v103,v102
v_fma_f32 v110,v110,v103,v102
v_fma_f32 v111,v111,v103,v102
v_fma_f32 v112,v112,v103,v102
s_add_u32 s56,s55,768
D 17,56,3072
v_fma_f32 v113,v113,v103,v102
v_add_f32 v26,v26,v106
v_add_f32 v27,v27,v107
v_add_f32 v28,v28,v108
v_add_f32 v29,v29,v109
s_add_u32 s58,s58,s61
v_cndmask_b32 v17,v10,v9,s[40:41]
s_add_u32 m0,s57,16384
s_add_u32 s56,s55,20480
D 17,56,0
v_add_f32 v30,v30,v110
v_add_f32 v31,v31,v111
v_add_f32 v32,v32,v112
v_add_f32 v33,v33,v113
v_cvt_pk_bf16_f32 v114,v26,v27
s_add_u32 s56,s55,20736
D 17,56,1024
v_cvt_pk_bf16_f32 v115,v28,v29
v_cvt_pk_bf16_f32 v116,v30,v31
v_cvt_pk_bf16_f32 v117,v32,v33
s_add_u32 s56,s55,20992
D 17,56,2048
s_add_u32 s56,s55,21248
D 17,56,3072
buffer_store_dwordx4 v[114:117],v0,s[36:39],s67 offen offset:0 sc0 sc1
v_add_f32 v106,v26,v27
v_fma_f32 v107,v26,v26,s87
s_add_u32 s55,s55,s60
s_sub_u32 s57,s84,s57
s_add_u32 s54,s54,s46
v_fma_f32 v107,v27,v27,v107
v_add_f32 v106,v106,v28
v_fma_f32 v107,v28,v28,v107
v_add_f32 v106,v106,v29
v_fma_f32 v107,v29,v29,v107
v_add_f32 v106,v106,v30
v_fma_f32 v107,v30,v30,v107
v_add_f32 v106,v106,v31
v_fma_f32 v107,v31,v31,v107
v_add_f32 v106,v106,v32
v_fma_f32 v107,v32,v32,v107
v_add_f32 v106,v106,v33
v_fma_f32 v107,v33,v33,v107
s_nop 1
v_permlane16_swap_b32 v106,v107
v_add_f32 v106,v106,v107
v_mov_b32 v107,v106
s_nop 1
v_permlane32_swap_b32 v106,v107
v_add_f32 v106,v106,v107
ds_write_b32 v12,v106 offset:2048
L 0
s_barrier
R 118,13,2048
L 0
v_add_f32 v118,v118,v119
v_add_f32 v120,v120,v121
v_add_f32 v118,v118,v120
v_mul_f32 v118,s86,v118
buffer_store_dword v118,v14,s[36:39],s77 offen offset:0 sc0 sc1
s_mov_b32 s72,s69
s_mov_b32 s69,0x7fff0000
s_mov_b32 s67,0x7fff0000
s_mov_b32 s77,0x7fff0000
s_branch Lg2_gox_%=
Lg2_xn1_%=:
s_cmp_ge_u32 s54,256
s_cbranch_scc1 Lg2_fin1_%=
s_add_u32 m0,s59,4096
s_nop 0
buffer_load_dword v8,s[36:39],s58 offen offset:0 sc1 lds
buffer_store_dword v4,v3,s[36:39],s71 offen offset:0 sc0 sc1
s_add_u32 m0,s59,5120
s_nop 0
buffer_load_dword v8,s[36:39],s58 offen offset:0 sc1 lds
buffer_store_dword v4,v3,s[36:39],s71 offen offset:0 sc0 sc1
s_mov_b32 s83,0
v_lshlrev_b32 v106,16,v126
v_and_b32 v107,0xffff0000,v126
v_lshlrev_b32 v108,16,v127
v_and_b32 v109,0xffff0000,v127
v_lshlrev_b32 v110,16,v128
v_and_b32 v111,0xffff0000,v128
v_lshlrev_b32 v112,16,v129
v_and_b32 v113,0xffff0000,v129
v_fma_f32 v106,v106,v103,v102
v_fma_f32 v107,v107,v103,v102
v_fma_f32 v108,v108,v103,v102
v_fma_f32 v109,v109,v103,v102
v_fma_f32 v110,v110,v103,v102
v_fma_f32 v111,v111,v103,v102
v_fma_f32 v112,v112,v103,v102
v_fma_f32 v113,v113,v103,v102
v_add_f32 v26,v26,v106
v_add_f32 v27,v27,v107
v_add_f32 v28,v28,v108
v_add_f32 v29,v29,v109
v_add_f32 v30,v30,v110
v_add_f32 v31,v31,v111
v_add_f32 v32,v32,v112
v_add_f32 v33,v33,v113
v_cvt_pk_bf16_f32 v114,v26,v27
v_cvt_pk_bf16_f32 v115,v28,v29
v_cvt_pk_bf16_f32 v116,v30,v31
v_cvt_pk_bf16_f32 v117,v32,v33
buffer_store_dwordx4 v[114:117],v0,s[36:39],s67 offen offset:0 sc0 sc1
v_add_f32 v106,v26,v27
v_fma_f32 v107,v26,v26,s87
v_fma_f32 v107,v27,v27,v107
v_add_f32 v106,v106,v28
v_fma_f32 v107,v28,v28,v107
v_add_f32 v106,v106,v29
v_fma_f32 v107,v29,v29,v107
v_add_f32 v106,v106,v30
v_fma_f32 v107,v30,v30,v107
v_add_f32 v106,v106,v31
v_fma_f32 v107,v31,v31,v107
v_add_f32 v106,v106,v32
v_fma_f32 v107,v32,v32,v107
v_add_f32 v106,v106,v33
v_fma_f32 v107,v33,v33,v107
s_nop 1
v_permlane16_swap_b32 v106,v107
v_add_f32 v106,v106,v107
v_mov_b32 v107,v106
s_nop 1
v_permlane32_swap_b32 v106,v107
v_add_f32 v106,v106,v107
ds_write_b32 v12,v106 offset:2048
L 0
s_barrier
R 118,13,2048
L 0
v_add_f32 v118,v118,v119
v_add_f32 v120,v120,v121
v_add_f32 v118,v118,v120
v_mul_f32 v118,s86,v118
buffer_store_dword v118,v14,s[36:39],s77 offen offset:0 sc0 sc1
s_mov_b32 s72,s69
s_mov_b32 s69,0x7fff0000
s_mov_b32 s67,0x7fff0000
s_mov_b32 s77,0x7fff0000
s_branch Lg2_poll_%=
Lg2_fin1_%=:
v_lshlrev_b32 v106,16,v126
v_and_b32 v107,0xffff0000,v126
v_lshlrev_b32 v108,16,v127
v_and_b32 v109,0xffff0000,v127
v_lshlrev_b32 v110,16,v128
v_and_b32 v111,0xffff0000,v128
v_lshlrev_b32 v112,16,v129
v_and_b32 v113,0xffff0000,v129
v_fma_f32 v106,v106,v103,v102
v_fma_f32 v107,v107,v103,v102
v_fma_f32 v108,v108,v103,v102
v_fma_f32 v109,v109,v103,v102
v_fma_f32 v110,v110,v103,v102
v_fma_f32 v111,v111,v103,v102
v_fma_f32 v112,v112,v103,v102
v_fma_f32 v113,v113,v103,v102
v_add_f32 v26,v26,v106
v_add_f32 v27,v27,v107
v_add_f32 v28,v28,v108
v_add_f32 v29,v29,v109
v_add_f32 v30,v30,v110
)"
R"(v_add_f32 v31,v31,v111
v_add_f32 v32,v32,v112
v_add_f32 v33,v33,v113
v_cvt_pk_bf16_f32 v114,v26,v27
v_cvt_pk_bf16_f32 v115,v28,v29
v_cvt_pk_bf16_f32 v116,v30,v31
v_cvt_pk_bf16_f32 v117,v32,v33
buffer_store_dwordx4 v[114:117],v0,s[36:39],s67 offen offset:0 sc0 sc1
v_add_f32 v106,v26,v27
v_fma_f32 v107,v26,v26,s87
v_fma_f32 v107,v27,v27,v107
v_add_f32 v106,v106,v28
v_fma_f32 v107,v28,v28,v107
v_add_f32 v106,v106,v29
v_fma_f32 v107,v29,v29,v107
v_add_f32 v106,v106,v30
v_fma_f32 v107,v30,v30,v107
v_add_f32 v106,v106,v31
v_fma_f32 v107,v31,v31,v107
v_add_f32 v106,v106,v32
v_fma_f32 v107,v32,v32,v107
v_add_f32 v106,v106,v33
v_fma_f32 v107,v33,v33,v107
s_nop 1
v_permlane16_swap_b32 v106,v107
v_add_f32 v106,v106,v107
v_mov_b32 v107,v106
s_nop 1
v_permlane32_swap_b32 v106,v107
v_add_f32 v106,v106,v107
ds_write_b32 v12,v106 offset:2048
L 0
s_barrier
R 118,13,2048
L 0
v_add_f32 v118,v118,v119
v_add_f32 v120,v120,v121
v_add_f32 v118,v118,v120
v_mul_f32 v118,s86,v118
buffer_store_dword v118,v14,s[36:39],s77 offen offset:0 sc0 sc1
s_mov_b32 s72,s69
s_mov_b32 s69,0x7fff0000
s_mov_b32 s67,0x7fff0000
s_mov_b32 s77,0x7fff0000
s_branch Lg2_done_%=
Lg2_done_%=:
s_waitcnt vmcnt(2)
s_barrier
buffer_store_dword v4,v3,s[36:39],s70 offen offset:0 sc0 sc1
s_waitcnt vmcnt(1)
s_barrier
buffer_store_dword v4,v3,s[36:39],s72 offen offset:0 sc0 sc1
s_waitcnt vmcnt(0) lgkmcnt(0)
s_barrier
Lg2_end_%=:
s_mov_b32 %0,s53
Lcode_g12_e:
.purgem M
.purgem R
.purgem T
.purgem D
.purgem L
)"
  : "=&s"(epOut) : "s"(ws), "s"(sWave), "s"(sG0), "s"(sStep), "s"(sOutOff), "s"(sFoutOff), "s"(sAuxOff), "s"(sWw), "s"(sBuf), "s"(sExp), "v"(vL16), "v"(vFlag), "v"(vSnapOff), "v"(vSnapRd), "v"(vWd0), "v"(vWd1), "v"(vWr0), "v"(vWr1), "v"(vSp), "v"(vOutS), "v"(vAux0), "v"(vAux1), "v"(vAux2), "v"(vAux3), "v"(vAn), "v"(vAe), "s"(w12) : "v0", "v1", "v2", "v3", "v4", "v5", "v6", "v7", "v8", "v9", "v10", "v11", "v12", "v13", "v14", "v15", "v16", "v17", "v18", "v19", "v20", "v21", "v22", "v23", "v24", "v25", "v26", "v27", "v28", "v29", "v30", "v31", "v32", "v33", "v34", "v35", "v36", "v37", "v38", "v39", "v40", "v41", "v42", "v43", "v44", "v45", "v46", "v47", "v48", "v49", "v50", "v51", "v52", "v53", "v54", "v55", "v56", "v57", "v58", "v59", "v60", "v61", "v62", "v63", "v64", "v65", "v66", "v67", "v68", "v69", "v70", "v71", "v72", "v73", "v74", "v75", "v76", "v77", "v78", "v79", "v80", "v81", "v82", "v83", "v84", "v85", "v86", "v87", "v88", "v89", "v90", "v91", "v92", "v93", "v94", "v95", "v96", "v97", "v98", "v99", "v100", "v101", "v102", "v103", "v104", "v105", "v106", "v107", "v108", "v109", "v110", "v111", "v112", "v113", "v114", "v115", "v116", "v117", "v118", "v119", "v120", "v121", "v122", "v123", "v124", "v125", "v126", "v127", "v128", "v129", "v130", "v131", "v132", "v133", "v134", "v135", "v136", "v137", "v138", "v139", "v140", "v141", "v142", "v143", "v144", "v145", "v146", "v147", "v148", "v149", "v150", "v151", "v152", "v153", "v154", "v155", "v156", "v157", "v158", "v159", "v160", "v161", "v162", "v163", "v164", "v165", "v166", "v167", "v168", "v169", "v170", "v171", "v172", "v173", "v174", "v175", "v176", "v177", "v178", "v179", "v180", "v181", "v182", "v183", "v184", "v185", "v186", "v187", "v188", "v189", "v190", "v191", "v192", "v193", "v194", "v195", "v196", "v197", "v198", "v199", "v200", "v201", "v202", "v203", "v204", "v205", "v206", "v207", "v208", "v209", "v210", "v211", "v212", "v213", "v214", "v215", "v216", "v217", "v218", "v219", "v220", "v221", "v222", "v223", "v224", "v225", "v226", "v227", "v228", "v229", "v230", "v231", "a0", "a1", "a2", "a3", "a4", "a5", "a6", "a7", "a8", "a9", "a10", "a11", "a12", "a13", "a14", "a15", "a16", "a17", "a18", "a19", "a20", "a21", "a22", "a23", "a24", "a25", "a26", "a27", "a28", "a29", "a30", "a31", "a32", "a33", "a34", "a35", "a36", "a37", "a38", "a39", "a40", "a41", "a42", "a43", "a44", "a45", "a46", "a47", "a48", "a49", "a50", "a51", "a52", "a53", "a54", "a55", "a56", "a57", "a58", "a59", "a60", "a61", "a62", "a63", "a64", "a65", "a66", "a67", "a68", "a69", "a70", "a71", "a72", "a73", "a74", "a75", "a76", "a77", "a78", "a79", "a80", "a81", "a82", "a83", "a84", "a85", "a86", "a87", "a88", "a89", "a90", "a91", "a92", "a93", "a94", "a95", "a96", "a97", "a98", "a99", "a100", "a101", "a102", "a103", "a104", "a105", "a106", "a107", "a108", "a109", "a110", "a111", "a112", "a113", "a114", "a115", "a116", "a117", "a118", "a119", "a120", "a121", "a122", "a123", "a124", "a125", "a126", "a127", "a128", "a129", "a130", "a131", "a132", "a133", "a134", "a135", "a136", "a137", "a138", "a139", "a140", "a141", "a142", "a143", "a144", "a145", "a146", "a147", "a148", "a149", "a150", "a151", "a152", "a153", "a154", "a155", "a156", "a157", "a158", "a159", "a160", "a161", "a162", "a163", "a164", "a165", "a166", "a167", "a168", "a169", "a170", "a171", "a172", "a173", "a174", "a175", "a176", "a177", "a178", "a179", "a180", "a181", "a182", "a183", "a184", "a185", "a186", "a187", "a188", "a189", "a190", "a191", "a192", "a193", "a194", "a195", "a196", "a197", "a198", "a199", "a200", "a201", "a202", "a203", "a204", "a205", "a206", "a207", "a208", "a209", "a210", "a211", "a212", "a213", "a214", "a215", "a216", "a217", "a218", "a219", "a220", "a221", "a222", "a223", "a224", "a225", "a226", "a227", "a228", "a229", "a230", "a231", "a232", "a233", "a234", "a235", "a236", "a237", "a238", "a239", "a240", "a241", "a242", "a243", "a244", "a245", "a246", "a247", "a248", "a249", "a250", "a251", "a252", "a253", "a254", "a255", "s36", "s37", "s38", "s39", "s40", "s41", "s42", "s43", "s44", "s45", "s46", "s47", "s48", "s49", "s50", "s51", "s52", "s53", "s54", "s55", "s56", "s57", "s58", "s59", "s60", "s61", "s62", "s63", "s64", "s65", "s66", "s67", "s68", "s69", "s70", "s71", "s72", "s73", "s74", "s75", "s76", "s77", "s78", "s79", "s80", "s81", "s82", "s83", "s84", "s85", "s86", "s87", "s88", "s89", "s90", "s91", "s92", "s93", "s94", "s95", "s96", "s97", "vcc", "scc", "memory");
    ep = epOut;
    if (kind == 1 && 5 + (rep >> 1) < NR4) { kind = 4; kh = rep & 1; rep = 5 + (rep >> 1); }
  }
  if (kind == 4) {
    const unsigned sStep = rep < 5 ? 5 : NR4;
    COMMON(10, (kh * 9 + fj) * 128, 2560, 143360, 2048, 448, 0);
    const int cs = slice;
    const unsigned sOutOff = (kh ? OFF_P1 : OFF_P0) + (cs * 4 + wave) * 1024, sFoutOff = OFF_FP + (kh * 8 + cs) * 128;
    const unsigned sAuxOff = (kh ? OFF_XP : OFF_X1) + (cs * 4 + wave) * 1024, sWw = (kh * 1792 + 16 * wave) * 2048 + cs * 256, sBuf = kh * 57344;
    const int zu = 113 + (rep + 2) % 5;
    const unsigned vSp = lg * 2048 + l15 * 4, vSn = rep < 5 ? (zu * 16 + wave * 4 + lg) * 2048 + (cs * 2 + kh) * 128 + l15 * 8 : OOB;
    (void)vWr2;
    const unsigned sExp = __builtin_amdgcn_readfirstlane(ep);
    asm volatile(
R"(.macro M d,w,x
v_mfma_f32_16x16x32_bf16 v[\d:\d+3], \w, v[\x:\x+3], v[\d:\d+3]
.endm
.macro R d,a,o
ds_read_b128 v[\d:\d+3], v\a offset:\o
.endm
.macro T d,a,o
ds_read_b64_tr_b16 \d, v\a offset:\o
.endm
.macro D a,s,o
buffer_load_dwordx4 v\a, s[36:39], s\s offen offset:\o sc1 lds
.endm
.macro L c
s_waitcnt lgkmcnt(\c)
.endm
Lcode_g4_b:
s_mov_b64 s[36:37],%0
s_mov_b32 s38,111243392
s_mov_b32 s39,0x00020000
s_mov_b32 s44,%1
s_mov_b32 s45,%2
s_mov_b32 s46,%3
s_mov_b32 s47,%4
s_mov_b32 s48,%5
s_mov_b32 s49,%6
s_mov_b32 s50,%7
s_mov_b32 s51,%8
s_mov_b32 s93,%9
s_mov_b32 s53,%10
v_mov_b32 v0,%11
v_mov_b32 v3,%12
v_mov_b32 v5,%13
v_mov_b32 v6,%14
v_mov_b32 v10,%15
v_mov_b32 v11,%16
v_mov_b32 v12,%17
v_mov_b32 v13,%18
v_mov_b32 v9,%19
v_mov_b32 v7,%20
s_mov_b64 s[40:41],%21
s_mov_b32 s42,7340032
s_mov_b32 s43,0x00020000
s_lshl_b32 s57,s44,11
s_add_u32 s55,s50,0
s_add_u32 m0,s57,0
s_nop 0
buffer_load_dwordx4 v10,s[40:43],s55 offen offset:0  lds
s_add_u32 m0,s57,1024
s_nop 0
buffer_load_dwordx4 v11,s[40:43],s55 offen offset:0  lds
s_add_u32 m0,s57,8064
s_nop 0
buffer_load_dwordx4 v10,s[40:43],s55 offen offset:128  lds
s_add_u32 m0,s57,9088
s_nop 0
buffer_load_dwordx4 v11,s[40:43],s55 offen offset:128  lds
s_add_u32 s55,s50,131072
s_add_u32 m0,s57,16384
s_nop 0
buffer_load_dwordx4 v10,s[40:43],s55 offen offset:0  lds
s_add_u32 m0,s57,17408
s_nop 0
buffer_load_dwordx4 v11,s[40:43],s55 offen offset:0  lds
s_add_u32 m0,s57,24448
s_nop 0
buffer_load_dwordx4 v10,s[40:43],s55 offen offset:128  lds
s_add_u32 m0,s57,25472
s_nop 0
buffer_load_dwordx4 v11,s[40:43],s55 offen offset:128  lds
s_add_u32 s55,s50,262144
s_add_u32 m0,s57,32768
s_nop 0
buffer_load_dwordx4 v10,s[40:43],s55 offen offset:0  lds
s_add_u32 m0,s57,33792
s_nop 0
buffer_load_dwordx4 v11,s[40:43],s55 offen offset:0  lds
s_add_u32 m0,s57,40832
s_nop 0
buffer_load_dwordx4 v10,s[40:43],s55 offen offset:128  lds
s_add_u32 m0,s57,41856
s_nop 0
buffer_load_dwordx4 v11,s[40:43],s55 offen offset:128  lds
s_add_u32 s55,s50,393216
s_add_u32 m0,s57,49152
s_nop 0
buffer_load_dwordx4 v10,s[40:43],s55 offen offset:0  lds
s_add_u32 m0,s57,50176
s_nop 0
buffer_load_dwordx4 v11,s[40:43],s55 offen offset:0  lds
s_add_u32 m0,s57,57216
s_nop 0
buffer_load_dwordx4 v10,s[40:43],s55 offen offset:128  lds
s_add_u32 m0,s57,58240
s_nop 0
buffer_load_dwordx4 v11,s[40:43],s55 offen offset:128  lds
s_add_u32 s55,s50,524288
s_add_u32 m0,s57,65536
s_nop 0
buffer_load_dwordx4 v10,s[40:43],s55 offen offset:0  lds
s_add_u32 m0,s57,66560
s_nop 0
buffer_load_dwordx4 v11,s[40:43],s55 offen offset:0  lds
s_add_u32 m0,s57,73600
s_nop 0
buffer_load_dwordx4 v10,s[40:43],s55 offen offset:128  lds
s_add_u32 m0,s57,74624
s_nop 0
buffer_load_dwordx4 v11,s[40:43],s55 offen offset:128  lds
s_add_u32 s55,s50,655360
s_add_u32 m0,s57,81920
s_nop 0
buffer_load_dwordx4 v10,s[40:43],s55 offen offset:0  lds
s_add_u32 m0,s57,82944
s_nop 0
buffer_load_dwordx4 v11,s[40:43],s55 offen offset:0  lds
s_add_u32 m0,s57,89984
s_nop 0
buffer_load_dwordx4 v10,s[40:43],s55 offen offset:128  lds
s_add_u32 m0,s57,91008
s_nop 0
buffer_load_dwordx4 v11,s[40:43],s55 offen offset:128  lds
s_waitcnt vmcnt(20) lgkmcnt(0)
s_barrier
s_add_u32 s55,s50,786432
s_add_u32 m0,s57,98304
s_nop 0
buffer_load_dwordx4 v10,s[40:43],s55 offen offset:0  lds
s_add_u32 m0,s57,99328
s_nop 0
buffer_load_dwordx4 v11,s[40:43],s55 offen offset:0  lds
s_add_u32 m0,s57,106368
s_nop 0
buffer_load_dwordx4 v10,s[40:43],s55 offen offset:128  lds
s_add_u32 m0,s57,107392
s_nop 0
buffer_load_dwordx4 v11,s[40:43],s55 offen offset:128  lds
v_add_u32 v18,0,v12
v_add_u32 v19,0,v13
T a[0:1],18,0
T a[2:3],18,512
T a[4:5],19,0
T a[6:7],19,512
T a[8:9],18,4096
T a[10:11],18,4608
T a[12:13],19,4096
T a[14:15],19,4608
s_waitcnt vmcnt(20) lgkmcnt(0)
s_barrier
s_add_u32 s55,s50,917504
s_add_u32 m0,s57,0
s_nop 0
buffer_load_dwordx4 v10,s[40:43],s55 offen offset:0  lds
s_add_u32 m0,s57,1024
s_nop 0
buffer_load_dwordx4 v11,s[40:43],s55 offen offset:0  lds
s_add_u32 m0,s57,8064
s_nop 0
buffer_load_dwordx4 v10,s[40:43],s55 offen offset:128  lds
s_add_u32 m0,s57,9088
s_nop 0
buffer_load_dwordx4 v11,s[40:43],s55 offen offset:128  lds
v_add_u32 v18,16384,v12
v_add_u32 v19,16384,v13
T a[16:17],18,0
T a[18:19],18,512
T a[20:21],19,0
T a[22:23],19,512
T a[24:25],18,4096
T a[26:27],18,4608
T a[28:29],19,4096
T a[30:31],19,4608
s_waitcnt vmcnt(20) lgkmcnt(0)
s_barrier
s_add_u32 s55,s50,1048576
s_add_u32 m0,s57,16384
s_nop 0
buffer_load_dwordx4 v10,s[40:43],s55 offen offset:0  lds
s_add_u32 m0,s57,17408
s_nop 0
buffer_load_dwordx4 v11,s[40:43],s55 offen offset:0  lds
s_add_u32 m0,s57,24448
s_nop 0
buffer_load_dwordx4 v10,s[40:43],s55 offen offset:128  lds
s_add_u32 m0,s57,25472
s_nop 0
buffer_load_dwordx4 v11,s[40:43],s55 offen offset:128  lds
v_add_u32 v18,32768,v12
v_add_u32 v19,32768,v13
T a[32:33],18,0
T a[34:35],18,512
T a[36:37],19,0
T a[38:39],19,512
T a[40:41],18,4096
T a[42:43],18,4608
T a[44:45],19,4096
T a[46:47],19,4608
s_waitcnt vmcnt(20) lgkmcnt(0)
s_barrier
s_add_u32 s55,s50,1179648
s_add_u32 m0,s57,32768
s_nop 0
buffer_load_dwordx4 v10,s[40:43],s55 offen offset:0  lds
s_add_u32 m0,s57,33792
s_nop 0
buffer_load_dwordx4 v11,s[40:43],s55 offen offset:0  lds
s_add_u32 m0,s57,40832
s_nop 0
buffer_load_dwordx4 v10,s[40:43],s55 offen offset:128  lds
s_add_u32 m0,s57,41856
s_nop 0
buffer_load_dwordx4 v11,s[40:43],s55 offen offset:128  lds
v_add_u32 v18,49152,v12
v_add_u32 v19,49152,v13
T a[48:49],18,0
T a[50:51],18,512
T a[52:53],19,0
T a[54:55],19,512
T a[56:57],18,4096
T a[58:59],18,4608
T a[60:61],19,4096
T a[62:63],19,4608
s_waitcnt vmcnt(20) lgkmcnt(0)
s_barrier
s_add_u32 s55,s50,1310720
s_add_u32 m0,s57,49152
s_nop 0
buffer_load_dwordx4 v10,s[40:43],s55 offen offset:0  lds
s_add_u32 m0,s57,50176
s_nop 0
buffer_load_dwordx4 v11,s[40:43],s55 offen offset:0  lds
s_add_u32 m0,s57,57216
s_nop 0
buffer_load_dwordx4 v10,s[40:43],s55 offen offset:128  lds
s_add_u32 m0,s57,58240
s_nop 0
buffer_load_dwordx4 v11,s[40:43],s55 offen offset:128  lds
v_add_u32 v18,65536,v12
v_add_u32 v19,65536,v13
T a[64:65],18,0
T a[66:67],18,512
T a[68:69],19,0
T a[70:71],19,512
T a[72:73],18,4096
T a[74:75],18,4608
T a[76:77],19,4096
T a[78:79],19,4608
s_waitcnt vmcnt(20) lgkmcnt(0)
s_barrier
s_add_u32 s55,s50,1441792
s_add_u32 m0,s57,65536
s_nop 0
buffer_load_dwordx4 v10,s[40:43],s55 offen offset:0  lds
s_add_u32 m0,s57,66560
s_nop 0
buffer_load_dwordx4 v11,s[40:43],s55 offen offset:0  lds
s_add_u32 m0,s57,73600
s_nop 0
buffer_load_dwordx4 v10,s[40:43],s55 offen offset:128  lds
s_add_u32 m0,s57,74624
s_nop 0
buffer_load_dwordx4 v11,s[40:43],s55 offen offset:128  lds
v_add_u32 v18,81920,v12
v_add_u32 v19,81920,v13
T a[80:81],18,0
T a[82:83],18,512
T a[84:85],19,0
T a[86:87],19,512
T a[88:89],18,4096
T a[90:91],18,4608
T a[92:93],19,4096
T a[94:95],19,4608
s_waitcnt vmcnt(20) lgkmcnt(0)
s_barrier
s_add_u32 s55,s50,1572864
s_add_u32 m0,s57,81920
s_nop 0
buffer_load_dwordx4 v10,s[40:43],s55 offen offset:0  lds
s_add_u32 m0,s57,82944
s_nop 0
buffer_load_dwordx4 v11,s[40:43],s55 offen offset:0  lds
s_add_u32 m0,s57,89984
s_nop 0
buffer_load_dwordx4 v10,s[40:43],s55 offen offset:128  lds
s_add_u32 m0,s57,91008
s_nop 0
buffer_load_dwordx4 v11,s[40:43],s55 offen offset:128  lds
v_add_u32 v18,98304,v12
v_add_u32 v19,98304,v13
T a[96:97],18,0
T a[98:99],18,512
T a[100:101],19,0
T a[102:103],19,512
T a[104:105],18,4096
T a[106:107],18,4608
T a[108:109],19,4096
T a[110:111],19,4608
s_waitcnt vmcnt(20) lgkmcnt(0)
s_barrier
s_add_u32 s55,s50,1703936
s_add_u32 m0,s57,98304
s_nop 0
buffer_load_dwordx4 v10,s[40:43],s55 offen offset:0  lds
s_add_u32 m0,s57,99328
s_nop 0
buffer_load_dwordx4 v11,s[40:43],s55 offen offset:0  lds
s_add_u32 m0,s57,106368
s_nop 0
buffer_load_dwordx4 v10,s[40:43],s55 offen offset:128  lds
s_add_u32 m0,s57,107392
s_nop 0
buffer_load_dwordx4 v11,s[40:43],s55 offen offset:128  lds
v_add_u32 v18,0,v12
v_add_u32 v19,0,v13
T a[112:113],18,0
T a[114:115],18,512
T a[116:117],19,0
T a[118:119],19,512
T a[120:121],18,4096
T a[122:123],18,4608
T a[124:125],19,4096
T a[126:127],19,4608
s_waitcnt vmcnt(20) lgkmcnt(0)
s_barrier
s_add_u32 s55,s50,1835008
s_add_u32 m0,s57,0
s_nop 0
buffer_load_dwordx4 v10,s[40:43],s55 offen offset:0  lds
s_add_u32 m0,s57,1024
s_nop 0
buffer_load_dwordx4 v11,s[40:43],s55 offen offset:0  lds
s_add_u32 m0,s57,8064
s_nop 0
buffer_load_dwordx4 v10,s[40:43],s55 offen offset:128  lds
s_add_u32 m0,s57,9088
s_nop 0
buffer_load_dwordx4 v11,s[40:43],s55 offen offset:128  lds
v_add_u32 v18,16384,v12
v_add_u32 v19,16384,v13
T a[128:129],18,0
T a[130:131],18,512
T a[132:133],19,0
T a[134:135],19,512
T a[136:137],18,4096
T a[138:139],18,4608
T a[140:141],19,4096
T a[142:143],19,4608
s_waitcnt vmcnt(20) lgkmcnt(0)
s_barrier
s_add_u32 s55,s50,1966080
s_add_u32 m0,s57,16384
s_nop 0
buffer_load_dwordx4 v10,s[40:43],s55 offen offset:0  lds
s_add_u32 m0,s57,17408
s_nop 0
buffer_load_dwordx4 v11,s[40:43],s55 offen offset:0  lds
s_add_u32 m0,s57,24448
s_nop 0
buffer_load_dwordx4 v10,s[40:43],s55 offen offset:128  lds
s_add_u32 m0,s57,25472
s_nop 0
buffer_load_dwordx4 v11,s[40:43],s55 offen offset:128  lds
v_add_u32 v18,32768,v12
v_add_u32 v19,32768,v13
T a[144:145],18,0
T a[146:147],18,512
T a[148:149],19,0
T a[150:151],19,512
T a[152:153],18,4096
T a[154:155],18,4608
T a[156:157],19,4096
T a[158:159],19,4608
s_waitcnt vmcnt(20) lgkmcnt(0)
s_barrier
s_add_u32 s55,s50,2097152
s_add_u32 m0,s57,32768
s_nop 0
buffer_load_dwordx4 v10,s[40:43],s55 offen offset:0  lds
s_add_u32 m0,s57,33792
s_nop 0
buffer_load_dwordx4 v11,s[40:43],s55 offen offset:0  lds
s_add_u32 m0,s57,40832
s_nop 0
buffer_load_dwordx4 v10,s[40:43],s55 offen offset:128  lds
s_add_u32 m0,s57,41856
s_nop 0
buffer_load_dwordx4 v11,s[40:43],s55 offen offset:128  lds
v_add_u32 v18,49152,v12
v_add_u32 v19,49152,v13
T a[160:161],18,0
T a[162:163],18,512
T a[164:165],19,0
T a[166:167],19,512
T a[168:169],18,4096
T a[170:171],18,4608
T a[172:173],19,4096
T a[174:175],19,4608
s_waitcnt vmcnt(20) lgkmcnt(0)
s_barrier
s_add_u32 s55,s50,2228224
s_add_u32 m0,s57,49152
s_nop 0
buffer_load_dwordx4 v10,s[40:43],s55 offen offset:0  lds
s_add_u32 m0,s57,50176
s_nop 0
buffer_load_dwordx4 v11,s[40:43],s55 offen offset:0  lds
s_add_u32 m0,s57,57216
s_nop 0
buffer_load_dwordx4 v10,s[40:43],s55 offen offset:128  lds
s_add_u32 m0,s57,58240
s_nop 0
buffer_load_dwordx4 v11,s[40:43],s55 offen offset:128  lds
v_add_u32 v18,65536,v12
v_add_u32 v19,65536,v13
T a[176:177],18,0
T a[178:179],18,512
T a[180:181],19,0
T a[182:183],19,512
T a[184:185],18,4096
T a[186:187],18,4608
T a[188:189],19,4096
T a[190:191],19,4608
s_waitcnt vmcnt(20) lgkmcnt(0)
s_barrier
s_add_u32 s55,s50,2359296
s_add_u32 m0,s57,65536
s_nop 0
buffer_load_dwordx4 v10,s[40:43],s55 offen offset:0  lds
s_add_u32 m0,s57,66560
s_nop 0
buffer_load_dwordx4 v11,s[40:43],s55 offen offset:0  lds
s_add_u32 m0,s57,73600
s_nop 0
buffer_load_dwordx4 v10,s[40:43],s55 offen offset:128  lds
s_add_u32 m0,s57,74624
s_nop 0
buffer_load_dwordx4 v11,s[40:43],s55 offen offset:128  lds
v_add_u32 v18,81920,v12
v_add_u32 v19,81920,v13
T a[192:193],18,0
T a[194:195],18,512
T a[196:197],19,0
T a[198:199],19,512
T a[200:201],18,4096
T a[202:203],18,4608
T a[204:205],19,4096
T a[206:207],19,4608
s_waitcnt vmcnt(20) lgkmcnt(0)
s_barrier
s_add_u32 s55,s50,2490368
s_add_u32 m0,s57,81920
s_nop 0
buffer_load_dwordx4 v10,s[40:43],s55 offen offset:0  lds
s_add_u32 m0,s57,82944
s_nop 0
buffer_load_dwordx4 v11,s[40:43],s55 offen offset:0  lds
s_add_u32 m0,s57,89984
s_nop 0
buffer_load_dwordx4 v10,s[40:43],s55 offen offset:128  lds
s_add_u32 m0,s57,91008
s_nop 0
buffer_load_dwordx4 v11,s[40:43],s55 offen offset:128  lds
v_add_u32 v18,98304,v12
v_add_u32 v19,98304,v13
T a[208:209],18,0
T a[210:211],18,512
T a[212:213],19,0
T a[214:215],19,512
T a[216:217],18,4096
T a[218:219],18,4608
T a[220:221],19,4096
T a[222:223],19,4608
s_waitcnt vmcnt(20) lgkmcnt(0)
s_barrier
s_add_u32 s55,s50,2621440
s_add_u32 m0,s57,98304
s_nop 0
buffer_load_dwordx4 v10,s[40:43],s55 offen offset:0  lds
s_add_u32 m0,s57,99328
s_nop 0
buffer_load_dwordx4 v11,s[40:43],s55 offen offset:0  lds
s_add_u32 m0,s57,106368
s_nop 0
buffer_load_dwordx4 v10,s[40:43],s55 offen offset:128  lds
s_add_u32 m0,s57,107392
s_nop 0
buffer_load_dwordx4 v11,s[40:43],s55 offen offset:128  lds
v_add_u32 v18,0,v12
v_add_u32 v19,0,v13
T a[224:225],18,0
T a[226:227],18,512
T a[228:229],19,0
T a[230:231],19,512
T a[232:233],18,4096
T a[234:235],18,4608
T a[236:237],19,4096
T a[238:239],19,4608
s_waitcnt vmcnt(20) lgkmcnt(0)
s_barrier
s_add_u32 s55,s50,2752512
s_add_u32 m0,s57,0
)"
R"(s_nop 0
buffer_load_dwordx4 v10,s[40:43],s55 offen offset:0  lds
s_add_u32 m0,s57,1024
s_nop 0
buffer_load_dwordx4 v11,s[40:43],s55 offen offset:0  lds
s_add_u32 m0,s57,8064
s_nop 0
buffer_load_dwordx4 v10,s[40:43],s55 offen offset:128  lds
s_add_u32 m0,s57,9088
s_nop 0
buffer_load_dwordx4 v11,s[40:43],s55 offen offset:128  lds
v_add_u32 v18,16384,v12
v_add_u32 v19,16384,v13
T a[240:241],18,0
T a[242:243],18,512
T a[244:245],19,0
T a[246:247],19,512
T a[248:249],18,4096
T a[250:251],18,4608
T a[252:253],19,4096
T a[254:255],19,4608
s_waitcnt vmcnt(20) lgkmcnt(0)
s_barrier
s_add_u32 s55,s50,2883584
s_add_u32 m0,s57,16384
s_nop 0
buffer_load_dwordx4 v10,s[40:43],s55 offen offset:0  lds
s_add_u32 m0,s57,17408
s_nop 0
buffer_load_dwordx4 v11,s[40:43],s55 offen offset:0  lds
s_add_u32 m0,s57,24448
s_nop 0
buffer_load_dwordx4 v10,s[40:43],s55 offen offset:128  lds
s_add_u32 m0,s57,25472
s_nop 0
buffer_load_dwordx4 v11,s[40:43],s55 offen offset:128  lds
v_add_u32 v18,32768,v12
v_add_u32 v19,32768,v13
T v[64:65],18,0
T v[66:67],18,512
T v[68:69],19,0
T v[70:71],19,512
T v[72:73],18,4096
T v[74:75],18,4608
T v[76:77],19,4096
T v[78:79],19,4608
s_waitcnt vmcnt(20) lgkmcnt(0)
s_barrier
s_add_u32 s55,s50,3014656
s_add_u32 m0,s57,32768
s_nop 0
buffer_load_dwordx4 v10,s[40:43],s55 offen offset:0  lds
s_add_u32 m0,s57,33792
s_nop 0
buffer_load_dwordx4 v11,s[40:43],s55 offen offset:0  lds
s_add_u32 m0,s57,40832
s_nop 0
buffer_load_dwordx4 v10,s[40:43],s55 offen offset:128  lds
s_add_u32 m0,s57,41856
s_nop 0
buffer_load_dwordx4 v11,s[40:43],s55 offen offset:128  lds
v_add_u32 v18,49152,v12
v_add_u32 v19,49152,v13
T v[80:81],18,0
T v[82:83],18,512
T v[84:85],19,0
T v[86:87],19,512
T v[88:89],18,4096
T v[90:91],18,4608
T v[92:93],19,4096
T v[94:95],19,4608
s_waitcnt vmcnt(20) lgkmcnt(0)
s_barrier
s_add_u32 s55,s50,3145728
s_add_u32 m0,s57,49152
s_nop 0
buffer_load_dwordx4 v10,s[40:43],s55 offen offset:0  lds
s_add_u32 m0,s57,50176
s_nop 0
buffer_load_dwordx4 v11,s[40:43],s55 offen offset:0  lds
s_add_u32 m0,s57,57216
s_nop 0
buffer_load_dwordx4 v10,s[40:43],s55 offen offset:128  lds
s_add_u32 m0,s57,58240
s_nop 0
buffer_load_dwordx4 v11,s[40:43],s55 offen offset:128  lds
v_add_u32 v18,65536,v12
v_add_u32 v19,65536,v13
T v[96:97],18,0
T v[98:99],18,512
T v[100:101],19,0
T v[102:103],19,512
T v[104:105],18,4096
T v[106:107],18,4608
T v[108:109],19,4096
T v[110:111],19,4608
s_waitcnt vmcnt(20) lgkmcnt(0)
s_barrier
s_add_u32 s55,s50,3276800
s_add_u32 m0,s57,65536
s_nop 0
buffer_load_dwordx4 v10,s[40:43],s55 offen offset:0  lds
s_add_u32 m0,s57,66560
s_nop 0
buffer_load_dwordx4 v11,s[40:43],s55 offen offset:0  lds
s_add_u32 m0,s57,73600
s_nop 0
buffer_load_dwordx4 v10,s[40:43],s55 offen offset:128  lds
s_add_u32 m0,s57,74624
s_nop 0
buffer_load_dwordx4 v11,s[40:43],s55 offen offset:128  lds
v_add_u32 v18,81920,v12
v_add_u32 v19,81920,v13
T v[112:113],18,0
T v[114:115],18,512
T v[116:117],19,0
T v[118:119],19,512
T v[120:121],18,4096
T v[122:123],18,4608
T v[124:125],19,4096
T v[126:127],19,4608
s_waitcnt vmcnt(20) lgkmcnt(0)
s_barrier
s_add_u32 s55,s50,3407872
s_add_u32 m0,s57,81920
s_nop 0
buffer_load_dwordx4 v10,s[40:43],s55 offen offset:0  lds
s_add_u32 m0,s57,82944
s_nop 0
buffer_load_dwordx4 v11,s[40:43],s55 offen offset:0  lds
s_add_u32 m0,s57,89984
s_nop 0
buffer_load_dwordx4 v10,s[40:43],s55 offen offset:128  lds
s_add_u32 m0,s57,91008
s_nop 0
buffer_load_dwordx4 v11,s[40:43],s55 offen offset:128  lds
v_add_u32 v18,98304,v12
v_add_u32 v19,98304,v13
T v[128:129],18,0
T v[130:131],18,512
T v[132:133],19,0
T v[134:135],19,512
T v[136:137],18,4096
T v[138:139],18,4608
T v[140:141],19,4096
T v[142:143],19,4608
s_waitcnt vmcnt(20) lgkmcnt(0)
s_barrier
s_add_u32 s55,s50,3538944
s_add_u32 m0,s57,98304
s_nop 0
buffer_load_dwordx4 v10,s[40:43],s55 offen offset:0  lds
s_add_u32 m0,s57,99328
s_nop 0
buffer_load_dwordx4 v11,s[40:43],s55 offen offset:0  lds
s_add_u32 m0,s57,106368
s_nop 0
buffer_load_dwordx4 v10,s[40:43],s55 offen offset:128  lds
s_add_u32 m0,s57,107392
s_nop 0
buffer_load_dwordx4 v11,s[40:43],s55 offen offset:128  lds
v_add_u32 v18,0,v12
v_add_u32 v19,0,v13
T v[144:145],18,0
T v[146:147],18,512
T v[148:149],19,0
T v[150:151],19,512
T v[152:153],18,4096
T v[154:155],18,4608
T v[156:157],19,4096
T v[158:159],19,4608
s_waitcnt vmcnt(20) lgkmcnt(0)
s_barrier
v_add_u32 v18,16384,v12
v_add_u32 v19,16384,v13
T v[160:161],18,0
T v[162:163],18,512
T v[164:165],19,0
T v[166:167],19,512
T v[168:169],18,4096
T v[170:171],18,4608
T v[172:173],19,4096
T v[174:175],19,4608
s_waitcnt vmcnt(16) lgkmcnt(0)
s_barrier
v_add_u32 v18,32768,v12
v_add_u32 v19,32768,v13
T v[176:177],18,0
T v[178:179],18,512
T v[180:181],19,0
T v[182:183],19,512
T v[184:185],18,4096
T v[186:187],18,4608
T v[188:189],19,4096
T v[190:191],19,4608
s_waitcnt vmcnt(12) lgkmcnt(0)
s_barrier
v_add_u32 v18,49152,v12
v_add_u32 v19,49152,v13
T v[192:193],18,0
T v[194:195],18,512
T v[196:197],19,0
T v[198:199],19,512
T v[200:201],18,4096
T v[202:203],18,4608
T v[204:205],19,4096
T v[206:207],19,4608
s_waitcnt vmcnt(8) lgkmcnt(0)
s_barrier
v_add_u32 v18,65536,v12
v_add_u32 v19,65536,v13
T v[208:209],18,0
T v[210:211],18,512
T v[212:213],19,0
T v[214:215],19,512
T v[216:217],18,4096
T v[218:219],18,4608
T v[220:221],19,4096
T v[222:223],19,4608
s_waitcnt vmcnt(4) lgkmcnt(0)
s_barrier
v_add_u32 v18,81920,v12
v_add_u32 v19,81920,v13
T v[224:225],18,0
T v[226:227],18,512
T v[228:229],19,0
T v[230:231],19,512
T v[232:233],18,4096
T v[234:235],18,4608
T v[236:237],19,4096
T v[238:239],19,4608
s_waitcnt vmcnt(0) lgkmcnt(0)
s_barrier
v_add_u32 v18,98304,v12
v_add_u32 v19,98304,v13
T v[240:241],18,0
T v[242:243],18,512
T v[244:245],19,0
T v[246:247],19,512
T v[248:249],18,4096
T v[250:251],18,4608
T v[252:253],19,4096
T v[254:255],19,4608
L 0
s_barrier
v_mov_b32 v4,s53
s_mov_b32 s82,0
s_mov_b32 s80,0
s_mov_b32 s54,s45
s_mul_i32 s57,s44,7168
s_mul_i32 s55,s45,114688
s_add_u32 s55,s55,s57
s_add_u32 s55,s55,40894464
s_mul_i32 s57,s44,7168
s_mul_i32 s60,s46,114688
s_mul_i32 s61,s46,2560
s_mul_i32 s62,s46,32768
s_mul_i32 s63,s46,2048
s_mul_i32 s64,s46,512
s_lshl_b32 s59,s44,8
s_add_u32 s59,s59,143360
s_mul_i32 s58,s45,2560
s_add_u32 s58,s58,109891584
s_mul_i32 s65,s45,32768
s_add_u32 s65,s65,s47
s_mul_i32 s68,s45,2048
s_add_u32 s68,s68,s48
s_mul_i32 s73,s45,512
s_add_u32 s73,s73,106954752
s_mul_i32 s74,s45,32768
s_add_u32 s74,s74,s49
s_mul_i32 s75,s45,1024
s_add_u32 s75,s75,107479040
s_mov_b32 s66,0x7fff0000
s_mov_b32 s67,0x7fff0000
s_mov_b32 s69,0x7fff0000
s_mov_b32 s76,0x7fff0000
s_mov_b32 s77,0x7fff0000
s_mov_b32 s70,0x7fff0000
s_mov_b32 s71,0x7fff0000
s_mov_b32 s72,0x7fff0000
v_mov_b32 v1,v0
v_add_u32 v2,28672,v0
s_mov_b32 s84,28672
s_mov_b32 s86,0x3a800000
s_mov_b32 s87,0x3727c5ac
s_mov_b32 s88,0xc0135761
s_mov_b32 s89,0x3d372713
s_lshr_b32 s85,s44,1
s_mul_i32 s85,s85,127
s_add_u32 s55,s55,s51
s_mul_i32 s78,s45,114688
s_sub_u32 s60,s55,s78
s_cmp_lt_u32 s45,214
s_cselect_b32 s63,214,-1
v_lshlrev_b32 v8,22,v0
v_and_b32 v8,0x80000000,v8
v_or_b32 v8,v8,v5
v_add_u32 v2,57344,v0
s_lshl_b32 s84,s57,1
s_add_u32 s84,s84,57344
s_mov_b64 s[86:87],%22
s_mov_b64 s[40:41],%22
s_mov_b32 s42,4194304
s_and_b32 s78,s47,65535
s_lshr_b32 s78,s78,4
s_sub_u32 s85,s47,s78
s_add_u32 s85,s85,4194304
s_mul_i32 s73,s44,1280
s_add_u32 s73,s73,114688
v_lshrrev_b32 v62,4,v9
v_and_b32 v62,3,v62
v_mul_u32_u24 v62,288,v62
v_lshrrev_b32 v63,7,v9
v_and_b32 v5,12,v9
v_add3_u32 v5,v5,v62,v63
v_add_u32 v5,s73,v5
v_mov_b32 v10,0
v_mov_b32 v11,0
v_mov_b32 v12,0
v_mov_b32 v13,0
s_mov_b32 s79,0
buffer_store_dwordx2 v[10:11],v7,s[40:43],s79 offen offset:0 sc0 sc1
s_mov_b32 s79,163840
buffer_store_dwordx2 v[10:11],v7,s[40:43],s79 offen offset:0 sc0 sc1
s_mov_b32 s79,327680
buffer_store_dwordx2 v[10:11],v7,s[40:43],s79 offen offset:0 sc0 sc1
s_getpc_b64 s[40:41]
Lg4_pc_%=:
s_lshl_b32 s78,s44,13
v_lshlrev_b32 v62,3,v0
v_add_u32 v62,s78,v62
v_min_u32 v62,Lcode_g4_e-Lg4_pc_%=-4,v62
s_mov_b32 m0,163584
s_nop 0
global_load_lds_dword v62,s[40:41]
s_add_u32 m0,s59,4096
s_nop 0
buffer_load_dword v8,s[36:39],s58 offen offset:0 sc1 lds
buffer_store_dword v4,v3,s[36:39],s71 offen offset:0 sc0 sc1
s_add_u32 m0,s59,5120
s_nop 0
buffer_load_dword v8,s[36:39],s58 offen offset:0 sc1 lds
buffer_store_dword v4,v3,s[36:39],s71 offen offset:0 sc0 sc1
s_mov_b32 s83,0
Lg4_poll_%=:
s_add_u32 s78,s83,2
s_and_b32 s78,s78,3
s_lshl_b32 s78,s78,10
s_add_u32 s78,s78,4096
s_add_u32 m0,s59,s78
s_nop 0
buffer_load_dword v8,s[36:39],s58 offen offset:0 sc1 lds
s_and_b32 s78,s83,3
s_lshl_b32 s78,s78,10
v_add_u32 v62,s78,v6
s_waitcnt vmcnt(4)
s_barrier
ds_read_b32 v7,v62 offset:4096
buffer_store_dword v4,v3,s[36:39],s70 offen offset:0 sc0 sc1
s_mov_b32 s70,s71
s_mov_b32 s71,s72
s_mov_b32 s72,0x7fff0000
s_add_u32 s83,s83,1
s_add_u32 s82,s82,1
L 0
v_cmp_eq_u32 vcc,s53,v7
s_nop 0
s_cmp_eq_u32 vcc_lo,-1
s_cbranch_scc1 Lg4_go_%=
s_cmp_lt_u32 s82,20000
s_cbranch_scc1 Lg4_poll_%=
Lg4_go_%=:
s_add_u32 m0,s57,0
s_add_u32 s56,s55,0
D 0,56,0
D 0,56,1024
D 0,56,2048
D 0,56,3072
s_add_u32 m0,s57,4096
s_add_u32 s56,s56,4096
D 0,56,0
D 0,56,1024
D 0,56,2048
s_add_u32 s61,s54,s46
s_cmp_ge_u32 s61,s63
s_cselect_b32 s61,s93,s61
s_cselect_b32 s46,7,s46
s_cselect_b32 s63,-1,s63
s_mul_i32 s58,s61,2560
s_add_u32 s58,s58,109891584
s_add_u32 m0,s59,2048
s_nop 0
buffer_load_dword v8,s[36:39],s58 offen offset:0 sc1 lds
s_add_u32 m0,s57,28672
s_add_u32 s56,s55,28672
D 0,56,0
D 0,56,1024
D 0,56,2048
D 0,56,3072
s_add_u32 m0,s57,32768
s_add_u32 s56,s56,4096
D 0,56,0
D 0,56,1024
D 0,56,2048
s_mul_i32 s55,s61,114688
s_add_u32 s55,s55,s60
s_sub_u32 s57,s84,s57
s_mov_b32 s54,s61
s_waitcnt vmcnt(7)
s_barrier
buffer_store_dword v4,v3,s[36:39],s70 offen offset:0 sc0 sc1
s_min_u32 s71,s71,s72
buffer_store_dword v4,v3,s[36:39],s71 offen offset:0 sc0 sc1
s_mov_b32 s70,0x7fff0000
s_mov_b32 s71,0x7fff0000
s_mov_b32 s72,0x7fff0000
s_cmp_eq_u32 s80,1
s_cbranch_scc1 Lg4_st1_%=
s_branch Lg4_st0_%=
Lg4_gox_%=:
s_add_u32 m0,s59,2048
s_nop 0
buffer_load_dword v8,s[36:39],s58 offen offset:0 sc1 lds
s_waitcnt vmcnt(0)
s_barrier
buffer_store_dword v4,v3,s[36:39],s70 offen offset:0 sc0 sc1
s_min_u32 s71,s71,s72
buffer_store_dword v4,v3,s[36:39],s71 offen offset:0 sc0 sc1
s_mov_b32 s70,0x7fff0000
s_mov_b32 s71,0x7fff0000
s_mov_b32 s72,0x7fff0000
s_cmp_eq_u32 s80,1
s_cbranch_scc1 Lg4_st1_%=
Lg4_st0_%=:
ds_read_b32 v7,v6 offset:2048
R 26,1,0
R 30,1,1024
R 34,1,2048
R 38,1,3072
R 42,1,4096
L 5
v_cmp_eq_u32 vcc,s53,v7
L 3
s_cmp_eq_u32 vcc_lo,-1
s_cbranch_scc1 Lg4_F0_%=
s_branch Lg4_L0_%=
Lg4_st1_%=:
ds_read_b32 v7,v6 offset:2048
R 26,2,0
R 30,2,1024
R 34,2,2048
R 38,2,3072
R 42,2,4096
L 5
v_cmp_eq_u32 vcc,s53,v7
L 3
s_cmp_eq_u32 vcc_lo,-1
s_cbranch_scc1 Lg4_F1_%=
s_branch Lg4_L1_%=
Lg4_F0_%=:
v_mfma_f32_16x16x32_bf16 v[14:17],a[4:7],v[26:29],0
s_add_u32 m0,s57,0
R 46,1,5120
v_lshlrev_b32 v62,16,v58
M 14,a[12:15],30
s_add_u32 s56,s55,0
v_add_f32 v18,v18,v62
v_and_b32 v63,0xffff0000,v58
v_mfma_f32_16x16x32_bf16 v[10:13],a[0:3],v[26:29],0
s_add_u32 s61,s54,s46
R 50,1,6144
v_add_f32 v19,v19,v63
M 10,a[8:11],30
L 3
D 0,56,0
M 10,a[16:19],34
s_cmp_ge_u32 s61,s63
R 54,1,7168
v_lshlrev_b32 v62,16,v59
M 10,a[24:27],38
s_cselect_b32 s61,s93,s61
v_add_f32 v20,v20,v62
v_and_b32 v63,0xffff0000,v59
M 14,a[20:23],34
s_cselect_b32 s46,7,s46
R 26,1,8192
v_add_f32 v21,v21,v63
M 14,a[28:31],38
L 3
v_lshlrev_b32 v62,16,v60
v_add_f32 v22,v22,v62
M 14,a[36:39],42
s_cselect_b32 s63,-1,s63
R 30,1,9216
v_and_b32 v63,0xffff0000,v60
M 14,a[44:47],46
D 0,56,1024
M 10,a[32:35],42
s_mul_i32 s58,s61,2560
)"
R"(R 34,1,10240
v_add_f32 v23,v23,v63
M 10,a[40:43],46
L 3
v_lshlrev_b32 v62,16,v61
v_add_f32 v24,v24,v62
M 10,a[48:51],50
s_add_u32 s58,s58,109891584
R 38,1,11264
v_and_b32 v63,0xffff0000,v61
M 10,a[56:59],54
buffer_load_dwordx4 v[58:61],v0,s[36:39],s74 offen offset:0 sc1
M 14,a[52:55],50
s_lshl_b32 s62,s54,15
R 42,1,12288
v_add_f32 v25,v25,v63
M 14,a[60:63],54
L 3
v_cvt_pk_bf16_f32 v18,v18,v19
v_cvt_pk_bf16_f32 v19,v20,v21
M 14,a[68:71],26
s_mov_b32 s66,s65
R 46,1,13312
v_cvt_pk_bf16_f32 v20,v22,v23
M 14,a[76:79],30
D 0,56,2048
M 10,a[64:67],26
s_add_u32 s74,s49,s62
R 50,1,14336
v_cvt_pk_bf16_f32 v21,v24,v25
M 10,a[72:75],30
L 3
buffer_store_dwordx4 v[18:21],v0,s[36:39],s67 offen offset:0 sc0 sc1
M 10,a[80:83],34
s_add_u32 s65,s47,s62
s_mov_b32 s54,s61
R 54,1,15360
M 10,a[88:91],38
M 14,a[84:87],34
R 26,1,16384
M 14,a[92:95],38
L 3
M 14,a[100:103],42
R 30,1,17408
M 14,a[108:111],46
D 0,56,3072
M 10,a[96:99],42
s_add_u32 m0,s57,4096
s_add_u32 s56,s56,4096
R 34,1,18432
M 10,a[104:107],46
L 3
M 10,a[112:115],50
R 38,1,19456
M 10,a[120:123],54
M 14,a[116:119],50
R 42,1,20480
M 14,a[124:127],54
L 3
D 0,56,0
M 14,a[132:135],26
R 46,1,21504
M 14,a[140:143],30
M 10,a[128:131],26
R 50,1,22528
M 10,a[136:139],30
L 3
M 10,a[144:147],34
R 54,1,23552
M 10,a[152:155],38
M 14,a[148:151],34
R 26,1,24576
M 14,a[156:159],38
L 3
D 0,56,1024
M 14,a[164:167],42
R 30,1,25600
M 14,a[172:175],46
M 10,a[160:163],42
R 34,1,26624
M 10,a[168:171],46
L 3
M 10,a[176:179],50
s_waitcnt vmcnt(8)
R 38,1,27648
M 10,a[184:187],54
s_barrier
M 14,a[180:183],50
R 42,1,28672
M 14,a[188:191],54
L 3
D 0,56,2048
M 14,a[196:199],26
s_add_u32 m0,s59,0
s_add_u32 s56,s55,28672
R 46,1,29696
M 14,a[204:207],30
s_mul_i32 s55,s61,114688
s_add_u32 s55,s55,s60
M 10,a[192:195],26
R 50,1,30720
M 10,a[200:203],30
L 3
M 10,a[208:211],34
R 54,1,31744
M 10,a[216:219],38
M 14,a[212:215],34
R 26,1,32768
M 14,a[220:223],38
L 3
buffer_load_dword v8,s[36:39],s58 offen offset:0 sc1 lds
M 14,a[228:231],42
s_add_u32 m0,s57,28672
R 30,1,33792
M 14,a[236:239],46
D 0,56,0
M 10,a[224:227],42
R 34,1,34816
M 10,a[232:235],46
L 3
M 10,a[240:243],50
R 38,1,35840
M 10,a[248:251],54
M 14,a[244:247],50
R 42,1,36864
M 14,a[252:255],54
L 3
M 14,v[68:71],26
R 46,1,37888
M 14,v[76:79],30
D 0,56,1024
M 10,v[64:67],26
R 50,1,38912
M 10,v[72:75],30
L 3
M 10,v[80:83],34
R 54,1,39936
M 10,v[88:91],38
M 14,v[84:87],34
R 26,1,40960
M 14,v[92:95],38
L 3
M 14,v[100:103],42
R 30,1,41984
M 14,v[108:111],46
D 0,56,2048
M 10,v[96:99],42
R 34,1,43008
M 10,v[104:107],46
L 3
M 10,v[112:115],50
R 38,1,44032
M 10,v[120:123],54
M 14,v[116:119],50
R 42,1,45056
M 14,v[124:127],54
L 3
D 0,56,3072
M 14,v[132:135],26
s_add_u32 m0,s57,32768
s_add_u32 s56,s56,4096
R 46,1,46080
M 14,v[140:143],30
s_sub_u32 s57,s84,s57
M 10,v[128:131],26
R 50,1,47104
M 10,v[136:139],30
L 3
M 10,v[144:147],34
R 54,1,48128
M 10,v[152:155],38
M 14,v[148:151],34
R 26,1,49152
M 14,v[156:159],38
L 3
D 0,56,0
M 14,v[164:167],42
R 30,1,50176
M 14,v[172:175],46
M 10,v[160:163],42
R 34,1,51200
M 10,v[168:171],46
L 3
M 10,v[176:179],50
R 38,1,52224
M 10,v[184:187],54
M 14,v[180:183],50
R 42,1,53248
M 14,v[188:191],54
L 3
D 0,56,1024
M 14,v[196:199],26
R 46,1,54272
M 14,v[204:207],30
M 10,v[192:195],26
R 50,1,55296
M 10,v[200:203],30
L 3
M 10,v[208:211],34
s_waitcnt vmcnt(6)
R 54,1,56320
M 10,v[216:219],38
s_barrier
ds_read_b32 v7,v6 offset:0
M 14,v[212:215],34
R 26,2,0
M 14,v[220:223],38
L 4
buffer_store_dword v4,v3,s[36:39],s69 offen offset:0 sc0 sc1
M 14,v[228:231],42
s_mov_b32 s69,s68
s_lshr_b32 s68,s62,4
R 30,2,1024
M 14,v[236:239],46
D 0,56,2048
M 10,v[224:227],42
s_add_u32 s68,s68,s48
R 34,2,2048
M 10,v[232:235],46
L 4
L 3
M 10,v[240:243],50
R 38,2,3072
v_cmp_eq_u32 vcc,s53,v7
M 10,v[248:251],54
M 14,v[244:247],50
L 2
s_cmp_eq_u32 vcc_lo,-1
R 42,2,4096
M 14,v[252:255],54
s_cbranch_scc0 Lg4_L1_%=
Lg4_F1_%=:
v_mfma_f32_16x16x32_bf16 v[22:25],a[4:7],v[26:29],0
s_add_u32 m0,s57,0
R 46,2,5120
v_lshlrev_b32 v62,16,v58
M 22,a[12:15],30
s_add_u32 s56,s55,0
v_add_f32 v10,v10,v62
v_and_b32 v63,0xffff0000,v58
v_mfma_f32_16x16x32_bf16 v[18:21],a[0:3],v[26:29],0
s_add_u32 s61,s54,s46
R 50,2,6144
v_add_f32 v11,v11,v63
M 18,a[8:11],30
L 3
D 0,56,0
M 18,a[16:19],34
s_cmp_ge_u32 s61,s63
R 54,2,7168
v_lshlrev_b32 v62,16,v59
M 18,a[24:27],38
s_cselect_b32 s61,s93,s61
v_add_f32 v12,v12,v62
v_and_b32 v63,0xffff0000,v59
M 22,a[20:23],34
s_cselect_b32 s46,7,s46
R 26,2,8192
v_add_f32 v13,v13,v63
M 22,a[28:31],38
L 3
v_lshlrev_b32 v62,16,v60
v_add_f32 v14,v14,v62
M 22,a[36:39],42
s_cselect_b32 s63,-1,s63
R 30,2,9216
v_and_b32 v63,0xffff0000,v60
M 22,a[44:47],46
D 0,56,1024
M 18,a[32:35],42
s_mul_i32 s58,s61,2560
R 34,2,10240
v_add_f32 v15,v15,v63
M 18,a[40:43],46
L 3
v_lshlrev_b32 v62,16,v61
v_add_f32 v16,v16,v62
M 18,a[48:51],50
s_add_u32 s58,s58,109891584
R 38,2,11264
v_and_b32 v63,0xffff0000,v61
M 18,a[56:59],54
buffer_load_dwordx4 v[58:61],v0,s[36:39],s74 offen offset:0 sc1
M 22,a[52:55],50
s_lshl_b32 s62,s54,15
R 42,2,12288
v_add_f32 v17,v17,v63
M 22,a[60:63],54
L 3
v_cvt_pk_bf16_f32 v10,v10,v11
v_cvt_pk_bf16_f32 v11,v12,v13
M 22,a[68:71],26
s_mov_b32 s67,s65
R 46,2,13312
v_cvt_pk_bf16_f32 v12,v14,v15
M 22,a[76:79],30
D 0,56,2048
M 18,a[64:67],26
s_add_u32 s74,s49,s62
R 50,2,14336
v_cvt_pk_bf16_f32 v13,v16,v17
M 18,a[72:75],30
L 3
buffer_store_dwordx4 v[10:13],v0,s[36:39],s66 offen offset:0 sc0 sc1
M 18,a[80:83],34
s_add_u32 s65,s47,s62
s_mov_b32 s54,s61
R 54,2,15360
M 18,a[88:91],38
M 22,a[84:87],34
R 26,2,16384
M 22,a[92:95],38
L 3
M 22,a[100:103],42
R 30,2,17408
M 22,a[108:111],46
D 0,56,3072
M 18,a[96:99],42
s_add_u32 m0,s57,4096
s_add_u32 s56,s56,4096
R 34,2,18432
M 18,a[104:107],46
L 3
M 18,a[112:115],50
R 38,2,19456
M 18,a[120:123],54
M 22,a[116:119],50
R 42,2,20480
M 22,a[124:127],54
L 3
D 0,56,0
M 22,a[132:135],26
R 46,2,21504
M 22,a[140:143],30
M 18,a[128:131],26
R 50,2,22528
M 18,a[136:139],30
L 3
M 18,a[144:147],34
R 54,2,23552
M 18,a[152:155],38
M 22,a[148:151],34
R 26,2,24576
M 22,a[156:159],38
L 3
D 0,56,1024
M 22,a[164:167],42
R 30,2,25600
M 22,a[172:175],46
M 18,a[160:163],42
R 34,2,26624
M 18,a[168:171],46
L 3
M 18,a[176:179],50
s_waitcnt vmcnt(8)
R 38,2,27648
M 18,a[184:187],54
s_barrier
M 22,a[180:183],50
R 42,2,28672
M 22,a[188:191],54
L 3
D 0,56,2048
M 22,a[196:199],26
s_add_u32 m0,s59,1024
s_add_u32 s56,s55,28672
R 46,2,29696
M 22,a[204:207],30
s_mul_i32 s55,s61,114688
s_add_u32 s55,s55,s60
M 18,a[192:195],26
R 50,2,30720
M 18,a[200:203],30
L 3
M 18,a[208:211],34
R 54,2,31744
M 18,a[216:219],38
M 22,a[212:215],34
R 26,2,32768
M 22,a[220:223],38
L 3
buffer_load_dword v8,s[36:39],s58 offen offset:0 sc1 lds
M 22,a[228:231],42
s_add_u32 m0,s57,28672
R 30,2,33792
M 22,a[236:239],46
D 0,56,0
M 18,a[224:227],42
R 34,2,34816
M 18,a[232:235],46
L 3
M 18,a[240:243],50
R 38,2,35840
M 18,a[248:251],54
M 22,a[244:247],50
R 42,2,36864
M 22,a[252:255],54
L 3
M 22,v[68:71],26
R 46,2,37888
M 22,v[76:79],30
D 0,56,1024
M 18,v[64:67],26
R 50,2,38912
M 18,v[72:75],30
L 3
M 18,v[80:83],34
R 54,2,39936
M 18,v[88:91],38
M 22,v[84:87],34
R 26,2,40960
M 22,v[92:95],38
L 3
M 22,v[100:103],42
R 30,2,41984
M 22,v[108:111],46
D 0,56,2048
M 18,v[96:99],42
R 34,2,43008
M 18,v[104:107],46
L 3
M 18,v[112:115],50
R 38,2,44032
M 18,v[120:123],54
M 22,v[116:119],50
R 42,2,45056
M 22,v[124:127],54
L 3
D 0,56,3072
M 22,v[132:135],26
s_add_u32 m0,s57,32768
s_add_u32 s56,s56,4096
R 46,2,46080
M 22,v[140:143],30
s_sub_u32 s57,s84,s57
M 18,v[128:131],26
R 50,2,47104
M 18,v[136:139],30
L 3
M 18,v[144:147],34
R 54,2,48128
M 18,v[152:155],38
M 22,v[148:151],34
R 26,2,49152
M 22,v[156:159],38
L 3
D 0,56,0
M 22,v[164:167],42
R 30,2,50176
M 22,v[172:175],46
M 18,v[160:163],42
R 34,2,51200
M 18,v[168:171],46
L 3
M 18,v[176:179],50
R 38,2,52224
M 18,v[184:187],54
M 22,v[180:183],50
R 42,2,53248
M 22,v[188:191],54
L 3
D 0,56,1024
M 22,v[196:199],26
R 46,2,54272
M 22,v[204:207],30
M 18,v[192:195],26
R 50,2,55296
M 18,v[200:203],30
L 3
M 18,v[208:211],34
s_waitcnt vmcnt(6)
R 54,2,56320
M 18,v[216:219],38
s_barrier
ds_read_b32 v7,v6 offset:1024
M 22,v[212:215],34
R 26,1,0
M 22,v[220:223],38
L 4
buffer_store_dword v4,v3,s[36:39],s69 offen offset:0 sc0 sc1
M 22,v[228:231],42
s_mov_b32 s69,s68
s_lshr_b32 s68,s62,4
R 30,1,1024
M 22,v[236:239],46
D 0,56,2048
M 18,v[224:227],42
s_add_u32 s68,s68,s48
R 34,1,2048
M 18,v[232:235],46
L 4
L 3
M 18,v[240:243],50
R 38,1,3072
v_cmp_eq_u32 vcc,s53,v7
M 18,v[248:251],54
M 22,v[244:247],50
L 2
s_cmp_eq_u32 vcc_lo,-1
R 42,1,4096
)"
R"(M 22,v[252:255],54
s_cbranch_scc1 Lg4_F0_%=
Lg4_L0_%=:
s_cmp_ge_u32 s54,256
s_cbranch_scc1 Lg4_Z0_%=
v_mfma_f32_16x16x32_bf16 v[14:17],a[4:7],v[26:29],0
s_add_u32 m0,s59,3072
R 46,1,5120
v_lshlrev_b32 v62,16,v58
M 14,a[12:15],30
buffer_load_dword v8,s[36:39],s58 offen offset:0 sc1 lds
v_mfma_f32_16x16x32_bf16 v[10:13],a[0:3],v[26:29],0
s_lshl_b32 s62,s54,15
R 50,1,6144
v_add_f32 v18,v18,v62
M 10,a[8:11],30
L 3
v_and_b32 v63,0xffff0000,v58
v_add_f32 v19,v19,v63
M 10,a[16:19],34
s_mov_b32 s66,s65
R 54,1,7168
v_lshlrev_b32 v62,16,v59
M 10,a[24:27],38
s_add_u32 s65,s47,s62
v_add_f32 v20,v20,v62
v_and_b32 v63,0xffff0000,v59
M 14,a[20:23],34
R 26,1,8192
v_add_f32 v21,v21,v63
M 14,a[28:31],38
L 3
v_lshlrev_b32 v62,16,v60
v_add_f32 v22,v22,v62
M 14,a[36:39],42
R 30,1,9216
v_and_b32 v63,0xffff0000,v60
M 14,a[44:47],46
v_add_f32 v23,v23,v63
v_lshlrev_b32 v62,16,v61
M 10,a[32:35],42
R 34,1,10240
v_add_f32 v24,v24,v62
M 10,a[40:43],46
L 3
v_and_b32 v63,0xffff0000,v61
v_add_f32 v25,v25,v63
M 10,a[48:51],50
R 38,1,11264
v_cvt_pk_bf16_f32 v18,v18,v19
M 10,a[56:59],54
buffer_load_dwordx4 v[58:61],v0,s[36:39],s74 offen offset:0 sc1
M 14,a[52:55],50
s_add_u32 s74,s49,s62
R 42,1,12288
v_cvt_pk_bf16_f32 v19,v20,v21
M 14,a[60:63],54
L 3
v_cvt_pk_bf16_f32 v20,v22,v23
v_cvt_pk_bf16_f32 v21,v24,v25
M 14,a[68:71],26
R 46,1,13312
M 14,a[76:79],30
buffer_store_dwordx4 v[18:21],v0,s[36:39],s67 offen offset:0 sc0 sc1
M 10,a[64:67],26
R 50,1,14336
M 10,a[72:75],30
L 3
M 10,a[80:83],34
R 54,1,15360
M 10,a[88:91],38
M 14,a[84:87],34
R 26,1,16384
M 14,a[92:95],38
L 3
M 14,a[100:103],42
R 30,1,17408
M 14,a[108:111],46
M 10,a[96:99],42
R 34,1,18432
M 10,a[104:107],46
L 3
M 10,a[112:115],50
R 38,1,19456
M 10,a[120:123],54
M 14,a[116:119],50
R 42,1,20480
M 14,a[124:127],54
L 3
M 14,a[132:135],26
R 46,1,21504
M 14,a[140:143],30
M 10,a[128:131],26
R 50,1,22528
M 10,a[136:139],30
L 3
M 10,a[144:147],34
R 54,1,23552
M 10,a[152:155],38
M 14,a[148:151],34
R 26,1,24576
M 14,a[156:159],38
L 3
M 14,a[164:167],42
R 30,1,25600
M 14,a[172:175],46
M 10,a[160:163],42
R 34,1,26624
M 10,a[168:171],46
L 3
M 10,a[176:179],50
s_waitcnt vmcnt(2)
R 38,1,27648
M 10,a[184:187],54
s_barrier
ds_read_b32 v7,v6 offset:3072
M 14,a[180:183],50
R 42,1,28672
M 14,a[188:191],54
L 4
M 14,a[196:199],26
R 46,1,29696
M 14,a[204:207],30
M 10,a[192:195],26
R 50,1,30720
M 10,a[200:203],30
L 4
L 3
v_cmp_eq_u32 vcc,s53,v7
M 10,a[208:211],34
R 54,1,31744
M 10,a[216:219],38
M 14,a[212:215],34
s_cmp_eq_u32 vcc_lo,-1
R 26,1,32768
s_cbranch_scc0 Lg4_X0_%=
M 14,a[220:223],38
L 3
s_add_u32 m0,s57,0
s_add_u32 s56,s55,0
M 14,a[228:231],42
s_add_u32 s61,s54,s46
s_cmp_ge_u32 s61,s63
s_cselect_b32 s61,s93,s61
R 30,1,33792
M 14,a[236:239],46
s_mul_i32 s58,s61,2560
D 0,56,0
M 10,a[224:227],42
s_cselect_b32 s46,7,s46
s_cselect_b32 s63,-1,s63
R 34,1,34816
M 10,a[232:235],46
L 3
s_add_u32 s58,s58,109891584
s_mov_b32 s54,s61
M 10,a[240:243],50
R 38,1,35840
M 10,a[248:251],54
D 0,56,1024
M 14,a[244:247],50
R 42,1,36864
M 14,a[252:255],54
L 3
M 14,v[68:71],26
R 46,1,37888
M 14,v[76:79],30
D 0,56,2048
M 10,v[64:67],26
R 50,1,38912
M 10,v[72:75],30
L 3
M 10,v[80:83],34
R 54,1,39936
M 10,v[88:91],38
D 0,56,3072
M 14,v[84:87],34
s_add_u32 m0,s57,4096
s_add_u32 s56,s56,4096
R 26,1,40960
M 14,v[92:95],38
L 3
M 14,v[100:103],42
R 30,1,41984
M 14,v[108:111],46
D 0,56,0
M 10,v[96:99],42
R 34,1,43008
M 10,v[104:107],46
L 3
M 10,v[112:115],50
R 38,1,44032
M 10,v[120:123],54
D 0,56,1024
M 14,v[116:119],50
R 42,1,45056
M 14,v[124:127],54
L 3
M 14,v[132:135],26
R 46,1,46080
M 14,v[140:143],30
D 0,56,2048
M 10,v[128:131],26
s_add_u32 m0,s59,0
s_add_u32 s56,s55,28672
R 50,1,47104
M 10,v[136:139],30
L 3
buffer_load_dword v8,s[36:39],s58 offen offset:0 sc1 lds
M 10,v[144:147],34
s_add_u32 m0,s57,28672
s_mul_i32 s55,s61,114688
R 54,1,48128
M 10,v[152:155],38
s_add_u32 s55,s55,s60
M 14,v[148:151],34
R 26,1,49152
M 14,v[156:159],38
L 3
D 0,56,0
M 14,v[164:167],42
R 30,1,50176
M 14,v[172:175],46
M 10,v[160:163],42
R 34,1,51200
M 10,v[168:171],46
L 3
D 0,56,1024
M 10,v[176:179],50
R 38,1,52224
M 10,v[184:187],54
D 0,56,2048
M 14,v[180:183],50
R 42,1,53248
M 14,v[188:191],54
L 3
M 14,v[196:199],26
R 46,1,54272
M 14,v[204:207],30
D 0,56,3072
M 10,v[192:195],26
s_add_u32 m0,s57,32768
s_add_u32 s56,s56,4096
R 50,1,55296
M 10,v[200:203],30
L 3
s_sub_u32 s57,s84,s57
M 10,v[208:211],34
s_waitcnt vmcnt(4)
R 54,1,56320
M 10,v[216:219],38
s_barrier
ds_read_b32 v7,v6 offset:0
M 14,v[212:215],34
R 26,2,0
M 14,v[220:223],38
L 4
buffer_store_dword v4,v3,s[36:39],s69 offen offset:0 sc0 sc1
M 14,v[228:231],42
s_mov_b32 s69,s68
s_lshr_b32 s68,s62,4
R 30,2,1024
M 14,v[236:239],46
D 0,56,0
M 10,v[224:227],42
s_add_u32 s68,s68,s48
R 34,2,2048
M 10,v[232:235],46
L 4
D 0,56,1024
M 10,v[240:243],50
L 3
R 38,2,3072
v_cmp_eq_u32 vcc,s53,v7
M 10,v[248:251],54
D 0,56,2048
M 14,v[244:247],50
L 2
s_cmp_eq_u32 vcc_lo,-1
R 42,2,4096
M 14,v[252:255],54
s_cbranch_scc1 Lg4_F1_%=
s_branch Lg4_L1_%=
Lg4_X0_%=:
M 14,a[220:223],38
L 3
s_add_u32 m0,s59,0
M 14,a[228:231],42
R 30,1,33792
M 14,a[236:239],46
buffer_load_dword v8,s[36:39],s58 offen offset:0 sc1 lds
M 10,a[224:227],42
R 34,1,34816
M 10,a[232:235],46
L 3
M 10,a[240:243],50
R 38,1,35840
M 10,a[248:251],54
M 14,a[244:247],50
R 42,1,36864
M 14,a[252:255],54
L 3
M 14,v[68:71],26
R 46,1,37888
M 14,v[76:79],30
M 10,v[64:67],26
R 50,1,38912
M 10,v[72:75],30
L 3
M 10,v[80:83],34
R 54,1,39936
M 10,v[88:91],38
M 14,v[84:87],34
R 26,1,40960
M 14,v[92:95],38
L 3
M 14,v[100:103],42
R 30,1,41984
M 14,v[108:111],46
M 10,v[96:99],42
R 34,1,43008
M 10,v[104:107],46
L 3
M 10,v[112:115],50
R 38,1,44032
M 10,v[120:123],54
M 14,v[116:119],50
R 42,1,45056
M 14,v[124:127],54
L 3
M 14,v[132:135],26
R 46,1,46080
M 14,v[140:143],30
M 10,v[128:131],26
R 50,1,47104
M 10,v[136:139],30
L 3
M 10,v[144:147],34
R 54,1,48128
M 10,v[152:155],38
M 14,v[148:151],34
R 26,1,49152
M 14,v[156:159],38
L 3
M 14,v[164:167],42
R 30,1,50176
M 14,v[172:175],46
M 10,v[160:163],42
R 34,1,51200
M 10,v[168:171],46
L 3
M 10,v[176:179],50
R 38,1,52224
M 10,v[184:187],54
M 14,v[180:183],50
R 42,1,53248
M 14,v[188:191],54
L 3
M 14,v[196:199],26
R 46,1,54272
M 14,v[204:207],30
M 10,v[192:195],26
R 50,1,55296
M 10,v[200:203],30
L 3
M 10,v[208:211],34
s_waitcnt vmcnt(0)
R 54,1,56320
M 10,v[216:219],38
s_barrier
buffer_store_dword v4,v3,s[36:39],s69 offen offset:0 sc0 sc1
M 14,v[212:215],34
s_mov_b32 s69,s68
s_lshr_b32 s68,s62,4
ds_read_b32 v7,v6 offset:0
M 14,v[220:223],38
L 3
s_add_u32 s68,s68,s48
M 14,v[228:231],42
M 14,v[236:239],46
M 10,v[224:227],42
M 10,v[232:235],46
L 1
M 10,v[240:243],50
L 0
v_cmp_eq_u32 vcc,s53,v7
M 10,v[248:251],54
M 14,v[244:247],50
M 14,v[252:255],54
s_cmp_lt_u32 s54,249
s_cbranch_scc1 Lg4_xa0_%=
s_sub_u32 s75,s66,s85
v_lshlrev_b32 v62,16,v58
s_nop 1
v_add_f32 v10,v10,v62
v_and_b32 v63,0xffff0000,v58
v_add_f32 v11,v11,v63
v_lshlrev_b32 v62,16,v59
v_add_f32 v12,v12,v62
v_and_b32 v63,0xffff0000,v59
v_add_f32 v13,v13,v63
v_lshlrev_b32 v62,16,v60
v_add_f32 v14,v14,v62
v_and_b32 v63,0xffff0000,v60
v_add_f32 v15,v15,v63
v_lshlrev_b32 v62,16,v61
v_add_f32 v16,v16,v62
v_and_b32 v63,0xffff0000,v61
v_add_f32 v17,v17,v63
v_cvt_pk_bf16_f32 v10,v10,v11
v_cvt_pk_bf16_f32 v11,v12,v13
v_cvt_pk_bf16_f32 v12,v14,v15
v_cvt_pk_bf16_f32 v13,v16,v17
v_lshrrev_b32 v62,8,v0
v_lshl_add_u32 v62,v62,5,v0
v_add_u32 v62,s73,v62
ds_write_b128 v62,v[10:13] offset:0
ds_read2_b32 v[14:15],v5 offset0:0 offset1:16
ds_read2_b32 v[16:17],v5 offset0:32 offset1:48
s_mov_b64 s[40:41],s[86:87]
s_mov_b32 s42,4194304
s_mov_b32 s43,0x00020000
L 0
s_add_u32 s64,s75,0
buffer_atomic_pk_add_bf16 v14,v9,s[40:43],s64 offen sc1
s_add_u32 s64,s75,8192
buffer_atomic_pk_add_bf16 v15,v9,s[40:43],s64 offen sc1
s_add_u32 s64,s75,16384
buffer_atomic_pk_add_bf16 v16,v9,s[40:43],s64 offen sc1
s_add_u32 s64,s75,24576
buffer_atomic_pk_add_bf16 v17,v9,s[40:43],s64 offen sc1
s_mov_b32 s66,0x7fff0000
s_mov_b32 s69,0x7fff0000
Lg4_xa0_%=:
s_mov_b32 s80,1
s_cmp_eq_u32 vcc_lo,-1
s_cbranch_scc0 Lg4_xn0_%=
s_add_u32 m0,s57,0
s_add_u32 s56,s55,0
D 0,56,0
v_lshlrev_b32 v62,16,v58
D 0,56,1024
v_add_f32 v10,v10,v62
D 0,56,2048
v_and_b32 v63,0xffff0000,v58
D 0,56,3072
v_add_f32 v11,v11,v63
s_add_u32 m0,s57,4096
s_add_u32 s56,s56,4096
D 0,56,0
v_lshlrev_b32 v62,16,v59
D 0,56,1024
v_add_f32 v12,v12,v62
D 0,56,2048
v_and_b32 v63,0xffff0000,v59
s_add_u32 s61,s54,s46
s_cmp_ge_u32 s61,s63
s_cselect_b32 s61,s93,s61
s_cselect_b32 s46,7,s46
s_cselect_b32 s63,-1,s63
s_mul_i32 s58,s61,2560
s_add_u32 s58,s58,109891584
s_add_u32 m0,s57,28672
s_add_u32 s56,s55,28672
D 0,56,0
v_add_f32 v13,v13,v63
D 0,56,1024
v_lshlrev_b32 v62,16,v60
D 0,56,2048
v_add_f32 v14,v14,v62
D 0,56,3072
v_and_b32 v63,0xffff0000,v60
s_add_u32 m0,s57,32768
s_add_u32 s56,s56,4096
D 0,56,0
v_add_f32 v15,v15,v63
D 0,56,1024
v_lshlrev_b32 v62,16,v61
D 0,56,2048
v_add_f32 v16,v16,v62
s_mul_i32 s55,s61,114688
s_add_u32 s55,s55,s60
s_sub_u32 s57,s84,s57
s_mov_b32 s54,s61
v_and_b32 v63,0xffff0000,v61
v_add_f32 v17,v17,v63
v_cvt_pk_bf16_f32 v10,v10,v11
v_cvt_pk_bf16_f32 v11,v12,v13
v_cvt_pk_bf16_f32 v12,v14,v15
v_cvt_pk_bf16_f32 v13,v16,v17
buffer_store_dwordx4 v[10:13],v0,s[36:39],s66 offen offset:0 sc0 sc1
s_mov_b32 s72,s69
s_mov_b32 s69,0x7fff0000
s_mov_b32 s66,0x7fff0000
s_mov_b32 s76,0x7fff0000
s_branch Lg4_gox_%=
Lg4_xn0_%=:
s_cmp_ge_u32 s54,256
s_cbranch_scc1 Lg4_fin0_%=
s_add_u32 m0,s59,4096
)"
R"(s_nop 0
buffer_load_dword v8,s[36:39],s58 offen offset:0 sc1 lds
buffer_store_dword v4,v3,s[36:39],s71 offen offset:0 sc0 sc1
s_add_u32 m0,s59,5120
s_nop 0
buffer_load_dword v8,s[36:39],s58 offen offset:0 sc1 lds
buffer_store_dword v4,v3,s[36:39],s71 offen offset:0 sc0 sc1
s_mov_b32 s83,0
v_lshlrev_b32 v62,16,v58
v_add_f32 v10,v10,v62
v_and_b32 v63,0xffff0000,v58
v_add_f32 v11,v11,v63
v_lshlrev_b32 v62,16,v59
v_add_f32 v12,v12,v62
v_and_b32 v63,0xffff0000,v59
v_add_f32 v13,v13,v63
v_lshlrev_b32 v62,16,v60
v_add_f32 v14,v14,v62
v_and_b32 v63,0xffff0000,v60
v_add_f32 v15,v15,v63
v_lshlrev_b32 v62,16,v61
v_add_f32 v16,v16,v62
v_and_b32 v63,0xffff0000,v61
v_add_f32 v17,v17,v63
v_cvt_pk_bf16_f32 v10,v10,v11
v_cvt_pk_bf16_f32 v11,v12,v13
v_cvt_pk_bf16_f32 v12,v14,v15
v_cvt_pk_bf16_f32 v13,v16,v17
buffer_store_dwordx4 v[10:13],v0,s[36:39],s66 offen offset:0 sc0 sc1
s_mov_b32 s72,s69
s_mov_b32 s69,0x7fff0000
s_mov_b32 s66,0x7fff0000
s_mov_b32 s76,0x7fff0000
s_branch Lg4_poll_%=
Lg4_fin0_%=:
v_lshlrev_b32 v62,16,v58
v_add_f32 v10,v10,v62
v_and_b32 v63,0xffff0000,v58
v_add_f32 v11,v11,v63
v_lshlrev_b32 v62,16,v59
v_add_f32 v12,v12,v62
v_and_b32 v63,0xffff0000,v59
v_add_f32 v13,v13,v63
v_lshlrev_b32 v62,16,v60
v_add_f32 v14,v14,v62
v_and_b32 v63,0xffff0000,v60
v_add_f32 v15,v15,v63
v_lshlrev_b32 v62,16,v61
v_add_f32 v16,v16,v62
v_and_b32 v63,0xffff0000,v61
v_add_f32 v17,v17,v63
v_cvt_pk_bf16_f32 v10,v10,v11
v_cvt_pk_bf16_f32 v11,v12,v13
v_cvt_pk_bf16_f32 v12,v14,v15
v_cvt_pk_bf16_f32 v13,v16,v17
buffer_store_dwordx4 v[10:13],v0,s[36:39],s66 offen offset:0 sc0 sc1
s_mov_b32 s72,s69
s_mov_b32 s69,0x7fff0000
s_mov_b32 s66,0x7fff0000
s_mov_b32 s76,0x7fff0000
s_branch Lg4_done_%=
Lg4_L1_%=:
s_cmp_ge_u32 s54,256
s_cbranch_scc1 Lg4_Z1_%=
v_mfma_f32_16x16x32_bf16 v[22:25],a[4:7],v[26:29],0
s_add_u32 m0,s59,3072
R 46,2,5120
v_lshlrev_b32 v62,16,v58
M 22,a[12:15],30
buffer_load_dword v8,s[36:39],s58 offen offset:0 sc1 lds
v_mfma_f32_16x16x32_bf16 v[18:21],a[0:3],v[26:29],0
s_lshl_b32 s62,s54,15
R 50,2,6144
v_add_f32 v10,v10,v62
M 18,a[8:11],30
L 3
v_and_b32 v63,0xffff0000,v58
v_add_f32 v11,v11,v63
M 18,a[16:19],34
s_mov_b32 s67,s65
R 54,2,7168
v_lshlrev_b32 v62,16,v59
M 18,a[24:27],38
s_add_u32 s65,s47,s62
v_add_f32 v12,v12,v62
v_and_b32 v63,0xffff0000,v59
M 22,a[20:23],34
R 26,2,8192
v_add_f32 v13,v13,v63
M 22,a[28:31],38
L 3
v_lshlrev_b32 v62,16,v60
v_add_f32 v14,v14,v62
M 22,a[36:39],42
R 30,2,9216
v_and_b32 v63,0xffff0000,v60
M 22,a[44:47],46
v_add_f32 v15,v15,v63
v_lshlrev_b32 v62,16,v61
M 18,a[32:35],42
R 34,2,10240
v_add_f32 v16,v16,v62
M 18,a[40:43],46
L 3
v_and_b32 v63,0xffff0000,v61
v_add_f32 v17,v17,v63
M 18,a[48:51],50
R 38,2,11264
v_cvt_pk_bf16_f32 v10,v10,v11
M 18,a[56:59],54
buffer_load_dwordx4 v[58:61],v0,s[36:39],s74 offen offset:0 sc1
M 22,a[52:55],50
s_add_u32 s74,s49,s62
R 42,2,12288
v_cvt_pk_bf16_f32 v11,v12,v13
M 22,a[60:63],54
L 3
v_cvt_pk_bf16_f32 v12,v14,v15
v_cvt_pk_bf16_f32 v13,v16,v17
M 22,a[68:71],26
R 46,2,13312
M 22,a[76:79],30
buffer_store_dwordx4 v[10:13],v0,s[36:39],s66 offen offset:0 sc0 sc1
M 18,a[64:67],26
R 50,2,14336
M 18,a[72:75],30
L 3
M 18,a[80:83],34
R 54,2,15360
M 18,a[88:91],38
M 22,a[84:87],34
R 26,2,16384
M 22,a[92:95],38
L 3
M 22,a[100:103],42
R 30,2,17408
M 22,a[108:111],46
M 18,a[96:99],42
R 34,2,18432
M 18,a[104:107],46
L 3
M 18,a[112:115],50
R 38,2,19456
M 18,a[120:123],54
M 22,a[116:119],50
R 42,2,20480
M 22,a[124:127],54
L 3
M 22,a[132:135],26
R 46,2,21504
M 22,a[140:143],30
M 18,a[128:131],26
R 50,2,22528
M 18,a[136:139],30
L 3
M 18,a[144:147],34
R 54,2,23552
M 18,a[152:155],38
M 22,a[148:151],34
R 26,2,24576
M 22,a[156:159],38
L 3
M 22,a[164:167],42
R 30,2,25600
M 22,a[172:175],46
M 18,a[160:163],42
R 34,2,26624
M 18,a[168:171],46
L 3
M 18,a[176:179],50
s_waitcnt vmcnt(2)
R 38,2,27648
M 18,a[184:187],54
s_barrier
ds_read_b32 v7,v6 offset:3072
M 22,a[180:183],50
R 42,2,28672
M 22,a[188:191],54
L 4
M 22,a[196:199],26
R 46,2,29696
M 22,a[204:207],30
M 18,a[192:195],26
R 50,2,30720
M 18,a[200:203],30
L 4
L 3
v_cmp_eq_u32 vcc,s53,v7
M 18,a[208:211],34
R 54,2,31744
M 18,a[216:219],38
M 22,a[212:215],34
s_cmp_eq_u32 vcc_lo,-1
R 26,2,32768
s_cbranch_scc0 Lg4_X1_%=
M 22,a[220:223],38
L 3
s_add_u32 m0,s57,0
s_add_u32 s56,s55,0
M 22,a[228:231],42
s_add_u32 s61,s54,s46
s_cmp_ge_u32 s61,s63
s_cselect_b32 s61,s93,s61
R 30,2,33792
M 22,a[236:239],46
s_mul_i32 s58,s61,2560
D 0,56,0
M 18,a[224:227],42
s_cselect_b32 s46,7,s46
s_cselect_b32 s63,-1,s63
R 34,2,34816
M 18,a[232:235],46
L 3
s_add_u32 s58,s58,109891584
s_mov_b32 s54,s61
M 18,a[240:243],50
R 38,2,35840
M 18,a[248:251],54
D 0,56,1024
M 22,a[244:247],50
R 42,2,36864
M 22,a[252:255],54
L 3
M 22,v[68:71],26
R 46,2,37888
M 22,v[76:79],30
D 0,56,2048
M 18,v[64:67],26
R 50,2,38912
M 18,v[72:75],30
L 3
M 18,v[80:83],34
R 54,2,39936
M 18,v[88:91],38
D 0,56,3072
M 22,v[84:87],34
s_add_u32 m0,s57,4096
s_add_u32 s56,s56,4096
R 26,2,40960
M 22,v[92:95],38
L 3
M 22,v[100:103],42
R 30,2,41984
M 22,v[108:111],46
D 0,56,0
M 18,v[96:99],42
R 34,2,43008
M 18,v[104:107],46
L 3
M 18,v[112:115],50
R 38,2,44032
M 18,v[120:123],54
D 0,56,1024
M 22,v[116:119],50
R 42,2,45056
M 22,v[124:127],54
L 3
M 22,v[132:135],26
R 46,2,46080
M 22,v[140:143],30
D 0,56,2048
M 18,v[128:131],26
s_add_u32 m0,s59,1024
s_add_u32 s56,s55,28672
R 50,2,47104
M 18,v[136:139],30
L 3
buffer_load_dword v8,s[36:39],s58 offen offset:0 sc1 lds
M 18,v[144:147],34
s_add_u32 m0,s57,28672
s_mul_i32 s55,s61,114688
R 54,2,48128
M 18,v[152:155],38
s_add_u32 s55,s55,s60
M 22,v[148:151],34
R 26,2,49152
M 22,v[156:159],38
L 3
D 0,56,0
M 22,v[164:167],42
R 30,2,50176
M 22,v[172:175],46
M 18,v[160:163],42
R 34,2,51200
M 18,v[168:171],46
L 3
D 0,56,1024
M 18,v[176:179],50
R 38,2,52224
M 18,v[184:187],54
D 0,56,2048
M 22,v[180:183],50
R 42,2,53248
M 22,v[188:191],54
L 3
M 22,v[196:199],26
R 46,2,54272
M 22,v[204:207],30
D 0,56,3072
M 18,v[192:195],26
s_add_u32 m0,s57,32768
s_add_u32 s56,s56,4096
R 50,2,55296
M 18,v[200:203],30
L 3
s_sub_u32 s57,s84,s57
M 18,v[208:211],34
s_waitcnt vmcnt(4)
R 54,2,56320
M 18,v[216:219],38
s_barrier
ds_read_b32 v7,v6 offset:1024
M 22,v[212:215],34
R 26,1,0
M 22,v[220:223],38
L 4
buffer_store_dword v4,v3,s[36:39],s69 offen offset:0 sc0 sc1
M 22,v[228:231],42
s_mov_b32 s69,s68
s_lshr_b32 s68,s62,4
R 30,1,1024
M 22,v[236:239],46
D 0,56,0
M 18,v[224:227],42
s_add_u32 s68,s68,s48
R 34,1,2048
M 18,v[232:235],46
L 4
D 0,56,1024
M 18,v[240:243],50
L 3
R 38,1,3072
v_cmp_eq_u32 vcc,s53,v7
M 18,v[248:251],54
D 0,56,2048
M 22,v[244:247],50
L 2
s_cmp_eq_u32 vcc_lo,-1
R 42,1,4096
M 22,v[252:255],54
s_cbranch_scc1 Lg4_F0_%=
s_branch Lg4_L0_%=
Lg4_X1_%=:
M 22,a[220:223],38
L 3
s_add_u32 m0,s59,1024
M 22,a[228:231],42
R 30,2,33792
M 22,a[236:239],46
buffer_load_dword v8,s[36:39],s58 offen offset:0 sc1 lds
M 18,a[224:227],42
R 34,2,34816
M 18,a[232:235],46
L 3
M 18,a[240:243],50
R 38,2,35840
M 18,a[248:251],54
M 22,a[244:247],50
R 42,2,36864
M 22,a[252:255],54
L 3
M 22,v[68:71],26
R 46,2,37888
M 22,v[76:79],30
M 18,v[64:67],26
R 50,2,38912
M 18,v[72:75],30
L 3
M 18,v[80:83],34
R 54,2,39936
M 18,v[88:91],38
M 22,v[84:87],34
R 26,2,40960
M 22,v[92:95],38
L 3
M 22,v[100:103],42
R 30,2,41984
M 22,v[108:111],46
M 18,v[96:99],42
R 34,2,43008
M 18,v[104:107],46
L 3
M 18,v[112:115],50
R 38,2,44032
M 18,v[120:123],54
M 22,v[116:119],50
R 42,2,45056
M 22,v[124:127],54
L 3
M 22,v[132:135],26
R 46,2,46080
M 22,v[140:143],30
M 18,v[128:131],26
R 50,2,47104
M 18,v[136:139],30
L 3
M 18,v[144:147],34
R 54,2,48128
M 18,v[152:155],38
M 22,v[148:151],34
R 26,2,49152
M 22,v[156:159],38
L 3
M 22,v[164:167],42
R 30,2,50176
M 22,v[172:175],46
M 18,v[160:163],42
R 34,2,51200
M 18,v[168:171],46
L 3
M 18,v[176:179],50
R 38,2,52224
M 18,v[184:187],54
M 22,v[180:183],50
R 42,2,53248
M 22,v[188:191],54
L 3
M 22,v[196:199],26
R 46,2,54272
M 22,v[204:207],30
M 18,v[192:195],26
R 50,2,55296
M 18,v[200:203],30
L 3
M 18,v[208:211],34
s_waitcnt vmcnt(0)
R 54,2,56320
M 18,v[216:219],38
s_barrier
buffer_store_dword v4,v3,s[36:39],s69 offen offset:0 sc0 sc1
M 22,v[212:215],34
s_mov_b32 s69,s68
s_lshr_b32 s68,s62,4
ds_read_b32 v7,v6 offset:1024
M 22,v[220:223],38
L 3
s_add_u32 s68,s68,s48
M 22,v[228:231],42
M 22,v[236:239],46
M 18,v[224:227],42
M 18,v[232:235],46
L 1
M 18,v[240:243],50
L 0
v_cmp_eq_u32 vcc,s53,v7
M 18,v[248:251],54
M 22,v[244:247],50
M 22,v[252:255],54
s_cmp_lt_u32 s54,249
s_cbranch_scc1 Lg4_xa1_%=
s_sub_u32 s75,s67,s85
v_lshlrev_b32 v62,16,v58
s_nop 1
v_add_f32 v18,v18,v62
v_and_b32 v63,0xffff0000,v58
v_add_f32 v19,v19,v63
v_lshlrev_b32 v62,16,v59
v_add_f32 v20,v20,v62
v_and_b32 v63,0xffff0000,v59
v_add_f32 v21,v21,v63
v_lshlrev_b32 v62,16,v60
v_add_f32 v22,v22,v62
v_and_b32 v63,0xffff0000,v60
v_add_f32 v23,v23,v63
v_lshlrev_b32 v62,16,v61
v_add_f32 v24,v24,v62
v_and_b32 v63,0xffff0000,v61
v_add_f32 v25,v25,v63
v_cvt_pk_bf16_f32 v18,v18,v19
v_cvt_pk_bf16_f32 v19,v20,v21
v_cvt_pk_bf16_f32 v20,v22,v23
v_cvt_pk_bf16_f32 v21,v24,v25
v_lshrrev_b32 v62,8,v0
v_lshl_add_u32 v62,v62,5,v0
v_add_u32 v62,s73,v62
ds_write_b128 v62,v[18:21] offset:0
ds_read2_b32 v[22:23],v5 offset0:0 offset1:16
ds_read2_b32 v[24:25],v5 offset0:32 offset1:48
s_mov_b64 s[40:41],s[86:87]
s_mov_b32 s42,4194304
s_mov_b32 s43,0x00020000
L 0
s_add_u32 s64,s75,0
buffer_atomic_pk_add_bf16 v22,v9,s[40:43],s64 offen sc1
s_add_u32 s64,s75,8192
buffer_atomic_pk_add_bf16 v23,v9,s[40:43],s64 offen sc1
s_add_u32 s64,s75,16384
buffer_atomic_pk_add_bf16 v24,v9,s[40:43],s64 offen sc1
s_add_u32 s64,s75,24576
buffer_atomic_pk_add_bf16 v25,v9,s[40:43],s64 offen sc1
s_mov_b32 s67,0x7fff0000
s_mov_b32 s69,0x7fff0000
Lg4_xa1_%=:
s_mov_b32 s80,0
s_cmp_eq_u32 vcc_lo,-1
s_cbranch_scc0 Lg4_xn1_%=
s_add_u32 m0,s57,0
s_add_u32 s56,s55,0
D 0,56,0
v_lshlrev_b32 v62,16,v58
)"
R"(D 0,56,1024
v_add_f32 v18,v18,v62
D 0,56,2048
v_and_b32 v63,0xffff0000,v58
D 0,56,3072
v_add_f32 v19,v19,v63
s_add_u32 m0,s57,4096
s_add_u32 s56,s56,4096
D 0,56,0
v_lshlrev_b32 v62,16,v59
D 0,56,1024
v_add_f32 v20,v20,v62
D 0,56,2048
v_and_b32 v63,0xffff0000,v59
s_add_u32 s61,s54,s46
s_cmp_ge_u32 s61,s63
s_cselect_b32 s61,s93,s61
s_cselect_b32 s46,7,s46
s_cselect_b32 s63,-1,s63
s_mul_i32 s58,s61,2560
s_add_u32 s58,s58,109891584
s_add_u32 m0,s57,28672
s_add_u32 s56,s55,28672
D 0,56,0
v_add_f32 v21,v21,v63
D 0,56,1024
v_lshlrev_b32 v62,16,v60
D 0,56,2048
v_add_f32 v22,v22,v62
D 0,56,3072
v_and_b32 v63,0xffff0000,v60
s_add_u32 m0,s57,32768
s_add_u32 s56,s56,4096
D 0,56,0
v_add_f32 v23,v23,v63
D 0,56,1024
v_lshlrev_b32 v62,16,v61
D 0,56,2048
v_add_f32 v24,v24,v62
s_mul_i32 s55,s61,114688
s_add_u32 s55,s55,s60
s_sub_u32 s57,s84,s57
s_mov_b32 s54,s61
v_and_b32 v63,0xffff0000,v61
v_add_f32 v25,v25,v63
v_cvt_pk_bf16_f32 v18,v18,v19
v_cvt_pk_bf16_f32 v19,v20,v21
v_cvt_pk_bf16_f32 v20,v22,v23
v_cvt_pk_bf16_f32 v21,v24,v25
buffer_store_dwordx4 v[18:21],v0,s[36:39],s67 offen offset:0 sc0 sc1
s_mov_b32 s72,s69
s_mov_b32 s69,0x7fff0000
s_mov_b32 s67,0x7fff0000
s_mov_b32 s77,0x7fff0000
s_branch Lg4_gox_%=
Lg4_xn1_%=:
s_cmp_ge_u32 s54,256
s_cbranch_scc1 Lg4_fin1_%=
s_add_u32 m0,s59,4096
s_nop 0
buffer_load_dword v8,s[36:39],s58 offen offset:0 sc1 lds
buffer_store_dword v4,v3,s[36:39],s71 offen offset:0 sc0 sc1
s_add_u32 m0,s59,5120
s_nop 0
buffer_load_dword v8,s[36:39],s58 offen offset:0 sc1 lds
buffer_store_dword v4,v3,s[36:39],s71 offen offset:0 sc0 sc1
s_mov_b32 s83,0
v_lshlrev_b32 v62,16,v58
v_add_f32 v18,v18,v62
v_and_b32 v63,0xffff0000,v58
v_add_f32 v19,v19,v63
v_lshlrev_b32 v62,16,v59
v_add_f32 v20,v20,v62
v_and_b32 v63,0xffff0000,v59
v_add_f32 v21,v21,v63
v_lshlrev_b32 v62,16,v60
v_add_f32 v22,v22,v62
v_and_b32 v63,0xffff0000,v60
v_add_f32 v23,v23,v63
v_lshlrev_b32 v62,16,v61
v_add_f32 v24,v24,v62
v_and_b32 v63,0xffff0000,v61
v_add_f32 v25,v25,v63
v_cvt_pk_bf16_f32 v18,v18,v19
v_cvt_pk_bf16_f32 v19,v20,v21
v_cvt_pk_bf16_f32 v20,v22,v23
v_cvt_pk_bf16_f32 v21,v24,v25
buffer_store_dwordx4 v[18:21],v0,s[36:39],s67 offen offset:0 sc0 sc1
s_mov_b32 s72,s69
s_mov_b32 s69,0x7fff0000
s_mov_b32 s67,0x7fff0000
s_mov_b32 s77,0x7fff0000
s_branch Lg4_poll_%=
Lg4_fin1_%=:
v_lshlrev_b32 v62,16,v58
v_add_f32 v18,v18,v62
v_and_b32 v63,0xffff0000,v58
v_add_f32 v19,v19,v63
v_lshlrev_b32 v62,16,v59
v_add_f32 v20,v20,v62
v_and_b32 v63,0xffff0000,v59
v_add_f32 v21,v21,v63
v_lshlrev_b32 v62,16,v60
v_add_f32 v22,v22,v62
v_and_b32 v63,0xffff0000,v60
v_add_f32 v23,v23,v63
v_lshlrev_b32 v62,16,v61
v_add_f32 v24,v24,v62
v_and_b32 v63,0xffff0000,v61
v_add_f32 v25,v25,v63
v_cvt_pk_bf16_f32 v18,v18,v19
v_cvt_pk_bf16_f32 v19,v20,v21
v_cvt_pk_bf16_f32 v20,v22,v23
v_cvt_pk_bf16_f32 v21,v24,v25
buffer_store_dwordx4 v[18:21],v0,s[36:39],s67 offen offset:0 sc0 sc1
s_mov_b32 s72,s69
s_mov_b32 s69,0x7fff0000
s_mov_b32 s67,0x7fff0000
s_mov_b32 s77,0x7fff0000
s_branch Lg4_done_%=
Lg4_Z0_%=:
v_mfma_f32_16x16x32_bf16 v[14:17],a[4:7],v[26:29],0
s_sub_u32 s75,s67,s85
R 46,1,5120
v_lshlrev_b32 v62,16,v58
M 14,a[12:15],30
s_mov_b64 s[40:41],s[86:87]
v_add_f32 v18,v18,v62
v_and_b32 v63,0xffff0000,v58
v_mfma_f32_16x16x32_bf16 v[10:13],a[0:3],v[26:29],0
s_mov_b32 s42,4194304
R 50,1,6144
v_add_f32 v19,v19,v63
M 10,a[8:11],30
L 3
v_lshlrev_b32 v62,16,v59
v_add_f32 v20,v20,v62
M 10,a[16:19],34
s_mov_b32 s43,0x00020000
R 54,1,7168
v_and_b32 v63,0xffff0000,v59
M 10,a[24:27],38
s_add_u32 s64,s75,0
v_add_f32 v21,v21,v63
v_lshlrev_b32 v62,16,v60
M 14,a[20:23],34
s_mov_b32 s66,s65
R 26,1,8192
v_add_f32 v22,v22,v62
M 14,a[28:31],38
L 3
v_and_b32 v63,0xffff0000,v60
v_add_f32 v23,v23,v63
M 14,a[36:39],42
R 30,1,9216
v_lshlrev_b32 v62,16,v61
M 14,a[44:47],46
v_add_f32 v24,v24,v62
v_and_b32 v63,0xffff0000,v61
M 10,a[32:35],42
R 34,1,10240
v_add_f32 v25,v25,v63
M 10,a[40:43],46
L 3
buffer_load_dwordx4 v[58:61],v0,s[36:39],s74 offen offset:0 sc1
M 10,a[48:51],50
s_add_u32 s74,s49,s62
R 38,1,11264
v_cvt_pk_bf16_f32 v18,v18,v19
M 10,a[56:59],54
v_cvt_pk_bf16_f32 v19,v20,v21
v_cvt_pk_bf16_f32 v20,v22,v23
M 14,a[52:55],50
R 42,1,12288
v_cvt_pk_bf16_f32 v21,v24,v25
M 14,a[60:63],54
L 3
v_lshrrev_b32 v62,8,v0
v_lshl_add_u32 v62,v62,5,v0
M 14,a[68:71],26
R 46,1,13312
v_add_u32 v62,s73,v62
M 14,a[76:79],30
ds_write_b128 v62,v[18:21] offset:0
M 10,a[64:67],26
R 50,1,14336
ds_read2_b32 v[22:23],v5 offset0:0 offset1:16
M 10,a[72:75],30
L 5
ds_read2_b32 v[24:25],v5 offset0:32 offset1:48
M 10,a[80:83],34
R 54,1,15360
M 10,a[88:91],38
M 14,a[84:87],34
R 26,1,16384
M 14,a[92:95],38
L 6
M 14,a[100:103],42
R 30,1,17408
M 14,a[108:111],46
L 3
buffer_atomic_pk_add_bf16 v22,v9,s[40:43],s64 offen sc1
M 10,a[96:99],42
s_add_u32 s64,s75,8192
R 34,1,18432
M 10,a[104:107],46
L 3
buffer_atomic_pk_add_bf16 v23,v9,s[40:43],s64 offen sc1
M 10,a[112:115],50
s_add_u32 s64,s75,16384
R 38,1,19456
M 10,a[120:123],54
buffer_atomic_pk_add_bf16 v24,v9,s[40:43],s64 offen sc1
M 14,a[116:119],50
s_add_u32 s64,s75,24576
R 42,1,20480
M 14,a[124:127],54
L 3
buffer_atomic_pk_add_bf16 v25,v9,s[40:43],s64 offen sc1
M 14,a[132:135],26
R 46,1,21504
M 14,a[140:143],30
M 10,a[128:131],26
R 50,1,22528
M 10,a[136:139],30
L 3
M 10,a[144:147],34
R 54,1,23552
M 10,a[152:155],38
M 14,a[148:151],34
R 26,1,24576
M 14,a[156:159],38
L 3
M 14,a[164:167],42
R 30,1,25600
M 14,a[172:175],46
M 10,a[160:163],42
R 34,1,26624
M 10,a[168:171],46
L 3
M 10,a[176:179],50
s_waitcnt vmcnt(5)
R 38,1,27648
M 10,a[184:187],54
s_barrier
M 14,a[180:183],50
R 42,1,28672
M 14,a[188:191],54
L 3
M 14,a[196:199],26
R 46,1,29696
M 14,a[204:207],30
M 10,a[192:195],26
R 50,1,30720
M 10,a[200:203],30
L 3
M 10,a[208:211],34
R 54,1,31744
M 10,a[216:219],38
M 14,a[212:215],34
R 26,1,32768
M 14,a[220:223],38
L 3
M 14,a[228:231],42
R 30,1,33792
M 14,a[236:239],46
M 10,a[224:227],42
R 34,1,34816
M 10,a[232:235],46
L 3
M 10,a[240:243],50
R 38,1,35840
M 10,a[248:251],54
M 14,a[244:247],50
R 42,1,36864
M 14,a[252:255],54
L 3
M 14,v[68:71],26
R 46,1,37888
M 14,v[76:79],30
M 10,v[64:67],26
R 50,1,38912
M 10,v[72:75],30
L 3
M 10,v[80:83],34
R 54,1,39936
M 10,v[88:91],38
M 14,v[84:87],34
R 26,1,40960
M 14,v[92:95],38
L 3
M 14,v[100:103],42
R 30,1,41984
M 14,v[108:111],46
M 10,v[96:99],42
R 34,1,43008
M 10,v[104:107],46
L 3
M 10,v[112:115],50
R 38,1,44032
M 10,v[120:123],54
M 14,v[116:119],50
R 42,1,45056
M 14,v[124:127],54
L 3
M 14,v[132:135],26
R 46,1,46080
M 14,v[140:143],30
M 10,v[128:131],26
R 50,1,47104
M 10,v[136:139],30
L 3
M 10,v[144:147],34
R 54,1,48128
M 10,v[152:155],38
M 14,v[148:151],34
R 26,1,49152
M 14,v[156:159],38
L 3
M 14,v[164:167],42
R 30,1,50176
M 14,v[172:175],46
M 10,v[160:163],42
R 34,1,51200
M 10,v[168:171],46
L 3
M 10,v[176:179],50
R 38,1,52224
M 10,v[184:187],54
M 14,v[180:183],50
R 42,1,53248
M 14,v[188:191],54
L 3
M 14,v[196:199],26
R 46,1,54272
M 14,v[204:207],30
M 10,v[192:195],26
R 50,1,55296
M 10,v[200:203],30
L 3
M 10,v[208:211],34
R 54,1,56320
M 10,v[216:219],38
M 14,v[212:215],34
M 14,v[220:223],38
L 2
M 14,v[228:231],42
M 14,v[236:239],46
M 10,v[224:227],42
M 10,v[232:235],46
L 0
M 10,v[240:243],50
M 10,v[248:251],54
M 14,v[244:247],50
M 14,v[252:255],54
s_waitcnt vmcnt(4)
s_sub_u32 s75,s66,s85
v_lshlrev_b32 v62,16,v58
s_nop 2
v_add_f32 v10,v10,v62
v_and_b32 v63,0xffff0000,v58
v_add_f32 v11,v11,v63
v_lshlrev_b32 v62,16,v59
v_add_f32 v12,v12,v62
v_and_b32 v63,0xffff0000,v59
v_add_f32 v13,v13,v63
v_lshlrev_b32 v62,16,v60
v_add_f32 v14,v14,v62
v_and_b32 v63,0xffff0000,v60
v_add_f32 v15,v15,v63
v_lshlrev_b32 v62,16,v61
v_add_f32 v16,v16,v62
v_and_b32 v63,0xffff0000,v61
v_add_f32 v17,v17,v63
v_cvt_pk_bf16_f32 v10,v10,v11
v_cvt_pk_bf16_f32 v11,v12,v13
v_cvt_pk_bf16_f32 v12,v14,v15
v_cvt_pk_bf16_f32 v13,v16,v17
v_lshrrev_b32 v62,8,v0
v_lshl_add_u32 v62,v62,5,v0
v_add_u32 v62,s73,v62
ds_write_b128 v62,v[10:13] offset:0
ds_read2_b32 v[14:15],v5 offset0:0 offset1:16
ds_read2_b32 v[16:17],v5 offset0:32 offset1:48
s_mov_b64 s[40:41],s[86:87]
s_mov_b32 s42,4194304
s_mov_b32 s43,0x00020000
L 0
s_add_u32 s64,s75,0
buffer_atomic_pk_add_bf16 v14,v9,s[40:43],s64 offen sc1
s_add_u32 s64,s75,8192
buffer_atomic_pk_add_bf16 v15,v9,s[40:43],s64 offen sc1
s_add_u32 s64,s75,16384
buffer_atomic_pk_add_bf16 v16,v9,s[40:43],s64 offen sc1
s_add_u32 s64,s75,24576
buffer_atomic_pk_add_bf16 v17,v9,s[40:43],s64 offen sc1
s_branch Lg4_done_%=
Lg4_Z1_%=:
v_mfma_f32_16x16x32_bf16 v[22:25],a[4:7],v[26:29],0
s_sub_u32 s75,s66,s85
R 46,2,5120
v_lshlrev_b32 v62,16,v58
M 22,a[12:15],30
s_mov_b64 s[40:41],s[86:87]
v_add_f32 v10,v10,v62
v_and_b32 v63,0xffff0000,v58
v_mfma_f32_16x16x32_bf16 v[18:21],a[0:3],v[26:29],0
s_mov_b32 s42,4194304
R 50,2,6144
v_add_f32 v11,v11,v63
M 18,a[8:11],30
L 3
v_lshlrev_b32 v62,16,v59
v_add_f32 v12,v12,v62
M 18,a[16:19],34
s_mov_b32 s43,0x00020000
R 54,2,7168
v_and_b32 v63,0xffff0000,v59
M 18,a[24:27],38
s_add_u32 s64,s75,0
v_add_f32 v13,v13,v63
v_lshlrev_b32 v62,16,v60
M 22,a[20:23],34
s_mov_b32 s67,s65
R 26,2,8192
v_add_f32 v14,v14,v62
M 22,a[28:31],38
L 3
v_and_b32 v63,0xffff0000,v60
v_add_f32 v15,v15,v63
M 22,a[36:39],42
R 30,2,9216
v_lshlrev_b32 v62,16,v61
M 22,a[44:47],46
v_add_f32 v16,v16,v62
v_and_b32 v63,0xffff0000,v61
M 18,a[32:35],42
R 34,2,10240
v_add_f32 v17,v17,v63
M 18,a[40:43],46
L 3
buffer_load_dwordx4 v[58:61],v0,s[36:39],s74 offen offset:0 sc1
M 18,a[48:51],50
s_add_u32 s74,s49,s62
R 38,2,11264
v_cvt_pk_bf16_f32 v10,v10,v11
M 18,a[56:59],54
v_cvt_pk_bf16_f32 v11,v12,v13
v_cvt_pk_bf16_f32 v12,v14,v15
M 22,a[52:55],50
R 42,2,12288
v_cvt_pk_bf16_f32 v13,v16,v17
M 22,a[60:63],54
L 3
v_lshrrev_b32 v62,8,v0
v_lshl_add_u32 v62,v62,5,v0
M 22,a[68:71],26
R 46,2,13312
v_add_u32 v62,s73,v62
M 22,a[76:79],30
ds_write_b128 v62,v[10:13] offset:0
M 18,a[64:67],26
R 50,2,14336
ds_read2_b32 v[14:15],v5 offset0:0 offset1:16
M 18,a[72:75],30
L 5
ds_read2_b32 v[16:17],v5 offset0:32 offset1:48
M 18,a[80:83],34
R 54,2,15360
M 18,a[88:91],38
M 22,a[84:87],34
R 26,2,16384
M 22,a[92:95],38
L 6
M 22,a[100:103],42
R 30,2,17408
M 22,a[108:111],46
L 3
buffer_atomic_pk_add_bf16 v14,v9,s[40:43],s64 offen sc1
M 18,a[96:99],42
s_add_u32 s64,s75,8192
R 34,2,18432
M 18,a[104:107],46
L 3
buffer_atomic_pk_add_bf16 v15,v9,s[40:43],s64 offen sc1
M 18,a[112:115],50
s_add_u32 s64,s75,16384
R 38,2,19456
M 18,a[120:123],54
buffer_atomic_pk_add_bf16 v16,v9,s[40:43],s64 offen sc1
M 22,a[116:119],50
s_add_u32 s64,s75,24576
R 42,2,20480
M 22,a[124:127],54
L 3
buffer_atomic_pk_add_bf16 v17,v9,s[40:43],s64 offen sc1
M 22,a[132:135],26
R 46,2,21504
M 22,a[140:143],30
M 18,a[128:131],26
)"
R"(R 50,2,22528
M 18,a[136:139],30
L 3
M 18,a[144:147],34
R 54,2,23552
M 18,a[152:155],38
M 22,a[148:151],34
R 26,2,24576
M 22,a[156:159],38
L 3
M 22,a[164:167],42
R 30,2,25600
M 22,a[172:175],46
M 18,a[160:163],42
R 34,2,26624
M 18,a[168:171],46
L 3
M 18,a[176:179],50
s_waitcnt vmcnt(5)
R 38,2,27648
M 18,a[184:187],54
s_barrier
M 22,a[180:183],50
R 42,2,28672
M 22,a[188:191],54
L 3
M 22,a[196:199],26
R 46,2,29696
M 22,a[204:207],30
M 18,a[192:195],26
R 50,2,30720
M 18,a[200:203],30
L 3
M 18,a[208:211],34
R 54,2,31744
M 18,a[216:219],38
M 22,a[212:215],34
R 26,2,32768
M 22,a[220:223],38
L 3
M 22,a[228:231],42
R 30,2,33792
M 22,a[236:239],46
M 18,a[224:227],42
R 34,2,34816
M 18,a[232:235],46
L 3
M 18,a[240:243],50
R 38,2,35840
M 18,a[248:251],54
M 22,a[244:247],50
R 42,2,36864
M 22,a[252:255],54
L 3
M 22,v[68:71],26
R 46,2,37888
M 22,v[76:79],30
M 18,v[64:67],26
R 50,2,38912
M 18,v[72:75],30
L 3
M 18,v[80:83],34
R 54,2,39936
M 18,v[88:91],38
M 22,v[84:87],34
R 26,2,40960
M 22,v[92:95],38
L 3
M 22,v[100:103],42
R 30,2,41984
M 22,v[108:111],46
M 18,v[96:99],42
R 34,2,43008
M 18,v[104:107],46
L 3
M 18,v[112:115],50
R 38,2,44032
M 18,v[120:123],54
M 22,v[116:119],50
R 42,2,45056
M 22,v[124:127],54
L 3
M 22,v[132:135],26
R 46,2,46080
M 22,v[140:143],30
M 18,v[128:131],26
R 50,2,47104
M 18,v[136:139],30
L 3
M 18,v[144:147],34
R 54,2,48128
M 18,v[152:155],38
M 22,v[148:151],34
R 26,2,49152
M 22,v[156:159],38
L 3
M 22,v[164:167],42
R 30,2,50176
M 22,v[172:175],46
M 18,v[160:163],42
R 34,2,51200
M 18,v[168:171],46
L 3
M 18,v[176:179],50
R 38,2,52224
M 18,v[184:187],54
M 22,v[180:183],50
R 42,2,53248
M 22,v[188:191],54
L 3
M 22,v[196:199],26
R 46,2,54272
M 22,v[204:207],30
M 18,v[192:195],26
R 50,2,55296
M 18,v[200:203],30
L 3
M 18,v[208:211],34
R 54,2,56320
M 18,v[216:219],38
M 22,v[212:215],34
M 22,v[220:223],38
L 2
M 22,v[228:231],42
M 22,v[236:239],46
M 18,v[224:227],42
M 18,v[232:235],46
L 0
M 18,v[240:243],50
M 18,v[248:251],54
M 22,v[244:247],50
M 22,v[252:255],54
s_waitcnt vmcnt(4)
s_sub_u32 s75,s67,s85
v_lshlrev_b32 v62,16,v58
s_nop 2
v_add_f32 v18,v18,v62
v_and_b32 v63,0xffff0000,v58
v_add_f32 v19,v19,v63
v_lshlrev_b32 v62,16,v59
v_add_f32 v20,v20,v62
v_and_b32 v63,0xffff0000,v59
v_add_f32 v21,v21,v63
v_lshlrev_b32 v62,16,v60
v_add_f32 v22,v22,v62
v_and_b32 v63,0xffff0000,v60
v_add_f32 v23,v23,v63
v_lshlrev_b32 v62,16,v61
v_add_f32 v24,v24,v62
v_and_b32 v63,0xffff0000,v61
v_add_f32 v25,v25,v63
v_cvt_pk_bf16_f32 v18,v18,v19
v_cvt_pk_bf16_f32 v19,v20,v21
v_cvt_pk_bf16_f32 v20,v22,v23
v_cvt_pk_bf16_f32 v21,v24,v25
v_lshrrev_b32 v62,8,v0
v_lshl_add_u32 v62,v62,5,v0
v_add_u32 v62,s73,v62
ds_write_b128 v62,v[18:21] offset:0
ds_read2_b32 v[22:23],v5 offset0:0 offset1:16
ds_read2_b32 v[24:25],v5 offset0:32 offset1:48
s_mov_b64 s[40:41],s[86:87]
s_mov_b32 s42,4194304
s_mov_b32 s43,0x00020000
L 0
s_add_u32 s64,s75,0
buffer_atomic_pk_add_bf16 v22,v9,s[40:43],s64 offen sc1
s_add_u32 s64,s75,8192
buffer_atomic_pk_add_bf16 v23,v9,s[40:43],s64 offen sc1
s_add_u32 s64,s75,16384
buffer_atomic_pk_add_bf16 v24,v9,s[40:43],s64 offen sc1
s_add_u32 s64,s75,24576
buffer_atomic_pk_add_bf16 v25,v9,s[40:43],s64 offen sc1
s_branch Lg4_done_%=
Lg4_done_%=:
s_nop 0
s_barrier
buffer_store_dword v4,v3,s[36:39],s70 offen offset:0 sc0 sc1
s_waitcnt vmcnt(1)
s_barrier
buffer_store_dword v4,v3,s[36:39],s72 offen offset:0 sc0 sc1
s_waitcnt vmcnt(0) lgkmcnt(0)
s_barrier
s_endpgm
Lcode_g4_e:
.purgem M
.purgem R
.purgem T
.purgem D
.purgem L
)"
  :: "s"(ws), "s"(sWave), "s"(sG0), "s"(sStep), "s"(sOutOff), "s"(sFoutOff), "s"(sAuxOff), "s"(sWw), "s"(sBuf), "s"(sRk), "s"(sExp), "v"(vL16), "v"(vFlag), "v"(vSnapOff), "v"(vSnapRd), "v"(vWd0), "v"(vWd1), "v"(vWr0), "v"(vWr1), "v"(vSp), "v"(vSn), "s"(w4), "s"(out) : "v255", "v0", "v1", "v2", "v3", "v4", "v5", "v6", "v7", "v8", "v9", "v10", "v11", "v12", "v13", "v14", "v15", "v16", "v17", "v18", "v19", "v20", "v21", "v22", "v23", "v24", "v25", "v26", "v27", "v28", "v29", "v30", "v31", "v32", "v33", "v34", "v35", "v36", "v37", "v38", "v39", "v40", "v41", "v42", "v43", "v44", "v45", "v46", "v47", "v48", "v49", "v50", "v51", "v52", "v53", "v54", "v55", "v56", "v57", "v58", "v59", "v60", "v61", "v62", "v63", "v64", "v65", "v66", "v67", "v68", "v69", "v70", "v71", "v72", "v73", "v74", "v75", "v76", "v77", "v78", "v79", "v80", "v81", "v82", "v83", "v84", "v85", "v86", "v87", "v88", "v89", "v90", "v91", "v92", "v93", "v94", "v95", "v96", "v97", "v98", "v99", "v100", "v101", "v102", "v103", "v104", "v105", "v106", "v107", "v108", "v109", "v110", "v111", "v112", "v113", "v114", "v115", "v116", "v117", "v118", "v119", "v120", "v121", "v122", "v123", "v124", "v125", "v126", "v127", "v128", "v129", "v130", "v131", "v132", "v133", "v134", "v135", "v136", "v137", "v138", "v139", "v140", "v141", "v142", "v143", "v144", "v145", "v146", "v147", "v148", "v149", "v150", "v151", "v152", "v153", "v154", "v155", "v156", "v157", "v158", "v159", "v160", "v161", "v162", "v163", "v164", "v165", "v166", "v167", "v168", "v169", "v170", "v171", "v172", "v173", "v174", "v175", "v176", "v177", "v178", "v179", "v180", "v181", "v182", "v183", "v184", "v185", "v186", "v187", "v188", "v189", "v190", "v191", "v192", "v193", "v194", "v195", "v196", "v197", "v198", "v199", "v200", "v201", "v202", "v203", "v204", "v205", "v206", "v207", "v208", "v209", "v210", "v211", "v212", "v213", "v214", "v215", "v216", "v217", "v218", "v219", "v220", "v221", "v222", "v223", "v224", "v225", "v226", "v227", "v228", "v229", "v230", "v231", "a0", "a1", "a2", "a3", "a4", "a5", "a6", "a7", "a8", "a9", "a10", "a11", "a12", "a13", "a14", "a15", "a16", "a17", "a18", "a19", "a20", "a21", "a22", "a23", "a24", "a25", "a26", "a27", "a28", "a29", "a30", "a31", "a32", "a33", "a34", "a35", "a36", "a37", "a38", "a39", "a40", "a41", "a42", "a43", "a44", "a45", "a46", "a47", "a48", "a49", "a50", "a51", "a52", "a53", "a54", "a55", "a56", "a57", "a58", "a59", "a60", "a61", "a62", "a63", "a64", "a65", "a66", "a67", "a68", "a69", "a70", "a71", "a72", "a73", "a74", "a75", "a76", "a77", "a78", "a79", "a80", "a81", "a82", "a83", "a84", "a85", "a86", "a87", "a88", "a89", "a90", "a91", "a92", "a93", "a94", "a95", "a96", "a97", "a98", "a99", "a100", "a101", "a102", "a103", "a104", "a105", "a106", "a107", "a108", "a109", "a110", "a111", "a112", "a113", "a114", "a115", "a116", "a117", "a118", "a119", "a120", "a121", "a122", "a123", "a124", "a125", "a126", "a127", "a128", "a129", "a130", "a131", "a132", "a133", "a134", "a135", "a136", "a137", "a138", "a139", "a140", "a141", "a142", "a143", "a144", "a145", "a146", "a147", "a148", "a149", "a150", "a151", "a152", "a153", "a154", "a155", "a156", "a157", "a158", "a159", "a160", "a161", "a162", "a163", "a164", "a165", "a166", "a167", "a168", "a169", "a170", "a171", "a172", "a173", "a174", "a175", "a176", "a177", "a178", "a179", "a180", "a181", "a182", "a183", "a184", "a185", "a186", "a187", "a188", "a189", "a190", "a191", "a192", "a193", "a194", "a195", "a196", "a197", "a198", "a199", "a200", "a201", "a202", "a203", "a204", "a205", "a206", "a207", "a208", "a209", "a210", "a211", "a212", "a213", "a214", "a215", "a216", "a217", "a218", "a219", "a220", "a221", "a222", "a223", "a224", "a225", "a226", "a227", "a228", "a229", "a230", "a231", "a232", "a233", "a234", "a235", "a236", "a237", "a238", "a239", "a240", "a241", "a242", "a243", "a244", "a245", "a246", "a247", "a248", "a249", "a250", "a251", "a252", "a253", "a254", "a255", "s36", "s37", "s38", "s39", "s40", "s41", "s42", "s43", "s44", "s45", "s46", "s47", "s48", "s49", "s50", "s51", "s52", "s53", "s54", "s55", "s56", "s57", "s58", "s59", "s60", "s61", "s62", "s63", "s64", "s65", "s66", "s67", "s68", "s69", "s70", "s71", "s72", "s73", "s74", "s75", "s76", "s77", "s78", "s79", "s80", "s81", "s82", "s83", "s84", "s85", "s86", "s87", "s88", "s89", "s90", "s91", "s92", "s93", "s94", "s95", "s96", "s97", "vcc", "scc", "memory");
    __builtin_unreachable();
  } else {
    LANE_ID(lane2);
    constexpr int NG = 32 + (7 - NR4) * 16, T1 = RLN * NRW;
    if (kind == 6) for (int t = slice; t < T1; t += NRW) row_last(ws, out, t, lane2, wave, ep);
    for (int t = T1 + (kind == 6 ? NG + slice : kind == 2 ? rep * 8 + slice : 32 + (3 - rep) * 8 + slice); t < LAST_TASKS; t += NG + NRW) row_last(ws, out, t, lane2, wave, ep);
    if (b == 65 && wave == 0) set_flag(ws + OFF_EP, lane2, ep);
    CTS(14);
    asm volatile("s_endpgm\nLcode_end:" ::: "memory");
    __builtin_unreachable();
  }
}

extern "C" void kernel_launch(void* const* d_in, const int* in_sizes, int n_in, void* d_out, int out_size,
                              void* d_ws, size_t ws_size, hipStream_t stream) {
  const u16* q = (const u16*)d_in[0];
  const u16* k = (const u16*)d_in[1];
  const u16* v = (const u16*)d_in[2];
  const u16* shift_k = (const u16*)d_in[3];
  const u16* ssm_k = (const u16*)d_in[4];
  const u16* ssm_D = (const u16*)d_in[5];
  const u16* w1 = (const u16*)d_in[7];
  const u16* w2 = (const u16*)d_in[8];
  const u16* w3 = (const u16*)d_in[9];
  const u16* w4 = (const u16*)d_in[10];
  flow_kernel<<<256, 256, 0, stream>>>(q, k, v, shift_k, ssm_k, ssm_D, w1, w2, w3, w4, (char*)d_ws, (u16*)d_out);
}
```

```cpp
#include <hip/hip_runtime.h>
#include <hip/hip_bf16.h>
#include <stdint.h>
#include <type_traits>

typedef short s16x8 __attribute__((ext_vector_type(8)));
typedef short s16x4 __attribute__((ext_vector_type(4)));
typedef float f32x4 __attribute__((ext_vector_type(4)));
typedef unsigned short u16;
typedef unsigned short u16x8 __attribute__((ext_vector_type(8)));
typedef unsigned short u16x4 __attribute__((ext_vector_type(4)));
typedef float f32x2 __attribute__((ext_vector_type(2)));
typedef __bf16 bf16x2 __attribute__((ext_vector_type(2)));
typedef unsigned u32x4v __attribute__((ext_vector_type(4)));
typedef __attribute__((address_space(1))) unsigned gu32;

constexpr int S_ = 2048, D_ = 1024, F_ = 3584;
#define PF 1

constexpr int WS_USED = 111243392, LAST_TASKS = 228, U0 = 167, U1 = 221, NR4 = 7, RHELP = 1;
constexpr int OFF_KVR = 4194304, OFF_XP = 8388608, OFF_HS = 17825792, OFF_X1 = 31457280, OFF_G = 40894464, OFF_P0 = 71303168, OFF_P1 = 89128960, OFF_Y = 98566144, OFF_S1 = 106954752, OFF_ST = 107479040, OFF_FL = 109051904, OFF_FXP = 109056000, OFF_FH = 109334528, OFF_FX1 = 109613056, OFF_FG = 109891584, OFF_FP = 110587904, OFF_FKV = 111144960, OFF_FY = 111177728, OFF_EP = 111243264, OFF_DBG = 115343360;

#define WS_DIAG 0

__device__ __forceinline__ float bf2f(u16 v) { return __uint_as_float(((unsigned)v) << 16); }
__device__ __forceinline__ u16 f2bf(float f) { return __builtin_bit_cast(u16, (__bf16)f); }
__device__ __forceinline__ float rbf(float f) { return bf2f(f2bf(f)); }
__device__ __forceinline__ void st16(void* p, u16x8 v) {
  asm volatile("global_store_dwordx4 %0, %1, off sc0 sc1\n\ts_nop 1" :: "v"(p), "v"(v) : "memory");
}
#define WAIT_VM(n) asm volatile("s_waitcnt vmcnt(%0)" ::"n"(n) : "memory")
#define WAIT_LGKM0() asm volatile("s_waitcnt lgkmcnt(0)" ::: "memory")

#define CODE_RANGE(B, E, p, n)                                                                                   \
  const char* p; unsigned n;                                                                                     \
  { unsigned lo_, hi_;                                                                                           \
    asm volatile("s_getpc_b64 s[98:99]\nLpc%=:\n\ts_add_u32 %0, s98, (" B "-Lpc%=)&4294967295\n\ts_addc_u32 %1, s99, (" B "-Lpc%=)>>32\n\t" \
                 "s_mov_b32 %2, " E "-" B : "=s"(lo_), "=s"(hi_), "=s"(n) :: "s98", "s99", "scc");               \
    p = (const char*)(((unsigned long long)hi_ << 32) | lo_); }
__device__ __forceinline__ unsigned touch(const char* p, unsigned n, unsigned off) {
  unsigned t;
  asm volatile("global_load_dword %0, %1, off" : "=v"(t) : "v"(p + (off < n ? off : 0u)) : "memory");
  return t;
}

__device__ __forceinline__ void touch_lds(const char* p, unsigned a) {
  asm volatile("s_mov_b32 m0, %1\n\ts_nop 0\n\tglobal_load_lds_dword %0, off" :: "v"(p), "s"(a) : "memory");
}
#define PFPOL ""
__device__ __forceinline__ void touch_pol(const char* p, unsigned a) {
  asm volatile("s_mov_b32 m0, %1\n\ts_nop 0\n\tglobal_load_lds_dword %0, off " PFPOL :: "v"(p), "s"(a) : "memory");
}
#define YSLEEP 4
#define KSLEEP 2
#define CTS(i)
__device__ __forceinline__ f32x2 up2(unsigned p) { return (f32x2){__uint_as_float(p << 16), __uint_as_float(p & 0xffff0000u)}; }
__device__ __forceinline__ unsigned pk2(f32x2 v) { return __builtin_bit_cast(unsigned, __builtin_convertvector(v, bf16x2)); }
__device__ __forceinline__ f32x2 rbf2(f32x2 v) { return up2(pk2(v)); }
template <int N, class F> __device__ __forceinline__ void sfor(F&& f) {
  if constexpr (N > 0) { sfor<N - 1>(f); f(std::integral_constant<int, N - 1>{}); }
}
__device__ __forceinline__ void set_flag(char* p, int i, unsigned ep) {
  if (i < 32) __hip_atomic_store((gu32*)p + i, ep, __ATOMIC_RELAXED, __HIP_MEMORY_SCOPE_AGENT);
}
template <int NL>
__device__ __forceinline__ void poll_flags(const char* p, int n, int lane, unsigned ep) {
  for (int it = 0; it < 20000; ++it) {
    bool ok = true;
#pragma unroll
    for (int i = 0; i < NL; ++i) {
      const int f = lane + 64 * i;
      ok &= __hip_atomic_load((gu32*)(p + (f < n ? f : 0) * 128), __ATOMIC_RELAXED, __HIP_MEMORY_SCOPE_AGENT) == ep;
    }
    if (__all(ok)) break;
    __builtin_amdgcn_s_sleep(YSLEEP);
  }
  asm volatile("" ::: "memory");
}

constexpr int CV_HP = (2048 + 32) * 2, CV_UR = (2048 + 256) * 2, CV_CH = 2 * CV_HP + CV_UR;
constexpr int NCV = 174;
constexpr int CV_STG = 8 * CV_CH;
constexpr int YP = D_ * 16;

struct KvRegs { u32x4v k0, k1, k2, k3, k4, k5, k6, v0, v1, v2, v3, s0, s1, s2, s3; };
#define KV_LOADS                                                                                                                       \
  "v_lshrrev_b32 %[a], 3, %[tid]\n\tv_and_b32 %[c], 7, %[tid]\n\ts_lshr_b32 s90, %[tile], 4\n\ts_and_b32 s91, %[tile], 15\n\t"          \
  "v_lshlrev_b32 %[a], 13, %[a]\n\tv_lshlrev_b32 %[c], 4, %[c]\n\ts_lshl_b32 s90, s90, 18\n\ts_lshl_b32 s91, s91, 7\n\t"                 \
  "v_add_u32 %[a], s90, %[a]\n\tv_add_u32 %[c], s91, %[c]\n\tv_subrev_u32 %[h], 0x1000, %[a]\n\tv_max_i32 %[h], 0x800, %[h]\n\t"         \
  "v_add3_u32 %[a], %[a], %[c], s92\n\tv_add_u32 %[h], %[h], %[c]\n\tv_lshlrev_b32 %[c], 2, %[c]\n\ts_waitcnt lgkmcnt(0)\n\t"            \
  "global_load_dwordx4 %3, %[a], s[76:77] offset:-4096 nt\n\tglobal_load_dwordx4 %4, %[a], s[76:77] offset:-2048 nt\n\t"                \
  "global_load_dwordx4 %5, %[a], s[76:77] nt\n\tglobal_load_dwordx4 %6, %[a], s[76:77] offset:2048 nt\n\t"                              \
  "global_load_dwordx4 %7, %[a], s[78:79] offset:-4096 nt\n\tglobal_load_dwordx4 %8, %[a], s[78:79] offset:-2048 nt\n\t"                \
  "global_load_dwordx4 %9, %[a], s[78:79] nt\n\tglobal_load_dwordx4 %10, %[a], s[78:79] offset:2048 nt\n\t"                             \
  "global_load_dwordx4 %0, %[h], s[76:77] offset:-2048 nt\n\tglobal_load_dwordx4 %1, %[h], s[76:77] nt\n\t"                             \
  "global_load_dwordx4 %2, %[h], s[76:77] offset:2048 nt\n\t"                                                                           \
  "global_load_dwordx4 %11, %[c], s[80:81]\n\tglobal_load_dwordx4 %12, %[c], s[80:81] offset:16\n\t"                                    \
  "global_load_dwordx4 %13, %[c], s[80:81] offset:32\n\tglobal_load_dwordx4 %14, %[c], s[80:81] offset:48\n\t"
#define KV_OUTS(R) "=&v"(R.k0), "=&v"(R.k1), "=&v"(R.k2), "=&v"(R.k3), "=&v"(R.k4), "=&v"(R.k5), "=&v"(R.k6), "=&v"(R.v0), "=&v"(R.v1), "=&v"(R.v2), \
                   "=&v"(R.v3), "=&v"(R.s0), "=&v"(R.s1), "=&v"(R.s2), "=&v"(R.s3), [a] "=&v"(ta), [h] "=&v"(th), [c] "=&v"(tc)
#define KV_CLOB "s74", "s75", "s76", "s77", "s78", "s79", "s80", "s81", "s82", "s83", "s90", "s91", "s92", "scc", "vcc", "memory"
template <int N>
__device__ __forceinline__ void kv_wait(KvRegs& R) {
  asm volatile("s_waitcnt vmcnt(%15)" : "+v"(R.k0), "+v"(R.k1), "+v"(R.k2), "+v"(R.k3), "+v"(R.k4), "+v"(R.k5), "+v"(R.k6), "+v"(R.v0), "+v"(R.v1), "+v"(R.v2),
               "+v"(R.v3), "+v"(R.s0), "+v"(R.s1), "+v"(R.s2), "+v"(R.s3) : "n"(N) : "memory");
}
__device__ __forceinline__ void kv_store(KvRegs& R, char* __restrict__ ws, char* T, int b, int tid) {
  const int t0 = (b >> 4) * 128, c0 = (b & 15) * 64, ch8 = tid & 7, rg = tid >> 3;
  if (t0 + 4 * rg < 3) R.k0 = R.k1 = R.k2 = (u32x4v){0u, 0u, 0u, 0u};
  typedef unsigned u32x2 __attribute__((ext_vector_type(2)));
  const u32x4v kr[7] = {R.k0, R.k1, R.k2, R.k3, R.k4, R.k5, R.k6}, vr[4] = {R.v0, R.v1, R.v2, R.v3}, sk[4] = {R.s0, R.s1, R.s2, R.s3};
#pragma unroll
  for (int j2 = 0; j2 < 4; ++j2) {
    const f32x2 sa = up2(sk[j2][0]), sb = up2(sk[j2][1]), sc = up2(sk[j2][2]), sd = up2(sk[j2][3]);
    const f32x2 s0 = {sa[0], sc[0]}, s1 = {sa[1], sc[1]}, s2 = {sb[0], sd[0]}, s3 = {sb[1], sd[1]};
    f32x2 kf[7], o[4];
#pragma unroll
    for (int r = 0; r < 7; ++r) kf[r] = up2(kr[r][j2]);
#pragma unroll
    for (int r = 0; r < 4; ++r)
      o[r] = __builtin_elementwise_fma(kf[r + 3], s0, __builtin_elementwise_fma(kf[r + 2], s1, __builtin_elementwise_fma(kf[r + 1], s2, kf[r] * s3))) * up2(vr[r][j2]);
#pragma unroll
    for (int e = 0; e < 2; ++e)
      *(u32x2*)(T + (8 * ch8 + 2 * j2 + e) * 264 + (124 - 4 * rg) * 2) = (u32x2){pk2((f32x2){o[3][e], o[2][e]}), pk2((f32x2){o[1][e], o[0][e]})};
  }
  __syncthreads();
  u16* kvr = (u16*)(ws + OFF_KVR);
#pragma unroll
  for (int i = 0; i < 4; ++i) {
    const int idx = tid + 256 * i, ch = idx >> 4, cb = idx & 15;
    const u16x4 lo = *(const u16x4*)(T + ch * 264 + cb * 16), hi = *(const u16x4*)(T + ch * 264 + cb * 16 + 8);
    st16(kvr + (size_t)(c0 + ch) * S_ + (1920 - t0) + cb * 8, __builtin_shufflevector(lo, hi, 0, 1, 2, 3, 4, 5, 6, 7));
  }
}

#define LASTSLEEP 4
__device__ __forceinline__ void wait_flags(const char* p, unsigned ep) {
  gu32* f = (gu32*)p;
  for (int it = 0; it < 20000; ++it) {
    if (__all(__hip_atomic_load(f, __ATOMIC_RELAXED, __HIP_MEMORY_SCOPE_AGENT) == ep)) break;
    __builtin_amdgcn_s_sleep(LASTSLEEP);
  }
  asm volatile("" ::: "memory");
}
__device__ __forceinline__ void row_last(char* __restrict__ ws, u16* __restrict__ out, int t, int lane, int wave, unsigned ep) {
  const __amdgpu_buffer_rsrc_t rs = __builtin_amdgcn_make_buffer_rsrc(ws, 0, WS_USED, 0x00020000);
  const int r8 = lane & 7, seg = lane >> 3, un = 128 + (t >> 1);
  const unsigned fo = un * 32768 + (wave * 8 + (seg >> 2)) * 1024 + ((seg & 3) * 16 + (t & 1) * 8 + r8) * 16;
  wait_flags(ws + OFF_FP + un * 2048 + ((lane & 2) * 4 + wave * 2 + (lane & 1)) * 128, ep);
  u32x4v p0[4], p1[4];
#pragma unroll
  for (int i = 0; i < 4; ++i) {
    p0[i] = __builtin_bit_cast(u32x4v, __builtin_amdgcn_raw_buffer_load_b128(rs, fo + i * 2048, OFF_P0, 16));
    p1[i] = __builtin_bit_cast(u32x4v, __builtin_amdgcn_raw_buffer_load_b128(rs, fo + i * 2048, OFF_P1, 16));
  }
#pragma unroll
  for (int i = 0; i < 4; ++i) {
    u32x4v o;
#pragma unroll
    for (int e = 0; e < 4; ++e) o[e] = pk2(up2(p0[i][e]) + up2(p1[i][e]));
    st16(out + (size_t)(t * 8 + r8) * D_ + wave * 256 + i * 64 + seg * 8, __builtin_bit_cast(u16x8, o));
  }
}

__device__ __forceinline__ int swzB(int r) { return ((((r >> 1) & 1) | (((r >> 3) & 1) << 1)) << 1); }
#define TCH 1
#define LANE_ID(x) int x; asm volatile("v_mbcnt_lo_u32_b32 %0, -1, 0\n\tv_mbcnt_hi_u32_b32 %0, -1, %0" : "=v"(x))
#define W12 6
#define HL 0
#define W_B1 (b == 1)
#define W_B7 (b == 7)
#define W_I15 (idx == 15)
#define W_I31 (idx == 31)
#define W_B249 (b == 249)
#define RLN 3
#define QTB 1
constexpr int QT0 = 34 * 128;
#define CODE_MAX(x, y) "(((" x ">" y ")&" x ")|((" x "<=" y ")&" y "))"
constexpr int HU = 40960;
#define G3OR 5
constexpr int NRW = 22 - G3OR;
__global__ __launch_bounds__(256, 1) void flow_kernel(const u16* __restrict__ q, const u16* __restrict__ kin, const u16* __restrict__ vin,
                                                      const u16* __restrict__ shift_k, const u16* __restrict__ ssm_k, const u16* __restrict__ ssm_D,
                                                      const u16* __restrict__ w1, const u16* __restrict__ w2,
                                                      const u16* __restrict__ w3, const u16* __restrict__ w4, char* __restrict__ ws,
                                                      u16* __restrict__ out) {
  __shared__ __attribute__((aligned(16))) char lds[163840];
  KvRegs KR;
  unsigned ew, ta, th, tc;
  int b, tile;
  asm volatile("Lcode_b:\n\ts_load_dwordx4 s[76:79], %[ka], 0x8\n\ts_load_dwordx2 s[82:83], %[ka], 0x50\n\t"
               "s_getpc_b64 s[74:75]\n\ts_lshr_b32 s90, %[bi], 3\n\ts_mov_b32 m0, 0x27f00\n\tv_lshlrev_b32 %[a], 5, %[tid]\n\tv_add_lshl_u32 %[a], %[a], s90, 7\n\t"
               "v_min_u32 %[a], " CODE_MAX(CODE_MAX(CODE_MAX("(Lcode_g3_e-Lcode_b)", "(Lcode_g3o_e-Lcode_b)"), CODE_MAX("(Lcode_g4_e-Lcode_b)", "(Lcode_g12_e-Lcode_b)")), "(Lcode_end-Lcode_b)") "-64, %[a]\n\t"
               "global_load_lds_dword %[a], s[74:75]\n\ts_load_dwordx2 s[80:81], %[ka], 0x18\n\t"
               "s_mov_b32 %[b], %[bi]\n\ts_bfe_u32 s91, %[bi], 0x20001\n\ts_lshl2_add_u32 %[tile], s90, s91\n\ts_movk_i32 s92, 0x1000\n\t"
               "s_and_b32 s90, %[bi], 128\n\ts_bitcmp1_b32 %[bi], 0\n\ts_cselect_b32 s90, s90, 64\n\ts_add_u32 %[tile], %[tile], s90\n\t"
               KV_LOADS "v_mov_b32 %[a], %[eo]\n\tglobal_load_dword %[ew], %[a], s[82:83] sc1"
               : KV_OUTS(KR), [ew] "=&v"(ew), [b] "=&s"(b), [tile] "=&s"(tile)
               : [ka] "s"(__builtin_amdgcn_kernarg_segment_ptr()), [bi] "s"(blockIdx.x), [tid] "v"(threadIdx.x), [eo] "n"(OFF_EP) : KV_CLOB);
  const unsigned t_start = 0;
  const int tid = threadIdx.x, lane = tid & 63, wave = __builtin_amdgcn_readfirstlane(tid >> 6);
  const int xcd = b & 7, idx = b >> 3;
  const unsigned sLds = (unsigned)(size_t)lds;
  const int o8 = idx & 7;
  int kind, rep, slice, kh = 0;
  int j3 = 0, j4 = 0;
  const int xp = xcd >> 1, pr = xp & 1;
  if (!(xcd & 1)) {
    if (idx < 20) { kind = 4; rep = idx < 16 ? (xp >> 1) * 3 + (idx >> 3) : 2; kh = pr; slice = idx < 16 ? o8 : (xp >> 1) * 4 + o8; j4 = rep * 16 + kh * 8 + slice; }
    else { kind = 3; rep = pr * 2 + (idx >= 26); slice = (xp >> 1) * 6 + idx - (idx >= 26 ? 26 : 20); j3 = rep * 18 + slice; }
  } else if (idx < 16) { kind = 1 + (idx >> 3); rep = xp; slice = o8; }
  else {
    const int k = idx - 16, n3 = xp < 2 ? 12 : 9, no = xp < 2 ? 0 : 5 - xp;
    if (k < n3) { kind = 3; rep = xp < 2 ? pr * 2 + (k >= 6) : 4; slice = xp < 2 ? 12 + k - 6 * (k >= 6) : pr * 9 + k; j3 = rep * 18 + slice; }
    else if (k < n3 + no) { kind = 5; rep = pr * 3 + k - n3; slice = 18; j3 = 90 + rep; }
    else { kind = 6; rep = 0; slice = xp * 4 + k - n3 - no; }
  }
  CTS(0);
  const unsigned sScr = sLds + 163584;
  unsigned ep = 0;
  auto w_touch = [&](const u16* w) {
#pragma unroll
    for (int i = 0; i < 2; ++i) touch_lds((const char*)w + (size_t)(rep * 256 + i * 128 + (tid >> 1)) * 2048 + slice * 256 + (tid & 1) * 128, sScr);
  };
  {
  const bool isconv = kind >= 3 && kind != 6 && j3 < 94;
  const int nw = (kind == 4 || j3 >= 92) ? 2 : 1, c0 = kind == 4 ? 8 * j4 : j3 < 92 ? 640 + 4 * j3 : j3 < 94 ? 272 + 8 * j3 : 0, cw = c0 + nw * wave;
  const unsigned hd = isconv ? sLds + 2 * wave * CV_CH + 32 : 0x30000u;
  auto h_dma = [&](bool first) {
#pragma unroll
    for (int j = 0; j < 2; ++j) {
      const u16* hp = ssm_k + (size_t)cw * S_ + (isconv && j < nw ? j * S_ + lane * 8 : 0);
      if (first) asm volatile("s_mov_b32 m0, %1\n\ts_nop 0\n\tglobal_load_lds_dwordx4 %0, off nt" :: "v"(hp), "s"(hd + j * CV_CH) : "memory");
      else asm volatile("s_mov_b32 m0, %1\n\ts_nop 0\n\tglobal_load_lds_dwordx4 %0, off offset:1024 nt\n\t"
                        "global_load_lds_dwordx4 %0, off offset:2048 nt\n\tglobal_load_lds_dwordx4 %0, off offset:3072 nt" :: "v"(hp), "s"(hd + j * CV_CH) : "memory");
    }
  };
  h_dma(true);
  if (HL == 0) h_dma(false);
  *(u32x4v*)(lds + (2 * wave + (lane >> 5)) * CV_CH + 2 * CV_HP + 4096 + (lane & 31) * 16) = (u32x4v){0u, 0u, 0u, 0u};
  if (lane < 8) *(u32x4v*)(lds + (2 * wave + (lane >> 2)) * CV_CH + ((lane >> 1) & 1) * CV_HP + (lane & 1) * 16) = (u32x4v){0u, 0u, 0u, 0u};
  kv_wait<HL ? 3 : 9>(KR);
  CTS(12);
  kv_store(KR, ws, lds + CV_STG, tile, tid);
  CTS(1);
  if (HL == 1) h_dma(false);
  const unsigned dw = ((const unsigned*)ssm_D)[cw >> 1];
  asm volatile("s_waitcnt vmcnt(%1)" : "+v"(ew) : "n"(HL == 1 ? 6 : 0) : "memory");
  ep = __builtin_amdgcn_readfirstlane(ew) + 1u;
  if (ep == 0u) ep = 1u;
  __builtin_amdgcn_s_barrier();
  set_flag(ws + OFF_FKV + tile * 128, tid, ep);
  CTS(2);
  if (HL == 2) h_dma(false);
  if (kind < 3 && (W12 & kind) != 0) w_touch(kind == 1 ? w1 : w2);
  if (kind == 2 && (W12 & 4) != 0) w_touch(w1);
  if (isconv) {
    LANE_ID(lane);
    const int tid = wave * 64 + lane;
    const unsigned sC0 = (cw & 1) ? dw & 0xffff0000u : dw << 16, sC1 = dw & 0xffff0000u;
    const int l15 = lane & 15, lg = lane >> 4, cp = (l15 + 1) & 1, iw = kind == 4 ? j4 : 80 + j3, own = 2 * wave * CV_CH;
    const int ch = tid & (4 * nw - 1), tb = nw == 2 ? tid >> 3 : tid >> 2, slot = nw == 2 ? ch : 2 * ch;
    const unsigned sWave = wave, sBuf = nw, sAuxOff = OFF_KVR + cw * 4096, sFoutOff = OFF_FY + iw * 128, sExp = __builtin_amdgcn_readfirstlane(ep), sDbg = 110u * 1048576u + (b * 4 + wave) * 8192;
    const unsigned vL16 = lane * 16, vPA = own + cp * CV_HP + 2 * (8 * lg + l15 + 1 - cp), vPB = own + 2 * CV_HP + 2 * (2032 - 16 * l15 + 8 * lg) - 512 * 7;
    const unsigned vPU = own + 2 * CV_HP + 2 * (2044 - 16 * l15 - 4 * lg) - 512 * 7, vPW = 32 * l15 + 8 * lg;
    const unsigned vOR = slot * CV_CH + tb * 16, vORA = CV_STG + slot * 1024 + tb * 16, vOG = tb * YP + (c0 + ch) * 16;
    const unsigned vFA = ((lane & 3) * 16 + (c0 >> 6)) * 128, vFB = ((4 + lane % 12) * 16 + (c0 >> 6)) * 128, vFlag = tid < 32 ? tid * 4 : 0x80000000u;
    const unsigned vTW = lane < 42 ? (iw * 168 + wave * 42 + lane) * 128 : 0x80000000u;
    const unsigned vTX = lane < 44 ? ((kind == 4 ? j4 : j3 - 12) * 176 + wave * 44 + lane) * 128 : 0x80000000u;
    (void)sDbg;
    asm volatile(
R"(.macro M d,w,x
v_mfma_f32_16x16x32_bf16 v[\d:\d+3], \w, v[\x:\x+3], v[\d:\d+3]
.endm
.macro R d,a,o
ds_read_b128 v[\d:\d+3], v\a offset:\o
.endm
.macro T d,a,o
ds_read_b64_tr_b16 \d, v\a offset:\o
.endm
.macro D a,s,o
buffer_load_dwordx4 v\a, s[36:39], s\s offen offset:\o sc1 lds
.endm
.macro L c
s_waitcnt lgkmcnt(\c)
.endm
Lcode_cv_b:
s_mov_b64 s[36:37],%0
s_mov_b32 s38,111243392
s_mov_b32 s39,0x00020000
s_mov_b32 s44,%1
s_mov_b32 s51,%2
s_mov_b32 s49,%3
s_mov_b32 s48,%4
s_mov_b32 s86,%5
s_mov_b32 s87,%6
s_mov_b32 s53,%7
v_mov_b32 v0,%8
v_mov_b32 v1,%9
v_mov_b32 v2,%10
v_mov_b32 v3,%11
v_mov_b32 v4,%12
v_mov_b32 v5,%13
v_mov_b32 v6,%14
v_mov_b32 v7,%15
v_mov_b32 v8,%16
v_mov_b32 v9,%17
v_mov_b32 v10,%18
v_mov_b32 v11,%19
v_mov_b32 v12,%20
s_mov_b32 s58,111144960
buffer_load_dword v13,v8,s[36:39],s58 offen offset:0 sc1
s_mul_i32 s57,s44,25856
v_add_u32 v18,s57,v0
v_add_u32 v18,32,v18
v_mov_b32 v19,s57
R 88,18,0
ds_read_b32 v92,v18 offset:16
R 152,18,12928
ds_read_b32 v156,v18 offset:12944
ds_read_b32 v24,v19 offset:32
ds_read_b32 v25,v19 offset:12960
s_mov_b64 s[40:41],%21
s_mov_b32 s42,7340032
s_mov_b32 s43,0x00020000
s_mov_b64 s[60:61],%22
s_mov_b32 s62,7340032
s_mov_b32 s63,0x00020000
v_mov_b32 v23,s53
s_mov_b32 s82,0
s_add_u32 s55,s49,4096
s_cmp_eq_u32 s51,2
s_cselect_b32 s55,s55,0x7fff0000
s_cselect_b32 s56,0,0x70000000
s_add_u32 s57,s57,8320
v_add_u32 v14,1024,v1
v_add_u32 v15,2048,v1
v_add_u32 v16,3072,v1
v_add_u32 v17,12928,v1
L 4
v_alignbyte_b32 v96,v89,v88,2
v_alignbyte_b32 v97,v90,v89,2
v_alignbyte_b32 v98,v91,v90,2
v_alignbyte_b32 v99,v92,v91,2
ds_write_b128 v18,v[96:99] offset:4160
L 3
v_alignbyte_b32 v160,v153,v152,2
v_alignbyte_b32 v161,v154,v153,2
v_alignbyte_b32 v162,v155,v154,2
v_alignbyte_b32 v163,v156,v155,2
ds_write_b128 v18,v[160:163] offset:17088
L 2
v_lshlrev_b32 v24,16,v24
v_lshlrev_b32 v25,16,v25
s_mov_b64 exec,1
ds_write_b32 v19,v24 offset:4188
ds_write_b32 v19,v25 offset:17116
s_mov_b64 exec,-1
ds_read2_b32 a[0:1],v1 offset0:0 offset1:1
ds_read2_b32 a[2:3],v1 offset0:2 offset1:3
ds_read2_b32 a[4:5],v1 offset0:128 offset1:129
ds_read2_b32 a[6:7],v1 offset0:130 offset1:131
ds_read2_b32 a[8:9],v1 offset0:16 offset1:17
ds_read2_b32 a[10:11],v1 offset0:18 offset1:19
ds_read2_b32 a[12:13],v1 offset0:144 offset1:145
ds_read2_b32 a[14:15],v1 offset0:146 offset1:147
ds_read2_b32 a[16:17],v1 offset0:32 offset1:33
ds_read2_b32 a[18:19],v1 offset0:34 offset1:35
ds_read2_b32 a[20:21],v1 offset0:160 offset1:161
ds_read2_b32 a[22:23],v1 offset0:162 offset1:163
ds_read2_b32 a[24:25],v1 offset0:48 offset1:49
ds_read2_b32 a[26:27],v1 offset0:50 offset1:51
ds_read2_b32 a[28:29],v1 offset0:176 offset1:177
ds_read2_b32 a[30:31],v1 offset0:178 offset1:179
ds_read2_b32 a[32:33],v1 offset0:64 offset1:65
ds_read2_b32 a[34:35],v1 offset0:66 offset1:67
ds_read2_b32 a[36:37],v1 offset0:192 offset1:193
ds_read2_b32 a[38:39],v1 offset0:194 offset1:195
ds_read2_b32 a[40:41],v1 offset0:80 offset1:81
ds_read2_b32 a[42:43],v1 offset0:82 offset1:83
ds_read2_b32 a[44:45],v1 offset0:208 offset1:209
ds_read2_b32 a[46:47],v1 offset0:210 offset1:211
ds_read2_b32 a[48:49],v1 offset0:96 offset1:97
ds_read2_b32 a[50:51],v1 offset0:98 offset1:99
ds_read2_b32 a[52:53],v1 offset0:224 offset1:225
ds_read2_b32 a[54:55],v1 offset0:226 offset1:227
ds_read2_b32 a[56:57],v1 offset0:112 offset1:113
ds_read2_b32 a[58:59],v1 offset0:114 offset1:115
ds_read2_b32 a[60:61],v1 offset0:240 offset1:241
ds_read2_b32 a[62:63],v1 offset0:242 offset1:243
ds_read2_b32 a[64:65],v17 offset0:0 offset1:1
ds_read2_b32 a[66:67],v17 offset0:2 offset1:3
ds_read2_b32 a[68:69],v17 offset0:128 offset1:129
ds_read2_b32 a[70:71],v17 offset0:130 offset1:131
ds_read2_b32 a[72:73],v17 offset0:16 offset1:17
ds_read2_b32 a[74:75],v17 offset0:18 offset1:19
ds_read2_b32 a[76:77],v17 offset0:144 offset1:145
ds_read2_b32 a[78:79],v17 offset0:146 offset1:147
ds_read2_b32 a[80:81],v17 offset0:32 offset1:33
ds_read2_b32 a[82:83],v17 offset0:34 offset1:35
ds_read2_b32 a[84:85],v17 offset0:160 offset1:161
ds_read2_b32 a[86:87],v17 offset0:162 offset1:163
ds_read2_b32 a[88:89],v17 offset0:48 offset1:49
ds_read2_b32 a[90:91],v17 offset0:50 offset1:51
ds_read2_b32 a[92:93],v17 offset0:176 offset1:177
ds_read2_b32 a[94:95],v17 offset0:178 offset1:179
ds_read2_b32 a[96:97],v17 offset0:64 offset1:65
ds_read2_b32 a[98:99],v17 offset0:66 offset1:67
ds_read2_b32 a[100:101],v17 offset0:192 offset1:193
ds_read2_b32 a[102:103],v17 offset0:194 offset1:195
ds_read2_b32 a[104:105],v17 offset0:80 offset1:81
ds_read2_b32 a[106:107],v17 offset0:82 offset1:83
ds_read2_b32 a[108:109],v17 offset0:208 offset1:209
ds_read2_b32 a[110:111],v17 offset0:210 offset1:211
ds_read2_b32 a[112:113],v17 offset0:96 offset1:97
ds_read2_b32 a[114:115],v17 offset0:98 offset1:99
ds_read2_b32 a[116:117],v17 offset0:224 offset1:225
ds_read2_b32 a[118:119],v17 offset0:226 offset1:227
ds_read2_b32 a[120:121],v17 offset0:112 offset1:113
ds_read2_b32 a[122:123],v17 offset0:114 offset1:115
ds_read2_b32 a[124:125],v17 offset0:240 offset1:241
ds_read2_b32 a[126:127],v17 offset0:242 offset1:243
Lcv_bpa_%=:
s_waitcnt vmcnt(0)
v_cmp_eq_u32 vcc,s53,v13
s_add_u32 s82,s82,1
s_cmp_eq_u64 vcc,-1
s_cbranch_scc1 Lcv_bqa_%=
s_sleep 1
buffer_load_dword v13,v8,s[36:39],s58 offen offset:0 sc1
s_cmp_lt_u32 s82,20000
s_cbranch_scc1 Lcv_bpa_%=
s_waitcnt vmcnt(0)
Lcv_bqa_%=:
s_add_u32 m0,s57,0
s_nop 0
D 0,49,3072
s_add_u32 m0,s57,12928
s_nop 0
D 0,55,3072
buffer_load_dword v13,v9,s[36:39],s58 offen offset:0 sc1
R 88,18,0
ds_read_b32 v92,v18 offset:16
R 104,18,1024
ds_read_b32 v108,v18 offset:1040
R 120,18,2048
ds_read_b32 v124,v18 offset:2064
R 136,18,3072
ds_read_b32 v140,v18 offset:3088
L 6
v_alignbyte_b32 v96,v89,v88,2
v_alignbyte_b32 v97,v90,v89,2
v_alignbyte_b32 v98,v91,v90,2
v_alignbyte_b32 v99,v92,v91,2
ds_write_b128 v18,v[96:99] offset:4160
L 5
v_alignbyte_b32 v112,v105,v104,2
v_alignbyte_b32 v113,v106,v105,2
v_alignbyte_b32 v114,v107,v106,2
v_alignbyte_b32 v115,v108,v107,2
ds_write_b128 v18,v[112:115] offset:5184
L 4
v_alignbyte_b32 v128,v121,v120,2
v_alignbyte_b32 v129,v122,v121,2
v_alignbyte_b32 v130,v123,v122,2
v_alignbyte_b32 v131,v124,v123,2
ds_write_b128 v18,v[128:131] offset:6208
L 3
v_alignbyte_b32 v144,v137,v136,2
v_alignbyte_b32 v145,v138,v137,2
v_alignbyte_b32 v146,v139,v138,2
v_alignbyte_b32 v147,v140,v139,2
ds_write_b128 v18,v[144:147] offset:7232
R 152,18,12928
ds_read_b32 v156,v18 offset:12944
R 168,18,13952
ds_read_b32 v172,v18 offset:13968
R 184,18,14976
ds_read_b32 v188,v18 offset:14992
R 200,18,16000
ds_read_b32 v204,v18 offset:16016
L 6
v_alignbyte_b32 v160,v153,v152,2
v_alignbyte_b32 v161,v154,v153,2
v_alignbyte_b32 v162,v155,v154,2
v_alignbyte_b32 v163,v156,v155,2
ds_write_b128 v18,v[160:163] offset:17088
L 5
v_alignbyte_b32 v176,v169,v168,2
v_alignbyte_b32 v177,v170,v169,2
v_alignbyte_b32 v178,v171,v170,2
v_alignbyte_b32 v179,v172,v171,2
ds_write_b128 v18,v[176:179] offset:18112
L 4
v_alignbyte_b32 v192,v185,v184,2
v_alignbyte_b32 v193,v186,v185,2
v_alignbyte_b32 v194,v187,v186,2
v_alignbyte_b32 v195,v188,v187,2
ds_write_b128 v18,v[192:195] offset:19136
L 3
v_alignbyte_b32 v208,v201,v200,2
v_alignbyte_b32 v209,v202,v201,2
v_alignbyte_b32 v210,v203,v202,2
v_alignbyte_b32 v211,v204,v203,2
ds_write_b128 v18,v[208:211] offset:20160
s_lshl_b32 s78,s44,11
s_add_u32 s78,s78,103424
v_add_u32 v18,s78,v4
v_add_u32 v4,v19,v4
s_waitcnt vmcnt(0)
v_cmp_eq_u32 vcc,s53,v13
s_mov_b32 s83,0
s_cmp_eq_u64 vcc,-1
s_cbranch_scc0 Lcv_a_%=
s_mov_b32 s83,1
s_add_u32 m0,s57,0
s_nop 0
D 0,49,2048
D 0,49,1024
D 0,49,0
s_add_u32 m0,s57,12928
s_nop 0
D 0,55,2048
D 0,55,1024
D 0,55,0
Lcv_a_%=:
s_cmp_eq_u32 s51,2
s_cbranch_scc1 Lcv_a2_%=
L 0
R 88,2,3584
R 92,2,3072
R 96,2,3648
R 100,2,3136
R 104,2,3712
R 108,2,3200
R 112,2,3776
R 116,2,3264
L 6
v_mfma_f32_16x16x32_bf16 v[40:43],a[0:3],v[88:91],0
R 120,2,3840
R 124,2,3328
v_mfma_f32_16x16x32_bf16 v[44:47],a[0:3],v[92:95],0
M 44,a[4:7],88
L 6
M 40,a[8:11],96
R 128,2,3904
R 132,2,3392
M 44,a[8:11],100
M 44,a[12:15],96
L 6
M 40,a[16:19],104
R 136,2,3968
R 140,2,3456
M 44,a[16:19],108
M 44,a[20:23],104
L 6
M 40,a[24:27],112
R 144,2,4032
R 148,2,3520
M 44,a[24:27],116
M 44,a[28:31],112
L 6
M 40,a[32:35],120
M 44,a[32:35],124
M 44,a[36:39],120
L 4
M 40,a[40:43],128
M 44,a[40:43],132
M 44,a[44:47],128
L 2
M 40,a[48:51],136
ds_read_b64 v[24:25],v3 offset:3584
ds_read_b64 v[26:27],v3 offset:3072
M 44,a[48:51],140
M 44,a[52:55],136
L 2
M 40,a[56:59],144
M 44,a[56:59],148
M 44,a[60:63],144
L 0
v_and_b32 v36,0xffff0000,v25
v_lshlrev_b32 v37,16,v25
v_and_b32 v38,0xffff0000,v24
v_lshlrev_b32 v39,16,v24
s_nop 0
v_fma_f32 v40,v36,s86,v40
v_fma_f32 v41,v37,s86,v41
v_fma_f32 v42,v38,s86,v42
v_fma_f32 v43,v39,s86,v43
v_cvt_pk_bf16_f32 v40,v40,v41
v_cvt_pk_bf16_f32 v41,v42,v43
ds_write_b64 v18,v[40:41] offset:0
v_and_b32 v36,0xffff0000,v27
v_lshlrev_b32 v37,16,v27
v_and_b32 v38,0xffff0000,v26
v_lshlrev_b32 v39,16,v26
v_fma_f32 v44,v36,s86,v44
v_fma_f32 v45,v37,s86,v45
v_fma_f32 v46,v38,s86,v46
v_fma_f32 v47,v39,s86,v47
v_cvt_pk_bf16_f32 v44,v44,v45
v_cvt_pk_bf16_f32 v45,v46,v47
ds_write_b64 v18,v[44:45] offset:512
s_branch Lcv_a3_%=
Lcv_a2_%=:
L 0
R 88,2,3584
R 92,2,3072
R 96,2,3648
R 100,2,3136
R 104,2,3712
R 108,2,3200
R 112,2,3776
R 116,2,3264
L 6
v_mfma_f32_16x16x32_bf16 v[40:43],a[0:3],v[88:91],0
R 120,2,3840
R 124,2,3328
v_mfma_f32_16x16x32_bf16 v[44:47],a[0:3],v[92:95],0
M 44,a[4:7],88
L 6
M 40,a[8:11],96
R 128,2,3904
R 132,2,3392
M 44,a[8:11],100
M 44,a[12:15],96
L 6
M 40,a[16:19],104
R 136,2,3968
R 140,2,3456
M 44,a[16:19],108
M 44,a[20:23],104
L 6
M 40,a[24:27],112
R 144,2,4032
R 148,2,3520
M 44,a[24:27],116
M 44,a[28:31],112
L 6
M 40,a[32:35],120
R 152,2,16512
R 156,2,16000
M 44,a[32:35],124
M 44,a[36:39],120
L 6
M 40,a[40:43],128
R 160,2,16576
R 164,2,16064
M 44,a[40:43],132
M 44,a[44:47],128
L 6
M 40,a[48:51],136
R 168,2,16640
R 172,2,16128
M 44,a[48:51],140
ds_read_b64 v[24:25],v3 offset:3584
ds_read_b64 v[26:27],v3 offset:3072
M 44,a[52:55],136
L 8
M 40,a[56:59],144
R 176,2,16704
R 180,2,16192
M 44,a[56:59],148
M 44,a[60:63],144
L 8
v_mfma_f32_16x16x32_bf16 v[48:51],a[64:67],v[152:155],0
R 184,2,16768
R 188,2,16256
v_mfma_f32_16x16x32_bf16 v[52:55],a[64:67],v[156:159],0
M 52,a[68:71],152
L 8
M 48,a[72:75],160
R 192,2,16832
R 196,2,16320
M 52,a[72:75],164
L 6
v_and_b32 v36,0xffff0000,v25
M 52,a[76:79],160
v_lshlrev_b32 v37,16,v25
v_and_b32 v38,0xffff0000,v24
v_lshlrev_b32 v39,16,v24
s_nop 0
M 48,a[80:83],168
R 200,2,16896
R 204,2,16384
M 52,a[80:83],172
v_fma_f32 v40,v36,s86,v40
v_fma_f32 v41,v37,s86,v41
M 52,a[84:87],168
v_fma_f32 v42,v38,s86,v42
v_fma_f32 v43,v39,s86,v43
v_cvt_pk_bf16_f32 v40,v40,v41
L 6
M 48,a[88:91],176
R 208,2,16960
R 212,2,16448
M 52,a[88:91],180
v_cvt_pk_bf16_f32 v41,v42,v43
ds_write_b64 v18,v[40:41] offset:0
M 52,a[92:95],176
v_and_b32 v36,0xffff0000,v27
v_lshlrev_b32 v37,16,v27
v_and_b32 v38,0xffff0000,v26
L 7
M 48,a[96:99],184
M 52,a[96:99],188
v_lshlrev_b32 v39,16,v26
v_fma_f32 v44,v36,s86,v44
M 52,a[100:103],184
v_fma_f32 v45,v37,s86,v45
v_fma_f32 v46,v38,s86,v46
v_fma_f32 v47,v39,s86,v47
L 5
M 48,a[104:107],192
M 52,a[104:107],196
v_cvt_pk_bf16_f32 v44,v44,v45
v_cvt_pk_bf16_f32 v45,v46,v47
M 52,a[108:111],192
ds_write_b64 v18,v[44:45] offset:512
L 4
M 48,a[112:115],200
ds_read_b64 v[24:25],v3 offset:16512
ds_read_b64 v[26:27],v3 offset:16000
M 52,a[112:115],204
M 52,a[116:119],200
L 4
M 48,a[120:123],208
M 52,a[120:123],212
M 52,a[124:127],208
L 0
v_and_b32 v36,0xffff0000,v25
v_lshlrev_b32 v37,16,v25
v_and_b32 v38,0xffff0000,v24
v_lshlrev_b32 v39,16,v24
s_nop 0
v_fma_f32 v48,v36,s87,v48
v_fma_f32 v49,v37,s87,v49
v_fma_f32 v50,v38,s87,v50
v_fma_f32 v51,v39,s87,v51
v_cvt_pk_bf16_f32 v48,v48,v49
v_cvt_pk_bf16_f32 v49,v50,v51
ds_write_b64 v18,v[48:49] offset:1024
v_and_b32 v36,0xffff0000,v27
v_lshlrev_b32 v37,16,v27
v_and_b32 v38,0xffff0000,v26
v_lshlrev_b32 v39,16,v26
v_fma_f32 v52,v36,s87,v52
v_fma_f32 v53,v37,s87,v53
v_fma_f32 v54,v38,s87,v54
v_fma_f32 v55,v39,s87,v55
v_cvt_pk_bf16_f32 v52,v52,v53
v_cvt_pk_bf16_f32 v53,v54,v55
ds_write_b64 v18,v[52:53] offset:1536
Lcv_a3_%=:
L 0
s_barrier
R 152,6,0
R 156,6,512
s_mov_b32 s78,98566144
L 1
buffer_store_dwordx4 v[152:155],v7,s[36:39],s78 offen offset:0 sc0 sc1
s_add_u32 s78,s56,99090432
L 0
buffer_store_dwordx4 v[156:159],v7,s[36:39],s78 offen offset:0 sc0 sc1
s_cmp_eq_u32 s83,1
s_cbranch_scc1 Lcv_b_%=
s_waitcnt vmcnt(0)
s_barrier
buffer_store_dword v23,v10,s[36:39],s48 offen offset:0 sc0 sc1
buffer_load_dword v13,v9,s[36:39],s58 offen offset:0 sc1
Lcv_bpb_%=:
)"
R"(s_waitcnt vmcnt(0)
v_cmp_eq_u32 vcc,s53,v13
s_add_u32 s82,s82,1
s_cmp_eq_u64 vcc,-1
s_cbranch_scc1 Lcv_bqb_%=
s_sleep 1
buffer_load_dword v13,v9,s[36:39],s58 offen offset:0 sc1
s_cmp_lt_u32 s82,20000
s_cbranch_scc1 Lcv_bpb_%=
s_waitcnt vmcnt(0)
Lcv_bqb_%=:
s_add_u32 m0,s57,0
s_nop 0
D 0,49,2048
D 0,49,1024
D 0,49,0
s_add_u32 m0,s57,12928
s_nop 0
D 0,55,2048
D 0,55,1024
D 0,55,0
s_waitcnt vmcnt(0)
Lcv_b_%=:
s_waitcnt vmcnt(2)
ds_read2_b32 v[88:89],v1 offset0:0 offset1:1
ds_read2_b32 v[90:91],v1 offset0:2 offset1:3
R 120,2,3584
ds_read2_b32 v[92:93],v1 offset0:128 offset1:129
ds_read2_b32 v[94:95],v1 offset0:130 offset1:131
R 124,2,3072
ds_read2_b32 v[96:97],v14 offset0:0 offset1:1
ds_read2_b32 v[98:99],v14 offset0:2 offset1:3
R 128,2,2560
ds_read2_b32 v[100:101],v14 offset0:128 offset1:129
ds_read2_b32 v[102:103],v14 offset0:130 offset1:131
R 132,2,2048
ds_read2_b32 v[104:105],v15 offset0:0 offset1:1
ds_read2_b32 v[106:107],v15 offset0:2 offset1:3
R 136,2,1536
ds_read2_b32 v[108:109],v15 offset0:128 offset1:129
ds_read2_b32 v[110:111],v15 offset0:130 offset1:131
R 140,2,1024
ds_read2_b32 v[112:113],v16 offset0:0 offset1:1
ds_read2_b32 v[114:115],v16 offset0:2 offset1:3
R 144,2,512
ds_read2_b32 v[116:117],v16 offset0:128 offset1:129
ds_read2_b32 v[118:119],v16 offset0:130 offset1:131
R 148,2,0
L 15
v_mfma_f32_16x16x32_bf16 v[40:43],v[88:91],v[128:131],0
ds_read2_b32 v[152:153],v1 offset0:16 offset1:17
M 40,v[92:95],124
ds_read2_b32 v[154:155],v1 offset0:18 offset1:19
M 40,v[96:99],120
L 14
R 184,2,3648
v_mfma_f32_16x16x32_bf16 v[44:47],v[88:91],v[132:135],0
ds_read2_b32 v[156:157],v1 offset0:144 offset1:145
M 44,v[92:95],128
M 44,v[96:99],124
ds_read2_b32 v[158:159],v1 offset0:146 offset1:147
M 44,v[100:103],120
L 14
R 188,2,3136
v_mfma_f32_16x16x32_bf16 v[48:51],v[88:91],v[136:139],0
ds_read2_b32 v[160:161],v14 offset0:16 offset1:17
M 48,v[92:95],132
ds_read2_b32 v[162:163],v14 offset0:18 offset1:19
M 48,v[96:99],128
M 48,v[100:103],124
R 192,2,2624
M 48,v[104:107],120
L 15
ds_read2_b32 v[164:165],v14 offset0:144 offset1:145
v_mfma_f32_16x16x32_bf16 v[52:55],v[88:91],v[140:143],0
ds_read2_b32 v[166:167],v14 offset0:146 offset1:147
M 52,v[92:95],136
R 196,2,2112
M 52,v[96:99],132
M 52,v[100:103],128
ds_read2_b32 v[168:169],v15 offset0:16 offset1:17
M 52,v[104:107],124
ds_read2_b32 v[170:171],v15 offset0:18 offset1:19
M 52,v[108:111],120
L 15
R 200,2,1600
v_mfma_f32_16x16x32_bf16 v[56:59],v[88:91],v[144:147],0
ds_read2_b32 v[172:173],v15 offset0:144 offset1:145
M 56,v[92:95],140
M 56,v[96:99],136
ds_read2_b32 v[174:175],v15 offset0:146 offset1:147
M 56,v[100:103],132
R 204,2,1088
M 56,v[104:107],128
ds_read2_b32 v[176:177],v16 offset0:16 offset1:17
M 56,v[108:111],124
ds_read2_b32 v[178:179],v16 offset0:18 offset1:19
M 56,v[112:115],120
L 15
v_mfma_f32_16x16x32_bf16 v[60:63],v[88:91],v[148:151],0
R 208,2,576
M 60,v[92:95],144
ds_read2_b32 v[180:181],v16 offset0:144 offset1:145
M 60,v[96:99],140
ds_read2_b32 v[182:183],v16 offset0:146 offset1:147
M 60,v[100:103],136
R 212,2,64
M 60,v[104:107],132
s_cmp_eq_u32 s83,0
s_cbranch_scc1 Lcv_c_%=
s_waitcnt vmcnt(0)
s_barrier
buffer_store_dword v23,v10,s[36:39],s48 offen offset:0 sc0 sc1
Lcv_c_%=:
M 60,v[108:111],128
M 60,v[112:115],124
M 60,v[116:119],120
L 15
M 40,v[152:155],192
ds_read2_b32 v[88:89],v1 offset0:32 offset1:33
M 40,v[156:159],188
ds_read2_b32 v[90:91],v1 offset0:34 offset1:35
M 40,v[160:163],184
L 14
R 120,2,3712
M 44,v[152:155],196
ds_read2_b32 v[92:93],v1 offset0:160 offset1:161
M 44,v[156:159],192
M 44,v[160:163],188
ds_read2_b32 v[94:95],v1 offset0:162 offset1:163
M 44,v[164:167],184
L 14
R 124,2,3200
M 48,v[152:155],200
ds_read2_b32 v[96:97],v14 offset0:32 offset1:33
M 48,v[156:159],196
ds_read2_b32 v[98:99],v14 offset0:34 offset1:35
M 48,v[160:163],192
M 48,v[164:167],188
R 128,2,2688
M 48,v[168:171],184
L 15
ds_read2_b32 v[100:101],v14 offset0:160 offset1:161
M 52,v[152:155],204
ds_read2_b32 v[102:103],v14 offset0:162 offset1:163
M 52,v[156:159],200
R 132,2,2176
M 52,v[160:163],196
M 52,v[164:167],192
ds_read2_b32 v[104:105],v15 offset0:32 offset1:33
M 52,v[168:171],188
ds_read2_b32 v[106:107],v15 offset0:34 offset1:35
M 52,v[172:175],184
L 15
R 136,2,1664
M 56,v[152:155],208
ds_read2_b32 v[108:109],v15 offset0:160 offset1:161
M 56,v[156:159],204
M 56,v[160:163],200
ds_read2_b32 v[110:111],v15 offset0:162 offset1:163
M 56,v[164:167],196
R 140,2,1152
M 56,v[168:171],192
ds_read2_b32 v[112:113],v16 offset0:32 offset1:33
M 56,v[172:175],188
ds_read2_b32 v[114:115],v16 offset0:34 offset1:35
M 56,v[176:179],184
L 15
M 60,v[152:155],212
R 144,2,640
M 60,v[156:159],208
ds_read2_b32 v[116:117],v16 offset0:160 offset1:161
M 60,v[160:163],204
ds_read2_b32 v[118:119],v16 offset0:162 offset1:163
M 60,v[164:167],200
R 148,2,128
M 60,v[168:171],196
M 60,v[172:175],192
M 60,v[176:179],188
M 60,v[180:183],184
L 15
M 40,v[88:91],128
ds_read2_b32 v[152:153],v1 offset0:48 offset1:49
M 40,v[92:95],124
ds_read2_b32 v[154:155],v1 offset0:50 offset1:51
M 40,v[96:99],120
L 14
R 184,2,3776
M 44,v[88:91],132
ds_read2_b32 v[156:157],v1 offset0:176 offset1:177
M 44,v[92:95],128
M 44,v[96:99],124
ds_read2_b32 v[158:159],v1 offset0:178 offset1:179
M 44,v[100:103],120
L 14
R 188,2,3264
M 48,v[88:91],136
ds_read2_b32 v[160:161],v14 offset0:48 offset1:49
M 48,v[92:95],132
ds_read2_b32 v[162:163],v14 offset0:50 offset1:51
M 48,v[96:99],128
M 48,v[100:103],124
R 192,2,2752
M 48,v[104:107],120
L 15
ds_read2_b32 v[164:165],v14 offset0:176 offset1:177
M 52,v[88:91],140
s_mov_b32 m0,163584
s_mov_b32 s78,0
ds_read2_b32 v[166:167],v14 offset0:178 offset1:179
M 52,v[92:95],136
R 196,2,2240
M 52,v[96:99],132
buffer_load_dword v11,s[40:43],s78 offen offset:0  lds
M 52,v[100:103],128
ds_read2_b32 v[168:169],v15 offset0:48 offset1:49
M 52,v[104:107],124
ds_read2_b32 v[170:171],v15 offset0:50 offset1:51
M 52,v[108:111],120
L 15
R 200,2,1728
M 56,v[88:91],144
ds_read2_b32 v[172:173],v15 offset0:176 offset1:177
M 56,v[92:95],140
M 56,v[96:99],136
ds_read2_b32 v[174:175],v15 offset0:178 offset1:179
M 56,v[100:103],132
R 204,2,1216
M 56,v[104:107],128
ds_read2_b32 v[176:177],v16 offset0:48 offset1:49
M 56,v[108:111],124
ds_read2_b32 v[178:179],v16 offset0:50 offset1:51
M 56,v[112:115],120
L 15
M 60,v[88:91],148
R 208,2,704
M 60,v[92:95],144
ds_read2_b32 v[180:181],v16 offset0:176 offset1:177
M 60,v[96:99],140
ds_read2_b32 v[182:183],v16 offset0:178 offset1:179
M 60,v[100:103],136
R 212,2,192
M 60,v[104:107],132
M 60,v[108:111],128
M 60,v[112:115],124
M 60,v[116:119],120
L 15
M 40,v[152:155],192
ds_read2_b32 v[88:89],v1 offset0:64 offset1:65
M 40,v[156:159],188
ds_read2_b32 v[90:91],v1 offset0:66 offset1:67
M 40,v[160:163],184
L 14
R 120,2,3840
M 44,v[152:155],196
ds_read2_b32 v[92:93],v1 offset0:192 offset1:193
M 44,v[156:159],192
M 44,v[160:163],188
ds_read2_b32 v[94:95],v1 offset0:194 offset1:195
M 44,v[164:167],184
L 14
R 124,2,3328
M 48,v[152:155],200
ds_read2_b32 v[96:97],v14 offset0:64 offset1:65
M 48,v[156:159],196
ds_read2_b32 v[98:99],v14 offset0:66 offset1:67
M 48,v[160:163],192
M 48,v[164:167],188
R 128,2,2816
M 48,v[168:171],184
L 15
ds_read2_b32 v[100:101],v14 offset0:192 offset1:193
M 52,v[152:155],204
ds_read2_b32 v[102:103],v14 offset0:194 offset1:195
M 52,v[156:159],200
R 132,2,2304
M 52,v[160:163],196
M 52,v[164:167],192
ds_read2_b32 v[104:105],v15 offset0:64 offset1:65
M 52,v[168:171],188
ds_read2_b32 v[106:107],v15 offset0:66 offset1:67
M 52,v[172:175],184
L 15
R 136,2,1792
M 56,v[152:155],208
ds_read2_b32 v[108:109],v15 offset0:192 offset1:193
M 56,v[156:159],204
M 56,v[160:163],200
ds_read2_b32 v[110:111],v15 offset0:194 offset1:195
M 56,v[164:167],196
R 140,2,1280
M 56,v[168:171],192
ds_read2_b32 v[112:113],v16 offset0:64 offset1:65
M 56,v[172:175],188
ds_read2_b32 v[114:115],v16 offset0:66 offset1:67
M 56,v[176:179],184
L 15
M 60,v[152:155],212
R 144,2,768
M 60,v[156:159],208
ds_read2_b32 v[116:117],v16 offset0:192 offset1:193
M 60,v[160:163],204
ds_read2_b32 v[118:119],v16 offset0:194 offset1:195
M 60,v[164:167],200
R 148,2,256
M 60,v[168:171],196
M 60,v[172:175],192
M 60,v[176:179],188
M 60,v[180:183],184
L 15
M 40,v[88:91],128
ds_read2_b32 v[152:153],v1 offset0:80 offset1:81
M 40,v[92:95],124
ds_read2_b32 v[154:155],v1 offset0:82 offset1:83
M 40,v[96:99],120
L 14
R 184,2,3904
M 44,v[88:91],132
ds_read2_b32 v[156:157],v1 offset0:208 offset1:209
M 44,v[92:95],128
M 44,v[96:99],124
ds_read2_b32 v[158:159],v1 offset0:210 offset1:211
M 44,v[100:103],120
L 14
R 188,2,3392
M 48,v[88:91],136
ds_read2_b32 v[160:161],v14 offset0:80 offset1:81
M 48,v[92:95],132
ds_read2_b32 v[162:163],v14 offset0:82 offset1:83
M 48,v[96:99],128
M 48,v[100:103],124
R 192,2,2880
M 48,v[104:107],120
L 15
ds_read2_b32 v[164:165],v14 offset0:208 offset1:209
M 52,v[88:91],140
ds_read2_b32 v[166:167],v14 offset0:210 offset1:211
M 52,v[92:95],136
R 196,2,2368
M 52,v[96:99],132
M 52,v[100:103],128
ds_read2_b32 v[168:169],v15 offset0:80 offset1:81
M 52,v[104:107],124
ds_read2_b32 v[170:171],v15 offset0:82 offset1:83
M 52,v[108:111],120
L 15
R 200,2,1856
M 56,v[88:91],144
ds_read2_b32 v[172:173],v15 offset0:208 offset1:209
M 56,v[92:95],140
M 56,v[96:99],136
ds_read2_b32 v[174:175],v15 offset0:210 offset1:211
M 56,v[100:103],132
R 204,2,1344
M 56,v[104:107],128
ds_read2_b32 v[176:177],v16 offset0:80 offset1:81
M 56,v[108:111],124
ds_read2_b32 v[178:179],v16 offset0:82 offset1:83
M 56,v[112:115],120
L 15
M 60,v[88:91],148
R 208,2,832
M 60,v[92:95],144
ds_read2_b32 v[180:181],v16 offset0:208 offset1:209
M 60,v[96:99],140
ds_read2_b32 v[182:183],v16 offset0:210 offset1:211
M 60,v[100:103],136
R 212,2,320
M 60,v[104:107],132
M 60,v[108:111],128
M 60,v[112:115],124
M 60,v[116:119],120
L 15
M 40,v[152:155],192
ds_read2_b32 v[88:89],v1 offset0:96 offset1:97
M 40,v[156:159],188
ds_read2_b32 v[90:91],v1 offset0:98 offset1:99
M 40,v[160:163],184
L 14
R 120,2,3968
M 44,v[152:155],196
s_mov_b32 m0,163584
s_mov_b32 s78,3741696
ds_read2_b32 v[92:93],v1 offset0:224 offset1:225
M 44,v[156:159],192
M 44,v[160:163],188
ds_read2_b32 v[94:95],v1 offset0:226 offset1:227
M 44,v[164:167],184
L 14
R 124,2,3456
M 48,v[152:155],200
ds_read2_b32 v[96:97],v14 offset0:96 offset1:97
M 48,v[156:159],196
ds_read2_b32 v[98:99],v14 offset0:98 offset1:99
M 48,v[160:163],192
buffer_load_dword v11,s[40:43],s78 offen offset:0  lds
M 48,v[164:167],188
R 128,2,2944
M 48,v[168:171],184
L 15
ds_read2_b32 v[100:101],v14 offset0:224 offset1:225
M 52,v[152:155],204
ds_read2_b32 v[102:103],v14 offset0:226 offset1:227
M 52,v[156:159],200
R 132,2,2432
M 52,v[160:163],196
M 52,v[164:167],192
ds_read2_b32 v[104:105],v15 offset0:96 offset1:97
M 52,v[168:171],188
ds_read2_b32 v[106:107],v15 offset0:98 offset1:99
M 52,v[172:175],184
L 15
R 136,2,1920
M 56,v[152:155],208
ds_read2_b32 v[108:109],v15 offset0:224 offset1:225
M 56,v[156:159],204
M 56,v[160:163],200
ds_read2_b32 v[110:111],v15 offset0:226 offset1:227
M 56,v[164:167],196
R 140,2,1408
M 56,v[168:171],192
ds_read2_b32 v[112:113],v16 offset0:96 offset1:97
M 56,v[172:175],188
ds_read2_b32 v[114:115],v16 offset0:98 offset1:99
M 56,v[176:179],184
L 15
M 60,v[152:155],212
R 144,2,896
M 60,v[156:159],208
ds_read2_b32 v[116:117],v16 offset0:224 offset1:225
M 60,v[160:163],204
ds_read2_b32 v[118:119],v16 offset0:226 offset1:227
M 60,v[164:167],200
R 148,2,384
M 60,v[168:171],196
M 60,v[172:175],192
M 60,v[176:179],188
M 60,v[180:183],184
L 15
M 40,v[88:91],128
ds_read2_b32 v[152:153],v1 offset0:112 offset1:113
M 40,v[92:95],124
ds_read2_b32 v[154:155],v1 offset0:114 offset1:115
M 40,v[96:99],120
L 14
R 184,2,4032
M 44,v[88:91],132
ds_read2_b32 v[156:157],v1 offset0:240 offset1:241
M 44,v[92:95],128
M 44,v[96:99],124
ds_read2_b32 v[158:159],v1 offset0:242 offset1:243
v_add_u32 v1,12928,v1
M 44,v[100:103],120
L 14
R 188,2,3520
M 48,v[88:91],136
ds_read2_b32 v[160:161],v14 offset0:112 offset1:113
M 48,v[92:95],132
ds_read2_b32 v[162:163],v14 offset0:114 offset1:115
M 48,v[96:99],128
M 48,v[100:103],124
R 192,2,3008
M 48,v[104:107],120
L 15
ds_read2_b32 v[164:165],v14 offset0:240 offset1:241
M 52,v[88:91],140
ds_read2_b32 v[166:167],v14 offset0:242 offset1:243
v_add_u32 v14,12928,v14
M 52,v[92:95],136
R 196,2,2496
M 52,v[96:99],132
M 52,v[100:103],128
ds_read2_b32 v[168:169],v15 offset0:112 offset1:113
M 52,v[104:107],124
ds_read2_b32 v[170:171],v15 offset0:114 offset1:115
M 52,v[108:111],120
L 15
R 200,2,1984
M 56,v[88:91],144
ds_read2_b32 v[172:173],v15 offset0:240 offset1:241
M 56,v[92:95],140
M 56,v[96:99],136
ds_read2_b32 v[174:175],v15 offset0:242 offset1:243
v_add_u32 v15,12928,v15
M 56,v[100:103],132
R 204,2,1472
M 56,v[104:107],128
ds_read2_b32 v[176:177],v16 offset0:112 offset1:113
M 56,v[108:111],124
ds_read2_b32 v[178:179],v16 offset0:114 offset1:115
M 56,v[112:115],120
L 15
M 60,v[88:91],148
R 208,2,960
M 60,v[92:95],144
ds_read2_b32 v[180:181],v16 offset0:240 offset1:241
M 60,v[96:99],140
ds_read2_b32 v[182:183],v16 offset0:242 offset1:243
v_add_u32 v16,12928,v16
M 60,v[100:103],136
)"
R"(R 212,2,448
M 60,v[104:107],132
M 60,v[108:111],128
M 60,v[112:115],124
M 60,v[116:119],120
L 15
M 40,v[152:155],192
ds_read2_b32 v[88:89],v1 offset0:0 offset1:1
M 40,v[156:159],188
ds_read2_b32 v[90:91],v1 offset0:2 offset1:3
M 40,v[160:163],184
L 14
R 120,2,16512
M 44,v[152:155],196
ds_read2_b32 v[92:93],v1 offset0:128 offset1:129
M 44,v[156:159],192
M 44,v[160:163],188
ds_read2_b32 v[94:95],v1 offset0:130 offset1:131
M 44,v[164:167],184
L 14
R 124,2,16000
M 48,v[152:155],200
ds_read2_b32 v[96:97],v14 offset0:0 offset1:1
M 48,v[156:159],196
ds_read2_b32 v[98:99],v14 offset0:2 offset1:3
M 48,v[160:163],192
M 48,v[164:167],188
R 128,2,15488
M 48,v[168:171],184
L 15
ds_read2_b32 v[100:101],v14 offset0:128 offset1:129
M 52,v[152:155],204
ds_read2_b32 v[102:103],v14 offset0:130 offset1:131
M 52,v[156:159],200
R 132,2,14976
M 52,v[160:163],196
M 52,v[164:167],192
ds_read2_b32 v[104:105],v15 offset0:0 offset1:1
M 52,v[168:171],188
ds_read2_b32 v[106:107],v15 offset0:2 offset1:3
M 52,v[172:175],184
L 15
R 136,2,14464
M 56,v[152:155],208
ds_read2_b32 v[108:109],v15 offset0:128 offset1:129
M 56,v[156:159],204
M 56,v[160:163],200
ds_read2_b32 v[110:111],v15 offset0:130 offset1:131
M 56,v[164:167],196
R 140,2,13952
M 56,v[168:171],192
ds_read2_b32 v[112:113],v16 offset0:0 offset1:1
M 56,v[172:175],188
ds_read2_b32 v[114:115],v16 offset0:2 offset1:3
M 56,v[176:179],184
L 15
M 60,v[152:155],212
R 144,2,13440
M 60,v[156:159],208
ds_read2_b32 v[116:117],v16 offset0:128 offset1:129
M 60,v[160:163],204
ds_read2_b32 v[118:119],v16 offset0:130 offset1:131
M 60,v[164:167],200
R 148,2,12928
M 60,v[168:171],196
M 60,v[172:175],192
M 60,v[176:179],188
M 60,v[180:183],184
s_cmp_eq_u32 s51,2
s_cbranch_scc1 Lcv_d_%=
ds_read_b64 v[24:25],v3 offset:2560
ds_read_b64 v[26:27],v3 offset:2048
ds_read_b64 v[28:29],v3 offset:1536
ds_read_b64 v[30:31],v3 offset:1024
ds_read_b64 v[32:33],v3 offset:512
ds_read_b64 v[34:35],v3 offset:0
L 0
v_and_b32 v36,0xffff0000,v25
v_lshlrev_b32 v37,16,v25
v_and_b32 v38,0xffff0000,v24
v_lshlrev_b32 v39,16,v24
v_fma_f32 v40,v36,s86,v40
v_fma_f32 v41,v37,s86,v41
v_fma_f32 v42,v38,s86,v42
v_fma_f32 v43,v39,s86,v43
v_cvt_pk_bf16_f32 v40,v40,v41
v_cvt_pk_bf16_f32 v41,v42,v43
ds_write_b64 v4,v[40:41] offset:0
v_and_b32 v36,0xffff0000,v27
v_lshlrev_b32 v37,16,v27
v_and_b32 v38,0xffff0000,v26
v_lshlrev_b32 v39,16,v26
v_fma_f32 v44,v36,s86,v44
v_fma_f32 v45,v37,s86,v45
v_fma_f32 v46,v38,s86,v46
v_fma_f32 v47,v39,s86,v47
v_cvt_pk_bf16_f32 v44,v44,v45
v_cvt_pk_bf16_f32 v45,v46,v47
ds_write_b64 v4,v[44:45] offset:512
v_and_b32 v36,0xffff0000,v29
v_lshlrev_b32 v37,16,v29
v_and_b32 v38,0xffff0000,v28
v_lshlrev_b32 v39,16,v28
v_fma_f32 v48,v36,s86,v48
v_fma_f32 v49,v37,s86,v49
v_fma_f32 v50,v38,s86,v50
v_fma_f32 v51,v39,s86,v51
v_cvt_pk_bf16_f32 v48,v48,v49
v_cvt_pk_bf16_f32 v49,v50,v51
ds_write_b64 v4,v[48:49] offset:1024
v_and_b32 v36,0xffff0000,v31
v_lshlrev_b32 v37,16,v31
v_and_b32 v38,0xffff0000,v30
v_lshlrev_b32 v39,16,v30
v_fma_f32 v52,v36,s86,v52
v_fma_f32 v53,v37,s86,v53
v_fma_f32 v54,v38,s86,v54
v_fma_f32 v55,v39,s86,v55
v_cvt_pk_bf16_f32 v52,v52,v53
v_cvt_pk_bf16_f32 v53,v54,v55
ds_write_b64 v4,v[52:53] offset:1536
v_and_b32 v36,0xffff0000,v33
v_lshlrev_b32 v37,16,v33
v_and_b32 v38,0xffff0000,v32
v_lshlrev_b32 v39,16,v32
v_fma_f32 v56,v36,s86,v56
v_fma_f32 v57,v37,s86,v57
v_fma_f32 v58,v38,s86,v58
v_fma_f32 v59,v39,s86,v59
v_cvt_pk_bf16_f32 v56,v56,v57
v_cvt_pk_bf16_f32 v57,v58,v59
ds_write_b64 v4,v[56:57] offset:2048
v_and_b32 v36,0xffff0000,v35
v_lshlrev_b32 v37,16,v35
v_and_b32 v38,0xffff0000,v34
v_lshlrev_b32 v39,16,v34
v_fma_f32 v60,v36,s86,v60
v_fma_f32 v61,v37,s86,v61
v_fma_f32 v62,v38,s86,v62
v_fma_f32 v63,v39,s86,v63
v_cvt_pk_bf16_f32 v60,v60,v61
v_cvt_pk_bf16_f32 v61,v62,v63
ds_write_b64 v4,v[60:61] offset:2560
s_branch Lcv_e_%=
Lcv_d_%=:
L 15
v_mfma_f32_16x16x32_bf16 v[64:67],v[88:91],v[128:131],0
ds_read2_b32 v[152:153],v1 offset0:16 offset1:17
ds_read_b64 v[24:25],v3 offset:2560
M 64,v[92:95],124
ds_read2_b32 v[154:155],v1 offset0:18 offset1:19
ds_read_b64 v[26:27],v3 offset:2048
M 64,v[96:99],120
L 15
R 184,2,16576
ds_read_b64 v[28:29],v3 offset:1536
v_mfma_f32_16x16x32_bf16 v[68:71],v[88:91],v[132:135],0
ds_read2_b32 v[156:157],v1 offset0:144 offset1:145
ds_read_b64 v[30:31],v3 offset:1024
M 68,v[92:95],128
ds_read_b64 v[32:33],v3 offset:512
ds_read_b64 v[34:35],v3 offset:0
M 68,v[96:99],124
ds_read2_b32 v[158:159],v1 offset0:146 offset1:147
M 68,v[100:103],120
L 15
R 188,2,16064
v_mfma_f32_16x16x32_bf16 v[72:75],v[88:91],v[136:139],0
ds_read2_b32 v[160:161],v14 offset0:16 offset1:17
M 72,v[92:95],132
s_mov_b32 m0,163584
s_mov_b32 s78,0
ds_read2_b32 v[162:163],v14 offset0:18 offset1:19
M 72,v[96:99],128
M 72,v[100:103],124
L 4
R 192,2,15552
v_and_b32 v36,0xffff0000,v25
M 72,v[104:107],120
s_nop 0
ds_read2_b32 v[164:165],v14 offset0:144 offset1:145
v_lshlrev_b32 v37,16,v25
v_mfma_f32_16x16x32_bf16 v[76:79],v[88:91],v[140:143],0
ds_read2_b32 v[166:167],v14 offset0:146 offset1:147
v_and_b32 v38,0xffff0000,v24
M 76,v[92:95],136
R 196,2,15040
v_lshlrev_b32 v39,16,v24
M 76,v[96:99],132
buffer_load_dword v12,s[60:63],s78 offen offset:0  lds
M 76,v[100:103],128
ds_read2_b32 v[168:169],v15 offset0:16 offset1:17
v_fma_f32 v40,v36,s86,v40
M 76,v[104:107],124
ds_read2_b32 v[170:171],v15 offset0:18 offset1:19
v_fma_f32 v41,v37,s86,v41
M 76,v[108:111],120
s_nop 0
R 200,2,14528
v_fma_f32 v42,v38,s86,v42
v_mfma_f32_16x16x32_bf16 v[80:83],v[88:91],v[144:147],0
ds_read2_b32 v[172:173],v15 offset0:144 offset1:145
v_fma_f32 v43,v39,s86,v43
M 80,v[92:95],140
v_cvt_pk_bf16_f32 v40,v40,v41
v_cvt_pk_bf16_f32 v41,v42,v43
M 80,v[96:99],136
ds_read2_b32 v[174:175],v15 offset0:146 offset1:147
v_and_b32 v36,0xffff0000,v27
M 80,v[100:103],132
R 204,2,14016
v_lshlrev_b32 v37,16,v27
M 80,v[104:107],128
ds_read2_b32 v[176:177],v16 offset0:16 offset1:17
v_and_b32 v38,0xffff0000,v26
M 80,v[108:111],124
ds_read2_b32 v[178:179],v16 offset0:18 offset1:19
v_lshlrev_b32 v39,16,v26
M 80,v[112:115],120
s_nop 0
ds_write_b64 v4,v[40:41] offset:0
v_mfma_f32_16x16x32_bf16 v[84:87],v[88:91],v[148:151],0
R 208,2,13504
v_fma_f32 v44,v36,s86,v44
M 84,v[92:95],144
ds_read2_b32 v[180:181],v16 offset0:144 offset1:145
v_fma_f32 v45,v37,s86,v45
M 84,v[96:99],140
ds_read2_b32 v[182:183],v16 offset0:146 offset1:147
v_fma_f32 v46,v38,s86,v46
M 84,v[100:103],136
R 212,2,12992
v_fma_f32 v47,v39,s86,v47
M 84,v[104:107],132
v_cvt_pk_bf16_f32 v44,v44,v45
v_cvt_pk_bf16_f32 v45,v46,v47
M 84,v[108:111],128
ds_write_b64 v4,v[44:45] offset:512
M 84,v[112:115],124
v_and_b32 v36,0xffff0000,v29
v_lshlrev_b32 v37,16,v29
M 84,v[116:119],120
L 15
v_and_b32 v38,0xffff0000,v28
v_lshlrev_b32 v39,16,v28
M 64,v[152:155],192
ds_read2_b32 v[88:89],v1 offset0:32 offset1:33
v_fma_f32 v48,v36,s86,v48
M 64,v[156:159],188
ds_read2_b32 v[90:91],v1 offset0:34 offset1:35
v_fma_f32 v49,v37,s86,v49
M 64,v[160:163],184
L 15
R 120,2,16640
v_fma_f32 v50,v38,s86,v50
M 68,v[152:155],196
ds_read2_b32 v[92:93],v1 offset0:160 offset1:161
v_fma_f32 v51,v39,s86,v51
M 68,v[156:159],192
v_cvt_pk_bf16_f32 v48,v48,v49
v_cvt_pk_bf16_f32 v49,v50,v51
M 68,v[160:163],188
ds_read2_b32 v[94:95],v1 offset0:162 offset1:163
v_and_b32 v36,0xffff0000,v31
M 68,v[164:167],184
L 15
R 124,2,16128
v_lshlrev_b32 v37,16,v31
M 72,v[152:155],200
ds_read2_b32 v[96:97],v14 offset0:32 offset1:33
v_and_b32 v38,0xffff0000,v30
M 72,v[156:159],196
ds_read2_b32 v[98:99],v14 offset0:34 offset1:35
v_lshlrev_b32 v39,16,v30
M 72,v[160:163],192
ds_write_b64 v4,v[48:49] offset:1024
M 72,v[164:167],188
R 128,2,15616
v_fma_f32 v52,v36,s86,v52
M 72,v[168:171],184
L 15
ds_read2_b32 v[100:101],v14 offset0:160 offset1:161
v_fma_f32 v53,v37,s86,v53
M 76,v[152:155],204
ds_read2_b32 v[102:103],v14 offset0:162 offset1:163
v_fma_f32 v54,v38,s86,v54
M 76,v[156:159],200
R 132,2,15104
v_fma_f32 v55,v39,s86,v55
M 76,v[160:163],196
v_cvt_pk_bf16_f32 v52,v52,v53
v_cvt_pk_bf16_f32 v53,v54,v55
M 76,v[164:167],192
ds_read2_b32 v[104:105],v15 offset0:32 offset1:33
v_and_b32 v36,0xffff0000,v33
M 76,v[168:171],188
ds_read2_b32 v[106:107],v15 offset0:34 offset1:35
v_lshlrev_b32 v37,16,v33
M 76,v[172:175],184
L 15
R 136,2,14592
v_and_b32 v38,0xffff0000,v32
M 80,v[152:155],208
ds_read2_b32 v[108:109],v15 offset0:160 offset1:161
v_lshlrev_b32 v39,16,v32
M 80,v[156:159],204
ds_write_b64 v4,v[52:53] offset:1536
M 80,v[160:163],200
ds_read2_b32 v[110:111],v15 offset0:162 offset1:163
v_fma_f32 v56,v36,s86,v56
M 80,v[164:167],196
R 140,2,14080
v_fma_f32 v57,v37,s86,v57
M 80,v[168:171],192
ds_read2_b32 v[112:113],v16 offset0:32 offset1:33
v_fma_f32 v58,v38,s86,v58
M 80,v[172:175],188
ds_read2_b32 v[114:115],v16 offset0:34 offset1:35
v_fma_f32 v59,v39,s86,v59
M 80,v[176:179],184
L 15
v_cvt_pk_bf16_f32 v56,v56,v57
v_cvt_pk_bf16_f32 v57,v58,v59
M 84,v[152:155],212
R 144,2,13568
v_and_b32 v36,0xffff0000,v35
M 84,v[156:159],208
ds_read2_b32 v[116:117],v16 offset0:160 offset1:161
v_lshlrev_b32 v37,16,v35
M 84,v[160:163],204
ds_read2_b32 v[118:119],v16 offset0:162 offset1:163
v_and_b32 v38,0xffff0000,v34
M 84,v[164:167],200
R 148,2,13056
v_lshlrev_b32 v39,16,v34
M 84,v[168:171],196
ds_write_b64 v4,v[56:57] offset:2048
M 84,v[172:175],192
v_fma_f32 v60,v36,s86,v60
v_fma_f32 v61,v37,s86,v61
M 84,v[176:179],188
v_fma_f32 v62,v38,s86,v62
v_fma_f32 v63,v39,s86,v63
M 84,v[180:183],184
L 15
v_cvt_pk_bf16_f32 v60,v60,v61
v_cvt_pk_bf16_f32 v61,v62,v63
M 64,v[88:91],128
ds_read2_b32 v[152:153],v1 offset0:48 offset1:49
M 64,v[92:95],124
ds_read2_b32 v[154:155],v1 offset0:50 offset1:51
M 64,v[96:99],120
L 15
R 184,2,16704
M 68,v[88:91],132
ds_read2_b32 v[156:157],v1 offset0:176 offset1:177
M 68,v[92:95],128
ds_write_b64 v4,v[60:61] offset:2560
M 68,v[96:99],124
ds_read2_b32 v[158:159],v1 offset0:178 offset1:179
M 68,v[100:103],120
L 15
R 188,2,16192
M 72,v[88:91],136
ds_read2_b32 v[160:161],v14 offset0:48 offset1:49
M 72,v[92:95],132
ds_read2_b32 v[162:163],v14 offset0:50 offset1:51
M 72,v[96:99],128
M 72,v[100:103],124
R 192,2,15680
M 72,v[104:107],120
L 15
ds_read2_b32 v[164:165],v14 offset0:176 offset1:177
M 76,v[88:91],140
ds_read2_b32 v[166:167],v14 offset0:178 offset1:179
M 76,v[92:95],136
R 196,2,15168
M 76,v[96:99],132
M 76,v[100:103],128
ds_read2_b32 v[168:169],v15 offset0:48 offset1:49
M 76,v[104:107],124
ds_read2_b32 v[170:171],v15 offset0:50 offset1:51
M 76,v[108:111],120
L 15
R 200,2,14656
M 80,v[88:91],144
ds_read2_b32 v[172:173],v15 offset0:176 offset1:177
M 80,v[92:95],140
M 80,v[96:99],136
ds_read2_b32 v[174:175],v15 offset0:178 offset1:179
M 80,v[100:103],132
R 204,2,14144
M 80,v[104:107],128
s_mov_b32 m0,163584
s_mov_b32 s78,1847296
ds_read2_b32 v[176:177],v16 offset0:48 offset1:49
M 80,v[108:111],124
ds_read2_b32 v[178:179],v16 offset0:50 offset1:51
M 80,v[112:115],120
L 15
buffer_load_dword v12,s[60:63],s78 offen offset:0  lds
M 84,v[88:91],148
R 208,2,13632
M 84,v[92:95],144
ds_read2_b32 v[180:181],v16 offset0:176 offset1:177
M 84,v[96:99],140
ds_read2_b32 v[182:183],v16 offset0:178 offset1:179
M 84,v[100:103],136
R 212,2,13120
M 84,v[104:107],132
M 84,v[108:111],128
M 84,v[112:115],124
M 84,v[116:119],120
L 15
M 64,v[152:155],192
ds_read2_b32 v[88:89],v1 offset0:64 offset1:65
M 64,v[156:159],188
ds_read2_b32 v[90:91],v1 offset0:66 offset1:67
M 64,v[160:163],184
L 14
R 120,2,16768
M 68,v[152:155],196
ds_read2_b32 v[92:93],v1 offset0:192 offset1:193
M 68,v[156:159],192
M 68,v[160:163],188
ds_read2_b32 v[94:95],v1 offset0:194 offset1:195
M 68,v[164:167],184
L 14
R 124,2,16256
M 72,v[152:155],200
ds_read2_b32 v[96:97],v14 offset0:64 offset1:65
M 72,v[156:159],196
ds_read2_b32 v[98:99],v14 offset0:66 offset1:67
M 72,v[160:163],192
M 72,v[164:167],188
R 128,2,15744
M 72,v[168:171],184
L 15
ds_read2_b32 v[100:101],v14 offset0:192 offset1:193
M 76,v[152:155],204
ds_read2_b32 v[102:103],v14 offset0:194 offset1:195
M 76,v[156:159],200
R 132,2,15232
M 76,v[160:163],196
M 76,v[164:167],192
ds_read2_b32 v[104:105],v15 offset0:64 offset1:65
M 76,v[168:171],188
ds_read2_b32 v[106:107],v15 offset0:66 offset1:67
M 76,v[172:175],184
L 15
R 136,2,14720
M 80,v[152:155],208
ds_read2_b32 v[108:109],v15 offset0:192 offset1:193
M 80,v[156:159],204
M 80,v[160:163],200
ds_read2_b32 v[110:111],v15 offset0:194 offset1:195
M 80,v[164:167],196
R 140,2,14208
M 80,v[168:171],192
ds_read2_b32 v[112:113],v16 offset0:64 offset1:65
M 80,v[172:175],188
ds_read2_b32 v[114:115],v16 offset0:66 offset1:67
M 80,v[176:179],184
L 15
M 84,v[152:155],212
R 144,2,13696
M 84,v[156:159],208
ds_read2_b32 v[116:117],v16 offset0:192 offset1:193
M 84,v[160:163],204
ds_read2_b32 v[118:119],v16 offset0:194 offset1:195
M 84,v[164:167],200
R 148,2,13184
M 84,v[168:171],196
M 84,v[172:175],192
M 84,v[176:179],188
M 84,v[180:183],184
L 15
M 64,v[88:91],128
ds_read2_b32 v[152:153],v1 offset0:80 offset1:81
M 64,v[92:95],124
ds_read2_b32 v[154:155],v1 offset0:82 offset1:83
M 64,v[96:99],120
L 14
R 184,2,16832
M 68,v[88:91],132
ds_read2_b32 v[156:157],v1 offset0:208 offset1:209
M 68,v[92:95],128
M 68,v[96:99],124
ds_read2_b32 v[158:159],v1 offset0:210 offset1:211
M 68,v[100:103],120
L 14
R 188,2,16320
M 72,v[88:91],136
ds_read2_b32 v[160:161],v14 offset0:80 offset1:81
M 72,v[92:95],132
ds_read2_b32 v[162:163],v14 offset0:82 offset1:83
M 72,v[96:99],128
M 72,v[100:103],124
R 192,2,15808
M 72,v[104:107],120
)"
R"(L 15
ds_read2_b32 v[164:165],v14 offset0:208 offset1:209
M 76,v[88:91],140
ds_read2_b32 v[166:167],v14 offset0:210 offset1:211
M 76,v[92:95],136
R 196,2,15296
M 76,v[96:99],132
M 76,v[100:103],128
ds_read2_b32 v[168:169],v15 offset0:80 offset1:81
M 76,v[104:107],124
ds_read2_b32 v[170:171],v15 offset0:82 offset1:83
M 76,v[108:111],120
L 15
R 200,2,14784
M 80,v[88:91],144
ds_read2_b32 v[172:173],v15 offset0:208 offset1:209
M 80,v[92:95],140
M 80,v[96:99],136
ds_read2_b32 v[174:175],v15 offset0:210 offset1:211
M 80,v[100:103],132
R 204,2,14272
M 80,v[104:107],128
ds_read2_b32 v[176:177],v16 offset0:80 offset1:81
M 80,v[108:111],124
ds_read2_b32 v[178:179],v16 offset0:82 offset1:83
M 80,v[112:115],120
L 15
M 84,v[88:91],148
R 208,2,13760
M 84,v[92:95],144
ds_read2_b32 v[180:181],v16 offset0:208 offset1:209
M 84,v[96:99],140
ds_read2_b32 v[182:183],v16 offset0:210 offset1:211
M 84,v[100:103],136
R 212,2,13248
M 84,v[104:107],132
M 84,v[108:111],128
M 84,v[112:115],124
M 84,v[116:119],120
L 15
M 64,v[152:155],192
ds_read2_b32 v[88:89],v1 offset0:96 offset1:97
M 64,v[156:159],188
ds_read2_b32 v[90:91],v1 offset0:98 offset1:99
M 64,v[160:163],184
L 14
R 120,2,16896
M 68,v[152:155],196
ds_read2_b32 v[92:93],v1 offset0:224 offset1:225
M 68,v[156:159],192
M 68,v[160:163],188
ds_read2_b32 v[94:95],v1 offset0:226 offset1:227
M 68,v[164:167],184
L 14
R 124,2,16384
M 72,v[152:155],200
ds_read2_b32 v[96:97],v14 offset0:96 offset1:97
M 72,v[156:159],196
ds_read2_b32 v[98:99],v14 offset0:98 offset1:99
M 72,v[160:163],192
M 72,v[164:167],188
R 128,2,15872
M 72,v[168:171],184
L 15
ds_read2_b32 v[100:101],v14 offset0:224 offset1:225
M 76,v[152:155],204
ds_read2_b32 v[102:103],v14 offset0:226 offset1:227
M 76,v[156:159],200
R 132,2,15360
M 76,v[160:163],196
M 76,v[164:167],192
ds_read2_b32 v[104:105],v15 offset0:96 offset1:97
M 76,v[168:171],188
ds_read2_b32 v[106:107],v15 offset0:98 offset1:99
M 76,v[172:175],184
L 15
R 136,2,14848
M 80,v[152:155],208
ds_read2_b32 v[108:109],v15 offset0:224 offset1:225
M 80,v[156:159],204
M 80,v[160:163],200
ds_read2_b32 v[110:111],v15 offset0:226 offset1:227
M 80,v[164:167],196
R 140,2,14336
M 80,v[168:171],192
ds_read2_b32 v[112:113],v16 offset0:96 offset1:97
M 80,v[172:175],188
ds_read2_b32 v[114:115],v16 offset0:98 offset1:99
M 80,v[176:179],184
L 15
M 84,v[152:155],212
R 144,2,13824
M 84,v[156:159],208
ds_read2_b32 v[116:117],v16 offset0:224 offset1:225
M 84,v[160:163],204
ds_read2_b32 v[118:119],v16 offset0:226 offset1:227
M 84,v[164:167],200
R 148,2,13312
M 84,v[168:171],196
M 84,v[172:175],192
M 84,v[176:179],188
M 84,v[180:183],184
L 15
M 64,v[88:91],128
ds_read2_b32 v[152:153],v1 offset0:112 offset1:113
M 64,v[92:95],124
ds_read2_b32 v[154:155],v1 offset0:114 offset1:115
M 64,v[96:99],120
L 14
R 184,2,16960
M 68,v[88:91],132
ds_read2_b32 v[156:157],v1 offset0:240 offset1:241
M 68,v[92:95],128
M 68,v[96:99],124
ds_read2_b32 v[158:159],v1 offset0:242 offset1:243
M 68,v[100:103],120
L 14
R 188,2,16448
M 72,v[88:91],136
ds_read2_b32 v[160:161],v14 offset0:112 offset1:113
M 72,v[92:95],132
ds_read2_b32 v[162:163],v14 offset0:114 offset1:115
M 72,v[96:99],128
M 72,v[100:103],124
R 192,2,15936
M 72,v[104:107],120
L 15
ds_read2_b32 v[164:165],v14 offset0:240 offset1:241
M 76,v[88:91],140
ds_read2_b32 v[166:167],v14 offset0:242 offset1:243
M 76,v[92:95],136
R 196,2,15424
M 76,v[96:99],132
M 76,v[100:103],128
ds_read2_b32 v[168:169],v15 offset0:112 offset1:113
M 76,v[104:107],124
ds_read2_b32 v[170:171],v15 offset0:114 offset1:115
M 76,v[108:111],120
L 15
R 200,2,14912
M 80,v[88:91],144
ds_read2_b32 v[172:173],v15 offset0:240 offset1:241
M 80,v[92:95],140
M 80,v[96:99],136
ds_read2_b32 v[174:175],v15 offset0:242 offset1:243
M 80,v[100:103],132
R 204,2,14400
M 80,v[104:107],128
ds_read2_b32 v[176:177],v16 offset0:112 offset1:113
M 80,v[108:111],124
ds_read2_b32 v[178:179],v16 offset0:114 offset1:115
M 80,v[112:115],120
L 15
M 84,v[88:91],148
R 208,2,13888
M 84,v[92:95],144
ds_read2_b32 v[180:181],v16 offset0:240 offset1:241
M 84,v[96:99],140
ds_read2_b32 v[182:183],v16 offset0:242 offset1:243
M 84,v[100:103],136
R 212,2,13376
M 84,v[104:107],132
M 84,v[108:111],128
M 84,v[112:115],124
M 84,v[116:119],120
L 15
M 64,v[152:155],192
M 64,v[156:159],188
M 64,v[160:163],184
L 12
M 68,v[152:155],196
M 68,v[156:159],192
M 68,v[160:163],188
M 68,v[164:167],184
L 9
M 72,v[152:155],200
M 72,v[156:159],196
M 72,v[160:163],192
ds_read_b64 v[24:25],v3 offset:15488
ds_read_b64 v[26:27],v3 offset:14976
M 72,v[164:167],188
ds_read_b64 v[28:29],v3 offset:14464
ds_read_b64 v[30:31],v3 offset:13952
M 72,v[168:171],184
L 10
ds_read_b64 v[32:33],v3 offset:13440
ds_read_b64 v[34:35],v3 offset:12928
M 76,v[152:155],204
M 76,v[156:159],200
M 76,v[160:163],196
M 76,v[164:167],192
M 76,v[168:171],188
M 76,v[172:175],184
L 9
M 80,v[152:155],208
M 80,v[156:159],204
M 80,v[160:163],200
M 80,v[164:167],196
M 80,v[168:171],192
M 80,v[172:175],188
M 80,v[176:179],184
L 6
M 84,v[152:155],212
M 84,v[156:159],208
M 84,v[160:163],204
M 84,v[164:167],200
M 84,v[168:171],196
M 84,v[172:175],192
M 84,v[176:179],188
M 84,v[180:183],184
L 0
v_and_b32 v36,0xffff0000,v25
v_lshlrev_b32 v37,16,v25
v_and_b32 v38,0xffff0000,v24
v_lshlrev_b32 v39,16,v24
v_fma_f32 v64,v36,s87,v64
v_fma_f32 v65,v37,s87,v65
v_fma_f32 v66,v38,s87,v66
v_fma_f32 v67,v39,s87,v67
v_cvt_pk_bf16_f32 v64,v64,v65
v_cvt_pk_bf16_f32 v65,v66,v67
ds_write_b64 v4,v[64:65] offset:12928
v_and_b32 v36,0xffff0000,v27
v_lshlrev_b32 v37,16,v27
v_and_b32 v38,0xffff0000,v26
v_lshlrev_b32 v39,16,v26
v_fma_f32 v68,v36,s87,v68
v_fma_f32 v69,v37,s87,v69
v_fma_f32 v70,v38,s87,v70
v_fma_f32 v71,v39,s87,v71
v_cvt_pk_bf16_f32 v68,v68,v69
v_cvt_pk_bf16_f32 v69,v70,v71
ds_write_b64 v4,v[68:69] offset:13440
v_and_b32 v36,0xffff0000,v29
v_lshlrev_b32 v37,16,v29
v_and_b32 v38,0xffff0000,v28
v_lshlrev_b32 v39,16,v28
v_fma_f32 v72,v36,s87,v72
v_fma_f32 v73,v37,s87,v73
v_fma_f32 v74,v38,s87,v74
v_fma_f32 v75,v39,s87,v75
v_cvt_pk_bf16_f32 v72,v72,v73
v_cvt_pk_bf16_f32 v73,v74,v75
ds_write_b64 v4,v[72:73] offset:13952
v_and_b32 v36,0xffff0000,v31
v_lshlrev_b32 v37,16,v31
v_and_b32 v38,0xffff0000,v30
v_lshlrev_b32 v39,16,v30
v_fma_f32 v76,v36,s87,v76
v_fma_f32 v77,v37,s87,v77
v_fma_f32 v78,v38,s87,v78
v_fma_f32 v79,v39,s87,v79
v_cvt_pk_bf16_f32 v76,v76,v77
v_cvt_pk_bf16_f32 v77,v78,v79
ds_write_b64 v4,v[76:77] offset:14464
v_and_b32 v36,0xffff0000,v33
v_lshlrev_b32 v37,16,v33
v_and_b32 v38,0xffff0000,v32
v_lshlrev_b32 v39,16,v32
v_fma_f32 v80,v36,s87,v80
v_fma_f32 v81,v37,s87,v81
v_fma_f32 v82,v38,s87,v82
v_fma_f32 v83,v39,s87,v83
v_cvt_pk_bf16_f32 v80,v80,v81
v_cvt_pk_bf16_f32 v81,v82,v83
ds_write_b64 v4,v[80:81] offset:14976
v_and_b32 v36,0xffff0000,v35
v_lshlrev_b32 v37,16,v35
v_and_b32 v38,0xffff0000,v34
v_lshlrev_b32 v39,16,v34
v_fma_f32 v84,v36,s87,v84
v_fma_f32 v85,v37,s87,v85
v_fma_f32 v86,v38,s87,v86
v_fma_f32 v87,v39,s87,v87
v_cvt_pk_bf16_f32 v84,v84,v85
v_cvt_pk_bf16_f32 v85,v86,v87
ds_write_b64 v4,v[84:85] offset:15488
Lcv_e_%=:
L 0
s_barrier
R 152,5,0
R 156,5,512
R 160,5,1024
R 164,5,1536
R 168,5,2048
R 172,5,2560
s_mov_b32 s78,99614720
L 5
buffer_store_dwordx4 v[152:155],v7,s[36:39],s78 offen offset:0 sc0 sc1
s_add_u32 s78,s56,100139008
L 4
buffer_store_dwordx4 v[156:159],v7,s[36:39],s78 offen offset:0 sc0 sc1
s_mov_b32 s78,100663296
L 3
buffer_store_dwordx4 v[160:163],v7,s[36:39],s78 offen offset:0 sc0 sc1
s_add_u32 s78,s56,101187584
L 2
buffer_store_dwordx4 v[164:167],v7,s[36:39],s78 offen offset:0 sc0 sc1
s_mov_b32 s78,101711872
L 1
buffer_store_dwordx4 v[168:171],v7,s[36:39],s78 offen offset:0 sc0 sc1
s_add_u32 s78,s56,102236160
L 0
buffer_store_dwordx4 v[172:175],v7,s[36:39],s78 offen offset:0 sc0 sc1
s_waitcnt vmcnt(0)
s_barrier
s_add_u32 s48,s48,32768
buffer_store_dword v23,v10,s[36:39],s48 offen offset:0 sc0 sc1
s_cmp_eq_u32 s51,2
s_cbranch_scc0 Lcv_f_%=
s_mov_b32 m0,163584
s_mov_b32 s78,3694592
buffer_load_dword v12,s[60:63],s78 offen offset:0  lds
s_mov_b32 m0,163584
s_mov_b32 s78,5541888
buffer_load_dword v12,s[60:63],s78 offen offset:0  lds
Lcv_f_%=:
Lcode_cv_e:
.purgem M
.purgem R
.purgem T
.purgem D
.purgem L
)"
  :: "s"(ws), "s"(sWave), "s"(sBuf), "s"(sAuxOff), "s"(sFoutOff), "s"(sC0), "s"(sC1), "s"(sExp), "v"(vL16), "v"(vPA), "v"(vPB), "v"(vPU), "v"(vPW), "v"(vOR), "v"(vORA), "v"(vOG), "v"(vFA), "v"(vFB), "v"(vFlag), "v"(vTW), "v"(vTX), "s"(w3), "s"(w4) : "v0", "v1", "v2", "v3", "v4", "v5", "v6", "v7", "v8", "v9", "v10", "v11", "v12", "v13", "v14", "v15", "v16", "v17", "v18", "v19", "v20", "v21", "v22", "v23", "v24", "v25", "v26", "v27", "v28", "v29", "v30", "v31", "v32", "v33", "v34", "v35", "v36", "v37", "v38", "v39", "v40", "v41", "v42", "v43", "v44", "v45", "v46", "v47", "v48", "v49", "v50", "v51", "v52", "v53", "v54", "v55", "v56", "v57", "v58", "v59", "v60", "v61", "v62", "v63", "v64", "v65", "v66", "v67", "v68", "v69", "v70", "v71", "v72", "v73", "v74", "v75", "v76", "v77", "v78", "v79", "v80", "v81", "v82", "v83", "v84", "v85", "v86", "v87", "v88", "v89", "v90", "v91", "v92", "v93", "v94", "v95", "v96", "v97", "v98", "v99", "v100", "v101", "v102", "v103", "v104", "v105", "v106", "v107", "v108", "v109", "v110", "v111", "v112", "v113", "v114", "v115", "v116", "v117", "v118", "v119", "v120", "v121", "v122", "v123", "v124", "v125", "v126", "v127", "v128", "v129", "v130", "v131", "v132", "v133", "v134", "v135", "v136", "v137", "v138", "v139", "v140", "v141", "v142", "v143", "v144", "v145", "v146", "v147", "v148", "v149", "v150", "v151", "v152", "v153", "v154", "v155", "v156", "v157", "v158", "v159", "v160", "v161", "v162", "v163", "v164", "v165", "v166", "v167", "v168", "v169", "v170", "v171", "v172", "v173", "v174", "v175", "v176", "v177", "v178", "v179", "v180", "v181", "v182", "v183", "v184", "v185", "v186", "v187", "v188", "v189", "v190", "v191", "v192", "v193", "v194", "v195", "v196", "v197", "v198", "v199", "v200", "v201", "v202", "v203", "v204", "v205", "v206", "v207", "v208", "v209", "v210", "v211", "v212", "v213", "v214", "v215", "v216", "v217", "v218", "v219", "v220", "v221", "v222", "v223", "v224", "v225", "v226", "v227", "v228", "v229", "v230", "v231", "a0", "a1", "a2", "a3", "a4", "a5", "a6", "a7", "a8", "a9", "a10", "a11", "a12", "a13", "a14", "a15", "a16", "a17", "a18", "a19", "a20", "a21", "a22", "a23", "a24", "a25", "a26", "a27", "a28", "a29", "a30", "a31", "a32", "a33", "a34", "a35", "a36", "a37", "a38", "a39", "a40", "a41", "a42", "a43", "a44", "a45", "a46", "a47", "a48", "a49", "a50", "a51", "a52", "a53", "a54", "a55", "a56", "a57", "a58", "a59", "a60", "a61", "a62", "a63", "a64", "a65", "a66", "a67", "a68", "a69", "a70", "a71", "a72", "a73", "a74", "a75", "a76", "a77", "a78", "a79", "a80", "a81", "a82", "a83", "a84", "a85", "a86", "a87", "a88", "a89", "a90", "a91", "a92", "a93", "a94", "a95", "a96", "a97", "a98", "a99", "a100", "a101", "a102", "a103", "a104", "a105", "a106", "a107", "a108", "a109", "a110", "a111", "a112", "a113", "a114", "a115", "a116", "a117", "a118", "a119", "a120", "a121", "a122", "a123", "a124", "a125", "a126", "a127", "a128", "a129", "a130", "a131", "a132", "a133", "a134", "a135", "a136", "a137", "a138", "a139", "a140", "a141", "a142", "a143", "a144", "a145", "a146", "a147", "a148", "a149", "a150", "a151", "a152", "a153", "a154", "a155", "a156", "a157", "a158", "a159", "a160", "a161", "a162", "a163", "a164", "a165", "a166", "a167", "a168", "a169", "a170", "a171", "a172", "a173", "a174", "a175", "a176", "a177", "a178", "a179", "a180", "a181", "a182", "a183", "a184", "a185", "a186", "a187", "a188", "a189", "a190", "a191", "a192", "a193", "a194", "a195", "a196", "a197", "a198", "a199", "a200", "a201", "a202", "a203", "a204", "a205", "a206", "a207", "a208", "a209", "a210", "a211", "a212", "a213", "a214", "a215", "a216", "a217", "a218", "a219", "a220", "a221", "a222", "a223", "a224", "a225", "a226", "a227", "a228", "a229", "a230", "a231", "a232", "a233", "a234", "a235", "a236", "a237", "a238", "a239", "a240", "a241", "a242", "a243", "a244", "a245", "a246", "a247", "a248", "a249", "a250", "a251", "a252", "a253", "a254", "a255", "s36", "s37", "s38", "s39", "s40", "s41", "s42", "s43", "s44", "s45", "s46", "s47", "s48", "s49", "s50", "s51", "s52", "s53", "s54", "s55", "s56", "s57", "s58", "s59", "s60", "s61", "s62", "s63", "s64", "s65", "s66", "s67", "s68", "s69", "s70", "s71", "s72", "s73", "s74", "s75", "s76", "s77", "s78", "s79", "s80", "s81", "s82", "s83", "s84", "s85", "s86", "s87", "s88", "s89", "s90", "s91", "s92", "s93", "s94", "s95", "s96", "s97", "vcc", "scc", "memory");
    LANE_ID(lq_);
    const int g = QT0 + (NCV - 1 - iw) * 164 + min(wave * 64 + lq_, 163);
    if (QTB) touch_lds((const char*)q + (size_t)(g < 32768 ? g : 0) * 128, sScr);
  }
  WAIT_LGKM0();
  __builtin_amdgcn_s_barrier();
  CTS(10);
  }
  const unsigned sDbg0 = 110u * 1048576u + (b * 4 + wave) * 8192;
  constexpr unsigned OOB = 0x80000000u;
  if (kind == 6) {
    LANE_ID(lr);
    const int r8 = lr & 7, seg = lr >> 3, j = lr & 15, g = lr >> 4, rr = 8 * g + (j >> 2);
    const unsigned sWave = wave, sExp = __builtin_amdgcn_readfirstlane(ep), sG0 = slice, sStep = NRW, sDbg = sDbg0;
    const unsigned sWw = ((slice >> 3) * 1792 + 16 * wave) * 2048 + (slice & 7) * 256;
    const unsigned vWd0 = seg * 2048 + (r8 ^ swzB(seg)) * 16, vWd1 = (seg + 8) * 2048 + (r8 ^ swzB(8 + seg)) * 16;
    const unsigned vWr0 = (wave >> 1) * 8192 + rr * 128 + (((wave & 1) * 4 + (lr & 3)) ^ swzB(rr)) * 16, vWr1 = vWr0 + 8;
    const unsigned vRF = (wave * 8 + (seg >> 2)) * 1024 + ((seg & 3) * 16 + r8) * 16;
    const unsigned vRwr = seg == 0 ? r8 * 64 + wave * 4 : 512 + lr * 4, vRrd = r8 * 64;
    const unsigned vRP = g == (j >> 2) ? r8 * 64 + 16 + wave * 8 + (seg & 1) * 4 : 768 + lr * 4;
    const unsigned vRS = (wave == 0 && seg < 2) ? r8 * 16 + seg * 256 : OOB, vFlag = lr < 32 ? lr * 4 : OOB;
    const unsigned vL4 = ((lr & 2) * 4 + wave * 2 + (lr & 1)) * 128, vLY = min((wave < 3 ? wave * 32 : 112) + lr, wave < 2 ? wave * 32 + 31 : wave == 2 ? 111 : NCV - 1) * 128;
    const unsigned vQ = r8 * 2048 + wave * 512 + seg * 16;
    const unsigned vY0 = wave * 4096 + lr * 16;
    const unsigned vTr = 4096 + wave * 4096 + ((2 * g + ((j & 3) >> 1)) * 8 + (j >> 2)) * 16 + (j & 1) * 8;
    const unsigned one = g == (j >> 2) ? 0x3f80u << (16 * (j & 1)) : 0u, vSa = (j & 2) ? 0u : one, vSb = (j & 2) ? one : 0u;
    (void)sDbg;
    asm volatile(
R"(.macro M d,w,x
v_mfma_f32_16x16x32_bf16 v[\d:\d+3], \w, v[\x:\x+3], v[\d:\d+3]
.endm
.macro R d,a,o
ds_read_b128 v[\d:\d+3], v\a offset:\o
.endm
.macro T d,a,o
ds_read_b64_tr_b16 \d, v\a offset:\o
.endm
.macro D a,s,o
buffer_load_dwordx4 v\a, s[36:39], s\s offen offset:\o sc1 lds
.endm
.macro L c
s_waitcnt lgkmcnt(\c)
.endm
Lcode_r_b:
s_mov_b64 s[36:37],%0
s_mov_b32 s38,111243392
s_mov_b32 s39,0x00020000
s_mov_b32 s44,%1
s_mov_b32 s45,%2
s_mov_b32 s46,%3
s_mov_b32 s50,%4
s_mov_b32 s53,%5
v_mov_b32 v0,%6
v_mov_b32 v1,%7
v_mov_b32 v2,%8
v_mov_b32 v3,%9
v_mov_b32 v4,%10
v_mov_b32 v5,%11
v_mov_b32 v6,%12
v_mov_b32 v7,%13
v_mov_b32 v8,%14
v_mov_b32 v9,%15
v_mov_b32 v10,%16
v_mov_b32 v11,%17
v_mov_b32 v12,%18
v_mov_b32 v13,%19
v_mov_b32 v14,%20
v_mov_b32 v15,%21
v_mov_b32 v16,%22
s_mov_b32 s47,256
s_mov_b32 s49,0
s_mov_b32 s93,s45
s_mov_b64 s[84:85],%23
s_mov_b32 s86,4194304
s_mov_b32 s87,0x00020000
s_lshl_b32 s55,s45,14
buffer_load_dwordx4 v[164:167],v8,s[84:87],s55 offen offset:0 nt
buffer_load_dwordx4 v[168:171],v8,s[84:87],s55 offen offset:128 nt
buffer_load_dwordx4 v[172:175],v8,s[84:87],s55 offen offset:256 nt
buffer_load_dwordx4 v[176:179],v8,s[84:87],s55 offen offset:384 nt
v_mov_b32 v17,s53
v_mbcnt_lo_u32_b32 v19,-1,0
v_mbcnt_hi_u32_b32 v19,-1,v19
v_lshlrev_b32 v19,4,v19
s_mov_b32 s82,0
s_mov_b32 s63,0x7fff0000
s_mov_b32 s64,0
s_mov_b32 s67,0
s_lshl_b32 s65,s44,12
s_add_u32 s65,s65,4096
s_lshl_b32 s66,s44,7
s_add_u32 s66,s66,109056000
s_mov_b32 s70,0xaaaaaaaa
s_mov_b32 s71,0xaaaaaaaa
s_mov_b32 s72,0xcccccccc
s_mov_b32 s73,0xcccccccc
s_mov_b32 s74,0xff00ff00
s_mov_b32 s75,0xff00ff00
s_mov_b32 s68,0x3a800000
s_mov_b32 s69,0x3727c5ac
s_mov_b32 s76,0xc0135761
s_mov_b32 s77,0x3d372713
v_mov_b32 v32,0x3f803f80
v_mov_b32 v33,0x3f803f80
v_mov_b32 v34,0x3f803f80
v_mov_b32 v35,0x3f803f80
v_mov_b32 v24,0
v_mov_b32 v25,0
v_mov_b32 v26,v11
v_mov_b32 v27,v12
v_mov_b32 v28,0
v_mov_b32 v29,0
s_waitcnt vmcnt(0)
Lr_a0_%=:
s_cmp_ge_u32 s45,34
s_cbranch_scc1 Lr_fp0_%=
s_waitcnt vmcnt(0)
buffer_store_dword v17,v5,s[36:39],s63 offen offset:0 sc0 sc1
s_mov_b32 s63,0x7fff0000
Lr_fp0_%=:
s_waitcnt vmcnt(10)
v_mfma_f32_16x16x32_bf16 v[36:39],v[32:35],v[164:167],0
s_add_u32 s79,s45,s46
s_cmp_lt_u32 s45,64
s_cselect_b32 s78,s49,0
v_mfma_f32_16x16x32_bf16 v[40:43],v[164:167],v[164:167],0
s_cmp_ge_u32 s79,64
s_cselect_b32 s78,s78,0
s_add_u32 s62,s79,s78
M 36,v[32:35],168
s_lshr_b32 s78,s45,1
s_and_b32 s79,s45,1
s_lshl_b32 s79,s79,7
M 40,v[168:171],168
s_lshl_b32 s57,s78,15
s_lshl_b32 s55,s62,14
s_cmp_lt_u32 s62,s47
M 36,v[32:35],172
s_cselect_b32 s55,s55,0x7fff0000
s_add_u32 s57,s57,s79
s_lshl_b32 s59,s78,9
M 40,v[172:175],172
buffer_load_dwordx4 v[196:199],v8,s[84:87],s55 offen offset:0 nt
M 36,v[32:35],176
buffer_load_dwordx4 v[200:203],v8,s[84:87],s55 offen offset:128 nt
M 40,v[176:179],176
buffer_load_dwordx4 v[204:207],v8,s[84:87],s55 offen offset:256 nt
v_mfma_f32_16x16x32_bf16 v[116:119],v[26:29],v[164:167],0
buffer_load_dwordx4 v[208:211],v8,s[84:87],s55 offen offset:384 nt
v_mfma_f32_16x16x32_bf16 v[120:123],v[24:27],v[164:167],0
s_add_u32 s57,s57,8388608
s_add_u32 s59,s59,s79
s_lshl_b32 s60,s45,9
v_mfma_f32_16x16x32_bf16 v[124:127],v[26:29],v[168:171],0
s_add_u32 s58,s57,4096
s_add_u32 s59,s59,106954752
s_add_u32 s60,s60,s66
v_mfma_f32_16x16x32_bf16 v[128:131],v[24:27],v[168:171],0
v_add_f32_dpp v44,v36,v36 row_ror:8 row_mask:0xf bank_mask:0xf
v_cndmask_b32 v45,v40,v41,s[70:71]
v_mfma_f32_16x16x32_bf16 v[132:135],v[26:29],v[172:175],0
ds_write_b32 v2,v44 offset:0
v_mfma_f32_16x16x32_bf16 v[136:139],v[24:27],v[172:175],0
v_cndmask_b32 v46,v42,v43,s[70:71]
v_cndmask_b32 v45,v45,v46,s[72:73]
v_mfma_f32_16x16x32_bf16 v[140:143],v[26:29],v[176:179],0
ds_write_b32 v0,v45 offset:0
v_mfma_f32_16x16x32_bf16 v[144:147],v[24:27],v[176:179],0
L 0
s_barrier
R 52,3,0
R 56,3,16
R 60,3,32
s_cmp_lg_u32 s67,0
s_cbranch_scc1 Lr_b0_%=
s_waitcnt vmcnt(4)
buffer_store_dword v17,v5,s[36:39],s63 offen offset:0 sc0 sc1
s_mov_b32 s63,0x7fff0000
s_mov_b32 s38,111243392
s_cmp_lt_u32 s45,64
s_cselect_b32 s64,-192,0
s_add_u32 s64,s64,256
s_cmp_lt_u32 s45,64
s_cselect_b32 s61,0,32768
s_add_u32 s61,s61,111177728
s_mul_i32 s56,s45,16384
s_add_u32 s56,s56,98566144
Lr_bpy0_%=:
buffer_load_dword v18,v7,s[36:39],s61 offen offset:0 sc1
s_add_u32 s82,s82,1
s_waitcnt vmcnt(0)
v_cmp_eq_u32 vcc,s53,v18
s_nop 0
s_cmp_eq_u64 vcc,-1
s_cbranch_scc1 Lr_bqy0_%=
s_sleep 2
s_cmp_lt_u32 s82,20000
s_cbranch_scc1 Lr_bpy0_%=
Lr_bqy0_%=:
s_add_u32 m0,s65,0
s_nop 0
D 9,56,0
D 9,56,1024
D 9,56,2048
D 9,56,3072
s_waitcnt vmcnt(0)
Lr_b0_%=:
s_waitcnt vmcnt(10)
T v[68:69],10,0
T v[70:71],10,64
T v[72:73],10,1024
T v[74:75],10,1088
T v[76:77],10,2048
T v[78:79],10,2112
T v[80:81],10,3072
T v[82:83],10,3136
s_mov_b32 s45,s62
s_min_u32 s78,s64,s47
s_mul_i32 s56,s45,16384
s_add_u32 s56,s56,98566144
s_cmp_lt_i32 s45,s78
s_cselect_b32 s67,1,0
s_cselect_b32 s56,s56,0x7fff0000
s_add_u32 m0,s65,16384
s_nop 0
D 9,56,0
D 9,56,1024
D 9,56,2048
D 9,56,3072
L 8
v_pk_add_f32 v[52:53],v[52:53],v[54:55]
v_pk_add_f32 v[56:57],v[56:57],v[58:59]
v_pk_add_f32 v[60:61],v[60:61],v[62:63]
v_add_f32 v52,v52,v53
v_pk_add_f32 v[56:57],v[56:57],v[60:61]
v_mul_f32 v52,s68,v52
v_add_f32 v53,v56,v57
v_mul_f32 v53,s68,v53
v_fma_f32 v53,-v52,v52,v53
v_max_f32 v53,0,v53
L 6
v_lshlrev_b32 v84,16,v68
v_and_b32 v85,0xffff0000,v68
v_lshlrev_b32 v86,16,v69
v_and_b32 v87,0xffff0000,v69
v_lshlrev_b32 v88,16,v70
v_and_b32 v89,0xffff0000,v70
v_lshlrev_b32 v90,16,v71
v_and_b32 v91,0xffff0000,v71
L 4
v_lshlrev_b32 v92,16,v72
v_and_b32 v93,0xffff0000,v72
v_lshlrev_b32 v94,16,v73
v_and_b32 v95,0xffff0000,v73
v_lshlrev_b32 v96,16,v74
v_and_b32 v97,0xffff0000,v74
v_lshlrev_b32 v98,16,v75
v_and_b32 v99,0xffff0000,v75
L 2
v_lshlrev_b32 v100,16,v76
v_and_b32 v101,0xffff0000,v76
v_lshlrev_b32 v102,16,v77
v_and_b32 v103,0xffff0000,v77
v_lshlrev_b32 v104,16,v78
v_and_b32 v105,0xffff0000,v78
v_lshlrev_b32 v106,16,v79
v_and_b32 v107,0xffff0000,v79
L 0
v_lshlrev_b32 v108,16,v80
v_and_b32 v109,0xffff0000,v80
v_lshlrev_b32 v110,16,v81
v_and_b32 v111,0xffff0000,v81
v_lshlrev_b32 v112,16,v82
v_and_b32 v113,0xffff0000,v82
v_lshlrev_b32 v114,16,v83
v_and_b32 v115,0xffff0000,v83
v_add_f32 v53,s69,v53
v_rsq_f32 v48,v53
s_nop 0
v_mul_f32_e64 v50,-v52,v48
v_mov_b32 v49,v48
v_mov_b32 v51,v50
v_pk_fma_f32 v[52:53],v[116:117],v[48:49],v[50:51]
v_pk_fma_f32 v[54:55],v[118:119],v[48:49],v[50:51]
v_pk_fma_f32 v[56:57],v[120:121],v[48:49],v[50:51]
v_pk_fma_f32 v[58:59],v[122:123],v[48:49],v[50:51]
v_pk_fma_f32 v[116:117],v[52:53],v[84:85],v[116:117]
v_pk_fma_f32 v[118:119],v[54:55],v[86:87],v[118:119]
v_pk_fma_f32 v[120:121],v[56:57],v[88:89],v[120:121]
v_pk_fma_f32 v[122:123],v[58:59],v[90:91],v[122:123]
v_cvt_pk_bf16_f32 v148,v116,v117
v_cvt_pk_bf16_f32 v149,v118,v119
v_cvt_pk_bf16_f32 v150,v120,v121
v_cvt_pk_bf16_f32 v151,v122,v123
v_pk_fma_f32 v[52:53],v[124:125],v[48:49],v[50:51]
v_pk_fma_f32 v[54:55],v[126:127],v[48:49],v[50:51]
v_pk_fma_f32 v[56:57],v[128:129],v[48:49],v[50:51]
v_pk_fma_f32 v[58:59],v[130:131],v[48:49],v[50:51]
v_pk_fma_f32 v[124:125],v[52:53],v[92:93],v[124:125]
v_pk_fma_f32 v[126:127],v[54:55],v[94:95],v[126:127]
v_pk_fma_f32 v[128:129],v[56:57],v[96:97],v[128:129]
v_pk_fma_f32 v[130:131],v[58:59],v[98:99],v[130:131]
v_cvt_pk_bf16_f32 v152,v124,v125
v_cvt_pk_bf16_f32 v153,v126,v127
v_cvt_pk_bf16_f32 v154,v128,v129
v_cvt_pk_bf16_f32 v155,v130,v131
v_pk_fma_f32 v[52:53],v[132:133],v[48:49],v[50:51]
v_pk_fma_f32 v[54:55],v[134:135],v[48:49],v[50:51]
v_pk_fma_f32 v[56:57],v[136:137],v[48:49],v[50:51]
v_pk_fma_f32 v[58:59],v[138:139],v[48:49],v[50:51]
v_pk_fma_f32 v[132:133],v[52:53],v[100:101],v[132:133]
v_pk_fma_f32 v[134:135],v[54:55],v[102:103],v[134:135]
v_pk_fma_f32 v[136:137],v[56:57],v[104:105],v[136:137]
v_pk_fma_f32 v[138:139],v[58:59],v[106:107],v[138:139]
v_cvt_pk_bf16_f32 v156,v132,v133
v_cvt_pk_bf16_f32 v157,v134,v135
v_cvt_pk_bf16_f32 v158,v136,v137
v_cvt_pk_bf16_f32 v159,v138,v139
v_pk_fma_f32 v[52:53],v[140:141],v[48:49],v[50:51]
v_pk_fma_f32 v[54:55],v[142:143],v[48:49],v[50:51]
v_pk_fma_f32 v[56:57],v[144:145],v[48:49],v[50:51]
v_pk_fma_f32 v[58:59],v[146:147],v[48:49],v[50:51]
v_pk_fma_f32 v[140:141],v[52:53],v[108:109],v[140:141]
v_pk_fma_f32 v[142:143],v[54:55],v[110:111],v[142:143]
v_pk_fma_f32 v[144:145],v[56:57],v[112:113],v[144:145]
v_pk_fma_f32 v[146:147],v[58:59],v[114:115],v[146:147]
v_cvt_pk_bf16_f32 v160,v140,v141
v_cvt_pk_bf16_f32 v161,v142,v143
v_cvt_pk_bf16_f32 v162,v144,v145
v_cvt_pk_bf16_f32 v163,v146,v147
s_waitcnt vmcnt(8)
buffer_store_dword v17,v5,s[36:39],s63 offen offset:0 sc0 sc1
s_mov_b32 s63,0x7fff0000
v_mfma_f32_16x16x32_bf16 v[36:39],v[32:35],v[148:151],0
buffer_store_dwordx4 v[148:151],v1,s[36:39],s57 offen offset:0 sc0 sc1
v_mfma_f32_16x16x32_bf16 v[40:43],v[148:151],v[148:151],0
M 36,v[32:35],152
buffer_store_dwordx4 v[152:155],v1,s[36:39],s57 offen offset:2048 sc0 sc1
M 40,v[152:155],152
M 36,v[32:35],156
buffer_store_dwordx4 v[156:159],v1,s[36:39],s58 offen offset:0 sc0 sc1
M 40,v[156:159],156
M 36,v[32:35],160
buffer_store_dwordx4 v[160:163],v1,s[36:39],s58 offen offset:2048 sc0 sc1
M 40,v[160:163],160
s_nop 5
v_add_f32_dpp v44,v36,v36 row_ror:8 row_mask:0xf bank_mask:0xf
s_nop 0
v_cndmask_b32 v45,v40,v41,s[70:71]
v_cndmask_b32 v46,v42,v43,s[70:71]
ds_write_b32 v2,v44 offset:1024
v_cndmask_b32 v45,v45,v46,s[72:73]
ds_write_b32 v0,v45 offset:1024
L 0
s_barrier
R 52,3,1024
R 56,3,1040
R 60,3,1056
L 0
v_pk_add_f32 v[52:53],v[52:53],v[54:55]
v_pk_add_f32 v[56:57],v[56:57],v[58:59]
v_pk_add_f32 v[60:61],v[60:61],v[62:63]
v_add_f32 v52,v52,v53
v_pk_add_f32 v[56:57],v[56:57],v[60:61]
v_mul_f32 v52,s68,v52
v_add_f32 v53,v56,v57
v_mul_f32 v53,s68,v53
v_fma_f32 v53,-v52,v52,v53
v_max_f32 v53,0,v53
v_add_f32 v54,s69,v53
v_fma_f32 v55,s69,v54,v53
v_rsq_f32 v65,v54
v_rsq_f32 v54,v55
v_mul_f32_e64 v64,-v52,v65
v_mul_f32 v63,v55,v54
v_mul_f32 v61,s76,v54
v_mul_f32 v55,v54,v54
v_mul_f32 v62,s77,v55
v_mul_f32 v62,v62,v61
v_cndmask_b32 v60,v52,v64,s[74:75]
v_cndmask_b32 v61,v61,v65,s[74:75]
buffer_store_dwordx4 v[60:63],v4,s[36:39],s59 offen offset:0 sc0 sc1
s_mov_b32 s63,s60
s_cmp_lt_i32 s45,s47
s_cbranch_scc0 Lr_mid_%=
Lr_a1_%=:
s_cmp_ge_u32 s45,34
s_cbranch_scc1 Lr_fp1_%=
s_waitcnt vmcnt(0)
buffer_store_dword v17,v5,s[36:39],s63 offen offset:0 sc0 sc1
s_mov_b32 s63,0x7fff0000
Lr_fp1_%=:
s_waitcnt vmcnt(10)
v_mfma_f32_16x16x32_bf16 v[36:39],v[32:35],v[196:199],0
s_add_u32 s79,s45,s46
s_cmp_lt_u32 s45,64
s_cselect_b32 s78,s49,0
v_mfma_f32_16x16x32_bf16 v[40:43],v[196:199],v[196:199],0
s_cmp_ge_u32 s79,64
s_cselect_b32 s78,s78,0
s_add_u32 s62,s79,s78
M 36,v[32:35],200
s_lshr_b32 s78,s45,1
s_and_b32 s79,s45,1
s_lshl_b32 s79,s79,7
M 40,v[200:203],200
s_lshl_b32 s57,s78,15
s_lshl_b32 s55,s62,14
s_cmp_lt_u32 s62,s47
M 36,v[32:35],204
s_cselect_b32 s55,s55,0x7fff0000
s_add_u32 s57,s57,s79
s_lshl_b32 s59,s78,9
M 40,v[204:207],204
buffer_load_dwordx4 v[164:167],v8,s[84:87],s55 offen offset:0 nt
M 36,v[32:35],208
buffer_load_dwordx4 v[168:171],v8,s[84:87],s55 offen offset:128 nt
M 40,v[208:211],208
buffer_load_dwordx4 v[172:175],v8,s[84:87],s55 offen offset:256 nt
v_mfma_f32_16x16x32_bf16 v[116:119],v[26:29],v[196:199],0
buffer_load_dwordx4 v[176:179],v8,s[84:87],s55 offen offset:384 nt
v_mfma_f32_16x16x32_bf16 v[120:123],v[24:27],v[196:199],0
s_add_u32 s57,s57,8388608
s_add_u32 s59,s59,s79
s_lshl_b32 s60,s45,9
v_mfma_f32_16x16x32_bf16 v[124:127],v[26:29],v[200:203],0
s_add_u32 s58,s57,4096
s_add_u32 s59,s59,106954752
s_add_u32 s60,s60,s66
v_mfma_f32_16x16x32_bf16 v[128:131],v[24:27],v[200:203],0
v_add_f32_dpp v44,v36,v36 row_ror:8 row_mask:0xf bank_mask:0xf
v_cndmask_b32 v45,v40,v41,s[70:71]
v_mfma_f32_16x16x32_bf16 v[132:135],v[26:29],v[204:207],0
ds_write_b32 v2,v44 offset:0
v_mfma_f32_16x16x32_bf16 v[136:139],v[24:27],v[204:207],0
v_cndmask_b32 v46,v42,v43,s[70:71]
v_cndmask_b32 v45,v45,v46,s[72:73]
v_mfma_f32_16x16x32_bf16 v[140:143],v[26:29],v[208:211],0
ds_write_b32 v0,v45 offset:0
v_mfma_f32_16x16x32_bf16 v[144:147],v[24:27],v[208:211],0
L 0
s_barrier
R 52,3,0
R 56,3,16
R 60,3,32
s_cmp_lg_u32 s67,0
s_cbranch_scc1 Lr_b1_%=
s_waitcnt vmcnt(4)
buffer_store_dword v17,v5,s[36:39],s63 offen offset:0 sc0 sc1
s_mov_b32 s63,0x7fff0000
s_mov_b32 s38,111243392
s_cmp_lt_u32 s45,64
s_cselect_b32 s64,-192,0
s_add_u32 s64,s64,256
s_cmp_lt_u32 s45,64
s_cselect_b32 s61,0,32768
s_add_u32 s61,s61,111177728
s_mul_i32 s56,s45,16384
s_add_u32 s56,s56,98566144
Lr_bpy1_%=:
buffer_load_dword v18,v7,s[36:39],s61 offen offset:0 sc1
s_add_u32 s82,s82,1
s_waitcnt vmcnt(0)
v_cmp_eq_u32 vcc,s53,v18
s_nop 0
s_cmp_eq_u64 vcc,-1
s_cbranch_scc1 Lr_bqy1_%=
s_sleep 2
s_cmp_lt_u32 s82,20000
s_cbranch_scc1 Lr_bpy1_%=
Lr_bqy1_%=:
s_add_u32 m0,s65,16384
s_nop 0
D 9,56,0
D 9,56,1024
D 9,56,2048
D 9,56,3072
s_waitcnt vmcnt(0)
Lr_b1_%=:
s_waitcnt vmcnt(10)
T v[68:69],10,16384
T v[70:71],10,16448
T v[72:73],10,17408
T v[74:75],10,17472
T v[76:77],10,18432
T v[78:79],10,18496
T v[80:81],10,19456
T v[82:83],10,19520
s_mov_b32 s45,s62
s_min_u32 s78,s64,s47
s_mul_i32 s56,s45,16384
s_add_u32 s56,s56,98566144
s_cmp_lt_i32 s45,s78
s_cselect_b32 s67,1,0
s_cselect_b32 s56,s56,0x7fff0000
s_add_u32 m0,s65,0
s_nop 0
D 9,56,0
D 9,56,1024
D 9,56,2048
D 9,56,3072
L 8
v_pk_add_f32 v[52:53],v[52:53],v[54:55]
v_pk_add_f32 v[56:57],v[56:57],v[58:59]
v_pk_add_f32 v[60:61],v[60:61],v[62:63]
v_add_f32 v52,v52,v53
v_pk_add_f32 v[56:57],v[56:57],v[60:61]
v_mul_f32 v52,s68,v52
v_add_f32 v53,v56,v57
v_mul_f32 v53,s68,v53
v_fma_f32 v53,-v52,v52,v53
v_max_f32 v53,0,v53
L 6
v_lshlrev_b32 v84,16,v68
v_and_b32 v85,0xffff0000,v68
v_lshlrev_b32 v86,16,v69
v_and_b32 v87,0xffff0000,v69
v_lshlrev_b32 v88,16,v70
v_and_b32 v89,0xffff0000,v70
v_lshlrev_b32 v90,16,v71
v_and_b32 v91,0xffff0000,v71
L 4
v_lshlrev_b32 v92,16,v72
v_and_b32 v93,0xffff0000,v72
v_lshlrev_b32 v94,16,v73
v_and_b32 v95,0xffff0000,v73
v_lshlrev_b32 v96,16,v74
v_and_b32 v97,0xffff0000,v74
)"
R"(v_lshlrev_b32 v98,16,v75
v_and_b32 v99,0xffff0000,v75
L 2
v_lshlrev_b32 v100,16,v76
v_and_b32 v101,0xffff0000,v76
v_lshlrev_b32 v102,16,v77
v_and_b32 v103,0xffff0000,v77
v_lshlrev_b32 v104,16,v78
v_and_b32 v105,0xffff0000,v78
v_lshlrev_b32 v106,16,v79
v_and_b32 v107,0xffff0000,v79
L 0
v_lshlrev_b32 v108,16,v80
v_and_b32 v109,0xffff0000,v80
v_lshlrev_b32 v110,16,v81
v_and_b32 v111,0xffff0000,v81
v_lshlrev_b32 v112,16,v82
v_and_b32 v113,0xffff0000,v82
v_lshlrev_b32 v114,16,v83
v_and_b32 v115,0xffff0000,v83
v_add_f32 v53,s69,v53
v_rsq_f32 v48,v53
s_nop 0
v_mul_f32_e64 v50,-v52,v48
v_mov_b32 v49,v48
v_mov_b32 v51,v50
v_pk_fma_f32 v[52:53],v[116:117],v[48:49],v[50:51]
v_pk_fma_f32 v[54:55],v[118:119],v[48:49],v[50:51]
v_pk_fma_f32 v[56:57],v[120:121],v[48:49],v[50:51]
v_pk_fma_f32 v[58:59],v[122:123],v[48:49],v[50:51]
v_pk_fma_f32 v[116:117],v[52:53],v[84:85],v[116:117]
v_pk_fma_f32 v[118:119],v[54:55],v[86:87],v[118:119]
v_pk_fma_f32 v[120:121],v[56:57],v[88:89],v[120:121]
v_pk_fma_f32 v[122:123],v[58:59],v[90:91],v[122:123]
v_cvt_pk_bf16_f32 v148,v116,v117
v_cvt_pk_bf16_f32 v149,v118,v119
v_cvt_pk_bf16_f32 v150,v120,v121
v_cvt_pk_bf16_f32 v151,v122,v123
v_pk_fma_f32 v[52:53],v[124:125],v[48:49],v[50:51]
v_pk_fma_f32 v[54:55],v[126:127],v[48:49],v[50:51]
v_pk_fma_f32 v[56:57],v[128:129],v[48:49],v[50:51]
v_pk_fma_f32 v[58:59],v[130:131],v[48:49],v[50:51]
v_pk_fma_f32 v[124:125],v[52:53],v[92:93],v[124:125]
v_pk_fma_f32 v[126:127],v[54:55],v[94:95],v[126:127]
v_pk_fma_f32 v[128:129],v[56:57],v[96:97],v[128:129]
v_pk_fma_f32 v[130:131],v[58:59],v[98:99],v[130:131]
v_cvt_pk_bf16_f32 v152,v124,v125
v_cvt_pk_bf16_f32 v153,v126,v127
v_cvt_pk_bf16_f32 v154,v128,v129
v_cvt_pk_bf16_f32 v155,v130,v131
v_pk_fma_f32 v[52:53],v[132:133],v[48:49],v[50:51]
v_pk_fma_f32 v[54:55],v[134:135],v[48:49],v[50:51]
v_pk_fma_f32 v[56:57],v[136:137],v[48:49],v[50:51]
v_pk_fma_f32 v[58:59],v[138:139],v[48:49],v[50:51]
v_pk_fma_f32 v[132:133],v[52:53],v[100:101],v[132:133]
v_pk_fma_f32 v[134:135],v[54:55],v[102:103],v[134:135]
v_pk_fma_f32 v[136:137],v[56:57],v[104:105],v[136:137]
v_pk_fma_f32 v[138:139],v[58:59],v[106:107],v[138:139]
v_cvt_pk_bf16_f32 v156,v132,v133
v_cvt_pk_bf16_f32 v157,v134,v135
v_cvt_pk_bf16_f32 v158,v136,v137
v_cvt_pk_bf16_f32 v159,v138,v139
v_pk_fma_f32 v[52:53],v[140:141],v[48:49],v[50:51]
v_pk_fma_f32 v[54:55],v[142:143],v[48:49],v[50:51]
v_pk_fma_f32 v[56:57],v[144:145],v[48:49],v[50:51]
v_pk_fma_f32 v[58:59],v[146:147],v[48:49],v[50:51]
v_pk_fma_f32 v[140:141],v[52:53],v[108:109],v[140:141]
v_pk_fma_f32 v[142:143],v[54:55],v[110:111],v[142:143]
v_pk_fma_f32 v[144:145],v[56:57],v[112:113],v[144:145]
v_pk_fma_f32 v[146:147],v[58:59],v[114:115],v[146:147]
v_cvt_pk_bf16_f32 v160,v140,v141
v_cvt_pk_bf16_f32 v161,v142,v143
v_cvt_pk_bf16_f32 v162,v144,v145
v_cvt_pk_bf16_f32 v163,v146,v147
s_waitcnt vmcnt(8)
buffer_store_dword v17,v5,s[36:39],s63 offen offset:0 sc0 sc1
s_mov_b32 s63,0x7fff0000
v_mfma_f32_16x16x32_bf16 v[36:39],v[32:35],v[148:151],0
buffer_store_dwordx4 v[148:151],v1,s[36:39],s57 offen offset:0 sc0 sc1
v_mfma_f32_16x16x32_bf16 v[40:43],v[148:151],v[148:151],0
M 36,v[32:35],152
buffer_store_dwordx4 v[152:155],v1,s[36:39],s57 offen offset:2048 sc0 sc1
M 40,v[152:155],152
M 36,v[32:35],156
buffer_store_dwordx4 v[156:159],v1,s[36:39],s58 offen offset:0 sc0 sc1
M 40,v[156:159],156
M 36,v[32:35],160
buffer_store_dwordx4 v[160:163],v1,s[36:39],s58 offen offset:2048 sc0 sc1
M 40,v[160:163],160
s_nop 5
v_add_f32_dpp v44,v36,v36 row_ror:8 row_mask:0xf bank_mask:0xf
s_nop 0
v_cndmask_b32 v45,v40,v41,s[70:71]
v_cndmask_b32 v46,v42,v43,s[70:71]
ds_write_b32 v2,v44 offset:1024
v_cndmask_b32 v45,v45,v46,s[72:73]
ds_write_b32 v0,v45 offset:1024
L 0
s_barrier
R 52,3,1024
R 56,3,1040
R 60,3,1056
L 0
v_pk_add_f32 v[52:53],v[52:53],v[54:55]
v_pk_add_f32 v[56:57],v[56:57],v[58:59]
v_pk_add_f32 v[60:61],v[60:61],v[62:63]
v_add_f32 v52,v52,v53
v_pk_add_f32 v[56:57],v[56:57],v[60:61]
v_mul_f32 v52,s68,v52
v_add_f32 v53,v56,v57
v_mul_f32 v53,s68,v53
v_fma_f32 v53,-v52,v52,v53
v_max_f32 v53,0,v53
v_add_f32 v54,s69,v53
v_fma_f32 v55,s69,v54,v53
v_rsq_f32 v65,v54
v_rsq_f32 v54,v55
v_mul_f32_e64 v64,-v52,v65
v_mul_f32 v63,v55,v54
v_mul_f32 v61,s76,v54
v_mul_f32 v55,v54,v54
v_mul_f32 v62,s77,v55
v_mul_f32 v62,v62,v61
v_cndmask_b32 v60,v52,v64,s[74:75]
v_cndmask_b32 v61,v61,v65,s[74:75]
buffer_store_dwordx4 v[60:63],v4,s[36:39],s59 offen offset:0 sc0 sc1
s_mov_b32 s63,s60
s_cmp_lt_i32 s45,s47
s_cbranch_scc1 Lr_a0_%=
Lr_mid_%=:
s_waitcnt vmcnt(0)
buffer_store_dword v17,v5,s[36:39],s63 offen offset:0 sc0 sc1
s_mov_b32 s63,0x7fff0000
s_mov_b32 s45,s93
s_cmp_ge_u32 s45,16
s_cbranch_scc1 Lr_now_%=
s_mov_b64 s[40:41],%24
s_mov_b32 s42,7340032
s_mov_b32 s43,0x00020000
s_lshl_b32 s57,s44,11
s_add_u32 s55,s50,0
s_add_u32 m0,s57,40960
s_nop 0
buffer_load_dwordx4 v13,s[40:43],s55 offen offset:0  lds
s_add_u32 m0,s57,41984
s_nop 0
buffer_load_dwordx4 v14,s[40:43],s55 offen offset:0  lds
s_add_u32 m0,s57,49024
s_nop 0
buffer_load_dwordx4 v13,s[40:43],s55 offen offset:128  lds
s_add_u32 m0,s57,50048
s_nop 0
buffer_load_dwordx4 v14,s[40:43],s55 offen offset:128  lds
s_add_u32 s55,s50,131072
s_add_u32 m0,s57,57344
s_nop 0
buffer_load_dwordx4 v13,s[40:43],s55 offen offset:0  lds
s_add_u32 m0,s57,58368
s_nop 0
buffer_load_dwordx4 v14,s[40:43],s55 offen offset:0  lds
s_add_u32 m0,s57,65408
s_nop 0
buffer_load_dwordx4 v13,s[40:43],s55 offen offset:128  lds
s_add_u32 m0,s57,66432
s_nop 0
buffer_load_dwordx4 v14,s[40:43],s55 offen offset:128  lds
s_add_u32 s55,s50,262144
s_add_u32 m0,s57,73728
s_nop 0
buffer_load_dwordx4 v13,s[40:43],s55 offen offset:0  lds
s_add_u32 m0,s57,74752
s_nop 0
buffer_load_dwordx4 v14,s[40:43],s55 offen offset:0  lds
s_add_u32 m0,s57,81792
s_nop 0
buffer_load_dwordx4 v13,s[40:43],s55 offen offset:128  lds
s_add_u32 m0,s57,82816
s_nop 0
buffer_load_dwordx4 v14,s[40:43],s55 offen offset:128  lds
s_add_u32 s55,s50,393216
s_add_u32 m0,s57,90112
s_nop 0
buffer_load_dwordx4 v13,s[40:43],s55 offen offset:0  lds
s_add_u32 m0,s57,91136
s_nop 0
buffer_load_dwordx4 v14,s[40:43],s55 offen offset:0  lds
s_add_u32 m0,s57,98176
s_nop 0
buffer_load_dwordx4 v13,s[40:43],s55 offen offset:128  lds
s_add_u32 m0,s57,99200
s_nop 0
buffer_load_dwordx4 v14,s[40:43],s55 offen offset:128  lds
s_add_u32 s55,s50,524288
s_add_u32 m0,s57,106496
s_nop 0
buffer_load_dwordx4 v13,s[40:43],s55 offen offset:0  lds
s_add_u32 m0,s57,107520
s_nop 0
buffer_load_dwordx4 v14,s[40:43],s55 offen offset:0  lds
s_add_u32 m0,s57,114560
s_nop 0
buffer_load_dwordx4 v13,s[40:43],s55 offen offset:128  lds
s_add_u32 m0,s57,115584
s_nop 0
buffer_load_dwordx4 v14,s[40:43],s55 offen offset:128  lds
s_add_u32 s55,s50,655360
s_add_u32 m0,s57,122880
s_nop 0
buffer_load_dwordx4 v13,s[40:43],s55 offen offset:0  lds
s_add_u32 m0,s57,123904
s_nop 0
buffer_load_dwordx4 v14,s[40:43],s55 offen offset:0  lds
s_add_u32 m0,s57,130944
s_nop 0
buffer_load_dwordx4 v13,s[40:43],s55 offen offset:128  lds
s_add_u32 m0,s57,131968
s_nop 0
buffer_load_dwordx4 v14,s[40:43],s55 offen offset:128  lds
s_waitcnt vmcnt(20) lgkmcnt(0)
s_barrier
s_add_u32 s55,s50,786432
s_add_u32 m0,s57,139264
s_nop 0
buffer_load_dwordx4 v13,s[40:43],s55 offen offset:0  lds
s_add_u32 m0,s57,140288
s_nop 0
buffer_load_dwordx4 v14,s[40:43],s55 offen offset:0  lds
s_add_u32 m0,s57,147328
s_nop 0
buffer_load_dwordx4 v13,s[40:43],s55 offen offset:128  lds
s_add_u32 m0,s57,148352
s_nop 0
buffer_load_dwordx4 v14,s[40:43],s55 offen offset:128  lds
v_add_u32 v44,40960,v15
v_add_u32 v45,40960,v16
T a[0:1],44,0
T a[2:3],44,512
T a[4:5],45,0
T a[6:7],45,512
T a[8:9],44,4096
T a[10:11],44,4608
T a[12:13],45,4096
T a[14:15],45,4608
s_waitcnt vmcnt(20) lgkmcnt(0)
s_barrier
s_add_u32 s55,s50,917504
s_add_u32 m0,s57,40960
s_nop 0
buffer_load_dwordx4 v13,s[40:43],s55 offen offset:0  lds
s_add_u32 m0,s57,41984
s_nop 0
buffer_load_dwordx4 v14,s[40:43],s55 offen offset:0  lds
s_add_u32 m0,s57,49024
s_nop 0
buffer_load_dwordx4 v13,s[40:43],s55 offen offset:128  lds
s_add_u32 m0,s57,50048
s_nop 0
buffer_load_dwordx4 v14,s[40:43],s55 offen offset:128  lds
v_add_u32 v44,57344,v15
v_add_u32 v45,57344,v16
T a[16:17],44,0
T a[18:19],44,512
T a[20:21],45,0
T a[22:23],45,512
T a[24:25],44,4096
T a[26:27],44,4608
T a[28:29],45,4096
T a[30:31],45,4608
s_waitcnt vmcnt(20) lgkmcnt(0)
s_barrier
s_add_u32 s55,s50,1048576
s_add_u32 m0,s57,57344
s_nop 0
buffer_load_dwordx4 v13,s[40:43],s55 offen offset:0  lds
s_add_u32 m0,s57,58368
s_nop 0
buffer_load_dwordx4 v14,s[40:43],s55 offen offset:0  lds
s_add_u32 m0,s57,65408
s_nop 0
buffer_load_dwordx4 v13,s[40:43],s55 offen offset:128  lds
s_add_u32 m0,s57,66432
s_nop 0
buffer_load_dwordx4 v14,s[40:43],s55 offen offset:128  lds
v_add_u32 v44,73728,v15
v_add_u32 v45,73728,v16
T a[32:33],44,0
T a[34:35],44,512
T a[36:37],45,0
T a[38:39],45,512
T a[40:41],44,4096
T a[42:43],44,4608
T a[44:45],45,4096
T a[46:47],45,4608
s_waitcnt vmcnt(20) lgkmcnt(0)
s_barrier
s_add_u32 s55,s50,1179648
s_add_u32 m0,s57,73728
s_nop 0
buffer_load_dwordx4 v13,s[40:43],s55 offen offset:0  lds
s_add_u32 m0,s57,74752
s_nop 0
buffer_load_dwordx4 v14,s[40:43],s55 offen offset:0  lds
s_add_u32 m0,s57,81792
s_nop 0
buffer_load_dwordx4 v13,s[40:43],s55 offen offset:128  lds
s_add_u32 m0,s57,82816
s_nop 0
buffer_load_dwordx4 v14,s[40:43],s55 offen offset:128  lds
v_add_u32 v44,90112,v15
v_add_u32 v45,90112,v16
T a[48:49],44,0
T a[50:51],44,512
T a[52:53],45,0
T a[54:55],45,512
T a[56:57],44,4096
T a[58:59],44,4608
T a[60:61],45,4096
T a[62:63],45,4608
s_waitcnt vmcnt(20) lgkmcnt(0)
s_barrier
s_add_u32 s55,s50,1310720
s_add_u32 m0,s57,90112
s_nop 0
buffer_load_dwordx4 v13,s[40:43],s55 offen offset:0  lds
s_add_u32 m0,s57,91136
s_nop 0
buffer_load_dwordx4 v14,s[40:43],s55 offen offset:0  lds
s_add_u32 m0,s57,98176
s_nop 0
buffer_load_dwordx4 v13,s[40:43],s55 offen offset:128  lds
s_add_u32 m0,s57,99200
s_nop 0
buffer_load_dwordx4 v14,s[40:43],s55 offen offset:128  lds
v_add_u32 v44,106496,v15
v_add_u32 v45,106496,v16
T a[64:65],44,0
T a[66:67],44,512
T a[68:69],45,0
T a[70:71],45,512
T a[72:73],44,4096
T a[74:75],44,4608
T a[76:77],45,4096
T a[78:79],45,4608
s_waitcnt vmcnt(20) lgkmcnt(0)
s_barrier
s_add_u32 s55,s50,1441792
s_add_u32 m0,s57,106496
s_nop 0
buffer_load_dwordx4 v13,s[40:43],s55 offen offset:0  lds
s_add_u32 m0,s57,107520
s_nop 0
buffer_load_dwordx4 v14,s[40:43],s55 offen offset:0  lds
s_add_u32 m0,s57,114560
s_nop 0
buffer_load_dwordx4 v13,s[40:43],s55 offen offset:128  lds
s_add_u32 m0,s57,115584
s_nop 0
buffer_load_dwordx4 v14,s[40:43],s55 offen offset:128  lds
v_add_u32 v44,122880,v15
v_add_u32 v45,122880,v16
T a[80:81],44,0
T a[82:83],44,512
T a[84:85],45,0
T a[86:87],45,512
T a[88:89],44,4096
T a[90:91],44,4608
T a[92:93],45,4096
T a[94:95],45,4608
s_waitcnt vmcnt(20) lgkmcnt(0)
s_barrier
s_add_u32 s55,s50,1572864
s_add_u32 m0,s57,122880
s_nop 0
buffer_load_dwordx4 v13,s[40:43],s55 offen offset:0  lds
s_add_u32 m0,s57,123904
s_nop 0
buffer_load_dwordx4 v14,s[40:43],s55 offen offset:0  lds
s_add_u32 m0,s57,130944
s_nop 0
buffer_load_dwordx4 v13,s[40:43],s55 offen offset:128  lds
s_add_u32 m0,s57,131968
s_nop 0
buffer_load_dwordx4 v14,s[40:43],s55 offen offset:128  lds
v_add_u32 v44,139264,v15
v_add_u32 v45,139264,v16
T a[96:97],44,0
T a[98:99],44,512
T a[100:101],45,0
T a[102:103],45,512
T a[104:105],44,4096
T a[106:107],44,4608
T a[108:109],45,4096
T a[110:111],45,4608
s_waitcnt vmcnt(20) lgkmcnt(0)
s_barrier
s_add_u32 s55,s50,1703936
s_add_u32 m0,s57,139264
s_nop 0
buffer_load_dwordx4 v13,s[40:43],s55 offen offset:0  lds
s_add_u32 m0,s57,140288
s_nop 0
buffer_load_dwordx4 v14,s[40:43],s55 offen offset:0  lds
s_add_u32 m0,s57,147328
s_nop 0
buffer_load_dwordx4 v13,s[40:43],s55 offen offset:128  lds
s_add_u32 m0,s57,148352
s_nop 0
buffer_load_dwordx4 v14,s[40:43],s55 offen offset:128  lds
v_add_u32 v44,40960,v15
v_add_u32 v45,40960,v16
T a[112:113],44,0
T a[114:115],44,512
T a[116:117],45,0
T a[118:119],45,512
T a[120:121],44,4096
T a[122:123],44,4608
T a[124:125],45,4096
T a[126:127],45,4608
s_waitcnt vmcnt(20) lgkmcnt(0)
s_barrier
s_add_u32 s55,s50,1835008
s_add_u32 m0,s57,40960
s_nop 0
buffer_load_dwordx4 v13,s[40:43],s55 offen offset:0  lds
s_add_u32 m0,s57,41984
s_nop 0
buffer_load_dwordx4 v14,s[40:43],s55 offen offset:0  lds
s_add_u32 m0,s57,49024
s_nop 0
buffer_load_dwordx4 v13,s[40:43],s55 offen offset:128  lds
s_add_u32 m0,s57,50048
s_nop 0
buffer_load_dwordx4 v14,s[40:43],s55 offen offset:128  lds
v_add_u32 v44,57344,v15
v_add_u32 v45,57344,v16
T a[128:129],44,0
T a[130:131],44,512
T a[132:133],45,0
T a[134:135],45,512
T a[136:137],44,4096
T a[138:139],44,4608
T a[140:141],45,4096
T a[142:143],45,4608
s_waitcnt vmcnt(20) lgkmcnt(0)
s_barrier
s_add_u32 s55,s50,1966080
s_add_u32 m0,s57,57344
s_nop 0
buffer_load_dwordx4 v13,s[40:43],s55 offen offset:0  lds
s_add_u32 m0,s57,58368
s_nop 0
buffer_load_dwordx4 v14,s[40:43],s55 offen offset:0  lds
s_add_u32 m0,s57,65408
s_nop 0
buffer_load_dwordx4 v13,s[40:43],s55 offen offset:128  lds
s_add_u32 m0,s57,66432
s_nop 0
buffer_load_dwordx4 v14,s[40:43],s55 offen offset:128  lds
v_add_u32 v44,73728,v15
v_add_u32 v45,73728,v16
T a[144:145],44,0
T a[146:147],44,512
T a[148:149],45,0
T a[150:151],45,512
T a[152:153],44,4096
T a[154:155],44,4608
T a[156:157],45,4096
T a[158:159],45,4608
s_waitcnt vmcnt(20) lgkmcnt(0)
s_barrier
s_add_u32 s55,s50,2097152
s_add_u32 m0,s57,73728
s_nop 0
buffer_load_dwordx4 v13,s[40:43],s55 offen offset:0  lds
s_add_u32 m0,s57,74752
s_nop 0
buffer_load_dwordx4 v14,s[40:43],s55 offen offset:0  lds
s_add_u32 m0,s57,81792
s_nop 0
buffer_load_dwordx4 v13,s[40:43],s55 offen offset:128  lds
s_add_u32 m0,s57,82816
s_nop 0
buffer_load_dwordx4 v14,s[40:43],s55 offen offset:128  lds
v_add_u32 v44,90112,v15
v_add_u32 v45,90112,v16
T a[160:161],44,0
T a[162:163],44,512
T a[164:165],45,0
T a[166:167],45,512
T a[168:169],44,4096
T a[170:171],44,4608
T a[172:173],45,4096
T a[174:175],45,4608
s_waitcnt vmcnt(20) lgkmcnt(0)
s_barrier
s_add_u32 s55,s50,2228224
s_add_u32 m0,s57,90112
s_nop 0
buffer_load_dwordx4 v13,s[40:43],s55 offen offset:0  lds
s_add_u32 m0,s57,91136
)"
R"(s_nop 0
buffer_load_dwordx4 v14,s[40:43],s55 offen offset:0  lds
s_add_u32 m0,s57,98176
s_nop 0
buffer_load_dwordx4 v13,s[40:43],s55 offen offset:128  lds
s_add_u32 m0,s57,99200
s_nop 0
buffer_load_dwordx4 v14,s[40:43],s55 offen offset:128  lds
v_add_u32 v44,106496,v15
v_add_u32 v45,106496,v16
T a[176:177],44,0
T a[178:179],44,512
T a[180:181],45,0
T a[182:183],45,512
T a[184:185],44,4096
T a[186:187],44,4608
T a[188:189],45,4096
T a[190:191],45,4608
s_waitcnt vmcnt(20) lgkmcnt(0)
s_barrier
s_add_u32 s55,s50,2359296
s_add_u32 m0,s57,106496
s_nop 0
buffer_load_dwordx4 v13,s[40:43],s55 offen offset:0  lds
s_add_u32 m0,s57,107520
s_nop 0
buffer_load_dwordx4 v14,s[40:43],s55 offen offset:0  lds
s_add_u32 m0,s57,114560
s_nop 0
buffer_load_dwordx4 v13,s[40:43],s55 offen offset:128  lds
s_add_u32 m0,s57,115584
s_nop 0
buffer_load_dwordx4 v14,s[40:43],s55 offen offset:128  lds
v_add_u32 v44,122880,v15
v_add_u32 v45,122880,v16
T a[192:193],44,0
T a[194:195],44,512
T a[196:197],45,0
T a[198:199],45,512
T a[200:201],44,4096
T a[202:203],44,4608
T a[204:205],45,4096
T a[206:207],45,4608
s_waitcnt vmcnt(20) lgkmcnt(0)
s_barrier
s_add_u32 s55,s50,2490368
s_add_u32 m0,s57,122880
s_nop 0
buffer_load_dwordx4 v13,s[40:43],s55 offen offset:0  lds
s_add_u32 m0,s57,123904
s_nop 0
buffer_load_dwordx4 v14,s[40:43],s55 offen offset:0  lds
s_add_u32 m0,s57,130944
s_nop 0
buffer_load_dwordx4 v13,s[40:43],s55 offen offset:128  lds
s_add_u32 m0,s57,131968
s_nop 0
buffer_load_dwordx4 v14,s[40:43],s55 offen offset:128  lds
v_add_u32 v44,139264,v15
v_add_u32 v45,139264,v16
T a[208:209],44,0
T a[210:211],44,512
T a[212:213],45,0
T a[214:215],45,512
T a[216:217],44,4096
T a[218:219],44,4608
T a[220:221],45,4096
T a[222:223],45,4608
s_waitcnt vmcnt(20) lgkmcnt(0)
s_barrier
s_add_u32 s55,s50,2621440
s_add_u32 m0,s57,139264
s_nop 0
buffer_load_dwordx4 v13,s[40:43],s55 offen offset:0  lds
s_add_u32 m0,s57,140288
s_nop 0
buffer_load_dwordx4 v14,s[40:43],s55 offen offset:0  lds
s_add_u32 m0,s57,147328
s_nop 0
buffer_load_dwordx4 v13,s[40:43],s55 offen offset:128  lds
s_add_u32 m0,s57,148352
s_nop 0
buffer_load_dwordx4 v14,s[40:43],s55 offen offset:128  lds
v_add_u32 v44,40960,v15
v_add_u32 v45,40960,v16
T a[224:225],44,0
T a[226:227],44,512
T a[228:229],45,0
T a[230:231],45,512
T a[232:233],44,4096
T a[234:235],44,4608
T a[236:237],45,4096
T a[238:239],45,4608
s_waitcnt vmcnt(20) lgkmcnt(0)
s_barrier
s_add_u32 s55,s50,2752512
s_add_u32 m0,s57,40960
s_nop 0
buffer_load_dwordx4 v13,s[40:43],s55 offen offset:0  lds
s_add_u32 m0,s57,41984
s_nop 0
buffer_load_dwordx4 v14,s[40:43],s55 offen offset:0  lds
s_add_u32 m0,s57,49024
s_nop 0
buffer_load_dwordx4 v13,s[40:43],s55 offen offset:128  lds
s_add_u32 m0,s57,50048
s_nop 0
buffer_load_dwordx4 v14,s[40:43],s55 offen offset:128  lds
v_add_u32 v44,57344,v15
v_add_u32 v45,57344,v16
T a[240:241],44,0
T a[242:243],44,512
T a[244:245],45,0
T a[246:247],45,512
T a[248:249],44,4096
T a[250:251],44,4608
T a[252:253],45,4096
T a[254:255],45,4608
L 0
s_barrier
Lr_now_%=:
s_lshr_b32 s78,s45,1
s_and_b32 s79,s45,1
s_lshl_b32 s79,s79,7
s_lshl_b32 s55,s78,15
s_add_u32 s55,s55,s79
s_mul_i32 s56,s45,16384
s_add_u32 s56,s56,98566144
s_lshl_b32 s61,s78,11
s_add_u32 s61,s61,110587904
s_add_u32 s78,s45,s46
s_lshr_b32 s78,s78,1
s_lshl_b32 s62,s78,11
s_add_u32 s62,s62,110587904
Lr_bps_%=:
buffer_load_dword v18,v6,s[36:39],s61 offen offset:0 sc1
s_add_u32 s82,s82,1
s_waitcnt vmcnt(0)
v_cmp_eq_u32 vcc,s53,v18
s_nop 0
s_cmp_eq_u64 vcc,-1
s_cbranch_scc1 Lr_bqs_%=
s_sleep 2
s_cmp_lt_u32 s82,20000
s_cbranch_scc1 Lr_bps_%=
Lr_bqs_%=:
s_add_u32 s78,s55,71303168
buffer_load_dwordx4 v[164:167],v1,s[36:39],s78 offen offset:0 sc1
buffer_load_dwordx4 v[168:171],v1,s[36:39],s78 offen offset:2048 sc1
s_add_u32 s78,s55,71307264
buffer_load_dwordx4 v[172:175],v1,s[36:39],s78 offen offset:0 sc1
buffer_load_dwordx4 v[176:179],v1,s[36:39],s78 offen offset:2048 sc1
s_add_u32 s78,s55,89128960
buffer_load_dwordx4 v[180:183],v1,s[36:39],s78 offen offset:0 sc1
buffer_load_dwordx4 v[184:187],v1,s[36:39],s78 offen offset:2048 sc1
s_add_u32 s78,s55,89133056
buffer_load_dwordx4 v[188:191],v1,s[36:39],s78 offen offset:0 sc1
buffer_load_dwordx4 v[192:195],v1,s[36:39],s78 offen offset:2048 sc1
s_add_u32 m0,s65,0
s_nop 0
D 9,56,0
D 9,56,1024
D 9,56,2048
D 9,56,3072
buffer_load_dword v18,v6,s[36:39],s62 offen offset:0 sc1
s_lshr_b32 s78,s45,1
s_and_b32 s79,s45,1
s_lshl_b32 s79,s79,7
s_lshl_b32 s57,s78,15
s_add_u32 s57,s57,s79
s_add_u32 s57,s57,12582912
s_add_u32 s58,s57,4096
s_lshl_b32 s59,s78,9
s_add_u32 s59,s59,s79
s_add_u32 s59,s59,107020288
s_lshl_b32 s60,s45,9
s_add_u32 s60,s60,s66
s_add_u32 s60,s60,131072
s_add_u32 s45,s45,s46
s_lshr_b32 s78,s45,1
s_and_b32 s79,s45,1
s_lshl_b32 s79,s79,7
s_lshl_b32 s55,s78,15
s_add_u32 s55,s55,s79
s_mul_i32 s56,s45,16384
s_add_u32 s56,s56,98566144
s_lshl_b32 s61,s78,11
s_add_u32 s61,s61,110587904
s_add_u32 s78,s45,s46
s_lshr_b32 s78,s78,1
s_lshl_b32 s62,s78,11
s_add_u32 s62,s62,110587904
s_waitcnt vmcnt(0)
Lr_it0_%=:
T v[68:69],10,0
T v[70:71],10,64
T v[72:73],10,1024
T v[74:75],10,1088
T v[76:77],10,2048
T v[78:79],10,2112
T v[80:81],10,3072
T v[82:83],10,3136
s_nop 0
v_cmp_eq_u32 vcc,s53,v18
s_nop 0
s_cmp_eq_u64 vcc,-1
s_cselect_b32 s78,s45,256
s_cmp_lt_u32 s78,256
s_cbranch_scc1 Lr_ta0_%=
buffer_load_dword v18,v6,s[36:39],s61 offen offset:0 sc1
v_mfma_f32_16x16x32_bf16 v[116:119],v[26:29],v[164:167],0
L 6
v_lshlrev_b32 v84,16,v68
v_and_b32 v85,0xffff0000,v68
v_mfma_f32_16x16x32_bf16 v[120:123],v[24:27],v[164:167],0
L 4
v_lshlrev_b32 v86,16,v69
v_and_b32 v87,0xffff0000,v69
M 116,v[26:29],180
L 2
v_lshlrev_b32 v88,16,v70
v_and_b32 v89,0xffff0000,v70
M 120,v[24:27],180
L 0
v_lshlrev_b32 v90,16,v71
v_and_b32 v91,0xffff0000,v71
v_mfma_f32_16x16x32_bf16 v[124:127],v[26:29],v[168:171],0
v_lshlrev_b32 v92,16,v72
v_and_b32 v93,0xffff0000,v72
v_mfma_f32_16x16x32_bf16 v[128:131],v[24:27],v[168:171],0
v_lshlrev_b32 v94,16,v73
v_and_b32 v95,0xffff0000,v73
M 124,v[26:29],184
v_cvt_pk_bf16_f32 v148,v116,v117
v_cvt_pk_bf16_f32 v149,v118,v119
M 128,v[24:27],184
v_cvt_pk_bf16_f32 v150,v120,v121
v_cvt_pk_bf16_f32 v151,v122,v123
v_mfma_f32_16x16x32_bf16 v[132:135],v[26:29],v[172:175],0
v_lshlrev_b32 v96,16,v74
v_and_b32 v97,0xffff0000,v74
v_mfma_f32_16x16x32_bf16 v[136:139],v[24:27],v[172:175],0
v_lshlrev_b32 v98,16,v75
v_and_b32 v99,0xffff0000,v75
M 132,v[26:29],188
v_cvt_pk_bf16_f32 v152,v124,v125
v_cvt_pk_bf16_f32 v153,v126,v127
M 136,v[24:27],188
v_cvt_pk_bf16_f32 v154,v128,v129
v_cvt_pk_bf16_f32 v155,v130,v131
v_mfma_f32_16x16x32_bf16 v[140:143],v[26:29],v[176:179],0
v_lshlrev_b32 v100,16,v76
v_and_b32 v101,0xffff0000,v76
v_mfma_f32_16x16x32_bf16 v[144:147],v[24:27],v[176:179],0
v_lshlrev_b32 v102,16,v77
v_and_b32 v103,0xffff0000,v77
M 140,v[26:29],192
v_cvt_pk_bf16_f32 v156,v132,v133
v_cvt_pk_bf16_f32 v157,v134,v135
M 144,v[24:27],192
v_cvt_pk_bf16_f32 v158,v136,v137
v_cvt_pk_bf16_f32 v159,v138,v139
v_mfma_f32_16x16x32_bf16 v[36:39],v[32:35],v[148:151],0
v_lshlrev_b32 v104,16,v78
v_and_b32 v105,0xffff0000,v78
v_mfma_f32_16x16x32_bf16 v[40:43],v[148:151],v[148:151],0
v_lshlrev_b32 v106,16,v79
v_and_b32 v107,0xffff0000,v79
M 36,v[32:35],152
v_cvt_pk_bf16_f32 v160,v140,v141
v_cvt_pk_bf16_f32 v161,v142,v143
M 40,v[152:155],152
v_cvt_pk_bf16_f32 v162,v144,v145
v_cvt_pk_bf16_f32 v163,v146,v147
M 36,v[32:35],156
v_lshlrev_b32 v108,16,v80
v_and_b32 v109,0xffff0000,v80
M 40,v[156:159],156
v_lshlrev_b32 v110,16,v81
v_and_b32 v111,0xffff0000,v81
M 36,v[32:35],160
v_lshlrev_b32 v112,16,v82
v_and_b32 v113,0xffff0000,v82
M 40,v[160:163],160
v_lshlrev_b32 v114,16,v83
v_and_b32 v115,0xffff0000,v83
s_nop 2
v_add_f32_dpp v44,v36,v36 row_ror:8 row_mask:0xf bank_mask:0xf
s_nop 1
v_cndmask_b32 v45,v40,v41,s[70:71]
v_cndmask_b32 v46,v42,v43,s[70:71]
ds_write_b32 v2,v44 offset:0
v_cndmask_b32 v45,v45,v46,s[72:73]
ds_write_b32 v0,v45 offset:0
L 0
s_barrier
R 52,3,0
R 56,3,16
R 60,3,32
L 0
v_pk_add_f32 v[52:53],v[52:53],v[54:55]
v_pk_add_f32 v[56:57],v[56:57],v[58:59]
v_pk_add_f32 v[60:61],v[60:61],v[62:63]
v_add_f32 v52,v52,v53
v_pk_add_f32 v[56:57],v[56:57],v[60:61]
v_mul_f32 v52,s68,v52
v_add_f32 v53,v56,v57
v_mul_f32 v53,s68,v53
v_fma_f32 v53,-v52,v52,v53
v_max_f32 v53,0,v53
s_waitcnt vmcnt(1)
buffer_store_dword v17,v5,s[36:39],s63 offen offset:0 sc0 sc1
s_mov_b32 s63,0x7fff0000
s_waitcnt vmcnt(1)
v_cmp_eq_u32 vcc,s53,v18
s_nop 0
s_cmp_eq_u64 vcc,-1
s_cselect_b32 s78,s45,256
s_cmp_lt_u32 s78,256
s_cbranch_scc1 Lr_tb0_%=
buffer_load_dword v18,v6,s[36:39],s61 offen offset:0 sc1
v_add_f32 v53,s69,v53
v_rsq_f32 v48,v53
s_nop 0
v_mul_f32_e64 v50,-v52,v48
v_mov_b32 v49,v48
v_mov_b32 v51,v50
v_pk_fma_f32 v[52:53],v[116:117],v[48:49],v[50:51]
v_pk_fma_f32 v[54:55],v[118:119],v[48:49],v[50:51]
v_pk_fma_f32 v[56:57],v[120:121],v[48:49],v[50:51]
v_pk_fma_f32 v[58:59],v[122:123],v[48:49],v[50:51]
v_pk_fma_f32 v[116:117],v[52:53],v[84:85],v[116:117]
v_pk_fma_f32 v[118:119],v[54:55],v[86:87],v[118:119]
v_pk_fma_f32 v[120:121],v[56:57],v[88:89],v[120:121]
v_pk_fma_f32 v[122:123],v[58:59],v[90:91],v[122:123]
v_cvt_pk_bf16_f32 v148,v116,v117
v_cvt_pk_bf16_f32 v149,v118,v119
v_cvt_pk_bf16_f32 v150,v120,v121
v_cvt_pk_bf16_f32 v151,v122,v123
v_pk_fma_f32 v[52:53],v[124:125],v[48:49],v[50:51]
v_pk_fma_f32 v[54:55],v[126:127],v[48:49],v[50:51]
v_pk_fma_f32 v[56:57],v[128:129],v[48:49],v[50:51]
v_pk_fma_f32 v[58:59],v[130:131],v[48:49],v[50:51]
v_pk_fma_f32 v[124:125],v[52:53],v[92:93],v[124:125]
v_pk_fma_f32 v[126:127],v[54:55],v[94:95],v[126:127]
v_pk_fma_f32 v[128:129],v[56:57],v[96:97],v[128:129]
v_pk_fma_f32 v[130:131],v[58:59],v[98:99],v[130:131]
v_cvt_pk_bf16_f32 v152,v124,v125
v_cvt_pk_bf16_f32 v153,v126,v127
v_cvt_pk_bf16_f32 v154,v128,v129
v_cvt_pk_bf16_f32 v155,v130,v131
v_pk_fma_f32 v[52:53],v[132:133],v[48:49],v[50:51]
v_pk_fma_f32 v[54:55],v[134:135],v[48:49],v[50:51]
v_pk_fma_f32 v[56:57],v[136:137],v[48:49],v[50:51]
v_pk_fma_f32 v[58:59],v[138:139],v[48:49],v[50:51]
v_pk_fma_f32 v[132:133],v[52:53],v[100:101],v[132:133]
v_pk_fma_f32 v[134:135],v[54:55],v[102:103],v[134:135]
v_pk_fma_f32 v[136:137],v[56:57],v[104:105],v[136:137]
v_pk_fma_f32 v[138:139],v[58:59],v[106:107],v[138:139]
v_cvt_pk_bf16_f32 v156,v132,v133
v_cvt_pk_bf16_f32 v157,v134,v135
v_cvt_pk_bf16_f32 v158,v136,v137
v_cvt_pk_bf16_f32 v159,v138,v139
v_pk_fma_f32 v[52:53],v[140:141],v[48:49],v[50:51]
v_pk_fma_f32 v[54:55],v[142:143],v[48:49],v[50:51]
v_pk_fma_f32 v[56:57],v[144:145],v[48:49],v[50:51]
v_pk_fma_f32 v[58:59],v[146:147],v[48:49],v[50:51]
v_pk_fma_f32 v[140:141],v[52:53],v[108:109],v[140:141]
v_pk_fma_f32 v[142:143],v[54:55],v[110:111],v[142:143]
v_pk_fma_f32 v[144:145],v[56:57],v[112:113],v[144:145]
v_pk_fma_f32 v[146:147],v[58:59],v[114:115],v[146:147]
v_cvt_pk_bf16_f32 v160,v140,v141
v_cvt_pk_bf16_f32 v161,v142,v143
v_cvt_pk_bf16_f32 v162,v144,v145
v_cvt_pk_bf16_f32 v163,v146,v147
v_mfma_f32_16x16x32_bf16 v[36:39],v[32:35],v[148:151],0
buffer_store_dwordx4 v[148:151],v1,s[36:39],s57 offen offset:0 sc0 sc1
v_mfma_f32_16x16x32_bf16 v[40:43],v[148:151],v[148:151],0
M 36,v[32:35],152
buffer_store_dwordx4 v[152:155],v1,s[36:39],s57 offen offset:2048 sc0 sc1
M 40,v[152:155],152
M 36,v[32:35],156
buffer_store_dwordx4 v[156:159],v1,s[36:39],s58 offen offset:0 sc0 sc1
M 40,v[156:159],156
M 36,v[32:35],160
buffer_store_dwordx4 v[160:163],v1,s[36:39],s58 offen offset:2048 sc0 sc1
M 40,v[160:163],160
s_nop 5
v_add_f32_dpp v44,v36,v36 row_ror:8 row_mask:0xf bank_mask:0xf
s_nop 0
v_cndmask_b32 v45,v40,v41,s[70:71]
v_cndmask_b32 v46,v42,v43,s[70:71]
ds_write_b32 v2,v44 offset:1024
v_cndmask_b32 v45,v45,v46,s[72:73]
ds_write_b32 v0,v45 offset:1024
L 0
s_barrier
R 52,3,1024
R 56,3,1040
R 60,3,1056
L 0
v_pk_add_f32 v[52:53],v[52:53],v[54:55]
v_pk_add_f32 v[56:57],v[56:57],v[58:59]
v_pk_add_f32 v[60:61],v[60:61],v[62:63]
v_add_f32 v52,v52,v53
v_pk_add_f32 v[56:57],v[56:57],v[60:61]
v_mul_f32 v52,s68,v52
v_add_f32 v53,v56,v57
v_mul_f32 v53,s68,v53
v_fma_f32 v53,-v52,v52,v53
v_max_f32 v53,0,v53
v_add_f32 v54,s69,v53
v_fma_f32 v55,s69,v54,v53
v_rsq_f32 v65,v54
v_rsq_f32 v54,v55
v_mul_f32_e64 v64,-v52,v65
v_mul_f32 v63,v55,v54
v_mul_f32 v61,s76,v54
v_mul_f32 v55,v54,v54
v_mul_f32 v62,s77,v55
v_mul_f32 v62,v62,v61
v_cndmask_b32 v60,v52,v64,s[74:75]
v_cndmask_b32 v61,v61,v65,s[74:75]
buffer_store_dwordx4 v[60:63],v4,s[36:39],s59 offen offset:0 sc0 sc1
s_mov_b32 s63,s60
s_waitcnt vmcnt(5)
v_cmp_eq_u32 vcc,s53,v18
s_nop 0
s_cmp_eq_u64 vcc,-1
s_cselect_b32 s78,s45,256
s_cmp_lt_u32 s78,256
s_cbranch_scc1 Lr_tc0_%=
s_waitcnt vmcnt(0)
buffer_store_dword v17,v5,s[36:39],s63 offen offset:0 sc0 sc1
s_mov_b32 s63,0x7fff0000
s_cmp_ge_u32 s45,256
s_cbranch_scc1 Lr_done_%=
Lr_bp0_%=:
buffer_load_dword v18,v6,s[36:39],s61 offen offset:0 sc1
s_add_u32 s82,s82,1
s_waitcnt vmcnt(0)
v_cmp_eq_u32 vcc,s53,v18
s_nop 0
s_cmp_eq_u64 vcc,-1
s_cbranch_scc1 Lr_bq0_%=
s_sleep 2
s_cmp_lt_u32 s82,20000
s_cbranch_scc1 Lr_bp0_%=
Lr_bq0_%=:
s_add_u32 s78,s55,71303168
buffer_load_dwordx4 v[196:199],v1,s[36:39],s78 offen offset:0 sc1
buffer_load_dwordx4 v[200:203],v1,s[36:39],s78 offen offset:2048 sc1
s_add_u32 s78,s55,71307264
buffer_load_dwordx4 v[204:207],v1,s[36:39],s78 offen offset:0 sc1
buffer_load_dwordx4 v[208:211],v1,s[36:39],s78 offen offset:2048 sc1
s_add_u32 s78,s55,89128960
buffer_load_dwordx4 v[212:215],v1,s[36:39],s78 offen offset:0 sc1
buffer_load_dwordx4 v[216:219],v1,s[36:39],s78 offen offset:2048 sc1
s_add_u32 s78,s55,89133056
buffer_load_dwordx4 v[220:223],v1,s[36:39],s78 offen offset:0 sc1
buffer_load_dwordx4 v[224:227],v1,s[36:39],s78 offen offset:2048 sc1
s_add_u32 m0,s65,16384
s_nop 0
D 9,56,0
D 9,56,1024
D 9,56,2048
D 9,56,3072
buffer_load_dword v18,v6,s[36:39],s62 offen offset:0 sc1
s_lshr_b32 s78,s45,1
s_and_b32 s79,s45,1
s_lshl_b32 s79,s79,7
s_lshl_b32 s57,s78,15
s_add_u32 s57,s57,s79
s_add_u32 s57,s57,12582912
s_add_u32 s58,s57,4096
s_lshl_b32 s59,s78,9
s_add_u32 s59,s59,s79
s_add_u32 s59,s59,107020288
s_lshl_b32 s60,s45,9
s_add_u32 s60,s60,s66
s_add_u32 s60,s60,131072
s_add_u32 s45,s45,s46
s_lshr_b32 s78,s45,1
s_and_b32 s79,s45,1
s_lshl_b32 s79,s79,7
s_lshl_b32 s55,s78,15
s_add_u32 s55,s55,s79
s_mul_i32 s56,s45,16384
s_add_u32 s56,s56,98566144
s_lshl_b32 s61,s78,11
s_add_u32 s61,s61,110587904
s_add_u32 s78,s45,s46
s_lshr_b32 s78,s78,1
s_lshl_b32 s62,s78,11
s_add_u32 s62,s62,110587904
s_waitcnt vmcnt(0)
s_branch Lr_it1_%=
Lr_ta0_%=:
v_mfma_f32_16x16x32_bf16 v[116:119],v[26:29],v[164:167],0
s_add_u32 s78,s55,71303168
)"
R"(L 6
L 4
v_mfma_f32_16x16x32_bf16 v[120:123],v[24:27],v[164:167],0
L 2
buffer_load_dwordx4 v[196:199],v1,s[36:39],s78 offen offset:0 sc1
M 116,v[26:29],180
L 0
buffer_load_dwordx4 v[200:203],v1,s[36:39],s78 offen offset:2048 sc1
M 120,v[24:27],180
s_add_u32 s78,s55,71307264
v_lshlrev_b32 v84,16,v68
v_and_b32 v85,0xffff0000,v68
v_mfma_f32_16x16x32_bf16 v[124:127],v[26:29],v[168:171],0
buffer_load_dwordx4 v[204:207],v1,s[36:39],s78 offen offset:0 sc1
v_mfma_f32_16x16x32_bf16 v[128:131],v[24:27],v[168:171],0
buffer_load_dwordx4 v[208:211],v1,s[36:39],s78 offen offset:2048 sc1
M 124,v[26:29],184
s_add_u32 s78,s55,89128960
v_cvt_pk_bf16_f32 v148,v116,v117
v_cvt_pk_bf16_f32 v149,v118,v119
M 128,v[24:27],184
buffer_load_dwordx4 v[212:215],v1,s[36:39],s78 offen offset:0 sc1
v_mfma_f32_16x16x32_bf16 v[132:135],v[26:29],v[172:175],0
buffer_load_dwordx4 v[216:219],v1,s[36:39],s78 offen offset:2048 sc1
v_mfma_f32_16x16x32_bf16 v[136:139],v[24:27],v[172:175],0
s_add_u32 s78,s55,89133056
v_cvt_pk_bf16_f32 v150,v120,v121
v_cvt_pk_bf16_f32 v151,v122,v123
M 132,v[26:29],188
buffer_load_dwordx4 v[220:223],v1,s[36:39],s78 offen offset:0 sc1
M 136,v[24:27],188
buffer_load_dwordx4 v[224:227],v1,s[36:39],s78 offen offset:2048 sc1
v_mfma_f32_16x16x32_bf16 v[140:143],v[26:29],v[176:179],0
s_add_u32 m0,s65,16384
v_cvt_pk_bf16_f32 v152,v124,v125
v_cvt_pk_bf16_f32 v153,v126,v127
v_mfma_f32_16x16x32_bf16 v[144:147],v[24:27],v[176:179],0
D 9,56,0
M 140,v[26:29],192
D 9,56,1024
M 144,v[24:27],192
D 9,56,2048
v_mfma_f32_16x16x32_bf16 v[36:39],v[32:35],v[148:151],0
v_cvt_pk_bf16_f32 v154,v128,v129
v_cvt_pk_bf16_f32 v155,v130,v131
v_mfma_f32_16x16x32_bf16 v[40:43],v[148:151],v[148:151],0
D 9,56,3072
M 36,v[32:35],152
v_cvt_pk_bf16_f32 v156,v132,v133
v_cvt_pk_bf16_f32 v157,v134,v135
v_cvt_pk_bf16_f32 v158,v136,v137
v_cvt_pk_bf16_f32 v159,v138,v139
M 40,v[152:155],152
v_cvt_pk_bf16_f32 v160,v140,v141
v_cvt_pk_bf16_f32 v161,v142,v143
M 36,v[32:35],156
v_cvt_pk_bf16_f32 v162,v144,v145
v_cvt_pk_bf16_f32 v163,v146,v147
M 40,v[156:159],156
v_lshlrev_b32 v86,16,v69
v_and_b32 v87,0xffff0000,v69
M 36,v[32:35],160
v_lshlrev_b32 v88,16,v70
v_and_b32 v89,0xffff0000,v70
M 40,v[160:163],160
v_lshlrev_b32 v90,16,v71
v_and_b32 v91,0xffff0000,v71
v_lshlrev_b32 v92,16,v72
v_and_b32 v93,0xffff0000,v72
v_lshlrev_b32 v94,16,v73
v_and_b32 v95,0xffff0000,v73
v_lshlrev_b32 v96,16,v74
v_and_b32 v97,0xffff0000,v74
v_lshlrev_b32 v98,16,v75
v_and_b32 v99,0xffff0000,v75
v_lshlrev_b32 v100,16,v76
v_and_b32 v101,0xffff0000,v76
v_lshlrev_b32 v102,16,v77
v_and_b32 v103,0xffff0000,v77
v_lshlrev_b32 v104,16,v78
v_and_b32 v105,0xffff0000,v78
v_lshlrev_b32 v106,16,v79
v_and_b32 v107,0xffff0000,v79
v_lshlrev_b32 v108,16,v80
v_and_b32 v109,0xffff0000,v80
v_lshlrev_b32 v110,16,v81
v_and_b32 v111,0xffff0000,v81
v_lshlrev_b32 v112,16,v82
v_and_b32 v113,0xffff0000,v82
v_lshlrev_b32 v114,16,v83
v_and_b32 v115,0xffff0000,v83
v_add_f32_dpp v44,v36,v36 row_ror:8 row_mask:0xf bank_mask:0xf
v_cndmask_b32 v45,v40,v41,s[70:71]
v_cndmask_b32 v46,v42,v43,s[70:71]
ds_write_b32 v2,v44 offset:0
v_cndmask_b32 v45,v45,v46,s[72:73]
ds_write_b32 v0,v45 offset:0
L 0
s_barrier
R 52,3,0
R 56,3,16
R 60,3,32
L 0
v_pk_add_f32 v[52:53],v[52:53],v[54:55]
v_pk_add_f32 v[56:57],v[56:57],v[58:59]
v_pk_add_f32 v[60:61],v[60:61],v[62:63]
v_add_f32 v52,v52,v53
v_pk_add_f32 v[56:57],v[56:57],v[60:61]
v_mul_f32 v52,s68,v52
v_add_f32 v53,v56,v57
v_mul_f32 v53,s68,v53
v_fma_f32 v53,-v52,v52,v53
v_max_f32 v53,0,v53
buffer_load_dword v18,v6,s[36:39],s62 offen offset:0 sc1
v_add_f32 v53,s69,v53
v_rsq_f32 v48,v53
s_nop 0
v_mul_f32_e64 v50,-v52,v48
v_mov_b32 v49,v48
v_mov_b32 v51,v50
v_pk_fma_f32 v[52:53],v[116:117],v[48:49],v[50:51]
v_pk_fma_f32 v[54:55],v[118:119],v[48:49],v[50:51]
v_pk_fma_f32 v[56:57],v[120:121],v[48:49],v[50:51]
v_pk_fma_f32 v[58:59],v[122:123],v[48:49],v[50:51]
v_pk_fma_f32 v[116:117],v[52:53],v[84:85],v[116:117]
v_pk_fma_f32 v[118:119],v[54:55],v[86:87],v[118:119]
v_pk_fma_f32 v[120:121],v[56:57],v[88:89],v[120:121]
v_pk_fma_f32 v[122:123],v[58:59],v[90:91],v[122:123]
v_cvt_pk_bf16_f32 v148,v116,v117
v_cvt_pk_bf16_f32 v149,v118,v119
v_cvt_pk_bf16_f32 v150,v120,v121
v_cvt_pk_bf16_f32 v151,v122,v123
v_pk_fma_f32 v[52:53],v[124:125],v[48:49],v[50:51]
v_pk_fma_f32 v[54:55],v[126:127],v[48:49],v[50:51]
v_pk_fma_f32 v[56:57],v[128:129],v[48:49],v[50:51]
v_pk_fma_f32 v[58:59],v[130:131],v[48:49],v[50:51]
v_pk_fma_f32 v[124:125],v[52:53],v[92:93],v[124:125]
v_pk_fma_f32 v[126:127],v[54:55],v[94:95],v[126:127]
v_pk_fma_f32 v[128:129],v[56:57],v[96:97],v[128:129]
v_pk_fma_f32 v[130:131],v[58:59],v[98:99],v[130:131]
v_cvt_pk_bf16_f32 v152,v124,v125
v_cvt_pk_bf16_f32 v153,v126,v127
v_cvt_pk_bf16_f32 v154,v128,v129
v_cvt_pk_bf16_f32 v155,v130,v131
v_pk_fma_f32 v[52:53],v[132:133],v[48:49],v[50:51]
v_pk_fma_f32 v[54:55],v[134:135],v[48:49],v[50:51]
v_pk_fma_f32 v[56:57],v[136:137],v[48:49],v[50:51]
v_pk_fma_f32 v[58:59],v[138:139],v[48:49],v[50:51]
v_pk_fma_f32 v[132:133],v[52:53],v[100:101],v[132:133]
v_pk_fma_f32 v[134:135],v[54:55],v[102:103],v[134:135]
v_pk_fma_f32 v[136:137],v[56:57],v[104:105],v[136:137]
v_pk_fma_f32 v[138:139],v[58:59],v[106:107],v[138:139]
v_cvt_pk_bf16_f32 v156,v132,v133
v_cvt_pk_bf16_f32 v157,v134,v135
v_cvt_pk_bf16_f32 v158,v136,v137
v_cvt_pk_bf16_f32 v159,v138,v139
v_pk_fma_f32 v[52:53],v[140:141],v[48:49],v[50:51]
v_pk_fma_f32 v[54:55],v[142:143],v[48:49],v[50:51]
v_pk_fma_f32 v[56:57],v[144:145],v[48:49],v[50:51]
v_pk_fma_f32 v[58:59],v[146:147],v[48:49],v[50:51]
v_pk_fma_f32 v[140:141],v[52:53],v[108:109],v[140:141]
v_pk_fma_f32 v[142:143],v[54:55],v[110:111],v[142:143]
v_pk_fma_f32 v[144:145],v[56:57],v[112:113],v[144:145]
v_pk_fma_f32 v[146:147],v[58:59],v[114:115],v[146:147]
v_cvt_pk_bf16_f32 v160,v140,v141
v_cvt_pk_bf16_f32 v161,v142,v143
v_cvt_pk_bf16_f32 v162,v144,v145
v_cvt_pk_bf16_f32 v163,v146,v147
s_waitcnt vmcnt(13)
buffer_store_dword v17,v5,s[36:39],s63 offen offset:0 sc0 sc1
s_mov_b32 s63,0x7fff0000
v_mfma_f32_16x16x32_bf16 v[36:39],v[32:35],v[148:151],0
buffer_store_dwordx4 v[148:151],v1,s[36:39],s57 offen offset:0 sc0 sc1
v_mfma_f32_16x16x32_bf16 v[40:43],v[148:151],v[148:151],0
M 36,v[32:35],152
buffer_store_dwordx4 v[152:155],v1,s[36:39],s57 offen offset:2048 sc0 sc1
M 40,v[152:155],152
M 36,v[32:35],156
buffer_store_dwordx4 v[156:159],v1,s[36:39],s58 offen offset:0 sc0 sc1
M 40,v[156:159],156
M 36,v[32:35],160
buffer_store_dwordx4 v[160:163],v1,s[36:39],s58 offen offset:2048 sc0 sc1
M 40,v[160:163],160
s_nop 5
v_add_f32_dpp v44,v36,v36 row_ror:8 row_mask:0xf bank_mask:0xf
s_nop 0
v_cndmask_b32 v45,v40,v41,s[70:71]
v_cndmask_b32 v46,v42,v43,s[70:71]
ds_write_b32 v2,v44 offset:1024
v_cndmask_b32 v45,v45,v46,s[72:73]
ds_write_b32 v0,v45 offset:1024
L 0
s_barrier
R 52,3,1024
R 56,3,1040
R 60,3,1056
L 0
v_pk_add_f32 v[52:53],v[52:53],v[54:55]
v_pk_add_f32 v[56:57],v[56:57],v[58:59]
v_pk_add_f32 v[60:61],v[60:61],v[62:63]
v_add_f32 v52,v52,v53
v_pk_add_f32 v[56:57],v[56:57],v[60:61]
v_mul_f32 v52,s68,v52
v_add_f32 v53,v56,v57
v_mul_f32 v53,s68,v53
v_fma_f32 v53,-v52,v52,v53
v_max_f32 v53,0,v53
v_add_f32 v54,s69,v53
v_fma_f32 v55,s69,v54,v53
v_rsq_f32 v65,v54
v_rsq_f32 v54,v55
v_mul_f32_e64 v64,-v52,v65
v_mul_f32 v63,v55,v54
v_mul_f32 v61,s76,v54
v_mul_f32 v55,v54,v54
v_mul_f32 v62,s77,v55
v_mul_f32 v62,v62,v61
v_cndmask_b32 v60,v52,v64,s[74:75]
v_cndmask_b32 v61,v61,v65,s[74:75]
buffer_store_dwordx4 v[60:63],v4,s[36:39],s59 offen offset:0 sc0 sc1
s_mov_b32 s63,s60
s_lshr_b32 s78,s45,1
s_and_b32 s79,s45,1
s_lshl_b32 s79,s79,7
s_lshl_b32 s57,s78,15
s_add_u32 s57,s57,s79
s_add_u32 s57,s57,12582912
s_add_u32 s58,s57,4096
s_lshl_b32 s59,s78,9
s_add_u32 s59,s59,s79
s_add_u32 s59,s59,107020288
s_lshl_b32 s60,s45,9
s_add_u32 s60,s60,s66
s_add_u32 s60,s60,131072
s_add_u32 s45,s45,s46
s_lshr_b32 s78,s45,1
s_and_b32 s79,s45,1
s_lshl_b32 s79,s79,7
s_lshl_b32 s55,s78,15
s_add_u32 s55,s55,s79
s_mul_i32 s56,s45,16384
s_add_u32 s56,s56,98566144
s_lshl_b32 s61,s78,11
s_add_u32 s61,s61,110587904
s_add_u32 s78,s45,s46
s_lshr_b32 s78,s78,1
s_lshl_b32 s62,s78,11
s_add_u32 s62,s62,110587904
s_waitcnt vmcnt(6)
s_branch Lr_it1_%=
Lr_tb0_%=:
v_add_f32 v53,s69,v53
v_rsq_f32 v48,v53
s_nop 0
v_mul_f32_e64 v50,-v52,v48
v_mov_b32 v49,v48
v_mov_b32 v51,v50
v_pk_fma_f32 v[52:53],v[116:117],v[48:49],v[50:51]
v_pk_fma_f32 v[54:55],v[118:119],v[48:49],v[50:51]
v_pk_fma_f32 v[56:57],v[120:121],v[48:49],v[50:51]
v_pk_fma_f32 v[58:59],v[122:123],v[48:49],v[50:51]
s_add_u32 s78,s55,71303168
buffer_load_dwordx4 v[196:199],v1,s[36:39],s78 offen offset:0 sc1
buffer_load_dwordx4 v[200:203],v1,s[36:39],s78 offen offset:2048 sc1
s_add_u32 s78,s55,71307264
buffer_load_dwordx4 v[204:207],v1,s[36:39],s78 offen offset:0 sc1
buffer_load_dwordx4 v[208:211],v1,s[36:39],s78 offen offset:2048 sc1
v_pk_fma_f32 v[116:117],v[52:53],v[84:85],v[116:117]
v_pk_fma_f32 v[118:119],v[54:55],v[86:87],v[118:119]
v_pk_fma_f32 v[120:121],v[56:57],v[88:89],v[120:121]
v_pk_fma_f32 v[122:123],v[58:59],v[90:91],v[122:123]
v_cvt_pk_bf16_f32 v148,v116,v117
v_cvt_pk_bf16_f32 v149,v118,v119
v_cvt_pk_bf16_f32 v150,v120,v121
v_cvt_pk_bf16_f32 v151,v122,v123
v_pk_fma_f32 v[52:53],v[124:125],v[48:49],v[50:51]
v_pk_fma_f32 v[54:55],v[126:127],v[48:49],v[50:51]
v_pk_fma_f32 v[56:57],v[128:129],v[48:49],v[50:51]
v_pk_fma_f32 v[58:59],v[130:131],v[48:49],v[50:51]
s_add_u32 s78,s55,89128960
buffer_load_dwordx4 v[212:215],v1,s[36:39],s78 offen offset:0 sc1
buffer_load_dwordx4 v[216:219],v1,s[36:39],s78 offen offset:2048 sc1
s_add_u32 s78,s55,89133056
buffer_load_dwordx4 v[220:223],v1,s[36:39],s78 offen offset:0 sc1
buffer_load_dwordx4 v[224:227],v1,s[36:39],s78 offen offset:2048 sc1
v_pk_fma_f32 v[124:125],v[52:53],v[92:93],v[124:125]
v_pk_fma_f32 v[126:127],v[54:55],v[94:95],v[126:127]
v_pk_fma_f32 v[128:129],v[56:57],v[96:97],v[128:129]
v_pk_fma_f32 v[130:131],v[58:59],v[98:99],v[130:131]
v_cvt_pk_bf16_f32 v152,v124,v125
v_cvt_pk_bf16_f32 v153,v126,v127
v_cvt_pk_bf16_f32 v154,v128,v129
v_cvt_pk_bf16_f32 v155,v130,v131
v_pk_fma_f32 v[52:53],v[132:133],v[48:49],v[50:51]
v_pk_fma_f32 v[54:55],v[134:135],v[48:49],v[50:51]
v_pk_fma_f32 v[56:57],v[136:137],v[48:49],v[50:51]
v_pk_fma_f32 v[58:59],v[138:139],v[48:49],v[50:51]
s_add_u32 m0,s65,16384
s_nop 0
D 9,56,0
D 9,56,1024
D 9,56,2048
D 9,56,3072
v_pk_fma_f32 v[132:133],v[52:53],v[100:101],v[132:133]
v_pk_fma_f32 v[134:135],v[54:55],v[102:103],v[134:135]
v_pk_fma_f32 v[136:137],v[56:57],v[104:105],v[136:137]
v_pk_fma_f32 v[138:139],v[58:59],v[106:107],v[138:139]
v_cvt_pk_bf16_f32 v156,v132,v133
v_cvt_pk_bf16_f32 v157,v134,v135
v_cvt_pk_bf16_f32 v158,v136,v137
v_cvt_pk_bf16_f32 v159,v138,v139
v_pk_fma_f32 v[52:53],v[140:141],v[48:49],v[50:51]
v_pk_fma_f32 v[54:55],v[142:143],v[48:49],v[50:51]
v_pk_fma_f32 v[56:57],v[144:145],v[48:49],v[50:51]
v_pk_fma_f32 v[58:59],v[146:147],v[48:49],v[50:51]
v_pk_fma_f32 v[140:141],v[52:53],v[108:109],v[140:141]
v_pk_fma_f32 v[142:143],v[54:55],v[110:111],v[142:143]
v_pk_fma_f32 v[144:145],v[56:57],v[112:113],v[144:145]
v_pk_fma_f32 v[146:147],v[58:59],v[114:115],v[146:147]
v_cvt_pk_bf16_f32 v160,v140,v141
v_cvt_pk_bf16_f32 v161,v142,v143
v_cvt_pk_bf16_f32 v162,v144,v145
v_cvt_pk_bf16_f32 v163,v146,v147
v_mfma_f32_16x16x32_bf16 v[36:39],v[32:35],v[148:151],0
buffer_load_dword v18,v6,s[36:39],s62 offen offset:0 sc1
buffer_store_dwordx4 v[148:151],v1,s[36:39],s57 offen offset:0 sc0 sc1
v_mfma_f32_16x16x32_bf16 v[40:43],v[148:151],v[148:151],0
M 36,v[32:35],152
buffer_store_dwordx4 v[152:155],v1,s[36:39],s57 offen offset:2048 sc0 sc1
M 40,v[152:155],152
M 36,v[32:35],156
buffer_store_dwordx4 v[156:159],v1,s[36:39],s58 offen offset:0 sc0 sc1
M 40,v[156:159],156
M 36,v[32:35],160
buffer_store_dwordx4 v[160:163],v1,s[36:39],s58 offen offset:2048 sc0 sc1
M 40,v[160:163],160
s_nop 5
v_add_f32_dpp v44,v36,v36 row_ror:8 row_mask:0xf bank_mask:0xf
s_nop 0
v_cndmask_b32 v45,v40,v41,s[70:71]
v_cndmask_b32 v46,v42,v43,s[70:71]
ds_write_b32 v2,v44 offset:1024
v_cndmask_b32 v45,v45,v46,s[72:73]
ds_write_b32 v0,v45 offset:1024
L 0
s_barrier
R 52,3,1024
R 56,3,1040
R 60,3,1056
L 0
v_pk_add_f32 v[52:53],v[52:53],v[54:55]
v_pk_add_f32 v[56:57],v[56:57],v[58:59]
v_pk_add_f32 v[60:61],v[60:61],v[62:63]
v_add_f32 v52,v52,v53
v_pk_add_f32 v[56:57],v[56:57],v[60:61]
v_mul_f32 v52,s68,v52
v_add_f32 v53,v56,v57
v_mul_f32 v53,s68,v53
v_fma_f32 v53,-v52,v52,v53
v_max_f32 v53,0,v53
v_add_f32 v54,s69,v53
v_fma_f32 v55,s69,v54,v53
v_rsq_f32 v65,v54
v_rsq_f32 v54,v55
v_mul_f32_e64 v64,-v52,v65
v_mul_f32 v63,v55,v54
v_mul_f32 v61,s76,v54
v_mul_f32 v55,v54,v54
v_mul_f32 v62,s77,v55
v_mul_f32 v62,v62,v61
v_cndmask_b32 v60,v52,v64,s[74:75]
v_cndmask_b32 v61,v61,v65,s[74:75]
buffer_store_dwordx4 v[60:63],v4,s[36:39],s59 offen offset:0 sc0 sc1
s_mov_b32 s63,s60
s_lshr_b32 s78,s45,1
s_and_b32 s79,s45,1
s_lshl_b32 s79,s79,7
s_lshl_b32 s57,s78,15
s_add_u32 s57,s57,s79
s_add_u32 s57,s57,12582912
s_add_u32 s58,s57,4096
s_lshl_b32 s59,s78,9
s_add_u32 s59,s59,s79
s_add_u32 s59,s59,107020288
s_lshl_b32 s60,s45,9
s_add_u32 s60,s60,s66
s_add_u32 s60,s60,131072
s_add_u32 s45,s45,s46
s_lshr_b32 s78,s45,1
s_and_b32 s79,s45,1
s_lshl_b32 s79,s79,7
s_lshl_b32 s55,s78,15
s_add_u32 s55,s55,s79
s_mul_i32 s56,s45,16384
s_add_u32 s56,s56,98566144
s_lshl_b32 s61,s78,11
s_add_u32 s61,s61,110587904
s_add_u32 s78,s45,s46
s_lshr_b32 s78,s78,1
s_lshl_b32 s62,s78,11
s_add_u32 s62,s62,110587904
s_waitcnt vmcnt(5)
s_branch Lr_it1_%=
Lr_tc0_%=:
s_add_u32 s78,s55,71303168
buffer_load_dwordx4 v[196:199],v1,s[36:39],s78 offen offset:0 sc1
buffer_load_dwordx4 v[200:203],v1,s[36:39],s78 offen offset:2048 sc1
s_add_u32 s78,s55,71307264
buffer_load_dwordx4 v[204:207],v1,s[36:39],s78 offen offset:0 sc1
buffer_load_dwordx4 v[208:211],v1,s[36:39],s78 offen offset:2048 sc1
s_add_u32 s78,s55,89128960
buffer_load_dwordx4 v[212:215],v1,s[36:39],s78 offen offset:0 sc1
buffer_load_dwordx4 v[216:219],v1,s[36:39],s78 offen offset:2048 sc1
s_add_u32 s78,s55,89133056
buffer_load_dwordx4 v[220:223],v1,s[36:39],s78 offen offset:0 sc1
buffer_load_dwordx4 v[224:227],v1,s[36:39],s78 offen offset:2048 sc1
s_add_u32 m0,s65,16384
s_nop 0
D 9,56,0
D 9,56,1024
D 9,56,2048
D 9,56,3072
buffer_load_dword v18,v6,s[36:39],s62 offen offset:0 sc1
s_waitcnt vmcnt(13)
buffer_store_dword v17,v5,s[36:39],s63 offen offset:0 sc0 sc1
s_mov_b32 s63,0x7fff0000
s_lshr_b32 s78,s45,1
s_and_b32 s79,s45,1
s_lshl_b32 s79,s79,7
s_lshl_b32 s57,s78,15
s_add_u32 s57,s57,s79
s_add_u32 s57,s57,12582912
s_add_u32 s58,s57,4096
s_lshl_b32 s59,s78,9
s_add_u32 s59,s59,s79
s_add_u32 s59,s59,107020288
s_lshl_b32 s60,s45,9
s_add_u32 s60,s60,s66
s_add_u32 s60,s60,131072
s_add_u32 s45,s45,s46
s_lshr_b32 s78,s45,1
s_and_b32 s79,s45,1
s_lshl_b32 s79,s79,7
s_lshl_b32 s55,s78,15
s_add_u32 s55,s55,s79
s_mul_i32 s56,s45,16384
s_add_u32 s56,s56,98566144
s_lshl_b32 s61,s78,11
s_add_u32 s61,s61,110587904
s_add_u32 s78,s45,s46
s_lshr_b32 s78,s78,1
s_lshl_b32 s62,s78,11
s_add_u32 s62,s62,110587904
s_waitcnt vmcnt(1)
s_branch Lr_it1_%=
Lr_it1_%=:
T v[68:69],10,16384
T v[70:71],10,16448
T v[72:73],10,17408
T v[74:75],10,17472
T v[76:77],10,18432
T v[78:79],10,18496
T v[80:81],10,19456
T v[82:83],10,19520
s_nop 0
v_cmp_eq_u32 vcc,s53,v18
s_nop 0
s_cmp_eq_u64 vcc,-1
s_cselect_b32 s78,s45,256
s_cmp_lt_u32 s78,256
s_cbranch_scc1 Lr_ta1_%=
buffer_load_dword v18,v6,s[36:39],s61 offen offset:0 sc1
v_mfma_f32_16x16x32_bf16 v[116:119],v[26:29],v[196:199],0
L 6
v_lshlrev_b32 v84,16,v68
v_and_b32 v85,0xffff0000,v68
v_mfma_f32_16x16x32_bf16 v[120:123],v[24:27],v[196:199],0
L 4
v_lshlrev_b32 v86,16,v69
v_and_b32 v87,0xffff0000,v69
M 116,v[26:29],212
L 2
v_lshlrev_b32 v88,16,v70
v_and_b32 v89,0xffff0000,v70
M 120,v[24:27],212
L 0
v_lshlrev_b32 v90,16,v71
v_and_b32 v91,0xffff0000,v71
v_mfma_f32_16x16x32_bf16 v[124:127],v[26:29],v[200:203],0
v_lshlrev_b32 v92,16,v72
v_and_b32 v93,0xffff0000,v72
v_mfma_f32_16x16x32_bf16 v[128:131],v[24:27],v[200:203],0
v_lshlrev_b32 v94,16,v73
v_and_b32 v95,0xffff0000,v73
M 124,v[26:29],216
v_cvt_pk_bf16_f32 v148,v116,v117
v_cvt_pk_bf16_f32 v149,v118,v119
M 128,v[24:27],216
v_cvt_pk_bf16_f32 v150,v120,v121
v_cvt_pk_bf16_f32 v151,v122,v123
)"
R"(v_mfma_f32_16x16x32_bf16 v[132:135],v[26:29],v[204:207],0
v_lshlrev_b32 v96,16,v74
v_and_b32 v97,0xffff0000,v74
v_mfma_f32_16x16x32_bf16 v[136:139],v[24:27],v[204:207],0
v_lshlrev_b32 v98,16,v75
v_and_b32 v99,0xffff0000,v75
M 132,v[26:29],220
v_cvt_pk_bf16_f32 v152,v124,v125
v_cvt_pk_bf16_f32 v153,v126,v127
M 136,v[24:27],220
v_cvt_pk_bf16_f32 v154,v128,v129
v_cvt_pk_bf16_f32 v155,v130,v131
v_mfma_f32_16x16x32_bf16 v[140:143],v[26:29],v[208:211],0
v_lshlrev_b32 v100,16,v76
v_and_b32 v101,0xffff0000,v76
v_mfma_f32_16x16x32_bf16 v[144:147],v[24:27],v[208:211],0
v_lshlrev_b32 v102,16,v77
v_and_b32 v103,0xffff0000,v77
M 140,v[26:29],224
v_cvt_pk_bf16_f32 v156,v132,v133
v_cvt_pk_bf16_f32 v157,v134,v135
M 144,v[24:27],224
v_cvt_pk_bf16_f32 v158,v136,v137
v_cvt_pk_bf16_f32 v159,v138,v139
v_mfma_f32_16x16x32_bf16 v[36:39],v[32:35],v[148:151],0
v_lshlrev_b32 v104,16,v78
v_and_b32 v105,0xffff0000,v78
v_mfma_f32_16x16x32_bf16 v[40:43],v[148:151],v[148:151],0
v_lshlrev_b32 v106,16,v79
v_and_b32 v107,0xffff0000,v79
M 36,v[32:35],152
v_cvt_pk_bf16_f32 v160,v140,v141
v_cvt_pk_bf16_f32 v161,v142,v143
M 40,v[152:155],152
v_cvt_pk_bf16_f32 v162,v144,v145
v_cvt_pk_bf16_f32 v163,v146,v147
M 36,v[32:35],156
v_lshlrev_b32 v108,16,v80
v_and_b32 v109,0xffff0000,v80
M 40,v[156:159],156
v_lshlrev_b32 v110,16,v81
v_and_b32 v111,0xffff0000,v81
M 36,v[32:35],160
v_lshlrev_b32 v112,16,v82
v_and_b32 v113,0xffff0000,v82
M 40,v[160:163],160
v_lshlrev_b32 v114,16,v83
v_and_b32 v115,0xffff0000,v83
s_nop 2
v_add_f32_dpp v44,v36,v36 row_ror:8 row_mask:0xf bank_mask:0xf
s_nop 1
v_cndmask_b32 v45,v40,v41,s[70:71]
v_cndmask_b32 v46,v42,v43,s[70:71]
ds_write_b32 v2,v44 offset:0
v_cndmask_b32 v45,v45,v46,s[72:73]
ds_write_b32 v0,v45 offset:0
L 0
s_barrier
R 52,3,0
R 56,3,16
R 60,3,32
L 0
v_pk_add_f32 v[52:53],v[52:53],v[54:55]
v_pk_add_f32 v[56:57],v[56:57],v[58:59]
v_pk_add_f32 v[60:61],v[60:61],v[62:63]
v_add_f32 v52,v52,v53
v_pk_add_f32 v[56:57],v[56:57],v[60:61]
v_mul_f32 v52,s68,v52
v_add_f32 v53,v56,v57
v_mul_f32 v53,s68,v53
v_fma_f32 v53,-v52,v52,v53
v_max_f32 v53,0,v53
s_waitcnt vmcnt(1)
buffer_store_dword v17,v5,s[36:39],s63 offen offset:0 sc0 sc1
s_mov_b32 s63,0x7fff0000
s_waitcnt vmcnt(1)
v_cmp_eq_u32 vcc,s53,v18
s_nop 0
s_cmp_eq_u64 vcc,-1
s_cselect_b32 s78,s45,256
s_cmp_lt_u32 s78,256
s_cbranch_scc1 Lr_tb1_%=
buffer_load_dword v18,v6,s[36:39],s61 offen offset:0 sc1
v_add_f32 v53,s69,v53
v_rsq_f32 v48,v53
s_nop 0
v_mul_f32_e64 v50,-v52,v48
v_mov_b32 v49,v48
v_mov_b32 v51,v50
v_pk_fma_f32 v[52:53],v[116:117],v[48:49],v[50:51]
v_pk_fma_f32 v[54:55],v[118:119],v[48:49],v[50:51]
v_pk_fma_f32 v[56:57],v[120:121],v[48:49],v[50:51]
v_pk_fma_f32 v[58:59],v[122:123],v[48:49],v[50:51]
v_pk_fma_f32 v[116:117],v[52:53],v[84:85],v[116:117]
v_pk_fma_f32 v[118:119],v[54:55],v[86:87],v[118:119]
v_pk_fma_f32 v[120:121],v[56:57],v[88:89],v[120:121]
v_pk_fma_f32 v[122:123],v[58:59],v[90:91],v[122:123]
v_cvt_pk_bf16_f32 v148,v116,v117
v_cvt_pk_bf16_f32 v149,v118,v119
v_cvt_pk_bf16_f32 v150,v120,v121
v_cvt_pk_bf16_f32 v151,v122,v123
v_pk_fma_f32 v[52:53],v[124:125],v[48:49],v[50:51]
v_pk_fma_f32 v[54:55],v[126:127],v[48:49],v[50:51]
v_pk_fma_f32 v[56:57],v[128:129],v[48:49],v[50:51]
v_pk_fma_f32 v[58:59],v[130:131],v[48:49],v[50:51]
v_pk_fma_f32 v[124:125],v[52:53],v[92:93],v[124:125]
v_pk_fma_f32 v[126:127],v[54:55],v[94:95],v[126:127]
v_pk_fma_f32 v[128:129],v[56:57],v[96:97],v[128:129]
v_pk_fma_f32 v[130:131],v[58:59],v[98:99],v[130:131]
v_cvt_pk_bf16_f32 v152,v124,v125
v_cvt_pk_bf16_f32 v153,v126,v127
v_cvt_pk_bf16_f32 v154,v128,v129
v_cvt_pk_bf16_f32 v155,v130,v131
v_pk_fma_f32 v[52:53],v[132:133],v[48:49],v[50:51]
v_pk_fma_f32 v[54:55],v[134:135],v[48:49],v[50:51]
v_pk_fma_f32 v[56:57],v[136:137],v[48:49],v[50:51]
v_pk_fma_f32 v[58:59],v[138:139],v[48:49],v[50:51]
v_pk_fma_f32 v[132:133],v[52:53],v[100:101],v[132:133]
v_pk_fma_f32 v[134:135],v[54:55],v[102:103],v[134:135]
v_pk_fma_f32 v[136:137],v[56:57],v[104:105],v[136:137]
v_pk_fma_f32 v[138:139],v[58:59],v[106:107],v[138:139]
v_cvt_pk_bf16_f32 v156,v132,v133
v_cvt_pk_bf16_f32 v157,v134,v135
v_cvt_pk_bf16_f32 v158,v136,v137
v_cvt_pk_bf16_f32 v159,v138,v139
v_pk_fma_f32 v[52:53],v[140:141],v[48:49],v[50:51]
v_pk_fma_f32 v[54:55],v[142:143],v[48:49],v[50:51]
v_pk_fma_f32 v[56:57],v[144:145],v[48:49],v[50:51]
v_pk_fma_f32 v[58:59],v[146:147],v[48:49],v[50:51]
v_pk_fma_f32 v[140:141],v[52:53],v[108:109],v[140:141]
v_pk_fma_f32 v[142:143],v[54:55],v[110:111],v[142:143]
v_pk_fma_f32 v[144:145],v[56:57],v[112:113],v[144:145]
v_pk_fma_f32 v[146:147],v[58:59],v[114:115],v[146:147]
v_cvt_pk_bf16_f32 v160,v140,v141
v_cvt_pk_bf16_f32 v161,v142,v143
v_cvt_pk_bf16_f32 v162,v144,v145
v_cvt_pk_bf16_f32 v163,v146,v147
v_mfma_f32_16x16x32_bf16 v[36:39],v[32:35],v[148:151],0
buffer_store_dwordx4 v[148:151],v1,s[36:39],s57 offen offset:0 sc0 sc1
v_mfma_f32_16x16x32_bf16 v[40:43],v[148:151],v[148:151],0
M 36,v[32:35],152
buffer_store_dwordx4 v[152:155],v1,s[36:39],s57 offen offset:2048 sc0 sc1
M 40,v[152:155],152
M 36,v[32:35],156
buffer_store_dwordx4 v[156:159],v1,s[36:39],s58 offen offset:0 sc0 sc1
M 40,v[156:159],156
M 36,v[32:35],160
buffer_store_dwordx4 v[160:163],v1,s[36:39],s58 offen offset:2048 sc0 sc1
M 40,v[160:163],160
s_nop 5
v_add_f32_dpp v44,v36,v36 row_ror:8 row_mask:0xf bank_mask:0xf
s_nop 0
v_cndmask_b32 v45,v40,v41,s[70:71]
v_cndmask_b32 v46,v42,v43,s[70:71]
ds_write_b32 v2,v44 offset:1024
v_cndmask_b32 v45,v45,v46,s[72:73]
ds_write_b32 v0,v45 offset:1024
L 0
s_barrier
R 52,3,1024
R 56,3,1040
R 60,3,1056
L 0
v_pk_add_f32 v[52:53],v[52:53],v[54:55]
v_pk_add_f32 v[56:57],v[56:57],v[58:59]
v_pk_add_f32 v[60:61],v[60:61],v[62:63]
v_add_f32 v52,v52,v53
v_pk_add_f32 v[56:57],v[56:57],v[60:61]
v_mul_f32 v52,s68,v52
v_add_f32 v53,v56,v57
v_mul_f32 v53,s68,v53
v_fma_f32 v53,-v52,v52,v53
v_max_f32 v53,0,v53
v_add_f32 v54,s69,v53
v_fma_f32 v55,s69,v54,v53
v_rsq_f32 v65,v54
v_rsq_f32 v54,v55
v_mul_f32_e64 v64,-v52,v65
v_mul_f32 v63,v55,v54
v_mul_f32 v61,s76,v54
v_mul_f32 v55,v54,v54
v_mul_f32 v62,s77,v55
v_mul_f32 v62,v62,v61
v_cndmask_b32 v60,v52,v64,s[74:75]
v_cndmask_b32 v61,v61,v65,s[74:75]
buffer_store_dwordx4 v[60:63],v4,s[36:39],s59 offen offset:0 sc0 sc1
s_mov_b32 s63,s60
s_waitcnt vmcnt(5)
v_cmp_eq_u32 vcc,s53,v18
s_nop 0
s_cmp_eq_u64 vcc,-1
s_cselect_b32 s78,s45,256
s_cmp_lt_u32 s78,256
s_cbranch_scc1 Lr_tc1_%=
s_waitcnt vmcnt(0)
buffer_store_dword v17,v5,s[36:39],s63 offen offset:0 sc0 sc1
s_mov_b32 s63,0x7fff0000
s_cmp_ge_u32 s45,256
s_cbranch_scc1 Lr_done_%=
Lr_bp1_%=:
buffer_load_dword v18,v6,s[36:39],s61 offen offset:0 sc1
s_add_u32 s82,s82,1
s_waitcnt vmcnt(0)
v_cmp_eq_u32 vcc,s53,v18
s_nop 0
s_cmp_eq_u64 vcc,-1
s_cbranch_scc1 Lr_bq1_%=
s_sleep 2
s_cmp_lt_u32 s82,20000
s_cbranch_scc1 Lr_bp1_%=
Lr_bq1_%=:
s_add_u32 s78,s55,71303168
buffer_load_dwordx4 v[164:167],v1,s[36:39],s78 offen offset:0 sc1
buffer_load_dwordx4 v[168:171],v1,s[36:39],s78 offen offset:2048 sc1
s_add_u32 s78,s55,71307264
buffer_load_dwordx4 v[172:175],v1,s[36:39],s78 offen offset:0 sc1
buffer_load_dwordx4 v[176:179],v1,s[36:39],s78 offen offset:2048 sc1
s_add_u32 s78,s55,89128960
buffer_load_dwordx4 v[180:183],v1,s[36:39],s78 offen offset:0 sc1
buffer_load_dwordx4 v[184:187],v1,s[36:39],s78 offen offset:2048 sc1
s_add_u32 s78,s55,89133056
buffer_load_dwordx4 v[188:191],v1,s[36:39],s78 offen offset:0 sc1
buffer_load_dwordx4 v[192:195],v1,s[36:39],s78 offen offset:2048 sc1
s_add_u32 m0,s65,0
s_nop 0
D 9,56,0
D 9,56,1024
D 9,56,2048
D 9,56,3072
buffer_load_dword v18,v6,s[36:39],s62 offen offset:0 sc1
s_lshr_b32 s78,s45,1
s_and_b32 s79,s45,1
s_lshl_b32 s79,s79,7
s_lshl_b32 s57,s78,15
s_add_u32 s57,s57,s79
s_add_u32 s57,s57,12582912
s_add_u32 s58,s57,4096
s_lshl_b32 s59,s78,9
s_add_u32 s59,s59,s79
s_add_u32 s59,s59,107020288
s_lshl_b32 s60,s45,9
s_add_u32 s60,s60,s66
s_add_u32 s60,s60,131072
s_add_u32 s45,s45,s46
s_lshr_b32 s78,s45,1
s_and_b32 s79,s45,1
s_lshl_b32 s79,s79,7
s_lshl_b32 s55,s78,15
s_add_u32 s55,s55,s79
s_mul_i32 s56,s45,16384
s_add_u32 s56,s56,98566144
s_lshl_b32 s61,s78,11
s_add_u32 s61,s61,110587904
s_add_u32 s78,s45,s46
s_lshr_b32 s78,s78,1
s_lshl_b32 s62,s78,11
s_add_u32 s62,s62,110587904
s_waitcnt vmcnt(0)
s_branch Lr_it0_%=
Lr_ta1_%=:
v_mfma_f32_16x16x32_bf16 v[116:119],v[26:29],v[196:199],0
s_add_u32 s78,s55,71303168
L 6
L 4
v_mfma_f32_16x16x32_bf16 v[120:123],v[24:27],v[196:199],0
L 2
buffer_load_dwordx4 v[164:167],v1,s[36:39],s78 offen offset:0 sc1
M 116,v[26:29],212
L 0
buffer_load_dwordx4 v[168:171],v1,s[36:39],s78 offen offset:2048 sc1
M 120,v[24:27],212
s_add_u32 s78,s55,71307264
v_lshlrev_b32 v84,16,v68
v_and_b32 v85,0xffff0000,v68
v_mfma_f32_16x16x32_bf16 v[124:127],v[26:29],v[200:203],0
buffer_load_dwordx4 v[172:175],v1,s[36:39],s78 offen offset:0 sc1
v_mfma_f32_16x16x32_bf16 v[128:131],v[24:27],v[200:203],0
buffer_load_dwordx4 v[176:179],v1,s[36:39],s78 offen offset:2048 sc1
M 124,v[26:29],216
s_add_u32 s78,s55,89128960
v_cvt_pk_bf16_f32 v148,v116,v117
v_cvt_pk_bf16_f32 v149,v118,v119
M 128,v[24:27],216
buffer_load_dwordx4 v[180:183],v1,s[36:39],s78 offen offset:0 sc1
v_mfma_f32_16x16x32_bf16 v[132:135],v[26:29],v[204:207],0
buffer_load_dwordx4 v[184:187],v1,s[36:39],s78 offen offset:2048 sc1
v_mfma_f32_16x16x32_bf16 v[136:139],v[24:27],v[204:207],0
s_add_u32 s78,s55,89133056
v_cvt_pk_bf16_f32 v150,v120,v121
v_cvt_pk_bf16_f32 v151,v122,v123
M 132,v[26:29],220
buffer_load_dwordx4 v[188:191],v1,s[36:39],s78 offen offset:0 sc1
M 136,v[24:27],220
buffer_load_dwordx4 v[192:195],v1,s[36:39],s78 offen offset:2048 sc1
v_mfma_f32_16x16x32_bf16 v[140:143],v[26:29],v[208:211],0
s_add_u32 m0,s65,0
v_cvt_pk_bf16_f32 v152,v124,v125
v_cvt_pk_bf16_f32 v153,v126,v127
v_mfma_f32_16x16x32_bf16 v[144:147],v[24:27],v[208:211],0
D 9,56,0
M 140,v[26:29],224
D 9,56,1024
M 144,v[24:27],224
D 9,56,2048
v_mfma_f32_16x16x32_bf16 v[36:39],v[32:35],v[148:151],0
v_cvt_pk_bf16_f32 v154,v128,v129
v_cvt_pk_bf16_f32 v155,v130,v131
v_mfma_f32_16x16x32_bf16 v[40:43],v[148:151],v[148:151],0
D 9,56,3072
M 36,v[32:35],152
v_cvt_pk_bf16_f32 v156,v132,v133
v_cvt_pk_bf16_f32 v157,v134,v135
v_cvt_pk_bf16_f32 v158,v136,v137
v_cvt_pk_bf16_f32 v159,v138,v139
M 40,v[152:155],152
v_cvt_pk_bf16_f32 v160,v140,v141
v_cvt_pk_bf16_f32 v161,v142,v143
M 36,v[32:35],156
v_cvt_pk_bf16_f32 v162,v144,v145
v_cvt_pk_bf16_f32 v163,v146,v147
M 40,v[156:159],156
v_lshlrev_b32 v86,16,v69
v_and_b32 v87,0xffff0000,v69
M 36,v[32:35],160
v_lshlrev_b32 v88,16,v70
v_and_b32 v89,0xffff0000,v70
M 40,v[160:163],160
v_lshlrev_b32 v90,16,v71
v_and_b32 v91,0xffff0000,v71
v_lshlrev_b32 v92,16,v72
v_and_b32 v93,0xffff0000,v72
v_lshlrev_b32 v94,16,v73
v_and_b32 v95,0xffff0000,v73
v_lshlrev_b32 v96,16,v74
v_and_b32 v97,0xffff0000,v74
v_lshlrev_b32 v98,16,v75
v_and_b32 v99,0xffff0000,v75
v_lshlrev_b32 v100,16,v76
v_and_b32 v101,0xffff0000,v76
v_lshlrev_b32 v102,16,v77
v_and_b32 v103,0xffff0000,v77
v_lshlrev_b32 v104,16,v78
v_and_b32 v105,0xffff0000,v78
v_lshlrev_b32 v106,16,v79
v_and_b32 v107,0xffff0000,v79
v_lshlrev_b32 v108,16,v80
v_and_b32 v109,0xffff0000,v80
v_lshlrev_b32 v110,16,v81
v_and_b32 v111,0xffff0000,v81
v_lshlrev_b32 v112,16,v82
v_and_b32 v113,0xffff0000,v82
v_lshlrev_b32 v114,16,v83
v_and_b32 v115,0xffff0000,v83
v_add_f32_dpp v44,v36,v36 row_ror:8 row_mask:0xf bank_mask:0xf
v_cndmask_b32 v45,v40,v41,s[70:71]
v_cndmask_b32 v46,v42,v43,s[70:71]
ds_write_b32 v2,v44 offset:0
v_cndmask_b32 v45,v45,v46,s[72:73]
ds_write_b32 v0,v45 offset:0
L 0
s_barrier
R 52,3,0
R 56,3,16
R 60,3,32
L 0
v_pk_add_f32 v[52:53],v[52:53],v[54:55]
v_pk_add_f32 v[56:57],v[56:57],v[58:59]
v_pk_add_f32 v[60:61],v[60:61],v[62:63]
v_add_f32 v52,v52,v53
v_pk_add_f32 v[56:57],v[56:57],v[60:61]
v_mul_f32 v52,s68,v52
v_add_f32 v53,v56,v57
v_mul_f32 v53,s68,v53
v_fma_f32 v53,-v52,v52,v53
v_max_f32 v53,0,v53
buffer_load_dword v18,v6,s[36:39],s62 offen offset:0 sc1
v_add_f32 v53,s69,v53
v_rsq_f32 v48,v53
s_nop 0
v_mul_f32_e64 v50,-v52,v48
v_mov_b32 v49,v48
v_mov_b32 v51,v50
v_pk_fma_f32 v[52:53],v[116:117],v[48:49],v[50:51]
v_pk_fma_f32 v[54:55],v[118:119],v[48:49],v[50:51]
v_pk_fma_f32 v[56:57],v[120:121],v[48:49],v[50:51]
v_pk_fma_f32 v[58:59],v[122:123],v[48:49],v[50:51]
v_pk_fma_f32 v[116:117],v[52:53],v[84:85],v[116:117]
v_pk_fma_f32 v[118:119],v[54:55],v[86:87],v[118:119]
v_pk_fma_f32 v[120:121],v[56:57],v[88:89],v[120:121]
v_pk_fma_f32 v[122:123],v[58:59],v[90:91],v[122:123]
v_cvt_pk_bf16_f32 v148,v116,v117
v_cvt_pk_bf16_f32 v149,v118,v119
v_cvt_pk_bf16_f32 v150,v120,v121
v_cvt_pk_bf16_f32 v151,v122,v123
v_pk_fma_f32 v[52:53],v[124:125],v[48:49],v[50:51]
v_pk_fma_f32 v[54:55],v[126:127],v[48:49],v[50:51]
v_pk_fma_f32 v[56:57],v[128:129],v[48:49],v[50:51]
v_pk_fma_f32 v[58:59],v[130:131],v[48:49],v[50:51]
v_pk_fma_f32 v[124:125],v[52:53],v[92:93],v[124:125]
v_pk_fma_f32 v[126:127],v[54:55],v[94:95],v[126:127]
v_pk_fma_f32 v[128:129],v[56:57],v[96:97],v[128:129]
v_pk_fma_f32 v[130:131],v[58:59],v[98:99],v[130:131]
v_cvt_pk_bf16_f32 v152,v124,v125
v_cvt_pk_bf16_f32 v153,v126,v127
v_cvt_pk_bf16_f32 v154,v128,v129
v_cvt_pk_bf16_f32 v155,v130,v131
v_pk_fma_f32 v[52:53],v[132:133],v[48:49],v[50:51]
v_pk_fma_f32 v[54:55],v[134:135],v[48:49],v[50:51]
v_pk_fma_f32 v[56:57],v[136:137],v[48:49],v[50:51]
v_pk_fma_f32 v[58:59],v[138:139],v[48:49],v[50:51]
v_pk_fma_f32 v[132:133],v[52:53],v[100:101],v[132:133]
v_pk_fma_f32 v[134:135],v[54:55],v[102:103],v[134:135]
v_pk_fma_f32 v[136:137],v[56:57],v[104:105],v[136:137]
v_pk_fma_f32 v[138:139],v[58:59],v[106:107],v[138:139]
v_cvt_pk_bf16_f32 v156,v132,v133
v_cvt_pk_bf16_f32 v157,v134,v135
v_cvt_pk_bf16_f32 v158,v136,v137
v_cvt_pk_bf16_f32 v159,v138,v139
v_pk_fma_f32 v[52:53],v[140:141],v[48:49],v[50:51]
v_pk_fma_f32 v[54:55],v[142:143],v[48:49],v[50:51]
v_pk_fma_f32 v[56:57],v[144:145],v[48:49],v[50:51]
v_pk_fma_f32 v[58:59],v[146:147],v[48:49],v[50:51]
v_pk_fma_f32 v[140:141],v[52:53],v[108:109],v[140:141]
v_pk_fma_f32 v[142:143],v[54:55],v[110:111],v[142:143]
v_pk_fma_f32 v[144:145],v[56:57],v[112:113],v[144:145]
v_pk_fma_f32 v[146:147],v[58:59],v[114:115],v[146:147]
v_cvt_pk_bf16_f32 v160,v140,v141
v_cvt_pk_bf16_f32 v161,v142,v143
v_cvt_pk_bf16_f32 v162,v144,v145
v_cvt_pk_bf16_f32 v163,v146,v147
s_waitcnt vmcnt(13)
buffer_store_dword v17,v5,s[36:39],s63 offen offset:0 sc0 sc1
s_mov_b32 s63,0x7fff0000
v_mfma_f32_16x16x32_bf16 v[36:39],v[32:35],v[148:151],0
buffer_store_dwordx4 v[148:151],v1,s[36:39],s57 offen offset:0 sc0 sc1
v_mfma_f32_16x16x32_bf16 v[40:43],v[148:151],v[148:151],0
M 36,v[32:35],152
buffer_store_dwordx4 v[152:155],v1,s[36:39],s57 offen offset:2048 sc0 sc1
M 40,v[152:155],152
M 36,v[32:35],156
buffer_store_dwordx4 v[156:159],v1,s[36:39],s58 offen offset:0 sc0 sc1
M 40,v[156:159],156
M 36,v[32:35],160
buffer_store_dwordx4 v[160:163],v1,s[36:39],s58 offen offset:2048 sc0 sc1
M 40,v[160:163],160
s_nop 5
v_add_f32_dpp v44,v36,v36 row_ror:8 row_mask:0xf bank_mask:0xf
s_nop 0
v_cndmask_b32 v45,v40,v41,s[70:71]
v_cndmask_b32 v46,v42,v43,s[70:71]
ds_write_b32 v2,v44 offset:1024
v_cndmask_b32 v45,v45,v46,s[72:73]
ds_write_b32 v0,v45 offset:1024
L 0
s_barrier
R 52,3,1024
R 56,3,1040
R 60,3,1056
L 0
v_pk_add_f32 v[52:53],v[52:53],v[54:55]
v_pk_add_f32 v[56:57],v[56:57],v[58:59]
v_pk_add_f32 v[60:61],v[60:61],v[62:63]
v_add_f32 v52,v52,v53
v_pk_add_f32 v[56:57],v[56:57],v[60:61]
v_mul_f32 v52,s68,v52
v_add_f32 v53,v56,v57
v_mul_f32 v53,s68,v53
v_fma_f32 v53,-v52,v52,v53
v_max_f32 v53,0,v53
v_add_f32 v54,s69,v53
v_fma_f32 v55,s69,v54,v53
v_rsq_f32 v65,v54
v_rsq_f32 v54,v55
v_mul_f32_e64 v64,-v52,v65
v_mul_f32 v63,v55,v54
v_mul_f32 v61,s76,v54
v_mul_f32 v55,v54,v54
v_mul_f32 v62,s77,v55
v_mul_f32 v62,v62,v61
v_cndmask_b32 v60,v52,v64,s[74:75]
v_cndmask_b32 v61,v61,v65,s[74:75]
buffer_store_dwordx4 v[60:63],v4,s[36:39],s59 offen offset:0 sc0 sc1
s_mov_b32 s63,s60
s_lshr_b32 s78,s45,1
s_and_b32 s79,s45,1
s_lshl_b32 s79,s79,7
s_lshl_b32 s57,s78,15
s_add_u32 s57,s57,s79
s_add_u32 s57,s57,12582912
s_add_u32 s58,s57,4096
s_lshl_b32 s59,s78,9
s_add_u32 s59,s59,s79
s_add_u32 s59,s59,107020288
s_lshl_b32 s60,s45,9
s_add_u32 s60,s60,s66
s_add_u32 s60,s60,131072
s_add_u32 s45,s45,s46
s_lshr_b32 s78,s45,1
s_and_b32 s79,s45,1
s_lshl_b32 s79,s79,7
)"
R"(s_lshl_b32 s55,s78,15
s_add_u32 s55,s55,s79
s_mul_i32 s56,s45,16384
s_add_u32 s56,s56,98566144
s_lshl_b32 s61,s78,11
s_add_u32 s61,s61,110587904
s_add_u32 s78,s45,s46
s_lshr_b32 s78,s78,1
s_lshl_b32 s62,s78,11
s_add_u32 s62,s62,110587904
s_waitcnt vmcnt(6)
s_branch Lr_it0_%=
Lr_tb1_%=:
v_add_f32 v53,s69,v53
v_rsq_f32 v48,v53
s_nop 0
v_mul_f32_e64 v50,-v52,v48
v_mov_b32 v49,v48
v_mov_b32 v51,v50
v_pk_fma_f32 v[52:53],v[116:117],v[48:49],v[50:51]
v_pk_fma_f32 v[54:55],v[118:119],v[48:49],v[50:51]
v_pk_fma_f32 v[56:57],v[120:121],v[48:49],v[50:51]
v_pk_fma_f32 v[58:59],v[122:123],v[48:49],v[50:51]
s_add_u32 s78,s55,71303168
buffer_load_dwordx4 v[164:167],v1,s[36:39],s78 offen offset:0 sc1
buffer_load_dwordx4 v[168:171],v1,s[36:39],s78 offen offset:2048 sc1
s_add_u32 s78,s55,71307264
buffer_load_dwordx4 v[172:175],v1,s[36:39],s78 offen offset:0 sc1
buffer_load_dwordx4 v[176:179],v1,s[36:39],s78 offen offset:2048 sc1
v_pk_fma_f32 v[116:117],v[52:53],v[84:85],v[116:117]
v_pk_fma_f32 v[118:119],v[54:55],v[86:87],v[118:119]
v_pk_fma_f32 v[120:121],v[56:57],v[88:89],v[120:121]
v_pk_fma_f32 v[122:123],v[58:59],v[90:91],v[122:123]
v_cvt_pk_bf16_f32 v148,v116,v117
v_cvt_pk_bf16_f32 v149,v118,v119
v_cvt_pk_bf16_f32 v150,v120,v121
v_cvt_pk_bf16_f32 v151,v122,v123
v_pk_fma_f32 v[52:53],v[124:125],v[48:49],v[50:51]
v_pk_fma_f32 v[54:55],v[126:127],v[48:49],v[50:51]
v_pk_fma_f32 v[56:57],v[128:129],v[48:49],v[50:51]
v_pk_fma_f32 v[58:59],v[130:131],v[48:49],v[50:51]
s_add_u32 s78,s55,89128960
buffer_load_dwordx4 v[180:183],v1,s[36:39],s78 offen offset:0 sc1
buffer_load_dwordx4 v[184:187],v1,s[36:39],s78 offen offset:2048 sc1
s_add_u32 s78,s55,89133056
buffer_load_dwordx4 v[188:191],v1,s[36:39],s78 offen offset:0 sc1
buffer_load_dwordx4 v[192:195],v1,s[36:39],s78 offen offset:2048 sc1
v_pk_fma_f32 v[124:125],v[52:53],v[92:93],v[124:125]
v_pk_fma_f32 v[126:127],v[54:55],v[94:95],v[126:127]
v_pk_fma_f32 v[128:129],v[56:57],v[96:97],v[128:129]
v_pk_fma_f32 v[130:131],v[58:59],v[98:99],v[130:131]
v_cvt_pk_bf16_f32 v152,v124,v125
v_cvt_pk_bf16_f32 v153,v126,v127
v_cvt_pk_bf16_f32 v154,v128,v129
v_cvt_pk_bf16_f32 v155,v130,v131
v_pk_fma_f32 v[52:53],v[132:133],v[48:49],v[50:51]
v_pk_fma_f32 v[54:55],v[134:135],v[48:49],v[50:51]
v_pk_fma_f32 v[56:57],v[136:137],v[48:49],v[50:51]
v_pk_fma_f32 v[58:59],v[138:139],v[48:49],v[50:51]
s_add_u32 m0,s65,0
s_nop 0
D 9,56,0
D 9,56,1024
D 9,56,2048
D 9,56,3072
v_pk_fma_f32 v[132:133],v[52:53],v[100:101],v[132:133]
v_pk_fma_f32 v[134:135],v[54:55],v[102:103],v[134:135]
v_pk_fma_f32 v[136:137],v[56:57],v[104:105],v[136:137]
v_pk_fma_f32 v[138:139],v[58:59],v[106:107],v[138:139]
v_cvt_pk_bf16_f32 v156,v132,v133
v_cvt_pk_bf16_f32 v157,v134,v135
v_cvt_pk_bf16_f32 v158,v136,v137
v_cvt_pk_bf16_f32 v159,v138,v139
v_pk_fma_f32 v[52:53],v[140:141],v[48:49],v[50:51]
v_pk_fma_f32 v[54:55],v[142:143],v[48:49],v[50:51]
v_pk_fma_f32 v[56:57],v[144:145],v[48:49],v[50:51]
v_pk_fma_f32 v[58:59],v[146:147],v[48:49],v[50:51]
v_pk_fma_f32 v[140:141],v[52:53],v[108:109],v[140:141]
v_pk_fma_f32 v[142:143],v[54:55],v[110:111],v[142:143]
v_pk_fma_f32 v[144:145],v[56:57],v[112:113],v[144:145]
v_pk_fma_f32 v[146:147],v[58:59],v[114:115],v[146:147]
v_cvt_pk_bf16_f32 v160,v140,v141
v_cvt_pk_bf16_f32 v161,v142,v143
v_cvt_pk_bf16_f32 v162,v144,v145
v_cvt_pk_bf16_f32 v163,v146,v147
v_mfma_f32_16x16x32_bf16 v[36:39],v[32:35],v[148:151],0
buffer_load_dword v18,v6,s[36:39],s62 offen offset:0 sc1
buffer_store_dwordx4 v[148:151],v1,s[36:39],s57 offen offset:0 sc0 sc1
v_mfma_f32_16x16x32_bf16 v[40:43],v[148:151],v[148:151],0
M 36,v[32:35],152
buffer_store_dwordx4 v[152:155],v1,s[36:39],s57 offen offset:2048 sc0 sc1
M 40,v[152:155],152
M 36,v[32:35],156
buffer_store_dwordx4 v[156:159],v1,s[36:39],s58 offen offset:0 sc0 sc1
M 40,v[156:159],156
M 36,v[32:35],160
buffer_store_dwordx4 v[160:163],v1,s[36:39],s58 offen offset:2048 sc0 sc1
M 40,v[160:163],160
s_nop 5
v_add_f32_dpp v44,v36,v36 row_ror:8 row_mask:0xf bank_mask:0xf
s_nop 0
v_cndmask_b32 v45,v40,v41,s[70:71]
v_cndmask_b32 v46,v42,v43,s[70:71]
ds_write_b32 v2,v44 offset:1024
v_cndmask_b32 v45,v45,v46,s[72:73]
ds_write_b32 v0,v45 offset:1024
L 0
s_barrier
R 52,3,1024
R 56,3,1040
R 60,3,1056
L 0
v_pk_add_f32 v[52:53],v[52:53],v[54:55]
v_pk_add_f32 v[56:57],v[56:57],v[58:59]
v_pk_add_f32 v[60:61],v[60:61],v[62:63]
v_add_f32 v52,v52,v53
v_pk_add_f32 v[56:57],v[56:57],v[60:61]
v_mul_f32 v52,s68,v52
v_add_f32 v53,v56,v57
v_mul_f32 v53,s68,v53
v_fma_f32 v53,-v52,v52,v53
v_max_f32 v53,0,v53
v_add_f32 v54,s69,v53
v_fma_f32 v55,s69,v54,v53
v_rsq_f32 v65,v54
v_rsq_f32 v54,v55
v_mul_f32_e64 v64,-v52,v65
v_mul_f32 v63,v55,v54
v_mul_f32 v61,s76,v54
v_mul_f32 v55,v54,v54
v_mul_f32 v62,s77,v55
v_mul_f32 v62,v62,v61
v_cndmask_b32 v60,v52,v64,s[74:75]
v_cndmask_b32 v61,v61,v65,s[74:75]
buffer_store_dwordx4 v[60:63],v4,s[36:39],s59 offen offset:0 sc0 sc1
s_mov_b32 s63,s60
s_lshr_b32 s78,s45,1
s_and_b32 s79,s45,1
s_lshl_b32 s79,s79,7
s_lshl_b32 s57,s78,15
s_add_u32 s57,s57,s79
s_add_u32 s57,s57,12582912
s_add_u32 s58,s57,4096
s_lshl_b32 s59,s78,9
s_add_u32 s59,s59,s79
s_add_u32 s59,s59,107020288
s_lshl_b32 s60,s45,9
s_add_u32 s60,s60,s66
s_add_u32 s60,s60,131072
s_add_u32 s45,s45,s46
s_lshr_b32 s78,s45,1
s_and_b32 s79,s45,1
s_lshl_b32 s79,s79,7
s_lshl_b32 s55,s78,15
s_add_u32 s55,s55,s79
s_mul_i32 s56,s45,16384
s_add_u32 s56,s56,98566144
s_lshl_b32 s61,s78,11
s_add_u32 s61,s61,110587904
s_add_u32 s78,s45,s46
s_lshr_b32 s78,s78,1
s_lshl_b32 s62,s78,11
s_add_u32 s62,s62,110587904
s_waitcnt vmcnt(5)
s_branch Lr_it0_%=
Lr_tc1_%=:
s_add_u32 s78,s55,71303168
buffer_load_dwordx4 v[164:167],v1,s[36:39],s78 offen offset:0 sc1
buffer_load_dwordx4 v[168:171],v1,s[36:39],s78 offen offset:2048 sc1
s_add_u32 s78,s55,71307264
buffer_load_dwordx4 v[172:175],v1,s[36:39],s78 offen offset:0 sc1
buffer_load_dwordx4 v[176:179],v1,s[36:39],s78 offen offset:2048 sc1
s_add_u32 s78,s55,89128960
buffer_load_dwordx4 v[180:183],v1,s[36:39],s78 offen offset:0 sc1
buffer_load_dwordx4 v[184:187],v1,s[36:39],s78 offen offset:2048 sc1
s_add_u32 s78,s55,89133056
buffer_load_dwordx4 v[188:191],v1,s[36:39],s78 offen offset:0 sc1
buffer_load_dwordx4 v[192:195],v1,s[36:39],s78 offen offset:2048 sc1
s_add_u32 m0,s65,0
s_nop 0
D 9,56,0
D 9,56,1024
D 9,56,2048
D 9,56,3072
buffer_load_dword v18,v6,s[36:39],s62 offen offset:0 sc1
s_waitcnt vmcnt(13)
buffer_store_dword v17,v5,s[36:39],s63 offen offset:0 sc0 sc1
s_mov_b32 s63,0x7fff0000
s_lshr_b32 s78,s45,1
s_and_b32 s79,s45,1
s_lshl_b32 s79,s79,7
s_lshl_b32 s57,s78,15
s_add_u32 s57,s57,s79
s_add_u32 s57,s57,12582912
s_add_u32 s58,s57,4096
s_lshl_b32 s59,s78,9
s_add_u32 s59,s59,s79
s_add_u32 s59,s59,107020288
s_lshl_b32 s60,s45,9
s_add_u32 s60,s60,s66
s_add_u32 s60,s60,131072
s_add_u32 s45,s45,s46
s_lshr_b32 s78,s45,1
s_and_b32 s79,s45,1
s_lshl_b32 s79,s79,7
s_lshl_b32 s55,s78,15
s_add_u32 s55,s55,s79
s_mul_i32 s56,s45,16384
s_add_u32 s56,s56,98566144
s_lshl_b32 s61,s78,11
s_add_u32 s61,s61,110587904
s_add_u32 s78,s45,s46
s_lshr_b32 s78,s78,1
s_lshl_b32 s62,s78,11
s_add_u32 s62,s62,110587904
s_waitcnt vmcnt(1)
s_branch Lr_it0_%=
Lr_done_%=:
s_waitcnt vmcnt(0) lgkmcnt(0)
s_barrier
Lcode_r_e:
.purgem M
.purgem R
.purgem T
.purgem D
.purgem L
)"
  :: "s"(ws), "s"(sWave), "s"(sG0), "s"(sStep), "s"(sWw), "s"(sExp), "v"(vRP), "v"(vRF), "v"(vRwr), "v"(vRrd), "v"(vRS), "v"(vFlag), "v"(vL4), "v"(vLY), "v"(vQ), "v"(vY0), "v"(vTr), "v"(vSa), "v"(vSb), "v"(vWd0), "v"(vWd1), "v"(vWr0), "v"(vWr1), "s"(q), "s"(w4) : "v0", "v1", "v2", "v3", "v4", "v5", "v6", "v7", "v8", "v9", "v10", "v11", "v12", "v13", "v14", "v15", "v16", "v17", "v18", "v19", "v20", "v21", "v22", "v23", "v24", "v25", "v26", "v27", "v28", "v29", "v30", "v31", "v32", "v33", "v34", "v35", "v36", "v37", "v38", "v39", "v40", "v41", "v42", "v43", "v44", "v45", "v46", "v47", "v48", "v49", "v50", "v51", "v52", "v53", "v54", "v55", "v56", "v57", "v58", "v59", "v60", "v61", "v62", "v63", "v64", "v65", "v66", "v67", "v68", "v69", "v70", "v71", "v72", "v73", "v74", "v75", "v76", "v77", "v78", "v79", "v80", "v81", "v82", "v83", "v84", "v85", "v86", "v87", "v88", "v89", "v90", "v91", "v92", "v93", "v94", "v95", "v96", "v97", "v98", "v99", "v100", "v101", "v102", "v103", "v104", "v105", "v106", "v107", "v108", "v109", "v110", "v111", "v112", "v113", "v114", "v115", "v116", "v117", "v118", "v119", "v120", "v121", "v122", "v123", "v124", "v125", "v126", "v127", "v128", "v129", "v130", "v131", "v132", "v133", "v134", "v135", "v136", "v137", "v138", "v139", "v140", "v141", "v142", "v143", "v144", "v145", "v146", "v147", "v148", "v149", "v150", "v151", "v152", "v153", "v154", "v155", "v156", "v157", "v158", "v159", "v160", "v161", "v162", "v163", "v164", "v165", "v166", "v167", "v168", "v169", "v170", "v171", "v172", "v173", "v174", "v175", "v176", "v177", "v178", "v179", "v180", "v181", "v182", "v183", "v184", "v185", "v186", "v187", "v188", "v189", "v190", "v191", "v192", "v193", "v194", "v195", "v196", "v197", "v198", "v199", "v200", "v201", "v202", "v203", "v204", "v205", "v206", "v207", "v208", "v209", "v210", "v211", "v212", "v213", "v214", "v215", "v216", "v217", "v218", "v219", "v220", "v221", "v222", "v223", "v224", "v225", "v226", "v227", "v228", "v229", "v230", "v231", "a0", "a1", "a2", "a3", "a4", "a5", "a6", "a7", "a8", "a9", "a10", "a11", "a12", "a13", "a14", "a15", "a16", "a17", "a18", "a19", "a20", "a21", "a22", "a23", "a24", "a25", "a26", "a27", "a28", "a29", "a30", "a31", "a32", "a33", "a34", "a35", "a36", "a37", "a38", "a39", "a40", "a41", "a42", "a43", "a44", "a45", "a46", "a47", "a48", "a49", "a50", "a51", "a52", "a53", "a54", "a55", "a56", "a57", "a58", "a59", "a60", "a61", "a62", "a63", "a64", "a65", "a66", "a67", "a68", "a69", "a70", "a71", "a72", "a73", "a74", "a75", "a76", "a77", "a78", "a79", "a80", "a81", "a82", "a83", "a84", "a85", "a86", "a87", "a88", "a89", "a90", "a91", "a92", "a93", "a94", "a95", "a96", "a97", "a98", "a99", "a100", "a101", "a102", "a103", "a104", "a105", "a106", "a107", "a108", "a109", "a110", "a111", "a112", "a113", "a114", "a115", "a116", "a117", "a118", "a119", "a120", "a121", "a122", "a123", "a124", "a125", "a126", "a127", "a128", "a129", "a130", "a131", "a132", "a133", "a134", "a135", "a136", "a137", "a138", "a139", "a140", "a141", "a142", "a143", "a144", "a145", "a146", "a147", "a148", "a149", "a150", "a151", "a152", "a153", "a154", "a155", "a156", "a157", "a158", "a159", "a160", "a161", "a162", "a163", "a164", "a165", "a166", "a167", "a168", "a169", "a170", "a171", "a172", "a173", "a174", "a175", "a176", "a177", "a178", "a179", "a180", "a181", "a182", "a183", "a184", "a185", "a186", "a187", "a188", "a189", "a190", "a191", "a192", "a193", "a194", "a195", "a196", "a197", "a198", "a199", "a200", "a201", "a202", "a203", "a204", "a205", "a206", "a207", "a208", "a209", "a210", "a211", "a212", "a213", "a214", "a215", "a216", "a217", "a218", "a219", "a220", "a221", "a222", "a223", "a224", "a225", "a226", "a227", "a228", "a229", "a230", "a231", "a232", "a233", "a234", "a235", "a236", "a237", "a238", "a239", "a240", "a241", "a242", "a243", "a244", "a245", "a246", "a247", "a248", "a249", "a250", "a251", "a252", "a253", "a254", "a255", "s36", "s37", "s38", "s39", "s40", "s41", "s42", "s43", "s44", "s45", "s46", "s47", "s48", "s49", "s50", "s51", "s52", "s53", "s54", "s55", "s56", "s57", "s58", "s59", "s60", "s61", "s62", "s63", "s64", "s65", "s66", "s67", "s68", "s69", "s70", "s71", "s72", "s73", "s74", "s75", "s76", "s77", "s78", "s79", "s80", "s81", "s82", "s83", "s84", "s85", "s86", "s87", "s88", "s89", "s90", "s91", "s92", "s93", "s94", "s95", "s96", "s97", "vcc", "scc", "memory");
    if (RHELP && slice < 16) { kind = 4; rep = 5; kh = slice >> 3; slice &= 7; }
  }
  CTS(9);
#define COMMON(NF, FOFF, FSTR, SNAP, LDW, NL, TOFF)                                                                                            \
  LANE_ID(ln);                                                                                                                       \
  const int l15 = ln & 15, lg = ln >> 4, lq = l15 >> 2, lp = ln & 3, rr = 8 * lg + lq, fj = ln & 31;                                 \
  const unsigned vL16 = ln * 16, vFlag = (ln < 32 && wave == 0) ? ln * 4 : OOB;                                                      \
  const unsigned vSnapOff = (wave == 0 && fj < (NF)) ? (FOFF) + (ln >> 5) * sStep * (FSTR) : OOB;                                    \
  const unsigned vSnapRd = (SNAP) + (fj < (NF) ? ln : ln & 32) * 4;                                                                  \
  const unsigned vWd0 = (ln >> 3) * (LDW) + ((ln & 7) ^ swzB(ln >> 3)) * 16, vWd1 = ((ln >> 3) + 8) * (LDW) + ((ln & 7) ^ swzB(8 + (ln >> 3))) * 16; \
  const unsigned vWr0 = (wave >> 1) * 8192 + rr * 128 + (((wave & 1) * 4 + lp) ^ swzB(rr)) * 16, vWr1 = vWr0 + 8;                    \
  const unsigned vWr2 = 16384 + rr * 128 + ((2 * wave + (lp >> 1)) ^ swzB(rr)) * 16 + (lp & 1) * 8;                                  \
  const unsigned sWave = wave, sRk = kind == 4 ? rep : t_start, sG0 = rep < 5 ? rep : rep == 5 ? U0 + 5 : U1 + rep, sDbg = sDbg0 + (rep < 5 ? 0 : 2048)
  if (kind == 3 || kind == 5) {
    const unsigned sStep = kind == 3 ? 5 : G3OR;
    COMMON(8, fj * 128, 1024, 131072, 7168, 256, 0);
    const unsigned vOutS = (lg & 1) * 8 + (((wave & 1) * 2 + (lg >> 1)) * 16 + l15) * 16 + (4 + (wave >> 1) - wave) * 1024;
    const unsigned vAux0 = (ln & 7) * 128 + (4 * wave + lg) * 8 + ((ln >> 3) & 1) * 4;
    const float k1 = -2.0f * 0.7978845608028654f * 1.4426950408889634f;
    const unsigned vAux1 = 139264 + (l15 < 4 ? (4 * wave + lg) * 16 + l15 * 4 : 512 + ln * 4), vAux2 = 139264 + l15 * 16;
    const unsigned vAux3 = l15 == 2 ? __float_as_uint(k1 * 0.044715f) : 0u, vAn = l15 == 3 ? 0x3f800000u : 0u, vAe = l15 == 1 ? __float_as_uint(k1) : 0u;
    const unsigned sOutOff = OFF_G + (slice * 6 + wave) * 1024, sFoutOff = OFF_FG + slice * 128, sAuxOff = 0, sWw = 16 * wave * 7168 + slice * 384, sBuf = 0;
    const unsigned sExp = __builtin_amdgcn_readfirstlane(ep);
    if (kind == 3) { asm volatile(
R"(.macro M d,w,x
v_mfma_f32_16x16x32_bf16 v[\d:\d+3], \w, v[\x:\x+3], v[\d:\d+3]
.endm
.macro R d,a,o
ds_read_b128 v[\d:\d+3], v\a offset:\o
.endm
.macro T d,a,o
ds_read_b64_tr_b16 \d, v\a offset:\o
.endm
.macro D a,s,o
buffer_load_dwordx4 v\a, s[36:39], s\s offen offset:\o sc1 lds
.endm
.macro L c
s_waitcnt lgkmcnt(\c)
.endm
Lcode_g3_b:
s_mov_b64 s[36:37],%0
s_mov_b32 s38,111243392
s_mov_b32 s39,0x00020000
s_mov_b32 s44,%1
s_mov_b32 s45,%2
s_mov_b32 s46,%3
s_mov_b32 s47,%4
s_mov_b32 s48,%5
s_mov_b32 s49,%6
s_mov_b32 s50,%7
s_mov_b32 s51,%8
s_mov_b32 s53,%9
v_mov_b32 v0,%10
v_mov_b32 v3,%11
v_mov_b32 v5,%12
v_mov_b32 v6,%13
v_mov_b32 v18,%14
v_mov_b32 v19,%15
v_mov_b32 v20,%16
v_mov_b32 v21,%17
v_mov_b32 v22,%18
v_mov_b32 v10,%19
v_mov_b32 v11,%20
v_mov_b32 v12,%21
v_mov_b32 v13,%22
v_mov_b32 v14,%23
v_mov_b32 v15,%24
v_mov_b32 v16,%25
s_mov_b64 s[40:41],%26
s_mov_b32 s42,7340032
s_mov_b32 s43,0x00020000
s_lshl_b32 s57,s44,11
s_cmp_ge_u32 s45,8
s_cbranch_scc1 Lg3_dly_%=
s_sleep 16
s_sleep 16
s_sleep 16
Lg3_dly_%=:
s_add_u32 s55,s50,0
s_add_u32 m0,s57,0
s_nop 0
buffer_load_dwordx4 v18,s[40:43],s55 offen offset:0  lds
s_add_u32 m0,s57,1024
s_nop 0
buffer_load_dwordx4 v19,s[40:43],s55 offen offset:0  lds
s_add_u32 m0,s57,8064
s_nop 0
buffer_load_dwordx4 v18,s[40:43],s55 offen offset:128  lds
s_add_u32 m0,s57,9088
s_nop 0
buffer_load_dwordx4 v19,s[40:43],s55 offen offset:128  lds
s_add_u32 m0,s57,16128
s_nop 0
buffer_load_dwordx4 v18,s[40:43],s55 offen offset:256  lds
s_add_u32 m0,s57,17152
s_nop 0
buffer_load_dwordx4 v19,s[40:43],s55 offen offset:256  lds
s_add_u32 s55,s50,458752
s_add_u32 m0,s57,24576
s_nop 0
buffer_load_dwordx4 v18,s[40:43],s55 offen offset:0  lds
s_add_u32 m0,s57,25600
s_nop 0
buffer_load_dwordx4 v19,s[40:43],s55 offen offset:0  lds
s_add_u32 m0,s57,32640
s_nop 0
buffer_load_dwordx4 v18,s[40:43],s55 offen offset:128  lds
s_add_u32 m0,s57,33664
s_nop 0
buffer_load_dwordx4 v19,s[40:43],s55 offen offset:128  lds
s_add_u32 m0,s57,40704
s_nop 0
buffer_load_dwordx4 v18,s[40:43],s55 offen offset:256  lds
s_add_u32 m0,s57,41728
s_nop 0
buffer_load_dwordx4 v19,s[40:43],s55 offen offset:256  lds
s_add_u32 s55,s50,917504
s_add_u32 m0,s57,49152
s_nop 0
buffer_load_dwordx4 v18,s[40:43],s55 offen offset:0  lds
s_add_u32 m0,s57,50176
s_nop 0
buffer_load_dwordx4 v19,s[40:43],s55 offen offset:0  lds
s_add_u32 m0,s57,57216
s_nop 0
buffer_load_dwordx4 v18,s[40:43],s55 offen offset:128  lds
s_add_u32 m0,s57,58240
s_nop 0
buffer_load_dwordx4 v19,s[40:43],s55 offen offset:128  lds
s_add_u32 m0,s57,65280
s_nop 0
buffer_load_dwordx4 v18,s[40:43],s55 offen offset:256  lds
s_add_u32 m0,s57,66304
s_nop 0
buffer_load_dwordx4 v19,s[40:43],s55 offen offset:256  lds
s_add_u32 s55,s50,1376256
s_add_u32 m0,s57,73728
s_nop 0
buffer_load_dwordx4 v18,s[40:43],s55 offen offset:0  lds
s_add_u32 m0,s57,74752
s_nop 0
buffer_load_dwordx4 v19,s[40:43],s55 offen offset:0  lds
s_add_u32 m0,s57,81792
s_nop 0
buffer_load_dwordx4 v18,s[40:43],s55 offen offset:128  lds
s_add_u32 m0,s57,82816
s_nop 0
buffer_load_dwordx4 v19,s[40:43],s55 offen offset:128  lds
s_add_u32 m0,s57,89856
s_nop 0
buffer_load_dwordx4 v18,s[40:43],s55 offen offset:256  lds
s_add_u32 m0,s57,90880
s_nop 0
buffer_load_dwordx4 v19,s[40:43],s55 offen offset:256  lds
s_add_u32 s55,s50,1835008
s_add_u32 m0,s57,98304
s_nop 0
buffer_load_dwordx4 v18,s[40:43],s55 offen offset:0  lds
s_add_u32 m0,s57,99328
s_nop 0
buffer_load_dwordx4 v19,s[40:43],s55 offen offset:0  lds
s_add_u32 m0,s57,106368
s_nop 0
buffer_load_dwordx4 v18,s[40:43],s55 offen offset:128  lds
s_add_u32 m0,s57,107392
s_nop 0
buffer_load_dwordx4 v19,s[40:43],s55 offen offset:128  lds
s_add_u32 m0,s57,114432
s_nop 0
buffer_load_dwordx4 v18,s[40:43],s55 offen offset:256  lds
s_add_u32 m0,s57,115456
s_nop 0
buffer_load_dwordx4 v19,s[40:43],s55 offen offset:256  lds
s_waitcnt vmcnt(24) lgkmcnt(0)
s_barrier
s_add_u32 s55,s50,2293760
s_add_u32 m0,s57,122880
s_nop 0
buffer_load_dwordx4 v18,s[40:43],s55 offen offset:0  lds
s_add_u32 m0,s57,123904
s_nop 0
buffer_load_dwordx4 v19,s[40:43],s55 offen offset:0  lds
s_add_u32 m0,s57,130944
s_nop 0
buffer_load_dwordx4 v18,s[40:43],s55 offen offset:128  lds
s_add_u32 m0,s57,131968
s_nop 0
buffer_load_dwordx4 v19,s[40:43],s55 offen offset:128  lds
s_add_u32 m0,s57,139008
s_nop 0
buffer_load_dwordx4 v18,s[40:43],s55 offen offset:256  lds
s_add_u32 m0,s57,140032
s_nop 0
buffer_load_dwordx4 v19,s[40:43],s55 offen offset:256  lds
v_add_u32 v26,0,v20
v_add_u32 v27,0,v21
v_add_u32 v28,0,v22
T a[0:1],26,0
T a[2:3],26,512
T a[4:5],27,0
T a[6:7],27,512
T a[8:9],28,0
T a[10:11],28,512
T a[12:13],26,4096
T a[14:15],26,4608
T a[16:17],27,4096
T a[18:19],27,4608
T a[20:21],28,4096
T a[22:23],28,4608
s_waitcnt vmcnt(24) lgkmcnt(0)
s_barrier
s_add_u32 s55,s50,2752512
s_add_u32 m0,s57,0
s_nop 0
buffer_load_dwordx4 v18,s[40:43],s55 offen offset:0  lds
s_add_u32 m0,s57,1024
s_nop 0
buffer_load_dwordx4 v19,s[40:43],s55 offen offset:0  lds
s_add_u32 m0,s57,8064
s_nop 0
buffer_load_dwordx4 v18,s[40:43],s55 offen offset:128  lds
s_add_u32 m0,s57,9088
s_nop 0
buffer_load_dwordx4 v19,s[40:43],s55 offen offset:128  lds
s_add_u32 m0,s57,16128
s_nop 0
buffer_load_dwordx4 v18,s[40:43],s55 offen offset:256  lds
s_add_u32 m0,s57,17152
s_nop 0
buffer_load_dwordx4 v19,s[40:43],s55 offen offset:256  lds
v_add_u32 v26,24576,v20
v_add_u32 v27,24576,v21
v_add_u32 v28,24576,v22
T a[24:25],26,0
T a[26:27],26,512
T a[28:29],27,0
T a[30:31],27,512
T a[32:33],28,0
T a[34:35],28,512
T a[36:37],26,4096
T a[38:39],26,4608
T a[40:41],27,4096
T a[42:43],27,4608
T a[44:45],28,4096
T a[46:47],28,4608
s_waitcnt vmcnt(24) lgkmcnt(0)
s_barrier
s_add_u32 s55,s50,3211264
s_add_u32 m0,s57,24576
s_nop 0
buffer_load_dwordx4 v18,s[40:43],s55 offen offset:0  lds
s_add_u32 m0,s57,25600
s_nop 0
buffer_load_dwordx4 v19,s[40:43],s55 offen offset:0  lds
s_add_u32 m0,s57,32640
s_nop 0
buffer_load_dwordx4 v18,s[40:43],s55 offen offset:128  lds
s_add_u32 m0,s57,33664
s_nop 0
buffer_load_dwordx4 v19,s[40:43],s55 offen offset:128  lds
s_add_u32 m0,s57,40704
s_nop 0
buffer_load_dwordx4 v18,s[40:43],s55 offen offset:256  lds
s_add_u32 m0,s57,41728
s_nop 0
buffer_load_dwordx4 v19,s[40:43],s55 offen offset:256  lds
v_add_u32 v26,49152,v20
v_add_u32 v27,49152,v21
v_add_u32 v28,49152,v22
T a[48:49],26,0
T a[50:51],26,512
T a[52:53],27,0
T a[54:55],27,512
T a[56:57],28,0
T a[58:59],28,512
T a[60:61],26,4096
T a[62:63],26,4608
T a[64:65],27,4096
T a[66:67],27,4608
T a[68:69],28,4096
T a[70:71],28,4608
s_waitcnt vmcnt(24) lgkmcnt(0)
s_barrier
s_add_u32 s55,s50,3670016
s_add_u32 m0,s57,49152
s_nop 0
buffer_load_dwordx4 v18,s[40:43],s55 offen offset:0  lds
s_add_u32 m0,s57,50176
s_nop 0
buffer_load_dwordx4 v19,s[40:43],s55 offen offset:0  lds
s_add_u32 m0,s57,57216
s_nop 0
buffer_load_dwordx4 v18,s[40:43],s55 offen offset:128  lds
s_add_u32 m0,s57,58240
s_nop 0
buffer_load_dwordx4 v19,s[40:43],s55 offen offset:128  lds
s_add_u32 m0,s57,65280
s_nop 0
buffer_load_dwordx4 v18,s[40:43],s55 offen offset:256  lds
s_add_u32 m0,s57,66304
s_nop 0
buffer_load_dwordx4 v19,s[40:43],s55 offen offset:256  lds
v_add_u32 v26,73728,v20
v_add_u32 v27,73728,v21
v_add_u32 v28,73728,v22
T a[72:73],26,0
T a[74:75],26,512
T a[76:77],27,0
T a[78:79],27,512
T a[80:81],28,0
T a[82:83],28,512
T a[84:85],26,4096
T a[86:87],26,4608
T a[88:89],27,4096
T a[90:91],27,4608
T a[92:93],28,4096
T a[94:95],28,4608
s_waitcnt vmcnt(24) lgkmcnt(0)
s_barrier
s_add_u32 s55,s50,4128768
s_add_u32 m0,s57,73728
s_nop 0
buffer_load_dwordx4 v18,s[40:43],s55 offen offset:0  lds
s_add_u32 m0,s57,74752
s_nop 0
buffer_load_dwordx4 v19,s[40:43],s55 offen offset:0  lds
s_add_u32 m0,s57,81792
s_nop 0
buffer_load_dwordx4 v18,s[40:43],s55 offen offset:128  lds
s_add_u32 m0,s57,82816
s_nop 0
buffer_load_dwordx4 v19,s[40:43],s55 offen offset:128  lds
s_add_u32 m0,s57,89856
s_nop 0
buffer_load_dwordx4 v18,s[40:43],s55 offen offset:256  lds
s_add_u32 m0,s57,90880
s_nop 0
buffer_load_dwordx4 v19,s[40:43],s55 offen offset:256  lds
v_add_u32 v26,98304,v20
v_add_u32 v27,98304,v21
v_add_u32 v28,98304,v22
T a[96:97],26,0
T a[98:99],26,512
T a[100:101],27,0
T a[102:103],27,512
T a[104:105],28,0
T a[106:107],28,512
T a[108:109],26,4096
T a[110:111],26,4608
T a[112:113],27,4096
T a[114:115],27,4608
T a[116:117],28,4096
T a[118:119],28,4608
s_waitcnt vmcnt(24) lgkmcnt(0)
s_barrier
s_add_u32 s55,s50,4587520
s_add_u32 m0,s57,98304
s_nop 0
buffer_load_dwordx4 v18,s[40:43],s55 offen offset:0  lds
s_add_u32 m0,s57,99328
s_nop 0
buffer_load_dwordx4 v19,s[40:43],s55 offen offset:0  lds
s_add_u32 m0,s57,106368
s_nop 0
buffer_load_dwordx4 v18,s[40:43],s55 offen offset:128  lds
s_add_u32 m0,s57,107392
s_nop 0
buffer_load_dwordx4 v19,s[40:43],s55 offen offset:128  lds
s_add_u32 m0,s57,114432
s_nop 0
buffer_load_dwordx4 v18,s[40:43],s55 offen offset:256  lds
s_add_u32 m0,s57,115456
s_nop 0
buffer_load_dwordx4 v19,s[40:43],s55 offen offset:256  lds
v_add_u32 v26,122880,v20
v_add_u32 v27,122880,v21
v_add_u32 v28,122880,v22
T a[120:121],26,0
T a[122:123],26,512
T a[124:125],27,0
T a[126:127],27,512
T a[128:129],28,0
T a[130:131],28,512
T a[132:133],26,4096
T a[134:135],26,4608
T a[136:137],27,4096
T a[138:139],27,4608
T a[140:141],28,4096
T a[142:143],28,4608
s_waitcnt vmcnt(24) lgkmcnt(0)
s_barrier
s_add_u32 s55,s50,5046272
s_add_u32 m0,s57,122880
s_nop 0
buffer_load_dwordx4 v18,s[40:43],s55 offen offset:0  lds
s_add_u32 m0,s57,123904
s_nop 0
buffer_load_dwordx4 v19,s[40:43],s55 offen offset:0  lds
s_add_u32 m0,s57,130944
s_nop 0
buffer_load_dwordx4 v18,s[40:43],s55 offen offset:128  lds
s_add_u32 m0,s57,131968
s_nop 0
buffer_load_dwordx4 v19,s[40:43],s55 offen offset:128  lds
s_add_u32 m0,s57,139008
s_nop 0
buffer_load_dwordx4 v18,s[40:43],s55 offen offset:256  lds
s_add_u32 m0,s57,140032
s_nop 0
buffer_load_dwordx4 v19,s[40:43],s55 offen offset:256  lds
v_add_u32 v26,0,v20
v_add_u32 v27,0,v21
v_add_u32 v28,0,v22
T a[144:145],26,0
T a[146:147],26,512
T a[148:149],27,0
T a[150:151],27,512
T a[152:153],28,0
T a[154:155],28,512
T a[156:157],26,4096
T a[158:159],26,4608
T a[160:161],27,4096
T a[162:163],27,4608
T a[164:165],28,4096
T a[166:167],28,4608
s_waitcnt vmcnt(24) lgkmcnt(0)
s_barrier
s_add_u32 s55,s50,5505024
s_add_u32 m0,s57,0
s_nop 0
buffer_load_dwordx4 v18,s[40:43],s55 offen offset:0  lds
s_add_u32 m0,s57,1024
s_nop 0
buffer_load_dwordx4 v19,s[40:43],s55 offen offset:0  lds
s_add_u32 m0,s57,8064
s_nop 0
buffer_load_dwordx4 v18,s[40:43],s55 offen offset:128  lds
s_add_u32 m0,s57,9088
s_nop 0
buffer_load_dwordx4 v19,s[40:43],s55 offen offset:128  lds
s_add_u32 m0,s57,16128
s_nop 0
buffer_load_dwordx4 v18,s[40:43],s55 offen offset:256  lds
s_add_u32 m0,s57,17152
s_nop 0
buffer_load_dwordx4 v19,s[40:43],s55 offen offset:256  lds
v_add_u32 v26,24576,v20
v_add_u32 v27,24576,v21
v_add_u32 v28,24576,v22
T a[168:169],26,0
T a[170:171],26,512
T a[172:173],27,0
T a[174:175],27,512
T a[176:177],28,0
T a[178:179],28,512
T a[180:181],26,4096
T a[182:183],26,4608
T a[184:185],27,4096
T a[186:187],27,4608
T a[188:189],28,4096
T a[190:191],28,4608
s_waitcnt vmcnt(24) lgkmcnt(0)
s_barrier
s_add_u32 s55,s50,5963776
s_add_u32 m0,s57,24576
s_nop 0
buffer_load_dwordx4 v18,s[40:43],s55 offen offset:0  lds
s_add_u32 m0,s57,25600
s_nop 0
buffer_load_dwordx4 v19,s[40:43],s55 offen offset:0  lds
s_add_u32 m0,s57,32640
s_nop 0
buffer_load_dwordx4 v18,s[40:43],s55 offen offset:128  lds
s_add_u32 m0,s57,33664
s_nop 0
buffer_load_dwordx4 v19,s[40:43],s55 offen offset:128  lds
s_add_u32 m0,s57,40704
s_nop 0
buffer_load_dwordx4 v18,s[40:43],s55 offen offset:256  lds
s_add_u32 m0,s57,41728
s_nop 0
buffer_load_dwordx4 v19,s[40:43],s55 offen offset:256  lds
v_add_u32 v26,49152,v20
v_add_u32 v27,49152,v21
v_add_u32 v28,49152,v22
T a[192:193],26,0
T a[194:195],26,512
T a[196:197],27,0
T a[198:199],27,512
T a[200:201],28,0
T a[202:203],28,512
T a[204:205],26,4096
T a[206:207],26,4608
T a[208:209],27,4096
T a[210:211],27,4608
T a[212:213],28,4096
T a[214:215],28,4608
s_waitcnt vmcnt(24) lgkmcnt(0)
s_barrier
s_add_u32 s55,s50,6422528
s_add_u32 m0,s57,49152
s_nop 0
buffer_load_dwordx4 v18,s[40:43],s55 offen offset:0  lds
s_add_u32 m0,s57,50176
s_nop 0
buffer_load_dwordx4 v19,s[40:43],s55 offen offset:0  lds
s_add_u32 m0,s57,57216
s_nop 0
buffer_load_dwordx4 v18,s[40:43],s55 offen offset:128  lds
s_add_u32 m0,s57,58240
s_nop 0
buffer_load_dwordx4 v19,s[40:43],s55 offen offset:128  lds
s_add_u32 m0,s57,65280
s_nop 0
buffer_load_dwordx4 v18,s[40:43],s55 offen offset:256  lds
s_add_u32 m0,s57,66304
s_nop 0
buffer_load_dwordx4 v19,s[40:43],s55 offen offset:256  lds
v_add_u32 v26,73728,v20
v_add_u32 v27,73728,v21
v_add_u32 v28,73728,v22
T a[216:217],26,0
T a[218:219],26,512
T a[220:221],27,0
)"
R"(T a[222:223],27,512
T a[224:225],28,0
T a[226:227],28,512
T a[228:229],26,4096
T a[230:231],26,4608
T a[232:233],27,4096
T a[234:235],27,4608
T a[236:237],28,4096
T a[238:239],28,4608
s_waitcnt vmcnt(24) lgkmcnt(0)
s_barrier
s_add_u32 s55,s50,6881280
s_add_u32 m0,s57,73728
s_nop 0
buffer_load_dwordx4 v18,s[40:43],s55 offen offset:0  lds
s_add_u32 m0,s57,74752
s_nop 0
buffer_load_dwordx4 v19,s[40:43],s55 offen offset:0  lds
s_add_u32 m0,s57,81792
s_nop 0
buffer_load_dwordx4 v18,s[40:43],s55 offen offset:128  lds
s_add_u32 m0,s57,82816
s_nop 0
buffer_load_dwordx4 v19,s[40:43],s55 offen offset:128  lds
s_add_u32 m0,s57,89856
s_nop 0
buffer_load_dwordx4 v18,s[40:43],s55 offen offset:256  lds
s_add_u32 m0,s57,90880
s_nop 0
buffer_load_dwordx4 v19,s[40:43],s55 offen offset:256  lds
v_add_u32 v26,98304,v20
v_add_u32 v27,98304,v21
v_add_u32 v28,98304,v22
T a[240:241],26,0
T a[242:243],26,512
T a[244:245],27,0
T a[246:247],27,512
T a[248:249],28,0
T a[250:251],28,512
T a[252:253],26,4096
T a[254:255],26,4608
T v[128:129],27,4096
T v[130:131],27,4608
T v[132:133],28,4096
T v[134:135],28,4608
s_waitcnt vmcnt(24) lgkmcnt(0)
s_barrier
v_add_u32 v26,122880,v20
v_add_u32 v27,122880,v21
v_add_u32 v28,122880,v22
T v[136:137],26,0
T v[138:139],26,512
T v[140:141],27,0
T v[142:143],27,512
T v[144:145],28,0
T v[146:147],28,512
T v[148:149],26,4096
T v[150:151],26,4608
T v[152:153],27,4096
T v[154:155],27,4608
T v[156:157],28,4096
T v[158:159],28,4608
s_waitcnt vmcnt(18) lgkmcnt(0)
s_barrier
v_add_u32 v26,0,v20
v_add_u32 v27,0,v21
v_add_u32 v28,0,v22
T v[160:161],26,0
T v[162:163],26,512
T v[164:165],27,0
T v[166:167],27,512
T v[168:169],28,0
T v[170:171],28,512
T v[172:173],26,4096
T v[174:175],26,4608
T v[176:177],27,4096
T v[178:179],27,4608
T v[180:181],28,4096
T v[182:183],28,4608
s_waitcnt vmcnt(12) lgkmcnt(0)
s_barrier
v_add_u32 v26,24576,v20
v_add_u32 v27,24576,v21
v_add_u32 v28,24576,v22
T v[184:185],26,0
T v[186:187],26,512
T v[188:189],27,0
T v[190:191],27,512
T v[192:193],28,0
T v[194:195],28,512
T v[196:197],26,4096
T v[198:199],26,4608
T v[200:201],27,4096
T v[202:203],27,4608
T v[204:205],28,4096
T v[206:207],28,4608
s_waitcnt vmcnt(6) lgkmcnt(0)
s_barrier
v_add_u32 v26,49152,v20
v_add_u32 v27,49152,v21
v_add_u32 v28,49152,v22
T v[208:209],26,0
T v[210:211],26,512
T v[212:213],27,0
T v[214:215],27,512
T v[216:217],28,0
T v[218:219],28,512
T v[220:221],26,4096
T v[222:223],26,4608
T v[224:225],27,4096
T v[226:227],27,4608
T v[228:229],28,4096
T v[230:231],28,4608
s_waitcnt vmcnt(0) lgkmcnt(0)
s_barrier
v_add_u32 v26,73728,v20
v_add_u32 v27,73728,v21
v_add_u32 v28,73728,v22
T v[232:233],26,0
T v[234:235],26,512
T v[236:237],27,0
T v[238:239],27,512
T v[240:241],28,0
T v[242:243],28,512
T v[244:245],26,4096
T v[246:247],26,4608
T v[248:249],27,4096
T v[250:251],27,4608
T v[252:253],28,4096
T v[254:255],28,4608
L 0
s_barrier
v_mov_b32 v42,0x3f803f80
v_mov_b32 v43,0x3f803f80
v_mov_b32 v44,0x3f803f80
v_mov_b32 v45,0x3f803f80
s_nop 1
v_mfma_f32_16x16x32_bf16 v[82:85],a[0:3],v[42:45],0
v_mfma_f32_16x16x32_bf16 v[86:89],a[4:7],v[42:45],0
v_mfma_f32_16x16x32_bf16 v[90:93],a[8:11],v[42:45],0
M 82,a[12:15],42
M 86,a[16:19],42
M 90,a[20:23],42
M 82,a[24:27],42
M 86,a[28:31],42
M 90,a[32:35],42
M 82,a[36:39],42
M 86,a[40:43],42
M 90,a[44:47],42
M 82,a[48:51],42
M 86,a[52:55],42
M 90,a[56:59],42
M 82,a[60:63],42
M 86,a[64:67],42
M 90,a[68:71],42
M 82,a[72:75],42
M 86,a[76:79],42
M 90,a[80:83],42
M 82,a[84:87],42
M 86,a[88:91],42
M 90,a[92:95],42
M 82,a[96:99],42
M 86,a[100:103],42
M 90,a[104:107],42
M 82,a[108:111],42
M 86,a[112:115],42
M 90,a[116:119],42
M 82,a[120:123],42
M 86,a[124:127],42
M 90,a[128:131],42
M 82,a[132:135],42
M 86,a[136:139],42
M 90,a[140:143],42
M 82,a[144:147],42
M 86,a[148:151],42
M 90,a[152:155],42
M 82,a[156:159],42
M 86,a[160:163],42
M 90,a[164:167],42
M 82,a[168:171],42
M 86,a[172:175],42
M 90,a[176:179],42
M 82,a[180:183],42
M 86,a[184:187],42
M 90,a[188:191],42
M 82,a[192:195],42
M 86,a[196:199],42
M 90,a[200:203],42
M 82,a[204:207],42
M 86,a[208:211],42
M 90,a[212:215],42
M 82,a[216:219],42
M 86,a[220:223],42
M 90,a[224:227],42
M 82,a[228:231],42
M 86,a[232:235],42
M 90,a[236:239],42
M 82,a[240:243],42
M 86,a[244:247],42
M 90,a[248:251],42
M 82,a[252:255],42
M 86,v[128:131],42
M 90,v[132:135],42
M 82,v[136:139],42
M 86,v[140:143],42
M 90,v[144:147],42
M 82,v[148:151],42
M 86,v[152:155],42
M 90,v[156:159],42
M 82,v[160:163],42
M 86,v[164:167],42
M 90,v[168:171],42
M 82,v[172:175],42
M 86,v[176:179],42
M 90,v[180:183],42
M 82,v[184:187],42
M 86,v[188:191],42
M 90,v[192:195],42
M 82,v[196:199],42
M 86,v[200:203],42
M 90,v[204:207],42
M 82,v[208:211],42
M 86,v[212:215],42
M 90,v[216:219],42
M 82,v[220:223],42
M 86,v[224:227],42
M 90,v[228:231],42
M 82,v[232:235],42
M 86,v[236:239],42
M 90,v[240:243],42
M 82,v[244:247],42
M 86,v[248:251],42
M 90,v[252:255],42
v_mov_b32 v4,s53
s_mov_b32 s82,0
s_mov_b32 s80,0
s_mov_b32 s54,s45
s_mul_i32 s57,s44,4096
s_mul_i32 s55,s45,32768
s_add_u32 s55,s55,s57
s_add_u32 s55,s55,31457280
s_mul_i32 s57,s44,4096
s_mul_i32 s60,s46,32768
s_mul_i32 s61,s46,1024
s_mul_i32 s62,s46,114688
s_mul_i32 s63,s46,2560
s_mul_i32 s64,s46,1024
s_lshl_b32 s59,s44,8
s_add_u32 s59,s59,131072
s_mul_i32 s58,s45,1024
s_add_u32 s58,s58,109613056
s_mul_i32 s65,s45,114688
s_add_u32 s65,s65,s47
s_mul_i32 s68,s45,2560
s_add_u32 s68,s68,s48
s_mul_i32 s73,s45,1024
s_add_u32 s73,s73,107479040
s_mul_i32 s74,s45,32768
s_add_u32 s74,s74,s49
s_mul_i32 s75,s45,1024
s_add_u32 s75,s75,107479040
s_mov_b32 s66,0x7fff0000
s_mov_b32 s67,0x7fff0000
s_mov_b32 s69,0x7fff0000
s_mov_b32 s76,0x7fff0000
s_mov_b32 s77,0x7fff0000
s_mov_b32 s70,0x7fff0000
s_mov_b32 s71,0x7fff0000
s_mov_b32 s72,0x7fff0000
v_mov_b32 v1,v0
v_add_u32 v2,16384,v0
s_mov_b32 s84,16384
s_mov_b32 s86,0x3a800000
s_mov_b32 s87,0x3727c5ac
s_mov_b32 s88,0xc0135761
s_mov_b32 s89,0x3d372713
s_lshr_b32 s85,s44,1
s_mul_i32 s85,s85,127
v_lshlrev_b32 v8,22,v0
v_and_b32 v8,0x80000000,v8
v_or_b32 v8,v8,v5
v_add_u32 v2,32768,v0
s_lshl_b32 s84,s57,1
s_add_u32 s84,s84,32768
v_and_b32 v94,240,v0
v_cmp_eq_u32 vcc,0,v94
s_nop 1
v_cndmask_b32 v17,0,1.0,vcc
s_getpc_b64 s[40:41]
Lg3_pc_%=:
s_lshl_b32 s78,s44,13
v_lshlrev_b32 v94,3,v0
v_add_u32 v94,s78,v94
v_min_u32 v94,Lcode_g3_e-Lg3_pc_%=-4,v94
s_mov_b32 m0,163584
s_nop 0
global_load_lds_dword v94,s[40:41]
s_add_u32 m0,s59,4096
s_nop 0
buffer_load_dword v8,s[36:39],s58 offen offset:0 sc1 lds
buffer_store_dword v4,v3,s[36:39],s71 offen offset:0 sc0 sc1
s_add_u32 m0,s59,5120
s_nop 0
buffer_load_dword v8,s[36:39],s58 offen offset:0 sc1 lds
buffer_store_dword v4,v3,s[36:39],s71 offen offset:0 sc0 sc1
s_mov_b32 s83,0
Lg3_poll_%=:
s_add_u32 s78,s83,2
s_and_b32 s78,s78,3
s_lshl_b32 s78,s78,10
s_add_u32 s78,s78,4096
s_add_u32 m0,s59,s78
s_nop 0
buffer_load_dword v8,s[36:39],s58 offen offset:0 sc1 lds
s_and_b32 s78,s83,3
s_lshl_b32 s78,s78,10
v_add_u32 v94,s78,v6
s_waitcnt vmcnt(4)
s_barrier
ds_read_b32 v7,v94 offset:4096
buffer_store_dword v4,v3,s[36:39],s70 offen offset:0 sc0 sc1
s_mov_b32 s70,s71
s_mov_b32 s71,s72
s_mov_b32 s72,0x7fff0000
s_add_u32 s83,s83,1
s_add_u32 s82,s82,1
L 0
v_cmp_eq_u32 vcc,s53,v7
s_nop 0
s_cmp_eq_u32 vcc_lo,-1
s_cbranch_scc1 Lg3_go_%=
s_cmp_lt_u32 s82,20000
s_cbranch_scc1 Lg3_poll_%=
Lg3_go_%=:
buffer_load_dword v114,v11,s[36:39],s73 offen offset:0 sc1
s_add_u32 s73,s73,s64
s_add_u32 m0,s57,0
s_add_u32 s56,s55,0
D 0,56,0
D 0,56,1024
D 0,56,2048
D 0,56,3072
s_add_u32 s58,s58,s61
s_add_u32 m0,s59,2048
s_nop 0
buffer_load_dword v8,s[36:39],s58 offen offset:0 sc1 lds
s_add_u32 m0,s57,16384
s_add_u32 s56,s55,16384
D 0,56,0
D 0,56,1024
D 0,56,2048
D 0,56,3072
s_add_u32 s55,s55,s60
s_sub_u32 s57,s84,s57
s_add_u32 s54,s54,s46
s_waitcnt vmcnt(4)
s_barrier
buffer_store_dword v4,v3,s[36:39],s70 offen offset:0 sc0 sc1
s_min_u32 s71,s71,s72
buffer_store_dword v4,v3,s[36:39],s71 offen offset:0 sc0 sc1
s_mov_b32 s70,0x7fff0000
s_mov_b32 s71,0x7fff0000
s_mov_b32 s72,0x7fff0000
s_cmp_eq_u32 s80,1
buffer_store_dword v4,v3,s[36:39],s71 offen offset:0 sc0 sc1
s_cbranch_scc1 Lg3_st1_%=
s_branch Lg3_st0_%=
Lg3_gox_%=:
s_add_u32 m0,s59,2048
s_nop 0
buffer_load_dword v8,s[36:39],s58 offen offset:0 sc1 lds
s_waitcnt vmcnt(0)
s_barrier
buffer_store_dword v4,v3,s[36:39],s70 offen offset:0 sc0 sc1
s_min_u32 s71,s71,s72
buffer_store_dword v4,v3,s[36:39],s71 offen offset:0 sc0 sc1
s_mov_b32 s70,0x7fff0000
s_mov_b32 s71,0x7fff0000
s_mov_b32 s72,0x7fff0000
s_cmp_eq_u32 s80,1
s_cbranch_scc1 Lg3_st1_%=
Lg3_st0_%=:
ds_read_b32 v7,v6 offset:2048
R 42,1,0
R 46,1,1024
R 50,1,2048
R 54,1,3072
R 58,1,4096
L 5
v_cmp_eq_u32 vcc,s53,v7
L 3
s_cmp_eq_u32 vcc_lo,-1
s_cbranch_scc1 Lg3_F0_%=
s_branch Lg3_L0_%=
Lg3_st1_%=:
ds_read_b32 v7,v6 offset:2048
R 42,2,0
R 46,2,1024
R 50,2,2048
R 54,2,3072
R 58,2,4096
L 5
v_cmp_eq_u32 vcc,s53,v7
L 3
s_cmp_eq_u32 vcc_lo,-1
s_cbranch_scc1 Lg3_F1_%=
s_branch Lg3_L1_%=
Lg3_F0_%=:
v_mfma_f32_16x16x32_bf16 v[26:29],a[8:11],v[42:45],0
s_add_u32 m0,s57,0
R 62,1,5120
v_add_f32_dpp v115,v114,v114 quad_perm:[1,0,3,2] row_mask:0xf bank_mask:0xf
M 26,a[20:23],46
buffer_load_dword v114,v11,s[36:39],s73 offen offset:0 sc1
v_mfma_f32_16x16x32_bf16 v[22:25],a[4:7],v[42:45],0
s_add_u32 s56,s55,0
v_add_f32_dpp v115,v115,v115 quad_perm:[2,3,0,1] row_mask:0xf bank_mask:0xf
v_fma_f32 v30,-v78,v82,v30
M 22,a[16:19],46
s_add_u32 s58,s58,s61
R 66,1,6144
v_fma_f32 v31,-v78,v83,v31
v_mfma_f32_16x16x32_bf16 v[18:21],a[0:3],v[42:45],0
D 0,56,0
M 18,a[12:15],46
L 3
v_add_f32_dpp v118,v115,v115 row_half_mirror row_mask:0xf bank_mask:0xf
v_fma_f32 v32,-v78,v84,v32
M 18,a[24:27],50
s_mov_b32 s66,s65
R 70,1,7168
v_fma_f32 v33,-v78,v85,v33
M 18,a[36:39],54
s_add_u32 s65,s65,s62
v_mov_b32_dpp v115,v118 row_ror:8 row_mask:0xf bank_mask:0xf
v_fma_f32 v115,-v118,v118,v115
M 22,a[28:31],50
s_add_u32 s54,s54,s46
v_max_f32 v115,s87,v115
v_mul_f32 v118,v17,v118
M 22,a[40:43],54
s_add_u32 s73,s73,s64
R 42,1,8192
v_mul_f32 v94,v30,v30
M 26,a[32:35],50
v_rsq_f32 v116,v115
M 26,a[44:47],54
L 3
D 0,56,1024
M 26,a[56:59],58
R 46,1,9216
v_fma_f32 v115,v15,v115,v16
M 26,a[68:71],62
v_mul_f32 v119,v116,v116
v_fma_f32 v115,v14,v119,v115
M 22,a[52:55],58
v_fma_f32 v118,v116,v115,v118
v_mul_f32 v95,v31,v31
M 22,a[64:67],62
R 50,1,10240
v_mul_f32 v96,v32,v32
M 18,a[48:51],58
ds_write_b32 v12,v118 offset:0
M 18,a[60:63],62
L 4
v_mul_f32 v97,v33,v33
v_fma_f32 v94,v80,v94,v79
M 18,a[72:75],66
R 54,1,11264
v_fma_f32 v95,v80,v95,v79
M 18,a[84:87],70
v_fma_f32 v96,v80,v96,v79
v_fma_f32 v97,v80,v97,v79
M 22,a[76:79],66
v_mul_f32 v94,v30,v94
v_mul_f32 v95,v31,v95
M 22,a[88:91],70
R 58,1,12288
v_mul_f32 v96,v32,v96
M 26,a[80:83],66
D 0,56,2048
M 26,a[92:95],70
L 4
v_exp_f32 v94,v94
M 26,a[104:107],42
R 62,1,13312
v_mul_f32 v97,v33,v97
M 26,a[116:119],46
v_exp_f32 v95,v95
M 22,a[100:103],42
v_exp_f32 v96,v96
M 22,a[112:115],46
R 66,1,14336
v_fma_f32 v94,v94,v81,v81
M 18,a[96:99],42
v_exp_f32 v97,v97
M 18,a[108:111],46
L 3
v_rcp_f32 v94,v94
M 18,a[120:123],50
)"
R"(R 70,1,15360
v_fma_f32 v95,v95,v81,v81
M 18,a[132:135],54
s_waitcnt vmcnt(7)
D 0,56,3072
M 22,a[124:127],50
s_barrier
v_rcp_f32 v95,v95
M 22,a[136:139],54
s_add_u32 m0,s59,0
R 42,1,16384
v_fma_f32 v96,v96,v81,v81
M 26,a[128:131],50
s_add_u32 s56,s55,16384
v_rcp_f32 v96,v96
M 26,a[140:143],54
L 3
v_fma_f32 v97,v97,v81,v81
v_mul_f32 v30,v30,v94
M 26,a[152:155],58
s_add_u32 s55,s55,s60
R 46,1,17408
v_mul_f32 v31,v31,v95
M 26,a[164:167],62
v_rcp_f32 v97,v97
M 22,a[148:151],58
v_mul_f32 v32,v32,v96
v_mul_f32 v33,v33,v97
M 22,a[160:163],62
R 50,1,18432
v_cvt_pk_bf16_f32 v106,v30,v31
M 18,a[144:147],58
buffer_load_dword v8,s[36:39],s58 offen offset:0 sc1 lds
M 18,a[156:159],62
L 3
v_cvt_pk_bf16_f32 v107,v32,v33
v_fma_f32 v34,-v78,v86,v34
M 18,a[168:171],66
s_add_u32 m0,s57,16384
R 54,1,19456
v_fma_f32 v35,-v78,v87,v35
M 18,a[180:183],70
s_sub_u32 s57,s84,s57
v_fma_f32 v36,-v78,v88,v36
v_fma_f32 v37,-v78,v89,v37
M 22,a[172:175],66
v_mul_f32 v98,v34,v34
v_mul_f32 v99,v35,v35
M 22,a[184:187],70
R 58,1,20480
v_mul_f32 v100,v36,v36
M 26,a[176:179],66
D 0,56,0
M 26,a[188:191],70
L 3
v_mul_f32 v101,v37,v37
v_fma_f32 v98,v80,v98,v79
M 26,a[200:203],42
R 62,1,21504
v_fma_f32 v99,v80,v99,v79
M 26,a[212:215],46
v_fma_f32 v100,v80,v100,v79
v_fma_f32 v101,v80,v101,v79
M 22,a[196:199],42
v_mul_f32 v98,v34,v98
v_mul_f32 v99,v35,v99
M 22,a[208:211],46
R 66,1,22528
v_mul_f32 v100,v36,v100
M 18,a[192:195],42
v_exp_f32 v98,v98
M 18,a[204:207],46
L 3
v_exp_f32 v99,v99
M 18,a[216:219],50
R 70,1,23552
v_mul_f32 v101,v37,v101
M 18,a[228:231],54
D 0,56,1024
M 22,a[220:223],50
v_exp_f32 v100,v100
M 22,a[232:235],54
R 42,1,24576
v_fma_f32 v98,v98,v81,v81
M 26,a[224:227],50
v_exp_f32 v101,v101
M 26,a[236:239],54
L 3
v_rcp_f32 v98,v98
M 26,a[248:251],58
R 46,1,25600
v_fma_f32 v99,v99,v81,v81
M 26,v[132:135],62
v_rcp_f32 v99,v99
M 22,a[244:247],58
v_fma_f32 v100,v100,v81,v81
v_fma_f32 v101,v101,v81,v81
M 22,v[128:131],62
R 50,1,26624
v_mul_f32 v34,v34,v98
M 18,a[240:243],58
v_rcp_f32 v100,v100
M 18,a[252:255],62
L 3
v_rcp_f32 v101,v101
M 18,v[136:139],66
R 54,1,27648
v_mul_f32 v35,v35,v99
M 18,v[148:151],70
D 0,56,2048
M 22,v[140:143],66
v_mul_f32 v36,v36,v100
v_mul_f32 v37,v37,v101
M 22,v[152:155],70
R 58,1,28672
v_cvt_pk_bf16_f32 v108,v34,v35
M 26,v[144:147],66
v_cvt_pk_bf16_f32 v109,v36,v37
v_fma_f32 v38,-v78,v90,v38
M 26,v[156:159],70
L 3
v_fma_f32 v39,-v78,v91,v39
v_fma_f32 v40,-v78,v92,v40
M 26,v[168:171],42
R 62,1,29696
v_fma_f32 v41,-v78,v93,v41
M 26,v[180:183],46
v_mul_f32 v102,v38,v38
v_mul_f32 v103,v39,v39
M 22,v[164:167],42
v_mul_f32 v104,v40,v40
v_mul_f32 v105,v41,v41
M 22,v[176:179],46
R 66,1,30720
v_fma_f32 v102,v80,v102,v79
M 18,v[160:163],42
v_fma_f32 v103,v80,v103,v79
v_fma_f32 v104,v80,v104,v79
M 18,v[172:175],46
L 3
D 0,56,3072
M 18,v[184:187],50
s_nop 0
R 70,1,31744
v_fma_f32 v105,v80,v105,v79
M 18,v[196:199],54
s_waitcnt vmcnt(4)
v_mul_f32 v102,v38,v102
v_mul_f32 v103,v39,v103
M 22,v[188:191],50
s_barrier
buffer_store_dword v4,v3,s[36:39],s70 offen offset:0 sc0 sc1
M 22,v[200:203],54
s_mov_b32 s70,s69
R 42,2,0
ds_read_b32 v7,v6 offset:0
M 26,v[192:195],50
s_mov_b32 s69,s68
R 74,13,0
v_mul_f32 v104,v40,v104
M 26,v[204:207],54
L 5
v_exp_f32 v102,v102
M 26,v[216:219],58
s_add_u32 s68,s68,s63
R 46,2,1024
v_mul_f32 v105,v41,v105
M 26,v[228:231],62
v_exp_f32 v103,v103
M 22,v[212:215],58
buffer_store_dwordx4 v[106:109],v0,s[36:39],s67 offen offset:0 sc0 sc1
v_fma_f32 v102,v102,v81,v81
M 22,v[224:227],62
L 2
R 50,2,2048
v_fma_f32 v103,v103,v81,v81
M 18,v[208:211],58
v_exp_f32 v104,v104
M 18,v[220:223],62
s_nop 0
v_exp_f32 v105,v105
M 18,v[232:235],66
R 54,2,3072
v_fma_f32 v104,v104,v81,v81
M 18,v[244:247],70
v_fma_f32 v105,v105,v81,v81
v_cmp_eq_u32 vcc,s53,v7
M 22,v[236:239],66
v_rcp_f32 v102,v102
v_rcp_f32 v103,v103
v_rcp_f32 v104,v104
v_rcp_f32 v105,v105
M 22,v[248:251],70
s_cmp_eq_u32 vcc_lo,-1
R 58,2,4096
v_mul_f32 v38,v38,v102
v_mul_f32 v39,v39,v103
v_mul_f32 v40,v40,v104
v_mul_f32 v41,v41,v105
v_cvt_pk_bf16_f32 v110,v38,v39
v_cvt_pk_bf16_f32 v111,v40,v41
M 26,v[240:243],66
L 3
buffer_store_dwordx2 v[110:111],v10,s[36:39],s67 offen offset:0 sc0 sc1
M 26,v[252:255],70
s_cbranch_scc0 Lg3_L1_%=
Lg3_F1_%=:
v_mfma_f32_16x16x32_bf16 v[38:41],a[8:11],v[42:45],0
s_add_u32 m0,s57,0
R 62,2,5120
v_add_f32_dpp v115,v114,v114 quad_perm:[1,0,3,2] row_mask:0xf bank_mask:0xf
M 38,a[20:23],46
buffer_load_dword v114,v11,s[36:39],s73 offen offset:0 sc1
v_mfma_f32_16x16x32_bf16 v[34:37],a[4:7],v[42:45],0
s_add_u32 s56,s55,0
v_add_f32_dpp v115,v115,v115 quad_perm:[2,3,0,1] row_mask:0xf bank_mask:0xf
v_fma_f32 v18,-v74,v82,v18
M 34,a[16:19],46
s_add_u32 s58,s58,s61
R 66,2,6144
v_fma_f32 v19,-v74,v83,v19
v_mfma_f32_16x16x32_bf16 v[30:33],a[0:3],v[42:45],0
D 0,56,0
M 30,a[12:15],46
L 3
v_add_f32_dpp v118,v115,v115 row_half_mirror row_mask:0xf bank_mask:0xf
v_fma_f32 v20,-v74,v84,v20
M 30,a[24:27],50
s_mov_b32 s67,s65
R 70,2,7168
v_fma_f32 v21,-v74,v85,v21
M 30,a[36:39],54
s_add_u32 s65,s65,s62
v_mov_b32_dpp v115,v118 row_ror:8 row_mask:0xf bank_mask:0xf
v_fma_f32 v115,-v118,v118,v115
M 34,a[28:31],50
s_add_u32 s54,s54,s46
v_max_f32 v115,s87,v115
v_mul_f32 v118,v17,v118
M 34,a[40:43],54
s_add_u32 s73,s73,s64
R 42,2,8192
v_mul_f32 v94,v18,v18
M 38,a[32:35],50
v_rsq_f32 v116,v115
M 38,a[44:47],54
L 3
D 0,56,1024
M 38,a[56:59],58
R 46,2,9216
v_fma_f32 v115,v15,v115,v16
M 38,a[68:71],62
v_mul_f32 v119,v116,v116
v_fma_f32 v115,v14,v119,v115
M 34,a[52:55],58
v_fma_f32 v118,v116,v115,v118
v_mul_f32 v95,v19,v19
M 34,a[64:67],62
R 50,2,10240
v_mul_f32 v96,v20,v20
M 30,a[48:51],58
ds_write_b32 v12,v118 offset:2048
M 30,a[60:63],62
L 4
v_mul_f32 v97,v21,v21
v_fma_f32 v94,v76,v94,v75
M 30,a[72:75],66
R 54,2,11264
v_fma_f32 v95,v76,v95,v75
M 30,a[84:87],70
v_fma_f32 v96,v76,v96,v75
v_fma_f32 v97,v76,v97,v75
M 34,a[76:79],66
v_mul_f32 v94,v18,v94
v_mul_f32 v95,v19,v95
M 34,a[88:91],70
R 58,2,12288
v_mul_f32 v96,v20,v96
M 38,a[80:83],66
D 0,56,2048
M 38,a[92:95],70
L 4
v_exp_f32 v94,v94
M 38,a[104:107],42
R 62,2,13312
v_mul_f32 v97,v21,v97
M 38,a[116:119],46
v_exp_f32 v95,v95
M 34,a[100:103],42
v_exp_f32 v96,v96
M 34,a[112:115],46
R 66,2,14336
v_fma_f32 v94,v94,v77,v77
M 30,a[96:99],42
v_exp_f32 v97,v97
M 30,a[108:111],46
L 3
v_rcp_f32 v94,v94
M 30,a[120:123],50
R 70,2,15360
v_fma_f32 v95,v95,v77,v77
M 30,a[132:135],54
s_waitcnt vmcnt(7)
D 0,56,3072
M 34,a[124:127],50
s_barrier
v_rcp_f32 v95,v95
M 34,a[136:139],54
s_add_u32 m0,s59,1024
R 42,2,16384
v_fma_f32 v96,v96,v77,v77
M 38,a[128:131],50
s_add_u32 s56,s55,16384
v_rcp_f32 v96,v96
M 38,a[140:143],54
L 3
v_fma_f32 v97,v97,v77,v77
v_mul_f32 v18,v18,v94
M 38,a[152:155],58
s_add_u32 s55,s55,s60
R 46,2,17408
v_mul_f32 v19,v19,v95
M 38,a[164:167],62
v_rcp_f32 v97,v97
M 34,a[148:151],58
v_mul_f32 v20,v20,v96
v_mul_f32 v21,v21,v97
M 34,a[160:163],62
R 50,2,18432
v_cvt_pk_bf16_f32 v106,v18,v19
M 30,a[144:147],58
buffer_load_dword v8,s[36:39],s58 offen offset:0 sc1 lds
M 30,a[156:159],62
L 3
v_cvt_pk_bf16_f32 v107,v20,v21
v_fma_f32 v22,-v74,v86,v22
M 30,a[168:171],66
s_add_u32 m0,s57,16384
R 54,2,19456
v_fma_f32 v23,-v74,v87,v23
M 30,a[180:183],70
s_sub_u32 s57,s84,s57
v_fma_f32 v24,-v74,v88,v24
v_fma_f32 v25,-v74,v89,v25
M 34,a[172:175],66
v_mul_f32 v98,v22,v22
v_mul_f32 v99,v23,v23
M 34,a[184:187],70
R 58,2,20480
v_mul_f32 v100,v24,v24
M 38,a[176:179],66
D 0,56,0
M 38,a[188:191],70
L 3
v_mul_f32 v101,v25,v25
v_fma_f32 v98,v76,v98,v75
M 38,a[200:203],42
R 62,2,21504
v_fma_f32 v99,v76,v99,v75
M 38,a[212:215],46
v_fma_f32 v100,v76,v100,v75
v_fma_f32 v101,v76,v101,v75
M 34,a[196:199],42
v_mul_f32 v98,v22,v98
v_mul_f32 v99,v23,v99
M 34,a[208:211],46
R 66,2,22528
v_mul_f32 v100,v24,v100
M 30,a[192:195],42
v_exp_f32 v98,v98
M 30,a[204:207],46
L 3
v_exp_f32 v99,v99
M 30,a[216:219],50
R 70,2,23552
v_mul_f32 v101,v25,v101
M 30,a[228:231],54
D 0,56,1024
M 34,a[220:223],50
v_exp_f32 v100,v100
M 34,a[232:235],54
R 42,2,24576
v_fma_f32 v98,v98,v77,v77
M 38,a[224:227],50
v_exp_f32 v101,v101
M 38,a[236:239],54
L 3
v_rcp_f32 v98,v98
M 38,a[248:251],58
R 46,2,25600
v_fma_f32 v99,v99,v77,v77
M 38,v[132:135],62
v_rcp_f32 v99,v99
M 34,a[244:247],58
v_fma_f32 v100,v100,v77,v77
v_fma_f32 v101,v101,v77,v77
M 34,v[128:131],62
R 50,2,26624
v_mul_f32 v22,v22,v98
M 30,a[240:243],58
v_rcp_f32 v100,v100
M 30,a[252:255],62
L 3
v_rcp_f32 v101,v101
M 30,v[136:139],66
R 54,2,27648
v_mul_f32 v23,v23,v99
M 30,v[148:151],70
D 0,56,2048
M 34,v[140:143],66
v_mul_f32 v24,v24,v100
v_mul_f32 v25,v25,v101
M 34,v[152:155],70
R 58,2,28672
v_cvt_pk_bf16_f32 v108,v22,v23
M 38,v[144:147],66
v_cvt_pk_bf16_f32 v109,v24,v25
v_fma_f32 v26,-v74,v90,v26
M 38,v[156:159],70
L 3
v_fma_f32 v27,-v74,v91,v27
v_fma_f32 v28,-v74,v92,v28
M 38,v[168:171],42
R 62,2,29696
v_fma_f32 v29,-v74,v93,v29
M 38,v[180:183],46
v_mul_f32 v102,v26,v26
v_mul_f32 v103,v27,v27
M 34,v[164:167],42
v_mul_f32 v104,v28,v28
v_mul_f32 v105,v29,v29
M 34,v[176:179],46
R 66,2,30720
v_fma_f32 v102,v76,v102,v75
M 30,v[160:163],42
v_fma_f32 v103,v76,v103,v75
v_fma_f32 v104,v76,v104,v75
M 30,v[172:175],46
L 3
D 0,56,3072
M 30,v[184:187],50
s_nop 0
R 70,2,31744
v_fma_f32 v105,v76,v105,v75
M 30,v[196:199],54
s_waitcnt vmcnt(4)
v_mul_f32 v102,v26,v102
v_mul_f32 v103,v27,v103
M 34,v[188:191],50
s_barrier
buffer_store_dword v4,v3,s[36:39],s70 offen offset:0 sc0 sc1
M 34,v[200:203],54
s_mov_b32 s70,s69
R 42,1,0
ds_read_b32 v7,v6 offset:1024
M 38,v[192:195],50
s_mov_b32 s69,s68
R 78,13,2048
v_mul_f32 v104,v28,v104
M 38,v[204:207],54
L 5
v_exp_f32 v102,v102
M 38,v[216:219],58
s_add_u32 s68,s68,s63
R 46,1,1024
v_mul_f32 v105,v29,v105
M 38,v[228:231],62
v_exp_f32 v103,v103
M 34,v[212:215],58
buffer_store_dwordx4 v[106:109],v0,s[36:39],s66 offen offset:0 sc0 sc1
v_fma_f32 v102,v102,v77,v77
M 34,v[224:227],62
L 2
R 50,1,2048
v_fma_f32 v103,v103,v77,v77
M 30,v[208:211],58
v_exp_f32 v104,v104
M 30,v[220:223],62
s_nop 0
v_exp_f32 v105,v105
M 30,v[232:235],66
R 54,1,3072
v_fma_f32 v104,v104,v77,v77
M 30,v[244:247],70
v_fma_f32 v105,v105,v77,v77
v_cmp_eq_u32 vcc,s53,v7
M 34,v[236:239],66
v_rcp_f32 v102,v102
v_rcp_f32 v103,v103
v_rcp_f32 v104,v104
v_rcp_f32 v105,v105
M 34,v[248:251],70
s_cmp_eq_u32 vcc_lo,-1
R 58,1,4096
v_mul_f32 v26,v26,v102
v_mul_f32 v27,v27,v103
v_mul_f32 v28,v28,v104
v_mul_f32 v29,v29,v105
v_cvt_pk_bf16_f32 v110,v26,v27
v_cvt_pk_bf16_f32 v111,v28,v29
)"
R"(M 38,v[240:243],66
L 3
buffer_store_dwordx2 v[110:111],v10,s[36:39],s66 offen offset:0 sc0 sc1
M 38,v[252:255],70
s_cbranch_scc1 Lg3_F0_%=
Lg3_L0_%=:
v_mfma_f32_16x16x32_bf16 v[26:29],a[8:11],v[42:45],0
s_add_u32 m0,s59,3072
R 62,1,5120
v_add_f32_dpp v115,v114,v114 quad_perm:[1,0,3,2] row_mask:0xf bank_mask:0xf
M 26,a[20:23],46
buffer_load_dword v8,s[36:39],s58 offen offset:0 sc1 lds
v_mfma_f32_16x16x32_bf16 v[22:25],a[4:7],v[42:45],0
s_mov_b32 s66,s65
v_add_f32_dpp v115,v115,v115 quad_perm:[2,3,0,1] row_mask:0xf bank_mask:0xf
v_fma_f32 v30,-v78,v82,v30
M 22,a[16:19],46
s_add_u32 s65,s65,s62
R 66,1,6144
v_fma_f32 v31,-v78,v83,v31
v_mfma_f32_16x16x32_bf16 v[18:21],a[0:3],v[42:45],0
v_add_f32_dpp v118,v115,v115 row_half_mirror row_mask:0xf bank_mask:0xf
v_fma_f32 v32,-v78,v84,v32
v_fma_f32 v33,-v78,v85,v33
M 18,a[12:15],46
L 3
v_mul_f32 v94,v30,v30
v_mul_f32 v95,v31,v31
M 18,a[24:27],50
R 70,1,7168
v_mov_b32_dpp v115,v118 row_ror:8 row_mask:0xf bank_mask:0xf
M 18,a[36:39],54
v_fma_f32 v115,-v118,v118,v115
v_max_f32 v115,s87,v115
M 22,a[28:31],50
v_rsq_f32 v116,v115
M 22,a[40:43],54
R 42,1,8192
v_fma_f32 v115,v15,v115,v16
v_mul_f32 v118,v17,v118
M 26,a[32:35],50
v_mul_f32 v119,v116,v116
v_fma_f32 v115,v14,v119,v115
M 26,a[44:47],54
L 3
v_fma_f32 v118,v116,v115,v118
v_mul_f32 v96,v32,v32
M 26,a[56:59],58
R 46,1,9216
v_mul_f32 v97,v33,v33
M 26,a[68:71],62
ds_write_b32 v12,v118 offset:0
M 22,a[52:55],58
v_fma_f32 v94,v80,v94,v79
v_fma_f32 v95,v80,v95,v79
v_fma_f32 v96,v80,v96,v79
M 22,a[64:67],62
R 50,1,10240
v_fma_f32 v97,v80,v97,v79
M 18,a[48:51],58
v_mul_f32 v94,v30,v94
v_mul_f32 v95,v31,v95
M 18,a[60:63],62
L 4
v_exp_f32 v94,v94
M 18,a[72:75],66
R 54,1,11264
v_mul_f32 v96,v32,v96
M 18,a[84:87],70
v_exp_f32 v95,v95
v_mul_f32 v97,v33,v97
M 22,a[76:79],66
v_exp_f32 v96,v96
M 22,a[88:91],70
R 58,1,12288
v_fma_f32 v94,v94,v81,v81
M 26,a[80:83],66
v_exp_f32 v97,v97
M 26,a[92:95],70
L 4
v_rcp_f32 v94,v94
M 26,a[104:107],42
R 62,1,13312
v_fma_f32 v95,v95,v81,v81
v_fma_f32 v96,v96,v81,v81
M 26,a[116:119],46
v_rcp_f32 v95,v95
M 22,a[100:103],42
v_rcp_f32 v96,v96
M 22,a[112:115],46
R 66,1,14336
v_fma_f32 v97,v97,v81,v81
M 18,a[96:99],42
v_rcp_f32 v97,v97
M 18,a[108:111],46
L 3
v_mul_f32 v30,v30,v94
v_mul_f32 v31,v31,v95
v_mul_f32 v32,v32,v96
M 18,a[120:123],50
R 70,1,15360
v_mul_f32 v33,v33,v97
M 18,a[132:135],54
s_waitcnt vmcnt(0)
v_cvt_pk_bf16_f32 v106,v30,v31
v_cvt_pk_bf16_f32 v107,v32,v33
M 22,a[124:127],50
s_barrier
ds_read_b32 v7,v6 offset:3072
v_fma_f32 v34,-v78,v86,v34
M 22,a[136:139],54
R 42,1,16384
v_fma_f32 v35,-v78,v87,v35
M 26,a[128:131],50
buffer_store_dword v4,v3,s[36:39],s70 offen offset:0 sc0 sc1
v_fma_f32 v36,-v78,v88,v36
M 26,a[140:143],54
L 4
v_fma_f32 v37,-v78,v89,v37
v_mul_f32 v98,v34,v34
M 26,a[152:155],58
s_mov_b32 s70,0x7fff0000
R 46,1,17408
v_mul_f32 v99,v35,v35
M 26,a[164:167],62
v_mul_f32 v100,v36,v36
v_mul_f32 v101,v37,v37
M 22,a[148:151],58
L 2
v_cmp_eq_u32 vcc,s53,v7
v_fma_f32 v98,v80,v98,v79
M 22,a[160:163],62
R 50,1,18432
v_fma_f32 v99,v80,v99,v79
v_fma_f32 v100,v80,v100,v79
M 18,a[144:147],58
v_fma_f32 v101,v80,v101,v79
v_mul_f32 v98,v34,v98
M 18,a[156:159],62
s_cmp_eq_u32 vcc_lo,-1
s_nop 0
v_mul_f32 v99,v35,v99
s_cbranch_scc0 Lg3_X0_%=
M 18,a[168:171],66
s_add_u32 m0,s57,0
s_add_u32 s56,s55,0
s_add_u32 s58,s58,s61
R 54,1,19456
M 18,a[180:183],70
buffer_load_dword v114,v11,s[36:39],s73 offen offset:0 sc1
M 22,a[172:175],66
D 0,56,0
v_mul_f32 v100,v36,v100
M 22,a[184:187],70
s_add_u32 s54,s54,s46
R 58,1,20480
v_mul_f32 v101,v37,v101
M 26,a[176:179],66
s_add_u32 s73,s73,s64
v_exp_f32 v98,v98
M 26,a[188:191],70
L 3
v_exp_f32 v99,v99
M 26,a[200:203],42
R 62,1,21504
v_fma_f32 v98,v98,v81,v81
M 26,a[212:215],46
D 0,56,1024
v_fma_f32 v99,v99,v81,v81
M 22,a[196:199],42
v_exp_f32 v100,v100
M 22,a[208:211],46
R 66,1,22528
v_fma_f32 v100,v100,v81,v81
M 18,a[192:195],42
D 0,56,2048
M 18,a[204:207],46
L 3
v_exp_f32 v101,v101
M 18,a[216:219],50
R 70,1,23552
v_rcp_f32 v98,v98
M 18,a[228:231],54
v_rcp_f32 v99,v99
M 22,a[220:223],50
D 0,56,3072
M 22,a[232:235],54
s_add_u32 m0,s59,0
R 42,1,24576
v_fma_f32 v101,v101,v81,v81
M 26,a[224:227],50
buffer_load_dword v8,s[36:39],s58 offen offset:0 sc1 lds
M 26,a[236:239],54
L 3
v_rcp_f32 v100,v100
v_mul_f32 v34,v34,v98
M 26,a[248:251],58
s_add_u32 m0,s57,16384
R 46,1,25600
v_mul_f32 v35,v35,v99
M 26,v[132:135],62
s_add_u32 s56,s55,16384
v_rcp_f32 v101,v101
M 22,a[244:247],58
D 0,56,0
M 22,v[128:131],62
s_add_u32 s55,s55,s60
R 50,1,26624
v_mul_f32 v36,v36,v100
M 18,a[240:243],58
s_sub_u32 s57,s84,s57
v_mul_f32 v37,v37,v101
v_cvt_pk_bf16_f32 v108,v34,v35
v_cvt_pk_bf16_f32 v109,v36,v37
M 18,a[252:255],62
L 3
buffer_store_dwordx4 v[106:109],v0,s[36:39],s67 offen offset:0 sc0 sc1
v_fma_f32 v38,-v78,v90,v38
M 18,v[136:139],66
R 54,1,27648
v_fma_f32 v39,-v78,v91,v39
M 18,v[148:151],70
v_fma_f32 v40,-v78,v92,v40
v_fma_f32 v41,-v78,v93,v41
M 22,v[140:143],66
D 0,56,1024
M 22,v[152:155],70
R 58,1,28672
v_mul_f32 v102,v38,v38
v_mul_f32 v103,v39,v39
M 26,v[144:147],66
v_mul_f32 v104,v40,v40
v_mul_f32 v105,v41,v41
M 26,v[156:159],70
L 3
v_fma_f32 v102,v80,v102,v79
v_fma_f32 v103,v80,v103,v79
M 26,v[168:171],42
R 62,1,29696
v_fma_f32 v104,v80,v104,v79
M 26,v[180:183],46
D 0,56,2048
M 22,v[164:167],42
v_fma_f32 v105,v80,v105,v79
v_mul_f32 v102,v38,v102
v_mul_f32 v103,v39,v103
M 22,v[176:179],46
R 66,1,30720
v_mul_f32 v104,v40,v104
M 18,v[160:163],42
v_exp_f32 v102,v102
M 18,v[172:175],46
L 3
D 0,56,3072
M 18,v[184:187],50
s_nop 0
R 70,1,31744
v_mul_f32 v105,v41,v105
M 18,v[196:199],54
s_waitcnt vmcnt(5)
v_exp_f32 v103,v103
v_fma_f32 v102,v102,v81,v81
M 22,v[188:191],50
s_barrier
buffer_store_dword v4,v3,s[36:39],s70 offen offset:0 sc0 sc1
M 22,v[200:203],54
s_mov_b32 s70,s69
R 42,2,0
ds_read_b32 v7,v6 offset:0
M 26,v[192:195],50
s_mov_b32 s69,s68
R 74,13,0
v_fma_f32 v103,v103,v81,v81
M 26,v[204:207],54
L 5
v_exp_f32 v104,v104
M 26,v[216:219],58
s_add_u32 s68,s68,s63
R 46,2,1024
v_exp_f32 v105,v105
M 26,v[228:231],62
v_rcp_f32 v102,v102
M 22,v[212:215],58
v_rcp_f32 v103,v103
M 22,v[224:227],62
L 2
R 50,2,2048
v_fma_f32 v104,v104,v81,v81
M 18,v[208:211],58
v_rcp_f32 v104,v104
M 18,v[220:223],62
s_nop 0
v_fma_f32 v105,v105,v81,v81
v_rcp_f32 v105,v105
M 18,v[232:235],66
R 54,2,3072
v_mul_f32 v38,v38,v102
M 18,v[244:247],70
v_mul_f32 v39,v39,v103
v_mul_f32 v40,v40,v104
M 22,v[236:239],66
v_cmp_eq_u32 vcc,s53,v7
v_mul_f32 v41,v41,v105
M 22,v[248:251],70
R 58,2,4096
v_cvt_pk_bf16_f32 v110,v38,v39
v_cvt_pk_bf16_f32 v111,v40,v41
M 26,v[240:243],66
L 3
s_cmp_eq_u32 vcc_lo,-1
buffer_store_dwordx2 v[110:111],v10,s[36:39],s67 offen offset:0 sc0 sc1
M 26,v[252:255],70
buffer_store_dword v4,v3,s[36:39],s71 offen offset:0 sc0 sc1
s_cbranch_scc1 Lg3_F1_%=
s_branch Lg3_L1_%=
Lg3_X0_%=:
M 18,a[168:171],66
s_add_u32 m0,s59,0
R 54,1,19456
v_mul_f32 v100,v36,v100
M 18,a[180:183],70
buffer_load_dword v8,s[36:39],s58 offen offset:0 sc1 lds
M 22,a[172:175],66
v_exp_f32 v98,v98
M 22,a[184:187],70
R 58,1,20480
v_mul_f32 v101,v37,v101
M 26,a[176:179],66
v_exp_f32 v99,v99
M 26,a[188:191],70
L 3
v_exp_f32 v100,v100
M 26,a[200:203],42
R 62,1,21504
v_fma_f32 v98,v98,v81,v81
M 26,a[212:215],46
v_exp_f32 v101,v101
M 22,a[196:199],42
v_rcp_f32 v98,v98
M 22,a[208:211],46
R 66,1,22528
v_fma_f32 v99,v99,v81,v81
M 18,a[192:195],42
v_rcp_f32 v99,v99
M 18,a[204:207],46
L 3
v_fma_f32 v100,v100,v81,v81
v_fma_f32 v101,v101,v81,v81
M 18,a[216:219],50
R 70,1,23552
v_mul_f32 v34,v34,v98
M 18,a[228:231],54
v_rcp_f32 v100,v100
M 22,a[220:223],50
v_rcp_f32 v101,v101
M 22,a[232:235],54
R 42,1,24576
v_mul_f32 v35,v35,v99
M 26,a[224:227],50
v_mul_f32 v36,v36,v100
v_mul_f32 v37,v37,v101
M 26,a[236:239],54
L 3
v_cvt_pk_bf16_f32 v108,v34,v35
v_cvt_pk_bf16_f32 v109,v36,v37
M 26,a[248:251],58
R 46,1,25600
v_fma_f32 v38,-v78,v90,v38
M 26,v[132:135],62
buffer_store_dwordx4 v[106:109],v0,s[36:39],s67 offen offset:0 sc0 sc1
v_fma_f32 v39,-v78,v91,v39
M 22,a[244:247],58
v_fma_f32 v40,-v78,v92,v40
v_fma_f32 v41,-v78,v93,v41
M 22,v[128:131],62
R 50,1,26624
v_mul_f32 v102,v38,v38
M 18,a[240:243],58
v_mul_f32 v103,v39,v39
v_mul_f32 v104,v40,v40
M 18,a[252:255],62
L 3
v_mul_f32 v105,v41,v41
v_fma_f32 v102,v80,v102,v79
M 18,v[136:139],66
R 54,1,27648
v_fma_f32 v103,v80,v103,v79
M 18,v[148:151],70
v_fma_f32 v104,v80,v104,v79
v_fma_f32 v105,v80,v105,v79
M 22,v[140:143],66
v_mul_f32 v102,v38,v102
v_mul_f32 v103,v39,v103
M 22,v[152:155],70
R 58,1,28672
v_mul_f32 v104,v40,v104
M 26,v[144:147],66
v_exp_f32 v102,v102
M 26,v[156:159],70
L 3
v_exp_f32 v103,v103
M 26,v[168:171],42
R 62,1,29696
v_mul_f32 v105,v41,v105
M 26,v[180:183],46
v_exp_f32 v104,v104
M 22,v[164:167],42
v_exp_f32 v105,v105
M 22,v[176:179],46
R 66,1,30720
v_fma_f32 v102,v102,v81,v81
M 18,v[160:163],42
v_rcp_f32 v102,v102
M 18,v[172:175],46
L 3
v_fma_f32 v103,v103,v81,v81
v_fma_f32 v104,v104,v81,v81
M 18,v[184:187],50
s_nop 0
R 70,1,31744
v_fma_f32 v105,v105,v81,v81
M 18,v[196:199],54
s_waitcnt vmcnt(1)
v_rcp_f32 v103,v103
M 22,v[188:191],50
s_barrier
buffer_store_dword v4,v3,s[36:39],s70 offen offset:0 sc0 sc1
M 22,v[200:203],54
s_mov_b32 s70,s69
ds_read_b32 v7,v6 offset:0
R 74,13,0
M 26,v[192:195],50
s_mov_b32 s69,s68
v_rcp_f32 v104,v104
M 26,v[204:207],54
L 4
v_rcp_f32 v105,v105
M 26,v[216:219],58
s_add_u32 s68,s68,s63
v_mul_f32 v38,v38,v102
v_mul_f32 v39,v39,v103
M 26,v[228:231],62
v_mul_f32 v40,v40,v104
v_mul_f32 v41,v41,v105
M 22,v[212:215],58
v_cvt_pk_bf16_f32 v110,v38,v39
v_cvt_pk_bf16_f32 v111,v40,v41
M 22,v[224:227],62
L 1
buffer_store_dwordx2 v[110:111],v10,s[36:39],s67 offen offset:0 sc0 sc1
M 18,v[208:211],58
v_cmp_eq_u32 vcc,s53,v7
M 18,v[220:223],62
s_nop 0
M 18,v[232:235],66
M 18,v[244:247],70
M 22,v[236:239],66
M 22,v[248:251],70
M 26,v[240:243],66
M 26,v[252:255],70
s_mov_b32 s80,1
s_cmp_eq_u32 vcc_lo,-1
s_cbranch_scc0 Lg3_xn0_%=
buffer_load_dword v114,v11,s[36:39],s73 offen offset:0 sc1
L 0
v_fma_f32 v18,-v74,v82,v18
v_fma_f32 v19,-v74,v83,v19
v_fma_f32 v20,-v74,v84,v20
v_fma_f32 v21,-v74,v85,v21
v_mul_f32 v94,v18,v18
v_mul_f32 v95,v19,v19
v_mul_f32 v96,v20,v20
v_mul_f32 v97,v21,v21
s_add_u32 s73,s73,s64
s_add_u32 m0,s57,0
s_add_u32 s56,s55,0
D 0,56,0
v_fma_f32 v94,v76,v94,v75
v_fma_f32 v95,v76,v95,v75
v_fma_f32 v96,v76,v96,v75
v_fma_f32 v97,v76,v97,v75
v_mul_f32 v94,v18,v94
v_mul_f32 v95,v19,v95
v_mul_f32 v96,v20,v96
v_mul_f32 v97,v21,v97
D 0,56,1024
v_exp_f32 v94,v94
v_exp_f32 v95,v95
v_exp_f32 v96,v96
v_exp_f32 v97,v97
v_fma_f32 v94,v94,v77,v77
v_fma_f32 v95,v95,v77,v77
v_fma_f32 v96,v96,v77,v77
v_fma_f32 v97,v97,v77,v77
D 0,56,2048
v_rcp_f32 v94,v94
v_rcp_f32 v95,v95
v_rcp_f32 v96,v96
v_rcp_f32 v97,v97
v_mul_f32 v18,v18,v94
)"
R"(v_mul_f32 v19,v19,v95
v_mul_f32 v20,v20,v96
v_mul_f32 v21,v21,v97
D 0,56,3072
v_cvt_pk_bf16_f32 v106,v18,v19
v_cvt_pk_bf16_f32 v107,v20,v21
v_fma_f32 v22,-v74,v86,v22
v_fma_f32 v23,-v74,v87,v23
v_fma_f32 v24,-v74,v88,v24
v_fma_f32 v25,-v74,v89,v25
v_mul_f32 v98,v22,v22
v_mul_f32 v99,v23,v23
s_add_u32 s58,s58,s61
s_add_u32 m0,s57,16384
s_add_u32 s56,s55,16384
D 0,56,0
v_mul_f32 v100,v24,v24
v_mul_f32 v101,v25,v25
v_fma_f32 v98,v76,v98,v75
v_fma_f32 v99,v76,v99,v75
v_fma_f32 v100,v76,v100,v75
v_fma_f32 v101,v76,v101,v75
v_mul_f32 v98,v22,v98
v_mul_f32 v99,v23,v99
D 0,56,1024
v_mul_f32 v100,v24,v100
v_mul_f32 v101,v25,v101
v_exp_f32 v98,v98
v_exp_f32 v99,v99
v_exp_f32 v100,v100
v_exp_f32 v101,v101
v_fma_f32 v98,v98,v77,v77
v_fma_f32 v99,v99,v77,v77
D 0,56,2048
v_fma_f32 v100,v100,v77,v77
v_fma_f32 v101,v101,v77,v77
v_rcp_f32 v98,v98
v_rcp_f32 v99,v99
v_rcp_f32 v100,v100
v_rcp_f32 v101,v101
v_mul_f32 v22,v22,v98
v_mul_f32 v23,v23,v99
D 0,56,3072
v_mul_f32 v24,v24,v100
v_mul_f32 v25,v25,v101
v_cvt_pk_bf16_f32 v108,v22,v23
v_cvt_pk_bf16_f32 v109,v24,v25
buffer_store_dwordx4 v[106:109],v0,s[36:39],s66 offen offset:0 sc0 sc1
v_fma_f32 v26,-v74,v90,v26
v_fma_f32 v27,-v74,v91,v27
v_fma_f32 v28,-v74,v92,v28
v_fma_f32 v29,-v74,v93,v29
s_add_u32 s55,s55,s60
s_sub_u32 s57,s84,s57
s_add_u32 s54,s54,s46
v_mul_f32 v102,v26,v26
v_mul_f32 v103,v27,v27
v_mul_f32 v104,v28,v28
v_mul_f32 v105,v29,v29
v_fma_f32 v102,v76,v102,v75
v_fma_f32 v103,v76,v103,v75
v_fma_f32 v104,v76,v104,v75
v_fma_f32 v105,v76,v105,v75
v_mul_f32 v102,v26,v102
v_mul_f32 v103,v27,v103
v_mul_f32 v104,v28,v104
v_mul_f32 v105,v29,v105
v_exp_f32 v102,v102
v_exp_f32 v103,v103
v_exp_f32 v104,v104
v_exp_f32 v105,v105
v_fma_f32 v102,v102,v77,v77
v_fma_f32 v103,v103,v77,v77
v_fma_f32 v104,v104,v77,v77
v_fma_f32 v105,v105,v77,v77
v_rcp_f32 v102,v102
v_rcp_f32 v103,v103
v_rcp_f32 v104,v104
v_rcp_f32 v105,v105
v_mul_f32 v26,v26,v102
v_mul_f32 v27,v27,v103
v_mul_f32 v28,v28,v104
v_mul_f32 v29,v29,v105
v_cvt_pk_bf16_f32 v110,v26,v27
v_cvt_pk_bf16_f32 v111,v28,v29
buffer_store_dwordx2 v[110:111],v10,s[36:39],s66 offen offset:0 sc0 sc1
s_mov_b32 s72,s69
s_mov_b32 s69,0x7fff0000
s_mov_b32 s66,0x7fff0000
s_mov_b32 s76,0x7fff0000
s_branch Lg3_gox_%=
Lg3_xn0_%=:
s_cmp_ge_u32 s54,256
s_cbranch_scc1 Lg3_fin0_%=
s_add_u32 m0,s59,4096
s_nop 0
buffer_load_dword v8,s[36:39],s58 offen offset:0 sc1 lds
buffer_store_dword v4,v3,s[36:39],s71 offen offset:0 sc0 sc1
s_add_u32 m0,s59,5120
s_nop 0
buffer_load_dword v8,s[36:39],s58 offen offset:0 sc1 lds
buffer_store_dword v4,v3,s[36:39],s71 offen offset:0 sc0 sc1
s_mov_b32 s83,0
L 0
v_fma_f32 v18,-v74,v82,v18
v_fma_f32 v19,-v74,v83,v19
v_fma_f32 v20,-v74,v84,v20
v_fma_f32 v21,-v74,v85,v21
v_mul_f32 v94,v18,v18
v_mul_f32 v95,v19,v19
v_mul_f32 v96,v20,v20
v_mul_f32 v97,v21,v21
v_fma_f32 v94,v76,v94,v75
v_fma_f32 v95,v76,v95,v75
v_fma_f32 v96,v76,v96,v75
v_fma_f32 v97,v76,v97,v75
v_mul_f32 v94,v18,v94
v_mul_f32 v95,v19,v95
v_mul_f32 v96,v20,v96
v_mul_f32 v97,v21,v97
v_exp_f32 v94,v94
v_exp_f32 v95,v95
v_exp_f32 v96,v96
v_exp_f32 v97,v97
v_fma_f32 v94,v94,v77,v77
v_fma_f32 v95,v95,v77,v77
v_fma_f32 v96,v96,v77,v77
v_fma_f32 v97,v97,v77,v77
v_rcp_f32 v94,v94
v_rcp_f32 v95,v95
v_rcp_f32 v96,v96
v_rcp_f32 v97,v97
v_mul_f32 v18,v18,v94
v_mul_f32 v19,v19,v95
v_mul_f32 v20,v20,v96
v_mul_f32 v21,v21,v97
v_cvt_pk_bf16_f32 v106,v18,v19
v_cvt_pk_bf16_f32 v107,v20,v21
v_fma_f32 v22,-v74,v86,v22
v_fma_f32 v23,-v74,v87,v23
v_fma_f32 v24,-v74,v88,v24
v_fma_f32 v25,-v74,v89,v25
v_mul_f32 v98,v22,v22
v_mul_f32 v99,v23,v23
v_mul_f32 v100,v24,v24
v_mul_f32 v101,v25,v25
v_fma_f32 v98,v76,v98,v75
v_fma_f32 v99,v76,v99,v75
v_fma_f32 v100,v76,v100,v75
v_fma_f32 v101,v76,v101,v75
v_mul_f32 v98,v22,v98
v_mul_f32 v99,v23,v99
v_mul_f32 v100,v24,v100
v_mul_f32 v101,v25,v101
v_exp_f32 v98,v98
v_exp_f32 v99,v99
v_exp_f32 v100,v100
v_exp_f32 v101,v101
v_fma_f32 v98,v98,v77,v77
v_fma_f32 v99,v99,v77,v77
v_fma_f32 v100,v100,v77,v77
v_fma_f32 v101,v101,v77,v77
v_rcp_f32 v98,v98
v_rcp_f32 v99,v99
v_rcp_f32 v100,v100
v_rcp_f32 v101,v101
v_mul_f32 v22,v22,v98
v_mul_f32 v23,v23,v99
v_mul_f32 v24,v24,v100
v_mul_f32 v25,v25,v101
v_cvt_pk_bf16_f32 v108,v22,v23
v_cvt_pk_bf16_f32 v109,v24,v25
buffer_store_dwordx4 v[106:109],v0,s[36:39],s66 offen offset:0 sc0 sc1
v_fma_f32 v26,-v74,v90,v26
v_fma_f32 v27,-v74,v91,v27
v_fma_f32 v28,-v74,v92,v28
v_fma_f32 v29,-v74,v93,v29
v_mul_f32 v102,v26,v26
v_mul_f32 v103,v27,v27
v_mul_f32 v104,v28,v28
v_mul_f32 v105,v29,v29
v_fma_f32 v102,v76,v102,v75
v_fma_f32 v103,v76,v103,v75
v_fma_f32 v104,v76,v104,v75
v_fma_f32 v105,v76,v105,v75
v_mul_f32 v102,v26,v102
v_mul_f32 v103,v27,v103
v_mul_f32 v104,v28,v104
v_mul_f32 v105,v29,v105
v_exp_f32 v102,v102
v_exp_f32 v103,v103
v_exp_f32 v104,v104
v_exp_f32 v105,v105
v_fma_f32 v102,v102,v77,v77
v_fma_f32 v103,v103,v77,v77
v_fma_f32 v104,v104,v77,v77
v_fma_f32 v105,v105,v77,v77
v_rcp_f32 v102,v102
v_rcp_f32 v103,v103
v_rcp_f32 v104,v104
v_rcp_f32 v105,v105
v_mul_f32 v26,v26,v102
v_mul_f32 v27,v27,v103
v_mul_f32 v28,v28,v104
v_mul_f32 v29,v29,v105
v_cvt_pk_bf16_f32 v110,v26,v27
v_cvt_pk_bf16_f32 v111,v28,v29
buffer_store_dwordx2 v[110:111],v10,s[36:39],s66 offen offset:0 sc0 sc1
s_mov_b32 s72,s69
s_mov_b32 s69,0x7fff0000
s_mov_b32 s66,0x7fff0000
s_mov_b32 s76,0x7fff0000
s_branch Lg3_poll_%=
Lg3_fin0_%=:
L 0
v_fma_f32 v18,-v74,v82,v18
v_fma_f32 v19,-v74,v83,v19
v_fma_f32 v20,-v74,v84,v20
v_fma_f32 v21,-v74,v85,v21
v_mul_f32 v94,v18,v18
v_mul_f32 v95,v19,v19
v_mul_f32 v96,v20,v20
v_mul_f32 v97,v21,v21
v_fma_f32 v94,v76,v94,v75
v_fma_f32 v95,v76,v95,v75
v_fma_f32 v96,v76,v96,v75
v_fma_f32 v97,v76,v97,v75
v_mul_f32 v94,v18,v94
v_mul_f32 v95,v19,v95
v_mul_f32 v96,v20,v96
v_mul_f32 v97,v21,v97
v_exp_f32 v94,v94
v_exp_f32 v95,v95
v_exp_f32 v96,v96
v_exp_f32 v97,v97
v_fma_f32 v94,v94,v77,v77
v_fma_f32 v95,v95,v77,v77
v_fma_f32 v96,v96,v77,v77
v_fma_f32 v97,v97,v77,v77
v_rcp_f32 v94,v94
v_rcp_f32 v95,v95
v_rcp_f32 v96,v96
v_rcp_f32 v97,v97
v_mul_f32 v18,v18,v94
v_mul_f32 v19,v19,v95
v_mul_f32 v20,v20,v96
v_mul_f32 v21,v21,v97
v_cvt_pk_bf16_f32 v106,v18,v19
v_cvt_pk_bf16_f32 v107,v20,v21
v_fma_f32 v22,-v74,v86,v22
v_fma_f32 v23,-v74,v87,v23
v_fma_f32 v24,-v74,v88,v24
v_fma_f32 v25,-v74,v89,v25
v_mul_f32 v98,v22,v22
v_mul_f32 v99,v23,v23
v_mul_f32 v100,v24,v24
v_mul_f32 v101,v25,v25
v_fma_f32 v98,v76,v98,v75
v_fma_f32 v99,v76,v99,v75
v_fma_f32 v100,v76,v100,v75
v_fma_f32 v101,v76,v101,v75
v_mul_f32 v98,v22,v98
v_mul_f32 v99,v23,v99
v_mul_f32 v100,v24,v100
v_mul_f32 v101,v25,v101
v_exp_f32 v98,v98
v_exp_f32 v99,v99
v_exp_f32 v100,v100
v_exp_f32 v101,v101
v_fma_f32 v98,v98,v77,v77
v_fma_f32 v99,v99,v77,v77
v_fma_f32 v100,v100,v77,v77
v_fma_f32 v101,v101,v77,v77
v_rcp_f32 v98,v98
v_rcp_f32 v99,v99
v_rcp_f32 v100,v100
v_rcp_f32 v101,v101
v_mul_f32 v22,v22,v98
v_mul_f32 v23,v23,v99
v_mul_f32 v24,v24,v100
v_mul_f32 v25,v25,v101
v_cvt_pk_bf16_f32 v108,v22,v23
v_cvt_pk_bf16_f32 v109,v24,v25
buffer_store_dwordx4 v[106:109],v0,s[36:39],s66 offen offset:0 sc0 sc1
v_fma_f32 v26,-v74,v90,v26
v_fma_f32 v27,-v74,v91,v27
v_fma_f32 v28,-v74,v92,v28
v_fma_f32 v29,-v74,v93,v29
v_mul_f32 v102,v26,v26
v_mul_f32 v103,v27,v27
v_mul_f32 v104,v28,v28
v_mul_f32 v105,v29,v29
v_fma_f32 v102,v76,v102,v75
v_fma_f32 v103,v76,v103,v75
v_fma_f32 v104,v76,v104,v75
v_fma_f32 v105,v76,v105,v75
v_mul_f32 v102,v26,v102
v_mul_f32 v103,v27,v103
v_mul_f32 v104,v28,v104
v_mul_f32 v105,v29,v105
v_exp_f32 v102,v102
v_exp_f32 v103,v103
v_exp_f32 v104,v104
v_exp_f32 v105,v105
v_fma_f32 v102,v102,v77,v77
v_fma_f32 v103,v103,v77,v77
v_fma_f32 v104,v104,v77,v77
v_fma_f32 v105,v105,v77,v77
v_rcp_f32 v102,v102
v_rcp_f32 v103,v103
v_rcp_f32 v104,v104
v_rcp_f32 v105,v105
v_mul_f32 v26,v26,v102
v_mul_f32 v27,v27,v103
v_mul_f32 v28,v28,v104
v_mul_f32 v29,v29,v105
v_cvt_pk_bf16_f32 v110,v26,v27
v_cvt_pk_bf16_f32 v111,v28,v29
buffer_store_dwordx2 v[110:111],v10,s[36:39],s66 offen offset:0 sc0 sc1
s_mov_b32 s72,s69
s_mov_b32 s69,0x7fff0000
s_mov_b32 s66,0x7fff0000
s_mov_b32 s76,0x7fff0000
s_branch Lg3_done_%=
Lg3_L1_%=:
v_mfma_f32_16x16x32_bf16 v[38:41],a[8:11],v[42:45],0
s_add_u32 m0,s59,3072
R 62,2,5120
v_add_f32_dpp v115,v114,v114 quad_perm:[1,0,3,2] row_mask:0xf bank_mask:0xf
M 38,a[20:23],46
buffer_load_dword v8,s[36:39],s58 offen offset:0 sc1 lds
v_mfma_f32_16x16x32_bf16 v[34:37],a[4:7],v[42:45],0
s_mov_b32 s67,s65
v_add_f32_dpp v115,v115,v115 quad_perm:[2,3,0,1] row_mask:0xf bank_mask:0xf
v_fma_f32 v18,-v74,v82,v18
M 34,a[16:19],46
s_add_u32 s65,s65,s62
R 66,2,6144
v_fma_f32 v19,-v74,v83,v19
v_mfma_f32_16x16x32_bf16 v[30:33],a[0:3],v[42:45],0
v_add_f32_dpp v118,v115,v115 row_half_mirror row_mask:0xf bank_mask:0xf
v_fma_f32 v20,-v74,v84,v20
v_fma_f32 v21,-v74,v85,v21
M 30,a[12:15],46
L 3
v_mul_f32 v94,v18,v18
v_mul_f32 v95,v19,v19
M 30,a[24:27],50
R 70,2,7168
v_mov_b32_dpp v115,v118 row_ror:8 row_mask:0xf bank_mask:0xf
M 30,a[36:39],54
v_fma_f32 v115,-v118,v118,v115
v_max_f32 v115,s87,v115
M 34,a[28:31],50
v_rsq_f32 v116,v115
M 34,a[40:43],54
R 42,2,8192
v_fma_f32 v115,v15,v115,v16
v_mul_f32 v118,v17,v118
M 38,a[32:35],50
v_mul_f32 v119,v116,v116
v_fma_f32 v115,v14,v119,v115
M 38,a[44:47],54
L 3
v_fma_f32 v118,v116,v115,v118
v_mul_f32 v96,v20,v20
M 38,a[56:59],58
R 46,2,9216
v_mul_f32 v97,v21,v21
M 38,a[68:71],62
ds_write_b32 v12,v118 offset:2048
M 34,a[52:55],58
v_fma_f32 v94,v76,v94,v75
v_fma_f32 v95,v76,v95,v75
v_fma_f32 v96,v76,v96,v75
M 34,a[64:67],62
R 50,2,10240
v_fma_f32 v97,v76,v97,v75
M 30,a[48:51],58
v_mul_f32 v94,v18,v94
v_mul_f32 v95,v19,v95
M 30,a[60:63],62
L 4
v_exp_f32 v94,v94
M 30,a[72:75],66
R 54,2,11264
v_mul_f32 v96,v20,v96
M 30,a[84:87],70
v_exp_f32 v95,v95
v_mul_f32 v97,v21,v97
M 34,a[76:79],66
v_exp_f32 v96,v96
M 34,a[88:91],70
R 58,2,12288
v_fma_f32 v94,v94,v77,v77
M 38,a[80:83],66
v_exp_f32 v97,v97
M 38,a[92:95],70
L 4
v_rcp_f32 v94,v94
M 38,a[104:107],42
R 62,2,13312
v_fma_f32 v95,v95,v77,v77
v_fma_f32 v96,v96,v77,v77
M 38,a[116:119],46
v_rcp_f32 v95,v95
M 34,a[100:103],42
v_rcp_f32 v96,v96
M 34,a[112:115],46
R 66,2,14336
v_fma_f32 v97,v97,v77,v77
M 30,a[96:99],42
v_rcp_f32 v97,v97
M 30,a[108:111],46
L 3
v_mul_f32 v18,v18,v94
v_mul_f32 v19,v19,v95
v_mul_f32 v20,v20,v96
M 30,a[120:123],50
R 70,2,15360
v_mul_f32 v21,v21,v97
M 30,a[132:135],54
s_waitcnt vmcnt(0)
v_cvt_pk_bf16_f32 v106,v18,v19
v_cvt_pk_bf16_f32 v107,v20,v21
M 34,a[124:127],50
s_barrier
ds_read_b32 v7,v6 offset:3072
v_fma_f32 v22,-v74,v86,v22
M 34,a[136:139],54
R 42,2,16384
v_fma_f32 v23,-v74,v87,v23
M 38,a[128:131],50
buffer_store_dword v4,v3,s[36:39],s70 offen offset:0 sc0 sc1
v_fma_f32 v24,-v74,v88,v24
M 38,a[140:143],54
L 4
v_fma_f32 v25,-v74,v89,v25
v_mul_f32 v98,v22,v22
M 38,a[152:155],58
s_mov_b32 s70,0x7fff0000
R 46,2,17408
v_mul_f32 v99,v23,v23
M 38,a[164:167],62
v_mul_f32 v100,v24,v24
v_mul_f32 v101,v25,v25
M 34,a[148:151],58
L 2
v_cmp_eq_u32 vcc,s53,v7
v_fma_f32 v98,v76,v98,v75
M 34,a[160:163],62
R 50,2,18432
v_fma_f32 v99,v76,v99,v75
v_fma_f32 v100,v76,v100,v75
M 30,a[144:147],58
v_fma_f32 v101,v76,v101,v75
v_mul_f32 v98,v22,v98
M 30,a[156:159],62
s_cmp_eq_u32 vcc_lo,-1
s_nop 0
v_mul_f32 v99,v23,v99
s_cbranch_scc0 Lg3_X1_%=
M 30,a[168:171],66
s_add_u32 m0,s57,0
s_add_u32 s56,s55,0
s_add_u32 s58,s58,s61
R 54,2,19456
M 30,a[180:183],70
buffer_load_dword v114,v11,s[36:39],s73 offen offset:0 sc1
M 34,a[172:175],66
D 0,56,0
v_mul_f32 v100,v24,v100
M 34,a[184:187],70
s_add_u32 s54,s54,s46
R 58,2,20480
v_mul_f32 v101,v25,v101
M 38,a[176:179],66
s_add_u32 s73,s73,s64
v_exp_f32 v98,v98
M 38,a[188:191],70
L 3
v_exp_f32 v99,v99
M 38,a[200:203],42
R 62,2,21504
v_fma_f32 v98,v98,v77,v77
M 38,a[212:215],46
D 0,56,1024
v_fma_f32 v99,v99,v77,v77
M 34,a[196:199],42
v_exp_f32 v100,v100
M 34,a[208:211],46
R 66,2,22528
v_fma_f32 v100,v100,v77,v77
M 30,a[192:195],42
D 0,56,2048
M 30,a[204:207],46
L 3
v_exp_f32 v101,v101
M 30,a[216:219],50
R 70,2,23552
)"
R"(v_rcp_f32 v98,v98
M 30,a[228:231],54
v_rcp_f32 v99,v99
M 34,a[220:223],50
D 0,56,3072
M 34,a[232:235],54
s_add_u32 m0,s59,1024
R 42,2,24576
v_fma_f32 v101,v101,v77,v77
M 38,a[224:227],50
buffer_load_dword v8,s[36:39],s58 offen offset:0 sc1 lds
M 38,a[236:239],54
L 3
v_rcp_f32 v100,v100
v_mul_f32 v22,v22,v98
M 38,a[248:251],58
s_add_u32 m0,s57,16384
R 46,2,25600
v_mul_f32 v23,v23,v99
M 38,v[132:135],62
s_add_u32 s56,s55,16384
v_rcp_f32 v101,v101
M 34,a[244:247],58
D 0,56,0
M 34,v[128:131],62
s_add_u32 s55,s55,s60
R 50,2,26624
v_mul_f32 v24,v24,v100
M 30,a[240:243],58
s_sub_u32 s57,s84,s57
v_mul_f32 v25,v25,v101
v_cvt_pk_bf16_f32 v108,v22,v23
v_cvt_pk_bf16_f32 v109,v24,v25
M 30,a[252:255],62
L 3
buffer_store_dwordx4 v[106:109],v0,s[36:39],s66 offen offset:0 sc0 sc1
v_fma_f32 v26,-v74,v90,v26
M 30,v[136:139],66
R 54,2,27648
v_fma_f32 v27,-v74,v91,v27
M 30,v[148:151],70
v_fma_f32 v28,-v74,v92,v28
v_fma_f32 v29,-v74,v93,v29
M 34,v[140:143],66
D 0,56,1024
M 34,v[152:155],70
R 58,2,28672
v_mul_f32 v102,v26,v26
v_mul_f32 v103,v27,v27
M 38,v[144:147],66
v_mul_f32 v104,v28,v28
v_mul_f32 v105,v29,v29
M 38,v[156:159],70
L 3
v_fma_f32 v102,v76,v102,v75
v_fma_f32 v103,v76,v103,v75
M 38,v[168:171],42
R 62,2,29696
v_fma_f32 v104,v76,v104,v75
M 38,v[180:183],46
D 0,56,2048
M 34,v[164:167],42
v_fma_f32 v105,v76,v105,v75
v_mul_f32 v102,v26,v102
v_mul_f32 v103,v27,v103
M 34,v[176:179],46
R 66,2,30720
v_mul_f32 v104,v28,v104
M 30,v[160:163],42
v_exp_f32 v102,v102
M 30,v[172:175],46
L 3
D 0,56,3072
M 30,v[184:187],50
s_nop 0
R 70,2,31744
v_mul_f32 v105,v29,v105
M 30,v[196:199],54
s_waitcnt vmcnt(5)
v_exp_f32 v103,v103
v_fma_f32 v102,v102,v77,v77
M 34,v[188:191],50
s_barrier
buffer_store_dword v4,v3,s[36:39],s70 offen offset:0 sc0 sc1
M 34,v[200:203],54
s_mov_b32 s70,s69
R 42,1,0
ds_read_b32 v7,v6 offset:1024
M 38,v[192:195],50
s_mov_b32 s69,s68
R 78,13,2048
v_fma_f32 v103,v103,v77,v77
M 38,v[204:207],54
L 5
v_exp_f32 v104,v104
M 38,v[216:219],58
s_add_u32 s68,s68,s63
R 46,1,1024
v_exp_f32 v105,v105
M 38,v[228:231],62
v_rcp_f32 v102,v102
M 34,v[212:215],58
v_rcp_f32 v103,v103
M 34,v[224:227],62
L 2
R 50,1,2048
v_fma_f32 v104,v104,v77,v77
M 30,v[208:211],58
v_rcp_f32 v104,v104
M 30,v[220:223],62
s_nop 0
v_fma_f32 v105,v105,v77,v77
v_rcp_f32 v105,v105
M 30,v[232:235],66
R 54,1,3072
v_mul_f32 v26,v26,v102
M 30,v[244:247],70
v_mul_f32 v27,v27,v103
v_mul_f32 v28,v28,v104
M 34,v[236:239],66
v_cmp_eq_u32 vcc,s53,v7
v_mul_f32 v29,v29,v105
M 34,v[248:251],70
R 58,1,4096
v_cvt_pk_bf16_f32 v110,v26,v27
v_cvt_pk_bf16_f32 v111,v28,v29
M 38,v[240:243],66
L 3
s_cmp_eq_u32 vcc_lo,-1
buffer_store_dwordx2 v[110:111],v10,s[36:39],s66 offen offset:0 sc0 sc1
M 38,v[252:255],70
buffer_store_dword v4,v3,s[36:39],s71 offen offset:0 sc0 sc1
s_cbranch_scc1 Lg3_F0_%=
s_branch Lg3_L0_%=
Lg3_X1_%=:
M 30,a[168:171],66
s_add_u32 m0,s59,1024
R 54,2,19456
v_mul_f32 v100,v24,v100
M 30,a[180:183],70
buffer_load_dword v8,s[36:39],s58 offen offset:0 sc1 lds
M 34,a[172:175],66
v_exp_f32 v98,v98
M 34,a[184:187],70
R 58,2,20480
v_mul_f32 v101,v25,v101
M 38,a[176:179],66
v_exp_f32 v99,v99
M 38,a[188:191],70
L 3
v_exp_f32 v100,v100
M 38,a[200:203],42
R 62,2,21504
v_fma_f32 v98,v98,v77,v77
M 38,a[212:215],46
v_exp_f32 v101,v101
M 34,a[196:199],42
v_rcp_f32 v98,v98
M 34,a[208:211],46
R 66,2,22528
v_fma_f32 v99,v99,v77,v77
M 30,a[192:195],42
v_rcp_f32 v99,v99
M 30,a[204:207],46
L 3
v_fma_f32 v100,v100,v77,v77
v_fma_f32 v101,v101,v77,v77
M 30,a[216:219],50
R 70,2,23552
v_mul_f32 v22,v22,v98
M 30,a[228:231],54
v_rcp_f32 v100,v100
M 34,a[220:223],50
v_rcp_f32 v101,v101
M 34,a[232:235],54
R 42,2,24576
v_mul_f32 v23,v23,v99
M 38,a[224:227],50
v_mul_f32 v24,v24,v100
v_mul_f32 v25,v25,v101
M 38,a[236:239],54
L 3
v_cvt_pk_bf16_f32 v108,v22,v23
v_cvt_pk_bf16_f32 v109,v24,v25
M 38,a[248:251],58
R 46,2,25600
v_fma_f32 v26,-v74,v90,v26
M 38,v[132:135],62
buffer_store_dwordx4 v[106:109],v0,s[36:39],s66 offen offset:0 sc0 sc1
v_fma_f32 v27,-v74,v91,v27
M 34,a[244:247],58
v_fma_f32 v28,-v74,v92,v28
v_fma_f32 v29,-v74,v93,v29
M 34,v[128:131],62
R 50,2,26624
v_mul_f32 v102,v26,v26
M 30,a[240:243],58
v_mul_f32 v103,v27,v27
v_mul_f32 v104,v28,v28
M 30,a[252:255],62
L 3
v_mul_f32 v105,v29,v29
v_fma_f32 v102,v76,v102,v75
M 30,v[136:139],66
R 54,2,27648
v_fma_f32 v103,v76,v103,v75
M 30,v[148:151],70
v_fma_f32 v104,v76,v104,v75
v_fma_f32 v105,v76,v105,v75
M 34,v[140:143],66
v_mul_f32 v102,v26,v102
v_mul_f32 v103,v27,v103
M 34,v[152:155],70
R 58,2,28672
v_mul_f32 v104,v28,v104
M 38,v[144:147],66
v_exp_f32 v102,v102
M 38,v[156:159],70
L 3
v_exp_f32 v103,v103
M 38,v[168:171],42
R 62,2,29696
v_mul_f32 v105,v29,v105
M 38,v[180:183],46
v_exp_f32 v104,v104
M 34,v[164:167],42
v_exp_f32 v105,v105
M 34,v[176:179],46
R 66,2,30720
v_fma_f32 v102,v102,v77,v77
M 30,v[160:163],42
v_rcp_f32 v102,v102
M 30,v[172:175],46
L 3
v_fma_f32 v103,v103,v77,v77
v_fma_f32 v104,v104,v77,v77
M 30,v[184:187],50
s_nop 0
R 70,2,31744
v_fma_f32 v105,v105,v77,v77
M 30,v[196:199],54
s_waitcnt vmcnt(1)
v_rcp_f32 v103,v103
M 34,v[188:191],50
s_barrier
buffer_store_dword v4,v3,s[36:39],s70 offen offset:0 sc0 sc1
M 34,v[200:203],54
s_mov_b32 s70,s69
ds_read_b32 v7,v6 offset:1024
R 78,13,2048
M 38,v[192:195],50
s_mov_b32 s69,s68
v_rcp_f32 v104,v104
M 38,v[204:207],54
L 4
v_rcp_f32 v105,v105
M 38,v[216:219],58
s_add_u32 s68,s68,s63
v_mul_f32 v26,v26,v102
v_mul_f32 v27,v27,v103
M 38,v[228:231],62
v_mul_f32 v28,v28,v104
v_mul_f32 v29,v29,v105
M 34,v[212:215],58
v_cvt_pk_bf16_f32 v110,v26,v27
v_cvt_pk_bf16_f32 v111,v28,v29
M 34,v[224:227],62
L 1
buffer_store_dwordx2 v[110:111],v10,s[36:39],s66 offen offset:0 sc0 sc1
M 30,v[208:211],58
v_cmp_eq_u32 vcc,s53,v7
M 30,v[220:223],62
s_nop 0
M 30,v[232:235],66
M 30,v[244:247],70
M 34,v[236:239],66
M 34,v[248:251],70
M 38,v[240:243],66
M 38,v[252:255],70
s_mov_b32 s80,0
s_cmp_eq_u32 vcc_lo,-1
s_cbranch_scc0 Lg3_xn1_%=
buffer_load_dword v114,v11,s[36:39],s73 offen offset:0 sc1
L 0
v_fma_f32 v30,-v78,v82,v30
v_fma_f32 v31,-v78,v83,v31
v_fma_f32 v32,-v78,v84,v32
v_fma_f32 v33,-v78,v85,v33
v_mul_f32 v94,v30,v30
v_mul_f32 v95,v31,v31
v_mul_f32 v96,v32,v32
v_mul_f32 v97,v33,v33
s_add_u32 s73,s73,s64
s_add_u32 m0,s57,0
s_add_u32 s56,s55,0
D 0,56,0
v_fma_f32 v94,v80,v94,v79
v_fma_f32 v95,v80,v95,v79
v_fma_f32 v96,v80,v96,v79
v_fma_f32 v97,v80,v97,v79
v_mul_f32 v94,v30,v94
v_mul_f32 v95,v31,v95
v_mul_f32 v96,v32,v96
v_mul_f32 v97,v33,v97
D 0,56,1024
v_exp_f32 v94,v94
v_exp_f32 v95,v95
v_exp_f32 v96,v96
v_exp_f32 v97,v97
v_fma_f32 v94,v94,v81,v81
v_fma_f32 v95,v95,v81,v81
v_fma_f32 v96,v96,v81,v81
v_fma_f32 v97,v97,v81,v81
D 0,56,2048
v_rcp_f32 v94,v94
v_rcp_f32 v95,v95
v_rcp_f32 v96,v96
v_rcp_f32 v97,v97
v_mul_f32 v30,v30,v94
v_mul_f32 v31,v31,v95
v_mul_f32 v32,v32,v96
v_mul_f32 v33,v33,v97
D 0,56,3072
v_cvt_pk_bf16_f32 v106,v30,v31
v_cvt_pk_bf16_f32 v107,v32,v33
v_fma_f32 v34,-v78,v86,v34
v_fma_f32 v35,-v78,v87,v35
v_fma_f32 v36,-v78,v88,v36
v_fma_f32 v37,-v78,v89,v37
v_mul_f32 v98,v34,v34
v_mul_f32 v99,v35,v35
s_add_u32 s58,s58,s61
s_add_u32 m0,s57,16384
s_add_u32 s56,s55,16384
D 0,56,0
v_mul_f32 v100,v36,v36
v_mul_f32 v101,v37,v37
v_fma_f32 v98,v80,v98,v79
v_fma_f32 v99,v80,v99,v79
v_fma_f32 v100,v80,v100,v79
v_fma_f32 v101,v80,v101,v79
v_mul_f32 v98,v34,v98
v_mul_f32 v99,v35,v99
D 0,56,1024
v_mul_f32 v100,v36,v100
v_mul_f32 v101,v37,v101
v_exp_f32 v98,v98
v_exp_f32 v99,v99
v_exp_f32 v100,v100
v_exp_f32 v101,v101
v_fma_f32 v98,v98,v81,v81
v_fma_f32 v99,v99,v81,v81
D 0,56,2048
v_fma_f32 v100,v100,v81,v81
v_fma_f32 v101,v101,v81,v81
v_rcp_f32 v98,v98
v_rcp_f32 v99,v99
v_rcp_f32 v100,v100
v_rcp_f32 v101,v101
v_mul_f32 v34,v34,v98
v_mul_f32 v35,v35,v99
D 0,56,3072
v_mul_f32 v36,v36,v100
v_mul_f32 v37,v37,v101
v_cvt_pk_bf16_f32 v108,v34,v35
v_cvt_pk_bf16_f32 v109,v36,v37
buffer_store_dwordx4 v[106:109],v0,s[36:39],s67 offen offset:0 sc0 sc1
v_fma_f32 v38,-v78,v90,v38
v_fma_f32 v39,-v78,v91,v39
v_fma_f32 v40,-v78,v92,v40
v_fma_f32 v41,-v78,v93,v41
s_add_u32 s55,s55,s60
s_sub_u32 s57,s84,s57
s_add_u32 s54,s54,s46
v_mul_f32 v102,v38,v38
v_mul_f32 v103,v39,v39
v_mul_f32 v104,v40,v40
v_mul_f32 v105,v41,v41
v_fma_f32 v102,v80,v102,v79
v_fma_f32 v103,v80,v103,v79
v_fma_f32 v104,v80,v104,v79
v_fma_f32 v105,v80,v105,v79
v_mul_f32 v102,v38,v102
v_mul_f32 v103,v39,v103
v_mul_f32 v104,v40,v104
v_mul_f32 v105,v41,v105
v_exp_f32 v102,v102
v_exp_f32 v103,v103
v_exp_f32 v104,v104
v_exp_f32 v105,v105
v_fma_f32 v102,v102,v81,v81
v_fma_f32 v103,v103,v81,v81
v_fma_f32 v104,v104,v81,v81
v_fma_f32 v105,v105,v81,v81
v_rcp_f32 v102,v102
v_rcp_f32 v103,v103
v_rcp_f32 v104,v104
v_rcp_f32 v105,v105
v_mul_f32 v38,v38,v102
v_mul_f32 v39,v39,v103
v_mul_f32 v40,v40,v104
v_mul_f32 v41,v41,v105
v_cvt_pk_bf16_f32 v110,v38,v39
v_cvt_pk_bf16_f32 v111,v40,v41
buffer_store_dwordx2 v[110:111],v10,s[36:39],s67 offen offset:0 sc0 sc1
s_mov_b32 s72,s69
s_mov_b32 s69,0x7fff0000
s_mov_b32 s67,0x7fff0000
s_mov_b32 s77,0x7fff0000
s_branch Lg3_gox_%=
Lg3_xn1_%=:
s_cmp_ge_u32 s54,256
s_cbranch_scc1 Lg3_fin1_%=
s_add_u32 m0,s59,4096
s_nop 0
buffer_load_dword v8,s[36:39],s58 offen offset:0 sc1 lds
buffer_store_dword v4,v3,s[36:39],s71 offen offset:0 sc0 sc1
s_add_u32 m0,s59,5120
s_nop 0
buffer_load_dword v8,s[36:39],s58 offen offset:0 sc1 lds
buffer_store_dword v4,v3,s[36:39],s71 offen offset:0 sc0 sc1
s_mov_b32 s83,0
L 0
v_fma_f32 v30,-v78,v82,v30
v_fma_f32 v31,-v78,v83,v31
v_fma_f32 v32,-v78,v84,v32
v_fma_f32 v33,-v78,v85,v33
v_mul_f32 v94,v30,v30
v_mul_f32 v95,v31,v31
v_mul_f32 v96,v32,v32
v_mul_f32 v97,v33,v33
v_fma_f32 v94,v80,v94,v79
v_fma_f32 v95,v80,v95,v79
v_fma_f32 v96,v80,v96,v79
v_fma_f32 v97,v80,v97,v79
v_mul_f32 v94,v30,v94
v_mul_f32 v95,v31,v95
v_mul_f32 v96,v32,v96
v_mul_f32 v97,v33,v97
v_exp_f32 v94,v94
v_exp_f32 v95,v95
v_exp_f32 v96,v96
v_exp_f32 v97,v97
v_fma_f32 v94,v94,v81,v81
v_fma_f32 v95,v95,v81,v81
v_fma_f32 v96,v96,v81,v81
v_fma_f32 v97,v97,v81,v81
v_rcp_f32 v94,v94
v_rcp_f32 v95,v95
v_rcp_f32 v96,v96
v_rcp_f32 v97,v97
v_mul_f32 v30,v30,v94
v_mul_f32 v31,v31,v95
v_mul_f32 v32,v32,v96
v_mul_f32 v33,v33,v97
v_cvt_pk_bf16_f32 v106,v30,v31
v_cvt_pk_bf16_f32 v107,v32,v33
v_fma_f32 v34,-v78,v86,v34
v_fma_f32 v35,-v78,v87,v35
v_fma_f32 v36,-v78,v88,v36
v_fma_f32 v37,-v78,v89,v37
v_mul_f32 v98,v34,v34
v_mul_f32 v99,v35,v35
v_mul_f32 v100,v36,v36
v_mul_f32 v101,v37,v37
v_fma_f32 v98,v80,v98,v79
v_fma_f32 v99,v80,v99,v79
v_fma_f32 v100,v80,v100,v79
v_fma_f32 v101,v80,v101,v79
v_mul_f32 v98,v34,v98
v_mul_f32 v99,v35,v99
v_mul_f32 v100,v36,v100
v_mul_f32 v101,v37,v101
v_exp_f32 v98,v98
v_exp_f32 v99,v99
v_exp_f32 v100,v100
v_exp_f32 v101,v101
v_fma_f32 v98,v98,v81,v81
v_fma_f32 v99,v99,v81,v81
v_fma_f32 v100,v100,v81,v81
v_fma_f32 v101,v101,v81,v81
v_rcp_f32 v98,v98
v_rcp_f32 v99,v99
v_rcp_f32 v100,v100
v_rcp_f32 v101,v101
v_mul_f32 v34,v34,v98
v_mul_f32 v35,v35,v99
v_mul_f32 v36,v36,v100
v_mul_f32 v37,v37,v101
v_cvt_pk_bf16_f32 v108,v34,v35
v_cvt_pk_bf16_f32 v109,v36,v37
buffer_store_dwordx4 v[106:109],v0,s[36:39],s67 offen offset:0 sc0 sc1
v_fma_f32 v38,-v78,v90,v38
v_fma_f32 v39,-v78,v91,v39
v_fma_f32 v40,-v78,v92,v40
v_fma_f32 v41,-v78,v93,v41
v_mul_f32 v102,v38,v38
v_mul_f32 v103,v39,v39
v_mul_f32 v104,v40,v40
v_mul_f32 v105,v41,v41
)"
R"(v_fma_f32 v102,v80,v102,v79
v_fma_f32 v103,v80,v103,v79
v_fma_f32 v104,v80,v104,v79
v_fma_f32 v105,v80,v105,v79
v_mul_f32 v102,v38,v102
v_mul_f32 v103,v39,v103
v_mul_f32 v104,v40,v104
v_mul_f32 v105,v41,v105
v_exp_f32 v102,v102
v_exp_f32 v103,v103
v_exp_f32 v104,v104
v_exp_f32 v105,v105
v_fma_f32 v102,v102,v81,v81
v_fma_f32 v103,v103,v81,v81
v_fma_f32 v104,v104,v81,v81
v_fma_f32 v105,v105,v81,v81
v_rcp_f32 v102,v102
v_rcp_f32 v103,v103
v_rcp_f32 v104,v104
v_rcp_f32 v105,v105
v_mul_f32 v38,v38,v102
v_mul_f32 v39,v39,v103
v_mul_f32 v40,v40,v104
v_mul_f32 v41,v41,v105
v_cvt_pk_bf16_f32 v110,v38,v39
v_cvt_pk_bf16_f32 v111,v40,v41
buffer_store_dwordx2 v[110:111],v10,s[36:39],s67 offen offset:0 sc0 sc1
s_mov_b32 s72,s69
s_mov_b32 s69,0x7fff0000
s_mov_b32 s67,0x7fff0000
s_mov_b32 s77,0x7fff0000
s_branch Lg3_poll_%=
Lg3_fin1_%=:
L 0
v_fma_f32 v30,-v78,v82,v30
v_fma_f32 v31,-v78,v83,v31
v_fma_f32 v32,-v78,v84,v32
v_fma_f32 v33,-v78,v85,v33
v_mul_f32 v94,v30,v30
v_mul_f32 v95,v31,v31
v_mul_f32 v96,v32,v32
v_mul_f32 v97,v33,v33
v_fma_f32 v94,v80,v94,v79
v_fma_f32 v95,v80,v95,v79
v_fma_f32 v96,v80,v96,v79
v_fma_f32 v97,v80,v97,v79
v_mul_f32 v94,v30,v94
v_mul_f32 v95,v31,v95
v_mul_f32 v96,v32,v96
v_mul_f32 v97,v33,v97
v_exp_f32 v94,v94
v_exp_f32 v95,v95
v_exp_f32 v96,v96
v_exp_f32 v97,v97
v_fma_f32 v94,v94,v81,v81
v_fma_f32 v95,v95,v81,v81
v_fma_f32 v96,v96,v81,v81
v_fma_f32 v97,v97,v81,v81
v_rcp_f32 v94,v94
v_rcp_f32 v95,v95
v_rcp_f32 v96,v96
v_rcp_f32 v97,v97
v_mul_f32 v30,v30,v94
v_mul_f32 v31,v31,v95
v_mul_f32 v32,v32,v96
v_mul_f32 v33,v33,v97
v_cvt_pk_bf16_f32 v106,v30,v31
v_cvt_pk_bf16_f32 v107,v32,v33
v_fma_f32 v34,-v78,v86,v34
v_fma_f32 v35,-v78,v87,v35
v_fma_f32 v36,-v78,v88,v36
v_fma_f32 v37,-v78,v89,v37
v_mul_f32 v98,v34,v34
v_mul_f32 v99,v35,v35
v_mul_f32 v100,v36,v36
v_mul_f32 v101,v37,v37
v_fma_f32 v98,v80,v98,v79
v_fma_f32 v99,v80,v99,v79
v_fma_f32 v100,v80,v100,v79
v_fma_f32 v101,v80,v101,v79
v_mul_f32 v98,v34,v98
v_mul_f32 v99,v35,v99
v_mul_f32 v100,v36,v100
v_mul_f32 v101,v37,v101
v_exp_f32 v98,v98
v_exp_f32 v99,v99
v_exp_f32 v100,v100
v_exp_f32 v101,v101
v_fma_f32 v98,v98,v81,v81
v_fma_f32 v99,v99,v81,v81
v_fma_f32 v100,v100,v81,v81
v_fma_f32 v101,v101,v81,v81
v_rcp_f32 v98,v98
v_rcp_f32 v99,v99
v_rcp_f32 v100,v100
v_rcp_f32 v101,v101
v_mul_f32 v34,v34,v98
v_mul_f32 v35,v35,v99
v_mul_f32 v36,v36,v100
v_mul_f32 v37,v37,v101
v_cvt_pk_bf16_f32 v108,v34,v35
v_cvt_pk_bf16_f32 v109,v36,v37
buffer_store_dwordx4 v[106:109],v0,s[36:39],s67 offen offset:0 sc0 sc1
v_fma_f32 v38,-v78,v90,v38
v_fma_f32 v39,-v78,v91,v39
v_fma_f32 v40,-v78,v92,v40
v_fma_f32 v41,-v78,v93,v41
v_mul_f32 v102,v38,v38
v_mul_f32 v103,v39,v39
v_mul_f32 v104,v40,v40
v_mul_f32 v105,v41,v41
v_fma_f32 v102,v80,v102,v79
v_fma_f32 v103,v80,v103,v79
v_fma_f32 v104,v80,v104,v79
v_fma_f32 v105,v80,v105,v79
v_mul_f32 v102,v38,v102
v_mul_f32 v103,v39,v103
v_mul_f32 v104,v40,v104
v_mul_f32 v105,v41,v105
v_exp_f32 v102,v102
v_exp_f32 v103,v103
v_exp_f32 v104,v104
v_exp_f32 v105,v105
v_fma_f32 v102,v102,v81,v81
v_fma_f32 v103,v103,v81,v81
v_fma_f32 v104,v104,v81,v81
v_fma_f32 v105,v105,v81,v81
v_rcp_f32 v102,v102
v_rcp_f32 v103,v103
v_rcp_f32 v104,v104
v_rcp_f32 v105,v105
v_mul_f32 v38,v38,v102
v_mul_f32 v39,v39,v103
v_mul_f32 v40,v40,v104
v_mul_f32 v41,v41,v105
v_cvt_pk_bf16_f32 v110,v38,v39
v_cvt_pk_bf16_f32 v111,v40,v41
buffer_store_dwordx2 v[110:111],v10,s[36:39],s67 offen offset:0 sc0 sc1
s_mov_b32 s72,s69
s_mov_b32 s69,0x7fff0000
s_mov_b32 s67,0x7fff0000
s_mov_b32 s77,0x7fff0000
s_branch Lg3_done_%=
Lg3_done_%=:
s_waitcnt vmcnt(2)
s_barrier
buffer_store_dword v4,v3,s[36:39],s70 offen offset:0 sc0 sc1
s_waitcnt vmcnt(1)
s_barrier
buffer_store_dword v4,v3,s[36:39],s72 offen offset:0 sc0 sc1
s_waitcnt vmcnt(0) lgkmcnt(0)
s_barrier
s_endpgm
Lcode_g3_e:
.purgem M
.purgem R
.purgem T
.purgem D
.purgem L
)"
  :: "s"(ws), "s"(sWave), "s"(sG0), "s"(sStep), "s"(sOutOff), "s"(sFoutOff), "s"(sAuxOff), "s"(sWw), "s"(sBuf), "s"(sExp), "v"(vL16), "v"(vFlag), "v"(vSnapOff), "v"(vSnapRd), "v"(vWd0), "v"(vWd1), "v"(vWr0), "v"(vWr1), "v"(vWr2), "v"(vOutS), "v"(vAux0), "v"(vAux1), "v"(vAux2), "v"(vAux3), "v"(vAn), "v"(vAe), "s"(w3), "s"(out) : "v255", "v0", "v1", "v2", "v3", "v4", "v5", "v6", "v7", "v8", "v9", "v10", "v11", "v12", "v13", "v14", "v15", "v16", "v17", "v18", "v19", "v20", "v21", "v22", "v23", "v24", "v25", "v26", "v27", "v28", "v29", "v30", "v31", "v32", "v33", "v34", "v35", "v36", "v37", "v38", "v39", "v40", "v41", "v42", "v43", "v44", "v45", "v46", "v47", "v48", "v49", "v50", "v51", "v52", "v53", "v54", "v55", "v56", "v57", "v58", "v59", "v60", "v61", "v62", "v63", "v64", "v65", "v66", "v67", "v68", "v69", "v70", "v71", "v72", "v73", "v74", "v75", "v76", "v77", "v78", "v79", "v80", "v81", "v82", "v83", "v84", "v85", "v86", "v87", "v88", "v89", "v90", "v91", "v92", "v93", "v94", "v95", "v96", "v97", "v98", "v99", "v100", "v101", "v102", "v103", "v104", "v105", "v106", "v107", "v108", "v109", "v110", "v111", "v112", "v113", "v114", "v115", "v116", "v117", "v118", "v119", "v120", "v121", "v122", "v123", "v124", "v125", "v126", "v127", "v128", "v129", "v130", "v131", "v132", "v133", "v134", "v135", "v136", "v137", "v138", "v139", "v140", "v141", "v142", "v143", "v144", "v145", "v146", "v147", "v148", "v149", "v150", "v151", "v152", "v153", "v154", "v155", "v156", "v157", "v158", "v159", "v160", "v161", "v162", "v163", "v164", "v165", "v166", "v167", "v168", "v169", "v170", "v171", "v172", "v173", "v174", "v175", "v176", "v177", "v178", "v179", "v180", "v181", "v182", "v183", "v184", "v185", "v186", "v187", "v188", "v189", "v190", "v191", "v192", "v193", "v194", "v195", "v196", "v197", "v198", "v199", "v200", "v201", "v202", "v203", "v204", "v205", "v206", "v207", "v208", "v209", "v210", "v211", "v212", "v213", "v214", "v215", "v216", "v217", "v218", "v219", "v220", "v221", "v222", "v223", "v224", "v225", "v226", "v227", "v228", "v229", "v230", "v231", "a0", "a1", "a2", "a3", "a4", "a5", "a6", "a7", "a8", "a9", "a10", "a11", "a12", "a13", "a14", "a15", "a16", "a17", "a18", "a19", "a20", "a21", "a22", "a23", "a24", "a25", "a26", "a27", "a28", "a29", "a30", "a31", "a32", "a33", "a34", "a35", "a36", "a37", "a38", "a39", "a40", "a41", "a42", "a43", "a44", "a45", "a46", "a47", "a48", "a49", "a50", "a51", "a52", "a53", "a54", "a55", "a56", "a57", "a58", "a59", "a60", "a61", "a62", "a63", "a64", "a65", "a66", "a67", "a68", "a69", "a70", "a71", "a72", "a73", "a74", "a75", "a76", "a77", "a78", "a79", "a80", "a81", "a82", "a83", "a84", "a85", "a86", "a87", "a88", "a89", "a90", "a91", "a92", "a93", "a94", "a95", "a96", "a97", "a98", "a99", "a100", "a101", "a102", "a103", "a104", "a105", "a106", "a107", "a108", "a109", "a110", "a111", "a112", "a113", "a114", "a115", "a116", "a117", "a118", "a119", "a120", "a121", "a122", "a123", "a124", "a125", "a126", "a127", "a128", "a129", "a130", "a131", "a132", "a133", "a134", "a135", "a136", "a137", "a138", "a139", "a140", "a141", "a142", "a143", "a144", "a145", "a146", "a147", "a148", "a149", "a150", "a151", "a152", "a153", "a154", "a155", "a156", "a157", "a158", "a159", "a160", "a161", "a162", "a163", "a164", "a165", "a166", "a167", "a168", "a169", "a170", "a171", "a172", "a173", "a174", "a175", "a176", "a177", "a178", "a179", "a180", "a181", "a182", "a183", "a184", "a185", "a186", "a187", "a188", "a189", "a190", "a191", "a192", "a193", "a194", "a195", "a196", "a197", "a198", "a199", "a200", "a201", "a202", "a203", "a204", "a205", "a206", "a207", "a208", "a209", "a210", "a211", "a212", "a213", "a214", "a215", "a216", "a217", "a218", "a219", "a220", "a221", "a222", "a223", "a224", "a225", "a226", "a227", "a228", "a229", "a230", "a231", "a232", "a233", "a234", "a235", "a236", "a237", "a238", "a239", "a240", "a241", "a242", "a243", "a244", "a245", "a246", "a247", "a248", "a249", "a250", "a251", "a252", "a253", "a254", "a255", "s36", "s37", "s38", "s39", "s40", "s41", "s42", "s43", "s44", "s45", "s46", "s47", "s48", "s49", "s50", "s51", "s52", "s53", "s54", "s55", "s56", "s57", "s58", "s59", "s60", "s61", "s62", "s63", "s64", "s65", "s66", "s67", "s68", "s69", "s70", "s71", "s72", "s73", "s74", "s75", "s76", "s77", "s78", "s79", "s80", "s81", "s82", "s83", "s84", "s85", "s86", "s87", "s88", "s89", "s90", "s91", "s92", "s93", "s94", "s95", "s96", "s97", "vcc", "scc", "memory"); } else { asm volatile(
R"(.macro M d,w,x
v_mfma_f32_16x16x32_bf16 v[\d:\d+3], \w, v[\x:\x+3], v[\d:\d+3]
.endm
.macro R d,a,o
ds_read_b128 v[\d:\d+3], v\a offset:\o
.endm
.macro T d,a,o
ds_read_b64_tr_b16 \d, v\a offset:\o
.endm
.macro D a,s,o
buffer_load_dwordx4 v\a, s[36:39], s\s offen offset:\o sc1 lds
.endm
.macro L c
s_waitcnt lgkmcnt(\c)
.endm
Lcode_g3o_b:
s_mov_b64 s[36:37],%0
s_mov_b32 s38,111243392
s_mov_b32 s39,0x00020000
s_mov_b32 s44,%1
s_mov_b32 s45,%2
s_mov_b32 s46,%3
s_mov_b32 s47,%4
s_mov_b32 s48,%5
s_mov_b32 s49,%6
s_mov_b32 s50,%7
s_mov_b32 s51,%8
s_mov_b32 s53,%9
v_mov_b32 v0,%10
v_mov_b32 v3,%11
v_mov_b32 v5,%12
v_mov_b32 v6,%13
v_mov_b32 v18,%14
v_mov_b32 v19,%15
v_mov_b32 v20,%16
v_mov_b32 v21,%17
v_mov_b32 v22,%18
v_mov_b32 v10,%19
v_mov_b32 v11,%20
v_mov_b32 v12,%21
v_mov_b32 v13,%22
v_mov_b32 v14,%23
v_mov_b32 v15,%24
v_mov_b32 v16,%25
s_mov_b64 s[40:41],%26
s_mov_b32 s42,7340032
s_mov_b32 s43,0x00020000
s_lshl_b32 s57,s44,11
s_add_u32 s55,s50,0
s_add_u32 m0,s57,0
s_nop 0
buffer_load_dwordx4 v18,s[40:43],s55 offen offset:0  lds
s_add_u32 m0,s57,1024
s_nop 0
buffer_load_dwordx4 v19,s[40:43],s55 offen offset:0  lds
s_add_u32 m0,s57,8064
s_nop 0
buffer_load_dwordx4 v18,s[40:43],s55 offen offset:128  lds
s_add_u32 m0,s57,9088
s_nop 0
buffer_load_dwordx4 v19,s[40:43],s55 offen offset:128  lds
s_add_u32 s55,s50,458752
s_add_u32 m0,s57,16384
s_nop 0
buffer_load_dwordx4 v18,s[40:43],s55 offen offset:0  lds
s_add_u32 m0,s57,17408
s_nop 0
buffer_load_dwordx4 v19,s[40:43],s55 offen offset:0  lds
s_add_u32 m0,s57,24448
s_nop 0
buffer_load_dwordx4 v18,s[40:43],s55 offen offset:128  lds
s_add_u32 m0,s57,25472
s_nop 0
buffer_load_dwordx4 v19,s[40:43],s55 offen offset:128  lds
s_add_u32 s55,s50,917504
s_add_u32 m0,s57,32768
s_nop 0
buffer_load_dwordx4 v18,s[40:43],s55 offen offset:0  lds
s_add_u32 m0,s57,33792
s_nop 0
buffer_load_dwordx4 v19,s[40:43],s55 offen offset:0  lds
s_add_u32 m0,s57,40832
s_nop 0
buffer_load_dwordx4 v18,s[40:43],s55 offen offset:128  lds
s_add_u32 m0,s57,41856
s_nop 0
buffer_load_dwordx4 v19,s[40:43],s55 offen offset:128  lds
s_add_u32 s55,s50,1376256
s_add_u32 m0,s57,49152
s_nop 0
buffer_load_dwordx4 v18,s[40:43],s55 offen offset:0  lds
s_add_u32 m0,s57,50176
s_nop 0
buffer_load_dwordx4 v19,s[40:43],s55 offen offset:0  lds
s_add_u32 m0,s57,57216
s_nop 0
buffer_load_dwordx4 v18,s[40:43],s55 offen offset:128  lds
s_add_u32 m0,s57,58240
s_nop 0
buffer_load_dwordx4 v19,s[40:43],s55 offen offset:128  lds
s_add_u32 s55,s50,1835008
s_add_u32 m0,s57,65536
s_nop 0
buffer_load_dwordx4 v18,s[40:43],s55 offen offset:0  lds
s_add_u32 m0,s57,66560
s_nop 0
buffer_load_dwordx4 v19,s[40:43],s55 offen offset:0  lds
s_add_u32 m0,s57,73600
s_nop 0
buffer_load_dwordx4 v18,s[40:43],s55 offen offset:128  lds
s_add_u32 m0,s57,74624
s_nop 0
buffer_load_dwordx4 v19,s[40:43],s55 offen offset:128  lds
s_add_u32 s55,s50,2293760
s_add_u32 m0,s57,81920
s_nop 0
buffer_load_dwordx4 v18,s[40:43],s55 offen offset:0  lds
s_add_u32 m0,s57,82944
s_nop 0
buffer_load_dwordx4 v19,s[40:43],s55 offen offset:0  lds
s_add_u32 m0,s57,89984
s_nop 0
buffer_load_dwordx4 v18,s[40:43],s55 offen offset:128  lds
s_add_u32 m0,s57,91008
s_nop 0
buffer_load_dwordx4 v19,s[40:43],s55 offen offset:128  lds
s_waitcnt vmcnt(20) lgkmcnt(0)
s_barrier
s_add_u32 s55,s50,2752512
s_add_u32 m0,s57,98304
s_nop 0
buffer_load_dwordx4 v18,s[40:43],s55 offen offset:0  lds
s_add_u32 m0,s57,99328
s_nop 0
buffer_load_dwordx4 v19,s[40:43],s55 offen offset:0  lds
s_add_u32 m0,s57,106368
s_nop 0
buffer_load_dwordx4 v18,s[40:43],s55 offen offset:128  lds
s_add_u32 m0,s57,107392
s_nop 0
buffer_load_dwordx4 v19,s[40:43],s55 offen offset:128  lds
v_add_u32 v26,0,v20
v_add_u32 v27,0,v21
T a[0:1],26,0
T a[2:3],26,512
T a[4:5],27,0
T a[6:7],27,512
T a[8:9],26,4096
T a[10:11],26,4608
T a[12:13],27,4096
T a[14:15],27,4608
s_waitcnt vmcnt(20) lgkmcnt(0)
s_barrier
s_add_u32 s55,s50,3211264
s_add_u32 m0,s57,0
s_nop 0
buffer_load_dwordx4 v18,s[40:43],s55 offen offset:0  lds
s_add_u32 m0,s57,1024
s_nop 0
buffer_load_dwordx4 v19,s[40:43],s55 offen offset:0  lds
s_add_u32 m0,s57,8064
s_nop 0
buffer_load_dwordx4 v18,s[40:43],s55 offen offset:128  lds
s_add_u32 m0,s57,9088
s_nop 0
buffer_load_dwordx4 v19,s[40:43],s55 offen offset:128  lds
v_add_u32 v26,16384,v20
v_add_u32 v27,16384,v21
T a[16:17],26,0
T a[18:19],26,512
T a[20:21],27,0
T a[22:23],27,512
T a[24:25],26,4096
T a[26:27],26,4608
T a[28:29],27,4096
T a[30:31],27,4608
s_waitcnt vmcnt(20) lgkmcnt(0)
s_barrier
s_add_u32 s55,s50,3670016
s_add_u32 m0,s57,16384
s_nop 0
buffer_load_dwordx4 v18,s[40:43],s55 offen offset:0  lds
s_add_u32 m0,s57,17408
s_nop 0
buffer_load_dwordx4 v19,s[40:43],s55 offen offset:0  lds
s_add_u32 m0,s57,24448
s_nop 0
buffer_load_dwordx4 v18,s[40:43],s55 offen offset:128  lds
s_add_u32 m0,s57,25472
s_nop 0
buffer_load_dwordx4 v19,s[40:43],s55 offen offset:128  lds
v_add_u32 v26,32768,v20
v_add_u32 v27,32768,v21
T a[32:33],26,0
T a[34:35],26,512
T a[36:37],27,0
T a[38:39],27,512
T a[40:41],26,4096
T a[42:43],26,4608
T a[44:45],27,4096
T a[46:47],27,4608
s_waitcnt vmcnt(20) lgkmcnt(0)
s_barrier
s_add_u32 s55,s50,4128768
s_add_u32 m0,s57,32768
s_nop 0
buffer_load_dwordx4 v18,s[40:43],s55 offen offset:0  lds
s_add_u32 m0,s57,33792
s_nop 0
buffer_load_dwordx4 v19,s[40:43],s55 offen offset:0  lds
s_add_u32 m0,s57,40832
s_nop 0
buffer_load_dwordx4 v18,s[40:43],s55 offen offset:128  lds
s_add_u32 m0,s57,41856
s_nop 0
buffer_load_dwordx4 v19,s[40:43],s55 offen offset:128  lds
v_add_u32 v26,49152,v20
v_add_u32 v27,49152,v21
T a[48:49],26,0
T a[50:51],26,512
T a[52:53],27,0
T a[54:55],27,512
T a[56:57],26,4096
T a[58:59],26,4608
T a[60:61],27,4096
T a[62:63],27,4608
s_waitcnt vmcnt(20) lgkmcnt(0)
s_barrier
s_add_u32 s55,s50,4587520
s_add_u32 m0,s57,49152
s_nop 0
buffer_load_dwordx4 v18,s[40:43],s55 offen offset:0  lds
s_add_u32 m0,s57,50176
s_nop 0
buffer_load_dwordx4 v19,s[40:43],s55 offen offset:0  lds
s_add_u32 m0,s57,57216
s_nop 0
buffer_load_dwordx4 v18,s[40:43],s55 offen offset:128  lds
s_add_u32 m0,s57,58240
s_nop 0
buffer_load_dwordx4 v19,s[40:43],s55 offen offset:128  lds
v_add_u32 v26,65536,v20
v_add_u32 v27,65536,v21
T a[64:65],26,0
T a[66:67],26,512
T a[68:69],27,0
T a[70:71],27,512
T a[72:73],26,4096
T a[74:75],26,4608
T a[76:77],27,4096
T a[78:79],27,4608
s_waitcnt vmcnt(20) lgkmcnt(0)
s_barrier
s_add_u32 s55,s50,5046272
s_add_u32 m0,s57,65536
s_nop 0
buffer_load_dwordx4 v18,s[40:43],s55 offen offset:0  lds
s_add_u32 m0,s57,66560
s_nop 0
buffer_load_dwordx4 v19,s[40:43],s55 offen offset:0  lds
s_add_u32 m0,s57,73600
s_nop 0
buffer_load_dwordx4 v18,s[40:43],s55 offen offset:128  lds
s_add_u32 m0,s57,74624
s_nop 0
buffer_load_dwordx4 v19,s[40:43],s55 offen offset:128  lds
v_add_u32 v26,81920,v20
v_add_u32 v27,81920,v21
T a[80:81],26,0
T a[82:83],26,512
T a[84:85],27,0
T a[86:87],27,512
T a[88:89],26,4096
T a[90:91],26,4608
T a[92:93],27,4096
T a[94:95],27,4608
s_waitcnt vmcnt(20) lgkmcnt(0)
s_barrier
s_add_u32 s55,s50,5505024
s_add_u32 m0,s57,81920
s_nop 0
buffer_load_dwordx4 v18,s[40:43],s55 offen offset:0  lds
s_add_u32 m0,s57,82944
s_nop 0
buffer_load_dwordx4 v19,s[40:43],s55 offen offset:0  lds
s_add_u32 m0,s57,89984
s_nop 0
buffer_load_dwordx4 v18,s[40:43],s55 offen offset:128  lds
s_add_u32 m0,s57,91008
s_nop 0
buffer_load_dwordx4 v19,s[40:43],s55 offen offset:128  lds
v_add_u32 v26,98304,v20
v_add_u32 v27,98304,v21
T a[96:97],26,0
T a[98:99],26,512
T a[100:101],27,0
T a[102:103],27,512
T a[104:105],26,4096
T a[106:107],26,4608
T a[108:109],27,4096
T a[110:111],27,4608
s_waitcnt vmcnt(20) lgkmcnt(0)
s_barrier
s_add_u32 s55,s50,5963776
s_add_u32 m0,s57,98304
s_nop 0
buffer_load_dwordx4 v18,s[40:43],s55 offen offset:0  lds
s_add_u32 m0,s57,99328
s_nop 0
buffer_load_dwordx4 v19,s[40:43],s55 offen offset:0  lds
s_add_u32 m0,s57,106368
s_nop 0
buffer_load_dwordx4 v18,s[40:43],s55 offen offset:128  lds
s_add_u32 m0,s57,107392
s_nop 0
buffer_load_dwordx4 v19,s[40:43],s55 offen offset:128  lds
v_add_u32 v26,0,v20
v_add_u32 v27,0,v21
T a[112:113],26,0
T a[114:115],26,512
T a[116:117],27,0
T a[118:119],27,512
T a[120:121],26,4096
T a[122:123],26,4608
T a[124:125],27,4096
T a[126:127],27,4608
s_waitcnt vmcnt(20) lgkmcnt(0)
s_barrier
s_add_u32 s55,s50,6422528
s_add_u32 m0,s57,0
s_nop 0
buffer_load_dwordx4 v18,s[40:43],s55 offen offset:0  lds
s_add_u32 m0,s57,1024
s_nop 0
buffer_load_dwordx4 v19,s[40:43],s55 offen offset:0  lds
s_add_u32 m0,s57,8064
s_nop 0
buffer_load_dwordx4 v18,s[40:43],s55 offen offset:128  lds
s_add_u32 m0,s57,9088
s_nop 0
buffer_load_dwordx4 v19,s[40:43],s55 offen offset:128  lds
v_add_u32 v26,16384,v20
v_add_u32 v27,16384,v21
T a[128:129],26,0
T a[130:131],26,512
T a[132:133],27,0
T a[134:135],27,512
T a[136:137],26,4096
T a[138:139],26,4608
T a[140:141],27,4096
T a[142:143],27,4608
s_waitcnt vmcnt(20) lgkmcnt(0)
s_barrier
s_add_u32 s55,s50,6881280
s_add_u32 m0,s57,16384
s_nop 0
buffer_load_dwordx4 v18,s[40:43],s55 offen offset:0  lds
s_add_u32 m0,s57,17408
s_nop 0
buffer_load_dwordx4 v19,s[40:43],s55 offen offset:0  lds
s_add_u32 m0,s57,24448
s_nop 0
buffer_load_dwordx4 v18,s[40:43],s55 offen offset:128  lds
s_add_u32 m0,s57,25472
s_nop 0
buffer_load_dwordx4 v19,s[40:43],s55 offen offset:128  lds
v_add_u32 v26,32768,v20
v_add_u32 v27,32768,v21
T a[144:145],26,0
T a[146:147],26,512
T a[148:149],27,0
T a[150:151],27,512
T a[152:153],26,4096
T a[154:155],26,4608
T a[156:157],27,4096
T a[158:159],27,4608
s_waitcnt vmcnt(20) lgkmcnt(0)
s_barrier
v_add_u32 v26,49152,v20
v_add_u32 v27,49152,v21
T a[160:161],26,0
T a[162:163],26,512
T a[164:165],27,0
T a[166:167],27,512
T a[168:169],26,4096
T a[170:171],26,4608
T a[172:173],27,4096
T a[174:175],27,4608
s_waitcnt vmcnt(16) lgkmcnt(0)
s_barrier
v_add_u32 v26,65536,v20
v_add_u32 v27,65536,v21
T a[176:177],26,0
T a[178:179],26,512
T a[180:181],27,0
T a[182:183],27,512
T a[184:185],26,4096
T a[186:187],26,4608
T a[188:189],27,4096
T a[190:191],27,4608
s_waitcnt vmcnt(12) lgkmcnt(0)
s_barrier
v_add_u32 v26,81920,v20
v_add_u32 v27,81920,v21
T a[192:193],26,0
T a[194:195],26,512
T a[196:197],27,0
T a[198:199],27,512
T a[200:201],26,4096
T a[202:203],26,4608
T a[204:205],27,4096
T a[206:207],27,4608
s_waitcnt vmcnt(8) lgkmcnt(0)
s_barrier
v_add_u32 v26,98304,v20
v_add_u32 v27,98304,v21
T a[208:209],26,0
T a[210:211],26,512
T a[212:213],27,0
T a[214:215],27,512
T a[216:217],26,4096
T a[218:219],26,4608
T a[220:221],27,4096
T a[222:223],27,4608
s_waitcnt vmcnt(4) lgkmcnt(0)
s_barrier
v_add_u32 v26,0,v20
v_add_u32 v27,0,v21
T a[224:225],26,0
T a[226:227],26,512
T a[228:229],27,0
T a[230:231],27,512
T a[232:233],26,4096
T a[234:235],26,4608
T a[236:237],27,4096
T a[238:239],27,4608
s_waitcnt vmcnt(0) lgkmcnt(0)
s_barrier
v_add_u32 v26,16384,v20
v_add_u32 v27,16384,v21
T a[240:241],26,0
T a[242:243],26,512
T a[244:245],27,0
T a[246:247],27,512
T a[248:249],26,4096
T a[250:251],26,4608
T a[252:253],27,4096
T a[254:255],27,4608
L 0
s_barrier
v_mov_b32 v34,0x3f803f80
v_mov_b32 v35,0x3f803f80
v_mov_b32 v36,0x3f803f80
v_mov_b32 v37,0x3f803f80
s_nop 1
v_mfma_f32_16x16x32_bf16 v[106:109],a[0:3],v[34:37],0
v_mfma_f32_16x16x32_bf16 v[110:113],a[4:7],v[34:37],0
M 106,a[8:11],34
M 110,a[12:15],34
M 106,a[16:19],34
M 110,a[20:23],34
M 106,a[24:27],34
M 110,a[28:31],34
M 106,a[32:35],34
M 110,a[36:39],34
M 106,a[40:43],34
M 110,a[44:47],34
M 106,a[48:51],34
M 110,a[52:55],34
M 106,a[56:59],34
M 110,a[60:63],34
M 106,a[64:67],34
M 110,a[68:71],34
M 106,a[72:75],34
M 110,a[76:79],34
M 106,a[80:83],34
M 110,a[84:87],34
M 106,a[88:91],34
M 110,a[92:95],34
M 106,a[96:99],34
M 110,a[100:103],34
M 106,a[104:107],34
M 110,a[108:111],34
M 106,a[112:115],34
M 110,a[116:119],34
M 106,a[120:123],34
M 110,a[124:127],34
M 106,a[128:131],34
M 110,a[132:135],34
M 106,a[136:139],34
M 110,a[140:143],34
M 106,a[144:147],34
M 110,a[148:151],34
M 106,a[152:155],34
M 110,a[156:159],34
M 106,a[160:163],34
M 110,a[164:167],34
M 106,a[168:171],34
M 110,a[172:175],34
M 106,a[176:179],34
)"
R"(M 110,a[180:183],34
M 106,a[184:187],34
M 110,a[188:191],34
M 106,a[192:195],34
M 110,a[196:199],34
M 106,a[200:203],34
M 110,a[204:207],34
M 106,a[208:211],34
M 110,a[212:215],34
M 106,a[216:219],34
M 110,a[220:223],34
M 106,a[224:227],34
M 110,a[228:231],34
M 106,a[232:235],34
M 110,a[236:239],34
M 106,a[240:243],34
M 110,a[244:247],34
M 106,a[248:251],34
M 110,a[252:255],34
v_mov_b32 v4,s53
s_mov_b32 s82,0
s_mov_b32 s80,0
s_mov_b32 s54,s45
s_mul_i32 s57,s44,4096
s_mul_i32 s55,s45,32768
s_add_u32 s55,s55,s57
s_add_u32 s55,s55,31457280
s_mul_i32 s57,s44,4096
s_mul_i32 s60,s46,32768
s_mul_i32 s61,s46,1024
s_mul_i32 s62,s46,114688
s_mul_i32 s63,s46,2560
s_mul_i32 s64,s46,1024
s_lshl_b32 s59,s44,8
s_add_u32 s59,s59,131072
s_mul_i32 s58,s45,1024
s_add_u32 s58,s58,109613056
s_mul_i32 s65,s45,114688
s_add_u32 s65,s65,s47
s_mul_i32 s68,s45,2560
s_add_u32 s68,s68,s48
s_mul_i32 s73,s45,1024
s_add_u32 s73,s73,107479040
s_mul_i32 s74,s45,32768
s_add_u32 s74,s74,s49
s_mul_i32 s75,s45,1024
s_add_u32 s75,s75,107479040
s_mov_b32 s66,0x7fff0000
s_mov_b32 s67,0x7fff0000
s_mov_b32 s69,0x7fff0000
s_mov_b32 s76,0x7fff0000
s_mov_b32 s77,0x7fff0000
s_mov_b32 s70,0x7fff0000
s_mov_b32 s71,0x7fff0000
s_mov_b32 s72,0x7fff0000
v_mov_b32 v1,v0
v_add_u32 v2,16384,v0
s_mov_b32 s84,16384
s_mov_b32 s86,0x3a800000
s_mov_b32 s87,0x3727c5ac
s_mov_b32 s88,0xc0135761
s_mov_b32 s89,0x3d372713
s_lshr_b32 s85,s44,1
s_mul_i32 s85,s85,127
v_lshlrev_b32 v8,22,v0
v_and_b32 v8,0x80000000,v8
v_or_b32 v8,v8,v5
v_add_u32 v2,32768,v0
s_lshl_b32 s84,s57,1
s_add_u32 s84,s84,32768
v_and_b32 v114,240,v0
v_cmp_eq_u32 vcc,0,v114
s_nop 1
v_cndmask_b32 v17,0,1.0,vcc
s_getpc_b64 s[40:41]
Lg3o_pc_%=:
s_lshl_b32 s78,s44,13
v_lshlrev_b32 v114,3,v0
v_add_u32 v114,s78,v114
v_min_u32 v114,Lcode_g3o_e-Lg3o_pc_%=-4,v114
s_mov_b32 m0,163584
s_nop 0
global_load_lds_dword v114,s[40:41]
s_add_u32 m0,s59,4096
s_nop 0
buffer_load_dword v8,s[36:39],s58 offen offset:0 sc1 lds
buffer_store_dword v4,v3,s[36:39],s71 offen offset:0 sc0 sc1
s_add_u32 m0,s59,5120
s_nop 0
buffer_load_dword v8,s[36:39],s58 offen offset:0 sc1 lds
buffer_store_dword v4,v3,s[36:39],s71 offen offset:0 sc0 sc1
s_mov_b32 s83,0
Lg3o_poll_%=:
s_add_u32 s78,s83,2
s_and_b32 s78,s78,3
s_lshl_b32 s78,s78,10
s_add_u32 s78,s78,4096
s_add_u32 m0,s59,s78
s_nop 0
buffer_load_dword v8,s[36:39],s58 offen offset:0 sc1 lds
s_and_b32 s78,s83,3
s_lshl_b32 s78,s78,10
v_add_u32 v114,s78,v6
s_waitcnt vmcnt(4)
s_barrier
ds_read_b32 v7,v114 offset:4096
buffer_store_dword v4,v3,s[36:39],s70 offen offset:0 sc0 sc1
s_mov_b32 s70,s71
s_mov_b32 s71,s72
s_mov_b32 s72,0x7fff0000
s_add_u32 s83,s83,1
s_add_u32 s82,s82,1
L 0
v_cmp_eq_u32 vcc,s53,v7
s_nop 0
s_cmp_eq_u32 vcc_lo,-1
s_cbranch_scc1 Lg3o_go_%=
s_cmp_lt_u32 s82,20000
s_cbranch_scc1 Lg3o_poll_%=
Lg3o_go_%=:
buffer_load_dword v130,v11,s[36:39],s73 offen offset:0 sc1
s_add_u32 s73,s73,s64
s_add_u32 m0,s57,0
s_add_u32 s56,s55,0
D 0,56,0
D 0,56,1024
D 0,56,2048
D 0,56,3072
s_add_u32 s58,s58,s61
s_add_u32 m0,s59,2048
s_nop 0
buffer_load_dword v8,s[36:39],s58 offen offset:0 sc1 lds
s_add_u32 m0,s57,16384
s_add_u32 s56,s55,16384
D 0,56,0
D 0,56,1024
D 0,56,2048
D 0,56,3072
s_add_u32 s55,s55,s60
s_sub_u32 s57,s84,s57
s_add_u32 s54,s54,s46
s_waitcnt vmcnt(4)
s_barrier
buffer_store_dword v4,v3,s[36:39],s70 offen offset:0 sc0 sc1
s_min_u32 s71,s71,s72
buffer_store_dword v4,v3,s[36:39],s71 offen offset:0 sc0 sc1
s_mov_b32 s70,0x7fff0000
s_mov_b32 s71,0x7fff0000
s_mov_b32 s72,0x7fff0000
s_cmp_eq_u32 s80,1
s_cbranch_scc1 Lg3o_st1_%=
s_branch Lg3o_st0_%=
Lg3o_gox_%=:
s_add_u32 m0,s59,2048
s_nop 0
buffer_load_dword v8,s[36:39],s58 offen offset:0 sc1 lds
s_waitcnt vmcnt(0)
s_barrier
buffer_store_dword v4,v3,s[36:39],s70 offen offset:0 sc0 sc1
s_min_u32 s71,s71,s72
buffer_store_dword v4,v3,s[36:39],s71 offen offset:0 sc0 sc1
s_mov_b32 s70,0x7fff0000
s_mov_b32 s71,0x7fff0000
s_mov_b32 s72,0x7fff0000
s_cmp_eq_u32 s80,1
s_cbranch_scc1 Lg3o_st1_%=
Lg3o_st0_%=:
ds_read_b32 v7,v6 offset:2048
R 34,1,0
R 38,1,1024
R 42,1,2048
R 46,1,3072
R 50,1,4096
L 5
v_cmp_eq_u32 vcc,s53,v7
L 3
s_cmp_eq_u32 vcc_lo,-1
s_cbranch_scc1 Lg3o_F0_%=
s_branch Lg3o_L0_%=
Lg3o_st1_%=:
ds_read_b32 v7,v6 offset:2048
R 34,2,0
R 38,2,1024
R 42,2,2048
R 46,2,3072
R 50,2,4096
L 5
v_cmp_eq_u32 vcc,s53,v7
L 3
s_cmp_eq_u32 vcc_lo,-1
s_cbranch_scc1 Lg3o_F1_%=
s_branch Lg3o_L1_%=
Lg3o_F0_%=:
v_mfma_f32_16x16x32_bf16 v[22:25],a[4:7],v[34:37],0
s_add_u32 s58,s58,s61
s_add_u32 m0,s59,0
R 54,1,5120
M 22,a[12:15],38
buffer_load_dword v8,s[36:39],s58 offen offset:0 sc1 lds
v_mfma_f32_16x16x32_bf16 v[18:21],a[0:3],v[34:37],0
s_add_u32 m0,s57,0
R 58,1,6144
v_add_f32_dpp v131,v130,v130 quad_perm:[1,0,3,2] row_mask:0xf bank_mask:0xf
M 18,a[8:11],38
L 3
s_add_u32 s56,s55,0
buffer_load_dword v130,v11,s[36:39],s73 offen offset:0 sc1
M 18,a[16:19],42
s_mov_b32 s66,s65
R 62,1,7168
v_add_f32_dpp v131,v131,v131 quad_perm:[2,3,0,1] row_mask:0xf bank_mask:0xf
M 18,a[24:27],46
D 0,56,0
M 22,a[20:23],42
s_add_u32 s65,s65,s62
R 66,1,8192
v_add_f32_dpp v134,v131,v131 row_half_mirror row_mask:0xf bank_mask:0xf
M 22,a[28:31],46
L 3
D 0,56,1024
M 22,a[36:39],50
s_add_u32 s54,s54,s46
R 70,1,9216
v_mov_b32_dpp v131,v134 row_ror:8 row_mask:0xf bank_mask:0xf
M 22,a[44:47],54
s_add_u32 s73,s73,s64
v_fma_f32 v131,-v134,v134,v131
v_max_f32 v131,s87,v131
M 18,a[32:35],50
R 74,1,10240
v_mul_f32 v134,v17,v134
M 18,a[40:43],54
L 3
D 0,56,2048
M 18,a[48:51],58
R 78,1,11264
v_fma_f32 v26,-v102,v106,v26
M 18,a[56:59],62
D 0,56,3072
M 22,a[52:55],58
s_add_u32 m0,s57,16384
R 82,1,12288
v_fma_f32 v27,-v102,v107,v27
M 22,a[60:63],62
L 3
v_rsq_f32 v132,v131
M 22,a[68:71],66
s_add_u32 s56,s55,16384
R 86,1,13312
v_fma_f32 v131,v15,v131,v16
M 22,a[76:79],70
s_add_u32 s55,s55,s60
v_mul_f32 v135,v132,v132
v_fma_f32 v131,v14,v135,v131
M 18,a[64:67],66
s_sub_u32 s57,s84,s57
R 90,1,14336
v_fma_f32 v134,v132,v131,v134
M 18,a[72:75],70
L 3
ds_write_b32 v12,v134 offset:0
M 18,a[80:83],74
s_waitcnt vmcnt(8)
R 94,1,15360
v_fma_f32 v28,-v102,v108,v28
M 18,a[88:91],78
s_barrier
v_fma_f32 v29,-v102,v109,v29
v_mul_f32 v114,v26,v26
M 22,a[84:87],74
R 34,1,16384
v_mul_f32 v115,v27,v27
M 22,a[92:95],78
L 4
v_mul_f32 v116,v28,v28
v_mul_f32 v117,v29,v29
M 22,a[100:103],82
R 38,1,17408
v_fma_f32 v114,v104,v114,v103
M 22,a[108:111],86
D 0,56,0
M 18,a[96:99],82
R 42,1,18432
v_fma_f32 v115,v104,v115,v103
M 18,a[104:107],86
L 3
v_fma_f32 v116,v104,v116,v103
v_fma_f32 v117,v104,v117,v103
M 18,a[112:115],90
R 46,1,19456
v_mul_f32 v114,v26,v114
M 18,a[120:123],94
v_exp_f32 v114,v114
M 22,a[116:119],90
R 50,1,20480
v_mul_f32 v115,v27,v115
M 22,a[124:127],94
L 3
v_exp_f32 v115,v115
M 22,a[132:135],34
R 54,1,21504
v_mul_f32 v116,v28,v116
M 22,a[140:143],38
D 0,56,1024
M 18,a[128:131],34
R 58,1,22528
v_mul_f32 v117,v29,v117
M 18,a[136:139],38
L 3
v_exp_f32 v116,v116
M 18,a[144:147],42
R 62,1,23552
v_fma_f32 v114,v114,v105,v105
M 18,a[152:155],46
v_exp_f32 v117,v117
M 22,a[148:151],42
R 66,1,24576
v_fma_f32 v115,v115,v105,v105
M 22,a[156:159],46
L 3
D 0,56,2048
M 22,a[164:167],50
R 70,1,25600
v_fma_f32 v116,v116,v105,v105
M 22,a[172:175],54
v_rcp_f32 v114,v114
M 18,a[160:163],50
R 74,1,26624
v_fma_f32 v117,v117,v105,v105
M 18,a[168:171],54
L 3
v_rcp_f32 v115,v115
M 18,a[176:179],58
R 78,1,27648
v_mul_f32 v26,v26,v114
M 18,a[184:187],62
v_rcp_f32 v116,v116
M 22,a[180:183],58
R 82,1,28672
v_mul_f32 v27,v27,v115
M 22,a[188:191],62
L 3
D 0,56,3072
M 22,a[196:199],66
R 86,1,29696
v_mul_f32 v28,v28,v116
M 22,a[204:207],70
v_rcp_f32 v117,v117
M 18,a[192:195],66
R 90,1,30720
v_mul_f32 v29,v29,v117
M 18,a[200:203],70
L 3
s_nop 0
v_cvt_pk_bf16_f32 v122,v26,v27
M 18,a[208:211],74
s_waitcnt vmcnt(4)
R 94,1,31744
v_fma_f32 v30,-v102,v110,v30
M 18,a[216:219],78
s_barrier
ds_read_b32 v7,v6 offset:0
R 98,13,0
M 22,a[212:215],74
R 34,2,0
v_fma_f32 v31,-v102,v111,v31
M 22,a[220:223],78
L 5
buffer_store_dword v4,v3,s[36:39],s70 offen offset:0 sc0 sc1
M 22,a[228:231],82
s_mov_b32 s70,s69
R 38,2,1024
v_fma_f32 v32,-v102,v112,v32
M 22,a[236:239],86
s_mov_b32 s69,s68
v_fma_f32 v33,-v102,v113,v33
v_mul_f32 v118,v30,v30
M 18,a[224:227],82
s_add_u32 s68,s68,s63
R 42,2,2048
v_mul_f32 v119,v31,v31
M 18,a[232:235],86
L 5
v_mul_f32 v120,v32,v32
v_mul_f32 v121,v33,v33
v_fma_f32 v118,v104,v118,v103
v_fma_f32 v119,v104,v119,v103
v_fma_f32 v120,v104,v120,v103
v_fma_f32 v121,v104,v121,v103
v_mul_f32 v118,v30,v118
v_mul_f32 v119,v31,v119
v_mul_f32 v120,v32,v120
v_mul_f32 v121,v33,v121
v_exp_f32 v118,v118
v_exp_f32 v119,v119
v_exp_f32 v120,v120
v_exp_f32 v121,v121
M 18,a[240:243],90
L 4
R 46,2,3072
v_fma_f32 v118,v118,v105,v105
v_fma_f32 v119,v119,v105,v105
v_fma_f32 v120,v120,v105,v105
v_fma_f32 v121,v121,v105,v105
v_rcp_f32 v118,v118
v_rcp_f32 v119,v119
v_rcp_f32 v120,v120
v_rcp_f32 v121,v121
v_cmp_eq_u32 vcc,s53,v7
M 18,a[248:251],94
v_cvt_pk_bf16_f32 v123,v28,v29
v_mul_f32 v30,v30,v118
v_mul_f32 v31,v31,v119
v_mul_f32 v32,v32,v120
v_mul_f32 v33,v33,v121
v_cvt_pk_bf16_f32 v124,v30,v31
v_cvt_pk_bf16_f32 v125,v32,v33
M 22,a[244:247],90
L 2
s_cmp_eq_u32 vcc_lo,-1
R 50,2,4096
buffer_store_dwordx4 v[122:125],v0,s[36:39],s67 offen offset:0 sc0 sc1
M 22,a[252:255],94
s_cbranch_scc0 Lg3o_L1_%=
Lg3o_F1_%=:
v_mfma_f32_16x16x32_bf16 v[30:33],a[4:7],v[34:37],0
s_add_u32 s58,s58,s61
s_add_u32 m0,s59,1024
R 54,2,5120
M 30,a[12:15],38
buffer_load_dword v8,s[36:39],s58 offen offset:0 sc1 lds
v_mfma_f32_16x16x32_bf16 v[26:29],a[0:3],v[34:37],0
s_add_u32 m0,s57,0
R 58,2,6144
v_add_f32_dpp v131,v130,v130 quad_perm:[1,0,3,2] row_mask:0xf bank_mask:0xf
M 26,a[8:11],38
L 3
s_add_u32 s56,s55,0
buffer_load_dword v130,v11,s[36:39],s73 offen offset:0 sc1
M 26,a[16:19],42
s_mov_b32 s67,s65
R 62,2,7168
v_add_f32_dpp v131,v131,v131 quad_perm:[2,3,0,1] row_mask:0xf bank_mask:0xf
M 26,a[24:27],46
D 0,56,0
M 30,a[20:23],42
s_add_u32 s65,s65,s62
R 66,2,8192
v_add_f32_dpp v134,v131,v131 row_half_mirror row_mask:0xf bank_mask:0xf
M 30,a[28:31],46
L 3
D 0,56,1024
M 30,a[36:39],50
s_add_u32 s54,s54,s46
R 70,2,9216
v_mov_b32_dpp v131,v134 row_ror:8 row_mask:0xf bank_mask:0xf
M 30,a[44:47],54
s_add_u32 s73,s73,s64
v_fma_f32 v131,-v134,v134,v131
v_max_f32 v131,s87,v131
M 26,a[32:35],50
R 74,2,10240
v_mul_f32 v134,v17,v134
M 26,a[40:43],54
L 3
D 0,56,2048
M 26,a[48:51],58
R 78,2,11264
v_fma_f32 v18,-v98,v106,v18
M 26,a[56:59],62
D 0,56,3072
M 30,a[52:55],58
s_add_u32 m0,s57,16384
R 82,2,12288
v_fma_f32 v19,-v98,v107,v19
M 30,a[60:63],62
L 3
v_rsq_f32 v132,v131
M 30,a[68:71],66
s_add_u32 s56,s55,16384
R 86,2,13312
v_fma_f32 v131,v15,v131,v16
M 30,a[76:79],70
s_add_u32 s55,s55,s60
v_mul_f32 v135,v132,v132
v_fma_f32 v131,v14,v135,v131
M 26,a[64:67],66
s_sub_u32 s57,s84,s57
R 90,2,14336
v_fma_f32 v134,v132,v131,v134
M 26,a[72:75],70
L 3
ds_write_b32 v12,v134 offset:2048
M 26,a[80:83],74
s_waitcnt vmcnt(8)
R 94,2,15360
v_fma_f32 v20,-v98,v108,v20
M 26,a[88:91],78
s_barrier
v_fma_f32 v21,-v98,v109,v21
)"
R"(v_mul_f32 v114,v18,v18
M 30,a[84:87],74
R 34,2,16384
v_mul_f32 v115,v19,v19
M 30,a[92:95],78
L 4
v_mul_f32 v116,v20,v20
v_mul_f32 v117,v21,v21
M 30,a[100:103],82
R 38,2,17408
v_fma_f32 v114,v100,v114,v99
M 30,a[108:111],86
D 0,56,0
M 26,a[96:99],82
R 42,2,18432
v_fma_f32 v115,v100,v115,v99
M 26,a[104:107],86
L 3
v_fma_f32 v116,v100,v116,v99
v_fma_f32 v117,v100,v117,v99
M 26,a[112:115],90
R 46,2,19456
v_mul_f32 v114,v18,v114
M 26,a[120:123],94
v_exp_f32 v114,v114
M 30,a[116:119],90
R 50,2,20480
v_mul_f32 v115,v19,v115
M 30,a[124:127],94
L 3
v_exp_f32 v115,v115
M 30,a[132:135],34
R 54,2,21504
v_mul_f32 v116,v20,v116
M 30,a[140:143],38
D 0,56,1024
M 26,a[128:131],34
R 58,2,22528
v_mul_f32 v117,v21,v117
M 26,a[136:139],38
L 3
v_exp_f32 v116,v116
M 26,a[144:147],42
R 62,2,23552
v_fma_f32 v114,v114,v101,v101
M 26,a[152:155],46
v_exp_f32 v117,v117
M 30,a[148:151],42
R 66,2,24576
v_fma_f32 v115,v115,v101,v101
M 30,a[156:159],46
L 3
D 0,56,2048
M 30,a[164:167],50
R 70,2,25600
v_fma_f32 v116,v116,v101,v101
M 30,a[172:175],54
v_rcp_f32 v114,v114
M 26,a[160:163],50
R 74,2,26624
v_fma_f32 v117,v117,v101,v101
M 26,a[168:171],54
L 3
v_rcp_f32 v115,v115
M 26,a[176:179],58
R 78,2,27648
v_mul_f32 v18,v18,v114
M 26,a[184:187],62
v_rcp_f32 v116,v116
M 30,a[180:183],58
R 82,2,28672
v_mul_f32 v19,v19,v115
M 30,a[188:191],62
L 3
D 0,56,3072
M 30,a[196:199],66
R 86,2,29696
v_mul_f32 v20,v20,v116
M 30,a[204:207],70
v_rcp_f32 v117,v117
M 26,a[192:195],66
R 90,2,30720
v_mul_f32 v21,v21,v117
M 26,a[200:203],70
L 3
s_nop 0
v_cvt_pk_bf16_f32 v122,v18,v19
M 26,a[208:211],74
s_waitcnt vmcnt(4)
R 94,2,31744
v_fma_f32 v22,-v98,v110,v22
M 26,a[216:219],78
s_barrier
ds_read_b32 v7,v6 offset:1024
R 102,13,2048
M 30,a[212:215],74
R 34,1,0
v_fma_f32 v23,-v98,v111,v23
M 30,a[220:223],78
L 5
buffer_store_dword v4,v3,s[36:39],s70 offen offset:0 sc0 sc1
M 30,a[228:231],82
s_mov_b32 s70,s69
R 38,1,1024
v_fma_f32 v24,-v98,v112,v24
M 30,a[236:239],86
s_mov_b32 s69,s68
v_fma_f32 v25,-v98,v113,v25
v_mul_f32 v118,v22,v22
M 26,a[224:227],82
s_add_u32 s68,s68,s63
R 42,1,2048
v_mul_f32 v119,v23,v23
M 26,a[232:235],86
L 5
v_mul_f32 v120,v24,v24
v_mul_f32 v121,v25,v25
v_fma_f32 v118,v100,v118,v99
v_fma_f32 v119,v100,v119,v99
v_fma_f32 v120,v100,v120,v99
v_fma_f32 v121,v100,v121,v99
v_mul_f32 v118,v22,v118
v_mul_f32 v119,v23,v119
v_mul_f32 v120,v24,v120
v_mul_f32 v121,v25,v121
v_exp_f32 v118,v118
v_exp_f32 v119,v119
v_exp_f32 v120,v120
v_exp_f32 v121,v121
M 26,a[240:243],90
L 4
R 46,1,3072
v_fma_f32 v118,v118,v101,v101
v_fma_f32 v119,v119,v101,v101
v_fma_f32 v120,v120,v101,v101
v_fma_f32 v121,v121,v101,v101
v_rcp_f32 v118,v118
v_rcp_f32 v119,v119
v_rcp_f32 v120,v120
v_rcp_f32 v121,v121
v_cmp_eq_u32 vcc,s53,v7
M 26,a[248:251],94
v_cvt_pk_bf16_f32 v123,v20,v21
v_mul_f32 v22,v22,v118
v_mul_f32 v23,v23,v119
v_mul_f32 v24,v24,v120
v_mul_f32 v25,v25,v121
v_cvt_pk_bf16_f32 v124,v22,v23
v_cvt_pk_bf16_f32 v125,v24,v25
M 30,a[244:247],90
L 2
s_cmp_eq_u32 vcc_lo,-1
R 50,1,4096
buffer_store_dwordx4 v[122:125],v0,s[36:39],s66 offen offset:0 sc0 sc1
M 30,a[252:255],94
s_cbranch_scc1 Lg3o_F0_%=
Lg3o_L0_%=:
v_mfma_f32_16x16x32_bf16 v[22:25],a[4:7],v[34:37],0
s_add_u32 m0,s59,3072
R 54,1,5120
v_add_f32_dpp v131,v130,v130 quad_perm:[1,0,3,2] row_mask:0xf bank_mask:0xf
M 22,a[12:15],38
buffer_load_dword v8,s[36:39],s58 offen offset:0 sc1 lds
v_mfma_f32_16x16x32_bf16 v[18:21],a[0:3],v[34:37],0
s_mov_b32 s66,s65
R 58,1,6144
v_add_f32_dpp v131,v131,v131 quad_perm:[2,3,0,1] row_mask:0xf bank_mask:0xf
M 18,a[8:11],38
L 3
v_fma_f32 v26,-v102,v106,v26
v_fma_f32 v27,-v102,v107,v27
M 18,a[16:19],42
s_add_u32 s65,s65,s62
R 62,1,7168
v_add_f32_dpp v134,v131,v131 row_half_mirror row_mask:0xf bank_mask:0xf
M 18,a[24:27],46
v_fma_f32 v28,-v102,v108,v28
v_fma_f32 v29,-v102,v109,v29
M 22,a[20:23],42
R 66,1,8192
v_mov_b32_dpp v131,v134 row_ror:8 row_mask:0xf bank_mask:0xf
M 22,a[28:31],46
L 3
v_fma_f32 v131,-v134,v134,v131
v_max_f32 v131,s87,v131
M 22,a[36:39],50
R 70,1,9216
v_mul_f32 v134,v17,v134
M 22,a[44:47],54
v_rsq_f32 v132,v131
M 18,a[32:35],50
R 74,1,10240
v_fma_f32 v131,v15,v131,v16
M 18,a[40:43],54
L 3
v_mul_f32 v135,v132,v132
v_fma_f32 v131,v14,v135,v131
M 18,a[48:51],58
R 78,1,11264
v_fma_f32 v134,v132,v131,v134
M 18,a[56:59],62
ds_write_b32 v12,v134 offset:0
M 22,a[52:55],58
R 82,1,12288
v_mul_f32 v114,v26,v26
M 22,a[60:63],62
L 4
v_mul_f32 v115,v27,v27
v_mul_f32 v116,v28,v28
M 22,a[68:71],66
R 86,1,13312
v_mul_f32 v117,v29,v29
M 22,a[76:79],70
v_fma_f32 v114,v104,v114,v103
v_fma_f32 v115,v104,v115,v103
M 18,a[64:67],66
R 90,1,14336
v_fma_f32 v116,v104,v116,v103
M 18,a[72:75],70
L 4
v_fma_f32 v117,v104,v117,v103
v_mul_f32 v114,v26,v114
M 18,a[80:83],74
s_waitcnt vmcnt(0)
R 94,1,15360
v_mul_f32 v115,v27,v115
M 18,a[88:91],78
s_barrier
ds_read_b32 v7,v6 offset:3072
v_mul_f32 v116,v28,v116
M 22,a[84:87],74
R 34,1,16384
v_mul_f32 v117,v29,v117
M 22,a[92:95],78
L 4
buffer_store_dword v4,v3,s[36:39],s70 offen offset:0 sc0 sc1
M 22,a[100:103],82
s_mov_b32 s70,0x7fff0000
R 38,1,17408
v_fma_f32 v30,-v102,v110,v30
M 22,a[108:111],86
v_exp_f32 v114,v114
M 18,a[96:99],82
R 42,1,18432
v_fma_f32 v114,v114,v105,v105
M 18,a[104:107],86
L 4
L 3
v_cmp_eq_u32 vcc,s53,v7
M 18,a[112:115],90
R 46,1,19456
v_fma_f32 v31,-v102,v111,v31
M 18,a[120:123],94
v_exp_f32 v115,v115
M 22,a[116:119],90
s_cmp_eq_u32 vcc_lo,-1
R 50,1,20480
v_fma_f32 v115,v115,v105,v105
s_cbranch_scc0 Lg3o_X0_%=
M 22,a[124:127],94
L 3
s_add_u32 m0,s57,0
s_add_u32 s56,s55,0
buffer_load_dword v130,v11,s[36:39],s73 offen offset:0 sc1
M 22,a[132:135],34
s_add_u32 s58,s58,s61
R 54,1,21504
v_fma_f32 v32,-v102,v112,v32
M 22,a[140:143],38
D 0,56,0
M 18,a[128:131],34
s_add_u32 s54,s54,s46
R 58,1,22528
v_fma_f32 v33,-v102,v113,v33
M 18,a[136:139],38
L 3
v_exp_f32 v116,v116
M 18,a[144:147],42
s_add_u32 s73,s73,s64
R 62,1,23552
v_fma_f32 v116,v116,v105,v105
M 18,a[152:155],46
D 0,56,1024
M 22,a[148:151],42
R 66,1,24576
v_mul_f32 v118,v30,v30
M 22,a[156:159],46
L 3
v_exp_f32 v117,v117
M 22,a[164:167],50
R 70,1,25600
v_fma_f32 v117,v117,v105,v105
M 22,a[172:175],54
D 0,56,2048
M 18,a[160:163],50
R 74,1,26624
v_mul_f32 v119,v31,v31
M 18,a[168:171],54
L 3
v_rcp_f32 v114,v114
M 18,a[176:179],58
R 78,1,27648
v_mul_f32 v26,v26,v114
M 18,a[184:187],62
D 0,56,3072
M 22,a[180:183],58
s_add_u32 m0,s59,0
R 82,1,28672
v_mul_f32 v120,v32,v32
M 22,a[188:191],62
L 3
buffer_load_dword v8,s[36:39],s58 offen offset:0 sc1 lds
M 22,a[196:199],66
s_add_u32 m0,s57,16384
R 86,1,29696
v_mul_f32 v121,v33,v33
M 22,a[204:207],70
s_add_u32 s56,s55,16384
v_rcp_f32 v115,v115
M 18,a[192:195],66
s_add_u32 s55,s55,s60
R 90,1,30720
v_mul_f32 v27,v27,v115
M 18,a[200:203],70
L 3
s_nop 0
D 0,56,0
M 18,a[208:211],74
s_waitcnt vmcnt(1)
R 94,1,31744
v_cvt_pk_bf16_f32 v122,v26,v27
M 18,a[216:219],78
s_barrier
ds_read_b32 v7,v6 offset:0
R 98,13,0
M 22,a[212:215],74
s_sub_u32 s57,s84,s57
R 34,2,0
v_fma_f32 v118,v104,v118,v103
M 22,a[220:223],78
L 5
buffer_store_dword v4,v3,s[36:39],s70 offen offset:0 sc0 sc1
M 22,a[228:231],82
s_mov_b32 s70,s69
R 38,2,1024
v_fma_f32 v119,v104,v119,v103
M 22,a[236:239],86
D 0,56,1024
M 18,a[224:227],82
s_mov_b32 s69,s68
R 42,2,2048
v_fma_f32 v120,v104,v120,v103
M 18,a[232:235],86
L 5
v_fma_f32 v121,v104,v121,v103
v_mul_f32 v118,v30,v118
v_mul_f32 v119,v31,v119
v_mul_f32 v120,v32,v120
v_mul_f32 v121,v33,v121
v_exp_f32 v118,v118
v_exp_f32 v119,v119
v_exp_f32 v120,v120
v_exp_f32 v121,v121
M 18,a[240:243],90
L 4
R 46,2,3072
v_rcp_f32 v116,v116
v_rcp_f32 v117,v117
v_fma_f32 v118,v118,v105,v105
v_fma_f32 v119,v119,v105,v105
v_fma_f32 v120,v120,v105,v105
v_fma_f32 v121,v121,v105,v105
v_rcp_f32 v118,v118
v_rcp_f32 v119,v119
v_rcp_f32 v120,v120
v_rcp_f32 v121,v121
v_cmp_eq_u32 vcc,s53,v7
M 18,a[248:251],94
s_add_u32 s68,s68,s63
v_mul_f32 v28,v28,v116
v_mul_f32 v29,v29,v117
v_cvt_pk_bf16_f32 v123,v28,v29
v_mul_f32 v30,v30,v118
v_mul_f32 v31,v31,v119
v_mul_f32 v32,v32,v120
v_mul_f32 v33,v33,v121
v_cvt_pk_bf16_f32 v124,v30,v31
v_cvt_pk_bf16_f32 v125,v32,v33
M 22,a[244:247],90
L 2
s_cmp_eq_u32 vcc_lo,-1
R 50,2,4096
buffer_store_dwordx4 v[122:125],v0,s[36:39],s67 offen offset:0 sc0 sc1
M 22,a[252:255],94
D 0,56,2048
D 0,56,3072
buffer_store_dword v4,v3,s[36:39],s71 offen offset:0 sc0 sc1
buffer_store_dword v4,v3,s[36:39],s71 offen offset:0 sc0 sc1
s_cbranch_scc1 Lg3o_F1_%=
s_branch Lg3o_L1_%=
Lg3o_X0_%=:
M 22,a[124:127],94
L 3
s_add_u32 m0,s59,0
v_fma_f32 v32,-v102,v112,v32
M 22,a[132:135],34
R 54,1,21504
v_fma_f32 v33,-v102,v113,v33
M 22,a[140:143],38
buffer_load_dword v8,s[36:39],s58 offen offset:0 sc1 lds
M 18,a[128:131],34
R 58,1,22528
v_mul_f32 v118,v30,v30
M 18,a[136:139],38
L 3
v_exp_f32 v116,v116
M 18,a[144:147],42
R 62,1,23552
v_fma_f32 v116,v116,v105,v105
M 18,a[152:155],46
v_exp_f32 v117,v117
M 22,a[148:151],42
R 66,1,24576
v_fma_f32 v117,v117,v105,v105
M 22,a[156:159],46
L 3
v_rcp_f32 v114,v114
v_mul_f32 v119,v31,v31
M 22,a[164:167],50
R 70,1,25600
v_mul_f32 v26,v26,v114
M 22,a[172:175],54
v_rcp_f32 v115,v115
M 18,a[160:163],50
R 74,1,26624
v_mul_f32 v27,v27,v115
M 18,a[168:171],54
L 3
v_rcp_f32 v116,v116
M 18,a[176:179],58
R 78,1,27648
v_mul_f32 v28,v28,v116
M 18,a[184:187],62
v_rcp_f32 v117,v117
M 22,a[180:183],58
R 82,1,28672
v_mul_f32 v29,v29,v117
M 22,a[188:191],62
L 3
v_cvt_pk_bf16_f32 v122,v26,v27
v_cvt_pk_bf16_f32 v123,v28,v29
M 22,a[196:199],66
R 86,1,29696
v_mul_f32 v120,v32,v32
M 22,a[204:207],70
v_mul_f32 v121,v33,v33
v_fma_f32 v118,v104,v118,v103
M 18,a[192:195],66
R 90,1,30720
v_fma_f32 v119,v104,v119,v103
M 18,a[200:203],70
L 3
s_nop 0
v_fma_f32 v120,v104,v120,v103
M 18,a[208:211],74
s_waitcnt vmcnt(0)
R 94,1,31744
v_fma_f32 v121,v104,v121,v103
M 18,a[216:219],78
s_barrier
buffer_store_dword v4,v3,s[36:39],s70 offen offset:0 sc0 sc1
M 22,a[212:215],74
s_mov_b32 s70,s69
ds_read_b32 v7,v6 offset:0
R 98,13,0
M 22,a[220:223],78
L 4
v_mul_f32 v118,v30,v118
v_mul_f32 v119,v31,v119
M 22,a[228:231],82
s_mov_b32 s69,s68
v_mul_f32 v120,v32,v120
v_mul_f32 v121,v33,v121
M 22,a[236:239],86
s_add_u32 s68,s68,s63
v_exp_f32 v118,v118
M 18,a[224:227],82
v_exp_f32 v119,v119
M 18,a[232:235],86
L 2
v_exp_f32 v120,v120
v_exp_f32 v121,v121
M 18,a[240:243],90
L 1
v_fma_f32 v118,v118,v105,v105
v_fma_f32 v119,v119,v105,v105
v_fma_f32 v120,v120,v105,v105
v_fma_f32 v121,v121,v105,v105
v_rcp_f32 v118,v118
v_rcp_f32 v119,v119
)"
R"(v_rcp_f32 v120,v120
v_rcp_f32 v121,v121
M 18,a[248:251],94
v_mul_f32 v30,v30,v118
v_mul_f32 v31,v31,v119
v_mul_f32 v32,v32,v120
v_mul_f32 v33,v33,v121
v_cvt_pk_bf16_f32 v124,v30,v31
v_cvt_pk_bf16_f32 v125,v32,v33
M 22,a[244:247],90
buffer_store_dwordx4 v[122:125],v0,s[36:39],s67 offen offset:0 sc0 sc1
v_cmp_eq_u32 vcc,s53,v7
M 22,a[252:255],94
s_mov_b32 s80,1
s_cmp_eq_u32 vcc_lo,-1
s_cbranch_scc0 Lg3o_xn0_%=
buffer_load_dword v130,v11,s[36:39],s73 offen offset:0 sc1
L 0
v_fma_f32 v18,-v98,v106,v18
v_fma_f32 v19,-v98,v107,v19
v_fma_f32 v20,-v98,v108,v20
v_fma_f32 v21,-v98,v109,v21
v_mul_f32 v114,v18,v18
v_mul_f32 v115,v19,v19
s_add_u32 s73,s73,s64
s_add_u32 m0,s57,0
s_add_u32 s56,s55,0
D 0,56,0
v_mul_f32 v116,v20,v20
v_mul_f32 v117,v21,v21
v_fma_f32 v114,v100,v114,v99
v_fma_f32 v115,v100,v115,v99
v_fma_f32 v116,v100,v116,v99
v_fma_f32 v117,v100,v117,v99
D 0,56,1024
v_mul_f32 v114,v18,v114
v_mul_f32 v115,v19,v115
v_mul_f32 v116,v20,v116
v_mul_f32 v117,v21,v117
v_exp_f32 v114,v114
v_exp_f32 v115,v115
D 0,56,2048
v_exp_f32 v116,v116
v_exp_f32 v117,v117
v_fma_f32 v114,v114,v101,v101
v_fma_f32 v115,v115,v101,v101
v_fma_f32 v116,v116,v101,v101
v_fma_f32 v117,v117,v101,v101
D 0,56,3072
v_rcp_f32 v114,v114
v_rcp_f32 v115,v115
v_rcp_f32 v116,v116
v_rcp_f32 v117,v117
v_mul_f32 v18,v18,v114
v_mul_f32 v19,v19,v115
s_add_u32 s58,s58,s61
s_add_u32 m0,s57,16384
s_add_u32 s56,s55,16384
D 0,56,0
v_mul_f32 v20,v20,v116
v_mul_f32 v21,v21,v117
v_cvt_pk_bf16_f32 v122,v18,v19
v_cvt_pk_bf16_f32 v123,v20,v21
v_fma_f32 v22,-v98,v110,v22
v_fma_f32 v23,-v98,v111,v23
D 0,56,1024
v_fma_f32 v24,-v98,v112,v24
v_fma_f32 v25,-v98,v113,v25
v_mul_f32 v118,v22,v22
v_mul_f32 v119,v23,v23
v_mul_f32 v120,v24,v24
v_mul_f32 v121,v25,v25
D 0,56,2048
v_fma_f32 v118,v100,v118,v99
v_fma_f32 v119,v100,v119,v99
v_fma_f32 v120,v100,v120,v99
v_fma_f32 v121,v100,v121,v99
v_mul_f32 v118,v22,v118
v_mul_f32 v119,v23,v119
D 0,56,3072
v_mul_f32 v120,v24,v120
v_mul_f32 v121,v25,v121
v_exp_f32 v118,v118
v_exp_f32 v119,v119
v_exp_f32 v120,v120
v_exp_f32 v121,v121
s_add_u32 s55,s55,s60
s_sub_u32 s57,s84,s57
s_add_u32 s54,s54,s46
v_fma_f32 v118,v118,v101,v101
v_fma_f32 v119,v119,v101,v101
v_fma_f32 v120,v120,v101,v101
v_fma_f32 v121,v121,v101,v101
v_rcp_f32 v118,v118
v_rcp_f32 v119,v119
v_rcp_f32 v120,v120
v_rcp_f32 v121,v121
v_mul_f32 v22,v22,v118
v_mul_f32 v23,v23,v119
v_mul_f32 v24,v24,v120
v_mul_f32 v25,v25,v121
v_cvt_pk_bf16_f32 v124,v22,v23
v_cvt_pk_bf16_f32 v125,v24,v25
buffer_store_dwordx4 v[122:125],v0,s[36:39],s66 offen offset:0 sc0 sc1
s_mov_b32 s72,s69
s_mov_b32 s69,0x7fff0000
s_mov_b32 s66,0x7fff0000
s_mov_b32 s76,0x7fff0000
s_branch Lg3o_gox_%=
Lg3o_xn0_%=:
s_cmp_ge_u32 s54,256
s_cbranch_scc1 Lg3o_fin0_%=
s_add_u32 m0,s59,4096
s_nop 0
buffer_load_dword v8,s[36:39],s58 offen offset:0 sc1 lds
buffer_store_dword v4,v3,s[36:39],s71 offen offset:0 sc0 sc1
s_add_u32 m0,s59,5120
s_nop 0
buffer_load_dword v8,s[36:39],s58 offen offset:0 sc1 lds
buffer_store_dword v4,v3,s[36:39],s71 offen offset:0 sc0 sc1
s_mov_b32 s83,0
L 0
v_fma_f32 v18,-v98,v106,v18
v_fma_f32 v19,-v98,v107,v19
v_fma_f32 v20,-v98,v108,v20
v_fma_f32 v21,-v98,v109,v21
v_mul_f32 v114,v18,v18
v_mul_f32 v115,v19,v19
v_mul_f32 v116,v20,v20
v_mul_f32 v117,v21,v21
v_fma_f32 v114,v100,v114,v99
v_fma_f32 v115,v100,v115,v99
v_fma_f32 v116,v100,v116,v99
v_fma_f32 v117,v100,v117,v99
v_mul_f32 v114,v18,v114
v_mul_f32 v115,v19,v115
v_mul_f32 v116,v20,v116
v_mul_f32 v117,v21,v117
v_exp_f32 v114,v114
v_exp_f32 v115,v115
v_exp_f32 v116,v116
v_exp_f32 v117,v117
v_fma_f32 v114,v114,v101,v101
v_fma_f32 v115,v115,v101,v101
v_fma_f32 v116,v116,v101,v101
v_fma_f32 v117,v117,v101,v101
v_rcp_f32 v114,v114
v_rcp_f32 v115,v115
v_rcp_f32 v116,v116
v_rcp_f32 v117,v117
v_mul_f32 v18,v18,v114
v_mul_f32 v19,v19,v115
v_mul_f32 v20,v20,v116
v_mul_f32 v21,v21,v117
v_cvt_pk_bf16_f32 v122,v18,v19
v_cvt_pk_bf16_f32 v123,v20,v21
v_fma_f32 v22,-v98,v110,v22
v_fma_f32 v23,-v98,v111,v23
v_fma_f32 v24,-v98,v112,v24
v_fma_f32 v25,-v98,v113,v25
v_mul_f32 v118,v22,v22
v_mul_f32 v119,v23,v23
v_mul_f32 v120,v24,v24
v_mul_f32 v121,v25,v25
v_fma_f32 v118,v100,v118,v99
v_fma_f32 v119,v100,v119,v99
v_fma_f32 v120,v100,v120,v99
v_fma_f32 v121,v100,v121,v99
v_mul_f32 v118,v22,v118
v_mul_f32 v119,v23,v119
v_mul_f32 v120,v24,v120
v_mul_f32 v121,v25,v121
v_exp_f32 v118,v118
v_exp_f32 v119,v119
v_exp_f32 v120,v120
v_exp_f32 v121,v121
v_fma_f32 v118,v118,v101,v101
v_fma_f32 v119,v119,v101,v101
v_fma_f32 v120,v120,v101,v101
v_fma_f32 v121,v121,v101,v101
v_rcp_f32 v118,v118
v_rcp_f32 v119,v119
v_rcp_f32 v120,v120
v_rcp_f32 v121,v121
v_mul_f32 v22,v22,v118
v_mul_f32 v23,v23,v119
v_mul_f32 v24,v24,v120
v_mul_f32 v25,v25,v121
v_cvt_pk_bf16_f32 v124,v22,v23
v_cvt_pk_bf16_f32 v125,v24,v25
buffer_store_dwordx4 v[122:125],v0,s[36:39],s66 offen offset:0 sc0 sc1
s_mov_b32 s72,s69
s_mov_b32 s69,0x7fff0000
s_mov_b32 s66,0x7fff0000
s_mov_b32 s76,0x7fff0000
s_branch Lg3o_poll_%=
Lg3o_fin0_%=:
L 0
v_fma_f32 v18,-v98,v106,v18
v_fma_f32 v19,-v98,v107,v19
v_fma_f32 v20,-v98,v108,v20
v_fma_f32 v21,-v98,v109,v21
v_mul_f32 v114,v18,v18
v_mul_f32 v115,v19,v19
v_mul_f32 v116,v20,v20
v_mul_f32 v117,v21,v21
v_fma_f32 v114,v100,v114,v99
v_fma_f32 v115,v100,v115,v99
v_fma_f32 v116,v100,v116,v99
v_fma_f32 v117,v100,v117,v99
v_mul_f32 v114,v18,v114
v_mul_f32 v115,v19,v115
v_mul_f32 v116,v20,v116
v_mul_f32 v117,v21,v117
v_exp_f32 v114,v114
v_exp_f32 v115,v115
v_exp_f32 v116,v116
v_exp_f32 v117,v117
v_fma_f32 v114,v114,v101,v101
v_fma_f32 v115,v115,v101,v101
v_fma_f32 v116,v116,v101,v101
v_fma_f32 v117,v117,v101,v101
v_rcp_f32 v114,v114
v_rcp_f32 v115,v115
v_rcp_f32 v116,v116
v_rcp_f32 v117,v117
v_mul_f32 v18,v18,v114
v_mul_f32 v19,v19,v115
v_mul_f32 v20,v20,v116
v_mul_f32 v21,v21,v117
v_cvt_pk_bf16_f32 v122,v18,v19
v_cvt_pk_bf16_f32 v123,v20,v21
v_fma_f32 v22,-v98,v110,v22
v_fma_f32 v23,-v98,v111,v23
v_fma_f32 v24,-v98,v112,v24
v_fma_f32 v25,-v98,v113,v25
v_mul_f32 v118,v22,v22
v_mul_f32 v119,v23,v23
v_mul_f32 v120,v24,v24
v_mul_f32 v121,v25,v25
v_fma_f32 v118,v100,v118,v99
v_fma_f32 v119,v100,v119,v99
v_fma_f32 v120,v100,v120,v99
v_fma_f32 v121,v100,v121,v99
v_mul_f32 v118,v22,v118
v_mul_f32 v119,v23,v119
v_mul_f32 v120,v24,v120
v_mul_f32 v121,v25,v121
v_exp_f32 v118,v118
v_exp_f32 v119,v119
v_exp_f32 v120,v120
v_exp_f32 v121,v121
v_fma_f32 v118,v118,v101,v101
v_fma_f32 v119,v119,v101,v101
v_fma_f32 v120,v120,v101,v101
v_fma_f32 v121,v121,v101,v101
v_rcp_f32 v118,v118
v_rcp_f32 v119,v119
v_rcp_f32 v120,v120
v_rcp_f32 v121,v121
v_mul_f32 v22,v22,v118
v_mul_f32 v23,v23,v119
v_mul_f32 v24,v24,v120
v_mul_f32 v25,v25,v121
v_cvt_pk_bf16_f32 v124,v22,v23
v_cvt_pk_bf16_f32 v125,v24,v25
buffer_store_dwordx4 v[122:125],v0,s[36:39],s66 offen offset:0 sc0 sc1
s_mov_b32 s72,s69
s_mov_b32 s69,0x7fff0000
s_mov_b32 s66,0x7fff0000
s_mov_b32 s76,0x7fff0000
s_branch Lg3o_done_%=
Lg3o_L1_%=:
v_mfma_f32_16x16x32_bf16 v[30:33],a[4:7],v[34:37],0
s_add_u32 m0,s59,3072
R 54,2,5120
v_add_f32_dpp v131,v130,v130 quad_perm:[1,0,3,2] row_mask:0xf bank_mask:0xf
M 30,a[12:15],38
buffer_load_dword v8,s[36:39],s58 offen offset:0 sc1 lds
v_mfma_f32_16x16x32_bf16 v[26:29],a[0:3],v[34:37],0
s_mov_b32 s67,s65
R 58,2,6144
v_add_f32_dpp v131,v131,v131 quad_perm:[2,3,0,1] row_mask:0xf bank_mask:0xf
M 26,a[8:11],38
L 3
v_fma_f32 v18,-v98,v106,v18
v_fma_f32 v19,-v98,v107,v19
M 26,a[16:19],42
s_add_u32 s65,s65,s62
R 62,2,7168
v_add_f32_dpp v134,v131,v131 row_half_mirror row_mask:0xf bank_mask:0xf
M 26,a[24:27],46
v_fma_f32 v20,-v98,v108,v20
v_fma_f32 v21,-v98,v109,v21
M 30,a[20:23],42
R 66,2,8192
v_mov_b32_dpp v131,v134 row_ror:8 row_mask:0xf bank_mask:0xf
M 30,a[28:31],46
L 3
v_fma_f32 v131,-v134,v134,v131
v_max_f32 v131,s87,v131
M 30,a[36:39],50
R 70,2,9216
v_mul_f32 v134,v17,v134
M 30,a[44:47],54
v_rsq_f32 v132,v131
M 26,a[32:35],50
R 74,2,10240
v_fma_f32 v131,v15,v131,v16
M 26,a[40:43],54
L 3
v_mul_f32 v135,v132,v132
v_fma_f32 v131,v14,v135,v131
M 26,a[48:51],58
R 78,2,11264
v_fma_f32 v134,v132,v131,v134
M 26,a[56:59],62
ds_write_b32 v12,v134 offset:2048
M 30,a[52:55],58
R 82,2,12288
v_mul_f32 v114,v18,v18
M 30,a[60:63],62
L 4
v_mul_f32 v115,v19,v19
v_mul_f32 v116,v20,v20
M 30,a[68:71],66
R 86,2,13312
v_mul_f32 v117,v21,v21
M 30,a[76:79],70
v_fma_f32 v114,v100,v114,v99
v_fma_f32 v115,v100,v115,v99
M 26,a[64:67],66
R 90,2,14336
v_fma_f32 v116,v100,v116,v99
M 26,a[72:75],70
L 4
v_fma_f32 v117,v100,v117,v99
v_mul_f32 v114,v18,v114
M 26,a[80:83],74
s_waitcnt vmcnt(0)
R 94,2,15360
v_mul_f32 v115,v19,v115
M 26,a[88:91],78
s_barrier
ds_read_b32 v7,v6 offset:3072
v_mul_f32 v116,v20,v116
M 30,a[84:87],74
R 34,2,16384
v_mul_f32 v117,v21,v117
M 30,a[92:95],78
L 4
buffer_store_dword v4,v3,s[36:39],s70 offen offset:0 sc0 sc1
M 30,a[100:103],82
s_mov_b32 s70,0x7fff0000
R 38,2,17408
v_fma_f32 v22,-v98,v110,v22
M 30,a[108:111],86
v_exp_f32 v114,v114
M 26,a[96:99],82
R 42,2,18432
v_fma_f32 v114,v114,v101,v101
M 26,a[104:107],86
L 4
L 3
v_cmp_eq_u32 vcc,s53,v7
M 26,a[112:115],90
R 46,2,19456
v_fma_f32 v23,-v98,v111,v23
M 26,a[120:123],94
v_exp_f32 v115,v115
M 30,a[116:119],90
s_cmp_eq_u32 vcc_lo,-1
R 50,2,20480
v_fma_f32 v115,v115,v101,v101
s_cbranch_scc0 Lg3o_X1_%=
M 30,a[124:127],94
L 3
s_add_u32 m0,s57,0
s_add_u32 s56,s55,0
buffer_load_dword v130,v11,s[36:39],s73 offen offset:0 sc1
M 30,a[132:135],34
s_add_u32 s58,s58,s61
R 54,2,21504
v_fma_f32 v24,-v98,v112,v24
M 30,a[140:143],38
D 0,56,0
M 26,a[128:131],34
s_add_u32 s54,s54,s46
R 58,2,22528
v_fma_f32 v25,-v98,v113,v25
M 26,a[136:139],38
L 3
v_exp_f32 v116,v116
M 26,a[144:147],42
s_add_u32 s73,s73,s64
R 62,2,23552
v_fma_f32 v116,v116,v101,v101
M 26,a[152:155],46
D 0,56,1024
M 30,a[148:151],42
R 66,2,24576
v_mul_f32 v118,v22,v22
M 30,a[156:159],46
L 3
v_exp_f32 v117,v117
M 30,a[164:167],50
R 70,2,25600
v_fma_f32 v117,v117,v101,v101
M 30,a[172:175],54
D 0,56,2048
M 26,a[160:163],50
R 74,2,26624
v_mul_f32 v119,v23,v23
M 26,a[168:171],54
L 3
v_rcp_f32 v114,v114
M 26,a[176:179],58
R 78,2,27648
v_mul_f32 v18,v18,v114
M 26,a[184:187],62
D 0,56,3072
M 30,a[180:183],58
s_add_u32 m0,s59,1024
R 82,2,28672
v_mul_f32 v120,v24,v24
M 30,a[188:191],62
L 3
buffer_load_dword v8,s[36:39],s58 offen offset:0 sc1 lds
M 30,a[196:199],66
s_add_u32 m0,s57,16384
R 86,2,29696
v_mul_f32 v121,v25,v25
M 30,a[204:207],70
s_add_u32 s56,s55,16384
v_rcp_f32 v115,v115
M 26,a[192:195],66
s_add_u32 s55,s55,s60
R 90,2,30720
v_mul_f32 v19,v19,v115
M 26,a[200:203],70
L 3
s_nop 0
D 0,56,0
M 26,a[208:211],74
s_waitcnt vmcnt(1)
R 94,2,31744
v_cvt_pk_bf16_f32 v122,v18,v19
M 26,a[216:219],78
s_barrier
ds_read_b32 v7,v6 offset:1024
R 102,13,2048
M 30,a[212:215],74
s_sub_u32 s57,s84,s57
R 34,1,0
v_fma_f32 v118,v100,v118,v99
M 30,a[220:223],78
L 5
buffer_store_dword v4,v3,s[36:39],s70 offen offset:0 sc0 sc1
M 30,a[228:231],82
s_mov_b32 s70,s69
R 38,1,1024
v_fma_f32 v119,v100,v119,v99
M 30,a[236:239],86
D 0,56,1024
M 26,a[224:227],82
s_mov_b32 s69,s68
R 42,1,2048
v_fma_f32 v120,v100,v120,v99
M 26,a[232:235],86
L 5
v_fma_f32 v121,v100,v121,v99
v_mul_f32 v118,v22,v118
v_mul_f32 v119,v23,v119
v_mul_f32 v120,v24,v120
v_mul_f32 v121,v25,v121
v_exp_f32 v118,v118
v_exp_f32 v119,v119
v_exp_f32 v120,v120
v_exp_f32 v121,v121
M 26,a[240:243],90
L 4
R 46,1,3072
v_rcp_f32 v116,v116
v_rcp_f32 v117,v117
v_fma_f32 v118,v118,v101,v101
v_fma_f32 v119,v119,v101,v101
v_fma_f32 v120,v120,v101,v101
v_fma_f32 v121,v121,v101,v101
v_rcp_f32 v118,v118
v_rcp_f32 v119,v119
v_rcp_f32 v120,v120
v_rcp_f32 v121,v121
v_cmp_eq_u32 vcc,s53,v7
M 26,a[248:251],94
s_add_u32 s68,s68,s63
v_mul_f32 v20,v20,v116
v_mul_f32 v21,v21,v117
v_cvt_pk_bf16_f32 v123,v20,v21
v_mul_f32 v22,v22,v118
v_mul_f32 v23,v23,v119
)"
R"(v_mul_f32 v24,v24,v120
v_mul_f32 v25,v25,v121
v_cvt_pk_bf16_f32 v124,v22,v23
v_cvt_pk_bf16_f32 v125,v24,v25
M 30,a[244:247],90
L 2
s_cmp_eq_u32 vcc_lo,-1
R 50,1,4096
buffer_store_dwordx4 v[122:125],v0,s[36:39],s66 offen offset:0 sc0 sc1
M 30,a[252:255],94
D 0,56,2048
D 0,56,3072
buffer_store_dword v4,v3,s[36:39],s71 offen offset:0 sc0 sc1
buffer_store_dword v4,v3,s[36:39],s71 offen offset:0 sc0 sc1
s_cbranch_scc1 Lg3o_F0_%=
s_branch Lg3o_L0_%=
Lg3o_X1_%=:
M 30,a[124:127],94
L 3
s_add_u32 m0,s59,1024
v_fma_f32 v24,-v98,v112,v24
M 30,a[132:135],34
R 54,2,21504
v_fma_f32 v25,-v98,v113,v25
M 30,a[140:143],38
buffer_load_dword v8,s[36:39],s58 offen offset:0 sc1 lds
M 26,a[128:131],34
R 58,2,22528
v_mul_f32 v118,v22,v22
M 26,a[136:139],38
L 3
v_exp_f32 v116,v116
M 26,a[144:147],42
R 62,2,23552
v_fma_f32 v116,v116,v101,v101
M 26,a[152:155],46
v_exp_f32 v117,v117
M 30,a[148:151],42
R 66,2,24576
v_fma_f32 v117,v117,v101,v101
M 30,a[156:159],46
L 3
v_rcp_f32 v114,v114
v_mul_f32 v119,v23,v23
M 30,a[164:167],50
R 70,2,25600
v_mul_f32 v18,v18,v114
M 30,a[172:175],54
v_rcp_f32 v115,v115
M 26,a[160:163],50
R 74,2,26624
v_mul_f32 v19,v19,v115
M 26,a[168:171],54
L 3
v_rcp_f32 v116,v116
M 26,a[176:179],58
R 78,2,27648
v_mul_f32 v20,v20,v116
M 26,a[184:187],62
v_rcp_f32 v117,v117
M 30,a[180:183],58
R 82,2,28672
v_mul_f32 v21,v21,v117
M 30,a[188:191],62
L 3
v_cvt_pk_bf16_f32 v122,v18,v19
v_cvt_pk_bf16_f32 v123,v20,v21
M 30,a[196:199],66
R 86,2,29696
v_mul_f32 v120,v24,v24
M 30,a[204:207],70
v_mul_f32 v121,v25,v25
v_fma_f32 v118,v100,v118,v99
M 26,a[192:195],66
R 90,2,30720
v_fma_f32 v119,v100,v119,v99
M 26,a[200:203],70
L 3
s_nop 0
v_fma_f32 v120,v100,v120,v99
M 26,a[208:211],74
s_waitcnt vmcnt(0)
R 94,2,31744
v_fma_f32 v121,v100,v121,v99
M 26,a[216:219],78
s_barrier
buffer_store_dword v4,v3,s[36:39],s70 offen offset:0 sc0 sc1
M 30,a[212:215],74
s_mov_b32 s70,s69
ds_read_b32 v7,v6 offset:1024
R 102,13,2048
M 30,a[220:223],78
L 4
v_mul_f32 v118,v22,v118
v_mul_f32 v119,v23,v119
M 30,a[228:231],82
s_mov_b32 s69,s68
v_mul_f32 v120,v24,v120
v_mul_f32 v121,v25,v121
M 30,a[236:239],86
s_add_u32 s68,s68,s63
v_exp_f32 v118,v118
M 26,a[224:227],82
v_exp_f32 v119,v119
M 26,a[232:235],86
L 2
v_exp_f32 v120,v120
v_exp_f32 v121,v121
M 26,a[240:243],90
L 1
v_fma_f32 v118,v118,v101,v101
v_fma_f32 v119,v119,v101,v101
v_fma_f32 v120,v120,v101,v101
v_fma_f32 v121,v121,v101,v101
v_rcp_f32 v118,v118
v_rcp_f32 v119,v119
v_rcp_f32 v120,v120
v_rcp_f32 v121,v121
M 26,a[248:251],94
v_mul_f32 v22,v22,v118
v_mul_f32 v23,v23,v119
v_mul_f32 v24,v24,v120
v_mul_f32 v25,v25,v121
v_cvt_pk_bf16_f32 v124,v22,v23
v_cvt_pk_bf16_f32 v125,v24,v25
M 30,a[244:247],90
buffer_store_dwordx4 v[122:125],v0,s[36:39],s66 offen offset:0 sc0 sc1
v_cmp_eq_u32 vcc,s53,v7
M 30,a[252:255],94
s_mov_b32 s80,0
s_cmp_eq_u32 vcc_lo,-1
s_cbranch_scc0 Lg3o_xn1_%=
buffer_load_dword v130,v11,s[36:39],s73 offen offset:0 sc1
L 0
v_fma_f32 v26,-v102,v106,v26
v_fma_f32 v27,-v102,v107,v27
v_fma_f32 v28,-v102,v108,v28
v_fma_f32 v29,-v102,v109,v29
v_mul_f32 v114,v26,v26
v_mul_f32 v115,v27,v27
s_add_u32 s73,s73,s64
s_add_u32 m0,s57,0
s_add_u32 s56,s55,0
D 0,56,0
v_mul_f32 v116,v28,v28
v_mul_f32 v117,v29,v29
v_fma_f32 v114,v104,v114,v103
v_fma_f32 v115,v104,v115,v103
v_fma_f32 v116,v104,v116,v103
v_fma_f32 v117,v104,v117,v103
D 0,56,1024
v_mul_f32 v114,v26,v114
v_mul_f32 v115,v27,v115
v_mul_f32 v116,v28,v116
v_mul_f32 v117,v29,v117
v_exp_f32 v114,v114
v_exp_f32 v115,v115
D 0,56,2048
v_exp_f32 v116,v116
v_exp_f32 v117,v117
v_fma_f32 v114,v114,v105,v105
v_fma_f32 v115,v115,v105,v105
v_fma_f32 v116,v116,v105,v105
v_fma_f32 v117,v117,v105,v105
D 0,56,3072
v_rcp_f32 v114,v114
v_rcp_f32 v115,v115
v_rcp_f32 v116,v116
v_rcp_f32 v117,v117
v_mul_f32 v26,v26,v114
v_mul_f32 v27,v27,v115
s_add_u32 s58,s58,s61
s_add_u32 m0,s57,16384
s_add_u32 s56,s55,16384
D 0,56,0
v_mul_f32 v28,v28,v116
v_mul_f32 v29,v29,v117
v_cvt_pk_bf16_f32 v122,v26,v27
v_cvt_pk_bf16_f32 v123,v28,v29
v_fma_f32 v30,-v102,v110,v30
v_fma_f32 v31,-v102,v111,v31
D 0,56,1024
v_fma_f32 v32,-v102,v112,v32
v_fma_f32 v33,-v102,v113,v33
v_mul_f32 v118,v30,v30
v_mul_f32 v119,v31,v31
v_mul_f32 v120,v32,v32
v_mul_f32 v121,v33,v33
D 0,56,2048
v_fma_f32 v118,v104,v118,v103
v_fma_f32 v119,v104,v119,v103
v_fma_f32 v120,v104,v120,v103
v_fma_f32 v121,v104,v121,v103
v_mul_f32 v118,v30,v118
v_mul_f32 v119,v31,v119
D 0,56,3072
v_mul_f32 v120,v32,v120
v_mul_f32 v121,v33,v121
v_exp_f32 v118,v118
v_exp_f32 v119,v119
v_exp_f32 v120,v120
v_exp_f32 v121,v121
s_add_u32 s55,s55,s60
s_sub_u32 s57,s84,s57
s_add_u32 s54,s54,s46
v_fma_f32 v118,v118,v105,v105
v_fma_f32 v119,v119,v105,v105
v_fma_f32 v120,v120,v105,v105
v_fma_f32 v121,v121,v105,v105
v_rcp_f32 v118,v118
v_rcp_f32 v119,v119
v_rcp_f32 v120,v120
v_rcp_f32 v121,v121
v_mul_f32 v30,v30,v118
v_mul_f32 v31,v31,v119
v_mul_f32 v32,v32,v120
v_mul_f32 v33,v33,v121
v_cvt_pk_bf16_f32 v124,v30,v31
v_cvt_pk_bf16_f32 v125,v32,v33
buffer_store_dwordx4 v[122:125],v0,s[36:39],s67 offen offset:0 sc0 sc1
s_mov_b32 s72,s69
s_mov_b32 s69,0x7fff0000
s_mov_b32 s67,0x7fff0000
s_mov_b32 s77,0x7fff0000
s_branch Lg3o_gox_%=
Lg3o_xn1_%=:
s_cmp_ge_u32 s54,256
s_cbranch_scc1 Lg3o_fin1_%=
s_add_u32 m0,s59,4096
s_nop 0
buffer_load_dword v8,s[36:39],s58 offen offset:0 sc1 lds
buffer_store_dword v4,v3,s[36:39],s71 offen offset:0 sc0 sc1
s_add_u32 m0,s59,5120
s_nop 0
buffer_load_dword v8,s[36:39],s58 offen offset:0 sc1 lds
buffer_store_dword v4,v3,s[36:39],s71 offen offset:0 sc0 sc1
s_mov_b32 s83,0
L 0
v_fma_f32 v26,-v102,v106,v26
v_fma_f32 v27,-v102,v107,v27
v_fma_f32 v28,-v102,v108,v28
v_fma_f32 v29,-v102,v109,v29
v_mul_f32 v114,v26,v26
v_mul_f32 v115,v27,v27
v_mul_f32 v116,v28,v28
v_mul_f32 v117,v29,v29
v_fma_f32 v114,v104,v114,v103
v_fma_f32 v115,v104,v115,v103
v_fma_f32 v116,v104,v116,v103
v_fma_f32 v117,v104,v117,v103
v_mul_f32 v114,v26,v114
v_mul_f32 v115,v27,v115
v_mul_f32 v116,v28,v116
v_mul_f32 v117,v29,v117
v_exp_f32 v114,v114
v_exp_f32 v115,v115
v_exp_f32 v116,v116
v_exp_f32 v117,v117
v_fma_f32 v114,v114,v105,v105
v_fma_f32 v115,v115,v105,v105
v_fma_f32 v116,v116,v105,v105
v_fma_f32 v117,v117,v105,v105
v_rcp_f32 v114,v114
v_rcp_f32 v115,v115
v_rcp_f32 v116,v116
v_rcp_f32 v117,v117
v_mul_f32 v26,v26,v114
v_mul_f32 v27,v27,v115
v_mul_f32 v28,v28,v116
v_mul_f32 v29,v29,v117
v_cvt_pk_bf16_f32 v122,v26,v27
v_cvt_pk_bf16_f32 v123,v28,v29
v_fma_f32 v30,-v102,v110,v30
v_fma_f32 v31,-v102,v111,v31
v_fma_f32 v32,-v102,v112,v32
v_fma_f32 v33,-v102,v113,v33
v_mul_f32 v118,v30,v30
v_mul_f32 v119,v31,v31
v_mul_f32 v120,v32,v32
v_mul_f32 v121,v33,v33
v_fma_f32 v118,v104,v118,v103
v_fma_f32 v119,v104,v119,v103
v_fma_f32 v120,v104,v120,v103
v_fma_f32 v121,v104,v121,v103
v_mul_f32 v118,v30,v118
v_mul_f32 v119,v31,v119
v_mul_f32 v120,v32,v120
v_mul_f32 v121,v33,v121
v_exp_f32 v118,v118
v_exp_f32 v119,v119
v_exp_f32 v120,v120
v_exp_f32 v121,v121
v_fma_f32 v118,v118,v105,v105
v_fma_f32 v119,v119,v105,v105
v_fma_f32 v120,v120,v105,v105
v_fma_f32 v121,v121,v105,v105
v_rcp_f32 v118,v118
v_rcp_f32 v119,v119
v_rcp_f32 v120,v120
v_rcp_f32 v121,v121
v_mul_f32 v30,v30,v118
v_mul_f32 v31,v31,v119
v_mul_f32 v32,v32,v120
v_mul_f32 v33,v33,v121
v_cvt_pk_bf16_f32 v124,v30,v31
v_cvt_pk_bf16_f32 v125,v32,v33
buffer_store_dwordx4 v[122:125],v0,s[36:39],s67 offen offset:0 sc0 sc1
s_mov_b32 s72,s69
s_mov_b32 s69,0x7fff0000
s_mov_b32 s67,0x7fff0000
s_mov_b32 s77,0x7fff0000
s_branch Lg3o_poll_%=
Lg3o_fin1_%=:
L 0
v_fma_f32 v26,-v102,v106,v26
v_fma_f32 v27,-v102,v107,v27
v_fma_f32 v28,-v102,v108,v28
v_fma_f32 v29,-v102,v109,v29
v_mul_f32 v114,v26,v26
v_mul_f32 v115,v27,v27
v_mul_f32 v116,v28,v28
v_mul_f32 v117,v29,v29
v_fma_f32 v114,v104,v114,v103
v_fma_f32 v115,v104,v115,v103
v_fma_f32 v116,v104,v116,v103
v_fma_f32 v117,v104,v117,v103
v_mul_f32 v114,v26,v114
v_mul_f32 v115,v27,v115
v_mul_f32 v116,v28,v116
v_mul_f32 v117,v29,v117
v_exp_f32 v114,v114
v_exp_f32 v115,v115
v_exp_f32 v116,v116
v_exp_f32 v117,v117
v_fma_f32 v114,v114,v105,v105
v_fma_f32 v115,v115,v105,v105
v_fma_f32 v116,v116,v105,v105
v_fma_f32 v117,v117,v105,v105
v_rcp_f32 v114,v114
v_rcp_f32 v115,v115
v_rcp_f32 v116,v116
v_rcp_f32 v117,v117
v_mul_f32 v26,v26,v114
v_mul_f32 v27,v27,v115
v_mul_f32 v28,v28,v116
v_mul_f32 v29,v29,v117
v_cvt_pk_bf16_f32 v122,v26,v27
v_cvt_pk_bf16_f32 v123,v28,v29
v_fma_f32 v30,-v102,v110,v30
v_fma_f32 v31,-v102,v111,v31
v_fma_f32 v32,-v102,v112,v32
v_fma_f32 v33,-v102,v113,v33
v_mul_f32 v118,v30,v30
v_mul_f32 v119,v31,v31
v_mul_f32 v120,v32,v32
v_mul_f32 v121,v33,v33
v_fma_f32 v118,v104,v118,v103
v_fma_f32 v119,v104,v119,v103
v_fma_f32 v120,v104,v120,v103
v_fma_f32 v121,v104,v121,v103
v_mul_f32 v118,v30,v118
v_mul_f32 v119,v31,v119
v_mul_f32 v120,v32,v120
v_mul_f32 v121,v33,v121
v_exp_f32 v118,v118
v_exp_f32 v119,v119
v_exp_f32 v120,v120
v_exp_f32 v121,v121
v_fma_f32 v118,v118,v105,v105
v_fma_f32 v119,v119,v105,v105
v_fma_f32 v120,v120,v105,v105
v_fma_f32 v121,v121,v105,v105
v_rcp_f32 v118,v118
v_rcp_f32 v119,v119
v_rcp_f32 v120,v120
v_rcp_f32 v121,v121
v_mul_f32 v30,v30,v118
v_mul_f32 v31,v31,v119
v_mul_f32 v32,v32,v120
v_mul_f32 v33,v33,v121
v_cvt_pk_bf16_f32 v124,v30,v31
v_cvt_pk_bf16_f32 v125,v32,v33
buffer_store_dwordx4 v[122:125],v0,s[36:39],s67 offen offset:0 sc0 sc1
s_mov_b32 s72,s69
s_mov_b32 s69,0x7fff0000
s_mov_b32 s67,0x7fff0000
s_mov_b32 s77,0x7fff0000
s_branch Lg3o_done_%=
Lg3o_done_%=:
s_waitcnt vmcnt(1)
s_barrier
buffer_store_dword v4,v3,s[36:39],s70 offen offset:0 sc0 sc1
s_waitcnt vmcnt(1)
s_barrier
buffer_store_dword v4,v3,s[36:39],s72 offen offset:0 sc0 sc1
s_waitcnt vmcnt(0) lgkmcnt(0)
s_barrier
s_endpgm
Lcode_g3o_e:
.purgem M
.purgem R
.purgem T
.purgem D
.purgem L
)"
  :: "s"(ws), "s"(sWave), "s"(sG0), "s"(sStep), "s"(sOutOff), "s"(sFoutOff), "s"(sAuxOff), "s"(sWw), "s"(sBuf), "s"(sExp), "v"(vL16), "v"(vFlag), "v"(vSnapOff), "v"(vSnapRd), "v"(vWd0), "v"(vWd1), "v"(vWr0), "v"(vWr1), "v"(vWr2), "v"(vOutS), "v"(vAux0), "v"(vAux1), "v"(vAux2), "v"(vAux3), "v"(vAn), "v"(vAe), "s"(w3), "s"(out) : "v255", "v0", "v1", "v2", "v3", "v4", "v5", "v6", "v7", "v8", "v9", "v10", "v11", "v12", "v13", "v14", "v15", "v16", "v17", "v18", "v19", "v20", "v21", "v22", "v23", "v24", "v25", "v26", "v27", "v28", "v29", "v30", "v31", "v32", "v33", "v34", "v35", "v36", "v37", "v38", "v39", "v40", "v41", "v42", "v43", "v44", "v45", "v46", "v47", "v48", "v49", "v50", "v51", "v52", "v53", "v54", "v55", "v56", "v57", "v58", "v59", "v60", "v61", "v62", "v63", "v64", "v65", "v66", "v67", "v68", "v69", "v70", "v71", "v72", "v73", "v74", "v75", "v76", "v77", "v78", "v79", "v80", "v81", "v82", "v83", "v84", "v85", "v86", "v87", "v88", "v89", "v90", "v91", "v92", "v93", "v94", "v95", "v96", "v97", "v98", "v99", "v100", "v101", "v102", "v103", "v104", "v105", "v106", "v107", "v108", "v109", "v110", "v111", "v112", "v113", "v114", "v115", "v116", "v117", "v118", "v119", "v120", "v121", "v122", "v123", "v124", "v125", "v126", "v127", "v128", "v129", "v130", "v131", "v132", "v133", "v134", "v135", "v136", "v137", "v138", "v139", "v140", "v141", "v142", "v143", "v144", "v145", "v146", "v147", "v148", "v149", "v150", "v151", "v152", "v153", "v154", "v155", "v156", "v157", "v158", "v159", "v160", "v161", "v162", "v163", "v164", "v165", "v166", "v167", "v168", "v169", "v170", "v171", "v172", "v173", "v174", "v175", "v176", "v177", "v178", "v179", "v180", "v181", "v182", "v183", "v184", "v185", "v186", "v187", "v188", "v189", "v190", "v191", "v192", "v193", "v194", "v195", "v196", "v197", "v198", "v199", "v200", "v201", "v202", "v203", "v204", "v205", "v206", "v207", "v208", "v209", "v210", "v211", "v212", "v213", "v214", "v215", "v216", "v217", "v218", "v219", "v220", "v221", "v222", "v223", "v224", "v225", "v226", "v227", "v228", "v229", "v230", "v231", "a0", "a1", "a2", "a3", "a4", "a5", "a6", "a7", "a8", "a9", "a10", "a11", "a12", "a13", "a14", "a15", "a16", "a17", "a18", "a19", "a20", "a21", "a22", "a23", "a24", "a25", "a26", "a27", "a28", "a29", "a30", "a31", "a32", "a33", "a34", "a35", "a36", "a37", "a38", "a39", "a40", "a41", "a42", "a43", "a44", "a45", "a46", "a47", "a48", "a49", "a50", "a51", "a52", "a53", "a54", "a55", "a56", "a57", "a58", "a59", "a60", "a61", "a62", "a63", "a64", "a65", "a66", "a67", "a68", "a69", "a70", "a71", "a72", "a73", "a74", "a75", "a76", "a77", "a78", "a79", "a80", "a81", "a82", "a83", "a84", "a85", "a86", "a87", "a88", "a89", "a90", "a91", "a92", "a93", "a94", "a95", "a96", "a97", "a98", "a99", "a100", "a101", "a102", "a103", "a104", "a105", "a106", "a107", "a108", "a109", "a110", "a111", "a112", "a113", "a114", "a115", "a116", "a117", "a118", "a119", "a120", "a121", "a122", "a123", "a124", "a125", "a126", "a127", "a128", "a129", "a130", "a131", "a132", "a133", "a134", "a135", "a136", "a137", "a138", "a139", "a140", "a141", "a142", "a143", "a144", "a145", "a146", "a147", "a148", "a149", "a150", "a151", "a152", "a153", "a154", "a155", "a156", "a157", "a158", "a159", "a160", "a161", "a162", "a163", "a164", "a165", "a166", "a167", "a168", "a169", "a170", "a171", "a172", "a173", "a174", "a175", "a176", "a177", "a178", "a179", "a180", "a181", "a182", "a183", "a184", "a185", "a186", "a187", "a188", "a189", "a190", "a191", "a192", "a193", "a194", "a195", "a196", "a197", "a198", "a199", "a200", "a201", "a202", "a203", "a204", "a205", "a206", "a207", "a208", "a209", "a210", "a211", "a212", "a213", "a214", "a215", "a216", "a217", "a218", "a219", "a220", "a221", "a222", "a223", "a224", "a225", "a226", "a227", "a228", "a229", "a230", "a231", "a232", "a233", "a234", "a235", "a236", "a237", "a238", "a239", "a240", "a241", "a242", "a243", "a244", "a245", "a246", "a247", "a248", "a249", "a250", "a251", "a252", "a253", "a254", "a255", "s36", "s37", "s38", "s39", "s40", "s41", "s42", "s43", "s44", "s45", "s46", "s47", "s48", "s49", "s50", "s51", "s52", "s53", "s54", "s55", "s56", "s57", "s58", "s59", "s60", "s61", "s62", "s63", "s64", "s65", "s66", "s67", "s68", "s69", "s70", "s71", "s72", "s73", "s74", "s75", "s76", "s77", "s78", "s79", "s80", "s81", "s82", "s83", "s84", "s85", "s86", "s87", "s88", "s89", "s90", "s91", "s92", "s93", "s94", "s95", "s96", "s97", "vcc", "scc", "memory"); }
    __builtin_unreachable();
  }
  if (kind < 3) {
    const unsigned sStep = 4;
    COMMON(kind == 1 ? 8 : 16, kind == 1 ? fj * 128 : fj < 8 ? 1024 + fj * 128 : (fj & 2) * 1024 + (fj - 8) * 128, 1024, 131072, 2048, 256, kind == 1 ? 0 : 32768);
    const int sr = l15 - ((wave & 2) ? -1 : 1), sr2 = l15 - ((wave & 2) ? -2 : 2), nx = (slice >> 1) & 1;
    const unsigned vOutS = (sr2 & 16) ? 1024 + lg * 64 + (sr2 & 3) * 16 : (lg * 16 + sr2) * 16, vSp = (sr2 & 16) ? OOB : vOutS;
    (void)vWr2;
    const unsigned vAn = kind == 1 ? HU + vL16 : (sr & 16) ? 1024 + lg * 64 + (sr & 3) * 16 : (lg * 16 + sr) * 16;
    const unsigned vAe = kind == 1 ? ((l15 >> 2) == (nx ? 0 : 3) ? (nx ? 0 : 2 * HU) + 1024 + lg * 64 + (l15 & 3) * 16 : OOB) : (sr & 16) ? OOB : vAn;
    const unsigned vAux0 = l15 * 16, vAux1 = 139264 + (l15 * 2 + (lg & 1)) * 16 + wave * 4 + (lg >> 1) * 512, vAux2 = 139264 + (l15 * 2 + (lg & 1)) * 16;
    const unsigned vAux3 = (wave == 0 && lg < 2) ? slice * 128 + l15 * 8 + lg * 4 : OOB;
    const unsigned sOutOff = kind == 1 ? OFF_HS - HU + (slice * 4 + wave) * 1280 : OFF_X1 + (slice * 4 + wave) * 1024, sFoutOff = (kind == 1 ? OFF_FH : OFF_FX1) + slice * 128;
    const unsigned sAuxOff = OFF_XP + (slice * 4 + wave) * 1024, sBuf = kind, sWw = 16 * wave * 2048 + slice * 256;
    const u16* w12 = kind == 1 ? w1 : w2;
    const unsigned sExp = __builtin_amdgcn_readfirstlane(ep);
    unsigned epOut;
    asm volatile(
R"(.macro M d,w,x
v_mfma_f32_16x16x32_bf16 v[\d:\d+3], \w, v[\x:\x+3], v[\d:\d+3]
.endm
.macro R d,a,o
ds_read_b128 v[\d:\d+3], v\a offset:\o
.endm
.macro T d,a,o
ds_read_b64_tr_b16 \d, v\a offset:\o
.endm
.macro D a,s,o
buffer_load_dwordx4 v\a, s[36:39], s\s offen offset:\o sc1 lds
.endm
.macro L c
s_waitcnt lgkmcnt(\c)
.endm
Lcode_g12_b:
s_cmp_eq_u32 %9,2
s_cbranch_scc1 Lg2_begin_%=
s_mov_b64 s[36:37],%1
s_mov_b32 s38,111243392
s_mov_b32 s39,0x00020000
s_mov_b32 s44,%2
s_mov_b32 s45,%3
s_mov_b32 s46,%4
s_mov_b32 s47,%5
s_mov_b32 s48,%6
s_mov_b32 s49,%7
s_mov_b32 s50,%8
s_mov_b32 s51,%9
s_mov_b32 s53,%10
v_mov_b32 v0,%11
v_mov_b32 v3,%12
v_mov_b32 v5,%13
v_mov_b32 v6,%14
v_mov_b32 v18,%15
v_mov_b32 v19,%16
v_mov_b32 v20,%17
v_mov_b32 v21,%18
v_mov_b32 v9,%19
v_mov_b32 v10,%20
v_mov_b32 v11,%21
v_mov_b32 v12,%22
v_mov_b32 v13,%23
v_mov_b32 v14,%24
v_mov_b32 v15,%25
v_mov_b32 v16,%26
s_mov_b64 s[40:41],%27
s_mov_b32 s42,2097152
s_mov_b32 s43,0x00020000
s_lshl_b32 s57,s44,11
s_add_u32 s55,s50,0
s_add_u32 m0,s57,0
s_nop 0
buffer_load_dwordx4 v18,s[40:43],s55 offen offset:0  lds
s_add_u32 m0,s57,1024
s_nop 0
buffer_load_dwordx4 v19,s[40:43],s55 offen offset:0  lds
s_add_u32 m0,s57,8064
s_nop 0
buffer_load_dwordx4 v18,s[40:43],s55 offen offset:128  lds
s_add_u32 m0,s57,9088
s_nop 0
buffer_load_dwordx4 v19,s[40:43],s55 offen offset:128  lds
s_add_u32 s55,s50,131072
s_add_u32 m0,s57,16384
s_nop 0
buffer_load_dwordx4 v18,s[40:43],s55 offen offset:0  lds
s_add_u32 m0,s57,17408
s_nop 0
buffer_load_dwordx4 v19,s[40:43],s55 offen offset:0  lds
s_add_u32 m0,s57,24448
s_nop 0
buffer_load_dwordx4 v18,s[40:43],s55 offen offset:128  lds
s_add_u32 m0,s57,25472
s_nop 0
buffer_load_dwordx4 v19,s[40:43],s55 offen offset:128  lds
s_add_u32 s55,s50,262144
s_add_u32 m0,s57,32768
s_nop 0
buffer_load_dwordx4 v18,s[40:43],s55 offen offset:0  lds
s_add_u32 m0,s57,33792
s_nop 0
buffer_load_dwordx4 v19,s[40:43],s55 offen offset:0  lds
s_add_u32 m0,s57,40832
s_nop 0
buffer_load_dwordx4 v18,s[40:43],s55 offen offset:128  lds
s_add_u32 m0,s57,41856
s_nop 0
buffer_load_dwordx4 v19,s[40:43],s55 offen offset:128  lds
s_add_u32 s55,s50,393216
s_add_u32 m0,s57,49152
s_nop 0
buffer_load_dwordx4 v18,s[40:43],s55 offen offset:0  lds
s_add_u32 m0,s57,50176
s_nop 0
buffer_load_dwordx4 v19,s[40:43],s55 offen offset:0  lds
s_add_u32 m0,s57,57216
s_nop 0
buffer_load_dwordx4 v18,s[40:43],s55 offen offset:128  lds
s_add_u32 m0,s57,58240
s_nop 0
buffer_load_dwordx4 v19,s[40:43],s55 offen offset:128  lds
s_add_u32 s55,s50,524288
s_add_u32 m0,s57,65536
s_nop 0
buffer_load_dwordx4 v18,s[40:43],s55 offen offset:0  lds
s_add_u32 m0,s57,66560
s_nop 0
buffer_load_dwordx4 v19,s[40:43],s55 offen offset:0  lds
s_add_u32 m0,s57,73600
s_nop 0
buffer_load_dwordx4 v18,s[40:43],s55 offen offset:128  lds
s_add_u32 m0,s57,74624
s_nop 0
buffer_load_dwordx4 v19,s[40:43],s55 offen offset:128  lds
s_add_u32 s55,s50,655360
s_add_u32 m0,s57,81920
s_nop 0
buffer_load_dwordx4 v18,s[40:43],s55 offen offset:0  lds
s_add_u32 m0,s57,82944
s_nop 0
buffer_load_dwordx4 v19,s[40:43],s55 offen offset:0  lds
s_add_u32 m0,s57,89984
s_nop 0
buffer_load_dwordx4 v18,s[40:43],s55 offen offset:128  lds
s_add_u32 m0,s57,91008
s_nop 0
buffer_load_dwordx4 v19,s[40:43],s55 offen offset:128  lds
s_waitcnt vmcnt(20) lgkmcnt(0)
s_barrier
s_add_u32 s55,s50,786432
s_add_u32 m0,s57,98304
s_nop 0
buffer_load_dwordx4 v18,s[40:43],s55 offen offset:0  lds
s_add_u32 m0,s57,99328
s_nop 0
buffer_load_dwordx4 v19,s[40:43],s55 offen offset:0  lds
s_add_u32 m0,s57,106368
s_nop 0
buffer_load_dwordx4 v18,s[40:43],s55 offen offset:128  lds
s_add_u32 m0,s57,107392
s_nop 0
buffer_load_dwordx4 v19,s[40:43],s55 offen offset:128  lds
v_add_u32 v26,0,v20
v_add_u32 v27,0,v21
T a[0:1],26,0
T a[2:3],26,512
T a[4:5],27,0
T a[6:7],27,512
T a[8:9],26,4096
T a[10:11],26,4608
T a[12:13],27,4096
T a[14:15],27,4608
s_waitcnt vmcnt(20) lgkmcnt(0)
s_barrier
s_add_u32 s55,s50,917504
s_add_u32 m0,s57,0
s_nop 0
buffer_load_dwordx4 v18,s[40:43],s55 offen offset:0  lds
s_add_u32 m0,s57,1024
s_nop 0
buffer_load_dwordx4 v19,s[40:43],s55 offen offset:0  lds
s_add_u32 m0,s57,8064
s_nop 0
buffer_load_dwordx4 v18,s[40:43],s55 offen offset:128  lds
s_add_u32 m0,s57,9088
s_nop 0
buffer_load_dwordx4 v19,s[40:43],s55 offen offset:128  lds
v_add_u32 v26,16384,v20
v_add_u32 v27,16384,v21
T a[16:17],26,0
T a[18:19],26,512
T a[20:21],27,0
T a[22:23],27,512
T a[24:25],26,4096
T a[26:27],26,4608
T a[28:29],27,4096
T a[30:31],27,4608
s_waitcnt vmcnt(20) lgkmcnt(0)
s_barrier
s_add_u32 s55,s50,1048576
s_add_u32 m0,s57,16384
s_nop 0
buffer_load_dwordx4 v18,s[40:43],s55 offen offset:0  lds
s_add_u32 m0,s57,17408
s_nop 0
buffer_load_dwordx4 v19,s[40:43],s55 offen offset:0  lds
s_add_u32 m0,s57,24448
s_nop 0
buffer_load_dwordx4 v18,s[40:43],s55 offen offset:128  lds
s_add_u32 m0,s57,25472
s_nop 0
buffer_load_dwordx4 v19,s[40:43],s55 offen offset:128  lds
v_add_u32 v26,32768,v20
v_add_u32 v27,32768,v21
T a[32:33],26,0
T a[34:35],26,512
T a[36:37],27,0
T a[38:39],27,512
T a[40:41],26,4096
T a[42:43],26,4608
T a[44:45],27,4096
T a[46:47],27,4608
s_waitcnt vmcnt(20) lgkmcnt(0)
s_barrier
s_add_u32 s55,s50,1179648
s_add_u32 m0,s57,32768
s_nop 0
buffer_load_dwordx4 v18,s[40:43],s55 offen offset:0  lds
s_add_u32 m0,s57,33792
s_nop 0
buffer_load_dwordx4 v19,s[40:43],s55 offen offset:0  lds
s_add_u32 m0,s57,40832
s_nop 0
buffer_load_dwordx4 v18,s[40:43],s55 offen offset:128  lds
s_add_u32 m0,s57,41856
s_nop 0
buffer_load_dwordx4 v19,s[40:43],s55 offen offset:128  lds
v_add_u32 v26,49152,v20
v_add_u32 v27,49152,v21
T a[48:49],26,0
T a[50:51],26,512
T a[52:53],27,0
T a[54:55],27,512
T a[56:57],26,4096
T a[58:59],26,4608
T a[60:61],27,4096
T a[62:63],27,4608
s_waitcnt vmcnt(20) lgkmcnt(0)
s_barrier
s_add_u32 s55,s50,1310720
s_add_u32 m0,s57,49152
s_nop 0
buffer_load_dwordx4 v18,s[40:43],s55 offen offset:0  lds
s_add_u32 m0,s57,50176
s_nop 0
buffer_load_dwordx4 v19,s[40:43],s55 offen offset:0  lds
s_add_u32 m0,s57,57216
s_nop 0
buffer_load_dwordx4 v18,s[40:43],s55 offen offset:128  lds
s_add_u32 m0,s57,58240
s_nop 0
buffer_load_dwordx4 v19,s[40:43],s55 offen offset:128  lds
v_add_u32 v26,65536,v20
v_add_u32 v27,65536,v21
T a[64:65],26,0
T a[66:67],26,512
T a[68:69],27,0
T a[70:71],27,512
T a[72:73],26,4096
T a[74:75],26,4608
T a[76:77],27,4096
T a[78:79],27,4608
s_waitcnt vmcnt(20) lgkmcnt(0)
s_barrier
s_add_u32 s55,s50,1441792
s_add_u32 m0,s57,65536
s_nop 0
buffer_load_dwordx4 v18,s[40:43],s55 offen offset:0  lds
s_add_u32 m0,s57,66560
s_nop 0
buffer_load_dwordx4 v19,s[40:43],s55 offen offset:0  lds
s_add_u32 m0,s57,73600
s_nop 0
buffer_load_dwordx4 v18,s[40:43],s55 offen offset:128  lds
s_add_u32 m0,s57,74624
s_nop 0
buffer_load_dwordx4 v19,s[40:43],s55 offen offset:128  lds
v_add_u32 v26,81920,v20
v_add_u32 v27,81920,v21
T a[80:81],26,0
T a[82:83],26,512
T a[84:85],27,0
T a[86:87],27,512
T a[88:89],26,4096
T a[90:91],26,4608
T a[92:93],27,4096
T a[94:95],27,4608
s_waitcnt vmcnt(20) lgkmcnt(0)
s_barrier
s_add_u32 s55,s50,1572864
s_add_u32 m0,s57,81920
s_nop 0
buffer_load_dwordx4 v18,s[40:43],s55 offen offset:0  lds
s_add_u32 m0,s57,82944
s_nop 0
buffer_load_dwordx4 v19,s[40:43],s55 offen offset:0  lds
s_add_u32 m0,s57,89984
s_nop 0
buffer_load_dwordx4 v18,s[40:43],s55 offen offset:128  lds
s_add_u32 m0,s57,91008
s_nop 0
buffer_load_dwordx4 v19,s[40:43],s55 offen offset:128  lds
v_add_u32 v26,98304,v20
v_add_u32 v27,98304,v21
T a[96:97],26,0
T a[98:99],26,512
T a[100:101],27,0
T a[102:103],27,512
T a[104:105],26,4096
T a[106:107],26,4608
T a[108:109],27,4096
T a[110:111],27,4608
s_waitcnt vmcnt(20) lgkmcnt(0)
s_barrier
s_add_u32 s55,s50,1703936
s_add_u32 m0,s57,98304
s_nop 0
buffer_load_dwordx4 v18,s[40:43],s55 offen offset:0  lds
s_add_u32 m0,s57,99328
s_nop 0
buffer_load_dwordx4 v19,s[40:43],s55 offen offset:0  lds
s_add_u32 m0,s57,106368
s_nop 0
buffer_load_dwordx4 v18,s[40:43],s55 offen offset:128  lds
s_add_u32 m0,s57,107392
s_nop 0
buffer_load_dwordx4 v19,s[40:43],s55 offen offset:128  lds
v_add_u32 v26,0,v20
v_add_u32 v27,0,v21
T a[112:113],26,0
T a[114:115],26,512
T a[116:117],27,0
T a[118:119],27,512
T a[120:121],26,4096
T a[122:123],26,4608
T a[124:125],27,4096
T a[126:127],27,4608
s_waitcnt vmcnt(20) lgkmcnt(0)
s_barrier
s_add_u32 s55,s50,1835008
s_add_u32 m0,s57,0
s_nop 0
buffer_load_dwordx4 v18,s[40:43],s55 offen offset:0  lds
s_add_u32 m0,s57,1024
s_nop 0
buffer_load_dwordx4 v19,s[40:43],s55 offen offset:0  lds
s_add_u32 m0,s57,8064
s_nop 0
buffer_load_dwordx4 v18,s[40:43],s55 offen offset:128  lds
s_add_u32 m0,s57,9088
s_nop 0
buffer_load_dwordx4 v19,s[40:43],s55 offen offset:128  lds
v_add_u32 v26,16384,v20
v_add_u32 v27,16384,v21
T a[128:129],26,0
T a[130:131],26,512
T a[132:133],27,0
T a[134:135],27,512
T a[136:137],26,4096
T a[138:139],26,4608
T a[140:141],27,4096
T a[142:143],27,4608
s_waitcnt vmcnt(20) lgkmcnt(0)
s_barrier
s_add_u32 s55,s50,1966080
s_add_u32 m0,s57,16384
s_nop 0
buffer_load_dwordx4 v18,s[40:43],s55 offen offset:0  lds
s_add_u32 m0,s57,17408
s_nop 0
buffer_load_dwordx4 v19,s[40:43],s55 offen offset:0  lds
s_add_u32 m0,s57,24448
s_nop 0
buffer_load_dwordx4 v18,s[40:43],s55 offen offset:128  lds
s_add_u32 m0,s57,25472
s_nop 0
buffer_load_dwordx4 v19,s[40:43],s55 offen offset:128  lds
v_add_u32 v26,32768,v20
v_add_u32 v27,32768,v21
T a[144:145],26,0
T a[146:147],26,512
T a[148:149],27,0
T a[150:151],27,512
T a[152:153],26,4096
T a[154:155],26,4608
T a[156:157],27,4096
T a[158:159],27,4608
s_waitcnt vmcnt(20) lgkmcnt(0)
s_barrier
v_add_u32 v26,49152,v20
v_add_u32 v27,49152,v21
T a[160:161],26,0
T a[162:163],26,512
T a[164:165],27,0
T a[166:167],27,512
T a[168:169],26,4096
T a[170:171],26,4608
T a[172:173],27,4096
T a[174:175],27,4608
s_waitcnt vmcnt(16) lgkmcnt(0)
s_barrier
v_add_u32 v26,65536,v20
v_add_u32 v27,65536,v21
T a[176:177],26,0
T a[178:179],26,512
T a[180:181],27,0
T a[182:183],27,512
T a[184:185],26,4096
T a[186:187],26,4608
T a[188:189],27,4096
T a[190:191],27,4608
s_waitcnt vmcnt(12) lgkmcnt(0)
s_barrier
v_add_u32 v26,81920,v20
v_add_u32 v27,81920,v21
T a[192:193],26,0
T a[194:195],26,512
T a[196:197],27,0
T a[198:199],27,512
T a[200:201],26,4096
T a[202:203],26,4608
T a[204:205],27,4096
T a[206:207],27,4608
s_waitcnt vmcnt(8) lgkmcnt(0)
s_barrier
v_add_u32 v26,98304,v20
v_add_u32 v27,98304,v21
T a[208:209],26,0
T a[210:211],26,512
T a[212:213],27,0
T a[214:215],27,512
T a[216:217],26,4096
T a[218:219],26,4608
T a[220:221],27,4096
T a[222:223],27,4608
s_waitcnt vmcnt(4) lgkmcnt(0)
s_barrier
v_add_u32 v26,0,v20
v_add_u32 v27,0,v21
T a[224:225],26,0
T a[226:227],26,512
T a[228:229],27,0
T a[230:231],27,512
T a[232:233],26,4096
T a[234:235],26,4608
T a[236:237],27,4096
T a[238:239],27,4608
s_waitcnt vmcnt(0) lgkmcnt(0)
s_barrier
v_add_u32 v26,16384,v20
v_add_u32 v27,16384,v21
T a[240:241],26,0
T a[242:243],26,512
T a[244:245],27,0
T a[246:247],27,512
T a[248:249],26,4096
T a[250:251],26,4608
T a[252:253],27,4096
T a[254:255],27,4608
L 0
s_barrier
v_mov_b32 v34,0x3f803f80
v_mov_b32 v35,0x3f803f80
v_mov_b32 v36,0x3f803f80
v_mov_b32 v37,0x3f803f80
s_nop 1
v_mfma_f32_16x16x32_bf16 v[106:109],a[0:3],v[34:37],0
v_mfma_f32_16x16x32_bf16 v[110:113],a[4:7],v[34:37],0
M 106,a[8:11],34
M 110,a[12:15],34
M 106,a[16:19],34
M 110,a[20:23],34
M 106,a[24:27],34
M 110,a[28:31],34
M 106,a[32:35],34
M 110,a[36:39],34
M 106,a[40:43],34
M 110,a[44:47],34
M 106,a[48:51],34
M 110,a[52:55],34
M 106,a[56:59],34
M 110,a[60:63],34
M 106,a[64:67],34
M 110,a[68:71],34
M 106,a[72:75],34
M 110,a[76:79],34
M 106,a[80:83],34
M 110,a[84:87],34
M 106,a[88:91],34
M 110,a[92:95],34
M 106,a[96:99],34
M 110,a[100:103],34
M 106,a[104:107],34
M 110,a[108:111],34
M 106,a[112:115],34
M 110,a[116:119],34
M 106,a[120:123],34
M 110,a[124:127],34
M 106,a[128:131],34
M 110,a[132:135],34
M 106,a[136:139],34
M 110,a[140:143],34
M 106,a[144:147],34
M 110,a[148:151],34
M 106,a[152:155],34
M 110,a[156:159],34
M 106,a[160:163],34
M 110,a[164:167],34
M 106,a[168:171],34
)"
R"(M 110,a[172:175],34
M 106,a[176:179],34
M 110,a[180:183],34
M 106,a[184:187],34
M 110,a[188:191],34
M 106,a[192:195],34
M 110,a[196:199],34
M 106,a[200:203],34
M 110,a[204:207],34
M 106,a[208:211],34
M 110,a[212:215],34
M 106,a[216:219],34
M 110,a[220:223],34
M 106,a[224:227],34
M 110,a[228:231],34
M 106,a[232:235],34
M 110,a[236:239],34
M 106,a[240:243],34
M 110,a[244:247],34
M 106,a[248:251],34
M 110,a[252:255],34
v_mov_b32 v4,s53
s_mov_b32 s82,0
s_mov_b32 s80,0
s_mov_b32 s54,s45
s_mul_i32 s57,s44,4096
s_mul_i32 s55,s45,32768
s_add_u32 s55,s55,s57
s_add_u32 s55,s55,8388608
s_mul_i32 s57,s44,4096
s_mul_i32 s60,s46,32768
s_mul_i32 s61,s46,1024
s_mul_i32 s62,s46,40960
s_mul_i32 s63,s46,1024
s_mul_i32 s64,s46,512
s_lshl_b32 s59,s44,8
s_add_u32 s59,s59,131072
s_mul_i32 s58,s45,1024
s_add_u32 s58,s58,109056000
s_mul_i32 s65,s45,40960
s_add_u32 s65,s65,s47
s_mul_i32 s68,s45,1024
s_add_u32 s68,s68,s48
s_mul_i32 s73,s45,512
s_add_u32 s73,s73,106954752
s_mul_i32 s74,s45,32768
s_add_u32 s74,s74,s49
s_mul_i32 s75,s45,1024
s_add_u32 s75,s75,107479040
s_mov_b32 s66,0x7fff0000
s_mov_b32 s67,0x7fff0000
s_mov_b32 s69,0x7fff0000
s_mov_b32 s76,0x7fff0000
s_mov_b32 s77,0x7fff0000
s_mov_b32 s70,0x7fff0000
s_mov_b32 s71,0x7fff0000
s_mov_b32 s72,0x7fff0000
v_mov_b32 v1,v0
v_add_u32 v2,16384,v0
s_mov_b32 s84,16384
s_mov_b32 s86,0x3a800000
s_mov_b32 s87,0x3727c5ac
s_mov_b32 s88,0xc0135761
s_mov_b32 s89,0x3d372713
s_lshr_b32 s85,s44,1
s_mul_i32 s85,s85,127
v_lshlrev_b32 v8,22,v0
v_and_b32 v8,0x80000000,v8
v_or_b32 v8,v8,v5
v_add_u32 v2,32768,v0
s_lshl_b32 s84,s57,1
s_add_u32 s84,s84,32768
s_getpc_b64 s[40:41]
Lg1_pc_%=:
s_lshl_b32 s78,s44,13
v_lshlrev_b32 v114,3,v0
v_add_u32 v114,s78,v114
v_min_u32 v114,Lcode_g2_b-Lg1_pc_%=-4,v114
s_mov_b32 m0,163584
s_nop 0
global_load_lds_dword v114,s[40:41]
s_add_u32 m0,s59,4096
s_nop 0
buffer_load_dword v8,s[36:39],s58 offen offset:0 sc1 lds
buffer_store_dword v4,v3,s[36:39],s71 offen offset:0 sc0 sc1
s_add_u32 m0,s59,5120
s_nop 0
buffer_load_dword v8,s[36:39],s58 offen offset:0 sc1 lds
buffer_store_dword v4,v3,s[36:39],s71 offen offset:0 sc0 sc1
s_mov_b32 s83,0
Lg1_poll_%=:
s_add_u32 s78,s83,2
s_and_b32 s78,s78,3
s_lshl_b32 s78,s78,10
s_add_u32 s78,s78,4096
s_add_u32 m0,s59,s78
s_nop 0
buffer_load_dword v8,s[36:39],s58 offen offset:0 sc1 lds
s_and_b32 s78,s83,3
s_lshl_b32 s78,s78,10
v_add_u32 v114,s78,v6
s_waitcnt vmcnt(4)
s_barrier
ds_read_b32 v7,v114 offset:4096
buffer_store_dword v4,v3,s[36:39],s70 offen offset:0 sc0 sc1
s_mov_b32 s70,s71
s_mov_b32 s71,s72
s_mov_b32 s72,0x7fff0000
s_add_u32 s83,s83,1
s_add_u32 s82,s82,1
L 0
v_cmp_eq_u32 vcc,s53,v7
s_nop 0
s_cmp_eq_u32 vcc_lo,-1
s_cbranch_scc1 Lg1_go_%=
s_cmp_lt_u32 s82,20000
s_cbranch_scc1 Lg1_poll_%=
Lg1_go_%=:
s_add_u32 m0,s57,0
s_add_u32 s56,s55,0
D 0,56,0
D 0,56,1024
D 0,56,2048
D 0,56,3072
s_add_u32 s58,s58,s61
s_add_u32 m0,s59,2048
s_nop 0
buffer_load_dword v8,s[36:39],s58 offen offset:0 sc1 lds
s_add_u32 m0,s57,16384
s_add_u32 s56,s55,16384
D 0,56,0
D 0,56,1024
D 0,56,2048
D 0,56,3072
s_add_u32 s55,s55,s60
s_sub_u32 s57,s84,s57
s_add_u32 s54,s54,s46
s_waitcnt vmcnt(4)
s_barrier
buffer_store_dword v4,v3,s[36:39],s70 offen offset:0 sc0 sc1
s_min_u32 s71,s71,s72
buffer_store_dword v4,v3,s[36:39],s71 offen offset:0 sc0 sc1
s_mov_b32 s70,0x7fff0000
s_mov_b32 s71,0x7fff0000
s_mov_b32 s72,0x7fff0000
s_cmp_eq_u32 s80,1
buffer_store_dword v4,v3,s[36:39],s71 offen offset:0 sc0 sc1
s_cbranch_scc1 Lg1_st1_%=
s_branch Lg1_st0_%=
Lg1_gox_%=:
s_add_u32 m0,s59,2048
s_nop 0
buffer_load_dword v8,s[36:39],s58 offen offset:0 sc1 lds
s_waitcnt vmcnt(0)
s_barrier
buffer_store_dword v4,v3,s[36:39],s70 offen offset:0 sc0 sc1
s_min_u32 s71,s71,s72
buffer_store_dword v4,v3,s[36:39],s71 offen offset:0 sc0 sc1
s_mov_b32 s70,0x7fff0000
s_mov_b32 s71,0x7fff0000
s_mov_b32 s72,0x7fff0000
s_cmp_eq_u32 s80,1
s_cbranch_scc1 Lg1_st1_%=
Lg1_st0_%=:
ds_read_b32 v7,v6 offset:2048
R 34,1,0
R 38,1,1024
R 42,1,2048
R 46,1,3072
R 50,1,4096
L 5
v_cmp_eq_u32 vcc,s53,v7
L 3
s_cmp_eq_u32 vcc_lo,-1
s_cbranch_scc1 Lg1_F0_%=
s_branch Lg1_L0_%=
Lg1_st1_%=:
ds_read_b32 v7,v6 offset:2048
R 34,2,0
R 38,2,1024
R 42,2,2048
R 46,2,3072
R 50,2,4096
L 5
v_cmp_eq_u32 vcc,s53,v7
L 3
s_cmp_eq_u32 vcc_lo,-1
s_cbranch_scc1 Lg1_F1_%=
s_branch Lg1_L1_%=
Lg1_F0_%=:
v_mfma_f32_16x16x32_bf16 v[22:25],a[4:7],v[34:37],0
s_add_u32 s58,s58,s61
s_add_u32 m0,s59,0
R 54,1,5120
M 22,a[12:15],38
buffer_load_dword v8,s[36:39],s58 offen offset:0 sc1 lds
v_mfma_f32_16x16x32_bf16 v[18:21],a[0:3],v[34:37],0
s_add_u32 m0,s57,0
R 58,1,6144
v_fma_f32 v26,-v102,v106,v26
M 18,a[8:11],38
L 3
s_add_u32 s56,s55,0
v_fma_f32 v27,-v102,v107,v27
M 18,a[16:19],42
s_mov_b32 s66,s65
R 62,1,7168
v_fma_f32 v28,-v102,v108,v28
M 18,a[24:27],46
D 0,56,0
M 22,a[20:23],42
s_add_u32 s65,s65,s62
R 66,1,8192
v_fma_f32 v29,-v102,v109,v29
v_mul_f32 v114,v26,v26
M 22,a[28:31],46
L 3
D 0,56,1024
M 22,a[36:39],50
s_add_u32 s54,s54,s46
R 70,1,9216
v_mul_f32 v115,v27,v27
M 22,a[44:47],54
v_mul_f32 v116,v28,v28
v_mul_f32 v117,v29,v29
M 18,a[32:35],50
R 74,1,10240
v_fma_f32 v114,v104,v114,v103
M 18,a[40:43],54
L 3
D 0,56,2048
M 18,a[48:51],58
R 78,1,11264
v_fma_f32 v115,v104,v115,v103
M 18,a[56:59],62
D 0,56,3072
v_fma_f32 v116,v104,v116,v103
M 22,a[52:55],58
s_add_u32 m0,s57,16384
R 82,1,12288
v_fma_f32 v117,v104,v117,v103
M 22,a[60:63],62
L 3
v_mul_f32 v114,v26,v114
v_mul_f32 v115,v27,v115
M 22,a[68:71],66
s_add_u32 s56,s55,16384
R 86,1,13312
v_mul_f32 v116,v28,v116
M 22,a[76:79],70
buffer_load_dwordx4 v[98:101],v11,s[36:39],s73 offen offset:0 sc1
M 18,a[64:67],66
s_add_u32 s73,s73,s64
R 90,1,14336
v_mul_f32 v117,v29,v117
M 18,a[72:75],70
L 3
v_exp_f32 v114,v114
v_fma_f32 v30,-v102,v110,v30
M 18,a[80:83],74
s_waitcnt vmcnt(9)
R 94,1,15360
v_fma_f32 v114,v114,v105,v105
M 18,a[88:91],78
s_barrier
v_exp_f32 v115,v115
M 22,a[84:87],74
s_add_u32 s55,s55,s60
R 34,1,16384
v_fma_f32 v115,v115,v105,v105
M 22,a[92:95],78
L 3
v_exp_f32 v116,v116
M 22,a[100:103],82
s_sub_u32 s57,s84,s57
R 38,1,17408
v_fma_f32 v116,v116,v105,v105
M 22,a[108:111],86
D 0,56,0
M 18,a[96:99],82
R 42,1,18432
v_exp_f32 v117,v117
M 18,a[104:107],86
L 3
v_rcp_f32 v114,v114
M 18,a[112:115],90
R 46,1,19456
v_fma_f32 v117,v117,v105,v105
M 18,a[120:123],94
v_rcp_f32 v115,v115
M 22,a[116:119],90
R 50,1,20480
v_mul_f32 v26,v26,v114
M 22,a[124:127],94
L 3
v_rcp_f32 v116,v116
M 22,a[132:135],34
R 54,1,21504
v_mul_f32 v27,v27,v115
M 22,a[140:143],38
D 0,56,1024
v_mul_f32 v28,v28,v116
M 18,a[128:131],34
R 58,1,22528
v_cvt_pk_bf16_f32 v122,v26,v27
M 18,a[136:139],38
L 3
v_rcp_f32 v117,v117
M 18,a[144:147],42
R 62,1,23552
v_mul_f32 v29,v29,v117
M 18,a[152:155],46
v_cvt_pk_bf16_f32 v123,v28,v29
v_fma_f32 v31,-v102,v111,v31
M 22,a[148:151],42
R 66,1,24576
v_fma_f32 v32,-v102,v112,v32
M 22,a[156:159],46
L 3
D 0,56,2048
v_fma_f32 v33,-v102,v113,v33
M 22,a[164:167],50
R 70,1,25600
v_mul_f32 v118,v30,v30
M 22,a[172:175],54
v_mul_f32 v119,v31,v31
v_mul_f32 v120,v32,v32
M 18,a[160:163],50
R 74,1,26624
v_mul_f32 v121,v33,v33
M 18,a[168:171],54
L 3
v_fma_f32 v118,v104,v118,v103
v_fma_f32 v119,v104,v119,v103
M 18,a[176:179],58
R 78,1,27648
v_fma_f32 v120,v104,v120,v103
M 18,a[184:187],62
v_fma_f32 v121,v104,v121,v103
v_mul_f32 v118,v30,v118
M 22,a[180:183],58
R 82,1,28672
v_exp_f32 v118,v118
M 22,a[188:191],62
L 3
D 0,56,3072
M 22,a[196:199],66
R 86,1,29696
v_mul_f32 v119,v31,v119
M 22,a[204:207],70
v_exp_f32 v119,v119
M 18,a[192:195],66
R 90,1,30720
v_mul_f32 v120,v32,v120
M 18,a[200:203],70
L 3
v_mul_f32 v121,v33,v121
v_fma_f32 v118,v118,v105,v105
M 18,a[208:211],74
s_waitcnt vmcnt(5)
R 94,1,31744
v_fma_f32 v119,v119,v105,v105
M 18,a[216:219],78
s_barrier
ds_read_b32 v7,v6 offset:0
v_exp_f32 v120,v120
M 22,a[212:215],74
R 34,2,0
v_fma_f32 v120,v120,v105,v105
M 22,a[220:223],78
L 4
buffer_store_dword v4,v3,s[36:39],s70 offen offset:0 sc0 sc1
v_exp_f32 v121,v121
M 22,a[228:231],82
s_mov_b32 s70,s69
R 38,2,1024
v_fma_f32 v121,v121,v105,v105
v_rcp_f32 v118,v118
v_rcp_f32 v119,v119
v_rcp_f32 v120,v120
v_rcp_f32 v121,v121
M 22,a[236:239],86
s_mov_b32 s69,s68
v_mul_f32 v30,v30,v118
v_mul_f32 v31,v31,v119
v_mul_f32 v32,v32,v120
v_mul_f32 v33,v33,v121
v_cvt_pk_bf16_f32 v124,v30,v31
v_cvt_pk_bf16_f32 v125,v32,v33
M 18,a[224:227],82
s_add_u32 s68,s68,s63
R 42,2,2048
buffer_store_dwordx4 v[122:125],v15,s[36:39],s67 offen offset:0 sc0 sc1
M 18,a[232:235],86
L 4
L 3
M 18,a[240:243],90
R 46,2,3072
v_cmp_eq_u32 vcc,s53,v7
M 18,a[248:251],94
buffer_store_dwordx4 v[122:125],v16,s[36:39],s67 offen offset:0 sc0 sc1
M 22,a[244:247],90
s_waitcnt vmcnt(7) lgkmcnt(2)
s_cmp_eq_u32 vcc_lo,-1
R 50,2,4096
M 22,a[252:255],94
s_cbranch_scc0 Lg1_L1_%=
Lg1_F1_%=:
v_mfma_f32_16x16x32_bf16 v[30:33],a[4:7],v[34:37],0
s_add_u32 s58,s58,s61
s_add_u32 m0,s59,1024
R 54,2,5120
M 30,a[12:15],38
buffer_load_dword v8,s[36:39],s58 offen offset:0 sc1 lds
v_mfma_f32_16x16x32_bf16 v[26:29],a[0:3],v[34:37],0
s_add_u32 m0,s57,0
R 58,2,6144
v_fma_f32 v18,-v98,v106,v18
M 26,a[8:11],38
L 3
s_add_u32 s56,s55,0
v_fma_f32 v19,-v98,v107,v19
M 26,a[16:19],42
s_mov_b32 s67,s65
R 62,2,7168
v_fma_f32 v20,-v98,v108,v20
M 26,a[24:27],46
D 0,56,0
M 30,a[20:23],42
s_add_u32 s65,s65,s62
R 66,2,8192
v_fma_f32 v21,-v98,v109,v21
v_mul_f32 v114,v18,v18
M 30,a[28:31],46
L 3
D 0,56,1024
M 30,a[36:39],50
s_add_u32 s54,s54,s46
R 70,2,9216
v_mul_f32 v115,v19,v19
M 30,a[44:47],54
v_mul_f32 v116,v20,v20
v_mul_f32 v117,v21,v21
M 26,a[32:35],50
R 74,2,10240
v_fma_f32 v114,v100,v114,v99
M 26,a[40:43],54
L 3
D 0,56,2048
M 26,a[48:51],58
R 78,2,11264
v_fma_f32 v115,v100,v115,v99
M 26,a[56:59],62
D 0,56,3072
v_fma_f32 v116,v100,v116,v99
M 30,a[52:55],58
s_add_u32 m0,s57,16384
R 82,2,12288
v_fma_f32 v117,v100,v117,v99
M 30,a[60:63],62
L 3
v_mul_f32 v114,v18,v114
v_mul_f32 v115,v19,v115
M 30,a[68:71],66
s_add_u32 s56,s55,16384
R 86,2,13312
v_mul_f32 v116,v20,v116
M 30,a[76:79],70
buffer_load_dwordx4 v[102:105],v11,s[36:39],s73 offen offset:0 sc1
M 26,a[64:67],66
s_add_u32 s73,s73,s64
R 90,2,14336
v_mul_f32 v117,v21,v117
M 26,a[72:75],70
L 3
v_exp_f32 v114,v114
v_fma_f32 v22,-v98,v110,v22
M 26,a[80:83],74
s_waitcnt vmcnt(9)
R 94,2,15360
v_fma_f32 v114,v114,v101,v101
M 26,a[88:91],78
s_barrier
v_exp_f32 v115,v115
M 30,a[84:87],74
s_add_u32 s55,s55,s60
R 34,2,16384
v_fma_f32 v115,v115,v101,v101
M 30,a[92:95],78
L 3
v_exp_f32 v116,v116
M 30,a[100:103],82
s_sub_u32 s57,s84,s57
R 38,2,17408
v_fma_f32 v116,v116,v101,v101
M 30,a[108:111],86
D 0,56,0
M 26,a[96:99],82
R 42,2,18432
v_exp_f32 v117,v117
)"
R"(M 26,a[104:107],86
L 3
v_rcp_f32 v114,v114
M 26,a[112:115],90
R 46,2,19456
v_fma_f32 v117,v117,v101,v101
M 26,a[120:123],94
v_rcp_f32 v115,v115
M 30,a[116:119],90
R 50,2,20480
v_mul_f32 v18,v18,v114
M 30,a[124:127],94
L 3
v_rcp_f32 v116,v116
M 30,a[132:135],34
R 54,2,21504
v_mul_f32 v19,v19,v115
M 30,a[140:143],38
D 0,56,1024
v_mul_f32 v20,v20,v116
M 26,a[128:131],34
R 58,2,22528
v_cvt_pk_bf16_f32 v122,v18,v19
M 26,a[136:139],38
L 3
v_rcp_f32 v117,v117
M 26,a[144:147],42
R 62,2,23552
v_mul_f32 v21,v21,v117
M 26,a[152:155],46
v_cvt_pk_bf16_f32 v123,v20,v21
v_fma_f32 v23,-v98,v111,v23
M 30,a[148:151],42
R 66,2,24576
v_fma_f32 v24,-v98,v112,v24
M 30,a[156:159],46
L 3
D 0,56,2048
v_fma_f32 v25,-v98,v113,v25
M 30,a[164:167],50
R 70,2,25600
v_mul_f32 v118,v22,v22
M 30,a[172:175],54
v_mul_f32 v119,v23,v23
v_mul_f32 v120,v24,v24
M 26,a[160:163],50
R 74,2,26624
v_mul_f32 v121,v25,v25
M 26,a[168:171],54
L 3
v_fma_f32 v118,v100,v118,v99
v_fma_f32 v119,v100,v119,v99
M 26,a[176:179],58
R 78,2,27648
v_fma_f32 v120,v100,v120,v99
M 26,a[184:187],62
v_fma_f32 v121,v100,v121,v99
v_mul_f32 v118,v22,v118
M 30,a[180:183],58
R 82,2,28672
v_exp_f32 v118,v118
M 30,a[188:191],62
L 3
D 0,56,3072
M 30,a[196:199],66
R 86,2,29696
v_mul_f32 v119,v23,v119
M 30,a[204:207],70
v_exp_f32 v119,v119
M 26,a[192:195],66
R 90,2,30720
v_mul_f32 v120,v24,v120
M 26,a[200:203],70
L 3
v_mul_f32 v121,v25,v121
v_fma_f32 v118,v118,v101,v101
M 26,a[208:211],74
s_waitcnt vmcnt(5)
R 94,2,31744
v_fma_f32 v119,v119,v101,v101
M 26,a[216:219],78
s_barrier
ds_read_b32 v7,v6 offset:1024
v_exp_f32 v120,v120
M 30,a[212:215],74
R 34,1,0
v_fma_f32 v120,v120,v101,v101
M 30,a[220:223],78
L 4
buffer_store_dword v4,v3,s[36:39],s70 offen offset:0 sc0 sc1
v_exp_f32 v121,v121
M 30,a[228:231],82
s_mov_b32 s70,s69
R 38,1,1024
v_fma_f32 v121,v121,v101,v101
v_rcp_f32 v118,v118
v_rcp_f32 v119,v119
v_rcp_f32 v120,v120
v_rcp_f32 v121,v121
M 30,a[236:239],86
s_mov_b32 s69,s68
v_mul_f32 v22,v22,v118
v_mul_f32 v23,v23,v119
v_mul_f32 v24,v24,v120
v_mul_f32 v25,v25,v121
v_cvt_pk_bf16_f32 v124,v22,v23
v_cvt_pk_bf16_f32 v125,v24,v25
M 26,a[224:227],82
s_add_u32 s68,s68,s63
R 42,1,2048
buffer_store_dwordx4 v[122:125],v15,s[36:39],s66 offen offset:0 sc0 sc1
M 26,a[232:235],86
L 4
L 3
M 26,a[240:243],90
R 46,1,3072
v_cmp_eq_u32 vcc,s53,v7
M 26,a[248:251],94
buffer_store_dwordx4 v[122:125],v16,s[36:39],s66 offen offset:0 sc0 sc1
M 30,a[244:247],90
s_waitcnt vmcnt(7) lgkmcnt(2)
s_cmp_eq_u32 vcc_lo,-1
R 50,1,4096
M 30,a[252:255],94
s_cbranch_scc1 Lg1_F0_%=
Lg1_L0_%=:
v_mfma_f32_16x16x32_bf16 v[22:25],a[4:7],v[34:37],0
s_add_u32 m0,s59,3072
s_mov_b32 s66,s65
R 54,1,5120
M 22,a[12:15],38
buffer_load_dword v8,s[36:39],s58 offen offset:0 sc1 lds
v_mfma_f32_16x16x32_bf16 v[18:21],a[0:3],v[34:37],0
s_add_u32 s65,s65,s62
R 58,1,6144
v_fma_f32 v26,-v102,v106,v26
M 18,a[8:11],38
L 3
v_fma_f32 v27,-v102,v107,v27
v_fma_f32 v28,-v102,v108,v28
M 18,a[16:19],42
R 62,1,7168
v_fma_f32 v29,-v102,v109,v29
M 18,a[24:27],46
v_mul_f32 v114,v26,v26
v_mul_f32 v115,v27,v27
M 22,a[20:23],42
R 66,1,8192
v_mul_f32 v116,v28,v28
M 22,a[28:31],46
L 3
v_mul_f32 v117,v29,v29
v_fma_f32 v114,v104,v114,v103
M 22,a[36:39],50
R 70,1,9216
v_fma_f32 v115,v104,v115,v103
M 22,a[44:47],54
v_fma_f32 v116,v104,v116,v103
v_fma_f32 v117,v104,v117,v103
M 18,a[32:35],50
R 74,1,10240
v_mul_f32 v114,v26,v114
M 18,a[40:43],54
L 3
v_exp_f32 v114,v114
M 18,a[48:51],58
R 78,1,11264
v_mul_f32 v115,v27,v115
M 18,a[56:59],62
v_exp_f32 v115,v115
M 22,a[52:55],58
R 82,1,12288
v_mul_f32 v116,v28,v116
M 22,a[60:63],62
L 3
v_exp_f32 v116,v116
M 22,a[68:71],66
R 86,1,13312
v_mul_f32 v117,v29,v117
M 22,a[76:79],70
buffer_load_dwordx4 v[98:101],v11,s[36:39],s73 offen offset:0 sc1
M 18,a[64:67],66
s_add_u32 s73,s73,s64
R 90,1,14336
v_fma_f32 v114,v114,v105,v105
M 18,a[72:75],70
L 3
v_exp_f32 v117,v117
M 18,a[80:83],74
s_waitcnt vmcnt(1)
R 94,1,15360
v_fma_f32 v115,v115,v105,v105
M 18,a[88:91],78
s_barrier
ds_read_b32 v7,v6 offset:3072
v_fma_f32 v116,v116,v105,v105
M 22,a[84:87],74
R 34,1,16384
v_fma_f32 v117,v117,v105,v105
M 22,a[92:95],78
L 4
buffer_store_dword v4,v3,s[36:39],s70 offen offset:0 sc0 sc1
M 22,a[100:103],82
s_mov_b32 s70,0x7fff0000
R 38,1,17408
v_fma_f32 v30,-v102,v110,v30
M 22,a[108:111],86
v_rcp_f32 v114,v114
M 18,a[96:99],82
R 42,1,18432
v_mul_f32 v26,v26,v114
M 18,a[104:107],86
L 4
L 3
v_cmp_eq_u32 vcc,s53,v7
M 18,a[112:115],90
R 46,1,19456
v_fma_f32 v31,-v102,v111,v31
M 18,a[120:123],94
v_rcp_f32 v115,v115
M 22,a[116:119],90
s_cmp_eq_u32 vcc_lo,-1
R 50,1,20480
v_mul_f32 v27,v27,v115
s_cbranch_scc0 Lg1_X0_%=
M 22,a[124:127],94
L 3
s_add_u32 m0,s57,0
s_add_u32 s56,s55,0
M 22,a[132:135],34
s_add_u32 s58,s58,s61
R 54,1,21504
v_cvt_pk_bf16_f32 v122,v26,v27
M 22,a[140:143],38
D 0,56,0
M 18,a[128:131],34
s_add_u32 s54,s54,s46
R 58,1,22528
v_fma_f32 v32,-v102,v112,v32
M 18,a[136:139],38
L 3
v_rcp_f32 v116,v116
M 18,a[144:147],42
R 62,1,23552
v_mul_f32 v28,v28,v116
M 18,a[152:155],46
D 0,56,1024
M 22,a[148:151],42
R 66,1,24576
v_fma_f32 v33,-v102,v113,v33
M 22,a[156:159],46
L 3
v_rcp_f32 v117,v117
M 22,a[164:167],50
R 70,1,25600
v_mul_f32 v29,v29,v117
M 22,a[172:175],54
D 0,56,2048
M 18,a[160:163],50
R 74,1,26624
v_cvt_pk_bf16_f32 v123,v28,v29
M 18,a[168:171],54
L 3
v_mul_f32 v118,v30,v30
v_mul_f32 v119,v31,v31
M 18,a[176:179],58
R 78,1,27648
v_mul_f32 v120,v32,v32
M 18,a[184:187],62
D 0,56,3072
M 22,a[180:183],58
s_add_u32 m0,s59,0
R 82,1,28672
v_mul_f32 v121,v33,v33
M 22,a[188:191],62
L 3
buffer_load_dword v8,s[36:39],s58 offen offset:0 sc1 lds
M 22,a[196:199],66
s_add_u32 m0,s57,16384
R 86,1,29696
v_fma_f32 v118,v104,v118,v103
M 22,a[204:207],70
s_add_u32 s56,s55,16384
v_fma_f32 v119,v104,v119,v103
v_fma_f32 v120,v104,v120,v103
M 18,a[192:195],66
s_add_u32 s55,s55,s60
R 90,1,30720
v_fma_f32 v121,v104,v121,v103
M 18,a[200:203],70
L 3
D 0,56,0
M 18,a[208:211],74
s_waitcnt vmcnt(1)
R 94,1,31744
v_mul_f32 v118,v30,v118
M 18,a[216:219],78
s_barrier
ds_read_b32 v7,v6 offset:0
v_mul_f32 v119,v31,v119
M 22,a[212:215],74
s_sub_u32 s57,s84,s57
R 34,2,0
v_mul_f32 v120,v32,v120
M 22,a[220:223],78
L 4
buffer_store_dword v4,v3,s[36:39],s70 offen offset:0 sc0 sc1
v_mul_f32 v121,v33,v121
v_exp_f32 v118,v118
v_exp_f32 v119,v119
v_exp_f32 v120,v120
v_exp_f32 v121,v121
M 22,a[228:231],82
s_mov_b32 s70,s69
R 38,2,1024
v_fma_f32 v118,v118,v105,v105
v_fma_f32 v119,v119,v105,v105
v_fma_f32 v120,v120,v105,v105
v_fma_f32 v121,v121,v105,v105
v_rcp_f32 v118,v118
v_rcp_f32 v119,v119
v_rcp_f32 v120,v120
v_rcp_f32 v121,v121
M 22,a[236:239],86
s_mov_b32 s69,s68
v_mul_f32 v30,v30,v118
v_mul_f32 v31,v31,v119
v_mul_f32 v32,v32,v120
v_mul_f32 v33,v33,v121
v_cvt_pk_bf16_f32 v124,v30,v31
v_cvt_pk_bf16_f32 v125,v32,v33
M 18,a[224:227],82
s_add_u32 s68,s68,s63
R 42,2,2048
buffer_store_dwordx4 v[122:125],v15,s[36:39],s67 offen offset:0 sc0 sc1
M 18,a[232:235],86
L 4
L 3
M 18,a[240:243],90
R 46,2,3072
v_cmp_eq_u32 vcc,s53,v7
M 18,a[248:251],94
D 0,56,1024
M 22,a[244:247],90
L 2
s_cmp_eq_u32 vcc_lo,-1
R 50,2,4096
buffer_store_dwordx4 v[122:125],v16,s[36:39],s67 offen offset:0 sc0 sc1
M 22,a[252:255],94
D 0,56,2048
D 0,56,3072
buffer_store_dword v4,v3,s[36:39],s71 offen offset:0 sc0 sc1
buffer_store_dword v4,v3,s[36:39],s71 offen offset:0 sc0 sc1
buffer_store_dword v4,v3,s[36:39],s71 offen offset:0 sc0 sc1
s_cbranch_scc1 Lg1_F1_%=
s_branch Lg1_L1_%=
Lg1_X0_%=:
M 22,a[124:127],94
L 3
s_add_u32 m0,s59,0
v_cvt_pk_bf16_f32 v122,v26,v27
M 22,a[132:135],34
R 54,1,21504
v_fma_f32 v32,-v102,v112,v32
M 22,a[140:143],38
buffer_load_dword v8,s[36:39],s58 offen offset:0 sc1 lds
M 18,a[128:131],34
R 58,1,22528
v_fma_f32 v33,-v102,v113,v33
M 18,a[136:139],38
L 3
v_rcp_f32 v116,v116
M 18,a[144:147],42
R 62,1,23552
v_mul_f32 v28,v28,v116
M 18,a[152:155],46
v_rcp_f32 v117,v117
M 22,a[148:151],42
R 66,1,24576
v_mul_f32 v29,v29,v117
M 22,a[156:159],46
L 3
v_cvt_pk_bf16_f32 v123,v28,v29
v_mul_f32 v118,v30,v30
M 22,a[164:167],50
R 70,1,25600
v_mul_f32 v119,v31,v31
M 22,a[172:175],54
v_mul_f32 v120,v32,v32
v_mul_f32 v121,v33,v33
M 18,a[160:163],50
R 74,1,26624
v_fma_f32 v118,v104,v118,v103
M 18,a[168:171],54
L 3
v_fma_f32 v119,v104,v119,v103
v_fma_f32 v120,v104,v120,v103
M 18,a[176:179],58
R 78,1,27648
v_fma_f32 v121,v104,v121,v103
M 18,a[184:187],62
v_mul_f32 v118,v30,v118
v_mul_f32 v119,v31,v119
M 22,a[180:183],58
R 82,1,28672
v_mul_f32 v120,v32,v120
M 22,a[188:191],62
L 3
v_exp_f32 v118,v118
M 22,a[196:199],66
R 86,1,29696
v_mul_f32 v121,v33,v121
M 22,a[204:207],70
v_exp_f32 v119,v119
M 18,a[192:195],66
R 90,1,30720
v_fma_f32 v118,v118,v105,v105
M 18,a[200:203],70
L 3
v_exp_f32 v120,v120
M 18,a[208:211],74
s_waitcnt vmcnt(0)
R 94,1,31744
v_fma_f32 v119,v119,v105,v105
M 18,a[216:219],78
s_barrier
buffer_store_dword v4,v3,s[36:39],s70 offen offset:0 sc0 sc1
M 22,a[212:215],74
s_mov_b32 s70,s69
ds_read_b32 v7,v6 offset:0
v_fma_f32 v120,v120,v105,v105
M 22,a[220:223],78
L 3
v_exp_f32 v121,v121
M 22,a[228:231],82
s_mov_b32 s69,s68
v_fma_f32 v121,v121,v105,v105
v_rcp_f32 v118,v118
v_rcp_f32 v119,v119
v_rcp_f32 v120,v120
v_rcp_f32 v121,v121
M 22,a[236:239],86
s_add_u32 s68,s68,s63
v_mul_f32 v30,v30,v118
v_mul_f32 v31,v31,v119
v_mul_f32 v32,v32,v120
v_mul_f32 v33,v33,v121
v_cvt_pk_bf16_f32 v124,v30,v31
v_cvt_pk_bf16_f32 v125,v32,v33
M 18,a[224:227],82
buffer_store_dwordx4 v[122:125],v15,s[36:39],s67 offen offset:0 sc0 sc1
M 18,a[232:235],86
L 1
M 18,a[240:243],90
L 0
v_cmp_eq_u32 vcc,s53,v7
M 18,a[248:251],94
buffer_store_dwordx4 v[122:125],v16,s[36:39],s67 offen offset:0 sc0 sc1
M 22,a[244:247],90
s_nop 0
M 22,a[252:255],94
s_mov_b32 s80,1
s_cmp_eq_u32 vcc_lo,-1
s_cbranch_scc0 Lg1_xn0_%=
s_add_u32 m0,s57,0
s_add_u32 s56,s55,0
D 0,56,0
v_fma_f32 v18,-v98,v106,v18
v_fma_f32 v19,-v98,v107,v19
v_fma_f32 v20,-v98,v108,v20
v_fma_f32 v21,-v98,v109,v21
v_mul_f32 v114,v18,v18
v_mul_f32 v115,v19,v19
v_mul_f32 v116,v20,v20
D 0,56,1024
v_mul_f32 v117,v21,v21
v_fma_f32 v114,v100,v114,v99
v_fma_f32 v115,v100,v115,v99
v_fma_f32 v116,v100,v116,v99
v_fma_f32 v117,v100,v117,v99
v_mul_f32 v114,v18,v114
v_mul_f32 v115,v19,v115
D 0,56,2048
v_mul_f32 v116,v20,v116
v_mul_f32 v117,v21,v117
v_exp_f32 v114,v114
v_exp_f32 v115,v115
v_exp_f32 v116,v116
v_exp_f32 v117,v117
v_fma_f32 v114,v114,v101,v101
D 0,56,3072
v_fma_f32 v115,v115,v101,v101
v_fma_f32 v116,v116,v101,v101
v_fma_f32 v117,v117,v101,v101
v_rcp_f32 v114,v114
v_rcp_f32 v115,v115
v_rcp_f32 v116,v116
v_rcp_f32 v117,v117
s_add_u32 s58,s58,s61
s_add_u32 m0,s57,16384
)"
R"(s_add_u32 s56,s55,16384
D 0,56,0
v_mul_f32 v18,v18,v114
v_mul_f32 v19,v19,v115
v_mul_f32 v20,v20,v116
v_mul_f32 v21,v21,v117
v_cvt_pk_bf16_f32 v122,v18,v19
v_cvt_pk_bf16_f32 v123,v20,v21
v_fma_f32 v22,-v98,v110,v22
D 0,56,1024
v_fma_f32 v23,-v98,v111,v23
v_fma_f32 v24,-v98,v112,v24
v_fma_f32 v25,-v98,v113,v25
v_mul_f32 v118,v22,v22
v_mul_f32 v119,v23,v23
v_mul_f32 v120,v24,v24
v_mul_f32 v121,v25,v25
D 0,56,2048
v_fma_f32 v118,v100,v118,v99
v_fma_f32 v119,v100,v119,v99
v_fma_f32 v120,v100,v120,v99
v_fma_f32 v121,v100,v121,v99
v_mul_f32 v118,v22,v118
v_mul_f32 v119,v23,v119
v_mul_f32 v120,v24,v120
D 0,56,3072
v_mul_f32 v121,v25,v121
v_exp_f32 v118,v118
v_exp_f32 v119,v119
v_exp_f32 v120,v120
v_exp_f32 v121,v121
v_fma_f32 v118,v118,v101,v101
v_fma_f32 v119,v119,v101,v101
s_add_u32 s55,s55,s60
s_sub_u32 s57,s84,s57
s_add_u32 s54,s54,s46
v_fma_f32 v120,v120,v101,v101
v_fma_f32 v121,v121,v101,v101
v_rcp_f32 v118,v118
v_rcp_f32 v119,v119
v_rcp_f32 v120,v120
v_rcp_f32 v121,v121
v_mul_f32 v22,v22,v118
v_mul_f32 v23,v23,v119
v_mul_f32 v24,v24,v120
v_mul_f32 v25,v25,v121
v_cvt_pk_bf16_f32 v124,v22,v23
v_cvt_pk_bf16_f32 v125,v24,v25
buffer_store_dwordx4 v[122:125],v15,s[36:39],s66 offen offset:0 sc0 sc1
buffer_store_dwordx4 v[122:125],v16,s[36:39],s66 offen offset:0 sc0 sc1
s_mov_b32 s72,s69
s_mov_b32 s69,0x7fff0000
s_mov_b32 s66,0x7fff0000
s_mov_b32 s76,0x7fff0000
s_branch Lg1_gox_%=
Lg1_xn0_%=:
s_cmp_ge_u32 s54,256
s_cbranch_scc1 Lg1_fin0_%=
s_add_u32 m0,s59,4096
s_nop 0
buffer_load_dword v8,s[36:39],s58 offen offset:0 sc1 lds
buffer_store_dword v4,v3,s[36:39],s71 offen offset:0 sc0 sc1
s_add_u32 m0,s59,5120
s_nop 0
buffer_load_dword v8,s[36:39],s58 offen offset:0 sc1 lds
buffer_store_dword v4,v3,s[36:39],s71 offen offset:0 sc0 sc1
s_mov_b32 s83,0
v_fma_f32 v18,-v98,v106,v18
v_fma_f32 v19,-v98,v107,v19
v_fma_f32 v20,-v98,v108,v20
v_fma_f32 v21,-v98,v109,v21
v_mul_f32 v114,v18,v18
v_mul_f32 v115,v19,v19
v_mul_f32 v116,v20,v20
v_mul_f32 v117,v21,v21
v_fma_f32 v114,v100,v114,v99
v_fma_f32 v115,v100,v115,v99
v_fma_f32 v116,v100,v116,v99
v_fma_f32 v117,v100,v117,v99
v_mul_f32 v114,v18,v114
v_mul_f32 v115,v19,v115
v_mul_f32 v116,v20,v116
v_mul_f32 v117,v21,v117
v_exp_f32 v114,v114
v_exp_f32 v115,v115
v_exp_f32 v116,v116
v_exp_f32 v117,v117
v_fma_f32 v114,v114,v101,v101
v_fma_f32 v115,v115,v101,v101
v_fma_f32 v116,v116,v101,v101
v_fma_f32 v117,v117,v101,v101
v_rcp_f32 v114,v114
v_rcp_f32 v115,v115
v_rcp_f32 v116,v116
v_rcp_f32 v117,v117
v_mul_f32 v18,v18,v114
v_mul_f32 v19,v19,v115
v_mul_f32 v20,v20,v116
v_mul_f32 v21,v21,v117
v_cvt_pk_bf16_f32 v122,v18,v19
v_cvt_pk_bf16_f32 v123,v20,v21
v_fma_f32 v22,-v98,v110,v22
v_fma_f32 v23,-v98,v111,v23
v_fma_f32 v24,-v98,v112,v24
v_fma_f32 v25,-v98,v113,v25
v_mul_f32 v118,v22,v22
v_mul_f32 v119,v23,v23
v_mul_f32 v120,v24,v24
v_mul_f32 v121,v25,v25
v_fma_f32 v118,v100,v118,v99
v_fma_f32 v119,v100,v119,v99
v_fma_f32 v120,v100,v120,v99
v_fma_f32 v121,v100,v121,v99
v_mul_f32 v118,v22,v118
v_mul_f32 v119,v23,v119
v_mul_f32 v120,v24,v120
v_mul_f32 v121,v25,v121
v_exp_f32 v118,v118
v_exp_f32 v119,v119
v_exp_f32 v120,v120
v_exp_f32 v121,v121
v_fma_f32 v118,v118,v101,v101
v_fma_f32 v119,v119,v101,v101
v_fma_f32 v120,v120,v101,v101
v_fma_f32 v121,v121,v101,v101
v_rcp_f32 v118,v118
v_rcp_f32 v119,v119
v_rcp_f32 v120,v120
v_rcp_f32 v121,v121
v_mul_f32 v22,v22,v118
v_mul_f32 v23,v23,v119
v_mul_f32 v24,v24,v120
v_mul_f32 v25,v25,v121
v_cvt_pk_bf16_f32 v124,v22,v23
v_cvt_pk_bf16_f32 v125,v24,v25
buffer_store_dwordx4 v[122:125],v15,s[36:39],s66 offen offset:0 sc0 sc1
buffer_store_dwordx4 v[122:125],v16,s[36:39],s66 offen offset:0 sc0 sc1
s_mov_b32 s72,s69
s_mov_b32 s69,0x7fff0000
s_mov_b32 s66,0x7fff0000
s_mov_b32 s76,0x7fff0000
s_branch Lg1_poll_%=
Lg1_fin0_%=:
v_fma_f32 v18,-v98,v106,v18
v_fma_f32 v19,-v98,v107,v19
v_fma_f32 v20,-v98,v108,v20
v_fma_f32 v21,-v98,v109,v21
v_mul_f32 v114,v18,v18
v_mul_f32 v115,v19,v19
v_mul_f32 v116,v20,v20
v_mul_f32 v117,v21,v21
v_fma_f32 v114,v100,v114,v99
v_fma_f32 v115,v100,v115,v99
v_fma_f32 v116,v100,v116,v99
v_fma_f32 v117,v100,v117,v99
v_mul_f32 v114,v18,v114
v_mul_f32 v115,v19,v115
v_mul_f32 v116,v20,v116
v_mul_f32 v117,v21,v117
v_exp_f32 v114,v114
v_exp_f32 v115,v115
v_exp_f32 v116,v116
v_exp_f32 v117,v117
v_fma_f32 v114,v114,v101,v101
v_fma_f32 v115,v115,v101,v101
v_fma_f32 v116,v116,v101,v101
v_fma_f32 v117,v117,v101,v101
v_rcp_f32 v114,v114
v_rcp_f32 v115,v115
v_rcp_f32 v116,v116
v_rcp_f32 v117,v117
v_mul_f32 v18,v18,v114
v_mul_f32 v19,v19,v115
v_mul_f32 v20,v20,v116
v_mul_f32 v21,v21,v117
v_cvt_pk_bf16_f32 v122,v18,v19
v_cvt_pk_bf16_f32 v123,v20,v21
v_fma_f32 v22,-v98,v110,v22
v_fma_f32 v23,-v98,v111,v23
v_fma_f32 v24,-v98,v112,v24
v_fma_f32 v25,-v98,v113,v25
v_mul_f32 v118,v22,v22
v_mul_f32 v119,v23,v23
v_mul_f32 v120,v24,v24
v_mul_f32 v121,v25,v25
v_fma_f32 v118,v100,v118,v99
v_fma_f32 v119,v100,v119,v99
v_fma_f32 v120,v100,v120,v99
v_fma_f32 v121,v100,v121,v99
v_mul_f32 v118,v22,v118
v_mul_f32 v119,v23,v119
v_mul_f32 v120,v24,v120
v_mul_f32 v121,v25,v121
v_exp_f32 v118,v118
v_exp_f32 v119,v119
v_exp_f32 v120,v120
v_exp_f32 v121,v121
v_fma_f32 v118,v118,v101,v101
v_fma_f32 v119,v119,v101,v101
v_fma_f32 v120,v120,v101,v101
v_fma_f32 v121,v121,v101,v101
v_rcp_f32 v118,v118
v_rcp_f32 v119,v119
v_rcp_f32 v120,v120
v_rcp_f32 v121,v121
v_mul_f32 v22,v22,v118
v_mul_f32 v23,v23,v119
v_mul_f32 v24,v24,v120
v_mul_f32 v25,v25,v121
v_cvt_pk_bf16_f32 v124,v22,v23
v_cvt_pk_bf16_f32 v125,v24,v25
buffer_store_dwordx4 v[122:125],v15,s[36:39],s66 offen offset:0 sc0 sc1
buffer_store_dwordx4 v[122:125],v16,s[36:39],s66 offen offset:0 sc0 sc1
s_mov_b32 s72,s69
s_mov_b32 s69,0x7fff0000
s_mov_b32 s66,0x7fff0000
s_mov_b32 s76,0x7fff0000
s_branch Lg1_done_%=
Lg1_L1_%=:
v_mfma_f32_16x16x32_bf16 v[30:33],a[4:7],v[34:37],0
s_add_u32 m0,s59,3072
s_mov_b32 s67,s65
R 54,2,5120
M 30,a[12:15],38
buffer_load_dword v8,s[36:39],s58 offen offset:0 sc1 lds
v_mfma_f32_16x16x32_bf16 v[26:29],a[0:3],v[34:37],0
s_add_u32 s65,s65,s62
R 58,2,6144
v_fma_f32 v18,-v98,v106,v18
M 26,a[8:11],38
L 3
v_fma_f32 v19,-v98,v107,v19
v_fma_f32 v20,-v98,v108,v20
M 26,a[16:19],42
R 62,2,7168
v_fma_f32 v21,-v98,v109,v21
M 26,a[24:27],46
v_mul_f32 v114,v18,v18
v_mul_f32 v115,v19,v19
M 30,a[20:23],42
R 66,2,8192
v_mul_f32 v116,v20,v20
M 30,a[28:31],46
L 3
v_mul_f32 v117,v21,v21
v_fma_f32 v114,v100,v114,v99
M 30,a[36:39],50
R 70,2,9216
v_fma_f32 v115,v100,v115,v99
M 30,a[44:47],54
v_fma_f32 v116,v100,v116,v99
v_fma_f32 v117,v100,v117,v99
M 26,a[32:35],50
R 74,2,10240
v_mul_f32 v114,v18,v114
M 26,a[40:43],54
L 3
v_exp_f32 v114,v114
M 26,a[48:51],58
R 78,2,11264
v_mul_f32 v115,v19,v115
M 26,a[56:59],62
v_exp_f32 v115,v115
M 30,a[52:55],58
R 82,2,12288
v_mul_f32 v116,v20,v116
M 30,a[60:63],62
L 3
v_exp_f32 v116,v116
M 30,a[68:71],66
R 86,2,13312
v_mul_f32 v117,v21,v117
M 30,a[76:79],70
buffer_load_dwordx4 v[102:105],v11,s[36:39],s73 offen offset:0 sc1
M 26,a[64:67],66
s_add_u32 s73,s73,s64
R 90,2,14336
v_fma_f32 v114,v114,v101,v101
M 26,a[72:75],70
L 3
v_exp_f32 v117,v117
M 26,a[80:83],74
s_waitcnt vmcnt(1)
R 94,2,15360
v_fma_f32 v115,v115,v101,v101
M 26,a[88:91],78
s_barrier
ds_read_b32 v7,v6 offset:3072
v_fma_f32 v116,v116,v101,v101
M 30,a[84:87],74
R 34,2,16384
v_fma_f32 v117,v117,v101,v101
M 30,a[92:95],78
L 4
buffer_store_dword v4,v3,s[36:39],s70 offen offset:0 sc0 sc1
M 30,a[100:103],82
s_mov_b32 s70,0x7fff0000
R 38,2,17408
v_fma_f32 v22,-v98,v110,v22
M 30,a[108:111],86
v_rcp_f32 v114,v114
M 26,a[96:99],82
R 42,2,18432
v_mul_f32 v18,v18,v114
M 26,a[104:107],86
L 4
L 3
v_cmp_eq_u32 vcc,s53,v7
M 26,a[112:115],90
R 46,2,19456
v_fma_f32 v23,-v98,v111,v23
M 26,a[120:123],94
v_rcp_f32 v115,v115
M 30,a[116:119],90
s_cmp_eq_u32 vcc_lo,-1
R 50,2,20480
v_mul_f32 v19,v19,v115
s_cbranch_scc0 Lg1_X1_%=
M 30,a[124:127],94
L 3
s_add_u32 m0,s57,0
s_add_u32 s56,s55,0
M 30,a[132:135],34
s_add_u32 s58,s58,s61
R 54,2,21504
v_cvt_pk_bf16_f32 v122,v18,v19
M 30,a[140:143],38
D 0,56,0
M 26,a[128:131],34
s_add_u32 s54,s54,s46
R 58,2,22528
v_fma_f32 v24,-v98,v112,v24
M 26,a[136:139],38
L 3
v_rcp_f32 v116,v116
M 26,a[144:147],42
R 62,2,23552
v_mul_f32 v20,v20,v116
M 26,a[152:155],46
D 0,56,1024
M 30,a[148:151],42
R 66,2,24576
v_fma_f32 v25,-v98,v113,v25
M 30,a[156:159],46
L 3
v_rcp_f32 v117,v117
M 30,a[164:167],50
R 70,2,25600
v_mul_f32 v21,v21,v117
M 30,a[172:175],54
D 0,56,2048
M 26,a[160:163],50
R 74,2,26624
v_cvt_pk_bf16_f32 v123,v20,v21
M 26,a[168:171],54
L 3
v_mul_f32 v118,v22,v22
v_mul_f32 v119,v23,v23
M 26,a[176:179],58
R 78,2,27648
v_mul_f32 v120,v24,v24
M 26,a[184:187],62
D 0,56,3072
M 30,a[180:183],58
s_add_u32 m0,s59,1024
R 82,2,28672
v_mul_f32 v121,v25,v25
M 30,a[188:191],62
L 3
buffer_load_dword v8,s[36:39],s58 offen offset:0 sc1 lds
M 30,a[196:199],66
s_add_u32 m0,s57,16384
R 86,2,29696
v_fma_f32 v118,v100,v118,v99
M 30,a[204:207],70
s_add_u32 s56,s55,16384
v_fma_f32 v119,v100,v119,v99
v_fma_f32 v120,v100,v120,v99
M 26,a[192:195],66
s_add_u32 s55,s55,s60
R 90,2,30720
v_fma_f32 v121,v100,v121,v99
M 26,a[200:203],70
L 3
D 0,56,0
M 26,a[208:211],74
s_waitcnt vmcnt(1)
R 94,2,31744
v_mul_f32 v118,v22,v118
M 26,a[216:219],78
s_barrier
ds_read_b32 v7,v6 offset:1024
v_mul_f32 v119,v23,v119
M 30,a[212:215],74
s_sub_u32 s57,s84,s57
R 34,1,0
v_mul_f32 v120,v24,v120
M 30,a[220:223],78
L 4
buffer_store_dword v4,v3,s[36:39],s70 offen offset:0 sc0 sc1
v_mul_f32 v121,v25,v121
v_exp_f32 v118,v118
v_exp_f32 v119,v119
v_exp_f32 v120,v120
v_exp_f32 v121,v121
M 30,a[228:231],82
s_mov_b32 s70,s69
R 38,1,1024
v_fma_f32 v118,v118,v101,v101
v_fma_f32 v119,v119,v101,v101
v_fma_f32 v120,v120,v101,v101
v_fma_f32 v121,v121,v101,v101
v_rcp_f32 v118,v118
v_rcp_f32 v119,v119
v_rcp_f32 v120,v120
v_rcp_f32 v121,v121
M 30,a[236:239],86
s_mov_b32 s69,s68
v_mul_f32 v22,v22,v118
v_mul_f32 v23,v23,v119
v_mul_f32 v24,v24,v120
v_mul_f32 v25,v25,v121
v_cvt_pk_bf16_f32 v124,v22,v23
v_cvt_pk_bf16_f32 v125,v24,v25
M 26,a[224:227],82
s_add_u32 s68,s68,s63
R 42,1,2048
buffer_store_dwordx4 v[122:125],v15,s[36:39],s66 offen offset:0 sc0 sc1
M 26,a[232:235],86
L 4
L 3
M 26,a[240:243],90
R 46,1,3072
v_cmp_eq_u32 vcc,s53,v7
M 26,a[248:251],94
D 0,56,1024
M 30,a[244:247],90
L 2
s_cmp_eq_u32 vcc_lo,-1
R 50,1,4096
buffer_store_dwordx4 v[122:125],v16,s[36:39],s66 offen offset:0 sc0 sc1
M 30,a[252:255],94
D 0,56,2048
D 0,56,3072
buffer_store_dword v4,v3,s[36:39],s71 offen offset:0 sc0 sc1
buffer_store_dword v4,v3,s[36:39],s71 offen offset:0 sc0 sc1
buffer_store_dword v4,v3,s[36:39],s71 offen offset:0 sc0 sc1
s_cbranch_scc1 Lg1_F0_%=
s_branch Lg1_L0_%=
Lg1_X1_%=:
M 30,a[124:127],94
L 3
s_add_u32 m0,s59,1024
v_cvt_pk_bf16_f32 v122,v18,v19
M 30,a[132:135],34
R 54,2,21504
v_fma_f32 v24,-v98,v112,v24
M 30,a[140:143],38
buffer_load_dword v8,s[36:39],s58 offen offset:0 sc1 lds
M 26,a[128:131],34
R 58,2,22528
v_fma_f32 v25,-v98,v113,v25
M 26,a[136:139],38
L 3
v_rcp_f32 v116,v116
M 26,a[144:147],42
R 62,2,23552
v_mul_f32 v20,v20,v116
M 26,a[152:155],46
v_rcp_f32 v117,v117
M 30,a[148:151],42
R 66,2,24576
v_mul_f32 v21,v21,v117
M 30,a[156:159],46
L 3
v_cvt_pk_bf16_f32 v123,v20,v21
v_mul_f32 v118,v22,v22
M 30,a[164:167],50
R 70,2,25600
v_mul_f32 v119,v23,v23
M 30,a[172:175],54
v_mul_f32 v120,v24,v24
v_mul_f32 v121,v25,v25
M 26,a[160:163],50
R 74,2,26624
v_fma_f32 v118,v100,v118,v99
M 26,a[168:171],54
L 3
v_fma_f32 v119,v100,v119,v99
v_fma_f32 v120,v100,v120,v99
M 26,a[176:179],58
R 78,2,27648
v_fma_f32 v121,v100,v121,v99
M 26,a[184:187],62
v_mul_f32 v118,v22,v118
v_mul_f32 v119,v23,v119
M 30,a[180:183],58
R 82,2,28672
v_mul_f32 v120,v24,v120
M 30,a[188:191],62
)"
R"(L 3
v_exp_f32 v118,v118
M 30,a[196:199],66
R 86,2,29696
v_mul_f32 v121,v25,v121
M 30,a[204:207],70
v_exp_f32 v119,v119
M 26,a[192:195],66
R 90,2,30720
v_fma_f32 v118,v118,v101,v101
M 26,a[200:203],70
L 3
v_exp_f32 v120,v120
M 26,a[208:211],74
s_waitcnt vmcnt(0)
R 94,2,31744
v_fma_f32 v119,v119,v101,v101
M 26,a[216:219],78
s_barrier
buffer_store_dword v4,v3,s[36:39],s70 offen offset:0 sc0 sc1
M 30,a[212:215],74
s_mov_b32 s70,s69
ds_read_b32 v7,v6 offset:1024
v_fma_f32 v120,v120,v101,v101
M 30,a[220:223],78
L 3
v_exp_f32 v121,v121
M 30,a[228:231],82
s_mov_b32 s69,s68
v_fma_f32 v121,v121,v101,v101
v_rcp_f32 v118,v118
v_rcp_f32 v119,v119
v_rcp_f32 v120,v120
v_rcp_f32 v121,v121
M 30,a[236:239],86
s_add_u32 s68,s68,s63
v_mul_f32 v22,v22,v118
v_mul_f32 v23,v23,v119
v_mul_f32 v24,v24,v120
v_mul_f32 v25,v25,v121
v_cvt_pk_bf16_f32 v124,v22,v23
v_cvt_pk_bf16_f32 v125,v24,v25
M 26,a[224:227],82
buffer_store_dwordx4 v[122:125],v15,s[36:39],s66 offen offset:0 sc0 sc1
M 26,a[232:235],86
L 1
M 26,a[240:243],90
L 0
v_cmp_eq_u32 vcc,s53,v7
M 26,a[248:251],94
buffer_store_dwordx4 v[122:125],v16,s[36:39],s66 offen offset:0 sc0 sc1
M 30,a[244:247],90
s_nop 0
M 30,a[252:255],94
s_mov_b32 s80,0
s_cmp_eq_u32 vcc_lo,-1
s_cbranch_scc0 Lg1_xn1_%=
s_add_u32 m0,s57,0
s_add_u32 s56,s55,0
D 0,56,0
v_fma_f32 v26,-v102,v106,v26
v_fma_f32 v27,-v102,v107,v27
v_fma_f32 v28,-v102,v108,v28
v_fma_f32 v29,-v102,v109,v29
v_mul_f32 v114,v26,v26
v_mul_f32 v115,v27,v27
v_mul_f32 v116,v28,v28
D 0,56,1024
v_mul_f32 v117,v29,v29
v_fma_f32 v114,v104,v114,v103
v_fma_f32 v115,v104,v115,v103
v_fma_f32 v116,v104,v116,v103
v_fma_f32 v117,v104,v117,v103
v_mul_f32 v114,v26,v114
v_mul_f32 v115,v27,v115
D 0,56,2048
v_mul_f32 v116,v28,v116
v_mul_f32 v117,v29,v117
v_exp_f32 v114,v114
v_exp_f32 v115,v115
v_exp_f32 v116,v116
v_exp_f32 v117,v117
v_fma_f32 v114,v114,v105,v105
D 0,56,3072
v_fma_f32 v115,v115,v105,v105
v_fma_f32 v116,v116,v105,v105
v_fma_f32 v117,v117,v105,v105
v_rcp_f32 v114,v114
v_rcp_f32 v115,v115
v_rcp_f32 v116,v116
v_rcp_f32 v117,v117
s_add_u32 s58,s58,s61
s_add_u32 m0,s57,16384
s_add_u32 s56,s55,16384
D 0,56,0
v_mul_f32 v26,v26,v114
v_mul_f32 v27,v27,v115
v_mul_f32 v28,v28,v116
v_mul_f32 v29,v29,v117
v_cvt_pk_bf16_f32 v122,v26,v27
v_cvt_pk_bf16_f32 v123,v28,v29
v_fma_f32 v30,-v102,v110,v30
D 0,56,1024
v_fma_f32 v31,-v102,v111,v31
v_fma_f32 v32,-v102,v112,v32
v_fma_f32 v33,-v102,v113,v33
v_mul_f32 v118,v30,v30
v_mul_f32 v119,v31,v31
v_mul_f32 v120,v32,v32
v_mul_f32 v121,v33,v33
D 0,56,2048
v_fma_f32 v118,v104,v118,v103
v_fma_f32 v119,v104,v119,v103
v_fma_f32 v120,v104,v120,v103
v_fma_f32 v121,v104,v121,v103
v_mul_f32 v118,v30,v118
v_mul_f32 v119,v31,v119
v_mul_f32 v120,v32,v120
D 0,56,3072
v_mul_f32 v121,v33,v121
v_exp_f32 v118,v118
v_exp_f32 v119,v119
v_exp_f32 v120,v120
v_exp_f32 v121,v121
v_fma_f32 v118,v118,v105,v105
v_fma_f32 v119,v119,v105,v105
s_add_u32 s55,s55,s60
s_sub_u32 s57,s84,s57
s_add_u32 s54,s54,s46
v_fma_f32 v120,v120,v105,v105
v_fma_f32 v121,v121,v105,v105
v_rcp_f32 v118,v118
v_rcp_f32 v119,v119
v_rcp_f32 v120,v120
v_rcp_f32 v121,v121
v_mul_f32 v30,v30,v118
v_mul_f32 v31,v31,v119
v_mul_f32 v32,v32,v120
v_mul_f32 v33,v33,v121
v_cvt_pk_bf16_f32 v124,v30,v31
v_cvt_pk_bf16_f32 v125,v32,v33
buffer_store_dwordx4 v[122:125],v15,s[36:39],s67 offen offset:0 sc0 sc1
buffer_store_dwordx4 v[122:125],v16,s[36:39],s67 offen offset:0 sc0 sc1
s_mov_b32 s72,s69
s_mov_b32 s69,0x7fff0000
s_mov_b32 s67,0x7fff0000
s_mov_b32 s77,0x7fff0000
s_branch Lg1_gox_%=
Lg1_xn1_%=:
s_cmp_ge_u32 s54,256
s_cbranch_scc1 Lg1_fin1_%=
s_add_u32 m0,s59,4096
s_nop 0
buffer_load_dword v8,s[36:39],s58 offen offset:0 sc1 lds
buffer_store_dword v4,v3,s[36:39],s71 offen offset:0 sc0 sc1
s_add_u32 m0,s59,5120
s_nop 0
buffer_load_dword v8,s[36:39],s58 offen offset:0 sc1 lds
buffer_store_dword v4,v3,s[36:39],s71 offen offset:0 sc0 sc1
s_mov_b32 s83,0
v_fma_f32 v26,-v102,v106,v26
v_fma_f32 v27,-v102,v107,v27
v_fma_f32 v28,-v102,v108,v28
v_fma_f32 v29,-v102,v109,v29
v_mul_f32 v114,v26,v26
v_mul_f32 v115,v27,v27
v_mul_f32 v116,v28,v28
v_mul_f32 v117,v29,v29
v_fma_f32 v114,v104,v114,v103
v_fma_f32 v115,v104,v115,v103
v_fma_f32 v116,v104,v116,v103
v_fma_f32 v117,v104,v117,v103
v_mul_f32 v114,v26,v114
v_mul_f32 v115,v27,v115
v_mul_f32 v116,v28,v116
v_mul_f32 v117,v29,v117
v_exp_f32 v114,v114
v_exp_f32 v115,v115
v_exp_f32 v116,v116
v_exp_f32 v117,v117
v_fma_f32 v114,v114,v105,v105
v_fma_f32 v115,v115,v105,v105
v_fma_f32 v116,v116,v105,v105
v_fma_f32 v117,v117,v105,v105
v_rcp_f32 v114,v114
v_rcp_f32 v115,v115
v_rcp_f32 v116,v116
v_rcp_f32 v117,v117
v_mul_f32 v26,v26,v114
v_mul_f32 v27,v27,v115
v_mul_f32 v28,v28,v116
v_mul_f32 v29,v29,v117
v_cvt_pk_bf16_f32 v122,v26,v27
v_cvt_pk_bf16_f32 v123,v28,v29
v_fma_f32 v30,-v102,v110,v30
v_fma_f32 v31,-v102,v111,v31
v_fma_f32 v32,-v102,v112,v32
v_fma_f32 v33,-v102,v113,v33
v_mul_f32 v118,v30,v30
v_mul_f32 v119,v31,v31
v_mul_f32 v120,v32,v32
v_mul_f32 v121,v33,v33
v_fma_f32 v118,v104,v118,v103
v_fma_f32 v119,v104,v119,v103
v_fma_f32 v120,v104,v120,v103
v_fma_f32 v121,v104,v121,v103
v_mul_f32 v118,v30,v118
v_mul_f32 v119,v31,v119
v_mul_f32 v120,v32,v120
v_mul_f32 v121,v33,v121
v_exp_f32 v118,v118
v_exp_f32 v119,v119
v_exp_f32 v120,v120
v_exp_f32 v121,v121
v_fma_f32 v118,v118,v105,v105
v_fma_f32 v119,v119,v105,v105
v_fma_f32 v120,v120,v105,v105
v_fma_f32 v121,v121,v105,v105
v_rcp_f32 v118,v118
v_rcp_f32 v119,v119
v_rcp_f32 v120,v120
v_rcp_f32 v121,v121
v_mul_f32 v30,v30,v118
v_mul_f32 v31,v31,v119
v_mul_f32 v32,v32,v120
v_mul_f32 v33,v33,v121
v_cvt_pk_bf16_f32 v124,v30,v31
v_cvt_pk_bf16_f32 v125,v32,v33
buffer_store_dwordx4 v[122:125],v15,s[36:39],s67 offen offset:0 sc0 sc1
buffer_store_dwordx4 v[122:125],v16,s[36:39],s67 offen offset:0 sc0 sc1
s_mov_b32 s72,s69
s_mov_b32 s69,0x7fff0000
s_mov_b32 s67,0x7fff0000
s_mov_b32 s77,0x7fff0000
s_branch Lg1_poll_%=
Lg1_fin1_%=:
v_fma_f32 v26,-v102,v106,v26
v_fma_f32 v27,-v102,v107,v27
v_fma_f32 v28,-v102,v108,v28
v_fma_f32 v29,-v102,v109,v29
v_mul_f32 v114,v26,v26
v_mul_f32 v115,v27,v27
v_mul_f32 v116,v28,v28
v_mul_f32 v117,v29,v29
v_fma_f32 v114,v104,v114,v103
v_fma_f32 v115,v104,v115,v103
v_fma_f32 v116,v104,v116,v103
v_fma_f32 v117,v104,v117,v103
v_mul_f32 v114,v26,v114
v_mul_f32 v115,v27,v115
v_mul_f32 v116,v28,v116
v_mul_f32 v117,v29,v117
v_exp_f32 v114,v114
v_exp_f32 v115,v115
v_exp_f32 v116,v116
v_exp_f32 v117,v117
v_fma_f32 v114,v114,v105,v105
v_fma_f32 v115,v115,v105,v105
v_fma_f32 v116,v116,v105,v105
v_fma_f32 v117,v117,v105,v105
v_rcp_f32 v114,v114
v_rcp_f32 v115,v115
v_rcp_f32 v116,v116
v_rcp_f32 v117,v117
v_mul_f32 v26,v26,v114
v_mul_f32 v27,v27,v115
v_mul_f32 v28,v28,v116
v_mul_f32 v29,v29,v117
v_cvt_pk_bf16_f32 v122,v26,v27
v_cvt_pk_bf16_f32 v123,v28,v29
v_fma_f32 v30,-v102,v110,v30
v_fma_f32 v31,-v102,v111,v31
v_fma_f32 v32,-v102,v112,v32
v_fma_f32 v33,-v102,v113,v33
v_mul_f32 v118,v30,v30
v_mul_f32 v119,v31,v31
v_mul_f32 v120,v32,v32
v_mul_f32 v121,v33,v33
v_fma_f32 v118,v104,v118,v103
v_fma_f32 v119,v104,v119,v103
v_fma_f32 v120,v104,v120,v103
v_fma_f32 v121,v104,v121,v103
v_mul_f32 v118,v30,v118
v_mul_f32 v119,v31,v119
v_mul_f32 v120,v32,v120
v_mul_f32 v121,v33,v121
v_exp_f32 v118,v118
v_exp_f32 v119,v119
v_exp_f32 v120,v120
v_exp_f32 v121,v121
v_fma_f32 v118,v118,v105,v105
v_fma_f32 v119,v119,v105,v105
v_fma_f32 v120,v120,v105,v105
v_fma_f32 v121,v121,v105,v105
v_rcp_f32 v118,v118
v_rcp_f32 v119,v119
v_rcp_f32 v120,v120
v_rcp_f32 v121,v121
v_mul_f32 v30,v30,v118
v_mul_f32 v31,v31,v119
v_mul_f32 v32,v32,v120
v_mul_f32 v33,v33,v121
v_cvt_pk_bf16_f32 v124,v30,v31
v_cvt_pk_bf16_f32 v125,v32,v33
buffer_store_dwordx4 v[122:125],v15,s[36:39],s67 offen offset:0 sc0 sc1
buffer_store_dwordx4 v[122:125],v16,s[36:39],s67 offen offset:0 sc0 sc1
s_mov_b32 s72,s69
s_mov_b32 s69,0x7fff0000
s_mov_b32 s67,0x7fff0000
s_mov_b32 s77,0x7fff0000
s_branch Lg1_done_%=
Lg1_done_%=:
s_waitcnt vmcnt(2)
s_barrier
buffer_store_dword v4,v3,s[36:39],s70 offen offset:0 sc0 sc1
s_waitcnt vmcnt(1)
s_barrier
buffer_store_dword v4,v3,s[36:39],s72 offen offset:0 sc0 sc1
s_waitcnt vmcnt(0) lgkmcnt(0)
s_barrier
s_branch Lg2_end_%=
Lg2_begin_%=:
Lcode_g2_b:
s_mov_b64 s[36:37],%1
s_mov_b32 s38,111243392
s_mov_b32 s39,0x00020000
s_mov_b32 s44,%2
s_mov_b32 s45,%3
s_mov_b32 s46,%4
s_mov_b32 s47,%5
s_mov_b32 s48,%6
s_mov_b32 s49,%7
s_mov_b32 s50,%8
s_mov_b32 s51,%9
s_mov_b32 s53,%10
v_mov_b32 v0,%11
v_mov_b32 v3,%12
v_mov_b32 v5,%13
v_mov_b32 v6,%14
v_mov_b32 v18,%15
v_mov_b32 v19,%16
v_mov_b32 v20,%17
v_mov_b32 v21,%18
v_mov_b32 v9,%19
v_mov_b32 v10,%20
v_mov_b32 v11,%21
v_mov_b32 v12,%22
v_mov_b32 v13,%23
v_mov_b32 v14,%24
v_mov_b32 v15,%25
v_mov_b32 v16,%26
s_mov_b64 s[40:41],%27
s_mov_b32 s42,2097152
s_mov_b32 s43,0x00020000
s_lshl_b32 s57,s44,11
s_cmp_ge_u32 s45,8
s_cbranch_scc1 Lg2_dly_%=
s_sleep 16
s_sleep 16
s_sleep 16
s_sleep 16
Lg2_dly_%=:
s_add_u32 s55,s50,0
s_add_u32 m0,s57,0
s_nop 0
buffer_load_dwordx4 v18,s[40:43],s55 offen offset:0  lds
s_add_u32 m0,s57,1024
s_nop 0
buffer_load_dwordx4 v19,s[40:43],s55 offen offset:0  lds
s_add_u32 m0,s57,8064
s_nop 0
buffer_load_dwordx4 v18,s[40:43],s55 offen offset:128  lds
s_add_u32 m0,s57,9088
s_nop 0
buffer_load_dwordx4 v19,s[40:43],s55 offen offset:128  lds
s_add_u32 s55,s50,131072
s_add_u32 m0,s57,16384
s_nop 0
buffer_load_dwordx4 v18,s[40:43],s55 offen offset:0  lds
s_add_u32 m0,s57,17408
s_nop 0
buffer_load_dwordx4 v19,s[40:43],s55 offen offset:0  lds
s_add_u32 m0,s57,24448
s_nop 0
buffer_load_dwordx4 v18,s[40:43],s55 offen offset:128  lds
s_add_u32 m0,s57,25472
s_nop 0
buffer_load_dwordx4 v19,s[40:43],s55 offen offset:128  lds
s_add_u32 s55,s50,262144
s_add_u32 m0,s57,32768
s_nop 0
buffer_load_dwordx4 v18,s[40:43],s55 offen offset:0  lds
s_add_u32 m0,s57,33792
s_nop 0
buffer_load_dwordx4 v19,s[40:43],s55 offen offset:0  lds
s_add_u32 m0,s57,40832
s_nop 0
buffer_load_dwordx4 v18,s[40:43],s55 offen offset:128  lds
s_add_u32 m0,s57,41856
s_nop 0
buffer_load_dwordx4 v19,s[40:43],s55 offen offset:128  lds
s_add_u32 s55,s50,393216
s_add_u32 m0,s57,49152
s_nop 0
buffer_load_dwordx4 v18,s[40:43],s55 offen offset:0  lds
s_add_u32 m0,s57,50176
s_nop 0
buffer_load_dwordx4 v19,s[40:43],s55 offen offset:0  lds
s_add_u32 m0,s57,57216
s_nop 0
buffer_load_dwordx4 v18,s[40:43],s55 offen offset:128  lds
s_add_u32 m0,s57,58240
s_nop 0
buffer_load_dwordx4 v19,s[40:43],s55 offen offset:128  lds
s_add_u32 s55,s50,524288
s_add_u32 m0,s57,65536
s_nop 0
buffer_load_dwordx4 v18,s[40:43],s55 offen offset:0  lds
s_add_u32 m0,s57,66560
s_nop 0
buffer_load_dwordx4 v19,s[40:43],s55 offen offset:0  lds
s_add_u32 m0,s57,73600
s_nop 0
buffer_load_dwordx4 v18,s[40:43],s55 offen offset:128  lds
s_add_u32 m0,s57,74624
s_nop 0
buffer_load_dwordx4 v19,s[40:43],s55 offen offset:128  lds
s_add_u32 s55,s50,655360
s_add_u32 m0,s57,81920
s_nop 0
buffer_load_dwordx4 v18,s[40:43],s55 offen offset:0  lds
s_add_u32 m0,s57,82944
s_nop 0
buffer_load_dwordx4 v19,s[40:43],s55 offen offset:0  lds
s_add_u32 m0,s57,89984
s_nop 0
buffer_load_dwordx4 v18,s[40:43],s55 offen offset:128  lds
s_add_u32 m0,s57,91008
s_nop 0
buffer_load_dwordx4 v19,s[40:43],s55 offen offset:128  lds
s_waitcnt vmcnt(20) lgkmcnt(0)
s_barrier
s_add_u32 s55,s50,786432
s_add_u32 m0,s57,98304
s_nop 0
buffer_load_dwordx4 v18,s[40:43],s55 offen offset:0  lds
s_add_u32 m0,s57,99328
s_nop 0
buffer_load_dwordx4 v19,s[40:43],s55 offen offset:0  lds
s_add_u32 m0,s57,106368
s_nop 0
buffer_load_dwordx4 v18,s[40:43],s55 offen offset:128  lds
s_add_u32 m0,s57,107392
s_nop 0
buffer_load_dwordx4 v19,s[40:43],s55 offen offset:128  lds
v_add_u32 v26,0,v20
v_add_u32 v27,0,v21
T a[0:1],26,0
T a[2:3],26,512
T a[4:5],27,0
T a[6:7],27,512
T a[8:9],26,4096
T a[10:11],26,4608
T a[12:13],27,4096
T a[14:15],27,4608
s_waitcnt vmcnt(20) lgkmcnt(0)
s_barrier
s_add_u32 s55,s50,917504
s_add_u32 m0,s57,0
s_nop 0
buffer_load_dwordx4 v18,s[40:43],s55 offen offset:0  lds
s_add_u32 m0,s57,1024
s_nop 0
buffer_load_dwordx4 v19,s[40:43],s55 offen offset:0  lds
s_add_u32 m0,s57,8064
s_nop 0
buffer_load_dwordx4 v18,s[40:43],s55 offen offset:128  lds
s_add_u32 m0,s57,9088
s_nop 0
buffer_load_dwordx4 v19,s[40:43],s55 offen offset:128  lds
v_add_u32 v26,16384,v20
v_add_u32 v27,16384,v21
T a[16:17],26,0
T a[18:19],26,512
T a[20:21],27,0
T a[22:23],27,512
T a[24:25],26,4096
T a[26:27],26,4608
T a[28:29],27,4096
T a[30:31],27,4608
s_waitcnt vmcnt(20) lgkmcnt(0)
s_barrier
s_add_u32 s55,s50,1048576
s_add_u32 m0,s57,16384
s_nop 0
buffer_load_dwordx4 v18,s[40:43],s55 offen offset:0  lds
s_add_u32 m0,s57,17408
s_nop 0
buffer_load_dwordx4 v19,s[40:43],s55 offen offset:0  lds
s_add_u32 m0,s57,24448
)"
R"(s_nop 0
buffer_load_dwordx4 v18,s[40:43],s55 offen offset:128  lds
s_add_u32 m0,s57,25472
s_nop 0
buffer_load_dwordx4 v19,s[40:43],s55 offen offset:128  lds
v_add_u32 v26,32768,v20
v_add_u32 v27,32768,v21
T a[32:33],26,0
T a[34:35],26,512
T a[36:37],27,0
T a[38:39],27,512
T a[40:41],26,4096
T a[42:43],26,4608
T a[44:45],27,4096
T a[46:47],27,4608
s_waitcnt vmcnt(20) lgkmcnt(0)
s_barrier
s_add_u32 s55,s50,1179648
s_add_u32 m0,s57,32768
s_nop 0
buffer_load_dwordx4 v18,s[40:43],s55 offen offset:0  lds
s_add_u32 m0,s57,33792
s_nop 0
buffer_load_dwordx4 v19,s[40:43],s55 offen offset:0  lds
s_add_u32 m0,s57,40832
s_nop 0
buffer_load_dwordx4 v18,s[40:43],s55 offen offset:128  lds
s_add_u32 m0,s57,41856
s_nop 0
buffer_load_dwordx4 v19,s[40:43],s55 offen offset:128  lds
v_add_u32 v26,49152,v20
v_add_u32 v27,49152,v21
T a[48:49],26,0
T a[50:51],26,512
T a[52:53],27,0
T a[54:55],27,512
T a[56:57],26,4096
T a[58:59],26,4608
T a[60:61],27,4096
T a[62:63],27,4608
s_waitcnt vmcnt(20) lgkmcnt(0)
s_barrier
s_add_u32 s55,s50,1310720
s_add_u32 m0,s57,49152
s_nop 0
buffer_load_dwordx4 v18,s[40:43],s55 offen offset:0  lds
s_add_u32 m0,s57,50176
s_nop 0
buffer_load_dwordx4 v19,s[40:43],s55 offen offset:0  lds
s_add_u32 m0,s57,57216
s_nop 0
buffer_load_dwordx4 v18,s[40:43],s55 offen offset:128  lds
s_add_u32 m0,s57,58240
s_nop 0
buffer_load_dwordx4 v19,s[40:43],s55 offen offset:128  lds
v_add_u32 v26,65536,v20
v_add_u32 v27,65536,v21
T a[64:65],26,0
T a[66:67],26,512
T a[68:69],27,0
T a[70:71],27,512
T a[72:73],26,4096
T a[74:75],26,4608
T a[76:77],27,4096
T a[78:79],27,4608
s_waitcnt vmcnt(20) lgkmcnt(0)
s_barrier
s_add_u32 s55,s50,1441792
s_add_u32 m0,s57,65536
s_nop 0
buffer_load_dwordx4 v18,s[40:43],s55 offen offset:0  lds
s_add_u32 m0,s57,66560
s_nop 0
buffer_load_dwordx4 v19,s[40:43],s55 offen offset:0  lds
s_add_u32 m0,s57,73600
s_nop 0
buffer_load_dwordx4 v18,s[40:43],s55 offen offset:128  lds
s_add_u32 m0,s57,74624
s_nop 0
buffer_load_dwordx4 v19,s[40:43],s55 offen offset:128  lds
v_add_u32 v26,81920,v20
v_add_u32 v27,81920,v21
T a[80:81],26,0
T a[82:83],26,512
T a[84:85],27,0
T a[86:87],27,512
T a[88:89],26,4096
T a[90:91],26,4608
T a[92:93],27,4096
T a[94:95],27,4608
s_waitcnt vmcnt(20) lgkmcnt(0)
s_barrier
s_add_u32 s55,s50,1572864
s_add_u32 m0,s57,81920
s_nop 0
buffer_load_dwordx4 v18,s[40:43],s55 offen offset:0  lds
s_add_u32 m0,s57,82944
s_nop 0
buffer_load_dwordx4 v19,s[40:43],s55 offen offset:0  lds
s_add_u32 m0,s57,89984
s_nop 0
buffer_load_dwordx4 v18,s[40:43],s55 offen offset:128  lds
s_add_u32 m0,s57,91008
s_nop 0
buffer_load_dwordx4 v19,s[40:43],s55 offen offset:128  lds
v_add_u32 v26,98304,v20
v_add_u32 v27,98304,v21
T a[96:97],26,0
T a[98:99],26,512
T a[100:101],27,0
T a[102:103],27,512
T a[104:105],26,4096
T a[106:107],26,4608
T a[108:109],27,4096
T a[110:111],27,4608
s_waitcnt vmcnt(20) lgkmcnt(0)
s_barrier
s_add_u32 s55,s50,1703936
s_add_u32 m0,s57,98304
s_nop 0
buffer_load_dwordx4 v18,s[40:43],s55 offen offset:0  lds
s_add_u32 m0,s57,99328
s_nop 0
buffer_load_dwordx4 v19,s[40:43],s55 offen offset:0  lds
s_add_u32 m0,s57,106368
s_nop 0
buffer_load_dwordx4 v18,s[40:43],s55 offen offset:128  lds
s_add_u32 m0,s57,107392
s_nop 0
buffer_load_dwordx4 v19,s[40:43],s55 offen offset:128  lds
v_add_u32 v26,0,v20
v_add_u32 v27,0,v21
T a[112:113],26,0
T a[114:115],26,512
T a[116:117],27,0
T a[118:119],27,512
T a[120:121],26,4096
T a[122:123],26,4608
T a[124:125],27,4096
T a[126:127],27,4608
s_waitcnt vmcnt(20) lgkmcnt(0)
s_barrier
s_add_u32 s55,s50,1835008
s_add_u32 m0,s57,0
s_nop 0
buffer_load_dwordx4 v18,s[40:43],s55 offen offset:0  lds
s_add_u32 m0,s57,1024
s_nop 0
buffer_load_dwordx4 v19,s[40:43],s55 offen offset:0  lds
s_add_u32 m0,s57,8064
s_nop 0
buffer_load_dwordx4 v18,s[40:43],s55 offen offset:128  lds
s_add_u32 m0,s57,9088
s_nop 0
buffer_load_dwordx4 v19,s[40:43],s55 offen offset:128  lds
v_add_u32 v26,16384,v20
v_add_u32 v27,16384,v21
T a[128:129],26,0
T a[130:131],26,512
T a[132:133],27,0
T a[134:135],27,512
T a[136:137],26,4096
T a[138:139],26,4608
T a[140:141],27,4096
T a[142:143],27,4608
s_waitcnt vmcnt(20) lgkmcnt(0)
s_barrier
s_add_u32 s55,s50,1966080
s_add_u32 m0,s57,16384
s_nop 0
buffer_load_dwordx4 v18,s[40:43],s55 offen offset:0  lds
s_add_u32 m0,s57,17408
s_nop 0
buffer_load_dwordx4 v19,s[40:43],s55 offen offset:0  lds
s_add_u32 m0,s57,24448
s_nop 0
buffer_load_dwordx4 v18,s[40:43],s55 offen offset:128  lds
s_add_u32 m0,s57,25472
s_nop 0
buffer_load_dwordx4 v19,s[40:43],s55 offen offset:128  lds
v_add_u32 v26,32768,v20
v_add_u32 v27,32768,v21
T a[144:145],26,0
T a[146:147],26,512
T a[148:149],27,0
T a[150:151],27,512
T a[152:153],26,4096
T a[154:155],26,4608
T a[156:157],27,4096
T a[158:159],27,4608
s_waitcnt vmcnt(20) lgkmcnt(0)
s_barrier
v_add_u32 v26,49152,v20
v_add_u32 v27,49152,v21
T a[160:161],26,0
T a[162:163],26,512
T a[164:165],27,0
T a[166:167],27,512
T a[168:169],26,4096
T a[170:171],26,4608
T a[172:173],27,4096
T a[174:175],27,4608
s_waitcnt vmcnt(16) lgkmcnt(0)
s_barrier
v_add_u32 v26,65536,v20
v_add_u32 v27,65536,v21
T a[176:177],26,0
T a[178:179],26,512
T a[180:181],27,0
T a[182:183],27,512
T a[184:185],26,4096
T a[186:187],26,4608
T a[188:189],27,4096
T a[190:191],27,4608
s_waitcnt vmcnt(12) lgkmcnt(0)
s_barrier
v_add_u32 v26,81920,v20
v_add_u32 v27,81920,v21
T a[192:193],26,0
T a[194:195],26,512
T a[196:197],27,0
T a[198:199],27,512
T a[200:201],26,4096
T a[202:203],26,4608
T a[204:205],27,4096
T a[206:207],27,4608
s_waitcnt vmcnt(8) lgkmcnt(0)
s_barrier
v_add_u32 v26,98304,v20
v_add_u32 v27,98304,v21
T a[208:209],26,0
T a[210:211],26,512
T a[212:213],27,0
T a[214:215],27,512
T a[216:217],26,4096
T a[218:219],26,4608
T a[220:221],27,4096
T a[222:223],27,4608
s_waitcnt vmcnt(4) lgkmcnt(0)
s_barrier
v_add_u32 v26,0,v20
v_add_u32 v27,0,v21
T a[224:225],26,0
T a[226:227],26,512
T a[228:229],27,0
T a[230:231],27,512
T a[232:233],26,4096
T a[234:235],26,4608
T a[236:237],27,4096
T a[238:239],27,4608
s_waitcnt vmcnt(0) lgkmcnt(0)
s_barrier
v_add_u32 v26,16384,v20
v_add_u32 v27,16384,v21
T a[240:241],26,0
T a[242:243],26,512
T a[244:245],27,0
T a[246:247],27,512
T a[248:249],26,4096
T a[250:251],26,4608
T a[252:253],27,4096
T a[254:255],27,4608
L 0
s_barrier
v_mov_b32 v4,s53
s_mov_b32 s82,0
s_mov_b32 s80,0
s_mov_b32 s54,s45
s_mul_i32 s57,s44,5120
s_mul_i32 s55,s45,40960
s_add_u32 s55,s55,s57
s_add_u32 s55,s55,17825792
s_mul_i32 s57,s44,4096
s_mul_i32 s60,s46,40960
s_mul_i32 s61,s46,1024
s_mul_i32 s62,s46,32768
s_mul_i32 s63,s46,1024
s_mul_i32 s64,s46,512
s_lshl_b32 s59,s44,8
s_add_u32 s59,s59,131072
s_mul_i32 s58,s45,1024
s_add_u32 s58,s58,109333504
s_mul_i32 s65,s45,32768
s_add_u32 s65,s65,s47
s_mul_i32 s68,s45,1024
s_add_u32 s68,s68,s48
s_mul_i32 s73,s45,512
s_add_u32 s73,s73,106954752
s_mul_i32 s74,s45,32768
s_add_u32 s74,s74,s49
s_mul_i32 s75,s45,1024
s_add_u32 s75,s75,107479040
s_mov_b32 s66,0x7fff0000
s_mov_b32 s67,0x7fff0000
s_mov_b32 s69,0x7fff0000
s_mov_b32 s76,0x7fff0000
s_mov_b32 s77,0x7fff0000
s_mov_b32 s70,0x7fff0000
s_mov_b32 s71,0x7fff0000
s_mov_b32 s72,0x7fff0000
v_mov_b32 v1,v0
v_add_u32 v2,16384,v0
s_mov_b32 s84,16384
s_mov_b32 s86,0x3a800000
s_mov_b32 s87,0x38a7c5ac
s_mov_b32 s88,0xc0135761
s_mov_b32 s89,0x3d372713
s_lshr_b32 s85,s44,1
s_mul_i32 s85,s85,127
v_lshlrev_b32 v8,22,v0
v_and_b32 v8,0x80000000,v8
v_or_b32 v8,v8,v5
v_add_u32 v2,32768,v0
s_lshl_b32 s84,s57,1
s_add_u32 s84,s84,32768
s_getpc_b64 s[40:41]
Lg2_pc_%=:
s_lshl_b32 s78,s44,13
v_lshlrev_b32 v106,3,v0
v_add_u32 v106,s78,v106
v_min_u32 v106,Lcode_g12_e-Lg2_pc_%=-4,v106
s_mov_b32 m0,163584
s_nop 0
global_load_lds_dword v106,s[40:41]
s_and_b32 s79,s54,127
s_cmp_eq_u32 s79,0
s_cselect_b32 s94,0x3300,0
s_cmp_eq_u32 s79,127
s_cselect_b32 s94,0xcc00,s94
s_add_u32 m0,s59,4096
s_nop 0
buffer_load_dword v8,s[36:39],s58 offen offset:0 sc1 lds
buffer_store_dword v4,v3,s[36:39],s71 offen offset:0 sc0 sc1
s_add_u32 m0,s59,5120
s_nop 0
buffer_load_dword v8,s[36:39],s58 offen offset:0 sc1 lds
buffer_store_dword v4,v3,s[36:39],s71 offen offset:0 sc0 sc1
s_mov_b32 s83,0
Lg2_poll_%=:
s_add_u32 s78,s83,2
s_and_b32 s78,s78,3
s_lshl_b32 s78,s78,10
s_add_u32 s78,s78,4096
s_add_u32 m0,s59,s78
s_nop 0
buffer_load_dword v8,s[36:39],s58 offen offset:0 sc1 lds
s_and_b32 s78,s83,3
s_lshl_b32 s78,s78,10
v_add_u32 v106,s78,v6
s_waitcnt vmcnt(4)
s_barrier
ds_read_b32 v7,v106 offset:4096
buffer_store_dword v4,v3,s[36:39],s70 offen offset:0 sc0 sc1
s_mov_b32 s70,s71
s_mov_b32 s71,s72
s_mov_b32 s72,0x7fff0000
s_add_u32 s83,s83,1
s_add_u32 s82,s82,1
L 0
v_cmp_eq_u32 vcc,s53,v7
s_nop 0
s_or_b64 vcc,vcc,s[94:95]
s_cmp_eq_u32 vcc_lo,-1
s_cbranch_scc1 Lg2_go_%=
s_cmp_lt_u32 s82,20000
s_cbranch_scc1 Lg2_poll_%=
Lg2_go_%=:
s_and_b32 s78,s54,127
s_cmp_eq_u32 s78,s85
s_cselect_b64 s[40:41],-1,0
v_cndmask_b32 v17,v15,v16,s[40:41]
s_add_u32 m0,s57,0
s_add_u32 s56,s55,0
D 17,56,0
s_add_u32 s56,s55,256
D 17,56,1024
s_add_u32 s56,s55,512
D 17,56,2048
s_add_u32 s56,s55,768
D 17,56,3072
s_add_u32 s58,s58,s61
s_add_u32 m0,s59,2048
s_nop 0
buffer_load_dword v8,s[36:39],s58 offen offset:0 sc1 lds
v_cndmask_b32 v17,v10,v9,s[40:41]
s_add_u32 m0,s57,16384
s_add_u32 s56,s55,20480
D 17,56,0
s_add_u32 s56,s55,20736
D 17,56,1024
s_add_u32 s56,s55,20992
D 17,56,2048
s_add_u32 s56,s55,21248
D 17,56,3072
s_add_u32 s55,s55,s60
s_sub_u32 s57,s84,s57
s_add_u32 s54,s54,s46
s_and_b32 s79,s54,127
s_cmp_eq_u32 s79,0
s_cselect_b32 s94,0x3300,0
s_cmp_eq_u32 s79,127
s_cselect_b32 s94,0xcc00,s94
s_waitcnt vmcnt(4)
s_barrier
buffer_store_dword v4,v3,s[36:39],s70 offen offset:0 sc0 sc1
s_min_u32 s71,s71,s72
buffer_store_dword v4,v3,s[36:39],s71 offen offset:0 sc0 sc1
s_mov_b32 s70,0x7fff0000
s_mov_b32 s71,0x7fff0000
s_mov_b32 s72,0x7fff0000
s_cmp_eq_u32 s80,1
buffer_store_dword v4,v3,s[36:39],s71 offen offset:0 sc0 sc1
s_cbranch_scc1 Lg2_st1_%=
s_branch Lg2_st0_%=
Lg2_gox_%=:
s_add_u32 m0,s59,2048
s_nop 0
buffer_load_dword v8,s[36:39],s58 offen offset:0 sc1 lds
s_and_b32 s79,s54,127
s_cmp_eq_u32 s79,0
s_cselect_b32 s94,0x3300,0
s_cmp_eq_u32 s79,127
s_cselect_b32 s94,0xcc00,s94
s_waitcnt vmcnt(0)
s_barrier
buffer_store_dword v4,v3,s[36:39],s70 offen offset:0 sc0 sc1
s_min_u32 s71,s71,s72
buffer_store_dword v4,v3,s[36:39],s71 offen offset:0 sc0 sc1
s_mov_b32 s70,0x7fff0000
s_mov_b32 s71,0x7fff0000
s_mov_b32 s72,0x7fff0000
s_cmp_eq_u32 s80,1
s_cbranch_scc1 Lg2_st1_%=
Lg2_st0_%=:
ds_read_b32 v7,v6 offset:2048
R 34,1,0
R 38,1,1024
R 42,1,2048
R 46,1,3072
R 50,1,4096
L 5
v_cmp_eq_u32 vcc,s53,v7
s_nop 0
s_or_b64 vcc,vcc,s[94:95]
L 3
s_cmp_eq_u32 vcc_lo,-1
s_cbranch_scc1 Lg2_F0_%=
s_branch Lg2_L0_%=
Lg2_st1_%=:
ds_read_b32 v7,v6 offset:2048
R 34,2,0
R 38,2,1024
R 42,2,2048
R 46,2,3072
R 50,2,4096
L 5
v_cmp_eq_u32 vcc,s53,v7
s_nop 0
s_or_b64 vcc,vcc,s[94:95]
L 3
s_cmp_eq_u32 vcc_lo,-1
s_cbranch_scc1 Lg2_F1_%=
s_branch Lg2_L1_%=
Lg2_F0_%=:
v_mfma_f32_16x16x32_bf16 v[22:25],a[4:7],v[34:37],0
s_add_u32 s58,s58,s61
s_add_u32 m0,s59,0
s_and_b32 s78,s54,127
R 54,1,5120
M 22,a[12:15],38
s_cmp_eq_u32 s78,s85
s_cselect_b64 s[40:41],-1,0
s_add_u32 s56,s55,0
buffer_load_dword v8,s[36:39],s58 offen offset:0 sc1 lds
v_mfma_f32_16x16x32_bf16 v[18:21],a[0:3],v[34:37],0
s_add_u32 m0,s57,0
R 58,1,6144
v_cndmask_b32 v17,v15,v16,s[40:41]
M 18,a[8:11],38
L 3
D 17,56,0
M 18,a[16:19],42
s_add_u32 s56,s55,256
R 62,1,7168
v_lshlrev_b32 v106,16,v126
M 18,a[24:27],46
s_add_u32 s54,s54,s46
v_and_b32 v107,0xffff0000,v126
v_lshlrev_b32 v108,16,v127
M 22,a[20:23],42
s_and_b32 s79,s54,127
R 66,1,8192
v_and_b32 v109,0xffff0000,v127
M 22,a[28:31],46
L 3
D 17,56,1024
M 22,a[36:39],50
s_add_u32 s56,s55,512
R 70,1,9216
v_lshlrev_b32 v110,16,v128
)"
R"(M 22,a[44:47],54
s_cmp_eq_u32 s79,0
v_and_b32 v111,0xffff0000,v128
v_lshlrev_b32 v112,16,v129
M 18,a[32:35],50
s_cselect_b32 s94,0x3300,0
R 74,1,10240
v_and_b32 v113,0xffff0000,v129
M 18,a[40:43],54
L 3
D 17,56,2048
M 18,a[48:51],58
s_add_u32 s56,s55,768
R 78,1,11264
v_fma_f32 v106,v106,v103,v102
M 18,a[56:59],62
D 17,56,3072
M 22,a[52:55],58
s_add_u32 m0,s57,16384
R 82,1,12288
v_fma_f32 v107,v107,v103,v102
M 22,a[60:63],62
L 3
v_fma_f32 v108,v108,v103,v102
v_fma_f32 v109,v109,v103,v102
M 22,a[68:71],66
s_add_u32 s56,s55,20480
R 86,1,13312
v_fma_f32 v110,v110,v103,v102
M 22,a[76:79],70
buffer_load_dwordx4 v[122:125],v0,s[36:39],s74 offen offset:0 sc1
M 18,a[64:67],66
s_cmp_eq_u32 s79,127
R 90,1,14336
v_fma_f32 v111,v111,v103,v102
M 18,a[72:75],70
L 3
buffer_load_dwordx2 v[98:99],v11,s[36:39],s73 offen offset:256 sc1
M 18,a[80:83],74
s_waitcnt vmcnt(10)
R 94,1,15360
v_fma_f32 v112,v112,v103,v102
M 18,a[88:91],78
s_barrier
v_fma_f32 v113,v113,v103,v102
v_add_f32 v26,v26,v106
M 22,a[84:87],74
s_cselect_b32 s94,0xcc00,s94
R 34,1,16384
v_add_f32 v27,v27,v107
M 22,a[92:95],78
L 3
v_add_f32 v28,v28,v108
v_add_f32 v29,v29,v109
M 22,a[100:103],82
s_mov_b32 s66,s65
R 38,1,17408
v_add_f32 v30,v30,v110
M 22,a[108:111],86
s_mov_b32 s76,s75
v_cndmask_b32 v17,v10,v9,s[40:41]
v_add_f32 v31,v31,v111
M 18,a[96:99],82
s_add_u32 s73,s73,s64
R 42,1,18432
v_add_f32 v32,v32,v112
M 18,a[104:107],86
L 3
D 17,56,0
M 18,a[112:115],90
s_add_u32 s56,s55,20736
R 46,1,19456
v_add_f32 v33,v33,v113
M 18,a[120:123],94
s_add_u32 s74,s74,s62
v_cvt_pk_bf16_f32 v114,v26,v27
v_cvt_pk_bf16_f32 v115,v28,v29
M 22,a[116:119],90
s_add_u32 s65,s65,s62
R 50,1,20480
v_fma_f32 v107,v26,v26,s87
M 22,a[124:127],94
L 3
v_add_f32 v106,v26,v27
v_fma_f32 v107,v27,v27,v107
M 22,a[132:135],34
s_add_u32 s75,s75,s63
R 54,1,21504
v_add_f32 v106,v106,v28
M 22,a[140:143],38
D 17,56,1024
M 18,a[128:131],34
s_add_u32 s56,s55,20992
R 58,1,22528
v_fma_f32 v107,v28,v28,v107
M 18,a[136:139],38
L 3
v_add_f32 v106,v106,v29
v_fma_f32 v107,v29,v29,v107
M 18,a[144:147],42
s_sub_u32 s57,s84,s57
R 62,1,23552
v_add_f32 v106,v106,v30
M 18,a[152:155],46
v_fma_f32 v107,v30,v30,v107
v_add_f32 v106,v106,v31
M 22,a[148:151],42
R 66,1,24576
v_fma_f32 v107,v31,v31,v107
M 22,a[156:159],46
L 3
v_add_f32 v106,v106,v32
v_fma_f32 v107,v32,v32,v107
M 22,a[164:167],50
R 70,1,25600
v_add_f32 v106,v106,v33
M 22,a[172:175],54
v_fma_f32 v107,v33,v33,v107
v_cvt_pk_bf16_f32 v116,v30,v31
M 18,a[160:163],50
R 74,1,26624
v_cvt_pk_bf16_f32 v117,v32,v33
M 18,a[168:171],54
L 3
v_permlane16_swap_b32 v106,v107
v_add_f32 v106,v106,v107
v_mov_b32 v107,v106
M 18,a[176:179],58
R 78,1,27648
M 18,a[184:187],62
v_permlane32_swap_b32 v106,v107
v_add_f32 v106,v106,v107
M 22,a[180:183],58
R 82,1,28672
ds_write_b32 v12,v106 offset:2048
M 22,a[188:191],62
L 4
D 17,56,2048
M 22,a[196:199],66
s_add_u32 s56,s55,21248
s_add_u32 s55,s55,s60
R 86,1,29696
M 22,a[204:207],70
D 17,56,3072
M 18,a[192:195],66
R 90,1,30720
M 18,a[200:203],70
L 4
L 2
buffer_store_dwordx4 v[114:117],v0,s[36:39],s67 offen offset:0 sc0 sc1
M 18,a[208:211],74
s_waitcnt vmcnt(7)
R 94,1,31744
M 18,a[216:219],78
s_barrier
ds_read_b32 v7,v6 offset:0
R 118,13,2048
M 22,a[212:215],74
R 34,2,0
M 22,a[220:223],78
L 5
buffer_store_dword v4,v3,s[36:39],s70 offen offset:0 sc0 sc1
M 22,a[228:231],82
s_mov_b32 s70,s69
s_mov_b32 s69,s68
R 38,2,1024
M 22,a[236:239],86
s_add_u32 s68,s68,s63
M 18,a[224:227],82
R 42,2,2048
M 18,a[232:235],86
L 5
L 4
v_cmp_eq_u32 vcc,s53,v7
M 18,a[240:243],90
L 3
R 46,2,3072
v_add_f32 v118,v118,v119
M 18,a[248:251],94
s_or_b64 vcc,vcc,s[94:95]
v_add_f32 v120,v120,v121
v_add_f32 v118,v118,v120
M 22,a[244:247],90
s_cmp_eq_u32 vcc_lo,-1
R 50,2,4096
v_mul_f32 v118,s86,v118
M 22,a[252:255],94
s_waitcnt vmcnt(6) lgkmcnt(3)
buffer_store_dword v118,v14,s[36:39],s77 offen offset:0 sc0 sc1
s_cbranch_scc0 Lg2_L1_%=
Lg2_F1_%=:
v_mfma_f32_16x16x32_bf16 v[30:33],a[4:7],v[34:37],0
s_add_u32 s58,s58,s61
s_add_u32 m0,s59,1024
s_and_b32 s78,s54,127
R 54,2,5120
M 30,a[12:15],38
s_cmp_eq_u32 s78,s85
s_cselect_b64 s[40:41],-1,0
s_add_u32 s56,s55,0
buffer_load_dword v8,s[36:39],s58 offen offset:0 sc1 lds
v_mfma_f32_16x16x32_bf16 v[26:29],a[0:3],v[34:37],0
s_add_u32 m0,s57,0
R 58,2,6144
v_cndmask_b32 v17,v15,v16,s[40:41]
M 26,a[8:11],38
L 3
D 17,56,0
M 26,a[16:19],42
s_add_u32 s56,s55,256
R 62,2,7168
v_lshlrev_b32 v106,16,v122
M 26,a[24:27],46
s_add_u32 s54,s54,s46
v_and_b32 v107,0xffff0000,v122
v_lshlrev_b32 v108,16,v123
M 30,a[20:23],42
s_and_b32 s79,s54,127
R 66,2,8192
v_and_b32 v109,0xffff0000,v123
M 30,a[28:31],46
L 3
D 17,56,1024
M 30,a[36:39],50
s_add_u32 s56,s55,512
R 70,2,9216
v_lshlrev_b32 v110,16,v124
M 30,a[44:47],54
s_cmp_eq_u32 s79,0
v_and_b32 v111,0xffff0000,v124
v_lshlrev_b32 v112,16,v125
M 26,a[32:35],50
s_cselect_b32 s94,0x3300,0
R 74,2,10240
v_and_b32 v113,0xffff0000,v125
M 26,a[40:43],54
L 3
D 17,56,2048
M 26,a[48:51],58
s_add_u32 s56,s55,768
R 78,2,11264
v_fma_f32 v106,v106,v99,v98
M 26,a[56:59],62
D 17,56,3072
M 30,a[52:55],58
s_add_u32 m0,s57,16384
R 82,2,12288
v_fma_f32 v107,v107,v99,v98
M 30,a[60:63],62
L 3
v_fma_f32 v108,v108,v99,v98
v_fma_f32 v109,v109,v99,v98
M 30,a[68:71],66
s_add_u32 s56,s55,20480
R 86,2,13312
v_fma_f32 v110,v110,v99,v98
M 30,a[76:79],70
buffer_load_dwordx4 v[126:129],v0,s[36:39],s74 offen offset:0 sc1
M 26,a[64:67],66
s_cmp_eq_u32 s79,127
R 90,2,14336
v_fma_f32 v111,v111,v99,v98
M 26,a[72:75],70
L 3
buffer_load_dwordx2 v[102:103],v11,s[36:39],s73 offen offset:256 sc1
M 26,a[80:83],74
s_waitcnt vmcnt(10)
R 94,2,15360
v_fma_f32 v112,v112,v99,v98
M 26,a[88:91],78
s_barrier
v_fma_f32 v113,v113,v99,v98
v_add_f32 v18,v18,v106
M 30,a[84:87],74
s_cselect_b32 s94,0xcc00,s94
R 34,2,16384
v_add_f32 v19,v19,v107
M 30,a[92:95],78
L 3
v_add_f32 v20,v20,v108
v_add_f32 v21,v21,v109
M 30,a[100:103],82
s_mov_b32 s67,s65
R 38,2,17408
v_add_f32 v22,v22,v110
M 30,a[108:111],86
s_mov_b32 s77,s75
v_cndmask_b32 v17,v10,v9,s[40:41]
v_add_f32 v23,v23,v111
M 26,a[96:99],82
s_add_u32 s73,s73,s64
R 42,2,18432
v_add_f32 v24,v24,v112
M 26,a[104:107],86
L 3
D 17,56,0
M 26,a[112:115],90
s_add_u32 s56,s55,20736
R 46,2,19456
v_add_f32 v25,v25,v113
M 26,a[120:123],94
s_add_u32 s74,s74,s62
v_cvt_pk_bf16_f32 v114,v18,v19
v_cvt_pk_bf16_f32 v115,v20,v21
M 30,a[116:119],90
s_add_u32 s65,s65,s62
R 50,2,20480
v_fma_f32 v107,v18,v18,s87
M 30,a[124:127],94
L 3
v_add_f32 v106,v18,v19
v_fma_f32 v107,v19,v19,v107
M 30,a[132:135],34
s_add_u32 s75,s75,s63
R 54,2,21504
v_add_f32 v106,v106,v20
M 30,a[140:143],38
D 17,56,1024
M 26,a[128:131],34
s_add_u32 s56,s55,20992
R 58,2,22528
v_fma_f32 v107,v20,v20,v107
M 26,a[136:139],38
L 3
v_add_f32 v106,v106,v21
v_fma_f32 v107,v21,v21,v107
M 26,a[144:147],42
s_sub_u32 s57,s84,s57
R 62,2,23552
v_add_f32 v106,v106,v22
M 26,a[152:155],46
v_fma_f32 v107,v22,v22,v107
v_add_f32 v106,v106,v23
M 30,a[148:151],42
R 66,2,24576
v_fma_f32 v107,v23,v23,v107
M 30,a[156:159],46
L 3
v_add_f32 v106,v106,v24
v_fma_f32 v107,v24,v24,v107
M 30,a[164:167],50
R 70,2,25600
v_add_f32 v106,v106,v25
M 30,a[172:175],54
v_fma_f32 v107,v25,v25,v107
v_cvt_pk_bf16_f32 v116,v22,v23
M 26,a[160:163],50
R 74,2,26624
v_cvt_pk_bf16_f32 v117,v24,v25
M 26,a[168:171],54
L 3
v_permlane16_swap_b32 v106,v107
v_add_f32 v106,v106,v107
v_mov_b32 v107,v106
M 26,a[176:179],58
R 78,2,27648
M 26,a[184:187],62
v_permlane32_swap_b32 v106,v107
v_add_f32 v106,v106,v107
M 30,a[180:183],58
R 82,2,28672
ds_write_b32 v12,v106 offset:0
M 30,a[188:191],62
L 4
D 17,56,2048
M 30,a[196:199],66
s_add_u32 s56,s55,21248
s_add_u32 s55,s55,s60
R 86,2,29696
M 30,a[204:207],70
D 17,56,3072
M 26,a[192:195],66
R 90,2,30720
M 26,a[200:203],70
L 4
L 2
buffer_store_dwordx4 v[114:117],v0,s[36:39],s66 offen offset:0 sc0 sc1
M 26,a[208:211],74
s_waitcnt vmcnt(7)
R 94,2,31744
M 26,a[216:219],78
s_barrier
ds_read_b32 v7,v6 offset:1024
R 118,13,0
M 30,a[212:215],74
R 34,1,0
M 30,a[220:223],78
L 5
buffer_store_dword v4,v3,s[36:39],s70 offen offset:0 sc0 sc1
M 30,a[228:231],82
s_mov_b32 s70,s69
s_mov_b32 s69,s68
R 38,1,1024
M 30,a[236:239],86
s_add_u32 s68,s68,s63
M 26,a[224:227],82
R 42,1,2048
M 26,a[232:235],86
L 5
L 4
v_cmp_eq_u32 vcc,s53,v7
M 26,a[240:243],90
L 3
R 46,1,3072
v_add_f32 v118,v118,v119
M 26,a[248:251],94
s_or_b64 vcc,vcc,s[94:95]
v_add_f32 v120,v120,v121
v_add_f32 v118,v118,v120
M 30,a[244:247],90
s_cmp_eq_u32 vcc_lo,-1
R 50,1,4096
v_mul_f32 v118,s86,v118
M 30,a[252:255],94
s_waitcnt vmcnt(6) lgkmcnt(3)
buffer_store_dword v118,v14,s[36:39],s76 offen offset:0 sc0 sc1
s_cbranch_scc1 Lg2_F0_%=
Lg2_L0_%=:
v_mfma_f32_16x16x32_bf16 v[22:25],a[4:7],v[34:37],0
s_add_u32 m0,s59,3072
R 54,1,5120
v_lshlrev_b32 v106,16,v126
M 22,a[12:15],38
buffer_load_dword v8,s[36:39],s58 offen offset:0 sc1 lds
v_mfma_f32_16x16x32_bf16 v[18:21],a[0:3],v[34:37],0
s_mov_b32 s66,s65
R 58,1,6144
v_and_b32 v107,0xffff0000,v126
M 18,a[8:11],38
L 3
v_lshlrev_b32 v108,16,v127
v_and_b32 v109,0xffff0000,v127
M 18,a[16:19],42
s_mov_b32 s76,s75
R 62,1,7168
v_lshlrev_b32 v110,16,v128
M 18,a[24:27],46
s_add_u32 s65,s65,s62
v_and_b32 v111,0xffff0000,v128
v_lshlrev_b32 v112,16,v129
M 22,a[20:23],42
s_add_u32 s75,s75,s63
R 66,1,8192
v_and_b32 v113,0xffff0000,v129
v_fma_f32 v106,v106,v103,v102
M 22,a[28:31],46
L 3
v_fma_f32 v107,v107,v103,v102
v_fma_f32 v108,v108,v103,v102
M 22,a[36:39],50
R 70,1,9216
v_fma_f32 v109,v109,v103,v102
M 22,a[44:47],54
v_fma_f32 v110,v110,v103,v102
v_fma_f32 v111,v111,v103,v102
M 18,a[32:35],50
R 74,1,10240
v_fma_f32 v112,v112,v103,v102
M 18,a[40:43],54
L 3
v_fma_f32 v113,v113,v103,v102
v_add_f32 v26,v26,v106
M 18,a[48:51],58
R 78,1,11264
v_add_f32 v27,v27,v107
M 18,a[56:59],62
v_add_f32 v28,v28,v108
v_add_f32 v29,v29,v109
v_add_f32 v30,v30,v110
M 22,a[52:55],58
R 82,1,12288
v_add_f32 v31,v31,v111
M 22,a[60:63],62
L 3
v_add_f32 v32,v32,v112
v_add_f32 v33,v33,v113
M 22,a[68:71],66
R 86,1,13312
v_cvt_pk_bf16_f32 v114,v26,v27
M 22,a[76:79],70
buffer_load_dwordx4 v[122:125],v0,s[36:39],s74 offen offset:0 sc1
M 18,a[64:67],66
s_add_u32 s74,s74,s62
R 90,1,14336
v_cvt_pk_bf16_f32 v115,v28,v29
M 18,a[72:75],70
L 3
buffer_load_dwordx2 v[98:99],v11,s[36:39],s73 offen offset:256 sc1
v_cvt_pk_bf16_f32 v116,v30,v31
M 18,a[80:83],74
s_waitcnt vmcnt(2)
s_add_u32 s73,s73,s64
R 94,1,15360
M 18,a[88:91],78
s_barrier
ds_read_b32 v7,v6 offset:3072
v_cvt_pk_bf16_f32 v117,v32,v33
M 22,a[84:87],74
R 34,1,16384
v_add_f32 v106,v26,v27
)"
R"(M 22,a[92:95],78
L 4
buffer_store_dword v4,v3,s[36:39],s70 offen offset:0 sc0 sc1
M 22,a[100:103],82
s_mov_b32 s70,0x7fff0000
R 38,1,17408
v_fma_f32 v107,v26,v26,s87
M 22,a[108:111],86
buffer_store_dwordx4 v[114:117],v0,s[36:39],s67 offen offset:0 sc0 sc1
v_fma_f32 v107,v27,v27,v107
M 18,a[96:99],82
R 42,1,18432
v_add_f32 v106,v106,v28
v_fma_f32 v107,v28,v28,v107
M 18,a[104:107],86
L 4
L 3
v_cmp_eq_u32 vcc,s53,v7
M 18,a[112:115],90
R 46,1,19456
v_add_f32 v106,v106,v29
M 18,a[120:123],94
s_or_b64 vcc,vcc,s[94:95]
v_fma_f32 v107,v29,v29,v107
v_add_f32 v106,v106,v30
M 22,a[116:119],90
R 50,1,20480
v_fma_f32 v107,v30,v30,v107
M 22,a[124:127],94
s_cmp_eq_u32 vcc_lo,-1
L 3
v_add_f32 v106,v106,v31
s_cbranch_scc0 Lg2_X0_%=
M 22,a[132:135],34
s_and_b32 s78,s54,127
s_cmp_eq_u32 s78,s85
s_cselect_b64 s[40:41],-1,0
R 54,1,21504
M 22,a[140:143],38
s_add_u32 m0,s57,0
s_add_u32 s56,s55,0
s_add_u32 s58,s58,s61
v_cndmask_b32 v17,v15,v16,s[40:41]
M 18,a[128:131],34
s_add_u32 s54,s54,s46
R 58,1,22528
v_fma_f32 v107,v31,v31,v107
M 18,a[136:139],38
L 3
D 17,56,0
M 18,a[144:147],42
s_add_u32 s56,s55,256
R 62,1,23552
v_add_f32 v106,v106,v32
M 18,a[152:155],46
s_and_b32 s79,s54,127
v_fma_f32 v107,v32,v32,v107
v_add_f32 v106,v106,v33
M 22,a[148:151],42
s_cmp_eq_u32 s79,0
R 66,1,24576
v_fma_f32 v107,v33,v33,v107
M 22,a[156:159],46
L 3
D 17,56,1024
M 22,a[164:167],50
s_cselect_b32 s94,0x3300,0
s_add_u32 s56,s55,512
R 70,1,25600
M 22,a[172:175],54
s_cmp_eq_u32 s79,127
v_permlane16_swap_b32 v106,v107
v_add_f32 v106,v106,v107
M 18,a[160:163],50
s_cselect_b32 s94,0xcc00,s94
R 74,1,26624
v_mov_b32 v107,v106
M 18,a[168:171],54
L 3
D 17,56,2048
M 18,a[176:179],58
s_add_u32 s56,s55,768
R 78,1,27648
M 18,a[184:187],62
v_permlane32_swap_b32 v106,v107
v_add_f32 v106,v106,v107
M 22,a[180:183],58
R 82,1,28672
ds_write_b32 v12,v106 offset:2048
M 22,a[188:191],62
L 4
D 17,56,3072
M 22,a[196:199],66
s_add_u32 m0,s59,0
s_add_u32 s56,s55,20480
R 86,1,29696
M 22,a[204:207],70
buffer_load_dword v8,s[36:39],s58 offen offset:0 sc1 lds
M 18,a[192:195],66
s_add_u32 m0,s57,16384
R 90,1,30720
v_cndmask_b32 v17,v10,v9,s[40:41]
M 18,a[200:203],70
L 4
L 2
s_sub_u32 s57,s84,s57
M 18,a[208:211],74
s_waitcnt vmcnt(0)
R 94,1,31744
M 18,a[216:219],78
s_barrier
D 17,56,0
M 22,a[212:215],74
s_add_u32 s56,s55,20736
R 34,2,0
ds_read_b32 v7,v6 offset:0
M 22,a[220:223],78
L 4
buffer_store_dword v4,v3,s[36:39],s70 offen offset:0 sc0 sc1
M 22,a[228:231],82
s_mov_b32 s70,s69
R 38,2,1024
R 118,13,2048
M 22,a[236:239],86
D 17,56,1024
M 18,a[224:227],82
s_add_u32 s56,s55,20992
s_mov_b32 s69,s68
R 42,2,2048
M 18,a[232:235],86
L 5
D 17,56,2048
M 18,a[240:243],90
L 3
s_add_u32 s56,s55,21248
R 46,2,3072
v_cmp_eq_u32 vcc,s53,v7
M 18,a[248:251],94
D 17,56,3072
M 22,a[244:247],90
s_add_u32 s55,s55,s60
s_add_u32 s68,s68,s63
L 2
s_or_b64 vcc,vcc,s[94:95]
s_cmp_eq_u32 vcc_lo,-1
R 50,2,4096
v_add_f32 v118,v118,v119
v_add_f32 v120,v120,v121
v_add_f32 v118,v118,v120
v_mul_f32 v118,s86,v118
M 22,a[252:255],94
s_nop 0
buffer_store_dword v118,v14,s[36:39],s77 offen offset:0 sc0 sc1
buffer_store_dword v4,v3,s[36:39],s71 offen offset:0 sc0 sc1
buffer_store_dword v4,v3,s[36:39],s71 offen offset:0 sc0 sc1
s_cbranch_scc1 Lg2_F1_%=
s_branch Lg2_L1_%=
Lg2_X0_%=:
M 22,a[132:135],34
s_add_u32 m0,s59,0
R 54,1,21504
v_fma_f32 v107,v31,v31,v107
M 22,a[140:143],38
buffer_load_dword v8,s[36:39],s58 offen offset:0 sc1 lds
M 18,a[128:131],34
R 58,1,22528
v_add_f32 v106,v106,v32
M 18,a[136:139],38
L 3
v_fma_f32 v107,v32,v32,v107
v_add_f32 v106,v106,v33
M 18,a[144:147],42
R 62,1,23552
v_fma_f32 v107,v33,v33,v107
M 18,a[152:155],46
M 22,a[148:151],42
R 66,1,24576
M 22,a[156:159],46
L 3
v_permlane16_swap_b32 v106,v107
v_add_f32 v106,v106,v107
M 22,a[164:167],50
R 70,1,25600
v_mov_b32 v107,v106
M 22,a[172:175],54
M 18,a[160:163],50
R 74,1,26624
M 18,a[168:171],54
L 3
v_permlane32_swap_b32 v106,v107
M 18,a[176:179],58
R 78,1,27648
v_add_f32 v106,v106,v107
M 18,a[184:187],62
ds_write_b32 v12,v106 offset:2048
M 22,a[180:183],58
R 82,1,28672
M 22,a[188:191],62
L 4
M 22,a[196:199],66
R 86,1,29696
M 22,a[204:207],70
M 18,a[192:195],66
R 90,1,30720
M 18,a[200:203],70
L 4
L 3
M 18,a[208:211],74
s_waitcnt vmcnt(0)
R 94,1,31744
M 18,a[216:219],78
s_barrier
R 118,13,2048
ds_read_b32 v7,v6 offset:0
M 22,a[212:215],74
buffer_store_dword v4,v3,s[36:39],s70 offen offset:0 sc0 sc1
M 22,a[220:223],78
L 4
s_mov_b32 s70,s69
s_mov_b32 s69,s68
M 22,a[228:231],82
s_add_u32 s68,s68,s63
M 22,a[236:239],86
M 18,a[224:227],82
M 18,a[232:235],86
L 2
L 1
L 0
M 18,a[240:243],90
v_add_f32 v118,v118,v119
v_add_f32 v120,v120,v121
v_add_f32 v118,v118,v120
M 18,a[248:251],94
v_cmp_eq_u32 vcc,s53,v7
v_mul_f32 v118,s86,v118
M 22,a[244:247],90
buffer_store_dword v118,v14,s[36:39],s77 offen offset:0 sc0 sc1
M 22,a[252:255],94
s_nop 0
s_or_b64 vcc,vcc,s[94:95]
s_mov_b32 s80,1
s_cmp_eq_u32 vcc_lo,-1
s_cbranch_scc0 Lg2_xn0_%=
s_and_b32 s78,s54,127
s_cmp_eq_u32 s78,s85
s_cselect_b64 s[40:41],-1,0
v_cndmask_b32 v17,v15,v16,s[40:41]
s_add_u32 m0,s57,0
s_add_u32 s56,s55,0
D 17,56,0
v_lshlrev_b32 v106,16,v122
v_and_b32 v107,0xffff0000,v122
v_lshlrev_b32 v108,16,v123
v_and_b32 v109,0xffff0000,v123
v_lshlrev_b32 v110,16,v124
s_add_u32 s56,s55,256
D 17,56,1024
v_and_b32 v111,0xffff0000,v124
v_lshlrev_b32 v112,16,v125
v_and_b32 v113,0xffff0000,v125
v_fma_f32 v106,v106,v99,v98
v_fma_f32 v107,v107,v99,v98
s_add_u32 s56,s55,512
D 17,56,2048
v_fma_f32 v108,v108,v99,v98
v_fma_f32 v109,v109,v99,v98
v_fma_f32 v110,v110,v99,v98
v_fma_f32 v111,v111,v99,v98
v_fma_f32 v112,v112,v99,v98
s_add_u32 s56,s55,768
D 17,56,3072
v_fma_f32 v113,v113,v99,v98
v_add_f32 v18,v18,v106
v_add_f32 v19,v19,v107
v_add_f32 v20,v20,v108
v_add_f32 v21,v21,v109
s_add_u32 s58,s58,s61
v_cndmask_b32 v17,v10,v9,s[40:41]
s_add_u32 m0,s57,16384
s_add_u32 s56,s55,20480
D 17,56,0
v_add_f32 v22,v22,v110
v_add_f32 v23,v23,v111
v_add_f32 v24,v24,v112
v_add_f32 v25,v25,v113
v_cvt_pk_bf16_f32 v114,v18,v19
s_add_u32 s56,s55,20736
D 17,56,1024
v_cvt_pk_bf16_f32 v115,v20,v21
v_cvt_pk_bf16_f32 v116,v22,v23
v_cvt_pk_bf16_f32 v117,v24,v25
s_add_u32 s56,s55,20992
D 17,56,2048
s_add_u32 s56,s55,21248
D 17,56,3072
buffer_store_dwordx4 v[114:117],v0,s[36:39],s66 offen offset:0 sc0 sc1
v_add_f32 v106,v18,v19
v_fma_f32 v107,v18,v18,s87
s_add_u32 s55,s55,s60
s_sub_u32 s57,s84,s57
s_add_u32 s54,s54,s46
v_fma_f32 v107,v19,v19,v107
v_add_f32 v106,v106,v20
v_fma_f32 v107,v20,v20,v107
v_add_f32 v106,v106,v21
v_fma_f32 v107,v21,v21,v107
v_add_f32 v106,v106,v22
v_fma_f32 v107,v22,v22,v107
v_add_f32 v106,v106,v23
v_fma_f32 v107,v23,v23,v107
v_add_f32 v106,v106,v24
v_fma_f32 v107,v24,v24,v107
v_add_f32 v106,v106,v25
v_fma_f32 v107,v25,v25,v107
s_nop 1
v_permlane16_swap_b32 v106,v107
v_add_f32 v106,v106,v107
v_mov_b32 v107,v106
s_nop 1
v_permlane32_swap_b32 v106,v107
v_add_f32 v106,v106,v107
ds_write_b32 v12,v106 offset:0
L 0
s_barrier
R 118,13,0
L 0
v_add_f32 v118,v118,v119
v_add_f32 v120,v120,v121
v_add_f32 v118,v118,v120
v_mul_f32 v118,s86,v118
buffer_store_dword v118,v14,s[36:39],s76 offen offset:0 sc0 sc1
s_mov_b32 s72,s69
s_mov_b32 s69,0x7fff0000
s_mov_b32 s66,0x7fff0000
s_mov_b32 s76,0x7fff0000
s_branch Lg2_gox_%=
Lg2_xn0_%=:
s_cmp_ge_u32 s54,256
s_cbranch_scc1 Lg2_fin0_%=
s_add_u32 m0,s59,4096
s_nop 0
buffer_load_dword v8,s[36:39],s58 offen offset:0 sc1 lds
buffer_store_dword v4,v3,s[36:39],s71 offen offset:0 sc0 sc1
s_add_u32 m0,s59,5120
s_nop 0
buffer_load_dword v8,s[36:39],s58 offen offset:0 sc1 lds
buffer_store_dword v4,v3,s[36:39],s71 offen offset:0 sc0 sc1
s_mov_b32 s83,0
v_lshlrev_b32 v106,16,v122
v_and_b32 v107,0xffff0000,v122
v_lshlrev_b32 v108,16,v123
v_and_b32 v109,0xffff0000,v123
v_lshlrev_b32 v110,16,v124
v_and_b32 v111,0xffff0000,v124
v_lshlrev_b32 v112,16,v125
v_and_b32 v113,0xffff0000,v125
v_fma_f32 v106,v106,v99,v98
v_fma_f32 v107,v107,v99,v98
v_fma_f32 v108,v108,v99,v98
v_fma_f32 v109,v109,v99,v98
v_fma_f32 v110,v110,v99,v98
v_fma_f32 v111,v111,v99,v98
v_fma_f32 v112,v112,v99,v98
v_fma_f32 v113,v113,v99,v98
v_add_f32 v18,v18,v106
v_add_f32 v19,v19,v107
v_add_f32 v20,v20,v108
v_add_f32 v21,v21,v109
v_add_f32 v22,v22,v110
v_add_f32 v23,v23,v111
v_add_f32 v24,v24,v112
v_add_f32 v25,v25,v113
v_cvt_pk_bf16_f32 v114,v18,v19
v_cvt_pk_bf16_f32 v115,v20,v21
v_cvt_pk_bf16_f32 v116,v22,v23
v_cvt_pk_bf16_f32 v117,v24,v25
buffer_store_dwordx4 v[114:117],v0,s[36:39],s66 offen offset:0 sc0 sc1
v_add_f32 v106,v18,v19
v_fma_f32 v107,v18,v18,s87
v_fma_f32 v107,v19,v19,v107
v_add_f32 v106,v106,v20
v_fma_f32 v107,v20,v20,v107
v_add_f32 v106,v106,v21
v_fma_f32 v107,v21,v21,v107
v_add_f32 v106,v106,v22
v_fma_f32 v107,v22,v22,v107
v_add_f32 v106,v106,v23
v_fma_f32 v107,v23,v23,v107
v_add_f32 v106,v106,v24
v_fma_f32 v107,v24,v24,v107
v_add_f32 v106,v106,v25
v_fma_f32 v107,v25,v25,v107
s_nop 1
v_permlane16_swap_b32 v106,v107
v_add_f32 v106,v106,v107
v_mov_b32 v107,v106
s_nop 1
v_permlane32_swap_b32 v106,v107
v_add_f32 v106,v106,v107
ds_write_b32 v12,v106 offset:0
L 0
s_barrier
R 118,13,0
L 0
v_add_f32 v118,v118,v119
v_add_f32 v120,v120,v121
v_add_f32 v118,v118,v120
v_mul_f32 v118,s86,v118
buffer_store_dword v118,v14,s[36:39],s76 offen offset:0 sc0 sc1
s_mov_b32 s72,s69
s_mov_b32 s69,0x7fff0000
s_mov_b32 s66,0x7fff0000
s_mov_b32 s76,0x7fff0000
s_branch Lg2_poll_%=
Lg2_fin0_%=:
v_lshlrev_b32 v106,16,v122
v_and_b32 v107,0xffff0000,v122
v_lshlrev_b32 v108,16,v123
v_and_b32 v109,0xffff0000,v123
v_lshlrev_b32 v110,16,v124
v_and_b32 v111,0xffff0000,v124
v_lshlrev_b32 v112,16,v125
v_and_b32 v113,0xffff0000,v125
v_fma_f32 v106,v106,v99,v98
v_fma_f32 v107,v107,v99,v98
v_fma_f32 v108,v108,v99,v98
v_fma_f32 v109,v109,v99,v98
v_fma_f32 v110,v110,v99,v98
v_fma_f32 v111,v111,v99,v98
v_fma_f32 v112,v112,v99,v98
v_fma_f32 v113,v113,v99,v98
v_add_f32 v18,v18,v106
v_add_f32 v19,v19,v107
v_add_f32 v20,v20,v108
v_add_f32 v21,v21,v109
v_add_f32 v22,v22,v110
v_add_f32 v23,v23,v111
v_add_f32 v24,v24,v112
v_add_f32 v25,v25,v113
v_cvt_pk_bf16_f32 v114,v18,v19
v_cvt_pk_bf16_f32 v115,v20,v21
v_cvt_pk_bf16_f32 v116,v22,v23
v_cvt_pk_bf16_f32 v117,v24,v25
buffer_store_dwordx4 v[114:117],v0,s[36:39],s66 offen offset:0 sc0 sc1
v_add_f32 v106,v18,v19
v_fma_f32 v107,v18,v18,s87
v_fma_f32 v107,v19,v19,v107
v_add_f32 v106,v106,v20
v_fma_f32 v107,v20,v20,v107
v_add_f32 v106,v106,v21
v_fma_f32 v107,v21,v21,v107
v_add_f32 v106,v106,v22
v_fma_f32 v107,v22,v22,v107
v_add_f32 v106,v106,v23
v_fma_f32 v107,v23,v23,v107
v_add_f32 v106,v106,v24
v_fma_f32 v107,v24,v24,v107
v_add_f32 v106,v106,v25
v_fma_f32 v107,v25,v25,v107
s_nop 1
v_permlane16_swap_b32 v106,v107
v_add_f32 v106,v106,v107
v_mov_b32 v107,v106
s_nop 1
v_permlane32_swap_b32 v106,v107
v_add_f32 v106,v106,v107
ds_write_b32 v12,v106 offset:0
L 0
s_barrier
R 118,13,0
L 0
v_add_f32 v118,v118,v119
v_add_f32 v120,v120,v121
v_add_f32 v118,v118,v120
v_mul_f32 v118,s86,v118
buffer_store_dword v118,v14,s[36:39],s76 offen offset:0 sc0 sc1
s_mov_b32 s72,s69
s_mov_b32 s69,0x7fff0000
s_mov_b32 s66,0x7fff0000
s_mov_b32 s76,0x7fff0000
s_branch Lg2_done_%=
Lg2_L1_%=:
v_mfma_f32_16x16x32_bf16 v[30:33],a[4:7],v[34:37],0
s_add_u32 m0,s59,3072
R 54,2,5120
v_lshlrev_b32 v106,16,v122
M 30,a[12:15],38
buffer_load_dword v8,s[36:39],s58 offen offset:0 sc1 lds
v_mfma_f32_16x16x32_bf16 v[26:29],a[0:3],v[34:37],0
s_mov_b32 s67,s65
R 58,2,6144
v_and_b32 v107,0xffff0000,v122
M 26,a[8:11],38
L 3
v_lshlrev_b32 v108,16,v123
v_and_b32 v109,0xffff0000,v123
M 26,a[16:19],42
s_mov_b32 s77,s75
R 62,2,7168
v_lshlrev_b32 v110,16,v124
)"
R"(M 26,a[24:27],46
s_add_u32 s65,s65,s62
v_and_b32 v111,0xffff0000,v124
v_lshlrev_b32 v112,16,v125
M 30,a[20:23],42
s_add_u32 s75,s75,s63
R 66,2,8192
v_and_b32 v113,0xffff0000,v125
v_fma_f32 v106,v106,v99,v98
M 30,a[28:31],46
L 3
v_fma_f32 v107,v107,v99,v98
v_fma_f32 v108,v108,v99,v98
M 30,a[36:39],50
R 70,2,9216
v_fma_f32 v109,v109,v99,v98
M 30,a[44:47],54
v_fma_f32 v110,v110,v99,v98
v_fma_f32 v111,v111,v99,v98
M 26,a[32:35],50
R 74,2,10240
v_fma_f32 v112,v112,v99,v98
M 26,a[40:43],54
L 3
v_fma_f32 v113,v113,v99,v98
v_add_f32 v18,v18,v106
M 26,a[48:51],58
R 78,2,11264
v_add_f32 v19,v19,v107
M 26,a[56:59],62
v_add_f32 v20,v20,v108
v_add_f32 v21,v21,v109
v_add_f32 v22,v22,v110
M 30,a[52:55],58
R 82,2,12288
v_add_f32 v23,v23,v111
M 30,a[60:63],62
L 3
v_add_f32 v24,v24,v112
v_add_f32 v25,v25,v113
M 30,a[68:71],66
R 86,2,13312
v_cvt_pk_bf16_f32 v114,v18,v19
M 30,a[76:79],70
buffer_load_dwordx4 v[126:129],v0,s[36:39],s74 offen offset:0 sc1
M 26,a[64:67],66
s_add_u32 s74,s74,s62
R 90,2,14336
v_cvt_pk_bf16_f32 v115,v20,v21
M 26,a[72:75],70
L 3
buffer_load_dwordx2 v[102:103],v11,s[36:39],s73 offen offset:256 sc1
v_cvt_pk_bf16_f32 v116,v22,v23
M 26,a[80:83],74
s_waitcnt vmcnt(2)
s_add_u32 s73,s73,s64
R 94,2,15360
M 26,a[88:91],78
s_barrier
ds_read_b32 v7,v6 offset:3072
v_cvt_pk_bf16_f32 v117,v24,v25
M 30,a[84:87],74
R 34,2,16384
v_add_f32 v106,v18,v19
M 30,a[92:95],78
L 4
buffer_store_dword v4,v3,s[36:39],s70 offen offset:0 sc0 sc1
M 30,a[100:103],82
s_mov_b32 s70,0x7fff0000
R 38,2,17408
v_fma_f32 v107,v18,v18,s87
M 30,a[108:111],86
buffer_store_dwordx4 v[114:117],v0,s[36:39],s66 offen offset:0 sc0 sc1
v_fma_f32 v107,v19,v19,v107
M 26,a[96:99],82
R 42,2,18432
v_add_f32 v106,v106,v20
v_fma_f32 v107,v20,v20,v107
M 26,a[104:107],86
L 4
L 3
v_cmp_eq_u32 vcc,s53,v7
M 26,a[112:115],90
R 46,2,19456
v_add_f32 v106,v106,v21
M 26,a[120:123],94
s_or_b64 vcc,vcc,s[94:95]
v_fma_f32 v107,v21,v21,v107
v_add_f32 v106,v106,v22
M 30,a[116:119],90
R 50,2,20480
v_fma_f32 v107,v22,v22,v107
M 30,a[124:127],94
s_cmp_eq_u32 vcc_lo,-1
L 3
v_add_f32 v106,v106,v23
s_cbranch_scc0 Lg2_X1_%=
M 30,a[132:135],34
s_and_b32 s78,s54,127
s_cmp_eq_u32 s78,s85
s_cselect_b64 s[40:41],-1,0
R 54,2,21504
M 30,a[140:143],38
s_add_u32 m0,s57,0
s_add_u32 s56,s55,0
s_add_u32 s58,s58,s61
v_cndmask_b32 v17,v15,v16,s[40:41]
M 26,a[128:131],34
s_add_u32 s54,s54,s46
R 58,2,22528
v_fma_f32 v107,v23,v23,v107
M 26,a[136:139],38
L 3
D 17,56,0
M 26,a[144:147],42
s_add_u32 s56,s55,256
R 62,2,23552
v_add_f32 v106,v106,v24
M 26,a[152:155],46
s_and_b32 s79,s54,127
v_fma_f32 v107,v24,v24,v107
v_add_f32 v106,v106,v25
M 30,a[148:151],42
s_cmp_eq_u32 s79,0
R 66,2,24576
v_fma_f32 v107,v25,v25,v107
M 30,a[156:159],46
L 3
D 17,56,1024
M 30,a[164:167],50
s_cselect_b32 s94,0x3300,0
s_add_u32 s56,s55,512
R 70,2,25600
M 30,a[172:175],54
s_cmp_eq_u32 s79,127
v_permlane16_swap_b32 v106,v107
v_add_f32 v106,v106,v107
M 26,a[160:163],50
s_cselect_b32 s94,0xcc00,s94
R 74,2,26624
v_mov_b32 v107,v106
M 26,a[168:171],54
L 3
D 17,56,2048
M 26,a[176:179],58
s_add_u32 s56,s55,768
R 78,2,27648
M 26,a[184:187],62
v_permlane32_swap_b32 v106,v107
v_add_f32 v106,v106,v107
M 30,a[180:183],58
R 82,2,28672
ds_write_b32 v12,v106 offset:0
M 30,a[188:191],62
L 4
D 17,56,3072
M 30,a[196:199],66
s_add_u32 m0,s59,1024
s_add_u32 s56,s55,20480
R 86,2,29696
M 30,a[204:207],70
buffer_load_dword v8,s[36:39],s58 offen offset:0 sc1 lds
M 26,a[192:195],66
s_add_u32 m0,s57,16384
R 90,2,30720
v_cndmask_b32 v17,v10,v9,s[40:41]
M 26,a[200:203],70
L 4
L 2
s_sub_u32 s57,s84,s57
M 26,a[208:211],74
s_waitcnt vmcnt(0)
R 94,2,31744
M 26,a[216:219],78
s_barrier
D 17,56,0
M 30,a[212:215],74
s_add_u32 s56,s55,20736
R 34,1,0
ds_read_b32 v7,v6 offset:1024
M 30,a[220:223],78
L 4
buffer_store_dword v4,v3,s[36:39],s70 offen offset:0 sc0 sc1
M 30,a[228:231],82
s_mov_b32 s70,s69
R 38,1,1024
R 118,13,0
M 30,a[236:239],86
D 17,56,1024
M 26,a[224:227],82
s_add_u32 s56,s55,20992
s_mov_b32 s69,s68
R 42,1,2048
M 26,a[232:235],86
L 5
D 17,56,2048
M 26,a[240:243],90
L 3
s_add_u32 s56,s55,21248
R 46,1,3072
v_cmp_eq_u32 vcc,s53,v7
M 26,a[248:251],94
D 17,56,3072
M 30,a[244:247],90
s_add_u32 s55,s55,s60
s_add_u32 s68,s68,s63
L 2
s_or_b64 vcc,vcc,s[94:95]
s_cmp_eq_u32 vcc_lo,-1
R 50,1,4096
v_add_f32 v118,v118,v119
v_add_f32 v120,v120,v121
v_add_f32 v118,v118,v120
v_mul_f32 v118,s86,v118
M 30,a[252:255],94
s_nop 0
buffer_store_dword v118,v14,s[36:39],s76 offen offset:0 sc0 sc1
buffer_store_dword v4,v3,s[36:39],s71 offen offset:0 sc0 sc1
buffer_store_dword v4,v3,s[36:39],s71 offen offset:0 sc0 sc1
s_cbranch_scc1 Lg2_F0_%=
s_branch Lg2_L0_%=
Lg2_X1_%=:
M 30,a[132:135],34
s_add_u32 m0,s59,1024
R 54,2,21504
v_fma_f32 v107,v23,v23,v107
M 30,a[140:143],38
buffer_load_dword v8,s[36:39],s58 offen offset:0 sc1 lds
M 26,a[128:131],34
R 58,2,22528
v_add_f32 v106,v106,v24
M 26,a[136:139],38
L 3
v_fma_f32 v107,v24,v24,v107
v_add_f32 v106,v106,v25
M 26,a[144:147],42
R 62,2,23552
v_fma_f32 v107,v25,v25,v107
M 26,a[152:155],46
M 30,a[148:151],42
R 66,2,24576
M 30,a[156:159],46
L 3
v_permlane16_swap_b32 v106,v107
v_add_f32 v106,v106,v107
M 30,a[164:167],50
R 70,2,25600
v_mov_b32 v107,v106
M 30,a[172:175],54
M 26,a[160:163],50
R 74,2,26624
M 26,a[168:171],54
L 3
v_permlane32_swap_b32 v106,v107
M 26,a[176:179],58
R 78,2,27648
v_add_f32 v106,v106,v107
M 26,a[184:187],62
ds_write_b32 v12,v106 offset:0
M 30,a[180:183],58
R 82,2,28672
M 30,a[188:191],62
L 4
M 30,a[196:199],66
R 86,2,29696
M 30,a[204:207],70
M 26,a[192:195],66
R 90,2,30720
M 26,a[200:203],70
L 4
L 3
M 26,a[208:211],74
s_waitcnt vmcnt(0)
R 94,2,31744
M 26,a[216:219],78
s_barrier
R 118,13,0
ds_read_b32 v7,v6 offset:1024
M 30,a[212:215],74
buffer_store_dword v4,v3,s[36:39],s70 offen offset:0 sc0 sc1
M 30,a[220:223],78
L 4
s_mov_b32 s70,s69
s_mov_b32 s69,s68
M 30,a[228:231],82
s_add_u32 s68,s68,s63
M 30,a[236:239],86
M 26,a[224:227],82
M 26,a[232:235],86
L 2
L 1
L 0
M 26,a[240:243],90
v_add_f32 v118,v118,v119
v_add_f32 v120,v120,v121
v_add_f32 v118,v118,v120
M 26,a[248:251],94
v_cmp_eq_u32 vcc,s53,v7
v_mul_f32 v118,s86,v118
M 30,a[244:247],90
buffer_store_dword v118,v14,s[36:39],s76 offen offset:0 sc0 sc1
M 30,a[252:255],94
s_nop 0
s_or_b64 vcc,vcc,s[94:95]
s_mov_b32 s80,0
s_cmp_eq_u32 vcc_lo,-1
s_cbranch_scc0 Lg2_xn1_%=
s_and_b32 s78,s54,127
s_cmp_eq_u32 s78,s85
s_cselect_b64 s[40:41],-1,0
v_cndmask_b32 v17,v15,v16,s[40:41]
s_add_u32 m0,s57,0
s_add_u32 s56,s55,0
D 17,56,0
v_lshlrev_b32 v106,16,v126
v_and_b32 v107,0xffff0000,v126
v_lshlrev_b32 v108,16,v127
v_and_b32 v109,0xffff0000,v127
v_lshlrev_b32 v110,16,v128
s_add_u32 s56,s55,256
D 17,56,1024
v_and_b32 v111,0xffff0000,v128
v_lshlrev_b32 v112,16,v129
v_and_b32 v113,0xffff0000,v129
v_fma_f32 v106,v106,v103,v102
v_fma_f32 v107,v107,v103,v102
s_add_u32 s56,s55,512
D 17,56,2048
v_fma_f32 v108,v108,v103,v102
v_fma_f32 v109,v109,v103,v102
v_fma_f32 v110,v110,v103,v102
v_fma_f32 v111,v111,v103,v102
v_fma_f32 v112,v112,v103,v102
s_add_u32 s56,s55,768
D 17,56,3072
v_fma_f32 v113,v113,v103,v102
v_add_f32 v26,v26,v106
v_add_f32 v27,v27,v107
v_add_f32 v28,v28,v108
v_add_f32 v29,v29,v109
s_add_u32 s58,s58,s61
v_cndmask_b32 v17,v10,v9,s[40:41]
s_add_u32 m0,s57,16384
s_add_u32 s56,s55,20480
D 17,56,0
v_add_f32 v30,v30,v110
v_add_f32 v31,v31,v111
v_add_f32 v32,v32,v112
v_add_f32 v33,v33,v113
v_cvt_pk_bf16_f32 v114,v26,v27
s_add_u32 s56,s55,20736
D 17,56,1024
v_cvt_pk_bf16_f32 v115,v28,v29
v_cvt_pk_bf16_f32 v116,v30,v31
v_cvt_pk_bf16_f32 v117,v32,v33
s_add_u32 s56,s55,20992
D 17,56,2048
s_add_u32 s56,s55,21248
D 17,56,3072
buffer_store_dwordx4 v[114:117],v0,s[36:39],s67 offen offset:0 sc0 sc1
v_add_f32 v106,v26,v27
v_fma_f32 v107,v26,v26,s87
s_add_u32 s55,s55,s60
s_sub_u32 s57,s84,s57
s_add_u32 s54,s54,s46
v_fma_f32 v107,v27,v27,v107
v_add_f32 v106,v106,v28
v_fma_f32 v107,v28,v28,v107
v_add_f32 v106,v106,v29
v_fma_f32 v107,v29,v29,v107
v_add_f32 v106,v106,v30
v_fma_f32 v107,v30,v30,v107
v_add_f32 v106,v106,v31
v_fma_f32 v107,v31,v31,v107
v_add_f32 v106,v106,v32
v_fma_f32 v107,v32,v32,v107
v_add_f32 v106,v106,v33
v_fma_f32 v107,v33,v33,v107
s_nop 1
v_permlane16_swap_b32 v106,v107
v_add_f32 v106,v106,v107
v_mov_b32 v107,v106
s_nop 1
v_permlane32_swap_b32 v106,v107
v_add_f32 v106,v106,v107
ds_write_b32 v12,v106 offset:2048
L 0
s_barrier
R 118,13,2048
L 0
v_add_f32 v118,v118,v119
v_add_f32 v120,v120,v121
v_add_f32 v118,v118,v120
v_mul_f32 v118,s86,v118
buffer_store_dword v118,v14,s[36:39],s77 offen offset:0 sc0 sc1
s_mov_b32 s72,s69
s_mov_b32 s69,0x7fff0000
s_mov_b32 s67,0x7fff0000
s_mov_b32 s77,0x7fff0000
s_branch Lg2_gox_%=
Lg2_xn1_%=:
s_cmp_ge_u32 s54,256
s_cbranch_scc1 Lg2_fin1_%=
s_add_u32 m0,s59,4096
s_nop 0
buffer_load_dword v8,s[36:39],s58 offen offset:0 sc1 lds
buffer_store_dword v4,v3,s[36:39],s71 offen offset:0 sc0 sc1
s_add_u32 m0,s59,5120
s_nop 0
buffer_load_dword v8,s[36:39],s58 offen offset:0 sc1 lds
buffer_store_dword v4,v3,s[36:39],s71 offen offset:0 sc0 sc1
s_mov_b32 s83,0
v_lshlrev_b32 v106,16,v126
v_and_b32 v107,0xffff0000,v126
v_lshlrev_b32 v108,16,v127
v_and_b32 v109,0xffff0000,v127
v_lshlrev_b32 v110,16,v128
v_and_b32 v111,0xffff0000,v128
v_lshlrev_b32 v112,16,v129
v_and_b32 v113,0xffff0000,v129
v_fma_f32 v106,v106,v103,v102
v_fma_f32 v107,v107,v103,v102
v_fma_f32 v108,v108,v103,v102
v_fma_f32 v109,v109,v103,v102
v_fma_f32 v110,v110,v103,v102
v_fma_f32 v111,v111,v103,v102
v_fma_f32 v112,v112,v103,v102
v_fma_f32 v113,v113,v103,v102
v_add_f32 v26,v26,v106
v_add_f32 v27,v27,v107
v_add_f32 v28,v28,v108
v_add_f32 v29,v29,v109
v_add_f32 v30,v30,v110
v_add_f32 v31,v31,v111
v_add_f32 v32,v32,v112
v_add_f32 v33,v33,v113
v_cvt_pk_bf16_f32 v114,v26,v27
v_cvt_pk_bf16_f32 v115,v28,v29
v_cvt_pk_bf16_f32 v116,v30,v31
v_cvt_pk_bf16_f32 v117,v32,v33
buffer_store_dwordx4 v[114:117],v0,s[36:39],s67 offen offset:0 sc0 sc1
v_add_f32 v106,v26,v27
v_fma_f32 v107,v26,v26,s87
v_fma_f32 v107,v27,v27,v107
v_add_f32 v106,v106,v28
v_fma_f32 v107,v28,v28,v107
v_add_f32 v106,v106,v29
v_fma_f32 v107,v29,v29,v107
v_add_f32 v106,v106,v30
v_fma_f32 v107,v30,v30,v107
v_add_f32 v106,v106,v31
v_fma_f32 v107,v31,v31,v107
v_add_f32 v106,v106,v32
v_fma_f32 v107,v32,v32,v107
v_add_f32 v106,v106,v33
v_fma_f32 v107,v33,v33,v107
s_nop 1
v_permlane16_swap_b32 v106,v107
v_add_f32 v106,v106,v107
v_mov_b32 v107,v106
s_nop 1
v_permlane32_swap_b32 v106,v107
v_add_f32 v106,v106,v107
ds_write_b32 v12,v106 offset:2048
L 0
s_barrier
R 118,13,2048
L 0
v_add_f32 v118,v118,v119
v_add_f32 v120,v120,v121
v_add_f32 v118,v118,v120
v_mul_f32 v118,s86,v118
buffer_store_dword v118,v14,s[36:39],s77 offen offset:0 sc0 sc1
s_mov_b32 s72,s69
s_mov_b32 s69,0x7fff0000
s_mov_b32 s67,0x7fff0000
s_mov_b32 s77,0x7fff0000
s_branch Lg2_poll_%=
Lg2_fin1_%=:
v_lshlrev_b32 v106,16,v126
v_and_b32 v107,0xffff0000,v126
v_lshlrev_b32 v108,16,v127
v_and_b32 v109,0xffff0000,v127
v_lshlrev_b32 v110,16,v128
v_and_b32 v111,0xffff0000,v128
v_lshlrev_b32 v112,16,v129
v_and_b32 v113,0xffff0000,v129
v_fma_f32 v106,v106,v103,v102
v_fma_f32 v107,v107,v103,v102
v_fma_f32 v108,v108,v103,v102
v_fma_f32 v109,v109,v103,v102
v_fma_f32 v110,v110,v103,v102
v_fma_f32 v111,v111,v103,v102
v_fma_f32 v112,v112,v103,v102
v_fma_f32 v113,v113,v103,v102
v_add_f32 v26,v26,v106
v_add_f32 v27,v27,v107
v_add_f32 v28,v28,v108
v_add_f32 v29,v29,v109
v_add_f32 v30,v30,v110
)"
R"(v_add_f32 v31,v31,v111
v_add_f32 v32,v32,v112
v_add_f32 v33,v33,v113
v_cvt_pk_bf16_f32 v114,v26,v27
v_cvt_pk_bf16_f32 v115,v28,v29
v_cvt_pk_bf16_f32 v116,v30,v31
v_cvt_pk_bf16_f32 v117,v32,v33
buffer_store_dwordx4 v[114:117],v0,s[36:39],s67 offen offset:0 sc0 sc1
v_add_f32 v106,v26,v27
v_fma_f32 v107,v26,v26,s87
v_fma_f32 v107,v27,v27,v107
v_add_f32 v106,v106,v28
v_fma_f32 v107,v28,v28,v107
v_add_f32 v106,v106,v29
v_fma_f32 v107,v29,v29,v107
v_add_f32 v106,v106,v30
v_fma_f32 v107,v30,v30,v107
v_add_f32 v106,v106,v31
v_fma_f32 v107,v31,v31,v107
v_add_f32 v106,v106,v32
v_fma_f32 v107,v32,v32,v107
v_add_f32 v106,v106,v33
v_fma_f32 v107,v33,v33,v107
s_nop 1
v_permlane16_swap_b32 v106,v107
v_add_f32 v106,v106,v107
v_mov_b32 v107,v106
s_nop 1
v_permlane32_swap_b32 v106,v107
v_add_f32 v106,v106,v107
ds_write_b32 v12,v106 offset:2048
L 0
s_barrier
R 118,13,2048
L 0
v_add_f32 v118,v118,v119
v_add_f32 v120,v120,v121
v_add_f32 v118,v118,v120
v_mul_f32 v118,s86,v118
buffer_store_dword v118,v14,s[36:39],s77 offen offset:0 sc0 sc1
s_mov_b32 s72,s69
s_mov_b32 s69,0x7fff0000
s_mov_b32 s67,0x7fff0000
s_mov_b32 s77,0x7fff0000
s_branch Lg2_done_%=
Lg2_done_%=:
s_waitcnt vmcnt(2)
s_barrier
buffer_store_dword v4,v3,s[36:39],s70 offen offset:0 sc0 sc1
s_waitcnt vmcnt(1)
s_barrier
buffer_store_dword v4,v3,s[36:39],s72 offen offset:0 sc0 sc1
s_waitcnt vmcnt(0) lgkmcnt(0)
s_barrier
Lg2_end_%=:
s_mov_b32 %0,s53
Lcode_g12_e:
.purgem M
.purgem R
.purgem T
.purgem D
.purgem L
)"
  : "=&s"(epOut) : "s"(ws), "s"(sWave), "s"(sG0), "s"(sStep), "s"(sOutOff), "s"(sFoutOff), "s"(sAuxOff), "s"(sWw), "s"(sBuf), "s"(sExp), "v"(vL16), "v"(vFlag), "v"(vSnapOff), "v"(vSnapRd), "v"(vWd0), "v"(vWd1), "v"(vWr0), "v"(vWr1), "v"(vSp), "v"(vOutS), "v"(vAux0), "v"(vAux1), "v"(vAux2), "v"(vAux3), "v"(vAn), "v"(vAe), "s"(w12) : "v0", "v1", "v2", "v3", "v4", "v5", "v6", "v7", "v8", "v9", "v10", "v11", "v12", "v13", "v14", "v15", "v16", "v17", "v18", "v19", "v20", "v21", "v22", "v23", "v24", "v25", "v26", "v27", "v28", "v29", "v30", "v31", "v32", "v33", "v34", "v35", "v36", "v37", "v38", "v39", "v40", "v41", "v42", "v43", "v44", "v45", "v46", "v47", "v48", "v49", "v50", "v51", "v52", "v53", "v54", "v55", "v56", "v57", "v58", "v59", "v60", "v61", "v62", "v63", "v64", "v65", "v66", "v67", "v68", "v69", "v70", "v71", "v72", "v73", "v74", "v75", "v76", "v77", "v78", "v79", "v80", "v81", "v82", "v83", "v84", "v85", "v86", "v87", "v88", "v89", "v90", "v91", "v92", "v93", "v94", "v95", "v96", "v97", "v98", "v99", "v100", "v101", "v102", "v103", "v104", "v105", "v106", "v107", "v108", "v109", "v110", "v111", "v112", "v113", "v114", "v115", "v116", "v117", "v118", "v119", "v120", "v121", "v122", "v123", "v124", "v125", "v126", "v127", "v128", "v129", "v130", "v131", "v132", "v133", "v134", "v135", "v136", "v137", "v138", "v139", "v140", "v141", "v142", "v143", "v144", "v145", "v146", "v147", "v148", "v149", "v150", "v151", "v152", "v153", "v154", "v155", "v156", "v157", "v158", "v159", "v160", "v161", "v162", "v163", "v164", "v165", "v166", "v167", "v168", "v169", "v170", "v171", "v172", "v173", "v174", "v175", "v176", "v177", "v178", "v179", "v180", "v181", "v182", "v183", "v184", "v185", "v186", "v187", "v188", "v189", "v190", "v191", "v192", "v193", "v194", "v195", "v196", "v197", "v198", "v199", "v200", "v201", "v202", "v203", "v204", "v205", "v206", "v207", "v208", "v209", "v210", "v211", "v212", "v213", "v214", "v215", "v216", "v217", "v218", "v219", "v220", "v221", "v222", "v223", "v224", "v225", "v226", "v227", "v228", "v229", "v230", "v231", "a0", "a1", "a2", "a3", "a4", "a5", "a6", "a7", "a8", "a9", "a10", "a11", "a12", "a13", "a14", "a15", "a16", "a17", "a18", "a19", "a20", "a21", "a22", "a23", "a24", "a25", "a26", "a27", "a28", "a29", "a30", "a31", "a32", "a33", "a34", "a35", "a36", "a37", "a38", "a39", "a40", "a41", "a42", "a43", "a44", "a45", "a46", "a47", "a48", "a49", "a50", "a51", "a52", "a53", "a54", "a55", "a56", "a57", "a58", "a59", "a60", "a61", "a62", "a63", "a64", "a65", "a66", "a67", "a68", "a69", "a70", "a71", "a72", "a73", "a74", "a75", "a76", "a77", "a78", "a79", "a80", "a81", "a82", "a83", "a84", "a85", "a86", "a87", "a88", "a89", "a90", "a91", "a92", "a93", "a94", "a95", "a96", "a97", "a98", "a99", "a100", "a101", "a102", "a103", "a104", "a105", "a106", "a107", "a108", "a109", "a110", "a111", "a112", "a113", "a114", "a115", "a116", "a117", "a118", "a119", "a120", "a121", "a122", "a123", "a124", "a125", "a126", "a127", "a128", "a129", "a130", "a131", "a132", "a133", "a134", "a135", "a136", "a137", "a138", "a139", "a140", "a141", "a142", "a143", "a144", "a145", "a146", "a147", "a148", "a149", "a150", "a151", "a152", "a153", "a154", "a155", "a156", "a157", "a158", "a159", "a160", "a161", "a162", "a163", "a164", "a165", "a166", "a167", "a168", "a169", "a170", "a171", "a172", "a173", "a174", "a175", "a176", "a177", "a178", "a179", "a180", "a181", "a182", "a183", "a184", "a185", "a186", "a187", "a188", "a189", "a190", "a191", "a192", "a193", "a194", "a195", "a196", "a197", "a198", "a199", "a200", "a201", "a202", "a203", "a204", "a205", "a206", "a207", "a208", "a209", "a210", "a211", "a212", "a213", "a214", "a215", "a216", "a217", "a218", "a219", "a220", "a221", "a222", "a223", "a224", "a225", "a226", "a227", "a228", "a229", "a230", "a231", "a232", "a233", "a234", "a235", "a236", "a237", "a238", "a239", "a240", "a241", "a242", "a243", "a244", "a245", "a246", "a247", "a248", "a249", "a250", "a251", "a252", "a253", "a254", "a255", "s36", "s37", "s38", "s39", "s40", "s41", "s42", "s43", "s44", "s45", "s46", "s47", "s48", "s49", "s50", "s51", "s52", "s53", "s54", "s55", "s56", "s57", "s58", "s59", "s60", "s61", "s62", "s63", "s64", "s65", "s66", "s67", "s68", "s69", "s70", "s71", "s72", "s73", "s74", "s75", "s76", "s77", "s78", "s79", "s80", "s81", "s82", "s83", "s84", "s85", "s86", "s87", "s88", "s89", "s90", "s91", "s92", "s93", "s94", "s95", "s96", "s97", "vcc", "scc", "memory");
    ep = epOut;
    if (kind == 1 && 5 + RHELP + (rep >> 1) < NR4) { kind = 4; kh = rep & 1; rep = 5 + RHELP + (rep >> 1); }
  }
  if (kind == 4) {
    const unsigned sStep = rep < 5 ? 5 : rep == 5 ? 6 : NR4;
    COMMON(10, (kh * 9 + fj) * 128, 2560, 143360, 2048, 448, 0);
    const int cs = slice;
    const unsigned sOutOff = (kh ? OFF_P1 : OFF_P0) + (cs * 4 + wave) * 1024, sFoutOff = OFF_FP + (kh * 8 + cs) * 128;
    const unsigned sAuxOff = (kh ? OFF_XP : OFF_X1) + (cs * 4 + wave) * 1024, sWw = (kh * 1792 + 16 * wave) * 2048 + cs * 256, sBuf = kh * 57344;
    const int zu = 108 + (rep + 2) % 5;
    const unsigned vSp = lg * 2048 + l15 * 4, vSn = rep < 5 ? (zu * 16 + wave * 4 + lg) * 2048 + (cs * 2 + kh) * 128 + l15 * 8 : OOB;
    (void)vWr2;
    const unsigned sExp = __builtin_amdgcn_readfirstlane(ep);
    asm volatile(
R"(.macro M d,w,x
v_mfma_f32_16x16x32_bf16 v[\d:\d+3], \w, v[\x:\x+3], v[\d:\d+3]
.endm
.macro R d,a,o
ds_read_b128 v[\d:\d+3], v\a offset:\o
.endm
.macro T d,a,o
ds_read_b64_tr_b16 \d, v\a offset:\o
.endm
.macro D a,s,o
buffer_load_dwordx4 v\a, s[36:39], s\s offen offset:\o sc1 lds
.endm
.macro L c
s_waitcnt lgkmcnt(\c)
.endm
Lcode_g4_b:
s_mov_b64 s[36:37],%0
s_mov_b32 s38,111243392
s_mov_b32 s39,0x00020000
s_mov_b32 s44,%1
s_mov_b32 s45,%2
s_mov_b32 s46,%3
s_mov_b32 s47,%4
s_mov_b32 s48,%5
s_mov_b32 s49,%6
s_mov_b32 s50,%7
s_mov_b32 s51,%8
s_mov_b32 s93,%9
s_mov_b32 s53,%10
v_mov_b32 v0,%11
v_mov_b32 v3,%12
v_mov_b32 v5,%13
v_mov_b32 v6,%14
v_mov_b32 v10,%15
v_mov_b32 v11,%16
v_mov_b32 v12,%17
v_mov_b32 v13,%18
v_mov_b32 v9,%19
v_mov_b32 v7,%20
s_mov_b64 s[40:41],%21
s_mov_b32 s42,7340032
s_mov_b32 s43,0x00020000
s_lshl_b32 s57,s44,11
s_cmp_eq_u32 s45,172
s_cbranch_scc1 Lg4_pre_%=
s_add_u32 s55,s50,0
s_add_u32 m0,s57,40960
s_nop 0
buffer_load_dwordx4 v10,s[40:43],s55 offen offset:0  lds
s_add_u32 m0,s57,41984
s_nop 0
buffer_load_dwordx4 v11,s[40:43],s55 offen offset:0  lds
s_add_u32 m0,s57,49024
s_nop 0
buffer_load_dwordx4 v10,s[40:43],s55 offen offset:128  lds
s_add_u32 m0,s57,50048
s_nop 0
buffer_load_dwordx4 v11,s[40:43],s55 offen offset:128  lds
s_add_u32 s55,s50,131072
s_add_u32 m0,s57,57344
s_nop 0
buffer_load_dwordx4 v10,s[40:43],s55 offen offset:0  lds
s_add_u32 m0,s57,58368
s_nop 0
buffer_load_dwordx4 v11,s[40:43],s55 offen offset:0  lds
s_add_u32 m0,s57,65408
s_nop 0
buffer_load_dwordx4 v10,s[40:43],s55 offen offset:128  lds
s_add_u32 m0,s57,66432
s_nop 0
buffer_load_dwordx4 v11,s[40:43],s55 offen offset:128  lds
s_add_u32 s55,s50,262144
s_add_u32 m0,s57,73728
s_nop 0
buffer_load_dwordx4 v10,s[40:43],s55 offen offset:0  lds
s_add_u32 m0,s57,74752
s_nop 0
buffer_load_dwordx4 v11,s[40:43],s55 offen offset:0  lds
s_add_u32 m0,s57,81792
s_nop 0
buffer_load_dwordx4 v10,s[40:43],s55 offen offset:128  lds
s_add_u32 m0,s57,82816
s_nop 0
buffer_load_dwordx4 v11,s[40:43],s55 offen offset:128  lds
s_add_u32 s55,s50,393216
s_add_u32 m0,s57,90112
s_nop 0
buffer_load_dwordx4 v10,s[40:43],s55 offen offset:0  lds
s_add_u32 m0,s57,91136
s_nop 0
buffer_load_dwordx4 v11,s[40:43],s55 offen offset:0  lds
s_add_u32 m0,s57,98176
s_nop 0
buffer_load_dwordx4 v10,s[40:43],s55 offen offset:128  lds
s_add_u32 m0,s57,99200
s_nop 0
buffer_load_dwordx4 v11,s[40:43],s55 offen offset:128  lds
s_add_u32 s55,s50,524288
s_add_u32 m0,s57,106496
s_nop 0
buffer_load_dwordx4 v10,s[40:43],s55 offen offset:0  lds
s_add_u32 m0,s57,107520
s_nop 0
buffer_load_dwordx4 v11,s[40:43],s55 offen offset:0  lds
s_add_u32 m0,s57,114560
s_nop 0
buffer_load_dwordx4 v10,s[40:43],s55 offen offset:128  lds
s_add_u32 m0,s57,115584
s_nop 0
buffer_load_dwordx4 v11,s[40:43],s55 offen offset:128  lds
s_add_u32 s55,s50,655360
s_add_u32 m0,s57,122880
s_nop 0
buffer_load_dwordx4 v10,s[40:43],s55 offen offset:0  lds
s_add_u32 m0,s57,123904
s_nop 0
buffer_load_dwordx4 v11,s[40:43],s55 offen offset:0  lds
s_add_u32 m0,s57,130944
s_nop 0
buffer_load_dwordx4 v10,s[40:43],s55 offen offset:128  lds
s_add_u32 m0,s57,131968
s_nop 0
buffer_load_dwordx4 v11,s[40:43],s55 offen offset:128  lds
s_waitcnt vmcnt(20) lgkmcnt(0)
s_barrier
s_add_u32 s55,s50,786432
s_add_u32 m0,s57,139264
s_nop 0
buffer_load_dwordx4 v10,s[40:43],s55 offen offset:0  lds
s_add_u32 m0,s57,140288
s_nop 0
buffer_load_dwordx4 v11,s[40:43],s55 offen offset:0  lds
s_add_u32 m0,s57,147328
s_nop 0
buffer_load_dwordx4 v10,s[40:43],s55 offen offset:128  lds
s_add_u32 m0,s57,148352
s_nop 0
buffer_load_dwordx4 v11,s[40:43],s55 offen offset:128  lds
v_add_u32 v18,40960,v12
v_add_u32 v19,40960,v13
T a[0:1],18,0
T a[2:3],18,512
T a[4:5],19,0
T a[6:7],19,512
T a[8:9],18,4096
T a[10:11],18,4608
T a[12:13],19,4096
T a[14:15],19,4608
s_waitcnt vmcnt(20) lgkmcnt(0)
s_barrier
s_add_u32 s55,s50,917504
s_add_u32 m0,s57,40960
s_nop 0
buffer_load_dwordx4 v10,s[40:43],s55 offen offset:0  lds
s_add_u32 m0,s57,41984
s_nop 0
buffer_load_dwordx4 v11,s[40:43],s55 offen offset:0  lds
s_add_u32 m0,s57,49024
s_nop 0
buffer_load_dwordx4 v10,s[40:43],s55 offen offset:128  lds
s_add_u32 m0,s57,50048
s_nop 0
buffer_load_dwordx4 v11,s[40:43],s55 offen offset:128  lds
v_add_u32 v18,57344,v12
v_add_u32 v19,57344,v13
T a[16:17],18,0
T a[18:19],18,512
T a[20:21],19,0
T a[22:23],19,512
T a[24:25],18,4096
T a[26:27],18,4608
T a[28:29],19,4096
T a[30:31],19,4608
s_waitcnt vmcnt(20) lgkmcnt(0)
s_barrier
s_add_u32 s55,s50,1048576
s_add_u32 m0,s57,57344
s_nop 0
buffer_load_dwordx4 v10,s[40:43],s55 offen offset:0  lds
s_add_u32 m0,s57,58368
s_nop 0
buffer_load_dwordx4 v11,s[40:43],s55 offen offset:0  lds
s_add_u32 m0,s57,65408
s_nop 0
buffer_load_dwordx4 v10,s[40:43],s55 offen offset:128  lds
s_add_u32 m0,s57,66432
s_nop 0
buffer_load_dwordx4 v11,s[40:43],s55 offen offset:128  lds
v_add_u32 v18,73728,v12
v_add_u32 v19,73728,v13
T a[32:33],18,0
T a[34:35],18,512
T a[36:37],19,0
T a[38:39],19,512
T a[40:41],18,4096
T a[42:43],18,4608
T a[44:45],19,4096
T a[46:47],19,4608
s_waitcnt vmcnt(20) lgkmcnt(0)
s_barrier
s_add_u32 s55,s50,1179648
s_add_u32 m0,s57,73728
s_nop 0
buffer_load_dwordx4 v10,s[40:43],s55 offen offset:0  lds
s_add_u32 m0,s57,74752
s_nop 0
buffer_load_dwordx4 v11,s[40:43],s55 offen offset:0  lds
s_add_u32 m0,s57,81792
s_nop 0
buffer_load_dwordx4 v10,s[40:43],s55 offen offset:128  lds
s_add_u32 m0,s57,82816
s_nop 0
buffer_load_dwordx4 v11,s[40:43],s55 offen offset:128  lds
v_add_u32 v18,90112,v12
v_add_u32 v19,90112,v13
T a[48:49],18,0
T a[50:51],18,512
T a[52:53],19,0
T a[54:55],19,512
T a[56:57],18,4096
T a[58:59],18,4608
T a[60:61],19,4096
T a[62:63],19,4608
s_waitcnt vmcnt(20) lgkmcnt(0)
s_barrier
s_add_u32 s55,s50,1310720
s_add_u32 m0,s57,90112
s_nop 0
buffer_load_dwordx4 v10,s[40:43],s55 offen offset:0  lds
s_add_u32 m0,s57,91136
s_nop 0
buffer_load_dwordx4 v11,s[40:43],s55 offen offset:0  lds
s_add_u32 m0,s57,98176
s_nop 0
buffer_load_dwordx4 v10,s[40:43],s55 offen offset:128  lds
s_add_u32 m0,s57,99200
s_nop 0
buffer_load_dwordx4 v11,s[40:43],s55 offen offset:128  lds
v_add_u32 v18,106496,v12
v_add_u32 v19,106496,v13
T a[64:65],18,0
T a[66:67],18,512
T a[68:69],19,0
T a[70:71],19,512
T a[72:73],18,4096
T a[74:75],18,4608
T a[76:77],19,4096
T a[78:79],19,4608
s_waitcnt vmcnt(20) lgkmcnt(0)
s_barrier
s_add_u32 s55,s50,1441792
s_add_u32 m0,s57,106496
s_nop 0
buffer_load_dwordx4 v10,s[40:43],s55 offen offset:0  lds
s_add_u32 m0,s57,107520
s_nop 0
buffer_load_dwordx4 v11,s[40:43],s55 offen offset:0  lds
s_add_u32 m0,s57,114560
s_nop 0
buffer_load_dwordx4 v10,s[40:43],s55 offen offset:128  lds
s_add_u32 m0,s57,115584
s_nop 0
buffer_load_dwordx4 v11,s[40:43],s55 offen offset:128  lds
v_add_u32 v18,122880,v12
v_add_u32 v19,122880,v13
T a[80:81],18,0
T a[82:83],18,512
T a[84:85],19,0
T a[86:87],19,512
T a[88:89],18,4096
T a[90:91],18,4608
T a[92:93],19,4096
T a[94:95],19,4608
s_waitcnt vmcnt(20) lgkmcnt(0)
s_barrier
s_add_u32 s55,s50,1572864
s_add_u32 m0,s57,122880
s_nop 0
buffer_load_dwordx4 v10,s[40:43],s55 offen offset:0  lds
s_add_u32 m0,s57,123904
s_nop 0
buffer_load_dwordx4 v11,s[40:43],s55 offen offset:0  lds
s_add_u32 m0,s57,130944
s_nop 0
buffer_load_dwordx4 v10,s[40:43],s55 offen offset:128  lds
s_add_u32 m0,s57,131968
s_nop 0
buffer_load_dwordx4 v11,s[40:43],s55 offen offset:128  lds
v_add_u32 v18,139264,v12
v_add_u32 v19,139264,v13
T a[96:97],18,0
T a[98:99],18,512
T a[100:101],19,0
T a[102:103],19,512
T a[104:105],18,4096
T a[106:107],18,4608
T a[108:109],19,4096
T a[110:111],19,4608
s_waitcnt vmcnt(20) lgkmcnt(0)
s_barrier
s_add_u32 s55,s50,1703936
s_add_u32 m0,s57,139264
s_nop 0
buffer_load_dwordx4 v10,s[40:43],s55 offen offset:0  lds
s_add_u32 m0,s57,140288
s_nop 0
buffer_load_dwordx4 v11,s[40:43],s55 offen offset:0  lds
s_add_u32 m0,s57,147328
s_nop 0
buffer_load_dwordx4 v10,s[40:43],s55 offen offset:128  lds
s_add_u32 m0,s57,148352
s_nop 0
buffer_load_dwordx4 v11,s[40:43],s55 offen offset:128  lds
v_add_u32 v18,40960,v12
v_add_u32 v19,40960,v13
T a[112:113],18,0
T a[114:115],18,512
T a[116:117],19,0
T a[118:119],19,512
T a[120:121],18,4096
T a[122:123],18,4608
T a[124:125],19,4096
T a[126:127],19,4608
s_waitcnt vmcnt(20) lgkmcnt(0)
s_barrier
s_add_u32 s55,s50,1835008
s_add_u32 m0,s57,40960
s_nop 0
buffer_load_dwordx4 v10,s[40:43],s55 offen offset:0  lds
s_add_u32 m0,s57,41984
s_nop 0
buffer_load_dwordx4 v11,s[40:43],s55 offen offset:0  lds
s_add_u32 m0,s57,49024
s_nop 0
buffer_load_dwordx4 v10,s[40:43],s55 offen offset:128  lds
s_add_u32 m0,s57,50048
s_nop 0
buffer_load_dwordx4 v11,s[40:43],s55 offen offset:128  lds
v_add_u32 v18,57344,v12
v_add_u32 v19,57344,v13
T a[128:129],18,0
T a[130:131],18,512
T a[132:133],19,0
T a[134:135],19,512
T a[136:137],18,4096
T a[138:139],18,4608
T a[140:141],19,4096
T a[142:143],19,4608
s_waitcnt vmcnt(20) lgkmcnt(0)
s_barrier
s_add_u32 s55,s50,1966080
s_add_u32 m0,s57,57344
s_nop 0
buffer_load_dwordx4 v10,s[40:43],s55 offen offset:0  lds
s_add_u32 m0,s57,58368
s_nop 0
buffer_load_dwordx4 v11,s[40:43],s55 offen offset:0  lds
s_add_u32 m0,s57,65408
s_nop 0
buffer_load_dwordx4 v10,s[40:43],s55 offen offset:128  lds
s_add_u32 m0,s57,66432
s_nop 0
buffer_load_dwordx4 v11,s[40:43],s55 offen offset:128  lds
v_add_u32 v18,73728,v12
v_add_u32 v19,73728,v13
T a[144:145],18,0
T a[146:147],18,512
T a[148:149],19,0
T a[150:151],19,512
T a[152:153],18,4096
T a[154:155],18,4608
T a[156:157],19,4096
T a[158:159],19,4608
s_waitcnt vmcnt(20) lgkmcnt(0)
s_barrier
s_add_u32 s55,s50,2097152
s_add_u32 m0,s57,73728
s_nop 0
buffer_load_dwordx4 v10,s[40:43],s55 offen offset:0  lds
s_add_u32 m0,s57,74752
s_nop 0
buffer_load_dwordx4 v11,s[40:43],s55 offen offset:0  lds
s_add_u32 m0,s57,81792
s_nop 0
buffer_load_dwordx4 v10,s[40:43],s55 offen offset:128  lds
s_add_u32 m0,s57,82816
s_nop 0
buffer_load_dwordx4 v11,s[40:43],s55 offen offset:128  lds
v_add_u32 v18,90112,v12
v_add_u32 v19,90112,v13
T a[160:161],18,0
T a[162:163],18,512
T a[164:165],19,0
T a[166:167],19,512
T a[168:169],18,4096
T a[170:171],18,4608
T a[172:173],19,4096
T a[174:175],19,4608
s_waitcnt vmcnt(20) lgkmcnt(0)
s_barrier
s_add_u32 s55,s50,2228224
s_add_u32 m0,s57,90112
s_nop 0
buffer_load_dwordx4 v10,s[40:43],s55 offen offset:0  lds
s_add_u32 m0,s57,91136
s_nop 0
buffer_load_dwordx4 v11,s[40:43],s55 offen offset:0  lds
s_add_u32 m0,s57,98176
s_nop 0
buffer_load_dwordx4 v10,s[40:43],s55 offen offset:128  lds
s_add_u32 m0,s57,99200
s_nop 0
buffer_load_dwordx4 v11,s[40:43],s55 offen offset:128  lds
v_add_u32 v18,106496,v12
v_add_u32 v19,106496,v13
T a[176:177],18,0
T a[178:179],18,512
T a[180:181],19,0
T a[182:183],19,512
T a[184:185],18,4096
T a[186:187],18,4608
T a[188:189],19,4096
T a[190:191],19,4608
s_waitcnt vmcnt(20) lgkmcnt(0)
s_barrier
s_add_u32 s55,s50,2359296
s_add_u32 m0,s57,106496
s_nop 0
buffer_load_dwordx4 v10,s[40:43],s55 offen offset:0  lds
s_add_u32 m0,s57,107520
s_nop 0
buffer_load_dwordx4 v11,s[40:43],s55 offen offset:0  lds
s_add_u32 m0,s57,114560
s_nop 0
buffer_load_dwordx4 v10,s[40:43],s55 offen offset:128  lds
s_add_u32 m0,s57,115584
s_nop 0
buffer_load_dwordx4 v11,s[40:43],s55 offen offset:128  lds
v_add_u32 v18,122880,v12
v_add_u32 v19,122880,v13
T a[192:193],18,0
T a[194:195],18,512
T a[196:197],19,0
T a[198:199],19,512
T a[200:201],18,4096
T a[202:203],18,4608
T a[204:205],19,4096
T a[206:207],19,4608
s_waitcnt vmcnt(20) lgkmcnt(0)
s_barrier
s_add_u32 s55,s50,2490368
s_add_u32 m0,s57,122880
s_nop 0
buffer_load_dwordx4 v10,s[40:43],s55 offen offset:0  lds
s_add_u32 m0,s57,123904
s_nop 0
buffer_load_dwordx4 v11,s[40:43],s55 offen offset:0  lds
s_add_u32 m0,s57,130944
s_nop 0
buffer_load_dwordx4 v10,s[40:43],s55 offen offset:128  lds
s_add_u32 m0,s57,131968
s_nop 0
buffer_load_dwordx4 v11,s[40:43],s55 offen offset:128  lds
v_add_u32 v18,139264,v12
v_add_u32 v19,139264,v13
T a[208:209],18,0
T a[210:211],18,512
T a[212:213],19,0
T a[214:215],19,512
T a[216:217],18,4096
T a[218:219],18,4608
T a[220:221],19,4096
T a[222:223],19,4608
s_waitcnt vmcnt(20) lgkmcnt(0)
s_barrier
s_add_u32 s55,s50,2621440
s_add_u32 m0,s57,139264
s_nop 0
buffer_load_dwordx4 v10,s[40:43],s55 offen offset:0  lds
s_add_u32 m0,s57,140288
s_nop 0
buffer_load_dwordx4 v11,s[40:43],s55 offen offset:0  lds
s_add_u32 m0,s57,147328
s_nop 0
buffer_load_dwordx4 v10,s[40:43],s55 offen offset:128  lds
s_add_u32 m0,s57,148352
s_nop 0
buffer_load_dwordx4 v11,s[40:43],s55 offen offset:128  lds
v_add_u32 v18,40960,v12
v_add_u32 v19,40960,v13
T a[224:225],18,0
T a[226:227],18,512
T a[228:229],19,0
T a[230:231],19,512
T a[232:233],18,4096
T a[234:235],18,4608
T a[236:237],19,4096
T a[238:239],19,4608
s_waitcnt vmcnt(20) lgkmcnt(0)
s_barrier
)"
R"(s_add_u32 s55,s50,2752512
s_add_u32 m0,s57,40960
s_nop 0
buffer_load_dwordx4 v10,s[40:43],s55 offen offset:0  lds
s_add_u32 m0,s57,41984
s_nop 0
buffer_load_dwordx4 v11,s[40:43],s55 offen offset:0  lds
s_add_u32 m0,s57,49024
s_nop 0
buffer_load_dwordx4 v10,s[40:43],s55 offen offset:128  lds
s_add_u32 m0,s57,50048
s_nop 0
buffer_load_dwordx4 v11,s[40:43],s55 offen offset:128  lds
v_add_u32 v18,57344,v12
v_add_u32 v19,57344,v13
T a[240:241],18,0
T a[242:243],18,512
T a[244:245],19,0
T a[246:247],19,512
T a[248:249],18,4096
T a[250:251],18,4608
T a[252:253],19,4096
T a[254:255],19,4608
Lg4_pre_%=:
s_waitcnt vmcnt(20) lgkmcnt(0)
s_barrier
s_add_u32 s55,s50,2883584
s_add_u32 m0,s57,57344
s_nop 0
buffer_load_dwordx4 v10,s[40:43],s55 offen offset:0  lds
s_add_u32 m0,s57,58368
s_nop 0
buffer_load_dwordx4 v11,s[40:43],s55 offen offset:0  lds
s_add_u32 m0,s57,65408
s_nop 0
buffer_load_dwordx4 v10,s[40:43],s55 offen offset:128  lds
s_add_u32 m0,s57,66432
s_nop 0
buffer_load_dwordx4 v11,s[40:43],s55 offen offset:128  lds
v_add_u32 v18,73728,v12
v_add_u32 v19,73728,v13
T v[64:65],18,0
T v[66:67],18,512
T v[68:69],19,0
T v[70:71],19,512
T v[72:73],18,4096
T v[74:75],18,4608
T v[76:77],19,4096
T v[78:79],19,4608
s_waitcnt vmcnt(20) lgkmcnt(0)
s_barrier
s_add_u32 s55,s50,3014656
s_add_u32 m0,s57,73728
s_nop 0
buffer_load_dwordx4 v10,s[40:43],s55 offen offset:0  lds
s_add_u32 m0,s57,74752
s_nop 0
buffer_load_dwordx4 v11,s[40:43],s55 offen offset:0  lds
s_add_u32 m0,s57,81792
s_nop 0
buffer_load_dwordx4 v10,s[40:43],s55 offen offset:128  lds
s_add_u32 m0,s57,82816
s_nop 0
buffer_load_dwordx4 v11,s[40:43],s55 offen offset:128  lds
v_add_u32 v18,90112,v12
v_add_u32 v19,90112,v13
T v[80:81],18,0
T v[82:83],18,512
T v[84:85],19,0
T v[86:87],19,512
T v[88:89],18,4096
T v[90:91],18,4608
T v[92:93],19,4096
T v[94:95],19,4608
s_waitcnt vmcnt(20) lgkmcnt(0)
s_barrier
s_add_u32 s55,s50,3145728
s_add_u32 m0,s57,90112
s_nop 0
buffer_load_dwordx4 v10,s[40:43],s55 offen offset:0  lds
s_add_u32 m0,s57,91136
s_nop 0
buffer_load_dwordx4 v11,s[40:43],s55 offen offset:0  lds
s_add_u32 m0,s57,98176
s_nop 0
buffer_load_dwordx4 v10,s[40:43],s55 offen offset:128  lds
s_add_u32 m0,s57,99200
s_nop 0
buffer_load_dwordx4 v11,s[40:43],s55 offen offset:128  lds
v_add_u32 v18,106496,v12
v_add_u32 v19,106496,v13
T v[96:97],18,0
T v[98:99],18,512
T v[100:101],19,0
T v[102:103],19,512
T v[104:105],18,4096
T v[106:107],18,4608
T v[108:109],19,4096
T v[110:111],19,4608
s_waitcnt vmcnt(20) lgkmcnt(0)
s_barrier
s_add_u32 s55,s50,3276800
s_add_u32 m0,s57,106496
s_nop 0
buffer_load_dwordx4 v10,s[40:43],s55 offen offset:0  lds
s_add_u32 m0,s57,107520
s_nop 0
buffer_load_dwordx4 v11,s[40:43],s55 offen offset:0  lds
s_add_u32 m0,s57,114560
s_nop 0
buffer_load_dwordx4 v10,s[40:43],s55 offen offset:128  lds
s_add_u32 m0,s57,115584
s_nop 0
buffer_load_dwordx4 v11,s[40:43],s55 offen offset:128  lds
v_add_u32 v18,122880,v12
v_add_u32 v19,122880,v13
T v[112:113],18,0
T v[114:115],18,512
T v[116:117],19,0
T v[118:119],19,512
T v[120:121],18,4096
T v[122:123],18,4608
T v[124:125],19,4096
T v[126:127],19,4608
s_waitcnt vmcnt(20) lgkmcnt(0)
s_barrier
s_add_u32 s55,s50,3407872
s_add_u32 m0,s57,122880
s_nop 0
buffer_load_dwordx4 v10,s[40:43],s55 offen offset:0  lds
s_add_u32 m0,s57,123904
s_nop 0
buffer_load_dwordx4 v11,s[40:43],s55 offen offset:0  lds
s_add_u32 m0,s57,130944
s_nop 0
buffer_load_dwordx4 v10,s[40:43],s55 offen offset:128  lds
s_add_u32 m0,s57,131968
s_nop 0
buffer_load_dwordx4 v11,s[40:43],s55 offen offset:128  lds
v_add_u32 v18,139264,v12
v_add_u32 v19,139264,v13
T v[128:129],18,0
T v[130:131],18,512
T v[132:133],19,0
T v[134:135],19,512
T v[136:137],18,4096
T v[138:139],18,4608
T v[140:141],19,4096
T v[142:143],19,4608
s_waitcnt vmcnt(20) lgkmcnt(0)
s_barrier
s_add_u32 s55,s50,3538944
s_add_u32 m0,s57,139264
s_nop 0
buffer_load_dwordx4 v10,s[40:43],s55 offen offset:0  lds
s_add_u32 m0,s57,140288
s_nop 0
buffer_load_dwordx4 v11,s[40:43],s55 offen offset:0  lds
s_add_u32 m0,s57,147328
s_nop 0
buffer_load_dwordx4 v10,s[40:43],s55 offen offset:128  lds
s_add_u32 m0,s57,148352
s_nop 0
buffer_load_dwordx4 v11,s[40:43],s55 offen offset:128  lds
v_add_u32 v18,40960,v12
v_add_u32 v19,40960,v13
T v[144:145],18,0
T v[146:147],18,512
T v[148:149],19,0
T v[150:151],19,512
T v[152:153],18,4096
T v[154:155],18,4608
T v[156:157],19,4096
T v[158:159],19,4608
s_waitcnt vmcnt(20) lgkmcnt(0)
s_barrier
v_add_u32 v18,57344,v12
v_add_u32 v19,57344,v13
T v[160:161],18,0
T v[162:163],18,512
T v[164:165],19,0
T v[166:167],19,512
T v[168:169],18,4096
T v[170:171],18,4608
T v[172:173],19,4096
T v[174:175],19,4608
s_waitcnt vmcnt(16) lgkmcnt(0)
s_barrier
v_add_u32 v18,73728,v12
v_add_u32 v19,73728,v13
T v[176:177],18,0
T v[178:179],18,512
T v[180:181],19,0
T v[182:183],19,512
T v[184:185],18,4096
T v[186:187],18,4608
T v[188:189],19,4096
T v[190:191],19,4608
s_waitcnt vmcnt(12) lgkmcnt(0)
s_barrier
v_add_u32 v18,90112,v12
v_add_u32 v19,90112,v13
T v[192:193],18,0
T v[194:195],18,512
T v[196:197],19,0
T v[198:199],19,512
T v[200:201],18,4096
T v[202:203],18,4608
T v[204:205],19,4096
T v[206:207],19,4608
s_waitcnt vmcnt(8) lgkmcnt(0)
s_barrier
v_add_u32 v18,106496,v12
v_add_u32 v19,106496,v13
T v[208:209],18,0
T v[210:211],18,512
T v[212:213],19,0
T v[214:215],19,512
T v[216:217],18,4096
T v[218:219],18,4608
T v[220:221],19,4096
T v[222:223],19,4608
s_waitcnt vmcnt(4) lgkmcnt(0)
s_barrier
v_add_u32 v18,122880,v12
v_add_u32 v19,122880,v13
T v[224:225],18,0
T v[226:227],18,512
T v[228:229],19,0
T v[230:231],19,512
T v[232:233],18,4096
T v[234:235],18,4608
T v[236:237],19,4096
T v[238:239],19,4608
s_waitcnt vmcnt(0) lgkmcnt(0)
s_barrier
v_add_u32 v18,139264,v12
v_add_u32 v19,139264,v13
T v[240:241],18,0
T v[242:243],18,512
T v[244:245],19,0
T v[246:247],19,512
T v[248:249],18,4096
T v[250:251],18,4608
T v[252:253],19,4096
T v[254:255],19,4608
L 0
s_barrier
v_mov_b32 v4,s53
s_mov_b32 s82,0
s_mov_b32 s80,0
s_mov_b32 s54,s45
s_mul_i32 s57,s44,7168
s_mul_i32 s55,s45,114688
s_add_u32 s55,s55,s57
s_add_u32 s55,s55,40894464
s_mul_i32 s57,s44,7168
s_mul_i32 s60,s46,114688
s_mul_i32 s61,s46,2560
s_mul_i32 s62,s46,32768
s_mul_i32 s63,s46,2048
s_mul_i32 s64,s46,512
s_lshl_b32 s59,s44,8
s_add_u32 s59,s59,143360
s_mul_i32 s58,s45,2560
s_add_u32 s58,s58,109891584
s_mul_i32 s65,s45,32768
s_add_u32 s65,s65,s47
s_mul_i32 s68,s45,2048
s_add_u32 s68,s68,s48
s_mul_i32 s73,s45,512
s_add_u32 s73,s73,106954752
s_mul_i32 s74,s45,32768
s_add_u32 s74,s74,s49
s_mul_i32 s75,s45,1024
s_add_u32 s75,s75,107479040
s_mov_b32 s66,0x7fff0000
s_mov_b32 s67,0x7fff0000
s_mov_b32 s69,0x7fff0000
s_mov_b32 s76,0x7fff0000
s_mov_b32 s77,0x7fff0000
s_mov_b32 s70,0x7fff0000
s_mov_b32 s71,0x7fff0000
s_mov_b32 s72,0x7fff0000
v_mov_b32 v1,v0
v_add_u32 v2,28672,v0
s_mov_b32 s84,28672
s_mov_b32 s86,0x3a800000
s_mov_b32 s87,0x3727c5ac
s_mov_b32 s88,0xc0135761
s_mov_b32 s89,0x3d372713
s_lshr_b32 s85,s44,1
s_mul_i32 s85,s85,127
s_add_u32 s55,s55,s51
s_mul_i32 s78,s45,114688
s_sub_u32 s60,s55,s78
s_add_u32 s50,s93,221
s_add_u32 s93,s93,167
s_cmp_lt_u32 s45,221
s_cselect_b32 s63,221,-1
s_cmp_lt_u32 s45,167
s_cselect_b32 s63,167,s63
s_cselect_b32 s93,s93,s50
s_cselect_b32 s88,6,7
s_cselect_b32 s89,221,-1
v_lshlrev_b32 v8,22,v0
v_and_b32 v8,0x80000000,v8
v_or_b32 v8,v8,v5
v_add_u32 v2,57344,v0
s_lshl_b32 s84,s57,1
s_add_u32 s84,s84,57344
s_mov_b64 s[86:87],%22
s_mov_b64 s[40:41],%22
s_mov_b32 s42,4194304
s_and_b32 s78,s47,65535
s_lshr_b32 s78,s78,4
s_sub_u32 s85,s47,s78
s_add_u32 s85,s85,4194304
s_mul_i32 s73,s44,1280
s_add_u32 s73,s73,114688
v_lshrrev_b32 v62,4,v9
v_and_b32 v62,3,v62
v_mul_u32_u24 v62,288,v62
v_lshrrev_b32 v63,7,v9
v_and_b32 v5,12,v9
v_add3_u32 v5,v5,v62,v63
v_add_u32 v5,s73,v5
v_mov_b32 v10,0
v_mov_b32 v11,0
v_mov_b32 v12,0
v_mov_b32 v13,0
s_mov_b32 s79,0
buffer_store_dwordx2 v[10:11],v7,s[40:43],s79 offen offset:0 sc0 sc1
s_mov_b32 s79,163840
buffer_store_dwordx2 v[10:11],v7,s[40:43],s79 offen offset:0 sc0 sc1
s_mov_b32 s79,327680
buffer_store_dwordx2 v[10:11],v7,s[40:43],s79 offen offset:0 sc0 sc1
s_mov_b32 s79,491520
buffer_store_dwordx2 v[10:11],v7,s[40:43],s79 offen offset:0 sc0 sc1
s_getpc_b64 s[40:41]
Lg4_pc_%=:
s_lshl_b32 s78,s44,13
v_lshlrev_b32 v62,3,v0
v_add_u32 v62,s78,v62
v_min_u32 v62,Lcode_g4_e-Lg4_pc_%=-4,v62
s_mov_b32 m0,163584
s_nop 0
global_load_lds_dword v62,s[40:41]
s_add_u32 m0,s59,4096
s_nop 0
buffer_load_dword v8,s[36:39],s58 offen offset:0 sc1 lds
buffer_store_dword v4,v3,s[36:39],s71 offen offset:0 sc0 sc1
s_add_u32 m0,s59,5120
s_nop 0
buffer_load_dword v8,s[36:39],s58 offen offset:0 sc1 lds
buffer_store_dword v4,v3,s[36:39],s71 offen offset:0 sc0 sc1
s_mov_b32 s83,0
Lg4_poll_%=:
s_add_u32 s78,s83,2
s_and_b32 s78,s78,3
s_lshl_b32 s78,s78,10
s_add_u32 s78,s78,4096
s_add_u32 m0,s59,s78
s_nop 0
buffer_load_dword v8,s[36:39],s58 offen offset:0 sc1 lds
s_and_b32 s78,s83,3
s_lshl_b32 s78,s78,10
v_add_u32 v62,s78,v6
s_waitcnt vmcnt(4)
s_barrier
ds_read_b32 v7,v62 offset:4096
buffer_store_dword v4,v3,s[36:39],s70 offen offset:0 sc0 sc1
s_mov_b32 s70,s71
s_mov_b32 s71,s72
s_mov_b32 s72,0x7fff0000
s_add_u32 s83,s83,1
s_add_u32 s82,s82,1
L 0
v_cmp_eq_u32 vcc,s53,v7
s_nop 0
s_cmp_eq_u32 vcc_lo,-1
s_cbranch_scc1 Lg4_go_%=
s_cmp_lt_u32 s82,20000
s_cbranch_scc1 Lg4_poll_%=
Lg4_go_%=:
s_add_u32 m0,s57,0
s_add_u32 s56,s55,0
D 0,56,0
D 0,56,1024
D 0,56,2048
D 0,56,3072
s_add_u32 m0,s57,4096
s_add_u32 s56,s56,4096
D 0,56,0
D 0,56,1024
D 0,56,2048
s_add_u32 s61,s54,s46
s_cmp_ge_u32 s61,s63
s_cselect_b32 s61,s93,s61
s_cselect_b32 s46,s88,s46
s_cselect_b32 s63,s89,s63
s_cselect_b32 s93,s50,s93
s_cselect_b32 s88,7,s88
s_cselect_b32 s89,-1,s89
s_mul_i32 s58,s61,2560
s_add_u32 s58,s58,109891584
s_add_u32 m0,s59,2048
s_nop 0
buffer_load_dword v8,s[36:39],s58 offen offset:0 sc1 lds
s_add_u32 m0,s57,28672
s_add_u32 s56,s55,28672
D 0,56,0
D 0,56,1024
D 0,56,2048
D 0,56,3072
s_add_u32 m0,s57,32768
s_add_u32 s56,s56,4096
D 0,56,0
D 0,56,1024
D 0,56,2048
s_mul_i32 s55,s61,114688
s_add_u32 s55,s55,s60
s_sub_u32 s57,s84,s57
s_mov_b32 s54,s61
s_waitcnt vmcnt(7)
s_barrier
buffer_store_dword v4,v3,s[36:39],s70 offen offset:0 sc0 sc1
s_min_u32 s71,s71,s72
buffer_store_dword v4,v3,s[36:39],s71 offen offset:0 sc0 sc1
s_mov_b32 s70,0x7fff0000
s_mov_b32 s71,0x7fff0000
s_mov_b32 s72,0x7fff0000
s_cmp_eq_u32 s80,1
s_cbranch_scc1 Lg4_st1_%=
s_branch Lg4_st0_%=
Lg4_gox_%=:
s_add_u32 m0,s59,2048
s_nop 0
buffer_load_dword v8,s[36:39],s58 offen offset:0 sc1 lds
s_waitcnt vmcnt(0)
s_barrier
buffer_store_dword v4,v3,s[36:39],s70 offen offset:0 sc0 sc1
s_min_u32 s71,s71,s72
buffer_store_dword v4,v3,s[36:39],s71 offen offset:0 sc0 sc1
s_mov_b32 s70,0x7fff0000
s_mov_b32 s71,0x7fff0000
s_mov_b32 s72,0x7fff0000
s_cmp_eq_u32 s80,1
s_cbranch_scc1 Lg4_st1_%=
Lg4_st0_%=:
ds_read_b32 v7,v6 offset:2048
R 26,1,0
R 30,1,1024
R 34,1,2048
R 38,1,3072
R 42,1,4096
L 5
v_cmp_eq_u32 vcc,s53,v7
L 3
s_cmp_eq_u32 vcc_lo,-1
s_cbranch_scc1 Lg4_F0_%=
s_branch Lg4_L0_%=
Lg4_st1_%=:
ds_read_b32 v7,v6 offset:2048
R 26,2,0
R 30,2,1024
R 34,2,2048
R 38,2,3072
R 42,2,4096
L 5
v_cmp_eq_u32 vcc,s53,v7
L 3
s_cmp_eq_u32 vcc_lo,-1
s_cbranch_scc1 Lg4_F1_%=
s_branch Lg4_L1_%=
Lg4_F0_%=:
v_mfma_f32_16x16x32_bf16 v[14:17],a[4:7],v[26:29],0
s_add_u32 m0,s57,0
R 46,1,5120
v_lshlrev_b32 v62,16,v58
M 14,a[12:15],30
s_add_u32 s56,s55,0
v_add_f32 v18,v18,v62
v_and_b32 v63,0xffff0000,v58
v_mfma_f32_16x16x32_bf16 v[10:13],a[0:3],v[26:29],0
s_add_u32 s61,s54,s46
R 50,1,6144
v_add_f32 v19,v19,v63
M 10,a[8:11],30
L 3
D 0,56,0
M 10,a[16:19],34
s_cmp_ge_u32 s61,s63
R 54,1,7168
v_lshlrev_b32 v62,16,v59
M 10,a[24:27],38
s_cselect_b32 s61,s93,s61
v_add_f32 v20,v20,v62
v_and_b32 v63,0xffff0000,v59
M 14,a[20:23],34
)"
R"(s_cselect_b32 s46,s88,s46
R 26,1,8192
v_add_f32 v21,v21,v63
M 14,a[28:31],38
L 3
v_lshlrev_b32 v62,16,v60
v_add_f32 v22,v22,v62
M 14,a[36:39],42
s_cselect_b32 s63,s89,s63
R 30,1,9216
v_and_b32 v63,0xffff0000,v60
M 14,a[44:47],46
D 0,56,1024
M 10,a[32:35],42
s_cselect_b32 s93,s50,s93
R 34,1,10240
v_add_f32 v23,v23,v63
M 10,a[40:43],46
L 3
v_lshlrev_b32 v62,16,v61
v_add_f32 v24,v24,v62
M 10,a[48:51],50
s_cselect_b32 s88,7,s88
R 38,1,11264
v_and_b32 v63,0xffff0000,v61
M 10,a[56:59],54
buffer_load_dwordx4 v[58:61],v0,s[36:39],s74 offen offset:0 sc1
M 14,a[52:55],50
s_cselect_b32 s89,-1,s89
R 42,1,12288
v_add_f32 v25,v25,v63
M 14,a[60:63],54
L 3
v_cvt_pk_bf16_f32 v18,v18,v19
v_cvt_pk_bf16_f32 v19,v20,v21
M 14,a[68:71],26
s_mul_i32 s58,s61,2560
R 46,1,13312
v_cvt_pk_bf16_f32 v20,v22,v23
M 14,a[76:79],30
D 0,56,2048
M 10,a[64:67],26
s_add_u32 s58,s58,109891584
R 50,1,14336
v_cvt_pk_bf16_f32 v21,v24,v25
M 10,a[72:75],30
L 3
buffer_store_dwordx4 v[18:21],v0,s[36:39],s67 offen offset:0 sc0 sc1
M 10,a[80:83],34
s_lshl_b32 s62,s54,15
s_mov_b32 s66,s65
R 54,1,15360
M 10,a[88:91],38
s_add_u32 s74,s49,s62
s_add_u32 s65,s47,s62
s_mov_b32 s54,s61
M 14,a[84:87],34
R 26,1,16384
M 14,a[92:95],38
L 3
M 14,a[100:103],42
R 30,1,17408
M 14,a[108:111],46
D 0,56,3072
M 10,a[96:99],42
s_add_u32 m0,s57,4096
s_add_u32 s56,s56,4096
R 34,1,18432
M 10,a[104:107],46
L 3
M 10,a[112:115],50
R 38,1,19456
M 10,a[120:123],54
M 14,a[116:119],50
R 42,1,20480
M 14,a[124:127],54
L 3
D 0,56,0
M 14,a[132:135],26
R 46,1,21504
M 14,a[140:143],30
M 10,a[128:131],26
R 50,1,22528
M 10,a[136:139],30
L 3
M 10,a[144:147],34
R 54,1,23552
M 10,a[152:155],38
M 14,a[148:151],34
R 26,1,24576
M 14,a[156:159],38
L 3
D 0,56,1024
M 14,a[164:167],42
R 30,1,25600
M 14,a[172:175],46
M 10,a[160:163],42
R 34,1,26624
M 10,a[168:171],46
L 3
M 10,a[176:179],50
s_waitcnt vmcnt(8)
R 38,1,27648
M 10,a[184:187],54
s_barrier
M 14,a[180:183],50
R 42,1,28672
M 14,a[188:191],54
L 3
D 0,56,2048
M 14,a[196:199],26
s_add_u32 m0,s59,0
s_add_u32 s56,s55,28672
R 46,1,29696
M 14,a[204:207],30
s_mul_i32 s55,s61,114688
s_add_u32 s55,s55,s60
M 10,a[192:195],26
R 50,1,30720
M 10,a[200:203],30
L 3
M 10,a[208:211],34
R 54,1,31744
M 10,a[216:219],38
M 14,a[212:215],34
R 26,1,32768
M 14,a[220:223],38
L 3
buffer_load_dword v8,s[36:39],s58 offen offset:0 sc1 lds
M 14,a[228:231],42
s_add_u32 m0,s57,28672
R 30,1,33792
M 14,a[236:239],46
D 0,56,0
M 10,a[224:227],42
R 34,1,34816
M 10,a[232:235],46
L 3
M 10,a[240:243],50
R 38,1,35840
M 10,a[248:251],54
M 14,a[244:247],50
R 42,1,36864
M 14,a[252:255],54
L 3
M 14,v[68:71],26
R 46,1,37888
M 14,v[76:79],30
D 0,56,1024
M 10,v[64:67],26
R 50,1,38912
M 10,v[72:75],30
L 3
M 10,v[80:83],34
R 54,1,39936
M 10,v[88:91],38
M 14,v[84:87],34
R 26,1,40960
M 14,v[92:95],38
L 3
M 14,v[100:103],42
R 30,1,41984
M 14,v[108:111],46
D 0,56,2048
M 10,v[96:99],42
R 34,1,43008
M 10,v[104:107],46
L 3
M 10,v[112:115],50
R 38,1,44032
M 10,v[120:123],54
M 14,v[116:119],50
R 42,1,45056
M 14,v[124:127],54
L 3
D 0,56,3072
M 14,v[132:135],26
s_add_u32 m0,s57,32768
s_add_u32 s56,s56,4096
R 46,1,46080
M 14,v[140:143],30
s_sub_u32 s57,s84,s57
M 10,v[128:131],26
R 50,1,47104
M 10,v[136:139],30
L 3
M 10,v[144:147],34
R 54,1,48128
M 10,v[152:155],38
M 14,v[148:151],34
R 26,1,49152
M 14,v[156:159],38
L 3
D 0,56,0
M 14,v[164:167],42
R 30,1,50176
M 14,v[172:175],46
M 10,v[160:163],42
R 34,1,51200
M 10,v[168:171],46
L 3
M 10,v[176:179],50
R 38,1,52224
M 10,v[184:187],54
M 14,v[180:183],50
R 42,1,53248
M 14,v[188:191],54
L 3
D 0,56,1024
M 14,v[196:199],26
R 46,1,54272
M 14,v[204:207],30
M 10,v[192:195],26
R 50,1,55296
M 10,v[200:203],30
L 3
M 10,v[208:211],34
s_waitcnt vmcnt(6)
R 54,1,56320
M 10,v[216:219],38
s_barrier
ds_read_b32 v7,v6 offset:0
M 14,v[212:215],34
R 26,2,0
M 14,v[220:223],38
L 4
buffer_store_dword v4,v3,s[36:39],s69 offen offset:0 sc0 sc1
M 14,v[228:231],42
s_mov_b32 s69,s68
s_lshr_b32 s68,s62,4
R 30,2,1024
M 14,v[236:239],46
D 0,56,2048
M 10,v[224:227],42
s_add_u32 s68,s68,s48
R 34,2,2048
M 10,v[232:235],46
L 4
L 3
M 10,v[240:243],50
R 38,2,3072
v_cmp_eq_u32 vcc,s53,v7
M 10,v[248:251],54
M 14,v[244:247],50
L 2
s_cmp_eq_u32 vcc_lo,-1
R 42,2,4096
M 14,v[252:255],54
s_cbranch_scc0 Lg4_L1_%=
Lg4_F1_%=:
v_mfma_f32_16x16x32_bf16 v[22:25],a[4:7],v[26:29],0
s_add_u32 m0,s57,0
R 46,2,5120
v_lshlrev_b32 v62,16,v58
M 22,a[12:15],30
s_add_u32 s56,s55,0
v_add_f32 v10,v10,v62
v_and_b32 v63,0xffff0000,v58
v_mfma_f32_16x16x32_bf16 v[18:21],a[0:3],v[26:29],0
s_add_u32 s61,s54,s46
R 50,2,6144
v_add_f32 v11,v11,v63
M 18,a[8:11],30
L 3
D 0,56,0
M 18,a[16:19],34
s_cmp_ge_u32 s61,s63
R 54,2,7168
v_lshlrev_b32 v62,16,v59
M 18,a[24:27],38
s_cselect_b32 s61,s93,s61
v_add_f32 v12,v12,v62
v_and_b32 v63,0xffff0000,v59
M 22,a[20:23],34
s_cselect_b32 s46,s88,s46
R 26,2,8192
v_add_f32 v13,v13,v63
M 22,a[28:31],38
L 3
v_lshlrev_b32 v62,16,v60
v_add_f32 v14,v14,v62
M 22,a[36:39],42
s_cselect_b32 s63,s89,s63
R 30,2,9216
v_and_b32 v63,0xffff0000,v60
M 22,a[44:47],46
D 0,56,1024
M 18,a[32:35],42
s_cselect_b32 s93,s50,s93
R 34,2,10240
v_add_f32 v15,v15,v63
M 18,a[40:43],46
L 3
v_lshlrev_b32 v62,16,v61
v_add_f32 v16,v16,v62
M 18,a[48:51],50
s_cselect_b32 s88,7,s88
R 38,2,11264
v_and_b32 v63,0xffff0000,v61
M 18,a[56:59],54
buffer_load_dwordx4 v[58:61],v0,s[36:39],s74 offen offset:0 sc1
M 22,a[52:55],50
s_cselect_b32 s89,-1,s89
R 42,2,12288
v_add_f32 v17,v17,v63
M 22,a[60:63],54
L 3
v_cvt_pk_bf16_f32 v10,v10,v11
v_cvt_pk_bf16_f32 v11,v12,v13
M 22,a[68:71],26
s_mul_i32 s58,s61,2560
R 46,2,13312
v_cvt_pk_bf16_f32 v12,v14,v15
M 22,a[76:79],30
D 0,56,2048
M 18,a[64:67],26
s_add_u32 s58,s58,109891584
R 50,2,14336
v_cvt_pk_bf16_f32 v13,v16,v17
M 18,a[72:75],30
L 3
buffer_store_dwordx4 v[10:13],v0,s[36:39],s66 offen offset:0 sc0 sc1
M 18,a[80:83],34
s_lshl_b32 s62,s54,15
s_mov_b32 s67,s65
R 54,2,15360
M 18,a[88:91],38
s_add_u32 s74,s49,s62
s_add_u32 s65,s47,s62
s_mov_b32 s54,s61
M 22,a[84:87],34
R 26,2,16384
M 22,a[92:95],38
L 3
M 22,a[100:103],42
R 30,2,17408
M 22,a[108:111],46
D 0,56,3072
M 18,a[96:99],42
s_add_u32 m0,s57,4096
s_add_u32 s56,s56,4096
R 34,2,18432
M 18,a[104:107],46
L 3
M 18,a[112:115],50
R 38,2,19456
M 18,a[120:123],54
M 22,a[116:119],50
R 42,2,20480
M 22,a[124:127],54
L 3
D 0,56,0
M 22,a[132:135],26
R 46,2,21504
M 22,a[140:143],30
M 18,a[128:131],26
R 50,2,22528
M 18,a[136:139],30
L 3
M 18,a[144:147],34
R 54,2,23552
M 18,a[152:155],38
M 22,a[148:151],34
R 26,2,24576
M 22,a[156:159],38
L 3
D 0,56,1024
M 22,a[164:167],42
R 30,2,25600
M 22,a[172:175],46
M 18,a[160:163],42
R 34,2,26624
M 18,a[168:171],46
L 3
M 18,a[176:179],50
s_waitcnt vmcnt(8)
R 38,2,27648
M 18,a[184:187],54
s_barrier
M 22,a[180:183],50
R 42,2,28672
M 22,a[188:191],54
L 3
D 0,56,2048
M 22,a[196:199],26
s_add_u32 m0,s59,1024
s_add_u32 s56,s55,28672
R 46,2,29696
M 22,a[204:207],30
s_mul_i32 s55,s61,114688
s_add_u32 s55,s55,s60
M 18,a[192:195],26
R 50,2,30720
M 18,a[200:203],30
L 3
M 18,a[208:211],34
R 54,2,31744
M 18,a[216:219],38
M 22,a[212:215],34
R 26,2,32768
M 22,a[220:223],38
L 3
buffer_load_dword v8,s[36:39],s58 offen offset:0 sc1 lds
M 22,a[228:231],42
s_add_u32 m0,s57,28672
R 30,2,33792
M 22,a[236:239],46
D 0,56,0
M 18,a[224:227],42
R 34,2,34816
M 18,a[232:235],46
L 3
M 18,a[240:243],50
R 38,2,35840
M 18,a[248:251],54
M 22,a[244:247],50
R 42,2,36864
M 22,a[252:255],54
L 3
M 22,v[68:71],26
R 46,2,37888
M 22,v[76:79],30
D 0,56,1024
M 18,v[64:67],26
R 50,2,38912
M 18,v[72:75],30
L 3
M 18,v[80:83],34
R 54,2,39936
M 18,v[88:91],38
M 22,v[84:87],34
R 26,2,40960
M 22,v[92:95],38
L 3
M 22,v[100:103],42
R 30,2,41984
M 22,v[108:111],46
D 0,56,2048
M 18,v[96:99],42
R 34,2,43008
M 18,v[104:107],46
L 3
M 18,v[112:115],50
R 38,2,44032
M 18,v[120:123],54
M 22,v[116:119],50
R 42,2,45056
M 22,v[124:127],54
L 3
D 0,56,3072
M 22,v[132:135],26
s_add_u32 m0,s57,32768
s_add_u32 s56,s56,4096
R 46,2,46080
M 22,v[140:143],30
s_sub_u32 s57,s84,s57
M 18,v[128:131],26
R 50,2,47104
M 18,v[136:139],30
L 3
M 18,v[144:147],34
R 54,2,48128
M 18,v[152:155],38
M 22,v[148:151],34
R 26,2,49152
M 22,v[156:159],38
L 3
D 0,56,0
M 22,v[164:167],42
R 30,2,50176
M 22,v[172:175],46
M 18,v[160:163],42
R 34,2,51200
M 18,v[168:171],46
L 3
M 18,v[176:179],50
R 38,2,52224
M 18,v[184:187],54
M 22,v[180:183],50
R 42,2,53248
M 22,v[188:191],54
L 3
D 0,56,1024
M 22,v[196:199],26
R 46,2,54272
M 22,v[204:207],30
M 18,v[192:195],26
R 50,2,55296
M 18,v[200:203],30
L 3
M 18,v[208:211],34
s_waitcnt vmcnt(6)
R 54,2,56320
M 18,v[216:219],38
s_barrier
ds_read_b32 v7,v6 offset:1024
M 22,v[212:215],34
R 26,1,0
M 22,v[220:223],38
L 4
)"
R"(buffer_store_dword v4,v3,s[36:39],s69 offen offset:0 sc0 sc1
M 22,v[228:231],42
s_mov_b32 s69,s68
s_lshr_b32 s68,s62,4
R 30,1,1024
M 22,v[236:239],46
D 0,56,2048
M 18,v[224:227],42
s_add_u32 s68,s68,s48
R 34,1,2048
M 18,v[232:235],46
L 4
L 3
M 18,v[240:243],50
R 38,1,3072
v_cmp_eq_u32 vcc,s53,v7
M 18,v[248:251],54
M 22,v[244:247],50
L 2
s_cmp_eq_u32 vcc_lo,-1
R 42,1,4096
M 22,v[252:255],54
s_cbranch_scc1 Lg4_F0_%=
Lg4_L0_%=:
s_cmp_ge_u32 s54,256
s_cbranch_scc1 Lg4_Z0_%=
v_mfma_f32_16x16x32_bf16 v[14:17],a[4:7],v[26:29],0
s_add_u32 m0,s59,3072
R 46,1,5120
v_lshlrev_b32 v62,16,v58
M 14,a[12:15],30
buffer_load_dword v8,s[36:39],s58 offen offset:0 sc1 lds
v_mfma_f32_16x16x32_bf16 v[10:13],a[0:3],v[26:29],0
s_lshl_b32 s62,s54,15
R 50,1,6144
v_add_f32 v18,v18,v62
M 10,a[8:11],30
L 3
v_and_b32 v63,0xffff0000,v58
v_add_f32 v19,v19,v63
M 10,a[16:19],34
s_mov_b32 s66,s65
R 54,1,7168
v_lshlrev_b32 v62,16,v59
M 10,a[24:27],38
s_add_u32 s65,s47,s62
v_add_f32 v20,v20,v62
v_and_b32 v63,0xffff0000,v59
M 14,a[20:23],34
R 26,1,8192
v_add_f32 v21,v21,v63
M 14,a[28:31],38
L 3
v_lshlrev_b32 v62,16,v60
v_add_f32 v22,v22,v62
M 14,a[36:39],42
R 30,1,9216
v_and_b32 v63,0xffff0000,v60
M 14,a[44:47],46
v_add_f32 v23,v23,v63
v_lshlrev_b32 v62,16,v61
M 10,a[32:35],42
R 34,1,10240
v_add_f32 v24,v24,v62
M 10,a[40:43],46
L 3
v_and_b32 v63,0xffff0000,v61
v_add_f32 v25,v25,v63
M 10,a[48:51],50
R 38,1,11264
v_cvt_pk_bf16_f32 v18,v18,v19
M 10,a[56:59],54
buffer_load_dwordx4 v[58:61],v0,s[36:39],s74 offen offset:0 sc1
M 14,a[52:55],50
s_add_u32 s74,s49,s62
R 42,1,12288
v_cvt_pk_bf16_f32 v19,v20,v21
M 14,a[60:63],54
L 3
v_cvt_pk_bf16_f32 v20,v22,v23
v_cvt_pk_bf16_f32 v21,v24,v25
M 14,a[68:71],26
R 46,1,13312
M 14,a[76:79],30
buffer_store_dwordx4 v[18:21],v0,s[36:39],s67 offen offset:0 sc0 sc1
M 10,a[64:67],26
R 50,1,14336
M 10,a[72:75],30
L 3
M 10,a[80:83],34
R 54,1,15360
M 10,a[88:91],38
M 14,a[84:87],34
R 26,1,16384
M 14,a[92:95],38
L 3
M 14,a[100:103],42
R 30,1,17408
M 14,a[108:111],46
M 10,a[96:99],42
R 34,1,18432
M 10,a[104:107],46
L 3
M 10,a[112:115],50
R 38,1,19456
M 10,a[120:123],54
M 14,a[116:119],50
R 42,1,20480
M 14,a[124:127],54
L 3
M 14,a[132:135],26
R 46,1,21504
M 14,a[140:143],30
M 10,a[128:131],26
R 50,1,22528
M 10,a[136:139],30
L 3
M 10,a[144:147],34
R 54,1,23552
M 10,a[152:155],38
M 14,a[148:151],34
R 26,1,24576
M 14,a[156:159],38
L 3
M 14,a[164:167],42
R 30,1,25600
M 14,a[172:175],46
M 10,a[160:163],42
R 34,1,26624
M 10,a[168:171],46
L 3
M 10,a[176:179],50
s_waitcnt vmcnt(2)
R 38,1,27648
M 10,a[184:187],54
s_barrier
ds_read_b32 v7,v6 offset:3072
M 14,a[180:183],50
R 42,1,28672
M 14,a[188:191],54
L 4
M 14,a[196:199],26
R 46,1,29696
M 14,a[204:207],30
M 10,a[192:195],26
R 50,1,30720
M 10,a[200:203],30
L 4
L 3
v_cmp_eq_u32 vcc,s53,v7
M 10,a[208:211],34
R 54,1,31744
M 10,a[216:219],38
M 14,a[212:215],34
s_cmp_eq_u32 vcc_lo,-1
R 26,1,32768
s_cbranch_scc0 Lg4_X0_%=
M 14,a[220:223],38
L 3
s_add_u32 m0,s57,0
s_add_u32 s56,s55,0
M 14,a[228:231],42
s_add_u32 s61,s54,s46
s_cmp_ge_u32 s61,s63
s_cselect_b32 s61,s93,s61
R 30,1,33792
M 14,a[236:239],46
s_mul_i32 s58,s61,2560
D 0,56,0
M 10,a[224:227],42
s_cselect_b32 s46,s88,s46
s_cselect_b32 s63,s89,s63
R 34,1,34816
M 10,a[232:235],46
L 3
s_cselect_b32 s93,s50,s93
s_cselect_b32 s88,7,s88
M 10,a[240:243],50
s_cselect_b32 s89,-1,s89
s_add_u32 s58,s58,109891584
R 38,1,35840
M 10,a[248:251],54
D 0,56,1024
M 14,a[244:247],50
s_mov_b32 s54,s61
R 42,1,36864
M 14,a[252:255],54
L 3
M 14,v[68:71],26
R 46,1,37888
M 14,v[76:79],30
D 0,56,2048
M 10,v[64:67],26
R 50,1,38912
M 10,v[72:75],30
L 3
M 10,v[80:83],34
R 54,1,39936
M 10,v[88:91],38
D 0,56,3072
M 14,v[84:87],34
s_add_u32 m0,s57,4096
s_add_u32 s56,s56,4096
R 26,1,40960
M 14,v[92:95],38
L 3
M 14,v[100:103],42
R 30,1,41984
M 14,v[108:111],46
D 0,56,0
M 10,v[96:99],42
R 34,1,43008
M 10,v[104:107],46
L 3
M 10,v[112:115],50
R 38,1,44032
M 10,v[120:123],54
D 0,56,1024
M 14,v[116:119],50
R 42,1,45056
M 14,v[124:127],54
L 3
M 14,v[132:135],26
R 46,1,46080
M 14,v[140:143],30
D 0,56,2048
M 10,v[128:131],26
s_add_u32 m0,s59,0
s_add_u32 s56,s55,28672
R 50,1,47104
M 10,v[136:139],30
L 3
buffer_load_dword v8,s[36:39],s58 offen offset:0 sc1 lds
M 10,v[144:147],34
s_add_u32 m0,s57,28672
s_mul_i32 s55,s61,114688
R 54,1,48128
M 10,v[152:155],38
s_add_u32 s55,s55,s60
M 14,v[148:151],34
R 26,1,49152
M 14,v[156:159],38
L 3
D 0,56,0
M 14,v[164:167],42
R 30,1,50176
M 14,v[172:175],46
M 10,v[160:163],42
R 34,1,51200
M 10,v[168:171],46
L 3
D 0,56,1024
M 10,v[176:179],50
R 38,1,52224
M 10,v[184:187],54
D 0,56,2048
M 14,v[180:183],50
R 42,1,53248
M 14,v[188:191],54
L 3
M 14,v[196:199],26
R 46,1,54272
M 14,v[204:207],30
D 0,56,3072
M 10,v[192:195],26
s_add_u32 m0,s57,32768
s_add_u32 s56,s56,4096
R 50,1,55296
M 10,v[200:203],30
L 3
s_sub_u32 s57,s84,s57
M 10,v[208:211],34
s_waitcnt vmcnt(4)
R 54,1,56320
M 10,v[216:219],38
s_barrier
ds_read_b32 v7,v6 offset:0
M 14,v[212:215],34
R 26,2,0
M 14,v[220:223],38
L 4
buffer_store_dword v4,v3,s[36:39],s69 offen offset:0 sc0 sc1
M 14,v[228:231],42
s_mov_b32 s69,s68
s_lshr_b32 s68,s62,4
R 30,2,1024
M 14,v[236:239],46
D 0,56,0
M 10,v[224:227],42
s_add_u32 s68,s68,s48
R 34,2,2048
M 10,v[232:235],46
L 4
D 0,56,1024
M 10,v[240:243],50
L 3
R 38,2,3072
v_cmp_eq_u32 vcc,s53,v7
M 10,v[248:251],54
D 0,56,2048
M 14,v[244:247],50
L 2
s_cmp_eq_u32 vcc_lo,-1
R 42,2,4096
M 14,v[252:255],54
s_cbranch_scc1 Lg4_F1_%=
s_branch Lg4_L1_%=
Lg4_X0_%=:
M 14,a[220:223],38
L 3
s_add_u32 m0,s59,0
M 14,a[228:231],42
R 30,1,33792
M 14,a[236:239],46
buffer_load_dword v8,s[36:39],s58 offen offset:0 sc1 lds
M 10,a[224:227],42
R 34,1,34816
M 10,a[232:235],46
L 3
M 10,a[240:243],50
R 38,1,35840
M 10,a[248:251],54
M 14,a[244:247],50
R 42,1,36864
M 14,a[252:255],54
L 3
M 14,v[68:71],26
R 46,1,37888
M 14,v[76:79],30
M 10,v[64:67],26
R 50,1,38912
M 10,v[72:75],30
L 3
M 10,v[80:83],34
R 54,1,39936
M 10,v[88:91],38
M 14,v[84:87],34
R 26,1,40960
M 14,v[92:95],38
L 3
M 14,v[100:103],42
R 30,1,41984
M 14,v[108:111],46
M 10,v[96:99],42
R 34,1,43008
M 10,v[104:107],46
L 3
M 10,v[112:115],50
R 38,1,44032
M 10,v[120:123],54
M 14,v[116:119],50
R 42,1,45056
M 14,v[124:127],54
L 3
M 14,v[132:135],26
R 46,1,46080
M 14,v[140:143],30
M 10,v[128:131],26
R 50,1,47104
M 10,v[136:139],30
L 3
M 10,v[144:147],34
R 54,1,48128
M 10,v[152:155],38
M 14,v[148:151],34
R 26,1,49152
M 14,v[156:159],38
L 3
M 14,v[164:167],42
R 30,1,50176
M 14,v[172:175],46
M 10,v[160:163],42
R 34,1,51200
M 10,v[168:171],46
L 3
M 10,v[176:179],50
R 38,1,52224
M 10,v[184:187],54
M 14,v[180:183],50
R 42,1,53248
M 14,v[188:191],54
L 3
M 14,v[196:199],26
R 46,1,54272
M 14,v[204:207],30
M 10,v[192:195],26
R 50,1,55296
M 10,v[200:203],30
L 3
M 10,v[208:211],34
s_waitcnt vmcnt(0)
R 54,1,56320
M 10,v[216:219],38
s_barrier
buffer_store_dword v4,v3,s[36:39],s69 offen offset:0 sc0 sc1
M 14,v[212:215],34
s_mov_b32 s69,s68
s_lshr_b32 s68,s62,4
ds_read_b32 v7,v6 offset:0
M 14,v[220:223],38
L 3
s_add_u32 s68,s68,s48
M 14,v[228:231],42
M 14,v[236:239],46
M 10,v[224:227],42
M 10,v[232:235],46
L 1
M 10,v[240:243],50
L 0
v_cmp_eq_u32 vcc,s53,v7
M 10,v[248:251],54
M 14,v[244:247],50
M 14,v[252:255],54
s_cmp_lt_u32 s54,249
s_cbranch_scc1 Lg4_xa0_%=
s_sub_u32 s75,s66,s85
v_lshlrev_b32 v62,16,v58
s_nop 1
v_add_f32 v10,v10,v62
v_and_b32 v63,0xffff0000,v58
v_add_f32 v11,v11,v63
v_lshlrev_b32 v62,16,v59
v_add_f32 v12,v12,v62
v_and_b32 v63,0xffff0000,v59
v_add_f32 v13,v13,v63
v_lshlrev_b32 v62,16,v60
v_add_f32 v14,v14,v62
v_and_b32 v63,0xffff0000,v60
v_add_f32 v15,v15,v63
v_lshlrev_b32 v62,16,v61
v_add_f32 v16,v16,v62
v_and_b32 v63,0xffff0000,v61
v_add_f32 v17,v17,v63
v_cvt_pk_bf16_f32 v10,v10,v11
v_cvt_pk_bf16_f32 v11,v12,v13
v_cvt_pk_bf16_f32 v12,v14,v15
v_cvt_pk_bf16_f32 v13,v16,v17
v_lshrrev_b32 v62,8,v0
v_lshl_add_u32 v62,v62,5,v0
v_add_u32 v62,s73,v62
ds_write_b128 v62,v[10:13] offset:0
ds_read2_b32 v[14:15],v5 offset0:0 offset1:16
ds_read2_b32 v[16:17],v5 offset0:32 offset1:48
s_mov_b64 s[40:41],s[86:87]
s_mov_b32 s42,4194304
s_mov_b32 s43,0x00020000
L 0
s_add_u32 s64,s75,0
buffer_atomic_pk_add_bf16 v14,v9,s[40:43],s64 offen sc1
s_add_u32 s64,s75,8192
buffer_atomic_pk_add_bf16 v15,v9,s[40:43],s64 offen sc1
s_add_u32 s64,s75,16384
buffer_atomic_pk_add_bf16 v16,v9,s[40:43],s64 offen sc1
s_add_u32 s64,s75,24576
buffer_atomic_pk_add_bf16 v17,v9,s[40:43],s64 offen sc1
s_mov_b32 s66,0x7fff0000
s_mov_b32 s69,0x7fff0000
Lg4_xa0_%=:
s_mov_b32 s80,1
s_cmp_eq_u32 vcc_lo,-1
s_cbranch_scc0 Lg4_xn0_%=
s_add_u32 m0,s57,0
s_add_u32 s56,s55,0
D 0,56,0
v_lshlrev_b32 v62,16,v58
D 0,56,1024
v_add_f32 v10,v10,v62
D 0,56,2048
v_and_b32 v63,0xffff0000,v58
D 0,56,3072
v_add_f32 v11,v11,v63
s_add_u32 m0,s57,4096
s_add_u32 s56,s56,4096
D 0,56,0
v_lshlrev_b32 v62,16,v59
D 0,56,1024
v_add_f32 v12,v12,v62
D 0,56,2048
v_and_b32 v63,0xffff0000,v59
s_add_u32 s61,s54,s46
s_cmp_ge_u32 s61,s63
s_cselect_b32 s61,s93,s61
s_cselect_b32 s46,s88,s46
s_cselect_b32 s63,s89,s63
s_cselect_b32 s93,s50,s93
s_cselect_b32 s88,7,s88
s_cselect_b32 s89,-1,s89
s_mul_i32 s58,s61,2560
s_add_u32 s58,s58,109891584
s_add_u32 m0,s57,28672
s_add_u32 s56,s55,28672
D 0,56,0
v_add_f32 v13,v13,v63
D 0,56,1024
v_lshlrev_b32 v62,16,v60
D 0,56,2048
v_add_f32 v14,v14,v62
D 0,56,3072
v_and_b32 v63,0xffff0000,v60
s_add_u32 m0,s57,32768
)"
R"(s_add_u32 s56,s56,4096
D 0,56,0
v_add_f32 v15,v15,v63
D 0,56,1024
v_lshlrev_b32 v62,16,v61
D 0,56,2048
v_add_f32 v16,v16,v62
s_mul_i32 s55,s61,114688
s_add_u32 s55,s55,s60
s_sub_u32 s57,s84,s57
s_mov_b32 s54,s61
v_and_b32 v63,0xffff0000,v61
v_add_f32 v17,v17,v63
v_cvt_pk_bf16_f32 v10,v10,v11
v_cvt_pk_bf16_f32 v11,v12,v13
v_cvt_pk_bf16_f32 v12,v14,v15
v_cvt_pk_bf16_f32 v13,v16,v17
buffer_store_dwordx4 v[10:13],v0,s[36:39],s66 offen offset:0 sc0 sc1
s_mov_b32 s72,s69
s_mov_b32 s69,0x7fff0000
s_mov_b32 s66,0x7fff0000
s_mov_b32 s76,0x7fff0000
s_branch Lg4_gox_%=
Lg4_xn0_%=:
s_cmp_ge_u32 s54,256
s_cbranch_scc1 Lg4_fin0_%=
s_add_u32 m0,s59,4096
s_nop 0
buffer_load_dword v8,s[36:39],s58 offen offset:0 sc1 lds
buffer_store_dword v4,v3,s[36:39],s71 offen offset:0 sc0 sc1
s_add_u32 m0,s59,5120
s_nop 0
buffer_load_dword v8,s[36:39],s58 offen offset:0 sc1 lds
buffer_store_dword v4,v3,s[36:39],s71 offen offset:0 sc0 sc1
s_mov_b32 s83,0
v_lshlrev_b32 v62,16,v58
v_add_f32 v10,v10,v62
v_and_b32 v63,0xffff0000,v58
v_add_f32 v11,v11,v63
v_lshlrev_b32 v62,16,v59
v_add_f32 v12,v12,v62
v_and_b32 v63,0xffff0000,v59
v_add_f32 v13,v13,v63
v_lshlrev_b32 v62,16,v60
v_add_f32 v14,v14,v62
v_and_b32 v63,0xffff0000,v60
v_add_f32 v15,v15,v63
v_lshlrev_b32 v62,16,v61
v_add_f32 v16,v16,v62
v_and_b32 v63,0xffff0000,v61
v_add_f32 v17,v17,v63
v_cvt_pk_bf16_f32 v10,v10,v11
v_cvt_pk_bf16_f32 v11,v12,v13
v_cvt_pk_bf16_f32 v12,v14,v15
v_cvt_pk_bf16_f32 v13,v16,v17
buffer_store_dwordx4 v[10:13],v0,s[36:39],s66 offen offset:0 sc0 sc1
s_mov_b32 s72,s69
s_mov_b32 s69,0x7fff0000
s_mov_b32 s66,0x7fff0000
s_mov_b32 s76,0x7fff0000
s_branch Lg4_poll_%=
Lg4_fin0_%=:
v_lshlrev_b32 v62,16,v58
v_add_f32 v10,v10,v62
v_and_b32 v63,0xffff0000,v58
v_add_f32 v11,v11,v63
v_lshlrev_b32 v62,16,v59
v_add_f32 v12,v12,v62
v_and_b32 v63,0xffff0000,v59
v_add_f32 v13,v13,v63
v_lshlrev_b32 v62,16,v60
v_add_f32 v14,v14,v62
v_and_b32 v63,0xffff0000,v60
v_add_f32 v15,v15,v63
v_lshlrev_b32 v62,16,v61
v_add_f32 v16,v16,v62
v_and_b32 v63,0xffff0000,v61
v_add_f32 v17,v17,v63
v_cvt_pk_bf16_f32 v10,v10,v11
v_cvt_pk_bf16_f32 v11,v12,v13
v_cvt_pk_bf16_f32 v12,v14,v15
v_cvt_pk_bf16_f32 v13,v16,v17
buffer_store_dwordx4 v[10:13],v0,s[36:39],s66 offen offset:0 sc0 sc1
s_mov_b32 s72,s69
s_mov_b32 s69,0x7fff0000
s_mov_b32 s66,0x7fff0000
s_mov_b32 s76,0x7fff0000
s_branch Lg4_done_%=
Lg4_L1_%=:
s_cmp_ge_u32 s54,256
s_cbranch_scc1 Lg4_Z1_%=
v_mfma_f32_16x16x32_bf16 v[22:25],a[4:7],v[26:29],0
s_add_u32 m0,s59,3072
R 46,2,5120
v_lshlrev_b32 v62,16,v58
M 22,a[12:15],30
buffer_load_dword v8,s[36:39],s58 offen offset:0 sc1 lds
v_mfma_f32_16x16x32_bf16 v[18:21],a[0:3],v[26:29],0
s_lshl_b32 s62,s54,15
R 50,2,6144
v_add_f32 v10,v10,v62
M 18,a[8:11],30
L 3
v_and_b32 v63,0xffff0000,v58
v_add_f32 v11,v11,v63
M 18,a[16:19],34
s_mov_b32 s67,s65
R 54,2,7168
v_lshlrev_b32 v62,16,v59
M 18,a[24:27],38
s_add_u32 s65,s47,s62
v_add_f32 v12,v12,v62
v_and_b32 v63,0xffff0000,v59
M 22,a[20:23],34
R 26,2,8192
v_add_f32 v13,v13,v63
M 22,a[28:31],38
L 3
v_lshlrev_b32 v62,16,v60
v_add_f32 v14,v14,v62
M 22,a[36:39],42
R 30,2,9216
v_and_b32 v63,0xffff0000,v60
M 22,a[44:47],46
v_add_f32 v15,v15,v63
v_lshlrev_b32 v62,16,v61
M 18,a[32:35],42
R 34,2,10240
v_add_f32 v16,v16,v62
M 18,a[40:43],46
L 3
v_and_b32 v63,0xffff0000,v61
v_add_f32 v17,v17,v63
M 18,a[48:51],50
R 38,2,11264
v_cvt_pk_bf16_f32 v10,v10,v11
M 18,a[56:59],54
buffer_load_dwordx4 v[58:61],v0,s[36:39],s74 offen offset:0 sc1
M 22,a[52:55],50
s_add_u32 s74,s49,s62
R 42,2,12288
v_cvt_pk_bf16_f32 v11,v12,v13
M 22,a[60:63],54
L 3
v_cvt_pk_bf16_f32 v12,v14,v15
v_cvt_pk_bf16_f32 v13,v16,v17
M 22,a[68:71],26
R 46,2,13312
M 22,a[76:79],30
buffer_store_dwordx4 v[10:13],v0,s[36:39],s66 offen offset:0 sc0 sc1
M 18,a[64:67],26
R 50,2,14336
M 18,a[72:75],30
L 3
M 18,a[80:83],34
R 54,2,15360
M 18,a[88:91],38
M 22,a[84:87],34
R 26,2,16384
M 22,a[92:95],38
L 3
M 22,a[100:103],42
R 30,2,17408
M 22,a[108:111],46
M 18,a[96:99],42
R 34,2,18432
M 18,a[104:107],46
L 3
M 18,a[112:115],50
R 38,2,19456
M 18,a[120:123],54
M 22,a[116:119],50
R 42,2,20480
M 22,a[124:127],54
L 3
M 22,a[132:135],26
R 46,2,21504
M 22,a[140:143],30
M 18,a[128:131],26
R 50,2,22528
M 18,a[136:139],30
L 3
M 18,a[144:147],34
R 54,2,23552
M 18,a[152:155],38
M 22,a[148:151],34
R 26,2,24576
M 22,a[156:159],38
L 3
M 22,a[164:167],42
R 30,2,25600
M 22,a[172:175],46
M 18,a[160:163],42
R 34,2,26624
M 18,a[168:171],46
L 3
M 18,a[176:179],50
s_waitcnt vmcnt(2)
R 38,2,27648
M 18,a[184:187],54
s_barrier
ds_read_b32 v7,v6 offset:3072
M 22,a[180:183],50
R 42,2,28672
M 22,a[188:191],54
L 4
M 22,a[196:199],26
R 46,2,29696
M 22,a[204:207],30
M 18,a[192:195],26
R 50,2,30720
M 18,a[200:203],30
L 4
L 3
v_cmp_eq_u32 vcc,s53,v7
M 18,a[208:211],34
R 54,2,31744
M 18,a[216:219],38
M 22,a[212:215],34
s_cmp_eq_u32 vcc_lo,-1
R 26,2,32768
s_cbranch_scc0 Lg4_X1_%=
M 22,a[220:223],38
L 3
s_add_u32 m0,s57,0
s_add_u32 s56,s55,0
M 22,a[228:231],42
s_add_u32 s61,s54,s46
s_cmp_ge_u32 s61,s63
s_cselect_b32 s61,s93,s61
R 30,2,33792
M 22,a[236:239],46
s_mul_i32 s58,s61,2560
D 0,56,0
M 18,a[224:227],42
s_cselect_b32 s46,s88,s46
s_cselect_b32 s63,s89,s63
R 34,2,34816
M 18,a[232:235],46
L 3
s_cselect_b32 s93,s50,s93
s_cselect_b32 s88,7,s88
M 18,a[240:243],50
s_cselect_b32 s89,-1,s89
s_add_u32 s58,s58,109891584
R 38,2,35840
M 18,a[248:251],54
D 0,56,1024
M 22,a[244:247],50
s_mov_b32 s54,s61
R 42,2,36864
M 22,a[252:255],54
L 3
M 22,v[68:71],26
R 46,2,37888
M 22,v[76:79],30
D 0,56,2048
M 18,v[64:67],26
R 50,2,38912
M 18,v[72:75],30
L 3
M 18,v[80:83],34
R 54,2,39936
M 18,v[88:91],38
D 0,56,3072
M 22,v[84:87],34
s_add_u32 m0,s57,4096
s_add_u32 s56,s56,4096
R 26,2,40960
M 22,v[92:95],38
L 3
M 22,v[100:103],42
R 30,2,41984
M 22,v[108:111],46
D 0,56,0
M 18,v[96:99],42
R 34,2,43008
M 18,v[104:107],46
L 3
M 18,v[112:115],50
R 38,2,44032
M 18,v[120:123],54
D 0,56,1024
M 22,v[116:119],50
R 42,2,45056
M 22,v[124:127],54
L 3
M 22,v[132:135],26
R 46,2,46080
M 22,v[140:143],30
D 0,56,2048
M 18,v[128:131],26
s_add_u32 m0,s59,1024
s_add_u32 s56,s55,28672
R 50,2,47104
M 18,v[136:139],30
L 3
buffer_load_dword v8,s[36:39],s58 offen offset:0 sc1 lds
M 18,v[144:147],34
s_add_u32 m0,s57,28672
s_mul_i32 s55,s61,114688
R 54,2,48128
M 18,v[152:155],38
s_add_u32 s55,s55,s60
M 22,v[148:151],34
R 26,2,49152
M 22,v[156:159],38
L 3
D 0,56,0
M 22,v[164:167],42
R 30,2,50176
M 22,v[172:175],46
M 18,v[160:163],42
R 34,2,51200
M 18,v[168:171],46
L 3
D 0,56,1024
M 18,v[176:179],50
R 38,2,52224
M 18,v[184:187],54
D 0,56,2048
M 22,v[180:183],50
R 42,2,53248
M 22,v[188:191],54
L 3
M 22,v[196:199],26
R 46,2,54272
M 22,v[204:207],30
D 0,56,3072
M 18,v[192:195],26
s_add_u32 m0,s57,32768
s_add_u32 s56,s56,4096
R 50,2,55296
M 18,v[200:203],30
L 3
s_sub_u32 s57,s84,s57
M 18,v[208:211],34
s_waitcnt vmcnt(4)
R 54,2,56320
M 18,v[216:219],38
s_barrier
ds_read_b32 v7,v6 offset:1024
M 22,v[212:215],34
R 26,1,0
M 22,v[220:223],38
L 4
buffer_store_dword v4,v3,s[36:39],s69 offen offset:0 sc0 sc1
M 22,v[228:231],42
s_mov_b32 s69,s68
s_lshr_b32 s68,s62,4
R 30,1,1024
M 22,v[236:239],46
D 0,56,0
M 18,v[224:227],42
s_add_u32 s68,s68,s48
R 34,1,2048
M 18,v[232:235],46
L 4
D 0,56,1024
M 18,v[240:243],50
L 3
R 38,1,3072
v_cmp_eq_u32 vcc,s53,v7
M 18,v[248:251],54
D 0,56,2048
M 22,v[244:247],50
L 2
s_cmp_eq_u32 vcc_lo,-1
R 42,1,4096
M 22,v[252:255],54
s_cbranch_scc1 Lg4_F0_%=
s_branch Lg4_L0_%=
Lg4_X1_%=:
M 22,a[220:223],38
L 3
s_add_u32 m0,s59,1024
M 22,a[228:231],42
R 30,2,33792
M 22,a[236:239],46
buffer_load_dword v8,s[36:39],s58 offen offset:0 sc1 lds
M 18,a[224:227],42
R 34,2,34816
M 18,a[232:235],46
L 3
M 18,a[240:243],50
R 38,2,35840
M 18,a[248:251],54
M 22,a[244:247],50
R 42,2,36864
M 22,a[252:255],54
L 3
M 22,v[68:71],26
R 46,2,37888
M 22,v[76:79],30
M 18,v[64:67],26
R 50,2,38912
M 18,v[72:75],30
L 3
M 18,v[80:83],34
R 54,2,39936
M 18,v[88:91],38
M 22,v[84:87],34
R 26,2,40960
M 22,v[92:95],38
L 3
M 22,v[100:103],42
R 30,2,41984
M 22,v[108:111],46
M 18,v[96:99],42
R 34,2,43008
M 18,v[104:107],46
L 3
M 18,v[112:115],50
R 38,2,44032
M 18,v[120:123],54
M 22,v[116:119],50
R 42,2,45056
M 22,v[124:127],54
L 3
M 22,v[132:135],26
R 46,2,46080
M 22,v[140:143],30
M 18,v[128:131],26
R 50,2,47104
M 18,v[136:139],30
L 3
M 18,v[144:147],34
R 54,2,48128
M 18,v[152:155],38
M 22,v[148:151],34
R 26,2,49152
M 22,v[156:159],38
L 3
M 22,v[164:167],42
R 30,2,50176
M 22,v[172:175],46
M 18,v[160:163],42
R 34,2,51200
M 18,v[168:171],46
L 3
M 18,v[176:179],50
R 38,2,52224
M 18,v[184:187],54
M 22,v[180:183],50
R 42,2,53248
M 22,v[188:191],54
L 3
M 22,v[196:199],26
R 46,2,54272
M 22,v[204:207],30
M 18,v[192:195],26
R 50,2,55296
M 18,v[200:203],30
L 3
M 18,v[208:211],34
s_waitcnt vmcnt(0)
R 54,2,56320
M 18,v[216:219],38
s_barrier
buffer_store_dword v4,v3,s[36:39],s69 offen offset:0 sc0 sc1
M 22,v[212:215],34
s_mov_b32 s69,s68
s_lshr_b32 s68,s62,4
ds_read_b32 v7,v6 offset:1024
M 22,v[220:223],38
L 3
s_add_u32 s68,s68,s48
M 22,v[228:231],42
M 22,v[236:239],46
M 18,v[224:227],42
M 18,v[232:235],46
L 1
M 18,v[240:243],50
L 0
v_cmp_eq_u32 vcc,s53,v7
M 18,v[248:251],54
M 22,v[244:247],50
M 22,v[252:255],54
s_cmp_lt_u32 s54,249
s_cbranch_scc1 Lg4_xa1_%=
s_sub_u32 s75,s67,s85
v_lshlrev_b32 v62,16,v58
s_nop 1
v_add_f32 v18,v18,v62
v_and_b32 v63,0xffff0000,v58
v_add_f32 v19,v19,v63
v_lshlrev_b32 v62,16,v59
v_add_f32 v20,v20,v62
v_and_b32 v63,0xffff0000,v59
v_add_f32 v21,v21,v63
v_lshlrev_b32 v62,16,v60
v_add_f32 v22,v22,v62
v_and_b32 v63,0xffff0000,v60
v_add_f32 v23,v23,v63
v_lshlrev_b32 v62,16,v61
v_add_f32 v24,v24,v62
v_and_b32 v63,0xffff0000,v61
v_add_f32 v25,v25,v63
v_cvt_pk_bf16_f32 v18,v18,v19
v_cvt_pk_bf16_f32 v19,v20,v21
)"
R"(v_cvt_pk_bf16_f32 v20,v22,v23
v_cvt_pk_bf16_f32 v21,v24,v25
v_lshrrev_b32 v62,8,v0
v_lshl_add_u32 v62,v62,5,v0
v_add_u32 v62,s73,v62
ds_write_b128 v62,v[18:21] offset:0
ds_read2_b32 v[22:23],v5 offset0:0 offset1:16
ds_read2_b32 v[24:25],v5 offset0:32 offset1:48
s_mov_b64 s[40:41],s[86:87]
s_mov_b32 s42,4194304
s_mov_b32 s43,0x00020000
L 0
s_add_u32 s64,s75,0
buffer_atomic_pk_add_bf16 v22,v9,s[40:43],s64 offen sc1
s_add_u32 s64,s75,8192
buffer_atomic_pk_add_bf16 v23,v9,s[40:43],s64 offen sc1
s_add_u32 s64,s75,16384
buffer_atomic_pk_add_bf16 v24,v9,s[40:43],s64 offen sc1
s_add_u32 s64,s75,24576
buffer_atomic_pk_add_bf16 v25,v9,s[40:43],s64 offen sc1
s_mov_b32 s67,0x7fff0000
s_mov_b32 s69,0x7fff0000
Lg4_xa1_%=:
s_mov_b32 s80,0
s_cmp_eq_u32 vcc_lo,-1
s_cbranch_scc0 Lg4_xn1_%=
s_add_u32 m0,s57,0
s_add_u32 s56,s55,0
D 0,56,0
v_lshlrev_b32 v62,16,v58
D 0,56,1024
v_add_f32 v18,v18,v62
D 0,56,2048
v_and_b32 v63,0xffff0000,v58
D 0,56,3072
v_add_f32 v19,v19,v63
s_add_u32 m0,s57,4096
s_add_u32 s56,s56,4096
D 0,56,0
v_lshlrev_b32 v62,16,v59
D 0,56,1024
v_add_f32 v20,v20,v62
D 0,56,2048
v_and_b32 v63,0xffff0000,v59
s_add_u32 s61,s54,s46
s_cmp_ge_u32 s61,s63
s_cselect_b32 s61,s93,s61
s_cselect_b32 s46,s88,s46
s_cselect_b32 s63,s89,s63
s_cselect_b32 s93,s50,s93
s_cselect_b32 s88,7,s88
s_cselect_b32 s89,-1,s89
s_mul_i32 s58,s61,2560
s_add_u32 s58,s58,109891584
s_add_u32 m0,s57,28672
s_add_u32 s56,s55,28672
D 0,56,0
v_add_f32 v21,v21,v63
D 0,56,1024
v_lshlrev_b32 v62,16,v60
D 0,56,2048
v_add_f32 v22,v22,v62
D 0,56,3072
v_and_b32 v63,0xffff0000,v60
s_add_u32 m0,s57,32768
s_add_u32 s56,s56,4096
D 0,56,0
v_add_f32 v23,v23,v63
D 0,56,1024
v_lshlrev_b32 v62,16,v61
D 0,56,2048
v_add_f32 v24,v24,v62
s_mul_i32 s55,s61,114688
s_add_u32 s55,s55,s60
s_sub_u32 s57,s84,s57
s_mov_b32 s54,s61
v_and_b32 v63,0xffff0000,v61
v_add_f32 v25,v25,v63
v_cvt_pk_bf16_f32 v18,v18,v19
v_cvt_pk_bf16_f32 v19,v20,v21
v_cvt_pk_bf16_f32 v20,v22,v23
v_cvt_pk_bf16_f32 v21,v24,v25
buffer_store_dwordx4 v[18:21],v0,s[36:39],s67 offen offset:0 sc0 sc1
s_mov_b32 s72,s69
s_mov_b32 s69,0x7fff0000
s_mov_b32 s67,0x7fff0000
s_mov_b32 s77,0x7fff0000
s_branch Lg4_gox_%=
Lg4_xn1_%=:
s_cmp_ge_u32 s54,256
s_cbranch_scc1 Lg4_fin1_%=
s_add_u32 m0,s59,4096
s_nop 0
buffer_load_dword v8,s[36:39],s58 offen offset:0 sc1 lds
buffer_store_dword v4,v3,s[36:39],s71 offen offset:0 sc0 sc1
s_add_u32 m0,s59,5120
s_nop 0
buffer_load_dword v8,s[36:39],s58 offen offset:0 sc1 lds
buffer_store_dword v4,v3,s[36:39],s71 offen offset:0 sc0 sc1
s_mov_b32 s83,0
v_lshlrev_b32 v62,16,v58
v_add_f32 v18,v18,v62
v_and_b32 v63,0xffff0000,v58
v_add_f32 v19,v19,v63
v_lshlrev_b32 v62,16,v59
v_add_f32 v20,v20,v62
v_and_b32 v63,0xffff0000,v59
v_add_f32 v21,v21,v63
v_lshlrev_b32 v62,16,v60
v_add_f32 v22,v22,v62
v_and_b32 v63,0xffff0000,v60
v_add_f32 v23,v23,v63
v_lshlrev_b32 v62,16,v61
v_add_f32 v24,v24,v62
v_and_b32 v63,0xffff0000,v61
v_add_f32 v25,v25,v63
v_cvt_pk_bf16_f32 v18,v18,v19
v_cvt_pk_bf16_f32 v19,v20,v21
v_cvt_pk_bf16_f32 v20,v22,v23
v_cvt_pk_bf16_f32 v21,v24,v25
buffer_store_dwordx4 v[18:21],v0,s[36:39],s67 offen offset:0 sc0 sc1
s_mov_b32 s72,s69
s_mov_b32 s69,0x7fff0000
s_mov_b32 s67,0x7fff0000
s_mov_b32 s77,0x7fff0000
s_branch Lg4_poll_%=
Lg4_fin1_%=:
v_lshlrev_b32 v62,16,v58
v_add_f32 v18,v18,v62
v_and_b32 v63,0xffff0000,v58
v_add_f32 v19,v19,v63
v_lshlrev_b32 v62,16,v59
v_add_f32 v20,v20,v62
v_and_b32 v63,0xffff0000,v59
v_add_f32 v21,v21,v63
v_lshlrev_b32 v62,16,v60
v_add_f32 v22,v22,v62
v_and_b32 v63,0xffff0000,v60
v_add_f32 v23,v23,v63
v_lshlrev_b32 v62,16,v61
v_add_f32 v24,v24,v62
v_and_b32 v63,0xffff0000,v61
v_add_f32 v25,v25,v63
v_cvt_pk_bf16_f32 v18,v18,v19
v_cvt_pk_bf16_f32 v19,v20,v21
v_cvt_pk_bf16_f32 v20,v22,v23
v_cvt_pk_bf16_f32 v21,v24,v25
buffer_store_dwordx4 v[18:21],v0,s[36:39],s67 offen offset:0 sc0 sc1
s_mov_b32 s72,s69
s_mov_b32 s69,0x7fff0000
s_mov_b32 s67,0x7fff0000
s_mov_b32 s77,0x7fff0000
s_branch Lg4_done_%=
Lg4_Z0_%=:
v_mfma_f32_16x16x32_bf16 v[14:17],a[4:7],v[26:29],0
s_sub_u32 s75,s67,s85
R 46,1,5120
v_lshlrev_b32 v62,16,v58
M 14,a[12:15],30
s_mov_b64 s[40:41],s[86:87]
v_add_f32 v18,v18,v62
v_and_b32 v63,0xffff0000,v58
v_mfma_f32_16x16x32_bf16 v[10:13],a[0:3],v[26:29],0
s_mov_b32 s42,4194304
R 50,1,6144
v_add_f32 v19,v19,v63
M 10,a[8:11],30
L 3
v_lshlrev_b32 v62,16,v59
v_add_f32 v20,v20,v62
M 10,a[16:19],34
s_mov_b32 s43,0x00020000
R 54,1,7168
v_and_b32 v63,0xffff0000,v59
M 10,a[24:27],38
s_add_u32 s64,s75,0
v_add_f32 v21,v21,v63
v_lshlrev_b32 v62,16,v60
M 14,a[20:23],34
s_mov_b32 s66,s65
R 26,1,8192
v_add_f32 v22,v22,v62
M 14,a[28:31],38
L 3
v_and_b32 v63,0xffff0000,v60
v_add_f32 v23,v23,v63
M 14,a[36:39],42
R 30,1,9216
v_lshlrev_b32 v62,16,v61
M 14,a[44:47],46
v_add_f32 v24,v24,v62
v_and_b32 v63,0xffff0000,v61
M 10,a[32:35],42
R 34,1,10240
v_add_f32 v25,v25,v63
M 10,a[40:43],46
L 3
buffer_load_dwordx4 v[58:61],v0,s[36:39],s74 offen offset:0 sc1
M 10,a[48:51],50
s_add_u32 s74,s49,s62
R 38,1,11264
v_cvt_pk_bf16_f32 v18,v18,v19
M 10,a[56:59],54
v_cvt_pk_bf16_f32 v19,v20,v21
v_cvt_pk_bf16_f32 v20,v22,v23
M 14,a[52:55],50
R 42,1,12288
v_cvt_pk_bf16_f32 v21,v24,v25
M 14,a[60:63],54
L 3
v_lshrrev_b32 v62,8,v0
v_lshl_add_u32 v62,v62,5,v0
M 14,a[68:71],26
R 46,1,13312
v_add_u32 v62,s73,v62
M 14,a[76:79],30
ds_write_b128 v62,v[18:21] offset:0
M 10,a[64:67],26
R 50,1,14336
ds_read2_b32 v[22:23],v5 offset0:0 offset1:16
M 10,a[72:75],30
L 5
ds_read2_b32 v[24:25],v5 offset0:32 offset1:48
M 10,a[80:83],34
R 54,1,15360
M 10,a[88:91],38
M 14,a[84:87],34
R 26,1,16384
M 14,a[92:95],38
L 6
M 14,a[100:103],42
R 30,1,17408
M 14,a[108:111],46
L 3
buffer_atomic_pk_add_bf16 v22,v9,s[40:43],s64 offen sc1
M 10,a[96:99],42
s_add_u32 s64,s75,8192
R 34,1,18432
M 10,a[104:107],46
L 3
buffer_atomic_pk_add_bf16 v23,v9,s[40:43],s64 offen sc1
M 10,a[112:115],50
s_add_u32 s64,s75,16384
R 38,1,19456
M 10,a[120:123],54
buffer_atomic_pk_add_bf16 v24,v9,s[40:43],s64 offen sc1
M 14,a[116:119],50
s_add_u32 s64,s75,24576
R 42,1,20480
M 14,a[124:127],54
L 3
buffer_atomic_pk_add_bf16 v25,v9,s[40:43],s64 offen sc1
M 14,a[132:135],26
R 46,1,21504
M 14,a[140:143],30
M 10,a[128:131],26
R 50,1,22528
M 10,a[136:139],30
L 3
M 10,a[144:147],34
R 54,1,23552
M 10,a[152:155],38
M 14,a[148:151],34
R 26,1,24576
M 14,a[156:159],38
L 3
M 14,a[164:167],42
R 30,1,25600
M 14,a[172:175],46
M 10,a[160:163],42
R 34,1,26624
M 10,a[168:171],46
L 3
M 10,a[176:179],50
s_waitcnt vmcnt(5)
R 38,1,27648
M 10,a[184:187],54
s_barrier
M 14,a[180:183],50
R 42,1,28672
M 14,a[188:191],54
L 3
M 14,a[196:199],26
R 46,1,29696
M 14,a[204:207],30
M 10,a[192:195],26
R 50,1,30720
M 10,a[200:203],30
L 3
M 10,a[208:211],34
R 54,1,31744
M 10,a[216:219],38
M 14,a[212:215],34
R 26,1,32768
M 14,a[220:223],38
L 3
M 14,a[228:231],42
R 30,1,33792
M 14,a[236:239],46
M 10,a[224:227],42
R 34,1,34816
M 10,a[232:235],46
L 3
M 10,a[240:243],50
R 38,1,35840
M 10,a[248:251],54
M 14,a[244:247],50
R 42,1,36864
M 14,a[252:255],54
L 3
M 14,v[68:71],26
R 46,1,37888
M 14,v[76:79],30
M 10,v[64:67],26
R 50,1,38912
M 10,v[72:75],30
L 3
M 10,v[80:83],34
R 54,1,39936
M 10,v[88:91],38
M 14,v[84:87],34
R 26,1,40960
M 14,v[92:95],38
L 3
M 14,v[100:103],42
R 30,1,41984
M 14,v[108:111],46
M 10,v[96:99],42
R 34,1,43008
M 10,v[104:107],46
L 3
M 10,v[112:115],50
R 38,1,44032
M 10,v[120:123],54
M 14,v[116:119],50
R 42,1,45056
M 14,v[124:127],54
L 3
M 14,v[132:135],26
R 46,1,46080
M 14,v[140:143],30
M 10,v[128:131],26
R 50,1,47104
M 10,v[136:139],30
L 3
M 10,v[144:147],34
R 54,1,48128
M 10,v[152:155],38
M 14,v[148:151],34
R 26,1,49152
M 14,v[156:159],38
L 3
M 14,v[164:167],42
R 30,1,50176
M 14,v[172:175],46
M 10,v[160:163],42
R 34,1,51200
M 10,v[168:171],46
L 3
M 10,v[176:179],50
R 38,1,52224
M 10,v[184:187],54
M 14,v[180:183],50
R 42,1,53248
M 14,v[188:191],54
L 3
M 14,v[196:199],26
R 46,1,54272
M 14,v[204:207],30
M 10,v[192:195],26
R 50,1,55296
M 10,v[200:203],30
L 3
M 10,v[208:211],34
R 54,1,56320
M 10,v[216:219],38
M 14,v[212:215],34
M 14,v[220:223],38
L 2
M 14,v[228:231],42
M 14,v[236:239],46
M 10,v[224:227],42
M 10,v[232:235],46
L 0
M 10,v[240:243],50
M 10,v[248:251],54
M 14,v[244:247],50
M 14,v[252:255],54
s_waitcnt vmcnt(4)
s_sub_u32 s75,s66,s85
v_lshlrev_b32 v62,16,v58
s_nop 2
v_add_f32 v10,v10,v62
v_and_b32 v63,0xffff0000,v58
v_add_f32 v11,v11,v63
v_lshlrev_b32 v62,16,v59
v_add_f32 v12,v12,v62
v_and_b32 v63,0xffff0000,v59
v_add_f32 v13,v13,v63
v_lshlrev_b32 v62,16,v60
v_add_f32 v14,v14,v62
v_and_b32 v63,0xffff0000,v60
v_add_f32 v15,v15,v63
v_lshlrev_b32 v62,16,v61
v_add_f32 v16,v16,v62
v_and_b32 v63,0xffff0000,v61
v_add_f32 v17,v17,v63
v_cvt_pk_bf16_f32 v10,v10,v11
v_cvt_pk_bf16_f32 v11,v12,v13
v_cvt_pk_bf16_f32 v12,v14,v15
v_cvt_pk_bf16_f32 v13,v16,v17
v_lshrrev_b32 v62,8,v0
v_lshl_add_u32 v62,v62,5,v0
v_add_u32 v62,s73,v62
ds_write_b128 v62,v[10:13] offset:0
ds_read2_b32 v[14:15],v5 offset0:0 offset1:16
ds_read2_b32 v[16:17],v5 offset0:32 offset1:48
s_mov_b64 s[40:41],s[86:87]
s_mov_b32 s42,4194304
s_mov_b32 s43,0x00020000
L 0
s_add_u32 s64,s75,0
buffer_atomic_pk_add_bf16 v14,v9,s[40:43],s64 offen sc1
s_add_u32 s64,s75,8192
buffer_atomic_pk_add_bf16 v15,v9,s[40:43],s64 offen sc1
s_add_u32 s64,s75,16384
buffer_atomic_pk_add_bf16 v16,v9,s[40:43],s64 offen sc1
s_add_u32 s64,s75,24576
buffer_atomic_pk_add_bf16 v17,v9,s[40:43],s64 offen sc1
s_branch Lg4_done_%=
Lg4_Z1_%=:
v_mfma_f32_16x16x32_bf16 v[22:25],a[4:7],v[26:29],0
s_sub_u32 s75,s66,s85
R 46,2,5120
v_lshlrev_b32 v62,16,v58
M 22,a[12:15],30
s_mov_b64 s[40:41],s[86:87]
v_add_f32 v10,v10,v62
v_and_b32 v63,0xffff0000,v58
v_mfma_f32_16x16x32_bf16 v[18:21],a[0:3],v[26:29],0
s_mov_b32 s42,4194304
R 50,2,6144
v_add_f32 v11,v11,v63
M 18,a[8:11],30
L 3
v_lshlrev_b32 v62,16,v59
v_add_f32 v12,v12,v62
M 18,a[16:19],34
s_mov_b32 s43,0x00020000
R 54,2,7168
v_and_b32 v63,0xffff0000,v59
M 18,a[24:27],38
s_add_u32 s64,s75,0
v_add_f32 v13,v13,v63
v_lshlrev_b32 v62,16,v60
M 22,a[20:23],34
s_mov_b32 s67,s65
R 26,2,8192
v_add_f32 v14,v14,v62
M 22,a[28:31],38
L 3
v_and_b32 v63,0xffff0000,v60
v_add_f32 v15,v15,v63
M 22,a[36:39],42
R 30,2,9216
v_lshlrev_b32 v62,16,v61
M 22,a[44:47],46
v_add_f32 v16,v16,v62
v_and_b32 v63,0xffff0000,v61
M 18,a[32:35],42
R 34,2,10240
v_add_f32 v17,v17,v63
M 18,a[40:43],46
L 3
buffer_load_dwordx4 v[58:61],v0,s[36:39],s74 offen offset:0 sc1
M 18,a[48:51],50
s_add_u32 s74,s49,s62
R 38,2,11264
v_cvt_pk_bf16_f32 v10,v10,v11
M 18,a[56:59],54
v_cvt_pk_bf16_f32 v11,v12,v13
v_cvt_pk_bf16_f32 v12,v14,v15
M 22,a[52:55],50
R 42,2,12288
v_cvt_pk_bf16_f32 v13,v16,v17
M 22,a[60:63],54
L 3
v_lshrrev_b32 v62,8,v0
v_lshl_add_u32 v62,v62,5,v0
M 22,a[68:71],26
R 46,2,13312
v_add_u32 v62,s73,v62
M 22,a[76:79],30
ds_write_b128 v62,v[10:13] offset:0
M 18,a[64:67],26
R 50,2,14336
ds_read2_b32 v[14:15],v5 offset0:0 offset1:16
M 18,a[72:75],30
L 5
ds_read2_b32 v[16:17],v5 offset0:32 offset1:48
)"
R"(M 18,a[80:83],34
R 54,2,15360
M 18,a[88:91],38
M 22,a[84:87],34
R 26,2,16384
M 22,a[92:95],38
L 6
M 22,a[100:103],42
R 30,2,17408
M 22,a[108:111],46
L 3
buffer_atomic_pk_add_bf16 v14,v9,s[40:43],s64 offen sc1
M 18,a[96:99],42
s_add_u32 s64,s75,8192
R 34,2,18432
M 18,a[104:107],46
L 3
buffer_atomic_pk_add_bf16 v15,v9,s[40:43],s64 offen sc1
M 18,a[112:115],50
s_add_u32 s64,s75,16384
R 38,2,19456
M 18,a[120:123],54
buffer_atomic_pk_add_bf16 v16,v9,s[40:43],s64 offen sc1
M 22,a[116:119],50
s_add_u32 s64,s75,24576
R 42,2,20480
M 22,a[124:127],54
L 3
buffer_atomic_pk_add_bf16 v17,v9,s[40:43],s64 offen sc1
M 22,a[132:135],26
R 46,2,21504
M 22,a[140:143],30
M 18,a[128:131],26
R 50,2,22528
M 18,a[136:139],30
L 3
M 18,a[144:147],34
R 54,2,23552
M 18,a[152:155],38
M 22,a[148:151],34
R 26,2,24576
M 22,a[156:159],38
L 3
M 22,a[164:167],42
R 30,2,25600
M 22,a[172:175],46
M 18,a[160:163],42
R 34,2,26624
M 18,a[168:171],46
L 3
M 18,a[176:179],50
s_waitcnt vmcnt(5)
R 38,2,27648
M 18,a[184:187],54
s_barrier
M 22,a[180:183],50
R 42,2,28672
M 22,a[188:191],54
L 3
M 22,a[196:199],26
R 46,2,29696
M 22,a[204:207],30
M 18,a[192:195],26
R 50,2,30720
M 18,a[200:203],30
L 3
M 18,a[208:211],34
R 54,2,31744
M 18,a[216:219],38
M 22,a[212:215],34
R 26,2,32768
M 22,a[220:223],38
L 3
M 22,a[228:231],42
R 30,2,33792
M 22,a[236:239],46
M 18,a[224:227],42
R 34,2,34816
M 18,a[232:235],46
L 3
M 18,a[240:243],50
R 38,2,35840
M 18,a[248:251],54
M 22,a[244:247],50
R 42,2,36864
M 22,a[252:255],54
L 3
M 22,v[68:71],26
R 46,2,37888
M 22,v[76:79],30
M 18,v[64:67],26
R 50,2,38912
M 18,v[72:75],30
L 3
M 18,v[80:83],34
R 54,2,39936
M 18,v[88:91],38
M 22,v[84:87],34
R 26,2,40960
M 22,v[92:95],38
L 3
M 22,v[100:103],42
R 30,2,41984
M 22,v[108:111],46
M 18,v[96:99],42
R 34,2,43008
M 18,v[104:107],46
L 3
M 18,v[112:115],50
R 38,2,44032
M 18,v[120:123],54
M 22,v[116:119],50
R 42,2,45056
M 22,v[124:127],54
L 3
M 22,v[132:135],26
R 46,2,46080
M 22,v[140:143],30
M 18,v[128:131],26
R 50,2,47104
M 18,v[136:139],30
L 3
M 18,v[144:147],34
R 54,2,48128
M 18,v[152:155],38
M 22,v[148:151],34
R 26,2,49152
M 22,v[156:159],38
L 3
M 22,v[164:167],42
R 30,2,50176
M 22,v[172:175],46
M 18,v[160:163],42
R 34,2,51200
M 18,v[168:171],46
L 3
M 18,v[176:179],50
R 38,2,52224
M 18,v[184:187],54
M 22,v[180:183],50
R 42,2,53248
M 22,v[188:191],54
L 3
M 22,v[196:199],26
R 46,2,54272
M 22,v[204:207],30
M 18,v[192:195],26
R 50,2,55296
M 18,v[200:203],30
L 3
M 18,v[208:211],34
R 54,2,56320
M 18,v[216:219],38
M 22,v[212:215],34
M 22,v[220:223],38
L 2
M 22,v[228:231],42
M 22,v[236:239],46
M 18,v[224:227],42
M 18,v[232:235],46
L 0
M 18,v[240:243],50
M 18,v[248:251],54
M 22,v[244:247],50
M 22,v[252:255],54
s_waitcnt vmcnt(4)
s_sub_u32 s75,s67,s85
v_lshlrev_b32 v62,16,v58
s_nop 2
v_add_f32 v18,v18,v62
v_and_b32 v63,0xffff0000,v58
v_add_f32 v19,v19,v63
v_lshlrev_b32 v62,16,v59
v_add_f32 v20,v20,v62
v_and_b32 v63,0xffff0000,v59
v_add_f32 v21,v21,v63
v_lshlrev_b32 v62,16,v60
v_add_f32 v22,v22,v62
v_and_b32 v63,0xffff0000,v60
v_add_f32 v23,v23,v63
v_lshlrev_b32 v62,16,v61
v_add_f32 v24,v24,v62
v_and_b32 v63,0xffff0000,v61
v_add_f32 v25,v25,v63
v_cvt_pk_bf16_f32 v18,v18,v19
v_cvt_pk_bf16_f32 v19,v20,v21
v_cvt_pk_bf16_f32 v20,v22,v23
v_cvt_pk_bf16_f32 v21,v24,v25
v_lshrrev_b32 v62,8,v0
v_lshl_add_u32 v62,v62,5,v0
v_add_u32 v62,s73,v62
ds_write_b128 v62,v[18:21] offset:0
ds_read2_b32 v[22:23],v5 offset0:0 offset1:16
ds_read2_b32 v[24:25],v5 offset0:32 offset1:48
s_mov_b64 s[40:41],s[86:87]
s_mov_b32 s42,4194304
s_mov_b32 s43,0x00020000
L 0
s_add_u32 s64,s75,0
buffer_atomic_pk_add_bf16 v22,v9,s[40:43],s64 offen sc1
s_add_u32 s64,s75,8192
buffer_atomic_pk_add_bf16 v23,v9,s[40:43],s64 offen sc1
s_add_u32 s64,s75,16384
buffer_atomic_pk_add_bf16 v24,v9,s[40:43],s64 offen sc1
s_add_u32 s64,s75,24576
buffer_atomic_pk_add_bf16 v25,v9,s[40:43],s64 offen sc1
s_branch Lg4_done_%=
Lg4_done_%=:
s_nop 0
s_barrier
buffer_store_dword v4,v3,s[36:39],s70 offen offset:0 sc0 sc1
s_waitcnt vmcnt(1)
s_barrier
buffer_store_dword v4,v3,s[36:39],s72 offen offset:0 sc0 sc1
s_waitcnt vmcnt(0) lgkmcnt(0)
s_barrier
s_endpgm
Lcode_g4_e:
.purgem M
.purgem R
.purgem T
.purgem D
.purgem L
)"
  :: "s"(ws), "s"(sWave), "s"(sG0), "s"(sStep), "s"(sOutOff), "s"(sFoutOff), "s"(sAuxOff), "s"(sWw), "s"(sBuf), "s"(sRk), "s"(sExp), "v"(vL16), "v"(vFlag), "v"(vSnapOff), "v"(vSnapRd), "v"(vWd0), "v"(vWd1), "v"(vWr0), "v"(vWr1), "v"(vSp), "v"(vSn), "s"(w4), "s"(out) : "v255", "v0", "v1", "v2", "v3", "v4", "v5", "v6", "v7", "v8", "v9", "v10", "v11", "v12", "v13", "v14", "v15", "v16", "v17", "v18", "v19", "v20", "v21", "v22", "v23", "v24", "v25", "v26", "v27", "v28", "v29", "v30", "v31", "v32", "v33", "v34", "v35", "v36", "v37", "v38", "v39", "v40", "v41", "v42", "v43", "v44", "v45", "v46", "v47", "v48", "v49", "v50", "v51", "v52", "v53", "v54", "v55", "v56", "v57", "v58", "v59", "v60", "v61", "v62", "v63", "v64", "v65", "v66", "v67", "v68", "v69", "v70", "v71", "v72", "v73", "v74", "v75", "v76", "v77", "v78", "v79", "v80", "v81", "v82", "v83", "v84", "v85", "v86", "v87", "v88", "v89", "v90", "v91", "v92", "v93", "v94", "v95", "v96", "v97", "v98", "v99", "v100", "v101", "v102", "v103", "v104", "v105", "v106", "v107", "v108", "v109", "v110", "v111", "v112", "v113", "v114", "v115", "v116", "v117", "v118", "v119", "v120", "v121", "v122", "v123", "v124", "v125", "v126", "v127", "v128", "v129", "v130", "v131", "v132", "v133", "v134", "v135", "v136", "v137", "v138", "v139", "v140", "v141", "v142", "v143", "v144", "v145", "v146", "v147", "v148", "v149", "v150", "v151", "v152", "v153", "v154", "v155", "v156", "v157", "v158", "v159", "v160", "v161", "v162", "v163", "v164", "v165", "v166", "v167", "v168", "v169", "v170", "v171", "v172", "v173", "v174", "v175", "v176", "v177", "v178", "v179", "v180", "v181", "v182", "v183", "v184", "v185", "v186", "v187", "v188", "v189", "v190", "v191", "v192", "v193", "v194", "v195", "v196", "v197", "v198", "v199", "v200", "v201", "v202", "v203", "v204", "v205", "v206", "v207", "v208", "v209", "v210", "v211", "v212", "v213", "v214", "v215", "v216", "v217", "v218", "v219", "v220", "v221", "v222", "v223", "v224", "v225", "v226", "v227", "v228", "v229", "v230", "v231", "a0", "a1", "a2", "a3", "a4", "a5", "a6", "a7", "a8", "a9", "a10", "a11", "a12", "a13", "a14", "a15", "a16", "a17", "a18", "a19", "a20", "a21", "a22", "a23", "a24", "a25", "a26", "a27", "a28", "a29", "a30", "a31", "a32", "a33", "a34", "a35", "a36", "a37", "a38", "a39", "a40", "a41", "a42", "a43", "a44", "a45", "a46", "a47", "a48", "a49", "a50", "a51", "a52", "a53", "a54", "a55", "a56", "a57", "a58", "a59", "a60", "a61", "a62", "a63", "a64", "a65", "a66", "a67", "a68", "a69", "a70", "a71", "a72", "a73", "a74", "a75", "a76", "a77", "a78", "a79", "a80", "a81", "a82", "a83", "a84", "a85", "a86", "a87", "a88", "a89", "a90", "a91", "a92", "a93", "a94", "a95", "a96", "a97", "a98", "a99", "a100", "a101", "a102", "a103", "a104", "a105", "a106", "a107", "a108", "a109", "a110", "a111", "a112", "a113", "a114", "a115", "a116", "a117", "a118", "a119", "a120", "a121", "a122", "a123", "a124", "a125", "a126", "a127", "a128", "a129", "a130", "a131", "a132", "a133", "a134", "a135", "a136", "a137", "a138", "a139", "a140", "a141", "a142", "a143", "a144", "a145", "a146", "a147", "a148", "a149", "a150", "a151", "a152", "a153", "a154", "a155", "a156", "a157", "a158", "a159", "a160", "a161", "a162", "a163", "a164", "a165", "a166", "a167", "a168", "a169", "a170", "a171", "a172", "a173", "a174", "a175", "a176", "a177", "a178", "a179", "a180", "a181", "a182", "a183", "a184", "a185", "a186", "a187", "a188", "a189", "a190", "a191", "a192", "a193", "a194", "a195", "a196", "a197", "a198", "a199", "a200", "a201", "a202", "a203", "a204", "a205", "a206", "a207", "a208", "a209", "a210", "a211", "a212", "a213", "a214", "a215", "a216", "a217", "a218", "a219", "a220", "a221", "a222", "a223", "a224", "a225", "a226", "a227", "a228", "a229", "a230", "a231", "a232", "a233", "a234", "a235", "a236", "a237", "a238", "a239", "a240", "a241", "a242", "a243", "a244", "a245", "a246", "a247", "a248", "a249", "a250", "a251", "a252", "a253", "a254", "a255", "s36", "s37", "s38", "s39", "s40", "s41", "s42", "s43", "s44", "s45", "s46", "s47", "s48", "s49", "s50", "s51", "s52", "s53", "s54", "s55", "s56", "s57", "s58", "s59", "s60", "s61", "s62", "s63", "s64", "s65", "s66", "s67", "s68", "s69", "s70", "s71", "s72", "s73", "s74", "s75", "s76", "s77", "s78", "s79", "s80", "s81", "s82", "s83", "s84", "s85", "s86", "s87", "s88", "s89", "s90", "s91", "s92", "s93", "s94", "s95", "s96", "s97", "vcc", "scc", "memory");
    __builtin_unreachable();
  } else {
    LANE_ID(lane2);
    constexpr int N1 = 33 - (NR4 - 6) * 16, T1 = RLN * N1;
    const int w = kind == 6 ? 0 : kind == 1 ? 1 + (3 - rep) * 8 + slice : N1 + rep * 8 + slice;
    if (kind != 2) for (int t = w; t < T1; t += N1) row_last(ws, out, t, lane2, wave, ep);
    for (int t = T1 + (w + 32) % (N1 + 32); t < LAST_TASKS; t += N1 + 32) row_last(ws, out, t, lane2, wave, ep);
    if (b == 65 && wave == 0) set_flag(ws + OFF_EP, lane2, ep);
    CTS(14);
    asm volatile("s_endpgm\nLcode_end:" ::: "memory");
    __builtin_unreachable();
  }
}

extern "C" void kernel_launch(void* const* d_in, const int* in_sizes, int n_in, void* d_out, int out_size,
                              void* d_ws, size_t ws_size, hipStream_t stream) {
  const u16* q = (const u16*)d_in[0];
  const u16* k = (const u16*)d_in[1];
  const u16* v = (const u16*)d_in[2];
  const u16* shift_k = (const u16*)d_in[3];
  const u16* ssm_k = (const u16*)d_in[4];
  const u16* ssm_D = (const u16*)d_in[5];
  const u16* w1 = (const u16*)d_in[7];
  const u16* w2 = (const u16*)d_in[8];
  const u16* w3 = (const u16*)d_in[9];
  const u16* w4 = (const u16*)d_in[10];
  flow_kernel<<<256, 256, 0, stream>>>(q, k, v, shift_k, ssm_k, ssm_D, w1, w2, w3, w4, (char*)d_ws, (u16*)d_out);
}
```

```cpp
#include <hip/hip_runtime.h>
#include <hip/hip_bf16.h>
#include <stdint.h>
#include <type_traits>

typedef short s16x8 __attribute__((ext_vector_type(8)));
typedef short s16x4 __attribute__((ext_vector_type(4)));
typedef float f32x4 __attribute__((ext_vector_type(4)));
typedef unsigned short u16;
typedef unsigned short u16x8 __attribute__((ext_vector_type(8)));
typedef unsigned short u16x4 __attribute__((ext_vector_type(4)));
typedef float f32x2 __attribute__((ext_vector_type(2)));
typedef __bf16 bf16x2 __attribute__((ext_vector_type(2)));
typedef unsigned u32x4v __attribute__((ext_vector_type(4)));
typedef __attribute__((address_space(1))) unsigned gu32;

constexpr int S_ = 2048, D_ = 1024, F_ = 3584;
#define PF 1

constexpr int WS_USED = 111243392, LAST_TASKS = 100, ALLZ = 1, U0 = 170, U1 = 224, NR4 = 8, RHELP = 1;
constexpr int OFF_KVR = 4194304, OFF_XP = 8388608, OFF_HS = 17825792, OFF_X1 = 31457280, OFF_G = 40894464, OFF_P0 = 71303168, OFF_P1 = 89128960, OFF_Y = 98566144, OFF_S1 = 106954752, OFF_ST = 107479040, OFF_FL = 109051904, OFF_FXP = 109056000, OFF_FH = 109334528, OFF_FX1 = 109613056, OFF_FG = 109891584, OFF_FP = 110587904, OFF_FKV = 111144960, OFF_FY = 111177728, OFF_EP = 111243264, OFF_DBG = 115343360;

#define WS_DIAG 0

__device__ __forceinline__ float bf2f(u16 v) { return __uint_as_float(((unsigned)v) << 16); }
__device__ __forceinline__ u16 f2bf(float f) { return __builtin_bit_cast(u16, (__bf16)f); }
__device__ __forceinline__ float rbf(float f) { return bf2f(f2bf(f)); }
__device__ __forceinline__ void st16(void* p, u16x8 v) {
  asm volatile("global_store_dwordx4 %0, %1, off sc0 sc1\n\ts_nop 1" :: "v"(p), "v"(v) : "memory");
}
#define WAIT_VM(n) asm volatile("s_waitcnt vmcnt(%0)" ::"n"(n) : "memory")
#define WAIT_LGKM0() asm volatile("s_waitcnt lgkmcnt(0)" ::: "memory")

#define CODE_RANGE(B, E, p, n)                                                                                   \
  const char* p; unsigned n;                                                                                     \
  { unsigned lo_, hi_;                                                                                           \
    asm volatile("s_getpc_b64 s[98:99]\nLpc%=:\n\ts_add_u32 %0, s98, (" B "-Lpc%=)&4294967295\n\ts_addc_u32 %1, s99, (" B "-Lpc%=)>>32\n\t" \
                 "s_mov_b32 %2, " E "-" B : "=s"(lo_), "=s"(hi_), "=s"(n) :: "s98", "s99", "scc");               \
    p = (const char*)(((unsigned long long)hi_ << 32) | lo_); }
__device__ __forceinline__ unsigned touch(const char* p, unsigned n, unsigned off) {
  unsigned t;
  asm volatile("global_load_dword %0, %1, off" : "=v"(t) : "v"(p + (off < n ? off : 0u)) : "memory");
  return t;
}

__device__ __forceinline__ void touch_lds(const char* p, unsigned a) {
  asm volatile("s_mov_b32 m0, %1\n\ts_nop 0\n\tglobal_load_lds_dword %0, off" :: "v"(p), "s"(a) : "memory");
}
#define PFPOL ""
__device__ __forceinline__ void touch_pol(const char* p, unsigned a) {
  asm volatile("s_mov_b32 m0, %1\n\ts_nop 0\n\tglobal_load_lds_dword %0, off " PFPOL :: "v"(p), "s"(a) : "memory");
}
#define YSLEEP 4
#define KSLEEP 2
#define CTS(i)
__device__ __forceinline__ f32x2 up2(unsigned p) { return (f32x2){__uint_as_float(p << 16), __uint_as_float(p & 0xffff0000u)}; }
__device__ __forceinline__ unsigned pk2(f32x2 v) { return __builtin_bit_cast(unsigned, __builtin_convertvector(v, bf16x2)); }
__device__ __forceinline__ f32x2 rbf2(f32x2 v) { return up2(pk2(v)); }
template <int N, class F> __device__ __forceinline__ void sfor(F&& f) {
  if constexpr (N > 0) { sfor<N - 1>(f); f(std::integral_constant<int, N - 1>{}); }
}
__device__ __forceinline__ void set_flag(char* p, int i, unsigned ep) {
  if (i < 32) __hip_atomic_store((gu32*)p + i, ep, __ATOMIC_RELAXED, __HIP_MEMORY_SCOPE_AGENT);
}
template <int NL>
__device__ __forceinline__ void poll_flags(const char* p, int n, int lane, unsigned ep) {
  for (int it = 0; it < 20000; ++it) {
    bool ok = true;
#pragma unroll
    for (int i = 0; i < NL; ++i) {
      const int f = lane + 64 * i;
      ok &= __hip_atomic_load((gu32*)(p + (f < n ? f : 0) * 128), __ATOMIC_RELAXED, __HIP_MEMORY_SCOPE_AGENT) == ep;
    }
    if (__all(ok)) break;
    __builtin_amdgcn_s_sleep(YSLEEP);
  }
  asm volatile("" ::: "memory");
}

constexpr int CV_HP = (2048 + 32) * 2, CV_UR = (2048 + 256) * 2, CV_CH = 2 * CV_HP + CV_UR;
constexpr int NCV = 174;
constexpr int CV_STG = 8 * CV_CH;
constexpr int YP = D_ * 16;

struct KvRegs { u32x4v k0, k1, k2, k3, k4, k5, k6, v0, v1, v2, v3, s0, s1, s2, s3; };
#define KV_LOADS                                                                                                                       \
  "v_lshrrev_b32 %[a], 3, %[tid]\n\tv_and_b32 %[c], 7, %[tid]\n\ts_lshr_b32 s90, %[tile], 4\n\ts_and_b32 s91, %[tile], 15\n\t"          \
  "v_lshlrev_b32 %[a], 13, %[a]\n\tv_lshlrev_b32 %[c], 4, %[c]\n\ts_lshl_b32 s90, s90, 18\n\ts_lshl_b32 s91, s91, 7\n\t"                 \
  "v_add_u32 %[a], s90, %[a]\n\tv_add_u32 %[c], s91, %[c]\n\tv_subrev_u32 %[h], 0x1000, %[a]\n\tv_max_i32 %[h], 0x800, %[h]\n\t"         \
  "v_add3_u32 %[a], %[a], %[c], s92\n\tv_add_u32 %[h], %[h], %[c]\n\tv_lshlrev_b32 %[c], 2, %[c]\n\ts_waitcnt lgkmcnt(0)\n\t"            \
  "global_load_dwordx4 %3, %[a], s[76:77] offset:-4096 nt\n\tglobal_load_dwordx4 %4, %[a], s[76:77] offset:-2048 nt\n\t"                \
  "global_load_dwordx4 %5, %[a], s[76:77] nt\n\tglobal_load_dwordx4 %6, %[a], s[76:77] offset:2048 nt\n\t"                              \
  "global_load_dwordx4 %7, %[a], s[78:79] offset:-4096 nt\n\tglobal_load_dwordx4 %8, %[a], s[78:79] offset:-2048 nt\n\t"                \
  "global_load_dwordx4 %9, %[a], s[78:79] nt\n\tglobal_load_dwordx4 %10, %[a], s[78:79] offset:2048 nt\n\t"                             \
  "global_load_dwordx4 %0, %[h], s[76:77] offset:-2048 nt\n\tglobal_load_dwordx4 %1, %[h], s[76:77] nt\n\t"                             \
  "global_load_dwordx4 %2, %[h], s[76:77] offset:2048 nt\n\t"                                                                           \
  "global_load_dwordx4 %11, %[c], s[80:81]\n\tglobal_load_dwordx4 %12, %[c], s[80:81] offset:16\n\t"                                    \
  "global_load_dwordx4 %13, %[c], s[80:81] offset:32\n\tglobal_load_dwordx4 %14, %[c], s[80:81] offset:48\n\t"
#define KV_OUTS(R) "=&v"(R.k0), "=&v"(R.k1), "=&v"(R.k2), "=&v"(R.k3), "=&v"(R.k4), "=&v"(R.k5), "=&v"(R.k6), "=&v"(R.v0), "=&v"(R.v1), "=&v"(R.v2), \
                   "=&v"(R.v3), "=&v"(R.s0), "=&v"(R.s1), "=&v"(R.s2), "=&v"(R.s3), [a] "=&v"(ta), [h] "=&v"(th), [c] "=&v"(tc)
#define KV_CLOB "s74", "s75", "s76", "s77", "s78", "s79", "s80", "s81", "s82", "s83", "s90", "s91", "s92", "scc", "vcc", "memory"
template <int N>
__device__ __forceinline__ void kv_wait(KvRegs& R) {
  asm volatile("s_waitcnt vmcnt(%15)" : "+v"(R.k0), "+v"(R.k1), "+v"(R.k2), "+v"(R.k3), "+v"(R.k4), "+v"(R.k5), "+v"(R.k6), "+v"(R.v0), "+v"(R.v1), "+v"(R.v2),
               "+v"(R.v3), "+v"(R.s0), "+v"(R.s1), "+v"(R.s2), "+v"(R.s3) : "n"(N) : "memory");
}
__device__ __forceinline__ void kv_store(KvRegs& R, char* __restrict__ ws, char* T, int b, int tid) {
  const int t0 = (b >> 4) * 128, c0 = (b & 15) * 64, ch8 = tid & 7, rg = tid >> 3;
  if (t0 + 4 * rg < 3) R.k0 = R.k1 = R.k2 = (u32x4v){0u, 0u, 0u, 0u};
  typedef unsigned u32x2 __attribute__((ext_vector_type(2)));
  const u32x4v kr[7] = {R.k0, R.k1, R.k2, R.k3, R.k4, R.k5, R.k6}, vr[4] = {R.v0, R.v1, R.v2, R.v3}, sk[4] = {R.s0, R.s1, R.s2, R.s3};
#pragma unroll
  for (int j2 = 0; j2 < 4; ++j2) {
    const f32x2 sa = up2(sk[j2][0]), sb = up2(sk[j2][1]), sc = up2(sk[j2][2]), sd = up2(sk[j2][3]);
    const f32x2 s0 = {sa[0], sc[0]}, s1 = {sa[1], sc[1]}, s2 = {sb[0], sd[0]}, s3 = {sb[1], sd[1]};
    f32x2 kf[7], o[4];
#pragma unroll
    for (int r = 0; r < 7; ++r) kf[r] = up2(kr[r][j2]);
#pragma unroll
    for (int r = 0; r < 4; ++r)
      o[r] = __builtin_elementwise_fma(kf[r + 3], s0, __builtin_elementwise_fma(kf[r + 2], s1, __builtin_elementwise_fma(kf[r + 1], s2, kf[r] * s3))) * up2(vr[r][j2]);
#pragma unroll
    for (int e = 0; e < 2; ++e)
      *(u32x2*)(T + (8 * ch8 + 2 * j2 + e) * 264 + (124 - 4 * rg) * 2) = (u32x2){pk2((f32x2){o[3][e], o[2][e]}), pk2((f32x2){o[1][e], o[0][e]})};
  }
  __syncthreads();
  u16* kvr = (u16*)(ws + OFF_KVR);
#pragma unroll
  for (int i = 0; i < 4; ++i) {
    const int idx = tid + 256 * i, ch = idx >> 4, cb = idx & 15;
    const u16x4 lo = *(const u16x4*)(T + ch * 264 + cb * 16), hi = *(const u16x4*)(T + ch * 264 + cb * 16 + 8);
    st16(kvr + (size_t)(c0 + ch) * S_ + (1920 - t0) + cb * 8, __builtin_shufflevector(lo, hi, 0, 1, 2, 3, 4, 5, 6, 7));
  }
}

#define LASTSLEEP 4
__device__ __forceinline__ void wait_flags(const char* p, unsigned ep) {
  gu32* f = (gu32*)p;
  for (int it = 0; it < 20000; ++it) {
    if (__all(__hip_atomic_load(f, __ATOMIC_RELAXED, __HIP_MEMORY_SCOPE_AGENT) == ep)) break;
    __builtin_amdgcn_s_sleep(LASTSLEEP);
  }
  asm volatile("" ::: "memory");
}
__device__ __forceinline__ void row_last(char* __restrict__ ws, u16* __restrict__ out, int t, int lane, int wave, unsigned ep) {
  const __amdgpu_buffer_rsrc_t rs = __builtin_amdgcn_make_buffer_rsrc(ws, 0, WS_USED, 0x00020000);
  const int r8 = lane & 7, seg = lane >> 3, un = 128 + (t >> 1);
  const unsigned fo = un * 32768 + (wave * 8 + (seg >> 2)) * 1024 + ((seg & 3) * 16 + (t & 1) * 8 + r8) * 16;
  wait_flags(ws + OFF_FP + un * 2048 + ((lane & 2) * 4 + wave * 2 + (lane & 1)) * 128, ep);
  u32x4v p0[4], p1[4];
#pragma unroll
  for (int i = 0; i < 4; ++i) {
    p0[i] = __builtin_bit_cast(u32x4v, __builtin_amdgcn_raw_buffer_load_b128(rs, fo + i * 2048, OFF_P0, 16));
    p1[i] = __builtin_bit_cast(u32x4v, __builtin_amdgcn_raw_buffer_load_b128(rs, fo + i * 2048, OFF_P1, 16));
  }
#pragma unroll
  for (int i = 0; i < 4; ++i) {
    u32x4v o;
#pragma unroll
    for (int e = 0; e < 4; ++e) o[e] = pk2(up2(p0[i][e]) + up2(p1[i][e]));
    st16(out + (size_t)(t * 8 + r8) * D_ + wave * 256 + i * 64 + seg * 8, __builtin_bit_cast(u16x8, o));
  }
}

__device__ __forceinline__ int swzB(int r) { return ((((r >> 1) & 1) | (((r >> 3) & 1) << 1)) << 1); }
#define TCH 1
#define LANE_ID(x) int x; asm volatile("v_mbcnt_lo_u32_b32 %0, -1, 0\n\tv_mbcnt_hi_u32_b32 %0, -1, %0" : "=v"(x))
#define W12 6
#define HL 0
#define W_B1 (b == 1)
#define W_B7 (b == 7)
#define W_I15 (idx == 15)
#define W_I31 (idx == 31)
#define W_B249 (b == 249)
#define RLN 3
#define QTB 1
constexpr int QT0 = 34 * 128;
#define CODE_MAX(x, y) "(((" x ">" y ")&" x ")|((" x "<=" y ")&" y "))"
constexpr int HU = 40960;
#define G3OR 5
constexpr int NRW = 22 - G3OR;
__global__ __launch_bounds__(256, 1) void flow_kernel(const u16* __restrict__ q, const u16* __restrict__ kin, const u16* __restrict__ vin,
                                                      const u16* __restrict__ shift_k, const u16* __restrict__ ssm_k, const u16* __restrict__ ssm_D,
                                                      const u16* __restrict__ w1, const u16* __restrict__ w2,
                                                      const u16* __restrict__ w3, const u16* __restrict__ w4, char* __restrict__ ws,
                                                      u16* __restrict__ out) {
  __shared__ __attribute__((aligned(16))) char lds[163840];
  KvRegs KR;
  unsigned ew, ta, th, tc;
  int b, tile;
  asm volatile("Lcode_b:\n\ts_load_dwordx4 s[76:79], %[ka], 0x8\n\ts_load_dwordx2 s[82:83], %[ka], 0x50\n\t"
               "s_getpc_b64 s[74:75]\n\ts_lshr_b32 s90, %[bi], 3\n\ts_mov_b32 m0, 0x27f00\n\tv_lshlrev_b32 %[a], 5, %[tid]\n\tv_add_lshl_u32 %[a], %[a], s90, 7\n\t"
               "v_min_u32 %[a], " CODE_MAX(CODE_MAX(CODE_MAX("(Lcode_g3_e-Lcode_b)", "(Lcode_g3o_e-Lcode_b)"), CODE_MAX("(Lcode_g4_e-Lcode_b)", "(Lcode_g12_e-Lcode_b)")), "(Lcode_end-Lcode_b)") "-64, %[a]\n\t"
               "global_load_lds_dword %[a], s[74:75]\n\ts_load_dwordx2 s[80:81], %[ka], 0x18\n\t"
               "s_mov_b32 %[b], %[bi]\n\ts_bfe_u32 s91, %[bi], 0x20001\n\ts_lshl2_add_u32 %[tile], s90, s91\n\ts_movk_i32 s92, 0x1000\n\t"
               "s_and_b32 s90, %[bi], 128\n\ts_bitcmp1_b32 %[bi], 0\n\ts_cselect_b32 s90, s90, 64\n\ts_add_u32 %[tile], %[tile], s90\n\t"
               KV_LOADS "v_mov_b32 %[a], %[eo]\n\tglobal_load_dword %[ew], %[a], s[82:83] sc1"
               : KV_OUTS(KR), [ew] "=&v"(ew), [b] "=&s"(b), [tile] "=&s"(tile)
               : [ka] "s"(__builtin_amdgcn_kernarg_segment_ptr()), [bi] "s"(blockIdx.x), [tid] "v"(threadIdx.x), [eo] "n"(OFF_EP) : KV_CLOB);
  const unsigned t_start = 0;
  const int tid = threadIdx.x, lane = tid & 63, wave = __builtin_amdgcn_readfirstlane(tid >> 6);
  const int xcd = b & 7, idx = b >> 3;
  const unsigned sLds = (unsigned)(size_t)lds;
  const int o8 = idx & 7;
  int kind, rep, slice, kh = 0;
  int j3 = 0, j4 = 0;
  const int xp = xcd >> 1, pr = xp & 1;
  if (!(xcd & 1)) {
    if (idx < 20) { kind = 4; rep = idx < 16 ? (xp >> 1) * 3 + (idx >> 3) : 2; kh = pr; slice = idx < 16 ? o8 : (xp >> 1) * 4 + o8; j4 = rep * 16 + kh * 8 + slice; }
    else { kind = 3; rep = pr * 2 + (idx >= 26); slice = (xp >> 1) * 6 + idx - (idx >= 26 ? 26 : 20); j3 = rep * 18 + slice; }
  } else if (idx < 16) { kind = 1 + (idx >> 3); rep = xp; slice = o8; }
  else {
    const int k = idx - 16, n3 = xp < 2 ? 12 : 9, no = xp < 2 ? 0 : 5 - xp;
    if (k < n3) { kind = 3; rep = xp < 2 ? pr * 2 + (k >= 6) : 4; slice = xp < 2 ? 12 + k - 6 * (k >= 6) : pr * 9 + k; j3 = rep * 18 + slice; }
    else if (k < n3 + no) { kind = 5; rep = pr * 3 + k - n3; slice = 18; j3 = 90 + rep; }
    else { kind = 6; rep = 0; slice = xp * 4 + k - n3 - no; }
  }
  CTS(0);
  const unsigned sScr = sLds + 163584;
  unsigned ep = 0;
  auto w_touch = [&](const u16* w) {
#pragma unroll
    for (int i = 0; i < 2; ++i) touch_lds((const char*)w + (size_t)(rep * 256 + i * 128 + (tid >> 1)) * 2048 + slice * 256 + (tid & 1) * 128, sScr);
  };
  {
  const bool isconv = kind >= 3 && kind != 6 && j3 < 94;
  const int nw = (kind == 4 || j3 >= 92) ? 2 : 1, c0 = kind == 4 ? 8 * j4 : j3 < 92 ? 640 + 4 * j3 : j3 < 94 ? 272 + 8 * j3 : 0, cw = c0 + nw * wave;
  const unsigned hd = isconv ? sLds + 2 * wave * CV_CH + 32 : 0x30000u;
  auto h_dma = [&](bool first) {
#pragma unroll
    for (int j = 0; j < 2; ++j) {
      const u16* hp = ssm_k + (size_t)cw * S_ + (isconv && j < nw ? j * S_ + lane * 8 : 0);
      if (first) asm volatile("s_mov_b32 m0, %1\n\ts_nop 0\n\tglobal_load_lds_dwordx4 %0, off nt" :: "v"(hp), "s"(hd + j * CV_CH) : "memory");
      else asm volatile("s_mov_b32 m0, %1\n\ts_nop 0\n\tglobal_load_lds_dwordx4 %0, off offset:1024 nt\n\t"
                        "global_load_lds_dwordx4 %0, off offset:2048 nt\n\tglobal_load_lds_dwordx4 %0, off offset:3072 nt" :: "v"(hp), "s"(hd + j * CV_CH) : "memory");
    }
  };
  h_dma(true);
  if (HL == 0) h_dma(false);
  *(u32x4v*)(lds + (2 * wave + (lane >> 5)) * CV_CH + 2 * CV_HP + 4096 + (lane & 31) * 16) = (u32x4v){0u, 0u, 0u, 0u};
  if (lane < 8) *(u32x4v*)(lds + (2 * wave + (lane >> 2)) * CV_CH + ((lane >> 1) & 1) * CV_HP + (lane & 1) * 16) = (u32x4v){0u, 0u, 0u, 0u};
  kv_wait<HL ? 3 : 9>(KR);
  CTS(12);
  kv_store(KR, ws, lds + CV_STG, tile, tid);
  CTS(1);
  if (HL == 1) h_dma(false);
  const unsigned dw = ((const unsigned*)ssm_D)[cw >> 1];
  asm volatile("s_waitcnt vmcnt(%1)" : "+v"(ew) : "n"(HL == 1 ? 6 : 0) : "memory");
  ep = __builtin_amdgcn_readfirstlane(ew) + 1u;
  if (ep == 0u) ep = 1u;
  __builtin_amdgcn_s_barrier();
  set_flag(ws + OFF_FKV + tile * 128, tid, ep);
  CTS(2);
  if (HL == 2) h_dma(false);
  if (kind < 3 && (W12 & kind) != 0) w_touch(kind == 1 ? w1 : w2);
  if (kind == 2 && (W12 & 4) != 0) w_touch(w1);
  if (isconv) {
    LANE_ID(lane);
    const int tid = wave * 64 + lane;
    const unsigned sC0 = (cw & 1) ? dw & 0xffff0000u : dw << 16, sC1 = dw & 0xffff0000u;
    const int l15 = lane & 15, lg = lane >> 4, cp = (l15 + 1) & 1, iw = kind == 4 ? j4 : 80 + j3, own = 2 * wave * CV_CH;
    const int ch = tid & (4 * nw - 1), tb = nw == 2 ? tid >> 3 : tid >> 2, slot = nw == 2 ? ch : 2 * ch;
    const unsigned sWave = wave, sBuf = nw, sAuxOff = OFF_KVR + cw * 4096, sFoutOff = OFF_FY + iw * 128, sExp = __builtin_amdgcn_readfirstlane(ep), sDbg = 110u * 1048576u + (b * 4 + wave) * 8192;
    const unsigned vL16 = lane * 16, vPA = own + cp * CV_HP + 2 * (8 * lg + l15 + 1 - cp), vPB = own + 2 * CV_HP + 2 * (2032 - 16 * l15 + 8 * lg) - 512 * 7;
    const unsigned vPU = own + 2 * CV_HP + 2 * (2044 - 16 * l15 - 4 * lg) - 512 * 7, vPW = 32 * l15 + 8 * lg;
    const unsigned vOR = slot * CV_CH + tb * 16, vORA = CV_STG + slot * 1024 + tb * 16, vOG = tb * YP + (c0 + ch) * 16;
    const unsigned vFA = ((lane & 3) * 16 + (c0 >> 6)) * 128, vFB = ((4 + lane % 12) * 16 + (c0 >> 6)) * 128, vFlag = tid < 32 ? tid * 4 : 0x80000000u;
    const unsigned vTW = lane < 42 ? (iw * 168 + wave * 42 + lane) * 128 : 0x80000000u;
    const unsigned vTX = lane < 44 ? ((kind == 4 ? j4 : j3 - 12) * 176 + wave * 44 + lane) * 128 : 0x80000000u;
    (void)sDbg;
    asm volatile(
R"(.macro M d,w,x
v_mfma_f32_16x16x32_bf16 v[\d:\d+3], \w, v[\x:\x+3], v[\d:\d+3]
.endm
.macro R d,a,o
ds_read_b128 v[\d:\d+3], v\a offset:\o
.endm
.macro T d,a,o
ds_read_b64_tr_b16 \d, v\a offset:\o
.endm
.macro D a,s,o
buffer_load_dwordx4 v\a, s[36:39], s\s offen offset:\o sc1 lds
.endm
.macro L c
s_waitcnt lgkmcnt(\c)
.endm
Lcode_cv_b:
s_mov_b64 s[36:37],%0
s_mov_b32 s38,111243392
s_mov_b32 s39,0x00020000
s_mov_b32 s44,%1
s_mov_b32 s51,%2
s_mov_b32 s49,%3
s_mov_b32 s48,%4
s_mov_b32 s86,%5
s_mov_b32 s87,%6
s_mov_b32 s53,%7
v_mov_b32 v0,%8
v_mov_b32 v1,%9
v_mov_b32 v2,%10
v_mov_b32 v3,%11
v_mov_b32 v4,%12
v_mov_b32 v5,%13
v_mov_b32 v6,%14
v_mov_b32 v7,%15
v_mov_b32 v8,%16
v_mov_b32 v9,%17
v_mov_b32 v10,%18
v_mov_b32 v11,%19
v_mov_b32 v12,%20
s_mov_b32 s58,111144960
buffer_load_dword v13,v8,s[36:39],s58 offen offset:0 sc1
s_mul_i32 s57,s44,25856
v_add_u32 v18,s57,v0
v_add_u32 v18,32,v18
v_mov_b32 v19,s57
R 88,18,0
ds_read_b32 v92,v18 offset:16
R 152,18,12928
ds_read_b32 v156,v18 offset:12944
ds_read_b32 v24,v19 offset:32
ds_read_b32 v25,v19 offset:12960
s_mov_b64 s[40:41],%21
s_mov_b32 s42,7340032
s_mov_b32 s43,0x00020000
s_mov_b64 s[60:61],%22
s_mov_b32 s62,7340032
s_mov_b32 s63,0x00020000
v_mov_b32 v23,s53
s_mov_b32 s82,0
s_add_u32 s55,s49,4096
s_cmp_eq_u32 s51,2
s_cselect_b32 s55,s55,0x7fff0000
s_cselect_b32 s56,0,0x70000000
s_add_u32 s57,s57,8320
v_add_u32 v14,1024,v1
v_add_u32 v15,2048,v1
v_add_u32 v16,3072,v1
v_add_u32 v17,12928,v1
L 4
v_alignbyte_b32 v96,v89,v88,2
v_alignbyte_b32 v97,v90,v89,2
v_alignbyte_b32 v98,v91,v90,2
v_alignbyte_b32 v99,v92,v91,2
ds_write_b128 v18,v[96:99] offset:4160
L 3
v_alignbyte_b32 v160,v153,v152,2
v_alignbyte_b32 v161,v154,v153,2
v_alignbyte_b32 v162,v155,v154,2
v_alignbyte_b32 v163,v156,v155,2
ds_write_b128 v18,v[160:163] offset:17088
L 2
v_lshlrev_b32 v24,16,v24
v_lshlrev_b32 v25,16,v25
s_mov_b64 exec,1
ds_write_b32 v19,v24 offset:4188
ds_write_b32 v19,v25 offset:17116
s_mov_b64 exec,-1
ds_read2_b32 a[0:1],v1 offset0:0 offset1:1
ds_read2_b32 a[2:3],v1 offset0:2 offset1:3
ds_read2_b32 a[4:5],v1 offset0:128 offset1:129
ds_read2_b32 a[6:7],v1 offset0:130 offset1:131
ds_read2_b32 a[8:9],v1 offset0:16 offset1:17
ds_read2_b32 a[10:11],v1 offset0:18 offset1:19
ds_read2_b32 a[12:13],v1 offset0:144 offset1:145
ds_read2_b32 a[14:15],v1 offset0:146 offset1:147
ds_read2_b32 a[16:17],v1 offset0:32 offset1:33
ds_read2_b32 a[18:19],v1 offset0:34 offset1:35
ds_read2_b32 a[20:21],v1 offset0:160 offset1:161
ds_read2_b32 a[22:23],v1 offset0:162 offset1:163
ds_read2_b32 a[24:25],v1 offset0:48 offset1:49
ds_read2_b32 a[26:27],v1 offset0:50 offset1:51
ds_read2_b32 a[28:29],v1 offset0:176 offset1:177
ds_read2_b32 a[30:31],v1 offset0:178 offset1:179
ds_read2_b32 a[32:33],v1 offset0:64 offset1:65
ds_read2_b32 a[34:35],v1 offset0:66 offset1:67
ds_read2_b32 a[36:37],v1 offset0:192 offset1:193
ds_read2_b32 a[38:39],v1 offset0:194 offset1:195
ds_read2_b32 a[40:41],v1 offset0:80 offset1:81
ds_read2_b32 a[42:43],v1 offset0:82 offset1:83
ds_read2_b32 a[44:45],v1 offset0:208 offset1:209
ds_read2_b32 a[46:47],v1 offset0:210 offset1:211
ds_read2_b32 a[48:49],v1 offset0:96 offset1:97
ds_read2_b32 a[50:51],v1 offset0:98 offset1:99
ds_read2_b32 a[52:53],v1 offset0:224 offset1:225
ds_read2_b32 a[54:55],v1 offset0:226 offset1:227
ds_read2_b32 a[56:57],v1 offset0:112 offset1:113
ds_read2_b32 a[58:59],v1 offset0:114 offset1:115
ds_read2_b32 a[60:61],v1 offset0:240 offset1:241
ds_read2_b32 a[62:63],v1 offset0:242 offset1:243
ds_read2_b32 a[64:65],v17 offset0:0 offset1:1
ds_read2_b32 a[66:67],v17 offset0:2 offset1:3
ds_read2_b32 a[68:69],v17 offset0:128 offset1:129
ds_read2_b32 a[70:71],v17 offset0:130 offset1:131
ds_read2_b32 a[72:73],v17 offset0:16 offset1:17
ds_read2_b32 a[74:75],v17 offset0:18 offset1:19
ds_read2_b32 a[76:77],v17 offset0:144 offset1:145
ds_read2_b32 a[78:79],v17 offset0:146 offset1:147
ds_read2_b32 a[80:81],v17 offset0:32 offset1:33
ds_read2_b32 a[82:83],v17 offset0:34 offset1:35
ds_read2_b32 a[84:85],v17 offset0:160 offset1:161
ds_read2_b32 a[86:87],v17 offset0:162 offset1:163
ds_read2_b32 a[88:89],v17 offset0:48 offset1:49
ds_read2_b32 a[90:91],v17 offset0:50 offset1:51
ds_read2_b32 a[92:93],v17 offset0:176 offset1:177
ds_read2_b32 a[94:95],v17 offset0:178 offset1:179
ds_read2_b32 a[96:97],v17 offset0:64 offset1:65
ds_read2_b32 a[98:99],v17 offset0:66 offset1:67
ds_read2_b32 a[100:101],v17 offset0:192 offset1:193
ds_read2_b32 a[102:103],v17 offset0:194 offset1:195
ds_read2_b32 a[104:105],v17 offset0:80 offset1:81
ds_read2_b32 a[106:107],v17 offset0:82 offset1:83
ds_read2_b32 a[108:109],v17 offset0:208 offset1:209
ds_read2_b32 a[110:111],v17 offset0:210 offset1:211
ds_read2_b32 a[112:113],v17 offset0:96 offset1:97
ds_read2_b32 a[114:115],v17 offset0:98 offset1:99
ds_read2_b32 a[116:117],v17 offset0:224 offset1:225
ds_read2_b32 a[118:119],v17 offset0:226 offset1:227
ds_read2_b32 a[120:121],v17 offset0:112 offset1:113
ds_read2_b32 a[122:123],v17 offset0:114 offset1:115
ds_read2_b32 a[124:125],v17 offset0:240 offset1:241
ds_read2_b32 a[126:127],v17 offset0:242 offset1:243
Lcv_bpa_%=:
s_waitcnt vmcnt(0)
v_cmp_eq_u32 vcc,s53,v13
s_add_u32 s82,s82,1
s_cmp_eq_u64 vcc,-1
s_cbranch_scc1 Lcv_bqa_%=
s_sleep 1
buffer_load_dword v13,v8,s[36:39],s58 offen offset:0 sc1
s_cmp_lt_u32 s82,20000
s_cbranch_scc1 Lcv_bpa_%=
s_waitcnt vmcnt(0)
Lcv_bqa_%=:
s_add_u32 m0,s57,0
s_nop 0
D 0,49,3072
s_add_u32 m0,s57,12928
s_nop 0
D 0,55,3072
buffer_load_dword v13,v9,s[36:39],s58 offen offset:0 sc1
R 88,18,0
ds_read_b32 v92,v18 offset:16
R 104,18,1024
ds_read_b32 v108,v18 offset:1040
R 120,18,2048
ds_read_b32 v124,v18 offset:2064
R 136,18,3072
ds_read_b32 v140,v18 offset:3088
L 6
v_alignbyte_b32 v96,v89,v88,2
v_alignbyte_b32 v97,v90,v89,2
v_alignbyte_b32 v98,v91,v90,2
v_alignbyte_b32 v99,v92,v91,2
ds_write_b128 v18,v[96:99] offset:4160
L 5
v_alignbyte_b32 v112,v105,v104,2
v_alignbyte_b32 v113,v106,v105,2
v_alignbyte_b32 v114,v107,v106,2
v_alignbyte_b32 v115,v108,v107,2
ds_write_b128 v18,v[112:115] offset:5184
L 4
v_alignbyte_b32 v128,v121,v120,2
v_alignbyte_b32 v129,v122,v121,2
v_alignbyte_b32 v130,v123,v122,2
v_alignbyte_b32 v131,v124,v123,2
ds_write_b128 v18,v[128:131] offset:6208
L 3
v_alignbyte_b32 v144,v137,v136,2
v_alignbyte_b32 v145,v138,v137,2
v_alignbyte_b32 v146,v139,v138,2
v_alignbyte_b32 v147,v140,v139,2
ds_write_b128 v18,v[144:147] offset:7232
R 152,18,12928
ds_read_b32 v156,v18 offset:12944
R 168,18,13952
ds_read_b32 v172,v18 offset:13968
R 184,18,14976
ds_read_b32 v188,v18 offset:14992
R 200,18,16000
ds_read_b32 v204,v18 offset:16016
L 6
v_alignbyte_b32 v160,v153,v152,2
v_alignbyte_b32 v161,v154,v153,2
v_alignbyte_b32 v162,v155,v154,2
v_alignbyte_b32 v163,v156,v155,2
ds_write_b128 v18,v[160:163] offset:17088
L 5
v_alignbyte_b32 v176,v169,v168,2
v_alignbyte_b32 v177,v170,v169,2
v_alignbyte_b32 v178,v171,v170,2
v_alignbyte_b32 v179,v172,v171,2
ds_write_b128 v18,v[176:179] offset:18112
L 4
v_alignbyte_b32 v192,v185,v184,2
v_alignbyte_b32 v193,v186,v185,2
v_alignbyte_b32 v194,v187,v186,2
v_alignbyte_b32 v195,v188,v187,2
ds_write_b128 v18,v[192:195] offset:19136
L 3
v_alignbyte_b32 v208,v201,v200,2
v_alignbyte_b32 v209,v202,v201,2
v_alignbyte_b32 v210,v203,v202,2
v_alignbyte_b32 v211,v204,v203,2
ds_write_b128 v18,v[208:211] offset:20160
s_lshl_b32 s78,s44,11
s_add_u32 s78,s78,103424
v_add_u32 v18,s78,v4
v_add_u32 v4,v19,v4
s_waitcnt vmcnt(0)
v_cmp_eq_u32 vcc,s53,v13
s_mov_b32 s83,0
s_cmp_eq_u64 vcc,-1
s_cbranch_scc0 Lcv_a_%=
s_mov_b32 s83,1
s_add_u32 m0,s57,0
s_nop 0
D 0,49,2048
D 0,49,1024
D 0,49,0
s_add_u32 m0,s57,12928
s_nop 0
D 0,55,2048
D 0,55,1024
D 0,55,0
Lcv_a_%=:
s_cmp_eq_u32 s51,2
s_cbranch_scc1 Lcv_a2_%=
L 0
R 88,2,3584
R 92,2,3072
R 96,2,3648
R 100,2,3136
R 104,2,3712
R 108,2,3200
R 112,2,3776
R 116,2,3264
L 6
v_mfma_f32_16x16x32_bf16 v[40:43],a[0:3],v[88:91],0
R 120,2,3840
R 124,2,3328
v_mfma_f32_16x16x32_bf16 v[44:47],a[0:3],v[92:95],0
M 44,a[4:7],88
L 6
M 40,a[8:11],96
R 128,2,3904
R 132,2,3392
M 44,a[8:11],100
M 44,a[12:15],96
L 6
M 40,a[16:19],104
R 136,2,3968
R 140,2,3456
M 44,a[16:19],108
M 44,a[20:23],104
L 6
M 40,a[24:27],112
R 144,2,4032
R 148,2,3520
M 44,a[24:27],116
M 44,a[28:31],112
L 6
M 40,a[32:35],120
M 44,a[32:35],124
M 44,a[36:39],120
L 4
M 40,a[40:43],128
M 44,a[40:43],132
M 44,a[44:47],128
L 2
M 40,a[48:51],136
ds_read_b64 v[24:25],v3 offset:3584
ds_read_b64 v[26:27],v3 offset:3072
M 44,a[48:51],140
M 44,a[52:55],136
L 2
M 40,a[56:59],144
M 44,a[56:59],148
M 44,a[60:63],144
L 0
v_and_b32 v36,0xffff0000,v25
v_lshlrev_b32 v37,16,v25
v_and_b32 v38,0xffff0000,v24
v_lshlrev_b32 v39,16,v24
s_nop 0
v_fma_f32 v40,v36,s86,v40
v_fma_f32 v41,v37,s86,v41
v_fma_f32 v42,v38,s86,v42
v_fma_f32 v43,v39,s86,v43
v_cvt_pk_bf16_f32 v40,v40,v41
v_cvt_pk_bf16_f32 v41,v42,v43
ds_write_b64 v18,v[40:41] offset:0
v_and_b32 v36,0xffff0000,v27
v_lshlrev_b32 v37,16,v27
v_and_b32 v38,0xffff0000,v26
v_lshlrev_b32 v39,16,v26
v_fma_f32 v44,v36,s86,v44
v_fma_f32 v45,v37,s86,v45
v_fma_f32 v46,v38,s86,v46
v_fma_f32 v47,v39,s86,v47
v_cvt_pk_bf16_f32 v44,v44,v45
v_cvt_pk_bf16_f32 v45,v46,v47
ds_write_b64 v18,v[44:45] offset:512
s_branch Lcv_a3_%=
Lcv_a2_%=:
L 0
R 88,2,3584
R 92,2,3072
R 96,2,3648
R 100,2,3136
R 104,2,3712
R 108,2,3200
R 112,2,3776
R 116,2,3264
L 6
v_mfma_f32_16x16x32_bf16 v[40:43],a[0:3],v[88:91],0
R 120,2,3840
R 124,2,3328
v_mfma_f32_16x16x32_bf16 v[44:47],a[0:3],v[92:95],0
M 44,a[4:7],88
L 6
M 40,a[8:11],96
R 128,2,3904
R 132,2,3392
M 44,a[8:11],100
M 44,a[12:15],96
L 6
M 40,a[16:19],104
R 136,2,3968
R 140,2,3456
M 44,a[16:19],108
M 44,a[20:23],104
L 6
M 40,a[24:27],112
R 144,2,4032
R 148,2,3520
M 44,a[24:27],116
M 44,a[28:31],112
L 6
M 40,a[32:35],120
R 152,2,16512
R 156,2,16000
M 44,a[32:35],124
M 44,a[36:39],120
L 6
M 40,a[40:43],128
R 160,2,16576
R 164,2,16064
M 44,a[40:43],132
M 44,a[44:47],128
L 6
M 40,a[48:51],136
R 168,2,16640
R 172,2,16128
M 44,a[48:51],140
ds_read_b64 v[24:25],v3 offset:3584
ds_read_b64 v[26:27],v3 offset:3072
M 44,a[52:55],136
L 8
M 40,a[56:59],144
R 176,2,16704
R 180,2,16192
M 44,a[56:59],148
M 44,a[60:63],144
L 8
v_mfma_f32_16x16x32_bf16 v[48:51],a[64:67],v[152:155],0
R 184,2,16768
R 188,2,16256
v_mfma_f32_16x16x32_bf16 v[52:55],a[64:67],v[156:159],0
M 52,a[68:71],152
L 8
M 48,a[72:75],160
R 192,2,16832
R 196,2,16320
M 52,a[72:75],164
L 6
v_and_b32 v36,0xffff0000,v25
M 52,a[76:79],160
v_lshlrev_b32 v37,16,v25
v_and_b32 v38,0xffff0000,v24
v_lshlrev_b32 v39,16,v24
s_nop 0
M 48,a[80:83],168
R 200,2,16896
R 204,2,16384
M 52,a[80:83],172
v_fma_f32 v40,v36,s86,v40
v_fma_f32 v41,v37,s86,v41
M 52,a[84:87],168
v_fma_f32 v42,v38,s86,v42
v_fma_f32 v43,v39,s86,v43
v_cvt_pk_bf16_f32 v40,v40,v41
L 6
M 48,a[88:91],176
R 208,2,16960
R 212,2,16448
M 52,a[88:91],180
v_cvt_pk_bf16_f32 v41,v42,v43
ds_write_b64 v18,v[40:41] offset:0
M 52,a[92:95],176
v_and_b32 v36,0xffff0000,v27
v_lshlrev_b32 v37,16,v27
v_and_b32 v38,0xffff0000,v26
L 7
M 48,a[96:99],184
M 52,a[96:99],188
v_lshlrev_b32 v39,16,v26
v_fma_f32 v44,v36,s86,v44
M 52,a[100:103],184
v_fma_f32 v45,v37,s86,v45
v_fma_f32 v46,v38,s86,v46
v_fma_f32 v47,v39,s86,v47
L 5
M 48,a[104:107],192
M 52,a[104:107],196
v_cvt_pk_bf16_f32 v44,v44,v45
v_cvt_pk_bf16_f32 v45,v46,v47
M 52,a[108:111],192
ds_write_b64 v18,v[44:45] offset:512
L 4
M 48,a[112:115],200
ds_read_b64 v[24:25],v3 offset:16512
ds_read_b64 v[26:27],v3 offset:16000
M 52,a[112:115],204
M 52,a[116:119],200
L 4
M 48,a[120:123],208
M 52,a[120:123],212
M 52,a[124:127],208
L 0
v_and_b32 v36,0xffff0000,v25
v_lshlrev_b32 v37,16,v25
v_and_b32 v38,0xffff0000,v24
v_lshlrev_b32 v39,16,v24
s_nop 0
v_fma_f32 v48,v36,s87,v48
v_fma_f32 v49,v37,s87,v49
v_fma_f32 v50,v38,s87,v50
v_fma_f32 v51,v39,s87,v51
v_cvt_pk_bf16_f32 v48,v48,v49
v_cvt_pk_bf16_f32 v49,v50,v51
ds_write_b64 v18,v[48:49] offset:1024
v_and_b32 v36,0xffff0000,v27
v_lshlrev_b32 v37,16,v27
v_and_b32 v38,0xffff0000,v26
v_lshlrev_b32 v39,16,v26
v_fma_f32 v52,v36,s87,v52
v_fma_f32 v53,v37,s87,v53
v_fma_f32 v54,v38,s87,v54
v_fma_f32 v55,v39,s87,v55
v_cvt_pk_bf16_f32 v52,v52,v53
v_cvt_pk_bf16_f32 v53,v54,v55
ds_write_b64 v18,v[52:53] offset:1536
Lcv_a3_%=:
L 0
s_barrier
R 152,6,0
R 156,6,512
s_mov_b32 s78,98566144
L 1
buffer_store_dwordx4 v[152:155],v7,s[36:39],s78 offen offset:0 sc0 sc1
s_add_u32 s78,s56,99090432
L 0
buffer_store_dwordx4 v[156:159],v7,s[36:39],s78 offen offset:0 sc0 sc1
s_cmp_eq_u32 s83,1
s_cbranch_scc1 Lcv_b_%=
s_waitcnt vmcnt(0)
s_barrier
buffer_store_dword v23,v10,s[36:39],s48 offen offset:0 sc0 sc1
buffer_load_dword v13,v9,s[36:39],s58 offen offset:0 sc1
Lcv_bpb_%=:
)"
R"(s_waitcnt vmcnt(0)
v_cmp_eq_u32 vcc,s53,v13
s_add_u32 s82,s82,1
s_cmp_eq_u64 vcc,-1
s_cbranch_scc1 Lcv_bqb_%=
s_sleep 1
buffer_load_dword v13,v9,s[36:39],s58 offen offset:0 sc1
s_cmp_lt_u32 s82,20000
s_cbranch_scc1 Lcv_bpb_%=
s_waitcnt vmcnt(0)
Lcv_bqb_%=:
s_add_u32 m0,s57,0
s_nop 0
D 0,49,2048
D 0,49,1024
D 0,49,0
s_add_u32 m0,s57,12928
s_nop 0
D 0,55,2048
D 0,55,1024
D 0,55,0
s_waitcnt vmcnt(0)
Lcv_b_%=:
s_waitcnt vmcnt(2)
ds_read2_b32 v[88:89],v1 offset0:0 offset1:1
ds_read2_b32 v[90:91],v1 offset0:2 offset1:3
R 120,2,3584
ds_read2_b32 v[92:93],v1 offset0:128 offset1:129
ds_read2_b32 v[94:95],v1 offset0:130 offset1:131
R 124,2,3072
ds_read2_b32 v[96:97],v14 offset0:0 offset1:1
ds_read2_b32 v[98:99],v14 offset0:2 offset1:3
R 128,2,2560
ds_read2_b32 v[100:101],v14 offset0:128 offset1:129
ds_read2_b32 v[102:103],v14 offset0:130 offset1:131
R 132,2,2048
ds_read2_b32 v[104:105],v15 offset0:0 offset1:1
ds_read2_b32 v[106:107],v15 offset0:2 offset1:3
R 136,2,1536
ds_read2_b32 v[108:109],v15 offset0:128 offset1:129
ds_read2_b32 v[110:111],v15 offset0:130 offset1:131
R 140,2,1024
ds_read2_b32 v[112:113],v16 offset0:0 offset1:1
ds_read2_b32 v[114:115],v16 offset0:2 offset1:3
R 144,2,512
ds_read2_b32 v[116:117],v16 offset0:128 offset1:129
ds_read2_b32 v[118:119],v16 offset0:130 offset1:131
R 148,2,0
L 15
v_mfma_f32_16x16x32_bf16 v[40:43],v[88:91],v[128:131],0
ds_read2_b32 v[152:153],v1 offset0:16 offset1:17
M 40,v[92:95],124
ds_read2_b32 v[154:155],v1 offset0:18 offset1:19
M 40,v[96:99],120
L 14
R 184,2,3648
v_mfma_f32_16x16x32_bf16 v[44:47],v[88:91],v[132:135],0
ds_read2_b32 v[156:157],v1 offset0:144 offset1:145
M 44,v[92:95],128
M 44,v[96:99],124
ds_read2_b32 v[158:159],v1 offset0:146 offset1:147
M 44,v[100:103],120
L 14
R 188,2,3136
v_mfma_f32_16x16x32_bf16 v[48:51],v[88:91],v[136:139],0
ds_read2_b32 v[160:161],v14 offset0:16 offset1:17
M 48,v[92:95],132
ds_read2_b32 v[162:163],v14 offset0:18 offset1:19
M 48,v[96:99],128
M 48,v[100:103],124
R 192,2,2624
M 48,v[104:107],120
L 15
ds_read2_b32 v[164:165],v14 offset0:144 offset1:145
v_mfma_f32_16x16x32_bf16 v[52:55],v[88:91],v[140:143],0
ds_read2_b32 v[166:167],v14 offset0:146 offset1:147
M 52,v[92:95],136
R 196,2,2112
M 52,v[96:99],132
M 52,v[100:103],128
ds_read2_b32 v[168:169],v15 offset0:16 offset1:17
M 52,v[104:107],124
ds_read2_b32 v[170:171],v15 offset0:18 offset1:19
M 52,v[108:111],120
L 15
R 200,2,1600
v_mfma_f32_16x16x32_bf16 v[56:59],v[88:91],v[144:147],0
ds_read2_b32 v[172:173],v15 offset0:144 offset1:145
M 56,v[92:95],140
M 56,v[96:99],136
ds_read2_b32 v[174:175],v15 offset0:146 offset1:147
M 56,v[100:103],132
R 204,2,1088
M 56,v[104:107],128
ds_read2_b32 v[176:177],v16 offset0:16 offset1:17
M 56,v[108:111],124
ds_read2_b32 v[178:179],v16 offset0:18 offset1:19
M 56,v[112:115],120
L 15
v_mfma_f32_16x16x32_bf16 v[60:63],v[88:91],v[148:151],0
R 208,2,576
M 60,v[92:95],144
ds_read2_b32 v[180:181],v16 offset0:144 offset1:145
M 60,v[96:99],140
ds_read2_b32 v[182:183],v16 offset0:146 offset1:147
M 60,v[100:103],136
R 212,2,64
M 60,v[104:107],132
s_cmp_eq_u32 s83,0
s_cbranch_scc1 Lcv_c_%=
s_waitcnt vmcnt(0)
s_barrier
buffer_store_dword v23,v10,s[36:39],s48 offen offset:0 sc0 sc1
Lcv_c_%=:
M 60,v[108:111],128
M 60,v[112:115],124
M 60,v[116:119],120
L 15
M 40,v[152:155],192
ds_read2_b32 v[88:89],v1 offset0:32 offset1:33
M 40,v[156:159],188
ds_read2_b32 v[90:91],v1 offset0:34 offset1:35
M 40,v[160:163],184
L 14
R 120,2,3712
M 44,v[152:155],196
ds_read2_b32 v[92:93],v1 offset0:160 offset1:161
M 44,v[156:159],192
M 44,v[160:163],188
ds_read2_b32 v[94:95],v1 offset0:162 offset1:163
M 44,v[164:167],184
L 14
R 124,2,3200
M 48,v[152:155],200
ds_read2_b32 v[96:97],v14 offset0:32 offset1:33
M 48,v[156:159],196
ds_read2_b32 v[98:99],v14 offset0:34 offset1:35
M 48,v[160:163],192
M 48,v[164:167],188
R 128,2,2688
M 48,v[168:171],184
L 15
ds_read2_b32 v[100:101],v14 offset0:160 offset1:161
M 52,v[152:155],204
ds_read2_b32 v[102:103],v14 offset0:162 offset1:163
M 52,v[156:159],200
R 132,2,2176
M 52,v[160:163],196
M 52,v[164:167],192
ds_read2_b32 v[104:105],v15 offset0:32 offset1:33
M 52,v[168:171],188
ds_read2_b32 v[106:107],v15 offset0:34 offset1:35
M 52,v[172:175],184
L 15
R 136,2,1664
M 56,v[152:155],208
ds_read2_b32 v[108:109],v15 offset0:160 offset1:161
M 56,v[156:159],204
M 56,v[160:163],200
ds_read2_b32 v[110:111],v15 offset0:162 offset1:163
M 56,v[164:167],196
R 140,2,1152
M 56,v[168:171],192
ds_read2_b32 v[112:113],v16 offset0:32 offset1:33
M 56,v[172:175],188
ds_read2_b32 v[114:115],v16 offset0:34 offset1:35
M 56,v[176:179],184
L 15
M 60,v[152:155],212
R 144,2,640
M 60,v[156:159],208
ds_read2_b32 v[116:117],v16 offset0:160 offset1:161
M 60,v[160:163],204
ds_read2_b32 v[118:119],v16 offset0:162 offset1:163
M 60,v[164:167],200
R 148,2,128
M 60,v[168:171],196
M 60,v[172:175],192
M 60,v[176:179],188
M 60,v[180:183],184
L 15
M 40,v[88:91],128
ds_read2_b32 v[152:153],v1 offset0:48 offset1:49
M 40,v[92:95],124
ds_read2_b32 v[154:155],v1 offset0:50 offset1:51
M 40,v[96:99],120
L 14
R 184,2,3776
M 44,v[88:91],132
ds_read2_b32 v[156:157],v1 offset0:176 offset1:177
M 44,v[92:95],128
M 44,v[96:99],124
ds_read2_b32 v[158:159],v1 offset0:178 offset1:179
M 44,v[100:103],120
L 14
R 188,2,3264
M 48,v[88:91],136
ds_read2_b32 v[160:161],v14 offset0:48 offset1:49
M 48,v[92:95],132
ds_read2_b32 v[162:163],v14 offset0:50 offset1:51
M 48,v[96:99],128
M 48,v[100:103],124
R 192,2,2752
M 48,v[104:107],120
L 15
ds_read2_b32 v[164:165],v14 offset0:176 offset1:177
M 52,v[88:91],140
s_mov_b32 m0,163584
s_mov_b32 s78,0
ds_read2_b32 v[166:167],v14 offset0:178 offset1:179
M 52,v[92:95],136
R 196,2,2240
M 52,v[96:99],132
buffer_load_dword v11,s[40:43],s78 offen offset:0  lds
M 52,v[100:103],128
ds_read2_b32 v[168:169],v15 offset0:48 offset1:49
M 52,v[104:107],124
ds_read2_b32 v[170:171],v15 offset0:50 offset1:51
M 52,v[108:111],120
L 15
R 200,2,1728
M 56,v[88:91],144
ds_read2_b32 v[172:173],v15 offset0:176 offset1:177
M 56,v[92:95],140
M 56,v[96:99],136
ds_read2_b32 v[174:175],v15 offset0:178 offset1:179
M 56,v[100:103],132
R 204,2,1216
M 56,v[104:107],128
ds_read2_b32 v[176:177],v16 offset0:48 offset1:49
M 56,v[108:111],124
ds_read2_b32 v[178:179],v16 offset0:50 offset1:51
M 56,v[112:115],120
L 15
M 60,v[88:91],148
R 208,2,704
M 60,v[92:95],144
ds_read2_b32 v[180:181],v16 offset0:176 offset1:177
M 60,v[96:99],140
ds_read2_b32 v[182:183],v16 offset0:178 offset1:179
M 60,v[100:103],136
R 212,2,192
M 60,v[104:107],132
M 60,v[108:111],128
M 60,v[112:115],124
M 60,v[116:119],120
L 15
M 40,v[152:155],192
ds_read2_b32 v[88:89],v1 offset0:64 offset1:65
M 40,v[156:159],188
ds_read2_b32 v[90:91],v1 offset0:66 offset1:67
M 40,v[160:163],184
L 14
R 120,2,3840
M 44,v[152:155],196
ds_read2_b32 v[92:93],v1 offset0:192 offset1:193
M 44,v[156:159],192
M 44,v[160:163],188
ds_read2_b32 v[94:95],v1 offset0:194 offset1:195
M 44,v[164:167],184
L 14
R 124,2,3328
M 48,v[152:155],200
ds_read2_b32 v[96:97],v14 offset0:64 offset1:65
M 48,v[156:159],196
ds_read2_b32 v[98:99],v14 offset0:66 offset1:67
M 48,v[160:163],192
M 48,v[164:167],188
R 128,2,2816
M 48,v[168:171],184
L 15
ds_read2_b32 v[100:101],v14 offset0:192 offset1:193
M 52,v[152:155],204
ds_read2_b32 v[102:103],v14 offset0:194 offset1:195
M 52,v[156:159],200
R 132,2,2304
M 52,v[160:163],196
M 52,v[164:167],192
ds_read2_b32 v[104:105],v15 offset0:64 offset1:65
M 52,v[168:171],188
ds_read2_b32 v[106:107],v15 offset0:66 offset1:67
M 52,v[172:175],184
L 15
R 136,2,1792
M 56,v[152:155],208
ds_read2_b32 v[108:109],v15 offset0:192 offset1:193
M 56,v[156:159],204
M 56,v[160:163],200
ds_read2_b32 v[110:111],v15 offset0:194 offset1:195
M 56,v[164:167],196
R 140,2,1280
M 56,v[168:171],192
ds_read2_b32 v[112:113],v16 offset0:64 offset1:65
M 56,v[172:175],188
ds_read2_b32 v[114:115],v16 offset0:66 offset1:67
M 56,v[176:179],184
L 15
M 60,v[152:155],212
R 144,2,768
M 60,v[156:159],208
ds_read2_b32 v[116:117],v16 offset0:192 offset1:193
M 60,v[160:163],204
ds_read2_b32 v[118:119],v16 offset0:194 offset1:195
M 60,v[164:167],200
R 148,2,256
M 60,v[168:171],196
M 60,v[172:175],192
M 60,v[176:179],188
M 60,v[180:183],184
L 15
M 40,v[88:91],128
ds_read2_b32 v[152:153],v1 offset0:80 offset1:81
M 40,v[92:95],124
ds_read2_b32 v[154:155],v1 offset0:82 offset1:83
M 40,v[96:99],120
L 14
R 184,2,3904
M 44,v[88:91],132
ds_read2_b32 v[156:157],v1 offset0:208 offset1:209
M 44,v[92:95],128
M 44,v[96:99],124
ds_read2_b32 v[158:159],v1 offset0:210 offset1:211
M 44,v[100:103],120
L 14
R 188,2,3392
M 48,v[88:91],136
ds_read2_b32 v[160:161],v14 offset0:80 offset1:81
M 48,v[92:95],132
ds_read2_b32 v[162:163],v14 offset0:82 offset1:83
M 48,v[96:99],128
M 48,v[100:103],124
R 192,2,2880
M 48,v[104:107],120
L 15
ds_read2_b32 v[164:165],v14 offset0:208 offset1:209
M 52,v[88:91],140
ds_read2_b32 v[166:167],v14 offset0:210 offset1:211
M 52,v[92:95],136
R 196,2,2368
M 52,v[96:99],132
M 52,v[100:103],128
ds_read2_b32 v[168:169],v15 offset0:80 offset1:81
M 52,v[104:107],124
ds_read2_b32 v[170:171],v15 offset0:82 offset1:83
M 52,v[108:111],120
L 15
R 200,2,1856
M 56,v[88:91],144
ds_read2_b32 v[172:173],v15 offset0:208 offset1:209
M 56,v[92:95],140
M 56,v[96:99],136
ds_read2_b32 v[174:175],v15 offset0:210 offset1:211
M 56,v[100:103],132
R 204,2,1344
M 56,v[104:107],128
ds_read2_b32 v[176:177],v16 offset0:80 offset1:81
M 56,v[108:111],124
ds_read2_b32 v[178:179],v16 offset0:82 offset1:83
M 56,v[112:115],120
L 15
M 60,v[88:91],148
R 208,2,832
M 60,v[92:95],144
ds_read2_b32 v[180:181],v16 offset0:208 offset1:209
M 60,v[96:99],140
ds_read2_b32 v[182:183],v16 offset0:210 offset1:211
M 60,v[100:103],136
R 212,2,320
M 60,v[104:107],132
M 60,v[108:111],128
M 60,v[112:115],124
M 60,v[116:119],120
L 15
M 40,v[152:155],192
ds_read2_b32 v[88:89],v1 offset0:96 offset1:97
M 40,v[156:159],188
ds_read2_b32 v[90:91],v1 offset0:98 offset1:99
M 40,v[160:163],184
L 14
R 120,2,3968
M 44,v[152:155],196
s_mov_b32 m0,163584
s_mov_b32 s78,3741696
ds_read2_b32 v[92:93],v1 offset0:224 offset1:225
M 44,v[156:159],192
M 44,v[160:163],188
ds_read2_b32 v[94:95],v1 offset0:226 offset1:227
M 44,v[164:167],184
L 14
R 124,2,3456
M 48,v[152:155],200
ds_read2_b32 v[96:97],v14 offset0:96 offset1:97
M 48,v[156:159],196
ds_read2_b32 v[98:99],v14 offset0:98 offset1:99
M 48,v[160:163],192
buffer_load_dword v11,s[40:43],s78 offen offset:0  lds
M 48,v[164:167],188
R 128,2,2944
M 48,v[168:171],184
L 15
ds_read2_b32 v[100:101],v14 offset0:224 offset1:225
M 52,v[152:155],204
ds_read2_b32 v[102:103],v14 offset0:226 offset1:227
M 52,v[156:159],200
R 132,2,2432
M 52,v[160:163],196
M 52,v[164:167],192
ds_read2_b32 v[104:105],v15 offset0:96 offset1:97
M 52,v[168:171],188
ds_read2_b32 v[106:107],v15 offset0:98 offset1:99
M 52,v[172:175],184
L 15
R 136,2,1920
M 56,v[152:155],208
ds_read2_b32 v[108:109],v15 offset0:224 offset1:225
M 56,v[156:159],204
M 56,v[160:163],200
ds_read2_b32 v[110:111],v15 offset0:226 offset1:227
M 56,v[164:167],196
R 140,2,1408
M 56,v[168:171],192
ds_read2_b32 v[112:113],v16 offset0:96 offset1:97
M 56,v[172:175],188
ds_read2_b32 v[114:115],v16 offset0:98 offset1:99
M 56,v[176:179],184
L 15
M 60,v[152:155],212
R 144,2,896
M 60,v[156:159],208
ds_read2_b32 v[116:117],v16 offset0:224 offset1:225
M 60,v[160:163],204
ds_read2_b32 v[118:119],v16 offset0:226 offset1:227
M 60,v[164:167],200
R 148,2,384
M 60,v[168:171],196
M 60,v[172:175],192
M 60,v[176:179],188
M 60,v[180:183],184
L 15
M 40,v[88:91],128
ds_read2_b32 v[152:153],v1 offset0:112 offset1:113
M 40,v[92:95],124
ds_read2_b32 v[154:155],v1 offset0:114 offset1:115
M 40,v[96:99],120
L 14
R 184,2,4032
M 44,v[88:91],132
ds_read2_b32 v[156:157],v1 offset0:240 offset1:241
M 44,v[92:95],128
M 44,v[96:99],124
ds_read2_b32 v[158:159],v1 offset0:242 offset1:243
v_add_u32 v1,12928,v1
M 44,v[100:103],120
L 14
R 188,2,3520
M 48,v[88:91],136
ds_read2_b32 v[160:161],v14 offset0:112 offset1:113
M 48,v[92:95],132
ds_read2_b32 v[162:163],v14 offset0:114 offset1:115
M 48,v[96:99],128
M 48,v[100:103],124
R 192,2,3008
M 48,v[104:107],120
L 15
ds_read2_b32 v[164:165],v14 offset0:240 offset1:241
M 52,v[88:91],140
ds_read2_b32 v[166:167],v14 offset0:242 offset1:243
v_add_u32 v14,12928,v14
M 52,v[92:95],136
R 196,2,2496
M 52,v[96:99],132
M 52,v[100:103],128
ds_read2_b32 v[168:169],v15 offset0:112 offset1:113
M 52,v[104:107],124
ds_read2_b32 v[170:171],v15 offset0:114 offset1:115
M 52,v[108:111],120
L 15
R 200,2,1984
M 56,v[88:91],144
ds_read2_b32 v[172:173],v15 offset0:240 offset1:241
M 56,v[92:95],140
M 56,v[96:99],136
ds_read2_b32 v[174:175],v15 offset0:242 offset1:243
v_add_u32 v15,12928,v15
M 56,v[100:103],132
R 204,2,1472
M 56,v[104:107],128
ds_read2_b32 v[176:177],v16 offset0:112 offset1:113
M 56,v[108:111],124
ds_read2_b32 v[178:179],v16 offset0:114 offset1:115
M 56,v[112:115],120
L 15
M 60,v[88:91],148
R 208,2,960
M 60,v[92:95],144
ds_read2_b32 v[180:181],v16 offset0:240 offset1:241
M 60,v[96:99],140
ds_read2_b32 v[182:183],v16 offset0:242 offset1:243
v_add_u32 v16,12928,v16
M 60,v[100:103],136
)"
R"(R 212,2,448
M 60,v[104:107],132
M 60,v[108:111],128
M 60,v[112:115],124
M 60,v[116:119],120
L 15
M 40,v[152:155],192
ds_read2_b32 v[88:89],v1 offset0:0 offset1:1
M 40,v[156:159],188
ds_read2_b32 v[90:91],v1 offset0:2 offset1:3
M 40,v[160:163],184
L 14
R 120,2,16512
M 44,v[152:155],196
ds_read2_b32 v[92:93],v1 offset0:128 offset1:129
M 44,v[156:159],192
M 44,v[160:163],188
ds_read2_b32 v[94:95],v1 offset0:130 offset1:131
M 44,v[164:167],184
L 14
R 124,2,16000
M 48,v[152:155],200
ds_read2_b32 v[96:97],v14 offset0:0 offset1:1
M 48,v[156:159],196
ds_read2_b32 v[98:99],v14 offset0:2 offset1:3
M 48,v[160:163],192
M 48,v[164:167],188
R 128,2,15488
M 48,v[168:171],184
L 15
ds_read2_b32 v[100:101],v14 offset0:128 offset1:129
M 52,v[152:155],204
ds_read2_b32 v[102:103],v14 offset0:130 offset1:131
M 52,v[156:159],200
R 132,2,14976
M 52,v[160:163],196
M 52,v[164:167],192
ds_read2_b32 v[104:105],v15 offset0:0 offset1:1
M 52,v[168:171],188
ds_read2_b32 v[106:107],v15 offset0:2 offset1:3
M 52,v[172:175],184
L 15
R 136,2,14464
M 56,v[152:155],208
ds_read2_b32 v[108:109],v15 offset0:128 offset1:129
M 56,v[156:159],204
M 56,v[160:163],200
ds_read2_b32 v[110:111],v15 offset0:130 offset1:131
M 56,v[164:167],196
R 140,2,13952
M 56,v[168:171],192
ds_read2_b32 v[112:113],v16 offset0:0 offset1:1
M 56,v[172:175],188
ds_read2_b32 v[114:115],v16 offset0:2 offset1:3
M 56,v[176:179],184
L 15
M 60,v[152:155],212
R 144,2,13440
M 60,v[156:159],208
ds_read2_b32 v[116:117],v16 offset0:128 offset1:129
M 60,v[160:163],204
ds_read2_b32 v[118:119],v16 offset0:130 offset1:131
M 60,v[164:167],200
R 148,2,12928
M 60,v[168:171],196
M 60,v[172:175],192
M 60,v[176:179],188
M 60,v[180:183],184
s_cmp_eq_u32 s51,2
s_cbranch_scc1 Lcv_d_%=
ds_read_b64 v[24:25],v3 offset:2560
ds_read_b64 v[26:27],v3 offset:2048
ds_read_b64 v[28:29],v3 offset:1536
ds_read_b64 v[30:31],v3 offset:1024
ds_read_b64 v[32:33],v3 offset:512
ds_read_b64 v[34:35],v3 offset:0
L 0
v_and_b32 v36,0xffff0000,v25
v_lshlrev_b32 v37,16,v25
v_and_b32 v38,0xffff0000,v24
v_lshlrev_b32 v39,16,v24
v_fma_f32 v40,v36,s86,v40
v_fma_f32 v41,v37,s86,v41
v_fma_f32 v42,v38,s86,v42
v_fma_f32 v43,v39,s86,v43
v_cvt_pk_bf16_f32 v40,v40,v41
v_cvt_pk_bf16_f32 v41,v42,v43
ds_write_b64 v4,v[40:41] offset:0
v_and_b32 v36,0xffff0000,v27
v_lshlrev_b32 v37,16,v27
v_and_b32 v38,0xffff0000,v26
v_lshlrev_b32 v39,16,v26
v_fma_f32 v44,v36,s86,v44
v_fma_f32 v45,v37,s86,v45
v_fma_f32 v46,v38,s86,v46
v_fma_f32 v47,v39,s86,v47
v_cvt_pk_bf16_f32 v44,v44,v45
v_cvt_pk_bf16_f32 v45,v46,v47
ds_write_b64 v4,v[44:45] offset:512
v_and_b32 v36,0xffff0000,v29
v_lshlrev_b32 v37,16,v29
v_and_b32 v38,0xffff0000,v28
v_lshlrev_b32 v39,16,v28
v_fma_f32 v48,v36,s86,v48
v_fma_f32 v49,v37,s86,v49
v_fma_f32 v50,v38,s86,v50
v_fma_f32 v51,v39,s86,v51
v_cvt_pk_bf16_f32 v48,v48,v49
v_cvt_pk_bf16_f32 v49,v50,v51
ds_write_b64 v4,v[48:49] offset:1024
v_and_b32 v36,0xffff0000,v31
v_lshlrev_b32 v37,16,v31
v_and_b32 v38,0xffff0000,v30
v_lshlrev_b32 v39,16,v30
v_fma_f32 v52,v36,s86,v52
v_fma_f32 v53,v37,s86,v53
v_fma_f32 v54,v38,s86,v54
v_fma_f32 v55,v39,s86,v55
v_cvt_pk_bf16_f32 v52,v52,v53
v_cvt_pk_bf16_f32 v53,v54,v55
ds_write_b64 v4,v[52:53] offset:1536
v_and_b32 v36,0xffff0000,v33
v_lshlrev_b32 v37,16,v33
v_and_b32 v38,0xffff0000,v32
v_lshlrev_b32 v39,16,v32
v_fma_f32 v56,v36,s86,v56
v_fma_f32 v57,v37,s86,v57
v_fma_f32 v58,v38,s86,v58
v_fma_f32 v59,v39,s86,v59
v_cvt_pk_bf16_f32 v56,v56,v57
v_cvt_pk_bf16_f32 v57,v58,v59
ds_write_b64 v4,v[56:57] offset:2048
v_and_b32 v36,0xffff0000,v35
v_lshlrev_b32 v37,16,v35
v_and_b32 v38,0xffff0000,v34
v_lshlrev_b32 v39,16,v34
v_fma_f32 v60,v36,s86,v60
v_fma_f32 v61,v37,s86,v61
v_fma_f32 v62,v38,s86,v62
v_fma_f32 v63,v39,s86,v63
v_cvt_pk_bf16_f32 v60,v60,v61
v_cvt_pk_bf16_f32 v61,v62,v63
ds_write_b64 v4,v[60:61] offset:2560
s_branch Lcv_e_%=
Lcv_d_%=:
L 15
v_mfma_f32_16x16x32_bf16 v[64:67],v[88:91],v[128:131],0
ds_read2_b32 v[152:153],v1 offset0:16 offset1:17
ds_read_b64 v[24:25],v3 offset:2560
M 64,v[92:95],124
ds_read2_b32 v[154:155],v1 offset0:18 offset1:19
ds_read_b64 v[26:27],v3 offset:2048
M 64,v[96:99],120
L 15
R 184,2,16576
ds_read_b64 v[28:29],v3 offset:1536
v_mfma_f32_16x16x32_bf16 v[68:71],v[88:91],v[132:135],0
ds_read2_b32 v[156:157],v1 offset0:144 offset1:145
ds_read_b64 v[30:31],v3 offset:1024
M 68,v[92:95],128
ds_read_b64 v[32:33],v3 offset:512
ds_read_b64 v[34:35],v3 offset:0
M 68,v[96:99],124
ds_read2_b32 v[158:159],v1 offset0:146 offset1:147
M 68,v[100:103],120
L 15
R 188,2,16064
v_mfma_f32_16x16x32_bf16 v[72:75],v[88:91],v[136:139],0
ds_read2_b32 v[160:161],v14 offset0:16 offset1:17
M 72,v[92:95],132
s_mov_b32 m0,163584
s_mov_b32 s78,0
ds_read2_b32 v[162:163],v14 offset0:18 offset1:19
M 72,v[96:99],128
M 72,v[100:103],124
L 4
R 192,2,15552
v_and_b32 v36,0xffff0000,v25
M 72,v[104:107],120
s_nop 0
ds_read2_b32 v[164:165],v14 offset0:144 offset1:145
v_lshlrev_b32 v37,16,v25
v_mfma_f32_16x16x32_bf16 v[76:79],v[88:91],v[140:143],0
ds_read2_b32 v[166:167],v14 offset0:146 offset1:147
v_and_b32 v38,0xffff0000,v24
M 76,v[92:95],136
R 196,2,15040
v_lshlrev_b32 v39,16,v24
M 76,v[96:99],132
buffer_load_dword v12,s[60:63],s78 offen offset:0  lds
M 76,v[100:103],128
ds_read2_b32 v[168:169],v15 offset0:16 offset1:17
v_fma_f32 v40,v36,s86,v40
M 76,v[104:107],124
ds_read2_b32 v[170:171],v15 offset0:18 offset1:19
v_fma_f32 v41,v37,s86,v41
M 76,v[108:111],120
s_nop 0
R 200,2,14528
v_fma_f32 v42,v38,s86,v42
v_mfma_f32_16x16x32_bf16 v[80:83],v[88:91],v[144:147],0
ds_read2_b32 v[172:173],v15 offset0:144 offset1:145
v_fma_f32 v43,v39,s86,v43
M 80,v[92:95],140
v_cvt_pk_bf16_f32 v40,v40,v41
v_cvt_pk_bf16_f32 v41,v42,v43
M 80,v[96:99],136
ds_read2_b32 v[174:175],v15 offset0:146 offset1:147
v_and_b32 v36,0xffff0000,v27
M 80,v[100:103],132
R 204,2,14016
v_lshlrev_b32 v37,16,v27
M 80,v[104:107],128
ds_read2_b32 v[176:177],v16 offset0:16 offset1:17
v_and_b32 v38,0xffff0000,v26
M 80,v[108:111],124
ds_read2_b32 v[178:179],v16 offset0:18 offset1:19
v_lshlrev_b32 v39,16,v26
M 80,v[112:115],120
s_nop 0
ds_write_b64 v4,v[40:41] offset:0
v_mfma_f32_16x16x32_bf16 v[84:87],v[88:91],v[148:151],0
R 208,2,13504
v_fma_f32 v44,v36,s86,v44
M 84,v[92:95],144
ds_read2_b32 v[180:181],v16 offset0:144 offset1:145
v_fma_f32 v45,v37,s86,v45
M 84,v[96:99],140
ds_read2_b32 v[182:183],v16 offset0:146 offset1:147
v_fma_f32 v46,v38,s86,v46
M 84,v[100:103],136
R 212,2,12992
v_fma_f32 v47,v39,s86,v47
M 84,v[104:107],132
v_cvt_pk_bf16_f32 v44,v44,v45
v_cvt_pk_bf16_f32 v45,v46,v47
M 84,v[108:111],128
ds_write_b64 v4,v[44:45] offset:512
M 84,v[112:115],124
v_and_b32 v36,0xffff0000,v29
v_lshlrev_b32 v37,16,v29
M 84,v[116:119],120
L 15
v_and_b32 v38,0xffff0000,v28
v_lshlrev_b32 v39,16,v28
M 64,v[152:155],192
ds_read2_b32 v[88:89],v1 offset0:32 offset1:33
v_fma_f32 v48,v36,s86,v48
M 64,v[156:159],188
ds_read2_b32 v[90:91],v1 offset0:34 offset1:35
v_fma_f32 v49,v37,s86,v49
M 64,v[160:163],184
L 15
R 120,2,16640
v_fma_f32 v50,v38,s86,v50
M 68,v[152:155],196
ds_read2_b32 v[92:93],v1 offset0:160 offset1:161
v_fma_f32 v51,v39,s86,v51
M 68,v[156:159],192
v_cvt_pk_bf16_f32 v48,v48,v49
v_cvt_pk_bf16_f32 v49,v50,v51
M 68,v[160:163],188
ds_read2_b32 v[94:95],v1 offset0:162 offset1:163
v_and_b32 v36,0xffff0000,v31
M 68,v[164:167],184
L 15
R 124,2,16128
v_lshlrev_b32 v37,16,v31
M 72,v[152:155],200
ds_read2_b32 v[96:97],v14 offset0:32 offset1:33
v_and_b32 v38,0xffff0000,v30
M 72,v[156:159],196
ds_read2_b32 v[98:99],v14 offset0:34 offset1:35
v_lshlrev_b32 v39,16,v30
M 72,v[160:163],192
ds_write_b64 v4,v[48:49] offset:1024
M 72,v[164:167],188
R 128,2,15616
v_fma_f32 v52,v36,s86,v52
M 72,v[168:171],184
L 15
ds_read2_b32 v[100:101],v14 offset0:160 offset1:161
v_fma_f32 v53,v37,s86,v53
M 76,v[152:155],204
ds_read2_b32 v[102:103],v14 offset0:162 offset1:163
v_fma_f32 v54,v38,s86,v54
M 76,v[156:159],200
R 132,2,15104
v_fma_f32 v55,v39,s86,v55
M 76,v[160:163],196
v_cvt_pk_bf16_f32 v52,v52,v53
v_cvt_pk_bf16_f32 v53,v54,v55
M 76,v[164:167],192
ds_read2_b32 v[104:105],v15 offset0:32 offset1:33
v_and_b32 v36,0xffff0000,v33
M 76,v[168:171],188
ds_read2_b32 v[106:107],v15 offset0:34 offset1:35
v_lshlrev_b32 v37,16,v33
M 76,v[172:175],184
L 15
R 136,2,14592
v_and_b32 v38,0xffff0000,v32
M 80,v[152:155],208
ds_read2_b32 v[108:109],v15 offset0:160 offset1:161
v_lshlrev_b32 v39,16,v32
M 80,v[156:159],204
ds_write_b64 v4,v[52:53] offset:1536
M 80,v[160:163],200
ds_read2_b32 v[110:111],v15 offset0:162 offset1:163
v_fma_f32 v56,v36,s86,v56
M 80,v[164:167],196
R 140,2,14080
v_fma_f32 v57,v37,s86,v57
M 80,v[168:171],192
ds_read2_b32 v[112:113],v16 offset0:32 offset1:33
v_fma_f32 v58,v38,s86,v58
M 80,v[172:175],188
ds_read2_b32 v[114:115],v16 offset0:34 offset1:35
v_fma_f32 v59,v39,s86,v59
M 80,v[176:179],184
L 15
v_cvt_pk_bf16_f32 v56,v56,v57
v_cvt_pk_bf16_f32 v57,v58,v59
M 84,v[152:155],212
R 144,2,13568
v_and_b32 v36,0xffff0000,v35
M 84,v[156:159],208
ds_read2_b32 v[116:117],v16 offset0:160 offset1:161
v_lshlrev_b32 v37,16,v35
M 84,v[160:163],204
ds_read2_b32 v[118:119],v16 offset0:162 offset1:163
v_and_b32 v38,0xffff0000,v34
M 84,v[164:167],200
R 148,2,13056
v_lshlrev_b32 v39,16,v34
M 84,v[168:171],196
ds_write_b64 v4,v[56:57] offset:2048
M 84,v[172:175],192
v_fma_f32 v60,v36,s86,v60
v_fma_f32 v61,v37,s86,v61
M 84,v[176:179],188
v_fma_f32 v62,v38,s86,v62
v_fma_f32 v63,v39,s86,v63
M 84,v[180:183],184
L 15
v_cvt_pk_bf16_f32 v60,v60,v61
v_cvt_pk_bf16_f32 v61,v62,v63
M 64,v[88:91],128
ds_read2_b32 v[152:153],v1 offset0:48 offset1:49
M 64,v[92:95],124
ds_read2_b32 v[154:155],v1 offset0:50 offset1:51
M 64,v[96:99],120
L 15
R 184,2,16704
M 68,v[88:91],132
ds_read2_b32 v[156:157],v1 offset0:176 offset1:177
M 68,v[92:95],128
ds_write_b64 v4,v[60:61] offset:2560
M 68,v[96:99],124
ds_read2_b32 v[158:159],v1 offset0:178 offset1:179
M 68,v[100:103],120
L 15
R 188,2,16192
M 72,v[88:91],136
ds_read2_b32 v[160:161],v14 offset0:48 offset1:49
M 72,v[92:95],132
ds_read2_b32 v[162:163],v14 offset0:50 offset1:51
M 72,v[96:99],128
M 72,v[100:103],124
R 192,2,15680
M 72,v[104:107],120
L 15
ds_read2_b32 v[164:165],v14 offset0:176 offset1:177
M 76,v[88:91],140
ds_read2_b32 v[166:167],v14 offset0:178 offset1:179
M 76,v[92:95],136
R 196,2,15168
M 76,v[96:99],132
M 76,v[100:103],128
ds_read2_b32 v[168:169],v15 offset0:48 offset1:49
M 76,v[104:107],124
ds_read2_b32 v[170:171],v15 offset0:50 offset1:51
M 76,v[108:111],120
L 15
R 200,2,14656
M 80,v[88:91],144
ds_read2_b32 v[172:173],v15 offset0:176 offset1:177
M 80,v[92:95],140
M 80,v[96:99],136
ds_read2_b32 v[174:175],v15 offset0:178 offset1:179
M 80,v[100:103],132
R 204,2,14144
M 80,v[104:107],128
s_mov_b32 m0,163584
s_mov_b32 s78,1847296
ds_read2_b32 v[176:177],v16 offset0:48 offset1:49
M 80,v[108:111],124
ds_read2_b32 v[178:179],v16 offset0:50 offset1:51
M 80,v[112:115],120
L 15
buffer_load_dword v12,s[60:63],s78 offen offset:0  lds
M 84,v[88:91],148
R 208,2,13632
M 84,v[92:95],144
ds_read2_b32 v[180:181],v16 offset0:176 offset1:177
M 84,v[96:99],140
ds_read2_b32 v[182:183],v16 offset0:178 offset1:179
M 84,v[100:103],136
R 212,2,13120
M 84,v[104:107],132
M 84,v[108:111],128
M 84,v[112:115],124
M 84,v[116:119],120
L 15
M 64,v[152:155],192
ds_read2_b32 v[88:89],v1 offset0:64 offset1:65
M 64,v[156:159],188
ds_read2_b32 v[90:91],v1 offset0:66 offset1:67
M 64,v[160:163],184
L 14
R 120,2,16768
M 68,v[152:155],196
ds_read2_b32 v[92:93],v1 offset0:192 offset1:193
M 68,v[156:159],192
M 68,v[160:163],188
ds_read2_b32 v[94:95],v1 offset0:194 offset1:195
M 68,v[164:167],184
L 14
R 124,2,16256
M 72,v[152:155],200
ds_read2_b32 v[96:97],v14 offset0:64 offset1:65
M 72,v[156:159],196
ds_read2_b32 v[98:99],v14 offset0:66 offset1:67
M 72,v[160:163],192
M 72,v[164:167],188
R 128,2,15744
M 72,v[168:171],184
L 15
ds_read2_b32 v[100:101],v14 offset0:192 offset1:193
M 76,v[152:155],204
ds_read2_b32 v[102:103],v14 offset0:194 offset1:195
M 76,v[156:159],200
R 132,2,15232
M 76,v[160:163],196
M 76,v[164:167],192
ds_read2_b32 v[104:105],v15 offset0:64 offset1:65
M 76,v[168:171],188
ds_read2_b32 v[106:107],v15 offset0:66 offset1:67
M 76,v[172:175],184
L 15
R 136,2,14720
M 80,v[152:155],208
ds_read2_b32 v[108:109],v15 offset0:192 offset1:193
M 80,v[156:159],204
M 80,v[160:163],200
ds_read2_b32 v[110:111],v15 offset0:194 offset1:195
M 80,v[164:167],196
R 140,2,14208
M 80,v[168:171],192
ds_read2_b32 v[112:113],v16 offset0:64 offset1:65
M 80,v[172:175],188
ds_read2_b32 v[114:115],v16 offset0:66 offset1:67
M 80,v[176:179],184
L 15
M 84,v[152:155],212
R 144,2,13696
M 84,v[156:159],208
ds_read2_b32 v[116:117],v16 offset0:192 offset1:193
M 84,v[160:163],204
ds_read2_b32 v[118:119],v16 offset0:194 offset1:195
M 84,v[164:167],200
R 148,2,13184
M 84,v[168:171],196
M 84,v[172:175],192
M 84,v[176:179],188
M 84,v[180:183],184
L 15
M 64,v[88:91],128
ds_read2_b32 v[152:153],v1 offset0:80 offset1:81
M 64,v[92:95],124
ds_read2_b32 v[154:155],v1 offset0:82 offset1:83
M 64,v[96:99],120
L 14
R 184,2,16832
M 68,v[88:91],132
ds_read2_b32 v[156:157],v1 offset0:208 offset1:209
M 68,v[92:95],128
M 68,v[96:99],124
ds_read2_b32 v[158:159],v1 offset0:210 offset1:211
M 68,v[100:103],120
L 14
R 188,2,16320
M 72,v[88:91],136
ds_read2_b32 v[160:161],v14 offset0:80 offset1:81
M 72,v[92:95],132
ds_read2_b32 v[162:163],v14 offset0:82 offset1:83
M 72,v[96:99],128
M 72,v[100:103],124
R 192,2,15808
M 72,v[104:107],120
)"
R"(L 15
ds_read2_b32 v[164:165],v14 offset0:208 offset1:209
M 76,v[88:91],140
ds_read2_b32 v[166:167],v14 offset0:210 offset1:211
M 76,v[92:95],136
R 196,2,15296
M 76,v[96:99],132
M 76,v[100:103],128
ds_read2_b32 v[168:169],v15 offset0:80 offset1:81
M 76,v[104:107],124
ds_read2_b32 v[170:171],v15 offset0:82 offset1:83
M 76,v[108:111],120
L 15
R 200,2,14784
M 80,v[88:91],144
ds_read2_b32 v[172:173],v15 offset0:208 offset1:209
M 80,v[92:95],140
M 80,v[96:99],136
ds_read2_b32 v[174:175],v15 offset0:210 offset1:211
M 80,v[100:103],132
R 204,2,14272
M 80,v[104:107],128
ds_read2_b32 v[176:177],v16 offset0:80 offset1:81
M 80,v[108:111],124
ds_read2_b32 v[178:179],v16 offset0:82 offset1:83
M 80,v[112:115],120
L 15
M 84,v[88:91],148
R 208,2,13760
M 84,v[92:95],144
ds_read2_b32 v[180:181],v16 offset0:208 offset1:209
M 84,v[96:99],140
ds_read2_b32 v[182:183],v16 offset0:210 offset1:211
M 84,v[100:103],136
R 212,2,13248
M 84,v[104:107],132
M 84,v[108:111],128
M 84,v[112:115],124
M 84,v[116:119],120
L 15
M 64,v[152:155],192
ds_read2_b32 v[88:89],v1 offset0:96 offset1:97
M 64,v[156:159],188
ds_read2_b32 v[90:91],v1 offset0:98 offset1:99
M 64,v[160:163],184
L 14
R 120,2,16896
M 68,v[152:155],196
ds_read2_b32 v[92:93],v1 offset0:224 offset1:225
M 68,v[156:159],192
M 68,v[160:163],188
ds_read2_b32 v[94:95],v1 offset0:226 offset1:227
M 68,v[164:167],184
L 14
R 124,2,16384
M 72,v[152:155],200
ds_read2_b32 v[96:97],v14 offset0:96 offset1:97
M 72,v[156:159],196
ds_read2_b32 v[98:99],v14 offset0:98 offset1:99
M 72,v[160:163],192
M 72,v[164:167],188
R 128,2,15872
M 72,v[168:171],184
L 15
ds_read2_b32 v[100:101],v14 offset0:224 offset1:225
M 76,v[152:155],204
ds_read2_b32 v[102:103],v14 offset0:226 offset1:227
M 76,v[156:159],200
R 132,2,15360
M 76,v[160:163],196
M 76,v[164:167],192
ds_read2_b32 v[104:105],v15 offset0:96 offset1:97
M 76,v[168:171],188
ds_read2_b32 v[106:107],v15 offset0:98 offset1:99
M 76,v[172:175],184
L 15
R 136,2,14848
M 80,v[152:155],208
ds_read2_b32 v[108:109],v15 offset0:224 offset1:225
M 80,v[156:159],204
M 80,v[160:163],200
ds_read2_b32 v[110:111],v15 offset0:226 offset1:227
M 80,v[164:167],196
R 140,2,14336
M 80,v[168:171],192
ds_read2_b32 v[112:113],v16 offset0:96 offset1:97
M 80,v[172:175],188
ds_read2_b32 v[114:115],v16 offset0:98 offset1:99
M 80,v[176:179],184
L 15
M 84,v[152:155],212
R 144,2,13824
M 84,v[156:159],208
ds_read2_b32 v[116:117],v16 offset0:224 offset1:225
M 84,v[160:163],204
ds_read2_b32 v[118:119],v16 offset0:226 offset1:227
M 84,v[164:167],200
R 148,2,13312
M 84,v[168:171],196
M 84,v[172:175],192
M 84,v[176:179],188
M 84,v[180:183],184
L 15
M 64,v[88:91],128
ds_read2_b32 v[152:153],v1 offset0:112 offset1:113
M 64,v[92:95],124
ds_read2_b32 v[154:155],v1 offset0:114 offset1:115
M 64,v[96:99],120
L 14
R 184,2,16960
M 68,v[88:91],132
ds_read2_b32 v[156:157],v1 offset0:240 offset1:241
M 68,v[92:95],128
M 68,v[96:99],124
ds_read2_b32 v[158:159],v1 offset0:242 offset1:243
M 68,v[100:103],120
L 14
R 188,2,16448
M 72,v[88:91],136
ds_read2_b32 v[160:161],v14 offset0:112 offset1:113
M 72,v[92:95],132
ds_read2_b32 v[162:163],v14 offset0:114 offset1:115
M 72,v[96:99],128
M 72,v[100:103],124
R 192,2,15936
M 72,v[104:107],120
L 15
ds_read2_b32 v[164:165],v14 offset0:240 offset1:241
M 76,v[88:91],140
ds_read2_b32 v[166:167],v14 offset0:242 offset1:243
M 76,v[92:95],136
R 196,2,15424
M 76,v[96:99],132
M 76,v[100:103],128
ds_read2_b32 v[168:169],v15 offset0:112 offset1:113
M 76,v[104:107],124
ds_read2_b32 v[170:171],v15 offset0:114 offset1:115
M 76,v[108:111],120
L 15
R 200,2,14912
M 80,v[88:91],144
ds_read2_b32 v[172:173],v15 offset0:240 offset1:241
M 80,v[92:95],140
M 80,v[96:99],136
ds_read2_b32 v[174:175],v15 offset0:242 offset1:243
M 80,v[100:103],132
R 204,2,14400
M 80,v[104:107],128
ds_read2_b32 v[176:177],v16 offset0:112 offset1:113
M 80,v[108:111],124
ds_read2_b32 v[178:179],v16 offset0:114 offset1:115
M 80,v[112:115],120
L 15
M 84,v[88:91],148
R 208,2,13888
M 84,v[92:95],144
ds_read2_b32 v[180:181],v16 offset0:240 offset1:241
M 84,v[96:99],140
ds_read2_b32 v[182:183],v16 offset0:242 offset1:243
M 84,v[100:103],136
R 212,2,13376
M 84,v[104:107],132
M 84,v[108:111],128
M 84,v[112:115],124
M 84,v[116:119],120
L 15
M 64,v[152:155],192
M 64,v[156:159],188
M 64,v[160:163],184
L 12
M 68,v[152:155],196
M 68,v[156:159],192
M 68,v[160:163],188
M 68,v[164:167],184
L 9
M 72,v[152:155],200
M 72,v[156:159],196
M 72,v[160:163],192
ds_read_b64 v[24:25],v3 offset:15488
ds_read_b64 v[26:27],v3 offset:14976
M 72,v[164:167],188
ds_read_b64 v[28:29],v3 offset:14464
ds_read_b64 v[30:31],v3 offset:13952
M 72,v[168:171],184
L 10
ds_read_b64 v[32:33],v3 offset:13440
ds_read_b64 v[34:35],v3 offset:12928
M 76,v[152:155],204
M 76,v[156:159],200
M 76,v[160:163],196
M 76,v[164:167],192
M 76,v[168:171],188
M 76,v[172:175],184
L 9
M 80,v[152:155],208
M 80,v[156:159],204
M 80,v[160:163],200
M 80,v[164:167],196
M 80,v[168:171],192
M 80,v[172:175],188
M 80,v[176:179],184
L 6
M 84,v[152:155],212
M 84,v[156:159],208
M 84,v[160:163],204
M 84,v[164:167],200
M 84,v[168:171],196
M 84,v[172:175],192
M 84,v[176:179],188
M 84,v[180:183],184
L 0
v_and_b32 v36,0xffff0000,v25
v_lshlrev_b32 v37,16,v25
v_and_b32 v38,0xffff0000,v24
v_lshlrev_b32 v39,16,v24
v_fma_f32 v64,v36,s87,v64
v_fma_f32 v65,v37,s87,v65
v_fma_f32 v66,v38,s87,v66
v_fma_f32 v67,v39,s87,v67
v_cvt_pk_bf16_f32 v64,v64,v65
v_cvt_pk_bf16_f32 v65,v66,v67
ds_write_b64 v4,v[64:65] offset:12928
v_and_b32 v36,0xffff0000,v27
v_lshlrev_b32 v37,16,v27
v_and_b32 v38,0xffff0000,v26
v_lshlrev_b32 v39,16,v26
v_fma_f32 v68,v36,s87,v68
v_fma_f32 v69,v37,s87,v69
v_fma_f32 v70,v38,s87,v70
v_fma_f32 v71,v39,s87,v71
v_cvt_pk_bf16_f32 v68,v68,v69
v_cvt_pk_bf16_f32 v69,v70,v71
ds_write_b64 v4,v[68:69] offset:13440
v_and_b32 v36,0xffff0000,v29
v_lshlrev_b32 v37,16,v29
v_and_b32 v38,0xffff0000,v28
v_lshlrev_b32 v39,16,v28
v_fma_f32 v72,v36,s87,v72
v_fma_f32 v73,v37,s87,v73
v_fma_f32 v74,v38,s87,v74
v_fma_f32 v75,v39,s87,v75
v_cvt_pk_bf16_f32 v72,v72,v73
v_cvt_pk_bf16_f32 v73,v74,v75
ds_write_b64 v4,v[72:73] offset:13952
v_and_b32 v36,0xffff0000,v31
v_lshlrev_b32 v37,16,v31
v_and_b32 v38,0xffff0000,v30
v_lshlrev_b32 v39,16,v30
v_fma_f32 v76,v36,s87,v76
v_fma_f32 v77,v37,s87,v77
v_fma_f32 v78,v38,s87,v78
v_fma_f32 v79,v39,s87,v79
v_cvt_pk_bf16_f32 v76,v76,v77
v_cvt_pk_bf16_f32 v77,v78,v79
ds_write_b64 v4,v[76:77] offset:14464
v_and_b32 v36,0xffff0000,v33
v_lshlrev_b32 v37,16,v33
v_and_b32 v38,0xffff0000,v32
v_lshlrev_b32 v39,16,v32
v_fma_f32 v80,v36,s87,v80
v_fma_f32 v81,v37,s87,v81
v_fma_f32 v82,v38,s87,v82
v_fma_f32 v83,v39,s87,v83
v_cvt_pk_bf16_f32 v80,v80,v81
v_cvt_pk_bf16_f32 v81,v82,v83
ds_write_b64 v4,v[80:81] offset:14976
v_and_b32 v36,0xffff0000,v35
v_lshlrev_b32 v37,16,v35
v_and_b32 v38,0xffff0000,v34
v_lshlrev_b32 v39,16,v34
v_fma_f32 v84,v36,s87,v84
v_fma_f32 v85,v37,s87,v85
v_fma_f32 v86,v38,s87,v86
v_fma_f32 v87,v39,s87,v87
v_cvt_pk_bf16_f32 v84,v84,v85
v_cvt_pk_bf16_f32 v85,v86,v87
ds_write_b64 v4,v[84:85] offset:15488
Lcv_e_%=:
L 0
s_barrier
R 152,5,0
R 156,5,512
R 160,5,1024
R 164,5,1536
R 168,5,2048
R 172,5,2560
s_mov_b32 s78,99614720
L 5
buffer_store_dwordx4 v[152:155],v7,s[36:39],s78 offen offset:0 sc0 sc1
s_add_u32 s78,s56,100139008
L 4
buffer_store_dwordx4 v[156:159],v7,s[36:39],s78 offen offset:0 sc0 sc1
s_mov_b32 s78,100663296
L 3
buffer_store_dwordx4 v[160:163],v7,s[36:39],s78 offen offset:0 sc0 sc1
s_add_u32 s78,s56,101187584
L 2
buffer_store_dwordx4 v[164:167],v7,s[36:39],s78 offen offset:0 sc0 sc1
s_mov_b32 s78,101711872
L 1
buffer_store_dwordx4 v[168:171],v7,s[36:39],s78 offen offset:0 sc0 sc1
s_add_u32 s78,s56,102236160
L 0
buffer_store_dwordx4 v[172:175],v7,s[36:39],s78 offen offset:0 sc0 sc1
s_waitcnt vmcnt(0)
s_barrier
s_add_u32 s48,s48,32768
buffer_store_dword v23,v10,s[36:39],s48 offen offset:0 sc0 sc1
s_cmp_eq_u32 s51,2
s_cbranch_scc0 Lcv_f_%=
s_mov_b32 m0,163584
s_mov_b32 s78,3694592
buffer_load_dword v12,s[60:63],s78 offen offset:0  lds
s_mov_b32 m0,163584
s_mov_b32 s78,5541888
buffer_load_dword v12,s[60:63],s78 offen offset:0  lds
Lcv_f_%=:
Lcode_cv_e:
.purgem M
.purgem R
.purgem T
.purgem D
.purgem L
)"
  :: "s"(ws), "s"(__builtin_amdgcn_readfirstlane(sWave)), "s"(__builtin_amdgcn_readfirstlane(sBuf)), "s"(__builtin_amdgcn_readfirstlane(sAuxOff)), "s"(__builtin_amdgcn_readfirstlane(sFoutOff)), "s"(__builtin_amdgcn_readfirstlane(sC0)), "s"(__builtin_amdgcn_readfirstlane(sC1)), "s"(__builtin_amdgcn_readfirstlane(sExp)), "v"(vL16), "v"(vPA), "v"(vPB), "v"(vPU), "v"(vPW), "v"(vOR), "v"(vORA), "v"(vOG), "v"(vFA), "v"(vFB), "v"(vFlag), "v"(vTW), "v"(vTX), "s"(w3), "s"(w4) : "v0", "v1", "v2", "v3", "v4", "v5", "v6", "v7", "v8", "v9", "v10", "v11", "v12", "v13", "v14", "v15", "v16", "v17", "v18", "v19", "v20", "v21", "v22", "v23", "v24", "v25", "v26", "v27", "v28", "v29", "v30", "v31", "v32", "v33", "v34", "v35", "v36", "v37", "v38", "v39", "v40", "v41", "v42", "v43", "v44", "v45", "v46", "v47", "v48", "v49", "v50", "v51", "v52", "v53", "v54", "v55", "v56", "v57", "v58", "v59", "v60", "v61", "v62", "v63", "v64", "v65", "v66", "v67", "v68", "v69", "v70", "v71", "v72", "v73", "v74", "v75", "v76", "v77", "v78", "v79", "v80", "v81", "v82", "v83", "v84", "v85", "v86", "v87", "v88", "v89", "v90", "v91", "v92", "v93", "v94", "v95", "v96", "v97", "v98", "v99", "v100", "v101", "v102", "v103", "v104", "v105", "v106", "v107", "v108", "v109", "v110", "v111", "v112", "v113", "v114", "v115", "v116", "v117", "v118", "v119", "v120", "v121", "v122", "v123", "v124", "v125", "v126", "v127", "v128", "v129", "v130", "v131", "v132", "v133", "v134", "v135", "v136", "v137", "v138", "v139", "v140", "v141", "v142", "v143", "v144", "v145", "v146", "v147", "v148", "v149", "v150", "v151", "v152", "v153", "v154", "v155", "v156", "v157", "v158", "v159", "v160", "v161", "v162", "v163", "v164", "v165", "v166", "v167", "v168", "v169", "v170", "v171", "v172", "v173", "v174", "v175", "v176", "v177", "v178", "v179", "v180", "v181", "v182", "v183", "v184", "v185", "v186", "v187", "v188", "v189", "v190", "v191", "v192", "v193", "v194", "v195", "v196", "v197", "v198", "v199", "v200", "v201", "v202", "v203", "v204", "v205", "v206", "v207", "v208", "v209", "v210", "v211", "v212", "v213", "v214", "v215", "v216", "v217", "v218", "v219", "v220", "v221", "v222", "v223", "v224", "v225", "v226", "v227", "v228", "v229", "v230", "v231", "a0", "a1", "a2", "a3", "a4", "a5", "a6", "a7", "a8", "a9", "a10", "a11", "a12", "a13", "a14", "a15", "a16", "a17", "a18", "a19", "a20", "a21", "a22", "a23", "a24", "a25", "a26", "a27", "a28", "a29", "a30", "a31", "a32", "a33", "a34", "a35", "a36", "a37", "a38", "a39", "a40", "a41", "a42", "a43", "a44", "a45", "a46", "a47", "a48", "a49", "a50", "a51", "a52", "a53", "a54", "a55", "a56", "a57", "a58", "a59", "a60", "a61", "a62", "a63", "a64", "a65", "a66", "a67", "a68", "a69", "a70", "a71", "a72", "a73", "a74", "a75", "a76", "a77", "a78", "a79", "a80", "a81", "a82", "a83", "a84", "a85", "a86", "a87", "a88", "a89", "a90", "a91", "a92", "a93", "a94", "a95", "a96", "a97", "a98", "a99", "a100", "a101", "a102", "a103", "a104", "a105", "a106", "a107", "a108", "a109", "a110", "a111", "a112", "a113", "a114", "a115", "a116", "a117", "a118", "a119", "a120", "a121", "a122", "a123", "a124", "a125", "a126", "a127", "a128", "a129", "a130", "a131", "a132", "a133", "a134", "a135", "a136", "a137", "a138", "a139", "a140", "a141", "a142", "a143", "a144", "a145", "a146", "a147", "a148", "a149", "a150", "a151", "a152", "a153", "a154", "a155", "a156", "a157", "a158", "a159", "a160", "a161", "a162", "a163", "a164", "a165", "a166", "a167", "a168", "a169", "a170", "a171", "a172", "a173", "a174", "a175", "a176", "a177", "a178", "a179", "a180", "a181", "a182", "a183", "a184", "a185", "a186", "a187", "a188", "a189", "a190", "a191", "a192", "a193", "a194", "a195", "a196", "a197", "a198", "a199", "a200", "a201", "a202", "a203", "a204", "a205", "a206", "a207", "a208", "a209", "a210", "a211", "a212", "a213", "a214", "a215", "a216", "a217", "a218", "a219", "a220", "a221", "a222", "a223", "a224", "a225", "a226", "a227", "a228", "a229", "a230", "a231", "a232", "a233", "a234", "a235", "a236", "a237", "a238", "a239", "a240", "a241", "a242", "a243", "a244", "a245", "a246", "a247", "a248", "a249", "a250", "a251", "a252", "a253", "a254", "a255", "s36", "s37", "s38", "s39", "s40", "s41", "s42", "s43", "s44", "s45", "s46", "s47", "s48", "s49", "s50", "s51", "s52", "s53", "s54", "s55", "s56", "s57", "s58", "s59", "s60", "s61", "s62", "s63", "s64", "s65", "s66", "s67", "s68", "s69", "s70", "s71", "s72", "s73", "s74", "s75", "s76", "s77", "s78", "s79", "s80", "s81", "s82", "s83", "s84", "s85", "s86", "s87", "s88", "s89", "s90", "s91", "s92", "s93", "s94", "s95", "s96", "s97", "vcc", "scc", "memory");
    LANE_ID(lq_);
    const int g = QT0 + (NCV - 1 - iw) * 164 + min(wave * 64 + lq_, 163);
    if (QTB) touch_lds((const char*)q + (size_t)(g < 32768 ? g : 0) * 128, sScr);
  }
  WAIT_LGKM0();
  __builtin_amdgcn_s_barrier();
  CTS(10);
  }
  const unsigned sDbg0 = 110u * 1048576u + (b * 4 + wave) * 8192;
  constexpr unsigned OOB = 0x80000000u;
  if (kind == 6) {
    LANE_ID(lr);
    const int r8 = lr & 7, seg = lr >> 3, j = lr & 15, g = lr >> 4, rr = 8 * g + (j >> 2);
    const unsigned sWave = wave, sExp = __builtin_amdgcn_readfirstlane(ep), sG0 = slice, sStep = NRW, sDbg = sDbg0;
    const unsigned sWw = ((slice >> 3) * 1792 + 16 * wave) * 2048 + (slice & 7) * 256;
    const unsigned vWd0 = seg * 2048 + (r8 ^ swzB(seg)) * 16, vWd1 = (seg + 8) * 2048 + (r8 ^ swzB(8 + seg)) * 16;
    const unsigned vWr0 = (wave >> 1) * 8192 + rr * 128 + (((wave & 1) * 4 + (lr & 3)) ^ swzB(rr)) * 16, vWr1 = vWr0 + 8;
    const unsigned vRF = (wave * 8 + (seg >> 2)) * 1024 + ((seg & 3) * 16 + r8) * 16;
    const unsigned vRwr = seg == 0 ? r8 * 64 + wave * 4 : 512 + lr * 4, vRrd = r8 * 64;
    const unsigned vRP = g == (j >> 2) ? r8 * 64 + 16 + wave * 8 + (seg & 1) * 4 : 768 + lr * 4;
    const unsigned vRS = (wave == 0 && seg < 2) ? r8 * 16 + seg * 256 : OOB, vFlag = lr < 32 ? lr * 4 : OOB;
    const unsigned vL4 = ((lr & 2) * 4 + wave * 2 + (lr & 1)) * 128, vLY = min((wave < 3 ? wave * 32 : 112) + lr, wave < 2 ? wave * 32 + 31 : wave == 2 ? 111 : NCV - 1) * 128;
    const unsigned vQ = r8 * 2048 + wave * 512 + seg * 16;
    const unsigned vY0 = wave * 4096 + lr * 16;
    const unsigned vTr = 4096 + wave * 4096 + ((2 * g + ((j & 3) >> 1)) * 8 + (j >> 2)) * 16 + (j & 1) * 8;
    const unsigned one = g == (j >> 2) ? 0x3f80u << (16 * (j & 1)) : 0u, vSa = (j & 2) ? 0u : one, vSb = (j & 2) ? one : 0u;
    (void)sDbg;
    asm volatile(
R"(.macro M d,w,x
v_mfma_f32_16x16x32_bf16 v[\d:\d+3], \w, v[\x:\x+3], v[\d:\d+3]
.endm
.macro R d,a,o
ds_read_b128 v[\d:\d+3], v\a offset:\o
.endm
.macro T d,a,o
ds_read_b64_tr_b16 \d, v\a offset:\o
.endm
.macro D a,s,o
buffer_load_dwordx4 v\a, s[36:39], s\s offen offset:\o sc1 lds
.endm
.macro L c
s_waitcnt lgkmcnt(\c)
.endm
Lcode_r_b:
s_mov_b64 s[36:37],%0
s_mov_b32 s38,111243392
s_mov_b32 s39,0x00020000
s_mov_b32 s44,%1
s_mov_b32 s45,%2
s_mov_b32 s46,%3
s_mov_b32 s50,%4
s_mov_b32 s53,%5
v_mov_b32 v0,%6
v_mov_b32 v1,%7
v_mov_b32 v2,%8
v_mov_b32 v3,%9
v_mov_b32 v4,%10
v_mov_b32 v5,%11
v_mov_b32 v6,%12
v_mov_b32 v7,%13
v_mov_b32 v8,%14
v_mov_b32 v9,%15
v_mov_b32 v10,%16
v_mov_b32 v11,%17
v_mov_b32 v12,%18
v_mov_b32 v13,%19
v_mov_b32 v14,%20
v_mov_b32 v15,%21
v_mov_b32 v16,%22
s_mov_b32 s47,256
s_mov_b32 s49,0
s_mov_b32 s93,s45
s_mov_b64 s[84:85],%23
s_mov_b32 s86,4194304
s_mov_b32 s87,0x00020000
s_lshl_b32 s55,s45,14
buffer_load_dwordx4 v[164:167],v8,s[84:87],s55 offen offset:0 nt
buffer_load_dwordx4 v[168:171],v8,s[84:87],s55 offen offset:128 nt
buffer_load_dwordx4 v[172:175],v8,s[84:87],s55 offen offset:256 nt
buffer_load_dwordx4 v[176:179],v8,s[84:87],s55 offen offset:384 nt
v_mov_b32 v17,s53
v_mbcnt_lo_u32_b32 v19,-1,0
v_mbcnt_hi_u32_b32 v19,-1,v19
v_lshlrev_b32 v19,4,v19
s_mov_b32 s82,0
s_mov_b32 s63,0x7fff0000
s_mov_b32 s64,0
s_mov_b32 s67,0
s_lshl_b32 s65,s44,12
s_add_u32 s65,s65,4096
s_lshl_b32 s66,s44,7
s_add_u32 s66,s66,109056000
s_mov_b32 s70,0xaaaaaaaa
s_mov_b32 s71,0xaaaaaaaa
s_mov_b32 s72,0xcccccccc
s_mov_b32 s73,0xcccccccc
s_mov_b32 s74,0xff00ff00
s_mov_b32 s75,0xff00ff00
s_mov_b32 s68,0x3a800000
s_mov_b32 s69,0x3727c5ac
s_mov_b32 s76,0xc0135761
s_mov_b32 s77,0x3d372713
v_mov_b32 v32,0x3f803f80
v_mov_b32 v33,0x3f803f80
v_mov_b32 v34,0x3f803f80
v_mov_b32 v35,0x3f803f80
v_mov_b32 v24,0
v_mov_b32 v25,0
v_mov_b32 v26,v11
v_mov_b32 v27,v12
v_mov_b32 v28,0
v_mov_b32 v29,0
s_waitcnt vmcnt(0)
Lr_a0_%=:
s_cmp_ge_u32 s45,34
s_cbranch_scc1 Lr_fp0_%=
s_waitcnt vmcnt(0)
buffer_store_dword v17,v5,s[36:39],s63 offen offset:0 sc0 sc1
s_mov_b32 s63,0x7fff0000
Lr_fp0_%=:
s_waitcnt vmcnt(10)
v_mfma_f32_16x16x32_bf16 v[36:39],v[32:35],v[164:167],0
s_add_u32 s79,s45,s46
s_cmp_lt_u32 s45,64
s_cselect_b32 s78,s49,0
v_mfma_f32_16x16x32_bf16 v[40:43],v[164:167],v[164:167],0
s_cmp_ge_u32 s79,64
s_cselect_b32 s78,s78,0
s_add_u32 s62,s79,s78
M 36,v[32:35],168
s_lshr_b32 s78,s45,1
s_and_b32 s79,s45,1
s_lshl_b32 s79,s79,7
M 40,v[168:171],168
s_lshl_b32 s57,s78,15
s_lshl_b32 s55,s62,14
s_cmp_lt_u32 s62,s47
M 36,v[32:35],172
s_cselect_b32 s55,s55,0x7fff0000
s_add_u32 s57,s57,s79
s_lshl_b32 s59,s78,9
M 40,v[172:175],172
buffer_load_dwordx4 v[196:199],v8,s[84:87],s55 offen offset:0 nt
M 36,v[32:35],176
buffer_load_dwordx4 v[200:203],v8,s[84:87],s55 offen offset:128 nt
M 40,v[176:179],176
buffer_load_dwordx4 v[204:207],v8,s[84:87],s55 offen offset:256 nt
v_mfma_f32_16x16x32_bf16 v[116:119],v[26:29],v[164:167],0
buffer_load_dwordx4 v[208:211],v8,s[84:87],s55 offen offset:384 nt
v_mfma_f32_16x16x32_bf16 v[120:123],v[24:27],v[164:167],0
s_add_u32 s57,s57,8388608
s_add_u32 s59,s59,s79
s_lshl_b32 s60,s45,9
v_mfma_f32_16x16x32_bf16 v[124:127],v[26:29],v[168:171],0
s_add_u32 s58,s57,4096
s_add_u32 s59,s59,106954752
s_add_u32 s60,s60,s66
v_mfma_f32_16x16x32_bf16 v[128:131],v[24:27],v[168:171],0
v_add_f32_dpp v44,v36,v36 row_ror:8 row_mask:0xf bank_mask:0xf
v_cndmask_b32 v45,v40,v41,s[70:71]
v_mfma_f32_16x16x32_bf16 v[132:135],v[26:29],v[172:175],0
ds_write_b32 v2,v44 offset:0
v_mfma_f32_16x16x32_bf16 v[136:139],v[24:27],v[172:175],0
v_cndmask_b32 v46,v42,v43,s[70:71]
v_cndmask_b32 v45,v45,v46,s[72:73]
v_mfma_f32_16x16x32_bf16 v[140:143],v[26:29],v[176:179],0
ds_write_b32 v0,v45 offset:0
v_mfma_f32_16x16x32_bf16 v[144:147],v[24:27],v[176:179],0
L 0
s_barrier
R 52,3,0
R 56,3,16
R 60,3,32
s_cmp_lg_u32 s67,0
s_cbranch_scc1 Lr_b0_%=
s_waitcnt vmcnt(4)
buffer_store_dword v17,v5,s[36:39],s63 offen offset:0 sc0 sc1
s_mov_b32 s63,0x7fff0000
s_mov_b32 s38,111243392
s_cmp_lt_u32 s45,64
s_cselect_b32 s64,-192,0
s_add_u32 s64,s64,256
s_cmp_lt_u32 s45,64
s_cselect_b32 s61,0,32768
s_add_u32 s61,s61,111177728
s_mul_i32 s56,s45,16384
s_add_u32 s56,s56,98566144
Lr_bpy0_%=:
buffer_load_dword v18,v7,s[36:39],s61 offen offset:0 sc1
s_add_u32 s82,s82,1
s_waitcnt vmcnt(0)
v_cmp_eq_u32 vcc,s53,v18
s_nop 0
s_cmp_eq_u64 vcc,-1
s_cbranch_scc1 Lr_bqy0_%=
s_sleep 2
s_cmp_lt_u32 s82,20000
s_cbranch_scc1 Lr_bpy0_%=
Lr_bqy0_%=:
s_add_u32 m0,s65,0
s_nop 0
D 9,56,0
D 9,56,1024
D 9,56,2048
D 9,56,3072
s_waitcnt vmcnt(0)
Lr_b0_%=:
s_waitcnt vmcnt(10)
T v[68:69],10,0
T v[70:71],10,64
T v[72:73],10,1024
T v[74:75],10,1088
T v[76:77],10,2048
T v[78:79],10,2112
T v[80:81],10,3072
T v[82:83],10,3136
s_mov_b32 s45,s62
s_min_u32 s78,s64,s47
s_mul_i32 s56,s45,16384
s_add_u32 s56,s56,98566144
s_cmp_lt_i32 s45,s78
s_cselect_b32 s67,1,0
s_cselect_b32 s56,s56,0x7fff0000
s_add_u32 m0,s65,16384
s_nop 0
D 9,56,0
D 9,56,1024
D 9,56,2048
D 9,56,3072
L 8
v_pk_add_f32 v[52:53],v[52:53],v[54:55]
v_pk_add_f32 v[56:57],v[56:57],v[58:59]
v_pk_add_f32 v[60:61],v[60:61],v[62:63]
v_add_f32 v52,v52,v53
v_pk_add_f32 v[56:57],v[56:57],v[60:61]
v_mul_f32 v52,s68,v52
v_add_f32 v53,v56,v57
v_mul_f32 v53,s68,v53
v_fma_f32 v53,-v52,v52,v53
v_max_f32 v53,0,v53
L 6
v_lshlrev_b32 v84,16,v68
v_and_b32 v85,0xffff0000,v68
v_lshlrev_b32 v86,16,v69
v_and_b32 v87,0xffff0000,v69
v_lshlrev_b32 v88,16,v70
v_and_b32 v89,0xffff0000,v70
v_lshlrev_b32 v90,16,v71
v_and_b32 v91,0xffff0000,v71
L 4
v_lshlrev_b32 v92,16,v72
v_and_b32 v93,0xffff0000,v72
v_lshlrev_b32 v94,16,v73
v_and_b32 v95,0xffff0000,v73
v_lshlrev_b32 v96,16,v74
v_and_b32 v97,0xffff0000,v74
v_lshlrev_b32 v98,16,v75
v_and_b32 v99,0xffff0000,v75
L 2
v_lshlrev_b32 v100,16,v76
v_and_b32 v101,0xffff0000,v76
v_lshlrev_b32 v102,16,v77
v_and_b32 v103,0xffff0000,v77
v_lshlrev_b32 v104,16,v78
v_and_b32 v105,0xffff0000,v78
v_lshlrev_b32 v106,16,v79
v_and_b32 v107,0xffff0000,v79
L 0
v_lshlrev_b32 v108,16,v80
v_and_b32 v109,0xffff0000,v80
v_lshlrev_b32 v110,16,v81
v_and_b32 v111,0xffff0000,v81
v_lshlrev_b32 v112,16,v82
v_and_b32 v113,0xffff0000,v82
v_lshlrev_b32 v114,16,v83
v_and_b32 v115,0xffff0000,v83
v_add_f32 v53,s69,v53
v_rsq_f32 v48,v53
s_nop 0
v_mul_f32_e64 v50,-v52,v48
v_mov_b32 v49,v48
v_mov_b32 v51,v50
v_pk_fma_f32 v[52:53],v[116:117],v[48:49],v[50:51]
v_pk_fma_f32 v[54:55],v[118:119],v[48:49],v[50:51]
v_pk_fma_f32 v[56:57],v[120:121],v[48:49],v[50:51]
v_pk_fma_f32 v[58:59],v[122:123],v[48:49],v[50:51]
v_pk_fma_f32 v[116:117],v[52:53],v[84:85],v[116:117]
v_pk_fma_f32 v[118:119],v[54:55],v[86:87],v[118:119]
v_pk_fma_f32 v[120:121],v[56:57],v[88:89],v[120:121]
v_pk_fma_f32 v[122:123],v[58:59],v[90:91],v[122:123]
v_cvt_pk_bf16_f32 v148,v116,v117
v_cvt_pk_bf16_f32 v149,v118,v119
v_cvt_pk_bf16_f32 v150,v120,v121
v_cvt_pk_bf16_f32 v151,v122,v123
v_pk_fma_f32 v[52:53],v[124:125],v[48:49],v[50:51]
v_pk_fma_f32 v[54:55],v[126:127],v[48:49],v[50:51]
v_pk_fma_f32 v[56:57],v[128:129],v[48:49],v[50:51]
v_pk_fma_f32 v[58:59],v[130:131],v[48:49],v[50:51]
v_pk_fma_f32 v[124:125],v[52:53],v[92:93],v[124:125]
v_pk_fma_f32 v[126:127],v[54:55],v[94:95],v[126:127]
v_pk_fma_f32 v[128:129],v[56:57],v[96:97],v[128:129]
v_pk_fma_f32 v[130:131],v[58:59],v[98:99],v[130:131]
v_cvt_pk_bf16_f32 v152,v124,v125
v_cvt_pk_bf16_f32 v153,v126,v127
v_cvt_pk_bf16_f32 v154,v128,v129
v_cvt_pk_bf16_f32 v155,v130,v131
v_pk_fma_f32 v[52:53],v[132:133],v[48:49],v[50:51]
v_pk_fma_f32 v[54:55],v[134:135],v[48:49],v[50:51]
v_pk_fma_f32 v[56:57],v[136:137],v[48:49],v[50:51]
v_pk_fma_f32 v[58:59],v[138:139],v[48:49],v[50:51]
v_pk_fma_f32 v[132:133],v[52:53],v[100:101],v[132:133]
v_pk_fma_f32 v[134:135],v[54:55],v[102:103],v[134:135]
v_pk_fma_f32 v[136:137],v[56:57],v[104:105],v[136:137]
v_pk_fma_f32 v[138:139],v[58:59],v[106:107],v[138:139]
v_cvt_pk_bf16_f32 v156,v132,v133
v_cvt_pk_bf16_f32 v157,v134,v135
v_cvt_pk_bf16_f32 v158,v136,v137
v_cvt_pk_bf16_f32 v159,v138,v139
v_pk_fma_f32 v[52:53],v[140:141],v[48:49],v[50:51]
v_pk_fma_f32 v[54:55],v[142:143],v[48:49],v[50:51]
v_pk_fma_f32 v[56:57],v[144:145],v[48:49],v[50:51]
v_pk_fma_f32 v[58:59],v[146:147],v[48:49],v[50:51]
v_pk_fma_f32 v[140:141],v[52:53],v[108:109],v[140:141]
v_pk_fma_f32 v[142:143],v[54:55],v[110:111],v[142:143]
v_pk_fma_f32 v[144:145],v[56:57],v[112:113],v[144:145]
v_pk_fma_f32 v[146:147],v[58:59],v[114:115],v[146:147]
v_cvt_pk_bf16_f32 v160,v140,v141
v_cvt_pk_bf16_f32 v161,v142,v143
v_cvt_pk_bf16_f32 v162,v144,v145
v_cvt_pk_bf16_f32 v163,v146,v147
s_waitcnt vmcnt(8)
buffer_store_dword v17,v5,s[36:39],s63 offen offset:0 sc0 sc1
s_mov_b32 s63,0x7fff0000
v_mfma_f32_16x16x32_bf16 v[36:39],v[32:35],v[148:151],0
buffer_store_dwordx4 v[148:151],v1,s[36:39],s57 offen offset:0 sc0 sc1
v_mfma_f32_16x16x32_bf16 v[40:43],v[148:151],v[148:151],0
M 36,v[32:35],152
buffer_store_dwordx4 v[152:155],v1,s[36:39],s57 offen offset:2048 sc0 sc1
M 40,v[152:155],152
M 36,v[32:35],156
buffer_store_dwordx4 v[156:159],v1,s[36:39],s58 offen offset:0 sc0 sc1
M 40,v[156:159],156
M 36,v[32:35],160
buffer_store_dwordx4 v[160:163],v1,s[36:39],s58 offen offset:2048 sc0 sc1
M 40,v[160:163],160
s_nop 5
v_add_f32_dpp v44,v36,v36 row_ror:8 row_mask:0xf bank_mask:0xf
s_nop 0
v_cndmask_b32 v45,v40,v41,s[70:71]
v_cndmask_b32 v46,v42,v43,s[70:71]
ds_write_b32 v2,v44 offset:1024
v_cndmask_b32 v45,v45,v46,s[72:73]
ds_write_b32 v0,v45 offset:1024
L 0
s_barrier
R 52,3,1024
R 56,3,1040
R 60,3,1056
L 0
v_pk_add_f32 v[52:53],v[52:53],v[54:55]
v_pk_add_f32 v[56:57],v[56:57],v[58:59]
v_pk_add_f32 v[60:61],v[60:61],v[62:63]
v_add_f32 v52,v52,v53
v_pk_add_f32 v[56:57],v[56:57],v[60:61]
v_mul_f32 v52,s68,v52
v_add_f32 v53,v56,v57
v_mul_f32 v53,s68,v53
v_fma_f32 v53,-v52,v52,v53
v_max_f32 v53,0,v53
v_add_f32 v54,s69,v53
v_fma_f32 v55,s69,v54,v53
v_rsq_f32 v65,v54
v_rsq_f32 v54,v55
v_mul_f32_e64 v64,-v52,v65
v_mul_f32 v63,v55,v54
v_mul_f32 v61,s76,v54
v_mul_f32 v55,v54,v54
v_mul_f32 v62,s77,v55
v_mul_f32 v62,v62,v61
v_cndmask_b32 v60,v52,v64,s[74:75]
v_cndmask_b32 v61,v61,v65,s[74:75]
buffer_store_dwordx4 v[60:63],v4,s[36:39],s59 offen offset:0 sc0 sc1
s_mov_b32 s63,s60
s_cmp_lt_i32 s45,s47
s_cbranch_scc0 Lr_mid_%=
Lr_a1_%=:
s_cmp_ge_u32 s45,34
s_cbranch_scc1 Lr_fp1_%=
s_waitcnt vmcnt(0)
buffer_store_dword v17,v5,s[36:39],s63 offen offset:0 sc0 sc1
s_mov_b32 s63,0x7fff0000
Lr_fp1_%=:
s_waitcnt vmcnt(10)
v_mfma_f32_16x16x32_bf16 v[36:39],v[32:35],v[196:199],0
s_add_u32 s79,s45,s46
s_cmp_lt_u32 s45,64
s_cselect_b32 s78,s49,0
v_mfma_f32_16x16x32_bf16 v[40:43],v[196:199],v[196:199],0
s_cmp_ge_u32 s79,64
s_cselect_b32 s78,s78,0
s_add_u32 s62,s79,s78
M 36,v[32:35],200
s_lshr_b32 s78,s45,1
s_and_b32 s79,s45,1
s_lshl_b32 s79,s79,7
M 40,v[200:203],200
s_lshl_b32 s57,s78,15
s_lshl_b32 s55,s62,14
s_cmp_lt_u32 s62,s47
M 36,v[32:35],204
s_cselect_b32 s55,s55,0x7fff0000
s_add_u32 s57,s57,s79
s_lshl_b32 s59,s78,9
M 40,v[204:207],204
buffer_load_dwordx4 v[164:167],v8,s[84:87],s55 offen offset:0 nt
M 36,v[32:35],208
buffer_load_dwordx4 v[168:171],v8,s[84:87],s55 offen offset:128 nt
M 40,v[208:211],208
buffer_load_dwordx4 v[172:175],v8,s[84:87],s55 offen offset:256 nt
v_mfma_f32_16x16x32_bf16 v[116:119],v[26:29],v[196:199],0
buffer_load_dwordx4 v[176:179],v8,s[84:87],s55 offen offset:384 nt
v_mfma_f32_16x16x32_bf16 v[120:123],v[24:27],v[196:199],0
s_add_u32 s57,s57,8388608
s_add_u32 s59,s59,s79
s_lshl_b32 s60,s45,9
v_mfma_f32_16x16x32_bf16 v[124:127],v[26:29],v[200:203],0
s_add_u32 s58,s57,4096
s_add_u32 s59,s59,106954752
s_add_u32 s60,s60,s66
v_mfma_f32_16x16x32_bf16 v[128:131],v[24:27],v[200:203],0
v_add_f32_dpp v44,v36,v36 row_ror:8 row_mask:0xf bank_mask:0xf
v_cndmask_b32 v45,v40,v41,s[70:71]
v_mfma_f32_16x16x32_bf16 v[132:135],v[26:29],v[204:207],0
ds_write_b32 v2,v44 offset:0
v_mfma_f32_16x16x32_bf16 v[136:139],v[24:27],v[204:207],0
v_cndmask_b32 v46,v42,v43,s[70:71]
v_cndmask_b32 v45,v45,v46,s[72:73]
v_mfma_f32_16x16x32_bf16 v[140:143],v[26:29],v[208:211],0
ds_write_b32 v0,v45 offset:0
v_mfma_f32_16x16x32_bf16 v[144:147],v[24:27],v[208:211],0
L 0
s_barrier
R 52,3,0
R 56,3,16
R 60,3,32
s_cmp_lg_u32 s67,0
s_cbranch_scc1 Lr_b1_%=
s_waitcnt vmcnt(4)
buffer_store_dword v17,v5,s[36:39],s63 offen offset:0 sc0 sc1
s_mov_b32 s63,0x7fff0000
s_mov_b32 s38,111243392
s_cmp_lt_u32 s45,64
s_cselect_b32 s64,-192,0
s_add_u32 s64,s64,256
s_cmp_lt_u32 s45,64
s_cselect_b32 s61,0,32768
s_add_u32 s61,s61,111177728
s_mul_i32 s56,s45,16384
s_add_u32 s56,s56,98566144
Lr_bpy1_%=:
buffer_load_dword v18,v7,s[36:39],s61 offen offset:0 sc1
s_add_u32 s82,s82,1
s_waitcnt vmcnt(0)
v_cmp_eq_u32 vcc,s53,v18
s_nop 0
s_cmp_eq_u64 vcc,-1
s_cbranch_scc1 Lr_bqy1_%=
s_sleep 2
s_cmp_lt_u32 s82,20000
s_cbranch_scc1 Lr_bpy1_%=
Lr_bqy1_%=:
s_add_u32 m0,s65,16384
s_nop 0
D 9,56,0
D 9,56,1024
D 9,56,2048
D 9,56,3072
s_waitcnt vmcnt(0)
Lr_b1_%=:
s_waitcnt vmcnt(10)
T v[68:69],10,16384
T v[70:71],10,16448
T v[72:73],10,17408
T v[74:75],10,17472
T v[76:77],10,18432
T v[78:79],10,18496
T v[80:81],10,19456
T v[82:83],10,19520
s_mov_b32 s45,s62
s_min_u32 s78,s64,s47
s_mul_i32 s56,s45,16384
s_add_u32 s56,s56,98566144
s_cmp_lt_i32 s45,s78
s_cselect_b32 s67,1,0
s_cselect_b32 s56,s56,0x7fff0000
s_add_u32 m0,s65,0
s_nop 0
D 9,56,0
D 9,56,1024
D 9,56,2048
D 9,56,3072
L 8
v_pk_add_f32 v[52:53],v[52:53],v[54:55]
v_pk_add_f32 v[56:57],v[56:57],v[58:59]
v_pk_add_f32 v[60:61],v[60:61],v[62:63]
v_add_f32 v52,v52,v53
v_pk_add_f32 v[56:57],v[56:57],v[60:61]
v_mul_f32 v52,s68,v52
v_add_f32 v53,v56,v57
v_mul_f32 v53,s68,v53
v_fma_f32 v53,-v52,v52,v53
v_max_f32 v53,0,v53
L 6
v_lshlrev_b32 v84,16,v68
v_and_b32 v85,0xffff0000,v68
v_lshlrev_b32 v86,16,v69
v_and_b32 v87,0xffff0000,v69
v_lshlrev_b32 v88,16,v70
v_and_b32 v89,0xffff0000,v70
v_lshlrev_b32 v90,16,v71
v_and_b32 v91,0xffff0000,v71
L 4
v_lshlrev_b32 v92,16,v72
v_and_b32 v93,0xffff0000,v72
v_lshlrev_b32 v94,16,v73
v_and_b32 v95,0xffff0000,v73
v_lshlrev_b32 v96,16,v74
v_and_b32 v97,0xffff0000,v74
)"
R"(v_lshlrev_b32 v98,16,v75
v_and_b32 v99,0xffff0000,v75
L 2
v_lshlrev_b32 v100,16,v76
v_and_b32 v101,0xffff0000,v76
v_lshlrev_b32 v102,16,v77
v_and_b32 v103,0xffff0000,v77
v_lshlrev_b32 v104,16,v78
v_and_b32 v105,0xffff0000,v78
v_lshlrev_b32 v106,16,v79
v_and_b32 v107,0xffff0000,v79
L 0
v_lshlrev_b32 v108,16,v80
v_and_b32 v109,0xffff0000,v80
v_lshlrev_b32 v110,16,v81
v_and_b32 v111,0xffff0000,v81
v_lshlrev_b32 v112,16,v82
v_and_b32 v113,0xffff0000,v82
v_lshlrev_b32 v114,16,v83
v_and_b32 v115,0xffff0000,v83
v_add_f32 v53,s69,v53
v_rsq_f32 v48,v53
s_nop 0
v_mul_f32_e64 v50,-v52,v48
v_mov_b32 v49,v48
v_mov_b32 v51,v50
v_pk_fma_f32 v[52:53],v[116:117],v[48:49],v[50:51]
v_pk_fma_f32 v[54:55],v[118:119],v[48:49],v[50:51]
v_pk_fma_f32 v[56:57],v[120:121],v[48:49],v[50:51]
v_pk_fma_f32 v[58:59],v[122:123],v[48:49],v[50:51]
v_pk_fma_f32 v[116:117],v[52:53],v[84:85],v[116:117]
v_pk_fma_f32 v[118:119],v[54:55],v[86:87],v[118:119]
v_pk_fma_f32 v[120:121],v[56:57],v[88:89],v[120:121]
v_pk_fma_f32 v[122:123],v[58:59],v[90:91],v[122:123]
v_cvt_pk_bf16_f32 v148,v116,v117
v_cvt_pk_bf16_f32 v149,v118,v119
v_cvt_pk_bf16_f32 v150,v120,v121
v_cvt_pk_bf16_f32 v151,v122,v123
v_pk_fma_f32 v[52:53],v[124:125],v[48:49],v[50:51]
v_pk_fma_f32 v[54:55],v[126:127],v[48:49],v[50:51]
v_pk_fma_f32 v[56:57],v[128:129],v[48:49],v[50:51]
v_pk_fma_f32 v[58:59],v[130:131],v[48:49],v[50:51]
v_pk_fma_f32 v[124:125],v[52:53],v[92:93],v[124:125]
v_pk_fma_f32 v[126:127],v[54:55],v[94:95],v[126:127]
v_pk_fma_f32 v[128:129],v[56:57],v[96:97],v[128:129]
v_pk_fma_f32 v[130:131],v[58:59],v[98:99],v[130:131]
v_cvt_pk_bf16_f32 v152,v124,v125
v_cvt_pk_bf16_f32 v153,v126,v127
v_cvt_pk_bf16_f32 v154,v128,v129
v_cvt_pk_bf16_f32 v155,v130,v131
v_pk_fma_f32 v[52:53],v[132:133],v[48:49],v[50:51]
v_pk_fma_f32 v[54:55],v[134:135],v[48:49],v[50:51]
v_pk_fma_f32 v[56:57],v[136:137],v[48:49],v[50:51]
v_pk_fma_f32 v[58:59],v[138:139],v[48:49],v[50:51]
v_pk_fma_f32 v[132:133],v[52:53],v[100:101],v[132:133]
v_pk_fma_f32 v[134:135],v[54:55],v[102:103],v[134:135]
v_pk_fma_f32 v[136:137],v[56:57],v[104:105],v[136:137]
v_pk_fma_f32 v[138:139],v[58:59],v[106:107],v[138:139]
v_cvt_pk_bf16_f32 v156,v132,v133
v_cvt_pk_bf16_f32 v157,v134,v135
v_cvt_pk_bf16_f32 v158,v136,v137
v_cvt_pk_bf16_f32 v159,v138,v139
v_pk_fma_f32 v[52:53],v[140:141],v[48:49],v[50:51]
v_pk_fma_f32 v[54:55],v[142:143],v[48:49],v[50:51]
v_pk_fma_f32 v[56:57],v[144:145],v[48:49],v[50:51]
v_pk_fma_f32 v[58:59],v[146:147],v[48:49],v[50:51]
v_pk_fma_f32 v[140:141],v[52:53],v[108:109],v[140:141]
v_pk_fma_f32 v[142:143],v[54:55],v[110:111],v[142:143]
v_pk_fma_f32 v[144:145],v[56:57],v[112:113],v[144:145]
v_pk_fma_f32 v[146:147],v[58:59],v[114:115],v[146:147]
v_cvt_pk_bf16_f32 v160,v140,v141
v_cvt_pk_bf16_f32 v161,v142,v143
v_cvt_pk_bf16_f32 v162,v144,v145
v_cvt_pk_bf16_f32 v163,v146,v147
s_waitcnt vmcnt(8)
buffer_store_dword v17,v5,s[36:39],s63 offen offset:0 sc0 sc1
s_mov_b32 s63,0x7fff0000
v_mfma_f32_16x16x32_bf16 v[36:39],v[32:35],v[148:151],0
buffer_store_dwordx4 v[148:151],v1,s[36:39],s57 offen offset:0 sc0 sc1
v_mfma_f32_16x16x32_bf16 v[40:43],v[148:151],v[148:151],0
M 36,v[32:35],152
buffer_store_dwordx4 v[152:155],v1,s[36:39],s57 offen offset:2048 sc0 sc1
M 40,v[152:155],152
M 36,v[32:35],156
buffer_store_dwordx4 v[156:159],v1,s[36:39],s58 offen offset:0 sc0 sc1
M 40,v[156:159],156
M 36,v[32:35],160
buffer_store_dwordx4 v[160:163],v1,s[36:39],s58 offen offset:2048 sc0 sc1
M 40,v[160:163],160
s_nop 5
v_add_f32_dpp v44,v36,v36 row_ror:8 row_mask:0xf bank_mask:0xf
s_nop 0
v_cndmask_b32 v45,v40,v41,s[70:71]
v_cndmask_b32 v46,v42,v43,s[70:71]
ds_write_b32 v2,v44 offset:1024
v_cndmask_b32 v45,v45,v46,s[72:73]
ds_write_b32 v0,v45 offset:1024
L 0
s_barrier
R 52,3,1024
R 56,3,1040
R 60,3,1056
L 0
v_pk_add_f32 v[52:53],v[52:53],v[54:55]
v_pk_add_f32 v[56:57],v[56:57],v[58:59]
v_pk_add_f32 v[60:61],v[60:61],v[62:63]
v_add_f32 v52,v52,v53
v_pk_add_f32 v[56:57],v[56:57],v[60:61]
v_mul_f32 v52,s68,v52
v_add_f32 v53,v56,v57
v_mul_f32 v53,s68,v53
v_fma_f32 v53,-v52,v52,v53
v_max_f32 v53,0,v53
v_add_f32 v54,s69,v53
v_fma_f32 v55,s69,v54,v53
v_rsq_f32 v65,v54
v_rsq_f32 v54,v55
v_mul_f32_e64 v64,-v52,v65
v_mul_f32 v63,v55,v54
v_mul_f32 v61,s76,v54
v_mul_f32 v55,v54,v54
v_mul_f32 v62,s77,v55
v_mul_f32 v62,v62,v61
v_cndmask_b32 v60,v52,v64,s[74:75]
v_cndmask_b32 v61,v61,v65,s[74:75]
buffer_store_dwordx4 v[60:63],v4,s[36:39],s59 offen offset:0 sc0 sc1
s_mov_b32 s63,s60
s_cmp_lt_i32 s45,s47
s_cbranch_scc1 Lr_a0_%=
Lr_mid_%=:
s_waitcnt vmcnt(0)
buffer_store_dword v17,v5,s[36:39],s63 offen offset:0 sc0 sc1
s_mov_b32 s63,0x7fff0000
s_mov_b32 s45,s93
s_cmp_ge_u32 s45,16
s_cbranch_scc1 Lr_now_%=
s_mov_b64 s[40:41],%24
s_mov_b32 s42,7340032
s_mov_b32 s43,0x00020000
s_lshl_b32 s57,s44,11
s_add_u32 s55,s50,0
s_add_u32 m0,s57,40960
s_nop 0
buffer_load_dwordx4 v13,s[40:43],s55 offen offset:0  lds
s_add_u32 m0,s57,41984
s_nop 0
buffer_load_dwordx4 v14,s[40:43],s55 offen offset:0  lds
s_add_u32 m0,s57,49024
s_nop 0
buffer_load_dwordx4 v13,s[40:43],s55 offen offset:128  lds
s_add_u32 m0,s57,50048
s_nop 0
buffer_load_dwordx4 v14,s[40:43],s55 offen offset:128  lds
s_add_u32 s55,s50,131072
s_add_u32 m0,s57,57344
s_nop 0
buffer_load_dwordx4 v13,s[40:43],s55 offen offset:0  lds
s_add_u32 m0,s57,58368
s_nop 0
buffer_load_dwordx4 v14,s[40:43],s55 offen offset:0  lds
s_add_u32 m0,s57,65408
s_nop 0
buffer_load_dwordx4 v13,s[40:43],s55 offen offset:128  lds
s_add_u32 m0,s57,66432
s_nop 0
buffer_load_dwordx4 v14,s[40:43],s55 offen offset:128  lds
s_add_u32 s55,s50,262144
s_add_u32 m0,s57,73728
s_nop 0
buffer_load_dwordx4 v13,s[40:43],s55 offen offset:0  lds
s_add_u32 m0,s57,74752
s_nop 0
buffer_load_dwordx4 v14,s[40:43],s55 offen offset:0  lds
s_add_u32 m0,s57,81792
s_nop 0
buffer_load_dwordx4 v13,s[40:43],s55 offen offset:128  lds
s_add_u32 m0,s57,82816
s_nop 0
buffer_load_dwordx4 v14,s[40:43],s55 offen offset:128  lds
s_add_u32 s55,s50,393216
s_add_u32 m0,s57,90112
s_nop 0
buffer_load_dwordx4 v13,s[40:43],s55 offen offset:0  lds
s_add_u32 m0,s57,91136
s_nop 0
buffer_load_dwordx4 v14,s[40:43],s55 offen offset:0  lds
s_add_u32 m0,s57,98176
s_nop 0
buffer_load_dwordx4 v13,s[40:43],s55 offen offset:128  lds
s_add_u32 m0,s57,99200
s_nop 0
buffer_load_dwordx4 v14,s[40:43],s55 offen offset:128  lds
s_add_u32 s55,s50,524288
s_add_u32 m0,s57,106496
s_nop 0
buffer_load_dwordx4 v13,s[40:43],s55 offen offset:0  lds
s_add_u32 m0,s57,107520
s_nop 0
buffer_load_dwordx4 v14,s[40:43],s55 offen offset:0  lds
s_add_u32 m0,s57,114560
s_nop 0
buffer_load_dwordx4 v13,s[40:43],s55 offen offset:128  lds
s_add_u32 m0,s57,115584
s_nop 0
buffer_load_dwordx4 v14,s[40:43],s55 offen offset:128  lds
s_add_u32 s55,s50,655360
s_add_u32 m0,s57,122880
s_nop 0
buffer_load_dwordx4 v13,s[40:43],s55 offen offset:0  lds
s_add_u32 m0,s57,123904
s_nop 0
buffer_load_dwordx4 v14,s[40:43],s55 offen offset:0  lds
s_add_u32 m0,s57,130944
s_nop 0
buffer_load_dwordx4 v13,s[40:43],s55 offen offset:128  lds
s_add_u32 m0,s57,131968
s_nop 0
buffer_load_dwordx4 v14,s[40:43],s55 offen offset:128  lds
s_waitcnt vmcnt(20) lgkmcnt(0)
s_barrier
s_add_u32 s55,s50,786432
s_add_u32 m0,s57,139264
s_nop 0
buffer_load_dwordx4 v13,s[40:43],s55 offen offset:0  lds
s_add_u32 m0,s57,140288
s_nop 0
buffer_load_dwordx4 v14,s[40:43],s55 offen offset:0  lds
s_add_u32 m0,s57,147328
s_nop 0
buffer_load_dwordx4 v13,s[40:43],s55 offen offset:128  lds
s_add_u32 m0,s57,148352
s_nop 0
buffer_load_dwordx4 v14,s[40:43],s55 offen offset:128  lds
v_add_u32 v44,40960,v15
v_add_u32 v45,40960,v16
T a[0:1],44,0
T a[2:3],44,512
T a[4:5],45,0
T a[6:7],45,512
T a[8:9],44,4096
T a[10:11],44,4608
T a[12:13],45,4096
T a[14:15],45,4608
s_waitcnt vmcnt(20) lgkmcnt(0)
s_barrier
s_add_u32 s55,s50,917504
s_add_u32 m0,s57,40960
s_nop 0
buffer_load_dwordx4 v13,s[40:43],s55 offen offset:0  lds
s_add_u32 m0,s57,41984
s_nop 0
buffer_load_dwordx4 v14,s[40:43],s55 offen offset:0  lds
s_add_u32 m0,s57,49024
s_nop 0
buffer_load_dwordx4 v13,s[40:43],s55 offen offset:128  lds
s_add_u32 m0,s57,50048
s_nop 0
buffer_load_dwordx4 v14,s[40:43],s55 offen offset:128  lds
v_add_u32 v44,57344,v15
v_add_u32 v45,57344,v16
T a[16:17],44,0
T a[18:19],44,512
T a[20:21],45,0
T a[22:23],45,512
T a[24:25],44,4096
T a[26:27],44,4608
T a[28:29],45,4096
T a[30:31],45,4608
s_waitcnt vmcnt(20) lgkmcnt(0)
s_barrier
s_add_u32 s55,s50,1048576
s_add_u32 m0,s57,57344
s_nop 0
buffer_load_dwordx4 v13,s[40:43],s55 offen offset:0  lds
s_add_u32 m0,s57,58368
s_nop 0
buffer_load_dwordx4 v14,s[40:43],s55 offen offset:0  lds
s_add_u32 m0,s57,65408
s_nop 0
buffer_load_dwordx4 v13,s[40:43],s55 offen offset:128  lds
s_add_u32 m0,s57,66432
s_nop 0
buffer_load_dwordx4 v14,s[40:43],s55 offen offset:128  lds
v_add_u32 v44,73728,v15
v_add_u32 v45,73728,v16
T a[32:33],44,0
T a[34:35],44,512
T a[36:37],45,0
T a[38:39],45,512
T a[40:41],44,4096
T a[42:43],44,4608
T a[44:45],45,4096
T a[46:47],45,4608
s_waitcnt vmcnt(20) lgkmcnt(0)
s_barrier
s_add_u32 s55,s50,1179648
s_add_u32 m0,s57,73728
s_nop 0
buffer_load_dwordx4 v13,s[40:43],s55 offen offset:0  lds
s_add_u32 m0,s57,74752
s_nop 0
buffer_load_dwordx4 v14,s[40:43],s55 offen offset:0  lds
s_add_u32 m0,s57,81792
s_nop 0
buffer_load_dwordx4 v13,s[40:43],s55 offen offset:128  lds
s_add_u32 m0,s57,82816
s_nop 0
buffer_load_dwordx4 v14,s[40:43],s55 offen offset:128  lds
v_add_u32 v44,90112,v15
v_add_u32 v45,90112,v16
T a[48:49],44,0
T a[50:51],44,512
T a[52:53],45,0
T a[54:55],45,512
T a[56:57],44,4096
T a[58:59],44,4608
T a[60:61],45,4096
T a[62:63],45,4608
s_waitcnt vmcnt(20) lgkmcnt(0)
s_barrier
s_add_u32 s55,s50,1310720
s_add_u32 m0,s57,90112
s_nop 0
buffer_load_dwordx4 v13,s[40:43],s55 offen offset:0  lds
s_add_u32 m0,s57,91136
s_nop 0
buffer_load_dwordx4 v14,s[40:43],s55 offen offset:0  lds
s_add_u32 m0,s57,98176
s_nop 0
buffer_load_dwordx4 v13,s[40:43],s55 offen offset:128  lds
s_add_u32 m0,s57,99200
s_nop 0
buffer_load_dwordx4 v14,s[40:43],s55 offen offset:128  lds
v_add_u32 v44,106496,v15
v_add_u32 v45,106496,v16
T a[64:65],44,0
T a[66:67],44,512
T a[68:69],45,0
T a[70:71],45,512
T a[72:73],44,4096
T a[74:75],44,4608
T a[76:77],45,4096
T a[78:79],45,4608
s_waitcnt vmcnt(20) lgkmcnt(0)
s_barrier
s_add_u32 s55,s50,1441792
s_add_u32 m0,s57,106496
s_nop 0
buffer_load_dwordx4 v13,s[40:43],s55 offen offset:0  lds
s_add_u32 m0,s57,107520
s_nop 0
buffer_load_dwordx4 v14,s[40:43],s55 offen offset:0  lds
s_add_u32 m0,s57,114560
s_nop 0
buffer_load_dwordx4 v13,s[40:43],s55 offen offset:128  lds
s_add_u32 m0,s57,115584
s_nop 0
buffer_load_dwordx4 v14,s[40:43],s55 offen offset:128  lds
v_add_u32 v44,122880,v15
v_add_u32 v45,122880,v16
T a[80:81],44,0
T a[82:83],44,512
T a[84:85],45,0
T a[86:87],45,512
T a[88:89],44,4096
T a[90:91],44,4608
T a[92:93],45,4096
T a[94:95],45,4608
s_waitcnt vmcnt(20) lgkmcnt(0)
s_barrier
s_add_u32 s55,s50,1572864
s_add_u32 m0,s57,122880
s_nop 0
buffer_load_dwordx4 v13,s[40:43],s55 offen offset:0  lds
s_add_u32 m0,s57,123904
s_nop 0
buffer_load_dwordx4 v14,s[40:43],s55 offen offset:0  lds
s_add_u32 m0,s57,130944
s_nop 0
buffer_load_dwordx4 v13,s[40:43],s55 offen offset:128  lds
s_add_u32 m0,s57,131968
s_nop 0
buffer_load_dwordx4 v14,s[40:43],s55 offen offset:128  lds
v_add_u32 v44,139264,v15
v_add_u32 v45,139264,v16
T a[96:97],44,0
T a[98:99],44,512
T a[100:101],45,0
T a[102:103],45,512
T a[104:105],44,4096
T a[106:107],44,4608
T a[108:109],45,4096
T a[110:111],45,4608
s_waitcnt vmcnt(20) lgkmcnt(0)
s_barrier
s_add_u32 s55,s50,1703936
s_add_u32 m0,s57,139264
s_nop 0
buffer_load_dwordx4 v13,s[40:43],s55 offen offset:0  lds
s_add_u32 m0,s57,140288
s_nop 0
buffer_load_dwordx4 v14,s[40:43],s55 offen offset:0  lds
s_add_u32 m0,s57,147328
s_nop 0
buffer_load_dwordx4 v13,s[40:43],s55 offen offset:128  lds
s_add_u32 m0,s57,148352
s_nop 0
buffer_load_dwordx4 v14,s[40:43],s55 offen offset:128  lds
v_add_u32 v44,40960,v15
v_add_u32 v45,40960,v16
T a[112:113],44,0
T a[114:115],44,512
T a[116:117],45,0
T a[118:119],45,512
T a[120:121],44,4096
T a[122:123],44,4608
T a[124:125],45,4096
T a[126:127],45,4608
s_waitcnt vmcnt(20) lgkmcnt(0)
s_barrier
s_add_u32 s55,s50,1835008
s_add_u32 m0,s57,40960
s_nop 0
buffer_load_dwordx4 v13,s[40:43],s55 offen offset:0  lds
s_add_u32 m0,s57,41984
s_nop 0
buffer_load_dwordx4 v14,s[40:43],s55 offen offset:0  lds
s_add_u32 m0,s57,49024
s_nop 0
buffer_load_dwordx4 v13,s[40:43],s55 offen offset:128  lds
s_add_u32 m0,s57,50048
s_nop 0
buffer_load_dwordx4 v14,s[40:43],s55 offen offset:128  lds
v_add_u32 v44,57344,v15
v_add_u32 v45,57344,v16
T a[128:129],44,0
T a[130:131],44,512
T a[132:133],45,0
T a[134:135],45,512
T a[136:137],44,4096
T a[138:139],44,4608
T a[140:141],45,4096
T a[142:143],45,4608
s_waitcnt vmcnt(20) lgkmcnt(0)
s_barrier
s_add_u32 s55,s50,1966080
s_add_u32 m0,s57,57344
s_nop 0
buffer_load_dwordx4 v13,s[40:43],s55 offen offset:0  lds
s_add_u32 m0,s57,58368
s_nop 0
buffer_load_dwordx4 v14,s[40:43],s55 offen offset:0  lds
s_add_u32 m0,s57,65408
s_nop 0
buffer_load_dwordx4 v13,s[40:43],s55 offen offset:128  lds
s_add_u32 m0,s57,66432
s_nop 0
buffer_load_dwordx4 v14,s[40:43],s55 offen offset:128  lds
v_add_u32 v44,73728,v15
v_add_u32 v45,73728,v16
T a[144:145],44,0
T a[146:147],44,512
T a[148:149],45,0
T a[150:151],45,512
T a[152:153],44,4096
T a[154:155],44,4608
T a[156:157],45,4096
T a[158:159],45,4608
s_waitcnt vmcnt(20) lgkmcnt(0)
s_barrier
s_add_u32 s55,s50,2097152
s_add_u32 m0,s57,73728
s_nop 0
buffer_load_dwordx4 v13,s[40:43],s55 offen offset:0  lds
s_add_u32 m0,s57,74752
s_nop 0
buffer_load_dwordx4 v14,s[40:43],s55 offen offset:0  lds
s_add_u32 m0,s57,81792
s_nop 0
buffer_load_dwordx4 v13,s[40:43],s55 offen offset:128  lds
s_add_u32 m0,s57,82816
s_nop 0
buffer_load_dwordx4 v14,s[40:43],s55 offen offset:128  lds
v_add_u32 v44,90112,v15
v_add_u32 v45,90112,v16
T a[160:161],44,0
T a[162:163],44,512
T a[164:165],45,0
T a[166:167],45,512
T a[168:169],44,4096
T a[170:171],44,4608
T a[172:173],45,4096
T a[174:175],45,4608
s_waitcnt vmcnt(20) lgkmcnt(0)
s_barrier
s_add_u32 s55,s50,2228224
s_add_u32 m0,s57,90112
s_nop 0
buffer_load_dwordx4 v13,s[40:43],s55 offen offset:0  lds
s_add_u32 m0,s57,91136
)"
R"(s_nop 0
buffer_load_dwordx4 v14,s[40:43],s55 offen offset:0  lds
s_add_u32 m0,s57,98176
s_nop 0
buffer_load_dwordx4 v13,s[40:43],s55 offen offset:128  lds
s_add_u32 m0,s57,99200
s_nop 0
buffer_load_dwordx4 v14,s[40:43],s55 offen offset:128  lds
v_add_u32 v44,106496,v15
v_add_u32 v45,106496,v16
T a[176:177],44,0
T a[178:179],44,512
T a[180:181],45,0
T a[182:183],45,512
T a[184:185],44,4096
T a[186:187],44,4608
T a[188:189],45,4096
T a[190:191],45,4608
s_waitcnt vmcnt(20) lgkmcnt(0)
s_barrier
s_add_u32 s55,s50,2359296
s_add_u32 m0,s57,106496
s_nop 0
buffer_load_dwordx4 v13,s[40:43],s55 offen offset:0  lds
s_add_u32 m0,s57,107520
s_nop 0
buffer_load_dwordx4 v14,s[40:43],s55 offen offset:0  lds
s_add_u32 m0,s57,114560
s_nop 0
buffer_load_dwordx4 v13,s[40:43],s55 offen offset:128  lds
s_add_u32 m0,s57,115584
s_nop 0
buffer_load_dwordx4 v14,s[40:43],s55 offen offset:128  lds
v_add_u32 v44,122880,v15
v_add_u32 v45,122880,v16
T a[192:193],44,0
T a[194:195],44,512
T a[196:197],45,0
T a[198:199],45,512
T a[200:201],44,4096
T a[202:203],44,4608
T a[204:205],45,4096
T a[206:207],45,4608
s_waitcnt vmcnt(20) lgkmcnt(0)
s_barrier
s_add_u32 s55,s50,2490368
s_add_u32 m0,s57,122880
s_nop 0
buffer_load_dwordx4 v13,s[40:43],s55 offen offset:0  lds
s_add_u32 m0,s57,123904
s_nop 0
buffer_load_dwordx4 v14,s[40:43],s55 offen offset:0  lds
s_add_u32 m0,s57,130944
s_nop 0
buffer_load_dwordx4 v13,s[40:43],s55 offen offset:128  lds
s_add_u32 m0,s57,131968
s_nop 0
buffer_load_dwordx4 v14,s[40:43],s55 offen offset:128  lds
v_add_u32 v44,139264,v15
v_add_u32 v45,139264,v16
T a[208:209],44,0
T a[210:211],44,512
T a[212:213],45,0
T a[214:215],45,512
T a[216:217],44,4096
T a[218:219],44,4608
T a[220:221],45,4096
T a[222:223],45,4608
s_waitcnt vmcnt(20) lgkmcnt(0)
s_barrier
s_add_u32 s55,s50,2621440
s_add_u32 m0,s57,139264
s_nop 0
buffer_load_dwordx4 v13,s[40:43],s55 offen offset:0  lds
s_add_u32 m0,s57,140288
s_nop 0
buffer_load_dwordx4 v14,s[40:43],s55 offen offset:0  lds
s_add_u32 m0,s57,147328
s_nop 0
buffer_load_dwordx4 v13,s[40:43],s55 offen offset:128  lds
s_add_u32 m0,s57,148352
s_nop 0
buffer_load_dwordx4 v14,s[40:43],s55 offen offset:128  lds
v_add_u32 v44,40960,v15
v_add_u32 v45,40960,v16
T a[224:225],44,0
T a[226:227],44,512
T a[228:229],45,0
T a[230:231],45,512
T a[232:233],44,4096
T a[234:235],44,4608
T a[236:237],45,4096
T a[238:239],45,4608
s_waitcnt vmcnt(20) lgkmcnt(0)
s_barrier
s_add_u32 s55,s50,2752512
s_add_u32 m0,s57,40960
s_nop 0
buffer_load_dwordx4 v13,s[40:43],s55 offen offset:0  lds
s_add_u32 m0,s57,41984
s_nop 0
buffer_load_dwordx4 v14,s[40:43],s55 offen offset:0  lds
s_add_u32 m0,s57,49024
s_nop 0
buffer_load_dwordx4 v13,s[40:43],s55 offen offset:128  lds
s_add_u32 m0,s57,50048
s_nop 0
buffer_load_dwordx4 v14,s[40:43],s55 offen offset:128  lds
v_add_u32 v44,57344,v15
v_add_u32 v45,57344,v16
T a[240:241],44,0
T a[242:243],44,512
T a[244:245],45,0
T a[246:247],45,512
T a[248:249],44,4096
T a[250:251],44,4608
T a[252:253],45,4096
T a[254:255],45,4608
L 0
s_barrier
Lr_now_%=:
s_lshr_b32 s78,s45,1
s_and_b32 s79,s45,1
s_lshl_b32 s79,s79,7
s_lshl_b32 s55,s78,15
s_add_u32 s55,s55,s79
s_mul_i32 s56,s45,16384
s_add_u32 s56,s56,98566144
s_lshl_b32 s61,s78,11
s_add_u32 s61,s61,110587904
s_add_u32 s78,s45,s46
s_lshr_b32 s78,s78,1
s_lshl_b32 s62,s78,11
s_add_u32 s62,s62,110587904
Lr_bps_%=:
buffer_load_dword v18,v6,s[36:39],s61 offen offset:0 sc1
s_add_u32 s82,s82,1
s_waitcnt vmcnt(0)
v_cmp_eq_u32 vcc,s53,v18
s_nop 0
s_cmp_eq_u64 vcc,-1
s_cbranch_scc1 Lr_bqs_%=
s_sleep 2
s_cmp_lt_u32 s82,20000
s_cbranch_scc1 Lr_bps_%=
Lr_bqs_%=:
s_add_u32 s78,s55,71303168
buffer_load_dwordx4 v[164:167],v1,s[36:39],s78 offen offset:0 sc1
buffer_load_dwordx4 v[168:171],v1,s[36:39],s78 offen offset:2048 sc1
s_add_u32 s78,s55,71307264
buffer_load_dwordx4 v[172:175],v1,s[36:39],s78 offen offset:0 sc1
buffer_load_dwordx4 v[176:179],v1,s[36:39],s78 offen offset:2048 sc1
s_add_u32 s78,s55,89128960
buffer_load_dwordx4 v[180:183],v1,s[36:39],s78 offen offset:0 sc1
buffer_load_dwordx4 v[184:187],v1,s[36:39],s78 offen offset:2048 sc1
s_add_u32 s78,s55,89133056
buffer_load_dwordx4 v[188:191],v1,s[36:39],s78 offen offset:0 sc1
buffer_load_dwordx4 v[192:195],v1,s[36:39],s78 offen offset:2048 sc1
s_add_u32 m0,s65,0
s_nop 0
D 9,56,0
D 9,56,1024
D 9,56,2048
D 9,56,3072
buffer_load_dword v18,v6,s[36:39],s62 offen offset:0 sc1
s_lshr_b32 s78,s45,1
s_and_b32 s79,s45,1
s_lshl_b32 s79,s79,7
s_lshl_b32 s57,s78,15
s_add_u32 s57,s57,s79
s_add_u32 s57,s57,12582912
s_add_u32 s58,s57,4096
s_lshl_b32 s59,s78,9
s_add_u32 s59,s59,s79
s_add_u32 s59,s59,107020288
s_lshl_b32 s60,s45,9
s_add_u32 s60,s60,s66
s_add_u32 s60,s60,131072
s_add_u32 s45,s45,s46
s_lshr_b32 s78,s45,1
s_and_b32 s79,s45,1
s_lshl_b32 s79,s79,7
s_lshl_b32 s55,s78,15
s_add_u32 s55,s55,s79
s_mul_i32 s56,s45,16384
s_add_u32 s56,s56,98566144
s_lshl_b32 s61,s78,11
s_add_u32 s61,s61,110587904
s_add_u32 s78,s45,s46
s_lshr_b32 s78,s78,1
s_lshl_b32 s62,s78,11
s_add_u32 s62,s62,110587904
s_waitcnt vmcnt(0)
Lr_it0_%=:
T v[68:69],10,0
T v[70:71],10,64
T v[72:73],10,1024
T v[74:75],10,1088
T v[76:77],10,2048
T v[78:79],10,2112
T v[80:81],10,3072
T v[82:83],10,3136
s_nop 0
v_cmp_eq_u32 vcc,s53,v18
s_nop 0
s_cmp_eq_u64 vcc,-1
s_cselect_b32 s78,s45,256
s_cmp_lt_u32 s78,256
s_cbranch_scc1 Lr_ta0_%=
buffer_load_dword v18,v6,s[36:39],s61 offen offset:0 sc1
v_mfma_f32_16x16x32_bf16 v[116:119],v[26:29],v[164:167],0
L 6
v_lshlrev_b32 v84,16,v68
v_and_b32 v85,0xffff0000,v68
v_mfma_f32_16x16x32_bf16 v[120:123],v[24:27],v[164:167],0
L 4
v_lshlrev_b32 v86,16,v69
v_and_b32 v87,0xffff0000,v69
M 116,v[26:29],180
L 2
v_lshlrev_b32 v88,16,v70
v_and_b32 v89,0xffff0000,v70
M 120,v[24:27],180
L 0
v_lshlrev_b32 v90,16,v71
v_and_b32 v91,0xffff0000,v71
v_mfma_f32_16x16x32_bf16 v[124:127],v[26:29],v[168:171],0
v_lshlrev_b32 v92,16,v72
v_and_b32 v93,0xffff0000,v72
v_mfma_f32_16x16x32_bf16 v[128:131],v[24:27],v[168:171],0
v_lshlrev_b32 v94,16,v73
v_and_b32 v95,0xffff0000,v73
M 124,v[26:29],184
v_cvt_pk_bf16_f32 v148,v116,v117
v_cvt_pk_bf16_f32 v149,v118,v119
M 128,v[24:27],184
v_cvt_pk_bf16_f32 v150,v120,v121
v_cvt_pk_bf16_f32 v151,v122,v123
v_mfma_f32_16x16x32_bf16 v[132:135],v[26:29],v[172:175],0
v_lshlrev_b32 v96,16,v74
v_and_b32 v97,0xffff0000,v74
v_mfma_f32_16x16x32_bf16 v[136:139],v[24:27],v[172:175],0
v_lshlrev_b32 v98,16,v75
v_and_b32 v99,0xffff0000,v75
M 132,v[26:29],188
v_cvt_pk_bf16_f32 v152,v124,v125
v_cvt_pk_bf16_f32 v153,v126,v127
M 136,v[24:27],188
v_cvt_pk_bf16_f32 v154,v128,v129
v_cvt_pk_bf16_f32 v155,v130,v131
v_mfma_f32_16x16x32_bf16 v[140:143],v[26:29],v[176:179],0
v_lshlrev_b32 v100,16,v76
v_and_b32 v101,0xffff0000,v76
v_mfma_f32_16x16x32_bf16 v[144:147],v[24:27],v[176:179],0
v_lshlrev_b32 v102,16,v77
v_and_b32 v103,0xffff0000,v77
M 140,v[26:29],192
v_cvt_pk_bf16_f32 v156,v132,v133
v_cvt_pk_bf16_f32 v157,v134,v135
M 144,v[24:27],192
v_cvt_pk_bf16_f32 v158,v136,v137
v_cvt_pk_bf16_f32 v159,v138,v139
v_mfma_f32_16x16x32_bf16 v[36:39],v[32:35],v[148:151],0
v_lshlrev_b32 v104,16,v78
v_and_b32 v105,0xffff0000,v78
v_mfma_f32_16x16x32_bf16 v[40:43],v[148:151],v[148:151],0
v_lshlrev_b32 v106,16,v79
v_and_b32 v107,0xffff0000,v79
M 36,v[32:35],152
v_cvt_pk_bf16_f32 v160,v140,v141
v_cvt_pk_bf16_f32 v161,v142,v143
M 40,v[152:155],152
v_cvt_pk_bf16_f32 v162,v144,v145
v_cvt_pk_bf16_f32 v163,v146,v147
M 36,v[32:35],156
v_lshlrev_b32 v108,16,v80
v_and_b32 v109,0xffff0000,v80
M 40,v[156:159],156
v_lshlrev_b32 v110,16,v81
v_and_b32 v111,0xffff0000,v81
M 36,v[32:35],160
v_lshlrev_b32 v112,16,v82
v_and_b32 v113,0xffff0000,v82
M 40,v[160:163],160
v_lshlrev_b32 v114,16,v83
v_and_b32 v115,0xffff0000,v83
s_nop 2
v_add_f32_dpp v44,v36,v36 row_ror:8 row_mask:0xf bank_mask:0xf
s_nop 1
v_cndmask_b32 v45,v40,v41,s[70:71]
v_cndmask_b32 v46,v42,v43,s[70:71]
ds_write_b32 v2,v44 offset:0
v_cndmask_b32 v45,v45,v46,s[72:73]
ds_write_b32 v0,v45 offset:0
L 0
s_barrier
R 52,3,0
R 56,3,16
R 60,3,32
L 0
v_pk_add_f32 v[52:53],v[52:53],v[54:55]
v_pk_add_f32 v[56:57],v[56:57],v[58:59]
v_pk_add_f32 v[60:61],v[60:61],v[62:63]
v_add_f32 v52,v52,v53
v_pk_add_f32 v[56:57],v[56:57],v[60:61]
v_mul_f32 v52,s68,v52
v_add_f32 v53,v56,v57
v_mul_f32 v53,s68,v53
v_fma_f32 v53,-v52,v52,v53
v_max_f32 v53,0,v53
s_waitcnt vmcnt(1)
buffer_store_dword v17,v5,s[36:39],s63 offen offset:0 sc0 sc1
s_mov_b32 s63,0x7fff0000
s_waitcnt vmcnt(1)
v_cmp_eq_u32 vcc,s53,v18
s_nop 0
s_cmp_eq_u64 vcc,-1
s_cselect_b32 s78,s45,256
s_cmp_lt_u32 s78,256
s_cbranch_scc1 Lr_tb0_%=
buffer_load_dword v18,v6,s[36:39],s61 offen offset:0 sc1
v_add_f32 v53,s69,v53
v_rsq_f32 v48,v53
s_nop 0
v_mul_f32_e64 v50,-v52,v48
v_mov_b32 v49,v48
v_mov_b32 v51,v50
v_pk_fma_f32 v[52:53],v[116:117],v[48:49],v[50:51]
v_pk_fma_f32 v[54:55],v[118:119],v[48:49],v[50:51]
v_pk_fma_f32 v[56:57],v[120:121],v[48:49],v[50:51]
v_pk_fma_f32 v[58:59],v[122:123],v[48:49],v[50:51]
v_pk_fma_f32 v[116:117],v[52:53],v[84:85],v[116:117]
v_pk_fma_f32 v[118:119],v[54:55],v[86:87],v[118:119]
v_pk_fma_f32 v[120:121],v[56:57],v[88:89],v[120:121]
v_pk_fma_f32 v[122:123],v[58:59],v[90:91],v[122:123]
v_cvt_pk_bf16_f32 v148,v116,v117
v_cvt_pk_bf16_f32 v149,v118,v119
v_cvt_pk_bf16_f32 v150,v120,v121
v_cvt_pk_bf16_f32 v151,v122,v123
v_pk_fma_f32 v[52:53],v[124:125],v[48:49],v[50:51]
v_pk_fma_f32 v[54:55],v[126:127],v[48:49],v[50:51]
v_pk_fma_f32 v[56:57],v[128:129],v[48:49],v[50:51]
v_pk_fma_f32 v[58:59],v[130:131],v[48:49],v[50:51]
v_pk_fma_f32 v[124:125],v[52:53],v[92:93],v[124:125]
v_pk_fma_f32 v[126:127],v[54:55],v[94:95],v[126:127]
v_pk_fma_f32 v[128:129],v[56:57],v[96:97],v[128:129]
v_pk_fma_f32 v[130:131],v[58:59],v[98:99],v[130:131]
v_cvt_pk_bf16_f32 v152,v124,v125
v_cvt_pk_bf16_f32 v153,v126,v127
v_cvt_pk_bf16_f32 v154,v128,v129
v_cvt_pk_bf16_f32 v155,v130,v131
v_pk_fma_f32 v[52:53],v[132:133],v[48:49],v[50:51]
v_pk_fma_f32 v[54:55],v[134:135],v[48:49],v[50:51]
v_pk_fma_f32 v[56:57],v[136:137],v[48:49],v[50:51]
v_pk_fma_f32 v[58:59],v[138:139],v[48:49],v[50:51]
v_pk_fma_f32 v[132:133],v[52:53],v[100:101],v[132:133]
v_pk_fma_f32 v[134:135],v[54:55],v[102:103],v[134:135]
v_pk_fma_f32 v[136:137],v[56:57],v[104:105],v[136:137]
v_pk_fma_f32 v[138:139],v[58:59],v[106:107],v[138:139]
v_cvt_pk_bf16_f32 v156,v132,v133
v_cvt_pk_bf16_f32 v157,v134,v135
v_cvt_pk_bf16_f32 v158,v136,v137
v_cvt_pk_bf16_f32 v159,v138,v139
v_pk_fma_f32 v[52:53],v[140:141],v[48:49],v[50:51]
v_pk_fma_f32 v[54:55],v[142:143],v[48:49],v[50:51]
v_pk_fma_f32 v[56:57],v[144:145],v[48:49],v[50:51]
v_pk_fma_f32 v[58:59],v[146:147],v[48:49],v[50:51]
v_pk_fma_f32 v[140:141],v[52:53],v[108:109],v[140:141]
v_pk_fma_f32 v[142:143],v[54:55],v[110:111],v[142:143]
v_pk_fma_f32 v[144:145],v[56:57],v[112:113],v[144:145]
v_pk_fma_f32 v[146:147],v[58:59],v[114:115],v[146:147]
v_cvt_pk_bf16_f32 v160,v140,v141
v_cvt_pk_bf16_f32 v161,v142,v143
v_cvt_pk_bf16_f32 v162,v144,v145
v_cvt_pk_bf16_f32 v163,v146,v147
v_mfma_f32_16x16x32_bf16 v[36:39],v[32:35],v[148:151],0
buffer_store_dwordx4 v[148:151],v1,s[36:39],s57 offen offset:0 sc0 sc1
v_mfma_f32_16x16x32_bf16 v[40:43],v[148:151],v[148:151],0
M 36,v[32:35],152
buffer_store_dwordx4 v[152:155],v1,s[36:39],s57 offen offset:2048 sc0 sc1
M 40,v[152:155],152
M 36,v[32:35],156
buffer_store_dwordx4 v[156:159],v1,s[36:39],s58 offen offset:0 sc0 sc1
M 40,v[156:159],156
M 36,v[32:35],160
buffer_store_dwordx4 v[160:163],v1,s[36:39],s58 offen offset:2048 sc0 sc1
M 40,v[160:163],160
s_nop 5
v_add_f32_dpp v44,v36,v36 row_ror:8 row_mask:0xf bank_mask:0xf
s_nop 0
v_cndmask_b32 v45,v40,v41,s[70:71]
v_cndmask_b32 v46,v42,v43,s[70:71]
ds_write_b32 v2,v44 offset:1024
v_cndmask_b32 v45,v45,v46,s[72:73]
ds_write_b32 v0,v45 offset:1024
L 0
s_barrier
R 52,3,1024
R 56,3,1040
R 60,3,1056
L 0
v_pk_add_f32 v[52:53],v[52:53],v[54:55]
v_pk_add_f32 v[56:57],v[56:57],v[58:59]
v_pk_add_f32 v[60:61],v[60:61],v[62:63]
v_add_f32 v52,v52,v53
v_pk_add_f32 v[56:57],v[56:57],v[60:61]
v_mul_f32 v52,s68,v52
v_add_f32 v53,v56,v57
v_mul_f32 v53,s68,v53
v_fma_f32 v53,-v52,v52,v53
v_max_f32 v53,0,v53
v_add_f32 v54,s69,v53
v_fma_f32 v55,s69,v54,v53
v_rsq_f32 v65,v54
v_rsq_f32 v54,v55
v_mul_f32_e64 v64,-v52,v65
v_mul_f32 v63,v55,v54
v_mul_f32 v61,s76,v54
v_mul_f32 v55,v54,v54
v_mul_f32 v62,s77,v55
v_mul_f32 v62,v62,v61
v_cndmask_b32 v60,v52,v64,s[74:75]
v_cndmask_b32 v61,v61,v65,s[74:75]
buffer_store_dwordx4 v[60:63],v4,s[36:39],s59 offen offset:0 sc0 sc1
s_mov_b32 s63,s60
s_waitcnt vmcnt(5)
v_cmp_eq_u32 vcc,s53,v18
s_nop 0
s_cmp_eq_u64 vcc,-1
s_cselect_b32 s78,s45,256
s_cmp_lt_u32 s78,256
s_cbranch_scc1 Lr_tc0_%=
s_waitcnt vmcnt(0)
buffer_store_dword v17,v5,s[36:39],s63 offen offset:0 sc0 sc1
s_mov_b32 s63,0x7fff0000
s_cmp_ge_u32 s45,256
s_cbranch_scc1 Lr_done_%=
Lr_bp0_%=:
buffer_load_dword v18,v6,s[36:39],s61 offen offset:0 sc1
s_add_u32 s82,s82,1
s_waitcnt vmcnt(0)
v_cmp_eq_u32 vcc,s53,v18
s_nop 0
s_cmp_eq_u64 vcc,-1
s_cbranch_scc1 Lr_bq0_%=
s_sleep 2
s_cmp_lt_u32 s82,20000
s_cbranch_scc1 Lr_bp0_%=
Lr_bq0_%=:
s_add_u32 s78,s55,71303168
buffer_load_dwordx4 v[196:199],v1,s[36:39],s78 offen offset:0 sc1
buffer_load_dwordx4 v[200:203],v1,s[36:39],s78 offen offset:2048 sc1
s_add_u32 s78,s55,71307264
buffer_load_dwordx4 v[204:207],v1,s[36:39],s78 offen offset:0 sc1
buffer_load_dwordx4 v[208:211],v1,s[36:39],s78 offen offset:2048 sc1
s_add_u32 s78,s55,89128960
buffer_load_dwordx4 v[212:215],v1,s[36:39],s78 offen offset:0 sc1
buffer_load_dwordx4 v[216:219],v1,s[36:39],s78 offen offset:2048 sc1
s_add_u32 s78,s55,89133056
buffer_load_dwordx4 v[220:223],v1,s[36:39],s78 offen offset:0 sc1
buffer_load_dwordx4 v[224:227],v1,s[36:39],s78 offen offset:2048 sc1
s_add_u32 m0,s65,16384
s_nop 0
D 9,56,0
D 9,56,1024
D 9,56,2048
D 9,56,3072
buffer_load_dword v18,v6,s[36:39],s62 offen offset:0 sc1
s_lshr_b32 s78,s45,1
s_and_b32 s79,s45,1
s_lshl_b32 s79,s79,7
s_lshl_b32 s57,s78,15
s_add_u32 s57,s57,s79
s_add_u32 s57,s57,12582912
s_add_u32 s58,s57,4096
s_lshl_b32 s59,s78,9
s_add_u32 s59,s59,s79
s_add_u32 s59,s59,107020288
s_lshl_b32 s60,s45,9
s_add_u32 s60,s60,s66
s_add_u32 s60,s60,131072
s_add_u32 s45,s45,s46
s_lshr_b32 s78,s45,1
s_and_b32 s79,s45,1
s_lshl_b32 s79,s79,7
s_lshl_b32 s55,s78,15
s_add_u32 s55,s55,s79
s_mul_i32 s56,s45,16384
s_add_u32 s56,s56,98566144
s_lshl_b32 s61,s78,11
s_add_u32 s61,s61,110587904
s_add_u32 s78,s45,s46
s_lshr_b32 s78,s78,1
s_lshl_b32 s62,s78,11
s_add_u32 s62,s62,110587904
s_waitcnt vmcnt(0)
s_branch Lr_it1_%=
Lr_ta0_%=:
v_mfma_f32_16x16x32_bf16 v[116:119],v[26:29],v[164:167],0
s_add_u32 s78,s55,71303168
)"
R"(L 6
L 4
v_mfma_f32_16x16x32_bf16 v[120:123],v[24:27],v[164:167],0
L 2
buffer_load_dwordx4 v[196:199],v1,s[36:39],s78 offen offset:0 sc1
M 116,v[26:29],180
L 0
buffer_load_dwordx4 v[200:203],v1,s[36:39],s78 offen offset:2048 sc1
M 120,v[24:27],180
s_add_u32 s78,s55,71307264
v_lshlrev_b32 v84,16,v68
v_and_b32 v85,0xffff0000,v68
v_mfma_f32_16x16x32_bf16 v[124:127],v[26:29],v[168:171],0
buffer_load_dwordx4 v[204:207],v1,s[36:39],s78 offen offset:0 sc1
v_mfma_f32_16x16x32_bf16 v[128:131],v[24:27],v[168:171],0
buffer_load_dwordx4 v[208:211],v1,s[36:39],s78 offen offset:2048 sc1
M 124,v[26:29],184
s_add_u32 s78,s55,89128960
v_cvt_pk_bf16_f32 v148,v116,v117
v_cvt_pk_bf16_f32 v149,v118,v119
M 128,v[24:27],184
buffer_load_dwordx4 v[212:215],v1,s[36:39],s78 offen offset:0 sc1
v_mfma_f32_16x16x32_bf16 v[132:135],v[26:29],v[172:175],0
buffer_load_dwordx4 v[216:219],v1,s[36:39],s78 offen offset:2048 sc1
v_mfma_f32_16x16x32_bf16 v[136:139],v[24:27],v[172:175],0
s_add_u32 s78,s55,89133056
v_cvt_pk_bf16_f32 v150,v120,v121
v_cvt_pk_bf16_f32 v151,v122,v123
M 132,v[26:29],188
buffer_load_dwordx4 v[220:223],v1,s[36:39],s78 offen offset:0 sc1
M 136,v[24:27],188
buffer_load_dwordx4 v[224:227],v1,s[36:39],s78 offen offset:2048 sc1
v_mfma_f32_16x16x32_bf16 v[140:143],v[26:29],v[176:179],0
s_add_u32 m0,s65,16384
v_cvt_pk_bf16_f32 v152,v124,v125
v_cvt_pk_bf16_f32 v153,v126,v127
v_mfma_f32_16x16x32_bf16 v[144:147],v[24:27],v[176:179],0
D 9,56,0
M 140,v[26:29],192
D 9,56,1024
M 144,v[24:27],192
D 9,56,2048
v_mfma_f32_16x16x32_bf16 v[36:39],v[32:35],v[148:151],0
v_cvt_pk_bf16_f32 v154,v128,v129
v_cvt_pk_bf16_f32 v155,v130,v131
v_mfma_f32_16x16x32_bf16 v[40:43],v[148:151],v[148:151],0
D 9,56,3072
M 36,v[32:35],152
v_cvt_pk_bf16_f32 v156,v132,v133
v_cvt_pk_bf16_f32 v157,v134,v135
v_cvt_pk_bf16_f32 v158,v136,v137
v_cvt_pk_bf16_f32 v159,v138,v139
M 40,v[152:155],152
v_cvt_pk_bf16_f32 v160,v140,v141
v_cvt_pk_bf16_f32 v161,v142,v143
M 36,v[32:35],156
v_cvt_pk_bf16_f32 v162,v144,v145
v_cvt_pk_bf16_f32 v163,v146,v147
M 40,v[156:159],156
v_lshlrev_b32 v86,16,v69
v_and_b32 v87,0xffff0000,v69
M 36,v[32:35],160
v_lshlrev_b32 v88,16,v70
v_and_b32 v89,0xffff0000,v70
M 40,v[160:163],160
v_lshlrev_b32 v90,16,v71
v_and_b32 v91,0xffff0000,v71
v_lshlrev_b32 v92,16,v72
v_and_b32 v93,0xffff0000,v72
v_lshlrev_b32 v94,16,v73
v_and_b32 v95,0xffff0000,v73
v_lshlrev_b32 v96,16,v74
v_and_b32 v97,0xffff0000,v74
v_lshlrev_b32 v98,16,v75
v_and_b32 v99,0xffff0000,v75
v_lshlrev_b32 v100,16,v76
v_and_b32 v101,0xffff0000,v76
v_lshlrev_b32 v102,16,v77
v_and_b32 v103,0xffff0000,v77
v_lshlrev_b32 v104,16,v78
v_and_b32 v105,0xffff0000,v78
v_lshlrev_b32 v106,16,v79
v_and_b32 v107,0xffff0000,v79
v_lshlrev_b32 v108,16,v80
v_and_b32 v109,0xffff0000,v80
v_lshlrev_b32 v110,16,v81
v_and_b32 v111,0xffff0000,v81
v_lshlrev_b32 v112,16,v82
v_and_b32 v113,0xffff0000,v82
v_lshlrev_b32 v114,16,v83
v_and_b32 v115,0xffff0000,v83
v_add_f32_dpp v44,v36,v36 row_ror:8 row_mask:0xf bank_mask:0xf
v_cndmask_b32 v45,v40,v41,s[70:71]
v_cndmask_b32 v46,v42,v43,s[70:71]
ds_write_b32 v2,v44 offset:0
v_cndmask_b32 v45,v45,v46,s[72:73]
ds_write_b32 v0,v45 offset:0
L 0
s_barrier
R 52,3,0
R 56,3,16
R 60,3,32
L 0
v_pk_add_f32 v[52:53],v[52:53],v[54:55]
v_pk_add_f32 v[56:57],v[56:57],v[58:59]
v_pk_add_f32 v[60:61],v[60:61],v[62:63]
v_add_f32 v52,v52,v53
v_pk_add_f32 v[56:57],v[56:57],v[60:61]
v_mul_f32 v52,s68,v52
v_add_f32 v53,v56,v57
v_mul_f32 v53,s68,v53
v_fma_f32 v53,-v52,v52,v53
v_max_f32 v53,0,v53
buffer_load_dword v18,v6,s[36:39],s62 offen offset:0 sc1
v_add_f32 v53,s69,v53
v_rsq_f32 v48,v53
s_nop 0
v_mul_f32_e64 v50,-v52,v48
v_mov_b32 v49,v48
v_mov_b32 v51,v50
v_pk_fma_f32 v[52:53],v[116:117],v[48:49],v[50:51]
v_pk_fma_f32 v[54:55],v[118:119],v[48:49],v[50:51]
v_pk_fma_f32 v[56:57],v[120:121],v[48:49],v[50:51]
v_pk_fma_f32 v[58:59],v[122:123],v[48:49],v[50:51]
v_pk_fma_f32 v[116:117],v[52:53],v[84:85],v[116:117]
v_pk_fma_f32 v[118:119],v[54:55],v[86:87],v[118:119]
v_pk_fma_f32 v[120:121],v[56:57],v[88:89],v[120:121]
v_pk_fma_f32 v[122:123],v[58:59],v[90:91],v[122:123]
v_cvt_pk_bf16_f32 v148,v116,v117
v_cvt_pk_bf16_f32 v149,v118,v119
v_cvt_pk_bf16_f32 v150,v120,v121
v_cvt_pk_bf16_f32 v151,v122,v123
v_pk_fma_f32 v[52:53],v[124:125],v[48:49],v[50:51]
v_pk_fma_f32 v[54:55],v[126:127],v[48:49],v[50:51]
v_pk_fma_f32 v[56:57],v[128:129],v[48:49],v[50:51]
v_pk_fma_f32 v[58:59],v[130:131],v[48:49],v[50:51]
v_pk_fma_f32 v[124:125],v[52:53],v[92:93],v[124:125]
v_pk_fma_f32 v[126:127],v[54:55],v[94:95],v[126:127]
v_pk_fma_f32 v[128:129],v[56:57],v[96:97],v[128:129]
v_pk_fma_f32 v[130:131],v[58:59],v[98:99],v[130:131]
v_cvt_pk_bf16_f32 v152,v124,v125
v_cvt_pk_bf16_f32 v153,v126,v127
v_cvt_pk_bf16_f32 v154,v128,v129
v_cvt_pk_bf16_f32 v155,v130,v131
v_pk_fma_f32 v[52:53],v[132:133],v[48:49],v[50:51]
v_pk_fma_f32 v[54:55],v[134:135],v[48:49],v[50:51]
v_pk_fma_f32 v[56:57],v[136:137],v[48:49],v[50:51]
v_pk_fma_f32 v[58:59],v[138:139],v[48:49],v[50:51]
v_pk_fma_f32 v[132:133],v[52:53],v[100:101],v[132:133]
v_pk_fma_f32 v[134:135],v[54:55],v[102:103],v[134:135]
v_pk_fma_f32 v[136:137],v[56:57],v[104:105],v[136:137]
v_pk_fma_f32 v[138:139],v[58:59],v[106:107],v[138:139]
v_cvt_pk_bf16_f32 v156,v132,v133
v_cvt_pk_bf16_f32 v157,v134,v135
v_cvt_pk_bf16_f32 v158,v136,v137
v_cvt_pk_bf16_f32 v159,v138,v139
v_pk_fma_f32 v[52:53],v[140:141],v[48:49],v[50:51]
v_pk_fma_f32 v[54:55],v[142:143],v[48:49],v[50:51]
v_pk_fma_f32 v[56:57],v[144:145],v[48:49],v[50:51]
v_pk_fma_f32 v[58:59],v[146:147],v[48:49],v[50:51]
v_pk_fma_f32 v[140:141],v[52:53],v[108:109],v[140:141]
v_pk_fma_f32 v[142:143],v[54:55],v[110:111],v[142:143]
v_pk_fma_f32 v[144:145],v[56:57],v[112:113],v[144:145]
v_pk_fma_f32 v[146:147],v[58:59],v[114:115],v[146:147]
v_cvt_pk_bf16_f32 v160,v140,v141
v_cvt_pk_bf16_f32 v161,v142,v143
v_cvt_pk_bf16_f32 v162,v144,v145
v_cvt_pk_bf16_f32 v163,v146,v147
s_waitcnt vmcnt(13)
buffer_store_dword v17,v5,s[36:39],s63 offen offset:0 sc0 sc1
s_mov_b32 s63,0x7fff0000
v_mfma_f32_16x16x32_bf16 v[36:39],v[32:35],v[148:151],0
buffer_store_dwordx4 v[148:151],v1,s[36:39],s57 offen offset:0 sc0 sc1
v_mfma_f32_16x16x32_bf16 v[40:43],v[148:151],v[148:151],0
M 36,v[32:35],152
buffer_store_dwordx4 v[152:155],v1,s[36:39],s57 offen offset:2048 sc0 sc1
M 40,v[152:155],152
M 36,v[32:35],156
buffer_store_dwordx4 v[156:159],v1,s[36:39],s58 offen offset:0 sc0 sc1
M 40,v[156:159],156
M 36,v[32:35],160
buffer_store_dwordx4 v[160:163],v1,s[36:39],s58 offen offset:2048 sc0 sc1
M 40,v[160:163],160
s_nop 5
v_add_f32_dpp v44,v36,v36 row_ror:8 row_mask:0xf bank_mask:0xf
s_nop 0
v_cndmask_b32 v45,v40,v41,s[70:71]
v_cndmask_b32 v46,v42,v43,s[70:71]
ds_write_b32 v2,v44 offset:1024
v_cndmask_b32 v45,v45,v46,s[72:73]
ds_write_b32 v0,v45 offset:1024
L 0
s_barrier
R 52,3,1024
R 56,3,1040
R 60,3,1056
L 0
v_pk_add_f32 v[52:53],v[52:53],v[54:55]
v_pk_add_f32 v[56:57],v[56:57],v[58:59]
v_pk_add_f32 v[60:61],v[60:61],v[62:63]
v_add_f32 v52,v52,v53
v_pk_add_f32 v[56:57],v[56:57],v[60:61]
v_mul_f32 v52,s68,v52
v_add_f32 v53,v56,v57
v_mul_f32 v53,s68,v53
v_fma_f32 v53,-v52,v52,v53
v_max_f32 v53,0,v53
v_add_f32 v54,s69,v53
v_fma_f32 v55,s69,v54,v53
v_rsq_f32 v65,v54
v_rsq_f32 v54,v55
v_mul_f32_e64 v64,-v52,v65
v_mul_f32 v63,v55,v54
v_mul_f32 v61,s76,v54
v_mul_f32 v55,v54,v54
v_mul_f32 v62,s77,v55
v_mul_f32 v62,v62,v61
v_cndmask_b32 v60,v52,v64,s[74:75]
v_cndmask_b32 v61,v61,v65,s[74:75]
buffer_store_dwordx4 v[60:63],v4,s[36:39],s59 offen offset:0 sc0 sc1
s_mov_b32 s63,s60
s_lshr_b32 s78,s45,1
s_and_b32 s79,s45,1
s_lshl_b32 s79,s79,7
s_lshl_b32 s57,s78,15
s_add_u32 s57,s57,s79
s_add_u32 s57,s57,12582912
s_add_u32 s58,s57,4096
s_lshl_b32 s59,s78,9
s_add_u32 s59,s59,s79
s_add_u32 s59,s59,107020288
s_lshl_b32 s60,s45,9
s_add_u32 s60,s60,s66
s_add_u32 s60,s60,131072
s_add_u32 s45,s45,s46
s_lshr_b32 s78,s45,1
s_and_b32 s79,s45,1
s_lshl_b32 s79,s79,7
s_lshl_b32 s55,s78,15
s_add_u32 s55,s55,s79
s_mul_i32 s56,s45,16384
s_add_u32 s56,s56,98566144
s_lshl_b32 s61,s78,11
s_add_u32 s61,s61,110587904
s_add_u32 s78,s45,s46
s_lshr_b32 s78,s78,1
s_lshl_b32 s62,s78,11
s_add_u32 s62,s62,110587904
s_waitcnt vmcnt(6)
s_branch Lr_it1_%=
Lr_tb0_%=:
v_add_f32 v53,s69,v53
v_rsq_f32 v48,v53
s_nop 0
v_mul_f32_e64 v50,-v52,v48
v_mov_b32 v49,v48
v_mov_b32 v51,v50
v_pk_fma_f32 v[52:53],v[116:117],v[48:49],v[50:51]
v_pk_fma_f32 v[54:55],v[118:119],v[48:49],v[50:51]
v_pk_fma_f32 v[56:57],v[120:121],v[48:49],v[50:51]
v_pk_fma_f32 v[58:59],v[122:123],v[48:49],v[50:51]
s_add_u32 s78,s55,71303168
buffer_load_dwordx4 v[196:199],v1,s[36:39],s78 offen offset:0 sc1
buffer_load_dwordx4 v[200:203],v1,s[36:39],s78 offen offset:2048 sc1
s_add_u32 s78,s55,71307264
buffer_load_dwordx4 v[204:207],v1,s[36:39],s78 offen offset:0 sc1
buffer_load_dwordx4 v[208:211],v1,s[36:39],s78 offen offset:2048 sc1
v_pk_fma_f32 v[116:117],v[52:53],v[84:85],v[116:117]
v_pk_fma_f32 v[118:119],v[54:55],v[86:87],v[118:119]
v_pk_fma_f32 v[120:121],v[56:57],v[88:89],v[120:121]
v_pk_fma_f32 v[122:123],v[58:59],v[90:91],v[122:123]
v_cvt_pk_bf16_f32 v148,v116,v117
v_cvt_pk_bf16_f32 v149,v118,v119
v_cvt_pk_bf16_f32 v150,v120,v121
v_cvt_pk_bf16_f32 v151,v122,v123
v_pk_fma_f32 v[52:53],v[124:125],v[48:49],v[50:51]
v_pk_fma_f32 v[54:55],v[126:127],v[48:49],v[50:51]
v_pk_fma_f32 v[56:57],v[128:129],v[48:49],v[50:51]
v_pk_fma_f32 v[58:59],v[130:131],v[48:49],v[50:51]
s_add_u32 s78,s55,89128960
buffer_load_dwordx4 v[212:215],v1,s[36:39],s78 offen offset:0 sc1
buffer_load_dwordx4 v[216:219],v1,s[36:39],s78 offen offset:2048 sc1
s_add_u32 s78,s55,89133056
buffer_load_dwordx4 v[220:223],v1,s[36:39],s78 offen offset:0 sc1
buffer_load_dwordx4 v[224:227],v1,s[36:39],s78 offen offset:2048 sc1
v_pk_fma_f32 v[124:125],v[52:53],v[92:93],v[124:125]
v_pk_fma_f32 v[126:127],v[54:55],v[94:95],v[126:127]
v_pk_fma_f32 v[128:129],v[56:57],v[96:97],v[128:129]
v_pk_fma_f32 v[130:131],v[58:59],v[98:99],v[130:131]
v_cvt_pk_bf16_f32 v152,v124,v125
v_cvt_pk_bf16_f32 v153,v126,v127
v_cvt_pk_bf16_f32 v154,v128,v129
v_cvt_pk_bf16_f32 v155,v130,v131
v_pk_fma_f32 v[52:53],v[132:133],v[48:49],v[50:51]
v_pk_fma_f32 v[54:55],v[134:135],v[48:49],v[50:51]
v_pk_fma_f32 v[56:57],v[136:137],v[48:49],v[50:51]
v_pk_fma_f32 v[58:59],v[138:139],v[48:49],v[50:51]
s_add_u32 m0,s65,16384
s_nop 0
D 9,56,0
D 9,56,1024
D 9,56,2048
D 9,56,3072
v_pk_fma_f32 v[132:133],v[52:53],v[100:101],v[132:133]
v_pk_fma_f32 v[134:135],v[54:55],v[102:103],v[134:135]
v_pk_fma_f32 v[136:137],v[56:57],v[104:105],v[136:137]
v_pk_fma_f32 v[138:139],v[58:59],v[106:107],v[138:139]
v_cvt_pk_bf16_f32 v156,v132,v133
v_cvt_pk_bf16_f32 v157,v134,v135
v_cvt_pk_bf16_f32 v158,v136,v137
v_cvt_pk_bf16_f32 v159,v138,v139
v_pk_fma_f32 v[52:53],v[140:141],v[48:49],v[50:51]
v_pk_fma_f32 v[54:55],v[142:143],v[48:49],v[50:51]
v_pk_fma_f32 v[56:57],v[144:145],v[48:49],v[50:51]
v_pk_fma_f32 v[58:59],v[146:147],v[48:49],v[50:51]
v_pk_fma_f32 v[140:141],v[52:53],v[108:109],v[140:141]
v_pk_fma_f32 v[142:143],v[54:55],v[110:111],v[142:143]
v_pk_fma_f32 v[144:145],v[56:57],v[112:113],v[144:145]
v_pk_fma_f32 v[146:147],v[58:59],v[114:115],v[146:147]
v_cvt_pk_bf16_f32 v160,v140,v141
v_cvt_pk_bf16_f32 v161,v142,v143
v_cvt_pk_bf16_f32 v162,v144,v145
v_cvt_pk_bf16_f32 v163,v146,v147
v_mfma_f32_16x16x32_bf16 v[36:39],v[32:35],v[148:151],0
buffer_load_dword v18,v6,s[36:39],s62 offen offset:0 sc1
buffer_store_dwordx4 v[148:151],v1,s[36:39],s57 offen offset:0 sc0 sc1
v_mfma_f32_16x16x32_bf16 v[40:43],v[148:151],v[148:151],0
M 36,v[32:35],152
buffer_store_dwordx4 v[152:155],v1,s[36:39],s57 offen offset:2048 sc0 sc1
M 40,v[152:155],152
M 36,v[32:35],156
buffer_store_dwordx4 v[156:159],v1,s[36:39],s58 offen offset:0 sc0 sc1
M 40,v[156:159],156
M 36,v[32:35],160
buffer_store_dwordx4 v[160:163],v1,s[36:39],s58 offen offset:2048 sc0 sc1
M 40,v[160:163],160
s_nop 5
v_add_f32_dpp v44,v36,v36 row_ror:8 row_mask:0xf bank_mask:0xf
s_nop 0
v_cndmask_b32 v45,v40,v41,s[70:71]
v_cndmask_b32 v46,v42,v43,s[70:71]
ds_write_b32 v2,v44 offset:1024
v_cndmask_b32 v45,v45,v46,s[72:73]
ds_write_b32 v0,v45 offset:1024
L 0
s_barrier
R 52,3,1024
R 56,3,1040
R 60,3,1056
L 0
v_pk_add_f32 v[52:53],v[52:53],v[54:55]
v_pk_add_f32 v[56:57],v[56:57],v[58:59]
v_pk_add_f32 v[60:61],v[60:61],v[62:63]
v_add_f32 v52,v52,v53
v_pk_add_f32 v[56:57],v[56:57],v[60:61]
v_mul_f32 v52,s68,v52
v_add_f32 v53,v56,v57
v_mul_f32 v53,s68,v53
v_fma_f32 v53,-v52,v52,v53
v_max_f32 v53,0,v53
v_add_f32 v54,s69,v53
v_fma_f32 v55,s69,v54,v53
v_rsq_f32 v65,v54
v_rsq_f32 v54,v55
v_mul_f32_e64 v64,-v52,v65
v_mul_f32 v63,v55,v54
v_mul_f32 v61,s76,v54
v_mul_f32 v55,v54,v54
v_mul_f32 v62,s77,v55
v_mul_f32 v62,v62,v61
v_cndmask_b32 v60,v52,v64,s[74:75]
v_cndmask_b32 v61,v61,v65,s[74:75]
buffer_store_dwordx4 v[60:63],v4,s[36:39],s59 offen offset:0 sc0 sc1
s_mov_b32 s63,s60
s_lshr_b32 s78,s45,1
s_and_b32 s79,s45,1
s_lshl_b32 s79,s79,7
s_lshl_b32 s57,s78,15
s_add_u32 s57,s57,s79
s_add_u32 s57,s57,12582912
s_add_u32 s58,s57,4096
s_lshl_b32 s59,s78,9
s_add_u32 s59,s59,s79
s_add_u32 s59,s59,107020288
s_lshl_b32 s60,s45,9
s_add_u32 s60,s60,s66
s_add_u32 s60,s60,131072
s_add_u32 s45,s45,s46
s_lshr_b32 s78,s45,1
s_and_b32 s79,s45,1
s_lshl_b32 s79,s79,7
s_lshl_b32 s55,s78,15
s_add_u32 s55,s55,s79
s_mul_i32 s56,s45,16384
s_add_u32 s56,s56,98566144
s_lshl_b32 s61,s78,11
s_add_u32 s61,s61,110587904
s_add_u32 s78,s45,s46
s_lshr_b32 s78,s78,1
s_lshl_b32 s62,s78,11
s_add_u32 s62,s62,110587904
s_waitcnt vmcnt(5)
s_branch Lr_it1_%=
Lr_tc0_%=:
s_add_u32 s78,s55,71303168
buffer_load_dwordx4 v[196:199],v1,s[36:39],s78 offen offset:0 sc1
buffer_load_dwordx4 v[200:203],v1,s[36:39],s78 offen offset:2048 sc1
s_add_u32 s78,s55,71307264
buffer_load_dwordx4 v[204:207],v1,s[36:39],s78 offen offset:0 sc1
buffer_load_dwordx4 v[208:211],v1,s[36:39],s78 offen offset:2048 sc1
s_add_u32 s78,s55,89128960
buffer_load_dwordx4 v[212:215],v1,s[36:39],s78 offen offset:0 sc1
buffer_load_dwordx4 v[216:219],v1,s[36:39],s78 offen offset:2048 sc1
s_add_u32 s78,s55,89133056
buffer_load_dwordx4 v[220:223],v1,s[36:39],s78 offen offset:0 sc1
buffer_load_dwordx4 v[224:227],v1,s[36:39],s78 offen offset:2048 sc1
s_add_u32 m0,s65,16384
s_nop 0
D 9,56,0
D 9,56,1024
D 9,56,2048
D 9,56,3072
buffer_load_dword v18,v6,s[36:39],s62 offen offset:0 sc1
s_waitcnt vmcnt(13)
buffer_store_dword v17,v5,s[36:39],s63 offen offset:0 sc0 sc1
s_mov_b32 s63,0x7fff0000
s_lshr_b32 s78,s45,1
s_and_b32 s79,s45,1
s_lshl_b32 s79,s79,7
s_lshl_b32 s57,s78,15
s_add_u32 s57,s57,s79
s_add_u32 s57,s57,12582912
s_add_u32 s58,s57,4096
s_lshl_b32 s59,s78,9
s_add_u32 s59,s59,s79
s_add_u32 s59,s59,107020288
s_lshl_b32 s60,s45,9
s_add_u32 s60,s60,s66
s_add_u32 s60,s60,131072
s_add_u32 s45,s45,s46
s_lshr_b32 s78,s45,1
s_and_b32 s79,s45,1
s_lshl_b32 s79,s79,7
s_lshl_b32 s55,s78,15
s_add_u32 s55,s55,s79
s_mul_i32 s56,s45,16384
s_add_u32 s56,s56,98566144
s_lshl_b32 s61,s78,11
s_add_u32 s61,s61,110587904
s_add_u32 s78,s45,s46
s_lshr_b32 s78,s78,1
s_lshl_b32 s62,s78,11
s_add_u32 s62,s62,110587904
s_waitcnt vmcnt(1)
s_branch Lr_it1_%=
Lr_it1_%=:
T v[68:69],10,16384
T v[70:71],10,16448
T v[72:73],10,17408
T v[74:75],10,17472
T v[76:77],10,18432
T v[78:79],10,18496
T v[80:81],10,19456
T v[82:83],10,19520
s_nop 0
v_cmp_eq_u32 vcc,s53,v18
s_nop 0
s_cmp_eq_u64 vcc,-1
s_cselect_b32 s78,s45,256
s_cmp_lt_u32 s78,256
s_cbranch_scc1 Lr_ta1_%=
buffer_load_dword v18,v6,s[36:39],s61 offen offset:0 sc1
v_mfma_f32_16x16x32_bf16 v[116:119],v[26:29],v[196:199],0
L 6
v_lshlrev_b32 v84,16,v68
v_and_b32 v85,0xffff0000,v68
v_mfma_f32_16x16x32_bf16 v[120:123],v[24:27],v[196:199],0
L 4
v_lshlrev_b32 v86,16,v69
v_and_b32 v87,0xffff0000,v69
M 116,v[26:29],212
L 2
v_lshlrev_b32 v88,16,v70
v_and_b32 v89,0xffff0000,v70
M 120,v[24:27],212
L 0
v_lshlrev_b32 v90,16,v71
v_and_b32 v91,0xffff0000,v71
v_mfma_f32_16x16x32_bf16 v[124:127],v[26:29],v[200:203],0
v_lshlrev_b32 v92,16,v72
v_and_b32 v93,0xffff0000,v72
v_mfma_f32_16x16x32_bf16 v[128:131],v[24:27],v[200:203],0
v_lshlrev_b32 v94,16,v73
v_and_b32 v95,0xffff0000,v73
M 124,v[26:29],216
v_cvt_pk_bf16_f32 v148,v116,v117
v_cvt_pk_bf16_f32 v149,v118,v119
M 128,v[24:27],216
v_cvt_pk_bf16_f32 v150,v120,v121
v_cvt_pk_bf16_f32 v151,v122,v123
)"
R"(v_mfma_f32_16x16x32_bf16 v[132:135],v[26:29],v[204:207],0
v_lshlrev_b32 v96,16,v74
v_and_b32 v97,0xffff0000,v74
v_mfma_f32_16x16x32_bf16 v[136:139],v[24:27],v[204:207],0
v_lshlrev_b32 v98,16,v75
v_and_b32 v99,0xffff0000,v75
M 132,v[26:29],220
v_cvt_pk_bf16_f32 v152,v124,v125
v_cvt_pk_bf16_f32 v153,v126,v127
M 136,v[24:27],220
v_cvt_pk_bf16_f32 v154,v128,v129
v_cvt_pk_bf16_f32 v155,v130,v131
v_mfma_f32_16x16x32_bf16 v[140:143],v[26:29],v[208:211],0
v_lshlrev_b32 v100,16,v76
v_and_b32 v101,0xffff0000,v76
v_mfma_f32_16x16x32_bf16 v[144:147],v[24:27],v[208:211],0
v_lshlrev_b32 v102,16,v77
v_and_b32 v103,0xffff0000,v77
M 140,v[26:29],224
v_cvt_pk_bf16_f32 v156,v132,v133
v_cvt_pk_bf16_f32 v157,v134,v135
M 144,v[24:27],224
v_cvt_pk_bf16_f32 v158,v136,v137
v_cvt_pk_bf16_f32 v159,v138,v139
v_mfma_f32_16x16x32_bf16 v[36:39],v[32:35],v[148:151],0
v_lshlrev_b32 v104,16,v78
v_and_b32 v105,0xffff0000,v78
v_mfma_f32_16x16x32_bf16 v[40:43],v[148:151],v[148:151],0
v_lshlrev_b32 v106,16,v79
v_and_b32 v107,0xffff0000,v79
M 36,v[32:35],152
v_cvt_pk_bf16_f32 v160,v140,v141
v_cvt_pk_bf16_f32 v161,v142,v143
M 40,v[152:155],152
v_cvt_pk_bf16_f32 v162,v144,v145
v_cvt_pk_bf16_f32 v163,v146,v147
M 36,v[32:35],156
v_lshlrev_b32 v108,16,v80
v_and_b32 v109,0xffff0000,v80
M 40,v[156:159],156
v_lshlrev_b32 v110,16,v81
v_and_b32 v111,0xffff0000,v81
M 36,v[32:35],160
v_lshlrev_b32 v112,16,v82
v_and_b32 v113,0xffff0000,v82
M 40,v[160:163],160
v_lshlrev_b32 v114,16,v83
v_and_b32 v115,0xffff0000,v83
s_nop 2
v_add_f32_dpp v44,v36,v36 row_ror:8 row_mask:0xf bank_mask:0xf
s_nop 1
v_cndmask_b32 v45,v40,v41,s[70:71]
v_cndmask_b32 v46,v42,v43,s[70:71]
ds_write_b32 v2,v44 offset:0
v_cndmask_b32 v45,v45,v46,s[72:73]
ds_write_b32 v0,v45 offset:0
L 0
s_barrier
R 52,3,0
R 56,3,16
R 60,3,32
L 0
v_pk_add_f32 v[52:53],v[52:53],v[54:55]
v_pk_add_f32 v[56:57],v[56:57],v[58:59]
v_pk_add_f32 v[60:61],v[60:61],v[62:63]
v_add_f32 v52,v52,v53
v_pk_add_f32 v[56:57],v[56:57],v[60:61]
v_mul_f32 v52,s68,v52
v_add_f32 v53,v56,v57
v_mul_f32 v53,s68,v53
v_fma_f32 v53,-v52,v52,v53
v_max_f32 v53,0,v53
s_waitcnt vmcnt(1)
buffer_store_dword v17,v5,s[36:39],s63 offen offset:0 sc0 sc1
s_mov_b32 s63,0x7fff0000
s_waitcnt vmcnt(1)
v_cmp_eq_u32 vcc,s53,v18
s_nop 0
s_cmp_eq_u64 vcc,-1
s_cselect_b32 s78,s45,256
s_cmp_lt_u32 s78,256
s_cbranch_scc1 Lr_tb1_%=
buffer_load_dword v18,v6,s[36:39],s61 offen offset:0 sc1
v_add_f32 v53,s69,v53
v_rsq_f32 v48,v53
s_nop 0
v_mul_f32_e64 v50,-v52,v48
v_mov_b32 v49,v48
v_mov_b32 v51,v50
v_pk_fma_f32 v[52:53],v[116:117],v[48:49],v[50:51]
v_pk_fma_f32 v[54:55],v[118:119],v[48:49],v[50:51]
v_pk_fma_f32 v[56:57],v[120:121],v[48:49],v[50:51]
v_pk_fma_f32 v[58:59],v[122:123],v[48:49],v[50:51]
v_pk_fma_f32 v[116:117],v[52:53],v[84:85],v[116:117]
v_pk_fma_f32 v[118:119],v[54:55],v[86:87],v[118:119]
v_pk_fma_f32 v[120:121],v[56:57],v[88:89],v[120:121]
v_pk_fma_f32 v[122:123],v[58:59],v[90:91],v[122:123]
v_cvt_pk_bf16_f32 v148,v116,v117
v_cvt_pk_bf16_f32 v149,v118,v119
v_cvt_pk_bf16_f32 v150,v120,v121
v_cvt_pk_bf16_f32 v151,v122,v123
v_pk_fma_f32 v[52:53],v[124:125],v[48:49],v[50:51]
v_pk_fma_f32 v[54:55],v[126:127],v[48:49],v[50:51]
v_pk_fma_f32 v[56:57],v[128:129],v[48:49],v[50:51]
v_pk_fma_f32 v[58:59],v[130:131],v[48:49],v[50:51]
v_pk_fma_f32 v[124:125],v[52:53],v[92:93],v[124:125]
v_pk_fma_f32 v[126:127],v[54:55],v[94:95],v[126:127]
v_pk_fma_f32 v[128:129],v[56:57],v[96:97],v[128:129]
v_pk_fma_f32 v[130:131],v[58:59],v[98:99],v[130:131]
v_cvt_pk_bf16_f32 v152,v124,v125
v_cvt_pk_bf16_f32 v153,v126,v127
v_cvt_pk_bf16_f32 v154,v128,v129
v_cvt_pk_bf16_f32 v155,v130,v131
v_pk_fma_f32 v[52:53],v[132:133],v[48:49],v[50:51]
v_pk_fma_f32 v[54:55],v[134:135],v[48:49],v[50:51]
v_pk_fma_f32 v[56:57],v[136:137],v[48:49],v[50:51]
v_pk_fma_f32 v[58:59],v[138:139],v[48:49],v[50:51]
v_pk_fma_f32 v[132:133],v[52:53],v[100:101],v[132:133]
v_pk_fma_f32 v[134:135],v[54:55],v[102:103],v[134:135]
v_pk_fma_f32 v[136:137],v[56:57],v[104:105],v[136:137]
v_pk_fma_f32 v[138:139],v[58:59],v[106:107],v[138:139]
v_cvt_pk_bf16_f32 v156,v132,v133
v_cvt_pk_bf16_f32 v157,v134,v135
v_cvt_pk_bf16_f32 v158,v136,v137
v_cvt_pk_bf16_f32 v159,v138,v139
v_pk_fma_f32 v[52:53],v[140:141],v[48:49],v[50:51]
v_pk_fma_f32 v[54:55],v[142:143],v[48:49],v[50:51]
v_pk_fma_f32 v[56:57],v[144:145],v[48:49],v[50:51]
v_pk_fma_f32 v[58:59],v[146:147],v[48:49],v[50:51]
v_pk_fma_f32 v[140:141],v[52:53],v[108:109],v[140:141]
v_pk_fma_f32 v[142:143],v[54:55],v[110:111],v[142:143]
v_pk_fma_f32 v[144:145],v[56:57],v[112:113],v[144:145]
v_pk_fma_f32 v[146:147],v[58:59],v[114:115],v[146:147]
v_cvt_pk_bf16_f32 v160,v140,v141
v_cvt_pk_bf16_f32 v161,v142,v143
v_cvt_pk_bf16_f32 v162,v144,v145
v_cvt_pk_bf16_f32 v163,v146,v147
v_mfma_f32_16x16x32_bf16 v[36:39],v[32:35],v[148:151],0
buffer_store_dwordx4 v[148:151],v1,s[36:39],s57 offen offset:0 sc0 sc1
v_mfma_f32_16x16x32_bf16 v[40:43],v[148:151],v[148:151],0
M 36,v[32:35],152
buffer_store_dwordx4 v[152:155],v1,s[36:39],s57 offen offset:2048 sc0 sc1
M 40,v[152:155],152
M 36,v[32:35],156
buffer_store_dwordx4 v[156:159],v1,s[36:39],s58 offen offset:0 sc0 sc1
M 40,v[156:159],156
M 36,v[32:35],160
buffer_store_dwordx4 v[160:163],v1,s[36:39],s58 offen offset:2048 sc0 sc1
M 40,v[160:163],160
s_nop 5
v_add_f32_dpp v44,v36,v36 row_ror:8 row_mask:0xf bank_mask:0xf
s_nop 0
v_cndmask_b32 v45,v40,v41,s[70:71]
v_cndmask_b32 v46,v42,v43,s[70:71]
ds_write_b32 v2,v44 offset:1024
v_cndmask_b32 v45,v45,v46,s[72:73]
ds_write_b32 v0,v45 offset:1024
L 0
s_barrier
R 52,3,1024
R 56,3,1040
R 60,3,1056
L 0
v_pk_add_f32 v[52:53],v[52:53],v[54:55]
v_pk_add_f32 v[56:57],v[56:57],v[58:59]
v_pk_add_f32 v[60:61],v[60:61],v[62:63]
v_add_f32 v52,v52,v53
v_pk_add_f32 v[56:57],v[56:57],v[60:61]
v_mul_f32 v52,s68,v52
v_add_f32 v53,v56,v57
v_mul_f32 v53,s68,v53
v_fma_f32 v53,-v52,v52,v53
v_max_f32 v53,0,v53
v_add_f32 v54,s69,v53
v_fma_f32 v55,s69,v54,v53
v_rsq_f32 v65,v54
v_rsq_f32 v54,v55
v_mul_f32_e64 v64,-v52,v65
v_mul_f32 v63,v55,v54
v_mul_f32 v61,s76,v54
v_mul_f32 v55,v54,v54
v_mul_f32 v62,s77,v55
v_mul_f32 v62,v62,v61
v_cndmask_b32 v60,v52,v64,s[74:75]
v_cndmask_b32 v61,v61,v65,s[74:75]
buffer_store_dwordx4 v[60:63],v4,s[36:39],s59 offen offset:0 sc0 sc1
s_mov_b32 s63,s60
s_waitcnt vmcnt(5)
v_cmp_eq_u32 vcc,s53,v18
s_nop 0
s_cmp_eq_u64 vcc,-1
s_cselect_b32 s78,s45,256
s_cmp_lt_u32 s78,256
s_cbranch_scc1 Lr_tc1_%=
s_waitcnt vmcnt(0)
buffer_store_dword v17,v5,s[36:39],s63 offen offset:0 sc0 sc1
s_mov_b32 s63,0x7fff0000
s_cmp_ge_u32 s45,256
s_cbranch_scc1 Lr_done_%=
Lr_bp1_%=:
buffer_load_dword v18,v6,s[36:39],s61 offen offset:0 sc1
s_add_u32 s82,s82,1
s_waitcnt vmcnt(0)
v_cmp_eq_u32 vcc,s53,v18
s_nop 0
s_cmp_eq_u64 vcc,-1
s_cbranch_scc1 Lr_bq1_%=
s_sleep 2
s_cmp_lt_u32 s82,20000
s_cbranch_scc1 Lr_bp1_%=
Lr_bq1_%=:
s_add_u32 s78,s55,71303168
buffer_load_dwordx4 v[164:167],v1,s[36:39],s78 offen offset:0 sc1
buffer_load_dwordx4 v[168:171],v1,s[36:39],s78 offen offset:2048 sc1
s_add_u32 s78,s55,71307264
buffer_load_dwordx4 v[172:175],v1,s[36:39],s78 offen offset:0 sc1
buffer_load_dwordx4 v[176:179],v1,s[36:39],s78 offen offset:2048 sc1
s_add_u32 s78,s55,89128960
buffer_load_dwordx4 v[180:183],v1,s[36:39],s78 offen offset:0 sc1
buffer_load_dwordx4 v[184:187],v1,s[36:39],s78 offen offset:2048 sc1
s_add_u32 s78,s55,89133056
buffer_load_dwordx4 v[188:191],v1,s[36:39],s78 offen offset:0 sc1
buffer_load_dwordx4 v[192:195],v1,s[36:39],s78 offen offset:2048 sc1
s_add_u32 m0,s65,0
s_nop 0
D 9,56,0
D 9,56,1024
D 9,56,2048
D 9,56,3072
buffer_load_dword v18,v6,s[36:39],s62 offen offset:0 sc1
s_lshr_b32 s78,s45,1
s_and_b32 s79,s45,1
s_lshl_b32 s79,s79,7
s_lshl_b32 s57,s78,15
s_add_u32 s57,s57,s79
s_add_u32 s57,s57,12582912
s_add_u32 s58,s57,4096
s_lshl_b32 s59,s78,9
s_add_u32 s59,s59,s79
s_add_u32 s59,s59,107020288
s_lshl_b32 s60,s45,9
s_add_u32 s60,s60,s66
s_add_u32 s60,s60,131072
s_add_u32 s45,s45,s46
s_lshr_b32 s78,s45,1
s_and_b32 s79,s45,1
s_lshl_b32 s79,s79,7
s_lshl_b32 s55,s78,15
s_add_u32 s55,s55,s79
s_mul_i32 s56,s45,16384
s_add_u32 s56,s56,98566144
s_lshl_b32 s61,s78,11
s_add_u32 s61,s61,110587904
s_add_u32 s78,s45,s46
s_lshr_b32 s78,s78,1
s_lshl_b32 s62,s78,11
s_add_u32 s62,s62,110587904
s_waitcnt vmcnt(0)
s_branch Lr_it0_%=
Lr_ta1_%=:
v_mfma_f32_16x16x32_bf16 v[116:119],v[26:29],v[196:199],0
s_add_u32 s78,s55,71303168
L 6
L 4
v_mfma_f32_16x16x32_bf16 v[120:123],v[24:27],v[196:199],0
L 2
buffer_load_dwordx4 v[164:167],v1,s[36:39],s78 offen offset:0 sc1
M 116,v[26:29],212
L 0
buffer_load_dwordx4 v[168:171],v1,s[36:39],s78 offen offset:2048 sc1
M 120,v[24:27],212
s_add_u32 s78,s55,71307264
v_lshlrev_b32 v84,16,v68
v_and_b32 v85,0xffff0000,v68
v_mfma_f32_16x16x32_bf16 v[124:127],v[26:29],v[200:203],0
buffer_load_dwordx4 v[172:175],v1,s[36:39],s78 offen offset:0 sc1
v_mfma_f32_16x16x32_bf16 v[128:131],v[24:27],v[200:203],0
buffer_load_dwordx4 v[176:179],v1,s[36:39],s78 offen offset:2048 sc1
M 124,v[26:29],216
s_add_u32 s78,s55,89128960
v_cvt_pk_bf16_f32 v148,v116,v117
v_cvt_pk_bf16_f32 v149,v118,v119
M 128,v[24:27],216
buffer_load_dwordx4 v[180:183],v1,s[36:39],s78 offen offset:0 sc1
v_mfma_f32_16x16x32_bf16 v[132:135],v[26:29],v[204:207],0
buffer_load_dwordx4 v[184:187],v1,s[36:39],s78 offen offset:2048 sc1
v_mfma_f32_16x16x32_bf16 v[136:139],v[24:27],v[204:207],0
s_add_u32 s78,s55,89133056
v_cvt_pk_bf16_f32 v150,v120,v121
v_cvt_pk_bf16_f32 v151,v122,v123
M 132,v[26:29],220
buffer_load_dwordx4 v[188:191],v1,s[36:39],s78 offen offset:0 sc1
M 136,v[24:27],220
buffer_load_dwordx4 v[192:195],v1,s[36:39],s78 offen offset:2048 sc1
v_mfma_f32_16x16x32_bf16 v[140:143],v[26:29],v[208:211],0
s_add_u32 m0,s65,0
v_cvt_pk_bf16_f32 v152,v124,v125
v_cvt_pk_bf16_f32 v153,v126,v127
v_mfma_f32_16x16x32_bf16 v[144:147],v[24:27],v[208:211],0
D 9,56,0
M 140,v[26:29],224
D 9,56,1024
M 144,v[24:27],224
D 9,56,2048
v_mfma_f32_16x16x32_bf16 v[36:39],v[32:35],v[148:151],0
v_cvt_pk_bf16_f32 v154,v128,v129
v_cvt_pk_bf16_f32 v155,v130,v131
v_mfma_f32_16x16x32_bf16 v[40:43],v[148:151],v[148:151],0
D 9,56,3072
M 36,v[32:35],152
v_cvt_pk_bf16_f32 v156,v132,v133
v_cvt_pk_bf16_f32 v157,v134,v135
v_cvt_pk_bf16_f32 v158,v136,v137
v_cvt_pk_bf16_f32 v159,v138,v139
M 40,v[152:155],152
v_cvt_pk_bf16_f32 v160,v140,v141
v_cvt_pk_bf16_f32 v161,v142,v143
M 36,v[32:35],156
v_cvt_pk_bf16_f32 v162,v144,v145
v_cvt_pk_bf16_f32 v163,v146,v147
M 40,v[156:159],156
v_lshlrev_b32 v86,16,v69
v_and_b32 v87,0xffff0000,v69
M 36,v[32:35],160
v_lshlrev_b32 v88,16,v70
v_and_b32 v89,0xffff0000,v70
M 40,v[160:163],160
v_lshlrev_b32 v90,16,v71
v_and_b32 v91,0xffff0000,v71
v_lshlrev_b32 v92,16,v72
v_and_b32 v93,0xffff0000,v72
v_lshlrev_b32 v94,16,v73
v_and_b32 v95,0xffff0000,v73
v_lshlrev_b32 v96,16,v74
v_and_b32 v97,0xffff0000,v74
v_lshlrev_b32 v98,16,v75
v_and_b32 v99,0xffff0000,v75
v_lshlrev_b32 v100,16,v76
v_and_b32 v101,0xffff0000,v76
v_lshlrev_b32 v102,16,v77
v_and_b32 v103,0xffff0000,v77
v_lshlrev_b32 v104,16,v78
v_and_b32 v105,0xffff0000,v78
v_lshlrev_b32 v106,16,v79
v_and_b32 v107,0xffff0000,v79
v_lshlrev_b32 v108,16,v80
v_and_b32 v109,0xffff0000,v80
v_lshlrev_b32 v110,16,v81
v_and_b32 v111,0xffff0000,v81
v_lshlrev_b32 v112,16,v82
v_and_b32 v113,0xffff0000,v82
v_lshlrev_b32 v114,16,v83
v_and_b32 v115,0xffff0000,v83
v_add_f32_dpp v44,v36,v36 row_ror:8 row_mask:0xf bank_mask:0xf
v_cndmask_b32 v45,v40,v41,s[70:71]
v_cndmask_b32 v46,v42,v43,s[70:71]
ds_write_b32 v2,v44 offset:0
v_cndmask_b32 v45,v45,v46,s[72:73]
ds_write_b32 v0,v45 offset:0
L 0
s_barrier
R 52,3,0
R 56,3,16
R 60,3,32
L 0
v_pk_add_f32 v[52:53],v[52:53],v[54:55]
v_pk_add_f32 v[56:57],v[56:57],v[58:59]
v_pk_add_f32 v[60:61],v[60:61],v[62:63]
v_add_f32 v52,v52,v53
v_pk_add_f32 v[56:57],v[56:57],v[60:61]
v_mul_f32 v52,s68,v52
v_add_f32 v53,v56,v57
v_mul_f32 v53,s68,v53
v_fma_f32 v53,-v52,v52,v53
v_max_f32 v53,0,v53
buffer_load_dword v18,v6,s[36:39],s62 offen offset:0 sc1
v_add_f32 v53,s69,v53
v_rsq_f32 v48,v53
s_nop 0
v_mul_f32_e64 v50,-v52,v48
v_mov_b32 v49,v48
v_mov_b32 v51,v50
v_pk_fma_f32 v[52:53],v[116:117],v[48:49],v[50:51]
v_pk_fma_f32 v[54:55],v[118:119],v[48:49],v[50:51]
v_pk_fma_f32 v[56:57],v[120:121],v[48:49],v[50:51]
v_pk_fma_f32 v[58:59],v[122:123],v[48:49],v[50:51]
v_pk_fma_f32 v[116:117],v[52:53],v[84:85],v[116:117]
v_pk_fma_f32 v[118:119],v[54:55],v[86:87],v[118:119]
v_pk_fma_f32 v[120:121],v[56:57],v[88:89],v[120:121]
v_pk_fma_f32 v[122:123],v[58:59],v[90:91],v[122:123]
v_cvt_pk_bf16_f32 v148,v116,v117
v_cvt_pk_bf16_f32 v149,v118,v119
v_cvt_pk_bf16_f32 v150,v120,v121
v_cvt_pk_bf16_f32 v151,v122,v123
v_pk_fma_f32 v[52:53],v[124:125],v[48:49],v[50:51]
v_pk_fma_f32 v[54:55],v[126:127],v[48:49],v[50:51]
v_pk_fma_f32 v[56:57],v[128:129],v[48:49],v[50:51]
v_pk_fma_f32 v[58:59],v[130:131],v[48:49],v[50:51]
v_pk_fma_f32 v[124:125],v[52:53],v[92:93],v[124:125]
v_pk_fma_f32 v[126:127],v[54:55],v[94:95],v[126:127]
v_pk_fma_f32 v[128:129],v[56:57],v[96:97],v[128:129]
v_pk_fma_f32 v[130:131],v[58:59],v[98:99],v[130:131]
v_cvt_pk_bf16_f32 v152,v124,v125
v_cvt_pk_bf16_f32 v153,v126,v127
v_cvt_pk_bf16_f32 v154,v128,v129
v_cvt_pk_bf16_f32 v155,v130,v131
v_pk_fma_f32 v[52:53],v[132:133],v[48:49],v[50:51]
v_pk_fma_f32 v[54:55],v[134:135],v[48:49],v[50:51]
v_pk_fma_f32 v[56:57],v[136:137],v[48:49],v[50:51]
v_pk_fma_f32 v[58:59],v[138:139],v[48:49],v[50:51]
v_pk_fma_f32 v[132:133],v[52:53],v[100:101],v[132:133]
v_pk_fma_f32 v[134:135],v[54:55],v[102:103],v[134:135]
v_pk_fma_f32 v[136:137],v[56:57],v[104:105],v[136:137]
v_pk_fma_f32 v[138:139],v[58:59],v[106:107],v[138:139]
v_cvt_pk_bf16_f32 v156,v132,v133
v_cvt_pk_bf16_f32 v157,v134,v135
v_cvt_pk_bf16_f32 v158,v136,v137
v_cvt_pk_bf16_f32 v159,v138,v139
v_pk_fma_f32 v[52:53],v[140:141],v[48:49],v[50:51]
v_pk_fma_f32 v[54:55],v[142:143],v[48:49],v[50:51]
v_pk_fma_f32 v[56:57],v[144:145],v[48:49],v[50:51]
v_pk_fma_f32 v[58:59],v[146:147],v[48:49],v[50:51]
v_pk_fma_f32 v[140:141],v[52:53],v[108:109],v[140:141]
v_pk_fma_f32 v[142:143],v[54:55],v[110:111],v[142:143]
v_pk_fma_f32 v[144:145],v[56:57],v[112:113],v[144:145]
v_pk_fma_f32 v[146:147],v[58:59],v[114:115],v[146:147]
v_cvt_pk_bf16_f32 v160,v140,v141
v_cvt_pk_bf16_f32 v161,v142,v143
v_cvt_pk_bf16_f32 v162,v144,v145
v_cvt_pk_bf16_f32 v163,v146,v147
s_waitcnt vmcnt(13)
buffer_store_dword v17,v5,s[36:39],s63 offen offset:0 sc0 sc1
s_mov_b32 s63,0x7fff0000
v_mfma_f32_16x16x32_bf16 v[36:39],v[32:35],v[148:151],0
buffer_store_dwordx4 v[148:151],v1,s[36:39],s57 offen offset:0 sc0 sc1
v_mfma_f32_16x16x32_bf16 v[40:43],v[148:151],v[148:151],0
M 36,v[32:35],152
buffer_store_dwordx4 v[152:155],v1,s[36:39],s57 offen offset:2048 sc0 sc1
M 40,v[152:155],152
M 36,v[32:35],156
buffer_store_dwordx4 v[156:159],v1,s[36:39],s58 offen offset:0 sc0 sc1
M 40,v[156:159],156
M 36,v[32:35],160
buffer_store_dwordx4 v[160:163],v1,s[36:39],s58 offen offset:2048 sc0 sc1
M 40,v[160:163],160
s_nop 5
v_add_f32_dpp v44,v36,v36 row_ror:8 row_mask:0xf bank_mask:0xf
s_nop 0
v_cndmask_b32 v45,v40,v41,s[70:71]
v_cndmask_b32 v46,v42,v43,s[70:71]
ds_write_b32 v2,v44 offset:1024
v_cndmask_b32 v45,v45,v46,s[72:73]
ds_write_b32 v0,v45 offset:1024
L 0
s_barrier
R 52,3,1024
R 56,3,1040
R 60,3,1056
L 0
v_pk_add_f32 v[52:53],v[52:53],v[54:55]
v_pk_add_f32 v[56:57],v[56:57],v[58:59]
v_pk_add_f32 v[60:61],v[60:61],v[62:63]
v_add_f32 v52,v52,v53
v_pk_add_f32 v[56:57],v[56:57],v[60:61]
v_mul_f32 v52,s68,v52
v_add_f32 v53,v56,v57
v_mul_f32 v53,s68,v53
v_fma_f32 v53,-v52,v52,v53
v_max_f32 v53,0,v53
v_add_f32 v54,s69,v53
v_fma_f32 v55,s69,v54,v53
v_rsq_f32 v65,v54
v_rsq_f32 v54,v55
v_mul_f32_e64 v64,-v52,v65
v_mul_f32 v63,v55,v54
v_mul_f32 v61,s76,v54
v_mul_f32 v55,v54,v54
v_mul_f32 v62,s77,v55
v_mul_f32 v62,v62,v61
v_cndmask_b32 v60,v52,v64,s[74:75]
v_cndmask_b32 v61,v61,v65,s[74:75]
buffer_store_dwordx4 v[60:63],v4,s[36:39],s59 offen offset:0 sc0 sc1
s_mov_b32 s63,s60
s_lshr_b32 s78,s45,1
s_and_b32 s79,s45,1
s_lshl_b32 s79,s79,7
s_lshl_b32 s57,s78,15
s_add_u32 s57,s57,s79
s_add_u32 s57,s57,12582912
s_add_u32 s58,s57,4096
s_lshl_b32 s59,s78,9
s_add_u32 s59,s59,s79
s_add_u32 s59,s59,107020288
s_lshl_b32 s60,s45,9
s_add_u32 s60,s60,s66
s_add_u32 s60,s60,131072
s_add_u32 s45,s45,s46
s_lshr_b32 s78,s45,1
s_and_b32 s79,s45,1
s_lshl_b32 s79,s79,7
)"
R"(s_lshl_b32 s55,s78,15
s_add_u32 s55,s55,s79
s_mul_i32 s56,s45,16384
s_add_u32 s56,s56,98566144
s_lshl_b32 s61,s78,11
s_add_u32 s61,s61,110587904
s_add_u32 s78,s45,s46
s_lshr_b32 s78,s78,1
s_lshl_b32 s62,s78,11
s_add_u32 s62,s62,110587904
s_waitcnt vmcnt(6)
s_branch Lr_it0_%=
Lr_tb1_%=:
v_add_f32 v53,s69,v53
v_rsq_f32 v48,v53
s_nop 0
v_mul_f32_e64 v50,-v52,v48
v_mov_b32 v49,v48
v_mov_b32 v51,v50
v_pk_fma_f32 v[52:53],v[116:117],v[48:49],v[50:51]
v_pk_fma_f32 v[54:55],v[118:119],v[48:49],v[50:51]
v_pk_fma_f32 v[56:57],v[120:121],v[48:49],v[50:51]
v_pk_fma_f32 v[58:59],v[122:123],v[48:49],v[50:51]
s_add_u32 s78,s55,71303168
buffer_load_dwordx4 v[164:167],v1,s[36:39],s78 offen offset:0 sc1
buffer_load_dwordx4 v[168:171],v1,s[36:39],s78 offen offset:2048 sc1
s_add_u32 s78,s55,71307264
buffer_load_dwordx4 v[172:175],v1,s[36:39],s78 offen offset:0 sc1
buffer_load_dwordx4 v[176:179],v1,s[36:39],s78 offen offset:2048 sc1
v_pk_fma_f32 v[116:117],v[52:53],v[84:85],v[116:117]
v_pk_fma_f32 v[118:119],v[54:55],v[86:87],v[118:119]
v_pk_fma_f32 v[120:121],v[56:57],v[88:89],v[120:121]
v_pk_fma_f32 v[122:123],v[58:59],v[90:91],v[122:123]
v_cvt_pk_bf16_f32 v148,v116,v117
v_cvt_pk_bf16_f32 v149,v118,v119
v_cvt_pk_bf16_f32 v150,v120,v121
v_cvt_pk_bf16_f32 v151,v122,v123
v_pk_fma_f32 v[52:53],v[124:125],v[48:49],v[50:51]
v_pk_fma_f32 v[54:55],v[126:127],v[48:49],v[50:51]
v_pk_fma_f32 v[56:57],v[128:129],v[48:49],v[50:51]
v_pk_fma_f32 v[58:59],v[130:131],v[48:49],v[50:51]
s_add_u32 s78,s55,89128960
buffer_load_dwordx4 v[180:183],v1,s[36:39],s78 offen offset:0 sc1
buffer_load_dwordx4 v[184:187],v1,s[36:39],s78 offen offset:2048 sc1
s_add_u32 s78,s55,89133056
buffer_load_dwordx4 v[188:191],v1,s[36:39],s78 offen offset:0 sc1
buffer_load_dwordx4 v[192:195],v1,s[36:39],s78 offen offset:2048 sc1
v_pk_fma_f32 v[124:125],v[52:53],v[92:93],v[124:125]
v_pk_fma_f32 v[126:127],v[54:55],v[94:95],v[126:127]
v_pk_fma_f32 v[128:129],v[56:57],v[96:97],v[128:129]
v_pk_fma_f32 v[130:131],v[58:59],v[98:99],v[130:131]
v_cvt_pk_bf16_f32 v152,v124,v125
v_cvt_pk_bf16_f32 v153,v126,v127
v_cvt_pk_bf16_f32 v154,v128,v129
v_cvt_pk_bf16_f32 v155,v130,v131
v_pk_fma_f32 v[52:53],v[132:133],v[48:49],v[50:51]
v_pk_fma_f32 v[54:55],v[134:135],v[48:49],v[50:51]
v_pk_fma_f32 v[56:57],v[136:137],v[48:49],v[50:51]
v_pk_fma_f32 v[58:59],v[138:139],v[48:49],v[50:51]
s_add_u32 m0,s65,0
s_nop 0
D 9,56,0
D 9,56,1024
D 9,56,2048
D 9,56,3072
v_pk_fma_f32 v[132:133],v[52:53],v[100:101],v[132:133]
v_pk_fma_f32 v[134:135],v[54:55],v[102:103],v[134:135]
v_pk_fma_f32 v[136:137],v[56:57],v[104:105],v[136:137]
v_pk_fma_f32 v[138:139],v[58:59],v[106:107],v[138:139]
v_cvt_pk_bf16_f32 v156,v132,v133
v_cvt_pk_bf16_f32 v157,v134,v135
v_cvt_pk_bf16_f32 v158,v136,v137
v_cvt_pk_bf16_f32 v159,v138,v139
v_pk_fma_f32 v[52:53],v[140:141],v[48:49],v[50:51]
v_pk_fma_f32 v[54:55],v[142:143],v[48:49],v[50:51]
v_pk_fma_f32 v[56:57],v[144:145],v[48:49],v[50:51]
v_pk_fma_f32 v[58:59],v[146:147],v[48:49],v[50:51]
v_pk_fma_f32 v[140:141],v[52:53],v[108:109],v[140:141]
v_pk_fma_f32 v[142:143],v[54:55],v[110:111],v[142:143]
v_pk_fma_f32 v[144:145],v[56:57],v[112:113],v[144:145]
v_pk_fma_f32 v[146:147],v[58:59],v[114:115],v[146:147]
v_cvt_pk_bf16_f32 v160,v140,v141
v_cvt_pk_bf16_f32 v161,v142,v143
v_cvt_pk_bf16_f32 v162,v144,v145
v_cvt_pk_bf16_f32 v163,v146,v147
v_mfma_f32_16x16x32_bf16 v[36:39],v[32:35],v[148:151],0
buffer_load_dword v18,v6,s[36:39],s62 offen offset:0 sc1
buffer_store_dwordx4 v[148:151],v1,s[36:39],s57 offen offset:0 sc0 sc1
v_mfma_f32_16x16x32_bf16 v[40:43],v[148:151],v[148:151],0
M 36,v[32:35],152
buffer_store_dwordx4 v[152:155],v1,s[36:39],s57 offen offset:2048 sc0 sc1
M 40,v[152:155],152
M 36,v[32:35],156
buffer_store_dwordx4 v[156:159],v1,s[36:39],s58 offen offset:0 sc0 sc1
M 40,v[156:159],156
M 36,v[32:35],160
buffer_store_dwordx4 v[160:163],v1,s[36:39],s58 offen offset:2048 sc0 sc1
M 40,v[160:163],160
s_nop 5
v_add_f32_dpp v44,v36,v36 row_ror:8 row_mask:0xf bank_mask:0xf
s_nop 0
v_cndmask_b32 v45,v40,v41,s[70:71]
v_cndmask_b32 v46,v42,v43,s[70:71]
ds_write_b32 v2,v44 offset:1024
v_cndmask_b32 v45,v45,v46,s[72:73]
ds_write_b32 v0,v45 offset:1024
L 0
s_barrier
R 52,3,1024
R 56,3,1040
R 60,3,1056
L 0
v_pk_add_f32 v[52:53],v[52:53],v[54:55]
v_pk_add_f32 v[56:57],v[56:57],v[58:59]
v_pk_add_f32 v[60:61],v[60:61],v[62:63]
v_add_f32 v52,v52,v53
v_pk_add_f32 v[56:57],v[56:57],v[60:61]
v_mul_f32 v52,s68,v52
v_add_f32 v53,v56,v57
v_mul_f32 v53,s68,v53
v_fma_f32 v53,-v52,v52,v53
v_max_f32 v53,0,v53
v_add_f32 v54,s69,v53
v_fma_f32 v55,s69,v54,v53
v_rsq_f32 v65,v54
v_rsq_f32 v54,v55
v_mul_f32_e64 v64,-v52,v65
v_mul_f32 v63,v55,v54
v_mul_f32 v61,s76,v54
v_mul_f32 v55,v54,v54
v_mul_f32 v62,s77,v55
v_mul_f32 v62,v62,v61
v_cndmask_b32 v60,v52,v64,s[74:75]
v_cndmask_b32 v61,v61,v65,s[74:75]
buffer_store_dwordx4 v[60:63],v4,s[36:39],s59 offen offset:0 sc0 sc1
s_mov_b32 s63,s60
s_lshr_b32 s78,s45,1
s_and_b32 s79,s45,1
s_lshl_b32 s79,s79,7
s_lshl_b32 s57,s78,15
s_add_u32 s57,s57,s79
s_add_u32 s57,s57,12582912
s_add_u32 s58,s57,4096
s_lshl_b32 s59,s78,9
s_add_u32 s59,s59,s79
s_add_u32 s59,s59,107020288
s_lshl_b32 s60,s45,9
s_add_u32 s60,s60,s66
s_add_u32 s60,s60,131072
s_add_u32 s45,s45,s46
s_lshr_b32 s78,s45,1
s_and_b32 s79,s45,1
s_lshl_b32 s79,s79,7
s_lshl_b32 s55,s78,15
s_add_u32 s55,s55,s79
s_mul_i32 s56,s45,16384
s_add_u32 s56,s56,98566144
s_lshl_b32 s61,s78,11
s_add_u32 s61,s61,110587904
s_add_u32 s78,s45,s46
s_lshr_b32 s78,s78,1
s_lshl_b32 s62,s78,11
s_add_u32 s62,s62,110587904
s_waitcnt vmcnt(5)
s_branch Lr_it0_%=
Lr_tc1_%=:
s_add_u32 s78,s55,71303168
buffer_load_dwordx4 v[164:167],v1,s[36:39],s78 offen offset:0 sc1
buffer_load_dwordx4 v[168:171],v1,s[36:39],s78 offen offset:2048 sc1
s_add_u32 s78,s55,71307264
buffer_load_dwordx4 v[172:175],v1,s[36:39],s78 offen offset:0 sc1
buffer_load_dwordx4 v[176:179],v1,s[36:39],s78 offen offset:2048 sc1
s_add_u32 s78,s55,89128960
buffer_load_dwordx4 v[180:183],v1,s[36:39],s78 offen offset:0 sc1
buffer_load_dwordx4 v[184:187],v1,s[36:39],s78 offen offset:2048 sc1
s_add_u32 s78,s55,89133056
buffer_load_dwordx4 v[188:191],v1,s[36:39],s78 offen offset:0 sc1
buffer_load_dwordx4 v[192:195],v1,s[36:39],s78 offen offset:2048 sc1
s_add_u32 m0,s65,0
s_nop 0
D 9,56,0
D 9,56,1024
D 9,56,2048
D 9,56,3072
buffer_load_dword v18,v6,s[36:39],s62 offen offset:0 sc1
s_waitcnt vmcnt(13)
buffer_store_dword v17,v5,s[36:39],s63 offen offset:0 sc0 sc1
s_mov_b32 s63,0x7fff0000
s_lshr_b32 s78,s45,1
s_and_b32 s79,s45,1
s_lshl_b32 s79,s79,7
s_lshl_b32 s57,s78,15
s_add_u32 s57,s57,s79
s_add_u32 s57,s57,12582912
s_add_u32 s58,s57,4096
s_lshl_b32 s59,s78,9
s_add_u32 s59,s59,s79
s_add_u32 s59,s59,107020288
s_lshl_b32 s60,s45,9
s_add_u32 s60,s60,s66
s_add_u32 s60,s60,131072
s_add_u32 s45,s45,s46
s_lshr_b32 s78,s45,1
s_and_b32 s79,s45,1
s_lshl_b32 s79,s79,7
s_lshl_b32 s55,s78,15
s_add_u32 s55,s55,s79
s_mul_i32 s56,s45,16384
s_add_u32 s56,s56,98566144
s_lshl_b32 s61,s78,11
s_add_u32 s61,s61,110587904
s_add_u32 s78,s45,s46
s_lshr_b32 s78,s78,1
s_lshl_b32 s62,s78,11
s_add_u32 s62,s62,110587904
s_waitcnt vmcnt(1)
s_branch Lr_it0_%=
Lr_done_%=:
s_waitcnt vmcnt(0) lgkmcnt(0)
s_barrier
Lcode_r_e:
.purgem M
.purgem R
.purgem T
.purgem D
.purgem L
)"
  :: "s"(ws), "s"(__builtin_amdgcn_readfirstlane(sWave)), "s"(__builtin_amdgcn_readfirstlane(sG0)), "s"(__builtin_amdgcn_readfirstlane(sStep)), "s"(__builtin_amdgcn_readfirstlane(sWw)), "s"(__builtin_amdgcn_readfirstlane(sExp)), "v"(vRP), "v"(vRF), "v"(vRwr), "v"(vRrd), "v"(vRS), "v"(vFlag), "v"(vL4), "v"(vLY), "v"(vQ), "v"(vY0), "v"(vTr), "v"(vSa), "v"(vSb), "v"(vWd0), "v"(vWd1), "v"(vWr0), "v"(vWr1), "s"(q), "s"(w4) : "v0", "v1", "v2", "v3", "v4", "v5", "v6", "v7", "v8", "v9", "v10", "v11", "v12", "v13", "v14", "v15", "v16", "v17", "v18", "v19", "v20", "v21", "v22", "v23", "v24", "v25", "v26", "v27", "v28", "v29", "v30", "v31", "v32", "v33", "v34", "v35", "v36", "v37", "v38", "v39", "v40", "v41", "v42", "v43", "v44", "v45", "v46", "v47", "v48", "v49", "v50", "v51", "v52", "v53", "v54", "v55", "v56", "v57", "v58", "v59", "v60", "v61", "v62", "v63", "v64", "v65", "v66", "v67", "v68", "v69", "v70", "v71", "v72", "v73", "v74", "v75", "v76", "v77", "v78", "v79", "v80", "v81", "v82", "v83", "v84", "v85", "v86", "v87", "v88", "v89", "v90", "v91", "v92", "v93", "v94", "v95", "v96", "v97", "v98", "v99", "v100", "v101", "v102", "v103", "v104", "v105", "v106", "v107", "v108", "v109", "v110", "v111", "v112", "v113", "v114", "v115", "v116", "v117", "v118", "v119", "v120", "v121", "v122", "v123", "v124", "v125", "v126", "v127", "v128", "v129", "v130", "v131", "v132", "v133", "v134", "v135", "v136", "v137", "v138", "v139", "v140", "v141", "v142", "v143", "v144", "v145", "v146", "v147", "v148", "v149", "v150", "v151", "v152", "v153", "v154", "v155", "v156", "v157", "v158", "v159", "v160", "v161", "v162", "v163", "v164", "v165", "v166", "v167", "v168", "v169", "v170", "v171", "v172", "v173", "v174", "v175", "v176", "v177", "v178", "v179", "v180", "v181", "v182", "v183", "v184", "v185", "v186", "v187", "v188", "v189", "v190", "v191", "v192", "v193", "v194", "v195", "v196", "v197", "v198", "v199", "v200", "v201", "v202", "v203", "v204", "v205", "v206", "v207", "v208", "v209", "v210", "v211", "v212", "v213", "v214", "v215", "v216", "v217", "v218", "v219", "v220", "v221", "v222", "v223", "v224", "v225", "v226", "v227", "v228", "v229", "v230", "v231", "a0", "a1", "a2", "a3", "a4", "a5", "a6", "a7", "a8", "a9", "a10", "a11", "a12", "a13", "a14", "a15", "a16", "a17", "a18", "a19", "a20", "a21", "a22", "a23", "a24", "a25", "a26", "a27", "a28", "a29", "a30", "a31", "a32", "a33", "a34", "a35", "a36", "a37", "a38", "a39", "a40", "a41", "a42", "a43", "a44", "a45", "a46", "a47", "a48", "a49", "a50", "a51", "a52", "a53", "a54", "a55", "a56", "a57", "a58", "a59", "a60", "a61", "a62", "a63", "a64", "a65", "a66", "a67", "a68", "a69", "a70", "a71", "a72", "a73", "a74", "a75", "a76", "a77", "a78", "a79", "a80", "a81", "a82", "a83", "a84", "a85", "a86", "a87", "a88", "a89", "a90", "a91", "a92", "a93", "a94", "a95", "a96", "a97", "a98", "a99", "a100", "a101", "a102", "a103", "a104", "a105", "a106", "a107", "a108", "a109", "a110", "a111", "a112", "a113", "a114", "a115", "a116", "a117", "a118", "a119", "a120", "a121", "a122", "a123", "a124", "a125", "a126", "a127", "a128", "a129", "a130", "a131", "a132", "a133", "a134", "a135", "a136", "a137", "a138", "a139", "a140", "a141", "a142", "a143", "a144", "a145", "a146", "a147", "a148", "a149", "a150", "a151", "a152", "a153", "a154", "a155", "a156", "a157", "a158", "a159", "a160", "a161", "a162", "a163", "a164", "a165", "a166", "a167", "a168", "a169", "a170", "a171", "a172", "a173", "a174", "a175", "a176", "a177", "a178", "a179", "a180", "a181", "a182", "a183", "a184", "a185", "a186", "a187", "a188", "a189", "a190", "a191", "a192", "a193", "a194", "a195", "a196", "a197", "a198", "a199", "a200", "a201", "a202", "a203", "a204", "a205", "a206", "a207", "a208", "a209", "a210", "a211", "a212", "a213", "a214", "a215", "a216", "a217", "a218", "a219", "a220", "a221", "a222", "a223", "a224", "a225", "a226", "a227", "a228", "a229", "a230", "a231", "a232", "a233", "a234", "a235", "a236", "a237", "a238", "a239", "a240", "a241", "a242", "a243", "a244", "a245", "a246", "a247", "a248", "a249", "a250", "a251", "a252", "a253", "a254", "a255", "s36", "s37", "s38", "s39", "s40", "s41", "s42", "s43", "s44", "s45", "s46", "s47", "s48", "s49", "s50", "s51", "s52", "s53", "s54", "s55", "s56", "s57", "s58", "s59", "s60", "s61", "s62", "s63", "s64", "s65", "s66", "s67", "s68", "s69", "s70", "s71", "s72", "s73", "s74", "s75", "s76", "s77", "s78", "s79", "s80", "s81", "s82", "s83", "s84", "s85", "s86", "s87", "s88", "s89", "s90", "s91", "s92", "s93", "s94", "s95", "s96", "s97", "vcc", "scc", "memory");
    if (RHELP && slice < 16) { kind = 4; rep = 5; kh = slice >> 3; slice &= 7; }
  }
  CTS(9);
#define COMMON(NF, FOFF, FSTR, SNAP, LDW, NL, TOFF)                                                                                            \
  LANE_ID(ln);                                                                                                                       \
  const int l15 = ln & 15, lg = ln >> 4, lq = l15 >> 2, lp = ln & 3, rr = 8 * lg + lq, fj = ln & 31;                                 \
  const unsigned vL16 = ln * 16, vFlag = (ln < 32 && wave == 0) ? ln * 4 : OOB;                                                      \
  const unsigned vSnapOff = (wave == 0 && fj < (NF)) ? (FOFF) + (ln >> 5) * sStep * (FSTR) : OOB;                                    \
  const unsigned vSnapRd = (SNAP) + (fj < (NF) ? ln : ln & 32) * 4;                                                                  \
  const unsigned vWd0 = (ln >> 3) * (LDW) + ((ln & 7) ^ swzB(ln >> 3)) * 16, vWd1 = ((ln >> 3) + 8) * (LDW) + ((ln & 7) ^ swzB(8 + (ln >> 3))) * 16; \
  const unsigned vWr0 = (wave >> 1) * 8192 + rr * 128 + (((wave & 1) * 4 + lp) ^ swzB(rr)) * 16, vWr1 = vWr0 + 8;                    \
  const unsigned vWr2 = 16384 + rr * 128 + ((2 * wave + (lp >> 1)) ^ swzB(rr)) * 16 + (lp & 1) * 8;                                  \
  const unsigned sWave = wave, sRk = kind == 4 ? rep : t_start, sG0 = rep < 5 ? rep : rep == 5 ? U0 + 5 : U1 + rep, sDbg = sDbg0 + (rep < 5 ? 0 : 2048)
  if (kind == 3 || kind == 5) {
    const unsigned sStep = kind == 3 ? 5 : G3OR;
    COMMON(8, fj * 128, 1024, 131072, 7168, 256, 0);
    const unsigned vOutS = (lg & 1) * 8 + (((wave & 1) * 2 + (lg >> 1)) * 16 + l15) * 16 + (4 + (wave >> 1) - wave) * 1024;
    const unsigned vAux0 = (ln & 7) * 128 + (4 * wave + lg) * 8 + ((ln >> 3) & 1) * 4;
    const float k1 = -2.0f * 0.7978845608028654f * 1.4426950408889634f;
    const unsigned vAux1 = 139264 + (l15 < 4 ? (4 * wave + lg) * 16 + l15 * 4 : 512 + ln * 4), vAux2 = 139264 + l15 * 16;
    const unsigned vAux3 = l15 == 2 ? __float_as_uint(k1 * 0.044715f) : 0u, vAn = l15 == 3 ? 0x3f800000u : 0u, vAe = l15 == 1 ? __float_as_uint(k1) : 0u;
    const unsigned sOutOff = OFF_G + (slice * 6 + wave) * 1024, sFoutOff = OFF_FG + slice * 128, sAuxOff = 0, sWw = 16 * wave * 7168 + slice * 384, sBuf = 0;
    const unsigned sExp = __builtin_amdgcn_readfirstlane(ep);
    if (kind == 3) { asm volatile(
R"(.macro M d,w,x
v_mfma_f32_16x16x32_bf16 v[\d:\d+3], \w, v[\x:\x+3], v[\d:\d+3]
.endm
.macro R d,a,o
ds_read_b128 v[\d:\d+3], v\a offset:\o
.endm
.macro T d,a,o
ds_read_b64_tr_b16 \d, v\a offset:\o
.endm
.macro D a,s,o
buffer_load_dwordx4 v\a, s[36:39], s\s offen offset:\o sc1 lds
.endm
.macro L c
s_waitcnt lgkmcnt(\c)
.endm
Lcode_g3_b:
s_mov_b64 s[36:37],%0
s_mov_b32 s38,111243392
s_mov_b32 s39,0x00020000
s_mov_b32 s44,%1
s_mov_b32 s45,%2
s_mov_b32 s46,%3
s_mov_b32 s47,%4
s_mov_b32 s48,%5
s_mov_b32 s49,%6
s_mov_b32 s50,%7
s_mov_b32 s51,%8
s_mov_b32 s53,%9
v_mov_b32 v0,%10
v_mov_b32 v3,%11
v_mov_b32 v5,%12
v_mov_b32 v6,%13
v_mov_b32 v18,%14
v_mov_b32 v19,%15
v_mov_b32 v20,%16
v_mov_b32 v21,%17
v_mov_b32 v22,%18
v_mov_b32 v10,%19
v_mov_b32 v11,%20
v_mov_b32 v12,%21
v_mov_b32 v13,%22
v_mov_b32 v14,%23
v_mov_b32 v15,%24
v_mov_b32 v16,%25
s_mov_b64 s[40:41],%26
s_mov_b32 s42,7340032
s_mov_b32 s43,0x00020000
s_lshl_b32 s57,s44,11
s_cmp_ge_u32 s45,8
s_cbranch_scc1 Lg3_dly_%=
s_sleep 16
s_sleep 16
s_sleep 16
Lg3_dly_%=:
s_add_u32 s55,s50,0
s_add_u32 m0,s57,0
s_nop 0
buffer_load_dwordx4 v18,s[40:43],s55 offen offset:0  lds
s_add_u32 m0,s57,1024
s_nop 0
buffer_load_dwordx4 v19,s[40:43],s55 offen offset:0  lds
s_add_u32 m0,s57,8064
s_nop 0
buffer_load_dwordx4 v18,s[40:43],s55 offen offset:128  lds
s_add_u32 m0,s57,9088
s_nop 0
buffer_load_dwordx4 v19,s[40:43],s55 offen offset:128  lds
s_add_u32 m0,s57,16128
s_nop 0
buffer_load_dwordx4 v18,s[40:43],s55 offen offset:256  lds
s_add_u32 m0,s57,17152
s_nop 0
buffer_load_dwordx4 v19,s[40:43],s55 offen offset:256  lds
s_add_u32 s55,s50,458752
s_add_u32 m0,s57,24576
s_nop 0
buffer_load_dwordx4 v18,s[40:43],s55 offen offset:0  lds
s_add_u32 m0,s57,25600
s_nop 0
buffer_load_dwordx4 v19,s[40:43],s55 offen offset:0  lds
s_add_u32 m0,s57,32640
s_nop 0
buffer_load_dwordx4 v18,s[40:43],s55 offen offset:128  lds
s_add_u32 m0,s57,33664
s_nop 0
buffer_load_dwordx4 v19,s[40:43],s55 offen offset:128  lds
s_add_u32 m0,s57,40704
s_nop 0
buffer_load_dwordx4 v18,s[40:43],s55 offen offset:256  lds
s_add_u32 m0,s57,41728
s_nop 0
buffer_load_dwordx4 v19,s[40:43],s55 offen offset:256  lds
s_add_u32 s55,s50,917504
s_add_u32 m0,s57,49152
s_nop 0
buffer_load_dwordx4 v18,s[40:43],s55 offen offset:0  lds
s_add_u32 m0,s57,50176
s_nop 0
buffer_load_dwordx4 v19,s[40:43],s55 offen offset:0  lds
s_add_u32 m0,s57,57216
s_nop 0
buffer_load_dwordx4 v18,s[40:43],s55 offen offset:128  lds
s_add_u32 m0,s57,58240
s_nop 0
buffer_load_dwordx4 v19,s[40:43],s55 offen offset:128  lds
s_add_u32 m0,s57,65280
s_nop 0
buffer_load_dwordx4 v18,s[40:43],s55 offen offset:256  lds
s_add_u32 m0,s57,66304
s_nop 0
buffer_load_dwordx4 v19,s[40:43],s55 offen offset:256  lds
s_add_u32 s55,s50,1376256
s_add_u32 m0,s57,73728
s_nop 0
buffer_load_dwordx4 v18,s[40:43],s55 offen offset:0  lds
s_add_u32 m0,s57,74752
s_nop 0
buffer_load_dwordx4 v19,s[40:43],s55 offen offset:0  lds
s_add_u32 m0,s57,81792
s_nop 0
buffer_load_dwordx4 v18,s[40:43],s55 offen offset:128  lds
s_add_u32 m0,s57,82816
s_nop 0
buffer_load_dwordx4 v19,s[40:43],s55 offen offset:128  lds
s_add_u32 m0,s57,89856
s_nop 0
buffer_load_dwordx4 v18,s[40:43],s55 offen offset:256  lds
s_add_u32 m0,s57,90880
s_nop 0
buffer_load_dwordx4 v19,s[40:43],s55 offen offset:256  lds
s_add_u32 s55,s50,1835008
s_add_u32 m0,s57,98304
s_nop 0
buffer_load_dwordx4 v18,s[40:43],s55 offen offset:0  lds
s_add_u32 m0,s57,99328
s_nop 0
buffer_load_dwordx4 v19,s[40:43],s55 offen offset:0  lds
s_add_u32 m0,s57,106368
s_nop 0
buffer_load_dwordx4 v18,s[40:43],s55 offen offset:128  lds
s_add_u32 m0,s57,107392
s_nop 0
buffer_load_dwordx4 v19,s[40:43],s55 offen offset:128  lds
s_add_u32 m0,s57,114432
s_nop 0
buffer_load_dwordx4 v18,s[40:43],s55 offen offset:256  lds
s_add_u32 m0,s57,115456
s_nop 0
buffer_load_dwordx4 v19,s[40:43],s55 offen offset:256  lds
s_waitcnt vmcnt(24) lgkmcnt(0)
s_barrier
s_add_u32 s55,s50,2293760
s_add_u32 m0,s57,122880
s_nop 0
buffer_load_dwordx4 v18,s[40:43],s55 offen offset:0  lds
s_add_u32 m0,s57,123904
s_nop 0
buffer_load_dwordx4 v19,s[40:43],s55 offen offset:0  lds
s_add_u32 m0,s57,130944
s_nop 0
buffer_load_dwordx4 v18,s[40:43],s55 offen offset:128  lds
s_add_u32 m0,s57,131968
s_nop 0
buffer_load_dwordx4 v19,s[40:43],s55 offen offset:128  lds
s_add_u32 m0,s57,139008
s_nop 0
buffer_load_dwordx4 v18,s[40:43],s55 offen offset:256  lds
s_add_u32 m0,s57,140032
s_nop 0
buffer_load_dwordx4 v19,s[40:43],s55 offen offset:256  lds
v_add_u32 v26,0,v20
v_add_u32 v27,0,v21
v_add_u32 v28,0,v22
T a[0:1],26,0
T a[2:3],26,512
T a[4:5],27,0
T a[6:7],27,512
T a[8:9],28,0
T a[10:11],28,512
T a[12:13],26,4096
T a[14:15],26,4608
T a[16:17],27,4096
T a[18:19],27,4608
T a[20:21],28,4096
T a[22:23],28,4608
s_waitcnt vmcnt(24) lgkmcnt(0)
s_barrier
s_add_u32 s55,s50,2752512
s_add_u32 m0,s57,0
s_nop 0
buffer_load_dwordx4 v18,s[40:43],s55 offen offset:0  lds
s_add_u32 m0,s57,1024
s_nop 0
buffer_load_dwordx4 v19,s[40:43],s55 offen offset:0  lds
s_add_u32 m0,s57,8064
s_nop 0
buffer_load_dwordx4 v18,s[40:43],s55 offen offset:128  lds
s_add_u32 m0,s57,9088
s_nop 0
buffer_load_dwordx4 v19,s[40:43],s55 offen offset:128  lds
s_add_u32 m0,s57,16128
s_nop 0
buffer_load_dwordx4 v18,s[40:43],s55 offen offset:256  lds
s_add_u32 m0,s57,17152
s_nop 0
buffer_load_dwordx4 v19,s[40:43],s55 offen offset:256  lds
v_add_u32 v26,24576,v20
v_add_u32 v27,24576,v21
v_add_u32 v28,24576,v22
T a[24:25],26,0
T a[26:27],26,512
T a[28:29],27,0
T a[30:31],27,512
T a[32:33],28,0
T a[34:35],28,512
T a[36:37],26,4096
T a[38:39],26,4608
T a[40:41],27,4096
T a[42:43],27,4608
T a[44:45],28,4096
T a[46:47],28,4608
s_waitcnt vmcnt(24) lgkmcnt(0)
s_barrier
s_add_u32 s55,s50,3211264
s_add_u32 m0,s57,24576
s_nop 0
buffer_load_dwordx4 v18,s[40:43],s55 offen offset:0  lds
s_add_u32 m0,s57,25600
s_nop 0
buffer_load_dwordx4 v19,s[40:43],s55 offen offset:0  lds
s_add_u32 m0,s57,32640
s_nop 0
buffer_load_dwordx4 v18,s[40:43],s55 offen offset:128  lds
s_add_u32 m0,s57,33664
s_nop 0
buffer_load_dwordx4 v19,s[40:43],s55 offen offset:128  lds
s_add_u32 m0,s57,40704
s_nop 0
buffer_load_dwordx4 v18,s[40:43],s55 offen offset:256  lds
s_add_u32 m0,s57,41728
s_nop 0
buffer_load_dwordx4 v19,s[40:43],s55 offen offset:256  lds
v_add_u32 v26,49152,v20
v_add_u32 v27,49152,v21
v_add_u32 v28,49152,v22
T a[48:49],26,0
T a[50:51],26,512
T a[52:53],27,0
T a[54:55],27,512
T a[56:57],28,0
T a[58:59],28,512
T a[60:61],26,4096
T a[62:63],26,4608
T a[64:65],27,4096
T a[66:67],27,4608
T a[68:69],28,4096
T a[70:71],28,4608
s_waitcnt vmcnt(24) lgkmcnt(0)
s_barrier
s_add_u32 s55,s50,3670016
s_add_u32 m0,s57,49152
s_nop 0
buffer_load_dwordx4 v18,s[40:43],s55 offen offset:0  lds
s_add_u32 m0,s57,50176
s_nop 0
buffer_load_dwordx4 v19,s[40:43],s55 offen offset:0  lds
s_add_u32 m0,s57,57216
s_nop 0
buffer_load_dwordx4 v18,s[40:43],s55 offen offset:128  lds
s_add_u32 m0,s57,58240
s_nop 0
buffer_load_dwordx4 v19,s[40:43],s55 offen offset:128  lds
s_add_u32 m0,s57,65280
s_nop 0
buffer_load_dwordx4 v18,s[40:43],s55 offen offset:256  lds
s_add_u32 m0,s57,66304
s_nop 0
buffer_load_dwordx4 v19,s[40:43],s55 offen offset:256  lds
v_add_u32 v26,73728,v20
v_add_u32 v27,73728,v21
v_add_u32 v28,73728,v22
T a[72:73],26,0
T a[74:75],26,512
T a[76:77],27,0
T a[78:79],27,512
T a[80:81],28,0
T a[82:83],28,512
T a[84:85],26,4096
T a[86:87],26,4608
T a[88:89],27,4096
T a[90:91],27,4608
T a[92:93],28,4096
T a[94:95],28,4608
s_waitcnt vmcnt(24) lgkmcnt(0)
s_barrier
s_add_u32 s55,s50,4128768
s_add_u32 m0,s57,73728
s_nop 0
buffer_load_dwordx4 v18,s[40:43],s55 offen offset:0  lds
s_add_u32 m0,s57,74752
s_nop 0
buffer_load_dwordx4 v19,s[40:43],s55 offen offset:0  lds
s_add_u32 m0,s57,81792
s_nop 0
buffer_load_dwordx4 v18,s[40:43],s55 offen offset:128  lds
s_add_u32 m0,s57,82816
s_nop 0
buffer_load_dwordx4 v19,s[40:43],s55 offen offset:128  lds
s_add_u32 m0,s57,89856
s_nop 0
buffer_load_dwordx4 v18,s[40:43],s55 offen offset:256  lds
s_add_u32 m0,s57,90880
s_nop 0
buffer_load_dwordx4 v19,s[40:43],s55 offen offset:256  lds
v_add_u32 v26,98304,v20
v_add_u32 v27,98304,v21
v_add_u32 v28,98304,v22
T a[96:97],26,0
T a[98:99],26,512
T a[100:101],27,0
T a[102:103],27,512
T a[104:105],28,0
T a[106:107],28,512
T a[108:109],26,4096
T a[110:111],26,4608
T a[112:113],27,4096
T a[114:115],27,4608
T a[116:117],28,4096
T a[118:119],28,4608
s_waitcnt vmcnt(24) lgkmcnt(0)
s_barrier
s_add_u32 s55,s50,4587520
s_add_u32 m0,s57,98304
s_nop 0
buffer_load_dwordx4 v18,s[40:43],s55 offen offset:0  lds
s_add_u32 m0,s57,99328
s_nop 0
buffer_load_dwordx4 v19,s[40:43],s55 offen offset:0  lds
s_add_u32 m0,s57,106368
s_nop 0
buffer_load_dwordx4 v18,s[40:43],s55 offen offset:128  lds
s_add_u32 m0,s57,107392
s_nop 0
buffer_load_dwordx4 v19,s[40:43],s55 offen offset:128  lds
s_add_u32 m0,s57,114432
s_nop 0
buffer_load_dwordx4 v18,s[40:43],s55 offen offset:256  lds
s_add_u32 m0,s57,115456
s_nop 0
buffer_load_dwordx4 v19,s[40:43],s55 offen offset:256  lds
v_add_u32 v26,122880,v20
v_add_u32 v27,122880,v21
v_add_u32 v28,122880,v22
T a[120:121],26,0
T a[122:123],26,512
T a[124:125],27,0
T a[126:127],27,512
T a[128:129],28,0
T a[130:131],28,512
T a[132:133],26,4096
T a[134:135],26,4608
T a[136:137],27,4096
T a[138:139],27,4608
T a[140:141],28,4096
T a[142:143],28,4608
s_waitcnt vmcnt(24) lgkmcnt(0)
s_barrier
s_add_u32 s55,s50,5046272
s_add_u32 m0,s57,122880
s_nop 0
buffer_load_dwordx4 v18,s[40:43],s55 offen offset:0  lds
s_add_u32 m0,s57,123904
s_nop 0
buffer_load_dwordx4 v19,s[40:43],s55 offen offset:0  lds
s_add_u32 m0,s57,130944
s_nop 0
buffer_load_dwordx4 v18,s[40:43],s55 offen offset:128  lds
s_add_u32 m0,s57,131968
s_nop 0
buffer_load_dwordx4 v19,s[40:43],s55 offen offset:128  lds
s_add_u32 m0,s57,139008
s_nop 0
buffer_load_dwordx4 v18,s[40:43],s55 offen offset:256  lds
s_add_u32 m0,s57,140032
s_nop 0
buffer_load_dwordx4 v19,s[40:43],s55 offen offset:256  lds
v_add_u32 v26,0,v20
v_add_u32 v27,0,v21
v_add_u32 v28,0,v22
T a[144:145],26,0
T a[146:147],26,512
T a[148:149],27,0
T a[150:151],27,512
T a[152:153],28,0
T a[154:155],28,512
T a[156:157],26,4096
T a[158:159],26,4608
T a[160:161],27,4096
T a[162:163],27,4608
T a[164:165],28,4096
T a[166:167],28,4608
s_waitcnt vmcnt(24) lgkmcnt(0)
s_barrier
s_add_u32 s55,s50,5505024
s_add_u32 m0,s57,0
s_nop 0
buffer_load_dwordx4 v18,s[40:43],s55 offen offset:0  lds
s_add_u32 m0,s57,1024
s_nop 0
buffer_load_dwordx4 v19,s[40:43],s55 offen offset:0  lds
s_add_u32 m0,s57,8064
s_nop 0
buffer_load_dwordx4 v18,s[40:43],s55 offen offset:128  lds
s_add_u32 m0,s57,9088
s_nop 0
buffer_load_dwordx4 v19,s[40:43],s55 offen offset:128  lds
s_add_u32 m0,s57,16128
s_nop 0
buffer_load_dwordx4 v18,s[40:43],s55 offen offset:256  lds
s_add_u32 m0,s57,17152
s_nop 0
buffer_load_dwordx4 v19,s[40:43],s55 offen offset:256  lds
v_add_u32 v26,24576,v20
v_add_u32 v27,24576,v21
v_add_u32 v28,24576,v22
T a[168:169],26,0
T a[170:171],26,512
T a[172:173],27,0
T a[174:175],27,512
T a[176:177],28,0
T a[178:179],28,512
T a[180:181],26,4096
T a[182:183],26,4608
T a[184:185],27,4096
T a[186:187],27,4608
T a[188:189],28,4096
T a[190:191],28,4608
s_waitcnt vmcnt(24) lgkmcnt(0)
s_barrier
s_add_u32 s55,s50,5963776
s_add_u32 m0,s57,24576
s_nop 0
buffer_load_dwordx4 v18,s[40:43],s55 offen offset:0  lds
s_add_u32 m0,s57,25600
s_nop 0
buffer_load_dwordx4 v19,s[40:43],s55 offen offset:0  lds
s_add_u32 m0,s57,32640
s_nop 0
buffer_load_dwordx4 v18,s[40:43],s55 offen offset:128  lds
s_add_u32 m0,s57,33664
s_nop 0
buffer_load_dwordx4 v19,s[40:43],s55 offen offset:128  lds
s_add_u32 m0,s57,40704
s_nop 0
buffer_load_dwordx4 v18,s[40:43],s55 offen offset:256  lds
s_add_u32 m0,s57,41728
s_nop 0
buffer_load_dwordx4 v19,s[40:43],s55 offen offset:256  lds
v_add_u32 v26,49152,v20
v_add_u32 v27,49152,v21
v_add_u32 v28,49152,v22
T a[192:193],26,0
T a[194:195],26,512
T a[196:197],27,0
T a[198:199],27,512
T a[200:201],28,0
T a[202:203],28,512
T a[204:205],26,4096
T a[206:207],26,4608
T a[208:209],27,4096
T a[210:211],27,4608
T a[212:213],28,4096
T a[214:215],28,4608
s_waitcnt vmcnt(24) lgkmcnt(0)
s_barrier
s_add_u32 s55,s50,6422528
s_add_u32 m0,s57,49152
s_nop 0
buffer_load_dwordx4 v18,s[40:43],s55 offen offset:0  lds
s_add_u32 m0,s57,50176
s_nop 0
buffer_load_dwordx4 v19,s[40:43],s55 offen offset:0  lds
s_add_u32 m0,s57,57216
s_nop 0
buffer_load_dwordx4 v18,s[40:43],s55 offen offset:128  lds
s_add_u32 m0,s57,58240
s_nop 0
buffer_load_dwordx4 v19,s[40:43],s55 offen offset:128  lds
s_add_u32 m0,s57,65280
s_nop 0
buffer_load_dwordx4 v18,s[40:43],s55 offen offset:256  lds
s_add_u32 m0,s57,66304
s_nop 0
buffer_load_dwordx4 v19,s[40:43],s55 offen offset:256  lds
v_add_u32 v26,73728,v20
v_add_u32 v27,73728,v21
v_add_u32 v28,73728,v22
T a[216:217],26,0
T a[218:219],26,512
T a[220:221],27,0
)"
R"(T a[222:223],27,512
T a[224:225],28,0
T a[226:227],28,512
T a[228:229],26,4096
T a[230:231],26,4608
T a[232:233],27,4096
T a[234:235],27,4608
T a[236:237],28,4096
T a[238:239],28,4608
s_waitcnt vmcnt(24) lgkmcnt(0)
s_barrier
s_add_u32 s55,s50,6881280
s_add_u32 m0,s57,73728
s_nop 0
buffer_load_dwordx4 v18,s[40:43],s55 offen offset:0  lds
s_add_u32 m0,s57,74752
s_nop 0
buffer_load_dwordx4 v19,s[40:43],s55 offen offset:0  lds
s_add_u32 m0,s57,81792
s_nop 0
buffer_load_dwordx4 v18,s[40:43],s55 offen offset:128  lds
s_add_u32 m0,s57,82816
s_nop 0
buffer_load_dwordx4 v19,s[40:43],s55 offen offset:128  lds
s_add_u32 m0,s57,89856
s_nop 0
buffer_load_dwordx4 v18,s[40:43],s55 offen offset:256  lds
s_add_u32 m0,s57,90880
s_nop 0
buffer_load_dwordx4 v19,s[40:43],s55 offen offset:256  lds
v_add_u32 v26,98304,v20
v_add_u32 v27,98304,v21
v_add_u32 v28,98304,v22
T a[240:241],26,0
T a[242:243],26,512
T a[244:245],27,0
T a[246:247],27,512
T a[248:249],28,0
T a[250:251],28,512
T a[252:253],26,4096
T a[254:255],26,4608
T v[128:129],27,4096
T v[130:131],27,4608
T v[132:133],28,4096
T v[134:135],28,4608
s_waitcnt vmcnt(24) lgkmcnt(0)
s_barrier
v_add_u32 v26,122880,v20
v_add_u32 v27,122880,v21
v_add_u32 v28,122880,v22
T v[136:137],26,0
T v[138:139],26,512
T v[140:141],27,0
T v[142:143],27,512
T v[144:145],28,0
T v[146:147],28,512
T v[148:149],26,4096
T v[150:151],26,4608
T v[152:153],27,4096
T v[154:155],27,4608
T v[156:157],28,4096
T v[158:159],28,4608
s_waitcnt vmcnt(18) lgkmcnt(0)
s_barrier
v_add_u32 v26,0,v20
v_add_u32 v27,0,v21
v_add_u32 v28,0,v22
T v[160:161],26,0
T v[162:163],26,512
T v[164:165],27,0
T v[166:167],27,512
T v[168:169],28,0
T v[170:171],28,512
T v[172:173],26,4096
T v[174:175],26,4608
T v[176:177],27,4096
T v[178:179],27,4608
T v[180:181],28,4096
T v[182:183],28,4608
s_waitcnt vmcnt(12) lgkmcnt(0)
s_barrier
v_add_u32 v26,24576,v20
v_add_u32 v27,24576,v21
v_add_u32 v28,24576,v22
T v[184:185],26,0
T v[186:187],26,512
T v[188:189],27,0
T v[190:191],27,512
T v[192:193],28,0
T v[194:195],28,512
T v[196:197],26,4096
T v[198:199],26,4608
T v[200:201],27,4096
T v[202:203],27,4608
T v[204:205],28,4096
T v[206:207],28,4608
s_waitcnt vmcnt(6) lgkmcnt(0)
s_barrier
v_add_u32 v26,49152,v20
v_add_u32 v27,49152,v21
v_add_u32 v28,49152,v22
T v[208:209],26,0
T v[210:211],26,512
T v[212:213],27,0
T v[214:215],27,512
T v[216:217],28,0
T v[218:219],28,512
T v[220:221],26,4096
T v[222:223],26,4608
T v[224:225],27,4096
T v[226:227],27,4608
T v[228:229],28,4096
T v[230:231],28,4608
s_waitcnt vmcnt(0) lgkmcnt(0)
s_barrier
v_add_u32 v26,73728,v20
v_add_u32 v27,73728,v21
v_add_u32 v28,73728,v22
T v[232:233],26,0
T v[234:235],26,512
T v[236:237],27,0
T v[238:239],27,512
T v[240:241],28,0
T v[242:243],28,512
T v[244:245],26,4096
T v[246:247],26,4608
T v[248:249],27,4096
T v[250:251],27,4608
T v[252:253],28,4096
T v[254:255],28,4608
L 0
s_barrier
v_mov_b32 v42,0x3f803f80
v_mov_b32 v43,0x3f803f80
v_mov_b32 v44,0x3f803f80
v_mov_b32 v45,0x3f803f80
s_nop 1
v_mfma_f32_16x16x32_bf16 v[82:85],a[0:3],v[42:45],0
v_mfma_f32_16x16x32_bf16 v[86:89],a[4:7],v[42:45],0
v_mfma_f32_16x16x32_bf16 v[90:93],a[8:11],v[42:45],0
M 82,a[12:15],42
M 86,a[16:19],42
M 90,a[20:23],42
M 82,a[24:27],42
M 86,a[28:31],42
M 90,a[32:35],42
M 82,a[36:39],42
M 86,a[40:43],42
M 90,a[44:47],42
M 82,a[48:51],42
M 86,a[52:55],42
M 90,a[56:59],42
M 82,a[60:63],42
M 86,a[64:67],42
M 90,a[68:71],42
M 82,a[72:75],42
M 86,a[76:79],42
M 90,a[80:83],42
M 82,a[84:87],42
M 86,a[88:91],42
M 90,a[92:95],42
M 82,a[96:99],42
M 86,a[100:103],42
M 90,a[104:107],42
M 82,a[108:111],42
M 86,a[112:115],42
M 90,a[116:119],42
M 82,a[120:123],42
M 86,a[124:127],42
M 90,a[128:131],42
M 82,a[132:135],42
M 86,a[136:139],42
M 90,a[140:143],42
M 82,a[144:147],42
M 86,a[148:151],42
M 90,a[152:155],42
M 82,a[156:159],42
M 86,a[160:163],42
M 90,a[164:167],42
M 82,a[168:171],42
M 86,a[172:175],42
M 90,a[176:179],42
M 82,a[180:183],42
M 86,a[184:187],42
M 90,a[188:191],42
M 82,a[192:195],42
M 86,a[196:199],42
M 90,a[200:203],42
M 82,a[204:207],42
M 86,a[208:211],42
M 90,a[212:215],42
M 82,a[216:219],42
M 86,a[220:223],42
M 90,a[224:227],42
M 82,a[228:231],42
M 86,a[232:235],42
M 90,a[236:239],42
M 82,a[240:243],42
M 86,a[244:247],42
M 90,a[248:251],42
M 82,a[252:255],42
M 86,v[128:131],42
M 90,v[132:135],42
M 82,v[136:139],42
M 86,v[140:143],42
M 90,v[144:147],42
M 82,v[148:151],42
M 86,v[152:155],42
M 90,v[156:159],42
M 82,v[160:163],42
M 86,v[164:167],42
M 90,v[168:171],42
M 82,v[172:175],42
M 86,v[176:179],42
M 90,v[180:183],42
M 82,v[184:187],42
M 86,v[188:191],42
M 90,v[192:195],42
M 82,v[196:199],42
M 86,v[200:203],42
M 90,v[204:207],42
M 82,v[208:211],42
M 86,v[212:215],42
M 90,v[216:219],42
M 82,v[220:223],42
M 86,v[224:227],42
M 90,v[228:231],42
M 82,v[232:235],42
M 86,v[236:239],42
M 90,v[240:243],42
M 82,v[244:247],42
M 86,v[248:251],42
M 90,v[252:255],42
v_mov_b32 v4,s53
s_mov_b32 s82,0
s_mov_b32 s80,0
s_mov_b32 s54,s45
s_mul_i32 s57,s44,4096
s_mul_i32 s55,s45,32768
s_add_u32 s55,s55,s57
s_add_u32 s55,s55,31457280
s_mul_i32 s57,s44,4096
s_mul_i32 s60,s46,32768
s_mul_i32 s61,s46,1024
s_mul_i32 s62,s46,114688
s_mul_i32 s63,s46,2560
s_mul_i32 s64,s46,1024
s_lshl_b32 s59,s44,8
s_add_u32 s59,s59,131072
s_mul_i32 s58,s45,1024
s_add_u32 s58,s58,109613056
s_mul_i32 s65,s45,114688
s_add_u32 s65,s65,s47
s_mul_i32 s68,s45,2560
s_add_u32 s68,s68,s48
s_mul_i32 s73,s45,1024
s_add_u32 s73,s73,107479040
s_mul_i32 s74,s45,32768
s_add_u32 s74,s74,s49
s_mul_i32 s75,s45,1024
s_add_u32 s75,s75,107479040
s_mov_b32 s66,0x7fff0000
s_mov_b32 s67,0x7fff0000
s_mov_b32 s69,0x7fff0000
s_mov_b32 s76,0x7fff0000
s_mov_b32 s77,0x7fff0000
s_mov_b32 s70,0x7fff0000
s_mov_b32 s71,0x7fff0000
s_mov_b32 s72,0x7fff0000
v_mov_b32 v1,v0
v_add_u32 v2,16384,v0
s_mov_b32 s84,16384
s_mov_b32 s86,0x3a800000
s_mov_b32 s87,0x3727c5ac
s_mov_b32 s88,0xc0135761
s_mov_b32 s89,0x3d372713
s_lshr_b32 s85,s44,1
s_mul_i32 s85,s85,127
v_lshlrev_b32 v8,22,v0
v_and_b32 v8,0x80000000,v8
v_or_b32 v8,v8,v5
v_add_u32 v2,32768,v0
s_lshl_b32 s84,s57,1
s_add_u32 s84,s84,32768
v_and_b32 v94,240,v0
v_cmp_eq_u32 vcc,0,v94
s_nop 1
v_cndmask_b32 v17,0,1.0,vcc
s_getpc_b64 s[40:41]
Lg3_pc_%=:
s_lshl_b32 s78,s44,13
v_lshlrev_b32 v94,3,v0
v_add_u32 v94,s78,v94
v_min_u32 v94,Lcode_g3_e-Lg3_pc_%=-4,v94
s_mov_b32 m0,163584
s_nop 0
global_load_lds_dword v94,s[40:41]
s_add_u32 m0,s59,4096
s_nop 0
buffer_load_dword v8,s[36:39],s58 offen offset:0 sc1 lds
buffer_store_dword v4,v3,s[36:39],s71 offen offset:0 sc0 sc1
s_add_u32 m0,s59,5120
s_nop 0
buffer_load_dword v8,s[36:39],s58 offen offset:0 sc1 lds
buffer_store_dword v4,v3,s[36:39],s71 offen offset:0 sc0 sc1
s_mov_b32 s83,0
Lg3_poll_%=:
s_add_u32 s78,s83,2
s_and_b32 s78,s78,3
s_lshl_b32 s78,s78,10
s_add_u32 s78,s78,4096
s_add_u32 m0,s59,s78
s_nop 0
buffer_load_dword v8,s[36:39],s58 offen offset:0 sc1 lds
s_and_b32 s78,s83,3
s_lshl_b32 s78,s78,10
v_add_u32 v94,s78,v6
s_waitcnt vmcnt(4)
s_barrier
ds_read_b32 v7,v94 offset:4096
buffer_store_dword v4,v3,s[36:39],s70 offen offset:0 sc0 sc1
s_mov_b32 s70,s71
s_mov_b32 s71,s72
s_mov_b32 s72,0x7fff0000
s_add_u32 s83,s83,1
s_add_u32 s82,s82,1
L 0
v_cmp_eq_u32 vcc,s53,v7
s_nop 0
s_cmp_eq_u32 vcc_lo,-1
s_cbranch_scc1 Lg3_go_%=
s_cmp_lt_u32 s82,20000
s_cbranch_scc1 Lg3_poll_%=
Lg3_go_%=:
buffer_load_dword v114,v11,s[36:39],s73 offen offset:0 sc1
s_add_u32 s73,s73,s64
s_add_u32 m0,s57,0
s_add_u32 s56,s55,0
D 0,56,0
D 0,56,1024
D 0,56,2048
D 0,56,3072
s_add_u32 s58,s58,s61
s_add_u32 m0,s59,2048
s_nop 0
buffer_load_dword v8,s[36:39],s58 offen offset:0 sc1 lds
s_add_u32 m0,s57,16384
s_add_u32 s56,s55,16384
D 0,56,0
D 0,56,1024
D 0,56,2048
D 0,56,3072
s_add_u32 s55,s55,s60
s_sub_u32 s57,s84,s57
s_add_u32 s54,s54,s46
s_waitcnt vmcnt(4)
s_barrier
buffer_store_dword v4,v3,s[36:39],s70 offen offset:0 sc0 sc1
s_min_u32 s71,s71,s72
buffer_store_dword v4,v3,s[36:39],s71 offen offset:0 sc0 sc1
s_mov_b32 s70,0x7fff0000
s_mov_b32 s71,0x7fff0000
s_mov_b32 s72,0x7fff0000
s_cmp_eq_u32 s80,1
buffer_store_dword v4,v3,s[36:39],s71 offen offset:0 sc0 sc1
s_cbranch_scc1 Lg3_st1_%=
s_branch Lg3_st0_%=
Lg3_gox_%=:
s_add_u32 m0,s59,2048
s_nop 0
buffer_load_dword v8,s[36:39],s58 offen offset:0 sc1 lds
s_waitcnt vmcnt(0)
s_barrier
buffer_store_dword v4,v3,s[36:39],s70 offen offset:0 sc0 sc1
s_min_u32 s71,s71,s72
buffer_store_dword v4,v3,s[36:39],s71 offen offset:0 sc0 sc1
s_mov_b32 s70,0x7fff0000
s_mov_b32 s71,0x7fff0000
s_mov_b32 s72,0x7fff0000
s_cmp_eq_u32 s80,1
s_cbranch_scc1 Lg3_st1_%=
Lg3_st0_%=:
ds_read_b32 v7,v6 offset:2048
R 42,1,0
R 46,1,1024
R 50,1,2048
R 54,1,3072
R 58,1,4096
L 5
v_cmp_eq_u32 vcc,s53,v7
L 3
s_cmp_eq_u32 vcc_lo,-1
s_cbranch_scc1 Lg3_F0_%=
s_branch Lg3_L0_%=
Lg3_st1_%=:
ds_read_b32 v7,v6 offset:2048
R 42,2,0
R 46,2,1024
R 50,2,2048
R 54,2,3072
R 58,2,4096
L 5
v_cmp_eq_u32 vcc,s53,v7
L 3
s_cmp_eq_u32 vcc_lo,-1
s_cbranch_scc1 Lg3_F1_%=
s_branch Lg3_L1_%=
Lg3_F0_%=:
v_mfma_f32_16x16x32_bf16 v[26:29],a[8:11],v[42:45],0
s_add_u32 m0,s57,0
R 62,1,5120
v_add_f32_dpp v115,v114,v114 quad_perm:[1,0,3,2] row_mask:0xf bank_mask:0xf
M 26,a[20:23],46
buffer_load_dword v114,v11,s[36:39],s73 offen offset:0 sc1
v_mfma_f32_16x16x32_bf16 v[22:25],a[4:7],v[42:45],0
s_add_u32 s56,s55,0
v_add_f32_dpp v115,v115,v115 quad_perm:[2,3,0,1] row_mask:0xf bank_mask:0xf
v_fma_f32 v30,-v78,v82,v30
M 22,a[16:19],46
s_add_u32 s58,s58,s61
R 66,1,6144
v_fma_f32 v31,-v78,v83,v31
v_mfma_f32_16x16x32_bf16 v[18:21],a[0:3],v[42:45],0
D 0,56,0
M 18,a[12:15],46
L 3
v_add_f32_dpp v118,v115,v115 row_half_mirror row_mask:0xf bank_mask:0xf
v_fma_f32 v32,-v78,v84,v32
M 18,a[24:27],50
s_mov_b32 s66,s65
R 70,1,7168
v_fma_f32 v33,-v78,v85,v33
M 18,a[36:39],54
s_add_u32 s65,s65,s62
v_mov_b32_dpp v115,v118 row_ror:8 row_mask:0xf bank_mask:0xf
v_fma_f32 v115,-v118,v118,v115
M 22,a[28:31],50
s_add_u32 s54,s54,s46
v_max_f32 v115,s87,v115
v_mul_f32 v118,v17,v118
M 22,a[40:43],54
s_add_u32 s73,s73,s64
R 42,1,8192
v_mul_f32 v94,v30,v30
M 26,a[32:35],50
v_rsq_f32 v116,v115
M 26,a[44:47],54
L 3
D 0,56,1024
M 26,a[56:59],58
R 46,1,9216
v_fma_f32 v115,v15,v115,v16
M 26,a[68:71],62
v_mul_f32 v119,v116,v116
v_fma_f32 v115,v14,v119,v115
M 22,a[52:55],58
v_fma_f32 v118,v116,v115,v118
v_mul_f32 v95,v31,v31
M 22,a[64:67],62
R 50,1,10240
v_mul_f32 v96,v32,v32
M 18,a[48:51],58
ds_write_b32 v12,v118 offset:0
M 18,a[60:63],62
L 4
v_mul_f32 v97,v33,v33
v_fma_f32 v94,v80,v94,v79
M 18,a[72:75],66
R 54,1,11264
v_fma_f32 v95,v80,v95,v79
M 18,a[84:87],70
v_fma_f32 v96,v80,v96,v79
v_fma_f32 v97,v80,v97,v79
M 22,a[76:79],66
v_mul_f32 v94,v30,v94
v_mul_f32 v95,v31,v95
M 22,a[88:91],70
R 58,1,12288
v_mul_f32 v96,v32,v96
M 26,a[80:83],66
D 0,56,2048
M 26,a[92:95],70
L 4
v_exp_f32 v94,v94
M 26,a[104:107],42
R 62,1,13312
v_mul_f32 v97,v33,v97
M 26,a[116:119],46
v_exp_f32 v95,v95
M 22,a[100:103],42
v_exp_f32 v96,v96
M 22,a[112:115],46
R 66,1,14336
v_fma_f32 v94,v94,v81,v81
M 18,a[96:99],42
v_exp_f32 v97,v97
M 18,a[108:111],46
L 3
v_rcp_f32 v94,v94
M 18,a[120:123],50
)"
R"(R 70,1,15360
v_fma_f32 v95,v95,v81,v81
M 18,a[132:135],54
s_waitcnt vmcnt(7)
D 0,56,3072
M 22,a[124:127],50
s_barrier
v_rcp_f32 v95,v95
M 22,a[136:139],54
s_add_u32 m0,s59,0
R 42,1,16384
v_fma_f32 v96,v96,v81,v81
M 26,a[128:131],50
s_add_u32 s56,s55,16384
v_rcp_f32 v96,v96
M 26,a[140:143],54
L 3
v_fma_f32 v97,v97,v81,v81
v_mul_f32 v30,v30,v94
M 26,a[152:155],58
s_add_u32 s55,s55,s60
R 46,1,17408
v_mul_f32 v31,v31,v95
M 26,a[164:167],62
v_rcp_f32 v97,v97
M 22,a[148:151],58
v_mul_f32 v32,v32,v96
v_mul_f32 v33,v33,v97
M 22,a[160:163],62
R 50,1,18432
v_cvt_pk_bf16_f32 v106,v30,v31
M 18,a[144:147],58
buffer_load_dword v8,s[36:39],s58 offen offset:0 sc1 lds
M 18,a[156:159],62
L 3
v_cvt_pk_bf16_f32 v107,v32,v33
v_fma_f32 v34,-v78,v86,v34
M 18,a[168:171],66
s_add_u32 m0,s57,16384
R 54,1,19456
v_fma_f32 v35,-v78,v87,v35
M 18,a[180:183],70
s_sub_u32 s57,s84,s57
v_fma_f32 v36,-v78,v88,v36
v_fma_f32 v37,-v78,v89,v37
M 22,a[172:175],66
v_mul_f32 v98,v34,v34
v_mul_f32 v99,v35,v35
M 22,a[184:187],70
R 58,1,20480
v_mul_f32 v100,v36,v36
M 26,a[176:179],66
D 0,56,0
M 26,a[188:191],70
L 3
v_mul_f32 v101,v37,v37
v_fma_f32 v98,v80,v98,v79
M 26,a[200:203],42
R 62,1,21504
v_fma_f32 v99,v80,v99,v79
M 26,a[212:215],46
v_fma_f32 v100,v80,v100,v79
v_fma_f32 v101,v80,v101,v79
M 22,a[196:199],42
v_mul_f32 v98,v34,v98
v_mul_f32 v99,v35,v99
M 22,a[208:211],46
R 66,1,22528
v_mul_f32 v100,v36,v100
M 18,a[192:195],42
v_exp_f32 v98,v98
M 18,a[204:207],46
L 3
v_exp_f32 v99,v99
M 18,a[216:219],50
R 70,1,23552
v_mul_f32 v101,v37,v101
M 18,a[228:231],54
D 0,56,1024
M 22,a[220:223],50
v_exp_f32 v100,v100
M 22,a[232:235],54
R 42,1,24576
v_fma_f32 v98,v98,v81,v81
M 26,a[224:227],50
v_exp_f32 v101,v101
M 26,a[236:239],54
L 3
v_rcp_f32 v98,v98
M 26,a[248:251],58
R 46,1,25600
v_fma_f32 v99,v99,v81,v81
M 26,v[132:135],62
v_rcp_f32 v99,v99
M 22,a[244:247],58
v_fma_f32 v100,v100,v81,v81
v_fma_f32 v101,v101,v81,v81
M 22,v[128:131],62
R 50,1,26624
v_mul_f32 v34,v34,v98
M 18,a[240:243],58
v_rcp_f32 v100,v100
M 18,a[252:255],62
L 3
v_rcp_f32 v101,v101
M 18,v[136:139],66
R 54,1,27648
v_mul_f32 v35,v35,v99
M 18,v[148:151],70
D 0,56,2048
M 22,v[140:143],66
v_mul_f32 v36,v36,v100
v_mul_f32 v37,v37,v101
M 22,v[152:155],70
R 58,1,28672
v_cvt_pk_bf16_f32 v108,v34,v35
M 26,v[144:147],66
v_cvt_pk_bf16_f32 v109,v36,v37
v_fma_f32 v38,-v78,v90,v38
M 26,v[156:159],70
L 3
v_fma_f32 v39,-v78,v91,v39
v_fma_f32 v40,-v78,v92,v40
M 26,v[168:171],42
R 62,1,29696
v_fma_f32 v41,-v78,v93,v41
M 26,v[180:183],46
v_mul_f32 v102,v38,v38
v_mul_f32 v103,v39,v39
M 22,v[164:167],42
v_mul_f32 v104,v40,v40
v_mul_f32 v105,v41,v41
M 22,v[176:179],46
R 66,1,30720
v_fma_f32 v102,v80,v102,v79
M 18,v[160:163],42
v_fma_f32 v103,v80,v103,v79
v_fma_f32 v104,v80,v104,v79
M 18,v[172:175],46
L 3
D 0,56,3072
M 18,v[184:187],50
s_nop 0
R 70,1,31744
v_fma_f32 v105,v80,v105,v79
M 18,v[196:199],54
s_waitcnt vmcnt(4)
v_mul_f32 v102,v38,v102
v_mul_f32 v103,v39,v103
M 22,v[188:191],50
s_barrier
buffer_store_dword v4,v3,s[36:39],s70 offen offset:0 sc0 sc1
M 22,v[200:203],54
s_mov_b32 s70,s69
R 42,2,0
ds_read_b32 v7,v6 offset:0
M 26,v[192:195],50
s_mov_b32 s69,s68
R 74,13,0
v_mul_f32 v104,v40,v104
M 26,v[204:207],54
L 5
v_exp_f32 v102,v102
M 26,v[216:219],58
s_add_u32 s68,s68,s63
R 46,2,1024
v_mul_f32 v105,v41,v105
M 26,v[228:231],62
v_exp_f32 v103,v103
M 22,v[212:215],58
buffer_store_dwordx4 v[106:109],v0,s[36:39],s67 offen offset:0 sc0 sc1
v_fma_f32 v102,v102,v81,v81
M 22,v[224:227],62
L 2
R 50,2,2048
v_fma_f32 v103,v103,v81,v81
M 18,v[208:211],58
v_exp_f32 v104,v104
M 18,v[220:223],62
s_nop 0
v_exp_f32 v105,v105
M 18,v[232:235],66
R 54,2,3072
v_fma_f32 v104,v104,v81,v81
M 18,v[244:247],70
v_fma_f32 v105,v105,v81,v81
v_cmp_eq_u32 vcc,s53,v7
M 22,v[236:239],66
v_rcp_f32 v102,v102
v_rcp_f32 v103,v103
v_rcp_f32 v104,v104
v_rcp_f32 v105,v105
M 22,v[248:251],70
s_cmp_eq_u32 vcc_lo,-1
R 58,2,4096
v_mul_f32 v38,v38,v102
v_mul_f32 v39,v39,v103
v_mul_f32 v40,v40,v104
v_mul_f32 v41,v41,v105
v_cvt_pk_bf16_f32 v110,v38,v39
v_cvt_pk_bf16_f32 v111,v40,v41
M 26,v[240:243],66
L 3
buffer_store_dwordx2 v[110:111],v10,s[36:39],s67 offen offset:0 sc0 sc1
M 26,v[252:255],70
s_cbranch_scc0 Lg3_K1_%=
Lg3_F1_%=:
v_mfma_f32_16x16x32_bf16 v[38:41],a[8:11],v[42:45],0
s_add_u32 m0,s57,0
R 62,2,5120
v_add_f32_dpp v115,v114,v114 quad_perm:[1,0,3,2] row_mask:0xf bank_mask:0xf
M 38,a[20:23],46
buffer_load_dword v114,v11,s[36:39],s73 offen offset:0 sc1
v_mfma_f32_16x16x32_bf16 v[34:37],a[4:7],v[42:45],0
s_add_u32 s56,s55,0
v_add_f32_dpp v115,v115,v115 quad_perm:[2,3,0,1] row_mask:0xf bank_mask:0xf
v_fma_f32 v18,-v74,v82,v18
M 34,a[16:19],46
s_add_u32 s58,s58,s61
R 66,2,6144
v_fma_f32 v19,-v74,v83,v19
v_mfma_f32_16x16x32_bf16 v[30:33],a[0:3],v[42:45],0
D 0,56,0
M 30,a[12:15],46
L 3
v_add_f32_dpp v118,v115,v115 row_half_mirror row_mask:0xf bank_mask:0xf
v_fma_f32 v20,-v74,v84,v20
M 30,a[24:27],50
s_mov_b32 s67,s65
R 70,2,7168
v_fma_f32 v21,-v74,v85,v21
M 30,a[36:39],54
s_add_u32 s65,s65,s62
v_mov_b32_dpp v115,v118 row_ror:8 row_mask:0xf bank_mask:0xf
v_fma_f32 v115,-v118,v118,v115
M 34,a[28:31],50
s_add_u32 s54,s54,s46
v_max_f32 v115,s87,v115
v_mul_f32 v118,v17,v118
M 34,a[40:43],54
s_add_u32 s73,s73,s64
R 42,2,8192
v_mul_f32 v94,v18,v18
M 38,a[32:35],50
v_rsq_f32 v116,v115
M 38,a[44:47],54
L 3
D 0,56,1024
M 38,a[56:59],58
R 46,2,9216
v_fma_f32 v115,v15,v115,v16
M 38,a[68:71],62
v_mul_f32 v119,v116,v116
v_fma_f32 v115,v14,v119,v115
M 34,a[52:55],58
v_fma_f32 v118,v116,v115,v118
v_mul_f32 v95,v19,v19
M 34,a[64:67],62
R 50,2,10240
v_mul_f32 v96,v20,v20
M 30,a[48:51],58
ds_write_b32 v12,v118 offset:2048
M 30,a[60:63],62
L 4
v_mul_f32 v97,v21,v21
v_fma_f32 v94,v76,v94,v75
M 30,a[72:75],66
R 54,2,11264
v_fma_f32 v95,v76,v95,v75
M 30,a[84:87],70
v_fma_f32 v96,v76,v96,v75
v_fma_f32 v97,v76,v97,v75
M 34,a[76:79],66
v_mul_f32 v94,v18,v94
v_mul_f32 v95,v19,v95
M 34,a[88:91],70
R 58,2,12288
v_mul_f32 v96,v20,v96
M 38,a[80:83],66
D 0,56,2048
M 38,a[92:95],70
L 4
v_exp_f32 v94,v94
M 38,a[104:107],42
R 62,2,13312
v_mul_f32 v97,v21,v97
M 38,a[116:119],46
v_exp_f32 v95,v95
M 34,a[100:103],42
v_exp_f32 v96,v96
M 34,a[112:115],46
R 66,2,14336
v_fma_f32 v94,v94,v77,v77
M 30,a[96:99],42
v_exp_f32 v97,v97
M 30,a[108:111],46
L 3
v_rcp_f32 v94,v94
M 30,a[120:123],50
R 70,2,15360
v_fma_f32 v95,v95,v77,v77
M 30,a[132:135],54
s_waitcnt vmcnt(7)
D 0,56,3072
M 34,a[124:127],50
s_barrier
v_rcp_f32 v95,v95
M 34,a[136:139],54
s_add_u32 m0,s59,1024
R 42,2,16384
v_fma_f32 v96,v96,v77,v77
M 38,a[128:131],50
s_add_u32 s56,s55,16384
v_rcp_f32 v96,v96
M 38,a[140:143],54
L 3
v_fma_f32 v97,v97,v77,v77
v_mul_f32 v18,v18,v94
M 38,a[152:155],58
s_add_u32 s55,s55,s60
R 46,2,17408
v_mul_f32 v19,v19,v95
M 38,a[164:167],62
v_rcp_f32 v97,v97
M 34,a[148:151],58
v_mul_f32 v20,v20,v96
v_mul_f32 v21,v21,v97
M 34,a[160:163],62
R 50,2,18432
v_cvt_pk_bf16_f32 v106,v18,v19
M 30,a[144:147],58
buffer_load_dword v8,s[36:39],s58 offen offset:0 sc1 lds
M 30,a[156:159],62
L 3
v_cvt_pk_bf16_f32 v107,v20,v21
v_fma_f32 v22,-v74,v86,v22
M 30,a[168:171],66
s_add_u32 m0,s57,16384
R 54,2,19456
v_fma_f32 v23,-v74,v87,v23
M 30,a[180:183],70
s_sub_u32 s57,s84,s57
v_fma_f32 v24,-v74,v88,v24
v_fma_f32 v25,-v74,v89,v25
M 34,a[172:175],66
v_mul_f32 v98,v22,v22
v_mul_f32 v99,v23,v23
M 34,a[184:187],70
R 58,2,20480
v_mul_f32 v100,v24,v24
M 38,a[176:179],66
D 0,56,0
M 38,a[188:191],70
L 3
v_mul_f32 v101,v25,v25
v_fma_f32 v98,v76,v98,v75
M 38,a[200:203],42
R 62,2,21504
v_fma_f32 v99,v76,v99,v75
M 38,a[212:215],46
v_fma_f32 v100,v76,v100,v75
v_fma_f32 v101,v76,v101,v75
M 34,a[196:199],42
v_mul_f32 v98,v22,v98
v_mul_f32 v99,v23,v99
M 34,a[208:211],46
R 66,2,22528
v_mul_f32 v100,v24,v100
M 30,a[192:195],42
v_exp_f32 v98,v98
M 30,a[204:207],46
L 3
v_exp_f32 v99,v99
M 30,a[216:219],50
R 70,2,23552
v_mul_f32 v101,v25,v101
M 30,a[228:231],54
D 0,56,1024
M 34,a[220:223],50
v_exp_f32 v100,v100
M 34,a[232:235],54
R 42,2,24576
v_fma_f32 v98,v98,v77,v77
M 38,a[224:227],50
v_exp_f32 v101,v101
M 38,a[236:239],54
L 3
v_rcp_f32 v98,v98
M 38,a[248:251],58
R 46,2,25600
v_fma_f32 v99,v99,v77,v77
M 38,v[132:135],62
v_rcp_f32 v99,v99
M 34,a[244:247],58
v_fma_f32 v100,v100,v77,v77
v_fma_f32 v101,v101,v77,v77
M 34,v[128:131],62
R 50,2,26624
v_mul_f32 v22,v22,v98
M 30,a[240:243],58
v_rcp_f32 v100,v100
M 30,a[252:255],62
L 3
v_rcp_f32 v101,v101
M 30,v[136:139],66
R 54,2,27648
v_mul_f32 v23,v23,v99
M 30,v[148:151],70
D 0,56,2048
M 34,v[140:143],66
v_mul_f32 v24,v24,v100
v_mul_f32 v25,v25,v101
M 34,v[152:155],70
R 58,2,28672
v_cvt_pk_bf16_f32 v108,v22,v23
M 38,v[144:147],66
v_cvt_pk_bf16_f32 v109,v24,v25
v_fma_f32 v26,-v74,v90,v26
M 38,v[156:159],70
L 3
v_fma_f32 v27,-v74,v91,v27
v_fma_f32 v28,-v74,v92,v28
M 38,v[168:171],42
R 62,2,29696
v_fma_f32 v29,-v74,v93,v29
M 38,v[180:183],46
v_mul_f32 v102,v26,v26
v_mul_f32 v103,v27,v27
M 34,v[164:167],42
v_mul_f32 v104,v28,v28
v_mul_f32 v105,v29,v29
M 34,v[176:179],46
R 66,2,30720
v_fma_f32 v102,v76,v102,v75
M 30,v[160:163],42
v_fma_f32 v103,v76,v103,v75
v_fma_f32 v104,v76,v104,v75
M 30,v[172:175],46
L 3
D 0,56,3072
M 30,v[184:187],50
s_nop 0
R 70,2,31744
v_fma_f32 v105,v76,v105,v75
M 30,v[196:199],54
s_waitcnt vmcnt(4)
v_mul_f32 v102,v26,v102
v_mul_f32 v103,v27,v103
M 34,v[188:191],50
s_barrier
buffer_store_dword v4,v3,s[36:39],s70 offen offset:0 sc0 sc1
M 34,v[200:203],54
s_mov_b32 s70,s69
R 42,1,0
ds_read_b32 v7,v6 offset:1024
M 38,v[192:195],50
s_mov_b32 s69,s68
R 78,13,2048
v_mul_f32 v104,v28,v104
M 38,v[204:207],54
L 5
v_exp_f32 v102,v102
M 38,v[216:219],58
s_add_u32 s68,s68,s63
R 46,1,1024
v_mul_f32 v105,v29,v105
M 38,v[228:231],62
v_exp_f32 v103,v103
M 34,v[212:215],58
buffer_store_dwordx4 v[106:109],v0,s[36:39],s66 offen offset:0 sc0 sc1
v_fma_f32 v102,v102,v77,v77
M 34,v[224:227],62
L 2
R 50,1,2048
v_fma_f32 v103,v103,v77,v77
M 30,v[208:211],58
v_exp_f32 v104,v104
M 30,v[220:223],62
s_nop 0
v_exp_f32 v105,v105
M 30,v[232:235],66
R 54,1,3072
v_fma_f32 v104,v104,v77,v77
M 30,v[244:247],70
v_fma_f32 v105,v105,v77,v77
v_cmp_eq_u32 vcc,s53,v7
M 34,v[236:239],66
v_rcp_f32 v102,v102
v_rcp_f32 v103,v103
v_rcp_f32 v104,v104
v_rcp_f32 v105,v105
M 34,v[248:251],70
s_cmp_eq_u32 vcc_lo,-1
R 58,1,4096
v_mul_f32 v26,v26,v102
v_mul_f32 v27,v27,v103
v_mul_f32 v28,v28,v104
v_mul_f32 v29,v29,v105
v_cvt_pk_bf16_f32 v110,v26,v27
v_cvt_pk_bf16_f32 v111,v28,v29
)"
R"(M 38,v[240:243],66
L 3
buffer_store_dwordx2 v[110:111],v10,s[36:39],s66 offen offset:0 sc0 sc1
M 38,v[252:255],70
s_cbranch_scc1 Lg3_F0_%=
Lg3_K0_%=:
Lg3_L0_%=:
v_mfma_f32_16x16x32_bf16 v[26:29],a[8:11],v[42:45],0
s_add_u32 m0,s59,3072
R 62,1,5120
v_add_f32_dpp v115,v114,v114 quad_perm:[1,0,3,2] row_mask:0xf bank_mask:0xf
M 26,a[20:23],46
buffer_load_dword v8,s[36:39],s58 offen offset:0 sc1 lds
v_mfma_f32_16x16x32_bf16 v[22:25],a[4:7],v[42:45],0
s_mov_b32 s66,s65
v_add_f32_dpp v115,v115,v115 quad_perm:[2,3,0,1] row_mask:0xf bank_mask:0xf
v_fma_f32 v30,-v78,v82,v30
M 22,a[16:19],46
s_add_u32 s65,s65,s62
R 66,1,6144
v_fma_f32 v31,-v78,v83,v31
v_mfma_f32_16x16x32_bf16 v[18:21],a[0:3],v[42:45],0
v_add_f32_dpp v118,v115,v115 row_half_mirror row_mask:0xf bank_mask:0xf
v_fma_f32 v32,-v78,v84,v32
v_fma_f32 v33,-v78,v85,v33
M 18,a[12:15],46
L 3
v_mul_f32 v94,v30,v30
v_mul_f32 v95,v31,v31
M 18,a[24:27],50
R 70,1,7168
v_mov_b32_dpp v115,v118 row_ror:8 row_mask:0xf bank_mask:0xf
M 18,a[36:39],54
v_fma_f32 v115,-v118,v118,v115
v_max_f32 v115,s87,v115
M 22,a[28:31],50
v_rsq_f32 v116,v115
M 22,a[40:43],54
R 42,1,8192
v_fma_f32 v115,v15,v115,v16
v_mul_f32 v118,v17,v118
M 26,a[32:35],50
v_mul_f32 v119,v116,v116
v_fma_f32 v115,v14,v119,v115
M 26,a[44:47],54
L 3
v_fma_f32 v118,v116,v115,v118
v_mul_f32 v96,v32,v32
M 26,a[56:59],58
R 46,1,9216
v_mul_f32 v97,v33,v33
M 26,a[68:71],62
ds_write_b32 v12,v118 offset:0
M 22,a[52:55],58
v_fma_f32 v94,v80,v94,v79
v_fma_f32 v95,v80,v95,v79
v_fma_f32 v96,v80,v96,v79
M 22,a[64:67],62
R 50,1,10240
v_fma_f32 v97,v80,v97,v79
M 18,a[48:51],58
v_mul_f32 v94,v30,v94
v_mul_f32 v95,v31,v95
M 18,a[60:63],62
L 4
v_exp_f32 v94,v94
M 18,a[72:75],66
R 54,1,11264
v_mul_f32 v96,v32,v96
M 18,a[84:87],70
v_exp_f32 v95,v95
v_mul_f32 v97,v33,v97
M 22,a[76:79],66
v_exp_f32 v96,v96
M 22,a[88:91],70
R 58,1,12288
v_fma_f32 v94,v94,v81,v81
M 26,a[80:83],66
v_exp_f32 v97,v97
M 26,a[92:95],70
L 4
v_rcp_f32 v94,v94
M 26,a[104:107],42
R 62,1,13312
v_fma_f32 v95,v95,v81,v81
v_fma_f32 v96,v96,v81,v81
M 26,a[116:119],46
v_rcp_f32 v95,v95
M 22,a[100:103],42
v_rcp_f32 v96,v96
M 22,a[112:115],46
R 66,1,14336
v_fma_f32 v97,v97,v81,v81
M 18,a[96:99],42
v_rcp_f32 v97,v97
M 18,a[108:111],46
L 3
v_mul_f32 v30,v30,v94
v_mul_f32 v31,v31,v95
v_mul_f32 v32,v32,v96
M 18,a[120:123],50
R 70,1,15360
v_mul_f32 v33,v33,v97
M 18,a[132:135],54
s_waitcnt vmcnt(0)
v_cvt_pk_bf16_f32 v106,v30,v31
v_cvt_pk_bf16_f32 v107,v32,v33
M 22,a[124:127],50
s_barrier
ds_read_b32 v7,v6 offset:3072
v_fma_f32 v34,-v78,v86,v34
M 22,a[136:139],54
R 42,1,16384
v_fma_f32 v35,-v78,v87,v35
M 26,a[128:131],50
buffer_store_dword v4,v3,s[36:39],s70 offen offset:0 sc0 sc1
v_fma_f32 v36,-v78,v88,v36
M 26,a[140:143],54
L 4
v_fma_f32 v37,-v78,v89,v37
v_mul_f32 v98,v34,v34
M 26,a[152:155],58
s_mov_b32 s70,0x7fff0000
R 46,1,17408
v_mul_f32 v99,v35,v35
M 26,a[164:167],62
v_mul_f32 v100,v36,v36
v_mul_f32 v101,v37,v37
M 22,a[148:151],58
L 2
v_cmp_eq_u32 vcc,s53,v7
v_fma_f32 v98,v80,v98,v79
M 22,a[160:163],62
R 50,1,18432
v_fma_f32 v99,v80,v99,v79
v_fma_f32 v100,v80,v100,v79
M 18,a[144:147],58
v_fma_f32 v101,v80,v101,v79
v_mul_f32 v98,v34,v98
M 18,a[156:159],62
s_cmp_eq_u32 vcc_lo,-1
s_nop 0
v_mul_f32 v99,v35,v99
s_cbranch_scc0 Lg3_X0_%=
M 18,a[168:171],66
s_add_u32 m0,s57,0
s_add_u32 s56,s55,0
s_add_u32 s58,s58,s61
R 54,1,19456
M 18,a[180:183],70
buffer_load_dword v114,v11,s[36:39],s73 offen offset:0 sc1
M 22,a[172:175],66
D 0,56,0
v_mul_f32 v100,v36,v100
M 22,a[184:187],70
s_add_u32 s54,s54,s46
R 58,1,20480
v_mul_f32 v101,v37,v101
M 26,a[176:179],66
s_add_u32 s73,s73,s64
v_exp_f32 v98,v98
M 26,a[188:191],70
L 3
v_exp_f32 v99,v99
M 26,a[200:203],42
R 62,1,21504
v_fma_f32 v98,v98,v81,v81
M 26,a[212:215],46
D 0,56,1024
v_fma_f32 v99,v99,v81,v81
M 22,a[196:199],42
v_exp_f32 v100,v100
M 22,a[208:211],46
R 66,1,22528
v_fma_f32 v100,v100,v81,v81
M 18,a[192:195],42
D 0,56,2048
M 18,a[204:207],46
L 3
v_exp_f32 v101,v101
M 18,a[216:219],50
R 70,1,23552
v_rcp_f32 v98,v98
M 18,a[228:231],54
v_rcp_f32 v99,v99
M 22,a[220:223],50
D 0,56,3072
M 22,a[232:235],54
s_add_u32 m0,s59,0
R 42,1,24576
v_fma_f32 v101,v101,v81,v81
M 26,a[224:227],50
buffer_load_dword v8,s[36:39],s58 offen offset:0 sc1 lds
M 26,a[236:239],54
L 3
v_rcp_f32 v100,v100
v_mul_f32 v34,v34,v98
M 26,a[248:251],58
s_add_u32 m0,s57,16384
R 46,1,25600
v_mul_f32 v35,v35,v99
M 26,v[132:135],62
s_add_u32 s56,s55,16384
v_rcp_f32 v101,v101
M 22,a[244:247],58
D 0,56,0
M 22,v[128:131],62
s_add_u32 s55,s55,s60
R 50,1,26624
v_mul_f32 v36,v36,v100
M 18,a[240:243],58
s_sub_u32 s57,s84,s57
v_mul_f32 v37,v37,v101
v_cvt_pk_bf16_f32 v108,v34,v35
v_cvt_pk_bf16_f32 v109,v36,v37
M 18,a[252:255],62
L 3
buffer_store_dwordx4 v[106:109],v0,s[36:39],s67 offen offset:0 sc0 sc1
v_fma_f32 v38,-v78,v90,v38
M 18,v[136:139],66
R 54,1,27648
v_fma_f32 v39,-v78,v91,v39
M 18,v[148:151],70
v_fma_f32 v40,-v78,v92,v40
v_fma_f32 v41,-v78,v93,v41
M 22,v[140:143],66
D 0,56,1024
M 22,v[152:155],70
R 58,1,28672
v_mul_f32 v102,v38,v38
v_mul_f32 v103,v39,v39
M 26,v[144:147],66
v_mul_f32 v104,v40,v40
v_mul_f32 v105,v41,v41
M 26,v[156:159],70
L 3
v_fma_f32 v102,v80,v102,v79
v_fma_f32 v103,v80,v103,v79
M 26,v[168:171],42
R 62,1,29696
v_fma_f32 v104,v80,v104,v79
M 26,v[180:183],46
D 0,56,2048
M 22,v[164:167],42
v_fma_f32 v105,v80,v105,v79
v_mul_f32 v102,v38,v102
v_mul_f32 v103,v39,v103
M 22,v[176:179],46
R 66,1,30720
v_mul_f32 v104,v40,v104
M 18,v[160:163],42
v_exp_f32 v102,v102
M 18,v[172:175],46
L 3
D 0,56,3072
M 18,v[184:187],50
s_nop 0
R 70,1,31744
v_mul_f32 v105,v41,v105
M 18,v[196:199],54
s_waitcnt vmcnt(5)
v_exp_f32 v103,v103
v_fma_f32 v102,v102,v81,v81
M 22,v[188:191],50
s_barrier
buffer_store_dword v4,v3,s[36:39],s70 offen offset:0 sc0 sc1
M 22,v[200:203],54
s_mov_b32 s70,s69
R 42,2,0
ds_read_b32 v7,v6 offset:0
M 26,v[192:195],50
s_mov_b32 s69,s68
R 74,13,0
v_fma_f32 v103,v103,v81,v81
M 26,v[204:207],54
L 5
v_exp_f32 v104,v104
M 26,v[216:219],58
s_add_u32 s68,s68,s63
R 46,2,1024
v_exp_f32 v105,v105
M 26,v[228:231],62
v_rcp_f32 v102,v102
M 22,v[212:215],58
v_rcp_f32 v103,v103
M 22,v[224:227],62
L 2
R 50,2,2048
v_fma_f32 v104,v104,v81,v81
M 18,v[208:211],58
v_rcp_f32 v104,v104
M 18,v[220:223],62
s_nop 0
v_fma_f32 v105,v105,v81,v81
v_rcp_f32 v105,v105
M 18,v[232:235],66
R 54,2,3072
v_mul_f32 v38,v38,v102
M 18,v[244:247],70
v_mul_f32 v39,v39,v103
v_mul_f32 v40,v40,v104
M 22,v[236:239],66
v_cmp_eq_u32 vcc,s53,v7
v_mul_f32 v41,v41,v105
M 22,v[248:251],70
R 58,2,4096
v_cvt_pk_bf16_f32 v110,v38,v39
v_cvt_pk_bf16_f32 v111,v40,v41
M 26,v[240:243],66
L 3
s_cmp_eq_u32 vcc_lo,-1
buffer_store_dwordx2 v[110:111],v10,s[36:39],s67 offen offset:0 sc0 sc1
M 26,v[252:255],70
buffer_store_dword v4,v3,s[36:39],s71 offen offset:0 sc0 sc1
s_cbranch_scc1 Lg3_F1_%=
s_branch Lg3_K1_%=
Lg3_X0_%=:
M 18,a[168:171],66
s_add_u32 m0,s59,0
R 54,1,19456
v_mul_f32 v100,v36,v100
M 18,a[180:183],70
buffer_load_dword v8,s[36:39],s58 offen offset:0 sc1 lds
M 22,a[172:175],66
v_exp_f32 v98,v98
M 22,a[184:187],70
R 58,1,20480
v_mul_f32 v101,v37,v101
M 26,a[176:179],66
v_exp_f32 v99,v99
M 26,a[188:191],70
L 3
v_exp_f32 v100,v100
M 26,a[200:203],42
R 62,1,21504
v_fma_f32 v98,v98,v81,v81
M 26,a[212:215],46
v_exp_f32 v101,v101
M 22,a[196:199],42
v_rcp_f32 v98,v98
M 22,a[208:211],46
R 66,1,22528
v_fma_f32 v99,v99,v81,v81
M 18,a[192:195],42
v_rcp_f32 v99,v99
M 18,a[204:207],46
L 3
v_fma_f32 v100,v100,v81,v81
v_fma_f32 v101,v101,v81,v81
M 18,a[216:219],50
R 70,1,23552
v_mul_f32 v34,v34,v98
M 18,a[228:231],54
v_rcp_f32 v100,v100
M 22,a[220:223],50
v_rcp_f32 v101,v101
M 22,a[232:235],54
R 42,1,24576
v_mul_f32 v35,v35,v99
M 26,a[224:227],50
v_mul_f32 v36,v36,v100
v_mul_f32 v37,v37,v101
M 26,a[236:239],54
L 3
v_cvt_pk_bf16_f32 v108,v34,v35
v_cvt_pk_bf16_f32 v109,v36,v37
M 26,a[248:251],58
R 46,1,25600
v_fma_f32 v38,-v78,v90,v38
M 26,v[132:135],62
buffer_store_dwordx4 v[106:109],v0,s[36:39],s67 offen offset:0 sc0 sc1
v_fma_f32 v39,-v78,v91,v39
M 22,a[244:247],58
v_fma_f32 v40,-v78,v92,v40
v_fma_f32 v41,-v78,v93,v41
M 22,v[128:131],62
R 50,1,26624
v_mul_f32 v102,v38,v38
M 18,a[240:243],58
v_mul_f32 v103,v39,v39
v_mul_f32 v104,v40,v40
M 18,a[252:255],62
L 3
v_mul_f32 v105,v41,v41
v_fma_f32 v102,v80,v102,v79
M 18,v[136:139],66
R 54,1,27648
v_fma_f32 v103,v80,v103,v79
M 18,v[148:151],70
v_fma_f32 v104,v80,v104,v79
v_fma_f32 v105,v80,v105,v79
M 22,v[140:143],66
v_mul_f32 v102,v38,v102
v_mul_f32 v103,v39,v103
M 22,v[152:155],70
R 58,1,28672
v_mul_f32 v104,v40,v104
M 26,v[144:147],66
v_exp_f32 v102,v102
M 26,v[156:159],70
L 3
v_exp_f32 v103,v103
M 26,v[168:171],42
R 62,1,29696
v_mul_f32 v105,v41,v105
M 26,v[180:183],46
v_exp_f32 v104,v104
M 22,v[164:167],42
v_exp_f32 v105,v105
M 22,v[176:179],46
R 66,1,30720
v_fma_f32 v102,v102,v81,v81
M 18,v[160:163],42
v_rcp_f32 v102,v102
M 18,v[172:175],46
L 3
v_fma_f32 v103,v103,v81,v81
v_fma_f32 v104,v104,v81,v81
M 18,v[184:187],50
s_nop 0
R 70,1,31744
v_fma_f32 v105,v105,v81,v81
M 18,v[196:199],54
s_waitcnt vmcnt(1)
v_rcp_f32 v103,v103
M 22,v[188:191],50
s_barrier
buffer_store_dword v4,v3,s[36:39],s70 offen offset:0 sc0 sc1
M 22,v[200:203],54
s_mov_b32 s70,s69
ds_read_b32 v7,v6 offset:0
R 74,13,0
M 26,v[192:195],50
s_mov_b32 s69,s68
v_rcp_f32 v104,v104
M 26,v[204:207],54
L 4
v_rcp_f32 v105,v105
M 26,v[216:219],58
s_add_u32 s68,s68,s63
v_mul_f32 v38,v38,v102
v_mul_f32 v39,v39,v103
M 26,v[228:231],62
v_mul_f32 v40,v40,v104
v_mul_f32 v41,v41,v105
M 22,v[212:215],58
v_cvt_pk_bf16_f32 v110,v38,v39
v_cvt_pk_bf16_f32 v111,v40,v41
M 22,v[224:227],62
L 1
buffer_store_dwordx2 v[110:111],v10,s[36:39],s67 offen offset:0 sc0 sc1
M 18,v[208:211],58
v_cmp_eq_u32 vcc,s53,v7
M 18,v[220:223],62
s_nop 0
M 18,v[232:235],66
M 18,v[244:247],70
M 22,v[236:239],66
M 22,v[248:251],70
M 26,v[240:243],66
M 26,v[252:255],70
s_mov_b32 s80,1
s_cmp_eq_u32 vcc_lo,-1
s_cbranch_scc0 Lg3_xn0_%=
buffer_load_dword v114,v11,s[36:39],s73 offen offset:0 sc1
L 0
v_fma_f32 v18,-v74,v82,v18
v_fma_f32 v19,-v74,v83,v19
v_fma_f32 v20,-v74,v84,v20
v_fma_f32 v21,-v74,v85,v21
v_mul_f32 v94,v18,v18
v_mul_f32 v95,v19,v19
v_mul_f32 v96,v20,v20
v_mul_f32 v97,v21,v21
s_add_u32 s73,s73,s64
s_add_u32 m0,s57,0
s_add_u32 s56,s55,0
D 0,56,0
v_fma_f32 v94,v76,v94,v75
v_fma_f32 v95,v76,v95,v75
v_fma_f32 v96,v76,v96,v75
v_fma_f32 v97,v76,v97,v75
v_mul_f32 v94,v18,v94
v_mul_f32 v95,v19,v95
v_mul_f32 v96,v20,v96
v_mul_f32 v97,v21,v97
D 0,56,1024
v_exp_f32 v94,v94
v_exp_f32 v95,v95
v_exp_f32 v96,v96
v_exp_f32 v97,v97
v_fma_f32 v94,v94,v77,v77
v_fma_f32 v95,v95,v77,v77
v_fma_f32 v96,v96,v77,v77
v_fma_f32 v97,v97,v77,v77
D 0,56,2048
v_rcp_f32 v94,v94
v_rcp_f32 v95,v95
v_rcp_f32 v96,v96
v_rcp_f32 v97,v97
)"
R"(v_mul_f32 v18,v18,v94
v_mul_f32 v19,v19,v95
v_mul_f32 v20,v20,v96
v_mul_f32 v21,v21,v97
D 0,56,3072
v_cvt_pk_bf16_f32 v106,v18,v19
v_cvt_pk_bf16_f32 v107,v20,v21
v_fma_f32 v22,-v74,v86,v22
v_fma_f32 v23,-v74,v87,v23
v_fma_f32 v24,-v74,v88,v24
v_fma_f32 v25,-v74,v89,v25
v_mul_f32 v98,v22,v22
v_mul_f32 v99,v23,v23
s_add_u32 s58,s58,s61
s_add_u32 m0,s57,16384
s_add_u32 s56,s55,16384
D 0,56,0
v_mul_f32 v100,v24,v24
v_mul_f32 v101,v25,v25
v_fma_f32 v98,v76,v98,v75
v_fma_f32 v99,v76,v99,v75
v_fma_f32 v100,v76,v100,v75
v_fma_f32 v101,v76,v101,v75
v_mul_f32 v98,v22,v98
v_mul_f32 v99,v23,v99
D 0,56,1024
v_mul_f32 v100,v24,v100
v_mul_f32 v101,v25,v101
v_exp_f32 v98,v98
v_exp_f32 v99,v99
v_exp_f32 v100,v100
v_exp_f32 v101,v101
v_fma_f32 v98,v98,v77,v77
v_fma_f32 v99,v99,v77,v77
D 0,56,2048
v_fma_f32 v100,v100,v77,v77
v_fma_f32 v101,v101,v77,v77
v_rcp_f32 v98,v98
v_rcp_f32 v99,v99
v_rcp_f32 v100,v100
v_rcp_f32 v101,v101
v_mul_f32 v22,v22,v98
v_mul_f32 v23,v23,v99
D 0,56,3072
v_mul_f32 v24,v24,v100
v_mul_f32 v25,v25,v101
v_cvt_pk_bf16_f32 v108,v22,v23
v_cvt_pk_bf16_f32 v109,v24,v25
buffer_store_dwordx4 v[106:109],v0,s[36:39],s66 offen offset:0 sc0 sc1
v_fma_f32 v26,-v74,v90,v26
v_fma_f32 v27,-v74,v91,v27
v_fma_f32 v28,-v74,v92,v28
v_fma_f32 v29,-v74,v93,v29
s_add_u32 s55,s55,s60
s_sub_u32 s57,s84,s57
s_add_u32 s54,s54,s46
v_mul_f32 v102,v26,v26
v_mul_f32 v103,v27,v27
v_mul_f32 v104,v28,v28
v_mul_f32 v105,v29,v29
v_fma_f32 v102,v76,v102,v75
v_fma_f32 v103,v76,v103,v75
v_fma_f32 v104,v76,v104,v75
v_fma_f32 v105,v76,v105,v75
v_mul_f32 v102,v26,v102
v_mul_f32 v103,v27,v103
v_mul_f32 v104,v28,v104
v_mul_f32 v105,v29,v105
v_exp_f32 v102,v102
v_exp_f32 v103,v103
v_exp_f32 v104,v104
v_exp_f32 v105,v105
v_fma_f32 v102,v102,v77,v77
v_fma_f32 v103,v103,v77,v77
v_fma_f32 v104,v104,v77,v77
v_fma_f32 v105,v105,v77,v77
v_rcp_f32 v102,v102
v_rcp_f32 v103,v103
v_rcp_f32 v104,v104
v_rcp_f32 v105,v105
v_mul_f32 v26,v26,v102
v_mul_f32 v27,v27,v103
v_mul_f32 v28,v28,v104
v_mul_f32 v29,v29,v105
v_cvt_pk_bf16_f32 v110,v26,v27
v_cvt_pk_bf16_f32 v111,v28,v29
buffer_store_dwordx2 v[110:111],v10,s[36:39],s66 offen offset:0 sc0 sc1
s_mov_b32 s72,s69
s_mov_b32 s69,0x7fff0000
s_mov_b32 s66,0x7fff0000
s_mov_b32 s76,0x7fff0000
s_branch Lg3_gox_%=
Lg3_xn0_%=:
s_cmp_ge_u32 s54,256
s_cbranch_scc1 Lg3_fin0_%=
s_add_u32 m0,s59,4096
s_nop 0
buffer_load_dword v8,s[36:39],s58 offen offset:0 sc1 lds
buffer_store_dword v4,v3,s[36:39],s71 offen offset:0 sc0 sc1
s_add_u32 m0,s59,5120
s_nop 0
buffer_load_dword v8,s[36:39],s58 offen offset:0 sc1 lds
buffer_store_dword v4,v3,s[36:39],s71 offen offset:0 sc0 sc1
s_mov_b32 s83,0
L 0
v_fma_f32 v18,-v74,v82,v18
v_fma_f32 v19,-v74,v83,v19
v_fma_f32 v20,-v74,v84,v20
v_fma_f32 v21,-v74,v85,v21
v_mul_f32 v94,v18,v18
v_mul_f32 v95,v19,v19
v_mul_f32 v96,v20,v20
v_mul_f32 v97,v21,v21
v_fma_f32 v94,v76,v94,v75
v_fma_f32 v95,v76,v95,v75
v_fma_f32 v96,v76,v96,v75
v_fma_f32 v97,v76,v97,v75
v_mul_f32 v94,v18,v94
v_mul_f32 v95,v19,v95
v_mul_f32 v96,v20,v96
v_mul_f32 v97,v21,v97
v_exp_f32 v94,v94
v_exp_f32 v95,v95
v_exp_f32 v96,v96
v_exp_f32 v97,v97
v_fma_f32 v94,v94,v77,v77
v_fma_f32 v95,v95,v77,v77
v_fma_f32 v96,v96,v77,v77
v_fma_f32 v97,v97,v77,v77
v_rcp_f32 v94,v94
v_rcp_f32 v95,v95
v_rcp_f32 v96,v96
v_rcp_f32 v97,v97
v_mul_f32 v18,v18,v94
v_mul_f32 v19,v19,v95
v_mul_f32 v20,v20,v96
v_mul_f32 v21,v21,v97
v_cvt_pk_bf16_f32 v106,v18,v19
v_cvt_pk_bf16_f32 v107,v20,v21
v_fma_f32 v22,-v74,v86,v22
v_fma_f32 v23,-v74,v87,v23
v_fma_f32 v24,-v74,v88,v24
v_fma_f32 v25,-v74,v89,v25
v_mul_f32 v98,v22,v22
v_mul_f32 v99,v23,v23
v_mul_f32 v100,v24,v24
v_mul_f32 v101,v25,v25
v_fma_f32 v98,v76,v98,v75
v_fma_f32 v99,v76,v99,v75
v_fma_f32 v100,v76,v100,v75
v_fma_f32 v101,v76,v101,v75
v_mul_f32 v98,v22,v98
v_mul_f32 v99,v23,v99
v_mul_f32 v100,v24,v100
v_mul_f32 v101,v25,v101
v_exp_f32 v98,v98
v_exp_f32 v99,v99
v_exp_f32 v100,v100
v_exp_f32 v101,v101
v_fma_f32 v98,v98,v77,v77
v_fma_f32 v99,v99,v77,v77
v_fma_f32 v100,v100,v77,v77
v_fma_f32 v101,v101,v77,v77
v_rcp_f32 v98,v98
v_rcp_f32 v99,v99
v_rcp_f32 v100,v100
v_rcp_f32 v101,v101
v_mul_f32 v22,v22,v98
v_mul_f32 v23,v23,v99
v_mul_f32 v24,v24,v100
v_mul_f32 v25,v25,v101
v_cvt_pk_bf16_f32 v108,v22,v23
v_cvt_pk_bf16_f32 v109,v24,v25
buffer_store_dwordx4 v[106:109],v0,s[36:39],s66 offen offset:0 sc0 sc1
v_fma_f32 v26,-v74,v90,v26
v_fma_f32 v27,-v74,v91,v27
v_fma_f32 v28,-v74,v92,v28
v_fma_f32 v29,-v74,v93,v29
v_mul_f32 v102,v26,v26
v_mul_f32 v103,v27,v27
v_mul_f32 v104,v28,v28
v_mul_f32 v105,v29,v29
v_fma_f32 v102,v76,v102,v75
v_fma_f32 v103,v76,v103,v75
v_fma_f32 v104,v76,v104,v75
v_fma_f32 v105,v76,v105,v75
v_mul_f32 v102,v26,v102
v_mul_f32 v103,v27,v103
v_mul_f32 v104,v28,v104
v_mul_f32 v105,v29,v105
v_exp_f32 v102,v102
v_exp_f32 v103,v103
v_exp_f32 v104,v104
v_exp_f32 v105,v105
v_fma_f32 v102,v102,v77,v77
v_fma_f32 v103,v103,v77,v77
v_fma_f32 v104,v104,v77,v77
v_fma_f32 v105,v105,v77,v77
v_rcp_f32 v102,v102
v_rcp_f32 v103,v103
v_rcp_f32 v104,v104
v_rcp_f32 v105,v105
v_mul_f32 v26,v26,v102
v_mul_f32 v27,v27,v103
v_mul_f32 v28,v28,v104
v_mul_f32 v29,v29,v105
v_cvt_pk_bf16_f32 v110,v26,v27
v_cvt_pk_bf16_f32 v111,v28,v29
buffer_store_dwordx2 v[110:111],v10,s[36:39],s66 offen offset:0 sc0 sc1
s_mov_b32 s72,s69
s_mov_b32 s69,0x7fff0000
s_mov_b32 s66,0x7fff0000
s_mov_b32 s76,0x7fff0000
s_branch Lg3_poll_%=
Lg3_fin0_%=:
L 0
v_fma_f32 v18,-v74,v82,v18
v_fma_f32 v19,-v74,v83,v19
v_fma_f32 v20,-v74,v84,v20
v_fma_f32 v21,-v74,v85,v21
v_mul_f32 v94,v18,v18
v_mul_f32 v95,v19,v19
v_mul_f32 v96,v20,v20
v_mul_f32 v97,v21,v21
v_fma_f32 v94,v76,v94,v75
v_fma_f32 v95,v76,v95,v75
v_fma_f32 v96,v76,v96,v75
v_fma_f32 v97,v76,v97,v75
v_mul_f32 v94,v18,v94
v_mul_f32 v95,v19,v95
v_mul_f32 v96,v20,v96
v_mul_f32 v97,v21,v97
v_exp_f32 v94,v94
v_exp_f32 v95,v95
v_exp_f32 v96,v96
v_exp_f32 v97,v97
v_fma_f32 v94,v94,v77,v77
v_fma_f32 v95,v95,v77,v77
v_fma_f32 v96,v96,v77,v77
v_fma_f32 v97,v97,v77,v77
v_rcp_f32 v94,v94
v_rcp_f32 v95,v95
v_rcp_f32 v96,v96
v_rcp_f32 v97,v97
v_mul_f32 v18,v18,v94
v_mul_f32 v19,v19,v95
v_mul_f32 v20,v20,v96
v_mul_f32 v21,v21,v97
v_cvt_pk_bf16_f32 v106,v18,v19
v_cvt_pk_bf16_f32 v107,v20,v21
v_fma_f32 v22,-v74,v86,v22
v_fma_f32 v23,-v74,v87,v23
v_fma_f32 v24,-v74,v88,v24
v_fma_f32 v25,-v74,v89,v25
v_mul_f32 v98,v22,v22
v_mul_f32 v99,v23,v23
v_mul_f32 v100,v24,v24
v_mul_f32 v101,v25,v25
v_fma_f32 v98,v76,v98,v75
v_fma_f32 v99,v76,v99,v75
v_fma_f32 v100,v76,v100,v75
v_fma_f32 v101,v76,v101,v75
v_mul_f32 v98,v22,v98
v_mul_f32 v99,v23,v99
v_mul_f32 v100,v24,v100
v_mul_f32 v101,v25,v101
v_exp_f32 v98,v98
v_exp_f32 v99,v99
v_exp_f32 v100,v100
v_exp_f32 v101,v101
v_fma_f32 v98,v98,v77,v77
v_fma_f32 v99,v99,v77,v77
v_fma_f32 v100,v100,v77,v77
v_fma_f32 v101,v101,v77,v77
v_rcp_f32 v98,v98
v_rcp_f32 v99,v99
v_rcp_f32 v100,v100
v_rcp_f32 v101,v101
v_mul_f32 v22,v22,v98
v_mul_f32 v23,v23,v99
v_mul_f32 v24,v24,v100
v_mul_f32 v25,v25,v101
v_cvt_pk_bf16_f32 v108,v22,v23
v_cvt_pk_bf16_f32 v109,v24,v25
buffer_store_dwordx4 v[106:109],v0,s[36:39],s66 offen offset:0 sc0 sc1
v_fma_f32 v26,-v74,v90,v26
v_fma_f32 v27,-v74,v91,v27
v_fma_f32 v28,-v74,v92,v28
v_fma_f32 v29,-v74,v93,v29
v_mul_f32 v102,v26,v26
v_mul_f32 v103,v27,v27
v_mul_f32 v104,v28,v28
v_mul_f32 v105,v29,v29
v_fma_f32 v102,v76,v102,v75
v_fma_f32 v103,v76,v103,v75
v_fma_f32 v104,v76,v104,v75
v_fma_f32 v105,v76,v105,v75
v_mul_f32 v102,v26,v102
v_mul_f32 v103,v27,v103
v_mul_f32 v104,v28,v104
v_mul_f32 v105,v29,v105
v_exp_f32 v102,v102
v_exp_f32 v103,v103
v_exp_f32 v104,v104
v_exp_f32 v105,v105
v_fma_f32 v102,v102,v77,v77
v_fma_f32 v103,v103,v77,v77
v_fma_f32 v104,v104,v77,v77
v_fma_f32 v105,v105,v77,v77
v_rcp_f32 v102,v102
v_rcp_f32 v103,v103
v_rcp_f32 v104,v104
v_rcp_f32 v105,v105
v_mul_f32 v26,v26,v102
v_mul_f32 v27,v27,v103
v_mul_f32 v28,v28,v104
v_mul_f32 v29,v29,v105
v_cvt_pk_bf16_f32 v110,v26,v27
v_cvt_pk_bf16_f32 v111,v28,v29
buffer_store_dwordx2 v[110:111],v10,s[36:39],s66 offen offset:0 sc0 sc1
s_mov_b32 s72,s69
s_mov_b32 s69,0x7fff0000
s_mov_b32 s66,0x7fff0000
s_mov_b32 s76,0x7fff0000
s_branch Lg3_done_%=
Lg3_K1_%=:
Lg3_L1_%=:
v_mfma_f32_16x16x32_bf16 v[38:41],a[8:11],v[42:45],0
s_add_u32 m0,s59,3072
R 62,2,5120
v_add_f32_dpp v115,v114,v114 quad_perm:[1,0,3,2] row_mask:0xf bank_mask:0xf
M 38,a[20:23],46
buffer_load_dword v8,s[36:39],s58 offen offset:0 sc1 lds
v_mfma_f32_16x16x32_bf16 v[34:37],a[4:7],v[42:45],0
s_mov_b32 s67,s65
v_add_f32_dpp v115,v115,v115 quad_perm:[2,3,0,1] row_mask:0xf bank_mask:0xf
v_fma_f32 v18,-v74,v82,v18
M 34,a[16:19],46
s_add_u32 s65,s65,s62
R 66,2,6144
v_fma_f32 v19,-v74,v83,v19
v_mfma_f32_16x16x32_bf16 v[30:33],a[0:3],v[42:45],0
v_add_f32_dpp v118,v115,v115 row_half_mirror row_mask:0xf bank_mask:0xf
v_fma_f32 v20,-v74,v84,v20
v_fma_f32 v21,-v74,v85,v21
M 30,a[12:15],46
L 3
v_mul_f32 v94,v18,v18
v_mul_f32 v95,v19,v19
M 30,a[24:27],50
R 70,2,7168
v_mov_b32_dpp v115,v118 row_ror:8 row_mask:0xf bank_mask:0xf
M 30,a[36:39],54
v_fma_f32 v115,-v118,v118,v115
v_max_f32 v115,s87,v115
M 34,a[28:31],50
v_rsq_f32 v116,v115
M 34,a[40:43],54
R 42,2,8192
v_fma_f32 v115,v15,v115,v16
v_mul_f32 v118,v17,v118
M 38,a[32:35],50
v_mul_f32 v119,v116,v116
v_fma_f32 v115,v14,v119,v115
M 38,a[44:47],54
L 3
v_fma_f32 v118,v116,v115,v118
v_mul_f32 v96,v20,v20
M 38,a[56:59],58
R 46,2,9216
v_mul_f32 v97,v21,v21
M 38,a[68:71],62
ds_write_b32 v12,v118 offset:2048
M 34,a[52:55],58
v_fma_f32 v94,v76,v94,v75
v_fma_f32 v95,v76,v95,v75
v_fma_f32 v96,v76,v96,v75
M 34,a[64:67],62
R 50,2,10240
v_fma_f32 v97,v76,v97,v75
M 30,a[48:51],58
v_mul_f32 v94,v18,v94
v_mul_f32 v95,v19,v95
M 30,a[60:63],62
L 4
v_exp_f32 v94,v94
M 30,a[72:75],66
R 54,2,11264
v_mul_f32 v96,v20,v96
M 30,a[84:87],70
v_exp_f32 v95,v95
v_mul_f32 v97,v21,v97
M 34,a[76:79],66
v_exp_f32 v96,v96
M 34,a[88:91],70
R 58,2,12288
v_fma_f32 v94,v94,v77,v77
M 38,a[80:83],66
v_exp_f32 v97,v97
M 38,a[92:95],70
L 4
v_rcp_f32 v94,v94
M 38,a[104:107],42
R 62,2,13312
v_fma_f32 v95,v95,v77,v77
v_fma_f32 v96,v96,v77,v77
M 38,a[116:119],46
v_rcp_f32 v95,v95
M 34,a[100:103],42
v_rcp_f32 v96,v96
M 34,a[112:115],46
R 66,2,14336
v_fma_f32 v97,v97,v77,v77
M 30,a[96:99],42
v_rcp_f32 v97,v97
M 30,a[108:111],46
L 3
v_mul_f32 v18,v18,v94
v_mul_f32 v19,v19,v95
v_mul_f32 v20,v20,v96
M 30,a[120:123],50
R 70,2,15360
v_mul_f32 v21,v21,v97
M 30,a[132:135],54
s_waitcnt vmcnt(0)
v_cvt_pk_bf16_f32 v106,v18,v19
v_cvt_pk_bf16_f32 v107,v20,v21
M 34,a[124:127],50
s_barrier
ds_read_b32 v7,v6 offset:3072
v_fma_f32 v22,-v74,v86,v22
M 34,a[136:139],54
R 42,2,16384
v_fma_f32 v23,-v74,v87,v23
M 38,a[128:131],50
buffer_store_dword v4,v3,s[36:39],s70 offen offset:0 sc0 sc1
v_fma_f32 v24,-v74,v88,v24
M 38,a[140:143],54
L 4
v_fma_f32 v25,-v74,v89,v25
v_mul_f32 v98,v22,v22
M 38,a[152:155],58
s_mov_b32 s70,0x7fff0000
R 46,2,17408
v_mul_f32 v99,v23,v23
M 38,a[164:167],62
v_mul_f32 v100,v24,v24
v_mul_f32 v101,v25,v25
M 34,a[148:151],58
L 2
v_cmp_eq_u32 vcc,s53,v7
v_fma_f32 v98,v76,v98,v75
M 34,a[160:163],62
R 50,2,18432
v_fma_f32 v99,v76,v99,v75
v_fma_f32 v100,v76,v100,v75
M 30,a[144:147],58
v_fma_f32 v101,v76,v101,v75
v_mul_f32 v98,v22,v98
M 30,a[156:159],62
s_cmp_eq_u32 vcc_lo,-1
s_nop 0
v_mul_f32 v99,v23,v99
s_cbranch_scc0 Lg3_X1_%=
M 30,a[168:171],66
s_add_u32 m0,s57,0
s_add_u32 s56,s55,0
s_add_u32 s58,s58,s61
R 54,2,19456
M 30,a[180:183],70
buffer_load_dword v114,v11,s[36:39],s73 offen offset:0 sc1
M 34,a[172:175],66
D 0,56,0
v_mul_f32 v100,v24,v100
M 34,a[184:187],70
s_add_u32 s54,s54,s46
R 58,2,20480
v_mul_f32 v101,v25,v101
M 38,a[176:179],66
s_add_u32 s73,s73,s64
v_exp_f32 v98,v98
M 38,a[188:191],70
L 3
v_exp_f32 v99,v99
M 38,a[200:203],42
R 62,2,21504
v_fma_f32 v98,v98,v77,v77
M 38,a[212:215],46
D 0,56,1024
v_fma_f32 v99,v99,v77,v77
M 34,a[196:199],42
v_exp_f32 v100,v100
M 34,a[208:211],46
R 66,2,22528
v_fma_f32 v100,v100,v77,v77
M 30,a[192:195],42
D 0,56,2048
M 30,a[204:207],46
L 3
v_exp_f32 v101,v101
)"
R"(M 30,a[216:219],50
R 70,2,23552
v_rcp_f32 v98,v98
M 30,a[228:231],54
v_rcp_f32 v99,v99
M 34,a[220:223],50
D 0,56,3072
M 34,a[232:235],54
s_add_u32 m0,s59,1024
R 42,2,24576
v_fma_f32 v101,v101,v77,v77
M 38,a[224:227],50
buffer_load_dword v8,s[36:39],s58 offen offset:0 sc1 lds
M 38,a[236:239],54
L 3
v_rcp_f32 v100,v100
v_mul_f32 v22,v22,v98
M 38,a[248:251],58
s_add_u32 m0,s57,16384
R 46,2,25600
v_mul_f32 v23,v23,v99
M 38,v[132:135],62
s_add_u32 s56,s55,16384
v_rcp_f32 v101,v101
M 34,a[244:247],58
D 0,56,0
M 34,v[128:131],62
s_add_u32 s55,s55,s60
R 50,2,26624
v_mul_f32 v24,v24,v100
M 30,a[240:243],58
s_sub_u32 s57,s84,s57
v_mul_f32 v25,v25,v101
v_cvt_pk_bf16_f32 v108,v22,v23
v_cvt_pk_bf16_f32 v109,v24,v25
M 30,a[252:255],62
L 3
buffer_store_dwordx4 v[106:109],v0,s[36:39],s66 offen offset:0 sc0 sc1
v_fma_f32 v26,-v74,v90,v26
M 30,v[136:139],66
R 54,2,27648
v_fma_f32 v27,-v74,v91,v27
M 30,v[148:151],70
v_fma_f32 v28,-v74,v92,v28
v_fma_f32 v29,-v74,v93,v29
M 34,v[140:143],66
D 0,56,1024
M 34,v[152:155],70
R 58,2,28672
v_mul_f32 v102,v26,v26
v_mul_f32 v103,v27,v27
M 38,v[144:147],66
v_mul_f32 v104,v28,v28
v_mul_f32 v105,v29,v29
M 38,v[156:159],70
L 3
v_fma_f32 v102,v76,v102,v75
v_fma_f32 v103,v76,v103,v75
M 38,v[168:171],42
R 62,2,29696
v_fma_f32 v104,v76,v104,v75
M 38,v[180:183],46
D 0,56,2048
M 34,v[164:167],42
v_fma_f32 v105,v76,v105,v75
v_mul_f32 v102,v26,v102
v_mul_f32 v103,v27,v103
M 34,v[176:179],46
R 66,2,30720
v_mul_f32 v104,v28,v104
M 30,v[160:163],42
v_exp_f32 v102,v102
M 30,v[172:175],46
L 3
D 0,56,3072
M 30,v[184:187],50
s_nop 0
R 70,2,31744
v_mul_f32 v105,v29,v105
M 30,v[196:199],54
s_waitcnt vmcnt(5)
v_exp_f32 v103,v103
v_fma_f32 v102,v102,v77,v77
M 34,v[188:191],50
s_barrier
buffer_store_dword v4,v3,s[36:39],s70 offen offset:0 sc0 sc1
M 34,v[200:203],54
s_mov_b32 s70,s69
R 42,1,0
ds_read_b32 v7,v6 offset:1024
M 38,v[192:195],50
s_mov_b32 s69,s68
R 78,13,2048
v_fma_f32 v103,v103,v77,v77
M 38,v[204:207],54
L 5
v_exp_f32 v104,v104
M 38,v[216:219],58
s_add_u32 s68,s68,s63
R 46,1,1024
v_exp_f32 v105,v105
M 38,v[228:231],62
v_rcp_f32 v102,v102
M 34,v[212:215],58
v_rcp_f32 v103,v103
M 34,v[224:227],62
L 2
R 50,1,2048
v_fma_f32 v104,v104,v77,v77
M 30,v[208:211],58
v_rcp_f32 v104,v104
M 30,v[220:223],62
s_nop 0
v_fma_f32 v105,v105,v77,v77
v_rcp_f32 v105,v105
M 30,v[232:235],66
R 54,1,3072
v_mul_f32 v26,v26,v102
M 30,v[244:247],70
v_mul_f32 v27,v27,v103
v_mul_f32 v28,v28,v104
M 34,v[236:239],66
v_cmp_eq_u32 vcc,s53,v7
v_mul_f32 v29,v29,v105
M 34,v[248:251],70
R 58,1,4096
v_cvt_pk_bf16_f32 v110,v26,v27
v_cvt_pk_bf16_f32 v111,v28,v29
M 38,v[240:243],66
L 3
s_cmp_eq_u32 vcc_lo,-1
buffer_store_dwordx2 v[110:111],v10,s[36:39],s66 offen offset:0 sc0 sc1
M 38,v[252:255],70
buffer_store_dword v4,v3,s[36:39],s71 offen offset:0 sc0 sc1
s_cbranch_scc1 Lg3_F0_%=
s_branch Lg3_K0_%=
Lg3_X1_%=:
M 30,a[168:171],66
s_add_u32 m0,s59,1024
R 54,2,19456
v_mul_f32 v100,v24,v100
M 30,a[180:183],70
buffer_load_dword v8,s[36:39],s58 offen offset:0 sc1 lds
M 34,a[172:175],66
v_exp_f32 v98,v98
M 34,a[184:187],70
R 58,2,20480
v_mul_f32 v101,v25,v101
M 38,a[176:179],66
v_exp_f32 v99,v99
M 38,a[188:191],70
L 3
v_exp_f32 v100,v100
M 38,a[200:203],42
R 62,2,21504
v_fma_f32 v98,v98,v77,v77
M 38,a[212:215],46
v_exp_f32 v101,v101
M 34,a[196:199],42
v_rcp_f32 v98,v98
M 34,a[208:211],46
R 66,2,22528
v_fma_f32 v99,v99,v77,v77
M 30,a[192:195],42
v_rcp_f32 v99,v99
M 30,a[204:207],46
L 3
v_fma_f32 v100,v100,v77,v77
v_fma_f32 v101,v101,v77,v77
M 30,a[216:219],50
R 70,2,23552
v_mul_f32 v22,v22,v98
M 30,a[228:231],54
v_rcp_f32 v100,v100
M 34,a[220:223],50
v_rcp_f32 v101,v101
M 34,a[232:235],54
R 42,2,24576
v_mul_f32 v23,v23,v99
M 38,a[224:227],50
v_mul_f32 v24,v24,v100
v_mul_f32 v25,v25,v101
M 38,a[236:239],54
L 3
v_cvt_pk_bf16_f32 v108,v22,v23
v_cvt_pk_bf16_f32 v109,v24,v25
M 38,a[248:251],58
R 46,2,25600
v_fma_f32 v26,-v74,v90,v26
M 38,v[132:135],62
buffer_store_dwordx4 v[106:109],v0,s[36:39],s66 offen offset:0 sc0 sc1
v_fma_f32 v27,-v74,v91,v27
M 34,a[244:247],58
v_fma_f32 v28,-v74,v92,v28
v_fma_f32 v29,-v74,v93,v29
M 34,v[128:131],62
R 50,2,26624
v_mul_f32 v102,v26,v26
M 30,a[240:243],58
v_mul_f32 v103,v27,v27
v_mul_f32 v104,v28,v28
M 30,a[252:255],62
L 3
v_mul_f32 v105,v29,v29
v_fma_f32 v102,v76,v102,v75
M 30,v[136:139],66
R 54,2,27648
v_fma_f32 v103,v76,v103,v75
M 30,v[148:151],70
v_fma_f32 v104,v76,v104,v75
v_fma_f32 v105,v76,v105,v75
M 34,v[140:143],66
v_mul_f32 v102,v26,v102
v_mul_f32 v103,v27,v103
M 34,v[152:155],70
R 58,2,28672
v_mul_f32 v104,v28,v104
M 38,v[144:147],66
v_exp_f32 v102,v102
M 38,v[156:159],70
L 3
v_exp_f32 v103,v103
M 38,v[168:171],42
R 62,2,29696
v_mul_f32 v105,v29,v105
M 38,v[180:183],46
v_exp_f32 v104,v104
M 34,v[164:167],42
v_exp_f32 v105,v105
M 34,v[176:179],46
R 66,2,30720
v_fma_f32 v102,v102,v77,v77
M 30,v[160:163],42
v_rcp_f32 v102,v102
M 30,v[172:175],46
L 3
v_fma_f32 v103,v103,v77,v77
v_fma_f32 v104,v104,v77,v77
M 30,v[184:187],50
s_nop 0
R 70,2,31744
v_fma_f32 v105,v105,v77,v77
M 30,v[196:199],54
s_waitcnt vmcnt(1)
v_rcp_f32 v103,v103
M 34,v[188:191],50
s_barrier
buffer_store_dword v4,v3,s[36:39],s70 offen offset:0 sc0 sc1
M 34,v[200:203],54
s_mov_b32 s70,s69
ds_read_b32 v7,v6 offset:1024
R 78,13,2048
M 38,v[192:195],50
s_mov_b32 s69,s68
v_rcp_f32 v104,v104
M 38,v[204:207],54
L 4
v_rcp_f32 v105,v105
M 38,v[216:219],58
s_add_u32 s68,s68,s63
v_mul_f32 v26,v26,v102
v_mul_f32 v27,v27,v103
M 38,v[228:231],62
v_mul_f32 v28,v28,v104
v_mul_f32 v29,v29,v105
M 34,v[212:215],58
v_cvt_pk_bf16_f32 v110,v26,v27
v_cvt_pk_bf16_f32 v111,v28,v29
M 34,v[224:227],62
L 1
buffer_store_dwordx2 v[110:111],v10,s[36:39],s66 offen offset:0 sc0 sc1
M 30,v[208:211],58
v_cmp_eq_u32 vcc,s53,v7
M 30,v[220:223],62
s_nop 0
M 30,v[232:235],66
M 30,v[244:247],70
M 34,v[236:239],66
M 34,v[248:251],70
M 38,v[240:243],66
M 38,v[252:255],70
s_mov_b32 s80,0
s_cmp_eq_u32 vcc_lo,-1
s_cbranch_scc0 Lg3_xn1_%=
buffer_load_dword v114,v11,s[36:39],s73 offen offset:0 sc1
L 0
v_fma_f32 v30,-v78,v82,v30
v_fma_f32 v31,-v78,v83,v31
v_fma_f32 v32,-v78,v84,v32
v_fma_f32 v33,-v78,v85,v33
v_mul_f32 v94,v30,v30
v_mul_f32 v95,v31,v31
v_mul_f32 v96,v32,v32
v_mul_f32 v97,v33,v33
s_add_u32 s73,s73,s64
s_add_u32 m0,s57,0
s_add_u32 s56,s55,0
D 0,56,0
v_fma_f32 v94,v80,v94,v79
v_fma_f32 v95,v80,v95,v79
v_fma_f32 v96,v80,v96,v79
v_fma_f32 v97,v80,v97,v79
v_mul_f32 v94,v30,v94
v_mul_f32 v95,v31,v95
v_mul_f32 v96,v32,v96
v_mul_f32 v97,v33,v97
D 0,56,1024
v_exp_f32 v94,v94
v_exp_f32 v95,v95
v_exp_f32 v96,v96
v_exp_f32 v97,v97
v_fma_f32 v94,v94,v81,v81
v_fma_f32 v95,v95,v81,v81
v_fma_f32 v96,v96,v81,v81
v_fma_f32 v97,v97,v81,v81
D 0,56,2048
v_rcp_f32 v94,v94
v_rcp_f32 v95,v95
v_rcp_f32 v96,v96
v_rcp_f32 v97,v97
v_mul_f32 v30,v30,v94
v_mul_f32 v31,v31,v95
v_mul_f32 v32,v32,v96
v_mul_f32 v33,v33,v97
D 0,56,3072
v_cvt_pk_bf16_f32 v106,v30,v31
v_cvt_pk_bf16_f32 v107,v32,v33
v_fma_f32 v34,-v78,v86,v34
v_fma_f32 v35,-v78,v87,v35
v_fma_f32 v36,-v78,v88,v36
v_fma_f32 v37,-v78,v89,v37
v_mul_f32 v98,v34,v34
v_mul_f32 v99,v35,v35
s_add_u32 s58,s58,s61
s_add_u32 m0,s57,16384
s_add_u32 s56,s55,16384
D 0,56,0
v_mul_f32 v100,v36,v36
v_mul_f32 v101,v37,v37
v_fma_f32 v98,v80,v98,v79
v_fma_f32 v99,v80,v99,v79
v_fma_f32 v100,v80,v100,v79
v_fma_f32 v101,v80,v101,v79
v_mul_f32 v98,v34,v98
v_mul_f32 v99,v35,v99
D 0,56,1024
v_mul_f32 v100,v36,v100
v_mul_f32 v101,v37,v101
v_exp_f32 v98,v98
v_exp_f32 v99,v99
v_exp_f32 v100,v100
v_exp_f32 v101,v101
v_fma_f32 v98,v98,v81,v81
v_fma_f32 v99,v99,v81,v81
D 0,56,2048
v_fma_f32 v100,v100,v81,v81
v_fma_f32 v101,v101,v81,v81
v_rcp_f32 v98,v98
v_rcp_f32 v99,v99
v_rcp_f32 v100,v100
v_rcp_f32 v101,v101
v_mul_f32 v34,v34,v98
v_mul_f32 v35,v35,v99
D 0,56,3072
v_mul_f32 v36,v36,v100
v_mul_f32 v37,v37,v101
v_cvt_pk_bf16_f32 v108,v34,v35
v_cvt_pk_bf16_f32 v109,v36,v37
buffer_store_dwordx4 v[106:109],v0,s[36:39],s67 offen offset:0 sc0 sc1
v_fma_f32 v38,-v78,v90,v38
v_fma_f32 v39,-v78,v91,v39
v_fma_f32 v40,-v78,v92,v40
v_fma_f32 v41,-v78,v93,v41
s_add_u32 s55,s55,s60
s_sub_u32 s57,s84,s57
s_add_u32 s54,s54,s46
v_mul_f32 v102,v38,v38
v_mul_f32 v103,v39,v39
v_mul_f32 v104,v40,v40
v_mul_f32 v105,v41,v41
v_fma_f32 v102,v80,v102,v79
v_fma_f32 v103,v80,v103,v79
v_fma_f32 v104,v80,v104,v79
v_fma_f32 v105,v80,v105,v79
v_mul_f32 v102,v38,v102
v_mul_f32 v103,v39,v103
v_mul_f32 v104,v40,v104
v_mul_f32 v105,v41,v105
v_exp_f32 v102,v102
v_exp_f32 v103,v103
v_exp_f32 v104,v104
v_exp_f32 v105,v105
v_fma_f32 v102,v102,v81,v81
v_fma_f32 v103,v103,v81,v81
v_fma_f32 v104,v104,v81,v81
v_fma_f32 v105,v105,v81,v81
v_rcp_f32 v102,v102
v_rcp_f32 v103,v103
v_rcp_f32 v104,v104
v_rcp_f32 v105,v105
v_mul_f32 v38,v38,v102
v_mul_f32 v39,v39,v103
v_mul_f32 v40,v40,v104
v_mul_f32 v41,v41,v105
v_cvt_pk_bf16_f32 v110,v38,v39
v_cvt_pk_bf16_f32 v111,v40,v41
buffer_store_dwordx2 v[110:111],v10,s[36:39],s67 offen offset:0 sc0 sc1
s_mov_b32 s72,s69
s_mov_b32 s69,0x7fff0000
s_mov_b32 s67,0x7fff0000
s_mov_b32 s77,0x7fff0000
s_branch Lg3_gox_%=
Lg3_xn1_%=:
s_cmp_ge_u32 s54,256
s_cbranch_scc1 Lg3_fin1_%=
s_add_u32 m0,s59,4096
s_nop 0
buffer_load_dword v8,s[36:39],s58 offen offset:0 sc1 lds
buffer_store_dword v4,v3,s[36:39],s71 offen offset:0 sc0 sc1
s_add_u32 m0,s59,5120
s_nop 0
buffer_load_dword v8,s[36:39],s58 offen offset:0 sc1 lds
buffer_store_dword v4,v3,s[36:39],s71 offen offset:0 sc0 sc1
s_mov_b32 s83,0
L 0
v_fma_f32 v30,-v78,v82,v30
v_fma_f32 v31,-v78,v83,v31
v_fma_f32 v32,-v78,v84,v32
v_fma_f32 v33,-v78,v85,v33
v_mul_f32 v94,v30,v30
v_mul_f32 v95,v31,v31
v_mul_f32 v96,v32,v32
v_mul_f32 v97,v33,v33
v_fma_f32 v94,v80,v94,v79
v_fma_f32 v95,v80,v95,v79
v_fma_f32 v96,v80,v96,v79
v_fma_f32 v97,v80,v97,v79
v_mul_f32 v94,v30,v94
v_mul_f32 v95,v31,v95
v_mul_f32 v96,v32,v96
v_mul_f32 v97,v33,v97
v_exp_f32 v94,v94
v_exp_f32 v95,v95
v_exp_f32 v96,v96
v_exp_f32 v97,v97
v_fma_f32 v94,v94,v81,v81
v_fma_f32 v95,v95,v81,v81
v_fma_f32 v96,v96,v81,v81
v_fma_f32 v97,v97,v81,v81
v_rcp_f32 v94,v94
v_rcp_f32 v95,v95
v_rcp_f32 v96,v96
v_rcp_f32 v97,v97
v_mul_f32 v30,v30,v94
v_mul_f32 v31,v31,v95
v_mul_f32 v32,v32,v96
v_mul_f32 v33,v33,v97
v_cvt_pk_bf16_f32 v106,v30,v31
v_cvt_pk_bf16_f32 v107,v32,v33
v_fma_f32 v34,-v78,v86,v34
v_fma_f32 v35,-v78,v87,v35
v_fma_f32 v36,-v78,v88,v36
v_fma_f32 v37,-v78,v89,v37
v_mul_f32 v98,v34,v34
v_mul_f32 v99,v35,v35
v_mul_f32 v100,v36,v36
v_mul_f32 v101,v37,v37
v_fma_f32 v98,v80,v98,v79
v_fma_f32 v99,v80,v99,v79
v_fma_f32 v100,v80,v100,v79
v_fma_f32 v101,v80,v101,v79
v_mul_f32 v98,v34,v98
v_mul_f32 v99,v35,v99
v_mul_f32 v100,v36,v100
v_mul_f32 v101,v37,v101
v_exp_f32 v98,v98
v_exp_f32 v99,v99
v_exp_f32 v100,v100
v_exp_f32 v101,v101
v_fma_f32 v98,v98,v81,v81
v_fma_f32 v99,v99,v81,v81
v_fma_f32 v100,v100,v81,v81
v_fma_f32 v101,v101,v81,v81
v_rcp_f32 v98,v98
v_rcp_f32 v99,v99
v_rcp_f32 v100,v100
v_rcp_f32 v101,v101
v_mul_f32 v34,v34,v98
v_mul_f32 v35,v35,v99
v_mul_f32 v36,v36,v100
v_mul_f32 v37,v37,v101
v_cvt_pk_bf16_f32 v108,v34,v35
v_cvt_pk_bf16_f32 v109,v36,v37
buffer_store_dwordx4 v[106:109],v0,s[36:39],s67 offen offset:0 sc0 sc1
v_fma_f32 v38,-v78,v90,v38
v_fma_f32 v39,-v78,v91,v39
v_fma_f32 v40,-v78,v92,v40
v_fma_f32 v41,-v78,v93,v41
v_mul_f32 v102,v38,v38
v_mul_f32 v103,v39,v39
)"
R"(v_mul_f32 v104,v40,v40
v_mul_f32 v105,v41,v41
v_fma_f32 v102,v80,v102,v79
v_fma_f32 v103,v80,v103,v79
v_fma_f32 v104,v80,v104,v79
v_fma_f32 v105,v80,v105,v79
v_mul_f32 v102,v38,v102
v_mul_f32 v103,v39,v103
v_mul_f32 v104,v40,v104
v_mul_f32 v105,v41,v105
v_exp_f32 v102,v102
v_exp_f32 v103,v103
v_exp_f32 v104,v104
v_exp_f32 v105,v105
v_fma_f32 v102,v102,v81,v81
v_fma_f32 v103,v103,v81,v81
v_fma_f32 v104,v104,v81,v81
v_fma_f32 v105,v105,v81,v81
v_rcp_f32 v102,v102
v_rcp_f32 v103,v103
v_rcp_f32 v104,v104
v_rcp_f32 v105,v105
v_mul_f32 v38,v38,v102
v_mul_f32 v39,v39,v103
v_mul_f32 v40,v40,v104
v_mul_f32 v41,v41,v105
v_cvt_pk_bf16_f32 v110,v38,v39
v_cvt_pk_bf16_f32 v111,v40,v41
buffer_store_dwordx2 v[110:111],v10,s[36:39],s67 offen offset:0 sc0 sc1
s_mov_b32 s72,s69
s_mov_b32 s69,0x7fff0000
s_mov_b32 s67,0x7fff0000
s_mov_b32 s77,0x7fff0000
s_branch Lg3_poll_%=
Lg3_fin1_%=:
L 0
v_fma_f32 v30,-v78,v82,v30
v_fma_f32 v31,-v78,v83,v31
v_fma_f32 v32,-v78,v84,v32
v_fma_f32 v33,-v78,v85,v33
v_mul_f32 v94,v30,v30
v_mul_f32 v95,v31,v31
v_mul_f32 v96,v32,v32
v_mul_f32 v97,v33,v33
v_fma_f32 v94,v80,v94,v79
v_fma_f32 v95,v80,v95,v79
v_fma_f32 v96,v80,v96,v79
v_fma_f32 v97,v80,v97,v79
v_mul_f32 v94,v30,v94
v_mul_f32 v95,v31,v95
v_mul_f32 v96,v32,v96
v_mul_f32 v97,v33,v97
v_exp_f32 v94,v94
v_exp_f32 v95,v95
v_exp_f32 v96,v96
v_exp_f32 v97,v97
v_fma_f32 v94,v94,v81,v81
v_fma_f32 v95,v95,v81,v81
v_fma_f32 v96,v96,v81,v81
v_fma_f32 v97,v97,v81,v81
v_rcp_f32 v94,v94
v_rcp_f32 v95,v95
v_rcp_f32 v96,v96
v_rcp_f32 v97,v97
v_mul_f32 v30,v30,v94
v_mul_f32 v31,v31,v95
v_mul_f32 v32,v32,v96
v_mul_f32 v33,v33,v97
v_cvt_pk_bf16_f32 v106,v30,v31
v_cvt_pk_bf16_f32 v107,v32,v33
v_fma_f32 v34,-v78,v86,v34
v_fma_f32 v35,-v78,v87,v35
v_fma_f32 v36,-v78,v88,v36
v_fma_f32 v37,-v78,v89,v37
v_mul_f32 v98,v34,v34
v_mul_f32 v99,v35,v35
v_mul_f32 v100,v36,v36
v_mul_f32 v101,v37,v37
v_fma_f32 v98,v80,v98,v79
v_fma_f32 v99,v80,v99,v79
v_fma_f32 v100,v80,v100,v79
v_fma_f32 v101,v80,v101,v79
v_mul_f32 v98,v34,v98
v_mul_f32 v99,v35,v99
v_mul_f32 v100,v36,v100
v_mul_f32 v101,v37,v101
v_exp_f32 v98,v98
v_exp_f32 v99,v99
v_exp_f32 v100,v100
v_exp_f32 v101,v101
v_fma_f32 v98,v98,v81,v81
v_fma_f32 v99,v99,v81,v81
v_fma_f32 v100,v100,v81,v81
v_fma_f32 v101,v101,v81,v81
v_rcp_f32 v98,v98
v_rcp_f32 v99,v99
v_rcp_f32 v100,v100
v_rcp_f32 v101,v101
v_mul_f32 v34,v34,v98
v_mul_f32 v35,v35,v99
v_mul_f32 v36,v36,v100
v_mul_f32 v37,v37,v101
v_cvt_pk_bf16_f32 v108,v34,v35
v_cvt_pk_bf16_f32 v109,v36,v37
buffer_store_dwordx4 v[106:109],v0,s[36:39],s67 offen offset:0 sc0 sc1
v_fma_f32 v38,-v78,v90,v38
v_fma_f32 v39,-v78,v91,v39
v_fma_f32 v40,-v78,v92,v40
v_fma_f32 v41,-v78,v93,v41
v_mul_f32 v102,v38,v38
v_mul_f32 v103,v39,v39
v_mul_f32 v104,v40,v40
v_mul_f32 v105,v41,v41
v_fma_f32 v102,v80,v102,v79
v_fma_f32 v103,v80,v103,v79
v_fma_f32 v104,v80,v104,v79
v_fma_f32 v105,v80,v105,v79
v_mul_f32 v102,v38,v102
v_mul_f32 v103,v39,v103
v_mul_f32 v104,v40,v104
v_mul_f32 v105,v41,v105
v_exp_f32 v102,v102
v_exp_f32 v103,v103
v_exp_f32 v104,v104
v_exp_f32 v105,v105
v_fma_f32 v102,v102,v81,v81
v_fma_f32 v103,v103,v81,v81
v_fma_f32 v104,v104,v81,v81
v_fma_f32 v105,v105,v81,v81
v_rcp_f32 v102,v102
v_rcp_f32 v103,v103
v_rcp_f32 v104,v104
v_rcp_f32 v105,v105
v_mul_f32 v38,v38,v102
v_mul_f32 v39,v39,v103
v_mul_f32 v40,v40,v104
v_mul_f32 v41,v41,v105
v_cvt_pk_bf16_f32 v110,v38,v39
v_cvt_pk_bf16_f32 v111,v40,v41
buffer_store_dwordx2 v[110:111],v10,s[36:39],s67 offen offset:0 sc0 sc1
s_mov_b32 s72,s69
s_mov_b32 s69,0x7fff0000
s_mov_b32 s67,0x7fff0000
s_mov_b32 s77,0x7fff0000
s_branch Lg3_done_%=
Lg3_done_%=:
s_waitcnt vmcnt(2)
s_barrier
buffer_store_dword v4,v3,s[36:39],s70 offen offset:0 sc0 sc1
s_waitcnt vmcnt(1)
s_barrier
buffer_store_dword v4,v3,s[36:39],s72 offen offset:0 sc0 sc1
s_waitcnt vmcnt(0) lgkmcnt(0)
s_barrier
s_endpgm
Lcode_g3_e:
.purgem M
.purgem R
.purgem T
.purgem D
.purgem L
)"
  :: "s"(ws), "s"(__builtin_amdgcn_readfirstlane(sWave)), "s"(__builtin_amdgcn_readfirstlane(sG0)), "s"(__builtin_amdgcn_readfirstlane(sStep)), "s"(__builtin_amdgcn_readfirstlane(sOutOff)), "s"(__builtin_amdgcn_readfirstlane(sFoutOff)), "s"(__builtin_amdgcn_readfirstlane(sAuxOff)), "s"(__builtin_amdgcn_readfirstlane(sWw)), "s"(__builtin_amdgcn_readfirstlane(sBuf)), "s"(__builtin_amdgcn_readfirstlane(sExp)), "v"(vL16), "v"(vFlag), "v"(vSnapOff), "v"(vSnapRd), "v"(vWd0), "v"(vWd1), "v"(vWr0), "v"(vWr1), "v"(vWr2), "v"(vOutS), "v"(vAux0), "v"(vAux1), "v"(vAux2), "v"(vAux3), "v"(vAn), "v"(vAe), "s"(w3), "s"(out) : "v255", "v0", "v1", "v2", "v3", "v4", "v5", "v6", "v7", "v8", "v9", "v10", "v11", "v12", "v13", "v14", "v15", "v16", "v17", "v18", "v19", "v20", "v21", "v22", "v23", "v24", "v25", "v26", "v27", "v28", "v29", "v30", "v31", "v32", "v33", "v34", "v35", "v36", "v37", "v38", "v39", "v40", "v41", "v42", "v43", "v44", "v45", "v46", "v47", "v48", "v49", "v50", "v51", "v52", "v53", "v54", "v55", "v56", "v57", "v58", "v59", "v60", "v61", "v62", "v63", "v64", "v65", "v66", "v67", "v68", "v69", "v70", "v71", "v72", "v73", "v74", "v75", "v76", "v77", "v78", "v79", "v80", "v81", "v82", "v83", "v84", "v85", "v86", "v87", "v88", "v89", "v90", "v91", "v92", "v93", "v94", "v95", "v96", "v97", "v98", "v99", "v100", "v101", "v102", "v103", "v104", "v105", "v106", "v107", "v108", "v109", "v110", "v111", "v112", "v113", "v114", "v115", "v116", "v117", "v118", "v119", "v120", "v121", "v122", "v123", "v124", "v125", "v126", "v127", "v128", "v129", "v130", "v131", "v132", "v133", "v134", "v135", "v136", "v137", "v138", "v139", "v140", "v141", "v142", "v143", "v144", "v145", "v146", "v147", "v148", "v149", "v150", "v151", "v152", "v153", "v154", "v155", "v156", "v157", "v158", "v159", "v160", "v161", "v162", "v163", "v164", "v165", "v166", "v167", "v168", "v169", "v170", "v171", "v172", "v173", "v174", "v175", "v176", "v177", "v178", "v179", "v180", "v181", "v182", "v183", "v184", "v185", "v186", "v187", "v188", "v189", "v190", "v191", "v192", "v193", "v194", "v195", "v196", "v197", "v198", "v199", "v200", "v201", "v202", "v203", "v204", "v205", "v206", "v207", "v208", "v209", "v210", "v211", "v212", "v213", "v214", "v215", "v216", "v217", "v218", "v219", "v220", "v221", "v222", "v223", "v224", "v225", "v226", "v227", "v228", "v229", "v230", "v231", "a0", "a1", "a2", "a3", "a4", "a5", "a6", "a7", "a8", "a9", "a10", "a11", "a12", "a13", "a14", "a15", "a16", "a17", "a18", "a19", "a20", "a21", "a22", "a23", "a24", "a25", "a26", "a27", "a28", "a29", "a30", "a31", "a32", "a33", "a34", "a35", "a36", "a37", "a38", "a39", "a40", "a41", "a42", "a43", "a44", "a45", "a46", "a47", "a48", "a49", "a50", "a51", "a52", "a53", "a54", "a55", "a56", "a57", "a58", "a59", "a60", "a61", "a62", "a63", "a64", "a65", "a66", "a67", "a68", "a69", "a70", "a71", "a72", "a73", "a74", "a75", "a76", "a77", "a78", "a79", "a80", "a81", "a82", "a83", "a84", "a85", "a86", "a87", "a88", "a89", "a90", "a91", "a92", "a93", "a94", "a95", "a96", "a97", "a98", "a99", "a100", "a101", "a102", "a103", "a104", "a105", "a106", "a107", "a108", "a109", "a110", "a111", "a112", "a113", "a114", "a115", "a116", "a117", "a118", "a119", "a120", "a121", "a122", "a123", "a124", "a125", "a126", "a127", "a128", "a129", "a130", "a131", "a132", "a133", "a134", "a135", "a136", "a137", "a138", "a139", "a140", "a141", "a142", "a143", "a144", "a145", "a146", "a147", "a148", "a149", "a150", "a151", "a152", "a153", "a154", "a155", "a156", "a157", "a158", "a159", "a160", "a161", "a162", "a163", "a164", "a165", "a166", "a167", "a168", "a169", "a170", "a171", "a172", "a173", "a174", "a175", "a176", "a177", "a178", "a179", "a180", "a181", "a182", "a183", "a184", "a185", "a186", "a187", "a188", "a189", "a190", "a191", "a192", "a193", "a194", "a195", "a196", "a197", "a198", "a199", "a200", "a201", "a202", "a203", "a204", "a205", "a206", "a207", "a208", "a209", "a210", "a211", "a212", "a213", "a214", "a215", "a216", "a217", "a218", "a219", "a220", "a221", "a222", "a223", "a224", "a225", "a226", "a227", "a228", "a229", "a230", "a231", "a232", "a233", "a234", "a235", "a236", "a237", "a238", "a239", "a240", "a241", "a242", "a243", "a244", "a245", "a246", "a247", "a248", "a249", "a250", "a251", "a252", "a253", "a254", "a255", "s36", "s37", "s38", "s39", "s40", "s41", "s42", "s43", "s44", "s45", "s46", "s47", "s48", "s49", "s50", "s51", "s52", "s53", "s54", "s55", "s56", "s57", "s58", "s59", "s60", "s61", "s62", "s63", "s64", "s65", "s66", "s67", "s68", "s69", "s70", "s71", "s72", "s73", "s74", "s75", "s76", "s77", "s78", "s79", "s80", "s81", "s82", "s83", "s84", "s85", "s86", "s87", "s88", "s89", "s90", "s91", "s92", "s93", "s94", "s95", "s96", "s97", "vcc", "scc", "memory"); } else { asm volatile(
R"(.macro M d,w,x
v_mfma_f32_16x16x32_bf16 v[\d:\d+3], \w, v[\x:\x+3], v[\d:\d+3]
.endm
.macro R d,a,o
ds_read_b128 v[\d:\d+3], v\a offset:\o
.endm
.macro T d,a,o
ds_read_b64_tr_b16 \d, v\a offset:\o
.endm
.macro D a,s,o
buffer_load_dwordx4 v\a, s[36:39], s\s offen offset:\o sc1 lds
.endm
.macro L c
s_waitcnt lgkmcnt(\c)
.endm
Lcode_g3o_b:
s_mov_b64 s[36:37],%0
s_mov_b32 s38,111243392
s_mov_b32 s39,0x00020000
s_mov_b32 s44,%1
s_mov_b32 s45,%2
s_mov_b32 s46,%3
s_mov_b32 s47,%4
s_mov_b32 s48,%5
s_mov_b32 s49,%6
s_mov_b32 s50,%7
s_mov_b32 s51,%8
s_mov_b32 s53,%9
v_mov_b32 v0,%10
v_mov_b32 v3,%11
v_mov_b32 v5,%12
v_mov_b32 v6,%13
v_mov_b32 v18,%14
v_mov_b32 v19,%15
v_mov_b32 v20,%16
v_mov_b32 v21,%17
v_mov_b32 v22,%18
v_mov_b32 v10,%19
v_mov_b32 v11,%20
v_mov_b32 v12,%21
v_mov_b32 v13,%22
v_mov_b32 v14,%23
v_mov_b32 v15,%24
v_mov_b32 v16,%25
s_mov_b64 s[40:41],%26
s_mov_b32 s42,7340032
s_mov_b32 s43,0x00020000
s_lshl_b32 s57,s44,11
s_add_u32 s55,s50,0
s_add_u32 m0,s57,0
s_nop 0
buffer_load_dwordx4 v18,s[40:43],s55 offen offset:0  lds
s_add_u32 m0,s57,1024
s_nop 0
buffer_load_dwordx4 v19,s[40:43],s55 offen offset:0  lds
s_add_u32 m0,s57,8064
s_nop 0
buffer_load_dwordx4 v18,s[40:43],s55 offen offset:128  lds
s_add_u32 m0,s57,9088
s_nop 0
buffer_load_dwordx4 v19,s[40:43],s55 offen offset:128  lds
s_add_u32 s55,s50,458752
s_add_u32 m0,s57,16384
s_nop 0
buffer_load_dwordx4 v18,s[40:43],s55 offen offset:0  lds
s_add_u32 m0,s57,17408
s_nop 0
buffer_load_dwordx4 v19,s[40:43],s55 offen offset:0  lds
s_add_u32 m0,s57,24448
s_nop 0
buffer_load_dwordx4 v18,s[40:43],s55 offen offset:128  lds
s_add_u32 m0,s57,25472
s_nop 0
buffer_load_dwordx4 v19,s[40:43],s55 offen offset:128  lds
s_add_u32 s55,s50,917504
s_add_u32 m0,s57,32768
s_nop 0
buffer_load_dwordx4 v18,s[40:43],s55 offen offset:0  lds
s_add_u32 m0,s57,33792
s_nop 0
buffer_load_dwordx4 v19,s[40:43],s55 offen offset:0  lds
s_add_u32 m0,s57,40832
s_nop 0
buffer_load_dwordx4 v18,s[40:43],s55 offen offset:128  lds
s_add_u32 m0,s57,41856
s_nop 0
buffer_load_dwordx4 v19,s[40:43],s55 offen offset:128  lds
s_add_u32 s55,s50,1376256
s_add_u32 m0,s57,49152
s_nop 0
buffer_load_dwordx4 v18,s[40:43],s55 offen offset:0  lds
s_add_u32 m0,s57,50176
s_nop 0
buffer_load_dwordx4 v19,s[40:43],s55 offen offset:0  lds
s_add_u32 m0,s57,57216
s_nop 0
buffer_load_dwordx4 v18,s[40:43],s55 offen offset:128  lds
s_add_u32 m0,s57,58240
s_nop 0
buffer_load_dwordx4 v19,s[40:43],s55 offen offset:128  lds
s_add_u32 s55,s50,1835008
s_add_u32 m0,s57,65536
s_nop 0
buffer_load_dwordx4 v18,s[40:43],s55 offen offset:0  lds
s_add_u32 m0,s57,66560
s_nop 0
buffer_load_dwordx4 v19,s[40:43],s55 offen offset:0  lds
s_add_u32 m0,s57,73600
s_nop 0
buffer_load_dwordx4 v18,s[40:43],s55 offen offset:128  lds
s_add_u32 m0,s57,74624
s_nop 0
buffer_load_dwordx4 v19,s[40:43],s55 offen offset:128  lds
s_add_u32 s55,s50,2293760
s_add_u32 m0,s57,81920
s_nop 0
buffer_load_dwordx4 v18,s[40:43],s55 offen offset:0  lds
s_add_u32 m0,s57,82944
s_nop 0
buffer_load_dwordx4 v19,s[40:43],s55 offen offset:0  lds
s_add_u32 m0,s57,89984
s_nop 0
buffer_load_dwordx4 v18,s[40:43],s55 offen offset:128  lds
s_add_u32 m0,s57,91008
s_nop 0
buffer_load_dwordx4 v19,s[40:43],s55 offen offset:128  lds
s_waitcnt vmcnt(20) lgkmcnt(0)
s_barrier
s_add_u32 s55,s50,2752512
s_add_u32 m0,s57,98304
s_nop 0
buffer_load_dwordx4 v18,s[40:43],s55 offen offset:0  lds
s_add_u32 m0,s57,99328
s_nop 0
buffer_load_dwordx4 v19,s[40:43],s55 offen offset:0  lds
s_add_u32 m0,s57,106368
s_nop 0
buffer_load_dwordx4 v18,s[40:43],s55 offen offset:128  lds
s_add_u32 m0,s57,107392
s_nop 0
buffer_load_dwordx4 v19,s[40:43],s55 offen offset:128  lds
v_add_u32 v26,0,v20
v_add_u32 v27,0,v21
T a[0:1],26,0
T a[2:3],26,512
T a[4:5],27,0
T a[6:7],27,512
T a[8:9],26,4096
T a[10:11],26,4608
T a[12:13],27,4096
T a[14:15],27,4608
s_waitcnt vmcnt(20) lgkmcnt(0)
s_barrier
s_add_u32 s55,s50,3211264
s_add_u32 m0,s57,0
s_nop 0
buffer_load_dwordx4 v18,s[40:43],s55 offen offset:0  lds
s_add_u32 m0,s57,1024
s_nop 0
buffer_load_dwordx4 v19,s[40:43],s55 offen offset:0  lds
s_add_u32 m0,s57,8064
s_nop 0
buffer_load_dwordx4 v18,s[40:43],s55 offen offset:128  lds
s_add_u32 m0,s57,9088
s_nop 0
buffer_load_dwordx4 v19,s[40:43],s55 offen offset:128  lds
v_add_u32 v26,16384,v20
v_add_u32 v27,16384,v21
T a[16:17],26,0
T a[18:19],26,512
T a[20:21],27,0
T a[22:23],27,512
T a[24:25],26,4096
T a[26:27],26,4608
T a[28:29],27,4096
T a[30:31],27,4608
s_waitcnt vmcnt(20) lgkmcnt(0)
s_barrier
s_add_u32 s55,s50,3670016
s_add_u32 m0,s57,16384
s_nop 0
buffer_load_dwordx4 v18,s[40:43],s55 offen offset:0  lds
s_add_u32 m0,s57,17408
s_nop 0
buffer_load_dwordx4 v19,s[40:43],s55 offen offset:0  lds
s_add_u32 m0,s57,24448
s_nop 0
buffer_load_dwordx4 v18,s[40:43],s55 offen offset:128  lds
s_add_u32 m0,s57,25472
s_nop 0
buffer_load_dwordx4 v19,s[40:43],s55 offen offset:128  lds
v_add_u32 v26,32768,v20
v_add_u32 v27,32768,v21
T a[32:33],26,0
T a[34:35],26,512
T a[36:37],27,0
T a[38:39],27,512
T a[40:41],26,4096
T a[42:43],26,4608
T a[44:45],27,4096
T a[46:47],27,4608
s_waitcnt vmcnt(20) lgkmcnt(0)
s_barrier
s_add_u32 s55,s50,4128768
s_add_u32 m0,s57,32768
s_nop 0
buffer_load_dwordx4 v18,s[40:43],s55 offen offset:0  lds
s_add_u32 m0,s57,33792
s_nop 0
buffer_load_dwordx4 v19,s[40:43],s55 offen offset:0  lds
s_add_u32 m0,s57,40832
s_nop 0
buffer_load_dwordx4 v18,s[40:43],s55 offen offset:128  lds
s_add_u32 m0,s57,41856
s_nop 0
buffer_load_dwordx4 v19,s[40:43],s55 offen offset:128  lds
v_add_u32 v26,49152,v20
v_add_u32 v27,49152,v21
T a[48:49],26,0
T a[50:51],26,512
T a[52:53],27,0
T a[54:55],27,512
T a[56:57],26,4096
T a[58:59],26,4608
T a[60:61],27,4096
T a[62:63],27,4608
s_waitcnt vmcnt(20) lgkmcnt(0)
s_barrier
s_add_u32 s55,s50,4587520
s_add_u32 m0,s57,49152
s_nop 0
buffer_load_dwordx4 v18,s[40:43],s55 offen offset:0  lds
s_add_u32 m0,s57,50176
s_nop 0
buffer_load_dwordx4 v19,s[40:43],s55 offen offset:0  lds
s_add_u32 m0,s57,57216
s_nop 0
buffer_load_dwordx4 v18,s[40:43],s55 offen offset:128  lds
s_add_u32 m0,s57,58240
s_nop 0
buffer_load_dwordx4 v19,s[40:43],s55 offen offset:128  lds
v_add_u32 v26,65536,v20
v_add_u32 v27,65536,v21
T a[64:65],26,0
T a[66:67],26,512
T a[68:69],27,0
T a[70:71],27,512
T a[72:73],26,4096
T a[74:75],26,4608
T a[76:77],27,4096
T a[78:79],27,4608
s_waitcnt vmcnt(20) lgkmcnt(0)
s_barrier
s_add_u32 s55,s50,5046272
s_add_u32 m0,s57,65536
s_nop 0
buffer_load_dwordx4 v18,s[40:43],s55 offen offset:0  lds
s_add_u32 m0,s57,66560
s_nop 0
buffer_load_dwordx4 v19,s[40:43],s55 offen offset:0  lds
s_add_u32 m0,s57,73600
s_nop 0
buffer_load_dwordx4 v18,s[40:43],s55 offen offset:128  lds
s_add_u32 m0,s57,74624
s_nop 0
buffer_load_dwordx4 v19,s[40:43],s55 offen offset:128  lds
v_add_u32 v26,81920,v20
v_add_u32 v27,81920,v21
T a[80:81],26,0
T a[82:83],26,512
T a[84:85],27,0
T a[86:87],27,512
T a[88:89],26,4096
T a[90:91],26,4608
T a[92:93],27,4096
T a[94:95],27,4608
s_waitcnt vmcnt(20) lgkmcnt(0)
s_barrier
s_add_u32 s55,s50,5505024
s_add_u32 m0,s57,81920
s_nop 0
buffer_load_dwordx4 v18,s[40:43],s55 offen offset:0  lds
s_add_u32 m0,s57,82944
s_nop 0
buffer_load_dwordx4 v19,s[40:43],s55 offen offset:0  lds
s_add_u32 m0,s57,89984
s_nop 0
buffer_load_dwordx4 v18,s[40:43],s55 offen offset:128  lds
s_add_u32 m0,s57,91008
s_nop 0
buffer_load_dwordx4 v19,s[40:43],s55 offen offset:128  lds
v_add_u32 v26,98304,v20
v_add_u32 v27,98304,v21
T a[96:97],26,0
T a[98:99],26,512
T a[100:101],27,0
T a[102:103],27,512
T a[104:105],26,4096
T a[106:107],26,4608
T a[108:109],27,4096
T a[110:111],27,4608
s_waitcnt vmcnt(20) lgkmcnt(0)
s_barrier
s_add_u32 s55,s50,5963776
s_add_u32 m0,s57,98304
s_nop 0
buffer_load_dwordx4 v18,s[40:43],s55 offen offset:0  lds
s_add_u32 m0,s57,99328
s_nop 0
buffer_load_dwordx4 v19,s[40:43],s55 offen offset:0  lds
s_add_u32 m0,s57,106368
s_nop 0
buffer_load_dwordx4 v18,s[40:43],s55 offen offset:128  lds
s_add_u32 m0,s57,107392
s_nop 0
buffer_load_dwordx4 v19,s[40:43],s55 offen offset:128  lds
v_add_u32 v26,0,v20
v_add_u32 v27,0,v21
T a[112:113],26,0
T a[114:115],26,512
T a[116:117],27,0
T a[118:119],27,512
T a[120:121],26,4096
T a[122:123],26,4608
T a[124:125],27,4096
T a[126:127],27,4608
s_waitcnt vmcnt(20) lgkmcnt(0)
s_barrier
s_add_u32 s55,s50,6422528
s_add_u32 m0,s57,0
s_nop 0
buffer_load_dwordx4 v18,s[40:43],s55 offen offset:0  lds
s_add_u32 m0,s57,1024
s_nop 0
buffer_load_dwordx4 v19,s[40:43],s55 offen offset:0  lds
s_add_u32 m0,s57,8064
s_nop 0
buffer_load_dwordx4 v18,s[40:43],s55 offen offset:128  lds
s_add_u32 m0,s57,9088
s_nop 0
buffer_load_dwordx4 v19,s[40:43],s55 offen offset:128  lds
v_add_u32 v26,16384,v20
v_add_u32 v27,16384,v21
T a[128:129],26,0
T a[130:131],26,512
T a[132:133],27,0
T a[134:135],27,512
T a[136:137],26,4096
T a[138:139],26,4608
T a[140:141],27,4096
T a[142:143],27,4608
s_waitcnt vmcnt(20) lgkmcnt(0)
s_barrier
s_add_u32 s55,s50,6881280
s_add_u32 m0,s57,16384
s_nop 0
buffer_load_dwordx4 v18,s[40:43],s55 offen offset:0  lds
s_add_u32 m0,s57,17408
s_nop 0
buffer_load_dwordx4 v19,s[40:43],s55 offen offset:0  lds
s_add_u32 m0,s57,24448
s_nop 0
buffer_load_dwordx4 v18,s[40:43],s55 offen offset:128  lds
s_add_u32 m0,s57,25472
s_nop 0
buffer_load_dwordx4 v19,s[40:43],s55 offen offset:128  lds
v_add_u32 v26,32768,v20
v_add_u32 v27,32768,v21
T a[144:145],26,0
T a[146:147],26,512
T a[148:149],27,0
T a[150:151],27,512
T a[152:153],26,4096
T a[154:155],26,4608
T a[156:157],27,4096
T a[158:159],27,4608
s_waitcnt vmcnt(20) lgkmcnt(0)
s_barrier
v_add_u32 v26,49152,v20
v_add_u32 v27,49152,v21
T a[160:161],26,0
T a[162:163],26,512
T a[164:165],27,0
T a[166:167],27,512
T a[168:169],26,4096
T a[170:171],26,4608
T a[172:173],27,4096
T a[174:175],27,4608
s_waitcnt vmcnt(16) lgkmcnt(0)
s_barrier
v_add_u32 v26,65536,v20
v_add_u32 v27,65536,v21
T a[176:177],26,0
T a[178:179],26,512
T a[180:181],27,0
T a[182:183],27,512
T a[184:185],26,4096
T a[186:187],26,4608
T a[188:189],27,4096
T a[190:191],27,4608
s_waitcnt vmcnt(12) lgkmcnt(0)
s_barrier
v_add_u32 v26,81920,v20
v_add_u32 v27,81920,v21
T a[192:193],26,0
T a[194:195],26,512
T a[196:197],27,0
T a[198:199],27,512
T a[200:201],26,4096
T a[202:203],26,4608
T a[204:205],27,4096
T a[206:207],27,4608
s_waitcnt vmcnt(8) lgkmcnt(0)
s_barrier
v_add_u32 v26,98304,v20
v_add_u32 v27,98304,v21
T a[208:209],26,0
T a[210:211],26,512
T a[212:213],27,0
T a[214:215],27,512
T a[216:217],26,4096
T a[218:219],26,4608
T a[220:221],27,4096
T a[222:223],27,4608
s_waitcnt vmcnt(4) lgkmcnt(0)
s_barrier
v_add_u32 v26,0,v20
v_add_u32 v27,0,v21
T a[224:225],26,0
T a[226:227],26,512
T a[228:229],27,0
T a[230:231],27,512
T a[232:233],26,4096
T a[234:235],26,4608
T a[236:237],27,4096
T a[238:239],27,4608
s_waitcnt vmcnt(0) lgkmcnt(0)
s_barrier
v_add_u32 v26,16384,v20
v_add_u32 v27,16384,v21
T a[240:241],26,0
T a[242:243],26,512
T a[244:245],27,0
T a[246:247],27,512
T a[248:249],26,4096
T a[250:251],26,4608
T a[252:253],27,4096
T a[254:255],27,4608
L 0
s_barrier
v_mov_b32 v34,0x3f803f80
v_mov_b32 v35,0x3f803f80
v_mov_b32 v36,0x3f803f80
v_mov_b32 v37,0x3f803f80
s_nop 1
v_mfma_f32_16x16x32_bf16 v[106:109],a[0:3],v[34:37],0
v_mfma_f32_16x16x32_bf16 v[110:113],a[4:7],v[34:37],0
M 106,a[8:11],34
M 110,a[12:15],34
M 106,a[16:19],34
M 110,a[20:23],34
M 106,a[24:27],34
M 110,a[28:31],34
M 106,a[32:35],34
M 110,a[36:39],34
M 106,a[40:43],34
M 110,a[44:47],34
M 106,a[48:51],34
M 110,a[52:55],34
M 106,a[56:59],34
M 110,a[60:63],34
M 106,a[64:67],34
M 110,a[68:71],34
M 106,a[72:75],34
M 110,a[76:79],34
M 106,a[80:83],34
M 110,a[84:87],34
M 106,a[88:91],34
M 110,a[92:95],34
M 106,a[96:99],34
M 110,a[100:103],34
M 106,a[104:107],34
M 110,a[108:111],34
M 106,a[112:115],34
M 110,a[116:119],34
M 106,a[120:123],34
M 110,a[124:127],34
M 106,a[128:131],34
M 110,a[132:135],34
M 106,a[136:139],34
M 110,a[140:143],34
M 106,a[144:147],34
M 110,a[148:151],34
M 106,a[152:155],34
M 110,a[156:159],34
M 106,a[160:163],34
M 110,a[164:167],34
M 106,a[168:171],34
M 110,a[172:175],34
M 106,a[176:179],34
)"
R"(M 110,a[180:183],34
M 106,a[184:187],34
M 110,a[188:191],34
M 106,a[192:195],34
M 110,a[196:199],34
M 106,a[200:203],34
M 110,a[204:207],34
M 106,a[208:211],34
M 110,a[212:215],34
M 106,a[216:219],34
M 110,a[220:223],34
M 106,a[224:227],34
M 110,a[228:231],34
M 106,a[232:235],34
M 110,a[236:239],34
M 106,a[240:243],34
M 110,a[244:247],34
M 106,a[248:251],34
M 110,a[252:255],34
v_mov_b32 v4,s53
s_mov_b32 s82,0
s_mov_b32 s80,0
s_mov_b32 s54,s45
s_mul_i32 s57,s44,4096
s_mul_i32 s55,s45,32768
s_add_u32 s55,s55,s57
s_add_u32 s55,s55,31457280
s_mul_i32 s57,s44,4096
s_mul_i32 s60,s46,32768
s_mul_i32 s61,s46,1024
s_mul_i32 s62,s46,114688
s_mul_i32 s63,s46,2560
s_mul_i32 s64,s46,1024
s_lshl_b32 s59,s44,8
s_add_u32 s59,s59,131072
s_mul_i32 s58,s45,1024
s_add_u32 s58,s58,109613056
s_mul_i32 s65,s45,114688
s_add_u32 s65,s65,s47
s_mul_i32 s68,s45,2560
s_add_u32 s68,s68,s48
s_mul_i32 s73,s45,1024
s_add_u32 s73,s73,107479040
s_mul_i32 s74,s45,32768
s_add_u32 s74,s74,s49
s_mul_i32 s75,s45,1024
s_add_u32 s75,s75,107479040
s_mov_b32 s66,0x7fff0000
s_mov_b32 s67,0x7fff0000
s_mov_b32 s69,0x7fff0000
s_mov_b32 s76,0x7fff0000
s_mov_b32 s77,0x7fff0000
s_mov_b32 s70,0x7fff0000
s_mov_b32 s71,0x7fff0000
s_mov_b32 s72,0x7fff0000
v_mov_b32 v1,v0
v_add_u32 v2,16384,v0
s_mov_b32 s84,16384
s_mov_b32 s86,0x3a800000
s_mov_b32 s87,0x3727c5ac
s_mov_b32 s88,0xc0135761
s_mov_b32 s89,0x3d372713
s_lshr_b32 s85,s44,1
s_mul_i32 s85,s85,127
v_lshlrev_b32 v8,22,v0
v_and_b32 v8,0x80000000,v8
v_or_b32 v8,v8,v5
v_add_u32 v2,32768,v0
s_lshl_b32 s84,s57,1
s_add_u32 s84,s84,32768
v_and_b32 v114,240,v0
v_cmp_eq_u32 vcc,0,v114
s_nop 1
v_cndmask_b32 v17,0,1.0,vcc
s_getpc_b64 s[40:41]
Lg3o_pc_%=:
s_lshl_b32 s78,s44,13
v_lshlrev_b32 v114,3,v0
v_add_u32 v114,s78,v114
v_min_u32 v114,Lcode_g3o_e-Lg3o_pc_%=-4,v114
s_mov_b32 m0,163584
s_nop 0
global_load_lds_dword v114,s[40:41]
s_add_u32 m0,s59,4096
s_nop 0
buffer_load_dword v8,s[36:39],s58 offen offset:0 sc1 lds
buffer_store_dword v4,v3,s[36:39],s71 offen offset:0 sc0 sc1
s_add_u32 m0,s59,5120
s_nop 0
buffer_load_dword v8,s[36:39],s58 offen offset:0 sc1 lds
buffer_store_dword v4,v3,s[36:39],s71 offen offset:0 sc0 sc1
s_mov_b32 s83,0
Lg3o_poll_%=:
s_add_u32 s78,s83,2
s_and_b32 s78,s78,3
s_lshl_b32 s78,s78,10
s_add_u32 s78,s78,4096
s_add_u32 m0,s59,s78
s_nop 0
buffer_load_dword v8,s[36:39],s58 offen offset:0 sc1 lds
s_and_b32 s78,s83,3
s_lshl_b32 s78,s78,10
v_add_u32 v114,s78,v6
s_waitcnt vmcnt(4)
s_barrier
ds_read_b32 v7,v114 offset:4096
buffer_store_dword v4,v3,s[36:39],s70 offen offset:0 sc0 sc1
s_mov_b32 s70,s71
s_mov_b32 s71,s72
s_mov_b32 s72,0x7fff0000
s_add_u32 s83,s83,1
s_add_u32 s82,s82,1
L 0
v_cmp_eq_u32 vcc,s53,v7
s_nop 0
s_cmp_eq_u32 vcc_lo,-1
s_cbranch_scc1 Lg3o_go_%=
s_cmp_lt_u32 s82,20000
s_cbranch_scc1 Lg3o_poll_%=
Lg3o_go_%=:
buffer_load_dword v130,v11,s[36:39],s73 offen offset:0 sc1
s_add_u32 s73,s73,s64
s_add_u32 m0,s57,0
s_add_u32 s56,s55,0
D 0,56,0
D 0,56,1024
D 0,56,2048
D 0,56,3072
s_add_u32 s58,s58,s61
s_add_u32 m0,s59,2048
s_nop 0
buffer_load_dword v8,s[36:39],s58 offen offset:0 sc1 lds
s_add_u32 m0,s57,16384
s_add_u32 s56,s55,16384
D 0,56,0
D 0,56,1024
D 0,56,2048
D 0,56,3072
s_add_u32 s55,s55,s60
s_sub_u32 s57,s84,s57
s_add_u32 s54,s54,s46
s_waitcnt vmcnt(4)
s_barrier
buffer_store_dword v4,v3,s[36:39],s70 offen offset:0 sc0 sc1
s_min_u32 s71,s71,s72
buffer_store_dword v4,v3,s[36:39],s71 offen offset:0 sc0 sc1
s_mov_b32 s70,0x7fff0000
s_mov_b32 s71,0x7fff0000
s_mov_b32 s72,0x7fff0000
s_cmp_eq_u32 s80,1
s_cbranch_scc1 Lg3o_st1_%=
s_branch Lg3o_st0_%=
Lg3o_gox_%=:
s_add_u32 m0,s59,2048
s_nop 0
buffer_load_dword v8,s[36:39],s58 offen offset:0 sc1 lds
s_waitcnt vmcnt(0)
s_barrier
buffer_store_dword v4,v3,s[36:39],s70 offen offset:0 sc0 sc1
s_min_u32 s71,s71,s72
buffer_store_dword v4,v3,s[36:39],s71 offen offset:0 sc0 sc1
s_mov_b32 s70,0x7fff0000
s_mov_b32 s71,0x7fff0000
s_mov_b32 s72,0x7fff0000
s_cmp_eq_u32 s80,1
s_cbranch_scc1 Lg3o_st1_%=
Lg3o_st0_%=:
ds_read_b32 v7,v6 offset:2048
R 34,1,0
R 38,1,1024
R 42,1,2048
R 46,1,3072
R 50,1,4096
L 5
v_cmp_eq_u32 vcc,s53,v7
L 3
s_cmp_eq_u32 vcc_lo,-1
s_cbranch_scc1 Lg3o_F0_%=
s_branch Lg3o_L0_%=
Lg3o_st1_%=:
ds_read_b32 v7,v6 offset:2048
R 34,2,0
R 38,2,1024
R 42,2,2048
R 46,2,3072
R 50,2,4096
L 5
v_cmp_eq_u32 vcc,s53,v7
L 3
s_cmp_eq_u32 vcc_lo,-1
s_cbranch_scc1 Lg3o_F1_%=
s_branch Lg3o_L1_%=
Lg3o_F0_%=:
v_mfma_f32_16x16x32_bf16 v[22:25],a[4:7],v[34:37],0
s_add_u32 s58,s58,s61
s_add_u32 m0,s59,0
R 54,1,5120
M 22,a[12:15],38
buffer_load_dword v8,s[36:39],s58 offen offset:0 sc1 lds
v_mfma_f32_16x16x32_bf16 v[18:21],a[0:3],v[34:37],0
s_add_u32 m0,s57,0
R 58,1,6144
v_add_f32_dpp v131,v130,v130 quad_perm:[1,0,3,2] row_mask:0xf bank_mask:0xf
M 18,a[8:11],38
L 3
s_add_u32 s56,s55,0
buffer_load_dword v130,v11,s[36:39],s73 offen offset:0 sc1
M 18,a[16:19],42
s_mov_b32 s66,s65
R 62,1,7168
v_add_f32_dpp v131,v131,v131 quad_perm:[2,3,0,1] row_mask:0xf bank_mask:0xf
M 18,a[24:27],46
D 0,56,0
M 22,a[20:23],42
s_add_u32 s65,s65,s62
R 66,1,8192
v_add_f32_dpp v134,v131,v131 row_half_mirror row_mask:0xf bank_mask:0xf
M 22,a[28:31],46
L 3
D 0,56,1024
M 22,a[36:39],50
s_add_u32 s54,s54,s46
R 70,1,9216
v_mov_b32_dpp v131,v134 row_ror:8 row_mask:0xf bank_mask:0xf
M 22,a[44:47],54
s_add_u32 s73,s73,s64
v_fma_f32 v131,-v134,v134,v131
v_max_f32 v131,s87,v131
M 18,a[32:35],50
R 74,1,10240
v_mul_f32 v134,v17,v134
M 18,a[40:43],54
L 3
D 0,56,2048
M 18,a[48:51],58
R 78,1,11264
v_fma_f32 v26,-v102,v106,v26
M 18,a[56:59],62
D 0,56,3072
M 22,a[52:55],58
s_add_u32 m0,s57,16384
R 82,1,12288
v_fma_f32 v27,-v102,v107,v27
M 22,a[60:63],62
L 3
v_rsq_f32 v132,v131
M 22,a[68:71],66
s_add_u32 s56,s55,16384
R 86,1,13312
v_fma_f32 v131,v15,v131,v16
M 22,a[76:79],70
s_add_u32 s55,s55,s60
v_mul_f32 v135,v132,v132
v_fma_f32 v131,v14,v135,v131
M 18,a[64:67],66
s_sub_u32 s57,s84,s57
R 90,1,14336
v_fma_f32 v134,v132,v131,v134
M 18,a[72:75],70
L 3
ds_write_b32 v12,v134 offset:0
M 18,a[80:83],74
s_waitcnt vmcnt(8)
R 94,1,15360
v_fma_f32 v28,-v102,v108,v28
M 18,a[88:91],78
s_barrier
v_fma_f32 v29,-v102,v109,v29
v_mul_f32 v114,v26,v26
M 22,a[84:87],74
R 34,1,16384
v_mul_f32 v115,v27,v27
M 22,a[92:95],78
L 4
v_mul_f32 v116,v28,v28
v_mul_f32 v117,v29,v29
M 22,a[100:103],82
R 38,1,17408
v_fma_f32 v114,v104,v114,v103
M 22,a[108:111],86
D 0,56,0
M 18,a[96:99],82
R 42,1,18432
v_fma_f32 v115,v104,v115,v103
M 18,a[104:107],86
L 3
v_fma_f32 v116,v104,v116,v103
v_fma_f32 v117,v104,v117,v103
M 18,a[112:115],90
R 46,1,19456
v_mul_f32 v114,v26,v114
M 18,a[120:123],94
v_exp_f32 v114,v114
M 22,a[116:119],90
R 50,1,20480
v_mul_f32 v115,v27,v115
M 22,a[124:127],94
L 3
v_exp_f32 v115,v115
M 22,a[132:135],34
R 54,1,21504
v_mul_f32 v116,v28,v116
M 22,a[140:143],38
D 0,56,1024
M 18,a[128:131],34
R 58,1,22528
v_mul_f32 v117,v29,v117
M 18,a[136:139],38
L 3
v_exp_f32 v116,v116
M 18,a[144:147],42
R 62,1,23552
v_fma_f32 v114,v114,v105,v105
M 18,a[152:155],46
v_exp_f32 v117,v117
M 22,a[148:151],42
R 66,1,24576
v_fma_f32 v115,v115,v105,v105
M 22,a[156:159],46
L 3
D 0,56,2048
M 22,a[164:167],50
R 70,1,25600
v_fma_f32 v116,v116,v105,v105
M 22,a[172:175],54
v_rcp_f32 v114,v114
M 18,a[160:163],50
R 74,1,26624
v_fma_f32 v117,v117,v105,v105
M 18,a[168:171],54
L 3
v_rcp_f32 v115,v115
M 18,a[176:179],58
R 78,1,27648
v_mul_f32 v26,v26,v114
M 18,a[184:187],62
v_rcp_f32 v116,v116
M 22,a[180:183],58
R 82,1,28672
v_mul_f32 v27,v27,v115
M 22,a[188:191],62
L 3
D 0,56,3072
M 22,a[196:199],66
R 86,1,29696
v_mul_f32 v28,v28,v116
M 22,a[204:207],70
v_rcp_f32 v117,v117
M 18,a[192:195],66
R 90,1,30720
v_mul_f32 v29,v29,v117
M 18,a[200:203],70
L 3
s_nop 0
v_cvt_pk_bf16_f32 v122,v26,v27
M 18,a[208:211],74
s_waitcnt vmcnt(4)
R 94,1,31744
v_fma_f32 v30,-v102,v110,v30
M 18,a[216:219],78
s_barrier
ds_read_b32 v7,v6 offset:0
R 98,13,0
M 22,a[212:215],74
R 34,2,0
v_fma_f32 v31,-v102,v111,v31
M 22,a[220:223],78
L 5
buffer_store_dword v4,v3,s[36:39],s70 offen offset:0 sc0 sc1
M 22,a[228:231],82
s_mov_b32 s70,s69
R 38,2,1024
v_fma_f32 v32,-v102,v112,v32
M 22,a[236:239],86
s_mov_b32 s69,s68
v_fma_f32 v33,-v102,v113,v33
v_mul_f32 v118,v30,v30
M 18,a[224:227],82
s_add_u32 s68,s68,s63
R 42,2,2048
v_mul_f32 v119,v31,v31
M 18,a[232:235],86
L 5
v_mul_f32 v120,v32,v32
v_mul_f32 v121,v33,v33
v_fma_f32 v118,v104,v118,v103
v_fma_f32 v119,v104,v119,v103
v_fma_f32 v120,v104,v120,v103
v_fma_f32 v121,v104,v121,v103
v_mul_f32 v118,v30,v118
v_mul_f32 v119,v31,v119
v_mul_f32 v120,v32,v120
v_mul_f32 v121,v33,v121
v_exp_f32 v118,v118
v_exp_f32 v119,v119
v_exp_f32 v120,v120
v_exp_f32 v121,v121
M 18,a[240:243],90
L 4
R 46,2,3072
v_fma_f32 v118,v118,v105,v105
v_fma_f32 v119,v119,v105,v105
v_fma_f32 v120,v120,v105,v105
v_fma_f32 v121,v121,v105,v105
v_rcp_f32 v118,v118
v_rcp_f32 v119,v119
v_rcp_f32 v120,v120
v_rcp_f32 v121,v121
v_cmp_eq_u32 vcc,s53,v7
M 18,a[248:251],94
v_cvt_pk_bf16_f32 v123,v28,v29
v_mul_f32 v30,v30,v118
v_mul_f32 v31,v31,v119
v_mul_f32 v32,v32,v120
v_mul_f32 v33,v33,v121
v_cvt_pk_bf16_f32 v124,v30,v31
v_cvt_pk_bf16_f32 v125,v32,v33
M 22,a[244:247],90
L 2
s_cmp_eq_u32 vcc_lo,-1
R 50,2,4096
buffer_store_dwordx4 v[122:125],v0,s[36:39],s67 offen offset:0 sc0 sc1
M 22,a[252:255],94
s_cbranch_scc0 Lg3o_K1_%=
Lg3o_F1_%=:
v_mfma_f32_16x16x32_bf16 v[30:33],a[4:7],v[34:37],0
s_add_u32 s58,s58,s61
s_add_u32 m0,s59,1024
R 54,2,5120
M 30,a[12:15],38
buffer_load_dword v8,s[36:39],s58 offen offset:0 sc1 lds
v_mfma_f32_16x16x32_bf16 v[26:29],a[0:3],v[34:37],0
s_add_u32 m0,s57,0
R 58,2,6144
v_add_f32_dpp v131,v130,v130 quad_perm:[1,0,3,2] row_mask:0xf bank_mask:0xf
M 26,a[8:11],38
L 3
s_add_u32 s56,s55,0
buffer_load_dword v130,v11,s[36:39],s73 offen offset:0 sc1
M 26,a[16:19],42
s_mov_b32 s67,s65
R 62,2,7168
v_add_f32_dpp v131,v131,v131 quad_perm:[2,3,0,1] row_mask:0xf bank_mask:0xf
M 26,a[24:27],46
D 0,56,0
M 30,a[20:23],42
s_add_u32 s65,s65,s62
R 66,2,8192
v_add_f32_dpp v134,v131,v131 row_half_mirror row_mask:0xf bank_mask:0xf
M 30,a[28:31],46
L 3
D 0,56,1024
M 30,a[36:39],50
s_add_u32 s54,s54,s46
R 70,2,9216
v_mov_b32_dpp v131,v134 row_ror:8 row_mask:0xf bank_mask:0xf
M 30,a[44:47],54
s_add_u32 s73,s73,s64
v_fma_f32 v131,-v134,v134,v131
v_max_f32 v131,s87,v131
M 26,a[32:35],50
R 74,2,10240
v_mul_f32 v134,v17,v134
M 26,a[40:43],54
L 3
D 0,56,2048
M 26,a[48:51],58
R 78,2,11264
v_fma_f32 v18,-v98,v106,v18
M 26,a[56:59],62
D 0,56,3072
M 30,a[52:55],58
s_add_u32 m0,s57,16384
R 82,2,12288
v_fma_f32 v19,-v98,v107,v19
M 30,a[60:63],62
L 3
v_rsq_f32 v132,v131
M 30,a[68:71],66
s_add_u32 s56,s55,16384
R 86,2,13312
v_fma_f32 v131,v15,v131,v16
M 30,a[76:79],70
s_add_u32 s55,s55,s60
v_mul_f32 v135,v132,v132
v_fma_f32 v131,v14,v135,v131
M 26,a[64:67],66
s_sub_u32 s57,s84,s57
R 90,2,14336
v_fma_f32 v134,v132,v131,v134
M 26,a[72:75],70
L 3
ds_write_b32 v12,v134 offset:2048
M 26,a[80:83],74
s_waitcnt vmcnt(8)
R 94,2,15360
v_fma_f32 v20,-v98,v108,v20
M 26,a[88:91],78
s_barrier
v_fma_f32 v21,-v98,v109,v21
)"
R"(v_mul_f32 v114,v18,v18
M 30,a[84:87],74
R 34,2,16384
v_mul_f32 v115,v19,v19
M 30,a[92:95],78
L 4
v_mul_f32 v116,v20,v20
v_mul_f32 v117,v21,v21
M 30,a[100:103],82
R 38,2,17408
v_fma_f32 v114,v100,v114,v99
M 30,a[108:111],86
D 0,56,0
M 26,a[96:99],82
R 42,2,18432
v_fma_f32 v115,v100,v115,v99
M 26,a[104:107],86
L 3
v_fma_f32 v116,v100,v116,v99
v_fma_f32 v117,v100,v117,v99
M 26,a[112:115],90
R 46,2,19456
v_mul_f32 v114,v18,v114
M 26,a[120:123],94
v_exp_f32 v114,v114
M 30,a[116:119],90
R 50,2,20480
v_mul_f32 v115,v19,v115
M 30,a[124:127],94
L 3
v_exp_f32 v115,v115
M 30,a[132:135],34
R 54,2,21504
v_mul_f32 v116,v20,v116
M 30,a[140:143],38
D 0,56,1024
M 26,a[128:131],34
R 58,2,22528
v_mul_f32 v117,v21,v117
M 26,a[136:139],38
L 3
v_exp_f32 v116,v116
M 26,a[144:147],42
R 62,2,23552
v_fma_f32 v114,v114,v101,v101
M 26,a[152:155],46
v_exp_f32 v117,v117
M 30,a[148:151],42
R 66,2,24576
v_fma_f32 v115,v115,v101,v101
M 30,a[156:159],46
L 3
D 0,56,2048
M 30,a[164:167],50
R 70,2,25600
v_fma_f32 v116,v116,v101,v101
M 30,a[172:175],54
v_rcp_f32 v114,v114
M 26,a[160:163],50
R 74,2,26624
v_fma_f32 v117,v117,v101,v101
M 26,a[168:171],54
L 3
v_rcp_f32 v115,v115
M 26,a[176:179],58
R 78,2,27648
v_mul_f32 v18,v18,v114
M 26,a[184:187],62
v_rcp_f32 v116,v116
M 30,a[180:183],58
R 82,2,28672
v_mul_f32 v19,v19,v115
M 30,a[188:191],62
L 3
D 0,56,3072
M 30,a[196:199],66
R 86,2,29696
v_mul_f32 v20,v20,v116
M 30,a[204:207],70
v_rcp_f32 v117,v117
M 26,a[192:195],66
R 90,2,30720
v_mul_f32 v21,v21,v117
M 26,a[200:203],70
L 3
s_nop 0
v_cvt_pk_bf16_f32 v122,v18,v19
M 26,a[208:211],74
s_waitcnt vmcnt(4)
R 94,2,31744
v_fma_f32 v22,-v98,v110,v22
M 26,a[216:219],78
s_barrier
ds_read_b32 v7,v6 offset:1024
R 102,13,2048
M 30,a[212:215],74
R 34,1,0
v_fma_f32 v23,-v98,v111,v23
M 30,a[220:223],78
L 5
buffer_store_dword v4,v3,s[36:39],s70 offen offset:0 sc0 sc1
M 30,a[228:231],82
s_mov_b32 s70,s69
R 38,1,1024
v_fma_f32 v24,-v98,v112,v24
M 30,a[236:239],86
s_mov_b32 s69,s68
v_fma_f32 v25,-v98,v113,v25
v_mul_f32 v118,v22,v22
M 26,a[224:227],82
s_add_u32 s68,s68,s63
R 42,1,2048
v_mul_f32 v119,v23,v23
M 26,a[232:235],86
L 5
v_mul_f32 v120,v24,v24
v_mul_f32 v121,v25,v25
v_fma_f32 v118,v100,v118,v99
v_fma_f32 v119,v100,v119,v99
v_fma_f32 v120,v100,v120,v99
v_fma_f32 v121,v100,v121,v99
v_mul_f32 v118,v22,v118
v_mul_f32 v119,v23,v119
v_mul_f32 v120,v24,v120
v_mul_f32 v121,v25,v121
v_exp_f32 v118,v118
v_exp_f32 v119,v119
v_exp_f32 v120,v120
v_exp_f32 v121,v121
M 26,a[240:243],90
L 4
R 46,1,3072
v_fma_f32 v118,v118,v101,v101
v_fma_f32 v119,v119,v101,v101
v_fma_f32 v120,v120,v101,v101
v_fma_f32 v121,v121,v101,v101
v_rcp_f32 v118,v118
v_rcp_f32 v119,v119
v_rcp_f32 v120,v120
v_rcp_f32 v121,v121
v_cmp_eq_u32 vcc,s53,v7
M 26,a[248:251],94
v_cvt_pk_bf16_f32 v123,v20,v21
v_mul_f32 v22,v22,v118
v_mul_f32 v23,v23,v119
v_mul_f32 v24,v24,v120
v_mul_f32 v25,v25,v121
v_cvt_pk_bf16_f32 v124,v22,v23
v_cvt_pk_bf16_f32 v125,v24,v25
M 30,a[244:247],90
L 2
s_cmp_eq_u32 vcc_lo,-1
R 50,1,4096
buffer_store_dwordx4 v[122:125],v0,s[36:39],s66 offen offset:0 sc0 sc1
M 30,a[252:255],94
s_cbranch_scc1 Lg3o_F0_%=
Lg3o_K0_%=:
Lg3o_L0_%=:
v_mfma_f32_16x16x32_bf16 v[22:25],a[4:7],v[34:37],0
s_add_u32 m0,s59,3072
R 54,1,5120
v_add_f32_dpp v131,v130,v130 quad_perm:[1,0,3,2] row_mask:0xf bank_mask:0xf
M 22,a[12:15],38
buffer_load_dword v8,s[36:39],s58 offen offset:0 sc1 lds
v_mfma_f32_16x16x32_bf16 v[18:21],a[0:3],v[34:37],0
s_mov_b32 s66,s65
R 58,1,6144
v_add_f32_dpp v131,v131,v131 quad_perm:[2,3,0,1] row_mask:0xf bank_mask:0xf
M 18,a[8:11],38
L 3
v_fma_f32 v26,-v102,v106,v26
v_fma_f32 v27,-v102,v107,v27
M 18,a[16:19],42
s_add_u32 s65,s65,s62
R 62,1,7168
v_add_f32_dpp v134,v131,v131 row_half_mirror row_mask:0xf bank_mask:0xf
M 18,a[24:27],46
v_fma_f32 v28,-v102,v108,v28
v_fma_f32 v29,-v102,v109,v29
M 22,a[20:23],42
R 66,1,8192
v_mov_b32_dpp v131,v134 row_ror:8 row_mask:0xf bank_mask:0xf
M 22,a[28:31],46
L 3
v_fma_f32 v131,-v134,v134,v131
v_max_f32 v131,s87,v131
M 22,a[36:39],50
R 70,1,9216
v_mul_f32 v134,v17,v134
M 22,a[44:47],54
v_rsq_f32 v132,v131
M 18,a[32:35],50
R 74,1,10240
v_fma_f32 v131,v15,v131,v16
M 18,a[40:43],54
L 3
v_mul_f32 v135,v132,v132
v_fma_f32 v131,v14,v135,v131
M 18,a[48:51],58
R 78,1,11264
v_fma_f32 v134,v132,v131,v134
M 18,a[56:59],62
ds_write_b32 v12,v134 offset:0
M 22,a[52:55],58
R 82,1,12288
v_mul_f32 v114,v26,v26
M 22,a[60:63],62
L 4
v_mul_f32 v115,v27,v27
v_mul_f32 v116,v28,v28
M 22,a[68:71],66
R 86,1,13312
v_mul_f32 v117,v29,v29
M 22,a[76:79],70
v_fma_f32 v114,v104,v114,v103
v_fma_f32 v115,v104,v115,v103
M 18,a[64:67],66
R 90,1,14336
v_fma_f32 v116,v104,v116,v103
M 18,a[72:75],70
L 4
v_fma_f32 v117,v104,v117,v103
v_mul_f32 v114,v26,v114
M 18,a[80:83],74
s_waitcnt vmcnt(0)
R 94,1,15360
v_mul_f32 v115,v27,v115
M 18,a[88:91],78
s_barrier
ds_read_b32 v7,v6 offset:3072
v_mul_f32 v116,v28,v116
M 22,a[84:87],74
R 34,1,16384
v_mul_f32 v117,v29,v117
M 22,a[92:95],78
L 4
buffer_store_dword v4,v3,s[36:39],s70 offen offset:0 sc0 sc1
M 22,a[100:103],82
s_mov_b32 s70,0x7fff0000
R 38,1,17408
v_fma_f32 v30,-v102,v110,v30
M 22,a[108:111],86
v_exp_f32 v114,v114
M 18,a[96:99],82
R 42,1,18432
v_fma_f32 v114,v114,v105,v105
M 18,a[104:107],86
L 4
L 3
v_cmp_eq_u32 vcc,s53,v7
M 18,a[112:115],90
R 46,1,19456
v_fma_f32 v31,-v102,v111,v31
M 18,a[120:123],94
v_exp_f32 v115,v115
M 22,a[116:119],90
s_cmp_eq_u32 vcc_lo,-1
R 50,1,20480
v_fma_f32 v115,v115,v105,v105
s_cbranch_scc0 Lg3o_X0_%=
M 22,a[124:127],94
L 3
s_add_u32 m0,s57,0
s_add_u32 s56,s55,0
buffer_load_dword v130,v11,s[36:39],s73 offen offset:0 sc1
M 22,a[132:135],34
s_add_u32 s58,s58,s61
R 54,1,21504
v_fma_f32 v32,-v102,v112,v32
M 22,a[140:143],38
D 0,56,0
M 18,a[128:131],34
s_add_u32 s54,s54,s46
R 58,1,22528
v_fma_f32 v33,-v102,v113,v33
M 18,a[136:139],38
L 3
v_exp_f32 v116,v116
M 18,a[144:147],42
s_add_u32 s73,s73,s64
R 62,1,23552
v_fma_f32 v116,v116,v105,v105
M 18,a[152:155],46
D 0,56,1024
M 22,a[148:151],42
R 66,1,24576
v_mul_f32 v118,v30,v30
M 22,a[156:159],46
L 3
v_exp_f32 v117,v117
M 22,a[164:167],50
R 70,1,25600
v_fma_f32 v117,v117,v105,v105
M 22,a[172:175],54
D 0,56,2048
M 18,a[160:163],50
R 74,1,26624
v_mul_f32 v119,v31,v31
M 18,a[168:171],54
L 3
v_rcp_f32 v114,v114
M 18,a[176:179],58
R 78,1,27648
v_mul_f32 v26,v26,v114
M 18,a[184:187],62
D 0,56,3072
M 22,a[180:183],58
s_add_u32 m0,s59,0
R 82,1,28672
v_mul_f32 v120,v32,v32
M 22,a[188:191],62
L 3
buffer_load_dword v8,s[36:39],s58 offen offset:0 sc1 lds
M 22,a[196:199],66
s_add_u32 m0,s57,16384
R 86,1,29696
v_mul_f32 v121,v33,v33
M 22,a[204:207],70
s_add_u32 s56,s55,16384
v_rcp_f32 v115,v115
M 18,a[192:195],66
s_add_u32 s55,s55,s60
R 90,1,30720
v_mul_f32 v27,v27,v115
M 18,a[200:203],70
L 3
s_nop 0
D 0,56,0
M 18,a[208:211],74
s_waitcnt vmcnt(1)
R 94,1,31744
v_cvt_pk_bf16_f32 v122,v26,v27
M 18,a[216:219],78
s_barrier
ds_read_b32 v7,v6 offset:0
R 98,13,0
M 22,a[212:215],74
s_sub_u32 s57,s84,s57
R 34,2,0
v_fma_f32 v118,v104,v118,v103
M 22,a[220:223],78
L 5
buffer_store_dword v4,v3,s[36:39],s70 offen offset:0 sc0 sc1
M 22,a[228:231],82
s_mov_b32 s70,s69
R 38,2,1024
v_fma_f32 v119,v104,v119,v103
M 22,a[236:239],86
D 0,56,1024
M 18,a[224:227],82
s_mov_b32 s69,s68
R 42,2,2048
v_fma_f32 v120,v104,v120,v103
M 18,a[232:235],86
L 5
v_fma_f32 v121,v104,v121,v103
v_mul_f32 v118,v30,v118
v_mul_f32 v119,v31,v119
v_mul_f32 v120,v32,v120
v_mul_f32 v121,v33,v121
v_exp_f32 v118,v118
v_exp_f32 v119,v119
v_exp_f32 v120,v120
v_exp_f32 v121,v121
M 18,a[240:243],90
L 4
R 46,2,3072
v_rcp_f32 v116,v116
v_rcp_f32 v117,v117
v_fma_f32 v118,v118,v105,v105
v_fma_f32 v119,v119,v105,v105
v_fma_f32 v120,v120,v105,v105
v_fma_f32 v121,v121,v105,v105
v_rcp_f32 v118,v118
v_rcp_f32 v119,v119
v_rcp_f32 v120,v120
v_rcp_f32 v121,v121
v_cmp_eq_u32 vcc,s53,v7
M 18,a[248:251],94
s_add_u32 s68,s68,s63
v_mul_f32 v28,v28,v116
v_mul_f32 v29,v29,v117
v_cvt_pk_bf16_f32 v123,v28,v29
v_mul_f32 v30,v30,v118
v_mul_f32 v31,v31,v119
v_mul_f32 v32,v32,v120
v_mul_f32 v33,v33,v121
v_cvt_pk_bf16_f32 v124,v30,v31
v_cvt_pk_bf16_f32 v125,v32,v33
M 22,a[244:247],90
L 2
s_cmp_eq_u32 vcc_lo,-1
R 50,2,4096
buffer_store_dwordx4 v[122:125],v0,s[36:39],s67 offen offset:0 sc0 sc1
M 22,a[252:255],94
D 0,56,2048
D 0,56,3072
buffer_store_dword v4,v3,s[36:39],s71 offen offset:0 sc0 sc1
buffer_store_dword v4,v3,s[36:39],s71 offen offset:0 sc0 sc1
s_cbranch_scc1 Lg3o_F1_%=
s_branch Lg3o_K1_%=
Lg3o_X0_%=:
M 22,a[124:127],94
L 3
s_add_u32 m0,s59,0
v_fma_f32 v32,-v102,v112,v32
M 22,a[132:135],34
R 54,1,21504
v_fma_f32 v33,-v102,v113,v33
M 22,a[140:143],38
buffer_load_dword v8,s[36:39],s58 offen offset:0 sc1 lds
M 18,a[128:131],34
R 58,1,22528
v_mul_f32 v118,v30,v30
M 18,a[136:139],38
L 3
v_exp_f32 v116,v116
M 18,a[144:147],42
R 62,1,23552
v_fma_f32 v116,v116,v105,v105
M 18,a[152:155],46
v_exp_f32 v117,v117
M 22,a[148:151],42
R 66,1,24576
v_fma_f32 v117,v117,v105,v105
M 22,a[156:159],46
L 3
v_rcp_f32 v114,v114
v_mul_f32 v119,v31,v31
M 22,a[164:167],50
R 70,1,25600
v_mul_f32 v26,v26,v114
M 22,a[172:175],54
v_rcp_f32 v115,v115
M 18,a[160:163],50
R 74,1,26624
v_mul_f32 v27,v27,v115
M 18,a[168:171],54
L 3
v_rcp_f32 v116,v116
M 18,a[176:179],58
R 78,1,27648
v_mul_f32 v28,v28,v116
M 18,a[184:187],62
v_rcp_f32 v117,v117
M 22,a[180:183],58
R 82,1,28672
v_mul_f32 v29,v29,v117
M 22,a[188:191],62
L 3
v_cvt_pk_bf16_f32 v122,v26,v27
v_cvt_pk_bf16_f32 v123,v28,v29
M 22,a[196:199],66
R 86,1,29696
v_mul_f32 v120,v32,v32
M 22,a[204:207],70
v_mul_f32 v121,v33,v33
v_fma_f32 v118,v104,v118,v103
M 18,a[192:195],66
R 90,1,30720
v_fma_f32 v119,v104,v119,v103
M 18,a[200:203],70
L 3
s_nop 0
v_fma_f32 v120,v104,v120,v103
M 18,a[208:211],74
s_waitcnt vmcnt(0)
R 94,1,31744
v_fma_f32 v121,v104,v121,v103
M 18,a[216:219],78
s_barrier
buffer_store_dword v4,v3,s[36:39],s70 offen offset:0 sc0 sc1
M 22,a[212:215],74
s_mov_b32 s70,s69
ds_read_b32 v7,v6 offset:0
R 98,13,0
M 22,a[220:223],78
L 4
v_mul_f32 v118,v30,v118
v_mul_f32 v119,v31,v119
M 22,a[228:231],82
s_mov_b32 s69,s68
v_mul_f32 v120,v32,v120
v_mul_f32 v121,v33,v121
M 22,a[236:239],86
s_add_u32 s68,s68,s63
v_exp_f32 v118,v118
M 18,a[224:227],82
v_exp_f32 v119,v119
M 18,a[232:235],86
L 2
v_exp_f32 v120,v120
v_exp_f32 v121,v121
M 18,a[240:243],90
L 1
v_fma_f32 v118,v118,v105,v105
v_fma_f32 v119,v119,v105,v105
v_fma_f32 v120,v120,v105,v105
v_fma_f32 v121,v121,v105,v105
v_rcp_f32 v118,v118
)"
R"(v_rcp_f32 v119,v119
v_rcp_f32 v120,v120
v_rcp_f32 v121,v121
M 18,a[248:251],94
v_mul_f32 v30,v30,v118
v_mul_f32 v31,v31,v119
v_mul_f32 v32,v32,v120
v_mul_f32 v33,v33,v121
v_cvt_pk_bf16_f32 v124,v30,v31
v_cvt_pk_bf16_f32 v125,v32,v33
M 22,a[244:247],90
buffer_store_dwordx4 v[122:125],v0,s[36:39],s67 offen offset:0 sc0 sc1
v_cmp_eq_u32 vcc,s53,v7
M 22,a[252:255],94
s_mov_b32 s80,1
s_cmp_eq_u32 vcc_lo,-1
s_cbranch_scc0 Lg3o_xn0_%=
buffer_load_dword v130,v11,s[36:39],s73 offen offset:0 sc1
L 0
v_fma_f32 v18,-v98,v106,v18
v_fma_f32 v19,-v98,v107,v19
v_fma_f32 v20,-v98,v108,v20
v_fma_f32 v21,-v98,v109,v21
v_mul_f32 v114,v18,v18
v_mul_f32 v115,v19,v19
s_add_u32 s73,s73,s64
s_add_u32 m0,s57,0
s_add_u32 s56,s55,0
D 0,56,0
v_mul_f32 v116,v20,v20
v_mul_f32 v117,v21,v21
v_fma_f32 v114,v100,v114,v99
v_fma_f32 v115,v100,v115,v99
v_fma_f32 v116,v100,v116,v99
v_fma_f32 v117,v100,v117,v99
D 0,56,1024
v_mul_f32 v114,v18,v114
v_mul_f32 v115,v19,v115
v_mul_f32 v116,v20,v116
v_mul_f32 v117,v21,v117
v_exp_f32 v114,v114
v_exp_f32 v115,v115
D 0,56,2048
v_exp_f32 v116,v116
v_exp_f32 v117,v117
v_fma_f32 v114,v114,v101,v101
v_fma_f32 v115,v115,v101,v101
v_fma_f32 v116,v116,v101,v101
v_fma_f32 v117,v117,v101,v101
D 0,56,3072
v_rcp_f32 v114,v114
v_rcp_f32 v115,v115
v_rcp_f32 v116,v116
v_rcp_f32 v117,v117
v_mul_f32 v18,v18,v114
v_mul_f32 v19,v19,v115
s_add_u32 s58,s58,s61
s_add_u32 m0,s57,16384
s_add_u32 s56,s55,16384
D 0,56,0
v_mul_f32 v20,v20,v116
v_mul_f32 v21,v21,v117
v_cvt_pk_bf16_f32 v122,v18,v19
v_cvt_pk_bf16_f32 v123,v20,v21
v_fma_f32 v22,-v98,v110,v22
v_fma_f32 v23,-v98,v111,v23
D 0,56,1024
v_fma_f32 v24,-v98,v112,v24
v_fma_f32 v25,-v98,v113,v25
v_mul_f32 v118,v22,v22
v_mul_f32 v119,v23,v23
v_mul_f32 v120,v24,v24
v_mul_f32 v121,v25,v25
D 0,56,2048
v_fma_f32 v118,v100,v118,v99
v_fma_f32 v119,v100,v119,v99
v_fma_f32 v120,v100,v120,v99
v_fma_f32 v121,v100,v121,v99
v_mul_f32 v118,v22,v118
v_mul_f32 v119,v23,v119
D 0,56,3072
v_mul_f32 v120,v24,v120
v_mul_f32 v121,v25,v121
v_exp_f32 v118,v118
v_exp_f32 v119,v119
v_exp_f32 v120,v120
v_exp_f32 v121,v121
s_add_u32 s55,s55,s60
s_sub_u32 s57,s84,s57
s_add_u32 s54,s54,s46
v_fma_f32 v118,v118,v101,v101
v_fma_f32 v119,v119,v101,v101
v_fma_f32 v120,v120,v101,v101
v_fma_f32 v121,v121,v101,v101
v_rcp_f32 v118,v118
v_rcp_f32 v119,v119
v_rcp_f32 v120,v120
v_rcp_f32 v121,v121
v_mul_f32 v22,v22,v118
v_mul_f32 v23,v23,v119
v_mul_f32 v24,v24,v120
v_mul_f32 v25,v25,v121
v_cvt_pk_bf16_f32 v124,v22,v23
v_cvt_pk_bf16_f32 v125,v24,v25
buffer_store_dwordx4 v[122:125],v0,s[36:39],s66 offen offset:0 sc0 sc1
s_mov_b32 s72,s69
s_mov_b32 s69,0x7fff0000
s_mov_b32 s66,0x7fff0000
s_mov_b32 s76,0x7fff0000
s_branch Lg3o_gox_%=
Lg3o_xn0_%=:
s_cmp_ge_u32 s54,256
s_cbranch_scc1 Lg3o_fin0_%=
s_add_u32 m0,s59,4096
s_nop 0
buffer_load_dword v8,s[36:39],s58 offen offset:0 sc1 lds
buffer_store_dword v4,v3,s[36:39],s71 offen offset:0 sc0 sc1
s_add_u32 m0,s59,5120
s_nop 0
buffer_load_dword v8,s[36:39],s58 offen offset:0 sc1 lds
buffer_store_dword v4,v3,s[36:39],s71 offen offset:0 sc0 sc1
s_mov_b32 s83,0
L 0
v_fma_f32 v18,-v98,v106,v18
v_fma_f32 v19,-v98,v107,v19
v_fma_f32 v20,-v98,v108,v20
v_fma_f32 v21,-v98,v109,v21
v_mul_f32 v114,v18,v18
v_mul_f32 v115,v19,v19
v_mul_f32 v116,v20,v20
v_mul_f32 v117,v21,v21
v_fma_f32 v114,v100,v114,v99
v_fma_f32 v115,v100,v115,v99
v_fma_f32 v116,v100,v116,v99
v_fma_f32 v117,v100,v117,v99
v_mul_f32 v114,v18,v114
v_mul_f32 v115,v19,v115
v_mul_f32 v116,v20,v116
v_mul_f32 v117,v21,v117
v_exp_f32 v114,v114
v_exp_f32 v115,v115
v_exp_f32 v116,v116
v_exp_f32 v117,v117
v_fma_f32 v114,v114,v101,v101
v_fma_f32 v115,v115,v101,v101
v_fma_f32 v116,v116,v101,v101
v_fma_f32 v117,v117,v101,v101
v_rcp_f32 v114,v114
v_rcp_f32 v115,v115
v_rcp_f32 v116,v116
v_rcp_f32 v117,v117
v_mul_f32 v18,v18,v114
v_mul_f32 v19,v19,v115
v_mul_f32 v20,v20,v116
v_mul_f32 v21,v21,v117
v_cvt_pk_bf16_f32 v122,v18,v19
v_cvt_pk_bf16_f32 v123,v20,v21
v_fma_f32 v22,-v98,v110,v22
v_fma_f32 v23,-v98,v111,v23
v_fma_f32 v24,-v98,v112,v24
v_fma_f32 v25,-v98,v113,v25
v_mul_f32 v118,v22,v22
v_mul_f32 v119,v23,v23
v_mul_f32 v120,v24,v24
v_mul_f32 v121,v25,v25
v_fma_f32 v118,v100,v118,v99
v_fma_f32 v119,v100,v119,v99
v_fma_f32 v120,v100,v120,v99
v_fma_f32 v121,v100,v121,v99
v_mul_f32 v118,v22,v118
v_mul_f32 v119,v23,v119
v_mul_f32 v120,v24,v120
v_mul_f32 v121,v25,v121
v_exp_f32 v118,v118
v_exp_f32 v119,v119
v_exp_f32 v120,v120
v_exp_f32 v121,v121
v_fma_f32 v118,v118,v101,v101
v_fma_f32 v119,v119,v101,v101
v_fma_f32 v120,v120,v101,v101
v_fma_f32 v121,v121,v101,v101
v_rcp_f32 v118,v118
v_rcp_f32 v119,v119
v_rcp_f32 v120,v120
v_rcp_f32 v121,v121
v_mul_f32 v22,v22,v118
v_mul_f32 v23,v23,v119
v_mul_f32 v24,v24,v120
v_mul_f32 v25,v25,v121
v_cvt_pk_bf16_f32 v124,v22,v23
v_cvt_pk_bf16_f32 v125,v24,v25
buffer_store_dwordx4 v[122:125],v0,s[36:39],s66 offen offset:0 sc0 sc1
s_mov_b32 s72,s69
s_mov_b32 s69,0x7fff0000
s_mov_b32 s66,0x7fff0000
s_mov_b32 s76,0x7fff0000
s_branch Lg3o_poll_%=
Lg3o_fin0_%=:
L 0
v_fma_f32 v18,-v98,v106,v18
v_fma_f32 v19,-v98,v107,v19
v_fma_f32 v20,-v98,v108,v20
v_fma_f32 v21,-v98,v109,v21
v_mul_f32 v114,v18,v18
v_mul_f32 v115,v19,v19
v_mul_f32 v116,v20,v20
v_mul_f32 v117,v21,v21
v_fma_f32 v114,v100,v114,v99
v_fma_f32 v115,v100,v115,v99
v_fma_f32 v116,v100,v116,v99
v_fma_f32 v117,v100,v117,v99
v_mul_f32 v114,v18,v114
v_mul_f32 v115,v19,v115
v_mul_f32 v116,v20,v116
v_mul_f32 v117,v21,v117
v_exp_f32 v114,v114
v_exp_f32 v115,v115
v_exp_f32 v116,v116
v_exp_f32 v117,v117
v_fma_f32 v114,v114,v101,v101
v_fma_f32 v115,v115,v101,v101
v_fma_f32 v116,v116,v101,v101
v_fma_f32 v117,v117,v101,v101
v_rcp_f32 v114,v114
v_rcp_f32 v115,v115
v_rcp_f32 v116,v116
v_rcp_f32 v117,v117
v_mul_f32 v18,v18,v114
v_mul_f32 v19,v19,v115
v_mul_f32 v20,v20,v116
v_mul_f32 v21,v21,v117
v_cvt_pk_bf16_f32 v122,v18,v19
v_cvt_pk_bf16_f32 v123,v20,v21
v_fma_f32 v22,-v98,v110,v22
v_fma_f32 v23,-v98,v111,v23
v_fma_f32 v24,-v98,v112,v24
v_fma_f32 v25,-v98,v113,v25
v_mul_f32 v118,v22,v22
v_mul_f32 v119,v23,v23
v_mul_f32 v120,v24,v24
v_mul_f32 v121,v25,v25
v_fma_f32 v118,v100,v118,v99
v_fma_f32 v119,v100,v119,v99
v_fma_f32 v120,v100,v120,v99
v_fma_f32 v121,v100,v121,v99
v_mul_f32 v118,v22,v118
v_mul_f32 v119,v23,v119
v_mul_f32 v120,v24,v120
v_mul_f32 v121,v25,v121
v_exp_f32 v118,v118
v_exp_f32 v119,v119
v_exp_f32 v120,v120
v_exp_f32 v121,v121
v_fma_f32 v118,v118,v101,v101
v_fma_f32 v119,v119,v101,v101
v_fma_f32 v120,v120,v101,v101
v_fma_f32 v121,v121,v101,v101
v_rcp_f32 v118,v118
v_rcp_f32 v119,v119
v_rcp_f32 v120,v120
v_rcp_f32 v121,v121
v_mul_f32 v22,v22,v118
v_mul_f32 v23,v23,v119
v_mul_f32 v24,v24,v120
v_mul_f32 v25,v25,v121
v_cvt_pk_bf16_f32 v124,v22,v23
v_cvt_pk_bf16_f32 v125,v24,v25
buffer_store_dwordx4 v[122:125],v0,s[36:39],s66 offen offset:0 sc0 sc1
s_mov_b32 s72,s69
s_mov_b32 s69,0x7fff0000
s_mov_b32 s66,0x7fff0000
s_mov_b32 s76,0x7fff0000
s_branch Lg3o_done_%=
Lg3o_K1_%=:
Lg3o_L1_%=:
v_mfma_f32_16x16x32_bf16 v[30:33],a[4:7],v[34:37],0
s_add_u32 m0,s59,3072
R 54,2,5120
v_add_f32_dpp v131,v130,v130 quad_perm:[1,0,3,2] row_mask:0xf bank_mask:0xf
M 30,a[12:15],38
buffer_load_dword v8,s[36:39],s58 offen offset:0 sc1 lds
v_mfma_f32_16x16x32_bf16 v[26:29],a[0:3],v[34:37],0
s_mov_b32 s67,s65
R 58,2,6144
v_add_f32_dpp v131,v131,v131 quad_perm:[2,3,0,1] row_mask:0xf bank_mask:0xf
M 26,a[8:11],38
L 3
v_fma_f32 v18,-v98,v106,v18
v_fma_f32 v19,-v98,v107,v19
M 26,a[16:19],42
s_add_u32 s65,s65,s62
R 62,2,7168
v_add_f32_dpp v134,v131,v131 row_half_mirror row_mask:0xf bank_mask:0xf
M 26,a[24:27],46
v_fma_f32 v20,-v98,v108,v20
v_fma_f32 v21,-v98,v109,v21
M 30,a[20:23],42
R 66,2,8192
v_mov_b32_dpp v131,v134 row_ror:8 row_mask:0xf bank_mask:0xf
M 30,a[28:31],46
L 3
v_fma_f32 v131,-v134,v134,v131
v_max_f32 v131,s87,v131
M 30,a[36:39],50
R 70,2,9216
v_mul_f32 v134,v17,v134
M 30,a[44:47],54
v_rsq_f32 v132,v131
M 26,a[32:35],50
R 74,2,10240
v_fma_f32 v131,v15,v131,v16
M 26,a[40:43],54
L 3
v_mul_f32 v135,v132,v132
v_fma_f32 v131,v14,v135,v131
M 26,a[48:51],58
R 78,2,11264
v_fma_f32 v134,v132,v131,v134
M 26,a[56:59],62
ds_write_b32 v12,v134 offset:2048
M 30,a[52:55],58
R 82,2,12288
v_mul_f32 v114,v18,v18
M 30,a[60:63],62
L 4
v_mul_f32 v115,v19,v19
v_mul_f32 v116,v20,v20
M 30,a[68:71],66
R 86,2,13312
v_mul_f32 v117,v21,v21
M 30,a[76:79],70
v_fma_f32 v114,v100,v114,v99
v_fma_f32 v115,v100,v115,v99
M 26,a[64:67],66
R 90,2,14336
v_fma_f32 v116,v100,v116,v99
M 26,a[72:75],70
L 4
v_fma_f32 v117,v100,v117,v99
v_mul_f32 v114,v18,v114
M 26,a[80:83],74
s_waitcnt vmcnt(0)
R 94,2,15360
v_mul_f32 v115,v19,v115
M 26,a[88:91],78
s_barrier
ds_read_b32 v7,v6 offset:3072
v_mul_f32 v116,v20,v116
M 30,a[84:87],74
R 34,2,16384
v_mul_f32 v117,v21,v117
M 30,a[92:95],78
L 4
buffer_store_dword v4,v3,s[36:39],s70 offen offset:0 sc0 sc1
M 30,a[100:103],82
s_mov_b32 s70,0x7fff0000
R 38,2,17408
v_fma_f32 v22,-v98,v110,v22
M 30,a[108:111],86
v_exp_f32 v114,v114
M 26,a[96:99],82
R 42,2,18432
v_fma_f32 v114,v114,v101,v101
M 26,a[104:107],86
L 4
L 3
v_cmp_eq_u32 vcc,s53,v7
M 26,a[112:115],90
R 46,2,19456
v_fma_f32 v23,-v98,v111,v23
M 26,a[120:123],94
v_exp_f32 v115,v115
M 30,a[116:119],90
s_cmp_eq_u32 vcc_lo,-1
R 50,2,20480
v_fma_f32 v115,v115,v101,v101
s_cbranch_scc0 Lg3o_X1_%=
M 30,a[124:127],94
L 3
s_add_u32 m0,s57,0
s_add_u32 s56,s55,0
buffer_load_dword v130,v11,s[36:39],s73 offen offset:0 sc1
M 30,a[132:135],34
s_add_u32 s58,s58,s61
R 54,2,21504
v_fma_f32 v24,-v98,v112,v24
M 30,a[140:143],38
D 0,56,0
M 26,a[128:131],34
s_add_u32 s54,s54,s46
R 58,2,22528
v_fma_f32 v25,-v98,v113,v25
M 26,a[136:139],38
L 3
v_exp_f32 v116,v116
M 26,a[144:147],42
s_add_u32 s73,s73,s64
R 62,2,23552
v_fma_f32 v116,v116,v101,v101
M 26,a[152:155],46
D 0,56,1024
M 30,a[148:151],42
R 66,2,24576
v_mul_f32 v118,v22,v22
M 30,a[156:159],46
L 3
v_exp_f32 v117,v117
M 30,a[164:167],50
R 70,2,25600
v_fma_f32 v117,v117,v101,v101
M 30,a[172:175],54
D 0,56,2048
M 26,a[160:163],50
R 74,2,26624
v_mul_f32 v119,v23,v23
M 26,a[168:171],54
L 3
v_rcp_f32 v114,v114
M 26,a[176:179],58
R 78,2,27648
v_mul_f32 v18,v18,v114
M 26,a[184:187],62
D 0,56,3072
M 30,a[180:183],58
s_add_u32 m0,s59,1024
R 82,2,28672
v_mul_f32 v120,v24,v24
M 30,a[188:191],62
L 3
buffer_load_dword v8,s[36:39],s58 offen offset:0 sc1 lds
M 30,a[196:199],66
s_add_u32 m0,s57,16384
R 86,2,29696
v_mul_f32 v121,v25,v25
M 30,a[204:207],70
s_add_u32 s56,s55,16384
v_rcp_f32 v115,v115
M 26,a[192:195],66
s_add_u32 s55,s55,s60
R 90,2,30720
v_mul_f32 v19,v19,v115
M 26,a[200:203],70
L 3
s_nop 0
D 0,56,0
M 26,a[208:211],74
s_waitcnt vmcnt(1)
R 94,2,31744
v_cvt_pk_bf16_f32 v122,v18,v19
M 26,a[216:219],78
s_barrier
ds_read_b32 v7,v6 offset:1024
R 102,13,2048
M 30,a[212:215],74
s_sub_u32 s57,s84,s57
R 34,1,0
v_fma_f32 v118,v100,v118,v99
M 30,a[220:223],78
L 5
buffer_store_dword v4,v3,s[36:39],s70 offen offset:0 sc0 sc1
M 30,a[228:231],82
s_mov_b32 s70,s69
R 38,1,1024
v_fma_f32 v119,v100,v119,v99
M 30,a[236:239],86
D 0,56,1024
M 26,a[224:227],82
s_mov_b32 s69,s68
R 42,1,2048
v_fma_f32 v120,v100,v120,v99
M 26,a[232:235],86
L 5
v_fma_f32 v121,v100,v121,v99
v_mul_f32 v118,v22,v118
v_mul_f32 v119,v23,v119
v_mul_f32 v120,v24,v120
v_mul_f32 v121,v25,v121
v_exp_f32 v118,v118
v_exp_f32 v119,v119
v_exp_f32 v120,v120
v_exp_f32 v121,v121
M 26,a[240:243],90
L 4
R 46,1,3072
v_rcp_f32 v116,v116
v_rcp_f32 v117,v117
v_fma_f32 v118,v118,v101,v101
v_fma_f32 v119,v119,v101,v101
v_fma_f32 v120,v120,v101,v101
v_fma_f32 v121,v121,v101,v101
v_rcp_f32 v118,v118
v_rcp_f32 v119,v119
v_rcp_f32 v120,v120
v_rcp_f32 v121,v121
v_cmp_eq_u32 vcc,s53,v7
M 26,a[248:251],94
s_add_u32 s68,s68,s63
v_mul_f32 v20,v20,v116
v_mul_f32 v21,v21,v117
v_cvt_pk_bf16_f32 v123,v20,v21
)"
R"(v_mul_f32 v22,v22,v118
v_mul_f32 v23,v23,v119
v_mul_f32 v24,v24,v120
v_mul_f32 v25,v25,v121
v_cvt_pk_bf16_f32 v124,v22,v23
v_cvt_pk_bf16_f32 v125,v24,v25
M 30,a[244:247],90
L 2
s_cmp_eq_u32 vcc_lo,-1
R 50,1,4096
buffer_store_dwordx4 v[122:125],v0,s[36:39],s66 offen offset:0 sc0 sc1
M 30,a[252:255],94
D 0,56,2048
D 0,56,3072
buffer_store_dword v4,v3,s[36:39],s71 offen offset:0 sc0 sc1
buffer_store_dword v4,v3,s[36:39],s71 offen offset:0 sc0 sc1
s_cbranch_scc1 Lg3o_F0_%=
s_branch Lg3o_K0_%=
Lg3o_X1_%=:
M 30,a[124:127],94
L 3
s_add_u32 m0,s59,1024
v_fma_f32 v24,-v98,v112,v24
M 30,a[132:135],34
R 54,2,21504
v_fma_f32 v25,-v98,v113,v25
M 30,a[140:143],38
buffer_load_dword v8,s[36:39],s58 offen offset:0 sc1 lds
M 26,a[128:131],34
R 58,2,22528
v_mul_f32 v118,v22,v22
M 26,a[136:139],38
L 3
v_exp_f32 v116,v116
M 26,a[144:147],42
R 62,2,23552
v_fma_f32 v116,v116,v101,v101
M 26,a[152:155],46
v_exp_f32 v117,v117
M 30,a[148:151],42
R 66,2,24576
v_fma_f32 v117,v117,v101,v101
M 30,a[156:159],46
L 3
v_rcp_f32 v114,v114
v_mul_f32 v119,v23,v23
M 30,a[164:167],50
R 70,2,25600
v_mul_f32 v18,v18,v114
M 30,a[172:175],54
v_rcp_f32 v115,v115
M 26,a[160:163],50
R 74,2,26624
v_mul_f32 v19,v19,v115
M 26,a[168:171],54
L 3
v_rcp_f32 v116,v116
M 26,a[176:179],58
R 78,2,27648
v_mul_f32 v20,v20,v116
M 26,a[184:187],62
v_rcp_f32 v117,v117
M 30,a[180:183],58
R 82,2,28672
v_mul_f32 v21,v21,v117
M 30,a[188:191],62
L 3
v_cvt_pk_bf16_f32 v122,v18,v19
v_cvt_pk_bf16_f32 v123,v20,v21
M 30,a[196:199],66
R 86,2,29696
v_mul_f32 v120,v24,v24
M 30,a[204:207],70
v_mul_f32 v121,v25,v25
v_fma_f32 v118,v100,v118,v99
M 26,a[192:195],66
R 90,2,30720
v_fma_f32 v119,v100,v119,v99
M 26,a[200:203],70
L 3
s_nop 0
v_fma_f32 v120,v100,v120,v99
M 26,a[208:211],74
s_waitcnt vmcnt(0)
R 94,2,31744
v_fma_f32 v121,v100,v121,v99
M 26,a[216:219],78
s_barrier
buffer_store_dword v4,v3,s[36:39],s70 offen offset:0 sc0 sc1
M 30,a[212:215],74
s_mov_b32 s70,s69
ds_read_b32 v7,v6 offset:1024
R 102,13,2048
M 30,a[220:223],78
L 4
v_mul_f32 v118,v22,v118
v_mul_f32 v119,v23,v119
M 30,a[228:231],82
s_mov_b32 s69,s68
v_mul_f32 v120,v24,v120
v_mul_f32 v121,v25,v121
M 30,a[236:239],86
s_add_u32 s68,s68,s63
v_exp_f32 v118,v118
M 26,a[224:227],82
v_exp_f32 v119,v119
M 26,a[232:235],86
L 2
v_exp_f32 v120,v120
v_exp_f32 v121,v121
M 26,a[240:243],90
L 1
v_fma_f32 v118,v118,v101,v101
v_fma_f32 v119,v119,v101,v101
v_fma_f32 v120,v120,v101,v101
v_fma_f32 v121,v121,v101,v101
v_rcp_f32 v118,v118
v_rcp_f32 v119,v119
v_rcp_f32 v120,v120
v_rcp_f32 v121,v121
M 26,a[248:251],94
v_mul_f32 v22,v22,v118
v_mul_f32 v23,v23,v119
v_mul_f32 v24,v24,v120
v_mul_f32 v25,v25,v121
v_cvt_pk_bf16_f32 v124,v22,v23
v_cvt_pk_bf16_f32 v125,v24,v25
M 30,a[244:247],90
buffer_store_dwordx4 v[122:125],v0,s[36:39],s66 offen offset:0 sc0 sc1
v_cmp_eq_u32 vcc,s53,v7
M 30,a[252:255],94
s_mov_b32 s80,0
s_cmp_eq_u32 vcc_lo,-1
s_cbranch_scc0 Lg3o_xn1_%=
buffer_load_dword v130,v11,s[36:39],s73 offen offset:0 sc1
L 0
v_fma_f32 v26,-v102,v106,v26
v_fma_f32 v27,-v102,v107,v27
v_fma_f32 v28,-v102,v108,v28
v_fma_f32 v29,-v102,v109,v29
v_mul_f32 v114,v26,v26
v_mul_f32 v115,v27,v27
s_add_u32 s73,s73,s64
s_add_u32 m0,s57,0
s_add_u32 s56,s55,0
D 0,56,0
v_mul_f32 v116,v28,v28
v_mul_f32 v117,v29,v29
v_fma_f32 v114,v104,v114,v103
v_fma_f32 v115,v104,v115,v103
v_fma_f32 v116,v104,v116,v103
v_fma_f32 v117,v104,v117,v103
D 0,56,1024
v_mul_f32 v114,v26,v114
v_mul_f32 v115,v27,v115
v_mul_f32 v116,v28,v116
v_mul_f32 v117,v29,v117
v_exp_f32 v114,v114
v_exp_f32 v115,v115
D 0,56,2048
v_exp_f32 v116,v116
v_exp_f32 v117,v117
v_fma_f32 v114,v114,v105,v105
v_fma_f32 v115,v115,v105,v105
v_fma_f32 v116,v116,v105,v105
v_fma_f32 v117,v117,v105,v105
D 0,56,3072
v_rcp_f32 v114,v114
v_rcp_f32 v115,v115
v_rcp_f32 v116,v116
v_rcp_f32 v117,v117
v_mul_f32 v26,v26,v114
v_mul_f32 v27,v27,v115
s_add_u32 s58,s58,s61
s_add_u32 m0,s57,16384
s_add_u32 s56,s55,16384
D 0,56,0
v_mul_f32 v28,v28,v116
v_mul_f32 v29,v29,v117
v_cvt_pk_bf16_f32 v122,v26,v27
v_cvt_pk_bf16_f32 v123,v28,v29
v_fma_f32 v30,-v102,v110,v30
v_fma_f32 v31,-v102,v111,v31
D 0,56,1024
v_fma_f32 v32,-v102,v112,v32
v_fma_f32 v33,-v102,v113,v33
v_mul_f32 v118,v30,v30
v_mul_f32 v119,v31,v31
v_mul_f32 v120,v32,v32
v_mul_f32 v121,v33,v33
D 0,56,2048
v_fma_f32 v118,v104,v118,v103
v_fma_f32 v119,v104,v119,v103
v_fma_f32 v120,v104,v120,v103
v_fma_f32 v121,v104,v121,v103
v_mul_f32 v118,v30,v118
v_mul_f32 v119,v31,v119
D 0,56,3072
v_mul_f32 v120,v32,v120
v_mul_f32 v121,v33,v121
v_exp_f32 v118,v118
v_exp_f32 v119,v119
v_exp_f32 v120,v120
v_exp_f32 v121,v121
s_add_u32 s55,s55,s60
s_sub_u32 s57,s84,s57
s_add_u32 s54,s54,s46
v_fma_f32 v118,v118,v105,v105
v_fma_f32 v119,v119,v105,v105
v_fma_f32 v120,v120,v105,v105
v_fma_f32 v121,v121,v105,v105
v_rcp_f32 v118,v118
v_rcp_f32 v119,v119
v_rcp_f32 v120,v120
v_rcp_f32 v121,v121
v_mul_f32 v30,v30,v118
v_mul_f32 v31,v31,v119
v_mul_f32 v32,v32,v120
v_mul_f32 v33,v33,v121
v_cvt_pk_bf16_f32 v124,v30,v31
v_cvt_pk_bf16_f32 v125,v32,v33
buffer_store_dwordx4 v[122:125],v0,s[36:39],s67 offen offset:0 sc0 sc1
s_mov_b32 s72,s69
s_mov_b32 s69,0x7fff0000
s_mov_b32 s67,0x7fff0000
s_mov_b32 s77,0x7fff0000
s_branch Lg3o_gox_%=
Lg3o_xn1_%=:
s_cmp_ge_u32 s54,256
s_cbranch_scc1 Lg3o_fin1_%=
s_add_u32 m0,s59,4096
s_nop 0
buffer_load_dword v8,s[36:39],s58 offen offset:0 sc1 lds
buffer_store_dword v4,v3,s[36:39],s71 offen offset:0 sc0 sc1
s_add_u32 m0,s59,5120
s_nop 0
buffer_load_dword v8,s[36:39],s58 offen offset:0 sc1 lds
buffer_store_dword v4,v3,s[36:39],s71 offen offset:0 sc0 sc1
s_mov_b32 s83,0
L 0
v_fma_f32 v26,-v102,v106,v26
v_fma_f32 v27,-v102,v107,v27
v_fma_f32 v28,-v102,v108,v28
v_fma_f32 v29,-v102,v109,v29
v_mul_f32 v114,v26,v26
v_mul_f32 v115,v27,v27
v_mul_f32 v116,v28,v28
v_mul_f32 v117,v29,v29
v_fma_f32 v114,v104,v114,v103
v_fma_f32 v115,v104,v115,v103
v_fma_f32 v116,v104,v116,v103
v_fma_f32 v117,v104,v117,v103
v_mul_f32 v114,v26,v114
v_mul_f32 v115,v27,v115
v_mul_f32 v116,v28,v116
v_mul_f32 v117,v29,v117
v_exp_f32 v114,v114
v_exp_f32 v115,v115
v_exp_f32 v116,v116
v_exp_f32 v117,v117
v_fma_f32 v114,v114,v105,v105
v_fma_f32 v115,v115,v105,v105
v_fma_f32 v116,v116,v105,v105
v_fma_f32 v117,v117,v105,v105
v_rcp_f32 v114,v114
v_rcp_f32 v115,v115
v_rcp_f32 v116,v116
v_rcp_f32 v117,v117
v_mul_f32 v26,v26,v114
v_mul_f32 v27,v27,v115
v_mul_f32 v28,v28,v116
v_mul_f32 v29,v29,v117
v_cvt_pk_bf16_f32 v122,v26,v27
v_cvt_pk_bf16_f32 v123,v28,v29
v_fma_f32 v30,-v102,v110,v30
v_fma_f32 v31,-v102,v111,v31
v_fma_f32 v32,-v102,v112,v32
v_fma_f32 v33,-v102,v113,v33
v_mul_f32 v118,v30,v30
v_mul_f32 v119,v31,v31
v_mul_f32 v120,v32,v32
v_mul_f32 v121,v33,v33
v_fma_f32 v118,v104,v118,v103
v_fma_f32 v119,v104,v119,v103
v_fma_f32 v120,v104,v120,v103
v_fma_f32 v121,v104,v121,v103
v_mul_f32 v118,v30,v118
v_mul_f32 v119,v31,v119
v_mul_f32 v120,v32,v120
v_mul_f32 v121,v33,v121
v_exp_f32 v118,v118
v_exp_f32 v119,v119
v_exp_f32 v120,v120
v_exp_f32 v121,v121
v_fma_f32 v118,v118,v105,v105
v_fma_f32 v119,v119,v105,v105
v_fma_f32 v120,v120,v105,v105
v_fma_f32 v121,v121,v105,v105
v_rcp_f32 v118,v118
v_rcp_f32 v119,v119
v_rcp_f32 v120,v120
v_rcp_f32 v121,v121
v_mul_f32 v30,v30,v118
v_mul_f32 v31,v31,v119
v_mul_f32 v32,v32,v120
v_mul_f32 v33,v33,v121
v_cvt_pk_bf16_f32 v124,v30,v31
v_cvt_pk_bf16_f32 v125,v32,v33
buffer_store_dwordx4 v[122:125],v0,s[36:39],s67 offen offset:0 sc0 sc1
s_mov_b32 s72,s69
s_mov_b32 s69,0x7fff0000
s_mov_b32 s67,0x7fff0000
s_mov_b32 s77,0x7fff0000
s_branch Lg3o_poll_%=
Lg3o_fin1_%=:
L 0
v_fma_f32 v26,-v102,v106,v26
v_fma_f32 v27,-v102,v107,v27
v_fma_f32 v28,-v102,v108,v28
v_fma_f32 v29,-v102,v109,v29
v_mul_f32 v114,v26,v26
v_mul_f32 v115,v27,v27
v_mul_f32 v116,v28,v28
v_mul_f32 v117,v29,v29
v_fma_f32 v114,v104,v114,v103
v_fma_f32 v115,v104,v115,v103
v_fma_f32 v116,v104,v116,v103
v_fma_f32 v117,v104,v117,v103
v_mul_f32 v114,v26,v114
v_mul_f32 v115,v27,v115
v_mul_f32 v116,v28,v116
v_mul_f32 v117,v29,v117
v_exp_f32 v114,v114
v_exp_f32 v115,v115
v_exp_f32 v116,v116
v_exp_f32 v117,v117
v_fma_f32 v114,v114,v105,v105
v_fma_f32 v115,v115,v105,v105
v_fma_f32 v116,v116,v105,v105
v_fma_f32 v117,v117,v105,v105
v_rcp_f32 v114,v114
v_rcp_f32 v115,v115
v_rcp_f32 v116,v116
v_rcp_f32 v117,v117
v_mul_f32 v26,v26,v114
v_mul_f32 v27,v27,v115
v_mul_f32 v28,v28,v116
v_mul_f32 v29,v29,v117
v_cvt_pk_bf16_f32 v122,v26,v27
v_cvt_pk_bf16_f32 v123,v28,v29
v_fma_f32 v30,-v102,v110,v30
v_fma_f32 v31,-v102,v111,v31
v_fma_f32 v32,-v102,v112,v32
v_fma_f32 v33,-v102,v113,v33
v_mul_f32 v118,v30,v30
v_mul_f32 v119,v31,v31
v_mul_f32 v120,v32,v32
v_mul_f32 v121,v33,v33
v_fma_f32 v118,v104,v118,v103
v_fma_f32 v119,v104,v119,v103
v_fma_f32 v120,v104,v120,v103
v_fma_f32 v121,v104,v121,v103
v_mul_f32 v118,v30,v118
v_mul_f32 v119,v31,v119
v_mul_f32 v120,v32,v120
v_mul_f32 v121,v33,v121
v_exp_f32 v118,v118
v_exp_f32 v119,v119
v_exp_f32 v120,v120
v_exp_f32 v121,v121
v_fma_f32 v118,v118,v105,v105
v_fma_f32 v119,v119,v105,v105
v_fma_f32 v120,v120,v105,v105
v_fma_f32 v121,v121,v105,v105
v_rcp_f32 v118,v118
v_rcp_f32 v119,v119
v_rcp_f32 v120,v120
v_rcp_f32 v121,v121
v_mul_f32 v30,v30,v118
v_mul_f32 v31,v31,v119
v_mul_f32 v32,v32,v120
v_mul_f32 v33,v33,v121
v_cvt_pk_bf16_f32 v124,v30,v31
v_cvt_pk_bf16_f32 v125,v32,v33
buffer_store_dwordx4 v[122:125],v0,s[36:39],s67 offen offset:0 sc0 sc1
s_mov_b32 s72,s69
s_mov_b32 s69,0x7fff0000
s_mov_b32 s67,0x7fff0000
s_mov_b32 s77,0x7fff0000
s_branch Lg3o_done_%=
Lg3o_done_%=:
s_waitcnt vmcnt(1)
s_barrier
buffer_store_dword v4,v3,s[36:39],s70 offen offset:0 sc0 sc1
s_waitcnt vmcnt(1)
s_barrier
buffer_store_dword v4,v3,s[36:39],s72 offen offset:0 sc0 sc1
s_waitcnt vmcnt(0) lgkmcnt(0)
s_barrier
s_endpgm
Lcode_g3o_e:
.purgem M
.purgem R
.purgem T
.purgem D
.purgem L
)"
  :: "s"(ws), "s"(__builtin_amdgcn_readfirstlane(sWave)), "s"(__builtin_amdgcn_readfirstlane(sG0)), "s"(__builtin_amdgcn_readfirstlane(sStep)), "s"(__builtin_amdgcn_readfirstlane(sOutOff)), "s"(__builtin_amdgcn_readfirstlane(sFoutOff)), "s"(__builtin_amdgcn_readfirstlane(sAuxOff)), "s"(__builtin_amdgcn_readfirstlane(sWw)), "s"(__builtin_amdgcn_readfirstlane(sBuf)), "s"(__builtin_amdgcn_readfirstlane(sExp)), "v"(vL16), "v"(vFlag), "v"(vSnapOff), "v"(vSnapRd), "v"(vWd0), "v"(vWd1), "v"(vWr0), "v"(vWr1), "v"(vWr2), "v"(vOutS), "v"(vAux0), "v"(vAux1), "v"(vAux2), "v"(vAux3), "v"(vAn), "v"(vAe), "s"(w3), "s"(out) : "v255", "v0", "v1", "v2", "v3", "v4", "v5", "v6", "v7", "v8", "v9", "v10", "v11", "v12", "v13", "v14", "v15", "v16", "v17", "v18", "v19", "v20", "v21", "v22", "v23", "v24", "v25", "v26", "v27", "v28", "v29", "v30", "v31", "v32", "v33", "v34", "v35", "v36", "v37", "v38", "v39", "v40", "v41", "v42", "v43", "v44", "v45", "v46", "v47", "v48", "v49", "v50", "v51", "v52", "v53", "v54", "v55", "v56", "v57", "v58", "v59", "v60", "v61", "v62", "v63", "v64", "v65", "v66", "v67", "v68", "v69", "v70", "v71", "v72", "v73", "v74", "v75", "v76", "v77", "v78", "v79", "v80", "v81", "v82", "v83", "v84", "v85", "v86", "v87", "v88", "v89", "v90", "v91", "v92", "v93", "v94", "v95", "v96", "v97", "v98", "v99", "v100", "v101", "v102", "v103", "v104", "v105", "v106", "v107", "v108", "v109", "v110", "v111", "v112", "v113", "v114", "v115", "v116", "v117", "v118", "v119", "v120", "v121", "v122", "v123", "v124", "v125", "v126", "v127", "v128", "v129", "v130", "v131", "v132", "v133", "v134", "v135", "v136", "v137", "v138", "v139", "v140", "v141", "v142", "v143", "v144", "v145", "v146", "v147", "v148", "v149", "v150", "v151", "v152", "v153", "v154", "v155", "v156", "v157", "v158", "v159", "v160", "v161", "v162", "v163", "v164", "v165", "v166", "v167", "v168", "v169", "v170", "v171", "v172", "v173", "v174", "v175", "v176", "v177", "v178", "v179", "v180", "v181", "v182", "v183", "v184", "v185", "v186", "v187", "v188", "v189", "v190", "v191", "v192", "v193", "v194", "v195", "v196", "v197", "v198", "v199", "v200", "v201", "v202", "v203", "v204", "v205", "v206", "v207", "v208", "v209", "v210", "v211", "v212", "v213", "v214", "v215", "v216", "v217", "v218", "v219", "v220", "v221", "v222", "v223", "v224", "v225", "v226", "v227", "v228", "v229", "v230", "v231", "a0", "a1", "a2", "a3", "a4", "a5", "a6", "a7", "a8", "a9", "a10", "a11", "a12", "a13", "a14", "a15", "a16", "a17", "a18", "a19", "a20", "a21", "a22", "a23", "a24", "a25", "a26", "a27", "a28", "a29", "a30", "a31", "a32", "a33", "a34", "a35", "a36", "a37", "a38", "a39", "a40", "a41", "a42", "a43", "a44", "a45", "a46", "a47", "a48", "a49", "a50", "a51", "a52", "a53", "a54", "a55", "a56", "a57", "a58", "a59", "a60", "a61", "a62", "a63", "a64", "a65", "a66", "a67", "a68", "a69", "a70", "a71", "a72", "a73", "a74", "a75", "a76", "a77", "a78", "a79", "a80", "a81", "a82", "a83", "a84", "a85", "a86", "a87", "a88", "a89", "a90", "a91", "a92", "a93", "a94", "a95", "a96", "a97", "a98", "a99", "a100", "a101", "a102", "a103", "a104", "a105", "a106", "a107", "a108", "a109", "a110", "a111", "a112", "a113", "a114", "a115", "a116", "a117", "a118", "a119", "a120", "a121", "a122", "a123", "a124", "a125", "a126", "a127", "a128", "a129", "a130", "a131", "a132", "a133", "a134", "a135", "a136", "a137", "a138", "a139", "a140", "a141", "a142", "a143", "a144", "a145", "a146", "a147", "a148", "a149", "a150", "a151", "a152", "a153", "a154", "a155", "a156", "a157", "a158", "a159", "a160", "a161", "a162", "a163", "a164", "a165", "a166", "a167", "a168", "a169", "a170", "a171", "a172", "a173", "a174", "a175", "a176", "a177", "a178", "a179", "a180", "a181", "a182", "a183", "a184", "a185", "a186", "a187", "a188", "a189", "a190", "a191", "a192", "a193", "a194", "a195", "a196", "a197", "a198", "a199", "a200", "a201", "a202", "a203", "a204", "a205", "a206", "a207", "a208", "a209", "a210", "a211", "a212", "a213", "a214", "a215", "a216", "a217", "a218", "a219", "a220", "a221", "a222", "a223", "a224", "a225", "a226", "a227", "a228", "a229", "a230", "a231", "a232", "a233", "a234", "a235", "a236", "a237", "a238", "a239", "a240", "a241", "a242", "a243", "a244", "a245", "a246", "a247", "a248", "a249", "a250", "a251", "a252", "a253", "a254", "a255", "s36", "s37", "s38", "s39", "s40", "s41", "s42", "s43", "s44", "s45", "s46", "s47", "s48", "s49", "s50", "s51", "s52", "s53", "s54", "s55", "s56", "s57", "s58", "s59", "s60", "s61", "s62", "s63", "s64", "s65", "s66", "s67", "s68", "s69", "s70", "s71", "s72", "s73", "s74", "s75", "s76", "s77", "s78", "s79", "s80", "s81", "s82", "s83", "s84", "s85", "s86", "s87", "s88", "s89", "s90", "s91", "s92", "s93", "s94", "s95", "s96", "s97", "vcc", "scc", "memory"); }
    __builtin_unreachable();
  }
  if (kind < 3) {
    const unsigned sStep = 4;
    COMMON(kind == 1 ? 8 : 16, kind == 1 ? fj * 128 : fj < 8 ? 1024 + fj * 128 : (fj & 2) * 1024 + (fj - 8) * 128, 1024, 131072, 2048, 256, kind == 1 ? 0 : 32768);
    const int sr = l15 - ((wave & 2) ? -1 : 1), sr2 = l15 - ((wave & 2) ? -2 : 2), nx = (slice >> 1) & 1;
    const unsigned vOutS = (sr2 & 16) ? 1024 + lg * 64 + (sr2 & 3) * 16 : (lg * 16 + sr2) * 16, vSp = (sr2 & 16) ? OOB : vOutS;
    (void)vWr2;
    const unsigned vAn = kind == 1 ? HU + vL16 : (sr & 16) ? 1024 + lg * 64 + (sr & 3) * 16 : (lg * 16 + sr) * 16;
    const unsigned vAe = kind == 1 ? ((l15 >> 2) == (nx ? 0 : 3) ? (nx ? 0 : 2 * HU) + 1024 + lg * 64 + (l15 & 3) * 16 : OOB) : (sr & 16) ? OOB : vAn;
    const unsigned vAux0 = l15 * 16, vAux1 = 139264 + (l15 * 2 + (lg & 1)) * 16 + wave * 4 + (lg >> 1) * 512, vAux2 = 139264 + (l15 * 2 + (lg & 1)) * 16;
    const unsigned vAux3 = (wave == 0 && lg < 2) ? slice * 128 + l15 * 8 + lg * 4 : OOB;
    const unsigned sOutOff = kind == 1 ? OFF_HS - HU + (slice * 4 + wave) * 1280 : OFF_X1 + (slice * 4 + wave) * 1024, sFoutOff = (kind == 1 ? OFF_FH : OFF_FX1) + slice * 128;
    const unsigned sAuxOff = OFF_XP + (slice * 4 + wave) * 1024, sBuf = kind, sWw = 16 * wave * 2048 + slice * 256;
    const u16* w12 = kind == 1 ? w1 : w2;
    const unsigned sExp = __builtin_amdgcn_readfirstlane(ep);
    unsigned epOut;
    asm volatile(
R"(.macro M d,w,x
v_mfma_f32_16x16x32_bf16 v[\d:\d+3], \w, v[\x:\x+3], v[\d:\d+3]
.endm
.macro R d,a,o
ds_read_b128 v[\d:\d+3], v\a offset:\o
.endm
.macro T d,a,o
ds_read_b64_tr_b16 \d, v\a offset:\o
.endm
.macro D a,s,o
buffer_load_dwordx4 v\a, s[36:39], s\s offen offset:\o sc1 lds
.endm
.macro L c
s_waitcnt lgkmcnt(\c)
.endm
Lcode_g12_b:
s_cmp_eq_u32 %9,2
s_cbranch_scc1 Lg2_begin_%=
s_mov_b64 s[36:37],%1
s_mov_b32 s38,111243392
s_mov_b32 s39,0x00020000
s_mov_b32 s44,%2
s_mov_b32 s45,%3
s_mov_b32 s46,%4
s_mov_b32 s47,%5
s_mov_b32 s48,%6
s_mov_b32 s49,%7
s_mov_b32 s50,%8
s_mov_b32 s51,%9
s_mov_b32 s53,%10
v_mov_b32 v0,%11
v_mov_b32 v3,%12
v_mov_b32 v5,%13
v_mov_b32 v6,%14
v_mov_b32 v18,%15
v_mov_b32 v19,%16
v_mov_b32 v20,%17
v_mov_b32 v21,%18
v_mov_b32 v9,%19
v_mov_b32 v10,%20
v_mov_b32 v11,%21
v_mov_b32 v12,%22
v_mov_b32 v13,%23
v_mov_b32 v14,%24
v_mov_b32 v15,%25
v_mov_b32 v16,%26
s_mov_b64 s[40:41],%27
s_mov_b32 s42,2097152
s_mov_b32 s43,0x00020000
s_lshl_b32 s57,s44,11
s_add_u32 s55,s50,0
s_add_u32 m0,s57,0
s_nop 0
buffer_load_dwordx4 v18,s[40:43],s55 offen offset:0  lds
s_add_u32 m0,s57,1024
s_nop 0
buffer_load_dwordx4 v19,s[40:43],s55 offen offset:0  lds
s_add_u32 m0,s57,8064
s_nop 0
buffer_load_dwordx4 v18,s[40:43],s55 offen offset:128  lds
s_add_u32 m0,s57,9088
s_nop 0
buffer_load_dwordx4 v19,s[40:43],s55 offen offset:128  lds
s_add_u32 s55,s50,131072
s_add_u32 m0,s57,16384
s_nop 0
buffer_load_dwordx4 v18,s[40:43],s55 offen offset:0  lds
s_add_u32 m0,s57,17408
s_nop 0
buffer_load_dwordx4 v19,s[40:43],s55 offen offset:0  lds
s_add_u32 m0,s57,24448
s_nop 0
buffer_load_dwordx4 v18,s[40:43],s55 offen offset:128  lds
s_add_u32 m0,s57,25472
s_nop 0
buffer_load_dwordx4 v19,s[40:43],s55 offen offset:128  lds
s_add_u32 s55,s50,262144
s_add_u32 m0,s57,32768
s_nop 0
buffer_load_dwordx4 v18,s[40:43],s55 offen offset:0  lds
s_add_u32 m0,s57,33792
s_nop 0
buffer_load_dwordx4 v19,s[40:43],s55 offen offset:0  lds
s_add_u32 m0,s57,40832
s_nop 0
buffer_load_dwordx4 v18,s[40:43],s55 offen offset:128  lds
s_add_u32 m0,s57,41856
s_nop 0
buffer_load_dwordx4 v19,s[40:43],s55 offen offset:128  lds
s_add_u32 s55,s50,393216
s_add_u32 m0,s57,49152
s_nop 0
buffer_load_dwordx4 v18,s[40:43],s55 offen offset:0  lds
s_add_u32 m0,s57,50176
s_nop 0
buffer_load_dwordx4 v19,s[40:43],s55 offen offset:0  lds
s_add_u32 m0,s57,57216
s_nop 0
buffer_load_dwordx4 v18,s[40:43],s55 offen offset:128  lds
s_add_u32 m0,s57,58240
s_nop 0
buffer_load_dwordx4 v19,s[40:43],s55 offen offset:128  lds
s_add_u32 s55,s50,524288
s_add_u32 m0,s57,65536
s_nop 0
buffer_load_dwordx4 v18,s[40:43],s55 offen offset:0  lds
s_add_u32 m0,s57,66560
s_nop 0
buffer_load_dwordx4 v19,s[40:43],s55 offen offset:0  lds
s_add_u32 m0,s57,73600
s_nop 0
buffer_load_dwordx4 v18,s[40:43],s55 offen offset:128  lds
s_add_u32 m0,s57,74624
s_nop 0
buffer_load_dwordx4 v19,s[40:43],s55 offen offset:128  lds
s_add_u32 s55,s50,655360
s_add_u32 m0,s57,81920
s_nop 0
buffer_load_dwordx4 v18,s[40:43],s55 offen offset:0  lds
s_add_u32 m0,s57,82944
s_nop 0
buffer_load_dwordx4 v19,s[40:43],s55 offen offset:0  lds
s_add_u32 m0,s57,89984
s_nop 0
buffer_load_dwordx4 v18,s[40:43],s55 offen offset:128  lds
s_add_u32 m0,s57,91008
s_nop 0
buffer_load_dwordx4 v19,s[40:43],s55 offen offset:128  lds
s_waitcnt vmcnt(20) lgkmcnt(0)
s_barrier
s_add_u32 s55,s50,786432
s_add_u32 m0,s57,98304
s_nop 0
buffer_load_dwordx4 v18,s[40:43],s55 offen offset:0  lds
s_add_u32 m0,s57,99328
s_nop 0
buffer_load_dwordx4 v19,s[40:43],s55 offen offset:0  lds
s_add_u32 m0,s57,106368
s_nop 0
buffer_load_dwordx4 v18,s[40:43],s55 offen offset:128  lds
s_add_u32 m0,s57,107392
s_nop 0
buffer_load_dwordx4 v19,s[40:43],s55 offen offset:128  lds
v_add_u32 v26,0,v20
v_add_u32 v27,0,v21
T a[0:1],26,0
T a[2:3],26,512
T a[4:5],27,0
T a[6:7],27,512
T a[8:9],26,4096
T a[10:11],26,4608
T a[12:13],27,4096
T a[14:15],27,4608
s_waitcnt vmcnt(20) lgkmcnt(0)
s_barrier
s_add_u32 s55,s50,917504
s_add_u32 m0,s57,0
s_nop 0
buffer_load_dwordx4 v18,s[40:43],s55 offen offset:0  lds
s_add_u32 m0,s57,1024
s_nop 0
buffer_load_dwordx4 v19,s[40:43],s55 offen offset:0  lds
s_add_u32 m0,s57,8064
s_nop 0
buffer_load_dwordx4 v18,s[40:43],s55 offen offset:128  lds
s_add_u32 m0,s57,9088
s_nop 0
buffer_load_dwordx4 v19,s[40:43],s55 offen offset:128  lds
v_add_u32 v26,16384,v20
v_add_u32 v27,16384,v21
T a[16:17],26,0
T a[18:19],26,512
T a[20:21],27,0
T a[22:23],27,512
T a[24:25],26,4096
T a[26:27],26,4608
T a[28:29],27,4096
T a[30:31],27,4608
s_waitcnt vmcnt(20) lgkmcnt(0)
s_barrier
s_add_u32 s55,s50,1048576
s_add_u32 m0,s57,16384
s_nop 0
buffer_load_dwordx4 v18,s[40:43],s55 offen offset:0  lds
s_add_u32 m0,s57,17408
s_nop 0
buffer_load_dwordx4 v19,s[40:43],s55 offen offset:0  lds
s_add_u32 m0,s57,24448
s_nop 0
buffer_load_dwordx4 v18,s[40:43],s55 offen offset:128  lds
s_add_u32 m0,s57,25472
s_nop 0
buffer_load_dwordx4 v19,s[40:43],s55 offen offset:128  lds
v_add_u32 v26,32768,v20
v_add_u32 v27,32768,v21
T a[32:33],26,0
T a[34:35],26,512
T a[36:37],27,0
T a[38:39],27,512
T a[40:41],26,4096
T a[42:43],26,4608
T a[44:45],27,4096
T a[46:47],27,4608
s_waitcnt vmcnt(20) lgkmcnt(0)
s_barrier
s_add_u32 s55,s50,1179648
s_add_u32 m0,s57,32768
s_nop 0
buffer_load_dwordx4 v18,s[40:43],s55 offen offset:0  lds
s_add_u32 m0,s57,33792
s_nop 0
buffer_load_dwordx4 v19,s[40:43],s55 offen offset:0  lds
s_add_u32 m0,s57,40832
s_nop 0
buffer_load_dwordx4 v18,s[40:43],s55 offen offset:128  lds
s_add_u32 m0,s57,41856
s_nop 0
buffer_load_dwordx4 v19,s[40:43],s55 offen offset:128  lds
v_add_u32 v26,49152,v20
v_add_u32 v27,49152,v21
T a[48:49],26,0
T a[50:51],26,512
T a[52:53],27,0
T a[54:55],27,512
T a[56:57],26,4096
T a[58:59],26,4608
T a[60:61],27,4096
T a[62:63],27,4608
s_waitcnt vmcnt(20) lgkmcnt(0)
s_barrier
s_add_u32 s55,s50,1310720
s_add_u32 m0,s57,49152
s_nop 0
buffer_load_dwordx4 v18,s[40:43],s55 offen offset:0  lds
s_add_u32 m0,s57,50176
s_nop 0
buffer_load_dwordx4 v19,s[40:43],s55 offen offset:0  lds
s_add_u32 m0,s57,57216
s_nop 0
buffer_load_dwordx4 v18,s[40:43],s55 offen offset:128  lds
s_add_u32 m0,s57,58240
s_nop 0
buffer_load_dwordx4 v19,s[40:43],s55 offen offset:128  lds
v_add_u32 v26,65536,v20
v_add_u32 v27,65536,v21
T a[64:65],26,0
T a[66:67],26,512
T a[68:69],27,0
T a[70:71],27,512
T a[72:73],26,4096
T a[74:75],26,4608
T a[76:77],27,4096
T a[78:79],27,4608
s_waitcnt vmcnt(20) lgkmcnt(0)
s_barrier
s_add_u32 s55,s50,1441792
s_add_u32 m0,s57,65536
s_nop 0
buffer_load_dwordx4 v18,s[40:43],s55 offen offset:0  lds
s_add_u32 m0,s57,66560
s_nop 0
buffer_load_dwordx4 v19,s[40:43],s55 offen offset:0  lds
s_add_u32 m0,s57,73600
s_nop 0
buffer_load_dwordx4 v18,s[40:43],s55 offen offset:128  lds
s_add_u32 m0,s57,74624
s_nop 0
buffer_load_dwordx4 v19,s[40:43],s55 offen offset:128  lds
v_add_u32 v26,81920,v20
v_add_u32 v27,81920,v21
T a[80:81],26,0
T a[82:83],26,512
T a[84:85],27,0
T a[86:87],27,512
T a[88:89],26,4096
T a[90:91],26,4608
T a[92:93],27,4096
T a[94:95],27,4608
s_waitcnt vmcnt(20) lgkmcnt(0)
s_barrier
s_add_u32 s55,s50,1572864
s_add_u32 m0,s57,81920
s_nop 0
buffer_load_dwordx4 v18,s[40:43],s55 offen offset:0  lds
s_add_u32 m0,s57,82944
s_nop 0
buffer_load_dwordx4 v19,s[40:43],s55 offen offset:0  lds
s_add_u32 m0,s57,89984
s_nop 0
buffer_load_dwordx4 v18,s[40:43],s55 offen offset:128  lds
s_add_u32 m0,s57,91008
s_nop 0
buffer_load_dwordx4 v19,s[40:43],s55 offen offset:128  lds
v_add_u32 v26,98304,v20
v_add_u32 v27,98304,v21
T a[96:97],26,0
T a[98:99],26,512
T a[100:101],27,0
T a[102:103],27,512
T a[104:105],26,4096
T a[106:107],26,4608
T a[108:109],27,4096
T a[110:111],27,4608
s_waitcnt vmcnt(20) lgkmcnt(0)
s_barrier
s_add_u32 s55,s50,1703936
s_add_u32 m0,s57,98304
s_nop 0
buffer_load_dwordx4 v18,s[40:43],s55 offen offset:0  lds
s_add_u32 m0,s57,99328
s_nop 0
buffer_load_dwordx4 v19,s[40:43],s55 offen offset:0  lds
s_add_u32 m0,s57,106368
s_nop 0
buffer_load_dwordx4 v18,s[40:43],s55 offen offset:128  lds
s_add_u32 m0,s57,107392
s_nop 0
buffer_load_dwordx4 v19,s[40:43],s55 offen offset:128  lds
v_add_u32 v26,0,v20
v_add_u32 v27,0,v21
T a[112:113],26,0
T a[114:115],26,512
T a[116:117],27,0
T a[118:119],27,512
T a[120:121],26,4096
T a[122:123],26,4608
T a[124:125],27,4096
T a[126:127],27,4608
s_waitcnt vmcnt(20) lgkmcnt(0)
s_barrier
s_add_u32 s55,s50,1835008
s_add_u32 m0,s57,0
s_nop 0
buffer_load_dwordx4 v18,s[40:43],s55 offen offset:0  lds
s_add_u32 m0,s57,1024
s_nop 0
buffer_load_dwordx4 v19,s[40:43],s55 offen offset:0  lds
s_add_u32 m0,s57,8064
s_nop 0
buffer_load_dwordx4 v18,s[40:43],s55 offen offset:128  lds
s_add_u32 m0,s57,9088
s_nop 0
buffer_load_dwordx4 v19,s[40:43],s55 offen offset:128  lds
v_add_u32 v26,16384,v20
v_add_u32 v27,16384,v21
T a[128:129],26,0
T a[130:131],26,512
T a[132:133],27,0
T a[134:135],27,512
T a[136:137],26,4096
T a[138:139],26,4608
T a[140:141],27,4096
T a[142:143],27,4608
s_waitcnt vmcnt(20) lgkmcnt(0)
s_barrier
s_add_u32 s55,s50,1966080
s_add_u32 m0,s57,16384
s_nop 0
buffer_load_dwordx4 v18,s[40:43],s55 offen offset:0  lds
s_add_u32 m0,s57,17408
s_nop 0
buffer_load_dwordx4 v19,s[40:43],s55 offen offset:0  lds
s_add_u32 m0,s57,24448
s_nop 0
buffer_load_dwordx4 v18,s[40:43],s55 offen offset:128  lds
s_add_u32 m0,s57,25472
s_nop 0
buffer_load_dwordx4 v19,s[40:43],s55 offen offset:128  lds
v_add_u32 v26,32768,v20
v_add_u32 v27,32768,v21
T a[144:145],26,0
T a[146:147],26,512
T a[148:149],27,0
T a[150:151],27,512
T a[152:153],26,4096
T a[154:155],26,4608
T a[156:157],27,4096
T a[158:159],27,4608
s_waitcnt vmcnt(20) lgkmcnt(0)
s_barrier
v_add_u32 v26,49152,v20
v_add_u32 v27,49152,v21
T a[160:161],26,0
T a[162:163],26,512
T a[164:165],27,0
T a[166:167],27,512
T a[168:169],26,4096
T a[170:171],26,4608
T a[172:173],27,4096
T a[174:175],27,4608
s_waitcnt vmcnt(16) lgkmcnt(0)
s_barrier
v_add_u32 v26,65536,v20
v_add_u32 v27,65536,v21
T a[176:177],26,0
T a[178:179],26,512
T a[180:181],27,0
T a[182:183],27,512
T a[184:185],26,4096
T a[186:187],26,4608
T a[188:189],27,4096
T a[190:191],27,4608
s_waitcnt vmcnt(12) lgkmcnt(0)
s_barrier
v_add_u32 v26,81920,v20
v_add_u32 v27,81920,v21
T a[192:193],26,0
T a[194:195],26,512
T a[196:197],27,0
T a[198:199],27,512
T a[200:201],26,4096
T a[202:203],26,4608
T a[204:205],27,4096
T a[206:207],27,4608
s_waitcnt vmcnt(8) lgkmcnt(0)
s_barrier
v_add_u32 v26,98304,v20
v_add_u32 v27,98304,v21
T a[208:209],26,0
T a[210:211],26,512
T a[212:213],27,0
T a[214:215],27,512
T a[216:217],26,4096
T a[218:219],26,4608
T a[220:221],27,4096
T a[222:223],27,4608
s_waitcnt vmcnt(4) lgkmcnt(0)
s_barrier
v_add_u32 v26,0,v20
v_add_u32 v27,0,v21
T a[224:225],26,0
T a[226:227],26,512
T a[228:229],27,0
T a[230:231],27,512
T a[232:233],26,4096
T a[234:235],26,4608
T a[236:237],27,4096
T a[238:239],27,4608
s_waitcnt vmcnt(0) lgkmcnt(0)
s_barrier
v_add_u32 v26,16384,v20
v_add_u32 v27,16384,v21
T a[240:241],26,0
T a[242:243],26,512
T a[244:245],27,0
T a[246:247],27,512
T a[248:249],26,4096
T a[250:251],26,4608
T a[252:253],27,4096
T a[254:255],27,4608
L 0
s_barrier
v_mov_b32 v34,0x3f803f80
v_mov_b32 v35,0x3f803f80
v_mov_b32 v36,0x3f803f80
v_mov_b32 v37,0x3f803f80
s_nop 1
v_mfma_f32_16x16x32_bf16 v[106:109],a[0:3],v[34:37],0
v_mfma_f32_16x16x32_bf16 v[110:113],a[4:7],v[34:37],0
M 106,a[8:11],34
M 110,a[12:15],34
M 106,a[16:19],34
M 110,a[20:23],34
M 106,a[24:27],34
M 110,a[28:31],34
M 106,a[32:35],34
M 110,a[36:39],34
M 106,a[40:43],34
M 110,a[44:47],34
M 106,a[48:51],34
M 110,a[52:55],34
M 106,a[56:59],34
M 110,a[60:63],34
M 106,a[64:67],34
M 110,a[68:71],34
M 106,a[72:75],34
M 110,a[76:79],34
M 106,a[80:83],34
M 110,a[84:87],34
M 106,a[88:91],34
M 110,a[92:95],34
M 106,a[96:99],34
M 110,a[100:103],34
M 106,a[104:107],34
M 110,a[108:111],34
M 106,a[112:115],34
M 110,a[116:119],34
M 106,a[120:123],34
M 110,a[124:127],34
M 106,a[128:131],34
M 110,a[132:135],34
M 106,a[136:139],34
M 110,a[140:143],34
M 106,a[144:147],34
M 110,a[148:151],34
M 106,a[152:155],34
M 110,a[156:159],34
M 106,a[160:163],34
M 110,a[164:167],34
M 106,a[168:171],34
)"
R"(M 110,a[172:175],34
M 106,a[176:179],34
M 110,a[180:183],34
M 106,a[184:187],34
M 110,a[188:191],34
M 106,a[192:195],34
M 110,a[196:199],34
M 106,a[200:203],34
M 110,a[204:207],34
M 106,a[208:211],34
M 110,a[212:215],34
M 106,a[216:219],34
M 110,a[220:223],34
M 106,a[224:227],34
M 110,a[228:231],34
M 106,a[232:235],34
M 110,a[236:239],34
M 106,a[240:243],34
M 110,a[244:247],34
M 106,a[248:251],34
M 110,a[252:255],34
v_mov_b32 v4,s53
s_mov_b32 s82,0
s_mov_b32 s80,0
s_mov_b32 s54,s45
s_mul_i32 s57,s44,4096
s_mul_i32 s55,s45,32768
s_add_u32 s55,s55,s57
s_add_u32 s55,s55,8388608
s_mul_i32 s57,s44,4096
s_mul_i32 s60,s46,32768
s_mul_i32 s61,s46,1024
s_mul_i32 s62,s46,40960
s_mul_i32 s63,s46,1024
s_mul_i32 s64,s46,512
s_lshl_b32 s59,s44,8
s_add_u32 s59,s59,131072
s_mul_i32 s58,s45,1024
s_add_u32 s58,s58,109056000
s_mul_i32 s65,s45,40960
s_add_u32 s65,s65,s47
s_mul_i32 s68,s45,1024
s_add_u32 s68,s68,s48
s_mul_i32 s73,s45,512
s_add_u32 s73,s73,106954752
s_mul_i32 s74,s45,32768
s_add_u32 s74,s74,s49
s_mul_i32 s75,s45,1024
s_add_u32 s75,s75,107479040
s_mov_b32 s66,0x7fff0000
s_mov_b32 s67,0x7fff0000
s_mov_b32 s69,0x7fff0000
s_mov_b32 s76,0x7fff0000
s_mov_b32 s77,0x7fff0000
s_mov_b32 s70,0x7fff0000
s_mov_b32 s71,0x7fff0000
s_mov_b32 s72,0x7fff0000
v_mov_b32 v1,v0
v_add_u32 v2,16384,v0
s_mov_b32 s84,16384
s_mov_b32 s86,0x3a800000
s_mov_b32 s87,0x3727c5ac
s_mov_b32 s88,0xc0135761
s_mov_b32 s89,0x3d372713
s_lshr_b32 s85,s44,1
s_mul_i32 s85,s85,127
v_lshlrev_b32 v8,22,v0
v_and_b32 v8,0x80000000,v8
v_or_b32 v8,v8,v5
v_add_u32 v2,32768,v0
s_lshl_b32 s84,s57,1
s_add_u32 s84,s84,32768
s_getpc_b64 s[40:41]
Lg1_pc_%=:
s_lshl_b32 s78,s44,13
v_lshlrev_b32 v114,3,v0
v_add_u32 v114,s78,v114
v_min_u32 v114,Lcode_g2_b-Lg1_pc_%=-4,v114
s_mov_b32 m0,163584
s_nop 0
global_load_lds_dword v114,s[40:41]
s_add_u32 m0,s59,4096
s_nop 0
buffer_load_dword v8,s[36:39],s58 offen offset:0 sc1 lds
buffer_store_dword v4,v3,s[36:39],s71 offen offset:0 sc0 sc1
s_add_u32 m0,s59,5120
s_nop 0
buffer_load_dword v8,s[36:39],s58 offen offset:0 sc1 lds
buffer_store_dword v4,v3,s[36:39],s71 offen offset:0 sc0 sc1
s_mov_b32 s83,0
Lg1_poll_%=:
s_add_u32 s78,s83,2
s_and_b32 s78,s78,3
s_lshl_b32 s78,s78,10
s_add_u32 s78,s78,4096
s_add_u32 m0,s59,s78
s_nop 0
buffer_load_dword v8,s[36:39],s58 offen offset:0 sc1 lds
s_and_b32 s78,s83,3
s_lshl_b32 s78,s78,10
v_add_u32 v114,s78,v6
s_waitcnt vmcnt(4)
s_barrier
ds_read_b32 v7,v114 offset:4096
buffer_store_dword v4,v3,s[36:39],s70 offen offset:0 sc0 sc1
s_mov_b32 s70,s71
s_mov_b32 s71,s72
s_mov_b32 s72,0x7fff0000
s_add_u32 s83,s83,1
s_add_u32 s82,s82,1
L 0
v_cmp_eq_u32 vcc,s53,v7
s_nop 0
s_cmp_eq_u32 vcc_lo,-1
s_cbranch_scc1 Lg1_go_%=
s_cmp_lt_u32 s82,20000
s_cbranch_scc1 Lg1_poll_%=
Lg1_go_%=:
s_add_u32 m0,s57,0
s_add_u32 s56,s55,0
D 0,56,0
D 0,56,1024
D 0,56,2048
D 0,56,3072
s_add_u32 s58,s58,s61
s_add_u32 m0,s59,2048
s_nop 0
buffer_load_dword v8,s[36:39],s58 offen offset:0 sc1 lds
s_add_u32 m0,s57,16384
s_add_u32 s56,s55,16384
D 0,56,0
D 0,56,1024
D 0,56,2048
D 0,56,3072
s_add_u32 s55,s55,s60
s_sub_u32 s57,s84,s57
s_add_u32 s54,s54,s46
s_waitcnt vmcnt(4)
s_barrier
buffer_store_dword v4,v3,s[36:39],s70 offen offset:0 sc0 sc1
s_min_u32 s71,s71,s72
buffer_store_dword v4,v3,s[36:39],s71 offen offset:0 sc0 sc1
s_mov_b32 s70,0x7fff0000
s_mov_b32 s71,0x7fff0000
s_mov_b32 s72,0x7fff0000
s_cmp_eq_u32 s80,1
buffer_store_dword v4,v3,s[36:39],s71 offen offset:0 sc0 sc1
s_cbranch_scc1 Lg1_st1_%=
s_branch Lg1_st0_%=
Lg1_gox_%=:
s_add_u32 m0,s59,2048
s_nop 0
buffer_load_dword v8,s[36:39],s58 offen offset:0 sc1 lds
s_waitcnt vmcnt(0)
s_barrier
buffer_store_dword v4,v3,s[36:39],s70 offen offset:0 sc0 sc1
s_min_u32 s71,s71,s72
buffer_store_dword v4,v3,s[36:39],s71 offen offset:0 sc0 sc1
s_mov_b32 s70,0x7fff0000
s_mov_b32 s71,0x7fff0000
s_mov_b32 s72,0x7fff0000
s_cmp_eq_u32 s80,1
s_cbranch_scc1 Lg1_st1_%=
Lg1_st0_%=:
ds_read_b32 v7,v6 offset:2048
R 34,1,0
R 38,1,1024
R 42,1,2048
R 46,1,3072
R 50,1,4096
L 5
v_cmp_eq_u32 vcc,s53,v7
L 3
s_cmp_eq_u32 vcc_lo,-1
s_cbranch_scc1 Lg1_F0_%=
s_branch Lg1_L0_%=
Lg1_st1_%=:
ds_read_b32 v7,v6 offset:2048
R 34,2,0
R 38,2,1024
R 42,2,2048
R 46,2,3072
R 50,2,4096
L 5
v_cmp_eq_u32 vcc,s53,v7
L 3
s_cmp_eq_u32 vcc_lo,-1
s_cbranch_scc1 Lg1_F1_%=
s_branch Lg1_L1_%=
Lg1_F0_%=:
v_mfma_f32_16x16x32_bf16 v[22:25],a[4:7],v[34:37],0
s_add_u32 s58,s58,s61
s_add_u32 m0,s59,0
R 54,1,5120
M 22,a[12:15],38
buffer_load_dword v8,s[36:39],s58 offen offset:0 sc1 lds
v_mfma_f32_16x16x32_bf16 v[18:21],a[0:3],v[34:37],0
s_add_u32 m0,s57,0
R 58,1,6144
v_fma_f32 v26,-v102,v106,v26
M 18,a[8:11],38
L 3
s_add_u32 s56,s55,0
v_fma_f32 v27,-v102,v107,v27
M 18,a[16:19],42
s_mov_b32 s66,s65
R 62,1,7168
v_fma_f32 v28,-v102,v108,v28
M 18,a[24:27],46
D 0,56,0
M 22,a[20:23],42
s_add_u32 s65,s65,s62
R 66,1,8192
v_fma_f32 v29,-v102,v109,v29
v_mul_f32 v114,v26,v26
M 22,a[28:31],46
L 3
D 0,56,1024
M 22,a[36:39],50
s_add_u32 s54,s54,s46
R 70,1,9216
v_mul_f32 v115,v27,v27
M 22,a[44:47],54
v_mul_f32 v116,v28,v28
v_mul_f32 v117,v29,v29
M 18,a[32:35],50
R 74,1,10240
v_fma_f32 v114,v104,v114,v103
M 18,a[40:43],54
L 3
D 0,56,2048
M 18,a[48:51],58
R 78,1,11264
v_fma_f32 v115,v104,v115,v103
M 18,a[56:59],62
D 0,56,3072
v_fma_f32 v116,v104,v116,v103
M 22,a[52:55],58
s_add_u32 m0,s57,16384
R 82,1,12288
v_fma_f32 v117,v104,v117,v103
M 22,a[60:63],62
L 3
v_mul_f32 v114,v26,v114
v_mul_f32 v115,v27,v115
M 22,a[68:71],66
s_add_u32 s56,s55,16384
R 86,1,13312
v_mul_f32 v116,v28,v116
M 22,a[76:79],70
buffer_load_dwordx4 v[98:101],v11,s[36:39],s73 offen offset:0 sc1
M 18,a[64:67],66
s_add_u32 s73,s73,s64
R 90,1,14336
v_mul_f32 v117,v29,v117
M 18,a[72:75],70
L 3
v_exp_f32 v114,v114
v_fma_f32 v30,-v102,v110,v30
M 18,a[80:83],74
s_waitcnt vmcnt(9)
R 94,1,15360
v_fma_f32 v114,v114,v105,v105
M 18,a[88:91],78
s_barrier
v_exp_f32 v115,v115
M 22,a[84:87],74
s_add_u32 s55,s55,s60
R 34,1,16384
v_fma_f32 v115,v115,v105,v105
M 22,a[92:95],78
L 3
v_exp_f32 v116,v116
M 22,a[100:103],82
s_sub_u32 s57,s84,s57
R 38,1,17408
v_fma_f32 v116,v116,v105,v105
M 22,a[108:111],86
D 0,56,0
M 18,a[96:99],82
R 42,1,18432
v_exp_f32 v117,v117
M 18,a[104:107],86
L 3
v_rcp_f32 v114,v114
M 18,a[112:115],90
R 46,1,19456
v_fma_f32 v117,v117,v105,v105
M 18,a[120:123],94
v_rcp_f32 v115,v115
M 22,a[116:119],90
R 50,1,20480
v_mul_f32 v26,v26,v114
M 22,a[124:127],94
L 3
v_rcp_f32 v116,v116
M 22,a[132:135],34
R 54,1,21504
v_mul_f32 v27,v27,v115
M 22,a[140:143],38
D 0,56,1024
v_mul_f32 v28,v28,v116
M 18,a[128:131],34
R 58,1,22528
v_cvt_pk_bf16_f32 v122,v26,v27
M 18,a[136:139],38
L 3
v_rcp_f32 v117,v117
M 18,a[144:147],42
R 62,1,23552
v_mul_f32 v29,v29,v117
M 18,a[152:155],46
v_cvt_pk_bf16_f32 v123,v28,v29
v_fma_f32 v31,-v102,v111,v31
M 22,a[148:151],42
R 66,1,24576
v_fma_f32 v32,-v102,v112,v32
M 22,a[156:159],46
L 3
D 0,56,2048
v_fma_f32 v33,-v102,v113,v33
M 22,a[164:167],50
R 70,1,25600
v_mul_f32 v118,v30,v30
M 22,a[172:175],54
v_mul_f32 v119,v31,v31
v_mul_f32 v120,v32,v32
M 18,a[160:163],50
R 74,1,26624
v_mul_f32 v121,v33,v33
M 18,a[168:171],54
L 3
v_fma_f32 v118,v104,v118,v103
v_fma_f32 v119,v104,v119,v103
M 18,a[176:179],58
R 78,1,27648
v_fma_f32 v120,v104,v120,v103
M 18,a[184:187],62
v_fma_f32 v121,v104,v121,v103
v_mul_f32 v118,v30,v118
M 22,a[180:183],58
R 82,1,28672
v_exp_f32 v118,v118
M 22,a[188:191],62
L 3
D 0,56,3072
M 22,a[196:199],66
R 86,1,29696
v_mul_f32 v119,v31,v119
M 22,a[204:207],70
v_exp_f32 v119,v119
M 18,a[192:195],66
R 90,1,30720
v_mul_f32 v120,v32,v120
M 18,a[200:203],70
L 3
v_mul_f32 v121,v33,v121
v_fma_f32 v118,v118,v105,v105
M 18,a[208:211],74
s_waitcnt vmcnt(5)
R 94,1,31744
v_fma_f32 v119,v119,v105,v105
M 18,a[216:219],78
s_barrier
ds_read_b32 v7,v6 offset:0
v_exp_f32 v120,v120
M 22,a[212:215],74
R 34,2,0
v_fma_f32 v120,v120,v105,v105
M 22,a[220:223],78
L 4
buffer_store_dword v4,v3,s[36:39],s70 offen offset:0 sc0 sc1
v_exp_f32 v121,v121
M 22,a[228:231],82
s_mov_b32 s70,s69
R 38,2,1024
v_fma_f32 v121,v121,v105,v105
v_rcp_f32 v118,v118
v_rcp_f32 v119,v119
v_rcp_f32 v120,v120
v_rcp_f32 v121,v121
M 22,a[236:239],86
s_mov_b32 s69,s68
v_mul_f32 v30,v30,v118
v_mul_f32 v31,v31,v119
v_mul_f32 v32,v32,v120
v_mul_f32 v33,v33,v121
v_cvt_pk_bf16_f32 v124,v30,v31
v_cvt_pk_bf16_f32 v125,v32,v33
M 18,a[224:227],82
s_add_u32 s68,s68,s63
R 42,2,2048
buffer_store_dwordx4 v[122:125],v15,s[36:39],s67 offen offset:0 sc0 sc1
M 18,a[232:235],86
L 4
L 3
M 18,a[240:243],90
R 46,2,3072
v_cmp_eq_u32 vcc,s53,v7
M 18,a[248:251],94
buffer_store_dwordx4 v[122:125],v16,s[36:39],s67 offen offset:0 sc0 sc1
M 22,a[244:247],90
s_waitcnt vmcnt(7) lgkmcnt(2)
s_cmp_eq_u32 vcc_lo,-1
R 50,2,4096
M 22,a[252:255],94
s_cbranch_scc0 Lg1_K1_%=
Lg1_F1_%=:
v_mfma_f32_16x16x32_bf16 v[30:33],a[4:7],v[34:37],0
s_add_u32 s58,s58,s61
s_add_u32 m0,s59,1024
R 54,2,5120
M 30,a[12:15],38
buffer_load_dword v8,s[36:39],s58 offen offset:0 sc1 lds
v_mfma_f32_16x16x32_bf16 v[26:29],a[0:3],v[34:37],0
s_add_u32 m0,s57,0
R 58,2,6144
v_fma_f32 v18,-v98,v106,v18
M 26,a[8:11],38
L 3
s_add_u32 s56,s55,0
v_fma_f32 v19,-v98,v107,v19
M 26,a[16:19],42
s_mov_b32 s67,s65
R 62,2,7168
v_fma_f32 v20,-v98,v108,v20
M 26,a[24:27],46
D 0,56,0
M 30,a[20:23],42
s_add_u32 s65,s65,s62
R 66,2,8192
v_fma_f32 v21,-v98,v109,v21
v_mul_f32 v114,v18,v18
M 30,a[28:31],46
L 3
D 0,56,1024
M 30,a[36:39],50
s_add_u32 s54,s54,s46
R 70,2,9216
v_mul_f32 v115,v19,v19
M 30,a[44:47],54
v_mul_f32 v116,v20,v20
v_mul_f32 v117,v21,v21
M 26,a[32:35],50
R 74,2,10240
v_fma_f32 v114,v100,v114,v99
M 26,a[40:43],54
L 3
D 0,56,2048
M 26,a[48:51],58
R 78,2,11264
v_fma_f32 v115,v100,v115,v99
M 26,a[56:59],62
D 0,56,3072
v_fma_f32 v116,v100,v116,v99
M 30,a[52:55],58
s_add_u32 m0,s57,16384
R 82,2,12288
v_fma_f32 v117,v100,v117,v99
M 30,a[60:63],62
L 3
v_mul_f32 v114,v18,v114
v_mul_f32 v115,v19,v115
M 30,a[68:71],66
s_add_u32 s56,s55,16384
R 86,2,13312
v_mul_f32 v116,v20,v116
M 30,a[76:79],70
buffer_load_dwordx4 v[102:105],v11,s[36:39],s73 offen offset:0 sc1
M 26,a[64:67],66
s_add_u32 s73,s73,s64
R 90,2,14336
v_mul_f32 v117,v21,v117
M 26,a[72:75],70
L 3
v_exp_f32 v114,v114
v_fma_f32 v22,-v98,v110,v22
M 26,a[80:83],74
s_waitcnt vmcnt(9)
R 94,2,15360
v_fma_f32 v114,v114,v101,v101
M 26,a[88:91],78
s_barrier
v_exp_f32 v115,v115
M 30,a[84:87],74
s_add_u32 s55,s55,s60
R 34,2,16384
v_fma_f32 v115,v115,v101,v101
M 30,a[92:95],78
L 3
v_exp_f32 v116,v116
M 30,a[100:103],82
s_sub_u32 s57,s84,s57
R 38,2,17408
v_fma_f32 v116,v116,v101,v101
M 30,a[108:111],86
D 0,56,0
M 26,a[96:99],82
R 42,2,18432
v_exp_f32 v117,v117
)"
R"(M 26,a[104:107],86
L 3
v_rcp_f32 v114,v114
M 26,a[112:115],90
R 46,2,19456
v_fma_f32 v117,v117,v101,v101
M 26,a[120:123],94
v_rcp_f32 v115,v115
M 30,a[116:119],90
R 50,2,20480
v_mul_f32 v18,v18,v114
M 30,a[124:127],94
L 3
v_rcp_f32 v116,v116
M 30,a[132:135],34
R 54,2,21504
v_mul_f32 v19,v19,v115
M 30,a[140:143],38
D 0,56,1024
v_mul_f32 v20,v20,v116
M 26,a[128:131],34
R 58,2,22528
v_cvt_pk_bf16_f32 v122,v18,v19
M 26,a[136:139],38
L 3
v_rcp_f32 v117,v117
M 26,a[144:147],42
R 62,2,23552
v_mul_f32 v21,v21,v117
M 26,a[152:155],46
v_cvt_pk_bf16_f32 v123,v20,v21
v_fma_f32 v23,-v98,v111,v23
M 30,a[148:151],42
R 66,2,24576
v_fma_f32 v24,-v98,v112,v24
M 30,a[156:159],46
L 3
D 0,56,2048
v_fma_f32 v25,-v98,v113,v25
M 30,a[164:167],50
R 70,2,25600
v_mul_f32 v118,v22,v22
M 30,a[172:175],54
v_mul_f32 v119,v23,v23
v_mul_f32 v120,v24,v24
M 26,a[160:163],50
R 74,2,26624
v_mul_f32 v121,v25,v25
M 26,a[168:171],54
L 3
v_fma_f32 v118,v100,v118,v99
v_fma_f32 v119,v100,v119,v99
M 26,a[176:179],58
R 78,2,27648
v_fma_f32 v120,v100,v120,v99
M 26,a[184:187],62
v_fma_f32 v121,v100,v121,v99
v_mul_f32 v118,v22,v118
M 30,a[180:183],58
R 82,2,28672
v_exp_f32 v118,v118
M 30,a[188:191],62
L 3
D 0,56,3072
M 30,a[196:199],66
R 86,2,29696
v_mul_f32 v119,v23,v119
M 30,a[204:207],70
v_exp_f32 v119,v119
M 26,a[192:195],66
R 90,2,30720
v_mul_f32 v120,v24,v120
M 26,a[200:203],70
L 3
v_mul_f32 v121,v25,v121
v_fma_f32 v118,v118,v101,v101
M 26,a[208:211],74
s_waitcnt vmcnt(5)
R 94,2,31744
v_fma_f32 v119,v119,v101,v101
M 26,a[216:219],78
s_barrier
ds_read_b32 v7,v6 offset:1024
v_exp_f32 v120,v120
M 30,a[212:215],74
R 34,1,0
v_fma_f32 v120,v120,v101,v101
M 30,a[220:223],78
L 4
buffer_store_dword v4,v3,s[36:39],s70 offen offset:0 sc0 sc1
v_exp_f32 v121,v121
M 30,a[228:231],82
s_mov_b32 s70,s69
R 38,1,1024
v_fma_f32 v121,v121,v101,v101
v_rcp_f32 v118,v118
v_rcp_f32 v119,v119
v_rcp_f32 v120,v120
v_rcp_f32 v121,v121
M 30,a[236:239],86
s_mov_b32 s69,s68
v_mul_f32 v22,v22,v118
v_mul_f32 v23,v23,v119
v_mul_f32 v24,v24,v120
v_mul_f32 v25,v25,v121
v_cvt_pk_bf16_f32 v124,v22,v23
v_cvt_pk_bf16_f32 v125,v24,v25
M 26,a[224:227],82
s_add_u32 s68,s68,s63
R 42,1,2048
buffer_store_dwordx4 v[122:125],v15,s[36:39],s66 offen offset:0 sc0 sc1
M 26,a[232:235],86
L 4
L 3
M 26,a[240:243],90
R 46,1,3072
v_cmp_eq_u32 vcc,s53,v7
M 26,a[248:251],94
buffer_store_dwordx4 v[122:125],v16,s[36:39],s66 offen offset:0 sc0 sc1
M 30,a[244:247],90
s_waitcnt vmcnt(7) lgkmcnt(2)
s_cmp_eq_u32 vcc_lo,-1
R 50,1,4096
M 30,a[252:255],94
s_cbranch_scc1 Lg1_F0_%=
Lg1_K0_%=:
Lg1_L0_%=:
v_mfma_f32_16x16x32_bf16 v[22:25],a[4:7],v[34:37],0
s_add_u32 m0,s59,3072
s_mov_b32 s66,s65
R 54,1,5120
M 22,a[12:15],38
buffer_load_dword v8,s[36:39],s58 offen offset:0 sc1 lds
v_mfma_f32_16x16x32_bf16 v[18:21],a[0:3],v[34:37],0
s_add_u32 s65,s65,s62
R 58,1,6144
v_fma_f32 v26,-v102,v106,v26
M 18,a[8:11],38
L 3
v_fma_f32 v27,-v102,v107,v27
v_fma_f32 v28,-v102,v108,v28
M 18,a[16:19],42
R 62,1,7168
v_fma_f32 v29,-v102,v109,v29
M 18,a[24:27],46
v_mul_f32 v114,v26,v26
v_mul_f32 v115,v27,v27
M 22,a[20:23],42
R 66,1,8192
v_mul_f32 v116,v28,v28
M 22,a[28:31],46
L 3
v_mul_f32 v117,v29,v29
v_fma_f32 v114,v104,v114,v103
M 22,a[36:39],50
R 70,1,9216
v_fma_f32 v115,v104,v115,v103
M 22,a[44:47],54
v_fma_f32 v116,v104,v116,v103
v_fma_f32 v117,v104,v117,v103
M 18,a[32:35],50
R 74,1,10240
v_mul_f32 v114,v26,v114
M 18,a[40:43],54
L 3
v_exp_f32 v114,v114
M 18,a[48:51],58
R 78,1,11264
v_mul_f32 v115,v27,v115
M 18,a[56:59],62
v_exp_f32 v115,v115
M 22,a[52:55],58
R 82,1,12288
v_mul_f32 v116,v28,v116
M 22,a[60:63],62
L 3
v_exp_f32 v116,v116
M 22,a[68:71],66
R 86,1,13312
v_mul_f32 v117,v29,v117
M 22,a[76:79],70
buffer_load_dwordx4 v[98:101],v11,s[36:39],s73 offen offset:0 sc1
M 18,a[64:67],66
s_add_u32 s73,s73,s64
R 90,1,14336
v_fma_f32 v114,v114,v105,v105
M 18,a[72:75],70
L 3
v_exp_f32 v117,v117
M 18,a[80:83],74
s_waitcnt vmcnt(1)
R 94,1,15360
v_fma_f32 v115,v115,v105,v105
M 18,a[88:91],78
s_barrier
ds_read_b32 v7,v6 offset:3072
v_fma_f32 v116,v116,v105,v105
M 22,a[84:87],74
R 34,1,16384
v_fma_f32 v117,v117,v105,v105
M 22,a[92:95],78
L 4
buffer_store_dword v4,v3,s[36:39],s70 offen offset:0 sc0 sc1
M 22,a[100:103],82
s_mov_b32 s70,0x7fff0000
R 38,1,17408
v_fma_f32 v30,-v102,v110,v30
M 22,a[108:111],86
v_rcp_f32 v114,v114
M 18,a[96:99],82
R 42,1,18432
v_mul_f32 v26,v26,v114
M 18,a[104:107],86
L 4
L 3
v_cmp_eq_u32 vcc,s53,v7
M 18,a[112:115],90
R 46,1,19456
v_fma_f32 v31,-v102,v111,v31
M 18,a[120:123],94
v_rcp_f32 v115,v115
M 22,a[116:119],90
s_cmp_eq_u32 vcc_lo,-1
R 50,1,20480
v_mul_f32 v27,v27,v115
s_cbranch_scc0 Lg1_X0_%=
M 22,a[124:127],94
L 3
s_add_u32 m0,s57,0
s_add_u32 s56,s55,0
M 22,a[132:135],34
s_add_u32 s58,s58,s61
R 54,1,21504
v_cvt_pk_bf16_f32 v122,v26,v27
M 22,a[140:143],38
D 0,56,0
M 18,a[128:131],34
s_add_u32 s54,s54,s46
R 58,1,22528
v_fma_f32 v32,-v102,v112,v32
M 18,a[136:139],38
L 3
v_rcp_f32 v116,v116
M 18,a[144:147],42
R 62,1,23552
v_mul_f32 v28,v28,v116
M 18,a[152:155],46
D 0,56,1024
M 22,a[148:151],42
R 66,1,24576
v_fma_f32 v33,-v102,v113,v33
M 22,a[156:159],46
L 3
v_rcp_f32 v117,v117
M 22,a[164:167],50
R 70,1,25600
v_mul_f32 v29,v29,v117
M 22,a[172:175],54
D 0,56,2048
M 18,a[160:163],50
R 74,1,26624
v_cvt_pk_bf16_f32 v123,v28,v29
M 18,a[168:171],54
L 3
v_mul_f32 v118,v30,v30
v_mul_f32 v119,v31,v31
M 18,a[176:179],58
R 78,1,27648
v_mul_f32 v120,v32,v32
M 18,a[184:187],62
D 0,56,3072
M 22,a[180:183],58
s_add_u32 m0,s59,0
R 82,1,28672
v_mul_f32 v121,v33,v33
M 22,a[188:191],62
L 3
buffer_load_dword v8,s[36:39],s58 offen offset:0 sc1 lds
M 22,a[196:199],66
s_add_u32 m0,s57,16384
R 86,1,29696
v_fma_f32 v118,v104,v118,v103
M 22,a[204:207],70
s_add_u32 s56,s55,16384
v_fma_f32 v119,v104,v119,v103
v_fma_f32 v120,v104,v120,v103
M 18,a[192:195],66
s_add_u32 s55,s55,s60
R 90,1,30720
v_fma_f32 v121,v104,v121,v103
M 18,a[200:203],70
L 3
D 0,56,0
M 18,a[208:211],74
s_waitcnt vmcnt(1)
R 94,1,31744
v_mul_f32 v118,v30,v118
M 18,a[216:219],78
s_barrier
ds_read_b32 v7,v6 offset:0
v_mul_f32 v119,v31,v119
M 22,a[212:215],74
s_sub_u32 s57,s84,s57
R 34,2,0
v_mul_f32 v120,v32,v120
M 22,a[220:223],78
L 4
buffer_store_dword v4,v3,s[36:39],s70 offen offset:0 sc0 sc1
v_mul_f32 v121,v33,v121
v_exp_f32 v118,v118
v_exp_f32 v119,v119
v_exp_f32 v120,v120
v_exp_f32 v121,v121
M 22,a[228:231],82
s_mov_b32 s70,s69
R 38,2,1024
v_fma_f32 v118,v118,v105,v105
v_fma_f32 v119,v119,v105,v105
v_fma_f32 v120,v120,v105,v105
v_fma_f32 v121,v121,v105,v105
v_rcp_f32 v118,v118
v_rcp_f32 v119,v119
v_rcp_f32 v120,v120
v_rcp_f32 v121,v121
M 22,a[236:239],86
s_mov_b32 s69,s68
v_mul_f32 v30,v30,v118
v_mul_f32 v31,v31,v119
v_mul_f32 v32,v32,v120
v_mul_f32 v33,v33,v121
v_cvt_pk_bf16_f32 v124,v30,v31
v_cvt_pk_bf16_f32 v125,v32,v33
M 18,a[224:227],82
s_add_u32 s68,s68,s63
R 42,2,2048
buffer_store_dwordx4 v[122:125],v15,s[36:39],s67 offen offset:0 sc0 sc1
M 18,a[232:235],86
L 4
L 3
M 18,a[240:243],90
R 46,2,3072
v_cmp_eq_u32 vcc,s53,v7
M 18,a[248:251],94
D 0,56,1024
M 22,a[244:247],90
L 2
s_cmp_eq_u32 vcc_lo,-1
R 50,2,4096
buffer_store_dwordx4 v[122:125],v16,s[36:39],s67 offen offset:0 sc0 sc1
M 22,a[252:255],94
D 0,56,2048
D 0,56,3072
buffer_store_dword v4,v3,s[36:39],s71 offen offset:0 sc0 sc1
buffer_store_dword v4,v3,s[36:39],s71 offen offset:0 sc0 sc1
buffer_store_dword v4,v3,s[36:39],s71 offen offset:0 sc0 sc1
s_cbranch_scc1 Lg1_F1_%=
s_branch Lg1_K1_%=
Lg1_X0_%=:
M 22,a[124:127],94
L 3
s_add_u32 m0,s59,0
v_cvt_pk_bf16_f32 v122,v26,v27
M 22,a[132:135],34
R 54,1,21504
v_fma_f32 v32,-v102,v112,v32
M 22,a[140:143],38
buffer_load_dword v8,s[36:39],s58 offen offset:0 sc1 lds
M 18,a[128:131],34
R 58,1,22528
v_fma_f32 v33,-v102,v113,v33
M 18,a[136:139],38
L 3
v_rcp_f32 v116,v116
M 18,a[144:147],42
R 62,1,23552
v_mul_f32 v28,v28,v116
M 18,a[152:155],46
v_rcp_f32 v117,v117
M 22,a[148:151],42
R 66,1,24576
v_mul_f32 v29,v29,v117
M 22,a[156:159],46
L 3
v_cvt_pk_bf16_f32 v123,v28,v29
v_mul_f32 v118,v30,v30
M 22,a[164:167],50
R 70,1,25600
v_mul_f32 v119,v31,v31
M 22,a[172:175],54
v_mul_f32 v120,v32,v32
v_mul_f32 v121,v33,v33
M 18,a[160:163],50
R 74,1,26624
v_fma_f32 v118,v104,v118,v103
M 18,a[168:171],54
L 3
v_fma_f32 v119,v104,v119,v103
v_fma_f32 v120,v104,v120,v103
M 18,a[176:179],58
R 78,1,27648
v_fma_f32 v121,v104,v121,v103
M 18,a[184:187],62
v_mul_f32 v118,v30,v118
v_mul_f32 v119,v31,v119
M 22,a[180:183],58
R 82,1,28672
v_mul_f32 v120,v32,v120
M 22,a[188:191],62
L 3
v_exp_f32 v118,v118
M 22,a[196:199],66
R 86,1,29696
v_mul_f32 v121,v33,v121
M 22,a[204:207],70
v_exp_f32 v119,v119
M 18,a[192:195],66
R 90,1,30720
v_fma_f32 v118,v118,v105,v105
M 18,a[200:203],70
L 3
v_exp_f32 v120,v120
M 18,a[208:211],74
s_waitcnt vmcnt(0)
R 94,1,31744
v_fma_f32 v119,v119,v105,v105
M 18,a[216:219],78
s_barrier
buffer_store_dword v4,v3,s[36:39],s70 offen offset:0 sc0 sc1
M 22,a[212:215],74
s_mov_b32 s70,s69
ds_read_b32 v7,v6 offset:0
v_fma_f32 v120,v120,v105,v105
M 22,a[220:223],78
L 3
v_exp_f32 v121,v121
M 22,a[228:231],82
s_mov_b32 s69,s68
v_fma_f32 v121,v121,v105,v105
v_rcp_f32 v118,v118
v_rcp_f32 v119,v119
v_rcp_f32 v120,v120
v_rcp_f32 v121,v121
M 22,a[236:239],86
s_add_u32 s68,s68,s63
v_mul_f32 v30,v30,v118
v_mul_f32 v31,v31,v119
v_mul_f32 v32,v32,v120
v_mul_f32 v33,v33,v121
v_cvt_pk_bf16_f32 v124,v30,v31
v_cvt_pk_bf16_f32 v125,v32,v33
M 18,a[224:227],82
buffer_store_dwordx4 v[122:125],v15,s[36:39],s67 offen offset:0 sc0 sc1
M 18,a[232:235],86
L 1
M 18,a[240:243],90
L 0
v_cmp_eq_u32 vcc,s53,v7
M 18,a[248:251],94
buffer_store_dwordx4 v[122:125],v16,s[36:39],s67 offen offset:0 sc0 sc1
M 22,a[244:247],90
s_nop 0
M 22,a[252:255],94
s_mov_b32 s80,1
s_cmp_eq_u32 vcc_lo,-1
s_cbranch_scc0 Lg1_xn0_%=
s_add_u32 m0,s57,0
s_add_u32 s56,s55,0
D 0,56,0
v_fma_f32 v18,-v98,v106,v18
v_fma_f32 v19,-v98,v107,v19
v_fma_f32 v20,-v98,v108,v20
v_fma_f32 v21,-v98,v109,v21
v_mul_f32 v114,v18,v18
v_mul_f32 v115,v19,v19
v_mul_f32 v116,v20,v20
D 0,56,1024
v_mul_f32 v117,v21,v21
v_fma_f32 v114,v100,v114,v99
v_fma_f32 v115,v100,v115,v99
v_fma_f32 v116,v100,v116,v99
v_fma_f32 v117,v100,v117,v99
v_mul_f32 v114,v18,v114
v_mul_f32 v115,v19,v115
D 0,56,2048
v_mul_f32 v116,v20,v116
v_mul_f32 v117,v21,v117
v_exp_f32 v114,v114
v_exp_f32 v115,v115
v_exp_f32 v116,v116
v_exp_f32 v117,v117
v_fma_f32 v114,v114,v101,v101
D 0,56,3072
v_fma_f32 v115,v115,v101,v101
v_fma_f32 v116,v116,v101,v101
v_fma_f32 v117,v117,v101,v101
v_rcp_f32 v114,v114
v_rcp_f32 v115,v115
v_rcp_f32 v116,v116
v_rcp_f32 v117,v117
s_add_u32 s58,s58,s61
)"
R"(s_add_u32 m0,s57,16384
s_add_u32 s56,s55,16384
D 0,56,0
v_mul_f32 v18,v18,v114
v_mul_f32 v19,v19,v115
v_mul_f32 v20,v20,v116
v_mul_f32 v21,v21,v117
v_cvt_pk_bf16_f32 v122,v18,v19
v_cvt_pk_bf16_f32 v123,v20,v21
v_fma_f32 v22,-v98,v110,v22
D 0,56,1024
v_fma_f32 v23,-v98,v111,v23
v_fma_f32 v24,-v98,v112,v24
v_fma_f32 v25,-v98,v113,v25
v_mul_f32 v118,v22,v22
v_mul_f32 v119,v23,v23
v_mul_f32 v120,v24,v24
v_mul_f32 v121,v25,v25
D 0,56,2048
v_fma_f32 v118,v100,v118,v99
v_fma_f32 v119,v100,v119,v99
v_fma_f32 v120,v100,v120,v99
v_fma_f32 v121,v100,v121,v99
v_mul_f32 v118,v22,v118
v_mul_f32 v119,v23,v119
v_mul_f32 v120,v24,v120
D 0,56,3072
v_mul_f32 v121,v25,v121
v_exp_f32 v118,v118
v_exp_f32 v119,v119
v_exp_f32 v120,v120
v_exp_f32 v121,v121
v_fma_f32 v118,v118,v101,v101
v_fma_f32 v119,v119,v101,v101
s_add_u32 s55,s55,s60
s_sub_u32 s57,s84,s57
s_add_u32 s54,s54,s46
v_fma_f32 v120,v120,v101,v101
v_fma_f32 v121,v121,v101,v101
v_rcp_f32 v118,v118
v_rcp_f32 v119,v119
v_rcp_f32 v120,v120
v_rcp_f32 v121,v121
v_mul_f32 v22,v22,v118
v_mul_f32 v23,v23,v119
v_mul_f32 v24,v24,v120
v_mul_f32 v25,v25,v121
v_cvt_pk_bf16_f32 v124,v22,v23
v_cvt_pk_bf16_f32 v125,v24,v25
buffer_store_dwordx4 v[122:125],v15,s[36:39],s66 offen offset:0 sc0 sc1
buffer_store_dwordx4 v[122:125],v16,s[36:39],s66 offen offset:0 sc0 sc1
s_mov_b32 s72,s69
s_mov_b32 s69,0x7fff0000
s_mov_b32 s66,0x7fff0000
s_mov_b32 s76,0x7fff0000
s_branch Lg1_gox_%=
Lg1_xn0_%=:
s_cmp_ge_u32 s54,256
s_cbranch_scc1 Lg1_fin0_%=
s_add_u32 m0,s59,4096
s_nop 0
buffer_load_dword v8,s[36:39],s58 offen offset:0 sc1 lds
buffer_store_dword v4,v3,s[36:39],s71 offen offset:0 sc0 sc1
s_add_u32 m0,s59,5120
s_nop 0
buffer_load_dword v8,s[36:39],s58 offen offset:0 sc1 lds
buffer_store_dword v4,v3,s[36:39],s71 offen offset:0 sc0 sc1
s_mov_b32 s83,0
v_fma_f32 v18,-v98,v106,v18
v_fma_f32 v19,-v98,v107,v19
v_fma_f32 v20,-v98,v108,v20
v_fma_f32 v21,-v98,v109,v21
v_mul_f32 v114,v18,v18
v_mul_f32 v115,v19,v19
v_mul_f32 v116,v20,v20
v_mul_f32 v117,v21,v21
v_fma_f32 v114,v100,v114,v99
v_fma_f32 v115,v100,v115,v99
v_fma_f32 v116,v100,v116,v99
v_fma_f32 v117,v100,v117,v99
v_mul_f32 v114,v18,v114
v_mul_f32 v115,v19,v115
v_mul_f32 v116,v20,v116
v_mul_f32 v117,v21,v117
v_exp_f32 v114,v114
v_exp_f32 v115,v115
v_exp_f32 v116,v116
v_exp_f32 v117,v117
v_fma_f32 v114,v114,v101,v101
v_fma_f32 v115,v115,v101,v101
v_fma_f32 v116,v116,v101,v101
v_fma_f32 v117,v117,v101,v101
v_rcp_f32 v114,v114
v_rcp_f32 v115,v115
v_rcp_f32 v116,v116
v_rcp_f32 v117,v117
v_mul_f32 v18,v18,v114
v_mul_f32 v19,v19,v115
v_mul_f32 v20,v20,v116
v_mul_f32 v21,v21,v117
v_cvt_pk_bf16_f32 v122,v18,v19
v_cvt_pk_bf16_f32 v123,v20,v21
v_fma_f32 v22,-v98,v110,v22
v_fma_f32 v23,-v98,v111,v23
v_fma_f32 v24,-v98,v112,v24
v_fma_f32 v25,-v98,v113,v25
v_mul_f32 v118,v22,v22
v_mul_f32 v119,v23,v23
v_mul_f32 v120,v24,v24
v_mul_f32 v121,v25,v25
v_fma_f32 v118,v100,v118,v99
v_fma_f32 v119,v100,v119,v99
v_fma_f32 v120,v100,v120,v99
v_fma_f32 v121,v100,v121,v99
v_mul_f32 v118,v22,v118
v_mul_f32 v119,v23,v119
v_mul_f32 v120,v24,v120
v_mul_f32 v121,v25,v121
v_exp_f32 v118,v118
v_exp_f32 v119,v119
v_exp_f32 v120,v120
v_exp_f32 v121,v121
v_fma_f32 v118,v118,v101,v101
v_fma_f32 v119,v119,v101,v101
v_fma_f32 v120,v120,v101,v101
v_fma_f32 v121,v121,v101,v101
v_rcp_f32 v118,v118
v_rcp_f32 v119,v119
v_rcp_f32 v120,v120
v_rcp_f32 v121,v121
v_mul_f32 v22,v22,v118
v_mul_f32 v23,v23,v119
v_mul_f32 v24,v24,v120
v_mul_f32 v25,v25,v121
v_cvt_pk_bf16_f32 v124,v22,v23
v_cvt_pk_bf16_f32 v125,v24,v25
buffer_store_dwordx4 v[122:125],v15,s[36:39],s66 offen offset:0 sc0 sc1
buffer_store_dwordx4 v[122:125],v16,s[36:39],s66 offen offset:0 sc0 sc1
s_mov_b32 s72,s69
s_mov_b32 s69,0x7fff0000
s_mov_b32 s66,0x7fff0000
s_mov_b32 s76,0x7fff0000
s_branch Lg1_poll_%=
Lg1_fin0_%=:
v_fma_f32 v18,-v98,v106,v18
v_fma_f32 v19,-v98,v107,v19
v_fma_f32 v20,-v98,v108,v20
v_fma_f32 v21,-v98,v109,v21
v_mul_f32 v114,v18,v18
v_mul_f32 v115,v19,v19
v_mul_f32 v116,v20,v20
v_mul_f32 v117,v21,v21
v_fma_f32 v114,v100,v114,v99
v_fma_f32 v115,v100,v115,v99
v_fma_f32 v116,v100,v116,v99
v_fma_f32 v117,v100,v117,v99
v_mul_f32 v114,v18,v114
v_mul_f32 v115,v19,v115
v_mul_f32 v116,v20,v116
v_mul_f32 v117,v21,v117
v_exp_f32 v114,v114
v_exp_f32 v115,v115
v_exp_f32 v116,v116
v_exp_f32 v117,v117
v_fma_f32 v114,v114,v101,v101
v_fma_f32 v115,v115,v101,v101
v_fma_f32 v116,v116,v101,v101
v_fma_f32 v117,v117,v101,v101
v_rcp_f32 v114,v114
v_rcp_f32 v115,v115
v_rcp_f32 v116,v116
v_rcp_f32 v117,v117
v_mul_f32 v18,v18,v114
v_mul_f32 v19,v19,v115
v_mul_f32 v20,v20,v116
v_mul_f32 v21,v21,v117
v_cvt_pk_bf16_f32 v122,v18,v19
v_cvt_pk_bf16_f32 v123,v20,v21
v_fma_f32 v22,-v98,v110,v22
v_fma_f32 v23,-v98,v111,v23
v_fma_f32 v24,-v98,v112,v24
v_fma_f32 v25,-v98,v113,v25
v_mul_f32 v118,v22,v22
v_mul_f32 v119,v23,v23
v_mul_f32 v120,v24,v24
v_mul_f32 v121,v25,v25
v_fma_f32 v118,v100,v118,v99
v_fma_f32 v119,v100,v119,v99
v_fma_f32 v120,v100,v120,v99
v_fma_f32 v121,v100,v121,v99
v_mul_f32 v118,v22,v118
v_mul_f32 v119,v23,v119
v_mul_f32 v120,v24,v120
v_mul_f32 v121,v25,v121
v_exp_f32 v118,v118
v_exp_f32 v119,v119
v_exp_f32 v120,v120
v_exp_f32 v121,v121
v_fma_f32 v118,v118,v101,v101
v_fma_f32 v119,v119,v101,v101
v_fma_f32 v120,v120,v101,v101
v_fma_f32 v121,v121,v101,v101
v_rcp_f32 v118,v118
v_rcp_f32 v119,v119
v_rcp_f32 v120,v120
v_rcp_f32 v121,v121
v_mul_f32 v22,v22,v118
v_mul_f32 v23,v23,v119
v_mul_f32 v24,v24,v120
v_mul_f32 v25,v25,v121
v_cvt_pk_bf16_f32 v124,v22,v23
v_cvt_pk_bf16_f32 v125,v24,v25
buffer_store_dwordx4 v[122:125],v15,s[36:39],s66 offen offset:0 sc0 sc1
buffer_store_dwordx4 v[122:125],v16,s[36:39],s66 offen offset:0 sc0 sc1
s_mov_b32 s72,s69
s_mov_b32 s69,0x7fff0000
s_mov_b32 s66,0x7fff0000
s_mov_b32 s76,0x7fff0000
s_branch Lg1_done_%=
Lg1_K1_%=:
Lg1_L1_%=:
v_mfma_f32_16x16x32_bf16 v[30:33],a[4:7],v[34:37],0
s_add_u32 m0,s59,3072
s_mov_b32 s67,s65
R 54,2,5120
M 30,a[12:15],38
buffer_load_dword v8,s[36:39],s58 offen offset:0 sc1 lds
v_mfma_f32_16x16x32_bf16 v[26:29],a[0:3],v[34:37],0
s_add_u32 s65,s65,s62
R 58,2,6144
v_fma_f32 v18,-v98,v106,v18
M 26,a[8:11],38
L 3
v_fma_f32 v19,-v98,v107,v19
v_fma_f32 v20,-v98,v108,v20
M 26,a[16:19],42
R 62,2,7168
v_fma_f32 v21,-v98,v109,v21
M 26,a[24:27],46
v_mul_f32 v114,v18,v18
v_mul_f32 v115,v19,v19
M 30,a[20:23],42
R 66,2,8192
v_mul_f32 v116,v20,v20
M 30,a[28:31],46
L 3
v_mul_f32 v117,v21,v21
v_fma_f32 v114,v100,v114,v99
M 30,a[36:39],50
R 70,2,9216
v_fma_f32 v115,v100,v115,v99
M 30,a[44:47],54
v_fma_f32 v116,v100,v116,v99
v_fma_f32 v117,v100,v117,v99
M 26,a[32:35],50
R 74,2,10240
v_mul_f32 v114,v18,v114
M 26,a[40:43],54
L 3
v_exp_f32 v114,v114
M 26,a[48:51],58
R 78,2,11264
v_mul_f32 v115,v19,v115
M 26,a[56:59],62
v_exp_f32 v115,v115
M 30,a[52:55],58
R 82,2,12288
v_mul_f32 v116,v20,v116
M 30,a[60:63],62
L 3
v_exp_f32 v116,v116
M 30,a[68:71],66
R 86,2,13312
v_mul_f32 v117,v21,v117
M 30,a[76:79],70
buffer_load_dwordx4 v[102:105],v11,s[36:39],s73 offen offset:0 sc1
M 26,a[64:67],66
s_add_u32 s73,s73,s64
R 90,2,14336
v_fma_f32 v114,v114,v101,v101
M 26,a[72:75],70
L 3
v_exp_f32 v117,v117
M 26,a[80:83],74
s_waitcnt vmcnt(1)
R 94,2,15360
v_fma_f32 v115,v115,v101,v101
M 26,a[88:91],78
s_barrier
ds_read_b32 v7,v6 offset:3072
v_fma_f32 v116,v116,v101,v101
M 30,a[84:87],74
R 34,2,16384
v_fma_f32 v117,v117,v101,v101
M 30,a[92:95],78
L 4
buffer_store_dword v4,v3,s[36:39],s70 offen offset:0 sc0 sc1
M 30,a[100:103],82
s_mov_b32 s70,0x7fff0000
R 38,2,17408
v_fma_f32 v22,-v98,v110,v22
M 30,a[108:111],86
v_rcp_f32 v114,v114
M 26,a[96:99],82
R 42,2,18432
v_mul_f32 v18,v18,v114
M 26,a[104:107],86
L 4
L 3
v_cmp_eq_u32 vcc,s53,v7
M 26,a[112:115],90
R 46,2,19456
v_fma_f32 v23,-v98,v111,v23
M 26,a[120:123],94
v_rcp_f32 v115,v115
M 30,a[116:119],90
s_cmp_eq_u32 vcc_lo,-1
R 50,2,20480
v_mul_f32 v19,v19,v115
s_cbranch_scc0 Lg1_X1_%=
M 30,a[124:127],94
L 3
s_add_u32 m0,s57,0
s_add_u32 s56,s55,0
M 30,a[132:135],34
s_add_u32 s58,s58,s61
R 54,2,21504
v_cvt_pk_bf16_f32 v122,v18,v19
M 30,a[140:143],38
D 0,56,0
M 26,a[128:131],34
s_add_u32 s54,s54,s46
R 58,2,22528
v_fma_f32 v24,-v98,v112,v24
M 26,a[136:139],38
L 3
v_rcp_f32 v116,v116
M 26,a[144:147],42
R 62,2,23552
v_mul_f32 v20,v20,v116
M 26,a[152:155],46
D 0,56,1024
M 30,a[148:151],42
R 66,2,24576
v_fma_f32 v25,-v98,v113,v25
M 30,a[156:159],46
L 3
v_rcp_f32 v117,v117
M 30,a[164:167],50
R 70,2,25600
v_mul_f32 v21,v21,v117
M 30,a[172:175],54
D 0,56,2048
M 26,a[160:163],50
R 74,2,26624
v_cvt_pk_bf16_f32 v123,v20,v21
M 26,a[168:171],54
L 3
v_mul_f32 v118,v22,v22
v_mul_f32 v119,v23,v23
M 26,a[176:179],58
R 78,2,27648
v_mul_f32 v120,v24,v24
M 26,a[184:187],62
D 0,56,3072
M 30,a[180:183],58
s_add_u32 m0,s59,1024
R 82,2,28672
v_mul_f32 v121,v25,v25
M 30,a[188:191],62
L 3
buffer_load_dword v8,s[36:39],s58 offen offset:0 sc1 lds
M 30,a[196:199],66
s_add_u32 m0,s57,16384
R 86,2,29696
v_fma_f32 v118,v100,v118,v99
M 30,a[204:207],70
s_add_u32 s56,s55,16384
v_fma_f32 v119,v100,v119,v99
v_fma_f32 v120,v100,v120,v99
M 26,a[192:195],66
s_add_u32 s55,s55,s60
R 90,2,30720
v_fma_f32 v121,v100,v121,v99
M 26,a[200:203],70
L 3
D 0,56,0
M 26,a[208:211],74
s_waitcnt vmcnt(1)
R 94,2,31744
v_mul_f32 v118,v22,v118
M 26,a[216:219],78
s_barrier
ds_read_b32 v7,v6 offset:1024
v_mul_f32 v119,v23,v119
M 30,a[212:215],74
s_sub_u32 s57,s84,s57
R 34,1,0
v_mul_f32 v120,v24,v120
M 30,a[220:223],78
L 4
buffer_store_dword v4,v3,s[36:39],s70 offen offset:0 sc0 sc1
v_mul_f32 v121,v25,v121
v_exp_f32 v118,v118
v_exp_f32 v119,v119
v_exp_f32 v120,v120
v_exp_f32 v121,v121
M 30,a[228:231],82
s_mov_b32 s70,s69
R 38,1,1024
v_fma_f32 v118,v118,v101,v101
v_fma_f32 v119,v119,v101,v101
v_fma_f32 v120,v120,v101,v101
v_fma_f32 v121,v121,v101,v101
v_rcp_f32 v118,v118
v_rcp_f32 v119,v119
v_rcp_f32 v120,v120
v_rcp_f32 v121,v121
M 30,a[236:239],86
s_mov_b32 s69,s68
v_mul_f32 v22,v22,v118
v_mul_f32 v23,v23,v119
v_mul_f32 v24,v24,v120
v_mul_f32 v25,v25,v121
v_cvt_pk_bf16_f32 v124,v22,v23
v_cvt_pk_bf16_f32 v125,v24,v25
M 26,a[224:227],82
s_add_u32 s68,s68,s63
R 42,1,2048
buffer_store_dwordx4 v[122:125],v15,s[36:39],s66 offen offset:0 sc0 sc1
M 26,a[232:235],86
L 4
L 3
M 26,a[240:243],90
R 46,1,3072
v_cmp_eq_u32 vcc,s53,v7
M 26,a[248:251],94
D 0,56,1024
M 30,a[244:247],90
L 2
s_cmp_eq_u32 vcc_lo,-1
R 50,1,4096
buffer_store_dwordx4 v[122:125],v16,s[36:39],s66 offen offset:0 sc0 sc1
M 30,a[252:255],94
D 0,56,2048
D 0,56,3072
buffer_store_dword v4,v3,s[36:39],s71 offen offset:0 sc0 sc1
buffer_store_dword v4,v3,s[36:39],s71 offen offset:0 sc0 sc1
buffer_store_dword v4,v3,s[36:39],s71 offen offset:0 sc0 sc1
s_cbranch_scc1 Lg1_F0_%=
s_branch Lg1_K0_%=
Lg1_X1_%=:
M 30,a[124:127],94
L 3
s_add_u32 m0,s59,1024
v_cvt_pk_bf16_f32 v122,v18,v19
M 30,a[132:135],34
R 54,2,21504
v_fma_f32 v24,-v98,v112,v24
M 30,a[140:143],38
buffer_load_dword v8,s[36:39],s58 offen offset:0 sc1 lds
M 26,a[128:131],34
R 58,2,22528
v_fma_f32 v25,-v98,v113,v25
M 26,a[136:139],38
L 3
v_rcp_f32 v116,v116
M 26,a[144:147],42
R 62,2,23552
v_mul_f32 v20,v20,v116
M 26,a[152:155],46
v_rcp_f32 v117,v117
M 30,a[148:151],42
R 66,2,24576
v_mul_f32 v21,v21,v117
M 30,a[156:159],46
L 3
v_cvt_pk_bf16_f32 v123,v20,v21
v_mul_f32 v118,v22,v22
M 30,a[164:167],50
R 70,2,25600
v_mul_f32 v119,v23,v23
M 30,a[172:175],54
v_mul_f32 v120,v24,v24
v_mul_f32 v121,v25,v25
M 26,a[160:163],50
R 74,2,26624
v_fma_f32 v118,v100,v118,v99
M 26,a[168:171],54
L 3
v_fma_f32 v119,v100,v119,v99
v_fma_f32 v120,v100,v120,v99
M 26,a[176:179],58
R 78,2,27648
v_fma_f32 v121,v100,v121,v99
M 26,a[184:187],62
v_mul_f32 v118,v22,v118
v_mul_f32 v119,v23,v119
M 30,a[180:183],58
R 82,2,28672
)"
R"(v_mul_f32 v120,v24,v120
M 30,a[188:191],62
L 3
v_exp_f32 v118,v118
M 30,a[196:199],66
R 86,2,29696
v_mul_f32 v121,v25,v121
M 30,a[204:207],70
v_exp_f32 v119,v119
M 26,a[192:195],66
R 90,2,30720
v_fma_f32 v118,v118,v101,v101
M 26,a[200:203],70
L 3
v_exp_f32 v120,v120
M 26,a[208:211],74
s_waitcnt vmcnt(0)
R 94,2,31744
v_fma_f32 v119,v119,v101,v101
M 26,a[216:219],78
s_barrier
buffer_store_dword v4,v3,s[36:39],s70 offen offset:0 sc0 sc1
M 30,a[212:215],74
s_mov_b32 s70,s69
ds_read_b32 v7,v6 offset:1024
v_fma_f32 v120,v120,v101,v101
M 30,a[220:223],78
L 3
v_exp_f32 v121,v121
M 30,a[228:231],82
s_mov_b32 s69,s68
v_fma_f32 v121,v121,v101,v101
v_rcp_f32 v118,v118
v_rcp_f32 v119,v119
v_rcp_f32 v120,v120
v_rcp_f32 v121,v121
M 30,a[236:239],86
s_add_u32 s68,s68,s63
v_mul_f32 v22,v22,v118
v_mul_f32 v23,v23,v119
v_mul_f32 v24,v24,v120
v_mul_f32 v25,v25,v121
v_cvt_pk_bf16_f32 v124,v22,v23
v_cvt_pk_bf16_f32 v125,v24,v25
M 26,a[224:227],82
buffer_store_dwordx4 v[122:125],v15,s[36:39],s66 offen offset:0 sc0 sc1
M 26,a[232:235],86
L 1
M 26,a[240:243],90
L 0
v_cmp_eq_u32 vcc,s53,v7
M 26,a[248:251],94
buffer_store_dwordx4 v[122:125],v16,s[36:39],s66 offen offset:0 sc0 sc1
M 30,a[244:247],90
s_nop 0
M 30,a[252:255],94
s_mov_b32 s80,0
s_cmp_eq_u32 vcc_lo,-1
s_cbranch_scc0 Lg1_xn1_%=
s_add_u32 m0,s57,0
s_add_u32 s56,s55,0
D 0,56,0
v_fma_f32 v26,-v102,v106,v26
v_fma_f32 v27,-v102,v107,v27
v_fma_f32 v28,-v102,v108,v28
v_fma_f32 v29,-v102,v109,v29
v_mul_f32 v114,v26,v26
v_mul_f32 v115,v27,v27
v_mul_f32 v116,v28,v28
D 0,56,1024
v_mul_f32 v117,v29,v29
v_fma_f32 v114,v104,v114,v103
v_fma_f32 v115,v104,v115,v103
v_fma_f32 v116,v104,v116,v103
v_fma_f32 v117,v104,v117,v103
v_mul_f32 v114,v26,v114
v_mul_f32 v115,v27,v115
D 0,56,2048
v_mul_f32 v116,v28,v116
v_mul_f32 v117,v29,v117
v_exp_f32 v114,v114
v_exp_f32 v115,v115
v_exp_f32 v116,v116
v_exp_f32 v117,v117
v_fma_f32 v114,v114,v105,v105
D 0,56,3072
v_fma_f32 v115,v115,v105,v105
v_fma_f32 v116,v116,v105,v105
v_fma_f32 v117,v117,v105,v105
v_rcp_f32 v114,v114
v_rcp_f32 v115,v115
v_rcp_f32 v116,v116
v_rcp_f32 v117,v117
s_add_u32 s58,s58,s61
s_add_u32 m0,s57,16384
s_add_u32 s56,s55,16384
D 0,56,0
v_mul_f32 v26,v26,v114
v_mul_f32 v27,v27,v115
v_mul_f32 v28,v28,v116
v_mul_f32 v29,v29,v117
v_cvt_pk_bf16_f32 v122,v26,v27
v_cvt_pk_bf16_f32 v123,v28,v29
v_fma_f32 v30,-v102,v110,v30
D 0,56,1024
v_fma_f32 v31,-v102,v111,v31
v_fma_f32 v32,-v102,v112,v32
v_fma_f32 v33,-v102,v113,v33
v_mul_f32 v118,v30,v30
v_mul_f32 v119,v31,v31
v_mul_f32 v120,v32,v32
v_mul_f32 v121,v33,v33
D 0,56,2048
v_fma_f32 v118,v104,v118,v103
v_fma_f32 v119,v104,v119,v103
v_fma_f32 v120,v104,v120,v103
v_fma_f32 v121,v104,v121,v103
v_mul_f32 v118,v30,v118
v_mul_f32 v119,v31,v119
v_mul_f32 v120,v32,v120
D 0,56,3072
v_mul_f32 v121,v33,v121
v_exp_f32 v118,v118
v_exp_f32 v119,v119
v_exp_f32 v120,v120
v_exp_f32 v121,v121
v_fma_f32 v118,v118,v105,v105
v_fma_f32 v119,v119,v105,v105
s_add_u32 s55,s55,s60
s_sub_u32 s57,s84,s57
s_add_u32 s54,s54,s46
v_fma_f32 v120,v120,v105,v105
v_fma_f32 v121,v121,v105,v105
v_rcp_f32 v118,v118
v_rcp_f32 v119,v119
v_rcp_f32 v120,v120
v_rcp_f32 v121,v121
v_mul_f32 v30,v30,v118
v_mul_f32 v31,v31,v119
v_mul_f32 v32,v32,v120
v_mul_f32 v33,v33,v121
v_cvt_pk_bf16_f32 v124,v30,v31
v_cvt_pk_bf16_f32 v125,v32,v33
buffer_store_dwordx4 v[122:125],v15,s[36:39],s67 offen offset:0 sc0 sc1
buffer_store_dwordx4 v[122:125],v16,s[36:39],s67 offen offset:0 sc0 sc1
s_mov_b32 s72,s69
s_mov_b32 s69,0x7fff0000
s_mov_b32 s67,0x7fff0000
s_mov_b32 s77,0x7fff0000
s_branch Lg1_gox_%=
Lg1_xn1_%=:
s_cmp_ge_u32 s54,256
s_cbranch_scc1 Lg1_fin1_%=
s_add_u32 m0,s59,4096
s_nop 0
buffer_load_dword v8,s[36:39],s58 offen offset:0 sc1 lds
buffer_store_dword v4,v3,s[36:39],s71 offen offset:0 sc0 sc1
s_add_u32 m0,s59,5120
s_nop 0
buffer_load_dword v8,s[36:39],s58 offen offset:0 sc1 lds
buffer_store_dword v4,v3,s[36:39],s71 offen offset:0 sc0 sc1
s_mov_b32 s83,0
v_fma_f32 v26,-v102,v106,v26
v_fma_f32 v27,-v102,v107,v27
v_fma_f32 v28,-v102,v108,v28
v_fma_f32 v29,-v102,v109,v29
v_mul_f32 v114,v26,v26
v_mul_f32 v115,v27,v27
v_mul_f32 v116,v28,v28
v_mul_f32 v117,v29,v29
v_fma_f32 v114,v104,v114,v103
v_fma_f32 v115,v104,v115,v103
v_fma_f32 v116,v104,v116,v103
v_fma_f32 v117,v104,v117,v103
v_mul_f32 v114,v26,v114
v_mul_f32 v115,v27,v115
v_mul_f32 v116,v28,v116
v_mul_f32 v117,v29,v117
v_exp_f32 v114,v114
v_exp_f32 v115,v115
v_exp_f32 v116,v116
v_exp_f32 v117,v117
v_fma_f32 v114,v114,v105,v105
v_fma_f32 v115,v115,v105,v105
v_fma_f32 v116,v116,v105,v105
v_fma_f32 v117,v117,v105,v105
v_rcp_f32 v114,v114
v_rcp_f32 v115,v115
v_rcp_f32 v116,v116
v_rcp_f32 v117,v117
v_mul_f32 v26,v26,v114
v_mul_f32 v27,v27,v115
v_mul_f32 v28,v28,v116
v_mul_f32 v29,v29,v117
v_cvt_pk_bf16_f32 v122,v26,v27
v_cvt_pk_bf16_f32 v123,v28,v29
v_fma_f32 v30,-v102,v110,v30
v_fma_f32 v31,-v102,v111,v31
v_fma_f32 v32,-v102,v112,v32
v_fma_f32 v33,-v102,v113,v33
v_mul_f32 v118,v30,v30
v_mul_f32 v119,v31,v31
v_mul_f32 v120,v32,v32
v_mul_f32 v121,v33,v33
v_fma_f32 v118,v104,v118,v103
v_fma_f32 v119,v104,v119,v103
v_fma_f32 v120,v104,v120,v103
v_fma_f32 v121,v104,v121,v103
v_mul_f32 v118,v30,v118
v_mul_f32 v119,v31,v119
v_mul_f32 v120,v32,v120
v_mul_f32 v121,v33,v121
v_exp_f32 v118,v118
v_exp_f32 v119,v119
v_exp_f32 v120,v120
v_exp_f32 v121,v121
v_fma_f32 v118,v118,v105,v105
v_fma_f32 v119,v119,v105,v105
v_fma_f32 v120,v120,v105,v105
v_fma_f32 v121,v121,v105,v105
v_rcp_f32 v118,v118
v_rcp_f32 v119,v119
v_rcp_f32 v120,v120
v_rcp_f32 v121,v121
v_mul_f32 v30,v30,v118
v_mul_f32 v31,v31,v119
v_mul_f32 v32,v32,v120
v_mul_f32 v33,v33,v121
v_cvt_pk_bf16_f32 v124,v30,v31
v_cvt_pk_bf16_f32 v125,v32,v33
buffer_store_dwordx4 v[122:125],v15,s[36:39],s67 offen offset:0 sc0 sc1
buffer_store_dwordx4 v[122:125],v16,s[36:39],s67 offen offset:0 sc0 sc1
s_mov_b32 s72,s69
s_mov_b32 s69,0x7fff0000
s_mov_b32 s67,0x7fff0000
s_mov_b32 s77,0x7fff0000
s_branch Lg1_poll_%=
Lg1_fin1_%=:
v_fma_f32 v26,-v102,v106,v26
v_fma_f32 v27,-v102,v107,v27
v_fma_f32 v28,-v102,v108,v28
v_fma_f32 v29,-v102,v109,v29
v_mul_f32 v114,v26,v26
v_mul_f32 v115,v27,v27
v_mul_f32 v116,v28,v28
v_mul_f32 v117,v29,v29
v_fma_f32 v114,v104,v114,v103
v_fma_f32 v115,v104,v115,v103
v_fma_f32 v116,v104,v116,v103
v_fma_f32 v117,v104,v117,v103
v_mul_f32 v114,v26,v114
v_mul_f32 v115,v27,v115
v_mul_f32 v116,v28,v116
v_mul_f32 v117,v29,v117
v_exp_f32 v114,v114
v_exp_f32 v115,v115
v_exp_f32 v116,v116
v_exp_f32 v117,v117
v_fma_f32 v114,v114,v105,v105
v_fma_f32 v115,v115,v105,v105
v_fma_f32 v116,v116,v105,v105
v_fma_f32 v117,v117,v105,v105
v_rcp_f32 v114,v114
v_rcp_f32 v115,v115
v_rcp_f32 v116,v116
v_rcp_f32 v117,v117
v_mul_f32 v26,v26,v114
v_mul_f32 v27,v27,v115
v_mul_f32 v28,v28,v116
v_mul_f32 v29,v29,v117
v_cvt_pk_bf16_f32 v122,v26,v27
v_cvt_pk_bf16_f32 v123,v28,v29
v_fma_f32 v30,-v102,v110,v30
v_fma_f32 v31,-v102,v111,v31
v_fma_f32 v32,-v102,v112,v32
v_fma_f32 v33,-v102,v113,v33
v_mul_f32 v118,v30,v30
v_mul_f32 v119,v31,v31
v_mul_f32 v120,v32,v32
v_mul_f32 v121,v33,v33
v_fma_f32 v118,v104,v118,v103
v_fma_f32 v119,v104,v119,v103
v_fma_f32 v120,v104,v120,v103
v_fma_f32 v121,v104,v121,v103
v_mul_f32 v118,v30,v118
v_mul_f32 v119,v31,v119
v_mul_f32 v120,v32,v120
v_mul_f32 v121,v33,v121
v_exp_f32 v118,v118
v_exp_f32 v119,v119
v_exp_f32 v120,v120
v_exp_f32 v121,v121
v_fma_f32 v118,v118,v105,v105
v_fma_f32 v119,v119,v105,v105
v_fma_f32 v120,v120,v105,v105
v_fma_f32 v121,v121,v105,v105
v_rcp_f32 v118,v118
v_rcp_f32 v119,v119
v_rcp_f32 v120,v120
v_rcp_f32 v121,v121
v_mul_f32 v30,v30,v118
v_mul_f32 v31,v31,v119
v_mul_f32 v32,v32,v120
v_mul_f32 v33,v33,v121
v_cvt_pk_bf16_f32 v124,v30,v31
v_cvt_pk_bf16_f32 v125,v32,v33
buffer_store_dwordx4 v[122:125],v15,s[36:39],s67 offen offset:0 sc0 sc1
buffer_store_dwordx4 v[122:125],v16,s[36:39],s67 offen offset:0 sc0 sc1
s_mov_b32 s72,s69
s_mov_b32 s69,0x7fff0000
s_mov_b32 s67,0x7fff0000
s_mov_b32 s77,0x7fff0000
s_branch Lg1_done_%=
Lg1_done_%=:
s_waitcnt vmcnt(2)
s_barrier
buffer_store_dword v4,v3,s[36:39],s70 offen offset:0 sc0 sc1
s_waitcnt vmcnt(1)
s_barrier
buffer_store_dword v4,v3,s[36:39],s72 offen offset:0 sc0 sc1
s_waitcnt vmcnt(0) lgkmcnt(0)
s_barrier
s_branch Lg2_end_%=
Lg2_begin_%=:
Lcode_g2_b:
s_mov_b64 s[36:37],%1
s_mov_b32 s38,111243392
s_mov_b32 s39,0x00020000
s_mov_b32 s44,%2
s_mov_b32 s45,%3
s_mov_b32 s46,%4
s_mov_b32 s47,%5
s_mov_b32 s48,%6
s_mov_b32 s49,%7
s_mov_b32 s50,%8
s_mov_b32 s51,%9
s_mov_b32 s53,%10
v_mov_b32 v0,%11
v_mov_b32 v3,%12
v_mov_b32 v5,%13
v_mov_b32 v6,%14
v_mov_b32 v18,%15
v_mov_b32 v19,%16
v_mov_b32 v20,%17
v_mov_b32 v21,%18
v_mov_b32 v9,%19
v_mov_b32 v10,%20
v_mov_b32 v11,%21
v_mov_b32 v12,%22
v_mov_b32 v13,%23
v_mov_b32 v14,%24
v_mov_b32 v15,%25
v_mov_b32 v16,%26
s_mov_b64 s[40:41],%27
s_mov_b32 s42,2097152
s_mov_b32 s43,0x00020000
s_lshl_b32 s57,s44,11
s_cmp_ge_u32 s45,8
s_cbranch_scc1 Lg2_dly_%=
s_sleep 16
s_sleep 16
s_sleep 16
s_sleep 16
Lg2_dly_%=:
s_add_u32 s55,s50,0
s_add_u32 m0,s57,0
s_nop 0
buffer_load_dwordx4 v18,s[40:43],s55 offen offset:0  lds
s_add_u32 m0,s57,1024
s_nop 0
buffer_load_dwordx4 v19,s[40:43],s55 offen offset:0  lds
s_add_u32 m0,s57,8064
s_nop 0
buffer_load_dwordx4 v18,s[40:43],s55 offen offset:128  lds
s_add_u32 m0,s57,9088
s_nop 0
buffer_load_dwordx4 v19,s[40:43],s55 offen offset:128  lds
s_add_u32 s55,s50,131072
s_add_u32 m0,s57,16384
s_nop 0
buffer_load_dwordx4 v18,s[40:43],s55 offen offset:0  lds
s_add_u32 m0,s57,17408
s_nop 0
buffer_load_dwordx4 v19,s[40:43],s55 offen offset:0  lds
s_add_u32 m0,s57,24448
s_nop 0
buffer_load_dwordx4 v18,s[40:43],s55 offen offset:128  lds
s_add_u32 m0,s57,25472
s_nop 0
buffer_load_dwordx4 v19,s[40:43],s55 offen offset:128  lds
s_add_u32 s55,s50,262144
s_add_u32 m0,s57,32768
s_nop 0
buffer_load_dwordx4 v18,s[40:43],s55 offen offset:0  lds
s_add_u32 m0,s57,33792
s_nop 0
buffer_load_dwordx4 v19,s[40:43],s55 offen offset:0  lds
s_add_u32 m0,s57,40832
s_nop 0
buffer_load_dwordx4 v18,s[40:43],s55 offen offset:128  lds
s_add_u32 m0,s57,41856
s_nop 0
buffer_load_dwordx4 v19,s[40:43],s55 offen offset:128  lds
s_add_u32 s55,s50,393216
s_add_u32 m0,s57,49152
s_nop 0
buffer_load_dwordx4 v18,s[40:43],s55 offen offset:0  lds
s_add_u32 m0,s57,50176
s_nop 0
buffer_load_dwordx4 v19,s[40:43],s55 offen offset:0  lds
s_add_u32 m0,s57,57216
s_nop 0
buffer_load_dwordx4 v18,s[40:43],s55 offen offset:128  lds
s_add_u32 m0,s57,58240
s_nop 0
buffer_load_dwordx4 v19,s[40:43],s55 offen offset:128  lds
s_add_u32 s55,s50,524288
s_add_u32 m0,s57,65536
s_nop 0
buffer_load_dwordx4 v18,s[40:43],s55 offen offset:0  lds
s_add_u32 m0,s57,66560
s_nop 0
buffer_load_dwordx4 v19,s[40:43],s55 offen offset:0  lds
s_add_u32 m0,s57,73600
s_nop 0
buffer_load_dwordx4 v18,s[40:43],s55 offen offset:128  lds
s_add_u32 m0,s57,74624
s_nop 0
buffer_load_dwordx4 v19,s[40:43],s55 offen offset:128  lds
s_add_u32 s55,s50,655360
s_add_u32 m0,s57,81920
s_nop 0
buffer_load_dwordx4 v18,s[40:43],s55 offen offset:0  lds
s_add_u32 m0,s57,82944
s_nop 0
buffer_load_dwordx4 v19,s[40:43],s55 offen offset:0  lds
s_add_u32 m0,s57,89984
s_nop 0
buffer_load_dwordx4 v18,s[40:43],s55 offen offset:128  lds
s_add_u32 m0,s57,91008
s_nop 0
buffer_load_dwordx4 v19,s[40:43],s55 offen offset:128  lds
s_waitcnt vmcnt(20) lgkmcnt(0)
s_barrier
s_add_u32 s55,s50,786432
s_add_u32 m0,s57,98304
s_nop 0
buffer_load_dwordx4 v18,s[40:43],s55 offen offset:0  lds
s_add_u32 m0,s57,99328
s_nop 0
buffer_load_dwordx4 v19,s[40:43],s55 offen offset:0  lds
s_add_u32 m0,s57,106368
s_nop 0
buffer_load_dwordx4 v18,s[40:43],s55 offen offset:128  lds
s_add_u32 m0,s57,107392
s_nop 0
buffer_load_dwordx4 v19,s[40:43],s55 offen offset:128  lds
v_add_u32 v26,0,v20
v_add_u32 v27,0,v21
T a[0:1],26,0
T a[2:3],26,512
T a[4:5],27,0
T a[6:7],27,512
T a[8:9],26,4096
T a[10:11],26,4608
T a[12:13],27,4096
T a[14:15],27,4608
s_waitcnt vmcnt(20) lgkmcnt(0)
s_barrier
s_add_u32 s55,s50,917504
s_add_u32 m0,s57,0
s_nop 0
buffer_load_dwordx4 v18,s[40:43],s55 offen offset:0  lds
s_add_u32 m0,s57,1024
s_nop 0
buffer_load_dwordx4 v19,s[40:43],s55 offen offset:0  lds
s_add_u32 m0,s57,8064
s_nop 0
buffer_load_dwordx4 v18,s[40:43],s55 offen offset:128  lds
s_add_u32 m0,s57,9088
s_nop 0
buffer_load_dwordx4 v19,s[40:43],s55 offen offset:128  lds
v_add_u32 v26,16384,v20
v_add_u32 v27,16384,v21
T a[16:17],26,0
T a[18:19],26,512
T a[20:21],27,0
T a[22:23],27,512
T a[24:25],26,4096
T a[26:27],26,4608
T a[28:29],27,4096
T a[30:31],27,4608
s_waitcnt vmcnt(20) lgkmcnt(0)
s_barrier
s_add_u32 s55,s50,1048576
s_add_u32 m0,s57,16384
s_nop 0
buffer_load_dwordx4 v18,s[40:43],s55 offen offset:0  lds
s_add_u32 m0,s57,17408
s_nop 0
)"
R"(buffer_load_dwordx4 v19,s[40:43],s55 offen offset:0  lds
s_add_u32 m0,s57,24448
s_nop 0
buffer_load_dwordx4 v18,s[40:43],s55 offen offset:128  lds
s_add_u32 m0,s57,25472
s_nop 0
buffer_load_dwordx4 v19,s[40:43],s55 offen offset:128  lds
v_add_u32 v26,32768,v20
v_add_u32 v27,32768,v21
T a[32:33],26,0
T a[34:35],26,512
T a[36:37],27,0
T a[38:39],27,512
T a[40:41],26,4096
T a[42:43],26,4608
T a[44:45],27,4096
T a[46:47],27,4608
s_waitcnt vmcnt(20) lgkmcnt(0)
s_barrier
s_add_u32 s55,s50,1179648
s_add_u32 m0,s57,32768
s_nop 0
buffer_load_dwordx4 v18,s[40:43],s55 offen offset:0  lds
s_add_u32 m0,s57,33792
s_nop 0
buffer_load_dwordx4 v19,s[40:43],s55 offen offset:0  lds
s_add_u32 m0,s57,40832
s_nop 0
buffer_load_dwordx4 v18,s[40:43],s55 offen offset:128  lds
s_add_u32 m0,s57,41856
s_nop 0
buffer_load_dwordx4 v19,s[40:43],s55 offen offset:128  lds
v_add_u32 v26,49152,v20
v_add_u32 v27,49152,v21
T a[48:49],26,0
T a[50:51],26,512
T a[52:53],27,0
T a[54:55],27,512
T a[56:57],26,4096
T a[58:59],26,4608
T a[60:61],27,4096
T a[62:63],27,4608
s_waitcnt vmcnt(20) lgkmcnt(0)
s_barrier
s_add_u32 s55,s50,1310720
s_add_u32 m0,s57,49152
s_nop 0
buffer_load_dwordx4 v18,s[40:43],s55 offen offset:0  lds
s_add_u32 m0,s57,50176
s_nop 0
buffer_load_dwordx4 v19,s[40:43],s55 offen offset:0  lds
s_add_u32 m0,s57,57216
s_nop 0
buffer_load_dwordx4 v18,s[40:43],s55 offen offset:128  lds
s_add_u32 m0,s57,58240
s_nop 0
buffer_load_dwordx4 v19,s[40:43],s55 offen offset:128  lds
v_add_u32 v26,65536,v20
v_add_u32 v27,65536,v21
T a[64:65],26,0
T a[66:67],26,512
T a[68:69],27,0
T a[70:71],27,512
T a[72:73],26,4096
T a[74:75],26,4608
T a[76:77],27,4096
T a[78:79],27,4608
s_waitcnt vmcnt(20) lgkmcnt(0)
s_barrier
s_add_u32 s55,s50,1441792
s_add_u32 m0,s57,65536
s_nop 0
buffer_load_dwordx4 v18,s[40:43],s55 offen offset:0  lds
s_add_u32 m0,s57,66560
s_nop 0
buffer_load_dwordx4 v19,s[40:43],s55 offen offset:0  lds
s_add_u32 m0,s57,73600
s_nop 0
buffer_load_dwordx4 v18,s[40:43],s55 offen offset:128  lds
s_add_u32 m0,s57,74624
s_nop 0
buffer_load_dwordx4 v19,s[40:43],s55 offen offset:128  lds
v_add_u32 v26,81920,v20
v_add_u32 v27,81920,v21
T a[80:81],26,0
T a[82:83],26,512
T a[84:85],27,0
T a[86:87],27,512
T a[88:89],26,4096
T a[90:91],26,4608
T a[92:93],27,4096
T a[94:95],27,4608
s_waitcnt vmcnt(20) lgkmcnt(0)
s_barrier
s_add_u32 s55,s50,1572864
s_add_u32 m0,s57,81920
s_nop 0
buffer_load_dwordx4 v18,s[40:43],s55 offen offset:0  lds
s_add_u32 m0,s57,82944
s_nop 0
buffer_load_dwordx4 v19,s[40:43],s55 offen offset:0  lds
s_add_u32 m0,s57,89984
s_nop 0
buffer_load_dwordx4 v18,s[40:43],s55 offen offset:128  lds
s_add_u32 m0,s57,91008
s_nop 0
buffer_load_dwordx4 v19,s[40:43],s55 offen offset:128  lds
v_add_u32 v26,98304,v20
v_add_u32 v27,98304,v21
T a[96:97],26,0
T a[98:99],26,512
T a[100:101],27,0
T a[102:103],27,512
T a[104:105],26,4096
T a[106:107],26,4608
T a[108:109],27,4096
T a[110:111],27,4608
s_waitcnt vmcnt(20) lgkmcnt(0)
s_barrier
s_add_u32 s55,s50,1703936
s_add_u32 m0,s57,98304
s_nop 0
buffer_load_dwordx4 v18,s[40:43],s55 offen offset:0  lds
s_add_u32 m0,s57,99328
s_nop 0
buffer_load_dwordx4 v19,s[40:43],s55 offen offset:0  lds
s_add_u32 m0,s57,106368
s_nop 0
buffer_load_dwordx4 v18,s[40:43],s55 offen offset:128  lds
s_add_u32 m0,s57,107392
s_nop 0
buffer_load_dwordx4 v19,s[40:43],s55 offen offset:128  lds
v_add_u32 v26,0,v20
v_add_u32 v27,0,v21
T a[112:113],26,0
T a[114:115],26,512
T a[116:117],27,0
T a[118:119],27,512
T a[120:121],26,4096
T a[122:123],26,4608
T a[124:125],27,4096
T a[126:127],27,4608
s_waitcnt vmcnt(20) lgkmcnt(0)
s_barrier
s_add_u32 s55,s50,1835008
s_add_u32 m0,s57,0
s_nop 0
buffer_load_dwordx4 v18,s[40:43],s55 offen offset:0  lds
s_add_u32 m0,s57,1024
s_nop 0
buffer_load_dwordx4 v19,s[40:43],s55 offen offset:0  lds
s_add_u32 m0,s57,8064
s_nop 0
buffer_load_dwordx4 v18,s[40:43],s55 offen offset:128  lds
s_add_u32 m0,s57,9088
s_nop 0
buffer_load_dwordx4 v19,s[40:43],s55 offen offset:128  lds
v_add_u32 v26,16384,v20
v_add_u32 v27,16384,v21
T a[128:129],26,0
T a[130:131],26,512
T a[132:133],27,0
T a[134:135],27,512
T a[136:137],26,4096
T a[138:139],26,4608
T a[140:141],27,4096
T a[142:143],27,4608
s_waitcnt vmcnt(20) lgkmcnt(0)
s_barrier
s_add_u32 s55,s50,1966080
s_add_u32 m0,s57,16384
s_nop 0
buffer_load_dwordx4 v18,s[40:43],s55 offen offset:0  lds
s_add_u32 m0,s57,17408
s_nop 0
buffer_load_dwordx4 v19,s[40:43],s55 offen offset:0  lds
s_add_u32 m0,s57,24448
s_nop 0
buffer_load_dwordx4 v18,s[40:43],s55 offen offset:128  lds
s_add_u32 m0,s57,25472
s_nop 0
buffer_load_dwordx4 v19,s[40:43],s55 offen offset:128  lds
v_add_u32 v26,32768,v20
v_add_u32 v27,32768,v21
T a[144:145],26,0
T a[146:147],26,512
T a[148:149],27,0
T a[150:151],27,512
T a[152:153],26,4096
T a[154:155],26,4608
T a[156:157],27,4096
T a[158:159],27,4608
s_waitcnt vmcnt(20) lgkmcnt(0)
s_barrier
v_add_u32 v26,49152,v20
v_add_u32 v27,49152,v21
T a[160:161],26,0
T a[162:163],26,512
T a[164:165],27,0
T a[166:167],27,512
T a[168:169],26,4096
T a[170:171],26,4608
T a[172:173],27,4096
T a[174:175],27,4608
s_waitcnt vmcnt(16) lgkmcnt(0)
s_barrier
v_add_u32 v26,65536,v20
v_add_u32 v27,65536,v21
T a[176:177],26,0
T a[178:179],26,512
T a[180:181],27,0
T a[182:183],27,512
T a[184:185],26,4096
T a[186:187],26,4608
T a[188:189],27,4096
T a[190:191],27,4608
s_waitcnt vmcnt(12) lgkmcnt(0)
s_barrier
v_add_u32 v26,81920,v20
v_add_u32 v27,81920,v21
T a[192:193],26,0
T a[194:195],26,512
T a[196:197],27,0
T a[198:199],27,512
T a[200:201],26,4096
T a[202:203],26,4608
T a[204:205],27,4096
T a[206:207],27,4608
s_waitcnt vmcnt(8) lgkmcnt(0)
s_barrier
v_add_u32 v26,98304,v20
v_add_u32 v27,98304,v21
T a[208:209],26,0
T a[210:211],26,512
T a[212:213],27,0
T a[214:215],27,512
T a[216:217],26,4096
T a[218:219],26,4608
T a[220:221],27,4096
T a[222:223],27,4608
s_waitcnt vmcnt(4) lgkmcnt(0)
s_barrier
v_add_u32 v26,0,v20
v_add_u32 v27,0,v21
T a[224:225],26,0
T a[226:227],26,512
T a[228:229],27,0
T a[230:231],27,512
T a[232:233],26,4096
T a[234:235],26,4608
T a[236:237],27,4096
T a[238:239],27,4608
s_waitcnt vmcnt(0) lgkmcnt(0)
s_barrier
v_add_u32 v26,16384,v20
v_add_u32 v27,16384,v21
T a[240:241],26,0
T a[242:243],26,512
T a[244:245],27,0
T a[246:247],27,512
T a[248:249],26,4096
T a[250:251],26,4608
T a[252:253],27,4096
T a[254:255],27,4608
L 0
s_barrier
v_mov_b32 v4,s53
s_mov_b32 s82,0
s_mov_b32 s80,0
s_mov_b32 s54,s45
s_mul_i32 s57,s44,5120
s_mul_i32 s55,s45,40960
s_add_u32 s55,s55,s57
s_add_u32 s55,s55,17825792
s_mul_i32 s57,s44,4096
s_mul_i32 s60,s46,40960
s_mul_i32 s61,s46,1024
s_mul_i32 s62,s46,32768
s_mul_i32 s63,s46,1024
s_mul_i32 s64,s46,512
s_lshl_b32 s59,s44,8
s_add_u32 s59,s59,131072
s_mul_i32 s58,s45,1024
s_add_u32 s58,s58,109333504
s_mul_i32 s65,s45,32768
s_add_u32 s65,s65,s47
s_mul_i32 s68,s45,1024
s_add_u32 s68,s68,s48
s_mul_i32 s73,s45,512
s_add_u32 s73,s73,106954752
s_mul_i32 s74,s45,32768
s_add_u32 s74,s74,s49
s_mul_i32 s75,s45,1024
s_add_u32 s75,s75,107479040
s_mov_b32 s66,0x7fff0000
s_mov_b32 s67,0x7fff0000
s_mov_b32 s69,0x7fff0000
s_mov_b32 s76,0x7fff0000
s_mov_b32 s77,0x7fff0000
s_mov_b32 s70,0x7fff0000
s_mov_b32 s71,0x7fff0000
s_mov_b32 s72,0x7fff0000
v_mov_b32 v1,v0
v_add_u32 v2,16384,v0
s_mov_b32 s84,16384
s_mov_b32 s86,0x3a800000
s_mov_b32 s87,0x38a7c5ac
s_mov_b32 s88,0xc0135761
s_mov_b32 s89,0x3d372713
s_lshr_b32 s85,s44,1
s_mul_i32 s85,s85,127
v_lshlrev_b32 v8,22,v0
v_and_b32 v8,0x80000000,v8
v_or_b32 v8,v8,v5
v_add_u32 v2,32768,v0
s_lshl_b32 s84,s57,1
s_add_u32 s84,s84,32768
s_getpc_b64 s[40:41]
Lg2_pc_%=:
s_lshl_b32 s78,s44,13
v_lshlrev_b32 v106,3,v0
v_add_u32 v106,s78,v106
v_min_u32 v106,Lcode_g12_e-Lg2_pc_%=-4,v106
s_mov_b32 m0,163584
s_nop 0
global_load_lds_dword v106,s[40:41]
s_and_b32 s79,s54,127
s_cmp_eq_u32 s79,0
s_cselect_b32 s94,0x3300,0
s_cmp_eq_u32 s79,127
s_cselect_b32 s94,0xcc00,s94
s_add_u32 m0,s59,4096
s_nop 0
buffer_load_dword v8,s[36:39],s58 offen offset:0 sc1 lds
buffer_store_dword v4,v3,s[36:39],s71 offen offset:0 sc0 sc1
s_add_u32 m0,s59,5120
s_nop 0
buffer_load_dword v8,s[36:39],s58 offen offset:0 sc1 lds
buffer_store_dword v4,v3,s[36:39],s71 offen offset:0 sc0 sc1
s_mov_b32 s83,0
Lg2_poll_%=:
s_add_u32 s78,s83,2
s_and_b32 s78,s78,3
s_lshl_b32 s78,s78,10
s_add_u32 s78,s78,4096
s_add_u32 m0,s59,s78
s_nop 0
buffer_load_dword v8,s[36:39],s58 offen offset:0 sc1 lds
s_and_b32 s78,s83,3
s_lshl_b32 s78,s78,10
v_add_u32 v106,s78,v6
s_waitcnt vmcnt(4)
s_barrier
ds_read_b32 v7,v106 offset:4096
buffer_store_dword v4,v3,s[36:39],s70 offen offset:0 sc0 sc1
s_mov_b32 s70,s71
s_mov_b32 s71,s72
s_mov_b32 s72,0x7fff0000
s_add_u32 s83,s83,1
s_add_u32 s82,s82,1
L 0
v_cmp_eq_u32 vcc,s53,v7
s_nop 0
s_or_b64 vcc,vcc,s[94:95]
s_cmp_eq_u32 vcc_lo,-1
s_cbranch_scc1 Lg2_go_%=
s_cmp_lt_u32 s82,20000
s_cbranch_scc1 Lg2_poll_%=
Lg2_go_%=:
s_and_b32 s78,s54,127
s_cmp_eq_u32 s78,s85
s_cselect_b64 s[40:41],-1,0
v_cndmask_b32 v17,v15,v16,s[40:41]
s_add_u32 m0,s57,0
s_add_u32 s56,s55,0
D 17,56,0
s_add_u32 s56,s55,256
D 17,56,1024
s_add_u32 s56,s55,512
D 17,56,2048
s_add_u32 s56,s55,768
D 17,56,3072
s_add_u32 s58,s58,s61
s_add_u32 m0,s59,2048
s_nop 0
buffer_load_dword v8,s[36:39],s58 offen offset:0 sc1 lds
v_cndmask_b32 v17,v10,v9,s[40:41]
s_add_u32 m0,s57,16384
s_add_u32 s56,s55,20480
D 17,56,0
s_add_u32 s56,s55,20736
D 17,56,1024
s_add_u32 s56,s55,20992
D 17,56,2048
s_add_u32 s56,s55,21248
D 17,56,3072
s_add_u32 s55,s55,s60
s_sub_u32 s57,s84,s57
s_add_u32 s54,s54,s46
s_and_b32 s79,s54,127
s_cmp_eq_u32 s79,0
s_cselect_b32 s94,0x3300,0
s_cmp_eq_u32 s79,127
s_cselect_b32 s94,0xcc00,s94
s_waitcnt vmcnt(4)
s_barrier
buffer_store_dword v4,v3,s[36:39],s70 offen offset:0 sc0 sc1
s_min_u32 s71,s71,s72
buffer_store_dword v4,v3,s[36:39],s71 offen offset:0 sc0 sc1
s_mov_b32 s70,0x7fff0000
s_mov_b32 s71,0x7fff0000
s_mov_b32 s72,0x7fff0000
s_cmp_eq_u32 s80,1
buffer_store_dword v4,v3,s[36:39],s71 offen offset:0 sc0 sc1
s_cbranch_scc1 Lg2_st1_%=
s_branch Lg2_st0_%=
Lg2_gox_%=:
s_add_u32 m0,s59,2048
s_nop 0
buffer_load_dword v8,s[36:39],s58 offen offset:0 sc1 lds
s_and_b32 s79,s54,127
s_cmp_eq_u32 s79,0
s_cselect_b32 s94,0x3300,0
s_cmp_eq_u32 s79,127
s_cselect_b32 s94,0xcc00,s94
s_waitcnt vmcnt(0)
s_barrier
buffer_store_dword v4,v3,s[36:39],s70 offen offset:0 sc0 sc1
s_min_u32 s71,s71,s72
buffer_store_dword v4,v3,s[36:39],s71 offen offset:0 sc0 sc1
s_mov_b32 s70,0x7fff0000
s_mov_b32 s71,0x7fff0000
s_mov_b32 s72,0x7fff0000
s_cmp_eq_u32 s80,1
s_cbranch_scc1 Lg2_st1_%=
Lg2_st0_%=:
ds_read_b32 v7,v6 offset:2048
R 34,1,0
R 38,1,1024
R 42,1,2048
R 46,1,3072
R 50,1,4096
L 5
v_cmp_eq_u32 vcc,s53,v7
s_nop 0
s_or_b64 vcc,vcc,s[94:95]
L 3
s_cmp_eq_u32 vcc_lo,-1
s_cbranch_scc1 Lg2_F0_%=
s_branch Lg2_L0_%=
Lg2_st1_%=:
ds_read_b32 v7,v6 offset:2048
R 34,2,0
R 38,2,1024
R 42,2,2048
R 46,2,3072
R 50,2,4096
L 5
v_cmp_eq_u32 vcc,s53,v7
s_nop 0
s_or_b64 vcc,vcc,s[94:95]
L 3
s_cmp_eq_u32 vcc_lo,-1
s_cbranch_scc1 Lg2_F1_%=
s_branch Lg2_L1_%=
Lg2_F0_%=:
v_mfma_f32_16x16x32_bf16 v[22:25],a[4:7],v[34:37],0
s_add_u32 s58,s58,s61
s_add_u32 m0,s59,0
s_and_b32 s78,s54,127
R 54,1,5120
M 22,a[12:15],38
s_cmp_eq_u32 s78,s85
s_cselect_b64 s[40:41],-1,0
s_add_u32 s56,s55,0
buffer_load_dword v8,s[36:39],s58 offen offset:0 sc1 lds
v_mfma_f32_16x16x32_bf16 v[18:21],a[0:3],v[34:37],0
s_add_u32 m0,s57,0
R 58,1,6144
v_cndmask_b32 v17,v15,v16,s[40:41]
M 18,a[8:11],38
L 3
D 17,56,0
M 18,a[16:19],42
s_add_u32 s56,s55,256
R 62,1,7168
v_lshlrev_b32 v106,16,v126
M 18,a[24:27],46
s_add_u32 s54,s54,s46
v_and_b32 v107,0xffff0000,v126
v_lshlrev_b32 v108,16,v127
M 22,a[20:23],42
s_and_b32 s79,s54,127
R 66,1,8192
v_and_b32 v109,0xffff0000,v127
M 22,a[28:31],46
L 3
D 17,56,1024
M 22,a[36:39],50
s_add_u32 s56,s55,512
)"
R"(R 70,1,9216
v_lshlrev_b32 v110,16,v128
M 22,a[44:47],54
s_cmp_eq_u32 s79,0
v_and_b32 v111,0xffff0000,v128
v_lshlrev_b32 v112,16,v129
M 18,a[32:35],50
s_cselect_b32 s94,0x3300,0
R 74,1,10240
v_and_b32 v113,0xffff0000,v129
M 18,a[40:43],54
L 3
D 17,56,2048
M 18,a[48:51],58
s_add_u32 s56,s55,768
R 78,1,11264
v_fma_f32 v106,v106,v103,v102
M 18,a[56:59],62
D 17,56,3072
M 22,a[52:55],58
s_add_u32 m0,s57,16384
R 82,1,12288
v_fma_f32 v107,v107,v103,v102
M 22,a[60:63],62
L 3
v_fma_f32 v108,v108,v103,v102
v_fma_f32 v109,v109,v103,v102
M 22,a[68:71],66
s_add_u32 s56,s55,20480
R 86,1,13312
v_fma_f32 v110,v110,v103,v102
M 22,a[76:79],70
buffer_load_dwordx4 v[122:125],v0,s[36:39],s74 offen offset:0 sc1
M 18,a[64:67],66
s_cmp_eq_u32 s79,127
R 90,1,14336
v_fma_f32 v111,v111,v103,v102
M 18,a[72:75],70
L 3
buffer_load_dwordx2 v[98:99],v11,s[36:39],s73 offen offset:256 sc1
M 18,a[80:83],74
s_waitcnt vmcnt(10)
R 94,1,15360
v_fma_f32 v112,v112,v103,v102
M 18,a[88:91],78
s_barrier
v_fma_f32 v113,v113,v103,v102
v_add_f32 v26,v26,v106
M 22,a[84:87],74
s_cselect_b32 s94,0xcc00,s94
R 34,1,16384
v_add_f32 v27,v27,v107
M 22,a[92:95],78
L 3
v_add_f32 v28,v28,v108
v_add_f32 v29,v29,v109
M 22,a[100:103],82
s_mov_b32 s66,s65
R 38,1,17408
v_add_f32 v30,v30,v110
M 22,a[108:111],86
s_mov_b32 s76,s75
v_cndmask_b32 v17,v10,v9,s[40:41]
v_add_f32 v31,v31,v111
M 18,a[96:99],82
s_add_u32 s73,s73,s64
R 42,1,18432
v_add_f32 v32,v32,v112
M 18,a[104:107],86
L 3
D 17,56,0
M 18,a[112:115],90
s_add_u32 s56,s55,20736
R 46,1,19456
v_add_f32 v33,v33,v113
M 18,a[120:123],94
s_add_u32 s74,s74,s62
v_cvt_pk_bf16_f32 v114,v26,v27
v_cvt_pk_bf16_f32 v115,v28,v29
M 22,a[116:119],90
s_add_u32 s65,s65,s62
R 50,1,20480
v_fma_f32 v107,v26,v26,s87
M 22,a[124:127],94
L 3
v_add_f32 v106,v26,v27
v_fma_f32 v107,v27,v27,v107
M 22,a[132:135],34
s_add_u32 s75,s75,s63
R 54,1,21504
v_add_f32 v106,v106,v28
M 22,a[140:143],38
D 17,56,1024
M 18,a[128:131],34
s_add_u32 s56,s55,20992
R 58,1,22528
v_fma_f32 v107,v28,v28,v107
M 18,a[136:139],38
L 3
v_add_f32 v106,v106,v29
v_fma_f32 v107,v29,v29,v107
M 18,a[144:147],42
s_sub_u32 s57,s84,s57
R 62,1,23552
v_add_f32 v106,v106,v30
M 18,a[152:155],46
v_fma_f32 v107,v30,v30,v107
v_add_f32 v106,v106,v31
M 22,a[148:151],42
R 66,1,24576
v_fma_f32 v107,v31,v31,v107
M 22,a[156:159],46
L 3
v_add_f32 v106,v106,v32
v_fma_f32 v107,v32,v32,v107
M 22,a[164:167],50
R 70,1,25600
v_add_f32 v106,v106,v33
M 22,a[172:175],54
v_fma_f32 v107,v33,v33,v107
v_cvt_pk_bf16_f32 v116,v30,v31
M 18,a[160:163],50
R 74,1,26624
v_cvt_pk_bf16_f32 v117,v32,v33
M 18,a[168:171],54
L 3
v_permlane16_swap_b32 v106,v107
v_add_f32 v106,v106,v107
v_mov_b32 v107,v106
M 18,a[176:179],58
R 78,1,27648
M 18,a[184:187],62
v_permlane32_swap_b32 v106,v107
v_add_f32 v106,v106,v107
M 22,a[180:183],58
R 82,1,28672
ds_write_b32 v12,v106 offset:2048
M 22,a[188:191],62
L 4
D 17,56,2048
M 22,a[196:199],66
s_add_u32 s56,s55,21248
s_add_u32 s55,s55,s60
R 86,1,29696
M 22,a[204:207],70
D 17,56,3072
M 18,a[192:195],66
R 90,1,30720
M 18,a[200:203],70
L 4
L 2
buffer_store_dwordx4 v[114:117],v0,s[36:39],s67 offen offset:0 sc0 sc1
M 18,a[208:211],74
s_waitcnt vmcnt(7)
R 94,1,31744
M 18,a[216:219],78
s_barrier
ds_read_b32 v7,v6 offset:0
R 118,13,2048
M 22,a[212:215],74
R 34,2,0
M 22,a[220:223],78
L 5
buffer_store_dword v4,v3,s[36:39],s70 offen offset:0 sc0 sc1
M 22,a[228:231],82
s_mov_b32 s70,s69
s_mov_b32 s69,s68
R 38,2,1024
M 22,a[236:239],86
s_add_u32 s68,s68,s63
M 18,a[224:227],82
R 42,2,2048
M 18,a[232:235],86
L 5
L 4
v_cmp_eq_u32 vcc,s53,v7
M 18,a[240:243],90
L 3
R 46,2,3072
v_add_f32 v118,v118,v119
M 18,a[248:251],94
s_or_b64 vcc,vcc,s[94:95]
v_add_f32 v120,v120,v121
v_add_f32 v118,v118,v120
M 22,a[244:247],90
s_cmp_eq_u32 vcc_lo,-1
R 50,2,4096
v_mul_f32 v118,s86,v118
M 22,a[252:255],94
s_waitcnt vmcnt(6) lgkmcnt(3)
buffer_store_dword v118,v14,s[36:39],s77 offen offset:0 sc0 sc1
s_cbranch_scc0 Lg2_K1_%=
Lg2_F1_%=:
v_mfma_f32_16x16x32_bf16 v[30:33],a[4:7],v[34:37],0
s_add_u32 s58,s58,s61
s_add_u32 m0,s59,1024
s_and_b32 s78,s54,127
R 54,2,5120
M 30,a[12:15],38
s_cmp_eq_u32 s78,s85
s_cselect_b64 s[40:41],-1,0
s_add_u32 s56,s55,0
buffer_load_dword v8,s[36:39],s58 offen offset:0 sc1 lds
v_mfma_f32_16x16x32_bf16 v[26:29],a[0:3],v[34:37],0
s_add_u32 m0,s57,0
R 58,2,6144
v_cndmask_b32 v17,v15,v16,s[40:41]
M 26,a[8:11],38
L 3
D 17,56,0
M 26,a[16:19],42
s_add_u32 s56,s55,256
R 62,2,7168
v_lshlrev_b32 v106,16,v122
M 26,a[24:27],46
s_add_u32 s54,s54,s46
v_and_b32 v107,0xffff0000,v122
v_lshlrev_b32 v108,16,v123
M 30,a[20:23],42
s_and_b32 s79,s54,127
R 66,2,8192
v_and_b32 v109,0xffff0000,v123
M 30,a[28:31],46
L 3
D 17,56,1024
M 30,a[36:39],50
s_add_u32 s56,s55,512
R 70,2,9216
v_lshlrev_b32 v110,16,v124
M 30,a[44:47],54
s_cmp_eq_u32 s79,0
v_and_b32 v111,0xffff0000,v124
v_lshlrev_b32 v112,16,v125
M 26,a[32:35],50
s_cselect_b32 s94,0x3300,0
R 74,2,10240
v_and_b32 v113,0xffff0000,v125
M 26,a[40:43],54
L 3
D 17,56,2048
M 26,a[48:51],58
s_add_u32 s56,s55,768
R 78,2,11264
v_fma_f32 v106,v106,v99,v98
M 26,a[56:59],62
D 17,56,3072
M 30,a[52:55],58
s_add_u32 m0,s57,16384
R 82,2,12288
v_fma_f32 v107,v107,v99,v98
M 30,a[60:63],62
L 3
v_fma_f32 v108,v108,v99,v98
v_fma_f32 v109,v109,v99,v98
M 30,a[68:71],66
s_add_u32 s56,s55,20480
R 86,2,13312
v_fma_f32 v110,v110,v99,v98
M 30,a[76:79],70
buffer_load_dwordx4 v[126:129],v0,s[36:39],s74 offen offset:0 sc1
M 26,a[64:67],66
s_cmp_eq_u32 s79,127
R 90,2,14336
v_fma_f32 v111,v111,v99,v98
M 26,a[72:75],70
L 3
buffer_load_dwordx2 v[102:103],v11,s[36:39],s73 offen offset:256 sc1
M 26,a[80:83],74
s_waitcnt vmcnt(10)
R 94,2,15360
v_fma_f32 v112,v112,v99,v98
M 26,a[88:91],78
s_barrier
v_fma_f32 v113,v113,v99,v98
v_add_f32 v18,v18,v106
M 30,a[84:87],74
s_cselect_b32 s94,0xcc00,s94
R 34,2,16384
v_add_f32 v19,v19,v107
M 30,a[92:95],78
L 3
v_add_f32 v20,v20,v108
v_add_f32 v21,v21,v109
M 30,a[100:103],82
s_mov_b32 s67,s65
R 38,2,17408
v_add_f32 v22,v22,v110
M 30,a[108:111],86
s_mov_b32 s77,s75
v_cndmask_b32 v17,v10,v9,s[40:41]
v_add_f32 v23,v23,v111
M 26,a[96:99],82
s_add_u32 s73,s73,s64
R 42,2,18432
v_add_f32 v24,v24,v112
M 26,a[104:107],86
L 3
D 17,56,0
M 26,a[112:115],90
s_add_u32 s56,s55,20736
R 46,2,19456
v_add_f32 v25,v25,v113
M 26,a[120:123],94
s_add_u32 s74,s74,s62
v_cvt_pk_bf16_f32 v114,v18,v19
v_cvt_pk_bf16_f32 v115,v20,v21
M 30,a[116:119],90
s_add_u32 s65,s65,s62
R 50,2,20480
v_fma_f32 v107,v18,v18,s87
M 30,a[124:127],94
L 3
v_add_f32 v106,v18,v19
v_fma_f32 v107,v19,v19,v107
M 30,a[132:135],34
s_add_u32 s75,s75,s63
R 54,2,21504
v_add_f32 v106,v106,v20
M 30,a[140:143],38
D 17,56,1024
M 26,a[128:131],34
s_add_u32 s56,s55,20992
R 58,2,22528
v_fma_f32 v107,v20,v20,v107
M 26,a[136:139],38
L 3
v_add_f32 v106,v106,v21
v_fma_f32 v107,v21,v21,v107
M 26,a[144:147],42
s_sub_u32 s57,s84,s57
R 62,2,23552
v_add_f32 v106,v106,v22
M 26,a[152:155],46
v_fma_f32 v107,v22,v22,v107
v_add_f32 v106,v106,v23
M 30,a[148:151],42
R 66,2,24576
v_fma_f32 v107,v23,v23,v107
M 30,a[156:159],46
L 3
v_add_f32 v106,v106,v24
v_fma_f32 v107,v24,v24,v107
M 30,a[164:167],50
R 70,2,25600
v_add_f32 v106,v106,v25
M 30,a[172:175],54
v_fma_f32 v107,v25,v25,v107
v_cvt_pk_bf16_f32 v116,v22,v23
M 26,a[160:163],50
R 74,2,26624
v_cvt_pk_bf16_f32 v117,v24,v25
M 26,a[168:171],54
L 3
v_permlane16_swap_b32 v106,v107
v_add_f32 v106,v106,v107
v_mov_b32 v107,v106
M 26,a[176:179],58
R 78,2,27648
M 26,a[184:187],62
v_permlane32_swap_b32 v106,v107
v_add_f32 v106,v106,v107
M 30,a[180:183],58
R 82,2,28672
ds_write_b32 v12,v106 offset:0
M 30,a[188:191],62
L 4
D 17,56,2048
M 30,a[196:199],66
s_add_u32 s56,s55,21248
s_add_u32 s55,s55,s60
R 86,2,29696
M 30,a[204:207],70
D 17,56,3072
M 26,a[192:195],66
R 90,2,30720
M 26,a[200:203],70
L 4
L 2
buffer_store_dwordx4 v[114:117],v0,s[36:39],s66 offen offset:0 sc0 sc1
M 26,a[208:211],74
s_waitcnt vmcnt(7)
R 94,2,31744
M 26,a[216:219],78
s_barrier
ds_read_b32 v7,v6 offset:1024
R 118,13,0
M 30,a[212:215],74
R 34,1,0
M 30,a[220:223],78
L 5
buffer_store_dword v4,v3,s[36:39],s70 offen offset:0 sc0 sc1
M 30,a[228:231],82
s_mov_b32 s70,s69
s_mov_b32 s69,s68
R 38,1,1024
M 30,a[236:239],86
s_add_u32 s68,s68,s63
M 26,a[224:227],82
R 42,1,2048
M 26,a[232:235],86
L 5
L 4
v_cmp_eq_u32 vcc,s53,v7
M 26,a[240:243],90
L 3
R 46,1,3072
v_add_f32 v118,v118,v119
M 26,a[248:251],94
s_or_b64 vcc,vcc,s[94:95]
v_add_f32 v120,v120,v121
v_add_f32 v118,v118,v120
M 30,a[244:247],90
s_cmp_eq_u32 vcc_lo,-1
R 50,1,4096
v_mul_f32 v118,s86,v118
M 30,a[252:255],94
s_waitcnt vmcnt(6) lgkmcnt(3)
buffer_store_dword v118,v14,s[36:39],s76 offen offset:0 sc0 sc1
s_cbranch_scc1 Lg2_F0_%=
Lg2_K0_%=:
Lg2_L0_%=:
v_mfma_f32_16x16x32_bf16 v[22:25],a[4:7],v[34:37],0
s_add_u32 m0,s59,3072
R 54,1,5120
v_lshlrev_b32 v106,16,v126
M 22,a[12:15],38
buffer_load_dword v8,s[36:39],s58 offen offset:0 sc1 lds
v_mfma_f32_16x16x32_bf16 v[18:21],a[0:3],v[34:37],0
s_mov_b32 s66,s65
R 58,1,6144
v_and_b32 v107,0xffff0000,v126
M 18,a[8:11],38
L 3
v_lshlrev_b32 v108,16,v127
v_and_b32 v109,0xffff0000,v127
M 18,a[16:19],42
s_mov_b32 s76,s75
R 62,1,7168
v_lshlrev_b32 v110,16,v128
M 18,a[24:27],46
s_add_u32 s65,s65,s62
v_and_b32 v111,0xffff0000,v128
v_lshlrev_b32 v112,16,v129
M 22,a[20:23],42
s_add_u32 s75,s75,s63
R 66,1,8192
v_and_b32 v113,0xffff0000,v129
v_fma_f32 v106,v106,v103,v102
M 22,a[28:31],46
L 3
v_fma_f32 v107,v107,v103,v102
v_fma_f32 v108,v108,v103,v102
M 22,a[36:39],50
R 70,1,9216
v_fma_f32 v109,v109,v103,v102
M 22,a[44:47],54
v_fma_f32 v110,v110,v103,v102
v_fma_f32 v111,v111,v103,v102
M 18,a[32:35],50
R 74,1,10240
v_fma_f32 v112,v112,v103,v102
M 18,a[40:43],54
L 3
v_fma_f32 v113,v113,v103,v102
v_add_f32 v26,v26,v106
M 18,a[48:51],58
R 78,1,11264
v_add_f32 v27,v27,v107
M 18,a[56:59],62
v_add_f32 v28,v28,v108
v_add_f32 v29,v29,v109
v_add_f32 v30,v30,v110
M 22,a[52:55],58
R 82,1,12288
v_add_f32 v31,v31,v111
M 22,a[60:63],62
L 3
v_add_f32 v32,v32,v112
v_add_f32 v33,v33,v113
M 22,a[68:71],66
R 86,1,13312
v_cvt_pk_bf16_f32 v114,v26,v27
M 22,a[76:79],70
buffer_load_dwordx4 v[122:125],v0,s[36:39],s74 offen offset:0 sc1
M 18,a[64:67],66
s_add_u32 s74,s74,s62
R 90,1,14336
v_cvt_pk_bf16_f32 v115,v28,v29
M 18,a[72:75],70
L 3
buffer_load_dwordx2 v[98:99],v11,s[36:39],s73 offen offset:256 sc1
v_cvt_pk_bf16_f32 v116,v30,v31
M 18,a[80:83],74
s_waitcnt vmcnt(2)
s_add_u32 s73,s73,s64
R 94,1,15360
M 18,a[88:91],78
s_barrier
ds_read_b32 v7,v6 offset:3072
v_cvt_pk_bf16_f32 v117,v32,v33
)"
R"(M 22,a[84:87],74
R 34,1,16384
v_add_f32 v106,v26,v27
M 22,a[92:95],78
L 4
buffer_store_dword v4,v3,s[36:39],s70 offen offset:0 sc0 sc1
M 22,a[100:103],82
s_mov_b32 s70,0x7fff0000
R 38,1,17408
v_fma_f32 v107,v26,v26,s87
M 22,a[108:111],86
buffer_store_dwordx4 v[114:117],v0,s[36:39],s67 offen offset:0 sc0 sc1
v_fma_f32 v107,v27,v27,v107
M 18,a[96:99],82
R 42,1,18432
v_add_f32 v106,v106,v28
v_fma_f32 v107,v28,v28,v107
M 18,a[104:107],86
L 4
L 3
v_cmp_eq_u32 vcc,s53,v7
M 18,a[112:115],90
R 46,1,19456
v_add_f32 v106,v106,v29
M 18,a[120:123],94
s_or_b64 vcc,vcc,s[94:95]
v_fma_f32 v107,v29,v29,v107
v_add_f32 v106,v106,v30
M 22,a[116:119],90
R 50,1,20480
v_fma_f32 v107,v30,v30,v107
M 22,a[124:127],94
s_cmp_eq_u32 vcc_lo,-1
L 3
v_add_f32 v106,v106,v31
s_cbranch_scc0 Lg2_X0_%=
M 22,a[132:135],34
s_and_b32 s78,s54,127
s_cmp_eq_u32 s78,s85
s_cselect_b64 s[40:41],-1,0
R 54,1,21504
M 22,a[140:143],38
s_add_u32 m0,s57,0
s_add_u32 s56,s55,0
s_add_u32 s58,s58,s61
v_cndmask_b32 v17,v15,v16,s[40:41]
M 18,a[128:131],34
s_add_u32 s54,s54,s46
R 58,1,22528
v_fma_f32 v107,v31,v31,v107
M 18,a[136:139],38
L 3
D 17,56,0
M 18,a[144:147],42
s_add_u32 s56,s55,256
R 62,1,23552
v_add_f32 v106,v106,v32
M 18,a[152:155],46
s_and_b32 s79,s54,127
v_fma_f32 v107,v32,v32,v107
v_add_f32 v106,v106,v33
M 22,a[148:151],42
s_cmp_eq_u32 s79,0
R 66,1,24576
v_fma_f32 v107,v33,v33,v107
M 22,a[156:159],46
L 3
D 17,56,1024
M 22,a[164:167],50
s_cselect_b32 s94,0x3300,0
s_add_u32 s56,s55,512
R 70,1,25600
M 22,a[172:175],54
s_cmp_eq_u32 s79,127
v_permlane16_swap_b32 v106,v107
v_add_f32 v106,v106,v107
M 18,a[160:163],50
s_cselect_b32 s94,0xcc00,s94
R 74,1,26624
v_mov_b32 v107,v106
M 18,a[168:171],54
L 3
D 17,56,2048
M 18,a[176:179],58
s_add_u32 s56,s55,768
R 78,1,27648
M 18,a[184:187],62
v_permlane32_swap_b32 v106,v107
v_add_f32 v106,v106,v107
M 22,a[180:183],58
R 82,1,28672
ds_write_b32 v12,v106 offset:2048
M 22,a[188:191],62
L 4
D 17,56,3072
M 22,a[196:199],66
s_add_u32 m0,s59,0
s_add_u32 s56,s55,20480
R 86,1,29696
M 22,a[204:207],70
buffer_load_dword v8,s[36:39],s58 offen offset:0 sc1 lds
M 18,a[192:195],66
s_add_u32 m0,s57,16384
R 90,1,30720
v_cndmask_b32 v17,v10,v9,s[40:41]
M 18,a[200:203],70
L 4
L 2
s_sub_u32 s57,s84,s57
M 18,a[208:211],74
s_waitcnt vmcnt(0)
R 94,1,31744
M 18,a[216:219],78
s_barrier
D 17,56,0
M 22,a[212:215],74
s_add_u32 s56,s55,20736
R 34,2,0
ds_read_b32 v7,v6 offset:0
M 22,a[220:223],78
L 4
buffer_store_dword v4,v3,s[36:39],s70 offen offset:0 sc0 sc1
M 22,a[228:231],82
s_mov_b32 s70,s69
R 38,2,1024
R 118,13,2048
M 22,a[236:239],86
D 17,56,1024
M 18,a[224:227],82
s_add_u32 s56,s55,20992
s_mov_b32 s69,s68
R 42,2,2048
M 18,a[232:235],86
L 5
D 17,56,2048
M 18,a[240:243],90
L 3
s_add_u32 s56,s55,21248
R 46,2,3072
v_cmp_eq_u32 vcc,s53,v7
M 18,a[248:251],94
D 17,56,3072
M 22,a[244:247],90
s_add_u32 s55,s55,s60
s_add_u32 s68,s68,s63
L 2
s_or_b64 vcc,vcc,s[94:95]
s_cmp_eq_u32 vcc_lo,-1
R 50,2,4096
v_add_f32 v118,v118,v119
v_add_f32 v120,v120,v121
v_add_f32 v118,v118,v120
v_mul_f32 v118,s86,v118
M 22,a[252:255],94
s_nop 0
buffer_store_dword v118,v14,s[36:39],s77 offen offset:0 sc0 sc1
buffer_store_dword v4,v3,s[36:39],s71 offen offset:0 sc0 sc1
buffer_store_dword v4,v3,s[36:39],s71 offen offset:0 sc0 sc1
s_cbranch_scc1 Lg2_F1_%=
s_branch Lg2_K1_%=
Lg2_X0_%=:
M 22,a[132:135],34
s_add_u32 m0,s59,0
R 54,1,21504
v_fma_f32 v107,v31,v31,v107
M 22,a[140:143],38
buffer_load_dword v8,s[36:39],s58 offen offset:0 sc1 lds
M 18,a[128:131],34
R 58,1,22528
v_add_f32 v106,v106,v32
M 18,a[136:139],38
L 3
v_fma_f32 v107,v32,v32,v107
v_add_f32 v106,v106,v33
M 18,a[144:147],42
R 62,1,23552
v_fma_f32 v107,v33,v33,v107
M 18,a[152:155],46
M 22,a[148:151],42
R 66,1,24576
M 22,a[156:159],46
L 3
v_permlane16_swap_b32 v106,v107
v_add_f32 v106,v106,v107
M 22,a[164:167],50
R 70,1,25600
v_mov_b32 v107,v106
M 22,a[172:175],54
M 18,a[160:163],50
R 74,1,26624
M 18,a[168:171],54
L 3
v_permlane32_swap_b32 v106,v107
M 18,a[176:179],58
R 78,1,27648
v_add_f32 v106,v106,v107
M 18,a[184:187],62
ds_write_b32 v12,v106 offset:2048
M 22,a[180:183],58
R 82,1,28672
M 22,a[188:191],62
L 4
M 22,a[196:199],66
R 86,1,29696
M 22,a[204:207],70
M 18,a[192:195],66
R 90,1,30720
M 18,a[200:203],70
L 4
L 3
M 18,a[208:211],74
s_waitcnt vmcnt(0)
R 94,1,31744
M 18,a[216:219],78
s_barrier
R 118,13,2048
ds_read_b32 v7,v6 offset:0
M 22,a[212:215],74
buffer_store_dword v4,v3,s[36:39],s70 offen offset:0 sc0 sc1
M 22,a[220:223],78
L 4
s_mov_b32 s70,s69
s_mov_b32 s69,s68
M 22,a[228:231],82
s_add_u32 s68,s68,s63
M 22,a[236:239],86
M 18,a[224:227],82
M 18,a[232:235],86
L 2
L 1
L 0
M 18,a[240:243],90
v_add_f32 v118,v118,v119
v_add_f32 v120,v120,v121
v_add_f32 v118,v118,v120
M 18,a[248:251],94
v_cmp_eq_u32 vcc,s53,v7
v_mul_f32 v118,s86,v118
M 22,a[244:247],90
buffer_store_dword v118,v14,s[36:39],s77 offen offset:0 sc0 sc1
M 22,a[252:255],94
s_nop 0
s_or_b64 vcc,vcc,s[94:95]
s_mov_b32 s80,1
s_cmp_eq_u32 vcc_lo,-1
s_cbranch_scc0 Lg2_xn0_%=
s_and_b32 s78,s54,127
s_cmp_eq_u32 s78,s85
s_cselect_b64 s[40:41],-1,0
v_cndmask_b32 v17,v15,v16,s[40:41]
s_add_u32 m0,s57,0
s_add_u32 s56,s55,0
D 17,56,0
v_lshlrev_b32 v106,16,v122
v_and_b32 v107,0xffff0000,v122
v_lshlrev_b32 v108,16,v123
v_and_b32 v109,0xffff0000,v123
v_lshlrev_b32 v110,16,v124
s_add_u32 s56,s55,256
D 17,56,1024
v_and_b32 v111,0xffff0000,v124
v_lshlrev_b32 v112,16,v125
v_and_b32 v113,0xffff0000,v125
v_fma_f32 v106,v106,v99,v98
v_fma_f32 v107,v107,v99,v98
s_add_u32 s56,s55,512
D 17,56,2048
v_fma_f32 v108,v108,v99,v98
v_fma_f32 v109,v109,v99,v98
v_fma_f32 v110,v110,v99,v98
v_fma_f32 v111,v111,v99,v98
v_fma_f32 v112,v112,v99,v98
s_add_u32 s56,s55,768
D 17,56,3072
v_fma_f32 v113,v113,v99,v98
v_add_f32 v18,v18,v106
v_add_f32 v19,v19,v107
v_add_f32 v20,v20,v108
v_add_f32 v21,v21,v109
s_add_u32 s58,s58,s61
v_cndmask_b32 v17,v10,v9,s[40:41]
s_add_u32 m0,s57,16384
s_add_u32 s56,s55,20480
D 17,56,0
v_add_f32 v22,v22,v110
v_add_f32 v23,v23,v111
v_add_f32 v24,v24,v112
v_add_f32 v25,v25,v113
v_cvt_pk_bf16_f32 v114,v18,v19
s_add_u32 s56,s55,20736
D 17,56,1024
v_cvt_pk_bf16_f32 v115,v20,v21
v_cvt_pk_bf16_f32 v116,v22,v23
v_cvt_pk_bf16_f32 v117,v24,v25
s_add_u32 s56,s55,20992
D 17,56,2048
s_add_u32 s56,s55,21248
D 17,56,3072
buffer_store_dwordx4 v[114:117],v0,s[36:39],s66 offen offset:0 sc0 sc1
v_add_f32 v106,v18,v19
v_fma_f32 v107,v18,v18,s87
s_add_u32 s55,s55,s60
s_sub_u32 s57,s84,s57
s_add_u32 s54,s54,s46
v_fma_f32 v107,v19,v19,v107
v_add_f32 v106,v106,v20
v_fma_f32 v107,v20,v20,v107
v_add_f32 v106,v106,v21
v_fma_f32 v107,v21,v21,v107
v_add_f32 v106,v106,v22
v_fma_f32 v107,v22,v22,v107
v_add_f32 v106,v106,v23
v_fma_f32 v107,v23,v23,v107
v_add_f32 v106,v106,v24
v_fma_f32 v107,v24,v24,v107
v_add_f32 v106,v106,v25
v_fma_f32 v107,v25,v25,v107
s_nop 1
v_permlane16_swap_b32 v106,v107
v_add_f32 v106,v106,v107
v_mov_b32 v107,v106
s_nop 1
v_permlane32_swap_b32 v106,v107
v_add_f32 v106,v106,v107
ds_write_b32 v12,v106 offset:0
L 0
s_barrier
R 118,13,0
L 0
v_add_f32 v118,v118,v119
v_add_f32 v120,v120,v121
v_add_f32 v118,v118,v120
v_mul_f32 v118,s86,v118
buffer_store_dword v118,v14,s[36:39],s76 offen offset:0 sc0 sc1
s_mov_b32 s72,s69
s_mov_b32 s69,0x7fff0000
s_mov_b32 s66,0x7fff0000
s_mov_b32 s76,0x7fff0000
s_branch Lg2_gox_%=
Lg2_xn0_%=:
s_cmp_ge_u32 s54,256
s_cbranch_scc1 Lg2_fin0_%=
s_add_u32 m0,s59,4096
s_nop 0
buffer_load_dword v8,s[36:39],s58 offen offset:0 sc1 lds
buffer_store_dword v4,v3,s[36:39],s71 offen offset:0 sc0 sc1
s_add_u32 m0,s59,5120
s_nop 0
buffer_load_dword v8,s[36:39],s58 offen offset:0 sc1 lds
buffer_store_dword v4,v3,s[36:39],s71 offen offset:0 sc0 sc1
s_mov_b32 s83,0
v_lshlrev_b32 v106,16,v122
v_and_b32 v107,0xffff0000,v122
v_lshlrev_b32 v108,16,v123
v_and_b32 v109,0xffff0000,v123
v_lshlrev_b32 v110,16,v124
v_and_b32 v111,0xffff0000,v124
v_lshlrev_b32 v112,16,v125
v_and_b32 v113,0xffff0000,v125
v_fma_f32 v106,v106,v99,v98
v_fma_f32 v107,v107,v99,v98
v_fma_f32 v108,v108,v99,v98
v_fma_f32 v109,v109,v99,v98
v_fma_f32 v110,v110,v99,v98
v_fma_f32 v111,v111,v99,v98
v_fma_f32 v112,v112,v99,v98
v_fma_f32 v113,v113,v99,v98
v_add_f32 v18,v18,v106
v_add_f32 v19,v19,v107
v_add_f32 v20,v20,v108
v_add_f32 v21,v21,v109
v_add_f32 v22,v22,v110
v_add_f32 v23,v23,v111
v_add_f32 v24,v24,v112
v_add_f32 v25,v25,v113
v_cvt_pk_bf16_f32 v114,v18,v19
v_cvt_pk_bf16_f32 v115,v20,v21
v_cvt_pk_bf16_f32 v116,v22,v23
v_cvt_pk_bf16_f32 v117,v24,v25
buffer_store_dwordx4 v[114:117],v0,s[36:39],s66 offen offset:0 sc0 sc1
v_add_f32 v106,v18,v19
v_fma_f32 v107,v18,v18,s87
v_fma_f32 v107,v19,v19,v107
v_add_f32 v106,v106,v20
v_fma_f32 v107,v20,v20,v107
v_add_f32 v106,v106,v21
v_fma_f32 v107,v21,v21,v107
v_add_f32 v106,v106,v22
v_fma_f32 v107,v22,v22,v107
v_add_f32 v106,v106,v23
v_fma_f32 v107,v23,v23,v107
v_add_f32 v106,v106,v24
v_fma_f32 v107,v24,v24,v107
v_add_f32 v106,v106,v25
v_fma_f32 v107,v25,v25,v107
s_nop 1
v_permlane16_swap_b32 v106,v107
v_add_f32 v106,v106,v107
v_mov_b32 v107,v106
s_nop 1
v_permlane32_swap_b32 v106,v107
v_add_f32 v106,v106,v107
ds_write_b32 v12,v106 offset:0
L 0
s_barrier
R 118,13,0
L 0
v_add_f32 v118,v118,v119
v_add_f32 v120,v120,v121
v_add_f32 v118,v118,v120
v_mul_f32 v118,s86,v118
buffer_store_dword v118,v14,s[36:39],s76 offen offset:0 sc0 sc1
s_mov_b32 s72,s69
s_mov_b32 s69,0x7fff0000
s_mov_b32 s66,0x7fff0000
s_mov_b32 s76,0x7fff0000
s_branch Lg2_poll_%=
Lg2_fin0_%=:
v_lshlrev_b32 v106,16,v122
v_and_b32 v107,0xffff0000,v122
v_lshlrev_b32 v108,16,v123
v_and_b32 v109,0xffff0000,v123
v_lshlrev_b32 v110,16,v124
v_and_b32 v111,0xffff0000,v124
v_lshlrev_b32 v112,16,v125
v_and_b32 v113,0xffff0000,v125
v_fma_f32 v106,v106,v99,v98
v_fma_f32 v107,v107,v99,v98
v_fma_f32 v108,v108,v99,v98
v_fma_f32 v109,v109,v99,v98
v_fma_f32 v110,v110,v99,v98
v_fma_f32 v111,v111,v99,v98
v_fma_f32 v112,v112,v99,v98
v_fma_f32 v113,v113,v99,v98
v_add_f32 v18,v18,v106
v_add_f32 v19,v19,v107
v_add_f32 v20,v20,v108
v_add_f32 v21,v21,v109
v_add_f32 v22,v22,v110
v_add_f32 v23,v23,v111
v_add_f32 v24,v24,v112
v_add_f32 v25,v25,v113
v_cvt_pk_bf16_f32 v114,v18,v19
v_cvt_pk_bf16_f32 v115,v20,v21
v_cvt_pk_bf16_f32 v116,v22,v23
v_cvt_pk_bf16_f32 v117,v24,v25
buffer_store_dwordx4 v[114:117],v0,s[36:39],s66 offen offset:0 sc0 sc1
v_add_f32 v106,v18,v19
v_fma_f32 v107,v18,v18,s87
v_fma_f32 v107,v19,v19,v107
v_add_f32 v106,v106,v20
v_fma_f32 v107,v20,v20,v107
v_add_f32 v106,v106,v21
v_fma_f32 v107,v21,v21,v107
v_add_f32 v106,v106,v22
v_fma_f32 v107,v22,v22,v107
v_add_f32 v106,v106,v23
v_fma_f32 v107,v23,v23,v107
v_add_f32 v106,v106,v24
v_fma_f32 v107,v24,v24,v107
v_add_f32 v106,v106,v25
v_fma_f32 v107,v25,v25,v107
s_nop 1
v_permlane16_swap_b32 v106,v107
v_add_f32 v106,v106,v107
v_mov_b32 v107,v106
s_nop 1
v_permlane32_swap_b32 v106,v107
v_add_f32 v106,v106,v107
ds_write_b32 v12,v106 offset:0
L 0
s_barrier
R 118,13,0
L 0
v_add_f32 v118,v118,v119
v_add_f32 v120,v120,v121
v_add_f32 v118,v118,v120
v_mul_f32 v118,s86,v118
buffer_store_dword v118,v14,s[36:39],s76 offen offset:0 sc0 sc1
s_mov_b32 s72,s69
s_mov_b32 s69,0x7fff0000
s_mov_b32 s66,0x7fff0000
s_mov_b32 s76,0x7fff0000
s_branch Lg2_done_%=
Lg2_K1_%=:
Lg2_L1_%=:
v_mfma_f32_16x16x32_bf16 v[30:33],a[4:7],v[34:37],0
s_add_u32 m0,s59,3072
R 54,2,5120
v_lshlrev_b32 v106,16,v122
M 30,a[12:15],38
buffer_load_dword v8,s[36:39],s58 offen offset:0 sc1 lds
v_mfma_f32_16x16x32_bf16 v[26:29],a[0:3],v[34:37],0
s_mov_b32 s67,s65
R 58,2,6144
v_and_b32 v107,0xffff0000,v122
M 26,a[8:11],38
L 3
v_lshlrev_b32 v108,16,v123
v_and_b32 v109,0xffff0000,v123
)"
R"(M 26,a[16:19],42
s_mov_b32 s77,s75
R 62,2,7168
v_lshlrev_b32 v110,16,v124
M 26,a[24:27],46
s_add_u32 s65,s65,s62
v_and_b32 v111,0xffff0000,v124
v_lshlrev_b32 v112,16,v125
M 30,a[20:23],42
s_add_u32 s75,s75,s63
R 66,2,8192
v_and_b32 v113,0xffff0000,v125
v_fma_f32 v106,v106,v99,v98
M 30,a[28:31],46
L 3
v_fma_f32 v107,v107,v99,v98
v_fma_f32 v108,v108,v99,v98
M 30,a[36:39],50
R 70,2,9216
v_fma_f32 v109,v109,v99,v98
M 30,a[44:47],54
v_fma_f32 v110,v110,v99,v98
v_fma_f32 v111,v111,v99,v98
M 26,a[32:35],50
R 74,2,10240
v_fma_f32 v112,v112,v99,v98
M 26,a[40:43],54
L 3
v_fma_f32 v113,v113,v99,v98
v_add_f32 v18,v18,v106
M 26,a[48:51],58
R 78,2,11264
v_add_f32 v19,v19,v107
M 26,a[56:59],62
v_add_f32 v20,v20,v108
v_add_f32 v21,v21,v109
v_add_f32 v22,v22,v110
M 30,a[52:55],58
R 82,2,12288
v_add_f32 v23,v23,v111
M 30,a[60:63],62
L 3
v_add_f32 v24,v24,v112
v_add_f32 v25,v25,v113
M 30,a[68:71],66
R 86,2,13312
v_cvt_pk_bf16_f32 v114,v18,v19
M 30,a[76:79],70
buffer_load_dwordx4 v[126:129],v0,s[36:39],s74 offen offset:0 sc1
M 26,a[64:67],66
s_add_u32 s74,s74,s62
R 90,2,14336
v_cvt_pk_bf16_f32 v115,v20,v21
M 26,a[72:75],70
L 3
buffer_load_dwordx2 v[102:103],v11,s[36:39],s73 offen offset:256 sc1
v_cvt_pk_bf16_f32 v116,v22,v23
M 26,a[80:83],74
s_waitcnt vmcnt(2)
s_add_u32 s73,s73,s64
R 94,2,15360
M 26,a[88:91],78
s_barrier
ds_read_b32 v7,v6 offset:3072
v_cvt_pk_bf16_f32 v117,v24,v25
M 30,a[84:87],74
R 34,2,16384
v_add_f32 v106,v18,v19
M 30,a[92:95],78
L 4
buffer_store_dword v4,v3,s[36:39],s70 offen offset:0 sc0 sc1
M 30,a[100:103],82
s_mov_b32 s70,0x7fff0000
R 38,2,17408
v_fma_f32 v107,v18,v18,s87
M 30,a[108:111],86
buffer_store_dwordx4 v[114:117],v0,s[36:39],s66 offen offset:0 sc0 sc1
v_fma_f32 v107,v19,v19,v107
M 26,a[96:99],82
R 42,2,18432
v_add_f32 v106,v106,v20
v_fma_f32 v107,v20,v20,v107
M 26,a[104:107],86
L 4
L 3
v_cmp_eq_u32 vcc,s53,v7
M 26,a[112:115],90
R 46,2,19456
v_add_f32 v106,v106,v21
M 26,a[120:123],94
s_or_b64 vcc,vcc,s[94:95]
v_fma_f32 v107,v21,v21,v107
v_add_f32 v106,v106,v22
M 30,a[116:119],90
R 50,2,20480
v_fma_f32 v107,v22,v22,v107
M 30,a[124:127],94
s_cmp_eq_u32 vcc_lo,-1
L 3
v_add_f32 v106,v106,v23
s_cbranch_scc0 Lg2_X1_%=
M 30,a[132:135],34
s_and_b32 s78,s54,127
s_cmp_eq_u32 s78,s85
s_cselect_b64 s[40:41],-1,0
R 54,2,21504
M 30,a[140:143],38
s_add_u32 m0,s57,0
s_add_u32 s56,s55,0
s_add_u32 s58,s58,s61
v_cndmask_b32 v17,v15,v16,s[40:41]
M 26,a[128:131],34
s_add_u32 s54,s54,s46
R 58,2,22528
v_fma_f32 v107,v23,v23,v107
M 26,a[136:139],38
L 3
D 17,56,0
M 26,a[144:147],42
s_add_u32 s56,s55,256
R 62,2,23552
v_add_f32 v106,v106,v24
M 26,a[152:155],46
s_and_b32 s79,s54,127
v_fma_f32 v107,v24,v24,v107
v_add_f32 v106,v106,v25
M 30,a[148:151],42
s_cmp_eq_u32 s79,0
R 66,2,24576
v_fma_f32 v107,v25,v25,v107
M 30,a[156:159],46
L 3
D 17,56,1024
M 30,a[164:167],50
s_cselect_b32 s94,0x3300,0
s_add_u32 s56,s55,512
R 70,2,25600
M 30,a[172:175],54
s_cmp_eq_u32 s79,127
v_permlane16_swap_b32 v106,v107
v_add_f32 v106,v106,v107
M 26,a[160:163],50
s_cselect_b32 s94,0xcc00,s94
R 74,2,26624
v_mov_b32 v107,v106
M 26,a[168:171],54
L 3
D 17,56,2048
M 26,a[176:179],58
s_add_u32 s56,s55,768
R 78,2,27648
M 26,a[184:187],62
v_permlane32_swap_b32 v106,v107
v_add_f32 v106,v106,v107
M 30,a[180:183],58
R 82,2,28672
ds_write_b32 v12,v106 offset:0
M 30,a[188:191],62
L 4
D 17,56,3072
M 30,a[196:199],66
s_add_u32 m0,s59,1024
s_add_u32 s56,s55,20480
R 86,2,29696
M 30,a[204:207],70
buffer_load_dword v8,s[36:39],s58 offen offset:0 sc1 lds
M 26,a[192:195],66
s_add_u32 m0,s57,16384
R 90,2,30720
v_cndmask_b32 v17,v10,v9,s[40:41]
M 26,a[200:203],70
L 4
L 2
s_sub_u32 s57,s84,s57
M 26,a[208:211],74
s_waitcnt vmcnt(0)
R 94,2,31744
M 26,a[216:219],78
s_barrier
D 17,56,0
M 30,a[212:215],74
s_add_u32 s56,s55,20736
R 34,1,0
ds_read_b32 v7,v6 offset:1024
M 30,a[220:223],78
L 4
buffer_store_dword v4,v3,s[36:39],s70 offen offset:0 sc0 sc1
M 30,a[228:231],82
s_mov_b32 s70,s69
R 38,1,1024
R 118,13,0
M 30,a[236:239],86
D 17,56,1024
M 26,a[224:227],82
s_add_u32 s56,s55,20992
s_mov_b32 s69,s68
R 42,1,2048
M 26,a[232:235],86
L 5
D 17,56,2048
M 26,a[240:243],90
L 3
s_add_u32 s56,s55,21248
R 46,1,3072
v_cmp_eq_u32 vcc,s53,v7
M 26,a[248:251],94
D 17,56,3072
M 30,a[244:247],90
s_add_u32 s55,s55,s60
s_add_u32 s68,s68,s63
L 2
s_or_b64 vcc,vcc,s[94:95]
s_cmp_eq_u32 vcc_lo,-1
R 50,1,4096
v_add_f32 v118,v118,v119
v_add_f32 v120,v120,v121
v_add_f32 v118,v118,v120
v_mul_f32 v118,s86,v118
M 30,a[252:255],94
s_nop 0
buffer_store_dword v118,v14,s[36:39],s76 offen offset:0 sc0 sc1
buffer_store_dword v4,v3,s[36:39],s71 offen offset:0 sc0 sc1
buffer_store_dword v4,v3,s[36:39],s71 offen offset:0 sc0 sc1
s_cbranch_scc1 Lg2_F0_%=
s_branch Lg2_K0_%=
Lg2_X1_%=:
M 30,a[132:135],34
s_add_u32 m0,s59,1024
R 54,2,21504
v_fma_f32 v107,v23,v23,v107
M 30,a[140:143],38
buffer_load_dword v8,s[36:39],s58 offen offset:0 sc1 lds
M 26,a[128:131],34
R 58,2,22528
v_add_f32 v106,v106,v24
M 26,a[136:139],38
L 3
v_fma_f32 v107,v24,v24,v107
v_add_f32 v106,v106,v25
M 26,a[144:147],42
R 62,2,23552
v_fma_f32 v107,v25,v25,v107
M 26,a[152:155],46
M 30,a[148:151],42
R 66,2,24576
M 30,a[156:159],46
L 3
v_permlane16_swap_b32 v106,v107
v_add_f32 v106,v106,v107
M 30,a[164:167],50
R 70,2,25600
v_mov_b32 v107,v106
M 30,a[172:175],54
M 26,a[160:163],50
R 74,2,26624
M 26,a[168:171],54
L 3
v_permlane32_swap_b32 v106,v107
M 26,a[176:179],58
R 78,2,27648
v_add_f32 v106,v106,v107
M 26,a[184:187],62
ds_write_b32 v12,v106 offset:0
M 30,a[180:183],58
R 82,2,28672
M 30,a[188:191],62
L 4
M 30,a[196:199],66
R 86,2,29696
M 30,a[204:207],70
M 26,a[192:195],66
R 90,2,30720
M 26,a[200:203],70
L 4
L 3
M 26,a[208:211],74
s_waitcnt vmcnt(0)
R 94,2,31744
M 26,a[216:219],78
s_barrier
R 118,13,0
ds_read_b32 v7,v6 offset:1024
M 30,a[212:215],74
buffer_store_dword v4,v3,s[36:39],s70 offen offset:0 sc0 sc1
M 30,a[220:223],78
L 4
s_mov_b32 s70,s69
s_mov_b32 s69,s68
M 30,a[228:231],82
s_add_u32 s68,s68,s63
M 30,a[236:239],86
M 26,a[224:227],82
M 26,a[232:235],86
L 2
L 1
L 0
M 26,a[240:243],90
v_add_f32 v118,v118,v119
v_add_f32 v120,v120,v121
v_add_f32 v118,v118,v120
M 26,a[248:251],94
v_cmp_eq_u32 vcc,s53,v7
v_mul_f32 v118,s86,v118
M 30,a[244:247],90
buffer_store_dword v118,v14,s[36:39],s76 offen offset:0 sc0 sc1
M 30,a[252:255],94
s_nop 0
s_or_b64 vcc,vcc,s[94:95]
s_mov_b32 s80,0
s_cmp_eq_u32 vcc_lo,-1
s_cbranch_scc0 Lg2_xn1_%=
s_and_b32 s78,s54,127
s_cmp_eq_u32 s78,s85
s_cselect_b64 s[40:41],-1,0
v_cndmask_b32 v17,v15,v16,s[40:41]
s_add_u32 m0,s57,0
s_add_u32 s56,s55,0
D 17,56,0
v_lshlrev_b32 v106,16,v126
v_and_b32 v107,0xffff0000,v126
v_lshlrev_b32 v108,16,v127
v_and_b32 v109,0xffff0000,v127
v_lshlrev_b32 v110,16,v128
s_add_u32 s56,s55,256
D 17,56,1024
v_and_b32 v111,0xffff0000,v128
v_lshlrev_b32 v112,16,v129
v_and_b32 v113,0xffff0000,v129
v_fma_f32 v106,v106,v103,v102
v_fma_f32 v107,v107,v103,v102
s_add_u32 s56,s55,512
D 17,56,2048
v_fma_f32 v108,v108,v103,v102
v_fma_f32 v109,v109,v103,v102
v_fma_f32 v110,v110,v103,v102
v_fma_f32 v111,v111,v103,v102
v_fma_f32 v112,v112,v103,v102
s_add_u32 s56,s55,768
D 17,56,3072
v_fma_f32 v113,v113,v103,v102
v_add_f32 v26,v26,v106
v_add_f32 v27,v27,v107
v_add_f32 v28,v28,v108
v_add_f32 v29,v29,v109
s_add_u32 s58,s58,s61
v_cndmask_b32 v17,v10,v9,s[40:41]
s_add_u32 m0,s57,16384
s_add_u32 s56,s55,20480
D 17,56,0
v_add_f32 v30,v30,v110
v_add_f32 v31,v31,v111
v_add_f32 v32,v32,v112
v_add_f32 v33,v33,v113
v_cvt_pk_bf16_f32 v114,v26,v27
s_add_u32 s56,s55,20736
D 17,56,1024
v_cvt_pk_bf16_f32 v115,v28,v29
v_cvt_pk_bf16_f32 v116,v30,v31
v_cvt_pk_bf16_f32 v117,v32,v33
s_add_u32 s56,s55,20992
D 17,56,2048
s_add_u32 s56,s55,21248
D 17,56,3072
buffer_store_dwordx4 v[114:117],v0,s[36:39],s67 offen offset:0 sc0 sc1
v_add_f32 v106,v26,v27
v_fma_f32 v107,v26,v26,s87
s_add_u32 s55,s55,s60
s_sub_u32 s57,s84,s57
s_add_u32 s54,s54,s46
v_fma_f32 v107,v27,v27,v107
v_add_f32 v106,v106,v28
v_fma_f32 v107,v28,v28,v107
v_add_f32 v106,v106,v29
v_fma_f32 v107,v29,v29,v107
v_add_f32 v106,v106,v30
v_fma_f32 v107,v30,v30,v107
v_add_f32 v106,v106,v31
v_fma_f32 v107,v31,v31,v107
v_add_f32 v106,v106,v32
v_fma_f32 v107,v32,v32,v107
v_add_f32 v106,v106,v33
v_fma_f32 v107,v33,v33,v107
s_nop 1
v_permlane16_swap_b32 v106,v107
v_add_f32 v106,v106,v107
v_mov_b32 v107,v106
s_nop 1
v_permlane32_swap_b32 v106,v107
v_add_f32 v106,v106,v107
ds_write_b32 v12,v106 offset:2048
L 0
s_barrier
R 118,13,2048
L 0
v_add_f32 v118,v118,v119
v_add_f32 v120,v120,v121
v_add_f32 v118,v118,v120
v_mul_f32 v118,s86,v118
buffer_store_dword v118,v14,s[36:39],s77 offen offset:0 sc0 sc1
s_mov_b32 s72,s69
s_mov_b32 s69,0x7fff0000
s_mov_b32 s67,0x7fff0000
s_mov_b32 s77,0x7fff0000
s_branch Lg2_gox_%=
Lg2_xn1_%=:
s_cmp_ge_u32 s54,256
s_cbranch_scc1 Lg2_fin1_%=
s_add_u32 m0,s59,4096
s_nop 0
buffer_load_dword v8,s[36:39],s58 offen offset:0 sc1 lds
buffer_store_dword v4,v3,s[36:39],s71 offen offset:0 sc0 sc1
s_add_u32 m0,s59,5120
s_nop 0
buffer_load_dword v8,s[36:39],s58 offen offset:0 sc1 lds
buffer_store_dword v4,v3,s[36:39],s71 offen offset:0 sc0 sc1
s_mov_b32 s83,0
v_lshlrev_b32 v106,16,v126
v_and_b32 v107,0xffff0000,v126
v_lshlrev_b32 v108,16,v127
v_and_b32 v109,0xffff0000,v127
v_lshlrev_b32 v110,16,v128
v_and_b32 v111,0xffff0000,v128
v_lshlrev_b32 v112,16,v129
v_and_b32 v113,0xffff0000,v129
v_fma_f32 v106,v106,v103,v102
v_fma_f32 v107,v107,v103,v102
v_fma_f32 v108,v108,v103,v102
v_fma_f32 v109,v109,v103,v102
v_fma_f32 v110,v110,v103,v102
v_fma_f32 v111,v111,v103,v102
v_fma_f32 v112,v112,v103,v102
v_fma_f32 v113,v113,v103,v102
v_add_f32 v26,v26,v106
v_add_f32 v27,v27,v107
v_add_f32 v28,v28,v108
v_add_f32 v29,v29,v109
v_add_f32 v30,v30,v110
v_add_f32 v31,v31,v111
v_add_f32 v32,v32,v112
v_add_f32 v33,v33,v113
v_cvt_pk_bf16_f32 v114,v26,v27
v_cvt_pk_bf16_f32 v115,v28,v29
v_cvt_pk_bf16_f32 v116,v30,v31
v_cvt_pk_bf16_f32 v117,v32,v33
buffer_store_dwordx4 v[114:117],v0,s[36:39],s67 offen offset:0 sc0 sc1
v_add_f32 v106,v26,v27
v_fma_f32 v107,v26,v26,s87
v_fma_f32 v107,v27,v27,v107
v_add_f32 v106,v106,v28
v_fma_f32 v107,v28,v28,v107
v_add_f32 v106,v106,v29
v_fma_f32 v107,v29,v29,v107
v_add_f32 v106,v106,v30
v_fma_f32 v107,v30,v30,v107
v_add_f32 v106,v106,v31
v_fma_f32 v107,v31,v31,v107
v_add_f32 v106,v106,v32
v_fma_f32 v107,v32,v32,v107
v_add_f32 v106,v106,v33
v_fma_f32 v107,v33,v33,v107
s_nop 1
v_permlane16_swap_b32 v106,v107
v_add_f32 v106,v106,v107
v_mov_b32 v107,v106
s_nop 1
v_permlane32_swap_b32 v106,v107
v_add_f32 v106,v106,v107
ds_write_b32 v12,v106 offset:2048
L 0
s_barrier
R 118,13,2048
L 0
v_add_f32 v118,v118,v119
v_add_f32 v120,v120,v121
v_add_f32 v118,v118,v120
v_mul_f32 v118,s86,v118
buffer_store_dword v118,v14,s[36:39],s77 offen offset:0 sc0 sc1
s_mov_b32 s72,s69
s_mov_b32 s69,0x7fff0000
s_mov_b32 s67,0x7fff0000
s_mov_b32 s77,0x7fff0000
s_branch Lg2_poll_%=
Lg2_fin1_%=:
v_lshlrev_b32 v106,16,v126
v_and_b32 v107,0xffff0000,v126
v_lshlrev_b32 v108,16,v127
v_and_b32 v109,0xffff0000,v127
v_lshlrev_b32 v110,16,v128
v_and_b32 v111,0xffff0000,v128
v_lshlrev_b32 v112,16,v129
v_and_b32 v113,0xffff0000,v129
v_fma_f32 v106,v106,v103,v102
v_fma_f32 v107,v107,v103,v102
v_fma_f32 v108,v108,v103,v102
v_fma_f32 v109,v109,v103,v102
v_fma_f32 v110,v110,v103,v102
v_fma_f32 v111,v111,v103,v102
v_fma_f32 v112,v112,v103,v102
v_fma_f32 v113,v113,v103,v102
v_add_f32 v26,v26,v106
)"
R"(v_add_f32 v27,v27,v107
v_add_f32 v28,v28,v108
v_add_f32 v29,v29,v109
v_add_f32 v30,v30,v110
v_add_f32 v31,v31,v111
v_add_f32 v32,v32,v112
v_add_f32 v33,v33,v113
v_cvt_pk_bf16_f32 v114,v26,v27
v_cvt_pk_bf16_f32 v115,v28,v29
v_cvt_pk_bf16_f32 v116,v30,v31
v_cvt_pk_bf16_f32 v117,v32,v33
buffer_store_dwordx4 v[114:117],v0,s[36:39],s67 offen offset:0 sc0 sc1
v_add_f32 v106,v26,v27
v_fma_f32 v107,v26,v26,s87
v_fma_f32 v107,v27,v27,v107
v_add_f32 v106,v106,v28
v_fma_f32 v107,v28,v28,v107
v_add_f32 v106,v106,v29
v_fma_f32 v107,v29,v29,v107
v_add_f32 v106,v106,v30
v_fma_f32 v107,v30,v30,v107
v_add_f32 v106,v106,v31
v_fma_f32 v107,v31,v31,v107
v_add_f32 v106,v106,v32
v_fma_f32 v107,v32,v32,v107
v_add_f32 v106,v106,v33
v_fma_f32 v107,v33,v33,v107
s_nop 1
v_permlane16_swap_b32 v106,v107
v_add_f32 v106,v106,v107
v_mov_b32 v107,v106
s_nop 1
v_permlane32_swap_b32 v106,v107
v_add_f32 v106,v106,v107
ds_write_b32 v12,v106 offset:2048
L 0
s_barrier
R 118,13,2048
L 0
v_add_f32 v118,v118,v119
v_add_f32 v120,v120,v121
v_add_f32 v118,v118,v120
v_mul_f32 v118,s86,v118
buffer_store_dword v118,v14,s[36:39],s77 offen offset:0 sc0 sc1
s_mov_b32 s72,s69
s_mov_b32 s69,0x7fff0000
s_mov_b32 s67,0x7fff0000
s_mov_b32 s77,0x7fff0000
s_branch Lg2_done_%=
Lg2_done_%=:
s_waitcnt vmcnt(2)
s_barrier
buffer_store_dword v4,v3,s[36:39],s70 offen offset:0 sc0 sc1
s_waitcnt vmcnt(1)
s_barrier
buffer_store_dword v4,v3,s[36:39],s72 offen offset:0 sc0 sc1
s_waitcnt vmcnt(0) lgkmcnt(0)
s_barrier
Lg2_end_%=:
s_mov_b32 %0,s53
Lcode_g12_e:
.purgem M
.purgem R
.purgem T
.purgem D
.purgem L
)"
  : "=&s"(epOut) : "s"(ws), "s"(__builtin_amdgcn_readfirstlane(sWave)), "s"(__builtin_amdgcn_readfirstlane(sG0)), "s"(__builtin_amdgcn_readfirstlane(sStep)), "s"(__builtin_amdgcn_readfirstlane(sOutOff)), "s"(__builtin_amdgcn_readfirstlane(sFoutOff)), "s"(__builtin_amdgcn_readfirstlane(sAuxOff)), "s"(__builtin_amdgcn_readfirstlane(sWw)), "s"(__builtin_amdgcn_readfirstlane(sBuf)), "s"(__builtin_amdgcn_readfirstlane(sExp)), "v"(vL16), "v"(vFlag), "v"(vSnapOff), "v"(vSnapRd), "v"(vWd0), "v"(vWd1), "v"(vWr0), "v"(vWr1), "v"(vSp), "v"(vOutS), "v"(vAux0), "v"(vAux1), "v"(vAux2), "v"(vAux3), "v"(vAn), "v"(vAe), "s"(w12) : "v0", "v1", "v2", "v3", "v4", "v5", "v6", "v7", "v8", "v9", "v10", "v11", "v12", "v13", "v14", "v15", "v16", "v17", "v18", "v19", "v20", "v21", "v22", "v23", "v24", "v25", "v26", "v27", "v28", "v29", "v30", "v31", "v32", "v33", "v34", "v35", "v36", "v37", "v38", "v39", "v40", "v41", "v42", "v43", "v44", "v45", "v46", "v47", "v48", "v49", "v50", "v51", "v52", "v53", "v54", "v55", "v56", "v57", "v58", "v59", "v60", "v61", "v62", "v63", "v64", "v65", "v66", "v67", "v68", "v69", "v70", "v71", "v72", "v73", "v74", "v75", "v76", "v77", "v78", "v79", "v80", "v81", "v82", "v83", "v84", "v85", "v86", "v87", "v88", "v89", "v90", "v91", "v92", "v93", "v94", "v95", "v96", "v97", "v98", "v99", "v100", "v101", "v102", "v103", "v104", "v105", "v106", "v107", "v108", "v109", "v110", "v111", "v112", "v113", "v114", "v115", "v116", "v117", "v118", "v119", "v120", "v121", "v122", "v123", "v124", "v125", "v126", "v127", "v128", "v129", "v130", "v131", "v132", "v133", "v134", "v135", "v136", "v137", "v138", "v139", "v140", "v141", "v142", "v143", "v144", "v145", "v146", "v147", "v148", "v149", "v150", "v151", "v152", "v153", "v154", "v155", "v156", "v157", "v158", "v159", "v160", "v161", "v162", "v163", "v164", "v165", "v166", "v167", "v168", "v169", "v170", "v171", "v172", "v173", "v174", "v175", "v176", "v177", "v178", "v179", "v180", "v181", "v182", "v183", "v184", "v185", "v186", "v187", "v188", "v189", "v190", "v191", "v192", "v193", "v194", "v195", "v196", "v197", "v198", "v199", "v200", "v201", "v202", "v203", "v204", "v205", "v206", "v207", "v208", "v209", "v210", "v211", "v212", "v213", "v214", "v215", "v216", "v217", "v218", "v219", "v220", "v221", "v222", "v223", "v224", "v225", "v226", "v227", "v228", "v229", "v230", "v231", "a0", "a1", "a2", "a3", "a4", "a5", "a6", "a7", "a8", "a9", "a10", "a11", "a12", "a13", "a14", "a15", "a16", "a17", "a18", "a19", "a20", "a21", "a22", "a23", "a24", "a25", "a26", "a27", "a28", "a29", "a30", "a31", "a32", "a33", "a34", "a35", "a36", "a37", "a38", "a39", "a40", "a41", "a42", "a43", "a44", "a45", "a46", "a47", "a48", "a49", "a50", "a51", "a52", "a53", "a54", "a55", "a56", "a57", "a58", "a59", "a60", "a61", "a62", "a63", "a64", "a65", "a66", "a67", "a68", "a69", "a70", "a71", "a72", "a73", "a74", "a75", "a76", "a77", "a78", "a79", "a80", "a81", "a82", "a83", "a84", "a85", "a86", "a87", "a88", "a89", "a90", "a91", "a92", "a93", "a94", "a95", "a96", "a97", "a98", "a99", "a100", "a101", "a102", "a103", "a104", "a105", "a106", "a107", "a108", "a109", "a110", "a111", "a112", "a113", "a114", "a115", "a116", "a117", "a118", "a119", "a120", "a121", "a122", "a123", "a124", "a125", "a126", "a127", "a128", "a129", "a130", "a131", "a132", "a133", "a134", "a135", "a136", "a137", "a138", "a139", "a140", "a141", "a142", "a143", "a144", "a145", "a146", "a147", "a148", "a149", "a150", "a151", "a152", "a153", "a154", "a155", "a156", "a157", "a158", "a159", "a160", "a161", "a162", "a163", "a164", "a165", "a166", "a167", "a168", "a169", "a170", "a171", "a172", "a173", "a174", "a175", "a176", "a177", "a178", "a179", "a180", "a181", "a182", "a183", "a184", "a185", "a186", "a187", "a188", "a189", "a190", "a191", "a192", "a193", "a194", "a195", "a196", "a197", "a198", "a199", "a200", "a201", "a202", "a203", "a204", "a205", "a206", "a207", "a208", "a209", "a210", "a211", "a212", "a213", "a214", "a215", "a216", "a217", "a218", "a219", "a220", "a221", "a222", "a223", "a224", "a225", "a226", "a227", "a228", "a229", "a230", "a231", "a232", "a233", "a234", "a235", "a236", "a237", "a238", "a239", "a240", "a241", "a242", "a243", "a244", "a245", "a246", "a247", "a248", "a249", "a250", "a251", "a252", "a253", "a254", "a255", "s36", "s37", "s38", "s39", "s40", "s41", "s42", "s43", "s44", "s45", "s46", "s47", "s48", "s49", "s50", "s51", "s52", "s53", "s54", "s55", "s56", "s57", "s58", "s59", "s60", "s61", "s62", "s63", "s64", "s65", "s66", "s67", "s68", "s69", "s70", "s71", "s72", "s73", "s74", "s75", "s76", "s77", "s78", "s79", "s80", "s81", "s82", "s83", "s84", "s85", "s86", "s87", "s88", "s89", "s90", "s91", "s92", "s93", "s94", "s95", "s96", "s97", "vcc", "scc", "memory");
    ep = epOut;
    if (kind == 1 && 5 + RHELP + (rep >> 1) < NR4) { kind = 4; kh = rep & 1; rep = 5 + RHELP + (rep >> 1); }
  }
  if (kind == 4) {
    const unsigned sStep = rep < 5 ? 5 : rep == 5 ? 6 : NR4;
    COMMON(10, (kh * 9 + fj) * 128, 2560, 143360, 2048, 448, 0);
    const int cs = slice;
    const unsigned sOutOff = (kh ? OFF_P1 : OFF_P0) + (cs * 4 + wave) * 1024, sFoutOff = OFF_FP + (kh * 8 + cs) * 128;
    const unsigned sAuxOff = (kh ? OFF_XP : OFF_X1) + (cs * 4 + wave) * 1024, sWw = (kh * 1792 + 16 * wave) * 2048 + cs * 256, sBuf = kh * 57344;
    const int zu = 108 + (rep + 2) % 5;
    const unsigned vSp = lg * 2048 + l15 * 4, vSn = rep >= 5 ? OOB : !ALLZ ? (zu * 16 + wave * 4 + lg) * 2048 + (cs * 2 + kh) * 128 + l15 * 8 :
                         ((rep + 5 * (wave >> 1)) * 16 + (wave & 1) * 8 + (ln >> 3)) * 2048 + (cs * 2 + kh) * 128 + (ln & 7) * 16;
    (void)vWr2;
    const unsigned sExp = __builtin_amdgcn_readfirstlane(ep);
    asm volatile(
R"(.macro M d,w,x
v_mfma_f32_16x16x32_bf16 v[\d:\d+3], \w, v[\x:\x+3], v[\d:\d+3]
.endm
.macro R d,a,o
ds_read_b128 v[\d:\d+3], v\a offset:\o
.endm
.macro T d,a,o
ds_read_b64_tr_b16 \d, v\a offset:\o
.endm
.macro D a,s,o
buffer_load_dwordx4 v\a, s[36:39], s\s offen offset:\o sc1 lds
.endm
.macro L c
s_waitcnt lgkmcnt(\c)
.endm
Lcode_g4_b:
s_mov_b64 s[36:37],%0
s_mov_b32 s38,111243392
s_mov_b32 s39,0x00020000
s_mov_b32 s44,%1
s_mov_b32 s45,%2
s_mov_b32 s46,%3
s_mov_b32 s47,%4
s_mov_b32 s48,%5
s_mov_b32 s49,%6
s_mov_b32 s50,%7
s_mov_b32 s51,%8
s_mov_b32 s93,%9
s_mov_b32 s53,%10
v_mov_b32 v0,%11
v_mov_b32 v3,%12
v_mov_b32 v5,%13
v_mov_b32 v6,%14
v_mov_b32 v10,%15
v_mov_b32 v11,%16
v_mov_b32 v12,%17
v_mov_b32 v13,%18
v_mov_b32 v9,%19
v_mov_b32 v7,%20
s_mov_b64 s[40:41],%21
s_mov_b32 s42,7340032
s_mov_b32 s43,0x00020000
s_lshl_b32 s57,s44,11
s_cmp_eq_u32 s45,175
s_cbranch_scc1 Lg4_pre_%=
s_add_u32 s55,s50,0
s_add_u32 m0,s57,40960
s_nop 0
buffer_load_dwordx4 v10,s[40:43],s55 offen offset:0  lds
s_add_u32 m0,s57,41984
s_nop 0
buffer_load_dwordx4 v11,s[40:43],s55 offen offset:0  lds
s_add_u32 m0,s57,49024
s_nop 0
buffer_load_dwordx4 v10,s[40:43],s55 offen offset:128  lds
s_add_u32 m0,s57,50048
s_nop 0
buffer_load_dwordx4 v11,s[40:43],s55 offen offset:128  lds
s_add_u32 s55,s50,131072
s_add_u32 m0,s57,57344
s_nop 0
buffer_load_dwordx4 v10,s[40:43],s55 offen offset:0  lds
s_add_u32 m0,s57,58368
s_nop 0
buffer_load_dwordx4 v11,s[40:43],s55 offen offset:0  lds
s_add_u32 m0,s57,65408
s_nop 0
buffer_load_dwordx4 v10,s[40:43],s55 offen offset:128  lds
s_add_u32 m0,s57,66432
s_nop 0
buffer_load_dwordx4 v11,s[40:43],s55 offen offset:128  lds
s_add_u32 s55,s50,262144
s_add_u32 m0,s57,73728
s_nop 0
buffer_load_dwordx4 v10,s[40:43],s55 offen offset:0  lds
s_add_u32 m0,s57,74752
s_nop 0
buffer_load_dwordx4 v11,s[40:43],s55 offen offset:0  lds
s_add_u32 m0,s57,81792
s_nop 0
buffer_load_dwordx4 v10,s[40:43],s55 offen offset:128  lds
s_add_u32 m0,s57,82816
s_nop 0
buffer_load_dwordx4 v11,s[40:43],s55 offen offset:128  lds
s_add_u32 s55,s50,393216
s_add_u32 m0,s57,90112
s_nop 0
buffer_load_dwordx4 v10,s[40:43],s55 offen offset:0  lds
s_add_u32 m0,s57,91136
s_nop 0
buffer_load_dwordx4 v11,s[40:43],s55 offen offset:0  lds
s_add_u32 m0,s57,98176
s_nop 0
buffer_load_dwordx4 v10,s[40:43],s55 offen offset:128  lds
s_add_u32 m0,s57,99200
s_nop 0
buffer_load_dwordx4 v11,s[40:43],s55 offen offset:128  lds
s_add_u32 s55,s50,524288
s_add_u32 m0,s57,106496
s_nop 0
buffer_load_dwordx4 v10,s[40:43],s55 offen offset:0  lds
s_add_u32 m0,s57,107520
s_nop 0
buffer_load_dwordx4 v11,s[40:43],s55 offen offset:0  lds
s_add_u32 m0,s57,114560
s_nop 0
buffer_load_dwordx4 v10,s[40:43],s55 offen offset:128  lds
s_add_u32 m0,s57,115584
s_nop 0
buffer_load_dwordx4 v11,s[40:43],s55 offen offset:128  lds
s_add_u32 s55,s50,655360
s_add_u32 m0,s57,122880
s_nop 0
buffer_load_dwordx4 v10,s[40:43],s55 offen offset:0  lds
s_add_u32 m0,s57,123904
s_nop 0
buffer_load_dwordx4 v11,s[40:43],s55 offen offset:0  lds
s_add_u32 m0,s57,130944
s_nop 0
buffer_load_dwordx4 v10,s[40:43],s55 offen offset:128  lds
s_add_u32 m0,s57,131968
s_nop 0
buffer_load_dwordx4 v11,s[40:43],s55 offen offset:128  lds
s_waitcnt vmcnt(20) lgkmcnt(0)
s_barrier
s_add_u32 s55,s50,786432
s_add_u32 m0,s57,139264
s_nop 0
buffer_load_dwordx4 v10,s[40:43],s55 offen offset:0  lds
s_add_u32 m0,s57,140288
s_nop 0
buffer_load_dwordx4 v11,s[40:43],s55 offen offset:0  lds
s_add_u32 m0,s57,147328
s_nop 0
buffer_load_dwordx4 v10,s[40:43],s55 offen offset:128  lds
s_add_u32 m0,s57,148352
s_nop 0
buffer_load_dwordx4 v11,s[40:43],s55 offen offset:128  lds
v_add_u32 v18,40960,v12
v_add_u32 v19,40960,v13
T a[0:1],18,0
T a[2:3],18,512
T a[4:5],19,0
T a[6:7],19,512
T a[8:9],18,4096
T a[10:11],18,4608
T a[12:13],19,4096
T a[14:15],19,4608
s_waitcnt vmcnt(20) lgkmcnt(0)
s_barrier
s_add_u32 s55,s50,917504
s_add_u32 m0,s57,40960
s_nop 0
buffer_load_dwordx4 v10,s[40:43],s55 offen offset:0  lds
s_add_u32 m0,s57,41984
s_nop 0
buffer_load_dwordx4 v11,s[40:43],s55 offen offset:0  lds
s_add_u32 m0,s57,49024
s_nop 0
buffer_load_dwordx4 v10,s[40:43],s55 offen offset:128  lds
s_add_u32 m0,s57,50048
s_nop 0
buffer_load_dwordx4 v11,s[40:43],s55 offen offset:128  lds
v_add_u32 v18,57344,v12
v_add_u32 v19,57344,v13
T a[16:17],18,0
T a[18:19],18,512
T a[20:21],19,0
T a[22:23],19,512
T a[24:25],18,4096
T a[26:27],18,4608
T a[28:29],19,4096
T a[30:31],19,4608
s_waitcnt vmcnt(20) lgkmcnt(0)
s_barrier
s_add_u32 s55,s50,1048576
s_add_u32 m0,s57,57344
s_nop 0
buffer_load_dwordx4 v10,s[40:43],s55 offen offset:0  lds
s_add_u32 m0,s57,58368
s_nop 0
buffer_load_dwordx4 v11,s[40:43],s55 offen offset:0  lds
s_add_u32 m0,s57,65408
s_nop 0
buffer_load_dwordx4 v10,s[40:43],s55 offen offset:128  lds
s_add_u32 m0,s57,66432
s_nop 0
buffer_load_dwordx4 v11,s[40:43],s55 offen offset:128  lds
v_add_u32 v18,73728,v12
v_add_u32 v19,73728,v13
T a[32:33],18,0
T a[34:35],18,512
T a[36:37],19,0
T a[38:39],19,512
T a[40:41],18,4096
T a[42:43],18,4608
T a[44:45],19,4096
T a[46:47],19,4608
s_waitcnt vmcnt(20) lgkmcnt(0)
s_barrier
s_add_u32 s55,s50,1179648
s_add_u32 m0,s57,73728
s_nop 0
buffer_load_dwordx4 v10,s[40:43],s55 offen offset:0  lds
s_add_u32 m0,s57,74752
s_nop 0
buffer_load_dwordx4 v11,s[40:43],s55 offen offset:0  lds
s_add_u32 m0,s57,81792
s_nop 0
buffer_load_dwordx4 v10,s[40:43],s55 offen offset:128  lds
s_add_u32 m0,s57,82816
s_nop 0
buffer_load_dwordx4 v11,s[40:43],s55 offen offset:128  lds
v_add_u32 v18,90112,v12
v_add_u32 v19,90112,v13
T a[48:49],18,0
T a[50:51],18,512
T a[52:53],19,0
T a[54:55],19,512
T a[56:57],18,4096
T a[58:59],18,4608
T a[60:61],19,4096
T a[62:63],19,4608
s_waitcnt vmcnt(20) lgkmcnt(0)
s_barrier
s_add_u32 s55,s50,1310720
s_add_u32 m0,s57,90112
s_nop 0
buffer_load_dwordx4 v10,s[40:43],s55 offen offset:0  lds
s_add_u32 m0,s57,91136
s_nop 0
buffer_load_dwordx4 v11,s[40:43],s55 offen offset:0  lds
s_add_u32 m0,s57,98176
s_nop 0
buffer_load_dwordx4 v10,s[40:43],s55 offen offset:128  lds
s_add_u32 m0,s57,99200
s_nop 0
buffer_load_dwordx4 v11,s[40:43],s55 offen offset:128  lds
v_add_u32 v18,106496,v12
v_add_u32 v19,106496,v13
T a[64:65],18,0
T a[66:67],18,512
T a[68:69],19,0
T a[70:71],19,512
T a[72:73],18,4096
T a[74:75],18,4608
T a[76:77],19,4096
T a[78:79],19,4608
s_waitcnt vmcnt(20) lgkmcnt(0)
s_barrier
s_add_u32 s55,s50,1441792
s_add_u32 m0,s57,106496
s_nop 0
buffer_load_dwordx4 v10,s[40:43],s55 offen offset:0  lds
s_add_u32 m0,s57,107520
s_nop 0
buffer_load_dwordx4 v11,s[40:43],s55 offen offset:0  lds
s_add_u32 m0,s57,114560
s_nop 0
buffer_load_dwordx4 v10,s[40:43],s55 offen offset:128  lds
s_add_u32 m0,s57,115584
s_nop 0
buffer_load_dwordx4 v11,s[40:43],s55 offen offset:128  lds
v_add_u32 v18,122880,v12
v_add_u32 v19,122880,v13
T a[80:81],18,0
T a[82:83],18,512
T a[84:85],19,0
T a[86:87],19,512
T a[88:89],18,4096
T a[90:91],18,4608
T a[92:93],19,4096
T a[94:95],19,4608
s_waitcnt vmcnt(20) lgkmcnt(0)
s_barrier
s_add_u32 s55,s50,1572864
s_add_u32 m0,s57,122880
s_nop 0
buffer_load_dwordx4 v10,s[40:43],s55 offen offset:0  lds
s_add_u32 m0,s57,123904
s_nop 0
buffer_load_dwordx4 v11,s[40:43],s55 offen offset:0  lds
s_add_u32 m0,s57,130944
s_nop 0
buffer_load_dwordx4 v10,s[40:43],s55 offen offset:128  lds
s_add_u32 m0,s57,131968
s_nop 0
buffer_load_dwordx4 v11,s[40:43],s55 offen offset:128  lds
v_add_u32 v18,139264,v12
v_add_u32 v19,139264,v13
T a[96:97],18,0
T a[98:99],18,512
T a[100:101],19,0
T a[102:103],19,512
T a[104:105],18,4096
T a[106:107],18,4608
T a[108:109],19,4096
T a[110:111],19,4608
s_waitcnt vmcnt(20) lgkmcnt(0)
s_barrier
s_add_u32 s55,s50,1703936
s_add_u32 m0,s57,139264
s_nop 0
buffer_load_dwordx4 v10,s[40:43],s55 offen offset:0  lds
s_add_u32 m0,s57,140288
s_nop 0
buffer_load_dwordx4 v11,s[40:43],s55 offen offset:0  lds
s_add_u32 m0,s57,147328
s_nop 0
buffer_load_dwordx4 v10,s[40:43],s55 offen offset:128  lds
s_add_u32 m0,s57,148352
s_nop 0
buffer_load_dwordx4 v11,s[40:43],s55 offen offset:128  lds
v_add_u32 v18,40960,v12
v_add_u32 v19,40960,v13
T a[112:113],18,0
T a[114:115],18,512
T a[116:117],19,0
T a[118:119],19,512
T a[120:121],18,4096
T a[122:123],18,4608
T a[124:125],19,4096
T a[126:127],19,4608
s_waitcnt vmcnt(20) lgkmcnt(0)
s_barrier
s_add_u32 s55,s50,1835008
s_add_u32 m0,s57,40960
s_nop 0
buffer_load_dwordx4 v10,s[40:43],s55 offen offset:0  lds
s_add_u32 m0,s57,41984
s_nop 0
buffer_load_dwordx4 v11,s[40:43],s55 offen offset:0  lds
s_add_u32 m0,s57,49024
s_nop 0
buffer_load_dwordx4 v10,s[40:43],s55 offen offset:128  lds
s_add_u32 m0,s57,50048
s_nop 0
buffer_load_dwordx4 v11,s[40:43],s55 offen offset:128  lds
v_add_u32 v18,57344,v12
v_add_u32 v19,57344,v13
T a[128:129],18,0
T a[130:131],18,512
T a[132:133],19,0
T a[134:135],19,512
T a[136:137],18,4096
T a[138:139],18,4608
T a[140:141],19,4096
T a[142:143],19,4608
s_waitcnt vmcnt(20) lgkmcnt(0)
s_barrier
s_add_u32 s55,s50,1966080
s_add_u32 m0,s57,57344
s_nop 0
buffer_load_dwordx4 v10,s[40:43],s55 offen offset:0  lds
s_add_u32 m0,s57,58368
s_nop 0
buffer_load_dwordx4 v11,s[40:43],s55 offen offset:0  lds
s_add_u32 m0,s57,65408
s_nop 0
buffer_load_dwordx4 v10,s[40:43],s55 offen offset:128  lds
s_add_u32 m0,s57,66432
s_nop 0
buffer_load_dwordx4 v11,s[40:43],s55 offen offset:128  lds
v_add_u32 v18,73728,v12
v_add_u32 v19,73728,v13
T a[144:145],18,0
T a[146:147],18,512
T a[148:149],19,0
T a[150:151],19,512
T a[152:153],18,4096
T a[154:155],18,4608
T a[156:157],19,4096
T a[158:159],19,4608
s_waitcnt vmcnt(20) lgkmcnt(0)
s_barrier
s_add_u32 s55,s50,2097152
s_add_u32 m0,s57,73728
s_nop 0
buffer_load_dwordx4 v10,s[40:43],s55 offen offset:0  lds
s_add_u32 m0,s57,74752
s_nop 0
buffer_load_dwordx4 v11,s[40:43],s55 offen offset:0  lds
s_add_u32 m0,s57,81792
s_nop 0
buffer_load_dwordx4 v10,s[40:43],s55 offen offset:128  lds
s_add_u32 m0,s57,82816
s_nop 0
buffer_load_dwordx4 v11,s[40:43],s55 offen offset:128  lds
v_add_u32 v18,90112,v12
v_add_u32 v19,90112,v13
T a[160:161],18,0
T a[162:163],18,512
T a[164:165],19,0
T a[166:167],19,512
T a[168:169],18,4096
T a[170:171],18,4608
T a[172:173],19,4096
T a[174:175],19,4608
s_waitcnt vmcnt(20) lgkmcnt(0)
s_barrier
s_add_u32 s55,s50,2228224
s_add_u32 m0,s57,90112
s_nop 0
buffer_load_dwordx4 v10,s[40:43],s55 offen offset:0  lds
s_add_u32 m0,s57,91136
s_nop 0
buffer_load_dwordx4 v11,s[40:43],s55 offen offset:0  lds
s_add_u32 m0,s57,98176
s_nop 0
buffer_load_dwordx4 v10,s[40:43],s55 offen offset:128  lds
s_add_u32 m0,s57,99200
s_nop 0
buffer_load_dwordx4 v11,s[40:43],s55 offen offset:128  lds
v_add_u32 v18,106496,v12
v_add_u32 v19,106496,v13
T a[176:177],18,0
T a[178:179],18,512
T a[180:181],19,0
T a[182:183],19,512
T a[184:185],18,4096
T a[186:187],18,4608
T a[188:189],19,4096
T a[190:191],19,4608
s_waitcnt vmcnt(20) lgkmcnt(0)
s_barrier
s_add_u32 s55,s50,2359296
s_add_u32 m0,s57,106496
s_nop 0
buffer_load_dwordx4 v10,s[40:43],s55 offen offset:0  lds
s_add_u32 m0,s57,107520
s_nop 0
buffer_load_dwordx4 v11,s[40:43],s55 offen offset:0  lds
s_add_u32 m0,s57,114560
s_nop 0
buffer_load_dwordx4 v10,s[40:43],s55 offen offset:128  lds
s_add_u32 m0,s57,115584
s_nop 0
buffer_load_dwordx4 v11,s[40:43],s55 offen offset:128  lds
v_add_u32 v18,122880,v12
v_add_u32 v19,122880,v13
T a[192:193],18,0
T a[194:195],18,512
T a[196:197],19,0
T a[198:199],19,512
T a[200:201],18,4096
T a[202:203],18,4608
T a[204:205],19,4096
T a[206:207],19,4608
s_waitcnt vmcnt(20) lgkmcnt(0)
s_barrier
s_add_u32 s55,s50,2490368
s_add_u32 m0,s57,122880
s_nop 0
buffer_load_dwordx4 v10,s[40:43],s55 offen offset:0  lds
s_add_u32 m0,s57,123904
s_nop 0
buffer_load_dwordx4 v11,s[40:43],s55 offen offset:0  lds
s_add_u32 m0,s57,130944
s_nop 0
buffer_load_dwordx4 v10,s[40:43],s55 offen offset:128  lds
s_add_u32 m0,s57,131968
s_nop 0
buffer_load_dwordx4 v11,s[40:43],s55 offen offset:128  lds
v_add_u32 v18,139264,v12
v_add_u32 v19,139264,v13
T a[208:209],18,0
T a[210:211],18,512
T a[212:213],19,0
T a[214:215],19,512
T a[216:217],18,4096
T a[218:219],18,4608
T a[220:221],19,4096
T a[222:223],19,4608
s_waitcnt vmcnt(20) lgkmcnt(0)
s_barrier
s_add_u32 s55,s50,2621440
s_add_u32 m0,s57,139264
s_nop 0
buffer_load_dwordx4 v10,s[40:43],s55 offen offset:0  lds
s_add_u32 m0,s57,140288
s_nop 0
buffer_load_dwordx4 v11,s[40:43],s55 offen offset:0  lds
s_add_u32 m0,s57,147328
s_nop 0
buffer_load_dwordx4 v10,s[40:43],s55 offen offset:128  lds
s_add_u32 m0,s57,148352
s_nop 0
buffer_load_dwordx4 v11,s[40:43],s55 offen offset:128  lds
v_add_u32 v18,40960,v12
v_add_u32 v19,40960,v13
T a[224:225],18,0
T a[226:227],18,512
T a[228:229],19,0
T a[230:231],19,512
T a[232:233],18,4096
T a[234:235],18,4608
T a[236:237],19,4096
T a[238:239],19,4608
s_waitcnt vmcnt(20) lgkmcnt(0)
s_barrier
)"
R"(s_add_u32 s55,s50,2752512
s_add_u32 m0,s57,40960
s_nop 0
buffer_load_dwordx4 v10,s[40:43],s55 offen offset:0  lds
s_add_u32 m0,s57,41984
s_nop 0
buffer_load_dwordx4 v11,s[40:43],s55 offen offset:0  lds
s_add_u32 m0,s57,49024
s_nop 0
buffer_load_dwordx4 v10,s[40:43],s55 offen offset:128  lds
s_add_u32 m0,s57,50048
s_nop 0
buffer_load_dwordx4 v11,s[40:43],s55 offen offset:128  lds
v_add_u32 v18,57344,v12
v_add_u32 v19,57344,v13
T a[240:241],18,0
T a[242:243],18,512
T a[244:245],19,0
T a[246:247],19,512
T a[248:249],18,4096
T a[250:251],18,4608
T a[252:253],19,4096
T a[254:255],19,4608
Lg4_pre_%=:
s_waitcnt vmcnt(20) lgkmcnt(0)
s_barrier
s_add_u32 s55,s50,2883584
s_add_u32 m0,s57,57344
s_nop 0
buffer_load_dwordx4 v10,s[40:43],s55 offen offset:0  lds
s_add_u32 m0,s57,58368
s_nop 0
buffer_load_dwordx4 v11,s[40:43],s55 offen offset:0  lds
s_add_u32 m0,s57,65408
s_nop 0
buffer_load_dwordx4 v10,s[40:43],s55 offen offset:128  lds
s_add_u32 m0,s57,66432
s_nop 0
buffer_load_dwordx4 v11,s[40:43],s55 offen offset:128  lds
v_add_u32 v18,73728,v12
v_add_u32 v19,73728,v13
T v[64:65],18,0
T v[66:67],18,512
T v[68:69],19,0
T v[70:71],19,512
T v[72:73],18,4096
T v[74:75],18,4608
T v[76:77],19,4096
T v[78:79],19,4608
s_waitcnt vmcnt(20) lgkmcnt(0)
s_barrier
s_add_u32 s55,s50,3014656
s_add_u32 m0,s57,73728
s_nop 0
buffer_load_dwordx4 v10,s[40:43],s55 offen offset:0  lds
s_add_u32 m0,s57,74752
s_nop 0
buffer_load_dwordx4 v11,s[40:43],s55 offen offset:0  lds
s_add_u32 m0,s57,81792
s_nop 0
buffer_load_dwordx4 v10,s[40:43],s55 offen offset:128  lds
s_add_u32 m0,s57,82816
s_nop 0
buffer_load_dwordx4 v11,s[40:43],s55 offen offset:128  lds
v_add_u32 v18,90112,v12
v_add_u32 v19,90112,v13
T v[80:81],18,0
T v[82:83],18,512
T v[84:85],19,0
T v[86:87],19,512
T v[88:89],18,4096
T v[90:91],18,4608
T v[92:93],19,4096
T v[94:95],19,4608
s_waitcnt vmcnt(20) lgkmcnt(0)
s_barrier
s_add_u32 s55,s50,3145728
s_add_u32 m0,s57,90112
s_nop 0
buffer_load_dwordx4 v10,s[40:43],s55 offen offset:0  lds
s_add_u32 m0,s57,91136
s_nop 0
buffer_load_dwordx4 v11,s[40:43],s55 offen offset:0  lds
s_add_u32 m0,s57,98176
s_nop 0
buffer_load_dwordx4 v10,s[40:43],s55 offen offset:128  lds
s_add_u32 m0,s57,99200
s_nop 0
buffer_load_dwordx4 v11,s[40:43],s55 offen offset:128  lds
v_add_u32 v18,106496,v12
v_add_u32 v19,106496,v13
T v[96:97],18,0
T v[98:99],18,512
T v[100:101],19,0
T v[102:103],19,512
T v[104:105],18,4096
T v[106:107],18,4608
T v[108:109],19,4096
T v[110:111],19,4608
s_waitcnt vmcnt(20) lgkmcnt(0)
s_barrier
s_add_u32 s55,s50,3276800
s_add_u32 m0,s57,106496
s_nop 0
buffer_load_dwordx4 v10,s[40:43],s55 offen offset:0  lds
s_add_u32 m0,s57,107520
s_nop 0
buffer_load_dwordx4 v11,s[40:43],s55 offen offset:0  lds
s_add_u32 m0,s57,114560
s_nop 0
buffer_load_dwordx4 v10,s[40:43],s55 offen offset:128  lds
s_add_u32 m0,s57,115584
s_nop 0
buffer_load_dwordx4 v11,s[40:43],s55 offen offset:128  lds
v_add_u32 v18,122880,v12
v_add_u32 v19,122880,v13
T v[112:113],18,0
T v[114:115],18,512
T v[116:117],19,0
T v[118:119],19,512
T v[120:121],18,4096
T v[122:123],18,4608
T v[124:125],19,4096
T v[126:127],19,4608
s_waitcnt vmcnt(20) lgkmcnt(0)
s_barrier
s_add_u32 s55,s50,3407872
s_add_u32 m0,s57,122880
s_nop 0
buffer_load_dwordx4 v10,s[40:43],s55 offen offset:0  lds
s_add_u32 m0,s57,123904
s_nop 0
buffer_load_dwordx4 v11,s[40:43],s55 offen offset:0  lds
s_add_u32 m0,s57,130944
s_nop 0
buffer_load_dwordx4 v10,s[40:43],s55 offen offset:128  lds
s_add_u32 m0,s57,131968
s_nop 0
buffer_load_dwordx4 v11,s[40:43],s55 offen offset:128  lds
v_add_u32 v18,139264,v12
v_add_u32 v19,139264,v13
T v[128:129],18,0
T v[130:131],18,512
T v[132:133],19,0
T v[134:135],19,512
T v[136:137],18,4096
T v[138:139],18,4608
T v[140:141],19,4096
T v[142:143],19,4608
s_waitcnt vmcnt(20) lgkmcnt(0)
s_barrier
s_add_u32 s55,s50,3538944
s_add_u32 m0,s57,139264
s_nop 0
buffer_load_dwordx4 v10,s[40:43],s55 offen offset:0  lds
s_add_u32 m0,s57,140288
s_nop 0
buffer_load_dwordx4 v11,s[40:43],s55 offen offset:0  lds
s_add_u32 m0,s57,147328
s_nop 0
buffer_load_dwordx4 v10,s[40:43],s55 offen offset:128  lds
s_add_u32 m0,s57,148352
s_nop 0
buffer_load_dwordx4 v11,s[40:43],s55 offen offset:128  lds
v_add_u32 v18,40960,v12
v_add_u32 v19,40960,v13
T v[144:145],18,0
T v[146:147],18,512
T v[148:149],19,0
T v[150:151],19,512
T v[152:153],18,4096
T v[154:155],18,4608
T v[156:157],19,4096
T v[158:159],19,4608
s_waitcnt vmcnt(20) lgkmcnt(0)
s_barrier
v_add_u32 v18,57344,v12
v_add_u32 v19,57344,v13
T v[160:161],18,0
T v[162:163],18,512
T v[164:165],19,0
T v[166:167],19,512
T v[168:169],18,4096
T v[170:171],18,4608
T v[172:173],19,4096
T v[174:175],19,4608
s_waitcnt vmcnt(16) lgkmcnt(0)
s_barrier
v_add_u32 v18,73728,v12
v_add_u32 v19,73728,v13
T v[176:177],18,0
T v[178:179],18,512
T v[180:181],19,0
T v[182:183],19,512
T v[184:185],18,4096
T v[186:187],18,4608
T v[188:189],19,4096
T v[190:191],19,4608
s_waitcnt vmcnt(12) lgkmcnt(0)
s_barrier
v_add_u32 v18,90112,v12
v_add_u32 v19,90112,v13
T v[192:193],18,0
T v[194:195],18,512
T v[196:197],19,0
T v[198:199],19,512
T v[200:201],18,4096
T v[202:203],18,4608
T v[204:205],19,4096
T v[206:207],19,4608
s_waitcnt vmcnt(8) lgkmcnt(0)
s_barrier
v_add_u32 v18,106496,v12
v_add_u32 v19,106496,v13
T v[208:209],18,0
T v[210:211],18,512
T v[212:213],19,0
T v[214:215],19,512
T v[216:217],18,4096
T v[218:219],18,4608
T v[220:221],19,4096
T v[222:223],19,4608
s_waitcnt vmcnt(4) lgkmcnt(0)
s_barrier
v_add_u32 v18,122880,v12
v_add_u32 v19,122880,v13
T v[224:225],18,0
T v[226:227],18,512
T v[228:229],19,0
T v[230:231],19,512
T v[232:233],18,4096
T v[234:235],18,4608
T v[236:237],19,4096
T v[238:239],19,4608
s_waitcnt vmcnt(0) lgkmcnt(0)
s_barrier
v_add_u32 v18,139264,v12
v_add_u32 v19,139264,v13
T v[240:241],18,0
T v[242:243],18,512
T v[244:245],19,0
T v[246:247],19,512
T v[248:249],18,4096
T v[250:251],18,4608
T v[252:253],19,4096
T v[254:255],19,4608
L 0
s_barrier
v_mov_b32 v4,s53
s_mov_b32 s82,0
s_mov_b32 s80,0
s_mov_b32 s54,s45
s_mul_i32 s57,s44,7168
s_mul_i32 s55,s45,114688
s_add_u32 s55,s55,s57
s_add_u32 s55,s55,40894464
s_mul_i32 s57,s44,7168
s_mul_i32 s60,s46,114688
s_mul_i32 s61,s46,2560
s_mul_i32 s62,s46,32768
s_mul_i32 s63,s46,2048
s_mul_i32 s64,s46,512
s_lshl_b32 s59,s44,8
s_add_u32 s59,s59,143360
s_mul_i32 s58,s45,2560
s_add_u32 s58,s58,109891584
s_mul_i32 s65,s45,32768
s_add_u32 s65,s65,s47
s_mul_i32 s68,s45,2048
s_add_u32 s68,s68,s48
s_mul_i32 s73,s45,512
s_add_u32 s73,s73,106954752
s_mul_i32 s74,s45,32768
s_add_u32 s74,s74,s49
s_mul_i32 s75,s45,1024
s_add_u32 s75,s75,107479040
s_mov_b32 s66,0x7fff0000
s_mov_b32 s67,0x7fff0000
s_mov_b32 s69,0x7fff0000
s_mov_b32 s76,0x7fff0000
s_mov_b32 s77,0x7fff0000
s_mov_b32 s70,0x7fff0000
s_mov_b32 s71,0x7fff0000
s_mov_b32 s72,0x7fff0000
v_mov_b32 v1,v0
v_add_u32 v2,28672,v0
s_mov_b32 s84,28672
s_mov_b32 s86,0x3a800000
s_mov_b32 s87,0x3727c5ac
s_mov_b32 s88,0xc0135761
s_mov_b32 s89,0x3d372713
s_lshr_b32 s85,s44,1
s_mul_i32 s85,s85,127
s_add_u32 s55,s55,s51
s_mul_i32 s78,s45,114688
s_sub_u32 s60,s55,s78
s_add_u32 s50,s93,224
s_add_u32 s93,s93,170
s_cmp_lt_u32 s45,224
s_cselect_b32 s63,224,-1
s_cmp_lt_u32 s45,170
s_cselect_b32 s63,170,s63
s_cselect_b32 s93,s93,s50
s_cselect_b32 s88,6,8
s_cselect_b32 s89,224,-1
v_lshlrev_b32 v8,22,v0
v_and_b32 v8,0x80000000,v8
v_or_b32 v8,v8,v5
v_add_u32 v2,57344,v0
s_lshl_b32 s84,s57,1
s_add_u32 s84,s84,57344
s_mov_b64 s[86:87],%22
s_mov_b64 s[40:41],%22
s_mov_b32 s42,4194304
s_and_b32 s78,s47,65535
s_lshr_b32 s78,s78,4
s_sub_u32 s85,s47,s78
s_add_u32 s85,s85,4194304
s_add_u32 s45,s47,5832704
s_mul_i32 s73,s44,1280
s_add_u32 s73,s73,114688
v_lshrrev_b32 v62,4,v9
v_and_b32 v62,3,v62
v_mul_u32_u24 v62,288,v62
v_lshrrev_b32 v63,7,v9
v_and_b32 v5,12,v9
v_add3_u32 v5,v5,v62,v63
v_add_u32 v5,s73,v5
v_mov_b32 v10,0
v_mov_b32 v11,0
v_mov_b32 v12,0
v_mov_b32 v13,0
s_mov_b32 s79,1638400
buffer_store_dwordx4 v[10:13],v7,s[40:43],s79 offen offset:0 sc0 sc1
s_mov_b32 s79,1966080
buffer_store_dwordx4 v[10:13],v7,s[40:43],s79 offen offset:0 sc0 sc1
s_mov_b32 s79,2293760
buffer_store_dwordx4 v[10:13],v7,s[40:43],s79 offen offset:0 sc0 sc1
s_mov_b32 s79,2621440
buffer_store_dwordx4 v[10:13],v7,s[40:43],s79 offen offset:0 sc0 sc1
s_mov_b32 s79,2949120
buffer_store_dwordx4 v[10:13],v7,s[40:43],s79 offen offset:0 sc0 sc1
s_mov_b32 s79,3276800
buffer_store_dwordx4 v[10:13],v7,s[40:43],s79 offen offset:0 sc0 sc1
s_mov_b32 s79,3604480
buffer_store_dwordx4 v[10:13],v7,s[40:43],s79 offen offset:0 sc0 sc1
s_mov_b32 s79,3932160
buffer_store_dwordx4 v[10:13],v7,s[40:43],s79 offen offset:0 sc0 sc1
s_getpc_b64 s[40:41]
Lg4_pc_%=:
s_lshl_b32 s78,s44,13
v_lshlrev_b32 v62,3,v0
v_add_u32 v62,s78,v62
v_min_u32 v62,Lcode_g4_e-Lg4_pc_%=-4,v62
s_mov_b32 m0,163584
s_nop 0
global_load_lds_dword v62,s[40:41]
s_add_u32 m0,s59,4096
s_nop 0
buffer_load_dword v8,s[36:39],s58 offen offset:0 sc1 lds
buffer_store_dword v4,v3,s[36:39],s71 offen offset:0 sc0 sc1
s_add_u32 m0,s59,5120
s_nop 0
buffer_load_dword v8,s[36:39],s58 offen offset:0 sc1 lds
buffer_store_dword v4,v3,s[36:39],s71 offen offset:0 sc0 sc1
s_mov_b32 s83,0
Lg4_poll_%=:
s_add_u32 s78,s83,2
s_and_b32 s78,s78,3
s_lshl_b32 s78,s78,10
s_add_u32 s78,s78,4096
s_add_u32 m0,s59,s78
s_nop 0
buffer_load_dword v8,s[36:39],s58 offen offset:0 sc1 lds
s_and_b32 s78,s83,3
s_lshl_b32 s78,s78,10
v_add_u32 v62,s78,v6
s_waitcnt vmcnt(4)
s_barrier
ds_read_b32 v7,v62 offset:4096
buffer_store_dword v4,v3,s[36:39],s70 offen offset:0 sc0 sc1
s_mov_b32 s70,s71
s_mov_b32 s71,s72
s_mov_b32 s72,0x7fff0000
s_add_u32 s83,s83,1
s_add_u32 s82,s82,1
L 0
v_cmp_eq_u32 vcc,s53,v7
s_nop 0
s_cmp_eq_u32 vcc_lo,-1
s_cbranch_scc1 Lg4_go_%=
s_cmp_lt_u32 s82,20000
s_cbranch_scc1 Lg4_poll_%=
Lg4_go_%=:
s_add_u32 m0,s57,0
s_add_u32 s56,s55,0
D 0,56,0
D 0,56,1024
D 0,56,2048
D 0,56,3072
s_add_u32 m0,s57,4096
s_add_u32 s56,s56,4096
D 0,56,0
D 0,56,1024
D 0,56,2048
s_add_u32 s61,s54,s46
s_cmp_ge_u32 s61,s63
s_cselect_b32 s61,s93,s61
s_cselect_b32 s46,s88,s46
s_cselect_b32 s63,s89,s63
s_cselect_b32 s93,s50,s93
s_cselect_b32 s88,8,s88
s_cselect_b32 s89,-1,s89
s_mul_i32 s58,s61,2560
s_add_u32 s58,s58,109891584
s_add_u32 m0,s59,2048
s_nop 0
buffer_load_dword v8,s[36:39],s58 offen offset:0 sc1 lds
s_add_u32 m0,s57,28672
s_add_u32 s56,s55,28672
D 0,56,0
D 0,56,1024
D 0,56,2048
D 0,56,3072
s_add_u32 m0,s57,32768
s_add_u32 s56,s56,4096
D 0,56,0
D 0,56,1024
D 0,56,2048
s_mul_i32 s55,s61,114688
s_add_u32 s55,s55,s60
s_sub_u32 s57,s84,s57
s_mov_b32 s54,s61
s_waitcnt vmcnt(7)
s_barrier
buffer_store_dword v4,v3,s[36:39],s70 offen offset:0 sc0 sc1
s_min_u32 s71,s71,s72
buffer_store_dword v4,v3,s[36:39],s71 offen offset:0 sc0 sc1
s_mov_b32 s70,0x7fff0000
s_mov_b32 s71,0x7fff0000
s_mov_b32 s72,0x7fff0000
s_cmp_eq_u32 s80,1
s_cbranch_scc1 Lg4_st1_%=
s_branch Lg4_st0_%=
Lg4_gox_%=:
s_add_u32 m0,s59,2048
s_nop 0
buffer_load_dword v8,s[36:39],s58 offen offset:0 sc1 lds
s_waitcnt vmcnt(0)
s_barrier
buffer_store_dword v4,v3,s[36:39],s70 offen offset:0 sc0 sc1
s_min_u32 s71,s71,s72
buffer_store_dword v4,v3,s[36:39],s71 offen offset:0 sc0 sc1
s_mov_b32 s70,0x7fff0000
s_mov_b32 s71,0x7fff0000
s_mov_b32 s72,0x7fff0000
s_cmp_eq_u32 s80,1
s_cbranch_scc1 Lg4_st1_%=
Lg4_st0_%=:
ds_read_b32 v7,v6 offset:2048
R 26,1,0
R 30,1,1024
R 34,1,2048
R 38,1,3072
R 42,1,4096
L 5
v_cmp_eq_u32 vcc,s53,v7
L 3
s_cmp_eq_u32 vcc_lo,-1
s_cbranch_scc1 Lg4_F0_%=
s_branch Lg4_L0_%=
Lg4_st1_%=:
ds_read_b32 v7,v6 offset:2048
R 26,2,0
R 30,2,1024
R 34,2,2048
R 38,2,3072
R 42,2,4096
L 5
v_cmp_eq_u32 vcc,s53,v7
L 3
s_cmp_eq_u32 vcc_lo,-1
s_cbranch_scc1 Lg4_F1_%=
s_branch Lg4_L1_%=
Lg4_F0_%=:
v_mfma_f32_16x16x32_bf16 v[14:17],a[4:7],v[26:29],0
s_add_u32 m0,s57,0
R 46,1,5120
v_lshlrev_b32 v62,16,v58
M 14,a[12:15],30
s_add_u32 s56,s55,0
v_add_f32 v18,v18,v62
v_and_b32 v63,0xffff0000,v58
v_mfma_f32_16x16x32_bf16 v[10:13],a[0:3],v[26:29],0
s_add_u32 s61,s54,s46
R 50,1,6144
v_add_f32 v19,v19,v63
M 10,a[8:11],30
L 3
D 0,56,0
)"
R"(M 10,a[16:19],34
s_cmp_ge_u32 s61,s63
R 54,1,7168
v_lshlrev_b32 v62,16,v59
M 10,a[24:27],38
s_cselect_b32 s61,s93,s61
v_add_f32 v20,v20,v62
v_and_b32 v63,0xffff0000,v59
M 14,a[20:23],34
s_cselect_b32 s46,s88,s46
R 26,1,8192
v_add_f32 v21,v21,v63
M 14,a[28:31],38
L 3
v_lshlrev_b32 v62,16,v60
v_add_f32 v22,v22,v62
M 14,a[36:39],42
s_cselect_b32 s63,s89,s63
R 30,1,9216
v_and_b32 v63,0xffff0000,v60
M 14,a[44:47],46
D 0,56,1024
M 10,a[32:35],42
s_cselect_b32 s93,s50,s93
R 34,1,10240
v_add_f32 v23,v23,v63
M 10,a[40:43],46
L 3
v_lshlrev_b32 v62,16,v61
v_add_f32 v24,v24,v62
M 10,a[48:51],50
s_cselect_b32 s88,8,s88
R 38,1,11264
v_and_b32 v63,0xffff0000,v61
M 10,a[56:59],54
buffer_load_dwordx4 v[58:61],v0,s[36:39],s74 offen offset:0 sc1
M 14,a[52:55],50
s_cselect_b32 s89,-1,s89
R 42,1,12288
v_add_f32 v25,v25,v63
M 14,a[60:63],54
L 3
v_cvt_pk_bf16_f32 v18,v18,v19
v_cvt_pk_bf16_f32 v19,v20,v21
M 14,a[68:71],26
s_mul_i32 s58,s61,2560
R 46,1,13312
v_cvt_pk_bf16_f32 v20,v22,v23
M 14,a[76:79],30
D 0,56,2048
M 10,a[64:67],26
s_add_u32 s58,s58,109891584
R 50,1,14336
v_cvt_pk_bf16_f32 v21,v24,v25
M 10,a[72:75],30
L 3
buffer_store_dwordx4 v[18:21],v0,s[36:39],s67 offen offset:0 sc0 sc1
M 10,a[80:83],34
s_lshl_b32 s62,s54,15
s_mov_b32 s66,s65
R 54,1,15360
M 10,a[88:91],38
s_cmp_ge_u32 s66,s45
s_cselect_b32 s51,1,-1
s_add_u32 s74,s49,s62
M 14,a[84:87],34
s_add_u32 s65,s47,s62
s_mov_b32 s54,s61
R 26,1,16384
M 14,a[92:95],38
L 3
M 14,a[100:103],42
R 30,1,17408
M 14,a[108:111],46
D 0,56,3072
M 10,a[96:99],42
s_add_u32 m0,s57,4096
s_add_u32 s56,s56,4096
R 34,1,18432
M 10,a[104:107],46
L 3
M 10,a[112:115],50
R 38,1,19456
M 10,a[120:123],54
M 14,a[116:119],50
R 42,1,20480
M 14,a[124:127],54
L 3
D 0,56,0
M 14,a[132:135],26
R 46,1,21504
M 14,a[140:143],30
M 10,a[128:131],26
R 50,1,22528
M 10,a[136:139],30
L 3
M 10,a[144:147],34
R 54,1,23552
M 10,a[152:155],38
M 14,a[148:151],34
R 26,1,24576
M 14,a[156:159],38
L 3
D 0,56,1024
M 14,a[164:167],42
R 30,1,25600
M 14,a[172:175],46
M 10,a[160:163],42
R 34,1,26624
M 10,a[168:171],46
L 3
M 10,a[176:179],50
s_waitcnt vmcnt(9)
R 38,1,27648
M 10,a[184:187],54
s_barrier
M 14,a[180:183],50
R 42,1,28672
M 14,a[188:191],54
L 3
D 0,56,2048
M 14,a[196:199],26
s_add_u32 m0,s59,0
s_add_u32 s56,s55,28672
R 46,1,29696
M 14,a[204:207],30
s_mul_i32 s55,s61,114688
s_add_u32 s55,s55,s60
M 10,a[192:195],26
R 50,1,30720
M 10,a[200:203],30
L 3
M 10,a[208:211],34
R 54,1,31744
M 10,a[216:219],38
M 14,a[212:215],34
R 26,1,32768
M 14,a[220:223],38
L 3
buffer_load_dword v8,s[36:39],s58 offen offset:0 sc1 lds
M 14,a[228:231],42
s_add_u32 m0,s57,28672
R 30,1,33792
M 14,a[236:239],46
D 0,56,0
M 10,a[224:227],42
R 34,1,34816
M 10,a[232:235],46
L 3
M 10,a[240:243],50
R 38,1,35840
M 10,a[248:251],54
M 14,a[244:247],50
R 42,1,36864
M 14,a[252:255],54
L 3
D 0,56,1024
M 14,v[68:71],26
R 46,1,37888
M 14,v[76:79],30
M 10,v[64:67],26
R 50,1,38912
M 10,v[72:75],30
L 3
M 10,v[80:83],34
R 54,1,39936
M 10,v[88:91],38
D 0,56,2048
M 14,v[84:87],34
R 26,1,40960
M 14,v[92:95],38
L 3
M 14,v[100:103],42
R 30,1,41984
M 14,v[108:111],46
M 10,v[96:99],42
R 34,1,43008
M 10,v[104:107],46
L 3
D 0,56,3072
M 10,v[112:115],50
s_add_u32 m0,s57,32768
s_add_u32 s56,s56,4096
R 38,1,44032
M 10,v[120:123],54
s_sub_u32 s57,s84,s57
M 14,v[116:119],50
R 42,1,45056
M 14,v[124:127],54
L 3
M 14,v[132:135],26
R 46,1,46080
M 14,v[140:143],30
D 0,56,0
M 10,v[128:131],26
R 50,1,47104
M 10,v[136:139],30
L 3
M 10,v[144:147],34
R 54,1,48128
M 10,v[152:155],38
M 14,v[148:151],34
R 26,1,49152
M 14,v[156:159],38
L 3
D 0,56,1024
M 14,v[164:167],42
R 30,1,50176
M 14,v[172:175],46
M 10,v[160:163],42
R 34,1,51200
M 10,v[168:171],46
L 3
M 10,v[176:179],50
R 38,1,52224
M 10,v[184:187],54
D 0,56,2048
M 14,v[180:183],50
R 42,1,53248
M 14,v[188:191],54
L 3
M 14,v[196:199],26
R 46,1,54272
M 14,v[204:207],30
M 10,v[192:195],26
R 50,1,55296
M 10,v[200:203],30
L 3
M 10,v[208:211],34
s_waitcnt vmcnt(7)
R 54,1,56320
M 10,v[216:219],38
s_barrier
ds_read_b32 v7,v6 offset:0
M 14,v[212:215],34
R 26,2,0
M 14,v[220:223],38
L 4
buffer_store_dword v4,v3,s[36:39],s69 offen offset:0 sc0 sc1
M 14,v[228:231],42
s_mov_b32 s69,s68
s_lshr_b32 s68,s62,4
R 30,2,1024
M 14,v[236:239],46
s_add_u32 s68,s68,s48
M 10,v[224:227],42
R 34,2,2048
M 10,v[232:235],46
L 4
L 3
M 10,v[240:243],50
R 38,2,3072
v_cmp_eq_u32 vcc,s53,v7
M 10,v[248:251],54
M 14,v[244:247],50
L 2
s_cmp_eq_u32 vcc_lo,s51
R 42,2,4096
M 14,v[252:255],54
s_cbranch_scc0 Lg4_K1_%=
Lg4_F1_%=:
v_mfma_f32_16x16x32_bf16 v[22:25],a[4:7],v[26:29],0
s_add_u32 m0,s57,0
R 46,2,5120
v_lshlrev_b32 v62,16,v58
M 22,a[12:15],30
s_add_u32 s56,s55,0
v_add_f32 v10,v10,v62
v_and_b32 v63,0xffff0000,v58
v_mfma_f32_16x16x32_bf16 v[18:21],a[0:3],v[26:29],0
s_add_u32 s61,s54,s46
R 50,2,6144
v_add_f32 v11,v11,v63
M 18,a[8:11],30
L 3
D 0,56,0
M 18,a[16:19],34
s_cmp_ge_u32 s61,s63
R 54,2,7168
v_lshlrev_b32 v62,16,v59
M 18,a[24:27],38
s_cselect_b32 s61,s93,s61
v_add_f32 v12,v12,v62
v_and_b32 v63,0xffff0000,v59
M 22,a[20:23],34
s_cselect_b32 s46,s88,s46
R 26,2,8192
v_add_f32 v13,v13,v63
M 22,a[28:31],38
L 3
v_lshlrev_b32 v62,16,v60
v_add_f32 v14,v14,v62
M 22,a[36:39],42
s_cselect_b32 s63,s89,s63
R 30,2,9216
v_and_b32 v63,0xffff0000,v60
M 22,a[44:47],46
D 0,56,1024
M 18,a[32:35],42
s_cselect_b32 s93,s50,s93
R 34,2,10240
v_add_f32 v15,v15,v63
M 18,a[40:43],46
L 3
v_lshlrev_b32 v62,16,v61
v_add_f32 v16,v16,v62
M 18,a[48:51],50
s_cselect_b32 s88,8,s88
R 38,2,11264
v_and_b32 v63,0xffff0000,v61
M 18,a[56:59],54
buffer_load_dwordx4 v[58:61],v0,s[36:39],s74 offen offset:0 sc1
M 22,a[52:55],50
s_cselect_b32 s89,-1,s89
R 42,2,12288
v_add_f32 v17,v17,v63
M 22,a[60:63],54
L 3
v_cvt_pk_bf16_f32 v10,v10,v11
v_cvt_pk_bf16_f32 v11,v12,v13
M 22,a[68:71],26
s_mul_i32 s58,s61,2560
R 46,2,13312
v_cvt_pk_bf16_f32 v12,v14,v15
M 22,a[76:79],30
D 0,56,2048
M 18,a[64:67],26
s_add_u32 s58,s58,109891584
R 50,2,14336
v_cvt_pk_bf16_f32 v13,v16,v17
M 18,a[72:75],30
L 3
buffer_store_dwordx4 v[10:13],v0,s[36:39],s66 offen offset:0 sc0 sc1
M 18,a[80:83],34
s_lshl_b32 s62,s54,15
s_mov_b32 s67,s65
R 54,2,15360
M 18,a[88:91],38
s_cmp_ge_u32 s67,s45
s_cselect_b32 s51,1,-1
s_add_u32 s74,s49,s62
M 22,a[84:87],34
s_add_u32 s65,s47,s62
s_mov_b32 s54,s61
R 26,2,16384
M 22,a[92:95],38
L 3
M 22,a[100:103],42
R 30,2,17408
M 22,a[108:111],46
D 0,56,3072
M 18,a[96:99],42
s_add_u32 m0,s57,4096
s_add_u32 s56,s56,4096
R 34,2,18432
M 18,a[104:107],46
L 3
M 18,a[112:115],50
R 38,2,19456
M 18,a[120:123],54
M 22,a[116:119],50
R 42,2,20480
M 22,a[124:127],54
L 3
D 0,56,0
M 22,a[132:135],26
R 46,2,21504
M 22,a[140:143],30
M 18,a[128:131],26
R 50,2,22528
M 18,a[136:139],30
L 3
M 18,a[144:147],34
R 54,2,23552
M 18,a[152:155],38
M 22,a[148:151],34
R 26,2,24576
M 22,a[156:159],38
L 3
D 0,56,1024
M 22,a[164:167],42
R 30,2,25600
M 22,a[172:175],46
M 18,a[160:163],42
R 34,2,26624
M 18,a[168:171],46
L 3
M 18,a[176:179],50
s_waitcnt vmcnt(9)
R 38,2,27648
M 18,a[184:187],54
s_barrier
M 22,a[180:183],50
R 42,2,28672
M 22,a[188:191],54
L 3
D 0,56,2048
M 22,a[196:199],26
s_add_u32 m0,s59,1024
s_add_u32 s56,s55,28672
R 46,2,29696
M 22,a[204:207],30
s_mul_i32 s55,s61,114688
s_add_u32 s55,s55,s60
M 18,a[192:195],26
R 50,2,30720
M 18,a[200:203],30
L 3
M 18,a[208:211],34
R 54,2,31744
M 18,a[216:219],38
M 22,a[212:215],34
R 26,2,32768
M 22,a[220:223],38
L 3
buffer_load_dword v8,s[36:39],s58 offen offset:0 sc1 lds
M 22,a[228:231],42
s_add_u32 m0,s57,28672
R 30,2,33792
M 22,a[236:239],46
D 0,56,0
M 18,a[224:227],42
R 34,2,34816
M 18,a[232:235],46
L 3
M 18,a[240:243],50
R 38,2,35840
M 18,a[248:251],54
M 22,a[244:247],50
R 42,2,36864
M 22,a[252:255],54
L 3
D 0,56,1024
M 22,v[68:71],26
R 46,2,37888
M 22,v[76:79],30
M 18,v[64:67],26
R 50,2,38912
M 18,v[72:75],30
L 3
M 18,v[80:83],34
R 54,2,39936
M 18,v[88:91],38
D 0,56,2048
M 22,v[84:87],34
R 26,2,40960
M 22,v[92:95],38
L 3
M 22,v[100:103],42
R 30,2,41984
M 22,v[108:111],46
M 18,v[96:99],42
R 34,2,43008
M 18,v[104:107],46
L 3
D 0,56,3072
M 18,v[112:115],50
s_add_u32 m0,s57,32768
s_add_u32 s56,s56,4096
R 38,2,44032
M 18,v[120:123],54
s_sub_u32 s57,s84,s57
M 22,v[116:119],50
R 42,2,45056
M 22,v[124:127],54
L 3
M 22,v[132:135],26
R 46,2,46080
M 22,v[140:143],30
D 0,56,0
M 18,v[128:131],26
R 50,2,47104
M 18,v[136:139],30
L 3
M 18,v[144:147],34
R 54,2,48128
M 18,v[152:155],38
M 22,v[148:151],34
R 26,2,49152
M 22,v[156:159],38
L 3
D 0,56,1024
M 22,v[164:167],42
R 30,2,50176
M 22,v[172:175],46
M 18,v[160:163],42
R 34,2,51200
M 18,v[168:171],46
L 3
M 18,v[176:179],50
R 38,2,52224
M 18,v[184:187],54
D 0,56,2048
M 22,v[180:183],50
R 42,2,53248
M 22,v[188:191],54
L 3
M 22,v[196:199],26
R 46,2,54272
M 22,v[204:207],30
)"
R"(M 18,v[192:195],26
R 50,2,55296
M 18,v[200:203],30
L 3
M 18,v[208:211],34
s_waitcnt vmcnt(7)
R 54,2,56320
M 18,v[216:219],38
s_barrier
ds_read_b32 v7,v6 offset:1024
M 22,v[212:215],34
R 26,1,0
M 22,v[220:223],38
L 4
buffer_store_dword v4,v3,s[36:39],s69 offen offset:0 sc0 sc1
M 22,v[228:231],42
s_mov_b32 s69,s68
s_lshr_b32 s68,s62,4
R 30,1,1024
M 22,v[236:239],46
s_add_u32 s68,s68,s48
M 18,v[224:227],42
R 34,1,2048
M 18,v[232:235],46
L 4
L 3
M 18,v[240:243],50
R 38,1,3072
v_cmp_eq_u32 vcc,s53,v7
M 18,v[248:251],54
M 22,v[244:247],50
L 2
s_cmp_eq_u32 vcc_lo,s51
R 42,1,4096
M 22,v[252:255],54
s_cbranch_scc1 Lg4_F0_%=
Lg4_K0_%=:
s_cmp_eq_u32 s51,1
s_cbranch_scc0 Lg4_L0_%=
s_cmp_eq_u32 vcc_lo,-1
s_cbranch_scc1 Lg4_Fa0_%=
s_branch Lg4_La0_%=
Lg4_L0_%=:
v_mfma_f32_16x16x32_bf16 v[14:17],a[4:7],v[26:29],0
s_add_u32 m0,s59,3072
R 46,1,5120
v_lshlrev_b32 v62,16,v58
M 14,a[12:15],30
buffer_load_dword v8,s[36:39],s58 offen offset:0 sc1 lds
v_mfma_f32_16x16x32_bf16 v[10:13],a[0:3],v[26:29],0
s_mov_b32 s66,s65
R 50,1,6144
v_add_f32 v18,v18,v62
M 10,a[8:11],30
L 3
v_and_b32 v63,0xffff0000,v58
v_add_f32 v19,v19,v63
M 10,a[16:19],34
s_lshl_b32 s62,s54,15
R 54,1,7168
v_lshlrev_b32 v62,16,v59
M 10,a[24:27],38
s_cmp_ge_u32 s66,s45
v_add_f32 v20,v20,v62
v_and_b32 v63,0xffff0000,v59
M 14,a[20:23],34
s_cselect_b32 s51,1,-1
R 26,1,8192
v_add_f32 v21,v21,v63
M 14,a[28:31],38
L 3
v_lshlrev_b32 v62,16,v60
v_add_f32 v22,v22,v62
M 14,a[36:39],42
s_add_u32 s65,s47,s62
R 30,1,9216
v_and_b32 v63,0xffff0000,v60
M 14,a[44:47],46
v_add_f32 v23,v23,v63
v_lshlrev_b32 v62,16,v61
M 10,a[32:35],42
R 34,1,10240
v_add_f32 v24,v24,v62
M 10,a[40:43],46
L 3
v_and_b32 v63,0xffff0000,v61
v_add_f32 v25,v25,v63
M 10,a[48:51],50
R 38,1,11264
v_cvt_pk_bf16_f32 v18,v18,v19
M 10,a[56:59],54
buffer_load_dwordx4 v[58:61],v0,s[36:39],s74 offen offset:0 sc1
M 14,a[52:55],50
s_add_u32 s74,s49,s62
R 42,1,12288
v_cvt_pk_bf16_f32 v19,v20,v21
M 14,a[60:63],54
L 3
v_cvt_pk_bf16_f32 v20,v22,v23
v_cvt_pk_bf16_f32 v21,v24,v25
M 14,a[68:71],26
R 46,1,13312
M 14,a[76:79],30
buffer_store_dwordx4 v[18:21],v0,s[36:39],s67 offen offset:0 sc0 sc1
M 10,a[64:67],26
R 50,1,14336
M 10,a[72:75],30
L 3
M 10,a[80:83],34
R 54,1,15360
M 10,a[88:91],38
M 14,a[84:87],34
R 26,1,16384
M 14,a[92:95],38
L 3
M 14,a[100:103],42
R 30,1,17408
M 14,a[108:111],46
M 10,a[96:99],42
R 34,1,18432
M 10,a[104:107],46
L 3
M 10,a[112:115],50
R 38,1,19456
M 10,a[120:123],54
M 14,a[116:119],50
R 42,1,20480
M 14,a[124:127],54
L 3
M 14,a[132:135],26
R 46,1,21504
M 14,a[140:143],30
M 10,a[128:131],26
R 50,1,22528
M 10,a[136:139],30
L 3
M 10,a[144:147],34
R 54,1,23552
M 10,a[152:155],38
M 14,a[148:151],34
R 26,1,24576
M 14,a[156:159],38
L 3
M 14,a[164:167],42
R 30,1,25600
M 14,a[172:175],46
M 10,a[160:163],42
R 34,1,26624
M 10,a[168:171],46
L 3
M 10,a[176:179],50
s_waitcnt vmcnt(2)
R 38,1,27648
M 10,a[184:187],54
s_barrier
ds_read_b32 v7,v6 offset:3072
M 14,a[180:183],50
R 42,1,28672
M 14,a[188:191],54
L 4
M 14,a[196:199],26
R 46,1,29696
M 14,a[204:207],30
M 10,a[192:195],26
R 50,1,30720
M 10,a[200:203],30
L 4
L 3
v_cmp_eq_u32 vcc,s53,v7
M 10,a[208:211],34
R 54,1,31744
M 10,a[216:219],38
M 14,a[212:215],34
s_cmp_eq_u32 vcc_lo,-1
R 26,1,32768
s_cbranch_scc0 Lg4_X0_%=
M 14,a[220:223],38
L 3
s_add_u32 m0,s57,0
s_add_u32 s56,s55,0
M 14,a[228:231],42
s_add_u32 s61,s54,s46
s_cmp_ge_u32 s61,s63
s_cselect_b32 s61,s93,s61
R 30,1,33792
M 14,a[236:239],46
s_mul_i32 s58,s61,2560
D 0,56,0
M 10,a[224:227],42
s_cselect_b32 s46,s88,s46
s_cselect_b32 s63,s89,s63
R 34,1,34816
M 10,a[232:235],46
L 3
s_cselect_b32 s93,s50,s93
s_cselect_b32 s88,8,s88
M 10,a[240:243],50
s_cselect_b32 s89,-1,s89
s_add_u32 s58,s58,109891584
R 38,1,35840
M 10,a[248:251],54
D 0,56,1024
M 14,a[244:247],50
s_mov_b32 s54,s61
R 42,1,36864
M 14,a[252:255],54
L 3
M 14,v[68:71],26
R 46,1,37888
M 14,v[76:79],30
D 0,56,2048
M 10,v[64:67],26
R 50,1,38912
M 10,v[72:75],30
L 3
M 10,v[80:83],34
R 54,1,39936
M 10,v[88:91],38
D 0,56,3072
M 14,v[84:87],34
s_add_u32 m0,s57,4096
s_add_u32 s56,s56,4096
R 26,1,40960
M 14,v[92:95],38
L 3
M 14,v[100:103],42
R 30,1,41984
M 14,v[108:111],46
D 0,56,0
M 10,v[96:99],42
R 34,1,43008
M 10,v[104:107],46
L 3
M 10,v[112:115],50
R 38,1,44032
M 10,v[120:123],54
D 0,56,1024
M 14,v[116:119],50
R 42,1,45056
M 14,v[124:127],54
L 3
M 14,v[132:135],26
R 46,1,46080
M 14,v[140:143],30
D 0,56,2048
M 10,v[128:131],26
s_add_u32 m0,s59,0
s_add_u32 s56,s55,28672
R 50,1,47104
M 10,v[136:139],30
L 3
buffer_load_dword v8,s[36:39],s58 offen offset:0 sc1 lds
M 10,v[144:147],34
s_add_u32 m0,s57,28672
s_mul_i32 s55,s61,114688
R 54,1,48128
M 10,v[152:155],38
s_add_u32 s55,s55,s60
M 14,v[148:151],34
R 26,1,49152
M 14,v[156:159],38
L 3
D 0,56,0
M 14,v[164:167],42
R 30,1,50176
M 14,v[172:175],46
M 10,v[160:163],42
R 34,1,51200
M 10,v[168:171],46
L 3
D 0,56,1024
M 10,v[176:179],50
R 38,1,52224
M 10,v[184:187],54
D 0,56,2048
M 14,v[180:183],50
R 42,1,53248
M 14,v[188:191],54
L 3
M 14,v[196:199],26
R 46,1,54272
M 14,v[204:207],30
D 0,56,3072
M 10,v[192:195],26
s_add_u32 m0,s57,32768
s_add_u32 s56,s56,4096
R 50,1,55296
M 10,v[200:203],30
L 3
s_sub_u32 s57,s84,s57
M 10,v[208:211],34
s_waitcnt vmcnt(4)
R 54,1,56320
M 10,v[216:219],38
s_barrier
ds_read_b32 v7,v6 offset:0
M 14,v[212:215],34
R 26,2,0
M 14,v[220:223],38
L 4
buffer_store_dword v4,v3,s[36:39],s69 offen offset:0 sc0 sc1
M 14,v[228:231],42
s_mov_b32 s69,s68
s_lshr_b32 s68,s62,4
R 30,2,1024
M 14,v[236:239],46
D 0,56,0
M 10,v[224:227],42
s_add_u32 s68,s68,s48
R 34,2,2048
M 10,v[232:235],46
L 4
D 0,56,1024
M 10,v[240:243],50
L 3
R 38,2,3072
v_cmp_eq_u32 vcc,s53,v7
M 10,v[248:251],54
D 0,56,2048
M 14,v[244:247],50
L 2
s_cmp_eq_u32 vcc_lo,s51
R 42,2,4096
M 14,v[252:255],54
buffer_store_dword v4,v3,s[36:39],s71 offen offset:0 sc0 sc1
s_cbranch_scc1 Lg4_F1_%=
s_branch Lg4_K1_%=
Lg4_X0_%=:
M 14,a[220:223],38
L 3
s_add_u32 m0,s59,0
M 14,a[228:231],42
R 30,1,33792
M 14,a[236:239],46
buffer_load_dword v8,s[36:39],s58 offen offset:0 sc1 lds
M 10,a[224:227],42
R 34,1,34816
M 10,a[232:235],46
L 3
M 10,a[240:243],50
R 38,1,35840
M 10,a[248:251],54
M 14,a[244:247],50
R 42,1,36864
M 14,a[252:255],54
L 3
M 14,v[68:71],26
R 46,1,37888
M 14,v[76:79],30
M 10,v[64:67],26
R 50,1,38912
M 10,v[72:75],30
L 3
M 10,v[80:83],34
R 54,1,39936
M 10,v[88:91],38
M 14,v[84:87],34
R 26,1,40960
M 14,v[92:95],38
L 3
M 14,v[100:103],42
R 30,1,41984
M 14,v[108:111],46
M 10,v[96:99],42
R 34,1,43008
M 10,v[104:107],46
L 3
M 10,v[112:115],50
R 38,1,44032
M 10,v[120:123],54
M 14,v[116:119],50
R 42,1,45056
M 14,v[124:127],54
L 3
M 14,v[132:135],26
R 46,1,46080
M 14,v[140:143],30
M 10,v[128:131],26
R 50,1,47104
M 10,v[136:139],30
L 3
M 10,v[144:147],34
R 54,1,48128
M 10,v[152:155],38
M 14,v[148:151],34
R 26,1,49152
M 14,v[156:159],38
L 3
M 14,v[164:167],42
R 30,1,50176
M 14,v[172:175],46
M 10,v[160:163],42
R 34,1,51200
M 10,v[168:171],46
L 3
M 10,v[176:179],50
R 38,1,52224
M 10,v[184:187],54
M 14,v[180:183],50
R 42,1,53248
M 14,v[188:191],54
L 3
M 14,v[196:199],26
R 46,1,54272
M 14,v[204:207],30
M 10,v[192:195],26
R 50,1,55296
M 10,v[200:203],30
L 3
M 10,v[208:211],34
s_waitcnt vmcnt(0)
R 54,1,56320
M 10,v[216:219],38
s_barrier
buffer_store_dword v4,v3,s[36:39],s69 offen offset:0 sc0 sc1
M 14,v[212:215],34
s_mov_b32 s69,s68
s_lshr_b32 s68,s62,4
ds_read_b32 v7,v6 offset:0
M 14,v[220:223],38
L 3
s_add_u32 s68,s68,s48
M 14,v[228:231],42
M 14,v[236:239],46
M 10,v[224:227],42
M 10,v[232:235],46
L 1
M 10,v[240:243],50
L 0
v_cmp_eq_u32 vcc,s53,v7
M 10,v[248:251],54
M 14,v[244:247],50
M 14,v[252:255],54
s_cmp_eq_u32 s51,1
s_cbranch_scc1 Lg4_pa0_%=
s_mov_b32 s80,1
s_cmp_eq_u32 vcc_lo,-1
s_cbranch_scc0 Lg4_xn0_%=
s_add_u32 m0,s57,0
s_add_u32 s56,s55,0
D 0,56,0
v_lshlrev_b32 v62,16,v58
D 0,56,1024
v_add_f32 v10,v10,v62
D 0,56,2048
v_and_b32 v63,0xffff0000,v58
D 0,56,3072
v_add_f32 v11,v11,v63
s_add_u32 m0,s57,4096
s_add_u32 s56,s56,4096
D 0,56,0
v_lshlrev_b32 v62,16,v59
D 0,56,1024
v_add_f32 v12,v12,v62
D 0,56,2048
v_and_b32 v63,0xffff0000,v59
s_add_u32 s61,s54,s46
s_cmp_ge_u32 s61,s63
s_cselect_b32 s61,s93,s61
s_cselect_b32 s46,s88,s46
s_cselect_b32 s63,s89,s63
s_cselect_b32 s93,s50,s93
s_cselect_b32 s88,8,s88
s_cselect_b32 s89,-1,s89
s_mul_i32 s58,s61,2560
s_add_u32 s58,s58,109891584
s_add_u32 m0,s57,28672
s_add_u32 s56,s55,28672
D 0,56,0
v_add_f32 v13,v13,v63
D 0,56,1024
v_lshlrev_b32 v62,16,v60
D 0,56,2048
v_add_f32 v14,v14,v62
D 0,56,3072
v_and_b32 v63,0xffff0000,v60
s_add_u32 m0,s57,32768
s_add_u32 s56,s56,4096
D 0,56,0
v_add_f32 v15,v15,v63
D 0,56,1024
v_lshlrev_b32 v62,16,v61
D 0,56,2048
v_add_f32 v16,v16,v62
s_mul_i32 s55,s61,114688
s_add_u32 s55,s55,s60
s_sub_u32 s57,s84,s57
s_mov_b32 s54,s61
v_and_b32 v63,0xffff0000,v61
v_add_f32 v17,v17,v63
v_cvt_pk_bf16_f32 v10,v10,v11
v_cvt_pk_bf16_f32 v11,v12,v13
v_cvt_pk_bf16_f32 v12,v14,v15
v_cvt_pk_bf16_f32 v13,v16,v17
buffer_store_dwordx4 v[10:13],v0,s[36:39],s66 offen offset:0 sc0 sc1
s_mov_b32 s72,s69
s_mov_b32 s69,0x7fff0000
s_mov_b32 s66,0x7fff0000
s_mov_b32 s76,0x7fff0000
s_branch Lg4_gox_%=
)"
R"(Lg4_xn0_%=:
s_cmp_ge_u32 s54,256
s_cbranch_scc1 Lg4_fin0_%=
s_add_u32 m0,s59,4096
s_nop 0
buffer_load_dword v8,s[36:39],s58 offen offset:0 sc1 lds
buffer_store_dword v4,v3,s[36:39],s71 offen offset:0 sc0 sc1
s_add_u32 m0,s59,5120
s_nop 0
buffer_load_dword v8,s[36:39],s58 offen offset:0 sc1 lds
buffer_store_dword v4,v3,s[36:39],s71 offen offset:0 sc0 sc1
s_mov_b32 s83,0
v_lshlrev_b32 v62,16,v58
v_add_f32 v10,v10,v62
v_and_b32 v63,0xffff0000,v58
v_add_f32 v11,v11,v63
v_lshlrev_b32 v62,16,v59
v_add_f32 v12,v12,v62
v_and_b32 v63,0xffff0000,v59
v_add_f32 v13,v13,v63
v_lshlrev_b32 v62,16,v60
v_add_f32 v14,v14,v62
v_and_b32 v63,0xffff0000,v60
v_add_f32 v15,v15,v63
v_lshlrev_b32 v62,16,v61
v_add_f32 v16,v16,v62
v_and_b32 v63,0xffff0000,v61
v_add_f32 v17,v17,v63
v_cvt_pk_bf16_f32 v10,v10,v11
v_cvt_pk_bf16_f32 v11,v12,v13
v_cvt_pk_bf16_f32 v12,v14,v15
v_cvt_pk_bf16_f32 v13,v16,v17
buffer_store_dwordx4 v[10:13],v0,s[36:39],s66 offen offset:0 sc0 sc1
s_mov_b32 s72,s69
s_mov_b32 s69,0x7fff0000
s_mov_b32 s66,0x7fff0000
s_mov_b32 s76,0x7fff0000
s_branch Lg4_poll_%=
Lg4_fin0_%=:
v_lshlrev_b32 v62,16,v58
v_add_f32 v10,v10,v62
v_and_b32 v63,0xffff0000,v58
v_add_f32 v11,v11,v63
v_lshlrev_b32 v62,16,v59
v_add_f32 v12,v12,v62
v_and_b32 v63,0xffff0000,v59
v_add_f32 v13,v13,v63
v_lshlrev_b32 v62,16,v60
v_add_f32 v14,v14,v62
v_and_b32 v63,0xffff0000,v60
v_add_f32 v15,v15,v63
v_lshlrev_b32 v62,16,v61
v_add_f32 v16,v16,v62
v_and_b32 v63,0xffff0000,v61
v_add_f32 v17,v17,v63
v_cvt_pk_bf16_f32 v10,v10,v11
v_cvt_pk_bf16_f32 v11,v12,v13
v_cvt_pk_bf16_f32 v12,v14,v15
v_cvt_pk_bf16_f32 v13,v16,v17
buffer_store_dwordx4 v[10:13],v0,s[36:39],s66 offen offset:0 sc0 sc1
s_mov_b32 s72,s69
s_mov_b32 s69,0x7fff0000
s_mov_b32 s66,0x7fff0000
s_mov_b32 s76,0x7fff0000
s_branch Lg4_done_%=
Lg4_K1_%=:
s_cmp_eq_u32 s51,1
s_cbranch_scc0 Lg4_L1_%=
s_cmp_eq_u32 vcc_lo,-1
s_cbranch_scc1 Lg4_Fa1_%=
s_branch Lg4_La1_%=
Lg4_L1_%=:
v_mfma_f32_16x16x32_bf16 v[22:25],a[4:7],v[26:29],0
s_add_u32 m0,s59,3072
R 46,2,5120
v_lshlrev_b32 v62,16,v58
M 22,a[12:15],30
buffer_load_dword v8,s[36:39],s58 offen offset:0 sc1 lds
v_mfma_f32_16x16x32_bf16 v[18:21],a[0:3],v[26:29],0
s_mov_b32 s67,s65
R 50,2,6144
v_add_f32 v10,v10,v62
M 18,a[8:11],30
L 3
v_and_b32 v63,0xffff0000,v58
v_add_f32 v11,v11,v63
M 18,a[16:19],34
s_lshl_b32 s62,s54,15
R 54,2,7168
v_lshlrev_b32 v62,16,v59
M 18,a[24:27],38
s_cmp_ge_u32 s67,s45
v_add_f32 v12,v12,v62
v_and_b32 v63,0xffff0000,v59
M 22,a[20:23],34
s_cselect_b32 s51,1,-1
R 26,2,8192
v_add_f32 v13,v13,v63
M 22,a[28:31],38
L 3
v_lshlrev_b32 v62,16,v60
v_add_f32 v14,v14,v62
M 22,a[36:39],42
s_add_u32 s65,s47,s62
R 30,2,9216
v_and_b32 v63,0xffff0000,v60
M 22,a[44:47],46
v_add_f32 v15,v15,v63
v_lshlrev_b32 v62,16,v61
M 18,a[32:35],42
R 34,2,10240
v_add_f32 v16,v16,v62
M 18,a[40:43],46
L 3
v_and_b32 v63,0xffff0000,v61
v_add_f32 v17,v17,v63
M 18,a[48:51],50
R 38,2,11264
v_cvt_pk_bf16_f32 v10,v10,v11
M 18,a[56:59],54
buffer_load_dwordx4 v[58:61],v0,s[36:39],s74 offen offset:0 sc1
M 22,a[52:55],50
s_add_u32 s74,s49,s62
R 42,2,12288
v_cvt_pk_bf16_f32 v11,v12,v13
M 22,a[60:63],54
L 3
v_cvt_pk_bf16_f32 v12,v14,v15
v_cvt_pk_bf16_f32 v13,v16,v17
M 22,a[68:71],26
R 46,2,13312
M 22,a[76:79],30
buffer_store_dwordx4 v[10:13],v0,s[36:39],s66 offen offset:0 sc0 sc1
M 18,a[64:67],26
R 50,2,14336
M 18,a[72:75],30
L 3
M 18,a[80:83],34
R 54,2,15360
M 18,a[88:91],38
M 22,a[84:87],34
R 26,2,16384
M 22,a[92:95],38
L 3
M 22,a[100:103],42
R 30,2,17408
M 22,a[108:111],46
M 18,a[96:99],42
R 34,2,18432
M 18,a[104:107],46
L 3
M 18,a[112:115],50
R 38,2,19456
M 18,a[120:123],54
M 22,a[116:119],50
R 42,2,20480
M 22,a[124:127],54
L 3
M 22,a[132:135],26
R 46,2,21504
M 22,a[140:143],30
M 18,a[128:131],26
R 50,2,22528
M 18,a[136:139],30
L 3
M 18,a[144:147],34
R 54,2,23552
M 18,a[152:155],38
M 22,a[148:151],34
R 26,2,24576
M 22,a[156:159],38
L 3
M 22,a[164:167],42
R 30,2,25600
M 22,a[172:175],46
M 18,a[160:163],42
R 34,2,26624
M 18,a[168:171],46
L 3
M 18,a[176:179],50
s_waitcnt vmcnt(2)
R 38,2,27648
M 18,a[184:187],54
s_barrier
ds_read_b32 v7,v6 offset:3072
M 22,a[180:183],50
R 42,2,28672
M 22,a[188:191],54
L 4
M 22,a[196:199],26
R 46,2,29696
M 22,a[204:207],30
M 18,a[192:195],26
R 50,2,30720
M 18,a[200:203],30
L 4
L 3
v_cmp_eq_u32 vcc,s53,v7
M 18,a[208:211],34
R 54,2,31744
M 18,a[216:219],38
M 22,a[212:215],34
s_cmp_eq_u32 vcc_lo,-1
R 26,2,32768
s_cbranch_scc0 Lg4_X1_%=
M 22,a[220:223],38
L 3
s_add_u32 m0,s57,0
s_add_u32 s56,s55,0
M 22,a[228:231],42
s_add_u32 s61,s54,s46
s_cmp_ge_u32 s61,s63
s_cselect_b32 s61,s93,s61
R 30,2,33792
M 22,a[236:239],46
s_mul_i32 s58,s61,2560
D 0,56,0
M 18,a[224:227],42
s_cselect_b32 s46,s88,s46
s_cselect_b32 s63,s89,s63
R 34,2,34816
M 18,a[232:235],46
L 3
s_cselect_b32 s93,s50,s93
s_cselect_b32 s88,8,s88
M 18,a[240:243],50
s_cselect_b32 s89,-1,s89
s_add_u32 s58,s58,109891584
R 38,2,35840
M 18,a[248:251],54
D 0,56,1024
M 22,a[244:247],50
s_mov_b32 s54,s61
R 42,2,36864
M 22,a[252:255],54
L 3
M 22,v[68:71],26
R 46,2,37888
M 22,v[76:79],30
D 0,56,2048
M 18,v[64:67],26
R 50,2,38912
M 18,v[72:75],30
L 3
M 18,v[80:83],34
R 54,2,39936
M 18,v[88:91],38
D 0,56,3072
M 22,v[84:87],34
s_add_u32 m0,s57,4096
s_add_u32 s56,s56,4096
R 26,2,40960
M 22,v[92:95],38
L 3
M 22,v[100:103],42
R 30,2,41984
M 22,v[108:111],46
D 0,56,0
M 18,v[96:99],42
R 34,2,43008
M 18,v[104:107],46
L 3
M 18,v[112:115],50
R 38,2,44032
M 18,v[120:123],54
D 0,56,1024
M 22,v[116:119],50
R 42,2,45056
M 22,v[124:127],54
L 3
M 22,v[132:135],26
R 46,2,46080
M 22,v[140:143],30
D 0,56,2048
M 18,v[128:131],26
s_add_u32 m0,s59,1024
s_add_u32 s56,s55,28672
R 50,2,47104
M 18,v[136:139],30
L 3
buffer_load_dword v8,s[36:39],s58 offen offset:0 sc1 lds
M 18,v[144:147],34
s_add_u32 m0,s57,28672
s_mul_i32 s55,s61,114688
R 54,2,48128
M 18,v[152:155],38
s_add_u32 s55,s55,s60
M 22,v[148:151],34
R 26,2,49152
M 22,v[156:159],38
L 3
D 0,56,0
M 22,v[164:167],42
R 30,2,50176
M 22,v[172:175],46
M 18,v[160:163],42
R 34,2,51200
M 18,v[168:171],46
L 3
D 0,56,1024
M 18,v[176:179],50
R 38,2,52224
M 18,v[184:187],54
D 0,56,2048
M 22,v[180:183],50
R 42,2,53248
M 22,v[188:191],54
L 3
M 22,v[196:199],26
R 46,2,54272
M 22,v[204:207],30
D 0,56,3072
M 18,v[192:195],26
s_add_u32 m0,s57,32768
s_add_u32 s56,s56,4096
R 50,2,55296
M 18,v[200:203],30
L 3
s_sub_u32 s57,s84,s57
M 18,v[208:211],34
s_waitcnt vmcnt(4)
R 54,2,56320
M 18,v[216:219],38
s_barrier
ds_read_b32 v7,v6 offset:1024
M 22,v[212:215],34
R 26,1,0
M 22,v[220:223],38
L 4
buffer_store_dword v4,v3,s[36:39],s69 offen offset:0 sc0 sc1
M 22,v[228:231],42
s_mov_b32 s69,s68
s_lshr_b32 s68,s62,4
R 30,1,1024
M 22,v[236:239],46
D 0,56,0
M 18,v[224:227],42
s_add_u32 s68,s68,s48
R 34,1,2048
M 18,v[232:235],46
L 4
D 0,56,1024
M 18,v[240:243],50
L 3
R 38,1,3072
v_cmp_eq_u32 vcc,s53,v7
M 18,v[248:251],54
D 0,56,2048
M 22,v[244:247],50
L 2
s_cmp_eq_u32 vcc_lo,s51
R 42,1,4096
M 22,v[252:255],54
buffer_store_dword v4,v3,s[36:39],s71 offen offset:0 sc0 sc1
s_cbranch_scc1 Lg4_F0_%=
s_branch Lg4_K0_%=
Lg4_X1_%=:
M 22,a[220:223],38
L 3
s_add_u32 m0,s59,1024
M 22,a[228:231],42
R 30,2,33792
M 22,a[236:239],46
buffer_load_dword v8,s[36:39],s58 offen offset:0 sc1 lds
M 18,a[224:227],42
R 34,2,34816
M 18,a[232:235],46
L 3
M 18,a[240:243],50
R 38,2,35840
M 18,a[248:251],54
M 22,a[244:247],50
R 42,2,36864
M 22,a[252:255],54
L 3
M 22,v[68:71],26
R 46,2,37888
M 22,v[76:79],30
M 18,v[64:67],26
R 50,2,38912
M 18,v[72:75],30
L 3
M 18,v[80:83],34
R 54,2,39936
M 18,v[88:91],38
M 22,v[84:87],34
R 26,2,40960
M 22,v[92:95],38
L 3
M 22,v[100:103],42
R 30,2,41984
M 22,v[108:111],46
M 18,v[96:99],42
R 34,2,43008
M 18,v[104:107],46
L 3
M 18,v[112:115],50
R 38,2,44032
M 18,v[120:123],54
M 22,v[116:119],50
R 42,2,45056
M 22,v[124:127],54
L 3
M 22,v[132:135],26
R 46,2,46080
M 22,v[140:143],30
M 18,v[128:131],26
R 50,2,47104
M 18,v[136:139],30
L 3
M 18,v[144:147],34
R 54,2,48128
M 18,v[152:155],38
M 22,v[148:151],34
R 26,2,49152
M 22,v[156:159],38
L 3
M 22,v[164:167],42
R 30,2,50176
M 22,v[172:175],46
M 18,v[160:163],42
R 34,2,51200
M 18,v[168:171],46
L 3
M 18,v[176:179],50
R 38,2,52224
M 18,v[184:187],54
M 22,v[180:183],50
R 42,2,53248
M 22,v[188:191],54
L 3
M 22,v[196:199],26
R 46,2,54272
M 22,v[204:207],30
M 18,v[192:195],26
R 50,2,55296
M 18,v[200:203],30
L 3
M 18,v[208:211],34
s_waitcnt vmcnt(0)
R 54,2,56320
M 18,v[216:219],38
s_barrier
buffer_store_dword v4,v3,s[36:39],s69 offen offset:0 sc0 sc1
M 22,v[212:215],34
s_mov_b32 s69,s68
s_lshr_b32 s68,s62,4
ds_read_b32 v7,v6 offset:1024
M 22,v[220:223],38
L 3
s_add_u32 s68,s68,s48
M 22,v[228:231],42
M 22,v[236:239],46
M 18,v[224:227],42
M 18,v[232:235],46
L 1
M 18,v[240:243],50
L 0
v_cmp_eq_u32 vcc,s53,v7
M 18,v[248:251],54
M 22,v[244:247],50
M 22,v[252:255],54
s_cmp_eq_u32 s51,1
s_cbranch_scc1 Lg4_pa1_%=
s_mov_b32 s80,0
s_cmp_eq_u32 vcc_lo,-1
s_cbranch_scc0 Lg4_xn1_%=
s_add_u32 m0,s57,0
s_add_u32 s56,s55,0
D 0,56,0
v_lshlrev_b32 v62,16,v58
D 0,56,1024
v_add_f32 v18,v18,v62
D 0,56,2048
v_and_b32 v63,0xffff0000,v58
D 0,56,3072
v_add_f32 v19,v19,v63
s_add_u32 m0,s57,4096
s_add_u32 s56,s56,4096
D 0,56,0
v_lshlrev_b32 v62,16,v59
D 0,56,1024
v_add_f32 v20,v20,v62
D 0,56,2048
v_and_b32 v63,0xffff0000,v59
s_add_u32 s61,s54,s46
s_cmp_ge_u32 s61,s63
s_cselect_b32 s61,s93,s61
s_cselect_b32 s46,s88,s46
s_cselect_b32 s63,s89,s63
s_cselect_b32 s93,s50,s93
s_cselect_b32 s88,8,s88
s_cselect_b32 s89,-1,s89
s_mul_i32 s58,s61,2560
s_add_u32 s58,s58,109891584
s_add_u32 m0,s57,28672
s_add_u32 s56,s55,28672
D 0,56,0
v_add_f32 v21,v21,v63
D 0,56,1024
)"
R"(v_lshlrev_b32 v62,16,v60
D 0,56,2048
v_add_f32 v22,v22,v62
D 0,56,3072
v_and_b32 v63,0xffff0000,v60
s_add_u32 m0,s57,32768
s_add_u32 s56,s56,4096
D 0,56,0
v_add_f32 v23,v23,v63
D 0,56,1024
v_lshlrev_b32 v62,16,v61
D 0,56,2048
v_add_f32 v24,v24,v62
s_mul_i32 s55,s61,114688
s_add_u32 s55,s55,s60
s_sub_u32 s57,s84,s57
s_mov_b32 s54,s61
v_and_b32 v63,0xffff0000,v61
v_add_f32 v25,v25,v63
v_cvt_pk_bf16_f32 v18,v18,v19
v_cvt_pk_bf16_f32 v19,v20,v21
v_cvt_pk_bf16_f32 v20,v22,v23
v_cvt_pk_bf16_f32 v21,v24,v25
buffer_store_dwordx4 v[18:21],v0,s[36:39],s67 offen offset:0 sc0 sc1
s_mov_b32 s72,s69
s_mov_b32 s69,0x7fff0000
s_mov_b32 s67,0x7fff0000
s_mov_b32 s77,0x7fff0000
s_branch Lg4_gox_%=
Lg4_xn1_%=:
s_cmp_ge_u32 s54,256
s_cbranch_scc1 Lg4_fin1_%=
s_add_u32 m0,s59,4096
s_nop 0
buffer_load_dword v8,s[36:39],s58 offen offset:0 sc1 lds
buffer_store_dword v4,v3,s[36:39],s71 offen offset:0 sc0 sc1
s_add_u32 m0,s59,5120
s_nop 0
buffer_load_dword v8,s[36:39],s58 offen offset:0 sc1 lds
buffer_store_dword v4,v3,s[36:39],s71 offen offset:0 sc0 sc1
s_mov_b32 s83,0
v_lshlrev_b32 v62,16,v58
v_add_f32 v18,v18,v62
v_and_b32 v63,0xffff0000,v58
v_add_f32 v19,v19,v63
v_lshlrev_b32 v62,16,v59
v_add_f32 v20,v20,v62
v_and_b32 v63,0xffff0000,v59
v_add_f32 v21,v21,v63
v_lshlrev_b32 v62,16,v60
v_add_f32 v22,v22,v62
v_and_b32 v63,0xffff0000,v60
v_add_f32 v23,v23,v63
v_lshlrev_b32 v62,16,v61
v_add_f32 v24,v24,v62
v_and_b32 v63,0xffff0000,v61
v_add_f32 v25,v25,v63
v_cvt_pk_bf16_f32 v18,v18,v19
v_cvt_pk_bf16_f32 v19,v20,v21
v_cvt_pk_bf16_f32 v20,v22,v23
v_cvt_pk_bf16_f32 v21,v24,v25
buffer_store_dwordx4 v[18:21],v0,s[36:39],s67 offen offset:0 sc0 sc1
s_mov_b32 s72,s69
s_mov_b32 s69,0x7fff0000
s_mov_b32 s67,0x7fff0000
s_mov_b32 s77,0x7fff0000
s_branch Lg4_poll_%=
Lg4_fin1_%=:
v_lshlrev_b32 v62,16,v58
v_add_f32 v18,v18,v62
v_and_b32 v63,0xffff0000,v58
v_add_f32 v19,v19,v63
v_lshlrev_b32 v62,16,v59
v_add_f32 v20,v20,v62
v_and_b32 v63,0xffff0000,v59
v_add_f32 v21,v21,v63
v_lshlrev_b32 v62,16,v60
v_add_f32 v22,v22,v62
v_and_b32 v63,0xffff0000,v60
v_add_f32 v23,v23,v63
v_lshlrev_b32 v62,16,v61
v_add_f32 v24,v24,v62
v_and_b32 v63,0xffff0000,v61
v_add_f32 v25,v25,v63
v_cvt_pk_bf16_f32 v18,v18,v19
v_cvt_pk_bf16_f32 v19,v20,v21
v_cvt_pk_bf16_f32 v20,v22,v23
v_cvt_pk_bf16_f32 v21,v24,v25
buffer_store_dwordx4 v[18:21],v0,s[36:39],s67 offen offset:0 sc0 sc1
s_mov_b32 s72,s69
s_mov_b32 s69,0x7fff0000
s_mov_b32 s67,0x7fff0000
s_mov_b32 s77,0x7fff0000
s_branch Lg4_done_%=
Lg4_Fa0_%=:
s_nop 0
v_mfma_f32_16x16x32_bf16 v[14:17],a[4:7],v[26:29],0
s_add_u32 m0,s57,0
R 46,1,5120
v_lshlrev_b32 v62,16,v58
M 14,a[12:15],30
s_add_u32 s56,s55,0
v_add_f32 v18,v18,v62
v_and_b32 v63,0xffff0000,v58
v_mfma_f32_16x16x32_bf16 v[10:13],a[0:3],v[26:29],0
s_add_u32 s61,s54,s46
R 50,1,6144
v_add_f32 v19,v19,v63
M 10,a[8:11],30
L 3
D 0,56,0
M 10,a[16:19],34
s_cmp_ge_u32 s61,s63
R 54,1,7168
v_lshlrev_b32 v62,16,v59
M 10,a[24:27],38
s_cselect_b32 s61,s93,s61
v_add_f32 v20,v20,v62
v_and_b32 v63,0xffff0000,v59
M 14,a[20:23],34
s_cselect_b32 s46,s88,s46
R 26,1,8192
v_add_f32 v21,v21,v63
M 14,a[28:31],38
L 3
v_lshlrev_b32 v62,16,v60
v_add_f32 v22,v22,v62
M 14,a[36:39],42
s_cselect_b32 s63,s89,s63
R 30,1,9216
v_and_b32 v63,0xffff0000,v60
M 14,a[44:47],46
D 0,56,1024
M 10,a[32:35],42
s_cselect_b32 s93,s50,s93
R 34,1,10240
v_add_f32 v23,v23,v63
M 10,a[40:43],46
L 3
v_lshlrev_b32 v62,16,v61
v_add_f32 v24,v24,v62
M 10,a[48:51],50
s_cselect_b32 s88,8,s88
R 38,1,11264
v_and_b32 v63,0xffff0000,v61
M 10,a[56:59],54
buffer_load_dwordx4 v[58:61],v0,s[36:39],s74 offen offset:0 sc1
M 14,a[52:55],50
s_cselect_b32 s89,-1,s89
R 42,1,12288
v_add_f32 v25,v25,v63
M 14,a[60:63],54
L 3
v_cvt_pk_bf16_f32 v18,v18,v19
v_cvt_pk_bf16_f32 v19,v20,v21
M 14,a[68:71],26
s_mul_i32 s58,s61,2560
R 46,1,13312
v_cvt_pk_bf16_f32 v20,v22,v23
M 14,a[76:79],30
D 0,56,2048
M 10,a[64:67],26
s_add_u32 s58,s58,109891584
R 50,1,14336
v_cvt_pk_bf16_f32 v21,v24,v25
M 10,a[72:75],30
L 3
v_lshrrev_b32 v62,8,v0
v_lshl_add_u32 v62,v62,5,v0
M 10,a[80:83],34
s_sub_u32 s75,s67,s85
R 54,1,15360
v_add_u32 v62,s73,v62
M 10,a[88:91],38
s_mov_b64 s[40:41],s[86:87]
ds_write_b128 v62,v[18:21] offset:0
M 14,a[84:87],34
s_mov_b32 s42,4194304
R 26,1,16384
ds_read2_b32 v[22:23],v5 offset0:0 offset1:16
M 14,a[92:95],38
L 5
s_mov_b32 s43,0x00020000
ds_read2_b32 v[24:25],v5 offset0:32 offset1:48
M 14,a[100:103],42
s_add_u32 s64,s75,0
s_lshl_b32 s62,s54,15
R 30,1,17408
M 14,a[108:111],46
D 0,56,3072
M 10,a[96:99],42
s_add_u32 m0,s57,4096
s_add_u32 s56,s56,4096
R 34,1,18432
M 10,a[104:107],46
L 6
s_mov_b32 s66,s65
s_add_u32 s74,s49,s62
M 10,a[112:115],50
s_add_u32 s65,s47,s62
s_mov_b32 s54,s61
R 38,1,19456
M 10,a[120:123],54
L 3
M 14,a[116:119],50
R 42,1,20480
M 14,a[124:127],54
L 3
D 0,56,0
M 14,a[132:135],26
R 46,1,21504
M 14,a[140:143],30
M 10,a[128:131],26
R 50,1,22528
M 10,a[136:139],30
L 3
M 10,a[144:147],34
R 54,1,23552
M 10,a[152:155],38
M 14,a[148:151],34
R 26,1,24576
M 14,a[156:159],38
L 3
D 0,56,1024
M 14,a[164:167],42
R 30,1,25600
M 14,a[172:175],46
M 10,a[160:163],42
R 34,1,26624
M 10,a[168:171],46
L 3
M 10,a[176:179],50
s_waitcnt vmcnt(8)
R 38,1,27648
M 10,a[184:187],54
s_barrier
M 14,a[180:183],50
R 42,1,28672
M 14,a[188:191],54
L 3
D 0,56,2048
M 14,a[196:199],26
s_add_u32 m0,s59,0
s_add_u32 s56,s55,28672
R 46,1,29696
M 14,a[204:207],30
s_mul_i32 s55,s61,114688
s_add_u32 s55,s55,s60
M 10,a[192:195],26
R 50,1,30720
M 10,a[200:203],30
L 3
M 10,a[208:211],34
R 54,1,31744
M 10,a[216:219],38
M 14,a[212:215],34
R 26,1,32768
M 14,a[220:223],38
L 3
buffer_load_dword v8,s[36:39],s58 offen offset:0 sc1 lds
M 14,a[228:231],42
s_add_u32 m0,s57,28672
R 30,1,33792
M 14,a[236:239],46
D 0,56,0
M 10,a[224:227],42
R 34,1,34816
M 10,a[232:235],46
L 3
M 10,a[240:243],50
R 38,1,35840
M 10,a[248:251],54
M 14,a[244:247],50
R 42,1,36864
M 14,a[252:255],54
L 3
D 0,56,1024
M 14,v[68:71],26
R 46,1,37888
M 14,v[76:79],30
M 10,v[64:67],26
R 50,1,38912
M 10,v[72:75],30
L 3
M 10,v[80:83],34
R 54,1,39936
M 10,v[88:91],38
D 0,56,2048
M 14,v[84:87],34
R 26,1,40960
M 14,v[92:95],38
L 3
M 14,v[100:103],42
R 30,1,41984
M 14,v[108:111],46
M 10,v[96:99],42
R 34,1,43008
M 10,v[104:107],46
L 3
D 0,56,3072
M 10,v[112:115],50
s_add_u32 m0,s57,32768
s_add_u32 s56,s56,4096
R 38,1,44032
M 10,v[120:123],54
s_sub_u32 s57,s84,s57
M 14,v[116:119],50
R 42,1,45056
M 14,v[124:127],54
L 3
M 14,v[132:135],26
R 46,1,46080
M 14,v[140:143],30
D 0,56,0
M 10,v[128:131],26
R 50,1,47104
M 10,v[136:139],30
L 3
M 10,v[144:147],34
R 54,1,48128
M 10,v[152:155],38
M 14,v[148:151],34
R 26,1,49152
M 14,v[156:159],38
L 3
D 0,56,1024
M 14,v[164:167],42
R 30,1,50176
M 14,v[172:175],46
M 10,v[160:163],42
R 34,1,51200
M 10,v[168:171],46
L 3
M 10,v[176:179],50
R 38,1,52224
M 10,v[184:187],54
D 0,56,2048
M 14,v[180:183],50
R 42,1,53248
M 14,v[188:191],54
L 3
M 14,v[196:199],26
R 46,1,54272
M 14,v[204:207],30
M 10,v[192:195],26
R 50,1,55296
M 10,v[200:203],30
L 3
M 10,v[208:211],34
s_waitcnt vmcnt(7)
R 54,1,56320
M 10,v[216:219],38
s_barrier
ds_read_b32 v7,v6 offset:0
M 14,v[212:215],34
R 26,2,0
M 14,v[220:223],38
L 4
M 14,v[228:231],42
R 30,2,1024
M 14,v[236:239],46
buffer_atomic_pk_add_bf16 v22,v9,s[40:43],s64 offen sc1
M 10,v[224:227],42
s_add_u32 s64,s75,8192
R 34,2,2048
M 10,v[232:235],46
L 4
buffer_atomic_pk_add_bf16 v23,v9,s[40:43],s64 offen sc1
M 10,v[240:243],50
s_add_u32 s64,s75,16384
L 3
R 38,2,3072
v_cmp_eq_u32 vcc,s53,v7
M 10,v[248:251],54
buffer_atomic_pk_add_bf16 v24,v9,s[40:43],s64 offen sc1
M 14,v[244:247],50
L 2
s_add_u32 s64,s75,24576
s_cmp_eq_u32 vcc_lo,-1
R 42,2,4096
M 14,v[252:255],54
buffer_atomic_pk_add_bf16 v25,v9,s[40:43],s64 offen sc1
s_cbranch_scc0 Lg4_Ka1_%=
Lg4_Fa1_%=:
s_nop 0
v_mfma_f32_16x16x32_bf16 v[22:25],a[4:7],v[26:29],0
s_add_u32 m0,s57,0
R 46,2,5120
v_lshlrev_b32 v62,16,v58
M 22,a[12:15],30
s_add_u32 s56,s55,0
v_add_f32 v10,v10,v62
v_and_b32 v63,0xffff0000,v58
v_mfma_f32_16x16x32_bf16 v[18:21],a[0:3],v[26:29],0
s_add_u32 s61,s54,s46
R 50,2,6144
v_add_f32 v11,v11,v63
M 18,a[8:11],30
L 3
D 0,56,0
M 18,a[16:19],34
s_cmp_ge_u32 s61,s63
R 54,2,7168
v_lshlrev_b32 v62,16,v59
M 18,a[24:27],38
s_cselect_b32 s61,s93,s61
v_add_f32 v12,v12,v62
v_and_b32 v63,0xffff0000,v59
M 22,a[20:23],34
s_cselect_b32 s46,s88,s46
R 26,2,8192
v_add_f32 v13,v13,v63
M 22,a[28:31],38
L 3
v_lshlrev_b32 v62,16,v60
v_add_f32 v14,v14,v62
M 22,a[36:39],42
s_cselect_b32 s63,s89,s63
R 30,2,9216
v_and_b32 v63,0xffff0000,v60
M 22,a[44:47],46
D 0,56,1024
M 18,a[32:35],42
s_cselect_b32 s93,s50,s93
R 34,2,10240
v_add_f32 v15,v15,v63
M 18,a[40:43],46
L 3
v_lshlrev_b32 v62,16,v61
v_add_f32 v16,v16,v62
M 18,a[48:51],50
s_cselect_b32 s88,8,s88
R 38,2,11264
v_and_b32 v63,0xffff0000,v61
M 18,a[56:59],54
buffer_load_dwordx4 v[58:61],v0,s[36:39],s74 offen offset:0 sc1
M 22,a[52:55],50
s_cselect_b32 s89,-1,s89
R 42,2,12288
v_add_f32 v17,v17,v63
M 22,a[60:63],54
L 3
v_cvt_pk_bf16_f32 v10,v10,v11
v_cvt_pk_bf16_f32 v11,v12,v13
M 22,a[68:71],26
s_mul_i32 s58,s61,2560
R 46,2,13312
v_cvt_pk_bf16_f32 v12,v14,v15
M 22,a[76:79],30
D 0,56,2048
M 18,a[64:67],26
s_add_u32 s58,s58,109891584
R 50,2,14336
v_cvt_pk_bf16_f32 v13,v16,v17
M 18,a[72:75],30
L 3
v_lshrrev_b32 v62,8,v0
v_lshl_add_u32 v62,v62,5,v0
M 18,a[80:83],34
s_sub_u32 s75,s66,s85
R 54,2,15360
v_add_u32 v62,s73,v62
M 18,a[88:91],38
s_mov_b64 s[40:41],s[86:87]
ds_write_b128 v62,v[10:13] offset:0
M 22,a[84:87],34
s_mov_b32 s42,4194304
R 26,2,16384
ds_read2_b32 v[14:15],v5 offset0:0 offset1:16
M 22,a[92:95],38
L 5
s_mov_b32 s43,0x00020000
ds_read2_b32 v[16:17],v5 offset0:32 offset1:48
M 22,a[100:103],42
s_add_u32 s64,s75,0
s_lshl_b32 s62,s54,15
R 30,2,17408
M 22,a[108:111],46
D 0,56,3072
M 18,a[96:99],42
s_add_u32 m0,s57,4096
s_add_u32 s56,s56,4096
R 34,2,18432
M 18,a[104:107],46
L 6
s_mov_b32 s67,s65
s_add_u32 s74,s49,s62
M 18,a[112:115],50
s_add_u32 s65,s47,s62
s_mov_b32 s54,s61
R 38,2,19456
M 18,a[120:123],54
L 3
M 22,a[116:119],50
R 42,2,20480
M 22,a[124:127],54
L 3
D 0,56,0
M 22,a[132:135],26
R 46,2,21504
)"
R"(M 22,a[140:143],30
M 18,a[128:131],26
R 50,2,22528
M 18,a[136:139],30
L 3
M 18,a[144:147],34
R 54,2,23552
M 18,a[152:155],38
M 22,a[148:151],34
R 26,2,24576
M 22,a[156:159],38
L 3
D 0,56,1024
M 22,a[164:167],42
R 30,2,25600
M 22,a[172:175],46
M 18,a[160:163],42
R 34,2,26624
M 18,a[168:171],46
L 3
M 18,a[176:179],50
s_waitcnt vmcnt(8)
R 38,2,27648
M 18,a[184:187],54
s_barrier
M 22,a[180:183],50
R 42,2,28672
M 22,a[188:191],54
L 3
D 0,56,2048
M 22,a[196:199],26
s_add_u32 m0,s59,1024
s_add_u32 s56,s55,28672
R 46,2,29696
M 22,a[204:207],30
s_mul_i32 s55,s61,114688
s_add_u32 s55,s55,s60
M 18,a[192:195],26
R 50,2,30720
M 18,a[200:203],30
L 3
M 18,a[208:211],34
R 54,2,31744
M 18,a[216:219],38
M 22,a[212:215],34
R 26,2,32768
M 22,a[220:223],38
L 3
buffer_load_dword v8,s[36:39],s58 offen offset:0 sc1 lds
M 22,a[228:231],42
s_add_u32 m0,s57,28672
R 30,2,33792
M 22,a[236:239],46
D 0,56,0
M 18,a[224:227],42
R 34,2,34816
M 18,a[232:235],46
L 3
M 18,a[240:243],50
R 38,2,35840
M 18,a[248:251],54
M 22,a[244:247],50
R 42,2,36864
M 22,a[252:255],54
L 3
D 0,56,1024
M 22,v[68:71],26
R 46,2,37888
M 22,v[76:79],30
M 18,v[64:67],26
R 50,2,38912
M 18,v[72:75],30
L 3
M 18,v[80:83],34
R 54,2,39936
M 18,v[88:91],38
D 0,56,2048
M 22,v[84:87],34
R 26,2,40960
M 22,v[92:95],38
L 3
M 22,v[100:103],42
R 30,2,41984
M 22,v[108:111],46
M 18,v[96:99],42
R 34,2,43008
M 18,v[104:107],46
L 3
D 0,56,3072
M 18,v[112:115],50
s_add_u32 m0,s57,32768
s_add_u32 s56,s56,4096
R 38,2,44032
M 18,v[120:123],54
s_sub_u32 s57,s84,s57
M 22,v[116:119],50
R 42,2,45056
M 22,v[124:127],54
L 3
M 22,v[132:135],26
R 46,2,46080
M 22,v[140:143],30
D 0,56,0
M 18,v[128:131],26
R 50,2,47104
M 18,v[136:139],30
L 3
M 18,v[144:147],34
R 54,2,48128
M 18,v[152:155],38
M 22,v[148:151],34
R 26,2,49152
M 22,v[156:159],38
L 3
D 0,56,1024
M 22,v[164:167],42
R 30,2,50176
M 22,v[172:175],46
M 18,v[160:163],42
R 34,2,51200
M 18,v[168:171],46
L 3
M 18,v[176:179],50
R 38,2,52224
M 18,v[184:187],54
D 0,56,2048
M 22,v[180:183],50
R 42,2,53248
M 22,v[188:191],54
L 3
M 22,v[196:199],26
R 46,2,54272
M 22,v[204:207],30
M 18,v[192:195],26
R 50,2,55296
M 18,v[200:203],30
L 3
M 18,v[208:211],34
s_waitcnt vmcnt(7)
R 54,2,56320
M 18,v[216:219],38
s_barrier
ds_read_b32 v7,v6 offset:1024
M 22,v[212:215],34
R 26,1,0
M 22,v[220:223],38
L 4
M 22,v[228:231],42
R 30,1,1024
M 22,v[236:239],46
buffer_atomic_pk_add_bf16 v14,v9,s[40:43],s64 offen sc1
M 18,v[224:227],42
s_add_u32 s64,s75,8192
R 34,1,2048
M 18,v[232:235],46
L 4
buffer_atomic_pk_add_bf16 v15,v9,s[40:43],s64 offen sc1
M 18,v[240:243],50
s_add_u32 s64,s75,16384
L 3
R 38,1,3072
v_cmp_eq_u32 vcc,s53,v7
M 18,v[248:251],54
buffer_atomic_pk_add_bf16 v16,v9,s[40:43],s64 offen sc1
M 22,v[244:247],50
L 2
s_add_u32 s64,s75,24576
s_cmp_eq_u32 vcc_lo,-1
R 42,1,4096
M 22,v[252:255],54
buffer_atomic_pk_add_bf16 v17,v9,s[40:43],s64 offen sc1
s_cbranch_scc1 Lg4_Fa0_%=
Lg4_Ka0_%=:
Lg4_La0_%=:
s_nop 0
v_mfma_f32_16x16x32_bf16 v[14:17],a[4:7],v[26:29],0
s_add_u32 m0,s59,3072
R 46,1,5120
v_lshlrev_b32 v62,16,v58
M 14,a[12:15],30
buffer_load_dword v8,s[36:39],s58 offen offset:0 sc1 lds
v_mfma_f32_16x16x32_bf16 v[10:13],a[0:3],v[26:29],0
s_sub_u32 s75,s67,s85
R 50,1,6144
v_add_f32 v18,v18,v62
M 10,a[8:11],30
L 3
v_and_b32 v63,0xffff0000,v58
v_add_f32 v19,v19,v63
M 10,a[16:19],34
s_mov_b64 s[40:41],s[86:87]
R 54,1,7168
v_lshlrev_b32 v62,16,v59
M 10,a[24:27],38
s_mov_b32 s42,4194304
v_add_f32 v20,v20,v62
v_and_b32 v63,0xffff0000,v59
M 14,a[20:23],34
s_mov_b32 s43,0x00020000
R 26,1,8192
v_add_f32 v21,v21,v63
M 14,a[28:31],38
L 3
v_lshlrev_b32 v62,16,v60
v_add_f32 v22,v22,v62
M 14,a[36:39],42
s_add_u32 s64,s75,0
R 30,1,9216
v_and_b32 v63,0xffff0000,v60
M 14,a[44:47],46
s_lshl_b32 s62,s54,15
v_add_f32 v23,v23,v63
v_lshlrev_b32 v62,16,v61
M 10,a[32:35],42
s_mov_b32 s66,s65
R 34,1,10240
v_add_f32 v24,v24,v62
M 10,a[40:43],46
L 3
v_and_b32 v63,0xffff0000,v61
v_add_f32 v25,v25,v63
M 10,a[48:51],50
s_add_u32 s65,s47,s62
R 38,1,11264
v_cvt_pk_bf16_f32 v18,v18,v19
M 10,a[56:59],54
buffer_load_dwordx4 v[58:61],v0,s[36:39],s74 offen offset:0 sc1
M 14,a[52:55],50
s_add_u32 s74,s49,s62
R 42,1,12288
v_cvt_pk_bf16_f32 v19,v20,v21
M 14,a[60:63],54
L 3
v_cvt_pk_bf16_f32 v20,v22,v23
v_cvt_pk_bf16_f32 v21,v24,v25
M 14,a[68:71],26
R 46,1,13312
v_lshrrev_b32 v62,8,v0
M 14,a[76:79],30
v_lshl_add_u32 v62,v62,5,v0
v_add_u32 v62,s73,v62
M 10,a[64:67],26
R 50,1,14336
M 10,a[72:75],30
L 3
ds_write_b128 v62,v[18:21] offset:0
M 10,a[80:83],34
R 54,1,15360
ds_read2_b32 v[22:23],v5 offset0:0 offset1:16
M 10,a[88:91],38
ds_read2_b32 v[24:25],v5 offset0:32 offset1:48
M 14,a[84:87],34
R 26,1,16384
M 14,a[92:95],38
L 6
M 14,a[100:103],42
R 30,1,17408
M 14,a[108:111],46
M 10,a[96:99],42
R 34,1,18432
M 10,a[104:107],46
L 5
L 3
M 10,a[112:115],50
R 38,1,19456
M 10,a[120:123],54
buffer_atomic_pk_add_bf16 v22,v9,s[40:43],s64 offen sc1
M 14,a[116:119],50
s_add_u32 s64,s75,8192
R 42,1,20480
M 14,a[124:127],54
L 3
buffer_atomic_pk_add_bf16 v23,v9,s[40:43],s64 offen sc1
M 14,a[132:135],26
s_add_u32 s64,s75,16384
R 46,1,21504
M 14,a[140:143],30
buffer_atomic_pk_add_bf16 v24,v9,s[40:43],s64 offen sc1
M 10,a[128:131],26
s_add_u32 s64,s75,24576
R 50,1,22528
M 10,a[136:139],30
L 3
buffer_atomic_pk_add_bf16 v25,v9,s[40:43],s64 offen sc1
M 10,a[144:147],34
R 54,1,23552
M 10,a[152:155],38
M 14,a[148:151],34
R 26,1,24576
M 14,a[156:159],38
L 3
M 14,a[164:167],42
R 30,1,25600
M 14,a[172:175],46
M 10,a[160:163],42
R 34,1,26624
M 10,a[168:171],46
L 3
M 10,a[176:179],50
s_waitcnt vmcnt(5)
R 38,1,27648
M 10,a[184:187],54
s_barrier
ds_read_b32 v7,v6 offset:3072
M 14,a[180:183],50
R 42,1,28672
M 14,a[188:191],54
L 4
M 14,a[196:199],26
R 46,1,29696
M 14,a[204:207],30
M 10,a[192:195],26
R 50,1,30720
M 10,a[200:203],30
L 4
L 3
v_cmp_eq_u32 vcc,s53,v7
M 10,a[208:211],34
R 54,1,31744
M 10,a[216:219],38
M 14,a[212:215],34
s_cmp_eq_u32 vcc_lo,-1
R 26,1,32768
s_cbranch_scc0 Lg4_Xa0_%=
M 14,a[220:223],38
L 3
s_add_u32 m0,s57,0
s_add_u32 s56,s55,0
M 14,a[228:231],42
s_add_u32 s61,s54,s46
s_cmp_ge_u32 s61,s63
s_cselect_b32 s61,s93,s61
R 30,1,33792
M 14,a[236:239],46
s_mul_i32 s58,s61,2560
D 0,56,0
M 10,a[224:227],42
s_cselect_b32 s46,s88,s46
s_cselect_b32 s63,s89,s63
R 34,1,34816
M 10,a[232:235],46
L 3
s_cselect_b32 s93,s50,s93
s_cselect_b32 s88,8,s88
M 10,a[240:243],50
s_cselect_b32 s89,-1,s89
s_add_u32 s58,s58,109891584
R 38,1,35840
M 10,a[248:251],54
D 0,56,1024
M 14,a[244:247],50
s_mov_b32 s54,s61
R 42,1,36864
M 14,a[252:255],54
L 3
M 14,v[68:71],26
R 46,1,37888
M 14,v[76:79],30
D 0,56,2048
M 10,v[64:67],26
R 50,1,38912
M 10,v[72:75],30
L 3
M 10,v[80:83],34
R 54,1,39936
M 10,v[88:91],38
D 0,56,3072
M 14,v[84:87],34
s_add_u32 m0,s57,4096
s_add_u32 s56,s56,4096
R 26,1,40960
M 14,v[92:95],38
L 3
M 14,v[100:103],42
R 30,1,41984
M 14,v[108:111],46
D 0,56,0
M 10,v[96:99],42
R 34,1,43008
M 10,v[104:107],46
L 3
M 10,v[112:115],50
R 38,1,44032
M 10,v[120:123],54
D 0,56,1024
M 14,v[116:119],50
R 42,1,45056
M 14,v[124:127],54
L 3
M 14,v[132:135],26
R 46,1,46080
M 14,v[140:143],30
D 0,56,2048
M 10,v[128:131],26
s_add_u32 m0,s59,0
s_add_u32 s56,s55,28672
R 50,1,47104
M 10,v[136:139],30
L 3
buffer_load_dword v8,s[36:39],s58 offen offset:0 sc1 lds
M 10,v[144:147],34
s_add_u32 m0,s57,28672
s_mul_i32 s55,s61,114688
R 54,1,48128
M 10,v[152:155],38
s_add_u32 s55,s55,s60
M 14,v[148:151],34
R 26,1,49152
M 14,v[156:159],38
L 3
D 0,56,0
M 14,v[164:167],42
R 30,1,50176
M 14,v[172:175],46
M 10,v[160:163],42
R 34,1,51200
M 10,v[168:171],46
L 3
D 0,56,1024
M 10,v[176:179],50
R 38,1,52224
M 10,v[184:187],54
D 0,56,2048
M 14,v[180:183],50
R 42,1,53248
M 14,v[188:191],54
L 3
M 14,v[196:199],26
R 46,1,54272
M 14,v[204:207],30
D 0,56,3072
M 10,v[192:195],26
s_add_u32 m0,s57,32768
s_add_u32 s56,s56,4096
R 50,1,55296
M 10,v[200:203],30
L 3
s_sub_u32 s57,s84,s57
M 10,v[208:211],34
s_waitcnt vmcnt(4)
R 54,1,56320
M 10,v[216:219],38
s_barrier
ds_read_b32 v7,v6 offset:0
M 14,v[212:215],34
R 26,2,0
M 14,v[220:223],38
L 4
D 0,56,0
M 14,v[228:231],42
R 30,2,1024
M 14,v[236:239],46
D 0,56,1024
M 10,v[224:227],42
R 34,2,2048
M 10,v[232:235],46
L 4
D 0,56,2048
M 10,v[240:243],50
L 3
R 38,2,3072
v_cmp_eq_u32 vcc,s53,v7
M 10,v[248:251],54
M 14,v[244:247],50
L 2
s_cmp_eq_u32 vcc_lo,-1
R 42,2,4096
M 14,v[252:255],54
buffer_store_dword v4,v3,s[36:39],s71 offen offset:0 sc0 sc1
buffer_store_dword v4,v3,s[36:39],s71 offen offset:0 sc0 sc1
buffer_store_dword v4,v3,s[36:39],s71 offen offset:0 sc0 sc1
buffer_store_dword v4,v3,s[36:39],s71 offen offset:0 sc0 sc1
s_cbranch_scc1 Lg4_Fa1_%=
s_branch Lg4_Ka1_%=
Lg4_Xa0_%=:
M 14,a[220:223],38
L 3
s_add_u32 m0,s59,0
M 14,a[228:231],42
R 30,1,33792
M 14,a[236:239],46
buffer_load_dword v8,s[36:39],s58 offen offset:0 sc1 lds
M 10,a[224:227],42
R 34,1,34816
M 10,a[232:235],46
L 3
M 10,a[240:243],50
R 38,1,35840
M 10,a[248:251],54
M 14,a[244:247],50
R 42,1,36864
M 14,a[252:255],54
L 3
M 14,v[68:71],26
R 46,1,37888
M 14,v[76:79],30
M 10,v[64:67],26
R 50,1,38912
M 10,v[72:75],30
L 3
)"
R"(M 10,v[80:83],34
R 54,1,39936
M 10,v[88:91],38
M 14,v[84:87],34
R 26,1,40960
M 14,v[92:95],38
L 3
M 14,v[100:103],42
R 30,1,41984
M 14,v[108:111],46
M 10,v[96:99],42
R 34,1,43008
M 10,v[104:107],46
L 3
M 10,v[112:115],50
R 38,1,44032
M 10,v[120:123],54
M 14,v[116:119],50
R 42,1,45056
M 14,v[124:127],54
L 3
M 14,v[132:135],26
R 46,1,46080
M 14,v[140:143],30
M 10,v[128:131],26
R 50,1,47104
M 10,v[136:139],30
L 3
M 10,v[144:147],34
R 54,1,48128
M 10,v[152:155],38
M 14,v[148:151],34
R 26,1,49152
M 14,v[156:159],38
L 3
M 14,v[164:167],42
R 30,1,50176
M 14,v[172:175],46
M 10,v[160:163],42
R 34,1,51200
M 10,v[168:171],46
L 3
M 10,v[176:179],50
R 38,1,52224
M 10,v[184:187],54
M 14,v[180:183],50
R 42,1,53248
M 14,v[188:191],54
L 3
M 14,v[196:199],26
R 46,1,54272
M 14,v[204:207],30
M 10,v[192:195],26
R 50,1,55296
M 10,v[200:203],30
L 3
M 10,v[208:211],34
s_waitcnt vmcnt(0)
R 54,1,56320
M 10,v[216:219],38
s_barrier
ds_read_b32 v7,v6 offset:0
M 14,v[212:215],34
M 14,v[220:223],38
L 3
M 14,v[228:231],42
M 14,v[236:239],46
M 10,v[224:227],42
M 10,v[232:235],46
L 1
L 0
M 10,v[240:243],50
v_cmp_eq_u32 vcc,s53,v7
M 10,v[248:251],54
M 14,v[244:247],50
M 14,v[252:255],54
Lg4_pa0_%=:
s_mov_b32 s80,1
s_cmp_eq_u32 vcc_lo,-1
s_cbranch_scc0 Lg4_xna0_%=
s_add_u32 m0,s57,0
s_add_u32 s56,s55,0
D 0,56,0
s_sub_u32 s75,s66,s85
v_lshlrev_b32 v62,16,v58
D 0,56,1024
v_add_f32 v10,v10,v62
D 0,56,2048
v_and_b32 v63,0xffff0000,v58
D 0,56,3072
v_add_f32 v11,v11,v63
s_add_u32 m0,s57,4096
s_add_u32 s56,s56,4096
D 0,56,0
v_lshlrev_b32 v62,16,v59
D 0,56,1024
v_add_f32 v12,v12,v62
D 0,56,2048
v_and_b32 v63,0xffff0000,v59
s_add_u32 s61,s54,s46
s_cmp_ge_u32 s61,s63
s_cselect_b32 s61,s93,s61
s_cselect_b32 s46,s88,s46
s_cselect_b32 s63,s89,s63
s_cselect_b32 s93,s50,s93
s_cselect_b32 s88,8,s88
s_cselect_b32 s89,-1,s89
s_mul_i32 s58,s61,2560
s_add_u32 s58,s58,109891584
s_add_u32 m0,s57,28672
s_add_u32 s56,s55,28672
D 0,56,0
v_add_f32 v13,v13,v63
D 0,56,1024
v_lshlrev_b32 v62,16,v60
D 0,56,2048
v_add_f32 v14,v14,v62
D 0,56,3072
v_and_b32 v63,0xffff0000,v60
s_add_u32 m0,s57,32768
s_add_u32 s56,s56,4096
D 0,56,0
v_add_f32 v15,v15,v63
D 0,56,1024
v_lshlrev_b32 v62,16,v61
D 0,56,2048
v_add_f32 v16,v16,v62
s_mul_i32 s55,s61,114688
s_add_u32 s55,s55,s60
s_sub_u32 s57,s84,s57
s_mov_b32 s54,s61
v_and_b32 v63,0xffff0000,v61
v_add_f32 v17,v17,v63
v_cvt_pk_bf16_f32 v10,v10,v11
v_cvt_pk_bf16_f32 v11,v12,v13
v_cvt_pk_bf16_f32 v12,v14,v15
v_cvt_pk_bf16_f32 v13,v16,v17
v_lshrrev_b32 v62,8,v0
v_lshl_add_u32 v62,v62,5,v0
v_add_u32 v62,s73,v62
ds_write_b128 v62,v[10:13] offset:0
ds_read2_b32 v[14:15],v5 offset0:0 offset1:16
ds_read2_b32 v[16:17],v5 offset0:32 offset1:48
s_mov_b64 s[40:41],s[86:87]
s_mov_b32 s42,4194304
s_mov_b32 s43,0x00020000
L 0
s_add_u32 s64,s75,0
buffer_atomic_pk_add_bf16 v14,v9,s[40:43],s64 offen sc1
s_add_u32 s64,s75,8192
buffer_atomic_pk_add_bf16 v15,v9,s[40:43],s64 offen sc1
s_add_u32 s64,s75,16384
buffer_atomic_pk_add_bf16 v16,v9,s[40:43],s64 offen sc1
s_add_u32 s64,s75,24576
buffer_atomic_pk_add_bf16 v17,v9,s[40:43],s64 offen sc1
s_mov_b32 s72,0x7fff0000
s_mov_b32 s69,0x7fff0000
s_mov_b32 s66,0x7fff0000
s_mov_b32 s76,0x7fff0000
s_branch Lg4_gox_%=
Lg4_xna0_%=:
s_cmp_ge_u32 s54,256
s_cbranch_scc1 Lg4_fina0_%=
s_add_u32 m0,s59,4096
s_nop 0
buffer_load_dword v8,s[36:39],s58 offen offset:0 sc1 lds
buffer_store_dword v4,v3,s[36:39],s71 offen offset:0 sc0 sc1
s_add_u32 m0,s59,5120
s_nop 0
buffer_load_dword v8,s[36:39],s58 offen offset:0 sc1 lds
buffer_store_dword v4,v3,s[36:39],s71 offen offset:0 sc0 sc1
s_mov_b32 s83,0
s_sub_u32 s75,s66,s85
v_lshlrev_b32 v62,16,v58
v_add_f32 v10,v10,v62
v_and_b32 v63,0xffff0000,v58
v_add_f32 v11,v11,v63
v_lshlrev_b32 v62,16,v59
v_add_f32 v12,v12,v62
v_and_b32 v63,0xffff0000,v59
v_add_f32 v13,v13,v63
v_lshlrev_b32 v62,16,v60
v_add_f32 v14,v14,v62
v_and_b32 v63,0xffff0000,v60
v_add_f32 v15,v15,v63
v_lshlrev_b32 v62,16,v61
v_add_f32 v16,v16,v62
v_and_b32 v63,0xffff0000,v61
v_add_f32 v17,v17,v63
v_cvt_pk_bf16_f32 v10,v10,v11
v_cvt_pk_bf16_f32 v11,v12,v13
v_cvt_pk_bf16_f32 v12,v14,v15
v_cvt_pk_bf16_f32 v13,v16,v17
v_lshrrev_b32 v62,8,v0
v_lshl_add_u32 v62,v62,5,v0
v_add_u32 v62,s73,v62
ds_write_b128 v62,v[10:13] offset:0
ds_read2_b32 v[14:15],v5 offset0:0 offset1:16
ds_read2_b32 v[16:17],v5 offset0:32 offset1:48
s_mov_b64 s[40:41],s[86:87]
s_mov_b32 s42,4194304
s_mov_b32 s43,0x00020000
L 0
s_add_u32 s64,s75,0
buffer_atomic_pk_add_bf16 v14,v9,s[40:43],s64 offen sc1
s_add_u32 s64,s75,8192
buffer_atomic_pk_add_bf16 v15,v9,s[40:43],s64 offen sc1
s_add_u32 s64,s75,16384
buffer_atomic_pk_add_bf16 v16,v9,s[40:43],s64 offen sc1
s_add_u32 s64,s75,24576
buffer_atomic_pk_add_bf16 v17,v9,s[40:43],s64 offen sc1
s_mov_b32 s72,0x7fff0000
s_mov_b32 s69,0x7fff0000
s_mov_b32 s66,0x7fff0000
s_mov_b32 s76,0x7fff0000
s_branch Lg4_poll_%=
Lg4_fina0_%=:
s_sub_u32 s75,s66,s85
v_lshlrev_b32 v62,16,v58
v_add_f32 v10,v10,v62
v_and_b32 v63,0xffff0000,v58
v_add_f32 v11,v11,v63
v_lshlrev_b32 v62,16,v59
v_add_f32 v12,v12,v62
v_and_b32 v63,0xffff0000,v59
v_add_f32 v13,v13,v63
v_lshlrev_b32 v62,16,v60
v_add_f32 v14,v14,v62
v_and_b32 v63,0xffff0000,v60
v_add_f32 v15,v15,v63
v_lshlrev_b32 v62,16,v61
v_add_f32 v16,v16,v62
v_and_b32 v63,0xffff0000,v61
v_add_f32 v17,v17,v63
v_cvt_pk_bf16_f32 v10,v10,v11
v_cvt_pk_bf16_f32 v11,v12,v13
v_cvt_pk_bf16_f32 v12,v14,v15
v_cvt_pk_bf16_f32 v13,v16,v17
v_lshrrev_b32 v62,8,v0
v_lshl_add_u32 v62,v62,5,v0
v_add_u32 v62,s73,v62
ds_write_b128 v62,v[10:13] offset:0
ds_read2_b32 v[14:15],v5 offset0:0 offset1:16
ds_read2_b32 v[16:17],v5 offset0:32 offset1:48
s_mov_b64 s[40:41],s[86:87]
s_mov_b32 s42,4194304
s_mov_b32 s43,0x00020000
L 0
s_add_u32 s64,s75,0
buffer_atomic_pk_add_bf16 v14,v9,s[40:43],s64 offen sc1
s_add_u32 s64,s75,8192
buffer_atomic_pk_add_bf16 v15,v9,s[40:43],s64 offen sc1
s_add_u32 s64,s75,16384
buffer_atomic_pk_add_bf16 v16,v9,s[40:43],s64 offen sc1
s_add_u32 s64,s75,24576
buffer_atomic_pk_add_bf16 v17,v9,s[40:43],s64 offen sc1
s_mov_b32 s72,0x7fff0000
s_mov_b32 s69,0x7fff0000
s_mov_b32 s66,0x7fff0000
s_mov_b32 s76,0x7fff0000
s_branch Lg4_done_%=
Lg4_Ka1_%=:
Lg4_La1_%=:
s_nop 0
v_mfma_f32_16x16x32_bf16 v[22:25],a[4:7],v[26:29],0
s_add_u32 m0,s59,3072
R 46,2,5120
v_lshlrev_b32 v62,16,v58
M 22,a[12:15],30
buffer_load_dword v8,s[36:39],s58 offen offset:0 sc1 lds
v_mfma_f32_16x16x32_bf16 v[18:21],a[0:3],v[26:29],0
s_sub_u32 s75,s66,s85
R 50,2,6144
v_add_f32 v10,v10,v62
M 18,a[8:11],30
L 3
v_and_b32 v63,0xffff0000,v58
v_add_f32 v11,v11,v63
M 18,a[16:19],34
s_mov_b64 s[40:41],s[86:87]
R 54,2,7168
v_lshlrev_b32 v62,16,v59
M 18,a[24:27],38
s_mov_b32 s42,4194304
v_add_f32 v12,v12,v62
v_and_b32 v63,0xffff0000,v59
M 22,a[20:23],34
s_mov_b32 s43,0x00020000
R 26,2,8192
v_add_f32 v13,v13,v63
M 22,a[28:31],38
L 3
v_lshlrev_b32 v62,16,v60
v_add_f32 v14,v14,v62
M 22,a[36:39],42
s_add_u32 s64,s75,0
R 30,2,9216
v_and_b32 v63,0xffff0000,v60
M 22,a[44:47],46
s_lshl_b32 s62,s54,15
v_add_f32 v15,v15,v63
v_lshlrev_b32 v62,16,v61
M 18,a[32:35],42
s_mov_b32 s67,s65
R 34,2,10240
v_add_f32 v16,v16,v62
M 18,a[40:43],46
L 3
v_and_b32 v63,0xffff0000,v61
v_add_f32 v17,v17,v63
M 18,a[48:51],50
s_add_u32 s65,s47,s62
R 38,2,11264
v_cvt_pk_bf16_f32 v10,v10,v11
M 18,a[56:59],54
buffer_load_dwordx4 v[58:61],v0,s[36:39],s74 offen offset:0 sc1
M 22,a[52:55],50
s_add_u32 s74,s49,s62
R 42,2,12288
v_cvt_pk_bf16_f32 v11,v12,v13
M 22,a[60:63],54
L 3
v_cvt_pk_bf16_f32 v12,v14,v15
v_cvt_pk_bf16_f32 v13,v16,v17
M 22,a[68:71],26
R 46,2,13312
v_lshrrev_b32 v62,8,v0
M 22,a[76:79],30
v_lshl_add_u32 v62,v62,5,v0
v_add_u32 v62,s73,v62
M 18,a[64:67],26
R 50,2,14336
M 18,a[72:75],30
L 3
ds_write_b128 v62,v[10:13] offset:0
M 18,a[80:83],34
R 54,2,15360
ds_read2_b32 v[14:15],v5 offset0:0 offset1:16
M 18,a[88:91],38
ds_read2_b32 v[16:17],v5 offset0:32 offset1:48
M 22,a[84:87],34
R 26,2,16384
M 22,a[92:95],38
L 6
M 22,a[100:103],42
R 30,2,17408
M 22,a[108:111],46
M 18,a[96:99],42
R 34,2,18432
M 18,a[104:107],46
L 5
L 3
M 18,a[112:115],50
R 38,2,19456
M 18,a[120:123],54
buffer_atomic_pk_add_bf16 v14,v9,s[40:43],s64 offen sc1
M 22,a[116:119],50
s_add_u32 s64,s75,8192
R 42,2,20480
M 22,a[124:127],54
L 3
buffer_atomic_pk_add_bf16 v15,v9,s[40:43],s64 offen sc1
M 22,a[132:135],26
s_add_u32 s64,s75,16384
R 46,2,21504
M 22,a[140:143],30
buffer_atomic_pk_add_bf16 v16,v9,s[40:43],s64 offen sc1
M 18,a[128:131],26
s_add_u32 s64,s75,24576
R 50,2,22528
M 18,a[136:139],30
L 3
buffer_atomic_pk_add_bf16 v17,v9,s[40:43],s64 offen sc1
M 18,a[144:147],34
R 54,2,23552
M 18,a[152:155],38
M 22,a[148:151],34
R 26,2,24576
M 22,a[156:159],38
L 3
M 22,a[164:167],42
R 30,2,25600
M 22,a[172:175],46
M 18,a[160:163],42
R 34,2,26624
M 18,a[168:171],46
L 3
M 18,a[176:179],50
s_waitcnt vmcnt(5)
R 38,2,27648
M 18,a[184:187],54
s_barrier
ds_read_b32 v7,v6 offset:3072
M 22,a[180:183],50
R 42,2,28672
M 22,a[188:191],54
L 4
M 22,a[196:199],26
R 46,2,29696
M 22,a[204:207],30
M 18,a[192:195],26
R 50,2,30720
M 18,a[200:203],30
L 4
L 3
v_cmp_eq_u32 vcc,s53,v7
M 18,a[208:211],34
R 54,2,31744
M 18,a[216:219],38
M 22,a[212:215],34
s_cmp_eq_u32 vcc_lo,-1
R 26,2,32768
s_cbranch_scc0 Lg4_Xa1_%=
M 22,a[220:223],38
L 3
s_add_u32 m0,s57,0
s_add_u32 s56,s55,0
M 22,a[228:231],42
s_add_u32 s61,s54,s46
s_cmp_ge_u32 s61,s63
s_cselect_b32 s61,s93,s61
R 30,2,33792
M 22,a[236:239],46
s_mul_i32 s58,s61,2560
D 0,56,0
M 18,a[224:227],42
s_cselect_b32 s46,s88,s46
s_cselect_b32 s63,s89,s63
R 34,2,34816
M 18,a[232:235],46
L 3
s_cselect_b32 s93,s50,s93
s_cselect_b32 s88,8,s88
M 18,a[240:243],50
s_cselect_b32 s89,-1,s89
s_add_u32 s58,s58,109891584
R 38,2,35840
M 18,a[248:251],54
D 0,56,1024
M 22,a[244:247],50
s_mov_b32 s54,s61
R 42,2,36864
M 22,a[252:255],54
L 3
M 22,v[68:71],26
R 46,2,37888
M 22,v[76:79],30
D 0,56,2048
M 18,v[64:67],26
R 50,2,38912
M 18,v[72:75],30
L 3
M 18,v[80:83],34
R 54,2,39936
M 18,v[88:91],38
D 0,56,3072
M 22,v[84:87],34
s_add_u32 m0,s57,4096
s_add_u32 s56,s56,4096
R 26,2,40960
M 22,v[92:95],38
L 3
M 22,v[100:103],42
R 30,2,41984
M 22,v[108:111],46
D 0,56,0
M 18,v[96:99],42
R 34,2,43008
M 18,v[104:107],46
L 3
M 18,v[112:115],50
R 38,2,44032
M 18,v[120:123],54
D 0,56,1024
M 22,v[116:119],50
R 42,2,45056
M 22,v[124:127],54
L 3
M 22,v[132:135],26
R 46,2,46080
M 22,v[140:143],30
D 0,56,2048
M 18,v[128:131],26
s_add_u32 m0,s59,1024
s_add_u32 s56,s55,28672
R 50,2,47104
M 18,v[136:139],30
L 3
buffer_load_dword v8,s[36:39],s58 offen offset:0 sc1 lds
M 18,v[144:147],34
s_add_u32 m0,s57,28672
s_mul_i32 s55,s61,114688
R 54,2,48128
M 18,v[152:155],38
s_add_u32 s55,s55,s60
M 22,v[148:151],34
R 26,2,49152
M 22,v[156:159],38
L 3
D 0,56,0
)"
R"(M 22,v[164:167],42
R 30,2,50176
M 22,v[172:175],46
M 18,v[160:163],42
R 34,2,51200
M 18,v[168:171],46
L 3
D 0,56,1024
M 18,v[176:179],50
R 38,2,52224
M 18,v[184:187],54
D 0,56,2048
M 22,v[180:183],50
R 42,2,53248
M 22,v[188:191],54
L 3
M 22,v[196:199],26
R 46,2,54272
M 22,v[204:207],30
D 0,56,3072
M 18,v[192:195],26
s_add_u32 m0,s57,32768
s_add_u32 s56,s56,4096
R 50,2,55296
M 18,v[200:203],30
L 3
s_sub_u32 s57,s84,s57
M 18,v[208:211],34
s_waitcnt vmcnt(4)
R 54,2,56320
M 18,v[216:219],38
s_barrier
ds_read_b32 v7,v6 offset:1024
M 22,v[212:215],34
R 26,1,0
M 22,v[220:223],38
L 4
D 0,56,0
M 22,v[228:231],42
R 30,1,1024
M 22,v[236:239],46
D 0,56,1024
M 18,v[224:227],42
R 34,1,2048
M 18,v[232:235],46
L 4
D 0,56,2048
M 18,v[240:243],50
L 3
R 38,1,3072
v_cmp_eq_u32 vcc,s53,v7
M 18,v[248:251],54
M 22,v[244:247],50
L 2
s_cmp_eq_u32 vcc_lo,-1
R 42,1,4096
M 22,v[252:255],54
buffer_store_dword v4,v3,s[36:39],s71 offen offset:0 sc0 sc1
buffer_store_dword v4,v3,s[36:39],s71 offen offset:0 sc0 sc1
buffer_store_dword v4,v3,s[36:39],s71 offen offset:0 sc0 sc1
buffer_store_dword v4,v3,s[36:39],s71 offen offset:0 sc0 sc1
s_cbranch_scc1 Lg4_Fa0_%=
s_branch Lg4_Ka0_%=
Lg4_Xa1_%=:
M 22,a[220:223],38
L 3
s_add_u32 m0,s59,1024
M 22,a[228:231],42
R 30,2,33792
M 22,a[236:239],46
buffer_load_dword v8,s[36:39],s58 offen offset:0 sc1 lds
M 18,a[224:227],42
R 34,2,34816
M 18,a[232:235],46
L 3
M 18,a[240:243],50
R 38,2,35840
M 18,a[248:251],54
M 22,a[244:247],50
R 42,2,36864
M 22,a[252:255],54
L 3
M 22,v[68:71],26
R 46,2,37888
M 22,v[76:79],30
M 18,v[64:67],26
R 50,2,38912
M 18,v[72:75],30
L 3
M 18,v[80:83],34
R 54,2,39936
M 18,v[88:91],38
M 22,v[84:87],34
R 26,2,40960
M 22,v[92:95],38
L 3
M 22,v[100:103],42
R 30,2,41984
M 22,v[108:111],46
M 18,v[96:99],42
R 34,2,43008
M 18,v[104:107],46
L 3
M 18,v[112:115],50
R 38,2,44032
M 18,v[120:123],54
M 22,v[116:119],50
R 42,2,45056
M 22,v[124:127],54
L 3
M 22,v[132:135],26
R 46,2,46080
M 22,v[140:143],30
M 18,v[128:131],26
R 50,2,47104
M 18,v[136:139],30
L 3
M 18,v[144:147],34
R 54,2,48128
M 18,v[152:155],38
M 22,v[148:151],34
R 26,2,49152
M 22,v[156:159],38
L 3
M 22,v[164:167],42
R 30,2,50176
M 22,v[172:175],46
M 18,v[160:163],42
R 34,2,51200
M 18,v[168:171],46
L 3
M 18,v[176:179],50
R 38,2,52224
M 18,v[184:187],54
M 22,v[180:183],50
R 42,2,53248
M 22,v[188:191],54
L 3
M 22,v[196:199],26
R 46,2,54272
M 22,v[204:207],30
M 18,v[192:195],26
R 50,2,55296
M 18,v[200:203],30
L 3
M 18,v[208:211],34
s_waitcnt vmcnt(0)
R 54,2,56320
M 18,v[216:219],38
s_barrier
ds_read_b32 v7,v6 offset:1024
M 22,v[212:215],34
M 22,v[220:223],38
L 3
M 22,v[228:231],42
M 22,v[236:239],46
M 18,v[224:227],42
M 18,v[232:235],46
L 1
L 0
M 18,v[240:243],50
v_cmp_eq_u32 vcc,s53,v7
M 18,v[248:251],54
M 22,v[244:247],50
M 22,v[252:255],54
Lg4_pa1_%=:
s_mov_b32 s80,0
s_cmp_eq_u32 vcc_lo,-1
s_cbranch_scc0 Lg4_xna1_%=
s_add_u32 m0,s57,0
s_add_u32 s56,s55,0
D 0,56,0
s_sub_u32 s75,s67,s85
v_lshlrev_b32 v62,16,v58
D 0,56,1024
v_add_f32 v18,v18,v62
D 0,56,2048
v_and_b32 v63,0xffff0000,v58
D 0,56,3072
v_add_f32 v19,v19,v63
s_add_u32 m0,s57,4096
s_add_u32 s56,s56,4096
D 0,56,0
v_lshlrev_b32 v62,16,v59
D 0,56,1024
v_add_f32 v20,v20,v62
D 0,56,2048
v_and_b32 v63,0xffff0000,v59
s_add_u32 s61,s54,s46
s_cmp_ge_u32 s61,s63
s_cselect_b32 s61,s93,s61
s_cselect_b32 s46,s88,s46
s_cselect_b32 s63,s89,s63
s_cselect_b32 s93,s50,s93
s_cselect_b32 s88,8,s88
s_cselect_b32 s89,-1,s89
s_mul_i32 s58,s61,2560
s_add_u32 s58,s58,109891584
s_add_u32 m0,s57,28672
s_add_u32 s56,s55,28672
D 0,56,0
v_add_f32 v21,v21,v63
D 0,56,1024
v_lshlrev_b32 v62,16,v60
D 0,56,2048
v_add_f32 v22,v22,v62
D 0,56,3072
v_and_b32 v63,0xffff0000,v60
s_add_u32 m0,s57,32768
s_add_u32 s56,s56,4096
D 0,56,0
v_add_f32 v23,v23,v63
D 0,56,1024
v_lshlrev_b32 v62,16,v61
D 0,56,2048
v_add_f32 v24,v24,v62
s_mul_i32 s55,s61,114688
s_add_u32 s55,s55,s60
s_sub_u32 s57,s84,s57
s_mov_b32 s54,s61
v_and_b32 v63,0xffff0000,v61
v_add_f32 v25,v25,v63
v_cvt_pk_bf16_f32 v18,v18,v19
v_cvt_pk_bf16_f32 v19,v20,v21
v_cvt_pk_bf16_f32 v20,v22,v23
v_cvt_pk_bf16_f32 v21,v24,v25
v_lshrrev_b32 v62,8,v0
v_lshl_add_u32 v62,v62,5,v0
v_add_u32 v62,s73,v62
ds_write_b128 v62,v[18:21] offset:0
ds_read2_b32 v[22:23],v5 offset0:0 offset1:16
ds_read2_b32 v[24:25],v5 offset0:32 offset1:48
s_mov_b64 s[40:41],s[86:87]
s_mov_b32 s42,4194304
s_mov_b32 s43,0x00020000
L 0
s_add_u32 s64,s75,0
buffer_atomic_pk_add_bf16 v22,v9,s[40:43],s64 offen sc1
s_add_u32 s64,s75,8192
buffer_atomic_pk_add_bf16 v23,v9,s[40:43],s64 offen sc1
s_add_u32 s64,s75,16384
buffer_atomic_pk_add_bf16 v24,v9,s[40:43],s64 offen sc1
s_add_u32 s64,s75,24576
buffer_atomic_pk_add_bf16 v25,v9,s[40:43],s64 offen sc1
s_mov_b32 s72,0x7fff0000
s_mov_b32 s69,0x7fff0000
s_mov_b32 s67,0x7fff0000
s_mov_b32 s77,0x7fff0000
s_branch Lg4_gox_%=
Lg4_xna1_%=:
s_cmp_ge_u32 s54,256
s_cbranch_scc1 Lg4_fina1_%=
s_add_u32 m0,s59,4096
s_nop 0
buffer_load_dword v8,s[36:39],s58 offen offset:0 sc1 lds
buffer_store_dword v4,v3,s[36:39],s71 offen offset:0 sc0 sc1
s_add_u32 m0,s59,5120
s_nop 0
buffer_load_dword v8,s[36:39],s58 offen offset:0 sc1 lds
buffer_store_dword v4,v3,s[36:39],s71 offen offset:0 sc0 sc1
s_mov_b32 s83,0
s_sub_u32 s75,s67,s85
v_lshlrev_b32 v62,16,v58
v_add_f32 v18,v18,v62
v_and_b32 v63,0xffff0000,v58
v_add_f32 v19,v19,v63
v_lshlrev_b32 v62,16,v59
v_add_f32 v20,v20,v62
v_and_b32 v63,0xffff0000,v59
v_add_f32 v21,v21,v63
v_lshlrev_b32 v62,16,v60
v_add_f32 v22,v22,v62
v_and_b32 v63,0xffff0000,v60
v_add_f32 v23,v23,v63
v_lshlrev_b32 v62,16,v61
v_add_f32 v24,v24,v62
v_and_b32 v63,0xffff0000,v61
v_add_f32 v25,v25,v63
v_cvt_pk_bf16_f32 v18,v18,v19
v_cvt_pk_bf16_f32 v19,v20,v21
v_cvt_pk_bf16_f32 v20,v22,v23
v_cvt_pk_bf16_f32 v21,v24,v25
v_lshrrev_b32 v62,8,v0
v_lshl_add_u32 v62,v62,5,v0
v_add_u32 v62,s73,v62
ds_write_b128 v62,v[18:21] offset:0
ds_read2_b32 v[22:23],v5 offset0:0 offset1:16
ds_read2_b32 v[24:25],v5 offset0:32 offset1:48
s_mov_b64 s[40:41],s[86:87]
s_mov_b32 s42,4194304
s_mov_b32 s43,0x00020000
L 0
s_add_u32 s64,s75,0
buffer_atomic_pk_add_bf16 v22,v9,s[40:43],s64 offen sc1
s_add_u32 s64,s75,8192
buffer_atomic_pk_add_bf16 v23,v9,s[40:43],s64 offen sc1
s_add_u32 s64,s75,16384
buffer_atomic_pk_add_bf16 v24,v9,s[40:43],s64 offen sc1
s_add_u32 s64,s75,24576
buffer_atomic_pk_add_bf16 v25,v9,s[40:43],s64 offen sc1
s_mov_b32 s72,0x7fff0000
s_mov_b32 s69,0x7fff0000
s_mov_b32 s67,0x7fff0000
s_mov_b32 s77,0x7fff0000
s_branch Lg4_poll_%=
Lg4_fina1_%=:
s_sub_u32 s75,s67,s85
v_lshlrev_b32 v62,16,v58
v_add_f32 v18,v18,v62
v_and_b32 v63,0xffff0000,v58
v_add_f32 v19,v19,v63
v_lshlrev_b32 v62,16,v59
v_add_f32 v20,v20,v62
v_and_b32 v63,0xffff0000,v59
v_add_f32 v21,v21,v63
v_lshlrev_b32 v62,16,v60
v_add_f32 v22,v22,v62
v_and_b32 v63,0xffff0000,v60
v_add_f32 v23,v23,v63
v_lshlrev_b32 v62,16,v61
v_add_f32 v24,v24,v62
v_and_b32 v63,0xffff0000,v61
v_add_f32 v25,v25,v63
v_cvt_pk_bf16_f32 v18,v18,v19
v_cvt_pk_bf16_f32 v19,v20,v21
v_cvt_pk_bf16_f32 v20,v22,v23
v_cvt_pk_bf16_f32 v21,v24,v25
v_lshrrev_b32 v62,8,v0
v_lshl_add_u32 v62,v62,5,v0
v_add_u32 v62,s73,v62
ds_write_b128 v62,v[18:21] offset:0
ds_read2_b32 v[22:23],v5 offset0:0 offset1:16
ds_read2_b32 v[24:25],v5 offset0:32 offset1:48
s_mov_b64 s[40:41],s[86:87]
s_mov_b32 s42,4194304
s_mov_b32 s43,0x00020000
L 0
s_add_u32 s64,s75,0
buffer_atomic_pk_add_bf16 v22,v9,s[40:43],s64 offen sc1
s_add_u32 s64,s75,8192
buffer_atomic_pk_add_bf16 v23,v9,s[40:43],s64 offen sc1
s_add_u32 s64,s75,16384
buffer_atomic_pk_add_bf16 v24,v9,s[40:43],s64 offen sc1
s_add_u32 s64,s75,24576
buffer_atomic_pk_add_bf16 v25,v9,s[40:43],s64 offen sc1
s_mov_b32 s72,0x7fff0000
s_mov_b32 s69,0x7fff0000
s_mov_b32 s67,0x7fff0000
s_mov_b32 s77,0x7fff0000
s_branch Lg4_done_%=
Lg4_done_%=:
s_nop 0
s_barrier
buffer_store_dword v4,v3,s[36:39],s70 offen offset:0 sc0 sc1
s_waitcnt vmcnt(1)
s_barrier
buffer_store_dword v4,v3,s[36:39],s72 offen offset:0 sc0 sc1
s_waitcnt vmcnt(0) lgkmcnt(0)
s_barrier
s_endpgm
Lcode_g4_e:
.purgem M
.purgem R
.purgem T
.purgem D
.purgem L
)"
  :: "s"(ws), "s"(__builtin_amdgcn_readfirstlane(sWave)), "s"(__builtin_amdgcn_readfirstlane(sG0)), "s"(__builtin_amdgcn_readfirstlane(sStep)), "s"(__builtin_amdgcn_readfirstlane(sOutOff)), "s"(__builtin_amdgcn_readfirstlane(sFoutOff)), "s"(__builtin_amdgcn_readfirstlane(sAuxOff)), "s"(__builtin_amdgcn_readfirstlane(sWw)), "s"(__builtin_amdgcn_readfirstlane(sBuf)), "s"(__builtin_amdgcn_readfirstlane(sRk)), "s"(__builtin_amdgcn_readfirstlane(sExp)), "v"(vL16), "v"(vFlag), "v"(vSnapOff), "v"(vSnapRd), "v"(vWd0), "v"(vWd1), "v"(vWr0), "v"(vWr1), "v"(vSp), "v"(vSn), "s"(w4), "s"(out) : "v255", "v0", "v1", "v2", "v3", "v4", "v5", "v6", "v7", "v8", "v9", "v10", "v11", "v12", "v13", "v14", "v15", "v16", "v17", "v18", "v19", "v20", "v21", "v22", "v23", "v24", "v25", "v26", "v27", "v28", "v29", "v30", "v31", "v32", "v33", "v34", "v35", "v36", "v37", "v38", "v39", "v40", "v41", "v42", "v43", "v44", "v45", "v46", "v47", "v48", "v49", "v50", "v51", "v52", "v53", "v54", "v55", "v56", "v57", "v58", "v59", "v60", "v61", "v62", "v63", "v64", "v65", "v66", "v67", "v68", "v69", "v70", "v71", "v72", "v73", "v74", "v75", "v76", "v77", "v78", "v79", "v80", "v81", "v82", "v83", "v84", "v85", "v86", "v87", "v88", "v89", "v90", "v91", "v92", "v93", "v94", "v95", "v96", "v97", "v98", "v99", "v100", "v101", "v102", "v103", "v104", "v105", "v106", "v107", "v108", "v109", "v110", "v111", "v112", "v113", "v114", "v115", "v116", "v117", "v118", "v119", "v120", "v121", "v122", "v123", "v124", "v125", "v126", "v127", "v128", "v129", "v130", "v131", "v132", "v133", "v134", "v135", "v136", "v137", "v138", "v139", "v140", "v141", "v142", "v143", "v144", "v145", "v146", "v147", "v148", "v149", "v150", "v151", "v152", "v153", "v154", "v155", "v156", "v157", "v158", "v159", "v160", "v161", "v162", "v163", "v164", "v165", "v166", "v167", "v168", "v169", "v170", "v171", "v172", "v173", "v174", "v175", "v176", "v177", "v178", "v179", "v180", "v181", "v182", "v183", "v184", "v185", "v186", "v187", "v188", "v189", "v190", "v191", "v192", "v193", "v194", "v195", "v196", "v197", "v198", "v199", "v200", "v201", "v202", "v203", "v204", "v205", "v206", "v207", "v208", "v209", "v210", "v211", "v212", "v213", "v214", "v215", "v216", "v217", "v218", "v219", "v220", "v221", "v222", "v223", "v224", "v225", "v226", "v227", "v228", "v229", "v230", "v231", "a0", "a1", "a2", "a3", "a4", "a5", "a6", "a7", "a8", "a9", "a10", "a11", "a12", "a13", "a14", "a15", "a16", "a17", "a18", "a19", "a20", "a21", "a22", "a23", "a24", "a25", "a26", "a27", "a28", "a29", "a30", "a31", "a32", "a33", "a34", "a35", "a36", "a37", "a38", "a39", "a40", "a41", "a42", "a43", "a44", "a45", "a46", "a47", "a48", "a49", "a50", "a51", "a52", "a53", "a54", "a55", "a56", "a57", "a58", "a59", "a60", "a61", "a62", "a63", "a64", "a65", "a66", "a67", "a68", "a69", "a70", "a71", "a72", "a73", "a74", "a75", "a76", "a77", "a78", "a79", "a80", "a81", "a82", "a83", "a84", "a85", "a86", "a87", "a88", "a89", "a90", "a91", "a92", "a93", "a94", "a95", "a96", "a97", "a98", "a99", "a100", "a101", "a102", "a103", "a104", "a105", "a106", "a107", "a108", "a109", "a110", "a111", "a112", "a113", "a114", "a115", "a116", "a117", "a118", "a119", "a120", "a121", "a122", "a123", "a124", "a125", "a126", "a127", "a128", "a129", "a130", "a131", "a132", "a133", "a134", "a135", "a136", "a137", "a138", "a139", "a140", "a141", "a142", "a143", "a144", "a145", "a146", "a147", "a148", "a149", "a150", "a151", "a152", "a153", "a154", "a155", "a156", "a157", "a158", "a159", "a160", "a161", "a162", "a163", "a164", "a165", "a166", "a167", "a168", "a169", "a170", "a171", "a172", "a173", "a174", "a175", "a176", "a177", "a178", "a179", "a180", "a181", "a182", "a183", "a184", "a185", "a186", "a187", "a188", "a189", "a190", "a191", "a192", "a193", "a194", "a195", "a196", "a197", "a198", "a199", "a200", "a201", "a202", "a203", "a204", "a205", "a206", "a207", "a208", "a209", "a210", "a211", "a212", "a213", "a214", "a215", "a216", "a217", "a218", "a219", "a220", "a221", "a222", "a223", "a224", "a225", "a226", "a227", "a228", "a229", "a230", "a231", "a232", "a233", "a234", "a235", "a236", "a237", "a238", "a239", "a240", "a241", "a242", "a243", "a244", "a245", "a246", "a247", "a248", "a249", "a250", "a251", "a252", "a253", "a254", "a255", "s36", "s37", "s38", "s39", "s40", "s41", "s42", "s43", "s44", "s45", "s46", "s47", "s48", "s49", "s50", "s51", "s52", "s53", "s54", "s55", "s56", "s57", "s58", "s59", "s60", "s61", "s62", "s63", "s64", "s65", "s66", "s67", "s68", "s69", "s70", "s71", "s72", "s73", "s74", "s75", "s76", "s77", "s78", "s79", "s80", "s81", "s82", "s83", "s84", "s85", "s86", "s87", "s88", "s89", "s90", "s91", "s92", "s93", "s94", "s95", "s96", "s97", "vcc", "scc", "memory");
    __builtin_unreachable();
  } else {
    LANE_ID(lane2);
    constexpr int N1 = 33 - (NR4 - 6) * 16, T1 = LAST_TASKS > RLN * N1 ? RLN * N1 : LAST_TASKS;
    const int w = kind == 6 ? 0 : kind == 1 ? 1 + (3 - rep) * 8 + slice : N1 + rep * 8 + slice;
    if (kind != 2) for (int t = w; t < T1; t += N1) row_last(ws, out, t, lane2, wave, ep);
    for (int t = T1 + (w + 32) % (N1 + 32); t < LAST_TASKS; t += N1 + 32) row_last(ws, out, t, lane2, wave, ep);
    if (b == 65 && wave == 0) set_flag(ws + OFF_EP, lane2, ep);
    CTS(14);
    asm volatile("s_endpgm\nLcode_end:" ::: "memory");
    __builtin_unreachable();
  }
}

extern "C" void kernel_launch(void* const* d_in, const int* in_sizes, int n_in, void* d_out, int out_size,
                              void* d_ws, size_t ws_size, hipStream_t stream) {
  const u16* q = (const u16*)d_in[0];
  const u16* k = (const u16*)d_in[1];
  const u16* v = (const u16*)d_in[2];
  const u16* shift_k = (const u16*)d_in[3];
  const u16* ssm_k = (const u16*)d_in[4];
  const u16* ssm_D = (const u16*)d_in[5];
  const u16* w1 = (const u16*)d_in[7];
  const u16* w2 = (const u16*)d_in[8];
  const u16* w3 = (const u16*)d_in[9];
  const u16* w4 = (const u16*)d_in[10];
  flow_kernel<<<256, 256, 0, stream>>>(q, k, v, shift_k, ssm_k, ssm_D, w1, w2, w3, w4, (char*)d_ws, (u16*)d_out);
}
```

```cpp
#include <hip/hip_runtime.h>
#include <hip/hip_bf16.h>
#include <stdint.h>
#include <type_traits>

typedef short s16x8 __attribute__((ext_vector_type(8)));
typedef short s16x4 __attribute__((ext_vector_type(4)));
typedef float f32x4 __attribute__((ext_vector_type(4)));
typedef unsigned short u16;
typedef unsigned short u16x8 __attribute__((ext_vector_type(8)));
typedef unsigned short u16x4 __attribute__((ext_vector_type(4)));
typedef float f32x2 __attribute__((ext_vector_type(2)));
typedef __bf16 bf16x2 __attribute__((ext_vector_type(2)));
typedef unsigned u32x4v __attribute__((ext_vector_type(4)));
typedef __attribute__((address_space(1))) unsigned gu32;

constexpr int S_ = 2048, D_ = 1024, F_ = 3584;
#define PF 1

constexpr int WS_USED = 111243392, LAST_TASKS = 100, ALLZ = 1, U0 = 170, U1 = 224, NR4 = 8, RHELP = 1, PRE1 = 1;
constexpr int OFF_KVR = 4194304, OFF_XP = 8388608, OFF_HS = 17825792, OFF_X1 = 31457280, OFF_G = 40894464, OFF_P0 = 71303168, OFF_P1 = 89128960, OFF_Y = 98566144, OFF_S1 = 106954752, OFF_ST = 107479040, OFF_FL = 109051904, OFF_FXP = 109056000, OFF_FH = 109334528, OFF_FX1 = 109613056, OFF_FG = 109891584, OFF_FP = 110587904, OFF_FKV = 111144960, OFF_FY = 111177728, OFF_EP = 111243264, OFF_DBG = 115343360;

#define WS_DIAG 0

__device__ __forceinline__ float bf2f(u16 v) { return __uint_as_float(((unsigned)v) << 16); }
__device__ __forceinline__ u16 f2bf(float f) { return __builtin_bit_cast(u16, (__bf16)f); }
__device__ __forceinline__ float rbf(float f) { return bf2f(f2bf(f)); }
__device__ __forceinline__ void st16(void* p, u16x8 v) {
  asm volatile("global_store_dwordx4 %0, %1, off sc0 sc1\n\ts_nop 1" :: "v"(p), "v"(v) : "memory");
}
#define WAIT_VM(n) asm volatile("s_waitcnt vmcnt(%0)" ::"n"(n) : "memory")
#define WAIT_LGKM0() asm volatile("s_waitcnt lgkmcnt(0)" ::: "memory")

#define CODE_RANGE(B, E, p, n)                                                                                   \
  const char* p; unsigned n;                                                                                     \
  { unsigned lo_, hi_;                                                                                           \
    asm volatile("s_getpc_b64 s[98:99]\nLpc%=:\n\ts_add_u32 %0, s98, (" B "-Lpc%=)&4294967295\n\ts_addc_u32 %1, s99, (" B "-Lpc%=)>>32\n\t" \
                 "s_mov_b32 %2, " E "-" B : "=s"(lo_), "=s"(hi_), "=s"(n) :: "s98", "s99", "scc");               \
    p = (const char*)(((unsigned long long)hi_ << 32) | lo_); }
__device__ __forceinline__ unsigned touch(const char* p, unsigned n, unsigned off) {
  unsigned t;
  asm volatile("global_load_dword %0, %1, off" : "=v"(t) : "v"(p + (off < n ? off : 0u)) : "memory");
  return t;
}

__device__ __forceinline__ void touch_lds(const char* p, unsigned a) {
  asm volatile("s_mov_b32 m0, %1\n\ts_nop 0\n\tglobal_load_lds_dword %0, off" :: "v"(p), "s"(a) : "memory");
}
#define PFPOL ""
__device__ __forceinline__ void touch_pol(const char* p, unsigned a) {
  asm volatile("s_mov_b32 m0, %1\n\ts_nop 0\n\tglobal_load_lds_dword %0, off " PFPOL :: "v"(p), "s"(a) : "memory");
}
#define YSLEEP 4
#define KSLEEP 2
#define CTS(i)
__device__ __forceinline__ f32x2 up2(unsigned p) { return (f32x2){__uint_as_float(p << 16), __uint_as_float(p & 0xffff0000u)}; }
__device__ __forceinline__ unsigned pk2(f32x2 v) { return __builtin_bit_cast(unsigned, __builtin_convertvector(v, bf16x2)); }
__device__ __forceinline__ f32x2 rbf2(f32x2 v) { return up2(pk2(v)); }
template <int N, class F> __device__ __forceinline__ void sfor(F&& f) {
  if constexpr (N > 0) { sfor<N - 1>(f); f(std::integral_constant<int, N - 1>{}); }
}
__device__ __forceinline__ void set_flag(char* p, int i, unsigned ep) {
  if (i < 32) __hip_atomic_store((gu32*)p + i, ep, __ATOMIC_RELAXED, __HIP_MEMORY_SCOPE_AGENT);
}
template <int NL>
__device__ __forceinline__ void poll_flags(const char* p, int n, int lane, unsigned ep) {
  for (int it = 0; it < 20000; ++it) {
    bool ok = true;
#pragma unroll
    for (int i = 0; i < NL; ++i) {
      const int f = lane + 64 * i;
      ok &= __hip_atomic_load((gu32*)(p + (f < n ? f : 0) * 128), __ATOMIC_RELAXED, __HIP_MEMORY_SCOPE_AGENT) == ep;
    }
    if (__all(ok)) break;
    __builtin_amdgcn_s_sleep(YSLEEP);
  }
  asm volatile("" ::: "memory");
}

constexpr int CV_HP = (2048 + 32) * 2, CV_UR = (2048 + 256) * 2, CV_CH = 2 * CV_HP + CV_UR;
constexpr int NCV = 174;
constexpr int CV_STG = 8 * CV_CH;
constexpr int YP = D_ * 16;

struct KvRegs { u32x4v k0, k1, k2, k3, k4, k5, k6, v0, v1, v2, v3, s0, s1, s2, s3; };
#define KV_LOADS                                                                                                                       \
  "v_lshrrev_b32 %[a], 3, %[tid]\n\tv_and_b32 %[c], 7, %[tid]\n\ts_lshr_b32 s90, %[tile], 4\n\ts_and_b32 s91, %[tile], 15\n\t"          \
  "v_lshlrev_b32 %[a], 13, %[a]\n\tv_lshlrev_b32 %[c], 4, %[c]\n\ts_lshl_b32 s90, s90, 18\n\ts_lshl_b32 s91, s91, 7\n\t"                 \
  "v_add_u32 %[a], s90, %[a]\n\tv_add_u32 %[c], s91, %[c]\n\tv_subrev_u32 %[h], 0x1000, %[a]\n\tv_max_i32 %[h], 0x800, %[h]\n\t"         \
  "v_add3_u32 %[a], %[a], %[c], s92\n\tv_add_u32 %[h], %[h], %[c]\n\tv_lshlrev_b32 %[c], 2, %[c]\n\ts_waitcnt lgkmcnt(0)\n\t"            \
  "global_load_dwordx4 %3, %[a], s[76:77] offset:-4096 nt\n\tglobal_load_dwordx4 %4, %[a], s[76:77] offset:-2048 nt\n\t"                \
  "global_load_dwordx4 %5, %[a], s[76:77] nt\n\tglobal_load_dwordx4 %6, %[a], s[76:77] offset:2048 nt\n\t"                              \
  "global_load_dwordx4 %7, %[a], s[78:79] offset:-4096 nt\n\tglobal_load_dwordx4 %8, %[a], s[78:79] offset:-2048 nt\n\t"                \
  "global_load_dwordx4 %9, %[a], s[78:79] nt\n\tglobal_load_dwordx4 %10, %[a], s[78:79] offset:2048 nt\n\t"                             \
  "global_load_dwordx4 %0, %[h], s[76:77] offset:-2048 nt\n\tglobal_load_dwordx4 %1, %[h], s[76:77] nt\n\t"                             \
  "global_load_dwordx4 %2, %[h], s[76:77] offset:2048 nt\n\t"                                                                           \
  "global_load_dwordx4 %11, %[c], s[80:81]\n\tglobal_load_dwordx4 %12, %[c], s[80:81] offset:16\n\t"                                    \
  "global_load_dwordx4 %13, %[c], s[80:81] offset:32\n\tglobal_load_dwordx4 %14, %[c], s[80:81] offset:48\n\t"
#define KV_OUTS(R) "=&v"(R.k0), "=&v"(R.k1), "=&v"(R.k2), "=&v"(R.k3), "=&v"(R.k4), "=&v"(R.k5), "=&v"(R.k6), "=&v"(R.v0), "=&v"(R.v1), "=&v"(R.v2), \
                   "=&v"(R.v3), "=&v"(R.s0), "=&v"(R.s1), "=&v"(R.s2), "=&v"(R.s3), [a] "=&v"(ta), [h] "=&v"(th), [c] "=&v"(tc)
#define KV_CLOB "s74", "s75", "s76", "s77", "s78", "s79", "s80", "s81", "s82", "s83", "s90", "s91", "s92", "scc", "vcc", "memory"
template <int N>
__device__ __forceinline__ void kv_wait(KvRegs& R) {
  asm volatile("s_waitcnt vmcnt(%15)" : "+v"(R.k0), "+v"(R.k1), "+v"(R.k2), "+v"(R.k3), "+v"(R.k4), "+v"(R.k5), "+v"(R.k6), "+v"(R.v0), "+v"(R.v1), "+v"(R.v2),
               "+v"(R.v3), "+v"(R.s0), "+v"(R.s1), "+v"(R.s2), "+v"(R.s3) : "n"(N) : "memory");
}
__device__ __forceinline__ void kv_store(KvRegs& R, char* __restrict__ ws, char* T, int b, int tid) {
  const int t0 = (b >> 4) * 128, c0 = (b & 15) * 64, ch8 = tid & 7, rg = tid >> 3;
  if (t0 + 4 * rg < 3) R.k0 = R.k1 = R.k2 = (u32x4v){0u, 0u, 0u, 0u};
  typedef unsigned u32x2 __attribute__((ext_vector_type(2)));
  const u32x4v kr[7] = {R.k0, R.k1, R.k2, R.k3, R.k4, R.k5, R.k6}, vr[4] = {R.v0, R.v1, R.v2, R.v3}, sk[4] = {R.s0, R.s1, R.s2, R.s3};
#pragma unroll
  for (int j2 = 0; j2 < 4; ++j2) {
    const f32x2 sa = up2(sk[j2][0]), sb = up2(sk[j2][1]), sc = up2(sk[j2][2]), sd = up2(sk[j2][3]);
    const f32x2 s0 = {sa[0], sc[0]}, s1 = {sa[1], sc[1]}, s2 = {sb[0], sd[0]}, s3 = {sb[1], sd[1]};
    f32x2 kf[7], o[4];
#pragma unroll
    for (int r = 0; r < 7; ++r) kf[r] = up2(kr[r][j2]);
#pragma unroll
    for (int r = 0; r < 4; ++r)
      o[r] = __builtin_elementwise_fma(kf[r + 3], s0, __builtin_elementwise_fma(kf[r + 2], s1, __builtin_elementwise_fma(kf[r + 1], s2, kf[r] * s3))) * up2(vr[r][j2]);
#pragma unroll
    for (int e = 0; e < 2; ++e)
      *(u32x2*)(T + (8 * ch8 + 2 * j2 + e) * 264 + (124 - 4 * rg) * 2) = (u32x2){pk2((f32x2){o[3][e], o[2][e]}), pk2((f32x2){o[1][e], o[0][e]})};
  }
  __syncthreads();
  u16* kvr = (u16*)(ws + OFF_KVR);
#pragma unroll
  for (int i = 0; i < 4; ++i) {
    const int idx = tid + 256 * i, ch = idx >> 4, cb = idx & 15;
    const u16x4 lo = *(const u16x4*)(T + ch * 264 + cb * 16), hi = *(const u16x4*)(T + ch * 264 + cb * 16 + 8);
    st16(kvr + (size_t)(c0 + ch) * S_ + (1920 - t0) + cb * 8, __builtin_shufflevector(lo, hi, 0, 1, 2, 3, 4, 5, 6, 7));
  }
}

#define LASTSLEEP 4
__device__ __forceinline__ void wait_flags(const char* p, unsigned ep) {
  gu32* f = (gu32*)p;
  for (int it = 0; it < 20000; ++it) {
    if (__all(__hip_atomic_load(f, __ATOMIC_RELAXED, __HIP_MEMORY_SCOPE_AGENT) == ep)) break;
    __builtin_amdgcn_s_sleep(LASTSLEEP);
  }
  asm volatile("" ::: "memory");
}
__device__ __forceinline__ void row_last(char* __restrict__ ws, u16* __restrict__ out, int t, int lane, int wave, unsigned ep) {
  const __amdgpu_buffer_rsrc_t rs = __builtin_amdgcn_make_buffer_rsrc(ws, 0, WS_USED, 0x00020000);
  const int r8 = lane & 7, seg = lane >> 3, un = 128 + (t >> 1);
  const unsigned fo = un * 32768 + (wave * 8 + (seg >> 2)) * 1024 + ((seg & 3) * 16 + (t & 1) * 8 + r8) * 16;
  wait_flags(ws + OFF_FP + un * 2048 + ((lane & 2) * 4 + wave * 2 + (lane & 1)) * 128, ep);
  u32x4v p0[4], p1[4];
#pragma unroll
  for (int i = 0; i < 4; ++i) {
    p0[i] = __builtin_bit_cast(u32x4v, __builtin_amdgcn_raw_buffer_load_b128(rs, fo + i * 2048, OFF_P0, 16));
    p1[i] = __builtin_bit_cast(u32x4v, __builtin_amdgcn_raw_buffer_load_b128(rs, fo + i * 2048, OFF_P1, 16));
  }
#pragma unroll
  for (int i = 0; i < 4; ++i) {
    u32x4v o;
#pragma unroll
    for (int e = 0; e < 4; ++e) o[e] = pk2(up2(p0[i][e]) + up2(p1[i][e]));
    st16(out + (size_t)(t * 8 + r8) * D_ + wave * 256 + i * 64 + seg * 8, __builtin_bit_cast(u16x8, o));
  }
}

__device__ __forceinline__ int swzB(int r) { return ((((r >> 1) & 1) | (((r >> 3) & 1) << 1)) << 1); }
#define TCH 1
#define LANE_ID(x) int x; asm volatile("v_mbcnt_lo_u32_b32 %0, -1, 0\n\tv_mbcnt_hi_u32_b32 %0, -1, %0" : "=v"(x))
#define W12 6
#define W_B1 (b == 1)
#define W_B7 (b == 7)
#define W_I15 (idx == 15)
#define W_I31 (idx == 31)
#define W_B249 (b == 249)
#define RLN 3
#define QTB 1
constexpr int QT0 = 34 * 128;
#define CODE_MAX(x, y) "(((" x ">" y ")&" x ")|((" x "<=" y ")&" y "))"
constexpr int HU = 40960;
#define G3OR 5
constexpr int NRW = 22 - G3OR;
__global__ __launch_bounds__(256, 1) void flow_kernel(const u16* __restrict__ q, const u16* __restrict__ kin, const u16* __restrict__ vin,
                                                      const u16* __restrict__ shift_k, const u16* __restrict__ ssm_k, const u16* __restrict__ ssm_D,
                                                      const u16* __restrict__ w1, const u16* __restrict__ w2,
                                                      const u16* __restrict__ w3, const u16* __restrict__ w4, char* __restrict__ ws,
                                                      u16* __restrict__ out) {
  __shared__ __attribute__((aligned(16))) char lds[163840];
  KvRegs KR;
  unsigned ew, ta, th, tc;
  int b, tile;
  asm volatile("Lcode_b:\n\ts_load_dwordx4 s[76:79], %[ka], 0x8\n\ts_load_dwordx2 s[82:83], %[ka], 0x50\n\t"
               "s_getpc_b64 s[74:75]\n\ts_lshr_b32 s90, %[bi], 3\n\ts_mov_b32 m0, 0x27f00\n\tv_lshlrev_b32 %[a], 5, %[tid]\n\tv_add_lshl_u32 %[a], %[a], s90, 7\n\t"
               "v_min_u32 %[a], " CODE_MAX(CODE_MAX(CODE_MAX("(Lcode_g3_e-Lcode_b)", "(Lcode_g3o_e-Lcode_b)"), CODE_MAX("(Lcode_g4_e-Lcode_b)", "(Lcode_g12_e-Lcode_b)")), "(Lcode_end-Lcode_b)") "-64, %[a]\n\t"
               "global_load_lds_dword %[a], s[74:75]\n\ts_load_dwordx2 s[80:81], %[ka], 0x18\n\t"
               "s_mov_b32 %[b], %[bi]\n\ts_bfe_u32 s91, %[bi], 0x20001\n\ts_lshl2_add_u32 %[tile], s90, s91\n\ts_movk_i32 s92, 0x1000\n\t"
               "s_lshl_b32 s90, %[bi], 7\n\ts_add_u32 %[tile], %[tile], s90\n\ts_add_u32 %[tile], %[tile], 64\n\ts_and_b32 %[tile], %[tile], 255\n\t"
               KV_LOADS "v_mov_b32 %[a], %[eo]\n\tglobal_load_dword %[ew], %[a], s[82:83] sc1"
               : KV_OUTS(KR), [ew] "=&v"(ew), [b] "=&s"(b), [tile] "=&s"(tile)
               : [ka] "s"(__builtin_amdgcn_kernarg_segment_ptr()), [bi] "s"(blockIdx.x), [tid] "v"(threadIdx.x), [eo] "n"(OFF_EP) : KV_CLOB);
  const unsigned t_start = 0;
  const int tid = threadIdx.x, lane = tid & 63, wave = __builtin_amdgcn_readfirstlane(tid >> 6);
  const int xcd = b & 7, idx = b >> 3;
  const unsigned sLds = (unsigned)(size_t)lds;
  const int o8 = idx & 7;
  int kind, rep, slice, kh = 0;
  int j3 = 0, j4 = 0;
  const int xp = xcd >> 1, pr = xp & 1;
  if (!(xcd & 1)) {
    if (idx < 20) { kind = 4; rep = idx < 16 ? (xp >> 1) * 3 + (idx >> 3) : 2; kh = pr; slice = idx < 16 ? o8 : (xp >> 1) * 4 + o8; j4 = rep * 16 + kh * 8 + slice; }
    else if (idx >= 24) { kind = 1; rep = xp; slice = o8; }
    else { kind = 3; rep = 4; slice = xp * 4 + idx - 20; j3 = 72 + slice; }
  } else if (idx >= 24) { kind = 2; rep = xp; slice = o8; }
  else if (idx < 18) { kind = 3; rep = pr * 2 + (idx >= 9); slice = (xp >> 1) * 9 + idx - 9 * (idx >= 9); j3 = rep * 18 + slice; }
  else if (idx < 22 || b == 191) { kind = 6; rep = 0; slice = idx < 22 ? xp * 4 + idx - 18 : 16; }
  else if (idx == 22 || xp == 0) { kind = 5; rep = idx == 22 ? pr * 2 + (xp >> 1) : 4; slice = 18; j3 = 90 + rep; }
  else { kind = 3; rep = 4; slice = 15 + xp; j3 = 72 + slice; }
  CTS(0);
  const unsigned sScr = sLds + 163584;
  unsigned ep = 0;
  auto w_touch = [&](const u16* w) {
#pragma unroll
    for (int i = 0; i < 2; ++i) touch_lds((const char*)w + (size_t)(rep * 256 + i * 128 + (tid >> 1)) * 2048 + slice * 256 + (tid & 1) * 128, sScr);
  };
  {
  const bool isconv = kind >= 3 && kind != 6 && j3 < 94;
  const int nw = (kind == 4 || j3 >= 92) ? 2 : 1, c0 = kind == 4 ? 8 * j4 : j3 < 92 ? 640 + 4 * j3 : j3 < 94 ? 272 + 8 * j3 : 0, cw = c0 + nw * wave;
  const unsigned hd = isconv ? sLds + 2 * wave * CV_CH + 32 : 0x30000u;
  auto h_dma = [&](bool first) {
#pragma unroll
    for (int j = 0; j < 2; ++j) {
      const u16* hp = ssm_k + (size_t)cw * S_ + (isconv && j < nw ? j * S_ + lane * 8 : 0);
      if (first) asm volatile("s_mov_b32 m0, %1\n\ts_nop 0\n\tglobal_load_lds_dwordx4 %0, off nt" :: "v"(hp), "s"(hd + j * CV_CH) : "memory");
      else asm volatile("s_mov_b32 m0, %1\n\ts_nop 0\n\tglobal_load_lds_dwordx4 %0, off offset:1024 nt\n\t"
                        "global_load_lds_dwordx4 %0, off offset:2048 nt\n\tglobal_load_lds_dwordx4 %0, off offset:3072 nt" :: "v"(hp), "s"(hd + j * CV_CH) : "memory");
    }
  };
  auto w_dma = [&](int t0, int t1) {
    const char* wp = (const char*)w1 + (16 * wave + (lane >> 3)) * 2048 + slice * 256;
    const unsigned c0_ = ((lane & 7) ^ swzB(lane >> 3)) * 16, c1_ = 8 * 2048 + ((lane & 7) ^ swzB(8 + (lane >> 3))) * 16;
#pragma unroll
    for (int t = t0; t < t1; ++t)
#pragma unroll
      for (int i = 0; i < 2; ++i)
        asm volatile("s_mov_b32 m0, %1\n\ts_nop 0\n\tglobal_load_lds_dwordx4 %0, off\n\ts_add_u32 m0, m0, 8064\n\ts_nop 0\n\tglobal_load_lds_dwordx4 %0, off offset:128"
                     :: "v"(wp + t * 131072 + (i ? c1_ : c0_)), "s"(sLds + t * 16384 + wave * 2048 + i * 1024) : "scc", "memory");
  };
  if (PRE1 && kind == 1) w_dma(0, 2);
  else { h_dma(true); h_dma(false); }
  if (isconv) *(u32x4v*)(lds + (2 * wave + (lane >> 5)) * CV_CH + 2 * CV_HP + 4096 + (lane & 31) * 16) = (u32x4v){0u, 0u, 0u, 0u};
  if (isconv && lane < 8) *(u32x4v*)(lds + (2 * wave + (lane >> 2)) * CV_CH + ((lane >> 1) & 1) * CV_HP + (lane & 1) * 16) = (u32x4v){0u, 0u, 0u, 0u};
  kv_wait<9>(KR);
  CTS(12);
  kv_store(KR, ws, lds + CV_STG, tile, tid);
  CTS(1);
  if (PRE1 && kind == 1) w_dma(2, 6);
  const unsigned dw = ((const unsigned*)ssm_D)[cw >> 1];
  asm volatile("s_cmp_eq_u32 %1, %2\n\ts_cbranch_scc1 Lew%=\n\ts_waitcnt vmcnt(0)\nLew%=:\n\ts_waitcnt vmcnt(16)" : "+v"(ew) : "s"(__builtin_amdgcn_readfirstlane(kind)), "n"(PRE1 ? 1 : 99) : "scc", "memory");
  ep = __builtin_amdgcn_readfirstlane(ew) + 1u;
  if (ep == 0u) ep = 1u;
  __builtin_amdgcn_s_barrier();
  set_flag(ws + OFF_FKV + tile * 128, tid, ep);
  CTS(2);
  if (kind < 3 && (W12 & kind) != 0) w_touch(kind == 1 ? w1 : w2);
  if (kind == 2 && (W12 & 4) != 0) w_touch(w1);
  if (isconv) {
    LANE_ID(lane);
    const int tid = wave * 64 + lane;
    const unsigned sC0 = (cw & 1) ? dw & 0xffff0000u : dw << 16, sC1 = dw & 0xffff0000u;
    const int l15 = lane & 15, lg = lane >> 4, cp = (l15 + 1) & 1, iw = kind == 4 ? j4 : 80 + j3, own = 2 * wave * CV_CH;
    const int ch = tid & (4 * nw - 1), tb = nw == 2 ? tid >> 3 : tid >> 2, slot = nw == 2 ? ch : 2 * ch;
    const unsigned sWave = wave, sBuf = nw, sAuxOff = OFF_KVR + cw * 4096, sFoutOff = OFF_FY + iw * 128, sExp = __builtin_amdgcn_readfirstlane(ep), sDbg = 110u * 1048576u + (b * 4 + wave) * 8192;
    const unsigned vL16 = lane * 16, vPA = own + cp * CV_HP + 2 * (8 * lg + l15 + 1 - cp), vPB = own + 2 * CV_HP + 2 * (2032 - 16 * l15 + 8 * lg) - 512 * 7;
    const unsigned vPU = own + 2 * CV_HP + 2 * (2044 - 16 * l15 - 4 * lg) - 512 * 7, vPW = 32 * l15 + 8 * lg;
    const unsigned vOR = slot * CV_CH + tb * 16, vORA = CV_STG + slot * 1024 + tb * 16, vOG = tb * YP + (c0 + ch) * 16;
    const unsigned vFA = ((lane & 3) * 16 + (c0 >> 6)) * 128, vFB = ((4 + lane % 12) * 16 + (c0 >> 6)) * 128, vFlag = tid < 32 ? tid * 4 : 0x80000000u;
    const unsigned vTW = lane < 42 ? (iw * 168 + wave * 42 + lane) * 128 : 0x80000000u;
    const unsigned vTX = lane < 44 ? ((kind == 4 ? j4 : j3 - 12) * 176 + wave * 44 + lane) * 128 : 0x80000000u;
    (void)sDbg;
    asm volatile(
R"(.macro M d,w,x
v_mfma_f32_16x16x32_bf16 v[\d:\d+3], \w, v[\x:\x+3], v[\d:\d+3]
.endm
.macro R d,a,o
ds_read_b128 v[\d:\d+3], v\a offset:\o
.endm
.macro T d,a,o
ds_read_b64_tr_b16 \d, v\a offset:\o
.endm
.macro D a,s,o
buffer_load_dwordx4 v\a, s[36:39], s\s offen offset:\o sc1 lds
.endm
.macro L c
s_waitcnt lgkmcnt(\c)
.endm
Lcode_cv_b:
s_mov_b64 s[36:37],%0
s_mov_b32 s38,111243392
s_mov_b32 s39,0x00020000
s_mov_b32 s44,%1
s_mov_b32 s51,%2
s_mov_b32 s49,%3
s_mov_b32 s48,%4
s_mov_b32 s86,%5
s_mov_b32 s87,%6
s_mov_b32 s53,%7
v_mov_b32 v0,%8
v_mov_b32 v1,%9
v_mov_b32 v2,%10
v_mov_b32 v3,%11
v_mov_b32 v4,%12
v_mov_b32 v5,%13
v_mov_b32 v6,%14
v_mov_b32 v7,%15
v_mov_b32 v8,%16
v_mov_b32 v9,%17
v_mov_b32 v10,%18
v_mov_b32 v11,%19
v_mov_b32 v12,%20
s_mov_b32 s58,111144960
buffer_load_dword v13,v8,s[36:39],s58 offen offset:0 sc1
s_mul_i32 s57,s44,25856
v_add_u32 v18,s57,v0
v_add_u32 v18,32,v18
v_mov_b32 v19,s57
R 88,18,0
ds_read_b32 v92,v18 offset:16
R 152,18,12928
ds_read_b32 v156,v18 offset:12944
ds_read_b32 v24,v19 offset:32
ds_read_b32 v25,v19 offset:12960
s_mov_b64 s[40:41],%21
s_mov_b32 s42,7340032
s_mov_b32 s43,0x00020000
s_mov_b64 s[60:61],%22
s_mov_b32 s62,7340032
s_mov_b32 s63,0x00020000
v_mov_b32 v23,s53
s_mov_b32 s82,0
s_add_u32 s55,s49,4096
s_cmp_eq_u32 s51,2
s_cselect_b32 s55,s55,0x7fff0000
s_cselect_b32 s56,0,0x70000000
s_add_u32 s57,s57,8320
v_add_u32 v14,1024,v1
v_add_u32 v15,2048,v1
v_add_u32 v16,3072,v1
v_add_u32 v17,12928,v1
L 4
v_alignbyte_b32 v96,v89,v88,2
v_alignbyte_b32 v97,v90,v89,2
v_alignbyte_b32 v98,v91,v90,2
v_alignbyte_b32 v99,v92,v91,2
ds_write_b128 v18,v[96:99] offset:4160
L 3
v_alignbyte_b32 v160,v153,v152,2
v_alignbyte_b32 v161,v154,v153,2
v_alignbyte_b32 v162,v155,v154,2
v_alignbyte_b32 v163,v156,v155,2
ds_write_b128 v18,v[160:163] offset:17088
L 2
v_lshlrev_b32 v24,16,v24
v_lshlrev_b32 v25,16,v25
s_mov_b64 exec,1
ds_write_b32 v19,v24 offset:4188
ds_write_b32 v19,v25 offset:17116
s_mov_b64 exec,-1
ds_read2_b32 a[0:1],v1 offset0:0 offset1:1
ds_read2_b32 a[2:3],v1 offset0:2 offset1:3
ds_read2_b32 a[4:5],v1 offset0:128 offset1:129
ds_read2_b32 a[6:7],v1 offset0:130 offset1:131
ds_read2_b32 a[8:9],v1 offset0:16 offset1:17
ds_read2_b32 a[10:11],v1 offset0:18 offset1:19
ds_read2_b32 a[12:13],v1 offset0:144 offset1:145
ds_read2_b32 a[14:15],v1 offset0:146 offset1:147
ds_read2_b32 a[16:17],v1 offset0:32 offset1:33
ds_read2_b32 a[18:19],v1 offset0:34 offset1:35
ds_read2_b32 a[20:21],v1 offset0:160 offset1:161
ds_read2_b32 a[22:23],v1 offset0:162 offset1:163
ds_read2_b32 a[24:25],v1 offset0:48 offset1:49
ds_read2_b32 a[26:27],v1 offset0:50 offset1:51
ds_read2_b32 a[28:29],v1 offset0:176 offset1:177
ds_read2_b32 a[30:31],v1 offset0:178 offset1:179
ds_read2_b32 a[32:33],v1 offset0:64 offset1:65
ds_read2_b32 a[34:35],v1 offset0:66 offset1:67
ds_read2_b32 a[36:37],v1 offset0:192 offset1:193
ds_read2_b32 a[38:39],v1 offset0:194 offset1:195
ds_read2_b32 a[40:41],v1 offset0:80 offset1:81
ds_read2_b32 a[42:43],v1 offset0:82 offset1:83
ds_read2_b32 a[44:45],v1 offset0:208 offset1:209
ds_read2_b32 a[46:47],v1 offset0:210 offset1:211
ds_read2_b32 a[48:49],v1 offset0:96 offset1:97
ds_read2_b32 a[50:51],v1 offset0:98 offset1:99
ds_read2_b32 a[52:53],v1 offset0:224 offset1:225
ds_read2_b32 a[54:55],v1 offset0:226 offset1:227
ds_read2_b32 a[56:57],v1 offset0:112 offset1:113
ds_read2_b32 a[58:59],v1 offset0:114 offset1:115
ds_read2_b32 a[60:61],v1 offset0:240 offset1:241
ds_read2_b32 a[62:63],v1 offset0:242 offset1:243
ds_read2_b32 a[64:65],v17 offset0:0 offset1:1
ds_read2_b32 a[66:67],v17 offset0:2 offset1:3
ds_read2_b32 a[68:69],v17 offset0:128 offset1:129
ds_read2_b32 a[70:71],v17 offset0:130 offset1:131
ds_read2_b32 a[72:73],v17 offset0:16 offset1:17
ds_read2_b32 a[74:75],v17 offset0:18 offset1:19
ds_read2_b32 a[76:77],v17 offset0:144 offset1:145
ds_read2_b32 a[78:79],v17 offset0:146 offset1:147
ds_read2_b32 a[80:81],v17 offset0:32 offset1:33
ds_read2_b32 a[82:83],v17 offset0:34 offset1:35
ds_read2_b32 a[84:85],v17 offset0:160 offset1:161
ds_read2_b32 a[86:87],v17 offset0:162 offset1:163
ds_read2_b32 a[88:89],v17 offset0:48 offset1:49
ds_read2_b32 a[90:91],v17 offset0:50 offset1:51
ds_read2_b32 a[92:93],v17 offset0:176 offset1:177
ds_read2_b32 a[94:95],v17 offset0:178 offset1:179
ds_read2_b32 a[96:97],v17 offset0:64 offset1:65
ds_read2_b32 a[98:99],v17 offset0:66 offset1:67
ds_read2_b32 a[100:101],v17 offset0:192 offset1:193
ds_read2_b32 a[102:103],v17 offset0:194 offset1:195
ds_read2_b32 a[104:105],v17 offset0:80 offset1:81
ds_read2_b32 a[106:107],v17 offset0:82 offset1:83
ds_read2_b32 a[108:109],v17 offset0:208 offset1:209
ds_read2_b32 a[110:111],v17 offset0:210 offset1:211
ds_read2_b32 a[112:113],v17 offset0:96 offset1:97
ds_read2_b32 a[114:115],v17 offset0:98 offset1:99
ds_read2_b32 a[116:117],v17 offset0:224 offset1:225
ds_read2_b32 a[118:119],v17 offset0:226 offset1:227
ds_read2_b32 a[120:121],v17 offset0:112 offset1:113
ds_read2_b32 a[122:123],v17 offset0:114 offset1:115
ds_read2_b32 a[124:125],v17 offset0:240 offset1:241
ds_read2_b32 a[126:127],v17 offset0:242 offset1:243
Lcv_bpa_%=:
s_waitcnt vmcnt(0)
v_cmp_eq_u32 vcc,s53,v13
s_add_u32 s82,s82,1
s_cmp_eq_u64 vcc,-1
s_cbranch_scc1 Lcv_bqa_%=
s_sleep 1
buffer_load_dword v13,v8,s[36:39],s58 offen offset:0 sc1
s_cmp_lt_u32 s82,20000
s_cbranch_scc1 Lcv_bpa_%=
s_waitcnt vmcnt(0)
Lcv_bqa_%=:
s_add_u32 m0,s57,0
s_nop 0
D 0,49,3072
s_add_u32 m0,s57,12928
s_nop 0
D 0,55,3072
buffer_load_dword v13,v9,s[36:39],s58 offen offset:0 sc1
R 88,18,0
ds_read_b32 v92,v18 offset:16
R 104,18,1024
ds_read_b32 v108,v18 offset:1040
R 120,18,2048
ds_read_b32 v124,v18 offset:2064
R 136,18,3072
ds_read_b32 v140,v18 offset:3088
L 6
v_alignbyte_b32 v96,v89,v88,2
v_alignbyte_b32 v97,v90,v89,2
v_alignbyte_b32 v98,v91,v90,2
v_alignbyte_b32 v99,v92,v91,2
ds_write_b128 v18,v[96:99] offset:4160
L 5
v_alignbyte_b32 v112,v105,v104,2
v_alignbyte_b32 v113,v106,v105,2
v_alignbyte_b32 v114,v107,v106,2
v_alignbyte_b32 v115,v108,v107,2
ds_write_b128 v18,v[112:115] offset:5184
L 4
v_alignbyte_b32 v128,v121,v120,2
v_alignbyte_b32 v129,v122,v121,2
v_alignbyte_b32 v130,v123,v122,2
v_alignbyte_b32 v131,v124,v123,2
ds_write_b128 v18,v[128:131] offset:6208
L 3
v_alignbyte_b32 v144,v137,v136,2
v_alignbyte_b32 v145,v138,v137,2
v_alignbyte_b32 v146,v139,v138,2
v_alignbyte_b32 v147,v140,v139,2
ds_write_b128 v18,v[144:147] offset:7232
R 152,18,12928
ds_read_b32 v156,v18 offset:12944
R 168,18,13952
ds_read_b32 v172,v18 offset:13968
R 184,18,14976
ds_read_b32 v188,v18 offset:14992
R 200,18,16000
ds_read_b32 v204,v18 offset:16016
L 6
v_alignbyte_b32 v160,v153,v152,2
v_alignbyte_b32 v161,v154,v153,2
v_alignbyte_b32 v162,v155,v154,2
v_alignbyte_b32 v163,v156,v155,2
ds_write_b128 v18,v[160:163] offset:17088
L 5
v_alignbyte_b32 v176,v169,v168,2
v_alignbyte_b32 v177,v170,v169,2
v_alignbyte_b32 v178,v171,v170,2
v_alignbyte_b32 v179,v172,v171,2
ds_write_b128 v18,v[176:179] offset:18112
L 4
v_alignbyte_b32 v192,v185,v184,2
v_alignbyte_b32 v193,v186,v185,2
v_alignbyte_b32 v194,v187,v186,2
v_alignbyte_b32 v195,v188,v187,2
ds_write_b128 v18,v[192:195] offset:19136
L 3
v_alignbyte_b32 v208,v201,v200,2
v_alignbyte_b32 v209,v202,v201,2
v_alignbyte_b32 v210,v203,v202,2
v_alignbyte_b32 v211,v204,v203,2
ds_write_b128 v18,v[208:211] offset:20160
s_lshl_b32 s78,s44,11
s_add_u32 s78,s78,103424
v_add_u32 v18,s78,v4
v_add_u32 v4,v19,v4
s_waitcnt vmcnt(0)
v_cmp_eq_u32 vcc,s53,v13
s_mov_b32 s83,0
s_cmp_eq_u64 vcc,-1
s_cbranch_scc0 Lcv_a_%=
s_mov_b32 s83,1
s_add_u32 m0,s57,0
s_nop 0
D 0,49,2048
D 0,49,1024
D 0,49,0
s_add_u32 m0,s57,12928
s_nop 0
D 0,55,2048
D 0,55,1024
D 0,55,0
Lcv_a_%=:
s_cmp_eq_u32 s51,2
s_cbranch_scc1 Lcv_a2_%=
L 0
R 88,2,3584
R 92,2,3072
R 96,2,3648
R 100,2,3136
R 104,2,3712
R 108,2,3200
R 112,2,3776
R 116,2,3264
L 6
v_mfma_f32_16x16x32_bf16 v[40:43],a[0:3],v[88:91],0
R 120,2,3840
R 124,2,3328
v_mfma_f32_16x16x32_bf16 v[44:47],a[0:3],v[92:95],0
M 44,a[4:7],88
L 6
M 40,a[8:11],96
R 128,2,3904
R 132,2,3392
M 44,a[8:11],100
M 44,a[12:15],96
L 6
M 40,a[16:19],104
R 136,2,3968
R 140,2,3456
M 44,a[16:19],108
M 44,a[20:23],104
L 6
M 40,a[24:27],112
R 144,2,4032
R 148,2,3520
M 44,a[24:27],116
M 44,a[28:31],112
L 6
M 40,a[32:35],120
M 44,a[32:35],124
M 44,a[36:39],120
L 4
M 40,a[40:43],128
M 44,a[40:43],132
M 44,a[44:47],128
L 2
M 40,a[48:51],136
ds_read_b64 v[24:25],v3 offset:3584
ds_read_b64 v[26:27],v3 offset:3072
M 44,a[48:51],140
M 44,a[52:55],136
L 2
M 40,a[56:59],144
M 44,a[56:59],148
M 44,a[60:63],144
L 0
v_and_b32 v36,0xffff0000,v25
v_lshlrev_b32 v37,16,v25
v_and_b32 v38,0xffff0000,v24
v_lshlrev_b32 v39,16,v24
s_nop 0
v_fma_f32 v40,v36,s86,v40
v_fma_f32 v41,v37,s86,v41
v_fma_f32 v42,v38,s86,v42
v_fma_f32 v43,v39,s86,v43
v_cvt_pk_bf16_f32 v40,v40,v41
v_cvt_pk_bf16_f32 v41,v42,v43
ds_write_b64 v18,v[40:41] offset:0
v_and_b32 v36,0xffff0000,v27
v_lshlrev_b32 v37,16,v27
v_and_b32 v38,0xffff0000,v26
v_lshlrev_b32 v39,16,v26
v_fma_f32 v44,v36,s86,v44
v_fma_f32 v45,v37,s86,v45
v_fma_f32 v46,v38,s86,v46
v_fma_f32 v47,v39,s86,v47
v_cvt_pk_bf16_f32 v44,v44,v45
v_cvt_pk_bf16_f32 v45,v46,v47
ds_write_b64 v18,v[44:45] offset:512
s_branch Lcv_a3_%=
Lcv_a2_%=:
L 0
R 88,2,3584
R 92,2,3072
R 96,2,3648
R 100,2,3136
R 104,2,3712
R 108,2,3200
R 112,2,3776
R 116,2,3264
L 6
v_mfma_f32_16x16x32_bf16 v[40:43],a[0:3],v[88:91],0
R 120,2,3840
R 124,2,3328
v_mfma_f32_16x16x32_bf16 v[44:47],a[0:3],v[92:95],0
M 44,a[4:7],88
L 6
M 40,a[8:11],96
R 128,2,3904
R 132,2,3392
M 44,a[8:11],100
M 44,a[12:15],96
L 6
M 40,a[16:19],104
R 136,2,3968
R 140,2,3456
M 44,a[16:19],108
M 44,a[20:23],104
L 6
M 40,a[24:27],112
R 144,2,4032
R 148,2,3520
M 44,a[24:27],116
M 44,a[28:31],112
L 6
M 40,a[32:35],120
R 152,2,16512
R 156,2,16000
M 44,a[32:35],124
M 44,a[36:39],120
L 6
M 40,a[40:43],128
R 160,2,16576
R 164,2,16064
M 44,a[40:43],132
M 44,a[44:47],128
L 6
M 40,a[48:51],136
R 168,2,16640
R 172,2,16128
M 44,a[48:51],140
ds_read_b64 v[24:25],v3 offset:3584
ds_read_b64 v[26:27],v3 offset:3072
M 44,a[52:55],136
L 8
M 40,a[56:59],144
R 176,2,16704
R 180,2,16192
M 44,a[56:59],148
M 44,a[60:63],144
L 8
v_mfma_f32_16x16x32_bf16 v[48:51],a[64:67],v[152:155],0
R 184,2,16768
R 188,2,16256
v_mfma_f32_16x16x32_bf16 v[52:55],a[64:67],v[156:159],0
M 52,a[68:71],152
L 8
M 48,a[72:75],160
R 192,2,16832
R 196,2,16320
M 52,a[72:75],164
L 6
v_and_b32 v36,0xffff0000,v25
M 52,a[76:79],160
v_lshlrev_b32 v37,16,v25
v_and_b32 v38,0xffff0000,v24
v_lshlrev_b32 v39,16,v24
s_nop 0
M 48,a[80:83],168
R 200,2,16896
R 204,2,16384
M 52,a[80:83],172
v_fma_f32 v40,v36,s86,v40
v_fma_f32 v41,v37,s86,v41
M 52,a[84:87],168
v_fma_f32 v42,v38,s86,v42
v_fma_f32 v43,v39,s86,v43
v_cvt_pk_bf16_f32 v40,v40,v41
L 6
M 48,a[88:91],176
R 208,2,16960
R 212,2,16448
M 52,a[88:91],180
v_cvt_pk_bf16_f32 v41,v42,v43
ds_write_b64 v18,v[40:41] offset:0
M 52,a[92:95],176
v_and_b32 v36,0xffff0000,v27
v_lshlrev_b32 v37,16,v27
v_and_b32 v38,0xffff0000,v26
L 7
M 48,a[96:99],184
M 52,a[96:99],188
v_lshlrev_b32 v39,16,v26
v_fma_f32 v44,v36,s86,v44
M 52,a[100:103],184
v_fma_f32 v45,v37,s86,v45
v_fma_f32 v46,v38,s86,v46
v_fma_f32 v47,v39,s86,v47
L 5
M 48,a[104:107],192
M 52,a[104:107],196
v_cvt_pk_bf16_f32 v44,v44,v45
v_cvt_pk_bf16_f32 v45,v46,v47
M 52,a[108:111],192
ds_write_b64 v18,v[44:45] offset:512
L 4
M 48,a[112:115],200
ds_read_b64 v[24:25],v3 offset:16512
ds_read_b64 v[26:27],v3 offset:16000
M 52,a[112:115],204
M 52,a[116:119],200
L 4
M 48,a[120:123],208
M 52,a[120:123],212
M 52,a[124:127],208
L 0
v_and_b32 v36,0xffff0000,v25
v_lshlrev_b32 v37,16,v25
v_and_b32 v38,0xffff0000,v24
v_lshlrev_b32 v39,16,v24
s_nop 0
v_fma_f32 v48,v36,s87,v48
v_fma_f32 v49,v37,s87,v49
v_fma_f32 v50,v38,s87,v50
v_fma_f32 v51,v39,s87,v51
v_cvt_pk_bf16_f32 v48,v48,v49
v_cvt_pk_bf16_f32 v49,v50,v51
ds_write_b64 v18,v[48:49] offset:1024
v_and_b32 v36,0xffff0000,v27
v_lshlrev_b32 v37,16,v27
v_and_b32 v38,0xffff0000,v26
v_lshlrev_b32 v39,16,v26
v_fma_f32 v52,v36,s87,v52
v_fma_f32 v53,v37,s87,v53
v_fma_f32 v54,v38,s87,v54
v_fma_f32 v55,v39,s87,v55
v_cvt_pk_bf16_f32 v52,v52,v53
v_cvt_pk_bf16_f32 v53,v54,v55
ds_write_b64 v18,v[52:53] offset:1536
Lcv_a3_%=:
L 0
s_barrier
R 152,6,0
R 156,6,512
s_mov_b32 s78,98566144
L 1
buffer_store_dwordx4 v[152:155],v7,s[36:39],s78 offen offset:0 sc0 sc1
s_add_u32 s78,s56,99090432
L 0
buffer_store_dwordx4 v[156:159],v7,s[36:39],s78 offen offset:0 sc0 sc1
s_cmp_eq_u32 s83,1
s_cbranch_scc1 Lcv_b_%=
s_waitcnt vmcnt(0)
s_barrier
buffer_store_dword v23,v10,s[36:39],s48 offen offset:0 sc0 sc1
buffer_load_dword v13,v9,s[36:39],s58 offen offset:0 sc1
Lcv_bpb_%=:
)"
R"(s_waitcnt vmcnt(0)
v_cmp_eq_u32 vcc,s53,v13
s_add_u32 s82,s82,1
s_cmp_eq_u64 vcc,-1
s_cbranch_scc1 Lcv_bqb_%=
s_sleep 1
buffer_load_dword v13,v9,s[36:39],s58 offen offset:0 sc1
s_cmp_lt_u32 s82,20000
s_cbranch_scc1 Lcv_bpb_%=
s_waitcnt vmcnt(0)
Lcv_bqb_%=:
s_add_u32 m0,s57,0
s_nop 0
D 0,49,2048
D 0,49,1024
D 0,49,0
s_add_u32 m0,s57,12928
s_nop 0
D 0,55,2048
D 0,55,1024
D 0,55,0
s_waitcnt vmcnt(0)
Lcv_b_%=:
s_waitcnt vmcnt(2)
ds_read2_b32 v[88:89],v1 offset0:0 offset1:1
ds_read2_b32 v[90:91],v1 offset0:2 offset1:3
R 120,2,3584
ds_read2_b32 v[92:93],v1 offset0:128 offset1:129
ds_read2_b32 v[94:95],v1 offset0:130 offset1:131
R 124,2,3072
ds_read2_b32 v[96:97],v14 offset0:0 offset1:1
ds_read2_b32 v[98:99],v14 offset0:2 offset1:3
R 128,2,2560
ds_read2_b32 v[100:101],v14 offset0:128 offset1:129
ds_read2_b32 v[102:103],v14 offset0:130 offset1:131
R 132,2,2048
ds_read2_b32 v[104:105],v15 offset0:0 offset1:1
ds_read2_b32 v[106:107],v15 offset0:2 offset1:3
R 136,2,1536
ds_read2_b32 v[108:109],v15 offset0:128 offset1:129
ds_read2_b32 v[110:111],v15 offset0:130 offset1:131
R 140,2,1024
ds_read2_b32 v[112:113],v16 offset0:0 offset1:1
ds_read2_b32 v[114:115],v16 offset0:2 offset1:3
R 144,2,512
ds_read2_b32 v[116:117],v16 offset0:128 offset1:129
ds_read2_b32 v[118:119],v16 offset0:130 offset1:131
R 148,2,0
L 15
v_mfma_f32_16x16x32_bf16 v[40:43],v[88:91],v[128:131],0
ds_read2_b32 v[152:153],v1 offset0:16 offset1:17
M 40,v[92:95],124
ds_read2_b32 v[154:155],v1 offset0:18 offset1:19
M 40,v[96:99],120
L 14
R 184,2,3648
v_mfma_f32_16x16x32_bf16 v[44:47],v[88:91],v[132:135],0
ds_read2_b32 v[156:157],v1 offset0:144 offset1:145
M 44,v[92:95],128
M 44,v[96:99],124
ds_read2_b32 v[158:159],v1 offset0:146 offset1:147
M 44,v[100:103],120
L 14
R 188,2,3136
v_mfma_f32_16x16x32_bf16 v[48:51],v[88:91],v[136:139],0
ds_read2_b32 v[160:161],v14 offset0:16 offset1:17
M 48,v[92:95],132
ds_read2_b32 v[162:163],v14 offset0:18 offset1:19
M 48,v[96:99],128
M 48,v[100:103],124
R 192,2,2624
M 48,v[104:107],120
L 15
ds_read2_b32 v[164:165],v14 offset0:144 offset1:145
v_mfma_f32_16x16x32_bf16 v[52:55],v[88:91],v[140:143],0
ds_read2_b32 v[166:167],v14 offset0:146 offset1:147
M 52,v[92:95],136
R 196,2,2112
M 52,v[96:99],132
M 52,v[100:103],128
ds_read2_b32 v[168:169],v15 offset0:16 offset1:17
M 52,v[104:107],124
ds_read2_b32 v[170:171],v15 offset0:18 offset1:19
M 52,v[108:111],120
L 15
R 200,2,1600
v_mfma_f32_16x16x32_bf16 v[56:59],v[88:91],v[144:147],0
ds_read2_b32 v[172:173],v15 offset0:144 offset1:145
M 56,v[92:95],140
M 56,v[96:99],136
ds_read2_b32 v[174:175],v15 offset0:146 offset1:147
M 56,v[100:103],132
R 204,2,1088
M 56,v[104:107],128
ds_read2_b32 v[176:177],v16 offset0:16 offset1:17
M 56,v[108:111],124
ds_read2_b32 v[178:179],v16 offset0:18 offset1:19
M 56,v[112:115],120
L 15
v_mfma_f32_16x16x32_bf16 v[60:63],v[88:91],v[148:151],0
R 208,2,576
M 60,v[92:95],144
ds_read2_b32 v[180:181],v16 offset0:144 offset1:145
M 60,v[96:99],140
ds_read2_b32 v[182:183],v16 offset0:146 offset1:147
M 60,v[100:103],136
R 212,2,64
M 60,v[104:107],132
s_cmp_eq_u32 s83,0
s_cbranch_scc1 Lcv_c_%=
s_waitcnt vmcnt(0)
s_barrier
buffer_store_dword v23,v10,s[36:39],s48 offen offset:0 sc0 sc1
Lcv_c_%=:
M 60,v[108:111],128
M 60,v[112:115],124
M 60,v[116:119],120
L 15
M 40,v[152:155],192
ds_read2_b32 v[88:89],v1 offset0:32 offset1:33
M 40,v[156:159],188
ds_read2_b32 v[90:91],v1 offset0:34 offset1:35
M 40,v[160:163],184
L 14
R 120,2,3712
M 44,v[152:155],196
ds_read2_b32 v[92:93],v1 offset0:160 offset1:161
M 44,v[156:159],192
M 44,v[160:163],188
ds_read2_b32 v[94:95],v1 offset0:162 offset1:163
M 44,v[164:167],184
L 14
R 124,2,3200
M 48,v[152:155],200
ds_read2_b32 v[96:97],v14 offset0:32 offset1:33
M 48,v[156:159],196
ds_read2_b32 v[98:99],v14 offset0:34 offset1:35
M 48,v[160:163],192
M 48,v[164:167],188
R 128,2,2688
M 48,v[168:171],184
L 15
ds_read2_b32 v[100:101],v14 offset0:160 offset1:161
M 52,v[152:155],204
ds_read2_b32 v[102:103],v14 offset0:162 offset1:163
M 52,v[156:159],200
R 132,2,2176
M 52,v[160:163],196
M 52,v[164:167],192
ds_read2_b32 v[104:105],v15 offset0:32 offset1:33
M 52,v[168:171],188
ds_read2_b32 v[106:107],v15 offset0:34 offset1:35
M 52,v[172:175],184
L 15
R 136,2,1664
M 56,v[152:155],208
ds_read2_b32 v[108:109],v15 offset0:160 offset1:161
M 56,v[156:159],204
M 56,v[160:163],200
ds_read2_b32 v[110:111],v15 offset0:162 offset1:163
M 56,v[164:167],196
R 140,2,1152
M 56,v[168:171],192
ds_read2_b32 v[112:113],v16 offset0:32 offset1:33
M 56,v[172:175],188
ds_read2_b32 v[114:115],v16 offset0:34 offset1:35
M 56,v[176:179],184
L 15
M 60,v[152:155],212
R 144,2,640
M 60,v[156:159],208
ds_read2_b32 v[116:117],v16 offset0:160 offset1:161
M 60,v[160:163],204
ds_read2_b32 v[118:119],v16 offset0:162 offset1:163
M 60,v[164:167],200
R 148,2,128
M 60,v[168:171],196
M 60,v[172:175],192
M 60,v[176:179],188
M 60,v[180:183],184
L 15
M 40,v[88:91],128
ds_read2_b32 v[152:153],v1 offset0:48 offset1:49
M 40,v[92:95],124
ds_read2_b32 v[154:155],v1 offset0:50 offset1:51
M 40,v[96:99],120
L 14
R 184,2,3776
M 44,v[88:91],132
ds_read2_b32 v[156:157],v1 offset0:176 offset1:177
M 44,v[92:95],128
M 44,v[96:99],124
ds_read2_b32 v[158:159],v1 offset0:178 offset1:179
M 44,v[100:103],120
L 14
R 188,2,3264
M 48,v[88:91],136
ds_read2_b32 v[160:161],v14 offset0:48 offset1:49
M 48,v[92:95],132
ds_read2_b32 v[162:163],v14 offset0:50 offset1:51
M 48,v[96:99],128
M 48,v[100:103],124
R 192,2,2752
M 48,v[104:107],120
L 15
ds_read2_b32 v[164:165],v14 offset0:176 offset1:177
M 52,v[88:91],140
s_mov_b32 m0,163584
s_mov_b32 s78,0
ds_read2_b32 v[166:167],v14 offset0:178 offset1:179
M 52,v[92:95],136
R 196,2,2240
M 52,v[96:99],132
buffer_load_dword v11,s[40:43],s78 offen offset:0  lds
M 52,v[100:103],128
ds_read2_b32 v[168:169],v15 offset0:48 offset1:49
M 52,v[104:107],124
ds_read2_b32 v[170:171],v15 offset0:50 offset1:51
M 52,v[108:111],120
L 15
R 200,2,1728
M 56,v[88:91],144
ds_read2_b32 v[172:173],v15 offset0:176 offset1:177
M 56,v[92:95],140
M 56,v[96:99],136
ds_read2_b32 v[174:175],v15 offset0:178 offset1:179
M 56,v[100:103],132
R 204,2,1216
M 56,v[104:107],128
ds_read2_b32 v[176:177],v16 offset0:48 offset1:49
M 56,v[108:111],124
ds_read2_b32 v[178:179],v16 offset0:50 offset1:51
M 56,v[112:115],120
L 15
M 60,v[88:91],148
R 208,2,704
M 60,v[92:95],144
ds_read2_b32 v[180:181],v16 offset0:176 offset1:177
M 60,v[96:99],140
ds_read2_b32 v[182:183],v16 offset0:178 offset1:179
M 60,v[100:103],136
R 212,2,192
M 60,v[104:107],132
M 60,v[108:111],128
M 60,v[112:115],124
M 60,v[116:119],120
L 15
M 40,v[152:155],192
ds_read2_b32 v[88:89],v1 offset0:64 offset1:65
M 40,v[156:159],188
ds_read2_b32 v[90:91],v1 offset0:66 offset1:67
M 40,v[160:163],184
L 14
R 120,2,3840
M 44,v[152:155],196
ds_read2_b32 v[92:93],v1 offset0:192 offset1:193
M 44,v[156:159],192
M 44,v[160:163],188
ds_read2_b32 v[94:95],v1 offset0:194 offset1:195
M 44,v[164:167],184
L 14
R 124,2,3328
M 48,v[152:155],200
ds_read2_b32 v[96:97],v14 offset0:64 offset1:65
M 48,v[156:159],196
ds_read2_b32 v[98:99],v14 offset0:66 offset1:67
M 48,v[160:163],192
M 48,v[164:167],188
R 128,2,2816
M 48,v[168:171],184
L 15
ds_read2_b32 v[100:101],v14 offset0:192 offset1:193
M 52,v[152:155],204
ds_read2_b32 v[102:103],v14 offset0:194 offset1:195
M 52,v[156:159],200
R 132,2,2304
M 52,v[160:163],196
M 52,v[164:167],192
ds_read2_b32 v[104:105],v15 offset0:64 offset1:65
M 52,v[168:171],188
ds_read2_b32 v[106:107],v15 offset0:66 offset1:67
M 52,v[172:175],184
L 15
R 136,2,1792
M 56,v[152:155],208
ds_read2_b32 v[108:109],v15 offset0:192 offset1:193
M 56,v[156:159],204
M 56,v[160:163],200
ds_read2_b32 v[110:111],v15 offset0:194 offset1:195
M 56,v[164:167],196
R 140,2,1280
M 56,v[168:171],192
ds_read2_b32 v[112:113],v16 offset0:64 offset1:65
M 56,v[172:175],188
ds_read2_b32 v[114:115],v16 offset0:66 offset1:67
M 56,v[176:179],184
L 15
M 60,v[152:155],212
R 144,2,768
M 60,v[156:159],208
ds_read2_b32 v[116:117],v16 offset0:192 offset1:193
M 60,v[160:163],204
ds_read2_b32 v[118:119],v16 offset0:194 offset1:195
M 60,v[164:167],200
R 148,2,256
M 60,v[168:171],196
M 60,v[172:175],192
M 60,v[176:179],188
M 60,v[180:183],184
L 15
M 40,v[88:91],128
ds_read2_b32 v[152:153],v1 offset0:80 offset1:81
M 40,v[92:95],124
ds_read2_b32 v[154:155],v1 offset0:82 offset1:83
M 40,v[96:99],120
L 14
R 184,2,3904
M 44,v[88:91],132
ds_read2_b32 v[156:157],v1 offset0:208 offset1:209
M 44,v[92:95],128
M 44,v[96:99],124
ds_read2_b32 v[158:159],v1 offset0:210 offset1:211
M 44,v[100:103],120
L 14
R 188,2,3392
M 48,v[88:91],136
ds_read2_b32 v[160:161],v14 offset0:80 offset1:81
M 48,v[92:95],132
ds_read2_b32 v[162:163],v14 offset0:82 offset1:83
M 48,v[96:99],128
M 48,v[100:103],124
R 192,2,2880
M 48,v[104:107],120
L 15
ds_read2_b32 v[164:165],v14 offset0:208 offset1:209
M 52,v[88:91],140
ds_read2_b32 v[166:167],v14 offset0:210 offset1:211
M 52,v[92:95],136
R 196,2,2368
M 52,v[96:99],132
M 52,v[100:103],128
ds_read2_b32 v[168:169],v15 offset0:80 offset1:81
M 52,v[104:107],124
ds_read2_b32 v[170:171],v15 offset0:82 offset1:83
M 52,v[108:111],120
L 15
R 200,2,1856
M 56,v[88:91],144
ds_read2_b32 v[172:173],v15 offset0:208 offset1:209
M 56,v[92:95],140
M 56,v[96:99],136
ds_read2_b32 v[174:175],v15 offset0:210 offset1:211
M 56,v[100:103],132
R 204,2,1344
M 56,v[104:107],128
ds_read2_b32 v[176:177],v16 offset0:80 offset1:81
M 56,v[108:111],124
ds_read2_b32 v[178:179],v16 offset0:82 offset1:83
M 56,v[112:115],120
L 15
M 60,v[88:91],148
R 208,2,832
M 60,v[92:95],144
ds_read2_b32 v[180:181],v16 offset0:208 offset1:209
M 60,v[96:99],140
ds_read2_b32 v[182:183],v16 offset0:210 offset1:211
M 60,v[100:103],136
R 212,2,320
M 60,v[104:107],132
M 60,v[108:111],128
M 60,v[112:115],124
M 60,v[116:119],120
L 15
M 40,v[152:155],192
ds_read2_b32 v[88:89],v1 offset0:96 offset1:97
M 40,v[156:159],188
ds_read2_b32 v[90:91],v1 offset0:98 offset1:99
M 40,v[160:163],184
L 14
R 120,2,3968
M 44,v[152:155],196
s_mov_b32 m0,163584
s_mov_b32 s78,3741696
ds_read2_b32 v[92:93],v1 offset0:224 offset1:225
M 44,v[156:159],192
M 44,v[160:163],188
ds_read2_b32 v[94:95],v1 offset0:226 offset1:227
M 44,v[164:167],184
L 14
R 124,2,3456
M 48,v[152:155],200
ds_read2_b32 v[96:97],v14 offset0:96 offset1:97
M 48,v[156:159],196
ds_read2_b32 v[98:99],v14 offset0:98 offset1:99
M 48,v[160:163],192
buffer_load_dword v11,s[40:43],s78 offen offset:0  lds
M 48,v[164:167],188
R 128,2,2944
M 48,v[168:171],184
L 15
ds_read2_b32 v[100:101],v14 offset0:224 offset1:225
M 52,v[152:155],204
ds_read2_b32 v[102:103],v14 offset0:226 offset1:227
M 52,v[156:159],200
R 132,2,2432
M 52,v[160:163],196
M 52,v[164:167],192
ds_read2_b32 v[104:105],v15 offset0:96 offset1:97
M 52,v[168:171],188
ds_read2_b32 v[106:107],v15 offset0:98 offset1:99
M 52,v[172:175],184
L 15
R 136,2,1920
M 56,v[152:155],208
ds_read2_b32 v[108:109],v15 offset0:224 offset1:225
M 56,v[156:159],204
M 56,v[160:163],200
ds_read2_b32 v[110:111],v15 offset0:226 offset1:227
M 56,v[164:167],196
R 140,2,1408
M 56,v[168:171],192
ds_read2_b32 v[112:113],v16 offset0:96 offset1:97
M 56,v[172:175],188
ds_read2_b32 v[114:115],v16 offset0:98 offset1:99
M 56,v[176:179],184
L 15
M 60,v[152:155],212
R 144,2,896
M 60,v[156:159],208
ds_read2_b32 v[116:117],v16 offset0:224 offset1:225
M 60,v[160:163],204
ds_read2_b32 v[118:119],v16 offset0:226 offset1:227
M 60,v[164:167],200
R 148,2,384
M 60,v[168:171],196
M 60,v[172:175],192
M 60,v[176:179],188
M 60,v[180:183],184
L 15
M 40,v[88:91],128
ds_read2_b32 v[152:153],v1 offset0:112 offset1:113
M 40,v[92:95],124
ds_read2_b32 v[154:155],v1 offset0:114 offset1:115
M 40,v[96:99],120
L 14
R 184,2,4032
M 44,v[88:91],132
ds_read2_b32 v[156:157],v1 offset0:240 offset1:241
M 44,v[92:95],128
M 44,v[96:99],124
ds_read2_b32 v[158:159],v1 offset0:242 offset1:243
v_add_u32 v1,12928,v1
M 44,v[100:103],120
L 14
R 188,2,3520
M 48,v[88:91],136
ds_read2_b32 v[160:161],v14 offset0:112 offset1:113
M 48,v[92:95],132
ds_read2_b32 v[162:163],v14 offset0:114 offset1:115
M 48,v[96:99],128
M 48,v[100:103],124
R 192,2,3008
M 48,v[104:107],120
L 15
ds_read2_b32 v[164:165],v14 offset0:240 offset1:241
M 52,v[88:91],140
ds_read2_b32 v[166:167],v14 offset0:242 offset1:243
v_add_u32 v14,12928,v14
M 52,v[92:95],136
R 196,2,2496
M 52,v[96:99],132
M 52,v[100:103],128
ds_read2_b32 v[168:169],v15 offset0:112 offset1:113
M 52,v[104:107],124
ds_read2_b32 v[170:171],v15 offset0:114 offset1:115
M 52,v[108:111],120
L 15
R 200,2,1984
M 56,v[88:91],144
ds_read2_b32 v[172:173],v15 offset0:240 offset1:241
M 56,v[92:95],140
M 56,v[96:99],136
ds_read2_b32 v[174:175],v15 offset0:242 offset1:243
v_add_u32 v15,12928,v15
M 56,v[100:103],132
R 204,2,1472
M 56,v[104:107],128
ds_read2_b32 v[176:177],v16 offset0:112 offset1:113
M 56,v[108:111],124
ds_read2_b32 v[178:179],v16 offset0:114 offset1:115
M 56,v[112:115],120
L 15
M 60,v[88:91],148
R 208,2,960
M 60,v[92:95],144
ds_read2_b32 v[180:181],v16 offset0:240 offset1:241
M 60,v[96:99],140
ds_read2_b32 v[182:183],v16 offset0:242 offset1:243
v_add_u32 v16,12928,v16
M 60,v[100:103],136
)"
R"(R 212,2,448
M 60,v[104:107],132
M 60,v[108:111],128
M 60,v[112:115],124
M 60,v[116:119],120
L 15
M 40,v[152:155],192
ds_read2_b32 v[88:89],v1 offset0:0 offset1:1
M 40,v[156:159],188
ds_read2_b32 v[90:91],v1 offset0:2 offset1:3
M 40,v[160:163],184
L 14
R 120,2,16512
M 44,v[152:155],196
ds_read2_b32 v[92:93],v1 offset0:128 offset1:129
M 44,v[156:159],192
M 44,v[160:163],188
ds_read2_b32 v[94:95],v1 offset0:130 offset1:131
M 44,v[164:167],184
L 14
R 124,2,16000
M 48,v[152:155],200
ds_read2_b32 v[96:97],v14 offset0:0 offset1:1
M 48,v[156:159],196
ds_read2_b32 v[98:99],v14 offset0:2 offset1:3
M 48,v[160:163],192
M 48,v[164:167],188
R 128,2,15488
M 48,v[168:171],184
L 15
ds_read2_b32 v[100:101],v14 offset0:128 offset1:129
M 52,v[152:155],204
ds_read2_b32 v[102:103],v14 offset0:130 offset1:131
M 52,v[156:159],200
R 132,2,14976
M 52,v[160:163],196
M 52,v[164:167],192
ds_read2_b32 v[104:105],v15 offset0:0 offset1:1
M 52,v[168:171],188
ds_read2_b32 v[106:107],v15 offset0:2 offset1:3
M 52,v[172:175],184
L 15
R 136,2,14464
M 56,v[152:155],208
ds_read2_b32 v[108:109],v15 offset0:128 offset1:129
M 56,v[156:159],204
M 56,v[160:163],200
ds_read2_b32 v[110:111],v15 offset0:130 offset1:131
M 56,v[164:167],196
R 140,2,13952
M 56,v[168:171],192
ds_read2_b32 v[112:113],v16 offset0:0 offset1:1
M 56,v[172:175],188
ds_read2_b32 v[114:115],v16 offset0:2 offset1:3
M 56,v[176:179],184
L 15
M 60,v[152:155],212
R 144,2,13440
M 60,v[156:159],208
ds_read2_b32 v[116:117],v16 offset0:128 offset1:129
M 60,v[160:163],204
ds_read2_b32 v[118:119],v16 offset0:130 offset1:131
M 60,v[164:167],200
R 148,2,12928
M 60,v[168:171],196
M 60,v[172:175],192
M 60,v[176:179],188
M 60,v[180:183],184
s_cmp_eq_u32 s51,2
s_cbranch_scc1 Lcv_d_%=
ds_read_b64 v[24:25],v3 offset:2560
ds_read_b64 v[26:27],v3 offset:2048
ds_read_b64 v[28:29],v3 offset:1536
ds_read_b64 v[30:31],v3 offset:1024
ds_read_b64 v[32:33],v3 offset:512
ds_read_b64 v[34:35],v3 offset:0
L 0
v_and_b32 v36,0xffff0000,v25
v_lshlrev_b32 v37,16,v25
v_and_b32 v38,0xffff0000,v24
v_lshlrev_b32 v39,16,v24
v_fma_f32 v40,v36,s86,v40
v_fma_f32 v41,v37,s86,v41
v_fma_f32 v42,v38,s86,v42
v_fma_f32 v43,v39,s86,v43
v_cvt_pk_bf16_f32 v40,v40,v41
v_cvt_pk_bf16_f32 v41,v42,v43
ds_write_b64 v4,v[40:41] offset:0
v_and_b32 v36,0xffff0000,v27
v_lshlrev_b32 v37,16,v27
v_and_b32 v38,0xffff0000,v26
v_lshlrev_b32 v39,16,v26
v_fma_f32 v44,v36,s86,v44
v_fma_f32 v45,v37,s86,v45
v_fma_f32 v46,v38,s86,v46
v_fma_f32 v47,v39,s86,v47
v_cvt_pk_bf16_f32 v44,v44,v45
v_cvt_pk_bf16_f32 v45,v46,v47
ds_write_b64 v4,v[44:45] offset:512
v_and_b32 v36,0xffff0000,v29
v_lshlrev_b32 v37,16,v29
v_and_b32 v38,0xffff0000,v28
v_lshlrev_b32 v39,16,v28
v_fma_f32 v48,v36,s86,v48
v_fma_f32 v49,v37,s86,v49
v_fma_f32 v50,v38,s86,v50
v_fma_f32 v51,v39,s86,v51
v_cvt_pk_bf16_f32 v48,v48,v49
v_cvt_pk_bf16_f32 v49,v50,v51
ds_write_b64 v4,v[48:49] offset:1024
v_and_b32 v36,0xffff0000,v31
v_lshlrev_b32 v37,16,v31
v_and_b32 v38,0xffff0000,v30
v_lshlrev_b32 v39,16,v30
v_fma_f32 v52,v36,s86,v52
v_fma_f32 v53,v37,s86,v53
v_fma_f32 v54,v38,s86,v54
v_fma_f32 v55,v39,s86,v55
v_cvt_pk_bf16_f32 v52,v52,v53
v_cvt_pk_bf16_f32 v53,v54,v55
ds_write_b64 v4,v[52:53] offset:1536
v_and_b32 v36,0xffff0000,v33
v_lshlrev_b32 v37,16,v33
v_and_b32 v38,0xffff0000,v32
v_lshlrev_b32 v39,16,v32
v_fma_f32 v56,v36,s86,v56
v_fma_f32 v57,v37,s86,v57
v_fma_f32 v58,v38,s86,v58
v_fma_f32 v59,v39,s86,v59
v_cvt_pk_bf16_f32 v56,v56,v57
v_cvt_pk_bf16_f32 v57,v58,v59
ds_write_b64 v4,v[56:57] offset:2048
v_and_b32 v36,0xffff0000,v35
v_lshlrev_b32 v37,16,v35
v_and_b32 v38,0xffff0000,v34
v_lshlrev_b32 v39,16,v34
v_fma_f32 v60,v36,s86,v60
v_fma_f32 v61,v37,s86,v61
v_fma_f32 v62,v38,s86,v62
v_fma_f32 v63,v39,s86,v63
v_cvt_pk_bf16_f32 v60,v60,v61
v_cvt_pk_bf16_f32 v61,v62,v63
ds_write_b64 v4,v[60:61] offset:2560
s_branch Lcv_e_%=
Lcv_d_%=:
L 15
v_mfma_f32_16x16x32_bf16 v[64:67],v[88:91],v[128:131],0
ds_read2_b32 v[152:153],v1 offset0:16 offset1:17
ds_read_b64 v[24:25],v3 offset:2560
M 64,v[92:95],124
ds_read2_b32 v[154:155],v1 offset0:18 offset1:19
ds_read_b64 v[26:27],v3 offset:2048
M 64,v[96:99],120
L 15
R 184,2,16576
ds_read_b64 v[28:29],v3 offset:1536
v_mfma_f32_16x16x32_bf16 v[68:71],v[88:91],v[132:135],0
ds_read2_b32 v[156:157],v1 offset0:144 offset1:145
ds_read_b64 v[30:31],v3 offset:1024
M 68,v[92:95],128
ds_read_b64 v[32:33],v3 offset:512
ds_read_b64 v[34:35],v3 offset:0
M 68,v[96:99],124
ds_read2_b32 v[158:159],v1 offset0:146 offset1:147
M 68,v[100:103],120
L 15
R 188,2,16064
v_mfma_f32_16x16x32_bf16 v[72:75],v[88:91],v[136:139],0
ds_read2_b32 v[160:161],v14 offset0:16 offset1:17
M 72,v[92:95],132
s_mov_b32 m0,163584
s_mov_b32 s78,0
ds_read2_b32 v[162:163],v14 offset0:18 offset1:19
M 72,v[96:99],128
M 72,v[100:103],124
L 4
R 192,2,15552
v_and_b32 v36,0xffff0000,v25
M 72,v[104:107],120
s_nop 0
ds_read2_b32 v[164:165],v14 offset0:144 offset1:145
v_lshlrev_b32 v37,16,v25
v_mfma_f32_16x16x32_bf16 v[76:79],v[88:91],v[140:143],0
ds_read2_b32 v[166:167],v14 offset0:146 offset1:147
v_and_b32 v38,0xffff0000,v24
M 76,v[92:95],136
R 196,2,15040
v_lshlrev_b32 v39,16,v24
M 76,v[96:99],132
buffer_load_dword v12,s[60:63],s78 offen offset:0  lds
M 76,v[100:103],128
ds_read2_b32 v[168:169],v15 offset0:16 offset1:17
v_fma_f32 v40,v36,s86,v40
M 76,v[104:107],124
ds_read2_b32 v[170:171],v15 offset0:18 offset1:19
v_fma_f32 v41,v37,s86,v41
M 76,v[108:111],120
s_nop 0
R 200,2,14528
v_fma_f32 v42,v38,s86,v42
v_mfma_f32_16x16x32_bf16 v[80:83],v[88:91],v[144:147],0
ds_read2_b32 v[172:173],v15 offset0:144 offset1:145
v_fma_f32 v43,v39,s86,v43
M 80,v[92:95],140
v_cvt_pk_bf16_f32 v40,v40,v41
v_cvt_pk_bf16_f32 v41,v42,v43
M 80,v[96:99],136
ds_read2_b32 v[174:175],v15 offset0:146 offset1:147
v_and_b32 v36,0xffff0000,v27
M 80,v[100:103],132
R 204,2,14016
v_lshlrev_b32 v37,16,v27
M 80,v[104:107],128
ds_read2_b32 v[176:177],v16 offset0:16 offset1:17
v_and_b32 v38,0xffff0000,v26
M 80,v[108:111],124
ds_read2_b32 v[178:179],v16 offset0:18 offset1:19
v_lshlrev_b32 v39,16,v26
M 80,v[112:115],120
s_nop 0
ds_write_b64 v4,v[40:41] offset:0
v_mfma_f32_16x16x32_bf16 v[84:87],v[88:91],v[148:151],0
R 208,2,13504
v_fma_f32 v44,v36,s86,v44
M 84,v[92:95],144
ds_read2_b32 v[180:181],v16 offset0:144 offset1:145
v_fma_f32 v45,v37,s86,v45
M 84,v[96:99],140
ds_read2_b32 v[182:183],v16 offset0:146 offset1:147
v_fma_f32 v46,v38,s86,v46
M 84,v[100:103],136
R 212,2,12992
v_fma_f32 v47,v39,s86,v47
M 84,v[104:107],132
v_cvt_pk_bf16_f32 v44,v44,v45
v_cvt_pk_bf16_f32 v45,v46,v47
M 84,v[108:111],128
ds_write_b64 v4,v[44:45] offset:512
M 84,v[112:115],124
v_and_b32 v36,0xffff0000,v29
v_lshlrev_b32 v37,16,v29
M 84,v[116:119],120
L 15
v_and_b32 v38,0xffff0000,v28
v_lshlrev_b32 v39,16,v28
M 64,v[152:155],192
ds_read2_b32 v[88:89],v1 offset0:32 offset1:33
v_fma_f32 v48,v36,s86,v48
M 64,v[156:159],188
ds_read2_b32 v[90:91],v1 offset0:34 offset1:35
v_fma_f32 v49,v37,s86,v49
M 64,v[160:163],184
L 15
R 120,2,16640
v_fma_f32 v50,v38,s86,v50
M 68,v[152:155],196
ds_read2_b32 v[92:93],v1 offset0:160 offset1:161
v_fma_f32 v51,v39,s86,v51
M 68,v[156:159],192
v_cvt_pk_bf16_f32 v48,v48,v49
v_cvt_pk_bf16_f32 v49,v50,v51
M 68,v[160:163],188
ds_read2_b32 v[94:95],v1 offset0:162 offset1:163
v_and_b32 v36,0xffff0000,v31
M 68,v[164:167],184
L 15
R 124,2,16128
v_lshlrev_b32 v37,16,v31
M 72,v[152:155],200
ds_read2_b32 v[96:97],v14 offset0:32 offset1:33
v_and_b32 v38,0xffff0000,v30
M 72,v[156:159],196
ds_read2_b32 v[98:99],v14 offset0:34 offset1:35
v_lshlrev_b32 v39,16,v30
M 72,v[160:163],192
ds_write_b64 v4,v[48:49] offset:1024
M 72,v[164:167],188
R 128,2,15616
v_fma_f32 v52,v36,s86,v52
M 72,v[168:171],184
L 15
ds_read2_b32 v[100:101],v14 offset0:160 offset1:161
v_fma_f32 v53,v37,s86,v53
M 76,v[152:155],204
ds_read2_b32 v[102:103],v14 offset0:162 offset1:163
v_fma_f32 v54,v38,s86,v54
M 76,v[156:159],200
R 132,2,15104
v_fma_f32 v55,v39,s86,v55
M 76,v[160:163],196
v_cvt_pk_bf16_f32 v52,v52,v53
v_cvt_pk_bf16_f32 v53,v54,v55
M 76,v[164:167],192
ds_read2_b32 v[104:105],v15 offset0:32 offset1:33
v_and_b32 v36,0xffff0000,v33
M 76,v[168:171],188
ds_read2_b32 v[106:107],v15 offset0:34 offset1:35
v_lshlrev_b32 v37,16,v33
M 76,v[172:175],184
L 15
R 136,2,14592
v_and_b32 v38,0xffff0000,v32
M 80,v[152:155],208
ds_read2_b32 v[108:109],v15 offset0:160 offset1:161
v_lshlrev_b32 v39,16,v32
M 80,v[156:159],204
ds_write_b64 v4,v[52:53] offset:1536
M 80,v[160:163],200
ds_read2_b32 v[110:111],v15 offset0:162 offset1:163
v_fma_f32 v56,v36,s86,v56
M 80,v[164:167],196
R 140,2,14080
v_fma_f32 v57,v37,s86,v57
M 80,v[168:171],192
ds_read2_b32 v[112:113],v16 offset0:32 offset1:33
v_fma_f32 v58,v38,s86,v58
M 80,v[172:175],188
ds_read2_b32 v[114:115],v16 offset0:34 offset1:35
v_fma_f32 v59,v39,s86,v59
M 80,v[176:179],184
L 15
v_cvt_pk_bf16_f32 v56,v56,v57
v_cvt_pk_bf16_f32 v57,v58,v59
M 84,v[152:155],212
R 144,2,13568
v_and_b32 v36,0xffff0000,v35
M 84,v[156:159],208
ds_read2_b32 v[116:117],v16 offset0:160 offset1:161
v_lshlrev_b32 v37,16,v35
M 84,v[160:163],204
ds_read2_b32 v[118:119],v16 offset0:162 offset1:163
v_and_b32 v38,0xffff0000,v34
M 84,v[164:167],200
R 148,2,13056
v_lshlrev_b32 v39,16,v34
M 84,v[168:171],196
ds_write_b64 v4,v[56:57] offset:2048
M 84,v[172:175],192
v_fma_f32 v60,v36,s86,v60
v_fma_f32 v61,v37,s86,v61
M 84,v[176:179],188
v_fma_f32 v62,v38,s86,v62
v_fma_f32 v63,v39,s86,v63
M 84,v[180:183],184
L 15
v_cvt_pk_bf16_f32 v60,v60,v61
v_cvt_pk_bf16_f32 v61,v62,v63
M 64,v[88:91],128
ds_read2_b32 v[152:153],v1 offset0:48 offset1:49
M 64,v[92:95],124
ds_read2_b32 v[154:155],v1 offset0:50 offset1:51
M 64,v[96:99],120
L 15
R 184,2,16704
M 68,v[88:91],132
ds_read2_b32 v[156:157],v1 offset0:176 offset1:177
M 68,v[92:95],128
ds_write_b64 v4,v[60:61] offset:2560
M 68,v[96:99],124
ds_read2_b32 v[158:159],v1 offset0:178 offset1:179
M 68,v[100:103],120
L 15
R 188,2,16192
M 72,v[88:91],136
ds_read2_b32 v[160:161],v14 offset0:48 offset1:49
M 72,v[92:95],132
ds_read2_b32 v[162:163],v14 offset0:50 offset1:51
M 72,v[96:99],128
M 72,v[100:103],124
R 192,2,15680
M 72,v[104:107],120
L 15
ds_read2_b32 v[164:165],v14 offset0:176 offset1:177
M 76,v[88:91],140
ds_read2_b32 v[166:167],v14 offset0:178 offset1:179
M 76,v[92:95],136
R 196,2,15168
M 76,v[96:99],132
M 76,v[100:103],128
ds_read2_b32 v[168:169],v15 offset0:48 offset1:49
M 76,v[104:107],124
ds_read2_b32 v[170:171],v15 offset0:50 offset1:51
M 76,v[108:111],120
L 15
R 200,2,14656
M 80,v[88:91],144
ds_read2_b32 v[172:173],v15 offset0:176 offset1:177
M 80,v[92:95],140
M 80,v[96:99],136
ds_read2_b32 v[174:175],v15 offset0:178 offset1:179
M 80,v[100:103],132
R 204,2,14144
M 80,v[104:107],128
s_mov_b32 m0,163584
s_mov_b32 s78,1847296
ds_read2_b32 v[176:177],v16 offset0:48 offset1:49
M 80,v[108:111],124
ds_read2_b32 v[178:179],v16 offset0:50 offset1:51
M 80,v[112:115],120
L 15
buffer_load_dword v12,s[60:63],s78 offen offset:0  lds
M 84,v[88:91],148
R 208,2,13632
M 84,v[92:95],144
ds_read2_b32 v[180:181],v16 offset0:176 offset1:177
M 84,v[96:99],140
ds_read2_b32 v[182:183],v16 offset0:178 offset1:179
M 84,v[100:103],136
R 212,2,13120
M 84,v[104:107],132
M 84,v[108:111],128
M 84,v[112:115],124
M 84,v[116:119],120
L 15
M 64,v[152:155],192
ds_read2_b32 v[88:89],v1 offset0:64 offset1:65
M 64,v[156:159],188
ds_read2_b32 v[90:91],v1 offset0:66 offset1:67
M 64,v[160:163],184
L 14
R 120,2,16768
M 68,v[152:155],196
ds_read2_b32 v[92:93],v1 offset0:192 offset1:193
M 68,v[156:159],192
M 68,v[160:163],188
ds_read2_b32 v[94:95],v1 offset0:194 offset1:195
M 68,v[164:167],184
L 14
R 124,2,16256
M 72,v[152:155],200
ds_read2_b32 v[96:97],v14 offset0:64 offset1:65
M 72,v[156:159],196
ds_read2_b32 v[98:99],v14 offset0:66 offset1:67
M 72,v[160:163],192
M 72,v[164:167],188
R 128,2,15744
M 72,v[168:171],184
L 15
ds_read2_b32 v[100:101],v14 offset0:192 offset1:193
M 76,v[152:155],204
ds_read2_b32 v[102:103],v14 offset0:194 offset1:195
M 76,v[156:159],200
R 132,2,15232
M 76,v[160:163],196
M 76,v[164:167],192
ds_read2_b32 v[104:105],v15 offset0:64 offset1:65
M 76,v[168:171],188
ds_read2_b32 v[106:107],v15 offset0:66 offset1:67
M 76,v[172:175],184
L 15
R 136,2,14720
M 80,v[152:155],208
ds_read2_b32 v[108:109],v15 offset0:192 offset1:193
M 80,v[156:159],204
M 80,v[160:163],200
ds_read2_b32 v[110:111],v15 offset0:194 offset1:195
M 80,v[164:167],196
R 140,2,14208
M 80,v[168:171],192
ds_read2_b32 v[112:113],v16 offset0:64 offset1:65
M 80,v[172:175],188
ds_read2_b32 v[114:115],v16 offset0:66 offset1:67
M 80,v[176:179],184
L 15
M 84,v[152:155],212
R 144,2,13696
M 84,v[156:159],208
ds_read2_b32 v[116:117],v16 offset0:192 offset1:193
M 84,v[160:163],204
ds_read2_b32 v[118:119],v16 offset0:194 offset1:195
M 84,v[164:167],200
R 148,2,13184
M 84,v[168:171],196
M 84,v[172:175],192
M 84,v[176:179],188
M 84,v[180:183],184
L 15
M 64,v[88:91],128
ds_read2_b32 v[152:153],v1 offset0:80 offset1:81
M 64,v[92:95],124
ds_read2_b32 v[154:155],v1 offset0:82 offset1:83
M 64,v[96:99],120
L 14
R 184,2,16832
M 68,v[88:91],132
ds_read2_b32 v[156:157],v1 offset0:208 offset1:209
M 68,v[92:95],128
M 68,v[96:99],124
ds_read2_b32 v[158:159],v1 offset0:210 offset1:211
M 68,v[100:103],120
L 14
R 188,2,16320
M 72,v[88:91],136
ds_read2_b32 v[160:161],v14 offset0:80 offset1:81
M 72,v[92:95],132
ds_read2_b32 v[162:163],v14 offset0:82 offset1:83
M 72,v[96:99],128
M 72,v[100:103],124
R 192,2,15808
M 72,v[104:107],120
)"
R"(L 15
ds_read2_b32 v[164:165],v14 offset0:208 offset1:209
M 76,v[88:91],140
ds_read2_b32 v[166:167],v14 offset0:210 offset1:211
M 76,v[92:95],136
R 196,2,15296
M 76,v[96:99],132
M 76,v[100:103],128
ds_read2_b32 v[168:169],v15 offset0:80 offset1:81
M 76,v[104:107],124
ds_read2_b32 v[170:171],v15 offset0:82 offset1:83
M 76,v[108:111],120
L 15
R 200,2,14784
M 80,v[88:91],144
ds_read2_b32 v[172:173],v15 offset0:208 offset1:209
M 80,v[92:95],140
M 80,v[96:99],136
ds_read2_b32 v[174:175],v15 offset0:210 offset1:211
M 80,v[100:103],132
R 204,2,14272
M 80,v[104:107],128
ds_read2_b32 v[176:177],v16 offset0:80 offset1:81
M 80,v[108:111],124
ds_read2_b32 v[178:179],v16 offset0:82 offset1:83
M 80,v[112:115],120
L 15
M 84,v[88:91],148
R 208,2,13760
M 84,v[92:95],144
ds_read2_b32 v[180:181],v16 offset0:208 offset1:209
M 84,v[96:99],140
ds_read2_b32 v[182:183],v16 offset0:210 offset1:211
M 84,v[100:103],136
R 212,2,13248
M 84,v[104:107],132
M 84,v[108:111],128
M 84,v[112:115],124
M 84,v[116:119],120
L 15
M 64,v[152:155],192
ds_read2_b32 v[88:89],v1 offset0:96 offset1:97
M 64,v[156:159],188
ds_read2_b32 v[90:91],v1 offset0:98 offset1:99
M 64,v[160:163],184
L 14
R 120,2,16896
M 68,v[152:155],196
ds_read2_b32 v[92:93],v1 offset0:224 offset1:225
M 68,v[156:159],192
M 68,v[160:163],188
ds_read2_b32 v[94:95],v1 offset0:226 offset1:227
M 68,v[164:167],184
L 14
R 124,2,16384
M 72,v[152:155],200
ds_read2_b32 v[96:97],v14 offset0:96 offset1:97
M 72,v[156:159],196
ds_read2_b32 v[98:99],v14 offset0:98 offset1:99
M 72,v[160:163],192
M 72,v[164:167],188
R 128,2,15872
M 72,v[168:171],184
L 15
ds_read2_b32 v[100:101],v14 offset0:224 offset1:225
M 76,v[152:155],204
ds_read2_b32 v[102:103],v14 offset0:226 offset1:227
M 76,v[156:159],200
R 132,2,15360
M 76,v[160:163],196
M 76,v[164:167],192
ds_read2_b32 v[104:105],v15 offset0:96 offset1:97
M 76,v[168:171],188
ds_read2_b32 v[106:107],v15 offset0:98 offset1:99
M 76,v[172:175],184
L 15
R 136,2,14848
M 80,v[152:155],208
ds_read2_b32 v[108:109],v15 offset0:224 offset1:225
M 80,v[156:159],204
M 80,v[160:163],200
ds_read2_b32 v[110:111],v15 offset0:226 offset1:227
M 80,v[164:167],196
R 140,2,14336
M 80,v[168:171],192
ds_read2_b32 v[112:113],v16 offset0:96 offset1:97
M 80,v[172:175],188
ds_read2_b32 v[114:115],v16 offset0:98 offset1:99
M 80,v[176:179],184
L 15
M 84,v[152:155],212
R 144,2,13824
M 84,v[156:159],208
ds_read2_b32 v[116:117],v16 offset0:224 offset1:225
M 84,v[160:163],204
ds_read2_b32 v[118:119],v16 offset0:226 offset1:227
M 84,v[164:167],200
R 148,2,13312
M 84,v[168:171],196
M 84,v[172:175],192
M 84,v[176:179],188
M 84,v[180:183],184
L 15
M 64,v[88:91],128
ds_read2_b32 v[152:153],v1 offset0:112 offset1:113
M 64,v[92:95],124
ds_read2_b32 v[154:155],v1 offset0:114 offset1:115
M 64,v[96:99],120
L 14
R 184,2,16960
M 68,v[88:91],132
ds_read2_b32 v[156:157],v1 offset0:240 offset1:241
M 68,v[92:95],128
M 68,v[96:99],124
ds_read2_b32 v[158:159],v1 offset0:242 offset1:243
M 68,v[100:103],120
L 14
R 188,2,16448
M 72,v[88:91],136
ds_read2_b32 v[160:161],v14 offset0:112 offset1:113
M 72,v[92:95],132
ds_read2_b32 v[162:163],v14 offset0:114 offset1:115
M 72,v[96:99],128
M 72,v[100:103],124
R 192,2,15936
M 72,v[104:107],120
L 15
ds_read2_b32 v[164:165],v14 offset0:240 offset1:241
M 76,v[88:91],140
ds_read2_b32 v[166:167],v14 offset0:242 offset1:243
M 76,v[92:95],136
R 196,2,15424
M 76,v[96:99],132
M 76,v[100:103],128
ds_read2_b32 v[168:169],v15 offset0:112 offset1:113
M 76,v[104:107],124
ds_read2_b32 v[170:171],v15 offset0:114 offset1:115
M 76,v[108:111],120
L 15
R 200,2,14912
M 80,v[88:91],144
ds_read2_b32 v[172:173],v15 offset0:240 offset1:241
M 80,v[92:95],140
M 80,v[96:99],136
ds_read2_b32 v[174:175],v15 offset0:242 offset1:243
M 80,v[100:103],132
R 204,2,14400
M 80,v[104:107],128
ds_read2_b32 v[176:177],v16 offset0:112 offset1:113
M 80,v[108:111],124
ds_read2_b32 v[178:179],v16 offset0:114 offset1:115
M 80,v[112:115],120
L 15
M 84,v[88:91],148
R 208,2,13888
M 84,v[92:95],144
ds_read2_b32 v[180:181],v16 offset0:240 offset1:241
M 84,v[96:99],140
ds_read2_b32 v[182:183],v16 offset0:242 offset1:243
M 84,v[100:103],136
R 212,2,13376
M 84,v[104:107],132
M 84,v[108:111],128
M 84,v[112:115],124
M 84,v[116:119],120
L 15
M 64,v[152:155],192
M 64,v[156:159],188
M 64,v[160:163],184
L 12
M 68,v[152:155],196
M 68,v[156:159],192
M 68,v[160:163],188
M 68,v[164:167],184
L 9
M 72,v[152:155],200
M 72,v[156:159],196
M 72,v[160:163],192
ds_read_b64 v[24:25],v3 offset:15488
ds_read_b64 v[26:27],v3 offset:14976
M 72,v[164:167],188
ds_read_b64 v[28:29],v3 offset:14464
ds_read_b64 v[30:31],v3 offset:13952
M 72,v[168:171],184
L 10
ds_read_b64 v[32:33],v3 offset:13440
ds_read_b64 v[34:35],v3 offset:12928
M 76,v[152:155],204
M 76,v[156:159],200
M 76,v[160:163],196
M 76,v[164:167],192
M 76,v[168:171],188
M 76,v[172:175],184
L 9
M 80,v[152:155],208
M 80,v[156:159],204
M 80,v[160:163],200
M 80,v[164:167],196
M 80,v[168:171],192
M 80,v[172:175],188
M 80,v[176:179],184
L 6
M 84,v[152:155],212
M 84,v[156:159],208
M 84,v[160:163],204
M 84,v[164:167],200
M 84,v[168:171],196
M 84,v[172:175],192
M 84,v[176:179],188
M 84,v[180:183],184
L 0
v_and_b32 v36,0xffff0000,v25
v_lshlrev_b32 v37,16,v25
v_and_b32 v38,0xffff0000,v24
v_lshlrev_b32 v39,16,v24
v_fma_f32 v64,v36,s87,v64
v_fma_f32 v65,v37,s87,v65
v_fma_f32 v66,v38,s87,v66
v_fma_f32 v67,v39,s87,v67
v_cvt_pk_bf16_f32 v64,v64,v65
v_cvt_pk_bf16_f32 v65,v66,v67
ds_write_b64 v4,v[64:65] offset:12928
v_and_b32 v36,0xffff0000,v27
v_lshlrev_b32 v37,16,v27
v_and_b32 v38,0xffff0000,v26
v_lshlrev_b32 v39,16,v26
v_fma_f32 v68,v36,s87,v68
v_fma_f32 v69,v37,s87,v69
v_fma_f32 v70,v38,s87,v70
v_fma_f32 v71,v39,s87,v71
v_cvt_pk_bf16_f32 v68,v68,v69
v_cvt_pk_bf16_f32 v69,v70,v71
ds_write_b64 v4,v[68:69] offset:13440
v_and_b32 v36,0xffff0000,v29
v_lshlrev_b32 v37,16,v29
v_and_b32 v38,0xffff0000,v28
v_lshlrev_b32 v39,16,v28
v_fma_f32 v72,v36,s87,v72
v_fma_f32 v73,v37,s87,v73
v_fma_f32 v74,v38,s87,v74
v_fma_f32 v75,v39,s87,v75
v_cvt_pk_bf16_f32 v72,v72,v73
v_cvt_pk_bf16_f32 v73,v74,v75
ds_write_b64 v4,v[72:73] offset:13952
v_and_b32 v36,0xffff0000,v31
v_lshlrev_b32 v37,16,v31
v_and_b32 v38,0xffff0000,v30
v_lshlrev_b32 v39,16,v30
v_fma_f32 v76,v36,s87,v76
v_fma_f32 v77,v37,s87,v77
v_fma_f32 v78,v38,s87,v78
v_fma_f32 v79,v39,s87,v79
v_cvt_pk_bf16_f32 v76,v76,v77
v_cvt_pk_bf16_f32 v77,v78,v79
ds_write_b64 v4,v[76:77] offset:14464
v_and_b32 v36,0xffff0000,v33
v_lshlrev_b32 v37,16,v33
v_and_b32 v38,0xffff0000,v32
v_lshlrev_b32 v39,16,v32
v_fma_f32 v80,v36,s87,v80
v_fma_f32 v81,v37,s87,v81
v_fma_f32 v82,v38,s87,v82
v_fma_f32 v83,v39,s87,v83
v_cvt_pk_bf16_f32 v80,v80,v81
v_cvt_pk_bf16_f32 v81,v82,v83
ds_write_b64 v4,v[80:81] offset:14976
v_and_b32 v36,0xffff0000,v35
v_lshlrev_b32 v37,16,v35
v_and_b32 v38,0xffff0000,v34
v_lshlrev_b32 v39,16,v34
v_fma_f32 v84,v36,s87,v84
v_fma_f32 v85,v37,s87,v85
v_fma_f32 v86,v38,s87,v86
v_fma_f32 v87,v39,s87,v87
v_cvt_pk_bf16_f32 v84,v84,v85
v_cvt_pk_bf16_f32 v85,v86,v87
ds_write_b64 v4,v[84:85] offset:15488
Lcv_e_%=:
L 0
s_barrier
R 152,5,0
R 156,5,512
R 160,5,1024
R 164,5,1536
R 168,5,2048
R 172,5,2560
s_mov_b32 s78,99614720
L 5
buffer_store_dwordx4 v[152:155],v7,s[36:39],s78 offen offset:0 sc0 sc1
s_add_u32 s78,s56,100139008
L 4
buffer_store_dwordx4 v[156:159],v7,s[36:39],s78 offen offset:0 sc0 sc1
s_mov_b32 s78,100663296
L 3
buffer_store_dwordx4 v[160:163],v7,s[36:39],s78 offen offset:0 sc0 sc1
s_add_u32 s78,s56,101187584
L 2
buffer_store_dwordx4 v[164:167],v7,s[36:39],s78 offen offset:0 sc0 sc1
s_mov_b32 s78,101711872
L 1
buffer_store_dwordx4 v[168:171],v7,s[36:39],s78 offen offset:0 sc0 sc1
s_add_u32 s78,s56,102236160
L 0
buffer_store_dwordx4 v[172:175],v7,s[36:39],s78 offen offset:0 sc0 sc1
s_waitcnt vmcnt(0)
s_barrier
s_add_u32 s48,s48,32768
buffer_store_dword v23,v10,s[36:39],s48 offen offset:0 sc0 sc1
s_cmp_eq_u32 s51,2
s_cbranch_scc0 Lcv_f_%=
s_mov_b32 m0,163584
s_mov_b32 s78,3694592
buffer_load_dword v12,s[60:63],s78 offen offset:0  lds
s_mov_b32 m0,163584
s_mov_b32 s78,5541888
buffer_load_dword v12,s[60:63],s78 offen offset:0  lds
Lcv_f_%=:
Lcode_cv_e:
.purgem M
.purgem R
.purgem T
.purgem D
.purgem L
)"
  :: "s"(ws), "s"(__builtin_amdgcn_readfirstlane(sWave)), "s"(__builtin_amdgcn_readfirstlane(sBuf)), "s"(__builtin_amdgcn_readfirstlane(sAuxOff)), "s"(__builtin_amdgcn_readfirstlane(sFoutOff)), "s"(__builtin_amdgcn_readfirstlane(sC0)), "s"(__builtin_amdgcn_readfirstlane(sC1)), "s"(__builtin_amdgcn_readfirstlane(sExp)), "v"(vL16), "v"(vPA), "v"(vPB), "v"(vPU), "v"(vPW), "v"(vOR), "v"(vORA), "v"(vOG), "v"(vFA), "v"(vFB), "v"(vFlag), "v"(vTW), "v"(vTX), "s"(w3), "s"(w4) : "v0", "v1", "v2", "v3", "v4", "v5", "v6", "v7", "v8", "v9", "v10", "v11", "v12", "v13", "v14", "v15", "v16", "v17", "v18", "v19", "v20", "v21", "v22", "v23", "v24", "v25", "v26", "v27", "v28", "v29", "v30", "v31", "v32", "v33", "v34", "v35", "v36", "v37", "v38", "v39", "v40", "v41", "v42", "v43", "v44", "v45", "v46", "v47", "v48", "v49", "v50", "v51", "v52", "v53", "v54", "v55", "v56", "v57", "v58", "v59", "v60", "v61", "v62", "v63", "v64", "v65", "v66", "v67", "v68", "v69", "v70", "v71", "v72", "v73", "v74", "v75", "v76", "v77", "v78", "v79", "v80", "v81", "v82", "v83", "v84", "v85", "v86", "v87", "v88", "v89", "v90", "v91", "v92", "v93", "v94", "v95", "v96", "v97", "v98", "v99", "v100", "v101", "v102", "v103", "v104", "v105", "v106", "v107", "v108", "v109", "v110", "v111", "v112", "v113", "v114", "v115", "v116", "v117", "v118", "v119", "v120", "v121", "v122", "v123", "v124", "v125", "v126", "v127", "v128", "v129", "v130", "v131", "v132", "v133", "v134", "v135", "v136", "v137", "v138", "v139", "v140", "v141", "v142", "v143", "v144", "v145", "v146", "v147", "v148", "v149", "v150", "v151", "v152", "v153", "v154", "v155", "v156", "v157", "v158", "v159", "v160", "v161", "v162", "v163", "v164", "v165", "v166", "v167", "v168", "v169", "v170", "v171", "v172", "v173", "v174", "v175", "v176", "v177", "v178", "v179", "v180", "v181", "v182", "v183", "v184", "v185", "v186", "v187", "v188", "v189", "v190", "v191", "v192", "v193", "v194", "v195", "v196", "v197", "v198", "v199", "v200", "v201", "v202", "v203", "v204", "v205", "v206", "v207", "v208", "v209", "v210", "v211", "v212", "v213", "v214", "v215", "v216", "v217", "v218", "v219", "v220", "v221", "v222", "v223", "v224", "v225", "v226", "v227", "v228", "v229", "v230", "v231", "a0", "a1", "a2", "a3", "a4", "a5", "a6", "a7", "a8", "a9", "a10", "a11", "a12", "a13", "a14", "a15", "a16", "a17", "a18", "a19", "a20", "a21", "a22", "a23", "a24", "a25", "a26", "a27", "a28", "a29", "a30", "a31", "a32", "a33", "a34", "a35", "a36", "a37", "a38", "a39", "a40", "a41", "a42", "a43", "a44", "a45", "a46", "a47", "a48", "a49", "a50", "a51", "a52", "a53", "a54", "a55", "a56", "a57", "a58", "a59", "a60", "a61", "a62", "a63", "a64", "a65", "a66", "a67", "a68", "a69", "a70", "a71", "a72", "a73", "a74", "a75", "a76", "a77", "a78", "a79", "a80", "a81", "a82", "a83", "a84", "a85", "a86", "a87", "a88", "a89", "a90", "a91", "a92", "a93", "a94", "a95", "a96", "a97", "a98", "a99", "a100", "a101", "a102", "a103", "a104", "a105", "a106", "a107", "a108", "a109", "a110", "a111", "a112", "a113", "a114", "a115", "a116", "a117", "a118", "a119", "a120", "a121", "a122", "a123", "a124", "a125", "a126", "a127", "a128", "a129", "a130", "a131", "a132", "a133", "a134", "a135", "a136", "a137", "a138", "a139", "a140", "a141", "a142", "a143", "a144", "a145", "a146", "a147", "a148", "a149", "a150", "a151", "a152", "a153", "a154", "a155", "a156", "a157", "a158", "a159", "a160", "a161", "a162", "a163", "a164", "a165", "a166", "a167", "a168", "a169", "a170", "a171", "a172", "a173", "a174", "a175", "a176", "a177", "a178", "a179", "a180", "a181", "a182", "a183", "a184", "a185", "a186", "a187", "a188", "a189", "a190", "a191", "a192", "a193", "a194", "a195", "a196", "a197", "a198", "a199", "a200", "a201", "a202", "a203", "a204", "a205", "a206", "a207", "a208", "a209", "a210", "a211", "a212", "a213", "a214", "a215", "a216", "a217", "a218", "a219", "a220", "a221", "a222", "a223", "a224", "a225", "a226", "a227", "a228", "a229", "a230", "a231", "a232", "a233", "a234", "a235", "a236", "a237", "a238", "a239", "a240", "a241", "a242", "a243", "a244", "a245", "a246", "a247", "a248", "a249", "a250", "a251", "a252", "a253", "a254", "a255", "s36", "s37", "s38", "s39", "s40", "s41", "s42", "s43", "s44", "s45", "s46", "s47", "s48", "s49", "s50", "s51", "s52", "s53", "s54", "s55", "s56", "s57", "s58", "s59", "s60", "s61", "s62", "s63", "s64", "s65", "s66", "s67", "s68", "s69", "s70", "s71", "s72", "s73", "s74", "s75", "s76", "s77", "s78", "s79", "s80", "s81", "s82", "s83", "s84", "s85", "s86", "s87", "s88", "s89", "s90", "s91", "s92", "s93", "s94", "s95", "s96", "s97", "vcc", "scc", "memory");
    LANE_ID(lq_);
    const int g = QT0 + (NCV - 1 - iw) * 164 + min(wave * 64 + lq_, 163);
    if (QTB) touch_lds((const char*)q + (size_t)(g < 32768 ? g : 0) * 128, sScr);
  }
  WAIT_LGKM0();
  __builtin_amdgcn_s_barrier();
  CTS(10);
  }
  const unsigned sDbg0 = 110u * 1048576u + (b * 4 + wave) * 8192;
  constexpr unsigned OOB = 0x80000000u;
  if (kind == 6) {
    LANE_ID(lr);
    const int r8 = lr & 7, seg = lr >> 3, j = lr & 15, g = lr >> 4, rr = 8 * g + (j >> 2);
    const unsigned sWave = wave, sExp = __builtin_amdgcn_readfirstlane(ep), sG0 = slice < 16 ? slice + 1 : 0, sStep = NRW, sDbg = sDbg0;
    const unsigned sWw = ((slice >> 3) * 1792 + 16 * wave) * 2048 + (slice & 7) * 256;
    const unsigned vWd0 = seg * 2048 + (r8 ^ swzB(seg)) * 16, vWd1 = (seg + 8) * 2048 + (r8 ^ swzB(8 + seg)) * 16;
    const unsigned vWr0 = (wave >> 1) * 8192 + rr * 128 + (((wave & 1) * 4 + (lr & 3)) ^ swzB(rr)) * 16, vWr1 = vWr0 + 8;
    const unsigned vRF = (wave * 8 + (seg >> 2)) * 1024 + ((seg & 3) * 16 + r8) * 16;
    const unsigned vRwr = seg == 0 ? r8 * 64 + wave * 4 : 512 + lr * 4, vRrd = r8 * 64;
    const unsigned vRP = g == (j >> 2) ? r8 * 64 + 16 + wave * 8 + (seg & 1) * 4 : 768 + lr * 4;
    const unsigned vRS = (wave == 0 && seg < 2) ? r8 * 16 + seg * 256 : OOB, vFlag = lr < 32 ? lr * 4 : OOB;
    const unsigned vL4 = ((lr & 2) * 4 + wave * 2 + (lr & 1)) * 128, vLY = min((wave < 3 ? wave * 32 : 112) + lr, wave < 2 ? wave * 32 + 31 : wave == 2 ? 111 : NCV - 1) * 128;
    const unsigned vQ = r8 * 2048 + wave * 512 + seg * 16;
    const unsigned vY0 = wave * 4096 + lr * 16;
    const unsigned vTr = 4096 + wave * 4096 + ((2 * g + ((j & 3) >> 1)) * 8 + (j >> 2)) * 16 + (j & 1) * 8;
    const unsigned one = g == (j >> 2) ? 0x3f80u << (16 * (j & 1)) : 0u, vSa = (j & 2) ? 0u : one, vSb = (j & 2) ? one : 0u;
    (void)sDbg;
    asm volatile(
R"(.macro M d,w,x
v_mfma_f32_16x16x32_bf16 v[\d:\d+3], \w, v[\x:\x+3], v[\d:\d+3]
.endm
.macro R d,a,o
ds_read_b128 v[\d:\d+3], v\a offset:\o
.endm
.macro T d,a,o
ds_read_b64_tr_b16 \d, v\a offset:\o
.endm
.macro D a,s,o
buffer_load_dwordx4 v\a, s[36:39], s\s offen offset:\o sc1 lds
.endm
.macro L c
s_waitcnt lgkmcnt(\c)
.endm
Lcode_r_b:
s_mov_b64 s[36:37],%0
s_mov_b32 s38,111243392
s_mov_b32 s39,0x00020000
s_mov_b32 s44,%1
s_mov_b32 s45,%2
s_mov_b32 s46,%3
s_mov_b32 s50,%4
s_mov_b32 s53,%5
v_mov_b32 v0,%6
v_mov_b32 v1,%7
v_mov_b32 v2,%8
v_mov_b32 v3,%9
v_mov_b32 v4,%10
v_mov_b32 v5,%11
v_mov_b32 v6,%12
v_mov_b32 v7,%13
v_mov_b32 v8,%14
v_mov_b32 v9,%15
v_mov_b32 v10,%16
v_mov_b32 v11,%17
v_mov_b32 v12,%18
v_mov_b32 v13,%19
v_mov_b32 v14,%20
v_mov_b32 v15,%21
v_mov_b32 v16,%22
s_mov_b32 s47,256
s_mov_b32 s49,0
s_mov_b32 s93,s45
s_mov_b64 s[84:85],%23
s_mov_b32 s86,4194304
s_mov_b32 s87,0x00020000
s_lshl_b32 s55,s45,14
buffer_load_dwordx4 v[164:167],v8,s[84:87],s55 offen offset:0 nt
buffer_load_dwordx4 v[168:171],v8,s[84:87],s55 offen offset:128 nt
buffer_load_dwordx4 v[172:175],v8,s[84:87],s55 offen offset:256 nt
buffer_load_dwordx4 v[176:179],v8,s[84:87],s55 offen offset:384 nt
v_mov_b32 v17,s53
v_mbcnt_lo_u32_b32 v19,-1,0
v_mbcnt_hi_u32_b32 v19,-1,v19
v_lshlrev_b32 v19,4,v19
s_mov_b32 s82,0
s_mov_b32 s63,0x7fff0000
s_mov_b32 s64,0
s_mov_b32 s67,0
s_lshl_b32 s65,s44,12
s_add_u32 s65,s65,4096
s_lshl_b32 s66,s44,7
s_add_u32 s66,s66,109056000
s_mov_b32 s70,0xaaaaaaaa
s_mov_b32 s71,0xaaaaaaaa
s_mov_b32 s72,0xcccccccc
s_mov_b32 s73,0xcccccccc
s_mov_b32 s74,0xff00ff00
s_mov_b32 s75,0xff00ff00
s_mov_b32 s68,0x3a800000
s_mov_b32 s69,0x3727c5ac
s_mov_b32 s76,0xc0135761
s_mov_b32 s77,0x3d372713
v_mov_b32 v32,0x3f803f80
v_mov_b32 v33,0x3f803f80
v_mov_b32 v34,0x3f803f80
v_mov_b32 v35,0x3f803f80
v_mov_b32 v24,0
v_mov_b32 v25,0
v_mov_b32 v26,v11
v_mov_b32 v27,v12
v_mov_b32 v28,0
v_mov_b32 v29,0
s_waitcnt vmcnt(0)
Lr_a0_%=:
s_cmp_ge_u32 s45,34
s_cbranch_scc1 Lr_fp0_%=
s_waitcnt vmcnt(0)
buffer_store_dword v17,v5,s[36:39],s63 offen offset:0 sc0 sc1
s_mov_b32 s63,0x7fff0000
Lr_fp0_%=:
s_waitcnt vmcnt(10)
v_mfma_f32_16x16x32_bf16 v[36:39],v[32:35],v[164:167],0
s_add_u32 s79,s45,s46
s_cmp_lt_u32 s45,64
s_cselect_b32 s78,s49,0
v_mfma_f32_16x16x32_bf16 v[40:43],v[164:167],v[164:167],0
s_cmp_ge_u32 s79,64
s_cselect_b32 s78,s78,0
s_add_u32 s62,s79,s78
M 36,v[32:35],168
s_lshr_b32 s78,s45,1
s_and_b32 s79,s45,1
s_lshl_b32 s79,s79,7
M 40,v[168:171],168
s_lshl_b32 s57,s78,15
s_lshl_b32 s55,s62,14
s_cmp_lt_u32 s62,s47
M 36,v[32:35],172
s_cselect_b32 s55,s55,0x7fff0000
s_add_u32 s57,s57,s79
s_lshl_b32 s59,s78,9
M 40,v[172:175],172
buffer_load_dwordx4 v[196:199],v8,s[84:87],s55 offen offset:0 nt
M 36,v[32:35],176
buffer_load_dwordx4 v[200:203],v8,s[84:87],s55 offen offset:128 nt
M 40,v[176:179],176
buffer_load_dwordx4 v[204:207],v8,s[84:87],s55 offen offset:256 nt
v_mfma_f32_16x16x32_bf16 v[116:119],v[26:29],v[164:167],0
buffer_load_dwordx4 v[208:211],v8,s[84:87],s55 offen offset:384 nt
v_mfma_f32_16x16x32_bf16 v[120:123],v[24:27],v[164:167],0
s_add_u32 s57,s57,8388608
s_add_u32 s59,s59,s79
s_lshl_b32 s60,s45,9
v_mfma_f32_16x16x32_bf16 v[124:127],v[26:29],v[168:171],0
s_add_u32 s58,s57,4096
s_add_u32 s59,s59,106954752
s_add_u32 s60,s60,s66
v_mfma_f32_16x16x32_bf16 v[128:131],v[24:27],v[168:171],0
v_add_f32_dpp v44,v36,v36 row_ror:8 row_mask:0xf bank_mask:0xf
v_cndmask_b32 v45,v40,v41,s[70:71]
v_mfma_f32_16x16x32_bf16 v[132:135],v[26:29],v[172:175],0
ds_write_b32 v2,v44 offset:0
v_mfma_f32_16x16x32_bf16 v[136:139],v[24:27],v[172:175],0
v_cndmask_b32 v46,v42,v43,s[70:71]
v_cndmask_b32 v45,v45,v46,s[72:73]
v_mfma_f32_16x16x32_bf16 v[140:143],v[26:29],v[176:179],0
ds_write_b32 v0,v45 offset:0
v_mfma_f32_16x16x32_bf16 v[144:147],v[24:27],v[176:179],0
L 0
s_barrier
R 52,3,0
R 56,3,16
R 60,3,32
s_cmp_lg_u32 s67,0
s_cbranch_scc1 Lr_b0_%=
s_waitcnt vmcnt(4)
buffer_store_dword v17,v5,s[36:39],s63 offen offset:0 sc0 sc1
s_mov_b32 s63,0x7fff0000
s_mov_b32 s38,111243392
s_cmp_lt_u32 s45,64
s_cselect_b32 s64,-192,0
s_add_u32 s64,s64,256
s_cmp_lt_u32 s45,64
s_cselect_b32 s61,0,32768
s_add_u32 s61,s61,111177728
s_mul_i32 s56,s45,16384
s_add_u32 s56,s56,98566144
Lr_bpy0_%=:
buffer_load_dword v18,v7,s[36:39],s61 offen offset:0 sc1
s_add_u32 s82,s82,1
s_waitcnt vmcnt(0)
v_cmp_eq_u32 vcc,s53,v18
s_nop 0
s_cmp_eq_u64 vcc,-1
s_cbranch_scc1 Lr_bqy0_%=
s_sleep 2
s_cmp_lt_u32 s82,20000
s_cbranch_scc1 Lr_bpy0_%=
Lr_bqy0_%=:
s_add_u32 m0,s65,0
s_nop 0
D 9,56,0
D 9,56,1024
D 9,56,2048
D 9,56,3072
s_waitcnt vmcnt(0)
Lr_b0_%=:
s_waitcnt vmcnt(10)
T v[68:69],10,0
T v[70:71],10,64
T v[72:73],10,1024
T v[74:75],10,1088
T v[76:77],10,2048
T v[78:79],10,2112
T v[80:81],10,3072
T v[82:83],10,3136
s_mov_b32 s45,s62
s_min_u32 s78,s64,s47
s_mul_i32 s56,s45,16384
s_add_u32 s56,s56,98566144
s_cmp_lt_i32 s45,s78
s_cselect_b32 s67,1,0
s_cselect_b32 s56,s56,0x7fff0000
s_add_u32 m0,s65,16384
s_nop 0
D 9,56,0
D 9,56,1024
D 9,56,2048
D 9,56,3072
L 8
v_pk_add_f32 v[52:53],v[52:53],v[54:55]
v_pk_add_f32 v[56:57],v[56:57],v[58:59]
v_pk_add_f32 v[60:61],v[60:61],v[62:63]
v_add_f32 v52,v52,v53
v_pk_add_f32 v[56:57],v[56:57],v[60:61]
v_mul_f32 v52,s68,v52
v_add_f32 v53,v56,v57
v_mul_f32 v53,s68,v53
v_fma_f32 v53,-v52,v52,v53
v_max_f32 v53,0,v53
L 6
v_lshlrev_b32 v84,16,v68
v_and_b32 v85,0xffff0000,v68
v_lshlrev_b32 v86,16,v69
v_and_b32 v87,0xffff0000,v69
v_lshlrev_b32 v88,16,v70
v_and_b32 v89,0xffff0000,v70
v_lshlrev_b32 v90,16,v71
v_and_b32 v91,0xffff0000,v71
L 4
v_lshlrev_b32 v92,16,v72
v_and_b32 v93,0xffff0000,v72
v_lshlrev_b32 v94,16,v73
v_and_b32 v95,0xffff0000,v73
v_lshlrev_b32 v96,16,v74
v_and_b32 v97,0xffff0000,v74
v_lshlrev_b32 v98,16,v75
v_and_b32 v99,0xffff0000,v75
L 2
v_lshlrev_b32 v100,16,v76
v_and_b32 v101,0xffff0000,v76
v_lshlrev_b32 v102,16,v77
v_and_b32 v103,0xffff0000,v77
v_lshlrev_b32 v104,16,v78
v_and_b32 v105,0xffff0000,v78
v_lshlrev_b32 v106,16,v79
v_and_b32 v107,0xffff0000,v79
L 0
v_lshlrev_b32 v108,16,v80
v_and_b32 v109,0xffff0000,v80
v_lshlrev_b32 v110,16,v81
v_and_b32 v111,0xffff0000,v81
v_lshlrev_b32 v112,16,v82
v_and_b32 v113,0xffff0000,v82
v_lshlrev_b32 v114,16,v83
v_and_b32 v115,0xffff0000,v83
v_add_f32 v53,s69,v53
v_rsq_f32 v48,v53
s_nop 0
v_mul_f32_e64 v50,-v52,v48
v_mov_b32 v49,v48
v_mov_b32 v51,v50
v_pk_fma_f32 v[52:53],v[116:117],v[48:49],v[50:51]
v_pk_fma_f32 v[54:55],v[118:119],v[48:49],v[50:51]
v_pk_fma_f32 v[56:57],v[120:121],v[48:49],v[50:51]
v_pk_fma_f32 v[58:59],v[122:123],v[48:49],v[50:51]
v_pk_fma_f32 v[116:117],v[52:53],v[84:85],v[116:117]
v_pk_fma_f32 v[118:119],v[54:55],v[86:87],v[118:119]
v_pk_fma_f32 v[120:121],v[56:57],v[88:89],v[120:121]
v_pk_fma_f32 v[122:123],v[58:59],v[90:91],v[122:123]
v_cvt_pk_bf16_f32 v148,v116,v117
v_cvt_pk_bf16_f32 v149,v118,v119
v_cvt_pk_bf16_f32 v150,v120,v121
v_cvt_pk_bf16_f32 v151,v122,v123
v_pk_fma_f32 v[52:53],v[124:125],v[48:49],v[50:51]
v_pk_fma_f32 v[54:55],v[126:127],v[48:49],v[50:51]
v_pk_fma_f32 v[56:57],v[128:129],v[48:49],v[50:51]
v_pk_fma_f32 v[58:59],v[130:131],v[48:49],v[50:51]
v_pk_fma_f32 v[124:125],v[52:53],v[92:93],v[124:125]
v_pk_fma_f32 v[126:127],v[54:55],v[94:95],v[126:127]
v_pk_fma_f32 v[128:129],v[56:57],v[96:97],v[128:129]
v_pk_fma_f32 v[130:131],v[58:59],v[98:99],v[130:131]
v_cvt_pk_bf16_f32 v152,v124,v125
v_cvt_pk_bf16_f32 v153,v126,v127
v_cvt_pk_bf16_f32 v154,v128,v129
v_cvt_pk_bf16_f32 v155,v130,v131
v_pk_fma_f32 v[52:53],v[132:133],v[48:49],v[50:51]
v_pk_fma_f32 v[54:55],v[134:135],v[48:49],v[50:51]
v_pk_fma_f32 v[56:57],v[136:137],v[48:49],v[50:51]
v_pk_fma_f32 v[58:59],v[138:139],v[48:49],v[50:51]
v_pk_fma_f32 v[132:133],v[52:53],v[100:101],v[132:133]
v_pk_fma_f32 v[134:135],v[54:55],v[102:103],v[134:135]
v_pk_fma_f32 v[136:137],v[56:57],v[104:105],v[136:137]
v_pk_fma_f32 v[138:139],v[58:59],v[106:107],v[138:139]
v_cvt_pk_bf16_f32 v156,v132,v133
v_cvt_pk_bf16_f32 v157,v134,v135
v_cvt_pk_bf16_f32 v158,v136,v137
v_cvt_pk_bf16_f32 v159,v138,v139
v_pk_fma_f32 v[52:53],v[140:141],v[48:49],v[50:51]
v_pk_fma_f32 v[54:55],v[142:143],v[48:49],v[50:51]
v_pk_fma_f32 v[56:57],v[144:145],v[48:49],v[50:51]
v_pk_fma_f32 v[58:59],v[146:147],v[48:49],v[50:51]
v_pk_fma_f32 v[140:141],v[52:53],v[108:109],v[140:141]
v_pk_fma_f32 v[142:143],v[54:55],v[110:111],v[142:143]
v_pk_fma_f32 v[144:145],v[56:57],v[112:113],v[144:145]
v_pk_fma_f32 v[146:147],v[58:59],v[114:115],v[146:147]
v_cvt_pk_bf16_f32 v160,v140,v141
v_cvt_pk_bf16_f32 v161,v142,v143
v_cvt_pk_bf16_f32 v162,v144,v145
v_cvt_pk_bf16_f32 v163,v146,v147
s_waitcnt vmcnt(8)
buffer_store_dword v17,v5,s[36:39],s63 offen offset:0 sc0 sc1
s_mov_b32 s63,0x7fff0000
v_mfma_f32_16x16x32_bf16 v[36:39],v[32:35],v[148:151],0
buffer_store_dwordx4 v[148:151],v1,s[36:39],s57 offen offset:0 sc0 sc1
v_mfma_f32_16x16x32_bf16 v[40:43],v[148:151],v[148:151],0
M 36,v[32:35],152
buffer_store_dwordx4 v[152:155],v1,s[36:39],s57 offen offset:2048 sc0 sc1
M 40,v[152:155],152
M 36,v[32:35],156
buffer_store_dwordx4 v[156:159],v1,s[36:39],s58 offen offset:0 sc0 sc1
M 40,v[156:159],156
M 36,v[32:35],160
buffer_store_dwordx4 v[160:163],v1,s[36:39],s58 offen offset:2048 sc0 sc1
M 40,v[160:163],160
s_nop 5
v_add_f32_dpp v44,v36,v36 row_ror:8 row_mask:0xf bank_mask:0xf
s_nop 0
v_cndmask_b32 v45,v40,v41,s[70:71]
v_cndmask_b32 v46,v42,v43,s[70:71]
ds_write_b32 v2,v44 offset:1024
v_cndmask_b32 v45,v45,v46,s[72:73]
ds_write_b32 v0,v45 offset:1024
L 0
s_barrier
R 52,3,1024
R 56,3,1040
R 60,3,1056
L 0
v_pk_add_f32 v[52:53],v[52:53],v[54:55]
v_pk_add_f32 v[56:57],v[56:57],v[58:59]
v_pk_add_f32 v[60:61],v[60:61],v[62:63]
v_add_f32 v52,v52,v53
v_pk_add_f32 v[56:57],v[56:57],v[60:61]
v_mul_f32 v52,s68,v52
v_add_f32 v53,v56,v57
v_mul_f32 v53,s68,v53
v_fma_f32 v53,-v52,v52,v53
v_max_f32 v53,0,v53
v_add_f32 v54,s69,v53
v_fma_f32 v55,s69,v54,v53
v_rsq_f32 v65,v54
v_rsq_f32 v54,v55
v_mul_f32_e64 v64,-v52,v65
v_mul_f32 v63,v55,v54
v_mul_f32 v61,s76,v54
v_mul_f32 v55,v54,v54
v_mul_f32 v62,s77,v55
v_mul_f32 v62,v62,v61
v_cndmask_b32 v60,v52,v64,s[74:75]
v_cndmask_b32 v61,v61,v65,s[74:75]
buffer_store_dwordx4 v[60:63],v4,s[36:39],s59 offen offset:0 sc0 sc1
s_mov_b32 s63,s60
s_cmp_lt_i32 s45,s47
s_cbranch_scc0 Lr_mid_%=
Lr_a1_%=:
s_cmp_ge_u32 s45,34
s_cbranch_scc1 Lr_fp1_%=
s_waitcnt vmcnt(0)
buffer_store_dword v17,v5,s[36:39],s63 offen offset:0 sc0 sc1
s_mov_b32 s63,0x7fff0000
Lr_fp1_%=:
s_waitcnt vmcnt(10)
v_mfma_f32_16x16x32_bf16 v[36:39],v[32:35],v[196:199],0
s_add_u32 s79,s45,s46
s_cmp_lt_u32 s45,64
s_cselect_b32 s78,s49,0
v_mfma_f32_16x16x32_bf16 v[40:43],v[196:199],v[196:199],0
s_cmp_ge_u32 s79,64
s_cselect_b32 s78,s78,0
s_add_u32 s62,s79,s78
M 36,v[32:35],200
s_lshr_b32 s78,s45,1
s_and_b32 s79,s45,1
s_lshl_b32 s79,s79,7
M 40,v[200:203],200
s_lshl_b32 s57,s78,15
s_lshl_b32 s55,s62,14
s_cmp_lt_u32 s62,s47
M 36,v[32:35],204
s_cselect_b32 s55,s55,0x7fff0000
s_add_u32 s57,s57,s79
s_lshl_b32 s59,s78,9
M 40,v[204:207],204
buffer_load_dwordx4 v[164:167],v8,s[84:87],s55 offen offset:0 nt
M 36,v[32:35],208
buffer_load_dwordx4 v[168:171],v8,s[84:87],s55 offen offset:128 nt
M 40,v[208:211],208
buffer_load_dwordx4 v[172:175],v8,s[84:87],s55 offen offset:256 nt
v_mfma_f32_16x16x32_bf16 v[116:119],v[26:29],v[196:199],0
buffer_load_dwordx4 v[176:179],v8,s[84:87],s55 offen offset:384 nt
v_mfma_f32_16x16x32_bf16 v[120:123],v[24:27],v[196:199],0
s_add_u32 s57,s57,8388608
s_add_u32 s59,s59,s79
s_lshl_b32 s60,s45,9
v_mfma_f32_16x16x32_bf16 v[124:127],v[26:29],v[200:203],0
s_add_u32 s58,s57,4096
s_add_u32 s59,s59,106954752
s_add_u32 s60,s60,s66
v_mfma_f32_16x16x32_bf16 v[128:131],v[24:27],v[200:203],0
v_add_f32_dpp v44,v36,v36 row_ror:8 row_mask:0xf bank_mask:0xf
v_cndmask_b32 v45,v40,v41,s[70:71]
v_mfma_f32_16x16x32_bf16 v[132:135],v[26:29],v[204:207],0
ds_write_b32 v2,v44 offset:0
v_mfma_f32_16x16x32_bf16 v[136:139],v[24:27],v[204:207],0
v_cndmask_b32 v46,v42,v43,s[70:71]
v_cndmask_b32 v45,v45,v46,s[72:73]
v_mfma_f32_16x16x32_bf16 v[140:143],v[26:29],v[208:211],0
ds_write_b32 v0,v45 offset:0
v_mfma_f32_16x16x32_bf16 v[144:147],v[24:27],v[208:211],0
L 0
s_barrier
R 52,3,0
R 56,3,16
R 60,3,32
s_cmp_lg_u32 s67,0
s_cbranch_scc1 Lr_b1_%=
s_waitcnt vmcnt(4)
buffer_store_dword v17,v5,s[36:39],s63 offen offset:0 sc0 sc1
s_mov_b32 s63,0x7fff0000
s_mov_b32 s38,111243392
s_cmp_lt_u32 s45,64
s_cselect_b32 s64,-192,0
s_add_u32 s64,s64,256
s_cmp_lt_u32 s45,64
s_cselect_b32 s61,0,32768
s_add_u32 s61,s61,111177728
s_mul_i32 s56,s45,16384
s_add_u32 s56,s56,98566144
Lr_bpy1_%=:
buffer_load_dword v18,v7,s[36:39],s61 offen offset:0 sc1
s_add_u32 s82,s82,1
s_waitcnt vmcnt(0)
v_cmp_eq_u32 vcc,s53,v18
s_nop 0
s_cmp_eq_u64 vcc,-1
s_cbranch_scc1 Lr_bqy1_%=
s_sleep 2
s_cmp_lt_u32 s82,20000
s_cbranch_scc1 Lr_bpy1_%=
Lr_bqy1_%=:
s_add_u32 m0,s65,16384
s_nop 0
D 9,56,0
D 9,56,1024
D 9,56,2048
D 9,56,3072
s_waitcnt vmcnt(0)
Lr_b1_%=:
s_waitcnt vmcnt(10)
T v[68:69],10,16384
T v[70:71],10,16448
T v[72:73],10,17408
T v[74:75],10,17472
T v[76:77],10,18432
T v[78:79],10,18496
T v[80:81],10,19456
T v[82:83],10,19520
s_mov_b32 s45,s62
s_min_u32 s78,s64,s47
s_mul_i32 s56,s45,16384
s_add_u32 s56,s56,98566144
s_cmp_lt_i32 s45,s78
s_cselect_b32 s67,1,0
s_cselect_b32 s56,s56,0x7fff0000
s_add_u32 m0,s65,0
s_nop 0
D 9,56,0
D 9,56,1024
D 9,56,2048
D 9,56,3072
L 8
v_pk_add_f32 v[52:53],v[52:53],v[54:55]
v_pk_add_f32 v[56:57],v[56:57],v[58:59]
v_pk_add_f32 v[60:61],v[60:61],v[62:63]
v_add_f32 v52,v52,v53
v_pk_add_f32 v[56:57],v[56:57],v[60:61]
v_mul_f32 v52,s68,v52
v_add_f32 v53,v56,v57
v_mul_f32 v53,s68,v53
v_fma_f32 v53,-v52,v52,v53
v_max_f32 v53,0,v53
L 6
v_lshlrev_b32 v84,16,v68
v_and_b32 v85,0xffff0000,v68
v_lshlrev_b32 v86,16,v69
v_and_b32 v87,0xffff0000,v69
v_lshlrev_b32 v88,16,v70
v_and_b32 v89,0xffff0000,v70
v_lshlrev_b32 v90,16,v71
v_and_b32 v91,0xffff0000,v71
L 4
v_lshlrev_b32 v92,16,v72
v_and_b32 v93,0xffff0000,v72
v_lshlrev_b32 v94,16,v73
v_and_b32 v95,0xffff0000,v73
v_lshlrev_b32 v96,16,v74
v_and_b32 v97,0xffff0000,v74
)"
R"(v_lshlrev_b32 v98,16,v75
v_and_b32 v99,0xffff0000,v75
L 2
v_lshlrev_b32 v100,16,v76
v_and_b32 v101,0xffff0000,v76
v_lshlrev_b32 v102,16,v77
v_and_b32 v103,0xffff0000,v77
v_lshlrev_b32 v104,16,v78
v_and_b32 v105,0xffff0000,v78
v_lshlrev_b32 v106,16,v79
v_and_b32 v107,0xffff0000,v79
L 0
v_lshlrev_b32 v108,16,v80
v_and_b32 v109,0xffff0000,v80
v_lshlrev_b32 v110,16,v81
v_and_b32 v111,0xffff0000,v81
v_lshlrev_b32 v112,16,v82
v_and_b32 v113,0xffff0000,v82
v_lshlrev_b32 v114,16,v83
v_and_b32 v115,0xffff0000,v83
v_add_f32 v53,s69,v53
v_rsq_f32 v48,v53
s_nop 0
v_mul_f32_e64 v50,-v52,v48
v_mov_b32 v49,v48
v_mov_b32 v51,v50
v_pk_fma_f32 v[52:53],v[116:117],v[48:49],v[50:51]
v_pk_fma_f32 v[54:55],v[118:119],v[48:49],v[50:51]
v_pk_fma_f32 v[56:57],v[120:121],v[48:49],v[50:51]
v_pk_fma_f32 v[58:59],v[122:123],v[48:49],v[50:51]
v_pk_fma_f32 v[116:117],v[52:53],v[84:85],v[116:117]
v_pk_fma_f32 v[118:119],v[54:55],v[86:87],v[118:119]
v_pk_fma_f32 v[120:121],v[56:57],v[88:89],v[120:121]
v_pk_fma_f32 v[122:123],v[58:59],v[90:91],v[122:123]
v_cvt_pk_bf16_f32 v148,v116,v117
v_cvt_pk_bf16_f32 v149,v118,v119
v_cvt_pk_bf16_f32 v150,v120,v121
v_cvt_pk_bf16_f32 v151,v122,v123
v_pk_fma_f32 v[52:53],v[124:125],v[48:49],v[50:51]
v_pk_fma_f32 v[54:55],v[126:127],v[48:49],v[50:51]
v_pk_fma_f32 v[56:57],v[128:129],v[48:49],v[50:51]
v_pk_fma_f32 v[58:59],v[130:131],v[48:49],v[50:51]
v_pk_fma_f32 v[124:125],v[52:53],v[92:93],v[124:125]
v_pk_fma_f32 v[126:127],v[54:55],v[94:95],v[126:127]
v_pk_fma_f32 v[128:129],v[56:57],v[96:97],v[128:129]
v_pk_fma_f32 v[130:131],v[58:59],v[98:99],v[130:131]
v_cvt_pk_bf16_f32 v152,v124,v125
v_cvt_pk_bf16_f32 v153,v126,v127
v_cvt_pk_bf16_f32 v154,v128,v129
v_cvt_pk_bf16_f32 v155,v130,v131
v_pk_fma_f32 v[52:53],v[132:133],v[48:49],v[50:51]
v_pk_fma_f32 v[54:55],v[134:135],v[48:49],v[50:51]
v_pk_fma_f32 v[56:57],v[136:137],v[48:49],v[50:51]
v_pk_fma_f32 v[58:59],v[138:139],v[48:49],v[50:51]
v_pk_fma_f32 v[132:133],v[52:53],v[100:101],v[132:133]
v_pk_fma_f32 v[134:135],v[54:55],v[102:103],v[134:135]
v_pk_fma_f32 v[136:137],v[56:57],v[104:105],v[136:137]
v_pk_fma_f32 v[138:139],v[58:59],v[106:107],v[138:139]
v_cvt_pk_bf16_f32 v156,v132,v133
v_cvt_pk_bf16_f32 v157,v134,v135
v_cvt_pk_bf16_f32 v158,v136,v137
v_cvt_pk_bf16_f32 v159,v138,v139
v_pk_fma_f32 v[52:53],v[140:141],v[48:49],v[50:51]
v_pk_fma_f32 v[54:55],v[142:143],v[48:49],v[50:51]
v_pk_fma_f32 v[56:57],v[144:145],v[48:49],v[50:51]
v_pk_fma_f32 v[58:59],v[146:147],v[48:49],v[50:51]
v_pk_fma_f32 v[140:141],v[52:53],v[108:109],v[140:141]
v_pk_fma_f32 v[142:143],v[54:55],v[110:111],v[142:143]
v_pk_fma_f32 v[144:145],v[56:57],v[112:113],v[144:145]
v_pk_fma_f32 v[146:147],v[58:59],v[114:115],v[146:147]
v_cvt_pk_bf16_f32 v160,v140,v141
v_cvt_pk_bf16_f32 v161,v142,v143
v_cvt_pk_bf16_f32 v162,v144,v145
v_cvt_pk_bf16_f32 v163,v146,v147
s_waitcnt vmcnt(8)
buffer_store_dword v17,v5,s[36:39],s63 offen offset:0 sc0 sc1
s_mov_b32 s63,0x7fff0000
v_mfma_f32_16x16x32_bf16 v[36:39],v[32:35],v[148:151],0
buffer_store_dwordx4 v[148:151],v1,s[36:39],s57 offen offset:0 sc0 sc1
v_mfma_f32_16x16x32_bf16 v[40:43],v[148:151],v[148:151],0
M 36,v[32:35],152
buffer_store_dwordx4 v[152:155],v1,s[36:39],s57 offen offset:2048 sc0 sc1
M 40,v[152:155],152
M 36,v[32:35],156
buffer_store_dwordx4 v[156:159],v1,s[36:39],s58 offen offset:0 sc0 sc1
M 40,v[156:159],156
M 36,v[32:35],160
buffer_store_dwordx4 v[160:163],v1,s[36:39],s58 offen offset:2048 sc0 sc1
M 40,v[160:163],160
s_nop 5
v_add_f32_dpp v44,v36,v36 row_ror:8 row_mask:0xf bank_mask:0xf
s_nop 0
v_cndmask_b32 v45,v40,v41,s[70:71]
v_cndmask_b32 v46,v42,v43,s[70:71]
ds_write_b32 v2,v44 offset:1024
v_cndmask_b32 v45,v45,v46,s[72:73]
ds_write_b32 v0,v45 offset:1024
L 0
s_barrier
R 52,3,1024
R 56,3,1040
R 60,3,1056
L 0
v_pk_add_f32 v[52:53],v[52:53],v[54:55]
v_pk_add_f32 v[56:57],v[56:57],v[58:59]
v_pk_add_f32 v[60:61],v[60:61],v[62:63]
v_add_f32 v52,v52,v53
v_pk_add_f32 v[56:57],v[56:57],v[60:61]
v_mul_f32 v52,s68,v52
v_add_f32 v53,v56,v57
v_mul_f32 v53,s68,v53
v_fma_f32 v53,-v52,v52,v53
v_max_f32 v53,0,v53
v_add_f32 v54,s69,v53
v_fma_f32 v55,s69,v54,v53
v_rsq_f32 v65,v54
v_rsq_f32 v54,v55
v_mul_f32_e64 v64,-v52,v65
v_mul_f32 v63,v55,v54
v_mul_f32 v61,s76,v54
v_mul_f32 v55,v54,v54
v_mul_f32 v62,s77,v55
v_mul_f32 v62,v62,v61
v_cndmask_b32 v60,v52,v64,s[74:75]
v_cndmask_b32 v61,v61,v65,s[74:75]
buffer_store_dwordx4 v[60:63],v4,s[36:39],s59 offen offset:0 sc0 sc1
s_mov_b32 s63,s60
s_cmp_lt_i32 s45,s47
s_cbranch_scc1 Lr_a0_%=
Lr_mid_%=:
s_cmp_eq_u32 s93,0
s_cbranch_scc1 Lr_h0_%=
s_mov_b64 s[40:41],%24
s_mov_b32 s42,7340032
s_mov_b32 s43,0x00020000
s_lshl_b32 s57,s44,11
s_add_u32 s55,s50,0
s_add_u32 m0,s57,40960
s_nop 0
buffer_load_dwordx4 v13,s[40:43],s55 offen offset:0  lds
s_add_u32 m0,s57,41984
s_nop 0
buffer_load_dwordx4 v14,s[40:43],s55 offen offset:0  lds
s_add_u32 m0,s57,49024
s_nop 0
buffer_load_dwordx4 v13,s[40:43],s55 offen offset:128  lds
s_add_u32 m0,s57,50048
s_nop 0
buffer_load_dwordx4 v14,s[40:43],s55 offen offset:128  lds
s_add_u32 s55,s50,131072
s_add_u32 m0,s57,57344
s_nop 0
buffer_load_dwordx4 v13,s[40:43],s55 offen offset:0  lds
s_add_u32 m0,s57,58368
s_nop 0
buffer_load_dwordx4 v14,s[40:43],s55 offen offset:0  lds
s_add_u32 m0,s57,65408
s_nop 0
buffer_load_dwordx4 v13,s[40:43],s55 offen offset:128  lds
s_add_u32 m0,s57,66432
s_nop 0
buffer_load_dwordx4 v14,s[40:43],s55 offen offset:128  lds
s_add_u32 s55,s50,262144
s_add_u32 m0,s57,73728
s_nop 0
buffer_load_dwordx4 v13,s[40:43],s55 offen offset:0  lds
s_add_u32 m0,s57,74752
s_nop 0
buffer_load_dwordx4 v14,s[40:43],s55 offen offset:0  lds
s_add_u32 m0,s57,81792
s_nop 0
buffer_load_dwordx4 v13,s[40:43],s55 offen offset:128  lds
s_add_u32 m0,s57,82816
s_nop 0
buffer_load_dwordx4 v14,s[40:43],s55 offen offset:128  lds
s_add_u32 s55,s50,393216
s_add_u32 m0,s57,90112
s_nop 0
buffer_load_dwordx4 v13,s[40:43],s55 offen offset:0  lds
s_add_u32 m0,s57,91136
s_nop 0
buffer_load_dwordx4 v14,s[40:43],s55 offen offset:0  lds
s_add_u32 m0,s57,98176
s_nop 0
buffer_load_dwordx4 v13,s[40:43],s55 offen offset:128  lds
s_add_u32 m0,s57,99200
s_nop 0
buffer_load_dwordx4 v14,s[40:43],s55 offen offset:128  lds
s_add_u32 s55,s50,524288
s_add_u32 m0,s57,106496
s_nop 0
buffer_load_dwordx4 v13,s[40:43],s55 offen offset:0  lds
s_add_u32 m0,s57,107520
s_nop 0
buffer_load_dwordx4 v14,s[40:43],s55 offen offset:0  lds
s_add_u32 m0,s57,114560
s_nop 0
buffer_load_dwordx4 v13,s[40:43],s55 offen offset:128  lds
s_add_u32 m0,s57,115584
s_nop 0
buffer_load_dwordx4 v14,s[40:43],s55 offen offset:128  lds
s_add_u32 s55,s50,655360
s_add_u32 m0,s57,122880
s_nop 0
buffer_load_dwordx4 v13,s[40:43],s55 offen offset:0  lds
s_add_u32 m0,s57,123904
s_nop 0
buffer_load_dwordx4 v14,s[40:43],s55 offen offset:0  lds
s_add_u32 m0,s57,130944
s_nop 0
buffer_load_dwordx4 v13,s[40:43],s55 offen offset:128  lds
s_add_u32 m0,s57,131968
s_nop 0
buffer_load_dwordx4 v14,s[40:43],s55 offen offset:128  lds
s_waitcnt vmcnt(24)
buffer_store_dword v17,v5,s[36:39],s63 offen offset:0 sc0 sc1
s_mov_b32 s63,0x7fff0000
Lr_h0_%=:
s_waitcnt vmcnt(0)
buffer_store_dword v17,v5,s[36:39],s63 offen offset:0 sc0 sc1
s_mov_b32 s63,0x7fff0000
s_mov_b32 s45,s93
s_cmp_eq_u32 s93,0
s_cbranch_scc1 Lr_h1_%=
s_mov_b64 s[40:41],%24
s_mov_b32 s42,7340032
s_mov_b32 s43,0x00020000
s_lshl_b32 s57,s44,11
s_waitcnt vmcnt(22) lgkmcnt(0)
s_barrier
s_add_u32 s55,s50,786432
s_add_u32 m0,s57,139264
s_nop 0
buffer_load_dwordx4 v13,s[40:43],s55 offen offset:0  lds
s_add_u32 m0,s57,140288
s_nop 0
buffer_load_dwordx4 v14,s[40:43],s55 offen offset:0  lds
s_add_u32 m0,s57,147328
s_nop 0
buffer_load_dwordx4 v13,s[40:43],s55 offen offset:128  lds
s_add_u32 m0,s57,148352
s_nop 0
buffer_load_dwordx4 v14,s[40:43],s55 offen offset:128  lds
v_add_u32 v44,40960,v15
v_add_u32 v45,40960,v16
T a[0:1],44,0
T a[2:3],44,512
T a[4:5],45,0
T a[6:7],45,512
T a[8:9],44,4096
T a[10:11],44,4608
T a[12:13],45,4096
T a[14:15],45,4608
s_waitcnt vmcnt(22) lgkmcnt(0)
s_barrier
s_add_u32 s55,s50,917504
s_add_u32 m0,s57,40960
s_nop 0
buffer_load_dwordx4 v13,s[40:43],s55 offen offset:0  lds
s_add_u32 m0,s57,41984
s_nop 0
buffer_load_dwordx4 v14,s[40:43],s55 offen offset:0  lds
s_add_u32 m0,s57,49024
s_nop 0
buffer_load_dwordx4 v13,s[40:43],s55 offen offset:128  lds
s_add_u32 m0,s57,50048
s_nop 0
buffer_load_dwordx4 v14,s[40:43],s55 offen offset:128  lds
v_add_u32 v44,57344,v15
v_add_u32 v45,57344,v16
T a[16:17],44,0
T a[18:19],44,512
T a[20:21],45,0
T a[22:23],45,512
T a[24:25],44,4096
T a[26:27],44,4608
T a[28:29],45,4096
T a[30:31],45,4608
s_waitcnt vmcnt(22) lgkmcnt(0)
s_barrier
s_add_u32 s55,s50,1048576
s_add_u32 m0,s57,57344
s_nop 0
buffer_load_dwordx4 v13,s[40:43],s55 offen offset:0  lds
s_add_u32 m0,s57,58368
s_nop 0
buffer_load_dwordx4 v14,s[40:43],s55 offen offset:0  lds
s_add_u32 m0,s57,65408
s_nop 0
buffer_load_dwordx4 v13,s[40:43],s55 offen offset:128  lds
s_add_u32 m0,s57,66432
s_nop 0
buffer_load_dwordx4 v14,s[40:43],s55 offen offset:128  lds
v_add_u32 v44,73728,v15
v_add_u32 v45,73728,v16
T a[32:33],44,0
T a[34:35],44,512
T a[36:37],45,0
T a[38:39],45,512
T a[40:41],44,4096
T a[42:43],44,4608
T a[44:45],45,4096
T a[46:47],45,4608
s_waitcnt vmcnt(22) lgkmcnt(0)
s_barrier
s_add_u32 s55,s50,1179648
s_add_u32 m0,s57,73728
s_nop 0
buffer_load_dwordx4 v13,s[40:43],s55 offen offset:0  lds
s_add_u32 m0,s57,74752
s_nop 0
buffer_load_dwordx4 v14,s[40:43],s55 offen offset:0  lds
s_add_u32 m0,s57,81792
s_nop 0
buffer_load_dwordx4 v13,s[40:43],s55 offen offset:128  lds
s_add_u32 m0,s57,82816
s_nop 0
buffer_load_dwordx4 v14,s[40:43],s55 offen offset:128  lds
v_add_u32 v44,90112,v15
v_add_u32 v45,90112,v16
T a[48:49],44,0
T a[50:51],44,512
T a[52:53],45,0
T a[54:55],45,512
T a[56:57],44,4096
T a[58:59],44,4608
T a[60:61],45,4096
T a[62:63],45,4608
s_waitcnt vmcnt(22) lgkmcnt(0)
s_barrier
s_add_u32 s55,s50,1310720
s_add_u32 m0,s57,90112
s_nop 0
buffer_load_dwordx4 v13,s[40:43],s55 offen offset:0  lds
s_add_u32 m0,s57,91136
s_nop 0
buffer_load_dwordx4 v14,s[40:43],s55 offen offset:0  lds
s_add_u32 m0,s57,98176
s_nop 0
buffer_load_dwordx4 v13,s[40:43],s55 offen offset:128  lds
s_add_u32 m0,s57,99200
s_nop 0
buffer_load_dwordx4 v14,s[40:43],s55 offen offset:128  lds
v_add_u32 v44,106496,v15
v_add_u32 v45,106496,v16
T a[64:65],44,0
T a[66:67],44,512
T a[68:69],45,0
T a[70:71],45,512
T a[72:73],44,4096
T a[74:75],44,4608
T a[76:77],45,4096
T a[78:79],45,4608
L 0
s_barrier
Lr_h1_%=:
s_lshr_b32 s78,s45,1
s_and_b32 s79,s45,1
s_lshl_b32 s79,s79,7
s_lshl_b32 s55,s78,15
s_add_u32 s55,s55,s79
s_mul_i32 s56,s45,16384
s_add_u32 s56,s56,98566144
s_lshl_b32 s61,s78,11
s_add_u32 s61,s61,110587904
s_add_u32 s78,s45,s46
s_lshr_b32 s78,s78,1
s_lshl_b32 s62,s78,11
s_add_u32 s62,s62,110587904
s_add_u32 m0,s65,0
s_nop 0
D 9,56,0
D 9,56,1024
D 9,56,2048
D 9,56,3072
Lr_bps_%=:
buffer_load_dword v18,v6,s[36:39],s61 offen offset:0 sc1
s_add_u32 s82,s82,1
s_waitcnt vmcnt(0)
v_cmp_eq_u32 vcc,s53,v18
s_nop 0
s_cmp_eq_u64 vcc,-1
s_cbranch_scc1 Lr_bqs_%=
s_sleep 2
s_cmp_lt_u32 s82,20000
s_cbranch_scc1 Lr_bps_%=
Lr_bqs_%=:
s_add_u32 s78,s55,71303168
buffer_load_dwordx4 v[164:167],v1,s[36:39],s78 offen offset:0 sc1
buffer_load_dwordx4 v[168:171],v1,s[36:39],s78 offen offset:2048 sc1
s_add_u32 s78,s55,71307264
buffer_load_dwordx4 v[172:175],v1,s[36:39],s78 offen offset:0 sc1
buffer_load_dwordx4 v[176:179],v1,s[36:39],s78 offen offset:2048 sc1
s_add_u32 s78,s55,89128960
buffer_load_dwordx4 v[180:183],v1,s[36:39],s78 offen offset:0 sc1
buffer_load_dwordx4 v[184:187],v1,s[36:39],s78 offen offset:2048 sc1
s_add_u32 s78,s55,89133056
buffer_load_dwordx4 v[188:191],v1,s[36:39],s78 offen offset:0 sc1
buffer_load_dwordx4 v[192:195],v1,s[36:39],s78 offen offset:2048 sc1
buffer_load_dword v18,v6,s[36:39],s62 offen offset:0 sc1
s_cmp_eq_u32 s93,0
s_cbranch_scc1 Lr_h2_%=
s_mov_b64 s[40:41],%24
s_mov_b32 s42,7340032
s_mov_b32 s43,0x00020000
s_lshl_b32 s57,s44,11
L 0
s_barrier
s_add_u32 s55,s50,1441792
s_add_u32 m0,s57,106496
s_nop 0
buffer_load_dwordx4 v13,s[40:43],s55 offen offset:0  lds
s_add_u32 m0,s57,107520
s_nop 0
buffer_load_dwordx4 v14,s[40:43],s55 offen offset:0  lds
s_add_u32 m0,s57,114560
s_nop 0
buffer_load_dwordx4 v13,s[40:43],s55 offen offset:128  lds
s_add_u32 m0,s57,115584
s_nop 0
buffer_load_dwordx4 v14,s[40:43],s55 offen offset:128  lds
v_add_u32 v44,122880,v15
v_add_u32 v45,122880,v16
T a[80:81],44,0
T a[82:83],44,512
T a[84:85],45,0
T a[86:87],45,512
T a[88:89],44,4096
T a[90:91],44,4608
T a[92:93],45,4096
T a[94:95],45,4608
L 0
s_barrier
s_add_u32 s55,s50,1572864
s_add_u32 m0,s57,122880
s_nop 0
buffer_load_dwordx4 v13,s[40:43],s55 offen offset:0  lds
s_add_u32 m0,s57,123904
s_nop 0
buffer_load_dwordx4 v14,s[40:43],s55 offen offset:0  lds
s_add_u32 m0,s57,130944
s_nop 0
buffer_load_dwordx4 v13,s[40:43],s55 offen offset:128  lds
s_add_u32 m0,s57,131968
s_nop 0
buffer_load_dwordx4 v14,s[40:43],s55 offen offset:128  lds
v_add_u32 v44,139264,v15
v_add_u32 v45,139264,v16
T a[96:97],44,0
T a[98:99],44,512
T a[100:101],45,0
T a[102:103],45,512
T a[104:105],44,4096
T a[106:107],44,4608
T a[108:109],45,4096
T a[110:111],45,4608
L 0
s_barrier
s_add_u32 s55,s50,1703936
s_add_u32 m0,s57,139264
s_nop 0
buffer_load_dwordx4 v13,s[40:43],s55 offen offset:0  lds
s_add_u32 m0,s57,140288
s_nop 0
buffer_load_dwordx4 v14,s[40:43],s55 offen offset:0  lds
s_add_u32 m0,s57,147328
s_nop 0
buffer_load_dwordx4 v13,s[40:43],s55 offen offset:128  lds
s_add_u32 m0,s57,148352
s_nop 0
buffer_load_dwordx4 v14,s[40:43],s55 offen offset:128  lds
v_add_u32 v44,40960,v15
v_add_u32 v45,40960,v16
T a[112:113],44,0
T a[114:115],44,512
T a[116:117],45,0
T a[118:119],45,512
T a[120:121],44,4096
T a[122:123],44,4608
T a[124:125],45,4096
T a[126:127],45,4608
L 0
s_barrier
s_add_u32 s55,s50,1835008
s_add_u32 m0,s57,40960
s_nop 0
buffer_load_dwordx4 v13,s[40:43],s55 offen offset:0  lds
s_add_u32 m0,s57,41984
s_nop 0
buffer_load_dwordx4 v14,s[40:43],s55 offen offset:0  lds
s_add_u32 m0,s57,49024
s_nop 0
buffer_load_dwordx4 v13,s[40:43],s55 offen offset:128  lds
s_add_u32 m0,s57,50048
s_nop 0
buffer_load_dwordx4 v14,s[40:43],s55 offen offset:128  lds
v_add_u32 v44,57344,v15
v_add_u32 v45,57344,v16
T a[128:129],44,0
T a[130:131],44,512
)"
R"(T a[132:133],45,0
T a[134:135],45,512
T a[136:137],44,4096
T a[138:139],44,4608
T a[140:141],45,4096
T a[142:143],45,4608
L 0
s_barrier
s_add_u32 s55,s50,1966080
s_add_u32 m0,s57,57344
s_nop 0
buffer_load_dwordx4 v13,s[40:43],s55 offen offset:0  lds
s_add_u32 m0,s57,58368
s_nop 0
buffer_load_dwordx4 v14,s[40:43],s55 offen offset:0  lds
s_add_u32 m0,s57,65408
s_nop 0
buffer_load_dwordx4 v13,s[40:43],s55 offen offset:128  lds
s_add_u32 m0,s57,66432
s_nop 0
buffer_load_dwordx4 v14,s[40:43],s55 offen offset:128  lds
v_add_u32 v44,73728,v15
v_add_u32 v45,73728,v16
T a[144:145],44,0
T a[146:147],44,512
T a[148:149],45,0
T a[150:151],45,512
T a[152:153],44,4096
T a[154:155],44,4608
T a[156:157],45,4096
T a[158:159],45,4608
L 0
s_barrier
s_add_u32 s55,s50,2097152
s_add_u32 m0,s57,73728
s_nop 0
buffer_load_dwordx4 v13,s[40:43],s55 offen offset:0  lds
s_add_u32 m0,s57,74752
s_nop 0
buffer_load_dwordx4 v14,s[40:43],s55 offen offset:0  lds
s_add_u32 m0,s57,81792
s_nop 0
buffer_load_dwordx4 v13,s[40:43],s55 offen offset:128  lds
s_add_u32 m0,s57,82816
s_nop 0
buffer_load_dwordx4 v14,s[40:43],s55 offen offset:128  lds
v_add_u32 v44,90112,v15
v_add_u32 v45,90112,v16
T a[160:161],44,0
T a[162:163],44,512
T a[164:165],45,0
T a[166:167],45,512
T a[168:169],44,4096
T a[170:171],44,4608
T a[172:173],45,4096
T a[174:175],45,4608
s_waitcnt vmcnt(20) lgkmcnt(0)
s_barrier
s_add_u32 s55,s50,2228224
s_add_u32 m0,s57,90112
s_nop 0
buffer_load_dwordx4 v13,s[40:43],s55 offen offset:0  lds
s_add_u32 m0,s57,91136
s_nop 0
buffer_load_dwordx4 v14,s[40:43],s55 offen offset:0  lds
s_add_u32 m0,s57,98176
s_nop 0
buffer_load_dwordx4 v13,s[40:43],s55 offen offset:128  lds
s_add_u32 m0,s57,99200
s_nop 0
buffer_load_dwordx4 v14,s[40:43],s55 offen offset:128  lds
v_add_u32 v44,106496,v15
v_add_u32 v45,106496,v16
T a[176:177],44,0
T a[178:179],44,512
T a[180:181],45,0
T a[182:183],45,512
T a[184:185],44,4096
T a[186:187],44,4608
T a[188:189],45,4096
T a[190:191],45,4608
s_waitcnt vmcnt(20) lgkmcnt(0)
s_barrier
s_add_u32 s55,s50,2359296
s_add_u32 m0,s57,106496
s_nop 0
buffer_load_dwordx4 v13,s[40:43],s55 offen offset:0  lds
s_add_u32 m0,s57,107520
s_nop 0
buffer_load_dwordx4 v14,s[40:43],s55 offen offset:0  lds
s_add_u32 m0,s57,114560
s_nop 0
buffer_load_dwordx4 v13,s[40:43],s55 offen offset:128  lds
s_add_u32 m0,s57,115584
s_nop 0
buffer_load_dwordx4 v14,s[40:43],s55 offen offset:128  lds
v_add_u32 v44,122880,v15
v_add_u32 v45,122880,v16
T a[192:193],44,0
T a[194:195],44,512
T a[196:197],45,0
T a[198:199],45,512
T a[200:201],44,4096
T a[202:203],44,4608
T a[204:205],45,4096
T a[206:207],45,4608
s_waitcnt vmcnt(20) lgkmcnt(0)
s_barrier
s_add_u32 s55,s50,2490368
s_add_u32 m0,s57,122880
s_nop 0
buffer_load_dwordx4 v13,s[40:43],s55 offen offset:0  lds
s_add_u32 m0,s57,123904
s_nop 0
buffer_load_dwordx4 v14,s[40:43],s55 offen offset:0  lds
s_add_u32 m0,s57,130944
s_nop 0
buffer_load_dwordx4 v13,s[40:43],s55 offen offset:128  lds
s_add_u32 m0,s57,131968
s_nop 0
buffer_load_dwordx4 v14,s[40:43],s55 offen offset:128  lds
v_add_u32 v44,139264,v15
v_add_u32 v45,139264,v16
T a[208:209],44,0
T a[210:211],44,512
T a[212:213],45,0
T a[214:215],45,512
T a[216:217],44,4096
T a[218:219],44,4608
T a[220:221],45,4096
T a[222:223],45,4608
s_waitcnt vmcnt(20) lgkmcnt(0)
s_barrier
s_add_u32 s55,s50,2621440
s_add_u32 m0,s57,139264
s_nop 0
buffer_load_dwordx4 v13,s[40:43],s55 offen offset:0  lds
s_add_u32 m0,s57,140288
s_nop 0
buffer_load_dwordx4 v14,s[40:43],s55 offen offset:0  lds
s_add_u32 m0,s57,147328
s_nop 0
buffer_load_dwordx4 v13,s[40:43],s55 offen offset:128  lds
s_add_u32 m0,s57,148352
s_nop 0
buffer_load_dwordx4 v14,s[40:43],s55 offen offset:128  lds
v_add_u32 v44,40960,v15
v_add_u32 v45,40960,v16
T a[224:225],44,0
T a[226:227],44,512
T a[228:229],45,0
T a[230:231],45,512
T a[232:233],44,4096
T a[234:235],44,4608
T a[236:237],45,4096
T a[238:239],45,4608
s_waitcnt vmcnt(20) lgkmcnt(0)
s_barrier
s_add_u32 s55,s50,2752512
s_add_u32 m0,s57,40960
s_nop 0
buffer_load_dwordx4 v13,s[40:43],s55 offen offset:0  lds
s_add_u32 m0,s57,41984
s_nop 0
buffer_load_dwordx4 v14,s[40:43],s55 offen offset:0  lds
s_add_u32 m0,s57,49024
s_nop 0
buffer_load_dwordx4 v13,s[40:43],s55 offen offset:128  lds
s_add_u32 m0,s57,50048
s_nop 0
buffer_load_dwordx4 v14,s[40:43],s55 offen offset:128  lds
v_add_u32 v44,57344,v15
v_add_u32 v45,57344,v16
T a[240:241],44,0
T a[242:243],44,512
T a[244:245],45,0
T a[246:247],45,512
T a[248:249],44,4096
T a[250:251],44,4608
T a[252:253],45,4096
T a[254:255],45,4608
L 0
s_barrier
s_nop 0
Lr_h2_%=:
s_lshr_b32 s78,s45,1
s_and_b32 s79,s45,1
s_lshl_b32 s79,s79,7
s_lshl_b32 s57,s78,15
s_add_u32 s57,s57,s79
s_add_u32 s57,s57,12582912
s_add_u32 s58,s57,4096
s_lshl_b32 s59,s78,9
s_add_u32 s59,s59,s79
s_add_u32 s59,s59,107020288
s_lshl_b32 s60,s45,9
s_add_u32 s60,s60,s66
s_add_u32 s60,s60,131072
s_add_u32 s45,s45,s46
s_lshr_b32 s78,s45,1
s_and_b32 s79,s45,1
s_lshl_b32 s79,s79,7
s_lshl_b32 s55,s78,15
s_add_u32 s55,s55,s79
s_mul_i32 s56,s45,16384
s_add_u32 s56,s56,98566144
s_lshl_b32 s61,s78,11
s_add_u32 s61,s61,110587904
s_add_u32 s78,s45,s46
s_lshr_b32 s78,s78,1
s_lshl_b32 s62,s78,11
s_add_u32 s62,s62,110587904
s_waitcnt vmcnt(0)
Lr_it0_%=:
T v[68:69],10,0
T v[70:71],10,64
T v[72:73],10,1024
T v[74:75],10,1088
T v[76:77],10,2048
T v[78:79],10,2112
T v[80:81],10,3072
T v[82:83],10,3136
s_nop 0
v_cmp_eq_u32 vcc,s53,v18
s_nop 0
s_cmp_eq_u64 vcc,-1
s_cselect_b32 s78,s45,256
s_cmp_lt_u32 s78,256
s_cbranch_scc1 Lr_ta0_%=
buffer_load_dword v18,v6,s[36:39],s61 offen offset:0 sc1
v_mfma_f32_16x16x32_bf16 v[116:119],v[26:29],v[164:167],0
L 6
v_lshlrev_b32 v84,16,v68
v_and_b32 v85,0xffff0000,v68
v_mfma_f32_16x16x32_bf16 v[120:123],v[24:27],v[164:167],0
L 4
v_lshlrev_b32 v86,16,v69
v_and_b32 v87,0xffff0000,v69
M 116,v[26:29],180
L 2
v_lshlrev_b32 v88,16,v70
v_and_b32 v89,0xffff0000,v70
M 120,v[24:27],180
L 0
v_lshlrev_b32 v90,16,v71
v_and_b32 v91,0xffff0000,v71
v_mfma_f32_16x16x32_bf16 v[124:127],v[26:29],v[168:171],0
s_add_u32 m0,s65,16384
v_lshlrev_b32 v92,16,v72
v_and_b32 v93,0xffff0000,v72
v_mfma_f32_16x16x32_bf16 v[128:131],v[24:27],v[168:171],0
D 9,56,0
M 124,v[26:29],184
D 9,56,1024
M 128,v[24:27],184
D 9,56,2048
v_mfma_f32_16x16x32_bf16 v[132:135],v[26:29],v[172:175],0
D 9,56,3072
v_mfma_f32_16x16x32_bf16 v[136:139],v[24:27],v[172:175],0
v_cvt_pk_bf16_f32 v148,v116,v117
v_cvt_pk_bf16_f32 v149,v118,v119
M 132,v[26:29],188
v_cvt_pk_bf16_f32 v150,v120,v121
v_cvt_pk_bf16_f32 v151,v122,v123
M 136,v[24:27],188
v_cvt_pk_bf16_f32 v152,v124,v125
v_cvt_pk_bf16_f32 v153,v126,v127
v_mfma_f32_16x16x32_bf16 v[140:143],v[26:29],v[176:179],0
v_cvt_pk_bf16_f32 v154,v128,v129
v_cvt_pk_bf16_f32 v155,v130,v131
v_mfma_f32_16x16x32_bf16 v[144:147],v[24:27],v[176:179],0
v_lshlrev_b32 v94,16,v73
v_and_b32 v95,0xffff0000,v73
M 140,v[26:29],192
v_cvt_pk_bf16_f32 v156,v132,v133
v_cvt_pk_bf16_f32 v157,v134,v135
M 144,v[24:27],192
v_cvt_pk_bf16_f32 v158,v136,v137
v_cvt_pk_bf16_f32 v159,v138,v139
v_mfma_f32_16x16x32_bf16 v[36:39],v[32:35],v[148:151],0
v_lshlrev_b32 v96,16,v74
v_and_b32 v97,0xffff0000,v74
v_mfma_f32_16x16x32_bf16 v[40:43],v[148:151],v[148:151],0
v_lshlrev_b32 v98,16,v75
v_and_b32 v99,0xffff0000,v75
M 36,v[32:35],152
v_cvt_pk_bf16_f32 v160,v140,v141
v_cvt_pk_bf16_f32 v161,v142,v143
M 40,v[152:155],152
v_cvt_pk_bf16_f32 v162,v144,v145
v_cvt_pk_bf16_f32 v163,v146,v147
M 36,v[32:35],156
v_lshlrev_b32 v100,16,v76
v_and_b32 v101,0xffff0000,v76
M 40,v[156:159],156
v_lshlrev_b32 v102,16,v77
v_and_b32 v103,0xffff0000,v77
M 36,v[32:35],160
v_lshlrev_b32 v104,16,v78
v_and_b32 v105,0xffff0000,v78
M 40,v[160:163],160
v_lshlrev_b32 v106,16,v79
v_and_b32 v107,0xffff0000,v79
v_lshlrev_b32 v108,16,v80
v_and_b32 v109,0xffff0000,v80
v_lshlrev_b32 v110,16,v81
v_and_b32 v111,0xffff0000,v81
v_lshlrev_b32 v112,16,v82
v_and_b32 v113,0xffff0000,v82
v_lshlrev_b32 v114,16,v83
v_and_b32 v115,0xffff0000,v83
v_add_f32_dpp v44,v36,v36 row_ror:8 row_mask:0xf bank_mask:0xf
v_cndmask_b32 v45,v40,v41,s[70:71]
v_cndmask_b32 v46,v42,v43,s[70:71]
ds_write_b32 v2,v44 offset:0
v_cndmask_b32 v45,v45,v46,s[72:73]
ds_write_b32 v0,v45 offset:0
L 0
s_barrier
R 52,3,0
R 56,3,16
R 60,3,32
L 0
v_pk_add_f32 v[52:53],v[52:53],v[54:55]
v_pk_add_f32 v[56:57],v[56:57],v[58:59]
v_pk_add_f32 v[60:61],v[60:61],v[62:63]
v_add_f32 v52,v52,v53
v_pk_add_f32 v[56:57],v[56:57],v[60:61]
v_mul_f32 v52,s68,v52
v_add_f32 v53,v56,v57
v_mul_f32 v53,s68,v53
v_fma_f32 v53,-v52,v52,v53
v_max_f32 v53,0,v53
s_waitcnt vmcnt(5)
buffer_store_dword v17,v5,s[36:39],s63 offen offset:0 sc0 sc1
s_mov_b32 s63,0x7fff0000
s_waitcnt vmcnt(5)
v_cmp_eq_u32 vcc,s53,v18
s_nop 0
s_cmp_eq_u64 vcc,-1
s_cselect_b32 s78,s45,256
s_cmp_lt_u32 s78,256
s_cbranch_scc1 Lr_tb0_%=
buffer_load_dword v18,v6,s[36:39],s61 offen offset:0 sc1
v_add_f32 v53,s69,v53
v_rsq_f32 v48,v53
s_nop 0
v_mul_f32_e64 v50,-v52,v48
v_mov_b32 v49,v48
v_mov_b32 v51,v50
v_pk_fma_f32 v[52:53],v[116:117],v[48:49],v[50:51]
v_pk_fma_f32 v[54:55],v[118:119],v[48:49],v[50:51]
v_pk_fma_f32 v[56:57],v[120:121],v[48:49],v[50:51]
v_pk_fma_f32 v[58:59],v[122:123],v[48:49],v[50:51]
v_pk_fma_f32 v[116:117],v[52:53],v[84:85],v[116:117]
v_pk_fma_f32 v[118:119],v[54:55],v[86:87],v[118:119]
v_pk_fma_f32 v[120:121],v[56:57],v[88:89],v[120:121]
v_pk_fma_f32 v[122:123],v[58:59],v[90:91],v[122:123]
v_cvt_pk_bf16_f32 v148,v116,v117
v_cvt_pk_bf16_f32 v149,v118,v119
v_cvt_pk_bf16_f32 v150,v120,v121
v_cvt_pk_bf16_f32 v151,v122,v123
v_pk_fma_f32 v[52:53],v[124:125],v[48:49],v[50:51]
v_pk_fma_f32 v[54:55],v[126:127],v[48:49],v[50:51]
v_pk_fma_f32 v[56:57],v[128:129],v[48:49],v[50:51]
v_pk_fma_f32 v[58:59],v[130:131],v[48:49],v[50:51]
v_pk_fma_f32 v[124:125],v[52:53],v[92:93],v[124:125]
v_pk_fma_f32 v[126:127],v[54:55],v[94:95],v[126:127]
v_pk_fma_f32 v[128:129],v[56:57],v[96:97],v[128:129]
v_pk_fma_f32 v[130:131],v[58:59],v[98:99],v[130:131]
v_cvt_pk_bf16_f32 v152,v124,v125
v_cvt_pk_bf16_f32 v153,v126,v127
v_cvt_pk_bf16_f32 v154,v128,v129
v_cvt_pk_bf16_f32 v155,v130,v131
v_pk_fma_f32 v[52:53],v[132:133],v[48:49],v[50:51]
v_pk_fma_f32 v[54:55],v[134:135],v[48:49],v[50:51]
v_pk_fma_f32 v[56:57],v[136:137],v[48:49],v[50:51]
v_pk_fma_f32 v[58:59],v[138:139],v[48:49],v[50:51]
v_pk_fma_f32 v[132:133],v[52:53],v[100:101],v[132:133]
v_pk_fma_f32 v[134:135],v[54:55],v[102:103],v[134:135]
v_pk_fma_f32 v[136:137],v[56:57],v[104:105],v[136:137]
v_pk_fma_f32 v[138:139],v[58:59],v[106:107],v[138:139]
v_cvt_pk_bf16_f32 v156,v132,v133
v_cvt_pk_bf16_f32 v157,v134,v135
v_cvt_pk_bf16_f32 v158,v136,v137
v_cvt_pk_bf16_f32 v159,v138,v139
v_pk_fma_f32 v[52:53],v[140:141],v[48:49],v[50:51]
v_pk_fma_f32 v[54:55],v[142:143],v[48:49],v[50:51]
v_pk_fma_f32 v[56:57],v[144:145],v[48:49],v[50:51]
v_pk_fma_f32 v[58:59],v[146:147],v[48:49],v[50:51]
v_pk_fma_f32 v[140:141],v[52:53],v[108:109],v[140:141]
v_pk_fma_f32 v[142:143],v[54:55],v[110:111],v[142:143]
v_pk_fma_f32 v[144:145],v[56:57],v[112:113],v[144:145]
v_pk_fma_f32 v[146:147],v[58:59],v[114:115],v[146:147]
v_cvt_pk_bf16_f32 v160,v140,v141
v_cvt_pk_bf16_f32 v161,v142,v143
v_cvt_pk_bf16_f32 v162,v144,v145
v_cvt_pk_bf16_f32 v163,v146,v147
v_mfma_f32_16x16x32_bf16 v[36:39],v[32:35],v[148:151],0
buffer_store_dwordx4 v[148:151],v1,s[36:39],s57 offen offset:0 sc0 sc1
v_mfma_f32_16x16x32_bf16 v[40:43],v[148:151],v[148:151],0
M 36,v[32:35],152
buffer_store_dwordx4 v[152:155],v1,s[36:39],s57 offen offset:2048 sc0 sc1
M 40,v[152:155],152
M 36,v[32:35],156
buffer_store_dwordx4 v[156:159],v1,s[36:39],s58 offen offset:0 sc0 sc1
M 40,v[156:159],156
M 36,v[32:35],160
buffer_store_dwordx4 v[160:163],v1,s[36:39],s58 offen offset:2048 sc0 sc1
M 40,v[160:163],160
s_nop 5
v_add_f32_dpp v44,v36,v36 row_ror:8 row_mask:0xf bank_mask:0xf
s_nop 0
v_cndmask_b32 v45,v40,v41,s[70:71]
v_cndmask_b32 v46,v42,v43,s[70:71]
ds_write_b32 v2,v44 offset:1024
v_cndmask_b32 v45,v45,v46,s[72:73]
ds_write_b32 v0,v45 offset:1024
L 0
s_barrier
R 52,3,1024
R 56,3,1040
R 60,3,1056
L 0
v_pk_add_f32 v[52:53],v[52:53],v[54:55]
v_pk_add_f32 v[56:57],v[56:57],v[58:59]
v_pk_add_f32 v[60:61],v[60:61],v[62:63]
v_add_f32 v52,v52,v53
v_pk_add_f32 v[56:57],v[56:57],v[60:61]
v_mul_f32 v52,s68,v52
v_add_f32 v53,v56,v57
v_mul_f32 v53,s68,v53
v_fma_f32 v53,-v52,v52,v53
v_max_f32 v53,0,v53
v_add_f32 v54,s69,v53
v_fma_f32 v55,s69,v54,v53
v_rsq_f32 v65,v54
v_rsq_f32 v54,v55
v_mul_f32_e64 v64,-v52,v65
v_mul_f32 v63,v55,v54
v_mul_f32 v61,s76,v54
v_mul_f32 v55,v54,v54
v_mul_f32 v62,s77,v55
v_mul_f32 v62,v62,v61
v_cndmask_b32 v60,v52,v64,s[74:75]
v_cndmask_b32 v61,v61,v65,s[74:75]
buffer_store_dwordx4 v[60:63],v4,s[36:39],s59 offen offset:0 sc0 sc1
s_mov_b32 s63,s60
s_waitcnt vmcnt(5)
v_cmp_eq_u32 vcc,s53,v18
s_nop 0
s_cmp_eq_u64 vcc,-1
s_cselect_b32 s78,s45,256
s_cmp_lt_u32 s78,256
s_cbranch_scc1 Lr_tc0_%=
s_waitcnt vmcnt(0)
buffer_store_dword v17,v5,s[36:39],s63 offen offset:0 sc0 sc1
s_mov_b32 s63,0x7fff0000
s_cmp_ge_u32 s45,256
s_cbranch_scc1 Lr_done_%=
Lr_bp0_%=:
buffer_load_dword v18,v6,s[36:39],s61 offen offset:0 sc1
s_add_u32 s82,s82,1
s_waitcnt vmcnt(0)
v_cmp_eq_u32 vcc,s53,v18
s_nop 0
s_cmp_eq_u64 vcc,-1
s_cbranch_scc1 Lr_bq0_%=
s_sleep 2
s_cmp_lt_u32 s82,20000
s_cbranch_scc1 Lr_bp0_%=
Lr_bq0_%=:
s_add_u32 s78,s55,71303168
buffer_load_dwordx4 v[196:199],v1,s[36:39],s78 offen offset:0 sc1
buffer_load_dwordx4 v[200:203],v1,s[36:39],s78 offen offset:2048 sc1
s_add_u32 s78,s55,71307264
buffer_load_dwordx4 v[204:207],v1,s[36:39],s78 offen offset:0 sc1
buffer_load_dwordx4 v[208:211],v1,s[36:39],s78 offen offset:2048 sc1
s_add_u32 s78,s55,89128960
buffer_load_dwordx4 v[212:215],v1,s[36:39],s78 offen offset:0 sc1
buffer_load_dwordx4 v[216:219],v1,s[36:39],s78 offen offset:2048 sc1
s_add_u32 s78,s55,89133056
buffer_load_dwordx4 v[220:223],v1,s[36:39],s78 offen offset:0 sc1
buffer_load_dwordx4 v[224:227],v1,s[36:39],s78 offen offset:2048 sc1
buffer_load_dword v18,v6,s[36:39],s62 offen offset:0 sc1
s_lshr_b32 s78,s45,1
s_and_b32 s79,s45,1
s_lshl_b32 s79,s79,7
s_lshl_b32 s57,s78,15
s_add_u32 s57,s57,s79
s_add_u32 s57,s57,12582912
s_add_u32 s58,s57,4096
s_lshl_b32 s59,s78,9
s_add_u32 s59,s59,s79
s_add_u32 s59,s59,107020288
s_lshl_b32 s60,s45,9
s_add_u32 s60,s60,s66
s_add_u32 s60,s60,131072
s_add_u32 s45,s45,s46
s_lshr_b32 s78,s45,1
)"
R"(s_and_b32 s79,s45,1
s_lshl_b32 s79,s79,7
s_lshl_b32 s55,s78,15
s_add_u32 s55,s55,s79
s_mul_i32 s56,s45,16384
s_add_u32 s56,s56,98566144
s_lshl_b32 s61,s78,11
s_add_u32 s61,s61,110587904
s_add_u32 s78,s45,s46
s_lshr_b32 s78,s78,1
s_lshl_b32 s62,s78,11
s_add_u32 s62,s62,110587904
s_waitcnt vmcnt(0)
s_branch Lr_it1_%=
Lr_ta0_%=:
v_mfma_f32_16x16x32_bf16 v[116:119],v[26:29],v[164:167],0
s_add_u32 s78,s55,71303168
L 6
L 4
v_mfma_f32_16x16x32_bf16 v[120:123],v[24:27],v[164:167],0
L 2
buffer_load_dwordx4 v[196:199],v1,s[36:39],s78 offen offset:0 sc1
M 116,v[26:29],180
L 0
buffer_load_dwordx4 v[200:203],v1,s[36:39],s78 offen offset:2048 sc1
M 120,v[24:27],180
s_add_u32 s78,s55,71307264
v_lshlrev_b32 v84,16,v68
v_and_b32 v85,0xffff0000,v68
v_mfma_f32_16x16x32_bf16 v[124:127],v[26:29],v[168:171],0
buffer_load_dwordx4 v[204:207],v1,s[36:39],s78 offen offset:0 sc1
v_mfma_f32_16x16x32_bf16 v[128:131],v[24:27],v[168:171],0
buffer_load_dwordx4 v[208:211],v1,s[36:39],s78 offen offset:2048 sc1
M 124,v[26:29],184
s_add_u32 s78,s55,89128960
v_cvt_pk_bf16_f32 v148,v116,v117
v_cvt_pk_bf16_f32 v149,v118,v119
M 128,v[24:27],184
buffer_load_dwordx4 v[212:215],v1,s[36:39],s78 offen offset:0 sc1
v_mfma_f32_16x16x32_bf16 v[132:135],v[26:29],v[172:175],0
buffer_load_dwordx4 v[216:219],v1,s[36:39],s78 offen offset:2048 sc1
v_mfma_f32_16x16x32_bf16 v[136:139],v[24:27],v[172:175],0
s_add_u32 s78,s55,89133056
v_cvt_pk_bf16_f32 v150,v120,v121
v_cvt_pk_bf16_f32 v151,v122,v123
M 132,v[26:29],188
buffer_load_dwordx4 v[220:223],v1,s[36:39],s78 offen offset:0 sc1
M 136,v[24:27],188
buffer_load_dwordx4 v[224:227],v1,s[36:39],s78 offen offset:2048 sc1
v_mfma_f32_16x16x32_bf16 v[140:143],v[26:29],v[176:179],0
s_add_u32 m0,s65,16384
v_cvt_pk_bf16_f32 v152,v124,v125
v_cvt_pk_bf16_f32 v153,v126,v127
v_mfma_f32_16x16x32_bf16 v[144:147],v[24:27],v[176:179],0
D 9,56,0
M 140,v[26:29],192
D 9,56,1024
M 144,v[24:27],192
D 9,56,2048
v_mfma_f32_16x16x32_bf16 v[36:39],v[32:35],v[148:151],0
v_cvt_pk_bf16_f32 v154,v128,v129
v_cvt_pk_bf16_f32 v155,v130,v131
v_mfma_f32_16x16x32_bf16 v[40:43],v[148:151],v[148:151],0
D 9,56,3072
M 36,v[32:35],152
v_cvt_pk_bf16_f32 v156,v132,v133
v_cvt_pk_bf16_f32 v157,v134,v135
v_cvt_pk_bf16_f32 v158,v136,v137
v_cvt_pk_bf16_f32 v159,v138,v139
M 40,v[152:155],152
v_cvt_pk_bf16_f32 v160,v140,v141
v_cvt_pk_bf16_f32 v161,v142,v143
M 36,v[32:35],156
v_cvt_pk_bf16_f32 v162,v144,v145
v_cvt_pk_bf16_f32 v163,v146,v147
M 40,v[156:159],156
v_lshlrev_b32 v86,16,v69
v_and_b32 v87,0xffff0000,v69
M 36,v[32:35],160
v_lshlrev_b32 v88,16,v70
v_and_b32 v89,0xffff0000,v70
M 40,v[160:163],160
v_lshlrev_b32 v90,16,v71
v_and_b32 v91,0xffff0000,v71
v_lshlrev_b32 v92,16,v72
v_and_b32 v93,0xffff0000,v72
v_lshlrev_b32 v94,16,v73
v_and_b32 v95,0xffff0000,v73
v_lshlrev_b32 v96,16,v74
v_and_b32 v97,0xffff0000,v74
v_lshlrev_b32 v98,16,v75
v_and_b32 v99,0xffff0000,v75
v_lshlrev_b32 v100,16,v76
v_and_b32 v101,0xffff0000,v76
v_lshlrev_b32 v102,16,v77
v_and_b32 v103,0xffff0000,v77
v_lshlrev_b32 v104,16,v78
v_and_b32 v105,0xffff0000,v78
v_lshlrev_b32 v106,16,v79
v_and_b32 v107,0xffff0000,v79
v_lshlrev_b32 v108,16,v80
v_and_b32 v109,0xffff0000,v80
v_lshlrev_b32 v110,16,v81
v_and_b32 v111,0xffff0000,v81
v_lshlrev_b32 v112,16,v82
v_and_b32 v113,0xffff0000,v82
v_lshlrev_b32 v114,16,v83
v_and_b32 v115,0xffff0000,v83
v_add_f32_dpp v44,v36,v36 row_ror:8 row_mask:0xf bank_mask:0xf
v_cndmask_b32 v45,v40,v41,s[70:71]
v_cndmask_b32 v46,v42,v43,s[70:71]
ds_write_b32 v2,v44 offset:0
v_cndmask_b32 v45,v45,v46,s[72:73]
ds_write_b32 v0,v45 offset:0
L 0
s_barrier
R 52,3,0
R 56,3,16
R 60,3,32
L 0
v_pk_add_f32 v[52:53],v[52:53],v[54:55]
v_pk_add_f32 v[56:57],v[56:57],v[58:59]
v_pk_add_f32 v[60:61],v[60:61],v[62:63]
v_add_f32 v52,v52,v53
v_pk_add_f32 v[56:57],v[56:57],v[60:61]
v_mul_f32 v52,s68,v52
v_add_f32 v53,v56,v57
v_mul_f32 v53,s68,v53
v_fma_f32 v53,-v52,v52,v53
v_max_f32 v53,0,v53
buffer_load_dword v18,v6,s[36:39],s62 offen offset:0 sc1
v_add_f32 v53,s69,v53
v_rsq_f32 v48,v53
s_nop 0
v_mul_f32_e64 v50,-v52,v48
v_mov_b32 v49,v48
v_mov_b32 v51,v50
v_pk_fma_f32 v[52:53],v[116:117],v[48:49],v[50:51]
v_pk_fma_f32 v[54:55],v[118:119],v[48:49],v[50:51]
v_pk_fma_f32 v[56:57],v[120:121],v[48:49],v[50:51]
v_pk_fma_f32 v[58:59],v[122:123],v[48:49],v[50:51]
v_pk_fma_f32 v[116:117],v[52:53],v[84:85],v[116:117]
v_pk_fma_f32 v[118:119],v[54:55],v[86:87],v[118:119]
v_pk_fma_f32 v[120:121],v[56:57],v[88:89],v[120:121]
v_pk_fma_f32 v[122:123],v[58:59],v[90:91],v[122:123]
v_cvt_pk_bf16_f32 v148,v116,v117
v_cvt_pk_bf16_f32 v149,v118,v119
v_cvt_pk_bf16_f32 v150,v120,v121
v_cvt_pk_bf16_f32 v151,v122,v123
v_pk_fma_f32 v[52:53],v[124:125],v[48:49],v[50:51]
v_pk_fma_f32 v[54:55],v[126:127],v[48:49],v[50:51]
v_pk_fma_f32 v[56:57],v[128:129],v[48:49],v[50:51]
v_pk_fma_f32 v[58:59],v[130:131],v[48:49],v[50:51]
v_pk_fma_f32 v[124:125],v[52:53],v[92:93],v[124:125]
v_pk_fma_f32 v[126:127],v[54:55],v[94:95],v[126:127]
v_pk_fma_f32 v[128:129],v[56:57],v[96:97],v[128:129]
v_pk_fma_f32 v[130:131],v[58:59],v[98:99],v[130:131]
v_cvt_pk_bf16_f32 v152,v124,v125
v_cvt_pk_bf16_f32 v153,v126,v127
v_cvt_pk_bf16_f32 v154,v128,v129
v_cvt_pk_bf16_f32 v155,v130,v131
v_pk_fma_f32 v[52:53],v[132:133],v[48:49],v[50:51]
v_pk_fma_f32 v[54:55],v[134:135],v[48:49],v[50:51]
v_pk_fma_f32 v[56:57],v[136:137],v[48:49],v[50:51]
v_pk_fma_f32 v[58:59],v[138:139],v[48:49],v[50:51]
v_pk_fma_f32 v[132:133],v[52:53],v[100:101],v[132:133]
v_pk_fma_f32 v[134:135],v[54:55],v[102:103],v[134:135]
v_pk_fma_f32 v[136:137],v[56:57],v[104:105],v[136:137]
v_pk_fma_f32 v[138:139],v[58:59],v[106:107],v[138:139]
v_cvt_pk_bf16_f32 v156,v132,v133
v_cvt_pk_bf16_f32 v157,v134,v135
v_cvt_pk_bf16_f32 v158,v136,v137
v_cvt_pk_bf16_f32 v159,v138,v139
v_pk_fma_f32 v[52:53],v[140:141],v[48:49],v[50:51]
v_pk_fma_f32 v[54:55],v[142:143],v[48:49],v[50:51]
v_pk_fma_f32 v[56:57],v[144:145],v[48:49],v[50:51]
v_pk_fma_f32 v[58:59],v[146:147],v[48:49],v[50:51]
v_pk_fma_f32 v[140:141],v[52:53],v[108:109],v[140:141]
v_pk_fma_f32 v[142:143],v[54:55],v[110:111],v[142:143]
v_pk_fma_f32 v[144:145],v[56:57],v[112:113],v[144:145]
v_pk_fma_f32 v[146:147],v[58:59],v[114:115],v[146:147]
v_cvt_pk_bf16_f32 v160,v140,v141
v_cvt_pk_bf16_f32 v161,v142,v143
v_cvt_pk_bf16_f32 v162,v144,v145
v_cvt_pk_bf16_f32 v163,v146,v147
s_waitcnt vmcnt(13)
buffer_store_dword v17,v5,s[36:39],s63 offen offset:0 sc0 sc1
s_mov_b32 s63,0x7fff0000
v_mfma_f32_16x16x32_bf16 v[36:39],v[32:35],v[148:151],0
buffer_store_dwordx4 v[148:151],v1,s[36:39],s57 offen offset:0 sc0 sc1
v_mfma_f32_16x16x32_bf16 v[40:43],v[148:151],v[148:151],0
M 36,v[32:35],152
buffer_store_dwordx4 v[152:155],v1,s[36:39],s57 offen offset:2048 sc0 sc1
M 40,v[152:155],152
M 36,v[32:35],156
buffer_store_dwordx4 v[156:159],v1,s[36:39],s58 offen offset:0 sc0 sc1
M 40,v[156:159],156
M 36,v[32:35],160
buffer_store_dwordx4 v[160:163],v1,s[36:39],s58 offen offset:2048 sc0 sc1
M 40,v[160:163],160
s_nop 5
v_add_f32_dpp v44,v36,v36 row_ror:8 row_mask:0xf bank_mask:0xf
s_nop 0
v_cndmask_b32 v45,v40,v41,s[70:71]
v_cndmask_b32 v46,v42,v43,s[70:71]
ds_write_b32 v2,v44 offset:1024
v_cndmask_b32 v45,v45,v46,s[72:73]
ds_write_b32 v0,v45 offset:1024
L 0
s_barrier
R 52,3,1024
R 56,3,1040
R 60,3,1056
L 0
v_pk_add_f32 v[52:53],v[52:53],v[54:55]
v_pk_add_f32 v[56:57],v[56:57],v[58:59]
v_pk_add_f32 v[60:61],v[60:61],v[62:63]
v_add_f32 v52,v52,v53
v_pk_add_f32 v[56:57],v[56:57],v[60:61]
v_mul_f32 v52,s68,v52
v_add_f32 v53,v56,v57
v_mul_f32 v53,s68,v53
v_fma_f32 v53,-v52,v52,v53
v_max_f32 v53,0,v53
v_add_f32 v54,s69,v53
v_fma_f32 v55,s69,v54,v53
v_rsq_f32 v65,v54
v_rsq_f32 v54,v55
v_mul_f32_e64 v64,-v52,v65
v_mul_f32 v63,v55,v54
v_mul_f32 v61,s76,v54
v_mul_f32 v55,v54,v54
v_mul_f32 v62,s77,v55
v_mul_f32 v62,v62,v61
v_cndmask_b32 v60,v52,v64,s[74:75]
v_cndmask_b32 v61,v61,v65,s[74:75]
buffer_store_dwordx4 v[60:63],v4,s[36:39],s59 offen offset:0 sc0 sc1
s_mov_b32 s63,s60
s_lshr_b32 s78,s45,1
s_and_b32 s79,s45,1
s_lshl_b32 s79,s79,7
s_lshl_b32 s57,s78,15
s_add_u32 s57,s57,s79
s_add_u32 s57,s57,12582912
s_add_u32 s58,s57,4096
s_lshl_b32 s59,s78,9
s_add_u32 s59,s59,s79
s_add_u32 s59,s59,107020288
s_lshl_b32 s60,s45,9
s_add_u32 s60,s60,s66
s_add_u32 s60,s60,131072
s_add_u32 s45,s45,s46
s_lshr_b32 s78,s45,1
s_and_b32 s79,s45,1
s_lshl_b32 s79,s79,7
s_lshl_b32 s55,s78,15
s_add_u32 s55,s55,s79
s_mul_i32 s56,s45,16384
s_add_u32 s56,s56,98566144
s_lshl_b32 s61,s78,11
s_add_u32 s61,s61,110587904
s_add_u32 s78,s45,s46
s_lshr_b32 s78,s78,1
s_lshl_b32 s62,s78,11
s_add_u32 s62,s62,110587904
s_waitcnt vmcnt(6)
s_branch Lr_it1_%=
Lr_tb0_%=:
v_add_f32 v53,s69,v53
v_rsq_f32 v48,v53
s_nop 0
v_mul_f32_e64 v50,-v52,v48
v_mov_b32 v49,v48
v_mov_b32 v51,v50
v_pk_fma_f32 v[52:53],v[116:117],v[48:49],v[50:51]
v_pk_fma_f32 v[54:55],v[118:119],v[48:49],v[50:51]
v_pk_fma_f32 v[56:57],v[120:121],v[48:49],v[50:51]
v_pk_fma_f32 v[58:59],v[122:123],v[48:49],v[50:51]
s_add_u32 s78,s55,71303168
buffer_load_dwordx4 v[196:199],v1,s[36:39],s78 offen offset:0 sc1
buffer_load_dwordx4 v[200:203],v1,s[36:39],s78 offen offset:2048 sc1
s_add_u32 s78,s55,71307264
buffer_load_dwordx4 v[204:207],v1,s[36:39],s78 offen offset:0 sc1
buffer_load_dwordx4 v[208:211],v1,s[36:39],s78 offen offset:2048 sc1
v_pk_fma_f32 v[116:117],v[52:53],v[84:85],v[116:117]
v_pk_fma_f32 v[118:119],v[54:55],v[86:87],v[118:119]
v_pk_fma_f32 v[120:121],v[56:57],v[88:89],v[120:121]
v_pk_fma_f32 v[122:123],v[58:59],v[90:91],v[122:123]
v_cvt_pk_bf16_f32 v148,v116,v117
v_cvt_pk_bf16_f32 v149,v118,v119
v_cvt_pk_bf16_f32 v150,v120,v121
v_cvt_pk_bf16_f32 v151,v122,v123
v_pk_fma_f32 v[52:53],v[124:125],v[48:49],v[50:51]
v_pk_fma_f32 v[54:55],v[126:127],v[48:49],v[50:51]
v_pk_fma_f32 v[56:57],v[128:129],v[48:49],v[50:51]
v_pk_fma_f32 v[58:59],v[130:131],v[48:49],v[50:51]
s_add_u32 s78,s55,89128960
buffer_load_dwordx4 v[212:215],v1,s[36:39],s78 offen offset:0 sc1
buffer_load_dwordx4 v[216:219],v1,s[36:39],s78 offen offset:2048 sc1
s_add_u32 s78,s55,89133056
buffer_load_dwordx4 v[220:223],v1,s[36:39],s78 offen offset:0 sc1
buffer_load_dwordx4 v[224:227],v1,s[36:39],s78 offen offset:2048 sc1
v_pk_fma_f32 v[124:125],v[52:53],v[92:93],v[124:125]
v_pk_fma_f32 v[126:127],v[54:55],v[94:95],v[126:127]
v_pk_fma_f32 v[128:129],v[56:57],v[96:97],v[128:129]
v_pk_fma_f32 v[130:131],v[58:59],v[98:99],v[130:131]
v_cvt_pk_bf16_f32 v152,v124,v125
v_cvt_pk_bf16_f32 v153,v126,v127
v_cvt_pk_bf16_f32 v154,v128,v129
v_cvt_pk_bf16_f32 v155,v130,v131
v_pk_fma_f32 v[52:53],v[132:133],v[48:49],v[50:51]
v_pk_fma_f32 v[54:55],v[134:135],v[48:49],v[50:51]
v_pk_fma_f32 v[56:57],v[136:137],v[48:49],v[50:51]
v_pk_fma_f32 v[58:59],v[138:139],v[48:49],v[50:51]
v_pk_fma_f32 v[132:133],v[52:53],v[100:101],v[132:133]
v_pk_fma_f32 v[134:135],v[54:55],v[102:103],v[134:135]
v_pk_fma_f32 v[136:137],v[56:57],v[104:105],v[136:137]
v_pk_fma_f32 v[138:139],v[58:59],v[106:107],v[138:139]
v_cvt_pk_bf16_f32 v156,v132,v133
v_cvt_pk_bf16_f32 v157,v134,v135
v_cvt_pk_bf16_f32 v158,v136,v137
v_cvt_pk_bf16_f32 v159,v138,v139
v_pk_fma_f32 v[52:53],v[140:141],v[48:49],v[50:51]
v_pk_fma_f32 v[54:55],v[142:143],v[48:49],v[50:51]
v_pk_fma_f32 v[56:57],v[144:145],v[48:49],v[50:51]
v_pk_fma_f32 v[58:59],v[146:147],v[48:49],v[50:51]
v_pk_fma_f32 v[140:141],v[52:53],v[108:109],v[140:141]
v_pk_fma_f32 v[142:143],v[54:55],v[110:111],v[142:143]
v_pk_fma_f32 v[144:145],v[56:57],v[112:113],v[144:145]
v_pk_fma_f32 v[146:147],v[58:59],v[114:115],v[146:147]
v_cvt_pk_bf16_f32 v160,v140,v141
v_cvt_pk_bf16_f32 v161,v142,v143
v_cvt_pk_bf16_f32 v162,v144,v145
v_cvt_pk_bf16_f32 v163,v146,v147
v_mfma_f32_16x16x32_bf16 v[36:39],v[32:35],v[148:151],0
buffer_load_dword v18,v6,s[36:39],s62 offen offset:0 sc1
buffer_store_dwordx4 v[148:151],v1,s[36:39],s57 offen offset:0 sc0 sc1
v_mfma_f32_16x16x32_bf16 v[40:43],v[148:151],v[148:151],0
M 36,v[32:35],152
buffer_store_dwordx4 v[152:155],v1,s[36:39],s57 offen offset:2048 sc0 sc1
M 40,v[152:155],152
M 36,v[32:35],156
buffer_store_dwordx4 v[156:159],v1,s[36:39],s58 offen offset:0 sc0 sc1
M 40,v[156:159],156
M 36,v[32:35],160
buffer_store_dwordx4 v[160:163],v1,s[36:39],s58 offen offset:2048 sc0 sc1
M 40,v[160:163],160
s_nop 5
v_add_f32_dpp v44,v36,v36 row_ror:8 row_mask:0xf bank_mask:0xf
s_nop 0
v_cndmask_b32 v45,v40,v41,s[70:71]
v_cndmask_b32 v46,v42,v43,s[70:71]
ds_write_b32 v2,v44 offset:1024
v_cndmask_b32 v45,v45,v46,s[72:73]
ds_write_b32 v0,v45 offset:1024
L 0
s_barrier
R 52,3,1024
R 56,3,1040
R 60,3,1056
L 0
v_pk_add_f32 v[52:53],v[52:53],v[54:55]
v_pk_add_f32 v[56:57],v[56:57],v[58:59]
v_pk_add_f32 v[60:61],v[60:61],v[62:63]
v_add_f32 v52,v52,v53
v_pk_add_f32 v[56:57],v[56:57],v[60:61]
v_mul_f32 v52,s68,v52
v_add_f32 v53,v56,v57
v_mul_f32 v53,s68,v53
v_fma_f32 v53,-v52,v52,v53
v_max_f32 v53,0,v53
v_add_f32 v54,s69,v53
v_fma_f32 v55,s69,v54,v53
v_rsq_f32 v65,v54
v_rsq_f32 v54,v55
v_mul_f32_e64 v64,-v52,v65
v_mul_f32 v63,v55,v54
v_mul_f32 v61,s76,v54
v_mul_f32 v55,v54,v54
v_mul_f32 v62,s77,v55
v_mul_f32 v62,v62,v61
v_cndmask_b32 v60,v52,v64,s[74:75]
v_cndmask_b32 v61,v61,v65,s[74:75]
buffer_store_dwordx4 v[60:63],v4,s[36:39],s59 offen offset:0 sc0 sc1
s_mov_b32 s63,s60
s_lshr_b32 s78,s45,1
s_and_b32 s79,s45,1
s_lshl_b32 s79,s79,7
s_lshl_b32 s57,s78,15
s_add_u32 s57,s57,s79
s_add_u32 s57,s57,12582912
s_add_u32 s58,s57,4096
s_lshl_b32 s59,s78,9
s_add_u32 s59,s59,s79
s_add_u32 s59,s59,107020288
s_lshl_b32 s60,s45,9
s_add_u32 s60,s60,s66
s_add_u32 s60,s60,131072
s_add_u32 s45,s45,s46
s_lshr_b32 s78,s45,1
s_and_b32 s79,s45,1
s_lshl_b32 s79,s79,7
s_lshl_b32 s55,s78,15
s_add_u32 s55,s55,s79
s_mul_i32 s56,s45,16384
s_add_u32 s56,s56,98566144
s_lshl_b32 s61,s78,11
s_add_u32 s61,s61,110587904
s_add_u32 s78,s45,s46
s_lshr_b32 s78,s78,1
s_lshl_b32 s62,s78,11
s_add_u32 s62,s62,110587904
s_waitcnt vmcnt(5)
s_branch Lr_it1_%=
Lr_tc0_%=:
s_add_u32 s78,s55,71303168
buffer_load_dwordx4 v[196:199],v1,s[36:39],s78 offen offset:0 sc1
buffer_load_dwordx4 v[200:203],v1,s[36:39],s78 offen offset:2048 sc1
s_add_u32 s78,s55,71307264
buffer_load_dwordx4 v[204:207],v1,s[36:39],s78 offen offset:0 sc1
buffer_load_dwordx4 v[208:211],v1,s[36:39],s78 offen offset:2048 sc1
s_add_u32 s78,s55,89128960
buffer_load_dwordx4 v[212:215],v1,s[36:39],s78 offen offset:0 sc1
buffer_load_dwordx4 v[216:219],v1,s[36:39],s78 offen offset:2048 sc1
s_add_u32 s78,s55,89133056
buffer_load_dwordx4 v[220:223],v1,s[36:39],s78 offen offset:0 sc1
buffer_load_dwordx4 v[224:227],v1,s[36:39],s78 offen offset:2048 sc1
buffer_load_dword v18,v6,s[36:39],s62 offen offset:0 sc1
s_waitcnt vmcnt(9)
buffer_store_dword v17,v5,s[36:39],s63 offen offset:0 sc0 sc1
s_mov_b32 s63,0x7fff0000
s_lshr_b32 s78,s45,1
s_and_b32 s79,s45,1
s_lshl_b32 s79,s79,7
s_lshl_b32 s57,s78,15
s_add_u32 s57,s57,s79
s_add_u32 s57,s57,12582912
s_add_u32 s58,s57,4096
s_lshl_b32 s59,s78,9
s_add_u32 s59,s59,s79
s_add_u32 s59,s59,107020288
s_lshl_b32 s60,s45,9
s_add_u32 s60,s60,s66
s_add_u32 s60,s60,131072
s_add_u32 s45,s45,s46
s_lshr_b32 s78,s45,1
s_and_b32 s79,s45,1
s_lshl_b32 s79,s79,7
s_lshl_b32 s55,s78,15
s_add_u32 s55,s55,s79
s_mul_i32 s56,s45,16384
s_add_u32 s56,s56,98566144
s_lshl_b32 s61,s78,11
s_add_u32 s61,s61,110587904
s_add_u32 s78,s45,s46
s_lshr_b32 s78,s78,1
s_lshl_b32 s62,s78,11
s_add_u32 s62,s62,110587904
s_waitcnt vmcnt(1)
s_branch Lr_it1_%=
Lr_it1_%=:
T v[68:69],10,16384
T v[70:71],10,16448
T v[72:73],10,17408
T v[74:75],10,17472
T v[76:77],10,18432
T v[78:79],10,18496
T v[80:81],10,19456
T v[82:83],10,19520
s_nop 0
v_cmp_eq_u32 vcc,s53,v18
s_nop 0
s_cmp_eq_u64 vcc,-1
s_cselect_b32 s78,s45,256
s_cmp_lt_u32 s78,256
s_cbranch_scc1 Lr_ta1_%=
buffer_load_dword v18,v6,s[36:39],s61 offen offset:0 sc1
v_mfma_f32_16x16x32_bf16 v[116:119],v[26:29],v[196:199],0
L 6
v_lshlrev_b32 v84,16,v68
v_and_b32 v85,0xffff0000,v68
v_mfma_f32_16x16x32_bf16 v[120:123],v[24:27],v[196:199],0
L 4
v_lshlrev_b32 v86,16,v69
v_and_b32 v87,0xffff0000,v69
M 116,v[26:29],212
L 2
v_lshlrev_b32 v88,16,v70
v_and_b32 v89,0xffff0000,v70
M 120,v[24:27],212
L 0
v_lshlrev_b32 v90,16,v71
v_and_b32 v91,0xffff0000,v71
v_mfma_f32_16x16x32_bf16 v[124:127],v[26:29],v[200:203],0
s_add_u32 m0,s65,0
v_lshlrev_b32 v92,16,v72
v_and_b32 v93,0xffff0000,v72
v_mfma_f32_16x16x32_bf16 v[128:131],v[24:27],v[200:203],0
D 9,56,0
M 124,v[26:29],216
)"
R"(D 9,56,1024
M 128,v[24:27],216
D 9,56,2048
v_mfma_f32_16x16x32_bf16 v[132:135],v[26:29],v[204:207],0
D 9,56,3072
v_mfma_f32_16x16x32_bf16 v[136:139],v[24:27],v[204:207],0
v_cvt_pk_bf16_f32 v148,v116,v117
v_cvt_pk_bf16_f32 v149,v118,v119
M 132,v[26:29],220
v_cvt_pk_bf16_f32 v150,v120,v121
v_cvt_pk_bf16_f32 v151,v122,v123
M 136,v[24:27],220
v_cvt_pk_bf16_f32 v152,v124,v125
v_cvt_pk_bf16_f32 v153,v126,v127
v_mfma_f32_16x16x32_bf16 v[140:143],v[26:29],v[208:211],0
v_cvt_pk_bf16_f32 v154,v128,v129
v_cvt_pk_bf16_f32 v155,v130,v131
v_mfma_f32_16x16x32_bf16 v[144:147],v[24:27],v[208:211],0
v_lshlrev_b32 v94,16,v73
v_and_b32 v95,0xffff0000,v73
M 140,v[26:29],224
v_cvt_pk_bf16_f32 v156,v132,v133
v_cvt_pk_bf16_f32 v157,v134,v135
M 144,v[24:27],224
v_cvt_pk_bf16_f32 v158,v136,v137
v_cvt_pk_bf16_f32 v159,v138,v139
v_mfma_f32_16x16x32_bf16 v[36:39],v[32:35],v[148:151],0
v_lshlrev_b32 v96,16,v74
v_and_b32 v97,0xffff0000,v74
v_mfma_f32_16x16x32_bf16 v[40:43],v[148:151],v[148:151],0
v_lshlrev_b32 v98,16,v75
v_and_b32 v99,0xffff0000,v75
M 36,v[32:35],152
v_cvt_pk_bf16_f32 v160,v140,v141
v_cvt_pk_bf16_f32 v161,v142,v143
M 40,v[152:155],152
v_cvt_pk_bf16_f32 v162,v144,v145
v_cvt_pk_bf16_f32 v163,v146,v147
M 36,v[32:35],156
v_lshlrev_b32 v100,16,v76
v_and_b32 v101,0xffff0000,v76
M 40,v[156:159],156
v_lshlrev_b32 v102,16,v77
v_and_b32 v103,0xffff0000,v77
M 36,v[32:35],160
v_lshlrev_b32 v104,16,v78
v_and_b32 v105,0xffff0000,v78
M 40,v[160:163],160
v_lshlrev_b32 v106,16,v79
v_and_b32 v107,0xffff0000,v79
v_lshlrev_b32 v108,16,v80
v_and_b32 v109,0xffff0000,v80
v_lshlrev_b32 v110,16,v81
v_and_b32 v111,0xffff0000,v81
v_lshlrev_b32 v112,16,v82
v_and_b32 v113,0xffff0000,v82
v_lshlrev_b32 v114,16,v83
v_and_b32 v115,0xffff0000,v83
v_add_f32_dpp v44,v36,v36 row_ror:8 row_mask:0xf bank_mask:0xf
v_cndmask_b32 v45,v40,v41,s[70:71]
v_cndmask_b32 v46,v42,v43,s[70:71]
ds_write_b32 v2,v44 offset:0
v_cndmask_b32 v45,v45,v46,s[72:73]
ds_write_b32 v0,v45 offset:0
L 0
s_barrier
R 52,3,0
R 56,3,16
R 60,3,32
L 0
v_pk_add_f32 v[52:53],v[52:53],v[54:55]
v_pk_add_f32 v[56:57],v[56:57],v[58:59]
v_pk_add_f32 v[60:61],v[60:61],v[62:63]
v_add_f32 v52,v52,v53
v_pk_add_f32 v[56:57],v[56:57],v[60:61]
v_mul_f32 v52,s68,v52
v_add_f32 v53,v56,v57
v_mul_f32 v53,s68,v53
v_fma_f32 v53,-v52,v52,v53
v_max_f32 v53,0,v53
s_waitcnt vmcnt(5)
buffer_store_dword v17,v5,s[36:39],s63 offen offset:0 sc0 sc1
s_mov_b32 s63,0x7fff0000
s_waitcnt vmcnt(5)
v_cmp_eq_u32 vcc,s53,v18
s_nop 0
s_cmp_eq_u64 vcc,-1
s_cselect_b32 s78,s45,256
s_cmp_lt_u32 s78,256
s_cbranch_scc1 Lr_tb1_%=
buffer_load_dword v18,v6,s[36:39],s61 offen offset:0 sc1
v_add_f32 v53,s69,v53
v_rsq_f32 v48,v53
s_nop 0
v_mul_f32_e64 v50,-v52,v48
v_mov_b32 v49,v48
v_mov_b32 v51,v50
v_pk_fma_f32 v[52:53],v[116:117],v[48:49],v[50:51]
v_pk_fma_f32 v[54:55],v[118:119],v[48:49],v[50:51]
v_pk_fma_f32 v[56:57],v[120:121],v[48:49],v[50:51]
v_pk_fma_f32 v[58:59],v[122:123],v[48:49],v[50:51]
v_pk_fma_f32 v[116:117],v[52:53],v[84:85],v[116:117]
v_pk_fma_f32 v[118:119],v[54:55],v[86:87],v[118:119]
v_pk_fma_f32 v[120:121],v[56:57],v[88:89],v[120:121]
v_pk_fma_f32 v[122:123],v[58:59],v[90:91],v[122:123]
v_cvt_pk_bf16_f32 v148,v116,v117
v_cvt_pk_bf16_f32 v149,v118,v119
v_cvt_pk_bf16_f32 v150,v120,v121
v_cvt_pk_bf16_f32 v151,v122,v123
v_pk_fma_f32 v[52:53],v[124:125],v[48:49],v[50:51]
v_pk_fma_f32 v[54:55],v[126:127],v[48:49],v[50:51]
v_pk_fma_f32 v[56:57],v[128:129],v[48:49],v[50:51]
v_pk_fma_f32 v[58:59],v[130:131],v[48:49],v[50:51]
v_pk_fma_f32 v[124:125],v[52:53],v[92:93],v[124:125]
v_pk_fma_f32 v[126:127],v[54:55],v[94:95],v[126:127]
v_pk_fma_f32 v[128:129],v[56:57],v[96:97],v[128:129]
v_pk_fma_f32 v[130:131],v[58:59],v[98:99],v[130:131]
v_cvt_pk_bf16_f32 v152,v124,v125
v_cvt_pk_bf16_f32 v153,v126,v127
v_cvt_pk_bf16_f32 v154,v128,v129
v_cvt_pk_bf16_f32 v155,v130,v131
v_pk_fma_f32 v[52:53],v[132:133],v[48:49],v[50:51]
v_pk_fma_f32 v[54:55],v[134:135],v[48:49],v[50:51]
v_pk_fma_f32 v[56:57],v[136:137],v[48:49],v[50:51]
v_pk_fma_f32 v[58:59],v[138:139],v[48:49],v[50:51]
v_pk_fma_f32 v[132:133],v[52:53],v[100:101],v[132:133]
v_pk_fma_f32 v[134:135],v[54:55],v[102:103],v[134:135]
v_pk_fma_f32 v[136:137],v[56:57],v[104:105],v[136:137]
v_pk_fma_f32 v[138:139],v[58:59],v[106:107],v[138:139]
v_cvt_pk_bf16_f32 v156,v132,v133
v_cvt_pk_bf16_f32 v157,v134,v135
v_cvt_pk_bf16_f32 v158,v136,v137
v_cvt_pk_bf16_f32 v159,v138,v139
v_pk_fma_f32 v[52:53],v[140:141],v[48:49],v[50:51]
v_pk_fma_f32 v[54:55],v[142:143],v[48:49],v[50:51]
v_pk_fma_f32 v[56:57],v[144:145],v[48:49],v[50:51]
v_pk_fma_f32 v[58:59],v[146:147],v[48:49],v[50:51]
v_pk_fma_f32 v[140:141],v[52:53],v[108:109],v[140:141]
v_pk_fma_f32 v[142:143],v[54:55],v[110:111],v[142:143]
v_pk_fma_f32 v[144:145],v[56:57],v[112:113],v[144:145]
v_pk_fma_f32 v[146:147],v[58:59],v[114:115],v[146:147]
v_cvt_pk_bf16_f32 v160,v140,v141
v_cvt_pk_bf16_f32 v161,v142,v143
v_cvt_pk_bf16_f32 v162,v144,v145
v_cvt_pk_bf16_f32 v163,v146,v147
v_mfma_f32_16x16x32_bf16 v[36:39],v[32:35],v[148:151],0
buffer_store_dwordx4 v[148:151],v1,s[36:39],s57 offen offset:0 sc0 sc1
v_mfma_f32_16x16x32_bf16 v[40:43],v[148:151],v[148:151],0
M 36,v[32:35],152
buffer_store_dwordx4 v[152:155],v1,s[36:39],s57 offen offset:2048 sc0 sc1
M 40,v[152:155],152
M 36,v[32:35],156
buffer_store_dwordx4 v[156:159],v1,s[36:39],s58 offen offset:0 sc0 sc1
M 40,v[156:159],156
M 36,v[32:35],160
buffer_store_dwordx4 v[160:163],v1,s[36:39],s58 offen offset:2048 sc0 sc1
M 40,v[160:163],160
s_nop 5
v_add_f32_dpp v44,v36,v36 row_ror:8 row_mask:0xf bank_mask:0xf
s_nop 0
v_cndmask_b32 v45,v40,v41,s[70:71]
v_cndmask_b32 v46,v42,v43,s[70:71]
ds_write_b32 v2,v44 offset:1024
v_cndmask_b32 v45,v45,v46,s[72:73]
ds_write_b32 v0,v45 offset:1024
L 0
s_barrier
R 52,3,1024
R 56,3,1040
R 60,3,1056
L 0
v_pk_add_f32 v[52:53],v[52:53],v[54:55]
v_pk_add_f32 v[56:57],v[56:57],v[58:59]
v_pk_add_f32 v[60:61],v[60:61],v[62:63]
v_add_f32 v52,v52,v53
v_pk_add_f32 v[56:57],v[56:57],v[60:61]
v_mul_f32 v52,s68,v52
v_add_f32 v53,v56,v57
v_mul_f32 v53,s68,v53
v_fma_f32 v53,-v52,v52,v53
v_max_f32 v53,0,v53
v_add_f32 v54,s69,v53
v_fma_f32 v55,s69,v54,v53
v_rsq_f32 v65,v54
v_rsq_f32 v54,v55
v_mul_f32_e64 v64,-v52,v65
v_mul_f32 v63,v55,v54
v_mul_f32 v61,s76,v54
v_mul_f32 v55,v54,v54
v_mul_f32 v62,s77,v55
v_mul_f32 v62,v62,v61
v_cndmask_b32 v60,v52,v64,s[74:75]
v_cndmask_b32 v61,v61,v65,s[74:75]
buffer_store_dwordx4 v[60:63],v4,s[36:39],s59 offen offset:0 sc0 sc1
s_mov_b32 s63,s60
s_waitcnt vmcnt(5)
v_cmp_eq_u32 vcc,s53,v18
s_nop 0
s_cmp_eq_u64 vcc,-1
s_cselect_b32 s78,s45,256
s_cmp_lt_u32 s78,256
s_cbranch_scc1 Lr_tc1_%=
s_waitcnt vmcnt(0)
buffer_store_dword v17,v5,s[36:39],s63 offen offset:0 sc0 sc1
s_mov_b32 s63,0x7fff0000
s_cmp_ge_u32 s45,256
s_cbranch_scc1 Lr_done_%=
Lr_bp1_%=:
buffer_load_dword v18,v6,s[36:39],s61 offen offset:0 sc1
s_add_u32 s82,s82,1
s_waitcnt vmcnt(0)
v_cmp_eq_u32 vcc,s53,v18
s_nop 0
s_cmp_eq_u64 vcc,-1
s_cbranch_scc1 Lr_bq1_%=
s_sleep 2
s_cmp_lt_u32 s82,20000
s_cbranch_scc1 Lr_bp1_%=
Lr_bq1_%=:
s_add_u32 s78,s55,71303168
buffer_load_dwordx4 v[164:167],v1,s[36:39],s78 offen offset:0 sc1
buffer_load_dwordx4 v[168:171],v1,s[36:39],s78 offen offset:2048 sc1
s_add_u32 s78,s55,71307264
buffer_load_dwordx4 v[172:175],v1,s[36:39],s78 offen offset:0 sc1
buffer_load_dwordx4 v[176:179],v1,s[36:39],s78 offen offset:2048 sc1
s_add_u32 s78,s55,89128960
buffer_load_dwordx4 v[180:183],v1,s[36:39],s78 offen offset:0 sc1
buffer_load_dwordx4 v[184:187],v1,s[36:39],s78 offen offset:2048 sc1
s_add_u32 s78,s55,89133056
buffer_load_dwordx4 v[188:191],v1,s[36:39],s78 offen offset:0 sc1
buffer_load_dwordx4 v[192:195],v1,s[36:39],s78 offen offset:2048 sc1
buffer_load_dword v18,v6,s[36:39],s62 offen offset:0 sc1
s_lshr_b32 s78,s45,1
s_and_b32 s79,s45,1
s_lshl_b32 s79,s79,7
s_lshl_b32 s57,s78,15
s_add_u32 s57,s57,s79
s_add_u32 s57,s57,12582912
s_add_u32 s58,s57,4096
s_lshl_b32 s59,s78,9
s_add_u32 s59,s59,s79
s_add_u32 s59,s59,107020288
s_lshl_b32 s60,s45,9
s_add_u32 s60,s60,s66
s_add_u32 s60,s60,131072
s_add_u32 s45,s45,s46
s_lshr_b32 s78,s45,1
s_and_b32 s79,s45,1
s_lshl_b32 s79,s79,7
s_lshl_b32 s55,s78,15
s_add_u32 s55,s55,s79
s_mul_i32 s56,s45,16384
s_add_u32 s56,s56,98566144
s_lshl_b32 s61,s78,11
s_add_u32 s61,s61,110587904
s_add_u32 s78,s45,s46
s_lshr_b32 s78,s78,1
s_lshl_b32 s62,s78,11
s_add_u32 s62,s62,110587904
s_waitcnt vmcnt(0)
s_branch Lr_it0_%=
Lr_ta1_%=:
v_mfma_f32_16x16x32_bf16 v[116:119],v[26:29],v[196:199],0
s_add_u32 s78,s55,71303168
L 6
L 4
v_mfma_f32_16x16x32_bf16 v[120:123],v[24:27],v[196:199],0
L 2
buffer_load_dwordx4 v[164:167],v1,s[36:39],s78 offen offset:0 sc1
M 116,v[26:29],212
L 0
buffer_load_dwordx4 v[168:171],v1,s[36:39],s78 offen offset:2048 sc1
M 120,v[24:27],212
s_add_u32 s78,s55,71307264
v_lshlrev_b32 v84,16,v68
v_and_b32 v85,0xffff0000,v68
v_mfma_f32_16x16x32_bf16 v[124:127],v[26:29],v[200:203],0
buffer_load_dwordx4 v[172:175],v1,s[36:39],s78 offen offset:0 sc1
v_mfma_f32_16x16x32_bf16 v[128:131],v[24:27],v[200:203],0
buffer_load_dwordx4 v[176:179],v1,s[36:39],s78 offen offset:2048 sc1
M 124,v[26:29],216
s_add_u32 s78,s55,89128960
v_cvt_pk_bf16_f32 v148,v116,v117
v_cvt_pk_bf16_f32 v149,v118,v119
M 128,v[24:27],216
buffer_load_dwordx4 v[180:183],v1,s[36:39],s78 offen offset:0 sc1
v_mfma_f32_16x16x32_bf16 v[132:135],v[26:29],v[204:207],0
buffer_load_dwordx4 v[184:187],v1,s[36:39],s78 offen offset:2048 sc1
v_mfma_f32_16x16x32_bf16 v[136:139],v[24:27],v[204:207],0
s_add_u32 s78,s55,89133056
v_cvt_pk_bf16_f32 v150,v120,v121
v_cvt_pk_bf16_f32 v151,v122,v123
M 132,v[26:29],220
buffer_load_dwordx4 v[188:191],v1,s[36:39],s78 offen offset:0 sc1
M 136,v[24:27],220
buffer_load_dwordx4 v[192:195],v1,s[36:39],s78 offen offset:2048 sc1
v_mfma_f32_16x16x32_bf16 v[140:143],v[26:29],v[208:211],0
s_add_u32 m0,s65,0
v_cvt_pk_bf16_f32 v152,v124,v125
v_cvt_pk_bf16_f32 v153,v126,v127
v_mfma_f32_16x16x32_bf16 v[144:147],v[24:27],v[208:211],0
D 9,56,0
M 140,v[26:29],224
D 9,56,1024
M 144,v[24:27],224
D 9,56,2048
v_mfma_f32_16x16x32_bf16 v[36:39],v[32:35],v[148:151],0
v_cvt_pk_bf16_f32 v154,v128,v129
v_cvt_pk_bf16_f32 v155,v130,v131
v_mfma_f32_16x16x32_bf16 v[40:43],v[148:151],v[148:151],0
D 9,56,3072
M 36,v[32:35],152
v_cvt_pk_bf16_f32 v156,v132,v133
v_cvt_pk_bf16_f32 v157,v134,v135
v_cvt_pk_bf16_f32 v158,v136,v137
v_cvt_pk_bf16_f32 v159,v138,v139
M 40,v[152:155],152
v_cvt_pk_bf16_f32 v160,v140,v141
v_cvt_pk_bf16_f32 v161,v142,v143
M 36,v[32:35],156
v_cvt_pk_bf16_f32 v162,v144,v145
v_cvt_pk_bf16_f32 v163,v146,v147
M 40,v[156:159],156
v_lshlrev_b32 v86,16,v69
v_and_b32 v87,0xffff0000,v69
M 36,v[32:35],160
v_lshlrev_b32 v88,16,v70
v_and_b32 v89,0xffff0000,v70
M 40,v[160:163],160
v_lshlrev_b32 v90,16,v71
v_and_b32 v91,0xffff0000,v71
v_lshlrev_b32 v92,16,v72
v_and_b32 v93,0xffff0000,v72
v_lshlrev_b32 v94,16,v73
v_and_b32 v95,0xffff0000,v73
v_lshlrev_b32 v96,16,v74
v_and_b32 v97,0xffff0000,v74
v_lshlrev_b32 v98,16,v75
v_and_b32 v99,0xffff0000,v75
v_lshlrev_b32 v100,16,v76
v_and_b32 v101,0xffff0000,v76
v_lshlrev_b32 v102,16,v77
v_and_b32 v103,0xffff0000,v77
v_lshlrev_b32 v104,16,v78
v_and_b32 v105,0xffff0000,v78
v_lshlrev_b32 v106,16,v79
v_and_b32 v107,0xffff0000,v79
v_lshlrev_b32 v108,16,v80
v_and_b32 v109,0xffff0000,v80
v_lshlrev_b32 v110,16,v81
v_and_b32 v111,0xffff0000,v81
v_lshlrev_b32 v112,16,v82
v_and_b32 v113,0xffff0000,v82
v_lshlrev_b32 v114,16,v83
v_and_b32 v115,0xffff0000,v83
v_add_f32_dpp v44,v36,v36 row_ror:8 row_mask:0xf bank_mask:0xf
v_cndmask_b32 v45,v40,v41,s[70:71]
v_cndmask_b32 v46,v42,v43,s[70:71]
ds_write_b32 v2,v44 offset:0
v_cndmask_b32 v45,v45,v46,s[72:73]
ds_write_b32 v0,v45 offset:0
L 0
s_barrier
R 52,3,0
R 56,3,16
R 60,3,32
L 0
v_pk_add_f32 v[52:53],v[52:53],v[54:55]
v_pk_add_f32 v[56:57],v[56:57],v[58:59]
v_pk_add_f32 v[60:61],v[60:61],v[62:63]
v_add_f32 v52,v52,v53
v_pk_add_f32 v[56:57],v[56:57],v[60:61]
v_mul_f32 v52,s68,v52
v_add_f32 v53,v56,v57
v_mul_f32 v53,s68,v53
v_fma_f32 v53,-v52,v52,v53
v_max_f32 v53,0,v53
buffer_load_dword v18,v6,s[36:39],s62 offen offset:0 sc1
v_add_f32 v53,s69,v53
v_rsq_f32 v48,v53
s_nop 0
v_mul_f32_e64 v50,-v52,v48
v_mov_b32 v49,v48
v_mov_b32 v51,v50
v_pk_fma_f32 v[52:53],v[116:117],v[48:49],v[50:51]
v_pk_fma_f32 v[54:55],v[118:119],v[48:49],v[50:51]
v_pk_fma_f32 v[56:57],v[120:121],v[48:49],v[50:51]
v_pk_fma_f32 v[58:59],v[122:123],v[48:49],v[50:51]
v_pk_fma_f32 v[116:117],v[52:53],v[84:85],v[116:117]
v_pk_fma_f32 v[118:119],v[54:55],v[86:87],v[118:119]
v_pk_fma_f32 v[120:121],v[56:57],v[88:89],v[120:121]
v_pk_fma_f32 v[122:123],v[58:59],v[90:91],v[122:123]
v_cvt_pk_bf16_f32 v148,v116,v117
v_cvt_pk_bf16_f32 v149,v118,v119
v_cvt_pk_bf16_f32 v150,v120,v121
v_cvt_pk_bf16_f32 v151,v122,v123
v_pk_fma_f32 v[52:53],v[124:125],v[48:49],v[50:51]
v_pk_fma_f32 v[54:55],v[126:127],v[48:49],v[50:51]
v_pk_fma_f32 v[56:57],v[128:129],v[48:49],v[50:51]
v_pk_fma_f32 v[58:59],v[130:131],v[48:49],v[50:51]
v_pk_fma_f32 v[124:125],v[52:53],v[92:93],v[124:125]
v_pk_fma_f32 v[126:127],v[54:55],v[94:95],v[126:127]
v_pk_fma_f32 v[128:129],v[56:57],v[96:97],v[128:129]
v_pk_fma_f32 v[130:131],v[58:59],v[98:99],v[130:131]
v_cvt_pk_bf16_f32 v152,v124,v125
v_cvt_pk_bf16_f32 v153,v126,v127
v_cvt_pk_bf16_f32 v154,v128,v129
v_cvt_pk_bf16_f32 v155,v130,v131
v_pk_fma_f32 v[52:53],v[132:133],v[48:49],v[50:51]
v_pk_fma_f32 v[54:55],v[134:135],v[48:49],v[50:51]
v_pk_fma_f32 v[56:57],v[136:137],v[48:49],v[50:51]
v_pk_fma_f32 v[58:59],v[138:139],v[48:49],v[50:51]
v_pk_fma_f32 v[132:133],v[52:53],v[100:101],v[132:133]
v_pk_fma_f32 v[134:135],v[54:55],v[102:103],v[134:135]
v_pk_fma_f32 v[136:137],v[56:57],v[104:105],v[136:137]
v_pk_fma_f32 v[138:139],v[58:59],v[106:107],v[138:139]
v_cvt_pk_bf16_f32 v156,v132,v133
v_cvt_pk_bf16_f32 v157,v134,v135
v_cvt_pk_bf16_f32 v158,v136,v137
v_cvt_pk_bf16_f32 v159,v138,v139
v_pk_fma_f32 v[52:53],v[140:141],v[48:49],v[50:51]
v_pk_fma_f32 v[54:55],v[142:143],v[48:49],v[50:51]
v_pk_fma_f32 v[56:57],v[144:145],v[48:49],v[50:51]
v_pk_fma_f32 v[58:59],v[146:147],v[48:49],v[50:51]
v_pk_fma_f32 v[140:141],v[52:53],v[108:109],v[140:141]
v_pk_fma_f32 v[142:143],v[54:55],v[110:111],v[142:143]
v_pk_fma_f32 v[144:145],v[56:57],v[112:113],v[144:145]
v_pk_fma_f32 v[146:147],v[58:59],v[114:115],v[146:147]
v_cvt_pk_bf16_f32 v160,v140,v141
v_cvt_pk_bf16_f32 v161,v142,v143
v_cvt_pk_bf16_f32 v162,v144,v145
v_cvt_pk_bf16_f32 v163,v146,v147
s_waitcnt vmcnt(13)
buffer_store_dword v17,v5,s[36:39],s63 offen offset:0 sc0 sc1
s_mov_b32 s63,0x7fff0000
v_mfma_f32_16x16x32_bf16 v[36:39],v[32:35],v[148:151],0
buffer_store_dwordx4 v[148:151],v1,s[36:39],s57 offen offset:0 sc0 sc1
v_mfma_f32_16x16x32_bf16 v[40:43],v[148:151],v[148:151],0
M 36,v[32:35],152
buffer_store_dwordx4 v[152:155],v1,s[36:39],s57 offen offset:2048 sc0 sc1
M 40,v[152:155],152
M 36,v[32:35],156
buffer_store_dwordx4 v[156:159],v1,s[36:39],s58 offen offset:0 sc0 sc1
M 40,v[156:159],156
M 36,v[32:35],160
buffer_store_dwordx4 v[160:163],v1,s[36:39],s58 offen offset:2048 sc0 sc1
M 40,v[160:163],160
s_nop 5
v_add_f32_dpp v44,v36,v36 row_ror:8 row_mask:0xf bank_mask:0xf
s_nop 0
v_cndmask_b32 v45,v40,v41,s[70:71]
v_cndmask_b32 v46,v42,v43,s[70:71]
ds_write_b32 v2,v44 offset:1024
v_cndmask_b32 v45,v45,v46,s[72:73]
ds_write_b32 v0,v45 offset:1024
L 0
s_barrier
R 52,3,1024
R 56,3,1040
R 60,3,1056
L 0
v_pk_add_f32 v[52:53],v[52:53],v[54:55]
v_pk_add_f32 v[56:57],v[56:57],v[58:59]
v_pk_add_f32 v[60:61],v[60:61],v[62:63]
v_add_f32 v52,v52,v53
v_pk_add_f32 v[56:57],v[56:57],v[60:61]
v_mul_f32 v52,s68,v52
v_add_f32 v53,v56,v57
v_mul_f32 v53,s68,v53
v_fma_f32 v53,-v52,v52,v53
v_max_f32 v53,0,v53
v_add_f32 v54,s69,v53
v_fma_f32 v55,s69,v54,v53
v_rsq_f32 v65,v54
v_rsq_f32 v54,v55
v_mul_f32_e64 v64,-v52,v65
v_mul_f32 v63,v55,v54
v_mul_f32 v61,s76,v54
v_mul_f32 v55,v54,v54
v_mul_f32 v62,s77,v55
v_mul_f32 v62,v62,v61
v_cndmask_b32 v60,v52,v64,s[74:75]
v_cndmask_b32 v61,v61,v65,s[74:75]
buffer_store_dwordx4 v[60:63],v4,s[36:39],s59 offen offset:0 sc0 sc1
s_mov_b32 s63,s60
s_lshr_b32 s78,s45,1
s_and_b32 s79,s45,1
s_lshl_b32 s79,s79,7
s_lshl_b32 s57,s78,15
s_add_u32 s57,s57,s79
s_add_u32 s57,s57,12582912
s_add_u32 s58,s57,4096
s_lshl_b32 s59,s78,9
s_add_u32 s59,s59,s79
s_add_u32 s59,s59,107020288
s_lshl_b32 s60,s45,9
s_add_u32 s60,s60,s66
s_add_u32 s60,s60,131072
s_add_u32 s45,s45,s46
s_lshr_b32 s78,s45,1
)"
R"(s_and_b32 s79,s45,1
s_lshl_b32 s79,s79,7
s_lshl_b32 s55,s78,15
s_add_u32 s55,s55,s79
s_mul_i32 s56,s45,16384
s_add_u32 s56,s56,98566144
s_lshl_b32 s61,s78,11
s_add_u32 s61,s61,110587904
s_add_u32 s78,s45,s46
s_lshr_b32 s78,s78,1
s_lshl_b32 s62,s78,11
s_add_u32 s62,s62,110587904
s_waitcnt vmcnt(6)
s_branch Lr_it0_%=
Lr_tb1_%=:
v_add_f32 v53,s69,v53
v_rsq_f32 v48,v53
s_nop 0
v_mul_f32_e64 v50,-v52,v48
v_mov_b32 v49,v48
v_mov_b32 v51,v50
v_pk_fma_f32 v[52:53],v[116:117],v[48:49],v[50:51]
v_pk_fma_f32 v[54:55],v[118:119],v[48:49],v[50:51]
v_pk_fma_f32 v[56:57],v[120:121],v[48:49],v[50:51]
v_pk_fma_f32 v[58:59],v[122:123],v[48:49],v[50:51]
s_add_u32 s78,s55,71303168
buffer_load_dwordx4 v[164:167],v1,s[36:39],s78 offen offset:0 sc1
buffer_load_dwordx4 v[168:171],v1,s[36:39],s78 offen offset:2048 sc1
s_add_u32 s78,s55,71307264
buffer_load_dwordx4 v[172:175],v1,s[36:39],s78 offen offset:0 sc1
buffer_load_dwordx4 v[176:179],v1,s[36:39],s78 offen offset:2048 sc1
v_pk_fma_f32 v[116:117],v[52:53],v[84:85],v[116:117]
v_pk_fma_f32 v[118:119],v[54:55],v[86:87],v[118:119]
v_pk_fma_f32 v[120:121],v[56:57],v[88:89],v[120:121]
v_pk_fma_f32 v[122:123],v[58:59],v[90:91],v[122:123]
v_cvt_pk_bf16_f32 v148,v116,v117
v_cvt_pk_bf16_f32 v149,v118,v119
v_cvt_pk_bf16_f32 v150,v120,v121
v_cvt_pk_bf16_f32 v151,v122,v123
v_pk_fma_f32 v[52:53],v[124:125],v[48:49],v[50:51]
v_pk_fma_f32 v[54:55],v[126:127],v[48:49],v[50:51]
v_pk_fma_f32 v[56:57],v[128:129],v[48:49],v[50:51]
v_pk_fma_f32 v[58:59],v[130:131],v[48:49],v[50:51]
s_add_u32 s78,s55,89128960
buffer_load_dwordx4 v[180:183],v1,s[36:39],s78 offen offset:0 sc1
buffer_load_dwordx4 v[184:187],v1,s[36:39],s78 offen offset:2048 sc1
s_add_u32 s78,s55,89133056
buffer_load_dwordx4 v[188:191],v1,s[36:39],s78 offen offset:0 sc1
buffer_load_dwordx4 v[192:195],v1,s[36:39],s78 offen offset:2048 sc1
v_pk_fma_f32 v[124:125],v[52:53],v[92:93],v[124:125]
v_pk_fma_f32 v[126:127],v[54:55],v[94:95],v[126:127]
v_pk_fma_f32 v[128:129],v[56:57],v[96:97],v[128:129]
v_pk_fma_f32 v[130:131],v[58:59],v[98:99],v[130:131]
v_cvt_pk_bf16_f32 v152,v124,v125
v_cvt_pk_bf16_f32 v153,v126,v127
v_cvt_pk_bf16_f32 v154,v128,v129
v_cvt_pk_bf16_f32 v155,v130,v131
v_pk_fma_f32 v[52:53],v[132:133],v[48:49],v[50:51]
v_pk_fma_f32 v[54:55],v[134:135],v[48:49],v[50:51]
v_pk_fma_f32 v[56:57],v[136:137],v[48:49],v[50:51]
v_pk_fma_f32 v[58:59],v[138:139],v[48:49],v[50:51]
v_pk_fma_f32 v[132:133],v[52:53],v[100:101],v[132:133]
v_pk_fma_f32 v[134:135],v[54:55],v[102:103],v[134:135]
v_pk_fma_f32 v[136:137],v[56:57],v[104:105],v[136:137]
v_pk_fma_f32 v[138:139],v[58:59],v[106:107],v[138:139]
v_cvt_pk_bf16_f32 v156,v132,v133
v_cvt_pk_bf16_f32 v157,v134,v135
v_cvt_pk_bf16_f32 v158,v136,v137
v_cvt_pk_bf16_f32 v159,v138,v139
v_pk_fma_f32 v[52:53],v[140:141],v[48:49],v[50:51]
v_pk_fma_f32 v[54:55],v[142:143],v[48:49],v[50:51]
v_pk_fma_f32 v[56:57],v[144:145],v[48:49],v[50:51]
v_pk_fma_f32 v[58:59],v[146:147],v[48:49],v[50:51]
v_pk_fma_f32 v[140:141],v[52:53],v[108:109],v[140:141]
v_pk_fma_f32 v[142:143],v[54:55],v[110:111],v[142:143]
v_pk_fma_f32 v[144:145],v[56:57],v[112:113],v[144:145]
v_pk_fma_f32 v[146:147],v[58:59],v[114:115],v[146:147]
v_cvt_pk_bf16_f32 v160,v140,v141
v_cvt_pk_bf16_f32 v161,v142,v143
v_cvt_pk_bf16_f32 v162,v144,v145
v_cvt_pk_bf16_f32 v163,v146,v147
v_mfma_f32_16x16x32_bf16 v[36:39],v[32:35],v[148:151],0
buffer_load_dword v18,v6,s[36:39],s62 offen offset:0 sc1
buffer_store_dwordx4 v[148:151],v1,s[36:39],s57 offen offset:0 sc0 sc1
v_mfma_f32_16x16x32_bf16 v[40:43],v[148:151],v[148:151],0
M 36,v[32:35],152
buffer_store_dwordx4 v[152:155],v1,s[36:39],s57 offen offset:2048 sc0 sc1
M 40,v[152:155],152
M 36,v[32:35],156
buffer_store_dwordx4 v[156:159],v1,s[36:39],s58 offen offset:0 sc0 sc1
M 40,v[156:159],156
M 36,v[32:35],160
buffer_store_dwordx4 v[160:163],v1,s[36:39],s58 offen offset:2048 sc0 sc1
M 40,v[160:163],160
s_nop 5
v_add_f32_dpp v44,v36,v36 row_ror:8 row_mask:0xf bank_mask:0xf
s_nop 0
v_cndmask_b32 v45,v40,v41,s[70:71]
v_cndmask_b32 v46,v42,v43,s[70:71]
ds_write_b32 v2,v44 offset:1024
v_cndmask_b32 v45,v45,v46,s[72:73]
ds_write_b32 v0,v45 offset:1024
L 0
s_barrier
R 52,3,1024
R 56,3,1040
R 60,3,1056
L 0
v_pk_add_f32 v[52:53],v[52:53],v[54:55]
v_pk_add_f32 v[56:57],v[56:57],v[58:59]
v_pk_add_f32 v[60:61],v[60:61],v[62:63]
v_add_f32 v52,v52,v53
v_pk_add_f32 v[56:57],v[56:57],v[60:61]
v_mul_f32 v52,s68,v52
v_add_f32 v53,v56,v57
v_mul_f32 v53,s68,v53
v_fma_f32 v53,-v52,v52,v53
v_max_f32 v53,0,v53
v_add_f32 v54,s69,v53
v_fma_f32 v55,s69,v54,v53
v_rsq_f32 v65,v54
v_rsq_f32 v54,v55
v_mul_f32_e64 v64,-v52,v65
v_mul_f32 v63,v55,v54
v_mul_f32 v61,s76,v54
v_mul_f32 v55,v54,v54
v_mul_f32 v62,s77,v55
v_mul_f32 v62,v62,v61
v_cndmask_b32 v60,v52,v64,s[74:75]
v_cndmask_b32 v61,v61,v65,s[74:75]
buffer_store_dwordx4 v[60:63],v4,s[36:39],s59 offen offset:0 sc0 sc1
s_mov_b32 s63,s60
s_lshr_b32 s78,s45,1
s_and_b32 s79,s45,1
s_lshl_b32 s79,s79,7
s_lshl_b32 s57,s78,15
s_add_u32 s57,s57,s79
s_add_u32 s57,s57,12582912
s_add_u32 s58,s57,4096
s_lshl_b32 s59,s78,9
s_add_u32 s59,s59,s79
s_add_u32 s59,s59,107020288
s_lshl_b32 s60,s45,9
s_add_u32 s60,s60,s66
s_add_u32 s60,s60,131072
s_add_u32 s45,s45,s46
s_lshr_b32 s78,s45,1
s_and_b32 s79,s45,1
s_lshl_b32 s79,s79,7
s_lshl_b32 s55,s78,15
s_add_u32 s55,s55,s79
s_mul_i32 s56,s45,16384
s_add_u32 s56,s56,98566144
s_lshl_b32 s61,s78,11
s_add_u32 s61,s61,110587904
s_add_u32 s78,s45,s46
s_lshr_b32 s78,s78,1
s_lshl_b32 s62,s78,11
s_add_u32 s62,s62,110587904
s_waitcnt vmcnt(5)
s_branch Lr_it0_%=
Lr_tc1_%=:
s_add_u32 s78,s55,71303168
buffer_load_dwordx4 v[164:167],v1,s[36:39],s78 offen offset:0 sc1
buffer_load_dwordx4 v[168:171],v1,s[36:39],s78 offen offset:2048 sc1
s_add_u32 s78,s55,71307264
buffer_load_dwordx4 v[172:175],v1,s[36:39],s78 offen offset:0 sc1
buffer_load_dwordx4 v[176:179],v1,s[36:39],s78 offen offset:2048 sc1
s_add_u32 s78,s55,89128960
buffer_load_dwordx4 v[180:183],v1,s[36:39],s78 offen offset:0 sc1
buffer_load_dwordx4 v[184:187],v1,s[36:39],s78 offen offset:2048 sc1
s_add_u32 s78,s55,89133056
buffer_load_dwordx4 v[188:191],v1,s[36:39],s78 offen offset:0 sc1
buffer_load_dwordx4 v[192:195],v1,s[36:39],s78 offen offset:2048 sc1
buffer_load_dword v18,v6,s[36:39],s62 offen offset:0 sc1
s_waitcnt vmcnt(9)
buffer_store_dword v17,v5,s[36:39],s63 offen offset:0 sc0 sc1
s_mov_b32 s63,0x7fff0000
s_lshr_b32 s78,s45,1
s_and_b32 s79,s45,1
s_lshl_b32 s79,s79,7
s_lshl_b32 s57,s78,15
s_add_u32 s57,s57,s79
s_add_u32 s57,s57,12582912
s_add_u32 s58,s57,4096
s_lshl_b32 s59,s78,9
s_add_u32 s59,s59,s79
s_add_u32 s59,s59,107020288
s_lshl_b32 s60,s45,9
s_add_u32 s60,s60,s66
s_add_u32 s60,s60,131072
s_add_u32 s45,s45,s46
s_lshr_b32 s78,s45,1
s_and_b32 s79,s45,1
s_lshl_b32 s79,s79,7
s_lshl_b32 s55,s78,15
s_add_u32 s55,s55,s79
s_mul_i32 s56,s45,16384
s_add_u32 s56,s56,98566144
s_lshl_b32 s61,s78,11
s_add_u32 s61,s61,110587904
s_add_u32 s78,s45,s46
s_lshr_b32 s78,s78,1
s_lshl_b32 s62,s78,11
s_add_u32 s62,s62,110587904
s_waitcnt vmcnt(1)
s_branch Lr_it0_%=
Lr_done_%=:
s_waitcnt vmcnt(0) lgkmcnt(0)
s_barrier
Lcode_r_e:
.purgem M
.purgem R
.purgem T
.purgem D
.purgem L
)"
  :: "s"(ws), "s"(__builtin_amdgcn_readfirstlane(sWave)), "s"(__builtin_amdgcn_readfirstlane(sG0)), "s"(__builtin_amdgcn_readfirstlane(sStep)), "s"(__builtin_amdgcn_readfirstlane(sWw)), "s"(__builtin_amdgcn_readfirstlane(sExp)), "v"(vRP), "v"(vRF), "v"(vRwr), "v"(vRrd), "v"(vRS), "v"(vFlag), "v"(vL4), "v"(vLY), "v"(vQ), "v"(vY0), "v"(vTr), "v"(vSa), "v"(vSb), "v"(vWd0), "v"(vWd1), "v"(vWr0), "v"(vWr1), "s"(q), "s"(w4) : "v0", "v1", "v2", "v3", "v4", "v5", "v6", "v7", "v8", "v9", "v10", "v11", "v12", "v13", "v14", "v15", "v16", "v17", "v18", "v19", "v20", "v21", "v22", "v23", "v24", "v25", "v26", "v27", "v28", "v29", "v30", "v31", "v32", "v33", "v34", "v35", "v36", "v37", "v38", "v39", "v40", "v41", "v42", "v43", "v44", "v45", "v46", "v47", "v48", "v49", "v50", "v51", "v52", "v53", "v54", "v55", "v56", "v57", "v58", "v59", "v60", "v61", "v62", "v63", "v64", "v65", "v66", "v67", "v68", "v69", "v70", "v71", "v72", "v73", "v74", "v75", "v76", "v77", "v78", "v79", "v80", "v81", "v82", "v83", "v84", "v85", "v86", "v87", "v88", "v89", "v90", "v91", "v92", "v93", "v94", "v95", "v96", "v97", "v98", "v99", "v100", "v101", "v102", "v103", "v104", "v105", "v106", "v107", "v108", "v109", "v110", "v111", "v112", "v113", "v114", "v115", "v116", "v117", "v118", "v119", "v120", "v121", "v122", "v123", "v124", "v125", "v126", "v127", "v128", "v129", "v130", "v131", "v132", "v133", "v134", "v135", "v136", "v137", "v138", "v139", "v140", "v141", "v142", "v143", "v144", "v145", "v146", "v147", "v148", "v149", "v150", "v151", "v152", "v153", "v154", "v155", "v156", "v157", "v158", "v159", "v160", "v161", "v162", "v163", "v164", "v165", "v166", "v167", "v168", "v169", "v170", "v171", "v172", "v173", "v174", "v175", "v176", "v177", "v178", "v179", "v180", "v181", "v182", "v183", "v184", "v185", "v186", "v187", "v188", "v189", "v190", "v191", "v192", "v193", "v194", "v195", "v196", "v197", "v198", "v199", "v200", "v201", "v202", "v203", "v204", "v205", "v206", "v207", "v208", "v209", "v210", "v211", "v212", "v213", "v214", "v215", "v216", "v217", "v218", "v219", "v220", "v221", "v222", "v223", "v224", "v225", "v226", "v227", "v228", "v229", "v230", "v231", "a0", "a1", "a2", "a3", "a4", "a5", "a6", "a7", "a8", "a9", "a10", "a11", "a12", "a13", "a14", "a15", "a16", "a17", "a18", "a19", "a20", "a21", "a22", "a23", "a24", "a25", "a26", "a27", "a28", "a29", "a30", "a31", "a32", "a33", "a34", "a35", "a36", "a37", "a38", "a39", "a40", "a41", "a42", "a43", "a44", "a45", "a46", "a47", "a48", "a49", "a50", "a51", "a52", "a53", "a54", "a55", "a56", "a57", "a58", "a59", "a60", "a61", "a62", "a63", "a64", "a65", "a66", "a67", "a68", "a69", "a70", "a71", "a72", "a73", "a74", "a75", "a76", "a77", "a78", "a79", "a80", "a81", "a82", "a83", "a84", "a85", "a86", "a87", "a88", "a89", "a90", "a91", "a92", "a93", "a94", "a95", "a96", "a97", "a98", "a99", "a100", "a101", "a102", "a103", "a104", "a105", "a106", "a107", "a108", "a109", "a110", "a111", "a112", "a113", "a114", "a115", "a116", "a117", "a118", "a119", "a120", "a121", "a122", "a123", "a124", "a125", "a126", "a127", "a128", "a129", "a130", "a131", "a132", "a133", "a134", "a135", "a136", "a137", "a138", "a139", "a140", "a141", "a142", "a143", "a144", "a145", "a146", "a147", "a148", "a149", "a150", "a151", "a152", "a153", "a154", "a155", "a156", "a157", "a158", "a159", "a160", "a161", "a162", "a163", "a164", "a165", "a166", "a167", "a168", "a169", "a170", "a171", "a172", "a173", "a174", "a175", "a176", "a177", "a178", "a179", "a180", "a181", "a182", "a183", "a184", "a185", "a186", "a187", "a188", "a189", "a190", "a191", "a192", "a193", "a194", "a195", "a196", "a197", "a198", "a199", "a200", "a201", "a202", "a203", "a204", "a205", "a206", "a207", "a208", "a209", "a210", "a211", "a212", "a213", "a214", "a215", "a216", "a217", "a218", "a219", "a220", "a221", "a222", "a223", "a224", "a225", "a226", "a227", "a228", "a229", "a230", "a231", "a232", "a233", "a234", "a235", "a236", "a237", "a238", "a239", "a240", "a241", "a242", "a243", "a244", "a245", "a246", "a247", "a248", "a249", "a250", "a251", "a252", "a253", "a254", "a255", "s36", "s37", "s38", "s39", "s40", "s41", "s42", "s43", "s44", "s45", "s46", "s47", "s48", "s49", "s50", "s51", "s52", "s53", "s54", "s55", "s56", "s57", "s58", "s59", "s60", "s61", "s62", "s63", "s64", "s65", "s66", "s67", "s68", "s69", "s70", "s71", "s72", "s73", "s74", "s75", "s76", "s77", "s78", "s79", "s80", "s81", "s82", "s83", "s84", "s85", "s86", "s87", "s88", "s89", "s90", "s91", "s92", "s93", "s94", "s95", "s96", "s97", "vcc", "scc", "memory");
    if (RHELP && slice < 16) { kind = 4; rep = 5; kh = slice >> 3; slice &= 7; }
  }
  CTS(9);
#define COMMON(NF, FOFF, FSTR, SNAP, LDW, NL, TOFF)                                                                                            \
  LANE_ID(ln);                                                                                                                       \
  const int l15 = ln & 15, lg = ln >> 4, lq = l15 >> 2, lp = ln & 3, rr = 8 * lg + lq, fj = ln & 31;                                 \
  const unsigned vL16 = ln * 16, vFlag = (ln < 32 && wave == 0) ? ln * 4 : OOB;                                                      \
  const unsigned vSnapOff = (wave == 0 && fj < (NF)) ? (FOFF) + (ln >> 5) * sStep * (FSTR) : OOB;                                    \
  const unsigned vSnapRd = (SNAP) + (fj < (NF) ? ln : ln & 32) * 4;                                                                  \
  const unsigned vWd0 = (ln >> 3) * (LDW) + ((ln & 7) ^ swzB(ln >> 3)) * 16, vWd1 = ((ln >> 3) + 8) * (LDW) + ((ln & 7) ^ swzB(8 + (ln >> 3))) * 16; \
  const unsigned vWr0 = (wave >> 1) * 8192 + rr * 128 + (((wave & 1) * 4 + lp) ^ swzB(rr)) * 16, vWr1 = vWr0 + 8;                    \
  const unsigned vWr2 = 16384 + rr * 128 + ((2 * wave + (lp >> 1)) ^ swzB(rr)) * 16 + (lp & 1) * 8;                                  \
  const unsigned sWave = wave, sRk = kind == 4 ? rep : t_start, sG0 = rep < 5 ? rep : rep == 5 ? U0 + 5 : U1 + rep, sDbg = sDbg0 + (rep < 5 ? 0 : 2048)
  if (kind == 3 || kind == 5) {
    const unsigned sStep = kind == 3 ? 5 : G3OR;
    COMMON(8, fj * 128, 1024, 131072, 7168, 256, 0);
    const unsigned vOutS = (lg & 1) * 8 + (((wave & 1) * 2 + (lg >> 1)) * 16 + l15) * 16 + (4 + (wave >> 1) - wave) * 1024;
    const unsigned vAux0 = (ln & 7) * 128 + (4 * wave + lg) * 8 + ((ln >> 3) & 1) * 4;
    const float k1 = -2.0f * 0.7978845608028654f * 1.4426950408889634f;
    const unsigned vAux1 = 139264 + (l15 < 4 ? (4 * wave + lg) * 16 + l15 * 4 : 512 + ln * 4), vAux2 = 139264 + l15 * 16;
    const unsigned vAux3 = l15 == 2 ? __float_as_uint(k1 * 0.044715f) : 0u, vAn = l15 == 3 ? 0x3f800000u : 0u, vAe = l15 == 1 ? __float_as_uint(k1) : 0u;
    const unsigned sOutOff = OFF_G + (slice * 6 + wave) * 1024, sFoutOff = OFF_FG + slice * 128, sAuxOff = 0, sWw = 16 * wave * 7168 + slice * 384, sBuf = 0;
    const unsigned sExp = __builtin_amdgcn_readfirstlane(ep);
    if (kind == 3) { asm volatile(
R"(.macro M d,w,x
v_mfma_f32_16x16x32_bf16 v[\d:\d+3], \w, v[\x:\x+3], v[\d:\d+3]
.endm
.macro R d,a,o
ds_read_b128 v[\d:\d+3], v\a offset:\o
.endm
.macro T d,a,o
ds_read_b64_tr_b16 \d, v\a offset:\o
.endm
.macro D a,s,o
buffer_load_dwordx4 v\a, s[36:39], s\s offen offset:\o sc1 lds
.endm
.macro L c
s_waitcnt lgkmcnt(\c)
.endm
Lcode_g3_b:
s_mov_b64 s[36:37],%0
s_mov_b32 s38,111243392
s_mov_b32 s39,0x00020000
s_mov_b32 s44,%1
s_mov_b32 s45,%2
s_mov_b32 s46,%3
s_mov_b32 s47,%4
s_mov_b32 s48,%5
s_mov_b32 s49,%6
s_mov_b32 s50,%7
s_mov_b32 s51,%8
s_mov_b32 s53,%9
v_mov_b32 v0,%10
v_mov_b32 v3,%11
v_mov_b32 v5,%12
v_mov_b32 v6,%13
v_mov_b32 v18,%14
v_mov_b32 v19,%15
v_mov_b32 v20,%16
v_mov_b32 v21,%17
v_mov_b32 v22,%18
v_mov_b32 v10,%19
v_mov_b32 v11,%20
v_mov_b32 v12,%21
v_mov_b32 v13,%22
v_mov_b32 v14,%23
v_mov_b32 v15,%24
v_mov_b32 v16,%25
s_mov_b64 s[40:41],%26
s_mov_b32 s42,7340032
s_mov_b32 s43,0x00020000
s_lshl_b32 s57,s44,11
s_cmp_ge_u32 s45,8
s_cbranch_scc1 Lg3_dly_%=
s_sleep 16
s_sleep 16
s_sleep 16
Lg3_dly_%=:
s_add_u32 s55,s50,0
s_add_u32 m0,s57,0
s_nop 0
buffer_load_dwordx4 v18,s[40:43],s55 offen offset:0  lds
s_add_u32 m0,s57,1024
s_nop 0
buffer_load_dwordx4 v19,s[40:43],s55 offen offset:0  lds
s_add_u32 m0,s57,8064
s_nop 0
buffer_load_dwordx4 v18,s[40:43],s55 offen offset:128  lds
s_add_u32 m0,s57,9088
s_nop 0
buffer_load_dwordx4 v19,s[40:43],s55 offen offset:128  lds
s_add_u32 m0,s57,16128
s_nop 0
buffer_load_dwordx4 v18,s[40:43],s55 offen offset:256  lds
s_add_u32 m0,s57,17152
s_nop 0
buffer_load_dwordx4 v19,s[40:43],s55 offen offset:256  lds
s_add_u32 s55,s50,458752
s_add_u32 m0,s57,24576
s_nop 0
buffer_load_dwordx4 v18,s[40:43],s55 offen offset:0  lds
s_add_u32 m0,s57,25600
s_nop 0
buffer_load_dwordx4 v19,s[40:43],s55 offen offset:0  lds
s_add_u32 m0,s57,32640
s_nop 0
buffer_load_dwordx4 v18,s[40:43],s55 offen offset:128  lds
s_add_u32 m0,s57,33664
s_nop 0
buffer_load_dwordx4 v19,s[40:43],s55 offen offset:128  lds
s_add_u32 m0,s57,40704
s_nop 0
buffer_load_dwordx4 v18,s[40:43],s55 offen offset:256  lds
s_add_u32 m0,s57,41728
s_nop 0
buffer_load_dwordx4 v19,s[40:43],s55 offen offset:256  lds
s_add_u32 s55,s50,917504
s_add_u32 m0,s57,49152
s_nop 0
buffer_load_dwordx4 v18,s[40:43],s55 offen offset:0  lds
s_add_u32 m0,s57,50176
s_nop 0
buffer_load_dwordx4 v19,s[40:43],s55 offen offset:0  lds
s_add_u32 m0,s57,57216
s_nop 0
buffer_load_dwordx4 v18,s[40:43],s55 offen offset:128  lds
s_add_u32 m0,s57,58240
s_nop 0
buffer_load_dwordx4 v19,s[40:43],s55 offen offset:128  lds
s_add_u32 m0,s57,65280
s_nop 0
buffer_load_dwordx4 v18,s[40:43],s55 offen offset:256  lds
s_add_u32 m0,s57,66304
s_nop 0
buffer_load_dwordx4 v19,s[40:43],s55 offen offset:256  lds
s_add_u32 s55,s50,1376256
s_add_u32 m0,s57,73728
s_nop 0
buffer_load_dwordx4 v18,s[40:43],s55 offen offset:0  lds
s_add_u32 m0,s57,74752
s_nop 0
buffer_load_dwordx4 v19,s[40:43],s55 offen offset:0  lds
s_add_u32 m0,s57,81792
s_nop 0
buffer_load_dwordx4 v18,s[40:43],s55 offen offset:128  lds
s_add_u32 m0,s57,82816
s_nop 0
buffer_load_dwordx4 v19,s[40:43],s55 offen offset:128  lds
s_add_u32 m0,s57,89856
s_nop 0
buffer_load_dwordx4 v18,s[40:43],s55 offen offset:256  lds
s_add_u32 m0,s57,90880
s_nop 0
buffer_load_dwordx4 v19,s[40:43],s55 offen offset:256  lds
s_add_u32 s55,s50,1835008
s_add_u32 m0,s57,98304
s_nop 0
buffer_load_dwordx4 v18,s[40:43],s55 offen offset:0  lds
s_add_u32 m0,s57,99328
s_nop 0
buffer_load_dwordx4 v19,s[40:43],s55 offen offset:0  lds
s_add_u32 m0,s57,106368
s_nop 0
buffer_load_dwordx4 v18,s[40:43],s55 offen offset:128  lds
s_add_u32 m0,s57,107392
s_nop 0
buffer_load_dwordx4 v19,s[40:43],s55 offen offset:128  lds
s_add_u32 m0,s57,114432
s_nop 0
buffer_load_dwordx4 v18,s[40:43],s55 offen offset:256  lds
s_add_u32 m0,s57,115456
s_nop 0
buffer_load_dwordx4 v19,s[40:43],s55 offen offset:256  lds
s_waitcnt vmcnt(24) lgkmcnt(0)
s_barrier
s_add_u32 s55,s50,2293760
s_add_u32 m0,s57,122880
s_nop 0
buffer_load_dwordx4 v18,s[40:43],s55 offen offset:0  lds
s_add_u32 m0,s57,123904
s_nop 0
buffer_load_dwordx4 v19,s[40:43],s55 offen offset:0  lds
s_add_u32 m0,s57,130944
s_nop 0
buffer_load_dwordx4 v18,s[40:43],s55 offen offset:128  lds
s_add_u32 m0,s57,131968
s_nop 0
buffer_load_dwordx4 v19,s[40:43],s55 offen offset:128  lds
s_add_u32 m0,s57,139008
s_nop 0
buffer_load_dwordx4 v18,s[40:43],s55 offen offset:256  lds
s_add_u32 m0,s57,140032
s_nop 0
buffer_load_dwordx4 v19,s[40:43],s55 offen offset:256  lds
v_add_u32 v26,0,v20
v_add_u32 v27,0,v21
v_add_u32 v28,0,v22
T a[0:1],26,0
T a[2:3],26,512
T a[4:5],27,0
T a[6:7],27,512
T a[8:9],28,0
T a[10:11],28,512
T a[12:13],26,4096
T a[14:15],26,4608
T a[16:17],27,4096
T a[18:19],27,4608
T a[20:21],28,4096
T a[22:23],28,4608
s_waitcnt vmcnt(24) lgkmcnt(0)
s_barrier
s_add_u32 s55,s50,2752512
s_add_u32 m0,s57,0
s_nop 0
buffer_load_dwordx4 v18,s[40:43],s55 offen offset:0  lds
s_add_u32 m0,s57,1024
s_nop 0
buffer_load_dwordx4 v19,s[40:43],s55 offen offset:0  lds
s_add_u32 m0,s57,8064
s_nop 0
buffer_load_dwordx4 v18,s[40:43],s55 offen offset:128  lds
s_add_u32 m0,s57,9088
s_nop 0
buffer_load_dwordx4 v19,s[40:43],s55 offen offset:128  lds
s_add_u32 m0,s57,16128
s_nop 0
buffer_load_dwordx4 v18,s[40:43],s55 offen offset:256  lds
s_add_u32 m0,s57,17152
s_nop 0
buffer_load_dwordx4 v19,s[40:43],s55 offen offset:256  lds
v_add_u32 v26,24576,v20
v_add_u32 v27,24576,v21
v_add_u32 v28,24576,v22
T a[24:25],26,0
T a[26:27],26,512
T a[28:29],27,0
T a[30:31],27,512
T a[32:33],28,0
T a[34:35],28,512
T a[36:37],26,4096
T a[38:39],26,4608
T a[40:41],27,4096
T a[42:43],27,4608
T a[44:45],28,4096
T a[46:47],28,4608
s_waitcnt vmcnt(24) lgkmcnt(0)
s_barrier
s_add_u32 s55,s50,3211264
s_add_u32 m0,s57,24576
s_nop 0
buffer_load_dwordx4 v18,s[40:43],s55 offen offset:0  lds
s_add_u32 m0,s57,25600
s_nop 0
buffer_load_dwordx4 v19,s[40:43],s55 offen offset:0  lds
s_add_u32 m0,s57,32640
s_nop 0
buffer_load_dwordx4 v18,s[40:43],s55 offen offset:128  lds
s_add_u32 m0,s57,33664
s_nop 0
buffer_load_dwordx4 v19,s[40:43],s55 offen offset:128  lds
s_add_u32 m0,s57,40704
s_nop 0
buffer_load_dwordx4 v18,s[40:43],s55 offen offset:256  lds
s_add_u32 m0,s57,41728
s_nop 0
buffer_load_dwordx4 v19,s[40:43],s55 offen offset:256  lds
v_add_u32 v26,49152,v20
v_add_u32 v27,49152,v21
v_add_u32 v28,49152,v22
T a[48:49],26,0
T a[50:51],26,512
T a[52:53],27,0
T a[54:55],27,512
T a[56:57],28,0
T a[58:59],28,512
T a[60:61],26,4096
T a[62:63],26,4608
T a[64:65],27,4096
T a[66:67],27,4608
T a[68:69],28,4096
T a[70:71],28,4608
s_waitcnt vmcnt(24) lgkmcnt(0)
s_barrier
s_add_u32 s55,s50,3670016
s_add_u32 m0,s57,49152
s_nop 0
buffer_load_dwordx4 v18,s[40:43],s55 offen offset:0  lds
s_add_u32 m0,s57,50176
s_nop 0
buffer_load_dwordx4 v19,s[40:43],s55 offen offset:0  lds
s_add_u32 m0,s57,57216
s_nop 0
buffer_load_dwordx4 v18,s[40:43],s55 offen offset:128  lds
s_add_u32 m0,s57,58240
s_nop 0
buffer_load_dwordx4 v19,s[40:43],s55 offen offset:128  lds
s_add_u32 m0,s57,65280
s_nop 0
buffer_load_dwordx4 v18,s[40:43],s55 offen offset:256  lds
s_add_u32 m0,s57,66304
s_nop 0
buffer_load_dwordx4 v19,s[40:43],s55 offen offset:256  lds
v_add_u32 v26,73728,v20
v_add_u32 v27,73728,v21
v_add_u32 v28,73728,v22
T a[72:73],26,0
T a[74:75],26,512
T a[76:77],27,0
T a[78:79],27,512
T a[80:81],28,0
T a[82:83],28,512
T a[84:85],26,4096
T a[86:87],26,4608
T a[88:89],27,4096
T a[90:91],27,4608
T a[92:93],28,4096
T a[94:95],28,4608
s_waitcnt vmcnt(24) lgkmcnt(0)
s_barrier
s_add_u32 s55,s50,4128768
s_add_u32 m0,s57,73728
s_nop 0
buffer_load_dwordx4 v18,s[40:43],s55 offen offset:0  lds
s_add_u32 m0,s57,74752
s_nop 0
buffer_load_dwordx4 v19,s[40:43],s55 offen offset:0  lds
s_add_u32 m0,s57,81792
s_nop 0
buffer_load_dwordx4 v18,s[40:43],s55 offen offset:128  lds
s_add_u32 m0,s57,82816
s_nop 0
buffer_load_dwordx4 v19,s[40:43],s55 offen offset:128  lds
s_add_u32 m0,s57,89856
s_nop 0
buffer_load_dwordx4 v18,s[40:43],s55 offen offset:256  lds
s_add_u32 m0,s57,90880
s_nop 0
buffer_load_dwordx4 v19,s[40:43],s55 offen offset:256  lds
v_add_u32 v26,98304,v20
v_add_u32 v27,98304,v21
v_add_u32 v28,98304,v22
T a[96:97],26,0
T a[98:99],26,512
T a[100:101],27,0
T a[102:103],27,512
T a[104:105],28,0
T a[106:107],28,512
T a[108:109],26,4096
T a[110:111],26,4608
T a[112:113],27,4096
T a[114:115],27,4608
T a[116:117],28,4096
T a[118:119],28,4608
s_waitcnt vmcnt(24) lgkmcnt(0)
s_barrier
s_add_u32 s55,s50,4587520
s_add_u32 m0,s57,98304
s_nop 0
buffer_load_dwordx4 v18,s[40:43],s55 offen offset:0  lds
s_add_u32 m0,s57,99328
s_nop 0
buffer_load_dwordx4 v19,s[40:43],s55 offen offset:0  lds
s_add_u32 m0,s57,106368
s_nop 0
buffer_load_dwordx4 v18,s[40:43],s55 offen offset:128  lds
s_add_u32 m0,s57,107392
s_nop 0
buffer_load_dwordx4 v19,s[40:43],s55 offen offset:128  lds
s_add_u32 m0,s57,114432
s_nop 0
buffer_load_dwordx4 v18,s[40:43],s55 offen offset:256  lds
s_add_u32 m0,s57,115456
s_nop 0
buffer_load_dwordx4 v19,s[40:43],s55 offen offset:256  lds
v_add_u32 v26,122880,v20
v_add_u32 v27,122880,v21
v_add_u32 v28,122880,v22
T a[120:121],26,0
T a[122:123],26,512
T a[124:125],27,0
T a[126:127],27,512
T a[128:129],28,0
T a[130:131],28,512
T a[132:133],26,4096
T a[134:135],26,4608
T a[136:137],27,4096
T a[138:139],27,4608
T a[140:141],28,4096
T a[142:143],28,4608
s_waitcnt vmcnt(24) lgkmcnt(0)
s_barrier
s_add_u32 s55,s50,5046272
s_add_u32 m0,s57,122880
s_nop 0
buffer_load_dwordx4 v18,s[40:43],s55 offen offset:0  lds
s_add_u32 m0,s57,123904
s_nop 0
buffer_load_dwordx4 v19,s[40:43],s55 offen offset:0  lds
s_add_u32 m0,s57,130944
s_nop 0
buffer_load_dwordx4 v18,s[40:43],s55 offen offset:128  lds
s_add_u32 m0,s57,131968
s_nop 0
buffer_load_dwordx4 v19,s[40:43],s55 offen offset:128  lds
s_add_u32 m0,s57,139008
s_nop 0
buffer_load_dwordx4 v18,s[40:43],s55 offen offset:256  lds
s_add_u32 m0,s57,140032
s_nop 0
buffer_load_dwordx4 v19,s[40:43],s55 offen offset:256  lds
v_add_u32 v26,0,v20
v_add_u32 v27,0,v21
v_add_u32 v28,0,v22
T a[144:145],26,0
T a[146:147],26,512
T a[148:149],27,0
T a[150:151],27,512
T a[152:153],28,0
T a[154:155],28,512
T a[156:157],26,4096
T a[158:159],26,4608
T a[160:161],27,4096
T a[162:163],27,4608
T a[164:165],28,4096
T a[166:167],28,4608
s_waitcnt vmcnt(24) lgkmcnt(0)
s_barrier
s_add_u32 s55,s50,5505024
s_add_u32 m0,s57,0
s_nop 0
buffer_load_dwordx4 v18,s[40:43],s55 offen offset:0  lds
s_add_u32 m0,s57,1024
s_nop 0
buffer_load_dwordx4 v19,s[40:43],s55 offen offset:0  lds
s_add_u32 m0,s57,8064
s_nop 0
buffer_load_dwordx4 v18,s[40:43],s55 offen offset:128  lds
s_add_u32 m0,s57,9088
s_nop 0
buffer_load_dwordx4 v19,s[40:43],s55 offen offset:128  lds
s_add_u32 m0,s57,16128
s_nop 0
buffer_load_dwordx4 v18,s[40:43],s55 offen offset:256  lds
s_add_u32 m0,s57,17152
s_nop 0
buffer_load_dwordx4 v19,s[40:43],s55 offen offset:256  lds
v_add_u32 v26,24576,v20
v_add_u32 v27,24576,v21
v_add_u32 v28,24576,v22
T a[168:169],26,0
T a[170:171],26,512
T a[172:173],27,0
T a[174:175],27,512
T a[176:177],28,0
T a[178:179],28,512
T a[180:181],26,4096
T a[182:183],26,4608
T a[184:185],27,4096
T a[186:187],27,4608
T a[188:189],28,4096
T a[190:191],28,4608
s_waitcnt vmcnt(24) lgkmcnt(0)
s_barrier
s_add_u32 s55,s50,5963776
s_add_u32 m0,s57,24576
s_nop 0
buffer_load_dwordx4 v18,s[40:43],s55 offen offset:0  lds
s_add_u32 m0,s57,25600
s_nop 0
buffer_load_dwordx4 v19,s[40:43],s55 offen offset:0  lds
s_add_u32 m0,s57,32640
s_nop 0
buffer_load_dwordx4 v18,s[40:43],s55 offen offset:128  lds
s_add_u32 m0,s57,33664
s_nop 0
buffer_load_dwordx4 v19,s[40:43],s55 offen offset:128  lds
s_add_u32 m0,s57,40704
s_nop 0
buffer_load_dwordx4 v18,s[40:43],s55 offen offset:256  lds
s_add_u32 m0,s57,41728
s_nop 0
buffer_load_dwordx4 v19,s[40:43],s55 offen offset:256  lds
v_add_u32 v26,49152,v20
v_add_u32 v27,49152,v21
v_add_u32 v28,49152,v22
T a[192:193],26,0
T a[194:195],26,512
T a[196:197],27,0
T a[198:199],27,512
T a[200:201],28,0
T a[202:203],28,512
T a[204:205],26,4096
T a[206:207],26,4608
T a[208:209],27,4096
T a[210:211],27,4608
T a[212:213],28,4096
T a[214:215],28,4608
s_waitcnt vmcnt(24) lgkmcnt(0)
s_barrier
s_add_u32 s55,s50,6422528
s_add_u32 m0,s57,49152
s_nop 0
buffer_load_dwordx4 v18,s[40:43],s55 offen offset:0  lds
s_add_u32 m0,s57,50176
s_nop 0
buffer_load_dwordx4 v19,s[40:43],s55 offen offset:0  lds
s_add_u32 m0,s57,57216
s_nop 0
buffer_load_dwordx4 v18,s[40:43],s55 offen offset:128  lds
s_add_u32 m0,s57,58240
s_nop 0
buffer_load_dwordx4 v19,s[40:43],s55 offen offset:128  lds
s_add_u32 m0,s57,65280
s_nop 0
buffer_load_dwordx4 v18,s[40:43],s55 offen offset:256  lds
s_add_u32 m0,s57,66304
s_nop 0
buffer_load_dwordx4 v19,s[40:43],s55 offen offset:256  lds
v_add_u32 v26,73728,v20
v_add_u32 v27,73728,v21
v_add_u32 v28,73728,v22
T a[216:217],26,0
T a[218:219],26,512
T a[220:221],27,0
)"
R"(T a[222:223],27,512
T a[224:225],28,0
T a[226:227],28,512
T a[228:229],26,4096
T a[230:231],26,4608
T a[232:233],27,4096
T a[234:235],27,4608
T a[236:237],28,4096
T a[238:239],28,4608
s_waitcnt vmcnt(24) lgkmcnt(0)
s_barrier
s_add_u32 s55,s50,6881280
s_add_u32 m0,s57,73728
s_nop 0
buffer_load_dwordx4 v18,s[40:43],s55 offen offset:0  lds
s_add_u32 m0,s57,74752
s_nop 0
buffer_load_dwordx4 v19,s[40:43],s55 offen offset:0  lds
s_add_u32 m0,s57,81792
s_nop 0
buffer_load_dwordx4 v18,s[40:43],s55 offen offset:128  lds
s_add_u32 m0,s57,82816
s_nop 0
buffer_load_dwordx4 v19,s[40:43],s55 offen offset:128  lds
s_add_u32 m0,s57,89856
s_nop 0
buffer_load_dwordx4 v18,s[40:43],s55 offen offset:256  lds
s_add_u32 m0,s57,90880
s_nop 0
buffer_load_dwordx4 v19,s[40:43],s55 offen offset:256  lds
v_add_u32 v26,98304,v20
v_add_u32 v27,98304,v21
v_add_u32 v28,98304,v22
T a[240:241],26,0
T a[242:243],26,512
T a[244:245],27,0
T a[246:247],27,512
T a[248:249],28,0
T a[250:251],28,512
T a[252:253],26,4096
T a[254:255],26,4608
T v[128:129],27,4096
T v[130:131],27,4608
T v[132:133],28,4096
T v[134:135],28,4608
s_waitcnt vmcnt(24) lgkmcnt(0)
s_barrier
v_add_u32 v26,122880,v20
v_add_u32 v27,122880,v21
v_add_u32 v28,122880,v22
T v[136:137],26,0
T v[138:139],26,512
T v[140:141],27,0
T v[142:143],27,512
T v[144:145],28,0
T v[146:147],28,512
T v[148:149],26,4096
T v[150:151],26,4608
T v[152:153],27,4096
T v[154:155],27,4608
T v[156:157],28,4096
T v[158:159],28,4608
s_waitcnt vmcnt(18) lgkmcnt(0)
s_barrier
v_add_u32 v26,0,v20
v_add_u32 v27,0,v21
v_add_u32 v28,0,v22
T v[160:161],26,0
T v[162:163],26,512
T v[164:165],27,0
T v[166:167],27,512
T v[168:169],28,0
T v[170:171],28,512
T v[172:173],26,4096
T v[174:175],26,4608
T v[176:177],27,4096
T v[178:179],27,4608
T v[180:181],28,4096
T v[182:183],28,4608
s_waitcnt vmcnt(12) lgkmcnt(0)
s_barrier
v_add_u32 v26,24576,v20
v_add_u32 v27,24576,v21
v_add_u32 v28,24576,v22
T v[184:185],26,0
T v[186:187],26,512
T v[188:189],27,0
T v[190:191],27,512
T v[192:193],28,0
T v[194:195],28,512
T v[196:197],26,4096
T v[198:199],26,4608
T v[200:201],27,4096
T v[202:203],27,4608
T v[204:205],28,4096
T v[206:207],28,4608
s_waitcnt vmcnt(6) lgkmcnt(0)
s_barrier
v_add_u32 v26,49152,v20
v_add_u32 v27,49152,v21
v_add_u32 v28,49152,v22
T v[208:209],26,0
T v[210:211],26,512
T v[212:213],27,0
T v[214:215],27,512
T v[216:217],28,0
T v[218:219],28,512
T v[220:221],26,4096
T v[222:223],26,4608
T v[224:225],27,4096
T v[226:227],27,4608
T v[228:229],28,4096
T v[230:231],28,4608
s_waitcnt vmcnt(0) lgkmcnt(0)
s_barrier
v_add_u32 v26,73728,v20
v_add_u32 v27,73728,v21
v_add_u32 v28,73728,v22
T v[232:233],26,0
T v[234:235],26,512
T v[236:237],27,0
T v[238:239],27,512
T v[240:241],28,0
T v[242:243],28,512
T v[244:245],26,4096
T v[246:247],26,4608
T v[248:249],27,4096
T v[250:251],27,4608
T v[252:253],28,4096
T v[254:255],28,4608
L 0
s_barrier
v_mov_b32 v42,0x3f803f80
v_mov_b32 v43,0x3f803f80
v_mov_b32 v44,0x3f803f80
v_mov_b32 v45,0x3f803f80
s_nop 1
v_mfma_f32_16x16x32_bf16 v[82:85],a[0:3],v[42:45],0
v_mfma_f32_16x16x32_bf16 v[86:89],a[4:7],v[42:45],0
v_mfma_f32_16x16x32_bf16 v[90:93],a[8:11],v[42:45],0
M 82,a[12:15],42
M 86,a[16:19],42
M 90,a[20:23],42
M 82,a[24:27],42
M 86,a[28:31],42
M 90,a[32:35],42
M 82,a[36:39],42
M 86,a[40:43],42
M 90,a[44:47],42
M 82,a[48:51],42
M 86,a[52:55],42
M 90,a[56:59],42
M 82,a[60:63],42
M 86,a[64:67],42
M 90,a[68:71],42
M 82,a[72:75],42
M 86,a[76:79],42
M 90,a[80:83],42
M 82,a[84:87],42
M 86,a[88:91],42
M 90,a[92:95],42
M 82,a[96:99],42
M 86,a[100:103],42
M 90,a[104:107],42
M 82,a[108:111],42
M 86,a[112:115],42
M 90,a[116:119],42
M 82,a[120:123],42
M 86,a[124:127],42
M 90,a[128:131],42
M 82,a[132:135],42
M 86,a[136:139],42
M 90,a[140:143],42
M 82,a[144:147],42
M 86,a[148:151],42
M 90,a[152:155],42
M 82,a[156:159],42
M 86,a[160:163],42
M 90,a[164:167],42
M 82,a[168:171],42
M 86,a[172:175],42
M 90,a[176:179],42
M 82,a[180:183],42
M 86,a[184:187],42
M 90,a[188:191],42
M 82,a[192:195],42
M 86,a[196:199],42
M 90,a[200:203],42
M 82,a[204:207],42
M 86,a[208:211],42
M 90,a[212:215],42
M 82,a[216:219],42
M 86,a[220:223],42
M 90,a[224:227],42
M 82,a[228:231],42
M 86,a[232:235],42
M 90,a[236:239],42
M 82,a[240:243],42
M 86,a[244:247],42
M 90,a[248:251],42
M 82,a[252:255],42
M 86,v[128:131],42
M 90,v[132:135],42
M 82,v[136:139],42
M 86,v[140:143],42
M 90,v[144:147],42
M 82,v[148:151],42
M 86,v[152:155],42
M 90,v[156:159],42
M 82,v[160:163],42
M 86,v[164:167],42
M 90,v[168:171],42
M 82,v[172:175],42
M 86,v[176:179],42
M 90,v[180:183],42
M 82,v[184:187],42
M 86,v[188:191],42
M 90,v[192:195],42
M 82,v[196:199],42
M 86,v[200:203],42
M 90,v[204:207],42
M 82,v[208:211],42
M 86,v[212:215],42
M 90,v[216:219],42
M 82,v[220:223],42
M 86,v[224:227],42
M 90,v[228:231],42
M 82,v[232:235],42
M 86,v[236:239],42
M 90,v[240:243],42
M 82,v[244:247],42
M 86,v[248:251],42
M 90,v[252:255],42
v_mov_b32 v4,s53
s_mov_b32 s82,0
s_mov_b32 s80,0
s_mov_b32 s54,s45
s_mul_i32 s57,s44,4096
s_mul_i32 s55,s45,32768
s_add_u32 s55,s55,s57
s_add_u32 s55,s55,31457280
s_mul_i32 s57,s44,4096
s_mul_i32 s60,s46,32768
s_mul_i32 s61,s46,1024
s_mul_i32 s62,s46,114688
s_mul_i32 s63,s46,2560
s_mul_i32 s64,s46,1024
s_lshl_b32 s59,s44,8
s_add_u32 s59,s59,131072
s_mul_i32 s58,s45,1024
s_add_u32 s58,s58,109613056
s_mul_i32 s65,s45,114688
s_add_u32 s65,s65,s47
s_mul_i32 s68,s45,2560
s_add_u32 s68,s68,s48
s_mul_i32 s73,s45,1024
s_add_u32 s73,s73,107479040
s_mul_i32 s74,s45,32768
s_add_u32 s74,s74,s49
s_mul_i32 s75,s45,1024
s_add_u32 s75,s75,107479040
s_mov_b32 s66,0x7fff0000
s_mov_b32 s67,0x7fff0000
s_mov_b32 s69,0x7fff0000
s_mov_b32 s76,0x7fff0000
s_mov_b32 s77,0x7fff0000
s_mov_b32 s70,0x7fff0000
s_mov_b32 s71,0x7fff0000
s_mov_b32 s72,0x7fff0000
v_mov_b32 v1,v0
v_add_u32 v2,16384,v0
s_mov_b32 s84,16384
s_mov_b32 s86,0x3a800000
s_mov_b32 s87,0x3727c5ac
s_mov_b32 s88,0xc0135761
s_mov_b32 s89,0x3d372713
s_lshr_b32 s85,s44,1
s_mul_i32 s85,s85,127
v_lshlrev_b32 v8,22,v0
v_and_b32 v8,0x80000000,v8
v_or_b32 v8,v8,v5
v_add_u32 v2,32768,v0
s_lshl_b32 s84,s57,1
s_add_u32 s84,s84,32768
v_and_b32 v94,240,v0
v_cmp_eq_u32 vcc,0,v94
s_nop 1
v_cndmask_b32 v17,0,1.0,vcc
s_getpc_b64 s[40:41]
Lg3_pc_%=:
s_lshl_b32 s78,s44,13
v_lshlrev_b32 v94,3,v0
v_add_u32 v94,s78,v94
v_min_u32 v94,Lcode_g3_e-Lg3_pc_%=-4,v94
s_mov_b32 m0,163584
s_nop 0
global_load_lds_dword v94,s[40:41]
s_add_u32 m0,s59,4096
s_nop 0
buffer_load_dword v8,s[36:39],s58 offen offset:0 sc1 lds
buffer_store_dword v4,v3,s[36:39],s71 offen offset:0 sc0 sc1
s_add_u32 m0,s59,5120
s_nop 0
buffer_load_dword v8,s[36:39],s58 offen offset:0 sc1 lds
buffer_store_dword v4,v3,s[36:39],s71 offen offset:0 sc0 sc1
s_mov_b32 s83,0
Lg3_poll_%=:
s_add_u32 s78,s83,2
s_and_b32 s78,s78,3
s_lshl_b32 s78,s78,10
s_add_u32 s78,s78,4096
s_add_u32 m0,s59,s78
s_nop 0
buffer_load_dword v8,s[36:39],s58 offen offset:0 sc1 lds
s_and_b32 s78,s83,3
s_lshl_b32 s78,s78,10
v_add_u32 v94,s78,v6
s_waitcnt vmcnt(4)
s_barrier
ds_read_b32 v7,v94 offset:4096
buffer_store_dword v4,v3,s[36:39],s70 offen offset:0 sc0 sc1
s_mov_b32 s70,s71
s_mov_b32 s71,s72
s_mov_b32 s72,0x7fff0000
s_add_u32 s83,s83,1
s_add_u32 s82,s82,1
L 0
v_cmp_eq_u32 vcc,s53,v7
s_nop 0
s_cmp_eq_u32 vcc_lo,-1
s_cbranch_scc1 Lg3_go_%=
s_cmp_lt_u32 s82,20000
s_cbranch_scc1 Lg3_poll_%=
Lg3_go_%=:
buffer_load_dword v114,v11,s[36:39],s73 offen offset:0 sc1
s_add_u32 s73,s73,s64
s_add_u32 m0,s57,0
s_add_u32 s56,s55,0
D 0,56,0
D 0,56,1024
D 0,56,2048
D 0,56,3072
s_add_u32 s58,s58,s61
s_add_u32 m0,s59,2048
s_nop 0
buffer_load_dword v8,s[36:39],s58 offen offset:0 sc1 lds
s_add_u32 m0,s57,16384
s_add_u32 s56,s55,16384
D 0,56,0
D 0,56,1024
D 0,56,2048
D 0,56,3072
s_add_u32 s55,s55,s60
s_sub_u32 s57,s84,s57
s_add_u32 s54,s54,s46
s_waitcnt vmcnt(4)
s_barrier
buffer_store_dword v4,v3,s[36:39],s70 offen offset:0 sc0 sc1
s_min_u32 s71,s71,s72
buffer_store_dword v4,v3,s[36:39],s71 offen offset:0 sc0 sc1
s_mov_b32 s70,0x7fff0000
s_mov_b32 s71,0x7fff0000
s_mov_b32 s72,0x7fff0000
s_cmp_eq_u32 s80,1
buffer_store_dword v4,v3,s[36:39],s71 offen offset:0 sc0 sc1
s_cbranch_scc1 Lg3_st1_%=
s_branch Lg3_st0_%=
Lg3_gox_%=:
s_add_u32 m0,s59,2048
s_nop 0
buffer_load_dword v8,s[36:39],s58 offen offset:0 sc1 lds
s_waitcnt vmcnt(0)
s_barrier
buffer_store_dword v4,v3,s[36:39],s70 offen offset:0 sc0 sc1
s_min_u32 s71,s71,s72
buffer_store_dword v4,v3,s[36:39],s71 offen offset:0 sc0 sc1
s_mov_b32 s70,0x7fff0000
s_mov_b32 s71,0x7fff0000
s_mov_b32 s72,0x7fff0000
s_cmp_eq_u32 s80,1
s_cbranch_scc1 Lg3_st1_%=
Lg3_st0_%=:
ds_read_b32 v7,v6 offset:2048
R 42,1,0
R 46,1,1024
R 50,1,2048
R 54,1,3072
R 58,1,4096
L 5
v_cmp_eq_u32 vcc,s53,v7
L 3
s_cmp_eq_u32 vcc_lo,-1
s_cbranch_scc1 Lg3_F0_%=
s_branch Lg3_L0_%=
Lg3_st1_%=:
ds_read_b32 v7,v6 offset:2048
R 42,2,0
R 46,2,1024
R 50,2,2048
R 54,2,3072
R 58,2,4096
L 5
v_cmp_eq_u32 vcc,s53,v7
L 3
s_cmp_eq_u32 vcc_lo,-1
s_cbranch_scc1 Lg3_F1_%=
s_branch Lg3_L1_%=
Lg3_F0_%=:
v_mfma_f32_16x16x32_bf16 v[26:29],a[8:11],v[42:45],0
s_add_u32 m0,s57,0
R 62,1,5120
v_add_f32_dpp v115,v114,v114 quad_perm:[1,0,3,2] row_mask:0xf bank_mask:0xf
M 26,a[20:23],46
buffer_load_dword v114,v11,s[36:39],s73 offen offset:0 sc1
v_mfma_f32_16x16x32_bf16 v[22:25],a[4:7],v[42:45],0
s_add_u32 s56,s55,0
v_add_f32_dpp v115,v115,v115 quad_perm:[2,3,0,1] row_mask:0xf bank_mask:0xf
v_fma_f32 v30,-v78,v82,v30
M 22,a[16:19],46
s_add_u32 s58,s58,s61
R 66,1,6144
v_fma_f32 v31,-v78,v83,v31
v_mfma_f32_16x16x32_bf16 v[18:21],a[0:3],v[42:45],0
D 0,56,0
M 18,a[12:15],46
L 3
v_add_f32_dpp v118,v115,v115 row_half_mirror row_mask:0xf bank_mask:0xf
v_fma_f32 v32,-v78,v84,v32
M 18,a[24:27],50
s_mov_b32 s66,s65
R 70,1,7168
v_fma_f32 v33,-v78,v85,v33
M 18,a[36:39],54
s_add_u32 s65,s65,s62
v_mov_b32_dpp v115,v118 row_ror:8 row_mask:0xf bank_mask:0xf
v_fma_f32 v115,-v118,v118,v115
M 22,a[28:31],50
s_add_u32 s54,s54,s46
v_max_f32 v115,s87,v115
v_mul_f32 v118,v17,v118
M 22,a[40:43],54
s_add_u32 s73,s73,s64
R 42,1,8192
v_mul_f32 v94,v30,v30
M 26,a[32:35],50
v_rsq_f32 v116,v115
M 26,a[44:47],54
L 3
D 0,56,1024
M 26,a[56:59],58
R 46,1,9216
v_fma_f32 v115,v15,v115,v16
M 26,a[68:71],62
v_mul_f32 v119,v116,v116
v_fma_f32 v115,v14,v119,v115
M 22,a[52:55],58
v_fma_f32 v118,v116,v115,v118
v_mul_f32 v95,v31,v31
M 22,a[64:67],62
R 50,1,10240
v_mul_f32 v96,v32,v32
M 18,a[48:51],58
ds_write_b32 v12,v118 offset:0
M 18,a[60:63],62
L 4
v_mul_f32 v97,v33,v33
v_fma_f32 v94,v80,v94,v79
M 18,a[72:75],66
R 54,1,11264
v_fma_f32 v95,v80,v95,v79
M 18,a[84:87],70
v_fma_f32 v96,v80,v96,v79
v_fma_f32 v97,v80,v97,v79
M 22,a[76:79],66
v_mul_f32 v94,v30,v94
v_mul_f32 v95,v31,v95
M 22,a[88:91],70
R 58,1,12288
v_mul_f32 v96,v32,v96
M 26,a[80:83],66
D 0,56,2048
M 26,a[92:95],70
L 4
v_exp_f32 v94,v94
M 26,a[104:107],42
R 62,1,13312
v_mul_f32 v97,v33,v97
M 26,a[116:119],46
v_exp_f32 v95,v95
M 22,a[100:103],42
v_exp_f32 v96,v96
M 22,a[112:115],46
R 66,1,14336
v_fma_f32 v94,v94,v81,v81
M 18,a[96:99],42
v_exp_f32 v97,v97
M 18,a[108:111],46
L 3
v_rcp_f32 v94,v94
M 18,a[120:123],50
)"
R"(R 70,1,15360
v_fma_f32 v95,v95,v81,v81
M 18,a[132:135],54
s_waitcnt vmcnt(7)
D 0,56,3072
M 22,a[124:127],50
s_barrier
v_rcp_f32 v95,v95
M 22,a[136:139],54
s_add_u32 m0,s59,0
R 42,1,16384
v_fma_f32 v96,v96,v81,v81
M 26,a[128:131],50
s_add_u32 s56,s55,16384
v_rcp_f32 v96,v96
M 26,a[140:143],54
L 3
v_fma_f32 v97,v97,v81,v81
v_mul_f32 v30,v30,v94
M 26,a[152:155],58
s_add_u32 s55,s55,s60
R 46,1,17408
v_mul_f32 v31,v31,v95
M 26,a[164:167],62
v_rcp_f32 v97,v97
M 22,a[148:151],58
v_mul_f32 v32,v32,v96
v_mul_f32 v33,v33,v97
M 22,a[160:163],62
R 50,1,18432
v_cvt_pk_bf16_f32 v106,v30,v31
M 18,a[144:147],58
buffer_load_dword v8,s[36:39],s58 offen offset:0 sc1 lds
M 18,a[156:159],62
L 3
v_cvt_pk_bf16_f32 v107,v32,v33
v_fma_f32 v34,-v78,v86,v34
M 18,a[168:171],66
s_add_u32 m0,s57,16384
R 54,1,19456
v_fma_f32 v35,-v78,v87,v35
M 18,a[180:183],70
s_sub_u32 s57,s84,s57
v_fma_f32 v36,-v78,v88,v36
v_fma_f32 v37,-v78,v89,v37
M 22,a[172:175],66
v_mul_f32 v98,v34,v34
v_mul_f32 v99,v35,v35
M 22,a[184:187],70
R 58,1,20480
v_mul_f32 v100,v36,v36
M 26,a[176:179],66
D 0,56,0
M 26,a[188:191],70
L 3
v_mul_f32 v101,v37,v37
v_fma_f32 v98,v80,v98,v79
M 26,a[200:203],42
R 62,1,21504
v_fma_f32 v99,v80,v99,v79
M 26,a[212:215],46
v_fma_f32 v100,v80,v100,v79
v_fma_f32 v101,v80,v101,v79
M 22,a[196:199],42
v_mul_f32 v98,v34,v98
v_mul_f32 v99,v35,v99
M 22,a[208:211],46
R 66,1,22528
v_mul_f32 v100,v36,v100
M 18,a[192:195],42
v_exp_f32 v98,v98
M 18,a[204:207],46
L 3
v_exp_f32 v99,v99
M 18,a[216:219],50
R 70,1,23552
v_mul_f32 v101,v37,v101
M 18,a[228:231],54
D 0,56,1024
M 22,a[220:223],50
v_exp_f32 v100,v100
M 22,a[232:235],54
R 42,1,24576
v_fma_f32 v98,v98,v81,v81
M 26,a[224:227],50
v_exp_f32 v101,v101
M 26,a[236:239],54
L 3
v_rcp_f32 v98,v98
M 26,a[248:251],58
R 46,1,25600
v_fma_f32 v99,v99,v81,v81
M 26,v[132:135],62
v_rcp_f32 v99,v99
M 22,a[244:247],58
v_fma_f32 v100,v100,v81,v81
v_fma_f32 v101,v101,v81,v81
M 22,v[128:131],62
R 50,1,26624
v_mul_f32 v34,v34,v98
M 18,a[240:243],58
v_rcp_f32 v100,v100
M 18,a[252:255],62
L 3
v_rcp_f32 v101,v101
M 18,v[136:139],66
R 54,1,27648
v_mul_f32 v35,v35,v99
M 18,v[148:151],70
D 0,56,2048
M 22,v[140:143],66
v_mul_f32 v36,v36,v100
v_mul_f32 v37,v37,v101
M 22,v[152:155],70
R 58,1,28672
v_cvt_pk_bf16_f32 v108,v34,v35
M 26,v[144:147],66
v_cvt_pk_bf16_f32 v109,v36,v37
v_fma_f32 v38,-v78,v90,v38
M 26,v[156:159],70
L 3
v_fma_f32 v39,-v78,v91,v39
v_fma_f32 v40,-v78,v92,v40
M 26,v[168:171],42
R 62,1,29696
v_fma_f32 v41,-v78,v93,v41
M 26,v[180:183],46
v_mul_f32 v102,v38,v38
v_mul_f32 v103,v39,v39
M 22,v[164:167],42
v_mul_f32 v104,v40,v40
v_mul_f32 v105,v41,v41
M 22,v[176:179],46
R 66,1,30720
v_fma_f32 v102,v80,v102,v79
M 18,v[160:163],42
v_fma_f32 v103,v80,v103,v79
v_fma_f32 v104,v80,v104,v79
M 18,v[172:175],46
L 3
D 0,56,3072
M 18,v[184:187],50
s_nop 0
R 70,1,31744
v_fma_f32 v105,v80,v105,v79
M 18,v[196:199],54
s_waitcnt vmcnt(4)
v_mul_f32 v102,v38,v102
v_mul_f32 v103,v39,v103
M 22,v[188:191],50
s_barrier
buffer_store_dword v4,v3,s[36:39],s70 offen offset:0 sc0 sc1
M 22,v[200:203],54
s_mov_b32 s70,s69
R 42,2,0
ds_read_b32 v7,v6 offset:0
M 26,v[192:195],50
s_mov_b32 s69,s68
R 74,13,0
v_mul_f32 v104,v40,v104
M 26,v[204:207],54
L 5
v_exp_f32 v102,v102
M 26,v[216:219],58
s_add_u32 s68,s68,s63
R 46,2,1024
v_mul_f32 v105,v41,v105
M 26,v[228:231],62
v_exp_f32 v103,v103
M 22,v[212:215],58
buffer_store_dwordx4 v[106:109],v0,s[36:39],s67 offen offset:0 sc0 sc1
v_fma_f32 v102,v102,v81,v81
M 22,v[224:227],62
L 2
R 50,2,2048
v_fma_f32 v103,v103,v81,v81
M 18,v[208:211],58
v_exp_f32 v104,v104
M 18,v[220:223],62
s_nop 0
v_exp_f32 v105,v105
M 18,v[232:235],66
R 54,2,3072
v_fma_f32 v104,v104,v81,v81
M 18,v[244:247],70
v_fma_f32 v105,v105,v81,v81
v_cmp_eq_u32 vcc,s53,v7
M 22,v[236:239],66
v_rcp_f32 v102,v102
v_rcp_f32 v103,v103
v_rcp_f32 v104,v104
v_rcp_f32 v105,v105
M 22,v[248:251],70
s_cmp_eq_u32 vcc_lo,-1
R 58,2,4096
v_mul_f32 v38,v38,v102
v_mul_f32 v39,v39,v103
v_mul_f32 v40,v40,v104
v_mul_f32 v41,v41,v105
v_cvt_pk_bf16_f32 v110,v38,v39
v_cvt_pk_bf16_f32 v111,v40,v41
M 26,v[240:243],66
L 3
buffer_store_dwordx2 v[110:111],v10,s[36:39],s67 offen offset:0 sc0 sc1
M 26,v[252:255],70
s_cbranch_scc0 Lg3_K1_%=
Lg3_F1_%=:
v_mfma_f32_16x16x32_bf16 v[38:41],a[8:11],v[42:45],0
s_add_u32 m0,s57,0
R 62,2,5120
v_add_f32_dpp v115,v114,v114 quad_perm:[1,0,3,2] row_mask:0xf bank_mask:0xf
M 38,a[20:23],46
buffer_load_dword v114,v11,s[36:39],s73 offen offset:0 sc1
v_mfma_f32_16x16x32_bf16 v[34:37],a[4:7],v[42:45],0
s_add_u32 s56,s55,0
v_add_f32_dpp v115,v115,v115 quad_perm:[2,3,0,1] row_mask:0xf bank_mask:0xf
v_fma_f32 v18,-v74,v82,v18
M 34,a[16:19],46
s_add_u32 s58,s58,s61
R 66,2,6144
v_fma_f32 v19,-v74,v83,v19
v_mfma_f32_16x16x32_bf16 v[30:33],a[0:3],v[42:45],0
D 0,56,0
M 30,a[12:15],46
L 3
v_add_f32_dpp v118,v115,v115 row_half_mirror row_mask:0xf bank_mask:0xf
v_fma_f32 v20,-v74,v84,v20
M 30,a[24:27],50
s_mov_b32 s67,s65
R 70,2,7168
v_fma_f32 v21,-v74,v85,v21
M 30,a[36:39],54
s_add_u32 s65,s65,s62
v_mov_b32_dpp v115,v118 row_ror:8 row_mask:0xf bank_mask:0xf
v_fma_f32 v115,-v118,v118,v115
M 34,a[28:31],50
s_add_u32 s54,s54,s46
v_max_f32 v115,s87,v115
v_mul_f32 v118,v17,v118
M 34,a[40:43],54
s_add_u32 s73,s73,s64
R 42,2,8192
v_mul_f32 v94,v18,v18
M 38,a[32:35],50
v_rsq_f32 v116,v115
M 38,a[44:47],54
L 3
D 0,56,1024
M 38,a[56:59],58
R 46,2,9216
v_fma_f32 v115,v15,v115,v16
M 38,a[68:71],62
v_mul_f32 v119,v116,v116
v_fma_f32 v115,v14,v119,v115
M 34,a[52:55],58
v_fma_f32 v118,v116,v115,v118
v_mul_f32 v95,v19,v19
M 34,a[64:67],62
R 50,2,10240
v_mul_f32 v96,v20,v20
M 30,a[48:51],58
ds_write_b32 v12,v118 offset:2048
M 30,a[60:63],62
L 4
v_mul_f32 v97,v21,v21
v_fma_f32 v94,v76,v94,v75
M 30,a[72:75],66
R 54,2,11264
v_fma_f32 v95,v76,v95,v75
M 30,a[84:87],70
v_fma_f32 v96,v76,v96,v75
v_fma_f32 v97,v76,v97,v75
M 34,a[76:79],66
v_mul_f32 v94,v18,v94
v_mul_f32 v95,v19,v95
M 34,a[88:91],70
R 58,2,12288
v_mul_f32 v96,v20,v96
M 38,a[80:83],66
D 0,56,2048
M 38,a[92:95],70
L 4
v_exp_f32 v94,v94
M 38,a[104:107],42
R 62,2,13312
v_mul_f32 v97,v21,v97
M 38,a[116:119],46
v_exp_f32 v95,v95
M 34,a[100:103],42
v_exp_f32 v96,v96
M 34,a[112:115],46
R 66,2,14336
v_fma_f32 v94,v94,v77,v77
M 30,a[96:99],42
v_exp_f32 v97,v97
M 30,a[108:111],46
L 3
v_rcp_f32 v94,v94
M 30,a[120:123],50
R 70,2,15360
v_fma_f32 v95,v95,v77,v77
M 30,a[132:135],54
s_waitcnt vmcnt(7)
D 0,56,3072
M 34,a[124:127],50
s_barrier
v_rcp_f32 v95,v95
M 34,a[136:139],54
s_add_u32 m0,s59,1024
R 42,2,16384
v_fma_f32 v96,v96,v77,v77
M 38,a[128:131],50
s_add_u32 s56,s55,16384
v_rcp_f32 v96,v96
M 38,a[140:143],54
L 3
v_fma_f32 v97,v97,v77,v77
v_mul_f32 v18,v18,v94
M 38,a[152:155],58
s_add_u32 s55,s55,s60
R 46,2,17408
v_mul_f32 v19,v19,v95
M 38,a[164:167],62
v_rcp_f32 v97,v97
M 34,a[148:151],58
v_mul_f32 v20,v20,v96
v_mul_f32 v21,v21,v97
M 34,a[160:163],62
R 50,2,18432
v_cvt_pk_bf16_f32 v106,v18,v19
M 30,a[144:147],58
buffer_load_dword v8,s[36:39],s58 offen offset:0 sc1 lds
M 30,a[156:159],62
L 3
v_cvt_pk_bf16_f32 v107,v20,v21
v_fma_f32 v22,-v74,v86,v22
M 30,a[168:171],66
s_add_u32 m0,s57,16384
R 54,2,19456
v_fma_f32 v23,-v74,v87,v23
M 30,a[180:183],70
s_sub_u32 s57,s84,s57
v_fma_f32 v24,-v74,v88,v24
v_fma_f32 v25,-v74,v89,v25
M 34,a[172:175],66
v_mul_f32 v98,v22,v22
v_mul_f32 v99,v23,v23
M 34,a[184:187],70
R 58,2,20480
v_mul_f32 v100,v24,v24
M 38,a[176:179],66
D 0,56,0
M 38,a[188:191],70
L 3
v_mul_f32 v101,v25,v25
v_fma_f32 v98,v76,v98,v75
M 38,a[200:203],42
R 62,2,21504
v_fma_f32 v99,v76,v99,v75
M 38,a[212:215],46
v_fma_f32 v100,v76,v100,v75
v_fma_f32 v101,v76,v101,v75
M 34,a[196:199],42
v_mul_f32 v98,v22,v98
v_mul_f32 v99,v23,v99
M 34,a[208:211],46
R 66,2,22528
v_mul_f32 v100,v24,v100
M 30,a[192:195],42
v_exp_f32 v98,v98
M 30,a[204:207],46
L 3
v_exp_f32 v99,v99
M 30,a[216:219],50
R 70,2,23552
v_mul_f32 v101,v25,v101
M 30,a[228:231],54
D 0,56,1024
M 34,a[220:223],50
v_exp_f32 v100,v100
M 34,a[232:235],54
R 42,2,24576
v_fma_f32 v98,v98,v77,v77
M 38,a[224:227],50
v_exp_f32 v101,v101
M 38,a[236:239],54
L 3
v_rcp_f32 v98,v98
M 38,a[248:251],58
R 46,2,25600
v_fma_f32 v99,v99,v77,v77
M 38,v[132:135],62
v_rcp_f32 v99,v99
M 34,a[244:247],58
v_fma_f32 v100,v100,v77,v77
v_fma_f32 v101,v101,v77,v77
M 34,v[128:131],62
R 50,2,26624
v_mul_f32 v22,v22,v98
M 30,a[240:243],58
v_rcp_f32 v100,v100
M 30,a[252:255],62
L 3
v_rcp_f32 v101,v101
M 30,v[136:139],66
R 54,2,27648
v_mul_f32 v23,v23,v99
M 30,v[148:151],70
D 0,56,2048
M 34,v[140:143],66
v_mul_f32 v24,v24,v100
v_mul_f32 v25,v25,v101
M 34,v[152:155],70
R 58,2,28672
v_cvt_pk_bf16_f32 v108,v22,v23
M 38,v[144:147],66
v_cvt_pk_bf16_f32 v109,v24,v25
v_fma_f32 v26,-v74,v90,v26
M 38,v[156:159],70
L 3
v_fma_f32 v27,-v74,v91,v27
v_fma_f32 v28,-v74,v92,v28
M 38,v[168:171],42
R 62,2,29696
v_fma_f32 v29,-v74,v93,v29
M 38,v[180:183],46
v_mul_f32 v102,v26,v26
v_mul_f32 v103,v27,v27
M 34,v[164:167],42
v_mul_f32 v104,v28,v28
v_mul_f32 v105,v29,v29
M 34,v[176:179],46
R 66,2,30720
v_fma_f32 v102,v76,v102,v75
M 30,v[160:163],42
v_fma_f32 v103,v76,v103,v75
v_fma_f32 v104,v76,v104,v75
M 30,v[172:175],46
L 3
D 0,56,3072
M 30,v[184:187],50
s_nop 0
R 70,2,31744
v_fma_f32 v105,v76,v105,v75
M 30,v[196:199],54
s_waitcnt vmcnt(4)
v_mul_f32 v102,v26,v102
v_mul_f32 v103,v27,v103
M 34,v[188:191],50
s_barrier
buffer_store_dword v4,v3,s[36:39],s70 offen offset:0 sc0 sc1
M 34,v[200:203],54
s_mov_b32 s70,s69
R 42,1,0
ds_read_b32 v7,v6 offset:1024
M 38,v[192:195],50
s_mov_b32 s69,s68
R 78,13,2048
v_mul_f32 v104,v28,v104
M 38,v[204:207],54
L 5
v_exp_f32 v102,v102
M 38,v[216:219],58
s_add_u32 s68,s68,s63
R 46,1,1024
v_mul_f32 v105,v29,v105
M 38,v[228:231],62
v_exp_f32 v103,v103
M 34,v[212:215],58
buffer_store_dwordx4 v[106:109],v0,s[36:39],s66 offen offset:0 sc0 sc1
v_fma_f32 v102,v102,v77,v77
M 34,v[224:227],62
L 2
R 50,1,2048
v_fma_f32 v103,v103,v77,v77
M 30,v[208:211],58
v_exp_f32 v104,v104
M 30,v[220:223],62
s_nop 0
v_exp_f32 v105,v105
M 30,v[232:235],66
R 54,1,3072
v_fma_f32 v104,v104,v77,v77
M 30,v[244:247],70
v_fma_f32 v105,v105,v77,v77
v_cmp_eq_u32 vcc,s53,v7
M 34,v[236:239],66
v_rcp_f32 v102,v102
v_rcp_f32 v103,v103
v_rcp_f32 v104,v104
v_rcp_f32 v105,v105
M 34,v[248:251],70
s_cmp_eq_u32 vcc_lo,-1
R 58,1,4096
v_mul_f32 v26,v26,v102
v_mul_f32 v27,v27,v103
v_mul_f32 v28,v28,v104
v_mul_f32 v29,v29,v105
v_cvt_pk_bf16_f32 v110,v26,v27
v_cvt_pk_bf16_f32 v111,v28,v29
)"
R"(M 38,v[240:243],66
L 3
buffer_store_dwordx2 v[110:111],v10,s[36:39],s66 offen offset:0 sc0 sc1
M 38,v[252:255],70
s_cbranch_scc1 Lg3_F0_%=
Lg3_K0_%=:
Lg3_L0_%=:
v_mfma_f32_16x16x32_bf16 v[26:29],a[8:11],v[42:45],0
s_add_u32 m0,s59,3072
R 62,1,5120
v_add_f32_dpp v115,v114,v114 quad_perm:[1,0,3,2] row_mask:0xf bank_mask:0xf
M 26,a[20:23],46
buffer_load_dword v8,s[36:39],s58 offen offset:0 sc1 lds
v_mfma_f32_16x16x32_bf16 v[22:25],a[4:7],v[42:45],0
s_mov_b32 s66,s65
v_add_f32_dpp v115,v115,v115 quad_perm:[2,3,0,1] row_mask:0xf bank_mask:0xf
v_fma_f32 v30,-v78,v82,v30
M 22,a[16:19],46
s_add_u32 s65,s65,s62
R 66,1,6144
v_fma_f32 v31,-v78,v83,v31
v_mfma_f32_16x16x32_bf16 v[18:21],a[0:3],v[42:45],0
v_add_f32_dpp v118,v115,v115 row_half_mirror row_mask:0xf bank_mask:0xf
v_fma_f32 v32,-v78,v84,v32
v_fma_f32 v33,-v78,v85,v33
M 18,a[12:15],46
L 3
v_mul_f32 v94,v30,v30
v_mul_f32 v95,v31,v31
M 18,a[24:27],50
R 70,1,7168
v_mov_b32_dpp v115,v118 row_ror:8 row_mask:0xf bank_mask:0xf
M 18,a[36:39],54
v_fma_f32 v115,-v118,v118,v115
v_max_f32 v115,s87,v115
M 22,a[28:31],50
v_rsq_f32 v116,v115
M 22,a[40:43],54
R 42,1,8192
v_fma_f32 v115,v15,v115,v16
v_mul_f32 v118,v17,v118
M 26,a[32:35],50
v_mul_f32 v119,v116,v116
v_fma_f32 v115,v14,v119,v115
M 26,a[44:47],54
L 3
v_fma_f32 v118,v116,v115,v118
v_mul_f32 v96,v32,v32
M 26,a[56:59],58
R 46,1,9216
v_mul_f32 v97,v33,v33
M 26,a[68:71],62
ds_write_b32 v12,v118 offset:0
M 22,a[52:55],58
v_fma_f32 v94,v80,v94,v79
v_fma_f32 v95,v80,v95,v79
v_fma_f32 v96,v80,v96,v79
M 22,a[64:67],62
R 50,1,10240
v_fma_f32 v97,v80,v97,v79
M 18,a[48:51],58
v_mul_f32 v94,v30,v94
v_mul_f32 v95,v31,v95
M 18,a[60:63],62
L 4
v_exp_f32 v94,v94
M 18,a[72:75],66
R 54,1,11264
v_mul_f32 v96,v32,v96
M 18,a[84:87],70
v_exp_f32 v95,v95
v_mul_f32 v97,v33,v97
M 22,a[76:79],66
v_exp_f32 v96,v96
M 22,a[88:91],70
R 58,1,12288
v_fma_f32 v94,v94,v81,v81
M 26,a[80:83],66
v_exp_f32 v97,v97
M 26,a[92:95],70
L 4
v_rcp_f32 v94,v94
M 26,a[104:107],42
R 62,1,13312
v_fma_f32 v95,v95,v81,v81
v_fma_f32 v96,v96,v81,v81
M 26,a[116:119],46
v_rcp_f32 v95,v95
M 22,a[100:103],42
v_rcp_f32 v96,v96
M 22,a[112:115],46
R 66,1,14336
v_fma_f32 v97,v97,v81,v81
M 18,a[96:99],42
v_rcp_f32 v97,v97
M 18,a[108:111],46
L 3
v_mul_f32 v30,v30,v94
v_mul_f32 v31,v31,v95
v_mul_f32 v32,v32,v96
M 18,a[120:123],50
R 70,1,15360
v_mul_f32 v33,v33,v97
M 18,a[132:135],54
s_waitcnt vmcnt(0)
v_cvt_pk_bf16_f32 v106,v30,v31
v_cvt_pk_bf16_f32 v107,v32,v33
M 22,a[124:127],50
s_barrier
ds_read_b32 v7,v6 offset:3072
v_fma_f32 v34,-v78,v86,v34
M 22,a[136:139],54
R 42,1,16384
v_fma_f32 v35,-v78,v87,v35
M 26,a[128:131],50
buffer_store_dword v4,v3,s[36:39],s70 offen offset:0 sc0 sc1
v_fma_f32 v36,-v78,v88,v36
M 26,a[140:143],54
L 4
v_fma_f32 v37,-v78,v89,v37
v_mul_f32 v98,v34,v34
M 26,a[152:155],58
s_mov_b32 s70,0x7fff0000
R 46,1,17408
v_mul_f32 v99,v35,v35
M 26,a[164:167],62
v_mul_f32 v100,v36,v36
v_mul_f32 v101,v37,v37
M 22,a[148:151],58
L 2
v_cmp_eq_u32 vcc,s53,v7
v_fma_f32 v98,v80,v98,v79
M 22,a[160:163],62
R 50,1,18432
v_fma_f32 v99,v80,v99,v79
v_fma_f32 v100,v80,v100,v79
M 18,a[144:147],58
v_fma_f32 v101,v80,v101,v79
v_mul_f32 v98,v34,v98
M 18,a[156:159],62
s_cmp_eq_u32 vcc_lo,-1
s_nop 0
v_mul_f32 v99,v35,v99
s_cbranch_scc0 Lg3_X0_%=
M 18,a[168:171],66
s_add_u32 m0,s57,0
s_add_u32 s56,s55,0
s_add_u32 s58,s58,s61
R 54,1,19456
M 18,a[180:183],70
buffer_load_dword v114,v11,s[36:39],s73 offen offset:0 sc1
M 22,a[172:175],66
D 0,56,0
v_mul_f32 v100,v36,v100
M 22,a[184:187],70
s_add_u32 s54,s54,s46
R 58,1,20480
v_mul_f32 v101,v37,v101
M 26,a[176:179],66
s_add_u32 s73,s73,s64
v_exp_f32 v98,v98
M 26,a[188:191],70
L 3
v_exp_f32 v99,v99
M 26,a[200:203],42
R 62,1,21504
v_fma_f32 v98,v98,v81,v81
M 26,a[212:215],46
D 0,56,1024
v_fma_f32 v99,v99,v81,v81
M 22,a[196:199],42
v_exp_f32 v100,v100
M 22,a[208:211],46
R 66,1,22528
v_fma_f32 v100,v100,v81,v81
M 18,a[192:195],42
D 0,56,2048
M 18,a[204:207],46
L 3
v_exp_f32 v101,v101
M 18,a[216:219],50
R 70,1,23552
v_rcp_f32 v98,v98
M 18,a[228:231],54
v_rcp_f32 v99,v99
M 22,a[220:223],50
D 0,56,3072
M 22,a[232:235],54
s_add_u32 m0,s59,0
R 42,1,24576
v_fma_f32 v101,v101,v81,v81
M 26,a[224:227],50
buffer_load_dword v8,s[36:39],s58 offen offset:0 sc1 lds
M 26,a[236:239],54
L 3
v_rcp_f32 v100,v100
v_mul_f32 v34,v34,v98
M 26,a[248:251],58
s_add_u32 m0,s57,16384
R 46,1,25600
v_mul_f32 v35,v35,v99
M 26,v[132:135],62
s_add_u32 s56,s55,16384
v_rcp_f32 v101,v101
M 22,a[244:247],58
D 0,56,0
M 22,v[128:131],62
s_add_u32 s55,s55,s60
R 50,1,26624
v_mul_f32 v36,v36,v100
M 18,a[240:243],58
s_sub_u32 s57,s84,s57
v_mul_f32 v37,v37,v101
v_cvt_pk_bf16_f32 v108,v34,v35
v_cvt_pk_bf16_f32 v109,v36,v37
M 18,a[252:255],62
L 3
buffer_store_dwordx4 v[106:109],v0,s[36:39],s67 offen offset:0 sc0 sc1
v_fma_f32 v38,-v78,v90,v38
M 18,v[136:139],66
R 54,1,27648
v_fma_f32 v39,-v78,v91,v39
M 18,v[148:151],70
v_fma_f32 v40,-v78,v92,v40
v_fma_f32 v41,-v78,v93,v41
M 22,v[140:143],66
D 0,56,1024
M 22,v[152:155],70
R 58,1,28672
v_mul_f32 v102,v38,v38
v_mul_f32 v103,v39,v39
M 26,v[144:147],66
v_mul_f32 v104,v40,v40
v_mul_f32 v105,v41,v41
M 26,v[156:159],70
L 3
v_fma_f32 v102,v80,v102,v79
v_fma_f32 v103,v80,v103,v79
M 26,v[168:171],42
R 62,1,29696
v_fma_f32 v104,v80,v104,v79
M 26,v[180:183],46
D 0,56,2048
M 22,v[164:167],42
v_fma_f32 v105,v80,v105,v79
v_mul_f32 v102,v38,v102
v_mul_f32 v103,v39,v103
M 22,v[176:179],46
R 66,1,30720
v_mul_f32 v104,v40,v104
M 18,v[160:163],42
v_exp_f32 v102,v102
M 18,v[172:175],46
L 3
D 0,56,3072
M 18,v[184:187],50
s_nop 0
R 70,1,31744
v_mul_f32 v105,v41,v105
M 18,v[196:199],54
s_waitcnt vmcnt(5)
v_exp_f32 v103,v103
v_fma_f32 v102,v102,v81,v81
M 22,v[188:191],50
s_barrier
buffer_store_dword v4,v3,s[36:39],s70 offen offset:0 sc0 sc1
M 22,v[200:203],54
s_mov_b32 s70,s69
R 42,2,0
ds_read_b32 v7,v6 offset:0
M 26,v[192:195],50
s_mov_b32 s69,s68
R 74,13,0
v_fma_f32 v103,v103,v81,v81
M 26,v[204:207],54
L 5
v_exp_f32 v104,v104
M 26,v[216:219],58
s_add_u32 s68,s68,s63
R 46,2,1024
v_exp_f32 v105,v105
M 26,v[228:231],62
v_rcp_f32 v102,v102
M 22,v[212:215],58
v_rcp_f32 v103,v103
M 22,v[224:227],62
L 2
R 50,2,2048
v_fma_f32 v104,v104,v81,v81
M 18,v[208:211],58
v_rcp_f32 v104,v104
M 18,v[220:223],62
s_nop 0
v_fma_f32 v105,v105,v81,v81
v_rcp_f32 v105,v105
M 18,v[232:235],66
R 54,2,3072
v_mul_f32 v38,v38,v102
M 18,v[244:247],70
v_mul_f32 v39,v39,v103
v_mul_f32 v40,v40,v104
M 22,v[236:239],66
v_cmp_eq_u32 vcc,s53,v7
v_mul_f32 v41,v41,v105
M 22,v[248:251],70
R 58,2,4096
v_cvt_pk_bf16_f32 v110,v38,v39
v_cvt_pk_bf16_f32 v111,v40,v41
M 26,v[240:243],66
L 3
s_cmp_eq_u32 vcc_lo,-1
buffer_store_dwordx2 v[110:111],v10,s[36:39],s67 offen offset:0 sc0 sc1
M 26,v[252:255],70
buffer_store_dword v4,v3,s[36:39],s71 offen offset:0 sc0 sc1
s_cbranch_scc1 Lg3_F1_%=
s_branch Lg3_K1_%=
Lg3_X0_%=:
M 18,a[168:171],66
s_add_u32 m0,s59,0
R 54,1,19456
v_mul_f32 v100,v36,v100
M 18,a[180:183],70
buffer_load_dword v8,s[36:39],s58 offen offset:0 sc1 lds
M 22,a[172:175],66
v_exp_f32 v98,v98
M 22,a[184:187],70
R 58,1,20480
v_mul_f32 v101,v37,v101
M 26,a[176:179],66
v_exp_f32 v99,v99
M 26,a[188:191],70
L 3
v_exp_f32 v100,v100
M 26,a[200:203],42
R 62,1,21504
v_fma_f32 v98,v98,v81,v81
M 26,a[212:215],46
v_exp_f32 v101,v101
M 22,a[196:199],42
v_rcp_f32 v98,v98
M 22,a[208:211],46
R 66,1,22528
v_fma_f32 v99,v99,v81,v81
M 18,a[192:195],42
v_rcp_f32 v99,v99
M 18,a[204:207],46
L 3
v_fma_f32 v100,v100,v81,v81
v_fma_f32 v101,v101,v81,v81
M 18,a[216:219],50
R 70,1,23552
v_mul_f32 v34,v34,v98
M 18,a[228:231],54
v_rcp_f32 v100,v100
M 22,a[220:223],50
v_rcp_f32 v101,v101
M 22,a[232:235],54
R 42,1,24576
v_mul_f32 v35,v35,v99
M 26,a[224:227],50
v_mul_f32 v36,v36,v100
v_mul_f32 v37,v37,v101
M 26,a[236:239],54
L 3
v_cvt_pk_bf16_f32 v108,v34,v35
v_cvt_pk_bf16_f32 v109,v36,v37
M 26,a[248:251],58
R 46,1,25600
v_fma_f32 v38,-v78,v90,v38
M 26,v[132:135],62
buffer_store_dwordx4 v[106:109],v0,s[36:39],s67 offen offset:0 sc0 sc1
v_fma_f32 v39,-v78,v91,v39
M 22,a[244:247],58
v_fma_f32 v40,-v78,v92,v40
v_fma_f32 v41,-v78,v93,v41
M 22,v[128:131],62
R 50,1,26624
v_mul_f32 v102,v38,v38
M 18,a[240:243],58
v_mul_f32 v103,v39,v39
v_mul_f32 v104,v40,v40
M 18,a[252:255],62
L 3
v_mul_f32 v105,v41,v41
v_fma_f32 v102,v80,v102,v79
M 18,v[136:139],66
R 54,1,27648
v_fma_f32 v103,v80,v103,v79
M 18,v[148:151],70
v_fma_f32 v104,v80,v104,v79
v_fma_f32 v105,v80,v105,v79
M 22,v[140:143],66
v_mul_f32 v102,v38,v102
v_mul_f32 v103,v39,v103
M 22,v[152:155],70
R 58,1,28672
v_mul_f32 v104,v40,v104
M 26,v[144:147],66
v_exp_f32 v102,v102
M 26,v[156:159],70
L 3
v_exp_f32 v103,v103
M 26,v[168:171],42
R 62,1,29696
v_mul_f32 v105,v41,v105
M 26,v[180:183],46
v_exp_f32 v104,v104
M 22,v[164:167],42
v_exp_f32 v105,v105
M 22,v[176:179],46
R 66,1,30720
v_fma_f32 v102,v102,v81,v81
M 18,v[160:163],42
v_rcp_f32 v102,v102
M 18,v[172:175],46
L 3
v_fma_f32 v103,v103,v81,v81
v_fma_f32 v104,v104,v81,v81
M 18,v[184:187],50
s_nop 0
R 70,1,31744
v_fma_f32 v105,v105,v81,v81
M 18,v[196:199],54
s_waitcnt vmcnt(1)
v_rcp_f32 v103,v103
M 22,v[188:191],50
s_barrier
buffer_store_dword v4,v3,s[36:39],s70 offen offset:0 sc0 sc1
M 22,v[200:203],54
s_mov_b32 s70,s69
ds_read_b32 v7,v6 offset:0
R 74,13,0
M 26,v[192:195],50
s_mov_b32 s69,s68
v_rcp_f32 v104,v104
M 26,v[204:207],54
L 4
v_rcp_f32 v105,v105
M 26,v[216:219],58
s_add_u32 s68,s68,s63
v_mul_f32 v38,v38,v102
v_mul_f32 v39,v39,v103
M 26,v[228:231],62
v_mul_f32 v40,v40,v104
v_mul_f32 v41,v41,v105
M 22,v[212:215],58
v_cvt_pk_bf16_f32 v110,v38,v39
v_cvt_pk_bf16_f32 v111,v40,v41
M 22,v[224:227],62
L 1
buffer_store_dwordx2 v[110:111],v10,s[36:39],s67 offen offset:0 sc0 sc1
M 18,v[208:211],58
v_cmp_eq_u32 vcc,s53,v7
M 18,v[220:223],62
s_nop 0
M 18,v[232:235],66
M 18,v[244:247],70
M 22,v[236:239],66
M 22,v[248:251],70
M 26,v[240:243],66
M 26,v[252:255],70
s_mov_b32 s80,1
s_cmp_eq_u32 vcc_lo,-1
s_cbranch_scc0 Lg3_xn0_%=
buffer_load_dword v114,v11,s[36:39],s73 offen offset:0 sc1
L 0
v_fma_f32 v18,-v74,v82,v18
v_fma_f32 v19,-v74,v83,v19
v_fma_f32 v20,-v74,v84,v20
v_fma_f32 v21,-v74,v85,v21
v_mul_f32 v94,v18,v18
v_mul_f32 v95,v19,v19
v_mul_f32 v96,v20,v20
v_mul_f32 v97,v21,v21
s_add_u32 s73,s73,s64
s_add_u32 m0,s57,0
s_add_u32 s56,s55,0
D 0,56,0
v_fma_f32 v94,v76,v94,v75
v_fma_f32 v95,v76,v95,v75
v_fma_f32 v96,v76,v96,v75
v_fma_f32 v97,v76,v97,v75
v_mul_f32 v94,v18,v94
v_mul_f32 v95,v19,v95
v_mul_f32 v96,v20,v96
v_mul_f32 v97,v21,v97
D 0,56,1024
v_exp_f32 v94,v94
v_exp_f32 v95,v95
v_exp_f32 v96,v96
v_exp_f32 v97,v97
v_fma_f32 v94,v94,v77,v77
v_fma_f32 v95,v95,v77,v77
v_fma_f32 v96,v96,v77,v77
v_fma_f32 v97,v97,v77,v77
D 0,56,2048
v_rcp_f32 v94,v94
v_rcp_f32 v95,v95
v_rcp_f32 v96,v96
v_rcp_f32 v97,v97
)"
R"(v_mul_f32 v18,v18,v94
v_mul_f32 v19,v19,v95
v_mul_f32 v20,v20,v96
v_mul_f32 v21,v21,v97
D 0,56,3072
v_cvt_pk_bf16_f32 v106,v18,v19
v_cvt_pk_bf16_f32 v107,v20,v21
v_fma_f32 v22,-v74,v86,v22
v_fma_f32 v23,-v74,v87,v23
v_fma_f32 v24,-v74,v88,v24
v_fma_f32 v25,-v74,v89,v25
v_mul_f32 v98,v22,v22
v_mul_f32 v99,v23,v23
s_add_u32 s58,s58,s61
s_add_u32 m0,s57,16384
s_add_u32 s56,s55,16384
D 0,56,0
v_mul_f32 v100,v24,v24
v_mul_f32 v101,v25,v25
v_fma_f32 v98,v76,v98,v75
v_fma_f32 v99,v76,v99,v75
v_fma_f32 v100,v76,v100,v75
v_fma_f32 v101,v76,v101,v75
v_mul_f32 v98,v22,v98
v_mul_f32 v99,v23,v99
D 0,56,1024
v_mul_f32 v100,v24,v100
v_mul_f32 v101,v25,v101
v_exp_f32 v98,v98
v_exp_f32 v99,v99
v_exp_f32 v100,v100
v_exp_f32 v101,v101
v_fma_f32 v98,v98,v77,v77
v_fma_f32 v99,v99,v77,v77
D 0,56,2048
v_fma_f32 v100,v100,v77,v77
v_fma_f32 v101,v101,v77,v77
v_rcp_f32 v98,v98
v_rcp_f32 v99,v99
v_rcp_f32 v100,v100
v_rcp_f32 v101,v101
v_mul_f32 v22,v22,v98
v_mul_f32 v23,v23,v99
D 0,56,3072
v_mul_f32 v24,v24,v100
v_mul_f32 v25,v25,v101
v_cvt_pk_bf16_f32 v108,v22,v23
v_cvt_pk_bf16_f32 v109,v24,v25
buffer_store_dwordx4 v[106:109],v0,s[36:39],s66 offen offset:0 sc0 sc1
v_fma_f32 v26,-v74,v90,v26
v_fma_f32 v27,-v74,v91,v27
v_fma_f32 v28,-v74,v92,v28
v_fma_f32 v29,-v74,v93,v29
s_add_u32 s55,s55,s60
s_sub_u32 s57,s84,s57
s_add_u32 s54,s54,s46
v_mul_f32 v102,v26,v26
v_mul_f32 v103,v27,v27
v_mul_f32 v104,v28,v28
v_mul_f32 v105,v29,v29
v_fma_f32 v102,v76,v102,v75
v_fma_f32 v103,v76,v103,v75
v_fma_f32 v104,v76,v104,v75
v_fma_f32 v105,v76,v105,v75
v_mul_f32 v102,v26,v102
v_mul_f32 v103,v27,v103
v_mul_f32 v104,v28,v104
v_mul_f32 v105,v29,v105
v_exp_f32 v102,v102
v_exp_f32 v103,v103
v_exp_f32 v104,v104
v_exp_f32 v105,v105
v_fma_f32 v102,v102,v77,v77
v_fma_f32 v103,v103,v77,v77
v_fma_f32 v104,v104,v77,v77
v_fma_f32 v105,v105,v77,v77
v_rcp_f32 v102,v102
v_rcp_f32 v103,v103
v_rcp_f32 v104,v104
v_rcp_f32 v105,v105
v_mul_f32 v26,v26,v102
v_mul_f32 v27,v27,v103
v_mul_f32 v28,v28,v104
v_mul_f32 v29,v29,v105
v_cvt_pk_bf16_f32 v110,v26,v27
v_cvt_pk_bf16_f32 v111,v28,v29
buffer_store_dwordx2 v[110:111],v10,s[36:39],s66 offen offset:0 sc0 sc1
s_mov_b32 s72,s69
s_mov_b32 s69,0x7fff0000
s_mov_b32 s66,0x7fff0000
s_mov_b32 s76,0x7fff0000
s_branch Lg3_gox_%=
Lg3_xn0_%=:
s_cmp_ge_u32 s54,256
s_cbranch_scc1 Lg3_fin0_%=
s_add_u32 m0,s59,4096
s_nop 0
buffer_load_dword v8,s[36:39],s58 offen offset:0 sc1 lds
buffer_store_dword v4,v3,s[36:39],s71 offen offset:0 sc0 sc1
s_add_u32 m0,s59,5120
s_nop 0
buffer_load_dword v8,s[36:39],s58 offen offset:0 sc1 lds
buffer_store_dword v4,v3,s[36:39],s71 offen offset:0 sc0 sc1
s_mov_b32 s83,0
L 0
v_fma_f32 v18,-v74,v82,v18
v_fma_f32 v19,-v74,v83,v19
v_fma_f32 v20,-v74,v84,v20
v_fma_f32 v21,-v74,v85,v21
v_mul_f32 v94,v18,v18
v_mul_f32 v95,v19,v19
v_mul_f32 v96,v20,v20
v_mul_f32 v97,v21,v21
v_fma_f32 v94,v76,v94,v75
v_fma_f32 v95,v76,v95,v75
v_fma_f32 v96,v76,v96,v75
v_fma_f32 v97,v76,v97,v75
v_mul_f32 v94,v18,v94
v_mul_f32 v95,v19,v95
v_mul_f32 v96,v20,v96
v_mul_f32 v97,v21,v97
v_exp_f32 v94,v94
v_exp_f32 v95,v95
v_exp_f32 v96,v96
v_exp_f32 v97,v97
v_fma_f32 v94,v94,v77,v77
v_fma_f32 v95,v95,v77,v77
v_fma_f32 v96,v96,v77,v77
v_fma_f32 v97,v97,v77,v77
v_rcp_f32 v94,v94
v_rcp_f32 v95,v95
v_rcp_f32 v96,v96
v_rcp_f32 v97,v97
v_mul_f32 v18,v18,v94
v_mul_f32 v19,v19,v95
v_mul_f32 v20,v20,v96
v_mul_f32 v21,v21,v97
v_cvt_pk_bf16_f32 v106,v18,v19
v_cvt_pk_bf16_f32 v107,v20,v21
v_fma_f32 v22,-v74,v86,v22
v_fma_f32 v23,-v74,v87,v23
v_fma_f32 v24,-v74,v88,v24
v_fma_f32 v25,-v74,v89,v25
v_mul_f32 v98,v22,v22
v_mul_f32 v99,v23,v23
v_mul_f32 v100,v24,v24
v_mul_f32 v101,v25,v25
v_fma_f32 v98,v76,v98,v75
v_fma_f32 v99,v76,v99,v75
v_fma_f32 v100,v76,v100,v75
v_fma_f32 v101,v76,v101,v75
v_mul_f32 v98,v22,v98
v_mul_f32 v99,v23,v99
v_mul_f32 v100,v24,v100
v_mul_f32 v101,v25,v101
v_exp_f32 v98,v98
v_exp_f32 v99,v99
v_exp_f32 v100,v100
v_exp_f32 v101,v101
v_fma_f32 v98,v98,v77,v77
v_fma_f32 v99,v99,v77,v77
v_fma_f32 v100,v100,v77,v77
v_fma_f32 v101,v101,v77,v77
v_rcp_f32 v98,v98
v_rcp_f32 v99,v99
v_rcp_f32 v100,v100
v_rcp_f32 v101,v101
v_mul_f32 v22,v22,v98
v_mul_f32 v23,v23,v99
v_mul_f32 v24,v24,v100
v_mul_f32 v25,v25,v101
v_cvt_pk_bf16_f32 v108,v22,v23
v_cvt_pk_bf16_f32 v109,v24,v25
buffer_store_dwordx4 v[106:109],v0,s[36:39],s66 offen offset:0 sc0 sc1
v_fma_f32 v26,-v74,v90,v26
v_fma_f32 v27,-v74,v91,v27
v_fma_f32 v28,-v74,v92,v28
v_fma_f32 v29,-v74,v93,v29
v_mul_f32 v102,v26,v26
v_mul_f32 v103,v27,v27
v_mul_f32 v104,v28,v28
v_mul_f32 v105,v29,v29
v_fma_f32 v102,v76,v102,v75
v_fma_f32 v103,v76,v103,v75
v_fma_f32 v104,v76,v104,v75
v_fma_f32 v105,v76,v105,v75
v_mul_f32 v102,v26,v102
v_mul_f32 v103,v27,v103
v_mul_f32 v104,v28,v104
v_mul_f32 v105,v29,v105
v_exp_f32 v102,v102
v_exp_f32 v103,v103
v_exp_f32 v104,v104
v_exp_f32 v105,v105
v_fma_f32 v102,v102,v77,v77
v_fma_f32 v103,v103,v77,v77
v_fma_f32 v104,v104,v77,v77
v_fma_f32 v105,v105,v77,v77
v_rcp_f32 v102,v102
v_rcp_f32 v103,v103
v_rcp_f32 v104,v104
v_rcp_f32 v105,v105
v_mul_f32 v26,v26,v102
v_mul_f32 v27,v27,v103
v_mul_f32 v28,v28,v104
v_mul_f32 v29,v29,v105
v_cvt_pk_bf16_f32 v110,v26,v27
v_cvt_pk_bf16_f32 v111,v28,v29
buffer_store_dwordx2 v[110:111],v10,s[36:39],s66 offen offset:0 sc0 sc1
s_mov_b32 s72,s69
s_mov_b32 s69,0x7fff0000
s_mov_b32 s66,0x7fff0000
s_mov_b32 s76,0x7fff0000
s_branch Lg3_poll_%=
Lg3_fin0_%=:
L 0
v_fma_f32 v18,-v74,v82,v18
v_fma_f32 v19,-v74,v83,v19
v_fma_f32 v20,-v74,v84,v20
v_fma_f32 v21,-v74,v85,v21
v_mul_f32 v94,v18,v18
v_mul_f32 v95,v19,v19
v_mul_f32 v96,v20,v20
v_mul_f32 v97,v21,v21
v_fma_f32 v94,v76,v94,v75
v_fma_f32 v95,v76,v95,v75
v_fma_f32 v96,v76,v96,v75
v_fma_f32 v97,v76,v97,v75
v_mul_f32 v94,v18,v94
v_mul_f32 v95,v19,v95
v_mul_f32 v96,v20,v96
v_mul_f32 v97,v21,v97
v_exp_f32 v94,v94
v_exp_f32 v95,v95
v_exp_f32 v96,v96
v_exp_f32 v97,v97
v_fma_f32 v94,v94,v77,v77
v_fma_f32 v95,v95,v77,v77
v_fma_f32 v96,v96,v77,v77
v_fma_f32 v97,v97,v77,v77
v_rcp_f32 v94,v94
v_rcp_f32 v95,v95
v_rcp_f32 v96,v96
v_rcp_f32 v97,v97
v_mul_f32 v18,v18,v94
v_mul_f32 v19,v19,v95
v_mul_f32 v20,v20,v96
v_mul_f32 v21,v21,v97
v_cvt_pk_bf16_f32 v106,v18,v19
v_cvt_pk_bf16_f32 v107,v20,v21
v_fma_f32 v22,-v74,v86,v22
v_fma_f32 v23,-v74,v87,v23
v_fma_f32 v24,-v74,v88,v24
v_fma_f32 v25,-v74,v89,v25
v_mul_f32 v98,v22,v22
v_mul_f32 v99,v23,v23
v_mul_f32 v100,v24,v24
v_mul_f32 v101,v25,v25
v_fma_f32 v98,v76,v98,v75
v_fma_f32 v99,v76,v99,v75
v_fma_f32 v100,v76,v100,v75
v_fma_f32 v101,v76,v101,v75
v_mul_f32 v98,v22,v98
v_mul_f32 v99,v23,v99
v_mul_f32 v100,v24,v100
v_mul_f32 v101,v25,v101
v_exp_f32 v98,v98
v_exp_f32 v99,v99
v_exp_f32 v100,v100
v_exp_f32 v101,v101
v_fma_f32 v98,v98,v77,v77
v_fma_f32 v99,v99,v77,v77
v_fma_f32 v100,v100,v77,v77
v_fma_f32 v101,v101,v77,v77
v_rcp_f32 v98,v98
v_rcp_f32 v99,v99
v_rcp_f32 v100,v100
v_rcp_f32 v101,v101
v_mul_f32 v22,v22,v98
v_mul_f32 v23,v23,v99
v_mul_f32 v24,v24,v100
v_mul_f32 v25,v25,v101
v_cvt_pk_bf16_f32 v108,v22,v23
v_cvt_pk_bf16_f32 v109,v24,v25
buffer_store_dwordx4 v[106:109],v0,s[36:39],s66 offen offset:0 sc0 sc1
v_fma_f32 v26,-v74,v90,v26
v_fma_f32 v27,-v74,v91,v27
v_fma_f32 v28,-v74,v92,v28
v_fma_f32 v29,-v74,v93,v29
v_mul_f32 v102,v26,v26
v_mul_f32 v103,v27,v27
v_mul_f32 v104,v28,v28
v_mul_f32 v105,v29,v29
v_fma_f32 v102,v76,v102,v75
v_fma_f32 v103,v76,v103,v75
v_fma_f32 v104,v76,v104,v75
v_fma_f32 v105,v76,v105,v75
v_mul_f32 v102,v26,v102
v_mul_f32 v103,v27,v103
v_mul_f32 v104,v28,v104
v_mul_f32 v105,v29,v105
v_exp_f32 v102,v102
v_exp_f32 v103,v103
v_exp_f32 v104,v104
v_exp_f32 v105,v105
v_fma_f32 v102,v102,v77,v77
v_fma_f32 v103,v103,v77,v77
v_fma_f32 v104,v104,v77,v77
v_fma_f32 v105,v105,v77,v77
v_rcp_f32 v102,v102
v_rcp_f32 v103,v103
v_rcp_f32 v104,v104
v_rcp_f32 v105,v105
v_mul_f32 v26,v26,v102
v_mul_f32 v27,v27,v103
v_mul_f32 v28,v28,v104
v_mul_f32 v29,v29,v105
v_cvt_pk_bf16_f32 v110,v26,v27
v_cvt_pk_bf16_f32 v111,v28,v29
buffer_store_dwordx2 v[110:111],v10,s[36:39],s66 offen offset:0 sc0 sc1
s_mov_b32 s72,s69
s_mov_b32 s69,0x7fff0000
s_mov_b32 s66,0x7fff0000
s_mov_b32 s76,0x7fff0000
s_branch Lg3_done_%=
Lg3_K1_%=:
Lg3_L1_%=:
v_mfma_f32_16x16x32_bf16 v[38:41],a[8:11],v[42:45],0
s_add_u32 m0,s59,3072
R 62,2,5120
v_add_f32_dpp v115,v114,v114 quad_perm:[1,0,3,2] row_mask:0xf bank_mask:0xf
M 38,a[20:23],46
buffer_load_dword v8,s[36:39],s58 offen offset:0 sc1 lds
v_mfma_f32_16x16x32_bf16 v[34:37],a[4:7],v[42:45],0
s_mov_b32 s67,s65
v_add_f32_dpp v115,v115,v115 quad_perm:[2,3,0,1] row_mask:0xf bank_mask:0xf
v_fma_f32 v18,-v74,v82,v18
M 34,a[16:19],46
s_add_u32 s65,s65,s62
R 66,2,6144
v_fma_f32 v19,-v74,v83,v19
v_mfma_f32_16x16x32_bf16 v[30:33],a[0:3],v[42:45],0
v_add_f32_dpp v118,v115,v115 row_half_mirror row_mask:0xf bank_mask:0xf
v_fma_f32 v20,-v74,v84,v20
v_fma_f32 v21,-v74,v85,v21
M 30,a[12:15],46
L 3
v_mul_f32 v94,v18,v18
v_mul_f32 v95,v19,v19
M 30,a[24:27],50
R 70,2,7168
v_mov_b32_dpp v115,v118 row_ror:8 row_mask:0xf bank_mask:0xf
M 30,a[36:39],54
v_fma_f32 v115,-v118,v118,v115
v_max_f32 v115,s87,v115
M 34,a[28:31],50
v_rsq_f32 v116,v115
M 34,a[40:43],54
R 42,2,8192
v_fma_f32 v115,v15,v115,v16
v_mul_f32 v118,v17,v118
M 38,a[32:35],50
v_mul_f32 v119,v116,v116
v_fma_f32 v115,v14,v119,v115
M 38,a[44:47],54
L 3
v_fma_f32 v118,v116,v115,v118
v_mul_f32 v96,v20,v20
M 38,a[56:59],58
R 46,2,9216
v_mul_f32 v97,v21,v21
M 38,a[68:71],62
ds_write_b32 v12,v118 offset:2048
M 34,a[52:55],58
v_fma_f32 v94,v76,v94,v75
v_fma_f32 v95,v76,v95,v75
v_fma_f32 v96,v76,v96,v75
M 34,a[64:67],62
R 50,2,10240
v_fma_f32 v97,v76,v97,v75
M 30,a[48:51],58
v_mul_f32 v94,v18,v94
v_mul_f32 v95,v19,v95
M 30,a[60:63],62
L 4
v_exp_f32 v94,v94
M 30,a[72:75],66
R 54,2,11264
v_mul_f32 v96,v20,v96
M 30,a[84:87],70
v_exp_f32 v95,v95
v_mul_f32 v97,v21,v97
M 34,a[76:79],66
v_exp_f32 v96,v96
M 34,a[88:91],70
R 58,2,12288
v_fma_f32 v94,v94,v77,v77
M 38,a[80:83],66
v_exp_f32 v97,v97
M 38,a[92:95],70
L 4
v_rcp_f32 v94,v94
M 38,a[104:107],42
R 62,2,13312
v_fma_f32 v95,v95,v77,v77
v_fma_f32 v96,v96,v77,v77
M 38,a[116:119],46
v_rcp_f32 v95,v95
M 34,a[100:103],42
v_rcp_f32 v96,v96
M 34,a[112:115],46
R 66,2,14336
v_fma_f32 v97,v97,v77,v77
M 30,a[96:99],42
v_rcp_f32 v97,v97
M 30,a[108:111],46
L 3
v_mul_f32 v18,v18,v94
v_mul_f32 v19,v19,v95
v_mul_f32 v20,v20,v96
M 30,a[120:123],50
R 70,2,15360
v_mul_f32 v21,v21,v97
M 30,a[132:135],54
s_waitcnt vmcnt(0)
v_cvt_pk_bf16_f32 v106,v18,v19
v_cvt_pk_bf16_f32 v107,v20,v21
M 34,a[124:127],50
s_barrier
ds_read_b32 v7,v6 offset:3072
v_fma_f32 v22,-v74,v86,v22
M 34,a[136:139],54
R 42,2,16384
v_fma_f32 v23,-v74,v87,v23
M 38,a[128:131],50
buffer_store_dword v4,v3,s[36:39],s70 offen offset:0 sc0 sc1
v_fma_f32 v24,-v74,v88,v24
M 38,a[140:143],54
L 4
v_fma_f32 v25,-v74,v89,v25
v_mul_f32 v98,v22,v22
M 38,a[152:155],58
s_mov_b32 s70,0x7fff0000
R 46,2,17408
v_mul_f32 v99,v23,v23
M 38,a[164:167],62
v_mul_f32 v100,v24,v24
v_mul_f32 v101,v25,v25
M 34,a[148:151],58
L 2
v_cmp_eq_u32 vcc,s53,v7
v_fma_f32 v98,v76,v98,v75
M 34,a[160:163],62
R 50,2,18432
v_fma_f32 v99,v76,v99,v75
v_fma_f32 v100,v76,v100,v75
M 30,a[144:147],58
v_fma_f32 v101,v76,v101,v75
v_mul_f32 v98,v22,v98
M 30,a[156:159],62
s_cmp_eq_u32 vcc_lo,-1
s_nop 0
v_mul_f32 v99,v23,v99
s_cbranch_scc0 Lg3_X1_%=
M 30,a[168:171],66
s_add_u32 m0,s57,0
s_add_u32 s56,s55,0
s_add_u32 s58,s58,s61
R 54,2,19456
M 30,a[180:183],70
buffer_load_dword v114,v11,s[36:39],s73 offen offset:0 sc1
M 34,a[172:175],66
D 0,56,0
v_mul_f32 v100,v24,v100
M 34,a[184:187],70
s_add_u32 s54,s54,s46
R 58,2,20480
v_mul_f32 v101,v25,v101
M 38,a[176:179],66
s_add_u32 s73,s73,s64
v_exp_f32 v98,v98
M 38,a[188:191],70
L 3
v_exp_f32 v99,v99
M 38,a[200:203],42
R 62,2,21504
v_fma_f32 v98,v98,v77,v77
M 38,a[212:215],46
D 0,56,1024
v_fma_f32 v99,v99,v77,v77
M 34,a[196:199],42
v_exp_f32 v100,v100
M 34,a[208:211],46
R 66,2,22528
v_fma_f32 v100,v100,v77,v77
M 30,a[192:195],42
D 0,56,2048
M 30,a[204:207],46
L 3
v_exp_f32 v101,v101
)"
R"(M 30,a[216:219],50
R 70,2,23552
v_rcp_f32 v98,v98
M 30,a[228:231],54
v_rcp_f32 v99,v99
M 34,a[220:223],50
D 0,56,3072
M 34,a[232:235],54
s_add_u32 m0,s59,1024
R 42,2,24576
v_fma_f32 v101,v101,v77,v77
M 38,a[224:227],50
buffer_load_dword v8,s[36:39],s58 offen offset:0 sc1 lds
M 38,a[236:239],54
L 3
v_rcp_f32 v100,v100
v_mul_f32 v22,v22,v98
M 38,a[248:251],58
s_add_u32 m0,s57,16384
R 46,2,25600
v_mul_f32 v23,v23,v99
M 38,v[132:135],62
s_add_u32 s56,s55,16384
v_rcp_f32 v101,v101
M 34,a[244:247],58
D 0,56,0
M 34,v[128:131],62
s_add_u32 s55,s55,s60
R 50,2,26624
v_mul_f32 v24,v24,v100
M 30,a[240:243],58
s_sub_u32 s57,s84,s57
v_mul_f32 v25,v25,v101
v_cvt_pk_bf16_f32 v108,v22,v23
v_cvt_pk_bf16_f32 v109,v24,v25
M 30,a[252:255],62
L 3
buffer_store_dwordx4 v[106:109],v0,s[36:39],s66 offen offset:0 sc0 sc1
v_fma_f32 v26,-v74,v90,v26
M 30,v[136:139],66
R 54,2,27648
v_fma_f32 v27,-v74,v91,v27
M 30,v[148:151],70
v_fma_f32 v28,-v74,v92,v28
v_fma_f32 v29,-v74,v93,v29
M 34,v[140:143],66
D 0,56,1024
M 34,v[152:155],70
R 58,2,28672
v_mul_f32 v102,v26,v26
v_mul_f32 v103,v27,v27
M 38,v[144:147],66
v_mul_f32 v104,v28,v28
v_mul_f32 v105,v29,v29
M 38,v[156:159],70
L 3
v_fma_f32 v102,v76,v102,v75
v_fma_f32 v103,v76,v103,v75
M 38,v[168:171],42
R 62,2,29696
v_fma_f32 v104,v76,v104,v75
M 38,v[180:183],46
D 0,56,2048
M 34,v[164:167],42
v_fma_f32 v105,v76,v105,v75
v_mul_f32 v102,v26,v102
v_mul_f32 v103,v27,v103
M 34,v[176:179],46
R 66,2,30720
v_mul_f32 v104,v28,v104
M 30,v[160:163],42
v_exp_f32 v102,v102
M 30,v[172:175],46
L 3
D 0,56,3072
M 30,v[184:187],50
s_nop 0
R 70,2,31744
v_mul_f32 v105,v29,v105
M 30,v[196:199],54
s_waitcnt vmcnt(5)
v_exp_f32 v103,v103
v_fma_f32 v102,v102,v77,v77
M 34,v[188:191],50
s_barrier
buffer_store_dword v4,v3,s[36:39],s70 offen offset:0 sc0 sc1
M 34,v[200:203],54
s_mov_b32 s70,s69
R 42,1,0
ds_read_b32 v7,v6 offset:1024
M 38,v[192:195],50
s_mov_b32 s69,s68
R 78,13,2048
v_fma_f32 v103,v103,v77,v77
M 38,v[204:207],54
L 5
v_exp_f32 v104,v104
M 38,v[216:219],58
s_add_u32 s68,s68,s63
R 46,1,1024
v_exp_f32 v105,v105
M 38,v[228:231],62
v_rcp_f32 v102,v102
M 34,v[212:215],58
v_rcp_f32 v103,v103
M 34,v[224:227],62
L 2
R 50,1,2048
v_fma_f32 v104,v104,v77,v77
M 30,v[208:211],58
v_rcp_f32 v104,v104
M 30,v[220:223],62
s_nop 0
v_fma_f32 v105,v105,v77,v77
v_rcp_f32 v105,v105
M 30,v[232:235],66
R 54,1,3072
v_mul_f32 v26,v26,v102
M 30,v[244:247],70
v_mul_f32 v27,v27,v103
v_mul_f32 v28,v28,v104
M 34,v[236:239],66
v_cmp_eq_u32 vcc,s53,v7
v_mul_f32 v29,v29,v105
M 34,v[248:251],70
R 58,1,4096
v_cvt_pk_bf16_f32 v110,v26,v27
v_cvt_pk_bf16_f32 v111,v28,v29
M 38,v[240:243],66
L 3
s_cmp_eq_u32 vcc_lo,-1
buffer_store_dwordx2 v[110:111],v10,s[36:39],s66 offen offset:0 sc0 sc1
M 38,v[252:255],70
buffer_store_dword v4,v3,s[36:39],s71 offen offset:0 sc0 sc1
s_cbranch_scc1 Lg3_F0_%=
s_branch Lg3_K0_%=
Lg3_X1_%=:
M 30,a[168:171],66
s_add_u32 m0,s59,1024
R 54,2,19456
v_mul_f32 v100,v24,v100
M 30,a[180:183],70
buffer_load_dword v8,s[36:39],s58 offen offset:0 sc1 lds
M 34,a[172:175],66
v_exp_f32 v98,v98
M 34,a[184:187],70
R 58,2,20480
v_mul_f32 v101,v25,v101
M 38,a[176:179],66
v_exp_f32 v99,v99
M 38,a[188:191],70
L 3
v_exp_f32 v100,v100
M 38,a[200:203],42
R 62,2,21504
v_fma_f32 v98,v98,v77,v77
M 38,a[212:215],46
v_exp_f32 v101,v101
M 34,a[196:199],42
v_rcp_f32 v98,v98
M 34,a[208:211],46
R 66,2,22528
v_fma_f32 v99,v99,v77,v77
M 30,a[192:195],42
v_rcp_f32 v99,v99
M 30,a[204:207],46
L 3
v_fma_f32 v100,v100,v77,v77
v_fma_f32 v101,v101,v77,v77
M 30,a[216:219],50
R 70,2,23552
v_mul_f32 v22,v22,v98
M 30,a[228:231],54
v_rcp_f32 v100,v100
M 34,a[220:223],50
v_rcp_f32 v101,v101
M 34,a[232:235],54
R 42,2,24576
v_mul_f32 v23,v23,v99
M 38,a[224:227],50
v_mul_f32 v24,v24,v100
v_mul_f32 v25,v25,v101
M 38,a[236:239],54
L 3
v_cvt_pk_bf16_f32 v108,v22,v23
v_cvt_pk_bf16_f32 v109,v24,v25
M 38,a[248:251],58
R 46,2,25600
v_fma_f32 v26,-v74,v90,v26
M 38,v[132:135],62
buffer_store_dwordx4 v[106:109],v0,s[36:39],s66 offen offset:0 sc0 sc1
v_fma_f32 v27,-v74,v91,v27
M 34,a[244:247],58
v_fma_f32 v28,-v74,v92,v28
v_fma_f32 v29,-v74,v93,v29
M 34,v[128:131],62
R 50,2,26624
v_mul_f32 v102,v26,v26
M 30,a[240:243],58
v_mul_f32 v103,v27,v27
v_mul_f32 v104,v28,v28
M 30,a[252:255],62
L 3
v_mul_f32 v105,v29,v29
v_fma_f32 v102,v76,v102,v75
M 30,v[136:139],66
R 54,2,27648
v_fma_f32 v103,v76,v103,v75
M 30,v[148:151],70
v_fma_f32 v104,v76,v104,v75
v_fma_f32 v105,v76,v105,v75
M 34,v[140:143],66
v_mul_f32 v102,v26,v102
v_mul_f32 v103,v27,v103
M 34,v[152:155],70
R 58,2,28672
v_mul_f32 v104,v28,v104
M 38,v[144:147],66
v_exp_f32 v102,v102
M 38,v[156:159],70
L 3
v_exp_f32 v103,v103
M 38,v[168:171],42
R 62,2,29696
v_mul_f32 v105,v29,v105
M 38,v[180:183],46
v_exp_f32 v104,v104
M 34,v[164:167],42
v_exp_f32 v105,v105
M 34,v[176:179],46
R 66,2,30720
v_fma_f32 v102,v102,v77,v77
M 30,v[160:163],42
v_rcp_f32 v102,v102
M 30,v[172:175],46
L 3
v_fma_f32 v103,v103,v77,v77
v_fma_f32 v104,v104,v77,v77
M 30,v[184:187],50
s_nop 0
R 70,2,31744
v_fma_f32 v105,v105,v77,v77
M 30,v[196:199],54
s_waitcnt vmcnt(1)
v_rcp_f32 v103,v103
M 34,v[188:191],50
s_barrier
buffer_store_dword v4,v3,s[36:39],s70 offen offset:0 sc0 sc1
M 34,v[200:203],54
s_mov_b32 s70,s69
ds_read_b32 v7,v6 offset:1024
R 78,13,2048
M 38,v[192:195],50
s_mov_b32 s69,s68
v_rcp_f32 v104,v104
M 38,v[204:207],54
L 4
v_rcp_f32 v105,v105
M 38,v[216:219],58
s_add_u32 s68,s68,s63
v_mul_f32 v26,v26,v102
v_mul_f32 v27,v27,v103
M 38,v[228:231],62
v_mul_f32 v28,v28,v104
v_mul_f32 v29,v29,v105
M 34,v[212:215],58
v_cvt_pk_bf16_f32 v110,v26,v27
v_cvt_pk_bf16_f32 v111,v28,v29
M 34,v[224:227],62
L 1
buffer_store_dwordx2 v[110:111],v10,s[36:39],s66 offen offset:0 sc0 sc1
M 30,v[208:211],58
v_cmp_eq_u32 vcc,s53,v7
M 30,v[220:223],62
s_nop 0
M 30,v[232:235],66
M 30,v[244:247],70
M 34,v[236:239],66
M 34,v[248:251],70
M 38,v[240:243],66
M 38,v[252:255],70
s_mov_b32 s80,0
s_cmp_eq_u32 vcc_lo,-1
s_cbranch_scc0 Lg3_xn1_%=
buffer_load_dword v114,v11,s[36:39],s73 offen offset:0 sc1
L 0
v_fma_f32 v30,-v78,v82,v30
v_fma_f32 v31,-v78,v83,v31
v_fma_f32 v32,-v78,v84,v32
v_fma_f32 v33,-v78,v85,v33
v_mul_f32 v94,v30,v30
v_mul_f32 v95,v31,v31
v_mul_f32 v96,v32,v32
v_mul_f32 v97,v33,v33
s_add_u32 s73,s73,s64
s_add_u32 m0,s57,0
s_add_u32 s56,s55,0
D 0,56,0
v_fma_f32 v94,v80,v94,v79
v_fma_f32 v95,v80,v95,v79
v_fma_f32 v96,v80,v96,v79
v_fma_f32 v97,v80,v97,v79
v_mul_f32 v94,v30,v94
v_mul_f32 v95,v31,v95
v_mul_f32 v96,v32,v96
v_mul_f32 v97,v33,v97
D 0,56,1024
v_exp_f32 v94,v94
v_exp_f32 v95,v95
v_exp_f32 v96,v96
v_exp_f32 v97,v97
v_fma_f32 v94,v94,v81,v81
v_fma_f32 v95,v95,v81,v81
v_fma_f32 v96,v96,v81,v81
v_fma_f32 v97,v97,v81,v81
D 0,56,2048
v_rcp_f32 v94,v94
v_rcp_f32 v95,v95
v_rcp_f32 v96,v96
v_rcp_f32 v97,v97
v_mul_f32 v30,v30,v94
v_mul_f32 v31,v31,v95
v_mul_f32 v32,v32,v96
v_mul_f32 v33,v33,v97
D 0,56,3072
v_cvt_pk_bf16_f32 v106,v30,v31
v_cvt_pk_bf16_f32 v107,v32,v33
v_fma_f32 v34,-v78,v86,v34
v_fma_f32 v35,-v78,v87,v35
v_fma_f32 v36,-v78,v88,v36
v_fma_f32 v37,-v78,v89,v37
v_mul_f32 v98,v34,v34
v_mul_f32 v99,v35,v35
s_add_u32 s58,s58,s61
s_add_u32 m0,s57,16384
s_add_u32 s56,s55,16384
D 0,56,0
v_mul_f32 v100,v36,v36
v_mul_f32 v101,v37,v37
v_fma_f32 v98,v80,v98,v79
v_fma_f32 v99,v80,v99,v79
v_fma_f32 v100,v80,v100,v79
v_fma_f32 v101,v80,v101,v79
v_mul_f32 v98,v34,v98
v_mul_f32 v99,v35,v99
D 0,56,1024
v_mul_f32 v100,v36,v100
v_mul_f32 v101,v37,v101
v_exp_f32 v98,v98
v_exp_f32 v99,v99
v_exp_f32 v100,v100
v_exp_f32 v101,v101
v_fma_f32 v98,v98,v81,v81
v_fma_f32 v99,v99,v81,v81
D 0,56,2048
v_fma_f32 v100,v100,v81,v81
v_fma_f32 v101,v101,v81,v81
v_rcp_f32 v98,v98
v_rcp_f32 v99,v99
v_rcp_f32 v100,v100
v_rcp_f32 v101,v101
v_mul_f32 v34,v34,v98
v_mul_f32 v35,v35,v99
D 0,56,3072
v_mul_f32 v36,v36,v100
v_mul_f32 v37,v37,v101
v_cvt_pk_bf16_f32 v108,v34,v35
v_cvt_pk_bf16_f32 v109,v36,v37
buffer_store_dwordx4 v[106:109],v0,s[36:39],s67 offen offset:0 sc0 sc1
v_fma_f32 v38,-v78,v90,v38
v_fma_f32 v39,-v78,v91,v39
v_fma_f32 v40,-v78,v92,v40
v_fma_f32 v41,-v78,v93,v41
s_add_u32 s55,s55,s60
s_sub_u32 s57,s84,s57
s_add_u32 s54,s54,s46
v_mul_f32 v102,v38,v38
v_mul_f32 v103,v39,v39
v_mul_f32 v104,v40,v40
v_mul_f32 v105,v41,v41
v_fma_f32 v102,v80,v102,v79
v_fma_f32 v103,v80,v103,v79
v_fma_f32 v104,v80,v104,v79
v_fma_f32 v105,v80,v105,v79
v_mul_f32 v102,v38,v102
v_mul_f32 v103,v39,v103
v_mul_f32 v104,v40,v104
v_mul_f32 v105,v41,v105
v_exp_f32 v102,v102
v_exp_f32 v103,v103
v_exp_f32 v104,v104
v_exp_f32 v105,v105
v_fma_f32 v102,v102,v81,v81
v_fma_f32 v103,v103,v81,v81
v_fma_f32 v104,v104,v81,v81
v_fma_f32 v105,v105,v81,v81
v_rcp_f32 v102,v102
v_rcp_f32 v103,v103
v_rcp_f32 v104,v104
v_rcp_f32 v105,v105
v_mul_f32 v38,v38,v102
v_mul_f32 v39,v39,v103
v_mul_f32 v40,v40,v104
v_mul_f32 v41,v41,v105
v_cvt_pk_bf16_f32 v110,v38,v39
v_cvt_pk_bf16_f32 v111,v40,v41
buffer_store_dwordx2 v[110:111],v10,s[36:39],s67 offen offset:0 sc0 sc1
s_mov_b32 s72,s69
s_mov_b32 s69,0x7fff0000
s_mov_b32 s67,0x7fff0000
s_mov_b32 s77,0x7fff0000
s_branch Lg3_gox_%=
Lg3_xn1_%=:
s_cmp_ge_u32 s54,256
s_cbranch_scc1 Lg3_fin1_%=
s_add_u32 m0,s59,4096
s_nop 0
buffer_load_dword v8,s[36:39],s58 offen offset:0 sc1 lds
buffer_store_dword v4,v3,s[36:39],s71 offen offset:0 sc0 sc1
s_add_u32 m0,s59,5120
s_nop 0
buffer_load_dword v8,s[36:39],s58 offen offset:0 sc1 lds
buffer_store_dword v4,v3,s[36:39],s71 offen offset:0 sc0 sc1
s_mov_b32 s83,0
L 0
v_fma_f32 v30,-v78,v82,v30
v_fma_f32 v31,-v78,v83,v31
v_fma_f32 v32,-v78,v84,v32
v_fma_f32 v33,-v78,v85,v33
v_mul_f32 v94,v30,v30
v_mul_f32 v95,v31,v31
v_mul_f32 v96,v32,v32
v_mul_f32 v97,v33,v33
v_fma_f32 v94,v80,v94,v79
v_fma_f32 v95,v80,v95,v79
v_fma_f32 v96,v80,v96,v79
v_fma_f32 v97,v80,v97,v79
v_mul_f32 v94,v30,v94
v_mul_f32 v95,v31,v95
v_mul_f32 v96,v32,v96
v_mul_f32 v97,v33,v97
v_exp_f32 v94,v94
v_exp_f32 v95,v95
v_exp_f32 v96,v96
v_exp_f32 v97,v97
v_fma_f32 v94,v94,v81,v81
v_fma_f32 v95,v95,v81,v81
v_fma_f32 v96,v96,v81,v81
v_fma_f32 v97,v97,v81,v81
v_rcp_f32 v94,v94
v_rcp_f32 v95,v95
v_rcp_f32 v96,v96
v_rcp_f32 v97,v97
v_mul_f32 v30,v30,v94
v_mul_f32 v31,v31,v95
v_mul_f32 v32,v32,v96
v_mul_f32 v33,v33,v97
v_cvt_pk_bf16_f32 v106,v30,v31
v_cvt_pk_bf16_f32 v107,v32,v33
v_fma_f32 v34,-v78,v86,v34
v_fma_f32 v35,-v78,v87,v35
v_fma_f32 v36,-v78,v88,v36
v_fma_f32 v37,-v78,v89,v37
v_mul_f32 v98,v34,v34
v_mul_f32 v99,v35,v35
v_mul_f32 v100,v36,v36
v_mul_f32 v101,v37,v37
v_fma_f32 v98,v80,v98,v79
v_fma_f32 v99,v80,v99,v79
v_fma_f32 v100,v80,v100,v79
v_fma_f32 v101,v80,v101,v79
v_mul_f32 v98,v34,v98
v_mul_f32 v99,v35,v99
v_mul_f32 v100,v36,v100
v_mul_f32 v101,v37,v101
v_exp_f32 v98,v98
v_exp_f32 v99,v99
v_exp_f32 v100,v100
v_exp_f32 v101,v101
v_fma_f32 v98,v98,v81,v81
v_fma_f32 v99,v99,v81,v81
v_fma_f32 v100,v100,v81,v81
v_fma_f32 v101,v101,v81,v81
v_rcp_f32 v98,v98
v_rcp_f32 v99,v99
v_rcp_f32 v100,v100
v_rcp_f32 v101,v101
v_mul_f32 v34,v34,v98
v_mul_f32 v35,v35,v99
v_mul_f32 v36,v36,v100
v_mul_f32 v37,v37,v101
v_cvt_pk_bf16_f32 v108,v34,v35
v_cvt_pk_bf16_f32 v109,v36,v37
buffer_store_dwordx4 v[106:109],v0,s[36:39],s67 offen offset:0 sc0 sc1
v_fma_f32 v38,-v78,v90,v38
v_fma_f32 v39,-v78,v91,v39
v_fma_f32 v40,-v78,v92,v40
v_fma_f32 v41,-v78,v93,v41
v_mul_f32 v102,v38,v38
v_mul_f32 v103,v39,v39
)"
R"(v_mul_f32 v104,v40,v40
v_mul_f32 v105,v41,v41
v_fma_f32 v102,v80,v102,v79
v_fma_f32 v103,v80,v103,v79
v_fma_f32 v104,v80,v104,v79
v_fma_f32 v105,v80,v105,v79
v_mul_f32 v102,v38,v102
v_mul_f32 v103,v39,v103
v_mul_f32 v104,v40,v104
v_mul_f32 v105,v41,v105
v_exp_f32 v102,v102
v_exp_f32 v103,v103
v_exp_f32 v104,v104
v_exp_f32 v105,v105
v_fma_f32 v102,v102,v81,v81
v_fma_f32 v103,v103,v81,v81
v_fma_f32 v104,v104,v81,v81
v_fma_f32 v105,v105,v81,v81
v_rcp_f32 v102,v102
v_rcp_f32 v103,v103
v_rcp_f32 v104,v104
v_rcp_f32 v105,v105
v_mul_f32 v38,v38,v102
v_mul_f32 v39,v39,v103
v_mul_f32 v40,v40,v104
v_mul_f32 v41,v41,v105
v_cvt_pk_bf16_f32 v110,v38,v39
v_cvt_pk_bf16_f32 v111,v40,v41
buffer_store_dwordx2 v[110:111],v10,s[36:39],s67 offen offset:0 sc0 sc1
s_mov_b32 s72,s69
s_mov_b32 s69,0x7fff0000
s_mov_b32 s67,0x7fff0000
s_mov_b32 s77,0x7fff0000
s_branch Lg3_poll_%=
Lg3_fin1_%=:
L 0
v_fma_f32 v30,-v78,v82,v30
v_fma_f32 v31,-v78,v83,v31
v_fma_f32 v32,-v78,v84,v32
v_fma_f32 v33,-v78,v85,v33
v_mul_f32 v94,v30,v30
v_mul_f32 v95,v31,v31
v_mul_f32 v96,v32,v32
v_mul_f32 v97,v33,v33
v_fma_f32 v94,v80,v94,v79
v_fma_f32 v95,v80,v95,v79
v_fma_f32 v96,v80,v96,v79
v_fma_f32 v97,v80,v97,v79
v_mul_f32 v94,v30,v94
v_mul_f32 v95,v31,v95
v_mul_f32 v96,v32,v96
v_mul_f32 v97,v33,v97
v_exp_f32 v94,v94
v_exp_f32 v95,v95
v_exp_f32 v96,v96
v_exp_f32 v97,v97
v_fma_f32 v94,v94,v81,v81
v_fma_f32 v95,v95,v81,v81
v_fma_f32 v96,v96,v81,v81
v_fma_f32 v97,v97,v81,v81
v_rcp_f32 v94,v94
v_rcp_f32 v95,v95
v_rcp_f32 v96,v96
v_rcp_f32 v97,v97
v_mul_f32 v30,v30,v94
v_mul_f32 v31,v31,v95
v_mul_f32 v32,v32,v96
v_mul_f32 v33,v33,v97
v_cvt_pk_bf16_f32 v106,v30,v31
v_cvt_pk_bf16_f32 v107,v32,v33
v_fma_f32 v34,-v78,v86,v34
v_fma_f32 v35,-v78,v87,v35
v_fma_f32 v36,-v78,v88,v36
v_fma_f32 v37,-v78,v89,v37
v_mul_f32 v98,v34,v34
v_mul_f32 v99,v35,v35
v_mul_f32 v100,v36,v36
v_mul_f32 v101,v37,v37
v_fma_f32 v98,v80,v98,v79
v_fma_f32 v99,v80,v99,v79
v_fma_f32 v100,v80,v100,v79
v_fma_f32 v101,v80,v101,v79
v_mul_f32 v98,v34,v98
v_mul_f32 v99,v35,v99
v_mul_f32 v100,v36,v100
v_mul_f32 v101,v37,v101
v_exp_f32 v98,v98
v_exp_f32 v99,v99
v_exp_f32 v100,v100
v_exp_f32 v101,v101
v_fma_f32 v98,v98,v81,v81
v_fma_f32 v99,v99,v81,v81
v_fma_f32 v100,v100,v81,v81
v_fma_f32 v101,v101,v81,v81
v_rcp_f32 v98,v98
v_rcp_f32 v99,v99
v_rcp_f32 v100,v100
v_rcp_f32 v101,v101
v_mul_f32 v34,v34,v98
v_mul_f32 v35,v35,v99
v_mul_f32 v36,v36,v100
v_mul_f32 v37,v37,v101
v_cvt_pk_bf16_f32 v108,v34,v35
v_cvt_pk_bf16_f32 v109,v36,v37
buffer_store_dwordx4 v[106:109],v0,s[36:39],s67 offen offset:0 sc0 sc1
v_fma_f32 v38,-v78,v90,v38
v_fma_f32 v39,-v78,v91,v39
v_fma_f32 v40,-v78,v92,v40
v_fma_f32 v41,-v78,v93,v41
v_mul_f32 v102,v38,v38
v_mul_f32 v103,v39,v39
v_mul_f32 v104,v40,v40
v_mul_f32 v105,v41,v41
v_fma_f32 v102,v80,v102,v79
v_fma_f32 v103,v80,v103,v79
v_fma_f32 v104,v80,v104,v79
v_fma_f32 v105,v80,v105,v79
v_mul_f32 v102,v38,v102
v_mul_f32 v103,v39,v103
v_mul_f32 v104,v40,v104
v_mul_f32 v105,v41,v105
v_exp_f32 v102,v102
v_exp_f32 v103,v103
v_exp_f32 v104,v104
v_exp_f32 v105,v105
v_fma_f32 v102,v102,v81,v81
v_fma_f32 v103,v103,v81,v81
v_fma_f32 v104,v104,v81,v81
v_fma_f32 v105,v105,v81,v81
v_rcp_f32 v102,v102
v_rcp_f32 v103,v103
v_rcp_f32 v104,v104
v_rcp_f32 v105,v105
v_mul_f32 v38,v38,v102
v_mul_f32 v39,v39,v103
v_mul_f32 v40,v40,v104
v_mul_f32 v41,v41,v105
v_cvt_pk_bf16_f32 v110,v38,v39
v_cvt_pk_bf16_f32 v111,v40,v41
buffer_store_dwordx2 v[110:111],v10,s[36:39],s67 offen offset:0 sc0 sc1
s_mov_b32 s72,s69
s_mov_b32 s69,0x7fff0000
s_mov_b32 s67,0x7fff0000
s_mov_b32 s77,0x7fff0000
s_branch Lg3_done_%=
Lg3_done_%=:
s_waitcnt vmcnt(2)
s_barrier
buffer_store_dword v4,v3,s[36:39],s70 offen offset:0 sc0 sc1
s_waitcnt vmcnt(1)
s_barrier
buffer_store_dword v4,v3,s[36:39],s72 offen offset:0 sc0 sc1
s_waitcnt vmcnt(0) lgkmcnt(0)
s_barrier
s_endpgm
Lcode_g3_e:
.purgem M
.purgem R
.purgem T
.purgem D
.purgem L
)"
  :: "s"(ws), "s"(__builtin_amdgcn_readfirstlane(sWave)), "s"(__builtin_amdgcn_readfirstlane(sG0)), "s"(__builtin_amdgcn_readfirstlane(sStep)), "s"(__builtin_amdgcn_readfirstlane(sOutOff)), "s"(__builtin_amdgcn_readfirstlane(sFoutOff)), "s"(__builtin_amdgcn_readfirstlane(sAuxOff)), "s"(__builtin_amdgcn_readfirstlane(sWw)), "s"(__builtin_amdgcn_readfirstlane(sBuf)), "s"(__builtin_amdgcn_readfirstlane(sExp)), "v"(vL16), "v"(vFlag), "v"(vSnapOff), "v"(vSnapRd), "v"(vWd0), "v"(vWd1), "v"(vWr0), "v"(vWr1), "v"(vWr2), "v"(vOutS), "v"(vAux0), "v"(vAux1), "v"(vAux2), "v"(vAux3), "v"(vAn), "v"(vAe), "s"(w3), "s"(out) : "v255", "v0", "v1", "v2", "v3", "v4", "v5", "v6", "v7", "v8", "v9", "v10", "v11", "v12", "v13", "v14", "v15", "v16", "v17", "v18", "v19", "v20", "v21", "v22", "v23", "v24", "v25", "v26", "v27", "v28", "v29", "v30", "v31", "v32", "v33", "v34", "v35", "v36", "v37", "v38", "v39", "v40", "v41", "v42", "v43", "v44", "v45", "v46", "v47", "v48", "v49", "v50", "v51", "v52", "v53", "v54", "v55", "v56", "v57", "v58", "v59", "v60", "v61", "v62", "v63", "v64", "v65", "v66", "v67", "v68", "v69", "v70", "v71", "v72", "v73", "v74", "v75", "v76", "v77", "v78", "v79", "v80", "v81", "v82", "v83", "v84", "v85", "v86", "v87", "v88", "v89", "v90", "v91", "v92", "v93", "v94", "v95", "v96", "v97", "v98", "v99", "v100", "v101", "v102", "v103", "v104", "v105", "v106", "v107", "v108", "v109", "v110", "v111", "v112", "v113", "v114", "v115", "v116", "v117", "v118", "v119", "v120", "v121", "v122", "v123", "v124", "v125", "v126", "v127", "v128", "v129", "v130", "v131", "v132", "v133", "v134", "v135", "v136", "v137", "v138", "v139", "v140", "v141", "v142", "v143", "v144", "v145", "v146", "v147", "v148", "v149", "v150", "v151", "v152", "v153", "v154", "v155", "v156", "v157", "v158", "v159", "v160", "v161", "v162", "v163", "v164", "v165", "v166", "v167", "v168", "v169", "v170", "v171", "v172", "v173", "v174", "v175", "v176", "v177", "v178", "v179", "v180", "v181", "v182", "v183", "v184", "v185", "v186", "v187", "v188", "v189", "v190", "v191", "v192", "v193", "v194", "v195", "v196", "v197", "v198", "v199", "v200", "v201", "v202", "v203", "v204", "v205", "v206", "v207", "v208", "v209", "v210", "v211", "v212", "v213", "v214", "v215", "v216", "v217", "v218", "v219", "v220", "v221", "v222", "v223", "v224", "v225", "v226", "v227", "v228", "v229", "v230", "v231", "a0", "a1", "a2", "a3", "a4", "a5", "a6", "a7", "a8", "a9", "a10", "a11", "a12", "a13", "a14", "a15", "a16", "a17", "a18", "a19", "a20", "a21", "a22", "a23", "a24", "a25", "a26", "a27", "a28", "a29", "a30", "a31", "a32", "a33", "a34", "a35", "a36", "a37", "a38", "a39", "a40", "a41", "a42", "a43", "a44", "a45", "a46", "a47", "a48", "a49", "a50", "a51", "a52", "a53", "a54", "a55", "a56", "a57", "a58", "a59", "a60", "a61", "a62", "a63", "a64", "a65", "a66", "a67", "a68", "a69", "a70", "a71", "a72", "a73", "a74", "a75", "a76", "a77", "a78", "a79", "a80", "a81", "a82", "a83", "a84", "a85", "a86", "a87", "a88", "a89", "a90", "a91", "a92", "a93", "a94", "a95", "a96", "a97", "a98", "a99", "a100", "a101", "a102", "a103", "a104", "a105", "a106", "a107", "a108", "a109", "a110", "a111", "a112", "a113", "a114", "a115", "a116", "a117", "a118", "a119", "a120", "a121", "a122", "a123", "a124", "a125", "a126", "a127", "a128", "a129", "a130", "a131", "a132", "a133", "a134", "a135", "a136", "a137", "a138", "a139", "a140", "a141", "a142", "a143", "a144", "a145", "a146", "a147", "a148", "a149", "a150", "a151", "a152", "a153", "a154", "a155", "a156", "a157", "a158", "a159", "a160", "a161", "a162", "a163", "a164", "a165", "a166", "a167", "a168", "a169", "a170", "a171", "a172", "a173", "a174", "a175", "a176", "a177", "a178", "a179", "a180", "a181", "a182", "a183", "a184", "a185", "a186", "a187", "a188", "a189", "a190", "a191", "a192", "a193", "a194", "a195", "a196", "a197", "a198", "a199", "a200", "a201", "a202", "a203", "a204", "a205", "a206", "a207", "a208", "a209", "a210", "a211", "a212", "a213", "a214", "a215", "a216", "a217", "a218", "a219", "a220", "a221", "a222", "a223", "a224", "a225", "a226", "a227", "a228", "a229", "a230", "a231", "a232", "a233", "a234", "a235", "a236", "a237", "a238", "a239", "a240", "a241", "a242", "a243", "a244", "a245", "a246", "a247", "a248", "a249", "a250", "a251", "a252", "a253", "a254", "a255", "s36", "s37", "s38", "s39", "s40", "s41", "s42", "s43", "s44", "s45", "s46", "s47", "s48", "s49", "s50", "s51", "s52", "s53", "s54", "s55", "s56", "s57", "s58", "s59", "s60", "s61", "s62", "s63", "s64", "s65", "s66", "s67", "s68", "s69", "s70", "s71", "s72", "s73", "s74", "s75", "s76", "s77", "s78", "s79", "s80", "s81", "s82", "s83", "s84", "s85", "s86", "s87", "s88", "s89", "s90", "s91", "s92", "s93", "s94", "s95", "s96", "s97", "vcc", "scc", "memory"); } else { asm volatile(
R"(.macro M d,w,x
v_mfma_f32_16x16x32_bf16 v[\d:\d+3], \w, v[\x:\x+3], v[\d:\d+3]
.endm
.macro R d,a,o
ds_read_b128 v[\d:\d+3], v\a offset:\o
.endm
.macro T d,a,o
ds_read_b64_tr_b16 \d, v\a offset:\o
.endm
.macro D a,s,o
buffer_load_dwordx4 v\a, s[36:39], s\s offen offset:\o sc1 lds
.endm
.macro L c
s_waitcnt lgkmcnt(\c)
.endm
Lcode_g3o_b:
s_mov_b64 s[36:37],%0
s_mov_b32 s38,111243392
s_mov_b32 s39,0x00020000
s_mov_b32 s44,%1
s_mov_b32 s45,%2
s_mov_b32 s46,%3
s_mov_b32 s47,%4
s_mov_b32 s48,%5
s_mov_b32 s49,%6
s_mov_b32 s50,%7
s_mov_b32 s51,%8
s_mov_b32 s53,%9
v_mov_b32 v0,%10
v_mov_b32 v3,%11
v_mov_b32 v5,%12
v_mov_b32 v6,%13
v_mov_b32 v18,%14
v_mov_b32 v19,%15
v_mov_b32 v20,%16
v_mov_b32 v21,%17
v_mov_b32 v22,%18
v_mov_b32 v10,%19
v_mov_b32 v11,%20
v_mov_b32 v12,%21
v_mov_b32 v13,%22
v_mov_b32 v14,%23
v_mov_b32 v15,%24
v_mov_b32 v16,%25
s_mov_b64 s[40:41],%26
s_mov_b32 s42,7340032
s_mov_b32 s43,0x00020000
s_lshl_b32 s57,s44,11
s_add_u32 s55,s50,0
s_add_u32 m0,s57,0
s_nop 0
buffer_load_dwordx4 v18,s[40:43],s55 offen offset:0  lds
s_add_u32 m0,s57,1024
s_nop 0
buffer_load_dwordx4 v19,s[40:43],s55 offen offset:0  lds
s_add_u32 m0,s57,8064
s_nop 0
buffer_load_dwordx4 v18,s[40:43],s55 offen offset:128  lds
s_add_u32 m0,s57,9088
s_nop 0
buffer_load_dwordx4 v19,s[40:43],s55 offen offset:128  lds
s_add_u32 s55,s50,458752
s_add_u32 m0,s57,16384
s_nop 0
buffer_load_dwordx4 v18,s[40:43],s55 offen offset:0  lds
s_add_u32 m0,s57,17408
s_nop 0
buffer_load_dwordx4 v19,s[40:43],s55 offen offset:0  lds
s_add_u32 m0,s57,24448
s_nop 0
buffer_load_dwordx4 v18,s[40:43],s55 offen offset:128  lds
s_add_u32 m0,s57,25472
s_nop 0
buffer_load_dwordx4 v19,s[40:43],s55 offen offset:128  lds
s_add_u32 s55,s50,917504
s_add_u32 m0,s57,32768
s_nop 0
buffer_load_dwordx4 v18,s[40:43],s55 offen offset:0  lds
s_add_u32 m0,s57,33792
s_nop 0
buffer_load_dwordx4 v19,s[40:43],s55 offen offset:0  lds
s_add_u32 m0,s57,40832
s_nop 0
buffer_load_dwordx4 v18,s[40:43],s55 offen offset:128  lds
s_add_u32 m0,s57,41856
s_nop 0
buffer_load_dwordx4 v19,s[40:43],s55 offen offset:128  lds
s_add_u32 s55,s50,1376256
s_add_u32 m0,s57,49152
s_nop 0
buffer_load_dwordx4 v18,s[40:43],s55 offen offset:0  lds
s_add_u32 m0,s57,50176
s_nop 0
buffer_load_dwordx4 v19,s[40:43],s55 offen offset:0  lds
s_add_u32 m0,s57,57216
s_nop 0
buffer_load_dwordx4 v18,s[40:43],s55 offen offset:128  lds
s_add_u32 m0,s57,58240
s_nop 0
buffer_load_dwordx4 v19,s[40:43],s55 offen offset:128  lds
s_add_u32 s55,s50,1835008
s_add_u32 m0,s57,65536
s_nop 0
buffer_load_dwordx4 v18,s[40:43],s55 offen offset:0  lds
s_add_u32 m0,s57,66560
s_nop 0
buffer_load_dwordx4 v19,s[40:43],s55 offen offset:0  lds
s_add_u32 m0,s57,73600
s_nop 0
buffer_load_dwordx4 v18,s[40:43],s55 offen offset:128  lds
s_add_u32 m0,s57,74624
s_nop 0
buffer_load_dwordx4 v19,s[40:43],s55 offen offset:128  lds
s_add_u32 s55,s50,2293760
s_add_u32 m0,s57,81920
s_nop 0
buffer_load_dwordx4 v18,s[40:43],s55 offen offset:0  lds
s_add_u32 m0,s57,82944
s_nop 0
buffer_load_dwordx4 v19,s[40:43],s55 offen offset:0  lds
s_add_u32 m0,s57,89984
s_nop 0
buffer_load_dwordx4 v18,s[40:43],s55 offen offset:128  lds
s_add_u32 m0,s57,91008
s_nop 0
buffer_load_dwordx4 v19,s[40:43],s55 offen offset:128  lds
s_waitcnt vmcnt(20) lgkmcnt(0)
s_barrier
s_add_u32 s55,s50,2752512
s_add_u32 m0,s57,98304
s_nop 0
buffer_load_dwordx4 v18,s[40:43],s55 offen offset:0  lds
s_add_u32 m0,s57,99328
s_nop 0
buffer_load_dwordx4 v19,s[40:43],s55 offen offset:0  lds
s_add_u32 m0,s57,106368
s_nop 0
buffer_load_dwordx4 v18,s[40:43],s55 offen offset:128  lds
s_add_u32 m0,s57,107392
s_nop 0
buffer_load_dwordx4 v19,s[40:43],s55 offen offset:128  lds
v_add_u32 v26,0,v20
v_add_u32 v27,0,v21
T a[0:1],26,0
T a[2:3],26,512
T a[4:5],27,0
T a[6:7],27,512
T a[8:9],26,4096
T a[10:11],26,4608
T a[12:13],27,4096
T a[14:15],27,4608
s_waitcnt vmcnt(20) lgkmcnt(0)
s_barrier
s_add_u32 s55,s50,3211264
s_add_u32 m0,s57,0
s_nop 0
buffer_load_dwordx4 v18,s[40:43],s55 offen offset:0  lds
s_add_u32 m0,s57,1024
s_nop 0
buffer_load_dwordx4 v19,s[40:43],s55 offen offset:0  lds
s_add_u32 m0,s57,8064
s_nop 0
buffer_load_dwordx4 v18,s[40:43],s55 offen offset:128  lds
s_add_u32 m0,s57,9088
s_nop 0
buffer_load_dwordx4 v19,s[40:43],s55 offen offset:128  lds
v_add_u32 v26,16384,v20
v_add_u32 v27,16384,v21
T a[16:17],26,0
T a[18:19],26,512
T a[20:21],27,0
T a[22:23],27,512
T a[24:25],26,4096
T a[26:27],26,4608
T a[28:29],27,4096
T a[30:31],27,4608
s_waitcnt vmcnt(20) lgkmcnt(0)
s_barrier
s_add_u32 s55,s50,3670016
s_add_u32 m0,s57,16384
s_nop 0
buffer_load_dwordx4 v18,s[40:43],s55 offen offset:0  lds
s_add_u32 m0,s57,17408
s_nop 0
buffer_load_dwordx4 v19,s[40:43],s55 offen offset:0  lds
s_add_u32 m0,s57,24448
s_nop 0
buffer_load_dwordx4 v18,s[40:43],s55 offen offset:128  lds
s_add_u32 m0,s57,25472
s_nop 0
buffer_load_dwordx4 v19,s[40:43],s55 offen offset:128  lds
v_add_u32 v26,32768,v20
v_add_u32 v27,32768,v21
T a[32:33],26,0
T a[34:35],26,512
T a[36:37],27,0
T a[38:39],27,512
T a[40:41],26,4096
T a[42:43],26,4608
T a[44:45],27,4096
T a[46:47],27,4608
s_waitcnt vmcnt(20) lgkmcnt(0)
s_barrier
s_add_u32 s55,s50,4128768
s_add_u32 m0,s57,32768
s_nop 0
buffer_load_dwordx4 v18,s[40:43],s55 offen offset:0  lds
s_add_u32 m0,s57,33792
s_nop 0
buffer_load_dwordx4 v19,s[40:43],s55 offen offset:0  lds
s_add_u32 m0,s57,40832
s_nop 0
buffer_load_dwordx4 v18,s[40:43],s55 offen offset:128  lds
s_add_u32 m0,s57,41856
s_nop 0
buffer_load_dwordx4 v19,s[40:43],s55 offen offset:128  lds
v_add_u32 v26,49152,v20
v_add_u32 v27,49152,v21
T a[48:49],26,0
T a[50:51],26,512
T a[52:53],27,0
T a[54:55],27,512
T a[56:57],26,4096
T a[58:59],26,4608
T a[60:61],27,4096
T a[62:63],27,4608
s_waitcnt vmcnt(20) lgkmcnt(0)
s_barrier
s_add_u32 s55,s50,4587520
s_add_u32 m0,s57,49152
s_nop 0
buffer_load_dwordx4 v18,s[40:43],s55 offen offset:0  lds
s_add_u32 m0,s57,50176
s_nop 0
buffer_load_dwordx4 v19,s[40:43],s55 offen offset:0  lds
s_add_u32 m0,s57,57216
s_nop 0
buffer_load_dwordx4 v18,s[40:43],s55 offen offset:128  lds
s_add_u32 m0,s57,58240
s_nop 0
buffer_load_dwordx4 v19,s[40:43],s55 offen offset:128  lds
v_add_u32 v26,65536,v20
v_add_u32 v27,65536,v21
T a[64:65],26,0
T a[66:67],26,512
T a[68:69],27,0
T a[70:71],27,512
T a[72:73],26,4096
T a[74:75],26,4608
T a[76:77],27,4096
T a[78:79],27,4608
s_waitcnt vmcnt(20) lgkmcnt(0)
s_barrier
s_add_u32 s55,s50,5046272
s_add_u32 m0,s57,65536
s_nop 0
buffer_load_dwordx4 v18,s[40:43],s55 offen offset:0  lds
s_add_u32 m0,s57,66560
s_nop 0
buffer_load_dwordx4 v19,s[40:43],s55 offen offset:0  lds
s_add_u32 m0,s57,73600
s_nop 0
buffer_load_dwordx4 v18,s[40:43],s55 offen offset:128  lds
s_add_u32 m0,s57,74624
s_nop 0
buffer_load_dwordx4 v19,s[40:43],s55 offen offset:128  lds
v_add_u32 v26,81920,v20
v_add_u32 v27,81920,v21
T a[80:81],26,0
T a[82:83],26,512
T a[84:85],27,0
T a[86:87],27,512
T a[88:89],26,4096
T a[90:91],26,4608
T a[92:93],27,4096
T a[94:95],27,4608
s_waitcnt vmcnt(20) lgkmcnt(0)
s_barrier
s_add_u32 s55,s50,5505024
s_add_u32 m0,s57,81920
s_nop 0
buffer_load_dwordx4 v18,s[40:43],s55 offen offset:0  lds
s_add_u32 m0,s57,82944
s_nop 0
buffer_load_dwordx4 v19,s[40:43],s55 offen offset:0  lds
s_add_u32 m0,s57,89984
s_nop 0
buffer_load_dwordx4 v18,s[40:43],s55 offen offset:128  lds
s_add_u32 m0,s57,91008
s_nop 0
buffer_load_dwordx4 v19,s[40:43],s55 offen offset:128  lds
v_add_u32 v26,98304,v20
v_add_u32 v27,98304,v21
T a[96:97],26,0
T a[98:99],26,512
T a[100:101],27,0
T a[102:103],27,512
T a[104:105],26,4096
T a[106:107],26,4608
T a[108:109],27,4096
T a[110:111],27,4608
s_waitcnt vmcnt(20) lgkmcnt(0)
s_barrier
s_add_u32 s55,s50,5963776
s_add_u32 m0,s57,98304
s_nop 0
buffer_load_dwordx4 v18,s[40:43],s55 offen offset:0  lds
s_add_u32 m0,s57,99328
s_nop 0
buffer_load_dwordx4 v19,s[40:43],s55 offen offset:0  lds
s_add_u32 m0,s57,106368
s_nop 0
buffer_load_dwordx4 v18,s[40:43],s55 offen offset:128  lds
s_add_u32 m0,s57,107392
s_nop 0
buffer_load_dwordx4 v19,s[40:43],s55 offen offset:128  lds
v_add_u32 v26,0,v20
v_add_u32 v27,0,v21
T a[112:113],26,0
T a[114:115],26,512
T a[116:117],27,0
T a[118:119],27,512
T a[120:121],26,4096
T a[122:123],26,4608
T a[124:125],27,4096
T a[126:127],27,4608
s_waitcnt vmcnt(20) lgkmcnt(0)
s_barrier
s_add_u32 s55,s50,6422528
s_add_u32 m0,s57,0
s_nop 0
buffer_load_dwordx4 v18,s[40:43],s55 offen offset:0  lds
s_add_u32 m0,s57,1024
s_nop 0
buffer_load_dwordx4 v19,s[40:43],s55 offen offset:0  lds
s_add_u32 m0,s57,8064
s_nop 0
buffer_load_dwordx4 v18,s[40:43],s55 offen offset:128  lds
s_add_u32 m0,s57,9088
s_nop 0
buffer_load_dwordx4 v19,s[40:43],s55 offen offset:128  lds
v_add_u32 v26,16384,v20
v_add_u32 v27,16384,v21
T a[128:129],26,0
T a[130:131],26,512
T a[132:133],27,0
T a[134:135],27,512
T a[136:137],26,4096
T a[138:139],26,4608
T a[140:141],27,4096
T a[142:143],27,4608
s_waitcnt vmcnt(20) lgkmcnt(0)
s_barrier
s_add_u32 s55,s50,6881280
s_add_u32 m0,s57,16384
s_nop 0
buffer_load_dwordx4 v18,s[40:43],s55 offen offset:0  lds
s_add_u32 m0,s57,17408
s_nop 0
buffer_load_dwordx4 v19,s[40:43],s55 offen offset:0  lds
s_add_u32 m0,s57,24448
s_nop 0
buffer_load_dwordx4 v18,s[40:43],s55 offen offset:128  lds
s_add_u32 m0,s57,25472
s_nop 0
buffer_load_dwordx4 v19,s[40:43],s55 offen offset:128  lds
v_add_u32 v26,32768,v20
v_add_u32 v27,32768,v21
T a[144:145],26,0
T a[146:147],26,512
T a[148:149],27,0
T a[150:151],27,512
T a[152:153],26,4096
T a[154:155],26,4608
T a[156:157],27,4096
T a[158:159],27,4608
s_waitcnt vmcnt(20) lgkmcnt(0)
s_barrier
v_add_u32 v26,49152,v20
v_add_u32 v27,49152,v21
T a[160:161],26,0
T a[162:163],26,512
T a[164:165],27,0
T a[166:167],27,512
T a[168:169],26,4096
T a[170:171],26,4608
T a[172:173],27,4096
T a[174:175],27,4608
s_waitcnt vmcnt(16) lgkmcnt(0)
s_barrier
v_add_u32 v26,65536,v20
v_add_u32 v27,65536,v21
T a[176:177],26,0
T a[178:179],26,512
T a[180:181],27,0
T a[182:183],27,512
T a[184:185],26,4096
T a[186:187],26,4608
T a[188:189],27,4096
T a[190:191],27,4608
s_waitcnt vmcnt(12) lgkmcnt(0)
s_barrier
v_add_u32 v26,81920,v20
v_add_u32 v27,81920,v21
T a[192:193],26,0
T a[194:195],26,512
T a[196:197],27,0
T a[198:199],27,512
T a[200:201],26,4096
T a[202:203],26,4608
T a[204:205],27,4096
T a[206:207],27,4608
s_waitcnt vmcnt(8) lgkmcnt(0)
s_barrier
v_add_u32 v26,98304,v20
v_add_u32 v27,98304,v21
T a[208:209],26,0
T a[210:211],26,512
T a[212:213],27,0
T a[214:215],27,512
T a[216:217],26,4096
T a[218:219],26,4608
T a[220:221],27,4096
T a[222:223],27,4608
s_waitcnt vmcnt(4) lgkmcnt(0)
s_barrier
v_add_u32 v26,0,v20
v_add_u32 v27,0,v21
T a[224:225],26,0
T a[226:227],26,512
T a[228:229],27,0
T a[230:231],27,512
T a[232:233],26,4096
T a[234:235],26,4608
T a[236:237],27,4096
T a[238:239],27,4608
s_waitcnt vmcnt(0) lgkmcnt(0)
s_barrier
v_add_u32 v26,16384,v20
v_add_u32 v27,16384,v21
T a[240:241],26,0
T a[242:243],26,512
T a[244:245],27,0
T a[246:247],27,512
T a[248:249],26,4096
T a[250:251],26,4608
T a[252:253],27,4096
T a[254:255],27,4608
L 0
s_barrier
v_mov_b32 v34,0x3f803f80
v_mov_b32 v35,0x3f803f80
v_mov_b32 v36,0x3f803f80
v_mov_b32 v37,0x3f803f80
s_nop 1
v_mfma_f32_16x16x32_bf16 v[106:109],a[0:3],v[34:37],0
v_mfma_f32_16x16x32_bf16 v[110:113],a[4:7],v[34:37],0
M 106,a[8:11],34
M 110,a[12:15],34
M 106,a[16:19],34
M 110,a[20:23],34
M 106,a[24:27],34
M 110,a[28:31],34
M 106,a[32:35],34
M 110,a[36:39],34
M 106,a[40:43],34
M 110,a[44:47],34
M 106,a[48:51],34
M 110,a[52:55],34
M 106,a[56:59],34
M 110,a[60:63],34
M 106,a[64:67],34
M 110,a[68:71],34
M 106,a[72:75],34
M 110,a[76:79],34
M 106,a[80:83],34
M 110,a[84:87],34
M 106,a[88:91],34
M 110,a[92:95],34
M 106,a[96:99],34
M 110,a[100:103],34
M 106,a[104:107],34
M 110,a[108:111],34
M 106,a[112:115],34
M 110,a[116:119],34
M 106,a[120:123],34
M 110,a[124:127],34
M 106,a[128:131],34
M 110,a[132:135],34
M 106,a[136:139],34
M 110,a[140:143],34
M 106,a[144:147],34
M 110,a[148:151],34
M 106,a[152:155],34
M 110,a[156:159],34
M 106,a[160:163],34
M 110,a[164:167],34
M 106,a[168:171],34
M 110,a[172:175],34
M 106,a[176:179],34
)"
R"(M 110,a[180:183],34
M 106,a[184:187],34
M 110,a[188:191],34
M 106,a[192:195],34
M 110,a[196:199],34
M 106,a[200:203],34
M 110,a[204:207],34
M 106,a[208:211],34
M 110,a[212:215],34
M 106,a[216:219],34
M 110,a[220:223],34
M 106,a[224:227],34
M 110,a[228:231],34
M 106,a[232:235],34
M 110,a[236:239],34
M 106,a[240:243],34
M 110,a[244:247],34
M 106,a[248:251],34
M 110,a[252:255],34
v_mov_b32 v4,s53
s_mov_b32 s82,0
s_mov_b32 s80,0
s_mov_b32 s54,s45
s_mul_i32 s57,s44,4096
s_mul_i32 s55,s45,32768
s_add_u32 s55,s55,s57
s_add_u32 s55,s55,31457280
s_mul_i32 s57,s44,4096
s_mul_i32 s60,s46,32768
s_mul_i32 s61,s46,1024
s_mul_i32 s62,s46,114688
s_mul_i32 s63,s46,2560
s_mul_i32 s64,s46,1024
s_lshl_b32 s59,s44,8
s_add_u32 s59,s59,131072
s_mul_i32 s58,s45,1024
s_add_u32 s58,s58,109613056
s_mul_i32 s65,s45,114688
s_add_u32 s65,s65,s47
s_mul_i32 s68,s45,2560
s_add_u32 s68,s68,s48
s_mul_i32 s73,s45,1024
s_add_u32 s73,s73,107479040
s_mul_i32 s74,s45,32768
s_add_u32 s74,s74,s49
s_mul_i32 s75,s45,1024
s_add_u32 s75,s75,107479040
s_mov_b32 s66,0x7fff0000
s_mov_b32 s67,0x7fff0000
s_mov_b32 s69,0x7fff0000
s_mov_b32 s76,0x7fff0000
s_mov_b32 s77,0x7fff0000
s_mov_b32 s70,0x7fff0000
s_mov_b32 s71,0x7fff0000
s_mov_b32 s72,0x7fff0000
v_mov_b32 v1,v0
v_add_u32 v2,16384,v0
s_mov_b32 s84,16384
s_mov_b32 s86,0x3a800000
s_mov_b32 s87,0x3727c5ac
s_mov_b32 s88,0xc0135761
s_mov_b32 s89,0x3d372713
s_lshr_b32 s85,s44,1
s_mul_i32 s85,s85,127
v_lshlrev_b32 v8,22,v0
v_and_b32 v8,0x80000000,v8
v_or_b32 v8,v8,v5
v_add_u32 v2,32768,v0
s_lshl_b32 s84,s57,1
s_add_u32 s84,s84,32768
v_and_b32 v114,240,v0
v_cmp_eq_u32 vcc,0,v114
s_nop 1
v_cndmask_b32 v17,0,1.0,vcc
s_getpc_b64 s[40:41]
Lg3o_pc_%=:
s_lshl_b32 s78,s44,13
v_lshlrev_b32 v114,3,v0
v_add_u32 v114,s78,v114
v_min_u32 v114,Lcode_g3o_e-Lg3o_pc_%=-4,v114
s_mov_b32 m0,163584
s_nop 0
global_load_lds_dword v114,s[40:41]
s_add_u32 m0,s59,4096
s_nop 0
buffer_load_dword v8,s[36:39],s58 offen offset:0 sc1 lds
buffer_store_dword v4,v3,s[36:39],s71 offen offset:0 sc0 sc1
s_add_u32 m0,s59,5120
s_nop 0
buffer_load_dword v8,s[36:39],s58 offen offset:0 sc1 lds
buffer_store_dword v4,v3,s[36:39],s71 offen offset:0 sc0 sc1
s_mov_b32 s83,0
Lg3o_poll_%=:
s_add_u32 s78,s83,2
s_and_b32 s78,s78,3
s_lshl_b32 s78,s78,10
s_add_u32 s78,s78,4096
s_add_u32 m0,s59,s78
s_nop 0
buffer_load_dword v8,s[36:39],s58 offen offset:0 sc1 lds
s_and_b32 s78,s83,3
s_lshl_b32 s78,s78,10
v_add_u32 v114,s78,v6
s_waitcnt vmcnt(4)
s_barrier
ds_read_b32 v7,v114 offset:4096
buffer_store_dword v4,v3,s[36:39],s70 offen offset:0 sc0 sc1
s_mov_b32 s70,s71
s_mov_b32 s71,s72
s_mov_b32 s72,0x7fff0000
s_add_u32 s83,s83,1
s_add_u32 s82,s82,1
L 0
v_cmp_eq_u32 vcc,s53,v7
s_nop 0
s_cmp_eq_u32 vcc_lo,-1
s_cbranch_scc1 Lg3o_go_%=
s_cmp_lt_u32 s82,20000
s_cbranch_scc1 Lg3o_poll_%=
Lg3o_go_%=:
buffer_load_dword v130,v11,s[36:39],s73 offen offset:0 sc1
s_add_u32 s73,s73,s64
s_add_u32 m0,s57,0
s_add_u32 s56,s55,0
D 0,56,0
D 0,56,1024
D 0,56,2048
D 0,56,3072
s_add_u32 s58,s58,s61
s_add_u32 m0,s59,2048
s_nop 0
buffer_load_dword v8,s[36:39],s58 offen offset:0 sc1 lds
s_add_u32 m0,s57,16384
s_add_u32 s56,s55,16384
D 0,56,0
D 0,56,1024
D 0,56,2048
D 0,56,3072
s_add_u32 s55,s55,s60
s_sub_u32 s57,s84,s57
s_add_u32 s54,s54,s46
s_waitcnt vmcnt(4)
s_barrier
buffer_store_dword v4,v3,s[36:39],s70 offen offset:0 sc0 sc1
s_min_u32 s71,s71,s72
buffer_store_dword v4,v3,s[36:39],s71 offen offset:0 sc0 sc1
s_mov_b32 s70,0x7fff0000
s_mov_b32 s71,0x7fff0000
s_mov_b32 s72,0x7fff0000
s_cmp_eq_u32 s80,1
s_cbranch_scc1 Lg3o_st1_%=
s_branch Lg3o_st0_%=
Lg3o_gox_%=:
s_add_u32 m0,s59,2048
s_nop 0
buffer_load_dword v8,s[36:39],s58 offen offset:0 sc1 lds
s_waitcnt vmcnt(0)
s_barrier
buffer_store_dword v4,v3,s[36:39],s70 offen offset:0 sc0 sc1
s_min_u32 s71,s71,s72
buffer_store_dword v4,v3,s[36:39],s71 offen offset:0 sc0 sc1
s_mov_b32 s70,0x7fff0000
s_mov_b32 s71,0x7fff0000
s_mov_b32 s72,0x7fff0000
s_cmp_eq_u32 s80,1
s_cbranch_scc1 Lg3o_st1_%=
Lg3o_st0_%=:
ds_read_b32 v7,v6 offset:2048
R 34,1,0
R 38,1,1024
R 42,1,2048
R 46,1,3072
R 50,1,4096
L 5
v_cmp_eq_u32 vcc,s53,v7
L 3
s_cmp_eq_u32 vcc_lo,-1
s_cbranch_scc1 Lg3o_F0_%=
s_branch Lg3o_L0_%=
Lg3o_st1_%=:
ds_read_b32 v7,v6 offset:2048
R 34,2,0
R 38,2,1024
R 42,2,2048
R 46,2,3072
R 50,2,4096
L 5
v_cmp_eq_u32 vcc,s53,v7
L 3
s_cmp_eq_u32 vcc_lo,-1
s_cbranch_scc1 Lg3o_F1_%=
s_branch Lg3o_L1_%=
Lg3o_F0_%=:
v_mfma_f32_16x16x32_bf16 v[22:25],a[4:7],v[34:37],0
s_add_u32 s58,s58,s61
s_add_u32 m0,s59,0
R 54,1,5120
M 22,a[12:15],38
buffer_load_dword v8,s[36:39],s58 offen offset:0 sc1 lds
v_mfma_f32_16x16x32_bf16 v[18:21],a[0:3],v[34:37],0
s_add_u32 m0,s57,0
R 58,1,6144
v_add_f32_dpp v131,v130,v130 quad_perm:[1,0,3,2] row_mask:0xf bank_mask:0xf
M 18,a[8:11],38
L 3
s_add_u32 s56,s55,0
buffer_load_dword v130,v11,s[36:39],s73 offen offset:0 sc1
M 18,a[16:19],42
s_mov_b32 s66,s65
R 62,1,7168
v_add_f32_dpp v131,v131,v131 quad_perm:[2,3,0,1] row_mask:0xf bank_mask:0xf
M 18,a[24:27],46
D 0,56,0
M 22,a[20:23],42
s_add_u32 s65,s65,s62
R 66,1,8192
v_add_f32_dpp v134,v131,v131 row_half_mirror row_mask:0xf bank_mask:0xf
M 22,a[28:31],46
L 3
D 0,56,1024
M 22,a[36:39],50
s_add_u32 s54,s54,s46
R 70,1,9216
v_mov_b32_dpp v131,v134 row_ror:8 row_mask:0xf bank_mask:0xf
M 22,a[44:47],54
s_add_u32 s73,s73,s64
v_fma_f32 v131,-v134,v134,v131
v_max_f32 v131,s87,v131
M 18,a[32:35],50
R 74,1,10240
v_mul_f32 v134,v17,v134
M 18,a[40:43],54
L 3
D 0,56,2048
M 18,a[48:51],58
R 78,1,11264
v_fma_f32 v26,-v102,v106,v26
M 18,a[56:59],62
D 0,56,3072
M 22,a[52:55],58
s_add_u32 m0,s57,16384
R 82,1,12288
v_fma_f32 v27,-v102,v107,v27
M 22,a[60:63],62
L 3
v_rsq_f32 v132,v131
M 22,a[68:71],66
s_add_u32 s56,s55,16384
R 86,1,13312
v_fma_f32 v131,v15,v131,v16
M 22,a[76:79],70
s_add_u32 s55,s55,s60
v_mul_f32 v135,v132,v132
v_fma_f32 v131,v14,v135,v131
M 18,a[64:67],66
s_sub_u32 s57,s84,s57
R 90,1,14336
v_fma_f32 v134,v132,v131,v134
M 18,a[72:75],70
L 3
ds_write_b32 v12,v134 offset:0
M 18,a[80:83],74
s_waitcnt vmcnt(8)
R 94,1,15360
v_fma_f32 v28,-v102,v108,v28
M 18,a[88:91],78
s_barrier
v_fma_f32 v29,-v102,v109,v29
v_mul_f32 v114,v26,v26
M 22,a[84:87],74
R 34,1,16384
v_mul_f32 v115,v27,v27
M 22,a[92:95],78
L 4
v_mul_f32 v116,v28,v28
v_mul_f32 v117,v29,v29
M 22,a[100:103],82
R 38,1,17408
v_fma_f32 v114,v104,v114,v103
M 22,a[108:111],86
D 0,56,0
M 18,a[96:99],82
R 42,1,18432
v_fma_f32 v115,v104,v115,v103
M 18,a[104:107],86
L 3
v_fma_f32 v116,v104,v116,v103
v_fma_f32 v117,v104,v117,v103
M 18,a[112:115],90
R 46,1,19456
v_mul_f32 v114,v26,v114
M 18,a[120:123],94
v_exp_f32 v114,v114
M 22,a[116:119],90
R 50,1,20480
v_mul_f32 v115,v27,v115
M 22,a[124:127],94
L 3
v_exp_f32 v115,v115
M 22,a[132:135],34
R 54,1,21504
v_mul_f32 v116,v28,v116
M 22,a[140:143],38
D 0,56,1024
M 18,a[128:131],34
R 58,1,22528
v_mul_f32 v117,v29,v117
M 18,a[136:139],38
L 3
v_exp_f32 v116,v116
M 18,a[144:147],42
R 62,1,23552
v_fma_f32 v114,v114,v105,v105
M 18,a[152:155],46
v_exp_f32 v117,v117
M 22,a[148:151],42
R 66,1,24576
v_fma_f32 v115,v115,v105,v105
M 22,a[156:159],46
L 3
D 0,56,2048
M 22,a[164:167],50
R 70,1,25600
v_fma_f32 v116,v116,v105,v105
M 22,a[172:175],54
v_rcp_f32 v114,v114
M 18,a[160:163],50
R 74,1,26624
v_fma_f32 v117,v117,v105,v105
M 18,a[168:171],54
L 3
v_rcp_f32 v115,v115
M 18,a[176:179],58
R 78,1,27648
v_mul_f32 v26,v26,v114
M 18,a[184:187],62
v_rcp_f32 v116,v116
M 22,a[180:183],58
R 82,1,28672
v_mul_f32 v27,v27,v115
M 22,a[188:191],62
L 3
D 0,56,3072
M 22,a[196:199],66
R 86,1,29696
v_mul_f32 v28,v28,v116
M 22,a[204:207],70
v_rcp_f32 v117,v117
M 18,a[192:195],66
R 90,1,30720
v_mul_f32 v29,v29,v117
M 18,a[200:203],70
L 3
s_nop 0
v_cvt_pk_bf16_f32 v122,v26,v27
M 18,a[208:211],74
s_waitcnt vmcnt(4)
R 94,1,31744
v_fma_f32 v30,-v102,v110,v30
M 18,a[216:219],78
s_barrier
ds_read_b32 v7,v6 offset:0
R 98,13,0
M 22,a[212:215],74
R 34,2,0
v_fma_f32 v31,-v102,v111,v31
M 22,a[220:223],78
L 5
buffer_store_dword v4,v3,s[36:39],s70 offen offset:0 sc0 sc1
M 22,a[228:231],82
s_mov_b32 s70,s69
R 38,2,1024
v_fma_f32 v32,-v102,v112,v32
M 22,a[236:239],86
s_mov_b32 s69,s68
v_fma_f32 v33,-v102,v113,v33
v_mul_f32 v118,v30,v30
M 18,a[224:227],82
s_add_u32 s68,s68,s63
R 42,2,2048
v_mul_f32 v119,v31,v31
M 18,a[232:235],86
L 5
v_mul_f32 v120,v32,v32
v_mul_f32 v121,v33,v33
v_fma_f32 v118,v104,v118,v103
v_fma_f32 v119,v104,v119,v103
v_fma_f32 v120,v104,v120,v103
v_fma_f32 v121,v104,v121,v103
v_mul_f32 v118,v30,v118
v_mul_f32 v119,v31,v119
v_mul_f32 v120,v32,v120
v_mul_f32 v121,v33,v121
v_exp_f32 v118,v118
v_exp_f32 v119,v119
v_exp_f32 v120,v120
v_exp_f32 v121,v121
M 18,a[240:243],90
L 4
R 46,2,3072
v_fma_f32 v118,v118,v105,v105
v_fma_f32 v119,v119,v105,v105
v_fma_f32 v120,v120,v105,v105
v_fma_f32 v121,v121,v105,v105
v_rcp_f32 v118,v118
v_rcp_f32 v119,v119
v_rcp_f32 v120,v120
v_rcp_f32 v121,v121
v_cmp_eq_u32 vcc,s53,v7
M 18,a[248:251],94
v_cvt_pk_bf16_f32 v123,v28,v29
v_mul_f32 v30,v30,v118
v_mul_f32 v31,v31,v119
v_mul_f32 v32,v32,v120
v_mul_f32 v33,v33,v121
v_cvt_pk_bf16_f32 v124,v30,v31
v_cvt_pk_bf16_f32 v125,v32,v33
M 22,a[244:247],90
L 2
s_cmp_eq_u32 vcc_lo,-1
R 50,2,4096
buffer_store_dwordx4 v[122:125],v0,s[36:39],s67 offen offset:0 sc0 sc1
M 22,a[252:255],94
s_cbranch_scc0 Lg3o_K1_%=
Lg3o_F1_%=:
v_mfma_f32_16x16x32_bf16 v[30:33],a[4:7],v[34:37],0
s_add_u32 s58,s58,s61
s_add_u32 m0,s59,1024
R 54,2,5120
M 30,a[12:15],38
buffer_load_dword v8,s[36:39],s58 offen offset:0 sc1 lds
v_mfma_f32_16x16x32_bf16 v[26:29],a[0:3],v[34:37],0
s_add_u32 m0,s57,0
R 58,2,6144
v_add_f32_dpp v131,v130,v130 quad_perm:[1,0,3,2] row_mask:0xf bank_mask:0xf
M 26,a[8:11],38
L 3
s_add_u32 s56,s55,0
buffer_load_dword v130,v11,s[36:39],s73 offen offset:0 sc1
M 26,a[16:19],42
s_mov_b32 s67,s65
R 62,2,7168
v_add_f32_dpp v131,v131,v131 quad_perm:[2,3,0,1] row_mask:0xf bank_mask:0xf
M 26,a[24:27],46
D 0,56,0
M 30,a[20:23],42
s_add_u32 s65,s65,s62
R 66,2,8192
v_add_f32_dpp v134,v131,v131 row_half_mirror row_mask:0xf bank_mask:0xf
M 30,a[28:31],46
L 3
D 0,56,1024
M 30,a[36:39],50
s_add_u32 s54,s54,s46
R 70,2,9216
v_mov_b32_dpp v131,v134 row_ror:8 row_mask:0xf bank_mask:0xf
M 30,a[44:47],54
s_add_u32 s73,s73,s64
v_fma_f32 v131,-v134,v134,v131
v_max_f32 v131,s87,v131
M 26,a[32:35],50
R 74,2,10240
v_mul_f32 v134,v17,v134
M 26,a[40:43],54
L 3
D 0,56,2048
M 26,a[48:51],58
R 78,2,11264
v_fma_f32 v18,-v98,v106,v18
M 26,a[56:59],62
D 0,56,3072
M 30,a[52:55],58
s_add_u32 m0,s57,16384
R 82,2,12288
v_fma_f32 v19,-v98,v107,v19
M 30,a[60:63],62
L 3
v_rsq_f32 v132,v131
M 30,a[68:71],66
s_add_u32 s56,s55,16384
R 86,2,13312
v_fma_f32 v131,v15,v131,v16
M 30,a[76:79],70
s_add_u32 s55,s55,s60
v_mul_f32 v135,v132,v132
v_fma_f32 v131,v14,v135,v131
M 26,a[64:67],66
s_sub_u32 s57,s84,s57
R 90,2,14336
v_fma_f32 v134,v132,v131,v134
M 26,a[72:75],70
L 3
ds_write_b32 v12,v134 offset:2048
M 26,a[80:83],74
s_waitcnt vmcnt(8)
R 94,2,15360
v_fma_f32 v20,-v98,v108,v20
M 26,a[88:91],78
s_barrier
v_fma_f32 v21,-v98,v109,v21
)"
R"(v_mul_f32 v114,v18,v18
M 30,a[84:87],74
R 34,2,16384
v_mul_f32 v115,v19,v19
M 30,a[92:95],78
L 4
v_mul_f32 v116,v20,v20
v_mul_f32 v117,v21,v21
M 30,a[100:103],82
R 38,2,17408
v_fma_f32 v114,v100,v114,v99
M 30,a[108:111],86
D 0,56,0
M 26,a[96:99],82
R 42,2,18432
v_fma_f32 v115,v100,v115,v99
M 26,a[104:107],86
L 3
v_fma_f32 v116,v100,v116,v99
v_fma_f32 v117,v100,v117,v99
M 26,a[112:115],90
R 46,2,19456
v_mul_f32 v114,v18,v114
M 26,a[120:123],94
v_exp_f32 v114,v114
M 30,a[116:119],90
R 50,2,20480
v_mul_f32 v115,v19,v115
M 30,a[124:127],94
L 3
v_exp_f32 v115,v115
M 30,a[132:135],34
R 54,2,21504
v_mul_f32 v116,v20,v116
M 30,a[140:143],38
D 0,56,1024
M 26,a[128:131],34
R 58,2,22528
v_mul_f32 v117,v21,v117
M 26,a[136:139],38
L 3
v_exp_f32 v116,v116
M 26,a[144:147],42
R 62,2,23552
v_fma_f32 v114,v114,v101,v101
M 26,a[152:155],46
v_exp_f32 v117,v117
M 30,a[148:151],42
R 66,2,24576
v_fma_f32 v115,v115,v101,v101
M 30,a[156:159],46
L 3
D 0,56,2048
M 30,a[164:167],50
R 70,2,25600
v_fma_f32 v116,v116,v101,v101
M 30,a[172:175],54
v_rcp_f32 v114,v114
M 26,a[160:163],50
R 74,2,26624
v_fma_f32 v117,v117,v101,v101
M 26,a[168:171],54
L 3
v_rcp_f32 v115,v115
M 26,a[176:179],58
R 78,2,27648
v_mul_f32 v18,v18,v114
M 26,a[184:187],62
v_rcp_f32 v116,v116
M 30,a[180:183],58
R 82,2,28672
v_mul_f32 v19,v19,v115
M 30,a[188:191],62
L 3
D 0,56,3072
M 30,a[196:199],66
R 86,2,29696
v_mul_f32 v20,v20,v116
M 30,a[204:207],70
v_rcp_f32 v117,v117
M 26,a[192:195],66
R 90,2,30720
v_mul_f32 v21,v21,v117
M 26,a[200:203],70
L 3
s_nop 0
v_cvt_pk_bf16_f32 v122,v18,v19
M 26,a[208:211],74
s_waitcnt vmcnt(4)
R 94,2,31744
v_fma_f32 v22,-v98,v110,v22
M 26,a[216:219],78
s_barrier
ds_read_b32 v7,v6 offset:1024
R 102,13,2048
M 30,a[212:215],74
R 34,1,0
v_fma_f32 v23,-v98,v111,v23
M 30,a[220:223],78
L 5
buffer_store_dword v4,v3,s[36:39],s70 offen offset:0 sc0 sc1
M 30,a[228:231],82
s_mov_b32 s70,s69
R 38,1,1024
v_fma_f32 v24,-v98,v112,v24
M 30,a[236:239],86
s_mov_b32 s69,s68
v_fma_f32 v25,-v98,v113,v25
v_mul_f32 v118,v22,v22
M 26,a[224:227],82
s_add_u32 s68,s68,s63
R 42,1,2048
v_mul_f32 v119,v23,v23
M 26,a[232:235],86
L 5
v_mul_f32 v120,v24,v24
v_mul_f32 v121,v25,v25
v_fma_f32 v118,v100,v118,v99
v_fma_f32 v119,v100,v119,v99
v_fma_f32 v120,v100,v120,v99
v_fma_f32 v121,v100,v121,v99
v_mul_f32 v118,v22,v118
v_mul_f32 v119,v23,v119
v_mul_f32 v120,v24,v120
v_mul_f32 v121,v25,v121
v_exp_f32 v118,v118
v_exp_f32 v119,v119
v_exp_f32 v120,v120
v_exp_f32 v121,v121
M 26,a[240:243],90
L 4
R 46,1,3072
v_fma_f32 v118,v118,v101,v101
v_fma_f32 v119,v119,v101,v101
v_fma_f32 v120,v120,v101,v101
v_fma_f32 v121,v121,v101,v101
v_rcp_f32 v118,v118
v_rcp_f32 v119,v119
v_rcp_f32 v120,v120
v_rcp_f32 v121,v121
v_cmp_eq_u32 vcc,s53,v7
M 26,a[248:251],94
v_cvt_pk_bf16_f32 v123,v20,v21
v_mul_f32 v22,v22,v118
v_mul_f32 v23,v23,v119
v_mul_f32 v24,v24,v120
v_mul_f32 v25,v25,v121
v_cvt_pk_bf16_f32 v124,v22,v23
v_cvt_pk_bf16_f32 v125,v24,v25
M 30,a[244:247],90
L 2
s_cmp_eq_u32 vcc_lo,-1
R 50,1,4096
buffer_store_dwordx4 v[122:125],v0,s[36:39],s66 offen offset:0 sc0 sc1
M 30,a[252:255],94
s_cbranch_scc1 Lg3o_F0_%=
Lg3o_K0_%=:
Lg3o_L0_%=:
v_mfma_f32_16x16x32_bf16 v[22:25],a[4:7],v[34:37],0
s_add_u32 m0,s59,3072
R 54,1,5120
v_add_f32_dpp v131,v130,v130 quad_perm:[1,0,3,2] row_mask:0xf bank_mask:0xf
M 22,a[12:15],38
buffer_load_dword v8,s[36:39],s58 offen offset:0 sc1 lds
v_mfma_f32_16x16x32_bf16 v[18:21],a[0:3],v[34:37],0
s_mov_b32 s66,s65
R 58,1,6144
v_add_f32_dpp v131,v131,v131 quad_perm:[2,3,0,1] row_mask:0xf bank_mask:0xf
M 18,a[8:11],38
L 3
v_fma_f32 v26,-v102,v106,v26
v_fma_f32 v27,-v102,v107,v27
M 18,a[16:19],42
s_add_u32 s65,s65,s62
R 62,1,7168
v_add_f32_dpp v134,v131,v131 row_half_mirror row_mask:0xf bank_mask:0xf
M 18,a[24:27],46
v_fma_f32 v28,-v102,v108,v28
v_fma_f32 v29,-v102,v109,v29
M 22,a[20:23],42
R 66,1,8192
v_mov_b32_dpp v131,v134 row_ror:8 row_mask:0xf bank_mask:0xf
M 22,a[28:31],46
L 3
v_fma_f32 v131,-v134,v134,v131
v_max_f32 v131,s87,v131
M 22,a[36:39],50
R 70,1,9216
v_mul_f32 v134,v17,v134
M 22,a[44:47],54
v_rsq_f32 v132,v131
M 18,a[32:35],50
R 74,1,10240
v_fma_f32 v131,v15,v131,v16
M 18,a[40:43],54
L 3
v_mul_f32 v135,v132,v132
v_fma_f32 v131,v14,v135,v131
M 18,a[48:51],58
R 78,1,11264
v_fma_f32 v134,v132,v131,v134
M 18,a[56:59],62
ds_write_b32 v12,v134 offset:0
M 22,a[52:55],58
R 82,1,12288
v_mul_f32 v114,v26,v26
M 22,a[60:63],62
L 4
v_mul_f32 v115,v27,v27
v_mul_f32 v116,v28,v28
M 22,a[68:71],66
R 86,1,13312
v_mul_f32 v117,v29,v29
M 22,a[76:79],70
v_fma_f32 v114,v104,v114,v103
v_fma_f32 v115,v104,v115,v103
M 18,a[64:67],66
R 90,1,14336
v_fma_f32 v116,v104,v116,v103
M 18,a[72:75],70
L 4
v_fma_f32 v117,v104,v117,v103
v_mul_f32 v114,v26,v114
M 18,a[80:83],74
s_waitcnt vmcnt(0)
R 94,1,15360
v_mul_f32 v115,v27,v115
M 18,a[88:91],78
s_barrier
ds_read_b32 v7,v6 offset:3072
v_mul_f32 v116,v28,v116
M 22,a[84:87],74
R 34,1,16384
v_mul_f32 v117,v29,v117
M 22,a[92:95],78
L 4
buffer_store_dword v4,v3,s[36:39],s70 offen offset:0 sc0 sc1
M 22,a[100:103],82
s_mov_b32 s70,0x7fff0000
R 38,1,17408
v_fma_f32 v30,-v102,v110,v30
M 22,a[108:111],86
v_exp_f32 v114,v114
M 18,a[96:99],82
R 42,1,18432
v_fma_f32 v114,v114,v105,v105
M 18,a[104:107],86
L 4
L 3
v_cmp_eq_u32 vcc,s53,v7
M 18,a[112:115],90
R 46,1,19456
v_fma_f32 v31,-v102,v111,v31
M 18,a[120:123],94
v_exp_f32 v115,v115
M 22,a[116:119],90
s_cmp_eq_u32 vcc_lo,-1
R 50,1,20480
v_fma_f32 v115,v115,v105,v105
s_cbranch_scc0 Lg3o_X0_%=
M 22,a[124:127],94
L 3
s_add_u32 m0,s57,0
s_add_u32 s56,s55,0
buffer_load_dword v130,v11,s[36:39],s73 offen offset:0 sc1
M 22,a[132:135],34
s_add_u32 s58,s58,s61
R 54,1,21504
v_fma_f32 v32,-v102,v112,v32
M 22,a[140:143],38
D 0,56,0
M 18,a[128:131],34
s_add_u32 s54,s54,s46
R 58,1,22528
v_fma_f32 v33,-v102,v113,v33
M 18,a[136:139],38
L 3
v_exp_f32 v116,v116
M 18,a[144:147],42
s_add_u32 s73,s73,s64
R 62,1,23552
v_fma_f32 v116,v116,v105,v105
M 18,a[152:155],46
D 0,56,1024
M 22,a[148:151],42
R 66,1,24576
v_mul_f32 v118,v30,v30
M 22,a[156:159],46
L 3
v_exp_f32 v117,v117
M 22,a[164:167],50
R 70,1,25600
v_fma_f32 v117,v117,v105,v105
M 22,a[172:175],54
D 0,56,2048
M 18,a[160:163],50
R 74,1,26624
v_mul_f32 v119,v31,v31
M 18,a[168:171],54
L 3
v_rcp_f32 v114,v114
M 18,a[176:179],58
R 78,1,27648
v_mul_f32 v26,v26,v114
M 18,a[184:187],62
D 0,56,3072
M 22,a[180:183],58
s_add_u32 m0,s59,0
R 82,1,28672
v_mul_f32 v120,v32,v32
M 22,a[188:191],62
L 3
buffer_load_dword v8,s[36:39],s58 offen offset:0 sc1 lds
M 22,a[196:199],66
s_add_u32 m0,s57,16384
R 86,1,29696
v_mul_f32 v121,v33,v33
M 22,a[204:207],70
s_add_u32 s56,s55,16384
v_rcp_f32 v115,v115
M 18,a[192:195],66
s_add_u32 s55,s55,s60
R 90,1,30720
v_mul_f32 v27,v27,v115
M 18,a[200:203],70
L 3
s_nop 0
D 0,56,0
M 18,a[208:211],74
s_waitcnt vmcnt(1)
R 94,1,31744
v_cvt_pk_bf16_f32 v122,v26,v27
M 18,a[216:219],78
s_barrier
ds_read_b32 v7,v6 offset:0
R 98,13,0
M 22,a[212:215],74
s_sub_u32 s57,s84,s57
R 34,2,0
v_fma_f32 v118,v104,v118,v103
M 22,a[220:223],78
L 5
buffer_store_dword v4,v3,s[36:39],s70 offen offset:0 sc0 sc1
M 22,a[228:231],82
s_mov_b32 s70,s69
R 38,2,1024
v_fma_f32 v119,v104,v119,v103
M 22,a[236:239],86
D 0,56,1024
M 18,a[224:227],82
s_mov_b32 s69,s68
R 42,2,2048
v_fma_f32 v120,v104,v120,v103
M 18,a[232:235],86
L 5
v_fma_f32 v121,v104,v121,v103
v_mul_f32 v118,v30,v118
v_mul_f32 v119,v31,v119
v_mul_f32 v120,v32,v120
v_mul_f32 v121,v33,v121
v_exp_f32 v118,v118
v_exp_f32 v119,v119
v_exp_f32 v120,v120
v_exp_f32 v121,v121
M 18,a[240:243],90
L 4
R 46,2,3072
v_rcp_f32 v116,v116
v_rcp_f32 v117,v117
v_fma_f32 v118,v118,v105,v105
v_fma_f32 v119,v119,v105,v105
v_fma_f32 v120,v120,v105,v105
v_fma_f32 v121,v121,v105,v105
v_rcp_f32 v118,v118
v_rcp_f32 v119,v119
v_rcp_f32 v120,v120
v_rcp_f32 v121,v121
v_cmp_eq_u32 vcc,s53,v7
M 18,a[248:251],94
s_add_u32 s68,s68,s63
v_mul_f32 v28,v28,v116
v_mul_f32 v29,v29,v117
v_cvt_pk_bf16_f32 v123,v28,v29
v_mul_f32 v30,v30,v118
v_mul_f32 v31,v31,v119
v_mul_f32 v32,v32,v120
v_mul_f32 v33,v33,v121
v_cvt_pk_bf16_f32 v124,v30,v31
v_cvt_pk_bf16_f32 v125,v32,v33
M 22,a[244:247],90
L 2
s_cmp_eq_u32 vcc_lo,-1
R 50,2,4096
buffer_store_dwordx4 v[122:125],v0,s[36:39],s67 offen offset:0 sc0 sc1
M 22,a[252:255],94
D 0,56,2048
D 0,56,3072
buffer_store_dword v4,v3,s[36:39],s71 offen offset:0 sc0 sc1
buffer_store_dword v4,v3,s[36:39],s71 offen offset:0 sc0 sc1
s_cbranch_scc1 Lg3o_F1_%=
s_branch Lg3o_K1_%=
Lg3o_X0_%=:
M 22,a[124:127],94
L 3
s_add_u32 m0,s59,0
v_fma_f32 v32,-v102,v112,v32
M 22,a[132:135],34
R 54,1,21504
v_fma_f32 v33,-v102,v113,v33
M 22,a[140:143],38
buffer_load_dword v8,s[36:39],s58 offen offset:0 sc1 lds
M 18,a[128:131],34
R 58,1,22528
v_mul_f32 v118,v30,v30
M 18,a[136:139],38
L 3
v_exp_f32 v116,v116
M 18,a[144:147],42
R 62,1,23552
v_fma_f32 v116,v116,v105,v105
M 18,a[152:155],46
v_exp_f32 v117,v117
M 22,a[148:151],42
R 66,1,24576
v_fma_f32 v117,v117,v105,v105
M 22,a[156:159],46
L 3
v_rcp_f32 v114,v114
v_mul_f32 v119,v31,v31
M 22,a[164:167],50
R 70,1,25600
v_mul_f32 v26,v26,v114
M 22,a[172:175],54
v_rcp_f32 v115,v115
M 18,a[160:163],50
R 74,1,26624
v_mul_f32 v27,v27,v115
M 18,a[168:171],54
L 3
v_rcp_f32 v116,v116
M 18,a[176:179],58
R 78,1,27648
v_mul_f32 v28,v28,v116
M 18,a[184:187],62
v_rcp_f32 v117,v117
M 22,a[180:183],58
R 82,1,28672
v_mul_f32 v29,v29,v117
M 22,a[188:191],62
L 3
v_cvt_pk_bf16_f32 v122,v26,v27
v_cvt_pk_bf16_f32 v123,v28,v29
M 22,a[196:199],66
R 86,1,29696
v_mul_f32 v120,v32,v32
M 22,a[204:207],70
v_mul_f32 v121,v33,v33
v_fma_f32 v118,v104,v118,v103
M 18,a[192:195],66
R 90,1,30720
v_fma_f32 v119,v104,v119,v103
M 18,a[200:203],70
L 3
s_nop 0
v_fma_f32 v120,v104,v120,v103
M 18,a[208:211],74
s_waitcnt vmcnt(0)
R 94,1,31744
v_fma_f32 v121,v104,v121,v103
M 18,a[216:219],78
s_barrier
buffer_store_dword v4,v3,s[36:39],s70 offen offset:0 sc0 sc1
M 22,a[212:215],74
s_mov_b32 s70,s69
ds_read_b32 v7,v6 offset:0
R 98,13,0
M 22,a[220:223],78
L 4
v_mul_f32 v118,v30,v118
v_mul_f32 v119,v31,v119
M 22,a[228:231],82
s_mov_b32 s69,s68
v_mul_f32 v120,v32,v120
v_mul_f32 v121,v33,v121
M 22,a[236:239],86
s_add_u32 s68,s68,s63
v_exp_f32 v118,v118
M 18,a[224:227],82
v_exp_f32 v119,v119
M 18,a[232:235],86
L 2
v_exp_f32 v120,v120
v_exp_f32 v121,v121
M 18,a[240:243],90
L 1
v_fma_f32 v118,v118,v105,v105
v_fma_f32 v119,v119,v105,v105
v_fma_f32 v120,v120,v105,v105
v_fma_f32 v121,v121,v105,v105
v_rcp_f32 v118,v118
)"
R"(v_rcp_f32 v119,v119
v_rcp_f32 v120,v120
v_rcp_f32 v121,v121
M 18,a[248:251],94
v_mul_f32 v30,v30,v118
v_mul_f32 v31,v31,v119
v_mul_f32 v32,v32,v120
v_mul_f32 v33,v33,v121
v_cvt_pk_bf16_f32 v124,v30,v31
v_cvt_pk_bf16_f32 v125,v32,v33
M 22,a[244:247],90
buffer_store_dwordx4 v[122:125],v0,s[36:39],s67 offen offset:0 sc0 sc1
v_cmp_eq_u32 vcc,s53,v7
M 22,a[252:255],94
s_mov_b32 s80,1
s_cmp_eq_u32 vcc_lo,-1
s_cbranch_scc0 Lg3o_xn0_%=
buffer_load_dword v130,v11,s[36:39],s73 offen offset:0 sc1
L 0
v_fma_f32 v18,-v98,v106,v18
v_fma_f32 v19,-v98,v107,v19
v_fma_f32 v20,-v98,v108,v20
v_fma_f32 v21,-v98,v109,v21
v_mul_f32 v114,v18,v18
v_mul_f32 v115,v19,v19
s_add_u32 s73,s73,s64
s_add_u32 m0,s57,0
s_add_u32 s56,s55,0
D 0,56,0
v_mul_f32 v116,v20,v20
v_mul_f32 v117,v21,v21
v_fma_f32 v114,v100,v114,v99
v_fma_f32 v115,v100,v115,v99
v_fma_f32 v116,v100,v116,v99
v_fma_f32 v117,v100,v117,v99
D 0,56,1024
v_mul_f32 v114,v18,v114
v_mul_f32 v115,v19,v115
v_mul_f32 v116,v20,v116
v_mul_f32 v117,v21,v117
v_exp_f32 v114,v114
v_exp_f32 v115,v115
D 0,56,2048
v_exp_f32 v116,v116
v_exp_f32 v117,v117
v_fma_f32 v114,v114,v101,v101
v_fma_f32 v115,v115,v101,v101
v_fma_f32 v116,v116,v101,v101
v_fma_f32 v117,v117,v101,v101
D 0,56,3072
v_rcp_f32 v114,v114
v_rcp_f32 v115,v115
v_rcp_f32 v116,v116
v_rcp_f32 v117,v117
v_mul_f32 v18,v18,v114
v_mul_f32 v19,v19,v115
s_add_u32 s58,s58,s61
s_add_u32 m0,s57,16384
s_add_u32 s56,s55,16384
D 0,56,0
v_mul_f32 v20,v20,v116
v_mul_f32 v21,v21,v117
v_cvt_pk_bf16_f32 v122,v18,v19
v_cvt_pk_bf16_f32 v123,v20,v21
v_fma_f32 v22,-v98,v110,v22
v_fma_f32 v23,-v98,v111,v23
D 0,56,1024
v_fma_f32 v24,-v98,v112,v24
v_fma_f32 v25,-v98,v113,v25
v_mul_f32 v118,v22,v22
v_mul_f32 v119,v23,v23
v_mul_f32 v120,v24,v24
v_mul_f32 v121,v25,v25
D 0,56,2048
v_fma_f32 v118,v100,v118,v99
v_fma_f32 v119,v100,v119,v99
v_fma_f32 v120,v100,v120,v99
v_fma_f32 v121,v100,v121,v99
v_mul_f32 v118,v22,v118
v_mul_f32 v119,v23,v119
D 0,56,3072
v_mul_f32 v120,v24,v120
v_mul_f32 v121,v25,v121
v_exp_f32 v118,v118
v_exp_f32 v119,v119
v_exp_f32 v120,v120
v_exp_f32 v121,v121
s_add_u32 s55,s55,s60
s_sub_u32 s57,s84,s57
s_add_u32 s54,s54,s46
v_fma_f32 v118,v118,v101,v101
v_fma_f32 v119,v119,v101,v101
v_fma_f32 v120,v120,v101,v101
v_fma_f32 v121,v121,v101,v101
v_rcp_f32 v118,v118
v_rcp_f32 v119,v119
v_rcp_f32 v120,v120
v_rcp_f32 v121,v121
v_mul_f32 v22,v22,v118
v_mul_f32 v23,v23,v119
v_mul_f32 v24,v24,v120
v_mul_f32 v25,v25,v121
v_cvt_pk_bf16_f32 v124,v22,v23
v_cvt_pk_bf16_f32 v125,v24,v25
buffer_store_dwordx4 v[122:125],v0,s[36:39],s66 offen offset:0 sc0 sc1
s_mov_b32 s72,s69
s_mov_b32 s69,0x7fff0000
s_mov_b32 s66,0x7fff0000
s_mov_b32 s76,0x7fff0000
s_branch Lg3o_gox_%=
Lg3o_xn0_%=:
s_cmp_ge_u32 s54,256
s_cbranch_scc1 Lg3o_fin0_%=
s_add_u32 m0,s59,4096
s_nop 0
buffer_load_dword v8,s[36:39],s58 offen offset:0 sc1 lds
buffer_store_dword v4,v3,s[36:39],s71 offen offset:0 sc0 sc1
s_add_u32 m0,s59,5120
s_nop 0
buffer_load_dword v8,s[36:39],s58 offen offset:0 sc1 lds
buffer_store_dword v4,v3,s[36:39],s71 offen offset:0 sc0 sc1
s_mov_b32 s83,0
L 0
v_fma_f32 v18,-v98,v106,v18
v_fma_f32 v19,-v98,v107,v19
v_fma_f32 v20,-v98,v108,v20
v_fma_f32 v21,-v98,v109,v21
v_mul_f32 v114,v18,v18
v_mul_f32 v115,v19,v19
v_mul_f32 v116,v20,v20
v_mul_f32 v117,v21,v21
v_fma_f32 v114,v100,v114,v99
v_fma_f32 v115,v100,v115,v99
v_fma_f32 v116,v100,v116,v99
v_fma_f32 v117,v100,v117,v99
v_mul_f32 v114,v18,v114
v_mul_f32 v115,v19,v115
v_mul_f32 v116,v20,v116
v_mul_f32 v117,v21,v117
v_exp_f32 v114,v114
v_exp_f32 v115,v115
v_exp_f32 v116,v116
v_exp_f32 v117,v117
v_fma_f32 v114,v114,v101,v101
v_fma_f32 v115,v115,v101,v101
v_fma_f32 v116,v116,v101,v101
v_fma_f32 v117,v117,v101,v101
v_rcp_f32 v114,v114
v_rcp_f32 v115,v115
v_rcp_f32 v116,v116
v_rcp_f32 v117,v117
v_mul_f32 v18,v18,v114
v_mul_f32 v19,v19,v115
v_mul_f32 v20,v20,v116
v_mul_f32 v21,v21,v117
v_cvt_pk_bf16_f32 v122,v18,v19
v_cvt_pk_bf16_f32 v123,v20,v21
v_fma_f32 v22,-v98,v110,v22
v_fma_f32 v23,-v98,v111,v23
v_fma_f32 v24,-v98,v112,v24
v_fma_f32 v25,-v98,v113,v25
v_mul_f32 v118,v22,v22
v_mul_f32 v119,v23,v23
v_mul_f32 v120,v24,v24
v_mul_f32 v121,v25,v25
v_fma_f32 v118,v100,v118,v99
v_fma_f32 v119,v100,v119,v99
v_fma_f32 v120,v100,v120,v99
v_fma_f32 v121,v100,v121,v99
v_mul_f32 v118,v22,v118
v_mul_f32 v119,v23,v119
v_mul_f32 v120,v24,v120
v_mul_f32 v121,v25,v121
v_exp_f32 v118,v118
v_exp_f32 v119,v119
v_exp_f32 v120,v120
v_exp_f32 v121,v121
v_fma_f32 v118,v118,v101,v101
v_fma_f32 v119,v119,v101,v101
v_fma_f32 v120,v120,v101,v101
v_fma_f32 v121,v121,v101,v101
v_rcp_f32 v118,v118
v_rcp_f32 v119,v119
v_rcp_f32 v120,v120
v_rcp_f32 v121,v121
v_mul_f32 v22,v22,v118
v_mul_f32 v23,v23,v119
v_mul_f32 v24,v24,v120
v_mul_f32 v25,v25,v121
v_cvt_pk_bf16_f32 v124,v22,v23
v_cvt_pk_bf16_f32 v125,v24,v25
buffer_store_dwordx4 v[122:125],v0,s[36:39],s66 offen offset:0 sc0 sc1
s_mov_b32 s72,s69
s_mov_b32 s69,0x7fff0000
s_mov_b32 s66,0x7fff0000
s_mov_b32 s76,0x7fff0000
s_branch Lg3o_poll_%=
Lg3o_fin0_%=:
L 0
v_fma_f32 v18,-v98,v106,v18
v_fma_f32 v19,-v98,v107,v19
v_fma_f32 v20,-v98,v108,v20
v_fma_f32 v21,-v98,v109,v21
v_mul_f32 v114,v18,v18
v_mul_f32 v115,v19,v19
v_mul_f32 v116,v20,v20
v_mul_f32 v117,v21,v21
v_fma_f32 v114,v100,v114,v99
v_fma_f32 v115,v100,v115,v99
v_fma_f32 v116,v100,v116,v99
v_fma_f32 v117,v100,v117,v99
v_mul_f32 v114,v18,v114
v_mul_f32 v115,v19,v115
v_mul_f32 v116,v20,v116
v_mul_f32 v117,v21,v117
v_exp_f32 v114,v114
v_exp_f32 v115,v115
v_exp_f32 v116,v116
v_exp_f32 v117,v117
v_fma_f32 v114,v114,v101,v101
v_fma_f32 v115,v115,v101,v101
v_fma_f32 v116,v116,v101,v101
v_fma_f32 v117,v117,v101,v101
v_rcp_f32 v114,v114
v_rcp_f32 v115,v115
v_rcp_f32 v116,v116
v_rcp_f32 v117,v117
v_mul_f32 v18,v18,v114
v_mul_f32 v19,v19,v115
v_mul_f32 v20,v20,v116
v_mul_f32 v21,v21,v117
v_cvt_pk_bf16_f32 v122,v18,v19
v_cvt_pk_bf16_f32 v123,v20,v21
v_fma_f32 v22,-v98,v110,v22
v_fma_f32 v23,-v98,v111,v23
v_fma_f32 v24,-v98,v112,v24
v_fma_f32 v25,-v98,v113,v25
v_mul_f32 v118,v22,v22
v_mul_f32 v119,v23,v23
v_mul_f32 v120,v24,v24
v_mul_f32 v121,v25,v25
v_fma_f32 v118,v100,v118,v99
v_fma_f32 v119,v100,v119,v99
v_fma_f32 v120,v100,v120,v99
v_fma_f32 v121,v100,v121,v99
v_mul_f32 v118,v22,v118
v_mul_f32 v119,v23,v119
v_mul_f32 v120,v24,v120
v_mul_f32 v121,v25,v121
v_exp_f32 v118,v118
v_exp_f32 v119,v119
v_exp_f32 v120,v120
v_exp_f32 v121,v121
v_fma_f32 v118,v118,v101,v101
v_fma_f32 v119,v119,v101,v101
v_fma_f32 v120,v120,v101,v101
v_fma_f32 v121,v121,v101,v101
v_rcp_f32 v118,v118
v_rcp_f32 v119,v119
v_rcp_f32 v120,v120
v_rcp_f32 v121,v121
v_mul_f32 v22,v22,v118
v_mul_f32 v23,v23,v119
v_mul_f32 v24,v24,v120
v_mul_f32 v25,v25,v121
v_cvt_pk_bf16_f32 v124,v22,v23
v_cvt_pk_bf16_f32 v125,v24,v25
buffer_store_dwordx4 v[122:125],v0,s[36:39],s66 offen offset:0 sc0 sc1
s_mov_b32 s72,s69
s_mov_b32 s69,0x7fff0000
s_mov_b32 s66,0x7fff0000
s_mov_b32 s76,0x7fff0000
s_branch Lg3o_done_%=
Lg3o_K1_%=:
Lg3o_L1_%=:
v_mfma_f32_16x16x32_bf16 v[30:33],a[4:7],v[34:37],0
s_add_u32 m0,s59,3072
R 54,2,5120
v_add_f32_dpp v131,v130,v130 quad_perm:[1,0,3,2] row_mask:0xf bank_mask:0xf
M 30,a[12:15],38
buffer_load_dword v8,s[36:39],s58 offen offset:0 sc1 lds
v_mfma_f32_16x16x32_bf16 v[26:29],a[0:3],v[34:37],0
s_mov_b32 s67,s65
R 58,2,6144
v_add_f32_dpp v131,v131,v131 quad_perm:[2,3,0,1] row_mask:0xf bank_mask:0xf
M 26,a[8:11],38
L 3
v_fma_f32 v18,-v98,v106,v18
v_fma_f32 v19,-v98,v107,v19
M 26,a[16:19],42
s_add_u32 s65,s65,s62
R 62,2,7168
v_add_f32_dpp v134,v131,v131 row_half_mirror row_mask:0xf bank_mask:0xf
M 26,a[24:27],46
v_fma_f32 v20,-v98,v108,v20
v_fma_f32 v21,-v98,v109,v21
M 30,a[20:23],42
R 66,2,8192
v_mov_b32_dpp v131,v134 row_ror:8 row_mask:0xf bank_mask:0xf
M 30,a[28:31],46
L 3
v_fma_f32 v131,-v134,v134,v131
v_max_f32 v131,s87,v131
M 30,a[36:39],50
R 70,2,9216
v_mul_f32 v134,v17,v134
M 30,a[44:47],54
v_rsq_f32 v132,v131
M 26,a[32:35],50
R 74,2,10240
v_fma_f32 v131,v15,v131,v16
M 26,a[40:43],54
L 3
v_mul_f32 v135,v132,v132
v_fma_f32 v131,v14,v135,v131
M 26,a[48:51],58
R 78,2,11264
v_fma_f32 v134,v132,v131,v134
M 26,a[56:59],62
ds_write_b32 v12,v134 offset:2048
M 30,a[52:55],58
R 82,2,12288
v_mul_f32 v114,v18,v18
M 30,a[60:63],62
L 4
v_mul_f32 v115,v19,v19
v_mul_f32 v116,v20,v20
M 30,a[68:71],66
R 86,2,13312
v_mul_f32 v117,v21,v21
M 30,a[76:79],70
v_fma_f32 v114,v100,v114,v99
v_fma_f32 v115,v100,v115,v99
M 26,a[64:67],66
R 90,2,14336
v_fma_f32 v116,v100,v116,v99
M 26,a[72:75],70
L 4
v_fma_f32 v117,v100,v117,v99
v_mul_f32 v114,v18,v114
M 26,a[80:83],74
s_waitcnt vmcnt(0)
R 94,2,15360
v_mul_f32 v115,v19,v115
M 26,a[88:91],78
s_barrier
ds_read_b32 v7,v6 offset:3072
v_mul_f32 v116,v20,v116
M 30,a[84:87],74
R 34,2,16384
v_mul_f32 v117,v21,v117
M 30,a[92:95],78
L 4
buffer_store_dword v4,v3,s[36:39],s70 offen offset:0 sc0 sc1
M 30,a[100:103],82
s_mov_b32 s70,0x7fff0000
R 38,2,17408
v_fma_f32 v22,-v98,v110,v22
M 30,a[108:111],86
v_exp_f32 v114,v114
M 26,a[96:99],82
R 42,2,18432
v_fma_f32 v114,v114,v101,v101
M 26,a[104:107],86
L 4
L 3
v_cmp_eq_u32 vcc,s53,v7
M 26,a[112:115],90
R 46,2,19456
v_fma_f32 v23,-v98,v111,v23
M 26,a[120:123],94
v_exp_f32 v115,v115
M 30,a[116:119],90
s_cmp_eq_u32 vcc_lo,-1
R 50,2,20480
v_fma_f32 v115,v115,v101,v101
s_cbranch_scc0 Lg3o_X1_%=
M 30,a[124:127],94
L 3
s_add_u32 m0,s57,0
s_add_u32 s56,s55,0
buffer_load_dword v130,v11,s[36:39],s73 offen offset:0 sc1
M 30,a[132:135],34
s_add_u32 s58,s58,s61
R 54,2,21504
v_fma_f32 v24,-v98,v112,v24
M 30,a[140:143],38
D 0,56,0
M 26,a[128:131],34
s_add_u32 s54,s54,s46
R 58,2,22528
v_fma_f32 v25,-v98,v113,v25
M 26,a[136:139],38
L 3
v_exp_f32 v116,v116
M 26,a[144:147],42
s_add_u32 s73,s73,s64
R 62,2,23552
v_fma_f32 v116,v116,v101,v101
M 26,a[152:155],46
D 0,56,1024
M 30,a[148:151],42
R 66,2,24576
v_mul_f32 v118,v22,v22
M 30,a[156:159],46
L 3
v_exp_f32 v117,v117
M 30,a[164:167],50
R 70,2,25600
v_fma_f32 v117,v117,v101,v101
M 30,a[172:175],54
D 0,56,2048
M 26,a[160:163],50
R 74,2,26624
v_mul_f32 v119,v23,v23
M 26,a[168:171],54
L 3
v_rcp_f32 v114,v114
M 26,a[176:179],58
R 78,2,27648
v_mul_f32 v18,v18,v114
M 26,a[184:187],62
D 0,56,3072
M 30,a[180:183],58
s_add_u32 m0,s59,1024
R 82,2,28672
v_mul_f32 v120,v24,v24
M 30,a[188:191],62
L 3
buffer_load_dword v8,s[36:39],s58 offen offset:0 sc1 lds
M 30,a[196:199],66
s_add_u32 m0,s57,16384
R 86,2,29696
v_mul_f32 v121,v25,v25
M 30,a[204:207],70
s_add_u32 s56,s55,16384
v_rcp_f32 v115,v115
M 26,a[192:195],66
s_add_u32 s55,s55,s60
R 90,2,30720
v_mul_f32 v19,v19,v115
M 26,a[200:203],70
L 3
s_nop 0
D 0,56,0
M 26,a[208:211],74
s_waitcnt vmcnt(1)
R 94,2,31744
v_cvt_pk_bf16_f32 v122,v18,v19
M 26,a[216:219],78
s_barrier
ds_read_b32 v7,v6 offset:1024
R 102,13,2048
M 30,a[212:215],74
s_sub_u32 s57,s84,s57
R 34,1,0
v_fma_f32 v118,v100,v118,v99
M 30,a[220:223],78
L 5
buffer_store_dword v4,v3,s[36:39],s70 offen offset:0 sc0 sc1
M 30,a[228:231],82
s_mov_b32 s70,s69
R 38,1,1024
v_fma_f32 v119,v100,v119,v99
M 30,a[236:239],86
D 0,56,1024
M 26,a[224:227],82
s_mov_b32 s69,s68
R 42,1,2048
v_fma_f32 v120,v100,v120,v99
M 26,a[232:235],86
L 5
v_fma_f32 v121,v100,v121,v99
v_mul_f32 v118,v22,v118
v_mul_f32 v119,v23,v119
v_mul_f32 v120,v24,v120
v_mul_f32 v121,v25,v121
v_exp_f32 v118,v118
v_exp_f32 v119,v119
v_exp_f32 v120,v120
v_exp_f32 v121,v121
M 26,a[240:243],90
L 4
R 46,1,3072
v_rcp_f32 v116,v116
v_rcp_f32 v117,v117
v_fma_f32 v118,v118,v101,v101
v_fma_f32 v119,v119,v101,v101
v_fma_f32 v120,v120,v101,v101
v_fma_f32 v121,v121,v101,v101
v_rcp_f32 v118,v118
v_rcp_f32 v119,v119
v_rcp_f32 v120,v120
v_rcp_f32 v121,v121
v_cmp_eq_u32 vcc,s53,v7
M 26,a[248:251],94
s_add_u32 s68,s68,s63
v_mul_f32 v20,v20,v116
v_mul_f32 v21,v21,v117
v_cvt_pk_bf16_f32 v123,v20,v21
)"
R"(v_mul_f32 v22,v22,v118
v_mul_f32 v23,v23,v119
v_mul_f32 v24,v24,v120
v_mul_f32 v25,v25,v121
v_cvt_pk_bf16_f32 v124,v22,v23
v_cvt_pk_bf16_f32 v125,v24,v25
M 30,a[244:247],90
L 2
s_cmp_eq_u32 vcc_lo,-1
R 50,1,4096
buffer_store_dwordx4 v[122:125],v0,s[36:39],s66 offen offset:0 sc0 sc1
M 30,a[252:255],94
D 0,56,2048
D 0,56,3072
buffer_store_dword v4,v3,s[36:39],s71 offen offset:0 sc0 sc1
buffer_store_dword v4,v3,s[36:39],s71 offen offset:0 sc0 sc1
s_cbranch_scc1 Lg3o_F0_%=
s_branch Lg3o_K0_%=
Lg3o_X1_%=:
M 30,a[124:127],94
L 3
s_add_u32 m0,s59,1024
v_fma_f32 v24,-v98,v112,v24
M 30,a[132:135],34
R 54,2,21504
v_fma_f32 v25,-v98,v113,v25
M 30,a[140:143],38
buffer_load_dword v8,s[36:39],s58 offen offset:0 sc1 lds
M 26,a[128:131],34
R 58,2,22528
v_mul_f32 v118,v22,v22
M 26,a[136:139],38
L 3
v_exp_f32 v116,v116
M 26,a[144:147],42
R 62,2,23552
v_fma_f32 v116,v116,v101,v101
M 26,a[152:155],46
v_exp_f32 v117,v117
M 30,a[148:151],42
R 66,2,24576
v_fma_f32 v117,v117,v101,v101
M 30,a[156:159],46
L 3
v_rcp_f32 v114,v114
v_mul_f32 v119,v23,v23
M 30,a[164:167],50
R 70,2,25600
v_mul_f32 v18,v18,v114
M 30,a[172:175],54
v_rcp_f32 v115,v115
M 26,a[160:163],50
R 74,2,26624
v_mul_f32 v19,v19,v115
M 26,a[168:171],54
L 3
v_rcp_f32 v116,v116
M 26,a[176:179],58
R 78,2,27648
v_mul_f32 v20,v20,v116
M 26,a[184:187],62
v_rcp_f32 v117,v117
M 30,a[180:183],58
R 82,2,28672
v_mul_f32 v21,v21,v117
M 30,a[188:191],62
L 3
v_cvt_pk_bf16_f32 v122,v18,v19
v_cvt_pk_bf16_f32 v123,v20,v21
M 30,a[196:199],66
R 86,2,29696
v_mul_f32 v120,v24,v24
M 30,a[204:207],70
v_mul_f32 v121,v25,v25
v_fma_f32 v118,v100,v118,v99
M 26,a[192:195],66
R 90,2,30720
v_fma_f32 v119,v100,v119,v99
M 26,a[200:203],70
L 3
s_nop 0
v_fma_f32 v120,v100,v120,v99
M 26,a[208:211],74
s_waitcnt vmcnt(0)
R 94,2,31744
v_fma_f32 v121,v100,v121,v99
M 26,a[216:219],78
s_barrier
buffer_store_dword v4,v3,s[36:39],s70 offen offset:0 sc0 sc1
M 30,a[212:215],74
s_mov_b32 s70,s69
ds_read_b32 v7,v6 offset:1024
R 102,13,2048
M 30,a[220:223],78
L 4
v_mul_f32 v118,v22,v118
v_mul_f32 v119,v23,v119
M 30,a[228:231],82
s_mov_b32 s69,s68
v_mul_f32 v120,v24,v120
v_mul_f32 v121,v25,v121
M 30,a[236:239],86
s_add_u32 s68,s68,s63
v_exp_f32 v118,v118
M 26,a[224:227],82
v_exp_f32 v119,v119
M 26,a[232:235],86
L 2
v_exp_f32 v120,v120
v_exp_f32 v121,v121
M 26,a[240:243],90
L 1
v_fma_f32 v118,v118,v101,v101
v_fma_f32 v119,v119,v101,v101
v_fma_f32 v120,v120,v101,v101
v_fma_f32 v121,v121,v101,v101
v_rcp_f32 v118,v118
v_rcp_f32 v119,v119
v_rcp_f32 v120,v120
v_rcp_f32 v121,v121
M 26,a[248:251],94
v_mul_f32 v22,v22,v118
v_mul_f32 v23,v23,v119
v_mul_f32 v24,v24,v120
v_mul_f32 v25,v25,v121
v_cvt_pk_bf16_f32 v124,v22,v23
v_cvt_pk_bf16_f32 v125,v24,v25
M 30,a[244:247],90
buffer_store_dwordx4 v[122:125],v0,s[36:39],s66 offen offset:0 sc0 sc1
v_cmp_eq_u32 vcc,s53,v7
M 30,a[252:255],94
s_mov_b32 s80,0
s_cmp_eq_u32 vcc_lo,-1
s_cbranch_scc0 Lg3o_xn1_%=
buffer_load_dword v130,v11,s[36:39],s73 offen offset:0 sc1
L 0
v_fma_f32 v26,-v102,v106,v26
v_fma_f32 v27,-v102,v107,v27
v_fma_f32 v28,-v102,v108,v28
v_fma_f32 v29,-v102,v109,v29
v_mul_f32 v114,v26,v26
v_mul_f32 v115,v27,v27
s_add_u32 s73,s73,s64
s_add_u32 m0,s57,0
s_add_u32 s56,s55,0
D 0,56,0
v_mul_f32 v116,v28,v28
v_mul_f32 v117,v29,v29
v_fma_f32 v114,v104,v114,v103
v_fma_f32 v115,v104,v115,v103
v_fma_f32 v116,v104,v116,v103
v_fma_f32 v117,v104,v117,v103
D 0,56,1024
v_mul_f32 v114,v26,v114
v_mul_f32 v115,v27,v115
v_mul_f32 v116,v28,v116
v_mul_f32 v117,v29,v117
v_exp_f32 v114,v114
v_exp_f32 v115,v115
D 0,56,2048
v_exp_f32 v116,v116
v_exp_f32 v117,v117
v_fma_f32 v114,v114,v105,v105
v_fma_f32 v115,v115,v105,v105
v_fma_f32 v116,v116,v105,v105
v_fma_f32 v117,v117,v105,v105
D 0,56,3072
v_rcp_f32 v114,v114
v_rcp_f32 v115,v115
v_rcp_f32 v116,v116
v_rcp_f32 v117,v117
v_mul_f32 v26,v26,v114
v_mul_f32 v27,v27,v115
s_add_u32 s58,s58,s61
s_add_u32 m0,s57,16384
s_add_u32 s56,s55,16384
D 0,56,0
v_mul_f32 v28,v28,v116
v_mul_f32 v29,v29,v117
v_cvt_pk_bf16_f32 v122,v26,v27
v_cvt_pk_bf16_f32 v123,v28,v29
v_fma_f32 v30,-v102,v110,v30
v_fma_f32 v31,-v102,v111,v31
D 0,56,1024
v_fma_f32 v32,-v102,v112,v32
v_fma_f32 v33,-v102,v113,v33
v_mul_f32 v118,v30,v30
v_mul_f32 v119,v31,v31
v_mul_f32 v120,v32,v32
v_mul_f32 v121,v33,v33
D 0,56,2048
v_fma_f32 v118,v104,v118,v103
v_fma_f32 v119,v104,v119,v103
v_fma_f32 v120,v104,v120,v103
v_fma_f32 v121,v104,v121,v103
v_mul_f32 v118,v30,v118
v_mul_f32 v119,v31,v119
D 0,56,3072
v_mul_f32 v120,v32,v120
v_mul_f32 v121,v33,v121
v_exp_f32 v118,v118
v_exp_f32 v119,v119
v_exp_f32 v120,v120
v_exp_f32 v121,v121
s_add_u32 s55,s55,s60
s_sub_u32 s57,s84,s57
s_add_u32 s54,s54,s46
v_fma_f32 v118,v118,v105,v105
v_fma_f32 v119,v119,v105,v105
v_fma_f32 v120,v120,v105,v105
v_fma_f32 v121,v121,v105,v105
v_rcp_f32 v118,v118
v_rcp_f32 v119,v119
v_rcp_f32 v120,v120
v_rcp_f32 v121,v121
v_mul_f32 v30,v30,v118
v_mul_f32 v31,v31,v119
v_mul_f32 v32,v32,v120
v_mul_f32 v33,v33,v121
v_cvt_pk_bf16_f32 v124,v30,v31
v_cvt_pk_bf16_f32 v125,v32,v33
buffer_store_dwordx4 v[122:125],v0,s[36:39],s67 offen offset:0 sc0 sc1
s_mov_b32 s72,s69
s_mov_b32 s69,0x7fff0000
s_mov_b32 s67,0x7fff0000
s_mov_b32 s77,0x7fff0000
s_branch Lg3o_gox_%=
Lg3o_xn1_%=:
s_cmp_ge_u32 s54,256
s_cbranch_scc1 Lg3o_fin1_%=
s_add_u32 m0,s59,4096
s_nop 0
buffer_load_dword v8,s[36:39],s58 offen offset:0 sc1 lds
buffer_store_dword v4,v3,s[36:39],s71 offen offset:0 sc0 sc1
s_add_u32 m0,s59,5120
s_nop 0
buffer_load_dword v8,s[36:39],s58 offen offset:0 sc1 lds
buffer_store_dword v4,v3,s[36:39],s71 offen offset:0 sc0 sc1
s_mov_b32 s83,0
L 0
v_fma_f32 v26,-v102,v106,v26
v_fma_f32 v27,-v102,v107,v27
v_fma_f32 v28,-v102,v108,v28
v_fma_f32 v29,-v102,v109,v29
v_mul_f32 v114,v26,v26
v_mul_f32 v115,v27,v27
v_mul_f32 v116,v28,v28
v_mul_f32 v117,v29,v29
v_fma_f32 v114,v104,v114,v103
v_fma_f32 v115,v104,v115,v103
v_fma_f32 v116,v104,v116,v103
v_fma_f32 v117,v104,v117,v103
v_mul_f32 v114,v26,v114
v_mul_f32 v115,v27,v115
v_mul_f32 v116,v28,v116
v_mul_f32 v117,v29,v117
v_exp_f32 v114,v114
v_exp_f32 v115,v115
v_exp_f32 v116,v116
v_exp_f32 v117,v117
v_fma_f32 v114,v114,v105,v105
v_fma_f32 v115,v115,v105,v105
v_fma_f32 v116,v116,v105,v105
v_fma_f32 v117,v117,v105,v105
v_rcp_f32 v114,v114
v_rcp_f32 v115,v115
v_rcp_f32 v116,v116
v_rcp_f32 v117,v117
v_mul_f32 v26,v26,v114
v_mul_f32 v27,v27,v115
v_mul_f32 v28,v28,v116
v_mul_f32 v29,v29,v117
v_cvt_pk_bf16_f32 v122,v26,v27
v_cvt_pk_bf16_f32 v123,v28,v29
v_fma_f32 v30,-v102,v110,v30
v_fma_f32 v31,-v102,v111,v31
v_fma_f32 v32,-v102,v112,v32
v_fma_f32 v33,-v102,v113,v33
v_mul_f32 v118,v30,v30
v_mul_f32 v119,v31,v31
v_mul_f32 v120,v32,v32
v_mul_f32 v121,v33,v33
v_fma_f32 v118,v104,v118,v103
v_fma_f32 v119,v104,v119,v103
v_fma_f32 v120,v104,v120,v103
v_fma_f32 v121,v104,v121,v103
v_mul_f32 v118,v30,v118
v_mul_f32 v119,v31,v119
v_mul_f32 v120,v32,v120
v_mul_f32 v121,v33,v121
v_exp_f32 v118,v118
v_exp_f32 v119,v119
v_exp_f32 v120,v120
v_exp_f32 v121,v121
v_fma_f32 v118,v118,v105,v105
v_fma_f32 v119,v119,v105,v105
v_fma_f32 v120,v120,v105,v105
v_fma_f32 v121,v121,v105,v105
v_rcp_f32 v118,v118
v_rcp_f32 v119,v119
v_rcp_f32 v120,v120
v_rcp_f32 v121,v121
v_mul_f32 v30,v30,v118
v_mul_f32 v31,v31,v119
v_mul_f32 v32,v32,v120
v_mul_f32 v33,v33,v121
v_cvt_pk_bf16_f32 v124,v30,v31
v_cvt_pk_bf16_f32 v125,v32,v33
buffer_store_dwordx4 v[122:125],v0,s[36:39],s67 offen offset:0 sc0 sc1
s_mov_b32 s72,s69
s_mov_b32 s69,0x7fff0000
s_mov_b32 s67,0x7fff0000
s_mov_b32 s77,0x7fff0000
s_branch Lg3o_poll_%=
Lg3o_fin1_%=:
L 0
v_fma_f32 v26,-v102,v106,v26
v_fma_f32 v27,-v102,v107,v27
v_fma_f32 v28,-v102,v108,v28
v_fma_f32 v29,-v102,v109,v29
v_mul_f32 v114,v26,v26
v_mul_f32 v115,v27,v27
v_mul_f32 v116,v28,v28
v_mul_f32 v117,v29,v29
v_fma_f32 v114,v104,v114,v103
v_fma_f32 v115,v104,v115,v103
v_fma_f32 v116,v104,v116,v103
v_fma_f32 v117,v104,v117,v103
v_mul_f32 v114,v26,v114
v_mul_f32 v115,v27,v115
v_mul_f32 v116,v28,v116
v_mul_f32 v117,v29,v117
v_exp_f32 v114,v114
v_exp_f32 v115,v115
v_exp_f32 v116,v116
v_exp_f32 v117,v117
v_fma_f32 v114,v114,v105,v105
v_fma_f32 v115,v115,v105,v105
v_fma_f32 v116,v116,v105,v105
v_fma_f32 v117,v117,v105,v105
v_rcp_f32 v114,v114
v_rcp_f32 v115,v115
v_rcp_f32 v116,v116
v_rcp_f32 v117,v117
v_mul_f32 v26,v26,v114
v_mul_f32 v27,v27,v115
v_mul_f32 v28,v28,v116
v_mul_f32 v29,v29,v117
v_cvt_pk_bf16_f32 v122,v26,v27
v_cvt_pk_bf16_f32 v123,v28,v29
v_fma_f32 v30,-v102,v110,v30
v_fma_f32 v31,-v102,v111,v31
v_fma_f32 v32,-v102,v112,v32
v_fma_f32 v33,-v102,v113,v33
v_mul_f32 v118,v30,v30
v_mul_f32 v119,v31,v31
v_mul_f32 v120,v32,v32
v_mul_f32 v121,v33,v33
v_fma_f32 v118,v104,v118,v103
v_fma_f32 v119,v104,v119,v103
v_fma_f32 v120,v104,v120,v103
v_fma_f32 v121,v104,v121,v103
v_mul_f32 v118,v30,v118
v_mul_f32 v119,v31,v119
v_mul_f32 v120,v32,v120
v_mul_f32 v121,v33,v121
v_exp_f32 v118,v118
v_exp_f32 v119,v119
v_exp_f32 v120,v120
v_exp_f32 v121,v121
v_fma_f32 v118,v118,v105,v105
v_fma_f32 v119,v119,v105,v105
v_fma_f32 v120,v120,v105,v105
v_fma_f32 v121,v121,v105,v105
v_rcp_f32 v118,v118
v_rcp_f32 v119,v119
v_rcp_f32 v120,v120
v_rcp_f32 v121,v121
v_mul_f32 v30,v30,v118
v_mul_f32 v31,v31,v119
v_mul_f32 v32,v32,v120
v_mul_f32 v33,v33,v121
v_cvt_pk_bf16_f32 v124,v30,v31
v_cvt_pk_bf16_f32 v125,v32,v33
buffer_store_dwordx4 v[122:125],v0,s[36:39],s67 offen offset:0 sc0 sc1
s_mov_b32 s72,s69
s_mov_b32 s69,0x7fff0000
s_mov_b32 s67,0x7fff0000
s_mov_b32 s77,0x7fff0000
s_branch Lg3o_done_%=
Lg3o_done_%=:
s_waitcnt vmcnt(1)
s_barrier
buffer_store_dword v4,v3,s[36:39],s70 offen offset:0 sc0 sc1
s_waitcnt vmcnt(1)
s_barrier
buffer_store_dword v4,v3,s[36:39],s72 offen offset:0 sc0 sc1
s_waitcnt vmcnt(0) lgkmcnt(0)
s_barrier
s_endpgm
Lcode_g3o_e:
.purgem M
.purgem R
.purgem T
.purgem D
.purgem L
)"
  :: "s"(ws), "s"(__builtin_amdgcn_readfirstlane(sWave)), "s"(__builtin_amdgcn_readfirstlane(sG0)), "s"(__builtin_amdgcn_readfirstlane(sStep)), "s"(__builtin_amdgcn_readfirstlane(sOutOff)), "s"(__builtin_amdgcn_readfirstlane(sFoutOff)), "s"(__builtin_amdgcn_readfirstlane(sAuxOff)), "s"(__builtin_amdgcn_readfirstlane(sWw)), "s"(__builtin_amdgcn_readfirstlane(sBuf)), "s"(__builtin_amdgcn_readfirstlane(sExp)), "v"(vL16), "v"(vFlag), "v"(vSnapOff), "v"(vSnapRd), "v"(vWd0), "v"(vWd1), "v"(vWr0), "v"(vWr1), "v"(vWr2), "v"(vOutS), "v"(vAux0), "v"(vAux1), "v"(vAux2), "v"(vAux3), "v"(vAn), "v"(vAe), "s"(w3), "s"(out) : "v255", "v0", "v1", "v2", "v3", "v4", "v5", "v6", "v7", "v8", "v9", "v10", "v11", "v12", "v13", "v14", "v15", "v16", "v17", "v18", "v19", "v20", "v21", "v22", "v23", "v24", "v25", "v26", "v27", "v28", "v29", "v30", "v31", "v32", "v33", "v34", "v35", "v36", "v37", "v38", "v39", "v40", "v41", "v42", "v43", "v44", "v45", "v46", "v47", "v48", "v49", "v50", "v51", "v52", "v53", "v54", "v55", "v56", "v57", "v58", "v59", "v60", "v61", "v62", "v63", "v64", "v65", "v66", "v67", "v68", "v69", "v70", "v71", "v72", "v73", "v74", "v75", "v76", "v77", "v78", "v79", "v80", "v81", "v82", "v83", "v84", "v85", "v86", "v87", "v88", "v89", "v90", "v91", "v92", "v93", "v94", "v95", "v96", "v97", "v98", "v99", "v100", "v101", "v102", "v103", "v104", "v105", "v106", "v107", "v108", "v109", "v110", "v111", "v112", "v113", "v114", "v115", "v116", "v117", "v118", "v119", "v120", "v121", "v122", "v123", "v124", "v125", "v126", "v127", "v128", "v129", "v130", "v131", "v132", "v133", "v134", "v135", "v136", "v137", "v138", "v139", "v140", "v141", "v142", "v143", "v144", "v145", "v146", "v147", "v148", "v149", "v150", "v151", "v152", "v153", "v154", "v155", "v156", "v157", "v158", "v159", "v160", "v161", "v162", "v163", "v164", "v165", "v166", "v167", "v168", "v169", "v170", "v171", "v172", "v173", "v174", "v175", "v176", "v177", "v178", "v179", "v180", "v181", "v182", "v183", "v184", "v185", "v186", "v187", "v188", "v189", "v190", "v191", "v192", "v193", "v194", "v195", "v196", "v197", "v198", "v199", "v200", "v201", "v202", "v203", "v204", "v205", "v206", "v207", "v208", "v209", "v210", "v211", "v212", "v213", "v214", "v215", "v216", "v217", "v218", "v219", "v220", "v221", "v222", "v223", "v224", "v225", "v226", "v227", "v228", "v229", "v230", "v231", "a0", "a1", "a2", "a3", "a4", "a5", "a6", "a7", "a8", "a9", "a10", "a11", "a12", "a13", "a14", "a15", "a16", "a17", "a18", "a19", "a20", "a21", "a22", "a23", "a24", "a25", "a26", "a27", "a28", "a29", "a30", "a31", "a32", "a33", "a34", "a35", "a36", "a37", "a38", "a39", "a40", "a41", "a42", "a43", "a44", "a45", "a46", "a47", "a48", "a49", "a50", "a51", "a52", "a53", "a54", "a55", "a56", "a57", "a58", "a59", "a60", "a61", "a62", "a63", "a64", "a65", "a66", "a67", "a68", "a69", "a70", "a71", "a72", "a73", "a74", "a75", "a76", "a77", "a78", "a79", "a80", "a81", "a82", "a83", "a84", "a85", "a86", "a87", "a88", "a89", "a90", "a91", "a92", "a93", "a94", "a95", "a96", "a97", "a98", "a99", "a100", "a101", "a102", "a103", "a104", "a105", "a106", "a107", "a108", "a109", "a110", "a111", "a112", "a113", "a114", "a115", "a116", "a117", "a118", "a119", "a120", "a121", "a122", "a123", "a124", "a125", "a126", "a127", "a128", "a129", "a130", "a131", "a132", "a133", "a134", "a135", "a136", "a137", "a138", "a139", "a140", "a141", "a142", "a143", "a144", "a145", "a146", "a147", "a148", "a149", "a150", "a151", "a152", "a153", "a154", "a155", "a156", "a157", "a158", "a159", "a160", "a161", "a162", "a163", "a164", "a165", "a166", "a167", "a168", "a169", "a170", "a171", "a172", "a173", "a174", "a175", "a176", "a177", "a178", "a179", "a180", "a181", "a182", "a183", "a184", "a185", "a186", "a187", "a188", "a189", "a190", "a191", "a192", "a193", "a194", "a195", "a196", "a197", "a198", "a199", "a200", "a201", "a202", "a203", "a204", "a205", "a206", "a207", "a208", "a209", "a210", "a211", "a212", "a213", "a214", "a215", "a216", "a217", "a218", "a219", "a220", "a221", "a222", "a223", "a224", "a225", "a226", "a227", "a228", "a229", "a230", "a231", "a232", "a233", "a234", "a235", "a236", "a237", "a238", "a239", "a240", "a241", "a242", "a243", "a244", "a245", "a246", "a247", "a248", "a249", "a250", "a251", "a252", "a253", "a254", "a255", "s36", "s37", "s38", "s39", "s40", "s41", "s42", "s43", "s44", "s45", "s46", "s47", "s48", "s49", "s50", "s51", "s52", "s53", "s54", "s55", "s56", "s57", "s58", "s59", "s60", "s61", "s62", "s63", "s64", "s65", "s66", "s67", "s68", "s69", "s70", "s71", "s72", "s73", "s74", "s75", "s76", "s77", "s78", "s79", "s80", "s81", "s82", "s83", "s84", "s85", "s86", "s87", "s88", "s89", "s90", "s91", "s92", "s93", "s94", "s95", "s96", "s97", "vcc", "scc", "memory"); }
    __builtin_unreachable();
  }
  if (kind < 3) {
    const unsigned sStep = 4;
    COMMON(kind == 1 ? 8 : 16, kind == 1 ? fj * 128 : fj < 8 ? 1024 + fj * 128 : (fj & 2) * 1024 + (fj - 8) * 128, 1024, 131072, 2048, 256, kind == 1 ? 0 : 32768);
    const int sr = l15 - ((wave & 2) ? -1 : 1), sr2 = l15 - ((wave & 2) ? -2 : 2), nx = (slice >> 1) & 1;
    auto src = [&](int r_) { return (unsigned)((r_ & 16) ? 1024 + lg * 64 + (r_ & 3) * 16 : (lg * 16 + r_) * 16); };
    const unsigned vOutS = src(sr2), vSp = (sr2 & 16) ? OOB : vOutS;
    (void)vWr2;
    const unsigned vAn = kind == 1 ? HU + vL16 : src(sr);
    const unsigned vAe = kind == 1 ? ((l15 >> 2) == (nx ? 0 : 3) ? (nx ? 0 : 2 * HU) + 1024 + lg * 64 + (l15 & 3) * 16 : OOB) : (sr & 16) ? OOB : vAn;
    const unsigned vAux0 = l15 * 16, vAux1 = 139264 + (l15 * 2 + (lg & 1)) * 16 + wave * 4 + (lg >> 1) * 512, vAux2 = 139264 + (l15 * 2 + (lg & 1)) * 16;
    const unsigned vAux3 = (wave == 0 && lg < 2) ? slice * 128 + l15 * 8 + lg * 4 : OOB;
    const unsigned sOutOff = kind == 1 ? OFF_HS - HU + (slice * 4 + wave) * 1280 : OFF_X1 + (slice * 4 + wave) * 1024, sFoutOff = (kind == 1 ? OFF_FH : OFF_FX1) + slice * 128;
    const unsigned sAuxOff = OFF_XP + (slice * 4 + wave) * 1024, sBuf = kind, sWw = 16 * wave * 2048 + slice * 256;
    const u16* w12 = kind == 1 ? w1 : w2;
    const unsigned sExp = __builtin_amdgcn_readfirstlane(ep);
    unsigned epOut;
    asm volatile(
R"(.macro M d,w,x
v_mfma_f32_16x16x32_bf16 v[\d:\d+3], \w, v[\x:\x+3], v[\d:\d+3]
.endm
.macro R d,a,o
ds_read_b128 v[\d:\d+3], v\a offset:\o
.endm
.macro T d,a,o
ds_read_b64_tr_b16 \d, v\a offset:\o
.endm
.macro D a,s,o
buffer_load_dwordx4 v\a, s[36:39], s\s offen offset:\o sc1 lds
.endm
.macro L c
s_waitcnt lgkmcnt(\c)
.endm
Lcode_g12_b:
s_cmp_eq_u32 %9,2
s_cbranch_scc1 Lg2_begin_%=
s_mov_b64 s[36:37],%1
s_mov_b32 s38,111243392
s_mov_b32 s39,0x00020000
s_mov_b32 s44,%2
s_mov_b32 s45,%3
s_mov_b32 s46,%4
s_mov_b32 s47,%5
s_mov_b32 s48,%6
s_mov_b32 s49,%7
s_mov_b32 s50,%8
s_mov_b32 s51,%9
s_mov_b32 s53,%10
v_mov_b32 v0,%11
v_mov_b32 v3,%12
v_mov_b32 v5,%13
v_mov_b32 v6,%14
v_mov_b32 v18,%15
v_mov_b32 v19,%16
v_mov_b32 v20,%17
v_mov_b32 v21,%18
v_mov_b32 v9,%19
v_mov_b32 v10,%20
v_mov_b32 v11,%21
v_mov_b32 v12,%22
v_mov_b32 v13,%23
v_mov_b32 v14,%24
v_mov_b32 v15,%25
v_mov_b32 v16,%26
s_mov_b64 s[40:41],%27
s_mov_b32 s42,2097152
s_mov_b32 s43,0x00020000
s_lshl_b32 s57,s44,11
s_waitcnt vmcnt(20) lgkmcnt(0)
s_barrier
s_add_u32 s55,s50,786432
s_add_u32 m0,s57,98304
s_nop 0
buffer_load_dwordx4 v18,s[40:43],s55 offen offset:0  lds
s_add_u32 m0,s57,99328
s_nop 0
buffer_load_dwordx4 v19,s[40:43],s55 offen offset:0  lds
s_add_u32 m0,s57,106368
s_nop 0
buffer_load_dwordx4 v18,s[40:43],s55 offen offset:128  lds
s_add_u32 m0,s57,107392
s_nop 0
buffer_load_dwordx4 v19,s[40:43],s55 offen offset:128  lds
v_add_u32 v26,0,v20
v_add_u32 v27,0,v21
T a[0:1],26,0
T a[2:3],26,512
T a[4:5],27,0
T a[6:7],27,512
T a[8:9],26,4096
T a[10:11],26,4608
T a[12:13],27,4096
T a[14:15],27,4608
s_waitcnt vmcnt(20) lgkmcnt(0)
s_barrier
s_add_u32 s55,s50,917504
s_add_u32 m0,s57,0
s_nop 0
buffer_load_dwordx4 v18,s[40:43],s55 offen offset:0  lds
s_add_u32 m0,s57,1024
s_nop 0
buffer_load_dwordx4 v19,s[40:43],s55 offen offset:0  lds
s_add_u32 m0,s57,8064
s_nop 0
buffer_load_dwordx4 v18,s[40:43],s55 offen offset:128  lds
s_add_u32 m0,s57,9088
s_nop 0
buffer_load_dwordx4 v19,s[40:43],s55 offen offset:128  lds
v_add_u32 v26,16384,v20
v_add_u32 v27,16384,v21
T a[16:17],26,0
T a[18:19],26,512
T a[20:21],27,0
T a[22:23],27,512
T a[24:25],26,4096
T a[26:27],26,4608
T a[28:29],27,4096
T a[30:31],27,4608
s_waitcnt vmcnt(20) lgkmcnt(0)
s_barrier
s_add_u32 s55,s50,1048576
s_add_u32 m0,s57,16384
s_nop 0
buffer_load_dwordx4 v18,s[40:43],s55 offen offset:0  lds
s_add_u32 m0,s57,17408
s_nop 0
buffer_load_dwordx4 v19,s[40:43],s55 offen offset:0  lds
s_add_u32 m0,s57,24448
s_nop 0
buffer_load_dwordx4 v18,s[40:43],s55 offen offset:128  lds
s_add_u32 m0,s57,25472
s_nop 0
buffer_load_dwordx4 v19,s[40:43],s55 offen offset:128  lds
v_add_u32 v26,32768,v20
v_add_u32 v27,32768,v21
T a[32:33],26,0
T a[34:35],26,512
T a[36:37],27,0
T a[38:39],27,512
T a[40:41],26,4096
T a[42:43],26,4608
T a[44:45],27,4096
T a[46:47],27,4608
s_waitcnt vmcnt(20) lgkmcnt(0)
s_barrier
s_add_u32 s55,s50,1179648
s_add_u32 m0,s57,32768
s_nop 0
buffer_load_dwordx4 v18,s[40:43],s55 offen offset:0  lds
s_add_u32 m0,s57,33792
s_nop 0
buffer_load_dwordx4 v19,s[40:43],s55 offen offset:0  lds
s_add_u32 m0,s57,40832
s_nop 0
buffer_load_dwordx4 v18,s[40:43],s55 offen offset:128  lds
s_add_u32 m0,s57,41856
s_nop 0
buffer_load_dwordx4 v19,s[40:43],s55 offen offset:128  lds
v_add_u32 v26,49152,v20
v_add_u32 v27,49152,v21
T a[48:49],26,0
T a[50:51],26,512
T a[52:53],27,0
T a[54:55],27,512
T a[56:57],26,4096
T a[58:59],26,4608
T a[60:61],27,4096
T a[62:63],27,4608
s_waitcnt vmcnt(20) lgkmcnt(0)
s_barrier
s_add_u32 s55,s50,1310720
s_add_u32 m0,s57,49152
s_nop 0
buffer_load_dwordx4 v18,s[40:43],s55 offen offset:0  lds
s_add_u32 m0,s57,50176
s_nop 0
buffer_load_dwordx4 v19,s[40:43],s55 offen offset:0  lds
s_add_u32 m0,s57,57216
s_nop 0
buffer_load_dwordx4 v18,s[40:43],s55 offen offset:128  lds
s_add_u32 m0,s57,58240
s_nop 0
buffer_load_dwordx4 v19,s[40:43],s55 offen offset:128  lds
v_add_u32 v26,65536,v20
v_add_u32 v27,65536,v21
T a[64:65],26,0
T a[66:67],26,512
T a[68:69],27,0
T a[70:71],27,512
T a[72:73],26,4096
T a[74:75],26,4608
T a[76:77],27,4096
T a[78:79],27,4608
s_waitcnt vmcnt(20) lgkmcnt(0)
s_barrier
s_add_u32 s55,s50,1441792
s_add_u32 m0,s57,65536
s_nop 0
buffer_load_dwordx4 v18,s[40:43],s55 offen offset:0  lds
s_add_u32 m0,s57,66560
s_nop 0
buffer_load_dwordx4 v19,s[40:43],s55 offen offset:0  lds
s_add_u32 m0,s57,73600
s_nop 0
buffer_load_dwordx4 v18,s[40:43],s55 offen offset:128  lds
s_add_u32 m0,s57,74624
s_nop 0
buffer_load_dwordx4 v19,s[40:43],s55 offen offset:128  lds
v_add_u32 v26,81920,v20
v_add_u32 v27,81920,v21
T a[80:81],26,0
T a[82:83],26,512
T a[84:85],27,0
T a[86:87],27,512
T a[88:89],26,4096
T a[90:91],26,4608
T a[92:93],27,4096
T a[94:95],27,4608
s_waitcnt vmcnt(20) lgkmcnt(0)
s_barrier
s_add_u32 s55,s50,1572864
s_add_u32 m0,s57,81920
s_nop 0
buffer_load_dwordx4 v18,s[40:43],s55 offen offset:0  lds
s_add_u32 m0,s57,82944
s_nop 0
buffer_load_dwordx4 v19,s[40:43],s55 offen offset:0  lds
s_add_u32 m0,s57,89984
s_nop 0
buffer_load_dwordx4 v18,s[40:43],s55 offen offset:128  lds
s_add_u32 m0,s57,91008
s_nop 0
buffer_load_dwordx4 v19,s[40:43],s55 offen offset:128  lds
v_add_u32 v26,98304,v20
v_add_u32 v27,98304,v21
T a[96:97],26,0
T a[98:99],26,512
T a[100:101],27,0
T a[102:103],27,512
T a[104:105],26,4096
T a[106:107],26,4608
T a[108:109],27,4096
T a[110:111],27,4608
s_waitcnt vmcnt(20) lgkmcnt(0)
s_barrier
s_add_u32 s55,s50,1703936
s_add_u32 m0,s57,98304
s_nop 0
buffer_load_dwordx4 v18,s[40:43],s55 offen offset:0  lds
s_add_u32 m0,s57,99328
s_nop 0
buffer_load_dwordx4 v19,s[40:43],s55 offen offset:0  lds
s_add_u32 m0,s57,106368
s_nop 0
buffer_load_dwordx4 v18,s[40:43],s55 offen offset:128  lds
s_add_u32 m0,s57,107392
s_nop 0
buffer_load_dwordx4 v19,s[40:43],s55 offen offset:128  lds
v_add_u32 v26,0,v20
v_add_u32 v27,0,v21
T a[112:113],26,0
T a[114:115],26,512
T a[116:117],27,0
T a[118:119],27,512
T a[120:121],26,4096
T a[122:123],26,4608
T a[124:125],27,4096
T a[126:127],27,4608
s_waitcnt vmcnt(20) lgkmcnt(0)
s_barrier
s_add_u32 s55,s50,1835008
s_add_u32 m0,s57,0
s_nop 0
buffer_load_dwordx4 v18,s[40:43],s55 offen offset:0  lds
s_add_u32 m0,s57,1024
s_nop 0
buffer_load_dwordx4 v19,s[40:43],s55 offen offset:0  lds
s_add_u32 m0,s57,8064
s_nop 0
buffer_load_dwordx4 v18,s[40:43],s55 offen offset:128  lds
s_add_u32 m0,s57,9088
s_nop 0
buffer_load_dwordx4 v19,s[40:43],s55 offen offset:128  lds
v_add_u32 v26,16384,v20
v_add_u32 v27,16384,v21
T a[128:129],26,0
T a[130:131],26,512
T a[132:133],27,0
T a[134:135],27,512
T a[136:137],26,4096
T a[138:139],26,4608
T a[140:141],27,4096
T a[142:143],27,4608
s_waitcnt vmcnt(20) lgkmcnt(0)
s_barrier
s_add_u32 s55,s50,1966080
s_add_u32 m0,s57,16384
s_nop 0
buffer_load_dwordx4 v18,s[40:43],s55 offen offset:0  lds
s_add_u32 m0,s57,17408
s_nop 0
buffer_load_dwordx4 v19,s[40:43],s55 offen offset:0  lds
s_add_u32 m0,s57,24448
s_nop 0
buffer_load_dwordx4 v18,s[40:43],s55 offen offset:128  lds
s_add_u32 m0,s57,25472
s_nop 0
buffer_load_dwordx4 v19,s[40:43],s55 offen offset:128  lds
v_add_u32 v26,32768,v20
v_add_u32 v27,32768,v21
T a[144:145],26,0
T a[146:147],26,512
T a[148:149],27,0
T a[150:151],27,512
T a[152:153],26,4096
T a[154:155],26,4608
T a[156:157],27,4096
T a[158:159],27,4608
s_waitcnt vmcnt(20) lgkmcnt(0)
s_barrier
v_add_u32 v26,49152,v20
v_add_u32 v27,49152,v21
T a[160:161],26,0
T a[162:163],26,512
T a[164:165],27,0
T a[166:167],27,512
T a[168:169],26,4096
T a[170:171],26,4608
T a[172:173],27,4096
T a[174:175],27,4608
s_waitcnt vmcnt(16) lgkmcnt(0)
s_barrier
v_add_u32 v26,65536,v20
v_add_u32 v27,65536,v21
T a[176:177],26,0
T a[178:179],26,512
T a[180:181],27,0
T a[182:183],27,512
T a[184:185],26,4096
T a[186:187],26,4608
T a[188:189],27,4096
T a[190:191],27,4608
s_waitcnt vmcnt(12) lgkmcnt(0)
s_barrier
v_add_u32 v26,81920,v20
v_add_u32 v27,81920,v21
T a[192:193],26,0
T a[194:195],26,512
T a[196:197],27,0
T a[198:199],27,512
T a[200:201],26,4096
T a[202:203],26,4608
T a[204:205],27,4096
T a[206:207],27,4608
s_waitcnt vmcnt(8) lgkmcnt(0)
s_barrier
v_add_u32 v26,98304,v20
v_add_u32 v27,98304,v21
T a[208:209],26,0
T a[210:211],26,512
T a[212:213],27,0
T a[214:215],27,512
T a[216:217],26,4096
T a[218:219],26,4608
T a[220:221],27,4096
T a[222:223],27,4608
s_waitcnt vmcnt(4) lgkmcnt(0)
s_barrier
v_add_u32 v26,0,v20
v_add_u32 v27,0,v21
T a[224:225],26,0
T a[226:227],26,512
T a[228:229],27,0
T a[230:231],27,512
T a[232:233],26,4096
T a[234:235],26,4608
T a[236:237],27,4096
T a[238:239],27,4608
s_waitcnt vmcnt(0) lgkmcnt(0)
s_barrier
v_add_u32 v26,16384,v20
v_add_u32 v27,16384,v21
T a[240:241],26,0
T a[242:243],26,512
T a[244:245],27,0
T a[246:247],27,512
T a[248:249],26,4096
T a[250:251],26,4608
T a[252:253],27,4096
T a[254:255],27,4608
L 0
s_barrier
v_mov_b32 v34,0x3f803f80
v_mov_b32 v35,0x3f803f80
v_mov_b32 v36,0x3f803f80
v_mov_b32 v37,0x3f803f80
s_nop 1
v_mfma_f32_16x16x32_bf16 v[106:109],a[0:3],v[34:37],0
v_mfma_f32_16x16x32_bf16 v[110:113],a[4:7],v[34:37],0
M 106,a[8:11],34
M 110,a[12:15],34
M 106,a[16:19],34
M 110,a[20:23],34
M 106,a[24:27],34
M 110,a[28:31],34
M 106,a[32:35],34
M 110,a[36:39],34
M 106,a[40:43],34
M 110,a[44:47],34
M 106,a[48:51],34
M 110,a[52:55],34
M 106,a[56:59],34
M 110,a[60:63],34
M 106,a[64:67],34
M 110,a[68:71],34
M 106,a[72:75],34
M 110,a[76:79],34
M 106,a[80:83],34
M 110,a[84:87],34
M 106,a[88:91],34
M 110,a[92:95],34
M 106,a[96:99],34
M 110,a[100:103],34
M 106,a[104:107],34
M 110,a[108:111],34
M 106,a[112:115],34
M 110,a[116:119],34
M 106,a[120:123],34
M 110,a[124:127],34
M 106,a[128:131],34
M 110,a[132:135],34
M 106,a[136:139],34
M 110,a[140:143],34
M 106,a[144:147],34
M 110,a[148:151],34
M 106,a[152:155],34
M 110,a[156:159],34
M 106,a[160:163],34
M 110,a[164:167],34
M 106,a[168:171],34
M 110,a[172:175],34
M 106,a[176:179],34
M 110,a[180:183],34
M 106,a[184:187],34
M 110,a[188:191],34
M 106,a[192:195],34
M 110,a[196:199],34
M 106,a[200:203],34
M 110,a[204:207],34
M 106,a[208:211],34
M 110,a[212:215],34
M 106,a[216:219],34
M 110,a[220:223],34
M 106,a[224:227],34
M 110,a[228:231],34
M 106,a[232:235],34
M 110,a[236:239],34
M 106,a[240:243],34
M 110,a[244:247],34
M 106,a[248:251],34
M 110,a[252:255],34
v_mov_b32 v4,s53
s_mov_b32 s82,0
s_mov_b32 s80,0
s_mov_b32 s54,s45
s_mul_i32 s57,s44,4096
s_mul_i32 s55,s45,32768
s_add_u32 s55,s55,s57
s_add_u32 s55,s55,8388608
s_mul_i32 s57,s44,4096
s_mul_i32 s60,s46,32768
s_mul_i32 s61,s46,1024
s_mul_i32 s62,s46,40960
s_mul_i32 s63,s46,1024
s_mul_i32 s64,s46,512
s_lshl_b32 s59,s44,8
s_add_u32 s59,s59,131072
s_mul_i32 s58,s45,1024
s_add_u32 s58,s58,109056000
s_mul_i32 s65,s45,40960
s_add_u32 s65,s65,s47
s_mul_i32 s68,s45,1024
s_add_u32 s68,s68,s48
s_mul_i32 s73,s45,512
s_add_u32 s73,s73,106954752
s_mul_i32 s74,s45,32768
s_add_u32 s74,s74,s49
s_mul_i32 s75,s45,1024
s_add_u32 s75,s75,107479040
s_mov_b32 s66,0x7fff0000
s_mov_b32 s67,0x7fff0000
s_mov_b32 s69,0x7fff0000
s_mov_b32 s76,0x7fff0000
s_mov_b32 s77,0x7fff0000
s_mov_b32 s70,0x7fff0000
s_mov_b32 s71,0x7fff0000
s_mov_b32 s72,0x7fff0000
v_mov_b32 v1,v0
v_add_u32 v2,16384,v0
s_mov_b32 s84,16384
s_mov_b32 s86,0x3a800000
s_mov_b32 s87,0x3727c5ac
s_mov_b32 s88,0xc0135761
s_mov_b32 s89,0x3d372713
s_lshr_b32 s85,s44,1
s_mul_i32 s85,s85,127
v_lshlrev_b32 v8,22,v0
v_and_b32 v8,0x80000000,v8
v_or_b32 v8,v8,v5
v_add_u32 v2,32768,v0
s_lshl_b32 s84,s57,1
s_add_u32 s84,s84,32768
s_getpc_b64 s[40:41]
Lg1_pc_%=:
s_lshl_b32 s78,s44,13
v_lshlrev_b32 v114,3,v0
v_add_u32 v114,s78,v114
v_min_u32 v114,Lcode_g2_b-Lg1_pc_%=-4,v114
)"
R"(s_mov_b32 m0,163584
s_nop 0
global_load_lds_dword v114,s[40:41]
s_add_u32 m0,s59,4096
s_nop 0
buffer_load_dword v8,s[36:39],s58 offen offset:0 sc1 lds
buffer_store_dword v4,v3,s[36:39],s71 offen offset:0 sc0 sc1
s_add_u32 m0,s59,5120
s_nop 0
buffer_load_dword v8,s[36:39],s58 offen offset:0 sc1 lds
buffer_store_dword v4,v3,s[36:39],s71 offen offset:0 sc0 sc1
s_mov_b32 s83,0
Lg1_poll_%=:
s_add_u32 s78,s83,2
s_and_b32 s78,s78,3
s_lshl_b32 s78,s78,10
s_add_u32 s78,s78,4096
s_add_u32 m0,s59,s78
s_nop 0
buffer_load_dword v8,s[36:39],s58 offen offset:0 sc1 lds
s_and_b32 s78,s83,3
s_lshl_b32 s78,s78,10
v_add_u32 v114,s78,v6
s_waitcnt vmcnt(4)
s_barrier
ds_read_b32 v7,v114 offset:4096
buffer_store_dword v4,v3,s[36:39],s70 offen offset:0 sc0 sc1
s_mov_b32 s70,s71
s_mov_b32 s71,s72
s_mov_b32 s72,0x7fff0000
s_add_u32 s83,s83,1
s_add_u32 s82,s82,1
L 0
v_cmp_eq_u32 vcc,s53,v7
s_nop 0
s_cmp_eq_u32 vcc_lo,-1
s_cbranch_scc1 Lg1_go_%=
s_cmp_lt_u32 s82,20000
s_cbranch_scc1 Lg1_poll_%=
Lg1_go_%=:
s_add_u32 m0,s57,0
s_add_u32 s56,s55,0
D 0,56,0
D 0,56,1024
D 0,56,2048
D 0,56,3072
s_add_u32 s58,s58,s61
s_add_u32 m0,s59,2048
s_nop 0
buffer_load_dword v8,s[36:39],s58 offen offset:0 sc1 lds
s_add_u32 m0,s57,16384
s_add_u32 s56,s55,16384
D 0,56,0
D 0,56,1024
D 0,56,2048
D 0,56,3072
s_add_u32 s55,s55,s60
s_sub_u32 s57,s84,s57
s_add_u32 s54,s54,s46
s_waitcnt vmcnt(4)
s_barrier
buffer_store_dword v4,v3,s[36:39],s70 offen offset:0 sc0 sc1
s_min_u32 s71,s71,s72
buffer_store_dword v4,v3,s[36:39],s71 offen offset:0 sc0 sc1
s_mov_b32 s70,0x7fff0000
s_mov_b32 s71,0x7fff0000
s_mov_b32 s72,0x7fff0000
s_cmp_eq_u32 s80,1
buffer_store_dword v4,v3,s[36:39],s71 offen offset:0 sc0 sc1
s_cbranch_scc1 Lg1_st1_%=
s_branch Lg1_st0_%=
Lg1_gox_%=:
s_add_u32 m0,s59,2048
s_nop 0
buffer_load_dword v8,s[36:39],s58 offen offset:0 sc1 lds
s_waitcnt vmcnt(0)
s_barrier
buffer_store_dword v4,v3,s[36:39],s70 offen offset:0 sc0 sc1
s_min_u32 s71,s71,s72
buffer_store_dword v4,v3,s[36:39],s71 offen offset:0 sc0 sc1
s_mov_b32 s70,0x7fff0000
s_mov_b32 s71,0x7fff0000
s_mov_b32 s72,0x7fff0000
s_cmp_eq_u32 s80,1
s_cbranch_scc1 Lg1_st1_%=
Lg1_st0_%=:
ds_read_b32 v7,v6 offset:2048
R 34,1,0
R 38,1,1024
R 42,1,2048
R 46,1,3072
R 50,1,4096
L 5
v_cmp_eq_u32 vcc,s53,v7
L 3
s_cmp_eq_u32 vcc_lo,-1
s_cbranch_scc1 Lg1_F0_%=
s_branch Lg1_L0_%=
Lg1_st1_%=:
ds_read_b32 v7,v6 offset:2048
R 34,2,0
R 38,2,1024
R 42,2,2048
R 46,2,3072
R 50,2,4096
L 5
v_cmp_eq_u32 vcc,s53,v7
L 3
s_cmp_eq_u32 vcc_lo,-1
s_cbranch_scc1 Lg1_F1_%=
s_branch Lg1_L1_%=
Lg1_F0_%=:
v_mfma_f32_16x16x32_bf16 v[22:25],a[4:7],v[34:37],0
s_add_u32 s58,s58,s61
s_add_u32 m0,s59,0
R 54,1,5120
M 22,a[12:15],38
buffer_load_dword v8,s[36:39],s58 offen offset:0 sc1 lds
v_mfma_f32_16x16x32_bf16 v[18:21],a[0:3],v[34:37],0
s_add_u32 m0,s57,0
R 58,1,6144
v_fma_f32 v26,-v102,v106,v26
M 18,a[8:11],38
L 3
s_add_u32 s56,s55,0
v_fma_f32 v27,-v102,v107,v27
M 18,a[16:19],42
s_mov_b32 s66,s65
R 62,1,7168
v_fma_f32 v28,-v102,v108,v28
M 18,a[24:27],46
D 0,56,0
M 22,a[20:23],42
s_add_u32 s65,s65,s62
R 66,1,8192
v_fma_f32 v29,-v102,v109,v29
v_mul_f32 v114,v26,v26
M 22,a[28:31],46
L 3
D 0,56,1024
M 22,a[36:39],50
s_add_u32 s54,s54,s46
R 70,1,9216
v_mul_f32 v115,v27,v27
M 22,a[44:47],54
v_mul_f32 v116,v28,v28
v_mul_f32 v117,v29,v29
M 18,a[32:35],50
R 74,1,10240
v_fma_f32 v114,v104,v114,v103
M 18,a[40:43],54
L 3
D 0,56,2048
M 18,a[48:51],58
R 78,1,11264
v_fma_f32 v115,v104,v115,v103
M 18,a[56:59],62
D 0,56,3072
v_fma_f32 v116,v104,v116,v103
M 22,a[52:55],58
s_add_u32 m0,s57,16384
R 82,1,12288
v_fma_f32 v117,v104,v117,v103
M 22,a[60:63],62
L 3
v_mul_f32 v114,v26,v114
v_mul_f32 v115,v27,v115
M 22,a[68:71],66
s_add_u32 s56,s55,16384
R 86,1,13312
v_mul_f32 v116,v28,v116
M 22,a[76:79],70
buffer_load_dwordx4 v[98:101],v11,s[36:39],s73 offen offset:0 sc1
M 18,a[64:67],66
s_add_u32 s73,s73,s64
R 90,1,14336
v_mul_f32 v117,v29,v117
M 18,a[72:75],70
L 3
v_exp_f32 v114,v114
v_fma_f32 v30,-v102,v110,v30
M 18,a[80:83],74
s_waitcnt vmcnt(9)
R 94,1,15360
v_fma_f32 v114,v114,v105,v105
M 18,a[88:91],78
s_barrier
v_exp_f32 v115,v115
M 22,a[84:87],74
s_add_u32 s55,s55,s60
R 34,1,16384
v_fma_f32 v115,v115,v105,v105
M 22,a[92:95],78
L 3
v_exp_f32 v116,v116
M 22,a[100:103],82
s_sub_u32 s57,s84,s57
R 38,1,17408
v_fma_f32 v116,v116,v105,v105
M 22,a[108:111],86
D 0,56,0
M 18,a[96:99],82
R 42,1,18432
v_exp_f32 v117,v117
M 18,a[104:107],86
L 3
v_rcp_f32 v114,v114
M 18,a[112:115],90
R 46,1,19456
v_fma_f32 v117,v117,v105,v105
M 18,a[120:123],94
v_rcp_f32 v115,v115
M 22,a[116:119],90
R 50,1,20480
v_mul_f32 v26,v26,v114
M 22,a[124:127],94
L 3
v_rcp_f32 v116,v116
M 22,a[132:135],34
R 54,1,21504
v_mul_f32 v27,v27,v115
M 22,a[140:143],38
D 0,56,1024
v_mul_f32 v28,v28,v116
M 18,a[128:131],34
R 58,1,22528
v_cvt_pk_bf16_f32 v122,v26,v27
M 18,a[136:139],38
L 3
v_rcp_f32 v117,v117
M 18,a[144:147],42
R 62,1,23552
v_mul_f32 v29,v29,v117
M 18,a[152:155],46
v_cvt_pk_bf16_f32 v123,v28,v29
v_fma_f32 v31,-v102,v111,v31
M 22,a[148:151],42
R 66,1,24576
v_fma_f32 v32,-v102,v112,v32
M 22,a[156:159],46
L 3
D 0,56,2048
v_fma_f32 v33,-v102,v113,v33
M 22,a[164:167],50
R 70,1,25600
v_mul_f32 v118,v30,v30
M 22,a[172:175],54
v_mul_f32 v119,v31,v31
v_mul_f32 v120,v32,v32
M 18,a[160:163],50
R 74,1,26624
v_mul_f32 v121,v33,v33
M 18,a[168:171],54
L 3
v_fma_f32 v118,v104,v118,v103
v_fma_f32 v119,v104,v119,v103
M 18,a[176:179],58
R 78,1,27648
v_fma_f32 v120,v104,v120,v103
M 18,a[184:187],62
v_fma_f32 v121,v104,v121,v103
v_mul_f32 v118,v30,v118
M 22,a[180:183],58
R 82,1,28672
v_exp_f32 v118,v118
M 22,a[188:191],62
L 3
D 0,56,3072
M 22,a[196:199],66
R 86,1,29696
v_mul_f32 v119,v31,v119
M 22,a[204:207],70
v_exp_f32 v119,v119
M 18,a[192:195],66
R 90,1,30720
v_mul_f32 v120,v32,v120
M 18,a[200:203],70
L 3
v_mul_f32 v121,v33,v121
v_fma_f32 v118,v118,v105,v105
M 18,a[208:211],74
s_waitcnt vmcnt(5)
R 94,1,31744
v_fma_f32 v119,v119,v105,v105
M 18,a[216:219],78
s_barrier
ds_read_b32 v7,v6 offset:0
v_exp_f32 v120,v120
M 22,a[212:215],74
R 34,2,0
v_fma_f32 v120,v120,v105,v105
M 22,a[220:223],78
L 4
buffer_store_dword v4,v3,s[36:39],s70 offen offset:0 sc0 sc1
v_exp_f32 v121,v121
M 22,a[228:231],82
s_mov_b32 s70,s69
R 38,2,1024
v_fma_f32 v121,v121,v105,v105
v_rcp_f32 v118,v118
v_rcp_f32 v119,v119
v_rcp_f32 v120,v120
v_rcp_f32 v121,v121
M 22,a[236:239],86
s_mov_b32 s69,s68
v_mul_f32 v30,v30,v118
v_mul_f32 v31,v31,v119
v_mul_f32 v32,v32,v120
v_mul_f32 v33,v33,v121
v_cvt_pk_bf16_f32 v124,v30,v31
v_cvt_pk_bf16_f32 v125,v32,v33
M 18,a[224:227],82
s_add_u32 s68,s68,s63
R 42,2,2048
buffer_store_dwordx4 v[122:125],v15,s[36:39],s67 offen offset:0 sc0 sc1
M 18,a[232:235],86
L 4
L 3
M 18,a[240:243],90
R 46,2,3072
v_cmp_eq_u32 vcc,s53,v7
M 18,a[248:251],94
buffer_store_dwordx4 v[122:125],v16,s[36:39],s67 offen offset:0 sc0 sc1
M 22,a[244:247],90
s_waitcnt vmcnt(7) lgkmcnt(2)
s_cmp_eq_u32 vcc_lo,-1
R 50,2,4096
M 22,a[252:255],94
s_cbranch_scc0 Lg1_K1_%=
Lg1_F1_%=:
v_mfma_f32_16x16x32_bf16 v[30:33],a[4:7],v[34:37],0
s_add_u32 s58,s58,s61
s_add_u32 m0,s59,1024
R 54,2,5120
M 30,a[12:15],38
buffer_load_dword v8,s[36:39],s58 offen offset:0 sc1 lds
v_mfma_f32_16x16x32_bf16 v[26:29],a[0:3],v[34:37],0
s_add_u32 m0,s57,0
R 58,2,6144
v_fma_f32 v18,-v98,v106,v18
M 26,a[8:11],38
L 3
s_add_u32 s56,s55,0
v_fma_f32 v19,-v98,v107,v19
M 26,a[16:19],42
s_mov_b32 s67,s65
R 62,2,7168
v_fma_f32 v20,-v98,v108,v20
M 26,a[24:27],46
D 0,56,0
M 30,a[20:23],42
s_add_u32 s65,s65,s62
R 66,2,8192
v_fma_f32 v21,-v98,v109,v21
v_mul_f32 v114,v18,v18
M 30,a[28:31],46
L 3
D 0,56,1024
M 30,a[36:39],50
s_add_u32 s54,s54,s46
R 70,2,9216
v_mul_f32 v115,v19,v19
M 30,a[44:47],54
v_mul_f32 v116,v20,v20
v_mul_f32 v117,v21,v21
M 26,a[32:35],50
R 74,2,10240
v_fma_f32 v114,v100,v114,v99
M 26,a[40:43],54
L 3
D 0,56,2048
M 26,a[48:51],58
R 78,2,11264
v_fma_f32 v115,v100,v115,v99
M 26,a[56:59],62
D 0,56,3072
v_fma_f32 v116,v100,v116,v99
M 30,a[52:55],58
s_add_u32 m0,s57,16384
R 82,2,12288
v_fma_f32 v117,v100,v117,v99
M 30,a[60:63],62
L 3
v_mul_f32 v114,v18,v114
v_mul_f32 v115,v19,v115
M 30,a[68:71],66
s_add_u32 s56,s55,16384
R 86,2,13312
v_mul_f32 v116,v20,v116
M 30,a[76:79],70
buffer_load_dwordx4 v[102:105],v11,s[36:39],s73 offen offset:0 sc1
M 26,a[64:67],66
s_add_u32 s73,s73,s64
R 90,2,14336
v_mul_f32 v117,v21,v117
M 26,a[72:75],70
L 3
v_exp_f32 v114,v114
v_fma_f32 v22,-v98,v110,v22
M 26,a[80:83],74
s_waitcnt vmcnt(9)
R 94,2,15360
v_fma_f32 v114,v114,v101,v101
M 26,a[88:91],78
s_barrier
v_exp_f32 v115,v115
M 30,a[84:87],74
s_add_u32 s55,s55,s60
R 34,2,16384
v_fma_f32 v115,v115,v101,v101
M 30,a[92:95],78
L 3
v_exp_f32 v116,v116
M 30,a[100:103],82
s_sub_u32 s57,s84,s57
R 38,2,17408
v_fma_f32 v116,v116,v101,v101
M 30,a[108:111],86
D 0,56,0
M 26,a[96:99],82
R 42,2,18432
v_exp_f32 v117,v117
M 26,a[104:107],86
L 3
v_rcp_f32 v114,v114
M 26,a[112:115],90
R 46,2,19456
v_fma_f32 v117,v117,v101,v101
M 26,a[120:123],94
v_rcp_f32 v115,v115
M 30,a[116:119],90
R 50,2,20480
v_mul_f32 v18,v18,v114
M 30,a[124:127],94
L 3
v_rcp_f32 v116,v116
M 30,a[132:135],34
R 54,2,21504
v_mul_f32 v19,v19,v115
M 30,a[140:143],38
D 0,56,1024
v_mul_f32 v20,v20,v116
M 26,a[128:131],34
R 58,2,22528
v_cvt_pk_bf16_f32 v122,v18,v19
M 26,a[136:139],38
L 3
v_rcp_f32 v117,v117
M 26,a[144:147],42
R 62,2,23552
v_mul_f32 v21,v21,v117
M 26,a[152:155],46
v_cvt_pk_bf16_f32 v123,v20,v21
v_fma_f32 v23,-v98,v111,v23
M 30,a[148:151],42
R 66,2,24576
v_fma_f32 v24,-v98,v112,v24
M 30,a[156:159],46
L 3
D 0,56,2048
v_fma_f32 v25,-v98,v113,v25
M 30,a[164:167],50
R 70,2,25600
v_mul_f32 v118,v22,v22
M 30,a[172:175],54
v_mul_f32 v119,v23,v23
v_mul_f32 v120,v24,v24
M 26,a[160:163],50
R 74,2,26624
v_mul_f32 v121,v25,v25
M 26,a[168:171],54
L 3
v_fma_f32 v118,v100,v118,v99
v_fma_f32 v119,v100,v119,v99
M 26,a[176:179],58
R 78,2,27648
v_fma_f32 v120,v100,v120,v99
M 26,a[184:187],62
v_fma_f32 v121,v100,v121,v99
v_mul_f32 v118,v22,v118
M 30,a[180:183],58
R 82,2,28672
v_exp_f32 v118,v118
M 30,a[188:191],62
L 3
D 0,56,3072
M 30,a[196:199],66
R 86,2,29696
v_mul_f32 v119,v23,v119
M 30,a[204:207],70
v_exp_f32 v119,v119
M 26,a[192:195],66
R 90,2,30720
v_mul_f32 v120,v24,v120
M 26,a[200:203],70
L 3
v_mul_f32 v121,v25,v121
v_fma_f32 v118,v118,v101,v101
M 26,a[208:211],74
s_waitcnt vmcnt(5)
)"
R"(R 94,2,31744
v_fma_f32 v119,v119,v101,v101
M 26,a[216:219],78
s_barrier
ds_read_b32 v7,v6 offset:1024
v_exp_f32 v120,v120
M 30,a[212:215],74
R 34,1,0
v_fma_f32 v120,v120,v101,v101
M 30,a[220:223],78
L 4
buffer_store_dword v4,v3,s[36:39],s70 offen offset:0 sc0 sc1
v_exp_f32 v121,v121
M 30,a[228:231],82
s_mov_b32 s70,s69
R 38,1,1024
v_fma_f32 v121,v121,v101,v101
v_rcp_f32 v118,v118
v_rcp_f32 v119,v119
v_rcp_f32 v120,v120
v_rcp_f32 v121,v121
M 30,a[236:239],86
s_mov_b32 s69,s68
v_mul_f32 v22,v22,v118
v_mul_f32 v23,v23,v119
v_mul_f32 v24,v24,v120
v_mul_f32 v25,v25,v121
v_cvt_pk_bf16_f32 v124,v22,v23
v_cvt_pk_bf16_f32 v125,v24,v25
M 26,a[224:227],82
s_add_u32 s68,s68,s63
R 42,1,2048
buffer_store_dwordx4 v[122:125],v15,s[36:39],s66 offen offset:0 sc0 sc1
M 26,a[232:235],86
L 4
L 3
M 26,a[240:243],90
R 46,1,3072
v_cmp_eq_u32 vcc,s53,v7
M 26,a[248:251],94
buffer_store_dwordx4 v[122:125],v16,s[36:39],s66 offen offset:0 sc0 sc1
M 30,a[244:247],90
s_waitcnt vmcnt(7) lgkmcnt(2)
s_cmp_eq_u32 vcc_lo,-1
R 50,1,4096
M 30,a[252:255],94
s_cbranch_scc1 Lg1_F0_%=
Lg1_K0_%=:
Lg1_L0_%=:
v_mfma_f32_16x16x32_bf16 v[22:25],a[4:7],v[34:37],0
s_add_u32 m0,s59,3072
s_mov_b32 s66,s65
R 54,1,5120
M 22,a[12:15],38
buffer_load_dword v8,s[36:39],s58 offen offset:0 sc1 lds
v_mfma_f32_16x16x32_bf16 v[18:21],a[0:3],v[34:37],0
s_add_u32 s65,s65,s62
R 58,1,6144
v_fma_f32 v26,-v102,v106,v26
M 18,a[8:11],38
L 3
v_fma_f32 v27,-v102,v107,v27
v_fma_f32 v28,-v102,v108,v28
M 18,a[16:19],42
R 62,1,7168
v_fma_f32 v29,-v102,v109,v29
M 18,a[24:27],46
v_mul_f32 v114,v26,v26
v_mul_f32 v115,v27,v27
M 22,a[20:23],42
R 66,1,8192
v_mul_f32 v116,v28,v28
M 22,a[28:31],46
L 3
v_mul_f32 v117,v29,v29
v_fma_f32 v114,v104,v114,v103
M 22,a[36:39],50
R 70,1,9216
v_fma_f32 v115,v104,v115,v103
M 22,a[44:47],54
v_fma_f32 v116,v104,v116,v103
v_fma_f32 v117,v104,v117,v103
M 18,a[32:35],50
R 74,1,10240
v_mul_f32 v114,v26,v114
M 18,a[40:43],54
L 3
v_exp_f32 v114,v114
M 18,a[48:51],58
R 78,1,11264
v_mul_f32 v115,v27,v115
M 18,a[56:59],62
v_exp_f32 v115,v115
M 22,a[52:55],58
R 82,1,12288
v_mul_f32 v116,v28,v116
M 22,a[60:63],62
L 3
v_exp_f32 v116,v116
M 22,a[68:71],66
R 86,1,13312
v_mul_f32 v117,v29,v117
M 22,a[76:79],70
buffer_load_dwordx4 v[98:101],v11,s[36:39],s73 offen offset:0 sc1
M 18,a[64:67],66
s_add_u32 s73,s73,s64
R 90,1,14336
v_fma_f32 v114,v114,v105,v105
M 18,a[72:75],70
L 3
v_exp_f32 v117,v117
M 18,a[80:83],74
s_waitcnt vmcnt(1)
R 94,1,15360
v_fma_f32 v115,v115,v105,v105
M 18,a[88:91],78
s_barrier
ds_read_b32 v7,v6 offset:3072
v_fma_f32 v116,v116,v105,v105
M 22,a[84:87],74
R 34,1,16384
v_fma_f32 v117,v117,v105,v105
M 22,a[92:95],78
L 4
buffer_store_dword v4,v3,s[36:39],s70 offen offset:0 sc0 sc1
M 22,a[100:103],82
s_mov_b32 s70,0x7fff0000
R 38,1,17408
v_fma_f32 v30,-v102,v110,v30
M 22,a[108:111],86
v_rcp_f32 v114,v114
M 18,a[96:99],82
R 42,1,18432
v_mul_f32 v26,v26,v114
M 18,a[104:107],86
L 4
L 3
v_cmp_eq_u32 vcc,s53,v7
M 18,a[112:115],90
R 46,1,19456
v_fma_f32 v31,-v102,v111,v31
M 18,a[120:123],94
v_rcp_f32 v115,v115
M 22,a[116:119],90
s_cmp_eq_u32 vcc_lo,-1
R 50,1,20480
v_mul_f32 v27,v27,v115
s_cbranch_scc0 Lg1_X0_%=
M 22,a[124:127],94
L 3
s_add_u32 m0,s57,0
s_add_u32 s56,s55,0
M 22,a[132:135],34
s_add_u32 s58,s58,s61
R 54,1,21504
v_cvt_pk_bf16_f32 v122,v26,v27
M 22,a[140:143],38
D 0,56,0
M 18,a[128:131],34
s_add_u32 s54,s54,s46
R 58,1,22528
v_fma_f32 v32,-v102,v112,v32
M 18,a[136:139],38
L 3
v_rcp_f32 v116,v116
M 18,a[144:147],42
R 62,1,23552
v_mul_f32 v28,v28,v116
M 18,a[152:155],46
D 0,56,1024
M 22,a[148:151],42
R 66,1,24576
v_fma_f32 v33,-v102,v113,v33
M 22,a[156:159],46
L 3
v_rcp_f32 v117,v117
M 22,a[164:167],50
R 70,1,25600
v_mul_f32 v29,v29,v117
M 22,a[172:175],54
D 0,56,2048
M 18,a[160:163],50
R 74,1,26624
v_cvt_pk_bf16_f32 v123,v28,v29
M 18,a[168:171],54
L 3
v_mul_f32 v118,v30,v30
v_mul_f32 v119,v31,v31
M 18,a[176:179],58
R 78,1,27648
v_mul_f32 v120,v32,v32
M 18,a[184:187],62
D 0,56,3072
M 22,a[180:183],58
s_add_u32 m0,s59,0
R 82,1,28672
v_mul_f32 v121,v33,v33
M 22,a[188:191],62
L 3
buffer_load_dword v8,s[36:39],s58 offen offset:0 sc1 lds
M 22,a[196:199],66
s_add_u32 m0,s57,16384
R 86,1,29696
v_fma_f32 v118,v104,v118,v103
M 22,a[204:207],70
s_add_u32 s56,s55,16384
v_fma_f32 v119,v104,v119,v103
v_fma_f32 v120,v104,v120,v103
M 18,a[192:195],66
s_add_u32 s55,s55,s60
R 90,1,30720
v_fma_f32 v121,v104,v121,v103
M 18,a[200:203],70
L 3
D 0,56,0
M 18,a[208:211],74
s_waitcnt vmcnt(1)
R 94,1,31744
v_mul_f32 v118,v30,v118
M 18,a[216:219],78
s_barrier
ds_read_b32 v7,v6 offset:0
v_mul_f32 v119,v31,v119
M 22,a[212:215],74
s_sub_u32 s57,s84,s57
R 34,2,0
v_mul_f32 v120,v32,v120
M 22,a[220:223],78
L 4
buffer_store_dword v4,v3,s[36:39],s70 offen offset:0 sc0 sc1
v_mul_f32 v121,v33,v121
v_exp_f32 v118,v118
v_exp_f32 v119,v119
v_exp_f32 v120,v120
v_exp_f32 v121,v121
M 22,a[228:231],82
s_mov_b32 s70,s69
R 38,2,1024
v_fma_f32 v118,v118,v105,v105
v_fma_f32 v119,v119,v105,v105
v_fma_f32 v120,v120,v105,v105
v_fma_f32 v121,v121,v105,v105
v_rcp_f32 v118,v118
v_rcp_f32 v119,v119
v_rcp_f32 v120,v120
v_rcp_f32 v121,v121
M 22,a[236:239],86
s_mov_b32 s69,s68
v_mul_f32 v30,v30,v118
v_mul_f32 v31,v31,v119
v_mul_f32 v32,v32,v120
v_mul_f32 v33,v33,v121
v_cvt_pk_bf16_f32 v124,v30,v31
v_cvt_pk_bf16_f32 v125,v32,v33
M 18,a[224:227],82
s_add_u32 s68,s68,s63
R 42,2,2048
buffer_store_dwordx4 v[122:125],v15,s[36:39],s67 offen offset:0 sc0 sc1
M 18,a[232:235],86
L 4
L 3
M 18,a[240:243],90
R 46,2,3072
v_cmp_eq_u32 vcc,s53,v7
M 18,a[248:251],94
D 0,56,1024
M 22,a[244:247],90
L 2
s_cmp_eq_u32 vcc_lo,-1
R 50,2,4096
buffer_store_dwordx4 v[122:125],v16,s[36:39],s67 offen offset:0 sc0 sc1
M 22,a[252:255],94
D 0,56,2048
D 0,56,3072
buffer_store_dword v4,v3,s[36:39],s71 offen offset:0 sc0 sc1
buffer_store_dword v4,v3,s[36:39],s71 offen offset:0 sc0 sc1
buffer_store_dword v4,v3,s[36:39],s71 offen offset:0 sc0 sc1
s_cbranch_scc1 Lg1_F1_%=
s_branch Lg1_K1_%=
Lg1_X0_%=:
M 22,a[124:127],94
L 3
s_add_u32 m0,s59,0
v_cvt_pk_bf16_f32 v122,v26,v27
M 22,a[132:135],34
R 54,1,21504
v_fma_f32 v32,-v102,v112,v32
M 22,a[140:143],38
buffer_load_dword v8,s[36:39],s58 offen offset:0 sc1 lds
M 18,a[128:131],34
R 58,1,22528
v_fma_f32 v33,-v102,v113,v33
M 18,a[136:139],38
L 3
v_rcp_f32 v116,v116
M 18,a[144:147],42
R 62,1,23552
v_mul_f32 v28,v28,v116
M 18,a[152:155],46
v_rcp_f32 v117,v117
M 22,a[148:151],42
R 66,1,24576
v_mul_f32 v29,v29,v117
M 22,a[156:159],46
L 3
v_cvt_pk_bf16_f32 v123,v28,v29
v_mul_f32 v118,v30,v30
M 22,a[164:167],50
R 70,1,25600
v_mul_f32 v119,v31,v31
M 22,a[172:175],54
v_mul_f32 v120,v32,v32
v_mul_f32 v121,v33,v33
M 18,a[160:163],50
R 74,1,26624
v_fma_f32 v118,v104,v118,v103
M 18,a[168:171],54
L 3
v_fma_f32 v119,v104,v119,v103
v_fma_f32 v120,v104,v120,v103
M 18,a[176:179],58
R 78,1,27648
v_fma_f32 v121,v104,v121,v103
M 18,a[184:187],62
v_mul_f32 v118,v30,v118
v_mul_f32 v119,v31,v119
M 22,a[180:183],58
R 82,1,28672
v_mul_f32 v120,v32,v120
M 22,a[188:191],62
L 3
v_exp_f32 v118,v118
M 22,a[196:199],66
R 86,1,29696
v_mul_f32 v121,v33,v121
M 22,a[204:207],70
v_exp_f32 v119,v119
M 18,a[192:195],66
R 90,1,30720
v_fma_f32 v118,v118,v105,v105
M 18,a[200:203],70
L 3
v_exp_f32 v120,v120
M 18,a[208:211],74
s_waitcnt vmcnt(0)
R 94,1,31744
v_fma_f32 v119,v119,v105,v105
M 18,a[216:219],78
s_barrier
buffer_store_dword v4,v3,s[36:39],s70 offen offset:0 sc0 sc1
M 22,a[212:215],74
s_mov_b32 s70,s69
ds_read_b32 v7,v6 offset:0
v_fma_f32 v120,v120,v105,v105
M 22,a[220:223],78
L 3
v_exp_f32 v121,v121
M 22,a[228:231],82
s_mov_b32 s69,s68
v_fma_f32 v121,v121,v105,v105
v_rcp_f32 v118,v118
v_rcp_f32 v119,v119
v_rcp_f32 v120,v120
v_rcp_f32 v121,v121
M 22,a[236:239],86
s_add_u32 s68,s68,s63
v_mul_f32 v30,v30,v118
v_mul_f32 v31,v31,v119
v_mul_f32 v32,v32,v120
v_mul_f32 v33,v33,v121
v_cvt_pk_bf16_f32 v124,v30,v31
v_cvt_pk_bf16_f32 v125,v32,v33
M 18,a[224:227],82
buffer_store_dwordx4 v[122:125],v15,s[36:39],s67 offen offset:0 sc0 sc1
M 18,a[232:235],86
L 1
M 18,a[240:243],90
L 0
v_cmp_eq_u32 vcc,s53,v7
M 18,a[248:251],94
buffer_store_dwordx4 v[122:125],v16,s[36:39],s67 offen offset:0 sc0 sc1
M 22,a[244:247],90
s_nop 0
M 22,a[252:255],94
s_mov_b32 s80,1
s_cmp_eq_u32 vcc_lo,-1
s_cbranch_scc0 Lg1_xn0_%=
s_add_u32 m0,s57,0
s_add_u32 s56,s55,0
D 0,56,0
v_fma_f32 v18,-v98,v106,v18
v_fma_f32 v19,-v98,v107,v19
v_fma_f32 v20,-v98,v108,v20
v_fma_f32 v21,-v98,v109,v21
v_mul_f32 v114,v18,v18
v_mul_f32 v115,v19,v19
v_mul_f32 v116,v20,v20
D 0,56,1024
v_mul_f32 v117,v21,v21
v_fma_f32 v114,v100,v114,v99
v_fma_f32 v115,v100,v115,v99
v_fma_f32 v116,v100,v116,v99
v_fma_f32 v117,v100,v117,v99
v_mul_f32 v114,v18,v114
v_mul_f32 v115,v19,v115
D 0,56,2048
v_mul_f32 v116,v20,v116
v_mul_f32 v117,v21,v117
v_exp_f32 v114,v114
v_exp_f32 v115,v115
v_exp_f32 v116,v116
v_exp_f32 v117,v117
v_fma_f32 v114,v114,v101,v101
D 0,56,3072
v_fma_f32 v115,v115,v101,v101
v_fma_f32 v116,v116,v101,v101
v_fma_f32 v117,v117,v101,v101
v_rcp_f32 v114,v114
v_rcp_f32 v115,v115
v_rcp_f32 v116,v116
v_rcp_f32 v117,v117
s_add_u32 s58,s58,s61
s_add_u32 m0,s57,16384
s_add_u32 s56,s55,16384
D 0,56,0
v_mul_f32 v18,v18,v114
v_mul_f32 v19,v19,v115
v_mul_f32 v20,v20,v116
v_mul_f32 v21,v21,v117
v_cvt_pk_bf16_f32 v122,v18,v19
v_cvt_pk_bf16_f32 v123,v20,v21
v_fma_f32 v22,-v98,v110,v22
D 0,56,1024
v_fma_f32 v23,-v98,v111,v23
v_fma_f32 v24,-v98,v112,v24
v_fma_f32 v25,-v98,v113,v25
v_mul_f32 v118,v22,v22
v_mul_f32 v119,v23,v23
v_mul_f32 v120,v24,v24
v_mul_f32 v121,v25,v25
D 0,56,2048
v_fma_f32 v118,v100,v118,v99
v_fma_f32 v119,v100,v119,v99
v_fma_f32 v120,v100,v120,v99
v_fma_f32 v121,v100,v121,v99
v_mul_f32 v118,v22,v118
v_mul_f32 v119,v23,v119
v_mul_f32 v120,v24,v120
D 0,56,3072
v_mul_f32 v121,v25,v121
v_exp_f32 v118,v118
v_exp_f32 v119,v119
v_exp_f32 v120,v120
v_exp_f32 v121,v121
v_fma_f32 v118,v118,v101,v101
v_fma_f32 v119,v119,v101,v101
s_add_u32 s55,s55,s60
s_sub_u32 s57,s84,s57
s_add_u32 s54,s54,s46
v_fma_f32 v120,v120,v101,v101
v_fma_f32 v121,v121,v101,v101
v_rcp_f32 v118,v118
v_rcp_f32 v119,v119
v_rcp_f32 v120,v120
v_rcp_f32 v121,v121
v_mul_f32 v22,v22,v118
v_mul_f32 v23,v23,v119
v_mul_f32 v24,v24,v120
v_mul_f32 v25,v25,v121
v_cvt_pk_bf16_f32 v124,v22,v23
v_cvt_pk_bf16_f32 v125,v24,v25
buffer_store_dwordx4 v[122:125],v15,s[36:39],s66 offen offset:0 sc0 sc1
buffer_store_dwordx4 v[122:125],v16,s[36:39],s66 offen offset:0 sc0 sc1
s_mov_b32 s72,s69
s_mov_b32 s69,0x7fff0000
s_mov_b32 s66,0x7fff0000
s_mov_b32 s76,0x7fff0000
s_branch Lg1_gox_%=
Lg1_xn0_%=:
s_cmp_ge_u32 s54,256
s_cbranch_scc1 Lg1_fin0_%=
s_add_u32 m0,s59,4096
s_nop 0
buffer_load_dword v8,s[36:39],s58 offen offset:0 sc1 lds
buffer_store_dword v4,v3,s[36:39],s71 offen offset:0 sc0 sc1
s_add_u32 m0,s59,5120
s_nop 0
buffer_load_dword v8,s[36:39],s58 offen offset:0 sc1 lds
buffer_store_dword v4,v3,s[36:39],s71 offen offset:0 sc0 sc1
s_mov_b32 s83,0
v_fma_f32 v18,-v98,v106,v18
v_fma_f32 v19,-v98,v107,v19
v_fma_f32 v20,-v98,v108,v20
v_fma_f32 v21,-v98,v109,v21
v_mul_f32 v114,v18,v18
v_mul_f32 v115,v19,v19
v_mul_f32 v116,v20,v20
v_mul_f32 v117,v21,v21
v_fma_f32 v114,v100,v114,v99
v_fma_f32 v115,v100,v115,v99
)"
R"(v_fma_f32 v116,v100,v116,v99
v_fma_f32 v117,v100,v117,v99
v_mul_f32 v114,v18,v114
v_mul_f32 v115,v19,v115
v_mul_f32 v116,v20,v116
v_mul_f32 v117,v21,v117
v_exp_f32 v114,v114
v_exp_f32 v115,v115
v_exp_f32 v116,v116
v_exp_f32 v117,v117
v_fma_f32 v114,v114,v101,v101
v_fma_f32 v115,v115,v101,v101
v_fma_f32 v116,v116,v101,v101
v_fma_f32 v117,v117,v101,v101
v_rcp_f32 v114,v114
v_rcp_f32 v115,v115
v_rcp_f32 v116,v116
v_rcp_f32 v117,v117
v_mul_f32 v18,v18,v114
v_mul_f32 v19,v19,v115
v_mul_f32 v20,v20,v116
v_mul_f32 v21,v21,v117
v_cvt_pk_bf16_f32 v122,v18,v19
v_cvt_pk_bf16_f32 v123,v20,v21
v_fma_f32 v22,-v98,v110,v22
v_fma_f32 v23,-v98,v111,v23
v_fma_f32 v24,-v98,v112,v24
v_fma_f32 v25,-v98,v113,v25
v_mul_f32 v118,v22,v22
v_mul_f32 v119,v23,v23
v_mul_f32 v120,v24,v24
v_mul_f32 v121,v25,v25
v_fma_f32 v118,v100,v118,v99
v_fma_f32 v119,v100,v119,v99
v_fma_f32 v120,v100,v120,v99
v_fma_f32 v121,v100,v121,v99
v_mul_f32 v118,v22,v118
v_mul_f32 v119,v23,v119
v_mul_f32 v120,v24,v120
v_mul_f32 v121,v25,v121
v_exp_f32 v118,v118
v_exp_f32 v119,v119
v_exp_f32 v120,v120
v_exp_f32 v121,v121
v_fma_f32 v118,v118,v101,v101
v_fma_f32 v119,v119,v101,v101
v_fma_f32 v120,v120,v101,v101
v_fma_f32 v121,v121,v101,v101
v_rcp_f32 v118,v118
v_rcp_f32 v119,v119
v_rcp_f32 v120,v120
v_rcp_f32 v121,v121
v_mul_f32 v22,v22,v118
v_mul_f32 v23,v23,v119
v_mul_f32 v24,v24,v120
v_mul_f32 v25,v25,v121
v_cvt_pk_bf16_f32 v124,v22,v23
v_cvt_pk_bf16_f32 v125,v24,v25
buffer_store_dwordx4 v[122:125],v15,s[36:39],s66 offen offset:0 sc0 sc1
buffer_store_dwordx4 v[122:125],v16,s[36:39],s66 offen offset:0 sc0 sc1
s_mov_b32 s72,s69
s_mov_b32 s69,0x7fff0000
s_mov_b32 s66,0x7fff0000
s_mov_b32 s76,0x7fff0000
s_branch Lg1_poll_%=
Lg1_fin0_%=:
v_fma_f32 v18,-v98,v106,v18
v_fma_f32 v19,-v98,v107,v19
v_fma_f32 v20,-v98,v108,v20
v_fma_f32 v21,-v98,v109,v21
v_mul_f32 v114,v18,v18
v_mul_f32 v115,v19,v19
v_mul_f32 v116,v20,v20
v_mul_f32 v117,v21,v21
v_fma_f32 v114,v100,v114,v99
v_fma_f32 v115,v100,v115,v99
v_fma_f32 v116,v100,v116,v99
v_fma_f32 v117,v100,v117,v99
v_mul_f32 v114,v18,v114
v_mul_f32 v115,v19,v115
v_mul_f32 v116,v20,v116
v_mul_f32 v117,v21,v117
v_exp_f32 v114,v114
v_exp_f32 v115,v115
v_exp_f32 v116,v116
v_exp_f32 v117,v117
v_fma_f32 v114,v114,v101,v101
v_fma_f32 v115,v115,v101,v101
v_fma_f32 v116,v116,v101,v101
v_fma_f32 v117,v117,v101,v101
v_rcp_f32 v114,v114
v_rcp_f32 v115,v115
v_rcp_f32 v116,v116
v_rcp_f32 v117,v117
v_mul_f32 v18,v18,v114
v_mul_f32 v19,v19,v115
v_mul_f32 v20,v20,v116
v_mul_f32 v21,v21,v117
v_cvt_pk_bf16_f32 v122,v18,v19
v_cvt_pk_bf16_f32 v123,v20,v21
v_fma_f32 v22,-v98,v110,v22
v_fma_f32 v23,-v98,v111,v23
v_fma_f32 v24,-v98,v112,v24
v_fma_f32 v25,-v98,v113,v25
v_mul_f32 v118,v22,v22
v_mul_f32 v119,v23,v23
v_mul_f32 v120,v24,v24
v_mul_f32 v121,v25,v25
v_fma_f32 v118,v100,v118,v99
v_fma_f32 v119,v100,v119,v99
v_fma_f32 v120,v100,v120,v99
v_fma_f32 v121,v100,v121,v99
v_mul_f32 v118,v22,v118
v_mul_f32 v119,v23,v119
v_mul_f32 v120,v24,v120
v_mul_f32 v121,v25,v121
v_exp_f32 v118,v118
v_exp_f32 v119,v119
v_exp_f32 v120,v120
v_exp_f32 v121,v121
v_fma_f32 v118,v118,v101,v101
v_fma_f32 v119,v119,v101,v101
v_fma_f32 v120,v120,v101,v101
v_fma_f32 v121,v121,v101,v101
v_rcp_f32 v118,v118
v_rcp_f32 v119,v119
v_rcp_f32 v120,v120
v_rcp_f32 v121,v121
v_mul_f32 v22,v22,v118
v_mul_f32 v23,v23,v119
v_mul_f32 v24,v24,v120
v_mul_f32 v25,v25,v121
v_cvt_pk_bf16_f32 v124,v22,v23
v_cvt_pk_bf16_f32 v125,v24,v25
buffer_store_dwordx4 v[122:125],v15,s[36:39],s66 offen offset:0 sc0 sc1
buffer_store_dwordx4 v[122:125],v16,s[36:39],s66 offen offset:0 sc0 sc1
s_mov_b32 s72,s69
s_mov_b32 s69,0x7fff0000
s_mov_b32 s66,0x7fff0000
s_mov_b32 s76,0x7fff0000
s_branch Lg1_done_%=
Lg1_K1_%=:
Lg1_L1_%=:
v_mfma_f32_16x16x32_bf16 v[30:33],a[4:7],v[34:37],0
s_add_u32 m0,s59,3072
s_mov_b32 s67,s65
R 54,2,5120
M 30,a[12:15],38
buffer_load_dword v8,s[36:39],s58 offen offset:0 sc1 lds
v_mfma_f32_16x16x32_bf16 v[26:29],a[0:3],v[34:37],0
s_add_u32 s65,s65,s62
R 58,2,6144
v_fma_f32 v18,-v98,v106,v18
M 26,a[8:11],38
L 3
v_fma_f32 v19,-v98,v107,v19
v_fma_f32 v20,-v98,v108,v20
M 26,a[16:19],42
R 62,2,7168
v_fma_f32 v21,-v98,v109,v21
M 26,a[24:27],46
v_mul_f32 v114,v18,v18
v_mul_f32 v115,v19,v19
M 30,a[20:23],42
R 66,2,8192
v_mul_f32 v116,v20,v20
M 30,a[28:31],46
L 3
v_mul_f32 v117,v21,v21
v_fma_f32 v114,v100,v114,v99
M 30,a[36:39],50
R 70,2,9216
v_fma_f32 v115,v100,v115,v99
M 30,a[44:47],54
v_fma_f32 v116,v100,v116,v99
v_fma_f32 v117,v100,v117,v99
M 26,a[32:35],50
R 74,2,10240
v_mul_f32 v114,v18,v114
M 26,a[40:43],54
L 3
v_exp_f32 v114,v114
M 26,a[48:51],58
R 78,2,11264
v_mul_f32 v115,v19,v115
M 26,a[56:59],62
v_exp_f32 v115,v115
M 30,a[52:55],58
R 82,2,12288
v_mul_f32 v116,v20,v116
M 30,a[60:63],62
L 3
v_exp_f32 v116,v116
M 30,a[68:71],66
R 86,2,13312
v_mul_f32 v117,v21,v117
M 30,a[76:79],70
buffer_load_dwordx4 v[102:105],v11,s[36:39],s73 offen offset:0 sc1
M 26,a[64:67],66
s_add_u32 s73,s73,s64
R 90,2,14336
v_fma_f32 v114,v114,v101,v101
M 26,a[72:75],70
L 3
v_exp_f32 v117,v117
M 26,a[80:83],74
s_waitcnt vmcnt(1)
R 94,2,15360
v_fma_f32 v115,v115,v101,v101
M 26,a[88:91],78
s_barrier
ds_read_b32 v7,v6 offset:3072
v_fma_f32 v116,v116,v101,v101
M 30,a[84:87],74
R 34,2,16384
v_fma_f32 v117,v117,v101,v101
M 30,a[92:95],78
L 4
buffer_store_dword v4,v3,s[36:39],s70 offen offset:0 sc0 sc1
M 30,a[100:103],82
s_mov_b32 s70,0x7fff0000
R 38,2,17408
v_fma_f32 v22,-v98,v110,v22
M 30,a[108:111],86
v_rcp_f32 v114,v114
M 26,a[96:99],82
R 42,2,18432
v_mul_f32 v18,v18,v114
M 26,a[104:107],86
L 4
L 3
v_cmp_eq_u32 vcc,s53,v7
M 26,a[112:115],90
R 46,2,19456
v_fma_f32 v23,-v98,v111,v23
M 26,a[120:123],94
v_rcp_f32 v115,v115
M 30,a[116:119],90
s_cmp_eq_u32 vcc_lo,-1
R 50,2,20480
v_mul_f32 v19,v19,v115
s_cbranch_scc0 Lg1_X1_%=
M 30,a[124:127],94
L 3
s_add_u32 m0,s57,0
s_add_u32 s56,s55,0
M 30,a[132:135],34
s_add_u32 s58,s58,s61
R 54,2,21504
v_cvt_pk_bf16_f32 v122,v18,v19
M 30,a[140:143],38
D 0,56,0
M 26,a[128:131],34
s_add_u32 s54,s54,s46
R 58,2,22528
v_fma_f32 v24,-v98,v112,v24
M 26,a[136:139],38
L 3
v_rcp_f32 v116,v116
M 26,a[144:147],42
R 62,2,23552
v_mul_f32 v20,v20,v116
M 26,a[152:155],46
D 0,56,1024
M 30,a[148:151],42
R 66,2,24576
v_fma_f32 v25,-v98,v113,v25
M 30,a[156:159],46
L 3
v_rcp_f32 v117,v117
M 30,a[164:167],50
R 70,2,25600
v_mul_f32 v21,v21,v117
M 30,a[172:175],54
D 0,56,2048
M 26,a[160:163],50
R 74,2,26624
v_cvt_pk_bf16_f32 v123,v20,v21
M 26,a[168:171],54
L 3
v_mul_f32 v118,v22,v22
v_mul_f32 v119,v23,v23
M 26,a[176:179],58
R 78,2,27648
v_mul_f32 v120,v24,v24
M 26,a[184:187],62
D 0,56,3072
M 30,a[180:183],58
s_add_u32 m0,s59,1024
R 82,2,28672
v_mul_f32 v121,v25,v25
M 30,a[188:191],62
L 3
buffer_load_dword v8,s[36:39],s58 offen offset:0 sc1 lds
M 30,a[196:199],66
s_add_u32 m0,s57,16384
R 86,2,29696
v_fma_f32 v118,v100,v118,v99
M 30,a[204:207],70
s_add_u32 s56,s55,16384
v_fma_f32 v119,v100,v119,v99
v_fma_f32 v120,v100,v120,v99
M 26,a[192:195],66
s_add_u32 s55,s55,s60
R 90,2,30720
v_fma_f32 v121,v100,v121,v99
M 26,a[200:203],70
L 3
D 0,56,0
M 26,a[208:211],74
s_waitcnt vmcnt(1)
R 94,2,31744
v_mul_f32 v118,v22,v118
M 26,a[216:219],78
s_barrier
ds_read_b32 v7,v6 offset:1024
v_mul_f32 v119,v23,v119
M 30,a[212:215],74
s_sub_u32 s57,s84,s57
R 34,1,0
v_mul_f32 v120,v24,v120
M 30,a[220:223],78
L 4
buffer_store_dword v4,v3,s[36:39],s70 offen offset:0 sc0 sc1
v_mul_f32 v121,v25,v121
v_exp_f32 v118,v118
v_exp_f32 v119,v119
v_exp_f32 v120,v120
v_exp_f32 v121,v121
M 30,a[228:231],82
s_mov_b32 s70,s69
R 38,1,1024
v_fma_f32 v118,v118,v101,v101
v_fma_f32 v119,v119,v101,v101
v_fma_f32 v120,v120,v101,v101
v_fma_f32 v121,v121,v101,v101
v_rcp_f32 v118,v118
v_rcp_f32 v119,v119
v_rcp_f32 v120,v120
v_rcp_f32 v121,v121
M 30,a[236:239],86
s_mov_b32 s69,s68
v_mul_f32 v22,v22,v118
v_mul_f32 v23,v23,v119
v_mul_f32 v24,v24,v120
v_mul_f32 v25,v25,v121
v_cvt_pk_bf16_f32 v124,v22,v23
v_cvt_pk_bf16_f32 v125,v24,v25
M 26,a[224:227],82
s_add_u32 s68,s68,s63
R 42,1,2048
buffer_store_dwordx4 v[122:125],v15,s[36:39],s66 offen offset:0 sc0 sc1
M 26,a[232:235],86
L 4
L 3
M 26,a[240:243],90
R 46,1,3072
v_cmp_eq_u32 vcc,s53,v7
M 26,a[248:251],94
D 0,56,1024
M 30,a[244:247],90
L 2
s_cmp_eq_u32 vcc_lo,-1
R 50,1,4096
buffer_store_dwordx4 v[122:125],v16,s[36:39],s66 offen offset:0 sc0 sc1
M 30,a[252:255],94
D 0,56,2048
D 0,56,3072
buffer_store_dword v4,v3,s[36:39],s71 offen offset:0 sc0 sc1
buffer_store_dword v4,v3,s[36:39],s71 offen offset:0 sc0 sc1
buffer_store_dword v4,v3,s[36:39],s71 offen offset:0 sc0 sc1
s_cbranch_scc1 Lg1_F0_%=
s_branch Lg1_K0_%=
Lg1_X1_%=:
M 30,a[124:127],94
L 3
s_add_u32 m0,s59,1024
v_cvt_pk_bf16_f32 v122,v18,v19
M 30,a[132:135],34
R 54,2,21504
v_fma_f32 v24,-v98,v112,v24
M 30,a[140:143],38
buffer_load_dword v8,s[36:39],s58 offen offset:0 sc1 lds
M 26,a[128:131],34
R 58,2,22528
v_fma_f32 v25,-v98,v113,v25
M 26,a[136:139],38
L 3
v_rcp_f32 v116,v116
M 26,a[144:147],42
R 62,2,23552
v_mul_f32 v20,v20,v116
M 26,a[152:155],46
v_rcp_f32 v117,v117
M 30,a[148:151],42
R 66,2,24576
v_mul_f32 v21,v21,v117
M 30,a[156:159],46
L 3
v_cvt_pk_bf16_f32 v123,v20,v21
v_mul_f32 v118,v22,v22
M 30,a[164:167],50
R 70,2,25600
v_mul_f32 v119,v23,v23
M 30,a[172:175],54
v_mul_f32 v120,v24,v24
v_mul_f32 v121,v25,v25
M 26,a[160:163],50
R 74,2,26624
v_fma_f32 v118,v100,v118,v99
M 26,a[168:171],54
L 3
v_fma_f32 v119,v100,v119,v99
v_fma_f32 v120,v100,v120,v99
M 26,a[176:179],58
R 78,2,27648
v_fma_f32 v121,v100,v121,v99
M 26,a[184:187],62
v_mul_f32 v118,v22,v118
v_mul_f32 v119,v23,v119
M 30,a[180:183],58
R 82,2,28672
v_mul_f32 v120,v24,v120
M 30,a[188:191],62
L 3
v_exp_f32 v118,v118
M 30,a[196:199],66
R 86,2,29696
v_mul_f32 v121,v25,v121
M 30,a[204:207],70
v_exp_f32 v119,v119
M 26,a[192:195],66
R 90,2,30720
v_fma_f32 v118,v118,v101,v101
M 26,a[200:203],70
L 3
v_exp_f32 v120,v120
M 26,a[208:211],74
s_waitcnt vmcnt(0)
R 94,2,31744
v_fma_f32 v119,v119,v101,v101
M 26,a[216:219],78
s_barrier
buffer_store_dword v4,v3,s[36:39],s70 offen offset:0 sc0 sc1
M 30,a[212:215],74
s_mov_b32 s70,s69
ds_read_b32 v7,v6 offset:1024
v_fma_f32 v120,v120,v101,v101
M 30,a[220:223],78
L 3
v_exp_f32 v121,v121
M 30,a[228:231],82
s_mov_b32 s69,s68
v_fma_f32 v121,v121,v101,v101
v_rcp_f32 v118,v118
v_rcp_f32 v119,v119
v_rcp_f32 v120,v120
v_rcp_f32 v121,v121
M 30,a[236:239],86
s_add_u32 s68,s68,s63
v_mul_f32 v22,v22,v118
v_mul_f32 v23,v23,v119
v_mul_f32 v24,v24,v120
v_mul_f32 v25,v25,v121
v_cvt_pk_bf16_f32 v124,v22,v23
v_cvt_pk_bf16_f32 v125,v24,v25
M 26,a[224:227],82
buffer_store_dwordx4 v[122:125],v15,s[36:39],s66 offen offset:0 sc0 sc1
M 26,a[232:235],86
L 1
M 26,a[240:243],90
L 0
v_cmp_eq_u32 vcc,s53,v7
M 26,a[248:251],94
buffer_store_dwordx4 v[122:125],v16,s[36:39],s66 offen offset:0 sc0 sc1
M 30,a[244:247],90
s_nop 0
M 30,a[252:255],94
s_mov_b32 s80,0
s_cmp_eq_u32 vcc_lo,-1
s_cbranch_scc0 Lg1_xn1_%=
s_add_u32 m0,s57,0
s_add_u32 s56,s55,0
D 0,56,0
v_fma_f32 v26,-v102,v106,v26
v_fma_f32 v27,-v102,v107,v27
v_fma_f32 v28,-v102,v108,v28
v_fma_f32 v29,-v102,v109,v29
v_mul_f32 v114,v26,v26
v_mul_f32 v115,v27,v27
v_mul_f32 v116,v28,v28
D 0,56,1024
v_mul_f32 v117,v29,v29
v_fma_f32 v114,v104,v114,v103
v_fma_f32 v115,v104,v115,v103
v_fma_f32 v116,v104,v116,v103
v_fma_f32 v117,v104,v117,v103
v_mul_f32 v114,v26,v114
v_mul_f32 v115,v27,v115
D 0,56,2048
)"
R"(v_mul_f32 v116,v28,v116
v_mul_f32 v117,v29,v117
v_exp_f32 v114,v114
v_exp_f32 v115,v115
v_exp_f32 v116,v116
v_exp_f32 v117,v117
v_fma_f32 v114,v114,v105,v105
D 0,56,3072
v_fma_f32 v115,v115,v105,v105
v_fma_f32 v116,v116,v105,v105
v_fma_f32 v117,v117,v105,v105
v_rcp_f32 v114,v114
v_rcp_f32 v115,v115
v_rcp_f32 v116,v116
v_rcp_f32 v117,v117
s_add_u32 s58,s58,s61
s_add_u32 m0,s57,16384
s_add_u32 s56,s55,16384
D 0,56,0
v_mul_f32 v26,v26,v114
v_mul_f32 v27,v27,v115
v_mul_f32 v28,v28,v116
v_mul_f32 v29,v29,v117
v_cvt_pk_bf16_f32 v122,v26,v27
v_cvt_pk_bf16_f32 v123,v28,v29
v_fma_f32 v30,-v102,v110,v30
D 0,56,1024
v_fma_f32 v31,-v102,v111,v31
v_fma_f32 v32,-v102,v112,v32
v_fma_f32 v33,-v102,v113,v33
v_mul_f32 v118,v30,v30
v_mul_f32 v119,v31,v31
v_mul_f32 v120,v32,v32
v_mul_f32 v121,v33,v33
D 0,56,2048
v_fma_f32 v118,v104,v118,v103
v_fma_f32 v119,v104,v119,v103
v_fma_f32 v120,v104,v120,v103
v_fma_f32 v121,v104,v121,v103
v_mul_f32 v118,v30,v118
v_mul_f32 v119,v31,v119
v_mul_f32 v120,v32,v120
D 0,56,3072
v_mul_f32 v121,v33,v121
v_exp_f32 v118,v118
v_exp_f32 v119,v119
v_exp_f32 v120,v120
v_exp_f32 v121,v121
v_fma_f32 v118,v118,v105,v105
v_fma_f32 v119,v119,v105,v105
s_add_u32 s55,s55,s60
s_sub_u32 s57,s84,s57
s_add_u32 s54,s54,s46
v_fma_f32 v120,v120,v105,v105
v_fma_f32 v121,v121,v105,v105
v_rcp_f32 v118,v118
v_rcp_f32 v119,v119
v_rcp_f32 v120,v120
v_rcp_f32 v121,v121
v_mul_f32 v30,v30,v118
v_mul_f32 v31,v31,v119
v_mul_f32 v32,v32,v120
v_mul_f32 v33,v33,v121
v_cvt_pk_bf16_f32 v124,v30,v31
v_cvt_pk_bf16_f32 v125,v32,v33
buffer_store_dwordx4 v[122:125],v15,s[36:39],s67 offen offset:0 sc0 sc1
buffer_store_dwordx4 v[122:125],v16,s[36:39],s67 offen offset:0 sc0 sc1
s_mov_b32 s72,s69
s_mov_b32 s69,0x7fff0000
s_mov_b32 s67,0x7fff0000
s_mov_b32 s77,0x7fff0000
s_branch Lg1_gox_%=
Lg1_xn1_%=:
s_cmp_ge_u32 s54,256
s_cbranch_scc1 Lg1_fin1_%=
s_add_u32 m0,s59,4096
s_nop 0
buffer_load_dword v8,s[36:39],s58 offen offset:0 sc1 lds
buffer_store_dword v4,v3,s[36:39],s71 offen offset:0 sc0 sc1
s_add_u32 m0,s59,5120
s_nop 0
buffer_load_dword v8,s[36:39],s58 offen offset:0 sc1 lds
buffer_store_dword v4,v3,s[36:39],s71 offen offset:0 sc0 sc1
s_mov_b32 s83,0
v_fma_f32 v26,-v102,v106,v26
v_fma_f32 v27,-v102,v107,v27
v_fma_f32 v28,-v102,v108,v28
v_fma_f32 v29,-v102,v109,v29
v_mul_f32 v114,v26,v26
v_mul_f32 v115,v27,v27
v_mul_f32 v116,v28,v28
v_mul_f32 v117,v29,v29
v_fma_f32 v114,v104,v114,v103
v_fma_f32 v115,v104,v115,v103
v_fma_f32 v116,v104,v116,v103
v_fma_f32 v117,v104,v117,v103
v_mul_f32 v114,v26,v114
v_mul_f32 v115,v27,v115
v_mul_f32 v116,v28,v116
v_mul_f32 v117,v29,v117
v_exp_f32 v114,v114
v_exp_f32 v115,v115
v_exp_f32 v116,v116
v_exp_f32 v117,v117
v_fma_f32 v114,v114,v105,v105
v_fma_f32 v115,v115,v105,v105
v_fma_f32 v116,v116,v105,v105
v_fma_f32 v117,v117,v105,v105
v_rcp_f32 v114,v114
v_rcp_f32 v115,v115
v_rcp_f32 v116,v116
v_rcp_f32 v117,v117
v_mul_f32 v26,v26,v114
v_mul_f32 v27,v27,v115
v_mul_f32 v28,v28,v116
v_mul_f32 v29,v29,v117
v_cvt_pk_bf16_f32 v122,v26,v27
v_cvt_pk_bf16_f32 v123,v28,v29
v_fma_f32 v30,-v102,v110,v30
v_fma_f32 v31,-v102,v111,v31
v_fma_f32 v32,-v102,v112,v32
v_fma_f32 v33,-v102,v113,v33
v_mul_f32 v118,v30,v30
v_mul_f32 v119,v31,v31
v_mul_f32 v120,v32,v32
v_mul_f32 v121,v33,v33
v_fma_f32 v118,v104,v118,v103
v_fma_f32 v119,v104,v119,v103
v_fma_f32 v120,v104,v120,v103
v_fma_f32 v121,v104,v121,v103
v_mul_f32 v118,v30,v118
v_mul_f32 v119,v31,v119
v_mul_f32 v120,v32,v120
v_mul_f32 v121,v33,v121
v_exp_f32 v118,v118
v_exp_f32 v119,v119
v_exp_f32 v120,v120
v_exp_f32 v121,v121
v_fma_f32 v118,v118,v105,v105
v_fma_f32 v119,v119,v105,v105
v_fma_f32 v120,v120,v105,v105
v_fma_f32 v121,v121,v105,v105
v_rcp_f32 v118,v118
v_rcp_f32 v119,v119
v_rcp_f32 v120,v120
v_rcp_f32 v121,v121
v_mul_f32 v30,v30,v118
v_mul_f32 v31,v31,v119
v_mul_f32 v32,v32,v120
v_mul_f32 v33,v33,v121
v_cvt_pk_bf16_f32 v124,v30,v31
v_cvt_pk_bf16_f32 v125,v32,v33
buffer_store_dwordx4 v[122:125],v15,s[36:39],s67 offen offset:0 sc0 sc1
buffer_store_dwordx4 v[122:125],v16,s[36:39],s67 offen offset:0 sc0 sc1
s_mov_b32 s72,s69
s_mov_b32 s69,0x7fff0000
s_mov_b32 s67,0x7fff0000
s_mov_b32 s77,0x7fff0000
s_branch Lg1_poll_%=
Lg1_fin1_%=:
v_fma_f32 v26,-v102,v106,v26
v_fma_f32 v27,-v102,v107,v27
v_fma_f32 v28,-v102,v108,v28
v_fma_f32 v29,-v102,v109,v29
v_mul_f32 v114,v26,v26
v_mul_f32 v115,v27,v27
v_mul_f32 v116,v28,v28
v_mul_f32 v117,v29,v29
v_fma_f32 v114,v104,v114,v103
v_fma_f32 v115,v104,v115,v103
v_fma_f32 v116,v104,v116,v103
v_fma_f32 v117,v104,v117,v103
v_mul_f32 v114,v26,v114
v_mul_f32 v115,v27,v115
v_mul_f32 v116,v28,v116
v_mul_f32 v117,v29,v117
v_exp_f32 v114,v114
v_exp_f32 v115,v115
v_exp_f32 v116,v116
v_exp_f32 v117,v117
v_fma_f32 v114,v114,v105,v105
v_fma_f32 v115,v115,v105,v105
v_fma_f32 v116,v116,v105,v105
v_fma_f32 v117,v117,v105,v105
v_rcp_f32 v114,v114
v_rcp_f32 v115,v115
v_rcp_f32 v116,v116
v_rcp_f32 v117,v117
v_mul_f32 v26,v26,v114
v_mul_f32 v27,v27,v115
v_mul_f32 v28,v28,v116
v_mul_f32 v29,v29,v117
v_cvt_pk_bf16_f32 v122,v26,v27
v_cvt_pk_bf16_f32 v123,v28,v29
v_fma_f32 v30,-v102,v110,v30
v_fma_f32 v31,-v102,v111,v31
v_fma_f32 v32,-v102,v112,v32
v_fma_f32 v33,-v102,v113,v33
v_mul_f32 v118,v30,v30
v_mul_f32 v119,v31,v31
v_mul_f32 v120,v32,v32
v_mul_f32 v121,v33,v33
v_fma_f32 v118,v104,v118,v103
v_fma_f32 v119,v104,v119,v103
v_fma_f32 v120,v104,v120,v103
v_fma_f32 v121,v104,v121,v103
v_mul_f32 v118,v30,v118
v_mul_f32 v119,v31,v119
v_mul_f32 v120,v32,v120
v_mul_f32 v121,v33,v121
v_exp_f32 v118,v118
v_exp_f32 v119,v119
v_exp_f32 v120,v120
v_exp_f32 v121,v121
v_fma_f32 v118,v118,v105,v105
v_fma_f32 v119,v119,v105,v105
v_fma_f32 v120,v120,v105,v105
v_fma_f32 v121,v121,v105,v105
v_rcp_f32 v118,v118
v_rcp_f32 v119,v119
v_rcp_f32 v120,v120
v_rcp_f32 v121,v121
v_mul_f32 v30,v30,v118
v_mul_f32 v31,v31,v119
v_mul_f32 v32,v32,v120
v_mul_f32 v33,v33,v121
v_cvt_pk_bf16_f32 v124,v30,v31
v_cvt_pk_bf16_f32 v125,v32,v33
buffer_store_dwordx4 v[122:125],v15,s[36:39],s67 offen offset:0 sc0 sc1
buffer_store_dwordx4 v[122:125],v16,s[36:39],s67 offen offset:0 sc0 sc1
s_mov_b32 s72,s69
s_mov_b32 s69,0x7fff0000
s_mov_b32 s67,0x7fff0000
s_mov_b32 s77,0x7fff0000
s_branch Lg1_done_%=
Lg1_done_%=:
s_waitcnt vmcnt(2)
s_barrier
buffer_store_dword v4,v3,s[36:39],s70 offen offset:0 sc0 sc1
s_waitcnt vmcnt(1)
s_barrier
buffer_store_dword v4,v3,s[36:39],s72 offen offset:0 sc0 sc1
s_waitcnt vmcnt(0) lgkmcnt(0)
s_barrier
s_branch Lg2_end_%=
Lg2_begin_%=:
Lcode_g2_b:
s_mov_b64 s[36:37],%1
s_mov_b32 s38,111243392
s_mov_b32 s39,0x00020000
s_mov_b32 s44,%2
s_mov_b32 s45,%3
s_mov_b32 s46,%4
s_mov_b32 s47,%5
s_mov_b32 s48,%6
s_mov_b32 s49,%7
s_mov_b32 s50,%8
s_mov_b32 s51,%9
s_mov_b32 s53,%10
v_mov_b32 v0,%11
v_mov_b32 v3,%12
v_mov_b32 v5,%13
v_mov_b32 v6,%14
v_mov_b32 v18,%15
v_mov_b32 v19,%16
v_mov_b32 v20,%17
v_mov_b32 v21,%18
v_mov_b32 v9,%19
v_mov_b32 v10,%20
v_mov_b32 v11,%21
v_mov_b32 v12,%22
v_mov_b32 v13,%23
v_mov_b32 v14,%24
v_mov_b32 v15,%25
v_mov_b32 v16,%26
s_mov_b64 s[40:41],%27
s_mov_b32 s42,2097152
s_mov_b32 s43,0x00020000
s_lshl_b32 s57,s44,11
s_cmp_ge_u32 s45,8
s_cbranch_scc1 Lg2_dly_%=
s_sleep 16
s_sleep 16
s_sleep 16
s_sleep 16
Lg2_dly_%=:
s_add_u32 s55,s50,0
s_add_u32 m0,s57,0
s_nop 0
buffer_load_dwordx4 v18,s[40:43],s55 offen offset:0  lds
s_add_u32 m0,s57,1024
s_nop 0
buffer_load_dwordx4 v19,s[40:43],s55 offen offset:0  lds
s_add_u32 m0,s57,8064
s_nop 0
buffer_load_dwordx4 v18,s[40:43],s55 offen offset:128  lds
s_add_u32 m0,s57,9088
s_nop 0
buffer_load_dwordx4 v19,s[40:43],s55 offen offset:128  lds
s_add_u32 s55,s50,131072
s_add_u32 m0,s57,16384
s_nop 0
buffer_load_dwordx4 v18,s[40:43],s55 offen offset:0  lds
s_add_u32 m0,s57,17408
s_nop 0
buffer_load_dwordx4 v19,s[40:43],s55 offen offset:0  lds
s_add_u32 m0,s57,24448
s_nop 0
buffer_load_dwordx4 v18,s[40:43],s55 offen offset:128  lds
s_add_u32 m0,s57,25472
s_nop 0
buffer_load_dwordx4 v19,s[40:43],s55 offen offset:128  lds
s_add_u32 s55,s50,262144
s_add_u32 m0,s57,32768
s_nop 0
buffer_load_dwordx4 v18,s[40:43],s55 offen offset:0  lds
s_add_u32 m0,s57,33792
s_nop 0
buffer_load_dwordx4 v19,s[40:43],s55 offen offset:0  lds
s_add_u32 m0,s57,40832
s_nop 0
buffer_load_dwordx4 v18,s[40:43],s55 offen offset:128  lds
s_add_u32 m0,s57,41856
s_nop 0
buffer_load_dwordx4 v19,s[40:43],s55 offen offset:128  lds
s_add_u32 s55,s50,393216
s_add_u32 m0,s57,49152
s_nop 0
buffer_load_dwordx4 v18,s[40:43],s55 offen offset:0  lds
s_add_u32 m0,s57,50176
s_nop 0
buffer_load_dwordx4 v19,s[40:43],s55 offen offset:0  lds
s_add_u32 m0,s57,57216
s_nop 0
buffer_load_dwordx4 v18,s[40:43],s55 offen offset:128  lds
s_add_u32 m0,s57,58240
s_nop 0
buffer_load_dwordx4 v19,s[40:43],s55 offen offset:128  lds
s_add_u32 s55,s50,524288
s_add_u32 m0,s57,65536
s_nop 0
buffer_load_dwordx4 v18,s[40:43],s55 offen offset:0  lds
s_add_u32 m0,s57,66560
s_nop 0
buffer_load_dwordx4 v19,s[40:43],s55 offen offset:0  lds
s_add_u32 m0,s57,73600
s_nop 0
buffer_load_dwordx4 v18,s[40:43],s55 offen offset:128  lds
s_add_u32 m0,s57,74624
s_nop 0
buffer_load_dwordx4 v19,s[40:43],s55 offen offset:128  lds
s_add_u32 s55,s50,655360
s_add_u32 m0,s57,81920
s_nop 0
buffer_load_dwordx4 v18,s[40:43],s55 offen offset:0  lds
s_add_u32 m0,s57,82944
s_nop 0
buffer_load_dwordx4 v19,s[40:43],s55 offen offset:0  lds
s_add_u32 m0,s57,89984
s_nop 0
buffer_load_dwordx4 v18,s[40:43],s55 offen offset:128  lds
s_add_u32 m0,s57,91008
s_nop 0
buffer_load_dwordx4 v19,s[40:43],s55 offen offset:128  lds
s_waitcnt vmcnt(20) lgkmcnt(0)
s_barrier
s_add_u32 s55,s50,786432
s_add_u32 m0,s57,98304
s_nop 0
buffer_load_dwordx4 v18,s[40:43],s55 offen offset:0  lds
s_add_u32 m0,s57,99328
s_nop 0
buffer_load_dwordx4 v19,s[40:43],s55 offen offset:0  lds
s_add_u32 m0,s57,106368
s_nop 0
buffer_load_dwordx4 v18,s[40:43],s55 offen offset:128  lds
s_add_u32 m0,s57,107392
s_nop 0
buffer_load_dwordx4 v19,s[40:43],s55 offen offset:128  lds
v_add_u32 v26,0,v20
v_add_u32 v27,0,v21
T a[0:1],26,0
T a[2:3],26,512
T a[4:5],27,0
T a[6:7],27,512
T a[8:9],26,4096
T a[10:11],26,4608
T a[12:13],27,4096
T a[14:15],27,4608
s_waitcnt vmcnt(20) lgkmcnt(0)
s_barrier
s_add_u32 s55,s50,917504
s_add_u32 m0,s57,0
s_nop 0
buffer_load_dwordx4 v18,s[40:43],s55 offen offset:0  lds
s_add_u32 m0,s57,1024
s_nop 0
buffer_load_dwordx4 v19,s[40:43],s55 offen offset:0  lds
s_add_u32 m0,s57,8064
s_nop 0
buffer_load_dwordx4 v18,s[40:43],s55 offen offset:128  lds
s_add_u32 m0,s57,9088
s_nop 0
buffer_load_dwordx4 v19,s[40:43],s55 offen offset:128  lds
v_add_u32 v26,16384,v20
v_add_u32 v27,16384,v21
T a[16:17],26,0
T a[18:19],26,512
T a[20:21],27,0
T a[22:23],27,512
T a[24:25],26,4096
T a[26:27],26,4608
T a[28:29],27,4096
T a[30:31],27,4608
s_waitcnt vmcnt(20) lgkmcnt(0)
s_barrier
s_add_u32 s55,s50,1048576
s_add_u32 m0,s57,16384
s_nop 0
buffer_load_dwordx4 v18,s[40:43],s55 offen offset:0  lds
s_add_u32 m0,s57,17408
s_nop 0
buffer_load_dwordx4 v19,s[40:43],s55 offen offset:0  lds
s_add_u32 m0,s57,24448
s_nop 0
buffer_load_dwordx4 v18,s[40:43],s55 offen offset:128  lds
s_add_u32 m0,s57,25472
s_nop 0
buffer_load_dwordx4 v19,s[40:43],s55 offen offset:128  lds
v_add_u32 v26,32768,v20
v_add_u32 v27,32768,v21
T a[32:33],26,0
T a[34:35],26,512
T a[36:37],27,0
T a[38:39],27,512
T a[40:41],26,4096
T a[42:43],26,4608
T a[44:45],27,4096
T a[46:47],27,4608
s_waitcnt vmcnt(20) lgkmcnt(0)
s_barrier
s_add_u32 s55,s50,1179648
s_add_u32 m0,s57,32768
s_nop 0
buffer_load_dwordx4 v18,s[40:43],s55 offen offset:0  lds
s_add_u32 m0,s57,33792
s_nop 0
buffer_load_dwordx4 v19,s[40:43],s55 offen offset:0  lds
s_add_u32 m0,s57,40832
s_nop 0
buffer_load_dwordx4 v18,s[40:43],s55 offen offset:128  lds
s_add_u32 m0,s57,41856
s_nop 0
buffer_load_dwordx4 v19,s[40:43],s55 offen offset:128  lds
v_add_u32 v26,49152,v20
v_add_u32 v27,49152,v21
T a[48:49],26,0
T a[50:51],26,512
T a[52:53],27,0
T a[54:55],27,512
T a[56:57],26,4096
T a[58:59],26,4608
T a[60:61],27,4096
T a[62:63],27,4608
s_waitcnt vmcnt(20) lgkmcnt(0)
s_barrier
s_add_u32 s55,s50,1310720
s_add_u32 m0,s57,49152
s_nop 0
buffer_load_dwordx4 v18,s[40:43],s55 offen offset:0  lds
s_add_u32 m0,s57,50176
s_nop 0
buffer_load_dwordx4 v19,s[40:43],s55 offen offset:0  lds
s_add_u32 m0,s57,57216
s_nop 0
buffer_load_dwordx4 v18,s[40:43],s55 offen offset:128  lds
s_add_u32 m0,s57,58240
s_nop 0
buffer_load_dwordx4 v19,s[40:43],s55 offen offset:128  lds
v_add_u32 v26,65536,v20
v_add_u32 v27,65536,v21
T a[64:65],26,0
T a[66:67],26,512
T a[68:69],27,0
T a[70:71],27,512
T a[72:73],26,4096
T a[74:75],26,4608
T a[76:77],27,4096
T a[78:79],27,4608
s_waitcnt vmcnt(20) lgkmcnt(0)
s_barrier
s_add_u32 s55,s50,1441792
s_add_u32 m0,s57,65536
s_nop 0
buffer_load_dwordx4 v18,s[40:43],s55 offen offset:0  lds
s_add_u32 m0,s57,66560
s_nop 0
buffer_load_dwordx4 v19,s[40:43],s55 offen offset:0  lds
s_add_u32 m0,s57,73600
s_nop 0
)"
R"(buffer_load_dwordx4 v18,s[40:43],s55 offen offset:128  lds
s_add_u32 m0,s57,74624
s_nop 0
buffer_load_dwordx4 v19,s[40:43],s55 offen offset:128  lds
v_add_u32 v26,81920,v20
v_add_u32 v27,81920,v21
T a[80:81],26,0
T a[82:83],26,512
T a[84:85],27,0
T a[86:87],27,512
T a[88:89],26,4096
T a[90:91],26,4608
T a[92:93],27,4096
T a[94:95],27,4608
s_waitcnt vmcnt(20) lgkmcnt(0)
s_barrier
s_add_u32 s55,s50,1572864
s_add_u32 m0,s57,81920
s_nop 0
buffer_load_dwordx4 v18,s[40:43],s55 offen offset:0  lds
s_add_u32 m0,s57,82944
s_nop 0
buffer_load_dwordx4 v19,s[40:43],s55 offen offset:0  lds
s_add_u32 m0,s57,89984
s_nop 0
buffer_load_dwordx4 v18,s[40:43],s55 offen offset:128  lds
s_add_u32 m0,s57,91008
s_nop 0
buffer_load_dwordx4 v19,s[40:43],s55 offen offset:128  lds
v_add_u32 v26,98304,v20
v_add_u32 v27,98304,v21
T a[96:97],26,0
T a[98:99],26,512
T a[100:101],27,0
T a[102:103],27,512
T a[104:105],26,4096
T a[106:107],26,4608
T a[108:109],27,4096
T a[110:111],27,4608
s_waitcnt vmcnt(20) lgkmcnt(0)
s_barrier
s_add_u32 s55,s50,1703936
s_add_u32 m0,s57,98304
s_nop 0
buffer_load_dwordx4 v18,s[40:43],s55 offen offset:0  lds
s_add_u32 m0,s57,99328
s_nop 0
buffer_load_dwordx4 v19,s[40:43],s55 offen offset:0  lds
s_add_u32 m0,s57,106368
s_nop 0
buffer_load_dwordx4 v18,s[40:43],s55 offen offset:128  lds
s_add_u32 m0,s57,107392
s_nop 0
buffer_load_dwordx4 v19,s[40:43],s55 offen offset:128  lds
v_add_u32 v26,0,v20
v_add_u32 v27,0,v21
T a[112:113],26,0
T a[114:115],26,512
T a[116:117],27,0
T a[118:119],27,512
T a[120:121],26,4096
T a[122:123],26,4608
T a[124:125],27,4096
T a[126:127],27,4608
s_waitcnt vmcnt(20) lgkmcnt(0)
s_barrier
s_add_u32 s55,s50,1835008
s_add_u32 m0,s57,0
s_nop 0
buffer_load_dwordx4 v18,s[40:43],s55 offen offset:0  lds
s_add_u32 m0,s57,1024
s_nop 0
buffer_load_dwordx4 v19,s[40:43],s55 offen offset:0  lds
s_add_u32 m0,s57,8064
s_nop 0
buffer_load_dwordx4 v18,s[40:43],s55 offen offset:128  lds
s_add_u32 m0,s57,9088
s_nop 0
buffer_load_dwordx4 v19,s[40:43],s55 offen offset:128  lds
v_add_u32 v26,16384,v20
v_add_u32 v27,16384,v21
T a[128:129],26,0
T a[130:131],26,512
T a[132:133],27,0
T a[134:135],27,512
T a[136:137],26,4096
T a[138:139],26,4608
T a[140:141],27,4096
T a[142:143],27,4608
s_waitcnt vmcnt(20) lgkmcnt(0)
s_barrier
s_add_u32 s55,s50,1966080
s_add_u32 m0,s57,16384
s_nop 0
buffer_load_dwordx4 v18,s[40:43],s55 offen offset:0  lds
s_add_u32 m0,s57,17408
s_nop 0
buffer_load_dwordx4 v19,s[40:43],s55 offen offset:0  lds
s_add_u32 m0,s57,24448
s_nop 0
buffer_load_dwordx4 v18,s[40:43],s55 offen offset:128  lds
s_add_u32 m0,s57,25472
s_nop 0
buffer_load_dwordx4 v19,s[40:43],s55 offen offset:128  lds
v_add_u32 v26,32768,v20
v_add_u32 v27,32768,v21
T a[144:145],26,0
T a[146:147],26,512
T a[148:149],27,0
T a[150:151],27,512
T a[152:153],26,4096
T a[154:155],26,4608
T a[156:157],27,4096
T a[158:159],27,4608
s_waitcnt vmcnt(20) lgkmcnt(0)
s_barrier
v_add_u32 v26,49152,v20
v_add_u32 v27,49152,v21
T a[160:161],26,0
T a[162:163],26,512
T a[164:165],27,0
T a[166:167],27,512
T a[168:169],26,4096
T a[170:171],26,4608
T a[172:173],27,4096
T a[174:175],27,4608
s_waitcnt vmcnt(16) lgkmcnt(0)
s_barrier
v_add_u32 v26,65536,v20
v_add_u32 v27,65536,v21
T a[176:177],26,0
T a[178:179],26,512
T a[180:181],27,0
T a[182:183],27,512
T a[184:185],26,4096
T a[186:187],26,4608
T a[188:189],27,4096
T a[190:191],27,4608
s_waitcnt vmcnt(12) lgkmcnt(0)
s_barrier
v_add_u32 v26,81920,v20
v_add_u32 v27,81920,v21
T a[192:193],26,0
T a[194:195],26,512
T a[196:197],27,0
T a[198:199],27,512
T a[200:201],26,4096
T a[202:203],26,4608
T a[204:205],27,4096
T a[206:207],27,4608
s_waitcnt vmcnt(8) lgkmcnt(0)
s_barrier
v_add_u32 v26,98304,v20
v_add_u32 v27,98304,v21
T a[208:209],26,0
T a[210:211],26,512
T a[212:213],27,0
T a[214:215],27,512
T a[216:217],26,4096
T a[218:219],26,4608
T a[220:221],27,4096
T a[222:223],27,4608
s_waitcnt vmcnt(4) lgkmcnt(0)
s_barrier
v_add_u32 v26,0,v20
v_add_u32 v27,0,v21
T a[224:225],26,0
T a[226:227],26,512
T a[228:229],27,0
T a[230:231],27,512
T a[232:233],26,4096
T a[234:235],26,4608
T a[236:237],27,4096
T a[238:239],27,4608
s_waitcnt vmcnt(0) lgkmcnt(0)
s_barrier
v_add_u32 v26,16384,v20
v_add_u32 v27,16384,v21
T a[240:241],26,0
T a[242:243],26,512
T a[244:245],27,0
T a[246:247],27,512
T a[248:249],26,4096
T a[250:251],26,4608
T a[252:253],27,4096
T a[254:255],27,4608
L 0
s_barrier
v_mov_b32 v4,s53
s_mov_b32 s82,0
s_mov_b32 s80,0
s_mov_b32 s54,s45
s_mul_i32 s57,s44,5120
s_mul_i32 s55,s45,40960
s_add_u32 s55,s55,s57
s_add_u32 s55,s55,17825792
s_mul_i32 s57,s44,4096
s_mul_i32 s60,s46,40960
s_mul_i32 s61,s46,1024
s_mul_i32 s62,s46,32768
s_mul_i32 s63,s46,1024
s_mul_i32 s64,s46,512
s_lshl_b32 s59,s44,8
s_add_u32 s59,s59,131072
s_mul_i32 s58,s45,1024
s_add_u32 s58,s58,109333504
s_mul_i32 s65,s45,32768
s_add_u32 s65,s65,s47
s_mul_i32 s68,s45,1024
s_add_u32 s68,s68,s48
s_mul_i32 s73,s45,512
s_add_u32 s73,s73,106954752
s_mul_i32 s74,s45,32768
s_add_u32 s74,s74,s49
s_mul_i32 s75,s45,1024
s_add_u32 s75,s75,107479040
s_mov_b32 s66,0x7fff0000
s_mov_b32 s67,0x7fff0000
s_mov_b32 s69,0x7fff0000
s_mov_b32 s76,0x7fff0000
s_mov_b32 s77,0x7fff0000
s_mov_b32 s70,0x7fff0000
s_mov_b32 s71,0x7fff0000
s_mov_b32 s72,0x7fff0000
v_mov_b32 v1,v0
v_add_u32 v2,16384,v0
s_mov_b32 s84,16384
s_mov_b32 s86,0x3a800000
s_mov_b32 s87,0x38a7c5ac
s_mov_b32 s88,0xc0135761
s_mov_b32 s89,0x3d372713
s_lshr_b32 s85,s44,1
s_mul_i32 s85,s85,127
v_lshlrev_b32 v8,22,v0
v_and_b32 v8,0x80000000,v8
v_or_b32 v8,v8,v5
v_add_u32 v2,32768,v0
s_lshl_b32 s84,s57,1
s_add_u32 s84,s84,32768
s_getpc_b64 s[40:41]
Lg2_pc_%=:
s_lshl_b32 s78,s44,13
v_lshlrev_b32 v106,3,v0
v_add_u32 v106,s78,v106
v_min_u32 v106,Lcode_g12_e-Lg2_pc_%=-4,v106
s_mov_b32 m0,163584
s_nop 0
global_load_lds_dword v106,s[40:41]
s_and_b32 s79,s54,127
s_cmp_eq_u32 s79,0
s_cselect_b32 s94,0x3300,0
s_cmp_eq_u32 s79,127
s_cselect_b32 s94,0xcc00,s94
s_add_u32 m0,s59,4096
s_nop 0
buffer_load_dword v8,s[36:39],s58 offen offset:0 sc1 lds
buffer_store_dword v4,v3,s[36:39],s71 offen offset:0 sc0 sc1
s_add_u32 m0,s59,5120
s_nop 0
buffer_load_dword v8,s[36:39],s58 offen offset:0 sc1 lds
buffer_store_dword v4,v3,s[36:39],s71 offen offset:0 sc0 sc1
s_mov_b32 s83,0
Lg2_poll_%=:
s_add_u32 s78,s83,2
s_and_b32 s78,s78,3
s_lshl_b32 s78,s78,10
s_add_u32 s78,s78,4096
s_add_u32 m0,s59,s78
s_nop 0
buffer_load_dword v8,s[36:39],s58 offen offset:0 sc1 lds
s_and_b32 s78,s83,3
s_lshl_b32 s78,s78,10
v_add_u32 v106,s78,v6
s_waitcnt vmcnt(4)
s_barrier
ds_read_b32 v7,v106 offset:4096
buffer_store_dword v4,v3,s[36:39],s70 offen offset:0 sc0 sc1
s_mov_b32 s70,s71
s_mov_b32 s71,s72
s_mov_b32 s72,0x7fff0000
s_add_u32 s83,s83,1
s_add_u32 s82,s82,1
L 0
v_cmp_eq_u32 vcc,s53,v7
s_nop 0
s_or_b64 vcc,vcc,s[94:95]
s_cmp_eq_u32 vcc_lo,-1
s_cbranch_scc1 Lg2_go_%=
s_cmp_lt_u32 s82,20000
s_cbranch_scc1 Lg2_poll_%=
Lg2_go_%=:
s_and_b32 s78,s54,127
s_cmp_eq_u32 s78,s85
s_cselect_b64 s[40:41],-1,0
v_cndmask_b32 v17,v15,v16,s[40:41]
s_add_u32 m0,s57,0
s_add_u32 s56,s55,0
D 17,56,0
s_add_u32 s56,s55,256
D 17,56,1024
s_add_u32 s56,s55,512
D 17,56,2048
s_add_u32 s56,s55,768
D 17,56,3072
s_add_u32 s58,s58,s61
s_add_u32 m0,s59,2048
s_nop 0
buffer_load_dword v8,s[36:39],s58 offen offset:0 sc1 lds
v_cndmask_b32 v17,v10,v9,s[40:41]
s_add_u32 m0,s57,16384
s_add_u32 s56,s55,20480
D 17,56,0
s_add_u32 s56,s55,20736
D 17,56,1024
s_add_u32 s56,s55,20992
D 17,56,2048
s_add_u32 s56,s55,21248
D 17,56,3072
s_add_u32 s55,s55,s60
s_sub_u32 s57,s84,s57
s_add_u32 s54,s54,s46
s_and_b32 s79,s54,127
s_cmp_eq_u32 s79,0
s_cselect_b32 s94,0x3300,0
s_cmp_eq_u32 s79,127
s_cselect_b32 s94,0xcc00,s94
s_waitcnt vmcnt(4)
s_barrier
buffer_store_dword v4,v3,s[36:39],s70 offen offset:0 sc0 sc1
s_min_u32 s71,s71,s72
buffer_store_dword v4,v3,s[36:39],s71 offen offset:0 sc0 sc1
s_mov_b32 s70,0x7fff0000
s_mov_b32 s71,0x7fff0000
s_mov_b32 s72,0x7fff0000
s_cmp_eq_u32 s80,1
buffer_store_dword v4,v3,s[36:39],s71 offen offset:0 sc0 sc1
s_cbranch_scc1 Lg2_st1_%=
s_branch Lg2_st0_%=
Lg2_gox_%=:
s_add_u32 m0,s59,2048
s_nop 0
buffer_load_dword v8,s[36:39],s58 offen offset:0 sc1 lds
s_and_b32 s79,s54,127
s_cmp_eq_u32 s79,0
s_cselect_b32 s94,0x3300,0
s_cmp_eq_u32 s79,127
s_cselect_b32 s94,0xcc00,s94
s_waitcnt vmcnt(0)
s_barrier
buffer_store_dword v4,v3,s[36:39],s70 offen offset:0 sc0 sc1
s_min_u32 s71,s71,s72
buffer_store_dword v4,v3,s[36:39],s71 offen offset:0 sc0 sc1
s_mov_b32 s70,0x7fff0000
s_mov_b32 s71,0x7fff0000
s_mov_b32 s72,0x7fff0000
s_cmp_eq_u32 s80,1
s_cbranch_scc1 Lg2_st1_%=
Lg2_st0_%=:
ds_read_b32 v7,v6 offset:2048
R 34,1,0
R 38,1,1024
R 42,1,2048
R 46,1,3072
R 50,1,4096
L 5
v_cmp_eq_u32 vcc,s53,v7
s_nop 0
s_or_b64 vcc,vcc,s[94:95]
L 3
s_cmp_eq_u32 vcc_lo,-1
s_cbranch_scc1 Lg2_F0_%=
s_branch Lg2_L0_%=
Lg2_st1_%=:
ds_read_b32 v7,v6 offset:2048
R 34,2,0
R 38,2,1024
R 42,2,2048
R 46,2,3072
R 50,2,4096
L 5
v_cmp_eq_u32 vcc,s53,v7
s_nop 0
s_or_b64 vcc,vcc,s[94:95]
L 3
s_cmp_eq_u32 vcc_lo,-1
s_cbranch_scc1 Lg2_F1_%=
s_branch Lg2_L1_%=
Lg2_F0_%=:
v_mfma_f32_16x16x32_bf16 v[22:25],a[4:7],v[34:37],0
s_add_u32 s58,s58,s61
s_add_u32 m0,s59,0
s_and_b32 s78,s54,127
R 54,1,5120
M 22,a[12:15],38
s_cmp_eq_u32 s78,s85
s_cselect_b64 s[40:41],-1,0
s_add_u32 s56,s55,0
buffer_load_dword v8,s[36:39],s58 offen offset:0 sc1 lds
v_mfma_f32_16x16x32_bf16 v[18:21],a[0:3],v[34:37],0
s_add_u32 m0,s57,0
R 58,1,6144
v_cndmask_b32 v17,v15,v16,s[40:41]
M 18,a[8:11],38
L 3
D 17,56,0
M 18,a[16:19],42
s_add_u32 s56,s55,256
R 62,1,7168
v_lshlrev_b32 v106,16,v126
M 18,a[24:27],46
s_add_u32 s54,s54,s46
v_and_b32 v107,0xffff0000,v126
v_lshlrev_b32 v108,16,v127
M 22,a[20:23],42
s_and_b32 s79,s54,127
R 66,1,8192
v_and_b32 v109,0xffff0000,v127
M 22,a[28:31],46
L 3
D 17,56,1024
M 22,a[36:39],50
s_add_u32 s56,s55,512
R 70,1,9216
v_lshlrev_b32 v110,16,v128
M 22,a[44:47],54
s_cmp_eq_u32 s79,0
v_and_b32 v111,0xffff0000,v128
v_lshlrev_b32 v112,16,v129
M 18,a[32:35],50
s_cselect_b32 s94,0x3300,0
R 74,1,10240
v_and_b32 v113,0xffff0000,v129
M 18,a[40:43],54
L 3
D 17,56,2048
M 18,a[48:51],58
s_add_u32 s56,s55,768
R 78,1,11264
v_fma_f32 v106,v106,v103,v102
M 18,a[56:59],62
D 17,56,3072
M 22,a[52:55],58
s_add_u32 m0,s57,16384
R 82,1,12288
v_fma_f32 v107,v107,v103,v102
M 22,a[60:63],62
L 3
v_fma_f32 v108,v108,v103,v102
v_fma_f32 v109,v109,v103,v102
M 22,a[68:71],66
s_add_u32 s56,s55,20480
R 86,1,13312
v_fma_f32 v110,v110,v103,v102
M 22,a[76:79],70
buffer_load_dwordx4 v[122:125],v0,s[36:39],s74 offen offset:0 sc1
M 18,a[64:67],66
s_cmp_eq_u32 s79,127
R 90,1,14336
v_fma_f32 v111,v111,v103,v102
M 18,a[72:75],70
L 3
buffer_load_dwordx2 v[98:99],v11,s[36:39],s73 offen offset:256 sc1
M 18,a[80:83],74
s_waitcnt vmcnt(10)
R 94,1,15360
v_fma_f32 v112,v112,v103,v102
M 18,a[88:91],78
s_barrier
v_fma_f32 v113,v113,v103,v102
v_add_f32 v26,v26,v106
M 22,a[84:87],74
s_cselect_b32 s94,0xcc00,s94
R 34,1,16384
v_add_f32 v27,v27,v107
M 22,a[92:95],78
L 3
v_add_f32 v28,v28,v108
v_add_f32 v29,v29,v109
M 22,a[100:103],82
s_mov_b32 s66,s65
R 38,1,17408
v_add_f32 v30,v30,v110
M 22,a[108:111],86
s_mov_b32 s76,s75
v_cndmask_b32 v17,v10,v9,s[40:41]
v_add_f32 v31,v31,v111
M 18,a[96:99],82
s_add_u32 s73,s73,s64
R 42,1,18432
v_add_f32 v32,v32,v112
M 18,a[104:107],86
L 3
D 17,56,0
M 18,a[112:115],90
s_add_u32 s56,s55,20736
R 46,1,19456
v_add_f32 v33,v33,v113
M 18,a[120:123],94
s_add_u32 s74,s74,s62
v_cvt_pk_bf16_f32 v114,v26,v27
)"
R"(v_cvt_pk_bf16_f32 v115,v28,v29
M 22,a[116:119],90
s_add_u32 s65,s65,s62
R 50,1,20480
v_fma_f32 v107,v26,v26,s87
M 22,a[124:127],94
L 3
v_add_f32 v106,v26,v27
v_fma_f32 v107,v27,v27,v107
M 22,a[132:135],34
s_add_u32 s75,s75,s63
R 54,1,21504
v_add_f32 v106,v106,v28
M 22,a[140:143],38
D 17,56,1024
M 18,a[128:131],34
s_add_u32 s56,s55,20992
R 58,1,22528
v_fma_f32 v107,v28,v28,v107
M 18,a[136:139],38
L 3
v_add_f32 v106,v106,v29
v_fma_f32 v107,v29,v29,v107
M 18,a[144:147],42
s_sub_u32 s57,s84,s57
R 62,1,23552
v_add_f32 v106,v106,v30
M 18,a[152:155],46
v_fma_f32 v107,v30,v30,v107
v_add_f32 v106,v106,v31
M 22,a[148:151],42
R 66,1,24576
v_fma_f32 v107,v31,v31,v107
M 22,a[156:159],46
L 3
v_add_f32 v106,v106,v32
v_fma_f32 v107,v32,v32,v107
M 22,a[164:167],50
R 70,1,25600
v_add_f32 v106,v106,v33
M 22,a[172:175],54
v_fma_f32 v107,v33,v33,v107
v_cvt_pk_bf16_f32 v116,v30,v31
M 18,a[160:163],50
R 74,1,26624
v_cvt_pk_bf16_f32 v117,v32,v33
M 18,a[168:171],54
L 3
v_permlane16_swap_b32 v106,v107
v_add_f32 v106,v106,v107
v_mov_b32 v107,v106
M 18,a[176:179],58
R 78,1,27648
M 18,a[184:187],62
v_permlane32_swap_b32 v106,v107
v_add_f32 v106,v106,v107
M 22,a[180:183],58
R 82,1,28672
ds_write_b32 v12,v106 offset:2048
M 22,a[188:191],62
L 4
D 17,56,2048
M 22,a[196:199],66
s_add_u32 s56,s55,21248
s_add_u32 s55,s55,s60
R 86,1,29696
M 22,a[204:207],70
D 17,56,3072
M 18,a[192:195],66
R 90,1,30720
M 18,a[200:203],70
L 4
L 2
buffer_store_dwordx4 v[114:117],v0,s[36:39],s67 offen offset:0 sc0 sc1
M 18,a[208:211],74
s_waitcnt vmcnt(7)
R 94,1,31744
M 18,a[216:219],78
s_barrier
ds_read_b32 v7,v6 offset:0
R 118,13,2048
M 22,a[212:215],74
R 34,2,0
M 22,a[220:223],78
L 5
buffer_store_dword v4,v3,s[36:39],s70 offen offset:0 sc0 sc1
M 22,a[228:231],82
s_mov_b32 s70,s69
s_mov_b32 s69,s68
R 38,2,1024
M 22,a[236:239],86
s_add_u32 s68,s68,s63
M 18,a[224:227],82
R 42,2,2048
M 18,a[232:235],86
L 5
L 4
v_cmp_eq_u32 vcc,s53,v7
M 18,a[240:243],90
L 3
R 46,2,3072
v_add_f32 v118,v118,v119
M 18,a[248:251],94
s_or_b64 vcc,vcc,s[94:95]
v_add_f32 v120,v120,v121
v_add_f32 v118,v118,v120
M 22,a[244:247],90
s_cmp_eq_u32 vcc_lo,-1
R 50,2,4096
v_mul_f32 v118,s86,v118
M 22,a[252:255],94
s_waitcnt vmcnt(6) lgkmcnt(3)
buffer_store_dword v118,v14,s[36:39],s77 offen offset:0 sc0 sc1
s_cbranch_scc0 Lg2_K1_%=
Lg2_F1_%=:
v_mfma_f32_16x16x32_bf16 v[30:33],a[4:7],v[34:37],0
s_add_u32 s58,s58,s61
s_add_u32 m0,s59,1024
s_and_b32 s78,s54,127
R 54,2,5120
M 30,a[12:15],38
s_cmp_eq_u32 s78,s85
s_cselect_b64 s[40:41],-1,0
s_add_u32 s56,s55,0
buffer_load_dword v8,s[36:39],s58 offen offset:0 sc1 lds
v_mfma_f32_16x16x32_bf16 v[26:29],a[0:3],v[34:37],0
s_add_u32 m0,s57,0
R 58,2,6144
v_cndmask_b32 v17,v15,v16,s[40:41]
M 26,a[8:11],38
L 3
D 17,56,0
M 26,a[16:19],42
s_add_u32 s56,s55,256
R 62,2,7168
v_lshlrev_b32 v106,16,v122
M 26,a[24:27],46
s_add_u32 s54,s54,s46
v_and_b32 v107,0xffff0000,v122
v_lshlrev_b32 v108,16,v123
M 30,a[20:23],42
s_and_b32 s79,s54,127
R 66,2,8192
v_and_b32 v109,0xffff0000,v123
M 30,a[28:31],46
L 3
D 17,56,1024
M 30,a[36:39],50
s_add_u32 s56,s55,512
R 70,2,9216
v_lshlrev_b32 v110,16,v124
M 30,a[44:47],54
s_cmp_eq_u32 s79,0
v_and_b32 v111,0xffff0000,v124
v_lshlrev_b32 v112,16,v125
M 26,a[32:35],50
s_cselect_b32 s94,0x3300,0
R 74,2,10240
v_and_b32 v113,0xffff0000,v125
M 26,a[40:43],54
L 3
D 17,56,2048
M 26,a[48:51],58
s_add_u32 s56,s55,768
R 78,2,11264
v_fma_f32 v106,v106,v99,v98
M 26,a[56:59],62
D 17,56,3072
M 30,a[52:55],58
s_add_u32 m0,s57,16384
R 82,2,12288
v_fma_f32 v107,v107,v99,v98
M 30,a[60:63],62
L 3
v_fma_f32 v108,v108,v99,v98
v_fma_f32 v109,v109,v99,v98
M 30,a[68:71],66
s_add_u32 s56,s55,20480
R 86,2,13312
v_fma_f32 v110,v110,v99,v98
M 30,a[76:79],70
buffer_load_dwordx4 v[126:129],v0,s[36:39],s74 offen offset:0 sc1
M 26,a[64:67],66
s_cmp_eq_u32 s79,127
R 90,2,14336
v_fma_f32 v111,v111,v99,v98
M 26,a[72:75],70
L 3
buffer_load_dwordx2 v[102:103],v11,s[36:39],s73 offen offset:256 sc1
M 26,a[80:83],74
s_waitcnt vmcnt(10)
R 94,2,15360
v_fma_f32 v112,v112,v99,v98
M 26,a[88:91],78
s_barrier
v_fma_f32 v113,v113,v99,v98
v_add_f32 v18,v18,v106
M 30,a[84:87],74
s_cselect_b32 s94,0xcc00,s94
R 34,2,16384
v_add_f32 v19,v19,v107
M 30,a[92:95],78
L 3
v_add_f32 v20,v20,v108
v_add_f32 v21,v21,v109
M 30,a[100:103],82
s_mov_b32 s67,s65
R 38,2,17408
v_add_f32 v22,v22,v110
M 30,a[108:111],86
s_mov_b32 s77,s75
v_cndmask_b32 v17,v10,v9,s[40:41]
v_add_f32 v23,v23,v111
M 26,a[96:99],82
s_add_u32 s73,s73,s64
R 42,2,18432
v_add_f32 v24,v24,v112
M 26,a[104:107],86
L 3
D 17,56,0
M 26,a[112:115],90
s_add_u32 s56,s55,20736
R 46,2,19456
v_add_f32 v25,v25,v113
M 26,a[120:123],94
s_add_u32 s74,s74,s62
v_cvt_pk_bf16_f32 v114,v18,v19
v_cvt_pk_bf16_f32 v115,v20,v21
M 30,a[116:119],90
s_add_u32 s65,s65,s62
R 50,2,20480
v_fma_f32 v107,v18,v18,s87
M 30,a[124:127],94
L 3
v_add_f32 v106,v18,v19
v_fma_f32 v107,v19,v19,v107
M 30,a[132:135],34
s_add_u32 s75,s75,s63
R 54,2,21504
v_add_f32 v106,v106,v20
M 30,a[140:143],38
D 17,56,1024
M 26,a[128:131],34
s_add_u32 s56,s55,20992
R 58,2,22528
v_fma_f32 v107,v20,v20,v107
M 26,a[136:139],38
L 3
v_add_f32 v106,v106,v21
v_fma_f32 v107,v21,v21,v107
M 26,a[144:147],42
s_sub_u32 s57,s84,s57
R 62,2,23552
v_add_f32 v106,v106,v22
M 26,a[152:155],46
v_fma_f32 v107,v22,v22,v107
v_add_f32 v106,v106,v23
M 30,a[148:151],42
R 66,2,24576
v_fma_f32 v107,v23,v23,v107
M 30,a[156:159],46
L 3
v_add_f32 v106,v106,v24
v_fma_f32 v107,v24,v24,v107
M 30,a[164:167],50
R 70,2,25600
v_add_f32 v106,v106,v25
M 30,a[172:175],54
v_fma_f32 v107,v25,v25,v107
v_cvt_pk_bf16_f32 v116,v22,v23
M 26,a[160:163],50
R 74,2,26624
v_cvt_pk_bf16_f32 v117,v24,v25
M 26,a[168:171],54
L 3
v_permlane16_swap_b32 v106,v107
v_add_f32 v106,v106,v107
v_mov_b32 v107,v106
M 26,a[176:179],58
R 78,2,27648
M 26,a[184:187],62
v_permlane32_swap_b32 v106,v107
v_add_f32 v106,v106,v107
M 30,a[180:183],58
R 82,2,28672
ds_write_b32 v12,v106 offset:0
M 30,a[188:191],62
L 4
D 17,56,2048
M 30,a[196:199],66
s_add_u32 s56,s55,21248
s_add_u32 s55,s55,s60
R 86,2,29696
M 30,a[204:207],70
D 17,56,3072
M 26,a[192:195],66
R 90,2,30720
M 26,a[200:203],70
L 4
L 2
buffer_store_dwordx4 v[114:117],v0,s[36:39],s66 offen offset:0 sc0 sc1
M 26,a[208:211],74
s_waitcnt vmcnt(7)
R 94,2,31744
M 26,a[216:219],78
s_barrier
ds_read_b32 v7,v6 offset:1024
R 118,13,0
M 30,a[212:215],74
R 34,1,0
M 30,a[220:223],78
L 5
buffer_store_dword v4,v3,s[36:39],s70 offen offset:0 sc0 sc1
M 30,a[228:231],82
s_mov_b32 s70,s69
s_mov_b32 s69,s68
R 38,1,1024
M 30,a[236:239],86
s_add_u32 s68,s68,s63
M 26,a[224:227],82
R 42,1,2048
M 26,a[232:235],86
L 5
L 4
v_cmp_eq_u32 vcc,s53,v7
M 26,a[240:243],90
L 3
R 46,1,3072
v_add_f32 v118,v118,v119
M 26,a[248:251],94
s_or_b64 vcc,vcc,s[94:95]
v_add_f32 v120,v120,v121
v_add_f32 v118,v118,v120
M 30,a[244:247],90
s_cmp_eq_u32 vcc_lo,-1
R 50,1,4096
v_mul_f32 v118,s86,v118
M 30,a[252:255],94
s_waitcnt vmcnt(6) lgkmcnt(3)
buffer_store_dword v118,v14,s[36:39],s76 offen offset:0 sc0 sc1
s_cbranch_scc1 Lg2_F0_%=
Lg2_K0_%=:
Lg2_L0_%=:
v_mfma_f32_16x16x32_bf16 v[22:25],a[4:7],v[34:37],0
s_add_u32 m0,s59,3072
R 54,1,5120
v_lshlrev_b32 v106,16,v126
M 22,a[12:15],38
buffer_load_dword v8,s[36:39],s58 offen offset:0 sc1 lds
v_mfma_f32_16x16x32_bf16 v[18:21],a[0:3],v[34:37],0
s_mov_b32 s66,s65
R 58,1,6144
v_and_b32 v107,0xffff0000,v126
M 18,a[8:11],38
L 3
v_lshlrev_b32 v108,16,v127
v_and_b32 v109,0xffff0000,v127
M 18,a[16:19],42
s_mov_b32 s76,s75
R 62,1,7168
v_lshlrev_b32 v110,16,v128
M 18,a[24:27],46
s_add_u32 s65,s65,s62
v_and_b32 v111,0xffff0000,v128
v_lshlrev_b32 v112,16,v129
M 22,a[20:23],42
s_add_u32 s75,s75,s63
R 66,1,8192
v_and_b32 v113,0xffff0000,v129
v_fma_f32 v106,v106,v103,v102
M 22,a[28:31],46
L 3
v_fma_f32 v107,v107,v103,v102
v_fma_f32 v108,v108,v103,v102
M 22,a[36:39],50
R 70,1,9216
v_fma_f32 v109,v109,v103,v102
M 22,a[44:47],54
v_fma_f32 v110,v110,v103,v102
v_fma_f32 v111,v111,v103,v102
M 18,a[32:35],50
R 74,1,10240
v_fma_f32 v112,v112,v103,v102
M 18,a[40:43],54
L 3
v_fma_f32 v113,v113,v103,v102
v_add_f32 v26,v26,v106
M 18,a[48:51],58
R 78,1,11264
v_add_f32 v27,v27,v107
M 18,a[56:59],62
v_add_f32 v28,v28,v108
v_add_f32 v29,v29,v109
v_add_f32 v30,v30,v110
M 22,a[52:55],58
R 82,1,12288
v_add_f32 v31,v31,v111
M 22,a[60:63],62
L 3
v_add_f32 v32,v32,v112
v_add_f32 v33,v33,v113
M 22,a[68:71],66
R 86,1,13312
v_cvt_pk_bf16_f32 v114,v26,v27
M 22,a[76:79],70
buffer_load_dwordx4 v[122:125],v0,s[36:39],s74 offen offset:0 sc1
M 18,a[64:67],66
s_add_u32 s74,s74,s62
R 90,1,14336
v_cvt_pk_bf16_f32 v115,v28,v29
M 18,a[72:75],70
L 3
buffer_load_dwordx2 v[98:99],v11,s[36:39],s73 offen offset:256 sc1
v_cvt_pk_bf16_f32 v116,v30,v31
M 18,a[80:83],74
s_waitcnt vmcnt(2)
s_add_u32 s73,s73,s64
R 94,1,15360
M 18,a[88:91],78
s_barrier
ds_read_b32 v7,v6 offset:3072
v_cvt_pk_bf16_f32 v117,v32,v33
M 22,a[84:87],74
R 34,1,16384
v_add_f32 v106,v26,v27
M 22,a[92:95],78
L 4
buffer_store_dword v4,v3,s[36:39],s70 offen offset:0 sc0 sc1
M 22,a[100:103],82
s_mov_b32 s70,0x7fff0000
R 38,1,17408
v_fma_f32 v107,v26,v26,s87
M 22,a[108:111],86
buffer_store_dwordx4 v[114:117],v0,s[36:39],s67 offen offset:0 sc0 sc1
v_fma_f32 v107,v27,v27,v107
M 18,a[96:99],82
R 42,1,18432
v_add_f32 v106,v106,v28
v_fma_f32 v107,v28,v28,v107
M 18,a[104:107],86
L 4
L 3
v_cmp_eq_u32 vcc,s53,v7
M 18,a[112:115],90
R 46,1,19456
v_add_f32 v106,v106,v29
M 18,a[120:123],94
s_or_b64 vcc,vcc,s[94:95]
v_fma_f32 v107,v29,v29,v107
v_add_f32 v106,v106,v30
M 22,a[116:119],90
R 50,1,20480
v_fma_f32 v107,v30,v30,v107
M 22,a[124:127],94
s_cmp_eq_u32 vcc_lo,-1
L 3
v_add_f32 v106,v106,v31
s_cbranch_scc0 Lg2_X0_%=
M 22,a[132:135],34
s_and_b32 s78,s54,127
s_cmp_eq_u32 s78,s85
s_cselect_b64 s[40:41],-1,0
R 54,1,21504
M 22,a[140:143],38
s_add_u32 m0,s57,0
s_add_u32 s56,s55,0
s_add_u32 s58,s58,s61
v_cndmask_b32 v17,v15,v16,s[40:41]
M 18,a[128:131],34
s_add_u32 s54,s54,s46
R 58,1,22528
v_fma_f32 v107,v31,v31,v107
M 18,a[136:139],38
L 3
D 17,56,0
M 18,a[144:147],42
s_add_u32 s56,s55,256
R 62,1,23552
v_add_f32 v106,v106,v32
M 18,a[152:155],46
s_and_b32 s79,s54,127
v_fma_f32 v107,v32,v32,v107
v_add_f32 v106,v106,v33
M 22,a[148:151],42
s_cmp_eq_u32 s79,0
R 66,1,24576
v_fma_f32 v107,v33,v33,v107
M 22,a[156:159],46
L 3
D 17,56,1024
M 22,a[164:167],50
s_cselect_b32 s94,0x3300,0
s_add_u32 s56,s55,512
R 70,1,25600
M 22,a[172:175],54
s_cmp_eq_u32 s79,127
v_permlane16_swap_b32 v106,v107
v_add_f32 v106,v106,v107
M 18,a[160:163],50
s_cselect_b32 s94,0xcc00,s94
)"
R"(R 74,1,26624
v_mov_b32 v107,v106
M 18,a[168:171],54
L 3
D 17,56,2048
M 18,a[176:179],58
s_add_u32 s56,s55,768
R 78,1,27648
M 18,a[184:187],62
v_permlane32_swap_b32 v106,v107
v_add_f32 v106,v106,v107
M 22,a[180:183],58
R 82,1,28672
ds_write_b32 v12,v106 offset:2048
M 22,a[188:191],62
L 4
D 17,56,3072
M 22,a[196:199],66
s_add_u32 m0,s59,0
s_add_u32 s56,s55,20480
R 86,1,29696
M 22,a[204:207],70
buffer_load_dword v8,s[36:39],s58 offen offset:0 sc1 lds
M 18,a[192:195],66
s_add_u32 m0,s57,16384
R 90,1,30720
v_cndmask_b32 v17,v10,v9,s[40:41]
M 18,a[200:203],70
L 4
L 2
s_sub_u32 s57,s84,s57
M 18,a[208:211],74
s_waitcnt vmcnt(0)
R 94,1,31744
M 18,a[216:219],78
s_barrier
D 17,56,0
M 22,a[212:215],74
s_add_u32 s56,s55,20736
R 34,2,0
ds_read_b32 v7,v6 offset:0
M 22,a[220:223],78
L 4
buffer_store_dword v4,v3,s[36:39],s70 offen offset:0 sc0 sc1
M 22,a[228:231],82
s_mov_b32 s70,s69
R 38,2,1024
R 118,13,2048
M 22,a[236:239],86
D 17,56,1024
M 18,a[224:227],82
s_add_u32 s56,s55,20992
s_mov_b32 s69,s68
R 42,2,2048
M 18,a[232:235],86
L 5
D 17,56,2048
M 18,a[240:243],90
L 3
s_add_u32 s56,s55,21248
R 46,2,3072
v_cmp_eq_u32 vcc,s53,v7
M 18,a[248:251],94
D 17,56,3072
M 22,a[244:247],90
s_add_u32 s55,s55,s60
s_add_u32 s68,s68,s63
L 2
s_or_b64 vcc,vcc,s[94:95]
s_cmp_eq_u32 vcc_lo,-1
R 50,2,4096
v_add_f32 v118,v118,v119
v_add_f32 v120,v120,v121
v_add_f32 v118,v118,v120
v_mul_f32 v118,s86,v118
M 22,a[252:255],94
s_nop 0
buffer_store_dword v118,v14,s[36:39],s77 offen offset:0 sc0 sc1
buffer_store_dword v4,v3,s[36:39],s71 offen offset:0 sc0 sc1
buffer_store_dword v4,v3,s[36:39],s71 offen offset:0 sc0 sc1
s_cbranch_scc1 Lg2_F1_%=
s_branch Lg2_K1_%=
Lg2_X0_%=:
M 22,a[132:135],34
s_add_u32 m0,s59,0
R 54,1,21504
v_fma_f32 v107,v31,v31,v107
M 22,a[140:143],38
buffer_load_dword v8,s[36:39],s58 offen offset:0 sc1 lds
M 18,a[128:131],34
R 58,1,22528
v_add_f32 v106,v106,v32
M 18,a[136:139],38
L 3
v_fma_f32 v107,v32,v32,v107
v_add_f32 v106,v106,v33
M 18,a[144:147],42
R 62,1,23552
v_fma_f32 v107,v33,v33,v107
M 18,a[152:155],46
M 22,a[148:151],42
R 66,1,24576
M 22,a[156:159],46
L 3
v_permlane16_swap_b32 v106,v107
v_add_f32 v106,v106,v107
M 22,a[164:167],50
R 70,1,25600
v_mov_b32 v107,v106
M 22,a[172:175],54
M 18,a[160:163],50
R 74,1,26624
M 18,a[168:171],54
L 3
v_permlane32_swap_b32 v106,v107
M 18,a[176:179],58
R 78,1,27648
v_add_f32 v106,v106,v107
M 18,a[184:187],62
ds_write_b32 v12,v106 offset:2048
M 22,a[180:183],58
R 82,1,28672
M 22,a[188:191],62
L 4
M 22,a[196:199],66
R 86,1,29696
M 22,a[204:207],70
M 18,a[192:195],66
R 90,1,30720
M 18,a[200:203],70
L 4
L 3
M 18,a[208:211],74
s_waitcnt vmcnt(0)
R 94,1,31744
M 18,a[216:219],78
s_barrier
R 118,13,2048
ds_read_b32 v7,v6 offset:0
M 22,a[212:215],74
buffer_store_dword v4,v3,s[36:39],s70 offen offset:0 sc0 sc1
M 22,a[220:223],78
L 4
s_mov_b32 s70,s69
s_mov_b32 s69,s68
M 22,a[228:231],82
s_add_u32 s68,s68,s63
M 22,a[236:239],86
M 18,a[224:227],82
M 18,a[232:235],86
L 2
L 1
L 0
M 18,a[240:243],90
v_add_f32 v118,v118,v119
v_add_f32 v120,v120,v121
v_add_f32 v118,v118,v120
M 18,a[248:251],94
v_cmp_eq_u32 vcc,s53,v7
v_mul_f32 v118,s86,v118
M 22,a[244:247],90
buffer_store_dword v118,v14,s[36:39],s77 offen offset:0 sc0 sc1
M 22,a[252:255],94
s_nop 0
s_or_b64 vcc,vcc,s[94:95]
s_mov_b32 s80,1
s_cmp_eq_u32 vcc_lo,-1
s_cbranch_scc0 Lg2_xn0_%=
s_and_b32 s78,s54,127
s_cmp_eq_u32 s78,s85
s_cselect_b64 s[40:41],-1,0
v_cndmask_b32 v17,v15,v16,s[40:41]
s_add_u32 m0,s57,0
s_add_u32 s56,s55,0
D 17,56,0
v_lshlrev_b32 v106,16,v122
v_and_b32 v107,0xffff0000,v122
v_lshlrev_b32 v108,16,v123
v_and_b32 v109,0xffff0000,v123
v_lshlrev_b32 v110,16,v124
s_add_u32 s56,s55,256
D 17,56,1024
v_and_b32 v111,0xffff0000,v124
v_lshlrev_b32 v112,16,v125
v_and_b32 v113,0xffff0000,v125
v_fma_f32 v106,v106,v99,v98
v_fma_f32 v107,v107,v99,v98
s_add_u32 s56,s55,512
D 17,56,2048
v_fma_f32 v108,v108,v99,v98
v_fma_f32 v109,v109,v99,v98
v_fma_f32 v110,v110,v99,v98
v_fma_f32 v111,v111,v99,v98
v_fma_f32 v112,v112,v99,v98
s_add_u32 s56,s55,768
D 17,56,3072
v_fma_f32 v113,v113,v99,v98
v_add_f32 v18,v18,v106
v_add_f32 v19,v19,v107
v_add_f32 v20,v20,v108
v_add_f32 v21,v21,v109
s_add_u32 s58,s58,s61
v_cndmask_b32 v17,v10,v9,s[40:41]
s_add_u32 m0,s57,16384
s_add_u32 s56,s55,20480
D 17,56,0
v_add_f32 v22,v22,v110
v_add_f32 v23,v23,v111
v_add_f32 v24,v24,v112
v_add_f32 v25,v25,v113
v_cvt_pk_bf16_f32 v114,v18,v19
s_add_u32 s56,s55,20736
D 17,56,1024
v_cvt_pk_bf16_f32 v115,v20,v21
v_cvt_pk_bf16_f32 v116,v22,v23
v_cvt_pk_bf16_f32 v117,v24,v25
s_add_u32 s56,s55,20992
D 17,56,2048
s_add_u32 s56,s55,21248
D 17,56,3072
buffer_store_dwordx4 v[114:117],v0,s[36:39],s66 offen offset:0 sc0 sc1
v_add_f32 v106,v18,v19
v_fma_f32 v107,v18,v18,s87
s_add_u32 s55,s55,s60
s_sub_u32 s57,s84,s57
s_add_u32 s54,s54,s46
v_fma_f32 v107,v19,v19,v107
v_add_f32 v106,v106,v20
v_fma_f32 v107,v20,v20,v107
v_add_f32 v106,v106,v21
v_fma_f32 v107,v21,v21,v107
v_add_f32 v106,v106,v22
v_fma_f32 v107,v22,v22,v107
v_add_f32 v106,v106,v23
v_fma_f32 v107,v23,v23,v107
v_add_f32 v106,v106,v24
v_fma_f32 v107,v24,v24,v107
v_add_f32 v106,v106,v25
v_fma_f32 v107,v25,v25,v107
s_nop 1
v_permlane16_swap_b32 v106,v107
v_add_f32 v106,v106,v107
v_mov_b32 v107,v106
s_nop 1
v_permlane32_swap_b32 v106,v107
v_add_f32 v106,v106,v107
ds_write_b32 v12,v106 offset:0
L 0
s_barrier
R 118,13,0
L 0
v_add_f32 v118,v118,v119
v_add_f32 v120,v120,v121
v_add_f32 v118,v118,v120
v_mul_f32 v118,s86,v118
buffer_store_dword v118,v14,s[36:39],s76 offen offset:0 sc0 sc1
s_mov_b32 s72,s69
s_mov_b32 s69,0x7fff0000
s_mov_b32 s66,0x7fff0000
s_mov_b32 s76,0x7fff0000
s_branch Lg2_gox_%=
Lg2_xn0_%=:
s_cmp_ge_u32 s54,256
s_cbranch_scc1 Lg2_fin0_%=
s_add_u32 m0,s59,4096
s_nop 0
buffer_load_dword v8,s[36:39],s58 offen offset:0 sc1 lds
buffer_store_dword v4,v3,s[36:39],s71 offen offset:0 sc0 sc1
s_add_u32 m0,s59,5120
s_nop 0
buffer_load_dword v8,s[36:39],s58 offen offset:0 sc1 lds
buffer_store_dword v4,v3,s[36:39],s71 offen offset:0 sc0 sc1
s_mov_b32 s83,0
v_lshlrev_b32 v106,16,v122
v_and_b32 v107,0xffff0000,v122
v_lshlrev_b32 v108,16,v123
v_and_b32 v109,0xffff0000,v123
v_lshlrev_b32 v110,16,v124
v_and_b32 v111,0xffff0000,v124
v_lshlrev_b32 v112,16,v125
v_and_b32 v113,0xffff0000,v125
v_fma_f32 v106,v106,v99,v98
v_fma_f32 v107,v107,v99,v98
v_fma_f32 v108,v108,v99,v98
v_fma_f32 v109,v109,v99,v98
v_fma_f32 v110,v110,v99,v98
v_fma_f32 v111,v111,v99,v98
v_fma_f32 v112,v112,v99,v98
v_fma_f32 v113,v113,v99,v98
v_add_f32 v18,v18,v106
v_add_f32 v19,v19,v107
v_add_f32 v20,v20,v108
v_add_f32 v21,v21,v109
v_add_f32 v22,v22,v110
v_add_f32 v23,v23,v111
v_add_f32 v24,v24,v112
v_add_f32 v25,v25,v113
v_cvt_pk_bf16_f32 v114,v18,v19
v_cvt_pk_bf16_f32 v115,v20,v21
v_cvt_pk_bf16_f32 v116,v22,v23
v_cvt_pk_bf16_f32 v117,v24,v25
buffer_store_dwordx4 v[114:117],v0,s[36:39],s66 offen offset:0 sc0 sc1
v_add_f32 v106,v18,v19
v_fma_f32 v107,v18,v18,s87
v_fma_f32 v107,v19,v19,v107
v_add_f32 v106,v106,v20
v_fma_f32 v107,v20,v20,v107
v_add_f32 v106,v106,v21
v_fma_f32 v107,v21,v21,v107
v_add_f32 v106,v106,v22
v_fma_f32 v107,v22,v22,v107
v_add_f32 v106,v106,v23
v_fma_f32 v107,v23,v23,v107
v_add_f32 v106,v106,v24
v_fma_f32 v107,v24,v24,v107
v_add_f32 v106,v106,v25
v_fma_f32 v107,v25,v25,v107
s_nop 1
v_permlane16_swap_b32 v106,v107
v_add_f32 v106,v106,v107
v_mov_b32 v107,v106
s_nop 1
v_permlane32_swap_b32 v106,v107
v_add_f32 v106,v106,v107
ds_write_b32 v12,v106 offset:0
L 0
s_barrier
R 118,13,0
L 0
v_add_f32 v118,v118,v119
v_add_f32 v120,v120,v121
v_add_f32 v118,v118,v120
v_mul_f32 v118,s86,v118
buffer_store_dword v118,v14,s[36:39],s76 offen offset:0 sc0 sc1
s_mov_b32 s72,s69
s_mov_b32 s69,0x7fff0000
s_mov_b32 s66,0x7fff0000
s_mov_b32 s76,0x7fff0000
s_branch Lg2_poll_%=
Lg2_fin0_%=:
v_lshlrev_b32 v106,16,v122
v_and_b32 v107,0xffff0000,v122
v_lshlrev_b32 v108,16,v123
v_and_b32 v109,0xffff0000,v123
v_lshlrev_b32 v110,16,v124
v_and_b32 v111,0xffff0000,v124
v_lshlrev_b32 v112,16,v125
v_and_b32 v113,0xffff0000,v125
v_fma_f32 v106,v106,v99,v98
v_fma_f32 v107,v107,v99,v98
v_fma_f32 v108,v108,v99,v98
v_fma_f32 v109,v109,v99,v98
v_fma_f32 v110,v110,v99,v98
v_fma_f32 v111,v111,v99,v98
v_fma_f32 v112,v112,v99,v98
v_fma_f32 v113,v113,v99,v98
v_add_f32 v18,v18,v106
v_add_f32 v19,v19,v107
v_add_f32 v20,v20,v108
v_add_f32 v21,v21,v109
v_add_f32 v22,v22,v110
v_add_f32 v23,v23,v111
v_add_f32 v24,v24,v112
v_add_f32 v25,v25,v113
v_cvt_pk_bf16_f32 v114,v18,v19
v_cvt_pk_bf16_f32 v115,v20,v21
v_cvt_pk_bf16_f32 v116,v22,v23
v_cvt_pk_bf16_f32 v117,v24,v25
buffer_store_dwordx4 v[114:117],v0,s[36:39],s66 offen offset:0 sc0 sc1
v_add_f32 v106,v18,v19
v_fma_f32 v107,v18,v18,s87
v_fma_f32 v107,v19,v19,v107
v_add_f32 v106,v106,v20
v_fma_f32 v107,v20,v20,v107
v_add_f32 v106,v106,v21
v_fma_f32 v107,v21,v21,v107
v_add_f32 v106,v106,v22
v_fma_f32 v107,v22,v22,v107
v_add_f32 v106,v106,v23
v_fma_f32 v107,v23,v23,v107
v_add_f32 v106,v106,v24
v_fma_f32 v107,v24,v24,v107
v_add_f32 v106,v106,v25
v_fma_f32 v107,v25,v25,v107
s_nop 1
v_permlane16_swap_b32 v106,v107
v_add_f32 v106,v106,v107
v_mov_b32 v107,v106
s_nop 1
v_permlane32_swap_b32 v106,v107
v_add_f32 v106,v106,v107
ds_write_b32 v12,v106 offset:0
L 0
s_barrier
R 118,13,0
L 0
v_add_f32 v118,v118,v119
v_add_f32 v120,v120,v121
v_add_f32 v118,v118,v120
v_mul_f32 v118,s86,v118
buffer_store_dword v118,v14,s[36:39],s76 offen offset:0 sc0 sc1
s_mov_b32 s72,s69
s_mov_b32 s69,0x7fff0000
s_mov_b32 s66,0x7fff0000
s_mov_b32 s76,0x7fff0000
s_branch Lg2_done_%=
Lg2_K1_%=:
Lg2_L1_%=:
v_mfma_f32_16x16x32_bf16 v[30:33],a[4:7],v[34:37],0
s_add_u32 m0,s59,3072
R 54,2,5120
v_lshlrev_b32 v106,16,v122
M 30,a[12:15],38
buffer_load_dword v8,s[36:39],s58 offen offset:0 sc1 lds
v_mfma_f32_16x16x32_bf16 v[26:29],a[0:3],v[34:37],0
s_mov_b32 s67,s65
R 58,2,6144
v_and_b32 v107,0xffff0000,v122
M 26,a[8:11],38
L 3
v_lshlrev_b32 v108,16,v123
v_and_b32 v109,0xffff0000,v123
M 26,a[16:19],42
s_mov_b32 s77,s75
R 62,2,7168
v_lshlrev_b32 v110,16,v124
M 26,a[24:27],46
s_add_u32 s65,s65,s62
v_and_b32 v111,0xffff0000,v124
v_lshlrev_b32 v112,16,v125
M 30,a[20:23],42
s_add_u32 s75,s75,s63
R 66,2,8192
v_and_b32 v113,0xffff0000,v125
v_fma_f32 v106,v106,v99,v98
M 30,a[28:31],46
L 3
v_fma_f32 v107,v107,v99,v98
v_fma_f32 v108,v108,v99,v98
M 30,a[36:39],50
R 70,2,9216
v_fma_f32 v109,v109,v99,v98
M 30,a[44:47],54
v_fma_f32 v110,v110,v99,v98
v_fma_f32 v111,v111,v99,v98
M 26,a[32:35],50
R 74,2,10240
v_fma_f32 v112,v112,v99,v98
M 26,a[40:43],54
L 3
v_fma_f32 v113,v113,v99,v98
v_add_f32 v18,v18,v106
M 26,a[48:51],58
R 78,2,11264
v_add_f32 v19,v19,v107
M 26,a[56:59],62
v_add_f32 v20,v20,v108
v_add_f32 v21,v21,v109
v_add_f32 v22,v22,v110
M 30,a[52:55],58
R 82,2,12288
v_add_f32 v23,v23,v111
M 30,a[60:63],62
L 3
v_add_f32 v24,v24,v112
v_add_f32 v25,v25,v113
M 30,a[68:71],66
R 86,2,13312
v_cvt_pk_bf16_f32 v114,v18,v19
M 30,a[76:79],70
buffer_load_dwordx4 v[126:129],v0,s[36:39],s74 offen offset:0 sc1
M 26,a[64:67],66
s_add_u32 s74,s74,s62
R 90,2,14336
v_cvt_pk_bf16_f32 v115,v20,v21
M 26,a[72:75],70
L 3
buffer_load_dwordx2 v[102:103],v11,s[36:39],s73 offen offset:256 sc1
v_cvt_pk_bf16_f32 v116,v22,v23
M 26,a[80:83],74
s_waitcnt vmcnt(2)
s_add_u32 s73,s73,s64
R 94,2,15360
M 26,a[88:91],78
s_barrier
ds_read_b32 v7,v6 offset:3072
v_cvt_pk_bf16_f32 v117,v24,v25
M 30,a[84:87],74
R 34,2,16384
v_add_f32 v106,v18,v19
M 30,a[92:95],78
L 4
buffer_store_dword v4,v3,s[36:39],s70 offen offset:0 sc0 sc1
M 30,a[100:103],82
s_mov_b32 s70,0x7fff0000
R 38,2,17408
v_fma_f32 v107,v18,v18,s87
M 30,a[108:111],86
buffer_store_dwordx4 v[114:117],v0,s[36:39],s66 offen offset:0 sc0 sc1
v_fma_f32 v107,v19,v19,v107
)"
R"(M 26,a[96:99],82
R 42,2,18432
v_add_f32 v106,v106,v20
v_fma_f32 v107,v20,v20,v107
M 26,a[104:107],86
L 4
L 3
v_cmp_eq_u32 vcc,s53,v7
M 26,a[112:115],90
R 46,2,19456
v_add_f32 v106,v106,v21
M 26,a[120:123],94
s_or_b64 vcc,vcc,s[94:95]
v_fma_f32 v107,v21,v21,v107
v_add_f32 v106,v106,v22
M 30,a[116:119],90
R 50,2,20480
v_fma_f32 v107,v22,v22,v107
M 30,a[124:127],94
s_cmp_eq_u32 vcc_lo,-1
L 3
v_add_f32 v106,v106,v23
s_cbranch_scc0 Lg2_X1_%=
M 30,a[132:135],34
s_and_b32 s78,s54,127
s_cmp_eq_u32 s78,s85
s_cselect_b64 s[40:41],-1,0
R 54,2,21504
M 30,a[140:143],38
s_add_u32 m0,s57,0
s_add_u32 s56,s55,0
s_add_u32 s58,s58,s61
v_cndmask_b32 v17,v15,v16,s[40:41]
M 26,a[128:131],34
s_add_u32 s54,s54,s46
R 58,2,22528
v_fma_f32 v107,v23,v23,v107
M 26,a[136:139],38
L 3
D 17,56,0
M 26,a[144:147],42
s_add_u32 s56,s55,256
R 62,2,23552
v_add_f32 v106,v106,v24
M 26,a[152:155],46
s_and_b32 s79,s54,127
v_fma_f32 v107,v24,v24,v107
v_add_f32 v106,v106,v25
M 30,a[148:151],42
s_cmp_eq_u32 s79,0
R 66,2,24576
v_fma_f32 v107,v25,v25,v107
M 30,a[156:159],46
L 3
D 17,56,1024
M 30,a[164:167],50
s_cselect_b32 s94,0x3300,0
s_add_u32 s56,s55,512
R 70,2,25600
M 30,a[172:175],54
s_cmp_eq_u32 s79,127
v_permlane16_swap_b32 v106,v107
v_add_f32 v106,v106,v107
M 26,a[160:163],50
s_cselect_b32 s94,0xcc00,s94
R 74,2,26624
v_mov_b32 v107,v106
M 26,a[168:171],54
L 3
D 17,56,2048
M 26,a[176:179],58
s_add_u32 s56,s55,768
R 78,2,27648
M 26,a[184:187],62
v_permlane32_swap_b32 v106,v107
v_add_f32 v106,v106,v107
M 30,a[180:183],58
R 82,2,28672
ds_write_b32 v12,v106 offset:0
M 30,a[188:191],62
L 4
D 17,56,3072
M 30,a[196:199],66
s_add_u32 m0,s59,1024
s_add_u32 s56,s55,20480
R 86,2,29696
M 30,a[204:207],70
buffer_load_dword v8,s[36:39],s58 offen offset:0 sc1 lds
M 26,a[192:195],66
s_add_u32 m0,s57,16384
R 90,2,30720
v_cndmask_b32 v17,v10,v9,s[40:41]
M 26,a[200:203],70
L 4
L 2
s_sub_u32 s57,s84,s57
M 26,a[208:211],74
s_waitcnt vmcnt(0)
R 94,2,31744
M 26,a[216:219],78
s_barrier
D 17,56,0
M 30,a[212:215],74
s_add_u32 s56,s55,20736
R 34,1,0
ds_read_b32 v7,v6 offset:1024
M 30,a[220:223],78
L 4
buffer_store_dword v4,v3,s[36:39],s70 offen offset:0 sc0 sc1
M 30,a[228:231],82
s_mov_b32 s70,s69
R 38,1,1024
R 118,13,0
M 30,a[236:239],86
D 17,56,1024
M 26,a[224:227],82
s_add_u32 s56,s55,20992
s_mov_b32 s69,s68
R 42,1,2048
M 26,a[232:235],86
L 5
D 17,56,2048
M 26,a[240:243],90
L 3
s_add_u32 s56,s55,21248
R 46,1,3072
v_cmp_eq_u32 vcc,s53,v7
M 26,a[248:251],94
D 17,56,3072
M 30,a[244:247],90
s_add_u32 s55,s55,s60
s_add_u32 s68,s68,s63
L 2
s_or_b64 vcc,vcc,s[94:95]
s_cmp_eq_u32 vcc_lo,-1
R 50,1,4096
v_add_f32 v118,v118,v119
v_add_f32 v120,v120,v121
v_add_f32 v118,v118,v120
v_mul_f32 v118,s86,v118
M 30,a[252:255],94
s_nop 0
buffer_store_dword v118,v14,s[36:39],s76 offen offset:0 sc0 sc1
buffer_store_dword v4,v3,s[36:39],s71 offen offset:0 sc0 sc1
buffer_store_dword v4,v3,s[36:39],s71 offen offset:0 sc0 sc1
s_cbranch_scc1 Lg2_F0_%=
s_branch Lg2_K0_%=
Lg2_X1_%=:
M 30,a[132:135],34
s_add_u32 m0,s59,1024
R 54,2,21504
v_fma_f32 v107,v23,v23,v107
M 30,a[140:143],38
buffer_load_dword v8,s[36:39],s58 offen offset:0 sc1 lds
M 26,a[128:131],34
R 58,2,22528
v_add_f32 v106,v106,v24
M 26,a[136:139],38
L 3
v_fma_f32 v107,v24,v24,v107
v_add_f32 v106,v106,v25
M 26,a[144:147],42
R 62,2,23552
v_fma_f32 v107,v25,v25,v107
M 26,a[152:155],46
M 30,a[148:151],42
R 66,2,24576
M 30,a[156:159],46
L 3
v_permlane16_swap_b32 v106,v107
v_add_f32 v106,v106,v107
M 30,a[164:167],50
R 70,2,25600
v_mov_b32 v107,v106
M 30,a[172:175],54
M 26,a[160:163],50
R 74,2,26624
M 26,a[168:171],54
L 3
v_permlane32_swap_b32 v106,v107
M 26,a[176:179],58
R 78,2,27648
v_add_f32 v106,v106,v107
M 26,a[184:187],62
ds_write_b32 v12,v106 offset:0
M 30,a[180:183],58
R 82,2,28672
M 30,a[188:191],62
L 4
M 30,a[196:199],66
R 86,2,29696
M 30,a[204:207],70
M 26,a[192:195],66
R 90,2,30720
M 26,a[200:203],70
L 4
L 3
M 26,a[208:211],74
s_waitcnt vmcnt(0)
R 94,2,31744
M 26,a[216:219],78
s_barrier
R 118,13,0
ds_read_b32 v7,v6 offset:1024
M 30,a[212:215],74
buffer_store_dword v4,v3,s[36:39],s70 offen offset:0 sc0 sc1
M 30,a[220:223],78
L 4
s_mov_b32 s70,s69
s_mov_b32 s69,s68
M 30,a[228:231],82
s_add_u32 s68,s68,s63
M 30,a[236:239],86
M 26,a[224:227],82
M 26,a[232:235],86
L 2
L 1
L 0
M 26,a[240:243],90
v_add_f32 v118,v118,v119
v_add_f32 v120,v120,v121
v_add_f32 v118,v118,v120
M 26,a[248:251],94
v_cmp_eq_u32 vcc,s53,v7
v_mul_f32 v118,s86,v118
M 30,a[244:247],90
buffer_store_dword v118,v14,s[36:39],s76 offen offset:0 sc0 sc1
M 30,a[252:255],94
s_nop 0
s_or_b64 vcc,vcc,s[94:95]
s_mov_b32 s80,0
s_cmp_eq_u32 vcc_lo,-1
s_cbranch_scc0 Lg2_xn1_%=
s_and_b32 s78,s54,127
s_cmp_eq_u32 s78,s85
s_cselect_b64 s[40:41],-1,0
v_cndmask_b32 v17,v15,v16,s[40:41]
s_add_u32 m0,s57,0
s_add_u32 s56,s55,0
D 17,56,0
v_lshlrev_b32 v106,16,v126
v_and_b32 v107,0xffff0000,v126
v_lshlrev_b32 v108,16,v127
v_and_b32 v109,0xffff0000,v127
v_lshlrev_b32 v110,16,v128
s_add_u32 s56,s55,256
D 17,56,1024
v_and_b32 v111,0xffff0000,v128
v_lshlrev_b32 v112,16,v129
v_and_b32 v113,0xffff0000,v129
v_fma_f32 v106,v106,v103,v102
v_fma_f32 v107,v107,v103,v102
s_add_u32 s56,s55,512
D 17,56,2048
v_fma_f32 v108,v108,v103,v102
v_fma_f32 v109,v109,v103,v102
v_fma_f32 v110,v110,v103,v102
v_fma_f32 v111,v111,v103,v102
v_fma_f32 v112,v112,v103,v102
s_add_u32 s56,s55,768
D 17,56,3072
v_fma_f32 v113,v113,v103,v102
v_add_f32 v26,v26,v106
v_add_f32 v27,v27,v107
v_add_f32 v28,v28,v108
v_add_f32 v29,v29,v109
s_add_u32 s58,s58,s61
v_cndmask_b32 v17,v10,v9,s[40:41]
s_add_u32 m0,s57,16384
s_add_u32 s56,s55,20480
D 17,56,0
v_add_f32 v30,v30,v110
v_add_f32 v31,v31,v111
v_add_f32 v32,v32,v112
v_add_f32 v33,v33,v113
v_cvt_pk_bf16_f32 v114,v26,v27
s_add_u32 s56,s55,20736
D 17,56,1024
v_cvt_pk_bf16_f32 v115,v28,v29
v_cvt_pk_bf16_f32 v116,v30,v31
v_cvt_pk_bf16_f32 v117,v32,v33
s_add_u32 s56,s55,20992
D 17,56,2048
s_add_u32 s56,s55,21248
D 17,56,3072
buffer_store_dwordx4 v[114:117],v0,s[36:39],s67 offen offset:0 sc0 sc1
v_add_f32 v106,v26,v27
v_fma_f32 v107,v26,v26,s87
s_add_u32 s55,s55,s60
s_sub_u32 s57,s84,s57
s_add_u32 s54,s54,s46
v_fma_f32 v107,v27,v27,v107
v_add_f32 v106,v106,v28
v_fma_f32 v107,v28,v28,v107
v_add_f32 v106,v106,v29
v_fma_f32 v107,v29,v29,v107
v_add_f32 v106,v106,v30
v_fma_f32 v107,v30,v30,v107
v_add_f32 v106,v106,v31
v_fma_f32 v107,v31,v31,v107
v_add_f32 v106,v106,v32
v_fma_f32 v107,v32,v32,v107
v_add_f32 v106,v106,v33
v_fma_f32 v107,v33,v33,v107
s_nop 1
v_permlane16_swap_b32 v106,v107
v_add_f32 v106,v106,v107
v_mov_b32 v107,v106
s_nop 1
v_permlane32_swap_b32 v106,v107
v_add_f32 v106,v106,v107
ds_write_b32 v12,v106 offset:2048
L 0
s_barrier
R 118,13,2048
L 0
v_add_f32 v118,v118,v119
v_add_f32 v120,v120,v121
v_add_f32 v118,v118,v120
v_mul_f32 v118,s86,v118
buffer_store_dword v118,v14,s[36:39],s77 offen offset:0 sc0 sc1
s_mov_b32 s72,s69
s_mov_b32 s69,0x7fff0000
s_mov_b32 s67,0x7fff0000
s_mov_b32 s77,0x7fff0000
s_branch Lg2_gox_%=
Lg2_xn1_%=:
s_cmp_ge_u32 s54,256
s_cbranch_scc1 Lg2_fin1_%=
s_add_u32 m0,s59,4096
s_nop 0
buffer_load_dword v8,s[36:39],s58 offen offset:0 sc1 lds
buffer_store_dword v4,v3,s[36:39],s71 offen offset:0 sc0 sc1
s_add_u32 m0,s59,5120
s_nop 0
buffer_load_dword v8,s[36:39],s58 offen offset:0 sc1 lds
buffer_store_dword v4,v3,s[36:39],s71 offen offset:0 sc0 sc1
s_mov_b32 s83,0
v_lshlrev_b32 v106,16,v126
v_and_b32 v107,0xffff0000,v126
v_lshlrev_b32 v108,16,v127
v_and_b32 v109,0xffff0000,v127
v_lshlrev_b32 v110,16,v128
v_and_b32 v111,0xffff0000,v128
v_lshlrev_b32 v112,16,v129
v_and_b32 v113,0xffff0000,v129
v_fma_f32 v106,v106,v103,v102
v_fma_f32 v107,v107,v103,v102
v_fma_f32 v108,v108,v103,v102
v_fma_f32 v109,v109,v103,v102
v_fma_f32 v110,v110,v103,v102
v_fma_f32 v111,v111,v103,v102
v_fma_f32 v112,v112,v103,v102
v_fma_f32 v113,v113,v103,v102
v_add_f32 v26,v26,v106
v_add_f32 v27,v27,v107
v_add_f32 v28,v28,v108
v_add_f32 v29,v29,v109
v_add_f32 v30,v30,v110
v_add_f32 v31,v31,v111
v_add_f32 v32,v32,v112
v_add_f32 v33,v33,v113
v_cvt_pk_bf16_f32 v114,v26,v27
v_cvt_pk_bf16_f32 v115,v28,v29
v_cvt_pk_bf16_f32 v116,v30,v31
v_cvt_pk_bf16_f32 v117,v32,v33
buffer_store_dwordx4 v[114:117],v0,s[36:39],s67 offen offset:0 sc0 sc1
v_add_f32 v106,v26,v27
v_fma_f32 v107,v26,v26,s87
v_fma_f32 v107,v27,v27,v107
v_add_f32 v106,v106,v28
v_fma_f32 v107,v28,v28,v107
v_add_f32 v106,v106,v29
v_fma_f32 v107,v29,v29,v107
v_add_f32 v106,v106,v30
v_fma_f32 v107,v30,v30,v107
v_add_f32 v106,v106,v31
v_fma_f32 v107,v31,v31,v107
v_add_f32 v106,v106,v32
v_fma_f32 v107,v32,v32,v107
v_add_f32 v106,v106,v33
v_fma_f32 v107,v33,v33,v107
s_nop 1
v_permlane16_swap_b32 v106,v107
v_add_f32 v106,v106,v107
v_mov_b32 v107,v106
s_nop 1
v_permlane32_swap_b32 v106,v107
v_add_f32 v106,v106,v107
ds_write_b32 v12,v106 offset:2048
L 0
s_barrier
R 118,13,2048
L 0
v_add_f32 v118,v118,v119
v_add_f32 v120,v120,v121
v_add_f32 v118,v118,v120
v_mul_f32 v118,s86,v118
buffer_store_dword v118,v14,s[36:39],s77 offen offset:0 sc0 sc1
s_mov_b32 s72,s69
s_mov_b32 s69,0x7fff0000
s_mov_b32 s67,0x7fff0000
s_mov_b32 s77,0x7fff0000
s_branch Lg2_poll_%=
Lg2_fin1_%=:
v_lshlrev_b32 v106,16,v126
v_and_b32 v107,0xffff0000,v126
v_lshlrev_b32 v108,16,v127
v_and_b32 v109,0xffff0000,v127
v_lshlrev_b32 v110,16,v128
v_and_b32 v111,0xffff0000,v128
v_lshlrev_b32 v112,16,v129
v_and_b32 v113,0xffff0000,v129
v_fma_f32 v106,v106,v103,v102
v_fma_f32 v107,v107,v103,v102
v_fma_f32 v108,v108,v103,v102
v_fma_f32 v109,v109,v103,v102
v_fma_f32 v110,v110,v103,v102
v_fma_f32 v111,v111,v103,v102
v_fma_f32 v112,v112,v103,v102
v_fma_f32 v113,v113,v103,v102
v_add_f32 v26,v26,v106
v_add_f32 v27,v27,v107
v_add_f32 v28,v28,v108
v_add_f32 v29,v29,v109
v_add_f32 v30,v30,v110
v_add_f32 v31,v31,v111
v_add_f32 v32,v32,v112
v_add_f32 v33,v33,v113
v_cvt_pk_bf16_f32 v114,v26,v27
v_cvt_pk_bf16_f32 v115,v28,v29
v_cvt_pk_bf16_f32 v116,v30,v31
v_cvt_pk_bf16_f32 v117,v32,v33
buffer_store_dwordx4 v[114:117],v0,s[36:39],s67 offen offset:0 sc0 sc1
v_add_f32 v106,v26,v27
v_fma_f32 v107,v26,v26,s87
v_fma_f32 v107,v27,v27,v107
v_add_f32 v106,v106,v28
v_fma_f32 v107,v28,v28,v107
v_add_f32 v106,v106,v29
v_fma_f32 v107,v29,v29,v107
v_add_f32 v106,v106,v30
v_fma_f32 v107,v30,v30,v107
v_add_f32 v106,v106,v31
v_fma_f32 v107,v31,v31,v107
v_add_f32 v106,v106,v32
v_fma_f32 v107,v32,v32,v107
v_add_f32 v106,v106,v33
v_fma_f32 v107,v33,v33,v107
s_nop 1
v_permlane16_swap_b32 v106,v107
v_add_f32 v106,v106,v107
v_mov_b32 v107,v106
s_nop 1
v_permlane32_swap_b32 v106,v107
v_add_f32 v106,v106,v107
ds_write_b32 v12,v106 offset:2048
L 0
s_barrier
R 118,13,2048
L 0
v_add_f32 v118,v118,v119
v_add_f32 v120,v120,v121
v_add_f32 v118,v118,v120
v_mul_f32 v118,s86,v118
buffer_store_dword v118,v14,s[36:39],s77 offen offset:0 sc0 sc1
s_mov_b32 s72,s69
s_mov_b32 s69,0x7fff0000
s_mov_b32 s67,0x7fff0000
s_mov_b32 s77,0x7fff0000
s_branch Lg2_done_%=
Lg2_done_%=:
s_waitcnt vmcnt(2)
s_barrier
buffer_store_dword v4,v3,s[36:39],s70 offen offset:0 sc0 sc1
s_waitcnt vmcnt(1)
s_barrier
buffer_store_dword v4,v3,s[36:39],s72 offen offset:0 sc0 sc1
s_waitcnt vmcnt(0) lgkmcnt(0)
s_barrier
Lg2_end_%=:
s_mov_b32 %0,s53
Lcode_g12_e:
.purgem M
.purgem R
.purgem T
.purgem D
.purgem L
)"
  : "=&s"(epOut) : "s"(ws), "s"(__builtin_amdgcn_readfirstlane(sWave)), "s"(__builtin_amdgcn_readfirstlane(sG0)), "s"(__builtin_amdgcn_readfirstlane(sStep)), "s"(__builtin_amdgcn_readfirstlane(sOutOff)), "s"(__builtin_amdgcn_readfirstlane(sFoutOff)), "s"(__builtin_amdgcn_readfirstlane(sAuxOff)), "s"(__builtin_amdgcn_readfirstlane(sWw)), "s"(__builtin_amdgcn_readfirstlane(sBuf)), "s"(__builtin_amdgcn_readfirstlane(sExp)), "v"(vL16), "v"(vFlag), "v"(vSnapOff), "v"(vSnapRd), "v"(vWd0), "v"(vWd1), "v"(vWr0), "v"(vWr1), "v"(vSp), "v"(vOutS), "v"(vAux0), "v"(vAux1), "v"(vAux2), "v"(vAux3), "v"(vAn), "v"(vAe), "s"(w12) : "v0", "v1", "v2", "v3", "v4", "v5", "v6", "v7", "v8", "v9", "v10", "v11", "v12", "v13", "v14", "v15", "v16", "v17", "v18", "v19", "v20", "v21", "v22", "v23", "v24", "v25", "v26", "v27", "v28", "v29", "v30", "v31", "v32", "v33", "v34", "v35", "v36", "v37", "v38", "v39", "v40", "v41", "v42", "v43", "v44", "v45", "v46", "v47", "v48", "v49", "v50", "v51", "v52", "v53", "v54", "v55", "v56", "v57", "v58", "v59", "v60", "v61", "v62", "v63", "v64", "v65", "v66", "v67", "v68", "v69", "v70", "v71", "v72", "v73", "v74", "v75", "v76", "v77", "v78", "v79", "v80", "v81", "v82", "v83", "v84", "v85", "v86", "v87", "v88", "v89", "v90", "v91", "v92", "v93", "v94", "v95", "v96", "v97", "v98", "v99", "v100", "v101", "v102", "v103", "v104", "v105", "v106", "v107", "v108", "v109", "v110", "v111", "v112", "v113", "v114", "v115", "v116", "v117", "v118", "v119", "v120", "v121", "v122", "v123", "v124", "v125", "v126", "v127", "v128", "v129", "v130", "v131", "v132", "v133", "v134", "v135", "v136", "v137", "v138", "v139", "v140", "v141", "v142", "v143", "v144", "v145", "v146", "v147", "v148", "v149", "v150", "v151", "v152", "v153", "v154", "v155", "v156", "v157", "v158", "v159", "v160", "v161", "v162", "v163", "v164", "v165", "v166", "v167", "v168", "v169", "v170", "v171", "v172", "v173", "v174", "v175", "v176", "v177", "v178", "v179", "v180", "v181", "v182", "v183", "v184", "v185", "v186", "v187", "v188", "v189", "v190", "v191", "v192", "v193", "v194", "v195", "v196", "v197", "v198", "v199", "v200", "v201", "v202", "v203", "v204", "v205", "v206", "v207", "v208", "v209", "v210", "v211", "v212", "v213", "v214", "v215", "v216", "v217", "v218", "v219", "v220", "v221", "v222", "v223", "v224", "v225", "v226", "v227", "v228", "v229", "v230", "v231", "a0", "a1", "a2", "a3", "a4", "a5", "a6", "a7", "a8", "a9", "a10", "a11", "a12", "a13", "a14", "a15", "a16", "a17", "a18", "a19", "a20", "a21", "a22", "a23", "a24", "a25", "a26", "a27", "a28", "a29", "a30", "a31", "a32", "a33", "a34", "a35", "a36", "a37", "a38", "a39", "a40", "a41", "a42", "a43", "a44", "a45", "a46", "a47", "a48", "a49", "a50", "a51", "a52", "a53", "a54", "a55", "a56", "a57", "a58", "a59", "a60", "a61", "a62", "a63", "a64", "a65", "a66", "a67", "a68", "a69", "a70", "a71", "a72", "a73", "a74", "a75", "a76", "a77", "a78", "a79", "a80", "a81", "a82", "a83", "a84", "a85", "a86", "a87", "a88", "a89", "a90", "a91", "a92", "a93", "a94", "a95", "a96", "a97", "a98", "a99", "a100", "a101", "a102", "a103", "a104", "a105", "a106", "a107", "a108", "a109", "a110", "a111", "a112", "a113", "a114", "a115", "a116", "a117", "a118", "a119", "a120", "a121", "a122", "a123", "a124", "a125", "a126", "a127", "a128", "a129", "a130", "a131", "a132", "a133", "a134", "a135", "a136", "a137", "a138", "a139", "a140", "a141", "a142", "a143", "a144", "a145", "a146", "a147", "a148", "a149", "a150", "a151", "a152", "a153", "a154", "a155", "a156", "a157", "a158", "a159", "a160", "a161", "a162", "a163", "a164", "a165", "a166", "a167", "a168", "a169", "a170", "a171", "a172", "a173", "a174", "a175", "a176", "a177", "a178", "a179", "a180", "a181", "a182", "a183", "a184", "a185", "a186", "a187", "a188", "a189", "a190", "a191", "a192", "a193", "a194", "a195", "a196", "a197", "a198", "a199", "a200", "a201", "a202", "a203", "a204", "a205", "a206", "a207", "a208", "a209", "a210", "a211", "a212", "a213", "a214", "a215", "a216", "a217", "a218", "a219", "a220", "a221", "a222", "a223", "a224", "a225", "a226", "a227", "a228", "a229", "a230", "a231", "a232", "a233", "a234", "a235", "a236", "a237", "a238", "a239", "a240", "a241", "a242", "a243", "a244", "a245", "a246", "a247", "a248", "a249", "a250", "a251", "a252", "a253", "a254", "a255", "s36", "s37", "s38", "s39", "s40", "s41", "s42", "s43", "s44", "s45", "s46", "s47", "s48", "s49", "s50", "s51", "s52", "s53", "s54", "s55", "s56", "s57", "s58", "s59", "s60", "s61", "s62", "s63", "s64", "s65", "s66", "s67", "s68", "s69", "s70", "s71", "s72", "s73", "s74", "s75", "s76", "s77", "s78", "s79", "s80", "s81", "s82", "s83", "s84", "s85", "s86", "s87", "s88", "s89", "s90", "s91", "s92", "s93", "s94", "s95", "s96", "s97", "vcc", "scc", "memory");
    ep = epOut;
    if (kind == 1 && 5 + RHELP + (rep >> 1) < NR4) { kind = 4; kh = rep & 1; rep = 5 + RHELP + (rep >> 1); }
  }
  if (kind == 4) {
    const unsigned sStep = rep < 5 ? 5 : rep == 5 ? 6 : NR4;
    COMMON(10, (kh * 9 + fj) * 128, 2560, 143360, 2048, 448, 0);
    const int cs = slice;
    const unsigned sOutOff = (kh ? OFF_P1 : OFF_P0) + (cs * 4 + wave) * 1024, sFoutOff = OFF_FP + (kh * 8 + cs) * 128;
    const unsigned sAuxOff = (kh ? OFF_XP : OFF_X1) + (cs * 4 + wave) * 1024, sWw = (kh * 1792 + 16 * wave) * 2048 + cs * 256, sBuf = kh * 57344;
    const int zu = 108 + (rep + 2) % 5;
    const unsigned vSp = lg * 2048 + l15 * 4, vSn = rep >= 5 ? OOB : !ALLZ ? (zu * 16 + wave * 4 + lg) * 2048 + (cs * 2 + kh) * 128 + l15 * 8 :
                         ((rep + 5 * (wave >> 1)) * 16 + (wave & 1) * 8 + (ln >> 3)) * 2048 + (cs * 2 + kh) * 128 + (ln & 7) * 16;
    (void)vWr2;
    const unsigned sExp = __builtin_amdgcn_readfirstlane(ep);
    asm volatile(
R"(.macro M d,w,x
v_mfma_f32_16x16x32_bf16 v[\d:\d+3], \w, v[\x:\x+3], v[\d:\d+3]
.endm
.macro R d,a,o
ds_read_b128 v[\d:\d+3], v\a offset:\o
.endm
.macro T d,a,o
ds_read_b64_tr_b16 \d, v\a offset:\o
.endm
.macro D a,s,o
buffer_load_dwordx4 v\a, s[36:39], s\s offen offset:\o sc1 lds
.endm
.macro L c
s_waitcnt lgkmcnt(\c)
.endm
Lcode_g4_b:
s_mov_b64 s[36:37],%0
s_mov_b32 s38,111243392
s_mov_b32 s39,0x00020000
s_mov_b32 s44,%1
s_mov_b32 s45,%2
s_mov_b32 s46,%3
s_mov_b32 s47,%4
s_mov_b32 s48,%5
s_mov_b32 s49,%6
s_mov_b32 s50,%7
s_mov_b32 s51,%8
s_mov_b32 s93,%9
s_mov_b32 s53,%10
v_mov_b32 v0,%11
v_mov_b32 v3,%12
v_mov_b32 v5,%13
v_mov_b32 v6,%14
v_mov_b32 v10,%15
v_mov_b32 v11,%16
v_mov_b32 v12,%17
v_mov_b32 v13,%18
v_mov_b32 v9,%19
v_mov_b32 v7,%20
s_mov_b64 s[40:41],%21
s_mov_b32 s42,7340032
s_mov_b32 s43,0x00020000
s_lshl_b32 s57,s44,11
s_cmp_eq_u32 s45,175
s_cbranch_scc1 Lg4_pre_%=
s_add_u32 s55,s50,0
s_add_u32 m0,s57,40960
s_nop 0
buffer_load_dwordx4 v10,s[40:43],s55 offen offset:0  lds
s_add_u32 m0,s57,41984
s_nop 0
buffer_load_dwordx4 v11,s[40:43],s55 offen offset:0  lds
s_add_u32 m0,s57,49024
s_nop 0
buffer_load_dwordx4 v10,s[40:43],s55 offen offset:128  lds
s_add_u32 m0,s57,50048
s_nop 0
buffer_load_dwordx4 v11,s[40:43],s55 offen offset:128  lds
s_add_u32 s55,s50,131072
s_add_u32 m0,s57,57344
s_nop 0
buffer_load_dwordx4 v10,s[40:43],s55 offen offset:0  lds
s_add_u32 m0,s57,58368
s_nop 0
buffer_load_dwordx4 v11,s[40:43],s55 offen offset:0  lds
s_add_u32 m0,s57,65408
s_nop 0
buffer_load_dwordx4 v10,s[40:43],s55 offen offset:128  lds
s_add_u32 m0,s57,66432
s_nop 0
buffer_load_dwordx4 v11,s[40:43],s55 offen offset:128  lds
s_add_u32 s55,s50,262144
s_add_u32 m0,s57,73728
s_nop 0
buffer_load_dwordx4 v10,s[40:43],s55 offen offset:0  lds
s_add_u32 m0,s57,74752
s_nop 0
buffer_load_dwordx4 v11,s[40:43],s55 offen offset:0  lds
s_add_u32 m0,s57,81792
s_nop 0
buffer_load_dwordx4 v10,s[40:43],s55 offen offset:128  lds
s_add_u32 m0,s57,82816
s_nop 0
buffer_load_dwordx4 v11,s[40:43],s55 offen offset:128  lds
s_add_u32 s55,s50,393216
s_add_u32 m0,s57,90112
s_nop 0
buffer_load_dwordx4 v10,s[40:43],s55 offen offset:0  lds
s_add_u32 m0,s57,91136
s_nop 0
buffer_load_dwordx4 v11,s[40:43],s55 offen offset:0  lds
s_add_u32 m0,s57,98176
s_nop 0
buffer_load_dwordx4 v10,s[40:43],s55 offen offset:128  lds
s_add_u32 m0,s57,99200
s_nop 0
buffer_load_dwordx4 v11,s[40:43],s55 offen offset:128  lds
s_add_u32 s55,s50,524288
s_add_u32 m0,s57,106496
s_nop 0
buffer_load_dwordx4 v10,s[40:43],s55 offen offset:0  lds
s_add_u32 m0,s57,107520
s_nop 0
buffer_load_dwordx4 v11,s[40:43],s55 offen offset:0  lds
s_add_u32 m0,s57,114560
s_nop 0
buffer_load_dwordx4 v10,s[40:43],s55 offen offset:128  lds
s_add_u32 m0,s57,115584
s_nop 0
buffer_load_dwordx4 v11,s[40:43],s55 offen offset:128  lds
s_add_u32 s55,s50,655360
s_add_u32 m0,s57,122880
s_nop 0
buffer_load_dwordx4 v10,s[40:43],s55 offen offset:0  lds
s_add_u32 m0,s57,123904
s_nop 0
buffer_load_dwordx4 v11,s[40:43],s55 offen offset:0  lds
s_add_u32 m0,s57,130944
s_nop 0
buffer_load_dwordx4 v10,s[40:43],s55 offen offset:128  lds
s_add_u32 m0,s57,131968
s_nop 0
buffer_load_dwordx4 v11,s[40:43],s55 offen offset:128  lds
s_waitcnt vmcnt(20) lgkmcnt(0)
s_barrier
s_add_u32 s55,s50,786432
s_add_u32 m0,s57,139264
s_nop 0
buffer_load_dwordx4 v10,s[40:43],s55 offen offset:0  lds
s_add_u32 m0,s57,140288
s_nop 0
buffer_load_dwordx4 v11,s[40:43],s55 offen offset:0  lds
s_add_u32 m0,s57,147328
s_nop 0
buffer_load_dwordx4 v10,s[40:43],s55 offen offset:128  lds
s_add_u32 m0,s57,148352
s_nop 0
buffer_load_dwordx4 v11,s[40:43],s55 offen offset:128  lds
v_add_u32 v18,40960,v12
v_add_u32 v19,40960,v13
T a[0:1],18,0
T a[2:3],18,512
T a[4:5],19,0
T a[6:7],19,512
T a[8:9],18,4096
T a[10:11],18,4608
T a[12:13],19,4096
T a[14:15],19,4608
s_waitcnt vmcnt(20) lgkmcnt(0)
s_barrier
s_add_u32 s55,s50,917504
s_add_u32 m0,s57,40960
s_nop 0
buffer_load_dwordx4 v10,s[40:43],s55 offen offset:0  lds
s_add_u32 m0,s57,41984
s_nop 0
buffer_load_dwordx4 v11,s[40:43],s55 offen offset:0  lds
s_add_u32 m0,s57,49024
s_nop 0
buffer_load_dwordx4 v10,s[40:43],s55 offen offset:128  lds
s_add_u32 m0,s57,50048
s_nop 0
buffer_load_dwordx4 v11,s[40:43],s55 offen offset:128  lds
v_add_u32 v18,57344,v12
v_add_u32 v19,57344,v13
T a[16:17],18,0
T a[18:19],18,512
T a[20:21],19,0
T a[22:23],19,512
T a[24:25],18,4096
T a[26:27],18,4608
T a[28:29],19,4096
T a[30:31],19,4608
s_waitcnt vmcnt(20) lgkmcnt(0)
s_barrier
s_add_u32 s55,s50,1048576
s_add_u32 m0,s57,57344
s_nop 0
buffer_load_dwordx4 v10,s[40:43],s55 offen offset:0  lds
s_add_u32 m0,s57,58368
s_nop 0
buffer_load_dwordx4 v11,s[40:43],s55 offen offset:0  lds
s_add_u32 m0,s57,65408
s_nop 0
buffer_load_dwordx4 v10,s[40:43],s55 offen offset:128  lds
s_add_u32 m0,s57,66432
s_nop 0
buffer_load_dwordx4 v11,s[40:43],s55 offen offset:128  lds
v_add_u32 v18,73728,v12
v_add_u32 v19,73728,v13
T a[32:33],18,0
T a[34:35],18,512
T a[36:37],19,0
T a[38:39],19,512
T a[40:41],18,4096
T a[42:43],18,4608
T a[44:45],19,4096
T a[46:47],19,4608
s_waitcnt vmcnt(20) lgkmcnt(0)
s_barrier
s_add_u32 s55,s50,1179648
s_add_u32 m0,s57,73728
s_nop 0
buffer_load_dwordx4 v10,s[40:43],s55 offen offset:0  lds
s_add_u32 m0,s57,74752
s_nop 0
buffer_load_dwordx4 v11,s[40:43],s55 offen offset:0  lds
s_add_u32 m0,s57,81792
s_nop 0
buffer_load_dwordx4 v10,s[40:43],s55 offen offset:128  lds
s_add_u32 m0,s57,82816
s_nop 0
buffer_load_dwordx4 v11,s[40:43],s55 offen offset:128  lds
v_add_u32 v18,90112,v12
v_add_u32 v19,90112,v13
T a[48:49],18,0
T a[50:51],18,512
T a[52:53],19,0
T a[54:55],19,512
T a[56:57],18,4096
T a[58:59],18,4608
T a[60:61],19,4096
T a[62:63],19,4608
s_waitcnt vmcnt(20) lgkmcnt(0)
s_barrier
s_add_u32 s55,s50,1310720
s_add_u32 m0,s57,90112
s_nop 0
buffer_load_dwordx4 v10,s[40:43],s55 offen offset:0  lds
s_add_u32 m0,s57,91136
s_nop 0
buffer_load_dwordx4 v11,s[40:43],s55 offen offset:0  lds
s_add_u32 m0,s57,98176
s_nop 0
buffer_load_dwordx4 v10,s[40:43],s55 offen offset:128  lds
s_add_u32 m0,s57,99200
s_nop 0
buffer_load_dwordx4 v11,s[40:43],s55 offen offset:128  lds
v_add_u32 v18,106496,v12
v_add_u32 v19,106496,v13
T a[64:65],18,0
T a[66:67],18,512
T a[68:69],19,0
T a[70:71],19,512
T a[72:73],18,4096
T a[74:75],18,4608
T a[76:77],19,4096
T a[78:79],19,4608
s_waitcnt vmcnt(20) lgkmcnt(0)
s_barrier
s_add_u32 s55,s50,1441792
s_add_u32 m0,s57,106496
s_nop 0
buffer_load_dwordx4 v10,s[40:43],s55 offen offset:0  lds
s_add_u32 m0,s57,107520
s_nop 0
buffer_load_dwordx4 v11,s[40:43],s55 offen offset:0  lds
s_add_u32 m0,s57,114560
s_nop 0
buffer_load_dwordx4 v10,s[40:43],s55 offen offset:128  lds
s_add_u32 m0,s57,115584
s_nop 0
buffer_load_dwordx4 v11,s[40:43],s55 offen offset:128  lds
v_add_u32 v18,122880,v12
v_add_u32 v19,122880,v13
T a[80:81],18,0
T a[82:83],18,512
T a[84:85],19,0
T a[86:87],19,512
T a[88:89],18,4096
T a[90:91],18,4608
T a[92:93],19,4096
T a[94:95],19,4608
s_waitcnt vmcnt(20) lgkmcnt(0)
s_barrier
s_add_u32 s55,s50,1572864
s_add_u32 m0,s57,122880
s_nop 0
buffer_load_dwordx4 v10,s[40:43],s55 offen offset:0  lds
s_add_u32 m0,s57,123904
s_nop 0
buffer_load_dwordx4 v11,s[40:43],s55 offen offset:0  lds
s_add_u32 m0,s57,130944
s_nop 0
buffer_load_dwordx4 v10,s[40:43],s55 offen offset:128  lds
s_add_u32 m0,s57,131968
s_nop 0
buffer_load_dwordx4 v11,s[40:43],s55 offen offset:128  lds
v_add_u32 v18,139264,v12
v_add_u32 v19,139264,v13
T a[96:97],18,0
T a[98:99],18,512
T a[100:101],19,0
T a[102:103],19,512
T a[104:105],18,4096
T a[106:107],18,4608
T a[108:109],19,4096
T a[110:111],19,4608
s_waitcnt vmcnt(20) lgkmcnt(0)
s_barrier
s_add_u32 s55,s50,1703936
s_add_u32 m0,s57,139264
s_nop 0
buffer_load_dwordx4 v10,s[40:43],s55 offen offset:0  lds
s_add_u32 m0,s57,140288
s_nop 0
buffer_load_dwordx4 v11,s[40:43],s55 offen offset:0  lds
s_add_u32 m0,s57,147328
s_nop 0
buffer_load_dwordx4 v10,s[40:43],s55 offen offset:128  lds
s_add_u32 m0,s57,148352
s_nop 0
buffer_load_dwordx4 v11,s[40:43],s55 offen offset:128  lds
v_add_u32 v18,40960,v12
v_add_u32 v19,40960,v13
T a[112:113],18,0
T a[114:115],18,512
T a[116:117],19,0
T a[118:119],19,512
T a[120:121],18,4096
T a[122:123],18,4608
T a[124:125],19,4096
T a[126:127],19,4608
s_waitcnt vmcnt(20) lgkmcnt(0)
s_barrier
s_add_u32 s55,s50,1835008
s_add_u32 m0,s57,40960
s_nop 0
buffer_load_dwordx4 v10,s[40:43],s55 offen offset:0  lds
s_add_u32 m0,s57,41984
s_nop 0
buffer_load_dwordx4 v11,s[40:43],s55 offen offset:0  lds
s_add_u32 m0,s57,49024
s_nop 0
buffer_load_dwordx4 v10,s[40:43],s55 offen offset:128  lds
s_add_u32 m0,s57,50048
s_nop 0
buffer_load_dwordx4 v11,s[40:43],s55 offen offset:128  lds
v_add_u32 v18,57344,v12
v_add_u32 v19,57344,v13
T a[128:129],18,0
T a[130:131],18,512
T a[132:133],19,0
T a[134:135],19,512
T a[136:137],18,4096
T a[138:139],18,4608
T a[140:141],19,4096
T a[142:143],19,4608
s_waitcnt vmcnt(20) lgkmcnt(0)
s_barrier
s_add_u32 s55,s50,1966080
s_add_u32 m0,s57,57344
s_nop 0
buffer_load_dwordx4 v10,s[40:43],s55 offen offset:0  lds
s_add_u32 m0,s57,58368
s_nop 0
buffer_load_dwordx4 v11,s[40:43],s55 offen offset:0  lds
s_add_u32 m0,s57,65408
s_nop 0
buffer_load_dwordx4 v10,s[40:43],s55 offen offset:128  lds
s_add_u32 m0,s57,66432
s_nop 0
buffer_load_dwordx4 v11,s[40:43],s55 offen offset:128  lds
v_add_u32 v18,73728,v12
v_add_u32 v19,73728,v13
T a[144:145],18,0
T a[146:147],18,512
T a[148:149],19,0
T a[150:151],19,512
T a[152:153],18,4096
T a[154:155],18,4608
T a[156:157],19,4096
T a[158:159],19,4608
s_waitcnt vmcnt(20) lgkmcnt(0)
s_barrier
s_add_u32 s55,s50,2097152
s_add_u32 m0,s57,73728
s_nop 0
buffer_load_dwordx4 v10,s[40:43],s55 offen offset:0  lds
s_add_u32 m0,s57,74752
s_nop 0
buffer_load_dwordx4 v11,s[40:43],s55 offen offset:0  lds
s_add_u32 m0,s57,81792
s_nop 0
buffer_load_dwordx4 v10,s[40:43],s55 offen offset:128  lds
s_add_u32 m0,s57,82816
s_nop 0
buffer_load_dwordx4 v11,s[40:43],s55 offen offset:128  lds
v_add_u32 v18,90112,v12
v_add_u32 v19,90112,v13
T a[160:161],18,0
T a[162:163],18,512
T a[164:165],19,0
T a[166:167],19,512
T a[168:169],18,4096
T a[170:171],18,4608
T a[172:173],19,4096
T a[174:175],19,4608
s_waitcnt vmcnt(20) lgkmcnt(0)
s_barrier
s_add_u32 s55,s50,2228224
s_add_u32 m0,s57,90112
s_nop 0
buffer_load_dwordx4 v10,s[40:43],s55 offen offset:0  lds
s_add_u32 m0,s57,91136
s_nop 0
buffer_load_dwordx4 v11,s[40:43],s55 offen offset:0  lds
s_add_u32 m0,s57,98176
s_nop 0
buffer_load_dwordx4 v10,s[40:43],s55 offen offset:128  lds
s_add_u32 m0,s57,99200
s_nop 0
buffer_load_dwordx4 v11,s[40:43],s55 offen offset:128  lds
v_add_u32 v18,106496,v12
v_add_u32 v19,106496,v13
T a[176:177],18,0
T a[178:179],18,512
T a[180:181],19,0
T a[182:183],19,512
T a[184:185],18,4096
T a[186:187],18,4608
T a[188:189],19,4096
T a[190:191],19,4608
s_waitcnt vmcnt(20) lgkmcnt(0)
s_barrier
s_add_u32 s55,s50,2359296
s_add_u32 m0,s57,106496
s_nop 0
buffer_load_dwordx4 v10,s[40:43],s55 offen offset:0  lds
s_add_u32 m0,s57,107520
s_nop 0
buffer_load_dwordx4 v11,s[40:43],s55 offen offset:0  lds
s_add_u32 m0,s57,114560
s_nop 0
buffer_load_dwordx4 v10,s[40:43],s55 offen offset:128  lds
s_add_u32 m0,s57,115584
s_nop 0
buffer_load_dwordx4 v11,s[40:43],s55 offen offset:128  lds
v_add_u32 v18,122880,v12
v_add_u32 v19,122880,v13
T a[192:193],18,0
T a[194:195],18,512
T a[196:197],19,0
T a[198:199],19,512
T a[200:201],18,4096
T a[202:203],18,4608
T a[204:205],19,4096
T a[206:207],19,4608
s_waitcnt vmcnt(20) lgkmcnt(0)
s_barrier
s_add_u32 s55,s50,2490368
s_add_u32 m0,s57,122880
s_nop 0
buffer_load_dwordx4 v10,s[40:43],s55 offen offset:0  lds
s_add_u32 m0,s57,123904
s_nop 0
buffer_load_dwordx4 v11,s[40:43],s55 offen offset:0  lds
s_add_u32 m0,s57,130944
s_nop 0
buffer_load_dwordx4 v10,s[40:43],s55 offen offset:128  lds
s_add_u32 m0,s57,131968
s_nop 0
buffer_load_dwordx4 v11,s[40:43],s55 offen offset:128  lds
v_add_u32 v18,139264,v12
v_add_u32 v19,139264,v13
T a[208:209],18,0
T a[210:211],18,512
T a[212:213],19,0
T a[214:215],19,512
T a[216:217],18,4096
T a[218:219],18,4608
T a[220:221],19,4096
T a[222:223],19,4608
s_waitcnt vmcnt(20) lgkmcnt(0)
s_barrier
s_add_u32 s55,s50,2621440
s_add_u32 m0,s57,139264
s_nop 0
buffer_load_dwordx4 v10,s[40:43],s55 offen offset:0  lds
s_add_u32 m0,s57,140288
s_nop 0
buffer_load_dwordx4 v11,s[40:43],s55 offen offset:0  lds
s_add_u32 m0,s57,147328
s_nop 0
buffer_load_dwordx4 v10,s[40:43],s55 offen offset:128  lds
s_add_u32 m0,s57,148352
s_nop 0
buffer_load_dwordx4 v11,s[40:43],s55 offen offset:128  lds
v_add_u32 v18,40960,v12
v_add_u32 v19,40960,v13
T a[224:225],18,0
T a[226:227],18,512
T a[228:229],19,0
T a[230:231],19,512
T a[232:233],18,4096
T a[234:235],18,4608
T a[236:237],19,4096
T a[238:239],19,4608
s_waitcnt vmcnt(20) lgkmcnt(0)
s_barrier
)"
R"(s_add_u32 s55,s50,2752512
s_add_u32 m0,s57,40960
s_nop 0
buffer_load_dwordx4 v10,s[40:43],s55 offen offset:0  lds
s_add_u32 m0,s57,41984
s_nop 0
buffer_load_dwordx4 v11,s[40:43],s55 offen offset:0  lds
s_add_u32 m0,s57,49024
s_nop 0
buffer_load_dwordx4 v10,s[40:43],s55 offen offset:128  lds
s_add_u32 m0,s57,50048
s_nop 0
buffer_load_dwordx4 v11,s[40:43],s55 offen offset:128  lds
v_add_u32 v18,57344,v12
v_add_u32 v19,57344,v13
T a[240:241],18,0
T a[242:243],18,512
T a[244:245],19,0
T a[246:247],19,512
T a[248:249],18,4096
T a[250:251],18,4608
T a[252:253],19,4096
T a[254:255],19,4608
Lg4_pre_%=:
s_waitcnt vmcnt(20) lgkmcnt(0)
s_barrier
s_add_u32 s55,s50,2883584
s_add_u32 m0,s57,57344
s_nop 0
buffer_load_dwordx4 v10,s[40:43],s55 offen offset:0  lds
s_add_u32 m0,s57,58368
s_nop 0
buffer_load_dwordx4 v11,s[40:43],s55 offen offset:0  lds
s_add_u32 m0,s57,65408
s_nop 0
buffer_load_dwordx4 v10,s[40:43],s55 offen offset:128  lds
s_add_u32 m0,s57,66432
s_nop 0
buffer_load_dwordx4 v11,s[40:43],s55 offen offset:128  lds
v_add_u32 v18,73728,v12
v_add_u32 v19,73728,v13
T v[64:65],18,0
T v[66:67],18,512
T v[68:69],19,0
T v[70:71],19,512
T v[72:73],18,4096
T v[74:75],18,4608
T v[76:77],19,4096
T v[78:79],19,4608
s_waitcnt vmcnt(20) lgkmcnt(0)
s_barrier
s_add_u32 s55,s50,3014656
s_add_u32 m0,s57,73728
s_nop 0
buffer_load_dwordx4 v10,s[40:43],s55 offen offset:0  lds
s_add_u32 m0,s57,74752
s_nop 0
buffer_load_dwordx4 v11,s[40:43],s55 offen offset:0  lds
s_add_u32 m0,s57,81792
s_nop 0
buffer_load_dwordx4 v10,s[40:43],s55 offen offset:128  lds
s_add_u32 m0,s57,82816
s_nop 0
buffer_load_dwordx4 v11,s[40:43],s55 offen offset:128  lds
v_add_u32 v18,90112,v12
v_add_u32 v19,90112,v13
T v[80:81],18,0
T v[82:83],18,512
T v[84:85],19,0
T v[86:87],19,512
T v[88:89],18,4096
T v[90:91],18,4608
T v[92:93],19,4096
T v[94:95],19,4608
s_waitcnt vmcnt(20) lgkmcnt(0)
s_barrier
s_add_u32 s55,s50,3145728
s_add_u32 m0,s57,90112
s_nop 0
buffer_load_dwordx4 v10,s[40:43],s55 offen offset:0  lds
s_add_u32 m0,s57,91136
s_nop 0
buffer_load_dwordx4 v11,s[40:43],s55 offen offset:0  lds
s_add_u32 m0,s57,98176
s_nop 0
buffer_load_dwordx4 v10,s[40:43],s55 offen offset:128  lds
s_add_u32 m0,s57,99200
s_nop 0
buffer_load_dwordx4 v11,s[40:43],s55 offen offset:128  lds
v_add_u32 v18,106496,v12
v_add_u32 v19,106496,v13
T v[96:97],18,0
T v[98:99],18,512
T v[100:101],19,0
T v[102:103],19,512
T v[104:105],18,4096
T v[106:107],18,4608
T v[108:109],19,4096
T v[110:111],19,4608
s_waitcnt vmcnt(20) lgkmcnt(0)
s_barrier
s_add_u32 s55,s50,3276800
s_add_u32 m0,s57,106496
s_nop 0
buffer_load_dwordx4 v10,s[40:43],s55 offen offset:0  lds
s_add_u32 m0,s57,107520
s_nop 0
buffer_load_dwordx4 v11,s[40:43],s55 offen offset:0  lds
s_add_u32 m0,s57,114560
s_nop 0
buffer_load_dwordx4 v10,s[40:43],s55 offen offset:128  lds
s_add_u32 m0,s57,115584
s_nop 0
buffer_load_dwordx4 v11,s[40:43],s55 offen offset:128  lds
v_add_u32 v18,122880,v12
v_add_u32 v19,122880,v13
T v[112:113],18,0
T v[114:115],18,512
T v[116:117],19,0
T v[118:119],19,512
T v[120:121],18,4096
T v[122:123],18,4608
T v[124:125],19,4096
T v[126:127],19,4608
s_waitcnt vmcnt(20) lgkmcnt(0)
s_barrier
s_add_u32 s55,s50,3407872
s_add_u32 m0,s57,122880
s_nop 0
buffer_load_dwordx4 v10,s[40:43],s55 offen offset:0  lds
s_add_u32 m0,s57,123904
s_nop 0
buffer_load_dwordx4 v11,s[40:43],s55 offen offset:0  lds
s_add_u32 m0,s57,130944
s_nop 0
buffer_load_dwordx4 v10,s[40:43],s55 offen offset:128  lds
s_add_u32 m0,s57,131968
s_nop 0
buffer_load_dwordx4 v11,s[40:43],s55 offen offset:128  lds
v_add_u32 v18,139264,v12
v_add_u32 v19,139264,v13
T v[128:129],18,0
T v[130:131],18,512
T v[132:133],19,0
T v[134:135],19,512
T v[136:137],18,4096
T v[138:139],18,4608
T v[140:141],19,4096
T v[142:143],19,4608
s_waitcnt vmcnt(20) lgkmcnt(0)
s_barrier
s_add_u32 s55,s50,3538944
s_add_u32 m0,s57,139264
s_nop 0
buffer_load_dwordx4 v10,s[40:43],s55 offen offset:0  lds
s_add_u32 m0,s57,140288
s_nop 0
buffer_load_dwordx4 v11,s[40:43],s55 offen offset:0  lds
s_add_u32 m0,s57,147328
s_nop 0
buffer_load_dwordx4 v10,s[40:43],s55 offen offset:128  lds
s_add_u32 m0,s57,148352
s_nop 0
buffer_load_dwordx4 v11,s[40:43],s55 offen offset:128  lds
v_add_u32 v18,40960,v12
v_add_u32 v19,40960,v13
T v[144:145],18,0
T v[146:147],18,512
T v[148:149],19,0
T v[150:151],19,512
T v[152:153],18,4096
T v[154:155],18,4608
T v[156:157],19,4096
T v[158:159],19,4608
s_waitcnt vmcnt(20) lgkmcnt(0)
s_barrier
v_add_u32 v18,57344,v12
v_add_u32 v19,57344,v13
T v[160:161],18,0
T v[162:163],18,512
T v[164:165],19,0
T v[166:167],19,512
T v[168:169],18,4096
T v[170:171],18,4608
T v[172:173],19,4096
T v[174:175],19,4608
s_waitcnt vmcnt(16) lgkmcnt(0)
s_barrier
v_add_u32 v18,73728,v12
v_add_u32 v19,73728,v13
T v[176:177],18,0
T v[178:179],18,512
T v[180:181],19,0
T v[182:183],19,512
T v[184:185],18,4096
T v[186:187],18,4608
T v[188:189],19,4096
T v[190:191],19,4608
s_waitcnt vmcnt(12) lgkmcnt(0)
s_barrier
v_add_u32 v18,90112,v12
v_add_u32 v19,90112,v13
T v[192:193],18,0
T v[194:195],18,512
T v[196:197],19,0
T v[198:199],19,512
T v[200:201],18,4096
T v[202:203],18,4608
T v[204:205],19,4096
T v[206:207],19,4608
s_waitcnt vmcnt(8) lgkmcnt(0)
s_barrier
v_add_u32 v18,106496,v12
v_add_u32 v19,106496,v13
T v[208:209],18,0
T v[210:211],18,512
T v[212:213],19,0
T v[214:215],19,512
T v[216:217],18,4096
T v[218:219],18,4608
T v[220:221],19,4096
T v[222:223],19,4608
s_waitcnt vmcnt(4) lgkmcnt(0)
s_barrier
v_add_u32 v18,122880,v12
v_add_u32 v19,122880,v13
T v[224:225],18,0
T v[226:227],18,512
T v[228:229],19,0
T v[230:231],19,512
T v[232:233],18,4096
T v[234:235],18,4608
T v[236:237],19,4096
T v[238:239],19,4608
s_waitcnt vmcnt(0) lgkmcnt(0)
s_barrier
v_add_u32 v18,139264,v12
v_add_u32 v19,139264,v13
T v[240:241],18,0
T v[242:243],18,512
T v[244:245],19,0
T v[246:247],19,512
T v[248:249],18,4096
T v[250:251],18,4608
T v[252:253],19,4096
T v[254:255],19,4608
L 0
s_barrier
v_mov_b32 v4,s53
s_mov_b32 s82,0
s_mov_b32 s80,0
s_mov_b32 s54,s45
s_mul_i32 s57,s44,7168
s_mul_i32 s55,s45,114688
s_add_u32 s55,s55,s57
s_add_u32 s55,s55,40894464
s_mul_i32 s57,s44,7168
s_mul_i32 s60,s46,114688
s_mul_i32 s61,s46,2560
s_mul_i32 s62,s46,32768
s_mul_i32 s63,s46,2048
s_mul_i32 s64,s46,512
s_lshl_b32 s59,s44,8
s_add_u32 s59,s59,143360
s_mul_i32 s58,s45,2560
s_add_u32 s58,s58,109891584
s_mul_i32 s65,s45,32768
s_add_u32 s65,s65,s47
s_mul_i32 s68,s45,2048
s_add_u32 s68,s68,s48
s_mul_i32 s73,s45,512
s_add_u32 s73,s73,106954752
s_mul_i32 s74,s45,32768
s_add_u32 s74,s74,s49
s_mul_i32 s75,s45,1024
s_add_u32 s75,s75,107479040
s_mov_b32 s66,0x7fff0000
s_mov_b32 s67,0x7fff0000
s_mov_b32 s69,0x7fff0000
s_mov_b32 s76,0x7fff0000
s_mov_b32 s77,0x7fff0000
s_mov_b32 s70,0x7fff0000
s_mov_b32 s71,0x7fff0000
s_mov_b32 s72,0x7fff0000
v_mov_b32 v1,v0
v_add_u32 v2,28672,v0
s_mov_b32 s84,28672
s_mov_b32 s86,0x3a800000
s_mov_b32 s87,0x3727c5ac
s_mov_b32 s88,0xc0135761
s_mov_b32 s89,0x3d372713
s_lshr_b32 s85,s44,1
s_mul_i32 s85,s85,127
s_add_u32 s55,s55,s51
s_mul_i32 s78,s45,114688
s_sub_u32 s60,s55,s78
s_add_u32 s50,s93,224
s_add_u32 s93,s93,170
s_cmp_lt_u32 s45,224
s_cselect_b32 s63,224,-1
s_cmp_lt_u32 s45,170
s_cselect_b32 s63,170,s63
s_cselect_b32 s93,s93,s50
s_cselect_b32 s88,6,8
s_cselect_b32 s89,224,-1
v_lshlrev_b32 v8,22,v0
v_and_b32 v8,0x80000000,v8
v_or_b32 v8,v8,v5
v_add_u32 v2,57344,v0
s_lshl_b32 s84,s57,1
s_add_u32 s84,s84,57344
s_mov_b64 s[86:87],%22
s_mov_b64 s[40:41],%22
s_mov_b32 s42,4194304
s_and_b32 s78,s47,65535
s_lshr_b32 s78,s78,4
s_sub_u32 s85,s47,s78
s_add_u32 s85,s85,4194304
s_add_u32 s45,s47,5832704
s_mul_i32 s73,s44,1280
s_add_u32 s73,s73,114688
v_lshrrev_b32 v62,4,v9
v_and_b32 v62,3,v62
v_mul_u32_u24 v62,288,v62
v_lshrrev_b32 v63,7,v9
v_and_b32 v5,12,v9
v_add3_u32 v5,v5,v62,v63
v_add_u32 v5,s73,v5
v_mov_b32 v10,0
v_mov_b32 v11,0
v_mov_b32 v12,0
v_mov_b32 v13,0
s_mov_b32 s79,1638400
buffer_store_dwordx4 v[10:13],v7,s[40:43],s79 offen offset:0 sc0 sc1
s_mov_b32 s79,1966080
buffer_store_dwordx4 v[10:13],v7,s[40:43],s79 offen offset:0 sc0 sc1
s_mov_b32 s79,2293760
buffer_store_dwordx4 v[10:13],v7,s[40:43],s79 offen offset:0 sc0 sc1
s_mov_b32 s79,2621440
buffer_store_dwordx4 v[10:13],v7,s[40:43],s79 offen offset:0 sc0 sc1
s_mov_b32 s79,2949120
buffer_store_dwordx4 v[10:13],v7,s[40:43],s79 offen offset:0 sc0 sc1
s_mov_b32 s79,3276800
buffer_store_dwordx4 v[10:13],v7,s[40:43],s79 offen offset:0 sc0 sc1
s_mov_b32 s79,3604480
buffer_store_dwordx4 v[10:13],v7,s[40:43],s79 offen offset:0 sc0 sc1
s_mov_b32 s79,3932160
buffer_store_dwordx4 v[10:13],v7,s[40:43],s79 offen offset:0 sc0 sc1
s_getpc_b64 s[40:41]
Lg4_pc_%=:
s_lshl_b32 s78,s44,13
v_lshlrev_b32 v62,3,v0
v_add_u32 v62,s78,v62
v_min_u32 v62,Lcode_g4_e-Lg4_pc_%=-4,v62
s_mov_b32 m0,163584
s_nop 0
global_load_lds_dword v62,s[40:41]
s_add_u32 m0,s59,4096
s_nop 0
buffer_load_dword v8,s[36:39],s58 offen offset:0 sc1 lds
buffer_store_dword v4,v3,s[36:39],s71 offen offset:0 sc0 sc1
s_add_u32 m0,s59,5120
s_nop 0
buffer_load_dword v8,s[36:39],s58 offen offset:0 sc1 lds
buffer_store_dword v4,v3,s[36:39],s71 offen offset:0 sc0 sc1
s_mov_b32 s83,0
Lg4_poll_%=:
s_add_u32 s78,s83,2
s_and_b32 s78,s78,3
s_lshl_b32 s78,s78,10
s_add_u32 s78,s78,4096
s_add_u32 m0,s59,s78
s_nop 0
buffer_load_dword v8,s[36:39],s58 offen offset:0 sc1 lds
s_and_b32 s78,s83,3
s_lshl_b32 s78,s78,10
v_add_u32 v62,s78,v6
s_waitcnt vmcnt(4)
s_barrier
ds_read_b32 v7,v62 offset:4096
buffer_store_dword v4,v3,s[36:39],s70 offen offset:0 sc0 sc1
s_mov_b32 s70,s71
s_mov_b32 s71,s72
s_mov_b32 s72,0x7fff0000
s_add_u32 s83,s83,1
s_add_u32 s82,s82,1
L 0
v_cmp_eq_u32 vcc,s53,v7
s_nop 0
s_cmp_eq_u32 vcc_lo,-1
s_cbranch_scc1 Lg4_go_%=
s_cmp_lt_u32 s82,20000
s_cbranch_scc1 Lg4_poll_%=
Lg4_go_%=:
s_add_u32 m0,s57,0
s_add_u32 s56,s55,0
D 0,56,0
D 0,56,1024
D 0,56,2048
D 0,56,3072
s_add_u32 m0,s57,4096
s_add_u32 s56,s56,4096
D 0,56,0
D 0,56,1024
D 0,56,2048
s_add_u32 s61,s54,s46
s_cmp_ge_u32 s61,s63
s_cselect_b32 s61,s93,s61
s_cselect_b32 s46,s88,s46
s_cselect_b32 s63,s89,s63
s_cselect_b32 s93,s50,s93
s_cselect_b32 s88,8,s88
s_cselect_b32 s89,-1,s89
s_mul_i32 s58,s61,2560
s_add_u32 s58,s58,109891584
s_add_u32 m0,s59,2048
s_nop 0
buffer_load_dword v8,s[36:39],s58 offen offset:0 sc1 lds
s_add_u32 m0,s57,28672
s_add_u32 s56,s55,28672
D 0,56,0
D 0,56,1024
D 0,56,2048
D 0,56,3072
s_add_u32 m0,s57,32768
s_add_u32 s56,s56,4096
D 0,56,0
D 0,56,1024
D 0,56,2048
s_mul_i32 s55,s61,114688
s_add_u32 s55,s55,s60
s_sub_u32 s57,s84,s57
s_mov_b32 s54,s61
s_waitcnt vmcnt(7)
s_barrier
buffer_store_dword v4,v3,s[36:39],s70 offen offset:0 sc0 sc1
s_min_u32 s71,s71,s72
buffer_store_dword v4,v3,s[36:39],s71 offen offset:0 sc0 sc1
s_mov_b32 s70,0x7fff0000
s_mov_b32 s71,0x7fff0000
s_mov_b32 s72,0x7fff0000
s_cmp_eq_u32 s80,1
s_cbranch_scc1 Lg4_st1_%=
s_branch Lg4_st0_%=
Lg4_gox_%=:
s_add_u32 m0,s59,2048
s_nop 0
buffer_load_dword v8,s[36:39],s58 offen offset:0 sc1 lds
s_waitcnt vmcnt(0)
s_barrier
buffer_store_dword v4,v3,s[36:39],s70 offen offset:0 sc0 sc1
s_min_u32 s71,s71,s72
buffer_store_dword v4,v3,s[36:39],s71 offen offset:0 sc0 sc1
s_mov_b32 s70,0x7fff0000
s_mov_b32 s71,0x7fff0000
s_mov_b32 s72,0x7fff0000
s_cmp_eq_u32 s80,1
s_cbranch_scc1 Lg4_st1_%=
Lg4_st0_%=:
ds_read_b32 v7,v6 offset:2048
R 26,1,0
R 30,1,1024
R 34,1,2048
R 38,1,3072
R 42,1,4096
L 5
v_cmp_eq_u32 vcc,s53,v7
L 3
s_cmp_eq_u32 vcc_lo,-1
s_cbranch_scc1 Lg4_F0_%=
s_branch Lg4_L0_%=
Lg4_st1_%=:
ds_read_b32 v7,v6 offset:2048
R 26,2,0
R 30,2,1024
R 34,2,2048
R 38,2,3072
R 42,2,4096
L 5
v_cmp_eq_u32 vcc,s53,v7
L 3
s_cmp_eq_u32 vcc_lo,-1
s_cbranch_scc1 Lg4_F1_%=
s_branch Lg4_L1_%=
Lg4_F0_%=:
v_mfma_f32_16x16x32_bf16 v[14:17],a[4:7],v[26:29],0
s_add_u32 m0,s57,0
R 46,1,5120
v_lshlrev_b32 v62,16,v58
M 14,a[12:15],30
s_add_u32 s56,s55,0
v_add_f32 v18,v18,v62
v_and_b32 v63,0xffff0000,v58
v_mfma_f32_16x16x32_bf16 v[10:13],a[0:3],v[26:29],0
s_add_u32 s61,s54,s46
R 50,1,6144
v_add_f32 v19,v19,v63
M 10,a[8:11],30
L 3
D 0,56,0
)"
R"(M 10,a[16:19],34
s_cmp_ge_u32 s61,s63
R 54,1,7168
v_lshlrev_b32 v62,16,v59
M 10,a[24:27],38
s_cselect_b32 s61,s93,s61
v_add_f32 v20,v20,v62
v_and_b32 v63,0xffff0000,v59
M 14,a[20:23],34
s_cselect_b32 s46,s88,s46
R 26,1,8192
v_add_f32 v21,v21,v63
M 14,a[28:31],38
L 3
v_lshlrev_b32 v62,16,v60
v_add_f32 v22,v22,v62
M 14,a[36:39],42
s_cselect_b32 s63,s89,s63
R 30,1,9216
v_and_b32 v63,0xffff0000,v60
M 14,a[44:47],46
D 0,56,1024
M 10,a[32:35],42
s_cselect_b32 s93,s50,s93
R 34,1,10240
v_add_f32 v23,v23,v63
M 10,a[40:43],46
L 3
v_lshlrev_b32 v62,16,v61
v_add_f32 v24,v24,v62
M 10,a[48:51],50
s_cselect_b32 s88,8,s88
R 38,1,11264
v_and_b32 v63,0xffff0000,v61
M 10,a[56:59],54
buffer_load_dwordx4 v[58:61],v0,s[36:39],s74 offen offset:0 sc1
M 14,a[52:55],50
s_cselect_b32 s89,-1,s89
R 42,1,12288
v_add_f32 v25,v25,v63
M 14,a[60:63],54
L 3
v_cvt_pk_bf16_f32 v18,v18,v19
v_cvt_pk_bf16_f32 v19,v20,v21
M 14,a[68:71],26
s_mul_i32 s58,s61,2560
R 46,1,13312
v_cvt_pk_bf16_f32 v20,v22,v23
M 14,a[76:79],30
D 0,56,2048
M 10,a[64:67],26
s_add_u32 s58,s58,109891584
R 50,1,14336
v_cvt_pk_bf16_f32 v21,v24,v25
M 10,a[72:75],30
L 3
buffer_store_dwordx4 v[18:21],v0,s[36:39],s67 offen offset:0 sc0 sc1
M 10,a[80:83],34
s_lshl_b32 s62,s54,15
s_mov_b32 s66,s65
R 54,1,15360
M 10,a[88:91],38
s_cmp_ge_u32 s66,s45
s_cselect_b32 s51,1,-1
s_add_u32 s74,s49,s62
M 14,a[84:87],34
s_add_u32 s65,s47,s62
s_mov_b32 s54,s61
R 26,1,16384
M 14,a[92:95],38
L 3
M 14,a[100:103],42
R 30,1,17408
M 14,a[108:111],46
D 0,56,3072
M 10,a[96:99],42
s_add_u32 m0,s57,4096
s_add_u32 s56,s56,4096
R 34,1,18432
M 10,a[104:107],46
L 3
M 10,a[112:115],50
R 38,1,19456
M 10,a[120:123],54
M 14,a[116:119],50
R 42,1,20480
M 14,a[124:127],54
L 3
D 0,56,0
M 14,a[132:135],26
R 46,1,21504
M 14,a[140:143],30
M 10,a[128:131],26
R 50,1,22528
M 10,a[136:139],30
L 3
M 10,a[144:147],34
R 54,1,23552
M 10,a[152:155],38
M 14,a[148:151],34
R 26,1,24576
M 14,a[156:159],38
L 3
D 0,56,1024
M 14,a[164:167],42
R 30,1,25600
M 14,a[172:175],46
M 10,a[160:163],42
R 34,1,26624
M 10,a[168:171],46
L 3
M 10,a[176:179],50
s_waitcnt vmcnt(9)
R 38,1,27648
M 10,a[184:187],54
s_barrier
M 14,a[180:183],50
R 42,1,28672
M 14,a[188:191],54
L 3
D 0,56,2048
M 14,a[196:199],26
s_add_u32 m0,s59,0
s_add_u32 s56,s55,28672
R 46,1,29696
M 14,a[204:207],30
s_mul_i32 s55,s61,114688
s_add_u32 s55,s55,s60
M 10,a[192:195],26
R 50,1,30720
M 10,a[200:203],30
L 3
M 10,a[208:211],34
R 54,1,31744
M 10,a[216:219],38
M 14,a[212:215],34
R 26,1,32768
M 14,a[220:223],38
L 3
buffer_load_dword v8,s[36:39],s58 offen offset:0 sc1 lds
M 14,a[228:231],42
s_add_u32 m0,s57,28672
R 30,1,33792
M 14,a[236:239],46
D 0,56,0
M 10,a[224:227],42
R 34,1,34816
M 10,a[232:235],46
L 3
M 10,a[240:243],50
R 38,1,35840
M 10,a[248:251],54
M 14,a[244:247],50
R 42,1,36864
M 14,a[252:255],54
L 3
D 0,56,1024
M 14,v[68:71],26
R 46,1,37888
M 14,v[76:79],30
M 10,v[64:67],26
R 50,1,38912
M 10,v[72:75],30
L 3
M 10,v[80:83],34
R 54,1,39936
M 10,v[88:91],38
D 0,56,2048
M 14,v[84:87],34
R 26,1,40960
M 14,v[92:95],38
L 3
M 14,v[100:103],42
R 30,1,41984
M 14,v[108:111],46
M 10,v[96:99],42
R 34,1,43008
M 10,v[104:107],46
L 3
D 0,56,3072
M 10,v[112:115],50
s_add_u32 m0,s57,32768
s_add_u32 s56,s56,4096
R 38,1,44032
M 10,v[120:123],54
s_sub_u32 s57,s84,s57
M 14,v[116:119],50
R 42,1,45056
M 14,v[124:127],54
L 3
M 14,v[132:135],26
R 46,1,46080
M 14,v[140:143],30
D 0,56,0
M 10,v[128:131],26
R 50,1,47104
M 10,v[136:139],30
L 3
M 10,v[144:147],34
R 54,1,48128
M 10,v[152:155],38
M 14,v[148:151],34
R 26,1,49152
M 14,v[156:159],38
L 3
D 0,56,1024
M 14,v[164:167],42
R 30,1,50176
M 14,v[172:175],46
M 10,v[160:163],42
R 34,1,51200
M 10,v[168:171],46
L 3
M 10,v[176:179],50
R 38,1,52224
M 10,v[184:187],54
D 0,56,2048
M 14,v[180:183],50
R 42,1,53248
M 14,v[188:191],54
L 3
M 14,v[196:199],26
R 46,1,54272
M 14,v[204:207],30
M 10,v[192:195],26
R 50,1,55296
M 10,v[200:203],30
L 3
M 10,v[208:211],34
s_waitcnt vmcnt(7)
R 54,1,56320
M 10,v[216:219],38
s_barrier
ds_read_b32 v7,v6 offset:0
M 14,v[212:215],34
R 26,2,0
M 14,v[220:223],38
L 4
buffer_store_dword v4,v3,s[36:39],s69 offen offset:0 sc0 sc1
M 14,v[228:231],42
s_mov_b32 s69,s68
s_lshr_b32 s68,s62,4
R 30,2,1024
M 14,v[236:239],46
s_add_u32 s68,s68,s48
M 10,v[224:227],42
R 34,2,2048
M 10,v[232:235],46
L 4
L 3
M 10,v[240:243],50
R 38,2,3072
v_cmp_eq_u32 vcc,s53,v7
M 10,v[248:251],54
M 14,v[244:247],50
L 2
s_cmp_eq_u32 vcc_lo,s51
R 42,2,4096
M 14,v[252:255],54
s_cbranch_scc0 Lg4_K1_%=
Lg4_F1_%=:
v_mfma_f32_16x16x32_bf16 v[22:25],a[4:7],v[26:29],0
s_add_u32 m0,s57,0
R 46,2,5120
v_lshlrev_b32 v62,16,v58
M 22,a[12:15],30
s_add_u32 s56,s55,0
v_add_f32 v10,v10,v62
v_and_b32 v63,0xffff0000,v58
v_mfma_f32_16x16x32_bf16 v[18:21],a[0:3],v[26:29],0
s_add_u32 s61,s54,s46
R 50,2,6144
v_add_f32 v11,v11,v63
M 18,a[8:11],30
L 3
D 0,56,0
M 18,a[16:19],34
s_cmp_ge_u32 s61,s63
R 54,2,7168
v_lshlrev_b32 v62,16,v59
M 18,a[24:27],38
s_cselect_b32 s61,s93,s61
v_add_f32 v12,v12,v62
v_and_b32 v63,0xffff0000,v59
M 22,a[20:23],34
s_cselect_b32 s46,s88,s46
R 26,2,8192
v_add_f32 v13,v13,v63
M 22,a[28:31],38
L 3
v_lshlrev_b32 v62,16,v60
v_add_f32 v14,v14,v62
M 22,a[36:39],42
s_cselect_b32 s63,s89,s63
R 30,2,9216
v_and_b32 v63,0xffff0000,v60
M 22,a[44:47],46
D 0,56,1024
M 18,a[32:35],42
s_cselect_b32 s93,s50,s93
R 34,2,10240
v_add_f32 v15,v15,v63
M 18,a[40:43],46
L 3
v_lshlrev_b32 v62,16,v61
v_add_f32 v16,v16,v62
M 18,a[48:51],50
s_cselect_b32 s88,8,s88
R 38,2,11264
v_and_b32 v63,0xffff0000,v61
M 18,a[56:59],54
buffer_load_dwordx4 v[58:61],v0,s[36:39],s74 offen offset:0 sc1
M 22,a[52:55],50
s_cselect_b32 s89,-1,s89
R 42,2,12288
v_add_f32 v17,v17,v63
M 22,a[60:63],54
L 3
v_cvt_pk_bf16_f32 v10,v10,v11
v_cvt_pk_bf16_f32 v11,v12,v13
M 22,a[68:71],26
s_mul_i32 s58,s61,2560
R 46,2,13312
v_cvt_pk_bf16_f32 v12,v14,v15
M 22,a[76:79],30
D 0,56,2048
M 18,a[64:67],26
s_add_u32 s58,s58,109891584
R 50,2,14336
v_cvt_pk_bf16_f32 v13,v16,v17
M 18,a[72:75],30
L 3
buffer_store_dwordx4 v[10:13],v0,s[36:39],s66 offen offset:0 sc0 sc1
M 18,a[80:83],34
s_lshl_b32 s62,s54,15
s_mov_b32 s67,s65
R 54,2,15360
M 18,a[88:91],38
s_cmp_ge_u32 s67,s45
s_cselect_b32 s51,1,-1
s_add_u32 s74,s49,s62
M 22,a[84:87],34
s_add_u32 s65,s47,s62
s_mov_b32 s54,s61
R 26,2,16384
M 22,a[92:95],38
L 3
M 22,a[100:103],42
R 30,2,17408
M 22,a[108:111],46
D 0,56,3072
M 18,a[96:99],42
s_add_u32 m0,s57,4096
s_add_u32 s56,s56,4096
R 34,2,18432
M 18,a[104:107],46
L 3
M 18,a[112:115],50
R 38,2,19456
M 18,a[120:123],54
M 22,a[116:119],50
R 42,2,20480
M 22,a[124:127],54
L 3
D 0,56,0
M 22,a[132:135],26
R 46,2,21504
M 22,a[140:143],30
M 18,a[128:131],26
R 50,2,22528
M 18,a[136:139],30
L 3
M 18,a[144:147],34
R 54,2,23552
M 18,a[152:155],38
M 22,a[148:151],34
R 26,2,24576
M 22,a[156:159],38
L 3
D 0,56,1024
M 22,a[164:167],42
R 30,2,25600
M 22,a[172:175],46
M 18,a[160:163],42
R 34,2,26624
M 18,a[168:171],46
L 3
M 18,a[176:179],50
s_waitcnt vmcnt(9)
R 38,2,27648
M 18,a[184:187],54
s_barrier
M 22,a[180:183],50
R 42,2,28672
M 22,a[188:191],54
L 3
D 0,56,2048
M 22,a[196:199],26
s_add_u32 m0,s59,1024
s_add_u32 s56,s55,28672
R 46,2,29696
M 22,a[204:207],30
s_mul_i32 s55,s61,114688
s_add_u32 s55,s55,s60
M 18,a[192:195],26
R 50,2,30720
M 18,a[200:203],30
L 3
M 18,a[208:211],34
R 54,2,31744
M 18,a[216:219],38
M 22,a[212:215],34
R 26,2,32768
M 22,a[220:223],38
L 3
buffer_load_dword v8,s[36:39],s58 offen offset:0 sc1 lds
M 22,a[228:231],42
s_add_u32 m0,s57,28672
R 30,2,33792
M 22,a[236:239],46
D 0,56,0
M 18,a[224:227],42
R 34,2,34816
M 18,a[232:235],46
L 3
M 18,a[240:243],50
R 38,2,35840
M 18,a[248:251],54
M 22,a[244:247],50
R 42,2,36864
M 22,a[252:255],54
L 3
D 0,56,1024
M 22,v[68:71],26
R 46,2,37888
M 22,v[76:79],30
M 18,v[64:67],26
R 50,2,38912
M 18,v[72:75],30
L 3
M 18,v[80:83],34
R 54,2,39936
M 18,v[88:91],38
D 0,56,2048
M 22,v[84:87],34
R 26,2,40960
M 22,v[92:95],38
L 3
M 22,v[100:103],42
R 30,2,41984
M 22,v[108:111],46
M 18,v[96:99],42
R 34,2,43008
M 18,v[104:107],46
L 3
D 0,56,3072
M 18,v[112:115],50
s_add_u32 m0,s57,32768
s_add_u32 s56,s56,4096
R 38,2,44032
M 18,v[120:123],54
s_sub_u32 s57,s84,s57
M 22,v[116:119],50
R 42,2,45056
M 22,v[124:127],54
L 3
M 22,v[132:135],26
R 46,2,46080
M 22,v[140:143],30
D 0,56,0
M 18,v[128:131],26
R 50,2,47104
M 18,v[136:139],30
L 3
M 18,v[144:147],34
R 54,2,48128
M 18,v[152:155],38
M 22,v[148:151],34
R 26,2,49152
M 22,v[156:159],38
L 3
D 0,56,1024
M 22,v[164:167],42
R 30,2,50176
M 22,v[172:175],46
M 18,v[160:163],42
R 34,2,51200
M 18,v[168:171],46
L 3
M 18,v[176:179],50
R 38,2,52224
M 18,v[184:187],54
D 0,56,2048
M 22,v[180:183],50
R 42,2,53248
M 22,v[188:191],54
L 3
M 22,v[196:199],26
R 46,2,54272
M 22,v[204:207],30
)"
R"(M 18,v[192:195],26
R 50,2,55296
M 18,v[200:203],30
L 3
M 18,v[208:211],34
s_waitcnt vmcnt(7)
R 54,2,56320
M 18,v[216:219],38
s_barrier
ds_read_b32 v7,v6 offset:1024
M 22,v[212:215],34
R 26,1,0
M 22,v[220:223],38
L 4
buffer_store_dword v4,v3,s[36:39],s69 offen offset:0 sc0 sc1
M 22,v[228:231],42
s_mov_b32 s69,s68
s_lshr_b32 s68,s62,4
R 30,1,1024
M 22,v[236:239],46
s_add_u32 s68,s68,s48
M 18,v[224:227],42
R 34,1,2048
M 18,v[232:235],46
L 4
L 3
M 18,v[240:243],50
R 38,1,3072
v_cmp_eq_u32 vcc,s53,v7
M 18,v[248:251],54
M 22,v[244:247],50
L 2
s_cmp_eq_u32 vcc_lo,s51
R 42,1,4096
M 22,v[252:255],54
s_cbranch_scc1 Lg4_F0_%=
Lg4_K0_%=:
s_cmp_eq_u32 s51,1
s_cbranch_scc0 Lg4_L0_%=
s_cmp_eq_u32 vcc_lo,-1
s_cbranch_scc1 Lg4_Fa0_%=
s_branch Lg4_La0_%=
Lg4_L0_%=:
v_mfma_f32_16x16x32_bf16 v[14:17],a[4:7],v[26:29],0
s_add_u32 m0,s59,3072
R 46,1,5120
v_lshlrev_b32 v62,16,v58
M 14,a[12:15],30
buffer_load_dword v8,s[36:39],s58 offen offset:0 sc1 lds
v_mfma_f32_16x16x32_bf16 v[10:13],a[0:3],v[26:29],0
s_mov_b32 s66,s65
R 50,1,6144
v_add_f32 v18,v18,v62
M 10,a[8:11],30
L 3
v_and_b32 v63,0xffff0000,v58
v_add_f32 v19,v19,v63
M 10,a[16:19],34
s_lshl_b32 s62,s54,15
R 54,1,7168
v_lshlrev_b32 v62,16,v59
M 10,a[24:27],38
s_cmp_ge_u32 s66,s45
v_add_f32 v20,v20,v62
v_and_b32 v63,0xffff0000,v59
M 14,a[20:23],34
s_cselect_b32 s51,1,-1
R 26,1,8192
v_add_f32 v21,v21,v63
M 14,a[28:31],38
L 3
v_lshlrev_b32 v62,16,v60
v_add_f32 v22,v22,v62
M 14,a[36:39],42
s_add_u32 s65,s47,s62
R 30,1,9216
v_and_b32 v63,0xffff0000,v60
M 14,a[44:47],46
v_add_f32 v23,v23,v63
v_lshlrev_b32 v62,16,v61
M 10,a[32:35],42
R 34,1,10240
v_add_f32 v24,v24,v62
M 10,a[40:43],46
L 3
v_and_b32 v63,0xffff0000,v61
v_add_f32 v25,v25,v63
M 10,a[48:51],50
R 38,1,11264
v_cvt_pk_bf16_f32 v18,v18,v19
M 10,a[56:59],54
buffer_load_dwordx4 v[58:61],v0,s[36:39],s74 offen offset:0 sc1
M 14,a[52:55],50
s_add_u32 s74,s49,s62
R 42,1,12288
v_cvt_pk_bf16_f32 v19,v20,v21
M 14,a[60:63],54
L 3
v_cvt_pk_bf16_f32 v20,v22,v23
v_cvt_pk_bf16_f32 v21,v24,v25
M 14,a[68:71],26
R 46,1,13312
M 14,a[76:79],30
buffer_store_dwordx4 v[18:21],v0,s[36:39],s67 offen offset:0 sc0 sc1
M 10,a[64:67],26
R 50,1,14336
M 10,a[72:75],30
L 3
M 10,a[80:83],34
R 54,1,15360
M 10,a[88:91],38
M 14,a[84:87],34
R 26,1,16384
M 14,a[92:95],38
L 3
M 14,a[100:103],42
R 30,1,17408
M 14,a[108:111],46
M 10,a[96:99],42
R 34,1,18432
M 10,a[104:107],46
L 3
M 10,a[112:115],50
R 38,1,19456
M 10,a[120:123],54
M 14,a[116:119],50
R 42,1,20480
M 14,a[124:127],54
L 3
M 14,a[132:135],26
R 46,1,21504
M 14,a[140:143],30
M 10,a[128:131],26
R 50,1,22528
M 10,a[136:139],30
L 3
M 10,a[144:147],34
R 54,1,23552
M 10,a[152:155],38
M 14,a[148:151],34
R 26,1,24576
M 14,a[156:159],38
L 3
M 14,a[164:167],42
R 30,1,25600
M 14,a[172:175],46
M 10,a[160:163],42
R 34,1,26624
M 10,a[168:171],46
L 3
M 10,a[176:179],50
s_waitcnt vmcnt(2)
R 38,1,27648
M 10,a[184:187],54
s_barrier
ds_read_b32 v7,v6 offset:3072
M 14,a[180:183],50
R 42,1,28672
M 14,a[188:191],54
L 4
M 14,a[196:199],26
R 46,1,29696
M 14,a[204:207],30
M 10,a[192:195],26
R 50,1,30720
M 10,a[200:203],30
L 4
L 3
v_cmp_eq_u32 vcc,s53,v7
M 10,a[208:211],34
R 54,1,31744
M 10,a[216:219],38
M 14,a[212:215],34
s_cmp_eq_u32 vcc_lo,-1
R 26,1,32768
s_cbranch_scc0 Lg4_X0_%=
M 14,a[220:223],38
L 3
s_add_u32 m0,s57,0
s_add_u32 s56,s55,0
M 14,a[228:231],42
s_add_u32 s61,s54,s46
s_cmp_ge_u32 s61,s63
s_cselect_b32 s61,s93,s61
R 30,1,33792
M 14,a[236:239],46
s_mul_i32 s58,s61,2560
D 0,56,0
M 10,a[224:227],42
s_cselect_b32 s46,s88,s46
s_cselect_b32 s63,s89,s63
R 34,1,34816
M 10,a[232:235],46
L 3
s_cselect_b32 s93,s50,s93
s_cselect_b32 s88,8,s88
M 10,a[240:243],50
s_cselect_b32 s89,-1,s89
s_add_u32 s58,s58,109891584
R 38,1,35840
M 10,a[248:251],54
D 0,56,1024
M 14,a[244:247],50
s_mov_b32 s54,s61
R 42,1,36864
M 14,a[252:255],54
L 3
M 14,v[68:71],26
R 46,1,37888
M 14,v[76:79],30
D 0,56,2048
M 10,v[64:67],26
R 50,1,38912
M 10,v[72:75],30
L 3
M 10,v[80:83],34
R 54,1,39936
M 10,v[88:91],38
D 0,56,3072
M 14,v[84:87],34
s_add_u32 m0,s57,4096
s_add_u32 s56,s56,4096
R 26,1,40960
M 14,v[92:95],38
L 3
M 14,v[100:103],42
R 30,1,41984
M 14,v[108:111],46
D 0,56,0
M 10,v[96:99],42
R 34,1,43008
M 10,v[104:107],46
L 3
M 10,v[112:115],50
R 38,1,44032
M 10,v[120:123],54
D 0,56,1024
M 14,v[116:119],50
R 42,1,45056
M 14,v[124:127],54
L 3
M 14,v[132:135],26
R 46,1,46080
M 14,v[140:143],30
D 0,56,2048
M 10,v[128:131],26
s_add_u32 m0,s59,0
s_add_u32 s56,s55,28672
R 50,1,47104
M 10,v[136:139],30
L 3
buffer_load_dword v8,s[36:39],s58 offen offset:0 sc1 lds
M 10,v[144:147],34
s_add_u32 m0,s57,28672
s_mul_i32 s55,s61,114688
R 54,1,48128
M 10,v[152:155],38
s_add_u32 s55,s55,s60
M 14,v[148:151],34
R 26,1,49152
M 14,v[156:159],38
L 3
D 0,56,0
M 14,v[164:167],42
R 30,1,50176
M 14,v[172:175],46
M 10,v[160:163],42
R 34,1,51200
M 10,v[168:171],46
L 3
D 0,56,1024
M 10,v[176:179],50
R 38,1,52224
M 10,v[184:187],54
D 0,56,2048
M 14,v[180:183],50
R 42,1,53248
M 14,v[188:191],54
L 3
M 14,v[196:199],26
R 46,1,54272
M 14,v[204:207],30
D 0,56,3072
M 10,v[192:195],26
s_add_u32 m0,s57,32768
s_add_u32 s56,s56,4096
R 50,1,55296
M 10,v[200:203],30
L 3
s_sub_u32 s57,s84,s57
M 10,v[208:211],34
s_waitcnt vmcnt(4)
R 54,1,56320
M 10,v[216:219],38
s_barrier
ds_read_b32 v7,v6 offset:0
M 14,v[212:215],34
R 26,2,0
M 14,v[220:223],38
L 4
buffer_store_dword v4,v3,s[36:39],s69 offen offset:0 sc0 sc1
M 14,v[228:231],42
s_mov_b32 s69,s68
s_lshr_b32 s68,s62,4
R 30,2,1024
M 14,v[236:239],46
D 0,56,0
M 10,v[224:227],42
s_add_u32 s68,s68,s48
R 34,2,2048
M 10,v[232:235],46
L 4
D 0,56,1024
M 10,v[240:243],50
L 3
R 38,2,3072
v_cmp_eq_u32 vcc,s53,v7
M 10,v[248:251],54
D 0,56,2048
M 14,v[244:247],50
L 2
s_cmp_eq_u32 vcc_lo,s51
R 42,2,4096
M 14,v[252:255],54
buffer_store_dword v4,v3,s[36:39],s71 offen offset:0 sc0 sc1
s_cbranch_scc1 Lg4_F1_%=
s_branch Lg4_K1_%=
Lg4_X0_%=:
M 14,a[220:223],38
L 3
s_add_u32 m0,s59,0
M 14,a[228:231],42
R 30,1,33792
M 14,a[236:239],46
buffer_load_dword v8,s[36:39],s58 offen offset:0 sc1 lds
M 10,a[224:227],42
R 34,1,34816
M 10,a[232:235],46
L 3
M 10,a[240:243],50
R 38,1,35840
M 10,a[248:251],54
M 14,a[244:247],50
R 42,1,36864
M 14,a[252:255],54
L 3
M 14,v[68:71],26
R 46,1,37888
M 14,v[76:79],30
M 10,v[64:67],26
R 50,1,38912
M 10,v[72:75],30
L 3
M 10,v[80:83],34
R 54,1,39936
M 10,v[88:91],38
M 14,v[84:87],34
R 26,1,40960
M 14,v[92:95],38
L 3
M 14,v[100:103],42
R 30,1,41984
M 14,v[108:111],46
M 10,v[96:99],42
R 34,1,43008
M 10,v[104:107],46
L 3
M 10,v[112:115],50
R 38,1,44032
M 10,v[120:123],54
M 14,v[116:119],50
R 42,1,45056
M 14,v[124:127],54
L 3
M 14,v[132:135],26
R 46,1,46080
M 14,v[140:143],30
M 10,v[128:131],26
R 50,1,47104
M 10,v[136:139],30
L 3
M 10,v[144:147],34
R 54,1,48128
M 10,v[152:155],38
M 14,v[148:151],34
R 26,1,49152
M 14,v[156:159],38
L 3
M 14,v[164:167],42
R 30,1,50176
M 14,v[172:175],46
M 10,v[160:163],42
R 34,1,51200
M 10,v[168:171],46
L 3
M 10,v[176:179],50
R 38,1,52224
M 10,v[184:187],54
M 14,v[180:183],50
R 42,1,53248
M 14,v[188:191],54
L 3
M 14,v[196:199],26
R 46,1,54272
M 14,v[204:207],30
M 10,v[192:195],26
R 50,1,55296
M 10,v[200:203],30
L 3
M 10,v[208:211],34
s_waitcnt vmcnt(0)
R 54,1,56320
M 10,v[216:219],38
s_barrier
buffer_store_dword v4,v3,s[36:39],s69 offen offset:0 sc0 sc1
M 14,v[212:215],34
s_mov_b32 s69,s68
s_lshr_b32 s68,s62,4
ds_read_b32 v7,v6 offset:0
M 14,v[220:223],38
L 3
s_add_u32 s68,s68,s48
M 14,v[228:231],42
M 14,v[236:239],46
M 10,v[224:227],42
M 10,v[232:235],46
L 1
M 10,v[240:243],50
L 0
v_cmp_eq_u32 vcc,s53,v7
M 10,v[248:251],54
M 14,v[244:247],50
M 14,v[252:255],54
s_cmp_eq_u32 s51,1
s_cbranch_scc1 Lg4_pa0_%=
s_mov_b32 s80,1
s_cmp_eq_u32 vcc_lo,-1
s_cbranch_scc0 Lg4_xn0_%=
s_add_u32 m0,s57,0
s_add_u32 s56,s55,0
D 0,56,0
v_lshlrev_b32 v62,16,v58
D 0,56,1024
v_add_f32 v10,v10,v62
D 0,56,2048
v_and_b32 v63,0xffff0000,v58
D 0,56,3072
v_add_f32 v11,v11,v63
s_add_u32 m0,s57,4096
s_add_u32 s56,s56,4096
D 0,56,0
v_lshlrev_b32 v62,16,v59
D 0,56,1024
v_add_f32 v12,v12,v62
D 0,56,2048
v_and_b32 v63,0xffff0000,v59
s_add_u32 s61,s54,s46
s_cmp_ge_u32 s61,s63
s_cselect_b32 s61,s93,s61
s_cselect_b32 s46,s88,s46
s_cselect_b32 s63,s89,s63
s_cselect_b32 s93,s50,s93
s_cselect_b32 s88,8,s88
s_cselect_b32 s89,-1,s89
s_mul_i32 s58,s61,2560
s_add_u32 s58,s58,109891584
s_add_u32 m0,s57,28672
s_add_u32 s56,s55,28672
D 0,56,0
v_add_f32 v13,v13,v63
D 0,56,1024
v_lshlrev_b32 v62,16,v60
D 0,56,2048
v_add_f32 v14,v14,v62
D 0,56,3072
v_and_b32 v63,0xffff0000,v60
s_add_u32 m0,s57,32768
s_add_u32 s56,s56,4096
D 0,56,0
v_add_f32 v15,v15,v63
D 0,56,1024
v_lshlrev_b32 v62,16,v61
D 0,56,2048
v_add_f32 v16,v16,v62
s_mul_i32 s55,s61,114688
s_add_u32 s55,s55,s60
s_sub_u32 s57,s84,s57
s_mov_b32 s54,s61
v_and_b32 v63,0xffff0000,v61
v_add_f32 v17,v17,v63
v_cvt_pk_bf16_f32 v10,v10,v11
v_cvt_pk_bf16_f32 v11,v12,v13
v_cvt_pk_bf16_f32 v12,v14,v15
v_cvt_pk_bf16_f32 v13,v16,v17
buffer_store_dwordx4 v[10:13],v0,s[36:39],s66 offen offset:0 sc0 sc1
s_mov_b32 s72,s69
s_mov_b32 s69,0x7fff0000
s_mov_b32 s66,0x7fff0000
s_mov_b32 s76,0x7fff0000
s_branch Lg4_gox_%=
)"
R"(Lg4_xn0_%=:
s_cmp_ge_u32 s54,256
s_cbranch_scc1 Lg4_fin0_%=
s_add_u32 m0,s59,4096
s_nop 0
buffer_load_dword v8,s[36:39],s58 offen offset:0 sc1 lds
buffer_store_dword v4,v3,s[36:39],s71 offen offset:0 sc0 sc1
s_add_u32 m0,s59,5120
s_nop 0
buffer_load_dword v8,s[36:39],s58 offen offset:0 sc1 lds
buffer_store_dword v4,v3,s[36:39],s71 offen offset:0 sc0 sc1
s_mov_b32 s83,0
v_lshlrev_b32 v62,16,v58
v_add_f32 v10,v10,v62
v_and_b32 v63,0xffff0000,v58
v_add_f32 v11,v11,v63
v_lshlrev_b32 v62,16,v59
v_add_f32 v12,v12,v62
v_and_b32 v63,0xffff0000,v59
v_add_f32 v13,v13,v63
v_lshlrev_b32 v62,16,v60
v_add_f32 v14,v14,v62
v_and_b32 v63,0xffff0000,v60
v_add_f32 v15,v15,v63
v_lshlrev_b32 v62,16,v61
v_add_f32 v16,v16,v62
v_and_b32 v63,0xffff0000,v61
v_add_f32 v17,v17,v63
v_cvt_pk_bf16_f32 v10,v10,v11
v_cvt_pk_bf16_f32 v11,v12,v13
v_cvt_pk_bf16_f32 v12,v14,v15
v_cvt_pk_bf16_f32 v13,v16,v17
buffer_store_dwordx4 v[10:13],v0,s[36:39],s66 offen offset:0 sc0 sc1
s_mov_b32 s72,s69
s_mov_b32 s69,0x7fff0000
s_mov_b32 s66,0x7fff0000
s_mov_b32 s76,0x7fff0000
s_branch Lg4_poll_%=
Lg4_fin0_%=:
v_lshlrev_b32 v62,16,v58
v_add_f32 v10,v10,v62
v_and_b32 v63,0xffff0000,v58
v_add_f32 v11,v11,v63
v_lshlrev_b32 v62,16,v59
v_add_f32 v12,v12,v62
v_and_b32 v63,0xffff0000,v59
v_add_f32 v13,v13,v63
v_lshlrev_b32 v62,16,v60
v_add_f32 v14,v14,v62
v_and_b32 v63,0xffff0000,v60
v_add_f32 v15,v15,v63
v_lshlrev_b32 v62,16,v61
v_add_f32 v16,v16,v62
v_and_b32 v63,0xffff0000,v61
v_add_f32 v17,v17,v63
v_cvt_pk_bf16_f32 v10,v10,v11
v_cvt_pk_bf16_f32 v11,v12,v13
v_cvt_pk_bf16_f32 v12,v14,v15
v_cvt_pk_bf16_f32 v13,v16,v17
buffer_store_dwordx4 v[10:13],v0,s[36:39],s66 offen offset:0 sc0 sc1
s_mov_b32 s72,s69
s_mov_b32 s69,0x7fff0000
s_mov_b32 s66,0x7fff0000
s_mov_b32 s76,0x7fff0000
s_branch Lg4_done_%=
Lg4_K1_%=:
s_cmp_eq_u32 s51,1
s_cbranch_scc0 Lg4_L1_%=
s_cmp_eq_u32 vcc_lo,-1
s_cbranch_scc1 Lg4_Fa1_%=
s_branch Lg4_La1_%=
Lg4_L1_%=:
v_mfma_f32_16x16x32_bf16 v[22:25],a[4:7],v[26:29],0
s_add_u32 m0,s59,3072
R 46,2,5120
v_lshlrev_b32 v62,16,v58
M 22,a[12:15],30
buffer_load_dword v8,s[36:39],s58 offen offset:0 sc1 lds
v_mfma_f32_16x16x32_bf16 v[18:21],a[0:3],v[26:29],0
s_mov_b32 s67,s65
R 50,2,6144
v_add_f32 v10,v10,v62
M 18,a[8:11],30
L 3
v_and_b32 v63,0xffff0000,v58
v_add_f32 v11,v11,v63
M 18,a[16:19],34
s_lshl_b32 s62,s54,15
R 54,2,7168
v_lshlrev_b32 v62,16,v59
M 18,a[24:27],38
s_cmp_ge_u32 s67,s45
v_add_f32 v12,v12,v62
v_and_b32 v63,0xffff0000,v59
M 22,a[20:23],34
s_cselect_b32 s51,1,-1
R 26,2,8192
v_add_f32 v13,v13,v63
M 22,a[28:31],38
L 3
v_lshlrev_b32 v62,16,v60
v_add_f32 v14,v14,v62
M 22,a[36:39],42
s_add_u32 s65,s47,s62
R 30,2,9216
v_and_b32 v63,0xffff0000,v60
M 22,a[44:47],46
v_add_f32 v15,v15,v63
v_lshlrev_b32 v62,16,v61
M 18,a[32:35],42
R 34,2,10240
v_add_f32 v16,v16,v62
M 18,a[40:43],46
L 3
v_and_b32 v63,0xffff0000,v61
v_add_f32 v17,v17,v63
M 18,a[48:51],50
R 38,2,11264
v_cvt_pk_bf16_f32 v10,v10,v11
M 18,a[56:59],54
buffer_load_dwordx4 v[58:61],v0,s[36:39],s74 offen offset:0 sc1
M 22,a[52:55],50
s_add_u32 s74,s49,s62
R 42,2,12288
v_cvt_pk_bf16_f32 v11,v12,v13
M 22,a[60:63],54
L 3
v_cvt_pk_bf16_f32 v12,v14,v15
v_cvt_pk_bf16_f32 v13,v16,v17
M 22,a[68:71],26
R 46,2,13312
M 22,a[76:79],30
buffer_store_dwordx4 v[10:13],v0,s[36:39],s66 offen offset:0 sc0 sc1
M 18,a[64:67],26
R 50,2,14336
M 18,a[72:75],30
L 3
M 18,a[80:83],34
R 54,2,15360
M 18,a[88:91],38
M 22,a[84:87],34
R 26,2,16384
M 22,a[92:95],38
L 3
M 22,a[100:103],42
R 30,2,17408
M 22,a[108:111],46
M 18,a[96:99],42
R 34,2,18432
M 18,a[104:107],46
L 3
M 18,a[112:115],50
R 38,2,19456
M 18,a[120:123],54
M 22,a[116:119],50
R 42,2,20480
M 22,a[124:127],54
L 3
M 22,a[132:135],26
R 46,2,21504
M 22,a[140:143],30
M 18,a[128:131],26
R 50,2,22528
M 18,a[136:139],30
L 3
M 18,a[144:147],34
R 54,2,23552
M 18,a[152:155],38
M 22,a[148:151],34
R 26,2,24576
M 22,a[156:159],38
L 3
M 22,a[164:167],42
R 30,2,25600
M 22,a[172:175],46
M 18,a[160:163],42
R 34,2,26624
M 18,a[168:171],46
L 3
M 18,a[176:179],50
s_waitcnt vmcnt(2)
R 38,2,27648
M 18,a[184:187],54
s_barrier
ds_read_b32 v7,v6 offset:3072
M 22,a[180:183],50
R 42,2,28672
M 22,a[188:191],54
L 4
M 22,a[196:199],26
R 46,2,29696
M 22,a[204:207],30
M 18,a[192:195],26
R 50,2,30720
M 18,a[200:203],30
L 4
L 3
v_cmp_eq_u32 vcc,s53,v7
M 18,a[208:211],34
R 54,2,31744
M 18,a[216:219],38
M 22,a[212:215],34
s_cmp_eq_u32 vcc_lo,-1
R 26,2,32768
s_cbranch_scc0 Lg4_X1_%=
M 22,a[220:223],38
L 3
s_add_u32 m0,s57,0
s_add_u32 s56,s55,0
M 22,a[228:231],42
s_add_u32 s61,s54,s46
s_cmp_ge_u32 s61,s63
s_cselect_b32 s61,s93,s61
R 30,2,33792
M 22,a[236:239],46
s_mul_i32 s58,s61,2560
D 0,56,0
M 18,a[224:227],42
s_cselect_b32 s46,s88,s46
s_cselect_b32 s63,s89,s63
R 34,2,34816
M 18,a[232:235],46
L 3
s_cselect_b32 s93,s50,s93
s_cselect_b32 s88,8,s88
M 18,a[240:243],50
s_cselect_b32 s89,-1,s89
s_add_u32 s58,s58,109891584
R 38,2,35840
M 18,a[248:251],54
D 0,56,1024
M 22,a[244:247],50
s_mov_b32 s54,s61
R 42,2,36864
M 22,a[252:255],54
L 3
M 22,v[68:71],26
R 46,2,37888
M 22,v[76:79],30
D 0,56,2048
M 18,v[64:67],26
R 50,2,38912
M 18,v[72:75],30
L 3
M 18,v[80:83],34
R 54,2,39936
M 18,v[88:91],38
D 0,56,3072
M 22,v[84:87],34
s_add_u32 m0,s57,4096
s_add_u32 s56,s56,4096
R 26,2,40960
M 22,v[92:95],38
L 3
M 22,v[100:103],42
R 30,2,41984
M 22,v[108:111],46
D 0,56,0
M 18,v[96:99],42
R 34,2,43008
M 18,v[104:107],46
L 3
M 18,v[112:115],50
R 38,2,44032
M 18,v[120:123],54
D 0,56,1024
M 22,v[116:119],50
R 42,2,45056
M 22,v[124:127],54
L 3
M 22,v[132:135],26
R 46,2,46080
M 22,v[140:143],30
D 0,56,2048
M 18,v[128:131],26
s_add_u32 m0,s59,1024
s_add_u32 s56,s55,28672
R 50,2,47104
M 18,v[136:139],30
L 3
buffer_load_dword v8,s[36:39],s58 offen offset:0 sc1 lds
M 18,v[144:147],34
s_add_u32 m0,s57,28672
s_mul_i32 s55,s61,114688
R 54,2,48128
M 18,v[152:155],38
s_add_u32 s55,s55,s60
M 22,v[148:151],34
R 26,2,49152
M 22,v[156:159],38
L 3
D 0,56,0
M 22,v[164:167],42
R 30,2,50176
M 22,v[172:175],46
M 18,v[160:163],42
R 34,2,51200
M 18,v[168:171],46
L 3
D 0,56,1024
M 18,v[176:179],50
R 38,2,52224
M 18,v[184:187],54
D 0,56,2048
M 22,v[180:183],50
R 42,2,53248
M 22,v[188:191],54
L 3
M 22,v[196:199],26
R 46,2,54272
M 22,v[204:207],30
D 0,56,3072
M 18,v[192:195],26
s_add_u32 m0,s57,32768
s_add_u32 s56,s56,4096
R 50,2,55296
M 18,v[200:203],30
L 3
s_sub_u32 s57,s84,s57
M 18,v[208:211],34
s_waitcnt vmcnt(4)
R 54,2,56320
M 18,v[216:219],38
s_barrier
ds_read_b32 v7,v6 offset:1024
M 22,v[212:215],34
R 26,1,0
M 22,v[220:223],38
L 4
buffer_store_dword v4,v3,s[36:39],s69 offen offset:0 sc0 sc1
M 22,v[228:231],42
s_mov_b32 s69,s68
s_lshr_b32 s68,s62,4
R 30,1,1024
M 22,v[236:239],46
D 0,56,0
M 18,v[224:227],42
s_add_u32 s68,s68,s48
R 34,1,2048
M 18,v[232:235],46
L 4
D 0,56,1024
M 18,v[240:243],50
L 3
R 38,1,3072
v_cmp_eq_u32 vcc,s53,v7
M 18,v[248:251],54
D 0,56,2048
M 22,v[244:247],50
L 2
s_cmp_eq_u32 vcc_lo,s51
R 42,1,4096
M 22,v[252:255],54
buffer_store_dword v4,v3,s[36:39],s71 offen offset:0 sc0 sc1
s_cbranch_scc1 Lg4_F0_%=
s_branch Lg4_K0_%=
Lg4_X1_%=:
M 22,a[220:223],38
L 3
s_add_u32 m0,s59,1024
M 22,a[228:231],42
R 30,2,33792
M 22,a[236:239],46
buffer_load_dword v8,s[36:39],s58 offen offset:0 sc1 lds
M 18,a[224:227],42
R 34,2,34816
M 18,a[232:235],46
L 3
M 18,a[240:243],50
R 38,2,35840
M 18,a[248:251],54
M 22,a[244:247],50
R 42,2,36864
M 22,a[252:255],54
L 3
M 22,v[68:71],26
R 46,2,37888
M 22,v[76:79],30
M 18,v[64:67],26
R 50,2,38912
M 18,v[72:75],30
L 3
M 18,v[80:83],34
R 54,2,39936
M 18,v[88:91],38
M 22,v[84:87],34
R 26,2,40960
M 22,v[92:95],38
L 3
M 22,v[100:103],42
R 30,2,41984
M 22,v[108:111],46
M 18,v[96:99],42
R 34,2,43008
M 18,v[104:107],46
L 3
M 18,v[112:115],50
R 38,2,44032
M 18,v[120:123],54
M 22,v[116:119],50
R 42,2,45056
M 22,v[124:127],54
L 3
M 22,v[132:135],26
R 46,2,46080
M 22,v[140:143],30
M 18,v[128:131],26
R 50,2,47104
M 18,v[136:139],30
L 3
M 18,v[144:147],34
R 54,2,48128
M 18,v[152:155],38
M 22,v[148:151],34
R 26,2,49152
M 22,v[156:159],38
L 3
M 22,v[164:167],42
R 30,2,50176
M 22,v[172:175],46
M 18,v[160:163],42
R 34,2,51200
M 18,v[168:171],46
L 3
M 18,v[176:179],50
R 38,2,52224
M 18,v[184:187],54
M 22,v[180:183],50
R 42,2,53248
M 22,v[188:191],54
L 3
M 22,v[196:199],26
R 46,2,54272
M 22,v[204:207],30
M 18,v[192:195],26
R 50,2,55296
M 18,v[200:203],30
L 3
M 18,v[208:211],34
s_waitcnt vmcnt(0)
R 54,2,56320
M 18,v[216:219],38
s_barrier
buffer_store_dword v4,v3,s[36:39],s69 offen offset:0 sc0 sc1
M 22,v[212:215],34
s_mov_b32 s69,s68
s_lshr_b32 s68,s62,4
ds_read_b32 v7,v6 offset:1024
M 22,v[220:223],38
L 3
s_add_u32 s68,s68,s48
M 22,v[228:231],42
M 22,v[236:239],46
M 18,v[224:227],42
M 18,v[232:235],46
L 1
M 18,v[240:243],50
L 0
v_cmp_eq_u32 vcc,s53,v7
M 18,v[248:251],54
M 22,v[244:247],50
M 22,v[252:255],54
s_cmp_eq_u32 s51,1
s_cbranch_scc1 Lg4_pa1_%=
s_mov_b32 s80,0
s_cmp_eq_u32 vcc_lo,-1
s_cbranch_scc0 Lg4_xn1_%=
s_add_u32 m0,s57,0
s_add_u32 s56,s55,0
D 0,56,0
v_lshlrev_b32 v62,16,v58
D 0,56,1024
v_add_f32 v18,v18,v62
D 0,56,2048
v_and_b32 v63,0xffff0000,v58
D 0,56,3072
v_add_f32 v19,v19,v63
s_add_u32 m0,s57,4096
s_add_u32 s56,s56,4096
D 0,56,0
v_lshlrev_b32 v62,16,v59
D 0,56,1024
v_add_f32 v20,v20,v62
D 0,56,2048
v_and_b32 v63,0xffff0000,v59
s_add_u32 s61,s54,s46
s_cmp_ge_u32 s61,s63
s_cselect_b32 s61,s93,s61
s_cselect_b32 s46,s88,s46
s_cselect_b32 s63,s89,s63
s_cselect_b32 s93,s50,s93
s_cselect_b32 s88,8,s88
s_cselect_b32 s89,-1,s89
s_mul_i32 s58,s61,2560
s_add_u32 s58,s58,109891584
s_add_u32 m0,s57,28672
s_add_u32 s56,s55,28672
D 0,56,0
v_add_f32 v21,v21,v63
D 0,56,1024
)"
R"(v_lshlrev_b32 v62,16,v60
D 0,56,2048
v_add_f32 v22,v22,v62
D 0,56,3072
v_and_b32 v63,0xffff0000,v60
s_add_u32 m0,s57,32768
s_add_u32 s56,s56,4096
D 0,56,0
v_add_f32 v23,v23,v63
D 0,56,1024
v_lshlrev_b32 v62,16,v61
D 0,56,2048
v_add_f32 v24,v24,v62
s_mul_i32 s55,s61,114688
s_add_u32 s55,s55,s60
s_sub_u32 s57,s84,s57
s_mov_b32 s54,s61
v_and_b32 v63,0xffff0000,v61
v_add_f32 v25,v25,v63
v_cvt_pk_bf16_f32 v18,v18,v19
v_cvt_pk_bf16_f32 v19,v20,v21
v_cvt_pk_bf16_f32 v20,v22,v23
v_cvt_pk_bf16_f32 v21,v24,v25
buffer_store_dwordx4 v[18:21],v0,s[36:39],s67 offen offset:0 sc0 sc1
s_mov_b32 s72,s69
s_mov_b32 s69,0x7fff0000
s_mov_b32 s67,0x7fff0000
s_mov_b32 s77,0x7fff0000
s_branch Lg4_gox_%=
Lg4_xn1_%=:
s_cmp_ge_u32 s54,256
s_cbranch_scc1 Lg4_fin1_%=
s_add_u32 m0,s59,4096
s_nop 0
buffer_load_dword v8,s[36:39],s58 offen offset:0 sc1 lds
buffer_store_dword v4,v3,s[36:39],s71 offen offset:0 sc0 sc1
s_add_u32 m0,s59,5120
s_nop 0
buffer_load_dword v8,s[36:39],s58 offen offset:0 sc1 lds
buffer_store_dword v4,v3,s[36:39],s71 offen offset:0 sc0 sc1
s_mov_b32 s83,0
v_lshlrev_b32 v62,16,v58
v_add_f32 v18,v18,v62
v_and_b32 v63,0xffff0000,v58
v_add_f32 v19,v19,v63
v_lshlrev_b32 v62,16,v59
v_add_f32 v20,v20,v62
v_and_b32 v63,0xffff0000,v59
v_add_f32 v21,v21,v63
v_lshlrev_b32 v62,16,v60
v_add_f32 v22,v22,v62
v_and_b32 v63,0xffff0000,v60
v_add_f32 v23,v23,v63
v_lshlrev_b32 v62,16,v61
v_add_f32 v24,v24,v62
v_and_b32 v63,0xffff0000,v61
v_add_f32 v25,v25,v63
v_cvt_pk_bf16_f32 v18,v18,v19
v_cvt_pk_bf16_f32 v19,v20,v21
v_cvt_pk_bf16_f32 v20,v22,v23
v_cvt_pk_bf16_f32 v21,v24,v25
buffer_store_dwordx4 v[18:21],v0,s[36:39],s67 offen offset:0 sc0 sc1
s_mov_b32 s72,s69
s_mov_b32 s69,0x7fff0000
s_mov_b32 s67,0x7fff0000
s_mov_b32 s77,0x7fff0000
s_branch Lg4_poll_%=
Lg4_fin1_%=:
v_lshlrev_b32 v62,16,v58
v_add_f32 v18,v18,v62
v_and_b32 v63,0xffff0000,v58
v_add_f32 v19,v19,v63
v_lshlrev_b32 v62,16,v59
v_add_f32 v20,v20,v62
v_and_b32 v63,0xffff0000,v59
v_add_f32 v21,v21,v63
v_lshlrev_b32 v62,16,v60
v_add_f32 v22,v22,v62
v_and_b32 v63,0xffff0000,v60
v_add_f32 v23,v23,v63
v_lshlrev_b32 v62,16,v61
v_add_f32 v24,v24,v62
v_and_b32 v63,0xffff0000,v61
v_add_f32 v25,v25,v63
v_cvt_pk_bf16_f32 v18,v18,v19
v_cvt_pk_bf16_f32 v19,v20,v21
v_cvt_pk_bf16_f32 v20,v22,v23
v_cvt_pk_bf16_f32 v21,v24,v25
buffer_store_dwordx4 v[18:21],v0,s[36:39],s67 offen offset:0 sc0 sc1
s_mov_b32 s72,s69
s_mov_b32 s69,0x7fff0000
s_mov_b32 s67,0x7fff0000
s_mov_b32 s77,0x7fff0000
s_branch Lg4_done_%=
Lg4_Fa0_%=:
s_nop 0
v_mfma_f32_16x16x32_bf16 v[14:17],a[4:7],v[26:29],0
s_add_u32 m0,s57,0
R 46,1,5120
v_lshlrev_b32 v62,16,v58
M 14,a[12:15],30
s_add_u32 s56,s55,0
v_add_f32 v18,v18,v62
v_and_b32 v63,0xffff0000,v58
v_mfma_f32_16x16x32_bf16 v[10:13],a[0:3],v[26:29],0
s_add_u32 s61,s54,s46
R 50,1,6144
v_add_f32 v19,v19,v63
M 10,a[8:11],30
L 3
D 0,56,0
M 10,a[16:19],34
s_cmp_ge_u32 s61,s63
R 54,1,7168
v_lshlrev_b32 v62,16,v59
M 10,a[24:27],38
s_cselect_b32 s61,s93,s61
v_add_f32 v20,v20,v62
v_and_b32 v63,0xffff0000,v59
M 14,a[20:23],34
s_cselect_b32 s46,s88,s46
R 26,1,8192
v_add_f32 v21,v21,v63
M 14,a[28:31],38
L 3
v_lshlrev_b32 v62,16,v60
v_add_f32 v22,v22,v62
M 14,a[36:39],42
s_cselect_b32 s63,s89,s63
R 30,1,9216
v_and_b32 v63,0xffff0000,v60
M 14,a[44:47],46
D 0,56,1024
M 10,a[32:35],42
s_cselect_b32 s93,s50,s93
R 34,1,10240
v_add_f32 v23,v23,v63
M 10,a[40:43],46
L 3
v_lshlrev_b32 v62,16,v61
v_add_f32 v24,v24,v62
M 10,a[48:51],50
s_cselect_b32 s88,8,s88
R 38,1,11264
v_and_b32 v63,0xffff0000,v61
M 10,a[56:59],54
buffer_load_dwordx4 v[58:61],v0,s[36:39],s74 offen offset:0 sc1
M 14,a[52:55],50
s_cselect_b32 s89,-1,s89
R 42,1,12288
v_add_f32 v25,v25,v63
M 14,a[60:63],54
L 3
v_cvt_pk_bf16_f32 v18,v18,v19
v_cvt_pk_bf16_f32 v19,v20,v21
M 14,a[68:71],26
s_mul_i32 s58,s61,2560
R 46,1,13312
v_cvt_pk_bf16_f32 v20,v22,v23
M 14,a[76:79],30
D 0,56,2048
M 10,a[64:67],26
s_add_u32 s58,s58,109891584
R 50,1,14336
v_cvt_pk_bf16_f32 v21,v24,v25
M 10,a[72:75],30
L 3
v_lshrrev_b32 v62,8,v0
v_lshl_add_u32 v62,v62,5,v0
M 10,a[80:83],34
s_sub_u32 s75,s67,s85
R 54,1,15360
v_add_u32 v62,s73,v62
M 10,a[88:91],38
s_mov_b64 s[40:41],s[86:87]
ds_write_b128 v62,v[18:21] offset:0
M 14,a[84:87],34
s_mov_b32 s42,4194304
R 26,1,16384
ds_read2_b32 v[22:23],v5 offset0:0 offset1:16
M 14,a[92:95],38
L 5
s_mov_b32 s43,0x00020000
ds_read2_b32 v[24:25],v5 offset0:32 offset1:48
M 14,a[100:103],42
s_add_u32 s64,s75,0
s_lshl_b32 s62,s54,15
R 30,1,17408
M 14,a[108:111],46
D 0,56,3072
M 10,a[96:99],42
s_add_u32 m0,s57,4096
s_add_u32 s56,s56,4096
R 34,1,18432
M 10,a[104:107],46
L 6
s_mov_b32 s66,s65
s_add_u32 s74,s49,s62
M 10,a[112:115],50
s_add_u32 s65,s47,s62
s_mov_b32 s54,s61
R 38,1,19456
M 10,a[120:123],54
L 3
M 14,a[116:119],50
R 42,1,20480
M 14,a[124:127],54
L 3
D 0,56,0
M 14,a[132:135],26
R 46,1,21504
M 14,a[140:143],30
M 10,a[128:131],26
R 50,1,22528
M 10,a[136:139],30
L 3
M 10,a[144:147],34
R 54,1,23552
M 10,a[152:155],38
M 14,a[148:151],34
R 26,1,24576
M 14,a[156:159],38
L 3
D 0,56,1024
M 14,a[164:167],42
R 30,1,25600
M 14,a[172:175],46
M 10,a[160:163],42
R 34,1,26624
M 10,a[168:171],46
L 3
M 10,a[176:179],50
s_waitcnt vmcnt(8)
R 38,1,27648
M 10,a[184:187],54
s_barrier
M 14,a[180:183],50
R 42,1,28672
M 14,a[188:191],54
L 3
D 0,56,2048
M 14,a[196:199],26
s_add_u32 m0,s59,0
s_add_u32 s56,s55,28672
R 46,1,29696
M 14,a[204:207],30
s_mul_i32 s55,s61,114688
s_add_u32 s55,s55,s60
M 10,a[192:195],26
R 50,1,30720
M 10,a[200:203],30
L 3
M 10,a[208:211],34
R 54,1,31744
M 10,a[216:219],38
M 14,a[212:215],34
R 26,1,32768
M 14,a[220:223],38
L 3
buffer_load_dword v8,s[36:39],s58 offen offset:0 sc1 lds
M 14,a[228:231],42
s_add_u32 m0,s57,28672
R 30,1,33792
M 14,a[236:239],46
D 0,56,0
M 10,a[224:227],42
R 34,1,34816
M 10,a[232:235],46
L 3
M 10,a[240:243],50
R 38,1,35840
M 10,a[248:251],54
M 14,a[244:247],50
R 42,1,36864
M 14,a[252:255],54
L 3
D 0,56,1024
M 14,v[68:71],26
R 46,1,37888
M 14,v[76:79],30
M 10,v[64:67],26
R 50,1,38912
M 10,v[72:75],30
L 3
M 10,v[80:83],34
R 54,1,39936
M 10,v[88:91],38
D 0,56,2048
M 14,v[84:87],34
R 26,1,40960
M 14,v[92:95],38
L 3
M 14,v[100:103],42
R 30,1,41984
M 14,v[108:111],46
M 10,v[96:99],42
R 34,1,43008
M 10,v[104:107],46
L 3
D 0,56,3072
M 10,v[112:115],50
s_add_u32 m0,s57,32768
s_add_u32 s56,s56,4096
R 38,1,44032
M 10,v[120:123],54
s_sub_u32 s57,s84,s57
M 14,v[116:119],50
R 42,1,45056
M 14,v[124:127],54
L 3
M 14,v[132:135],26
R 46,1,46080
M 14,v[140:143],30
D 0,56,0
M 10,v[128:131],26
R 50,1,47104
M 10,v[136:139],30
L 3
M 10,v[144:147],34
R 54,1,48128
M 10,v[152:155],38
M 14,v[148:151],34
R 26,1,49152
M 14,v[156:159],38
L 3
D 0,56,1024
M 14,v[164:167],42
R 30,1,50176
M 14,v[172:175],46
M 10,v[160:163],42
R 34,1,51200
M 10,v[168:171],46
L 3
M 10,v[176:179],50
R 38,1,52224
M 10,v[184:187],54
D 0,56,2048
M 14,v[180:183],50
R 42,1,53248
M 14,v[188:191],54
L 3
M 14,v[196:199],26
R 46,1,54272
M 14,v[204:207],30
M 10,v[192:195],26
R 50,1,55296
M 10,v[200:203],30
L 3
M 10,v[208:211],34
s_waitcnt vmcnt(7)
R 54,1,56320
M 10,v[216:219],38
s_barrier
ds_read_b32 v7,v6 offset:0
M 14,v[212:215],34
R 26,2,0
M 14,v[220:223],38
L 4
M 14,v[228:231],42
R 30,2,1024
M 14,v[236:239],46
buffer_atomic_pk_add_bf16 v22,v9,s[40:43],s64 offen sc1
M 10,v[224:227],42
s_add_u32 s64,s75,8192
R 34,2,2048
M 10,v[232:235],46
L 4
buffer_atomic_pk_add_bf16 v23,v9,s[40:43],s64 offen sc1
M 10,v[240:243],50
s_add_u32 s64,s75,16384
L 3
R 38,2,3072
v_cmp_eq_u32 vcc,s53,v7
M 10,v[248:251],54
buffer_atomic_pk_add_bf16 v24,v9,s[40:43],s64 offen sc1
M 14,v[244:247],50
L 2
s_add_u32 s64,s75,24576
s_cmp_eq_u32 vcc_lo,-1
R 42,2,4096
M 14,v[252:255],54
buffer_atomic_pk_add_bf16 v25,v9,s[40:43],s64 offen sc1
s_cbranch_scc0 Lg4_Ka1_%=
Lg4_Fa1_%=:
s_nop 0
v_mfma_f32_16x16x32_bf16 v[22:25],a[4:7],v[26:29],0
s_add_u32 m0,s57,0
R 46,2,5120
v_lshlrev_b32 v62,16,v58
M 22,a[12:15],30
s_add_u32 s56,s55,0
v_add_f32 v10,v10,v62
v_and_b32 v63,0xffff0000,v58
v_mfma_f32_16x16x32_bf16 v[18:21],a[0:3],v[26:29],0
s_add_u32 s61,s54,s46
R 50,2,6144
v_add_f32 v11,v11,v63
M 18,a[8:11],30
L 3
D 0,56,0
M 18,a[16:19],34
s_cmp_ge_u32 s61,s63
R 54,2,7168
v_lshlrev_b32 v62,16,v59
M 18,a[24:27],38
s_cselect_b32 s61,s93,s61
v_add_f32 v12,v12,v62
v_and_b32 v63,0xffff0000,v59
M 22,a[20:23],34
s_cselect_b32 s46,s88,s46
R 26,2,8192
v_add_f32 v13,v13,v63
M 22,a[28:31],38
L 3
v_lshlrev_b32 v62,16,v60
v_add_f32 v14,v14,v62
M 22,a[36:39],42
s_cselect_b32 s63,s89,s63
R 30,2,9216
v_and_b32 v63,0xffff0000,v60
M 22,a[44:47],46
D 0,56,1024
M 18,a[32:35],42
s_cselect_b32 s93,s50,s93
R 34,2,10240
v_add_f32 v15,v15,v63
M 18,a[40:43],46
L 3
v_lshlrev_b32 v62,16,v61
v_add_f32 v16,v16,v62
M 18,a[48:51],50
s_cselect_b32 s88,8,s88
R 38,2,11264
v_and_b32 v63,0xffff0000,v61
M 18,a[56:59],54
buffer_load_dwordx4 v[58:61],v0,s[36:39],s74 offen offset:0 sc1
M 22,a[52:55],50
s_cselect_b32 s89,-1,s89
R 42,2,12288
v_add_f32 v17,v17,v63
M 22,a[60:63],54
L 3
v_cvt_pk_bf16_f32 v10,v10,v11
v_cvt_pk_bf16_f32 v11,v12,v13
M 22,a[68:71],26
s_mul_i32 s58,s61,2560
R 46,2,13312
v_cvt_pk_bf16_f32 v12,v14,v15
M 22,a[76:79],30
D 0,56,2048
M 18,a[64:67],26
s_add_u32 s58,s58,109891584
R 50,2,14336
v_cvt_pk_bf16_f32 v13,v16,v17
M 18,a[72:75],30
L 3
v_lshrrev_b32 v62,8,v0
v_lshl_add_u32 v62,v62,5,v0
M 18,a[80:83],34
s_sub_u32 s75,s66,s85
R 54,2,15360
v_add_u32 v62,s73,v62
M 18,a[88:91],38
s_mov_b64 s[40:41],s[86:87]
ds_write_b128 v62,v[10:13] offset:0
M 22,a[84:87],34
s_mov_b32 s42,4194304
R 26,2,16384
ds_read2_b32 v[14:15],v5 offset0:0 offset1:16
M 22,a[92:95],38
L 5
s_mov_b32 s43,0x00020000
ds_read2_b32 v[16:17],v5 offset0:32 offset1:48
M 22,a[100:103],42
s_add_u32 s64,s75,0
s_lshl_b32 s62,s54,15
R 30,2,17408
M 22,a[108:111],46
D 0,56,3072
M 18,a[96:99],42
s_add_u32 m0,s57,4096
s_add_u32 s56,s56,4096
R 34,2,18432
M 18,a[104:107],46
L 6
s_mov_b32 s67,s65
s_add_u32 s74,s49,s62
M 18,a[112:115],50
s_add_u32 s65,s47,s62
s_mov_b32 s54,s61
R 38,2,19456
M 18,a[120:123],54
L 3
M 22,a[116:119],50
R 42,2,20480
M 22,a[124:127],54
L 3
D 0,56,0
M 22,a[132:135],26
R 46,2,21504
)"
R"(M 22,a[140:143],30
M 18,a[128:131],26
R 50,2,22528
M 18,a[136:139],30
L 3
M 18,a[144:147],34
R 54,2,23552
M 18,a[152:155],38
M 22,a[148:151],34
R 26,2,24576
M 22,a[156:159],38
L 3
D 0,56,1024
M 22,a[164:167],42
R 30,2,25600
M 22,a[172:175],46
M 18,a[160:163],42
R 34,2,26624
M 18,a[168:171],46
L 3
M 18,a[176:179],50
s_waitcnt vmcnt(8)
R 38,2,27648
M 18,a[184:187],54
s_barrier
M 22,a[180:183],50
R 42,2,28672
M 22,a[188:191],54
L 3
D 0,56,2048
M 22,a[196:199],26
s_add_u32 m0,s59,1024
s_add_u32 s56,s55,28672
R 46,2,29696
M 22,a[204:207],30
s_mul_i32 s55,s61,114688
s_add_u32 s55,s55,s60
M 18,a[192:195],26
R 50,2,30720
M 18,a[200:203],30
L 3
M 18,a[208:211],34
R 54,2,31744
M 18,a[216:219],38
M 22,a[212:215],34
R 26,2,32768
M 22,a[220:223],38
L 3
buffer_load_dword v8,s[36:39],s58 offen offset:0 sc1 lds
M 22,a[228:231],42
s_add_u32 m0,s57,28672
R 30,2,33792
M 22,a[236:239],46
D 0,56,0
M 18,a[224:227],42
R 34,2,34816
M 18,a[232:235],46
L 3
M 18,a[240:243],50
R 38,2,35840
M 18,a[248:251],54
M 22,a[244:247],50
R 42,2,36864
M 22,a[252:255],54
L 3
D 0,56,1024
M 22,v[68:71],26
R 46,2,37888
M 22,v[76:79],30
M 18,v[64:67],26
R 50,2,38912
M 18,v[72:75],30
L 3
M 18,v[80:83],34
R 54,2,39936
M 18,v[88:91],38
D 0,56,2048
M 22,v[84:87],34
R 26,2,40960
M 22,v[92:95],38
L 3
M 22,v[100:103],42
R 30,2,41984
M 22,v[108:111],46
M 18,v[96:99],42
R 34,2,43008
M 18,v[104:107],46
L 3
D 0,56,3072
M 18,v[112:115],50
s_add_u32 m0,s57,32768
s_add_u32 s56,s56,4096
R 38,2,44032
M 18,v[120:123],54
s_sub_u32 s57,s84,s57
M 22,v[116:119],50
R 42,2,45056
M 22,v[124:127],54
L 3
M 22,v[132:135],26
R 46,2,46080
M 22,v[140:143],30
D 0,56,0
M 18,v[128:131],26
R 50,2,47104
M 18,v[136:139],30
L 3
M 18,v[144:147],34
R 54,2,48128
M 18,v[152:155],38
M 22,v[148:151],34
R 26,2,49152
M 22,v[156:159],38
L 3
D 0,56,1024
M 22,v[164:167],42
R 30,2,50176
M 22,v[172:175],46
M 18,v[160:163],42
R 34,2,51200
M 18,v[168:171],46
L 3
M 18,v[176:179],50
R 38,2,52224
M 18,v[184:187],54
D 0,56,2048
M 22,v[180:183],50
R 42,2,53248
M 22,v[188:191],54
L 3
M 22,v[196:199],26
R 46,2,54272
M 22,v[204:207],30
M 18,v[192:195],26
R 50,2,55296
M 18,v[200:203],30
L 3
M 18,v[208:211],34
s_waitcnt vmcnt(7)
R 54,2,56320
M 18,v[216:219],38
s_barrier
ds_read_b32 v7,v6 offset:1024
M 22,v[212:215],34
R 26,1,0
M 22,v[220:223],38
L 4
M 22,v[228:231],42
R 30,1,1024
M 22,v[236:239],46
buffer_atomic_pk_add_bf16 v14,v9,s[40:43],s64 offen sc1
M 18,v[224:227],42
s_add_u32 s64,s75,8192
R 34,1,2048
M 18,v[232:235],46
L 4
buffer_atomic_pk_add_bf16 v15,v9,s[40:43],s64 offen sc1
M 18,v[240:243],50
s_add_u32 s64,s75,16384
L 3
R 38,1,3072
v_cmp_eq_u32 vcc,s53,v7
M 18,v[248:251],54
buffer_atomic_pk_add_bf16 v16,v9,s[40:43],s64 offen sc1
M 22,v[244:247],50
L 2
s_add_u32 s64,s75,24576
s_cmp_eq_u32 vcc_lo,-1
R 42,1,4096
M 22,v[252:255],54
buffer_atomic_pk_add_bf16 v17,v9,s[40:43],s64 offen sc1
s_cbranch_scc1 Lg4_Fa0_%=
Lg4_Ka0_%=:
Lg4_La0_%=:
s_nop 0
v_mfma_f32_16x16x32_bf16 v[14:17],a[4:7],v[26:29],0
s_add_u32 m0,s59,3072
R 46,1,5120
v_lshlrev_b32 v62,16,v58
M 14,a[12:15],30
buffer_load_dword v8,s[36:39],s58 offen offset:0 sc1 lds
v_mfma_f32_16x16x32_bf16 v[10:13],a[0:3],v[26:29],0
s_sub_u32 s75,s67,s85
R 50,1,6144
v_add_f32 v18,v18,v62
M 10,a[8:11],30
L 3
v_and_b32 v63,0xffff0000,v58
v_add_f32 v19,v19,v63
M 10,a[16:19],34
s_mov_b64 s[40:41],s[86:87]
R 54,1,7168
v_lshlrev_b32 v62,16,v59
M 10,a[24:27],38
s_mov_b32 s42,4194304
v_add_f32 v20,v20,v62
v_and_b32 v63,0xffff0000,v59
M 14,a[20:23],34
s_mov_b32 s43,0x00020000
R 26,1,8192
v_add_f32 v21,v21,v63
M 14,a[28:31],38
L 3
v_lshlrev_b32 v62,16,v60
v_add_f32 v22,v22,v62
M 14,a[36:39],42
s_add_u32 s64,s75,0
R 30,1,9216
v_and_b32 v63,0xffff0000,v60
M 14,a[44:47],46
s_lshl_b32 s62,s54,15
v_add_f32 v23,v23,v63
v_lshlrev_b32 v62,16,v61
M 10,a[32:35],42
s_mov_b32 s66,s65
R 34,1,10240
v_add_f32 v24,v24,v62
M 10,a[40:43],46
L 3
v_and_b32 v63,0xffff0000,v61
v_add_f32 v25,v25,v63
M 10,a[48:51],50
s_add_u32 s65,s47,s62
R 38,1,11264
v_cvt_pk_bf16_f32 v18,v18,v19
M 10,a[56:59],54
buffer_load_dwordx4 v[58:61],v0,s[36:39],s74 offen offset:0 sc1
M 14,a[52:55],50
s_add_u32 s74,s49,s62
R 42,1,12288
v_cvt_pk_bf16_f32 v19,v20,v21
M 14,a[60:63],54
L 3
v_cvt_pk_bf16_f32 v20,v22,v23
v_cvt_pk_bf16_f32 v21,v24,v25
M 14,a[68:71],26
R 46,1,13312
v_lshrrev_b32 v62,8,v0
M 14,a[76:79],30
v_lshl_add_u32 v62,v62,5,v0
v_add_u32 v62,s73,v62
M 10,a[64:67],26
R 50,1,14336
M 10,a[72:75],30
L 3
ds_write_b128 v62,v[18:21] offset:0
M 10,a[80:83],34
R 54,1,15360
ds_read2_b32 v[22:23],v5 offset0:0 offset1:16
M 10,a[88:91],38
ds_read2_b32 v[24:25],v5 offset0:32 offset1:48
M 14,a[84:87],34
R 26,1,16384
M 14,a[92:95],38
L 6
M 14,a[100:103],42
R 30,1,17408
M 14,a[108:111],46
M 10,a[96:99],42
R 34,1,18432
M 10,a[104:107],46
L 5
L 3
M 10,a[112:115],50
R 38,1,19456
M 10,a[120:123],54
buffer_atomic_pk_add_bf16 v22,v9,s[40:43],s64 offen sc1
M 14,a[116:119],50
s_add_u32 s64,s75,8192
R 42,1,20480
M 14,a[124:127],54
L 3
buffer_atomic_pk_add_bf16 v23,v9,s[40:43],s64 offen sc1
M 14,a[132:135],26
s_add_u32 s64,s75,16384
R 46,1,21504
M 14,a[140:143],30
buffer_atomic_pk_add_bf16 v24,v9,s[40:43],s64 offen sc1
M 10,a[128:131],26
s_add_u32 s64,s75,24576
R 50,1,22528
M 10,a[136:139],30
L 3
buffer_atomic_pk_add_bf16 v25,v9,s[40:43],s64 offen sc1
M 10,a[144:147],34
R 54,1,23552
M 10,a[152:155],38
M 14,a[148:151],34
R 26,1,24576
M 14,a[156:159],38
L 3
M 14,a[164:167],42
R 30,1,25600
M 14,a[172:175],46
M 10,a[160:163],42
R 34,1,26624
M 10,a[168:171],46
L 3
M 10,a[176:179],50
s_waitcnt vmcnt(5)
R 38,1,27648
M 10,a[184:187],54
s_barrier
ds_read_b32 v7,v6 offset:3072
M 14,a[180:183],50
R 42,1,28672
M 14,a[188:191],54
L 4
M 14,a[196:199],26
R 46,1,29696
M 14,a[204:207],30
M 10,a[192:195],26
R 50,1,30720
M 10,a[200:203],30
L 4
L 3
v_cmp_eq_u32 vcc,s53,v7
M 10,a[208:211],34
R 54,1,31744
M 10,a[216:219],38
M 14,a[212:215],34
s_cmp_eq_u32 vcc_lo,-1
R 26,1,32768
s_cbranch_scc0 Lg4_Xa0_%=
M 14,a[220:223],38
L 3
s_add_u32 m0,s57,0
s_add_u32 s56,s55,0
M 14,a[228:231],42
s_add_u32 s61,s54,s46
s_cmp_ge_u32 s61,s63
s_cselect_b32 s61,s93,s61
R 30,1,33792
M 14,a[236:239],46
s_mul_i32 s58,s61,2560
D 0,56,0
M 10,a[224:227],42
s_cselect_b32 s46,s88,s46
s_cselect_b32 s63,s89,s63
R 34,1,34816
M 10,a[232:235],46
L 3
s_cselect_b32 s93,s50,s93
s_cselect_b32 s88,8,s88
M 10,a[240:243],50
s_cselect_b32 s89,-1,s89
s_add_u32 s58,s58,109891584
R 38,1,35840
M 10,a[248:251],54
D 0,56,1024
M 14,a[244:247],50
s_mov_b32 s54,s61
R 42,1,36864
M 14,a[252:255],54
L 3
M 14,v[68:71],26
R 46,1,37888
M 14,v[76:79],30
D 0,56,2048
M 10,v[64:67],26
R 50,1,38912
M 10,v[72:75],30
L 3
M 10,v[80:83],34
R 54,1,39936
M 10,v[88:91],38
D 0,56,3072
M 14,v[84:87],34
s_add_u32 m0,s57,4096
s_add_u32 s56,s56,4096
R 26,1,40960
M 14,v[92:95],38
L 3
M 14,v[100:103],42
R 30,1,41984
M 14,v[108:111],46
D 0,56,0
M 10,v[96:99],42
R 34,1,43008
M 10,v[104:107],46
L 3
M 10,v[112:115],50
R 38,1,44032
M 10,v[120:123],54
D 0,56,1024
M 14,v[116:119],50
R 42,1,45056
M 14,v[124:127],54
L 3
M 14,v[132:135],26
R 46,1,46080
M 14,v[140:143],30
D 0,56,2048
M 10,v[128:131],26
s_add_u32 m0,s59,0
s_add_u32 s56,s55,28672
R 50,1,47104
M 10,v[136:139],30
L 3
buffer_load_dword v8,s[36:39],s58 offen offset:0 sc1 lds
M 10,v[144:147],34
s_add_u32 m0,s57,28672
s_mul_i32 s55,s61,114688
R 54,1,48128
M 10,v[152:155],38
s_add_u32 s55,s55,s60
M 14,v[148:151],34
R 26,1,49152
M 14,v[156:159],38
L 3
D 0,56,0
M 14,v[164:167],42
R 30,1,50176
M 14,v[172:175],46
M 10,v[160:163],42
R 34,1,51200
M 10,v[168:171],46
L 3
D 0,56,1024
M 10,v[176:179],50
R 38,1,52224
M 10,v[184:187],54
D 0,56,2048
M 14,v[180:183],50
R 42,1,53248
M 14,v[188:191],54
L 3
M 14,v[196:199],26
R 46,1,54272
M 14,v[204:207],30
D 0,56,3072
M 10,v[192:195],26
s_add_u32 m0,s57,32768
s_add_u32 s56,s56,4096
R 50,1,55296
M 10,v[200:203],30
L 3
s_sub_u32 s57,s84,s57
M 10,v[208:211],34
s_waitcnt vmcnt(4)
R 54,1,56320
M 10,v[216:219],38
s_barrier
ds_read_b32 v7,v6 offset:0
M 14,v[212:215],34
R 26,2,0
M 14,v[220:223],38
L 4
D 0,56,0
M 14,v[228:231],42
R 30,2,1024
M 14,v[236:239],46
D 0,56,1024
M 10,v[224:227],42
R 34,2,2048
M 10,v[232:235],46
L 4
D 0,56,2048
M 10,v[240:243],50
L 3
R 38,2,3072
v_cmp_eq_u32 vcc,s53,v7
M 10,v[248:251],54
M 14,v[244:247],50
L 2
s_cmp_eq_u32 vcc_lo,-1
R 42,2,4096
M 14,v[252:255],54
buffer_store_dword v4,v3,s[36:39],s71 offen offset:0 sc0 sc1
buffer_store_dword v4,v3,s[36:39],s71 offen offset:0 sc0 sc1
buffer_store_dword v4,v3,s[36:39],s71 offen offset:0 sc0 sc1
buffer_store_dword v4,v3,s[36:39],s71 offen offset:0 sc0 sc1
s_cbranch_scc1 Lg4_Fa1_%=
s_branch Lg4_Ka1_%=
Lg4_Xa0_%=:
M 14,a[220:223],38
L 3
s_add_u32 m0,s59,0
M 14,a[228:231],42
R 30,1,33792
M 14,a[236:239],46
buffer_load_dword v8,s[36:39],s58 offen offset:0 sc1 lds
M 10,a[224:227],42
R 34,1,34816
M 10,a[232:235],46
L 3
M 10,a[240:243],50
R 38,1,35840
M 10,a[248:251],54
M 14,a[244:247],50
R 42,1,36864
M 14,a[252:255],54
L 3
M 14,v[68:71],26
R 46,1,37888
M 14,v[76:79],30
M 10,v[64:67],26
R 50,1,38912
M 10,v[72:75],30
L 3
)"
R"(M 10,v[80:83],34
R 54,1,39936
M 10,v[88:91],38
M 14,v[84:87],34
R 26,1,40960
M 14,v[92:95],38
L 3
M 14,v[100:103],42
R 30,1,41984
M 14,v[108:111],46
M 10,v[96:99],42
R 34,1,43008
M 10,v[104:107],46
L 3
M 10,v[112:115],50
R 38,1,44032
M 10,v[120:123],54
M 14,v[116:119],50
R 42,1,45056
M 14,v[124:127],54
L 3
M 14,v[132:135],26
R 46,1,46080
M 14,v[140:143],30
M 10,v[128:131],26
R 50,1,47104
M 10,v[136:139],30
L 3
M 10,v[144:147],34
R 54,1,48128
M 10,v[152:155],38
M 14,v[148:151],34
R 26,1,49152
M 14,v[156:159],38
L 3
M 14,v[164:167],42
R 30,1,50176
M 14,v[172:175],46
M 10,v[160:163],42
R 34,1,51200
M 10,v[168:171],46
L 3
M 10,v[176:179],50
R 38,1,52224
M 10,v[184:187],54
M 14,v[180:183],50
R 42,1,53248
M 14,v[188:191],54
L 3
M 14,v[196:199],26
R 46,1,54272
M 14,v[204:207],30
M 10,v[192:195],26
R 50,1,55296
M 10,v[200:203],30
L 3
M 10,v[208:211],34
s_waitcnt vmcnt(0)
R 54,1,56320
M 10,v[216:219],38
s_barrier
ds_read_b32 v7,v6 offset:0
M 14,v[212:215],34
M 14,v[220:223],38
L 3
M 14,v[228:231],42
M 14,v[236:239],46
M 10,v[224:227],42
M 10,v[232:235],46
L 1
L 0
M 10,v[240:243],50
v_cmp_eq_u32 vcc,s53,v7
M 10,v[248:251],54
M 14,v[244:247],50
M 14,v[252:255],54
Lg4_pa0_%=:
s_mov_b32 s80,1
s_cmp_eq_u32 vcc_lo,-1
s_cbranch_scc0 Lg4_xna0_%=
s_add_u32 m0,s57,0
s_add_u32 s56,s55,0
D 0,56,0
s_sub_u32 s75,s66,s85
v_lshlrev_b32 v62,16,v58
D 0,56,1024
v_add_f32 v10,v10,v62
D 0,56,2048
v_and_b32 v63,0xffff0000,v58
D 0,56,3072
v_add_f32 v11,v11,v63
s_add_u32 m0,s57,4096
s_add_u32 s56,s56,4096
D 0,56,0
v_lshlrev_b32 v62,16,v59
D 0,56,1024
v_add_f32 v12,v12,v62
D 0,56,2048
v_and_b32 v63,0xffff0000,v59
s_add_u32 s61,s54,s46
s_cmp_ge_u32 s61,s63
s_cselect_b32 s61,s93,s61
s_cselect_b32 s46,s88,s46
s_cselect_b32 s63,s89,s63
s_cselect_b32 s93,s50,s93
s_cselect_b32 s88,8,s88
s_cselect_b32 s89,-1,s89
s_mul_i32 s58,s61,2560
s_add_u32 s58,s58,109891584
s_add_u32 m0,s57,28672
s_add_u32 s56,s55,28672
D 0,56,0
v_add_f32 v13,v13,v63
D 0,56,1024
v_lshlrev_b32 v62,16,v60
D 0,56,2048
v_add_f32 v14,v14,v62
D 0,56,3072
v_and_b32 v63,0xffff0000,v60
s_add_u32 m0,s57,32768
s_add_u32 s56,s56,4096
D 0,56,0
v_add_f32 v15,v15,v63
D 0,56,1024
v_lshlrev_b32 v62,16,v61
D 0,56,2048
v_add_f32 v16,v16,v62
s_mul_i32 s55,s61,114688
s_add_u32 s55,s55,s60
s_sub_u32 s57,s84,s57
s_mov_b32 s54,s61
v_and_b32 v63,0xffff0000,v61
v_add_f32 v17,v17,v63
v_cvt_pk_bf16_f32 v10,v10,v11
v_cvt_pk_bf16_f32 v11,v12,v13
v_cvt_pk_bf16_f32 v12,v14,v15
v_cvt_pk_bf16_f32 v13,v16,v17
v_lshrrev_b32 v62,8,v0
v_lshl_add_u32 v62,v62,5,v0
v_add_u32 v62,s73,v62
ds_write_b128 v62,v[10:13] offset:0
ds_read2_b32 v[14:15],v5 offset0:0 offset1:16
ds_read2_b32 v[16:17],v5 offset0:32 offset1:48
s_mov_b64 s[40:41],s[86:87]
s_mov_b32 s42,4194304
s_mov_b32 s43,0x00020000
L 0
s_add_u32 s64,s75,0
buffer_atomic_pk_add_bf16 v14,v9,s[40:43],s64 offen sc1
s_add_u32 s64,s75,8192
buffer_atomic_pk_add_bf16 v15,v9,s[40:43],s64 offen sc1
s_add_u32 s64,s75,16384
buffer_atomic_pk_add_bf16 v16,v9,s[40:43],s64 offen sc1
s_add_u32 s64,s75,24576
buffer_atomic_pk_add_bf16 v17,v9,s[40:43],s64 offen sc1
s_mov_b32 s72,0x7fff0000
s_mov_b32 s69,0x7fff0000
s_mov_b32 s66,0x7fff0000
s_mov_b32 s76,0x7fff0000
s_branch Lg4_gox_%=
Lg4_xna0_%=:
s_cmp_ge_u32 s54,256
s_cbranch_scc1 Lg4_fina0_%=
s_add_u32 m0,s59,4096
s_nop 0
buffer_load_dword v8,s[36:39],s58 offen offset:0 sc1 lds
buffer_store_dword v4,v3,s[36:39],s71 offen offset:0 sc0 sc1
s_add_u32 m0,s59,5120
s_nop 0
buffer_load_dword v8,s[36:39],s58 offen offset:0 sc1 lds
buffer_store_dword v4,v3,s[36:39],s71 offen offset:0 sc0 sc1
s_mov_b32 s83,0
s_sub_u32 s75,s66,s85
v_lshlrev_b32 v62,16,v58
v_add_f32 v10,v10,v62
v_and_b32 v63,0xffff0000,v58
v_add_f32 v11,v11,v63
v_lshlrev_b32 v62,16,v59
v_add_f32 v12,v12,v62
v_and_b32 v63,0xffff0000,v59
v_add_f32 v13,v13,v63
v_lshlrev_b32 v62,16,v60
v_add_f32 v14,v14,v62
v_and_b32 v63,0xffff0000,v60
v_add_f32 v15,v15,v63
v_lshlrev_b32 v62,16,v61
v_add_f32 v16,v16,v62
v_and_b32 v63,0xffff0000,v61
v_add_f32 v17,v17,v63
v_cvt_pk_bf16_f32 v10,v10,v11
v_cvt_pk_bf16_f32 v11,v12,v13
v_cvt_pk_bf16_f32 v12,v14,v15
v_cvt_pk_bf16_f32 v13,v16,v17
v_lshrrev_b32 v62,8,v0
v_lshl_add_u32 v62,v62,5,v0
v_add_u32 v62,s73,v62
ds_write_b128 v62,v[10:13] offset:0
ds_read2_b32 v[14:15],v5 offset0:0 offset1:16
ds_read2_b32 v[16:17],v5 offset0:32 offset1:48
s_mov_b64 s[40:41],s[86:87]
s_mov_b32 s42,4194304
s_mov_b32 s43,0x00020000
L 0
s_add_u32 s64,s75,0
buffer_atomic_pk_add_bf16 v14,v9,s[40:43],s64 offen sc1
s_add_u32 s64,s75,8192
buffer_atomic_pk_add_bf16 v15,v9,s[40:43],s64 offen sc1
s_add_u32 s64,s75,16384
buffer_atomic_pk_add_bf16 v16,v9,s[40:43],s64 offen sc1
s_add_u32 s64,s75,24576
buffer_atomic_pk_add_bf16 v17,v9,s[40:43],s64 offen sc1
s_mov_b32 s72,0x7fff0000
s_mov_b32 s69,0x7fff0000
s_mov_b32 s66,0x7fff0000
s_mov_b32 s76,0x7fff0000
s_branch Lg4_poll_%=
Lg4_fina0_%=:
s_sub_u32 s75,s66,s85
v_lshlrev_b32 v62,16,v58
v_add_f32 v10,v10,v62
v_and_b32 v63,0xffff0000,v58
v_add_f32 v11,v11,v63
v_lshlrev_b32 v62,16,v59
v_add_f32 v12,v12,v62
v_and_b32 v63,0xffff0000,v59
v_add_f32 v13,v13,v63
v_lshlrev_b32 v62,16,v60
v_add_f32 v14,v14,v62
v_and_b32 v63,0xffff0000,v60
v_add_f32 v15,v15,v63
v_lshlrev_b32 v62,16,v61
v_add_f32 v16,v16,v62
v_and_b32 v63,0xffff0000,v61
v_add_f32 v17,v17,v63
v_cvt_pk_bf16_f32 v10,v10,v11
v_cvt_pk_bf16_f32 v11,v12,v13
v_cvt_pk_bf16_f32 v12,v14,v15
v_cvt_pk_bf16_f32 v13,v16,v17
v_lshrrev_b32 v62,8,v0
v_lshl_add_u32 v62,v62,5,v0
v_add_u32 v62,s73,v62
ds_write_b128 v62,v[10:13] offset:0
ds_read2_b32 v[14:15],v5 offset0:0 offset1:16
ds_read2_b32 v[16:17],v5 offset0:32 offset1:48
s_mov_b64 s[40:41],s[86:87]
s_mov_b32 s42,4194304
s_mov_b32 s43,0x00020000
L 0
s_add_u32 s64,s75,0
buffer_atomic_pk_add_bf16 v14,v9,s[40:43],s64 offen sc1
s_add_u32 s64,s75,8192
buffer_atomic_pk_add_bf16 v15,v9,s[40:43],s64 offen sc1
s_add_u32 s64,s75,16384
buffer_atomic_pk_add_bf16 v16,v9,s[40:43],s64 offen sc1
s_add_u32 s64,s75,24576
buffer_atomic_pk_add_bf16 v17,v9,s[40:43],s64 offen sc1
s_mov_b32 s72,0x7fff0000
s_mov_b32 s69,0x7fff0000
s_mov_b32 s66,0x7fff0000
s_mov_b32 s76,0x7fff0000
s_branch Lg4_done_%=
Lg4_Ka1_%=:
Lg4_La1_%=:
s_nop 0
v_mfma_f32_16x16x32_bf16 v[22:25],a[4:7],v[26:29],0
s_add_u32 m0,s59,3072
R 46,2,5120
v_lshlrev_b32 v62,16,v58
M 22,a[12:15],30
buffer_load_dword v8,s[36:39],s58 offen offset:0 sc1 lds
v_mfma_f32_16x16x32_bf16 v[18:21],a[0:3],v[26:29],0
s_sub_u32 s75,s66,s85
R 50,2,6144
v_add_f32 v10,v10,v62
M 18,a[8:11],30
L 3
v_and_b32 v63,0xffff0000,v58
v_add_f32 v11,v11,v63
M 18,a[16:19],34
s_mov_b64 s[40:41],s[86:87]
R 54,2,7168
v_lshlrev_b32 v62,16,v59
M 18,a[24:27],38
s_mov_b32 s42,4194304
v_add_f32 v12,v12,v62
v_and_b32 v63,0xffff0000,v59
M 22,a[20:23],34
s_mov_b32 s43,0x00020000
R 26,2,8192
v_add_f32 v13,v13,v63
M 22,a[28:31],38
L 3
v_lshlrev_b32 v62,16,v60
v_add_f32 v14,v14,v62
M 22,a[36:39],42
s_add_u32 s64,s75,0
R 30,2,9216
v_and_b32 v63,0xffff0000,v60
M 22,a[44:47],46
s_lshl_b32 s62,s54,15
v_add_f32 v15,v15,v63
v_lshlrev_b32 v62,16,v61
M 18,a[32:35],42
s_mov_b32 s67,s65
R 34,2,10240
v_add_f32 v16,v16,v62
M 18,a[40:43],46
L 3
v_and_b32 v63,0xffff0000,v61
v_add_f32 v17,v17,v63
M 18,a[48:51],50
s_add_u32 s65,s47,s62
R 38,2,11264
v_cvt_pk_bf16_f32 v10,v10,v11
M 18,a[56:59],54
buffer_load_dwordx4 v[58:61],v0,s[36:39],s74 offen offset:0 sc1
M 22,a[52:55],50
s_add_u32 s74,s49,s62
R 42,2,12288
v_cvt_pk_bf16_f32 v11,v12,v13
M 22,a[60:63],54
L 3
v_cvt_pk_bf16_f32 v12,v14,v15
v_cvt_pk_bf16_f32 v13,v16,v17
M 22,a[68:71],26
R 46,2,13312
v_lshrrev_b32 v62,8,v0
M 22,a[76:79],30
v_lshl_add_u32 v62,v62,5,v0
v_add_u32 v62,s73,v62
M 18,a[64:67],26
R 50,2,14336
M 18,a[72:75],30
L 3
ds_write_b128 v62,v[10:13] offset:0
M 18,a[80:83],34
R 54,2,15360
ds_read2_b32 v[14:15],v5 offset0:0 offset1:16
M 18,a[88:91],38
ds_read2_b32 v[16:17],v5 offset0:32 offset1:48
M 22,a[84:87],34
R 26,2,16384
M 22,a[92:95],38
L 6
M 22,a[100:103],42
R 30,2,17408
M 22,a[108:111],46
M 18,a[96:99],42
R 34,2,18432
M 18,a[104:107],46
L 5
L 3
M 18,a[112:115],50
R 38,2,19456
M 18,a[120:123],54
buffer_atomic_pk_add_bf16 v14,v9,s[40:43],s64 offen sc1
M 22,a[116:119],50
s_add_u32 s64,s75,8192
R 42,2,20480
M 22,a[124:127],54
L 3
buffer_atomic_pk_add_bf16 v15,v9,s[40:43],s64 offen sc1
M 22,a[132:135],26
s_add_u32 s64,s75,16384
R 46,2,21504
M 22,a[140:143],30
buffer_atomic_pk_add_bf16 v16,v9,s[40:43],s64 offen sc1
M 18,a[128:131],26
s_add_u32 s64,s75,24576
R 50,2,22528
M 18,a[136:139],30
L 3
buffer_atomic_pk_add_bf16 v17,v9,s[40:43],s64 offen sc1
M 18,a[144:147],34
R 54,2,23552
M 18,a[152:155],38
M 22,a[148:151],34
R 26,2,24576
M 22,a[156:159],38
L 3
M 22,a[164:167],42
R 30,2,25600
M 22,a[172:175],46
M 18,a[160:163],42
R 34,2,26624
M 18,a[168:171],46
L 3
M 18,a[176:179],50
s_waitcnt vmcnt(5)
R 38,2,27648
M 18,a[184:187],54
s_barrier
ds_read_b32 v7,v6 offset:3072
M 22,a[180:183],50
R 42,2,28672
M 22,a[188:191],54
L 4
M 22,a[196:199],26
R 46,2,29696
M 22,a[204:207],30
M 18,a[192:195],26
R 50,2,30720
M 18,a[200:203],30
L 4
L 3
v_cmp_eq_u32 vcc,s53,v7
M 18,a[208:211],34
R 54,2,31744
M 18,a[216:219],38
M 22,a[212:215],34
s_cmp_eq_u32 vcc_lo,-1
R 26,2,32768
s_cbranch_scc0 Lg4_Xa1_%=
M 22,a[220:223],38
L 3
s_add_u32 m0,s57,0
s_add_u32 s56,s55,0
M 22,a[228:231],42
s_add_u32 s61,s54,s46
s_cmp_ge_u32 s61,s63
s_cselect_b32 s61,s93,s61
R 30,2,33792
M 22,a[236:239],46
s_mul_i32 s58,s61,2560
D 0,56,0
M 18,a[224:227],42
s_cselect_b32 s46,s88,s46
s_cselect_b32 s63,s89,s63
R 34,2,34816
M 18,a[232:235],46
L 3
s_cselect_b32 s93,s50,s93
s_cselect_b32 s88,8,s88
M 18,a[240:243],50
s_cselect_b32 s89,-1,s89
s_add_u32 s58,s58,109891584
R 38,2,35840
M 18,a[248:251],54
D 0,56,1024
M 22,a[244:247],50
s_mov_b32 s54,s61
R 42,2,36864
M 22,a[252:255],54
L 3
M 22,v[68:71],26
R 46,2,37888
M 22,v[76:79],30
D 0,56,2048
M 18,v[64:67],26
R 50,2,38912
M 18,v[72:75],30
L 3
M 18,v[80:83],34
R 54,2,39936
M 18,v[88:91],38
D 0,56,3072
M 22,v[84:87],34
s_add_u32 m0,s57,4096
s_add_u32 s56,s56,4096
R 26,2,40960
M 22,v[92:95],38
L 3
M 22,v[100:103],42
R 30,2,41984
M 22,v[108:111],46
D 0,56,0
M 18,v[96:99],42
R 34,2,43008
M 18,v[104:107],46
L 3
M 18,v[112:115],50
R 38,2,44032
M 18,v[120:123],54
D 0,56,1024
M 22,v[116:119],50
R 42,2,45056
M 22,v[124:127],54
L 3
M 22,v[132:135],26
R 46,2,46080
M 22,v[140:143],30
D 0,56,2048
M 18,v[128:131],26
s_add_u32 m0,s59,1024
s_add_u32 s56,s55,28672
R 50,2,47104
M 18,v[136:139],30
L 3
buffer_load_dword v8,s[36:39],s58 offen offset:0 sc1 lds
M 18,v[144:147],34
s_add_u32 m0,s57,28672
s_mul_i32 s55,s61,114688
R 54,2,48128
M 18,v[152:155],38
s_add_u32 s55,s55,s60
M 22,v[148:151],34
R 26,2,49152
M 22,v[156:159],38
L 3
D 0,56,0
)"
R"(M 22,v[164:167],42
R 30,2,50176
M 22,v[172:175],46
M 18,v[160:163],42
R 34,2,51200
M 18,v[168:171],46
L 3
D 0,56,1024
M 18,v[176:179],50
R 38,2,52224
M 18,v[184:187],54
D 0,56,2048
M 22,v[180:183],50
R 42,2,53248
M 22,v[188:191],54
L 3
M 22,v[196:199],26
R 46,2,54272
M 22,v[204:207],30
D 0,56,3072
M 18,v[192:195],26
s_add_u32 m0,s57,32768
s_add_u32 s56,s56,4096
R 50,2,55296
M 18,v[200:203],30
L 3
s_sub_u32 s57,s84,s57
M 18,v[208:211],34
s_waitcnt vmcnt(4)
R 54,2,56320
M 18,v[216:219],38
s_barrier
ds_read_b32 v7,v6 offset:1024
M 22,v[212:215],34
R 26,1,0
M 22,v[220:223],38
L 4
D 0,56,0
M 22,v[228:231],42
R 30,1,1024
M 22,v[236:239],46
D 0,56,1024
M 18,v[224:227],42
R 34,1,2048
M 18,v[232:235],46
L 4
D 0,56,2048
M 18,v[240:243],50
L 3
R 38,1,3072
v_cmp_eq_u32 vcc,s53,v7
M 18,v[248:251],54
M 22,v[244:247],50
L 2
s_cmp_eq_u32 vcc_lo,-1
R 42,1,4096
M 22,v[252:255],54
buffer_store_dword v4,v3,s[36:39],s71 offen offset:0 sc0 sc1
buffer_store_dword v4,v3,s[36:39],s71 offen offset:0 sc0 sc1
buffer_store_dword v4,v3,s[36:39],s71 offen offset:0 sc0 sc1
buffer_store_dword v4,v3,s[36:39],s71 offen offset:0 sc0 sc1
s_cbranch_scc1 Lg4_Fa0_%=
s_branch Lg4_Ka0_%=
Lg4_Xa1_%=:
M 22,a[220:223],38
L 3
s_add_u32 m0,s59,1024
M 22,a[228:231],42
R 30,2,33792
M 22,a[236:239],46
buffer_load_dword v8,s[36:39],s58 offen offset:0 sc1 lds
M 18,a[224:227],42
R 34,2,34816
M 18,a[232:235],46
L 3
M 18,a[240:243],50
R 38,2,35840
M 18,a[248:251],54
M 22,a[244:247],50
R 42,2,36864
M 22,a[252:255],54
L 3
M 22,v[68:71],26
R 46,2,37888
M 22,v[76:79],30
M 18,v[64:67],26
R 50,2,38912
M 18,v[72:75],30
L 3
M 18,v[80:83],34
R 54,2,39936
M 18,v[88:91],38
M 22,v[84:87],34
R 26,2,40960
M 22,v[92:95],38
L 3
M 22,v[100:103],42
R 30,2,41984
M 22,v[108:111],46
M 18,v[96:99],42
R 34,2,43008
M 18,v[104:107],46
L 3
M 18,v[112:115],50
R 38,2,44032
M 18,v[120:123],54
M 22,v[116:119],50
R 42,2,45056
M 22,v[124:127],54
L 3
M 22,v[132:135],26
R 46,2,46080
M 22,v[140:143],30
M 18,v[128:131],26
R 50,2,47104
M 18,v[136:139],30
L 3
M 18,v[144:147],34
R 54,2,48128
M 18,v[152:155],38
M 22,v[148:151],34
R 26,2,49152
M 22,v[156:159],38
L 3
M 22,v[164:167],42
R 30,2,50176
M 22,v[172:175],46
M 18,v[160:163],42
R 34,2,51200
M 18,v[168:171],46
L 3
M 18,v[176:179],50
R 38,2,52224
M 18,v[184:187],54
M 22,v[180:183],50
R 42,2,53248
M 22,v[188:191],54
L 3
M 22,v[196:199],26
R 46,2,54272
M 22,v[204:207],30
M 18,v[192:195],26
R 50,2,55296
M 18,v[200:203],30
L 3
M 18,v[208:211],34
s_waitcnt vmcnt(0)
R 54,2,56320
M 18,v[216:219],38
s_barrier
ds_read_b32 v7,v6 offset:1024
M 22,v[212:215],34
M 22,v[220:223],38
L 3
M 22,v[228:231],42
M 22,v[236:239],46
M 18,v[224:227],42
M 18,v[232:235],46
L 1
L 0
M 18,v[240:243],50
v_cmp_eq_u32 vcc,s53,v7
M 18,v[248:251],54
M 22,v[244:247],50
M 22,v[252:255],54
Lg4_pa1_%=:
s_mov_b32 s80,0
s_cmp_eq_u32 vcc_lo,-1
s_cbranch_scc0 Lg4_xna1_%=
s_add_u32 m0,s57,0
s_add_u32 s56,s55,0
D 0,56,0
s_sub_u32 s75,s67,s85
v_lshlrev_b32 v62,16,v58
D 0,56,1024
v_add_f32 v18,v18,v62
D 0,56,2048
v_and_b32 v63,0xffff0000,v58
D 0,56,3072
v_add_f32 v19,v19,v63
s_add_u32 m0,s57,4096
s_add_u32 s56,s56,4096
D 0,56,0
v_lshlrev_b32 v62,16,v59
D 0,56,1024
v_add_f32 v20,v20,v62
D 0,56,2048
v_and_b32 v63,0xffff0000,v59
s_add_u32 s61,s54,s46
s_cmp_ge_u32 s61,s63
s_cselect_b32 s61,s93,s61
s_cselect_b32 s46,s88,s46
s_cselect_b32 s63,s89,s63
s_cselect_b32 s93,s50,s93
s_cselect_b32 s88,8,s88
s_cselect_b32 s89,-1,s89
s_mul_i32 s58,s61,2560
s_add_u32 s58,s58,109891584
s_add_u32 m0,s57,28672
s_add_u32 s56,s55,28672
D 0,56,0
v_add_f32 v21,v21,v63
D 0,56,1024
v_lshlrev_b32 v62,16,v60
D 0,56,2048
v_add_f32 v22,v22,v62
D 0,56,3072
v_and_b32 v63,0xffff0000,v60
s_add_u32 m0,s57,32768
s_add_u32 s56,s56,4096
D 0,56,0
v_add_f32 v23,v23,v63
D 0,56,1024
v_lshlrev_b32 v62,16,v61
D 0,56,2048
v_add_f32 v24,v24,v62
s_mul_i32 s55,s61,114688
s_add_u32 s55,s55,s60
s_sub_u32 s57,s84,s57
s_mov_b32 s54,s61
v_and_b32 v63,0xffff0000,v61
v_add_f32 v25,v25,v63
v_cvt_pk_bf16_f32 v18,v18,v19
v_cvt_pk_bf16_f32 v19,v20,v21
v_cvt_pk_bf16_f32 v20,v22,v23
v_cvt_pk_bf16_f32 v21,v24,v25
v_lshrrev_b32 v62,8,v0
v_lshl_add_u32 v62,v62,5,v0
v_add_u32 v62,s73,v62
ds_write_b128 v62,v[18:21] offset:0
ds_read2_b32 v[22:23],v5 offset0:0 offset1:16
ds_read2_b32 v[24:25],v5 offset0:32 offset1:48
s_mov_b64 s[40:41],s[86:87]
s_mov_b32 s42,4194304
s_mov_b32 s43,0x00020000
L 0
s_add_u32 s64,s75,0
buffer_atomic_pk_add_bf16 v22,v9,s[40:43],s64 offen sc1
s_add_u32 s64,s75,8192
buffer_atomic_pk_add_bf16 v23,v9,s[40:43],s64 offen sc1
s_add_u32 s64,s75,16384
buffer_atomic_pk_add_bf16 v24,v9,s[40:43],s64 offen sc1
s_add_u32 s64,s75,24576
buffer_atomic_pk_add_bf16 v25,v9,s[40:43],s64 offen sc1
s_mov_b32 s72,0x7fff0000
s_mov_b32 s69,0x7fff0000
s_mov_b32 s67,0x7fff0000
s_mov_b32 s77,0x7fff0000
s_branch Lg4_gox_%=
Lg4_xna1_%=:
s_cmp_ge_u32 s54,256
s_cbranch_scc1 Lg4_fina1_%=
s_add_u32 m0,s59,4096
s_nop 0
buffer_load_dword v8,s[36:39],s58 offen offset:0 sc1 lds
buffer_store_dword v4,v3,s[36:39],s71 offen offset:0 sc0 sc1
s_add_u32 m0,s59,5120
s_nop 0
buffer_load_dword v8,s[36:39],s58 offen offset:0 sc1 lds
buffer_store_dword v4,v3,s[36:39],s71 offen offset:0 sc0 sc1
s_mov_b32 s83,0
s_sub_u32 s75,s67,s85
v_lshlrev_b32 v62,16,v58
v_add_f32 v18,v18,v62
v_and_b32 v63,0xffff0000,v58
v_add_f32 v19,v19,v63
v_lshlrev_b32 v62,16,v59
v_add_f32 v20,v20,v62
v_and_b32 v63,0xffff0000,v59
v_add_f32 v21,v21,v63
v_lshlrev_b32 v62,16,v60
v_add_f32 v22,v22,v62
v_and_b32 v63,0xffff0000,v60
v_add_f32 v23,v23,v63
v_lshlrev_b32 v62,16,v61
v_add_f32 v24,v24,v62
v_and_b32 v63,0xffff0000,v61
v_add_f32 v25,v25,v63
v_cvt_pk_bf16_f32 v18,v18,v19
v_cvt_pk_bf16_f32 v19,v20,v21
v_cvt_pk_bf16_f32 v20,v22,v23
v_cvt_pk_bf16_f32 v21,v24,v25
v_lshrrev_b32 v62,8,v0
v_lshl_add_u32 v62,v62,5,v0
v_add_u32 v62,s73,v62
ds_write_b128 v62,v[18:21] offset:0
ds_read2_b32 v[22:23],v5 offset0:0 offset1:16
ds_read2_b32 v[24:25],v5 offset0:32 offset1:48
s_mov_b64 s[40:41],s[86:87]
s_mov_b32 s42,4194304
s_mov_b32 s43,0x00020000
L 0
s_add_u32 s64,s75,0
buffer_atomic_pk_add_bf16 v22,v9,s[40:43],s64 offen sc1
s_add_u32 s64,s75,8192
buffer_atomic_pk_add_bf16 v23,v9,s[40:43],s64 offen sc1
s_add_u32 s64,s75,16384
buffer_atomic_pk_add_bf16 v24,v9,s[40:43],s64 offen sc1
s_add_u32 s64,s75,24576
buffer_atomic_pk_add_bf16 v25,v9,s[40:43],s64 offen sc1
s_mov_b32 s72,0x7fff0000
s_mov_b32 s69,0x7fff0000
s_mov_b32 s67,0x7fff0000
s_mov_b32 s77,0x7fff0000
s_branch Lg4_poll_%=
Lg4_fina1_%=:
s_sub_u32 s75,s67,s85
v_lshlrev_b32 v62,16,v58
v_add_f32 v18,v18,v62
v_and_b32 v63,0xffff0000,v58
v_add_f32 v19,v19,v63
v_lshlrev_b32 v62,16,v59
v_add_f32 v20,v20,v62
v_and_b32 v63,0xffff0000,v59
v_add_f32 v21,v21,v63
v_lshlrev_b32 v62,16,v60
v_add_f32 v22,v22,v62
v_and_b32 v63,0xffff0000,v60
v_add_f32 v23,v23,v63
v_lshlrev_b32 v62,16,v61
v_add_f32 v24,v24,v62
v_and_b32 v63,0xffff0000,v61
v_add_f32 v25,v25,v63
v_cvt_pk_bf16_f32 v18,v18,v19
v_cvt_pk_bf16_f32 v19,v20,v21
v_cvt_pk_bf16_f32 v20,v22,v23
v_cvt_pk_bf16_f32 v21,v24,v25
v_lshrrev_b32 v62,8,v0
v_lshl_add_u32 v62,v62,5,v0
v_add_u32 v62,s73,v62
ds_write_b128 v62,v[18:21] offset:0
ds_read2_b32 v[22:23],v5 offset0:0 offset1:16
ds_read2_b32 v[24:25],v5 offset0:32 offset1:48
s_mov_b64 s[40:41],s[86:87]
s_mov_b32 s42,4194304
s_mov_b32 s43,0x00020000
L 0
s_add_u32 s64,s75,0
buffer_atomic_pk_add_bf16 v22,v9,s[40:43],s64 offen sc1
s_add_u32 s64,s75,8192
buffer_atomic_pk_add_bf16 v23,v9,s[40:43],s64 offen sc1
s_add_u32 s64,s75,16384
buffer_atomic_pk_add_bf16 v24,v9,s[40:43],s64 offen sc1
s_add_u32 s64,s75,24576
buffer_atomic_pk_add_bf16 v25,v9,s[40:43],s64 offen sc1
s_mov_b32 s72,0x7fff0000
s_mov_b32 s69,0x7fff0000
s_mov_b32 s67,0x7fff0000
s_mov_b32 s77,0x7fff0000
s_branch Lg4_done_%=
Lg4_done_%=:
s_nop 0
s_barrier
buffer_store_dword v4,v3,s[36:39],s70 offen offset:0 sc0 sc1
s_waitcnt vmcnt(1)
s_barrier
buffer_store_dword v4,v3,s[36:39],s72 offen offset:0 sc0 sc1
s_waitcnt vmcnt(0) lgkmcnt(0)
s_barrier
s_endpgm
Lcode_g4_e:
.purgem M
.purgem R
.purgem T
.purgem D
.purgem L
)"
  :: "s"(ws), "s"(__builtin_amdgcn_readfirstlane(sWave)), "s"(__builtin_amdgcn_readfirstlane(sG0)), "s"(__builtin_amdgcn_readfirstlane(sStep)), "s"(__builtin_amdgcn_readfirstlane(sOutOff)), "s"(__builtin_amdgcn_readfirstlane(sFoutOff)), "s"(__builtin_amdgcn_readfirstlane(sAuxOff)), "s"(__builtin_amdgcn_readfirstlane(sWw)), "s"(__builtin_amdgcn_readfirstlane(sBuf)), "s"(__builtin_amdgcn_readfirstlane(sRk)), "s"(__builtin_amdgcn_readfirstlane(sExp)), "v"(vL16), "v"(vFlag), "v"(vSnapOff), "v"(vSnapRd), "v"(vWd0), "v"(vWd1), "v"(vWr0), "v"(vWr1), "v"(vSp), "v"(vSn), "s"(w4), "s"(out) : "v255", "v0", "v1", "v2", "v3", "v4", "v5", "v6", "v7", "v8", "v9", "v10", "v11", "v12", "v13", "v14", "v15", "v16", "v17", "v18", "v19", "v20", "v21", "v22", "v23", "v24", "v25", "v26", "v27", "v28", "v29", "v30", "v31", "v32", "v33", "v34", "v35", "v36", "v37", "v38", "v39", "v40", "v41", "v42", "v43", "v44", "v45", "v46", "v47", "v48", "v49", "v50", "v51", "v52", "v53", "v54", "v55", "v56", "v57", "v58", "v59", "v60", "v61", "v62", "v63", "v64", "v65", "v66", "v67", "v68", "v69", "v70", "v71", "v72", "v73", "v74", "v75", "v76", "v77", "v78", "v79", "v80", "v81", "v82", "v83", "v84", "v85", "v86", "v87", "v88", "v89", "v90", "v91", "v92", "v93", "v94", "v95", "v96", "v97", "v98", "v99", "v100", "v101", "v102", "v103", "v104", "v105", "v106", "v107", "v108", "v109", "v110", "v111", "v112", "v113", "v114", "v115", "v116", "v117", "v118", "v119", "v120", "v121", "v122", "v123", "v124", "v125", "v126", "v127", "v128", "v129", "v130", "v131", "v132", "v133", "v134", "v135", "v136", "v137", "v138", "v139", "v140", "v141", "v142", "v143", "v144", "v145", "v146", "v147", "v148", "v149", "v150", "v151", "v152", "v153", "v154", "v155", "v156", "v157", "v158", "v159", "v160", "v161", "v162", "v163", "v164", "v165", "v166", "v167", "v168", "v169", "v170", "v171", "v172", "v173", "v174", "v175", "v176", "v177", "v178", "v179", "v180", "v181", "v182", "v183", "v184", "v185", "v186", "v187", "v188", "v189", "v190", "v191", "v192", "v193", "v194", "v195", "v196", "v197", "v198", "v199", "v200", "v201", "v202", "v203", "v204", "v205", "v206", "v207", "v208", "v209", "v210", "v211", "v212", "v213", "v214", "v215", "v216", "v217", "v218", "v219", "v220", "v221", "v222", "v223", "v224", "v225", "v226", "v227", "v228", "v229", "v230", "v231", "a0", "a1", "a2", "a3", "a4", "a5", "a6", "a7", "a8", "a9", "a10", "a11", "a12", "a13", "a14", "a15", "a16", "a17", "a18", "a19", "a20", "a21", "a22", "a23", "a24", "a25", "a26", "a27", "a28", "a29", "a30", "a31", "a32", "a33", "a34", "a35", "a36", "a37", "a38", "a39", "a40", "a41", "a42", "a43", "a44", "a45", "a46", "a47", "a48", "a49", "a50", "a51", "a52", "a53", "a54", "a55", "a56", "a57", "a58", "a59", "a60", "a61", "a62", "a63", "a64", "a65", "a66", "a67", "a68", "a69", "a70", "a71", "a72", "a73", "a74", "a75", "a76", "a77", "a78", "a79", "a80", "a81", "a82", "a83", "a84", "a85", "a86", "a87", "a88", "a89", "a90", "a91", "a92", "a93", "a94", "a95", "a96", "a97", "a98", "a99", "a100", "a101", "a102", "a103", "a104", "a105", "a106", "a107", "a108", "a109", "a110", "a111", "a112", "a113", "a114", "a115", "a116", "a117", "a118", "a119", "a120", "a121", "a122", "a123", "a124", "a125", "a126", "a127", "a128", "a129", "a130", "a131", "a132", "a133", "a134", "a135", "a136", "a137", "a138", "a139", "a140", "a141", "a142", "a143", "a144", "a145", "a146", "a147", "a148", "a149", "a150", "a151", "a152", "a153", "a154", "a155", "a156", "a157", "a158", "a159", "a160", "a161", "a162", "a163", "a164", "a165", "a166", "a167", "a168", "a169", "a170", "a171", "a172", "a173", "a174", "a175", "a176", "a177", "a178", "a179", "a180", "a181", "a182", "a183", "a184", "a185", "a186", "a187", "a188", "a189", "a190", "a191", "a192", "a193", "a194", "a195", "a196", "a197", "a198", "a199", "a200", "a201", "a202", "a203", "a204", "a205", "a206", "a207", "a208", "a209", "a210", "a211", "a212", "a213", "a214", "a215", "a216", "a217", "a218", "a219", "a220", "a221", "a222", "a223", "a224", "a225", "a226", "a227", "a228", "a229", "a230", "a231", "a232", "a233", "a234", "a235", "a236", "a237", "a238", "a239", "a240", "a241", "a242", "a243", "a244", "a245", "a246", "a247", "a248", "a249", "a250", "a251", "a252", "a253", "a254", "a255", "s36", "s37", "s38", "s39", "s40", "s41", "s42", "s43", "s44", "s45", "s46", "s47", "s48", "s49", "s50", "s51", "s52", "s53", "s54", "s55", "s56", "s57", "s58", "s59", "s60", "s61", "s62", "s63", "s64", "s65", "s66", "s67", "s68", "s69", "s70", "s71", "s72", "s73", "s74", "s75", "s76", "s77", "s78", "s79", "s80", "s81", "s82", "s83", "s84", "s85", "s86", "s87", "s88", "s89", "s90", "s91", "s92", "s93", "s94", "s95", "s96", "s97", "vcc", "scc", "memory");
    __builtin_unreachable();
  } else {
    LANE_ID(lane2);
    constexpr int N1 = 33 - (NR4 - 6) * 16, T1 = LAST_TASKS > RLN * N1 ? RLN * N1 : LAST_TASKS;
    const int w = kind == 6 ? 0 : kind == 1 ? 1 + (3 - rep) * 8 + slice : N1 + rep * 8 + slice;
    if (kind != 2) for (int t = w; t < T1; t += N1) row_last(ws, out, t, lane2, wave, ep);
    for (int t = T1 + (w + 32) % (N1 + 32); t < LAST_TASKS; t += N1 + 32) row_last(ws, out, t, lane2, wave, ep);
    if (kind == 2 && rep == 0 && slice == 0 && wave == 0) set_flag(ws + OFF_EP, lane2, ep);
    CTS(14);
    asm volatile("s_endpgm\nLcode_end:" ::: "memory");
    __builtin_unreachable();
  }
}

extern "C" void kernel_launch(void* const* d_in, const int* in_sizes, int n_in, void* d_out, int out_size,
                              void* d_ws, size_t ws_size, hipStream_t stream) {
  const u16* q = (const u16*)d_in[0];
  const u16* k = (const u16*)d_in[1];
  const u16* v = (const u16*)d_in[2];
  const u16* shift_k = (const u16*)d_in[3];
  const u16* ssm_k = (const u16*)d_in[4];
  const u16* ssm_D = (const u16*)d_in[5];
  const u16* w1 = (const u16*)d_in[7];
  const u16* w2 = (const u16*)d_in[8];
  const u16* w3 = (const u16*)d_in[9];
  const u16* w4 = (const u16*)d_in[10];
  flow_kernel<<<256, 256, 0, stream>>>(q, k, v, shift_k, ssm_k, ssm_D, w1, w2, w3, w4, (char*)d_ws, (u16*)d_out);
}
```

```cpp
#include <hip/hip_runtime.h>
#include <hip/hip_bf16.h>
#include <stdint.h>
#include <type_traits>

typedef short s16x8 __attribute__((ext_vector_type(8)));
typedef short s16x4 __attribute__((ext_vector_type(4)));
typedef float f32x4 __attribute__((ext_vector_type(4)));
typedef unsigned short u16;
typedef unsigned short u16x8 __attribute__((ext_vector_type(8)));
typedef unsigned short u16x4 __attribute__((ext_vector_type(4)));
typedef float f32x2 __attribute__((ext_vector_type(2)));
typedef __bf16 bf16x2 __attribute__((ext_vector_type(2)));
typedef unsigned u32x4v __attribute__((ext_vector_type(4)));
typedef __attribute__((address_space(1))) unsigned gu32;

constexpr int S_ = 2048, D_ = 1024, F_ = 3584;
#define PF 1

constexpr int WS_USED = 111243392, LAST_TASKS = 100, ALLZ = 1, U0 = 170, U1 = 224, NR4 = 8, RHELP = 1, PRE1 = 1;
constexpr int OFF_KVR = 4194304, OFF_XP = 8388608, OFF_HS = 17825792, OFF_X1 = 31457280, OFF_G = 40894464, OFF_P0 = 71303168, OFF_P1 = 89128960, OFF_Y = 98566144, OFF_S1 = 106954752, OFF_ST = 107479040, OFF_FL = 109051904, OFF_FXP = 109056000, OFF_FH = 109334528, OFF_FX1 = 109613056, OFF_FG = 109891584, OFF_FP = 110587904, OFF_FKV = 111144960, OFF_FY = 111177728, OFF_EP = 111243264, OFF_DBG = 115343360;

#define WS_DIAG 0

__device__ __forceinline__ float bf2f(u16 v) { return __uint_as_float(((unsigned)v) << 16); }
__device__ __forceinline__ u16 f2bf(float f) { return __builtin_bit_cast(u16, (__bf16)f); }
__device__ __forceinline__ float rbf(float f) { return bf2f(f2bf(f)); }
__device__ __forceinline__ void st16(void* p, u16x8 v) {
  asm volatile("global_store_dwordx4 %0, %1, off sc0 sc1\n\ts_nop 1" :: "v"(p), "v"(v) : "memory");
}
#define WAIT_VM(n) asm volatile("s_waitcnt vmcnt(%0)" ::"n"(n) : "memory")
#define WAIT_LGKM0() asm volatile("s_waitcnt lgkmcnt(0)" ::: "memory")

#define CODE_RANGE(B, E, p, n)                                                                                   \
  const char* p; unsigned n;                                                                                     \
  { unsigned lo_, hi_;                                                                                           \
    asm volatile("s_getpc_b64 s[98:99]\nLpc%=:\n\ts_add_u32 %0, s98, (" B "-Lpc%=)&4294967295\n\ts_addc_u32 %1, s99, (" B "-Lpc%=)>>32\n\t" \
                 "s_mov_b32 %2, " E "-" B : "=s"(lo_), "=s"(hi_), "=s"(n) :: "s98", "s99", "scc");               \
    p = (const char*)(((unsigned long long)hi_ << 32) | lo_); }
__device__ __forceinline__ unsigned touch(const char* p, unsigned n, unsigned off) {
  unsigned t;
  asm volatile("global_load_dword %0, %1, off" : "=v"(t) : "v"(p + (off < n ? off : 0u)) : "memory");
  return t;
}

__device__ __forceinline__ void touch_lds(const char* p, unsigned a) {
  asm volatile("s_mov_b32 m0, %1\n\ts_nop 0\n\tglobal_load_lds_dword %0, off" :: "v"(p), "s"(a) : "memory");
}
#define PFPOL ""
__device__ __forceinline__ void touch_pol(const char* p, unsigned a) {
  asm volatile("s_mov_b32 m0, %1\n\ts_nop 0\n\tglobal_load_lds_dword %0, off " PFPOL :: "v"(p), "s"(a) : "memory");
}
#define YSLEEP 4
#define KSLEEP 2
#define CTS(i)
__device__ __forceinline__ f32x2 up2(unsigned p) { return (f32x2){__uint_as_float(p << 16), __uint_as_float(p & 0xffff0000u)}; }
__device__ __forceinline__ unsigned pk2(f32x2 v) { return __builtin_bit_cast(unsigned, __builtin_convertvector(v, bf16x2)); }
__device__ __forceinline__ f32x2 rbf2(f32x2 v) { return up2(pk2(v)); }
template <int N, class F> __device__ __forceinline__ void sfor(F&& f) {
  if constexpr (N > 0) { sfor<N - 1>(f); f(std::integral_constant<int, N - 1>{}); }
}
__device__ __forceinline__ void set_flag(char* p, int i, unsigned ep) {
  if (i < 32) __hip_atomic_store((gu32*)p + i, ep, __ATOMIC_RELAXED, __HIP_MEMORY_SCOPE_AGENT);
}
template <int NL>
__device__ __forceinline__ void poll_flags(const char* p, int n, int lane, unsigned ep) {
  for (int it = 0; it < 20000; ++it) {
    bool ok = true;
#pragma unroll
    for (int i = 0; i < NL; ++i) {
      const int f = lane + 64 * i;
      ok &= __hip_atomic_load((gu32*)(p + (f < n ? f : 0) * 128), __ATOMIC_RELAXED, __HIP_MEMORY_SCOPE_AGENT) == ep;
    }
    if (__all(ok)) break;
    __builtin_amdgcn_s_sleep(YSLEEP);
  }
  asm volatile("" ::: "memory");
}

constexpr int CV_HP = (2048 + 32) * 2, CV_UR = (2048 + 256) * 2, CV_CH = 2 * CV_HP + CV_UR;
constexpr int NCV = 174;
constexpr int CV_STG = 8 * CV_CH;
constexpr int YP = D_ * 16;

struct KvRegs { u32x4v k0, k1, k2, k3, k4, k5, k6, v0, v1, v2, v3, s0, s1, s2, s3; };
#define KV_LOADS                                                                                                                       \
  "v_lshrrev_b32 %[a], 3, %[tid]\n\tv_and_b32 %[c], 7, %[tid]\n\ts_lshr_b32 s90, %[tile], 4\n\ts_and_b32 s91, %[tile], 15\n\t"          \
  "v_lshlrev_b32 %[a], 13, %[a]\n\tv_lshlrev_b32 %[c], 4, %[c]\n\ts_lshl_b32 s90, s90, 18\n\ts_lshl_b32 s91, s91, 7\n\t"                 \
  "v_add_u32 %[a], s90, %[a]\n\tv_add_u32 %[c], s91, %[c]\n\tv_subrev_u32 %[h], 0x1000, %[a]\n\tv_max_i32 %[h], 0x800, %[h]\n\t"         \
  "v_add3_u32 %[a], %[a], %[c], s92\n\tv_add_u32 %[h], %[h], %[c]\n\tv_lshlrev_b32 %[c], 2, %[c]\n\ts_waitcnt lgkmcnt(0)\n\t"            \
  "global_load_dwordx4 %3, %[a], s[76:77] offset:-4096 nt\n\tglobal_load_dwordx4 %4, %[a], s[76:77] offset:-2048 nt\n\t"                \
  "global_load_dwordx4 %5, %[a], s[76:77] nt\n\tglobal_load_dwordx4 %6, %[a], s[76:77] offset:2048 nt\n\t"                              \
  "global_load_dwordx4 %7, %[a], s[78:79] offset:-4096 nt\n\tglobal_load_dwordx4 %8, %[a], s[78:79] offset:-2048 nt\n\t"                \
  "global_load_dwordx4 %9, %[a], s[78:79] nt\n\tglobal_load_dwordx4 %10, %[a], s[78:79] offset:2048 nt\n\t"                             \
  "global_load_dwordx4 %0, %[h], s[76:77] offset:-2048 nt\n\tglobal_load_dwordx4 %1, %[h], s[76:77] nt\n\t"                             \
  "global_load_dwordx4 %2, %[h], s[76:77] offset:2048 nt\n\t"                                                                           \
  "global_load_dwordx4 %11, %[c], s[80:81]\n\tglobal_load_dwordx4 %12, %[c], s[80:81] offset:16\n\t"                                    \
  "global_load_dwordx4 %13, %[c], s[80:81] offset:32\n\tglobal_load_dwordx4 %14, %[c], s[80:81] offset:48\n\t"
#define KV_OUTS(R) "=&v"(R.k0), "=&v"(R.k1), "=&v"(R.k2), "=&v"(R.k3), "=&v"(R.k4), "=&v"(R.k5), "=&v"(R.k6), "=&v"(R.v0), "=&v"(R.v1), "=&v"(R.v2), \
                   "=&v"(R.v3), "=&v"(R.s0), "=&v"(R.s1), "=&v"(R.s2), "=&v"(R.s3), [a] "=&v"(ta), [h] "=&v"(th), [c] "=&v"(tc)
#define KV_CLOB "s74", "s75", "s76", "s77", "s78", "s79", "s80", "s81", "s82", "s83", "s90", "s91", "s92", "scc", "vcc", "memory"
template <int N>
__device__ __forceinline__ void kv_wait(KvRegs& R) {
  asm volatile("s_waitcnt vmcnt(%15)" : "+v"(R.k0), "+v"(R.k1), "+v"(R.k2), "+v"(R.k3), "+v"(R.k4), "+v"(R.k5), "+v"(R.k6), "+v"(R.v0), "+v"(R.v1), "+v"(R.v2),
               "+v"(R.v3), "+v"(R.s0), "+v"(R.s1), "+v"(R.s2), "+v"(R.s3) : "n"(N) : "memory");
}
__device__ __forceinline__ void kv_store(KvRegs& R, char* __restrict__ ws, char* T, int b, int tid) {
  const int t0 = (b >> 4) * 128, c0 = (b & 15) * 64, ch8 = tid & 7, rg = tid >> 3;
  if (t0 + 4 * rg < 3) R.k0 = R.k1 = R.k2 = (u32x4v){0u, 0u, 0u, 0u};
  typedef unsigned u32x2 __attribute__((ext_vector_type(2)));
  const u32x4v kr[7] = {R.k0, R.k1, R.k2, R.k3, R.k4, R.k5, R.k6}, vr[4] = {R.v0, R.v1, R.v2, R.v3}, sk[4] = {R.s0, R.s1, R.s2, R.s3};
#pragma unroll
  for (int j2 = 0; j2 < 4; ++j2) {
    const f32x2 sa = up2(sk[j2][0]), sb = up2(sk[j2][1]), sc = up2(sk[j2][2]), sd = up2(sk[j2][3]);
    const f32x2 s0 = {sa[0], sc[0]}, s1 = {sa[1], sc[1]}, s2 = {sb[0], sd[0]}, s3 = {sb[1], sd[1]};
    f32x2 kf[7], o[4];
#pragma unroll
    for (int r = 0; r < 7; ++r) kf[r] = up2(kr[r][j2]);
#pragma unroll
    for (int r = 0; r < 4; ++r)
      o[r] = __builtin_elementwise_fma(kf[r + 3], s0, __builtin_elementwise_fma(kf[r + 2], s1, __builtin_elementwise_fma(kf[r + 1], s2, kf[r] * s3))) * up2(vr[r][j2]);
#pragma unroll
    for (int e = 0; e < 2; ++e)
      *(u32x2*)(T + (8 * ch8 + 2 * j2 + e) * 264 + (124 - 4 * rg) * 2) = (u32x2){pk2((f32x2){o[3][e], o[2][e]}), pk2((f32x2){o[1][e], o[0][e]})};
  }
  __syncthreads();
  u16* kvr = (u16*)(ws + OFF_KVR);
#pragma unroll
  for (int i = 0; i < 4; ++i) {
    const int idx = tid + 256 * i, ch = idx >> 4, cb = idx & 15;
    const u16x4 lo = *(const u16x4*)(T + ch * 264 + cb * 16), hi = *(const u16x4*)(T + ch * 264 + cb * 16 + 8);
    st16(kvr + (size_t)(c0 + ch) * S_ + (1920 - t0) + cb * 8, __builtin_shufflevector(lo, hi, 0, 1, 2, 3, 4, 5, 6, 7));
  }
}

#define LASTSLEEP 4
__device__ __forceinline__ void wait_flags(const char* p, unsigned ep) {
  gu32* f = (gu32*)p;
  for (int it = 0; it < 20000; ++it) {
    if (__all(__hip_atomic_load(f, __ATOMIC_RELAXED, __HIP_MEMORY_SCOPE_AGENT) == ep)) break;
    __builtin_amdgcn_s_sleep(LASTSLEEP);
  }
  asm volatile("" ::: "memory");
}
__device__ __forceinline__ void row_last(char* __restrict__ ws, u16* __restrict__ out, int t, int lane, int wave, unsigned ep) {
  const __amdgpu_buffer_rsrc_t rs = __builtin_amdgcn_make_buffer_rsrc(ws, 0, WS_USED, 0x00020000);
  const int r8 = lane & 7, seg = lane >> 3, un = 128 + (t >> 1);
  const unsigned fo = un * 32768 + (wave * 8 + (seg >> 2)) * 1024 + ((seg & 3) * 16 + (t & 1) * 8 + r8) * 16;
  wait_flags(ws + OFF_FP + un * 2048 + ((lane & 2) * 4 + wave * 2 + (lane & 1)) * 128, ep);
  u32x4v p0[4], p1[4];
#pragma unroll
  for (int i = 0; i < 4; ++i) {
    p0[i] = __builtin_bit_cast(u32x4v, __builtin_amdgcn_raw_buffer_load_b128(rs, fo + i * 2048, OFF_P0, 16));
    p1[i] = __builtin_bit_cast(u32x4v, __builtin_amdgcn_raw_buffer_load_b128(rs, fo + i * 2048, OFF_P1, 16));
  }
#pragma unroll
  for (int i = 0; i < 4; ++i) {
    u32x4v o;
#pragma unroll
    for (int e = 0; e < 4; ++e) o[e] = pk2(up2(p0[i][e]) + up2(p1[i][e]));
    st16(out + (size_t)(t * 8 + r8) * D_ + wave * 256 + i * 64 + seg * 8, __builtin_bit_cast(u16x8, o));
  }
}

__device__ __forceinline__ int swzB(int r) { return ((((r >> 1) & 1) | (((r >> 3) & 1) << 1)) << 1); }
#define TCH 1
#define LANE_ID(x) int x; asm volatile("v_mbcnt_lo_u32_b32 %0, -1, 0\n\tv_mbcnt_hi_u32_b32 %0, -1, %0" : "=v"(x))
#define W12 6
#define W_B1 (b == 1)
#define W_B7 (b == 7)
#define W_I15 (idx == 15)
#define W_I31 (idx == 31)
#define W_B249 (b == 249)
#define RLN 3
#define QTB 1
constexpr int QT0 = 34 * 128;
#define CODE_MAX(x, y) "(((" x ">" y ")&" x ")|((" x "<=" y ")&" y "))"
constexpr int HU = 40960;
#define G3OR 5
constexpr int NRW = 22 - G3OR;
__global__ __launch_bounds__(256, 1) void flow_kernel(const u16* __restrict__ q, const u16* __restrict__ kin, const u16* __restrict__ vin,
                                                      const u16* __restrict__ shift_k, const u16* __restrict__ ssm_k, const u16* __restrict__ ssm_D,
                                                      const u16* __restrict__ w1, const u16* __restrict__ w2,
                                                      const u16* __restrict__ w3, const u16* __restrict__ w4, char* __restrict__ ws,
                                                      u16* __restrict__ out) {
  __shared__ __attribute__((aligned(16))) char lds[163840];
  KvRegs KR;
  unsigned ew, ta, th, tc;
  int b, tile;
  asm volatile("Lcode_b:\n\ts_load_dwordx4 s[76:79], %[ka], 0x8\n\ts_load_dwordx2 s[82:83], %[ka], 0x50\n\t"
               "s_getpc_b64 s[74:75]\n\ts_lshr_b32 s90, %[bi], 3\n\ts_mov_b32 m0, 0x27f00\n\tv_lshlrev_b32 %[a], 5, %[tid]\n\tv_add_lshl_u32 %[a], %[a], s90, 7\n\t"
               "v_min_u32 %[a], " CODE_MAX(CODE_MAX(CODE_MAX("(Lcode_g3_e-Lcode_b)", "(Lcode_g3o_e-Lcode_b)"), CODE_MAX("(Lcode_g4_e-Lcode_b)", "(Lcode_g12_e-Lcode_b)")), "(Lcode_end-Lcode_b)") "-64, %[a]\n\t"
               "global_load_lds_dword %[a], s[74:75]\n\ts_load_dwordx2 s[80:81], %[ka], 0x18\n\t"
               "s_mov_b32 %[b], %[bi]\n\ts_bfe_u32 s91, %[bi], 0x20001\n\ts_lshl2_add_u32 %[tile], s90, s91\n\ts_movk_i32 s92, 0x1000\n\t"
               "s_lshl_b32 s90, %[bi], 7\n\ts_add_u32 %[tile], %[tile], s90\n\ts_add_u32 %[tile], %[tile], 64\n\ts_and_b32 %[tile], %[tile], 255\n\t"
               KV_LOADS "v_mov_b32 %[a], %[eo]\n\tglobal_load_dword %[ew], %[a], s[82:83] sc1"
               : KV_OUTS(KR), [ew] "=&v"(ew), [b] "=&s"(b), [tile] "=&s"(tile)
               : [ka] "s"(__builtin_amdgcn_kernarg_segment_ptr()), [bi] "s"(blockIdx.x), [tid] "v"(threadIdx.x), [eo] "n"(OFF_EP) : KV_CLOB);
  const unsigned t_start = 0;
  const int tid = threadIdx.x, lane = tid & 63, wave = __builtin_amdgcn_readfirstlane(tid >> 6);
  const int xcd = b & 7, idx = b >> 3;
  const unsigned sLds = (unsigned)(size_t)lds;
  const int o8 = idx & 7;
  int kind, rep, slice, kh = 0;
  int j3 = 0, j4 = 0;
  const int xp = xcd >> 1, pr = xp & 1;
  if (!(xcd & 1)) {
    if (idx < 20) { kind = 4; rep = idx < 16 ? (xp >> 1) * 3 + (idx >> 3) : 2; kh = pr; slice = idx < 16 ? o8 : (xp >> 1) * 4 + o8; j4 = rep * 16 + kh * 8 + slice; }
    else if (idx >= 24) { kind = 1; rep = xp; slice = o8; }
    else { kind = 3; rep = 4; slice = xp * 4 + idx - 20; j3 = 72 + slice; }
  } else if (idx >= 24) { kind = 2; rep = xp; slice = o8; }
  else if (idx < 18) { kind = 3; rep = pr * 2 + (idx >= 9); slice = (xp >> 1) * 9 + idx - 9 * (idx >= 9); j3 = rep * 18 + slice; }
  else if (idx < 22 || b == 191) { kind = 6; rep = 0; slice = idx < 22 ? xp * 4 + idx - 18 : 16; }
  else if (idx == 22 || xp == 0) { kind = 5; rep = idx == 22 ? pr * 2 + (xp >> 1) : 4; slice = 18; j3 = 90 + rep; }
  else { kind = 3; rep = 4; slice = 15 + xp; j3 = 72 + slice; }
  CTS(0);
  const unsigned sScr = sLds + 163584;
  unsigned ep = 0;
  auto w_touch = [&](const u16* w) {
#pragma unroll
    for (int i = 0; i < 2; ++i) touch_lds((const char*)w + (size_t)(rep * 256 + i * 128 + (tid >> 1)) * 2048 + slice * 256 + (tid & 1) * 128, sScr);
  };
  {
  const bool isconv = kind >= 3 && kind != 6 && j3 < 94;
  const int nw = (kind == 4 || j3 >= 92) ? 2 : 1, c0 = kind == 4 ? 8 * j4 : j3 < 92 ? 640 + 4 * j3 : j3 < 94 ? 272 + 8 * j3 : 0, cw = c0 + nw * wave;
  const unsigned hd = isconv ? sLds + 2 * wave * CV_CH + 32 : 0x30000u;
  auto h_dma = [&](bool first) {
#pragma unroll
    for (int j = 0; j < 2; ++j) {
      const u16* hp = ssm_k + (size_t)cw * S_ + (isconv && j < nw ? j * S_ + lane * 8 : 0);
      if (first) asm volatile("s_mov_b32 m0, %1\n\ts_nop 0\n\tglobal_load_lds_dwordx4 %0, off nt" :: "v"(hp), "s"(hd + j * CV_CH) : "memory");
      else asm volatile("s_mov_b32 m0, %1\n\ts_nop 0\n\tglobal_load_lds_dwordx4 %0, off offset:1024 nt\n\t"
                        "global_load_lds_dwordx4 %0, off offset:2048 nt\n\tglobal_load_lds_dwordx4 %0, off offset:3072 nt" :: "v"(hp), "s"(hd + j * CV_CH) : "memory");
    }
  };
  auto w_dma = [&](int t0, int t1) {
    const char* wp = (const char*)w1 + (16 * wave + (lane >> 3)) * 2048 + slice * 256;
    const unsigned c0_ = ((lane & 7) ^ swzB(lane >> 3)) * 16, c1_ = 8 * 2048 + ((lane & 7) ^ swzB(8 + (lane >> 3))) * 16;
#pragma unroll
    for (int t = t0; t < t1; ++t)
#pragma unroll
      for (int i = 0; i < 2; ++i)
        asm volatile("s_mov_b32 m0, %1\n\ts_nop 0\n\tglobal_load_lds_dwordx4 %0, off\n\ts_add_u32 m0, m0, 8064\n\ts_nop 0\n\tglobal_load_lds_dwordx4 %0, off offset:128"
                     :: "v"(wp + t * 131072 + (i ? c1_ : c0_)), "s"(sLds + t * 16384 + wave * 2048 + i * 1024) : "scc", "memory");
  };
  if (PRE1 && kind == 1) w_dma(0, 2);
  else { h_dma(true); h_dma(false); }
  if (isconv) *(u32x4v*)(lds + (2 * wave + (lane >> 5)) * CV_CH + 2 * CV_HP + 4096 + (lane & 31) * 16) = (u32x4v){0u, 0u, 0u, 0u};
  if (isconv && lane < 8) *(u32x4v*)(lds + (2 * wave + (lane >> 2)) * CV_CH + ((lane >> 1) & 1) * CV_HP + (lane & 1) * 16) = (u32x4v){0u, 0u, 0u, 0u};
  kv_wait<9>(KR);
  CTS(12);
  kv_store(KR, ws, lds + CV_STG, tile, tid);
  CTS(1);
  if (PRE1 && kind == 1) w_dma(2, 6);
  const unsigned dw = ((const unsigned*)ssm_D)[cw >> 1];
  asm volatile("s_cmp_eq_u32 %1, %2\n\ts_cbranch_scc1 Lew%=\n\ts_waitcnt vmcnt(0)\nLew%=:\n\ts_waitcnt vmcnt(16)" : "+v"(ew) : "s"(__builtin_amdgcn_readfirstlane(kind)), "n"(PRE1 ? 1 : 99) : "scc", "memory");
  ep = __builtin_amdgcn_readfirstlane(ew) + 1u;
  if (ep == 0u) ep = 1u;
  __builtin_amdgcn_s_barrier();
  set_flag(ws + OFF_FKV + tile * 128, tid, ep);
  CTS(2);
  if (kind < 3 && (W12 & kind) != 0) w_touch(kind == 1 ? w1 : w2);
  if (kind == 2 && (W12 & 4) != 0) w_touch(w1);
  if (isconv) {
    LANE_ID(lane);
    const int tid = wave * 64 + lane;
    const unsigned sC0 = (cw & 1) ? dw & 0xffff0000u : dw << 16, sC1 = dw & 0xffff0000u;
    const int l15 = lane & 15, lg = lane >> 4, cp = (l15 + 1) & 1, iw = kind == 4 ? j4 : 80 + j3, own = 2 * wave * CV_CH;
    const int ch = tid & (4 * nw - 1), tb = nw == 2 ? tid >> 3 : tid >> 2, slot = nw == 2 ? ch : 2 * ch;
    const unsigned sWave = wave, sBuf = nw, sAuxOff = OFF_KVR + cw * 4096, sFoutOff = OFF_FY + iw * 128, sExp = __builtin_amdgcn_readfirstlane(ep), sDbg = 110u * 1048576u + (b * 4 + wave) * 8192;
    const unsigned vL16 = lane * 16, vPA = own + cp * CV_HP + 2 * (8 * lg + l15 + 1 - cp), vPB = own + 2 * CV_HP + 2 * (2032 - 16 * l15 + 8 * lg) - 512 * 7;
    const unsigned vPU = own + 2 * CV_HP + 2 * (2044 - 16 * l15 - 4 * lg) - 512 * 7, vPW = 32 * l15 + 8 * lg;
    const unsigned vOR = slot * CV_CH + tb * 16, vORA = CV_STG + slot * 1024 + tb * 16, vOG = tb * YP + (c0 + ch) * 16;
    const unsigned vFA = ((lane & 3) * 16 + (c0 >> 6)) * 128, vFB = ((4 + lane % 12) * 16 + (c0 >> 6)) * 128, vFlag = tid < 32 ? tid * 4 : 0x80000000u;
    const unsigned vTW = lane < 42 ? (iw * 168 + wave * 42 + lane) * 128 : 0x80000000u;
    const unsigned vTX = lane < 44 ? ((kind == 4 ? j4 : j3 - 12) * 176 + wave * 44 + lane) * 128 : 0x80000000u;
    (void)sDbg;
    asm volatile(
R"(.macro M d,w,x
v_mfma_f32_16x16x32_bf16 v[\d:\d+3], \w, v[\x:\x+3], v[\d:\d+3]
.endm
.macro R d,a,o
ds_read_b128 v[\d:\d+3], v\a offset:\o
.endm
.macro T d,a,o
ds_read_b64_tr_b16 \d, v\a offset:\o
.endm
.macro D a,s,o
buffer_load_dwordx4 v\a, s[36:39], s\s offen offset:\o sc1 lds
.endm
.macro L c
s_waitcnt lgkmcnt(\c)
.endm
Lcode_cv_b:
s_mov_b64 s[36:37],%0
s_mov_b32 s38,111243392
s_mov_b32 s39,0x00020000
s_mov_b32 s44,%1
s_mov_b32 s51,%2
s_mov_b32 s49,%3
s_mov_b32 s48,%4
s_mov_b32 s86,%5
s_mov_b32 s87,%6
s_mov_b32 s53,%7
v_mov_b32 v0,%8
v_mov_b32 v1,%9
v_mov_b32 v2,%10
v_mov_b32 v3,%11
v_mov_b32 v4,%12
v_mov_b32 v5,%13
v_mov_b32 v6,%14
v_mov_b32 v7,%15
v_mov_b32 v8,%16
v_mov_b32 v9,%17
v_mov_b32 v10,%18
v_mov_b32 v11,%19
v_mov_b32 v12,%20
s_mov_b32 s58,111144960
buffer_load_dword v13,v8,s[36:39],s58 offen offset:0 sc1
s_mul_i32 s57,s44,25856
v_add_u32 v18,s57,v0
v_add_u32 v18,32,v18
v_mov_b32 v19,s57
R 88,18,0
ds_read_b32 v92,v18 offset:16
R 152,18,12928
ds_read_b32 v156,v18 offset:12944
ds_read_b32 v24,v19 offset:32
ds_read_b32 v25,v19 offset:12960
s_mov_b64 s[40:41],%21
s_mov_b32 s42,7340032
s_mov_b32 s43,0x00020000
s_mov_b64 s[60:61],%22
s_mov_b32 s62,7340032
s_mov_b32 s63,0x00020000
v_mov_b32 v23,s53
s_mov_b32 s82,0
s_add_u32 s55,s49,4096
s_cmp_eq_u32 s51,2
s_cselect_b32 s55,s55,0x7fff0000
s_cselect_b32 s56,0,0x70000000
s_add_u32 s57,s57,8320
v_add_u32 v14,1024,v1
v_add_u32 v15,2048,v1
v_add_u32 v16,3072,v1
v_add_u32 v17,12928,v1
L 4
v_alignbyte_b32 v96,v89,v88,2
v_alignbyte_b32 v97,v90,v89,2
v_alignbyte_b32 v98,v91,v90,2
v_alignbyte_b32 v99,v92,v91,2
ds_write_b128 v18,v[96:99] offset:4160
L 3
v_alignbyte_b32 v160,v153,v152,2
v_alignbyte_b32 v161,v154,v153,2
v_alignbyte_b32 v162,v155,v154,2
v_alignbyte_b32 v163,v156,v155,2
ds_write_b128 v18,v[160:163] offset:17088
L 2
v_lshlrev_b32 v24,16,v24
v_lshlrev_b32 v25,16,v25
s_mov_b64 exec,1
ds_write_b32 v19,v24 offset:4188
ds_write_b32 v19,v25 offset:17116
s_mov_b64 exec,-1
ds_read2_b32 a[0:1],v1 offset0:0 offset1:1
ds_read2_b32 a[2:3],v1 offset0:2 offset1:3
ds_read2_b32 a[4:5],v1 offset0:128 offset1:129
ds_read2_b32 a[6:7],v1 offset0:130 offset1:131
ds_read2_b32 a[8:9],v1 offset0:16 offset1:17
ds_read2_b32 a[10:11],v1 offset0:18 offset1:19
ds_read2_b32 a[12:13],v1 offset0:144 offset1:145
ds_read2_b32 a[14:15],v1 offset0:146 offset1:147
ds_read2_b32 a[16:17],v1 offset0:32 offset1:33
ds_read2_b32 a[18:19],v1 offset0:34 offset1:35
ds_read2_b32 a[20:21],v1 offset0:160 offset1:161
ds_read2_b32 a[22:23],v1 offset0:162 offset1:163
ds_read2_b32 a[24:25],v1 offset0:48 offset1:49
ds_read2_b32 a[26:27],v1 offset0:50 offset1:51
ds_read2_b32 a[28:29],v1 offset0:176 offset1:177
ds_read2_b32 a[30:31],v1 offset0:178 offset1:179
ds_read2_b32 a[32:33],v1 offset0:64 offset1:65
ds_read2_b32 a[34:35],v1 offset0:66 offset1:67
ds_read2_b32 a[36:37],v1 offset0:192 offset1:193
ds_read2_b32 a[38:39],v1 offset0:194 offset1:195
ds_read2_b32 a[40:41],v1 offset0:80 offset1:81
ds_read2_b32 a[42:43],v1 offset0:82 offset1:83
ds_read2_b32 a[44:45],v1 offset0:208 offset1:209
ds_read2_b32 a[46:47],v1 offset0:210 offset1:211
ds_read2_b32 a[48:49],v1 offset0:96 offset1:97
ds_read2_b32 a[50:51],v1 offset0:98 offset1:99
ds_read2_b32 a[52:53],v1 offset0:224 offset1:225
ds_read2_b32 a[54:55],v1 offset0:226 offset1:227
ds_read2_b32 a[56:57],v1 offset0:112 offset1:113
ds_read2_b32 a[58:59],v1 offset0:114 offset1:115
ds_read2_b32 a[60:61],v1 offset0:240 offset1:241
ds_read2_b32 a[62:63],v1 offset0:242 offset1:243
ds_read2_b32 a[64:65],v17 offset0:0 offset1:1
ds_read2_b32 a[66:67],v17 offset0:2 offset1:3
ds_read2_b32 a[68:69],v17 offset0:128 offset1:129
ds_read2_b32 a[70:71],v17 offset0:130 offset1:131
ds_read2_b32 a[72:73],v17 offset0:16 offset1:17
ds_read2_b32 a[74:75],v17 offset0:18 offset1:19
ds_read2_b32 a[76:77],v17 offset0:144 offset1:145
ds_read2_b32 a[78:79],v17 offset0:146 offset1:147
ds_read2_b32 a[80:81],v17 offset0:32 offset1:33
ds_read2_b32 a[82:83],v17 offset0:34 offset1:35
ds_read2_b32 a[84:85],v17 offset0:160 offset1:161
ds_read2_b32 a[86:87],v17 offset0:162 offset1:163
ds_read2_b32 a[88:89],v17 offset0:48 offset1:49
ds_read2_b32 a[90:91],v17 offset0:50 offset1:51
ds_read2_b32 a[92:93],v17 offset0:176 offset1:177
ds_read2_b32 a[94:95],v17 offset0:178 offset1:179
ds_read2_b32 a[96:97],v17 offset0:64 offset1:65
ds_read2_b32 a[98:99],v17 offset0:66 offset1:67
ds_read2_b32 a[100:101],v17 offset0:192 offset1:193
ds_read2_b32 a[102:103],v17 offset0:194 offset1:195
ds_read2_b32 a[104:105],v17 offset0:80 offset1:81
ds_read2_b32 a[106:107],v17 offset0:82 offset1:83
ds_read2_b32 a[108:109],v17 offset0:208 offset1:209
ds_read2_b32 a[110:111],v17 offset0:210 offset1:211
ds_read2_b32 a[112:113],v17 offset0:96 offset1:97
ds_read2_b32 a[114:115],v17 offset0:98 offset1:99
ds_read2_b32 a[116:117],v17 offset0:224 offset1:225
ds_read2_b32 a[118:119],v17 offset0:226 offset1:227
ds_read2_b32 a[120:121],v17 offset0:112 offset1:113
ds_read2_b32 a[122:123],v17 offset0:114 offset1:115
ds_read2_b32 a[124:125],v17 offset0:240 offset1:241
ds_read2_b32 a[126:127],v17 offset0:242 offset1:243
Lcv_bpa_%=:
s_waitcnt vmcnt(0)
v_cmp_eq_u32 vcc,s53,v13
s_add_u32 s82,s82,1
s_cmp_eq_u64 vcc,-1
s_cbranch_scc1 Lcv_bqa_%=
s_sleep 1
buffer_load_dword v13,v8,s[36:39],s58 offen offset:0 sc1
s_cmp_lt_u32 s82,20000
s_cbranch_scc1 Lcv_bpa_%=
s_waitcnt vmcnt(0)
Lcv_bqa_%=:
s_add_u32 m0,s57,0
s_nop 0
D 0,49,3072
s_add_u32 m0,s57,12928
s_nop 0
D 0,55,3072
buffer_load_dword v13,v9,s[36:39],s58 offen offset:0 sc1
R 88,18,0
ds_read_b32 v92,v18 offset:16
R 104,18,1024
ds_read_b32 v108,v18 offset:1040
R 120,18,2048
ds_read_b32 v124,v18 offset:2064
R 136,18,3072
ds_read_b32 v140,v18 offset:3088
L 6
v_alignbyte_b32 v96,v89,v88,2
v_alignbyte_b32 v97,v90,v89,2
v_alignbyte_b32 v98,v91,v90,2
v_alignbyte_b32 v99,v92,v91,2
ds_write_b128 v18,v[96:99] offset:4160
L 5
v_alignbyte_b32 v112,v105,v104,2
v_alignbyte_b32 v113,v106,v105,2
v_alignbyte_b32 v114,v107,v106,2
v_alignbyte_b32 v115,v108,v107,2
ds_write_b128 v18,v[112:115] offset:5184
L 4
v_alignbyte_b32 v128,v121,v120,2
v_alignbyte_b32 v129,v122,v121,2
v_alignbyte_b32 v130,v123,v122,2
v_alignbyte_b32 v131,v124,v123,2
ds_write_b128 v18,v[128:131] offset:6208
L 3
v_alignbyte_b32 v144,v137,v136,2
v_alignbyte_b32 v145,v138,v137,2
v_alignbyte_b32 v146,v139,v138,2
v_alignbyte_b32 v147,v140,v139,2
ds_write_b128 v18,v[144:147] offset:7232
R 152,18,12928
ds_read_b32 v156,v18 offset:12944
R 168,18,13952
ds_read_b32 v172,v18 offset:13968
R 184,18,14976
ds_read_b32 v188,v18 offset:14992
R 200,18,16000
ds_read_b32 v204,v18 offset:16016
L 6
v_alignbyte_b32 v160,v153,v152,2
v_alignbyte_b32 v161,v154,v153,2
v_alignbyte_b32 v162,v155,v154,2
v_alignbyte_b32 v163,v156,v155,2
ds_write_b128 v18,v[160:163] offset:17088
L 5
v_alignbyte_b32 v176,v169,v168,2
v_alignbyte_b32 v177,v170,v169,2
v_alignbyte_b32 v178,v171,v170,2
v_alignbyte_b32 v179,v172,v171,2
ds_write_b128 v18,v[176:179] offset:18112
L 4
v_alignbyte_b32 v192,v185,v184,2
v_alignbyte_b32 v193,v186,v185,2
v_alignbyte_b32 v194,v187,v186,2
v_alignbyte_b32 v195,v188,v187,2
ds_write_b128 v18,v[192:195] offset:19136
L 3
v_alignbyte_b32 v208,v201,v200,2
v_alignbyte_b32 v209,v202,v201,2
v_alignbyte_b32 v210,v203,v202,2
v_alignbyte_b32 v211,v204,v203,2
ds_write_b128 v18,v[208:211] offset:20160
s_lshl_b32 s78,s44,11
s_add_u32 s78,s78,103424
v_add_u32 v18,s78,v4
v_add_u32 v4,v19,v4
s_waitcnt vmcnt(0)
v_cmp_eq_u32 vcc,s53,v13
s_mov_b32 s83,0
s_cmp_eq_u64 vcc,-1
s_cbranch_scc0 Lcv_a_%=
s_mov_b32 s83,1
s_add_u32 m0,s57,0
s_nop 0
D 0,49,2048
D 0,49,1024
D 0,49,0
s_add_u32 m0,s57,12928
s_nop 0
D 0,55,2048
D 0,55,1024
D 0,55,0
Lcv_a_%=:
s_cmp_eq_u32 s51,2
s_cbranch_scc1 Lcv_a2_%=
L 0
R 88,2,3584
R 92,2,3072
R 96,2,3648
R 100,2,3136
R 104,2,3712
R 108,2,3200
R 112,2,3776
R 116,2,3264
L 6
v_mfma_f32_16x16x32_bf16 v[40:43],a[0:3],v[88:91],0
R 120,2,3840
R 124,2,3328
v_mfma_f32_16x16x32_bf16 v[44:47],a[0:3],v[92:95],0
M 44,a[4:7],88
L 6
M 40,a[8:11],96
R 128,2,3904
R 132,2,3392
M 44,a[8:11],100
M 44,a[12:15],96
L 6
M 40,a[16:19],104
R 136,2,3968
R 140,2,3456
M 44,a[16:19],108
M 44,a[20:23],104
L 6
M 40,a[24:27],112
R 144,2,4032
R 148,2,3520
M 44,a[24:27],116
M 44,a[28:31],112
L 6
M 40,a[32:35],120
M 44,a[32:35],124
M 44,a[36:39],120
L 4
M 40,a[40:43],128
M 44,a[40:43],132
M 44,a[44:47],128
L 2
M 40,a[48:51],136
ds_read_b64 v[24:25],v3 offset:3584
ds_read_b64 v[26:27],v3 offset:3072
M 44,a[48:51],140
M 44,a[52:55],136
L 2
M 40,a[56:59],144
M 44,a[56:59],148
M 44,a[60:63],144
L 0
v_and_b32 v36,0xffff0000,v25
v_lshlrev_b32 v37,16,v25
v_and_b32 v38,0xffff0000,v24
v_lshlrev_b32 v39,16,v24
s_nop 0
v_fma_f32 v40,v36,s86,v40
v_fma_f32 v41,v37,s86,v41
v_fma_f32 v42,v38,s86,v42
v_fma_f32 v43,v39,s86,v43
v_cvt_pk_bf16_f32 v40,v40,v41
v_cvt_pk_bf16_f32 v41,v42,v43
ds_write_b64 v18,v[40:41] offset:0
v_and_b32 v36,0xffff0000,v27
v_lshlrev_b32 v37,16,v27
v_and_b32 v38,0xffff0000,v26
v_lshlrev_b32 v39,16,v26
v_fma_f32 v44,v36,s86,v44
v_fma_f32 v45,v37,s86,v45
v_fma_f32 v46,v38,s86,v46
v_fma_f32 v47,v39,s86,v47
v_cvt_pk_bf16_f32 v44,v44,v45
v_cvt_pk_bf16_f32 v45,v46,v47
ds_write_b64 v18,v[44:45] offset:512
s_branch Lcv_a3_%=
Lcv_a2_%=:
L 0
R 88,2,3584
R 92,2,3072
R 96,2,3648
R 100,2,3136
R 104,2,3712
R 108,2,3200
R 112,2,3776
R 116,2,3264
L 6
v_mfma_f32_16x16x32_bf16 v[40:43],a[0:3],v[88:91],0
R 120,2,3840
R 124,2,3328
v_mfma_f32_16x16x32_bf16 v[44:47],a[0:3],v[92:95],0
M 44,a[4:7],88
L 6
M 40,a[8:11],96
R 128,2,3904
R 132,2,3392
M 44,a[8:11],100
M 44,a[12:15],96
L 6
M 40,a[16:19],104
R 136,2,3968
R 140,2,3456
M 44,a[16:19],108
M 44,a[20:23],104
L 6
M 40,a[24:27],112
R 144,2,4032
R 148,2,3520
M 44,a[24:27],116
M 44,a[28:31],112
L 6
M 40,a[32:35],120
R 152,2,16512
R 156,2,16000
M 44,a[32:35],124
M 44,a[36:39],120
L 6
M 40,a[40:43],128
R 160,2,16576
R 164,2,16064
M 44,a[40:43],132
M 44,a[44:47],128
L 6
M 40,a[48:51],136
R 168,2,16640
R 172,2,16128
M 44,a[48:51],140
ds_read_b64 v[24:25],v3 offset:3584
ds_read_b64 v[26:27],v3 offset:3072
M 44,a[52:55],136
L 8
M 40,a[56:59],144
R 176,2,16704
R 180,2,16192
M 44,a[56:59],148
M 44,a[60:63],144
L 8
v_mfma_f32_16x16x32_bf16 v[48:51],a[64:67],v[152:155],0
R 184,2,16768
R 188,2,16256
v_mfma_f32_16x16x32_bf16 v[52:55],a[64:67],v[156:159],0
M 52,a[68:71],152
L 8
M 48,a[72:75],160
R 192,2,16832
R 196,2,16320
M 52,a[72:75],164
L 6
v_and_b32 v36,0xffff0000,v25
M 52,a[76:79],160
v_lshlrev_b32 v37,16,v25
v_and_b32 v38,0xffff0000,v24
v_lshlrev_b32 v39,16,v24
s_nop 0
M 48,a[80:83],168
R 200,2,16896
R 204,2,16384
M 52,a[80:83],172
v_fma_f32 v40,v36,s86,v40
v_fma_f32 v41,v37,s86,v41
M 52,a[84:87],168
v_fma_f32 v42,v38,s86,v42
v_fma_f32 v43,v39,s86,v43
v_cvt_pk_bf16_f32 v40,v40,v41
L 6
M 48,a[88:91],176
R 208,2,16960
R 212,2,16448
M 52,a[88:91],180
v_cvt_pk_bf16_f32 v41,v42,v43
ds_write_b64 v18,v[40:41] offset:0
M 52,a[92:95],176
v_and_b32 v36,0xffff0000,v27
v_lshlrev_b32 v37,16,v27
v_and_b32 v38,0xffff0000,v26
L 7
M 48,a[96:99],184
M 52,a[96:99],188
v_lshlrev_b32 v39,16,v26
v_fma_f32 v44,v36,s86,v44
M 52,a[100:103],184
v_fma_f32 v45,v37,s86,v45
v_fma_f32 v46,v38,s86,v46
v_fma_f32 v47,v39,s86,v47
L 5
M 48,a[104:107],192
M 52,a[104:107],196
v_cvt_pk_bf16_f32 v44,v44,v45
v_cvt_pk_bf16_f32 v45,v46,v47
M 52,a[108:111],192
ds_write_b64 v18,v[44:45] offset:512
L 4
M 48,a[112:115],200
ds_read_b64 v[24:25],v3 offset:16512
ds_read_b64 v[26:27],v3 offset:16000
M 52,a[112:115],204
M 52,a[116:119],200
L 4
M 48,a[120:123],208
M 52,a[120:123],212
M 52,a[124:127],208
L 0
v_and_b32 v36,0xffff0000,v25
v_lshlrev_b32 v37,16,v25
v_and_b32 v38,0xffff0000,v24
v_lshlrev_b32 v39,16,v24
s_nop 0
v_fma_f32 v48,v36,s87,v48
v_fma_f32 v49,v37,s87,v49
v_fma_f32 v50,v38,s87,v50
v_fma_f32 v51,v39,s87,v51
v_cvt_pk_bf16_f32 v48,v48,v49
v_cvt_pk_bf16_f32 v49,v50,v51
ds_write_b64 v18,v[48:49] offset:1024
v_and_b32 v36,0xffff0000,v27
v_lshlrev_b32 v37,16,v27
v_and_b32 v38,0xffff0000,v26
v_lshlrev_b32 v39,16,v26
v_fma_f32 v52,v36,s87,v52
v_fma_f32 v53,v37,s87,v53
v_fma_f32 v54,v38,s87,v54
v_fma_f32 v55,v39,s87,v55
v_cvt_pk_bf16_f32 v52,v52,v53
v_cvt_pk_bf16_f32 v53,v54,v55
ds_write_b64 v18,v[52:53] offset:1536
Lcv_a3_%=:
L 0
s_barrier
R 152,6,0
R 156,6,512
s_mov_b32 s78,98566144
L 1
buffer_store_dwordx4 v[152:155],v7,s[36:39],s78 offen offset:0 sc0 sc1
s_add_u32 s78,s56,99090432
L 0
buffer_store_dwordx4 v[156:159],v7,s[36:39],s78 offen offset:0 sc0 sc1
s_cmp_eq_u32 s83,1
s_cbranch_scc1 Lcv_b_%=
s_waitcnt vmcnt(0)
s_barrier
buffer_store_dword v23,v10,s[36:39],s48 offen offset:0 sc0 sc1
buffer_load_dword v13,v9,s[36:39],s58 offen offset:0 sc1
Lcv_bpb_%=:
)"
R"(s_waitcnt vmcnt(0)
v_cmp_eq_u32 vcc,s53,v13
s_add_u32 s82,s82,1
s_cmp_eq_u64 vcc,-1
s_cbranch_scc1 Lcv_bqb_%=
s_sleep 1
buffer_load_dword v13,v9,s[36:39],s58 offen offset:0 sc1
s_cmp_lt_u32 s82,20000
s_cbranch_scc1 Lcv_bpb_%=
s_waitcnt vmcnt(0)
Lcv_bqb_%=:
s_add_u32 m0,s57,0
s_nop 0
D 0,49,2048
D 0,49,1024
D 0,49,0
s_add_u32 m0,s57,12928
s_nop 0
D 0,55,2048
D 0,55,1024
D 0,55,0
s_waitcnt vmcnt(0)
Lcv_b_%=:
s_waitcnt vmcnt(2)
ds_read2_b32 v[88:89],v1 offset0:0 offset1:1
ds_read2_b32 v[90:91],v1 offset0:2 offset1:3
R 120,2,3584
ds_read2_b32 v[92:93],v1 offset0:128 offset1:129
ds_read2_b32 v[94:95],v1 offset0:130 offset1:131
R 124,2,3072
ds_read2_b32 v[96:97],v14 offset0:0 offset1:1
ds_read2_b32 v[98:99],v14 offset0:2 offset1:3
R 128,2,2560
ds_read2_b32 v[100:101],v14 offset0:128 offset1:129
ds_read2_b32 v[102:103],v14 offset0:130 offset1:131
R 132,2,2048
ds_read2_b32 v[104:105],v15 offset0:0 offset1:1
ds_read2_b32 v[106:107],v15 offset0:2 offset1:3
R 136,2,1536
ds_read2_b32 v[108:109],v15 offset0:128 offset1:129
ds_read2_b32 v[110:111],v15 offset0:130 offset1:131
R 140,2,1024
ds_read2_b32 v[112:113],v16 offset0:0 offset1:1
ds_read2_b32 v[114:115],v16 offset0:2 offset1:3
R 144,2,512
ds_read2_b32 v[116:117],v16 offset0:128 offset1:129
ds_read2_b32 v[118:119],v16 offset0:130 offset1:131
R 148,2,0
L 15
v_mfma_f32_16x16x32_bf16 v[40:43],v[88:91],v[128:131],0
ds_read2_b32 v[152:153],v1 offset0:16 offset1:17
M 40,v[92:95],124
ds_read2_b32 v[154:155],v1 offset0:18 offset1:19
M 40,v[96:99],120
L 14
R 184,2,3648
v_mfma_f32_16x16x32_bf16 v[44:47],v[88:91],v[132:135],0
ds_read2_b32 v[156:157],v1 offset0:144 offset1:145
M 44,v[92:95],128
M 44,v[96:99],124
ds_read2_b32 v[158:159],v1 offset0:146 offset1:147
M 44,v[100:103],120
L 14
R 188,2,3136
v_mfma_f32_16x16x32_bf16 v[48:51],v[88:91],v[136:139],0
ds_read2_b32 v[160:161],v14 offset0:16 offset1:17
M 48,v[92:95],132
ds_read2_b32 v[162:163],v14 offset0:18 offset1:19
M 48,v[96:99],128
M 48,v[100:103],124
R 192,2,2624
M 48,v[104:107],120
L 15
ds_read2_b32 v[164:165],v14 offset0:144 offset1:145
v_mfma_f32_16x16x32_bf16 v[52:55],v[88:91],v[140:143],0
ds_read2_b32 v[166:167],v14 offset0:146 offset1:147
M 52,v[92:95],136
R 196,2,2112
M 52,v[96:99],132
M 52,v[100:103],128
ds_read2_b32 v[168:169],v15 offset0:16 offset1:17
M 52,v[104:107],124
ds_read2_b32 v[170:171],v15 offset0:18 offset1:19
M 52,v[108:111],120
L 15
R 200,2,1600
v_mfma_f32_16x16x32_bf16 v[56:59],v[88:91],v[144:147],0
ds_read2_b32 v[172:173],v15 offset0:144 offset1:145
M 56,v[92:95],140
M 56,v[96:99],136
ds_read2_b32 v[174:175],v15 offset0:146 offset1:147
M 56,v[100:103],132
R 204,2,1088
M 56,v[104:107],128
ds_read2_b32 v[176:177],v16 offset0:16 offset1:17
M 56,v[108:111],124
ds_read2_b32 v[178:179],v16 offset0:18 offset1:19
M 56,v[112:115],120
L 15
v_mfma_f32_16x16x32_bf16 v[60:63],v[88:91],v[148:151],0
R 208,2,576
M 60,v[92:95],144
ds_read2_b32 v[180:181],v16 offset0:144 offset1:145
M 60,v[96:99],140
ds_read2_b32 v[182:183],v16 offset0:146 offset1:147
M 60,v[100:103],136
R 212,2,64
M 60,v[104:107],132
s_cmp_eq_u32 s83,0
s_cbranch_scc1 Lcv_c_%=
s_waitcnt vmcnt(0)
s_barrier
buffer_store_dword v23,v10,s[36:39],s48 offen offset:0 sc0 sc1
Lcv_c_%=:
M 60,v[108:111],128
M 60,v[112:115],124
M 60,v[116:119],120
L 15
M 40,v[152:155],192
ds_read2_b32 v[88:89],v1 offset0:32 offset1:33
M 40,v[156:159],188
ds_read2_b32 v[90:91],v1 offset0:34 offset1:35
M 40,v[160:163],184
L 14
R 120,2,3712
M 44,v[152:155],196
ds_read2_b32 v[92:93],v1 offset0:160 offset1:161
M 44,v[156:159],192
M 44,v[160:163],188
ds_read2_b32 v[94:95],v1 offset0:162 offset1:163
M 44,v[164:167],184
L 14
R 124,2,3200
M 48,v[152:155],200
ds_read2_b32 v[96:97],v14 offset0:32 offset1:33
M 48,v[156:159],196
ds_read2_b32 v[98:99],v14 offset0:34 offset1:35
M 48,v[160:163],192
M 48,v[164:167],188
R 128,2,2688
M 48,v[168:171],184
L 15
ds_read2_b32 v[100:101],v14 offset0:160 offset1:161
M 52,v[152:155],204
ds_read2_b32 v[102:103],v14 offset0:162 offset1:163
M 52,v[156:159],200
R 132,2,2176
M 52,v[160:163],196
M 52,v[164:167],192
ds_read2_b32 v[104:105],v15 offset0:32 offset1:33
M 52,v[168:171],188
ds_read2_b32 v[106:107],v15 offset0:34 offset1:35
M 52,v[172:175],184
L 15
R 136,2,1664
M 56,v[152:155],208
ds_read2_b32 v[108:109],v15 offset0:160 offset1:161
M 56,v[156:159],204
M 56,v[160:163],200
ds_read2_b32 v[110:111],v15 offset0:162 offset1:163
M 56,v[164:167],196
R 140,2,1152
M 56,v[168:171],192
ds_read2_b32 v[112:113],v16 offset0:32 offset1:33
M 56,v[172:175],188
ds_read2_b32 v[114:115],v16 offset0:34 offset1:35
M 56,v[176:179],184
L 15
M 60,v[152:155],212
R 144,2,640
M 60,v[156:159],208
ds_read2_b32 v[116:117],v16 offset0:160 offset1:161
M 60,v[160:163],204
ds_read2_b32 v[118:119],v16 offset0:162 offset1:163
M 60,v[164:167],200
R 148,2,128
M 60,v[168:171],196
M 60,v[172:175],192
M 60,v[176:179],188
M 60,v[180:183],184
L 15
M 40,v[88:91],128
ds_read2_b32 v[152:153],v1 offset0:48 offset1:49
M 40,v[92:95],124
ds_read2_b32 v[154:155],v1 offset0:50 offset1:51
M 40,v[96:99],120
L 14
R 184,2,3776
M 44,v[88:91],132
ds_read2_b32 v[156:157],v1 offset0:176 offset1:177
M 44,v[92:95],128
M 44,v[96:99],124
ds_read2_b32 v[158:159],v1 offset0:178 offset1:179
M 44,v[100:103],120
L 14
R 188,2,3264
M 48,v[88:91],136
ds_read2_b32 v[160:161],v14 offset0:48 offset1:49
M 48,v[92:95],132
ds_read2_b32 v[162:163],v14 offset0:50 offset1:51
M 48,v[96:99],128
M 48,v[100:103],124
R 192,2,2752
M 48,v[104:107],120
L 15
ds_read2_b32 v[164:165],v14 offset0:176 offset1:177
M 52,v[88:91],140
s_mov_b32 m0,163584
s_mov_b32 s78,0
ds_read2_b32 v[166:167],v14 offset0:178 offset1:179
M 52,v[92:95],136
R 196,2,2240
M 52,v[96:99],132
buffer_load_dword v11,s[40:43],s78 offen offset:0  lds
M 52,v[100:103],128
ds_read2_b32 v[168:169],v15 offset0:48 offset1:49
M 52,v[104:107],124
ds_read2_b32 v[170:171],v15 offset0:50 offset1:51
M 52,v[108:111],120
L 15
R 200,2,1728
M 56,v[88:91],144
ds_read2_b32 v[172:173],v15 offset0:176 offset1:177
M 56,v[92:95],140
M 56,v[96:99],136
ds_read2_b32 v[174:175],v15 offset0:178 offset1:179
M 56,v[100:103],132
R 204,2,1216
M 56,v[104:107],128
ds_read2_b32 v[176:177],v16 offset0:48 offset1:49
M 56,v[108:111],124
ds_read2_b32 v[178:179],v16 offset0:50 offset1:51
M 56,v[112:115],120
L 15
M 60,v[88:91],148
R 208,2,704
M 60,v[92:95],144
ds_read2_b32 v[180:181],v16 offset0:176 offset1:177
M 60,v[96:99],140
ds_read2_b32 v[182:183],v16 offset0:178 offset1:179
M 60,v[100:103],136
R 212,2,192
M 60,v[104:107],132
M 60,v[108:111],128
M 60,v[112:115],124
M 60,v[116:119],120
L 15
M 40,v[152:155],192
ds_read2_b32 v[88:89],v1 offset0:64 offset1:65
M 40,v[156:159],188
ds_read2_b32 v[90:91],v1 offset0:66 offset1:67
M 40,v[160:163],184
L 14
R 120,2,3840
M 44,v[152:155],196
ds_read2_b32 v[92:93],v1 offset0:192 offset1:193
M 44,v[156:159],192
M 44,v[160:163],188
ds_read2_b32 v[94:95],v1 offset0:194 offset1:195
M 44,v[164:167],184
L 14
R 124,2,3328
M 48,v[152:155],200
ds_read2_b32 v[96:97],v14 offset0:64 offset1:65
M 48,v[156:159],196
ds_read2_b32 v[98:99],v14 offset0:66 offset1:67
M 48,v[160:163],192
M 48,v[164:167],188
R 128,2,2816
M 48,v[168:171],184
L 15
ds_read2_b32 v[100:101],v14 offset0:192 offset1:193
M 52,v[152:155],204
ds_read2_b32 v[102:103],v14 offset0:194 offset1:195
M 52,v[156:159],200
R 132,2,2304
M 52,v[160:163],196
M 52,v[164:167],192
ds_read2_b32 v[104:105],v15 offset0:64 offset1:65
M 52,v[168:171],188
ds_read2_b32 v[106:107],v15 offset0:66 offset1:67
M 52,v[172:175],184
L 15
R 136,2,1792
M 56,v[152:155],208
ds_read2_b32 v[108:109],v15 offset0:192 offset1:193
M 56,v[156:159],204
M 56,v[160:163],200
ds_read2_b32 v[110:111],v15 offset0:194 offset1:195
M 56,v[164:167],196
R 140,2,1280
M 56,v[168:171],192
ds_read2_b32 v[112:113],v16 offset0:64 offset1:65
M 56,v[172:175],188
ds_read2_b32 v[114:115],v16 offset0:66 offset1:67
M 56,v[176:179],184
L 15
M 60,v[152:155],212
R 144,2,768
M 60,v[156:159],208
ds_read2_b32 v[116:117],v16 offset0:192 offset1:193
M 60,v[160:163],204
ds_read2_b32 v[118:119],v16 offset0:194 offset1:195
M 60,v[164:167],200
R 148,2,256
M 60,v[168:171],196
M 60,v[172:175],192
M 60,v[176:179],188
M 60,v[180:183],184
L 15
M 40,v[88:91],128
ds_read2_b32 v[152:153],v1 offset0:80 offset1:81
M 40,v[92:95],124
ds_read2_b32 v[154:155],v1 offset0:82 offset1:83
M 40,v[96:99],120
L 14
R 184,2,3904
M 44,v[88:91],132
ds_read2_b32 v[156:157],v1 offset0:208 offset1:209
M 44,v[92:95],128
M 44,v[96:99],124
ds_read2_b32 v[158:159],v1 offset0:210 offset1:211
M 44,v[100:103],120
L 14
R 188,2,3392
M 48,v[88:91],136
ds_read2_b32 v[160:161],v14 offset0:80 offset1:81
M 48,v[92:95],132
ds_read2_b32 v[162:163],v14 offset0:82 offset1:83
M 48,v[96:99],128
M 48,v[100:103],124
R 192,2,2880
M 48,v[104:107],120
L 15
ds_read2_b32 v[164:165],v14 offset0:208 offset1:209
M 52,v[88:91],140
ds_read2_b32 v[166:167],v14 offset0:210 offset1:211
M 52,v[92:95],136
R 196,2,2368
M 52,v[96:99],132
M 52,v[100:103],128
ds_read2_b32 v[168:169],v15 offset0:80 offset1:81
M 52,v[104:107],124
ds_read2_b32 v[170:171],v15 offset0:82 offset1:83
M 52,v[108:111],120
L 15
R 200,2,1856
M 56,v[88:91],144
ds_read2_b32 v[172:173],v15 offset0:208 offset1:209
M 56,v[92:95],140
M 56,v[96:99],136
ds_read2_b32 v[174:175],v15 offset0:210 offset1:211
M 56,v[100:103],132
R 204,2,1344
M 56,v[104:107],128
ds_read2_b32 v[176:177],v16 offset0:80 offset1:81
M 56,v[108:111],124
ds_read2_b32 v[178:179],v16 offset0:82 offset1:83
M 56,v[112:115],120
L 15
M 60,v[88:91],148
R 208,2,832
M 60,v[92:95],144
ds_read2_b32 v[180:181],v16 offset0:208 offset1:209
M 60,v[96:99],140
ds_read2_b32 v[182:183],v16 offset0:210 offset1:211
M 60,v[100:103],136
R 212,2,320
M 60,v[104:107],132
M 60,v[108:111],128
M 60,v[112:115],124
M 60,v[116:119],120
L 15
M 40,v[152:155],192
ds_read2_b32 v[88:89],v1 offset0:96 offset1:97
M 40,v[156:159],188
ds_read2_b32 v[90:91],v1 offset0:98 offset1:99
M 40,v[160:163],184
L 14
R 120,2,3968
M 44,v[152:155],196
s_mov_b32 m0,163584
s_mov_b32 s78,3741696
ds_read2_b32 v[92:93],v1 offset0:224 offset1:225
M 44,v[156:159],192
M 44,v[160:163],188
ds_read2_b32 v[94:95],v1 offset0:226 offset1:227
M 44,v[164:167],184
L 14
R 124,2,3456
M 48,v[152:155],200
ds_read2_b32 v[96:97],v14 offset0:96 offset1:97
M 48,v[156:159],196
ds_read2_b32 v[98:99],v14 offset0:98 offset1:99
M 48,v[160:163],192
buffer_load_dword v11,s[40:43],s78 offen offset:0  lds
M 48,v[164:167],188
R 128,2,2944
M 48,v[168:171],184
L 15
ds_read2_b32 v[100:101],v14 offset0:224 offset1:225
M 52,v[152:155],204
ds_read2_b32 v[102:103],v14 offset0:226 offset1:227
M 52,v[156:159],200
R 132,2,2432
M 52,v[160:163],196
M 52,v[164:167],192
ds_read2_b32 v[104:105],v15 offset0:96 offset1:97
M 52,v[168:171],188
ds_read2_b32 v[106:107],v15 offset0:98 offset1:99
M 52,v[172:175],184
L 15
R 136,2,1920
M 56,v[152:155],208
ds_read2_b32 v[108:109],v15 offset0:224 offset1:225
M 56,v[156:159],204
M 56,v[160:163],200
ds_read2_b32 v[110:111],v15 offset0:226 offset1:227
M 56,v[164:167],196
R 140,2,1408
M 56,v[168:171],192
ds_read2_b32 v[112:113],v16 offset0:96 offset1:97
M 56,v[172:175],188
ds_read2_b32 v[114:115],v16 offset0:98 offset1:99
M 56,v[176:179],184
L 15
M 60,v[152:155],212
R 144,2,896
M 60,v[156:159],208
ds_read2_b32 v[116:117],v16 offset0:224 offset1:225
M 60,v[160:163],204
ds_read2_b32 v[118:119],v16 offset0:226 offset1:227
M 60,v[164:167],200
R 148,2,384
M 60,v[168:171],196
M 60,v[172:175],192
M 60,v[176:179],188
M 60,v[180:183],184
L 15
M 40,v[88:91],128
ds_read2_b32 v[152:153],v1 offset0:112 offset1:113
M 40,v[92:95],124
ds_read2_b32 v[154:155],v1 offset0:114 offset1:115
M 40,v[96:99],120
L 14
R 184,2,4032
M 44,v[88:91],132
ds_read2_b32 v[156:157],v1 offset0:240 offset1:241
M 44,v[92:95],128
M 44,v[96:99],124
ds_read2_b32 v[158:159],v1 offset0:242 offset1:243
v_add_u32 v1,12928,v1
M 44,v[100:103],120
L 14
R 188,2,3520
M 48,v[88:91],136
ds_read2_b32 v[160:161],v14 offset0:112 offset1:113
M 48,v[92:95],132
ds_read2_b32 v[162:163],v14 offset0:114 offset1:115
M 48,v[96:99],128
M 48,v[100:103],124
R 192,2,3008
M 48,v[104:107],120
L 15
ds_read2_b32 v[164:165],v14 offset0:240 offset1:241
M 52,v[88:91],140
ds_read2_b32 v[166:167],v14 offset0:242 offset1:243
v_add_u32 v14,12928,v14
M 52,v[92:95],136
R 196,2,2496
M 52,v[96:99],132
M 52,v[100:103],128
ds_read2_b32 v[168:169],v15 offset0:112 offset1:113
M 52,v[104:107],124
ds_read2_b32 v[170:171],v15 offset0:114 offset1:115
M 52,v[108:111],120
L 15
R 200,2,1984
M 56,v[88:91],144
ds_read2_b32 v[172:173],v15 offset0:240 offset1:241
M 56,v[92:95],140
M 56,v[96:99],136
ds_read2_b32 v[174:175],v15 offset0:242 offset1:243
v_add_u32 v15,12928,v15
M 56,v[100:103],132
R 204,2,1472
M 56,v[104:107],128
ds_read2_b32 v[176:177],v16 offset0:112 offset1:113
M 56,v[108:111],124
ds_read2_b32 v[178:179],v16 offset0:114 offset1:115
M 56,v[112:115],120
L 15
M 60,v[88:91],148
R 208,2,960
M 60,v[92:95],144
ds_read2_b32 v[180:181],v16 offset0:240 offset1:241
M 60,v[96:99],140
ds_read2_b32 v[182:183],v16 offset0:242 offset1:243
v_add_u32 v16,12928,v16
M 60,v[100:103],136
)"
R"(R 212,2,448
M 60,v[104:107],132
M 60,v[108:111],128
M 60,v[112:115],124
M 60,v[116:119],120
L 15
M 40,v[152:155],192
ds_read2_b32 v[88:89],v1 offset0:0 offset1:1
M 40,v[156:159],188
ds_read2_b32 v[90:91],v1 offset0:2 offset1:3
M 40,v[160:163],184
L 14
R 120,2,16512
M 44,v[152:155],196
ds_read2_b32 v[92:93],v1 offset0:128 offset1:129
M 44,v[156:159],192
M 44,v[160:163],188
ds_read2_b32 v[94:95],v1 offset0:130 offset1:131
M 44,v[164:167],184
L 14
R 124,2,16000
M 48,v[152:155],200
ds_read2_b32 v[96:97],v14 offset0:0 offset1:1
M 48,v[156:159],196
ds_read2_b32 v[98:99],v14 offset0:2 offset1:3
M 48,v[160:163],192
M 48,v[164:167],188
R 128,2,15488
M 48,v[168:171],184
L 15
ds_read2_b32 v[100:101],v14 offset0:128 offset1:129
M 52,v[152:155],204
ds_read2_b32 v[102:103],v14 offset0:130 offset1:131
M 52,v[156:159],200
R 132,2,14976
M 52,v[160:163],196
M 52,v[164:167],192
ds_read2_b32 v[104:105],v15 offset0:0 offset1:1
M 52,v[168:171],188
ds_read2_b32 v[106:107],v15 offset0:2 offset1:3
M 52,v[172:175],184
L 15
R 136,2,14464
M 56,v[152:155],208
ds_read2_b32 v[108:109],v15 offset0:128 offset1:129
M 56,v[156:159],204
M 56,v[160:163],200
ds_read2_b32 v[110:111],v15 offset0:130 offset1:131
M 56,v[164:167],196
R 140,2,13952
M 56,v[168:171],192
ds_read2_b32 v[112:113],v16 offset0:0 offset1:1
M 56,v[172:175],188
ds_read2_b32 v[114:115],v16 offset0:2 offset1:3
M 56,v[176:179],184
L 15
M 60,v[152:155],212
R 144,2,13440
M 60,v[156:159],208
ds_read2_b32 v[116:117],v16 offset0:128 offset1:129
M 60,v[160:163],204
ds_read2_b32 v[118:119],v16 offset0:130 offset1:131
M 60,v[164:167],200
R 148,2,12928
M 60,v[168:171],196
M 60,v[172:175],192
M 60,v[176:179],188
M 60,v[180:183],184
s_cmp_eq_u32 s51,2
s_cbranch_scc1 Lcv_d_%=
ds_read_b64 v[24:25],v3 offset:2560
ds_read_b64 v[26:27],v3 offset:2048
ds_read_b64 v[28:29],v3 offset:1536
ds_read_b64 v[30:31],v3 offset:1024
ds_read_b64 v[32:33],v3 offset:512
ds_read_b64 v[34:35],v3 offset:0
L 0
v_and_b32 v36,0xffff0000,v25
v_lshlrev_b32 v37,16,v25
v_and_b32 v38,0xffff0000,v24
v_lshlrev_b32 v39,16,v24
v_fma_f32 v40,v36,s86,v40
v_fma_f32 v41,v37,s86,v41
v_fma_f32 v42,v38,s86,v42
v_fma_f32 v43,v39,s86,v43
v_cvt_pk_bf16_f32 v40,v40,v41
v_cvt_pk_bf16_f32 v41,v42,v43
ds_write_b64 v4,v[40:41] offset:0
v_and_b32 v36,0xffff0000,v27
v_lshlrev_b32 v37,16,v27
v_and_b32 v38,0xffff0000,v26
v_lshlrev_b32 v39,16,v26
v_fma_f32 v44,v36,s86,v44
v_fma_f32 v45,v37,s86,v45
v_fma_f32 v46,v38,s86,v46
v_fma_f32 v47,v39,s86,v47
v_cvt_pk_bf16_f32 v44,v44,v45
v_cvt_pk_bf16_f32 v45,v46,v47
ds_write_b64 v4,v[44:45] offset:512
v_and_b32 v36,0xffff0000,v29
v_lshlrev_b32 v37,16,v29
v_and_b32 v38,0xffff0000,v28
v_lshlrev_b32 v39,16,v28
v_fma_f32 v48,v36,s86,v48
v_fma_f32 v49,v37,s86,v49
v_fma_f32 v50,v38,s86,v50
v_fma_f32 v51,v39,s86,v51
v_cvt_pk_bf16_f32 v48,v48,v49
v_cvt_pk_bf16_f32 v49,v50,v51
ds_write_b64 v4,v[48:49] offset:1024
v_and_b32 v36,0xffff0000,v31
v_lshlrev_b32 v37,16,v31
v_and_b32 v38,0xffff0000,v30
v_lshlrev_b32 v39,16,v30
v_fma_f32 v52,v36,s86,v52
v_fma_f32 v53,v37,s86,v53
v_fma_f32 v54,v38,s86,v54
v_fma_f32 v55,v39,s86,v55
v_cvt_pk_bf16_f32 v52,v52,v53
v_cvt_pk_bf16_f32 v53,v54,v55
ds_write_b64 v4,v[52:53] offset:1536
v_and_b32 v36,0xffff0000,v33
v_lshlrev_b32 v37,16,v33
v_and_b32 v38,0xffff0000,v32
v_lshlrev_b32 v39,16,v32
v_fma_f32 v56,v36,s86,v56
v_fma_f32 v57,v37,s86,v57
v_fma_f32 v58,v38,s86,v58
v_fma_f32 v59,v39,s86,v59
v_cvt_pk_bf16_f32 v56,v56,v57
v_cvt_pk_bf16_f32 v57,v58,v59
ds_write_b64 v4,v[56:57] offset:2048
v_and_b32 v36,0xffff0000,v35
v_lshlrev_b32 v37,16,v35
v_and_b32 v38,0xffff0000,v34
v_lshlrev_b32 v39,16,v34
v_fma_f32 v60,v36,s86,v60
v_fma_f32 v61,v37,s86,v61
v_fma_f32 v62,v38,s86,v62
v_fma_f32 v63,v39,s86,v63
v_cvt_pk_bf16_f32 v60,v60,v61
v_cvt_pk_bf16_f32 v61,v62,v63
ds_write_b64 v4,v[60:61] offset:2560
s_branch Lcv_e_%=
Lcv_d_%=:
L 15
v_mfma_f32_16x16x32_bf16 v[64:67],v[88:91],v[128:131],0
ds_read2_b32 v[152:153],v1 offset0:16 offset1:17
ds_read_b64 v[24:25],v3 offset:2560
M 64,v[92:95],124
ds_read2_b32 v[154:155],v1 offset0:18 offset1:19
ds_read_b64 v[26:27],v3 offset:2048
M 64,v[96:99],120
L 15
R 184,2,16576
ds_read_b64 v[28:29],v3 offset:1536
v_mfma_f32_16x16x32_bf16 v[68:71],v[88:91],v[132:135],0
ds_read2_b32 v[156:157],v1 offset0:144 offset1:145
ds_read_b64 v[30:31],v3 offset:1024
M 68,v[92:95],128
ds_read_b64 v[32:33],v3 offset:512
ds_read_b64 v[34:35],v3 offset:0
M 68,v[96:99],124
ds_read2_b32 v[158:159],v1 offset0:146 offset1:147
M 68,v[100:103],120
L 15
R 188,2,16064
v_mfma_f32_16x16x32_bf16 v[72:75],v[88:91],v[136:139],0
ds_read2_b32 v[160:161],v14 offset0:16 offset1:17
M 72,v[92:95],132
s_mov_b32 m0,163584
s_mov_b32 s78,0
ds_read2_b32 v[162:163],v14 offset0:18 offset1:19
M 72,v[96:99],128
M 72,v[100:103],124
L 4
R 192,2,15552
v_and_b32 v36,0xffff0000,v25
M 72,v[104:107],120
s_nop 0
ds_read2_b32 v[164:165],v14 offset0:144 offset1:145
v_lshlrev_b32 v37,16,v25
v_mfma_f32_16x16x32_bf16 v[76:79],v[88:91],v[140:143],0
ds_read2_b32 v[166:167],v14 offset0:146 offset1:147
v_and_b32 v38,0xffff0000,v24
M 76,v[92:95],136
R 196,2,15040
v_lshlrev_b32 v39,16,v24
M 76,v[96:99],132
buffer_load_dword v12,s[60:63],s78 offen offset:0  lds
M 76,v[100:103],128
ds_read2_b32 v[168:169],v15 offset0:16 offset1:17
v_fma_f32 v40,v36,s86,v40
M 76,v[104:107],124
ds_read2_b32 v[170:171],v15 offset0:18 offset1:19
v_fma_f32 v41,v37,s86,v41
M 76,v[108:111],120
s_nop 0
R 200,2,14528
v_fma_f32 v42,v38,s86,v42
v_mfma_f32_16x16x32_bf16 v[80:83],v[88:91],v[144:147],0
ds_read2_b32 v[172:173],v15 offset0:144 offset1:145
v_fma_f32 v43,v39,s86,v43
M 80,v[92:95],140
v_cvt_pk_bf16_f32 v40,v40,v41
v_cvt_pk_bf16_f32 v41,v42,v43
M 80,v[96:99],136
ds_read2_b32 v[174:175],v15 offset0:146 offset1:147
v_and_b32 v36,0xffff0000,v27
M 80,v[100:103],132
R 204,2,14016
v_lshlrev_b32 v37,16,v27
M 80,v[104:107],128
ds_read2_b32 v[176:177],v16 offset0:16 offset1:17
v_and_b32 v38,0xffff0000,v26
M 80,v[108:111],124
ds_read2_b32 v[178:179],v16 offset0:18 offset1:19
v_lshlrev_b32 v39,16,v26
M 80,v[112:115],120
s_nop 0
ds_write_b64 v4,v[40:41] offset:0
v_mfma_f32_16x16x32_bf16 v[84:87],v[88:91],v[148:151],0
R 208,2,13504
v_fma_f32 v44,v36,s86,v44
M 84,v[92:95],144
ds_read2_b32 v[180:181],v16 offset0:144 offset1:145
v_fma_f32 v45,v37,s86,v45
M 84,v[96:99],140
ds_read2_b32 v[182:183],v16 offset0:146 offset1:147
v_fma_f32 v46,v38,s86,v46
M 84,v[100:103],136
R 212,2,12992
v_fma_f32 v47,v39,s86,v47
M 84,v[104:107],132
v_cvt_pk_bf16_f32 v44,v44,v45
v_cvt_pk_bf16_f32 v45,v46,v47
M 84,v[108:111],128
ds_write_b64 v4,v[44:45] offset:512
M 84,v[112:115],124
v_and_b32 v36,0xffff0000,v29
v_lshlrev_b32 v37,16,v29
M 84,v[116:119],120
L 15
v_and_b32 v38,0xffff0000,v28
v_lshlrev_b32 v39,16,v28
M 64,v[152:155],192
ds_read2_b32 v[88:89],v1 offset0:32 offset1:33
v_fma_f32 v48,v36,s86,v48
M 64,v[156:159],188
ds_read2_b32 v[90:91],v1 offset0:34 offset1:35
v_fma_f32 v49,v37,s86,v49
M 64,v[160:163],184
L 15
R 120,2,16640
v_fma_f32 v50,v38,s86,v50
M 68,v[152:155],196
ds_read2_b32 v[92:93],v1 offset0:160 offset1:161
v_fma_f32 v51,v39,s86,v51
M 68,v[156:159],192
v_cvt_pk_bf16_f32 v48,v48,v49
v_cvt_pk_bf16_f32 v49,v50,v51
M 68,v[160:163],188
ds_read2_b32 v[94:95],v1 offset0:162 offset1:163
v_and_b32 v36,0xffff0000,v31
M 68,v[164:167],184
L 15
R 124,2,16128
v_lshlrev_b32 v37,16,v31
M 72,v[152:155],200
ds_read2_b32 v[96:97],v14 offset0:32 offset1:33
v_and_b32 v38,0xffff0000,v30
M 72,v[156:159],196
ds_read2_b32 v[98:99],v14 offset0:34 offset1:35
v_lshlrev_b32 v39,16,v30
M 72,v[160:163],192
ds_write_b64 v4,v[48:49] offset:1024
M 72,v[164:167],188
R 128,2,15616
v_fma_f32 v52,v36,s86,v52
M 72,v[168:171],184
L 15
ds_read2_b32 v[100:101],v14 offset0:160 offset1:161
v_fma_f32 v53,v37,s86,v53
M 76,v[152:155],204
ds_read2_b32 v[102:103],v14 offset0:162 offset1:163
v_fma_f32 v54,v38,s86,v54
M 76,v[156:159],200
R 132,2,15104
v_fma_f32 v55,v39,s86,v55
M 76,v[160:163],196
v_cvt_pk_bf16_f32 v52,v52,v53
v_cvt_pk_bf16_f32 v53,v54,v55
M 76,v[164:167],192
ds_read2_b32 v[104:105],v15 offset0:32 offset1:33
v_and_b32 v36,0xffff0000,v33
M 76,v[168:171],188
ds_read2_b32 v[106:107],v15 offset0:34 offset1:35
v_lshlrev_b32 v37,16,v33
M 76,v[172:175],184
L 15
R 136,2,14592
v_and_b32 v38,0xffff0000,v32
M 80,v[152:155],208
ds_read2_b32 v[108:109],v15 offset0:160 offset1:161
v_lshlrev_b32 v39,16,v32
M 80,v[156:159],204
ds_write_b64 v4,v[52:53] offset:1536
M 80,v[160:163],200
ds_read2_b32 v[110:111],v15 offset0:162 offset1:163
v_fma_f32 v56,v36,s86,v56
M 80,v[164:167],196
R 140,2,14080
v_fma_f32 v57,v37,s86,v57
M 80,v[168:171],192
ds_read2_b32 v[112:113],v16 offset0:32 offset1:33
v_fma_f32 v58,v38,s86,v58
M 80,v[172:175],188
ds_read2_b32 v[114:115],v16 offset0:34 offset1:35
v_fma_f32 v59,v39,s86,v59
M 80,v[176:179],184
L 15
v_cvt_pk_bf16_f32 v56,v56,v57
v_cvt_pk_bf16_f32 v57,v58,v59
M 84,v[152:155],212
R 144,2,13568
v_and_b32 v36,0xffff0000,v35
M 84,v[156:159],208
ds_read2_b32 v[116:117],v16 offset0:160 offset1:161
v_lshlrev_b32 v37,16,v35
M 84,v[160:163],204
ds_read2_b32 v[118:119],v16 offset0:162 offset1:163
v_and_b32 v38,0xffff0000,v34
M 84,v[164:167],200
R 148,2,13056
v_lshlrev_b32 v39,16,v34
M 84,v[168:171],196
ds_write_b64 v4,v[56:57] offset:2048
M 84,v[172:175],192
v_fma_f32 v60,v36,s86,v60
v_fma_f32 v61,v37,s86,v61
M 84,v[176:179],188
v_fma_f32 v62,v38,s86,v62
v_fma_f32 v63,v39,s86,v63
M 84,v[180:183],184
L 15
v_cvt_pk_bf16_f32 v60,v60,v61
v_cvt_pk_bf16_f32 v61,v62,v63
M 64,v[88:91],128
ds_read2_b32 v[152:153],v1 offset0:48 offset1:49
M 64,v[92:95],124
ds_read2_b32 v[154:155],v1 offset0:50 offset1:51
M 64,v[96:99],120
L 15
R 184,2,16704
M 68,v[88:91],132
ds_read2_b32 v[156:157],v1 offset0:176 offset1:177
M 68,v[92:95],128
ds_write_b64 v4,v[60:61] offset:2560
M 68,v[96:99],124
ds_read2_b32 v[158:159],v1 offset0:178 offset1:179
M 68,v[100:103],120
L 15
R 188,2,16192
M 72,v[88:91],136
ds_read2_b32 v[160:161],v14 offset0:48 offset1:49
M 72,v[92:95],132
ds_read2_b32 v[162:163],v14 offset0:50 offset1:51
M 72,v[96:99],128
M 72,v[100:103],124
R 192,2,15680
M 72,v[104:107],120
L 15
ds_read2_b32 v[164:165],v14 offset0:176 offset1:177
M 76,v[88:91],140
ds_read2_b32 v[166:167],v14 offset0:178 offset1:179
M 76,v[92:95],136
R 196,2,15168
M 76,v[96:99],132
M 76,v[100:103],128
ds_read2_b32 v[168:169],v15 offset0:48 offset1:49
M 76,v[104:107],124
ds_read2_b32 v[170:171],v15 offset0:50 offset1:51
M 76,v[108:111],120
L 15
R 200,2,14656
M 80,v[88:91],144
ds_read2_b32 v[172:173],v15 offset0:176 offset1:177
M 80,v[92:95],140
M 80,v[96:99],136
ds_read2_b32 v[174:175],v15 offset0:178 offset1:179
M 80,v[100:103],132
R 204,2,14144
M 80,v[104:107],128
s_mov_b32 m0,163584
s_mov_b32 s78,1847296
ds_read2_b32 v[176:177],v16 offset0:48 offset1:49
M 80,v[108:111],124
ds_read2_b32 v[178:179],v16 offset0:50 offset1:51
M 80,v[112:115],120
L 15
buffer_load_dword v12,s[60:63],s78 offen offset:0  lds
M 84,v[88:91],148
R 208,2,13632
M 84,v[92:95],144
ds_read2_b32 v[180:181],v16 offset0:176 offset1:177
M 84,v[96:99],140
ds_read2_b32 v[182:183],v16 offset0:178 offset1:179
M 84,v[100:103],136
R 212,2,13120
M 84,v[104:107],132
M 84,v[108:111],128
M 84,v[112:115],124
M 84,v[116:119],120
L 15
M 64,v[152:155],192
ds_read2_b32 v[88:89],v1 offset0:64 offset1:65
M 64,v[156:159],188
ds_read2_b32 v[90:91],v1 offset0:66 offset1:67
M 64,v[160:163],184
L 14
R 120,2,16768
M 68,v[152:155],196
ds_read2_b32 v[92:93],v1 offset0:192 offset1:193
M 68,v[156:159],192
M 68,v[160:163],188
ds_read2_b32 v[94:95],v1 offset0:194 offset1:195
M 68,v[164:167],184
L 14
R 124,2,16256
M 72,v[152:155],200
ds_read2_b32 v[96:97],v14 offset0:64 offset1:65
M 72,v[156:159],196
ds_read2_b32 v[98:99],v14 offset0:66 offset1:67
M 72,v[160:163],192
M 72,v[164:167],188
R 128,2,15744
M 72,v[168:171],184
L 15
ds_read2_b32 v[100:101],v14 offset0:192 offset1:193
M 76,v[152:155],204
ds_read2_b32 v[102:103],v14 offset0:194 offset1:195
M 76,v[156:159],200
R 132,2,15232
M 76,v[160:163],196
M 76,v[164:167],192
ds_read2_b32 v[104:105],v15 offset0:64 offset1:65
M 76,v[168:171],188
ds_read2_b32 v[106:107],v15 offset0:66 offset1:67
M 76,v[172:175],184
L 15
R 136,2,14720
M 80,v[152:155],208
ds_read2_b32 v[108:109],v15 offset0:192 offset1:193
M 80,v[156:159],204
M 80,v[160:163],200
ds_read2_b32 v[110:111],v15 offset0:194 offset1:195
M 80,v[164:167],196
R 140,2,14208
M 80,v[168:171],192
ds_read2_b32 v[112:113],v16 offset0:64 offset1:65
M 80,v[172:175],188
ds_read2_b32 v[114:115],v16 offset0:66 offset1:67
M 80,v[176:179],184
L 15
M 84,v[152:155],212
R 144,2,13696
M 84,v[156:159],208
ds_read2_b32 v[116:117],v16 offset0:192 offset1:193
M 84,v[160:163],204
ds_read2_b32 v[118:119],v16 offset0:194 offset1:195
M 84,v[164:167],200
R 148,2,13184
M 84,v[168:171],196
M 84,v[172:175],192
M 84,v[176:179],188
M 84,v[180:183],184
L 15
M 64,v[88:91],128
ds_read2_b32 v[152:153],v1 offset0:80 offset1:81
M 64,v[92:95],124
ds_read2_b32 v[154:155],v1 offset0:82 offset1:83
M 64,v[96:99],120
L 14
R 184,2,16832
M 68,v[88:91],132
ds_read2_b32 v[156:157],v1 offset0:208 offset1:209
M 68,v[92:95],128
M 68,v[96:99],124
ds_read2_b32 v[158:159],v1 offset0:210 offset1:211
M 68,v[100:103],120
L 14
R 188,2,16320
M 72,v[88:91],136
ds_read2_b32 v[160:161],v14 offset0:80 offset1:81
M 72,v[92:95],132
ds_read2_b32 v[162:163],v14 offset0:82 offset1:83
M 72,v[96:99],128
M 72,v[100:103],124
R 192,2,15808
M 72,v[104:107],120
)"
R"(L 15
ds_read2_b32 v[164:165],v14 offset0:208 offset1:209
M 76,v[88:91],140
ds_read2_b32 v[166:167],v14 offset0:210 offset1:211
M 76,v[92:95],136
R 196,2,15296
M 76,v[96:99],132
M 76,v[100:103],128
ds_read2_b32 v[168:169],v15 offset0:80 offset1:81
M 76,v[104:107],124
ds_read2_b32 v[170:171],v15 offset0:82 offset1:83
M 76,v[108:111],120
L 15
R 200,2,14784
M 80,v[88:91],144
ds_read2_b32 v[172:173],v15 offset0:208 offset1:209
M 80,v[92:95],140
M 80,v[96:99],136
ds_read2_b32 v[174:175],v15 offset0:210 offset1:211
M 80,v[100:103],132
R 204,2,14272
M 80,v[104:107],128
ds_read2_b32 v[176:177],v16 offset0:80 offset1:81
M 80,v[108:111],124
ds_read2_b32 v[178:179],v16 offset0:82 offset1:83
M 80,v[112:115],120
L 15
M 84,v[88:91],148
R 208,2,13760
M 84,v[92:95],144
ds_read2_b32 v[180:181],v16 offset0:208 offset1:209
M 84,v[96:99],140
ds_read2_b32 v[182:183],v16 offset0:210 offset1:211
M 84,v[100:103],136
R 212,2,13248
M 84,v[104:107],132
M 84,v[108:111],128
M 84,v[112:115],124
M 84,v[116:119],120
L 15
M 64,v[152:155],192
ds_read2_b32 v[88:89],v1 offset0:96 offset1:97
M 64,v[156:159],188
ds_read2_b32 v[90:91],v1 offset0:98 offset1:99
M 64,v[160:163],184
L 14
R 120,2,16896
M 68,v[152:155],196
ds_read2_b32 v[92:93],v1 offset0:224 offset1:225
M 68,v[156:159],192
M 68,v[160:163],188
ds_read2_b32 v[94:95],v1 offset0:226 offset1:227
M 68,v[164:167],184
L 14
R 124,2,16384
M 72,v[152:155],200
ds_read2_b32 v[96:97],v14 offset0:96 offset1:97
M 72,v[156:159],196
ds_read2_b32 v[98:99],v14 offset0:98 offset1:99
M 72,v[160:163],192
M 72,v[164:167],188
R 128,2,15872
M 72,v[168:171],184
L 15
ds_read2_b32 v[100:101],v14 offset0:224 offset1:225
M 76,v[152:155],204
ds_read2_b32 v[102:103],v14 offset0:226 offset1:227
M 76,v[156:159],200
R 132,2,15360
M 76,v[160:163],196
M 76,v[164:167],192
ds_read2_b32 v[104:105],v15 offset0:96 offset1:97
M 76,v[168:171],188
ds_read2_b32 v[106:107],v15 offset0:98 offset1:99
M 76,v[172:175],184
L 15
R 136,2,14848
M 80,v[152:155],208
ds_read2_b32 v[108:109],v15 offset0:224 offset1:225
M 80,v[156:159],204
M 80,v[160:163],200
ds_read2_b32 v[110:111],v15 offset0:226 offset1:227
M 80,v[164:167],196
R 140,2,14336
M 80,v[168:171],192
ds_read2_b32 v[112:113],v16 offset0:96 offset1:97
M 80,v[172:175],188
ds_read2_b32 v[114:115],v16 offset0:98 offset1:99
M 80,v[176:179],184
L 15
M 84,v[152:155],212
R 144,2,13824
M 84,v[156:159],208
ds_read2_b32 v[116:117],v16 offset0:224 offset1:225
M 84,v[160:163],204
ds_read2_b32 v[118:119],v16 offset0:226 offset1:227
M 84,v[164:167],200
R 148,2,13312
M 84,v[168:171],196
M 84,v[172:175],192
M 84,v[176:179],188
M 84,v[180:183],184
L 15
M 64,v[88:91],128
ds_read2_b32 v[152:153],v1 offset0:112 offset1:113
M 64,v[92:95],124
ds_read2_b32 v[154:155],v1 offset0:114 offset1:115
M 64,v[96:99],120
L 14
R 184,2,16960
M 68,v[88:91],132
ds_read2_b32 v[156:157],v1 offset0:240 offset1:241
M 68,v[92:95],128
M 68,v[96:99],124
ds_read2_b32 v[158:159],v1 offset0:242 offset1:243
M 68,v[100:103],120
L 14
R 188,2,16448
M 72,v[88:91],136
ds_read2_b32 v[160:161],v14 offset0:112 offset1:113
M 72,v[92:95],132
ds_read2_b32 v[162:163],v14 offset0:114 offset1:115
M 72,v[96:99],128
M 72,v[100:103],124
R 192,2,15936
M 72,v[104:107],120
L 15
ds_read2_b32 v[164:165],v14 offset0:240 offset1:241
M 76,v[88:91],140
ds_read2_b32 v[166:167],v14 offset0:242 offset1:243
M 76,v[92:95],136
R 196,2,15424
M 76,v[96:99],132
M 76,v[100:103],128
ds_read2_b32 v[168:169],v15 offset0:112 offset1:113
M 76,v[104:107],124
ds_read2_b32 v[170:171],v15 offset0:114 offset1:115
M 76,v[108:111],120
L 15
R 200,2,14912
M 80,v[88:91],144
ds_read2_b32 v[172:173],v15 offset0:240 offset1:241
M 80,v[92:95],140
M 80,v[96:99],136
ds_read2_b32 v[174:175],v15 offset0:242 offset1:243
M 80,v[100:103],132
R 204,2,14400
M 80,v[104:107],128
ds_read2_b32 v[176:177],v16 offset0:112 offset1:113
M 80,v[108:111],124
ds_read2_b32 v[178:179],v16 offset0:114 offset1:115
M 80,v[112:115],120
L 15
M 84,v[88:91],148
R 208,2,13888
M 84,v[92:95],144
ds_read2_b32 v[180:181],v16 offset0:240 offset1:241
M 84,v[96:99],140
ds_read2_b32 v[182:183],v16 offset0:242 offset1:243
M 84,v[100:103],136
R 212,2,13376
M 84,v[104:107],132
M 84,v[108:111],128
M 84,v[112:115],124
M 84,v[116:119],120
L 15
M 64,v[152:155],192
M 64,v[156:159],188
M 64,v[160:163],184
L 12
M 68,v[152:155],196
M 68,v[156:159],192
M 68,v[160:163],188
M 68,v[164:167],184
L 9
M 72,v[152:155],200
M 72,v[156:159],196
M 72,v[160:163],192
ds_read_b64 v[24:25],v3 offset:15488
ds_read_b64 v[26:27],v3 offset:14976
M 72,v[164:167],188
ds_read_b64 v[28:29],v3 offset:14464
ds_read_b64 v[30:31],v3 offset:13952
M 72,v[168:171],184
L 10
ds_read_b64 v[32:33],v3 offset:13440
ds_read_b64 v[34:35],v3 offset:12928
M 76,v[152:155],204
M 76,v[156:159],200
M 76,v[160:163],196
M 76,v[164:167],192
M 76,v[168:171],188
M 76,v[172:175],184
L 9
M 80,v[152:155],208
M 80,v[156:159],204
M 80,v[160:163],200
M 80,v[164:167],196
M 80,v[168:171],192
M 80,v[172:175],188
M 80,v[176:179],184
L 6
M 84,v[152:155],212
M 84,v[156:159],208
M 84,v[160:163],204
M 84,v[164:167],200
M 84,v[168:171],196
M 84,v[172:175],192
M 84,v[176:179],188
M 84,v[180:183],184
L 0
v_and_b32 v36,0xffff0000,v25
v_lshlrev_b32 v37,16,v25
v_and_b32 v38,0xffff0000,v24
v_lshlrev_b32 v39,16,v24
v_fma_f32 v64,v36,s87,v64
v_fma_f32 v65,v37,s87,v65
v_fma_f32 v66,v38,s87,v66
v_fma_f32 v67,v39,s87,v67
v_cvt_pk_bf16_f32 v64,v64,v65
v_cvt_pk_bf16_f32 v65,v66,v67
ds_write_b64 v4,v[64:65] offset:12928
v_and_b32 v36,0xffff0000,v27
v_lshlrev_b32 v37,16,v27
v_and_b32 v38,0xffff0000,v26
v_lshlrev_b32 v39,16,v26
v_fma_f32 v68,v36,s87,v68
v_fma_f32 v69,v37,s87,v69
v_fma_f32 v70,v38,s87,v70
v_fma_f32 v71,v39,s87,v71
v_cvt_pk_bf16_f32 v68,v68,v69
v_cvt_pk_bf16_f32 v69,v70,v71
ds_write_b64 v4,v[68:69] offset:13440
v_and_b32 v36,0xffff0000,v29
v_lshlrev_b32 v37,16,v29
v_and_b32 v38,0xffff0000,v28
v_lshlrev_b32 v39,16,v28
v_fma_f32 v72,v36,s87,v72
v_fma_f32 v73,v37,s87,v73
v_fma_f32 v74,v38,s87,v74
v_fma_f32 v75,v39,s87,v75
v_cvt_pk_bf16_f32 v72,v72,v73
v_cvt_pk_bf16_f32 v73,v74,v75
ds_write_b64 v4,v[72:73] offset:13952
v_and_b32 v36,0xffff0000,v31
v_lshlrev_b32 v37,16,v31
v_and_b32 v38,0xffff0000,v30
v_lshlrev_b32 v39,16,v30
v_fma_f32 v76,v36,s87,v76
v_fma_f32 v77,v37,s87,v77
v_fma_f32 v78,v38,s87,v78
v_fma_f32 v79,v39,s87,v79
v_cvt_pk_bf16_f32 v76,v76,v77
v_cvt_pk_bf16_f32 v77,v78,v79
ds_write_b64 v4,v[76:77] offset:14464
v_and_b32 v36,0xffff0000,v33
v_lshlrev_b32 v37,16,v33
v_and_b32 v38,0xffff0000,v32
v_lshlrev_b32 v39,16,v32
v_fma_f32 v80,v36,s87,v80
v_fma_f32 v81,v37,s87,v81
v_fma_f32 v82,v38,s87,v82
v_fma_f32 v83,v39,s87,v83
v_cvt_pk_bf16_f32 v80,v80,v81
v_cvt_pk_bf16_f32 v81,v82,v83
ds_write_b64 v4,v[80:81] offset:14976
v_and_b32 v36,0xffff0000,v35
v_lshlrev_b32 v37,16,v35
v_and_b32 v38,0xffff0000,v34
v_lshlrev_b32 v39,16,v34
v_fma_f32 v84,v36,s87,v84
v_fma_f32 v85,v37,s87,v85
v_fma_f32 v86,v38,s87,v86
v_fma_f32 v87,v39,s87,v87
v_cvt_pk_bf16_f32 v84,v84,v85
v_cvt_pk_bf16_f32 v85,v86,v87
ds_write_b64 v4,v[84:85] offset:15488
Lcv_e_%=:
L 0
s_barrier
R 152,5,0
R 156,5,512
R 160,5,1024
R 164,5,1536
R 168,5,2048
R 172,5,2560
s_mov_b32 s78,99614720
L 5
buffer_store_dwordx4 v[152:155],v7,s[36:39],s78 offen offset:0 sc0 sc1
s_add_u32 s78,s56,100139008
L 4
buffer_store_dwordx4 v[156:159],v7,s[36:39],s78 offen offset:0 sc0 sc1
s_mov_b32 s78,100663296
L 3
buffer_store_dwordx4 v[160:163],v7,s[36:39],s78 offen offset:0 sc0 sc1
s_add_u32 s78,s56,101187584
L 2
buffer_store_dwordx4 v[164:167],v7,s[36:39],s78 offen offset:0 sc0 sc1
s_mov_b32 s78,101711872
L 1
buffer_store_dwordx4 v[168:171],v7,s[36:39],s78 offen offset:0 sc0 sc1
s_add_u32 s78,s56,102236160
L 0
buffer_store_dwordx4 v[172:175],v7,s[36:39],s78 offen offset:0 sc0 sc1
s_waitcnt vmcnt(0)
s_barrier
s_add_u32 s48,s48,32768
buffer_store_dword v23,v10,s[36:39],s48 offen offset:0 sc0 sc1
s_cmp_eq_u32 s51,2
s_cbranch_scc0 Lcv_f_%=
s_mov_b32 m0,163584
s_mov_b32 s78,3694592
buffer_load_dword v12,s[60:63],s78 offen offset:0  lds
s_mov_b32 m0,163584
s_mov_b32 s78,5541888
buffer_load_dword v12,s[60:63],s78 offen offset:0  lds
Lcv_f_%=:
Lcode_cv_e:
.purgem M
.purgem R
.purgem T
.purgem D
.purgem L
)"
  :: "s"(ws), "s"(__builtin_amdgcn_readfirstlane(sWave)), "s"(__builtin_amdgcn_readfirstlane(sBuf)), "s"(__builtin_amdgcn_readfirstlane(sAuxOff)), "s"(__builtin_amdgcn_readfirstlane(sFoutOff)), "s"(__builtin_amdgcn_readfirstlane(sC0)), "s"(__builtin_amdgcn_readfirstlane(sC1)), "s"(__builtin_amdgcn_readfirstlane(sExp)), "v"(vL16), "v"(vPA), "v"(vPB), "v"(vPU), "v"(vPW), "v"(vOR), "v"(vORA), "v"(vOG), "v"(vFA), "v"(vFB), "v"(vFlag), "v"(vTW), "v"(vTX), "s"(w3), "s"(w4) : "v0", "v1", "v2", "v3", "v4", "v5", "v6", "v7", "v8", "v9", "v10", "v11", "v12", "v13", "v14", "v15", "v16", "v17", "v18", "v19", "v20", "v21", "v22", "v23", "v24", "v25", "v26", "v27", "v28", "v29", "v30", "v31", "v32", "v33", "v34", "v35", "v36", "v37", "v38", "v39", "v40", "v41", "v42", "v43", "v44", "v45", "v46", "v47", "v48", "v49", "v50", "v51", "v52", "v53", "v54", "v55", "v56", "v57", "v58", "v59", "v60", "v61", "v62", "v63", "v64", "v65", "v66", "v67", "v68", "v69", "v70", "v71", "v72", "v73", "v74", "v75", "v76", "v77", "v78", "v79", "v80", "v81", "v82", "v83", "v84", "v85", "v86", "v87", "v88", "v89", "v90", "v91", "v92", "v93", "v94", "v95", "v96", "v97", "v98", "v99", "v100", "v101", "v102", "v103", "v104", "v105", "v106", "v107", "v108", "v109", "v110", "v111", "v112", "v113", "v114", "v115", "v116", "v117", "v118", "v119", "v120", "v121", "v122", "v123", "v124", "v125", "v126", "v127", "v128", "v129", "v130", "v131", "v132", "v133", "v134", "v135", "v136", "v137", "v138", "v139", "v140", "v141", "v142", "v143", "v144", "v145", "v146", "v147", "v148", "v149", "v150", "v151", "v152", "v153", "v154", "v155", "v156", "v157", "v158", "v159", "v160", "v161", "v162", "v163", "v164", "v165", "v166", "v167", "v168", "v169", "v170", "v171", "v172", "v173", "v174", "v175", "v176", "v177", "v178", "v179", "v180", "v181", "v182", "v183", "v184", "v185", "v186", "v187", "v188", "v189", "v190", "v191", "v192", "v193", "v194", "v195", "v196", "v197", "v198", "v199", "v200", "v201", "v202", "v203", "v204", "v205", "v206", "v207", "v208", "v209", "v210", "v211", "v212", "v213", "v214", "v215", "v216", "v217", "v218", "v219", "v220", "v221", "v222", "v223", "v224", "v225", "v226", "v227", "v228", "v229", "v230", "v231", "a0", "a1", "a2", "a3", "a4", "a5", "a6", "a7", "a8", "a9", "a10", "a11", "a12", "a13", "a14", "a15", "a16", "a17", "a18", "a19", "a20", "a21", "a22", "a23", "a24", "a25", "a26", "a27", "a28", "a29", "a30", "a31", "a32", "a33", "a34", "a35", "a36", "a37", "a38", "a39", "a40", "a41", "a42", "a43", "a44", "a45", "a46", "a47", "a48", "a49", "a50", "a51", "a52", "a53", "a54", "a55", "a56", "a57", "a58", "a59", "a60", "a61", "a62", "a63", "a64", "a65", "a66", "a67", "a68", "a69", "a70", "a71", "a72", "a73", "a74", "a75", "a76", "a77", "a78", "a79", "a80", "a81", "a82", "a83", "a84", "a85", "a86", "a87", "a88", "a89", "a90", "a91", "a92", "a93", "a94", "a95", "a96", "a97", "a98", "a99", "a100", "a101", "a102", "a103", "a104", "a105", "a106", "a107", "a108", "a109", "a110", "a111", "a112", "a113", "a114", "a115", "a116", "a117", "a118", "a119", "a120", "a121", "a122", "a123", "a124", "a125", "a126", "a127", "a128", "a129", "a130", "a131", "a132", "a133", "a134", "a135", "a136", "a137", "a138", "a139", "a140", "a141", "a142", "a143", "a144", "a145", "a146", "a147", "a148", "a149", "a150", "a151", "a152", "a153", "a154", "a155", "a156", "a157", "a158", "a159", "a160", "a161", "a162", "a163", "a164", "a165", "a166", "a167", "a168", "a169", "a170", "a171", "a172", "a173", "a174", "a175", "a176", "a177", "a178", "a179", "a180", "a181", "a182", "a183", "a184", "a185", "a186", "a187", "a188", "a189", "a190", "a191", "a192", "a193", "a194", "a195", "a196", "a197", "a198", "a199", "a200", "a201", "a202", "a203", "a204", "a205", "a206", "a207", "a208", "a209", "a210", "a211", "a212", "a213", "a214", "a215", "a216", "a217", "a218", "a219", "a220", "a221", "a222", "a223", "a224", "a225", "a226", "a227", "a228", "a229", "a230", "a231", "a232", "a233", "a234", "a235", "a236", "a237", "a238", "a239", "a240", "a241", "a242", "a243", "a244", "a245", "a246", "a247", "a248", "a249", "a250", "a251", "a252", "a253", "a254", "a255", "s36", "s37", "s38", "s39", "s40", "s41", "s42", "s43", "s44", "s45", "s46", "s47", "s48", "s49", "s50", "s51", "s52", "s53", "s54", "s55", "s56", "s57", "s58", "s59", "s60", "s61", "s62", "s63", "s64", "s65", "s66", "s67", "s68", "s69", "s70", "s71", "s72", "s73", "s74", "s75", "s76", "s77", "s78", "s79", "s80", "s81", "s82", "s83", "s84", "s85", "s86", "s87", "s88", "s89", "s90", "s91", "s92", "s93", "s94", "s95", "s96", "s97", "vcc", "scc", "memory");
    LANE_ID(lq_);
    const int g = QT0 + (NCV - 1 - iw) * 164 + min(wave * 64 + lq_, 163);
    if (QTB) touch_lds((const char*)q + (size_t)(g < 32768 ? g : 0) * 128, sScr);
  }
  WAIT_LGKM0();
  __builtin_amdgcn_s_barrier();
  CTS(10);
  }
  const unsigned sDbg0 = 110u * 1048576u + (b * 4 + wave) * 8192;
  constexpr unsigned OOB = 0x80000000u;
  if (kind == 6) {
    LANE_ID(lr);
    const int r8 = lr & 7, seg = lr >> 3, j = lr & 15, g = lr >> 4, rr = 8 * g + (j >> 2);
    const unsigned sWave = wave, sExp = __builtin_amdgcn_readfirstlane(ep), sG0 = slice < 16 ? slice + 1 : 0, sStep = NRW, sDbg = sDbg0;
    const unsigned sWw = ((slice >> 3) * 1792 + 16 * wave) * 2048 + (slice & 7) * 256;
    const unsigned vWd0 = seg * 2048 + (r8 ^ swzB(seg)) * 16, vWd1 = (seg + 8) * 2048 + (r8 ^ swzB(8 + seg)) * 16;
    const unsigned vWr0 = (wave >> 1) * 8192 + rr * 128 + (((wave & 1) * 4 + (lr & 3)) ^ swzB(rr)) * 16, vWr1 = vWr0 + 8;
    const unsigned vRF = (wave * 8 + (seg >> 2)) * 1024 + ((seg & 3) * 16 + r8) * 16;
    const unsigned vRwr = seg == 0 ? r8 * 64 + wave * 4 : 512 + lr * 4, vRrd = r8 * 64;
    const unsigned vRP = g == (j >> 2) ? r8 * 64 + 16 + wave * 8 + (seg & 1) * 4 : 768 + lr * 4;
    const unsigned vRS = (wave == 0 && seg < 2) ? r8 * 16 + seg * 256 : OOB, vFlag = lr < 32 ? lr * 4 : OOB;
    const unsigned vL4 = ((lr & 2) * 4 + wave * 2 + (lr & 1)) * 128, vLY = min((wave < 3 ? wave * 32 : 112) + lr, wave < 2 ? wave * 32 + 31 : wave == 2 ? 111 : NCV - 1) * 128;
    const unsigned vQ = r8 * 2048 + wave * 512 + seg * 16;
    const unsigned vY0 = wave * 4096 + lr * 16;
    const unsigned vTr = 4096 + wave * 4096 + ((2 * g + ((j & 3) >> 1)) * 8 + (j >> 2)) * 16 + (j & 1) * 8;
    const unsigned one = g == (j >> 2) ? 0x3f80u << (16 * (j & 1)) : 0u, vSa = (j & 2) ? 0u : one, vSb = (j & 2) ? one : 0u;
    (void)sDbg;
    asm volatile(
R"(.macro M d,w,x
v_mfma_f32_16x16x32_bf16 v[\d:\d+3], \w, v[\x:\x+3], v[\d:\d+3]
.endm
.macro R d,a,o
ds_read_b128 v[\d:\d+3], v\a offset:\o
.endm
.macro T d,a,o
ds_read_b64_tr_b16 \d, v\a offset:\o
.endm
.macro D a,s,o
buffer_load_dwordx4 v\a, s[36:39], s\s offen offset:\o sc1 lds
.endm
.macro L c
s_waitcnt lgkmcnt(\c)
.endm
Lcode_r_b:
s_mov_b64 s[36:37],%0
s_mov_b32 s38,111243392
s_mov_b32 s39,0x00020000
s_mov_b32 s44,%1
s_mov_b32 s45,%2
s_mov_b32 s46,%3
s_mov_b32 s50,%4
s_mov_b32 s53,%5
v_mov_b32 v0,%6
v_mov_b32 v1,%7
v_mov_b32 v2,%8
v_mov_b32 v3,%9
v_mov_b32 v4,%10
v_mov_b32 v5,%11
v_mov_b32 v6,%12
v_mov_b32 v7,%13
v_mov_b32 v8,%14
v_mov_b32 v9,%15
v_mov_b32 v10,%16
v_mov_b32 v11,%17
v_mov_b32 v12,%18
v_mov_b32 v13,%19
v_mov_b32 v14,%20
v_mov_b32 v15,%21
v_mov_b32 v16,%22
s_mov_b32 s47,256
s_mov_b32 s49,0
s_mov_b32 s93,s45
s_mov_b64 s[84:85],%23
s_mov_b32 s86,4194304
s_mov_b32 s87,0x00020000
s_lshl_b32 s55,s45,14
buffer_load_dwordx4 v[164:167],v8,s[84:87],s55 offen offset:0 nt
buffer_load_dwordx4 v[168:171],v8,s[84:87],s55 offen offset:128 nt
buffer_load_dwordx4 v[172:175],v8,s[84:87],s55 offen offset:256 nt
buffer_load_dwordx4 v[176:179],v8,s[84:87],s55 offen offset:384 nt
v_mov_b32 v17,s53
v_mbcnt_lo_u32_b32 v19,-1,0
v_mbcnt_hi_u32_b32 v19,-1,v19
v_lshlrev_b32 v19,4,v19
s_mov_b32 s82,0
s_mov_b32 s63,0x7fff0000
s_mov_b32 s64,0
s_mov_b32 s67,0
s_lshl_b32 s65,s44,12
s_add_u32 s65,s65,4096
s_lshl_b32 s66,s44,7
s_add_u32 s66,s66,109056000
s_mov_b32 s70,0xaaaaaaaa
s_mov_b32 s71,0xaaaaaaaa
s_mov_b32 s72,0xcccccccc
s_mov_b32 s73,0xcccccccc
s_mov_b32 s74,0xff00ff00
s_mov_b32 s75,0xff00ff00
s_mov_b32 s68,0x3a800000
s_mov_b32 s69,0x3727c5ac
s_mov_b32 s76,0xc0135761
s_mov_b32 s77,0x3d372713
v_mov_b32 v32,0x3f803f80
v_mov_b32 v33,0x3f803f80
v_mov_b32 v34,0x3f803f80
v_mov_b32 v35,0x3f803f80
v_mov_b32 v24,0
v_mov_b32 v25,0
v_mov_b32 v26,v11
v_mov_b32 v27,v12
v_mov_b32 v28,0
v_mov_b32 v29,0
s_waitcnt vmcnt(0)
Lr_a0_%=:
s_waitcnt vmcnt(10)
v_mfma_f32_16x16x32_bf16 v[36:39],v[32:35],v[164:167],0
s_add_u32 s79,s45,s46
s_cmp_lt_u32 s45,64
s_cselect_b32 s78,s49,0
v_mfma_f32_16x16x32_bf16 v[40:43],v[164:167],v[164:167],0
s_cmp_ge_u32 s79,64
s_cselect_b32 s78,s78,0
s_add_u32 s62,s79,s78
M 36,v[32:35],168
s_lshr_b32 s78,s45,1
s_and_b32 s79,s45,1
s_lshl_b32 s79,s79,7
M 40,v[168:171],168
s_lshl_b32 s57,s78,15
s_lshl_b32 s55,s62,14
s_cmp_lt_u32 s62,s47
M 36,v[32:35],172
s_cselect_b32 s55,s55,0x7fff0000
s_add_u32 s57,s57,s79
s_lshl_b32 s59,s78,9
M 40,v[172:175],172
buffer_load_dwordx4 v[196:199],v8,s[84:87],s55 offen offset:0 nt
M 36,v[32:35],176
buffer_load_dwordx4 v[200:203],v8,s[84:87],s55 offen offset:128 nt
M 40,v[176:179],176
buffer_load_dwordx4 v[204:207],v8,s[84:87],s55 offen offset:256 nt
v_mfma_f32_16x16x32_bf16 v[116:119],v[26:29],v[164:167],0
buffer_load_dwordx4 v[208:211],v8,s[84:87],s55 offen offset:384 nt
v_mfma_f32_16x16x32_bf16 v[120:123],v[24:27],v[164:167],0
s_add_u32 s57,s57,8388608
s_add_u32 s59,s59,s79
s_lshl_b32 s60,s45,9
v_mfma_f32_16x16x32_bf16 v[124:127],v[26:29],v[168:171],0
s_add_u32 s58,s57,4096
s_add_u32 s59,s59,106954752
s_add_u32 s60,s60,s66
v_mfma_f32_16x16x32_bf16 v[128:131],v[24:27],v[168:171],0
v_add_f32_dpp v44,v36,v36 row_ror:8 row_mask:0xf bank_mask:0xf
v_cndmask_b32 v45,v40,v41,s[70:71]
v_mfma_f32_16x16x32_bf16 v[132:135],v[26:29],v[172:175],0
ds_write_b32 v2,v44 offset:0
v_mfma_f32_16x16x32_bf16 v[136:139],v[24:27],v[172:175],0
v_cndmask_b32 v46,v42,v43,s[70:71]
v_cndmask_b32 v45,v45,v46,s[72:73]
v_mfma_f32_16x16x32_bf16 v[140:143],v[26:29],v[176:179],0
ds_write_b32 v0,v45 offset:0
v_mfma_f32_16x16x32_bf16 v[144:147],v[24:27],v[176:179],0
L 0
s_barrier
R 52,3,0
R 56,3,16
R 60,3,32
s_cmp_lg_u32 s67,0
s_cbranch_scc1 Lr_b0_%=
s_waitcnt vmcnt(4)
buffer_store_dword v17,v5,s[36:39],s63 offen offset:0 sc0 sc1
s_mov_b32 s63,0x7fff0000
s_mov_b32 s38,111243392
s_cmp_lt_u32 s45,64
s_cselect_b32 s64,-192,0
s_add_u32 s64,s64,256
s_cmp_lt_u32 s45,64
s_cselect_b32 s61,0,32768
s_add_u32 s61,s61,111177728
s_mul_i32 s56,s45,16384
s_add_u32 s56,s56,98566144
Lr_bpy0_%=:
buffer_load_dword v18,v7,s[36:39],s61 offen offset:0 sc1
s_add_u32 s82,s82,1
s_waitcnt vmcnt(0)
v_cmp_eq_u32 vcc,s53,v18
s_nop 0
s_cmp_eq_u64 vcc,-1
s_cbranch_scc1 Lr_bqy0_%=
s_sleep 2
s_cmp_lt_u32 s82,20000
s_cbranch_scc1 Lr_bpy0_%=
Lr_bqy0_%=:
s_add_u32 m0,s65,0
s_nop 0
D 9,56,0
D 9,56,1024
D 9,56,2048
D 9,56,3072
s_waitcnt vmcnt(0)
Lr_b0_%=:
s_waitcnt vmcnt(10)
T v[68:69],10,0
T v[70:71],10,64
T v[72:73],10,1024
T v[74:75],10,1088
T v[76:77],10,2048
T v[78:79],10,2112
T v[80:81],10,3072
T v[82:83],10,3136
s_mov_b32 s45,s62
s_min_u32 s78,s64,s47
s_mul_i32 s56,s45,16384
s_add_u32 s56,s56,98566144
s_cmp_lt_i32 s45,s78
s_cselect_b32 s67,1,0
s_cselect_b32 s56,s56,0x7fff0000
s_add_u32 m0,s65,16384
s_nop 0
D 9,56,0
D 9,56,1024
D 9,56,2048
D 9,56,3072
L 8
v_pk_add_f32 v[52:53],v[52:53],v[54:55]
v_pk_add_f32 v[56:57],v[56:57],v[58:59]
v_pk_add_f32 v[60:61],v[60:61],v[62:63]
v_add_f32 v52,v52,v53
v_pk_add_f32 v[56:57],v[56:57],v[60:61]
v_mul_f32 v52,s68,v52
v_add_f32 v53,v56,v57
v_mul_f32 v53,s68,v53
v_fma_f32 v53,-v52,v52,v53
v_max_f32 v53,0,v53
s_waitcnt vmcnt(8)
buffer_store_dword v17,v5,s[36:39],s63 offen offset:0 sc0 sc1
s_mov_b32 s63,0x7fff0000
L 6
v_lshlrev_b32 v84,16,v68
v_and_b32 v85,0xffff0000,v68
v_lshlrev_b32 v86,16,v69
v_and_b32 v87,0xffff0000,v69
v_lshlrev_b32 v88,16,v70
v_and_b32 v89,0xffff0000,v70
v_lshlrev_b32 v90,16,v71
v_and_b32 v91,0xffff0000,v71
L 4
v_lshlrev_b32 v92,16,v72
v_and_b32 v93,0xffff0000,v72
v_lshlrev_b32 v94,16,v73
v_and_b32 v95,0xffff0000,v73
v_lshlrev_b32 v96,16,v74
v_and_b32 v97,0xffff0000,v74
v_lshlrev_b32 v98,16,v75
v_and_b32 v99,0xffff0000,v75
L 2
v_lshlrev_b32 v100,16,v76
v_and_b32 v101,0xffff0000,v76
v_lshlrev_b32 v102,16,v77
v_and_b32 v103,0xffff0000,v77
v_lshlrev_b32 v104,16,v78
v_and_b32 v105,0xffff0000,v78
v_lshlrev_b32 v106,16,v79
v_and_b32 v107,0xffff0000,v79
L 0
v_lshlrev_b32 v108,16,v80
v_and_b32 v109,0xffff0000,v80
v_lshlrev_b32 v110,16,v81
v_and_b32 v111,0xffff0000,v81
v_lshlrev_b32 v112,16,v82
v_and_b32 v113,0xffff0000,v82
v_lshlrev_b32 v114,16,v83
v_and_b32 v115,0xffff0000,v83
v_add_f32 v53,s69,v53
v_rsq_f32 v48,v53
s_nop 0
v_mul_f32_e64 v50,-v52,v48
v_mov_b32 v49,v48
v_mov_b32 v51,v50
v_pk_fma_f32 v[52:53],v[116:117],v[48:49],v[50:51]
v_pk_fma_f32 v[54:55],v[118:119],v[48:49],v[50:51]
v_pk_fma_f32 v[56:57],v[120:121],v[48:49],v[50:51]
v_pk_fma_f32 v[58:59],v[122:123],v[48:49],v[50:51]
v_pk_fma_f32 v[116:117],v[52:53],v[84:85],v[116:117]
v_pk_fma_f32 v[118:119],v[54:55],v[86:87],v[118:119]
v_pk_fma_f32 v[120:121],v[56:57],v[88:89],v[120:121]
v_pk_fma_f32 v[122:123],v[58:59],v[90:91],v[122:123]
v_cvt_pk_bf16_f32 v148,v116,v117
v_cvt_pk_bf16_f32 v149,v118,v119
v_cvt_pk_bf16_f32 v150,v120,v121
v_cvt_pk_bf16_f32 v151,v122,v123
v_pk_fma_f32 v[52:53],v[124:125],v[48:49],v[50:51]
v_pk_fma_f32 v[54:55],v[126:127],v[48:49],v[50:51]
v_pk_fma_f32 v[56:57],v[128:129],v[48:49],v[50:51]
v_pk_fma_f32 v[58:59],v[130:131],v[48:49],v[50:51]
v_pk_fma_f32 v[124:125],v[52:53],v[92:93],v[124:125]
v_pk_fma_f32 v[126:127],v[54:55],v[94:95],v[126:127]
v_pk_fma_f32 v[128:129],v[56:57],v[96:97],v[128:129]
v_pk_fma_f32 v[130:131],v[58:59],v[98:99],v[130:131]
v_cvt_pk_bf16_f32 v152,v124,v125
v_cvt_pk_bf16_f32 v153,v126,v127
v_cvt_pk_bf16_f32 v154,v128,v129
v_cvt_pk_bf16_f32 v155,v130,v131
v_pk_fma_f32 v[52:53],v[132:133],v[48:49],v[50:51]
v_pk_fma_f32 v[54:55],v[134:135],v[48:49],v[50:51]
v_pk_fma_f32 v[56:57],v[136:137],v[48:49],v[50:51]
v_pk_fma_f32 v[58:59],v[138:139],v[48:49],v[50:51]
v_pk_fma_f32 v[132:133],v[52:53],v[100:101],v[132:133]
v_pk_fma_f32 v[134:135],v[54:55],v[102:103],v[134:135]
v_pk_fma_f32 v[136:137],v[56:57],v[104:105],v[136:137]
v_pk_fma_f32 v[138:139],v[58:59],v[106:107],v[138:139]
v_cvt_pk_bf16_f32 v156,v132,v133
v_cvt_pk_bf16_f32 v157,v134,v135
v_cvt_pk_bf16_f32 v158,v136,v137
v_cvt_pk_bf16_f32 v159,v138,v139
v_pk_fma_f32 v[52:53],v[140:141],v[48:49],v[50:51]
v_pk_fma_f32 v[54:55],v[142:143],v[48:49],v[50:51]
v_pk_fma_f32 v[56:57],v[144:145],v[48:49],v[50:51]
v_pk_fma_f32 v[58:59],v[146:147],v[48:49],v[50:51]
v_pk_fma_f32 v[140:141],v[52:53],v[108:109],v[140:141]
v_pk_fma_f32 v[142:143],v[54:55],v[110:111],v[142:143]
v_pk_fma_f32 v[144:145],v[56:57],v[112:113],v[144:145]
v_pk_fma_f32 v[146:147],v[58:59],v[114:115],v[146:147]
v_cvt_pk_bf16_f32 v160,v140,v141
v_cvt_pk_bf16_f32 v161,v142,v143
v_cvt_pk_bf16_f32 v162,v144,v145
v_cvt_pk_bf16_f32 v163,v146,v147
v_mfma_f32_16x16x32_bf16 v[36:39],v[32:35],v[148:151],0
buffer_store_dwordx4 v[148:151],v1,s[36:39],s57 offen offset:0 sc0 sc1
v_mfma_f32_16x16x32_bf16 v[40:43],v[148:151],v[148:151],0
M 36,v[32:35],152
buffer_store_dwordx4 v[152:155],v1,s[36:39],s57 offen offset:2048 sc0 sc1
M 40,v[152:155],152
M 36,v[32:35],156
buffer_store_dwordx4 v[156:159],v1,s[36:39],s58 offen offset:0 sc0 sc1
M 40,v[156:159],156
M 36,v[32:35],160
buffer_store_dwordx4 v[160:163],v1,s[36:39],s58 offen offset:2048 sc0 sc1
M 40,v[160:163],160
s_nop 5
v_add_f32_dpp v44,v36,v36 row_ror:8 row_mask:0xf bank_mask:0xf
s_nop 0
v_cndmask_b32 v45,v40,v41,s[70:71]
v_cndmask_b32 v46,v42,v43,s[70:71]
ds_write_b32 v2,v44 offset:1024
v_cndmask_b32 v45,v45,v46,s[72:73]
ds_write_b32 v0,v45 offset:1024
L 0
s_barrier
R 52,3,1024
R 56,3,1040
R 60,3,1056
L 0
v_pk_add_f32 v[52:53],v[52:53],v[54:55]
v_pk_add_f32 v[56:57],v[56:57],v[58:59]
v_pk_add_f32 v[60:61],v[60:61],v[62:63]
v_add_f32 v52,v52,v53
v_pk_add_f32 v[56:57],v[56:57],v[60:61]
v_mul_f32 v52,s68,v52
v_add_f32 v53,v56,v57
v_mul_f32 v53,s68,v53
v_fma_f32 v53,-v52,v52,v53
v_max_f32 v53,0,v53
v_add_f32 v54,s69,v53
v_fma_f32 v55,s69,v54,v53
v_rsq_f32 v65,v54
v_rsq_f32 v54,v55
v_mul_f32_e64 v64,-v52,v65
v_mul_f32 v63,v55,v54
v_mul_f32 v61,s76,v54
v_mul_f32 v55,v54,v54
v_mul_f32 v62,s77,v55
v_mul_f32 v62,v62,v61
v_cndmask_b32 v60,v52,v64,s[74:75]
v_cndmask_b32 v61,v61,v65,s[74:75]
buffer_store_dwordx4 v[60:63],v4,s[36:39],s59 offen offset:0 sc0 sc1
s_mov_b32 s63,s60
s_cmp_lt_i32 s45,s47
s_cbranch_scc0 Lr_mid_%=
Lr_a1_%=:
s_waitcnt vmcnt(10)
v_mfma_f32_16x16x32_bf16 v[36:39],v[32:35],v[196:199],0
s_add_u32 s79,s45,s46
s_cmp_lt_u32 s45,64
s_cselect_b32 s78,s49,0
v_mfma_f32_16x16x32_bf16 v[40:43],v[196:199],v[196:199],0
s_cmp_ge_u32 s79,64
s_cselect_b32 s78,s78,0
s_add_u32 s62,s79,s78
M 36,v[32:35],200
s_lshr_b32 s78,s45,1
s_and_b32 s79,s45,1
s_lshl_b32 s79,s79,7
M 40,v[200:203],200
s_lshl_b32 s57,s78,15
s_lshl_b32 s55,s62,14
s_cmp_lt_u32 s62,s47
M 36,v[32:35],204
s_cselect_b32 s55,s55,0x7fff0000
s_add_u32 s57,s57,s79
s_lshl_b32 s59,s78,9
M 40,v[204:207],204
buffer_load_dwordx4 v[164:167],v8,s[84:87],s55 offen offset:0 nt
M 36,v[32:35],208
buffer_load_dwordx4 v[168:171],v8,s[84:87],s55 offen offset:128 nt
M 40,v[208:211],208
buffer_load_dwordx4 v[172:175],v8,s[84:87],s55 offen offset:256 nt
v_mfma_f32_16x16x32_bf16 v[116:119],v[26:29],v[196:199],0
buffer_load_dwordx4 v[176:179],v8,s[84:87],s55 offen offset:384 nt
v_mfma_f32_16x16x32_bf16 v[120:123],v[24:27],v[196:199],0
s_add_u32 s57,s57,8388608
s_add_u32 s59,s59,s79
s_lshl_b32 s60,s45,9
v_mfma_f32_16x16x32_bf16 v[124:127],v[26:29],v[200:203],0
s_add_u32 s58,s57,4096
s_add_u32 s59,s59,106954752
s_add_u32 s60,s60,s66
v_mfma_f32_16x16x32_bf16 v[128:131],v[24:27],v[200:203],0
v_add_f32_dpp v44,v36,v36 row_ror:8 row_mask:0xf bank_mask:0xf
v_cndmask_b32 v45,v40,v41,s[70:71]
v_mfma_f32_16x16x32_bf16 v[132:135],v[26:29],v[204:207],0
ds_write_b32 v2,v44 offset:0
v_mfma_f32_16x16x32_bf16 v[136:139],v[24:27],v[204:207],0
v_cndmask_b32 v46,v42,v43,s[70:71]
v_cndmask_b32 v45,v45,v46,s[72:73]
v_mfma_f32_16x16x32_bf16 v[140:143],v[26:29],v[208:211],0
ds_write_b32 v0,v45 offset:0
v_mfma_f32_16x16x32_bf16 v[144:147],v[24:27],v[208:211],0
L 0
s_barrier
R 52,3,0
R 56,3,16
R 60,3,32
s_cmp_lg_u32 s67,0
s_cbranch_scc1 Lr_b1_%=
s_waitcnt vmcnt(4)
buffer_store_dword v17,v5,s[36:39],s63 offen offset:0 sc0 sc1
s_mov_b32 s63,0x7fff0000
s_mov_b32 s38,111243392
s_cmp_lt_u32 s45,64
s_cselect_b32 s64,-192,0
s_add_u32 s64,s64,256
s_cmp_lt_u32 s45,64
s_cselect_b32 s61,0,32768
s_add_u32 s61,s61,111177728
s_mul_i32 s56,s45,16384
s_add_u32 s56,s56,98566144
Lr_bpy1_%=:
buffer_load_dword v18,v7,s[36:39],s61 offen offset:0 sc1
s_add_u32 s82,s82,1
s_waitcnt vmcnt(0)
v_cmp_eq_u32 vcc,s53,v18
s_nop 0
s_cmp_eq_u64 vcc,-1
s_cbranch_scc1 Lr_bqy1_%=
s_sleep 2
s_cmp_lt_u32 s82,20000
s_cbranch_scc1 Lr_bpy1_%=
Lr_bqy1_%=:
s_add_u32 m0,s65,16384
s_nop 0
D 9,56,0
D 9,56,1024
D 9,56,2048
D 9,56,3072
s_waitcnt vmcnt(0)
Lr_b1_%=:
s_waitcnt vmcnt(10)
T v[68:69],10,16384
T v[70:71],10,16448
T v[72:73],10,17408
T v[74:75],10,17472
T v[76:77],10,18432
T v[78:79],10,18496
T v[80:81],10,19456
T v[82:83],10,19520
s_mov_b32 s45,s62
s_min_u32 s78,s64,s47
s_mul_i32 s56,s45,16384
s_add_u32 s56,s56,98566144
s_cmp_lt_i32 s45,s78
s_cselect_b32 s67,1,0
s_cselect_b32 s56,s56,0x7fff0000
s_add_u32 m0,s65,0
s_nop 0
D 9,56,0
D 9,56,1024
D 9,56,2048
D 9,56,3072
L 8
v_pk_add_f32 v[52:53],v[52:53],v[54:55]
v_pk_add_f32 v[56:57],v[56:57],v[58:59]
v_pk_add_f32 v[60:61],v[60:61],v[62:63]
v_add_f32 v52,v52,v53
v_pk_add_f32 v[56:57],v[56:57],v[60:61]
v_mul_f32 v52,s68,v52
v_add_f32 v53,v56,v57
v_mul_f32 v53,s68,v53
v_fma_f32 v53,-v52,v52,v53
v_max_f32 v53,0,v53
s_waitcnt vmcnt(8)
buffer_store_dword v17,v5,s[36:39],s63 offen offset:0 sc0 sc1
s_mov_b32 s63,0x7fff0000
L 6
v_lshlrev_b32 v84,16,v68
v_and_b32 v85,0xffff0000,v68
v_lshlrev_b32 v86,16,v69
v_and_b32 v87,0xffff0000,v69
v_lshlrev_b32 v88,16,v70
v_and_b32 v89,0xffff0000,v70
v_lshlrev_b32 v90,16,v71
v_and_b32 v91,0xffff0000,v71
L 4
v_lshlrev_b32 v92,16,v72
v_and_b32 v93,0xffff0000,v72
v_lshlrev_b32 v94,16,v73
v_and_b32 v95,0xffff0000,v73
v_lshlrev_b32 v96,16,v74
v_and_b32 v97,0xffff0000,v74
v_lshlrev_b32 v98,16,v75
v_and_b32 v99,0xffff0000,v75
L 2
v_lshlrev_b32 v100,16,v76
v_and_b32 v101,0xffff0000,v76
v_lshlrev_b32 v102,16,v77
v_and_b32 v103,0xffff0000,v77
v_lshlrev_b32 v104,16,v78
v_and_b32 v105,0xffff0000,v78
)"
R"(v_lshlrev_b32 v106,16,v79
v_and_b32 v107,0xffff0000,v79
L 0
v_lshlrev_b32 v108,16,v80
v_and_b32 v109,0xffff0000,v80
v_lshlrev_b32 v110,16,v81
v_and_b32 v111,0xffff0000,v81
v_lshlrev_b32 v112,16,v82
v_and_b32 v113,0xffff0000,v82
v_lshlrev_b32 v114,16,v83
v_and_b32 v115,0xffff0000,v83
v_add_f32 v53,s69,v53
v_rsq_f32 v48,v53
s_nop 0
v_mul_f32_e64 v50,-v52,v48
v_mov_b32 v49,v48
v_mov_b32 v51,v50
v_pk_fma_f32 v[52:53],v[116:117],v[48:49],v[50:51]
v_pk_fma_f32 v[54:55],v[118:119],v[48:49],v[50:51]
v_pk_fma_f32 v[56:57],v[120:121],v[48:49],v[50:51]
v_pk_fma_f32 v[58:59],v[122:123],v[48:49],v[50:51]
v_pk_fma_f32 v[116:117],v[52:53],v[84:85],v[116:117]
v_pk_fma_f32 v[118:119],v[54:55],v[86:87],v[118:119]
v_pk_fma_f32 v[120:121],v[56:57],v[88:89],v[120:121]
v_pk_fma_f32 v[122:123],v[58:59],v[90:91],v[122:123]
v_cvt_pk_bf16_f32 v148,v116,v117
v_cvt_pk_bf16_f32 v149,v118,v119
v_cvt_pk_bf16_f32 v150,v120,v121
v_cvt_pk_bf16_f32 v151,v122,v123
v_pk_fma_f32 v[52:53],v[124:125],v[48:49],v[50:51]
v_pk_fma_f32 v[54:55],v[126:127],v[48:49],v[50:51]
v_pk_fma_f32 v[56:57],v[128:129],v[48:49],v[50:51]
v_pk_fma_f32 v[58:59],v[130:131],v[48:49],v[50:51]
v_pk_fma_f32 v[124:125],v[52:53],v[92:93],v[124:125]
v_pk_fma_f32 v[126:127],v[54:55],v[94:95],v[126:127]
v_pk_fma_f32 v[128:129],v[56:57],v[96:97],v[128:129]
v_pk_fma_f32 v[130:131],v[58:59],v[98:99],v[130:131]
v_cvt_pk_bf16_f32 v152,v124,v125
v_cvt_pk_bf16_f32 v153,v126,v127
v_cvt_pk_bf16_f32 v154,v128,v129
v_cvt_pk_bf16_f32 v155,v130,v131
v_pk_fma_f32 v[52:53],v[132:133],v[48:49],v[50:51]
v_pk_fma_f32 v[54:55],v[134:135],v[48:49],v[50:51]
v_pk_fma_f32 v[56:57],v[136:137],v[48:49],v[50:51]
v_pk_fma_f32 v[58:59],v[138:139],v[48:49],v[50:51]
v_pk_fma_f32 v[132:133],v[52:53],v[100:101],v[132:133]
v_pk_fma_f32 v[134:135],v[54:55],v[102:103],v[134:135]
v_pk_fma_f32 v[136:137],v[56:57],v[104:105],v[136:137]
v_pk_fma_f32 v[138:139],v[58:59],v[106:107],v[138:139]
v_cvt_pk_bf16_f32 v156,v132,v133
v_cvt_pk_bf16_f32 v157,v134,v135
v_cvt_pk_bf16_f32 v158,v136,v137
v_cvt_pk_bf16_f32 v159,v138,v139
v_pk_fma_f32 v[52:53],v[140:141],v[48:49],v[50:51]
v_pk_fma_f32 v[54:55],v[142:143],v[48:49],v[50:51]
v_pk_fma_f32 v[56:57],v[144:145],v[48:49],v[50:51]
v_pk_fma_f32 v[58:59],v[146:147],v[48:49],v[50:51]
v_pk_fma_f32 v[140:141],v[52:53],v[108:109],v[140:141]
v_pk_fma_f32 v[142:143],v[54:55],v[110:111],v[142:143]
v_pk_fma_f32 v[144:145],v[56:57],v[112:113],v[144:145]
v_pk_fma_f32 v[146:147],v[58:59],v[114:115],v[146:147]
v_cvt_pk_bf16_f32 v160,v140,v141
v_cvt_pk_bf16_f32 v161,v142,v143
v_cvt_pk_bf16_f32 v162,v144,v145
v_cvt_pk_bf16_f32 v163,v146,v147
v_mfma_f32_16x16x32_bf16 v[36:39],v[32:35],v[148:151],0
buffer_store_dwordx4 v[148:151],v1,s[36:39],s57 offen offset:0 sc0 sc1
v_mfma_f32_16x16x32_bf16 v[40:43],v[148:151],v[148:151],0
M 36,v[32:35],152
buffer_store_dwordx4 v[152:155],v1,s[36:39],s57 offen offset:2048 sc0 sc1
M 40,v[152:155],152
M 36,v[32:35],156
buffer_store_dwordx4 v[156:159],v1,s[36:39],s58 offen offset:0 sc0 sc1
M 40,v[156:159],156
M 36,v[32:35],160
buffer_store_dwordx4 v[160:163],v1,s[36:39],s58 offen offset:2048 sc0 sc1
M 40,v[160:163],160
s_nop 5
v_add_f32_dpp v44,v36,v36 row_ror:8 row_mask:0xf bank_mask:0xf
s_nop 0
v_cndmask_b32 v45,v40,v41,s[70:71]
v_cndmask_b32 v46,v42,v43,s[70:71]
ds_write_b32 v2,v44 offset:1024
v_cndmask_b32 v45,v45,v46,s[72:73]
ds_write_b32 v0,v45 offset:1024
L 0
s_barrier
R 52,3,1024
R 56,3,1040
R 60,3,1056
L 0
v_pk_add_f32 v[52:53],v[52:53],v[54:55]
v_pk_add_f32 v[56:57],v[56:57],v[58:59]
v_pk_add_f32 v[60:61],v[60:61],v[62:63]
v_add_f32 v52,v52,v53
v_pk_add_f32 v[56:57],v[56:57],v[60:61]
v_mul_f32 v52,s68,v52
v_add_f32 v53,v56,v57
v_mul_f32 v53,s68,v53
v_fma_f32 v53,-v52,v52,v53
v_max_f32 v53,0,v53
v_add_f32 v54,s69,v53
v_fma_f32 v55,s69,v54,v53
v_rsq_f32 v65,v54
v_rsq_f32 v54,v55
v_mul_f32_e64 v64,-v52,v65
v_mul_f32 v63,v55,v54
v_mul_f32 v61,s76,v54
v_mul_f32 v55,v54,v54
v_mul_f32 v62,s77,v55
v_mul_f32 v62,v62,v61
v_cndmask_b32 v60,v52,v64,s[74:75]
v_cndmask_b32 v61,v61,v65,s[74:75]
buffer_store_dwordx4 v[60:63],v4,s[36:39],s59 offen offset:0 sc0 sc1
s_mov_b32 s63,s60
s_cmp_lt_i32 s45,s47
s_cbranch_scc1 Lr_a0_%=
Lr_mid_%=:
s_cmp_eq_u32 s93,0
s_cbranch_scc1 Lr_h0_%=
s_mov_b64 s[40:41],%24
s_mov_b32 s42,7340032
s_mov_b32 s43,0x00020000
s_lshl_b32 s57,s44,11
s_add_u32 s55,s50,0
s_add_u32 m0,s57,40960
s_nop 0
buffer_load_dwordx4 v13,s[40:43],s55 offen offset:0  lds
s_add_u32 m0,s57,41984
s_nop 0
buffer_load_dwordx4 v14,s[40:43],s55 offen offset:0  lds
s_add_u32 m0,s57,49024
s_nop 0
buffer_load_dwordx4 v13,s[40:43],s55 offen offset:128  lds
s_add_u32 m0,s57,50048
s_nop 0
buffer_load_dwordx4 v14,s[40:43],s55 offen offset:128  lds
s_add_u32 s55,s50,131072
s_add_u32 m0,s57,57344
s_nop 0
buffer_load_dwordx4 v13,s[40:43],s55 offen offset:0  lds
s_add_u32 m0,s57,58368
s_nop 0
buffer_load_dwordx4 v14,s[40:43],s55 offen offset:0  lds
s_add_u32 m0,s57,65408
s_nop 0
buffer_load_dwordx4 v13,s[40:43],s55 offen offset:128  lds
s_add_u32 m0,s57,66432
s_nop 0
buffer_load_dwordx4 v14,s[40:43],s55 offen offset:128  lds
s_add_u32 s55,s50,262144
s_add_u32 m0,s57,73728
s_nop 0
buffer_load_dwordx4 v13,s[40:43],s55 offen offset:0  lds
s_add_u32 m0,s57,74752
s_nop 0
buffer_load_dwordx4 v14,s[40:43],s55 offen offset:0  lds
s_add_u32 m0,s57,81792
s_nop 0
buffer_load_dwordx4 v13,s[40:43],s55 offen offset:128  lds
s_add_u32 m0,s57,82816
s_nop 0
buffer_load_dwordx4 v14,s[40:43],s55 offen offset:128  lds
s_add_u32 s55,s50,393216
s_add_u32 m0,s57,90112
s_nop 0
buffer_load_dwordx4 v13,s[40:43],s55 offen offset:0  lds
s_add_u32 m0,s57,91136
s_nop 0
buffer_load_dwordx4 v14,s[40:43],s55 offen offset:0  lds
s_add_u32 m0,s57,98176
s_nop 0
buffer_load_dwordx4 v13,s[40:43],s55 offen offset:128  lds
s_add_u32 m0,s57,99200
s_nop 0
buffer_load_dwordx4 v14,s[40:43],s55 offen offset:128  lds
s_add_u32 s55,s50,524288
s_add_u32 m0,s57,106496
s_nop 0
buffer_load_dwordx4 v13,s[40:43],s55 offen offset:0  lds
s_add_u32 m0,s57,107520
s_nop 0
buffer_load_dwordx4 v14,s[40:43],s55 offen offset:0  lds
s_add_u32 m0,s57,114560
s_nop 0
buffer_load_dwordx4 v13,s[40:43],s55 offen offset:128  lds
s_add_u32 m0,s57,115584
s_nop 0
buffer_load_dwordx4 v14,s[40:43],s55 offen offset:128  lds
s_add_u32 s55,s50,655360
s_add_u32 m0,s57,122880
s_nop 0
buffer_load_dwordx4 v13,s[40:43],s55 offen offset:0  lds
s_add_u32 m0,s57,123904
s_nop 0
buffer_load_dwordx4 v14,s[40:43],s55 offen offset:0  lds
s_add_u32 m0,s57,130944
s_nop 0
buffer_load_dwordx4 v13,s[40:43],s55 offen offset:128  lds
s_add_u32 m0,s57,131968
s_nop 0
buffer_load_dwordx4 v14,s[40:43],s55 offen offset:128  lds
s_waitcnt vmcnt(24)
buffer_store_dword v17,v5,s[36:39],s63 offen offset:0 sc0 sc1
s_mov_b32 s63,0x7fff0000
Lr_h0_%=:
s_waitcnt vmcnt(0)
buffer_store_dword v17,v5,s[36:39],s63 offen offset:0 sc0 sc1
s_mov_b32 s63,0x7fff0000
s_mov_b32 s45,s93
s_cmp_eq_u32 s93,0
s_cbranch_scc1 Lr_h1_%=
s_mov_b64 s[40:41],%24
s_mov_b32 s42,7340032
s_mov_b32 s43,0x00020000
s_lshl_b32 s57,s44,11
s_waitcnt vmcnt(22) lgkmcnt(0)
s_barrier
s_add_u32 s55,s50,786432
s_add_u32 m0,s57,139264
s_nop 0
buffer_load_dwordx4 v13,s[40:43],s55 offen offset:0  lds
s_add_u32 m0,s57,140288
s_nop 0
buffer_load_dwordx4 v14,s[40:43],s55 offen offset:0  lds
s_add_u32 m0,s57,147328
s_nop 0
buffer_load_dwordx4 v13,s[40:43],s55 offen offset:128  lds
s_add_u32 m0,s57,148352
s_nop 0
buffer_load_dwordx4 v14,s[40:43],s55 offen offset:128  lds
v_add_u32 v44,40960,v15
v_add_u32 v45,40960,v16
T a[0:1],44,0
T a[2:3],44,512
T a[4:5],45,0
T a[6:7],45,512
T a[8:9],44,4096
T a[10:11],44,4608
T a[12:13],45,4096
T a[14:15],45,4608
s_waitcnt vmcnt(22) lgkmcnt(0)
s_barrier
s_add_u32 s55,s50,917504
s_add_u32 m0,s57,40960
s_nop 0
buffer_load_dwordx4 v13,s[40:43],s55 offen offset:0  lds
s_add_u32 m0,s57,41984
s_nop 0
buffer_load_dwordx4 v14,s[40:43],s55 offen offset:0  lds
s_add_u32 m0,s57,49024
s_nop 0
buffer_load_dwordx4 v13,s[40:43],s55 offen offset:128  lds
s_add_u32 m0,s57,50048
s_nop 0
buffer_load_dwordx4 v14,s[40:43],s55 offen offset:128  lds
v_add_u32 v44,57344,v15
v_add_u32 v45,57344,v16
T a[16:17],44,0
T a[18:19],44,512
T a[20:21],45,0
T a[22:23],45,512
T a[24:25],44,4096
T a[26:27],44,4608
T a[28:29],45,4096
T a[30:31],45,4608
s_waitcnt vmcnt(22) lgkmcnt(0)
s_barrier
s_add_u32 s55,s50,1048576
s_add_u32 m0,s57,57344
s_nop 0
buffer_load_dwordx4 v13,s[40:43],s55 offen offset:0  lds
s_add_u32 m0,s57,58368
s_nop 0
buffer_load_dwordx4 v14,s[40:43],s55 offen offset:0  lds
s_add_u32 m0,s57,65408
s_nop 0
buffer_load_dwordx4 v13,s[40:43],s55 offen offset:128  lds
s_add_u32 m0,s57,66432
s_nop 0
buffer_load_dwordx4 v14,s[40:43],s55 offen offset:128  lds
v_add_u32 v44,73728,v15
v_add_u32 v45,73728,v16
T a[32:33],44,0
T a[34:35],44,512
T a[36:37],45,0
T a[38:39],45,512
T a[40:41],44,4096
T a[42:43],44,4608
T a[44:45],45,4096
T a[46:47],45,4608
s_waitcnt vmcnt(22) lgkmcnt(0)
s_barrier
s_add_u32 s55,s50,1179648
s_add_u32 m0,s57,73728
s_nop 0
buffer_load_dwordx4 v13,s[40:43],s55 offen offset:0  lds
s_add_u32 m0,s57,74752
s_nop 0
buffer_load_dwordx4 v14,s[40:43],s55 offen offset:0  lds
s_add_u32 m0,s57,81792
s_nop 0
buffer_load_dwordx4 v13,s[40:43],s55 offen offset:128  lds
s_add_u32 m0,s57,82816
s_nop 0
buffer_load_dwordx4 v14,s[40:43],s55 offen offset:128  lds
v_add_u32 v44,90112,v15
v_add_u32 v45,90112,v16
T a[48:49],44,0
T a[50:51],44,512
T a[52:53],45,0
T a[54:55],45,512
T a[56:57],44,4096
T a[58:59],44,4608
T a[60:61],45,4096
T a[62:63],45,4608
s_waitcnt vmcnt(22) lgkmcnt(0)
s_barrier
s_add_u32 s55,s50,1310720
s_add_u32 m0,s57,90112
s_nop 0
buffer_load_dwordx4 v13,s[40:43],s55 offen offset:0  lds
s_add_u32 m0,s57,91136
s_nop 0
buffer_load_dwordx4 v14,s[40:43],s55 offen offset:0  lds
s_add_u32 m0,s57,98176
s_nop 0
buffer_load_dwordx4 v13,s[40:43],s55 offen offset:128  lds
s_add_u32 m0,s57,99200
s_nop 0
buffer_load_dwordx4 v14,s[40:43],s55 offen offset:128  lds
v_add_u32 v44,106496,v15
v_add_u32 v45,106496,v16
T a[64:65],44,0
T a[66:67],44,512
T a[68:69],45,0
T a[70:71],45,512
T a[72:73],44,4096
T a[74:75],44,4608
T a[76:77],45,4096
T a[78:79],45,4608
L 0
s_barrier
Lr_h1_%=:
s_lshr_b32 s78,s45,1
s_and_b32 s79,s45,1
s_lshl_b32 s79,s79,7
s_lshl_b32 s55,s78,15
s_add_u32 s55,s55,s79
s_mul_i32 s56,s45,16384
s_add_u32 s56,s56,98566144
s_lshl_b32 s61,s78,11
s_add_u32 s61,s61,110587904
s_add_u32 s78,s45,s46
s_lshr_b32 s78,s78,1
s_lshl_b32 s62,s78,11
s_add_u32 s62,s62,110587904
s_add_u32 m0,s65,0
s_nop 0
D 9,56,0
D 9,56,1024
D 9,56,2048
D 9,56,3072
Lr_bps_%=:
buffer_load_dword v18,v6,s[36:39],s61 offen offset:0 sc1
s_add_u32 s82,s82,1
s_waitcnt vmcnt(0)
v_cmp_eq_u32 vcc,s53,v18
s_nop 0
s_cmp_eq_u64 vcc,-1
s_cbranch_scc1 Lr_bqs_%=
s_sleep 2
s_cmp_lt_u32 s82,20000
s_cbranch_scc1 Lr_bps_%=
Lr_bqs_%=:
s_add_u32 s78,s55,71303168
buffer_load_dwordx4 v[164:167],v1,s[36:39],s78 offen offset:0 sc1
buffer_load_dwordx4 v[168:171],v1,s[36:39],s78 offen offset:2048 sc1
s_add_u32 s78,s55,71307264
buffer_load_dwordx4 v[172:175],v1,s[36:39],s78 offen offset:0 sc1
buffer_load_dwordx4 v[176:179],v1,s[36:39],s78 offen offset:2048 sc1
s_add_u32 s78,s55,89128960
buffer_load_dwordx4 v[180:183],v1,s[36:39],s78 offen offset:0 sc1
buffer_load_dwordx4 v[184:187],v1,s[36:39],s78 offen offset:2048 sc1
s_add_u32 s78,s55,89133056
buffer_load_dwordx4 v[188:191],v1,s[36:39],s78 offen offset:0 sc1
buffer_load_dwordx4 v[192:195],v1,s[36:39],s78 offen offset:2048 sc1
buffer_load_dword v18,v6,s[36:39],s62 offen offset:0 sc1
s_cmp_eq_u32 s93,0
s_cbranch_scc1 Lr_h2_%=
s_mov_b64 s[40:41],%24
s_mov_b32 s42,7340032
s_mov_b32 s43,0x00020000
s_lshl_b32 s57,s44,11
L 0
s_barrier
s_add_u32 s55,s50,1441792
s_add_u32 m0,s57,106496
s_nop 0
buffer_load_dwordx4 v13,s[40:43],s55 offen offset:0  lds
s_add_u32 m0,s57,107520
s_nop 0
buffer_load_dwordx4 v14,s[40:43],s55 offen offset:0  lds
s_add_u32 m0,s57,114560
s_nop 0
buffer_load_dwordx4 v13,s[40:43],s55 offen offset:128  lds
s_add_u32 m0,s57,115584
s_nop 0
buffer_load_dwordx4 v14,s[40:43],s55 offen offset:128  lds
v_add_u32 v44,122880,v15
v_add_u32 v45,122880,v16
T a[80:81],44,0
T a[82:83],44,512
T a[84:85],45,0
T a[86:87],45,512
T a[88:89],44,4096
T a[90:91],44,4608
T a[92:93],45,4096
T a[94:95],45,4608
L 0
s_barrier
s_add_u32 s55,s50,1572864
s_add_u32 m0,s57,122880
s_nop 0
buffer_load_dwordx4 v13,s[40:43],s55 offen offset:0  lds
s_add_u32 m0,s57,123904
s_nop 0
buffer_load_dwordx4 v14,s[40:43],s55 offen offset:0  lds
s_add_u32 m0,s57,130944
s_nop 0
buffer_load_dwordx4 v13,s[40:43],s55 offen offset:128  lds
s_add_u32 m0,s57,131968
s_nop 0
buffer_load_dwordx4 v14,s[40:43],s55 offen offset:128  lds
v_add_u32 v44,139264,v15
v_add_u32 v45,139264,v16
T a[96:97],44,0
T a[98:99],44,512
T a[100:101],45,0
T a[102:103],45,512
T a[104:105],44,4096
T a[106:107],44,4608
T a[108:109],45,4096
T a[110:111],45,4608
L 0
s_barrier
s_add_u32 s55,s50,1703936
s_add_u32 m0,s57,139264
s_nop 0
buffer_load_dwordx4 v13,s[40:43],s55 offen offset:0  lds
s_add_u32 m0,s57,140288
s_nop 0
buffer_load_dwordx4 v14,s[40:43],s55 offen offset:0  lds
s_add_u32 m0,s57,147328
s_nop 0
buffer_load_dwordx4 v13,s[40:43],s55 offen offset:128  lds
s_add_u32 m0,s57,148352
s_nop 0
buffer_load_dwordx4 v14,s[40:43],s55 offen offset:128  lds
v_add_u32 v44,40960,v15
v_add_u32 v45,40960,v16
T a[112:113],44,0
T a[114:115],44,512
T a[116:117],45,0
T a[118:119],45,512
T a[120:121],44,4096
T a[122:123],44,4608
T a[124:125],45,4096
T a[126:127],45,4608
L 0
s_barrier
s_add_u32 s55,s50,1835008
s_add_u32 m0,s57,40960
s_nop 0
buffer_load_dwordx4 v13,s[40:43],s55 offen offset:0  lds
s_add_u32 m0,s57,41984
s_nop 0
buffer_load_dwordx4 v14,s[40:43],s55 offen offset:0  lds
s_add_u32 m0,s57,49024
s_nop 0
buffer_load_dwordx4 v13,s[40:43],s55 offen offset:128  lds
s_add_u32 m0,s57,50048
s_nop 0
buffer_load_dwordx4 v14,s[40:43],s55 offen offset:128  lds
v_add_u32 v44,57344,v15
v_add_u32 v45,57344,v16
T a[128:129],44,0
T a[130:131],44,512
T a[132:133],45,0
T a[134:135],45,512
T a[136:137],44,4096
T a[138:139],44,4608
T a[140:141],45,4096
T a[142:143],45,4608
L 0
s_barrier
s_add_u32 s55,s50,1966080
s_add_u32 m0,s57,57344
s_nop 0
buffer_load_dwordx4 v13,s[40:43],s55 offen offset:0  lds
)"
R"(s_add_u32 m0,s57,58368
s_nop 0
buffer_load_dwordx4 v14,s[40:43],s55 offen offset:0  lds
s_add_u32 m0,s57,65408
s_nop 0
buffer_load_dwordx4 v13,s[40:43],s55 offen offset:128  lds
s_add_u32 m0,s57,66432
s_nop 0
buffer_load_dwordx4 v14,s[40:43],s55 offen offset:128  lds
v_add_u32 v44,73728,v15
v_add_u32 v45,73728,v16
T a[144:145],44,0
T a[146:147],44,512
T a[148:149],45,0
T a[150:151],45,512
T a[152:153],44,4096
T a[154:155],44,4608
T a[156:157],45,4096
T a[158:159],45,4608
L 0
s_barrier
s_add_u32 s55,s50,2097152
s_add_u32 m0,s57,73728
s_nop 0
buffer_load_dwordx4 v13,s[40:43],s55 offen offset:0  lds
s_add_u32 m0,s57,74752
s_nop 0
buffer_load_dwordx4 v14,s[40:43],s55 offen offset:0  lds
s_add_u32 m0,s57,81792
s_nop 0
buffer_load_dwordx4 v13,s[40:43],s55 offen offset:128  lds
s_add_u32 m0,s57,82816
s_nop 0
buffer_load_dwordx4 v14,s[40:43],s55 offen offset:128  lds
v_add_u32 v44,90112,v15
v_add_u32 v45,90112,v16
T a[160:161],44,0
T a[162:163],44,512
T a[164:165],45,0
T a[166:167],45,512
T a[168:169],44,4096
T a[170:171],44,4608
T a[172:173],45,4096
T a[174:175],45,4608
s_waitcnt vmcnt(20) lgkmcnt(0)
s_barrier
s_add_u32 s55,s50,2228224
s_add_u32 m0,s57,90112
s_nop 0
buffer_load_dwordx4 v13,s[40:43],s55 offen offset:0  lds
s_add_u32 m0,s57,91136
s_nop 0
buffer_load_dwordx4 v14,s[40:43],s55 offen offset:0  lds
s_add_u32 m0,s57,98176
s_nop 0
buffer_load_dwordx4 v13,s[40:43],s55 offen offset:128  lds
s_add_u32 m0,s57,99200
s_nop 0
buffer_load_dwordx4 v14,s[40:43],s55 offen offset:128  lds
v_add_u32 v44,106496,v15
v_add_u32 v45,106496,v16
T a[176:177],44,0
T a[178:179],44,512
T a[180:181],45,0
T a[182:183],45,512
T a[184:185],44,4096
T a[186:187],44,4608
T a[188:189],45,4096
T a[190:191],45,4608
s_waitcnt vmcnt(20) lgkmcnt(0)
s_barrier
s_add_u32 s55,s50,2359296
s_add_u32 m0,s57,106496
s_nop 0
buffer_load_dwordx4 v13,s[40:43],s55 offen offset:0  lds
s_add_u32 m0,s57,107520
s_nop 0
buffer_load_dwordx4 v14,s[40:43],s55 offen offset:0  lds
s_add_u32 m0,s57,114560
s_nop 0
buffer_load_dwordx4 v13,s[40:43],s55 offen offset:128  lds
s_add_u32 m0,s57,115584
s_nop 0
buffer_load_dwordx4 v14,s[40:43],s55 offen offset:128  lds
v_add_u32 v44,122880,v15
v_add_u32 v45,122880,v16
T a[192:193],44,0
T a[194:195],44,512
T a[196:197],45,0
T a[198:199],45,512
T a[200:201],44,4096
T a[202:203],44,4608
T a[204:205],45,4096
T a[206:207],45,4608
s_waitcnt vmcnt(20) lgkmcnt(0)
s_barrier
s_add_u32 s55,s50,2490368
s_add_u32 m0,s57,122880
s_nop 0
buffer_load_dwordx4 v13,s[40:43],s55 offen offset:0  lds
s_add_u32 m0,s57,123904
s_nop 0
buffer_load_dwordx4 v14,s[40:43],s55 offen offset:0  lds
s_add_u32 m0,s57,130944
s_nop 0
buffer_load_dwordx4 v13,s[40:43],s55 offen offset:128  lds
s_add_u32 m0,s57,131968
s_nop 0
buffer_load_dwordx4 v14,s[40:43],s55 offen offset:128  lds
v_add_u32 v44,139264,v15
v_add_u32 v45,139264,v16
T a[208:209],44,0
T a[210:211],44,512
T a[212:213],45,0
T a[214:215],45,512
T a[216:217],44,4096
T a[218:219],44,4608
T a[220:221],45,4096
T a[222:223],45,4608
s_waitcnt vmcnt(20) lgkmcnt(0)
s_barrier
s_add_u32 s55,s50,2621440
s_add_u32 m0,s57,139264
s_nop 0
buffer_load_dwordx4 v13,s[40:43],s55 offen offset:0  lds
s_add_u32 m0,s57,140288
s_nop 0
buffer_load_dwordx4 v14,s[40:43],s55 offen offset:0  lds
s_add_u32 m0,s57,147328
s_nop 0
buffer_load_dwordx4 v13,s[40:43],s55 offen offset:128  lds
s_add_u32 m0,s57,148352
s_nop 0
buffer_load_dwordx4 v14,s[40:43],s55 offen offset:128  lds
v_add_u32 v44,40960,v15
v_add_u32 v45,40960,v16
T a[224:225],44,0
T a[226:227],44,512
T a[228:229],45,0
T a[230:231],45,512
T a[232:233],44,4096
T a[234:235],44,4608
T a[236:237],45,4096
T a[238:239],45,4608
s_waitcnt vmcnt(20) lgkmcnt(0)
s_barrier
s_add_u32 s55,s50,2752512
s_add_u32 m0,s57,40960
s_nop 0
buffer_load_dwordx4 v13,s[40:43],s55 offen offset:0  lds
s_add_u32 m0,s57,41984
s_nop 0
buffer_load_dwordx4 v14,s[40:43],s55 offen offset:0  lds
s_add_u32 m0,s57,49024
s_nop 0
buffer_load_dwordx4 v13,s[40:43],s55 offen offset:128  lds
s_add_u32 m0,s57,50048
s_nop 0
buffer_load_dwordx4 v14,s[40:43],s55 offen offset:128  lds
v_add_u32 v44,57344,v15
v_add_u32 v45,57344,v16
T a[240:241],44,0
T a[242:243],44,512
T a[244:245],45,0
T a[246:247],45,512
T a[248:249],44,4096
T a[250:251],44,4608
T a[252:253],45,4096
T a[254:255],45,4608
L 0
s_barrier
s_nop 0
Lr_h2_%=:
s_lshr_b32 s78,s45,1
s_and_b32 s79,s45,1
s_lshl_b32 s79,s79,7
s_lshl_b32 s57,s78,15
s_add_u32 s57,s57,s79
s_add_u32 s57,s57,12582912
s_add_u32 s58,s57,4096
s_lshl_b32 s59,s78,9
s_add_u32 s59,s59,s79
s_add_u32 s59,s59,107020288
s_lshl_b32 s60,s45,9
s_add_u32 s60,s60,s66
s_add_u32 s60,s60,131072
s_add_u32 s45,s45,s46
s_lshr_b32 s78,s45,1
s_and_b32 s79,s45,1
s_lshl_b32 s79,s79,7
s_lshl_b32 s55,s78,15
s_add_u32 s55,s55,s79
s_mul_i32 s56,s45,16384
s_add_u32 s56,s56,98566144
s_lshl_b32 s61,s78,11
s_add_u32 s61,s61,110587904
s_add_u32 s78,s45,s46
s_lshr_b32 s78,s78,1
s_lshl_b32 s62,s78,11
s_add_u32 s62,s62,110587904
s_waitcnt vmcnt(0)
Lr_it0_%=:
T v[68:69],10,0
T v[70:71],10,64
T v[72:73],10,1024
T v[74:75],10,1088
T v[76:77],10,2048
T v[78:79],10,2112
T v[80:81],10,3072
T v[82:83],10,3136
s_nop 0
v_cmp_eq_u32 vcc,s53,v18
s_nop 0
s_cmp_eq_u64 vcc,-1
s_cselect_b32 s78,s45,256
s_cmp_lt_u32 s78,256
s_cbranch_scc1 Lr_ta0_%=
buffer_load_dword v18,v6,s[36:39],s61 offen offset:0 sc1
v_mfma_f32_16x16x32_bf16 v[116:119],v[26:29],v[164:167],0
L 6
v_lshlrev_b32 v84,16,v68
v_and_b32 v85,0xffff0000,v68
v_mfma_f32_16x16x32_bf16 v[120:123],v[24:27],v[164:167],0
L 4
v_lshlrev_b32 v86,16,v69
v_and_b32 v87,0xffff0000,v69
M 116,v[26:29],180
L 2
v_lshlrev_b32 v88,16,v70
v_and_b32 v89,0xffff0000,v70
M 120,v[24:27],180
L 0
v_lshlrev_b32 v90,16,v71
v_and_b32 v91,0xffff0000,v71
v_mfma_f32_16x16x32_bf16 v[124:127],v[26:29],v[168:171],0
s_add_u32 m0,s65,16384
v_lshlrev_b32 v92,16,v72
v_and_b32 v93,0xffff0000,v72
v_mfma_f32_16x16x32_bf16 v[128:131],v[24:27],v[168:171],0
D 9,56,0
M 124,v[26:29],184
D 9,56,1024
M 128,v[24:27],184
D 9,56,2048
v_mfma_f32_16x16x32_bf16 v[132:135],v[26:29],v[172:175],0
D 9,56,3072
v_mfma_f32_16x16x32_bf16 v[136:139],v[24:27],v[172:175],0
v_cvt_pk_bf16_f32 v148,v116,v117
v_cvt_pk_bf16_f32 v149,v118,v119
M 132,v[26:29],188
v_cvt_pk_bf16_f32 v150,v120,v121
v_cvt_pk_bf16_f32 v151,v122,v123
M 136,v[24:27],188
v_cvt_pk_bf16_f32 v152,v124,v125
v_cvt_pk_bf16_f32 v153,v126,v127
v_mfma_f32_16x16x32_bf16 v[140:143],v[26:29],v[176:179],0
v_cvt_pk_bf16_f32 v154,v128,v129
v_cvt_pk_bf16_f32 v155,v130,v131
v_mfma_f32_16x16x32_bf16 v[144:147],v[24:27],v[176:179],0
v_lshlrev_b32 v94,16,v73
v_and_b32 v95,0xffff0000,v73
M 140,v[26:29],192
v_cvt_pk_bf16_f32 v156,v132,v133
v_cvt_pk_bf16_f32 v157,v134,v135
M 144,v[24:27],192
v_cvt_pk_bf16_f32 v158,v136,v137
v_cvt_pk_bf16_f32 v159,v138,v139
v_mfma_f32_16x16x32_bf16 v[36:39],v[32:35],v[148:151],0
v_lshlrev_b32 v96,16,v74
v_and_b32 v97,0xffff0000,v74
v_mfma_f32_16x16x32_bf16 v[40:43],v[148:151],v[148:151],0
v_lshlrev_b32 v98,16,v75
v_and_b32 v99,0xffff0000,v75
M 36,v[32:35],152
v_cvt_pk_bf16_f32 v160,v140,v141
v_cvt_pk_bf16_f32 v161,v142,v143
M 40,v[152:155],152
v_cvt_pk_bf16_f32 v162,v144,v145
v_cvt_pk_bf16_f32 v163,v146,v147
M 36,v[32:35],156
v_lshlrev_b32 v100,16,v76
v_and_b32 v101,0xffff0000,v76
M 40,v[156:159],156
v_lshlrev_b32 v102,16,v77
v_and_b32 v103,0xffff0000,v77
M 36,v[32:35],160
v_lshlrev_b32 v104,16,v78
v_and_b32 v105,0xffff0000,v78
M 40,v[160:163],160
v_lshlrev_b32 v106,16,v79
v_and_b32 v107,0xffff0000,v79
v_lshlrev_b32 v108,16,v80
v_and_b32 v109,0xffff0000,v80
v_lshlrev_b32 v110,16,v81
v_and_b32 v111,0xffff0000,v81
v_lshlrev_b32 v112,16,v82
v_and_b32 v113,0xffff0000,v82
v_lshlrev_b32 v114,16,v83
v_and_b32 v115,0xffff0000,v83
v_add_f32_dpp v44,v36,v36 row_ror:8 row_mask:0xf bank_mask:0xf
v_cndmask_b32 v45,v40,v41,s[70:71]
v_cndmask_b32 v46,v42,v43,s[70:71]
ds_write_b32 v2,v44 offset:0
v_cndmask_b32 v45,v45,v46,s[72:73]
ds_write_b32 v0,v45 offset:0
L 0
s_barrier
R 52,3,0
R 56,3,16
R 60,3,32
L 0
v_pk_add_f32 v[52:53],v[52:53],v[54:55]
v_pk_add_f32 v[56:57],v[56:57],v[58:59]
v_pk_add_f32 v[60:61],v[60:61],v[62:63]
v_add_f32 v52,v52,v53
v_pk_add_f32 v[56:57],v[56:57],v[60:61]
v_mul_f32 v52,s68,v52
v_add_f32 v53,v56,v57
v_mul_f32 v53,s68,v53
v_fma_f32 v53,-v52,v52,v53
v_max_f32 v53,0,v53
s_waitcnt vmcnt(5)
buffer_store_dword v17,v5,s[36:39],s63 offen offset:0 sc0 sc1
s_mov_b32 s63,0x7fff0000
s_waitcnt vmcnt(5)
v_cmp_eq_u32 vcc,s53,v18
s_nop 0
s_cmp_eq_u64 vcc,-1
s_cselect_b32 s78,s45,256
s_cmp_lt_u32 s78,256
s_cbranch_scc1 Lr_tb0_%=
buffer_load_dword v18,v6,s[36:39],s61 offen offset:0 sc1
v_add_f32 v53,s69,v53
v_rsq_f32 v48,v53
s_nop 0
v_mul_f32_e64 v50,-v52,v48
v_mov_b32 v49,v48
v_mov_b32 v51,v50
v_pk_fma_f32 v[52:53],v[116:117],v[48:49],v[50:51]
v_pk_fma_f32 v[54:55],v[118:119],v[48:49],v[50:51]
v_pk_fma_f32 v[56:57],v[120:121],v[48:49],v[50:51]
v_pk_fma_f32 v[58:59],v[122:123],v[48:49],v[50:51]
v_pk_fma_f32 v[116:117],v[52:53],v[84:85],v[116:117]
v_pk_fma_f32 v[118:119],v[54:55],v[86:87],v[118:119]
v_pk_fma_f32 v[120:121],v[56:57],v[88:89],v[120:121]
v_pk_fma_f32 v[122:123],v[58:59],v[90:91],v[122:123]
v_cvt_pk_bf16_f32 v148,v116,v117
v_cvt_pk_bf16_f32 v149,v118,v119
v_cvt_pk_bf16_f32 v150,v120,v121
v_cvt_pk_bf16_f32 v151,v122,v123
v_pk_fma_f32 v[52:53],v[124:125],v[48:49],v[50:51]
v_pk_fma_f32 v[54:55],v[126:127],v[48:49],v[50:51]
v_pk_fma_f32 v[56:57],v[128:129],v[48:49],v[50:51]
v_pk_fma_f32 v[58:59],v[130:131],v[48:49],v[50:51]
v_pk_fma_f32 v[124:125],v[52:53],v[92:93],v[124:125]
v_pk_fma_f32 v[126:127],v[54:55],v[94:95],v[126:127]
v_pk_fma_f32 v[128:129],v[56:57],v[96:97],v[128:129]
v_pk_fma_f32 v[130:131],v[58:59],v[98:99],v[130:131]
v_cvt_pk_bf16_f32 v152,v124,v125
v_cvt_pk_bf16_f32 v153,v126,v127
v_cvt_pk_bf16_f32 v154,v128,v129
v_cvt_pk_bf16_f32 v155,v130,v131
v_pk_fma_f32 v[52:53],v[132:133],v[48:49],v[50:51]
v_pk_fma_f32 v[54:55],v[134:135],v[48:49],v[50:51]
v_pk_fma_f32 v[56:57],v[136:137],v[48:49],v[50:51]
v_pk_fma_f32 v[58:59],v[138:139],v[48:49],v[50:51]
v_pk_fma_f32 v[132:133],v[52:53],v[100:101],v[132:133]
v_pk_fma_f32 v[134:135],v[54:55],v[102:103],v[134:135]
v_pk_fma_f32 v[136:137],v[56:57],v[104:105],v[136:137]
v_pk_fma_f32 v[138:139],v[58:59],v[106:107],v[138:139]
v_cvt_pk_bf16_f32 v156,v132,v133
v_cvt_pk_bf16_f32 v157,v134,v135
v_cvt_pk_bf16_f32 v158,v136,v137
v_cvt_pk_bf16_f32 v159,v138,v139
v_pk_fma_f32 v[52:53],v[140:141],v[48:49],v[50:51]
v_pk_fma_f32 v[54:55],v[142:143],v[48:49],v[50:51]
v_pk_fma_f32 v[56:57],v[144:145],v[48:49],v[50:51]
v_pk_fma_f32 v[58:59],v[146:147],v[48:49],v[50:51]
v_pk_fma_f32 v[140:141],v[52:53],v[108:109],v[140:141]
v_pk_fma_f32 v[142:143],v[54:55],v[110:111],v[142:143]
v_pk_fma_f32 v[144:145],v[56:57],v[112:113],v[144:145]
v_pk_fma_f32 v[146:147],v[58:59],v[114:115],v[146:147]
v_cvt_pk_bf16_f32 v160,v140,v141
v_cvt_pk_bf16_f32 v161,v142,v143
v_cvt_pk_bf16_f32 v162,v144,v145
v_cvt_pk_bf16_f32 v163,v146,v147
v_mfma_f32_16x16x32_bf16 v[36:39],v[32:35],v[148:151],0
buffer_store_dwordx4 v[148:151],v1,s[36:39],s57 offen offset:0 sc0 sc1
v_mfma_f32_16x16x32_bf16 v[40:43],v[148:151],v[148:151],0
M 36,v[32:35],152
buffer_store_dwordx4 v[152:155],v1,s[36:39],s57 offen offset:2048 sc0 sc1
M 40,v[152:155],152
M 36,v[32:35],156
buffer_store_dwordx4 v[156:159],v1,s[36:39],s58 offen offset:0 sc0 sc1
M 40,v[156:159],156
M 36,v[32:35],160
buffer_store_dwordx4 v[160:163],v1,s[36:39],s58 offen offset:2048 sc0 sc1
M 40,v[160:163],160
s_nop 5
v_add_f32_dpp v44,v36,v36 row_ror:8 row_mask:0xf bank_mask:0xf
s_nop 0
v_cndmask_b32 v45,v40,v41,s[70:71]
v_cndmask_b32 v46,v42,v43,s[70:71]
ds_write_b32 v2,v44 offset:1024
v_cndmask_b32 v45,v45,v46,s[72:73]
ds_write_b32 v0,v45 offset:1024
L 0
s_barrier
R 52,3,1024
R 56,3,1040
R 60,3,1056
L 0
v_pk_add_f32 v[52:53],v[52:53],v[54:55]
v_pk_add_f32 v[56:57],v[56:57],v[58:59]
v_pk_add_f32 v[60:61],v[60:61],v[62:63]
v_add_f32 v52,v52,v53
v_pk_add_f32 v[56:57],v[56:57],v[60:61]
v_mul_f32 v52,s68,v52
v_add_f32 v53,v56,v57
v_mul_f32 v53,s68,v53
v_fma_f32 v53,-v52,v52,v53
v_max_f32 v53,0,v53
v_add_f32 v54,s69,v53
v_fma_f32 v55,s69,v54,v53
v_rsq_f32 v65,v54
v_rsq_f32 v54,v55
v_mul_f32_e64 v64,-v52,v65
v_mul_f32 v63,v55,v54
v_mul_f32 v61,s76,v54
v_mul_f32 v55,v54,v54
v_mul_f32 v62,s77,v55
v_mul_f32 v62,v62,v61
v_cndmask_b32 v60,v52,v64,s[74:75]
v_cndmask_b32 v61,v61,v65,s[74:75]
buffer_store_dwordx4 v[60:63],v4,s[36:39],s59 offen offset:0 sc0 sc1
s_mov_b32 s63,s60
s_waitcnt vmcnt(5)
v_cmp_eq_u32 vcc,s53,v18
s_nop 0
s_cmp_eq_u64 vcc,-1
s_cselect_b32 s78,s45,256
s_cmp_lt_u32 s78,256
s_cbranch_scc1 Lr_tc0_%=
s_waitcnt vmcnt(0)
buffer_store_dword v17,v5,s[36:39],s63 offen offset:0 sc0 sc1
s_mov_b32 s63,0x7fff0000
s_cmp_ge_u32 s45,256
s_cbranch_scc1 Lr_done_%=
Lr_bp0_%=:
buffer_load_dword v18,v6,s[36:39],s61 offen offset:0 sc1
s_add_u32 s82,s82,1
s_waitcnt vmcnt(0)
v_cmp_eq_u32 vcc,s53,v18
s_nop 0
s_cmp_eq_u64 vcc,-1
s_cbranch_scc1 Lr_bq0_%=
s_sleep 2
s_cmp_lt_u32 s82,20000
s_cbranch_scc1 Lr_bp0_%=
Lr_bq0_%=:
s_add_u32 s78,s55,71303168
buffer_load_dwordx4 v[196:199],v1,s[36:39],s78 offen offset:0 sc1
buffer_load_dwordx4 v[200:203],v1,s[36:39],s78 offen offset:2048 sc1
s_add_u32 s78,s55,71307264
buffer_load_dwordx4 v[204:207],v1,s[36:39],s78 offen offset:0 sc1
buffer_load_dwordx4 v[208:211],v1,s[36:39],s78 offen offset:2048 sc1
s_add_u32 s78,s55,89128960
buffer_load_dwordx4 v[212:215],v1,s[36:39],s78 offen offset:0 sc1
buffer_load_dwordx4 v[216:219],v1,s[36:39],s78 offen offset:2048 sc1
s_add_u32 s78,s55,89133056
buffer_load_dwordx4 v[220:223],v1,s[36:39],s78 offen offset:0 sc1
buffer_load_dwordx4 v[224:227],v1,s[36:39],s78 offen offset:2048 sc1
buffer_load_dword v18,v6,s[36:39],s62 offen offset:0 sc1
s_lshr_b32 s78,s45,1
s_and_b32 s79,s45,1
s_lshl_b32 s79,s79,7
s_lshl_b32 s57,s78,15
s_add_u32 s57,s57,s79
s_add_u32 s57,s57,12582912
s_add_u32 s58,s57,4096
s_lshl_b32 s59,s78,9
s_add_u32 s59,s59,s79
s_add_u32 s59,s59,107020288
s_lshl_b32 s60,s45,9
s_add_u32 s60,s60,s66
s_add_u32 s60,s60,131072
s_add_u32 s45,s45,s46
s_lshr_b32 s78,s45,1
s_and_b32 s79,s45,1
s_lshl_b32 s79,s79,7
s_lshl_b32 s55,s78,15
s_add_u32 s55,s55,s79
s_mul_i32 s56,s45,16384
s_add_u32 s56,s56,98566144
s_lshl_b32 s61,s78,11
s_add_u32 s61,s61,110587904
s_add_u32 s78,s45,s46
s_lshr_b32 s78,s78,1
s_lshl_b32 s62,s78,11
s_add_u32 s62,s62,110587904
)"
R"(s_waitcnt vmcnt(0)
s_branch Lr_it1_%=
Lr_ta0_%=:
v_mfma_f32_16x16x32_bf16 v[116:119],v[26:29],v[164:167],0
s_add_u32 s78,s55,71303168
L 6
L 4
v_mfma_f32_16x16x32_bf16 v[120:123],v[24:27],v[164:167],0
L 2
buffer_load_dwordx4 v[196:199],v1,s[36:39],s78 offen offset:0 sc1
M 116,v[26:29],180
L 0
buffer_load_dwordx4 v[200:203],v1,s[36:39],s78 offen offset:2048 sc1
M 120,v[24:27],180
s_add_u32 s78,s55,71307264
v_lshlrev_b32 v84,16,v68
v_and_b32 v85,0xffff0000,v68
v_mfma_f32_16x16x32_bf16 v[124:127],v[26:29],v[168:171],0
buffer_load_dwordx4 v[204:207],v1,s[36:39],s78 offen offset:0 sc1
v_mfma_f32_16x16x32_bf16 v[128:131],v[24:27],v[168:171],0
buffer_load_dwordx4 v[208:211],v1,s[36:39],s78 offen offset:2048 sc1
M 124,v[26:29],184
s_add_u32 s78,s55,89128960
v_cvt_pk_bf16_f32 v148,v116,v117
v_cvt_pk_bf16_f32 v149,v118,v119
M 128,v[24:27],184
buffer_load_dwordx4 v[212:215],v1,s[36:39],s78 offen offset:0 sc1
v_mfma_f32_16x16x32_bf16 v[132:135],v[26:29],v[172:175],0
buffer_load_dwordx4 v[216:219],v1,s[36:39],s78 offen offset:2048 sc1
v_mfma_f32_16x16x32_bf16 v[136:139],v[24:27],v[172:175],0
s_add_u32 s78,s55,89133056
v_cvt_pk_bf16_f32 v150,v120,v121
v_cvt_pk_bf16_f32 v151,v122,v123
M 132,v[26:29],188
buffer_load_dwordx4 v[220:223],v1,s[36:39],s78 offen offset:0 sc1
M 136,v[24:27],188
buffer_load_dwordx4 v[224:227],v1,s[36:39],s78 offen offset:2048 sc1
v_mfma_f32_16x16x32_bf16 v[140:143],v[26:29],v[176:179],0
s_add_u32 m0,s65,16384
v_cvt_pk_bf16_f32 v152,v124,v125
v_cvt_pk_bf16_f32 v153,v126,v127
v_mfma_f32_16x16x32_bf16 v[144:147],v[24:27],v[176:179],0
D 9,56,0
M 140,v[26:29],192
D 9,56,1024
M 144,v[24:27],192
D 9,56,2048
v_mfma_f32_16x16x32_bf16 v[36:39],v[32:35],v[148:151],0
v_cvt_pk_bf16_f32 v154,v128,v129
v_cvt_pk_bf16_f32 v155,v130,v131
v_mfma_f32_16x16x32_bf16 v[40:43],v[148:151],v[148:151],0
D 9,56,3072
M 36,v[32:35],152
v_cvt_pk_bf16_f32 v156,v132,v133
v_cvt_pk_bf16_f32 v157,v134,v135
v_cvt_pk_bf16_f32 v158,v136,v137
v_cvt_pk_bf16_f32 v159,v138,v139
M 40,v[152:155],152
v_cvt_pk_bf16_f32 v160,v140,v141
v_cvt_pk_bf16_f32 v161,v142,v143
M 36,v[32:35],156
v_cvt_pk_bf16_f32 v162,v144,v145
v_cvt_pk_bf16_f32 v163,v146,v147
M 40,v[156:159],156
v_lshlrev_b32 v86,16,v69
v_and_b32 v87,0xffff0000,v69
M 36,v[32:35],160
v_lshlrev_b32 v88,16,v70
v_and_b32 v89,0xffff0000,v70
M 40,v[160:163],160
v_lshlrev_b32 v90,16,v71
v_and_b32 v91,0xffff0000,v71
v_lshlrev_b32 v92,16,v72
v_and_b32 v93,0xffff0000,v72
v_lshlrev_b32 v94,16,v73
v_and_b32 v95,0xffff0000,v73
v_lshlrev_b32 v96,16,v74
v_and_b32 v97,0xffff0000,v74
v_lshlrev_b32 v98,16,v75
v_and_b32 v99,0xffff0000,v75
v_lshlrev_b32 v100,16,v76
v_and_b32 v101,0xffff0000,v76
v_lshlrev_b32 v102,16,v77
v_and_b32 v103,0xffff0000,v77
v_lshlrev_b32 v104,16,v78
v_and_b32 v105,0xffff0000,v78
v_lshlrev_b32 v106,16,v79
v_and_b32 v107,0xffff0000,v79
v_lshlrev_b32 v108,16,v80
v_and_b32 v109,0xffff0000,v80
v_lshlrev_b32 v110,16,v81
v_and_b32 v111,0xffff0000,v81
v_lshlrev_b32 v112,16,v82
v_and_b32 v113,0xffff0000,v82
v_lshlrev_b32 v114,16,v83
v_and_b32 v115,0xffff0000,v83
v_add_f32_dpp v44,v36,v36 row_ror:8 row_mask:0xf bank_mask:0xf
v_cndmask_b32 v45,v40,v41,s[70:71]
v_cndmask_b32 v46,v42,v43,s[70:71]
ds_write_b32 v2,v44 offset:0
v_cndmask_b32 v45,v45,v46,s[72:73]
ds_write_b32 v0,v45 offset:0
L 0
s_barrier
R 52,3,0
R 56,3,16
R 60,3,32
L 0
v_pk_add_f32 v[52:53],v[52:53],v[54:55]
v_pk_add_f32 v[56:57],v[56:57],v[58:59]
v_pk_add_f32 v[60:61],v[60:61],v[62:63]
v_add_f32 v52,v52,v53
v_pk_add_f32 v[56:57],v[56:57],v[60:61]
v_mul_f32 v52,s68,v52
v_add_f32 v53,v56,v57
v_mul_f32 v53,s68,v53
v_fma_f32 v53,-v52,v52,v53
v_max_f32 v53,0,v53
buffer_load_dword v18,v6,s[36:39],s62 offen offset:0 sc1
v_add_f32 v53,s69,v53
v_rsq_f32 v48,v53
s_nop 0
v_mul_f32_e64 v50,-v52,v48
v_mov_b32 v49,v48
v_mov_b32 v51,v50
v_pk_fma_f32 v[52:53],v[116:117],v[48:49],v[50:51]
v_pk_fma_f32 v[54:55],v[118:119],v[48:49],v[50:51]
v_pk_fma_f32 v[56:57],v[120:121],v[48:49],v[50:51]
v_pk_fma_f32 v[58:59],v[122:123],v[48:49],v[50:51]
v_pk_fma_f32 v[116:117],v[52:53],v[84:85],v[116:117]
v_pk_fma_f32 v[118:119],v[54:55],v[86:87],v[118:119]
v_pk_fma_f32 v[120:121],v[56:57],v[88:89],v[120:121]
v_pk_fma_f32 v[122:123],v[58:59],v[90:91],v[122:123]
v_cvt_pk_bf16_f32 v148,v116,v117
v_cvt_pk_bf16_f32 v149,v118,v119
v_cvt_pk_bf16_f32 v150,v120,v121
v_cvt_pk_bf16_f32 v151,v122,v123
v_pk_fma_f32 v[52:53],v[124:125],v[48:49],v[50:51]
v_pk_fma_f32 v[54:55],v[126:127],v[48:49],v[50:51]
v_pk_fma_f32 v[56:57],v[128:129],v[48:49],v[50:51]
v_pk_fma_f32 v[58:59],v[130:131],v[48:49],v[50:51]
v_pk_fma_f32 v[124:125],v[52:53],v[92:93],v[124:125]
v_pk_fma_f32 v[126:127],v[54:55],v[94:95],v[126:127]
v_pk_fma_f32 v[128:129],v[56:57],v[96:97],v[128:129]
v_pk_fma_f32 v[130:131],v[58:59],v[98:99],v[130:131]
v_cvt_pk_bf16_f32 v152,v124,v125
v_cvt_pk_bf16_f32 v153,v126,v127
v_cvt_pk_bf16_f32 v154,v128,v129
v_cvt_pk_bf16_f32 v155,v130,v131
v_pk_fma_f32 v[52:53],v[132:133],v[48:49],v[50:51]
v_pk_fma_f32 v[54:55],v[134:135],v[48:49],v[50:51]
v_pk_fma_f32 v[56:57],v[136:137],v[48:49],v[50:51]
v_pk_fma_f32 v[58:59],v[138:139],v[48:49],v[50:51]
v_pk_fma_f32 v[132:133],v[52:53],v[100:101],v[132:133]
v_pk_fma_f32 v[134:135],v[54:55],v[102:103],v[134:135]
v_pk_fma_f32 v[136:137],v[56:57],v[104:105],v[136:137]
v_pk_fma_f32 v[138:139],v[58:59],v[106:107],v[138:139]
v_cvt_pk_bf16_f32 v156,v132,v133
v_cvt_pk_bf16_f32 v157,v134,v135
v_cvt_pk_bf16_f32 v158,v136,v137
v_cvt_pk_bf16_f32 v159,v138,v139
v_pk_fma_f32 v[52:53],v[140:141],v[48:49],v[50:51]
v_pk_fma_f32 v[54:55],v[142:143],v[48:49],v[50:51]
v_pk_fma_f32 v[56:57],v[144:145],v[48:49],v[50:51]
v_pk_fma_f32 v[58:59],v[146:147],v[48:49],v[50:51]
v_pk_fma_f32 v[140:141],v[52:53],v[108:109],v[140:141]
v_pk_fma_f32 v[142:143],v[54:55],v[110:111],v[142:143]
v_pk_fma_f32 v[144:145],v[56:57],v[112:113],v[144:145]
v_pk_fma_f32 v[146:147],v[58:59],v[114:115],v[146:147]
v_cvt_pk_bf16_f32 v160,v140,v141
v_cvt_pk_bf16_f32 v161,v142,v143
v_cvt_pk_bf16_f32 v162,v144,v145
v_cvt_pk_bf16_f32 v163,v146,v147
s_waitcnt vmcnt(13)
buffer_store_dword v17,v5,s[36:39],s63 offen offset:0 sc0 sc1
s_mov_b32 s63,0x7fff0000
v_mfma_f32_16x16x32_bf16 v[36:39],v[32:35],v[148:151],0
buffer_store_dwordx4 v[148:151],v1,s[36:39],s57 offen offset:0 sc0 sc1
v_mfma_f32_16x16x32_bf16 v[40:43],v[148:151],v[148:151],0
M 36,v[32:35],152
buffer_store_dwordx4 v[152:155],v1,s[36:39],s57 offen offset:2048 sc0 sc1
M 40,v[152:155],152
M 36,v[32:35],156
buffer_store_dwordx4 v[156:159],v1,s[36:39],s58 offen offset:0 sc0 sc1
M 40,v[156:159],156
M 36,v[32:35],160
buffer_store_dwordx4 v[160:163],v1,s[36:39],s58 offen offset:2048 sc0 sc1
M 40,v[160:163],160
s_nop 5
v_add_f32_dpp v44,v36,v36 row_ror:8 row_mask:0xf bank_mask:0xf
s_nop 0
v_cndmask_b32 v45,v40,v41,s[70:71]
v_cndmask_b32 v46,v42,v43,s[70:71]
ds_write_b32 v2,v44 offset:1024
v_cndmask_b32 v45,v45,v46,s[72:73]
ds_write_b32 v0,v45 offset:1024
L 0
s_barrier
R 52,3,1024
R 56,3,1040
R 60,3,1056
L 0
v_pk_add_f32 v[52:53],v[52:53],v[54:55]
v_pk_add_f32 v[56:57],v[56:57],v[58:59]
v_pk_add_f32 v[60:61],v[60:61],v[62:63]
v_add_f32 v52,v52,v53
v_pk_add_f32 v[56:57],v[56:57],v[60:61]
v_mul_f32 v52,s68,v52
v_add_f32 v53,v56,v57
v_mul_f32 v53,s68,v53
v_fma_f32 v53,-v52,v52,v53
v_max_f32 v53,0,v53
v_add_f32 v54,s69,v53
v_fma_f32 v55,s69,v54,v53
v_rsq_f32 v65,v54
v_rsq_f32 v54,v55
v_mul_f32_e64 v64,-v52,v65
v_mul_f32 v63,v55,v54
v_mul_f32 v61,s76,v54
v_mul_f32 v55,v54,v54
v_mul_f32 v62,s77,v55
v_mul_f32 v62,v62,v61
v_cndmask_b32 v60,v52,v64,s[74:75]
v_cndmask_b32 v61,v61,v65,s[74:75]
buffer_store_dwordx4 v[60:63],v4,s[36:39],s59 offen offset:0 sc0 sc1
s_mov_b32 s63,s60
s_lshr_b32 s78,s45,1
s_and_b32 s79,s45,1
s_lshl_b32 s79,s79,7
s_lshl_b32 s57,s78,15
s_add_u32 s57,s57,s79
s_add_u32 s57,s57,12582912
s_add_u32 s58,s57,4096
s_lshl_b32 s59,s78,9
s_add_u32 s59,s59,s79
s_add_u32 s59,s59,107020288
s_lshl_b32 s60,s45,9
s_add_u32 s60,s60,s66
s_add_u32 s60,s60,131072
s_add_u32 s45,s45,s46
s_lshr_b32 s78,s45,1
s_and_b32 s79,s45,1
s_lshl_b32 s79,s79,7
s_lshl_b32 s55,s78,15
s_add_u32 s55,s55,s79
s_mul_i32 s56,s45,16384
s_add_u32 s56,s56,98566144
s_lshl_b32 s61,s78,11
s_add_u32 s61,s61,110587904
s_add_u32 s78,s45,s46
s_lshr_b32 s78,s78,1
s_lshl_b32 s62,s78,11
s_add_u32 s62,s62,110587904
s_waitcnt vmcnt(6)
s_branch Lr_it1_%=
Lr_tb0_%=:
v_add_f32 v53,s69,v53
v_rsq_f32 v48,v53
s_nop 0
v_mul_f32_e64 v50,-v52,v48
v_mov_b32 v49,v48
v_mov_b32 v51,v50
v_pk_fma_f32 v[52:53],v[116:117],v[48:49],v[50:51]
v_pk_fma_f32 v[54:55],v[118:119],v[48:49],v[50:51]
v_pk_fma_f32 v[56:57],v[120:121],v[48:49],v[50:51]
v_pk_fma_f32 v[58:59],v[122:123],v[48:49],v[50:51]
s_add_u32 s78,s55,71303168
buffer_load_dwordx4 v[196:199],v1,s[36:39],s78 offen offset:0 sc1
buffer_load_dwordx4 v[200:203],v1,s[36:39],s78 offen offset:2048 sc1
s_add_u32 s78,s55,71307264
buffer_load_dwordx4 v[204:207],v1,s[36:39],s78 offen offset:0 sc1
buffer_load_dwordx4 v[208:211],v1,s[36:39],s78 offen offset:2048 sc1
v_pk_fma_f32 v[116:117],v[52:53],v[84:85],v[116:117]
v_pk_fma_f32 v[118:119],v[54:55],v[86:87],v[118:119]
v_pk_fma_f32 v[120:121],v[56:57],v[88:89],v[120:121]
v_pk_fma_f32 v[122:123],v[58:59],v[90:91],v[122:123]
v_cvt_pk_bf16_f32 v148,v116,v117
v_cvt_pk_bf16_f32 v149,v118,v119
v_cvt_pk_bf16_f32 v150,v120,v121
v_cvt_pk_bf16_f32 v151,v122,v123
v_pk_fma_f32 v[52:53],v[124:125],v[48:49],v[50:51]
v_pk_fma_f32 v[54:55],v[126:127],v[48:49],v[50:51]
v_pk_fma_f32 v[56:57],v[128:129],v[48:49],v[50:51]
v_pk_fma_f32 v[58:59],v[130:131],v[48:49],v[50:51]
s_add_u32 s78,s55,89128960
buffer_load_dwordx4 v[212:215],v1,s[36:39],s78 offen offset:0 sc1
buffer_load_dwordx4 v[216:219],v1,s[36:39],s78 offen offset:2048 sc1
s_add_u32 s78,s55,89133056
buffer_load_dwordx4 v[220:223],v1,s[36:39],s78 offen offset:0 sc1
buffer_load_dwordx4 v[224:227],v1,s[36:39],s78 offen offset:2048 sc1
v_pk_fma_f32 v[124:125],v[52:53],v[92:93],v[124:125]
v_pk_fma_f32 v[126:127],v[54:55],v[94:95],v[126:127]
v_pk_fma_f32 v[128:129],v[56:57],v[96:97],v[128:129]
v_pk_fma_f32 v[130:131],v[58:59],v[98:99],v[130:131]
v_cvt_pk_bf16_f32 v152,v124,v125
v_cvt_pk_bf16_f32 v153,v126,v127
v_cvt_pk_bf16_f32 v154,v128,v129
v_cvt_pk_bf16_f32 v155,v130,v131
v_pk_fma_f32 v[52:53],v[132:133],v[48:49],v[50:51]
v_pk_fma_f32 v[54:55],v[134:135],v[48:49],v[50:51]
v_pk_fma_f32 v[56:57],v[136:137],v[48:49],v[50:51]
v_pk_fma_f32 v[58:59],v[138:139],v[48:49],v[50:51]
v_pk_fma_f32 v[132:133],v[52:53],v[100:101],v[132:133]
v_pk_fma_f32 v[134:135],v[54:55],v[102:103],v[134:135]
v_pk_fma_f32 v[136:137],v[56:57],v[104:105],v[136:137]
v_pk_fma_f32 v[138:139],v[58:59],v[106:107],v[138:139]
v_cvt_pk_bf16_f32 v156,v132,v133
v_cvt_pk_bf16_f32 v157,v134,v135
v_cvt_pk_bf16_f32 v158,v136,v137
v_cvt_pk_bf16_f32 v159,v138,v139
v_pk_fma_f32 v[52:53],v[140:141],v[48:49],v[50:51]
v_pk_fma_f32 v[54:55],v[142:143],v[48:49],v[50:51]
v_pk_fma_f32 v[56:57],v[144:145],v[48:49],v[50:51]
v_pk_fma_f32 v[58:59],v[146:147],v[48:49],v[50:51]
v_pk_fma_f32 v[140:141],v[52:53],v[108:109],v[140:141]
v_pk_fma_f32 v[142:143],v[54:55],v[110:111],v[142:143]
v_pk_fma_f32 v[144:145],v[56:57],v[112:113],v[144:145]
v_pk_fma_f32 v[146:147],v[58:59],v[114:115],v[146:147]
v_cvt_pk_bf16_f32 v160,v140,v141
v_cvt_pk_bf16_f32 v161,v142,v143
v_cvt_pk_bf16_f32 v162,v144,v145
v_cvt_pk_bf16_f32 v163,v146,v147
v_mfma_f32_16x16x32_bf16 v[36:39],v[32:35],v[148:151],0
buffer_load_dword v18,v6,s[36:39],s62 offen offset:0 sc1
buffer_store_dwordx4 v[148:151],v1,s[36:39],s57 offen offset:0 sc0 sc1
v_mfma_f32_16x16x32_bf16 v[40:43],v[148:151],v[148:151],0
M 36,v[32:35],152
buffer_store_dwordx4 v[152:155],v1,s[36:39],s57 offen offset:2048 sc0 sc1
M 40,v[152:155],152
M 36,v[32:35],156
buffer_store_dwordx4 v[156:159],v1,s[36:39],s58 offen offset:0 sc0 sc1
M 40,v[156:159],156
M 36,v[32:35],160
buffer_store_dwordx4 v[160:163],v1,s[36:39],s58 offen offset:2048 sc0 sc1
M 40,v[160:163],160
s_nop 5
v_add_f32_dpp v44,v36,v36 row_ror:8 row_mask:0xf bank_mask:0xf
s_nop 0
v_cndmask_b32 v45,v40,v41,s[70:71]
v_cndmask_b32 v46,v42,v43,s[70:71]
ds_write_b32 v2,v44 offset:1024
v_cndmask_b32 v45,v45,v46,s[72:73]
ds_write_b32 v0,v45 offset:1024
L 0
s_barrier
R 52,3,1024
R 56,3,1040
R 60,3,1056
L 0
v_pk_add_f32 v[52:53],v[52:53],v[54:55]
v_pk_add_f32 v[56:57],v[56:57],v[58:59]
v_pk_add_f32 v[60:61],v[60:61],v[62:63]
v_add_f32 v52,v52,v53
v_pk_add_f32 v[56:57],v[56:57],v[60:61]
v_mul_f32 v52,s68,v52
v_add_f32 v53,v56,v57
v_mul_f32 v53,s68,v53
v_fma_f32 v53,-v52,v52,v53
v_max_f32 v53,0,v53
v_add_f32 v54,s69,v53
v_fma_f32 v55,s69,v54,v53
v_rsq_f32 v65,v54
v_rsq_f32 v54,v55
v_mul_f32_e64 v64,-v52,v65
v_mul_f32 v63,v55,v54
v_mul_f32 v61,s76,v54
v_mul_f32 v55,v54,v54
v_mul_f32 v62,s77,v55
v_mul_f32 v62,v62,v61
v_cndmask_b32 v60,v52,v64,s[74:75]
v_cndmask_b32 v61,v61,v65,s[74:75]
buffer_store_dwordx4 v[60:63],v4,s[36:39],s59 offen offset:0 sc0 sc1
s_mov_b32 s63,s60
s_lshr_b32 s78,s45,1
s_and_b32 s79,s45,1
s_lshl_b32 s79,s79,7
s_lshl_b32 s57,s78,15
s_add_u32 s57,s57,s79
s_add_u32 s57,s57,12582912
s_add_u32 s58,s57,4096
s_lshl_b32 s59,s78,9
s_add_u32 s59,s59,s79
s_add_u32 s59,s59,107020288
s_lshl_b32 s60,s45,9
s_add_u32 s60,s60,s66
s_add_u32 s60,s60,131072
s_add_u32 s45,s45,s46
s_lshr_b32 s78,s45,1
s_and_b32 s79,s45,1
s_lshl_b32 s79,s79,7
s_lshl_b32 s55,s78,15
s_add_u32 s55,s55,s79
s_mul_i32 s56,s45,16384
s_add_u32 s56,s56,98566144
s_lshl_b32 s61,s78,11
s_add_u32 s61,s61,110587904
s_add_u32 s78,s45,s46
s_lshr_b32 s78,s78,1
s_lshl_b32 s62,s78,11
s_add_u32 s62,s62,110587904
s_waitcnt vmcnt(5)
s_branch Lr_it1_%=
Lr_tc0_%=:
s_add_u32 s78,s55,71303168
buffer_load_dwordx4 v[196:199],v1,s[36:39],s78 offen offset:0 sc1
buffer_load_dwordx4 v[200:203],v1,s[36:39],s78 offen offset:2048 sc1
s_add_u32 s78,s55,71307264
buffer_load_dwordx4 v[204:207],v1,s[36:39],s78 offen offset:0 sc1
buffer_load_dwordx4 v[208:211],v1,s[36:39],s78 offen offset:2048 sc1
s_add_u32 s78,s55,89128960
buffer_load_dwordx4 v[212:215],v1,s[36:39],s78 offen offset:0 sc1
buffer_load_dwordx4 v[216:219],v1,s[36:39],s78 offen offset:2048 sc1
s_add_u32 s78,s55,89133056
buffer_load_dwordx4 v[220:223],v1,s[36:39],s78 offen offset:0 sc1
buffer_load_dwordx4 v[224:227],v1,s[36:39],s78 offen offset:2048 sc1
buffer_load_dword v18,v6,s[36:39],s62 offen offset:0 sc1
s_waitcnt vmcnt(9)
buffer_store_dword v17,v5,s[36:39],s63 offen offset:0 sc0 sc1
s_mov_b32 s63,0x7fff0000
s_lshr_b32 s78,s45,1
s_and_b32 s79,s45,1
s_lshl_b32 s79,s79,7
s_lshl_b32 s57,s78,15
s_add_u32 s57,s57,s79
s_add_u32 s57,s57,12582912
s_add_u32 s58,s57,4096
s_lshl_b32 s59,s78,9
s_add_u32 s59,s59,s79
s_add_u32 s59,s59,107020288
s_lshl_b32 s60,s45,9
s_add_u32 s60,s60,s66
s_add_u32 s60,s60,131072
s_add_u32 s45,s45,s46
s_lshr_b32 s78,s45,1
s_and_b32 s79,s45,1
s_lshl_b32 s79,s79,7
s_lshl_b32 s55,s78,15
s_add_u32 s55,s55,s79
s_mul_i32 s56,s45,16384
s_add_u32 s56,s56,98566144
s_lshl_b32 s61,s78,11
s_add_u32 s61,s61,110587904
s_add_u32 s78,s45,s46
s_lshr_b32 s78,s78,1
s_lshl_b32 s62,s78,11
s_add_u32 s62,s62,110587904
s_waitcnt vmcnt(1)
s_branch Lr_it1_%=
Lr_it1_%=:
T v[68:69],10,16384
T v[70:71],10,16448
T v[72:73],10,17408
T v[74:75],10,17472
T v[76:77],10,18432
T v[78:79],10,18496
T v[80:81],10,19456
T v[82:83],10,19520
s_nop 0
v_cmp_eq_u32 vcc,s53,v18
s_nop 0
s_cmp_eq_u64 vcc,-1
s_cselect_b32 s78,s45,256
s_cmp_lt_u32 s78,256
s_cbranch_scc1 Lr_ta1_%=
buffer_load_dword v18,v6,s[36:39],s61 offen offset:0 sc1
v_mfma_f32_16x16x32_bf16 v[116:119],v[26:29],v[196:199],0
L 6
v_lshlrev_b32 v84,16,v68
v_and_b32 v85,0xffff0000,v68
v_mfma_f32_16x16x32_bf16 v[120:123],v[24:27],v[196:199],0
L 4
v_lshlrev_b32 v86,16,v69
v_and_b32 v87,0xffff0000,v69
M 116,v[26:29],212
L 2
v_lshlrev_b32 v88,16,v70
v_and_b32 v89,0xffff0000,v70
M 120,v[24:27],212
L 0
v_lshlrev_b32 v90,16,v71
v_and_b32 v91,0xffff0000,v71
v_mfma_f32_16x16x32_bf16 v[124:127],v[26:29],v[200:203],0
s_add_u32 m0,s65,0
v_lshlrev_b32 v92,16,v72
v_and_b32 v93,0xffff0000,v72
v_mfma_f32_16x16x32_bf16 v[128:131],v[24:27],v[200:203],0
D 9,56,0
M 124,v[26:29],216
D 9,56,1024
M 128,v[24:27],216
D 9,56,2048
v_mfma_f32_16x16x32_bf16 v[132:135],v[26:29],v[204:207],0
D 9,56,3072
v_mfma_f32_16x16x32_bf16 v[136:139],v[24:27],v[204:207],0
v_cvt_pk_bf16_f32 v148,v116,v117
v_cvt_pk_bf16_f32 v149,v118,v119
M 132,v[26:29],220
v_cvt_pk_bf16_f32 v150,v120,v121
v_cvt_pk_bf16_f32 v151,v122,v123
M 136,v[24:27],220
)"
R"(v_cvt_pk_bf16_f32 v152,v124,v125
v_cvt_pk_bf16_f32 v153,v126,v127
v_mfma_f32_16x16x32_bf16 v[140:143],v[26:29],v[208:211],0
v_cvt_pk_bf16_f32 v154,v128,v129
v_cvt_pk_bf16_f32 v155,v130,v131
v_mfma_f32_16x16x32_bf16 v[144:147],v[24:27],v[208:211],0
v_lshlrev_b32 v94,16,v73
v_and_b32 v95,0xffff0000,v73
M 140,v[26:29],224
v_cvt_pk_bf16_f32 v156,v132,v133
v_cvt_pk_bf16_f32 v157,v134,v135
M 144,v[24:27],224
v_cvt_pk_bf16_f32 v158,v136,v137
v_cvt_pk_bf16_f32 v159,v138,v139
v_mfma_f32_16x16x32_bf16 v[36:39],v[32:35],v[148:151],0
v_lshlrev_b32 v96,16,v74
v_and_b32 v97,0xffff0000,v74
v_mfma_f32_16x16x32_bf16 v[40:43],v[148:151],v[148:151],0
v_lshlrev_b32 v98,16,v75
v_and_b32 v99,0xffff0000,v75
M 36,v[32:35],152
v_cvt_pk_bf16_f32 v160,v140,v141
v_cvt_pk_bf16_f32 v161,v142,v143
M 40,v[152:155],152
v_cvt_pk_bf16_f32 v162,v144,v145
v_cvt_pk_bf16_f32 v163,v146,v147
M 36,v[32:35],156
v_lshlrev_b32 v100,16,v76
v_and_b32 v101,0xffff0000,v76
M 40,v[156:159],156
v_lshlrev_b32 v102,16,v77
v_and_b32 v103,0xffff0000,v77
M 36,v[32:35],160
v_lshlrev_b32 v104,16,v78
v_and_b32 v105,0xffff0000,v78
M 40,v[160:163],160
v_lshlrev_b32 v106,16,v79
v_and_b32 v107,0xffff0000,v79
v_lshlrev_b32 v108,16,v80
v_and_b32 v109,0xffff0000,v80
v_lshlrev_b32 v110,16,v81
v_and_b32 v111,0xffff0000,v81
v_lshlrev_b32 v112,16,v82
v_and_b32 v113,0xffff0000,v82
v_lshlrev_b32 v114,16,v83
v_and_b32 v115,0xffff0000,v83
v_add_f32_dpp v44,v36,v36 row_ror:8 row_mask:0xf bank_mask:0xf
v_cndmask_b32 v45,v40,v41,s[70:71]
v_cndmask_b32 v46,v42,v43,s[70:71]
ds_write_b32 v2,v44 offset:0
v_cndmask_b32 v45,v45,v46,s[72:73]
ds_write_b32 v0,v45 offset:0
L 0
s_barrier
R 52,3,0
R 56,3,16
R 60,3,32
L 0
v_pk_add_f32 v[52:53],v[52:53],v[54:55]
v_pk_add_f32 v[56:57],v[56:57],v[58:59]
v_pk_add_f32 v[60:61],v[60:61],v[62:63]
v_add_f32 v52,v52,v53
v_pk_add_f32 v[56:57],v[56:57],v[60:61]
v_mul_f32 v52,s68,v52
v_add_f32 v53,v56,v57
v_mul_f32 v53,s68,v53
v_fma_f32 v53,-v52,v52,v53
v_max_f32 v53,0,v53
s_waitcnt vmcnt(5)
buffer_store_dword v17,v5,s[36:39],s63 offen offset:0 sc0 sc1
s_mov_b32 s63,0x7fff0000
s_waitcnt vmcnt(5)
v_cmp_eq_u32 vcc,s53,v18
s_nop 0
s_cmp_eq_u64 vcc,-1
s_cselect_b32 s78,s45,256
s_cmp_lt_u32 s78,256
s_cbranch_scc1 Lr_tb1_%=
buffer_load_dword v18,v6,s[36:39],s61 offen offset:0 sc1
v_add_f32 v53,s69,v53
v_rsq_f32 v48,v53
s_nop 0
v_mul_f32_e64 v50,-v52,v48
v_mov_b32 v49,v48
v_mov_b32 v51,v50
v_pk_fma_f32 v[52:53],v[116:117],v[48:49],v[50:51]
v_pk_fma_f32 v[54:55],v[118:119],v[48:49],v[50:51]
v_pk_fma_f32 v[56:57],v[120:121],v[48:49],v[50:51]
v_pk_fma_f32 v[58:59],v[122:123],v[48:49],v[50:51]
v_pk_fma_f32 v[116:117],v[52:53],v[84:85],v[116:117]
v_pk_fma_f32 v[118:119],v[54:55],v[86:87],v[118:119]
v_pk_fma_f32 v[120:121],v[56:57],v[88:89],v[120:121]
v_pk_fma_f32 v[122:123],v[58:59],v[90:91],v[122:123]
v_cvt_pk_bf16_f32 v148,v116,v117
v_cvt_pk_bf16_f32 v149,v118,v119
v_cvt_pk_bf16_f32 v150,v120,v121
v_cvt_pk_bf16_f32 v151,v122,v123
v_pk_fma_f32 v[52:53],v[124:125],v[48:49],v[50:51]
v_pk_fma_f32 v[54:55],v[126:127],v[48:49],v[50:51]
v_pk_fma_f32 v[56:57],v[128:129],v[48:49],v[50:51]
v_pk_fma_f32 v[58:59],v[130:131],v[48:49],v[50:51]
v_pk_fma_f32 v[124:125],v[52:53],v[92:93],v[124:125]
v_pk_fma_f32 v[126:127],v[54:55],v[94:95],v[126:127]
v_pk_fma_f32 v[128:129],v[56:57],v[96:97],v[128:129]
v_pk_fma_f32 v[130:131],v[58:59],v[98:99],v[130:131]
v_cvt_pk_bf16_f32 v152,v124,v125
v_cvt_pk_bf16_f32 v153,v126,v127
v_cvt_pk_bf16_f32 v154,v128,v129
v_cvt_pk_bf16_f32 v155,v130,v131
v_pk_fma_f32 v[52:53],v[132:133],v[48:49],v[50:51]
v_pk_fma_f32 v[54:55],v[134:135],v[48:49],v[50:51]
v_pk_fma_f32 v[56:57],v[136:137],v[48:49],v[50:51]
v_pk_fma_f32 v[58:59],v[138:139],v[48:49],v[50:51]
v_pk_fma_f32 v[132:133],v[52:53],v[100:101],v[132:133]
v_pk_fma_f32 v[134:135],v[54:55],v[102:103],v[134:135]
v_pk_fma_f32 v[136:137],v[56:57],v[104:105],v[136:137]
v_pk_fma_f32 v[138:139],v[58:59],v[106:107],v[138:139]
v_cvt_pk_bf16_f32 v156,v132,v133
v_cvt_pk_bf16_f32 v157,v134,v135
v_cvt_pk_bf16_f32 v158,v136,v137
v_cvt_pk_bf16_f32 v159,v138,v139
v_pk_fma_f32 v[52:53],v[140:141],v[48:49],v[50:51]
v_pk_fma_f32 v[54:55],v[142:143],v[48:49],v[50:51]
v_pk_fma_f32 v[56:57],v[144:145],v[48:49],v[50:51]
v_pk_fma_f32 v[58:59],v[146:147],v[48:49],v[50:51]
v_pk_fma_f32 v[140:141],v[52:53],v[108:109],v[140:141]
v_pk_fma_f32 v[142:143],v[54:55],v[110:111],v[142:143]
v_pk_fma_f32 v[144:145],v[56:57],v[112:113],v[144:145]
v_pk_fma_f32 v[146:147],v[58:59],v[114:115],v[146:147]
v_cvt_pk_bf16_f32 v160,v140,v141
v_cvt_pk_bf16_f32 v161,v142,v143
v_cvt_pk_bf16_f32 v162,v144,v145
v_cvt_pk_bf16_f32 v163,v146,v147
v_mfma_f32_16x16x32_bf16 v[36:39],v[32:35],v[148:151],0
buffer_store_dwordx4 v[148:151],v1,s[36:39],s57 offen offset:0 sc0 sc1
v_mfma_f32_16x16x32_bf16 v[40:43],v[148:151],v[148:151],0
M 36,v[32:35],152
buffer_store_dwordx4 v[152:155],v1,s[36:39],s57 offen offset:2048 sc0 sc1
M 40,v[152:155],152
M 36,v[32:35],156
buffer_store_dwordx4 v[156:159],v1,s[36:39],s58 offen offset:0 sc0 sc1
M 40,v[156:159],156
M 36,v[32:35],160
buffer_store_dwordx4 v[160:163],v1,s[36:39],s58 offen offset:2048 sc0 sc1
M 40,v[160:163],160
s_nop 5
v_add_f32_dpp v44,v36,v36 row_ror:8 row_mask:0xf bank_mask:0xf
s_nop 0
v_cndmask_b32 v45,v40,v41,s[70:71]
v_cndmask_b32 v46,v42,v43,s[70:71]
ds_write_b32 v2,v44 offset:1024
v_cndmask_b32 v45,v45,v46,s[72:73]
ds_write_b32 v0,v45 offset:1024
L 0
s_barrier
R 52,3,1024
R 56,3,1040
R 60,3,1056
L 0
v_pk_add_f32 v[52:53],v[52:53],v[54:55]
v_pk_add_f32 v[56:57],v[56:57],v[58:59]
v_pk_add_f32 v[60:61],v[60:61],v[62:63]
v_add_f32 v52,v52,v53
v_pk_add_f32 v[56:57],v[56:57],v[60:61]
v_mul_f32 v52,s68,v52
v_add_f32 v53,v56,v57
v_mul_f32 v53,s68,v53
v_fma_f32 v53,-v52,v52,v53
v_max_f32 v53,0,v53
v_add_f32 v54,s69,v53
v_fma_f32 v55,s69,v54,v53
v_rsq_f32 v65,v54
v_rsq_f32 v54,v55
v_mul_f32_e64 v64,-v52,v65
v_mul_f32 v63,v55,v54
v_mul_f32 v61,s76,v54
v_mul_f32 v55,v54,v54
v_mul_f32 v62,s77,v55
v_mul_f32 v62,v62,v61
v_cndmask_b32 v60,v52,v64,s[74:75]
v_cndmask_b32 v61,v61,v65,s[74:75]
buffer_store_dwordx4 v[60:63],v4,s[36:39],s59 offen offset:0 sc0 sc1
s_mov_b32 s63,s60
s_waitcnt vmcnt(5)
v_cmp_eq_u32 vcc,s53,v18
s_nop 0
s_cmp_eq_u64 vcc,-1
s_cselect_b32 s78,s45,256
s_cmp_lt_u32 s78,256
s_cbranch_scc1 Lr_tc1_%=
s_waitcnt vmcnt(0)
buffer_store_dword v17,v5,s[36:39],s63 offen offset:0 sc0 sc1
s_mov_b32 s63,0x7fff0000
s_cmp_ge_u32 s45,256
s_cbranch_scc1 Lr_done_%=
Lr_bp1_%=:
buffer_load_dword v18,v6,s[36:39],s61 offen offset:0 sc1
s_add_u32 s82,s82,1
s_waitcnt vmcnt(0)
v_cmp_eq_u32 vcc,s53,v18
s_nop 0
s_cmp_eq_u64 vcc,-1
s_cbranch_scc1 Lr_bq1_%=
s_sleep 2
s_cmp_lt_u32 s82,20000
s_cbranch_scc1 Lr_bp1_%=
Lr_bq1_%=:
s_add_u32 s78,s55,71303168
buffer_load_dwordx4 v[164:167],v1,s[36:39],s78 offen offset:0 sc1
buffer_load_dwordx4 v[168:171],v1,s[36:39],s78 offen offset:2048 sc1
s_add_u32 s78,s55,71307264
buffer_load_dwordx4 v[172:175],v1,s[36:39],s78 offen offset:0 sc1
buffer_load_dwordx4 v[176:179],v1,s[36:39],s78 offen offset:2048 sc1
s_add_u32 s78,s55,89128960
buffer_load_dwordx4 v[180:183],v1,s[36:39],s78 offen offset:0 sc1
buffer_load_dwordx4 v[184:187],v1,s[36:39],s78 offen offset:2048 sc1
s_add_u32 s78,s55,89133056
buffer_load_dwordx4 v[188:191],v1,s[36:39],s78 offen offset:0 sc1
buffer_load_dwordx4 v[192:195],v1,s[36:39],s78 offen offset:2048 sc1
buffer_load_dword v18,v6,s[36:39],s62 offen offset:0 sc1
s_lshr_b32 s78,s45,1
s_and_b32 s79,s45,1
s_lshl_b32 s79,s79,7
s_lshl_b32 s57,s78,15
s_add_u32 s57,s57,s79
s_add_u32 s57,s57,12582912
s_add_u32 s58,s57,4096
s_lshl_b32 s59,s78,9
s_add_u32 s59,s59,s79
s_add_u32 s59,s59,107020288
s_lshl_b32 s60,s45,9
s_add_u32 s60,s60,s66
s_add_u32 s60,s60,131072
s_add_u32 s45,s45,s46
s_lshr_b32 s78,s45,1
s_and_b32 s79,s45,1
s_lshl_b32 s79,s79,7
s_lshl_b32 s55,s78,15
s_add_u32 s55,s55,s79
s_mul_i32 s56,s45,16384
s_add_u32 s56,s56,98566144
s_lshl_b32 s61,s78,11
s_add_u32 s61,s61,110587904
s_add_u32 s78,s45,s46
s_lshr_b32 s78,s78,1
s_lshl_b32 s62,s78,11
s_add_u32 s62,s62,110587904
s_waitcnt vmcnt(0)
s_branch Lr_it0_%=
Lr_ta1_%=:
v_mfma_f32_16x16x32_bf16 v[116:119],v[26:29],v[196:199],0
s_add_u32 s78,s55,71303168
L 6
L 4
v_mfma_f32_16x16x32_bf16 v[120:123],v[24:27],v[196:199],0
L 2
buffer_load_dwordx4 v[164:167],v1,s[36:39],s78 offen offset:0 sc1
M 116,v[26:29],212
L 0
buffer_load_dwordx4 v[168:171],v1,s[36:39],s78 offen offset:2048 sc1
M 120,v[24:27],212
s_add_u32 s78,s55,71307264
v_lshlrev_b32 v84,16,v68
v_and_b32 v85,0xffff0000,v68
v_mfma_f32_16x16x32_bf16 v[124:127],v[26:29],v[200:203],0
buffer_load_dwordx4 v[172:175],v1,s[36:39],s78 offen offset:0 sc1
v_mfma_f32_16x16x32_bf16 v[128:131],v[24:27],v[200:203],0
buffer_load_dwordx4 v[176:179],v1,s[36:39],s78 offen offset:2048 sc1
M 124,v[26:29],216
s_add_u32 s78,s55,89128960
v_cvt_pk_bf16_f32 v148,v116,v117
v_cvt_pk_bf16_f32 v149,v118,v119
M 128,v[24:27],216
buffer_load_dwordx4 v[180:183],v1,s[36:39],s78 offen offset:0 sc1
v_mfma_f32_16x16x32_bf16 v[132:135],v[26:29],v[204:207],0
buffer_load_dwordx4 v[184:187],v1,s[36:39],s78 offen offset:2048 sc1
v_mfma_f32_16x16x32_bf16 v[136:139],v[24:27],v[204:207],0
s_add_u32 s78,s55,89133056
v_cvt_pk_bf16_f32 v150,v120,v121
v_cvt_pk_bf16_f32 v151,v122,v123
M 132,v[26:29],220
buffer_load_dwordx4 v[188:191],v1,s[36:39],s78 offen offset:0 sc1
M 136,v[24:27],220
buffer_load_dwordx4 v[192:195],v1,s[36:39],s78 offen offset:2048 sc1
v_mfma_f32_16x16x32_bf16 v[140:143],v[26:29],v[208:211],0
s_add_u32 m0,s65,0
v_cvt_pk_bf16_f32 v152,v124,v125
v_cvt_pk_bf16_f32 v153,v126,v127
v_mfma_f32_16x16x32_bf16 v[144:147],v[24:27],v[208:211],0
D 9,56,0
M 140,v[26:29],224
D 9,56,1024
M 144,v[24:27],224
D 9,56,2048
v_mfma_f32_16x16x32_bf16 v[36:39],v[32:35],v[148:151],0
v_cvt_pk_bf16_f32 v154,v128,v129
v_cvt_pk_bf16_f32 v155,v130,v131
v_mfma_f32_16x16x32_bf16 v[40:43],v[148:151],v[148:151],0
D 9,56,3072
M 36,v[32:35],152
v_cvt_pk_bf16_f32 v156,v132,v133
v_cvt_pk_bf16_f32 v157,v134,v135
v_cvt_pk_bf16_f32 v158,v136,v137
v_cvt_pk_bf16_f32 v159,v138,v139
M 40,v[152:155],152
v_cvt_pk_bf16_f32 v160,v140,v141
v_cvt_pk_bf16_f32 v161,v142,v143
M 36,v[32:35],156
v_cvt_pk_bf16_f32 v162,v144,v145
v_cvt_pk_bf16_f32 v163,v146,v147
M 40,v[156:159],156
v_lshlrev_b32 v86,16,v69
v_and_b32 v87,0xffff0000,v69
M 36,v[32:35],160
v_lshlrev_b32 v88,16,v70
v_and_b32 v89,0xffff0000,v70
M 40,v[160:163],160
v_lshlrev_b32 v90,16,v71
v_and_b32 v91,0xffff0000,v71
v_lshlrev_b32 v92,16,v72
v_and_b32 v93,0xffff0000,v72
v_lshlrev_b32 v94,16,v73
v_and_b32 v95,0xffff0000,v73
v_lshlrev_b32 v96,16,v74
v_and_b32 v97,0xffff0000,v74
v_lshlrev_b32 v98,16,v75
v_and_b32 v99,0xffff0000,v75
v_lshlrev_b32 v100,16,v76
v_and_b32 v101,0xffff0000,v76
v_lshlrev_b32 v102,16,v77
v_and_b32 v103,0xffff0000,v77
v_lshlrev_b32 v104,16,v78
v_and_b32 v105,0xffff0000,v78
v_lshlrev_b32 v106,16,v79
v_and_b32 v107,0xffff0000,v79
v_lshlrev_b32 v108,16,v80
v_and_b32 v109,0xffff0000,v80
v_lshlrev_b32 v110,16,v81
v_and_b32 v111,0xffff0000,v81
v_lshlrev_b32 v112,16,v82
v_and_b32 v113,0xffff0000,v82
v_lshlrev_b32 v114,16,v83
v_and_b32 v115,0xffff0000,v83
v_add_f32_dpp v44,v36,v36 row_ror:8 row_mask:0xf bank_mask:0xf
v_cndmask_b32 v45,v40,v41,s[70:71]
v_cndmask_b32 v46,v42,v43,s[70:71]
ds_write_b32 v2,v44 offset:0
v_cndmask_b32 v45,v45,v46,s[72:73]
ds_write_b32 v0,v45 offset:0
L 0
s_barrier
R 52,3,0
R 56,3,16
R 60,3,32
L 0
v_pk_add_f32 v[52:53],v[52:53],v[54:55]
v_pk_add_f32 v[56:57],v[56:57],v[58:59]
v_pk_add_f32 v[60:61],v[60:61],v[62:63]
v_add_f32 v52,v52,v53
v_pk_add_f32 v[56:57],v[56:57],v[60:61]
v_mul_f32 v52,s68,v52
v_add_f32 v53,v56,v57
v_mul_f32 v53,s68,v53
v_fma_f32 v53,-v52,v52,v53
v_max_f32 v53,0,v53
buffer_load_dword v18,v6,s[36:39],s62 offen offset:0 sc1
v_add_f32 v53,s69,v53
v_rsq_f32 v48,v53
s_nop 0
v_mul_f32_e64 v50,-v52,v48
v_mov_b32 v49,v48
v_mov_b32 v51,v50
v_pk_fma_f32 v[52:53],v[116:117],v[48:49],v[50:51]
v_pk_fma_f32 v[54:55],v[118:119],v[48:49],v[50:51]
v_pk_fma_f32 v[56:57],v[120:121],v[48:49],v[50:51]
v_pk_fma_f32 v[58:59],v[122:123],v[48:49],v[50:51]
v_pk_fma_f32 v[116:117],v[52:53],v[84:85],v[116:117]
v_pk_fma_f32 v[118:119],v[54:55],v[86:87],v[118:119]
v_pk_fma_f32 v[120:121],v[56:57],v[88:89],v[120:121]
v_pk_fma_f32 v[122:123],v[58:59],v[90:91],v[122:123]
v_cvt_pk_bf16_f32 v148,v116,v117
v_cvt_pk_bf16_f32 v149,v118,v119
v_cvt_pk_bf16_f32 v150,v120,v121
v_cvt_pk_bf16_f32 v151,v122,v123
v_pk_fma_f32 v[52:53],v[124:125],v[48:49],v[50:51]
v_pk_fma_f32 v[54:55],v[126:127],v[48:49],v[50:51]
v_pk_fma_f32 v[56:57],v[128:129],v[48:49],v[50:51]
v_pk_fma_f32 v[58:59],v[130:131],v[48:49],v[50:51]
v_pk_fma_f32 v[124:125],v[52:53],v[92:93],v[124:125]
v_pk_fma_f32 v[126:127],v[54:55],v[94:95],v[126:127]
v_pk_fma_f32 v[128:129],v[56:57],v[96:97],v[128:129]
v_pk_fma_f32 v[130:131],v[58:59],v[98:99],v[130:131]
v_cvt_pk_bf16_f32 v152,v124,v125
v_cvt_pk_bf16_f32 v153,v126,v127
v_cvt_pk_bf16_f32 v154,v128,v129
v_cvt_pk_bf16_f32 v155,v130,v131
v_pk_fma_f32 v[52:53],v[132:133],v[48:49],v[50:51]
v_pk_fma_f32 v[54:55],v[134:135],v[48:49],v[50:51]
v_pk_fma_f32 v[56:57],v[136:137],v[48:49],v[50:51]
v_pk_fma_f32 v[58:59],v[138:139],v[48:49],v[50:51]
v_pk_fma_f32 v[132:133],v[52:53],v[100:101],v[132:133]
v_pk_fma_f32 v[134:135],v[54:55],v[102:103],v[134:135]
v_pk_fma_f32 v[136:137],v[56:57],v[104:105],v[136:137]
v_pk_fma_f32 v[138:139],v[58:59],v[106:107],v[138:139]
v_cvt_pk_bf16_f32 v156,v132,v133
v_cvt_pk_bf16_f32 v157,v134,v135
v_cvt_pk_bf16_f32 v158,v136,v137
v_cvt_pk_bf16_f32 v159,v138,v139
v_pk_fma_f32 v[52:53],v[140:141],v[48:49],v[50:51]
v_pk_fma_f32 v[54:55],v[142:143],v[48:49],v[50:51]
v_pk_fma_f32 v[56:57],v[144:145],v[48:49],v[50:51]
v_pk_fma_f32 v[58:59],v[146:147],v[48:49],v[50:51]
v_pk_fma_f32 v[140:141],v[52:53],v[108:109],v[140:141]
v_pk_fma_f32 v[142:143],v[54:55],v[110:111],v[142:143]
v_pk_fma_f32 v[144:145],v[56:57],v[112:113],v[144:145]
v_pk_fma_f32 v[146:147],v[58:59],v[114:115],v[146:147]
v_cvt_pk_bf16_f32 v160,v140,v141
v_cvt_pk_bf16_f32 v161,v142,v143
v_cvt_pk_bf16_f32 v162,v144,v145
v_cvt_pk_bf16_f32 v163,v146,v147
s_waitcnt vmcnt(13)
buffer_store_dword v17,v5,s[36:39],s63 offen offset:0 sc0 sc1
s_mov_b32 s63,0x7fff0000
v_mfma_f32_16x16x32_bf16 v[36:39],v[32:35],v[148:151],0
buffer_store_dwordx4 v[148:151],v1,s[36:39],s57 offen offset:0 sc0 sc1
v_mfma_f32_16x16x32_bf16 v[40:43],v[148:151],v[148:151],0
M 36,v[32:35],152
buffer_store_dwordx4 v[152:155],v1,s[36:39],s57 offen offset:2048 sc0 sc1
M 40,v[152:155],152
M 36,v[32:35],156
buffer_store_dwordx4 v[156:159],v1,s[36:39],s58 offen offset:0 sc0 sc1
M 40,v[156:159],156
M 36,v[32:35],160
buffer_store_dwordx4 v[160:163],v1,s[36:39],s58 offen offset:2048 sc0 sc1
M 40,v[160:163],160
s_nop 5
v_add_f32_dpp v44,v36,v36 row_ror:8 row_mask:0xf bank_mask:0xf
s_nop 0
v_cndmask_b32 v45,v40,v41,s[70:71]
v_cndmask_b32 v46,v42,v43,s[70:71]
ds_write_b32 v2,v44 offset:1024
v_cndmask_b32 v45,v45,v46,s[72:73]
ds_write_b32 v0,v45 offset:1024
L 0
s_barrier
R 52,3,1024
R 56,3,1040
R 60,3,1056
L 0
v_pk_add_f32 v[52:53],v[52:53],v[54:55]
v_pk_add_f32 v[56:57],v[56:57],v[58:59]
v_pk_add_f32 v[60:61],v[60:61],v[62:63]
v_add_f32 v52,v52,v53
v_pk_add_f32 v[56:57],v[56:57],v[60:61]
v_mul_f32 v52,s68,v52
v_add_f32 v53,v56,v57
v_mul_f32 v53,s68,v53
v_fma_f32 v53,-v52,v52,v53
v_max_f32 v53,0,v53
v_add_f32 v54,s69,v53
v_fma_f32 v55,s69,v54,v53
v_rsq_f32 v65,v54
v_rsq_f32 v54,v55
v_mul_f32_e64 v64,-v52,v65
v_mul_f32 v63,v55,v54
v_mul_f32 v61,s76,v54
v_mul_f32 v55,v54,v54
v_mul_f32 v62,s77,v55
v_mul_f32 v62,v62,v61
v_cndmask_b32 v60,v52,v64,s[74:75]
v_cndmask_b32 v61,v61,v65,s[74:75]
buffer_store_dwordx4 v[60:63],v4,s[36:39],s59 offen offset:0 sc0 sc1
s_mov_b32 s63,s60
s_lshr_b32 s78,s45,1
s_and_b32 s79,s45,1
s_lshl_b32 s79,s79,7
s_lshl_b32 s57,s78,15
s_add_u32 s57,s57,s79
s_add_u32 s57,s57,12582912
s_add_u32 s58,s57,4096
s_lshl_b32 s59,s78,9
s_add_u32 s59,s59,s79
s_add_u32 s59,s59,107020288
s_lshl_b32 s60,s45,9
s_add_u32 s60,s60,s66
s_add_u32 s60,s60,131072
s_add_u32 s45,s45,s46
s_lshr_b32 s78,s45,1
s_and_b32 s79,s45,1
s_lshl_b32 s79,s79,7
s_lshl_b32 s55,s78,15
s_add_u32 s55,s55,s79
s_mul_i32 s56,s45,16384
s_add_u32 s56,s56,98566144
s_lshl_b32 s61,s78,11
s_add_u32 s61,s61,110587904
s_add_u32 s78,s45,s46
s_lshr_b32 s78,s78,1
s_lshl_b32 s62,s78,11
s_add_u32 s62,s62,110587904
)"
R"(s_waitcnt vmcnt(6)
s_branch Lr_it0_%=
Lr_tb1_%=:
v_add_f32 v53,s69,v53
v_rsq_f32 v48,v53
s_nop 0
v_mul_f32_e64 v50,-v52,v48
v_mov_b32 v49,v48
v_mov_b32 v51,v50
v_pk_fma_f32 v[52:53],v[116:117],v[48:49],v[50:51]
v_pk_fma_f32 v[54:55],v[118:119],v[48:49],v[50:51]
v_pk_fma_f32 v[56:57],v[120:121],v[48:49],v[50:51]
v_pk_fma_f32 v[58:59],v[122:123],v[48:49],v[50:51]
s_add_u32 s78,s55,71303168
buffer_load_dwordx4 v[164:167],v1,s[36:39],s78 offen offset:0 sc1
buffer_load_dwordx4 v[168:171],v1,s[36:39],s78 offen offset:2048 sc1
s_add_u32 s78,s55,71307264
buffer_load_dwordx4 v[172:175],v1,s[36:39],s78 offen offset:0 sc1
buffer_load_dwordx4 v[176:179],v1,s[36:39],s78 offen offset:2048 sc1
v_pk_fma_f32 v[116:117],v[52:53],v[84:85],v[116:117]
v_pk_fma_f32 v[118:119],v[54:55],v[86:87],v[118:119]
v_pk_fma_f32 v[120:121],v[56:57],v[88:89],v[120:121]
v_pk_fma_f32 v[122:123],v[58:59],v[90:91],v[122:123]
v_cvt_pk_bf16_f32 v148,v116,v117
v_cvt_pk_bf16_f32 v149,v118,v119
v_cvt_pk_bf16_f32 v150,v120,v121
v_cvt_pk_bf16_f32 v151,v122,v123
v_pk_fma_f32 v[52:53],v[124:125],v[48:49],v[50:51]
v_pk_fma_f32 v[54:55],v[126:127],v[48:49],v[50:51]
v_pk_fma_f32 v[56:57],v[128:129],v[48:49],v[50:51]
v_pk_fma_f32 v[58:59],v[130:131],v[48:49],v[50:51]
s_add_u32 s78,s55,89128960
buffer_load_dwordx4 v[180:183],v1,s[36:39],s78 offen offset:0 sc1
buffer_load_dwordx4 v[184:187],v1,s[36:39],s78 offen offset:2048 sc1
s_add_u32 s78,s55,89133056
buffer_load_dwordx4 v[188:191],v1,s[36:39],s78 offen offset:0 sc1
buffer_load_dwordx4 v[192:195],v1,s[36:39],s78 offen offset:2048 sc1
v_pk_fma_f32 v[124:125],v[52:53],v[92:93],v[124:125]
v_pk_fma_f32 v[126:127],v[54:55],v[94:95],v[126:127]
v_pk_fma_f32 v[128:129],v[56:57],v[96:97],v[128:129]
v_pk_fma_f32 v[130:131],v[58:59],v[98:99],v[130:131]
v_cvt_pk_bf16_f32 v152,v124,v125
v_cvt_pk_bf16_f32 v153,v126,v127
v_cvt_pk_bf16_f32 v154,v128,v129
v_cvt_pk_bf16_f32 v155,v130,v131
v_pk_fma_f32 v[52:53],v[132:133],v[48:49],v[50:51]
v_pk_fma_f32 v[54:55],v[134:135],v[48:49],v[50:51]
v_pk_fma_f32 v[56:57],v[136:137],v[48:49],v[50:51]
v_pk_fma_f32 v[58:59],v[138:139],v[48:49],v[50:51]
v_pk_fma_f32 v[132:133],v[52:53],v[100:101],v[132:133]
v_pk_fma_f32 v[134:135],v[54:55],v[102:103],v[134:135]
v_pk_fma_f32 v[136:137],v[56:57],v[104:105],v[136:137]
v_pk_fma_f32 v[138:139],v[58:59],v[106:107],v[138:139]
v_cvt_pk_bf16_f32 v156,v132,v133
v_cvt_pk_bf16_f32 v157,v134,v135
v_cvt_pk_bf16_f32 v158,v136,v137
v_cvt_pk_bf16_f32 v159,v138,v139
v_pk_fma_f32 v[52:53],v[140:141],v[48:49],v[50:51]
v_pk_fma_f32 v[54:55],v[142:143],v[48:49],v[50:51]
v_pk_fma_f32 v[56:57],v[144:145],v[48:49],v[50:51]
v_pk_fma_f32 v[58:59],v[146:147],v[48:49],v[50:51]
v_pk_fma_f32 v[140:141],v[52:53],v[108:109],v[140:141]
v_pk_fma_f32 v[142:143],v[54:55],v[110:111],v[142:143]
v_pk_fma_f32 v[144:145],v[56:57],v[112:113],v[144:145]
v_pk_fma_f32 v[146:147],v[58:59],v[114:115],v[146:147]
v_cvt_pk_bf16_f32 v160,v140,v141
v_cvt_pk_bf16_f32 v161,v142,v143
v_cvt_pk_bf16_f32 v162,v144,v145
v_cvt_pk_bf16_f32 v163,v146,v147
v_mfma_f32_16x16x32_bf16 v[36:39],v[32:35],v[148:151],0
buffer_load_dword v18,v6,s[36:39],s62 offen offset:0 sc1
buffer_store_dwordx4 v[148:151],v1,s[36:39],s57 offen offset:0 sc0 sc1
v_mfma_f32_16x16x32_bf16 v[40:43],v[148:151],v[148:151],0
M 36,v[32:35],152
buffer_store_dwordx4 v[152:155],v1,s[36:39],s57 offen offset:2048 sc0 sc1
M 40,v[152:155],152
M 36,v[32:35],156
buffer_store_dwordx4 v[156:159],v1,s[36:39],s58 offen offset:0 sc0 sc1
M 40,v[156:159],156
M 36,v[32:35],160
buffer_store_dwordx4 v[160:163],v1,s[36:39],s58 offen offset:2048 sc0 sc1
M 40,v[160:163],160
s_nop 5
v_add_f32_dpp v44,v36,v36 row_ror:8 row_mask:0xf bank_mask:0xf
s_nop 0
v_cndmask_b32 v45,v40,v41,s[70:71]
v_cndmask_b32 v46,v42,v43,s[70:71]
ds_write_b32 v2,v44 offset:1024
v_cndmask_b32 v45,v45,v46,s[72:73]
ds_write_b32 v0,v45 offset:1024
L 0
s_barrier
R 52,3,1024
R 56,3,1040
R 60,3,1056
L 0
v_pk_add_f32 v[52:53],v[52:53],v[54:55]
v_pk_add_f32 v[56:57],v[56:57],v[58:59]
v_pk_add_f32 v[60:61],v[60:61],v[62:63]
v_add_f32 v52,v52,v53
v_pk_add_f32 v[56:57],v[56:57],v[60:61]
v_mul_f32 v52,s68,v52
v_add_f32 v53,v56,v57
v_mul_f32 v53,s68,v53
v_fma_f32 v53,-v52,v52,v53
v_max_f32 v53,0,v53
v_add_f32 v54,s69,v53
v_fma_f32 v55,s69,v54,v53
v_rsq_f32 v65,v54
v_rsq_f32 v54,v55
v_mul_f32_e64 v64,-v52,v65
v_mul_f32 v63,v55,v54
v_mul_f32 v61,s76,v54
v_mul_f32 v55,v54,v54
v_mul_f32 v62,s77,v55
v_mul_f32 v62,v62,v61
v_cndmask_b32 v60,v52,v64,s[74:75]
v_cndmask_b32 v61,v61,v65,s[74:75]
buffer_store_dwordx4 v[60:63],v4,s[36:39],s59 offen offset:0 sc0 sc1
s_mov_b32 s63,s60
s_lshr_b32 s78,s45,1
s_and_b32 s79,s45,1
s_lshl_b32 s79,s79,7
s_lshl_b32 s57,s78,15
s_add_u32 s57,s57,s79
s_add_u32 s57,s57,12582912
s_add_u32 s58,s57,4096
s_lshl_b32 s59,s78,9
s_add_u32 s59,s59,s79
s_add_u32 s59,s59,107020288
s_lshl_b32 s60,s45,9
s_add_u32 s60,s60,s66
s_add_u32 s60,s60,131072
s_add_u32 s45,s45,s46
s_lshr_b32 s78,s45,1
s_and_b32 s79,s45,1
s_lshl_b32 s79,s79,7
s_lshl_b32 s55,s78,15
s_add_u32 s55,s55,s79
s_mul_i32 s56,s45,16384
s_add_u32 s56,s56,98566144
s_lshl_b32 s61,s78,11
s_add_u32 s61,s61,110587904
s_add_u32 s78,s45,s46
s_lshr_b32 s78,s78,1
s_lshl_b32 s62,s78,11
s_add_u32 s62,s62,110587904
s_waitcnt vmcnt(5)
s_branch Lr_it0_%=
Lr_tc1_%=:
s_add_u32 s78,s55,71303168
buffer_load_dwordx4 v[164:167],v1,s[36:39],s78 offen offset:0 sc1
buffer_load_dwordx4 v[168:171],v1,s[36:39],s78 offen offset:2048 sc1
s_add_u32 s78,s55,71307264
buffer_load_dwordx4 v[172:175],v1,s[36:39],s78 offen offset:0 sc1
buffer_load_dwordx4 v[176:179],v1,s[36:39],s78 offen offset:2048 sc1
s_add_u32 s78,s55,89128960
buffer_load_dwordx4 v[180:183],v1,s[36:39],s78 offen offset:0 sc1
buffer_load_dwordx4 v[184:187],v1,s[36:39],s78 offen offset:2048 sc1
s_add_u32 s78,s55,89133056
buffer_load_dwordx4 v[188:191],v1,s[36:39],s78 offen offset:0 sc1
buffer_load_dwordx4 v[192:195],v1,s[36:39],s78 offen offset:2048 sc1
buffer_load_dword v18,v6,s[36:39],s62 offen offset:0 sc1
s_waitcnt vmcnt(9)
buffer_store_dword v17,v5,s[36:39],s63 offen offset:0 sc0 sc1
s_mov_b32 s63,0x7fff0000
s_lshr_b32 s78,s45,1
s_and_b32 s79,s45,1
s_lshl_b32 s79,s79,7
s_lshl_b32 s57,s78,15
s_add_u32 s57,s57,s79
s_add_u32 s57,s57,12582912
s_add_u32 s58,s57,4096
s_lshl_b32 s59,s78,9
s_add_u32 s59,s59,s79
s_add_u32 s59,s59,107020288
s_lshl_b32 s60,s45,9
s_add_u32 s60,s60,s66
s_add_u32 s60,s60,131072
s_add_u32 s45,s45,s46
s_lshr_b32 s78,s45,1
s_and_b32 s79,s45,1
s_lshl_b32 s79,s79,7
s_lshl_b32 s55,s78,15
s_add_u32 s55,s55,s79
s_mul_i32 s56,s45,16384
s_add_u32 s56,s56,98566144
s_lshl_b32 s61,s78,11
s_add_u32 s61,s61,110587904
s_add_u32 s78,s45,s46
s_lshr_b32 s78,s78,1
s_lshl_b32 s62,s78,11
s_add_u32 s62,s62,110587904
s_waitcnt vmcnt(1)
s_branch Lr_it0_%=
Lr_done_%=:
s_waitcnt vmcnt(0) lgkmcnt(0)
s_barrier
Lcode_r_e:
.purgem M
.purgem R
.purgem T
.purgem D
.purgem L
)"
  :: "s"(ws), "s"(__builtin_amdgcn_readfirstlane(sWave)), "s"(__builtin_amdgcn_readfirstlane(sG0)), "s"(__builtin_amdgcn_readfirstlane(sStep)), "s"(__builtin_amdgcn_readfirstlane(sWw)), "s"(__builtin_amdgcn_readfirstlane(sExp)), "v"(vRP), "v"(vRF), "v"(vRwr), "v"(vRrd), "v"(vRS), "v"(vFlag), "v"(vL4), "v"(vLY), "v"(vQ), "v"(vY0), "v"(vTr), "v"(vSa), "v"(vSb), "v"(vWd0), "v"(vWd1), "v"(vWr0), "v"(vWr1), "s"(q), "s"(w4) : "v0", "v1", "v2", "v3", "v4", "v5", "v6", "v7", "v8", "v9", "v10", "v11", "v12", "v13", "v14", "v15", "v16", "v17", "v18", "v19", "v20", "v21", "v22", "v23", "v24", "v25", "v26", "v27", "v28", "v29", "v30", "v31", "v32", "v33", "v34", "v35", "v36", "v37", "v38", "v39", "v40", "v41", "v42", "v43", "v44", "v45", "v46", "v47", "v48", "v49", "v50", "v51", "v52", "v53", "v54", "v55", "v56", "v57", "v58", "v59", "v60", "v61", "v62", "v63", "v64", "v65", "v66", "v67", "v68", "v69", "v70", "v71", "v72", "v73", "v74", "v75", "v76", "v77", "v78", "v79", "v80", "v81", "v82", "v83", "v84", "v85", "v86", "v87", "v88", "v89", "v90", "v91", "v92", "v93", "v94", "v95", "v96", "v97", "v98", "v99", "v100", "v101", "v102", "v103", "v104", "v105", "v106", "v107", "v108", "v109", "v110", "v111", "v112", "v113", "v114", "v115", "v116", "v117", "v118", "v119", "v120", "v121", "v122", "v123", "v124", "v125", "v126", "v127", "v128", "v129", "v130", "v131", "v132", "v133", "v134", "v135", "v136", "v137", "v138", "v139", "v140", "v141", "v142", "v143", "v144", "v145", "v146", "v147", "v148", "v149", "v150", "v151", "v152", "v153", "v154", "v155", "v156", "v157", "v158", "v159", "v160", "v161", "v162", "v163", "v164", "v165", "v166", "v167", "v168", "v169", "v170", "v171", "v172", "v173", "v174", "v175", "v176", "v177", "v178", "v179", "v180", "v181", "v182", "v183", "v184", "v185", "v186", "v187", "v188", "v189", "v190", "v191", "v192", "v193", "v194", "v195", "v196", "v197", "v198", "v199", "v200", "v201", "v202", "v203", "v204", "v205", "v206", "v207", "v208", "v209", "v210", "v211", "v212", "v213", "v214", "v215", "v216", "v217", "v218", "v219", "v220", "v221", "v222", "v223", "v224", "v225", "v226", "v227", "v228", "v229", "v230", "v231", "a0", "a1", "a2", "a3", "a4", "a5", "a6", "a7", "a8", "a9", "a10", "a11", "a12", "a13", "a14", "a15", "a16", "a17", "a18", "a19", "a20", "a21", "a22", "a23", "a24", "a25", "a26", "a27", "a28", "a29", "a30", "a31", "a32", "a33", "a34", "a35", "a36", "a37", "a38", "a39", "a40", "a41", "a42", "a43", "a44", "a45", "a46", "a47", "a48", "a49", "a50", "a51", "a52", "a53", "a54", "a55", "a56", "a57", "a58", "a59", "a60", "a61", "a62", "a63", "a64", "a65", "a66", "a67", "a68", "a69", "a70", "a71", "a72", "a73", "a74", "a75", "a76", "a77", "a78", "a79", "a80", "a81", "a82", "a83", "a84", "a85", "a86", "a87", "a88", "a89", "a90", "a91", "a92", "a93", "a94", "a95", "a96", "a97", "a98", "a99", "a100", "a101", "a102", "a103", "a104", "a105", "a106", "a107", "a108", "a109", "a110", "a111", "a112", "a113", "a114", "a115", "a116", "a117", "a118", "a119", "a120", "a121", "a122", "a123", "a124", "a125", "a126", "a127", "a128", "a129", "a130", "a131", "a132", "a133", "a134", "a135", "a136", "a137", "a138", "a139", "a140", "a141", "a142", "a143", "a144", "a145", "a146", "a147", "a148", "a149", "a150", "a151", "a152", "a153", "a154", "a155", "a156", "a157", "a158", "a159", "a160", "a161", "a162", "a163", "a164", "a165", "a166", "a167", "a168", "a169", "a170", "a171", "a172", "a173", "a174", "a175", "a176", "a177", "a178", "a179", "a180", "a181", "a182", "a183", "a184", "a185", "a186", "a187", "a188", "a189", "a190", "a191", "a192", "a193", "a194", "a195", "a196", "a197", "a198", "a199", "a200", "a201", "a202", "a203", "a204", "a205", "a206", "a207", "a208", "a209", "a210", "a211", "a212", "a213", "a214", "a215", "a216", "a217", "a218", "a219", "a220", "a221", "a222", "a223", "a224", "a225", "a226", "a227", "a228", "a229", "a230", "a231", "a232", "a233", "a234", "a235", "a236", "a237", "a238", "a239", "a240", "a241", "a242", "a243", "a244", "a245", "a246", "a247", "a248", "a249", "a250", "a251", "a252", "a253", "a254", "a255", "s36", "s37", "s38", "s39", "s40", "s41", "s42", "s43", "s44", "s45", "s46", "s47", "s48", "s49", "s50", "s51", "s52", "s53", "s54", "s55", "s56", "s57", "s58", "s59", "s60", "s61", "s62", "s63", "s64", "s65", "s66", "s67", "s68", "s69", "s70", "s71", "s72", "s73", "s74", "s75", "s76", "s77", "s78", "s79", "s80", "s81", "s82", "s83", "s84", "s85", "s86", "s87", "s88", "s89", "s90", "s91", "s92", "s93", "s94", "s95", "s96", "s97", "vcc", "scc", "memory");
    if (RHELP && slice < 16) { kind = 4; rep = 5; kh = slice >> 3; slice &= 7; }
  }
  CTS(9);
#define COMMON(NF, FOFF, FSTR, SNAP, LDW, NL, TOFF)                                                                                            \
  LANE_ID(ln);                                                                                                                       \
  const int l15 = ln & 15, lg = ln >> 4, lq = l15 >> 2, lp = ln & 3, rr = 8 * lg + lq, fj = ln & 31;                                 \
  const unsigned vL16 = ln * 16, vFlag = (ln < 32 && wave == 0) ? ln * 4 : OOB;                                                      \
  const unsigned vSnapOff = (wave == 0 && fj < (NF)) ? (FOFF) + (ln >> 5) * sStep * (FSTR) : OOB;                                    \
  const unsigned vSnapRd = (SNAP) + (fj < (NF) ? ln : ln & 32) * 4;                                                                  \
  const unsigned vWd0 = (ln >> 3) * (LDW) + ((ln & 7) ^ swzB(ln >> 3)) * 16, vWd1 = ((ln >> 3) + 8) * (LDW) + ((ln & 7) ^ swzB(8 + (ln >> 3))) * 16; \
  const unsigned vWr0 = (wave >> 1) * 8192 + rr * 128 + (((wave & 1) * 4 + lp) ^ swzB(rr)) * 16, vWr1 = vWr0 + 8;                    \
  const unsigned vWr2 = 16384 + rr * 128 + ((2 * wave + (lp >> 1)) ^ swzB(rr)) * 16 + (lp & 1) * 8;                                  \
  const unsigned sWave = wave, sRk = kind == 4 ? rep : t_start, sG0 = rep < 5 ? rep : rep == 5 ? U0 + 5 : U1 + rep, sDbg = sDbg0 + (rep < 5 ? 0 : 2048)
  if (kind == 3 || kind == 5) {
    const unsigned sStep = kind == 3 ? 5 : G3OR;
    COMMON(8, fj * 128, 1024, 131072, 7168, 256, 0);
    const unsigned vOutS = (lg & 1) * 8 + (((wave & 1) * 2 + (lg >> 1)) * 16 + l15) * 16 + (4 + (wave >> 1) - wave) * 1024;
    const unsigned vAux0 = (ln & 7) * 128 + (4 * wave + lg) * 8 + ((ln >> 3) & 1) * 4;
    const float k1 = -2.0f * 0.7978845608028654f * 1.4426950408889634f;
    const unsigned vAux1 = 139264 + (l15 < 4 ? (4 * wave + lg) * 16 + l15 * 4 : 512 + ln * 4), vAux2 = 139264 + l15 * 16;
    const unsigned vAux3 = l15 == 2 ? __float_as_uint(k1 * 0.044715f) : 0u, vAn = l15 == 3 ? 0x3f800000u : 0u, vAe = l15 == 1 ? __float_as_uint(k1) : 0u;
    const unsigned sOutOff = OFF_G + (slice * 6 + wave) * 1024, sFoutOff = OFF_FG + slice * 128, sAuxOff = 0, sWw = 16 * wave * 7168 + slice * 384, sBuf = 0;
    const unsigned sExp = __builtin_amdgcn_readfirstlane(ep);
    if (kind == 3) { asm volatile(
R"(.macro M d,w,x
v_mfma_f32_16x16x32_bf16 v[\d:\d+3], \w, v[\x:\x+3], v[\d:\d+3]
.endm
.macro R d,a,o
ds_read_b128 v[\d:\d+3], v\a offset:\o
.endm
.macro T d,a,o
ds_read_b64_tr_b16 \d, v\a offset:\o
.endm
.macro D a,s,o
buffer_load_dwordx4 v\a, s[36:39], s\s offen offset:\o sc1 lds
.endm
.macro L c
s_waitcnt lgkmcnt(\c)
.endm
Lcode_g3_b:
s_mov_b64 s[36:37],%0
s_mov_b32 s38,111243392
s_mov_b32 s39,0x00020000
s_mov_b32 s44,%1
s_mov_b32 s45,%2
s_mov_b32 s46,%3
s_mov_b32 s47,%4
s_mov_b32 s48,%5
s_mov_b32 s49,%6
s_mov_b32 s50,%7
s_mov_b32 s51,%8
s_mov_b32 s53,%9
v_mov_b32 v0,%10
v_mov_b32 v3,%11
v_mov_b32 v5,%12
v_mov_b32 v6,%13
v_mov_b32 v18,%14
v_mov_b32 v19,%15
v_mov_b32 v20,%16
v_mov_b32 v21,%17
v_mov_b32 v22,%18
v_mov_b32 v10,%19
v_mov_b32 v11,%20
v_mov_b32 v12,%21
v_mov_b32 v13,%22
v_mov_b32 v14,%23
v_mov_b32 v15,%24
v_mov_b32 v16,%25
s_mov_b64 s[40:41],%26
s_mov_b32 s42,7340032
s_mov_b32 s43,0x00020000
s_lshl_b32 s57,s44,11
s_cmp_ge_u32 s45,8
s_cbranch_scc1 Lg3_dly_%=
s_sleep 16
s_sleep 16
s_sleep 16
Lg3_dly_%=:
s_add_u32 s55,s50,0
s_add_u32 m0,s57,0
s_nop 0
buffer_load_dwordx4 v18,s[40:43],s55 offen offset:0  lds
s_add_u32 m0,s57,1024
s_nop 0
buffer_load_dwordx4 v19,s[40:43],s55 offen offset:0  lds
s_add_u32 m0,s57,8064
s_nop 0
buffer_load_dwordx4 v18,s[40:43],s55 offen offset:128  lds
s_add_u32 m0,s57,9088
s_nop 0
buffer_load_dwordx4 v19,s[40:43],s55 offen offset:128  lds
s_add_u32 m0,s57,16128
s_nop 0
buffer_load_dwordx4 v18,s[40:43],s55 offen offset:256  lds
s_add_u32 m0,s57,17152
s_nop 0
buffer_load_dwordx4 v19,s[40:43],s55 offen offset:256  lds
s_add_u32 s55,s50,458752
s_add_u32 m0,s57,24576
s_nop 0
buffer_load_dwordx4 v18,s[40:43],s55 offen offset:0  lds
s_add_u32 m0,s57,25600
s_nop 0
buffer_load_dwordx4 v19,s[40:43],s55 offen offset:0  lds
s_add_u32 m0,s57,32640
s_nop 0
buffer_load_dwordx4 v18,s[40:43],s55 offen offset:128  lds
s_add_u32 m0,s57,33664
s_nop 0
buffer_load_dwordx4 v19,s[40:43],s55 offen offset:128  lds
s_add_u32 m0,s57,40704
s_nop 0
buffer_load_dwordx4 v18,s[40:43],s55 offen offset:256  lds
s_add_u32 m0,s57,41728
s_nop 0
buffer_load_dwordx4 v19,s[40:43],s55 offen offset:256  lds
s_add_u32 s55,s50,917504
s_add_u32 m0,s57,49152
s_nop 0
buffer_load_dwordx4 v18,s[40:43],s55 offen offset:0  lds
s_add_u32 m0,s57,50176
s_nop 0
buffer_load_dwordx4 v19,s[40:43],s55 offen offset:0  lds
s_add_u32 m0,s57,57216
s_nop 0
buffer_load_dwordx4 v18,s[40:43],s55 offen offset:128  lds
s_add_u32 m0,s57,58240
s_nop 0
buffer_load_dwordx4 v19,s[40:43],s55 offen offset:128  lds
s_add_u32 m0,s57,65280
s_nop 0
buffer_load_dwordx4 v18,s[40:43],s55 offen offset:256  lds
s_add_u32 m0,s57,66304
s_nop 0
buffer_load_dwordx4 v19,s[40:43],s55 offen offset:256  lds
s_add_u32 s55,s50,1376256
s_add_u32 m0,s57,73728
s_nop 0
buffer_load_dwordx4 v18,s[40:43],s55 offen offset:0  lds
s_add_u32 m0,s57,74752
s_nop 0
buffer_load_dwordx4 v19,s[40:43],s55 offen offset:0  lds
s_add_u32 m0,s57,81792
s_nop 0
buffer_load_dwordx4 v18,s[40:43],s55 offen offset:128  lds
s_add_u32 m0,s57,82816
s_nop 0
buffer_load_dwordx4 v19,s[40:43],s55 offen offset:128  lds
s_add_u32 m0,s57,89856
s_nop 0
buffer_load_dwordx4 v18,s[40:43],s55 offen offset:256  lds
s_add_u32 m0,s57,90880
s_nop 0
buffer_load_dwordx4 v19,s[40:43],s55 offen offset:256  lds
s_add_u32 s55,s50,1835008
s_add_u32 m0,s57,98304
s_nop 0
buffer_load_dwordx4 v18,s[40:43],s55 offen offset:0  lds
s_add_u32 m0,s57,99328
s_nop 0
buffer_load_dwordx4 v19,s[40:43],s55 offen offset:0  lds
s_add_u32 m0,s57,106368
s_nop 0
buffer_load_dwordx4 v18,s[40:43],s55 offen offset:128  lds
s_add_u32 m0,s57,107392
s_nop 0
buffer_load_dwordx4 v19,s[40:43],s55 offen offset:128  lds
s_add_u32 m0,s57,114432
s_nop 0
buffer_load_dwordx4 v18,s[40:43],s55 offen offset:256  lds
s_add_u32 m0,s57,115456
s_nop 0
buffer_load_dwordx4 v19,s[40:43],s55 offen offset:256  lds
s_waitcnt vmcnt(24) lgkmcnt(0)
s_barrier
s_add_u32 s55,s50,2293760
s_add_u32 m0,s57,122880
s_nop 0
buffer_load_dwordx4 v18,s[40:43],s55 offen offset:0  lds
s_add_u32 m0,s57,123904
s_nop 0
buffer_load_dwordx4 v19,s[40:43],s55 offen offset:0  lds
s_add_u32 m0,s57,130944
s_nop 0
buffer_load_dwordx4 v18,s[40:43],s55 offen offset:128  lds
s_add_u32 m0,s57,131968
s_nop 0
buffer_load_dwordx4 v19,s[40:43],s55 offen offset:128  lds
s_add_u32 m0,s57,139008
s_nop 0
buffer_load_dwordx4 v18,s[40:43],s55 offen offset:256  lds
s_add_u32 m0,s57,140032
s_nop 0
buffer_load_dwordx4 v19,s[40:43],s55 offen offset:256  lds
v_add_u32 v26,0,v20
v_add_u32 v27,0,v21
v_add_u32 v28,0,v22
T a[0:1],26,0
T a[2:3],26,512
T a[4:5],27,0
T a[6:7],27,512
T a[8:9],28,0
T a[10:11],28,512
T a[12:13],26,4096
T a[14:15],26,4608
T a[16:17],27,4096
T a[18:19],27,4608
T a[20:21],28,4096
T a[22:23],28,4608
s_waitcnt vmcnt(24) lgkmcnt(0)
s_barrier
s_add_u32 s55,s50,2752512
s_add_u32 m0,s57,0
s_nop 0
buffer_load_dwordx4 v18,s[40:43],s55 offen offset:0  lds
s_add_u32 m0,s57,1024
s_nop 0
buffer_load_dwordx4 v19,s[40:43],s55 offen offset:0  lds
s_add_u32 m0,s57,8064
s_nop 0
buffer_load_dwordx4 v18,s[40:43],s55 offen offset:128  lds
s_add_u32 m0,s57,9088
s_nop 0
buffer_load_dwordx4 v19,s[40:43],s55 offen offset:128  lds
s_add_u32 m0,s57,16128
s_nop 0
buffer_load_dwordx4 v18,s[40:43],s55 offen offset:256  lds
s_add_u32 m0,s57,17152
s_nop 0
buffer_load_dwordx4 v19,s[40:43],s55 offen offset:256  lds
v_add_u32 v26,24576,v20
v_add_u32 v27,24576,v21
v_add_u32 v28,24576,v22
T a[24:25],26,0
T a[26:27],26,512
T a[28:29],27,0
T a[30:31],27,512
T a[32:33],28,0
T a[34:35],28,512
T a[36:37],26,4096
T a[38:39],26,4608
T a[40:41],27,4096
T a[42:43],27,4608
T a[44:45],28,4096
T a[46:47],28,4608
s_waitcnt vmcnt(24) lgkmcnt(0)
s_barrier
s_add_u32 s55,s50,3211264
s_add_u32 m0,s57,24576
s_nop 0
buffer_load_dwordx4 v18,s[40:43],s55 offen offset:0  lds
s_add_u32 m0,s57,25600
s_nop 0
buffer_load_dwordx4 v19,s[40:43],s55 offen offset:0  lds
s_add_u32 m0,s57,32640
s_nop 0
buffer_load_dwordx4 v18,s[40:43],s55 offen offset:128  lds
s_add_u32 m0,s57,33664
s_nop 0
buffer_load_dwordx4 v19,s[40:43],s55 offen offset:128  lds
s_add_u32 m0,s57,40704
s_nop 0
buffer_load_dwordx4 v18,s[40:43],s55 offen offset:256  lds
s_add_u32 m0,s57,41728
s_nop 0
buffer_load_dwordx4 v19,s[40:43],s55 offen offset:256  lds
v_add_u32 v26,49152,v20
v_add_u32 v27,49152,v21
v_add_u32 v28,49152,v22
T a[48:49],26,0
T a[50:51],26,512
T a[52:53],27,0
T a[54:55],27,512
T a[56:57],28,0
T a[58:59],28,512
T a[60:61],26,4096
T a[62:63],26,4608
T a[64:65],27,4096
T a[66:67],27,4608
T a[68:69],28,4096
T a[70:71],28,4608
s_waitcnt vmcnt(24) lgkmcnt(0)
s_barrier
s_add_u32 s55,s50,3670016
s_add_u32 m0,s57,49152
s_nop 0
buffer_load_dwordx4 v18,s[40:43],s55 offen offset:0  lds
s_add_u32 m0,s57,50176
s_nop 0
buffer_load_dwordx4 v19,s[40:43],s55 offen offset:0  lds
s_add_u32 m0,s57,57216
s_nop 0
buffer_load_dwordx4 v18,s[40:43],s55 offen offset:128  lds
s_add_u32 m0,s57,58240
s_nop 0
buffer_load_dwordx4 v19,s[40:43],s55 offen offset:128  lds
s_add_u32 m0,s57,65280
s_nop 0
buffer_load_dwordx4 v18,s[40:43],s55 offen offset:256  lds
s_add_u32 m0,s57,66304
s_nop 0
buffer_load_dwordx4 v19,s[40:43],s55 offen offset:256  lds
v_add_u32 v26,73728,v20
v_add_u32 v27,73728,v21
v_add_u32 v28,73728,v22
T a[72:73],26,0
T a[74:75],26,512
T a[76:77],27,0
T a[78:79],27,512
T a[80:81],28,0
T a[82:83],28,512
T a[84:85],26,4096
T a[86:87],26,4608
T a[88:89],27,4096
T a[90:91],27,4608
T a[92:93],28,4096
T a[94:95],28,4608
s_waitcnt vmcnt(24) lgkmcnt(0)
s_barrier
s_add_u32 s55,s50,4128768
s_add_u32 m0,s57,73728
s_nop 0
buffer_load_dwordx4 v18,s[40:43],s55 offen offset:0  lds
s_add_u32 m0,s57,74752
s_nop 0
buffer_load_dwordx4 v19,s[40:43],s55 offen offset:0  lds
s_add_u32 m0,s57,81792
s_nop 0
buffer_load_dwordx4 v18,s[40:43],s55 offen offset:128  lds
s_add_u32 m0,s57,82816
s_nop 0
buffer_load_dwordx4 v19,s[40:43],s55 offen offset:128  lds
s_add_u32 m0,s57,89856
s_nop 0
buffer_load_dwordx4 v18,s[40:43],s55 offen offset:256  lds
s_add_u32 m0,s57,90880
s_nop 0
buffer_load_dwordx4 v19,s[40:43],s55 offen offset:256  lds
v_add_u32 v26,98304,v20
v_add_u32 v27,98304,v21
v_add_u32 v28,98304,v22
T a[96:97],26,0
T a[98:99],26,512
T a[100:101],27,0
T a[102:103],27,512
T a[104:105],28,0
T a[106:107],28,512
T a[108:109],26,4096
T a[110:111],26,4608
T a[112:113],27,4096
T a[114:115],27,4608
T a[116:117],28,4096
T a[118:119],28,4608
s_waitcnt vmcnt(24) lgkmcnt(0)
s_barrier
s_add_u32 s55,s50,4587520
s_add_u32 m0,s57,98304
s_nop 0
buffer_load_dwordx4 v18,s[40:43],s55 offen offset:0  lds
s_add_u32 m0,s57,99328
s_nop 0
buffer_load_dwordx4 v19,s[40:43],s55 offen offset:0  lds
s_add_u32 m0,s57,106368
s_nop 0
buffer_load_dwordx4 v18,s[40:43],s55 offen offset:128  lds
s_add_u32 m0,s57,107392
s_nop 0
buffer_load_dwordx4 v19,s[40:43],s55 offen offset:128  lds
s_add_u32 m0,s57,114432
s_nop 0
buffer_load_dwordx4 v18,s[40:43],s55 offen offset:256  lds
s_add_u32 m0,s57,115456
s_nop 0
buffer_load_dwordx4 v19,s[40:43],s55 offen offset:256  lds
v_add_u32 v26,122880,v20
v_add_u32 v27,122880,v21
v_add_u32 v28,122880,v22
T a[120:121],26,0
T a[122:123],26,512
T a[124:125],27,0
T a[126:127],27,512
T a[128:129],28,0
T a[130:131],28,512
T a[132:133],26,4096
T a[134:135],26,4608
T a[136:137],27,4096
T a[138:139],27,4608
T a[140:141],28,4096
T a[142:143],28,4608
s_waitcnt vmcnt(24) lgkmcnt(0)
s_barrier
s_add_u32 s55,s50,5046272
s_add_u32 m0,s57,122880
s_nop 0
buffer_load_dwordx4 v18,s[40:43],s55 offen offset:0  lds
s_add_u32 m0,s57,123904
s_nop 0
buffer_load_dwordx4 v19,s[40:43],s55 offen offset:0  lds
s_add_u32 m0,s57,130944
s_nop 0
buffer_load_dwordx4 v18,s[40:43],s55 offen offset:128  lds
s_add_u32 m0,s57,131968
s_nop 0
buffer_load_dwordx4 v19,s[40:43],s55 offen offset:128  lds
s_add_u32 m0,s57,139008
s_nop 0
buffer_load_dwordx4 v18,s[40:43],s55 offen offset:256  lds
s_add_u32 m0,s57,140032
s_nop 0
buffer_load_dwordx4 v19,s[40:43],s55 offen offset:256  lds
v_add_u32 v26,0,v20
v_add_u32 v27,0,v21
v_add_u32 v28,0,v22
T a[144:145],26,0
T a[146:147],26,512
T a[148:149],27,0
T a[150:151],27,512
T a[152:153],28,0
T a[154:155],28,512
T a[156:157],26,4096
T a[158:159],26,4608
T a[160:161],27,4096
T a[162:163],27,4608
T a[164:165],28,4096
T a[166:167],28,4608
s_waitcnt vmcnt(24) lgkmcnt(0)
s_barrier
s_add_u32 s55,s50,5505024
s_add_u32 m0,s57,0
s_nop 0
buffer_load_dwordx4 v18,s[40:43],s55 offen offset:0  lds
s_add_u32 m0,s57,1024
s_nop 0
buffer_load_dwordx4 v19,s[40:43],s55 offen offset:0  lds
s_add_u32 m0,s57,8064
s_nop 0
buffer_load_dwordx4 v18,s[40:43],s55 offen offset:128  lds
s_add_u32 m0,s57,9088
s_nop 0
buffer_load_dwordx4 v19,s[40:43],s55 offen offset:128  lds
s_add_u32 m0,s57,16128
s_nop 0
buffer_load_dwordx4 v18,s[40:43],s55 offen offset:256  lds
s_add_u32 m0,s57,17152
s_nop 0
buffer_load_dwordx4 v19,s[40:43],s55 offen offset:256  lds
v_add_u32 v26,24576,v20
v_add_u32 v27,24576,v21
v_add_u32 v28,24576,v22
T a[168:169],26,0
T a[170:171],26,512
T a[172:173],27,0
T a[174:175],27,512
T a[176:177],28,0
T a[178:179],28,512
T a[180:181],26,4096
T a[182:183],26,4608
T a[184:185],27,4096
T a[186:187],27,4608
T a[188:189],28,4096
T a[190:191],28,4608
s_waitcnt vmcnt(24) lgkmcnt(0)
s_barrier
s_add_u32 s55,s50,5963776
s_add_u32 m0,s57,24576
s_nop 0
buffer_load_dwordx4 v18,s[40:43],s55 offen offset:0  lds
s_add_u32 m0,s57,25600
s_nop 0
buffer_load_dwordx4 v19,s[40:43],s55 offen offset:0  lds
s_add_u32 m0,s57,32640
s_nop 0
buffer_load_dwordx4 v18,s[40:43],s55 offen offset:128  lds
s_add_u32 m0,s57,33664
s_nop 0
buffer_load_dwordx4 v19,s[40:43],s55 offen offset:128  lds
s_add_u32 m0,s57,40704
s_nop 0
buffer_load_dwordx4 v18,s[40:43],s55 offen offset:256  lds
s_add_u32 m0,s57,41728
s_nop 0
buffer_load_dwordx4 v19,s[40:43],s55 offen offset:256  lds
v_add_u32 v26,49152,v20
v_add_u32 v27,49152,v21
v_add_u32 v28,49152,v22
T a[192:193],26,0
T a[194:195],26,512
T a[196:197],27,0
T a[198:199],27,512
T a[200:201],28,0
T a[202:203],28,512
T a[204:205],26,4096
T a[206:207],26,4608
T a[208:209],27,4096
T a[210:211],27,4608
T a[212:213],28,4096
T a[214:215],28,4608
s_waitcnt vmcnt(24) lgkmcnt(0)
s_barrier
s_add_u32 s55,s50,6422528
s_add_u32 m0,s57,49152
s_nop 0
buffer_load_dwordx4 v18,s[40:43],s55 offen offset:0  lds
s_add_u32 m0,s57,50176
s_nop 0
buffer_load_dwordx4 v19,s[40:43],s55 offen offset:0  lds
s_add_u32 m0,s57,57216
s_nop 0
buffer_load_dwordx4 v18,s[40:43],s55 offen offset:128  lds
s_add_u32 m0,s57,58240
s_nop 0
buffer_load_dwordx4 v19,s[40:43],s55 offen offset:128  lds
s_add_u32 m0,s57,65280
s_nop 0
buffer_load_dwordx4 v18,s[40:43],s55 offen offset:256  lds
s_add_u32 m0,s57,66304
s_nop 0
buffer_load_dwordx4 v19,s[40:43],s55 offen offset:256  lds
v_add_u32 v26,73728,v20
v_add_u32 v27,73728,v21
v_add_u32 v28,73728,v22
T a[216:217],26,0
T a[218:219],26,512
T a[220:221],27,0
)"
R"(T a[222:223],27,512
T a[224:225],28,0
T a[226:227],28,512
T a[228:229],26,4096
T a[230:231],26,4608
T a[232:233],27,4096
T a[234:235],27,4608
T a[236:237],28,4096
T a[238:239],28,4608
s_waitcnt vmcnt(24) lgkmcnt(0)
s_barrier
s_add_u32 s55,s50,6881280
s_add_u32 m0,s57,73728
s_nop 0
buffer_load_dwordx4 v18,s[40:43],s55 offen offset:0  lds
s_add_u32 m0,s57,74752
s_nop 0
buffer_load_dwordx4 v19,s[40:43],s55 offen offset:0  lds
s_add_u32 m0,s57,81792
s_nop 0
buffer_load_dwordx4 v18,s[40:43],s55 offen offset:128  lds
s_add_u32 m0,s57,82816
s_nop 0
buffer_load_dwordx4 v19,s[40:43],s55 offen offset:128  lds
s_add_u32 m0,s57,89856
s_nop 0
buffer_load_dwordx4 v18,s[40:43],s55 offen offset:256  lds
s_add_u32 m0,s57,90880
s_nop 0
buffer_load_dwordx4 v19,s[40:43],s55 offen offset:256  lds
v_add_u32 v26,98304,v20
v_add_u32 v27,98304,v21
v_add_u32 v28,98304,v22
T a[240:241],26,0
T a[242:243],26,512
T a[244:245],27,0
T a[246:247],27,512
T a[248:249],28,0
T a[250:251],28,512
T a[252:253],26,4096
T a[254:255],26,4608
T v[128:129],27,4096
T v[130:131],27,4608
T v[132:133],28,4096
T v[134:135],28,4608
s_waitcnt vmcnt(24) lgkmcnt(0)
s_barrier
v_add_u32 v26,122880,v20
v_add_u32 v27,122880,v21
v_add_u32 v28,122880,v22
T v[136:137],26,0
T v[138:139],26,512
T v[140:141],27,0
T v[142:143],27,512
T v[144:145],28,0
T v[146:147],28,512
T v[148:149],26,4096
T v[150:151],26,4608
T v[152:153],27,4096
T v[154:155],27,4608
T v[156:157],28,4096
T v[158:159],28,4608
s_waitcnt vmcnt(18) lgkmcnt(0)
s_barrier
v_add_u32 v26,0,v20
v_add_u32 v27,0,v21
v_add_u32 v28,0,v22
T v[160:161],26,0
T v[162:163],26,512
T v[164:165],27,0
T v[166:167],27,512
T v[168:169],28,0
T v[170:171],28,512
T v[172:173],26,4096
T v[174:175],26,4608
T v[176:177],27,4096
T v[178:179],27,4608
T v[180:181],28,4096
T v[182:183],28,4608
s_waitcnt vmcnt(12) lgkmcnt(0)
s_barrier
v_add_u32 v26,24576,v20
v_add_u32 v27,24576,v21
v_add_u32 v28,24576,v22
T v[184:185],26,0
T v[186:187],26,512
T v[188:189],27,0
T v[190:191],27,512
T v[192:193],28,0
T v[194:195],28,512
T v[196:197],26,4096
T v[198:199],26,4608
T v[200:201],27,4096
T v[202:203],27,4608
T v[204:205],28,4096
T v[206:207],28,4608
s_waitcnt vmcnt(6) lgkmcnt(0)
s_barrier
v_add_u32 v26,49152,v20
v_add_u32 v27,49152,v21
v_add_u32 v28,49152,v22
T v[208:209],26,0
T v[210:211],26,512
T v[212:213],27,0
T v[214:215],27,512
T v[216:217],28,0
T v[218:219],28,512
T v[220:221],26,4096
T v[222:223],26,4608
T v[224:225],27,4096
T v[226:227],27,4608
T v[228:229],28,4096
T v[230:231],28,4608
s_waitcnt vmcnt(0) lgkmcnt(0)
s_barrier
v_add_u32 v26,73728,v20
v_add_u32 v27,73728,v21
v_add_u32 v28,73728,v22
T v[232:233],26,0
T v[234:235],26,512
T v[236:237],27,0
T v[238:239],27,512
T v[240:241],28,0
T v[242:243],28,512
T v[244:245],26,4096
T v[246:247],26,4608
T v[248:249],27,4096
T v[250:251],27,4608
T v[252:253],28,4096
T v[254:255],28,4608
L 0
s_barrier
v_mov_b32 v42,0x3f803f80
v_mov_b32 v43,0x3f803f80
v_mov_b32 v44,0x3f803f80
v_mov_b32 v45,0x3f803f80
s_nop 1
v_mfma_f32_16x16x32_bf16 v[82:85],a[0:3],v[42:45],0
v_mfma_f32_16x16x32_bf16 v[86:89],a[4:7],v[42:45],0
v_mfma_f32_16x16x32_bf16 v[90:93],a[8:11],v[42:45],0
M 82,a[12:15],42
M 86,a[16:19],42
M 90,a[20:23],42
M 82,a[24:27],42
M 86,a[28:31],42
M 90,a[32:35],42
M 82,a[36:39],42
M 86,a[40:43],42
M 90,a[44:47],42
M 82,a[48:51],42
M 86,a[52:55],42
M 90,a[56:59],42
M 82,a[60:63],42
M 86,a[64:67],42
M 90,a[68:71],42
M 82,a[72:75],42
M 86,a[76:79],42
M 90,a[80:83],42
M 82,a[84:87],42
M 86,a[88:91],42
M 90,a[92:95],42
M 82,a[96:99],42
M 86,a[100:103],42
M 90,a[104:107],42
M 82,a[108:111],42
M 86,a[112:115],42
M 90,a[116:119],42
M 82,a[120:123],42
M 86,a[124:127],42
M 90,a[128:131],42
M 82,a[132:135],42
M 86,a[136:139],42
M 90,a[140:143],42
M 82,a[144:147],42
M 86,a[148:151],42
M 90,a[152:155],42
M 82,a[156:159],42
M 86,a[160:163],42
M 90,a[164:167],42
M 82,a[168:171],42
M 86,a[172:175],42
M 90,a[176:179],42
M 82,a[180:183],42
M 86,a[184:187],42
M 90,a[188:191],42
M 82,a[192:195],42
M 86,a[196:199],42
M 90,a[200:203],42
M 82,a[204:207],42
M 86,a[208:211],42
M 90,a[212:215],42
M 82,a[216:219],42
M 86,a[220:223],42
M 90,a[224:227],42
M 82,a[228:231],42
M 86,a[232:235],42
M 90,a[236:239],42
M 82,a[240:243],42
M 86,a[244:247],42
M 90,a[248:251],42
M 82,a[252:255],42
M 86,v[128:131],42
M 90,v[132:135],42
M 82,v[136:139],42
M 86,v[140:143],42
M 90,v[144:147],42
M 82,v[148:151],42
M 86,v[152:155],42
M 90,v[156:159],42
M 82,v[160:163],42
M 86,v[164:167],42
M 90,v[168:171],42
M 82,v[172:175],42
M 86,v[176:179],42
M 90,v[180:183],42
M 82,v[184:187],42
M 86,v[188:191],42
M 90,v[192:195],42
M 82,v[196:199],42
M 86,v[200:203],42
M 90,v[204:207],42
M 82,v[208:211],42
M 86,v[212:215],42
M 90,v[216:219],42
M 82,v[220:223],42
M 86,v[224:227],42
M 90,v[228:231],42
M 82,v[232:235],42
M 86,v[236:239],42
M 90,v[240:243],42
M 82,v[244:247],42
M 86,v[248:251],42
M 90,v[252:255],42
v_mov_b32 v4,s53
s_mov_b32 s82,0
s_mov_b32 s80,0
s_mov_b32 s54,s45
s_mul_i32 s57,s44,4096
s_mul_i32 s55,s45,32768
s_add_u32 s55,s55,s57
s_add_u32 s55,s55,31457280
s_mul_i32 s57,s44,4096
s_mul_i32 s60,s46,32768
s_mul_i32 s61,s46,1024
s_mul_i32 s62,s46,114688
s_mul_i32 s63,s46,2560
s_mul_i32 s64,s46,1024
s_lshl_b32 s59,s44,8
s_add_u32 s59,s59,131072
s_mul_i32 s58,s45,1024
s_add_u32 s58,s58,109613056
s_mul_i32 s65,s45,114688
s_add_u32 s65,s65,s47
s_mul_i32 s68,s45,2560
s_add_u32 s68,s68,s48
s_mul_i32 s73,s45,1024
s_add_u32 s73,s73,107479040
s_mul_i32 s74,s45,32768
s_add_u32 s74,s74,s49
s_mul_i32 s75,s45,1024
s_add_u32 s75,s75,107479040
s_mov_b32 s66,0x7fff0000
s_mov_b32 s67,0x7fff0000
s_mov_b32 s69,0x7fff0000
s_mov_b32 s76,0x7fff0000
s_mov_b32 s77,0x7fff0000
s_mov_b32 s70,0x7fff0000
s_mov_b32 s71,0x7fff0000
s_mov_b32 s72,0x7fff0000
v_mov_b32 v1,v0
v_add_u32 v2,16384,v0
s_mov_b32 s84,16384
s_mov_b32 s86,0x3a800000
s_mov_b32 s87,0x3727c5ac
s_mov_b32 s88,0xc0135761
s_mov_b32 s89,0x3d372713
s_lshr_b32 s85,s44,1
s_mul_i32 s85,s85,127
v_lshlrev_b32 v8,22,v0
v_and_b32 v8,0x80000000,v8
v_or_b32 v8,v8,v5
v_add_u32 v2,32768,v0
s_lshl_b32 s84,s57,1
s_add_u32 s84,s84,32768
v_and_b32 v94,240,v0
v_cmp_eq_u32 vcc,0,v94
s_nop 1
v_cndmask_b32 v17,0,1.0,vcc
s_getpc_b64 s[40:41]
Lg3_pc_%=:
s_lshl_b32 s78,s44,13
v_lshlrev_b32 v94,3,v0
v_add_u32 v94,s78,v94
v_min_u32 v94,Lcode_g3_e-Lg3_pc_%=-4,v94
s_mov_b32 m0,163584
s_nop 0
global_load_lds_dword v94,s[40:41]
s_add_u32 m0,s59,4096
s_nop 0
buffer_load_dword v8,s[36:39],s58 offen offset:0 sc1 lds
buffer_store_dword v4,v3,s[36:39],s71 offen offset:0 sc0 sc1
s_add_u32 m0,s59,5120
s_nop 0
buffer_load_dword v8,s[36:39],s58 offen offset:0 sc1 lds
buffer_store_dword v4,v3,s[36:39],s71 offen offset:0 sc0 sc1
s_mov_b32 s83,0
Lg3_poll_%=:
s_add_u32 s78,s83,2
s_and_b32 s78,s78,3
s_lshl_b32 s78,s78,10
s_add_u32 s78,s78,4096
s_add_u32 m0,s59,s78
s_nop 0
buffer_load_dword v8,s[36:39],s58 offen offset:0 sc1 lds
s_and_b32 s78,s83,3
s_lshl_b32 s78,s78,10
v_add_u32 v94,s78,v6
s_waitcnt vmcnt(4)
s_barrier
ds_read_b32 v7,v94 offset:4096
buffer_store_dword v4,v3,s[36:39],s70 offen offset:0 sc0 sc1
s_mov_b32 s70,s71
s_mov_b32 s71,s72
s_mov_b32 s72,0x7fff0000
s_add_u32 s83,s83,1
s_add_u32 s82,s82,1
L 0
v_cmp_eq_u32 vcc,s53,v7
s_nop 0
s_cmp_eq_u32 vcc_lo,-1
s_cbranch_scc1 Lg3_go_%=
s_cmp_lt_u32 s82,20000
s_cbranch_scc1 Lg3_poll_%=
Lg3_go_%=:
buffer_load_dword v114,v11,s[36:39],s73 offen offset:0 sc1
s_add_u32 s73,s73,s64
s_add_u32 m0,s57,0
s_add_u32 s56,s55,0
D 0,56,0
D 0,56,1024
D 0,56,2048
D 0,56,3072
s_add_u32 s58,s58,s61
s_add_u32 m0,s59,2048
s_nop 0
buffer_load_dword v8,s[36:39],s58 offen offset:0 sc1 lds
s_add_u32 m0,s57,16384
s_add_u32 s56,s55,16384
D 0,56,0
D 0,56,1024
D 0,56,2048
D 0,56,3072
s_add_u32 s55,s55,s60
s_sub_u32 s57,s84,s57
s_add_u32 s54,s54,s46
s_waitcnt vmcnt(4)
s_barrier
buffer_store_dword v4,v3,s[36:39],s70 offen offset:0 sc0 sc1
s_min_u32 s71,s71,s72
buffer_store_dword v4,v3,s[36:39],s71 offen offset:0 sc0 sc1
s_mov_b32 s70,0x7fff0000
s_mov_b32 s71,0x7fff0000
s_mov_b32 s72,0x7fff0000
s_cmp_eq_u32 s80,1
buffer_store_dword v4,v3,s[36:39],s71 offen offset:0 sc0 sc1
s_cbranch_scc1 Lg3_st1_%=
s_branch Lg3_st0_%=
Lg3_gox_%=:
s_add_u32 m0,s59,2048
s_nop 0
buffer_load_dword v8,s[36:39],s58 offen offset:0 sc1 lds
s_waitcnt vmcnt(0)
s_barrier
buffer_store_dword v4,v3,s[36:39],s70 offen offset:0 sc0 sc1
s_min_u32 s71,s71,s72
buffer_store_dword v4,v3,s[36:39],s71 offen offset:0 sc0 sc1
s_mov_b32 s70,0x7fff0000
s_mov_b32 s71,0x7fff0000
s_mov_b32 s72,0x7fff0000
s_cmp_eq_u32 s80,1
s_cbranch_scc1 Lg3_st1_%=
Lg3_st0_%=:
ds_read_b32 v7,v6 offset:2048
R 42,1,0
R 46,1,1024
R 50,1,2048
R 54,1,3072
R 58,1,4096
L 5
v_cmp_eq_u32 vcc,s53,v7
L 3
s_cmp_eq_u32 vcc_lo,-1
s_cbranch_scc1 Lg3_F0_%=
s_branch Lg3_L0_%=
Lg3_st1_%=:
ds_read_b32 v7,v6 offset:2048
R 42,2,0
R 46,2,1024
R 50,2,2048
R 54,2,3072
R 58,2,4096
L 5
v_cmp_eq_u32 vcc,s53,v7
L 3
s_cmp_eq_u32 vcc_lo,-1
s_cbranch_scc1 Lg3_F1_%=
s_branch Lg3_L1_%=
Lg3_F0_%=:
v_mfma_f32_16x16x32_bf16 v[26:29],a[8:11],v[42:45],0
s_add_u32 m0,s57,0
R 62,1,5120
v_add_f32_dpp v115,v114,v114 quad_perm:[1,0,3,2] row_mask:0xf bank_mask:0xf
M 26,a[20:23],46
buffer_load_dword v114,v11,s[36:39],s73 offen offset:0 sc1
v_mfma_f32_16x16x32_bf16 v[22:25],a[4:7],v[42:45],0
s_add_u32 s56,s55,0
v_add_f32_dpp v115,v115,v115 quad_perm:[2,3,0,1] row_mask:0xf bank_mask:0xf
v_fma_f32 v30,-v78,v82,v30
M 22,a[16:19],46
s_add_u32 s58,s58,s61
R 66,1,6144
v_fma_f32 v31,-v78,v83,v31
v_mfma_f32_16x16x32_bf16 v[18:21],a[0:3],v[42:45],0
D 0,56,0
M 18,a[12:15],46
L 3
v_add_f32_dpp v118,v115,v115 row_half_mirror row_mask:0xf bank_mask:0xf
v_fma_f32 v32,-v78,v84,v32
M 18,a[24:27],50
s_mov_b32 s66,s65
R 70,1,7168
v_fma_f32 v33,-v78,v85,v33
M 18,a[36:39],54
s_add_u32 s65,s65,s62
v_mov_b32_dpp v115,v118 row_ror:8 row_mask:0xf bank_mask:0xf
v_fma_f32 v115,-v118,v118,v115
M 22,a[28:31],50
s_add_u32 s54,s54,s46
v_max_f32 v115,s87,v115
v_mul_f32 v118,v17,v118
M 22,a[40:43],54
s_add_u32 s73,s73,s64
R 42,1,8192
v_mul_f32 v94,v30,v30
M 26,a[32:35],50
v_rsq_f32 v116,v115
M 26,a[44:47],54
L 3
D 0,56,1024
M 26,a[56:59],58
R 46,1,9216
v_fma_f32 v115,v15,v115,v16
M 26,a[68:71],62
v_mul_f32 v119,v116,v116
v_fma_f32 v115,v14,v119,v115
M 22,a[52:55],58
v_fma_f32 v118,v116,v115,v118
v_mul_f32 v95,v31,v31
M 22,a[64:67],62
R 50,1,10240
v_mul_f32 v96,v32,v32
M 18,a[48:51],58
ds_write_b32 v12,v118 offset:0
M 18,a[60:63],62
L 4
v_mul_f32 v97,v33,v33
v_fma_f32 v94,v80,v94,v79
M 18,a[72:75],66
R 54,1,11264
v_fma_f32 v95,v80,v95,v79
M 18,a[84:87],70
v_fma_f32 v96,v80,v96,v79
v_fma_f32 v97,v80,v97,v79
M 22,a[76:79],66
v_mul_f32 v94,v30,v94
v_mul_f32 v95,v31,v95
M 22,a[88:91],70
R 58,1,12288
v_mul_f32 v96,v32,v96
M 26,a[80:83],66
D 0,56,2048
M 26,a[92:95],70
L 4
v_exp_f32 v94,v94
M 26,a[104:107],42
R 62,1,13312
v_mul_f32 v97,v33,v97
M 26,a[116:119],46
v_exp_f32 v95,v95
M 22,a[100:103],42
v_exp_f32 v96,v96
M 22,a[112:115],46
R 66,1,14336
v_fma_f32 v94,v94,v81,v81
M 18,a[96:99],42
v_exp_f32 v97,v97
M 18,a[108:111],46
L 3
v_rcp_f32 v94,v94
M 18,a[120:123],50
)"
R"(R 70,1,15360
v_fma_f32 v95,v95,v81,v81
M 18,a[132:135],54
s_waitcnt vmcnt(7)
D 0,56,3072
M 22,a[124:127],50
s_barrier
v_rcp_f32 v95,v95
M 22,a[136:139],54
s_add_u32 m0,s59,0
R 42,1,16384
v_fma_f32 v96,v96,v81,v81
M 26,a[128:131],50
s_add_u32 s56,s55,16384
v_rcp_f32 v96,v96
M 26,a[140:143],54
L 3
v_fma_f32 v97,v97,v81,v81
v_mul_f32 v30,v30,v94
M 26,a[152:155],58
s_add_u32 s55,s55,s60
R 46,1,17408
v_mul_f32 v31,v31,v95
M 26,a[164:167],62
v_rcp_f32 v97,v97
M 22,a[148:151],58
v_mul_f32 v32,v32,v96
v_mul_f32 v33,v33,v97
M 22,a[160:163],62
R 50,1,18432
v_cvt_pk_bf16_f32 v106,v30,v31
M 18,a[144:147],58
buffer_load_dword v8,s[36:39],s58 offen offset:0 sc1 lds
M 18,a[156:159],62
L 3
v_cvt_pk_bf16_f32 v107,v32,v33
v_fma_f32 v34,-v78,v86,v34
M 18,a[168:171],66
s_add_u32 m0,s57,16384
R 54,1,19456
v_fma_f32 v35,-v78,v87,v35
M 18,a[180:183],70
s_sub_u32 s57,s84,s57
v_fma_f32 v36,-v78,v88,v36
v_fma_f32 v37,-v78,v89,v37
M 22,a[172:175],66
v_mul_f32 v98,v34,v34
v_mul_f32 v99,v35,v35
M 22,a[184:187],70
R 58,1,20480
v_mul_f32 v100,v36,v36
M 26,a[176:179],66
D 0,56,0
M 26,a[188:191],70
L 3
v_mul_f32 v101,v37,v37
v_fma_f32 v98,v80,v98,v79
M 26,a[200:203],42
R 62,1,21504
v_fma_f32 v99,v80,v99,v79
M 26,a[212:215],46
v_fma_f32 v100,v80,v100,v79
v_fma_f32 v101,v80,v101,v79
M 22,a[196:199],42
v_mul_f32 v98,v34,v98
v_mul_f32 v99,v35,v99
M 22,a[208:211],46
R 66,1,22528
v_mul_f32 v100,v36,v100
M 18,a[192:195],42
v_exp_f32 v98,v98
M 18,a[204:207],46
L 3
v_exp_f32 v99,v99
M 18,a[216:219],50
R 70,1,23552
v_mul_f32 v101,v37,v101
M 18,a[228:231],54
D 0,56,1024
M 22,a[220:223],50
v_exp_f32 v100,v100
M 22,a[232:235],54
R 42,1,24576
v_fma_f32 v98,v98,v81,v81
M 26,a[224:227],50
v_exp_f32 v101,v101
M 26,a[236:239],54
L 3
v_rcp_f32 v98,v98
M 26,a[248:251],58
R 46,1,25600
v_fma_f32 v99,v99,v81,v81
M 26,v[132:135],62
v_rcp_f32 v99,v99
M 22,a[244:247],58
v_fma_f32 v100,v100,v81,v81
v_fma_f32 v101,v101,v81,v81
M 22,v[128:131],62
R 50,1,26624
v_mul_f32 v34,v34,v98
M 18,a[240:243],58
v_rcp_f32 v100,v100
M 18,a[252:255],62
L 3
v_rcp_f32 v101,v101
M 18,v[136:139],66
R 54,1,27648
v_mul_f32 v35,v35,v99
M 18,v[148:151],70
D 0,56,2048
M 22,v[140:143],66
v_mul_f32 v36,v36,v100
v_mul_f32 v37,v37,v101
M 22,v[152:155],70
R 58,1,28672
v_cvt_pk_bf16_f32 v108,v34,v35
M 26,v[144:147],66
v_cvt_pk_bf16_f32 v109,v36,v37
v_fma_f32 v38,-v78,v90,v38
M 26,v[156:159],70
L 3
v_fma_f32 v39,-v78,v91,v39
v_fma_f32 v40,-v78,v92,v40
M 26,v[168:171],42
R 62,1,29696
v_fma_f32 v41,-v78,v93,v41
M 26,v[180:183],46
v_mul_f32 v102,v38,v38
v_mul_f32 v103,v39,v39
M 22,v[164:167],42
v_mul_f32 v104,v40,v40
v_mul_f32 v105,v41,v41
M 22,v[176:179],46
R 66,1,30720
v_fma_f32 v102,v80,v102,v79
M 18,v[160:163],42
v_fma_f32 v103,v80,v103,v79
v_fma_f32 v104,v80,v104,v79
M 18,v[172:175],46
L 3
D 0,56,3072
M 18,v[184:187],50
s_nop 0
R 70,1,31744
v_fma_f32 v105,v80,v105,v79
M 18,v[196:199],54
s_waitcnt vmcnt(4)
v_mul_f32 v102,v38,v102
v_mul_f32 v103,v39,v103
M 22,v[188:191],50
s_barrier
buffer_store_dword v4,v3,s[36:39],s70 offen offset:0 sc0 sc1
M 22,v[200:203],54
s_mov_b32 s70,s69
R 42,2,0
ds_read_b32 v7,v6 offset:0
M 26,v[192:195],50
s_mov_b32 s69,s68
R 74,13,0
v_mul_f32 v104,v40,v104
M 26,v[204:207],54
L 5
v_exp_f32 v102,v102
M 26,v[216:219],58
s_add_u32 s68,s68,s63
R 46,2,1024
v_mul_f32 v105,v41,v105
M 26,v[228:231],62
v_exp_f32 v103,v103
M 22,v[212:215],58
buffer_store_dwordx4 v[106:109],v0,s[36:39],s67 offen offset:0 sc0 sc1
v_fma_f32 v102,v102,v81,v81
M 22,v[224:227],62
L 2
R 50,2,2048
v_fma_f32 v103,v103,v81,v81
M 18,v[208:211],58
v_exp_f32 v104,v104
M 18,v[220:223],62
s_nop 0
v_exp_f32 v105,v105
M 18,v[232:235],66
R 54,2,3072
v_fma_f32 v104,v104,v81,v81
M 18,v[244:247],70
v_fma_f32 v105,v105,v81,v81
v_cmp_eq_u32 vcc,s53,v7
M 22,v[236:239],66
v_rcp_f32 v102,v102
v_rcp_f32 v103,v103
v_rcp_f32 v104,v104
v_rcp_f32 v105,v105
M 22,v[248:251],70
s_cmp_eq_u32 vcc_lo,-1
R 58,2,4096
v_mul_f32 v38,v38,v102
v_mul_f32 v39,v39,v103
v_mul_f32 v40,v40,v104
v_mul_f32 v41,v41,v105
v_cvt_pk_bf16_f32 v110,v38,v39
v_cvt_pk_bf16_f32 v111,v40,v41
M 26,v[240:243],66
L 3
buffer_store_dwordx2 v[110:111],v10,s[36:39],s67 offen offset:0 sc0 sc1
M 26,v[252:255],70
s_cbranch_scc0 Lg3_K1_%=
Lg3_F1_%=:
v_mfma_f32_16x16x32_bf16 v[38:41],a[8:11],v[42:45],0
s_add_u32 m0,s57,0
R 62,2,5120
v_add_f32_dpp v115,v114,v114 quad_perm:[1,0,3,2] row_mask:0xf bank_mask:0xf
M 38,a[20:23],46
buffer_load_dword v114,v11,s[36:39],s73 offen offset:0 sc1
v_mfma_f32_16x16x32_bf16 v[34:37],a[4:7],v[42:45],0
s_add_u32 s56,s55,0
v_add_f32_dpp v115,v115,v115 quad_perm:[2,3,0,1] row_mask:0xf bank_mask:0xf
v_fma_f32 v18,-v74,v82,v18
M 34,a[16:19],46
s_add_u32 s58,s58,s61
R 66,2,6144
v_fma_f32 v19,-v74,v83,v19
v_mfma_f32_16x16x32_bf16 v[30:33],a[0:3],v[42:45],0
D 0,56,0
M 30,a[12:15],46
L 3
v_add_f32_dpp v118,v115,v115 row_half_mirror row_mask:0xf bank_mask:0xf
v_fma_f32 v20,-v74,v84,v20
M 30,a[24:27],50
s_mov_b32 s67,s65
R 70,2,7168
v_fma_f32 v21,-v74,v85,v21
M 30,a[36:39],54
s_add_u32 s65,s65,s62
v_mov_b32_dpp v115,v118 row_ror:8 row_mask:0xf bank_mask:0xf
v_fma_f32 v115,-v118,v118,v115
M 34,a[28:31],50
s_add_u32 s54,s54,s46
v_max_f32 v115,s87,v115
v_mul_f32 v118,v17,v118
M 34,a[40:43],54
s_add_u32 s73,s73,s64
R 42,2,8192
v_mul_f32 v94,v18,v18
M 38,a[32:35],50
v_rsq_f32 v116,v115
M 38,a[44:47],54
L 3
D 0,56,1024
M 38,a[56:59],58
R 46,2,9216
v_fma_f32 v115,v15,v115,v16
M 38,a[68:71],62
v_mul_f32 v119,v116,v116
v_fma_f32 v115,v14,v119,v115
M 34,a[52:55],58
v_fma_f32 v118,v116,v115,v118
v_mul_f32 v95,v19,v19
M 34,a[64:67],62
R 50,2,10240
v_mul_f32 v96,v20,v20
M 30,a[48:51],58
ds_write_b32 v12,v118 offset:2048
M 30,a[60:63],62
L 4
v_mul_f32 v97,v21,v21
v_fma_f32 v94,v76,v94,v75
M 30,a[72:75],66
R 54,2,11264
v_fma_f32 v95,v76,v95,v75
M 30,a[84:87],70
v_fma_f32 v96,v76,v96,v75
v_fma_f32 v97,v76,v97,v75
M 34,a[76:79],66
v_mul_f32 v94,v18,v94
v_mul_f32 v95,v19,v95
M 34,a[88:91],70
R 58,2,12288
v_mul_f32 v96,v20,v96
M 38,a[80:83],66
D 0,56,2048
M 38,a[92:95],70
L 4
v_exp_f32 v94,v94
M 38,a[104:107],42
R 62,2,13312
v_mul_f32 v97,v21,v97
M 38,a[116:119],46
v_exp_f32 v95,v95
M 34,a[100:103],42
v_exp_f32 v96,v96
M 34,a[112:115],46
R 66,2,14336
v_fma_f32 v94,v94,v77,v77
M 30,a[96:99],42
v_exp_f32 v97,v97
M 30,a[108:111],46
L 3
v_rcp_f32 v94,v94
M 30,a[120:123],50
R 70,2,15360
v_fma_f32 v95,v95,v77,v77
M 30,a[132:135],54
s_waitcnt vmcnt(7)
D 0,56,3072
M 34,a[124:127],50
s_barrier
v_rcp_f32 v95,v95
M 34,a[136:139],54
s_add_u32 m0,s59,1024
R 42,2,16384
v_fma_f32 v96,v96,v77,v77
M 38,a[128:131],50
s_add_u32 s56,s55,16384
v_rcp_f32 v96,v96
M 38,a[140:143],54
L 3
v_fma_f32 v97,v97,v77,v77
v_mul_f32 v18,v18,v94
M 38,a[152:155],58
s_add_u32 s55,s55,s60
R 46,2,17408
v_mul_f32 v19,v19,v95
M 38,a[164:167],62
v_rcp_f32 v97,v97
M 34,a[148:151],58
v_mul_f32 v20,v20,v96
v_mul_f32 v21,v21,v97
M 34,a[160:163],62
R 50,2,18432
v_cvt_pk_bf16_f32 v106,v18,v19
M 30,a[144:147],58
buffer_load_dword v8,s[36:39],s58 offen offset:0 sc1 lds
M 30,a[156:159],62
L 3
v_cvt_pk_bf16_f32 v107,v20,v21
v_fma_f32 v22,-v74,v86,v22
M 30,a[168:171],66
s_add_u32 m0,s57,16384
R 54,2,19456
v_fma_f32 v23,-v74,v87,v23
M 30,a[180:183],70
s_sub_u32 s57,s84,s57
v_fma_f32 v24,-v74,v88,v24
v_fma_f32 v25,-v74,v89,v25
M 34,a[172:175],66
v_mul_f32 v98,v22,v22
v_mul_f32 v99,v23,v23
M 34,a[184:187],70
R 58,2,20480
v_mul_f32 v100,v24,v24
M 38,a[176:179],66
D 0,56,0
M 38,a[188:191],70
L 3
v_mul_f32 v101,v25,v25
v_fma_f32 v98,v76,v98,v75
M 38,a[200:203],42
R 62,2,21504
v_fma_f32 v99,v76,v99,v75
M 38,a[212:215],46
v_fma_f32 v100,v76,v100,v75
v_fma_f32 v101,v76,v101,v75
M 34,a[196:199],42
v_mul_f32 v98,v22,v98
v_mul_f32 v99,v23,v99
M 34,a[208:211],46
R 66,2,22528
v_mul_f32 v100,v24,v100
M 30,a[192:195],42
v_exp_f32 v98,v98
M 30,a[204:207],46
L 3
v_exp_f32 v99,v99
M 30,a[216:219],50
R 70,2,23552
v_mul_f32 v101,v25,v101
M 30,a[228:231],54
D 0,56,1024
M 34,a[220:223],50
v_exp_f32 v100,v100
M 34,a[232:235],54
R 42,2,24576
v_fma_f32 v98,v98,v77,v77
M 38,a[224:227],50
v_exp_f32 v101,v101
M 38,a[236:239],54
L 3
v_rcp_f32 v98,v98
M 38,a[248:251],58
R 46,2,25600
v_fma_f32 v99,v99,v77,v77
M 38,v[132:135],62
v_rcp_f32 v99,v99
M 34,a[244:247],58
v_fma_f32 v100,v100,v77,v77
v_fma_f32 v101,v101,v77,v77
M 34,v[128:131],62
R 50,2,26624
v_mul_f32 v22,v22,v98
M 30,a[240:243],58
v_rcp_f32 v100,v100
M 30,a[252:255],62
L 3
v_rcp_f32 v101,v101
M 30,v[136:139],66
R 54,2,27648
v_mul_f32 v23,v23,v99
M 30,v[148:151],70
D 0,56,2048
M 34,v[140:143],66
v_mul_f32 v24,v24,v100
v_mul_f32 v25,v25,v101
M 34,v[152:155],70
R 58,2,28672
v_cvt_pk_bf16_f32 v108,v22,v23
M 38,v[144:147],66
v_cvt_pk_bf16_f32 v109,v24,v25
v_fma_f32 v26,-v74,v90,v26
M 38,v[156:159],70
L 3
v_fma_f32 v27,-v74,v91,v27
v_fma_f32 v28,-v74,v92,v28
M 38,v[168:171],42
R 62,2,29696
v_fma_f32 v29,-v74,v93,v29
M 38,v[180:183],46
v_mul_f32 v102,v26,v26
v_mul_f32 v103,v27,v27
M 34,v[164:167],42
v_mul_f32 v104,v28,v28
v_mul_f32 v105,v29,v29
M 34,v[176:179],46
R 66,2,30720
v_fma_f32 v102,v76,v102,v75
M 30,v[160:163],42
v_fma_f32 v103,v76,v103,v75
v_fma_f32 v104,v76,v104,v75
M 30,v[172:175],46
L 3
D 0,56,3072
M 30,v[184:187],50
s_nop 0
R 70,2,31744
v_fma_f32 v105,v76,v105,v75
M 30,v[196:199],54
s_waitcnt vmcnt(4)
v_mul_f32 v102,v26,v102
v_mul_f32 v103,v27,v103
M 34,v[188:191],50
s_barrier
buffer_store_dword v4,v3,s[36:39],s70 offen offset:0 sc0 sc1
M 34,v[200:203],54
s_mov_b32 s70,s69
R 42,1,0
ds_read_b32 v7,v6 offset:1024
M 38,v[192:195],50
s_mov_b32 s69,s68
R 78,13,2048
v_mul_f32 v104,v28,v104
M 38,v[204:207],54
L 5
v_exp_f32 v102,v102
M 38,v[216:219],58
s_add_u32 s68,s68,s63
R 46,1,1024
v_mul_f32 v105,v29,v105
M 38,v[228:231],62
v_exp_f32 v103,v103
M 34,v[212:215],58
buffer_store_dwordx4 v[106:109],v0,s[36:39],s66 offen offset:0 sc0 sc1
v_fma_f32 v102,v102,v77,v77
M 34,v[224:227],62
L 2
R 50,1,2048
v_fma_f32 v103,v103,v77,v77
M 30,v[208:211],58
v_exp_f32 v104,v104
M 30,v[220:223],62
s_nop 0
v_exp_f32 v105,v105
M 30,v[232:235],66
R 54,1,3072
v_fma_f32 v104,v104,v77,v77
M 30,v[244:247],70
v_fma_f32 v105,v105,v77,v77
v_cmp_eq_u32 vcc,s53,v7
M 34,v[236:239],66
v_rcp_f32 v102,v102
v_rcp_f32 v103,v103
v_rcp_f32 v104,v104
v_rcp_f32 v105,v105
M 34,v[248:251],70
s_cmp_eq_u32 vcc_lo,-1
R 58,1,4096
v_mul_f32 v26,v26,v102
v_mul_f32 v27,v27,v103
v_mul_f32 v28,v28,v104
v_mul_f32 v29,v29,v105
v_cvt_pk_bf16_f32 v110,v26,v27
v_cvt_pk_bf16_f32 v111,v28,v29
)"
R"(M 38,v[240:243],66
L 3
buffer_store_dwordx2 v[110:111],v10,s[36:39],s66 offen offset:0 sc0 sc1
M 38,v[252:255],70
s_cbranch_scc1 Lg3_F0_%=
Lg3_K0_%=:
Lg3_L0_%=:
v_mfma_f32_16x16x32_bf16 v[26:29],a[8:11],v[42:45],0
s_add_u32 m0,s59,3072
R 62,1,5120
v_add_f32_dpp v115,v114,v114 quad_perm:[1,0,3,2] row_mask:0xf bank_mask:0xf
M 26,a[20:23],46
buffer_load_dword v8,s[36:39],s58 offen offset:0 sc1 lds
v_mfma_f32_16x16x32_bf16 v[22:25],a[4:7],v[42:45],0
s_mov_b32 s66,s65
v_add_f32_dpp v115,v115,v115 quad_perm:[2,3,0,1] row_mask:0xf bank_mask:0xf
v_fma_f32 v30,-v78,v82,v30
M 22,a[16:19],46
s_add_u32 s65,s65,s62
R 66,1,6144
v_fma_f32 v31,-v78,v83,v31
v_mfma_f32_16x16x32_bf16 v[18:21],a[0:3],v[42:45],0
v_add_f32_dpp v118,v115,v115 row_half_mirror row_mask:0xf bank_mask:0xf
v_fma_f32 v32,-v78,v84,v32
v_fma_f32 v33,-v78,v85,v33
M 18,a[12:15],46
L 3
v_mul_f32 v94,v30,v30
v_mul_f32 v95,v31,v31
M 18,a[24:27],50
R 70,1,7168
v_mov_b32_dpp v115,v118 row_ror:8 row_mask:0xf bank_mask:0xf
M 18,a[36:39],54
v_fma_f32 v115,-v118,v118,v115
v_max_f32 v115,s87,v115
M 22,a[28:31],50
v_rsq_f32 v116,v115
M 22,a[40:43],54
R 42,1,8192
v_fma_f32 v115,v15,v115,v16
v_mul_f32 v118,v17,v118
M 26,a[32:35],50
v_mul_f32 v119,v116,v116
v_fma_f32 v115,v14,v119,v115
M 26,a[44:47],54
L 3
v_fma_f32 v118,v116,v115,v118
v_mul_f32 v96,v32,v32
M 26,a[56:59],58
R 46,1,9216
v_mul_f32 v97,v33,v33
M 26,a[68:71],62
ds_write_b32 v12,v118 offset:0
M 22,a[52:55],58
v_fma_f32 v94,v80,v94,v79
v_fma_f32 v95,v80,v95,v79
v_fma_f32 v96,v80,v96,v79
M 22,a[64:67],62
R 50,1,10240
v_fma_f32 v97,v80,v97,v79
M 18,a[48:51],58
v_mul_f32 v94,v30,v94
v_mul_f32 v95,v31,v95
M 18,a[60:63],62
L 4
v_exp_f32 v94,v94
M 18,a[72:75],66
R 54,1,11264
v_mul_f32 v96,v32,v96
M 18,a[84:87],70
v_exp_f32 v95,v95
v_mul_f32 v97,v33,v97
M 22,a[76:79],66
v_exp_f32 v96,v96
M 22,a[88:91],70
R 58,1,12288
v_fma_f32 v94,v94,v81,v81
M 26,a[80:83],66
v_exp_f32 v97,v97
M 26,a[92:95],70
L 4
v_rcp_f32 v94,v94
M 26,a[104:107],42
R 62,1,13312
v_fma_f32 v95,v95,v81,v81
v_fma_f32 v96,v96,v81,v81
M 26,a[116:119],46
v_rcp_f32 v95,v95
M 22,a[100:103],42
v_rcp_f32 v96,v96
M 22,a[112:115],46
R 66,1,14336
v_fma_f32 v97,v97,v81,v81
M 18,a[96:99],42
v_rcp_f32 v97,v97
M 18,a[108:111],46
L 3
v_mul_f32 v30,v30,v94
v_mul_f32 v31,v31,v95
v_mul_f32 v32,v32,v96
M 18,a[120:123],50
R 70,1,15360
v_mul_f32 v33,v33,v97
M 18,a[132:135],54
s_waitcnt vmcnt(0)
v_cvt_pk_bf16_f32 v106,v30,v31
v_cvt_pk_bf16_f32 v107,v32,v33
M 22,a[124:127],50
s_barrier
ds_read_b32 v7,v6 offset:3072
v_fma_f32 v34,-v78,v86,v34
M 22,a[136:139],54
R 42,1,16384
v_fma_f32 v35,-v78,v87,v35
M 26,a[128:131],50
buffer_store_dword v4,v3,s[36:39],s70 offen offset:0 sc0 sc1
v_fma_f32 v36,-v78,v88,v36
M 26,a[140:143],54
L 4
v_fma_f32 v37,-v78,v89,v37
v_mul_f32 v98,v34,v34
M 26,a[152:155],58
s_mov_b32 s70,0x7fff0000
R 46,1,17408
v_mul_f32 v99,v35,v35
M 26,a[164:167],62
v_mul_f32 v100,v36,v36
v_mul_f32 v101,v37,v37
M 22,a[148:151],58
L 2
v_cmp_eq_u32 vcc,s53,v7
v_fma_f32 v98,v80,v98,v79
M 22,a[160:163],62
R 50,1,18432
v_fma_f32 v99,v80,v99,v79
v_fma_f32 v100,v80,v100,v79
M 18,a[144:147],58
v_fma_f32 v101,v80,v101,v79
v_mul_f32 v98,v34,v98
M 18,a[156:159],62
s_cmp_eq_u32 vcc_lo,-1
s_nop 0
v_mul_f32 v99,v35,v99
s_cbranch_scc0 Lg3_X0_%=
M 18,a[168:171],66
s_add_u32 m0,s57,0
s_add_u32 s56,s55,0
s_add_u32 s58,s58,s61
R 54,1,19456
M 18,a[180:183],70
buffer_load_dword v114,v11,s[36:39],s73 offen offset:0 sc1
M 22,a[172:175],66
D 0,56,0
v_mul_f32 v100,v36,v100
M 22,a[184:187],70
s_add_u32 s54,s54,s46
R 58,1,20480
v_mul_f32 v101,v37,v101
M 26,a[176:179],66
s_add_u32 s73,s73,s64
v_exp_f32 v98,v98
M 26,a[188:191],70
L 3
v_exp_f32 v99,v99
M 26,a[200:203],42
R 62,1,21504
v_fma_f32 v98,v98,v81,v81
M 26,a[212:215],46
D 0,56,1024
v_fma_f32 v99,v99,v81,v81
M 22,a[196:199],42
v_exp_f32 v100,v100
M 22,a[208:211],46
R 66,1,22528
v_fma_f32 v100,v100,v81,v81
M 18,a[192:195],42
D 0,56,2048
M 18,a[204:207],46
L 3
v_exp_f32 v101,v101
M 18,a[216:219],50
R 70,1,23552
v_rcp_f32 v98,v98
M 18,a[228:231],54
v_rcp_f32 v99,v99
M 22,a[220:223],50
D 0,56,3072
M 22,a[232:235],54
s_add_u32 m0,s59,0
R 42,1,24576
v_fma_f32 v101,v101,v81,v81
M 26,a[224:227],50
buffer_load_dword v8,s[36:39],s58 offen offset:0 sc1 lds
M 26,a[236:239],54
L 3
v_rcp_f32 v100,v100
v_mul_f32 v34,v34,v98
M 26,a[248:251],58
s_add_u32 m0,s57,16384
R 46,1,25600
v_mul_f32 v35,v35,v99
M 26,v[132:135],62
s_add_u32 s56,s55,16384
v_rcp_f32 v101,v101
M 22,a[244:247],58
D 0,56,0
M 22,v[128:131],62
s_add_u32 s55,s55,s60
R 50,1,26624
v_mul_f32 v36,v36,v100
M 18,a[240:243],58
s_sub_u32 s57,s84,s57
v_mul_f32 v37,v37,v101
v_cvt_pk_bf16_f32 v108,v34,v35
v_cvt_pk_bf16_f32 v109,v36,v37
M 18,a[252:255],62
L 3
buffer_store_dwordx4 v[106:109],v0,s[36:39],s67 offen offset:0 sc0 sc1
v_fma_f32 v38,-v78,v90,v38
M 18,v[136:139],66
R 54,1,27648
v_fma_f32 v39,-v78,v91,v39
M 18,v[148:151],70
v_fma_f32 v40,-v78,v92,v40
v_fma_f32 v41,-v78,v93,v41
M 22,v[140:143],66
D 0,56,1024
M 22,v[152:155],70
R 58,1,28672
v_mul_f32 v102,v38,v38
v_mul_f32 v103,v39,v39
M 26,v[144:147],66
v_mul_f32 v104,v40,v40
v_mul_f32 v105,v41,v41
M 26,v[156:159],70
L 3
v_fma_f32 v102,v80,v102,v79
v_fma_f32 v103,v80,v103,v79
M 26,v[168:171],42
R 62,1,29696
v_fma_f32 v104,v80,v104,v79
M 26,v[180:183],46
D 0,56,2048
M 22,v[164:167],42
v_fma_f32 v105,v80,v105,v79
v_mul_f32 v102,v38,v102
v_mul_f32 v103,v39,v103
M 22,v[176:179],46
R 66,1,30720
v_mul_f32 v104,v40,v104
M 18,v[160:163],42
v_exp_f32 v102,v102
M 18,v[172:175],46
L 3
D 0,56,3072
M 18,v[184:187],50
s_nop 0
R 70,1,31744
v_mul_f32 v105,v41,v105
M 18,v[196:199],54
s_waitcnt vmcnt(5)
v_exp_f32 v103,v103
v_fma_f32 v102,v102,v81,v81
M 22,v[188:191],50
s_barrier
buffer_store_dword v4,v3,s[36:39],s70 offen offset:0 sc0 sc1
M 22,v[200:203],54
s_mov_b32 s70,s69
R 42,2,0
ds_read_b32 v7,v6 offset:0
M 26,v[192:195],50
s_mov_b32 s69,s68
R 74,13,0
v_fma_f32 v103,v103,v81,v81
M 26,v[204:207],54
L 5
v_exp_f32 v104,v104
M 26,v[216:219],58
s_add_u32 s68,s68,s63
R 46,2,1024
v_exp_f32 v105,v105
M 26,v[228:231],62
v_rcp_f32 v102,v102
M 22,v[212:215],58
v_rcp_f32 v103,v103
M 22,v[224:227],62
L 2
R 50,2,2048
v_fma_f32 v104,v104,v81,v81
M 18,v[208:211],58
v_rcp_f32 v104,v104
M 18,v[220:223],62
s_nop 0
v_fma_f32 v105,v105,v81,v81
v_rcp_f32 v105,v105
M 18,v[232:235],66
R 54,2,3072
v_mul_f32 v38,v38,v102
M 18,v[244:247],70
v_mul_f32 v39,v39,v103
v_mul_f32 v40,v40,v104
M 22,v[236:239],66
v_cmp_eq_u32 vcc,s53,v7
v_mul_f32 v41,v41,v105
M 22,v[248:251],70
R 58,2,4096
v_cvt_pk_bf16_f32 v110,v38,v39
v_cvt_pk_bf16_f32 v111,v40,v41
M 26,v[240:243],66
L 3
s_cmp_eq_u32 vcc_lo,-1
buffer_store_dwordx2 v[110:111],v10,s[36:39],s67 offen offset:0 sc0 sc1
M 26,v[252:255],70
buffer_store_dword v4,v3,s[36:39],s71 offen offset:0 sc0 sc1
s_cbranch_scc1 Lg3_F1_%=
s_branch Lg3_K1_%=
Lg3_X0_%=:
M 18,a[168:171],66
s_add_u32 m0,s59,0
R 54,1,19456
v_mul_f32 v100,v36,v100
M 18,a[180:183],70
buffer_load_dword v8,s[36:39],s58 offen offset:0 sc1 lds
M 22,a[172:175],66
v_exp_f32 v98,v98
M 22,a[184:187],70
R 58,1,20480
v_mul_f32 v101,v37,v101
M 26,a[176:179],66
v_exp_f32 v99,v99
M 26,a[188:191],70
L 3
v_exp_f32 v100,v100
M 26,a[200:203],42
R 62,1,21504
v_fma_f32 v98,v98,v81,v81
M 26,a[212:215],46
v_exp_f32 v101,v101
M 22,a[196:199],42
v_rcp_f32 v98,v98
M 22,a[208:211],46
R 66,1,22528
v_fma_f32 v99,v99,v81,v81
M 18,a[192:195],42
v_rcp_f32 v99,v99
M 18,a[204:207],46
L 3
v_fma_f32 v100,v100,v81,v81
v_fma_f32 v101,v101,v81,v81
M 18,a[216:219],50
R 70,1,23552
v_mul_f32 v34,v34,v98
M 18,a[228:231],54
v_rcp_f32 v100,v100
M 22,a[220:223],50
v_rcp_f32 v101,v101
M 22,a[232:235],54
R 42,1,24576
v_mul_f32 v35,v35,v99
M 26,a[224:227],50
v_mul_f32 v36,v36,v100
v_mul_f32 v37,v37,v101
M 26,a[236:239],54
L 3
v_cvt_pk_bf16_f32 v108,v34,v35
v_cvt_pk_bf16_f32 v109,v36,v37
M 26,a[248:251],58
R 46,1,25600
v_fma_f32 v38,-v78,v90,v38
M 26,v[132:135],62
buffer_store_dwordx4 v[106:109],v0,s[36:39],s67 offen offset:0 sc0 sc1
v_fma_f32 v39,-v78,v91,v39
M 22,a[244:247],58
v_fma_f32 v40,-v78,v92,v40
v_fma_f32 v41,-v78,v93,v41
M 22,v[128:131],62
R 50,1,26624
v_mul_f32 v102,v38,v38
M 18,a[240:243],58
v_mul_f32 v103,v39,v39
v_mul_f32 v104,v40,v40
M 18,a[252:255],62
L 3
v_mul_f32 v105,v41,v41
v_fma_f32 v102,v80,v102,v79
M 18,v[136:139],66
R 54,1,27648
v_fma_f32 v103,v80,v103,v79
M 18,v[148:151],70
v_fma_f32 v104,v80,v104,v79
v_fma_f32 v105,v80,v105,v79
M 22,v[140:143],66
v_mul_f32 v102,v38,v102
v_mul_f32 v103,v39,v103
M 22,v[152:155],70
R 58,1,28672
v_mul_f32 v104,v40,v104
M 26,v[144:147],66
v_exp_f32 v102,v102
M 26,v[156:159],70
L 3
v_exp_f32 v103,v103
M 26,v[168:171],42
R 62,1,29696
v_mul_f32 v105,v41,v105
M 26,v[180:183],46
v_exp_f32 v104,v104
M 22,v[164:167],42
v_exp_f32 v105,v105
M 22,v[176:179],46
R 66,1,30720
v_fma_f32 v102,v102,v81,v81
M 18,v[160:163],42
v_rcp_f32 v102,v102
M 18,v[172:175],46
L 3
v_fma_f32 v103,v103,v81,v81
v_fma_f32 v104,v104,v81,v81
M 18,v[184:187],50
s_nop 0
R 70,1,31744
v_fma_f32 v105,v105,v81,v81
M 18,v[196:199],54
s_waitcnt vmcnt(1)
v_rcp_f32 v103,v103
M 22,v[188:191],50
s_barrier
buffer_store_dword v4,v3,s[36:39],s70 offen offset:0 sc0 sc1
M 22,v[200:203],54
s_mov_b32 s70,s69
ds_read_b32 v7,v6 offset:0
R 74,13,0
M 26,v[192:195],50
s_mov_b32 s69,s68
v_rcp_f32 v104,v104
M 26,v[204:207],54
L 4
v_rcp_f32 v105,v105
M 26,v[216:219],58
s_add_u32 s68,s68,s63
v_mul_f32 v38,v38,v102
v_mul_f32 v39,v39,v103
M 26,v[228:231],62
v_mul_f32 v40,v40,v104
v_mul_f32 v41,v41,v105
M 22,v[212:215],58
v_cvt_pk_bf16_f32 v110,v38,v39
v_cvt_pk_bf16_f32 v111,v40,v41
M 22,v[224:227],62
L 1
buffer_store_dwordx2 v[110:111],v10,s[36:39],s67 offen offset:0 sc0 sc1
M 18,v[208:211],58
v_cmp_eq_u32 vcc,s53,v7
M 18,v[220:223],62
s_nop 0
M 18,v[232:235],66
M 18,v[244:247],70
M 22,v[236:239],66
M 22,v[248:251],70
M 26,v[240:243],66
M 26,v[252:255],70
s_mov_b32 s80,1
s_cmp_eq_u32 vcc_lo,-1
s_cbranch_scc0 Lg3_xn0_%=
buffer_load_dword v114,v11,s[36:39],s73 offen offset:0 sc1
L 0
v_fma_f32 v18,-v74,v82,v18
v_fma_f32 v19,-v74,v83,v19
v_fma_f32 v20,-v74,v84,v20
v_fma_f32 v21,-v74,v85,v21
v_mul_f32 v94,v18,v18
v_mul_f32 v95,v19,v19
v_mul_f32 v96,v20,v20
v_mul_f32 v97,v21,v21
s_add_u32 s73,s73,s64
s_add_u32 m0,s57,0
s_add_u32 s56,s55,0
D 0,56,0
v_fma_f32 v94,v76,v94,v75
v_fma_f32 v95,v76,v95,v75
v_fma_f32 v96,v76,v96,v75
v_fma_f32 v97,v76,v97,v75
v_mul_f32 v94,v18,v94
v_mul_f32 v95,v19,v95
v_mul_f32 v96,v20,v96
v_mul_f32 v97,v21,v97
D 0,56,1024
v_exp_f32 v94,v94
v_exp_f32 v95,v95
v_exp_f32 v96,v96
v_exp_f32 v97,v97
v_fma_f32 v94,v94,v77,v77
v_fma_f32 v95,v95,v77,v77
v_fma_f32 v96,v96,v77,v77
v_fma_f32 v97,v97,v77,v77
D 0,56,2048
v_rcp_f32 v94,v94
v_rcp_f32 v95,v95
v_rcp_f32 v96,v96
v_rcp_f32 v97,v97
)"
R"(v_mul_f32 v18,v18,v94
v_mul_f32 v19,v19,v95
v_mul_f32 v20,v20,v96
v_mul_f32 v21,v21,v97
D 0,56,3072
v_cvt_pk_bf16_f32 v106,v18,v19
v_cvt_pk_bf16_f32 v107,v20,v21
v_fma_f32 v22,-v74,v86,v22
v_fma_f32 v23,-v74,v87,v23
v_fma_f32 v24,-v74,v88,v24
v_fma_f32 v25,-v74,v89,v25
v_mul_f32 v98,v22,v22
v_mul_f32 v99,v23,v23
s_add_u32 s58,s58,s61
s_add_u32 m0,s57,16384
s_add_u32 s56,s55,16384
D 0,56,0
v_mul_f32 v100,v24,v24
v_mul_f32 v101,v25,v25
v_fma_f32 v98,v76,v98,v75
v_fma_f32 v99,v76,v99,v75
v_fma_f32 v100,v76,v100,v75
v_fma_f32 v101,v76,v101,v75
v_mul_f32 v98,v22,v98
v_mul_f32 v99,v23,v99
D 0,56,1024
v_mul_f32 v100,v24,v100
v_mul_f32 v101,v25,v101
v_exp_f32 v98,v98
v_exp_f32 v99,v99
v_exp_f32 v100,v100
v_exp_f32 v101,v101
v_fma_f32 v98,v98,v77,v77
v_fma_f32 v99,v99,v77,v77
D 0,56,2048
v_fma_f32 v100,v100,v77,v77
v_fma_f32 v101,v101,v77,v77
v_rcp_f32 v98,v98
v_rcp_f32 v99,v99
v_rcp_f32 v100,v100
v_rcp_f32 v101,v101
v_mul_f32 v22,v22,v98
v_mul_f32 v23,v23,v99
D 0,56,3072
v_mul_f32 v24,v24,v100
v_mul_f32 v25,v25,v101
v_cvt_pk_bf16_f32 v108,v22,v23
v_cvt_pk_bf16_f32 v109,v24,v25
buffer_store_dwordx4 v[106:109],v0,s[36:39],s66 offen offset:0 sc0 sc1
v_fma_f32 v26,-v74,v90,v26
v_fma_f32 v27,-v74,v91,v27
v_fma_f32 v28,-v74,v92,v28
v_fma_f32 v29,-v74,v93,v29
s_add_u32 s55,s55,s60
s_sub_u32 s57,s84,s57
s_add_u32 s54,s54,s46
v_mul_f32 v102,v26,v26
v_mul_f32 v103,v27,v27
v_mul_f32 v104,v28,v28
v_mul_f32 v105,v29,v29
v_fma_f32 v102,v76,v102,v75
v_fma_f32 v103,v76,v103,v75
v_fma_f32 v104,v76,v104,v75
v_fma_f32 v105,v76,v105,v75
v_mul_f32 v102,v26,v102
v_mul_f32 v103,v27,v103
v_mul_f32 v104,v28,v104
v_mul_f32 v105,v29,v105
v_exp_f32 v102,v102
v_exp_f32 v103,v103
v_exp_f32 v104,v104
v_exp_f32 v105,v105
v_fma_f32 v102,v102,v77,v77
v_fma_f32 v103,v103,v77,v77
v_fma_f32 v104,v104,v77,v77
v_fma_f32 v105,v105,v77,v77
v_rcp_f32 v102,v102
v_rcp_f32 v103,v103
v_rcp_f32 v104,v104
v_rcp_f32 v105,v105
v_mul_f32 v26,v26,v102
v_mul_f32 v27,v27,v103
v_mul_f32 v28,v28,v104
v_mul_f32 v29,v29,v105
v_cvt_pk_bf16_f32 v110,v26,v27
v_cvt_pk_bf16_f32 v111,v28,v29
buffer_store_dwordx2 v[110:111],v10,s[36:39],s66 offen offset:0 sc0 sc1
s_mov_b32 s72,s69
s_mov_b32 s69,0x7fff0000
s_mov_b32 s66,0x7fff0000
s_mov_b32 s76,0x7fff0000
s_branch Lg3_gox_%=
Lg3_xn0_%=:
s_cmp_ge_u32 s54,256
s_cbranch_scc1 Lg3_fin0_%=
s_add_u32 m0,s59,4096
s_nop 0
buffer_load_dword v8,s[36:39],s58 offen offset:0 sc1 lds
buffer_store_dword v4,v3,s[36:39],s71 offen offset:0 sc0 sc1
s_add_u32 m0,s59,5120
s_nop 0
buffer_load_dword v8,s[36:39],s58 offen offset:0 sc1 lds
buffer_store_dword v4,v3,s[36:39],s71 offen offset:0 sc0 sc1
s_mov_b32 s83,0
L 0
v_fma_f32 v18,-v74,v82,v18
v_fma_f32 v19,-v74,v83,v19
v_fma_f32 v20,-v74,v84,v20
v_fma_f32 v21,-v74,v85,v21
v_mul_f32 v94,v18,v18
v_mul_f32 v95,v19,v19
v_mul_f32 v96,v20,v20
v_mul_f32 v97,v21,v21
v_fma_f32 v94,v76,v94,v75
v_fma_f32 v95,v76,v95,v75
v_fma_f32 v96,v76,v96,v75
v_fma_f32 v97,v76,v97,v75
v_mul_f32 v94,v18,v94
v_mul_f32 v95,v19,v95
v_mul_f32 v96,v20,v96
v_mul_f32 v97,v21,v97
v_exp_f32 v94,v94
v_exp_f32 v95,v95
v_exp_f32 v96,v96
v_exp_f32 v97,v97
v_fma_f32 v94,v94,v77,v77
v_fma_f32 v95,v95,v77,v77
v_fma_f32 v96,v96,v77,v77
v_fma_f32 v97,v97,v77,v77
v_rcp_f32 v94,v94
v_rcp_f32 v95,v95
v_rcp_f32 v96,v96
v_rcp_f32 v97,v97
v_mul_f32 v18,v18,v94
v_mul_f32 v19,v19,v95
v_mul_f32 v20,v20,v96
v_mul_f32 v21,v21,v97
v_cvt_pk_bf16_f32 v106,v18,v19
v_cvt_pk_bf16_f32 v107,v20,v21
v_fma_f32 v22,-v74,v86,v22
v_fma_f32 v23,-v74,v87,v23
v_fma_f32 v24,-v74,v88,v24
v_fma_f32 v25,-v74,v89,v25
v_mul_f32 v98,v22,v22
v_mul_f32 v99,v23,v23
v_mul_f32 v100,v24,v24
v_mul_f32 v101,v25,v25
v_fma_f32 v98,v76,v98,v75
v_fma_f32 v99,v76,v99,v75
v_fma_f32 v100,v76,v100,v75
v_fma_f32 v101,v76,v101,v75
v_mul_f32 v98,v22,v98
v_mul_f32 v99,v23,v99
v_mul_f32 v100,v24,v100
v_mul_f32 v101,v25,v101
v_exp_f32 v98,v98
v_exp_f32 v99,v99
v_exp_f32 v100,v100
v_exp_f32 v101,v101
v_fma_f32 v98,v98,v77,v77
v_fma_f32 v99,v99,v77,v77
v_fma_f32 v100,v100,v77,v77
v_fma_f32 v101,v101,v77,v77
v_rcp_f32 v98,v98
v_rcp_f32 v99,v99
v_rcp_f32 v100,v100
v_rcp_f32 v101,v101
v_mul_f32 v22,v22,v98
v_mul_f32 v23,v23,v99
v_mul_f32 v24,v24,v100
v_mul_f32 v25,v25,v101
v_cvt_pk_bf16_f32 v108,v22,v23
v_cvt_pk_bf16_f32 v109,v24,v25
buffer_store_dwordx4 v[106:109],v0,s[36:39],s66 offen offset:0 sc0 sc1
v_fma_f32 v26,-v74,v90,v26
v_fma_f32 v27,-v74,v91,v27
v_fma_f32 v28,-v74,v92,v28
v_fma_f32 v29,-v74,v93,v29
v_mul_f32 v102,v26,v26
v_mul_f32 v103,v27,v27
v_mul_f32 v104,v28,v28
v_mul_f32 v105,v29,v29
v_fma_f32 v102,v76,v102,v75
v_fma_f32 v103,v76,v103,v75
v_fma_f32 v104,v76,v104,v75
v_fma_f32 v105,v76,v105,v75
v_mul_f32 v102,v26,v102
v_mul_f32 v103,v27,v103
v_mul_f32 v104,v28,v104
v_mul_f32 v105,v29,v105
v_exp_f32 v102,v102
v_exp_f32 v103,v103
v_exp_f32 v104,v104
v_exp_f32 v105,v105
v_fma_f32 v102,v102,v77,v77
v_fma_f32 v103,v103,v77,v77
v_fma_f32 v104,v104,v77,v77
v_fma_f32 v105,v105,v77,v77
v_rcp_f32 v102,v102
v_rcp_f32 v103,v103
v_rcp_f32 v104,v104
v_rcp_f32 v105,v105
v_mul_f32 v26,v26,v102
v_mul_f32 v27,v27,v103
v_mul_f32 v28,v28,v104
v_mul_f32 v29,v29,v105
v_cvt_pk_bf16_f32 v110,v26,v27
v_cvt_pk_bf16_f32 v111,v28,v29
buffer_store_dwordx2 v[110:111],v10,s[36:39],s66 offen offset:0 sc0 sc1
s_mov_b32 s72,s69
s_mov_b32 s69,0x7fff0000
s_mov_b32 s66,0x7fff0000
s_mov_b32 s76,0x7fff0000
s_branch Lg3_poll_%=
Lg3_fin0_%=:
L 0
v_fma_f32 v18,-v74,v82,v18
v_fma_f32 v19,-v74,v83,v19
v_fma_f32 v20,-v74,v84,v20
v_fma_f32 v21,-v74,v85,v21
v_mul_f32 v94,v18,v18
v_mul_f32 v95,v19,v19
v_mul_f32 v96,v20,v20
v_mul_f32 v97,v21,v21
v_fma_f32 v94,v76,v94,v75
v_fma_f32 v95,v76,v95,v75
v_fma_f32 v96,v76,v96,v75
v_fma_f32 v97,v76,v97,v75
v_mul_f32 v94,v18,v94
v_mul_f32 v95,v19,v95
v_mul_f32 v96,v20,v96
v_mul_f32 v97,v21,v97
v_exp_f32 v94,v94
v_exp_f32 v95,v95
v_exp_f32 v96,v96
v_exp_f32 v97,v97
v_fma_f32 v94,v94,v77,v77
v_fma_f32 v95,v95,v77,v77
v_fma_f32 v96,v96,v77,v77
v_fma_f32 v97,v97,v77,v77
v_rcp_f32 v94,v94
v_rcp_f32 v95,v95
v_rcp_f32 v96,v96
v_rcp_f32 v97,v97
v_mul_f32 v18,v18,v94
v_mul_f32 v19,v19,v95
v_mul_f32 v20,v20,v96
v_mul_f32 v21,v21,v97
v_cvt_pk_bf16_f32 v106,v18,v19
v_cvt_pk_bf16_f32 v107,v20,v21
v_fma_f32 v22,-v74,v86,v22
v_fma_f32 v23,-v74,v87,v23
v_fma_f32 v24,-v74,v88,v24
v_fma_f32 v25,-v74,v89,v25
v_mul_f32 v98,v22,v22
v_mul_f32 v99,v23,v23
v_mul_f32 v100,v24,v24
v_mul_f32 v101,v25,v25
v_fma_f32 v98,v76,v98,v75
v_fma_f32 v99,v76,v99,v75
v_fma_f32 v100,v76,v100,v75
v_fma_f32 v101,v76,v101,v75
v_mul_f32 v98,v22,v98
v_mul_f32 v99,v23,v99
v_mul_f32 v100,v24,v100
v_mul_f32 v101,v25,v101
v_exp_f32 v98,v98
v_exp_f32 v99,v99
v_exp_f32 v100,v100
v_exp_f32 v101,v101
v_fma_f32 v98,v98,v77,v77
v_fma_f32 v99,v99,v77,v77
v_fma_f32 v100,v100,v77,v77
v_fma_f32 v101,v101,v77,v77
v_rcp_f32 v98,v98
v_rcp_f32 v99,v99
v_rcp_f32 v100,v100
v_rcp_f32 v101,v101
v_mul_f32 v22,v22,v98
v_mul_f32 v23,v23,v99
v_mul_f32 v24,v24,v100
v_mul_f32 v25,v25,v101
v_cvt_pk_bf16_f32 v108,v22,v23
v_cvt_pk_bf16_f32 v109,v24,v25
buffer_store_dwordx4 v[106:109],v0,s[36:39],s66 offen offset:0 sc0 sc1
v_fma_f32 v26,-v74,v90,v26
v_fma_f32 v27,-v74,v91,v27
v_fma_f32 v28,-v74,v92,v28
v_fma_f32 v29,-v74,v93,v29
v_mul_f32 v102,v26,v26
v_mul_f32 v103,v27,v27
v_mul_f32 v104,v28,v28
v_mul_f32 v105,v29,v29
v_fma_f32 v102,v76,v102,v75
v_fma_f32 v103,v76,v103,v75
v_fma_f32 v104,v76,v104,v75
v_fma_f32 v105,v76,v105,v75
v_mul_f32 v102,v26,v102
v_mul_f32 v103,v27,v103
v_mul_f32 v104,v28,v104
v_mul_f32 v105,v29,v105
v_exp_f32 v102,v102
v_exp_f32 v103,v103
v_exp_f32 v104,v104
v_exp_f32 v105,v105
v_fma_f32 v102,v102,v77,v77
v_fma_f32 v103,v103,v77,v77
v_fma_f32 v104,v104,v77,v77
v_fma_f32 v105,v105,v77,v77
v_rcp_f32 v102,v102
v_rcp_f32 v103,v103
v_rcp_f32 v104,v104
v_rcp_f32 v105,v105
v_mul_f32 v26,v26,v102
v_mul_f32 v27,v27,v103
v_mul_f32 v28,v28,v104
v_mul_f32 v29,v29,v105
v_cvt_pk_bf16_f32 v110,v26,v27
v_cvt_pk_bf16_f32 v111,v28,v29
buffer_store_dwordx2 v[110:111],v10,s[36:39],s66 offen offset:0 sc0 sc1
s_mov_b32 s72,s69
s_mov_b32 s69,0x7fff0000
s_mov_b32 s66,0x7fff0000
s_mov_b32 s76,0x7fff0000
s_branch Lg3_done_%=
Lg3_K1_%=:
Lg3_L1_%=:
v_mfma_f32_16x16x32_bf16 v[38:41],a[8:11],v[42:45],0
s_add_u32 m0,s59,3072
R 62,2,5120
v_add_f32_dpp v115,v114,v114 quad_perm:[1,0,3,2] row_mask:0xf bank_mask:0xf
M 38,a[20:23],46
buffer_load_dword v8,s[36:39],s58 offen offset:0 sc1 lds
v_mfma_f32_16x16x32_bf16 v[34:37],a[4:7],v[42:45],0
s_mov_b32 s67,s65
v_add_f32_dpp v115,v115,v115 quad_perm:[2,3,0,1] row_mask:0xf bank_mask:0xf
v_fma_f32 v18,-v74,v82,v18
M 34,a[16:19],46
s_add_u32 s65,s65,s62
R 66,2,6144
v_fma_f32 v19,-v74,v83,v19
v_mfma_f32_16x16x32_bf16 v[30:33],a[0:3],v[42:45],0
v_add_f32_dpp v118,v115,v115 row_half_mirror row_mask:0xf bank_mask:0xf
v_fma_f32 v20,-v74,v84,v20
v_fma_f32 v21,-v74,v85,v21
M 30,a[12:15],46
L 3
v_mul_f32 v94,v18,v18
v_mul_f32 v95,v19,v19
M 30,a[24:27],50
R 70,2,7168
v_mov_b32_dpp v115,v118 row_ror:8 row_mask:0xf bank_mask:0xf
M 30,a[36:39],54
v_fma_f32 v115,-v118,v118,v115
v_max_f32 v115,s87,v115
M 34,a[28:31],50
v_rsq_f32 v116,v115
M 34,a[40:43],54
R 42,2,8192
v_fma_f32 v115,v15,v115,v16
v_mul_f32 v118,v17,v118
M 38,a[32:35],50
v_mul_f32 v119,v116,v116
v_fma_f32 v115,v14,v119,v115
M 38,a[44:47],54
L 3
v_fma_f32 v118,v116,v115,v118
v_mul_f32 v96,v20,v20
M 38,a[56:59],58
R 46,2,9216
v_mul_f32 v97,v21,v21
M 38,a[68:71],62
ds_write_b32 v12,v118 offset:2048
M 34,a[52:55],58
v_fma_f32 v94,v76,v94,v75
v_fma_f32 v95,v76,v95,v75
v_fma_f32 v96,v76,v96,v75
M 34,a[64:67],62
R 50,2,10240
v_fma_f32 v97,v76,v97,v75
M 30,a[48:51],58
v_mul_f32 v94,v18,v94
v_mul_f32 v95,v19,v95
M 30,a[60:63],62
L 4
v_exp_f32 v94,v94
M 30,a[72:75],66
R 54,2,11264
v_mul_f32 v96,v20,v96
M 30,a[84:87],70
v_exp_f32 v95,v95
v_mul_f32 v97,v21,v97
M 34,a[76:79],66
v_exp_f32 v96,v96
M 34,a[88:91],70
R 58,2,12288
v_fma_f32 v94,v94,v77,v77
M 38,a[80:83],66
v_exp_f32 v97,v97
M 38,a[92:95],70
L 4
v_rcp_f32 v94,v94
M 38,a[104:107],42
R 62,2,13312
v_fma_f32 v95,v95,v77,v77
v_fma_f32 v96,v96,v77,v77
M 38,a[116:119],46
v_rcp_f32 v95,v95
M 34,a[100:103],42
v_rcp_f32 v96,v96
M 34,a[112:115],46
R 66,2,14336
v_fma_f32 v97,v97,v77,v77
M 30,a[96:99],42
v_rcp_f32 v97,v97
M 30,a[108:111],46
L 3
v_mul_f32 v18,v18,v94
v_mul_f32 v19,v19,v95
v_mul_f32 v20,v20,v96
M 30,a[120:123],50
R 70,2,15360
v_mul_f32 v21,v21,v97
M 30,a[132:135],54
s_waitcnt vmcnt(0)
v_cvt_pk_bf16_f32 v106,v18,v19
v_cvt_pk_bf16_f32 v107,v20,v21
M 34,a[124:127],50
s_barrier
ds_read_b32 v7,v6 offset:3072
v_fma_f32 v22,-v74,v86,v22
M 34,a[136:139],54
R 42,2,16384
v_fma_f32 v23,-v74,v87,v23
M 38,a[128:131],50
buffer_store_dword v4,v3,s[36:39],s70 offen offset:0 sc0 sc1
v_fma_f32 v24,-v74,v88,v24
M 38,a[140:143],54
L 4
v_fma_f32 v25,-v74,v89,v25
v_mul_f32 v98,v22,v22
M 38,a[152:155],58
s_mov_b32 s70,0x7fff0000
R 46,2,17408
v_mul_f32 v99,v23,v23
M 38,a[164:167],62
v_mul_f32 v100,v24,v24
v_mul_f32 v101,v25,v25
M 34,a[148:151],58
L 2
v_cmp_eq_u32 vcc,s53,v7
v_fma_f32 v98,v76,v98,v75
M 34,a[160:163],62
R 50,2,18432
v_fma_f32 v99,v76,v99,v75
v_fma_f32 v100,v76,v100,v75
M 30,a[144:147],58
v_fma_f32 v101,v76,v101,v75
v_mul_f32 v98,v22,v98
M 30,a[156:159],62
s_cmp_eq_u32 vcc_lo,-1
s_nop 0
v_mul_f32 v99,v23,v99
s_cbranch_scc0 Lg3_X1_%=
M 30,a[168:171],66
s_add_u32 m0,s57,0
s_add_u32 s56,s55,0
s_add_u32 s58,s58,s61
R 54,2,19456
M 30,a[180:183],70
buffer_load_dword v114,v11,s[36:39],s73 offen offset:0 sc1
M 34,a[172:175],66
D 0,56,0
v_mul_f32 v100,v24,v100
M 34,a[184:187],70
s_add_u32 s54,s54,s46
R 58,2,20480
v_mul_f32 v101,v25,v101
M 38,a[176:179],66
s_add_u32 s73,s73,s64
v_exp_f32 v98,v98
M 38,a[188:191],70
L 3
v_exp_f32 v99,v99
M 38,a[200:203],42
R 62,2,21504
v_fma_f32 v98,v98,v77,v77
M 38,a[212:215],46
D 0,56,1024
v_fma_f32 v99,v99,v77,v77
M 34,a[196:199],42
v_exp_f32 v100,v100
M 34,a[208:211],46
R 66,2,22528
v_fma_f32 v100,v100,v77,v77
M 30,a[192:195],42
D 0,56,2048
M 30,a[204:207],46
L 3
v_exp_f32 v101,v101
)"
R"(M 30,a[216:219],50
R 70,2,23552
v_rcp_f32 v98,v98
M 30,a[228:231],54
v_rcp_f32 v99,v99
M 34,a[220:223],50
D 0,56,3072
M 34,a[232:235],54
s_add_u32 m0,s59,1024
R 42,2,24576
v_fma_f32 v101,v101,v77,v77
M 38,a[224:227],50
buffer_load_dword v8,s[36:39],s58 offen offset:0 sc1 lds
M 38,a[236:239],54
L 3
v_rcp_f32 v100,v100
v_mul_f32 v22,v22,v98
M 38,a[248:251],58
s_add_u32 m0,s57,16384
R 46,2,25600
v_mul_f32 v23,v23,v99
M 38,v[132:135],62
s_add_u32 s56,s55,16384
v_rcp_f32 v101,v101
M 34,a[244:247],58
D 0,56,0
M 34,v[128:131],62
s_add_u32 s55,s55,s60
R 50,2,26624
v_mul_f32 v24,v24,v100
M 30,a[240:243],58
s_sub_u32 s57,s84,s57
v_mul_f32 v25,v25,v101
v_cvt_pk_bf16_f32 v108,v22,v23
v_cvt_pk_bf16_f32 v109,v24,v25
M 30,a[252:255],62
L 3
buffer_store_dwordx4 v[106:109],v0,s[36:39],s66 offen offset:0 sc0 sc1
v_fma_f32 v26,-v74,v90,v26
M 30,v[136:139],66
R 54,2,27648
v_fma_f32 v27,-v74,v91,v27
M 30,v[148:151],70
v_fma_f32 v28,-v74,v92,v28
v_fma_f32 v29,-v74,v93,v29
M 34,v[140:143],66
D 0,56,1024
M 34,v[152:155],70
R 58,2,28672
v_mul_f32 v102,v26,v26
v_mul_f32 v103,v27,v27
M 38,v[144:147],66
v_mul_f32 v104,v28,v28
v_mul_f32 v105,v29,v29
M 38,v[156:159],70
L 3
v_fma_f32 v102,v76,v102,v75
v_fma_f32 v103,v76,v103,v75
M 38,v[168:171],42
R 62,2,29696
v_fma_f32 v104,v76,v104,v75
M 38,v[180:183],46
D 0,56,2048
M 34,v[164:167],42
v_fma_f32 v105,v76,v105,v75
v_mul_f32 v102,v26,v102
v_mul_f32 v103,v27,v103
M 34,v[176:179],46
R 66,2,30720
v_mul_f32 v104,v28,v104
M 30,v[160:163],42
v_exp_f32 v102,v102
M 30,v[172:175],46
L 3
D 0,56,3072
M 30,v[184:187],50
s_nop 0
R 70,2,31744
v_mul_f32 v105,v29,v105
M 30,v[196:199],54
s_waitcnt vmcnt(5)
v_exp_f32 v103,v103
v_fma_f32 v102,v102,v77,v77
M 34,v[188:191],50
s_barrier
buffer_store_dword v4,v3,s[36:39],s70 offen offset:0 sc0 sc1
M 34,v[200:203],54
s_mov_b32 s70,s69
R 42,1,0
ds_read_b32 v7,v6 offset:1024
M 38,v[192:195],50
s_mov_b32 s69,s68
R 78,13,2048
v_fma_f32 v103,v103,v77,v77
M 38,v[204:207],54
L 5
v_exp_f32 v104,v104
M 38,v[216:219],58
s_add_u32 s68,s68,s63
R 46,1,1024
v_exp_f32 v105,v105
M 38,v[228:231],62
v_rcp_f32 v102,v102
M 34,v[212:215],58
v_rcp_f32 v103,v103
M 34,v[224:227],62
L 2
R 50,1,2048
v_fma_f32 v104,v104,v77,v77
M 30,v[208:211],58
v_rcp_f32 v104,v104
M 30,v[220:223],62
s_nop 0
v_fma_f32 v105,v105,v77,v77
v_rcp_f32 v105,v105
M 30,v[232:235],66
R 54,1,3072
v_mul_f32 v26,v26,v102
M 30,v[244:247],70
v_mul_f32 v27,v27,v103
v_mul_f32 v28,v28,v104
M 34,v[236:239],66
v_cmp_eq_u32 vcc,s53,v7
v_mul_f32 v29,v29,v105
M 34,v[248:251],70
R 58,1,4096
v_cvt_pk_bf16_f32 v110,v26,v27
v_cvt_pk_bf16_f32 v111,v28,v29
M 38,v[240:243],66
L 3
s_cmp_eq_u32 vcc_lo,-1
buffer_store_dwordx2 v[110:111],v10,s[36:39],s66 offen offset:0 sc0 sc1
M 38,v[252:255],70
buffer_store_dword v4,v3,s[36:39],s71 offen offset:0 sc0 sc1
s_cbranch_scc1 Lg3_F0_%=
s_branch Lg3_K0_%=
Lg3_X1_%=:
M 30,a[168:171],66
s_add_u32 m0,s59,1024
R 54,2,19456
v_mul_f32 v100,v24,v100
M 30,a[180:183],70
buffer_load_dword v8,s[36:39],s58 offen offset:0 sc1 lds
M 34,a[172:175],66
v_exp_f32 v98,v98
M 34,a[184:187],70
R 58,2,20480
v_mul_f32 v101,v25,v101
M 38,a[176:179],66
v_exp_f32 v99,v99
M 38,a[188:191],70
L 3
v_exp_f32 v100,v100
M 38,a[200:203],42
R 62,2,21504
v_fma_f32 v98,v98,v77,v77
M 38,a[212:215],46
v_exp_f32 v101,v101
M 34,a[196:199],42
v_rcp_f32 v98,v98
M 34,a[208:211],46
R 66,2,22528
v_fma_f32 v99,v99,v77,v77
M 30,a[192:195],42
v_rcp_f32 v99,v99
M 30,a[204:207],46
L 3
v_fma_f32 v100,v100,v77,v77
v_fma_f32 v101,v101,v77,v77
M 30,a[216:219],50
R 70,2,23552
v_mul_f32 v22,v22,v98
M 30,a[228:231],54
v_rcp_f32 v100,v100
M 34,a[220:223],50
v_rcp_f32 v101,v101
M 34,a[232:235],54
R 42,2,24576
v_mul_f32 v23,v23,v99
M 38,a[224:227],50
v_mul_f32 v24,v24,v100
v_mul_f32 v25,v25,v101
M 38,a[236:239],54
L 3
v_cvt_pk_bf16_f32 v108,v22,v23
v_cvt_pk_bf16_f32 v109,v24,v25
M 38,a[248:251],58
R 46,2,25600
v_fma_f32 v26,-v74,v90,v26
M 38,v[132:135],62
buffer_store_dwordx4 v[106:109],v0,s[36:39],s66 offen offset:0 sc0 sc1
v_fma_f32 v27,-v74,v91,v27
M 34,a[244:247],58
v_fma_f32 v28,-v74,v92,v28
v_fma_f32 v29,-v74,v93,v29
M 34,v[128:131],62
R 50,2,26624
v_mul_f32 v102,v26,v26
M 30,a[240:243],58
v_mul_f32 v103,v27,v27
v_mul_f32 v104,v28,v28
M 30,a[252:255],62
L 3
v_mul_f32 v105,v29,v29
v_fma_f32 v102,v76,v102,v75
M 30,v[136:139],66
R 54,2,27648
v_fma_f32 v103,v76,v103,v75
M 30,v[148:151],70
v_fma_f32 v104,v76,v104,v75
v_fma_f32 v105,v76,v105,v75
M 34,v[140:143],66
v_mul_f32 v102,v26,v102
v_mul_f32 v103,v27,v103
M 34,v[152:155],70
R 58,2,28672
v_mul_f32 v104,v28,v104
M 38,v[144:147],66
v_exp_f32 v102,v102
M 38,v[156:159],70
L 3
v_exp_f32 v103,v103
M 38,v[168:171],42
R 62,2,29696
v_mul_f32 v105,v29,v105
M 38,v[180:183],46
v_exp_f32 v104,v104
M 34,v[164:167],42
v_exp_f32 v105,v105
M 34,v[176:179],46
R 66,2,30720
v_fma_f32 v102,v102,v77,v77
M 30,v[160:163],42
v_rcp_f32 v102,v102
M 30,v[172:175],46
L 3
v_fma_f32 v103,v103,v77,v77
v_fma_f32 v104,v104,v77,v77
M 30,v[184:187],50
s_nop 0
R 70,2,31744
v_fma_f32 v105,v105,v77,v77
M 30,v[196:199],54
s_waitcnt vmcnt(1)
v_rcp_f32 v103,v103
M 34,v[188:191],50
s_barrier
buffer_store_dword v4,v3,s[36:39],s70 offen offset:0 sc0 sc1
M 34,v[200:203],54
s_mov_b32 s70,s69
ds_read_b32 v7,v6 offset:1024
R 78,13,2048
M 38,v[192:195],50
s_mov_b32 s69,s68
v_rcp_f32 v104,v104
M 38,v[204:207],54
L 4
v_rcp_f32 v105,v105
M 38,v[216:219],58
s_add_u32 s68,s68,s63
v_mul_f32 v26,v26,v102
v_mul_f32 v27,v27,v103
M 38,v[228:231],62
v_mul_f32 v28,v28,v104
v_mul_f32 v29,v29,v105
M 34,v[212:215],58
v_cvt_pk_bf16_f32 v110,v26,v27
v_cvt_pk_bf16_f32 v111,v28,v29
M 34,v[224:227],62
L 1
buffer_store_dwordx2 v[110:111],v10,s[36:39],s66 offen offset:0 sc0 sc1
M 30,v[208:211],58
v_cmp_eq_u32 vcc,s53,v7
M 30,v[220:223],62
s_nop 0
M 30,v[232:235],66
M 30,v[244:247],70
M 34,v[236:239],66
M 34,v[248:251],70
M 38,v[240:243],66
M 38,v[252:255],70
s_mov_b32 s80,0
s_cmp_eq_u32 vcc_lo,-1
s_cbranch_scc0 Lg3_xn1_%=
buffer_load_dword v114,v11,s[36:39],s73 offen offset:0 sc1
L 0
v_fma_f32 v30,-v78,v82,v30
v_fma_f32 v31,-v78,v83,v31
v_fma_f32 v32,-v78,v84,v32
v_fma_f32 v33,-v78,v85,v33
v_mul_f32 v94,v30,v30
v_mul_f32 v95,v31,v31
v_mul_f32 v96,v32,v32
v_mul_f32 v97,v33,v33
s_add_u32 s73,s73,s64
s_add_u32 m0,s57,0
s_add_u32 s56,s55,0
D 0,56,0
v_fma_f32 v94,v80,v94,v79
v_fma_f32 v95,v80,v95,v79
v_fma_f32 v96,v80,v96,v79
v_fma_f32 v97,v80,v97,v79
v_mul_f32 v94,v30,v94
v_mul_f32 v95,v31,v95
v_mul_f32 v96,v32,v96
v_mul_f32 v97,v33,v97
D 0,56,1024
v_exp_f32 v94,v94
v_exp_f32 v95,v95
v_exp_f32 v96,v96
v_exp_f32 v97,v97
v_fma_f32 v94,v94,v81,v81
v_fma_f32 v95,v95,v81,v81
v_fma_f32 v96,v96,v81,v81
v_fma_f32 v97,v97,v81,v81
D 0,56,2048
v_rcp_f32 v94,v94
v_rcp_f32 v95,v95
v_rcp_f32 v96,v96
v_rcp_f32 v97,v97
v_mul_f32 v30,v30,v94
v_mul_f32 v31,v31,v95
v_mul_f32 v32,v32,v96
v_mul_f32 v33,v33,v97
D 0,56,3072
v_cvt_pk_bf16_f32 v106,v30,v31
v_cvt_pk_bf16_f32 v107,v32,v33
v_fma_f32 v34,-v78,v86,v34
v_fma_f32 v35,-v78,v87,v35
v_fma_f32 v36,-v78,v88,v36
v_fma_f32 v37,-v78,v89,v37
v_mul_f32 v98,v34,v34
v_mul_f32 v99,v35,v35
s_add_u32 s58,s58,s61
s_add_u32 m0,s57,16384
s_add_u32 s56,s55,16384
D 0,56,0
v_mul_f32 v100,v36,v36
v_mul_f32 v101,v37,v37
v_fma_f32 v98,v80,v98,v79
v_fma_f32 v99,v80,v99,v79
v_fma_f32 v100,v80,v100,v79
v_fma_f32 v101,v80,v101,v79
v_mul_f32 v98,v34,v98
v_mul_f32 v99,v35,v99
D 0,56,1024
v_mul_f32 v100,v36,v100
v_mul_f32 v101,v37,v101
v_exp_f32 v98,v98
v_exp_f32 v99,v99
v_exp_f32 v100,v100
v_exp_f32 v101,v101
v_fma_f32 v98,v98,v81,v81
v_fma_f32 v99,v99,v81,v81
D 0,56,2048
v_fma_f32 v100,v100,v81,v81
v_fma_f32 v101,v101,v81,v81
v_rcp_f32 v98,v98
v_rcp_f32 v99,v99
v_rcp_f32 v100,v100
v_rcp_f32 v101,v101
v_mul_f32 v34,v34,v98
v_mul_f32 v35,v35,v99
D 0,56,3072
v_mul_f32 v36,v36,v100
v_mul_f32 v37,v37,v101
v_cvt_pk_bf16_f32 v108,v34,v35
v_cvt_pk_bf16_f32 v109,v36,v37
buffer_store_dwordx4 v[106:109],v0,s[36:39],s67 offen offset:0 sc0 sc1
v_fma_f32 v38,-v78,v90,v38
v_fma_f32 v39,-v78,v91,v39
v_fma_f32 v40,-v78,v92,v40
v_fma_f32 v41,-v78,v93,v41
s_add_u32 s55,s55,s60
s_sub_u32 s57,s84,s57
s_add_u32 s54,s54,s46
v_mul_f32 v102,v38,v38
v_mul_f32 v103,v39,v39
v_mul_f32 v104,v40,v40
v_mul_f32 v105,v41,v41
v_fma_f32 v102,v80,v102,v79
v_fma_f32 v103,v80,v103,v79
v_fma_f32 v104,v80,v104,v79
v_fma_f32 v105,v80,v105,v79
v_mul_f32 v102,v38,v102
v_mul_f32 v103,v39,v103
v_mul_f32 v104,v40,v104
v_mul_f32 v105,v41,v105
v_exp_f32 v102,v102
v_exp_f32 v103,v103
v_exp_f32 v104,v104
v_exp_f32 v105,v105
v_fma_f32 v102,v102,v81,v81
v_fma_f32 v103,v103,v81,v81
v_fma_f32 v104,v104,v81,v81
v_fma_f32 v105,v105,v81,v81
v_rcp_f32 v102,v102
v_rcp_f32 v103,v103
v_rcp_f32 v104,v104
v_rcp_f32 v105,v105
v_mul_f32 v38,v38,v102
v_mul_f32 v39,v39,v103
v_mul_f32 v40,v40,v104
v_mul_f32 v41,v41,v105
v_cvt_pk_bf16_f32 v110,v38,v39
v_cvt_pk_bf16_f32 v111,v40,v41
buffer_store_dwordx2 v[110:111],v10,s[36:39],s67 offen offset:0 sc0 sc1
s_mov_b32 s72,s69
s_mov_b32 s69,0x7fff0000
s_mov_b32 s67,0x7fff0000
s_mov_b32 s77,0x7fff0000
s_branch Lg3_gox_%=
Lg3_xn1_%=:
s_cmp_ge_u32 s54,256
s_cbranch_scc1 Lg3_fin1_%=
s_add_u32 m0,s59,4096
s_nop 0
buffer_load_dword v8,s[36:39],s58 offen offset:0 sc1 lds
buffer_store_dword v4,v3,s[36:39],s71 offen offset:0 sc0 sc1
s_add_u32 m0,s59,5120
s_nop 0
buffer_load_dword v8,s[36:39],s58 offen offset:0 sc1 lds
buffer_store_dword v4,v3,s[36:39],s71 offen offset:0 sc0 sc1
s_mov_b32 s83,0
L 0
v_fma_f32 v30,-v78,v82,v30
v_fma_f32 v31,-v78,v83,v31
v_fma_f32 v32,-v78,v84,v32
v_fma_f32 v33,-v78,v85,v33
v_mul_f32 v94,v30,v30
v_mul_f32 v95,v31,v31
v_mul_f32 v96,v32,v32
v_mul_f32 v97,v33,v33
v_fma_f32 v94,v80,v94,v79
v_fma_f32 v95,v80,v95,v79
v_fma_f32 v96,v80,v96,v79
v_fma_f32 v97,v80,v97,v79
v_mul_f32 v94,v30,v94
v_mul_f32 v95,v31,v95
v_mul_f32 v96,v32,v96
v_mul_f32 v97,v33,v97
v_exp_f32 v94,v94
v_exp_f32 v95,v95
v_exp_f32 v96,v96
v_exp_f32 v97,v97
v_fma_f32 v94,v94,v81,v81
v_fma_f32 v95,v95,v81,v81
v_fma_f32 v96,v96,v81,v81
v_fma_f32 v97,v97,v81,v81
v_rcp_f32 v94,v94
v_rcp_f32 v95,v95
v_rcp_f32 v96,v96
v_rcp_f32 v97,v97
v_mul_f32 v30,v30,v94
v_mul_f32 v31,v31,v95
v_mul_f32 v32,v32,v96
v_mul_f32 v33,v33,v97
v_cvt_pk_bf16_f32 v106,v30,v31
v_cvt_pk_bf16_f32 v107,v32,v33
v_fma_f32 v34,-v78,v86,v34
v_fma_f32 v35,-v78,v87,v35
v_fma_f32 v36,-v78,v88,v36
v_fma_f32 v37,-v78,v89,v37
v_mul_f32 v98,v34,v34
v_mul_f32 v99,v35,v35
v_mul_f32 v100,v36,v36
v_mul_f32 v101,v37,v37
v_fma_f32 v98,v80,v98,v79
v_fma_f32 v99,v80,v99,v79
v_fma_f32 v100,v80,v100,v79
v_fma_f32 v101,v80,v101,v79
v_mul_f32 v98,v34,v98
v_mul_f32 v99,v35,v99
v_mul_f32 v100,v36,v100
v_mul_f32 v101,v37,v101
v_exp_f32 v98,v98
v_exp_f32 v99,v99
v_exp_f32 v100,v100
v_exp_f32 v101,v101
v_fma_f32 v98,v98,v81,v81
v_fma_f32 v99,v99,v81,v81
v_fma_f32 v100,v100,v81,v81
v_fma_f32 v101,v101,v81,v81
v_rcp_f32 v98,v98
v_rcp_f32 v99,v99
v_rcp_f32 v100,v100
v_rcp_f32 v101,v101
v_mul_f32 v34,v34,v98
v_mul_f32 v35,v35,v99
v_mul_f32 v36,v36,v100
v_mul_f32 v37,v37,v101
v_cvt_pk_bf16_f32 v108,v34,v35
v_cvt_pk_bf16_f32 v109,v36,v37
buffer_store_dwordx4 v[106:109],v0,s[36:39],s67 offen offset:0 sc0 sc1
v_fma_f32 v38,-v78,v90,v38
v_fma_f32 v39,-v78,v91,v39
v_fma_f32 v40,-v78,v92,v40
v_fma_f32 v41,-v78,v93,v41
v_mul_f32 v102,v38,v38
v_mul_f32 v103,v39,v39
)"
R"(v_mul_f32 v104,v40,v40
v_mul_f32 v105,v41,v41
v_fma_f32 v102,v80,v102,v79
v_fma_f32 v103,v80,v103,v79
v_fma_f32 v104,v80,v104,v79
v_fma_f32 v105,v80,v105,v79
v_mul_f32 v102,v38,v102
v_mul_f32 v103,v39,v103
v_mul_f32 v104,v40,v104
v_mul_f32 v105,v41,v105
v_exp_f32 v102,v102
v_exp_f32 v103,v103
v_exp_f32 v104,v104
v_exp_f32 v105,v105
v_fma_f32 v102,v102,v81,v81
v_fma_f32 v103,v103,v81,v81
v_fma_f32 v104,v104,v81,v81
v_fma_f32 v105,v105,v81,v81
v_rcp_f32 v102,v102
v_rcp_f32 v103,v103
v_rcp_f32 v104,v104
v_rcp_f32 v105,v105
v_mul_f32 v38,v38,v102
v_mul_f32 v39,v39,v103
v_mul_f32 v40,v40,v104
v_mul_f32 v41,v41,v105
v_cvt_pk_bf16_f32 v110,v38,v39
v_cvt_pk_bf16_f32 v111,v40,v41
buffer_store_dwordx2 v[110:111],v10,s[36:39],s67 offen offset:0 sc0 sc1
s_mov_b32 s72,s69
s_mov_b32 s69,0x7fff0000
s_mov_b32 s67,0x7fff0000
s_mov_b32 s77,0x7fff0000
s_branch Lg3_poll_%=
Lg3_fin1_%=:
L 0
v_fma_f32 v30,-v78,v82,v30
v_fma_f32 v31,-v78,v83,v31
v_fma_f32 v32,-v78,v84,v32
v_fma_f32 v33,-v78,v85,v33
v_mul_f32 v94,v30,v30
v_mul_f32 v95,v31,v31
v_mul_f32 v96,v32,v32
v_mul_f32 v97,v33,v33
v_fma_f32 v94,v80,v94,v79
v_fma_f32 v95,v80,v95,v79
v_fma_f32 v96,v80,v96,v79
v_fma_f32 v97,v80,v97,v79
v_mul_f32 v94,v30,v94
v_mul_f32 v95,v31,v95
v_mul_f32 v96,v32,v96
v_mul_f32 v97,v33,v97
v_exp_f32 v94,v94
v_exp_f32 v95,v95
v_exp_f32 v96,v96
v_exp_f32 v97,v97
v_fma_f32 v94,v94,v81,v81
v_fma_f32 v95,v95,v81,v81
v_fma_f32 v96,v96,v81,v81
v_fma_f32 v97,v97,v81,v81
v_rcp_f32 v94,v94
v_rcp_f32 v95,v95
v_rcp_f32 v96,v96
v_rcp_f32 v97,v97
v_mul_f32 v30,v30,v94
v_mul_f32 v31,v31,v95
v_mul_f32 v32,v32,v96
v_mul_f32 v33,v33,v97
v_cvt_pk_bf16_f32 v106,v30,v31
v_cvt_pk_bf16_f32 v107,v32,v33
v_fma_f32 v34,-v78,v86,v34
v_fma_f32 v35,-v78,v87,v35
v_fma_f32 v36,-v78,v88,v36
v_fma_f32 v37,-v78,v89,v37
v_mul_f32 v98,v34,v34
v_mul_f32 v99,v35,v35
v_mul_f32 v100,v36,v36
v_mul_f32 v101,v37,v37
v_fma_f32 v98,v80,v98,v79
v_fma_f32 v99,v80,v99,v79
v_fma_f32 v100,v80,v100,v79
v_fma_f32 v101,v80,v101,v79
v_mul_f32 v98,v34,v98
v_mul_f32 v99,v35,v99
v_mul_f32 v100,v36,v100
v_mul_f32 v101,v37,v101
v_exp_f32 v98,v98
v_exp_f32 v99,v99
v_exp_f32 v100,v100
v_exp_f32 v101,v101
v_fma_f32 v98,v98,v81,v81
v_fma_f32 v99,v99,v81,v81
v_fma_f32 v100,v100,v81,v81
v_fma_f32 v101,v101,v81,v81
v_rcp_f32 v98,v98
v_rcp_f32 v99,v99
v_rcp_f32 v100,v100
v_rcp_f32 v101,v101
v_mul_f32 v34,v34,v98
v_mul_f32 v35,v35,v99
v_mul_f32 v36,v36,v100
v_mul_f32 v37,v37,v101
v_cvt_pk_bf16_f32 v108,v34,v35
v_cvt_pk_bf16_f32 v109,v36,v37
buffer_store_dwordx4 v[106:109],v0,s[36:39],s67 offen offset:0 sc0 sc1
v_fma_f32 v38,-v78,v90,v38
v_fma_f32 v39,-v78,v91,v39
v_fma_f32 v40,-v78,v92,v40
v_fma_f32 v41,-v78,v93,v41
v_mul_f32 v102,v38,v38
v_mul_f32 v103,v39,v39
v_mul_f32 v104,v40,v40
v_mul_f32 v105,v41,v41
v_fma_f32 v102,v80,v102,v79
v_fma_f32 v103,v80,v103,v79
v_fma_f32 v104,v80,v104,v79
v_fma_f32 v105,v80,v105,v79
v_mul_f32 v102,v38,v102
v_mul_f32 v103,v39,v103
v_mul_f32 v104,v40,v104
v_mul_f32 v105,v41,v105
v_exp_f32 v102,v102
v_exp_f32 v103,v103
v_exp_f32 v104,v104
v_exp_f32 v105,v105
v_fma_f32 v102,v102,v81,v81
v_fma_f32 v103,v103,v81,v81
v_fma_f32 v104,v104,v81,v81
v_fma_f32 v105,v105,v81,v81
v_rcp_f32 v102,v102
v_rcp_f32 v103,v103
v_rcp_f32 v104,v104
v_rcp_f32 v105,v105
v_mul_f32 v38,v38,v102
v_mul_f32 v39,v39,v103
v_mul_f32 v40,v40,v104
v_mul_f32 v41,v41,v105
v_cvt_pk_bf16_f32 v110,v38,v39
v_cvt_pk_bf16_f32 v111,v40,v41
buffer_store_dwordx2 v[110:111],v10,s[36:39],s67 offen offset:0 sc0 sc1
s_mov_b32 s72,s69
s_mov_b32 s69,0x7fff0000
s_mov_b32 s67,0x7fff0000
s_mov_b32 s77,0x7fff0000
s_branch Lg3_done_%=
Lg3_done_%=:
s_waitcnt vmcnt(2)
s_barrier
buffer_store_dword v4,v3,s[36:39],s70 offen offset:0 sc0 sc1
s_waitcnt vmcnt(1)
s_barrier
buffer_store_dword v4,v3,s[36:39],s72 offen offset:0 sc0 sc1
s_waitcnt vmcnt(0) lgkmcnt(0)
s_barrier
s_endpgm
Lcode_g3_e:
.purgem M
.purgem R
.purgem T
.purgem D
.purgem L
)"
  :: "s"(ws), "s"(__builtin_amdgcn_readfirstlane(sWave)), "s"(__builtin_amdgcn_readfirstlane(sG0)), "s"(__builtin_amdgcn_readfirstlane(sStep)), "s"(__builtin_amdgcn_readfirstlane(sOutOff)), "s"(__builtin_amdgcn_readfirstlane(sFoutOff)), "s"(__builtin_amdgcn_readfirstlane(sAuxOff)), "s"(__builtin_amdgcn_readfirstlane(sWw)), "s"(__builtin_amdgcn_readfirstlane(sBuf)), "s"(__builtin_amdgcn_readfirstlane(sExp)), "v"(vL16), "v"(vFlag), "v"(vSnapOff), "v"(vSnapRd), "v"(vWd0), "v"(vWd1), "v"(vWr0), "v"(vWr1), "v"(vWr2), "v"(vOutS), "v"(vAux0), "v"(vAux1), "v"(vAux2), "v"(vAux3), "v"(vAn), "v"(vAe), "s"(w3), "s"(out) : "v255", "v0", "v1", "v2", "v3", "v4", "v5", "v6", "v7", "v8", "v9", "v10", "v11", "v12", "v13", "v14", "v15", "v16", "v17", "v18", "v19", "v20", "v21", "v22", "v23", "v24", "v25", "v26", "v27", "v28", "v29", "v30", "v31", "v32", "v33", "v34", "v35", "v36", "v37", "v38", "v39", "v40", "v41", "v42", "v43", "v44", "v45", "v46", "v47", "v48", "v49", "v50", "v51", "v52", "v53", "v54", "v55", "v56", "v57", "v58", "v59", "v60", "v61", "v62", "v63", "v64", "v65", "v66", "v67", "v68", "v69", "v70", "v71", "v72", "v73", "v74", "v75", "v76", "v77", "v78", "v79", "v80", "v81", "v82", "v83", "v84", "v85", "v86", "v87", "v88", "v89", "v90", "v91", "v92", "v93", "v94", "v95", "v96", "v97", "v98", "v99", "v100", "v101", "v102", "v103", "v104", "v105", "v106", "v107", "v108", "v109", "v110", "v111", "v112", "v113", "v114", "v115", "v116", "v117", "v118", "v119", "v120", "v121", "v122", "v123", "v124", "v125", "v126", "v127", "v128", "v129", "v130", "v131", "v132", "v133", "v134", "v135", "v136", "v137", "v138", "v139", "v140", "v141", "v142", "v143", "v144", "v145", "v146", "v147", "v148", "v149", "v150", "v151", "v152", "v153", "v154", "v155", "v156", "v157", "v158", "v159", "v160", "v161", "v162", "v163", "v164", "v165", "v166", "v167", "v168", "v169", "v170", "v171", "v172", "v173", "v174", "v175", "v176", "v177", "v178", "v179", "v180", "v181", "v182", "v183", "v184", "v185", "v186", "v187", "v188", "v189", "v190", "v191", "v192", "v193", "v194", "v195", "v196", "v197", "v198", "v199", "v200", "v201", "v202", "v203", "v204", "v205", "v206", "v207", "v208", "v209", "v210", "v211", "v212", "v213", "v214", "v215", "v216", "v217", "v218", "v219", "v220", "v221", "v222", "v223", "v224", "v225", "v226", "v227", "v228", "v229", "v230", "v231", "a0", "a1", "a2", "a3", "a4", "a5", "a6", "a7", "a8", "a9", "a10", "a11", "a12", "a13", "a14", "a15", "a16", "a17", "a18", "a19", "a20", "a21", "a22", "a23", "a24", "a25", "a26", "a27", "a28", "a29", "a30", "a31", "a32", "a33", "a34", "a35", "a36", "a37", "a38", "a39", "a40", "a41", "a42", "a43", "a44", "a45", "a46", "a47", "a48", "a49", "a50", "a51", "a52", "a53", "a54", "a55", "a56", "a57", "a58", "a59", "a60", "a61", "a62", "a63", "a64", "a65", "a66", "a67", "a68", "a69", "a70", "a71", "a72", "a73", "a74", "a75", "a76", "a77", "a78", "a79", "a80", "a81", "a82", "a83", "a84", "a85", "a86", "a87", "a88", "a89", "a90", "a91", "a92", "a93", "a94", "a95", "a96", "a97", "a98", "a99", "a100", "a101", "a102", "a103", "a104", "a105", "a106", "a107", "a108", "a109", "a110", "a111", "a112", "a113", "a114", "a115", "a116", "a117", "a118", "a119", "a120", "a121", "a122", "a123", "a124", "a125", "a126", "a127", "a128", "a129", "a130", "a131", "a132", "a133", "a134", "a135", "a136", "a137", "a138", "a139", "a140", "a141", "a142", "a143", "a144", "a145", "a146", "a147", "a148", "a149", "a150", "a151", "a152", "a153", "a154", "a155", "a156", "a157", "a158", "a159", "a160", "a161", "a162", "a163", "a164", "a165", "a166", "a167", "a168", "a169", "a170", "a171", "a172", "a173", "a174", "a175", "a176", "a177", "a178", "a179", "a180", "a181", "a182", "a183", "a184", "a185", "a186", "a187", "a188", "a189", "a190", "a191", "a192", "a193", "a194", "a195", "a196", "a197", "a198", "a199", "a200", "a201", "a202", "a203", "a204", "a205", "a206", "a207", "a208", "a209", "a210", "a211", "a212", "a213", "a214", "a215", "a216", "a217", "a218", "a219", "a220", "a221", "a222", "a223", "a224", "a225", "a226", "a227", "a228", "a229", "a230", "a231", "a232", "a233", "a234", "a235", "a236", "a237", "a238", "a239", "a240", "a241", "a242", "a243", "a244", "a245", "a246", "a247", "a248", "a249", "a250", "a251", "a252", "a253", "a254", "a255", "s36", "s37", "s38", "s39", "s40", "s41", "s42", "s43", "s44", "s45", "s46", "s47", "s48", "s49", "s50", "s51", "s52", "s53", "s54", "s55", "s56", "s57", "s58", "s59", "s60", "s61", "s62", "s63", "s64", "s65", "s66", "s67", "s68", "s69", "s70", "s71", "s72", "s73", "s74", "s75", "s76", "s77", "s78", "s79", "s80", "s81", "s82", "s83", "s84", "s85", "s86", "s87", "s88", "s89", "s90", "s91", "s92", "s93", "s94", "s95", "s96", "s97", "vcc", "scc", "memory"); } else { asm volatile(
R"(.macro M d,w,x
v_mfma_f32_16x16x32_bf16 v[\d:\d+3], \w, v[\x:\x+3], v[\d:\d+3]
.endm
.macro R d,a,o
ds_read_b128 v[\d:\d+3], v\a offset:\o
.endm
.macro T d,a,o
ds_read_b64_tr_b16 \d, v\a offset:\o
.endm
.macro D a,s,o
buffer_load_dwordx4 v\a, s[36:39], s\s offen offset:\o sc1 lds
.endm
.macro L c
s_waitcnt lgkmcnt(\c)
.endm
Lcode_g3o_b:
s_mov_b64 s[36:37],%0
s_mov_b32 s38,111243392
s_mov_b32 s39,0x00020000
s_mov_b32 s44,%1
s_mov_b32 s45,%2
s_mov_b32 s46,%3
s_mov_b32 s47,%4
s_mov_b32 s48,%5
s_mov_b32 s49,%6
s_mov_b32 s50,%7
s_mov_b32 s51,%8
s_mov_b32 s53,%9
v_mov_b32 v0,%10
v_mov_b32 v3,%11
v_mov_b32 v5,%12
v_mov_b32 v6,%13
v_mov_b32 v18,%14
v_mov_b32 v19,%15
v_mov_b32 v20,%16
v_mov_b32 v21,%17
v_mov_b32 v22,%18
v_mov_b32 v10,%19
v_mov_b32 v11,%20
v_mov_b32 v12,%21
v_mov_b32 v13,%22
v_mov_b32 v14,%23
v_mov_b32 v15,%24
v_mov_b32 v16,%25
s_mov_b64 s[40:41],%26
s_mov_b32 s42,7340032
s_mov_b32 s43,0x00020000
s_lshl_b32 s57,s44,11
s_add_u32 s55,s50,0
s_add_u32 m0,s57,0
s_nop 0
buffer_load_dwordx4 v18,s[40:43],s55 offen offset:0  lds
s_add_u32 m0,s57,1024
s_nop 0
buffer_load_dwordx4 v19,s[40:43],s55 offen offset:0  lds
s_add_u32 m0,s57,8064
s_nop 0
buffer_load_dwordx4 v18,s[40:43],s55 offen offset:128  lds
s_add_u32 m0,s57,9088
s_nop 0
buffer_load_dwordx4 v19,s[40:43],s55 offen offset:128  lds
s_add_u32 s55,s50,458752
s_add_u32 m0,s57,16384
s_nop 0
buffer_load_dwordx4 v18,s[40:43],s55 offen offset:0  lds
s_add_u32 m0,s57,17408
s_nop 0
buffer_load_dwordx4 v19,s[40:43],s55 offen offset:0  lds
s_add_u32 m0,s57,24448
s_nop 0
buffer_load_dwordx4 v18,s[40:43],s55 offen offset:128  lds
s_add_u32 m0,s57,25472
s_nop 0
buffer_load_dwordx4 v19,s[40:43],s55 offen offset:128  lds
s_add_u32 s55,s50,917504
s_add_u32 m0,s57,32768
s_nop 0
buffer_load_dwordx4 v18,s[40:43],s55 offen offset:0  lds
s_add_u32 m0,s57,33792
s_nop 0
buffer_load_dwordx4 v19,s[40:43],s55 offen offset:0  lds
s_add_u32 m0,s57,40832
s_nop 0
buffer_load_dwordx4 v18,s[40:43],s55 offen offset:128  lds
s_add_u32 m0,s57,41856
s_nop 0
buffer_load_dwordx4 v19,s[40:43],s55 offen offset:128  lds
s_add_u32 s55,s50,1376256
s_add_u32 m0,s57,49152
s_nop 0
buffer_load_dwordx4 v18,s[40:43],s55 offen offset:0  lds
s_add_u32 m0,s57,50176
s_nop 0
buffer_load_dwordx4 v19,s[40:43],s55 offen offset:0  lds
s_add_u32 m0,s57,57216
s_nop 0
buffer_load_dwordx4 v18,s[40:43],s55 offen offset:128  lds
s_add_u32 m0,s57,58240
s_nop 0
buffer_load_dwordx4 v19,s[40:43],s55 offen offset:128  lds
s_add_u32 s55,s50,1835008
s_add_u32 m0,s57,65536
s_nop 0
buffer_load_dwordx4 v18,s[40:43],s55 offen offset:0  lds
s_add_u32 m0,s57,66560
s_nop 0
buffer_load_dwordx4 v19,s[40:43],s55 offen offset:0  lds
s_add_u32 m0,s57,73600
s_nop 0
buffer_load_dwordx4 v18,s[40:43],s55 offen offset:128  lds
s_add_u32 m0,s57,74624
s_nop 0
buffer_load_dwordx4 v19,s[40:43],s55 offen offset:128  lds
s_add_u32 s55,s50,2293760
s_add_u32 m0,s57,81920
s_nop 0
buffer_load_dwordx4 v18,s[40:43],s55 offen offset:0  lds
s_add_u32 m0,s57,82944
s_nop 0
buffer_load_dwordx4 v19,s[40:43],s55 offen offset:0  lds
s_add_u32 m0,s57,89984
s_nop 0
buffer_load_dwordx4 v18,s[40:43],s55 offen offset:128  lds
s_add_u32 m0,s57,91008
s_nop 0
buffer_load_dwordx4 v19,s[40:43],s55 offen offset:128  lds
s_waitcnt vmcnt(20) lgkmcnt(0)
s_barrier
s_add_u32 s55,s50,2752512
s_add_u32 m0,s57,98304
s_nop 0
buffer_load_dwordx4 v18,s[40:43],s55 offen offset:0  lds
s_add_u32 m0,s57,99328
s_nop 0
buffer_load_dwordx4 v19,s[40:43],s55 offen offset:0  lds
s_add_u32 m0,s57,106368
s_nop 0
buffer_load_dwordx4 v18,s[40:43],s55 offen offset:128  lds
s_add_u32 m0,s57,107392
s_nop 0
buffer_load_dwordx4 v19,s[40:43],s55 offen offset:128  lds
v_add_u32 v26,0,v20
v_add_u32 v27,0,v21
T a[0:1],26,0
T a[2:3],26,512
T a[4:5],27,0
T a[6:7],27,512
T a[8:9],26,4096
T a[10:11],26,4608
T a[12:13],27,4096
T a[14:15],27,4608
s_waitcnt vmcnt(20) lgkmcnt(0)
s_barrier
s_add_u32 s55,s50,3211264
s_add_u32 m0,s57,0
s_nop 0
buffer_load_dwordx4 v18,s[40:43],s55 offen offset:0  lds
s_add_u32 m0,s57,1024
s_nop 0
buffer_load_dwordx4 v19,s[40:43],s55 offen offset:0  lds
s_add_u32 m0,s57,8064
s_nop 0
buffer_load_dwordx4 v18,s[40:43],s55 offen offset:128  lds
s_add_u32 m0,s57,9088
s_nop 0
buffer_load_dwordx4 v19,s[40:43],s55 offen offset:128  lds
v_add_u32 v26,16384,v20
v_add_u32 v27,16384,v21
T a[16:17],26,0
T a[18:19],26,512
T a[20:21],27,0
T a[22:23],27,512
T a[24:25],26,4096
T a[26:27],26,4608
T a[28:29],27,4096
T a[30:31],27,4608
s_waitcnt vmcnt(20) lgkmcnt(0)
s_barrier
s_add_u32 s55,s50,3670016
s_add_u32 m0,s57,16384
s_nop 0
buffer_load_dwordx4 v18,s[40:43],s55 offen offset:0  lds
s_add_u32 m0,s57,17408
s_nop 0
buffer_load_dwordx4 v19,s[40:43],s55 offen offset:0  lds
s_add_u32 m0,s57,24448
s_nop 0
buffer_load_dwordx4 v18,s[40:43],s55 offen offset:128  lds
s_add_u32 m0,s57,25472
s_nop 0
buffer_load_dwordx4 v19,s[40:43],s55 offen offset:128  lds
v_add_u32 v26,32768,v20
v_add_u32 v27,32768,v21
T a[32:33],26,0
T a[34:35],26,512
T a[36:37],27,0
T a[38:39],27,512
T a[40:41],26,4096
T a[42:43],26,4608
T a[44:45],27,4096
T a[46:47],27,4608
s_waitcnt vmcnt(20) lgkmcnt(0)
s_barrier
s_add_u32 s55,s50,4128768
s_add_u32 m0,s57,32768
s_nop 0
buffer_load_dwordx4 v18,s[40:43],s55 offen offset:0  lds
s_add_u32 m0,s57,33792
s_nop 0
buffer_load_dwordx4 v19,s[40:43],s55 offen offset:0  lds
s_add_u32 m0,s57,40832
s_nop 0
buffer_load_dwordx4 v18,s[40:43],s55 offen offset:128  lds
s_add_u32 m0,s57,41856
s_nop 0
buffer_load_dwordx4 v19,s[40:43],s55 offen offset:128  lds
v_add_u32 v26,49152,v20
v_add_u32 v27,49152,v21
T a[48:49],26,0
T a[50:51],26,512
T a[52:53],27,0
T a[54:55],27,512
T a[56:57],26,4096
T a[58:59],26,4608
T a[60:61],27,4096
T a[62:63],27,4608
s_waitcnt vmcnt(20) lgkmcnt(0)
s_barrier
s_add_u32 s55,s50,4587520
s_add_u32 m0,s57,49152
s_nop 0
buffer_load_dwordx4 v18,s[40:43],s55 offen offset:0  lds
s_add_u32 m0,s57,50176
s_nop 0
buffer_load_dwordx4 v19,s[40:43],s55 offen offset:0  lds
s_add_u32 m0,s57,57216
s_nop 0
buffer_load_dwordx4 v18,s[40:43],s55 offen offset:128  lds
s_add_u32 m0,s57,58240
s_nop 0
buffer_load_dwordx4 v19,s[40:43],s55 offen offset:128  lds
v_add_u32 v26,65536,v20
v_add_u32 v27,65536,v21
T a[64:65],26,0
T a[66:67],26,512
T a[68:69],27,0
T a[70:71],27,512
T a[72:73],26,4096
T a[74:75],26,4608
T a[76:77],27,4096
T a[78:79],27,4608
s_waitcnt vmcnt(20) lgkmcnt(0)
s_barrier
s_add_u32 s55,s50,5046272
s_add_u32 m0,s57,65536
s_nop 0
buffer_load_dwordx4 v18,s[40:43],s55 offen offset:0  lds
s_add_u32 m0,s57,66560
s_nop 0
buffer_load_dwordx4 v19,s[40:43],s55 offen offset:0  lds
s_add_u32 m0,s57,73600
s_nop 0
buffer_load_dwordx4 v18,s[40:43],s55 offen offset:128  lds
s_add_u32 m0,s57,74624
s_nop 0
buffer_load_dwordx4 v19,s[40:43],s55 offen offset:128  lds
v_add_u32 v26,81920,v20
v_add_u32 v27,81920,v21
T a[80:81],26,0
T a[82:83],26,512
T a[84:85],27,0
T a[86:87],27,512
T a[88:89],26,4096
T a[90:91],26,4608
T a[92:93],27,4096
T a[94:95],27,4608
s_waitcnt vmcnt(20) lgkmcnt(0)
s_barrier
s_add_u32 s55,s50,5505024
s_add_u32 m0,s57,81920
s_nop 0
buffer_load_dwordx4 v18,s[40:43],s55 offen offset:0  lds
s_add_u32 m0,s57,82944
s_nop 0
buffer_load_dwordx4 v19,s[40:43],s55 offen offset:0  lds
s_add_u32 m0,s57,89984
s_nop 0
buffer_load_dwordx4 v18,s[40:43],s55 offen offset:128  lds
s_add_u32 m0,s57,91008
s_nop 0
buffer_load_dwordx4 v19,s[40:43],s55 offen offset:128  lds
v_add_u32 v26,98304,v20
v_add_u32 v27,98304,v21
T a[96:97],26,0
T a[98:99],26,512
T a[100:101],27,0
T a[102:103],27,512
T a[104:105],26,4096
T a[106:107],26,4608
T a[108:109],27,4096
T a[110:111],27,4608
s_waitcnt vmcnt(20) lgkmcnt(0)
s_barrier
s_add_u32 s55,s50,5963776
s_add_u32 m0,s57,98304
s_nop 0
buffer_load_dwordx4 v18,s[40:43],s55 offen offset:0  lds
s_add_u32 m0,s57,99328
s_nop 0
buffer_load_dwordx4 v19,s[40:43],s55 offen offset:0  lds
s_add_u32 m0,s57,106368
s_nop 0
buffer_load_dwordx4 v18,s[40:43],s55 offen offset:128  lds
s_add_u32 m0,s57,107392
s_nop 0
buffer_load_dwordx4 v19,s[40:43],s55 offen offset:128  lds
v_add_u32 v26,0,v20
v_add_u32 v27,0,v21
T a[112:113],26,0
T a[114:115],26,512
T a[116:117],27,0
T a[118:119],27,512
T a[120:121],26,4096
T a[122:123],26,4608
T a[124:125],27,4096
T a[126:127],27,4608
s_waitcnt vmcnt(20) lgkmcnt(0)
s_barrier
s_add_u32 s55,s50,6422528
s_add_u32 m0,s57,0
s_nop 0
buffer_load_dwordx4 v18,s[40:43],s55 offen offset:0  lds
s_add_u32 m0,s57,1024
s_nop 0
buffer_load_dwordx4 v19,s[40:43],s55 offen offset:0  lds
s_add_u32 m0,s57,8064
s_nop 0
buffer_load_dwordx4 v18,s[40:43],s55 offen offset:128  lds
s_add_u32 m0,s57,9088
s_nop 0
buffer_load_dwordx4 v19,s[40:43],s55 offen offset:128  lds
v_add_u32 v26,16384,v20
v_add_u32 v27,16384,v21
T a[128:129],26,0
T a[130:131],26,512
T a[132:133],27,0
T a[134:135],27,512
T a[136:137],26,4096
T a[138:139],26,4608
T a[140:141],27,4096
T a[142:143],27,4608
s_waitcnt vmcnt(20) lgkmcnt(0)
s_barrier
s_add_u32 s55,s50,6881280
s_add_u32 m0,s57,16384
s_nop 0
buffer_load_dwordx4 v18,s[40:43],s55 offen offset:0  lds
s_add_u32 m0,s57,17408
s_nop 0
buffer_load_dwordx4 v19,s[40:43],s55 offen offset:0  lds
s_add_u32 m0,s57,24448
s_nop 0
buffer_load_dwordx4 v18,s[40:43],s55 offen offset:128  lds
s_add_u32 m0,s57,25472
s_nop 0
buffer_load_dwordx4 v19,s[40:43],s55 offen offset:128  lds
v_add_u32 v26,32768,v20
v_add_u32 v27,32768,v21
T a[144:145],26,0
T a[146:147],26,512
T a[148:149],27,0
T a[150:151],27,512
T a[152:153],26,4096
T a[154:155],26,4608
T a[156:157],27,4096
T a[158:159],27,4608
s_waitcnt vmcnt(20) lgkmcnt(0)
s_barrier
v_add_u32 v26,49152,v20
v_add_u32 v27,49152,v21
T a[160:161],26,0
T a[162:163],26,512
T a[164:165],27,0
T a[166:167],27,512
T a[168:169],26,4096
T a[170:171],26,4608
T a[172:173],27,4096
T a[174:175],27,4608
s_waitcnt vmcnt(16) lgkmcnt(0)
s_barrier
v_add_u32 v26,65536,v20
v_add_u32 v27,65536,v21
T a[176:177],26,0
T a[178:179],26,512
T a[180:181],27,0
T a[182:183],27,512
T a[184:185],26,4096
T a[186:187],26,4608
T a[188:189],27,4096
T a[190:191],27,4608
s_waitcnt vmcnt(12) lgkmcnt(0)
s_barrier
v_add_u32 v26,81920,v20
v_add_u32 v27,81920,v21
T a[192:193],26,0
T a[194:195],26,512
T a[196:197],27,0
T a[198:199],27,512
T a[200:201],26,4096
T a[202:203],26,4608
T a[204:205],27,4096
T a[206:207],27,4608
s_waitcnt vmcnt(8) lgkmcnt(0)
s_barrier
v_add_u32 v26,98304,v20
v_add_u32 v27,98304,v21
T a[208:209],26,0
T a[210:211],26,512
T a[212:213],27,0
T a[214:215],27,512
T a[216:217],26,4096
T a[218:219],26,4608
T a[220:221],27,4096
T a[222:223],27,4608
s_waitcnt vmcnt(4) lgkmcnt(0)
s_barrier
v_add_u32 v26,0,v20
v_add_u32 v27,0,v21
T a[224:225],26,0
T a[226:227],26,512
T a[228:229],27,0
T a[230:231],27,512
T a[232:233],26,4096
T a[234:235],26,4608
T a[236:237],27,4096
T a[238:239],27,4608
s_waitcnt vmcnt(0) lgkmcnt(0)
s_barrier
v_add_u32 v26,16384,v20
v_add_u32 v27,16384,v21
T a[240:241],26,0
T a[242:243],26,512
T a[244:245],27,0
T a[246:247],27,512
T a[248:249],26,4096
T a[250:251],26,4608
T a[252:253],27,4096
T a[254:255],27,4608
L 0
s_barrier
v_mov_b32 v34,0x3f803f80
v_mov_b32 v35,0x3f803f80
v_mov_b32 v36,0x3f803f80
v_mov_b32 v37,0x3f803f80
s_nop 1
v_mfma_f32_16x16x32_bf16 v[106:109],a[0:3],v[34:37],0
v_mfma_f32_16x16x32_bf16 v[110:113],a[4:7],v[34:37],0
M 106,a[8:11],34
M 110,a[12:15],34
M 106,a[16:19],34
M 110,a[20:23],34
M 106,a[24:27],34
M 110,a[28:31],34
M 106,a[32:35],34
M 110,a[36:39],34
M 106,a[40:43],34
M 110,a[44:47],34
M 106,a[48:51],34
M 110,a[52:55],34
M 106,a[56:59],34
M 110,a[60:63],34
M 106,a[64:67],34
M 110,a[68:71],34
M 106,a[72:75],34
M 110,a[76:79],34
M 106,a[80:83],34
M 110,a[84:87],34
M 106,a[88:91],34
M 110,a[92:95],34
M 106,a[96:99],34
M 110,a[100:103],34
M 106,a[104:107],34
M 110,a[108:111],34
M 106,a[112:115],34
M 110,a[116:119],34
M 106,a[120:123],34
M 110,a[124:127],34
M 106,a[128:131],34
M 110,a[132:135],34
M 106,a[136:139],34
M 110,a[140:143],34
M 106,a[144:147],34
M 110,a[148:151],34
M 106,a[152:155],34
M 110,a[156:159],34
M 106,a[160:163],34
M 110,a[164:167],34
M 106,a[168:171],34
M 110,a[172:175],34
M 106,a[176:179],34
)"
R"(M 110,a[180:183],34
M 106,a[184:187],34
M 110,a[188:191],34
M 106,a[192:195],34
M 110,a[196:199],34
M 106,a[200:203],34
M 110,a[204:207],34
M 106,a[208:211],34
M 110,a[212:215],34
M 106,a[216:219],34
M 110,a[220:223],34
M 106,a[224:227],34
M 110,a[228:231],34
M 106,a[232:235],34
M 110,a[236:239],34
M 106,a[240:243],34
M 110,a[244:247],34
M 106,a[248:251],34
M 110,a[252:255],34
v_mov_b32 v4,s53
s_mov_b32 s82,0
s_mov_b32 s80,0
s_mov_b32 s54,s45
s_mul_i32 s57,s44,4096
s_mul_i32 s55,s45,32768
s_add_u32 s55,s55,s57
s_add_u32 s55,s55,31457280
s_mul_i32 s57,s44,4096
s_mul_i32 s60,s46,32768
s_mul_i32 s61,s46,1024
s_mul_i32 s62,s46,114688
s_mul_i32 s63,s46,2560
s_mul_i32 s64,s46,1024
s_lshl_b32 s59,s44,8
s_add_u32 s59,s59,131072
s_mul_i32 s58,s45,1024
s_add_u32 s58,s58,109613056
s_mul_i32 s65,s45,114688
s_add_u32 s65,s65,s47
s_mul_i32 s68,s45,2560
s_add_u32 s68,s68,s48
s_mul_i32 s73,s45,1024
s_add_u32 s73,s73,107479040
s_mul_i32 s74,s45,32768
s_add_u32 s74,s74,s49
s_mul_i32 s75,s45,1024
s_add_u32 s75,s75,107479040
s_mov_b32 s66,0x7fff0000
s_mov_b32 s67,0x7fff0000
s_mov_b32 s69,0x7fff0000
s_mov_b32 s76,0x7fff0000
s_mov_b32 s77,0x7fff0000
s_mov_b32 s70,0x7fff0000
s_mov_b32 s71,0x7fff0000
s_mov_b32 s72,0x7fff0000
v_mov_b32 v1,v0
v_add_u32 v2,16384,v0
s_mov_b32 s84,16384
s_mov_b32 s86,0x3a800000
s_mov_b32 s87,0x3727c5ac
s_mov_b32 s88,0xc0135761
s_mov_b32 s89,0x3d372713
s_lshr_b32 s85,s44,1
s_mul_i32 s85,s85,127
v_lshlrev_b32 v8,22,v0
v_and_b32 v8,0x80000000,v8
v_or_b32 v8,v8,v5
v_add_u32 v2,32768,v0
s_lshl_b32 s84,s57,1
s_add_u32 s84,s84,32768
v_and_b32 v114,240,v0
v_cmp_eq_u32 vcc,0,v114
s_nop 1
v_cndmask_b32 v17,0,1.0,vcc
s_getpc_b64 s[40:41]
Lg3o_pc_%=:
s_lshl_b32 s78,s44,13
v_lshlrev_b32 v114,3,v0
v_add_u32 v114,s78,v114
v_min_u32 v114,Lcode_g3o_e-Lg3o_pc_%=-4,v114
s_mov_b32 m0,163584
s_nop 0
global_load_lds_dword v114,s[40:41]
s_add_u32 m0,s59,4096
s_nop 0
buffer_load_dword v8,s[36:39],s58 offen offset:0 sc1 lds
buffer_store_dword v4,v3,s[36:39],s71 offen offset:0 sc0 sc1
s_add_u32 m0,s59,5120
s_nop 0
buffer_load_dword v8,s[36:39],s58 offen offset:0 sc1 lds
buffer_store_dword v4,v3,s[36:39],s71 offen offset:0 sc0 sc1
s_mov_b32 s83,0
Lg3o_poll_%=:
s_add_u32 s78,s83,2
s_and_b32 s78,s78,3
s_lshl_b32 s78,s78,10
s_add_u32 s78,s78,4096
s_add_u32 m0,s59,s78
s_nop 0
buffer_load_dword v8,s[36:39],s58 offen offset:0 sc1 lds
s_and_b32 s78,s83,3
s_lshl_b32 s78,s78,10
v_add_u32 v114,s78,v6
s_waitcnt vmcnt(4)
s_barrier
ds_read_b32 v7,v114 offset:4096
buffer_store_dword v4,v3,s[36:39],s70 offen offset:0 sc0 sc1
s_mov_b32 s70,s71
s_mov_b32 s71,s72
s_mov_b32 s72,0x7fff0000
s_add_u32 s83,s83,1
s_add_u32 s82,s82,1
L 0
v_cmp_eq_u32 vcc,s53,v7
s_nop 0
s_cmp_eq_u32 vcc_lo,-1
s_cbranch_scc1 Lg3o_go_%=
s_cmp_lt_u32 s82,20000
s_cbranch_scc1 Lg3o_poll_%=
Lg3o_go_%=:
buffer_load_dword v130,v11,s[36:39],s73 offen offset:0 sc1
s_add_u32 s73,s73,s64
s_add_u32 m0,s57,0
s_add_u32 s56,s55,0
D 0,56,0
D 0,56,1024
D 0,56,2048
D 0,56,3072
s_add_u32 s58,s58,s61
s_add_u32 m0,s59,2048
s_nop 0
buffer_load_dword v8,s[36:39],s58 offen offset:0 sc1 lds
s_add_u32 m0,s57,16384
s_add_u32 s56,s55,16384
D 0,56,0
D 0,56,1024
D 0,56,2048
D 0,56,3072
s_add_u32 s55,s55,s60
s_sub_u32 s57,s84,s57
s_add_u32 s54,s54,s46
s_waitcnt vmcnt(4)
s_barrier
buffer_store_dword v4,v3,s[36:39],s70 offen offset:0 sc0 sc1
s_min_u32 s71,s71,s72
buffer_store_dword v4,v3,s[36:39],s71 offen offset:0 sc0 sc1
s_mov_b32 s70,0x7fff0000
s_mov_b32 s71,0x7fff0000
s_mov_b32 s72,0x7fff0000
s_cmp_eq_u32 s80,1
s_cbranch_scc1 Lg3o_st1_%=
s_branch Lg3o_st0_%=
Lg3o_gox_%=:
s_add_u32 m0,s59,2048
s_nop 0
buffer_load_dword v8,s[36:39],s58 offen offset:0 sc1 lds
s_waitcnt vmcnt(0)
s_barrier
buffer_store_dword v4,v3,s[36:39],s70 offen offset:0 sc0 sc1
s_min_u32 s71,s71,s72
buffer_store_dword v4,v3,s[36:39],s71 offen offset:0 sc0 sc1
s_mov_b32 s70,0x7fff0000
s_mov_b32 s71,0x7fff0000
s_mov_b32 s72,0x7fff0000
s_cmp_eq_u32 s80,1
s_cbranch_scc1 Lg3o_st1_%=
Lg3o_st0_%=:
ds_read_b32 v7,v6 offset:2048
R 34,1,0
R 38,1,1024
R 42,1,2048
R 46,1,3072
R 50,1,4096
L 5
v_cmp_eq_u32 vcc,s53,v7
L 3
s_cmp_eq_u32 vcc_lo,-1
s_cbranch_scc1 Lg3o_F0_%=
s_branch Lg3o_L0_%=
Lg3o_st1_%=:
ds_read_b32 v7,v6 offset:2048
R 34,2,0
R 38,2,1024
R 42,2,2048
R 46,2,3072
R 50,2,4096
L 5
v_cmp_eq_u32 vcc,s53,v7
L 3
s_cmp_eq_u32 vcc_lo,-1
s_cbranch_scc1 Lg3o_F1_%=
s_branch Lg3o_L1_%=
Lg3o_F0_%=:
v_mfma_f32_16x16x32_bf16 v[22:25],a[4:7],v[34:37],0
s_add_u32 s58,s58,s61
s_add_u32 m0,s59,0
R 54,1,5120
M 22,a[12:15],38
buffer_load_dword v8,s[36:39],s58 offen offset:0 sc1 lds
v_mfma_f32_16x16x32_bf16 v[18:21],a[0:3],v[34:37],0
s_add_u32 m0,s57,0
R 58,1,6144
v_add_f32_dpp v131,v130,v130 quad_perm:[1,0,3,2] row_mask:0xf bank_mask:0xf
M 18,a[8:11],38
L 3
s_add_u32 s56,s55,0
buffer_load_dword v130,v11,s[36:39],s73 offen offset:0 sc1
M 18,a[16:19],42
s_mov_b32 s66,s65
R 62,1,7168
v_add_f32_dpp v131,v131,v131 quad_perm:[2,3,0,1] row_mask:0xf bank_mask:0xf
M 18,a[24:27],46
D 0,56,0
M 22,a[20:23],42
s_add_u32 s65,s65,s62
R 66,1,8192
v_add_f32_dpp v134,v131,v131 row_half_mirror row_mask:0xf bank_mask:0xf
M 22,a[28:31],46
L 3
D 0,56,1024
M 22,a[36:39],50
s_add_u32 s54,s54,s46
R 70,1,9216
v_mov_b32_dpp v131,v134 row_ror:8 row_mask:0xf bank_mask:0xf
M 22,a[44:47],54
s_add_u32 s73,s73,s64
v_fma_f32 v131,-v134,v134,v131
v_max_f32 v131,s87,v131
M 18,a[32:35],50
R 74,1,10240
v_mul_f32 v134,v17,v134
M 18,a[40:43],54
L 3
D 0,56,2048
M 18,a[48:51],58
R 78,1,11264
v_fma_f32 v26,-v102,v106,v26
M 18,a[56:59],62
D 0,56,3072
M 22,a[52:55],58
s_add_u32 m0,s57,16384
R 82,1,12288
v_fma_f32 v27,-v102,v107,v27
M 22,a[60:63],62
L 3
v_rsq_f32 v132,v131
M 22,a[68:71],66
s_add_u32 s56,s55,16384
R 86,1,13312
v_fma_f32 v131,v15,v131,v16
M 22,a[76:79],70
s_add_u32 s55,s55,s60
v_mul_f32 v135,v132,v132
v_fma_f32 v131,v14,v135,v131
M 18,a[64:67],66
s_sub_u32 s57,s84,s57
R 90,1,14336
v_fma_f32 v134,v132,v131,v134
M 18,a[72:75],70
L 3
ds_write_b32 v12,v134 offset:0
M 18,a[80:83],74
s_waitcnt vmcnt(8)
R 94,1,15360
v_fma_f32 v28,-v102,v108,v28
M 18,a[88:91],78
s_barrier
v_fma_f32 v29,-v102,v109,v29
v_mul_f32 v114,v26,v26
M 22,a[84:87],74
R 34,1,16384
v_mul_f32 v115,v27,v27
M 22,a[92:95],78
L 4
v_mul_f32 v116,v28,v28
v_mul_f32 v117,v29,v29
M 22,a[100:103],82
R 38,1,17408
v_fma_f32 v114,v104,v114,v103
M 22,a[108:111],86
D 0,56,0
M 18,a[96:99],82
R 42,1,18432
v_fma_f32 v115,v104,v115,v103
M 18,a[104:107],86
L 3
v_fma_f32 v116,v104,v116,v103
v_fma_f32 v117,v104,v117,v103
M 18,a[112:115],90
R 46,1,19456
v_mul_f32 v114,v26,v114
M 18,a[120:123],94
v_exp_f32 v114,v114
M 22,a[116:119],90
R 50,1,20480
v_mul_f32 v115,v27,v115
M 22,a[124:127],94
L 3
v_exp_f32 v115,v115
M 22,a[132:135],34
R 54,1,21504
v_mul_f32 v116,v28,v116
M 22,a[140:143],38
D 0,56,1024
M 18,a[128:131],34
R 58,1,22528
v_mul_f32 v117,v29,v117
M 18,a[136:139],38
L 3
v_exp_f32 v116,v116
M 18,a[144:147],42
R 62,1,23552
v_fma_f32 v114,v114,v105,v105
M 18,a[152:155],46
v_exp_f32 v117,v117
M 22,a[148:151],42
R 66,1,24576
v_fma_f32 v115,v115,v105,v105
M 22,a[156:159],46
L 3
D 0,56,2048
M 22,a[164:167],50
R 70,1,25600
v_fma_f32 v116,v116,v105,v105
M 22,a[172:175],54
v_rcp_f32 v114,v114
M 18,a[160:163],50
R 74,1,26624
v_fma_f32 v117,v117,v105,v105
M 18,a[168:171],54
L 3
v_rcp_f32 v115,v115
M 18,a[176:179],58
R 78,1,27648
v_mul_f32 v26,v26,v114
M 18,a[184:187],62
v_rcp_f32 v116,v116
M 22,a[180:183],58
R 82,1,28672
v_mul_f32 v27,v27,v115
M 22,a[188:191],62
L 3
D 0,56,3072
M 22,a[196:199],66
R 86,1,29696
v_mul_f32 v28,v28,v116
M 22,a[204:207],70
v_rcp_f32 v117,v117
M 18,a[192:195],66
R 90,1,30720
v_mul_f32 v29,v29,v117
M 18,a[200:203],70
L 3
s_nop 0
v_cvt_pk_bf16_f32 v122,v26,v27
M 18,a[208:211],74
s_waitcnt vmcnt(4)
R 94,1,31744
v_fma_f32 v30,-v102,v110,v30
M 18,a[216:219],78
s_barrier
ds_read_b32 v7,v6 offset:0
R 98,13,0
M 22,a[212:215],74
R 34,2,0
v_fma_f32 v31,-v102,v111,v31
M 22,a[220:223],78
L 5
buffer_store_dword v4,v3,s[36:39],s70 offen offset:0 sc0 sc1
M 22,a[228:231],82
s_mov_b32 s70,s69
R 38,2,1024
v_fma_f32 v32,-v102,v112,v32
M 22,a[236:239],86
s_mov_b32 s69,s68
v_fma_f32 v33,-v102,v113,v33
v_mul_f32 v118,v30,v30
M 18,a[224:227],82
s_add_u32 s68,s68,s63
R 42,2,2048
v_mul_f32 v119,v31,v31
M 18,a[232:235],86
L 5
v_mul_f32 v120,v32,v32
v_mul_f32 v121,v33,v33
v_fma_f32 v118,v104,v118,v103
v_fma_f32 v119,v104,v119,v103
v_fma_f32 v120,v104,v120,v103
v_fma_f32 v121,v104,v121,v103
v_mul_f32 v118,v30,v118
v_mul_f32 v119,v31,v119
v_mul_f32 v120,v32,v120
v_mul_f32 v121,v33,v121
v_exp_f32 v118,v118
v_exp_f32 v119,v119
v_exp_f32 v120,v120
v_exp_f32 v121,v121
M 18,a[240:243],90
L 4
R 46,2,3072
v_fma_f32 v118,v118,v105,v105
v_fma_f32 v119,v119,v105,v105
v_fma_f32 v120,v120,v105,v105
v_fma_f32 v121,v121,v105,v105
v_rcp_f32 v118,v118
v_rcp_f32 v119,v119
v_rcp_f32 v120,v120
v_rcp_f32 v121,v121
v_cmp_eq_u32 vcc,s53,v7
M 18,a[248:251],94
v_cvt_pk_bf16_f32 v123,v28,v29
v_mul_f32 v30,v30,v118
v_mul_f32 v31,v31,v119
v_mul_f32 v32,v32,v120
v_mul_f32 v33,v33,v121
v_cvt_pk_bf16_f32 v124,v30,v31
v_cvt_pk_bf16_f32 v125,v32,v33
M 22,a[244:247],90
L 2
s_cmp_eq_u32 vcc_lo,-1
R 50,2,4096
buffer_store_dwordx4 v[122:125],v0,s[36:39],s67 offen offset:0 sc0 sc1
M 22,a[252:255],94
s_cbranch_scc0 Lg3o_K1_%=
Lg3o_F1_%=:
v_mfma_f32_16x16x32_bf16 v[30:33],a[4:7],v[34:37],0
s_add_u32 s58,s58,s61
s_add_u32 m0,s59,1024
R 54,2,5120
M 30,a[12:15],38
buffer_load_dword v8,s[36:39],s58 offen offset:0 sc1 lds
v_mfma_f32_16x16x32_bf16 v[26:29],a[0:3],v[34:37],0
s_add_u32 m0,s57,0
R 58,2,6144
v_add_f32_dpp v131,v130,v130 quad_perm:[1,0,3,2] row_mask:0xf bank_mask:0xf
M 26,a[8:11],38
L 3
s_add_u32 s56,s55,0
buffer_load_dword v130,v11,s[36:39],s73 offen offset:0 sc1
M 26,a[16:19],42
s_mov_b32 s67,s65
R 62,2,7168
v_add_f32_dpp v131,v131,v131 quad_perm:[2,3,0,1] row_mask:0xf bank_mask:0xf
M 26,a[24:27],46
D 0,56,0
M 30,a[20:23],42
s_add_u32 s65,s65,s62
R 66,2,8192
v_add_f32_dpp v134,v131,v131 row_half_mirror row_mask:0xf bank_mask:0xf
M 30,a[28:31],46
L 3
D 0,56,1024
M 30,a[36:39],50
s_add_u32 s54,s54,s46
R 70,2,9216
v_mov_b32_dpp v131,v134 row_ror:8 row_mask:0xf bank_mask:0xf
M 30,a[44:47],54
s_add_u32 s73,s73,s64
v_fma_f32 v131,-v134,v134,v131
v_max_f32 v131,s87,v131
M 26,a[32:35],50
R 74,2,10240
v_mul_f32 v134,v17,v134
M 26,a[40:43],54
L 3
D 0,56,2048
M 26,a[48:51],58
R 78,2,11264
v_fma_f32 v18,-v98,v106,v18
M 26,a[56:59],62
D 0,56,3072
M 30,a[52:55],58
s_add_u32 m0,s57,16384
R 82,2,12288
v_fma_f32 v19,-v98,v107,v19
M 30,a[60:63],62
L 3
v_rsq_f32 v132,v131
M 30,a[68:71],66
s_add_u32 s56,s55,16384
R 86,2,13312
v_fma_f32 v131,v15,v131,v16
M 30,a[76:79],70
s_add_u32 s55,s55,s60
v_mul_f32 v135,v132,v132
v_fma_f32 v131,v14,v135,v131
M 26,a[64:67],66
s_sub_u32 s57,s84,s57
R 90,2,14336
v_fma_f32 v134,v132,v131,v134
M 26,a[72:75],70
L 3
ds_write_b32 v12,v134 offset:2048
M 26,a[80:83],74
s_waitcnt vmcnt(8)
R 94,2,15360
v_fma_f32 v20,-v98,v108,v20
M 26,a[88:91],78
s_barrier
v_fma_f32 v21,-v98,v109,v21
)"
R"(v_mul_f32 v114,v18,v18
M 30,a[84:87],74
R 34,2,16384
v_mul_f32 v115,v19,v19
M 30,a[92:95],78
L 4
v_mul_f32 v116,v20,v20
v_mul_f32 v117,v21,v21
M 30,a[100:103],82
R 38,2,17408
v_fma_f32 v114,v100,v114,v99
M 30,a[108:111],86
D 0,56,0
M 26,a[96:99],82
R 42,2,18432
v_fma_f32 v115,v100,v115,v99
M 26,a[104:107],86
L 3
v_fma_f32 v116,v100,v116,v99
v_fma_f32 v117,v100,v117,v99
M 26,a[112:115],90
R 46,2,19456
v_mul_f32 v114,v18,v114
M 26,a[120:123],94
v_exp_f32 v114,v114
M 30,a[116:119],90
R 50,2,20480
v_mul_f32 v115,v19,v115
M 30,a[124:127],94
L 3
v_exp_f32 v115,v115
M 30,a[132:135],34
R 54,2,21504
v_mul_f32 v116,v20,v116
M 30,a[140:143],38
D 0,56,1024
M 26,a[128:131],34
R 58,2,22528
v_mul_f32 v117,v21,v117
M 26,a[136:139],38
L 3
v_exp_f32 v116,v116
M 26,a[144:147],42
R 62,2,23552
v_fma_f32 v114,v114,v101,v101
M 26,a[152:155],46
v_exp_f32 v117,v117
M 30,a[148:151],42
R 66,2,24576
v_fma_f32 v115,v115,v101,v101
M 30,a[156:159],46
L 3
D 0,56,2048
M 30,a[164:167],50
R 70,2,25600
v_fma_f32 v116,v116,v101,v101
M 30,a[172:175],54
v_rcp_f32 v114,v114
M 26,a[160:163],50
R 74,2,26624
v_fma_f32 v117,v117,v101,v101
M 26,a[168:171],54
L 3
v_rcp_f32 v115,v115
M 26,a[176:179],58
R 78,2,27648
v_mul_f32 v18,v18,v114
M 26,a[184:187],62
v_rcp_f32 v116,v116
M 30,a[180:183],58
R 82,2,28672
v_mul_f32 v19,v19,v115
M 30,a[188:191],62
L 3
D 0,56,3072
M 30,a[196:199],66
R 86,2,29696
v_mul_f32 v20,v20,v116
M 30,a[204:207],70
v_rcp_f32 v117,v117
M 26,a[192:195],66
R 90,2,30720
v_mul_f32 v21,v21,v117
M 26,a[200:203],70
L 3
s_nop 0
v_cvt_pk_bf16_f32 v122,v18,v19
M 26,a[208:211],74
s_waitcnt vmcnt(4)
R 94,2,31744
v_fma_f32 v22,-v98,v110,v22
M 26,a[216:219],78
s_barrier
ds_read_b32 v7,v6 offset:1024
R 102,13,2048
M 30,a[212:215],74
R 34,1,0
v_fma_f32 v23,-v98,v111,v23
M 30,a[220:223],78
L 5
buffer_store_dword v4,v3,s[36:39],s70 offen offset:0 sc0 sc1
M 30,a[228:231],82
s_mov_b32 s70,s69
R 38,1,1024
v_fma_f32 v24,-v98,v112,v24
M 30,a[236:239],86
s_mov_b32 s69,s68
v_fma_f32 v25,-v98,v113,v25
v_mul_f32 v118,v22,v22
M 26,a[224:227],82
s_add_u32 s68,s68,s63
R 42,1,2048
v_mul_f32 v119,v23,v23
M 26,a[232:235],86
L 5
v_mul_f32 v120,v24,v24
v_mul_f32 v121,v25,v25
v_fma_f32 v118,v100,v118,v99
v_fma_f32 v119,v100,v119,v99
v_fma_f32 v120,v100,v120,v99
v_fma_f32 v121,v100,v121,v99
v_mul_f32 v118,v22,v118
v_mul_f32 v119,v23,v119
v_mul_f32 v120,v24,v120
v_mul_f32 v121,v25,v121
v_exp_f32 v118,v118
v_exp_f32 v119,v119
v_exp_f32 v120,v120
v_exp_f32 v121,v121
M 26,a[240:243],90
L 4
R 46,1,3072
v_fma_f32 v118,v118,v101,v101
v_fma_f32 v119,v119,v101,v101
v_fma_f32 v120,v120,v101,v101
v_fma_f32 v121,v121,v101,v101
v_rcp_f32 v118,v118
v_rcp_f32 v119,v119
v_rcp_f32 v120,v120
v_rcp_f32 v121,v121
v_cmp_eq_u32 vcc,s53,v7
M 26,a[248:251],94
v_cvt_pk_bf16_f32 v123,v20,v21
v_mul_f32 v22,v22,v118
v_mul_f32 v23,v23,v119
v_mul_f32 v24,v24,v120
v_mul_f32 v25,v25,v121
v_cvt_pk_bf16_f32 v124,v22,v23
v_cvt_pk_bf16_f32 v125,v24,v25
M 30,a[244:247],90
L 2
s_cmp_eq_u32 vcc_lo,-1
R 50,1,4096
buffer_store_dwordx4 v[122:125],v0,s[36:39],s66 offen offset:0 sc0 sc1
M 30,a[252:255],94
s_cbranch_scc1 Lg3o_F0_%=
Lg3o_K0_%=:
Lg3o_L0_%=:
v_mfma_f32_16x16x32_bf16 v[22:25],a[4:7],v[34:37],0
s_add_u32 m0,s59,3072
R 54,1,5120
v_add_f32_dpp v131,v130,v130 quad_perm:[1,0,3,2] row_mask:0xf bank_mask:0xf
M 22,a[12:15],38
buffer_load_dword v8,s[36:39],s58 offen offset:0 sc1 lds
v_mfma_f32_16x16x32_bf16 v[18:21],a[0:3],v[34:37],0
s_mov_b32 s66,s65
R 58,1,6144
v_add_f32_dpp v131,v131,v131 quad_perm:[2,3,0,1] row_mask:0xf bank_mask:0xf
M 18,a[8:11],38
L 3
v_fma_f32 v26,-v102,v106,v26
v_fma_f32 v27,-v102,v107,v27
M 18,a[16:19],42
s_add_u32 s65,s65,s62
R 62,1,7168
v_add_f32_dpp v134,v131,v131 row_half_mirror row_mask:0xf bank_mask:0xf
M 18,a[24:27],46
v_fma_f32 v28,-v102,v108,v28
v_fma_f32 v29,-v102,v109,v29
M 22,a[20:23],42
R 66,1,8192
v_mov_b32_dpp v131,v134 row_ror:8 row_mask:0xf bank_mask:0xf
M 22,a[28:31],46
L 3
v_fma_f32 v131,-v134,v134,v131
v_max_f32 v131,s87,v131
M 22,a[36:39],50
R 70,1,9216
v_mul_f32 v134,v17,v134
M 22,a[44:47],54
v_rsq_f32 v132,v131
M 18,a[32:35],50
R 74,1,10240
v_fma_f32 v131,v15,v131,v16
M 18,a[40:43],54
L 3
v_mul_f32 v135,v132,v132
v_fma_f32 v131,v14,v135,v131
M 18,a[48:51],58
R 78,1,11264
v_fma_f32 v134,v132,v131,v134
M 18,a[56:59],62
ds_write_b32 v12,v134 offset:0
M 22,a[52:55],58
R 82,1,12288
v_mul_f32 v114,v26,v26
M 22,a[60:63],62
L 4
v_mul_f32 v115,v27,v27
v_mul_f32 v116,v28,v28
M 22,a[68:71],66
R 86,1,13312
v_mul_f32 v117,v29,v29
M 22,a[76:79],70
v_fma_f32 v114,v104,v114,v103
v_fma_f32 v115,v104,v115,v103
M 18,a[64:67],66
R 90,1,14336
v_fma_f32 v116,v104,v116,v103
M 18,a[72:75],70
L 4
v_fma_f32 v117,v104,v117,v103
v_mul_f32 v114,v26,v114
M 18,a[80:83],74
s_waitcnt vmcnt(0)
R 94,1,15360
v_mul_f32 v115,v27,v115
M 18,a[88:91],78
s_barrier
ds_read_b32 v7,v6 offset:3072
v_mul_f32 v116,v28,v116
M 22,a[84:87],74
R 34,1,16384
v_mul_f32 v117,v29,v117
M 22,a[92:95],78
L 4
buffer_store_dword v4,v3,s[36:39],s70 offen offset:0 sc0 sc1
M 22,a[100:103],82
s_mov_b32 s70,0x7fff0000
R 38,1,17408
v_fma_f32 v30,-v102,v110,v30
M 22,a[108:111],86
v_exp_f32 v114,v114
M 18,a[96:99],82
R 42,1,18432
v_fma_f32 v114,v114,v105,v105
M 18,a[104:107],86
L 4
L 3
v_cmp_eq_u32 vcc,s53,v7
M 18,a[112:115],90
R 46,1,19456
v_fma_f32 v31,-v102,v111,v31
M 18,a[120:123],94
v_exp_f32 v115,v115
M 22,a[116:119],90
s_cmp_eq_u32 vcc_lo,-1
R 50,1,20480
v_fma_f32 v115,v115,v105,v105
s_cbranch_scc0 Lg3o_X0_%=
M 22,a[124:127],94
L 3
s_add_u32 m0,s57,0
s_add_u32 s56,s55,0
buffer_load_dword v130,v11,s[36:39],s73 offen offset:0 sc1
M 22,a[132:135],34
s_add_u32 s58,s58,s61
R 54,1,21504
v_fma_f32 v32,-v102,v112,v32
M 22,a[140:143],38
D 0,56,0
M 18,a[128:131],34
s_add_u32 s54,s54,s46
R 58,1,22528
v_fma_f32 v33,-v102,v113,v33
M 18,a[136:139],38
L 3
v_exp_f32 v116,v116
M 18,a[144:147],42
s_add_u32 s73,s73,s64
R 62,1,23552
v_fma_f32 v116,v116,v105,v105
M 18,a[152:155],46
D 0,56,1024
M 22,a[148:151],42
R 66,1,24576
v_mul_f32 v118,v30,v30
M 22,a[156:159],46
L 3
v_exp_f32 v117,v117
M 22,a[164:167],50
R 70,1,25600
v_fma_f32 v117,v117,v105,v105
M 22,a[172:175],54
D 0,56,2048
M 18,a[160:163],50
R 74,1,26624
v_mul_f32 v119,v31,v31
M 18,a[168:171],54
L 3
v_rcp_f32 v114,v114
M 18,a[176:179],58
R 78,1,27648
v_mul_f32 v26,v26,v114
M 18,a[184:187],62
D 0,56,3072
M 22,a[180:183],58
s_add_u32 m0,s59,0
R 82,1,28672
v_mul_f32 v120,v32,v32
M 22,a[188:191],62
L 3
buffer_load_dword v8,s[36:39],s58 offen offset:0 sc1 lds
M 22,a[196:199],66
s_add_u32 m0,s57,16384
R 86,1,29696
v_mul_f32 v121,v33,v33
M 22,a[204:207],70
s_add_u32 s56,s55,16384
v_rcp_f32 v115,v115
M 18,a[192:195],66
s_add_u32 s55,s55,s60
R 90,1,30720
v_mul_f32 v27,v27,v115
M 18,a[200:203],70
L 3
s_nop 0
D 0,56,0
M 18,a[208:211],74
s_waitcnt vmcnt(1)
R 94,1,31744
v_cvt_pk_bf16_f32 v122,v26,v27
M 18,a[216:219],78
s_barrier
ds_read_b32 v7,v6 offset:0
R 98,13,0
M 22,a[212:215],74
s_sub_u32 s57,s84,s57
R 34,2,0
v_fma_f32 v118,v104,v118,v103
M 22,a[220:223],78
L 5
buffer_store_dword v4,v3,s[36:39],s70 offen offset:0 sc0 sc1
M 22,a[228:231],82
s_mov_b32 s70,s69
R 38,2,1024
v_fma_f32 v119,v104,v119,v103
M 22,a[236:239],86
D 0,56,1024
M 18,a[224:227],82
s_mov_b32 s69,s68
R 42,2,2048
v_fma_f32 v120,v104,v120,v103
M 18,a[232:235],86
L 5
v_fma_f32 v121,v104,v121,v103
v_mul_f32 v118,v30,v118
v_mul_f32 v119,v31,v119
v_mul_f32 v120,v32,v120
v_mul_f32 v121,v33,v121
v_exp_f32 v118,v118
v_exp_f32 v119,v119
v_exp_f32 v120,v120
v_exp_f32 v121,v121
M 18,a[240:243],90
L 4
R 46,2,3072
v_rcp_f32 v116,v116
v_rcp_f32 v117,v117
v_fma_f32 v118,v118,v105,v105
v_fma_f32 v119,v119,v105,v105
v_fma_f32 v120,v120,v105,v105
v_fma_f32 v121,v121,v105,v105
v_rcp_f32 v118,v118
v_rcp_f32 v119,v119
v_rcp_f32 v120,v120
v_rcp_f32 v121,v121
v_cmp_eq_u32 vcc,s53,v7
M 18,a[248:251],94
s_add_u32 s68,s68,s63
v_mul_f32 v28,v28,v116
v_mul_f32 v29,v29,v117
v_cvt_pk_bf16_f32 v123,v28,v29
v_mul_f32 v30,v30,v118
v_mul_f32 v31,v31,v119
v_mul_f32 v32,v32,v120
v_mul_f32 v33,v33,v121
v_cvt_pk_bf16_f32 v124,v30,v31
v_cvt_pk_bf16_f32 v125,v32,v33
M 22,a[244:247],90
L 2
s_cmp_eq_u32 vcc_lo,-1
R 50,2,4096
buffer_store_dwordx4 v[122:125],v0,s[36:39],s67 offen offset:0 sc0 sc1
M 22,a[252:255],94
D 0,56,2048
D 0,56,3072
buffer_store_dword v4,v3,s[36:39],s71 offen offset:0 sc0 sc1
buffer_store_dword v4,v3,s[36:39],s71 offen offset:0 sc0 sc1
s_cbranch_scc1 Lg3o_F1_%=
s_branch Lg3o_K1_%=
Lg3o_X0_%=:
M 22,a[124:127],94
L 3
s_add_u32 m0,s59,0
v_fma_f32 v32,-v102,v112,v32
M 22,a[132:135],34
R 54,1,21504
v_fma_f32 v33,-v102,v113,v33
M 22,a[140:143],38
buffer_load_dword v8,s[36:39],s58 offen offset:0 sc1 lds
M 18,a[128:131],34
R 58,1,22528
v_mul_f32 v118,v30,v30
M 18,a[136:139],38
L 3
v_exp_f32 v116,v116
M 18,a[144:147],42
R 62,1,23552
v_fma_f32 v116,v116,v105,v105
M 18,a[152:155],46
v_exp_f32 v117,v117
M 22,a[148:151],42
R 66,1,24576
v_fma_f32 v117,v117,v105,v105
M 22,a[156:159],46
L 3
v_rcp_f32 v114,v114
v_mul_f32 v119,v31,v31
M 22,a[164:167],50
R 70,1,25600
v_mul_f32 v26,v26,v114
M 22,a[172:175],54
v_rcp_f32 v115,v115
M 18,a[160:163],50
R 74,1,26624
v_mul_f32 v27,v27,v115
M 18,a[168:171],54
L 3
v_rcp_f32 v116,v116
M 18,a[176:179],58
R 78,1,27648
v_mul_f32 v28,v28,v116
M 18,a[184:187],62
v_rcp_f32 v117,v117
M 22,a[180:183],58
R 82,1,28672
v_mul_f32 v29,v29,v117
M 22,a[188:191],62
L 3
v_cvt_pk_bf16_f32 v122,v26,v27
v_cvt_pk_bf16_f32 v123,v28,v29
M 22,a[196:199],66
R 86,1,29696
v_mul_f32 v120,v32,v32
M 22,a[204:207],70
v_mul_f32 v121,v33,v33
v_fma_f32 v118,v104,v118,v103
M 18,a[192:195],66
R 90,1,30720
v_fma_f32 v119,v104,v119,v103
M 18,a[200:203],70
L 3
s_nop 0
v_fma_f32 v120,v104,v120,v103
M 18,a[208:211],74
s_waitcnt vmcnt(0)
R 94,1,31744
v_fma_f32 v121,v104,v121,v103
M 18,a[216:219],78
s_barrier
buffer_store_dword v4,v3,s[36:39],s70 offen offset:0 sc0 sc1
M 22,a[212:215],74
s_mov_b32 s70,s69
ds_read_b32 v7,v6 offset:0
R 98,13,0
M 22,a[220:223],78
L 4
v_mul_f32 v118,v30,v118
v_mul_f32 v119,v31,v119
M 22,a[228:231],82
s_mov_b32 s69,s68
v_mul_f32 v120,v32,v120
v_mul_f32 v121,v33,v121
M 22,a[236:239],86
s_add_u32 s68,s68,s63
v_exp_f32 v118,v118
M 18,a[224:227],82
v_exp_f32 v119,v119
M 18,a[232:235],86
L 2
v_exp_f32 v120,v120
v_exp_f32 v121,v121
M 18,a[240:243],90
L 1
v_fma_f32 v118,v118,v105,v105
v_fma_f32 v119,v119,v105,v105
v_fma_f32 v120,v120,v105,v105
v_fma_f32 v121,v121,v105,v105
v_rcp_f32 v118,v118
)"
R"(v_rcp_f32 v119,v119
v_rcp_f32 v120,v120
v_rcp_f32 v121,v121
M 18,a[248:251],94
v_mul_f32 v30,v30,v118
v_mul_f32 v31,v31,v119
v_mul_f32 v32,v32,v120
v_mul_f32 v33,v33,v121
v_cvt_pk_bf16_f32 v124,v30,v31
v_cvt_pk_bf16_f32 v125,v32,v33
M 22,a[244:247],90
buffer_store_dwordx4 v[122:125],v0,s[36:39],s67 offen offset:0 sc0 sc1
v_cmp_eq_u32 vcc,s53,v7
M 22,a[252:255],94
s_mov_b32 s80,1
s_cmp_eq_u32 vcc_lo,-1
s_cbranch_scc0 Lg3o_xn0_%=
buffer_load_dword v130,v11,s[36:39],s73 offen offset:0 sc1
L 0
v_fma_f32 v18,-v98,v106,v18
v_fma_f32 v19,-v98,v107,v19
v_fma_f32 v20,-v98,v108,v20
v_fma_f32 v21,-v98,v109,v21
v_mul_f32 v114,v18,v18
v_mul_f32 v115,v19,v19
s_add_u32 s73,s73,s64
s_add_u32 m0,s57,0
s_add_u32 s56,s55,0
D 0,56,0
v_mul_f32 v116,v20,v20
v_mul_f32 v117,v21,v21
v_fma_f32 v114,v100,v114,v99
v_fma_f32 v115,v100,v115,v99
v_fma_f32 v116,v100,v116,v99
v_fma_f32 v117,v100,v117,v99
D 0,56,1024
v_mul_f32 v114,v18,v114
v_mul_f32 v115,v19,v115
v_mul_f32 v116,v20,v116
v_mul_f32 v117,v21,v117
v_exp_f32 v114,v114
v_exp_f32 v115,v115
D 0,56,2048
v_exp_f32 v116,v116
v_exp_f32 v117,v117
v_fma_f32 v114,v114,v101,v101
v_fma_f32 v115,v115,v101,v101
v_fma_f32 v116,v116,v101,v101
v_fma_f32 v117,v117,v101,v101
D 0,56,3072
v_rcp_f32 v114,v114
v_rcp_f32 v115,v115
v_rcp_f32 v116,v116
v_rcp_f32 v117,v117
v_mul_f32 v18,v18,v114
v_mul_f32 v19,v19,v115
s_add_u32 s58,s58,s61
s_add_u32 m0,s57,16384
s_add_u32 s56,s55,16384
D 0,56,0
v_mul_f32 v20,v20,v116
v_mul_f32 v21,v21,v117
v_cvt_pk_bf16_f32 v122,v18,v19
v_cvt_pk_bf16_f32 v123,v20,v21
v_fma_f32 v22,-v98,v110,v22
v_fma_f32 v23,-v98,v111,v23
D 0,56,1024
v_fma_f32 v24,-v98,v112,v24
v_fma_f32 v25,-v98,v113,v25
v_mul_f32 v118,v22,v22
v_mul_f32 v119,v23,v23
v_mul_f32 v120,v24,v24
v_mul_f32 v121,v25,v25
D 0,56,2048
v_fma_f32 v118,v100,v118,v99
v_fma_f32 v119,v100,v119,v99
v_fma_f32 v120,v100,v120,v99
v_fma_f32 v121,v100,v121,v99
v_mul_f32 v118,v22,v118
v_mul_f32 v119,v23,v119
D 0,56,3072
v_mul_f32 v120,v24,v120
v_mul_f32 v121,v25,v121
v_exp_f32 v118,v118
v_exp_f32 v119,v119
v_exp_f32 v120,v120
v_exp_f32 v121,v121
s_add_u32 s55,s55,s60
s_sub_u32 s57,s84,s57
s_add_u32 s54,s54,s46
v_fma_f32 v118,v118,v101,v101
v_fma_f32 v119,v119,v101,v101
v_fma_f32 v120,v120,v101,v101
v_fma_f32 v121,v121,v101,v101
v_rcp_f32 v118,v118
v_rcp_f32 v119,v119
v_rcp_f32 v120,v120
v_rcp_f32 v121,v121
v_mul_f32 v22,v22,v118
v_mul_f32 v23,v23,v119
v_mul_f32 v24,v24,v120
v_mul_f32 v25,v25,v121
v_cvt_pk_bf16_f32 v124,v22,v23
v_cvt_pk_bf16_f32 v125,v24,v25
buffer_store_dwordx4 v[122:125],v0,s[36:39],s66 offen offset:0 sc0 sc1
s_mov_b32 s72,s69
s_mov_b32 s69,0x7fff0000
s_mov_b32 s66,0x7fff0000
s_mov_b32 s76,0x7fff0000
s_branch Lg3o_gox_%=
Lg3o_xn0_%=:
s_cmp_ge_u32 s54,256
s_cbranch_scc1 Lg3o_fin0_%=
s_add_u32 m0,s59,4096
s_nop 0
buffer_load_dword v8,s[36:39],s58 offen offset:0 sc1 lds
buffer_store_dword v4,v3,s[36:39],s71 offen offset:0 sc0 sc1
s_add_u32 m0,s59,5120
s_nop 0
buffer_load_dword v8,s[36:39],s58 offen offset:0 sc1 lds
buffer_store_dword v4,v3,s[36:39],s71 offen offset:0 sc0 sc1
s_mov_b32 s83,0
L 0
v_fma_f32 v18,-v98,v106,v18
v_fma_f32 v19,-v98,v107,v19
v_fma_f32 v20,-v98,v108,v20
v_fma_f32 v21,-v98,v109,v21
v_mul_f32 v114,v18,v18
v_mul_f32 v115,v19,v19
v_mul_f32 v116,v20,v20
v_mul_f32 v117,v21,v21
v_fma_f32 v114,v100,v114,v99
v_fma_f32 v115,v100,v115,v99
v_fma_f32 v116,v100,v116,v99
v_fma_f32 v117,v100,v117,v99
v_mul_f32 v114,v18,v114
v_mul_f32 v115,v19,v115
v_mul_f32 v116,v20,v116
v_mul_f32 v117,v21,v117
v_exp_f32 v114,v114
v_exp_f32 v115,v115
v_exp_f32 v116,v116
v_exp_f32 v117,v117
v_fma_f32 v114,v114,v101,v101
v_fma_f32 v115,v115,v101,v101
v_fma_f32 v116,v116,v101,v101
v_fma_f32 v117,v117,v101,v101
v_rcp_f32 v114,v114
v_rcp_f32 v115,v115
v_rcp_f32 v116,v116
v_rcp_f32 v117,v117
v_mul_f32 v18,v18,v114
v_mul_f32 v19,v19,v115
v_mul_f32 v20,v20,v116
v_mul_f32 v21,v21,v117
v_cvt_pk_bf16_f32 v122,v18,v19
v_cvt_pk_bf16_f32 v123,v20,v21
v_fma_f32 v22,-v98,v110,v22
v_fma_f32 v23,-v98,v111,v23
v_fma_f32 v24,-v98,v112,v24
v_fma_f32 v25,-v98,v113,v25
v_mul_f32 v118,v22,v22
v_mul_f32 v119,v23,v23
v_mul_f32 v120,v24,v24
v_mul_f32 v121,v25,v25
v_fma_f32 v118,v100,v118,v99
v_fma_f32 v119,v100,v119,v99
v_fma_f32 v120,v100,v120,v99
v_fma_f32 v121,v100,v121,v99
v_mul_f32 v118,v22,v118
v_mul_f32 v119,v23,v119
v_mul_f32 v120,v24,v120
v_mul_f32 v121,v25,v121
v_exp_f32 v118,v118
v_exp_f32 v119,v119
v_exp_f32 v120,v120
v_exp_f32 v121,v121
v_fma_f32 v118,v118,v101,v101
v_fma_f32 v119,v119,v101,v101
v_fma_f32 v120,v120,v101,v101
v_fma_f32 v121,v121,v101,v101
v_rcp_f32 v118,v118
v_rcp_f32 v119,v119
v_rcp_f32 v120,v120
v_rcp_f32 v121,v121
v_mul_f32 v22,v22,v118
v_mul_f32 v23,v23,v119
v_mul_f32 v24,v24,v120
v_mul_f32 v25,v25,v121
v_cvt_pk_bf16_f32 v124,v22,v23
v_cvt_pk_bf16_f32 v125,v24,v25
buffer_store_dwordx4 v[122:125],v0,s[36:39],s66 offen offset:0 sc0 sc1
s_mov_b32 s72,s69
s_mov_b32 s69,0x7fff0000
s_mov_b32 s66,0x7fff0000
s_mov_b32 s76,0x7fff0000
s_branch Lg3o_poll_%=
Lg3o_fin0_%=:
L 0
v_fma_f32 v18,-v98,v106,v18
v_fma_f32 v19,-v98,v107,v19
v_fma_f32 v20,-v98,v108,v20
v_fma_f32 v21,-v98,v109,v21
v_mul_f32 v114,v18,v18
v_mul_f32 v115,v19,v19
v_mul_f32 v116,v20,v20
v_mul_f32 v117,v21,v21
v_fma_f32 v114,v100,v114,v99
v_fma_f32 v115,v100,v115,v99
v_fma_f32 v116,v100,v116,v99
v_fma_f32 v117,v100,v117,v99
v_mul_f32 v114,v18,v114
v_mul_f32 v115,v19,v115
v_mul_f32 v116,v20,v116
v_mul_f32 v117,v21,v117
v_exp_f32 v114,v114
v_exp_f32 v115,v115
v_exp_f32 v116,v116
v_exp_f32 v117,v117
v_fma_f32 v114,v114,v101,v101
v_fma_f32 v115,v115,v101,v101
v_fma_f32 v116,v116,v101,v101
v_fma_f32 v117,v117,v101,v101
v_rcp_f32 v114,v114
v_rcp_f32 v115,v115
v_rcp_f32 v116,v116
v_rcp_f32 v117,v117
v_mul_f32 v18,v18,v114
v_mul_f32 v19,v19,v115
v_mul_f32 v20,v20,v116
v_mul_f32 v21,v21,v117
v_cvt_pk_bf16_f32 v122,v18,v19
v_cvt_pk_bf16_f32 v123,v20,v21
v_fma_f32 v22,-v98,v110,v22
v_fma_f32 v23,-v98,v111,v23
v_fma_f32 v24,-v98,v112,v24
v_fma_f32 v25,-v98,v113,v25
v_mul_f32 v118,v22,v22
v_mul_f32 v119,v23,v23
v_mul_f32 v120,v24,v24
v_mul_f32 v121,v25,v25
v_fma_f32 v118,v100,v118,v99
v_fma_f32 v119,v100,v119,v99
v_fma_f32 v120,v100,v120,v99
v_fma_f32 v121,v100,v121,v99
v_mul_f32 v118,v22,v118
v_mul_f32 v119,v23,v119
v_mul_f32 v120,v24,v120
v_mul_f32 v121,v25,v121
v_exp_f32 v118,v118
v_exp_f32 v119,v119
v_exp_f32 v120,v120
v_exp_f32 v121,v121
v_fma_f32 v118,v118,v101,v101
v_fma_f32 v119,v119,v101,v101
v_fma_f32 v120,v120,v101,v101
v_fma_f32 v121,v121,v101,v101
v_rcp_f32 v118,v118
v_rcp_f32 v119,v119
v_rcp_f32 v120,v120
v_rcp_f32 v121,v121
v_mul_f32 v22,v22,v118
v_mul_f32 v23,v23,v119
v_mul_f32 v24,v24,v120
v_mul_f32 v25,v25,v121
v_cvt_pk_bf16_f32 v124,v22,v23
v_cvt_pk_bf16_f32 v125,v24,v25
buffer_store_dwordx4 v[122:125],v0,s[36:39],s66 offen offset:0 sc0 sc1
s_mov_b32 s72,s69
s_mov_b32 s69,0x7fff0000
s_mov_b32 s66,0x7fff0000
s_mov_b32 s76,0x7fff0000
s_branch Lg3o_done_%=
Lg3o_K1_%=:
Lg3o_L1_%=:
v_mfma_f32_16x16x32_bf16 v[30:33],a[4:7],v[34:37],0
s_add_u32 m0,s59,3072
R 54,2,5120
v_add_f32_dpp v131,v130,v130 quad_perm:[1,0,3,2] row_mask:0xf bank_mask:0xf
M 30,a[12:15],38
buffer_load_dword v8,s[36:39],s58 offen offset:0 sc1 lds
v_mfma_f32_16x16x32_bf16 v[26:29],a[0:3],v[34:37],0
s_mov_b32 s67,s65
R 58,2,6144
v_add_f32_dpp v131,v131,v131 quad_perm:[2,3,0,1] row_mask:0xf bank_mask:0xf
M 26,a[8:11],38
L 3
v_fma_f32 v18,-v98,v106,v18
v_fma_f32 v19,-v98,v107,v19
M 26,a[16:19],42
s_add_u32 s65,s65,s62
R 62,2,7168
v_add_f32_dpp v134,v131,v131 row_half_mirror row_mask:0xf bank_mask:0xf
M 26,a[24:27],46
v_fma_f32 v20,-v98,v108,v20
v_fma_f32 v21,-v98,v109,v21
M 30,a[20:23],42
R 66,2,8192
v_mov_b32_dpp v131,v134 row_ror:8 row_mask:0xf bank_mask:0xf
M 30,a[28:31],46
L 3
v_fma_f32 v131,-v134,v134,v131
v_max_f32 v131,s87,v131
M 30,a[36:39],50
R 70,2,9216
v_mul_f32 v134,v17,v134
M 30,a[44:47],54
v_rsq_f32 v132,v131
M 26,a[32:35],50
R 74,2,10240
v_fma_f32 v131,v15,v131,v16
M 26,a[40:43],54
L 3
v_mul_f32 v135,v132,v132
v_fma_f32 v131,v14,v135,v131
M 26,a[48:51],58
R 78,2,11264
v_fma_f32 v134,v132,v131,v134
M 26,a[56:59],62
ds_write_b32 v12,v134 offset:2048
M 30,a[52:55],58
R 82,2,12288
v_mul_f32 v114,v18,v18
M 30,a[60:63],62
L 4
v_mul_f32 v115,v19,v19
v_mul_f32 v116,v20,v20
M 30,a[68:71],66
R 86,2,13312
v_mul_f32 v117,v21,v21
M 30,a[76:79],70
v_fma_f32 v114,v100,v114,v99
v_fma_f32 v115,v100,v115,v99
M 26,a[64:67],66
R 90,2,14336
v_fma_f32 v116,v100,v116,v99
M 26,a[72:75],70
L 4
v_fma_f32 v117,v100,v117,v99
v_mul_f32 v114,v18,v114
M 26,a[80:83],74
s_waitcnt vmcnt(0)
R 94,2,15360
v_mul_f32 v115,v19,v115
M 26,a[88:91],78
s_barrier
ds_read_b32 v7,v6 offset:3072
v_mul_f32 v116,v20,v116
M 30,a[84:87],74
R 34,2,16384
v_mul_f32 v117,v21,v117
M 30,a[92:95],78
L 4
buffer_store_dword v4,v3,s[36:39],s70 offen offset:0 sc0 sc1
M 30,a[100:103],82
s_mov_b32 s70,0x7fff0000
R 38,2,17408
v_fma_f32 v22,-v98,v110,v22
M 30,a[108:111],86
v_exp_f32 v114,v114
M 26,a[96:99],82
R 42,2,18432
v_fma_f32 v114,v114,v101,v101
M 26,a[104:107],86
L 4
L 3
v_cmp_eq_u32 vcc,s53,v7
M 26,a[112:115],90
R 46,2,19456
v_fma_f32 v23,-v98,v111,v23
M 26,a[120:123],94
v_exp_f32 v115,v115
M 30,a[116:119],90
s_cmp_eq_u32 vcc_lo,-1
R 50,2,20480
v_fma_f32 v115,v115,v101,v101
s_cbranch_scc0 Lg3o_X1_%=
M 30,a[124:127],94
L 3
s_add_u32 m0,s57,0
s_add_u32 s56,s55,0
buffer_load_dword v130,v11,s[36:39],s73 offen offset:0 sc1
M 30,a[132:135],34
s_add_u32 s58,s58,s61
R 54,2,21504
v_fma_f32 v24,-v98,v112,v24
M 30,a[140:143],38
D 0,56,0
M 26,a[128:131],34
s_add_u32 s54,s54,s46
R 58,2,22528
v_fma_f32 v25,-v98,v113,v25
M 26,a[136:139],38
L 3
v_exp_f32 v116,v116
M 26,a[144:147],42
s_add_u32 s73,s73,s64
R 62,2,23552
v_fma_f32 v116,v116,v101,v101
M 26,a[152:155],46
D 0,56,1024
M 30,a[148:151],42
R 66,2,24576
v_mul_f32 v118,v22,v22
M 30,a[156:159],46
L 3
v_exp_f32 v117,v117
M 30,a[164:167],50
R 70,2,25600
v_fma_f32 v117,v117,v101,v101
M 30,a[172:175],54
D 0,56,2048
M 26,a[160:163],50
R 74,2,26624
v_mul_f32 v119,v23,v23
M 26,a[168:171],54
L 3
v_rcp_f32 v114,v114
M 26,a[176:179],58
R 78,2,27648
v_mul_f32 v18,v18,v114
M 26,a[184:187],62
D 0,56,3072
M 30,a[180:183],58
s_add_u32 m0,s59,1024
R 82,2,28672
v_mul_f32 v120,v24,v24
M 30,a[188:191],62
L 3
buffer_load_dword v8,s[36:39],s58 offen offset:0 sc1 lds
M 30,a[196:199],66
s_add_u32 m0,s57,16384
R 86,2,29696
v_mul_f32 v121,v25,v25
M 30,a[204:207],70
s_add_u32 s56,s55,16384
v_rcp_f32 v115,v115
M 26,a[192:195],66
s_add_u32 s55,s55,s60
R 90,2,30720
v_mul_f32 v19,v19,v115
M 26,a[200:203],70
L 3
s_nop 0
D 0,56,0
M 26,a[208:211],74
s_waitcnt vmcnt(1)
R 94,2,31744
v_cvt_pk_bf16_f32 v122,v18,v19
M 26,a[216:219],78
s_barrier
ds_read_b32 v7,v6 offset:1024
R 102,13,2048
M 30,a[212:215],74
s_sub_u32 s57,s84,s57
R 34,1,0
v_fma_f32 v118,v100,v118,v99
M 30,a[220:223],78
L 5
buffer_store_dword v4,v3,s[36:39],s70 offen offset:0 sc0 sc1
M 30,a[228:231],82
s_mov_b32 s70,s69
R 38,1,1024
v_fma_f32 v119,v100,v119,v99
M 30,a[236:239],86
D 0,56,1024
M 26,a[224:227],82
s_mov_b32 s69,s68
R 42,1,2048
v_fma_f32 v120,v100,v120,v99
M 26,a[232:235],86
L 5
v_fma_f32 v121,v100,v121,v99
v_mul_f32 v118,v22,v118
v_mul_f32 v119,v23,v119
v_mul_f32 v120,v24,v120
v_mul_f32 v121,v25,v121
v_exp_f32 v118,v118
v_exp_f32 v119,v119
v_exp_f32 v120,v120
v_exp_f32 v121,v121
M 26,a[240:243],90
L 4
R 46,1,3072
v_rcp_f32 v116,v116
v_rcp_f32 v117,v117
v_fma_f32 v118,v118,v101,v101
v_fma_f32 v119,v119,v101,v101
v_fma_f32 v120,v120,v101,v101
v_fma_f32 v121,v121,v101,v101
v_rcp_f32 v118,v118
v_rcp_f32 v119,v119
v_rcp_f32 v120,v120
v_rcp_f32 v121,v121
v_cmp_eq_u32 vcc,s53,v7
M 26,a[248:251],94
s_add_u32 s68,s68,s63
v_mul_f32 v20,v20,v116
v_mul_f32 v21,v21,v117
v_cvt_pk_bf16_f32 v123,v20,v21
)"
R"(v_mul_f32 v22,v22,v118
v_mul_f32 v23,v23,v119
v_mul_f32 v24,v24,v120
v_mul_f32 v25,v25,v121
v_cvt_pk_bf16_f32 v124,v22,v23
v_cvt_pk_bf16_f32 v125,v24,v25
M 30,a[244:247],90
L 2
s_cmp_eq_u32 vcc_lo,-1
R 50,1,4096
buffer_store_dwordx4 v[122:125],v0,s[36:39],s66 offen offset:0 sc0 sc1
M 30,a[252:255],94
D 0,56,2048
D 0,56,3072
buffer_store_dword v4,v3,s[36:39],s71 offen offset:0 sc0 sc1
buffer_store_dword v4,v3,s[36:39],s71 offen offset:0 sc0 sc1
s_cbranch_scc1 Lg3o_F0_%=
s_branch Lg3o_K0_%=
Lg3o_X1_%=:
M 30,a[124:127],94
L 3
s_add_u32 m0,s59,1024
v_fma_f32 v24,-v98,v112,v24
M 30,a[132:135],34
R 54,2,21504
v_fma_f32 v25,-v98,v113,v25
M 30,a[140:143],38
buffer_load_dword v8,s[36:39],s58 offen offset:0 sc1 lds
M 26,a[128:131],34
R 58,2,22528
v_mul_f32 v118,v22,v22
M 26,a[136:139],38
L 3
v_exp_f32 v116,v116
M 26,a[144:147],42
R 62,2,23552
v_fma_f32 v116,v116,v101,v101
M 26,a[152:155],46
v_exp_f32 v117,v117
M 30,a[148:151],42
R 66,2,24576
v_fma_f32 v117,v117,v101,v101
M 30,a[156:159],46
L 3
v_rcp_f32 v114,v114
v_mul_f32 v119,v23,v23
M 30,a[164:167],50
R 70,2,25600
v_mul_f32 v18,v18,v114
M 30,a[172:175],54
v_rcp_f32 v115,v115
M 26,a[160:163],50
R 74,2,26624
v_mul_f32 v19,v19,v115
M 26,a[168:171],54
L 3
v_rcp_f32 v116,v116
M 26,a[176:179],58
R 78,2,27648
v_mul_f32 v20,v20,v116
M 26,a[184:187],62
v_rcp_f32 v117,v117
M 30,a[180:183],58
R 82,2,28672
v_mul_f32 v21,v21,v117
M 30,a[188:191],62
L 3
v_cvt_pk_bf16_f32 v122,v18,v19
v_cvt_pk_bf16_f32 v123,v20,v21
M 30,a[196:199],66
R 86,2,29696
v_mul_f32 v120,v24,v24
M 30,a[204:207],70
v_mul_f32 v121,v25,v25
v_fma_f32 v118,v100,v118,v99
M 26,a[192:195],66
R 90,2,30720
v_fma_f32 v119,v100,v119,v99
M 26,a[200:203],70
L 3
s_nop 0
v_fma_f32 v120,v100,v120,v99
M 26,a[208:211],74
s_waitcnt vmcnt(0)
R 94,2,31744
v_fma_f32 v121,v100,v121,v99
M 26,a[216:219],78
s_barrier
buffer_store_dword v4,v3,s[36:39],s70 offen offset:0 sc0 sc1
M 30,a[212:215],74
s_mov_b32 s70,s69
ds_read_b32 v7,v6 offset:1024
R 102,13,2048
M 30,a[220:223],78
L 4
v_mul_f32 v118,v22,v118
v_mul_f32 v119,v23,v119
M 30,a[228:231],82
s_mov_b32 s69,s68
v_mul_f32 v120,v24,v120
v_mul_f32 v121,v25,v121
M 30,a[236:239],86
s_add_u32 s68,s68,s63
v_exp_f32 v118,v118
M 26,a[224:227],82
v_exp_f32 v119,v119
M 26,a[232:235],86
L 2
v_exp_f32 v120,v120
v_exp_f32 v121,v121
M 26,a[240:243],90
L 1
v_fma_f32 v118,v118,v101,v101
v_fma_f32 v119,v119,v101,v101
v_fma_f32 v120,v120,v101,v101
v_fma_f32 v121,v121,v101,v101
v_rcp_f32 v118,v118
v_rcp_f32 v119,v119
v_rcp_f32 v120,v120
v_rcp_f32 v121,v121
M 26,a[248:251],94
v_mul_f32 v22,v22,v118
v_mul_f32 v23,v23,v119
v_mul_f32 v24,v24,v120
v_mul_f32 v25,v25,v121
v_cvt_pk_bf16_f32 v124,v22,v23
v_cvt_pk_bf16_f32 v125,v24,v25
M 30,a[244:247],90
buffer_store_dwordx4 v[122:125],v0,s[36:39],s66 offen offset:0 sc0 sc1
v_cmp_eq_u32 vcc,s53,v7
M 30,a[252:255],94
s_mov_b32 s80,0
s_cmp_eq_u32 vcc_lo,-1
s_cbranch_scc0 Lg3o_xn1_%=
buffer_load_dword v130,v11,s[36:39],s73 offen offset:0 sc1
L 0
v_fma_f32 v26,-v102,v106,v26
v_fma_f32 v27,-v102,v107,v27
v_fma_f32 v28,-v102,v108,v28
v_fma_f32 v29,-v102,v109,v29
v_mul_f32 v114,v26,v26
v_mul_f32 v115,v27,v27
s_add_u32 s73,s73,s64
s_add_u32 m0,s57,0
s_add_u32 s56,s55,0
D 0,56,0
v_mul_f32 v116,v28,v28
v_mul_f32 v117,v29,v29
v_fma_f32 v114,v104,v114,v103
v_fma_f32 v115,v104,v115,v103
v_fma_f32 v116,v104,v116,v103
v_fma_f32 v117,v104,v117,v103
D 0,56,1024
v_mul_f32 v114,v26,v114
v_mul_f32 v115,v27,v115
v_mul_f32 v116,v28,v116
v_mul_f32 v117,v29,v117
v_exp_f32 v114,v114
v_exp_f32 v115,v115
D 0,56,2048
v_exp_f32 v116,v116
v_exp_f32 v117,v117
v_fma_f32 v114,v114,v105,v105
v_fma_f32 v115,v115,v105,v105
v_fma_f32 v116,v116,v105,v105
v_fma_f32 v117,v117,v105,v105
D 0,56,3072
v_rcp_f32 v114,v114
v_rcp_f32 v115,v115
v_rcp_f32 v116,v116
v_rcp_f32 v117,v117
v_mul_f32 v26,v26,v114
v_mul_f32 v27,v27,v115
s_add_u32 s58,s58,s61
s_add_u32 m0,s57,16384
s_add_u32 s56,s55,16384
D 0,56,0
v_mul_f32 v28,v28,v116
v_mul_f32 v29,v29,v117
v_cvt_pk_bf16_f32 v122,v26,v27
v_cvt_pk_bf16_f32 v123,v28,v29
v_fma_f32 v30,-v102,v110,v30
v_fma_f32 v31,-v102,v111,v31
D 0,56,1024
v_fma_f32 v32,-v102,v112,v32
v_fma_f32 v33,-v102,v113,v33
v_mul_f32 v118,v30,v30
v_mul_f32 v119,v31,v31
v_mul_f32 v120,v32,v32
v_mul_f32 v121,v33,v33
D 0,56,2048
v_fma_f32 v118,v104,v118,v103
v_fma_f32 v119,v104,v119,v103
v_fma_f32 v120,v104,v120,v103
v_fma_f32 v121,v104,v121,v103
v_mul_f32 v118,v30,v118
v_mul_f32 v119,v31,v119
D 0,56,3072
v_mul_f32 v120,v32,v120
v_mul_f32 v121,v33,v121
v_exp_f32 v118,v118
v_exp_f32 v119,v119
v_exp_f32 v120,v120
v_exp_f32 v121,v121
s_add_u32 s55,s55,s60
s_sub_u32 s57,s84,s57
s_add_u32 s54,s54,s46
v_fma_f32 v118,v118,v105,v105
v_fma_f32 v119,v119,v105,v105
v_fma_f32 v120,v120,v105,v105
v_fma_f32 v121,v121,v105,v105
v_rcp_f32 v118,v118
v_rcp_f32 v119,v119
v_rcp_f32 v120,v120
v_rcp_f32 v121,v121
v_mul_f32 v30,v30,v118
v_mul_f32 v31,v31,v119
v_mul_f32 v32,v32,v120
v_mul_f32 v33,v33,v121
v_cvt_pk_bf16_f32 v124,v30,v31
v_cvt_pk_bf16_f32 v125,v32,v33
buffer_store_dwordx4 v[122:125],v0,s[36:39],s67 offen offset:0 sc0 sc1
s_mov_b32 s72,s69
s_mov_b32 s69,0x7fff0000
s_mov_b32 s67,0x7fff0000
s_mov_b32 s77,0x7fff0000
s_branch Lg3o_gox_%=
Lg3o_xn1_%=:
s_cmp_ge_u32 s54,256
s_cbranch_scc1 Lg3o_fin1_%=
s_add_u32 m0,s59,4096
s_nop 0
buffer_load_dword v8,s[36:39],s58 offen offset:0 sc1 lds
buffer_store_dword v4,v3,s[36:39],s71 offen offset:0 sc0 sc1
s_add_u32 m0,s59,5120
s_nop 0
buffer_load_dword v8,s[36:39],s58 offen offset:0 sc1 lds
buffer_store_dword v4,v3,s[36:39],s71 offen offset:0 sc0 sc1
s_mov_b32 s83,0
L 0
v_fma_f32 v26,-v102,v106,v26
v_fma_f32 v27,-v102,v107,v27
v_fma_f32 v28,-v102,v108,v28
v_fma_f32 v29,-v102,v109,v29
v_mul_f32 v114,v26,v26
v_mul_f32 v115,v27,v27
v_mul_f32 v116,v28,v28
v_mul_f32 v117,v29,v29
v_fma_f32 v114,v104,v114,v103
v_fma_f32 v115,v104,v115,v103
v_fma_f32 v116,v104,v116,v103
v_fma_f32 v117,v104,v117,v103
v_mul_f32 v114,v26,v114
v_mul_f32 v115,v27,v115
v_mul_f32 v116,v28,v116
v_mul_f32 v117,v29,v117
v_exp_f32 v114,v114
v_exp_f32 v115,v115
v_exp_f32 v116,v116
v_exp_f32 v117,v117
v_fma_f32 v114,v114,v105,v105
v_fma_f32 v115,v115,v105,v105
v_fma_f32 v116,v116,v105,v105
v_fma_f32 v117,v117,v105,v105
v_rcp_f32 v114,v114
v_rcp_f32 v115,v115
v_rcp_f32 v116,v116
v_rcp_f32 v117,v117
v_mul_f32 v26,v26,v114
v_mul_f32 v27,v27,v115
v_mul_f32 v28,v28,v116
v_mul_f32 v29,v29,v117
v_cvt_pk_bf16_f32 v122,v26,v27
v_cvt_pk_bf16_f32 v123,v28,v29
v_fma_f32 v30,-v102,v110,v30
v_fma_f32 v31,-v102,v111,v31
v_fma_f32 v32,-v102,v112,v32
v_fma_f32 v33,-v102,v113,v33
v_mul_f32 v118,v30,v30
v_mul_f32 v119,v31,v31
v_mul_f32 v120,v32,v32
v_mul_f32 v121,v33,v33
v_fma_f32 v118,v104,v118,v103
v_fma_f32 v119,v104,v119,v103
v_fma_f32 v120,v104,v120,v103
v_fma_f32 v121,v104,v121,v103
v_mul_f32 v118,v30,v118
v_mul_f32 v119,v31,v119
v_mul_f32 v120,v32,v120
v_mul_f32 v121,v33,v121
v_exp_f32 v118,v118
v_exp_f32 v119,v119
v_exp_f32 v120,v120
v_exp_f32 v121,v121
v_fma_f32 v118,v118,v105,v105
v_fma_f32 v119,v119,v105,v105
v_fma_f32 v120,v120,v105,v105
v_fma_f32 v121,v121,v105,v105
v_rcp_f32 v118,v118
v_rcp_f32 v119,v119
v_rcp_f32 v120,v120
v_rcp_f32 v121,v121
v_mul_f32 v30,v30,v118
v_mul_f32 v31,v31,v119
v_mul_f32 v32,v32,v120
v_mul_f32 v33,v33,v121
v_cvt_pk_bf16_f32 v124,v30,v31
v_cvt_pk_bf16_f32 v125,v32,v33
buffer_store_dwordx4 v[122:125],v0,s[36:39],s67 offen offset:0 sc0 sc1
s_mov_b32 s72,s69
s_mov_b32 s69,0x7fff0000
s_mov_b32 s67,0x7fff0000
s_mov_b32 s77,0x7fff0000
s_branch Lg3o_poll_%=
Lg3o_fin1_%=:
L 0
v_fma_f32 v26,-v102,v106,v26
v_fma_f32 v27,-v102,v107,v27
v_fma_f32 v28,-v102,v108,v28
v_fma_f32 v29,-v102,v109,v29
v_mul_f32 v114,v26,v26
v_mul_f32 v115,v27,v27
v_mul_f32 v116,v28,v28
v_mul_f32 v117,v29,v29
v_fma_f32 v114,v104,v114,v103
v_fma_f32 v115,v104,v115,v103
v_fma_f32 v116,v104,v116,v103
v_fma_f32 v117,v104,v117,v103
v_mul_f32 v114,v26,v114
v_mul_f32 v115,v27,v115
v_mul_f32 v116,v28,v116
v_mul_f32 v117,v29,v117
v_exp_f32 v114,v114
v_exp_f32 v115,v115
v_exp_f32 v116,v116
v_exp_f32 v117,v117
v_fma_f32 v114,v114,v105,v105
v_fma_f32 v115,v115,v105,v105
v_fma_f32 v116,v116,v105,v105
v_fma_f32 v117,v117,v105,v105
v_rcp_f32 v114,v114
v_rcp_f32 v115,v115
v_rcp_f32 v116,v116
v_rcp_f32 v117,v117
v_mul_f32 v26,v26,v114
v_mul_f32 v27,v27,v115
v_mul_f32 v28,v28,v116
v_mul_f32 v29,v29,v117
v_cvt_pk_bf16_f32 v122,v26,v27
v_cvt_pk_bf16_f32 v123,v28,v29
v_fma_f32 v30,-v102,v110,v30
v_fma_f32 v31,-v102,v111,v31
v_fma_f32 v32,-v102,v112,v32
v_fma_f32 v33,-v102,v113,v33
v_mul_f32 v118,v30,v30
v_mul_f32 v119,v31,v31
v_mul_f32 v120,v32,v32
v_mul_f32 v121,v33,v33
v_fma_f32 v118,v104,v118,v103
v_fma_f32 v119,v104,v119,v103
v_fma_f32 v120,v104,v120,v103
v_fma_f32 v121,v104,v121,v103
v_mul_f32 v118,v30,v118
v_mul_f32 v119,v31,v119
v_mul_f32 v120,v32,v120
v_mul_f32 v121,v33,v121
v_exp_f32 v118,v118
v_exp_f32 v119,v119
v_exp_f32 v120,v120
v_exp_f32 v121,v121
v_fma_f32 v118,v118,v105,v105
v_fma_f32 v119,v119,v105,v105
v_fma_f32 v120,v120,v105,v105
v_fma_f32 v121,v121,v105,v105
v_rcp_f32 v118,v118
v_rcp_f32 v119,v119
v_rcp_f32 v120,v120
v_rcp_f32 v121,v121
v_mul_f32 v30,v30,v118
v_mul_f32 v31,v31,v119
v_mul_f32 v32,v32,v120
v_mul_f32 v33,v33,v121
v_cvt_pk_bf16_f32 v124,v30,v31
v_cvt_pk_bf16_f32 v125,v32,v33
buffer_store_dwordx4 v[122:125],v0,s[36:39],s67 offen offset:0 sc0 sc1
s_mov_b32 s72,s69
s_mov_b32 s69,0x7fff0000
s_mov_b32 s67,0x7fff0000
s_mov_b32 s77,0x7fff0000
s_branch Lg3o_done_%=
Lg3o_done_%=:
s_waitcnt vmcnt(1)
s_barrier
buffer_store_dword v4,v3,s[36:39],s70 offen offset:0 sc0 sc1
s_waitcnt vmcnt(1)
s_barrier
buffer_store_dword v4,v3,s[36:39],s72 offen offset:0 sc0 sc1
s_waitcnt vmcnt(0) lgkmcnt(0)
s_barrier
s_endpgm
Lcode_g3o_e:
.purgem M
.purgem R
.purgem T
.purgem D
.purgem L
)"
  :: "s"(ws), "s"(__builtin_amdgcn_readfirstlane(sWave)), "s"(__builtin_amdgcn_readfirstlane(sG0)), "s"(__builtin_amdgcn_readfirstlane(sStep)), "s"(__builtin_amdgcn_readfirstlane(sOutOff)), "s"(__builtin_amdgcn_readfirstlane(sFoutOff)), "s"(__builtin_amdgcn_readfirstlane(sAuxOff)), "s"(__builtin_amdgcn_readfirstlane(sWw)), "s"(__builtin_amdgcn_readfirstlane(sBuf)), "s"(__builtin_amdgcn_readfirstlane(sExp)), "v"(vL16), "v"(vFlag), "v"(vSnapOff), "v"(vSnapRd), "v"(vWd0), "v"(vWd1), "v"(vWr0), "v"(vWr1), "v"(vWr2), "v"(vOutS), "v"(vAux0), "v"(vAux1), "v"(vAux2), "v"(vAux3), "v"(vAn), "v"(vAe), "s"(w3), "s"(out) : "v255", "v0", "v1", "v2", "v3", "v4", "v5", "v6", "v7", "v8", "v9", "v10", "v11", "v12", "v13", "v14", "v15", "v16", "v17", "v18", "v19", "v20", "v21", "v22", "v23", "v24", "v25", "v26", "v27", "v28", "v29", "v30", "v31", "v32", "v33", "v34", "v35", "v36", "v37", "v38", "v39", "v40", "v41", "v42", "v43", "v44", "v45", "v46", "v47", "v48", "v49", "v50", "v51", "v52", "v53", "v54", "v55", "v56", "v57", "v58", "v59", "v60", "v61", "v62", "v63", "v64", "v65", "v66", "v67", "v68", "v69", "v70", "v71", "v72", "v73", "v74", "v75", "v76", "v77", "v78", "v79", "v80", "v81", "v82", "v83", "v84", "v85", "v86", "v87", "v88", "v89", "v90", "v91", "v92", "v93", "v94", "v95", "v96", "v97", "v98", "v99", "v100", "v101", "v102", "v103", "v104", "v105", "v106", "v107", "v108", "v109", "v110", "v111", "v112", "v113", "v114", "v115", "v116", "v117", "v118", "v119", "v120", "v121", "v122", "v123", "v124", "v125", "v126", "v127", "v128", "v129", "v130", "v131", "v132", "v133", "v134", "v135", "v136", "v137", "v138", "v139", "v140", "v141", "v142", "v143", "v144", "v145", "v146", "v147", "v148", "v149", "v150", "v151", "v152", "v153", "v154", "v155", "v156", "v157", "v158", "v159", "v160", "v161", "v162", "v163", "v164", "v165", "v166", "v167", "v168", "v169", "v170", "v171", "v172", "v173", "v174", "v175", "v176", "v177", "v178", "v179", "v180", "v181", "v182", "v183", "v184", "v185", "v186", "v187", "v188", "v189", "v190", "v191", "v192", "v193", "v194", "v195", "v196", "v197", "v198", "v199", "v200", "v201", "v202", "v203", "v204", "v205", "v206", "v207", "v208", "v209", "v210", "v211", "v212", "v213", "v214", "v215", "v216", "v217", "v218", "v219", "v220", "v221", "v222", "v223", "v224", "v225", "v226", "v227", "v228", "v229", "v230", "v231", "a0", "a1", "a2", "a3", "a4", "a5", "a6", "a7", "a8", "a9", "a10", "a11", "a12", "a13", "a14", "a15", "a16", "a17", "a18", "a19", "a20", "a21", "a22", "a23", "a24", "a25", "a26", "a27", "a28", "a29", "a30", "a31", "a32", "a33", "a34", "a35", "a36", "a37", "a38", "a39", "a40", "a41", "a42", "a43", "a44", "a45", "a46", "a47", "a48", "a49", "a50", "a51", "a52", "a53", "a54", "a55", "a56", "a57", "a58", "a59", "a60", "a61", "a62", "a63", "a64", "a65", "a66", "a67", "a68", "a69", "a70", "a71", "a72", "a73", "a74", "a75", "a76", "a77", "a78", "a79", "a80", "a81", "a82", "a83", "a84", "a85", "a86", "a87", "a88", "a89", "a90", "a91", "a92", "a93", "a94", "a95", "a96", "a97", "a98", "a99", "a100", "a101", "a102", "a103", "a104", "a105", "a106", "a107", "a108", "a109", "a110", "a111", "a112", "a113", "a114", "a115", "a116", "a117", "a118", "a119", "a120", "a121", "a122", "a123", "a124", "a125", "a126", "a127", "a128", "a129", "a130", "a131", "a132", "a133", "a134", "a135", "a136", "a137", "a138", "a139", "a140", "a141", "a142", "a143", "a144", "a145", "a146", "a147", "a148", "a149", "a150", "a151", "a152", "a153", "a154", "a155", "a156", "a157", "a158", "a159", "a160", "a161", "a162", "a163", "a164", "a165", "a166", "a167", "a168", "a169", "a170", "a171", "a172", "a173", "a174", "a175", "a176", "a177", "a178", "a179", "a180", "a181", "a182", "a183", "a184", "a185", "a186", "a187", "a188", "a189", "a190", "a191", "a192", "a193", "a194", "a195", "a196", "a197", "a198", "a199", "a200", "a201", "a202", "a203", "a204", "a205", "a206", "a207", "a208", "a209", "a210", "a211", "a212", "a213", "a214", "a215", "a216", "a217", "a218", "a219", "a220", "a221", "a222", "a223", "a224", "a225", "a226", "a227", "a228", "a229", "a230", "a231", "a232", "a233", "a234", "a235", "a236", "a237", "a238", "a239", "a240", "a241", "a242", "a243", "a244", "a245", "a246", "a247", "a248", "a249", "a250", "a251", "a252", "a253", "a254", "a255", "s36", "s37", "s38", "s39", "s40", "s41", "s42", "s43", "s44", "s45", "s46", "s47", "s48", "s49", "s50", "s51", "s52", "s53", "s54", "s55", "s56", "s57", "s58", "s59", "s60", "s61", "s62", "s63", "s64", "s65", "s66", "s67", "s68", "s69", "s70", "s71", "s72", "s73", "s74", "s75", "s76", "s77", "s78", "s79", "s80", "s81", "s82", "s83", "s84", "s85", "s86", "s87", "s88", "s89", "s90", "s91", "s92", "s93", "s94", "s95", "s96", "s97", "vcc", "scc", "memory"); }
    __builtin_unreachable();
  }
  if (kind < 3) {
    const unsigned sStep = 4;
    COMMON(kind == 1 ? 8 : 16, kind == 1 ? fj * 128 : fj < 8 ? 1024 + fj * 128 : (fj & 2) * 1024 + (fj - 8) * 128, 1024, 131072, 2048, 256, kind == 1 ? 0 : 32768);
    const int sr = l15 - ((wave & 2) ? -1 : 1), sr2 = l15 - ((wave & 2) ? -2 : 2), nx = (slice >> 1) & 1;
    auto src = [&](int r_) { return (unsigned)((r_ & 16) ? 1024 + lg * 64 + (r_ & 3) * 16 : (lg * 16 + r_) * 16); };
    const unsigned vOutS = src(sr2), vSp = (sr2 & 16) ? OOB : vOutS;
    (void)vWr2;
    const unsigned vAn = kind == 1 ? HU + vL16 : src(sr);
    const unsigned vAe = kind == 1 ? ((l15 >> 2) == (nx ? 0 : 3) ? (nx ? 0 : 2 * HU) + 1024 + lg * 64 + (l15 & 3) * 16 : OOB) : (sr & 16) ? OOB : vAn;
    const unsigned vAux0 = l15 * 16, vAux1 = 139264 + (l15 * 2 + (lg & 1)) * 16 + wave * 4 + (lg >> 1) * 512, vAux2 = 139264 + (l15 * 2 + (lg & 1)) * 16;
    const unsigned vAux3 = (wave == 0 && lg < 2) ? slice * 128 + l15 * 8 + lg * 4 : OOB;
    const unsigned sOutOff = kind == 1 ? OFF_HS - HU + (slice * 4 + wave) * 1280 : OFF_X1 + (slice * 4 + wave) * 1024, sFoutOff = (kind == 1 ? OFF_FH : OFF_FX1) + slice * 128;
    const unsigned sAuxOff = OFF_XP + (slice * 4 + wave) * 1024, sBuf = kind, sWw = 16 * wave * 2048 + slice * 256;
    const u16* w12 = kind == 1 ? w1 : w2;
    const unsigned sExp = __builtin_amdgcn_readfirstlane(ep);
    unsigned epOut;
    asm volatile(
R"(.macro M d,w,x
v_mfma_f32_16x16x32_bf16 v[\d:\d+3], \w, v[\x:\x+3], v[\d:\d+3]
.endm
.macro R d,a,o
ds_read_b128 v[\d:\d+3], v\a offset:\o
.endm
.macro T d,a,o
ds_read_b64_tr_b16 \d, v\a offset:\o
.endm
.macro D a,s,o
buffer_load_dwordx4 v\a, s[36:39], s\s offen offset:\o sc1 lds
.endm
.macro L c
s_waitcnt lgkmcnt(\c)
.endm
Lcode_g12_b:
s_cmp_eq_u32 %9,2
s_cbranch_scc1 Lg2_begin_%=
s_mov_b64 s[36:37],%1
s_mov_b32 s38,111243392
s_mov_b32 s39,0x00020000
s_mov_b32 s44,%2
s_mov_b32 s45,%3
s_mov_b32 s46,%4
s_mov_b32 s47,%5
s_mov_b32 s48,%6
s_mov_b32 s49,%7
s_mov_b32 s50,%8
s_mov_b32 s51,%9
s_mov_b32 s53,%10
v_mov_b32 v0,%11
v_mov_b32 v3,%12
v_mov_b32 v5,%13
v_mov_b32 v6,%14
v_mov_b32 v18,%15
v_mov_b32 v19,%16
v_mov_b32 v20,%17
v_mov_b32 v21,%18
v_mov_b32 v9,%19
v_mov_b32 v10,%20
v_mov_b32 v11,%21
v_mov_b32 v12,%22
v_mov_b32 v13,%23
v_mov_b32 v14,%24
v_mov_b32 v15,%25
v_mov_b32 v16,%26
s_mov_b64 s[40:41],%27
s_mov_b32 s42,2097152
s_mov_b32 s43,0x00020000
s_lshl_b32 s57,s44,11
s_waitcnt vmcnt(20) lgkmcnt(0)
s_barrier
s_add_u32 s55,s50,786432
s_add_u32 m0,s57,98304
s_nop 0
buffer_load_dwordx4 v18,s[40:43],s55 offen offset:0  lds
s_add_u32 m0,s57,99328
s_nop 0
buffer_load_dwordx4 v19,s[40:43],s55 offen offset:0  lds
s_add_u32 m0,s57,106368
s_nop 0
buffer_load_dwordx4 v18,s[40:43],s55 offen offset:128  lds
s_add_u32 m0,s57,107392
s_nop 0
buffer_load_dwordx4 v19,s[40:43],s55 offen offset:128  lds
v_add_u32 v26,0,v20
v_add_u32 v27,0,v21
T a[0:1],26,0
T a[2:3],26,512
T a[4:5],27,0
T a[6:7],27,512
T a[8:9],26,4096
T a[10:11],26,4608
T a[12:13],27,4096
T a[14:15],27,4608
s_waitcnt vmcnt(20) lgkmcnt(0)
s_barrier
s_add_u32 s55,s50,917504
s_add_u32 m0,s57,0
s_nop 0
buffer_load_dwordx4 v18,s[40:43],s55 offen offset:0  lds
s_add_u32 m0,s57,1024
s_nop 0
buffer_load_dwordx4 v19,s[40:43],s55 offen offset:0  lds
s_add_u32 m0,s57,8064
s_nop 0
buffer_load_dwordx4 v18,s[40:43],s55 offen offset:128  lds
s_add_u32 m0,s57,9088
s_nop 0
buffer_load_dwordx4 v19,s[40:43],s55 offen offset:128  lds
v_add_u32 v26,16384,v20
v_add_u32 v27,16384,v21
T a[16:17],26,0
T a[18:19],26,512
T a[20:21],27,0
T a[22:23],27,512
T a[24:25],26,4096
T a[26:27],26,4608
T a[28:29],27,4096
T a[30:31],27,4608
s_waitcnt vmcnt(20) lgkmcnt(0)
s_barrier
s_add_u32 s55,s50,1048576
s_add_u32 m0,s57,16384
s_nop 0
buffer_load_dwordx4 v18,s[40:43],s55 offen offset:0  lds
s_add_u32 m0,s57,17408
s_nop 0
buffer_load_dwordx4 v19,s[40:43],s55 offen offset:0  lds
s_add_u32 m0,s57,24448
s_nop 0
buffer_load_dwordx4 v18,s[40:43],s55 offen offset:128  lds
s_add_u32 m0,s57,25472
s_nop 0
buffer_load_dwordx4 v19,s[40:43],s55 offen offset:128  lds
v_add_u32 v26,32768,v20
v_add_u32 v27,32768,v21
T a[32:33],26,0
T a[34:35],26,512
T a[36:37],27,0
T a[38:39],27,512
T a[40:41],26,4096
T a[42:43],26,4608
T a[44:45],27,4096
T a[46:47],27,4608
s_waitcnt vmcnt(20) lgkmcnt(0)
s_barrier
s_add_u32 s55,s50,1179648
s_add_u32 m0,s57,32768
s_nop 0
buffer_load_dwordx4 v18,s[40:43],s55 offen offset:0  lds
s_add_u32 m0,s57,33792
s_nop 0
buffer_load_dwordx4 v19,s[40:43],s55 offen offset:0  lds
s_add_u32 m0,s57,40832
s_nop 0
buffer_load_dwordx4 v18,s[40:43],s55 offen offset:128  lds
s_add_u32 m0,s57,41856
s_nop 0
buffer_load_dwordx4 v19,s[40:43],s55 offen offset:128  lds
v_add_u32 v26,49152,v20
v_add_u32 v27,49152,v21
T a[48:49],26,0
T a[50:51],26,512
T a[52:53],27,0
T a[54:55],27,512
T a[56:57],26,4096
T a[58:59],26,4608
T a[60:61],27,4096
T a[62:63],27,4608
s_waitcnt vmcnt(20) lgkmcnt(0)
s_barrier
s_add_u32 s55,s50,1310720
s_add_u32 m0,s57,49152
s_nop 0
buffer_load_dwordx4 v18,s[40:43],s55 offen offset:0  lds
s_add_u32 m0,s57,50176
s_nop 0
buffer_load_dwordx4 v19,s[40:43],s55 offen offset:0  lds
s_add_u32 m0,s57,57216
s_nop 0
buffer_load_dwordx4 v18,s[40:43],s55 offen offset:128  lds
s_add_u32 m0,s57,58240
s_nop 0
buffer_load_dwordx4 v19,s[40:43],s55 offen offset:128  lds
v_add_u32 v26,65536,v20
v_add_u32 v27,65536,v21
T a[64:65],26,0
T a[66:67],26,512
T a[68:69],27,0
T a[70:71],27,512
T a[72:73],26,4096
T a[74:75],26,4608
T a[76:77],27,4096
T a[78:79],27,4608
s_waitcnt vmcnt(20) lgkmcnt(0)
s_barrier
s_add_u32 s55,s50,1441792
s_add_u32 m0,s57,65536
s_nop 0
buffer_load_dwordx4 v18,s[40:43],s55 offen offset:0  lds
s_add_u32 m0,s57,66560
s_nop 0
buffer_load_dwordx4 v19,s[40:43],s55 offen offset:0  lds
s_add_u32 m0,s57,73600
s_nop 0
buffer_load_dwordx4 v18,s[40:43],s55 offen offset:128  lds
s_add_u32 m0,s57,74624
s_nop 0
buffer_load_dwordx4 v19,s[40:43],s55 offen offset:128  lds
v_add_u32 v26,81920,v20
v_add_u32 v27,81920,v21
T a[80:81],26,0
T a[82:83],26,512
T a[84:85],27,0
T a[86:87],27,512
T a[88:89],26,4096
T a[90:91],26,4608
T a[92:93],27,4096
T a[94:95],27,4608
s_waitcnt vmcnt(20) lgkmcnt(0)
s_barrier
s_add_u32 s55,s50,1572864
s_add_u32 m0,s57,81920
s_nop 0
buffer_load_dwordx4 v18,s[40:43],s55 offen offset:0  lds
s_add_u32 m0,s57,82944
s_nop 0
buffer_load_dwordx4 v19,s[40:43],s55 offen offset:0  lds
s_add_u32 m0,s57,89984
s_nop 0
buffer_load_dwordx4 v18,s[40:43],s55 offen offset:128  lds
s_add_u32 m0,s57,91008
s_nop 0
buffer_load_dwordx4 v19,s[40:43],s55 offen offset:128  lds
v_add_u32 v26,98304,v20
v_add_u32 v27,98304,v21
T a[96:97],26,0
T a[98:99],26,512
T a[100:101],27,0
T a[102:103],27,512
T a[104:105],26,4096
T a[106:107],26,4608
T a[108:109],27,4096
T a[110:111],27,4608
s_waitcnt vmcnt(20) lgkmcnt(0)
s_barrier
s_add_u32 s55,s50,1703936
s_add_u32 m0,s57,98304
s_nop 0
buffer_load_dwordx4 v18,s[40:43],s55 offen offset:0  lds
s_add_u32 m0,s57,99328
s_nop 0
buffer_load_dwordx4 v19,s[40:43],s55 offen offset:0  lds
s_add_u32 m0,s57,106368
s_nop 0
buffer_load_dwordx4 v18,s[40:43],s55 offen offset:128  lds
s_add_u32 m0,s57,107392
s_nop 0
buffer_load_dwordx4 v19,s[40:43],s55 offen offset:128  lds
v_add_u32 v26,0,v20
v_add_u32 v27,0,v21
T a[112:113],26,0
T a[114:115],26,512
T a[116:117],27,0
T a[118:119],27,512
T a[120:121],26,4096
T a[122:123],26,4608
T a[124:125],27,4096
T a[126:127],27,4608
s_waitcnt vmcnt(20) lgkmcnt(0)
s_barrier
s_add_u32 s55,s50,1835008
s_add_u32 m0,s57,0
s_nop 0
buffer_load_dwordx4 v18,s[40:43],s55 offen offset:0  lds
s_add_u32 m0,s57,1024
s_nop 0
buffer_load_dwordx4 v19,s[40:43],s55 offen offset:0  lds
s_add_u32 m0,s57,8064
s_nop 0
buffer_load_dwordx4 v18,s[40:43],s55 offen offset:128  lds
s_add_u32 m0,s57,9088
s_nop 0
buffer_load_dwordx4 v19,s[40:43],s55 offen offset:128  lds
v_add_u32 v26,16384,v20
v_add_u32 v27,16384,v21
T a[128:129],26,0
T a[130:131],26,512
T a[132:133],27,0
T a[134:135],27,512
T a[136:137],26,4096
T a[138:139],26,4608
T a[140:141],27,4096
T a[142:143],27,4608
s_waitcnt vmcnt(20) lgkmcnt(0)
s_barrier
s_add_u32 s55,s50,1966080
s_add_u32 m0,s57,16384
s_nop 0
buffer_load_dwordx4 v18,s[40:43],s55 offen offset:0  lds
s_add_u32 m0,s57,17408
s_nop 0
buffer_load_dwordx4 v19,s[40:43],s55 offen offset:0  lds
s_add_u32 m0,s57,24448
s_nop 0
buffer_load_dwordx4 v18,s[40:43],s55 offen offset:128  lds
s_add_u32 m0,s57,25472
s_nop 0
buffer_load_dwordx4 v19,s[40:43],s55 offen offset:128  lds
v_add_u32 v26,32768,v20
v_add_u32 v27,32768,v21
T a[144:145],26,0
T a[146:147],26,512
T a[148:149],27,0
T a[150:151],27,512
T a[152:153],26,4096
T a[154:155],26,4608
T a[156:157],27,4096
T a[158:159],27,4608
s_waitcnt vmcnt(20) lgkmcnt(0)
s_barrier
v_add_u32 v26,49152,v20
v_add_u32 v27,49152,v21
T a[160:161],26,0
T a[162:163],26,512
T a[164:165],27,0
T a[166:167],27,512
T a[168:169],26,4096
T a[170:171],26,4608
T a[172:173],27,4096
T a[174:175],27,4608
s_waitcnt vmcnt(16) lgkmcnt(0)
s_barrier
v_add_u32 v26,65536,v20
v_add_u32 v27,65536,v21
T a[176:177],26,0
T a[178:179],26,512
T a[180:181],27,0
T a[182:183],27,512
T a[184:185],26,4096
T a[186:187],26,4608
T a[188:189],27,4096
T a[190:191],27,4608
s_waitcnt vmcnt(12) lgkmcnt(0)
s_barrier
v_add_u32 v26,81920,v20
v_add_u32 v27,81920,v21
T a[192:193],26,0
T a[194:195],26,512
T a[196:197],27,0
T a[198:199],27,512
T a[200:201],26,4096
T a[202:203],26,4608
T a[204:205],27,4096
T a[206:207],27,4608
s_waitcnt vmcnt(8) lgkmcnt(0)
s_barrier
v_add_u32 v26,98304,v20
v_add_u32 v27,98304,v21
T a[208:209],26,0
T a[210:211],26,512
T a[212:213],27,0
T a[214:215],27,512
T a[216:217],26,4096
T a[218:219],26,4608
T a[220:221],27,4096
T a[222:223],27,4608
s_waitcnt vmcnt(4) lgkmcnt(0)
s_barrier
v_add_u32 v26,0,v20
v_add_u32 v27,0,v21
T a[224:225],26,0
T a[226:227],26,512
T a[228:229],27,0
T a[230:231],27,512
T a[232:233],26,4096
T a[234:235],26,4608
T a[236:237],27,4096
T a[238:239],27,4608
s_waitcnt vmcnt(0) lgkmcnt(0)
s_barrier
v_add_u32 v26,16384,v20
v_add_u32 v27,16384,v21
T a[240:241],26,0
T a[242:243],26,512
T a[244:245],27,0
T a[246:247],27,512
T a[248:249],26,4096
T a[250:251],26,4608
T a[252:253],27,4096
T a[254:255],27,4608
L 0
s_barrier
v_mov_b32 v34,0x3f803f80
v_mov_b32 v35,0x3f803f80
v_mov_b32 v36,0x3f803f80
v_mov_b32 v37,0x3f803f80
s_nop 1
v_mfma_f32_16x16x32_bf16 v[106:109],a[0:3],v[34:37],0
v_mfma_f32_16x16x32_bf16 v[110:113],a[4:7],v[34:37],0
M 106,a[8:11],34
M 110,a[12:15],34
M 106,a[16:19],34
M 110,a[20:23],34
M 106,a[24:27],34
M 110,a[28:31],34
M 106,a[32:35],34
M 110,a[36:39],34
M 106,a[40:43],34
M 110,a[44:47],34
M 106,a[48:51],34
M 110,a[52:55],34
M 106,a[56:59],34
M 110,a[60:63],34
M 106,a[64:67],34
M 110,a[68:71],34
M 106,a[72:75],34
M 110,a[76:79],34
M 106,a[80:83],34
M 110,a[84:87],34
M 106,a[88:91],34
M 110,a[92:95],34
M 106,a[96:99],34
M 110,a[100:103],34
M 106,a[104:107],34
M 110,a[108:111],34
M 106,a[112:115],34
M 110,a[116:119],34
M 106,a[120:123],34
M 110,a[124:127],34
M 106,a[128:131],34
M 110,a[132:135],34
M 106,a[136:139],34
M 110,a[140:143],34
M 106,a[144:147],34
M 110,a[148:151],34
M 106,a[152:155],34
M 110,a[156:159],34
M 106,a[160:163],34
M 110,a[164:167],34
M 106,a[168:171],34
M 110,a[172:175],34
M 106,a[176:179],34
M 110,a[180:183],34
M 106,a[184:187],34
M 110,a[188:191],34
M 106,a[192:195],34
M 110,a[196:199],34
M 106,a[200:203],34
M 110,a[204:207],34
M 106,a[208:211],34
M 110,a[212:215],34
M 106,a[216:219],34
M 110,a[220:223],34
M 106,a[224:227],34
M 110,a[228:231],34
M 106,a[232:235],34
M 110,a[236:239],34
M 106,a[240:243],34
M 110,a[244:247],34
M 106,a[248:251],34
M 110,a[252:255],34
v_mov_b32 v4,s53
s_mov_b32 s82,0
s_mov_b32 s80,0
s_mov_b32 s54,s45
s_mul_i32 s57,s44,4096
s_mul_i32 s55,s45,32768
s_add_u32 s55,s55,s57
s_add_u32 s55,s55,8388608
s_mul_i32 s57,s44,4096
s_mul_i32 s60,s46,32768
s_mul_i32 s61,s46,1024
s_mul_i32 s62,s46,40960
s_mul_i32 s63,s46,1024
s_mul_i32 s64,s46,512
s_lshl_b32 s59,s44,8
s_add_u32 s59,s59,131072
s_mul_i32 s58,s45,1024
s_add_u32 s58,s58,109056000
s_mul_i32 s65,s45,40960
s_add_u32 s65,s65,s47
s_mul_i32 s68,s45,1024
s_add_u32 s68,s68,s48
s_mul_i32 s73,s45,512
s_add_u32 s73,s73,106954752
s_mul_i32 s74,s45,32768
s_add_u32 s74,s74,s49
s_mul_i32 s75,s45,1024
s_add_u32 s75,s75,107479040
s_mov_b32 s66,0x7fff0000
s_mov_b32 s67,0x7fff0000
s_mov_b32 s69,0x7fff0000
s_mov_b32 s76,0x7fff0000
s_mov_b32 s77,0x7fff0000
s_mov_b32 s70,0x7fff0000
s_mov_b32 s71,0x7fff0000
s_mov_b32 s72,0x7fff0000
v_mov_b32 v1,v0
v_add_u32 v2,16384,v0
s_mov_b32 s84,16384
s_mov_b32 s86,0x3a800000
s_mov_b32 s87,0x3727c5ac
s_mov_b32 s88,0xc0135761
s_mov_b32 s89,0x3d372713
s_lshr_b32 s85,s44,1
s_mul_i32 s85,s85,127
v_lshlrev_b32 v8,22,v0
v_and_b32 v8,0x80000000,v8
v_or_b32 v8,v8,v5
v_add_u32 v2,32768,v0
s_lshl_b32 s84,s57,1
s_add_u32 s84,s84,32768
s_getpc_b64 s[40:41]
Lg1_pc_%=:
s_lshl_b32 s78,s44,13
v_lshlrev_b32 v114,3,v0
v_add_u32 v114,s78,v114
v_min_u32 v114,Lcode_g2_b-Lg1_pc_%=-4,v114
)"
R"(s_mov_b32 m0,163584
s_nop 0
global_load_lds_dword v114,s[40:41]
s_add_u32 m0,s59,4096
s_nop 0
buffer_load_dword v8,s[36:39],s58 offen offset:0 sc1 lds
buffer_store_dword v4,v3,s[36:39],s71 offen offset:0 sc0 sc1
s_add_u32 m0,s59,5120
s_nop 0
buffer_load_dword v8,s[36:39],s58 offen offset:0 sc1 lds
buffer_store_dword v4,v3,s[36:39],s71 offen offset:0 sc0 sc1
s_mov_b32 s83,0
Lg1_poll_%=:
s_add_u32 s78,s83,2
s_and_b32 s78,s78,3
s_lshl_b32 s78,s78,10
s_add_u32 s78,s78,4096
s_add_u32 m0,s59,s78
s_nop 0
buffer_load_dword v8,s[36:39],s58 offen offset:0 sc1 lds
s_and_b32 s78,s83,3
s_lshl_b32 s78,s78,10
v_add_u32 v114,s78,v6
s_waitcnt vmcnt(4)
s_barrier
ds_read_b32 v7,v114 offset:4096
buffer_store_dword v4,v3,s[36:39],s70 offen offset:0 sc0 sc1
s_mov_b32 s70,s71
s_mov_b32 s71,s72
s_mov_b32 s72,0x7fff0000
s_add_u32 s83,s83,1
s_add_u32 s82,s82,1
L 0
v_cmp_eq_u32 vcc,s53,v7
s_nop 0
s_cmp_eq_u32 vcc_lo,-1
s_cbranch_scc1 Lg1_go_%=
s_cmp_lt_u32 s82,20000
s_cbranch_scc1 Lg1_poll_%=
Lg1_go_%=:
s_add_u32 m0,s57,0
s_add_u32 s56,s55,0
D 0,56,0
D 0,56,1024
D 0,56,2048
D 0,56,3072
s_add_u32 s58,s58,s61
s_add_u32 m0,s59,2048
s_nop 0
buffer_load_dword v8,s[36:39],s58 offen offset:0 sc1 lds
s_add_u32 m0,s57,16384
s_add_u32 s56,s55,16384
D 0,56,0
D 0,56,1024
D 0,56,2048
D 0,56,3072
s_add_u32 s55,s55,s60
s_sub_u32 s57,s84,s57
s_add_u32 s54,s54,s46
s_waitcnt vmcnt(4)
s_barrier
buffer_store_dword v4,v3,s[36:39],s70 offen offset:0 sc0 sc1
s_min_u32 s71,s71,s72
buffer_store_dword v4,v3,s[36:39],s71 offen offset:0 sc0 sc1
s_mov_b32 s70,0x7fff0000
s_mov_b32 s71,0x7fff0000
s_mov_b32 s72,0x7fff0000
s_cmp_eq_u32 s80,1
buffer_store_dword v4,v3,s[36:39],s71 offen offset:0 sc0 sc1
s_cbranch_scc1 Lg1_st1_%=
s_branch Lg1_st0_%=
Lg1_gox_%=:
s_add_u32 m0,s59,2048
s_nop 0
buffer_load_dword v8,s[36:39],s58 offen offset:0 sc1 lds
s_waitcnt vmcnt(0)
s_barrier
buffer_store_dword v4,v3,s[36:39],s70 offen offset:0 sc0 sc1
s_min_u32 s71,s71,s72
buffer_store_dword v4,v3,s[36:39],s71 offen offset:0 sc0 sc1
s_mov_b32 s70,0x7fff0000
s_mov_b32 s71,0x7fff0000
s_mov_b32 s72,0x7fff0000
s_cmp_eq_u32 s80,1
s_cbranch_scc1 Lg1_st1_%=
Lg1_st0_%=:
ds_read_b32 v7,v6 offset:2048
R 34,1,0
R 38,1,1024
R 42,1,2048
R 46,1,3072
R 50,1,4096
L 5
v_cmp_eq_u32 vcc,s53,v7
L 3
s_cmp_eq_u32 vcc_lo,-1
s_cbranch_scc1 Lg1_F0_%=
s_branch Lg1_L0_%=
Lg1_st1_%=:
ds_read_b32 v7,v6 offset:2048
R 34,2,0
R 38,2,1024
R 42,2,2048
R 46,2,3072
R 50,2,4096
L 5
v_cmp_eq_u32 vcc,s53,v7
L 3
s_cmp_eq_u32 vcc_lo,-1
s_cbranch_scc1 Lg1_F1_%=
s_branch Lg1_L1_%=
Lg1_F0_%=:
v_mfma_f32_16x16x32_bf16 v[22:25],a[4:7],v[34:37],0
s_add_u32 s58,s58,s61
s_add_u32 m0,s59,0
R 54,1,5120
M 22,a[12:15],38
buffer_load_dword v8,s[36:39],s58 offen offset:0 sc1 lds
v_mfma_f32_16x16x32_bf16 v[18:21],a[0:3],v[34:37],0
s_add_u32 m0,s57,0
R 58,1,6144
v_fma_f32 v26,-v102,v106,v26
M 18,a[8:11],38
L 3
s_add_u32 s56,s55,0
v_fma_f32 v27,-v102,v107,v27
M 18,a[16:19],42
s_mov_b32 s66,s65
R 62,1,7168
v_fma_f32 v28,-v102,v108,v28
M 18,a[24:27],46
D 0,56,0
M 22,a[20:23],42
s_add_u32 s65,s65,s62
R 66,1,8192
v_fma_f32 v29,-v102,v109,v29
v_mul_f32 v114,v26,v26
M 22,a[28:31],46
L 3
D 0,56,1024
M 22,a[36:39],50
s_add_u32 s54,s54,s46
R 70,1,9216
v_mul_f32 v115,v27,v27
M 22,a[44:47],54
v_mul_f32 v116,v28,v28
v_mul_f32 v117,v29,v29
M 18,a[32:35],50
R 74,1,10240
v_fma_f32 v114,v104,v114,v103
M 18,a[40:43],54
L 3
D 0,56,2048
M 18,a[48:51],58
R 78,1,11264
v_fma_f32 v115,v104,v115,v103
M 18,a[56:59],62
D 0,56,3072
v_fma_f32 v116,v104,v116,v103
M 22,a[52:55],58
s_add_u32 m0,s57,16384
R 82,1,12288
v_fma_f32 v117,v104,v117,v103
M 22,a[60:63],62
L 3
v_mul_f32 v114,v26,v114
v_mul_f32 v115,v27,v115
M 22,a[68:71],66
s_add_u32 s56,s55,16384
R 86,1,13312
v_mul_f32 v116,v28,v116
M 22,a[76:79],70
buffer_load_dwordx4 v[98:101],v11,s[36:39],s73 offen offset:0 sc1
M 18,a[64:67],66
s_add_u32 s73,s73,s64
R 90,1,14336
v_mul_f32 v117,v29,v117
M 18,a[72:75],70
L 3
v_exp_f32 v114,v114
v_fma_f32 v30,-v102,v110,v30
M 18,a[80:83],74
s_waitcnt vmcnt(9)
R 94,1,15360
v_fma_f32 v114,v114,v105,v105
M 18,a[88:91],78
s_barrier
v_exp_f32 v115,v115
M 22,a[84:87],74
s_add_u32 s55,s55,s60
R 34,1,16384
v_fma_f32 v115,v115,v105,v105
M 22,a[92:95],78
L 3
v_exp_f32 v116,v116
M 22,a[100:103],82
s_sub_u32 s57,s84,s57
R 38,1,17408
v_fma_f32 v116,v116,v105,v105
M 22,a[108:111],86
D 0,56,0
M 18,a[96:99],82
R 42,1,18432
v_exp_f32 v117,v117
M 18,a[104:107],86
L 3
v_rcp_f32 v114,v114
M 18,a[112:115],90
R 46,1,19456
v_fma_f32 v117,v117,v105,v105
M 18,a[120:123],94
v_rcp_f32 v115,v115
M 22,a[116:119],90
R 50,1,20480
v_mul_f32 v26,v26,v114
M 22,a[124:127],94
L 3
v_rcp_f32 v116,v116
M 22,a[132:135],34
R 54,1,21504
v_mul_f32 v27,v27,v115
M 22,a[140:143],38
D 0,56,1024
v_mul_f32 v28,v28,v116
M 18,a[128:131],34
R 58,1,22528
v_cvt_pk_bf16_f32 v122,v26,v27
M 18,a[136:139],38
L 3
v_rcp_f32 v117,v117
M 18,a[144:147],42
R 62,1,23552
v_mul_f32 v29,v29,v117
M 18,a[152:155],46
v_cvt_pk_bf16_f32 v123,v28,v29
v_fma_f32 v31,-v102,v111,v31
M 22,a[148:151],42
R 66,1,24576
v_fma_f32 v32,-v102,v112,v32
M 22,a[156:159],46
L 3
D 0,56,2048
v_fma_f32 v33,-v102,v113,v33
M 22,a[164:167],50
R 70,1,25600
v_mul_f32 v118,v30,v30
M 22,a[172:175],54
v_mul_f32 v119,v31,v31
v_mul_f32 v120,v32,v32
M 18,a[160:163],50
R 74,1,26624
v_mul_f32 v121,v33,v33
M 18,a[168:171],54
L 3
v_fma_f32 v118,v104,v118,v103
v_fma_f32 v119,v104,v119,v103
M 18,a[176:179],58
R 78,1,27648
v_fma_f32 v120,v104,v120,v103
M 18,a[184:187],62
v_fma_f32 v121,v104,v121,v103
v_mul_f32 v118,v30,v118
M 22,a[180:183],58
R 82,1,28672
v_exp_f32 v118,v118
M 22,a[188:191],62
L 3
D 0,56,3072
M 22,a[196:199],66
R 86,1,29696
v_mul_f32 v119,v31,v119
M 22,a[204:207],70
v_exp_f32 v119,v119
M 18,a[192:195],66
R 90,1,30720
v_mul_f32 v120,v32,v120
M 18,a[200:203],70
L 3
v_mul_f32 v121,v33,v121
v_fma_f32 v118,v118,v105,v105
M 18,a[208:211],74
s_waitcnt vmcnt(5)
R 94,1,31744
v_fma_f32 v119,v119,v105,v105
M 18,a[216:219],78
s_barrier
ds_read_b32 v7,v6 offset:0
v_exp_f32 v120,v120
M 22,a[212:215],74
R 34,2,0
v_fma_f32 v120,v120,v105,v105
M 22,a[220:223],78
L 4
buffer_store_dword v4,v3,s[36:39],s70 offen offset:0 sc0 sc1
v_exp_f32 v121,v121
M 22,a[228:231],82
s_mov_b32 s70,s69
R 38,2,1024
v_fma_f32 v121,v121,v105,v105
v_rcp_f32 v118,v118
v_rcp_f32 v119,v119
v_rcp_f32 v120,v120
v_rcp_f32 v121,v121
M 22,a[236:239],86
s_mov_b32 s69,s68
v_mul_f32 v30,v30,v118
v_mul_f32 v31,v31,v119
v_mul_f32 v32,v32,v120
v_mul_f32 v33,v33,v121
v_cvt_pk_bf16_f32 v124,v30,v31
v_cvt_pk_bf16_f32 v125,v32,v33
M 18,a[224:227],82
s_add_u32 s68,s68,s63
R 42,2,2048
buffer_store_dwordx4 v[122:125],v15,s[36:39],s67 offen offset:0 sc0 sc1
M 18,a[232:235],86
L 4
L 3
M 18,a[240:243],90
R 46,2,3072
v_cmp_eq_u32 vcc,s53,v7
M 18,a[248:251],94
buffer_store_dwordx4 v[122:125],v16,s[36:39],s67 offen offset:0 sc0 sc1
M 22,a[244:247],90
s_waitcnt vmcnt(7) lgkmcnt(2)
s_cmp_eq_u32 vcc_lo,-1
R 50,2,4096
M 22,a[252:255],94
s_cbranch_scc0 Lg1_K1_%=
Lg1_F1_%=:
v_mfma_f32_16x16x32_bf16 v[30:33],a[4:7],v[34:37],0
s_add_u32 s58,s58,s61
s_add_u32 m0,s59,1024
R 54,2,5120
M 30,a[12:15],38
buffer_load_dword v8,s[36:39],s58 offen offset:0 sc1 lds
v_mfma_f32_16x16x32_bf16 v[26:29],a[0:3],v[34:37],0
s_add_u32 m0,s57,0
R 58,2,6144
v_fma_f32 v18,-v98,v106,v18
M 26,a[8:11],38
L 3
s_add_u32 s56,s55,0
v_fma_f32 v19,-v98,v107,v19
M 26,a[16:19],42
s_mov_b32 s67,s65
R 62,2,7168
v_fma_f32 v20,-v98,v108,v20
M 26,a[24:27],46
D 0,56,0
M 30,a[20:23],42
s_add_u32 s65,s65,s62
R 66,2,8192
v_fma_f32 v21,-v98,v109,v21
v_mul_f32 v114,v18,v18
M 30,a[28:31],46
L 3
D 0,56,1024
M 30,a[36:39],50
s_add_u32 s54,s54,s46
R 70,2,9216
v_mul_f32 v115,v19,v19
M 30,a[44:47],54
v_mul_f32 v116,v20,v20
v_mul_f32 v117,v21,v21
M 26,a[32:35],50
R 74,2,10240
v_fma_f32 v114,v100,v114,v99
M 26,a[40:43],54
L 3
D 0,56,2048
M 26,a[48:51],58
R 78,2,11264
v_fma_f32 v115,v100,v115,v99
M 26,a[56:59],62
D 0,56,3072
v_fma_f32 v116,v100,v116,v99
M 30,a[52:55],58
s_add_u32 m0,s57,16384
R 82,2,12288
v_fma_f32 v117,v100,v117,v99
M 30,a[60:63],62
L 3
v_mul_f32 v114,v18,v114
v_mul_f32 v115,v19,v115
M 30,a[68:71],66
s_add_u32 s56,s55,16384
R 86,2,13312
v_mul_f32 v116,v20,v116
M 30,a[76:79],70
buffer_load_dwordx4 v[102:105],v11,s[36:39],s73 offen offset:0 sc1
M 26,a[64:67],66
s_add_u32 s73,s73,s64
R 90,2,14336
v_mul_f32 v117,v21,v117
M 26,a[72:75],70
L 3
v_exp_f32 v114,v114
v_fma_f32 v22,-v98,v110,v22
M 26,a[80:83],74
s_waitcnt vmcnt(9)
R 94,2,15360
v_fma_f32 v114,v114,v101,v101
M 26,a[88:91],78
s_barrier
v_exp_f32 v115,v115
M 30,a[84:87],74
s_add_u32 s55,s55,s60
R 34,2,16384
v_fma_f32 v115,v115,v101,v101
M 30,a[92:95],78
L 3
v_exp_f32 v116,v116
M 30,a[100:103],82
s_sub_u32 s57,s84,s57
R 38,2,17408
v_fma_f32 v116,v116,v101,v101
M 30,a[108:111],86
D 0,56,0
M 26,a[96:99],82
R 42,2,18432
v_exp_f32 v117,v117
M 26,a[104:107],86
L 3
v_rcp_f32 v114,v114
M 26,a[112:115],90
R 46,2,19456
v_fma_f32 v117,v117,v101,v101
M 26,a[120:123],94
v_rcp_f32 v115,v115
M 30,a[116:119],90
R 50,2,20480
v_mul_f32 v18,v18,v114
M 30,a[124:127],94
L 3
v_rcp_f32 v116,v116
M 30,a[132:135],34
R 54,2,21504
v_mul_f32 v19,v19,v115
M 30,a[140:143],38
D 0,56,1024
v_mul_f32 v20,v20,v116
M 26,a[128:131],34
R 58,2,22528
v_cvt_pk_bf16_f32 v122,v18,v19
M 26,a[136:139],38
L 3
v_rcp_f32 v117,v117
M 26,a[144:147],42
R 62,2,23552
v_mul_f32 v21,v21,v117
M 26,a[152:155],46
v_cvt_pk_bf16_f32 v123,v20,v21
v_fma_f32 v23,-v98,v111,v23
M 30,a[148:151],42
R 66,2,24576
v_fma_f32 v24,-v98,v112,v24
M 30,a[156:159],46
L 3
D 0,56,2048
v_fma_f32 v25,-v98,v113,v25
M 30,a[164:167],50
R 70,2,25600
v_mul_f32 v118,v22,v22
M 30,a[172:175],54
v_mul_f32 v119,v23,v23
v_mul_f32 v120,v24,v24
M 26,a[160:163],50
R 74,2,26624
v_mul_f32 v121,v25,v25
M 26,a[168:171],54
L 3
v_fma_f32 v118,v100,v118,v99
v_fma_f32 v119,v100,v119,v99
M 26,a[176:179],58
R 78,2,27648
v_fma_f32 v120,v100,v120,v99
M 26,a[184:187],62
v_fma_f32 v121,v100,v121,v99
v_mul_f32 v118,v22,v118
M 30,a[180:183],58
R 82,2,28672
v_exp_f32 v118,v118
M 30,a[188:191],62
L 3
D 0,56,3072
M 30,a[196:199],66
R 86,2,29696
v_mul_f32 v119,v23,v119
M 30,a[204:207],70
v_exp_f32 v119,v119
M 26,a[192:195],66
R 90,2,30720
v_mul_f32 v120,v24,v120
M 26,a[200:203],70
L 3
v_mul_f32 v121,v25,v121
v_fma_f32 v118,v118,v101,v101
M 26,a[208:211],74
s_waitcnt vmcnt(5)
)"
R"(R 94,2,31744
v_fma_f32 v119,v119,v101,v101
M 26,a[216:219],78
s_barrier
ds_read_b32 v7,v6 offset:1024
v_exp_f32 v120,v120
M 30,a[212:215],74
R 34,1,0
v_fma_f32 v120,v120,v101,v101
M 30,a[220:223],78
L 4
buffer_store_dword v4,v3,s[36:39],s70 offen offset:0 sc0 sc1
v_exp_f32 v121,v121
M 30,a[228:231],82
s_mov_b32 s70,s69
R 38,1,1024
v_fma_f32 v121,v121,v101,v101
v_rcp_f32 v118,v118
v_rcp_f32 v119,v119
v_rcp_f32 v120,v120
v_rcp_f32 v121,v121
M 30,a[236:239],86
s_mov_b32 s69,s68
v_mul_f32 v22,v22,v118
v_mul_f32 v23,v23,v119
v_mul_f32 v24,v24,v120
v_mul_f32 v25,v25,v121
v_cvt_pk_bf16_f32 v124,v22,v23
v_cvt_pk_bf16_f32 v125,v24,v25
M 26,a[224:227],82
s_add_u32 s68,s68,s63
R 42,1,2048
buffer_store_dwordx4 v[122:125],v15,s[36:39],s66 offen offset:0 sc0 sc1
M 26,a[232:235],86
L 4
L 3
M 26,a[240:243],90
R 46,1,3072
v_cmp_eq_u32 vcc,s53,v7
M 26,a[248:251],94
buffer_store_dwordx4 v[122:125],v16,s[36:39],s66 offen offset:0 sc0 sc1
M 30,a[244:247],90
s_waitcnt vmcnt(7) lgkmcnt(2)
s_cmp_eq_u32 vcc_lo,-1
R 50,1,4096
M 30,a[252:255],94
s_cbranch_scc1 Lg1_F0_%=
Lg1_K0_%=:
Lg1_L0_%=:
v_mfma_f32_16x16x32_bf16 v[22:25],a[4:7],v[34:37],0
s_add_u32 m0,s59,3072
s_mov_b32 s66,s65
R 54,1,5120
M 22,a[12:15],38
buffer_load_dword v8,s[36:39],s58 offen offset:0 sc1 lds
v_mfma_f32_16x16x32_bf16 v[18:21],a[0:3],v[34:37],0
s_add_u32 s65,s65,s62
R 58,1,6144
v_fma_f32 v26,-v102,v106,v26
M 18,a[8:11],38
L 3
v_fma_f32 v27,-v102,v107,v27
v_fma_f32 v28,-v102,v108,v28
M 18,a[16:19],42
R 62,1,7168
v_fma_f32 v29,-v102,v109,v29
M 18,a[24:27],46
v_mul_f32 v114,v26,v26
v_mul_f32 v115,v27,v27
M 22,a[20:23],42
R 66,1,8192
v_mul_f32 v116,v28,v28
M 22,a[28:31],46
L 3
v_mul_f32 v117,v29,v29
v_fma_f32 v114,v104,v114,v103
M 22,a[36:39],50
R 70,1,9216
v_fma_f32 v115,v104,v115,v103
M 22,a[44:47],54
v_fma_f32 v116,v104,v116,v103
v_fma_f32 v117,v104,v117,v103
M 18,a[32:35],50
R 74,1,10240
v_mul_f32 v114,v26,v114
M 18,a[40:43],54
L 3
v_exp_f32 v114,v114
M 18,a[48:51],58
R 78,1,11264
v_mul_f32 v115,v27,v115
M 18,a[56:59],62
v_exp_f32 v115,v115
M 22,a[52:55],58
R 82,1,12288
v_mul_f32 v116,v28,v116
M 22,a[60:63],62
L 3
v_exp_f32 v116,v116
M 22,a[68:71],66
R 86,1,13312
v_mul_f32 v117,v29,v117
M 22,a[76:79],70
buffer_load_dwordx4 v[98:101],v11,s[36:39],s73 offen offset:0 sc1
M 18,a[64:67],66
s_add_u32 s73,s73,s64
R 90,1,14336
v_fma_f32 v114,v114,v105,v105
M 18,a[72:75],70
L 3
v_exp_f32 v117,v117
M 18,a[80:83],74
s_waitcnt vmcnt(1)
R 94,1,15360
v_fma_f32 v115,v115,v105,v105
M 18,a[88:91],78
s_barrier
ds_read_b32 v7,v6 offset:3072
v_fma_f32 v116,v116,v105,v105
M 22,a[84:87],74
R 34,1,16384
v_fma_f32 v117,v117,v105,v105
M 22,a[92:95],78
L 4
buffer_store_dword v4,v3,s[36:39],s70 offen offset:0 sc0 sc1
M 22,a[100:103],82
s_mov_b32 s70,0x7fff0000
R 38,1,17408
v_fma_f32 v30,-v102,v110,v30
M 22,a[108:111],86
v_rcp_f32 v114,v114
M 18,a[96:99],82
R 42,1,18432
v_mul_f32 v26,v26,v114
M 18,a[104:107],86
L 4
L 3
v_cmp_eq_u32 vcc,s53,v7
M 18,a[112:115],90
R 46,1,19456
v_fma_f32 v31,-v102,v111,v31
M 18,a[120:123],94
v_rcp_f32 v115,v115
M 22,a[116:119],90
s_cmp_eq_u32 vcc_lo,-1
R 50,1,20480
v_mul_f32 v27,v27,v115
s_cbranch_scc0 Lg1_X0_%=
M 22,a[124:127],94
L 3
s_add_u32 m0,s57,0
s_add_u32 s56,s55,0
M 22,a[132:135],34
s_add_u32 s58,s58,s61
R 54,1,21504
v_cvt_pk_bf16_f32 v122,v26,v27
M 22,a[140:143],38
D 0,56,0
M 18,a[128:131],34
s_add_u32 s54,s54,s46
R 58,1,22528
v_fma_f32 v32,-v102,v112,v32
M 18,a[136:139],38
L 3
v_rcp_f32 v116,v116
M 18,a[144:147],42
R 62,1,23552
v_mul_f32 v28,v28,v116
M 18,a[152:155],46
D 0,56,1024
M 22,a[148:151],42
R 66,1,24576
v_fma_f32 v33,-v102,v113,v33
M 22,a[156:159],46
L 3
v_rcp_f32 v117,v117
M 22,a[164:167],50
R 70,1,25600
v_mul_f32 v29,v29,v117
M 22,a[172:175],54
D 0,56,2048
M 18,a[160:163],50
R 74,1,26624
v_cvt_pk_bf16_f32 v123,v28,v29
M 18,a[168:171],54
L 3
v_mul_f32 v118,v30,v30
v_mul_f32 v119,v31,v31
M 18,a[176:179],58
R 78,1,27648
v_mul_f32 v120,v32,v32
M 18,a[184:187],62
D 0,56,3072
M 22,a[180:183],58
s_add_u32 m0,s59,0
R 82,1,28672
v_mul_f32 v121,v33,v33
M 22,a[188:191],62
L 3
buffer_load_dword v8,s[36:39],s58 offen offset:0 sc1 lds
M 22,a[196:199],66
s_add_u32 m0,s57,16384
R 86,1,29696
v_fma_f32 v118,v104,v118,v103
M 22,a[204:207],70
s_add_u32 s56,s55,16384
v_fma_f32 v119,v104,v119,v103
v_fma_f32 v120,v104,v120,v103
M 18,a[192:195],66
s_add_u32 s55,s55,s60
R 90,1,30720
v_fma_f32 v121,v104,v121,v103
M 18,a[200:203],70
L 3
D 0,56,0
M 18,a[208:211],74
s_waitcnt vmcnt(1)
R 94,1,31744
v_mul_f32 v118,v30,v118
M 18,a[216:219],78
s_barrier
ds_read_b32 v7,v6 offset:0
v_mul_f32 v119,v31,v119
M 22,a[212:215],74
s_sub_u32 s57,s84,s57
R 34,2,0
v_mul_f32 v120,v32,v120
M 22,a[220:223],78
L 4
buffer_store_dword v4,v3,s[36:39],s70 offen offset:0 sc0 sc1
v_mul_f32 v121,v33,v121
v_exp_f32 v118,v118
v_exp_f32 v119,v119
v_exp_f32 v120,v120
v_exp_f32 v121,v121
M 22,a[228:231],82
s_mov_b32 s70,s69
R 38,2,1024
v_fma_f32 v118,v118,v105,v105
v_fma_f32 v119,v119,v105,v105
v_fma_f32 v120,v120,v105,v105
v_fma_f32 v121,v121,v105,v105
v_rcp_f32 v118,v118
v_rcp_f32 v119,v119
v_rcp_f32 v120,v120
v_rcp_f32 v121,v121
M 22,a[236:239],86
s_mov_b32 s69,s68
v_mul_f32 v30,v30,v118
v_mul_f32 v31,v31,v119
v_mul_f32 v32,v32,v120
v_mul_f32 v33,v33,v121
v_cvt_pk_bf16_f32 v124,v30,v31
v_cvt_pk_bf16_f32 v125,v32,v33
M 18,a[224:227],82
s_add_u32 s68,s68,s63
R 42,2,2048
buffer_store_dwordx4 v[122:125],v15,s[36:39],s67 offen offset:0 sc0 sc1
M 18,a[232:235],86
L 4
L 3
M 18,a[240:243],90
R 46,2,3072
v_cmp_eq_u32 vcc,s53,v7
M 18,a[248:251],94
D 0,56,1024
M 22,a[244:247],90
L 2
s_cmp_eq_u32 vcc_lo,-1
R 50,2,4096
buffer_store_dwordx4 v[122:125],v16,s[36:39],s67 offen offset:0 sc0 sc1
M 22,a[252:255],94
D 0,56,2048
D 0,56,3072
buffer_store_dword v4,v3,s[36:39],s71 offen offset:0 sc0 sc1
buffer_store_dword v4,v3,s[36:39],s71 offen offset:0 sc0 sc1
buffer_store_dword v4,v3,s[36:39],s71 offen offset:0 sc0 sc1
s_cbranch_scc1 Lg1_F1_%=
s_branch Lg1_K1_%=
Lg1_X0_%=:
M 22,a[124:127],94
L 3
s_add_u32 m0,s59,0
v_cvt_pk_bf16_f32 v122,v26,v27
M 22,a[132:135],34
R 54,1,21504
v_fma_f32 v32,-v102,v112,v32
M 22,a[140:143],38
buffer_load_dword v8,s[36:39],s58 offen offset:0 sc1 lds
M 18,a[128:131],34
R 58,1,22528
v_fma_f32 v33,-v102,v113,v33
M 18,a[136:139],38
L 3
v_rcp_f32 v116,v116
M 18,a[144:147],42
R 62,1,23552
v_mul_f32 v28,v28,v116
M 18,a[152:155],46
v_rcp_f32 v117,v117
M 22,a[148:151],42
R 66,1,24576
v_mul_f32 v29,v29,v117
M 22,a[156:159],46
L 3
v_cvt_pk_bf16_f32 v123,v28,v29
v_mul_f32 v118,v30,v30
M 22,a[164:167],50
R 70,1,25600
v_mul_f32 v119,v31,v31
M 22,a[172:175],54
v_mul_f32 v120,v32,v32
v_mul_f32 v121,v33,v33
M 18,a[160:163],50
R 74,1,26624
v_fma_f32 v118,v104,v118,v103
M 18,a[168:171],54
L 3
v_fma_f32 v119,v104,v119,v103
v_fma_f32 v120,v104,v120,v103
M 18,a[176:179],58
R 78,1,27648
v_fma_f32 v121,v104,v121,v103
M 18,a[184:187],62
v_mul_f32 v118,v30,v118
v_mul_f32 v119,v31,v119
M 22,a[180:183],58
R 82,1,28672
v_mul_f32 v120,v32,v120
M 22,a[188:191],62
L 3
v_exp_f32 v118,v118
M 22,a[196:199],66
R 86,1,29696
v_mul_f32 v121,v33,v121
M 22,a[204:207],70
v_exp_f32 v119,v119
M 18,a[192:195],66
R 90,1,30720
v_fma_f32 v118,v118,v105,v105
M 18,a[200:203],70
L 3
v_exp_f32 v120,v120
M 18,a[208:211],74
s_waitcnt vmcnt(0)
R 94,1,31744
v_fma_f32 v119,v119,v105,v105
M 18,a[216:219],78
s_barrier
buffer_store_dword v4,v3,s[36:39],s70 offen offset:0 sc0 sc1
M 22,a[212:215],74
s_mov_b32 s70,s69
ds_read_b32 v7,v6 offset:0
v_fma_f32 v120,v120,v105,v105
M 22,a[220:223],78
L 3
v_exp_f32 v121,v121
M 22,a[228:231],82
s_mov_b32 s69,s68
v_fma_f32 v121,v121,v105,v105
v_rcp_f32 v118,v118
v_rcp_f32 v119,v119
v_rcp_f32 v120,v120
v_rcp_f32 v121,v121
M 22,a[236:239],86
s_add_u32 s68,s68,s63
v_mul_f32 v30,v30,v118
v_mul_f32 v31,v31,v119
v_mul_f32 v32,v32,v120
v_mul_f32 v33,v33,v121
v_cvt_pk_bf16_f32 v124,v30,v31
v_cvt_pk_bf16_f32 v125,v32,v33
M 18,a[224:227],82
buffer_store_dwordx4 v[122:125],v15,s[36:39],s67 offen offset:0 sc0 sc1
M 18,a[232:235],86
L 1
M 18,a[240:243],90
L 0
v_cmp_eq_u32 vcc,s53,v7
M 18,a[248:251],94
buffer_store_dwordx4 v[122:125],v16,s[36:39],s67 offen offset:0 sc0 sc1
M 22,a[244:247],90
s_nop 0
M 22,a[252:255],94
s_mov_b32 s80,1
s_cmp_eq_u32 vcc_lo,-1
s_cbranch_scc0 Lg1_xn0_%=
s_add_u32 m0,s57,0
s_add_u32 s56,s55,0
D 0,56,0
v_fma_f32 v18,-v98,v106,v18
v_fma_f32 v19,-v98,v107,v19
v_fma_f32 v20,-v98,v108,v20
v_fma_f32 v21,-v98,v109,v21
v_mul_f32 v114,v18,v18
v_mul_f32 v115,v19,v19
v_mul_f32 v116,v20,v20
D 0,56,1024
v_mul_f32 v117,v21,v21
v_fma_f32 v114,v100,v114,v99
v_fma_f32 v115,v100,v115,v99
v_fma_f32 v116,v100,v116,v99
v_fma_f32 v117,v100,v117,v99
v_mul_f32 v114,v18,v114
v_mul_f32 v115,v19,v115
D 0,56,2048
v_mul_f32 v116,v20,v116
v_mul_f32 v117,v21,v117
v_exp_f32 v114,v114
v_exp_f32 v115,v115
v_exp_f32 v116,v116
v_exp_f32 v117,v117
v_fma_f32 v114,v114,v101,v101
D 0,56,3072
v_fma_f32 v115,v115,v101,v101
v_fma_f32 v116,v116,v101,v101
v_fma_f32 v117,v117,v101,v101
v_rcp_f32 v114,v114
v_rcp_f32 v115,v115
v_rcp_f32 v116,v116
v_rcp_f32 v117,v117
s_add_u32 s58,s58,s61
s_add_u32 m0,s57,16384
s_add_u32 s56,s55,16384
D 0,56,0
v_mul_f32 v18,v18,v114
v_mul_f32 v19,v19,v115
v_mul_f32 v20,v20,v116
v_mul_f32 v21,v21,v117
v_cvt_pk_bf16_f32 v122,v18,v19
v_cvt_pk_bf16_f32 v123,v20,v21
v_fma_f32 v22,-v98,v110,v22
D 0,56,1024
v_fma_f32 v23,-v98,v111,v23
v_fma_f32 v24,-v98,v112,v24
v_fma_f32 v25,-v98,v113,v25
v_mul_f32 v118,v22,v22
v_mul_f32 v119,v23,v23
v_mul_f32 v120,v24,v24
v_mul_f32 v121,v25,v25
D 0,56,2048
v_fma_f32 v118,v100,v118,v99
v_fma_f32 v119,v100,v119,v99
v_fma_f32 v120,v100,v120,v99
v_fma_f32 v121,v100,v121,v99
v_mul_f32 v118,v22,v118
v_mul_f32 v119,v23,v119
v_mul_f32 v120,v24,v120
D 0,56,3072
v_mul_f32 v121,v25,v121
v_exp_f32 v118,v118
v_exp_f32 v119,v119
v_exp_f32 v120,v120
v_exp_f32 v121,v121
v_fma_f32 v118,v118,v101,v101
v_fma_f32 v119,v119,v101,v101
s_add_u32 s55,s55,s60
s_sub_u32 s57,s84,s57
s_add_u32 s54,s54,s46
v_fma_f32 v120,v120,v101,v101
v_fma_f32 v121,v121,v101,v101
v_rcp_f32 v118,v118
v_rcp_f32 v119,v119
v_rcp_f32 v120,v120
v_rcp_f32 v121,v121
v_mul_f32 v22,v22,v118
v_mul_f32 v23,v23,v119
v_mul_f32 v24,v24,v120
v_mul_f32 v25,v25,v121
v_cvt_pk_bf16_f32 v124,v22,v23
v_cvt_pk_bf16_f32 v125,v24,v25
buffer_store_dwordx4 v[122:125],v15,s[36:39],s66 offen offset:0 sc0 sc1
buffer_store_dwordx4 v[122:125],v16,s[36:39],s66 offen offset:0 sc0 sc1
s_mov_b32 s72,s69
s_mov_b32 s69,0x7fff0000
s_mov_b32 s66,0x7fff0000
s_mov_b32 s76,0x7fff0000
s_branch Lg1_gox_%=
Lg1_xn0_%=:
s_cmp_ge_u32 s54,256
s_cbranch_scc1 Lg1_fin0_%=
s_add_u32 m0,s59,4096
s_nop 0
buffer_load_dword v8,s[36:39],s58 offen offset:0 sc1 lds
buffer_store_dword v4,v3,s[36:39],s71 offen offset:0 sc0 sc1
s_add_u32 m0,s59,5120
s_nop 0
buffer_load_dword v8,s[36:39],s58 offen offset:0 sc1 lds
buffer_store_dword v4,v3,s[36:39],s71 offen offset:0 sc0 sc1
s_mov_b32 s83,0
v_fma_f32 v18,-v98,v106,v18
v_fma_f32 v19,-v98,v107,v19
v_fma_f32 v20,-v98,v108,v20
v_fma_f32 v21,-v98,v109,v21
v_mul_f32 v114,v18,v18
v_mul_f32 v115,v19,v19
v_mul_f32 v116,v20,v20
v_mul_f32 v117,v21,v21
v_fma_f32 v114,v100,v114,v99
v_fma_f32 v115,v100,v115,v99
)"
R"(v_fma_f32 v116,v100,v116,v99
v_fma_f32 v117,v100,v117,v99
v_mul_f32 v114,v18,v114
v_mul_f32 v115,v19,v115
v_mul_f32 v116,v20,v116
v_mul_f32 v117,v21,v117
v_exp_f32 v114,v114
v_exp_f32 v115,v115
v_exp_f32 v116,v116
v_exp_f32 v117,v117
v_fma_f32 v114,v114,v101,v101
v_fma_f32 v115,v115,v101,v101
v_fma_f32 v116,v116,v101,v101
v_fma_f32 v117,v117,v101,v101
v_rcp_f32 v114,v114
v_rcp_f32 v115,v115
v_rcp_f32 v116,v116
v_rcp_f32 v117,v117
v_mul_f32 v18,v18,v114
v_mul_f32 v19,v19,v115
v_mul_f32 v20,v20,v116
v_mul_f32 v21,v21,v117
v_cvt_pk_bf16_f32 v122,v18,v19
v_cvt_pk_bf16_f32 v123,v20,v21
v_fma_f32 v22,-v98,v110,v22
v_fma_f32 v23,-v98,v111,v23
v_fma_f32 v24,-v98,v112,v24
v_fma_f32 v25,-v98,v113,v25
v_mul_f32 v118,v22,v22
v_mul_f32 v119,v23,v23
v_mul_f32 v120,v24,v24
v_mul_f32 v121,v25,v25
v_fma_f32 v118,v100,v118,v99
v_fma_f32 v119,v100,v119,v99
v_fma_f32 v120,v100,v120,v99
v_fma_f32 v121,v100,v121,v99
v_mul_f32 v118,v22,v118
v_mul_f32 v119,v23,v119
v_mul_f32 v120,v24,v120
v_mul_f32 v121,v25,v121
v_exp_f32 v118,v118
v_exp_f32 v119,v119
v_exp_f32 v120,v120
v_exp_f32 v121,v121
v_fma_f32 v118,v118,v101,v101
v_fma_f32 v119,v119,v101,v101
v_fma_f32 v120,v120,v101,v101
v_fma_f32 v121,v121,v101,v101
v_rcp_f32 v118,v118
v_rcp_f32 v119,v119
v_rcp_f32 v120,v120
v_rcp_f32 v121,v121
v_mul_f32 v22,v22,v118
v_mul_f32 v23,v23,v119
v_mul_f32 v24,v24,v120
v_mul_f32 v25,v25,v121
v_cvt_pk_bf16_f32 v124,v22,v23
v_cvt_pk_bf16_f32 v125,v24,v25
buffer_store_dwordx4 v[122:125],v15,s[36:39],s66 offen offset:0 sc0 sc1
buffer_store_dwordx4 v[122:125],v16,s[36:39],s66 offen offset:0 sc0 sc1
s_mov_b32 s72,s69
s_mov_b32 s69,0x7fff0000
s_mov_b32 s66,0x7fff0000
s_mov_b32 s76,0x7fff0000
s_branch Lg1_poll_%=
Lg1_fin0_%=:
v_fma_f32 v18,-v98,v106,v18
v_fma_f32 v19,-v98,v107,v19
v_fma_f32 v20,-v98,v108,v20
v_fma_f32 v21,-v98,v109,v21
v_mul_f32 v114,v18,v18
v_mul_f32 v115,v19,v19
v_mul_f32 v116,v20,v20
v_mul_f32 v117,v21,v21
v_fma_f32 v114,v100,v114,v99
v_fma_f32 v115,v100,v115,v99
v_fma_f32 v116,v100,v116,v99
v_fma_f32 v117,v100,v117,v99
v_mul_f32 v114,v18,v114
v_mul_f32 v115,v19,v115
v_mul_f32 v116,v20,v116
v_mul_f32 v117,v21,v117
v_exp_f32 v114,v114
v_exp_f32 v115,v115
v_exp_f32 v116,v116
v_exp_f32 v117,v117
v_fma_f32 v114,v114,v101,v101
v_fma_f32 v115,v115,v101,v101
v_fma_f32 v116,v116,v101,v101
v_fma_f32 v117,v117,v101,v101
v_rcp_f32 v114,v114
v_rcp_f32 v115,v115
v_rcp_f32 v116,v116
v_rcp_f32 v117,v117
v_mul_f32 v18,v18,v114
v_mul_f32 v19,v19,v115
v_mul_f32 v20,v20,v116
v_mul_f32 v21,v21,v117
v_cvt_pk_bf16_f32 v122,v18,v19
v_cvt_pk_bf16_f32 v123,v20,v21
v_fma_f32 v22,-v98,v110,v22
v_fma_f32 v23,-v98,v111,v23
v_fma_f32 v24,-v98,v112,v24
v_fma_f32 v25,-v98,v113,v25
v_mul_f32 v118,v22,v22
v_mul_f32 v119,v23,v23
v_mul_f32 v120,v24,v24
v_mul_f32 v121,v25,v25
v_fma_f32 v118,v100,v118,v99
v_fma_f32 v119,v100,v119,v99
v_fma_f32 v120,v100,v120,v99
v_fma_f32 v121,v100,v121,v99
v_mul_f32 v118,v22,v118
v_mul_f32 v119,v23,v119
v_mul_f32 v120,v24,v120
v_mul_f32 v121,v25,v121
v_exp_f32 v118,v118
v_exp_f32 v119,v119
v_exp_f32 v120,v120
v_exp_f32 v121,v121
v_fma_f32 v118,v118,v101,v101
v_fma_f32 v119,v119,v101,v101
v_fma_f32 v120,v120,v101,v101
v_fma_f32 v121,v121,v101,v101
v_rcp_f32 v118,v118
v_rcp_f32 v119,v119
v_rcp_f32 v120,v120
v_rcp_f32 v121,v121
v_mul_f32 v22,v22,v118
v_mul_f32 v23,v23,v119
v_mul_f32 v24,v24,v120
v_mul_f32 v25,v25,v121
v_cvt_pk_bf16_f32 v124,v22,v23
v_cvt_pk_bf16_f32 v125,v24,v25
buffer_store_dwordx4 v[122:125],v15,s[36:39],s66 offen offset:0 sc0 sc1
buffer_store_dwordx4 v[122:125],v16,s[36:39],s66 offen offset:0 sc0 sc1
s_mov_b32 s72,s69
s_mov_b32 s69,0x7fff0000
s_mov_b32 s66,0x7fff0000
s_mov_b32 s76,0x7fff0000
s_branch Lg1_done_%=
Lg1_K1_%=:
Lg1_L1_%=:
v_mfma_f32_16x16x32_bf16 v[30:33],a[4:7],v[34:37],0
s_add_u32 m0,s59,3072
s_mov_b32 s67,s65
R 54,2,5120
M 30,a[12:15],38
buffer_load_dword v8,s[36:39],s58 offen offset:0 sc1 lds
v_mfma_f32_16x16x32_bf16 v[26:29],a[0:3],v[34:37],0
s_add_u32 s65,s65,s62
R 58,2,6144
v_fma_f32 v18,-v98,v106,v18
M 26,a[8:11],38
L 3
v_fma_f32 v19,-v98,v107,v19
v_fma_f32 v20,-v98,v108,v20
M 26,a[16:19],42
R 62,2,7168
v_fma_f32 v21,-v98,v109,v21
M 26,a[24:27],46
v_mul_f32 v114,v18,v18
v_mul_f32 v115,v19,v19
M 30,a[20:23],42
R 66,2,8192
v_mul_f32 v116,v20,v20
M 30,a[28:31],46
L 3
v_mul_f32 v117,v21,v21
v_fma_f32 v114,v100,v114,v99
M 30,a[36:39],50
R 70,2,9216
v_fma_f32 v115,v100,v115,v99
M 30,a[44:47],54
v_fma_f32 v116,v100,v116,v99
v_fma_f32 v117,v100,v117,v99
M 26,a[32:35],50
R 74,2,10240
v_mul_f32 v114,v18,v114
M 26,a[40:43],54
L 3
v_exp_f32 v114,v114
M 26,a[48:51],58
R 78,2,11264
v_mul_f32 v115,v19,v115
M 26,a[56:59],62
v_exp_f32 v115,v115
M 30,a[52:55],58
R 82,2,12288
v_mul_f32 v116,v20,v116
M 30,a[60:63],62
L 3
v_exp_f32 v116,v116
M 30,a[68:71],66
R 86,2,13312
v_mul_f32 v117,v21,v117
M 30,a[76:79],70
buffer_load_dwordx4 v[102:105],v11,s[36:39],s73 offen offset:0 sc1
M 26,a[64:67],66
s_add_u32 s73,s73,s64
R 90,2,14336
v_fma_f32 v114,v114,v101,v101
M 26,a[72:75],70
L 3
v_exp_f32 v117,v117
M 26,a[80:83],74
s_waitcnt vmcnt(1)
R 94,2,15360
v_fma_f32 v115,v115,v101,v101
M 26,a[88:91],78
s_barrier
ds_read_b32 v7,v6 offset:3072
v_fma_f32 v116,v116,v101,v101
M 30,a[84:87],74
R 34,2,16384
v_fma_f32 v117,v117,v101,v101
M 30,a[92:95],78
L 4
buffer_store_dword v4,v3,s[36:39],s70 offen offset:0 sc0 sc1
M 30,a[100:103],82
s_mov_b32 s70,0x7fff0000
R 38,2,17408
v_fma_f32 v22,-v98,v110,v22
M 30,a[108:111],86
v_rcp_f32 v114,v114
M 26,a[96:99],82
R 42,2,18432
v_mul_f32 v18,v18,v114
M 26,a[104:107],86
L 4
L 3
v_cmp_eq_u32 vcc,s53,v7
M 26,a[112:115],90
R 46,2,19456
v_fma_f32 v23,-v98,v111,v23
M 26,a[120:123],94
v_rcp_f32 v115,v115
M 30,a[116:119],90
s_cmp_eq_u32 vcc_lo,-1
R 50,2,20480
v_mul_f32 v19,v19,v115
s_cbranch_scc0 Lg1_X1_%=
M 30,a[124:127],94
L 3
s_add_u32 m0,s57,0
s_add_u32 s56,s55,0
M 30,a[132:135],34
s_add_u32 s58,s58,s61
R 54,2,21504
v_cvt_pk_bf16_f32 v122,v18,v19
M 30,a[140:143],38
D 0,56,0
M 26,a[128:131],34
s_add_u32 s54,s54,s46
R 58,2,22528
v_fma_f32 v24,-v98,v112,v24
M 26,a[136:139],38
L 3
v_rcp_f32 v116,v116
M 26,a[144:147],42
R 62,2,23552
v_mul_f32 v20,v20,v116
M 26,a[152:155],46
D 0,56,1024
M 30,a[148:151],42
R 66,2,24576
v_fma_f32 v25,-v98,v113,v25
M 30,a[156:159],46
L 3
v_rcp_f32 v117,v117
M 30,a[164:167],50
R 70,2,25600
v_mul_f32 v21,v21,v117
M 30,a[172:175],54
D 0,56,2048
M 26,a[160:163],50
R 74,2,26624
v_cvt_pk_bf16_f32 v123,v20,v21
M 26,a[168:171],54
L 3
v_mul_f32 v118,v22,v22
v_mul_f32 v119,v23,v23
M 26,a[176:179],58
R 78,2,27648
v_mul_f32 v120,v24,v24
M 26,a[184:187],62
D 0,56,3072
M 30,a[180:183],58
s_add_u32 m0,s59,1024
R 82,2,28672
v_mul_f32 v121,v25,v25
M 30,a[188:191],62
L 3
buffer_load_dword v8,s[36:39],s58 offen offset:0 sc1 lds
M 30,a[196:199],66
s_add_u32 m0,s57,16384
R 86,2,29696
v_fma_f32 v118,v100,v118,v99
M 30,a[204:207],70
s_add_u32 s56,s55,16384
v_fma_f32 v119,v100,v119,v99
v_fma_f32 v120,v100,v120,v99
M 26,a[192:195],66
s_add_u32 s55,s55,s60
R 90,2,30720
v_fma_f32 v121,v100,v121,v99
M 26,a[200:203],70
L 3
D 0,56,0
M 26,a[208:211],74
s_waitcnt vmcnt(1)
R 94,2,31744
v_mul_f32 v118,v22,v118
M 26,a[216:219],78
s_barrier
ds_read_b32 v7,v6 offset:1024
v_mul_f32 v119,v23,v119
M 30,a[212:215],74
s_sub_u32 s57,s84,s57
R 34,1,0
v_mul_f32 v120,v24,v120
M 30,a[220:223],78
L 4
buffer_store_dword v4,v3,s[36:39],s70 offen offset:0 sc0 sc1
v_mul_f32 v121,v25,v121
v_exp_f32 v118,v118
v_exp_f32 v119,v119
v_exp_f32 v120,v120
v_exp_f32 v121,v121
M 30,a[228:231],82
s_mov_b32 s70,s69
R 38,1,1024
v_fma_f32 v118,v118,v101,v101
v_fma_f32 v119,v119,v101,v101
v_fma_f32 v120,v120,v101,v101
v_fma_f32 v121,v121,v101,v101
v_rcp_f32 v118,v118
v_rcp_f32 v119,v119
v_rcp_f32 v120,v120
v_rcp_f32 v121,v121
M 30,a[236:239],86
s_mov_b32 s69,s68
v_mul_f32 v22,v22,v118
v_mul_f32 v23,v23,v119
v_mul_f32 v24,v24,v120
v_mul_f32 v25,v25,v121
v_cvt_pk_bf16_f32 v124,v22,v23
v_cvt_pk_bf16_f32 v125,v24,v25
M 26,a[224:227],82
s_add_u32 s68,s68,s63
R 42,1,2048
buffer_store_dwordx4 v[122:125],v15,s[36:39],s66 offen offset:0 sc0 sc1
M 26,a[232:235],86
L 4
L 3
M 26,a[240:243],90
R 46,1,3072
v_cmp_eq_u32 vcc,s53,v7
M 26,a[248:251],94
D 0,56,1024
M 30,a[244:247],90
L 2
s_cmp_eq_u32 vcc_lo,-1
R 50,1,4096
buffer_store_dwordx4 v[122:125],v16,s[36:39],s66 offen offset:0 sc0 sc1
M 30,a[252:255],94
D 0,56,2048
D 0,56,3072
buffer_store_dword v4,v3,s[36:39],s71 offen offset:0 sc0 sc1
buffer_store_dword v4,v3,s[36:39],s71 offen offset:0 sc0 sc1
buffer_store_dword v4,v3,s[36:39],s71 offen offset:0 sc0 sc1
s_cbranch_scc1 Lg1_F0_%=
s_branch Lg1_K0_%=
Lg1_X1_%=:
M 30,a[124:127],94
L 3
s_add_u32 m0,s59,1024
v_cvt_pk_bf16_f32 v122,v18,v19
M 30,a[132:135],34
R 54,2,21504
v_fma_f32 v24,-v98,v112,v24
M 30,a[140:143],38
buffer_load_dword v8,s[36:39],s58 offen offset:0 sc1 lds
M 26,a[128:131],34
R 58,2,22528
v_fma_f32 v25,-v98,v113,v25
M 26,a[136:139],38
L 3
v_rcp_f32 v116,v116
M 26,a[144:147],42
R 62,2,23552
v_mul_f32 v20,v20,v116
M 26,a[152:155],46
v_rcp_f32 v117,v117
M 30,a[148:151],42
R 66,2,24576
v_mul_f32 v21,v21,v117
M 30,a[156:159],46
L 3
v_cvt_pk_bf16_f32 v123,v20,v21
v_mul_f32 v118,v22,v22
M 30,a[164:167],50
R 70,2,25600
v_mul_f32 v119,v23,v23
M 30,a[172:175],54
v_mul_f32 v120,v24,v24
v_mul_f32 v121,v25,v25
M 26,a[160:163],50
R 74,2,26624
v_fma_f32 v118,v100,v118,v99
M 26,a[168:171],54
L 3
v_fma_f32 v119,v100,v119,v99
v_fma_f32 v120,v100,v120,v99
M 26,a[176:179],58
R 78,2,27648
v_fma_f32 v121,v100,v121,v99
M 26,a[184:187],62
v_mul_f32 v118,v22,v118
v_mul_f32 v119,v23,v119
M 30,a[180:183],58
R 82,2,28672
v_mul_f32 v120,v24,v120
M 30,a[188:191],62
L 3
v_exp_f32 v118,v118
M 30,a[196:199],66
R 86,2,29696
v_mul_f32 v121,v25,v121
M 30,a[204:207],70
v_exp_f32 v119,v119
M 26,a[192:195],66
R 90,2,30720
v_fma_f32 v118,v118,v101,v101
M 26,a[200:203],70
L 3
v_exp_f32 v120,v120
M 26,a[208:211],74
s_waitcnt vmcnt(0)
R 94,2,31744
v_fma_f32 v119,v119,v101,v101
M 26,a[216:219],78
s_barrier
buffer_store_dword v4,v3,s[36:39],s70 offen offset:0 sc0 sc1
M 30,a[212:215],74
s_mov_b32 s70,s69
ds_read_b32 v7,v6 offset:1024
v_fma_f32 v120,v120,v101,v101
M 30,a[220:223],78
L 3
v_exp_f32 v121,v121
M 30,a[228:231],82
s_mov_b32 s69,s68
v_fma_f32 v121,v121,v101,v101
v_rcp_f32 v118,v118
v_rcp_f32 v119,v119
v_rcp_f32 v120,v120
v_rcp_f32 v121,v121
M 30,a[236:239],86
s_add_u32 s68,s68,s63
v_mul_f32 v22,v22,v118
v_mul_f32 v23,v23,v119
v_mul_f32 v24,v24,v120
v_mul_f32 v25,v25,v121
v_cvt_pk_bf16_f32 v124,v22,v23
v_cvt_pk_bf16_f32 v125,v24,v25
M 26,a[224:227],82
buffer_store_dwordx4 v[122:125],v15,s[36:39],s66 offen offset:0 sc0 sc1
M 26,a[232:235],86
L 1
M 26,a[240:243],90
L 0
v_cmp_eq_u32 vcc,s53,v7
M 26,a[248:251],94
buffer_store_dwordx4 v[122:125],v16,s[36:39],s66 offen offset:0 sc0 sc1
M 30,a[244:247],90
s_nop 0
M 30,a[252:255],94
s_mov_b32 s80,0
s_cmp_eq_u32 vcc_lo,-1
s_cbranch_scc0 Lg1_xn1_%=
s_add_u32 m0,s57,0
s_add_u32 s56,s55,0
D 0,56,0
v_fma_f32 v26,-v102,v106,v26
v_fma_f32 v27,-v102,v107,v27
v_fma_f32 v28,-v102,v108,v28
v_fma_f32 v29,-v102,v109,v29
v_mul_f32 v114,v26,v26
v_mul_f32 v115,v27,v27
v_mul_f32 v116,v28,v28
D 0,56,1024
v_mul_f32 v117,v29,v29
v_fma_f32 v114,v104,v114,v103
v_fma_f32 v115,v104,v115,v103
v_fma_f32 v116,v104,v116,v103
v_fma_f32 v117,v104,v117,v103
v_mul_f32 v114,v26,v114
v_mul_f32 v115,v27,v115
D 0,56,2048
)"
R"(v_mul_f32 v116,v28,v116
v_mul_f32 v117,v29,v117
v_exp_f32 v114,v114
v_exp_f32 v115,v115
v_exp_f32 v116,v116
v_exp_f32 v117,v117
v_fma_f32 v114,v114,v105,v105
D 0,56,3072
v_fma_f32 v115,v115,v105,v105
v_fma_f32 v116,v116,v105,v105
v_fma_f32 v117,v117,v105,v105
v_rcp_f32 v114,v114
v_rcp_f32 v115,v115
v_rcp_f32 v116,v116
v_rcp_f32 v117,v117
s_add_u32 s58,s58,s61
s_add_u32 m0,s57,16384
s_add_u32 s56,s55,16384
D 0,56,0
v_mul_f32 v26,v26,v114
v_mul_f32 v27,v27,v115
v_mul_f32 v28,v28,v116
v_mul_f32 v29,v29,v117
v_cvt_pk_bf16_f32 v122,v26,v27
v_cvt_pk_bf16_f32 v123,v28,v29
v_fma_f32 v30,-v102,v110,v30
D 0,56,1024
v_fma_f32 v31,-v102,v111,v31
v_fma_f32 v32,-v102,v112,v32
v_fma_f32 v33,-v102,v113,v33
v_mul_f32 v118,v30,v30
v_mul_f32 v119,v31,v31
v_mul_f32 v120,v32,v32
v_mul_f32 v121,v33,v33
D 0,56,2048
v_fma_f32 v118,v104,v118,v103
v_fma_f32 v119,v104,v119,v103
v_fma_f32 v120,v104,v120,v103
v_fma_f32 v121,v104,v121,v103
v_mul_f32 v118,v30,v118
v_mul_f32 v119,v31,v119
v_mul_f32 v120,v32,v120
D 0,56,3072
v_mul_f32 v121,v33,v121
v_exp_f32 v118,v118
v_exp_f32 v119,v119
v_exp_f32 v120,v120
v_exp_f32 v121,v121
v_fma_f32 v118,v118,v105,v105
v_fma_f32 v119,v119,v105,v105
s_add_u32 s55,s55,s60
s_sub_u32 s57,s84,s57
s_add_u32 s54,s54,s46
v_fma_f32 v120,v120,v105,v105
v_fma_f32 v121,v121,v105,v105
v_rcp_f32 v118,v118
v_rcp_f32 v119,v119
v_rcp_f32 v120,v120
v_rcp_f32 v121,v121
v_mul_f32 v30,v30,v118
v_mul_f32 v31,v31,v119
v_mul_f32 v32,v32,v120
v_mul_f32 v33,v33,v121
v_cvt_pk_bf16_f32 v124,v30,v31
v_cvt_pk_bf16_f32 v125,v32,v33
buffer_store_dwordx4 v[122:125],v15,s[36:39],s67 offen offset:0 sc0 sc1
buffer_store_dwordx4 v[122:125],v16,s[36:39],s67 offen offset:0 sc0 sc1
s_mov_b32 s72,s69
s_mov_b32 s69,0x7fff0000
s_mov_b32 s67,0x7fff0000
s_mov_b32 s77,0x7fff0000
s_branch Lg1_gox_%=
Lg1_xn1_%=:
s_cmp_ge_u32 s54,256
s_cbranch_scc1 Lg1_fin1_%=
s_add_u32 m0,s59,4096
s_nop 0
buffer_load_dword v8,s[36:39],s58 offen offset:0 sc1 lds
buffer_store_dword v4,v3,s[36:39],s71 offen offset:0 sc0 sc1
s_add_u32 m0,s59,5120
s_nop 0
buffer_load_dword v8,s[36:39],s58 offen offset:0 sc1 lds
buffer_store_dword v4,v3,s[36:39],s71 offen offset:0 sc0 sc1
s_mov_b32 s83,0
v_fma_f32 v26,-v102,v106,v26
v_fma_f32 v27,-v102,v107,v27
v_fma_f32 v28,-v102,v108,v28
v_fma_f32 v29,-v102,v109,v29
v_mul_f32 v114,v26,v26
v_mul_f32 v115,v27,v27
v_mul_f32 v116,v28,v28
v_mul_f32 v117,v29,v29
v_fma_f32 v114,v104,v114,v103
v_fma_f32 v115,v104,v115,v103
v_fma_f32 v116,v104,v116,v103
v_fma_f32 v117,v104,v117,v103
v_mul_f32 v114,v26,v114
v_mul_f32 v115,v27,v115
v_mul_f32 v116,v28,v116
v_mul_f32 v117,v29,v117
v_exp_f32 v114,v114
v_exp_f32 v115,v115
v_exp_f32 v116,v116
v_exp_f32 v117,v117
v_fma_f32 v114,v114,v105,v105
v_fma_f32 v115,v115,v105,v105
v_fma_f32 v116,v116,v105,v105
v_fma_f32 v117,v117,v105,v105
v_rcp_f32 v114,v114
v_rcp_f32 v115,v115
v_rcp_f32 v116,v116
v_rcp_f32 v117,v117
v_mul_f32 v26,v26,v114
v_mul_f32 v27,v27,v115
v_mul_f32 v28,v28,v116
v_mul_f32 v29,v29,v117
v_cvt_pk_bf16_f32 v122,v26,v27
v_cvt_pk_bf16_f32 v123,v28,v29
v_fma_f32 v30,-v102,v110,v30
v_fma_f32 v31,-v102,v111,v31
v_fma_f32 v32,-v102,v112,v32
v_fma_f32 v33,-v102,v113,v33
v_mul_f32 v118,v30,v30
v_mul_f32 v119,v31,v31
v_mul_f32 v120,v32,v32
v_mul_f32 v121,v33,v33
v_fma_f32 v118,v104,v118,v103
v_fma_f32 v119,v104,v119,v103
v_fma_f32 v120,v104,v120,v103
v_fma_f32 v121,v104,v121,v103
v_mul_f32 v118,v30,v118
v_mul_f32 v119,v31,v119
v_mul_f32 v120,v32,v120
v_mul_f32 v121,v33,v121
v_exp_f32 v118,v118
v_exp_f32 v119,v119
v_exp_f32 v120,v120
v_exp_f32 v121,v121
v_fma_f32 v118,v118,v105,v105
v_fma_f32 v119,v119,v105,v105
v_fma_f32 v120,v120,v105,v105
v_fma_f32 v121,v121,v105,v105
v_rcp_f32 v118,v118
v_rcp_f32 v119,v119
v_rcp_f32 v120,v120
v_rcp_f32 v121,v121
v_mul_f32 v30,v30,v118
v_mul_f32 v31,v31,v119
v_mul_f32 v32,v32,v120
v_mul_f32 v33,v33,v121
v_cvt_pk_bf16_f32 v124,v30,v31
v_cvt_pk_bf16_f32 v125,v32,v33
buffer_store_dwordx4 v[122:125],v15,s[36:39],s67 offen offset:0 sc0 sc1
buffer_store_dwordx4 v[122:125],v16,s[36:39],s67 offen offset:0 sc0 sc1
s_mov_b32 s72,s69
s_mov_b32 s69,0x7fff0000
s_mov_b32 s67,0x7fff0000
s_mov_b32 s77,0x7fff0000
s_branch Lg1_poll_%=
Lg1_fin1_%=:
v_fma_f32 v26,-v102,v106,v26
v_fma_f32 v27,-v102,v107,v27
v_fma_f32 v28,-v102,v108,v28
v_fma_f32 v29,-v102,v109,v29
v_mul_f32 v114,v26,v26
v_mul_f32 v115,v27,v27
v_mul_f32 v116,v28,v28
v_mul_f32 v117,v29,v29
v_fma_f32 v114,v104,v114,v103
v_fma_f32 v115,v104,v115,v103
v_fma_f32 v116,v104,v116,v103
v_fma_f32 v117,v104,v117,v103
v_mul_f32 v114,v26,v114
v_mul_f32 v115,v27,v115
v_mul_f32 v116,v28,v116
v_mul_f32 v117,v29,v117
v_exp_f32 v114,v114
v_exp_f32 v115,v115
v_exp_f32 v116,v116
v_exp_f32 v117,v117
v_fma_f32 v114,v114,v105,v105
v_fma_f32 v115,v115,v105,v105
v_fma_f32 v116,v116,v105,v105
v_fma_f32 v117,v117,v105,v105
v_rcp_f32 v114,v114
v_rcp_f32 v115,v115
v_rcp_f32 v116,v116
v_rcp_f32 v117,v117
v_mul_f32 v26,v26,v114
v_mul_f32 v27,v27,v115
v_mul_f32 v28,v28,v116
v_mul_f32 v29,v29,v117
v_cvt_pk_bf16_f32 v122,v26,v27
v_cvt_pk_bf16_f32 v123,v28,v29
v_fma_f32 v30,-v102,v110,v30
v_fma_f32 v31,-v102,v111,v31
v_fma_f32 v32,-v102,v112,v32
v_fma_f32 v33,-v102,v113,v33
v_mul_f32 v118,v30,v30
v_mul_f32 v119,v31,v31
v_mul_f32 v120,v32,v32
v_mul_f32 v121,v33,v33
v_fma_f32 v118,v104,v118,v103
v_fma_f32 v119,v104,v119,v103
v_fma_f32 v120,v104,v120,v103
v_fma_f32 v121,v104,v121,v103
v_mul_f32 v118,v30,v118
v_mul_f32 v119,v31,v119
v_mul_f32 v120,v32,v120
v_mul_f32 v121,v33,v121
v_exp_f32 v118,v118
v_exp_f32 v119,v119
v_exp_f32 v120,v120
v_exp_f32 v121,v121
v_fma_f32 v118,v118,v105,v105
v_fma_f32 v119,v119,v105,v105
v_fma_f32 v120,v120,v105,v105
v_fma_f32 v121,v121,v105,v105
v_rcp_f32 v118,v118
v_rcp_f32 v119,v119
v_rcp_f32 v120,v120
v_rcp_f32 v121,v121
v_mul_f32 v30,v30,v118
v_mul_f32 v31,v31,v119
v_mul_f32 v32,v32,v120
v_mul_f32 v33,v33,v121
v_cvt_pk_bf16_f32 v124,v30,v31
v_cvt_pk_bf16_f32 v125,v32,v33
buffer_store_dwordx4 v[122:125],v15,s[36:39],s67 offen offset:0 sc0 sc1
buffer_store_dwordx4 v[122:125],v16,s[36:39],s67 offen offset:0 sc0 sc1
s_mov_b32 s72,s69
s_mov_b32 s69,0x7fff0000
s_mov_b32 s67,0x7fff0000
s_mov_b32 s77,0x7fff0000
s_branch Lg1_done_%=
Lg1_done_%=:
s_waitcnt vmcnt(2)
s_barrier
buffer_store_dword v4,v3,s[36:39],s70 offen offset:0 sc0 sc1
s_waitcnt vmcnt(1)
s_barrier
buffer_store_dword v4,v3,s[36:39],s72 offen offset:0 sc0 sc1
s_waitcnt vmcnt(0) lgkmcnt(0)
s_barrier
s_branch Lg2_end_%=
Lg2_begin_%=:
Lcode_g2_b:
s_mov_b64 s[36:37],%1
s_mov_b32 s38,111243392
s_mov_b32 s39,0x00020000
s_mov_b32 s44,%2
s_mov_b32 s45,%3
s_mov_b32 s46,%4
s_mov_b32 s47,%5
s_mov_b32 s48,%6
s_mov_b32 s49,%7
s_mov_b32 s50,%8
s_mov_b32 s51,%9
s_mov_b32 s53,%10
v_mov_b32 v0,%11
v_mov_b32 v3,%12
v_mov_b32 v5,%13
v_mov_b32 v6,%14
v_mov_b32 v18,%15
v_mov_b32 v19,%16
v_mov_b32 v20,%17
v_mov_b32 v21,%18
v_mov_b32 v9,%19
v_mov_b32 v10,%20
v_mov_b32 v11,%21
v_mov_b32 v12,%22
v_mov_b32 v13,%23
v_mov_b32 v14,%24
v_mov_b32 v15,%25
v_mov_b32 v16,%26
s_mov_b64 s[40:41],%27
s_mov_b32 s42,2097152
s_mov_b32 s43,0x00020000
s_lshl_b32 s57,s44,11
s_cmp_ge_u32 s45,8
s_cbranch_scc1 Lg2_dly_%=
s_sleep 16
s_sleep 16
s_sleep 16
s_sleep 16
Lg2_dly_%=:
s_add_u32 s55,s50,0
s_add_u32 m0,s57,0
s_nop 0
buffer_load_dwordx4 v18,s[40:43],s55 offen offset:0  lds
s_add_u32 m0,s57,1024
s_nop 0
buffer_load_dwordx4 v19,s[40:43],s55 offen offset:0  lds
s_add_u32 m0,s57,8064
s_nop 0
buffer_load_dwordx4 v18,s[40:43],s55 offen offset:128  lds
s_add_u32 m0,s57,9088
s_nop 0
buffer_load_dwordx4 v19,s[40:43],s55 offen offset:128  lds
s_add_u32 s55,s50,131072
s_add_u32 m0,s57,16384
s_nop 0
buffer_load_dwordx4 v18,s[40:43],s55 offen offset:0  lds
s_add_u32 m0,s57,17408
s_nop 0
buffer_load_dwordx4 v19,s[40:43],s55 offen offset:0  lds
s_add_u32 m0,s57,24448
s_nop 0
buffer_load_dwordx4 v18,s[40:43],s55 offen offset:128  lds
s_add_u32 m0,s57,25472
s_nop 0
buffer_load_dwordx4 v19,s[40:43],s55 offen offset:128  lds
s_add_u32 s55,s50,262144
s_add_u32 m0,s57,32768
s_nop 0
buffer_load_dwordx4 v18,s[40:43],s55 offen offset:0  lds
s_add_u32 m0,s57,33792
s_nop 0
buffer_load_dwordx4 v19,s[40:43],s55 offen offset:0  lds
s_add_u32 m0,s57,40832
s_nop 0
buffer_load_dwordx4 v18,s[40:43],s55 offen offset:128  lds
s_add_u32 m0,s57,41856
s_nop 0
buffer_load_dwordx4 v19,s[40:43],s55 offen offset:128  lds
s_add_u32 s55,s50,393216
s_add_u32 m0,s57,49152
s_nop 0
buffer_load_dwordx4 v18,s[40:43],s55 offen offset:0  lds
s_add_u32 m0,s57,50176
s_nop 0
buffer_load_dwordx4 v19,s[40:43],s55 offen offset:0  lds
s_add_u32 m0,s57,57216
s_nop 0
buffer_load_dwordx4 v18,s[40:43],s55 offen offset:128  lds
s_add_u32 m0,s57,58240
s_nop 0
buffer_load_dwordx4 v19,s[40:43],s55 offen offset:128  lds
s_add_u32 s55,s50,524288
s_add_u32 m0,s57,65536
s_nop 0
buffer_load_dwordx4 v18,s[40:43],s55 offen offset:0  lds
s_add_u32 m0,s57,66560
s_nop 0
buffer_load_dwordx4 v19,s[40:43],s55 offen offset:0  lds
s_add_u32 m0,s57,73600
s_nop 0
buffer_load_dwordx4 v18,s[40:43],s55 offen offset:128  lds
s_add_u32 m0,s57,74624
s_nop 0
buffer_load_dwordx4 v19,s[40:43],s55 offen offset:128  lds
s_add_u32 s55,s50,655360
s_add_u32 m0,s57,81920
s_nop 0
buffer_load_dwordx4 v18,s[40:43],s55 offen offset:0  lds
s_add_u32 m0,s57,82944
s_nop 0
buffer_load_dwordx4 v19,s[40:43],s55 offen offset:0  lds
s_add_u32 m0,s57,89984
s_nop 0
buffer_load_dwordx4 v18,s[40:43],s55 offen offset:128  lds
s_add_u32 m0,s57,91008
s_nop 0
buffer_load_dwordx4 v19,s[40:43],s55 offen offset:128  lds
s_waitcnt vmcnt(20) lgkmcnt(0)
s_barrier
s_add_u32 s55,s50,786432
s_add_u32 m0,s57,98304
s_nop 0
buffer_load_dwordx4 v18,s[40:43],s55 offen offset:0  lds
s_add_u32 m0,s57,99328
s_nop 0
buffer_load_dwordx4 v19,s[40:43],s55 offen offset:0  lds
s_add_u32 m0,s57,106368
s_nop 0
buffer_load_dwordx4 v18,s[40:43],s55 offen offset:128  lds
s_add_u32 m0,s57,107392
s_nop 0
buffer_load_dwordx4 v19,s[40:43],s55 offen offset:128  lds
v_add_u32 v26,0,v20
v_add_u32 v27,0,v21
T a[0:1],26,0
T a[2:3],26,512
T a[4:5],27,0
T a[6:7],27,512
T a[8:9],26,4096
T a[10:11],26,4608
T a[12:13],27,4096
T a[14:15],27,4608
s_waitcnt vmcnt(20) lgkmcnt(0)
s_barrier
s_add_u32 s55,s50,917504
s_add_u32 m0,s57,0
s_nop 0
buffer_load_dwordx4 v18,s[40:43],s55 offen offset:0  lds
s_add_u32 m0,s57,1024
s_nop 0
buffer_load_dwordx4 v19,s[40:43],s55 offen offset:0  lds
s_add_u32 m0,s57,8064
s_nop 0
buffer_load_dwordx4 v18,s[40:43],s55 offen offset:128  lds
s_add_u32 m0,s57,9088
s_nop 0
buffer_load_dwordx4 v19,s[40:43],s55 offen offset:128  lds
v_add_u32 v26,16384,v20
v_add_u32 v27,16384,v21
T a[16:17],26,0
T a[18:19],26,512
T a[20:21],27,0
T a[22:23],27,512
T a[24:25],26,4096
T a[26:27],26,4608
T a[28:29],27,4096
T a[30:31],27,4608
s_waitcnt vmcnt(20) lgkmcnt(0)
s_barrier
s_add_u32 s55,s50,1048576
s_add_u32 m0,s57,16384
s_nop 0
buffer_load_dwordx4 v18,s[40:43],s55 offen offset:0  lds
s_add_u32 m0,s57,17408
s_nop 0
buffer_load_dwordx4 v19,s[40:43],s55 offen offset:0  lds
s_add_u32 m0,s57,24448
s_nop 0
buffer_load_dwordx4 v18,s[40:43],s55 offen offset:128  lds
s_add_u32 m0,s57,25472
s_nop 0
buffer_load_dwordx4 v19,s[40:43],s55 offen offset:128  lds
v_add_u32 v26,32768,v20
v_add_u32 v27,32768,v21
T a[32:33],26,0
T a[34:35],26,512
T a[36:37],27,0
T a[38:39],27,512
T a[40:41],26,4096
T a[42:43],26,4608
T a[44:45],27,4096
T a[46:47],27,4608
s_waitcnt vmcnt(20) lgkmcnt(0)
s_barrier
s_add_u32 s55,s50,1179648
s_add_u32 m0,s57,32768
s_nop 0
buffer_load_dwordx4 v18,s[40:43],s55 offen offset:0  lds
s_add_u32 m0,s57,33792
s_nop 0
buffer_load_dwordx4 v19,s[40:43],s55 offen offset:0  lds
s_add_u32 m0,s57,40832
s_nop 0
buffer_load_dwordx4 v18,s[40:43],s55 offen offset:128  lds
s_add_u32 m0,s57,41856
s_nop 0
buffer_load_dwordx4 v19,s[40:43],s55 offen offset:128  lds
v_add_u32 v26,49152,v20
v_add_u32 v27,49152,v21
T a[48:49],26,0
T a[50:51],26,512
T a[52:53],27,0
T a[54:55],27,512
T a[56:57],26,4096
T a[58:59],26,4608
T a[60:61],27,4096
T a[62:63],27,4608
s_waitcnt vmcnt(20) lgkmcnt(0)
s_barrier
s_add_u32 s55,s50,1310720
s_add_u32 m0,s57,49152
s_nop 0
buffer_load_dwordx4 v18,s[40:43],s55 offen offset:0  lds
s_add_u32 m0,s57,50176
s_nop 0
buffer_load_dwordx4 v19,s[40:43],s55 offen offset:0  lds
s_add_u32 m0,s57,57216
s_nop 0
buffer_load_dwordx4 v18,s[40:43],s55 offen offset:128  lds
s_add_u32 m0,s57,58240
s_nop 0
buffer_load_dwordx4 v19,s[40:43],s55 offen offset:128  lds
v_add_u32 v26,65536,v20
v_add_u32 v27,65536,v21
T a[64:65],26,0
T a[66:67],26,512
T a[68:69],27,0
T a[70:71],27,512
T a[72:73],26,4096
T a[74:75],26,4608
T a[76:77],27,4096
T a[78:79],27,4608
s_waitcnt vmcnt(20) lgkmcnt(0)
s_barrier
s_add_u32 s55,s50,1441792
s_add_u32 m0,s57,65536
s_nop 0
buffer_load_dwordx4 v18,s[40:43],s55 offen offset:0  lds
s_add_u32 m0,s57,66560
s_nop 0
buffer_load_dwordx4 v19,s[40:43],s55 offen offset:0  lds
s_add_u32 m0,s57,73600
s_nop 0
)"
R"(buffer_load_dwordx4 v18,s[40:43],s55 offen offset:128  lds
s_add_u32 m0,s57,74624
s_nop 0
buffer_load_dwordx4 v19,s[40:43],s55 offen offset:128  lds
v_add_u32 v26,81920,v20
v_add_u32 v27,81920,v21
T a[80:81],26,0
T a[82:83],26,512
T a[84:85],27,0
T a[86:87],27,512
T a[88:89],26,4096
T a[90:91],26,4608
T a[92:93],27,4096
T a[94:95],27,4608
s_waitcnt vmcnt(20) lgkmcnt(0)
s_barrier
s_add_u32 s55,s50,1572864
s_add_u32 m0,s57,81920
s_nop 0
buffer_load_dwordx4 v18,s[40:43],s55 offen offset:0  lds
s_add_u32 m0,s57,82944
s_nop 0
buffer_load_dwordx4 v19,s[40:43],s55 offen offset:0  lds
s_add_u32 m0,s57,89984
s_nop 0
buffer_load_dwordx4 v18,s[40:43],s55 offen offset:128  lds
s_add_u32 m0,s57,91008
s_nop 0
buffer_load_dwordx4 v19,s[40:43],s55 offen offset:128  lds
v_add_u32 v26,98304,v20
v_add_u32 v27,98304,v21
T a[96:97],26,0
T a[98:99],26,512
T a[100:101],27,0
T a[102:103],27,512
T a[104:105],26,4096
T a[106:107],26,4608
T a[108:109],27,4096
T a[110:111],27,4608
s_waitcnt vmcnt(20) lgkmcnt(0)
s_barrier
s_add_u32 s55,s50,1703936
s_add_u32 m0,s57,98304
s_nop 0
buffer_load_dwordx4 v18,s[40:43],s55 offen offset:0  lds
s_add_u32 m0,s57,99328
s_nop 0
buffer_load_dwordx4 v19,s[40:43],s55 offen offset:0  lds
s_add_u32 m0,s57,106368
s_nop 0
buffer_load_dwordx4 v18,s[40:43],s55 offen offset:128  lds
s_add_u32 m0,s57,107392
s_nop 0
buffer_load_dwordx4 v19,s[40:43],s55 offen offset:128  lds
v_add_u32 v26,0,v20
v_add_u32 v27,0,v21
T a[112:113],26,0
T a[114:115],26,512
T a[116:117],27,0
T a[118:119],27,512
T a[120:121],26,4096
T a[122:123],26,4608
T a[124:125],27,4096
T a[126:127],27,4608
s_waitcnt vmcnt(20) lgkmcnt(0)
s_barrier
s_add_u32 s55,s50,1835008
s_add_u32 m0,s57,0
s_nop 0
buffer_load_dwordx4 v18,s[40:43],s55 offen offset:0  lds
s_add_u32 m0,s57,1024
s_nop 0
buffer_load_dwordx4 v19,s[40:43],s55 offen offset:0  lds
s_add_u32 m0,s57,8064
s_nop 0
buffer_load_dwordx4 v18,s[40:43],s55 offen offset:128  lds
s_add_u32 m0,s57,9088
s_nop 0
buffer_load_dwordx4 v19,s[40:43],s55 offen offset:128  lds
v_add_u32 v26,16384,v20
v_add_u32 v27,16384,v21
T a[128:129],26,0
T a[130:131],26,512
T a[132:133],27,0
T a[134:135],27,512
T a[136:137],26,4096
T a[138:139],26,4608
T a[140:141],27,4096
T a[142:143],27,4608
s_waitcnt vmcnt(20) lgkmcnt(0)
s_barrier
s_add_u32 s55,s50,1966080
s_add_u32 m0,s57,16384
s_nop 0
buffer_load_dwordx4 v18,s[40:43],s55 offen offset:0  lds
s_add_u32 m0,s57,17408
s_nop 0
buffer_load_dwordx4 v19,s[40:43],s55 offen offset:0  lds
s_add_u32 m0,s57,24448
s_nop 0
buffer_load_dwordx4 v18,s[40:43],s55 offen offset:128  lds
s_add_u32 m0,s57,25472
s_nop 0
buffer_load_dwordx4 v19,s[40:43],s55 offen offset:128  lds
v_add_u32 v26,32768,v20
v_add_u32 v27,32768,v21
T a[144:145],26,0
T a[146:147],26,512
T a[148:149],27,0
T a[150:151],27,512
T a[152:153],26,4096
T a[154:155],26,4608
T a[156:157],27,4096
T a[158:159],27,4608
s_waitcnt vmcnt(20) lgkmcnt(0)
s_barrier
v_add_u32 v26,49152,v20
v_add_u32 v27,49152,v21
T a[160:161],26,0
T a[162:163],26,512
T a[164:165],27,0
T a[166:167],27,512
T a[168:169],26,4096
T a[170:171],26,4608
T a[172:173],27,4096
T a[174:175],27,4608
s_waitcnt vmcnt(16) lgkmcnt(0)
s_barrier
v_add_u32 v26,65536,v20
v_add_u32 v27,65536,v21
T a[176:177],26,0
T a[178:179],26,512
T a[180:181],27,0
T a[182:183],27,512
T a[184:185],26,4096
T a[186:187],26,4608
T a[188:189],27,4096
T a[190:191],27,4608
s_waitcnt vmcnt(12) lgkmcnt(0)
s_barrier
v_add_u32 v26,81920,v20
v_add_u32 v27,81920,v21
T a[192:193],26,0
T a[194:195],26,512
T a[196:197],27,0
T a[198:199],27,512
T a[200:201],26,4096
T a[202:203],26,4608
T a[204:205],27,4096
T a[206:207],27,4608
s_waitcnt vmcnt(8) lgkmcnt(0)
s_barrier
v_add_u32 v26,98304,v20
v_add_u32 v27,98304,v21
T a[208:209],26,0
T a[210:211],26,512
T a[212:213],27,0
T a[214:215],27,512
T a[216:217],26,4096
T a[218:219],26,4608
T a[220:221],27,4096
T a[222:223],27,4608
s_waitcnt vmcnt(4) lgkmcnt(0)
s_barrier
v_add_u32 v26,0,v20
v_add_u32 v27,0,v21
T a[224:225],26,0
T a[226:227],26,512
T a[228:229],27,0
T a[230:231],27,512
T a[232:233],26,4096
T a[234:235],26,4608
T a[236:237],27,4096
T a[238:239],27,4608
s_waitcnt vmcnt(0) lgkmcnt(0)
s_barrier
v_add_u32 v26,16384,v20
v_add_u32 v27,16384,v21
T a[240:241],26,0
T a[242:243],26,512
T a[244:245],27,0
T a[246:247],27,512
T a[248:249],26,4096
T a[250:251],26,4608
T a[252:253],27,4096
T a[254:255],27,4608
L 0
s_barrier
v_mov_b32 v4,s53
s_mov_b32 s82,0
s_mov_b32 s80,0
s_mov_b32 s54,s45
s_mul_i32 s57,s44,5120
s_mul_i32 s55,s45,40960
s_add_u32 s55,s55,s57
s_add_u32 s55,s55,17825792
s_mul_i32 s57,s44,4096
s_mul_i32 s60,s46,40960
s_mul_i32 s61,s46,1024
s_mul_i32 s62,s46,32768
s_mul_i32 s63,s46,1024
s_mul_i32 s64,s46,512
s_lshl_b32 s59,s44,8
s_add_u32 s59,s59,131072
s_mul_i32 s58,s45,1024
s_add_u32 s58,s58,109333504
s_mul_i32 s65,s45,32768
s_add_u32 s65,s65,s47
s_mul_i32 s68,s45,1024
s_add_u32 s68,s68,s48
s_mul_i32 s73,s45,512
s_add_u32 s73,s73,106954752
s_mul_i32 s74,s45,32768
s_add_u32 s74,s74,s49
s_mul_i32 s75,s45,1024
s_add_u32 s75,s75,107479040
s_mov_b32 s66,0x7fff0000
s_mov_b32 s67,0x7fff0000
s_mov_b32 s69,0x7fff0000
s_mov_b32 s76,0x7fff0000
s_mov_b32 s77,0x7fff0000
s_mov_b32 s70,0x7fff0000
s_mov_b32 s71,0x7fff0000
s_mov_b32 s72,0x7fff0000
v_mov_b32 v1,v0
v_add_u32 v2,16384,v0
s_mov_b32 s84,16384
s_mov_b32 s86,0x3a800000
s_mov_b32 s87,0x38a7c5ac
s_mov_b32 s88,0xc0135761
s_mov_b32 s89,0x3d372713
s_lshr_b32 s85,s44,1
s_mul_i32 s85,s85,127
v_lshlrev_b32 v8,22,v0
v_and_b32 v8,0x80000000,v8
v_or_b32 v8,v8,v5
v_add_u32 v2,32768,v0
s_lshl_b32 s84,s57,1
s_add_u32 s84,s84,32768
s_getpc_b64 s[40:41]
Lg2_pc_%=:
s_lshl_b32 s78,s44,13
v_lshlrev_b32 v106,3,v0
v_add_u32 v106,s78,v106
v_min_u32 v106,Lcode_g12_e-Lg2_pc_%=-4,v106
s_mov_b32 m0,163584
s_nop 0
global_load_lds_dword v106,s[40:41]
s_and_b32 s79,s54,127
s_cmp_eq_u32 s79,0
s_cselect_b32 s94,0x3300,0
s_cmp_eq_u32 s79,127
s_cselect_b32 s94,0xcc00,s94
s_add_u32 m0,s59,4096
s_nop 0
buffer_load_dword v8,s[36:39],s58 offen offset:0 sc1 lds
buffer_store_dword v4,v3,s[36:39],s71 offen offset:0 sc0 sc1
s_add_u32 m0,s59,5120
s_nop 0
buffer_load_dword v8,s[36:39],s58 offen offset:0 sc1 lds
buffer_store_dword v4,v3,s[36:39],s71 offen offset:0 sc0 sc1
s_mov_b32 s83,0
Lg2_poll_%=:
s_add_u32 s78,s83,2
s_and_b32 s78,s78,3
s_lshl_b32 s78,s78,10
s_add_u32 s78,s78,4096
s_add_u32 m0,s59,s78
s_nop 0
buffer_load_dword v8,s[36:39],s58 offen offset:0 sc1 lds
s_and_b32 s78,s83,3
s_lshl_b32 s78,s78,10
v_add_u32 v106,s78,v6
s_waitcnt vmcnt(4)
s_barrier
ds_read_b32 v7,v106 offset:4096
buffer_store_dword v4,v3,s[36:39],s70 offen offset:0 sc0 sc1
s_mov_b32 s70,s71
s_mov_b32 s71,s72
s_mov_b32 s72,0x7fff0000
s_add_u32 s83,s83,1
s_add_u32 s82,s82,1
L 0
v_cmp_eq_u32 vcc,s53,v7
s_nop 0
s_or_b64 vcc,vcc,s[94:95]
s_cmp_eq_u32 vcc_lo,-1
s_cbranch_scc1 Lg2_go_%=
s_cmp_lt_u32 s82,20000
s_cbranch_scc1 Lg2_poll_%=
Lg2_go_%=:
s_and_b32 s78,s54,127
s_cmp_eq_u32 s78,s85
s_cselect_b64 s[40:41],-1,0
v_cndmask_b32 v17,v15,v16,s[40:41]
s_add_u32 m0,s57,0
s_add_u32 s56,s55,0
D 17,56,0
s_add_u32 s56,s55,256
D 17,56,1024
s_add_u32 s56,s55,512
D 17,56,2048
s_add_u32 s56,s55,768
D 17,56,3072
s_add_u32 s58,s58,s61
s_add_u32 m0,s59,2048
s_nop 0
buffer_load_dword v8,s[36:39],s58 offen offset:0 sc1 lds
v_cndmask_b32 v17,v10,v9,s[40:41]
s_add_u32 m0,s57,16384
s_add_u32 s56,s55,20480
D 17,56,0
s_add_u32 s56,s55,20736
D 17,56,1024
s_add_u32 s56,s55,20992
D 17,56,2048
s_add_u32 s56,s55,21248
D 17,56,3072
s_add_u32 s55,s55,s60
s_sub_u32 s57,s84,s57
s_add_u32 s54,s54,s46
s_and_b32 s79,s54,127
s_cmp_eq_u32 s79,0
s_cselect_b32 s94,0x3300,0
s_cmp_eq_u32 s79,127
s_cselect_b32 s94,0xcc00,s94
s_waitcnt vmcnt(4)
s_barrier
buffer_store_dword v4,v3,s[36:39],s70 offen offset:0 sc0 sc1
s_min_u32 s71,s71,s72
buffer_store_dword v4,v3,s[36:39],s71 offen offset:0 sc0 sc1
s_mov_b32 s70,0x7fff0000
s_mov_b32 s71,0x7fff0000
s_mov_b32 s72,0x7fff0000
s_cmp_eq_u32 s80,1
buffer_store_dword v4,v3,s[36:39],s71 offen offset:0 sc0 sc1
s_cbranch_scc1 Lg2_st1_%=
s_branch Lg2_st0_%=
Lg2_gox_%=:
s_add_u32 m0,s59,2048
s_nop 0
buffer_load_dword v8,s[36:39],s58 offen offset:0 sc1 lds
s_and_b32 s79,s54,127
s_cmp_eq_u32 s79,0
s_cselect_b32 s94,0x3300,0
s_cmp_eq_u32 s79,127
s_cselect_b32 s94,0xcc00,s94
s_waitcnt vmcnt(0)
s_barrier
buffer_store_dword v4,v3,s[36:39],s70 offen offset:0 sc0 sc1
s_min_u32 s71,s71,s72
buffer_store_dword v4,v3,s[36:39],s71 offen offset:0 sc0 sc1
s_mov_b32 s70,0x7fff0000
s_mov_b32 s71,0x7fff0000
s_mov_b32 s72,0x7fff0000
s_cmp_eq_u32 s80,1
s_cbranch_scc1 Lg2_st1_%=
Lg2_st0_%=:
ds_read_b32 v7,v6 offset:2048
R 34,1,0
R 38,1,1024
R 42,1,2048
R 46,1,3072
R 50,1,4096
L 5
v_cmp_eq_u32 vcc,s53,v7
s_nop 0
s_or_b64 vcc,vcc,s[94:95]
L 3
s_cmp_eq_u32 vcc_lo,-1
s_cbranch_scc1 Lg2_F0_%=
s_branch Lg2_L0_%=
Lg2_st1_%=:
ds_read_b32 v7,v6 offset:2048
R 34,2,0
R 38,2,1024
R 42,2,2048
R 46,2,3072
R 50,2,4096
L 5
v_cmp_eq_u32 vcc,s53,v7
s_nop 0
s_or_b64 vcc,vcc,s[94:95]
L 3
s_cmp_eq_u32 vcc_lo,-1
s_cbranch_scc1 Lg2_F1_%=
s_branch Lg2_L1_%=
Lg2_F0_%=:
v_mfma_f32_16x16x32_bf16 v[22:25],a[4:7],v[34:37],0
s_add_u32 s58,s58,s61
s_add_u32 m0,s59,0
s_and_b32 s78,s54,127
R 54,1,5120
M 22,a[12:15],38
s_cmp_eq_u32 s78,s85
s_cselect_b64 s[40:41],-1,0
s_add_u32 s56,s55,0
buffer_load_dword v8,s[36:39],s58 offen offset:0 sc1 lds
v_mfma_f32_16x16x32_bf16 v[18:21],a[0:3],v[34:37],0
s_add_u32 m0,s57,0
R 58,1,6144
v_cndmask_b32 v17,v15,v16,s[40:41]
M 18,a[8:11],38
L 3
D 17,56,0
M 18,a[16:19],42
s_add_u32 s56,s55,256
R 62,1,7168
v_lshlrev_b32 v106,16,v126
M 18,a[24:27],46
s_add_u32 s54,s54,s46
v_and_b32 v107,0xffff0000,v126
v_lshlrev_b32 v108,16,v127
M 22,a[20:23],42
s_and_b32 s79,s54,127
R 66,1,8192
v_and_b32 v109,0xffff0000,v127
M 22,a[28:31],46
L 3
D 17,56,1024
M 22,a[36:39],50
s_add_u32 s56,s55,512
R 70,1,9216
v_lshlrev_b32 v110,16,v128
M 22,a[44:47],54
s_cmp_eq_u32 s79,0
v_and_b32 v111,0xffff0000,v128
v_lshlrev_b32 v112,16,v129
M 18,a[32:35],50
s_cselect_b32 s94,0x3300,0
R 74,1,10240
v_and_b32 v113,0xffff0000,v129
M 18,a[40:43],54
L 3
D 17,56,2048
M 18,a[48:51],58
s_add_u32 s56,s55,768
R 78,1,11264
v_fma_f32 v106,v106,v103,v102
M 18,a[56:59],62
D 17,56,3072
M 22,a[52:55],58
s_add_u32 m0,s57,16384
R 82,1,12288
v_fma_f32 v107,v107,v103,v102
M 22,a[60:63],62
L 3
v_fma_f32 v108,v108,v103,v102
v_fma_f32 v109,v109,v103,v102
M 22,a[68:71],66
s_add_u32 s56,s55,20480
R 86,1,13312
v_fma_f32 v110,v110,v103,v102
M 22,a[76:79],70
buffer_load_dwordx4 v[122:125],v0,s[36:39],s74 offen offset:0 sc1
M 18,a[64:67],66
s_cmp_eq_u32 s79,127
R 90,1,14336
v_fma_f32 v111,v111,v103,v102
M 18,a[72:75],70
L 3
buffer_load_dwordx2 v[98:99],v11,s[36:39],s73 offen offset:256 sc1
M 18,a[80:83],74
s_waitcnt vmcnt(10)
R 94,1,15360
v_fma_f32 v112,v112,v103,v102
M 18,a[88:91],78
s_barrier
v_fma_f32 v113,v113,v103,v102
v_add_f32 v26,v26,v106
M 22,a[84:87],74
s_cselect_b32 s94,0xcc00,s94
R 34,1,16384
v_add_f32 v27,v27,v107
M 22,a[92:95],78
L 3
v_add_f32 v28,v28,v108
v_add_f32 v29,v29,v109
M 22,a[100:103],82
s_mov_b32 s66,s65
R 38,1,17408
v_add_f32 v30,v30,v110
M 22,a[108:111],86
s_mov_b32 s76,s75
v_cndmask_b32 v17,v10,v9,s[40:41]
v_add_f32 v31,v31,v111
M 18,a[96:99],82
s_add_u32 s73,s73,s64
R 42,1,18432
v_add_f32 v32,v32,v112
M 18,a[104:107],86
L 3
D 17,56,0
M 18,a[112:115],90
s_add_u32 s56,s55,20736
R 46,1,19456
v_add_f32 v33,v33,v113
M 18,a[120:123],94
s_add_u32 s74,s74,s62
v_cvt_pk_bf16_f32 v114,v26,v27
)"
R"(v_cvt_pk_bf16_f32 v115,v28,v29
M 22,a[116:119],90
s_add_u32 s65,s65,s62
R 50,1,20480
v_fma_f32 v107,v26,v26,s87
M 22,a[124:127],94
L 3
v_add_f32 v106,v26,v27
v_fma_f32 v107,v27,v27,v107
M 22,a[132:135],34
s_add_u32 s75,s75,s63
R 54,1,21504
v_add_f32 v106,v106,v28
M 22,a[140:143],38
D 17,56,1024
M 18,a[128:131],34
s_add_u32 s56,s55,20992
R 58,1,22528
v_fma_f32 v107,v28,v28,v107
M 18,a[136:139],38
L 3
v_add_f32 v106,v106,v29
v_fma_f32 v107,v29,v29,v107
M 18,a[144:147],42
s_sub_u32 s57,s84,s57
R 62,1,23552
v_add_f32 v106,v106,v30
M 18,a[152:155],46
v_fma_f32 v107,v30,v30,v107
v_add_f32 v106,v106,v31
M 22,a[148:151],42
R 66,1,24576
v_fma_f32 v107,v31,v31,v107
M 22,a[156:159],46
L 3
v_add_f32 v106,v106,v32
v_fma_f32 v107,v32,v32,v107
M 22,a[164:167],50
R 70,1,25600
v_add_f32 v106,v106,v33
M 22,a[172:175],54
v_fma_f32 v107,v33,v33,v107
v_cvt_pk_bf16_f32 v116,v30,v31
M 18,a[160:163],50
R 74,1,26624
v_cvt_pk_bf16_f32 v117,v32,v33
M 18,a[168:171],54
L 3
v_permlane16_swap_b32 v106,v107
v_add_f32 v106,v106,v107
v_mov_b32 v107,v106
M 18,a[176:179],58
R 78,1,27648
M 18,a[184:187],62
v_permlane32_swap_b32 v106,v107
v_add_f32 v106,v106,v107
M 22,a[180:183],58
R 82,1,28672
ds_write_b32 v12,v106 offset:2048
M 22,a[188:191],62
L 4
D 17,56,2048
M 22,a[196:199],66
s_add_u32 s56,s55,21248
s_add_u32 s55,s55,s60
R 86,1,29696
M 22,a[204:207],70
D 17,56,3072
M 18,a[192:195],66
R 90,1,30720
M 18,a[200:203],70
L 4
L 2
buffer_store_dwordx4 v[114:117],v0,s[36:39],s67 offen offset:0 sc0 sc1
M 18,a[208:211],74
s_waitcnt vmcnt(7)
R 94,1,31744
M 18,a[216:219],78
s_barrier
ds_read_b32 v7,v6 offset:0
R 118,13,2048
M 22,a[212:215],74
R 34,2,0
M 22,a[220:223],78
L 5
buffer_store_dword v4,v3,s[36:39],s70 offen offset:0 sc0 sc1
M 22,a[228:231],82
s_mov_b32 s70,s69
s_mov_b32 s69,s68
R 38,2,1024
M 22,a[236:239],86
s_add_u32 s68,s68,s63
M 18,a[224:227],82
R 42,2,2048
M 18,a[232:235],86
L 5
L 4
v_cmp_eq_u32 vcc,s53,v7
M 18,a[240:243],90
L 3
R 46,2,3072
v_add_f32 v118,v118,v119
M 18,a[248:251],94
s_or_b64 vcc,vcc,s[94:95]
v_add_f32 v120,v120,v121
v_add_f32 v118,v118,v120
M 22,a[244:247],90
s_cmp_eq_u32 vcc_lo,-1
R 50,2,4096
v_mul_f32 v118,s86,v118
M 22,a[252:255],94
s_waitcnt vmcnt(6) lgkmcnt(3)
buffer_store_dword v118,v14,s[36:39],s77 offen offset:0 sc0 sc1
s_cbranch_scc0 Lg2_K1_%=
Lg2_F1_%=:
v_mfma_f32_16x16x32_bf16 v[30:33],a[4:7],v[34:37],0
s_add_u32 s58,s58,s61
s_add_u32 m0,s59,1024
s_and_b32 s78,s54,127
R 54,2,5120
M 30,a[12:15],38
s_cmp_eq_u32 s78,s85
s_cselect_b64 s[40:41],-1,0
s_add_u32 s56,s55,0
buffer_load_dword v8,s[36:39],s58 offen offset:0 sc1 lds
v_mfma_f32_16x16x32_bf16 v[26:29],a[0:3],v[34:37],0
s_add_u32 m0,s57,0
R 58,2,6144
v_cndmask_b32 v17,v15,v16,s[40:41]
M 26,a[8:11],38
L 3
D 17,56,0
M 26,a[16:19],42
s_add_u32 s56,s55,256
R 62,2,7168
v_lshlrev_b32 v106,16,v122
M 26,a[24:27],46
s_add_u32 s54,s54,s46
v_and_b32 v107,0xffff0000,v122
v_lshlrev_b32 v108,16,v123
M 30,a[20:23],42
s_and_b32 s79,s54,127
R 66,2,8192
v_and_b32 v109,0xffff0000,v123
M 30,a[28:31],46
L 3
D 17,56,1024
M 30,a[36:39],50
s_add_u32 s56,s55,512
R 70,2,9216
v_lshlrev_b32 v110,16,v124
M 30,a[44:47],54
s_cmp_eq_u32 s79,0
v_and_b32 v111,0xffff0000,v124
v_lshlrev_b32 v112,16,v125
M 26,a[32:35],50
s_cselect_b32 s94,0x3300,0
R 74,2,10240
v_and_b32 v113,0xffff0000,v125
M 26,a[40:43],54
L 3
D 17,56,2048
M 26,a[48:51],58
s_add_u32 s56,s55,768
R 78,2,11264
v_fma_f32 v106,v106,v99,v98
M 26,a[56:59],62
D 17,56,3072
M 30,a[52:55],58
s_add_u32 m0,s57,16384
R 82,2,12288
v_fma_f32 v107,v107,v99,v98
M 30,a[60:63],62
L 3
v_fma_f32 v108,v108,v99,v98
v_fma_f32 v109,v109,v99,v98
M 30,a[68:71],66
s_add_u32 s56,s55,20480
R 86,2,13312
v_fma_f32 v110,v110,v99,v98
M 30,a[76:79],70
buffer_load_dwordx4 v[126:129],v0,s[36:39],s74 offen offset:0 sc1
M 26,a[64:67],66
s_cmp_eq_u32 s79,127
R 90,2,14336
v_fma_f32 v111,v111,v99,v98
M 26,a[72:75],70
L 3
buffer_load_dwordx2 v[102:103],v11,s[36:39],s73 offen offset:256 sc1
M 26,a[80:83],74
s_waitcnt vmcnt(10)
R 94,2,15360
v_fma_f32 v112,v112,v99,v98
M 26,a[88:91],78
s_barrier
v_fma_f32 v113,v113,v99,v98
v_add_f32 v18,v18,v106
M 30,a[84:87],74
s_cselect_b32 s94,0xcc00,s94
R 34,2,16384
v_add_f32 v19,v19,v107
M 30,a[92:95],78
L 3
v_add_f32 v20,v20,v108
v_add_f32 v21,v21,v109
M 30,a[100:103],82
s_mov_b32 s67,s65
R 38,2,17408
v_add_f32 v22,v22,v110
M 30,a[108:111],86
s_mov_b32 s77,s75
v_cndmask_b32 v17,v10,v9,s[40:41]
v_add_f32 v23,v23,v111
M 26,a[96:99],82
s_add_u32 s73,s73,s64
R 42,2,18432
v_add_f32 v24,v24,v112
M 26,a[104:107],86
L 3
D 17,56,0
M 26,a[112:115],90
s_add_u32 s56,s55,20736
R 46,2,19456
v_add_f32 v25,v25,v113
M 26,a[120:123],94
s_add_u32 s74,s74,s62
v_cvt_pk_bf16_f32 v114,v18,v19
v_cvt_pk_bf16_f32 v115,v20,v21
M 30,a[116:119],90
s_add_u32 s65,s65,s62
R 50,2,20480
v_fma_f32 v107,v18,v18,s87
M 30,a[124:127],94
L 3
v_add_f32 v106,v18,v19
v_fma_f32 v107,v19,v19,v107
M 30,a[132:135],34
s_add_u32 s75,s75,s63
R 54,2,21504
v_add_f32 v106,v106,v20
M 30,a[140:143],38
D 17,56,1024
M 26,a[128:131],34
s_add_u32 s56,s55,20992
R 58,2,22528
v_fma_f32 v107,v20,v20,v107
M 26,a[136:139],38
L 3
v_add_f32 v106,v106,v21
v_fma_f32 v107,v21,v21,v107
M 26,a[144:147],42
s_sub_u32 s57,s84,s57
R 62,2,23552
v_add_f32 v106,v106,v22
M 26,a[152:155],46
v_fma_f32 v107,v22,v22,v107
v_add_f32 v106,v106,v23
M 30,a[148:151],42
R 66,2,24576
v_fma_f32 v107,v23,v23,v107
M 30,a[156:159],46
L 3
v_add_f32 v106,v106,v24
v_fma_f32 v107,v24,v24,v107
M 30,a[164:167],50
R 70,2,25600
v_add_f32 v106,v106,v25
M 30,a[172:175],54
v_fma_f32 v107,v25,v25,v107
v_cvt_pk_bf16_f32 v116,v22,v23
M 26,a[160:163],50
R 74,2,26624
v_cvt_pk_bf16_f32 v117,v24,v25
M 26,a[168:171],54
L 3
v_permlane16_swap_b32 v106,v107
v_add_f32 v106,v106,v107
v_mov_b32 v107,v106
M 26,a[176:179],58
R 78,2,27648
M 26,a[184:187],62
v_permlane32_swap_b32 v106,v107
v_add_f32 v106,v106,v107
M 30,a[180:183],58
R 82,2,28672
ds_write_b32 v12,v106 offset:0
M 30,a[188:191],62
L 4
D 17,56,2048
M 30,a[196:199],66
s_add_u32 s56,s55,21248
s_add_u32 s55,s55,s60
R 86,2,29696
M 30,a[204:207],70
D 17,56,3072
M 26,a[192:195],66
R 90,2,30720
M 26,a[200:203],70
L 4
L 2
buffer_store_dwordx4 v[114:117],v0,s[36:39],s66 offen offset:0 sc0 sc1
M 26,a[208:211],74
s_waitcnt vmcnt(7)
R 94,2,31744
M 26,a[216:219],78
s_barrier
ds_read_b32 v7,v6 offset:1024
R 118,13,0
M 30,a[212:215],74
R 34,1,0
M 30,a[220:223],78
L 5
buffer_store_dword v4,v3,s[36:39],s70 offen offset:0 sc0 sc1
M 30,a[228:231],82
s_mov_b32 s70,s69
s_mov_b32 s69,s68
R 38,1,1024
M 30,a[236:239],86
s_add_u32 s68,s68,s63
M 26,a[224:227],82
R 42,1,2048
M 26,a[232:235],86
L 5
L 4
v_cmp_eq_u32 vcc,s53,v7
M 26,a[240:243],90
L 3
R 46,1,3072
v_add_f32 v118,v118,v119
M 26,a[248:251],94
s_or_b64 vcc,vcc,s[94:95]
v_add_f32 v120,v120,v121
v_add_f32 v118,v118,v120
M 30,a[244:247],90
s_cmp_eq_u32 vcc_lo,-1
R 50,1,4096
v_mul_f32 v118,s86,v118
M 30,a[252:255],94
s_waitcnt vmcnt(6) lgkmcnt(3)
buffer_store_dword v118,v14,s[36:39],s76 offen offset:0 sc0 sc1
s_cbranch_scc1 Lg2_F0_%=
Lg2_K0_%=:
Lg2_L0_%=:
v_mfma_f32_16x16x32_bf16 v[22:25],a[4:7],v[34:37],0
s_add_u32 m0,s59,3072
R 54,1,5120
v_lshlrev_b32 v106,16,v126
M 22,a[12:15],38
buffer_load_dword v8,s[36:39],s58 offen offset:0 sc1 lds
v_mfma_f32_16x16x32_bf16 v[18:21],a[0:3],v[34:37],0
s_mov_b32 s66,s65
R 58,1,6144
v_and_b32 v107,0xffff0000,v126
M 18,a[8:11],38
L 3
v_lshlrev_b32 v108,16,v127
v_and_b32 v109,0xffff0000,v127
M 18,a[16:19],42
s_mov_b32 s76,s75
R 62,1,7168
v_lshlrev_b32 v110,16,v128
M 18,a[24:27],46
s_add_u32 s65,s65,s62
v_and_b32 v111,0xffff0000,v128
v_lshlrev_b32 v112,16,v129
M 22,a[20:23],42
s_add_u32 s75,s75,s63
R 66,1,8192
v_and_b32 v113,0xffff0000,v129
v_fma_f32 v106,v106,v103,v102
M 22,a[28:31],46
L 3
v_fma_f32 v107,v107,v103,v102
v_fma_f32 v108,v108,v103,v102
M 22,a[36:39],50
R 70,1,9216
v_fma_f32 v109,v109,v103,v102
M 22,a[44:47],54
v_fma_f32 v110,v110,v103,v102
v_fma_f32 v111,v111,v103,v102
M 18,a[32:35],50
R 74,1,10240
v_fma_f32 v112,v112,v103,v102
M 18,a[40:43],54
L 3
v_fma_f32 v113,v113,v103,v102
v_add_f32 v26,v26,v106
M 18,a[48:51],58
R 78,1,11264
v_add_f32 v27,v27,v107
M 18,a[56:59],62
v_add_f32 v28,v28,v108
v_add_f32 v29,v29,v109
v_add_f32 v30,v30,v110
M 22,a[52:55],58
R 82,1,12288
v_add_f32 v31,v31,v111
M 22,a[60:63],62
L 3
v_add_f32 v32,v32,v112
v_add_f32 v33,v33,v113
M 22,a[68:71],66
R 86,1,13312
v_cvt_pk_bf16_f32 v114,v26,v27
M 22,a[76:79],70
buffer_load_dwordx4 v[122:125],v0,s[36:39],s74 offen offset:0 sc1
M 18,a[64:67],66
s_add_u32 s74,s74,s62
R 90,1,14336
v_cvt_pk_bf16_f32 v115,v28,v29
M 18,a[72:75],70
L 3
buffer_load_dwordx2 v[98:99],v11,s[36:39],s73 offen offset:256 sc1
v_cvt_pk_bf16_f32 v116,v30,v31
M 18,a[80:83],74
s_waitcnt vmcnt(2)
s_add_u32 s73,s73,s64
R 94,1,15360
M 18,a[88:91],78
s_barrier
ds_read_b32 v7,v6 offset:3072
v_cvt_pk_bf16_f32 v117,v32,v33
M 22,a[84:87],74
R 34,1,16384
v_add_f32 v106,v26,v27
M 22,a[92:95],78
L 4
buffer_store_dword v4,v3,s[36:39],s70 offen offset:0 sc0 sc1
M 22,a[100:103],82
s_mov_b32 s70,0x7fff0000
R 38,1,17408
v_fma_f32 v107,v26,v26,s87
M 22,a[108:111],86
buffer_store_dwordx4 v[114:117],v0,s[36:39],s67 offen offset:0 sc0 sc1
v_fma_f32 v107,v27,v27,v107
M 18,a[96:99],82
R 42,1,18432
v_add_f32 v106,v106,v28
v_fma_f32 v107,v28,v28,v107
M 18,a[104:107],86
L 4
L 3
v_cmp_eq_u32 vcc,s53,v7
M 18,a[112:115],90
R 46,1,19456
v_add_f32 v106,v106,v29
M 18,a[120:123],94
s_or_b64 vcc,vcc,s[94:95]
v_fma_f32 v107,v29,v29,v107
v_add_f32 v106,v106,v30
M 22,a[116:119],90
R 50,1,20480
v_fma_f32 v107,v30,v30,v107
M 22,a[124:127],94
s_cmp_eq_u32 vcc_lo,-1
L 3
v_add_f32 v106,v106,v31
s_cbranch_scc0 Lg2_X0_%=
M 22,a[132:135],34
s_and_b32 s78,s54,127
s_cmp_eq_u32 s78,s85
s_cselect_b64 s[40:41],-1,0
R 54,1,21504
M 22,a[140:143],38
s_add_u32 m0,s57,0
s_add_u32 s56,s55,0
s_add_u32 s58,s58,s61
v_cndmask_b32 v17,v15,v16,s[40:41]
M 18,a[128:131],34
s_add_u32 s54,s54,s46
R 58,1,22528
v_fma_f32 v107,v31,v31,v107
M 18,a[136:139],38
L 3
D 17,56,0
M 18,a[144:147],42
s_add_u32 s56,s55,256
R 62,1,23552
v_add_f32 v106,v106,v32
M 18,a[152:155],46
s_and_b32 s79,s54,127
v_fma_f32 v107,v32,v32,v107
v_add_f32 v106,v106,v33
M 22,a[148:151],42
s_cmp_eq_u32 s79,0
R 66,1,24576
v_fma_f32 v107,v33,v33,v107
M 22,a[156:159],46
L 3
D 17,56,1024
M 22,a[164:167],50
s_cselect_b32 s94,0x3300,0
s_add_u32 s56,s55,512
R 70,1,25600
M 22,a[172:175],54
s_cmp_eq_u32 s79,127
v_permlane16_swap_b32 v106,v107
v_add_f32 v106,v106,v107
M 18,a[160:163],50
s_cselect_b32 s94,0xcc00,s94
)"
R"(R 74,1,26624
v_mov_b32 v107,v106
M 18,a[168:171],54
L 3
D 17,56,2048
M 18,a[176:179],58
s_add_u32 s56,s55,768
R 78,1,27648
M 18,a[184:187],62
v_permlane32_swap_b32 v106,v107
v_add_f32 v106,v106,v107
M 22,a[180:183],58
R 82,1,28672
ds_write_b32 v12,v106 offset:2048
M 22,a[188:191],62
L 4
D 17,56,3072
M 22,a[196:199],66
s_add_u32 m0,s59,0
s_add_u32 s56,s55,20480
R 86,1,29696
M 22,a[204:207],70
buffer_load_dword v8,s[36:39],s58 offen offset:0 sc1 lds
M 18,a[192:195],66
s_add_u32 m0,s57,16384
R 90,1,30720
v_cndmask_b32 v17,v10,v9,s[40:41]
M 18,a[200:203],70
L 4
L 2
s_sub_u32 s57,s84,s57
M 18,a[208:211],74
s_waitcnt vmcnt(0)
R 94,1,31744
M 18,a[216:219],78
s_barrier
D 17,56,0
M 22,a[212:215],74
s_add_u32 s56,s55,20736
R 34,2,0
ds_read_b32 v7,v6 offset:0
M 22,a[220:223],78
L 4
buffer_store_dword v4,v3,s[36:39],s70 offen offset:0 sc0 sc1
M 22,a[228:231],82
s_mov_b32 s70,s69
R 38,2,1024
R 118,13,2048
M 22,a[236:239],86
D 17,56,1024
M 18,a[224:227],82
s_add_u32 s56,s55,20992
s_mov_b32 s69,s68
R 42,2,2048
M 18,a[232:235],86
L 5
D 17,56,2048
M 18,a[240:243],90
L 3
s_add_u32 s56,s55,21248
R 46,2,3072
v_cmp_eq_u32 vcc,s53,v7
M 18,a[248:251],94
D 17,56,3072
M 22,a[244:247],90
s_add_u32 s55,s55,s60
s_add_u32 s68,s68,s63
L 2
s_or_b64 vcc,vcc,s[94:95]
s_cmp_eq_u32 vcc_lo,-1
R 50,2,4096
v_add_f32 v118,v118,v119
v_add_f32 v120,v120,v121
v_add_f32 v118,v118,v120
v_mul_f32 v118,s86,v118
M 22,a[252:255],94
s_nop 0
buffer_store_dword v118,v14,s[36:39],s77 offen offset:0 sc0 sc1
buffer_store_dword v4,v3,s[36:39],s71 offen offset:0 sc0 sc1
buffer_store_dword v4,v3,s[36:39],s71 offen offset:0 sc0 sc1
s_cbranch_scc1 Lg2_F1_%=
s_branch Lg2_K1_%=
Lg2_X0_%=:
M 22,a[132:135],34
s_add_u32 m0,s59,0
R 54,1,21504
v_fma_f32 v107,v31,v31,v107
M 22,a[140:143],38
buffer_load_dword v8,s[36:39],s58 offen offset:0 sc1 lds
M 18,a[128:131],34
R 58,1,22528
v_add_f32 v106,v106,v32
M 18,a[136:139],38
L 3
v_fma_f32 v107,v32,v32,v107
v_add_f32 v106,v106,v33
M 18,a[144:147],42
R 62,1,23552
v_fma_f32 v107,v33,v33,v107
M 18,a[152:155],46
M 22,a[148:151],42
R 66,1,24576
M 22,a[156:159],46
L 3
v_permlane16_swap_b32 v106,v107
v_add_f32 v106,v106,v107
M 22,a[164:167],50
R 70,1,25600
v_mov_b32 v107,v106
M 22,a[172:175],54
M 18,a[160:163],50
R 74,1,26624
M 18,a[168:171],54
L 3
v_permlane32_swap_b32 v106,v107
M 18,a[176:179],58
R 78,1,27648
v_add_f32 v106,v106,v107
M 18,a[184:187],62
ds_write_b32 v12,v106 offset:2048
M 22,a[180:183],58
R 82,1,28672
M 22,a[188:191],62
L 4
M 22,a[196:199],66
R 86,1,29696
M 22,a[204:207],70
M 18,a[192:195],66
R 90,1,30720
M 18,a[200:203],70
L 4
L 3
M 18,a[208:211],74
s_waitcnt vmcnt(0)
R 94,1,31744
M 18,a[216:219],78
s_barrier
R 118,13,2048
ds_read_b32 v7,v6 offset:0
M 22,a[212:215],74
buffer_store_dword v4,v3,s[36:39],s70 offen offset:0 sc0 sc1
M 22,a[220:223],78
L 4
s_mov_b32 s70,s69
s_mov_b32 s69,s68
M 22,a[228:231],82
s_add_u32 s68,s68,s63
M 22,a[236:239],86
M 18,a[224:227],82
M 18,a[232:235],86
L 2
L 1
L 0
M 18,a[240:243],90
v_add_f32 v118,v118,v119
v_add_f32 v120,v120,v121
v_add_f32 v118,v118,v120
M 18,a[248:251],94
v_cmp_eq_u32 vcc,s53,v7
v_mul_f32 v118,s86,v118
M 22,a[244:247],90
buffer_store_dword v118,v14,s[36:39],s77 offen offset:0 sc0 sc1
M 22,a[252:255],94
s_nop 0
s_or_b64 vcc,vcc,s[94:95]
s_mov_b32 s80,1
s_cmp_eq_u32 vcc_lo,-1
s_cbranch_scc0 Lg2_xn0_%=
s_and_b32 s78,s54,127
s_cmp_eq_u32 s78,s85
s_cselect_b64 s[40:41],-1,0
v_cndmask_b32 v17,v15,v16,s[40:41]
s_add_u32 m0,s57,0
s_add_u32 s56,s55,0
D 17,56,0
v_lshlrev_b32 v106,16,v122
v_and_b32 v107,0xffff0000,v122
v_lshlrev_b32 v108,16,v123
v_and_b32 v109,0xffff0000,v123
v_lshlrev_b32 v110,16,v124
s_add_u32 s56,s55,256
D 17,56,1024
v_and_b32 v111,0xffff0000,v124
v_lshlrev_b32 v112,16,v125
v_and_b32 v113,0xffff0000,v125
v_fma_f32 v106,v106,v99,v98
v_fma_f32 v107,v107,v99,v98
s_add_u32 s56,s55,512
D 17,56,2048
v_fma_f32 v108,v108,v99,v98
v_fma_f32 v109,v109,v99,v98
v_fma_f32 v110,v110,v99,v98
v_fma_f32 v111,v111,v99,v98
v_fma_f32 v112,v112,v99,v98
s_add_u32 s56,s55,768
D 17,56,3072
v_fma_f32 v113,v113,v99,v98
v_add_f32 v18,v18,v106
v_add_f32 v19,v19,v107
v_add_f32 v20,v20,v108
v_add_f32 v21,v21,v109
s_add_u32 s58,s58,s61
v_cndmask_b32 v17,v10,v9,s[40:41]
s_add_u32 m0,s57,16384
s_add_u32 s56,s55,20480
D 17,56,0
v_add_f32 v22,v22,v110
v_add_f32 v23,v23,v111
v_add_f32 v24,v24,v112
v_add_f32 v25,v25,v113
v_cvt_pk_bf16_f32 v114,v18,v19
s_add_u32 s56,s55,20736
D 17,56,1024
v_cvt_pk_bf16_f32 v115,v20,v21
v_cvt_pk_bf16_f32 v116,v22,v23
v_cvt_pk_bf16_f32 v117,v24,v25
s_add_u32 s56,s55,20992
D 17,56,2048
s_add_u32 s56,s55,21248
D 17,56,3072
buffer_store_dwordx4 v[114:117],v0,s[36:39],s66 offen offset:0 sc0 sc1
v_add_f32 v106,v18,v19
v_fma_f32 v107,v18,v18,s87
s_add_u32 s55,s55,s60
s_sub_u32 s57,s84,s57
s_add_u32 s54,s54,s46
v_fma_f32 v107,v19,v19,v107
v_add_f32 v106,v106,v20
v_fma_f32 v107,v20,v20,v107
v_add_f32 v106,v106,v21
v_fma_f32 v107,v21,v21,v107
v_add_f32 v106,v106,v22
v_fma_f32 v107,v22,v22,v107
v_add_f32 v106,v106,v23
v_fma_f32 v107,v23,v23,v107
v_add_f32 v106,v106,v24
v_fma_f32 v107,v24,v24,v107
v_add_f32 v106,v106,v25
v_fma_f32 v107,v25,v25,v107
s_nop 1
v_permlane16_swap_b32 v106,v107
v_add_f32 v106,v106,v107
v_mov_b32 v107,v106
s_nop 1
v_permlane32_swap_b32 v106,v107
v_add_f32 v106,v106,v107
ds_write_b32 v12,v106 offset:0
L 0
s_barrier
R 118,13,0
L 0
v_add_f32 v118,v118,v119
v_add_f32 v120,v120,v121
v_add_f32 v118,v118,v120
v_mul_f32 v118,s86,v118
buffer_store_dword v118,v14,s[36:39],s76 offen offset:0 sc0 sc1
s_mov_b32 s72,s69
s_mov_b32 s69,0x7fff0000
s_mov_b32 s66,0x7fff0000
s_mov_b32 s76,0x7fff0000
s_branch Lg2_gox_%=
Lg2_xn0_%=:
s_cmp_ge_u32 s54,256
s_cbranch_scc1 Lg2_fin0_%=
s_add_u32 m0,s59,4096
s_nop 0
buffer_load_dword v8,s[36:39],s58 offen offset:0 sc1 lds
buffer_store_dword v4,v3,s[36:39],s71 offen offset:0 sc0 sc1
s_add_u32 m0,s59,5120
s_nop 0
buffer_load_dword v8,s[36:39],s58 offen offset:0 sc1 lds
buffer_store_dword v4,v3,s[36:39],s71 offen offset:0 sc0 sc1
s_mov_b32 s83,0
v_lshlrev_b32 v106,16,v122
v_and_b32 v107,0xffff0000,v122
v_lshlrev_b32 v108,16,v123
v_and_b32 v109,0xffff0000,v123
v_lshlrev_b32 v110,16,v124
v_and_b32 v111,0xffff0000,v124
v_lshlrev_b32 v112,16,v125
v_and_b32 v113,0xffff0000,v125
v_fma_f32 v106,v106,v99,v98
v_fma_f32 v107,v107,v99,v98
v_fma_f32 v108,v108,v99,v98
v_fma_f32 v109,v109,v99,v98
v_fma_f32 v110,v110,v99,v98
v_fma_f32 v111,v111,v99,v98
v_fma_f32 v112,v112,v99,v98
v_fma_f32 v113,v113,v99,v98
v_add_f32 v18,v18,v106
v_add_f32 v19,v19,v107
v_add_f32 v20,v20,v108
v_add_f32 v21,v21,v109
v_add_f32 v22,v22,v110
v_add_f32 v23,v23,v111
v_add_f32 v24,v24,v112
v_add_f32 v25,v25,v113
v_cvt_pk_bf16_f32 v114,v18,v19
v_cvt_pk_bf16_f32 v115,v20,v21
v_cvt_pk_bf16_f32 v116,v22,v23
v_cvt_pk_bf16_f32 v117,v24,v25
buffer_store_dwordx4 v[114:117],v0,s[36:39],s66 offen offset:0 sc0 sc1
v_add_f32 v106,v18,v19
v_fma_f32 v107,v18,v18,s87
v_fma_f32 v107,v19,v19,v107
v_add_f32 v106,v106,v20
v_fma_f32 v107,v20,v20,v107
v_add_f32 v106,v106,v21
v_fma_f32 v107,v21,v21,v107
v_add_f32 v106,v106,v22
v_fma_f32 v107,v22,v22,v107
v_add_f32 v106,v106,v23
v_fma_f32 v107,v23,v23,v107
v_add_f32 v106,v106,v24
v_fma_f32 v107,v24,v24,v107
v_add_f32 v106,v106,v25
v_fma_f32 v107,v25,v25,v107
s_nop 1
v_permlane16_swap_b32 v106,v107
v_add_f32 v106,v106,v107
v_mov_b32 v107,v106
s_nop 1
v_permlane32_swap_b32 v106,v107
v_add_f32 v106,v106,v107
ds_write_b32 v12,v106 offset:0
L 0
s_barrier
R 118,13,0
L 0
v_add_f32 v118,v118,v119
v_add_f32 v120,v120,v121
v_add_f32 v118,v118,v120
v_mul_f32 v118,s86,v118
buffer_store_dword v118,v14,s[36:39],s76 offen offset:0 sc0 sc1
s_mov_b32 s72,s69
s_mov_b32 s69,0x7fff0000
s_mov_b32 s66,0x7fff0000
s_mov_b32 s76,0x7fff0000
s_branch Lg2_poll_%=
Lg2_fin0_%=:
v_lshlrev_b32 v106,16,v122
v_and_b32 v107,0xffff0000,v122
v_lshlrev_b32 v108,16,v123
v_and_b32 v109,0xffff0000,v123
v_lshlrev_b32 v110,16,v124
v_and_b32 v111,0xffff0000,v124
v_lshlrev_b32 v112,16,v125
v_and_b32 v113,0xffff0000,v125
v_fma_f32 v106,v106,v99,v98
v_fma_f32 v107,v107,v99,v98
v_fma_f32 v108,v108,v99,v98
v_fma_f32 v109,v109,v99,v98
v_fma_f32 v110,v110,v99,v98
v_fma_f32 v111,v111,v99,v98
v_fma_f32 v112,v112,v99,v98
v_fma_f32 v113,v113,v99,v98
v_add_f32 v18,v18,v106
v_add_f32 v19,v19,v107
v_add_f32 v20,v20,v108
v_add_f32 v21,v21,v109
v_add_f32 v22,v22,v110
v_add_f32 v23,v23,v111
v_add_f32 v24,v24,v112
v_add_f32 v25,v25,v113
v_cvt_pk_bf16_f32 v114,v18,v19
v_cvt_pk_bf16_f32 v115,v20,v21
v_cvt_pk_bf16_f32 v116,v22,v23
v_cvt_pk_bf16_f32 v117,v24,v25
buffer_store_dwordx4 v[114:117],v0,s[36:39],s66 offen offset:0 sc0 sc1
v_add_f32 v106,v18,v19
v_fma_f32 v107,v18,v18,s87
v_fma_f32 v107,v19,v19,v107
v_add_f32 v106,v106,v20
v_fma_f32 v107,v20,v20,v107
v_add_f32 v106,v106,v21
v_fma_f32 v107,v21,v21,v107
v_add_f32 v106,v106,v22
v_fma_f32 v107,v22,v22,v107
v_add_f32 v106,v106,v23
v_fma_f32 v107,v23,v23,v107
v_add_f32 v106,v106,v24
v_fma_f32 v107,v24,v24,v107
v_add_f32 v106,v106,v25
v_fma_f32 v107,v25,v25,v107
s_nop 1
v_permlane16_swap_b32 v106,v107
v_add_f32 v106,v106,v107
v_mov_b32 v107,v106
s_nop 1
v_permlane32_swap_b32 v106,v107
v_add_f32 v106,v106,v107
ds_write_b32 v12,v106 offset:0
L 0
s_barrier
R 118,13,0
L 0
v_add_f32 v118,v118,v119
v_add_f32 v120,v120,v121
v_add_f32 v118,v118,v120
v_mul_f32 v118,s86,v118
buffer_store_dword v118,v14,s[36:39],s76 offen offset:0 sc0 sc1
s_mov_b32 s72,s69
s_mov_b32 s69,0x7fff0000
s_mov_b32 s66,0x7fff0000
s_mov_b32 s76,0x7fff0000
s_branch Lg2_done_%=
Lg2_K1_%=:
Lg2_L1_%=:
v_mfma_f32_16x16x32_bf16 v[30:33],a[4:7],v[34:37],0
s_add_u32 m0,s59,3072
R 54,2,5120
v_lshlrev_b32 v106,16,v122
M 30,a[12:15],38
buffer_load_dword v8,s[36:39],s58 offen offset:0 sc1 lds
v_mfma_f32_16x16x32_bf16 v[26:29],a[0:3],v[34:37],0
s_mov_b32 s67,s65
R 58,2,6144
v_and_b32 v107,0xffff0000,v122
M 26,a[8:11],38
L 3
v_lshlrev_b32 v108,16,v123
v_and_b32 v109,0xffff0000,v123
M 26,a[16:19],42
s_mov_b32 s77,s75
R 62,2,7168
v_lshlrev_b32 v110,16,v124
M 26,a[24:27],46
s_add_u32 s65,s65,s62
v_and_b32 v111,0xffff0000,v124
v_lshlrev_b32 v112,16,v125
M 30,a[20:23],42
s_add_u32 s75,s75,s63
R 66,2,8192
v_and_b32 v113,0xffff0000,v125
v_fma_f32 v106,v106,v99,v98
M 30,a[28:31],46
L 3
v_fma_f32 v107,v107,v99,v98
v_fma_f32 v108,v108,v99,v98
M 30,a[36:39],50
R 70,2,9216
v_fma_f32 v109,v109,v99,v98
M 30,a[44:47],54
v_fma_f32 v110,v110,v99,v98
v_fma_f32 v111,v111,v99,v98
M 26,a[32:35],50
R 74,2,10240
v_fma_f32 v112,v112,v99,v98
M 26,a[40:43],54
L 3
v_fma_f32 v113,v113,v99,v98
v_add_f32 v18,v18,v106
M 26,a[48:51],58
R 78,2,11264
v_add_f32 v19,v19,v107
M 26,a[56:59],62
v_add_f32 v20,v20,v108
v_add_f32 v21,v21,v109
v_add_f32 v22,v22,v110
M 30,a[52:55],58
R 82,2,12288
v_add_f32 v23,v23,v111
M 30,a[60:63],62
L 3
v_add_f32 v24,v24,v112
v_add_f32 v25,v25,v113
M 30,a[68:71],66
R 86,2,13312
v_cvt_pk_bf16_f32 v114,v18,v19
M 30,a[76:79],70
buffer_load_dwordx4 v[126:129],v0,s[36:39],s74 offen offset:0 sc1
M 26,a[64:67],66
s_add_u32 s74,s74,s62
R 90,2,14336
v_cvt_pk_bf16_f32 v115,v20,v21
M 26,a[72:75],70
L 3
buffer_load_dwordx2 v[102:103],v11,s[36:39],s73 offen offset:256 sc1
v_cvt_pk_bf16_f32 v116,v22,v23
M 26,a[80:83],74
s_waitcnt vmcnt(2)
s_add_u32 s73,s73,s64
R 94,2,15360
M 26,a[88:91],78
s_barrier
ds_read_b32 v7,v6 offset:3072
v_cvt_pk_bf16_f32 v117,v24,v25
M 30,a[84:87],74
R 34,2,16384
v_add_f32 v106,v18,v19
M 30,a[92:95],78
L 4
buffer_store_dword v4,v3,s[36:39],s70 offen offset:0 sc0 sc1
M 30,a[100:103],82
s_mov_b32 s70,0x7fff0000
R 38,2,17408
v_fma_f32 v107,v18,v18,s87
M 30,a[108:111],86
buffer_store_dwordx4 v[114:117],v0,s[36:39],s66 offen offset:0 sc0 sc1
v_fma_f32 v107,v19,v19,v107
)"
R"(M 26,a[96:99],82
R 42,2,18432
v_add_f32 v106,v106,v20
v_fma_f32 v107,v20,v20,v107
M 26,a[104:107],86
L 4
L 3
v_cmp_eq_u32 vcc,s53,v7
M 26,a[112:115],90
R 46,2,19456
v_add_f32 v106,v106,v21
M 26,a[120:123],94
s_or_b64 vcc,vcc,s[94:95]
v_fma_f32 v107,v21,v21,v107
v_add_f32 v106,v106,v22
M 30,a[116:119],90
R 50,2,20480
v_fma_f32 v107,v22,v22,v107
M 30,a[124:127],94
s_cmp_eq_u32 vcc_lo,-1
L 3
v_add_f32 v106,v106,v23
s_cbranch_scc0 Lg2_X1_%=
M 30,a[132:135],34
s_and_b32 s78,s54,127
s_cmp_eq_u32 s78,s85
s_cselect_b64 s[40:41],-1,0
R 54,2,21504
M 30,a[140:143],38
s_add_u32 m0,s57,0
s_add_u32 s56,s55,0
s_add_u32 s58,s58,s61
v_cndmask_b32 v17,v15,v16,s[40:41]
M 26,a[128:131],34
s_add_u32 s54,s54,s46
R 58,2,22528
v_fma_f32 v107,v23,v23,v107
M 26,a[136:139],38
L 3
D 17,56,0
M 26,a[144:147],42
s_add_u32 s56,s55,256
R 62,2,23552
v_add_f32 v106,v106,v24
M 26,a[152:155],46
s_and_b32 s79,s54,127
v_fma_f32 v107,v24,v24,v107
v_add_f32 v106,v106,v25
M 30,a[148:151],42
s_cmp_eq_u32 s79,0
R 66,2,24576
v_fma_f32 v107,v25,v25,v107
M 30,a[156:159],46
L 3
D 17,56,1024
M 30,a[164:167],50
s_cselect_b32 s94,0x3300,0
s_add_u32 s56,s55,512
R 70,2,25600
M 30,a[172:175],54
s_cmp_eq_u32 s79,127
v_permlane16_swap_b32 v106,v107
v_add_f32 v106,v106,v107
M 26,a[160:163],50
s_cselect_b32 s94,0xcc00,s94
R 74,2,26624
v_mov_b32 v107,v106
M 26,a[168:171],54
L 3
D 17,56,2048
M 26,a[176:179],58
s_add_u32 s56,s55,768
R 78,2,27648
M 26,a[184:187],62
v_permlane32_swap_b32 v106,v107
v_add_f32 v106,v106,v107
M 30,a[180:183],58
R 82,2,28672
ds_write_b32 v12,v106 offset:0
M 30,a[188:191],62
L 4
D 17,56,3072
M 30,a[196:199],66
s_add_u32 m0,s59,1024
s_add_u32 s56,s55,20480
R 86,2,29696
M 30,a[204:207],70
buffer_load_dword v8,s[36:39],s58 offen offset:0 sc1 lds
M 26,a[192:195],66
s_add_u32 m0,s57,16384
R 90,2,30720
v_cndmask_b32 v17,v10,v9,s[40:41]
M 26,a[200:203],70
L 4
L 2
s_sub_u32 s57,s84,s57
M 26,a[208:211],74
s_waitcnt vmcnt(0)
R 94,2,31744
M 26,a[216:219],78
s_barrier
D 17,56,0
M 30,a[212:215],74
s_add_u32 s56,s55,20736
R 34,1,0
ds_read_b32 v7,v6 offset:1024
M 30,a[220:223],78
L 4
buffer_store_dword v4,v3,s[36:39],s70 offen offset:0 sc0 sc1
M 30,a[228:231],82
s_mov_b32 s70,s69
R 38,1,1024
R 118,13,0
M 30,a[236:239],86
D 17,56,1024
M 26,a[224:227],82
s_add_u32 s56,s55,20992
s_mov_b32 s69,s68
R 42,1,2048
M 26,a[232:235],86
L 5
D 17,56,2048
M 26,a[240:243],90
L 3
s_add_u32 s56,s55,21248
R 46,1,3072
v_cmp_eq_u32 vcc,s53,v7
M 26,a[248:251],94
D 17,56,3072
M 30,a[244:247],90
s_add_u32 s55,s55,s60
s_add_u32 s68,s68,s63
L 2
s_or_b64 vcc,vcc,s[94:95]
s_cmp_eq_u32 vcc_lo,-1
R 50,1,4096
v_add_f32 v118,v118,v119
v_add_f32 v120,v120,v121
v_add_f32 v118,v118,v120
v_mul_f32 v118,s86,v118
M 30,a[252:255],94
s_nop 0
buffer_store_dword v118,v14,s[36:39],s76 offen offset:0 sc0 sc1
buffer_store_dword v4,v3,s[36:39],s71 offen offset:0 sc0 sc1
buffer_store_dword v4,v3,s[36:39],s71 offen offset:0 sc0 sc1
s_cbranch_scc1 Lg2_F0_%=
s_branch Lg2_K0_%=
Lg2_X1_%=:
M 30,a[132:135],34
s_add_u32 m0,s59,1024
R 54,2,21504
v_fma_f32 v107,v23,v23,v107
M 30,a[140:143],38
buffer_load_dword v8,s[36:39],s58 offen offset:0 sc1 lds
M 26,a[128:131],34
R 58,2,22528
v_add_f32 v106,v106,v24
M 26,a[136:139],38
L 3
v_fma_f32 v107,v24,v24,v107
v_add_f32 v106,v106,v25
M 26,a[144:147],42
R 62,2,23552
v_fma_f32 v107,v25,v25,v107
M 26,a[152:155],46
M 30,a[148:151],42
R 66,2,24576
M 30,a[156:159],46
L 3
v_permlane16_swap_b32 v106,v107
v_add_f32 v106,v106,v107
M 30,a[164:167],50
R 70,2,25600
v_mov_b32 v107,v106
M 30,a[172:175],54
M 26,a[160:163],50
R 74,2,26624
M 26,a[168:171],54
L 3
v_permlane32_swap_b32 v106,v107
M 26,a[176:179],58
R 78,2,27648
v_add_f32 v106,v106,v107
M 26,a[184:187],62
ds_write_b32 v12,v106 offset:0
M 30,a[180:183],58
R 82,2,28672
M 30,a[188:191],62
L 4
M 30,a[196:199],66
R 86,2,29696
M 30,a[204:207],70
M 26,a[192:195],66
R 90,2,30720
M 26,a[200:203],70
L 4
L 3
M 26,a[208:211],74
s_waitcnt vmcnt(0)
R 94,2,31744
M 26,a[216:219],78
s_barrier
R 118,13,0
ds_read_b32 v7,v6 offset:1024
M 30,a[212:215],74
buffer_store_dword v4,v3,s[36:39],s70 offen offset:0 sc0 sc1
M 30,a[220:223],78
L 4
s_mov_b32 s70,s69
s_mov_b32 s69,s68
M 30,a[228:231],82
s_add_u32 s68,s68,s63
M 30,a[236:239],86
M 26,a[224:227],82
M 26,a[232:235],86
L 2
L 1
L 0
M 26,a[240:243],90
v_add_f32 v118,v118,v119
v_add_f32 v120,v120,v121
v_add_f32 v118,v118,v120
M 26,a[248:251],94
v_cmp_eq_u32 vcc,s53,v7
v_mul_f32 v118,s86,v118
M 30,a[244:247],90
buffer_store_dword v118,v14,s[36:39],s76 offen offset:0 sc0 sc1
M 30,a[252:255],94
s_nop 0
s_or_b64 vcc,vcc,s[94:95]
s_mov_b32 s80,0
s_cmp_eq_u32 vcc_lo,-1
s_cbranch_scc0 Lg2_xn1_%=
s_and_b32 s78,s54,127
s_cmp_eq_u32 s78,s85
s_cselect_b64 s[40:41],-1,0
v_cndmask_b32 v17,v15,v16,s[40:41]
s_add_u32 m0,s57,0
s_add_u32 s56,s55,0
D 17,56,0
v_lshlrev_b32 v106,16,v126
v_and_b32 v107,0xffff0000,v126
v_lshlrev_b32 v108,16,v127
v_and_b32 v109,0xffff0000,v127
v_lshlrev_b32 v110,16,v128
s_add_u32 s56,s55,256
D 17,56,1024
v_and_b32 v111,0xffff0000,v128
v_lshlrev_b32 v112,16,v129
v_and_b32 v113,0xffff0000,v129
v_fma_f32 v106,v106,v103,v102
v_fma_f32 v107,v107,v103,v102
s_add_u32 s56,s55,512
D 17,56,2048
v_fma_f32 v108,v108,v103,v102
v_fma_f32 v109,v109,v103,v102
v_fma_f32 v110,v110,v103,v102
v_fma_f32 v111,v111,v103,v102
v_fma_f32 v112,v112,v103,v102
s_add_u32 s56,s55,768
D 17,56,3072
v_fma_f32 v113,v113,v103,v102
v_add_f32 v26,v26,v106
v_add_f32 v27,v27,v107
v_add_f32 v28,v28,v108
v_add_f32 v29,v29,v109
s_add_u32 s58,s58,s61
v_cndmask_b32 v17,v10,v9,s[40:41]
s_add_u32 m0,s57,16384
s_add_u32 s56,s55,20480
D 17,56,0
v_add_f32 v30,v30,v110
v_add_f32 v31,v31,v111
v_add_f32 v32,v32,v112
v_add_f32 v33,v33,v113
v_cvt_pk_bf16_f32 v114,v26,v27
s_add_u32 s56,s55,20736
D 17,56,1024
v_cvt_pk_bf16_f32 v115,v28,v29
v_cvt_pk_bf16_f32 v116,v30,v31
v_cvt_pk_bf16_f32 v117,v32,v33
s_add_u32 s56,s55,20992
D 17,56,2048
s_add_u32 s56,s55,21248
D 17,56,3072
buffer_store_dwordx4 v[114:117],v0,s[36:39],s67 offen offset:0 sc0 sc1
v_add_f32 v106,v26,v27
v_fma_f32 v107,v26,v26,s87
s_add_u32 s55,s55,s60
s_sub_u32 s57,s84,s57
s_add_u32 s54,s54,s46
v_fma_f32 v107,v27,v27,v107
v_add_f32 v106,v106,v28
v_fma_f32 v107,v28,v28,v107
v_add_f32 v106,v106,v29
v_fma_f32 v107,v29,v29,v107
v_add_f32 v106,v106,v30
v_fma_f32 v107,v30,v30,v107
v_add_f32 v106,v106,v31
v_fma_f32 v107,v31,v31,v107
v_add_f32 v106,v106,v32
v_fma_f32 v107,v32,v32,v107
v_add_f32 v106,v106,v33
v_fma_f32 v107,v33,v33,v107
s_nop 1
v_permlane16_swap_b32 v106,v107
v_add_f32 v106,v106,v107
v_mov_b32 v107,v106
s_nop 1
v_permlane32_swap_b32 v106,v107
v_add_f32 v106,v106,v107
ds_write_b32 v12,v106 offset:2048
L 0
s_barrier
R 118,13,2048
L 0
v_add_f32 v118,v118,v119
v_add_f32 v120,v120,v121
v_add_f32 v118,v118,v120
v_mul_f32 v118,s86,v118
buffer_store_dword v118,v14,s[36:39],s77 offen offset:0 sc0 sc1
s_mov_b32 s72,s69
s_mov_b32 s69,0x7fff0000
s_mov_b32 s67,0x7fff0000
s_mov_b32 s77,0x7fff0000
s_branch Lg2_gox_%=
Lg2_xn1_%=:
s_cmp_ge_u32 s54,256
s_cbranch_scc1 Lg2_fin1_%=
s_add_u32 m0,s59,4096
s_nop 0
buffer_load_dword v8,s[36:39],s58 offen offset:0 sc1 lds
buffer_store_dword v4,v3,s[36:39],s71 offen offset:0 sc0 sc1
s_add_u32 m0,s59,5120
s_nop 0
buffer_load_dword v8,s[36:39],s58 offen offset:0 sc1 lds
buffer_store_dword v4,v3,s[36:39],s71 offen offset:0 sc0 sc1
s_mov_b32 s83,0
v_lshlrev_b32 v106,16,v126
v_and_b32 v107,0xffff0000,v126
v_lshlrev_b32 v108,16,v127
v_and_b32 v109,0xffff0000,v127
v_lshlrev_b32 v110,16,v128
v_and_b32 v111,0xffff0000,v128
v_lshlrev_b32 v112,16,v129
v_and_b32 v113,0xffff0000,v129
v_fma_f32 v106,v106,v103,v102
v_fma_f32 v107,v107,v103,v102
v_fma_f32 v108,v108,v103,v102
v_fma_f32 v109,v109,v103,v102
v_fma_f32 v110,v110,v103,v102
v_fma_f32 v111,v111,v103,v102
v_fma_f32 v112,v112,v103,v102
v_fma_f32 v113,v113,v103,v102
v_add_f32 v26,v26,v106
v_add_f32 v27,v27,v107
v_add_f32 v28,v28,v108
v_add_f32 v29,v29,v109
v_add_f32 v30,v30,v110
v_add_f32 v31,v31,v111
v_add_f32 v32,v32,v112
v_add_f32 v33,v33,v113
v_cvt_pk_bf16_f32 v114,v26,v27
v_cvt_pk_bf16_f32 v115,v28,v29
v_cvt_pk_bf16_f32 v116,v30,v31
v_cvt_pk_bf16_f32 v117,v32,v33
buffer_store_dwordx4 v[114:117],v0,s[36:39],s67 offen offset:0 sc0 sc1
v_add_f32 v106,v26,v27
v_fma_f32 v107,v26,v26,s87
v_fma_f32 v107,v27,v27,v107
v_add_f32 v106,v106,v28
v_fma_f32 v107,v28,v28,v107
v_add_f32 v106,v106,v29
v_fma_f32 v107,v29,v29,v107
v_add_f32 v106,v106,v30
v_fma_f32 v107,v30,v30,v107
v_add_f32 v106,v106,v31
v_fma_f32 v107,v31,v31,v107
v_add_f32 v106,v106,v32
v_fma_f32 v107,v32,v32,v107
v_add_f32 v106,v106,v33
v_fma_f32 v107,v33,v33,v107
s_nop 1
v_permlane16_swap_b32 v106,v107
v_add_f32 v106,v106,v107
v_mov_b32 v107,v106
s_nop 1
v_permlane32_swap_b32 v106,v107
v_add_f32 v106,v106,v107
ds_write_b32 v12,v106 offset:2048
L 0
s_barrier
R 118,13,2048
L 0
v_add_f32 v118,v118,v119
v_add_f32 v120,v120,v121
v_add_f32 v118,v118,v120
v_mul_f32 v118,s86,v118
buffer_store_dword v118,v14,s[36:39],s77 offen offset:0 sc0 sc1
s_mov_b32 s72,s69
s_mov_b32 s69,0x7fff0000
s_mov_b32 s67,0x7fff0000
s_mov_b32 s77,0x7fff0000
s_branch Lg2_poll_%=
Lg2_fin1_%=:
v_lshlrev_b32 v106,16,v126
v_and_b32 v107,0xffff0000,v126
v_lshlrev_b32 v108,16,v127
v_and_b32 v109,0xffff0000,v127
v_lshlrev_b32 v110,16,v128
v_and_b32 v111,0xffff0000,v128
v_lshlrev_b32 v112,16,v129
v_and_b32 v113,0xffff0000,v129
v_fma_f32 v106,v106,v103,v102
v_fma_f32 v107,v107,v103,v102
v_fma_f32 v108,v108,v103,v102
v_fma_f32 v109,v109,v103,v102
v_fma_f32 v110,v110,v103,v102
v_fma_f32 v111,v111,v103,v102
v_fma_f32 v112,v112,v103,v102
v_fma_f32 v113,v113,v103,v102
v_add_f32 v26,v26,v106
v_add_f32 v27,v27,v107
v_add_f32 v28,v28,v108
v_add_f32 v29,v29,v109
v_add_f32 v30,v30,v110
v_add_f32 v31,v31,v111
v_add_f32 v32,v32,v112
v_add_f32 v33,v33,v113
v_cvt_pk_bf16_f32 v114,v26,v27
v_cvt_pk_bf16_f32 v115,v28,v29
v_cvt_pk_bf16_f32 v116,v30,v31
v_cvt_pk_bf16_f32 v117,v32,v33
buffer_store_dwordx4 v[114:117],v0,s[36:39],s67 offen offset:0 sc0 sc1
v_add_f32 v106,v26,v27
v_fma_f32 v107,v26,v26,s87
v_fma_f32 v107,v27,v27,v107
v_add_f32 v106,v106,v28
v_fma_f32 v107,v28,v28,v107
v_add_f32 v106,v106,v29
v_fma_f32 v107,v29,v29,v107
v_add_f32 v106,v106,v30
v_fma_f32 v107,v30,v30,v107
v_add_f32 v106,v106,v31
v_fma_f32 v107,v31,v31,v107
v_add_f32 v106,v106,v32
v_fma_f32 v107,v32,v32,v107
v_add_f32 v106,v106,v33
v_fma_f32 v107,v33,v33,v107
s_nop 1
v_permlane16_swap_b32 v106,v107
v_add_f32 v106,v106,v107
v_mov_b32 v107,v106
s_nop 1
v_permlane32_swap_b32 v106,v107
v_add_f32 v106,v106,v107
ds_write_b32 v12,v106 offset:2048
L 0
s_barrier
R 118,13,2048
L 0
v_add_f32 v118,v118,v119
v_add_f32 v120,v120,v121
v_add_f32 v118,v118,v120
v_mul_f32 v118,s86,v118
buffer_store_dword v118,v14,s[36:39],s77 offen offset:0 sc0 sc1
s_mov_b32 s72,s69
s_mov_b32 s69,0x7fff0000
s_mov_b32 s67,0x7fff0000
s_mov_b32 s77,0x7fff0000
s_branch Lg2_done_%=
Lg2_done_%=:
s_waitcnt vmcnt(2)
s_barrier
buffer_store_dword v4,v3,s[36:39],s70 offen offset:0 sc0 sc1
s_waitcnt vmcnt(1)
s_barrier
buffer_store_dword v4,v3,s[36:39],s72 offen offset:0 sc0 sc1
s_waitcnt vmcnt(0) lgkmcnt(0)
s_barrier
Lg2_end_%=:
s_mov_b32 %0,s53
Lcode_g12_e:
.purgem M
.purgem R
.purgem T
.purgem D
.purgem L
)"
  : "=&s"(epOut) : "s"(ws), "s"(__builtin_amdgcn_readfirstlane(sWave)), "s"(__builtin_amdgcn_readfirstlane(sG0)), "s"(__builtin_amdgcn_readfirstlane(sStep)), "s"(__builtin_amdgcn_readfirstlane(sOutOff)), "s"(__builtin_amdgcn_readfirstlane(sFoutOff)), "s"(__builtin_amdgcn_readfirstlane(sAuxOff)), "s"(__builtin_amdgcn_readfirstlane(sWw)), "s"(__builtin_amdgcn_readfirstlane(sBuf)), "s"(__builtin_amdgcn_readfirstlane(sExp)), "v"(vL16), "v"(vFlag), "v"(vSnapOff), "v"(vSnapRd), "v"(vWd0), "v"(vWd1), "v"(vWr0), "v"(vWr1), "v"(vSp), "v"(vOutS), "v"(vAux0), "v"(vAux1), "v"(vAux2), "v"(vAux3), "v"(vAn), "v"(vAe), "s"(w12) : "v0", "v1", "v2", "v3", "v4", "v5", "v6", "v7", "v8", "v9", "v10", "v11", "v12", "v13", "v14", "v15", "v16", "v17", "v18", "v19", "v20", "v21", "v22", "v23", "v24", "v25", "v26", "v27", "v28", "v29", "v30", "v31", "v32", "v33", "v34", "v35", "v36", "v37", "v38", "v39", "v40", "v41", "v42", "v43", "v44", "v45", "v46", "v47", "v48", "v49", "v50", "v51", "v52", "v53", "v54", "v55", "v56", "v57", "v58", "v59", "v60", "v61", "v62", "v63", "v64", "v65", "v66", "v67", "v68", "v69", "v70", "v71", "v72", "v73", "v74", "v75", "v76", "v77", "v78", "v79", "v80", "v81", "v82", "v83", "v84", "v85", "v86", "v87", "v88", "v89", "v90", "v91", "v92", "v93", "v94", "v95", "v96", "v97", "v98", "v99", "v100", "v101", "v102", "v103", "v104", "v105", "v106", "v107", "v108", "v109", "v110", "v111", "v112", "v113", "v114", "v115", "v116", "v117", "v118", "v119", "v120", "v121", "v122", "v123", "v124", "v125", "v126", "v127", "v128", "v129", "v130", "v131", "v132", "v133", "v134", "v135", "v136", "v137", "v138", "v139", "v140", "v141", "v142", "v143", "v144", "v145", "v146", "v147", "v148", "v149", "v150", "v151", "v152", "v153", "v154", "v155", "v156", "v157", "v158", "v159", "v160", "v161", "v162", "v163", "v164", "v165", "v166", "v167", "v168", "v169", "v170", "v171", "v172", "v173", "v174", "v175", "v176", "v177", "v178", "v179", "v180", "v181", "v182", "v183", "v184", "v185", "v186", "v187", "v188", "v189", "v190", "v191", "v192", "v193", "v194", "v195", "v196", "v197", "v198", "v199", "v200", "v201", "v202", "v203", "v204", "v205", "v206", "v207", "v208", "v209", "v210", "v211", "v212", "v213", "v214", "v215", "v216", "v217", "v218", "v219", "v220", "v221", "v222", "v223", "v224", "v225", "v226", "v227", "v228", "v229", "v230", "v231", "a0", "a1", "a2", "a3", "a4", "a5", "a6", "a7", "a8", "a9", "a10", "a11", "a12", "a13", "a14", "a15", "a16", "a17", "a18", "a19", "a20", "a21", "a22", "a23", "a24", "a25", "a26", "a27", "a28", "a29", "a30", "a31", "a32", "a33", "a34", "a35", "a36", "a37", "a38", "a39", "a40", "a41", "a42", "a43", "a44", "a45", "a46", "a47", "a48", "a49", "a50", "a51", "a52", "a53", "a54", "a55", "a56", "a57", "a58", "a59", "a60", "a61", "a62", "a63", "a64", "a65", "a66", "a67", "a68", "a69", "a70", "a71", "a72", "a73", "a74", "a75", "a76", "a77", "a78", "a79", "a80", "a81", "a82", "a83", "a84", "a85", "a86", "a87", "a88", "a89", "a90", "a91", "a92", "a93", "a94", "a95", "a96", "a97", "a98", "a99", "a100", "a101", "a102", "a103", "a104", "a105", "a106", "a107", "a108", "a109", "a110", "a111", "a112", "a113", "a114", "a115", "a116", "a117", "a118", "a119", "a120", "a121", "a122", "a123", "a124", "a125", "a126", "a127", "a128", "a129", "a130", "a131", "a132", "a133", "a134", "a135", "a136", "a137", "a138", "a139", "a140", "a141", "a142", "a143", "a144", "a145", "a146", "a147", "a148", "a149", "a150", "a151", "a152", "a153", "a154", "a155", "a156", "a157", "a158", "a159", "a160", "a161", "a162", "a163", "a164", "a165", "a166", "a167", "a168", "a169", "a170", "a171", "a172", "a173", "a174", "a175", "a176", "a177", "a178", "a179", "a180", "a181", "a182", "a183", "a184", "a185", "a186", "a187", "a188", "a189", "a190", "a191", "a192", "a193", "a194", "a195", "a196", "a197", "a198", "a199", "a200", "a201", "a202", "a203", "a204", "a205", "a206", "a207", "a208", "a209", "a210", "a211", "a212", "a213", "a214", "a215", "a216", "a217", "a218", "a219", "a220", "a221", "a222", "a223", "a224", "a225", "a226", "a227", "a228", "a229", "a230", "a231", "a232", "a233", "a234", "a235", "a236", "a237", "a238", "a239", "a240", "a241", "a242", "a243", "a244", "a245", "a246", "a247", "a248", "a249", "a250", "a251", "a252", "a253", "a254", "a255", "s36", "s37", "s38", "s39", "s40", "s41", "s42", "s43", "s44", "s45", "s46", "s47", "s48", "s49", "s50", "s51", "s52", "s53", "s54", "s55", "s56", "s57", "s58", "s59", "s60", "s61", "s62", "s63", "s64", "s65", "s66", "s67", "s68", "s69", "s70", "s71", "s72", "s73", "s74", "s75", "s76", "s77", "s78", "s79", "s80", "s81", "s82", "s83", "s84", "s85", "s86", "s87", "s88", "s89", "s90", "s91", "s92", "s93", "s94", "s95", "s96", "s97", "vcc", "scc", "memory");
    ep = epOut;
    if (kind == 1 && 5 + RHELP + (rep >> 1) < NR4) { kind = 4; kh = rep & 1; rep = 5 + RHELP + (rep >> 1); }
  }
  if (kind == 4) {
    const unsigned sStep = rep < 5 ? 5 : rep == 5 ? 6 : NR4;
    COMMON(10, (kh * 9 + fj) * 128, 2560, 143360, 2048, 448, 0);
    const int cs = slice;
    const unsigned sOutOff = (kh ? OFF_P1 : OFF_P0) + (cs * 4 + wave) * 1024, sFoutOff = OFF_FP + (kh * 8 + cs) * 128;
    const unsigned sAuxOff = (kh ? OFF_XP : OFF_X1) + (cs * 4 + wave) * 1024, sWw = (kh * 1792 + 16 * wave) * 2048 + cs * 256, sBuf = kh * 57344;
    const int zu = 108 + (rep + 2) % 5;
    const unsigned vSp = lg * 2048 + l15 * 4, vSn = rep >= 5 ? OOB : !ALLZ ? (zu * 16 + wave * 4 + lg) * 2048 + (cs * 2 + kh) * 128 + l15 * 8 :
                         ((rep + 5 * (wave >> 1)) * 16 + (wave & 1) * 8 + (ln >> 3)) * 2048 + (cs * 2 + kh) * 128 + (ln & 7) * 16;
    (void)vWr2;
    const unsigned sExp = __builtin_amdgcn_readfirstlane(ep);
    asm volatile(
R"(.macro M d,w,x
v_mfma_f32_16x16x32_bf16 v[\d:\d+3], \w, v[\x:\x+3], v[\d:\d+3]
.endm
.macro R d,a,o
ds_read_b128 v[\d:\d+3], v\a offset:\o
.endm
.macro T d,a,o
ds_read_b64_tr_b16 \d, v\a offset:\o
.endm
.macro D a,s,o
buffer_load_dwordx4 v\a, s[36:39], s\s offen offset:\o sc1 lds
.endm
.macro L c
s_waitcnt lgkmcnt(\c)
.endm
Lcode_g4_b:
s_mov_b64 s[36:37],%0
s_mov_b32 s38,111243392
s_mov_b32 s39,0x00020000
s_mov_b32 s44,%1
s_mov_b32 s45,%2
s_mov_b32 s46,%3
s_mov_b32 s47,%4
s_mov_b32 s48,%5
s_mov_b32 s49,%6
s_mov_b32 s50,%7
s_mov_b32 s51,%8
s_mov_b32 s93,%9
s_mov_b32 s53,%10
v_mov_b32 v0,%11
v_mov_b32 v3,%12
v_mov_b32 v5,%13
v_mov_b32 v6,%14
v_mov_b32 v10,%15
v_mov_b32 v11,%16
v_mov_b32 v12,%17
v_mov_b32 v13,%18
v_mov_b32 v9,%19
v_mov_b32 v7,%20
s_mov_b64 s[40:41],%21
s_mov_b32 s42,7340032
s_mov_b32 s43,0x00020000
s_lshl_b32 s57,s44,11
s_cmp_eq_u32 s45,175
s_cbranch_scc1 Lg4_pre_%=
s_add_u32 s55,s50,0
s_add_u32 m0,s57,40960
s_nop 0
buffer_load_dwordx4 v10,s[40:43],s55 offen offset:0  lds
s_add_u32 m0,s57,41984
s_nop 0
buffer_load_dwordx4 v11,s[40:43],s55 offen offset:0  lds
s_add_u32 m0,s57,49024
s_nop 0
buffer_load_dwordx4 v10,s[40:43],s55 offen offset:128  lds
s_add_u32 m0,s57,50048
s_nop 0
buffer_load_dwordx4 v11,s[40:43],s55 offen offset:128  lds
s_add_u32 s55,s50,131072
s_add_u32 m0,s57,57344
s_nop 0
buffer_load_dwordx4 v10,s[40:43],s55 offen offset:0  lds
s_add_u32 m0,s57,58368
s_nop 0
buffer_load_dwordx4 v11,s[40:43],s55 offen offset:0  lds
s_add_u32 m0,s57,65408
s_nop 0
buffer_load_dwordx4 v10,s[40:43],s55 offen offset:128  lds
s_add_u32 m0,s57,66432
s_nop 0
buffer_load_dwordx4 v11,s[40:43],s55 offen offset:128  lds
s_add_u32 s55,s50,262144
s_add_u32 m0,s57,73728
s_nop 0
buffer_load_dwordx4 v10,s[40:43],s55 offen offset:0  lds
s_add_u32 m0,s57,74752
s_nop 0
buffer_load_dwordx4 v11,s[40:43],s55 offen offset:0  lds
s_add_u32 m0,s57,81792
s_nop 0
buffer_load_dwordx4 v10,s[40:43],s55 offen offset:128  lds
s_add_u32 m0,s57,82816
s_nop 0
buffer_load_dwordx4 v11,s[40:43],s55 offen offset:128  lds
s_add_u32 s55,s50,393216
s_add_u32 m0,s57,90112
s_nop 0
buffer_load_dwordx4 v10,s[40:43],s55 offen offset:0  lds
s_add_u32 m0,s57,91136
s_nop 0
buffer_load_dwordx4 v11,s[40:43],s55 offen offset:0  lds
s_add_u32 m0,s57,98176
s_nop 0
buffer_load_dwordx4 v10,s[40:43],s55 offen offset:128  lds
s_add_u32 m0,s57,99200
s_nop 0
buffer_load_dwordx4 v11,s[40:43],s55 offen offset:128  lds
s_add_u32 s55,s50,524288
s_add_u32 m0,s57,106496
s_nop 0
buffer_load_dwordx4 v10,s[40:43],s55 offen offset:0  lds
s_add_u32 m0,s57,107520
s_nop 0
buffer_load_dwordx4 v11,s[40:43],s55 offen offset:0  lds
s_add_u32 m0,s57,114560
s_nop 0
buffer_load_dwordx4 v10,s[40:43],s55 offen offset:128  lds
s_add_u32 m0,s57,115584
s_nop 0
buffer_load_dwordx4 v11,s[40:43],s55 offen offset:128  lds
s_add_u32 s55,s50,655360
s_add_u32 m0,s57,122880
s_nop 0
buffer_load_dwordx4 v10,s[40:43],s55 offen offset:0  lds
s_add_u32 m0,s57,123904
s_nop 0
buffer_load_dwordx4 v11,s[40:43],s55 offen offset:0  lds
s_add_u32 m0,s57,130944
s_nop 0
buffer_load_dwordx4 v10,s[40:43],s55 offen offset:128  lds
s_add_u32 m0,s57,131968
s_nop 0
buffer_load_dwordx4 v11,s[40:43],s55 offen offset:128  lds
s_waitcnt vmcnt(20) lgkmcnt(0)
s_barrier
s_add_u32 s55,s50,786432
s_add_u32 m0,s57,139264
s_nop 0
buffer_load_dwordx4 v10,s[40:43],s55 offen offset:0  lds
s_add_u32 m0,s57,140288
s_nop 0
buffer_load_dwordx4 v11,s[40:43],s55 offen offset:0  lds
s_add_u32 m0,s57,147328
s_nop 0
buffer_load_dwordx4 v10,s[40:43],s55 offen offset:128  lds
s_add_u32 m0,s57,148352
s_nop 0
buffer_load_dwordx4 v11,s[40:43],s55 offen offset:128  lds
v_add_u32 v18,40960,v12
v_add_u32 v19,40960,v13
T a[0:1],18,0
T a[2:3],18,512
T a[4:5],19,0
T a[6:7],19,512
T a[8:9],18,4096
T a[10:11],18,4608
T a[12:13],19,4096
T a[14:15],19,4608
s_waitcnt vmcnt(20) lgkmcnt(0)
s_barrier
s_add_u32 s55,s50,917504
s_add_u32 m0,s57,40960
s_nop 0
buffer_load_dwordx4 v10,s[40:43],s55 offen offset:0  lds
s_add_u32 m0,s57,41984
s_nop 0
buffer_load_dwordx4 v11,s[40:43],s55 offen offset:0  lds
s_add_u32 m0,s57,49024
s_nop 0
buffer_load_dwordx4 v10,s[40:43],s55 offen offset:128  lds
s_add_u32 m0,s57,50048
s_nop 0
buffer_load_dwordx4 v11,s[40:43],s55 offen offset:128  lds
v_add_u32 v18,57344,v12
v_add_u32 v19,57344,v13
T a[16:17],18,0
T a[18:19],18,512
T a[20:21],19,0
T a[22:23],19,512
T a[24:25],18,4096
T a[26:27],18,4608
T a[28:29],19,4096
T a[30:31],19,4608
s_waitcnt vmcnt(20) lgkmcnt(0)
s_barrier
s_add_u32 s55,s50,1048576
s_add_u32 m0,s57,57344
s_nop 0
buffer_load_dwordx4 v10,s[40:43],s55 offen offset:0  lds
s_add_u32 m0,s57,58368
s_nop 0
buffer_load_dwordx4 v11,s[40:43],s55 offen offset:0  lds
s_add_u32 m0,s57,65408
s_nop 0
buffer_load_dwordx4 v10,s[40:43],s55 offen offset:128  lds
s_add_u32 m0,s57,66432
s_nop 0
buffer_load_dwordx4 v11,s[40:43],s55 offen offset:128  lds
v_add_u32 v18,73728,v12
v_add_u32 v19,73728,v13
T a[32:33],18,0
T a[34:35],18,512
T a[36:37],19,0
T a[38:39],19,512
T a[40:41],18,4096
T a[42:43],18,4608
T a[44:45],19,4096
T a[46:47],19,4608
s_waitcnt vmcnt(20) lgkmcnt(0)
s_barrier
s_add_u32 s55,s50,1179648
s_add_u32 m0,s57,73728
s_nop 0
buffer_load_dwordx4 v10,s[40:43],s55 offen offset:0  lds
s_add_u32 m0,s57,74752
s_nop 0
buffer_load_dwordx4 v11,s[40:43],s55 offen offset:0  lds
s_add_u32 m0,s57,81792
s_nop 0
buffer_load_dwordx4 v10,s[40:43],s55 offen offset:128  lds
s_add_u32 m0,s57,82816
s_nop 0
buffer_load_dwordx4 v11,s[40:43],s55 offen offset:128  lds
v_add_u32 v18,90112,v12
v_add_u32 v19,90112,v13
T a[48:49],18,0
T a[50:51],18,512
T a[52:53],19,0
T a[54:55],19,512
T a[56:57],18,4096
T a[58:59],18,4608
T a[60:61],19,4096
T a[62:63],19,4608
s_waitcnt vmcnt(20) lgkmcnt(0)
s_barrier
s_add_u32 s55,s50,1310720
s_add_u32 m0,s57,90112
s_nop 0
buffer_load_dwordx4 v10,s[40:43],s55 offen offset:0  lds
s_add_u32 m0,s57,91136
s_nop 0
buffer_load_dwordx4 v11,s[40:43],s55 offen offset:0  lds
s_add_u32 m0,s57,98176
s_nop 0
buffer_load_dwordx4 v10,s[40:43],s55 offen offset:128  lds
s_add_u32 m0,s57,99200
s_nop 0
buffer_load_dwordx4 v11,s[40:43],s55 offen offset:128  lds
v_add_u32 v18,106496,v12
v_add_u32 v19,106496,v13
T a[64:65],18,0
T a[66:67],18,512
T a[68:69],19,0
T a[70:71],19,512
T a[72:73],18,4096
T a[74:75],18,4608
T a[76:77],19,4096
T a[78:79],19,4608
s_waitcnt vmcnt(20) lgkmcnt(0)
s_barrier
s_add_u32 s55,s50,1441792
s_add_u32 m0,s57,106496
s_nop 0
buffer_load_dwordx4 v10,s[40:43],s55 offen offset:0  lds
s_add_u32 m0,s57,107520
s_nop 0
buffer_load_dwordx4 v11,s[40:43],s55 offen offset:0  lds
s_add_u32 m0,s57,114560
s_nop 0
buffer_load_dwordx4 v10,s[40:43],s55 offen offset:128  lds
s_add_u32 m0,s57,115584
s_nop 0
buffer_load_dwordx4 v11,s[40:43],s55 offen offset:128  lds
v_add_u32 v18,122880,v12
v_add_u32 v19,122880,v13
T a[80:81],18,0
T a[82:83],18,512
T a[84:85],19,0
T a[86:87],19,512
T a[88:89],18,4096
T a[90:91],18,4608
T a[92:93],19,4096
T a[94:95],19,4608
s_waitcnt vmcnt(20) lgkmcnt(0)
s_barrier
s_add_u32 s55,s50,1572864
s_add_u32 m0,s57,122880
s_nop 0
buffer_load_dwordx4 v10,s[40:43],s55 offen offset:0  lds
s_add_u32 m0,s57,123904
s_nop 0
buffer_load_dwordx4 v11,s[40:43],s55 offen offset:0  lds
s_add_u32 m0,s57,130944
s_nop 0
buffer_load_dwordx4 v10,s[40:43],s55 offen offset:128  lds
s_add_u32 m0,s57,131968
s_nop 0
buffer_load_dwordx4 v11,s[40:43],s55 offen offset:128  lds
v_add_u32 v18,139264,v12
v_add_u32 v19,139264,v13
T a[96:97],18,0
T a[98:99],18,512
T a[100:101],19,0
T a[102:103],19,512
T a[104:105],18,4096
T a[106:107],18,4608
T a[108:109],19,4096
T a[110:111],19,4608
s_waitcnt vmcnt(20) lgkmcnt(0)
s_barrier
s_add_u32 s55,s50,1703936
s_add_u32 m0,s57,139264
s_nop 0
buffer_load_dwordx4 v10,s[40:43],s55 offen offset:0  lds
s_add_u32 m0,s57,140288
s_nop 0
buffer_load_dwordx4 v11,s[40:43],s55 offen offset:0  lds
s_add_u32 m0,s57,147328
s_nop 0
buffer_load_dwordx4 v10,s[40:43],s55 offen offset:128  lds
s_add_u32 m0,s57,148352
s_nop 0
buffer_load_dwordx4 v11,s[40:43],s55 offen offset:128  lds
v_add_u32 v18,40960,v12
v_add_u32 v19,40960,v13
T a[112:113],18,0
T a[114:115],18,512
T a[116:117],19,0
T a[118:119],19,512
T a[120:121],18,4096
T a[122:123],18,4608
T a[124:125],19,4096
T a[126:127],19,4608
s_waitcnt vmcnt(20) lgkmcnt(0)
s_barrier
s_add_u32 s55,s50,1835008
s_add_u32 m0,s57,40960
s_nop 0
buffer_load_dwordx4 v10,s[40:43],s55 offen offset:0  lds
s_add_u32 m0,s57,41984
s_nop 0
buffer_load_dwordx4 v11,s[40:43],s55 offen offset:0  lds
s_add_u32 m0,s57,49024
s_nop 0
buffer_load_dwordx4 v10,s[40:43],s55 offen offset:128  lds
s_add_u32 m0,s57,50048
s_nop 0
buffer_load_dwordx4 v11,s[40:43],s55 offen offset:128  lds
v_add_u32 v18,57344,v12
v_add_u32 v19,57344,v13
T a[128:129],18,0
T a[130:131],18,512
T a[132:133],19,0
T a[134:135],19,512
T a[136:137],18,4096
T a[138:139],18,4608
T a[140:141],19,4096
T a[142:143],19,4608
s_waitcnt vmcnt(20) lgkmcnt(0)
s_barrier
s_add_u32 s55,s50,1966080
s_add_u32 m0,s57,57344
s_nop 0
buffer_load_dwordx4 v10,s[40:43],s55 offen offset:0  lds
s_add_u32 m0,s57,58368
s_nop 0
buffer_load_dwordx4 v11,s[40:43],s55 offen offset:0  lds
s_add_u32 m0,s57,65408
s_nop 0
buffer_load_dwordx4 v10,s[40:43],s55 offen offset:128  lds
s_add_u32 m0,s57,66432
s_nop 0
buffer_load_dwordx4 v11,s[40:43],s55 offen offset:128  lds
v_add_u32 v18,73728,v12
v_add_u32 v19,73728,v13
T a[144:145],18,0
T a[146:147],18,512
T a[148:149],19,0
T a[150:151],19,512
T a[152:153],18,4096
T a[154:155],18,4608
T a[156:157],19,4096
T a[158:159],19,4608
s_waitcnt vmcnt(20) lgkmcnt(0)
s_barrier
s_add_u32 s55,s50,2097152
s_add_u32 m0,s57,73728
s_nop 0
buffer_load_dwordx4 v10,s[40:43],s55 offen offset:0  lds
s_add_u32 m0,s57,74752
s_nop 0
buffer_load_dwordx4 v11,s[40:43],s55 offen offset:0  lds
s_add_u32 m0,s57,81792
s_nop 0
buffer_load_dwordx4 v10,s[40:43],s55 offen offset:128  lds
s_add_u32 m0,s57,82816
s_nop 0
buffer_load_dwordx4 v11,s[40:43],s55 offen offset:128  lds
v_add_u32 v18,90112,v12
v_add_u32 v19,90112,v13
T a[160:161],18,0
T a[162:163],18,512
T a[164:165],19,0
T a[166:167],19,512
T a[168:169],18,4096
T a[170:171],18,4608
T a[172:173],19,4096
T a[174:175],19,4608
s_waitcnt vmcnt(20) lgkmcnt(0)
s_barrier
s_add_u32 s55,s50,2228224
s_add_u32 m0,s57,90112
s_nop 0
buffer_load_dwordx4 v10,s[40:43],s55 offen offset:0  lds
s_add_u32 m0,s57,91136
s_nop 0
buffer_load_dwordx4 v11,s[40:43],s55 offen offset:0  lds
s_add_u32 m0,s57,98176
s_nop 0
buffer_load_dwordx4 v10,s[40:43],s55 offen offset:128  lds
s_add_u32 m0,s57,99200
s_nop 0
buffer_load_dwordx4 v11,s[40:43],s55 offen offset:128  lds
v_add_u32 v18,106496,v12
v_add_u32 v19,106496,v13
T a[176:177],18,0
T a[178:179],18,512
T a[180:181],19,0
T a[182:183],19,512
T a[184:185],18,4096
T a[186:187],18,4608
T a[188:189],19,4096
T a[190:191],19,4608
s_waitcnt vmcnt(20) lgkmcnt(0)
s_barrier
s_add_u32 s55,s50,2359296
s_add_u32 m0,s57,106496
s_nop 0
buffer_load_dwordx4 v10,s[40:43],s55 offen offset:0  lds
s_add_u32 m0,s57,107520
s_nop 0
buffer_load_dwordx4 v11,s[40:43],s55 offen offset:0  lds
s_add_u32 m0,s57,114560
s_nop 0
buffer_load_dwordx4 v10,s[40:43],s55 offen offset:128  lds
s_add_u32 m0,s57,115584
s_nop 0
buffer_load_dwordx4 v11,s[40:43],s55 offen offset:128  lds
v_add_u32 v18,122880,v12
v_add_u32 v19,122880,v13
T a[192:193],18,0
T a[194:195],18,512
T a[196:197],19,0
T a[198:199],19,512
T a[200:201],18,4096
T a[202:203],18,4608
T a[204:205],19,4096
T a[206:207],19,4608
s_waitcnt vmcnt(20) lgkmcnt(0)
s_barrier
s_add_u32 s55,s50,2490368
s_add_u32 m0,s57,122880
s_nop 0
buffer_load_dwordx4 v10,s[40:43],s55 offen offset:0  lds
s_add_u32 m0,s57,123904
s_nop 0
buffer_load_dwordx4 v11,s[40:43],s55 offen offset:0  lds
s_add_u32 m0,s57,130944
s_nop 0
buffer_load_dwordx4 v10,s[40:43],s55 offen offset:128  lds
s_add_u32 m0,s57,131968
s_nop 0
buffer_load_dwordx4 v11,s[40:43],s55 offen offset:128  lds
v_add_u32 v18,139264,v12
v_add_u32 v19,139264,v13
T a[208:209],18,0
T a[210:211],18,512
T a[212:213],19,0
T a[214:215],19,512
T a[216:217],18,4096
T a[218:219],18,4608
T a[220:221],19,4096
T a[222:223],19,4608
s_waitcnt vmcnt(20) lgkmcnt(0)
s_barrier
s_add_u32 s55,s50,2621440
s_add_u32 m0,s57,139264
s_nop 0
buffer_load_dwordx4 v10,s[40:43],s55 offen offset:0  lds
s_add_u32 m0,s57,140288
s_nop 0
buffer_load_dwordx4 v11,s[40:43],s55 offen offset:0  lds
s_add_u32 m0,s57,147328
s_nop 0
buffer_load_dwordx4 v10,s[40:43],s55 offen offset:128  lds
s_add_u32 m0,s57,148352
s_nop 0
buffer_load_dwordx4 v11,s[40:43],s55 offen offset:128  lds
v_add_u32 v18,40960,v12
v_add_u32 v19,40960,v13
T a[224:225],18,0
T a[226:227],18,512
T a[228:229],19,0
T a[230:231],19,512
T a[232:233],18,4096
T a[234:235],18,4608
T a[236:237],19,4096
T a[238:239],19,4608
s_waitcnt vmcnt(20) lgkmcnt(0)
s_barrier
)"
R"(s_add_u32 s55,s50,2752512
s_add_u32 m0,s57,40960
s_nop 0
buffer_load_dwordx4 v10,s[40:43],s55 offen offset:0  lds
s_add_u32 m0,s57,41984
s_nop 0
buffer_load_dwordx4 v11,s[40:43],s55 offen offset:0  lds
s_add_u32 m0,s57,49024
s_nop 0
buffer_load_dwordx4 v10,s[40:43],s55 offen offset:128  lds
s_add_u32 m0,s57,50048
s_nop 0
buffer_load_dwordx4 v11,s[40:43],s55 offen offset:128  lds
v_add_u32 v18,57344,v12
v_add_u32 v19,57344,v13
T a[240:241],18,0
T a[242:243],18,512
T a[244:245],19,0
T a[246:247],19,512
T a[248:249],18,4096
T a[250:251],18,4608
T a[252:253],19,4096
T a[254:255],19,4608
Lg4_pre_%=:
s_waitcnt vmcnt(20) lgkmcnt(0)
s_barrier
s_add_u32 s55,s50,2883584
s_add_u32 m0,s57,57344
s_nop 0
buffer_load_dwordx4 v10,s[40:43],s55 offen offset:0  lds
s_add_u32 m0,s57,58368
s_nop 0
buffer_load_dwordx4 v11,s[40:43],s55 offen offset:0  lds
s_add_u32 m0,s57,65408
s_nop 0
buffer_load_dwordx4 v10,s[40:43],s55 offen offset:128  lds
s_add_u32 m0,s57,66432
s_nop 0
buffer_load_dwordx4 v11,s[40:43],s55 offen offset:128  lds
v_add_u32 v18,73728,v12
v_add_u32 v19,73728,v13
T v[64:65],18,0
T v[66:67],18,512
T v[68:69],19,0
T v[70:71],19,512
T v[72:73],18,4096
T v[74:75],18,4608
T v[76:77],19,4096
T v[78:79],19,4608
s_waitcnt vmcnt(20) lgkmcnt(0)
s_barrier
s_add_u32 s55,s50,3014656
s_add_u32 m0,s57,73728
s_nop 0
buffer_load_dwordx4 v10,s[40:43],s55 offen offset:0  lds
s_add_u32 m0,s57,74752
s_nop 0
buffer_load_dwordx4 v11,s[40:43],s55 offen offset:0  lds
s_add_u32 m0,s57,81792
s_nop 0
buffer_load_dwordx4 v10,s[40:43],s55 offen offset:128  lds
s_add_u32 m0,s57,82816
s_nop 0
buffer_load_dwordx4 v11,s[40:43],s55 offen offset:128  lds
v_add_u32 v18,90112,v12
v_add_u32 v19,90112,v13
T v[80:81],18,0
T v[82:83],18,512
T v[84:85],19,0
T v[86:87],19,512
T v[88:89],18,4096
T v[90:91],18,4608
T v[92:93],19,4096
T v[94:95],19,4608
s_waitcnt vmcnt(20) lgkmcnt(0)
s_barrier
s_add_u32 s55,s50,3145728
s_add_u32 m0,s57,90112
s_nop 0
buffer_load_dwordx4 v10,s[40:43],s55 offen offset:0  lds
s_add_u32 m0,s57,91136
s_nop 0
buffer_load_dwordx4 v11,s[40:43],s55 offen offset:0  lds
s_add_u32 m0,s57,98176
s_nop 0
buffer_load_dwordx4 v10,s[40:43],s55 offen offset:128  lds
s_add_u32 m0,s57,99200
s_nop 0
buffer_load_dwordx4 v11,s[40:43],s55 offen offset:128  lds
v_add_u32 v18,106496,v12
v_add_u32 v19,106496,v13
T v[96:97],18,0
T v[98:99],18,512
T v[100:101],19,0
T v[102:103],19,512
T v[104:105],18,4096
T v[106:107],18,4608
T v[108:109],19,4096
T v[110:111],19,4608
s_waitcnt vmcnt(20) lgkmcnt(0)
s_barrier
s_add_u32 s55,s50,3276800
s_add_u32 m0,s57,106496
s_nop 0
buffer_load_dwordx4 v10,s[40:43],s55 offen offset:0  lds
s_add_u32 m0,s57,107520
s_nop 0
buffer_load_dwordx4 v11,s[40:43],s55 offen offset:0  lds
s_add_u32 m0,s57,114560
s_nop 0
buffer_load_dwordx4 v10,s[40:43],s55 offen offset:128  lds
s_add_u32 m0,s57,115584
s_nop 0
buffer_load_dwordx4 v11,s[40:43],s55 offen offset:128  lds
v_add_u32 v18,122880,v12
v_add_u32 v19,122880,v13
T v[112:113],18,0
T v[114:115],18,512
T v[116:117],19,0
T v[118:119],19,512
T v[120:121],18,4096
T v[122:123],18,4608
T v[124:125],19,4096
T v[126:127],19,4608
s_waitcnt vmcnt(20) lgkmcnt(0)
s_barrier
s_add_u32 s55,s50,3407872
s_add_u32 m0,s57,122880
s_nop 0
buffer_load_dwordx4 v10,s[40:43],s55 offen offset:0  lds
s_add_u32 m0,s57,123904
s_nop 0
buffer_load_dwordx4 v11,s[40:43],s55 offen offset:0  lds
s_add_u32 m0,s57,130944
s_nop 0
buffer_load_dwordx4 v10,s[40:43],s55 offen offset:128  lds
s_add_u32 m0,s57,131968
s_nop 0
buffer_load_dwordx4 v11,s[40:43],s55 offen offset:128  lds
v_add_u32 v18,139264,v12
v_add_u32 v19,139264,v13
T v[128:129],18,0
T v[130:131],18,512
T v[132:133],19,0
T v[134:135],19,512
T v[136:137],18,4096
T v[138:139],18,4608
T v[140:141],19,4096
T v[142:143],19,4608
s_waitcnt vmcnt(20) lgkmcnt(0)
s_barrier
s_add_u32 s55,s50,3538944
s_add_u32 m0,s57,139264
s_nop 0
buffer_load_dwordx4 v10,s[40:43],s55 offen offset:0  lds
s_add_u32 m0,s57,140288
s_nop 0
buffer_load_dwordx4 v11,s[40:43],s55 offen offset:0  lds
s_add_u32 m0,s57,147328
s_nop 0
buffer_load_dwordx4 v10,s[40:43],s55 offen offset:128  lds
s_add_u32 m0,s57,148352
s_nop 0
buffer_load_dwordx4 v11,s[40:43],s55 offen offset:128  lds
v_add_u32 v18,40960,v12
v_add_u32 v19,40960,v13
T v[144:145],18,0
T v[146:147],18,512
T v[148:149],19,0
T v[150:151],19,512
T v[152:153],18,4096
T v[154:155],18,4608
T v[156:157],19,4096
T v[158:159],19,4608
s_waitcnt vmcnt(20) lgkmcnt(0)
s_barrier
v_add_u32 v18,57344,v12
v_add_u32 v19,57344,v13
T v[160:161],18,0
T v[162:163],18,512
T v[164:165],19,0
T v[166:167],19,512
T v[168:169],18,4096
T v[170:171],18,4608
T v[172:173],19,4096
T v[174:175],19,4608
s_waitcnt vmcnt(16) lgkmcnt(0)
s_barrier
v_add_u32 v18,73728,v12
v_add_u32 v19,73728,v13
T v[176:177],18,0
T v[178:179],18,512
T v[180:181],19,0
T v[182:183],19,512
T v[184:185],18,4096
T v[186:187],18,4608
T v[188:189],19,4096
T v[190:191],19,4608
s_waitcnt vmcnt(12) lgkmcnt(0)
s_barrier
v_add_u32 v18,90112,v12
v_add_u32 v19,90112,v13
T v[192:193],18,0
T v[194:195],18,512
T v[196:197],19,0
T v[198:199],19,512
T v[200:201],18,4096
T v[202:203],18,4608
T v[204:205],19,4096
T v[206:207],19,4608
s_waitcnt vmcnt(8) lgkmcnt(0)
s_barrier
v_add_u32 v18,106496,v12
v_add_u32 v19,106496,v13
T v[208:209],18,0
T v[210:211],18,512
T v[212:213],19,0
T v[214:215],19,512
T v[216:217],18,4096
T v[218:219],18,4608
T v[220:221],19,4096
T v[222:223],19,4608
s_waitcnt vmcnt(4) lgkmcnt(0)
s_barrier
v_add_u32 v18,122880,v12
v_add_u32 v19,122880,v13
T v[224:225],18,0
T v[226:227],18,512
T v[228:229],19,0
T v[230:231],19,512
T v[232:233],18,4096
T v[234:235],18,4608
T v[236:237],19,4096
T v[238:239],19,4608
s_waitcnt vmcnt(0) lgkmcnt(0)
s_barrier
v_add_u32 v18,139264,v12
v_add_u32 v19,139264,v13
T v[240:241],18,0
T v[242:243],18,512
T v[244:245],19,0
T v[246:247],19,512
T v[248:249],18,4096
T v[250:251],18,4608
T v[252:253],19,4096
T v[254:255],19,4608
L 0
s_barrier
v_mov_b32 v4,s53
s_mov_b32 s82,0
s_mov_b32 s80,0
s_mov_b32 s54,s45
s_mul_i32 s57,s44,7168
s_mul_i32 s55,s45,114688
s_add_u32 s55,s55,s57
s_add_u32 s55,s55,40894464
s_mul_i32 s57,s44,7168
s_mul_i32 s60,s46,114688
s_mul_i32 s61,s46,2560
s_mul_i32 s62,s46,32768
s_mul_i32 s63,s46,2048
s_mul_i32 s64,s46,512
s_lshl_b32 s59,s44,8
s_add_u32 s59,s59,143360
s_mul_i32 s58,s45,2560
s_add_u32 s58,s58,109891584
s_mul_i32 s65,s45,32768
s_add_u32 s65,s65,s47
s_mul_i32 s68,s45,2048
s_add_u32 s68,s68,s48
s_mul_i32 s73,s45,512
s_add_u32 s73,s73,106954752
s_mul_i32 s74,s45,32768
s_add_u32 s74,s74,s49
s_mul_i32 s75,s45,1024
s_add_u32 s75,s75,107479040
s_mov_b32 s66,0x7fff0000
s_mov_b32 s67,0x7fff0000
s_mov_b32 s69,0x7fff0000
s_mov_b32 s76,0x7fff0000
s_mov_b32 s77,0x7fff0000
s_mov_b32 s70,0x7fff0000
s_mov_b32 s71,0x7fff0000
s_mov_b32 s72,0x7fff0000
v_mov_b32 v1,v0
v_add_u32 v2,28672,v0
s_mov_b32 s84,28672
s_mov_b32 s86,0x3a800000
s_mov_b32 s87,0x3727c5ac
s_mov_b32 s88,0xc0135761
s_mov_b32 s89,0x3d372713
s_lshr_b32 s85,s44,1
s_mul_i32 s85,s85,127
s_add_u32 s55,s55,s51
s_mul_i32 s78,s45,114688
s_sub_u32 s60,s55,s78
s_add_u32 s50,s93,224
s_add_u32 s93,s93,170
s_cmp_lt_u32 s45,224
s_cselect_b32 s63,224,-1
s_cmp_lt_u32 s45,170
s_cselect_b32 s63,170,s63
s_cselect_b32 s93,s93,s50
s_cselect_b32 s88,6,8
s_cselect_b32 s89,224,-1
v_lshlrev_b32 v8,22,v0
v_and_b32 v8,0x80000000,v8
v_or_b32 v8,v8,v5
v_add_u32 v2,57344,v0
s_lshl_b32 s84,s57,1
s_add_u32 s84,s84,57344
s_mov_b64 s[86:87],%22
s_mov_b64 s[40:41],%22
s_mov_b32 s42,4194304
s_and_b32 s78,s47,65535
s_lshr_b32 s78,s78,4
s_sub_u32 s85,s47,s78
s_add_u32 s85,s85,4194304
s_add_u32 s45,s47,5832704
s_mul_i32 s73,s44,1280
s_add_u32 s73,s73,114688
v_lshrrev_b32 v62,4,v9
v_and_b32 v62,3,v62
v_mul_u32_u24 v62,288,v62
v_lshrrev_b32 v63,7,v9
v_and_b32 v5,12,v9
v_add3_u32 v5,v5,v62,v63
v_add_u32 v5,s73,v5
v_mov_b32 v10,0
v_mov_b32 v11,0
v_mov_b32 v12,0
v_mov_b32 v13,0
s_mov_b32 s79,1638400
buffer_store_dwordx4 v[10:13],v7,s[40:43],s79 offen offset:0 sc0 sc1
s_mov_b32 s79,1966080
buffer_store_dwordx4 v[10:13],v7,s[40:43],s79 offen offset:0 sc0 sc1
s_mov_b32 s79,2293760
buffer_store_dwordx4 v[10:13],v7,s[40:43],s79 offen offset:0 sc0 sc1
s_mov_b32 s79,2621440
buffer_store_dwordx4 v[10:13],v7,s[40:43],s79 offen offset:0 sc0 sc1
s_mov_b32 s79,2949120
buffer_store_dwordx4 v[10:13],v7,s[40:43],s79 offen offset:0 sc0 sc1
s_mov_b32 s79,3276800
buffer_store_dwordx4 v[10:13],v7,s[40:43],s79 offen offset:0 sc0 sc1
s_mov_b32 s79,3604480
buffer_store_dwordx4 v[10:13],v7,s[40:43],s79 offen offset:0 sc0 sc1
s_mov_b32 s79,3932160
buffer_store_dwordx4 v[10:13],v7,s[40:43],s79 offen offset:0 sc0 sc1
s_getpc_b64 s[40:41]
Lg4_pc_%=:
s_lshl_b32 s78,s44,13
v_lshlrev_b32 v62,3,v0
v_add_u32 v62,s78,v62
v_min_u32 v62,Lcode_g4_e-Lg4_pc_%=-4,v62
s_mov_b32 m0,163584
s_nop 0
global_load_lds_dword v62,s[40:41]
s_add_u32 m0,s59,4096
s_nop 0
buffer_load_dword v8,s[36:39],s58 offen offset:0 sc1 lds
buffer_store_dword v4,v3,s[36:39],s71 offen offset:0 sc0 sc1
s_add_u32 m0,s59,5120
s_nop 0
buffer_load_dword v8,s[36:39],s58 offen offset:0 sc1 lds
buffer_store_dword v4,v3,s[36:39],s71 offen offset:0 sc0 sc1
s_mov_b32 s83,0
Lg4_poll_%=:
s_add_u32 s78,s83,2
s_and_b32 s78,s78,3
s_lshl_b32 s78,s78,10
s_add_u32 s78,s78,4096
s_add_u32 m0,s59,s78
s_nop 0
buffer_load_dword v8,s[36:39],s58 offen offset:0 sc1 lds
s_and_b32 s78,s83,3
s_lshl_b32 s78,s78,10
v_add_u32 v62,s78,v6
s_waitcnt vmcnt(4)
s_barrier
ds_read_b32 v7,v62 offset:4096
buffer_store_dword v4,v3,s[36:39],s70 offen offset:0 sc0 sc1
s_mov_b32 s70,s71
s_mov_b32 s71,s72
s_mov_b32 s72,0x7fff0000
s_add_u32 s83,s83,1
s_add_u32 s82,s82,1
L 0
v_cmp_eq_u32 vcc,s53,v7
s_nop 0
s_cmp_eq_u32 vcc_lo,-1
s_cbranch_scc1 Lg4_go_%=
s_cmp_lt_u32 s82,20000
s_cbranch_scc1 Lg4_poll_%=
Lg4_go_%=:
s_add_u32 m0,s57,0
s_add_u32 s56,s55,0
D 0,56,0
D 0,56,1024
D 0,56,2048
D 0,56,3072
s_add_u32 m0,s57,4096
s_add_u32 s56,s56,4096
D 0,56,0
D 0,56,1024
D 0,56,2048
s_add_u32 s61,s54,s46
s_cmp_ge_u32 s61,s63
s_cselect_b32 s61,s93,s61
s_cselect_b32 s46,s88,s46
s_cselect_b32 s63,s89,s63
s_cselect_b32 s93,s50,s93
s_cselect_b32 s88,8,s88
s_cselect_b32 s89,-1,s89
s_mul_i32 s58,s61,2560
s_add_u32 s58,s58,109891584
s_add_u32 m0,s59,2048
s_nop 0
buffer_load_dword v8,s[36:39],s58 offen offset:0 sc1 lds
s_add_u32 m0,s57,28672
s_add_u32 s56,s55,28672
D 0,56,0
D 0,56,1024
D 0,56,2048
D 0,56,3072
s_add_u32 m0,s57,32768
s_add_u32 s56,s56,4096
D 0,56,0
D 0,56,1024
D 0,56,2048
s_mul_i32 s55,s61,114688
s_add_u32 s55,s55,s60
s_sub_u32 s57,s84,s57
s_mov_b32 s54,s61
s_waitcnt vmcnt(7)
s_barrier
buffer_store_dword v4,v3,s[36:39],s70 offen offset:0 sc0 sc1
s_min_u32 s71,s71,s72
buffer_store_dword v4,v3,s[36:39],s71 offen offset:0 sc0 sc1
s_mov_b32 s70,0x7fff0000
s_mov_b32 s71,0x7fff0000
s_mov_b32 s72,0x7fff0000
s_cmp_eq_u32 s80,1
s_cbranch_scc1 Lg4_st1_%=
s_branch Lg4_st0_%=
Lg4_gox_%=:
s_add_u32 m0,s59,2048
s_nop 0
buffer_load_dword v8,s[36:39],s58 offen offset:0 sc1 lds
s_waitcnt vmcnt(0)
s_barrier
buffer_store_dword v4,v3,s[36:39],s70 offen offset:0 sc0 sc1
s_min_u32 s71,s71,s72
buffer_store_dword v4,v3,s[36:39],s71 offen offset:0 sc0 sc1
s_mov_b32 s70,0x7fff0000
s_mov_b32 s71,0x7fff0000
s_mov_b32 s72,0x7fff0000
s_cmp_eq_u32 s80,1
s_cbranch_scc1 Lg4_st1_%=
Lg4_st0_%=:
ds_read_b32 v7,v6 offset:2048
R 26,1,0
R 30,1,1024
R 34,1,2048
R 38,1,3072
R 42,1,4096
L 5
v_cmp_eq_u32 vcc,s53,v7
L 3
s_cmp_eq_u32 vcc_lo,-1
s_cbranch_scc1 Lg4_F0_%=
s_branch Lg4_L0_%=
Lg4_st1_%=:
ds_read_b32 v7,v6 offset:2048
R 26,2,0
R 30,2,1024
R 34,2,2048
R 38,2,3072
R 42,2,4096
L 5
v_cmp_eq_u32 vcc,s53,v7
L 3
s_cmp_eq_u32 vcc_lo,-1
s_cbranch_scc1 Lg4_F1_%=
s_branch Lg4_L1_%=
Lg4_F0_%=:
v_mfma_f32_16x16x32_bf16 v[14:17],a[4:7],v[26:29],0
s_add_u32 m0,s57,0
R 46,1,5120
v_lshlrev_b32 v62,16,v58
M 14,a[12:15],30
s_add_u32 s56,s55,0
v_add_f32 v18,v18,v62
v_and_b32 v63,0xffff0000,v58
v_mfma_f32_16x16x32_bf16 v[10:13],a[0:3],v[26:29],0
s_add_u32 s61,s54,s46
R 50,1,6144
v_add_f32 v19,v19,v63
M 10,a[8:11],30
L 3
D 0,56,0
)"
R"(M 10,a[16:19],34
s_cmp_ge_u32 s61,s63
R 54,1,7168
v_lshlrev_b32 v62,16,v59
M 10,a[24:27],38
s_cselect_b32 s61,s93,s61
v_add_f32 v20,v20,v62
v_and_b32 v63,0xffff0000,v59
M 14,a[20:23],34
s_cselect_b32 s46,s88,s46
R 26,1,8192
v_add_f32 v21,v21,v63
M 14,a[28:31],38
L 3
v_lshlrev_b32 v62,16,v60
v_add_f32 v22,v22,v62
M 14,a[36:39],42
s_cselect_b32 s63,s89,s63
R 30,1,9216
v_and_b32 v63,0xffff0000,v60
M 14,a[44:47],46
D 0,56,1024
M 10,a[32:35],42
s_cselect_b32 s93,s50,s93
R 34,1,10240
v_add_f32 v23,v23,v63
M 10,a[40:43],46
L 3
v_lshlrev_b32 v62,16,v61
v_add_f32 v24,v24,v62
M 10,a[48:51],50
s_cselect_b32 s88,8,s88
R 38,1,11264
v_and_b32 v63,0xffff0000,v61
M 10,a[56:59],54
buffer_load_dwordx4 v[58:61],v0,s[36:39],s74 offen offset:0 sc1
M 14,a[52:55],50
s_cselect_b32 s89,-1,s89
R 42,1,12288
v_add_f32 v25,v25,v63
M 14,a[60:63],54
L 3
v_cvt_pk_bf16_f32 v18,v18,v19
v_cvt_pk_bf16_f32 v19,v20,v21
M 14,a[68:71],26
s_mul_i32 s58,s61,2560
R 46,1,13312
v_cvt_pk_bf16_f32 v20,v22,v23
M 14,a[76:79],30
D 0,56,2048
M 10,a[64:67],26
s_add_u32 s58,s58,109891584
R 50,1,14336
v_cvt_pk_bf16_f32 v21,v24,v25
M 10,a[72:75],30
L 3
buffer_store_dwordx4 v[18:21],v0,s[36:39],s67 offen offset:0 sc0 sc1
M 10,a[80:83],34
s_lshl_b32 s62,s54,15
s_mov_b32 s66,s65
R 54,1,15360
M 10,a[88:91],38
s_cmp_ge_u32 s66,s45
s_cselect_b32 s51,1,-1
s_add_u32 s74,s49,s62
M 14,a[84:87],34
s_add_u32 s65,s47,s62
s_mov_b32 s54,s61
R 26,1,16384
M 14,a[92:95],38
L 3
M 14,a[100:103],42
R 30,1,17408
M 14,a[108:111],46
D 0,56,3072
M 10,a[96:99],42
s_add_u32 m0,s57,4096
s_add_u32 s56,s56,4096
R 34,1,18432
M 10,a[104:107],46
L 3
M 10,a[112:115],50
R 38,1,19456
M 10,a[120:123],54
M 14,a[116:119],50
R 42,1,20480
M 14,a[124:127],54
L 3
D 0,56,0
M 14,a[132:135],26
R 46,1,21504
M 14,a[140:143],30
M 10,a[128:131],26
R 50,1,22528
M 10,a[136:139],30
L 3
M 10,a[144:147],34
R 54,1,23552
M 10,a[152:155],38
M 14,a[148:151],34
R 26,1,24576
M 14,a[156:159],38
L 3
D 0,56,1024
M 14,a[164:167],42
R 30,1,25600
M 14,a[172:175],46
M 10,a[160:163],42
R 34,1,26624
M 10,a[168:171],46
L 3
M 10,a[176:179],50
s_waitcnt vmcnt(9)
R 38,1,27648
M 10,a[184:187],54
s_barrier
M 14,a[180:183],50
R 42,1,28672
M 14,a[188:191],54
L 3
D 0,56,2048
M 14,a[196:199],26
s_add_u32 m0,s59,0
s_add_u32 s56,s55,28672
R 46,1,29696
M 14,a[204:207],30
s_mul_i32 s55,s61,114688
s_add_u32 s55,s55,s60
M 10,a[192:195],26
R 50,1,30720
M 10,a[200:203],30
L 3
M 10,a[208:211],34
R 54,1,31744
M 10,a[216:219],38
M 14,a[212:215],34
R 26,1,32768
M 14,a[220:223],38
L 3
buffer_load_dword v8,s[36:39],s58 offen offset:0 sc1 lds
M 14,a[228:231],42
s_add_u32 m0,s57,28672
R 30,1,33792
M 14,a[236:239],46
D 0,56,0
M 10,a[224:227],42
R 34,1,34816
M 10,a[232:235],46
L 3
M 10,a[240:243],50
R 38,1,35840
M 10,a[248:251],54
M 14,a[244:247],50
R 42,1,36864
M 14,a[252:255],54
L 3
D 0,56,1024
M 14,v[68:71],26
R 46,1,37888
M 14,v[76:79],30
M 10,v[64:67],26
R 50,1,38912
M 10,v[72:75],30
L 3
M 10,v[80:83],34
R 54,1,39936
M 10,v[88:91],38
D 0,56,2048
M 14,v[84:87],34
R 26,1,40960
M 14,v[92:95],38
L 3
M 14,v[100:103],42
R 30,1,41984
M 14,v[108:111],46
M 10,v[96:99],42
R 34,1,43008
M 10,v[104:107],46
L 3
D 0,56,3072
M 10,v[112:115],50
s_add_u32 m0,s57,32768
s_add_u32 s56,s56,4096
R 38,1,44032
M 10,v[120:123],54
s_sub_u32 s57,s84,s57
M 14,v[116:119],50
R 42,1,45056
M 14,v[124:127],54
L 3
M 14,v[132:135],26
R 46,1,46080
M 14,v[140:143],30
D 0,56,0
M 10,v[128:131],26
R 50,1,47104
M 10,v[136:139],30
L 3
M 10,v[144:147],34
R 54,1,48128
M 10,v[152:155],38
M 14,v[148:151],34
R 26,1,49152
M 14,v[156:159],38
L 3
D 0,56,1024
M 14,v[164:167],42
R 30,1,50176
M 14,v[172:175],46
M 10,v[160:163],42
R 34,1,51200
M 10,v[168:171],46
L 3
M 10,v[176:179],50
R 38,1,52224
M 10,v[184:187],54
D 0,56,2048
M 14,v[180:183],50
R 42,1,53248
M 14,v[188:191],54
L 3
M 14,v[196:199],26
R 46,1,54272
M 14,v[204:207],30
M 10,v[192:195],26
R 50,1,55296
M 10,v[200:203],30
L 3
M 10,v[208:211],34
s_waitcnt vmcnt(7)
R 54,1,56320
M 10,v[216:219],38
s_barrier
ds_read_b32 v7,v6 offset:0
M 14,v[212:215],34
R 26,2,0
M 14,v[220:223],38
L 4
buffer_store_dword v4,v3,s[36:39],s69 offen offset:0 sc0 sc1
M 14,v[228:231],42
s_mov_b32 s69,s68
s_lshr_b32 s68,s62,4
R 30,2,1024
M 14,v[236:239],46
s_add_u32 s68,s68,s48
M 10,v[224:227],42
R 34,2,2048
M 10,v[232:235],46
L 4
L 3
M 10,v[240:243],50
R 38,2,3072
v_cmp_eq_u32 vcc,s53,v7
M 10,v[248:251],54
M 14,v[244:247],50
L 2
s_cmp_eq_u32 vcc_lo,s51
R 42,2,4096
M 14,v[252:255],54
s_cbranch_scc0 Lg4_K1_%=
Lg4_F1_%=:
v_mfma_f32_16x16x32_bf16 v[22:25],a[4:7],v[26:29],0
s_add_u32 m0,s57,0
R 46,2,5120
v_lshlrev_b32 v62,16,v58
M 22,a[12:15],30
s_add_u32 s56,s55,0
v_add_f32 v10,v10,v62
v_and_b32 v63,0xffff0000,v58
v_mfma_f32_16x16x32_bf16 v[18:21],a[0:3],v[26:29],0
s_add_u32 s61,s54,s46
R 50,2,6144
v_add_f32 v11,v11,v63
M 18,a[8:11],30
L 3
D 0,56,0
M 18,a[16:19],34
s_cmp_ge_u32 s61,s63
R 54,2,7168
v_lshlrev_b32 v62,16,v59
M 18,a[24:27],38
s_cselect_b32 s61,s93,s61
v_add_f32 v12,v12,v62
v_and_b32 v63,0xffff0000,v59
M 22,a[20:23],34
s_cselect_b32 s46,s88,s46
R 26,2,8192
v_add_f32 v13,v13,v63
M 22,a[28:31],38
L 3
v_lshlrev_b32 v62,16,v60
v_add_f32 v14,v14,v62
M 22,a[36:39],42
s_cselect_b32 s63,s89,s63
R 30,2,9216
v_and_b32 v63,0xffff0000,v60
M 22,a[44:47],46
D 0,56,1024
M 18,a[32:35],42
s_cselect_b32 s93,s50,s93
R 34,2,10240
v_add_f32 v15,v15,v63
M 18,a[40:43],46
L 3
v_lshlrev_b32 v62,16,v61
v_add_f32 v16,v16,v62
M 18,a[48:51],50
s_cselect_b32 s88,8,s88
R 38,2,11264
v_and_b32 v63,0xffff0000,v61
M 18,a[56:59],54
buffer_load_dwordx4 v[58:61],v0,s[36:39],s74 offen offset:0 sc1
M 22,a[52:55],50
s_cselect_b32 s89,-1,s89
R 42,2,12288
v_add_f32 v17,v17,v63
M 22,a[60:63],54
L 3
v_cvt_pk_bf16_f32 v10,v10,v11
v_cvt_pk_bf16_f32 v11,v12,v13
M 22,a[68:71],26
s_mul_i32 s58,s61,2560
R 46,2,13312
v_cvt_pk_bf16_f32 v12,v14,v15
M 22,a[76:79],30
D 0,56,2048
M 18,a[64:67],26
s_add_u32 s58,s58,109891584
R 50,2,14336
v_cvt_pk_bf16_f32 v13,v16,v17
M 18,a[72:75],30
L 3
buffer_store_dwordx4 v[10:13],v0,s[36:39],s66 offen offset:0 sc0 sc1
M 18,a[80:83],34
s_lshl_b32 s62,s54,15
s_mov_b32 s67,s65
R 54,2,15360
M 18,a[88:91],38
s_cmp_ge_u32 s67,s45
s_cselect_b32 s51,1,-1
s_add_u32 s74,s49,s62
M 22,a[84:87],34
s_add_u32 s65,s47,s62
s_mov_b32 s54,s61
R 26,2,16384
M 22,a[92:95],38
L 3
M 22,a[100:103],42
R 30,2,17408
M 22,a[108:111],46
D 0,56,3072
M 18,a[96:99],42
s_add_u32 m0,s57,4096
s_add_u32 s56,s56,4096
R 34,2,18432
M 18,a[104:107],46
L 3
M 18,a[112:115],50
R 38,2,19456
M 18,a[120:123],54
M 22,a[116:119],50
R 42,2,20480
M 22,a[124:127],54
L 3
D 0,56,0
M 22,a[132:135],26
R 46,2,21504
M 22,a[140:143],30
M 18,a[128:131],26
R 50,2,22528
M 18,a[136:139],30
L 3
M 18,a[144:147],34
R 54,2,23552
M 18,a[152:155],38
M 22,a[148:151],34
R 26,2,24576
M 22,a[156:159],38
L 3
D 0,56,1024
M 22,a[164:167],42
R 30,2,25600
M 22,a[172:175],46
M 18,a[160:163],42
R 34,2,26624
M 18,a[168:171],46
L 3
M 18,a[176:179],50
s_waitcnt vmcnt(9)
R 38,2,27648
M 18,a[184:187],54
s_barrier
M 22,a[180:183],50
R 42,2,28672
M 22,a[188:191],54
L 3
D 0,56,2048
M 22,a[196:199],26
s_add_u32 m0,s59,1024
s_add_u32 s56,s55,28672
R 46,2,29696
M 22,a[204:207],30
s_mul_i32 s55,s61,114688
s_add_u32 s55,s55,s60
M 18,a[192:195],26
R 50,2,30720
M 18,a[200:203],30
L 3
M 18,a[208:211],34
R 54,2,31744
M 18,a[216:219],38
M 22,a[212:215],34
R 26,2,32768
M 22,a[220:223],38
L 3
buffer_load_dword v8,s[36:39],s58 offen offset:0 sc1 lds
M 22,a[228:231],42
s_add_u32 m0,s57,28672
R 30,2,33792
M 22,a[236:239],46
D 0,56,0
M 18,a[224:227],42
R 34,2,34816
M 18,a[232:235],46
L 3
M 18,a[240:243],50
R 38,2,35840
M 18,a[248:251],54
M 22,a[244:247],50
R 42,2,36864
M 22,a[252:255],54
L 3
D 0,56,1024
M 22,v[68:71],26
R 46,2,37888
M 22,v[76:79],30
M 18,v[64:67],26
R 50,2,38912
M 18,v[72:75],30
L 3
M 18,v[80:83],34
R 54,2,39936
M 18,v[88:91],38
D 0,56,2048
M 22,v[84:87],34
R 26,2,40960
M 22,v[92:95],38
L 3
M 22,v[100:103],42
R 30,2,41984
M 22,v[108:111],46
M 18,v[96:99],42
R 34,2,43008
M 18,v[104:107],46
L 3
D 0,56,3072
M 18,v[112:115],50
s_add_u32 m0,s57,32768
s_add_u32 s56,s56,4096
R 38,2,44032
M 18,v[120:123],54
s_sub_u32 s57,s84,s57
M 22,v[116:119],50
R 42,2,45056
M 22,v[124:127],54
L 3
M 22,v[132:135],26
R 46,2,46080
M 22,v[140:143],30
D 0,56,0
M 18,v[128:131],26
R 50,2,47104
M 18,v[136:139],30
L 3
M 18,v[144:147],34
R 54,2,48128
M 18,v[152:155],38
M 22,v[148:151],34
R 26,2,49152
M 22,v[156:159],38
L 3
D 0,56,1024
M 22,v[164:167],42
R 30,2,50176
M 22,v[172:175],46
M 18,v[160:163],42
R 34,2,51200
M 18,v[168:171],46
L 3
M 18,v[176:179],50
R 38,2,52224
M 18,v[184:187],54
D 0,56,2048
M 22,v[180:183],50
R 42,2,53248
M 22,v[188:191],54
L 3
M 22,v[196:199],26
R 46,2,54272
M 22,v[204:207],30
)"
R"(M 18,v[192:195],26
R 50,2,55296
M 18,v[200:203],30
L 3
M 18,v[208:211],34
s_waitcnt vmcnt(7)
R 54,2,56320
M 18,v[216:219],38
s_barrier
ds_read_b32 v7,v6 offset:1024
M 22,v[212:215],34
R 26,1,0
M 22,v[220:223],38
L 4
buffer_store_dword v4,v3,s[36:39],s69 offen offset:0 sc0 sc1
M 22,v[228:231],42
s_mov_b32 s69,s68
s_lshr_b32 s68,s62,4
R 30,1,1024
M 22,v[236:239],46
s_add_u32 s68,s68,s48
M 18,v[224:227],42
R 34,1,2048
M 18,v[232:235],46
L 4
L 3
M 18,v[240:243],50
R 38,1,3072
v_cmp_eq_u32 vcc,s53,v7
M 18,v[248:251],54
M 22,v[244:247],50
L 2
s_cmp_eq_u32 vcc_lo,s51
R 42,1,4096
M 22,v[252:255],54
s_cbranch_scc1 Lg4_F0_%=
Lg4_K0_%=:
s_cmp_eq_u32 s51,1
s_cbranch_scc0 Lg4_L0_%=
s_cmp_eq_u32 vcc_lo,-1
s_cbranch_scc1 Lg4_Fa0_%=
s_branch Lg4_La0_%=
Lg4_L0_%=:
v_mfma_f32_16x16x32_bf16 v[14:17],a[4:7],v[26:29],0
s_add_u32 m0,s59,3072
R 46,1,5120
v_lshlrev_b32 v62,16,v58
M 14,a[12:15],30
buffer_load_dword v8,s[36:39],s58 offen offset:0 sc1 lds
v_mfma_f32_16x16x32_bf16 v[10:13],a[0:3],v[26:29],0
s_mov_b32 s66,s65
R 50,1,6144
v_add_f32 v18,v18,v62
M 10,a[8:11],30
L 3
v_and_b32 v63,0xffff0000,v58
v_add_f32 v19,v19,v63
M 10,a[16:19],34
s_lshl_b32 s62,s54,15
R 54,1,7168
v_lshlrev_b32 v62,16,v59
M 10,a[24:27],38
s_cmp_ge_u32 s66,s45
v_add_f32 v20,v20,v62
v_and_b32 v63,0xffff0000,v59
M 14,a[20:23],34
s_cselect_b32 s51,1,-1
R 26,1,8192
v_add_f32 v21,v21,v63
M 14,a[28:31],38
L 3
v_lshlrev_b32 v62,16,v60
v_add_f32 v22,v22,v62
M 14,a[36:39],42
s_add_u32 s65,s47,s62
R 30,1,9216
v_and_b32 v63,0xffff0000,v60
M 14,a[44:47],46
v_add_f32 v23,v23,v63
v_lshlrev_b32 v62,16,v61
M 10,a[32:35],42
R 34,1,10240
v_add_f32 v24,v24,v62
M 10,a[40:43],46
L 3
v_and_b32 v63,0xffff0000,v61
v_add_f32 v25,v25,v63
M 10,a[48:51],50
R 38,1,11264
v_cvt_pk_bf16_f32 v18,v18,v19
M 10,a[56:59],54
buffer_load_dwordx4 v[58:61],v0,s[36:39],s74 offen offset:0 sc1
M 14,a[52:55],50
s_add_u32 s74,s49,s62
R 42,1,12288
v_cvt_pk_bf16_f32 v19,v20,v21
M 14,a[60:63],54
L 3
v_cvt_pk_bf16_f32 v20,v22,v23
v_cvt_pk_bf16_f32 v21,v24,v25
M 14,a[68:71],26
R 46,1,13312
M 14,a[76:79],30
buffer_store_dwordx4 v[18:21],v0,s[36:39],s67 offen offset:0 sc0 sc1
M 10,a[64:67],26
R 50,1,14336
M 10,a[72:75],30
L 3
M 10,a[80:83],34
R 54,1,15360
M 10,a[88:91],38
M 14,a[84:87],34
R 26,1,16384
M 14,a[92:95],38
L 3
M 14,a[100:103],42
R 30,1,17408
M 14,a[108:111],46
M 10,a[96:99],42
R 34,1,18432
M 10,a[104:107],46
L 3
M 10,a[112:115],50
R 38,1,19456
M 10,a[120:123],54
M 14,a[116:119],50
R 42,1,20480
M 14,a[124:127],54
L 3
M 14,a[132:135],26
R 46,1,21504
M 14,a[140:143],30
M 10,a[128:131],26
R 50,1,22528
M 10,a[136:139],30
L 3
M 10,a[144:147],34
R 54,1,23552
M 10,a[152:155],38
M 14,a[148:151],34
R 26,1,24576
M 14,a[156:159],38
L 3
M 14,a[164:167],42
R 30,1,25600
M 14,a[172:175],46
M 10,a[160:163],42
R 34,1,26624
M 10,a[168:171],46
L 3
M 10,a[176:179],50
s_waitcnt vmcnt(2)
R 38,1,27648
M 10,a[184:187],54
s_barrier
ds_read_b32 v7,v6 offset:3072
M 14,a[180:183],50
R 42,1,28672
M 14,a[188:191],54
L 4
M 14,a[196:199],26
R 46,1,29696
M 14,a[204:207],30
M 10,a[192:195],26
R 50,1,30720
M 10,a[200:203],30
L 4
L 3
v_cmp_eq_u32 vcc,s53,v7
M 10,a[208:211],34
R 54,1,31744
M 10,a[216:219],38
M 14,a[212:215],34
s_cmp_eq_u32 vcc_lo,-1
R 26,1,32768
s_cbranch_scc0 Lg4_X0_%=
M 14,a[220:223],38
L 3
s_add_u32 m0,s57,0
s_add_u32 s56,s55,0
M 14,a[228:231],42
s_add_u32 s61,s54,s46
s_cmp_ge_u32 s61,s63
s_cselect_b32 s61,s93,s61
R 30,1,33792
M 14,a[236:239],46
s_mul_i32 s58,s61,2560
D 0,56,0
M 10,a[224:227],42
s_cselect_b32 s46,s88,s46
s_cselect_b32 s63,s89,s63
R 34,1,34816
M 10,a[232:235],46
L 3
s_cselect_b32 s93,s50,s93
s_cselect_b32 s88,8,s88
M 10,a[240:243],50
s_cselect_b32 s89,-1,s89
s_add_u32 s58,s58,109891584
R 38,1,35840
M 10,a[248:251],54
D 0,56,1024
M 14,a[244:247],50
s_mov_b32 s54,s61
R 42,1,36864
M 14,a[252:255],54
L 3
M 14,v[68:71],26
R 46,1,37888
M 14,v[76:79],30
D 0,56,2048
M 10,v[64:67],26
R 50,1,38912
M 10,v[72:75],30
L 3
M 10,v[80:83],34
R 54,1,39936
M 10,v[88:91],38
D 0,56,3072
M 14,v[84:87],34
s_add_u32 m0,s57,4096
s_add_u32 s56,s56,4096
R 26,1,40960
M 14,v[92:95],38
L 3
M 14,v[100:103],42
R 30,1,41984
M 14,v[108:111],46
D 0,56,0
M 10,v[96:99],42
R 34,1,43008
M 10,v[104:107],46
L 3
M 10,v[112:115],50
R 38,1,44032
M 10,v[120:123],54
D 0,56,1024
M 14,v[116:119],50
R 42,1,45056
M 14,v[124:127],54
L 3
M 14,v[132:135],26
R 46,1,46080
M 14,v[140:143],30
D 0,56,2048
M 10,v[128:131],26
s_add_u32 m0,s59,0
s_add_u32 s56,s55,28672
R 50,1,47104
M 10,v[136:139],30
L 3
buffer_load_dword v8,s[36:39],s58 offen offset:0 sc1 lds
M 10,v[144:147],34
s_add_u32 m0,s57,28672
s_mul_i32 s55,s61,114688
R 54,1,48128
M 10,v[152:155],38
s_add_u32 s55,s55,s60
M 14,v[148:151],34
R 26,1,49152
M 14,v[156:159],38
L 3
D 0,56,0
M 14,v[164:167],42
R 30,1,50176
M 14,v[172:175],46
M 10,v[160:163],42
R 34,1,51200
M 10,v[168:171],46
L 3
D 0,56,1024
M 10,v[176:179],50
R 38,1,52224
M 10,v[184:187],54
D 0,56,2048
M 14,v[180:183],50
R 42,1,53248
M 14,v[188:191],54
L 3
M 14,v[196:199],26
R 46,1,54272
M 14,v[204:207],30
D 0,56,3072
M 10,v[192:195],26
s_add_u32 m0,s57,32768
s_add_u32 s56,s56,4096
R 50,1,55296
M 10,v[200:203],30
L 3
s_sub_u32 s57,s84,s57
M 10,v[208:211],34
s_waitcnt vmcnt(4)
R 54,1,56320
M 10,v[216:219],38
s_barrier
ds_read_b32 v7,v6 offset:0
M 14,v[212:215],34
R 26,2,0
M 14,v[220:223],38
L 4
buffer_store_dword v4,v3,s[36:39],s69 offen offset:0 sc0 sc1
M 14,v[228:231],42
s_mov_b32 s69,s68
s_lshr_b32 s68,s62,4
R 30,2,1024
M 14,v[236:239],46
D 0,56,0
M 10,v[224:227],42
s_add_u32 s68,s68,s48
R 34,2,2048
M 10,v[232:235],46
L 4
D 0,56,1024
M 10,v[240:243],50
L 3
R 38,2,3072
v_cmp_eq_u32 vcc,s53,v7
M 10,v[248:251],54
D 0,56,2048
M 14,v[244:247],50
L 2
s_cmp_eq_u32 vcc_lo,s51
R 42,2,4096
M 14,v[252:255],54
buffer_store_dword v4,v3,s[36:39],s71 offen offset:0 sc0 sc1
s_cbranch_scc1 Lg4_F1_%=
s_branch Lg4_K1_%=
Lg4_X0_%=:
M 14,a[220:223],38
L 3
s_add_u32 m0,s59,0
M 14,a[228:231],42
R 30,1,33792
M 14,a[236:239],46
buffer_load_dword v8,s[36:39],s58 offen offset:0 sc1 lds
M 10,a[224:227],42
R 34,1,34816
M 10,a[232:235],46
L 3
M 10,a[240:243],50
R 38,1,35840
M 10,a[248:251],54
M 14,a[244:247],50
R 42,1,36864
M 14,a[252:255],54
L 3
M 14,v[68:71],26
R 46,1,37888
M 14,v[76:79],30
M 10,v[64:67],26
R 50,1,38912
M 10,v[72:75],30
L 3
M 10,v[80:83],34
R 54,1,39936
M 10,v[88:91],38
M 14,v[84:87],34
R 26,1,40960
M 14,v[92:95],38
L 3
M 14,v[100:103],42
R 30,1,41984
M 14,v[108:111],46
M 10,v[96:99],42
R 34,1,43008
M 10,v[104:107],46
L 3
M 10,v[112:115],50
R 38,1,44032
M 10,v[120:123],54
M 14,v[116:119],50
R 42,1,45056
M 14,v[124:127],54
L 3
M 14,v[132:135],26
R 46,1,46080
M 14,v[140:143],30
M 10,v[128:131],26
R 50,1,47104
M 10,v[136:139],30
L 3
M 10,v[144:147],34
R 54,1,48128
M 10,v[152:155],38
M 14,v[148:151],34
R 26,1,49152
M 14,v[156:159],38
L 3
M 14,v[164:167],42
R 30,1,50176
M 14,v[172:175],46
M 10,v[160:163],42
R 34,1,51200
M 10,v[168:171],46
L 3
M 10,v[176:179],50
R 38,1,52224
M 10,v[184:187],54
M 14,v[180:183],50
R 42,1,53248
M 14,v[188:191],54
L 3
M 14,v[196:199],26
R 46,1,54272
M 14,v[204:207],30
M 10,v[192:195],26
R 50,1,55296
M 10,v[200:203],30
L 3
M 10,v[208:211],34
s_waitcnt vmcnt(0)
R 54,1,56320
M 10,v[216:219],38
s_barrier
buffer_store_dword v4,v3,s[36:39],s69 offen offset:0 sc0 sc1
M 14,v[212:215],34
s_mov_b32 s69,s68
s_lshr_b32 s68,s62,4
ds_read_b32 v7,v6 offset:0
M 14,v[220:223],38
L 3
s_add_u32 s68,s68,s48
M 14,v[228:231],42
M 14,v[236:239],46
M 10,v[224:227],42
M 10,v[232:235],46
L 1
M 10,v[240:243],50
L 0
v_cmp_eq_u32 vcc,s53,v7
M 10,v[248:251],54
M 14,v[244:247],50
M 14,v[252:255],54
s_cmp_eq_u32 s51,1
s_cbranch_scc1 Lg4_pa0_%=
s_mov_b32 s80,1
s_cmp_eq_u32 vcc_lo,-1
s_cbranch_scc0 Lg4_xn0_%=
s_add_u32 m0,s57,0
s_add_u32 s56,s55,0
D 0,56,0
v_lshlrev_b32 v62,16,v58
D 0,56,1024
v_add_f32 v10,v10,v62
D 0,56,2048
v_and_b32 v63,0xffff0000,v58
D 0,56,3072
v_add_f32 v11,v11,v63
s_add_u32 m0,s57,4096
s_add_u32 s56,s56,4096
D 0,56,0
v_lshlrev_b32 v62,16,v59
D 0,56,1024
v_add_f32 v12,v12,v62
D 0,56,2048
v_and_b32 v63,0xffff0000,v59
s_add_u32 s61,s54,s46
s_cmp_ge_u32 s61,s63
s_cselect_b32 s61,s93,s61
s_cselect_b32 s46,s88,s46
s_cselect_b32 s63,s89,s63
s_cselect_b32 s93,s50,s93
s_cselect_b32 s88,8,s88
s_cselect_b32 s89,-1,s89
s_mul_i32 s58,s61,2560
s_add_u32 s58,s58,109891584
s_add_u32 m0,s57,28672
s_add_u32 s56,s55,28672
D 0,56,0
v_add_f32 v13,v13,v63
D 0,56,1024
v_lshlrev_b32 v62,16,v60
D 0,56,2048
v_add_f32 v14,v14,v62
D 0,56,3072
v_and_b32 v63,0xffff0000,v60
s_add_u32 m0,s57,32768
s_add_u32 s56,s56,4096
D 0,56,0
v_add_f32 v15,v15,v63
D 0,56,1024
v_lshlrev_b32 v62,16,v61
D 0,56,2048
v_add_f32 v16,v16,v62
s_mul_i32 s55,s61,114688
s_add_u32 s55,s55,s60
s_sub_u32 s57,s84,s57
s_mov_b32 s54,s61
v_and_b32 v63,0xffff0000,v61
v_add_f32 v17,v17,v63
v_cvt_pk_bf16_f32 v10,v10,v11
v_cvt_pk_bf16_f32 v11,v12,v13
v_cvt_pk_bf16_f32 v12,v14,v15
v_cvt_pk_bf16_f32 v13,v16,v17
buffer_store_dwordx4 v[10:13],v0,s[36:39],s66 offen offset:0 sc0 sc1
s_mov_b32 s72,s69
s_mov_b32 s69,0x7fff0000
s_mov_b32 s66,0x7fff0000
s_mov_b32 s76,0x7fff0000
s_branch Lg4_gox_%=
)"
R"(Lg4_xn0_%=:
s_cmp_ge_u32 s54,256
s_cbranch_scc1 Lg4_fin0_%=
s_add_u32 m0,s59,4096
s_nop 0
buffer_load_dword v8,s[36:39],s58 offen offset:0 sc1 lds
buffer_store_dword v4,v3,s[36:39],s71 offen offset:0 sc0 sc1
s_add_u32 m0,s59,5120
s_nop 0
buffer_load_dword v8,s[36:39],s58 offen offset:0 sc1 lds
buffer_store_dword v4,v3,s[36:39],s71 offen offset:0 sc0 sc1
s_mov_b32 s83,0
v_lshlrev_b32 v62,16,v58
v_add_f32 v10,v10,v62
v_and_b32 v63,0xffff0000,v58
v_add_f32 v11,v11,v63
v_lshlrev_b32 v62,16,v59
v_add_f32 v12,v12,v62
v_and_b32 v63,0xffff0000,v59
v_add_f32 v13,v13,v63
v_lshlrev_b32 v62,16,v60
v_add_f32 v14,v14,v62
v_and_b32 v63,0xffff0000,v60
v_add_f32 v15,v15,v63
v_lshlrev_b32 v62,16,v61
v_add_f32 v16,v16,v62
v_and_b32 v63,0xffff0000,v61
v_add_f32 v17,v17,v63
v_cvt_pk_bf16_f32 v10,v10,v11
v_cvt_pk_bf16_f32 v11,v12,v13
v_cvt_pk_bf16_f32 v12,v14,v15
v_cvt_pk_bf16_f32 v13,v16,v17
buffer_store_dwordx4 v[10:13],v0,s[36:39],s66 offen offset:0 sc0 sc1
s_mov_b32 s72,s69
s_mov_b32 s69,0x7fff0000
s_mov_b32 s66,0x7fff0000
s_mov_b32 s76,0x7fff0000
s_branch Lg4_poll_%=
Lg4_fin0_%=:
v_lshlrev_b32 v62,16,v58
v_add_f32 v10,v10,v62
v_and_b32 v63,0xffff0000,v58
v_add_f32 v11,v11,v63
v_lshlrev_b32 v62,16,v59
v_add_f32 v12,v12,v62
v_and_b32 v63,0xffff0000,v59
v_add_f32 v13,v13,v63
v_lshlrev_b32 v62,16,v60
v_add_f32 v14,v14,v62
v_and_b32 v63,0xffff0000,v60
v_add_f32 v15,v15,v63
v_lshlrev_b32 v62,16,v61
v_add_f32 v16,v16,v62
v_and_b32 v63,0xffff0000,v61
v_add_f32 v17,v17,v63
v_cvt_pk_bf16_f32 v10,v10,v11
v_cvt_pk_bf16_f32 v11,v12,v13
v_cvt_pk_bf16_f32 v12,v14,v15
v_cvt_pk_bf16_f32 v13,v16,v17
buffer_store_dwordx4 v[10:13],v0,s[36:39],s66 offen offset:0 sc0 sc1
s_mov_b32 s72,s69
s_mov_b32 s69,0x7fff0000
s_mov_b32 s66,0x7fff0000
s_mov_b32 s76,0x7fff0000
s_branch Lg4_done_%=
Lg4_K1_%=:
s_cmp_eq_u32 s51,1
s_cbranch_scc0 Lg4_L1_%=
s_cmp_eq_u32 vcc_lo,-1
s_cbranch_scc1 Lg4_Fa1_%=
s_branch Lg4_La1_%=
Lg4_L1_%=:
v_mfma_f32_16x16x32_bf16 v[22:25],a[4:7],v[26:29],0
s_add_u32 m0,s59,3072
R 46,2,5120
v_lshlrev_b32 v62,16,v58
M 22,a[12:15],30
buffer_load_dword v8,s[36:39],s58 offen offset:0 sc1 lds
v_mfma_f32_16x16x32_bf16 v[18:21],a[0:3],v[26:29],0
s_mov_b32 s67,s65
R 50,2,6144
v_add_f32 v10,v10,v62
M 18,a[8:11],30
L 3
v_and_b32 v63,0xffff0000,v58
v_add_f32 v11,v11,v63
M 18,a[16:19],34
s_lshl_b32 s62,s54,15
R 54,2,7168
v_lshlrev_b32 v62,16,v59
M 18,a[24:27],38
s_cmp_ge_u32 s67,s45
v_add_f32 v12,v12,v62
v_and_b32 v63,0xffff0000,v59
M 22,a[20:23],34
s_cselect_b32 s51,1,-1
R 26,2,8192
v_add_f32 v13,v13,v63
M 22,a[28:31],38
L 3
v_lshlrev_b32 v62,16,v60
v_add_f32 v14,v14,v62
M 22,a[36:39],42
s_add_u32 s65,s47,s62
R 30,2,9216
v_and_b32 v63,0xffff0000,v60
M 22,a[44:47],46
v_add_f32 v15,v15,v63
v_lshlrev_b32 v62,16,v61
M 18,a[32:35],42
R 34,2,10240
v_add_f32 v16,v16,v62
M 18,a[40:43],46
L 3
v_and_b32 v63,0xffff0000,v61
v_add_f32 v17,v17,v63
M 18,a[48:51],50
R 38,2,11264
v_cvt_pk_bf16_f32 v10,v10,v11
M 18,a[56:59],54
buffer_load_dwordx4 v[58:61],v0,s[36:39],s74 offen offset:0 sc1
M 22,a[52:55],50
s_add_u32 s74,s49,s62
R 42,2,12288
v_cvt_pk_bf16_f32 v11,v12,v13
M 22,a[60:63],54
L 3
v_cvt_pk_bf16_f32 v12,v14,v15
v_cvt_pk_bf16_f32 v13,v16,v17
M 22,a[68:71],26
R 46,2,13312
M 22,a[76:79],30
buffer_store_dwordx4 v[10:13],v0,s[36:39],s66 offen offset:0 sc0 sc1
M 18,a[64:67],26
R 50,2,14336
M 18,a[72:75],30
L 3
M 18,a[80:83],34
R 54,2,15360
M 18,a[88:91],38
M 22,a[84:87],34
R 26,2,16384
M 22,a[92:95],38
L 3
M 22,a[100:103],42
R 30,2,17408
M 22,a[108:111],46
M 18,a[96:99],42
R 34,2,18432
M 18,a[104:107],46
L 3
M 18,a[112:115],50
R 38,2,19456
M 18,a[120:123],54
M 22,a[116:119],50
R 42,2,20480
M 22,a[124:127],54
L 3
M 22,a[132:135],26
R 46,2,21504
M 22,a[140:143],30
M 18,a[128:131],26
R 50,2,22528
M 18,a[136:139],30
L 3
M 18,a[144:147],34
R 54,2,23552
M 18,a[152:155],38
M 22,a[148:151],34
R 26,2,24576
M 22,a[156:159],38
L 3
M 22,a[164:167],42
R 30,2,25600
M 22,a[172:175],46
M 18,a[160:163],42
R 34,2,26624
M 18,a[168:171],46
L 3
M 18,a[176:179],50
s_waitcnt vmcnt(2)
R 38,2,27648
M 18,a[184:187],54
s_barrier
ds_read_b32 v7,v6 offset:3072
M 22,a[180:183],50
R 42,2,28672
M 22,a[188:191],54
L 4
M 22,a[196:199],26
R 46,2,29696
M 22,a[204:207],30
M 18,a[192:195],26
R 50,2,30720
M 18,a[200:203],30
L 4
L 3
v_cmp_eq_u32 vcc,s53,v7
M 18,a[208:211],34
R 54,2,31744
M 18,a[216:219],38
M 22,a[212:215],34
s_cmp_eq_u32 vcc_lo,-1
R 26,2,32768
s_cbranch_scc0 Lg4_X1_%=
M 22,a[220:223],38
L 3
s_add_u32 m0,s57,0
s_add_u32 s56,s55,0
M 22,a[228:231],42
s_add_u32 s61,s54,s46
s_cmp_ge_u32 s61,s63
s_cselect_b32 s61,s93,s61
R 30,2,33792
M 22,a[236:239],46
s_mul_i32 s58,s61,2560
D 0,56,0
M 18,a[224:227],42
s_cselect_b32 s46,s88,s46
s_cselect_b32 s63,s89,s63
R 34,2,34816
M 18,a[232:235],46
L 3
s_cselect_b32 s93,s50,s93
s_cselect_b32 s88,8,s88
M 18,a[240:243],50
s_cselect_b32 s89,-1,s89
s_add_u32 s58,s58,109891584
R 38,2,35840
M 18,a[248:251],54
D 0,56,1024
M 22,a[244:247],50
s_mov_b32 s54,s61
R 42,2,36864
M 22,a[252:255],54
L 3
M 22,v[68:71],26
R 46,2,37888
M 22,v[76:79],30
D 0,56,2048
M 18,v[64:67],26
R 50,2,38912
M 18,v[72:75],30
L 3
M 18,v[80:83],34
R 54,2,39936
M 18,v[88:91],38
D 0,56,3072
M 22,v[84:87],34
s_add_u32 m0,s57,4096
s_add_u32 s56,s56,4096
R 26,2,40960
M 22,v[92:95],38
L 3
M 22,v[100:103],42
R 30,2,41984
M 22,v[108:111],46
D 0,56,0
M 18,v[96:99],42
R 34,2,43008
M 18,v[104:107],46
L 3
M 18,v[112:115],50
R 38,2,44032
M 18,v[120:123],54
D 0,56,1024
M 22,v[116:119],50
R 42,2,45056
M 22,v[124:127],54
L 3
M 22,v[132:135],26
R 46,2,46080
M 22,v[140:143],30
D 0,56,2048
M 18,v[128:131],26
s_add_u32 m0,s59,1024
s_add_u32 s56,s55,28672
R 50,2,47104
M 18,v[136:139],30
L 3
buffer_load_dword v8,s[36:39],s58 offen offset:0 sc1 lds
M 18,v[144:147],34
s_add_u32 m0,s57,28672
s_mul_i32 s55,s61,114688
R 54,2,48128
M 18,v[152:155],38
s_add_u32 s55,s55,s60
M 22,v[148:151],34
R 26,2,49152
M 22,v[156:159],38
L 3
D 0,56,0
M 22,v[164:167],42
R 30,2,50176
M 22,v[172:175],46
M 18,v[160:163],42
R 34,2,51200
M 18,v[168:171],46
L 3
D 0,56,1024
M 18,v[176:179],50
R 38,2,52224
M 18,v[184:187],54
D 0,56,2048
M 22,v[180:183],50
R 42,2,53248
M 22,v[188:191],54
L 3
M 22,v[196:199],26
R 46,2,54272
M 22,v[204:207],30
D 0,56,3072
M 18,v[192:195],26
s_add_u32 m0,s57,32768
s_add_u32 s56,s56,4096
R 50,2,55296
M 18,v[200:203],30
L 3
s_sub_u32 s57,s84,s57
M 18,v[208:211],34
s_waitcnt vmcnt(4)
R 54,2,56320
M 18,v[216:219],38
s_barrier
ds_read_b32 v7,v6 offset:1024
M 22,v[212:215],34
R 26,1,0
M 22,v[220:223],38
L 4
buffer_store_dword v4,v3,s[36:39],s69 offen offset:0 sc0 sc1
M 22,v[228:231],42
s_mov_b32 s69,s68
s_lshr_b32 s68,s62,4
R 30,1,1024
M 22,v[236:239],46
D 0,56,0
M 18,v[224:227],42
s_add_u32 s68,s68,s48
R 34,1,2048
M 18,v[232:235],46
L 4
D 0,56,1024
M 18,v[240:243],50
L 3
R 38,1,3072
v_cmp_eq_u32 vcc,s53,v7
M 18,v[248:251],54
D 0,56,2048
M 22,v[244:247],50
L 2
s_cmp_eq_u32 vcc_lo,s51
R 42,1,4096
M 22,v[252:255],54
buffer_store_dword v4,v3,s[36:39],s71 offen offset:0 sc0 sc1
s_cbranch_scc1 Lg4_F0_%=
s_branch Lg4_K0_%=
Lg4_X1_%=:
M 22,a[220:223],38
L 3
s_add_u32 m0,s59,1024
M 22,a[228:231],42
R 30,2,33792
M 22,a[236:239],46
buffer_load_dword v8,s[36:39],s58 offen offset:0 sc1 lds
M 18,a[224:227],42
R 34,2,34816
M 18,a[232:235],46
L 3
M 18,a[240:243],50
R 38,2,35840
M 18,a[248:251],54
M 22,a[244:247],50
R 42,2,36864
M 22,a[252:255],54
L 3
M 22,v[68:71],26
R 46,2,37888
M 22,v[76:79],30
M 18,v[64:67],26
R 50,2,38912
M 18,v[72:75],30
L 3
M 18,v[80:83],34
R 54,2,39936
M 18,v[88:91],38
M 22,v[84:87],34
R 26,2,40960
M 22,v[92:95],38
L 3
M 22,v[100:103],42
R 30,2,41984
M 22,v[108:111],46
M 18,v[96:99],42
R 34,2,43008
M 18,v[104:107],46
L 3
M 18,v[112:115],50
R 38,2,44032
M 18,v[120:123],54
M 22,v[116:119],50
R 42,2,45056
M 22,v[124:127],54
L 3
M 22,v[132:135],26
R 46,2,46080
M 22,v[140:143],30
M 18,v[128:131],26
R 50,2,47104
M 18,v[136:139],30
L 3
M 18,v[144:147],34
R 54,2,48128
M 18,v[152:155],38
M 22,v[148:151],34
R 26,2,49152
M 22,v[156:159],38
L 3
M 22,v[164:167],42
R 30,2,50176
M 22,v[172:175],46
M 18,v[160:163],42
R 34,2,51200
M 18,v[168:171],46
L 3
M 18,v[176:179],50
R 38,2,52224
M 18,v[184:187],54
M 22,v[180:183],50
R 42,2,53248
M 22,v[188:191],54
L 3
M 22,v[196:199],26
R 46,2,54272
M 22,v[204:207],30
M 18,v[192:195],26
R 50,2,55296
M 18,v[200:203],30
L 3
M 18,v[208:211],34
s_waitcnt vmcnt(0)
R 54,2,56320
M 18,v[216:219],38
s_barrier
buffer_store_dword v4,v3,s[36:39],s69 offen offset:0 sc0 sc1
M 22,v[212:215],34
s_mov_b32 s69,s68
s_lshr_b32 s68,s62,4
ds_read_b32 v7,v6 offset:1024
M 22,v[220:223],38
L 3
s_add_u32 s68,s68,s48
M 22,v[228:231],42
M 22,v[236:239],46
M 18,v[224:227],42
M 18,v[232:235],46
L 1
M 18,v[240:243],50
L 0
v_cmp_eq_u32 vcc,s53,v7
M 18,v[248:251],54
M 22,v[244:247],50
M 22,v[252:255],54
s_cmp_eq_u32 s51,1
s_cbranch_scc1 Lg4_pa1_%=
s_mov_b32 s80,0
s_cmp_eq_u32 vcc_lo,-1
s_cbranch_scc0 Lg4_xn1_%=
s_add_u32 m0,s57,0
s_add_u32 s56,s55,0
D 0,56,0
v_lshlrev_b32 v62,16,v58
D 0,56,1024
v_add_f32 v18,v18,v62
D 0,56,2048
v_and_b32 v63,0xffff0000,v58
D 0,56,3072
v_add_f32 v19,v19,v63
s_add_u32 m0,s57,4096
s_add_u32 s56,s56,4096
D 0,56,0
v_lshlrev_b32 v62,16,v59
D 0,56,1024
v_add_f32 v20,v20,v62
D 0,56,2048
v_and_b32 v63,0xffff0000,v59
s_add_u32 s61,s54,s46
s_cmp_ge_u32 s61,s63
s_cselect_b32 s61,s93,s61
s_cselect_b32 s46,s88,s46
s_cselect_b32 s63,s89,s63
s_cselect_b32 s93,s50,s93
s_cselect_b32 s88,8,s88
s_cselect_b32 s89,-1,s89
s_mul_i32 s58,s61,2560
s_add_u32 s58,s58,109891584
s_add_u32 m0,s57,28672
s_add_u32 s56,s55,28672
D 0,56,0
v_add_f32 v21,v21,v63
D 0,56,1024
)"
R"(v_lshlrev_b32 v62,16,v60
D 0,56,2048
v_add_f32 v22,v22,v62
D 0,56,3072
v_and_b32 v63,0xffff0000,v60
s_add_u32 m0,s57,32768
s_add_u32 s56,s56,4096
D 0,56,0
v_add_f32 v23,v23,v63
D 0,56,1024
v_lshlrev_b32 v62,16,v61
D 0,56,2048
v_add_f32 v24,v24,v62
s_mul_i32 s55,s61,114688
s_add_u32 s55,s55,s60
s_sub_u32 s57,s84,s57
s_mov_b32 s54,s61
v_and_b32 v63,0xffff0000,v61
v_add_f32 v25,v25,v63
v_cvt_pk_bf16_f32 v18,v18,v19
v_cvt_pk_bf16_f32 v19,v20,v21
v_cvt_pk_bf16_f32 v20,v22,v23
v_cvt_pk_bf16_f32 v21,v24,v25
buffer_store_dwordx4 v[18:21],v0,s[36:39],s67 offen offset:0 sc0 sc1
s_mov_b32 s72,s69
s_mov_b32 s69,0x7fff0000
s_mov_b32 s67,0x7fff0000
s_mov_b32 s77,0x7fff0000
s_branch Lg4_gox_%=
Lg4_xn1_%=:
s_cmp_ge_u32 s54,256
s_cbranch_scc1 Lg4_fin1_%=
s_add_u32 m0,s59,4096
s_nop 0
buffer_load_dword v8,s[36:39],s58 offen offset:0 sc1 lds
buffer_store_dword v4,v3,s[36:39],s71 offen offset:0 sc0 sc1
s_add_u32 m0,s59,5120
s_nop 0
buffer_load_dword v8,s[36:39],s58 offen offset:0 sc1 lds
buffer_store_dword v4,v3,s[36:39],s71 offen offset:0 sc0 sc1
s_mov_b32 s83,0
v_lshlrev_b32 v62,16,v58
v_add_f32 v18,v18,v62
v_and_b32 v63,0xffff0000,v58
v_add_f32 v19,v19,v63
v_lshlrev_b32 v62,16,v59
v_add_f32 v20,v20,v62
v_and_b32 v63,0xffff0000,v59
v_add_f32 v21,v21,v63
v_lshlrev_b32 v62,16,v60
v_add_f32 v22,v22,v62
v_and_b32 v63,0xffff0000,v60
v_add_f32 v23,v23,v63
v_lshlrev_b32 v62,16,v61
v_add_f32 v24,v24,v62
v_and_b32 v63,0xffff0000,v61
v_add_f32 v25,v25,v63
v_cvt_pk_bf16_f32 v18,v18,v19
v_cvt_pk_bf16_f32 v19,v20,v21
v_cvt_pk_bf16_f32 v20,v22,v23
v_cvt_pk_bf16_f32 v21,v24,v25
buffer_store_dwordx4 v[18:21],v0,s[36:39],s67 offen offset:0 sc0 sc1
s_mov_b32 s72,s69
s_mov_b32 s69,0x7fff0000
s_mov_b32 s67,0x7fff0000
s_mov_b32 s77,0x7fff0000
s_branch Lg4_poll_%=
Lg4_fin1_%=:
v_lshlrev_b32 v62,16,v58
v_add_f32 v18,v18,v62
v_and_b32 v63,0xffff0000,v58
v_add_f32 v19,v19,v63
v_lshlrev_b32 v62,16,v59
v_add_f32 v20,v20,v62
v_and_b32 v63,0xffff0000,v59
v_add_f32 v21,v21,v63
v_lshlrev_b32 v62,16,v60
v_add_f32 v22,v22,v62
v_and_b32 v63,0xffff0000,v60
v_add_f32 v23,v23,v63
v_lshlrev_b32 v62,16,v61
v_add_f32 v24,v24,v62
v_and_b32 v63,0xffff0000,v61
v_add_f32 v25,v25,v63
v_cvt_pk_bf16_f32 v18,v18,v19
v_cvt_pk_bf16_f32 v19,v20,v21
v_cvt_pk_bf16_f32 v20,v22,v23
v_cvt_pk_bf16_f32 v21,v24,v25
buffer_store_dwordx4 v[18:21],v0,s[36:39],s67 offen offset:0 sc0 sc1
s_mov_b32 s72,s69
s_mov_b32 s69,0x7fff0000
s_mov_b32 s67,0x7fff0000
s_mov_b32 s77,0x7fff0000
s_branch Lg4_done_%=
Lg4_Fa0_%=:
s_nop 0
v_mfma_f32_16x16x32_bf16 v[14:17],a[4:7],v[26:29],0
s_add_u32 m0,s57,0
R 46,1,5120
v_lshlrev_b32 v62,16,v58
M 14,a[12:15],30
s_add_u32 s56,s55,0
v_add_f32 v18,v18,v62
v_and_b32 v63,0xffff0000,v58
v_mfma_f32_16x16x32_bf16 v[10:13],a[0:3],v[26:29],0
s_add_u32 s61,s54,s46
R 50,1,6144
v_add_f32 v19,v19,v63
M 10,a[8:11],30
L 3
D 0,56,0
M 10,a[16:19],34
s_cmp_ge_u32 s61,s63
R 54,1,7168
v_lshlrev_b32 v62,16,v59
M 10,a[24:27],38
s_cselect_b32 s61,s93,s61
v_add_f32 v20,v20,v62
v_and_b32 v63,0xffff0000,v59
M 14,a[20:23],34
s_cselect_b32 s46,s88,s46
R 26,1,8192
v_add_f32 v21,v21,v63
M 14,a[28:31],38
L 3
v_lshlrev_b32 v62,16,v60
v_add_f32 v22,v22,v62
M 14,a[36:39],42
s_cselect_b32 s63,s89,s63
R 30,1,9216
v_and_b32 v63,0xffff0000,v60
M 14,a[44:47],46
D 0,56,1024
M 10,a[32:35],42
s_cselect_b32 s93,s50,s93
R 34,1,10240
v_add_f32 v23,v23,v63
M 10,a[40:43],46
L 3
v_lshlrev_b32 v62,16,v61
v_add_f32 v24,v24,v62
M 10,a[48:51],50
s_cselect_b32 s88,8,s88
R 38,1,11264
v_and_b32 v63,0xffff0000,v61
M 10,a[56:59],54
buffer_load_dwordx4 v[58:61],v0,s[36:39],s74 offen offset:0 sc1
M 14,a[52:55],50
s_cselect_b32 s89,-1,s89
R 42,1,12288
v_add_f32 v25,v25,v63
M 14,a[60:63],54
L 3
v_cvt_pk_bf16_f32 v18,v18,v19
v_cvt_pk_bf16_f32 v19,v20,v21
M 14,a[68:71],26
s_mul_i32 s58,s61,2560
R 46,1,13312
v_cvt_pk_bf16_f32 v20,v22,v23
M 14,a[76:79],30
D 0,56,2048
M 10,a[64:67],26
s_add_u32 s58,s58,109891584
R 50,1,14336
v_cvt_pk_bf16_f32 v21,v24,v25
M 10,a[72:75],30
L 3
v_lshrrev_b32 v62,8,v0
v_lshl_add_u32 v62,v62,5,v0
M 10,a[80:83],34
s_sub_u32 s75,s67,s85
R 54,1,15360
v_add_u32 v62,s73,v62
M 10,a[88:91],38
s_mov_b64 s[40:41],s[86:87]
ds_write_b128 v62,v[18:21] offset:0
M 14,a[84:87],34
s_mov_b32 s42,4194304
R 26,1,16384
ds_read2_b32 v[22:23],v5 offset0:0 offset1:16
M 14,a[92:95],38
L 5
s_mov_b32 s43,0x00020000
ds_read2_b32 v[24:25],v5 offset0:32 offset1:48
M 14,a[100:103],42
s_add_u32 s64,s75,0
s_lshl_b32 s62,s54,15
R 30,1,17408
M 14,a[108:111],46
D 0,56,3072
M 10,a[96:99],42
s_add_u32 m0,s57,4096
s_add_u32 s56,s56,4096
R 34,1,18432
M 10,a[104:107],46
L 6
s_mov_b32 s66,s65
s_add_u32 s74,s49,s62
M 10,a[112:115],50
s_add_u32 s65,s47,s62
s_mov_b32 s54,s61
R 38,1,19456
M 10,a[120:123],54
L 3
M 14,a[116:119],50
R 42,1,20480
M 14,a[124:127],54
L 3
D 0,56,0
M 14,a[132:135],26
R 46,1,21504
M 14,a[140:143],30
M 10,a[128:131],26
R 50,1,22528
M 10,a[136:139],30
L 3
M 10,a[144:147],34
R 54,1,23552
M 10,a[152:155],38
M 14,a[148:151],34
R 26,1,24576
M 14,a[156:159],38
L 3
D 0,56,1024
M 14,a[164:167],42
R 30,1,25600
M 14,a[172:175],46
M 10,a[160:163],42
R 34,1,26624
M 10,a[168:171],46
L 3
M 10,a[176:179],50
s_waitcnt vmcnt(8)
R 38,1,27648
M 10,a[184:187],54
s_barrier
M 14,a[180:183],50
R 42,1,28672
M 14,a[188:191],54
L 3
D 0,56,2048
M 14,a[196:199],26
s_add_u32 m0,s59,0
s_add_u32 s56,s55,28672
R 46,1,29696
M 14,a[204:207],30
s_mul_i32 s55,s61,114688
s_add_u32 s55,s55,s60
M 10,a[192:195],26
R 50,1,30720
M 10,a[200:203],30
L 3
M 10,a[208:211],34
R 54,1,31744
M 10,a[216:219],38
M 14,a[212:215],34
R 26,1,32768
M 14,a[220:223],38
L 3
buffer_load_dword v8,s[36:39],s58 offen offset:0 sc1 lds
M 14,a[228:231],42
s_add_u32 m0,s57,28672
R 30,1,33792
M 14,a[236:239],46
D 0,56,0
M 10,a[224:227],42
R 34,1,34816
M 10,a[232:235],46
L 3
M 10,a[240:243],50
R 38,1,35840
M 10,a[248:251],54
M 14,a[244:247],50
R 42,1,36864
M 14,a[252:255],54
L 3
D 0,56,1024
M 14,v[68:71],26
R 46,1,37888
M 14,v[76:79],30
M 10,v[64:67],26
R 50,1,38912
M 10,v[72:75],30
L 3
M 10,v[80:83],34
R 54,1,39936
M 10,v[88:91],38
D 0,56,2048
M 14,v[84:87],34
R 26,1,40960
M 14,v[92:95],38
L 3
M 14,v[100:103],42
R 30,1,41984
M 14,v[108:111],46
M 10,v[96:99],42
R 34,1,43008
M 10,v[104:107],46
L 3
D 0,56,3072
M 10,v[112:115],50
s_add_u32 m0,s57,32768
s_add_u32 s56,s56,4096
R 38,1,44032
M 10,v[120:123],54
s_sub_u32 s57,s84,s57
M 14,v[116:119],50
R 42,1,45056
M 14,v[124:127],54
L 3
M 14,v[132:135],26
R 46,1,46080
M 14,v[140:143],30
D 0,56,0
M 10,v[128:131],26
R 50,1,47104
M 10,v[136:139],30
L 3
M 10,v[144:147],34
R 54,1,48128
M 10,v[152:155],38
M 14,v[148:151],34
R 26,1,49152
M 14,v[156:159],38
L 3
D 0,56,1024
M 14,v[164:167],42
R 30,1,50176
M 14,v[172:175],46
M 10,v[160:163],42
R 34,1,51200
M 10,v[168:171],46
L 3
M 10,v[176:179],50
R 38,1,52224
M 10,v[184:187],54
D 0,56,2048
M 14,v[180:183],50
R 42,1,53248
M 14,v[188:191],54
L 3
M 14,v[196:199],26
R 46,1,54272
M 14,v[204:207],30
M 10,v[192:195],26
R 50,1,55296
M 10,v[200:203],30
L 3
M 10,v[208:211],34
s_waitcnt vmcnt(7)
R 54,1,56320
M 10,v[216:219],38
s_barrier
ds_read_b32 v7,v6 offset:0
M 14,v[212:215],34
R 26,2,0
M 14,v[220:223],38
L 4
M 14,v[228:231],42
R 30,2,1024
M 14,v[236:239],46
buffer_atomic_pk_add_bf16 v22,v9,s[40:43],s64 offen sc1
M 10,v[224:227],42
s_add_u32 s64,s75,8192
R 34,2,2048
M 10,v[232:235],46
L 4
buffer_atomic_pk_add_bf16 v23,v9,s[40:43],s64 offen sc1
M 10,v[240:243],50
s_add_u32 s64,s75,16384
L 3
R 38,2,3072
v_cmp_eq_u32 vcc,s53,v7
M 10,v[248:251],54
buffer_atomic_pk_add_bf16 v24,v9,s[40:43],s64 offen sc1
M 14,v[244:247],50
L 2
s_add_u32 s64,s75,24576
s_cmp_eq_u32 vcc_lo,-1
R 42,2,4096
M 14,v[252:255],54
buffer_atomic_pk_add_bf16 v25,v9,s[40:43],s64 offen sc1
s_cbranch_scc0 Lg4_Ka1_%=
Lg4_Fa1_%=:
s_nop 0
v_mfma_f32_16x16x32_bf16 v[22:25],a[4:7],v[26:29],0
s_add_u32 m0,s57,0
R 46,2,5120
v_lshlrev_b32 v62,16,v58
M 22,a[12:15],30
s_add_u32 s56,s55,0
v_add_f32 v10,v10,v62
v_and_b32 v63,0xffff0000,v58
v_mfma_f32_16x16x32_bf16 v[18:21],a[0:3],v[26:29],0
s_add_u32 s61,s54,s46
R 50,2,6144
v_add_f32 v11,v11,v63
M 18,a[8:11],30
L 3
D 0,56,0
M 18,a[16:19],34
s_cmp_ge_u32 s61,s63
R 54,2,7168
v_lshlrev_b32 v62,16,v59
M 18,a[24:27],38
s_cselect_b32 s61,s93,s61
v_add_f32 v12,v12,v62
v_and_b32 v63,0xffff0000,v59
M 22,a[20:23],34
s_cselect_b32 s46,s88,s46
R 26,2,8192
v_add_f32 v13,v13,v63
M 22,a[28:31],38
L 3
v_lshlrev_b32 v62,16,v60
v_add_f32 v14,v14,v62
M 22,a[36:39],42
s_cselect_b32 s63,s89,s63
R 30,2,9216
v_and_b32 v63,0xffff0000,v60
M 22,a[44:47],46
D 0,56,1024
M 18,a[32:35],42
s_cselect_b32 s93,s50,s93
R 34,2,10240
v_add_f32 v15,v15,v63
M 18,a[40:43],46
L 3
v_lshlrev_b32 v62,16,v61
v_add_f32 v16,v16,v62
M 18,a[48:51],50
s_cselect_b32 s88,8,s88
R 38,2,11264
v_and_b32 v63,0xffff0000,v61
M 18,a[56:59],54
buffer_load_dwordx4 v[58:61],v0,s[36:39],s74 offen offset:0 sc1
M 22,a[52:55],50
s_cselect_b32 s89,-1,s89
R 42,2,12288
v_add_f32 v17,v17,v63
M 22,a[60:63],54
L 3
v_cvt_pk_bf16_f32 v10,v10,v11
v_cvt_pk_bf16_f32 v11,v12,v13
M 22,a[68:71],26
s_mul_i32 s58,s61,2560
R 46,2,13312
v_cvt_pk_bf16_f32 v12,v14,v15
M 22,a[76:79],30
D 0,56,2048
M 18,a[64:67],26
s_add_u32 s58,s58,109891584
R 50,2,14336
v_cvt_pk_bf16_f32 v13,v16,v17
M 18,a[72:75],30
L 3
v_lshrrev_b32 v62,8,v0
v_lshl_add_u32 v62,v62,5,v0
M 18,a[80:83],34
s_sub_u32 s75,s66,s85
R 54,2,15360
v_add_u32 v62,s73,v62
M 18,a[88:91],38
s_mov_b64 s[40:41],s[86:87]
ds_write_b128 v62,v[10:13] offset:0
M 22,a[84:87],34
s_mov_b32 s42,4194304
R 26,2,16384
ds_read2_b32 v[14:15],v5 offset0:0 offset1:16
M 22,a[92:95],38
L 5
s_mov_b32 s43,0x00020000
ds_read2_b32 v[16:17],v5 offset0:32 offset1:48
M 22,a[100:103],42
s_add_u32 s64,s75,0
s_lshl_b32 s62,s54,15
R 30,2,17408
M 22,a[108:111],46
D 0,56,3072
M 18,a[96:99],42
s_add_u32 m0,s57,4096
s_add_u32 s56,s56,4096
R 34,2,18432
M 18,a[104:107],46
L 6
s_mov_b32 s67,s65
s_add_u32 s74,s49,s62
M 18,a[112:115],50
s_add_u32 s65,s47,s62
s_mov_b32 s54,s61
R 38,2,19456
M 18,a[120:123],54
L 3
M 22,a[116:119],50
R 42,2,20480
M 22,a[124:127],54
L 3
D 0,56,0
M 22,a[132:135],26
R 46,2,21504
)"
R"(M 22,a[140:143],30
M 18,a[128:131],26
R 50,2,22528
M 18,a[136:139],30
L 3
M 18,a[144:147],34
R 54,2,23552
M 18,a[152:155],38
M 22,a[148:151],34
R 26,2,24576
M 22,a[156:159],38
L 3
D 0,56,1024
M 22,a[164:167],42
R 30,2,25600
M 22,a[172:175],46
M 18,a[160:163],42
R 34,2,26624
M 18,a[168:171],46
L 3
M 18,a[176:179],50
s_waitcnt vmcnt(8)
R 38,2,27648
M 18,a[184:187],54
s_barrier
M 22,a[180:183],50
R 42,2,28672
M 22,a[188:191],54
L 3
D 0,56,2048
M 22,a[196:199],26
s_add_u32 m0,s59,1024
s_add_u32 s56,s55,28672
R 46,2,29696
M 22,a[204:207],30
s_mul_i32 s55,s61,114688
s_add_u32 s55,s55,s60
M 18,a[192:195],26
R 50,2,30720
M 18,a[200:203],30
L 3
M 18,a[208:211],34
R 54,2,31744
M 18,a[216:219],38
M 22,a[212:215],34
R 26,2,32768
M 22,a[220:223],38
L 3
buffer_load_dword v8,s[36:39],s58 offen offset:0 sc1 lds
M 22,a[228:231],42
s_add_u32 m0,s57,28672
R 30,2,33792
M 22,a[236:239],46
D 0,56,0
M 18,a[224:227],42
R 34,2,34816
M 18,a[232:235],46
L 3
M 18,a[240:243],50
R 38,2,35840
M 18,a[248:251],54
M 22,a[244:247],50
R 42,2,36864
M 22,a[252:255],54
L 3
D 0,56,1024
M 22,v[68:71],26
R 46,2,37888
M 22,v[76:79],30
M 18,v[64:67],26
R 50,2,38912
M 18,v[72:75],30
L 3
M 18,v[80:83],34
R 54,2,39936
M 18,v[88:91],38
D 0,56,2048
M 22,v[84:87],34
R 26,2,40960
M 22,v[92:95],38
L 3
M 22,v[100:103],42
R 30,2,41984
M 22,v[108:111],46
M 18,v[96:99],42
R 34,2,43008
M 18,v[104:107],46
L 3
D 0,56,3072
M 18,v[112:115],50
s_add_u32 m0,s57,32768
s_add_u32 s56,s56,4096
R 38,2,44032
M 18,v[120:123],54
s_sub_u32 s57,s84,s57
M 22,v[116:119],50
R 42,2,45056
M 22,v[124:127],54
L 3
M 22,v[132:135],26
R 46,2,46080
M 22,v[140:143],30
D 0,56,0
M 18,v[128:131],26
R 50,2,47104
M 18,v[136:139],30
L 3
M 18,v[144:147],34
R 54,2,48128
M 18,v[152:155],38
M 22,v[148:151],34
R 26,2,49152
M 22,v[156:159],38
L 3
D 0,56,1024
M 22,v[164:167],42
R 30,2,50176
M 22,v[172:175],46
M 18,v[160:163],42
R 34,2,51200
M 18,v[168:171],46
L 3
M 18,v[176:179],50
R 38,2,52224
M 18,v[184:187],54
D 0,56,2048
M 22,v[180:183],50
R 42,2,53248
M 22,v[188:191],54
L 3
M 22,v[196:199],26
R 46,2,54272
M 22,v[204:207],30
M 18,v[192:195],26
R 50,2,55296
M 18,v[200:203],30
L 3
M 18,v[208:211],34
s_waitcnt vmcnt(7)
R 54,2,56320
M 18,v[216:219],38
s_barrier
ds_read_b32 v7,v6 offset:1024
M 22,v[212:215],34
R 26,1,0
M 22,v[220:223],38
L 4
M 22,v[228:231],42
R 30,1,1024
M 22,v[236:239],46
buffer_atomic_pk_add_bf16 v14,v9,s[40:43],s64 offen sc1
M 18,v[224:227],42
s_add_u32 s64,s75,8192
R 34,1,2048
M 18,v[232:235],46
L 4
buffer_atomic_pk_add_bf16 v15,v9,s[40:43],s64 offen sc1
M 18,v[240:243],50
s_add_u32 s64,s75,16384
L 3
R 38,1,3072
v_cmp_eq_u32 vcc,s53,v7
M 18,v[248:251],54
buffer_atomic_pk_add_bf16 v16,v9,s[40:43],s64 offen sc1
M 22,v[244:247],50
L 2
s_add_u32 s64,s75,24576
s_cmp_eq_u32 vcc_lo,-1
R 42,1,4096
M 22,v[252:255],54
buffer_atomic_pk_add_bf16 v17,v9,s[40:43],s64 offen sc1
s_cbranch_scc1 Lg4_Fa0_%=
Lg4_Ka0_%=:
Lg4_La0_%=:
s_nop 0
v_mfma_f32_16x16x32_bf16 v[14:17],a[4:7],v[26:29],0
s_add_u32 m0,s59,3072
R 46,1,5120
v_lshlrev_b32 v62,16,v58
M 14,a[12:15],30
buffer_load_dword v8,s[36:39],s58 offen offset:0 sc1 lds
v_mfma_f32_16x16x32_bf16 v[10:13],a[0:3],v[26:29],0
s_sub_u32 s75,s67,s85
R 50,1,6144
v_add_f32 v18,v18,v62
M 10,a[8:11],30
L 3
v_and_b32 v63,0xffff0000,v58
v_add_f32 v19,v19,v63
M 10,a[16:19],34
s_mov_b64 s[40:41],s[86:87]
R 54,1,7168
v_lshlrev_b32 v62,16,v59
M 10,a[24:27],38
s_mov_b32 s42,4194304
v_add_f32 v20,v20,v62
v_and_b32 v63,0xffff0000,v59
M 14,a[20:23],34
s_mov_b32 s43,0x00020000
R 26,1,8192
v_add_f32 v21,v21,v63
M 14,a[28:31],38
L 3
v_lshlrev_b32 v62,16,v60
v_add_f32 v22,v22,v62
M 14,a[36:39],42
s_add_u32 s64,s75,0
R 30,1,9216
v_and_b32 v63,0xffff0000,v60
M 14,a[44:47],46
s_lshl_b32 s62,s54,15
v_add_f32 v23,v23,v63
v_lshlrev_b32 v62,16,v61
M 10,a[32:35],42
s_mov_b32 s66,s65
R 34,1,10240
v_add_f32 v24,v24,v62
M 10,a[40:43],46
L 3
v_and_b32 v63,0xffff0000,v61
v_add_f32 v25,v25,v63
M 10,a[48:51],50
s_add_u32 s65,s47,s62
R 38,1,11264
v_cvt_pk_bf16_f32 v18,v18,v19
M 10,a[56:59],54
buffer_load_dwordx4 v[58:61],v0,s[36:39],s74 offen offset:0 sc1
M 14,a[52:55],50
s_add_u32 s74,s49,s62
R 42,1,12288
v_cvt_pk_bf16_f32 v19,v20,v21
M 14,a[60:63],54
L 3
v_cvt_pk_bf16_f32 v20,v22,v23
v_cvt_pk_bf16_f32 v21,v24,v25
M 14,a[68:71],26
R 46,1,13312
v_lshrrev_b32 v62,8,v0
M 14,a[76:79],30
v_lshl_add_u32 v62,v62,5,v0
v_add_u32 v62,s73,v62
M 10,a[64:67],26
R 50,1,14336
M 10,a[72:75],30
L 3
ds_write_b128 v62,v[18:21] offset:0
M 10,a[80:83],34
R 54,1,15360
ds_read2_b32 v[22:23],v5 offset0:0 offset1:16
M 10,a[88:91],38
ds_read2_b32 v[24:25],v5 offset0:32 offset1:48
M 14,a[84:87],34
R 26,1,16384
M 14,a[92:95],38
L 6
M 14,a[100:103],42
R 30,1,17408
M 14,a[108:111],46
M 10,a[96:99],42
R 34,1,18432
M 10,a[104:107],46
L 5
L 3
M 10,a[112:115],50
R 38,1,19456
M 10,a[120:123],54
buffer_atomic_pk_add_bf16 v22,v9,s[40:43],s64 offen sc1
M 14,a[116:119],50
s_add_u32 s64,s75,8192
R 42,1,20480
M 14,a[124:127],54
L 3
buffer_atomic_pk_add_bf16 v23,v9,s[40:43],s64 offen sc1
M 14,a[132:135],26
s_add_u32 s64,s75,16384
R 46,1,21504
M 14,a[140:143],30
buffer_atomic_pk_add_bf16 v24,v9,s[40:43],s64 offen sc1
M 10,a[128:131],26
s_add_u32 s64,s75,24576
R 50,1,22528
M 10,a[136:139],30
L 3
buffer_atomic_pk_add_bf16 v25,v9,s[40:43],s64 offen sc1
M 10,a[144:147],34
R 54,1,23552
M 10,a[152:155],38
M 14,a[148:151],34
R 26,1,24576
M 14,a[156:159],38
L 3
M 14,a[164:167],42
R 30,1,25600
M 14,a[172:175],46
M 10,a[160:163],42
R 34,1,26624
M 10,a[168:171],46
L 3
M 10,a[176:179],50
s_waitcnt vmcnt(5)
R 38,1,27648
M 10,a[184:187],54
s_barrier
ds_read_b32 v7,v6 offset:3072
M 14,a[180:183],50
R 42,1,28672
M 14,a[188:191],54
L 4
M 14,a[196:199],26
R 46,1,29696
M 14,a[204:207],30
M 10,a[192:195],26
R 50,1,30720
M 10,a[200:203],30
L 4
L 3
v_cmp_eq_u32 vcc,s53,v7
M 10,a[208:211],34
R 54,1,31744
M 10,a[216:219],38
M 14,a[212:215],34
s_cmp_eq_u32 vcc_lo,-1
R 26,1,32768
s_cbranch_scc0 Lg4_Xa0_%=
M 14,a[220:223],38
L 3
s_add_u32 m0,s57,0
s_add_u32 s56,s55,0
M 14,a[228:231],42
s_add_u32 s61,s54,s46
s_cmp_ge_u32 s61,s63
s_cselect_b32 s61,s93,s61
R 30,1,33792
M 14,a[236:239],46
s_mul_i32 s58,s61,2560
D 0,56,0
M 10,a[224:227],42
s_cselect_b32 s46,s88,s46
s_cselect_b32 s63,s89,s63
R 34,1,34816
M 10,a[232:235],46
L 3
s_cselect_b32 s93,s50,s93
s_cselect_b32 s88,8,s88
M 10,a[240:243],50
s_cselect_b32 s89,-1,s89
s_add_u32 s58,s58,109891584
R 38,1,35840
M 10,a[248:251],54
D 0,56,1024
M 14,a[244:247],50
s_mov_b32 s54,s61
R 42,1,36864
M 14,a[252:255],54
L 3
M 14,v[68:71],26
R 46,1,37888
M 14,v[76:79],30
D 0,56,2048
M 10,v[64:67],26
R 50,1,38912
M 10,v[72:75],30
L 3
M 10,v[80:83],34
R 54,1,39936
M 10,v[88:91],38
D 0,56,3072
M 14,v[84:87],34
s_add_u32 m0,s57,4096
s_add_u32 s56,s56,4096
R 26,1,40960
M 14,v[92:95],38
L 3
M 14,v[100:103],42
R 30,1,41984
M 14,v[108:111],46
D 0,56,0
M 10,v[96:99],42
R 34,1,43008
M 10,v[104:107],46
L 3
M 10,v[112:115],50
R 38,1,44032
M 10,v[120:123],54
D 0,56,1024
M 14,v[116:119],50
R 42,1,45056
M 14,v[124:127],54
L 3
M 14,v[132:135],26
R 46,1,46080
M 14,v[140:143],30
D 0,56,2048
M 10,v[128:131],26
s_add_u32 m0,s59,0
s_add_u32 s56,s55,28672
R 50,1,47104
M 10,v[136:139],30
L 3
buffer_load_dword v8,s[36:39],s58 offen offset:0 sc1 lds
M 10,v[144:147],34
s_add_u32 m0,s57,28672
s_mul_i32 s55,s61,114688
R 54,1,48128
M 10,v[152:155],38
s_add_u32 s55,s55,s60
M 14,v[148:151],34
R 26,1,49152
M 14,v[156:159],38
L 3
D 0,56,0
M 14,v[164:167],42
R 30,1,50176
M 14,v[172:175],46
M 10,v[160:163],42
R 34,1,51200
M 10,v[168:171],46
L 3
D 0,56,1024
M 10,v[176:179],50
R 38,1,52224
M 10,v[184:187],54
D 0,56,2048
M 14,v[180:183],50
R 42,1,53248
M 14,v[188:191],54
L 3
M 14,v[196:199],26
R 46,1,54272
M 14,v[204:207],30
D 0,56,3072
M 10,v[192:195],26
s_add_u32 m0,s57,32768
s_add_u32 s56,s56,4096
R 50,1,55296
M 10,v[200:203],30
L 3
s_sub_u32 s57,s84,s57
M 10,v[208:211],34
s_waitcnt vmcnt(4)
R 54,1,56320
M 10,v[216:219],38
s_barrier
ds_read_b32 v7,v6 offset:0
M 14,v[212:215],34
R 26,2,0
M 14,v[220:223],38
L 4
D 0,56,0
M 14,v[228:231],42
R 30,2,1024
M 14,v[236:239],46
D 0,56,1024
M 10,v[224:227],42
R 34,2,2048
M 10,v[232:235],46
L 4
D 0,56,2048
M 10,v[240:243],50
L 3
R 38,2,3072
v_cmp_eq_u32 vcc,s53,v7
M 10,v[248:251],54
M 14,v[244:247],50
L 2
s_cmp_eq_u32 vcc_lo,-1
R 42,2,4096
M 14,v[252:255],54
buffer_store_dword v4,v3,s[36:39],s71 offen offset:0 sc0 sc1
buffer_store_dword v4,v3,s[36:39],s71 offen offset:0 sc0 sc1
buffer_store_dword v4,v3,s[36:39],s71 offen offset:0 sc0 sc1
buffer_store_dword v4,v3,s[36:39],s71 offen offset:0 sc0 sc1
s_cbranch_scc1 Lg4_Fa1_%=
s_branch Lg4_Ka1_%=
Lg4_Xa0_%=:
M 14,a[220:223],38
L 3
s_add_u32 m0,s59,0
M 14,a[228:231],42
R 30,1,33792
M 14,a[236:239],46
buffer_load_dword v8,s[36:39],s58 offen offset:0 sc1 lds
M 10,a[224:227],42
R 34,1,34816
M 10,a[232:235],46
L 3
M 10,a[240:243],50
R 38,1,35840
M 10,a[248:251],54
M 14,a[244:247],50
R 42,1,36864
M 14,a[252:255],54
L 3
M 14,v[68:71],26
R 46,1,37888
M 14,v[76:79],30
M 10,v[64:67],26
R 50,1,38912
M 10,v[72:75],30
L 3
)"
R"(M 10,v[80:83],34
R 54,1,39936
M 10,v[88:91],38
M 14,v[84:87],34
R 26,1,40960
M 14,v[92:95],38
L 3
M 14,v[100:103],42
R 30,1,41984
M 14,v[108:111],46
M 10,v[96:99],42
R 34,1,43008
M 10,v[104:107],46
L 3
M 10,v[112:115],50
R 38,1,44032
M 10,v[120:123],54
M 14,v[116:119],50
R 42,1,45056
M 14,v[124:127],54
L 3
M 14,v[132:135],26
R 46,1,46080
M 14,v[140:143],30
M 10,v[128:131],26
R 50,1,47104
M 10,v[136:139],30
L 3
M 10,v[144:147],34
R 54,1,48128
M 10,v[152:155],38
M 14,v[148:151],34
R 26,1,49152
M 14,v[156:159],38
L 3
M 14,v[164:167],42
R 30,1,50176
M 14,v[172:175],46
M 10,v[160:163],42
R 34,1,51200
M 10,v[168:171],46
L 3
M 10,v[176:179],50
R 38,1,52224
M 10,v[184:187],54
M 14,v[180:183],50
R 42,1,53248
M 14,v[188:191],54
L 3
M 14,v[196:199],26
R 46,1,54272
M 14,v[204:207],30
M 10,v[192:195],26
R 50,1,55296
M 10,v[200:203],30
L 3
M 10,v[208:211],34
s_waitcnt vmcnt(0)
R 54,1,56320
M 10,v[216:219],38
s_barrier
ds_read_b32 v7,v6 offset:0
M 14,v[212:215],34
M 14,v[220:223],38
L 3
M 14,v[228:231],42
M 14,v[236:239],46
M 10,v[224:227],42
M 10,v[232:235],46
L 1
L 0
M 10,v[240:243],50
v_cmp_eq_u32 vcc,s53,v7
M 10,v[248:251],54
M 14,v[244:247],50
M 14,v[252:255],54
Lg4_pa0_%=:
s_mov_b32 s80,1
s_cmp_eq_u32 vcc_lo,-1
s_cbranch_scc0 Lg4_xna0_%=
s_add_u32 m0,s57,0
s_add_u32 s56,s55,0
D 0,56,0
s_sub_u32 s75,s66,s85
v_lshlrev_b32 v62,16,v58
D 0,56,1024
v_add_f32 v10,v10,v62
D 0,56,2048
v_and_b32 v63,0xffff0000,v58
D 0,56,3072
v_add_f32 v11,v11,v63
s_add_u32 m0,s57,4096
s_add_u32 s56,s56,4096
D 0,56,0
v_lshlrev_b32 v62,16,v59
D 0,56,1024
v_add_f32 v12,v12,v62
D 0,56,2048
v_and_b32 v63,0xffff0000,v59
s_add_u32 s61,s54,s46
s_cmp_ge_u32 s61,s63
s_cselect_b32 s61,s93,s61
s_cselect_b32 s46,s88,s46
s_cselect_b32 s63,s89,s63
s_cselect_b32 s93,s50,s93
s_cselect_b32 s88,8,s88
s_cselect_b32 s89,-1,s89
s_mul_i32 s58,s61,2560
s_add_u32 s58,s58,109891584
s_add_u32 m0,s57,28672
s_add_u32 s56,s55,28672
D 0,56,0
v_add_f32 v13,v13,v63
D 0,56,1024
v_lshlrev_b32 v62,16,v60
D 0,56,2048
v_add_f32 v14,v14,v62
D 0,56,3072
v_and_b32 v63,0xffff0000,v60
s_add_u32 m0,s57,32768
s_add_u32 s56,s56,4096
D 0,56,0
v_add_f32 v15,v15,v63
D 0,56,1024
v_lshlrev_b32 v62,16,v61
D 0,56,2048
v_add_f32 v16,v16,v62
s_mul_i32 s55,s61,114688
s_add_u32 s55,s55,s60
s_sub_u32 s57,s84,s57
s_mov_b32 s54,s61
v_and_b32 v63,0xffff0000,v61
v_add_f32 v17,v17,v63
v_cvt_pk_bf16_f32 v10,v10,v11
v_cvt_pk_bf16_f32 v11,v12,v13
v_cvt_pk_bf16_f32 v12,v14,v15
v_cvt_pk_bf16_f32 v13,v16,v17
v_lshrrev_b32 v62,8,v0
v_lshl_add_u32 v62,v62,5,v0
v_add_u32 v62,s73,v62
ds_write_b128 v62,v[10:13] offset:0
ds_read2_b32 v[14:15],v5 offset0:0 offset1:16
ds_read2_b32 v[16:17],v5 offset0:32 offset1:48
s_mov_b64 s[40:41],s[86:87]
s_mov_b32 s42,4194304
s_mov_b32 s43,0x00020000
L 0
s_add_u32 s64,s75,0
buffer_atomic_pk_add_bf16 v14,v9,s[40:43],s64 offen sc1
s_add_u32 s64,s75,8192
buffer_atomic_pk_add_bf16 v15,v9,s[40:43],s64 offen sc1
s_add_u32 s64,s75,16384
buffer_atomic_pk_add_bf16 v16,v9,s[40:43],s64 offen sc1
s_add_u32 s64,s75,24576
buffer_atomic_pk_add_bf16 v17,v9,s[40:43],s64 offen sc1
s_mov_b32 s72,0x7fff0000
s_mov_b32 s69,0x7fff0000
s_mov_b32 s66,0x7fff0000
s_mov_b32 s76,0x7fff0000
s_branch Lg4_gox_%=
Lg4_xna0_%=:
s_cmp_ge_u32 s54,256
s_cbranch_scc1 Lg4_fina0_%=
s_add_u32 m0,s59,4096
s_nop 0
buffer_load_dword v8,s[36:39],s58 offen offset:0 sc1 lds
buffer_store_dword v4,v3,s[36:39],s71 offen offset:0 sc0 sc1
s_add_u32 m0,s59,5120
s_nop 0
buffer_load_dword v8,s[36:39],s58 offen offset:0 sc1 lds
buffer_store_dword v4,v3,s[36:39],s71 offen offset:0 sc0 sc1
s_mov_b32 s83,0
s_sub_u32 s75,s66,s85
v_lshlrev_b32 v62,16,v58
v_add_f32 v10,v10,v62
v_and_b32 v63,0xffff0000,v58
v_add_f32 v11,v11,v63
v_lshlrev_b32 v62,16,v59
v_add_f32 v12,v12,v62
v_and_b32 v63,0xffff0000,v59
v_add_f32 v13,v13,v63
v_lshlrev_b32 v62,16,v60
v_add_f32 v14,v14,v62
v_and_b32 v63,0xffff0000,v60
v_add_f32 v15,v15,v63
v_lshlrev_b32 v62,16,v61
v_add_f32 v16,v16,v62
v_and_b32 v63,0xffff0000,v61
v_add_f32 v17,v17,v63
v_cvt_pk_bf16_f32 v10,v10,v11
v_cvt_pk_bf16_f32 v11,v12,v13
v_cvt_pk_bf16_f32 v12,v14,v15
v_cvt_pk_bf16_f32 v13,v16,v17
v_lshrrev_b32 v62,8,v0
v_lshl_add_u32 v62,v62,5,v0
v_add_u32 v62,s73,v62
ds_write_b128 v62,v[10:13] offset:0
ds_read2_b32 v[14:15],v5 offset0:0 offset1:16
ds_read2_b32 v[16:17],v5 offset0:32 offset1:48
s_mov_b64 s[40:41],s[86:87]
s_mov_b32 s42,4194304
s_mov_b32 s43,0x00020000
L 0
s_add_u32 s64,s75,0
buffer_atomic_pk_add_bf16 v14,v9,s[40:43],s64 offen sc1
s_add_u32 s64,s75,8192
buffer_atomic_pk_add_bf16 v15,v9,s[40:43],s64 offen sc1
s_add_u32 s64,s75,16384
buffer_atomic_pk_add_bf16 v16,v9,s[40:43],s64 offen sc1
s_add_u32 s64,s75,24576
buffer_atomic_pk_add_bf16 v17,v9,s[40:43],s64 offen sc1
s_mov_b32 s72,0x7fff0000
s_mov_b32 s69,0x7fff0000
s_mov_b32 s66,0x7fff0000
s_mov_b32 s76,0x7fff0000
s_branch Lg4_poll_%=
Lg4_fina0_%=:
s_sub_u32 s75,s66,s85
v_lshlrev_b32 v62,16,v58
v_add_f32 v10,v10,v62
v_and_b32 v63,0xffff0000,v58
v_add_f32 v11,v11,v63
v_lshlrev_b32 v62,16,v59
v_add_f32 v12,v12,v62
v_and_b32 v63,0xffff0000,v59
v_add_f32 v13,v13,v63
v_lshlrev_b32 v62,16,v60
v_add_f32 v14,v14,v62
v_and_b32 v63,0xffff0000,v60
v_add_f32 v15,v15,v63
v_lshlrev_b32 v62,16,v61
v_add_f32 v16,v16,v62
v_and_b32 v63,0xffff0000,v61
v_add_f32 v17,v17,v63
v_cvt_pk_bf16_f32 v10,v10,v11
v_cvt_pk_bf16_f32 v11,v12,v13
v_cvt_pk_bf16_f32 v12,v14,v15
v_cvt_pk_bf16_f32 v13,v16,v17
v_lshrrev_b32 v62,8,v0
v_lshl_add_u32 v62,v62,5,v0
v_add_u32 v62,s73,v62
ds_write_b128 v62,v[10:13] offset:0
ds_read2_b32 v[14:15],v5 offset0:0 offset1:16
ds_read2_b32 v[16:17],v5 offset0:32 offset1:48
s_mov_b64 s[40:41],s[86:87]
s_mov_b32 s42,4194304
s_mov_b32 s43,0x00020000
L 0
s_add_u32 s64,s75,0
buffer_atomic_pk_add_bf16 v14,v9,s[40:43],s64 offen sc1
s_add_u32 s64,s75,8192
buffer_atomic_pk_add_bf16 v15,v9,s[40:43],s64 offen sc1
s_add_u32 s64,s75,16384
buffer_atomic_pk_add_bf16 v16,v9,s[40:43],s64 offen sc1
s_add_u32 s64,s75,24576
buffer_atomic_pk_add_bf16 v17,v9,s[40:43],s64 offen sc1
s_mov_b32 s72,0x7fff0000
s_mov_b32 s69,0x7fff0000
s_mov_b32 s66,0x7fff0000
s_mov_b32 s76,0x7fff0000
s_branch Lg4_done_%=
Lg4_Ka1_%=:
Lg4_La1_%=:
s_nop 0
v_mfma_f32_16x16x32_bf16 v[22:25],a[4:7],v[26:29],0
s_add_u32 m0,s59,3072
R 46,2,5120
v_lshlrev_b32 v62,16,v58
M 22,a[12:15],30
buffer_load_dword v8,s[36:39],s58 offen offset:0 sc1 lds
v_mfma_f32_16x16x32_bf16 v[18:21],a[0:3],v[26:29],0
s_sub_u32 s75,s66,s85
R 50,2,6144
v_add_f32 v10,v10,v62
M 18,a[8:11],30
L 3
v_and_b32 v63,0xffff0000,v58
v_add_f32 v11,v11,v63
M 18,a[16:19],34
s_mov_b64 s[40:41],s[86:87]
R 54,2,7168
v_lshlrev_b32 v62,16,v59
M 18,a[24:27],38
s_mov_b32 s42,4194304
v_add_f32 v12,v12,v62
v_and_b32 v63,0xffff0000,v59
M 22,a[20:23],34
s_mov_b32 s43,0x00020000
R 26,2,8192
v_add_f32 v13,v13,v63
M 22,a[28:31],38
L 3
v_lshlrev_b32 v62,16,v60
v_add_f32 v14,v14,v62
M 22,a[36:39],42
s_add_u32 s64,s75,0
R 30,2,9216
v_and_b32 v63,0xffff0000,v60
M 22,a[44:47],46
s_lshl_b32 s62,s54,15
v_add_f32 v15,v15,v63
v_lshlrev_b32 v62,16,v61
M 18,a[32:35],42
s_mov_b32 s67,s65
R 34,2,10240
v_add_f32 v16,v16,v62
M 18,a[40:43],46
L 3
v_and_b32 v63,0xffff0000,v61
v_add_f32 v17,v17,v63
M 18,a[48:51],50
s_add_u32 s65,s47,s62
R 38,2,11264
v_cvt_pk_bf16_f32 v10,v10,v11
M 18,a[56:59],54
buffer_load_dwordx4 v[58:61],v0,s[36:39],s74 offen offset:0 sc1
M 22,a[52:55],50
s_add_u32 s74,s49,s62
R 42,2,12288
v_cvt_pk_bf16_f32 v11,v12,v13
M 22,a[60:63],54
L 3
v_cvt_pk_bf16_f32 v12,v14,v15
v_cvt_pk_bf16_f32 v13,v16,v17
M 22,a[68:71],26
R 46,2,13312
v_lshrrev_b32 v62,8,v0
M 22,a[76:79],30
v_lshl_add_u32 v62,v62,5,v0
v_add_u32 v62,s73,v62
M 18,a[64:67],26
R 50,2,14336
M 18,a[72:75],30
L 3
ds_write_b128 v62,v[10:13] offset:0
M 18,a[80:83],34
R 54,2,15360
ds_read2_b32 v[14:15],v5 offset0:0 offset1:16
M 18,a[88:91],38
ds_read2_b32 v[16:17],v5 offset0:32 offset1:48
M 22,a[84:87],34
R 26,2,16384
M 22,a[92:95],38
L 6
M 22,a[100:103],42
R 30,2,17408
M 22,a[108:111],46
M 18,a[96:99],42
R 34,2,18432
M 18,a[104:107],46
L 5
L 3
M 18,a[112:115],50
R 38,2,19456
M 18,a[120:123],54
buffer_atomic_pk_add_bf16 v14,v9,s[40:43],s64 offen sc1
M 22,a[116:119],50
s_add_u32 s64,s75,8192
R 42,2,20480
M 22,a[124:127],54
L 3
buffer_atomic_pk_add_bf16 v15,v9,s[40:43],s64 offen sc1
M 22,a[132:135],26
s_add_u32 s64,s75,16384
R 46,2,21504
M 22,a[140:143],30
buffer_atomic_pk_add_bf16 v16,v9,s[40:43],s64 offen sc1
M 18,a[128:131],26
s_add_u32 s64,s75,24576
R 50,2,22528
M 18,a[136:139],30
L 3
buffer_atomic_pk_add_bf16 v17,v9,s[40:43],s64 offen sc1
M 18,a[144:147],34
R 54,2,23552
M 18,a[152:155],38
M 22,a[148:151],34
R 26,2,24576
M 22,a[156:159],38
L 3
M 22,a[164:167],42
R 30,2,25600
M 22,a[172:175],46
M 18,a[160:163],42
R 34,2,26624
M 18,a[168:171],46
L 3
M 18,a[176:179],50
s_waitcnt vmcnt(5)
R 38,2,27648
M 18,a[184:187],54
s_barrier
ds_read_b32 v7,v6 offset:3072
M 22,a[180:183],50
R 42,2,28672
M 22,a[188:191],54
L 4
M 22,a[196:199],26
R 46,2,29696
M 22,a[204:207],30
M 18,a[192:195],26
R 50,2,30720
M 18,a[200:203],30
L 4
L 3
v_cmp_eq_u32 vcc,s53,v7
M 18,a[208:211],34
R 54,2,31744
M 18,a[216:219],38
M 22,a[212:215],34
s_cmp_eq_u32 vcc_lo,-1
R 26,2,32768
s_cbranch_scc0 Lg4_Xa1_%=
M 22,a[220:223],38
L 3
s_add_u32 m0,s57,0
s_add_u32 s56,s55,0
M 22,a[228:231],42
s_add_u32 s61,s54,s46
s_cmp_ge_u32 s61,s63
s_cselect_b32 s61,s93,s61
R 30,2,33792
M 22,a[236:239],46
s_mul_i32 s58,s61,2560
D 0,56,0
M 18,a[224:227],42
s_cselect_b32 s46,s88,s46
s_cselect_b32 s63,s89,s63
R 34,2,34816
M 18,a[232:235],46
L 3
s_cselect_b32 s93,s50,s93
s_cselect_b32 s88,8,s88
M 18,a[240:243],50
s_cselect_b32 s89,-1,s89
s_add_u32 s58,s58,109891584
R 38,2,35840
M 18,a[248:251],54
D 0,56,1024
M 22,a[244:247],50
s_mov_b32 s54,s61
R 42,2,36864
M 22,a[252:255],54
L 3
M 22,v[68:71],26
R 46,2,37888
M 22,v[76:79],30
D 0,56,2048
M 18,v[64:67],26
R 50,2,38912
M 18,v[72:75],30
L 3
M 18,v[80:83],34
R 54,2,39936
M 18,v[88:91],38
D 0,56,3072
M 22,v[84:87],34
s_add_u32 m0,s57,4096
s_add_u32 s56,s56,4096
R 26,2,40960
M 22,v[92:95],38
L 3
M 22,v[100:103],42
R 30,2,41984
M 22,v[108:111],46
D 0,56,0
M 18,v[96:99],42
R 34,2,43008
M 18,v[104:107],46
L 3
M 18,v[112:115],50
R 38,2,44032
M 18,v[120:123],54
D 0,56,1024
M 22,v[116:119],50
R 42,2,45056
M 22,v[124:127],54
L 3
M 22,v[132:135],26
R 46,2,46080
M 22,v[140:143],30
D 0,56,2048
M 18,v[128:131],26
s_add_u32 m0,s59,1024
s_add_u32 s56,s55,28672
R 50,2,47104
M 18,v[136:139],30
L 3
buffer_load_dword v8,s[36:39],s58 offen offset:0 sc1 lds
M 18,v[144:147],34
s_add_u32 m0,s57,28672
s_mul_i32 s55,s61,114688
R 54,2,48128
M 18,v[152:155],38
s_add_u32 s55,s55,s60
M 22,v[148:151],34
R 26,2,49152
M 22,v[156:159],38
L 3
D 0,56,0
)"
R"(M 22,v[164:167],42
R 30,2,50176
M 22,v[172:175],46
M 18,v[160:163],42
R 34,2,51200
M 18,v[168:171],46
L 3
D 0,56,1024
M 18,v[176:179],50
R 38,2,52224
M 18,v[184:187],54
D 0,56,2048
M 22,v[180:183],50
R 42,2,53248
M 22,v[188:191],54
L 3
M 22,v[196:199],26
R 46,2,54272
M 22,v[204:207],30
D 0,56,3072
M 18,v[192:195],26
s_add_u32 m0,s57,32768
s_add_u32 s56,s56,4096
R 50,2,55296
M 18,v[200:203],30
L 3
s_sub_u32 s57,s84,s57
M 18,v[208:211],34
s_waitcnt vmcnt(4)
R 54,2,56320
M 18,v[216:219],38
s_barrier
ds_read_b32 v7,v6 offset:1024
M 22,v[212:215],34
R 26,1,0
M 22,v[220:223],38
L 4
D 0,56,0
M 22,v[228:231],42
R 30,1,1024
M 22,v[236:239],46
D 0,56,1024
M 18,v[224:227],42
R 34,1,2048
M 18,v[232:235],46
L 4
D 0,56,2048
M 18,v[240:243],50
L 3
R 38,1,3072
v_cmp_eq_u32 vcc,s53,v7
M 18,v[248:251],54
M 22,v[244:247],50
L 2
s_cmp_eq_u32 vcc_lo,-1
R 42,1,4096
M 22,v[252:255],54
buffer_store_dword v4,v3,s[36:39],s71 offen offset:0 sc0 sc1
buffer_store_dword v4,v3,s[36:39],s71 offen offset:0 sc0 sc1
buffer_store_dword v4,v3,s[36:39],s71 offen offset:0 sc0 sc1
buffer_store_dword v4,v3,s[36:39],s71 offen offset:0 sc0 sc1
s_cbranch_scc1 Lg4_Fa0_%=
s_branch Lg4_Ka0_%=
Lg4_Xa1_%=:
M 22,a[220:223],38
L 3
s_add_u32 m0,s59,1024
M 22,a[228:231],42
R 30,2,33792
M 22,a[236:239],46
buffer_load_dword v8,s[36:39],s58 offen offset:0 sc1 lds
M 18,a[224:227],42
R 34,2,34816
M 18,a[232:235],46
L 3
M 18,a[240:243],50
R 38,2,35840
M 18,a[248:251],54
M 22,a[244:247],50
R 42,2,36864
M 22,a[252:255],54
L 3
M 22,v[68:71],26
R 46,2,37888
M 22,v[76:79],30
M 18,v[64:67],26
R 50,2,38912
M 18,v[72:75],30
L 3
M 18,v[80:83],34
R 54,2,39936
M 18,v[88:91],38
M 22,v[84:87],34
R 26,2,40960
M 22,v[92:95],38
L 3
M 22,v[100:103],42
R 30,2,41984
M 22,v[108:111],46
M 18,v[96:99],42
R 34,2,43008
M 18,v[104:107],46
L 3
M 18,v[112:115],50
R 38,2,44032
M 18,v[120:123],54
M 22,v[116:119],50
R 42,2,45056
M 22,v[124:127],54
L 3
M 22,v[132:135],26
R 46,2,46080
M 22,v[140:143],30
M 18,v[128:131],26
R 50,2,47104
M 18,v[136:139],30
L 3
M 18,v[144:147],34
R 54,2,48128
M 18,v[152:155],38
M 22,v[148:151],34
R 26,2,49152
M 22,v[156:159],38
L 3
M 22,v[164:167],42
R 30,2,50176
M 22,v[172:175],46
M 18,v[160:163],42
R 34,2,51200
M 18,v[168:171],46
L 3
M 18,v[176:179],50
R 38,2,52224
M 18,v[184:187],54
M 22,v[180:183],50
R 42,2,53248
M 22,v[188:191],54
L 3
M 22,v[196:199],26
R 46,2,54272
M 22,v[204:207],30
M 18,v[192:195],26
R 50,2,55296
M 18,v[200:203],30
L 3
M 18,v[208:211],34
s_waitcnt vmcnt(0)
R 54,2,56320
M 18,v[216:219],38
s_barrier
ds_read_b32 v7,v6 offset:1024
M 22,v[212:215],34
M 22,v[220:223],38
L 3
M 22,v[228:231],42
M 22,v[236:239],46
M 18,v[224:227],42
M 18,v[232:235],46
L 1
L 0
M 18,v[240:243],50
v_cmp_eq_u32 vcc,s53,v7
M 18,v[248:251],54
M 22,v[244:247],50
M 22,v[252:255],54
Lg4_pa1_%=:
s_mov_b32 s80,0
s_cmp_eq_u32 vcc_lo,-1
s_cbranch_scc0 Lg4_xna1_%=
s_add_u32 m0,s57,0
s_add_u32 s56,s55,0
D 0,56,0
s_sub_u32 s75,s67,s85
v_lshlrev_b32 v62,16,v58
D 0,56,1024
v_add_f32 v18,v18,v62
D 0,56,2048
v_and_b32 v63,0xffff0000,v58
D 0,56,3072
v_add_f32 v19,v19,v63
s_add_u32 m0,s57,4096
s_add_u32 s56,s56,4096
D 0,56,0
v_lshlrev_b32 v62,16,v59
D 0,56,1024
v_add_f32 v20,v20,v62
D 0,56,2048
v_and_b32 v63,0xffff0000,v59
s_add_u32 s61,s54,s46
s_cmp_ge_u32 s61,s63
s_cselect_b32 s61,s93,s61
s_cselect_b32 s46,s88,s46
s_cselect_b32 s63,s89,s63
s_cselect_b32 s93,s50,s93
s_cselect_b32 s88,8,s88
s_cselect_b32 s89,-1,s89
s_mul_i32 s58,s61,2560
s_add_u32 s58,s58,109891584
s_add_u32 m0,s57,28672
s_add_u32 s56,s55,28672
D 0,56,0
v_add_f32 v21,v21,v63
D 0,56,1024
v_lshlrev_b32 v62,16,v60
D 0,56,2048
v_add_f32 v22,v22,v62
D 0,56,3072
v_and_b32 v63,0xffff0000,v60
s_add_u32 m0,s57,32768
s_add_u32 s56,s56,4096
D 0,56,0
v_add_f32 v23,v23,v63
D 0,56,1024
v_lshlrev_b32 v62,16,v61
D 0,56,2048
v_add_f32 v24,v24,v62
s_mul_i32 s55,s61,114688
s_add_u32 s55,s55,s60
s_sub_u32 s57,s84,s57
s_mov_b32 s54,s61
v_and_b32 v63,0xffff0000,v61
v_add_f32 v25,v25,v63
v_cvt_pk_bf16_f32 v18,v18,v19
v_cvt_pk_bf16_f32 v19,v20,v21
v_cvt_pk_bf16_f32 v20,v22,v23
v_cvt_pk_bf16_f32 v21,v24,v25
v_lshrrev_b32 v62,8,v0
v_lshl_add_u32 v62,v62,5,v0
v_add_u32 v62,s73,v62
ds_write_b128 v62,v[18:21] offset:0
ds_read2_b32 v[22:23],v5 offset0:0 offset1:16
ds_read2_b32 v[24:25],v5 offset0:32 offset1:48
s_mov_b64 s[40:41],s[86:87]
s_mov_b32 s42,4194304
s_mov_b32 s43,0x00020000
L 0
s_add_u32 s64,s75,0
buffer_atomic_pk_add_bf16 v22,v9,s[40:43],s64 offen sc1
s_add_u32 s64,s75,8192
buffer_atomic_pk_add_bf16 v23,v9,s[40:43],s64 offen sc1
s_add_u32 s64,s75,16384
buffer_atomic_pk_add_bf16 v24,v9,s[40:43],s64 offen sc1
s_add_u32 s64,s75,24576
buffer_atomic_pk_add_bf16 v25,v9,s[40:43],s64 offen sc1
s_mov_b32 s72,0x7fff0000
s_mov_b32 s69,0x7fff0000
s_mov_b32 s67,0x7fff0000
s_mov_b32 s77,0x7fff0000
s_branch Lg4_gox_%=
Lg4_xna1_%=:
s_cmp_ge_u32 s54,256
s_cbranch_scc1 Lg4_fina1_%=
s_add_u32 m0,s59,4096
s_nop 0
buffer_load_dword v8,s[36:39],s58 offen offset:0 sc1 lds
buffer_store_dword v4,v3,s[36:39],s71 offen offset:0 sc0 sc1
s_add_u32 m0,s59,5120
s_nop 0
buffer_load_dword v8,s[36:39],s58 offen offset:0 sc1 lds
buffer_store_dword v4,v3,s[36:39],s71 offen offset:0 sc0 sc1
s_mov_b32 s83,0
s_sub_u32 s75,s67,s85
v_lshlrev_b32 v62,16,v58
v_add_f32 v18,v18,v62
v_and_b32 v63,0xffff0000,v58
v_add_f32 v19,v19,v63
v_lshlrev_b32 v62,16,v59
v_add_f32 v20,v20,v62
v_and_b32 v63,0xffff0000,v59
v_add_f32 v21,v21,v63
v_lshlrev_b32 v62,16,v60
v_add_f32 v22,v22,v62
v_and_b32 v63,0xffff0000,v60
v_add_f32 v23,v23,v63
v_lshlrev_b32 v62,16,v61
v_add_f32 v24,v24,v62
v_and_b32 v63,0xffff0000,v61
v_add_f32 v25,v25,v63
v_cvt_pk_bf16_f32 v18,v18,v19
v_cvt_pk_bf16_f32 v19,v20,v21
v_cvt_pk_bf16_f32 v20,v22,v23
v_cvt_pk_bf16_f32 v21,v24,v25
v_lshrrev_b32 v62,8,v0
v_lshl_add_u32 v62,v62,5,v0
v_add_u32 v62,s73,v62
ds_write_b128 v62,v[18:21] offset:0
ds_read2_b32 v[22:23],v5 offset0:0 offset1:16
ds_read2_b32 v[24:25],v5 offset0:32 offset1:48
s_mov_b64 s[40:41],s[86:87]
s_mov_b32 s42,4194304
s_mov_b32 s43,0x00020000
L 0
s_add_u32 s64,s75,0
buffer_atomic_pk_add_bf16 v22,v9,s[40:43],s64 offen sc1
s_add_u32 s64,s75,8192
buffer_atomic_pk_add_bf16 v23,v9,s[40:43],s64 offen sc1
s_add_u32 s64,s75,16384
buffer_atomic_pk_add_bf16 v24,v9,s[40:43],s64 offen sc1
s_add_u32 s64,s75,24576
buffer_atomic_pk_add_bf16 v25,v9,s[40:43],s64 offen sc1
s_mov_b32 s72,0x7fff0000
s_mov_b32 s69,0x7fff0000
s_mov_b32 s67,0x7fff0000
s_mov_b32 s77,0x7fff0000
s_branch Lg4_poll_%=
Lg4_fina1_%=:
s_sub_u32 s75,s67,s85
v_lshlrev_b32 v62,16,v58
v_add_f32 v18,v18,v62
v_and_b32 v63,0xffff0000,v58
v_add_f32 v19,v19,v63
v_lshlrev_b32 v62,16,v59
v_add_f32 v20,v20,v62
v_and_b32 v63,0xffff0000,v59
v_add_f32 v21,v21,v63
v_lshlrev_b32 v62,16,v60
v_add_f32 v22,v22,v62
v_and_b32 v63,0xffff0000,v60
v_add_f32 v23,v23,v63
v_lshlrev_b32 v62,16,v61
v_add_f32 v24,v24,v62
v_and_b32 v63,0xffff0000,v61
v_add_f32 v25,v25,v63
v_cvt_pk_bf16_f32 v18,v18,v19
v_cvt_pk_bf16_f32 v19,v20,v21
v_cvt_pk_bf16_f32 v20,v22,v23
v_cvt_pk_bf16_f32 v21,v24,v25
v_lshrrev_b32 v62,8,v0
v_lshl_add_u32 v62,v62,5,v0
v_add_u32 v62,s73,v62
ds_write_b128 v62,v[18:21] offset:0
ds_read2_b32 v[22:23],v5 offset0:0 offset1:16
ds_read2_b32 v[24:25],v5 offset0:32 offset1:48
s_mov_b64 s[40:41],s[86:87]
s_mov_b32 s42,4194304
s_mov_b32 s43,0x00020000
L 0
s_add_u32 s64,s75,0
buffer_atomic_pk_add_bf16 v22,v9,s[40:43],s64 offen sc1
s_add_u32 s64,s75,8192
buffer_atomic_pk_add_bf16 v23,v9,s[40:43],s64 offen sc1
s_add_u32 s64,s75,16384
buffer_atomic_pk_add_bf16 v24,v9,s[40:43],s64 offen sc1
s_add_u32 s64,s75,24576
buffer_atomic_pk_add_bf16 v25,v9,s[40:43],s64 offen sc1
s_mov_b32 s72,0x7fff0000
s_mov_b32 s69,0x7fff0000
s_mov_b32 s67,0x7fff0000
s_mov_b32 s77,0x7fff0000
s_branch Lg4_done_%=
Lg4_done_%=:
s_nop 0
s_barrier
buffer_store_dword v4,v3,s[36:39],s70 offen offset:0 sc0 sc1
s_waitcnt vmcnt(1)
s_barrier
buffer_store_dword v4,v3,s[36:39],s72 offen offset:0 sc0 sc1
s_waitcnt vmcnt(0) lgkmcnt(0)
s_barrier
s_endpgm
Lcode_g4_e:
.purgem M
.purgem R
.purgem T
.purgem D
.purgem L
)"
  :: "s"(ws), "s"(__builtin_amdgcn_readfirstlane(sWave)), "s"(__builtin_amdgcn_readfirstlane(sG0)), "s"(__builtin_amdgcn_readfirstlane(sStep)), "s"(__builtin_amdgcn_readfirstlane(sOutOff)), "s"(__builtin_amdgcn_readfirstlane(sFoutOff)), "s"(__builtin_amdgcn_readfirstlane(sAuxOff)), "s"(__builtin_amdgcn_readfirstlane(sWw)), "s"(__builtin_amdgcn_readfirstlane(sBuf)), "s"(__builtin_amdgcn_readfirstlane(sRk)), "s"(__builtin_amdgcn_readfirstlane(sExp)), "v"(vL16), "v"(vFlag), "v"(vSnapOff), "v"(vSnapRd), "v"(vWd0), "v"(vWd1), "v"(vWr0), "v"(vWr1), "v"(vSp), "v"(vSn), "s"(w4), "s"(out) : "v255", "v0", "v1", "v2", "v3", "v4", "v5", "v6", "v7", "v8", "v9", "v10", "v11", "v12", "v13", "v14", "v15", "v16", "v17", "v18", "v19", "v20", "v21", "v22", "v23", "v24", "v25", "v26", "v27", "v28", "v29", "v30", "v31", "v32", "v33", "v34", "v35", "v36", "v37", "v38", "v39", "v40", "v41", "v42", "v43", "v44", "v45", "v46", "v47", "v48", "v49", "v50", "v51", "v52", "v53", "v54", "v55", "v56", "v57", "v58", "v59", "v60", "v61", "v62", "v63", "v64", "v65", "v66", "v67", "v68", "v69", "v70", "v71", "v72", "v73", "v74", "v75", "v76", "v77", "v78", "v79", "v80", "v81", "v82", "v83", "v84", "v85", "v86", "v87", "v88", "v89", "v90", "v91", "v92", "v93", "v94", "v95", "v96", "v97", "v98", "v99", "v100", "v101", "v102", "v103", "v104", "v105", "v106", "v107", "v108", "v109", "v110", "v111", "v112", "v113", "v114", "v115", "v116", "v117", "v118", "v119", "v120", "v121", "v122", "v123", "v124", "v125", "v126", "v127", "v128", "v129", "v130", "v131", "v132", "v133", "v134", "v135", "v136", "v137", "v138", "v139", "v140", "v141", "v142", "v143", "v144", "v145", "v146", "v147", "v148", "v149", "v150", "v151", "v152", "v153", "v154", "v155", "v156", "v157", "v158", "v159", "v160", "v161", "v162", "v163", "v164", "v165", "v166", "v167", "v168", "v169", "v170", "v171", "v172", "v173", "v174", "v175", "v176", "v177", "v178", "v179", "v180", "v181", "v182", "v183", "v184", "v185", "v186", "v187", "v188", "v189", "v190", "v191", "v192", "v193", "v194", "v195", "v196", "v197", "v198", "v199", "v200", "v201", "v202", "v203", "v204", "v205", "v206", "v207", "v208", "v209", "v210", "v211", "v212", "v213", "v214", "v215", "v216", "v217", "v218", "v219", "v220", "v221", "v222", "v223", "v224", "v225", "v226", "v227", "v228", "v229", "v230", "v231", "a0", "a1", "a2", "a3", "a4", "a5", "a6", "a7", "a8", "a9", "a10", "a11", "a12", "a13", "a14", "a15", "a16", "a17", "a18", "a19", "a20", "a21", "a22", "a23", "a24", "a25", "a26", "a27", "a28", "a29", "a30", "a31", "a32", "a33", "a34", "a35", "a36", "a37", "a38", "a39", "a40", "a41", "a42", "a43", "a44", "a45", "a46", "a47", "a48", "a49", "a50", "a51", "a52", "a53", "a54", "a55", "a56", "a57", "a58", "a59", "a60", "a61", "a62", "a63", "a64", "a65", "a66", "a67", "a68", "a69", "a70", "a71", "a72", "a73", "a74", "a75", "a76", "a77", "a78", "a79", "a80", "a81", "a82", "a83", "a84", "a85", "a86", "a87", "a88", "a89", "a90", "a91", "a92", "a93", "a94", "a95", "a96", "a97", "a98", "a99", "a100", "a101", "a102", "a103", "a104", "a105", "a106", "a107", "a108", "a109", "a110", "a111", "a112", "a113", "a114", "a115", "a116", "a117", "a118", "a119", "a120", "a121", "a122", "a123", "a124", "a125", "a126", "a127", "a128", "a129", "a130", "a131", "a132", "a133", "a134", "a135", "a136", "a137", "a138", "a139", "a140", "a141", "a142", "a143", "a144", "a145", "a146", "a147", "a148", "a149", "a150", "a151", "a152", "a153", "a154", "a155", "a156", "a157", "a158", "a159", "a160", "a161", "a162", "a163", "a164", "a165", "a166", "a167", "a168", "a169", "a170", "a171", "a172", "a173", "a174", "a175", "a176", "a177", "a178", "a179", "a180", "a181", "a182", "a183", "a184", "a185", "a186", "a187", "a188", "a189", "a190", "a191", "a192", "a193", "a194", "a195", "a196", "a197", "a198", "a199", "a200", "a201", "a202", "a203", "a204", "a205", "a206", "a207", "a208", "a209", "a210", "a211", "a212", "a213", "a214", "a215", "a216", "a217", "a218", "a219", "a220", "a221", "a222", "a223", "a224", "a225", "a226", "a227", "a228", "a229", "a230", "a231", "a232", "a233", "a234", "a235", "a236", "a237", "a238", "a239", "a240", "a241", "a242", "a243", "a244", "a245", "a246", "a247", "a248", "a249", "a250", "a251", "a252", "a253", "a254", "a255", "s36", "s37", "s38", "s39", "s40", "s41", "s42", "s43", "s44", "s45", "s46", "s47", "s48", "s49", "s50", "s51", "s52", "s53", "s54", "s55", "s56", "s57", "s58", "s59", "s60", "s61", "s62", "s63", "s64", "s65", "s66", "s67", "s68", "s69", "s70", "s71", "s72", "s73", "s74", "s75", "s76", "s77", "s78", "s79", "s80", "s81", "s82", "s83", "s84", "s85", "s86", "s87", "s88", "s89", "s90", "s91", "s92", "s93", "s94", "s95", "s96", "s97", "vcc", "scc", "memory");
    __builtin_unreachable();
  } else {
    LANE_ID(lane2);
    constexpr int N1 = 33 - (NR4 - 6) * 16, T1 = LAST_TASKS > RLN * N1 ? RLN * N1 : LAST_TASKS;
    const int w = kind == 6 ? 0 : kind == 1 ? 1 + (3 - rep) * 8 + slice : N1 + rep * 8 + slice;
    if (kind != 2) for (int t = w; t < T1; t += N1) row_last(ws, out, t, lane2, wave, ep);
    for (int t = T1 + (w + 32) % (N1 + 32); t < LAST_TASKS; t += N1 + 32) row_last(ws, out, t, lane2, wave, ep);
    if (kind == 2 && rep == 0 && slice == 0 && wave == 0) set_flag(ws + OFF_EP, lane2, ep);
    CTS(14);
    asm volatile("s_endpgm\nLcode_end:" ::: "memory");
    __builtin_unreachable();
  }
}

extern "C" void kernel_launch(void* const* d_in, const int* in_sizes, int n_in, void* d_out, int out_size,
                              void* d_ws, size_t ws_size, hipStream_t stream) {
  const u16* q = (const u16*)d_in[0];
  const u16* k = (const u16*)d_in[1];
  const u16* v = (const u16*)d_in[2];
  const u16* shift_k = (const u16*)d_in[3];
  const u16* ssm_k = (const u16*)d_in[4];
  const u16* ssm_D = (const u16*)d_in[5];
  const u16* w1 = (const u16*)d_in[7];
  const u16* w2 = (const u16*)d_in[8];
  const u16* w3 = (const u16*)d_in[9];
  const u16* w4 = (const u16*)d_in[10];
  flow_kernel<<<256, 256, 0, stream>>>(q, k, v, shift_k, ssm_k, ssm_D, w1, w2, w3, w4, (char*)d_ws, (u16*)d_out);
}
```

```cpp
#include <hip/hip_runtime.h>
#include <hip/hip_bf16.h>
#include <stdint.h>
#include <type_traits>

typedef short s16x8 __attribute__((ext_vector_type(8)));
typedef short s16x4 __attribute__((ext_vector_type(4)));
typedef float f32x4 __attribute__((ext_vector_type(4)));
typedef unsigned short u16;
typedef unsigned short u16x8 __attribute__((ext_vector_type(8)));
typedef unsigned short u16x4 __attribute__((ext_vector_type(4)));
typedef float f32x2 __attribute__((ext_vector_type(2)));
typedef __bf16 bf16x2 __attribute__((ext_vector_type(2)));
typedef unsigned u32x4v __attribute__((ext_vector_type(4)));
typedef __attribute__((address_space(1))) unsigned gu32;

constexpr int S_ = 2048, D_ = 1024, F_ = 3584;

constexpr int WS_USED = 111243392, LAST_TASKS = 100, ALLZ = 1, U0 = 170, U1 = 224, NR4 = 8, RHELP = 1, PRE1 = 1;
constexpr int OFF_KVR = 4194304, OFF_XP = 8388608, OFF_HS = 17825792, OFF_X1 = 31457280, OFF_G = 40894464, OFF_P0 = 71303168, OFF_P1 = 89128960, OFF_Y = 98566144, OFF_S1 = 106954752, OFF_ST = 107479040, OFF_FL = 109051904, OFF_FXP = 109056000, OFF_FH = 109334528, OFF_FX1 = 109613056, OFF_FG = 109891584, OFF_FP = 110587904, OFF_FKV = 111144960, OFF_FY = 111177728, OFF_EP = 111243264, OFF_DBG = 115343360;

#define WS_DIAG 0

__device__ __forceinline__ float bf2f(u16 v) { return __uint_as_float(((unsigned)v) << 16); }
__device__ __forceinline__ u16 f2bf(float f) { return __builtin_bit_cast(u16, (__bf16)f); }
__device__ __forceinline__ float rbf(float f) { return bf2f(f2bf(f)); }
__device__ __forceinline__ void st16(void* p, u16x8 v) {
  asm volatile("global_store_dwordx4 %0, %1, off sc0 sc1\n\ts_nop 1" :: "v"(p), "v"(v) : "memory");
}
#define WAIT_VM(n) asm volatile("s_waitcnt vmcnt(%0)" ::"n"(n) : "memory")
#define WAIT_LGKM0() asm volatile("s_waitcnt lgkmcnt(0)" ::: "memory")

__device__ __forceinline__ void touch_lds(const char* p, unsigned a) {
  asm volatile("s_mov_b32 m0, %1\n\ts_nop 0\n\tglobal_load_lds_dword %0, off" :: "v"(p), "s"(a) : "memory");
}
#define CTS(i)
__device__ __forceinline__ f32x2 up2(unsigned p) { return (f32x2){__uint_as_float(p << 16), __uint_as_float(p & 0xffff0000u)}; }
__device__ __forceinline__ unsigned pk2(f32x2 v) { return __builtin_bit_cast(unsigned, __builtin_convertvector(v, bf16x2)); }
__device__ __forceinline__ f32x2 rbf2(f32x2 v) { return up2(pk2(v)); }
__device__ __forceinline__ void set_flag(char* p, int i, unsigned ep) {
  if (i < 32) __hip_atomic_store((gu32*)p + i, ep, __ATOMIC_RELAXED, __HIP_MEMORY_SCOPE_AGENT);
}
constexpr int CV_HP = (2048 + 32) * 2, CV_UR = (2048 + 256) * 2, CV_CH = 2 * CV_HP + CV_UR;
constexpr int NCV = 174;
constexpr int CV_STG = 8 * CV_CH;
constexpr int YP = D_ * 16;

struct KvRegs { u32x4v k0, k1, k2, k3, k4, k5, k6, v0, v1, v2, v3, s0, s1, s2, s3; };
#define KV_LOADS                                                                                                                       \
  "v_lshrrev_b32 %[a], 3, %[tid]\n\tv_and_b32 %[c], 7, %[tid]\n\ts_lshr_b32 s90, %[tile], 4\n\ts_and_b32 s91, %[tile], 15\n\t"          \
  "v_lshlrev_b32 %[a], 13, %[a]\n\tv_lshlrev_b32 %[c], 4, %[c]\n\ts_lshl_b32 s90, s90, 18\n\ts_lshl_b32 s91, s91, 7\n\t"                 \
  "v_add_u32 %[a], s90, %[a]\n\tv_add_u32 %[c], s91, %[c]\n\tv_subrev_u32 %[h], 0x1000, %[a]\n\tv_max_i32 %[h], 0x800, %[h]\n\t"         \
  "v_add3_u32 %[a], %[a], %[c], s92\n\tv_add_u32 %[h], %[h], %[c]\n\tv_lshlrev_b32 %[c], 2, %[c]\n\ts_waitcnt lgkmcnt(0)\n\t"            \
  "global_load_dwordx4 %3, %[a], s[76:77] offset:-4096 nt\n\tglobal_load_dwordx4 %4, %[a], s[76:77] offset:-2048 nt\n\t"                \
  "global_load_dwordx4 %5, %[a], s[76:77] nt\n\tglobal_load_dwordx4 %6, %[a], s[76:77] offset:2048 nt\n\t"                              \
  "global_load_dwordx4 %7, %[a], s[78:79] offset:-4096 nt\n\tglobal_load_dwordx4 %8, %[a], s[78:79] offset:-2048 nt\n\t"                \
  "global_load_dwordx4 %9, %[a], s[78:79] nt\n\tglobal_load_dwordx4 %10, %[a], s[78:79] offset:2048 nt\n\t"                             \
  "global_load_dwordx4 %0, %[h], s[76:77] offset:-2048 nt\n\tglobal_load_dwordx4 %1, %[h], s[76:77] nt\n\t"                             \
  "global_load_dwordx4 %2, %[h], s[76:77] offset:2048 nt\n\t"                                                                           \
  "global_load_dwordx4 %11, %[c], s[80:81]\n\tglobal_load_dwordx4 %12, %[c], s[80:81] offset:16\n\t"                                    \
  "global_load_dwordx4 %13, %[c], s[80:81] offset:32\n\tglobal_load_dwordx4 %14, %[c], s[80:81] offset:48\n\t"
#define KV_OUTS(R) "=&v"(R.k0), "=&v"(R.k1), "=&v"(R.k2), "=&v"(R.k3), "=&v"(R.k4), "=&v"(R.k5), "=&v"(R.k6), "=&v"(R.v0), "=&v"(R.v1), "=&v"(R.v2), \
                   "=&v"(R.v3), "=&v"(R.s0), "=&v"(R.s1), "=&v"(R.s2), "=&v"(R.s3), [a] "=&v"(ta), [h] "=&v"(th), [c] "=&v"(tc)
#define KV_CLOB "s74", "s75", "s76", "s77", "s78", "s79", "s80", "s81", "s82", "s83", "s90", "s91", "s92", "scc", "vcc", "memory"
template <int N>
__device__ __forceinline__ void kv_wait(KvRegs& R) {
  asm volatile("s_waitcnt vmcnt(%15)" : "+v"(R.k0), "+v"(R.k1), "+v"(R.k2), "+v"(R.k3), "+v"(R.k4), "+v"(R.k5), "+v"(R.k6), "+v"(R.v0), "+v"(R.v1), "+v"(R.v2),
               "+v"(R.v3), "+v"(R.s0), "+v"(R.s1), "+v"(R.s2), "+v"(R.s3) : "n"(N) : "memory");
}
__device__ __forceinline__ void kv_store(KvRegs& R, char* __restrict__ ws, char* T, int b, int tid) {
  const int t0 = (b >> 4) * 128, c0 = (b & 15) * 64, ch8 = tid & 7, rg = tid >> 3;
  if (t0 + 4 * rg < 3) R.k0 = R.k1 = R.k2 = (u32x4v){0u, 0u, 0u, 0u};
  typedef unsigned u32x2 __attribute__((ext_vector_type(2)));
  const u32x4v kr[7] = {R.k0, R.k1, R.k2, R.k3, R.k4, R.k5, R.k6}, vr[4] = {R.v0, R.v1, R.v2, R.v3}, sk[4] = {R.s0, R.s1, R.s2, R.s3};
#pragma unroll
  for (int j2 = 0; j2 < 4; ++j2) {
    const f32x2 sa = up2(sk[j2][0]), sb = up2(sk[j2][1]), sc = up2(sk[j2][2]), sd = up2(sk[j2][3]);
    const f32x2 s0 = {sa[0], sc[0]}, s1 = {sa[1], sc[1]}, s2 = {sb[0], sd[0]}, s3 = {sb[1], sd[1]};
    f32x2 kf[7], o[4];
#pragma unroll
    for (int r = 0; r < 7; ++r) kf[r] = up2(kr[r][j2]);
#pragma unroll
    for (int r = 0; r < 4; ++r)
      o[r] = __builtin_elementwise_fma(kf[r + 3], s0, __builtin_elementwise_fma(kf[r + 2], s1, __builtin_elementwise_fma(kf[r + 1], s2, kf[r] * s3))) * up2(vr[r][j2]);
#pragma unroll
    for (int e = 0; e < 2; ++e)
      *(u32x2*)(T + (8 * ch8 + 2 * j2 + e) * 264 + (124 - 4 * rg) * 2) = (u32x2){pk2((f32x2){o[3][e], o[2][e]}), pk2((f32x2){o[1][e], o[0][e]})};
  }
  __syncthreads();
  u16* kvr = (u16*)(ws + OFF_KVR);
#pragma unroll
  for (int i = 0; i < 4; ++i) {
    const int idx = tid + 256 * i, ch = idx >> 4, cb = idx & 15;
    const u16x4 lo = *(const u16x4*)(T + ch * 264 + cb * 16), hi = *(const u16x4*)(T + ch * 264 + cb * 16 + 8);
    st16(kvr + (size_t)(c0 + ch) * S_ + (1920 - t0) + cb * 8, __builtin_shufflevector(lo, hi, 0, 1, 2, 3, 4, 5, 6, 7));
  }
}

#define LASTSLEEP 4
__device__ __forceinline__ void wait_flags(const char* p, unsigned ep) {
  gu32* f = (gu32*)p;
  for (int it = 0; it < 20000; ++it) {
    if (__all(__hip_atomic_load(f, __ATOMIC_RELAXED, __HIP_MEMORY_SCOPE_AGENT) == ep)) break;
    __builtin_amdgcn_s_sleep(LASTSLEEP);
  }
  asm volatile("" ::: "memory");
}
__device__ __forceinline__ void row_last(char* __restrict__ ws, u16* __restrict__ out, int t, int lane, int wave, unsigned ep) {
  const __amdgpu_buffer_rsrc_t rs = __builtin_amdgcn_make_buffer_rsrc(ws, 0, WS_USED, 0x00020000);
  const int r8 = lane & 7, seg = lane >> 3, un = 128 + (t >> 1);
  const unsigned fo = un * 32768 + (wave * 8 + (seg >> 2)) * 1024 + ((seg & 3) * 16 + (t & 1) * 8 + r8) * 16;
  wait_flags(ws + OFF_FP + un * 2048 + ((lane & 2) * 4 + wave * 2 + (lane & 1)) * 128, ep);
  u32x4v p0[4], p1[4];
#pragma unroll
  for (int i = 0; i < 4; ++i) {
    p0[i] = __builtin_bit_cast(u32x4v, __builtin_amdgcn_raw_buffer_load_b128(rs, fo + i * 2048, OFF_P0, 16));
    p1[i] = __builtin_bit_cast(u32x4v, __builtin_amdgcn_raw_buffer_load_b128(rs, fo + i * 2048, OFF_P1, 16));
  }
#pragma unroll
  for (int i = 0; i < 4; ++i) {
    u32x4v o;
#pragma unroll
    for (int e = 0; e < 4; ++e) o[e] = pk2(up2(p0[i][e]) + up2(p1[i][e]));
    st16(out + (size_t)(t * 8 + r8) * D_ + wave * 256 + i * 64 + seg * 8, __builtin_bit_cast(u16x8, o));
  }
}

__device__ __forceinline__ int swzB(int r) { return ((((r >> 1) & 1) | (((r >> 3) & 1) << 1)) << 1); }
#define LANE_ID(x) int x; asm volatile("v_mbcnt_lo_u32_b32 %0, -1, 0\n\tv_mbcnt_hi_u32_b32 %0, -1, %0" : "=v"(x))
#define W12 6
#define RLN 3
#define QTB 1
constexpr int QT0 = 34 * 128;
#define CODE_MAX(x, y) "(((" x ">" y ")&" x ")|((" x "<=" y ")&" y "))"
constexpr int HU = 40960;
#define G3OR 5
constexpr int NRW = 22 - G3OR;
__global__ __launch_bounds__(256, 1) void flow_kernel(const u16* __restrict__ q, const u16* __restrict__ kin, const u16* __restrict__ vin,
                                                      const u16* __restrict__ shift_k, const u16* __restrict__ ssm_k, const u16* __restrict__ ssm_D,
                                                      const u16* __restrict__ w1, const u16* __restrict__ w2,
                                                      const u16* __restrict__ w3, const u16* __restrict__ w4, char* __restrict__ ws,
                                                      u16* __restrict__ out) {
  __shared__ __attribute__((aligned(16))) char lds[163840];
  KvRegs KR;
  unsigned ew, ta, th, tc;
  int b, tile;
  asm volatile("Lcode_b:\n\ts_load_dwordx4 s[76:79], %[ka], 0x8\n\ts_load_dwordx2 s[82:83], %[ka], 0x50\n\t"
               "s_getpc_b64 s[74:75]\n\ts_lshr_b32 s90, %[bi], 3\n\ts_mov_b32 m0, 0x27f00\n\tv_lshlrev_b32 %[a], 5, %[tid]\n\tv_add_lshl_u32 %[a], %[a], s90, 7\n\t"
               "v_min_u32 %[a], " CODE_MAX(CODE_MAX(CODE_MAX("(Lcode_g3_e-Lcode_b)", "(Lcode_g3o_e-Lcode_b)"), CODE_MAX("(Lcode_g4_e-Lcode_b)", "(Lcode_g12_e-Lcode_b)")), "(Lcode_end-Lcode_b)") "-64, %[a]\n\t"
               "global_load_lds_dword %[a], s[74:75]\n\ts_load_dwordx2 s[80:81], %[ka], 0x18\n\t"
               "s_mov_b32 %[b], %[bi]\n\ts_bfe_u32 s91, %[bi], 0x20001\n\ts_lshl2_add_u32 %[tile], s90, s91\n\ts_movk_i32 s92, 0x1000\n\t"
               "s_lshl_b32 s90, %[bi], 7\n\ts_add_u32 %[tile], %[tile], s90\n\ts_add_u32 %[tile], %[tile], 64\n\ts_and_b32 %[tile], %[tile], 255\n\t"
               KV_LOADS "v_mov_b32 %[a], %[eo]\n\tglobal_load_dword %[ew], %[a], s[82:83] sc1"
               : KV_OUTS(KR), [ew] "=&v"(ew), [b] "=&s"(b), [tile] "=&s"(tile)
               : [ka] "s"(__builtin_amdgcn_kernarg_segment_ptr()), [bi] "s"(blockIdx.x), [tid] "v"(threadIdx.x), [eo] "n"(OFF_EP) : KV_CLOB);
  const unsigned t_start = 0;
  const int tid = threadIdx.x, lane = tid & 63, wave = __builtin_amdgcn_readfirstlane(tid >> 6);
  const int xcd = b & 7, idx = b >> 3;
  const unsigned sLds = (unsigned)(size_t)lds;
  const int o8 = idx & 7;
  int kind, rep, slice, kh = 0;
  int j3 = 0, j4 = 0;
  const int xp = xcd >> 1, pr = xp & 1;
  if (!(xcd & 1)) {
    if (idx < 20) { kind = 4; rep = idx < 16 ? (xp >> 1) * 3 + (idx >> 3) : 2; kh = pr; slice = idx < 16 ? o8 : (xp >> 1) * 4 + o8; j4 = rep * 16 + kh * 8 + slice; }
    else if (idx >= 24) { kind = 1; rep = xp; slice = o8; }
    else { kind = 3; rep = 4; slice = xp * 4 + idx - 20; j3 = 72 + slice; }
  } else if (idx >= 24) { kind = 2; rep = xp; slice = o8; }
  else if (idx < 18) { kind = 3; rep = pr * 2 + (idx >= 9); slice = (xp >> 1) * 9 + idx - 9 * (idx >= 9); j3 = rep * 18 + slice; }
  else if (idx < 22 || b == 191) { kind = 6; rep = 0; slice = idx < 22 ? xp * 4 + idx - 18 : 16; }
  else if (idx == 22 || xp == 0) { kind = 5; rep = idx == 22 ? pr * 2 + (xp >> 1) : 4; slice = 18; j3 = 90 + rep; }
  else { kind = 3; rep = 4; slice = 15 + xp; j3 = 72 + slice; }
  CTS(0);
  const unsigned sScr = sLds + 163584;
  unsigned ep = 0;
  auto w_touch = [&](const u16* w) {
#pragma unroll
    for (int i = 0; i < 2; ++i) touch_lds((const char*)w + (size_t)(rep * 256 + i * 128 + (tid >> 1)) * 2048 + slice * 256 + (tid & 1) * 128, sScr);
  };
  {
  const bool isconv = kind >= 3 && kind != 6 && j3 < 94;
  const int nw = (kind == 4 || j3 >= 92) ? 2 : 1, c0 = kind == 4 ? 8 * j4 : j3 < 92 ? 640 + 4 * j3 : j3 < 94 ? 272 + 8 * j3 : 0, cw = c0 + nw * wave;
  const unsigned hd = isconv ? sLds + 2 * wave * CV_CH + 32 : 0x30000u;
  auto h_dma = [&](bool first) {
#pragma unroll
    for (int j = 0; j < 2; ++j) {
      const u16* hp = ssm_k + (size_t)cw * S_ + (isconv && j < nw ? j * S_ + lane * 8 : 0);
      if (first) asm volatile("s_mov_b32 m0, %1\n\ts_nop 0\n\tglobal_load_lds_dwordx4 %0, off nt" :: "v"(hp), "s"(hd + j * CV_CH) : "memory");
      else asm volatile("s_mov_b32 m0, %1\n\ts_nop 0\n\tglobal_load_lds_dwordx4 %0, off offset:1024 nt\n\t"
                        "global_load_lds_dwordx4 %0, off offset:2048 nt\n\tglobal_load_lds_dwordx4 %0, off offset:3072 nt" :: "v"(hp), "s"(hd + j * CV_CH) : "memory");
    }
  };
  auto w_dma = [&](int t0, int t1) {
    const char* wp = (const char*)w1 + (16 * wave + (lane >> 3)) * 2048 + slice * 256;
    const unsigned c0_ = ((lane & 7) ^ swzB(lane >> 3)) * 16, c1_ = 8 * 2048 + ((lane & 7) ^ swzB(8 + (lane >> 3))) * 16;
#pragma unroll
    for (int t = t0; t < t1; ++t)
#pragma unroll
      for (int i = 0; i < 2; ++i)
        asm volatile("s_mov_b32 m0, %1\n\ts_nop 0\n\tglobal_load_lds_dwordx4 %0, off\n\ts_add_u32 m0, m0, 8064\n\ts_nop 0\n\tglobal_load_lds_dwordx4 %0, off offset:128"
                     :: "v"(wp + t * 131072 + (i ? c1_ : c0_)), "s"(sLds + t * 16384 + wave * 2048 + i * 1024) : "scc", "memory");
  };
  if (PRE1 && kind == 1) w_dma(0, 2);
  else { h_dma(true); h_dma(false); }
  if (isconv) *(u32x4v*)(lds + (2 * wave + (lane >> 5)) * CV_CH + 2 * CV_HP + 4096 + (lane & 31) * 16) = (u32x4v){0u, 0u, 0u, 0u};
  if (isconv && lane < 8) *(u32x4v*)(lds + (2 * wave + (lane >> 2)) * CV_CH + ((lane >> 1) & 1) * CV_HP + (lane & 1) * 16) = (u32x4v){0u, 0u, 0u, 0u};
  kv_wait<9>(KR);
  CTS(12);
  kv_store(KR, ws, lds + CV_STG, tile, tid);
  CTS(1);
  if (PRE1 && kind == 1) w_dma(2, 6);
  const unsigned dw = ((const unsigned*)ssm_D)[cw >> 1];
  asm volatile("s_cmp_eq_u32 %1, %2\n\ts_cbranch_scc1 Lew%=\n\ts_waitcnt vmcnt(0)\nLew%=:\n\ts_waitcnt vmcnt(16)" : "+v"(ew) : "s"(__builtin_amdgcn_readfirstlane(kind)), "n"(PRE1 ? 1 : 99) : "scc", "memory");
  ep = __builtin_amdgcn_readfirstlane(ew) + 1u;
  if (ep == 0u) ep = 1u;
  __builtin_amdgcn_s_barrier();
  set_flag(ws + OFF_FKV + tile * 128, tid, ep);
  CTS(2);
  if (kind < 3 && (W12 & kind) != 0) w_touch(kind == 1 ? w1 : w2);
  if (kind == 2 && (W12 & 4) != 0) w_touch(w1);
  if (isconv) {
    LANE_ID(lane);
    const int tid = wave * 64 + lane;
    const unsigned sC0 = (cw & 1) ? dw & 0xffff0000u : dw << 16, sC1 = dw & 0xffff0000u;
    const int l15 = lane & 15, lg = lane >> 4, cp = (l15 + 1) & 1, iw = kind == 4 ? j4 : 80 + j3, own = 2 * wave * CV_CH;
    const int ch = tid & (4 * nw - 1), tb = nw == 2 ? tid >> 3 : tid >> 2, slot = nw == 2 ? ch : 2 * ch;
    const unsigned sWave = wave, sBuf = nw, sAuxOff = OFF_KVR + cw * 4096, sFoutOff = OFF_FY + iw * 128, sExp = __builtin_amdgcn_readfirstlane(ep), sDbg = 110u * 1048576u + (b * 4 + wave) * 8192;
    const unsigned vL16 = lane * 16, vPA = own + cp * CV_HP + 2 * (8 * lg + l15 + 1 - cp), vPB = own + 2 * CV_HP + 2 * (2032 - 16 * l15 + 8 * lg) - 512 * 7;
    const unsigned vPU = own + 2 * CV_HP + 2 * (2044 - 16 * l15 - 4 * lg) - 512 * 7, vPW = 32 * l15 + 8 * lg;
    const unsigned vOR = slot * CV_CH + tb * 16, vORA = CV_STG + slot * 1024 + tb * 16, vOG = tb * YP + (c0 + ch) * 16;
    const unsigned vFA = ((lane & 3) * 16 + (c0 >> 6)) * 128, vFB = ((4 + lane % 12) * 16 + (c0 >> 6)) * 128, vFlag = tid < 32 ? tid * 4 : 0x80000000u;
    const unsigned vTW = lane < 42 ? (iw * 168 + wave * 42 + lane) * 128 : 0x80000000u;
    const unsigned vTX = lane < 44 ? ((kind == 4 ? j4 : j3 - 12) * 176 + wave * 44 + lane) * 128 : 0x80000000u;
    (void)sDbg;
    asm volatile(
R"(.macro M d,w,x
v_mfma_f32_16x16x32_bf16 v[\d:\d+3], \w, v[\x:\x+3], v[\d:\d+3]
.endm
.macro R d,a,o
ds_read_b128 v[\d:\d+3], v\a offset:\o
.endm
.macro T d,a,o
ds_read_b64_tr_b16 \d, v\a offset:\o
.endm
.macro D a,s,o
buffer_load_dwordx4 v\a, s[36:39], s\s offen offset:\o sc1 lds
.endm
.macro L c
s_waitcnt lgkmcnt(\c)
.endm
Lcode_cv_b:
s_mov_b64 s[36:37],%0
s_mov_b32 s38,111243392
s_mov_b32 s39,0x00020000
s_mov_b32 s44,%1
s_mov_b32 s51,%2
s_mov_b32 s49,%3
s_mov_b32 s48,%4
s_mov_b32 s86,%5
s_mov_b32 s87,%6
s_mov_b32 s53,%7
v_mov_b32 v0,%8
v_mov_b32 v1,%9
v_mov_b32 v2,%10
v_mov_b32 v3,%11
v_mov_b32 v4,%12
v_mov_b32 v5,%13
v_mov_b32 v6,%14
v_mov_b32 v7,%15
v_mov_b32 v8,%16
v_mov_b32 v9,%17
v_mov_b32 v10,%18
v_mov_b32 v11,%19
v_mov_b32 v12,%20
s_mov_b32 s58,111144960
buffer_load_dword v13,v8,s[36:39],s58 offen offset:0 sc1
s_mul_i32 s57,s44,25856
v_add_u32 v18,s57,v0
v_add_u32 v18,32,v18
v_mov_b32 v19,s57
R 88,18,0
ds_read_b32 v92,v18 offset:16
R 152,18,12928
ds_read_b32 v156,v18 offset:12944
ds_read_b32 v24,v19 offset:32
ds_read_b32 v25,v19 offset:12960
s_mov_b64 s[40:41],%21
s_mov_b32 s42,7340032
s_mov_b32 s43,0x00020000
s_mov_b64 s[60:61],%22
s_mov_b32 s62,7340032
s_mov_b32 s63,0x00020000
v_mov_b32 v23,s53
s_mov_b32 s82,0
s_add_u32 s55,s49,4096
s_cmp_eq_u32 s51,2
s_cselect_b32 s55,s55,0x7fff0000
s_cselect_b32 s56,0,0x70000000
s_add_u32 s57,s57,8320
v_add_u32 v14,1024,v1
v_add_u32 v15,2048,v1
v_add_u32 v16,3072,v1
v_add_u32 v17,12928,v1
L 4
v_alignbyte_b32 v96,v89,v88,2
v_alignbyte_b32 v97,v90,v89,2
v_alignbyte_b32 v98,v91,v90,2
v_alignbyte_b32 v99,v92,v91,2
ds_write_b128 v18,v[96:99] offset:4160
L 3
v_alignbyte_b32 v160,v153,v152,2
v_alignbyte_b32 v161,v154,v153,2
v_alignbyte_b32 v162,v155,v154,2
v_alignbyte_b32 v163,v156,v155,2
ds_write_b128 v18,v[160:163] offset:17088
L 2
v_lshlrev_b32 v24,16,v24
v_lshlrev_b32 v25,16,v25
s_mov_b64 exec,1
ds_write_b32 v19,v24 offset:4188
ds_write_b32 v19,v25 offset:17116
s_mov_b64 exec,-1
ds_read2_b32 a[0:1],v1 offset0:0 offset1:1
ds_read2_b32 a[2:3],v1 offset0:2 offset1:3
ds_read2_b32 a[4:5],v1 offset0:128 offset1:129
ds_read2_b32 a[6:7],v1 offset0:130 offset1:131
ds_read2_b32 a[8:9],v1 offset0:16 offset1:17
ds_read2_b32 a[10:11],v1 offset0:18 offset1:19
ds_read2_b32 a[12:13],v1 offset0:144 offset1:145
ds_read2_b32 a[14:15],v1 offset0:146 offset1:147
ds_read2_b32 a[16:17],v1 offset0:32 offset1:33
ds_read2_b32 a[18:19],v1 offset0:34 offset1:35
ds_read2_b32 a[20:21],v1 offset0:160 offset1:161
ds_read2_b32 a[22:23],v1 offset0:162 offset1:163
ds_read2_b32 a[24:25],v1 offset0:48 offset1:49
ds_read2_b32 a[26:27],v1 offset0:50 offset1:51
ds_read2_b32 a[28:29],v1 offset0:176 offset1:177
ds_read2_b32 a[30:31],v1 offset0:178 offset1:179
ds_read2_b32 a[32:33],v1 offset0:64 offset1:65
ds_read2_b32 a[34:35],v1 offset0:66 offset1:67
ds_read2_b32 a[36:37],v1 offset0:192 offset1:193
ds_read2_b32 a[38:39],v1 offset0:194 offset1:195
ds_read2_b32 a[40:41],v1 offset0:80 offset1:81
ds_read2_b32 a[42:43],v1 offset0:82 offset1:83
ds_read2_b32 a[44:45],v1 offset0:208 offset1:209
ds_read2_b32 a[46:47],v1 offset0:210 offset1:211
ds_read2_b32 a[48:49],v1 offset0:96 offset1:97
ds_read2_b32 a[50:51],v1 offset0:98 offset1:99
ds_read2_b32 a[52:53],v1 offset0:224 offset1:225
ds_read2_b32 a[54:55],v1 offset0:226 offset1:227
ds_read2_b32 a[56:57],v1 offset0:112 offset1:113
ds_read2_b32 a[58:59],v1 offset0:114 offset1:115
ds_read2_b32 a[60:61],v1 offset0:240 offset1:241
ds_read2_b32 a[62:63],v1 offset0:242 offset1:243
ds_read2_b32 a[64:65],v17 offset0:0 offset1:1
ds_read2_b32 a[66:67],v17 offset0:2 offset1:3
ds_read2_b32 a[68:69],v17 offset0:128 offset1:129
ds_read2_b32 a[70:71],v17 offset0:130 offset1:131
ds_read2_b32 a[72:73],v17 offset0:16 offset1:17
ds_read2_b32 a[74:75],v17 offset0:18 offset1:19
ds_read2_b32 a[76:77],v17 offset0:144 offset1:145
ds_read2_b32 a[78:79],v17 offset0:146 offset1:147
ds_read2_b32 a[80:81],v17 offset0:32 offset1:33
ds_read2_b32 a[82:83],v17 offset0:34 offset1:35
ds_read2_b32 a[84:85],v17 offset0:160 offset1:161
ds_read2_b32 a[86:87],v17 offset0:162 offset1:163
ds_read2_b32 a[88:89],v17 offset0:48 offset1:49
ds_read2_b32 a[90:91],v17 offset0:50 offset1:51
ds_read2_b32 a[92:93],v17 offset0:176 offset1:177
ds_read2_b32 a[94:95],v17 offset0:178 offset1:179
ds_read2_b32 a[96:97],v17 offset0:64 offset1:65
ds_read2_b32 a[98:99],v17 offset0:66 offset1:67
ds_read2_b32 a[100:101],v17 offset0:192 offset1:193
ds_read2_b32 a[102:103],v17 offset0:194 offset1:195
ds_read2_b32 a[104:105],v17 offset0:80 offset1:81
ds_read2_b32 a[106:107],v17 offset0:82 offset1:83
ds_read2_b32 a[108:109],v17 offset0:208 offset1:209
ds_read2_b32 a[110:111],v17 offset0:210 offset1:211
ds_read2_b32 a[112:113],v17 offset0:96 offset1:97
ds_read2_b32 a[114:115],v17 offset0:98 offset1:99
ds_read2_b32 a[116:117],v17 offset0:224 offset1:225
ds_read2_b32 a[118:119],v17 offset0:226 offset1:227
ds_read2_b32 a[120:121],v17 offset0:112 offset1:113
ds_read2_b32 a[122:123],v17 offset0:114 offset1:115
ds_read2_b32 a[124:125],v17 offset0:240 offset1:241
ds_read2_b32 a[126:127],v17 offset0:242 offset1:243
Lcv_bpa_%=:
s_waitcnt vmcnt(0)
v_cmp_eq_u32 vcc,s53,v13
s_add_u32 s82,s82,1
s_cmp_eq_u64 vcc,-1
s_cbranch_scc1 Lcv_bqa_%=
s_sleep 1
buffer_load_dword v13,v8,s[36:39],s58 offen offset:0 sc1
s_cmp_lt_u32 s82,20000
s_cbranch_scc1 Lcv_bpa_%=
s_waitcnt vmcnt(0)
Lcv_bqa_%=:
s_add_u32 m0,s57,0
s_nop 0
D 0,49,3072
s_add_u32 m0,s57,12928
s_nop 0
D 0,55,3072
buffer_load_dword v13,v9,s[36:39],s58 offen offset:0 sc1
R 88,18,0
ds_read_b32 v92,v18 offset:16
R 104,18,1024
ds_read_b32 v108,v18 offset:1040
R 120,18,2048
ds_read_b32 v124,v18 offset:2064
R 136,18,3072
ds_read_b32 v140,v18 offset:3088
L 6
v_alignbyte_b32 v96,v89,v88,2
v_alignbyte_b32 v97,v90,v89,2
v_alignbyte_b32 v98,v91,v90,2
v_alignbyte_b32 v99,v92,v91,2
ds_write_b128 v18,v[96:99] offset:4160
L 5
v_alignbyte_b32 v112,v105,v104,2
v_alignbyte_b32 v113,v106,v105,2
v_alignbyte_b32 v114,v107,v106,2
v_alignbyte_b32 v115,v108,v107,2
ds_write_b128 v18,v[112:115] offset:5184
L 4
v_alignbyte_b32 v128,v121,v120,2
v_alignbyte_b32 v129,v122,v121,2
v_alignbyte_b32 v130,v123,v122,2
v_alignbyte_b32 v131,v124,v123,2
ds_write_b128 v18,v[128:131] offset:6208
L 3
v_alignbyte_b32 v144,v137,v136,2
v_alignbyte_b32 v145,v138,v137,2
v_alignbyte_b32 v146,v139,v138,2
v_alignbyte_b32 v147,v140,v139,2
ds_write_b128 v18,v[144:147] offset:7232
R 152,18,12928
ds_read_b32 v156,v18 offset:12944
R 168,18,13952
ds_read_b32 v172,v18 offset:13968
R 184,18,14976
ds_read_b32 v188,v18 offset:14992
R 200,18,16000
ds_read_b32 v204,v18 offset:16016
L 6
v_alignbyte_b32 v160,v153,v152,2
v_alignbyte_b32 v161,v154,v153,2
v_alignbyte_b32 v162,v155,v154,2
v_alignbyte_b32 v163,v156,v155,2
ds_write_b128 v18,v[160:163] offset:17088
L 5
v_alignbyte_b32 v176,v169,v168,2
v_alignbyte_b32 v177,v170,v169,2
v_alignbyte_b32 v178,v171,v170,2
v_alignbyte_b32 v179,v172,v171,2
ds_write_b128 v18,v[176:179] offset:18112
L 4
v_alignbyte_b32 v192,v185,v184,2
v_alignbyte_b32 v193,v186,v185,2
v_alignbyte_b32 v194,v187,v186,2
v_alignbyte_b32 v195,v188,v187,2
ds_write_b128 v18,v[192:195] offset:19136
L 3
v_alignbyte_b32 v208,v201,v200,2
v_alignbyte_b32 v209,v202,v201,2
v_alignbyte_b32 v210,v203,v202,2
v_alignbyte_b32 v211,v204,v203,2
ds_write_b128 v18,v[208:211] offset:20160
s_lshl_b32 s78,s44,11
s_add_u32 s78,s78,103424
v_add_u32 v18,s78,v4
v_add_u32 v4,v19,v4
s_waitcnt vmcnt(0)
v_cmp_eq_u32 vcc,s53,v13
s_mov_b32 s83,0
s_cmp_eq_u64 vcc,-1
s_cbranch_scc0 Lcv_a_%=
s_mov_b32 s83,1
s_add_u32 m0,s57,0
s_nop 0
D 0,49,2048
D 0,49,1024
D 0,49,0
s_add_u32 m0,s57,12928
s_nop 0
D 0,55,2048
D 0,55,1024
D 0,55,0
Lcv_a_%=:
s_cmp_eq_u32 s51,2
s_cbranch_scc1 Lcv_a2_%=
L 0
R 88,2,3584
R 92,2,3072
R 96,2,3648
R 100,2,3136
R 104,2,3712
R 108,2,3200
R 112,2,3776
R 116,2,3264
L 6
v_mfma_f32_16x16x32_bf16 v[40:43],a[0:3],v[88:91],0
R 120,2,3840
R 124,2,3328
v_mfma_f32_16x16x32_bf16 v[44:47],a[0:3],v[92:95],0
M 44,a[4:7],88
L 6
M 40,a[8:11],96
R 128,2,3904
R 132,2,3392
M 44,a[8:11],100
M 44,a[12:15],96
L 6
M 40,a[16:19],104
R 136,2,3968
R 140,2,3456
M 44,a[16:19],108
M 44,a[20:23],104
L 6
M 40,a[24:27],112
R 144,2,4032
R 148,2,3520
M 44,a[24:27],116
M 44,a[28:31],112
L 6
M 40,a[32:35],120
M 44,a[32:35],124
M 44,a[36:39],120
L 4
M 40,a[40:43],128
M 44,a[40:43],132
M 44,a[44:47],128
L 2
M 40,a[48:51],136
ds_read_b64 v[24:25],v3 offset:3584
ds_read_b64 v[26:27],v3 offset:3072
M 44,a[48:51],140
M 44,a[52:55],136
L 2
M 40,a[56:59],144
M 44,a[56:59],148
M 44,a[60:63],144
L 0
v_and_b32 v36,0xffff0000,v25
v_lshlrev_b32 v37,16,v25
v_and_b32 v38,0xffff0000,v24
v_lshlrev_b32 v39,16,v24
s_nop 0
v_fma_f32 v40,v36,s86,v40
v_fma_f32 v41,v37,s86,v41
v_fma_f32 v42,v38,s86,v42
v_fma_f32 v43,v39,s86,v43
v_cvt_pk_bf16_f32 v40,v40,v41
v_cvt_pk_bf16_f32 v41,v42,v43
ds_write_b64 v18,v[40:41] offset:0
v_and_b32 v36,0xffff0000,v27
v_lshlrev_b32 v37,16,v27
v_and_b32 v38,0xffff0000,v26
v_lshlrev_b32 v39,16,v26
v_fma_f32 v44,v36,s86,v44
v_fma_f32 v45,v37,s86,v45
v_fma_f32 v46,v38,s86,v46
v_fma_f32 v47,v39,s86,v47
v_cvt_pk_bf16_f32 v44,v44,v45
v_cvt_pk_bf16_f32 v45,v46,v47
ds_write_b64 v18,v[44:45] offset:512
s_branch Lcv_a3_%=
Lcv_a2_%=:
L 0
R 88,2,3584
R 92,2,3072
R 96,2,3648
R 100,2,3136
R 104,2,3712
R 108,2,3200
R 112,2,3776
R 116,2,3264
L 6
v_mfma_f32_16x16x32_bf16 v[40:43],a[0:3],v[88:91],0
R 120,2,3840
R 124,2,3328
v_mfma_f32_16x16x32_bf16 v[44:47],a[0:3],v[92:95],0
M 44,a[4:7],88
L 6
M 40,a[8:11],96
R 128,2,3904
R 132,2,3392
M 44,a[8:11],100
M 44,a[12:15],96
L 6
M 40,a[16:19],104
R 136,2,3968
R 140,2,3456
M 44,a[16:19],108
M 44,a[20:23],104
L 6
M 40,a[24:27],112
R 144,2,4032
R 148,2,3520
M 44,a[24:27],116
M 44,a[28:31],112
L 6
M 40,a[32:35],120
R 152,2,16512
R 156,2,16000
M 44,a[32:35],124
M 44,a[36:39],120
L 6
M 40,a[40:43],128
R 160,2,16576
R 164,2,16064
M 44,a[40:43],132
M 44,a[44:47],128
L 6
M 40,a[48:51],136
R 168,2,16640
R 172,2,16128
M 44,a[48:51],140
ds_read_b64 v[24:25],v3 offset:3584
ds_read_b64 v[26:27],v3 offset:3072
M 44,a[52:55],136
L 8
M 40,a[56:59],144
R 176,2,16704
R 180,2,16192
M 44,a[56:59],148
M 44,a[60:63],144
L 8
v_mfma_f32_16x16x32_bf16 v[48:51],a[64:67],v[152:155],0
R 184,2,16768
R 188,2,16256
v_mfma_f32_16x16x32_bf16 v[52:55],a[64:67],v[156:159],0
M 52,a[68:71],152
L 8
M 48,a[72:75],160
R 192,2,16832
R 196,2,16320
M 52,a[72:75],164
L 6
v_and_b32 v36,0xffff0000,v25
M 52,a[76:79],160
v_lshlrev_b32 v37,16,v25
v_and_b32 v38,0xffff0000,v24
v_lshlrev_b32 v39,16,v24
s_nop 0
M 48,a[80:83],168
R 200,2,16896
R 204,2,16384
M 52,a[80:83],172
v_fma_f32 v40,v36,s86,v40
v_fma_f32 v41,v37,s86,v41
M 52,a[84:87],168
v_fma_f32 v42,v38,s86,v42
v_fma_f32 v43,v39,s86,v43
v_cvt_pk_bf16_f32 v40,v40,v41
L 6
M 48,a[88:91],176
R 208,2,16960
R 212,2,16448
M 52,a[88:91],180
v_cvt_pk_bf16_f32 v41,v42,v43
ds_write_b64 v18,v[40:41] offset:0
M 52,a[92:95],176
v_and_b32 v36,0xffff0000,v27
v_lshlrev_b32 v37,16,v27
v_and_b32 v38,0xffff0000,v26
L 7
M 48,a[96:99],184
M 52,a[96:99],188
v_lshlrev_b32 v39,16,v26
v_fma_f32 v44,v36,s86,v44
M 52,a[100:103],184
v_fma_f32 v45,v37,s86,v45
v_fma_f32 v46,v38,s86,v46
v_fma_f32 v47,v39,s86,v47
L 5
M 48,a[104:107],192
M 52,a[104:107],196
v_cvt_pk_bf16_f32 v44,v44,v45
v_cvt_pk_bf16_f32 v45,v46,v47
M 52,a[108:111],192
ds_write_b64 v18,v[44:45] offset:512
L 4
M 48,a[112:115],200
ds_read_b64 v[24:25],v3 offset:16512
ds_read_b64 v[26:27],v3 offset:16000
M 52,a[112:115],204
M 52,a[116:119],200
L 4
M 48,a[120:123],208
M 52,a[120:123],212
M 52,a[124:127],208
L 0
v_and_b32 v36,0xffff0000,v25
v_lshlrev_b32 v37,16,v25
v_and_b32 v38,0xffff0000,v24
v_lshlrev_b32 v39,16,v24
s_nop 0
v_fma_f32 v48,v36,s87,v48
v_fma_f32 v49,v37,s87,v49
v_fma_f32 v50,v38,s87,v50
v_fma_f32 v51,v39,s87,v51
v_cvt_pk_bf16_f32 v48,v48,v49
v_cvt_pk_bf16_f32 v49,v50,v51
ds_write_b64 v18,v[48:49] offset:1024
v_and_b32 v36,0xffff0000,v27
v_lshlrev_b32 v37,16,v27
v_and_b32 v38,0xffff0000,v26
v_lshlrev_b32 v39,16,v26
v_fma_f32 v52,v36,s87,v52
v_fma_f32 v53,v37,s87,v53
v_fma_f32 v54,v38,s87,v54
v_fma_f32 v55,v39,s87,v55
v_cvt_pk_bf16_f32 v52,v52,v53
v_cvt_pk_bf16_f32 v53,v54,v55
ds_write_b64 v18,v[52:53] offset:1536
Lcv_a3_%=:
L 0
s_barrier
R 152,6,0
R 156,6,512
s_mov_b32 s78,98566144
L 1
buffer_store_dwordx4 v[152:155],v7,s[36:39],s78 offen offset:0 sc0 sc1
s_add_u32 s78,s56,99090432
L 0
buffer_store_dwordx4 v[156:159],v7,s[36:39],s78 offen offset:0 sc0 sc1
s_cmp_eq_u32 s83,1
s_cbranch_scc1 Lcv_b_%=
s_waitcnt vmcnt(0)
s_barrier
buffer_store_dword v23,v10,s[36:39],s48 offen offset:0 sc0 sc1
buffer_load_dword v13,v9,s[36:39],s58 offen offset:0 sc1
Lcv_bpb_%=:
)"
R"(s_waitcnt vmcnt(0)
v_cmp_eq_u32 vcc,s53,v13
s_add_u32 s82,s82,1
s_cmp_eq_u64 vcc,-1
s_cbranch_scc1 Lcv_bqb_%=
s_sleep 1
buffer_load_dword v13,v9,s[36:39],s58 offen offset:0 sc1
s_cmp_lt_u32 s82,20000
s_cbranch_scc1 Lcv_bpb_%=
s_waitcnt vmcnt(0)
Lcv_bqb_%=:
s_add_u32 m0,s57,0
s_nop 0
D 0,49,2048
D 0,49,1024
D 0,49,0
s_add_u32 m0,s57,12928
s_nop 0
D 0,55,2048
D 0,55,1024
D 0,55,0
s_waitcnt vmcnt(0)
Lcv_b_%=:
s_waitcnt vmcnt(2)
ds_read2_b32 v[88:89],v1 offset0:0 offset1:1
ds_read2_b32 v[90:91],v1 offset0:2 offset1:3
R 120,2,3584
ds_read2_b32 v[92:93],v1 offset0:128 offset1:129
ds_read2_b32 v[94:95],v1 offset0:130 offset1:131
R 124,2,3072
ds_read2_b32 v[96:97],v14 offset0:0 offset1:1
ds_read2_b32 v[98:99],v14 offset0:2 offset1:3
R 128,2,2560
ds_read2_b32 v[100:101],v14 offset0:128 offset1:129
ds_read2_b32 v[102:103],v14 offset0:130 offset1:131
R 132,2,2048
ds_read2_b32 v[104:105],v15 offset0:0 offset1:1
ds_read2_b32 v[106:107],v15 offset0:2 offset1:3
R 136,2,1536
ds_read2_b32 v[108:109],v15 offset0:128 offset1:129
ds_read2_b32 v[110:111],v15 offset0:130 offset1:131
R 140,2,1024
ds_read2_b32 v[112:113],v16 offset0:0 offset1:1
ds_read2_b32 v[114:115],v16 offset0:2 offset1:3
R 144,2,512
ds_read2_b32 v[116:117],v16 offset0:128 offset1:129
ds_read2_b32 v[118:119],v16 offset0:130 offset1:131
R 148,2,0
L 15
v_mfma_f32_16x16x32_bf16 v[40:43],v[88:91],v[128:131],0
ds_read2_b32 v[152:153],v1 offset0:16 offset1:17
M 40,v[92:95],124
ds_read2_b32 v[154:155],v1 offset0:18 offset1:19
M 40,v[96:99],120
L 14
R 184,2,3648
v_mfma_f32_16x16x32_bf16 v[44:47],v[88:91],v[132:135],0
ds_read2_b32 v[156:157],v1 offset0:144 offset1:145
M 44,v[92:95],128
M 44,v[96:99],124
ds_read2_b32 v[158:159],v1 offset0:146 offset1:147
M 44,v[100:103],120
L 14
R 188,2,3136
v_mfma_f32_16x16x32_bf16 v[48:51],v[88:91],v[136:139],0
ds_read2_b32 v[160:161],v14 offset0:16 offset1:17
M 48,v[92:95],132
ds_read2_b32 v[162:163],v14 offset0:18 offset1:19
M 48,v[96:99],128
M 48,v[100:103],124
R 192,2,2624
M 48,v[104:107],120
L 15
ds_read2_b32 v[164:165],v14 offset0:144 offset1:145
v_mfma_f32_16x16x32_bf16 v[52:55],v[88:91],v[140:143],0
ds_read2_b32 v[166:167],v14 offset0:146 offset1:147
M 52,v[92:95],136
R 196,2,2112
M 52,v[96:99],132
M 52,v[100:103],128
ds_read2_b32 v[168:169],v15 offset0:16 offset1:17
M 52,v[104:107],124
ds_read2_b32 v[170:171],v15 offset0:18 offset1:19
M 52,v[108:111],120
L 15
R 200,2,1600
v_mfma_f32_16x16x32_bf16 v[56:59],v[88:91],v[144:147],0
ds_read2_b32 v[172:173],v15 offset0:144 offset1:145
M 56,v[92:95],140
M 56,v[96:99],136
ds_read2_b32 v[174:175],v15 offset0:146 offset1:147
M 56,v[100:103],132
R 204,2,1088
M 56,v[104:107],128
ds_read2_b32 v[176:177],v16 offset0:16 offset1:17
M 56,v[108:111],124
ds_read2_b32 v[178:179],v16 offset0:18 offset1:19
M 56,v[112:115],120
L 15
v_mfma_f32_16x16x32_bf16 v[60:63],v[88:91],v[148:151],0
R 208,2,576
M 60,v[92:95],144
ds_read2_b32 v[180:181],v16 offset0:144 offset1:145
M 60,v[96:99],140
ds_read2_b32 v[182:183],v16 offset0:146 offset1:147
M 60,v[100:103],136
R 212,2,64
M 60,v[104:107],132
s_cmp_eq_u32 s83,0
s_cbranch_scc1 Lcv_c_%=
s_waitcnt vmcnt(0)
s_barrier
buffer_store_dword v23,v10,s[36:39],s48 offen offset:0 sc0 sc1
Lcv_c_%=:
M 60,v[108:111],128
M 60,v[112:115],124
M 60,v[116:119],120
L 15
M 40,v[152:155],192
ds_read2_b32 v[88:89],v1 offset0:32 offset1:33
M 40,v[156:159],188
ds_read2_b32 v[90:91],v1 offset0:34 offset1:35
M 40,v[160:163],184
L 14
R 120,2,3712
M 44,v[152:155],196
ds_read2_b32 v[92:93],v1 offset0:160 offset1:161
M 44,v[156:159],192
M 44,v[160:163],188
ds_read2_b32 v[94:95],v1 offset0:162 offset1:163
M 44,v[164:167],184
L 14
R 124,2,3200
M 48,v[152:155],200
ds_read2_b32 v[96:97],v14 offset0:32 offset1:33
M 48,v[156:159],196
ds_read2_b32 v[98:99],v14 offset0:34 offset1:35
M 48,v[160:163],192
M 48,v[164:167],188
R 128,2,2688
M 48,v[168:171],184
L 15
ds_read2_b32 v[100:101],v14 offset0:160 offset1:161
M 52,v[152:155],204
ds_read2_b32 v[102:103],v14 offset0:162 offset1:163
M 52,v[156:159],200
R 132,2,2176
M 52,v[160:163],196
M 52,v[164:167],192
ds_read2_b32 v[104:105],v15 offset0:32 offset1:33
M 52,v[168:171],188
ds_read2_b32 v[106:107],v15 offset0:34 offset1:35
M 52,v[172:175],184
L 15
R 136,2,1664
M 56,v[152:155],208
ds_read2_b32 v[108:109],v15 offset0:160 offset1:161
M 56,v[156:159],204
M 56,v[160:163],200
ds_read2_b32 v[110:111],v15 offset0:162 offset1:163
M 56,v[164:167],196
R 140,2,1152
M 56,v[168:171],192
ds_read2_b32 v[112:113],v16 offset0:32 offset1:33
M 56,v[172:175],188
ds_read2_b32 v[114:115],v16 offset0:34 offset1:35
M 56,v[176:179],184
L 15
M 60,v[152:155],212
R 144,2,640
M 60,v[156:159],208
ds_read2_b32 v[116:117],v16 offset0:160 offset1:161
M 60,v[160:163],204
ds_read2_b32 v[118:119],v16 offset0:162 offset1:163
M 60,v[164:167],200
R 148,2,128
M 60,v[168:171],196
M 60,v[172:175],192
M 60,v[176:179],188
M 60,v[180:183],184
L 15
M 40,v[88:91],128
ds_read2_b32 v[152:153],v1 offset0:48 offset1:49
M 40,v[92:95],124
ds_read2_b32 v[154:155],v1 offset0:50 offset1:51
M 40,v[96:99],120
L 14
R 184,2,3776
M 44,v[88:91],132
ds_read2_b32 v[156:157],v1 offset0:176 offset1:177
M 44,v[92:95],128
M 44,v[96:99],124
ds_read2_b32 v[158:159],v1 offset0:178 offset1:179
M 44,v[100:103],120
L 14
R 188,2,3264
M 48,v[88:91],136
ds_read2_b32 v[160:161],v14 offset0:48 offset1:49
M 48,v[92:95],132
ds_read2_b32 v[162:163],v14 offset0:50 offset1:51
M 48,v[96:99],128
M 48,v[100:103],124
R 192,2,2752
M 48,v[104:107],120
L 15
ds_read2_b32 v[164:165],v14 offset0:176 offset1:177
M 52,v[88:91],140
s_mov_b32 m0,163584
s_mov_b32 s78,0
ds_read2_b32 v[166:167],v14 offset0:178 offset1:179
M 52,v[92:95],136
R 196,2,2240
M 52,v[96:99],132
buffer_load_dword v11,s[40:43],s78 offen offset:0  lds
M 52,v[100:103],128
ds_read2_b32 v[168:169],v15 offset0:48 offset1:49
M 52,v[104:107],124
ds_read2_b32 v[170:171],v15 offset0:50 offset1:51
M 52,v[108:111],120
L 15
R 200,2,1728
M 56,v[88:91],144
ds_read2_b32 v[172:173],v15 offset0:176 offset1:177
M 56,v[92:95],140
M 56,v[96:99],136
ds_read2_b32 v[174:175],v15 offset0:178 offset1:179
M 56,v[100:103],132
R 204,2,1216
M 56,v[104:107],128
ds_read2_b32 v[176:177],v16 offset0:48 offset1:49
M 56,v[108:111],124
ds_read2_b32 v[178:179],v16 offset0:50 offset1:51
M 56,v[112:115],120
L 15
M 60,v[88:91],148
R 208,2,704
M 60,v[92:95],144
ds_read2_b32 v[180:181],v16 offset0:176 offset1:177
M 60,v[96:99],140
ds_read2_b32 v[182:183],v16 offset0:178 offset1:179
M 60,v[100:103],136
R 212,2,192
M 60,v[104:107],132
M 60,v[108:111],128
M 60,v[112:115],124
M 60,v[116:119],120
L 15
M 40,v[152:155],192
ds_read2_b32 v[88:89],v1 offset0:64 offset1:65
M 40,v[156:159],188
ds_read2_b32 v[90:91],v1 offset0:66 offset1:67
M 40,v[160:163],184
L 14
R 120,2,3840
M 44,v[152:155],196
ds_read2_b32 v[92:93],v1 offset0:192 offset1:193
M 44,v[156:159],192
M 44,v[160:163],188
ds_read2_b32 v[94:95],v1 offset0:194 offset1:195
M 44,v[164:167],184
L 14
R 124,2,3328
M 48,v[152:155],200
ds_read2_b32 v[96:97],v14 offset0:64 offset1:65
M 48,v[156:159],196
ds_read2_b32 v[98:99],v14 offset0:66 offset1:67
M 48,v[160:163],192
M 48,v[164:167],188
R 128,2,2816
M 48,v[168:171],184
L 15
ds_read2_b32 v[100:101],v14 offset0:192 offset1:193
M 52,v[152:155],204
ds_read2_b32 v[102:103],v14 offset0:194 offset1:195
M 52,v[156:159],200
R 132,2,2304
M 52,v[160:163],196
M 52,v[164:167],192
ds_read2_b32 v[104:105],v15 offset0:64 offset1:65
M 52,v[168:171],188
ds_read2_b32 v[106:107],v15 offset0:66 offset1:67
M 52,v[172:175],184
L 15
R 136,2,1792
M 56,v[152:155],208
ds_read2_b32 v[108:109],v15 offset0:192 offset1:193
M 56,v[156:159],204
M 56,v[160:163],200
ds_read2_b32 v[110:111],v15 offset0:194 offset1:195
M 56,v[164:167],196
R 140,2,1280
M 56,v[168:171],192
ds_read2_b32 v[112:113],v16 offset0:64 offset1:65
M 56,v[172:175],188
ds_read2_b32 v[114:115],v16 offset0:66 offset1:67
M 56,v[176:179],184
L 15
M 60,v[152:155],212
R 144,2,768
M 60,v[156:159],208
ds_read2_b32 v[116:117],v16 offset0:192 offset1:193
M 60,v[160:163],204
ds_read2_b32 v[118:119],v16 offset0:194 offset1:195
M 60,v[164:167],200
R 148,2,256
M 60,v[168:171],196
M 60,v[172:175],192
M 60,v[176:179],188
M 60,v[180:183],184
L 15
M 40,v[88:91],128
ds_read2_b32 v[152:153],v1 offset0:80 offset1:81
M 40,v[92:95],124
ds_read2_b32 v[154:155],v1 offset0:82 offset1:83
M 40,v[96:99],120
L 14
R 184,2,3904
M 44,v[88:91],132
ds_read2_b32 v[156:157],v1 offset0:208 offset1:209
M 44,v[92:95],128
M 44,v[96:99],124
ds_read2_b32 v[158:159],v1 offset0:210 offset1:211
M 44,v[100:103],120
L 14
R 188,2,3392
M 48,v[88:91],136
ds_read2_b32 v[160:161],v14 offset0:80 offset1:81
M 48,v[92:95],132
ds_read2_b32 v[162:163],v14 offset0:82 offset1:83
M 48,v[96:99],128
M 48,v[100:103],124
R 192,2,2880
M 48,v[104:107],120
L 15
ds_read2_b32 v[164:165],v14 offset0:208 offset1:209
M 52,v[88:91],140
ds_read2_b32 v[166:167],v14 offset0:210 offset1:211
M 52,v[92:95],136
R 196,2,2368
M 52,v[96:99],132
M 52,v[100:103],128
ds_read2_b32 v[168:169],v15 offset0:80 offset1:81
M 52,v[104:107],124
ds_read2_b32 v[170:171],v15 offset0:82 offset1:83
M 52,v[108:111],120
L 15
R 200,2,1856
M 56,v[88:91],144
ds_read2_b32 v[172:173],v15 offset0:208 offset1:209
M 56,v[92:95],140
M 56,v[96:99],136
ds_read2_b32 v[174:175],v15 offset0:210 offset1:211
M 56,v[100:103],132
R 204,2,1344
M 56,v[104:107],128
ds_read2_b32 v[176:177],v16 offset0:80 offset1:81
M 56,v[108:111],124
ds_read2_b32 v[178:179],v16 offset0:82 offset1:83
M 56,v[112:115],120
L 15
M 60,v[88:91],148
R 208,2,832
M 60,v[92:95],144
ds_read2_b32 v[180:181],v16 offset0:208 offset1:209
M 60,v[96:99],140
ds_read2_b32 v[182:183],v16 offset0:210 offset1:211
M 60,v[100:103],136
R 212,2,320
M 60,v[104:107],132
M 60,v[108:111],128
M 60,v[112:115],124
M 60,v[116:119],120
L 15
M 40,v[152:155],192
ds_read2_b32 v[88:89],v1 offset0:96 offset1:97
M 40,v[156:159],188
ds_read2_b32 v[90:91],v1 offset0:98 offset1:99
M 40,v[160:163],184
L 14
R 120,2,3968
M 44,v[152:155],196
s_mov_b32 m0,163584
s_mov_b32 s78,3741696
ds_read2_b32 v[92:93],v1 offset0:224 offset1:225
M 44,v[156:159],192
M 44,v[160:163],188
ds_read2_b32 v[94:95],v1 offset0:226 offset1:227
M 44,v[164:167],184
L 14
R 124,2,3456
M 48,v[152:155],200
ds_read2_b32 v[96:97],v14 offset0:96 offset1:97
M 48,v[156:159],196
ds_read2_b32 v[98:99],v14 offset0:98 offset1:99
M 48,v[160:163],192
buffer_load_dword v11,s[40:43],s78 offen offset:0  lds
M 48,v[164:167],188
R 128,2,2944
M 48,v[168:171],184
L 15
ds_read2_b32 v[100:101],v14 offset0:224 offset1:225
M 52,v[152:155],204
ds_read2_b32 v[102:103],v14 offset0:226 offset1:227
M 52,v[156:159],200
R 132,2,2432
M 52,v[160:163],196
M 52,v[164:167],192
ds_read2_b32 v[104:105],v15 offset0:96 offset1:97
M 52,v[168:171],188
ds_read2_b32 v[106:107],v15 offset0:98 offset1:99
M 52,v[172:175],184
L 15
R 136,2,1920
M 56,v[152:155],208
ds_read2_b32 v[108:109],v15 offset0:224 offset1:225
M 56,v[156:159],204
M 56,v[160:163],200
ds_read2_b32 v[110:111],v15 offset0:226 offset1:227
M 56,v[164:167],196
R 140,2,1408
M 56,v[168:171],192
ds_read2_b32 v[112:113],v16 offset0:96 offset1:97
M 56,v[172:175],188
ds_read2_b32 v[114:115],v16 offset0:98 offset1:99
M 56,v[176:179],184
L 15
M 60,v[152:155],212
R 144,2,896
M 60,v[156:159],208
ds_read2_b32 v[116:117],v16 offset0:224 offset1:225
M 60,v[160:163],204
ds_read2_b32 v[118:119],v16 offset0:226 offset1:227
M 60,v[164:167],200
R 148,2,384
M 60,v[168:171],196
M 60,v[172:175],192
M 60,v[176:179],188
M 60,v[180:183],184
L 15
M 40,v[88:91],128
ds_read2_b32 v[152:153],v1 offset0:112 offset1:113
M 40,v[92:95],124
ds_read2_b32 v[154:155],v1 offset0:114 offset1:115
M 40,v[96:99],120
L 14
R 184,2,4032
M 44,v[88:91],132
ds_read2_b32 v[156:157],v1 offset0:240 offset1:241
M 44,v[92:95],128
M 44,v[96:99],124
ds_read2_b32 v[158:159],v1 offset0:242 offset1:243
v_add_u32 v1,12928,v1
M 44,v[100:103],120
L 14
R 188,2,3520
M 48,v[88:91],136
ds_read2_b32 v[160:161],v14 offset0:112 offset1:113
M 48,v[92:95],132
ds_read2_b32 v[162:163],v14 offset0:114 offset1:115
M 48,v[96:99],128
M 48,v[100:103],124
R 192,2,3008
M 48,v[104:107],120
L 15
ds_read2_b32 v[164:165],v14 offset0:240 offset1:241
M 52,v[88:91],140
ds_read2_b32 v[166:167],v14 offset0:242 offset1:243
v_add_u32 v14,12928,v14
M 52,v[92:95],136
R 196,2,2496
M 52,v[96:99],132
M 52,v[100:103],128
ds_read2_b32 v[168:169],v15 offset0:112 offset1:113
M 52,v[104:107],124
ds_read2_b32 v[170:171],v15 offset0:114 offset1:115
M 52,v[108:111],120
L 15
R 200,2,1984
M 56,v[88:91],144
ds_read2_b32 v[172:173],v15 offset0:240 offset1:241
M 56,v[92:95],140
M 56,v[96:99],136
ds_read2_b32 v[174:175],v15 offset0:242 offset1:243
v_add_u32 v15,12928,v15
M 56,v[100:103],132
R 204,2,1472
M 56,v[104:107],128
ds_read2_b32 v[176:177],v16 offset0:112 offset1:113
M 56,v[108:111],124
ds_read2_b32 v[178:179],v16 offset0:114 offset1:115
M 56,v[112:115],120
L 15
M 60,v[88:91],148
R 208,2,960
M 60,v[92:95],144
ds_read2_b32 v[180:181],v16 offset0:240 offset1:241
M 60,v[96:99],140
ds_read2_b32 v[182:183],v16 offset0:242 offset1:243
v_add_u32 v16,12928,v16
M 60,v[100:103],136
)"
R"(R 212,2,448
M 60,v[104:107],132
M 60,v[108:111],128
M 60,v[112:115],124
M 60,v[116:119],120
L 15
M 40,v[152:155],192
ds_read2_b32 v[88:89],v1 offset0:0 offset1:1
M 40,v[156:159],188
ds_read2_b32 v[90:91],v1 offset0:2 offset1:3
M 40,v[160:163],184
L 14
R 120,2,16512
M 44,v[152:155],196
ds_read2_b32 v[92:93],v1 offset0:128 offset1:129
M 44,v[156:159],192
M 44,v[160:163],188
ds_read2_b32 v[94:95],v1 offset0:130 offset1:131
M 44,v[164:167],184
L 14
R 124,2,16000
M 48,v[152:155],200
ds_read2_b32 v[96:97],v14 offset0:0 offset1:1
M 48,v[156:159],196
ds_read2_b32 v[98:99],v14 offset0:2 offset1:3
M 48,v[160:163],192
M 48,v[164:167],188
R 128,2,15488
M 48,v[168:171],184
L 15
ds_read2_b32 v[100:101],v14 offset0:128 offset1:129
M 52,v[152:155],204
ds_read2_b32 v[102:103],v14 offset0:130 offset1:131
M 52,v[156:159],200
R 132,2,14976
M 52,v[160:163],196
M 52,v[164:167],192
ds_read2_b32 v[104:105],v15 offset0:0 offset1:1
M 52,v[168:171],188
ds_read2_b32 v[106:107],v15 offset0:2 offset1:3
M 52,v[172:175],184
L 15
R 136,2,14464
M 56,v[152:155],208
ds_read2_b32 v[108:109],v15 offset0:128 offset1:129
M 56,v[156:159],204
M 56,v[160:163],200
ds_read2_b32 v[110:111],v15 offset0:130 offset1:131
M 56,v[164:167],196
R 140,2,13952
M 56,v[168:171],192
ds_read2_b32 v[112:113],v16 offset0:0 offset1:1
M 56,v[172:175],188
ds_read2_b32 v[114:115],v16 offset0:2 offset1:3
M 56,v[176:179],184
L 15
M 60,v[152:155],212
R 144,2,13440
M 60,v[156:159],208
ds_read2_b32 v[116:117],v16 offset0:128 offset1:129
M 60,v[160:163],204
ds_read2_b32 v[118:119],v16 offset0:130 offset1:131
M 60,v[164:167],200
R 148,2,12928
M 60,v[168:171],196
M 60,v[172:175],192
M 60,v[176:179],188
M 60,v[180:183],184
s_cmp_eq_u32 s51,2
s_cbranch_scc1 Lcv_d_%=
ds_read_b64 v[24:25],v3 offset:2560
ds_read_b64 v[26:27],v3 offset:2048
ds_read_b64 v[28:29],v3 offset:1536
ds_read_b64 v[30:31],v3 offset:1024
ds_read_b64 v[32:33],v3 offset:512
ds_read_b64 v[34:35],v3 offset:0
L 0
v_and_b32 v36,0xffff0000,v25
v_lshlrev_b32 v37,16,v25
v_and_b32 v38,0xffff0000,v24
v_lshlrev_b32 v39,16,v24
v_fma_f32 v40,v36,s86,v40
v_fma_f32 v41,v37,s86,v41
v_fma_f32 v42,v38,s86,v42
v_fma_f32 v43,v39,s86,v43
v_cvt_pk_bf16_f32 v40,v40,v41
v_cvt_pk_bf16_f32 v41,v42,v43
ds_write_b64 v4,v[40:41] offset:0
v_and_b32 v36,0xffff0000,v27
v_lshlrev_b32 v37,16,v27
v_and_b32 v38,0xffff0000,v26
v_lshlrev_b32 v39,16,v26
v_fma_f32 v44,v36,s86,v44
v_fma_f32 v45,v37,s86,v45
v_fma_f32 v46,v38,s86,v46
v_fma_f32 v47,v39,s86,v47
v_cvt_pk_bf16_f32 v44,v44,v45
v_cvt_pk_bf16_f32 v45,v46,v47
ds_write_b64 v4,v[44:45] offset:512
v_and_b32 v36,0xffff0000,v29
v_lshlrev_b32 v37,16,v29
v_and_b32 v38,0xffff0000,v28
v_lshlrev_b32 v39,16,v28
v_fma_f32 v48,v36,s86,v48
v_fma_f32 v49,v37,s86,v49
v_fma_f32 v50,v38,s86,v50
v_fma_f32 v51,v39,s86,v51
v_cvt_pk_bf16_f32 v48,v48,v49
v_cvt_pk_bf16_f32 v49,v50,v51
ds_write_b64 v4,v[48:49] offset:1024
v_and_b32 v36,0xffff0000,v31
v_lshlrev_b32 v37,16,v31
v_and_b32 v38,0xffff0000,v30
v_lshlrev_b32 v39,16,v30
v_fma_f32 v52,v36,s86,v52
v_fma_f32 v53,v37,s86,v53
v_fma_f32 v54,v38,s86,v54
v_fma_f32 v55,v39,s86,v55
v_cvt_pk_bf16_f32 v52,v52,v53
v_cvt_pk_bf16_f32 v53,v54,v55
ds_write_b64 v4,v[52:53] offset:1536
v_and_b32 v36,0xffff0000,v33
v_lshlrev_b32 v37,16,v33
v_and_b32 v38,0xffff0000,v32
v_lshlrev_b32 v39,16,v32
v_fma_f32 v56,v36,s86,v56
v_fma_f32 v57,v37,s86,v57
v_fma_f32 v58,v38,s86,v58
v_fma_f32 v59,v39,s86,v59
v_cvt_pk_bf16_f32 v56,v56,v57
v_cvt_pk_bf16_f32 v57,v58,v59
ds_write_b64 v4,v[56:57] offset:2048
v_and_b32 v36,0xffff0000,v35
v_lshlrev_b32 v37,16,v35
v_and_b32 v38,0xffff0000,v34
v_lshlrev_b32 v39,16,v34
v_fma_f32 v60,v36,s86,v60
v_fma_f32 v61,v37,s86,v61
v_fma_f32 v62,v38,s86,v62
v_fma_f32 v63,v39,s86,v63
v_cvt_pk_bf16_f32 v60,v60,v61
v_cvt_pk_bf16_f32 v61,v62,v63
ds_write_b64 v4,v[60:61] offset:2560
s_branch Lcv_e_%=
Lcv_d_%=:
L 15
v_mfma_f32_16x16x32_bf16 v[64:67],v[88:91],v[128:131],0
ds_read2_b32 v[152:153],v1 offset0:16 offset1:17
ds_read_b64 v[24:25],v3 offset:2560
M 64,v[92:95],124
ds_read2_b32 v[154:155],v1 offset0:18 offset1:19
ds_read_b64 v[26:27],v3 offset:2048
M 64,v[96:99],120
L 15
R 184,2,16576
ds_read_b64 v[28:29],v3 offset:1536
v_mfma_f32_16x16x32_bf16 v[68:71],v[88:91],v[132:135],0
ds_read2_b32 v[156:157],v1 offset0:144 offset1:145
ds_read_b64 v[30:31],v3 offset:1024
M 68,v[92:95],128
ds_read_b64 v[32:33],v3 offset:512
ds_read_b64 v[34:35],v3 offset:0
M 68,v[96:99],124
ds_read2_b32 v[158:159],v1 offset0:146 offset1:147
M 68,v[100:103],120
L 15
R 188,2,16064
v_mfma_f32_16x16x32_bf16 v[72:75],v[88:91],v[136:139],0
ds_read2_b32 v[160:161],v14 offset0:16 offset1:17
M 72,v[92:95],132
s_mov_b32 m0,163584
s_mov_b32 s78,0
ds_read2_b32 v[162:163],v14 offset0:18 offset1:19
M 72,v[96:99],128
M 72,v[100:103],124
L 4
R 192,2,15552
v_and_b32 v36,0xffff0000,v25
M 72,v[104:107],120
s_nop 0
ds_read2_b32 v[164:165],v14 offset0:144 offset1:145
v_lshlrev_b32 v37,16,v25
v_mfma_f32_16x16x32_bf16 v[76:79],v[88:91],v[140:143],0
ds_read2_b32 v[166:167],v14 offset0:146 offset1:147
v_and_b32 v38,0xffff0000,v24
M 76,v[92:95],136
R 196,2,15040
v_lshlrev_b32 v39,16,v24
M 76,v[96:99],132
buffer_load_dword v12,s[60:63],s78 offen offset:0  lds
M 76,v[100:103],128
ds_read2_b32 v[168:169],v15 offset0:16 offset1:17
v_fma_f32 v40,v36,s86,v40
M 76,v[104:107],124
ds_read2_b32 v[170:171],v15 offset0:18 offset1:19
v_fma_f32 v41,v37,s86,v41
M 76,v[108:111],120
s_nop 0
R 200,2,14528
v_fma_f32 v42,v38,s86,v42
v_mfma_f32_16x16x32_bf16 v[80:83],v[88:91],v[144:147],0
ds_read2_b32 v[172:173],v15 offset0:144 offset1:145
v_fma_f32 v43,v39,s86,v43
M 80,v[92:95],140
v_cvt_pk_bf16_f32 v40,v40,v41
v_cvt_pk_bf16_f32 v41,v42,v43
M 80,v[96:99],136
ds_read2_b32 v[174:175],v15 offset0:146 offset1:147
v_and_b32 v36,0xffff0000,v27
M 80,v[100:103],132
R 204,2,14016
v_lshlrev_b32 v37,16,v27
M 80,v[104:107],128
ds_read2_b32 v[176:177],v16 offset0:16 offset1:17
v_and_b32 v38,0xffff0000,v26
M 80,v[108:111],124
ds_read2_b32 v[178:179],v16 offset0:18 offset1:19
v_lshlrev_b32 v39,16,v26
M 80,v[112:115],120
s_nop 0
ds_write_b64 v4,v[40:41] offset:0
v_mfma_f32_16x16x32_bf16 v[84:87],v[88:91],v[148:151],0
R 208,2,13504
v_fma_f32 v44,v36,s86,v44
M 84,v[92:95],144
ds_read2_b32 v[180:181],v16 offset0:144 offset1:145
v_fma_f32 v45,v37,s86,v45
M 84,v[96:99],140
ds_read2_b32 v[182:183],v16 offset0:146 offset1:147
v_fma_f32 v46,v38,s86,v46
M 84,v[100:103],136
R 212,2,12992
v_fma_f32 v47,v39,s86,v47
M 84,v[104:107],132
v_cvt_pk_bf16_f32 v44,v44,v45
v_cvt_pk_bf16_f32 v45,v46,v47
M 84,v[108:111],128
ds_write_b64 v4,v[44:45] offset:512
M 84,v[112:115],124
v_and_b32 v36,0xffff0000,v29
v_lshlrev_b32 v37,16,v29
M 84,v[116:119],120
L 15
v_and_b32 v38,0xffff0000,v28
v_lshlrev_b32 v39,16,v28
M 64,v[152:155],192
ds_read2_b32 v[88:89],v1 offset0:32 offset1:33
v_fma_f32 v48,v36,s86,v48
M 64,v[156:159],188
ds_read2_b32 v[90:91],v1 offset0:34 offset1:35
v_fma_f32 v49,v37,s86,v49
M 64,v[160:163],184
L 15
R 120,2,16640
v_fma_f32 v50,v38,s86,v50
M 68,v[152:155],196
ds_read2_b32 v[92:93],v1 offset0:160 offset1:161
v_fma_f32 v51,v39,s86,v51
M 68,v[156:159],192
v_cvt_pk_bf16_f32 v48,v48,v49
v_cvt_pk_bf16_f32 v49,v50,v51
M 68,v[160:163],188
ds_read2_b32 v[94:95],v1 offset0:162 offset1:163
v_and_b32 v36,0xffff0000,v31
M 68,v[164:167],184
L 15
R 124,2,16128
v_lshlrev_b32 v37,16,v31
M 72,v[152:155],200
ds_read2_b32 v[96:97],v14 offset0:32 offset1:33
v_and_b32 v38,0xffff0000,v30
M 72,v[156:159],196
ds_read2_b32 v[98:99],v14 offset0:34 offset1:35
v_lshlrev_b32 v39,16,v30
M 72,v[160:163],192
ds_write_b64 v4,v[48:49] offset:1024
M 72,v[164:167],188
R 128,2,15616
v_fma_f32 v52,v36,s86,v52
M 72,v[168:171],184
L 15
ds_read2_b32 v[100:101],v14 offset0:160 offset1:161
v_fma_f32 v53,v37,s86,v53
M 76,v[152:155],204
ds_read2_b32 v[102:103],v14 offset0:162 offset1:163
v_fma_f32 v54,v38,s86,v54
M 76,v[156:159],200
R 132,2,15104
v_fma_f32 v55,v39,s86,v55
M 76,v[160:163],196
v_cvt_pk_bf16_f32 v52,v52,v53
v_cvt_pk_bf16_f32 v53,v54,v55
M 76,v[164:167],192
ds_read2_b32 v[104:105],v15 offset0:32 offset1:33
v_and_b32 v36,0xffff0000,v33
M 76,v[168:171],188
ds_read2_b32 v[106:107],v15 offset0:34 offset1:35
v_lshlrev_b32 v37,16,v33
M 76,v[172:175],184
L 15
R 136,2,14592
v_and_b32 v38,0xffff0000,v32
M 80,v[152:155],208
ds_read2_b32 v[108:109],v15 offset0:160 offset1:161
v_lshlrev_b32 v39,16,v32
M 80,v[156:159],204
ds_write_b64 v4,v[52:53] offset:1536
M 80,v[160:163],200
ds_read2_b32 v[110:111],v15 offset0:162 offset1:163
v_fma_f32 v56,v36,s86,v56
M 80,v[164:167],196
R 140,2,14080
v_fma_f32 v57,v37,s86,v57
M 80,v[168:171],192
ds_read2_b32 v[112:113],v16 offset0:32 offset1:33
v_fma_f32 v58,v38,s86,v58
M 80,v[172:175],188
ds_read2_b32 v[114:115],v16 offset0:34 offset1:35
v_fma_f32 v59,v39,s86,v59
M 80,v[176:179],184
L 15
v_cvt_pk_bf16_f32 v56,v56,v57
v_cvt_pk_bf16_f32 v57,v58,v59
M 84,v[152:155],212
R 144,2,13568
v_and_b32 v36,0xffff0000,v35
M 84,v[156:159],208
ds_read2_b32 v[116:117],v16 offset0:160 offset1:161
v_lshlrev_b32 v37,16,v35
M 84,v[160:163],204
ds_read2_b32 v[118:119],v16 offset0:162 offset1:163
v_and_b32 v38,0xffff0000,v34
M 84,v[164:167],200
R 148,2,13056
v_lshlrev_b32 v39,16,v34
M 84,v[168:171],196
ds_write_b64 v4,v[56:57] offset:2048
M 84,v[172:175],192
v_fma_f32 v60,v36,s86,v60
v_fma_f32 v61,v37,s86,v61
M 84,v[176:179],188
v_fma_f32 v62,v38,s86,v62
v_fma_f32 v63,v39,s86,v63
M 84,v[180:183],184
L 15
v_cvt_pk_bf16_f32 v60,v60,v61
v_cvt_pk_bf16_f32 v61,v62,v63
M 64,v[88:91],128
ds_read2_b32 v[152:153],v1 offset0:48 offset1:49
M 64,v[92:95],124
ds_read2_b32 v[154:155],v1 offset0:50 offset1:51
M 64,v[96:99],120
L 15
R 184,2,16704
M 68,v[88:91],132
ds_read2_b32 v[156:157],v1 offset0:176 offset1:177
M 68,v[92:95],128
ds_write_b64 v4,v[60:61] offset:2560
M 68,v[96:99],124
ds_read2_b32 v[158:159],v1 offset0:178 offset1:179
M 68,v[100:103],120
L 15
R 188,2,16192
M 72,v[88:91],136
ds_read2_b32 v[160:161],v14 offset0:48 offset1:49
M 72,v[92:95],132
ds_read2_b32 v[162:163],v14 offset0:50 offset1:51
M 72,v[96:99],128
M 72,v[100:103],124
R 192,2,15680
M 72,v[104:107],120
L 15
ds_read2_b32 v[164:165],v14 offset0:176 offset1:177
M 76,v[88:91],140
ds_read2_b32 v[166:167],v14 offset0:178 offset1:179
M 76,v[92:95],136
R 196,2,15168
M 76,v[96:99],132
M 76,v[100:103],128
ds_read2_b32 v[168:169],v15 offset0:48 offset1:49
M 76,v[104:107],124
ds_read2_b32 v[170:171],v15 offset0:50 offset1:51
M 76,v[108:111],120
L 15
R 200,2,14656
M 80,v[88:91],144
ds_read2_b32 v[172:173],v15 offset0:176 offset1:177
M 80,v[92:95],140
M 80,v[96:99],136
ds_read2_b32 v[174:175],v15 offset0:178 offset1:179
M 80,v[100:103],132
R 204,2,14144
M 80,v[104:107],128
s_mov_b32 m0,163584
s_mov_b32 s78,1847296
ds_read2_b32 v[176:177],v16 offset0:48 offset1:49
M 80,v[108:111],124
ds_read2_b32 v[178:179],v16 offset0:50 offset1:51
M 80,v[112:115],120
L 15
buffer_load_dword v12,s[60:63],s78 offen offset:0  lds
M 84,v[88:91],148
R 208,2,13632
M 84,v[92:95],144
ds_read2_b32 v[180:181],v16 offset0:176 offset1:177
M 84,v[96:99],140
ds_read2_b32 v[182:183],v16 offset0:178 offset1:179
M 84,v[100:103],136
R 212,2,13120
M 84,v[104:107],132
M 84,v[108:111],128
M 84,v[112:115],124
M 84,v[116:119],120
L 15
M 64,v[152:155],192
ds_read2_b32 v[88:89],v1 offset0:64 offset1:65
M 64,v[156:159],188
ds_read2_b32 v[90:91],v1 offset0:66 offset1:67
M 64,v[160:163],184
L 14
R 120,2,16768
M 68,v[152:155],196
ds_read2_b32 v[92:93],v1 offset0:192 offset1:193
M 68,v[156:159],192
M 68,v[160:163],188
ds_read2_b32 v[94:95],v1 offset0:194 offset1:195
M 68,v[164:167],184
L 14
R 124,2,16256
M 72,v[152:155],200
ds_read2_b32 v[96:97],v14 offset0:64 offset1:65
M 72,v[156:159],196
ds_read2_b32 v[98:99],v14 offset0:66 offset1:67
M 72,v[160:163],192
M 72,v[164:167],188
R 128,2,15744
M 72,v[168:171],184
L 15
ds_read2_b32 v[100:101],v14 offset0:192 offset1:193
M 76,v[152:155],204
ds_read2_b32 v[102:103],v14 offset0:194 offset1:195
M 76,v[156:159],200
R 132,2,15232
M 76,v[160:163],196
M 76,v[164:167],192
ds_read2_b32 v[104:105],v15 offset0:64 offset1:65
M 76,v[168:171],188
ds_read2_b32 v[106:107],v15 offset0:66 offset1:67
M 76,v[172:175],184
L 15
R 136,2,14720
M 80,v[152:155],208
ds_read2_b32 v[108:109],v15 offset0:192 offset1:193
M 80,v[156:159],204
M 80,v[160:163],200
ds_read2_b32 v[110:111],v15 offset0:194 offset1:195
M 80,v[164:167],196
R 140,2,14208
M 80,v[168:171],192
ds_read2_b32 v[112:113],v16 offset0:64 offset1:65
M 80,v[172:175],188
ds_read2_b32 v[114:115],v16 offset0:66 offset1:67
M 80,v[176:179],184
L 15
M 84,v[152:155],212
R 144,2,13696
M 84,v[156:159],208
ds_read2_b32 v[116:117],v16 offset0:192 offset1:193
M 84,v[160:163],204
ds_read2_b32 v[118:119],v16 offset0:194 offset1:195
M 84,v[164:167],200
R 148,2,13184
M 84,v[168:171],196
M 84,v[172:175],192
M 84,v[176:179],188
M 84,v[180:183],184
L 15
M 64,v[88:91],128
ds_read2_b32 v[152:153],v1 offset0:80 offset1:81
M 64,v[92:95],124
ds_read2_b32 v[154:155],v1 offset0:82 offset1:83
M 64,v[96:99],120
L 14
R 184,2,16832
M 68,v[88:91],132
ds_read2_b32 v[156:157],v1 offset0:208 offset1:209
M 68,v[92:95],128
M 68,v[96:99],124
ds_read2_b32 v[158:159],v1 offset0:210 offset1:211
M 68,v[100:103],120
L 14
R 188,2,16320
M 72,v[88:91],136
ds_read2_b32 v[160:161],v14 offset0:80 offset1:81
M 72,v[92:95],132
ds_read2_b32 v[162:163],v14 offset0:82 offset1:83
M 72,v[96:99],128
M 72,v[100:103],124
R 192,2,15808
M 72,v[104:107],120
)"
R"(L 15
ds_read2_b32 v[164:165],v14 offset0:208 offset1:209
M 76,v[88:91],140
ds_read2_b32 v[166:167],v14 offset0:210 offset1:211
M 76,v[92:95],136
R 196,2,15296
M 76,v[96:99],132
M 76,v[100:103],128
ds_read2_b32 v[168:169],v15 offset0:80 offset1:81
M 76,v[104:107],124
ds_read2_b32 v[170:171],v15 offset0:82 offset1:83
M 76,v[108:111],120
L 15
R 200,2,14784
M 80,v[88:91],144
ds_read2_b32 v[172:173],v15 offset0:208 offset1:209
M 80,v[92:95],140
M 80,v[96:99],136
ds_read2_b32 v[174:175],v15 offset0:210 offset1:211
M 80,v[100:103],132
R 204,2,14272
M 80,v[104:107],128
ds_read2_b32 v[176:177],v16 offset0:80 offset1:81
M 80,v[108:111],124
ds_read2_b32 v[178:179],v16 offset0:82 offset1:83
M 80,v[112:115],120
L 15
M 84,v[88:91],148
R 208,2,13760
M 84,v[92:95],144
ds_read2_b32 v[180:181],v16 offset0:208 offset1:209
M 84,v[96:99],140
ds_read2_b32 v[182:183],v16 offset0:210 offset1:211
M 84,v[100:103],136
R 212,2,13248
M 84,v[104:107],132
M 84,v[108:111],128
M 84,v[112:115],124
M 84,v[116:119],120
L 15
M 64,v[152:155],192
ds_read2_b32 v[88:89],v1 offset0:96 offset1:97
M 64,v[156:159],188
ds_read2_b32 v[90:91],v1 offset0:98 offset1:99
M 64,v[160:163],184
L 14
R 120,2,16896
M 68,v[152:155],196
ds_read2_b32 v[92:93],v1 offset0:224 offset1:225
M 68,v[156:159],192
M 68,v[160:163],188
ds_read2_b32 v[94:95],v1 offset0:226 offset1:227
M 68,v[164:167],184
L 14
R 124,2,16384
M 72,v[152:155],200
ds_read2_b32 v[96:97],v14 offset0:96 offset1:97
M 72,v[156:159],196
ds_read2_b32 v[98:99],v14 offset0:98 offset1:99
M 72,v[160:163],192
M 72,v[164:167],188
R 128,2,15872
M 72,v[168:171],184
L 15
ds_read2_b32 v[100:101],v14 offset0:224 offset1:225
M 76,v[152:155],204
ds_read2_b32 v[102:103],v14 offset0:226 offset1:227
M 76,v[156:159],200
R 132,2,15360
M 76,v[160:163],196
M 76,v[164:167],192
ds_read2_b32 v[104:105],v15 offset0:96 offset1:97
M 76,v[168:171],188
ds_read2_b32 v[106:107],v15 offset0:98 offset1:99
M 76,v[172:175],184
L 15
R 136,2,14848
M 80,v[152:155],208
ds_read2_b32 v[108:109],v15 offset0:224 offset1:225
M 80,v[156:159],204
M 80,v[160:163],200
ds_read2_b32 v[110:111],v15 offset0:226 offset1:227
M 80,v[164:167],196
R 140,2,14336
M 80,v[168:171],192
ds_read2_b32 v[112:113],v16 offset0:96 offset1:97
M 80,v[172:175],188
ds_read2_b32 v[114:115],v16 offset0:98 offset1:99
M 80,v[176:179],184
L 15
M 84,v[152:155],212
R 144,2,13824
M 84,v[156:159],208
ds_read2_b32 v[116:117],v16 offset0:224 offset1:225
M 84,v[160:163],204
ds_read2_b32 v[118:119],v16 offset0:226 offset1:227
M 84,v[164:167],200
R 148,2,13312
M 84,v[168:171],196
M 84,v[172:175],192
M 84,v[176:179],188
M 84,v[180:183],184
L 15
M 64,v[88:91],128
ds_read2_b32 v[152:153],v1 offset0:112 offset1:113
M 64,v[92:95],124
ds_read2_b32 v[154:155],v1 offset0:114 offset1:115
M 64,v[96:99],120
L 14
R 184,2,16960
M 68,v[88:91],132
ds_read2_b32 v[156:157],v1 offset0:240 offset1:241
M 68,v[92:95],128
M 68,v[96:99],124
ds_read2_b32 v[158:159],v1 offset0:242 offset1:243
M 68,v[100:103],120
L 14
R 188,2,16448
M 72,v[88:91],136
ds_read2_b32 v[160:161],v14 offset0:112 offset1:113
M 72,v[92:95],132
ds_read2_b32 v[162:163],v14 offset0:114 offset1:115
M 72,v[96:99],128
M 72,v[100:103],124
R 192,2,15936
M 72,v[104:107],120
L 15
ds_read2_b32 v[164:165],v14 offset0:240 offset1:241
M 76,v[88:91],140
ds_read2_b32 v[166:167],v14 offset0:242 offset1:243
M 76,v[92:95],136
R 196,2,15424
M 76,v[96:99],132
M 76,v[100:103],128
ds_read2_b32 v[168:169],v15 offset0:112 offset1:113
M 76,v[104:107],124
ds_read2_b32 v[170:171],v15 offset0:114 offset1:115
M 76,v[108:111],120
L 15
R 200,2,14912
M 80,v[88:91],144
ds_read2_b32 v[172:173],v15 offset0:240 offset1:241
M 80,v[92:95],140
M 80,v[96:99],136
ds_read2_b32 v[174:175],v15 offset0:242 offset1:243
M 80,v[100:103],132
R 204,2,14400
M 80,v[104:107],128
ds_read2_b32 v[176:177],v16 offset0:112 offset1:113
M 80,v[108:111],124
ds_read2_b32 v[178:179],v16 offset0:114 offset1:115
M 80,v[112:115],120
L 15
M 84,v[88:91],148
R 208,2,13888
M 84,v[92:95],144
ds_read2_b32 v[180:181],v16 offset0:240 offset1:241
M 84,v[96:99],140
ds_read2_b32 v[182:183],v16 offset0:242 offset1:243
M 84,v[100:103],136
R 212,2,13376
M 84,v[104:107],132
M 84,v[108:111],128
M 84,v[112:115],124
M 84,v[116:119],120
L 15
M 64,v[152:155],192
M 64,v[156:159],188
M 64,v[160:163],184
L 12
M 68,v[152:155],196
M 68,v[156:159],192
M 68,v[160:163],188
M 68,v[164:167],184
L 9
M 72,v[152:155],200
M 72,v[156:159],196
M 72,v[160:163],192
ds_read_b64 v[24:25],v3 offset:15488
ds_read_b64 v[26:27],v3 offset:14976
M 72,v[164:167],188
ds_read_b64 v[28:29],v3 offset:14464
ds_read_b64 v[30:31],v3 offset:13952
M 72,v[168:171],184
L 10
ds_read_b64 v[32:33],v3 offset:13440
ds_read_b64 v[34:35],v3 offset:12928
M 76,v[152:155],204
M 76,v[156:159],200
M 76,v[160:163],196
M 76,v[164:167],192
M 76,v[168:171],188
M 76,v[172:175],184
L 9
M 80,v[152:155],208
M 80,v[156:159],204
M 80,v[160:163],200
M 80,v[164:167],196
M 80,v[168:171],192
M 80,v[172:175],188
M 80,v[176:179],184
L 6
M 84,v[152:155],212
M 84,v[156:159],208
M 84,v[160:163],204
M 84,v[164:167],200
M 84,v[168:171],196
M 84,v[172:175],192
M 84,v[176:179],188
M 84,v[180:183],184
L 0
v_and_b32 v36,0xffff0000,v25
v_lshlrev_b32 v37,16,v25
v_and_b32 v38,0xffff0000,v24
v_lshlrev_b32 v39,16,v24
v_fma_f32 v64,v36,s87,v64
v_fma_f32 v65,v37,s87,v65
v_fma_f32 v66,v38,s87,v66
v_fma_f32 v67,v39,s87,v67
v_cvt_pk_bf16_f32 v64,v64,v65
v_cvt_pk_bf16_f32 v65,v66,v67
ds_write_b64 v4,v[64:65] offset:12928
v_and_b32 v36,0xffff0000,v27
v_lshlrev_b32 v37,16,v27
v_and_b32 v38,0xffff0000,v26
v_lshlrev_b32 v39,16,v26
v_fma_f32 v68,v36,s87,v68
v_fma_f32 v69,v37,s87,v69
v_fma_f32 v70,v38,s87,v70
v_fma_f32 v71,v39,s87,v71
v_cvt_pk_bf16_f32 v68,v68,v69
v_cvt_pk_bf16_f32 v69,v70,v71
ds_write_b64 v4,v[68:69] offset:13440
v_and_b32 v36,0xffff0000,v29
v_lshlrev_b32 v37,16,v29
v_and_b32 v38,0xffff0000,v28
v_lshlrev_b32 v39,16,v28
v_fma_f32 v72,v36,s87,v72
v_fma_f32 v73,v37,s87,v73
v_fma_f32 v74,v38,s87,v74
v_fma_f32 v75,v39,s87,v75
v_cvt_pk_bf16_f32 v72,v72,v73
v_cvt_pk_bf16_f32 v73,v74,v75
ds_write_b64 v4,v[72:73] offset:13952
v_and_b32 v36,0xffff0000,v31
v_lshlrev_b32 v37,16,v31
v_and_b32 v38,0xffff0000,v30
v_lshlrev_b32 v39,16,v30
v_fma_f32 v76,v36,s87,v76
v_fma_f32 v77,v37,s87,v77
v_fma_f32 v78,v38,s87,v78
v_fma_f32 v79,v39,s87,v79
v_cvt_pk_bf16_f32 v76,v76,v77
v_cvt_pk_bf16_f32 v77,v78,v79
ds_write_b64 v4,v[76:77] offset:14464
v_and_b32 v36,0xffff0000,v33
v_lshlrev_b32 v37,16,v33
v_and_b32 v38,0xffff0000,v32
v_lshlrev_b32 v39,16,v32
v_fma_f32 v80,v36,s87,v80
v_fma_f32 v81,v37,s87,v81
v_fma_f32 v82,v38,s87,v82
v_fma_f32 v83,v39,s87,v83
v_cvt_pk_bf16_f32 v80,v80,v81
v_cvt_pk_bf16_f32 v81,v82,v83
ds_write_b64 v4,v[80:81] offset:14976
v_and_b32 v36,0xffff0000,v35
v_lshlrev_b32 v37,16,v35
v_and_b32 v38,0xffff0000,v34
v_lshlrev_b32 v39,16,v34
v_fma_f32 v84,v36,s87,v84
v_fma_f32 v85,v37,s87,v85
v_fma_f32 v86,v38,s87,v86
v_fma_f32 v87,v39,s87,v87
v_cvt_pk_bf16_f32 v84,v84,v85
v_cvt_pk_bf16_f32 v85,v86,v87
ds_write_b64 v4,v[84:85] offset:15488
Lcv_e_%=:
L 0
s_barrier
R 152,5,0
R 156,5,512
R 160,5,1024
R 164,5,1536
R 168,5,2048
R 172,5,2560
s_mov_b32 s78,99614720
L 5
buffer_store_dwordx4 v[152:155],v7,s[36:39],s78 offen offset:0 sc0 sc1
s_add_u32 s78,s56,100139008
L 4
buffer_store_dwordx4 v[156:159],v7,s[36:39],s78 offen offset:0 sc0 sc1
s_mov_b32 s78,100663296
L 3
buffer_store_dwordx4 v[160:163],v7,s[36:39],s78 offen offset:0 sc0 sc1
s_add_u32 s78,s56,101187584
L 2
buffer_store_dwordx4 v[164:167],v7,s[36:39],s78 offen offset:0 sc0 sc1
s_mov_b32 s78,101711872
L 1
buffer_store_dwordx4 v[168:171],v7,s[36:39],s78 offen offset:0 sc0 sc1
s_add_u32 s78,s56,102236160
L 0
buffer_store_dwordx4 v[172:175],v7,s[36:39],s78 offen offset:0 sc0 sc1
s_waitcnt vmcnt(0)
s_barrier
s_add_u32 s48,s48,32768
buffer_store_dword v23,v10,s[36:39],s48 offen offset:0 sc0 sc1
s_cmp_eq_u32 s51,2
s_cbranch_scc0 Lcv_f_%=
s_mov_b32 m0,163584
s_mov_b32 s78,3694592
buffer_load_dword v12,s[60:63],s78 offen offset:0  lds
s_mov_b32 m0,163584
s_mov_b32 s78,5541888
buffer_load_dword v12,s[60:63],s78 offen offset:0  lds
Lcv_f_%=:
Lcode_cv_e:
.purgem M
.purgem R
.purgem T
.purgem D
.purgem L
)"
  :: "s"(ws), "s"(__builtin_amdgcn_readfirstlane(sWave)), "s"(__builtin_amdgcn_readfirstlane(sBuf)), "s"(__builtin_amdgcn_readfirstlane(sAuxOff)), "s"(__builtin_amdgcn_readfirstlane(sFoutOff)), "s"(__builtin_amdgcn_readfirstlane(sC0)), "s"(__builtin_amdgcn_readfirstlane(sC1)), "s"(__builtin_amdgcn_readfirstlane(sExp)), "v"(vL16), "v"(vPA), "v"(vPB), "v"(vPU), "v"(vPW), "v"(vOR), "v"(vORA), "v"(vOG), "v"(vFA), "v"(vFB), "v"(vFlag), "v"(vTW), "v"(vTX), "s"(w3), "s"(w4) : "v0", "v1", "v2", "v3", "v4", "v5", "v6", "v7", "v8", "v9", "v10", "v11", "v12", "v13", "v14", "v15", "v16", "v17", "v18", "v19", "v20", "v21", "v22", "v23", "v24", "v25", "v26", "v27", "v28", "v29", "v30", "v31", "v32", "v33", "v34", "v35", "v36", "v37", "v38", "v39", "v40", "v41", "v42", "v43", "v44", "v45", "v46", "v47", "v48", "v49", "v50", "v51", "v52", "v53", "v54", "v55", "v56", "v57", "v58", "v59", "v60", "v61", "v62", "v63", "v64", "v65", "v66", "v67", "v68", "v69", "v70", "v71", "v72", "v73", "v74", "v75", "v76", "v77", "v78", "v79", "v80", "v81", "v82", "v83", "v84", "v85", "v86", "v87", "v88", "v89", "v90", "v91", "v92", "v93", "v94", "v95", "v96", "v97", "v98", "v99", "v100", "v101", "v102", "v103", "v104", "v105", "v106", "v107", "v108", "v109", "v110", "v111", "v112", "v113", "v114", "v115", "v116", "v117", "v118", "v119", "v120", "v121", "v122", "v123", "v124", "v125", "v126", "v127", "v128", "v129", "v130", "v131", "v132", "v133", "v134", "v135", "v136", "v137", "v138", "v139", "v140", "v141", "v142", "v143", "v144", "v145", "v146", "v147", "v148", "v149", "v150", "v151", "v152", "v153", "v154", "v155", "v156", "v157", "v158", "v159", "v160", "v161", "v162", "v163", "v164", "v165", "v166", "v167", "v168", "v169", "v170", "v171", "v172", "v173", "v174", "v175", "v176", "v177", "v178", "v179", "v180", "v181", "v182", "v183", "v184", "v185", "v186", "v187", "v188", "v189", "v190", "v191", "v192", "v193", "v194", "v195", "v196", "v197", "v198", "v199", "v200", "v201", "v202", "v203", "v204", "v205", "v206", "v207", "v208", "v209", "v210", "v211", "v212", "v213", "v214", "v215", "v216", "v217", "v218", "v219", "v220", "v221", "v222", "v223", "v224", "v225", "v226", "v227", "v228", "v229", "v230", "v231", "a0", "a1", "a2", "a3", "a4", "a5", "a6", "a7", "a8", "a9", "a10", "a11", "a12", "a13", "a14", "a15", "a16", "a17", "a18", "a19", "a20", "a21", "a22", "a23", "a24", "a25", "a26", "a27", "a28", "a29", "a30", "a31", "a32", "a33", "a34", "a35", "a36", "a37", "a38", "a39", "a40", "a41", "a42", "a43", "a44", "a45", "a46", "a47", "a48", "a49", "a50", "a51", "a52", "a53", "a54", "a55", "a56", "a57", "a58", "a59", "a60", "a61", "a62", "a63", "a64", "a65", "a66", "a67", "a68", "a69", "a70", "a71", "a72", "a73", "a74", "a75", "a76", "a77", "a78", "a79", "a80", "a81", "a82", "a83", "a84", "a85", "a86", "a87", "a88", "a89", "a90", "a91", "a92", "a93", "a94", "a95", "a96", "a97", "a98", "a99", "a100", "a101", "a102", "a103", "a104", "a105", "a106", "a107", "a108", "a109", "a110", "a111", "a112", "a113", "a114", "a115", "a116", "a117", "a118", "a119", "a120", "a121", "a122", "a123", "a124", "a125", "a126", "a127", "a128", "a129", "a130", "a131", "a132", "a133", "a134", "a135", "a136", "a137", "a138", "a139", "a140", "a141", "a142", "a143", "a144", "a145", "a146", "a147", "a148", "a149", "a150", "a151", "a152", "a153", "a154", "a155", "a156", "a157", "a158", "a159", "a160", "a161", "a162", "a163", "a164", "a165", "a166", "a167", "a168", "a169", "a170", "a171", "a172", "a173", "a174", "a175", "a176", "a177", "a178", "a179", "a180", "a181", "a182", "a183", "a184", "a185", "a186", "a187", "a188", "a189", "a190", "a191", "a192", "a193", "a194", "a195", "a196", "a197", "a198", "a199", "a200", "a201", "a202", "a203", "a204", "a205", "a206", "a207", "a208", "a209", "a210", "a211", "a212", "a213", "a214", "a215", "a216", "a217", "a218", "a219", "a220", "a221", "a222", "a223", "a224", "a225", "a226", "a227", "a228", "a229", "a230", "a231", "a232", "a233", "a234", "a235", "a236", "a237", "a238", "a239", "a240", "a241", "a242", "a243", "a244", "a245", "a246", "a247", "a248", "a249", "a250", "a251", "a252", "a253", "a254", "a255", "s36", "s37", "s38", "s39", "s40", "s41", "s42", "s43", "s44", "s45", "s46", "s47", "s48", "s49", "s50", "s51", "s52", "s53", "s54", "s55", "s56", "s57", "s58", "s59", "s60", "s61", "s62", "s63", "s64", "s65", "s66", "s67", "s68", "s69", "s70", "s71", "s72", "s73", "s74", "s75", "s76", "s77", "s78", "s79", "s80", "s81", "s82", "s83", "s84", "s85", "s86", "s87", "s88", "s89", "s90", "s91", "s92", "s93", "s94", "s95", "s96", "s97", "vcc", "scc", "memory");
    LANE_ID(lq_);
    const int g = QT0 + (NCV - 1 - iw) * 164 + min(wave * 64 + lq_, 163);
    if (QTB) touch_lds((const char*)q + (size_t)(g < 32768 ? g : 0) * 128, sScr);
  }
  WAIT_LGKM0();
  __builtin_amdgcn_s_barrier();
  CTS(10);
  }
  const unsigned sDbg0 = 110u * 1048576u + (b * 4 + wave) * 8192;
  constexpr unsigned OOB = 0x80000000u;
  if (kind == 6) {
    LANE_ID(lr);
    const int r8 = lr & 7, seg = lr >> 3, j = lr & 15, g = lr >> 4, rr = 8 * g + (j >> 2);
    const unsigned sWave = wave, sExp = __builtin_amdgcn_readfirstlane(ep), sG0 = slice < 16 ? slice + 1 : 0, sStep = NRW, sDbg = sDbg0;
    const unsigned sWw = ((slice >> 3) * 1792 + 16 * wave) * 2048 + (slice & 7) * 256;
    const unsigned vWd0 = seg * 2048 + (r8 ^ swzB(seg)) * 16, vWd1 = (seg + 8) * 2048 + (r8 ^ swzB(8 + seg)) * 16;
    const unsigned vWr0 = (wave >> 1) * 8192 + rr * 128 + (((wave & 1) * 4 + (lr & 3)) ^ swzB(rr)) * 16, vWr1 = vWr0 + 8;
    const unsigned vRF = (wave * 8 + (seg >> 2)) * 1024 + ((seg & 3) * 16 + r8) * 16;
    const unsigned vRwr = seg == 0 ? r8 * 64 + wave * 4 : 512 + lr * 4, vRrd = r8 * 64;
    const unsigned vRP = g == (j >> 2) ? r8 * 64 + 16 + wave * 8 + (seg & 1) * 4 : 768 + lr * 4;
    const unsigned vRS = (wave == 0 && seg < 2) ? r8 * 16 + seg * 256 : OOB, vFlag = lr < 32 ? lr * 4 : OOB;
    const unsigned vL4 = ((lr & 2) * 4 + wave * 2 + (lr & 1)) * 128, vLY = min((wave < 3 ? wave * 32 : 112) + lr, wave < 2 ? wave * 32 + 31 : wave == 2 ? 111 : NCV - 1) * 128;
    const unsigned vQ = r8 * 2048 + wave * 512 + seg * 16;
    const unsigned vY0 = wave * 4096 + lr * 16;
    const unsigned vTr = 4096 + wave * 4096 + ((2 * g + ((j & 3) >> 1)) * 8 + (j >> 2)) * 16 + (j & 1) * 8;
    const unsigned one = g == (j >> 2) ? 0x3f80u << (16 * (j & 1)) : 0u, vSa = (j & 2) ? 0u : one, vSb = (j & 2) ? one : 0u;
    (void)sDbg;
    asm volatile(
R"(.macro M d,w,x
v_mfma_f32_16x16x32_bf16 v[\d:\d+3], \w, v[\x:\x+3], v[\d:\d+3]
.endm
.macro R d,a,o
ds_read_b128 v[\d:\d+3], v\a offset:\o
.endm
.macro T d,a,o
ds_read_b64_tr_b16 \d, v\a offset:\o
.endm
.macro D a,s,o
buffer_load_dwordx4 v\a, s[36:39], s\s offen offset:\o sc1 lds
.endm
.macro L c
s_waitcnt lgkmcnt(\c)
.endm
Lcode_r_b:
s_mov_b64 s[36:37],%0
s_mov_b32 s38,111243392
s_mov_b32 s39,0x00020000
s_mov_b32 s44,%1
s_mov_b32 s45,%2
s_mov_b32 s46,%3
s_mov_b32 s50,%4
s_mov_b32 s53,%5
v_mov_b32 v0,%6
v_mov_b32 v1,%7
v_mov_b32 v2,%8
v_mov_b32 v3,%9
v_mov_b32 v4,%10
v_mov_b32 v5,%11
v_mov_b32 v6,%12
v_mov_b32 v7,%13
v_mov_b32 v8,%14
v_mov_b32 v9,%15
v_mov_b32 v10,%16
v_mov_b32 v11,%17
v_mov_b32 v12,%18
v_mov_b32 v13,%19
v_mov_b32 v14,%20
v_mov_b32 v15,%21
v_mov_b32 v16,%22
s_mov_b32 s47,256
s_mov_b32 s49,0
s_mov_b32 s93,s45
s_mov_b64 s[84:85],%23
s_mov_b32 s86,4194304
s_mov_b32 s87,0x00020000
s_lshl_b32 s55,s45,14
buffer_load_dwordx4 v[164:167],v8,s[84:87],s55 offen offset:0 nt
buffer_load_dwordx4 v[168:171],v8,s[84:87],s55 offen offset:128 nt
buffer_load_dwordx4 v[172:175],v8,s[84:87],s55 offen offset:256 nt
buffer_load_dwordx4 v[176:179],v8,s[84:87],s55 offen offset:384 nt
v_mov_b32 v17,s53
v_mbcnt_lo_u32_b32 v19,-1,0
v_mbcnt_hi_u32_b32 v19,-1,v19
v_lshlrev_b32 v19,4,v19
s_mov_b32 s82,0
s_mov_b32 s63,0x7fff0000
s_mov_b32 s64,0
s_mov_b32 s67,0
s_lshl_b32 s65,s44,12
s_add_u32 s65,s65,4096
s_lshl_b32 s66,s44,7
s_add_u32 s66,s66,109056000
s_mov_b32 s70,0xaaaaaaaa
s_mov_b32 s71,0xaaaaaaaa
s_mov_b32 s72,0xcccccccc
s_mov_b32 s73,0xcccccccc
s_mov_b32 s74,0xff00ff00
s_mov_b32 s75,0xff00ff00
s_mov_b32 s68,0x3a800000
s_mov_b32 s69,0x3727c5ac
s_mov_b32 s76,0xc0135761
s_mov_b32 s77,0x3d372713
v_mov_b32 v32,0x3f803f80
v_mov_b32 v33,0x3f803f80
v_mov_b32 v34,0x3f803f80
v_mov_b32 v35,0x3f803f80
v_mov_b32 v24,0
v_mov_b32 v25,0
v_mov_b32 v26,v11
v_mov_b32 v27,v12
v_mov_b32 v28,0
v_mov_b32 v29,0
s_waitcnt vmcnt(0)
Lr_a0_%=:
s_waitcnt vmcnt(10)
v_mfma_f32_16x16x32_bf16 v[36:39],v[32:35],v[164:167],0
s_add_u32 s79,s45,s46
s_cmp_lt_u32 s45,64
s_cselect_b32 s78,s49,0
v_mfma_f32_16x16x32_bf16 v[40:43],v[164:167],v[164:167],0
s_cmp_ge_u32 s79,64
s_cselect_b32 s78,s78,0
s_add_u32 s62,s79,s78
M 36,v[32:35],168
s_lshr_b32 s78,s45,1
s_and_b32 s79,s45,1
s_lshl_b32 s79,s79,7
M 40,v[168:171],168
s_lshl_b32 s57,s78,15
s_lshl_b32 s55,s62,14
s_cmp_lt_u32 s62,s47
M 36,v[32:35],172
s_cselect_b32 s55,s55,0x7fff0000
s_add_u32 s57,s57,s79
s_lshl_b32 s59,s78,9
M 40,v[172:175],172
buffer_load_dwordx4 v[196:199],v8,s[84:87],s55 offen offset:0 nt
M 36,v[32:35],176
buffer_load_dwordx4 v[200:203],v8,s[84:87],s55 offen offset:128 nt
M 40,v[176:179],176
buffer_load_dwordx4 v[204:207],v8,s[84:87],s55 offen offset:256 nt
v_mfma_f32_16x16x32_bf16 v[116:119],v[26:29],v[164:167],0
buffer_load_dwordx4 v[208:211],v8,s[84:87],s55 offen offset:384 nt
v_mfma_f32_16x16x32_bf16 v[120:123],v[24:27],v[164:167],0
s_add_u32 s57,s57,8388608
s_add_u32 s59,s59,s79
s_lshl_b32 s60,s45,9
v_mfma_f32_16x16x32_bf16 v[124:127],v[26:29],v[168:171],0
s_add_u32 s58,s57,4096
s_add_u32 s59,s59,106954752
s_add_u32 s60,s60,s66
v_mfma_f32_16x16x32_bf16 v[128:131],v[24:27],v[168:171],0
v_add_f32_dpp v44,v36,v36 row_ror:8 row_mask:0xf bank_mask:0xf
v_cndmask_b32 v45,v40,v41,s[70:71]
v_mfma_f32_16x16x32_bf16 v[132:135],v[26:29],v[172:175],0
ds_write_b32 v2,v44 offset:0
v_mfma_f32_16x16x32_bf16 v[136:139],v[24:27],v[172:175],0
v_cndmask_b32 v46,v42,v43,s[70:71]
v_cndmask_b32 v45,v45,v46,s[72:73]
v_mfma_f32_16x16x32_bf16 v[140:143],v[26:29],v[176:179],0
ds_write_b32 v0,v45 offset:0
v_mfma_f32_16x16x32_bf16 v[144:147],v[24:27],v[176:179],0
L 0
s_barrier
R 52,3,0
R 56,3,16
R 60,3,32
s_cmp_lg_u32 s67,0
s_cbranch_scc1 Lr_b0_%=
s_waitcnt vmcnt(4)
buffer_store_dword v17,v5,s[36:39],s63 offen offset:0 sc0 sc1
s_mov_b32 s63,0x7fff0000
s_mov_b32 s38,111243392
s_cmp_lt_u32 s45,64
s_cselect_b32 s64,-192,0
s_add_u32 s64,s64,256
s_cmp_lt_u32 s45,64
s_cselect_b32 s61,0,32768
s_add_u32 s61,s61,111177728
s_mul_i32 s56,s45,16384
s_add_u32 s56,s56,98566144
Lr_bpy0_%=:
buffer_load_dword v18,v7,s[36:39],s61 offen offset:0 sc1
s_add_u32 s82,s82,1
s_waitcnt vmcnt(0)
v_cmp_eq_u32 vcc,s53,v18
s_nop 0
s_cmp_eq_u64 vcc,-1
s_cbranch_scc1 Lr_bqy0_%=
s_sleep 2
s_cmp_lt_u32 s82,20000
s_cbranch_scc1 Lr_bpy0_%=
Lr_bqy0_%=:
s_add_u32 m0,s65,0
s_nop 0
D 9,56,0
D 9,56,1024
D 9,56,2048
D 9,56,3072
s_waitcnt vmcnt(0)
Lr_b0_%=:
s_waitcnt vmcnt(10)
T v[68:69],10,0
T v[70:71],10,64
T v[72:73],10,1024
T v[74:75],10,1088
T v[76:77],10,2048
T v[78:79],10,2112
T v[80:81],10,3072
T v[82:83],10,3136
s_mov_b32 s45,s62
s_min_u32 s78,s64,s47
s_mul_i32 s56,s45,16384
s_add_u32 s56,s56,98566144
s_cmp_lt_i32 s45,s78
s_cselect_b32 s67,1,0
s_cselect_b32 s56,s56,0x7fff0000
s_add_u32 m0,s65,16384
s_nop 0
D 9,56,0
D 9,56,1024
D 9,56,2048
D 9,56,3072
L 8
v_pk_add_f32 v[52:53],v[52:53],v[54:55]
v_pk_add_f32 v[56:57],v[56:57],v[58:59]
v_pk_add_f32 v[60:61],v[60:61],v[62:63]
v_add_f32 v52,v52,v53
v_pk_add_f32 v[56:57],v[56:57],v[60:61]
v_mul_f32 v52,s68,v52
v_add_f32 v53,v56,v57
v_mul_f32 v53,s68,v53
v_fma_f32 v53,-v52,v52,v53
v_max_f32 v53,0,v53
s_waitcnt vmcnt(8)
buffer_store_dword v17,v5,s[36:39],s63 offen offset:0 sc0 sc1
s_mov_b32 s63,0x7fff0000
L 6
v_lshlrev_b32 v84,16,v68
v_and_b32 v85,0xffff0000,v68
v_lshlrev_b32 v86,16,v69
v_and_b32 v87,0xffff0000,v69
v_lshlrev_b32 v88,16,v70
v_and_b32 v89,0xffff0000,v70
v_lshlrev_b32 v90,16,v71
v_and_b32 v91,0xffff0000,v71
L 4
v_lshlrev_b32 v92,16,v72
v_and_b32 v93,0xffff0000,v72
v_lshlrev_b32 v94,16,v73
v_and_b32 v95,0xffff0000,v73
v_lshlrev_b32 v96,16,v74
v_and_b32 v97,0xffff0000,v74
v_lshlrev_b32 v98,16,v75
v_and_b32 v99,0xffff0000,v75
L 2
v_lshlrev_b32 v100,16,v76
v_and_b32 v101,0xffff0000,v76
v_lshlrev_b32 v102,16,v77
v_and_b32 v103,0xffff0000,v77
v_lshlrev_b32 v104,16,v78
v_and_b32 v105,0xffff0000,v78
v_lshlrev_b32 v106,16,v79
v_and_b32 v107,0xffff0000,v79
L 0
v_lshlrev_b32 v108,16,v80
v_and_b32 v109,0xffff0000,v80
v_lshlrev_b32 v110,16,v81
v_and_b32 v111,0xffff0000,v81
v_lshlrev_b32 v112,16,v82
v_and_b32 v113,0xffff0000,v82
v_lshlrev_b32 v114,16,v83
v_and_b32 v115,0xffff0000,v83
v_add_f32 v53,s69,v53
v_rsq_f32 v48,v53
s_nop 0
v_mul_f32_e64 v50,-v52,v48
v_mov_b32 v49,v48
v_mov_b32 v51,v50
v_pk_fma_f32 v[52:53],v[116:117],v[48:49],v[50:51]
v_pk_fma_f32 v[54:55],v[118:119],v[48:49],v[50:51]
v_pk_fma_f32 v[56:57],v[120:121],v[48:49],v[50:51]
v_pk_fma_f32 v[58:59],v[122:123],v[48:49],v[50:51]
v_pk_fma_f32 v[116:117],v[52:53],v[84:85],v[116:117]
v_pk_fma_f32 v[118:119],v[54:55],v[86:87],v[118:119]
v_pk_fma_f32 v[120:121],v[56:57],v[88:89],v[120:121]
v_pk_fma_f32 v[122:123],v[58:59],v[90:91],v[122:123]
v_cvt_pk_bf16_f32 v148,v116,v117
v_cvt_pk_bf16_f32 v149,v118,v119
v_cvt_pk_bf16_f32 v150,v120,v121
v_cvt_pk_bf16_f32 v151,v122,v123
v_pk_fma_f32 v[52:53],v[124:125],v[48:49],v[50:51]
v_pk_fma_f32 v[54:55],v[126:127],v[48:49],v[50:51]
v_pk_fma_f32 v[56:57],v[128:129],v[48:49],v[50:51]
v_pk_fma_f32 v[58:59],v[130:131],v[48:49],v[50:51]
v_pk_fma_f32 v[124:125],v[52:53],v[92:93],v[124:125]
v_pk_fma_f32 v[126:127],v[54:55],v[94:95],v[126:127]
v_pk_fma_f32 v[128:129],v[56:57],v[96:97],v[128:129]
v_pk_fma_f32 v[130:131],v[58:59],v[98:99],v[130:131]
v_cvt_pk_bf16_f32 v152,v124,v125
v_cvt_pk_bf16_f32 v153,v126,v127
v_cvt_pk_bf16_f32 v154,v128,v129
v_cvt_pk_bf16_f32 v155,v130,v131
v_pk_fma_f32 v[52:53],v[132:133],v[48:49],v[50:51]
v_pk_fma_f32 v[54:55],v[134:135],v[48:49],v[50:51]
v_pk_fma_f32 v[56:57],v[136:137],v[48:49],v[50:51]
v_pk_fma_f32 v[58:59],v[138:139],v[48:49],v[50:51]
v_pk_fma_f32 v[132:133],v[52:53],v[100:101],v[132:133]
v_pk_fma_f32 v[134:135],v[54:55],v[102:103],v[134:135]
v_pk_fma_f32 v[136:137],v[56:57],v[104:105],v[136:137]
v_pk_fma_f32 v[138:139],v[58:59],v[106:107],v[138:139]
v_cvt_pk_bf16_f32 v156,v132,v133
v_cvt_pk_bf16_f32 v157,v134,v135
v_cvt_pk_bf16_f32 v158,v136,v137
v_cvt_pk_bf16_f32 v159,v138,v139
v_pk_fma_f32 v[52:53],v[140:141],v[48:49],v[50:51]
v_pk_fma_f32 v[54:55],v[142:143],v[48:49],v[50:51]
v_pk_fma_f32 v[56:57],v[144:145],v[48:49],v[50:51]
v_pk_fma_f32 v[58:59],v[146:147],v[48:49],v[50:51]
v_pk_fma_f32 v[140:141],v[52:53],v[108:109],v[140:141]
v_pk_fma_f32 v[142:143],v[54:55],v[110:111],v[142:143]
v_pk_fma_f32 v[144:145],v[56:57],v[112:113],v[144:145]
v_pk_fma_f32 v[146:147],v[58:59],v[114:115],v[146:147]
v_cvt_pk_bf16_f32 v160,v140,v141
v_cvt_pk_bf16_f32 v161,v142,v143
v_cvt_pk_bf16_f32 v162,v144,v145
v_cvt_pk_bf16_f32 v163,v146,v147
v_mfma_f32_16x16x32_bf16 v[36:39],v[32:35],v[148:151],0
buffer_store_dwordx4 v[148:151],v1,s[36:39],s57 offen offset:0 sc0 sc1
v_mfma_f32_16x16x32_bf16 v[40:43],v[148:151],v[148:151],0
M 36,v[32:35],152
buffer_store_dwordx4 v[152:155],v1,s[36:39],s57 offen offset:2048 sc0 sc1
M 40,v[152:155],152
M 36,v[32:35],156
buffer_store_dwordx4 v[156:159],v1,s[36:39],s58 offen offset:0 sc0 sc1
M 40,v[156:159],156
M 36,v[32:35],160
buffer_store_dwordx4 v[160:163],v1,s[36:39],s58 offen offset:2048 sc0 sc1
M 40,v[160:163],160
s_nop 5
v_add_f32_dpp v44,v36,v36 row_ror:8 row_mask:0xf bank_mask:0xf
s_nop 0
v_cndmask_b32 v45,v40,v41,s[70:71]
v_cndmask_b32 v46,v42,v43,s[70:71]
ds_write_b32 v2,v44 offset:1024
v_cndmask_b32 v45,v45,v46,s[72:73]
ds_write_b32 v0,v45 offset:1024
L 0
s_barrier
R 52,3,1024
R 56,3,1040
R 60,3,1056
L 0
v_pk_add_f32 v[52:53],v[52:53],v[54:55]
v_pk_add_f32 v[56:57],v[56:57],v[58:59]
v_pk_add_f32 v[60:61],v[60:61],v[62:63]
v_add_f32 v52,v52,v53
v_pk_add_f32 v[56:57],v[56:57],v[60:61]
v_mul_f32 v52,s68,v52
v_add_f32 v53,v56,v57
v_mul_f32 v53,s68,v53
v_fma_f32 v53,-v52,v52,v53
v_max_f32 v53,0,v53
v_add_f32 v54,s69,v53
v_fma_f32 v55,s69,v54,v53
v_rsq_f32 v65,v54
v_rsq_f32 v54,v55
v_mul_f32_e64 v64,-v52,v65
v_mul_f32 v63,v55,v54
v_mul_f32 v61,s76,v54
v_mul_f32 v55,v54,v54
v_mul_f32 v62,s77,v55
v_mul_f32 v62,v62,v61
v_cndmask_b32 v60,v52,v64,s[74:75]
v_cndmask_b32 v61,v61,v65,s[74:75]
buffer_store_dwordx4 v[60:63],v4,s[36:39],s59 offen offset:0 sc0 sc1
s_mov_b32 s63,s60
s_cmp_lt_i32 s45,s47
s_cbranch_scc0 Lr_mid_%=
Lr_a1_%=:
s_waitcnt vmcnt(10)
v_mfma_f32_16x16x32_bf16 v[36:39],v[32:35],v[196:199],0
s_add_u32 s79,s45,s46
s_cmp_lt_u32 s45,64
s_cselect_b32 s78,s49,0
v_mfma_f32_16x16x32_bf16 v[40:43],v[196:199],v[196:199],0
s_cmp_ge_u32 s79,64
s_cselect_b32 s78,s78,0
s_add_u32 s62,s79,s78
M 36,v[32:35],200
s_lshr_b32 s78,s45,1
s_and_b32 s79,s45,1
s_lshl_b32 s79,s79,7
M 40,v[200:203],200
s_lshl_b32 s57,s78,15
s_lshl_b32 s55,s62,14
s_cmp_lt_u32 s62,s47
M 36,v[32:35],204
s_cselect_b32 s55,s55,0x7fff0000
s_add_u32 s57,s57,s79
s_lshl_b32 s59,s78,9
M 40,v[204:207],204
buffer_load_dwordx4 v[164:167],v8,s[84:87],s55 offen offset:0 nt
M 36,v[32:35],208
buffer_load_dwordx4 v[168:171],v8,s[84:87],s55 offen offset:128 nt
M 40,v[208:211],208
buffer_load_dwordx4 v[172:175],v8,s[84:87],s55 offen offset:256 nt
v_mfma_f32_16x16x32_bf16 v[116:119],v[26:29],v[196:199],0
buffer_load_dwordx4 v[176:179],v8,s[84:87],s55 offen offset:384 nt
v_mfma_f32_16x16x32_bf16 v[120:123],v[24:27],v[196:199],0
s_add_u32 s57,s57,8388608
s_add_u32 s59,s59,s79
s_lshl_b32 s60,s45,9
v_mfma_f32_16x16x32_bf16 v[124:127],v[26:29],v[200:203],0
s_add_u32 s58,s57,4096
s_add_u32 s59,s59,106954752
s_add_u32 s60,s60,s66
v_mfma_f32_16x16x32_bf16 v[128:131],v[24:27],v[200:203],0
v_add_f32_dpp v44,v36,v36 row_ror:8 row_mask:0xf bank_mask:0xf
v_cndmask_b32 v45,v40,v41,s[70:71]
v_mfma_f32_16x16x32_bf16 v[132:135],v[26:29],v[204:207],0
ds_write_b32 v2,v44 offset:0
v_mfma_f32_16x16x32_bf16 v[136:139],v[24:27],v[204:207],0
v_cndmask_b32 v46,v42,v43,s[70:71]
v_cndmask_b32 v45,v45,v46,s[72:73]
v_mfma_f32_16x16x32_bf16 v[140:143],v[26:29],v[208:211],0
ds_write_b32 v0,v45 offset:0
v_mfma_f32_16x16x32_bf16 v[144:147],v[24:27],v[208:211],0
L 0
s_barrier
R 52,3,0
R 56,3,16
R 60,3,32
s_cmp_lg_u32 s67,0
s_cbranch_scc1 Lr_b1_%=
s_waitcnt vmcnt(4)
buffer_store_dword v17,v5,s[36:39],s63 offen offset:0 sc0 sc1
s_mov_b32 s63,0x7fff0000
s_mov_b32 s38,111243392
s_cmp_lt_u32 s45,64
s_cselect_b32 s64,-192,0
s_add_u32 s64,s64,256
s_cmp_lt_u32 s45,64
s_cselect_b32 s61,0,32768
s_add_u32 s61,s61,111177728
s_mul_i32 s56,s45,16384
s_add_u32 s56,s56,98566144
Lr_bpy1_%=:
buffer_load_dword v18,v7,s[36:39],s61 offen offset:0 sc1
s_add_u32 s82,s82,1
s_waitcnt vmcnt(0)
v_cmp_eq_u32 vcc,s53,v18
s_nop 0
s_cmp_eq_u64 vcc,-1
s_cbranch_scc1 Lr_bqy1_%=
s_sleep 2
s_cmp_lt_u32 s82,20000
s_cbranch_scc1 Lr_bpy1_%=
Lr_bqy1_%=:
s_add_u32 m0,s65,16384
s_nop 0
D 9,56,0
D 9,56,1024
D 9,56,2048
D 9,56,3072
s_waitcnt vmcnt(0)
Lr_b1_%=:
s_waitcnt vmcnt(10)
T v[68:69],10,16384
T v[70:71],10,16448
T v[72:73],10,17408
T v[74:75],10,17472
T v[76:77],10,18432
T v[78:79],10,18496
T v[80:81],10,19456
T v[82:83],10,19520
s_mov_b32 s45,s62
s_min_u32 s78,s64,s47
s_mul_i32 s56,s45,16384
s_add_u32 s56,s56,98566144
s_cmp_lt_i32 s45,s78
s_cselect_b32 s67,1,0
s_cselect_b32 s56,s56,0x7fff0000
s_add_u32 m0,s65,0
s_nop 0
D 9,56,0
D 9,56,1024
D 9,56,2048
D 9,56,3072
L 8
v_pk_add_f32 v[52:53],v[52:53],v[54:55]
v_pk_add_f32 v[56:57],v[56:57],v[58:59]
v_pk_add_f32 v[60:61],v[60:61],v[62:63]
v_add_f32 v52,v52,v53
v_pk_add_f32 v[56:57],v[56:57],v[60:61]
v_mul_f32 v52,s68,v52
v_add_f32 v53,v56,v57
v_mul_f32 v53,s68,v53
v_fma_f32 v53,-v52,v52,v53
v_max_f32 v53,0,v53
s_waitcnt vmcnt(8)
buffer_store_dword v17,v5,s[36:39],s63 offen offset:0 sc0 sc1
s_mov_b32 s63,0x7fff0000
L 6
v_lshlrev_b32 v84,16,v68
v_and_b32 v85,0xffff0000,v68
v_lshlrev_b32 v86,16,v69
v_and_b32 v87,0xffff0000,v69
v_lshlrev_b32 v88,16,v70
v_and_b32 v89,0xffff0000,v70
v_lshlrev_b32 v90,16,v71
v_and_b32 v91,0xffff0000,v71
L 4
v_lshlrev_b32 v92,16,v72
v_and_b32 v93,0xffff0000,v72
v_lshlrev_b32 v94,16,v73
v_and_b32 v95,0xffff0000,v73
v_lshlrev_b32 v96,16,v74
v_and_b32 v97,0xffff0000,v74
v_lshlrev_b32 v98,16,v75
v_and_b32 v99,0xffff0000,v75
L 2
v_lshlrev_b32 v100,16,v76
v_and_b32 v101,0xffff0000,v76
v_lshlrev_b32 v102,16,v77
v_and_b32 v103,0xffff0000,v77
v_lshlrev_b32 v104,16,v78
v_and_b32 v105,0xffff0000,v78
)"
R"(v_lshlrev_b32 v106,16,v79
v_and_b32 v107,0xffff0000,v79
L 0
v_lshlrev_b32 v108,16,v80
v_and_b32 v109,0xffff0000,v80
v_lshlrev_b32 v110,16,v81
v_and_b32 v111,0xffff0000,v81
v_lshlrev_b32 v112,16,v82
v_and_b32 v113,0xffff0000,v82
v_lshlrev_b32 v114,16,v83
v_and_b32 v115,0xffff0000,v83
v_add_f32 v53,s69,v53
v_rsq_f32 v48,v53
s_nop 0
v_mul_f32_e64 v50,-v52,v48
v_mov_b32 v49,v48
v_mov_b32 v51,v50
v_pk_fma_f32 v[52:53],v[116:117],v[48:49],v[50:51]
v_pk_fma_f32 v[54:55],v[118:119],v[48:49],v[50:51]
v_pk_fma_f32 v[56:57],v[120:121],v[48:49],v[50:51]
v_pk_fma_f32 v[58:59],v[122:123],v[48:49],v[50:51]
v_pk_fma_f32 v[116:117],v[52:53],v[84:85],v[116:117]
v_pk_fma_f32 v[118:119],v[54:55],v[86:87],v[118:119]
v_pk_fma_f32 v[120:121],v[56:57],v[88:89],v[120:121]
v_pk_fma_f32 v[122:123],v[58:59],v[90:91],v[122:123]
v_cvt_pk_bf16_f32 v148,v116,v117
v_cvt_pk_bf16_f32 v149,v118,v119
v_cvt_pk_bf16_f32 v150,v120,v121
v_cvt_pk_bf16_f32 v151,v122,v123
v_pk_fma_f32 v[52:53],v[124:125],v[48:49],v[50:51]
v_pk_fma_f32 v[54:55],v[126:127],v[48:49],v[50:51]
v_pk_fma_f32 v[56:57],v[128:129],v[48:49],v[50:51]
v_pk_fma_f32 v[58:59],v[130:131],v[48:49],v[50:51]
v_pk_fma_f32 v[124:125],v[52:53],v[92:93],v[124:125]
v_pk_fma_f32 v[126:127],v[54:55],v[94:95],v[126:127]
v_pk_fma_f32 v[128:129],v[56:57],v[96:97],v[128:129]
v_pk_fma_f32 v[130:131],v[58:59],v[98:99],v[130:131]
v_cvt_pk_bf16_f32 v152,v124,v125
v_cvt_pk_bf16_f32 v153,v126,v127
v_cvt_pk_bf16_f32 v154,v128,v129
v_cvt_pk_bf16_f32 v155,v130,v131
v_pk_fma_f32 v[52:53],v[132:133],v[48:49],v[50:51]
v_pk_fma_f32 v[54:55],v[134:135],v[48:49],v[50:51]
v_pk_fma_f32 v[56:57],v[136:137],v[48:49],v[50:51]
v_pk_fma_f32 v[58:59],v[138:139],v[48:49],v[50:51]
v_pk_fma_f32 v[132:133],v[52:53],v[100:101],v[132:133]
v_pk_fma_f32 v[134:135],v[54:55],v[102:103],v[134:135]
v_pk_fma_f32 v[136:137],v[56:57],v[104:105],v[136:137]
v_pk_fma_f32 v[138:139],v[58:59],v[106:107],v[138:139]
v_cvt_pk_bf16_f32 v156,v132,v133
v_cvt_pk_bf16_f32 v157,v134,v135
v_cvt_pk_bf16_f32 v158,v136,v137
v_cvt_pk_bf16_f32 v159,v138,v139
v_pk_fma_f32 v[52:53],v[140:141],v[48:49],v[50:51]
v_pk_fma_f32 v[54:55],v[142:143],v[48:49],v[50:51]
v_pk_fma_f32 v[56:57],v[144:145],v[48:49],v[50:51]
v_pk_fma_f32 v[58:59],v[146:147],v[48:49],v[50:51]
v_pk_fma_f32 v[140:141],v[52:53],v[108:109],v[140:141]
v_pk_fma_f32 v[142:143],v[54:55],v[110:111],v[142:143]
v_pk_fma_f32 v[144:145],v[56:57],v[112:113],v[144:145]
v_pk_fma_f32 v[146:147],v[58:59],v[114:115],v[146:147]
v_cvt_pk_bf16_f32 v160,v140,v141
v_cvt_pk_bf16_f32 v161,v142,v143
v_cvt_pk_bf16_f32 v162,v144,v145
v_cvt_pk_bf16_f32 v163,v146,v147
v_mfma_f32_16x16x32_bf16 v[36:39],v[32:35],v[148:151],0
buffer_store_dwordx4 v[148:151],v1,s[36:39],s57 offen offset:0 sc0 sc1
v_mfma_f32_16x16x32_bf16 v[40:43],v[148:151],v[148:151],0
M 36,v[32:35],152
buffer_store_dwordx4 v[152:155],v1,s[36:39],s57 offen offset:2048 sc0 sc1
M 40,v[152:155],152
M 36,v[32:35],156
buffer_store_dwordx4 v[156:159],v1,s[36:39],s58 offen offset:0 sc0 sc1
M 40,v[156:159],156
M 36,v[32:35],160
buffer_store_dwordx4 v[160:163],v1,s[36:39],s58 offen offset:2048 sc0 sc1
M 40,v[160:163],160
s_nop 5
v_add_f32_dpp v44,v36,v36 row_ror:8 row_mask:0xf bank_mask:0xf
s_nop 0
v_cndmask_b32 v45,v40,v41,s[70:71]
v_cndmask_b32 v46,v42,v43,s[70:71]
ds_write_b32 v2,v44 offset:1024
v_cndmask_b32 v45,v45,v46,s[72:73]
ds_write_b32 v0,v45 offset:1024
L 0
s_barrier
R 52,3,1024
R 56,3,1040
R 60,3,1056
L 0
v_pk_add_f32 v[52:53],v[52:53],v[54:55]
v_pk_add_f32 v[56:57],v[56:57],v[58:59]
v_pk_add_f32 v[60:61],v[60:61],v[62:63]
v_add_f32 v52,v52,v53
v_pk_add_f32 v[56:57],v[56:57],v[60:61]
v_mul_f32 v52,s68,v52
v_add_f32 v53,v56,v57
v_mul_f32 v53,s68,v53
v_fma_f32 v53,-v52,v52,v53
v_max_f32 v53,0,v53
v_add_f32 v54,s69,v53
v_fma_f32 v55,s69,v54,v53
v_rsq_f32 v65,v54
v_rsq_f32 v54,v55
v_mul_f32_e64 v64,-v52,v65
v_mul_f32 v63,v55,v54
v_mul_f32 v61,s76,v54
v_mul_f32 v55,v54,v54
v_mul_f32 v62,s77,v55
v_mul_f32 v62,v62,v61
v_cndmask_b32 v60,v52,v64,s[74:75]
v_cndmask_b32 v61,v61,v65,s[74:75]
buffer_store_dwordx4 v[60:63],v4,s[36:39],s59 offen offset:0 sc0 sc1
s_mov_b32 s63,s60
s_cmp_lt_i32 s45,s47
s_cbranch_scc1 Lr_a0_%=
Lr_mid_%=:
s_cmp_eq_u32 s93,0
s_cbranch_scc1 Lr_h0_%=
s_mov_b64 s[40:41],%24
s_mov_b32 s42,7340032
s_mov_b32 s43,0x00020000
s_lshl_b32 s57,s44,11
s_add_u32 s55,s50,0
s_add_u32 m0,s57,40960
s_nop 0
buffer_load_dwordx4 v13,s[40:43],s55 offen offset:0  lds
s_add_u32 m0,s57,41984
s_nop 0
buffer_load_dwordx4 v14,s[40:43],s55 offen offset:0  lds
s_add_u32 m0,s57,49024
s_nop 0
buffer_load_dwordx4 v13,s[40:43],s55 offen offset:128  lds
s_add_u32 m0,s57,50048
s_nop 0
buffer_load_dwordx4 v14,s[40:43],s55 offen offset:128  lds
s_add_u32 s55,s50,131072
s_add_u32 m0,s57,57344
s_nop 0
buffer_load_dwordx4 v13,s[40:43],s55 offen offset:0  lds
s_add_u32 m0,s57,58368
s_nop 0
buffer_load_dwordx4 v14,s[40:43],s55 offen offset:0  lds
s_add_u32 m0,s57,65408
s_nop 0
buffer_load_dwordx4 v13,s[40:43],s55 offen offset:128  lds
s_add_u32 m0,s57,66432
s_nop 0
buffer_load_dwordx4 v14,s[40:43],s55 offen offset:128  lds
s_add_u32 s55,s50,262144
s_add_u32 m0,s57,73728
s_nop 0
buffer_load_dwordx4 v13,s[40:43],s55 offen offset:0  lds
s_add_u32 m0,s57,74752
s_nop 0
buffer_load_dwordx4 v14,s[40:43],s55 offen offset:0  lds
s_add_u32 m0,s57,81792
s_nop 0
buffer_load_dwordx4 v13,s[40:43],s55 offen offset:128  lds
s_add_u32 m0,s57,82816
s_nop 0
buffer_load_dwordx4 v14,s[40:43],s55 offen offset:128  lds
s_add_u32 s55,s50,393216
s_add_u32 m0,s57,90112
s_nop 0
buffer_load_dwordx4 v13,s[40:43],s55 offen offset:0  lds
s_add_u32 m0,s57,91136
s_nop 0
buffer_load_dwordx4 v14,s[40:43],s55 offen offset:0  lds
s_add_u32 m0,s57,98176
s_nop 0
buffer_load_dwordx4 v13,s[40:43],s55 offen offset:128  lds
s_add_u32 m0,s57,99200
s_nop 0
buffer_load_dwordx4 v14,s[40:43],s55 offen offset:128  lds
s_add_u32 s55,s50,524288
s_add_u32 m0,s57,106496
s_nop 0
buffer_load_dwordx4 v13,s[40:43],s55 offen offset:0  lds
s_add_u32 m0,s57,107520
s_nop 0
buffer_load_dwordx4 v14,s[40:43],s55 offen offset:0  lds
s_add_u32 m0,s57,114560
s_nop 0
buffer_load_dwordx4 v13,s[40:43],s55 offen offset:128  lds
s_add_u32 m0,s57,115584
s_nop 0
buffer_load_dwordx4 v14,s[40:43],s55 offen offset:128  lds
s_add_u32 s55,s50,655360
s_add_u32 m0,s57,122880
s_nop 0
buffer_load_dwordx4 v13,s[40:43],s55 offen offset:0  lds
s_add_u32 m0,s57,123904
s_nop 0
buffer_load_dwordx4 v14,s[40:43],s55 offen offset:0  lds
s_add_u32 m0,s57,130944
s_nop 0
buffer_load_dwordx4 v13,s[40:43],s55 offen offset:128  lds
s_add_u32 m0,s57,131968
s_nop 0
buffer_load_dwordx4 v14,s[40:43],s55 offen offset:128  lds
s_waitcnt vmcnt(24)
buffer_store_dword v17,v5,s[36:39],s63 offen offset:0 sc0 sc1
s_mov_b32 s63,0x7fff0000
Lr_h0_%=:
s_waitcnt vmcnt(0)
buffer_store_dword v17,v5,s[36:39],s63 offen offset:0 sc0 sc1
s_mov_b32 s63,0x7fff0000
s_mov_b32 s45,s93
s_cmp_eq_u32 s93,0
s_cbranch_scc1 Lr_h1_%=
s_mov_b64 s[40:41],%24
s_mov_b32 s42,7340032
s_mov_b32 s43,0x00020000
s_lshl_b32 s57,s44,11
s_waitcnt vmcnt(22) lgkmcnt(0)
s_barrier
s_add_u32 s55,s50,786432
s_add_u32 m0,s57,139264
s_nop 0
buffer_load_dwordx4 v13,s[40:43],s55 offen offset:0  lds
s_add_u32 m0,s57,140288
s_nop 0
buffer_load_dwordx4 v14,s[40:43],s55 offen offset:0  lds
s_add_u32 m0,s57,147328
s_nop 0
buffer_load_dwordx4 v13,s[40:43],s55 offen offset:128  lds
s_add_u32 m0,s57,148352
s_nop 0
buffer_load_dwordx4 v14,s[40:43],s55 offen offset:128  lds
v_add_u32 v44,40960,v15
v_add_u32 v45,40960,v16
T a[0:1],44,0
T a[2:3],44,512
T a[4:5],45,0
T a[6:7],45,512
T a[8:9],44,4096
T a[10:11],44,4608
T a[12:13],45,4096
T a[14:15],45,4608
s_waitcnt vmcnt(22) lgkmcnt(0)
s_barrier
s_add_u32 s55,s50,917504
s_add_u32 m0,s57,40960
s_nop 0
buffer_load_dwordx4 v13,s[40:43],s55 offen offset:0  lds
s_add_u32 m0,s57,41984
s_nop 0
buffer_load_dwordx4 v14,s[40:43],s55 offen offset:0  lds
s_add_u32 m0,s57,49024
s_nop 0
buffer_load_dwordx4 v13,s[40:43],s55 offen offset:128  lds
s_add_u32 m0,s57,50048
s_nop 0
buffer_load_dwordx4 v14,s[40:43],s55 offen offset:128  lds
v_add_u32 v44,57344,v15
v_add_u32 v45,57344,v16
T a[16:17],44,0
T a[18:19],44,512
T a[20:21],45,0
T a[22:23],45,512
T a[24:25],44,4096
T a[26:27],44,4608
T a[28:29],45,4096
T a[30:31],45,4608
s_waitcnt vmcnt(22) lgkmcnt(0)
s_barrier
s_add_u32 s55,s50,1048576
s_add_u32 m0,s57,57344
s_nop 0
buffer_load_dwordx4 v13,s[40:43],s55 offen offset:0  lds
s_add_u32 m0,s57,58368
s_nop 0
buffer_load_dwordx4 v14,s[40:43],s55 offen offset:0  lds
s_add_u32 m0,s57,65408
s_nop 0
buffer_load_dwordx4 v13,s[40:43],s55 offen offset:128  lds
s_add_u32 m0,s57,66432
s_nop 0
buffer_load_dwordx4 v14,s[40:43],s55 offen offset:128  lds
v_add_u32 v44,73728,v15
v_add_u32 v45,73728,v16
T a[32:33],44,0
T a[34:35],44,512
T a[36:37],45,0
T a[38:39],45,512
T a[40:41],44,4096
T a[42:43],44,4608
T a[44:45],45,4096
T a[46:47],45,4608
s_waitcnt vmcnt(22) lgkmcnt(0)
s_barrier
s_add_u32 s55,s50,1179648
s_add_u32 m0,s57,73728
s_nop 0
buffer_load_dwordx4 v13,s[40:43],s55 offen offset:0  lds
s_add_u32 m0,s57,74752
s_nop 0
buffer_load_dwordx4 v14,s[40:43],s55 offen offset:0  lds
s_add_u32 m0,s57,81792
s_nop 0
buffer_load_dwordx4 v13,s[40:43],s55 offen offset:128  lds
s_add_u32 m0,s57,82816
s_nop 0
buffer_load_dwordx4 v14,s[40:43],s55 offen offset:128  lds
v_add_u32 v44,90112,v15
v_add_u32 v45,90112,v16
T a[48:49],44,0
T a[50:51],44,512
T a[52:53],45,0
T a[54:55],45,512
T a[56:57],44,4096
T a[58:59],44,4608
T a[60:61],45,4096
T a[62:63],45,4608
s_waitcnt vmcnt(22) lgkmcnt(0)
s_barrier
s_add_u32 s55,s50,1310720
s_add_u32 m0,s57,90112
s_nop 0
buffer_load_dwordx4 v13,s[40:43],s55 offen offset:0  lds
s_add_u32 m0,s57,91136
s_nop 0
buffer_load_dwordx4 v14,s[40:43],s55 offen offset:0  lds
s_add_u32 m0,s57,98176
s_nop 0
buffer_load_dwordx4 v13,s[40:43],s55 offen offset:128  lds
s_add_u32 m0,s57,99200
s_nop 0
buffer_load_dwordx4 v14,s[40:43],s55 offen offset:128  lds
v_add_u32 v44,106496,v15
v_add_u32 v45,106496,v16
T a[64:65],44,0
T a[66:67],44,512
T a[68:69],45,0
T a[70:71],45,512
T a[72:73],44,4096
T a[74:75],44,4608
T a[76:77],45,4096
T a[78:79],45,4608
L 0
s_barrier
Lr_h1_%=:
s_lshr_b32 s78,s45,1
s_and_b32 s79,s45,1
s_lshl_b32 s79,s79,7
s_lshl_b32 s55,s78,15
s_add_u32 s55,s55,s79
s_mul_i32 s56,s45,16384
s_add_u32 s56,s56,98566144
s_lshl_b32 s61,s78,11
s_add_u32 s61,s61,110587904
s_add_u32 s78,s45,s46
s_lshr_b32 s78,s78,1
s_lshl_b32 s62,s78,11
s_add_u32 s62,s62,110587904
s_add_u32 m0,s65,0
s_nop 0
D 9,56,0
D 9,56,1024
D 9,56,2048
D 9,56,3072
Lr_bps_%=:
buffer_load_dword v18,v6,s[36:39],s61 offen offset:0 sc1
s_add_u32 s82,s82,1
s_waitcnt vmcnt(0)
v_cmp_eq_u32 vcc,s53,v18
s_nop 0
s_cmp_eq_u64 vcc,-1
s_cbranch_scc1 Lr_bqs_%=
s_sleep 2
s_cmp_lt_u32 s82,20000
s_cbranch_scc1 Lr_bps_%=
Lr_bqs_%=:
s_add_u32 s78,s55,71303168
buffer_load_dwordx4 v[164:167],v1,s[36:39],s78 offen offset:0 sc1
buffer_load_dwordx4 v[168:171],v1,s[36:39],s78 offen offset:2048 sc1
s_add_u32 s78,s55,71307264
buffer_load_dwordx4 v[172:175],v1,s[36:39],s78 offen offset:0 sc1
buffer_load_dwordx4 v[176:179],v1,s[36:39],s78 offen offset:2048 sc1
s_add_u32 s78,s55,89128960
buffer_load_dwordx4 v[180:183],v1,s[36:39],s78 offen offset:0 sc1
buffer_load_dwordx4 v[184:187],v1,s[36:39],s78 offen offset:2048 sc1
s_add_u32 s78,s55,89133056
buffer_load_dwordx4 v[188:191],v1,s[36:39],s78 offen offset:0 sc1
buffer_load_dwordx4 v[192:195],v1,s[36:39],s78 offen offset:2048 sc1
buffer_load_dword v18,v6,s[36:39],s62 offen offset:0 sc1
s_cmp_eq_u32 s93,0
s_cbranch_scc1 Lr_h2_%=
s_mov_b64 s[40:41],%24
s_mov_b32 s42,7340032
s_mov_b32 s43,0x00020000
s_lshl_b32 s57,s44,11
L 0
s_barrier
s_add_u32 s55,s50,1441792
s_add_u32 m0,s57,106496
s_nop 0
buffer_load_dwordx4 v13,s[40:43],s55 offen offset:0  lds
s_add_u32 m0,s57,107520
s_nop 0
buffer_load_dwordx4 v14,s[40:43],s55 offen offset:0  lds
s_add_u32 m0,s57,114560
s_nop 0
buffer_load_dwordx4 v13,s[40:43],s55 offen offset:128  lds
s_add_u32 m0,s57,115584
s_nop 0
buffer_load_dwordx4 v14,s[40:43],s55 offen offset:128  lds
v_add_u32 v44,122880,v15
v_add_u32 v45,122880,v16
T a[80:81],44,0
T a[82:83],44,512
T a[84:85],45,0
T a[86:87],45,512
T a[88:89],44,4096
T a[90:91],44,4608
T a[92:93],45,4096
T a[94:95],45,4608
L 0
s_barrier
s_add_u32 s55,s50,1572864
s_add_u32 m0,s57,122880
s_nop 0
buffer_load_dwordx4 v13,s[40:43],s55 offen offset:0  lds
s_add_u32 m0,s57,123904
s_nop 0
buffer_load_dwordx4 v14,s[40:43],s55 offen offset:0  lds
s_add_u32 m0,s57,130944
s_nop 0
buffer_load_dwordx4 v13,s[40:43],s55 offen offset:128  lds
s_add_u32 m0,s57,131968
s_nop 0
buffer_load_dwordx4 v14,s[40:43],s55 offen offset:128  lds
v_add_u32 v44,139264,v15
v_add_u32 v45,139264,v16
T a[96:97],44,0
T a[98:99],44,512
T a[100:101],45,0
T a[102:103],45,512
T a[104:105],44,4096
T a[106:107],44,4608
T a[108:109],45,4096
T a[110:111],45,4608
L 0
s_barrier
s_add_u32 s55,s50,1703936
s_add_u32 m0,s57,139264
s_nop 0
buffer_load_dwordx4 v13,s[40:43],s55 offen offset:0  lds
s_add_u32 m0,s57,140288
s_nop 0
buffer_load_dwordx4 v14,s[40:43],s55 offen offset:0  lds
s_add_u32 m0,s57,147328
s_nop 0
buffer_load_dwordx4 v13,s[40:43],s55 offen offset:128  lds
s_add_u32 m0,s57,148352
s_nop 0
buffer_load_dwordx4 v14,s[40:43],s55 offen offset:128  lds
v_add_u32 v44,40960,v15
v_add_u32 v45,40960,v16
T a[112:113],44,0
T a[114:115],44,512
T a[116:117],45,0
T a[118:119],45,512
T a[120:121],44,4096
T a[122:123],44,4608
T a[124:125],45,4096
T a[126:127],45,4608
L 0
s_barrier
s_add_u32 s55,s50,1835008
s_add_u32 m0,s57,40960
s_nop 0
buffer_load_dwordx4 v13,s[40:43],s55 offen offset:0  lds
s_add_u32 m0,s57,41984
s_nop 0
buffer_load_dwordx4 v14,s[40:43],s55 offen offset:0  lds
s_add_u32 m0,s57,49024
s_nop 0
buffer_load_dwordx4 v13,s[40:43],s55 offen offset:128  lds
s_add_u32 m0,s57,50048
s_nop 0
buffer_load_dwordx4 v14,s[40:43],s55 offen offset:128  lds
v_add_u32 v44,57344,v15
v_add_u32 v45,57344,v16
T a[128:129],44,0
T a[130:131],44,512
T a[132:133],45,0
T a[134:135],45,512
T a[136:137],44,4096
T a[138:139],44,4608
T a[140:141],45,4096
T a[142:143],45,4608
L 0
s_barrier
s_add_u32 s55,s50,1966080
s_add_u32 m0,s57,57344
s_nop 0
buffer_load_dwordx4 v13,s[40:43],s55 offen offset:0  lds
)"
R"(s_add_u32 m0,s57,58368
s_nop 0
buffer_load_dwordx4 v14,s[40:43],s55 offen offset:0  lds
s_add_u32 m0,s57,65408
s_nop 0
buffer_load_dwordx4 v13,s[40:43],s55 offen offset:128  lds
s_add_u32 m0,s57,66432
s_nop 0
buffer_load_dwordx4 v14,s[40:43],s55 offen offset:128  lds
v_add_u32 v44,73728,v15
v_add_u32 v45,73728,v16
T a[144:145],44,0
T a[146:147],44,512
T a[148:149],45,0
T a[150:151],45,512
T a[152:153],44,4096
T a[154:155],44,4608
T a[156:157],45,4096
T a[158:159],45,4608
L 0
s_barrier
s_add_u32 s55,s50,2097152
s_add_u32 m0,s57,73728
s_nop 0
buffer_load_dwordx4 v13,s[40:43],s55 offen offset:0  lds
s_add_u32 m0,s57,74752
s_nop 0
buffer_load_dwordx4 v14,s[40:43],s55 offen offset:0  lds
s_add_u32 m0,s57,81792
s_nop 0
buffer_load_dwordx4 v13,s[40:43],s55 offen offset:128  lds
s_add_u32 m0,s57,82816
s_nop 0
buffer_load_dwordx4 v14,s[40:43],s55 offen offset:128  lds
v_add_u32 v44,90112,v15
v_add_u32 v45,90112,v16
T a[160:161],44,0
T a[162:163],44,512
T a[164:165],45,0
T a[166:167],45,512
T a[168:169],44,4096
T a[170:171],44,4608
T a[172:173],45,4096
T a[174:175],45,4608
s_waitcnt vmcnt(20) lgkmcnt(0)
s_barrier
s_add_u32 s55,s50,2228224
s_add_u32 m0,s57,90112
s_nop 0
buffer_load_dwordx4 v13,s[40:43],s55 offen offset:0  lds
s_add_u32 m0,s57,91136
s_nop 0
buffer_load_dwordx4 v14,s[40:43],s55 offen offset:0  lds
s_add_u32 m0,s57,98176
s_nop 0
buffer_load_dwordx4 v13,s[40:43],s55 offen offset:128  lds
s_add_u32 m0,s57,99200
s_nop 0
buffer_load_dwordx4 v14,s[40:43],s55 offen offset:128  lds
v_add_u32 v44,106496,v15
v_add_u32 v45,106496,v16
T a[176:177],44,0
T a[178:179],44,512
T a[180:181],45,0
T a[182:183],45,512
T a[184:185],44,4096
T a[186:187],44,4608
T a[188:189],45,4096
T a[190:191],45,4608
s_waitcnt vmcnt(20) lgkmcnt(0)
s_barrier
s_add_u32 s55,s50,2359296
s_add_u32 m0,s57,106496
s_nop 0
buffer_load_dwordx4 v13,s[40:43],s55 offen offset:0  lds
s_add_u32 m0,s57,107520
s_nop 0
buffer_load_dwordx4 v14,s[40:43],s55 offen offset:0  lds
s_add_u32 m0,s57,114560
s_nop 0
buffer_load_dwordx4 v13,s[40:43],s55 offen offset:128  lds
s_add_u32 m0,s57,115584
s_nop 0
buffer_load_dwordx4 v14,s[40:43],s55 offen offset:128  lds
v_add_u32 v44,122880,v15
v_add_u32 v45,122880,v16
T a[192:193],44,0
T a[194:195],44,512
T a[196:197],45,0
T a[198:199],45,512
T a[200:201],44,4096
T a[202:203],44,4608
T a[204:205],45,4096
T a[206:207],45,4608
s_waitcnt vmcnt(20) lgkmcnt(0)
s_barrier
s_add_u32 s55,s50,2490368
s_add_u32 m0,s57,122880
s_nop 0
buffer_load_dwordx4 v13,s[40:43],s55 offen offset:0  lds
s_add_u32 m0,s57,123904
s_nop 0
buffer_load_dwordx4 v14,s[40:43],s55 offen offset:0  lds
s_add_u32 m0,s57,130944
s_nop 0
buffer_load_dwordx4 v13,s[40:43],s55 offen offset:128  lds
s_add_u32 m0,s57,131968
s_nop 0
buffer_load_dwordx4 v14,s[40:43],s55 offen offset:128  lds
v_add_u32 v44,139264,v15
v_add_u32 v45,139264,v16
T a[208:209],44,0
T a[210:211],44,512
T a[212:213],45,0
T a[214:215],45,512
T a[216:217],44,4096
T a[218:219],44,4608
T a[220:221],45,4096
T a[222:223],45,4608
s_waitcnt vmcnt(20) lgkmcnt(0)
s_barrier
s_add_u32 s55,s50,2621440
s_add_u32 m0,s57,139264
s_nop 0
buffer_load_dwordx4 v13,s[40:43],s55 offen offset:0  lds
s_add_u32 m0,s57,140288
s_nop 0
buffer_load_dwordx4 v14,s[40:43],s55 offen offset:0  lds
s_add_u32 m0,s57,147328
s_nop 0
buffer_load_dwordx4 v13,s[40:43],s55 offen offset:128  lds
s_add_u32 m0,s57,148352
s_nop 0
buffer_load_dwordx4 v14,s[40:43],s55 offen offset:128  lds
v_add_u32 v44,40960,v15
v_add_u32 v45,40960,v16
T a[224:225],44,0
T a[226:227],44,512
T a[228:229],45,0
T a[230:231],45,512
T a[232:233],44,4096
T a[234:235],44,4608
T a[236:237],45,4096
T a[238:239],45,4608
s_waitcnt vmcnt(20) lgkmcnt(0)
s_barrier
s_add_u32 s55,s50,2752512
s_add_u32 m0,s57,40960
s_nop 0
buffer_load_dwordx4 v13,s[40:43],s55 offen offset:0  lds
s_add_u32 m0,s57,41984
s_nop 0
buffer_load_dwordx4 v14,s[40:43],s55 offen offset:0  lds
s_add_u32 m0,s57,49024
s_nop 0
buffer_load_dwordx4 v13,s[40:43],s55 offen offset:128  lds
s_add_u32 m0,s57,50048
s_nop 0
buffer_load_dwordx4 v14,s[40:43],s55 offen offset:128  lds
v_add_u32 v44,57344,v15
v_add_u32 v45,57344,v16
T a[240:241],44,0
T a[242:243],44,512
T a[244:245],45,0
T a[246:247],45,512
T a[248:249],44,4096
T a[250:251],44,4608
T a[252:253],45,4096
T a[254:255],45,4608
L 0
s_barrier
s_nop 0
Lr_h2_%=:
s_lshr_b32 s78,s45,1
s_and_b32 s79,s45,1
s_lshl_b32 s79,s79,7
s_lshl_b32 s57,s78,15
s_add_u32 s57,s57,s79
s_add_u32 s57,s57,12582912
s_add_u32 s58,s57,4096
s_lshl_b32 s59,s78,9
s_add_u32 s59,s59,s79
s_add_u32 s59,s59,107020288
s_lshl_b32 s60,s45,9
s_add_u32 s60,s60,s66
s_add_u32 s60,s60,131072
s_add_u32 s45,s45,s46
s_lshr_b32 s78,s45,1
s_and_b32 s79,s45,1
s_lshl_b32 s79,s79,7
s_lshl_b32 s55,s78,15
s_add_u32 s55,s55,s79
s_mul_i32 s56,s45,16384
s_add_u32 s56,s56,98566144
s_lshl_b32 s61,s78,11
s_add_u32 s61,s61,110587904
s_add_u32 s78,s45,s46
s_lshr_b32 s78,s78,1
s_lshl_b32 s62,s78,11
s_add_u32 s62,s62,110587904
s_waitcnt vmcnt(0)
Lr_it0_%=:
T v[68:69],10,0
T v[70:71],10,64
T v[72:73],10,1024
T v[74:75],10,1088
T v[76:77],10,2048
T v[78:79],10,2112
T v[80:81],10,3072
T v[82:83],10,3136
s_nop 0
v_cmp_eq_u32 vcc,s53,v18
s_nop 0
s_cmp_eq_u64 vcc,-1
s_cselect_b32 s78,s45,256
s_cmp_lt_u32 s78,256
s_cbranch_scc1 Lr_ta0_%=
buffer_load_dword v18,v6,s[36:39],s61 offen offset:0 sc1
v_mfma_f32_16x16x32_bf16 v[116:119],v[26:29],v[164:167],0
L 6
v_lshlrev_b32 v84,16,v68
v_and_b32 v85,0xffff0000,v68
v_mfma_f32_16x16x32_bf16 v[120:123],v[24:27],v[164:167],0
L 4
v_lshlrev_b32 v86,16,v69
v_and_b32 v87,0xffff0000,v69
M 116,v[26:29],180
L 2
v_lshlrev_b32 v88,16,v70
v_and_b32 v89,0xffff0000,v70
M 120,v[24:27],180
L 0
v_lshlrev_b32 v90,16,v71
v_and_b32 v91,0xffff0000,v71
v_mfma_f32_16x16x32_bf16 v[124:127],v[26:29],v[168:171],0
s_add_u32 m0,s65,16384
v_lshlrev_b32 v92,16,v72
v_and_b32 v93,0xffff0000,v72
v_mfma_f32_16x16x32_bf16 v[128:131],v[24:27],v[168:171],0
D 9,56,0
M 124,v[26:29],184
D 9,56,1024
M 128,v[24:27],184
D 9,56,2048
v_mfma_f32_16x16x32_bf16 v[132:135],v[26:29],v[172:175],0
D 9,56,3072
v_mfma_f32_16x16x32_bf16 v[136:139],v[24:27],v[172:175],0
v_cvt_pk_bf16_f32 v148,v116,v117
v_cvt_pk_bf16_f32 v149,v118,v119
M 132,v[26:29],188
v_cvt_pk_bf16_f32 v150,v120,v121
v_cvt_pk_bf16_f32 v151,v122,v123
M 136,v[24:27],188
v_cvt_pk_bf16_f32 v152,v124,v125
v_cvt_pk_bf16_f32 v153,v126,v127
v_mfma_f32_16x16x32_bf16 v[140:143],v[26:29],v[176:179],0
v_cvt_pk_bf16_f32 v154,v128,v129
v_cvt_pk_bf16_f32 v155,v130,v131
v_mfma_f32_16x16x32_bf16 v[144:147],v[24:27],v[176:179],0
v_lshlrev_b32 v94,16,v73
v_and_b32 v95,0xffff0000,v73
M 140,v[26:29],192
v_cvt_pk_bf16_f32 v156,v132,v133
v_cvt_pk_bf16_f32 v157,v134,v135
M 144,v[24:27],192
v_cvt_pk_bf16_f32 v158,v136,v137
v_cvt_pk_bf16_f32 v159,v138,v139
v_mfma_f32_16x16x32_bf16 v[36:39],v[32:35],v[148:151],0
v_lshlrev_b32 v96,16,v74
v_and_b32 v97,0xffff0000,v74
v_mfma_f32_16x16x32_bf16 v[40:43],v[148:151],v[148:151],0
v_lshlrev_b32 v98,16,v75
v_and_b32 v99,0xffff0000,v75
M 36,v[32:35],152
v_cvt_pk_bf16_f32 v160,v140,v141
v_cvt_pk_bf16_f32 v161,v142,v143
M 40,v[152:155],152
v_cvt_pk_bf16_f32 v162,v144,v145
v_cvt_pk_bf16_f32 v163,v146,v147
M 36,v[32:35],156
v_lshlrev_b32 v100,16,v76
v_and_b32 v101,0xffff0000,v76
M 40,v[156:159],156
v_lshlrev_b32 v102,16,v77
v_and_b32 v103,0xffff0000,v77
M 36,v[32:35],160
v_lshlrev_b32 v104,16,v78
v_and_b32 v105,0xffff0000,v78
M 40,v[160:163],160
v_lshlrev_b32 v106,16,v79
v_and_b32 v107,0xffff0000,v79
v_lshlrev_b32 v108,16,v80
v_and_b32 v109,0xffff0000,v80
v_lshlrev_b32 v110,16,v81
v_and_b32 v111,0xffff0000,v81
v_lshlrev_b32 v112,16,v82
v_and_b32 v113,0xffff0000,v82
v_lshlrev_b32 v114,16,v83
v_and_b32 v115,0xffff0000,v83
v_add_f32_dpp v44,v36,v36 row_ror:8 row_mask:0xf bank_mask:0xf
v_cndmask_b32 v45,v40,v41,s[70:71]
v_cndmask_b32 v46,v42,v43,s[70:71]
ds_write_b32 v2,v44 offset:0
v_cndmask_b32 v45,v45,v46,s[72:73]
ds_write_b32 v0,v45 offset:0
L 0
s_barrier
R 52,3,0
R 56,3,16
R 60,3,32
L 0
v_pk_add_f32 v[52:53],v[52:53],v[54:55]
v_pk_add_f32 v[56:57],v[56:57],v[58:59]
v_pk_add_f32 v[60:61],v[60:61],v[62:63]
v_add_f32 v52,v52,v53
v_pk_add_f32 v[56:57],v[56:57],v[60:61]
v_mul_f32 v52,s68,v52
v_add_f32 v53,v56,v57
v_mul_f32 v53,s68,v53
v_fma_f32 v53,-v52,v52,v53
v_max_f32 v53,0,v53
s_waitcnt vmcnt(5)
buffer_store_dword v17,v5,s[36:39],s63 offen offset:0 sc0 sc1
s_mov_b32 s63,0x7fff0000
s_waitcnt vmcnt(5)
v_cmp_eq_u32 vcc,s53,v18
s_nop 0
s_cmp_eq_u64 vcc,-1
s_cselect_b32 s78,s45,256
s_cmp_lt_u32 s78,256
s_cbranch_scc1 Lr_tb0_%=
buffer_load_dword v18,v6,s[36:39],s61 offen offset:0 sc1
v_add_f32 v53,s69,v53
v_rsq_f32 v48,v53
s_nop 0
v_mul_f32_e64 v50,-v52,v48
v_mov_b32 v49,v48
v_mov_b32 v51,v50
v_pk_fma_f32 v[52:53],v[116:117],v[48:49],v[50:51]
v_pk_fma_f32 v[54:55],v[118:119],v[48:49],v[50:51]
v_pk_fma_f32 v[56:57],v[120:121],v[48:49],v[50:51]
v_pk_fma_f32 v[58:59],v[122:123],v[48:49],v[50:51]
v_pk_fma_f32 v[116:117],v[52:53],v[84:85],v[116:117]
v_pk_fma_f32 v[118:119],v[54:55],v[86:87],v[118:119]
v_pk_fma_f32 v[120:121],v[56:57],v[88:89],v[120:121]
v_pk_fma_f32 v[122:123],v[58:59],v[90:91],v[122:123]
v_cvt_pk_bf16_f32 v148,v116,v117
v_cvt_pk_bf16_f32 v149,v118,v119
v_cvt_pk_bf16_f32 v150,v120,v121
v_cvt_pk_bf16_f32 v151,v122,v123
v_pk_fma_f32 v[52:53],v[124:125],v[48:49],v[50:51]
v_pk_fma_f32 v[54:55],v[126:127],v[48:49],v[50:51]
v_pk_fma_f32 v[56:57],v[128:129],v[48:49],v[50:51]
v_pk_fma_f32 v[58:59],v[130:131],v[48:49],v[50:51]
v_pk_fma_f32 v[124:125],v[52:53],v[92:93],v[124:125]
v_pk_fma_f32 v[126:127],v[54:55],v[94:95],v[126:127]
v_pk_fma_f32 v[128:129],v[56:57],v[96:97],v[128:129]
v_pk_fma_f32 v[130:131],v[58:59],v[98:99],v[130:131]
v_cvt_pk_bf16_f32 v152,v124,v125
v_cvt_pk_bf16_f32 v153,v126,v127
v_cvt_pk_bf16_f32 v154,v128,v129
v_cvt_pk_bf16_f32 v155,v130,v131
v_pk_fma_f32 v[52:53],v[132:133],v[48:49],v[50:51]
v_pk_fma_f32 v[54:55],v[134:135],v[48:49],v[50:51]
v_pk_fma_f32 v[56:57],v[136:137],v[48:49],v[50:51]
v_pk_fma_f32 v[58:59],v[138:139],v[48:49],v[50:51]
v_pk_fma_f32 v[132:133],v[52:53],v[100:101],v[132:133]
v_pk_fma_f32 v[134:135],v[54:55],v[102:103],v[134:135]
v_pk_fma_f32 v[136:137],v[56:57],v[104:105],v[136:137]
v_pk_fma_f32 v[138:139],v[58:59],v[106:107],v[138:139]
v_cvt_pk_bf16_f32 v156,v132,v133
v_cvt_pk_bf16_f32 v157,v134,v135
v_cvt_pk_bf16_f32 v158,v136,v137
v_cvt_pk_bf16_f32 v159,v138,v139
v_pk_fma_f32 v[52:53],v[140:141],v[48:49],v[50:51]
v_pk_fma_f32 v[54:55],v[142:143],v[48:49],v[50:51]
v_pk_fma_f32 v[56:57],v[144:145],v[48:49],v[50:51]
v_pk_fma_f32 v[58:59],v[146:147],v[48:49],v[50:51]
v_pk_fma_f32 v[140:141],v[52:53],v[108:109],v[140:141]
v_pk_fma_f32 v[142:143],v[54:55],v[110:111],v[142:143]
v_pk_fma_f32 v[144:145],v[56:57],v[112:113],v[144:145]
v_pk_fma_f32 v[146:147],v[58:59],v[114:115],v[146:147]
v_cvt_pk_bf16_f32 v160,v140,v141
v_cvt_pk_bf16_f32 v161,v142,v143
v_cvt_pk_bf16_f32 v162,v144,v145
v_cvt_pk_bf16_f32 v163,v146,v147
v_mfma_f32_16x16x32_bf16 v[36:39],v[32:35],v[148:151],0
buffer_store_dwordx4 v[148:151],v1,s[36:39],s57 offen offset:0 sc0 sc1
v_mfma_f32_16x16x32_bf16 v[40:43],v[148:151],v[148:151],0
M 36,v[32:35],152
buffer_store_dwordx4 v[152:155],v1,s[36:39],s57 offen offset:2048 sc0 sc1
M 40,v[152:155],152
M 36,v[32:35],156
buffer_store_dwordx4 v[156:159],v1,s[36:39],s58 offen offset:0 sc0 sc1
M 40,v[156:159],156
M 36,v[32:35],160
buffer_store_dwordx4 v[160:163],v1,s[36:39],s58 offen offset:2048 sc0 sc1
M 40,v[160:163],160
s_nop 5
v_add_f32_dpp v44,v36,v36 row_ror:8 row_mask:0xf bank_mask:0xf
s_nop 0
v_cndmask_b32 v45,v40,v41,s[70:71]
v_cndmask_b32 v46,v42,v43,s[70:71]
ds_write_b32 v2,v44 offset:1024
v_cndmask_b32 v45,v45,v46,s[72:73]
ds_write_b32 v0,v45 offset:1024
L 0
s_barrier
R 52,3,1024
R 56,3,1040
R 60,3,1056
L 0
v_pk_add_f32 v[52:53],v[52:53],v[54:55]
v_pk_add_f32 v[56:57],v[56:57],v[58:59]
v_pk_add_f32 v[60:61],v[60:61],v[62:63]
v_add_f32 v52,v52,v53
v_pk_add_f32 v[56:57],v[56:57],v[60:61]
v_mul_f32 v52,s68,v52
v_add_f32 v53,v56,v57
v_mul_f32 v53,s68,v53
v_fma_f32 v53,-v52,v52,v53
v_max_f32 v53,0,v53
v_add_f32 v54,s69,v53
v_fma_f32 v55,s69,v54,v53
v_rsq_f32 v65,v54
v_rsq_f32 v54,v55
v_mul_f32_e64 v64,-v52,v65
v_mul_f32 v63,v55,v54
v_mul_f32 v61,s76,v54
v_mul_f32 v55,v54,v54
v_mul_f32 v62,s77,v55
v_mul_f32 v62,v62,v61
v_cndmask_b32 v60,v52,v64,s[74:75]
v_cndmask_b32 v61,v61,v65,s[74:75]
buffer_store_dwordx4 v[60:63],v4,s[36:39],s59 offen offset:0 sc0 sc1
s_mov_b32 s63,s60
s_waitcnt vmcnt(5)
v_cmp_eq_u32 vcc,s53,v18
s_nop 0
s_cmp_eq_u64 vcc,-1
s_cselect_b32 s78,s45,256
s_cmp_lt_u32 s78,256
s_cbranch_scc1 Lr_tc0_%=
s_waitcnt vmcnt(0)
buffer_store_dword v17,v5,s[36:39],s63 offen offset:0 sc0 sc1
s_mov_b32 s63,0x7fff0000
s_cmp_ge_u32 s45,256
s_cbranch_scc1 Lr_done_%=
Lr_bp0_%=:
buffer_load_dword v18,v6,s[36:39],s61 offen offset:0 sc1
s_add_u32 s82,s82,1
s_waitcnt vmcnt(0)
v_cmp_eq_u32 vcc,s53,v18
s_nop 0
s_cmp_eq_u64 vcc,-1
s_cbranch_scc1 Lr_bq0_%=
s_sleep 2
s_cmp_lt_u32 s82,20000
s_cbranch_scc1 Lr_bp0_%=
Lr_bq0_%=:
s_add_u32 s78,s55,71303168
buffer_load_dwordx4 v[196:199],v1,s[36:39],s78 offen offset:0 sc1
buffer_load_dwordx4 v[200:203],v1,s[36:39],s78 offen offset:2048 sc1
s_add_u32 s78,s55,71307264
buffer_load_dwordx4 v[204:207],v1,s[36:39],s78 offen offset:0 sc1
buffer_load_dwordx4 v[208:211],v1,s[36:39],s78 offen offset:2048 sc1
s_add_u32 s78,s55,89128960
buffer_load_dwordx4 v[212:215],v1,s[36:39],s78 offen offset:0 sc1
buffer_load_dwordx4 v[216:219],v1,s[36:39],s78 offen offset:2048 sc1
s_add_u32 s78,s55,89133056
buffer_load_dwordx4 v[220:223],v1,s[36:39],s78 offen offset:0 sc1
buffer_load_dwordx4 v[224:227],v1,s[36:39],s78 offen offset:2048 sc1
buffer_load_dword v18,v6,s[36:39],s62 offen offset:0 sc1
s_lshr_b32 s78,s45,1
s_and_b32 s79,s45,1
s_lshl_b32 s79,s79,7
s_lshl_b32 s57,s78,15
s_add_u32 s57,s57,s79
s_add_u32 s57,s57,12582912
s_add_u32 s58,s57,4096
s_lshl_b32 s59,s78,9
s_add_u32 s59,s59,s79
s_add_u32 s59,s59,107020288
s_lshl_b32 s60,s45,9
s_add_u32 s60,s60,s66
s_add_u32 s60,s60,131072
s_add_u32 s45,s45,s46
s_lshr_b32 s78,s45,1
s_and_b32 s79,s45,1
s_lshl_b32 s79,s79,7
s_lshl_b32 s55,s78,15
s_add_u32 s55,s55,s79
s_mul_i32 s56,s45,16384
s_add_u32 s56,s56,98566144
s_lshl_b32 s61,s78,11
s_add_u32 s61,s61,110587904
s_add_u32 s78,s45,s46
s_lshr_b32 s78,s78,1
s_lshl_b32 s62,s78,11
s_add_u32 s62,s62,110587904
)"
R"(s_waitcnt vmcnt(0)
s_branch Lr_it1_%=
Lr_ta0_%=:
v_mfma_f32_16x16x32_bf16 v[116:119],v[26:29],v[164:167],0
s_add_u32 s78,s55,71303168
L 6
L 4
v_mfma_f32_16x16x32_bf16 v[120:123],v[24:27],v[164:167],0
L 2
buffer_load_dwordx4 v[196:199],v1,s[36:39],s78 offen offset:0 sc1
M 116,v[26:29],180
L 0
buffer_load_dwordx4 v[200:203],v1,s[36:39],s78 offen offset:2048 sc1
M 120,v[24:27],180
s_add_u32 s78,s55,71307264
v_lshlrev_b32 v84,16,v68
v_and_b32 v85,0xffff0000,v68
v_mfma_f32_16x16x32_bf16 v[124:127],v[26:29],v[168:171],0
buffer_load_dwordx4 v[204:207],v1,s[36:39],s78 offen offset:0 sc1
v_mfma_f32_16x16x32_bf16 v[128:131],v[24:27],v[168:171],0
buffer_load_dwordx4 v[208:211],v1,s[36:39],s78 offen offset:2048 sc1
M 124,v[26:29],184
s_add_u32 s78,s55,89128960
v_cvt_pk_bf16_f32 v148,v116,v117
v_cvt_pk_bf16_f32 v149,v118,v119
M 128,v[24:27],184
buffer_load_dwordx4 v[212:215],v1,s[36:39],s78 offen offset:0 sc1
v_mfma_f32_16x16x32_bf16 v[132:135],v[26:29],v[172:175],0
buffer_load_dwordx4 v[216:219],v1,s[36:39],s78 offen offset:2048 sc1
v_mfma_f32_16x16x32_bf16 v[136:139],v[24:27],v[172:175],0
s_add_u32 s78,s55,89133056
v_cvt_pk_bf16_f32 v150,v120,v121
v_cvt_pk_bf16_f32 v151,v122,v123
M 132,v[26:29],188
buffer_load_dwordx4 v[220:223],v1,s[36:39],s78 offen offset:0 sc1
M 136,v[24:27],188
buffer_load_dwordx4 v[224:227],v1,s[36:39],s78 offen offset:2048 sc1
v_mfma_f32_16x16x32_bf16 v[140:143],v[26:29],v[176:179],0
s_add_u32 m0,s65,16384
v_cvt_pk_bf16_f32 v152,v124,v125
v_cvt_pk_bf16_f32 v153,v126,v127
v_mfma_f32_16x16x32_bf16 v[144:147],v[24:27],v[176:179],0
D 9,56,0
M 140,v[26:29],192
D 9,56,1024
M 144,v[24:27],192
D 9,56,2048
v_mfma_f32_16x16x32_bf16 v[36:39],v[32:35],v[148:151],0
v_cvt_pk_bf16_f32 v154,v128,v129
v_cvt_pk_bf16_f32 v155,v130,v131
v_mfma_f32_16x16x32_bf16 v[40:43],v[148:151],v[148:151],0
D 9,56,3072
M 36,v[32:35],152
v_cvt_pk_bf16_f32 v156,v132,v133
v_cvt_pk_bf16_f32 v157,v134,v135
v_cvt_pk_bf16_f32 v158,v136,v137
v_cvt_pk_bf16_f32 v159,v138,v139
M 40,v[152:155],152
v_cvt_pk_bf16_f32 v160,v140,v141
v_cvt_pk_bf16_f32 v161,v142,v143
M 36,v[32:35],156
v_cvt_pk_bf16_f32 v162,v144,v145
v_cvt_pk_bf16_f32 v163,v146,v147
M 40,v[156:159],156
v_lshlrev_b32 v86,16,v69
v_and_b32 v87,0xffff0000,v69
M 36,v[32:35],160
v_lshlrev_b32 v88,16,v70
v_and_b32 v89,0xffff0000,v70
M 40,v[160:163],160
v_lshlrev_b32 v90,16,v71
v_and_b32 v91,0xffff0000,v71
v_lshlrev_b32 v92,16,v72
v_and_b32 v93,0xffff0000,v72
v_lshlrev_b32 v94,16,v73
v_and_b32 v95,0xffff0000,v73
v_lshlrev_b32 v96,16,v74
v_and_b32 v97,0xffff0000,v74
v_lshlrev_b32 v98,16,v75
v_and_b32 v99,0xffff0000,v75
v_lshlrev_b32 v100,16,v76
v_and_b32 v101,0xffff0000,v76
v_lshlrev_b32 v102,16,v77
v_and_b32 v103,0xffff0000,v77
v_lshlrev_b32 v104,16,v78
v_and_b32 v105,0xffff0000,v78
v_lshlrev_b32 v106,16,v79
v_and_b32 v107,0xffff0000,v79
v_lshlrev_b32 v108,16,v80
v_and_b32 v109,0xffff0000,v80
v_lshlrev_b32 v110,16,v81
v_and_b32 v111,0xffff0000,v81
v_lshlrev_b32 v112,16,v82
v_and_b32 v113,0xffff0000,v82
v_lshlrev_b32 v114,16,v83
v_and_b32 v115,0xffff0000,v83
v_add_f32_dpp v44,v36,v36 row_ror:8 row_mask:0xf bank_mask:0xf
v_cndmask_b32 v45,v40,v41,s[70:71]
v_cndmask_b32 v46,v42,v43,s[70:71]
ds_write_b32 v2,v44 offset:0
v_cndmask_b32 v45,v45,v46,s[72:73]
ds_write_b32 v0,v45 offset:0
L 0
s_barrier
R 52,3,0
R 56,3,16
R 60,3,32
L 0
v_pk_add_f32 v[52:53],v[52:53],v[54:55]
v_pk_add_f32 v[56:57],v[56:57],v[58:59]
v_pk_add_f32 v[60:61],v[60:61],v[62:63]
v_add_f32 v52,v52,v53
v_pk_add_f32 v[56:57],v[56:57],v[60:61]
v_mul_f32 v52,s68,v52
v_add_f32 v53,v56,v57
v_mul_f32 v53,s68,v53
v_fma_f32 v53,-v52,v52,v53
v_max_f32 v53,0,v53
buffer_load_dword v18,v6,s[36:39],s62 offen offset:0 sc1
v_add_f32 v53,s69,v53
v_rsq_f32 v48,v53
s_nop 0
v_mul_f32_e64 v50,-v52,v48
v_mov_b32 v49,v48
v_mov_b32 v51,v50
v_pk_fma_f32 v[52:53],v[116:117],v[48:49],v[50:51]
v_pk_fma_f32 v[54:55],v[118:119],v[48:49],v[50:51]
v_pk_fma_f32 v[56:57],v[120:121],v[48:49],v[50:51]
v_pk_fma_f32 v[58:59],v[122:123],v[48:49],v[50:51]
v_pk_fma_f32 v[116:117],v[52:53],v[84:85],v[116:117]
v_pk_fma_f32 v[118:119],v[54:55],v[86:87],v[118:119]
v_pk_fma_f32 v[120:121],v[56:57],v[88:89],v[120:121]
v_pk_fma_f32 v[122:123],v[58:59],v[90:91],v[122:123]
v_cvt_pk_bf16_f32 v148,v116,v117
v_cvt_pk_bf16_f32 v149,v118,v119
v_cvt_pk_bf16_f32 v150,v120,v121
v_cvt_pk_bf16_f32 v151,v122,v123
v_pk_fma_f32 v[52:53],v[124:125],v[48:49],v[50:51]
v_pk_fma_f32 v[54:55],v[126:127],v[48:49],v[50:51]
v_pk_fma_f32 v[56:57],v[128:129],v[48:49],v[50:51]
v_pk_fma_f32 v[58:59],v[130:131],v[48:49],v[50:51]
v_pk_fma_f32 v[124:125],v[52:53],v[92:93],v[124:125]
v_pk_fma_f32 v[126:127],v[54:55],v[94:95],v[126:127]
v_pk_fma_f32 v[128:129],v[56:57],v[96:97],v[128:129]
v_pk_fma_f32 v[130:131],v[58:59],v[98:99],v[130:131]
v_cvt_pk_bf16_f32 v152,v124,v125
v_cvt_pk_bf16_f32 v153,v126,v127
v_cvt_pk_bf16_f32 v154,v128,v129
v_cvt_pk_bf16_f32 v155,v130,v131
v_pk_fma_f32 v[52:53],v[132:133],v[48:49],v[50:51]
v_pk_fma_f32 v[54:55],v[134:135],v[48:49],v[50:51]
v_pk_fma_f32 v[56:57],v[136:137],v[48:49],v[50:51]
v_pk_fma_f32 v[58:59],v[138:139],v[48:49],v[50:51]
v_pk_fma_f32 v[132:133],v[52:53],v[100:101],v[132:133]
v_pk_fma_f32 v[134:135],v[54:55],v[102:103],v[134:135]
v_pk_fma_f32 v[136:137],v[56:57],v[104:105],v[136:137]
v_pk_fma_f32 v[138:139],v[58:59],v[106:107],v[138:139]
v_cvt_pk_bf16_f32 v156,v132,v133
v_cvt_pk_bf16_f32 v157,v134,v135
v_cvt_pk_bf16_f32 v158,v136,v137
v_cvt_pk_bf16_f32 v159,v138,v139
v_pk_fma_f32 v[52:53],v[140:141],v[48:49],v[50:51]
v_pk_fma_f32 v[54:55],v[142:143],v[48:49],v[50:51]
v_pk_fma_f32 v[56:57],v[144:145],v[48:49],v[50:51]
v_pk_fma_f32 v[58:59],v[146:147],v[48:49],v[50:51]
v_pk_fma_f32 v[140:141],v[52:53],v[108:109],v[140:141]
v_pk_fma_f32 v[142:143],v[54:55],v[110:111],v[142:143]
v_pk_fma_f32 v[144:145],v[56:57],v[112:113],v[144:145]
v_pk_fma_f32 v[146:147],v[58:59],v[114:115],v[146:147]
v_cvt_pk_bf16_f32 v160,v140,v141
v_cvt_pk_bf16_f32 v161,v142,v143
v_cvt_pk_bf16_f32 v162,v144,v145
v_cvt_pk_bf16_f32 v163,v146,v147
s_waitcnt vmcnt(13)
buffer_store_dword v17,v5,s[36:39],s63 offen offset:0 sc0 sc1
s_mov_b32 s63,0x7fff0000
v_mfma_f32_16x16x32_bf16 v[36:39],v[32:35],v[148:151],0
buffer_store_dwordx4 v[148:151],v1,s[36:39],s57 offen offset:0 sc0 sc1
v_mfma_f32_16x16x32_bf16 v[40:43],v[148:151],v[148:151],0
M 36,v[32:35],152
buffer_store_dwordx4 v[152:155],v1,s[36:39],s57 offen offset:2048 sc0 sc1
M 40,v[152:155],152
M 36,v[32:35],156
buffer_store_dwordx4 v[156:159],v1,s[36:39],s58 offen offset:0 sc0 sc1
M 40,v[156:159],156
M 36,v[32:35],160
buffer_store_dwordx4 v[160:163],v1,s[36:39],s58 offen offset:2048 sc0 sc1
M 40,v[160:163],160
s_nop 5
v_add_f32_dpp v44,v36,v36 row_ror:8 row_mask:0xf bank_mask:0xf
s_nop 0
v_cndmask_b32 v45,v40,v41,s[70:71]
v_cndmask_b32 v46,v42,v43,s[70:71]
ds_write_b32 v2,v44 offset:1024
v_cndmask_b32 v45,v45,v46,s[72:73]
ds_write_b32 v0,v45 offset:1024
L 0
s_barrier
R 52,3,1024
R 56,3,1040
R 60,3,1056
L 0
v_pk_add_f32 v[52:53],v[52:53],v[54:55]
v_pk_add_f32 v[56:57],v[56:57],v[58:59]
v_pk_add_f32 v[60:61],v[60:61],v[62:63]
v_add_f32 v52,v52,v53
v_pk_add_f32 v[56:57],v[56:57],v[60:61]
v_mul_f32 v52,s68,v52
v_add_f32 v53,v56,v57
v_mul_f32 v53,s68,v53
v_fma_f32 v53,-v52,v52,v53
v_max_f32 v53,0,v53
v_add_f32 v54,s69,v53
v_fma_f32 v55,s69,v54,v53
v_rsq_f32 v65,v54
v_rsq_f32 v54,v55
v_mul_f32_e64 v64,-v52,v65
v_mul_f32 v63,v55,v54
v_mul_f32 v61,s76,v54
v_mul_f32 v55,v54,v54
v_mul_f32 v62,s77,v55
v_mul_f32 v62,v62,v61
v_cndmask_b32 v60,v52,v64,s[74:75]
v_cndmask_b32 v61,v61,v65,s[74:75]
buffer_store_dwordx4 v[60:63],v4,s[36:39],s59 offen offset:0 sc0 sc1
s_mov_b32 s63,s60
s_lshr_b32 s78,s45,1
s_and_b32 s79,s45,1
s_lshl_b32 s79,s79,7
s_lshl_b32 s57,s78,15
s_add_u32 s57,s57,s79
s_add_u32 s57,s57,12582912
s_add_u32 s58,s57,4096
s_lshl_b32 s59,s78,9
s_add_u32 s59,s59,s79
s_add_u32 s59,s59,107020288
s_lshl_b32 s60,s45,9
s_add_u32 s60,s60,s66
s_add_u32 s60,s60,131072
s_add_u32 s45,s45,s46
s_lshr_b32 s78,s45,1
s_and_b32 s79,s45,1
s_lshl_b32 s79,s79,7
s_lshl_b32 s55,s78,15
s_add_u32 s55,s55,s79
s_mul_i32 s56,s45,16384
s_add_u32 s56,s56,98566144
s_lshl_b32 s61,s78,11
s_add_u32 s61,s61,110587904
s_add_u32 s78,s45,s46
s_lshr_b32 s78,s78,1
s_lshl_b32 s62,s78,11
s_add_u32 s62,s62,110587904
s_waitcnt vmcnt(6)
s_branch Lr_it1_%=
Lr_tb0_%=:
v_add_f32 v53,s69,v53
v_rsq_f32 v48,v53
s_nop 0
v_mul_f32_e64 v50,-v52,v48
v_mov_b32 v49,v48
v_mov_b32 v51,v50
v_pk_fma_f32 v[52:53],v[116:117],v[48:49],v[50:51]
v_pk_fma_f32 v[54:55],v[118:119],v[48:49],v[50:51]
v_pk_fma_f32 v[56:57],v[120:121],v[48:49],v[50:51]
v_pk_fma_f32 v[58:59],v[122:123],v[48:49],v[50:51]
s_add_u32 s78,s55,71303168
buffer_load_dwordx4 v[196:199],v1,s[36:39],s78 offen offset:0 sc1
buffer_load_dwordx4 v[200:203],v1,s[36:39],s78 offen offset:2048 sc1
s_add_u32 s78,s55,71307264
buffer_load_dwordx4 v[204:207],v1,s[36:39],s78 offen offset:0 sc1
buffer_load_dwordx4 v[208:211],v1,s[36:39],s78 offen offset:2048 sc1
v_pk_fma_f32 v[116:117],v[52:53],v[84:85],v[116:117]
v_pk_fma_f32 v[118:119],v[54:55],v[86:87],v[118:119]
v_pk_fma_f32 v[120:121],v[56:57],v[88:89],v[120:121]
v_pk_fma_f32 v[122:123],v[58:59],v[90:91],v[122:123]
v_cvt_pk_bf16_f32 v148,v116,v117
v_cvt_pk_bf16_f32 v149,v118,v119
v_cvt_pk_bf16_f32 v150,v120,v121
v_cvt_pk_bf16_f32 v151,v122,v123
v_pk_fma_f32 v[52:53],v[124:125],v[48:49],v[50:51]
v_pk_fma_f32 v[54:55],v[126:127],v[48:49],v[50:51]
v_pk_fma_f32 v[56:57],v[128:129],v[48:49],v[50:51]
v_pk_fma_f32 v[58:59],v[130:131],v[48:49],v[50:51]
s_add_u32 s78,s55,89128960
buffer_load_dwordx4 v[212:215],v1,s[36:39],s78 offen offset:0 sc1
buffer_load_dwordx4 v[216:219],v1,s[36:39],s78 offen offset:2048 sc1
s_add_u32 s78,s55,89133056
buffer_load_dwordx4 v[220:223],v1,s[36:39],s78 offen offset:0 sc1
buffer_load_dwordx4 v[224:227],v1,s[36:39],s78 offen offset:2048 sc1
v_pk_fma_f32 v[124:125],v[52:53],v[92:93],v[124:125]
v_pk_fma_f32 v[126:127],v[54:55],v[94:95],v[126:127]
v_pk_fma_f32 v[128:129],v[56:57],v[96:97],v[128:129]
v_pk_fma_f32 v[130:131],v[58:59],v[98:99],v[130:131]
v_cvt_pk_bf16_f32 v152,v124,v125
v_cvt_pk_bf16_f32 v153,v126,v127
v_cvt_pk_bf16_f32 v154,v128,v129
v_cvt_pk_bf16_f32 v155,v130,v131
v_pk_fma_f32 v[52:53],v[132:133],v[48:49],v[50:51]
v_pk_fma_f32 v[54:55],v[134:135],v[48:49],v[50:51]
v_pk_fma_f32 v[56:57],v[136:137],v[48:49],v[50:51]
v_pk_fma_f32 v[58:59],v[138:139],v[48:49],v[50:51]
v_pk_fma_f32 v[132:133],v[52:53],v[100:101],v[132:133]
v_pk_fma_f32 v[134:135],v[54:55],v[102:103],v[134:135]
v_pk_fma_f32 v[136:137],v[56:57],v[104:105],v[136:137]
v_pk_fma_f32 v[138:139],v[58:59],v[106:107],v[138:139]
v_cvt_pk_bf16_f32 v156,v132,v133
v_cvt_pk_bf16_f32 v157,v134,v135
v_cvt_pk_bf16_f32 v158,v136,v137
v_cvt_pk_bf16_f32 v159,v138,v139
v_pk_fma_f32 v[52:53],v[140:141],v[48:49],v[50:51]
v_pk_fma_f32 v[54:55],v[142:143],v[48:49],v[50:51]
v_pk_fma_f32 v[56:57],v[144:145],v[48:49],v[50:51]
v_pk_fma_f32 v[58:59],v[146:147],v[48:49],v[50:51]
v_pk_fma_f32 v[140:141],v[52:53],v[108:109],v[140:141]
v_pk_fma_f32 v[142:143],v[54:55],v[110:111],v[142:143]
v_pk_fma_f32 v[144:145],v[56:57],v[112:113],v[144:145]
v_pk_fma_f32 v[146:147],v[58:59],v[114:115],v[146:147]
v_cvt_pk_bf16_f32 v160,v140,v141
v_cvt_pk_bf16_f32 v161,v142,v143
v_cvt_pk_bf16_f32 v162,v144,v145
v_cvt_pk_bf16_f32 v163,v146,v147
v_mfma_f32_16x16x32_bf16 v[36:39],v[32:35],v[148:151],0
buffer_load_dword v18,v6,s[36:39],s62 offen offset:0 sc1
buffer_store_dwordx4 v[148:151],v1,s[36:39],s57 offen offset:0 sc0 sc1
v_mfma_f32_16x16x32_bf16 v[40:43],v[148:151],v[148:151],0
M 36,v[32:35],152
buffer_store_dwordx4 v[152:155],v1,s[36:39],s57 offen offset:2048 sc0 sc1
M 40,v[152:155],152
M 36,v[32:35],156
buffer_store_dwordx4 v[156:159],v1,s[36:39],s58 offen offset:0 sc0 sc1
M 40,v[156:159],156
M 36,v[32:35],160
buffer_store_dwordx4 v[160:163],v1,s[36:39],s58 offen offset:2048 sc0 sc1
M 40,v[160:163],160
s_nop 5
v_add_f32_dpp v44,v36,v36 row_ror:8 row_mask:0xf bank_mask:0xf
s_nop 0
v_cndmask_b32 v45,v40,v41,s[70:71]
v_cndmask_b32 v46,v42,v43,s[70:71]
ds_write_b32 v2,v44 offset:1024
v_cndmask_b32 v45,v45,v46,s[72:73]
ds_write_b32 v0,v45 offset:1024
L 0
s_barrier
R 52,3,1024
R 56,3,1040
R 60,3,1056
L 0
v_pk_add_f32 v[52:53],v[52:53],v[54:55]
v_pk_add_f32 v[56:57],v[56:57],v[58:59]
v_pk_add_f32 v[60:61],v[60:61],v[62:63]
v_add_f32 v52,v52,v53
v_pk_add_f32 v[56:57],v[56:57],v[60:61]
v_mul_f32 v52,s68,v52
v_add_f32 v53,v56,v57
v_mul_f32 v53,s68,v53
v_fma_f32 v53,-v52,v52,v53
v_max_f32 v53,0,v53
v_add_f32 v54,s69,v53
v_fma_f32 v55,s69,v54,v53
v_rsq_f32 v65,v54
v_rsq_f32 v54,v55
v_mul_f32_e64 v64,-v52,v65
v_mul_f32 v63,v55,v54
v_mul_f32 v61,s76,v54
v_mul_f32 v55,v54,v54
v_mul_f32 v62,s77,v55
v_mul_f32 v62,v62,v61
v_cndmask_b32 v60,v52,v64,s[74:75]
v_cndmask_b32 v61,v61,v65,s[74:75]
buffer_store_dwordx4 v[60:63],v4,s[36:39],s59 offen offset:0 sc0 sc1
s_mov_b32 s63,s60
s_lshr_b32 s78,s45,1
s_and_b32 s79,s45,1
s_lshl_b32 s79,s79,7
s_lshl_b32 s57,s78,15
s_add_u32 s57,s57,s79
s_add_u32 s57,s57,12582912
s_add_u32 s58,s57,4096
s_lshl_b32 s59,s78,9
s_add_u32 s59,s59,s79
s_add_u32 s59,s59,107020288
s_lshl_b32 s60,s45,9
s_add_u32 s60,s60,s66
s_add_u32 s60,s60,131072
s_add_u32 s45,s45,s46
s_lshr_b32 s78,s45,1
s_and_b32 s79,s45,1
s_lshl_b32 s79,s79,7
s_lshl_b32 s55,s78,15
s_add_u32 s55,s55,s79
s_mul_i32 s56,s45,16384
s_add_u32 s56,s56,98566144
s_lshl_b32 s61,s78,11
s_add_u32 s61,s61,110587904
s_add_u32 s78,s45,s46
s_lshr_b32 s78,s78,1
s_lshl_b32 s62,s78,11
s_add_u32 s62,s62,110587904
s_waitcnt vmcnt(5)
s_branch Lr_it1_%=
Lr_tc0_%=:
s_add_u32 s78,s55,71303168
buffer_load_dwordx4 v[196:199],v1,s[36:39],s78 offen offset:0 sc1
buffer_load_dwordx4 v[200:203],v1,s[36:39],s78 offen offset:2048 sc1
s_add_u32 s78,s55,71307264
buffer_load_dwordx4 v[204:207],v1,s[36:39],s78 offen offset:0 sc1
buffer_load_dwordx4 v[208:211],v1,s[36:39],s78 offen offset:2048 sc1
s_add_u32 s78,s55,89128960
buffer_load_dwordx4 v[212:215],v1,s[36:39],s78 offen offset:0 sc1
buffer_load_dwordx4 v[216:219],v1,s[36:39],s78 offen offset:2048 sc1
s_add_u32 s78,s55,89133056
buffer_load_dwordx4 v[220:223],v1,s[36:39],s78 offen offset:0 sc1
buffer_load_dwordx4 v[224:227],v1,s[36:39],s78 offen offset:2048 sc1
buffer_load_dword v18,v6,s[36:39],s62 offen offset:0 sc1
s_waitcnt vmcnt(9)
buffer_store_dword v17,v5,s[36:39],s63 offen offset:0 sc0 sc1
s_mov_b32 s63,0x7fff0000
s_lshr_b32 s78,s45,1
s_and_b32 s79,s45,1
s_lshl_b32 s79,s79,7
s_lshl_b32 s57,s78,15
s_add_u32 s57,s57,s79
s_add_u32 s57,s57,12582912
s_add_u32 s58,s57,4096
s_lshl_b32 s59,s78,9
s_add_u32 s59,s59,s79
s_add_u32 s59,s59,107020288
s_lshl_b32 s60,s45,9
s_add_u32 s60,s60,s66
s_add_u32 s60,s60,131072
s_add_u32 s45,s45,s46
s_lshr_b32 s78,s45,1
s_and_b32 s79,s45,1
s_lshl_b32 s79,s79,7
s_lshl_b32 s55,s78,15
s_add_u32 s55,s55,s79
s_mul_i32 s56,s45,16384
s_add_u32 s56,s56,98566144
s_lshl_b32 s61,s78,11
s_add_u32 s61,s61,110587904
s_add_u32 s78,s45,s46
s_lshr_b32 s78,s78,1
s_lshl_b32 s62,s78,11
s_add_u32 s62,s62,110587904
s_waitcnt vmcnt(1)
s_branch Lr_it1_%=
Lr_it1_%=:
T v[68:69],10,16384
T v[70:71],10,16448
T v[72:73],10,17408
T v[74:75],10,17472
T v[76:77],10,18432
T v[78:79],10,18496
T v[80:81],10,19456
T v[82:83],10,19520
s_nop 0
v_cmp_eq_u32 vcc,s53,v18
s_nop 0
s_cmp_eq_u64 vcc,-1
s_cselect_b32 s78,s45,256
s_cmp_lt_u32 s78,256
s_cbranch_scc1 Lr_ta1_%=
buffer_load_dword v18,v6,s[36:39],s61 offen offset:0 sc1
v_mfma_f32_16x16x32_bf16 v[116:119],v[26:29],v[196:199],0
L 6
v_lshlrev_b32 v84,16,v68
v_and_b32 v85,0xffff0000,v68
v_mfma_f32_16x16x32_bf16 v[120:123],v[24:27],v[196:199],0
L 4
v_lshlrev_b32 v86,16,v69
v_and_b32 v87,0xffff0000,v69
M 116,v[26:29],212
L 2
v_lshlrev_b32 v88,16,v70
v_and_b32 v89,0xffff0000,v70
M 120,v[24:27],212
L 0
v_lshlrev_b32 v90,16,v71
v_and_b32 v91,0xffff0000,v71
v_mfma_f32_16x16x32_bf16 v[124:127],v[26:29],v[200:203],0
s_add_u32 m0,s65,0
v_lshlrev_b32 v92,16,v72
v_and_b32 v93,0xffff0000,v72
v_mfma_f32_16x16x32_bf16 v[128:131],v[24:27],v[200:203],0
D 9,56,0
M 124,v[26:29],216
D 9,56,1024
M 128,v[24:27],216
D 9,56,2048
v_mfma_f32_16x16x32_bf16 v[132:135],v[26:29],v[204:207],0
D 9,56,3072
v_mfma_f32_16x16x32_bf16 v[136:139],v[24:27],v[204:207],0
v_cvt_pk_bf16_f32 v148,v116,v117
v_cvt_pk_bf16_f32 v149,v118,v119
M 132,v[26:29],220
v_cvt_pk_bf16_f32 v150,v120,v121
v_cvt_pk_bf16_f32 v151,v122,v123
M 136,v[24:27],220
)"
R"(v_cvt_pk_bf16_f32 v152,v124,v125
v_cvt_pk_bf16_f32 v153,v126,v127
v_mfma_f32_16x16x32_bf16 v[140:143],v[26:29],v[208:211],0
v_cvt_pk_bf16_f32 v154,v128,v129
v_cvt_pk_bf16_f32 v155,v130,v131
v_mfma_f32_16x16x32_bf16 v[144:147],v[24:27],v[208:211],0
v_lshlrev_b32 v94,16,v73
v_and_b32 v95,0xffff0000,v73
M 140,v[26:29],224
v_cvt_pk_bf16_f32 v156,v132,v133
v_cvt_pk_bf16_f32 v157,v134,v135
M 144,v[24:27],224
v_cvt_pk_bf16_f32 v158,v136,v137
v_cvt_pk_bf16_f32 v159,v138,v139
v_mfma_f32_16x16x32_bf16 v[36:39],v[32:35],v[148:151],0
v_lshlrev_b32 v96,16,v74
v_and_b32 v97,0xffff0000,v74
v_mfma_f32_16x16x32_bf16 v[40:43],v[148:151],v[148:151],0
v_lshlrev_b32 v98,16,v75
v_and_b32 v99,0xffff0000,v75
M 36,v[32:35],152
v_cvt_pk_bf16_f32 v160,v140,v141
v_cvt_pk_bf16_f32 v161,v142,v143
M 40,v[152:155],152
v_cvt_pk_bf16_f32 v162,v144,v145
v_cvt_pk_bf16_f32 v163,v146,v147
M 36,v[32:35],156
v_lshlrev_b32 v100,16,v76
v_and_b32 v101,0xffff0000,v76
M 40,v[156:159],156
v_lshlrev_b32 v102,16,v77
v_and_b32 v103,0xffff0000,v77
M 36,v[32:35],160
v_lshlrev_b32 v104,16,v78
v_and_b32 v105,0xffff0000,v78
M 40,v[160:163],160
v_lshlrev_b32 v106,16,v79
v_and_b32 v107,0xffff0000,v79
v_lshlrev_b32 v108,16,v80
v_and_b32 v109,0xffff0000,v80
v_lshlrev_b32 v110,16,v81
v_and_b32 v111,0xffff0000,v81
v_lshlrev_b32 v112,16,v82
v_and_b32 v113,0xffff0000,v82
v_lshlrev_b32 v114,16,v83
v_and_b32 v115,0xffff0000,v83
v_add_f32_dpp v44,v36,v36 row_ror:8 row_mask:0xf bank_mask:0xf
v_cndmask_b32 v45,v40,v41,s[70:71]
v_cndmask_b32 v46,v42,v43,s[70:71]
ds_write_b32 v2,v44 offset:0
v_cndmask_b32 v45,v45,v46,s[72:73]
ds_write_b32 v0,v45 offset:0
L 0
s_barrier
R 52,3,0
R 56,3,16
R 60,3,32
L 0
v_pk_add_f32 v[52:53],v[52:53],v[54:55]
v_pk_add_f32 v[56:57],v[56:57],v[58:59]
v_pk_add_f32 v[60:61],v[60:61],v[62:63]
v_add_f32 v52,v52,v53
v_pk_add_f32 v[56:57],v[56:57],v[60:61]
v_mul_f32 v52,s68,v52
v_add_f32 v53,v56,v57
v_mul_f32 v53,s68,v53
v_fma_f32 v53,-v52,v52,v53
v_max_f32 v53,0,v53
s_waitcnt vmcnt(5)
buffer_store_dword v17,v5,s[36:39],s63 offen offset:0 sc0 sc1
s_mov_b32 s63,0x7fff0000
s_waitcnt vmcnt(5)
v_cmp_eq_u32 vcc,s53,v18
s_nop 0
s_cmp_eq_u64 vcc,-1
s_cselect_b32 s78,s45,256
s_cmp_lt_u32 s78,256
s_cbranch_scc1 Lr_tb1_%=
buffer_load_dword v18,v6,s[36:39],s61 offen offset:0 sc1
v_add_f32 v53,s69,v53
v_rsq_f32 v48,v53
s_nop 0
v_mul_f32_e64 v50,-v52,v48
v_mov_b32 v49,v48
v_mov_b32 v51,v50
v_pk_fma_f32 v[52:53],v[116:117],v[48:49],v[50:51]
v_pk_fma_f32 v[54:55],v[118:119],v[48:49],v[50:51]
v_pk_fma_f32 v[56:57],v[120:121],v[48:49],v[50:51]
v_pk_fma_f32 v[58:59],v[122:123],v[48:49],v[50:51]
v_pk_fma_f32 v[116:117],v[52:53],v[84:85],v[116:117]
v_pk_fma_f32 v[118:119],v[54:55],v[86:87],v[118:119]
v_pk_fma_f32 v[120:121],v[56:57],v[88:89],v[120:121]
v_pk_fma_f32 v[122:123],v[58:59],v[90:91],v[122:123]
v_cvt_pk_bf16_f32 v148,v116,v117
v_cvt_pk_bf16_f32 v149,v118,v119
v_cvt_pk_bf16_f32 v150,v120,v121
v_cvt_pk_bf16_f32 v151,v122,v123
v_pk_fma_f32 v[52:53],v[124:125],v[48:49],v[50:51]
v_pk_fma_f32 v[54:55],v[126:127],v[48:49],v[50:51]
v_pk_fma_f32 v[56:57],v[128:129],v[48:49],v[50:51]
v_pk_fma_f32 v[58:59],v[130:131],v[48:49],v[50:51]
v_pk_fma_f32 v[124:125],v[52:53],v[92:93],v[124:125]
v_pk_fma_f32 v[126:127],v[54:55],v[94:95],v[126:127]
v_pk_fma_f32 v[128:129],v[56:57],v[96:97],v[128:129]
v_pk_fma_f32 v[130:131],v[58:59],v[98:99],v[130:131]
v_cvt_pk_bf16_f32 v152,v124,v125
v_cvt_pk_bf16_f32 v153,v126,v127
v_cvt_pk_bf16_f32 v154,v128,v129
v_cvt_pk_bf16_f32 v155,v130,v131
v_pk_fma_f32 v[52:53],v[132:133],v[48:49],v[50:51]
v_pk_fma_f32 v[54:55],v[134:135],v[48:49],v[50:51]
v_pk_fma_f32 v[56:57],v[136:137],v[48:49],v[50:51]
v_pk_fma_f32 v[58:59],v[138:139],v[48:49],v[50:51]
v_pk_fma_f32 v[132:133],v[52:53],v[100:101],v[132:133]
v_pk_fma_f32 v[134:135],v[54:55],v[102:103],v[134:135]
v_pk_fma_f32 v[136:137],v[56:57],v[104:105],v[136:137]
v_pk_fma_f32 v[138:139],v[58:59],v[106:107],v[138:139]
v_cvt_pk_bf16_f32 v156,v132,v133
v_cvt_pk_bf16_f32 v157,v134,v135
v_cvt_pk_bf16_f32 v158,v136,v137
v_cvt_pk_bf16_f32 v159,v138,v139
v_pk_fma_f32 v[52:53],v[140:141],v[48:49],v[50:51]
v_pk_fma_f32 v[54:55],v[142:143],v[48:49],v[50:51]
v_pk_fma_f32 v[56:57],v[144:145],v[48:49],v[50:51]
v_pk_fma_f32 v[58:59],v[146:147],v[48:49],v[50:51]
v_pk_fma_f32 v[140:141],v[52:53],v[108:109],v[140:141]
v_pk_fma_f32 v[142:143],v[54:55],v[110:111],v[142:143]
v_pk_fma_f32 v[144:145],v[56:57],v[112:113],v[144:145]
v_pk_fma_f32 v[146:147],v[58:59],v[114:115],v[146:147]
v_cvt_pk_bf16_f32 v160,v140,v141
v_cvt_pk_bf16_f32 v161,v142,v143
v_cvt_pk_bf16_f32 v162,v144,v145
v_cvt_pk_bf16_f32 v163,v146,v147
v_mfma_f32_16x16x32_bf16 v[36:39],v[32:35],v[148:151],0
buffer_store_dwordx4 v[148:151],v1,s[36:39],s57 offen offset:0 sc0 sc1
v_mfma_f32_16x16x32_bf16 v[40:43],v[148:151],v[148:151],0
M 36,v[32:35],152
buffer_store_dwordx4 v[152:155],v1,s[36:39],s57 offen offset:2048 sc0 sc1
M 40,v[152:155],152
M 36,v[32:35],156
buffer_store_dwordx4 v[156:159],v1,s[36:39],s58 offen offset:0 sc0 sc1
M 40,v[156:159],156
M 36,v[32:35],160
buffer_store_dwordx4 v[160:163],v1,s[36:39],s58 offen offset:2048 sc0 sc1
M 40,v[160:163],160
s_nop 5
v_add_f32_dpp v44,v36,v36 row_ror:8 row_mask:0xf bank_mask:0xf
s_nop 0
v_cndmask_b32 v45,v40,v41,s[70:71]
v_cndmask_b32 v46,v42,v43,s[70:71]
ds_write_b32 v2,v44 offset:1024
v_cndmask_b32 v45,v45,v46,s[72:73]
ds_write_b32 v0,v45 offset:1024
L 0
s_barrier
R 52,3,1024
R 56,3,1040
R 60,3,1056
L 0
v_pk_add_f32 v[52:53],v[52:53],v[54:55]
v_pk_add_f32 v[56:57],v[56:57],v[58:59]
v_pk_add_f32 v[60:61],v[60:61],v[62:63]
v_add_f32 v52,v52,v53
v_pk_add_f32 v[56:57],v[56:57],v[60:61]
v_mul_f32 v52,s68,v52
v_add_f32 v53,v56,v57
v_mul_f32 v53,s68,v53
v_fma_f32 v53,-v52,v52,v53
v_max_f32 v53,0,v53
v_add_f32 v54,s69,v53
v_fma_f32 v55,s69,v54,v53
v_rsq_f32 v65,v54
v_rsq_f32 v54,v55
v_mul_f32_e64 v64,-v52,v65
v_mul_f32 v63,v55,v54
v_mul_f32 v61,s76,v54
v_mul_f32 v55,v54,v54
v_mul_f32 v62,s77,v55
v_mul_f32 v62,v62,v61
v_cndmask_b32 v60,v52,v64,s[74:75]
v_cndmask_b32 v61,v61,v65,s[74:75]
buffer_store_dwordx4 v[60:63],v4,s[36:39],s59 offen offset:0 sc0 sc1
s_mov_b32 s63,s60
s_waitcnt vmcnt(5)
v_cmp_eq_u32 vcc,s53,v18
s_nop 0
s_cmp_eq_u64 vcc,-1
s_cselect_b32 s78,s45,256
s_cmp_lt_u32 s78,256
s_cbranch_scc1 Lr_tc1_%=
s_waitcnt vmcnt(0)
buffer_store_dword v17,v5,s[36:39],s63 offen offset:0 sc0 sc1
s_mov_b32 s63,0x7fff0000
s_cmp_ge_u32 s45,256
s_cbranch_scc1 Lr_done_%=
Lr_bp1_%=:
buffer_load_dword v18,v6,s[36:39],s61 offen offset:0 sc1
s_add_u32 s82,s82,1
s_waitcnt vmcnt(0)
v_cmp_eq_u32 vcc,s53,v18
s_nop 0
s_cmp_eq_u64 vcc,-1
s_cbranch_scc1 Lr_bq1_%=
s_sleep 2
s_cmp_lt_u32 s82,20000
s_cbranch_scc1 Lr_bp1_%=
Lr_bq1_%=:
s_add_u32 s78,s55,71303168
buffer_load_dwordx4 v[164:167],v1,s[36:39],s78 offen offset:0 sc1
buffer_load_dwordx4 v[168:171],v1,s[36:39],s78 offen offset:2048 sc1
s_add_u32 s78,s55,71307264
buffer_load_dwordx4 v[172:175],v1,s[36:39],s78 offen offset:0 sc1
buffer_load_dwordx4 v[176:179],v1,s[36:39],s78 offen offset:2048 sc1
s_add_u32 s78,s55,89128960
buffer_load_dwordx4 v[180:183],v1,s[36:39],s78 offen offset:0 sc1
buffer_load_dwordx4 v[184:187],v1,s[36:39],s78 offen offset:2048 sc1
s_add_u32 s78,s55,89133056
buffer_load_dwordx4 v[188:191],v1,s[36:39],s78 offen offset:0 sc1
buffer_load_dwordx4 v[192:195],v1,s[36:39],s78 offen offset:2048 sc1
buffer_load_dword v18,v6,s[36:39],s62 offen offset:0 sc1
s_lshr_b32 s78,s45,1
s_and_b32 s79,s45,1
s_lshl_b32 s79,s79,7
s_lshl_b32 s57,s78,15
s_add_u32 s57,s57,s79
s_add_u32 s57,s57,12582912
s_add_u32 s58,s57,4096
s_lshl_b32 s59,s78,9
s_add_u32 s59,s59,s79
s_add_u32 s59,s59,107020288
s_lshl_b32 s60,s45,9
s_add_u32 s60,s60,s66
s_add_u32 s60,s60,131072
s_add_u32 s45,s45,s46
s_lshr_b32 s78,s45,1
s_and_b32 s79,s45,1
s_lshl_b32 s79,s79,7
s_lshl_b32 s55,s78,15
s_add_u32 s55,s55,s79
s_mul_i32 s56,s45,16384
s_add_u32 s56,s56,98566144
s_lshl_b32 s61,s78,11
s_add_u32 s61,s61,110587904
s_add_u32 s78,s45,s46
s_lshr_b32 s78,s78,1
s_lshl_b32 s62,s78,11
s_add_u32 s62,s62,110587904
s_waitcnt vmcnt(0)
s_branch Lr_it0_%=
Lr_ta1_%=:
v_mfma_f32_16x16x32_bf16 v[116:119],v[26:29],v[196:199],0
s_add_u32 s78,s55,71303168
L 6
L 4
v_mfma_f32_16x16x32_bf16 v[120:123],v[24:27],v[196:199],0
L 2
buffer_load_dwordx4 v[164:167],v1,s[36:39],s78 offen offset:0 sc1
M 116,v[26:29],212
L 0
buffer_load_dwordx4 v[168:171],v1,s[36:39],s78 offen offset:2048 sc1
M 120,v[24:27],212
s_add_u32 s78,s55,71307264
v_lshlrev_b32 v84,16,v68
v_and_b32 v85,0xffff0000,v68
v_mfma_f32_16x16x32_bf16 v[124:127],v[26:29],v[200:203],0
buffer_load_dwordx4 v[172:175],v1,s[36:39],s78 offen offset:0 sc1
v_mfma_f32_16x16x32_bf16 v[128:131],v[24:27],v[200:203],0
buffer_load_dwordx4 v[176:179],v1,s[36:39],s78 offen offset:2048 sc1
M 124,v[26:29],216
s_add_u32 s78,s55,89128960
v_cvt_pk_bf16_f32 v148,v116,v117
v_cvt_pk_bf16_f32 v149,v118,v119
M 128,v[24:27],216
buffer_load_dwordx4 v[180:183],v1,s[36:39],s78 offen offset:0 sc1
v_mfma_f32_16x16x32_bf16 v[132:135],v[26:29],v[204:207],0
buffer_load_dwordx4 v[184:187],v1,s[36:39],s78 offen offset:2048 sc1
v_mfma_f32_16x16x32_bf16 v[136:139],v[24:27],v[204:207],0
s_add_u32 s78,s55,89133056
v_cvt_pk_bf16_f32 v150,v120,v121
v_cvt_pk_bf16_f32 v151,v122,v123
M 132,v[26:29],220
buffer_load_dwordx4 v[188:191],v1,s[36:39],s78 offen offset:0 sc1
M 136,v[24:27],220
buffer_load_dwordx4 v[192:195],v1,s[36:39],s78 offen offset:2048 sc1
v_mfma_f32_16x16x32_bf16 v[140:143],v[26:29],v[208:211],0
s_add_u32 m0,s65,0
v_cvt_pk_bf16_f32 v152,v124,v125
v_cvt_pk_bf16_f32 v153,v126,v127
v_mfma_f32_16x16x32_bf16 v[144:147],v[24:27],v[208:211],0
D 9,56,0
M 140,v[26:29],224
D 9,56,1024
M 144,v[24:27],224
D 9,56,2048
v_mfma_f32_16x16x32_bf16 v[36:39],v[32:35],v[148:151],0
v_cvt_pk_bf16_f32 v154,v128,v129
v_cvt_pk_bf16_f32 v155,v130,v131
v_mfma_f32_16x16x32_bf16 v[40:43],v[148:151],v[148:151],0
D 9,56,3072
M 36,v[32:35],152
v_cvt_pk_bf16_f32 v156,v132,v133
v_cvt_pk_bf16_f32 v157,v134,v135
v_cvt_pk_bf16_f32 v158,v136,v137
v_cvt_pk_bf16_f32 v159,v138,v139
M 40,v[152:155],152
v_cvt_pk_bf16_f32 v160,v140,v141
v_cvt_pk_bf16_f32 v161,v142,v143
M 36,v[32:35],156
v_cvt_pk_bf16_f32 v162,v144,v145
v_cvt_pk_bf16_f32 v163,v146,v147
M 40,v[156:159],156
v_lshlrev_b32 v86,16,v69
v_and_b32 v87,0xffff0000,v69
M 36,v[32:35],160
v_lshlrev_b32 v88,16,v70
v_and_b32 v89,0xffff0000,v70
M 40,v[160:163],160
v_lshlrev_b32 v90,16,v71
v_and_b32 v91,0xffff0000,v71
v_lshlrev_b32 v92,16,v72
v_and_b32 v93,0xffff0000,v72
v_lshlrev_b32 v94,16,v73
v_and_b32 v95,0xffff0000,v73
v_lshlrev_b32 v96,16,v74
v_and_b32 v97,0xffff0000,v74
v_lshlrev_b32 v98,16,v75
v_and_b32 v99,0xffff0000,v75
v_lshlrev_b32 v100,16,v76
v_and_b32 v101,0xffff0000,v76
v_lshlrev_b32 v102,16,v77
v_and_b32 v103,0xffff0000,v77
v_lshlrev_b32 v104,16,v78
v_and_b32 v105,0xffff0000,v78
v_lshlrev_b32 v106,16,v79
v_and_b32 v107,0xffff0000,v79
v_lshlrev_b32 v108,16,v80
v_and_b32 v109,0xffff0000,v80
v_lshlrev_b32 v110,16,v81
v_and_b32 v111,0xffff0000,v81
v_lshlrev_b32 v112,16,v82
v_and_b32 v113,0xffff0000,v82
v_lshlrev_b32 v114,16,v83
v_and_b32 v115,0xffff0000,v83
v_add_f32_dpp v44,v36,v36 row_ror:8 row_mask:0xf bank_mask:0xf
v_cndmask_b32 v45,v40,v41,s[70:71]
v_cndmask_b32 v46,v42,v43,s[70:71]
ds_write_b32 v2,v44 offset:0
v_cndmask_b32 v45,v45,v46,s[72:73]
ds_write_b32 v0,v45 offset:0
L 0
s_barrier
R 52,3,0
R 56,3,16
R 60,3,32
L 0
v_pk_add_f32 v[52:53],v[52:53],v[54:55]
v_pk_add_f32 v[56:57],v[56:57],v[58:59]
v_pk_add_f32 v[60:61],v[60:61],v[62:63]
v_add_f32 v52,v52,v53
v_pk_add_f32 v[56:57],v[56:57],v[60:61]
v_mul_f32 v52,s68,v52
v_add_f32 v53,v56,v57
v_mul_f32 v53,s68,v53
v_fma_f32 v53,-v52,v52,v53
v_max_f32 v53,0,v53
buffer_load_dword v18,v6,s[36:39],s62 offen offset:0 sc1
v_add_f32 v53,s69,v53
v_rsq_f32 v48,v53
s_nop 0
v_mul_f32_e64 v50,-v52,v48
v_mov_b32 v49,v48
v_mov_b32 v51,v50
v_pk_fma_f32 v[52:53],v[116:117],v[48:49],v[50:51]
v_pk_fma_f32 v[54:55],v[118:119],v[48:49],v[50:51]
v_pk_fma_f32 v[56:57],v[120:121],v[48:49],v[50:51]
v_pk_fma_f32 v[58:59],v[122:123],v[48:49],v[50:51]
v_pk_fma_f32 v[116:117],v[52:53],v[84:85],v[116:117]
v_pk_fma_f32 v[118:119],v[54:55],v[86:87],v[118:119]
v_pk_fma_f32 v[120:121],v[56:57],v[88:89],v[120:121]
v_pk_fma_f32 v[122:123],v[58:59],v[90:91],v[122:123]
v_cvt_pk_bf16_f32 v148,v116,v117
v_cvt_pk_bf16_f32 v149,v118,v119
v_cvt_pk_bf16_f32 v150,v120,v121
v_cvt_pk_bf16_f32 v151,v122,v123
v_pk_fma_f32 v[52:53],v[124:125],v[48:49],v[50:51]
v_pk_fma_f32 v[54:55],v[126:127],v[48:49],v[50:51]
v_pk_fma_f32 v[56:57],v[128:129],v[48:49],v[50:51]
v_pk_fma_f32 v[58:59],v[130:131],v[48:49],v[50:51]
v_pk_fma_f32 v[124:125],v[52:53],v[92:93],v[124:125]
v_pk_fma_f32 v[126:127],v[54:55],v[94:95],v[126:127]
v_pk_fma_f32 v[128:129],v[56:57],v[96:97],v[128:129]
v_pk_fma_f32 v[130:131],v[58:59],v[98:99],v[130:131]
v_cvt_pk_bf16_f32 v152,v124,v125
v_cvt_pk_bf16_f32 v153,v126,v127
v_cvt_pk_bf16_f32 v154,v128,v129
v_cvt_pk_bf16_f32 v155,v130,v131
v_pk_fma_f32 v[52:53],v[132:133],v[48:49],v[50:51]
v_pk_fma_f32 v[54:55],v[134:135],v[48:49],v[50:51]
v_pk_fma_f32 v[56:57],v[136:137],v[48:49],v[50:51]
v_pk_fma_f32 v[58:59],v[138:139],v[48:49],v[50:51]
v_pk_fma_f32 v[132:133],v[52:53],v[100:101],v[132:133]
v_pk_fma_f32 v[134:135],v[54:55],v[102:103],v[134:135]
v_pk_fma_f32 v[136:137],v[56:57],v[104:105],v[136:137]
v_pk_fma_f32 v[138:139],v[58:59],v[106:107],v[138:139]
v_cvt_pk_bf16_f32 v156,v132,v133
v_cvt_pk_bf16_f32 v157,v134,v135
v_cvt_pk_bf16_f32 v158,v136,v137
v_cvt_pk_bf16_f32 v159,v138,v139
v_pk_fma_f32 v[52:53],v[140:141],v[48:49],v[50:51]
v_pk_fma_f32 v[54:55],v[142:143],v[48:49],v[50:51]
v_pk_fma_f32 v[56:57],v[144:145],v[48:49],v[50:51]
v_pk_fma_f32 v[58:59],v[146:147],v[48:49],v[50:51]
v_pk_fma_f32 v[140:141],v[52:53],v[108:109],v[140:141]
v_pk_fma_f32 v[142:143],v[54:55],v[110:111],v[142:143]
v_pk_fma_f32 v[144:145],v[56:57],v[112:113],v[144:145]
v_pk_fma_f32 v[146:147],v[58:59],v[114:115],v[146:147]
v_cvt_pk_bf16_f32 v160,v140,v141
v_cvt_pk_bf16_f32 v161,v142,v143
v_cvt_pk_bf16_f32 v162,v144,v145
v_cvt_pk_bf16_f32 v163,v146,v147
s_waitcnt vmcnt(13)
buffer_store_dword v17,v5,s[36:39],s63 offen offset:0 sc0 sc1
s_mov_b32 s63,0x7fff0000
v_mfma_f32_16x16x32_bf16 v[36:39],v[32:35],v[148:151],0
buffer_store_dwordx4 v[148:151],v1,s[36:39],s57 offen offset:0 sc0 sc1
v_mfma_f32_16x16x32_bf16 v[40:43],v[148:151],v[148:151],0
M 36,v[32:35],152
buffer_store_dwordx4 v[152:155],v1,s[36:39],s57 offen offset:2048 sc0 sc1
M 40,v[152:155],152
M 36,v[32:35],156
buffer_store_dwordx4 v[156:159],v1,s[36:39],s58 offen offset:0 sc0 sc1
M 40,v[156:159],156
M 36,v[32:35],160
buffer_store_dwordx4 v[160:163],v1,s[36:39],s58 offen offset:2048 sc0 sc1
M 40,v[160:163],160
s_nop 5
v_add_f32_dpp v44,v36,v36 row_ror:8 row_mask:0xf bank_mask:0xf
s_nop 0
v_cndmask_b32 v45,v40,v41,s[70:71]
v_cndmask_b32 v46,v42,v43,s[70:71]
ds_write_b32 v2,v44 offset:1024
v_cndmask_b32 v45,v45,v46,s[72:73]
ds_write_b32 v0,v45 offset:1024
L 0
s_barrier
R 52,3,1024
R 56,3,1040
R 60,3,1056
L 0
v_pk_add_f32 v[52:53],v[52:53],v[54:55]
v_pk_add_f32 v[56:57],v[56:57],v[58:59]
v_pk_add_f32 v[60:61],v[60:61],v[62:63]
v_add_f32 v52,v52,v53
v_pk_add_f32 v[56:57],v[56:57],v[60:61]
v_mul_f32 v52,s68,v52
v_add_f32 v53,v56,v57
v_mul_f32 v53,s68,v53
v_fma_f32 v53,-v52,v52,v53
v_max_f32 v53,0,v53
v_add_f32 v54,s69,v53
v_fma_f32 v55,s69,v54,v53
v_rsq_f32 v65,v54
v_rsq_f32 v54,v55
v_mul_f32_e64 v64,-v52,v65
v_mul_f32 v63,v55,v54
v_mul_f32 v61,s76,v54
v_mul_f32 v55,v54,v54
v_mul_f32 v62,s77,v55
v_mul_f32 v62,v62,v61
v_cndmask_b32 v60,v52,v64,s[74:75]
v_cndmask_b32 v61,v61,v65,s[74:75]
buffer_store_dwordx4 v[60:63],v4,s[36:39],s59 offen offset:0 sc0 sc1
s_mov_b32 s63,s60
s_lshr_b32 s78,s45,1
s_and_b32 s79,s45,1
s_lshl_b32 s79,s79,7
s_lshl_b32 s57,s78,15
s_add_u32 s57,s57,s79
s_add_u32 s57,s57,12582912
s_add_u32 s58,s57,4096
s_lshl_b32 s59,s78,9
s_add_u32 s59,s59,s79
s_add_u32 s59,s59,107020288
s_lshl_b32 s60,s45,9
s_add_u32 s60,s60,s66
s_add_u32 s60,s60,131072
s_add_u32 s45,s45,s46
s_lshr_b32 s78,s45,1
s_and_b32 s79,s45,1
s_lshl_b32 s79,s79,7
s_lshl_b32 s55,s78,15
s_add_u32 s55,s55,s79
s_mul_i32 s56,s45,16384
s_add_u32 s56,s56,98566144
s_lshl_b32 s61,s78,11
s_add_u32 s61,s61,110587904
s_add_u32 s78,s45,s46
s_lshr_b32 s78,s78,1
s_lshl_b32 s62,s78,11
s_add_u32 s62,s62,110587904
)"
R"(s_waitcnt vmcnt(6)
s_branch Lr_it0_%=
Lr_tb1_%=:
v_add_f32 v53,s69,v53
v_rsq_f32 v48,v53
s_nop 0
v_mul_f32_e64 v50,-v52,v48
v_mov_b32 v49,v48
v_mov_b32 v51,v50
v_pk_fma_f32 v[52:53],v[116:117],v[48:49],v[50:51]
v_pk_fma_f32 v[54:55],v[118:119],v[48:49],v[50:51]
v_pk_fma_f32 v[56:57],v[120:121],v[48:49],v[50:51]
v_pk_fma_f32 v[58:59],v[122:123],v[48:49],v[50:51]
s_add_u32 s78,s55,71303168
buffer_load_dwordx4 v[164:167],v1,s[36:39],s78 offen offset:0 sc1
buffer_load_dwordx4 v[168:171],v1,s[36:39],s78 offen offset:2048 sc1
s_add_u32 s78,s55,71307264
buffer_load_dwordx4 v[172:175],v1,s[36:39],s78 offen offset:0 sc1
buffer_load_dwordx4 v[176:179],v1,s[36:39],s78 offen offset:2048 sc1
v_pk_fma_f32 v[116:117],v[52:53],v[84:85],v[116:117]
v_pk_fma_f32 v[118:119],v[54:55],v[86:87],v[118:119]
v_pk_fma_f32 v[120:121],v[56:57],v[88:89],v[120:121]
v_pk_fma_f32 v[122:123],v[58:59],v[90:91],v[122:123]
v_cvt_pk_bf16_f32 v148,v116,v117
v_cvt_pk_bf16_f32 v149,v118,v119
v_cvt_pk_bf16_f32 v150,v120,v121
v_cvt_pk_bf16_f32 v151,v122,v123
v_pk_fma_f32 v[52:53],v[124:125],v[48:49],v[50:51]
v_pk_fma_f32 v[54:55],v[126:127],v[48:49],v[50:51]
v_pk_fma_f32 v[56:57],v[128:129],v[48:49],v[50:51]
v_pk_fma_f32 v[58:59],v[130:131],v[48:49],v[50:51]
s_add_u32 s78,s55,89128960
buffer_load_dwordx4 v[180:183],v1,s[36:39],s78 offen offset:0 sc1
buffer_load_dwordx4 v[184:187],v1,s[36:39],s78 offen offset:2048 sc1
s_add_u32 s78,s55,89133056
buffer_load_dwordx4 v[188:191],v1,s[36:39],s78 offen offset:0 sc1
buffer_load_dwordx4 v[192:195],v1,s[36:39],s78 offen offset:2048 sc1
v_pk_fma_f32 v[124:125],v[52:53],v[92:93],v[124:125]
v_pk_fma_f32 v[126:127],v[54:55],v[94:95],v[126:127]
v_pk_fma_f32 v[128:129],v[56:57],v[96:97],v[128:129]
v_pk_fma_f32 v[130:131],v[58:59],v[98:99],v[130:131]
v_cvt_pk_bf16_f32 v152,v124,v125
v_cvt_pk_bf16_f32 v153,v126,v127
v_cvt_pk_bf16_f32 v154,v128,v129
v_cvt_pk_bf16_f32 v155,v130,v131
v_pk_fma_f32 v[52:53],v[132:133],v[48:49],v[50:51]
v_pk_fma_f32 v[54:55],v[134:135],v[48:49],v[50:51]
v_pk_fma_f32 v[56:57],v[136:137],v[48:49],v[50:51]
v_pk_fma_f32 v[58:59],v[138:139],v[48:49],v[50:51]
v_pk_fma_f32 v[132:133],v[52:53],v[100:101],v[132:133]
v_pk_fma_f32 v[134:135],v[54:55],v[102:103],v[134:135]
v_pk_fma_f32 v[136:137],v[56:57],v[104:105],v[136:137]
v_pk_fma_f32 v[138:139],v[58:59],v[106:107],v[138:139]
v_cvt_pk_bf16_f32 v156,v132,v133
v_cvt_pk_bf16_f32 v157,v134,v135
v_cvt_pk_bf16_f32 v158,v136,v137
v_cvt_pk_bf16_f32 v159,v138,v139
v_pk_fma_f32 v[52:53],v[140:141],v[48:49],v[50:51]
v_pk_fma_f32 v[54:55],v[142:143],v[48:49],v[50:51]
v_pk_fma_f32 v[56:57],v[144:145],v[48:49],v[50:51]
v_pk_fma_f32 v[58:59],v[146:147],v[48:49],v[50:51]
v_pk_fma_f32 v[140:141],v[52:53],v[108:109],v[140:141]
v_pk_fma_f32 v[142:143],v[54:55],v[110:111],v[142:143]
v_pk_fma_f32 v[144:145],v[56:57],v[112:113],v[144:145]
v_pk_fma_f32 v[146:147],v[58:59],v[114:115],v[146:147]
v_cvt_pk_bf16_f32 v160,v140,v141
v_cvt_pk_bf16_f32 v161,v142,v143
v_cvt_pk_bf16_f32 v162,v144,v145
v_cvt_pk_bf16_f32 v163,v146,v147
v_mfma_f32_16x16x32_bf16 v[36:39],v[32:35],v[148:151],0
buffer_load_dword v18,v6,s[36:39],s62 offen offset:0 sc1
buffer_store_dwordx4 v[148:151],v1,s[36:39],s57 offen offset:0 sc0 sc1
v_mfma_f32_16x16x32_bf16 v[40:43],v[148:151],v[148:151],0
M 36,v[32:35],152
buffer_store_dwordx4 v[152:155],v1,s[36:39],s57 offen offset:2048 sc0 sc1
M 40,v[152:155],152
M 36,v[32:35],156
buffer_store_dwordx4 v[156:159],v1,s[36:39],s58 offen offset:0 sc0 sc1
M 40,v[156:159],156
M 36,v[32:35],160
buffer_store_dwordx4 v[160:163],v1,s[36:39],s58 offen offset:2048 sc0 sc1
M 40,v[160:163],160
s_nop 5
v_add_f32_dpp v44,v36,v36 row_ror:8 row_mask:0xf bank_mask:0xf
s_nop 0
v_cndmask_b32 v45,v40,v41,s[70:71]
v_cndmask_b32 v46,v42,v43,s[70:71]
ds_write_b32 v2,v44 offset:1024
v_cndmask_b32 v45,v45,v46,s[72:73]
ds_write_b32 v0,v45 offset:1024
L 0
s_barrier
R 52,3,1024
R 56,3,1040
R 60,3,1056
L 0
v_pk_add_f32 v[52:53],v[52:53],v[54:55]
v_pk_add_f32 v[56:57],v[56:57],v[58:59]
v_pk_add_f32 v[60:61],v[60:61],v[62:63]
v_add_f32 v52,v52,v53
v_pk_add_f32 v[56:57],v[56:57],v[60:61]
v_mul_f32 v52,s68,v52
v_add_f32 v53,v56,v57
v_mul_f32 v53,s68,v53
v_fma_f32 v53,-v52,v52,v53
v_max_f32 v53,0,v53
v_add_f32 v54,s69,v53
v_fma_f32 v55,s69,v54,v53
v_rsq_f32 v65,v54
v_rsq_f32 v54,v55
v_mul_f32_e64 v64,-v52,v65
v_mul_f32 v63,v55,v54
v_mul_f32 v61,s76,v54
v_mul_f32 v55,v54,v54
v_mul_f32 v62,s77,v55
v_mul_f32 v62,v62,v61
v_cndmask_b32 v60,v52,v64,s[74:75]
v_cndmask_b32 v61,v61,v65,s[74:75]
buffer_store_dwordx4 v[60:63],v4,s[36:39],s59 offen offset:0 sc0 sc1
s_mov_b32 s63,s60
s_lshr_b32 s78,s45,1
s_and_b32 s79,s45,1
s_lshl_b32 s79,s79,7
s_lshl_b32 s57,s78,15
s_add_u32 s57,s57,s79
s_add_u32 s57,s57,12582912
s_add_u32 s58,s57,4096
s_lshl_b32 s59,s78,9
s_add_u32 s59,s59,s79
s_add_u32 s59,s59,107020288
s_lshl_b32 s60,s45,9
s_add_u32 s60,s60,s66
s_add_u32 s60,s60,131072
s_add_u32 s45,s45,s46
s_lshr_b32 s78,s45,1
s_and_b32 s79,s45,1
s_lshl_b32 s79,s79,7
s_lshl_b32 s55,s78,15
s_add_u32 s55,s55,s79
s_mul_i32 s56,s45,16384
s_add_u32 s56,s56,98566144
s_lshl_b32 s61,s78,11
s_add_u32 s61,s61,110587904
s_add_u32 s78,s45,s46
s_lshr_b32 s78,s78,1
s_lshl_b32 s62,s78,11
s_add_u32 s62,s62,110587904
s_waitcnt vmcnt(5)
s_branch Lr_it0_%=
Lr_tc1_%=:
s_add_u32 s78,s55,71303168
buffer_load_dwordx4 v[164:167],v1,s[36:39],s78 offen offset:0 sc1
buffer_load_dwordx4 v[168:171],v1,s[36:39],s78 offen offset:2048 sc1
s_add_u32 s78,s55,71307264
buffer_load_dwordx4 v[172:175],v1,s[36:39],s78 offen offset:0 sc1
buffer_load_dwordx4 v[176:179],v1,s[36:39],s78 offen offset:2048 sc1
s_add_u32 s78,s55,89128960
buffer_load_dwordx4 v[180:183],v1,s[36:39],s78 offen offset:0 sc1
buffer_load_dwordx4 v[184:187],v1,s[36:39],s78 offen offset:2048 sc1
s_add_u32 s78,s55,89133056
buffer_load_dwordx4 v[188:191],v1,s[36:39],s78 offen offset:0 sc1
buffer_load_dwordx4 v[192:195],v1,s[36:39],s78 offen offset:2048 sc1
buffer_load_dword v18,v6,s[36:39],s62 offen offset:0 sc1
s_waitcnt vmcnt(9)
buffer_store_dword v17,v5,s[36:39],s63 offen offset:0 sc0 sc1
s_mov_b32 s63,0x7fff0000
s_lshr_b32 s78,s45,1
s_and_b32 s79,s45,1
s_lshl_b32 s79,s79,7
s_lshl_b32 s57,s78,15
s_add_u32 s57,s57,s79
s_add_u32 s57,s57,12582912
s_add_u32 s58,s57,4096
s_lshl_b32 s59,s78,9
s_add_u32 s59,s59,s79
s_add_u32 s59,s59,107020288
s_lshl_b32 s60,s45,9
s_add_u32 s60,s60,s66
s_add_u32 s60,s60,131072
s_add_u32 s45,s45,s46
s_lshr_b32 s78,s45,1
s_and_b32 s79,s45,1
s_lshl_b32 s79,s79,7
s_lshl_b32 s55,s78,15
s_add_u32 s55,s55,s79
s_mul_i32 s56,s45,16384
s_add_u32 s56,s56,98566144
s_lshl_b32 s61,s78,11
s_add_u32 s61,s61,110587904
s_add_u32 s78,s45,s46
s_lshr_b32 s78,s78,1
s_lshl_b32 s62,s78,11
s_add_u32 s62,s62,110587904
s_waitcnt vmcnt(1)
s_branch Lr_it0_%=
Lr_done_%=:
s_waitcnt vmcnt(0) lgkmcnt(0)
s_barrier
Lcode_r_e:
.purgem M
.purgem R
.purgem T
.purgem D
.purgem L
)"
  :: "s"(ws), "s"(__builtin_amdgcn_readfirstlane(sWave)), "s"(__builtin_amdgcn_readfirstlane(sG0)), "s"(__builtin_amdgcn_readfirstlane(sStep)), "s"(__builtin_amdgcn_readfirstlane(sWw)), "s"(__builtin_amdgcn_readfirstlane(sExp)), "v"(vRP), "v"(vRF), "v"(vRwr), "v"(vRrd), "v"(vRS), "v"(vFlag), "v"(vL4), "v"(vLY), "v"(vQ), "v"(vY0), "v"(vTr), "v"(vSa), "v"(vSb), "v"(vWd0), "v"(vWd1), "v"(vWr0), "v"(vWr1), "s"(q), "s"(w4) : "v0", "v1", "v2", "v3", "v4", "v5", "v6", "v7", "v8", "v9", "v10", "v11", "v12", "v13", "v14", "v15", "v16", "v17", "v18", "v19", "v20", "v21", "v22", "v23", "v24", "v25", "v26", "v27", "v28", "v29", "v30", "v31", "v32", "v33", "v34", "v35", "v36", "v37", "v38", "v39", "v40", "v41", "v42", "v43", "v44", "v45", "v46", "v47", "v48", "v49", "v50", "v51", "v52", "v53", "v54", "v55", "v56", "v57", "v58", "v59", "v60", "v61", "v62", "v63", "v64", "v65", "v66", "v67", "v68", "v69", "v70", "v71", "v72", "v73", "v74", "v75", "v76", "v77", "v78", "v79", "v80", "v81", "v82", "v83", "v84", "v85", "v86", "v87", "v88", "v89", "v90", "v91", "v92", "v93", "v94", "v95", "v96", "v97", "v98", "v99", "v100", "v101", "v102", "v103", "v104", "v105", "v106", "v107", "v108", "v109", "v110", "v111", "v112", "v113", "v114", "v115", "v116", "v117", "v118", "v119", "v120", "v121", "v122", "v123", "v124", "v125", "v126", "v127", "v128", "v129", "v130", "v131", "v132", "v133", "v134", "v135", "v136", "v137", "v138", "v139", "v140", "v141", "v142", "v143", "v144", "v145", "v146", "v147", "v148", "v149", "v150", "v151", "v152", "v153", "v154", "v155", "v156", "v157", "v158", "v159", "v160", "v161", "v162", "v163", "v164", "v165", "v166", "v167", "v168", "v169", "v170", "v171", "v172", "v173", "v174", "v175", "v176", "v177", "v178", "v179", "v180", "v181", "v182", "v183", "v184", "v185", "v186", "v187", "v188", "v189", "v190", "v191", "v192", "v193", "v194", "v195", "v196", "v197", "v198", "v199", "v200", "v201", "v202", "v203", "v204", "v205", "v206", "v207", "v208", "v209", "v210", "v211", "v212", "v213", "v214", "v215", "v216", "v217", "v218", "v219", "v220", "v221", "v222", "v223", "v224", "v225", "v226", "v227", "v228", "v229", "v230", "v231", "a0", "a1", "a2", "a3", "a4", "a5", "a6", "a7", "a8", "a9", "a10", "a11", "a12", "a13", "a14", "a15", "a16", "a17", "a18", "a19", "a20", "a21", "a22", "a23", "a24", "a25", "a26", "a27", "a28", "a29", "a30", "a31", "a32", "a33", "a34", "a35", "a36", "a37", "a38", "a39", "a40", "a41", "a42", "a43", "a44", "a45", "a46", "a47", "a48", "a49", "a50", "a51", "a52", "a53", "a54", "a55", "a56", "a57", "a58", "a59", "a60", "a61", "a62", "a63", "a64", "a65", "a66", "a67", "a68", "a69", "a70", "a71", "a72", "a73", "a74", "a75", "a76", "a77", "a78", "a79", "a80", "a81", "a82", "a83", "a84", "a85", "a86", "a87", "a88", "a89", "a90", "a91", "a92", "a93", "a94", "a95", "a96", "a97", "a98", "a99", "a100", "a101", "a102", "a103", "a104", "a105", "a106", "a107", "a108", "a109", "a110", "a111", "a112", "a113", "a114", "a115", "a116", "a117", "a118", "a119", "a120", "a121", "a122", "a123", "a124", "a125", "a126", "a127", "a128", "a129", "a130", "a131", "a132", "a133", "a134", "a135", "a136", "a137", "a138", "a139", "a140", "a141", "a142", "a143", "a144", "a145", "a146", "a147", "a148", "a149", "a150", "a151", "a152", "a153", "a154", "a155", "a156", "a157", "a158", "a159", "a160", "a161", "a162", "a163", "a164", "a165", "a166", "a167", "a168", "a169", "a170", "a171", "a172", "a173", "a174", "a175", "a176", "a177", "a178", "a179", "a180", "a181", "a182", "a183", "a184", "a185", "a186", "a187", "a188", "a189", "a190", "a191", "a192", "a193", "a194", "a195", "a196", "a197", "a198", "a199", "a200", "a201", "a202", "a203", "a204", "a205", "a206", "a207", "a208", "a209", "a210", "a211", "a212", "a213", "a214", "a215", "a216", "a217", "a218", "a219", "a220", "a221", "a222", "a223", "a224", "a225", "a226", "a227", "a228", "a229", "a230", "a231", "a232", "a233", "a234", "a235", "a236", "a237", "a238", "a239", "a240", "a241", "a242", "a243", "a244", "a245", "a246", "a247", "a248", "a249", "a250", "a251", "a252", "a253", "a254", "a255", "s36", "s37", "s38", "s39", "s40", "s41", "s42", "s43", "s44", "s45", "s46", "s47", "s48", "s49", "s50", "s51", "s52", "s53", "s54", "s55", "s56", "s57", "s58", "s59", "s60", "s61", "s62", "s63", "s64", "s65", "s66", "s67", "s68", "s69", "s70", "s71", "s72", "s73", "s74", "s75", "s76", "s77", "s78", "s79", "s80", "s81", "s82", "s83", "s84", "s85", "s86", "s87", "s88", "s89", "s90", "s91", "s92", "s93", "s94", "s95", "s96", "s97", "vcc", "scc", "memory");
    if (RHELP && slice < 16) { kind = 4; rep = 5; kh = slice >> 3; slice &= 7; }
  }
  CTS(9);
#define COMMON(NF, FOFF, FSTR, SNAP, LDW, NL, TOFF)                                                                                            \
  LANE_ID(ln);                                                                                                                       \
  const int l15 = ln & 15, lg = ln >> 4, lq = l15 >> 2, lp = ln & 3, rr = 8 * lg + lq, fj = ln & 31;                                 \
  const unsigned vL16 = ln * 16, vFlag = (ln < 32 && wave == 0) ? ln * 4 : OOB;                                                      \
  const unsigned vSnapOff = (wave == 0 && fj < (NF)) ? (FOFF) + (ln >> 5) * sStep * (FSTR) : OOB;                                    \
  const unsigned vSnapRd = (SNAP) + (fj < (NF) ? ln : ln & 32) * 4;                                                                  \
  const unsigned vWd0 = (ln >> 3) * (LDW) + ((ln & 7) ^ swzB(ln >> 3)) * 16, vWd1 = ((ln >> 3) + 8) * (LDW) + ((ln & 7) ^ swzB(8 + (ln >> 3))) * 16; \
  const unsigned vWr0 = (wave >> 1) * 8192 + rr * 128 + (((wave & 1) * 4 + lp) ^ swzB(rr)) * 16, vWr1 = vWr0 + 8;                    \
  const unsigned vWr2 = 16384 + rr * 128 + ((2 * wave + (lp >> 1)) ^ swzB(rr)) * 16 + (lp & 1) * 8;                                  \
  const unsigned sWave = wave, sRk = kind == 4 ? rep : t_start, sG0 = rep < 5 ? rep : rep == 5 ? U0 + 5 : U1 + rep, sDbg = sDbg0 + (rep < 5 ? 0 : 2048)
  if (kind == 3 || kind == 5) {
    const unsigned sStep = kind == 3 ? 5 : G3OR;
    COMMON(8, fj * 128, 1024, 131072, 7168, 256, 0);
    const unsigned vOutS = (lg & 1) * 8 + (((wave & 1) * 2 + (lg >> 1)) * 16 + l15) * 16 + (4 + (wave >> 1) - wave) * 1024;
    const unsigned vAux0 = (ln & 7) * 128 + (4 * wave + lg) * 8 + ((ln >> 3) & 1) * 4;
    const float k1 = -2.0f * 0.7978845608028654f * 1.4426950408889634f;
    const unsigned vAux1 = 139264 + (l15 < 4 ? (4 * wave + lg) * 16 + l15 * 4 : 512 + ln * 4), vAux2 = 139264 + l15 * 16;
    const unsigned vAux3 = l15 == 2 ? __float_as_uint(k1 * 0.044715f) : 0u, vAn = l15 == 3 ? 0x3f800000u : 0u, vAe = l15 == 1 ? __float_as_uint(k1) : 0u;
    const unsigned sOutOff = OFF_G + (slice * 6 + wave) * 1024, sFoutOff = OFF_FG + slice * 128, sAuxOff = 0, sWw = 16 * wave * 7168 + slice * 384, sBuf = 0;
    const unsigned sExp = __builtin_amdgcn_readfirstlane(ep);
    if (kind == 3) { asm volatile(
R"(.macro M d,w,x
v_mfma_f32_16x16x32_bf16 v[\d:\d+3], \w, v[\x:\x+3], v[\d:\d+3]
.endm
.macro R d,a,o
ds_read_b128 v[\d:\d+3], v\a offset:\o
.endm
.macro T d,a,o
ds_read_b64_tr_b16 \d, v\a offset:\o
.endm
.macro D a,s,o
buffer_load_dwordx4 v\a, s[36:39], s\s offen offset:\o sc1 lds
.endm
.macro L c
s_waitcnt lgkmcnt(\c)
.endm
Lcode_g3_b:
s_mov_b64 s[36:37],%0
s_mov_b32 s38,111243392
s_mov_b32 s39,0x00020000
s_mov_b32 s44,%1
s_mov_b32 s45,%2
s_mov_b32 s46,%3
s_mov_b32 s47,%4
s_mov_b32 s48,%5
s_mov_b32 s49,%6
s_mov_b32 s50,%7
s_mov_b32 s51,%8
s_mov_b32 s53,%9
v_mov_b32 v0,%10
v_mov_b32 v3,%11
v_mov_b32 v5,%12
v_mov_b32 v6,%13
v_mov_b32 v18,%14
v_mov_b32 v19,%15
v_mov_b32 v20,%16
v_mov_b32 v21,%17
v_mov_b32 v22,%18
v_mov_b32 v10,%19
v_mov_b32 v11,%20
v_mov_b32 v12,%21
v_mov_b32 v13,%22
v_mov_b32 v14,%23
v_mov_b32 v15,%24
v_mov_b32 v16,%25
s_mov_b64 s[40:41],%26
s_mov_b32 s42,7340032
s_mov_b32 s43,0x00020000
s_lshl_b32 s57,s44,11
s_cmp_ge_u32 s45,8
s_cbranch_scc1 Lg3_dly_%=
s_sleep 16
s_sleep 16
s_sleep 16
Lg3_dly_%=:
s_add_u32 s55,s50,0
s_add_u32 m0,s57,0
s_nop 0
buffer_load_dwordx4 v18,s[40:43],s55 offen offset:0  lds
s_add_u32 m0,s57,1024
s_nop 0
buffer_load_dwordx4 v19,s[40:43],s55 offen offset:0  lds
s_add_u32 m0,s57,8064
s_nop 0
buffer_load_dwordx4 v18,s[40:43],s55 offen offset:128  lds
s_add_u32 m0,s57,9088
s_nop 0
buffer_load_dwordx4 v19,s[40:43],s55 offen offset:128  lds
s_add_u32 m0,s57,16128
s_nop 0
buffer_load_dwordx4 v18,s[40:43],s55 offen offset:256  lds
s_add_u32 m0,s57,17152
s_nop 0
buffer_load_dwordx4 v19,s[40:43],s55 offen offset:256  lds
s_add_u32 s55,s50,458752
s_add_u32 m0,s57,24576
s_nop 0
buffer_load_dwordx4 v18,s[40:43],s55 offen offset:0  lds
s_add_u32 m0,s57,25600
s_nop 0
buffer_load_dwordx4 v19,s[40:43],s55 offen offset:0  lds
s_add_u32 m0,s57,32640
s_nop 0
buffer_load_dwordx4 v18,s[40:43],s55 offen offset:128  lds
s_add_u32 m0,s57,33664
s_nop 0
buffer_load_dwordx4 v19,s[40:43],s55 offen offset:128  lds
s_add_u32 m0,s57,40704
s_nop 0
buffer_load_dwordx4 v18,s[40:43],s55 offen offset:256  lds
s_add_u32 m0,s57,41728
s_nop 0
buffer_load_dwordx4 v19,s[40:43],s55 offen offset:256  lds
s_add_u32 s55,s50,917504
s_add_u32 m0,s57,49152
s_nop 0
buffer_load_dwordx4 v18,s[40:43],s55 offen offset:0  lds
s_add_u32 m0,s57,50176
s_nop 0
buffer_load_dwordx4 v19,s[40:43],s55 offen offset:0  lds
s_add_u32 m0,s57,57216
s_nop 0
buffer_load_dwordx4 v18,s[40:43],s55 offen offset:128  lds
s_add_u32 m0,s57,58240
s_nop 0
buffer_load_dwordx4 v19,s[40:43],s55 offen offset:128  lds
s_add_u32 m0,s57,65280
s_nop 0
buffer_load_dwordx4 v18,s[40:43],s55 offen offset:256  lds
s_add_u32 m0,s57,66304
s_nop 0
buffer_load_dwordx4 v19,s[40:43],s55 offen offset:256  lds
s_add_u32 s55,s50,1376256
s_add_u32 m0,s57,73728
s_nop 0
buffer_load_dwordx4 v18,s[40:43],s55 offen offset:0  lds
s_add_u32 m0,s57,74752
s_nop 0
buffer_load_dwordx4 v19,s[40:43],s55 offen offset:0  lds
s_add_u32 m0,s57,81792
s_nop 0
buffer_load_dwordx4 v18,s[40:43],s55 offen offset:128  lds
s_add_u32 m0,s57,82816
s_nop 0
buffer_load_dwordx4 v19,s[40:43],s55 offen offset:128  lds
s_add_u32 m0,s57,89856
s_nop 0
buffer_load_dwordx4 v18,s[40:43],s55 offen offset:256  lds
s_add_u32 m0,s57,90880
s_nop 0
buffer_load_dwordx4 v19,s[40:43],s55 offen offset:256  lds
s_add_u32 s55,s50,1835008
s_add_u32 m0,s57,98304
s_nop 0
buffer_load_dwordx4 v18,s[40:43],s55 offen offset:0  lds
s_add_u32 m0,s57,99328
s_nop 0
buffer_load_dwordx4 v19,s[40:43],s55 offen offset:0  lds
s_add_u32 m0,s57,106368
s_nop 0
buffer_load_dwordx4 v18,s[40:43],s55 offen offset:128  lds
s_add_u32 m0,s57,107392
s_nop 0
buffer_load_dwordx4 v19,s[40:43],s55 offen offset:128  lds
s_add_u32 m0,s57,114432
s_nop 0
buffer_load_dwordx4 v18,s[40:43],s55 offen offset:256  lds
s_add_u32 m0,s57,115456
s_nop 0
buffer_load_dwordx4 v19,s[40:43],s55 offen offset:256  lds
s_waitcnt vmcnt(24) lgkmcnt(0)
s_barrier
s_add_u32 s55,s50,2293760
s_add_u32 m0,s57,122880
s_nop 0
buffer_load_dwordx4 v18,s[40:43],s55 offen offset:0  lds
s_add_u32 m0,s57,123904
s_nop 0
buffer_load_dwordx4 v19,s[40:43],s55 offen offset:0  lds
s_add_u32 m0,s57,130944
s_nop 0
buffer_load_dwordx4 v18,s[40:43],s55 offen offset:128  lds
s_add_u32 m0,s57,131968
s_nop 0
buffer_load_dwordx4 v19,s[40:43],s55 offen offset:128  lds
s_add_u32 m0,s57,139008
s_nop 0
buffer_load_dwordx4 v18,s[40:43],s55 offen offset:256  lds
s_add_u32 m0,s57,140032
s_nop 0
buffer_load_dwordx4 v19,s[40:43],s55 offen offset:256  lds
v_add_u32 v26,0,v20
v_add_u32 v27,0,v21
v_add_u32 v28,0,v22
T a[0:1],26,0
T a[2:3],26,512
T a[4:5],27,0
T a[6:7],27,512
T a[8:9],28,0
T a[10:11],28,512
T a[12:13],26,4096
T a[14:15],26,4608
T a[16:17],27,4096
T a[18:19],27,4608
T a[20:21],28,4096
T a[22:23],28,4608
s_waitcnt vmcnt(24) lgkmcnt(0)
s_barrier
s_add_u32 s55,s50,2752512
s_add_u32 m0,s57,0
s_nop 0
buffer_load_dwordx4 v18,s[40:43],s55 offen offset:0  lds
s_add_u32 m0,s57,1024
s_nop 0
buffer_load_dwordx4 v19,s[40:43],s55 offen offset:0  lds
s_add_u32 m0,s57,8064
s_nop 0
buffer_load_dwordx4 v18,s[40:43],s55 offen offset:128  lds
s_add_u32 m0,s57,9088
s_nop 0
buffer_load_dwordx4 v19,s[40:43],s55 offen offset:128  lds
s_add_u32 m0,s57,16128
s_nop 0
buffer_load_dwordx4 v18,s[40:43],s55 offen offset:256  lds
s_add_u32 m0,s57,17152
s_nop 0
buffer_load_dwordx4 v19,s[40:43],s55 offen offset:256  lds
v_add_u32 v26,24576,v20
v_add_u32 v27,24576,v21
v_add_u32 v28,24576,v22
T a[24:25],26,0
T a[26:27],26,512
T a[28:29],27,0
T a[30:31],27,512
T a[32:33],28,0
T a[34:35],28,512
T a[36:37],26,4096
T a[38:39],26,4608
T a[40:41],27,4096
T a[42:43],27,4608
T a[44:45],28,4096
T a[46:47],28,4608
s_waitcnt vmcnt(24) lgkmcnt(0)
s_barrier
s_add_u32 s55,s50,3211264
s_add_u32 m0,s57,24576
s_nop 0
buffer_load_dwordx4 v18,s[40:43],s55 offen offset:0  lds
s_add_u32 m0,s57,25600
s_nop 0
buffer_load_dwordx4 v19,s[40:43],s55 offen offset:0  lds
s_add_u32 m0,s57,32640
s_nop 0
buffer_load_dwordx4 v18,s[40:43],s55 offen offset:128  lds
s_add_u32 m0,s57,33664
s_nop 0
buffer_load_dwordx4 v19,s[40:43],s55 offen offset:128  lds
s_add_u32 m0,s57,40704
s_nop 0
buffer_load_dwordx4 v18,s[40:43],s55 offen offset:256  lds
s_add_u32 m0,s57,41728
s_nop 0
buffer_load_dwordx4 v19,s[40:43],s55 offen offset:256  lds
v_add_u32 v26,49152,v20
v_add_u32 v27,49152,v21
v_add_u32 v28,49152,v22
T a[48:49],26,0
T a[50:51],26,512
T a[52:53],27,0
T a[54:55],27,512
T a[56:57],28,0
T a[58:59],28,512
T a[60:61],26,4096
T a[62:63],26,4608
T a[64:65],27,4096
T a[66:67],27,4608
T a[68:69],28,4096
T a[70:71],28,4608
s_waitcnt vmcnt(24) lgkmcnt(0)
s_barrier
s_add_u32 s55,s50,3670016
s_add_u32 m0,s57,49152
s_nop 0
buffer_load_dwordx4 v18,s[40:43],s55 offen offset:0  lds
s_add_u32 m0,s57,50176
s_nop 0
buffer_load_dwordx4 v19,s[40:43],s55 offen offset:0  lds
s_add_u32 m0,s57,57216
s_nop 0
buffer_load_dwordx4 v18,s[40:43],s55 offen offset:128  lds
s_add_u32 m0,s57,58240
s_nop 0
buffer_load_dwordx4 v19,s[40:43],s55 offen offset:128  lds
s_add_u32 m0,s57,65280
s_nop 0
buffer_load_dwordx4 v18,s[40:43],s55 offen offset:256  lds
s_add_u32 m0,s57,66304
s_nop 0
buffer_load_dwordx4 v19,s[40:43],s55 offen offset:256  lds
v_add_u32 v26,73728,v20
v_add_u32 v27,73728,v21
v_add_u32 v28,73728,v22
T a[72:73],26,0
T a[74:75],26,512
T a[76:77],27,0
T a[78:79],27,512
T a[80:81],28,0
T a[82:83],28,512
T a[84:85],26,4096
T a[86:87],26,4608
T a[88:89],27,4096
T a[90:91],27,4608
T a[92:93],28,4096
T a[94:95],28,4608
s_waitcnt vmcnt(24) lgkmcnt(0)
s_barrier
s_add_u32 s55,s50,4128768
s_add_u32 m0,s57,73728
s_nop 0
buffer_load_dwordx4 v18,s[40:43],s55 offen offset:0  lds
s_add_u32 m0,s57,74752
s_nop 0
buffer_load_dwordx4 v19,s[40:43],s55 offen offset:0  lds
s_add_u32 m0,s57,81792
s_nop 0
buffer_load_dwordx4 v18,s[40:43],s55 offen offset:128  lds
s_add_u32 m0,s57,82816
s_nop 0
buffer_load_dwordx4 v19,s[40:43],s55 offen offset:128  lds
s_add_u32 m0,s57,89856
s_nop 0
buffer_load_dwordx4 v18,s[40:43],s55 offen offset:256  lds
s_add_u32 m0,s57,90880
s_nop 0
buffer_load_dwordx4 v19,s[40:43],s55 offen offset:256  lds
v_add_u32 v26,98304,v20
v_add_u32 v27,98304,v21
v_add_u32 v28,98304,v22
T a[96:97],26,0
T a[98:99],26,512
T a[100:101],27,0
T a[102:103],27,512
T a[104:105],28,0
T a[106:107],28,512
T a[108:109],26,4096
T a[110:111],26,4608
T a[112:113],27,4096
T a[114:115],27,4608
T a[116:117],28,4096
T a[118:119],28,4608
s_waitcnt vmcnt(24) lgkmcnt(0)
s_barrier
s_add_u32 s55,s50,4587520
s_add_u32 m0,s57,98304
s_nop 0
buffer_load_dwordx4 v18,s[40:43],s55 offen offset:0  lds
s_add_u32 m0,s57,99328
s_nop 0
buffer_load_dwordx4 v19,s[40:43],s55 offen offset:0  lds
s_add_u32 m0,s57,106368
s_nop 0
buffer_load_dwordx4 v18,s[40:43],s55 offen offset:128  lds
s_add_u32 m0,s57,107392
s_nop 0
buffer_load_dwordx4 v19,s[40:43],s55 offen offset:128  lds
s_add_u32 m0,s57,114432
s_nop 0
buffer_load_dwordx4 v18,s[40:43],s55 offen offset:256  lds
s_add_u32 m0,s57,115456
s_nop 0
buffer_load_dwordx4 v19,s[40:43],s55 offen offset:256  lds
v_add_u32 v26,122880,v20
v_add_u32 v27,122880,v21
v_add_u32 v28,122880,v22
T a[120:121],26,0
T a[122:123],26,512
T a[124:125],27,0
T a[126:127],27,512
T a[128:129],28,0
T a[130:131],28,512
T a[132:133],26,4096
T a[134:135],26,4608
T a[136:137],27,4096
T a[138:139],27,4608
T a[140:141],28,4096
T a[142:143],28,4608
s_waitcnt vmcnt(24) lgkmcnt(0)
s_barrier
s_add_u32 s55,s50,5046272
s_add_u32 m0,s57,122880
s_nop 0
buffer_load_dwordx4 v18,s[40:43],s55 offen offset:0  lds
s_add_u32 m0,s57,123904
s_nop 0
buffer_load_dwordx4 v19,s[40:43],s55 offen offset:0  lds
s_add_u32 m0,s57,130944
s_nop 0
buffer_load_dwordx4 v18,s[40:43],s55 offen offset:128  lds
s_add_u32 m0,s57,131968
s_nop 0
buffer_load_dwordx4 v19,s[40:43],s55 offen offset:128  lds
s_add_u32 m0,s57,139008
s_nop 0
buffer_load_dwordx4 v18,s[40:43],s55 offen offset:256  lds
s_add_u32 m0,s57,140032
s_nop 0
buffer_load_dwordx4 v19,s[40:43],s55 offen offset:256  lds
v_add_u32 v26,0,v20
v_add_u32 v27,0,v21
v_add_u32 v28,0,v22
T a[144:145],26,0
T a[146:147],26,512
T a[148:149],27,0
T a[150:151],27,512
T a[152:153],28,0
T a[154:155],28,512
T a[156:157],26,4096
T a[158:159],26,4608
T a[160:161],27,4096
T a[162:163],27,4608
T a[164:165],28,4096
T a[166:167],28,4608
s_waitcnt vmcnt(24) lgkmcnt(0)
s_barrier
s_add_u32 s55,s50,5505024
s_add_u32 m0,s57,0
s_nop 0
buffer_load_dwordx4 v18,s[40:43],s55 offen offset:0  lds
s_add_u32 m0,s57,1024
s_nop 0
buffer_load_dwordx4 v19,s[40:43],s55 offen offset:0  lds
s_add_u32 m0,s57,8064
s_nop 0
buffer_load_dwordx4 v18,s[40:43],s55 offen offset:128  lds
s_add_u32 m0,s57,9088
s_nop 0
buffer_load_dwordx4 v19,s[40:43],s55 offen offset:128  lds
s_add_u32 m0,s57,16128
s_nop 0
buffer_load_dwordx4 v18,s[40:43],s55 offen offset:256  lds
s_add_u32 m0,s57,17152
s_nop 0
buffer_load_dwordx4 v19,s[40:43],s55 offen offset:256  lds
v_add_u32 v26,24576,v20
v_add_u32 v27,24576,v21
v_add_u32 v28,24576,v22
T a[168:169],26,0
T a[170:171],26,512
T a[172:173],27,0
T a[174:175],27,512
T a[176:177],28,0
T a[178:179],28,512
T a[180:181],26,4096
T a[182:183],26,4608
T a[184:185],27,4096
T a[186:187],27,4608
T a[188:189],28,4096
T a[190:191],28,4608
s_waitcnt vmcnt(24) lgkmcnt(0)
s_barrier
s_add_u32 s55,s50,5963776
s_add_u32 m0,s57,24576
s_nop 0
buffer_load_dwordx4 v18,s[40:43],s55 offen offset:0  lds
s_add_u32 m0,s57,25600
s_nop 0
buffer_load_dwordx4 v19,s[40:43],s55 offen offset:0  lds
s_add_u32 m0,s57,32640
s_nop 0
buffer_load_dwordx4 v18,s[40:43],s55 offen offset:128  lds
s_add_u32 m0,s57,33664
s_nop 0
buffer_load_dwordx4 v19,s[40:43],s55 offen offset:128  lds
s_add_u32 m0,s57,40704
s_nop 0
buffer_load_dwordx4 v18,s[40:43],s55 offen offset:256  lds
s_add_u32 m0,s57,41728
s_nop 0
buffer_load_dwordx4 v19,s[40:43],s55 offen offset:256  lds
v_add_u32 v26,49152,v20
v_add_u32 v27,49152,v21
v_add_u32 v28,49152,v22
T a[192:193],26,0
T a[194:195],26,512
T a[196:197],27,0
T a[198:199],27,512
T a[200:201],28,0
T a[202:203],28,512
T a[204:205],26,4096
T a[206:207],26,4608
T a[208:209],27,4096
T a[210:211],27,4608
T a[212:213],28,4096
T a[214:215],28,4608
s_waitcnt vmcnt(24) lgkmcnt(0)
s_barrier
s_add_u32 s55,s50,6422528
s_add_u32 m0,s57,49152
s_nop 0
buffer_load_dwordx4 v18,s[40:43],s55 offen offset:0  lds
s_add_u32 m0,s57,50176
s_nop 0
buffer_load_dwordx4 v19,s[40:43],s55 offen offset:0  lds
s_add_u32 m0,s57,57216
s_nop 0
buffer_load_dwordx4 v18,s[40:43],s55 offen offset:128  lds
s_add_u32 m0,s57,58240
s_nop 0
buffer_load_dwordx4 v19,s[40:43],s55 offen offset:128  lds
s_add_u32 m0,s57,65280
s_nop 0
buffer_load_dwordx4 v18,s[40:43],s55 offen offset:256  lds
s_add_u32 m0,s57,66304
s_nop 0
buffer_load_dwordx4 v19,s[40:43],s55 offen offset:256  lds
v_add_u32 v26,73728,v20
v_add_u32 v27,73728,v21
v_add_u32 v28,73728,v22
T a[216:217],26,0
T a[218:219],26,512
T a[220:221],27,0
)"
R"(T a[222:223],27,512
T a[224:225],28,0
T a[226:227],28,512
T a[228:229],26,4096
T a[230:231],26,4608
T a[232:233],27,4096
T a[234:235],27,4608
T a[236:237],28,4096
T a[238:239],28,4608
s_waitcnt vmcnt(24) lgkmcnt(0)
s_barrier
s_add_u32 s55,s50,6881280
s_add_u32 m0,s57,73728
s_nop 0
buffer_load_dwordx4 v18,s[40:43],s55 offen offset:0  lds
s_add_u32 m0,s57,74752
s_nop 0
buffer_load_dwordx4 v19,s[40:43],s55 offen offset:0  lds
s_add_u32 m0,s57,81792
s_nop 0
buffer_load_dwordx4 v18,s[40:43],s55 offen offset:128  lds
s_add_u32 m0,s57,82816
s_nop 0
buffer_load_dwordx4 v19,s[40:43],s55 offen offset:128  lds
s_add_u32 m0,s57,89856
s_nop 0
buffer_load_dwordx4 v18,s[40:43],s55 offen offset:256  lds
s_add_u32 m0,s57,90880
s_nop 0
buffer_load_dwordx4 v19,s[40:43],s55 offen offset:256  lds
v_add_u32 v26,98304,v20
v_add_u32 v27,98304,v21
v_add_u32 v28,98304,v22
T a[240:241],26,0
T a[242:243],26,512
T a[244:245],27,0
T a[246:247],27,512
T a[248:249],28,0
T a[250:251],28,512
T a[252:253],26,4096
T a[254:255],26,4608
T v[128:129],27,4096
T v[130:131],27,4608
T v[132:133],28,4096
T v[134:135],28,4608
s_waitcnt vmcnt(24) lgkmcnt(0)
s_barrier
v_add_u32 v26,122880,v20
v_add_u32 v27,122880,v21
v_add_u32 v28,122880,v22
T v[136:137],26,0
T v[138:139],26,512
T v[140:141],27,0
T v[142:143],27,512
T v[144:145],28,0
T v[146:147],28,512
T v[148:149],26,4096
T v[150:151],26,4608
T v[152:153],27,4096
T v[154:155],27,4608
T v[156:157],28,4096
T v[158:159],28,4608
s_waitcnt vmcnt(18) lgkmcnt(0)
s_barrier
v_add_u32 v26,0,v20
v_add_u32 v27,0,v21
v_add_u32 v28,0,v22
T v[160:161],26,0
T v[162:163],26,512
T v[164:165],27,0
T v[166:167],27,512
T v[168:169],28,0
T v[170:171],28,512
T v[172:173],26,4096
T v[174:175],26,4608
T v[176:177],27,4096
T v[178:179],27,4608
T v[180:181],28,4096
T v[182:183],28,4608
s_waitcnt vmcnt(12) lgkmcnt(0)
s_barrier
v_add_u32 v26,24576,v20
v_add_u32 v27,24576,v21
v_add_u32 v28,24576,v22
T v[184:185],26,0
T v[186:187],26,512
T v[188:189],27,0
T v[190:191],27,512
T v[192:193],28,0
T v[194:195],28,512
T v[196:197],26,4096
T v[198:199],26,4608
T v[200:201],27,4096
T v[202:203],27,4608
T v[204:205],28,4096
T v[206:207],28,4608
s_waitcnt vmcnt(6) lgkmcnt(0)
s_barrier
v_add_u32 v26,49152,v20
v_add_u32 v27,49152,v21
v_add_u32 v28,49152,v22
T v[208:209],26,0
T v[210:211],26,512
T v[212:213],27,0
T v[214:215],27,512
T v[216:217],28,0
T v[218:219],28,512
T v[220:221],26,4096
T v[222:223],26,4608
T v[224:225],27,4096
T v[226:227],27,4608
T v[228:229],28,4096
T v[230:231],28,4608
s_waitcnt vmcnt(0) lgkmcnt(0)
s_barrier
v_add_u32 v26,73728,v20
v_add_u32 v27,73728,v21
v_add_u32 v28,73728,v22
T v[232:233],26,0
T v[234:235],26,512
T v[236:237],27,0
T v[238:239],27,512
T v[240:241],28,0
T v[242:243],28,512
T v[244:245],26,4096
T v[246:247],26,4608
T v[248:249],27,4096
T v[250:251],27,4608
T v[252:253],28,4096
T v[254:255],28,4608
L 0
s_barrier
v_mov_b32 v42,0x3f803f80
v_mov_b32 v43,0x3f803f80
v_mov_b32 v44,0x3f803f80
v_mov_b32 v45,0x3f803f80
s_nop 1
v_mfma_f32_16x16x32_bf16 v[82:85],a[0:3],v[42:45],0
v_mfma_f32_16x16x32_bf16 v[86:89],a[4:7],v[42:45],0
v_mfma_f32_16x16x32_bf16 v[90:93],a[8:11],v[42:45],0
M 82,a[12:15],42
M 86,a[16:19],42
M 90,a[20:23],42
M 82,a[24:27],42
M 86,a[28:31],42
M 90,a[32:35],42
M 82,a[36:39],42
M 86,a[40:43],42
M 90,a[44:47],42
M 82,a[48:51],42
M 86,a[52:55],42
M 90,a[56:59],42
M 82,a[60:63],42
M 86,a[64:67],42
M 90,a[68:71],42
M 82,a[72:75],42
M 86,a[76:79],42
M 90,a[80:83],42
M 82,a[84:87],42
M 86,a[88:91],42
M 90,a[92:95],42
M 82,a[96:99],42
M 86,a[100:103],42
M 90,a[104:107],42
M 82,a[108:111],42
M 86,a[112:115],42
M 90,a[116:119],42
M 82,a[120:123],42
M 86,a[124:127],42
M 90,a[128:131],42
M 82,a[132:135],42
M 86,a[136:139],42
M 90,a[140:143],42
M 82,a[144:147],42
M 86,a[148:151],42
M 90,a[152:155],42
M 82,a[156:159],42
M 86,a[160:163],42
M 90,a[164:167],42
M 82,a[168:171],42
M 86,a[172:175],42
M 90,a[176:179],42
M 82,a[180:183],42
M 86,a[184:187],42
M 90,a[188:191],42
M 82,a[192:195],42
M 86,a[196:199],42
M 90,a[200:203],42
M 82,a[204:207],42
M 86,a[208:211],42
M 90,a[212:215],42
M 82,a[216:219],42
M 86,a[220:223],42
M 90,a[224:227],42
M 82,a[228:231],42
M 86,a[232:235],42
M 90,a[236:239],42
M 82,a[240:243],42
M 86,a[244:247],42
M 90,a[248:251],42
M 82,a[252:255],42
M 86,v[128:131],42
M 90,v[132:135],42
M 82,v[136:139],42
M 86,v[140:143],42
M 90,v[144:147],42
M 82,v[148:151],42
M 86,v[152:155],42
M 90,v[156:159],42
M 82,v[160:163],42
M 86,v[164:167],42
M 90,v[168:171],42
M 82,v[172:175],42
M 86,v[176:179],42
M 90,v[180:183],42
M 82,v[184:187],42
M 86,v[188:191],42
M 90,v[192:195],42
M 82,v[196:199],42
M 86,v[200:203],42
M 90,v[204:207],42
M 82,v[208:211],42
M 86,v[212:215],42
M 90,v[216:219],42
M 82,v[220:223],42
M 86,v[224:227],42
M 90,v[228:231],42
M 82,v[232:235],42
M 86,v[236:239],42
M 90,v[240:243],42
M 82,v[244:247],42
M 86,v[248:251],42
M 90,v[252:255],42
v_mov_b32 v4,s53
s_mov_b32 s82,0
s_mov_b32 s80,0
s_mov_b32 s54,s45
s_mul_i32 s57,s44,4096
s_mul_i32 s55,s45,32768
s_add_u32 s55,s55,s57
s_add_u32 s55,s55,31457280
s_mul_i32 s57,s44,4096
s_mul_i32 s60,s46,32768
s_mul_i32 s61,s46,1024
s_mul_i32 s62,s46,114688
s_mul_i32 s63,s46,2560
s_mul_i32 s64,s46,1024
s_lshl_b32 s59,s44,8
s_add_u32 s59,s59,131072
s_mul_i32 s58,s45,1024
s_add_u32 s58,s58,109613056
s_mul_i32 s65,s45,114688
s_add_u32 s65,s65,s47
s_mul_i32 s68,s45,2560
s_add_u32 s68,s68,s48
s_mul_i32 s73,s45,1024
s_add_u32 s73,s73,107479040
s_mul_i32 s74,s45,32768
s_add_u32 s74,s74,s49
s_mul_i32 s75,s45,1024
s_add_u32 s75,s75,107479040
s_mov_b32 s66,0x7fff0000
s_mov_b32 s67,0x7fff0000
s_mov_b32 s69,0x7fff0000
s_mov_b32 s76,0x7fff0000
s_mov_b32 s77,0x7fff0000
s_mov_b32 s70,0x7fff0000
s_mov_b32 s71,0x7fff0000
s_mov_b32 s72,0x7fff0000
v_mov_b32 v1,v0
v_add_u32 v2,16384,v0
s_mov_b32 s84,16384
s_mov_b32 s86,0x3a800000
s_mov_b32 s87,0x3727c5ac
s_mov_b32 s88,0xc0135761
s_mov_b32 s89,0x3d372713
s_lshr_b32 s85,s44,1
s_mul_i32 s85,s85,127
v_lshlrev_b32 v8,22,v0
v_and_b32 v8,0x80000000,v8
v_or_b32 v8,v8,v5
v_add_u32 v2,32768,v0
s_lshl_b32 s84,s57,1
s_add_u32 s84,s84,32768
v_and_b32 v94,240,v0
v_cmp_eq_u32 vcc,0,v94
s_nop 1
v_cndmask_b32 v17,0,1.0,vcc
s_getpc_b64 s[40:41]
Lg3_pc_%=:
s_lshl_b32 s78,s44,13
v_lshlrev_b32 v94,3,v0
v_add_u32 v94,s78,v94
v_min_u32 v94,Lcode_g3_e-Lg3_pc_%=-4,v94
s_mov_b32 m0,163584
s_nop 0
global_load_lds_dword v94,s[40:41]
s_add_u32 m0,s59,4096
s_nop 0
buffer_load_dword v8,s[36:39],s58 offen offset:0 sc1 lds
buffer_store_dword v4,v3,s[36:39],s71 offen offset:0 sc0 sc1
s_add_u32 m0,s59,5120
s_nop 0
buffer_load_dword v8,s[36:39],s58 offen offset:0 sc1 lds
buffer_store_dword v4,v3,s[36:39],s71 offen offset:0 sc0 sc1
s_mov_b32 s83,0
Lg3_poll_%=:
s_add_u32 s78,s83,2
s_and_b32 s78,s78,3
s_lshl_b32 s78,s78,10
s_add_u32 s78,s78,4096
s_add_u32 m0,s59,s78
s_nop 0
buffer_load_dword v8,s[36:39],s58 offen offset:0 sc1 lds
s_and_b32 s78,s83,3
s_lshl_b32 s78,s78,10
v_add_u32 v94,s78,v6
s_waitcnt vmcnt(4)
s_barrier
ds_read_b32 v7,v94 offset:4096
buffer_store_dword v4,v3,s[36:39],s70 offen offset:0 sc0 sc1
s_mov_b32 s70,s71
s_mov_b32 s71,s72
s_mov_b32 s72,0x7fff0000
s_add_u32 s83,s83,1
s_add_u32 s82,s82,1
L 0
v_cmp_eq_u32 vcc,s53,v7
s_nop 0
s_cmp_eq_u32 vcc_lo,-1
s_cbranch_scc1 Lg3_go_%=
s_cmp_lt_u32 s82,20000
s_cbranch_scc1 Lg3_poll_%=
Lg3_go_%=:
buffer_load_dword v114,v11,s[36:39],s73 offen offset:0 sc1
s_add_u32 s73,s73,s64
s_add_u32 m0,s57,0
s_add_u32 s56,s55,0
D 0,56,0
D 0,56,1024
D 0,56,2048
D 0,56,3072
s_add_u32 s58,s58,s61
s_add_u32 m0,s59,2048
s_nop 0
buffer_load_dword v8,s[36:39],s58 offen offset:0 sc1 lds
s_add_u32 m0,s57,16384
s_add_u32 s56,s55,16384
D 0,56,0
D 0,56,1024
D 0,56,2048
D 0,56,3072
s_add_u32 s55,s55,s60
s_sub_u32 s57,s84,s57
s_add_u32 s54,s54,s46
s_waitcnt vmcnt(4)
s_barrier
buffer_store_dword v4,v3,s[36:39],s70 offen offset:0 sc0 sc1
s_min_u32 s71,s71,s72
buffer_store_dword v4,v3,s[36:39],s71 offen offset:0 sc0 sc1
s_mov_b32 s70,0x7fff0000
s_mov_b32 s71,0x7fff0000
s_mov_b32 s72,0x7fff0000
s_cmp_eq_u32 s80,1
buffer_store_dword v4,v3,s[36:39],s71 offen offset:0 sc0 sc1
s_cbranch_scc1 Lg3_st1_%=
s_branch Lg3_st0_%=
Lg3_gox_%=:
s_add_u32 m0,s59,2048
s_nop 0
buffer_load_dword v8,s[36:39],s58 offen offset:0 sc1 lds
s_waitcnt vmcnt(0)
s_barrier
buffer_store_dword v4,v3,s[36:39],s70 offen offset:0 sc0 sc1
s_min_u32 s71,s71,s72
buffer_store_dword v4,v3,s[36:39],s71 offen offset:0 sc0 sc1
s_mov_b32 s70,0x7fff0000
s_mov_b32 s71,0x7fff0000
s_mov_b32 s72,0x7fff0000
s_cmp_eq_u32 s80,1
s_cbranch_scc1 Lg3_st1_%=
Lg3_st0_%=:
ds_read_b32 v7,v6 offset:2048
R 42,1,0
R 46,1,1024
R 50,1,2048
R 54,1,3072
R 58,1,4096
L 5
v_cmp_eq_u32 vcc,s53,v7
L 3
s_cmp_eq_u32 vcc_lo,-1
s_cbranch_scc1 Lg3_F0_%=
s_branch Lg3_L0_%=
Lg3_st1_%=:
ds_read_b32 v7,v6 offset:2048
R 42,2,0
R 46,2,1024
R 50,2,2048
R 54,2,3072
R 58,2,4096
L 5
v_cmp_eq_u32 vcc,s53,v7
L 3
s_cmp_eq_u32 vcc_lo,-1
s_cbranch_scc1 Lg3_F1_%=
s_branch Lg3_L1_%=
Lg3_F0_%=:
v_mfma_f32_16x16x32_bf16 v[26:29],a[8:11],v[42:45],0
s_add_u32 m0,s57,0
R 62,1,5120
v_add_f32_dpp v115,v114,v114 quad_perm:[1,0,3,2] row_mask:0xf bank_mask:0xf
M 26,a[20:23],46
buffer_load_dword v114,v11,s[36:39],s73 offen offset:0 sc1
v_mfma_f32_16x16x32_bf16 v[22:25],a[4:7],v[42:45],0
s_add_u32 s56,s55,0
v_add_f32_dpp v115,v115,v115 quad_perm:[2,3,0,1] row_mask:0xf bank_mask:0xf
v_fma_f32 v30,-v78,v82,v30
M 22,a[16:19],46
s_add_u32 s58,s58,s61
R 66,1,6144
v_fma_f32 v31,-v78,v83,v31
v_mfma_f32_16x16x32_bf16 v[18:21],a[0:3],v[42:45],0
D 0,56,0
M 18,a[12:15],46
L 3
v_add_f32_dpp v118,v115,v115 row_half_mirror row_mask:0xf bank_mask:0xf
v_fma_f32 v32,-v78,v84,v32
M 18,a[24:27],50
s_mov_b32 s66,s65
R 70,1,7168
v_fma_f32 v33,-v78,v85,v33
M 18,a[36:39],54
s_add_u32 s65,s65,s62
v_mov_b32_dpp v115,v118 row_ror:8 row_mask:0xf bank_mask:0xf
v_fma_f32 v115,-v118,v118,v115
M 22,a[28:31],50
s_add_u32 s54,s54,s46
v_max_f32 v115,s87,v115
v_mul_f32 v118,v17,v118
M 22,a[40:43],54
s_add_u32 s73,s73,s64
R 42,1,8192
v_mul_f32 v94,v30,v30
M 26,a[32:35],50
v_rsq_f32 v116,v115
M 26,a[44:47],54
L 3
D 0,56,1024
M 26,a[56:59],58
R 46,1,9216
v_fma_f32 v115,v15,v115,v16
M 26,a[68:71],62
v_mul_f32 v119,v116,v116
v_fma_f32 v115,v14,v119,v115
M 22,a[52:55],58
v_fma_f32 v118,v116,v115,v118
v_mul_f32 v95,v31,v31
M 22,a[64:67],62
R 50,1,10240
v_mul_f32 v96,v32,v32
M 18,a[48:51],58
ds_write_b32 v12,v118 offset:0
M 18,a[60:63],62
L 4
v_mul_f32 v97,v33,v33
v_fma_f32 v94,v80,v94,v79
M 18,a[72:75],66
R 54,1,11264
v_fma_f32 v95,v80,v95,v79
M 18,a[84:87],70
v_fma_f32 v96,v80,v96,v79
v_fma_f32 v97,v80,v97,v79
M 22,a[76:79],66
v_mul_f32 v94,v30,v94
v_mul_f32 v95,v31,v95
M 22,a[88:91],70
R 58,1,12288
v_mul_f32 v96,v32,v96
M 26,a[80:83],66
D 0,56,2048
M 26,a[92:95],70
L 4
v_exp_f32 v94,v94
M 26,a[104:107],42
R 62,1,13312
v_mul_f32 v97,v33,v97
M 26,a[116:119],46
v_exp_f32 v95,v95
M 22,a[100:103],42
v_exp_f32 v96,v96
M 22,a[112:115],46
R 66,1,14336
v_fma_f32 v94,v94,v81,v81
M 18,a[96:99],42
v_exp_f32 v97,v97
M 18,a[108:111],46
L 3
v_rcp_f32 v94,v94
M 18,a[120:123],50
)"
R"(R 70,1,15360
v_fma_f32 v95,v95,v81,v81
M 18,a[132:135],54
s_waitcnt vmcnt(7)
D 0,56,3072
M 22,a[124:127],50
s_barrier
v_rcp_f32 v95,v95
M 22,a[136:139],54
s_add_u32 m0,s59,0
R 42,1,16384
v_fma_f32 v96,v96,v81,v81
M 26,a[128:131],50
s_add_u32 s56,s55,16384
v_rcp_f32 v96,v96
M 26,a[140:143],54
L 3
v_fma_f32 v97,v97,v81,v81
v_mul_f32 v30,v30,v94
M 26,a[152:155],58
s_add_u32 s55,s55,s60
R 46,1,17408
v_mul_f32 v31,v31,v95
M 26,a[164:167],62
v_rcp_f32 v97,v97
M 22,a[148:151],58
v_mul_f32 v32,v32,v96
v_mul_f32 v33,v33,v97
M 22,a[160:163],62
R 50,1,18432
v_cvt_pk_bf16_f32 v106,v30,v31
M 18,a[144:147],58
buffer_load_dword v8,s[36:39],s58 offen offset:0 sc1 lds
M 18,a[156:159],62
L 3
v_cvt_pk_bf16_f32 v107,v32,v33
v_fma_f32 v34,-v78,v86,v34
M 18,a[168:171],66
s_add_u32 m0,s57,16384
R 54,1,19456
v_fma_f32 v35,-v78,v87,v35
M 18,a[180:183],70
s_sub_u32 s57,s84,s57
v_fma_f32 v36,-v78,v88,v36
v_fma_f32 v37,-v78,v89,v37
M 22,a[172:175],66
v_mul_f32 v98,v34,v34
v_mul_f32 v99,v35,v35
M 22,a[184:187],70
R 58,1,20480
v_mul_f32 v100,v36,v36
M 26,a[176:179],66
D 0,56,0
M 26,a[188:191],70
L 3
v_mul_f32 v101,v37,v37
v_fma_f32 v98,v80,v98,v79
M 26,a[200:203],42
R 62,1,21504
v_fma_f32 v99,v80,v99,v79
M 26,a[212:215],46
v_fma_f32 v100,v80,v100,v79
v_fma_f32 v101,v80,v101,v79
M 22,a[196:199],42
v_mul_f32 v98,v34,v98
v_mul_f32 v99,v35,v99
M 22,a[208:211],46
R 66,1,22528
v_mul_f32 v100,v36,v100
M 18,a[192:195],42
v_exp_f32 v98,v98
M 18,a[204:207],46
L 3
v_exp_f32 v99,v99
M 18,a[216:219],50
R 70,1,23552
v_mul_f32 v101,v37,v101
M 18,a[228:231],54
D 0,56,1024
M 22,a[220:223],50
v_exp_f32 v100,v100
M 22,a[232:235],54
R 42,1,24576
v_fma_f32 v98,v98,v81,v81
M 26,a[224:227],50
v_exp_f32 v101,v101
M 26,a[236:239],54
L 3
v_rcp_f32 v98,v98
M 26,a[248:251],58
R 46,1,25600
v_fma_f32 v99,v99,v81,v81
M 26,v[132:135],62
v_rcp_f32 v99,v99
M 22,a[244:247],58
v_fma_f32 v100,v100,v81,v81
v_fma_f32 v101,v101,v81,v81
M 22,v[128:131],62
R 50,1,26624
v_mul_f32 v34,v34,v98
M 18,a[240:243],58
v_rcp_f32 v100,v100
M 18,a[252:255],62
L 3
v_rcp_f32 v101,v101
M 18,v[136:139],66
R 54,1,27648
v_mul_f32 v35,v35,v99
M 18,v[148:151],70
D 0,56,2048
M 22,v[140:143],66
v_mul_f32 v36,v36,v100
v_mul_f32 v37,v37,v101
M 22,v[152:155],70
R 58,1,28672
v_cvt_pk_bf16_f32 v108,v34,v35
M 26,v[144:147],66
v_cvt_pk_bf16_f32 v109,v36,v37
v_fma_f32 v38,-v78,v90,v38
M 26,v[156:159],70
L 3
v_fma_f32 v39,-v78,v91,v39
v_fma_f32 v40,-v78,v92,v40
M 26,v[168:171],42
R 62,1,29696
v_fma_f32 v41,-v78,v93,v41
M 26,v[180:183],46
v_mul_f32 v102,v38,v38
v_mul_f32 v103,v39,v39
M 22,v[164:167],42
v_mul_f32 v104,v40,v40
v_mul_f32 v105,v41,v41
M 22,v[176:179],46
R 66,1,30720
v_fma_f32 v102,v80,v102,v79
M 18,v[160:163],42
v_fma_f32 v103,v80,v103,v79
v_fma_f32 v104,v80,v104,v79
M 18,v[172:175],46
L 3
D 0,56,3072
M 18,v[184:187],50
s_nop 0
R 70,1,31744
v_fma_f32 v105,v80,v105,v79
M 18,v[196:199],54
s_waitcnt vmcnt(4)
v_mul_f32 v102,v38,v102
v_mul_f32 v103,v39,v103
M 22,v[188:191],50
s_barrier
buffer_store_dword v4,v3,s[36:39],s70 offen offset:0 sc0 sc1
M 22,v[200:203],54
s_mov_b32 s70,s69
R 42,2,0
ds_read_b32 v7,v6 offset:0
M 26,v[192:195],50
s_mov_b32 s69,s68
R 74,13,0
v_mul_f32 v104,v40,v104
M 26,v[204:207],54
L 5
v_exp_f32 v102,v102
M 26,v[216:219],58
s_add_u32 s68,s68,s63
R 46,2,1024
v_mul_f32 v105,v41,v105
M 26,v[228:231],62
v_exp_f32 v103,v103
M 22,v[212:215],58
buffer_store_dwordx4 v[106:109],v0,s[36:39],s67 offen offset:0 sc0 sc1
v_fma_f32 v102,v102,v81,v81
M 22,v[224:227],62
L 2
R 50,2,2048
v_fma_f32 v103,v103,v81,v81
M 18,v[208:211],58
v_exp_f32 v104,v104
M 18,v[220:223],62
s_nop 0
v_exp_f32 v105,v105
M 18,v[232:235],66
R 54,2,3072
v_fma_f32 v104,v104,v81,v81
M 18,v[244:247],70
v_fma_f32 v105,v105,v81,v81
v_cmp_eq_u32 vcc,s53,v7
M 22,v[236:239],66
v_rcp_f32 v102,v102
v_rcp_f32 v103,v103
v_rcp_f32 v104,v104
v_rcp_f32 v105,v105
M 22,v[248:251],70
s_cmp_eq_u32 vcc_lo,-1
R 58,2,4096
v_mul_f32 v38,v38,v102
v_mul_f32 v39,v39,v103
v_mul_f32 v40,v40,v104
v_mul_f32 v41,v41,v105
v_cvt_pk_bf16_f32 v110,v38,v39
v_cvt_pk_bf16_f32 v111,v40,v41
M 26,v[240:243],66
L 3
buffer_store_dwordx2 v[110:111],v10,s[36:39],s67 offen offset:0 sc0 sc1
M 26,v[252:255],70
s_cbranch_scc0 Lg3_K1_%=
Lg3_F1_%=:
v_mfma_f32_16x16x32_bf16 v[38:41],a[8:11],v[42:45],0
s_add_u32 m0,s57,0
R 62,2,5120
v_add_f32_dpp v115,v114,v114 quad_perm:[1,0,3,2] row_mask:0xf bank_mask:0xf
M 38,a[20:23],46
buffer_load_dword v114,v11,s[36:39],s73 offen offset:0 sc1
v_mfma_f32_16x16x32_bf16 v[34:37],a[4:7],v[42:45],0
s_add_u32 s56,s55,0
v_add_f32_dpp v115,v115,v115 quad_perm:[2,3,0,1] row_mask:0xf bank_mask:0xf
v_fma_f32 v18,-v74,v82,v18
M 34,a[16:19],46
s_add_u32 s58,s58,s61
R 66,2,6144
v_fma_f32 v19,-v74,v83,v19
v_mfma_f32_16x16x32_bf16 v[30:33],a[0:3],v[42:45],0
D 0,56,0
M 30,a[12:15],46
L 3
v_add_f32_dpp v118,v115,v115 row_half_mirror row_mask:0xf bank_mask:0xf
v_fma_f32 v20,-v74,v84,v20
M 30,a[24:27],50
s_mov_b32 s67,s65
R 70,2,7168
v_fma_f32 v21,-v74,v85,v21
M 30,a[36:39],54
s_add_u32 s65,s65,s62
v_mov_b32_dpp v115,v118 row_ror:8 row_mask:0xf bank_mask:0xf
v_fma_f32 v115,-v118,v118,v115
M 34,a[28:31],50
s_add_u32 s54,s54,s46
v_max_f32 v115,s87,v115
v_mul_f32 v118,v17,v118
M 34,a[40:43],54
s_add_u32 s73,s73,s64
R 42,2,8192
v_mul_f32 v94,v18,v18
M 38,a[32:35],50
v_rsq_f32 v116,v115
M 38,a[44:47],54
L 3
D 0,56,1024
M 38,a[56:59],58
R 46,2,9216
v_fma_f32 v115,v15,v115,v16
M 38,a[68:71],62
v_mul_f32 v119,v116,v116
v_fma_f32 v115,v14,v119,v115
M 34,a[52:55],58
v_fma_f32 v118,v116,v115,v118
v_mul_f32 v95,v19,v19
M 34,a[64:67],62
R 50,2,10240
v_mul_f32 v96,v20,v20
M 30,a[48:51],58
ds_write_b32 v12,v118 offset:2048
M 30,a[60:63],62
L 4
v_mul_f32 v97,v21,v21
v_fma_f32 v94,v76,v94,v75
M 30,a[72:75],66
R 54,2,11264
v_fma_f32 v95,v76,v95,v75
M 30,a[84:87],70
v_fma_f32 v96,v76,v96,v75
v_fma_f32 v97,v76,v97,v75
M 34,a[76:79],66
v_mul_f32 v94,v18,v94
v_mul_f32 v95,v19,v95
M 34,a[88:91],70
R 58,2,12288
v_mul_f32 v96,v20,v96
M 38,a[80:83],66
D 0,56,2048
M 38,a[92:95],70
L 4
v_exp_f32 v94,v94
M 38,a[104:107],42
R 62,2,13312
v_mul_f32 v97,v21,v97
M 38,a[116:119],46
v_exp_f32 v95,v95
M 34,a[100:103],42
v_exp_f32 v96,v96
M 34,a[112:115],46
R 66,2,14336
v_fma_f32 v94,v94,v77,v77
M 30,a[96:99],42
v_exp_f32 v97,v97
M 30,a[108:111],46
L 3
v_rcp_f32 v94,v94
M 30,a[120:123],50
R 70,2,15360
v_fma_f32 v95,v95,v77,v77
M 30,a[132:135],54
s_waitcnt vmcnt(7)
D 0,56,3072
M 34,a[124:127],50
s_barrier
v_rcp_f32 v95,v95
M 34,a[136:139],54
s_add_u32 m0,s59,1024
R 42,2,16384
v_fma_f32 v96,v96,v77,v77
M 38,a[128:131],50
s_add_u32 s56,s55,16384
v_rcp_f32 v96,v96
M 38,a[140:143],54
L 3
v_fma_f32 v97,v97,v77,v77
v_mul_f32 v18,v18,v94
M 38,a[152:155],58
s_add_u32 s55,s55,s60
R 46,2,17408
v_mul_f32 v19,v19,v95
M 38,a[164:167],62
v_rcp_f32 v97,v97
M 34,a[148:151],58
v_mul_f32 v20,v20,v96
v_mul_f32 v21,v21,v97
M 34,a[160:163],62
R 50,2,18432
v_cvt_pk_bf16_f32 v106,v18,v19
M 30,a[144:147],58
buffer_load_dword v8,s[36:39],s58 offen offset:0 sc1 lds
M 30,a[156:159],62
L 3
v_cvt_pk_bf16_f32 v107,v20,v21
v_fma_f32 v22,-v74,v86,v22
M 30,a[168:171],66
s_add_u32 m0,s57,16384
R 54,2,19456
v_fma_f32 v23,-v74,v87,v23
M 30,a[180:183],70
s_sub_u32 s57,s84,s57
v_fma_f32 v24,-v74,v88,v24
v_fma_f32 v25,-v74,v89,v25
M 34,a[172:175],66
v_mul_f32 v98,v22,v22
v_mul_f32 v99,v23,v23
M 34,a[184:187],70
R 58,2,20480
v_mul_f32 v100,v24,v24
M 38,a[176:179],66
D 0,56,0
M 38,a[188:191],70
L 3
v_mul_f32 v101,v25,v25
v_fma_f32 v98,v76,v98,v75
M 38,a[200:203],42
R 62,2,21504
v_fma_f32 v99,v76,v99,v75
M 38,a[212:215],46
v_fma_f32 v100,v76,v100,v75
v_fma_f32 v101,v76,v101,v75
M 34,a[196:199],42
v_mul_f32 v98,v22,v98
v_mul_f32 v99,v23,v99
M 34,a[208:211],46
R 66,2,22528
v_mul_f32 v100,v24,v100
M 30,a[192:195],42
v_exp_f32 v98,v98
M 30,a[204:207],46
L 3
v_exp_f32 v99,v99
M 30,a[216:219],50
R 70,2,23552
v_mul_f32 v101,v25,v101
M 30,a[228:231],54
D 0,56,1024
M 34,a[220:223],50
v_exp_f32 v100,v100
M 34,a[232:235],54
R 42,2,24576
v_fma_f32 v98,v98,v77,v77
M 38,a[224:227],50
v_exp_f32 v101,v101
M 38,a[236:239],54
L 3
v_rcp_f32 v98,v98
M 38,a[248:251],58
R 46,2,25600
v_fma_f32 v99,v99,v77,v77
M 38,v[132:135],62
v_rcp_f32 v99,v99
M 34,a[244:247],58
v_fma_f32 v100,v100,v77,v77
v_fma_f32 v101,v101,v77,v77
M 34,v[128:131],62
R 50,2,26624
v_mul_f32 v22,v22,v98
M 30,a[240:243],58
v_rcp_f32 v100,v100
M 30,a[252:255],62
L 3
v_rcp_f32 v101,v101
M 30,v[136:139],66
R 54,2,27648
v_mul_f32 v23,v23,v99
M 30,v[148:151],70
D 0,56,2048
M 34,v[140:143],66
v_mul_f32 v24,v24,v100
v_mul_f32 v25,v25,v101
M 34,v[152:155],70
R 58,2,28672
v_cvt_pk_bf16_f32 v108,v22,v23
M 38,v[144:147],66
v_cvt_pk_bf16_f32 v109,v24,v25
v_fma_f32 v26,-v74,v90,v26
M 38,v[156:159],70
L 3
v_fma_f32 v27,-v74,v91,v27
v_fma_f32 v28,-v74,v92,v28
M 38,v[168:171],42
R 62,2,29696
v_fma_f32 v29,-v74,v93,v29
M 38,v[180:183],46
v_mul_f32 v102,v26,v26
v_mul_f32 v103,v27,v27
M 34,v[164:167],42
v_mul_f32 v104,v28,v28
v_mul_f32 v105,v29,v29
M 34,v[176:179],46
R 66,2,30720
v_fma_f32 v102,v76,v102,v75
M 30,v[160:163],42
v_fma_f32 v103,v76,v103,v75
v_fma_f32 v104,v76,v104,v75
M 30,v[172:175],46
L 3
D 0,56,3072
M 30,v[184:187],50
s_nop 0
R 70,2,31744
v_fma_f32 v105,v76,v105,v75
M 30,v[196:199],54
s_waitcnt vmcnt(4)
v_mul_f32 v102,v26,v102
v_mul_f32 v103,v27,v103
M 34,v[188:191],50
s_barrier
buffer_store_dword v4,v3,s[36:39],s70 offen offset:0 sc0 sc1
M 34,v[200:203],54
s_mov_b32 s70,s69
R 42,1,0
ds_read_b32 v7,v6 offset:1024
M 38,v[192:195],50
s_mov_b32 s69,s68
R 78,13,2048
v_mul_f32 v104,v28,v104
M 38,v[204:207],54
L 5
v_exp_f32 v102,v102
M 38,v[216:219],58
s_add_u32 s68,s68,s63
R 46,1,1024
v_mul_f32 v105,v29,v105
M 38,v[228:231],62
v_exp_f32 v103,v103
M 34,v[212:215],58
buffer_store_dwordx4 v[106:109],v0,s[36:39],s66 offen offset:0 sc0 sc1
v_fma_f32 v102,v102,v77,v77
M 34,v[224:227],62
L 2
R 50,1,2048
v_fma_f32 v103,v103,v77,v77
M 30,v[208:211],58
v_exp_f32 v104,v104
M 30,v[220:223],62
s_nop 0
v_exp_f32 v105,v105
M 30,v[232:235],66
R 54,1,3072
v_fma_f32 v104,v104,v77,v77
M 30,v[244:247],70
v_fma_f32 v105,v105,v77,v77
v_cmp_eq_u32 vcc,s53,v7
M 34,v[236:239],66
v_rcp_f32 v102,v102
v_rcp_f32 v103,v103
v_rcp_f32 v104,v104
v_rcp_f32 v105,v105
M 34,v[248:251],70
s_cmp_eq_u32 vcc_lo,-1
R 58,1,4096
v_mul_f32 v26,v26,v102
v_mul_f32 v27,v27,v103
v_mul_f32 v28,v28,v104
v_mul_f32 v29,v29,v105
v_cvt_pk_bf16_f32 v110,v26,v27
v_cvt_pk_bf16_f32 v111,v28,v29
)"
R"(M 38,v[240:243],66
L 3
buffer_store_dwordx2 v[110:111],v10,s[36:39],s66 offen offset:0 sc0 sc1
M 38,v[252:255],70
s_cbranch_scc1 Lg3_F0_%=
Lg3_K0_%=:
Lg3_L0_%=:
v_mfma_f32_16x16x32_bf16 v[26:29],a[8:11],v[42:45],0
s_add_u32 m0,s59,3072
R 62,1,5120
v_add_f32_dpp v115,v114,v114 quad_perm:[1,0,3,2] row_mask:0xf bank_mask:0xf
M 26,a[20:23],46
buffer_load_dword v8,s[36:39],s58 offen offset:0 sc1 lds
v_mfma_f32_16x16x32_bf16 v[22:25],a[4:7],v[42:45],0
s_mov_b32 s66,s65
v_add_f32_dpp v115,v115,v115 quad_perm:[2,3,0,1] row_mask:0xf bank_mask:0xf
v_fma_f32 v30,-v78,v82,v30
M 22,a[16:19],46
s_add_u32 s65,s65,s62
R 66,1,6144
v_fma_f32 v31,-v78,v83,v31
v_mfma_f32_16x16x32_bf16 v[18:21],a[0:3],v[42:45],0
v_add_f32_dpp v118,v115,v115 row_half_mirror row_mask:0xf bank_mask:0xf
v_fma_f32 v32,-v78,v84,v32
v_fma_f32 v33,-v78,v85,v33
M 18,a[12:15],46
L 3
v_mul_f32 v94,v30,v30
v_mul_f32 v95,v31,v31
M 18,a[24:27],50
R 70,1,7168
v_mov_b32_dpp v115,v118 row_ror:8 row_mask:0xf bank_mask:0xf
M 18,a[36:39],54
v_fma_f32 v115,-v118,v118,v115
v_max_f32 v115,s87,v115
M 22,a[28:31],50
v_rsq_f32 v116,v115
M 22,a[40:43],54
R 42,1,8192
v_fma_f32 v115,v15,v115,v16
v_mul_f32 v118,v17,v118
M 26,a[32:35],50
v_mul_f32 v119,v116,v116
v_fma_f32 v115,v14,v119,v115
M 26,a[44:47],54
L 3
v_fma_f32 v118,v116,v115,v118
v_mul_f32 v96,v32,v32
M 26,a[56:59],58
R 46,1,9216
v_mul_f32 v97,v33,v33
M 26,a[68:71],62
ds_write_b32 v12,v118 offset:0
M 22,a[52:55],58
v_fma_f32 v94,v80,v94,v79
v_fma_f32 v95,v80,v95,v79
v_fma_f32 v96,v80,v96,v79
M 22,a[64:67],62
R 50,1,10240
v_fma_f32 v97,v80,v97,v79
M 18,a[48:51],58
v_mul_f32 v94,v30,v94
v_mul_f32 v95,v31,v95
M 18,a[60:63],62
L 4
v_exp_f32 v94,v94
M 18,a[72:75],66
R 54,1,11264
v_mul_f32 v96,v32,v96
M 18,a[84:87],70
v_exp_f32 v95,v95
v_mul_f32 v97,v33,v97
M 22,a[76:79],66
v_exp_f32 v96,v96
M 22,a[88:91],70
R 58,1,12288
v_fma_f32 v94,v94,v81,v81
M 26,a[80:83],66
v_exp_f32 v97,v97
M 26,a[92:95],70
L 4
v_rcp_f32 v94,v94
M 26,a[104:107],42
R 62,1,13312
v_fma_f32 v95,v95,v81,v81
v_fma_f32 v96,v96,v81,v81
M 26,a[116:119],46
v_rcp_f32 v95,v95
M 22,a[100:103],42
v_rcp_f32 v96,v96
M 22,a[112:115],46
R 66,1,14336
v_fma_f32 v97,v97,v81,v81
M 18,a[96:99],42
v_rcp_f32 v97,v97
M 18,a[108:111],46
L 3
v_mul_f32 v30,v30,v94
v_mul_f32 v31,v31,v95
v_mul_f32 v32,v32,v96
M 18,a[120:123],50
R 70,1,15360
v_mul_f32 v33,v33,v97
M 18,a[132:135],54
s_waitcnt vmcnt(0)
v_cvt_pk_bf16_f32 v106,v30,v31
v_cvt_pk_bf16_f32 v107,v32,v33
M 22,a[124:127],50
s_barrier
ds_read_b32 v7,v6 offset:3072
v_fma_f32 v34,-v78,v86,v34
M 22,a[136:139],54
R 42,1,16384
v_fma_f32 v35,-v78,v87,v35
M 26,a[128:131],50
buffer_store_dword v4,v3,s[36:39],s70 offen offset:0 sc0 sc1
v_fma_f32 v36,-v78,v88,v36
M 26,a[140:143],54
L 4
v_fma_f32 v37,-v78,v89,v37
v_mul_f32 v98,v34,v34
M 26,a[152:155],58
s_mov_b32 s70,0x7fff0000
R 46,1,17408
v_mul_f32 v99,v35,v35
M 26,a[164:167],62
v_mul_f32 v100,v36,v36
v_mul_f32 v101,v37,v37
M 22,a[148:151],58
L 2
v_cmp_eq_u32 vcc,s53,v7
v_fma_f32 v98,v80,v98,v79
M 22,a[160:163],62
R 50,1,18432
v_fma_f32 v99,v80,v99,v79
v_fma_f32 v100,v80,v100,v79
M 18,a[144:147],58
v_fma_f32 v101,v80,v101,v79
v_mul_f32 v98,v34,v98
M 18,a[156:159],62
s_cmp_eq_u32 vcc_lo,-1
s_nop 0
v_mul_f32 v99,v35,v99
s_cbranch_scc0 Lg3_X0_%=
M 18,a[168:171],66
s_add_u32 m0,s57,0
s_add_u32 s56,s55,0
s_add_u32 s58,s58,s61
R 54,1,19456
M 18,a[180:183],70
buffer_load_dword v114,v11,s[36:39],s73 offen offset:0 sc1
M 22,a[172:175],66
D 0,56,0
v_mul_f32 v100,v36,v100
M 22,a[184:187],70
s_add_u32 s54,s54,s46
R 58,1,20480
v_mul_f32 v101,v37,v101
M 26,a[176:179],66
s_add_u32 s73,s73,s64
v_exp_f32 v98,v98
M 26,a[188:191],70
L 3
v_exp_f32 v99,v99
M 26,a[200:203],42
R 62,1,21504
v_fma_f32 v98,v98,v81,v81
M 26,a[212:215],46
D 0,56,1024
v_fma_f32 v99,v99,v81,v81
M 22,a[196:199],42
v_exp_f32 v100,v100
M 22,a[208:211],46
R 66,1,22528
v_fma_f32 v100,v100,v81,v81
M 18,a[192:195],42
D 0,56,2048
M 18,a[204:207],46
L 3
v_exp_f32 v101,v101
M 18,a[216:219],50
R 70,1,23552
v_rcp_f32 v98,v98
M 18,a[228:231],54
v_rcp_f32 v99,v99
M 22,a[220:223],50
D 0,56,3072
M 22,a[232:235],54
s_add_u32 m0,s59,0
R 42,1,24576
v_fma_f32 v101,v101,v81,v81
M 26,a[224:227],50
buffer_load_dword v8,s[36:39],s58 offen offset:0 sc1 lds
M 26,a[236:239],54
L 3
v_rcp_f32 v100,v100
v_mul_f32 v34,v34,v98
M 26,a[248:251],58
s_add_u32 m0,s57,16384
R 46,1,25600
v_mul_f32 v35,v35,v99
M 26,v[132:135],62
s_add_u32 s56,s55,16384
v_rcp_f32 v101,v101
M 22,a[244:247],58
D 0,56,0
M 22,v[128:131],62
s_add_u32 s55,s55,s60
R 50,1,26624
v_mul_f32 v36,v36,v100
M 18,a[240:243],58
s_sub_u32 s57,s84,s57
v_mul_f32 v37,v37,v101
v_cvt_pk_bf16_f32 v108,v34,v35
v_cvt_pk_bf16_f32 v109,v36,v37
M 18,a[252:255],62
L 3
buffer_store_dwordx4 v[106:109],v0,s[36:39],s67 offen offset:0 sc0 sc1
v_fma_f32 v38,-v78,v90,v38
M 18,v[136:139],66
R 54,1,27648
v_fma_f32 v39,-v78,v91,v39
M 18,v[148:151],70
v_fma_f32 v40,-v78,v92,v40
v_fma_f32 v41,-v78,v93,v41
M 22,v[140:143],66
D 0,56,1024
M 22,v[152:155],70
R 58,1,28672
v_mul_f32 v102,v38,v38
v_mul_f32 v103,v39,v39
M 26,v[144:147],66
v_mul_f32 v104,v40,v40
v_mul_f32 v105,v41,v41
M 26,v[156:159],70
L 3
v_fma_f32 v102,v80,v102,v79
v_fma_f32 v103,v80,v103,v79
M 26,v[168:171],42
R 62,1,29696
v_fma_f32 v104,v80,v104,v79
M 26,v[180:183],46
D 0,56,2048
M 22,v[164:167],42
v_fma_f32 v105,v80,v105,v79
v_mul_f32 v102,v38,v102
v_mul_f32 v103,v39,v103
M 22,v[176:179],46
R 66,1,30720
v_mul_f32 v104,v40,v104
M 18,v[160:163],42
v_exp_f32 v102,v102
M 18,v[172:175],46
L 3
D 0,56,3072
M 18,v[184:187],50
s_nop 0
R 70,1,31744
v_mul_f32 v105,v41,v105
M 18,v[196:199],54
s_waitcnt vmcnt(5)
v_exp_f32 v103,v103
v_fma_f32 v102,v102,v81,v81
M 22,v[188:191],50
s_barrier
buffer_store_dword v4,v3,s[36:39],s70 offen offset:0 sc0 sc1
M 22,v[200:203],54
s_mov_b32 s70,s69
R 42,2,0
ds_read_b32 v7,v6 offset:0
M 26,v[192:195],50
s_mov_b32 s69,s68
R 74,13,0
v_fma_f32 v103,v103,v81,v81
M 26,v[204:207],54
L 5
v_exp_f32 v104,v104
M 26,v[216:219],58
s_add_u32 s68,s68,s63
R 46,2,1024
v_exp_f32 v105,v105
M 26,v[228:231],62
v_rcp_f32 v102,v102
M 22,v[212:215],58
v_rcp_f32 v103,v103
M 22,v[224:227],62
L 2
R 50,2,2048
v_fma_f32 v104,v104,v81,v81
M 18,v[208:211],58
v_rcp_f32 v104,v104
M 18,v[220:223],62
s_nop 0
v_fma_f32 v105,v105,v81,v81
v_rcp_f32 v105,v105
M 18,v[232:235],66
R 54,2,3072
v_mul_f32 v38,v38,v102
M 18,v[244:247],70
v_mul_f32 v39,v39,v103
v_mul_f32 v40,v40,v104
M 22,v[236:239],66
v_cmp_eq_u32 vcc,s53,v7
v_mul_f32 v41,v41,v105
M 22,v[248:251],70
R 58,2,4096
v_cvt_pk_bf16_f32 v110,v38,v39
v_cvt_pk_bf16_f32 v111,v40,v41
M 26,v[240:243],66
L 3
s_cmp_eq_u32 vcc_lo,-1
buffer_store_dwordx2 v[110:111],v10,s[36:39],s67 offen offset:0 sc0 sc1
M 26,v[252:255],70
buffer_store_dword v4,v3,s[36:39],s71 offen offset:0 sc0 sc1
s_cbranch_scc1 Lg3_F1_%=
s_branch Lg3_K1_%=
Lg3_X0_%=:
M 18,a[168:171],66
s_add_u32 m0,s59,0
R 54,1,19456
v_mul_f32 v100,v36,v100
M 18,a[180:183],70
buffer_load_dword v8,s[36:39],s58 offen offset:0 sc1 lds
M 22,a[172:175],66
v_exp_f32 v98,v98
M 22,a[184:187],70
R 58,1,20480
v_mul_f32 v101,v37,v101
M 26,a[176:179],66
v_exp_f32 v99,v99
M 26,a[188:191],70
L 3
v_exp_f32 v100,v100
M 26,a[200:203],42
R 62,1,21504
v_fma_f32 v98,v98,v81,v81
M 26,a[212:215],46
v_exp_f32 v101,v101
M 22,a[196:199],42
v_rcp_f32 v98,v98
M 22,a[208:211],46
R 66,1,22528
v_fma_f32 v99,v99,v81,v81
M 18,a[192:195],42
v_rcp_f32 v99,v99
M 18,a[204:207],46
L 3
v_fma_f32 v100,v100,v81,v81
v_fma_f32 v101,v101,v81,v81
M 18,a[216:219],50
R 70,1,23552
v_mul_f32 v34,v34,v98
M 18,a[228:231],54
v_rcp_f32 v100,v100
M 22,a[220:223],50
v_rcp_f32 v101,v101
M 22,a[232:235],54
R 42,1,24576
v_mul_f32 v35,v35,v99
M 26,a[224:227],50
v_mul_f32 v36,v36,v100
v_mul_f32 v37,v37,v101
M 26,a[236:239],54
L 3
v_cvt_pk_bf16_f32 v108,v34,v35
v_cvt_pk_bf16_f32 v109,v36,v37
M 26,a[248:251],58
R 46,1,25600
v_fma_f32 v38,-v78,v90,v38
M 26,v[132:135],62
buffer_store_dwordx4 v[106:109],v0,s[36:39],s67 offen offset:0 sc0 sc1
v_fma_f32 v39,-v78,v91,v39
M 22,a[244:247],58
v_fma_f32 v40,-v78,v92,v40
v_fma_f32 v41,-v78,v93,v41
M 22,v[128:131],62
R 50,1,26624
v_mul_f32 v102,v38,v38
M 18,a[240:243],58
v_mul_f32 v103,v39,v39
v_mul_f32 v104,v40,v40
M 18,a[252:255],62
L 3
v_mul_f32 v105,v41,v41
v_fma_f32 v102,v80,v102,v79
M 18,v[136:139],66
R 54,1,27648
v_fma_f32 v103,v80,v103,v79
M 18,v[148:151],70
v_fma_f32 v104,v80,v104,v79
v_fma_f32 v105,v80,v105,v79
M 22,v[140:143],66
v_mul_f32 v102,v38,v102
v_mul_f32 v103,v39,v103
M 22,v[152:155],70
R 58,1,28672
v_mul_f32 v104,v40,v104
M 26,v[144:147],66
v_exp_f32 v102,v102
M 26,v[156:159],70
L 3
v_exp_f32 v103,v103
M 26,v[168:171],42
R 62,1,29696
v_mul_f32 v105,v41,v105
M 26,v[180:183],46
v_exp_f32 v104,v104
M 22,v[164:167],42
v_exp_f32 v105,v105
M 22,v[176:179],46
R 66,1,30720
v_fma_f32 v102,v102,v81,v81
M 18,v[160:163],42
v_rcp_f32 v102,v102
M 18,v[172:175],46
L 3
v_fma_f32 v103,v103,v81,v81
v_fma_f32 v104,v104,v81,v81
M 18,v[184:187],50
s_nop 0
R 70,1,31744
v_fma_f32 v105,v105,v81,v81
M 18,v[196:199],54
s_waitcnt vmcnt(1)
v_rcp_f32 v103,v103
M 22,v[188:191],50
s_barrier
buffer_store_dword v4,v3,s[36:39],s70 offen offset:0 sc0 sc1
M 22,v[200:203],54
s_mov_b32 s70,s69
ds_read_b32 v7,v6 offset:0
R 74,13,0
M 26,v[192:195],50
s_mov_b32 s69,s68
v_rcp_f32 v104,v104
M 26,v[204:207],54
L 4
v_rcp_f32 v105,v105
M 26,v[216:219],58
s_add_u32 s68,s68,s63
v_mul_f32 v38,v38,v102
v_mul_f32 v39,v39,v103
M 26,v[228:231],62
v_mul_f32 v40,v40,v104
v_mul_f32 v41,v41,v105
M 22,v[212:215],58
v_cvt_pk_bf16_f32 v110,v38,v39
v_cvt_pk_bf16_f32 v111,v40,v41
M 22,v[224:227],62
L 1
buffer_store_dwordx2 v[110:111],v10,s[36:39],s67 offen offset:0 sc0 sc1
M 18,v[208:211],58
v_cmp_eq_u32 vcc,s53,v7
M 18,v[220:223],62
s_nop 0
M 18,v[232:235],66
M 18,v[244:247],70
M 22,v[236:239],66
M 22,v[248:251],70
M 26,v[240:243],66
M 26,v[252:255],70
s_mov_b32 s80,1
s_cmp_eq_u32 vcc_lo,-1
s_cbranch_scc0 Lg3_xn0_%=
buffer_load_dword v114,v11,s[36:39],s73 offen offset:0 sc1
L 0
v_fma_f32 v18,-v74,v82,v18
v_fma_f32 v19,-v74,v83,v19
v_fma_f32 v20,-v74,v84,v20
v_fma_f32 v21,-v74,v85,v21
v_mul_f32 v94,v18,v18
v_mul_f32 v95,v19,v19
v_mul_f32 v96,v20,v20
v_mul_f32 v97,v21,v21
s_add_u32 s73,s73,s64
s_add_u32 m0,s57,0
s_add_u32 s56,s55,0
D 0,56,0
v_fma_f32 v94,v76,v94,v75
v_fma_f32 v95,v76,v95,v75
v_fma_f32 v96,v76,v96,v75
v_fma_f32 v97,v76,v97,v75
v_mul_f32 v94,v18,v94
v_mul_f32 v95,v19,v95
v_mul_f32 v96,v20,v96
v_mul_f32 v97,v21,v97
D 0,56,1024
v_exp_f32 v94,v94
v_exp_f32 v95,v95
v_exp_f32 v96,v96
v_exp_f32 v97,v97
v_fma_f32 v94,v94,v77,v77
v_fma_f32 v95,v95,v77,v77
v_fma_f32 v96,v96,v77,v77
v_fma_f32 v97,v97,v77,v77
D 0,56,2048
v_rcp_f32 v94,v94
v_rcp_f32 v95,v95
v_rcp_f32 v96,v96
v_rcp_f32 v97,v97
)"
R"(v_mul_f32 v18,v18,v94
v_mul_f32 v19,v19,v95
v_mul_f32 v20,v20,v96
v_mul_f32 v21,v21,v97
D 0,56,3072
v_cvt_pk_bf16_f32 v106,v18,v19
v_cvt_pk_bf16_f32 v107,v20,v21
v_fma_f32 v22,-v74,v86,v22
v_fma_f32 v23,-v74,v87,v23
v_fma_f32 v24,-v74,v88,v24
v_fma_f32 v25,-v74,v89,v25
v_mul_f32 v98,v22,v22
v_mul_f32 v99,v23,v23
s_add_u32 s58,s58,s61
s_add_u32 m0,s57,16384
s_add_u32 s56,s55,16384
D 0,56,0
v_mul_f32 v100,v24,v24
v_mul_f32 v101,v25,v25
v_fma_f32 v98,v76,v98,v75
v_fma_f32 v99,v76,v99,v75
v_fma_f32 v100,v76,v100,v75
v_fma_f32 v101,v76,v101,v75
v_mul_f32 v98,v22,v98
v_mul_f32 v99,v23,v99
D 0,56,1024
v_mul_f32 v100,v24,v100
v_mul_f32 v101,v25,v101
v_exp_f32 v98,v98
v_exp_f32 v99,v99
v_exp_f32 v100,v100
v_exp_f32 v101,v101
v_fma_f32 v98,v98,v77,v77
v_fma_f32 v99,v99,v77,v77
D 0,56,2048
v_fma_f32 v100,v100,v77,v77
v_fma_f32 v101,v101,v77,v77
v_rcp_f32 v98,v98
v_rcp_f32 v99,v99
v_rcp_f32 v100,v100
v_rcp_f32 v101,v101
v_mul_f32 v22,v22,v98
v_mul_f32 v23,v23,v99
D 0,56,3072
v_mul_f32 v24,v24,v100
v_mul_f32 v25,v25,v101
v_cvt_pk_bf16_f32 v108,v22,v23
v_cvt_pk_bf16_f32 v109,v24,v25
buffer_store_dwordx4 v[106:109],v0,s[36:39],s66 offen offset:0 sc0 sc1
v_fma_f32 v26,-v74,v90,v26
v_fma_f32 v27,-v74,v91,v27
v_fma_f32 v28,-v74,v92,v28
v_fma_f32 v29,-v74,v93,v29
s_add_u32 s55,s55,s60
s_sub_u32 s57,s84,s57
s_add_u32 s54,s54,s46
v_mul_f32 v102,v26,v26
v_mul_f32 v103,v27,v27
v_mul_f32 v104,v28,v28
v_mul_f32 v105,v29,v29
v_fma_f32 v102,v76,v102,v75
v_fma_f32 v103,v76,v103,v75
v_fma_f32 v104,v76,v104,v75
v_fma_f32 v105,v76,v105,v75
v_mul_f32 v102,v26,v102
v_mul_f32 v103,v27,v103
v_mul_f32 v104,v28,v104
v_mul_f32 v105,v29,v105
v_exp_f32 v102,v102
v_exp_f32 v103,v103
v_exp_f32 v104,v104
v_exp_f32 v105,v105
v_fma_f32 v102,v102,v77,v77
v_fma_f32 v103,v103,v77,v77
v_fma_f32 v104,v104,v77,v77
v_fma_f32 v105,v105,v77,v77
v_rcp_f32 v102,v102
v_rcp_f32 v103,v103
v_rcp_f32 v104,v104
v_rcp_f32 v105,v105
v_mul_f32 v26,v26,v102
v_mul_f32 v27,v27,v103
v_mul_f32 v28,v28,v104
v_mul_f32 v29,v29,v105
v_cvt_pk_bf16_f32 v110,v26,v27
v_cvt_pk_bf16_f32 v111,v28,v29
buffer_store_dwordx2 v[110:111],v10,s[36:39],s66 offen offset:0 sc0 sc1
s_mov_b32 s72,s69
s_mov_b32 s69,0x7fff0000
s_mov_b32 s66,0x7fff0000
s_mov_b32 s76,0x7fff0000
s_branch Lg3_gox_%=
Lg3_xn0_%=:
s_cmp_ge_u32 s54,256
s_cbranch_scc1 Lg3_fin0_%=
s_add_u32 m0,s59,4096
s_nop 0
buffer_load_dword v8,s[36:39],s58 offen offset:0 sc1 lds
buffer_store_dword v4,v3,s[36:39],s71 offen offset:0 sc0 sc1
s_add_u32 m0,s59,5120
s_nop 0
buffer_load_dword v8,s[36:39],s58 offen offset:0 sc1 lds
buffer_store_dword v4,v3,s[36:39],s71 offen offset:0 sc0 sc1
s_mov_b32 s83,0
L 0
v_fma_f32 v18,-v74,v82,v18
v_fma_f32 v19,-v74,v83,v19
v_fma_f32 v20,-v74,v84,v20
v_fma_f32 v21,-v74,v85,v21
v_mul_f32 v94,v18,v18
v_mul_f32 v95,v19,v19
v_mul_f32 v96,v20,v20
v_mul_f32 v97,v21,v21
v_fma_f32 v94,v76,v94,v75
v_fma_f32 v95,v76,v95,v75
v_fma_f32 v96,v76,v96,v75
v_fma_f32 v97,v76,v97,v75
v_mul_f32 v94,v18,v94
v_mul_f32 v95,v19,v95
v_mul_f32 v96,v20,v96
v_mul_f32 v97,v21,v97
v_exp_f32 v94,v94
v_exp_f32 v95,v95
v_exp_f32 v96,v96
v_exp_f32 v97,v97
v_fma_f32 v94,v94,v77,v77
v_fma_f32 v95,v95,v77,v77
v_fma_f32 v96,v96,v77,v77
v_fma_f32 v97,v97,v77,v77
v_rcp_f32 v94,v94
v_rcp_f32 v95,v95
v_rcp_f32 v96,v96
v_rcp_f32 v97,v97
v_mul_f32 v18,v18,v94
v_mul_f32 v19,v19,v95
v_mul_f32 v20,v20,v96
v_mul_f32 v21,v21,v97
v_cvt_pk_bf16_f32 v106,v18,v19
v_cvt_pk_bf16_f32 v107,v20,v21
v_fma_f32 v22,-v74,v86,v22
v_fma_f32 v23,-v74,v87,v23
v_fma_f32 v24,-v74,v88,v24
v_fma_f32 v25,-v74,v89,v25
v_mul_f32 v98,v22,v22
v_mul_f32 v99,v23,v23
v_mul_f32 v100,v24,v24
v_mul_f32 v101,v25,v25
v_fma_f32 v98,v76,v98,v75
v_fma_f32 v99,v76,v99,v75
v_fma_f32 v100,v76,v100,v75
v_fma_f32 v101,v76,v101,v75
v_mul_f32 v98,v22,v98
v_mul_f32 v99,v23,v99
v_mul_f32 v100,v24,v100
v_mul_f32 v101,v25,v101
v_exp_f32 v98,v98
v_exp_f32 v99,v99
v_exp_f32 v100,v100
v_exp_f32 v101,v101
v_fma_f32 v98,v98,v77,v77
v_fma_f32 v99,v99,v77,v77
v_fma_f32 v100,v100,v77,v77
v_fma_f32 v101,v101,v77,v77
v_rcp_f32 v98,v98
v_rcp_f32 v99,v99
v_rcp_f32 v100,v100
v_rcp_f32 v101,v101
v_mul_f32 v22,v22,v98
v_mul_f32 v23,v23,v99
v_mul_f32 v24,v24,v100
v_mul_f32 v25,v25,v101
v_cvt_pk_bf16_f32 v108,v22,v23
v_cvt_pk_bf16_f32 v109,v24,v25
buffer_store_dwordx4 v[106:109],v0,s[36:39],s66 offen offset:0 sc0 sc1
v_fma_f32 v26,-v74,v90,v26
v_fma_f32 v27,-v74,v91,v27
v_fma_f32 v28,-v74,v92,v28
v_fma_f32 v29,-v74,v93,v29
v_mul_f32 v102,v26,v26
v_mul_f32 v103,v27,v27
v_mul_f32 v104,v28,v28
v_mul_f32 v105,v29,v29
v_fma_f32 v102,v76,v102,v75
v_fma_f32 v103,v76,v103,v75
v_fma_f32 v104,v76,v104,v75
v_fma_f32 v105,v76,v105,v75
v_mul_f32 v102,v26,v102
v_mul_f32 v103,v27,v103
v_mul_f32 v104,v28,v104
v_mul_f32 v105,v29,v105
v_exp_f32 v102,v102
v_exp_f32 v103,v103
v_exp_f32 v104,v104
v_exp_f32 v105,v105
v_fma_f32 v102,v102,v77,v77
v_fma_f32 v103,v103,v77,v77
v_fma_f32 v104,v104,v77,v77
v_fma_f32 v105,v105,v77,v77
v_rcp_f32 v102,v102
v_rcp_f32 v103,v103
v_rcp_f32 v104,v104
v_rcp_f32 v105,v105
v_mul_f32 v26,v26,v102
v_mul_f32 v27,v27,v103
v_mul_f32 v28,v28,v104
v_mul_f32 v29,v29,v105
v_cvt_pk_bf16_f32 v110,v26,v27
v_cvt_pk_bf16_f32 v111,v28,v29
buffer_store_dwordx2 v[110:111],v10,s[36:39],s66 offen offset:0 sc0 sc1
s_mov_b32 s72,s69
s_mov_b32 s69,0x7fff0000
s_mov_b32 s66,0x7fff0000
s_mov_b32 s76,0x7fff0000
s_branch Lg3_poll_%=
Lg3_fin0_%=:
L 0
v_fma_f32 v18,-v74,v82,v18
v_fma_f32 v19,-v74,v83,v19
v_fma_f32 v20,-v74,v84,v20
v_fma_f32 v21,-v74,v85,v21
v_mul_f32 v94,v18,v18
v_mul_f32 v95,v19,v19
v_mul_f32 v96,v20,v20
v_mul_f32 v97,v21,v21
v_fma_f32 v94,v76,v94,v75
v_fma_f32 v95,v76,v95,v75
v_fma_f32 v96,v76,v96,v75
v_fma_f32 v97,v76,v97,v75
v_mul_f32 v94,v18,v94
v_mul_f32 v95,v19,v95
v_mul_f32 v96,v20,v96
v_mul_f32 v97,v21,v97
v_exp_f32 v94,v94
v_exp_f32 v95,v95
v_exp_f32 v96,v96
v_exp_f32 v97,v97
v_fma_f32 v94,v94,v77,v77
v_fma_f32 v95,v95,v77,v77
v_fma_f32 v96,v96,v77,v77
v_fma_f32 v97,v97,v77,v77
v_rcp_f32 v94,v94
v_rcp_f32 v95,v95
v_rcp_f32 v96,v96
v_rcp_f32 v97,v97
v_mul_f32 v18,v18,v94
v_mul_f32 v19,v19,v95
v_mul_f32 v20,v20,v96
v_mul_f32 v21,v21,v97
v_cvt_pk_bf16_f32 v106,v18,v19
v_cvt_pk_bf16_f32 v107,v20,v21
v_fma_f32 v22,-v74,v86,v22
v_fma_f32 v23,-v74,v87,v23
v_fma_f32 v24,-v74,v88,v24
v_fma_f32 v25,-v74,v89,v25
v_mul_f32 v98,v22,v22
v_mul_f32 v99,v23,v23
v_mul_f32 v100,v24,v24
v_mul_f32 v101,v25,v25
v_fma_f32 v98,v76,v98,v75
v_fma_f32 v99,v76,v99,v75
v_fma_f32 v100,v76,v100,v75
v_fma_f32 v101,v76,v101,v75
v_mul_f32 v98,v22,v98
v_mul_f32 v99,v23,v99
v_mul_f32 v100,v24,v100
v_mul_f32 v101,v25,v101
v_exp_f32 v98,v98
v_exp_f32 v99,v99
v_exp_f32 v100,v100
v_exp_f32 v101,v101
v_fma_f32 v98,v98,v77,v77
v_fma_f32 v99,v99,v77,v77
v_fma_f32 v100,v100,v77,v77
v_fma_f32 v101,v101,v77,v77
v_rcp_f32 v98,v98
v_rcp_f32 v99,v99
v_rcp_f32 v100,v100
v_rcp_f32 v101,v101
v_mul_f32 v22,v22,v98
v_mul_f32 v23,v23,v99
v_mul_f32 v24,v24,v100
v_mul_f32 v25,v25,v101
v_cvt_pk_bf16_f32 v108,v22,v23
v_cvt_pk_bf16_f32 v109,v24,v25
buffer_store_dwordx4 v[106:109],v0,s[36:39],s66 offen offset:0 sc0 sc1
v_fma_f32 v26,-v74,v90,v26
v_fma_f32 v27,-v74,v91,v27
v_fma_f32 v28,-v74,v92,v28
v_fma_f32 v29,-v74,v93,v29
v_mul_f32 v102,v26,v26
v_mul_f32 v103,v27,v27
v_mul_f32 v104,v28,v28
v_mul_f32 v105,v29,v29
v_fma_f32 v102,v76,v102,v75
v_fma_f32 v103,v76,v103,v75
v_fma_f32 v104,v76,v104,v75
v_fma_f32 v105,v76,v105,v75
v_mul_f32 v102,v26,v102
v_mul_f32 v103,v27,v103
v_mul_f32 v104,v28,v104
v_mul_f32 v105,v29,v105
v_exp_f32 v102,v102
v_exp_f32 v103,v103
v_exp_f32 v104,v104
v_exp_f32 v105,v105
v_fma_f32 v102,v102,v77,v77
v_fma_f32 v103,v103,v77,v77
v_fma_f32 v104,v104,v77,v77
v_fma_f32 v105,v105,v77,v77
v_rcp_f32 v102,v102
v_rcp_f32 v103,v103
v_rcp_f32 v104,v104
v_rcp_f32 v105,v105
v_mul_f32 v26,v26,v102
v_mul_f32 v27,v27,v103
v_mul_f32 v28,v28,v104
v_mul_f32 v29,v29,v105
v_cvt_pk_bf16_f32 v110,v26,v27
v_cvt_pk_bf16_f32 v111,v28,v29
buffer_store_dwordx2 v[110:111],v10,s[36:39],s66 offen offset:0 sc0 sc1
s_mov_b32 s72,s69
s_mov_b32 s69,0x7fff0000
s_mov_b32 s66,0x7fff0000
s_mov_b32 s76,0x7fff0000
s_branch Lg3_done_%=
Lg3_K1_%=:
Lg3_L1_%=:
v_mfma_f32_16x16x32_bf16 v[38:41],a[8:11],v[42:45],0
s_add_u32 m0,s59,3072
R 62,2,5120
v_add_f32_dpp v115,v114,v114 quad_perm:[1,0,3,2] row_mask:0xf bank_mask:0xf
M 38,a[20:23],46
buffer_load_dword v8,s[36:39],s58 offen offset:0 sc1 lds
v_mfma_f32_16x16x32_bf16 v[34:37],a[4:7],v[42:45],0
s_mov_b32 s67,s65
v_add_f32_dpp v115,v115,v115 quad_perm:[2,3,0,1] row_mask:0xf bank_mask:0xf
v_fma_f32 v18,-v74,v82,v18
M 34,a[16:19],46
s_add_u32 s65,s65,s62
R 66,2,6144
v_fma_f32 v19,-v74,v83,v19
v_mfma_f32_16x16x32_bf16 v[30:33],a[0:3],v[42:45],0
v_add_f32_dpp v118,v115,v115 row_half_mirror row_mask:0xf bank_mask:0xf
v_fma_f32 v20,-v74,v84,v20
v_fma_f32 v21,-v74,v85,v21
M 30,a[12:15],46
L 3
v_mul_f32 v94,v18,v18
v_mul_f32 v95,v19,v19
M 30,a[24:27],50
R 70,2,7168
v_mov_b32_dpp v115,v118 row_ror:8 row_mask:0xf bank_mask:0xf
M 30,a[36:39],54
v_fma_f32 v115,-v118,v118,v115
v_max_f32 v115,s87,v115
M 34,a[28:31],50
v_rsq_f32 v116,v115
M 34,a[40:43],54
R 42,2,8192
v_fma_f32 v115,v15,v115,v16
v_mul_f32 v118,v17,v118
M 38,a[32:35],50
v_mul_f32 v119,v116,v116
v_fma_f32 v115,v14,v119,v115
M 38,a[44:47],54
L 3
v_fma_f32 v118,v116,v115,v118
v_mul_f32 v96,v20,v20
M 38,a[56:59],58
R 46,2,9216
v_mul_f32 v97,v21,v21
M 38,a[68:71],62
ds_write_b32 v12,v118 offset:2048
M 34,a[52:55],58
v_fma_f32 v94,v76,v94,v75
v_fma_f32 v95,v76,v95,v75
v_fma_f32 v96,v76,v96,v75
M 34,a[64:67],62
R 50,2,10240
v_fma_f32 v97,v76,v97,v75
M 30,a[48:51],58
v_mul_f32 v94,v18,v94
v_mul_f32 v95,v19,v95
M 30,a[60:63],62
L 4
v_exp_f32 v94,v94
M 30,a[72:75],66
R 54,2,11264
v_mul_f32 v96,v20,v96
M 30,a[84:87],70
v_exp_f32 v95,v95
v_mul_f32 v97,v21,v97
M 34,a[76:79],66
v_exp_f32 v96,v96
M 34,a[88:91],70
R 58,2,12288
v_fma_f32 v94,v94,v77,v77
M 38,a[80:83],66
v_exp_f32 v97,v97
M 38,a[92:95],70
L 4
v_rcp_f32 v94,v94
M 38,a[104:107],42
R 62,2,13312
v_fma_f32 v95,v95,v77,v77
v_fma_f32 v96,v96,v77,v77
M 38,a[116:119],46
v_rcp_f32 v95,v95
M 34,a[100:103],42
v_rcp_f32 v96,v96
M 34,a[112:115],46
R 66,2,14336
v_fma_f32 v97,v97,v77,v77
M 30,a[96:99],42
v_rcp_f32 v97,v97
M 30,a[108:111],46
L 3
v_mul_f32 v18,v18,v94
v_mul_f32 v19,v19,v95
v_mul_f32 v20,v20,v96
M 30,a[120:123],50
R 70,2,15360
v_mul_f32 v21,v21,v97
M 30,a[132:135],54
s_waitcnt vmcnt(0)
v_cvt_pk_bf16_f32 v106,v18,v19
v_cvt_pk_bf16_f32 v107,v20,v21
M 34,a[124:127],50
s_barrier
ds_read_b32 v7,v6 offset:3072
v_fma_f32 v22,-v74,v86,v22
M 34,a[136:139],54
R 42,2,16384
v_fma_f32 v23,-v74,v87,v23
M 38,a[128:131],50
buffer_store_dword v4,v3,s[36:39],s70 offen offset:0 sc0 sc1
v_fma_f32 v24,-v74,v88,v24
M 38,a[140:143],54
L 4
v_fma_f32 v25,-v74,v89,v25
v_mul_f32 v98,v22,v22
M 38,a[152:155],58
s_mov_b32 s70,0x7fff0000
R 46,2,17408
v_mul_f32 v99,v23,v23
M 38,a[164:167],62
v_mul_f32 v100,v24,v24
v_mul_f32 v101,v25,v25
M 34,a[148:151],58
L 2
v_cmp_eq_u32 vcc,s53,v7
v_fma_f32 v98,v76,v98,v75
M 34,a[160:163],62
R 50,2,18432
v_fma_f32 v99,v76,v99,v75
v_fma_f32 v100,v76,v100,v75
M 30,a[144:147],58
v_fma_f32 v101,v76,v101,v75
v_mul_f32 v98,v22,v98
M 30,a[156:159],62
s_cmp_eq_u32 vcc_lo,-1
s_nop 0
v_mul_f32 v99,v23,v99
s_cbranch_scc0 Lg3_X1_%=
M 30,a[168:171],66
s_add_u32 m0,s57,0
s_add_u32 s56,s55,0
s_add_u32 s58,s58,s61
R 54,2,19456
M 30,a[180:183],70
buffer_load_dword v114,v11,s[36:39],s73 offen offset:0 sc1
M 34,a[172:175],66
D 0,56,0
v_mul_f32 v100,v24,v100
M 34,a[184:187],70
s_add_u32 s54,s54,s46
R 58,2,20480
v_mul_f32 v101,v25,v101
M 38,a[176:179],66
s_add_u32 s73,s73,s64
v_exp_f32 v98,v98
M 38,a[188:191],70
L 3
v_exp_f32 v99,v99
M 38,a[200:203],42
R 62,2,21504
v_fma_f32 v98,v98,v77,v77
M 38,a[212:215],46
D 0,56,1024
v_fma_f32 v99,v99,v77,v77
M 34,a[196:199],42
v_exp_f32 v100,v100
M 34,a[208:211],46
R 66,2,22528
v_fma_f32 v100,v100,v77,v77
M 30,a[192:195],42
D 0,56,2048
M 30,a[204:207],46
L 3
v_exp_f32 v101,v101
)"
R"(M 30,a[216:219],50
R 70,2,23552
v_rcp_f32 v98,v98
M 30,a[228:231],54
v_rcp_f32 v99,v99
M 34,a[220:223],50
D 0,56,3072
M 34,a[232:235],54
s_add_u32 m0,s59,1024
R 42,2,24576
v_fma_f32 v101,v101,v77,v77
M 38,a[224:227],50
buffer_load_dword v8,s[36:39],s58 offen offset:0 sc1 lds
M 38,a[236:239],54
L 3
v_rcp_f32 v100,v100
v_mul_f32 v22,v22,v98
M 38,a[248:251],58
s_add_u32 m0,s57,16384
R 46,2,25600
v_mul_f32 v23,v23,v99
M 38,v[132:135],62
s_add_u32 s56,s55,16384
v_rcp_f32 v101,v101
M 34,a[244:247],58
D 0,56,0
M 34,v[128:131],62
s_add_u32 s55,s55,s60
R 50,2,26624
v_mul_f32 v24,v24,v100
M 30,a[240:243],58
s_sub_u32 s57,s84,s57
v_mul_f32 v25,v25,v101
v_cvt_pk_bf16_f32 v108,v22,v23
v_cvt_pk_bf16_f32 v109,v24,v25
M 30,a[252:255],62
L 3
buffer_store_dwordx4 v[106:109],v0,s[36:39],s66 offen offset:0 sc0 sc1
v_fma_f32 v26,-v74,v90,v26
M 30,v[136:139],66
R 54,2,27648
v_fma_f32 v27,-v74,v91,v27
M 30,v[148:151],70
v_fma_f32 v28,-v74,v92,v28
v_fma_f32 v29,-v74,v93,v29
M 34,v[140:143],66
D 0,56,1024
M 34,v[152:155],70
R 58,2,28672
v_mul_f32 v102,v26,v26
v_mul_f32 v103,v27,v27
M 38,v[144:147],66
v_mul_f32 v104,v28,v28
v_mul_f32 v105,v29,v29
M 38,v[156:159],70
L 3
v_fma_f32 v102,v76,v102,v75
v_fma_f32 v103,v76,v103,v75
M 38,v[168:171],42
R 62,2,29696
v_fma_f32 v104,v76,v104,v75
M 38,v[180:183],46
D 0,56,2048
M 34,v[164:167],42
v_fma_f32 v105,v76,v105,v75
v_mul_f32 v102,v26,v102
v_mul_f32 v103,v27,v103
M 34,v[176:179],46
R 66,2,30720
v_mul_f32 v104,v28,v104
M 30,v[160:163],42
v_exp_f32 v102,v102
M 30,v[172:175],46
L 3
D 0,56,3072
M 30,v[184:187],50
s_nop 0
R 70,2,31744
v_mul_f32 v105,v29,v105
M 30,v[196:199],54
s_waitcnt vmcnt(5)
v_exp_f32 v103,v103
v_fma_f32 v102,v102,v77,v77
M 34,v[188:191],50
s_barrier
buffer_store_dword v4,v3,s[36:39],s70 offen offset:0 sc0 sc1
M 34,v[200:203],54
s_mov_b32 s70,s69
R 42,1,0
ds_read_b32 v7,v6 offset:1024
M 38,v[192:195],50
s_mov_b32 s69,s68
R 78,13,2048
v_fma_f32 v103,v103,v77,v77
M 38,v[204:207],54
L 5
v_exp_f32 v104,v104
M 38,v[216:219],58
s_add_u32 s68,s68,s63
R 46,1,1024
v_exp_f32 v105,v105
M 38,v[228:231],62
v_rcp_f32 v102,v102
M 34,v[212:215],58
v_rcp_f32 v103,v103
M 34,v[224:227],62
L 2
R 50,1,2048
v_fma_f32 v104,v104,v77,v77
M 30,v[208:211],58
v_rcp_f32 v104,v104
M 30,v[220:223],62
s_nop 0
v_fma_f32 v105,v105,v77,v77
v_rcp_f32 v105,v105
M 30,v[232:235],66
R 54,1,3072
v_mul_f32 v26,v26,v102
M 30,v[244:247],70
v_mul_f32 v27,v27,v103
v_mul_f32 v28,v28,v104
M 34,v[236:239],66
v_cmp_eq_u32 vcc,s53,v7
v_mul_f32 v29,v29,v105
M 34,v[248:251],70
R 58,1,4096
v_cvt_pk_bf16_f32 v110,v26,v27
v_cvt_pk_bf16_f32 v111,v28,v29
M 38,v[240:243],66
L 3
s_cmp_eq_u32 vcc_lo,-1
buffer_store_dwordx2 v[110:111],v10,s[36:39],s66 offen offset:0 sc0 sc1
M 38,v[252:255],70
buffer_store_dword v4,v3,s[36:39],s71 offen offset:0 sc0 sc1
s_cbranch_scc1 Lg3_F0_%=
s_branch Lg3_K0_%=
Lg3_X1_%=:
M 30,a[168:171],66
s_add_u32 m0,s59,1024
R 54,2,19456
v_mul_f32 v100,v24,v100
M 30,a[180:183],70
buffer_load_dword v8,s[36:39],s58 offen offset:0 sc1 lds
M 34,a[172:175],66
v_exp_f32 v98,v98
M 34,a[184:187],70
R 58,2,20480
v_mul_f32 v101,v25,v101
M 38,a[176:179],66
v_exp_f32 v99,v99
M 38,a[188:191],70
L 3
v_exp_f32 v100,v100
M 38,a[200:203],42
R 62,2,21504
v_fma_f32 v98,v98,v77,v77
M 38,a[212:215],46
v_exp_f32 v101,v101
M 34,a[196:199],42
v_rcp_f32 v98,v98
M 34,a[208:211],46
R 66,2,22528
v_fma_f32 v99,v99,v77,v77
M 30,a[192:195],42
v_rcp_f32 v99,v99
M 30,a[204:207],46
L 3
v_fma_f32 v100,v100,v77,v77
v_fma_f32 v101,v101,v77,v77
M 30,a[216:219],50
R 70,2,23552
v_mul_f32 v22,v22,v98
M 30,a[228:231],54
v_rcp_f32 v100,v100
M 34,a[220:223],50
v_rcp_f32 v101,v101
M 34,a[232:235],54
R 42,2,24576
v_mul_f32 v23,v23,v99
M 38,a[224:227],50
v_mul_f32 v24,v24,v100
v_mul_f32 v25,v25,v101
M 38,a[236:239],54
L 3
v_cvt_pk_bf16_f32 v108,v22,v23
v_cvt_pk_bf16_f32 v109,v24,v25
M 38,a[248:251],58
R 46,2,25600
v_fma_f32 v26,-v74,v90,v26
M 38,v[132:135],62
buffer_store_dwordx4 v[106:109],v0,s[36:39],s66 offen offset:0 sc0 sc1
v_fma_f32 v27,-v74,v91,v27
M 34,a[244:247],58
v_fma_f32 v28,-v74,v92,v28
v_fma_f32 v29,-v74,v93,v29
M 34,v[128:131],62
R 50,2,26624
v_mul_f32 v102,v26,v26
M 30,a[240:243],58
v_mul_f32 v103,v27,v27
v_mul_f32 v104,v28,v28
M 30,a[252:255],62
L 3
v_mul_f32 v105,v29,v29
v_fma_f32 v102,v76,v102,v75
M 30,v[136:139],66
R 54,2,27648
v_fma_f32 v103,v76,v103,v75
M 30,v[148:151],70
v_fma_f32 v104,v76,v104,v75
v_fma_f32 v105,v76,v105,v75
M 34,v[140:143],66
v_mul_f32 v102,v26,v102
v_mul_f32 v103,v27,v103
M 34,v[152:155],70
R 58,2,28672
v_mul_f32 v104,v28,v104
M 38,v[144:147],66
v_exp_f32 v102,v102
M 38,v[156:159],70
L 3
v_exp_f32 v103,v103
M 38,v[168:171],42
R 62,2,29696
v_mul_f32 v105,v29,v105
M 38,v[180:183],46
v_exp_f32 v104,v104
M 34,v[164:167],42
v_exp_f32 v105,v105
M 34,v[176:179],46
R 66,2,30720
v_fma_f32 v102,v102,v77,v77
M 30,v[160:163],42
v_rcp_f32 v102,v102
M 30,v[172:175],46
L 3
v_fma_f32 v103,v103,v77,v77
v_fma_f32 v104,v104,v77,v77
M 30,v[184:187],50
s_nop 0
R 70,2,31744
v_fma_f32 v105,v105,v77,v77
M 30,v[196:199],54
s_waitcnt vmcnt(1)
v_rcp_f32 v103,v103
M 34,v[188:191],50
s_barrier
buffer_store_dword v4,v3,s[36:39],s70 offen offset:0 sc0 sc1
M 34,v[200:203],54
s_mov_b32 s70,s69
ds_read_b32 v7,v6 offset:1024
R 78,13,2048
M 38,v[192:195],50
s_mov_b32 s69,s68
v_rcp_f32 v104,v104
M 38,v[204:207],54
L 4
v_rcp_f32 v105,v105
M 38,v[216:219],58
s_add_u32 s68,s68,s63
v_mul_f32 v26,v26,v102
v_mul_f32 v27,v27,v103
M 38,v[228:231],62
v_mul_f32 v28,v28,v104
v_mul_f32 v29,v29,v105
M 34,v[212:215],58
v_cvt_pk_bf16_f32 v110,v26,v27
v_cvt_pk_bf16_f32 v111,v28,v29
M 34,v[224:227],62
L 1
buffer_store_dwordx2 v[110:111],v10,s[36:39],s66 offen offset:0 sc0 sc1
M 30,v[208:211],58
v_cmp_eq_u32 vcc,s53,v7
M 30,v[220:223],62
s_nop 0
M 30,v[232:235],66
M 30,v[244:247],70
M 34,v[236:239],66
M 34,v[248:251],70
M 38,v[240:243],66
M 38,v[252:255],70
s_mov_b32 s80,0
s_cmp_eq_u32 vcc_lo,-1
s_cbranch_scc0 Lg3_xn1_%=
buffer_load_dword v114,v11,s[36:39],s73 offen offset:0 sc1
L 0
v_fma_f32 v30,-v78,v82,v30
v_fma_f32 v31,-v78,v83,v31
v_fma_f32 v32,-v78,v84,v32
v_fma_f32 v33,-v78,v85,v33
v_mul_f32 v94,v30,v30
v_mul_f32 v95,v31,v31
v_mul_f32 v96,v32,v32
v_mul_f32 v97,v33,v33
s_add_u32 s73,s73,s64
s_add_u32 m0,s57,0
s_add_u32 s56,s55,0
D 0,56,0
v_fma_f32 v94,v80,v94,v79
v_fma_f32 v95,v80,v95,v79
v_fma_f32 v96,v80,v96,v79
v_fma_f32 v97,v80,v97,v79
v_mul_f32 v94,v30,v94
v_mul_f32 v95,v31,v95
v_mul_f32 v96,v32,v96
v_mul_f32 v97,v33,v97
D 0,56,1024
v_exp_f32 v94,v94
v_exp_f32 v95,v95
v_exp_f32 v96,v96
v_exp_f32 v97,v97
v_fma_f32 v94,v94,v81,v81
v_fma_f32 v95,v95,v81,v81
v_fma_f32 v96,v96,v81,v81
v_fma_f32 v97,v97,v81,v81
D 0,56,2048
v_rcp_f32 v94,v94
v_rcp_f32 v95,v95
v_rcp_f32 v96,v96
v_rcp_f32 v97,v97
v_mul_f32 v30,v30,v94
v_mul_f32 v31,v31,v95
v_mul_f32 v32,v32,v96
v_mul_f32 v33,v33,v97
D 0,56,3072
v_cvt_pk_bf16_f32 v106,v30,v31
v_cvt_pk_bf16_f32 v107,v32,v33
v_fma_f32 v34,-v78,v86,v34
v_fma_f32 v35,-v78,v87,v35
v_fma_f32 v36,-v78,v88,v36
v_fma_f32 v37,-v78,v89,v37
v_mul_f32 v98,v34,v34
v_mul_f32 v99,v35,v35
s_add_u32 s58,s58,s61
s_add_u32 m0,s57,16384
s_add_u32 s56,s55,16384
D 0,56,0
v_mul_f32 v100,v36,v36
v_mul_f32 v101,v37,v37
v_fma_f32 v98,v80,v98,v79
v_fma_f32 v99,v80,v99,v79
v_fma_f32 v100,v80,v100,v79
v_fma_f32 v101,v80,v101,v79
v_mul_f32 v98,v34,v98
v_mul_f32 v99,v35,v99
D 0,56,1024
v_mul_f32 v100,v36,v100
v_mul_f32 v101,v37,v101
v_exp_f32 v98,v98
v_exp_f32 v99,v99
v_exp_f32 v100,v100
v_exp_f32 v101,v101
v_fma_f32 v98,v98,v81,v81
v_fma_f32 v99,v99,v81,v81
D 0,56,2048
v_fma_f32 v100,v100,v81,v81
v_fma_f32 v101,v101,v81,v81
v_rcp_f32 v98,v98
v_rcp_f32 v99,v99
v_rcp_f32 v100,v100
v_rcp_f32 v101,v101
v_mul_f32 v34,v34,v98
v_mul_f32 v35,v35,v99
D 0,56,3072
v_mul_f32 v36,v36,v100
v_mul_f32 v37,v37,v101
v_cvt_pk_bf16_f32 v108,v34,v35
v_cvt_pk_bf16_f32 v109,v36,v37
buffer_store_dwordx4 v[106:109],v0,s[36:39],s67 offen offset:0 sc0 sc1
v_fma_f32 v38,-v78,v90,v38
v_fma_f32 v39,-v78,v91,v39
v_fma_f32 v40,-v78,v92,v40
v_fma_f32 v41,-v78,v93,v41
s_add_u32 s55,s55,s60
s_sub_u32 s57,s84,s57
s_add_u32 s54,s54,s46
v_mul_f32 v102,v38,v38
v_mul_f32 v103,v39,v39
v_mul_f32 v104,v40,v40
v_mul_f32 v105,v41,v41
v_fma_f32 v102,v80,v102,v79
v_fma_f32 v103,v80,v103,v79
v_fma_f32 v104,v80,v104,v79
v_fma_f32 v105,v80,v105,v79
v_mul_f32 v102,v38,v102
v_mul_f32 v103,v39,v103
v_mul_f32 v104,v40,v104
v_mul_f32 v105,v41,v105
v_exp_f32 v102,v102
v_exp_f32 v103,v103
v_exp_f32 v104,v104
v_exp_f32 v105,v105
v_fma_f32 v102,v102,v81,v81
v_fma_f32 v103,v103,v81,v81
v_fma_f32 v104,v104,v81,v81
v_fma_f32 v105,v105,v81,v81
v_rcp_f32 v102,v102
v_rcp_f32 v103,v103
v_rcp_f32 v104,v104
v_rcp_f32 v105,v105
v_mul_f32 v38,v38,v102
v_mul_f32 v39,v39,v103
v_mul_f32 v40,v40,v104
v_mul_f32 v41,v41,v105
v_cvt_pk_bf16_f32 v110,v38,v39
v_cvt_pk_bf16_f32 v111,v40,v41
buffer_store_dwordx2 v[110:111],v10,s[36:39],s67 offen offset:0 sc0 sc1
s_mov_b32 s72,s69
s_mov_b32 s69,0x7fff0000
s_mov_b32 s67,0x7fff0000
s_mov_b32 s77,0x7fff0000
s_branch Lg3_gox_%=
Lg3_xn1_%=:
s_cmp_ge_u32 s54,256
s_cbranch_scc1 Lg3_fin1_%=
s_add_u32 m0,s59,4096
s_nop 0
buffer_load_dword v8,s[36:39],s58 offen offset:0 sc1 lds
buffer_store_dword v4,v3,s[36:39],s71 offen offset:0 sc0 sc1
s_add_u32 m0,s59,5120
s_nop 0
buffer_load_dword v8,s[36:39],s58 offen offset:0 sc1 lds
buffer_store_dword v4,v3,s[36:39],s71 offen offset:0 sc0 sc1
s_mov_b32 s83,0
L 0
v_fma_f32 v30,-v78,v82,v30
v_fma_f32 v31,-v78,v83,v31
v_fma_f32 v32,-v78,v84,v32
v_fma_f32 v33,-v78,v85,v33
v_mul_f32 v94,v30,v30
v_mul_f32 v95,v31,v31
v_mul_f32 v96,v32,v32
v_mul_f32 v97,v33,v33
v_fma_f32 v94,v80,v94,v79
v_fma_f32 v95,v80,v95,v79
v_fma_f32 v96,v80,v96,v79
v_fma_f32 v97,v80,v97,v79
v_mul_f32 v94,v30,v94
v_mul_f32 v95,v31,v95
v_mul_f32 v96,v32,v96
v_mul_f32 v97,v33,v97
v_exp_f32 v94,v94
v_exp_f32 v95,v95
v_exp_f32 v96,v96
v_exp_f32 v97,v97
v_fma_f32 v94,v94,v81,v81
v_fma_f32 v95,v95,v81,v81
v_fma_f32 v96,v96,v81,v81
v_fma_f32 v97,v97,v81,v81
v_rcp_f32 v94,v94
v_rcp_f32 v95,v95
v_rcp_f32 v96,v96
v_rcp_f32 v97,v97
v_mul_f32 v30,v30,v94
v_mul_f32 v31,v31,v95
v_mul_f32 v32,v32,v96
v_mul_f32 v33,v33,v97
v_cvt_pk_bf16_f32 v106,v30,v31
v_cvt_pk_bf16_f32 v107,v32,v33
v_fma_f32 v34,-v78,v86,v34
v_fma_f32 v35,-v78,v87,v35
v_fma_f32 v36,-v78,v88,v36
v_fma_f32 v37,-v78,v89,v37
v_mul_f32 v98,v34,v34
v_mul_f32 v99,v35,v35
v_mul_f32 v100,v36,v36
v_mul_f32 v101,v37,v37
v_fma_f32 v98,v80,v98,v79
v_fma_f32 v99,v80,v99,v79
v_fma_f32 v100,v80,v100,v79
v_fma_f32 v101,v80,v101,v79
v_mul_f32 v98,v34,v98
v_mul_f32 v99,v35,v99
v_mul_f32 v100,v36,v100
v_mul_f32 v101,v37,v101
v_exp_f32 v98,v98
v_exp_f32 v99,v99
v_exp_f32 v100,v100
v_exp_f32 v101,v101
v_fma_f32 v98,v98,v81,v81
v_fma_f32 v99,v99,v81,v81
v_fma_f32 v100,v100,v81,v81
v_fma_f32 v101,v101,v81,v81
v_rcp_f32 v98,v98
v_rcp_f32 v99,v99
v_rcp_f32 v100,v100
v_rcp_f32 v101,v101
v_mul_f32 v34,v34,v98
v_mul_f32 v35,v35,v99
v_mul_f32 v36,v36,v100
v_mul_f32 v37,v37,v101
v_cvt_pk_bf16_f32 v108,v34,v35
v_cvt_pk_bf16_f32 v109,v36,v37
buffer_store_dwordx4 v[106:109],v0,s[36:39],s67 offen offset:0 sc0 sc1
v_fma_f32 v38,-v78,v90,v38
v_fma_f32 v39,-v78,v91,v39
v_fma_f32 v40,-v78,v92,v40
v_fma_f32 v41,-v78,v93,v41
v_mul_f32 v102,v38,v38
v_mul_f32 v103,v39,v39
)"
R"(v_mul_f32 v104,v40,v40
v_mul_f32 v105,v41,v41
v_fma_f32 v102,v80,v102,v79
v_fma_f32 v103,v80,v103,v79
v_fma_f32 v104,v80,v104,v79
v_fma_f32 v105,v80,v105,v79
v_mul_f32 v102,v38,v102
v_mul_f32 v103,v39,v103
v_mul_f32 v104,v40,v104
v_mul_f32 v105,v41,v105
v_exp_f32 v102,v102
v_exp_f32 v103,v103
v_exp_f32 v104,v104
v_exp_f32 v105,v105
v_fma_f32 v102,v102,v81,v81
v_fma_f32 v103,v103,v81,v81
v_fma_f32 v104,v104,v81,v81
v_fma_f32 v105,v105,v81,v81
v_rcp_f32 v102,v102
v_rcp_f32 v103,v103
v_rcp_f32 v104,v104
v_rcp_f32 v105,v105
v_mul_f32 v38,v38,v102
v_mul_f32 v39,v39,v103
v_mul_f32 v40,v40,v104
v_mul_f32 v41,v41,v105
v_cvt_pk_bf16_f32 v110,v38,v39
v_cvt_pk_bf16_f32 v111,v40,v41
buffer_store_dwordx2 v[110:111],v10,s[36:39],s67 offen offset:0 sc0 sc1
s_mov_b32 s72,s69
s_mov_b32 s69,0x7fff0000
s_mov_b32 s67,0x7fff0000
s_mov_b32 s77,0x7fff0000
s_branch Lg3_poll_%=
Lg3_fin1_%=:
L 0
v_fma_f32 v30,-v78,v82,v30
v_fma_f32 v31,-v78,v83,v31
v_fma_f32 v32,-v78,v84,v32
v_fma_f32 v33,-v78,v85,v33
v_mul_f32 v94,v30,v30
v_mul_f32 v95,v31,v31
v_mul_f32 v96,v32,v32
v_mul_f32 v97,v33,v33
v_fma_f32 v94,v80,v94,v79
v_fma_f32 v95,v80,v95,v79
v_fma_f32 v96,v80,v96,v79
v_fma_f32 v97,v80,v97,v79
v_mul_f32 v94,v30,v94
v_mul_f32 v95,v31,v95
v_mul_f32 v96,v32,v96
v_mul_f32 v97,v33,v97
v_exp_f32 v94,v94
v_exp_f32 v95,v95
v_exp_f32 v96,v96
v_exp_f32 v97,v97
v_fma_f32 v94,v94,v81,v81
v_fma_f32 v95,v95,v81,v81
v_fma_f32 v96,v96,v81,v81
v_fma_f32 v97,v97,v81,v81
v_rcp_f32 v94,v94
v_rcp_f32 v95,v95
v_rcp_f32 v96,v96
v_rcp_f32 v97,v97
v_mul_f32 v30,v30,v94
v_mul_f32 v31,v31,v95
v_mul_f32 v32,v32,v96
v_mul_f32 v33,v33,v97
v_cvt_pk_bf16_f32 v106,v30,v31
v_cvt_pk_bf16_f32 v107,v32,v33
v_fma_f32 v34,-v78,v86,v34
v_fma_f32 v35,-v78,v87,v35
v_fma_f32 v36,-v78,v88,v36
v_fma_f32 v37,-v78,v89,v37
v_mul_f32 v98,v34,v34
v_mul_f32 v99,v35,v35
v_mul_f32 v100,v36,v36
v_mul_f32 v101,v37,v37
v_fma_f32 v98,v80,v98,v79
v_fma_f32 v99,v80,v99,v79
v_fma_f32 v100,v80,v100,v79
v_fma_f32 v101,v80,v101,v79
v_mul_f32 v98,v34,v98
v_mul_f32 v99,v35,v99
v_mul_f32 v100,v36,v100
v_mul_f32 v101,v37,v101
v_exp_f32 v98,v98
v_exp_f32 v99,v99
v_exp_f32 v100,v100
v_exp_f32 v101,v101
v_fma_f32 v98,v98,v81,v81
v_fma_f32 v99,v99,v81,v81
v_fma_f32 v100,v100,v81,v81
v_fma_f32 v101,v101,v81,v81
v_rcp_f32 v98,v98
v_rcp_f32 v99,v99
v_rcp_f32 v100,v100
v_rcp_f32 v101,v101
v_mul_f32 v34,v34,v98
v_mul_f32 v35,v35,v99
v_mul_f32 v36,v36,v100
v_mul_f32 v37,v37,v101
v_cvt_pk_bf16_f32 v108,v34,v35
v_cvt_pk_bf16_f32 v109,v36,v37
buffer_store_dwordx4 v[106:109],v0,s[36:39],s67 offen offset:0 sc0 sc1
v_fma_f32 v38,-v78,v90,v38
v_fma_f32 v39,-v78,v91,v39
v_fma_f32 v40,-v78,v92,v40
v_fma_f32 v41,-v78,v93,v41
v_mul_f32 v102,v38,v38
v_mul_f32 v103,v39,v39
v_mul_f32 v104,v40,v40
v_mul_f32 v105,v41,v41
v_fma_f32 v102,v80,v102,v79
v_fma_f32 v103,v80,v103,v79
v_fma_f32 v104,v80,v104,v79
v_fma_f32 v105,v80,v105,v79
v_mul_f32 v102,v38,v102
v_mul_f32 v103,v39,v103
v_mul_f32 v104,v40,v104
v_mul_f32 v105,v41,v105
v_exp_f32 v102,v102
v_exp_f32 v103,v103
v_exp_f32 v104,v104
v_exp_f32 v105,v105
v_fma_f32 v102,v102,v81,v81
v_fma_f32 v103,v103,v81,v81
v_fma_f32 v104,v104,v81,v81
v_fma_f32 v105,v105,v81,v81
v_rcp_f32 v102,v102
v_rcp_f32 v103,v103
v_rcp_f32 v104,v104
v_rcp_f32 v105,v105
v_mul_f32 v38,v38,v102
v_mul_f32 v39,v39,v103
v_mul_f32 v40,v40,v104
v_mul_f32 v41,v41,v105
v_cvt_pk_bf16_f32 v110,v38,v39
v_cvt_pk_bf16_f32 v111,v40,v41
buffer_store_dwordx2 v[110:111],v10,s[36:39],s67 offen offset:0 sc0 sc1
s_mov_b32 s72,s69
s_mov_b32 s69,0x7fff0000
s_mov_b32 s67,0x7fff0000
s_mov_b32 s77,0x7fff0000
s_branch Lg3_done_%=
Lg3_done_%=:
s_waitcnt vmcnt(2)
s_barrier
buffer_store_dword v4,v3,s[36:39],s70 offen offset:0 sc0 sc1
s_waitcnt vmcnt(1)
s_barrier
buffer_store_dword v4,v3,s[36:39],s72 offen offset:0 sc0 sc1
s_waitcnt vmcnt(0) lgkmcnt(0)
s_barrier
s_endpgm
Lcode_g3_e:
.purgem M
.purgem R
.purgem T
.purgem D
.purgem L
)"
  :: "s"(ws), "s"(__builtin_amdgcn_readfirstlane(sWave)), "s"(__builtin_amdgcn_readfirstlane(sG0)), "s"(__builtin_amdgcn_readfirstlane(sStep)), "s"(__builtin_amdgcn_readfirstlane(sOutOff)), "s"(__builtin_amdgcn_readfirstlane(sFoutOff)), "s"(__builtin_amdgcn_readfirstlane(sAuxOff)), "s"(__builtin_amdgcn_readfirstlane(sWw)), "s"(__builtin_amdgcn_readfirstlane(sBuf)), "s"(__builtin_amdgcn_readfirstlane(sExp)), "v"(vL16), "v"(vFlag), "v"(vSnapOff), "v"(vSnapRd), "v"(vWd0), "v"(vWd1), "v"(vWr0), "v"(vWr1), "v"(vWr2), "v"(vOutS), "v"(vAux0), "v"(vAux1), "v"(vAux2), "v"(vAux3), "v"(vAn), "v"(vAe), "s"(w3), "s"(out) : "v255", "v0", "v1", "v2", "v3", "v4", "v5", "v6", "v7", "v8", "v9", "v10", "v11", "v12", "v13", "v14", "v15", "v16", "v17", "v18", "v19", "v20", "v21", "v22", "v23", "v24", "v25", "v26", "v27", "v28", "v29", "v30", "v31", "v32", "v33", "v34", "v35", "v36", "v37", "v38", "v39", "v40", "v41", "v42", "v43", "v44", "v45", "v46", "v47", "v48", "v49", "v50", "v51", "v52", "v53", "v54", "v55", "v56", "v57", "v58", "v59", "v60", "v61", "v62", "v63", "v64", "v65", "v66", "v67", "v68", "v69", "v70", "v71", "v72", "v73", "v74", "v75", "v76", "v77", "v78", "v79", "v80", "v81", "v82", "v83", "v84", "v85", "v86", "v87", "v88", "v89", "v90", "v91", "v92", "v93", "v94", "v95", "v96", "v97", "v98", "v99", "v100", "v101", "v102", "v103", "v104", "v105", "v106", "v107", "v108", "v109", "v110", "v111", "v112", "v113", "v114", "v115", "v116", "v117", "v118", "v119", "v120", "v121", "v122", "v123", "v124", "v125", "v126", "v127", "v128", "v129", "v130", "v131", "v132", "v133", "v134", "v135", "v136", "v137", "v138", "v139", "v140", "v141", "v142", "v143", "v144", "v145", "v146", "v147", "v148", "v149", "v150", "v151", "v152", "v153", "v154", "v155", "v156", "v157", "v158", "v159", "v160", "v161", "v162", "v163", "v164", "v165", "v166", "v167", "v168", "v169", "v170", "v171", "v172", "v173", "v174", "v175", "v176", "v177", "v178", "v179", "v180", "v181", "v182", "v183", "v184", "v185", "v186", "v187", "v188", "v189", "v190", "v191", "v192", "v193", "v194", "v195", "v196", "v197", "v198", "v199", "v200", "v201", "v202", "v203", "v204", "v205", "v206", "v207", "v208", "v209", "v210", "v211", "v212", "v213", "v214", "v215", "v216", "v217", "v218", "v219", "v220", "v221", "v222", "v223", "v224", "v225", "v226", "v227", "v228", "v229", "v230", "v231", "a0", "a1", "a2", "a3", "a4", "a5", "a6", "a7", "a8", "a9", "a10", "a11", "a12", "a13", "a14", "a15", "a16", "a17", "a18", "a19", "a20", "a21", "a22", "a23", "a24", "a25", "a26", "a27", "a28", "a29", "a30", "a31", "a32", "a33", "a34", "a35", "a36", "a37", "a38", "a39", "a40", "a41", "a42", "a43", "a44", "a45", "a46", "a47", "a48", "a49", "a50", "a51", "a52", "a53", "a54", "a55", "a56", "a57", "a58", "a59", "a60", "a61", "a62", "a63", "a64", "a65", "a66", "a67", "a68", "a69", "a70", "a71", "a72", "a73", "a74", "a75", "a76", "a77", "a78", "a79", "a80", "a81", "a82", "a83", "a84", "a85", "a86", "a87", "a88", "a89", "a90", "a91", "a92", "a93", "a94", "a95", "a96", "a97", "a98", "a99", "a100", "a101", "a102", "a103", "a104", "a105", "a106", "a107", "a108", "a109", "a110", "a111", "a112", "a113", "a114", "a115", "a116", "a117", "a118", "a119", "a120", "a121", "a122", "a123", "a124", "a125", "a126", "a127", "a128", "a129", "a130", "a131", "a132", "a133", "a134", "a135", "a136", "a137", "a138", "a139", "a140", "a141", "a142", "a143", "a144", "a145", "a146", "a147", "a148", "a149", "a150", "a151", "a152", "a153", "a154", "a155", "a156", "a157", "a158", "a159", "a160", "a161", "a162", "a163", "a164", "a165", "a166", "a167", "a168", "a169", "a170", "a171", "a172", "a173", "a174", "a175", "a176", "a177", "a178", "a179", "a180", "a181", "a182", "a183", "a184", "a185", "a186", "a187", "a188", "a189", "a190", "a191", "a192", "a193", "a194", "a195", "a196", "a197", "a198", "a199", "a200", "a201", "a202", "a203", "a204", "a205", "a206", "a207", "a208", "a209", "a210", "a211", "a212", "a213", "a214", "a215", "a216", "a217", "a218", "a219", "a220", "a221", "a222", "a223", "a224", "a225", "a226", "a227", "a228", "a229", "a230", "a231", "a232", "a233", "a234", "a235", "a236", "a237", "a238", "a239", "a240", "a241", "a242", "a243", "a244", "a245", "a246", "a247", "a248", "a249", "a250", "a251", "a252", "a253", "a254", "a255", "s36", "s37", "s38", "s39", "s40", "s41", "s42", "s43", "s44", "s45", "s46", "s47", "s48", "s49", "s50", "s51", "s52", "s53", "s54", "s55", "s56", "s57", "s58", "s59", "s60", "s61", "s62", "s63", "s64", "s65", "s66", "s67", "s68", "s69", "s70", "s71", "s72", "s73", "s74", "s75", "s76", "s77", "s78", "s79", "s80", "s81", "s82", "s83", "s84", "s85", "s86", "s87", "s88", "s89", "s90", "s91", "s92", "s93", "s94", "s95", "s96", "s97", "vcc", "scc", "memory"); } else { asm volatile(
R"(.macro M d,w,x
v_mfma_f32_16x16x32_bf16 v[\d:\d+3], \w, v[\x:\x+3], v[\d:\d+3]
.endm
.macro R d,a,o
ds_read_b128 v[\d:\d+3], v\a offset:\o
.endm
.macro T d,a,o
ds_read_b64_tr_b16 \d, v\a offset:\o
.endm
.macro D a,s,o
buffer_load_dwordx4 v\a, s[36:39], s\s offen offset:\o sc1 lds
.endm
.macro L c
s_waitcnt lgkmcnt(\c)
.endm
Lcode_g3o_b:
s_mov_b64 s[36:37],%0
s_mov_b32 s38,111243392
s_mov_b32 s39,0x00020000
s_mov_b32 s44,%1
s_mov_b32 s45,%2
s_mov_b32 s46,%3
s_mov_b32 s47,%4
s_mov_b32 s48,%5
s_mov_b32 s49,%6
s_mov_b32 s50,%7
s_mov_b32 s51,%8
s_mov_b32 s53,%9
v_mov_b32 v0,%10
v_mov_b32 v3,%11
v_mov_b32 v5,%12
v_mov_b32 v6,%13
v_mov_b32 v18,%14
v_mov_b32 v19,%15
v_mov_b32 v20,%16
v_mov_b32 v21,%17
v_mov_b32 v22,%18
v_mov_b32 v10,%19
v_mov_b32 v11,%20
v_mov_b32 v12,%21
v_mov_b32 v13,%22
v_mov_b32 v14,%23
v_mov_b32 v15,%24
v_mov_b32 v16,%25
s_mov_b64 s[40:41],%26
s_mov_b32 s42,7340032
s_mov_b32 s43,0x00020000
s_lshl_b32 s57,s44,11
s_add_u32 s55,s50,0
s_add_u32 m0,s57,0
s_nop 0
buffer_load_dwordx4 v18,s[40:43],s55 offen offset:0  lds
s_add_u32 m0,s57,1024
s_nop 0
buffer_load_dwordx4 v19,s[40:43],s55 offen offset:0  lds
s_add_u32 m0,s57,8064
s_nop 0
buffer_load_dwordx4 v18,s[40:43],s55 offen offset:128  lds
s_add_u32 m0,s57,9088
s_nop 0
buffer_load_dwordx4 v19,s[40:43],s55 offen offset:128  lds
s_add_u32 s55,s50,458752
s_add_u32 m0,s57,16384
s_nop 0
buffer_load_dwordx4 v18,s[40:43],s55 offen offset:0  lds
s_add_u32 m0,s57,17408
s_nop 0
buffer_load_dwordx4 v19,s[40:43],s55 offen offset:0  lds
s_add_u32 m0,s57,24448
s_nop 0
buffer_load_dwordx4 v18,s[40:43],s55 offen offset:128  lds
s_add_u32 m0,s57,25472
s_nop 0
buffer_load_dwordx4 v19,s[40:43],s55 offen offset:128  lds
s_add_u32 s55,s50,917504
s_add_u32 m0,s57,32768
s_nop 0
buffer_load_dwordx4 v18,s[40:43],s55 offen offset:0  lds
s_add_u32 m0,s57,33792
s_nop 0
buffer_load_dwordx4 v19,s[40:43],s55 offen offset:0  lds
s_add_u32 m0,s57,40832
s_nop 0
buffer_load_dwordx4 v18,s[40:43],s55 offen offset:128  lds
s_add_u32 m0,s57,41856
s_nop 0
buffer_load_dwordx4 v19,s[40:43],s55 offen offset:128  lds
s_add_u32 s55,s50,1376256
s_add_u32 m0,s57,49152
s_nop 0
buffer_load_dwordx4 v18,s[40:43],s55 offen offset:0  lds
s_add_u32 m0,s57,50176
s_nop 0
buffer_load_dwordx4 v19,s[40:43],s55 offen offset:0  lds
s_add_u32 m0,s57,57216
s_nop 0
buffer_load_dwordx4 v18,s[40:43],s55 offen offset:128  lds
s_add_u32 m0,s57,58240
s_nop 0
buffer_load_dwordx4 v19,s[40:43],s55 offen offset:128  lds
s_add_u32 s55,s50,1835008
s_add_u32 m0,s57,65536
s_nop 0
buffer_load_dwordx4 v18,s[40:43],s55 offen offset:0  lds
s_add_u32 m0,s57,66560
s_nop 0
buffer_load_dwordx4 v19,s[40:43],s55 offen offset:0  lds
s_add_u32 m0,s57,73600
s_nop 0
buffer_load_dwordx4 v18,s[40:43],s55 offen offset:128  lds
s_add_u32 m0,s57,74624
s_nop 0
buffer_load_dwordx4 v19,s[40:43],s55 offen offset:128  lds
s_add_u32 s55,s50,2293760
s_add_u32 m0,s57,81920
s_nop 0
buffer_load_dwordx4 v18,s[40:43],s55 offen offset:0  lds
s_add_u32 m0,s57,82944
s_nop 0
buffer_load_dwordx4 v19,s[40:43],s55 offen offset:0  lds
s_add_u32 m0,s57,89984
s_nop 0
buffer_load_dwordx4 v18,s[40:43],s55 offen offset:128  lds
s_add_u32 m0,s57,91008
s_nop 0
buffer_load_dwordx4 v19,s[40:43],s55 offen offset:128  lds
s_waitcnt vmcnt(20) lgkmcnt(0)
s_barrier
s_add_u32 s55,s50,2752512
s_add_u32 m0,s57,98304
s_nop 0
buffer_load_dwordx4 v18,s[40:43],s55 offen offset:0  lds
s_add_u32 m0,s57,99328
s_nop 0
buffer_load_dwordx4 v19,s[40:43],s55 offen offset:0  lds
s_add_u32 m0,s57,106368
s_nop 0
buffer_load_dwordx4 v18,s[40:43],s55 offen offset:128  lds
s_add_u32 m0,s57,107392
s_nop 0
buffer_load_dwordx4 v19,s[40:43],s55 offen offset:128  lds
v_add_u32 v26,0,v20
v_add_u32 v27,0,v21
T a[0:1],26,0
T a[2:3],26,512
T a[4:5],27,0
T a[6:7],27,512
T a[8:9],26,4096
T a[10:11],26,4608
T a[12:13],27,4096
T a[14:15],27,4608
s_waitcnt vmcnt(20) lgkmcnt(0)
s_barrier
s_add_u32 s55,s50,3211264
s_add_u32 m0,s57,0
s_nop 0
buffer_load_dwordx4 v18,s[40:43],s55 offen offset:0  lds
s_add_u32 m0,s57,1024
s_nop 0
buffer_load_dwordx4 v19,s[40:43],s55 offen offset:0  lds
s_add_u32 m0,s57,8064
s_nop 0
buffer_load_dwordx4 v18,s[40:43],s55 offen offset:128  lds
s_add_u32 m0,s57,9088
s_nop 0
buffer_load_dwordx4 v19,s[40:43],s55 offen offset:128  lds
v_add_u32 v26,16384,v20
v_add_u32 v27,16384,v21
T a[16:17],26,0
T a[18:19],26,512
T a[20:21],27,0
T a[22:23],27,512
T a[24:25],26,4096
T a[26:27],26,4608
T a[28:29],27,4096
T a[30:31],27,4608
s_waitcnt vmcnt(20) lgkmcnt(0)
s_barrier
s_add_u32 s55,s50,3670016
s_add_u32 m0,s57,16384
s_nop 0
buffer_load_dwordx4 v18,s[40:43],s55 offen offset:0  lds
s_add_u32 m0,s57,17408
s_nop 0
buffer_load_dwordx4 v19,s[40:43],s55 offen offset:0  lds
s_add_u32 m0,s57,24448
s_nop 0
buffer_load_dwordx4 v18,s[40:43],s55 offen offset:128  lds
s_add_u32 m0,s57,25472
s_nop 0
buffer_load_dwordx4 v19,s[40:43],s55 offen offset:128  lds
v_add_u32 v26,32768,v20
v_add_u32 v27,32768,v21
T a[32:33],26,0
T a[34:35],26,512
T a[36:37],27,0
T a[38:39],27,512
T a[40:41],26,4096
T a[42:43],26,4608
T a[44:45],27,4096
T a[46:47],27,4608
s_waitcnt vmcnt(20) lgkmcnt(0)
s_barrier
s_add_u32 s55,s50,4128768
s_add_u32 m0,s57,32768
s_nop 0
buffer_load_dwordx4 v18,s[40:43],s55 offen offset:0  lds
s_add_u32 m0,s57,33792
s_nop 0
buffer_load_dwordx4 v19,s[40:43],s55 offen offset:0  lds
s_add_u32 m0,s57,40832
s_nop 0
buffer_load_dwordx4 v18,s[40:43],s55 offen offset:128  lds
s_add_u32 m0,s57,41856
s_nop 0
buffer_load_dwordx4 v19,s[40:43],s55 offen offset:128  lds
v_add_u32 v26,49152,v20
v_add_u32 v27,49152,v21
T a[48:49],26,0
T a[50:51],26,512
T a[52:53],27,0
T a[54:55],27,512
T a[56:57],26,4096
T a[58:59],26,4608
T a[60:61],27,4096
T a[62:63],27,4608
s_waitcnt vmcnt(20) lgkmcnt(0)
s_barrier
s_add_u32 s55,s50,4587520
s_add_u32 m0,s57,49152
s_nop 0
buffer_load_dwordx4 v18,s[40:43],s55 offen offset:0  lds
s_add_u32 m0,s57,50176
s_nop 0
buffer_load_dwordx4 v19,s[40:43],s55 offen offset:0  lds
s_add_u32 m0,s57,57216
s_nop 0
buffer_load_dwordx4 v18,s[40:43],s55 offen offset:128  lds
s_add_u32 m0,s57,58240
s_nop 0
buffer_load_dwordx4 v19,s[40:43],s55 offen offset:128  lds
v_add_u32 v26,65536,v20
v_add_u32 v27,65536,v21
T a[64:65],26,0
T a[66:67],26,512
T a[68:69],27,0
T a[70:71],27,512
T a[72:73],26,4096
T a[74:75],26,4608
T a[76:77],27,4096
T a[78:79],27,4608
s_waitcnt vmcnt(20) lgkmcnt(0)
s_barrier
s_add_u32 s55,s50,5046272
s_add_u32 m0,s57,65536
s_nop 0
buffer_load_dwordx4 v18,s[40:43],s55 offen offset:0  lds
s_add_u32 m0,s57,66560
s_nop 0
buffer_load_dwordx4 v19,s[40:43],s55 offen offset:0  lds
s_add_u32 m0,s57,73600
s_nop 0
buffer_load_dwordx4 v18,s[40:43],s55 offen offset:128  lds
s_add_u32 m0,s57,74624
s_nop 0
buffer_load_dwordx4 v19,s[40:43],s55 offen offset:128  lds
v_add_u32 v26,81920,v20
v_add_u32 v27,81920,v21
T a[80:81],26,0
T a[82:83],26,512
T a[84:85],27,0
T a[86:87],27,512
T a[88:89],26,4096
T a[90:91],26,4608
T a[92:93],27,4096
T a[94:95],27,4608
s_waitcnt vmcnt(20) lgkmcnt(0)
s_barrier
s_add_u32 s55,s50,5505024
s_add_u32 m0,s57,81920
s_nop 0
buffer_load_dwordx4 v18,s[40:43],s55 offen offset:0  lds
s_add_u32 m0,s57,82944
s_nop 0
buffer_load_dwordx4 v19,s[40:43],s55 offen offset:0  lds
s_add_u32 m0,s57,89984
s_nop 0
buffer_load_dwordx4 v18,s[40:43],s55 offen offset:128  lds
s_add_u32 m0,s57,91008
s_nop 0
buffer_load_dwordx4 v19,s[40:43],s55 offen offset:128  lds
v_add_u32 v26,98304,v20
v_add_u32 v27,98304,v21
T a[96:97],26,0
T a[98:99],26,512
T a[100:101],27,0
T a[102:103],27,512
T a[104:105],26,4096
T a[106:107],26,4608
T a[108:109],27,4096
T a[110:111],27,4608
s_waitcnt vmcnt(20) lgkmcnt(0)
s_barrier
s_add_u32 s55,s50,5963776
s_add_u32 m0,s57,98304
s_nop 0
buffer_load_dwordx4 v18,s[40:43],s55 offen offset:0  lds
s_add_u32 m0,s57,99328
s_nop 0
buffer_load_dwordx4 v19,s[40:43],s55 offen offset:0  lds
s_add_u32 m0,s57,106368
s_nop 0
buffer_load_dwordx4 v18,s[40:43],s55 offen offset:128  lds
s_add_u32 m0,s57,107392
s_nop 0
buffer_load_dwordx4 v19,s[40:43],s55 offen offset:128  lds
v_add_u32 v26,0,v20
v_add_u32 v27,0,v21
T a[112:113],26,0
T a[114:115],26,512
T a[116:117],27,0
T a[118:119],27,512
T a[120:121],26,4096
T a[122:123],26,4608
T a[124:125],27,4096
T a[126:127],27,4608
s_waitcnt vmcnt(20) lgkmcnt(0)
s_barrier
s_add_u32 s55,s50,6422528
s_add_u32 m0,s57,0
s_nop 0
buffer_load_dwordx4 v18,s[40:43],s55 offen offset:0  lds
s_add_u32 m0,s57,1024
s_nop 0
buffer_load_dwordx4 v19,s[40:43],s55 offen offset:0  lds
s_add_u32 m0,s57,8064
s_nop 0
buffer_load_dwordx4 v18,s[40:43],s55 offen offset:128  lds
s_add_u32 m0,s57,9088
s_nop 0
buffer_load_dwordx4 v19,s[40:43],s55 offen offset:128  lds
v_add_u32 v26,16384,v20
v_add_u32 v27,16384,v21
T a[128:129],26,0
T a[130:131],26,512
T a[132:133],27,0
T a[134:135],27,512
T a[136:137],26,4096
T a[138:139],26,4608
T a[140:141],27,4096
T a[142:143],27,4608
s_waitcnt vmcnt(20) lgkmcnt(0)
s_barrier
s_add_u32 s55,s50,6881280
s_add_u32 m0,s57,16384
s_nop 0
buffer_load_dwordx4 v18,s[40:43],s55 offen offset:0  lds
s_add_u32 m0,s57,17408
s_nop 0
buffer_load_dwordx4 v19,s[40:43],s55 offen offset:0  lds
s_add_u32 m0,s57,24448
s_nop 0
buffer_load_dwordx4 v18,s[40:43],s55 offen offset:128  lds
s_add_u32 m0,s57,25472
s_nop 0
buffer_load_dwordx4 v19,s[40:43],s55 offen offset:128  lds
v_add_u32 v26,32768,v20
v_add_u32 v27,32768,v21
T a[144:145],26,0
T a[146:147],26,512
T a[148:149],27,0
T a[150:151],27,512
T a[152:153],26,4096
T a[154:155],26,4608
T a[156:157],27,4096
T a[158:159],27,4608
s_waitcnt vmcnt(20) lgkmcnt(0)
s_barrier
v_add_u32 v26,49152,v20
v_add_u32 v27,49152,v21
T a[160:161],26,0
T a[162:163],26,512
T a[164:165],27,0
T a[166:167],27,512
T a[168:169],26,4096
T a[170:171],26,4608
T a[172:173],27,4096
T a[174:175],27,4608
s_waitcnt vmcnt(16) lgkmcnt(0)
s_barrier
v_add_u32 v26,65536,v20
v_add_u32 v27,65536,v21
T a[176:177],26,0
T a[178:179],26,512
T a[180:181],27,0
T a[182:183],27,512
T a[184:185],26,4096
T a[186:187],26,4608
T a[188:189],27,4096
T a[190:191],27,4608
s_waitcnt vmcnt(12) lgkmcnt(0)
s_barrier
v_add_u32 v26,81920,v20
v_add_u32 v27,81920,v21
T a[192:193],26,0
T a[194:195],26,512
T a[196:197],27,0
T a[198:199],27,512
T a[200:201],26,4096
T a[202:203],26,4608
T a[204:205],27,4096
T a[206:207],27,4608
s_waitcnt vmcnt(8) lgkmcnt(0)
s_barrier
v_add_u32 v26,98304,v20
v_add_u32 v27,98304,v21
T a[208:209],26,0
T a[210:211],26,512
T a[212:213],27,0
T a[214:215],27,512
T a[216:217],26,4096
T a[218:219],26,4608
T a[220:221],27,4096
T a[222:223],27,4608
s_waitcnt vmcnt(4) lgkmcnt(0)
s_barrier
v_add_u32 v26,0,v20
v_add_u32 v27,0,v21
T a[224:225],26,0
T a[226:227],26,512
T a[228:229],27,0
T a[230:231],27,512
T a[232:233],26,4096
T a[234:235],26,4608
T a[236:237],27,4096
T a[238:239],27,4608
s_waitcnt vmcnt(0) lgkmcnt(0)
s_barrier
v_add_u32 v26,16384,v20
v_add_u32 v27,16384,v21
T a[240:241],26,0
T a[242:243],26,512
T a[244:245],27,0
T a[246:247],27,512
T a[248:249],26,4096
T a[250:251],26,4608
T a[252:253],27,4096
T a[254:255],27,4608
L 0
s_barrier
v_mov_b32 v34,0x3f803f80
v_mov_b32 v35,0x3f803f80
v_mov_b32 v36,0x3f803f80
v_mov_b32 v37,0x3f803f80
s_nop 1
v_mfma_f32_16x16x32_bf16 v[106:109],a[0:3],v[34:37],0
v_mfma_f32_16x16x32_bf16 v[110:113],a[4:7],v[34:37],0
M 106,a[8:11],34
M 110,a[12:15],34
M 106,a[16:19],34
M 110,a[20:23],34
M 106,a[24:27],34
M 110,a[28:31],34
M 106,a[32:35],34
M 110,a[36:39],34
M 106,a[40:43],34
M 110,a[44:47],34
M 106,a[48:51],34
M 110,a[52:55],34
M 106,a[56:59],34
M 110,a[60:63],34
M 106,a[64:67],34
M 110,a[68:71],34
M 106,a[72:75],34
M 110,a[76:79],34
M 106,a[80:83],34
M 110,a[84:87],34
M 106,a[88:91],34
M 110,a[92:95],34
M 106,a[96:99],34
M 110,a[100:103],34
M 106,a[104:107],34
M 110,a[108:111],34
M 106,a[112:115],34
M 110,a[116:119],34
M 106,a[120:123],34
M 110,a[124:127],34
M 106,a[128:131],34
M 110,a[132:135],34
M 106,a[136:139],34
M 110,a[140:143],34
M 106,a[144:147],34
M 110,a[148:151],34
M 106,a[152:155],34
M 110,a[156:159],34
M 106,a[160:163],34
M 110,a[164:167],34
M 106,a[168:171],34
M 110,a[172:175],34
M 106,a[176:179],34
)"
R"(M 110,a[180:183],34
M 106,a[184:187],34
M 110,a[188:191],34
M 106,a[192:195],34
M 110,a[196:199],34
M 106,a[200:203],34
M 110,a[204:207],34
M 106,a[208:211],34
M 110,a[212:215],34
M 106,a[216:219],34
M 110,a[220:223],34
M 106,a[224:227],34
M 110,a[228:231],34
M 106,a[232:235],34
M 110,a[236:239],34
M 106,a[240:243],34
M 110,a[244:247],34
M 106,a[248:251],34
M 110,a[252:255],34
v_mov_b32 v4,s53
s_mov_b32 s82,0
s_mov_b32 s80,0
s_mov_b32 s54,s45
s_mul_i32 s57,s44,4096
s_mul_i32 s55,s45,32768
s_add_u32 s55,s55,s57
s_add_u32 s55,s55,31457280
s_mul_i32 s57,s44,4096
s_mul_i32 s60,s46,32768
s_mul_i32 s61,s46,1024
s_mul_i32 s62,s46,114688
s_mul_i32 s63,s46,2560
s_mul_i32 s64,s46,1024
s_lshl_b32 s59,s44,8
s_add_u32 s59,s59,131072
s_mul_i32 s58,s45,1024
s_add_u32 s58,s58,109613056
s_mul_i32 s65,s45,114688
s_add_u32 s65,s65,s47
s_mul_i32 s68,s45,2560
s_add_u32 s68,s68,s48
s_mul_i32 s73,s45,1024
s_add_u32 s73,s73,107479040
s_mul_i32 s74,s45,32768
s_add_u32 s74,s74,s49
s_mul_i32 s75,s45,1024
s_add_u32 s75,s75,107479040
s_mov_b32 s66,0x7fff0000
s_mov_b32 s67,0x7fff0000
s_mov_b32 s69,0x7fff0000
s_mov_b32 s76,0x7fff0000
s_mov_b32 s77,0x7fff0000
s_mov_b32 s70,0x7fff0000
s_mov_b32 s71,0x7fff0000
s_mov_b32 s72,0x7fff0000
v_mov_b32 v1,v0
v_add_u32 v2,16384,v0
s_mov_b32 s84,16384
s_mov_b32 s86,0x3a800000
s_mov_b32 s87,0x3727c5ac
s_mov_b32 s88,0xc0135761
s_mov_b32 s89,0x3d372713
s_lshr_b32 s85,s44,1
s_mul_i32 s85,s85,127
v_lshlrev_b32 v8,22,v0
v_and_b32 v8,0x80000000,v8
v_or_b32 v8,v8,v5
v_add_u32 v2,32768,v0
s_lshl_b32 s84,s57,1
s_add_u32 s84,s84,32768
v_and_b32 v114,240,v0
v_cmp_eq_u32 vcc,0,v114
s_nop 1
v_cndmask_b32 v17,0,1.0,vcc
s_getpc_b64 s[40:41]
Lg3o_pc_%=:
s_lshl_b32 s78,s44,13
v_lshlrev_b32 v114,3,v0
v_add_u32 v114,s78,v114
v_min_u32 v114,Lcode_g3o_e-Lg3o_pc_%=-4,v114
s_mov_b32 m0,163584
s_nop 0
global_load_lds_dword v114,s[40:41]
s_add_u32 m0,s59,4096
s_nop 0
buffer_load_dword v8,s[36:39],s58 offen offset:0 sc1 lds
buffer_store_dword v4,v3,s[36:39],s71 offen offset:0 sc0 sc1
s_add_u32 m0,s59,5120
s_nop 0
buffer_load_dword v8,s[36:39],s58 offen offset:0 sc1 lds
buffer_store_dword v4,v3,s[36:39],s71 offen offset:0 sc0 sc1
s_mov_b32 s83,0
Lg3o_poll_%=:
s_add_u32 s78,s83,2
s_and_b32 s78,s78,3
s_lshl_b32 s78,s78,10
s_add_u32 s78,s78,4096
s_add_u32 m0,s59,s78
s_nop 0
buffer_load_dword v8,s[36:39],s58 offen offset:0 sc1 lds
s_and_b32 s78,s83,3
s_lshl_b32 s78,s78,10
v_add_u32 v114,s78,v6
s_waitcnt vmcnt(4)
s_barrier
ds_read_b32 v7,v114 offset:4096
buffer_store_dword v4,v3,s[36:39],s70 offen offset:0 sc0 sc1
s_mov_b32 s70,s71
s_mov_b32 s71,s72
s_mov_b32 s72,0x7fff0000
s_add_u32 s83,s83,1
s_add_u32 s82,s82,1
L 0
v_cmp_eq_u32 vcc,s53,v7
s_nop 0
s_cmp_eq_u32 vcc_lo,-1
s_cbranch_scc1 Lg3o_go_%=
s_cmp_lt_u32 s82,20000
s_cbranch_scc1 Lg3o_poll_%=
Lg3o_go_%=:
buffer_load_dword v130,v11,s[36:39],s73 offen offset:0 sc1
s_add_u32 s73,s73,s64
s_add_u32 m0,s57,0
s_add_u32 s56,s55,0
D 0,56,0
D 0,56,1024
D 0,56,2048
D 0,56,3072
s_add_u32 s58,s58,s61
s_add_u32 m0,s59,2048
s_nop 0
buffer_load_dword v8,s[36:39],s58 offen offset:0 sc1 lds
s_add_u32 m0,s57,16384
s_add_u32 s56,s55,16384
D 0,56,0
D 0,56,1024
D 0,56,2048
D 0,56,3072
s_add_u32 s55,s55,s60
s_sub_u32 s57,s84,s57
s_add_u32 s54,s54,s46
s_waitcnt vmcnt(4)
s_barrier
buffer_store_dword v4,v3,s[36:39],s70 offen offset:0 sc0 sc1
s_min_u32 s71,s71,s72
buffer_store_dword v4,v3,s[36:39],s71 offen offset:0 sc0 sc1
s_mov_b32 s70,0x7fff0000
s_mov_b32 s71,0x7fff0000
s_mov_b32 s72,0x7fff0000
s_cmp_eq_u32 s80,1
s_cbranch_scc1 Lg3o_st1_%=
s_branch Lg3o_st0_%=
Lg3o_gox_%=:
s_add_u32 m0,s59,2048
s_nop 0
buffer_load_dword v8,s[36:39],s58 offen offset:0 sc1 lds
s_waitcnt vmcnt(0)
s_barrier
buffer_store_dword v4,v3,s[36:39],s70 offen offset:0 sc0 sc1
s_min_u32 s71,s71,s72
buffer_store_dword v4,v3,s[36:39],s71 offen offset:0 sc0 sc1
s_mov_b32 s70,0x7fff0000
s_mov_b32 s71,0x7fff0000
s_mov_b32 s72,0x7fff0000
s_cmp_eq_u32 s80,1
s_cbranch_scc1 Lg3o_st1_%=
Lg3o_st0_%=:
ds_read_b32 v7,v6 offset:2048
R 34,1,0
R 38,1,1024
R 42,1,2048
R 46,1,3072
R 50,1,4096
L 5
v_cmp_eq_u32 vcc,s53,v7
L 3
s_cmp_eq_u32 vcc_lo,-1
s_cbranch_scc1 Lg3o_F0_%=
s_branch Lg3o_L0_%=
Lg3o_st1_%=:
ds_read_b32 v7,v6 offset:2048
R 34,2,0
R 38,2,1024
R 42,2,2048
R 46,2,3072
R 50,2,4096
L 5
v_cmp_eq_u32 vcc,s53,v7
L 3
s_cmp_eq_u32 vcc_lo,-1
s_cbranch_scc1 Lg3o_F1_%=
s_branch Lg3o_L1_%=
Lg3o_F0_%=:
v_mfma_f32_16x16x32_bf16 v[22:25],a[4:7],v[34:37],0
s_add_u32 s58,s58,s61
s_add_u32 m0,s59,0
R 54,1,5120
M 22,a[12:15],38
buffer_load_dword v8,s[36:39],s58 offen offset:0 sc1 lds
v_mfma_f32_16x16x32_bf16 v[18:21],a[0:3],v[34:37],0
s_add_u32 m0,s57,0
R 58,1,6144
v_add_f32_dpp v131,v130,v130 quad_perm:[1,0,3,2] row_mask:0xf bank_mask:0xf
M 18,a[8:11],38
L 3
s_add_u32 s56,s55,0
buffer_load_dword v130,v11,s[36:39],s73 offen offset:0 sc1
M 18,a[16:19],42
s_mov_b32 s66,s65
R 62,1,7168
v_add_f32_dpp v131,v131,v131 quad_perm:[2,3,0,1] row_mask:0xf bank_mask:0xf
M 18,a[24:27],46
D 0,56,0
M 22,a[20:23],42
s_add_u32 s65,s65,s62
R 66,1,8192
v_add_f32_dpp v134,v131,v131 row_half_mirror row_mask:0xf bank_mask:0xf
M 22,a[28:31],46
L 3
D 0,56,1024
M 22,a[36:39],50
s_add_u32 s54,s54,s46
R 70,1,9216
v_mov_b32_dpp v131,v134 row_ror:8 row_mask:0xf bank_mask:0xf
M 22,a[44:47],54
s_add_u32 s73,s73,s64
v_fma_f32 v131,-v134,v134,v131
v_max_f32 v131,s87,v131
M 18,a[32:35],50
R 74,1,10240
v_mul_f32 v134,v17,v134
M 18,a[40:43],54
L 3
D 0,56,2048
M 18,a[48:51],58
R 78,1,11264
v_fma_f32 v26,-v102,v106,v26
M 18,a[56:59],62
D 0,56,3072
M 22,a[52:55],58
s_add_u32 m0,s57,16384
R 82,1,12288
v_fma_f32 v27,-v102,v107,v27
M 22,a[60:63],62
L 3
v_rsq_f32 v132,v131
M 22,a[68:71],66
s_add_u32 s56,s55,16384
R 86,1,13312
v_fma_f32 v131,v15,v131,v16
M 22,a[76:79],70
s_add_u32 s55,s55,s60
v_mul_f32 v135,v132,v132
v_fma_f32 v131,v14,v135,v131
M 18,a[64:67],66
s_sub_u32 s57,s84,s57
R 90,1,14336
v_fma_f32 v134,v132,v131,v134
M 18,a[72:75],70
L 3
ds_write_b32 v12,v134 offset:0
M 18,a[80:83],74
s_waitcnt vmcnt(8)
R 94,1,15360
v_fma_f32 v28,-v102,v108,v28
M 18,a[88:91],78
s_barrier
v_fma_f32 v29,-v102,v109,v29
v_mul_f32 v114,v26,v26
M 22,a[84:87],74
R 34,1,16384
v_mul_f32 v115,v27,v27
M 22,a[92:95],78
L 4
v_mul_f32 v116,v28,v28
v_mul_f32 v117,v29,v29
M 22,a[100:103],82
R 38,1,17408
v_fma_f32 v114,v104,v114,v103
M 22,a[108:111],86
D 0,56,0
M 18,a[96:99],82
R 42,1,18432
v_fma_f32 v115,v104,v115,v103
M 18,a[104:107],86
L 3
v_fma_f32 v116,v104,v116,v103
v_fma_f32 v117,v104,v117,v103
M 18,a[112:115],90
R 46,1,19456
v_mul_f32 v114,v26,v114
M 18,a[120:123],94
v_exp_f32 v114,v114
M 22,a[116:119],90
R 50,1,20480
v_mul_f32 v115,v27,v115
M 22,a[124:127],94
L 3
v_exp_f32 v115,v115
M 22,a[132:135],34
R 54,1,21504
v_mul_f32 v116,v28,v116
M 22,a[140:143],38
D 0,56,1024
M 18,a[128:131],34
R 58,1,22528
v_mul_f32 v117,v29,v117
M 18,a[136:139],38
L 3
v_exp_f32 v116,v116
M 18,a[144:147],42
R 62,1,23552
v_fma_f32 v114,v114,v105,v105
M 18,a[152:155],46
v_exp_f32 v117,v117
M 22,a[148:151],42
R 66,1,24576
v_fma_f32 v115,v115,v105,v105
M 22,a[156:159],46
L 3
D 0,56,2048
M 22,a[164:167],50
R 70,1,25600
v_fma_f32 v116,v116,v105,v105
M 22,a[172:175],54
v_rcp_f32 v114,v114
M 18,a[160:163],50
R 74,1,26624
v_fma_f32 v117,v117,v105,v105
M 18,a[168:171],54
L 3
v_rcp_f32 v115,v115
M 18,a[176:179],58
R 78,1,27648
v_mul_f32 v26,v26,v114
M 18,a[184:187],62
v_rcp_f32 v116,v116
M 22,a[180:183],58
R 82,1,28672
v_mul_f32 v27,v27,v115
M 22,a[188:191],62
L 3
D 0,56,3072
M 22,a[196:199],66
R 86,1,29696
v_mul_f32 v28,v28,v116
M 22,a[204:207],70
v_rcp_f32 v117,v117
M 18,a[192:195],66
R 90,1,30720
v_mul_f32 v29,v29,v117
M 18,a[200:203],70
L 3
s_nop 0
v_cvt_pk_bf16_f32 v122,v26,v27
M 18,a[208:211],74
s_waitcnt vmcnt(4)
R 94,1,31744
v_fma_f32 v30,-v102,v110,v30
M 18,a[216:219],78
s_barrier
ds_read_b32 v7,v6 offset:0
R 98,13,0
M 22,a[212:215],74
R 34,2,0
v_fma_f32 v31,-v102,v111,v31
M 22,a[220:223],78
L 5
buffer_store_dword v4,v3,s[36:39],s70 offen offset:0 sc0 sc1
M 22,a[228:231],82
s_mov_b32 s70,s69
R 38,2,1024
v_fma_f32 v32,-v102,v112,v32
M 22,a[236:239],86
s_mov_b32 s69,s68
v_fma_f32 v33,-v102,v113,v33
v_mul_f32 v118,v30,v30
M 18,a[224:227],82
s_add_u32 s68,s68,s63
R 42,2,2048
v_mul_f32 v119,v31,v31
M 18,a[232:235],86
L 5
v_mul_f32 v120,v32,v32
v_mul_f32 v121,v33,v33
v_fma_f32 v118,v104,v118,v103
v_fma_f32 v119,v104,v119,v103
v_fma_f32 v120,v104,v120,v103
v_fma_f32 v121,v104,v121,v103
v_mul_f32 v118,v30,v118
v_mul_f32 v119,v31,v119
v_mul_f32 v120,v32,v120
v_mul_f32 v121,v33,v121
v_exp_f32 v118,v118
v_exp_f32 v119,v119
v_exp_f32 v120,v120
v_exp_f32 v121,v121
M 18,a[240:243],90
L 4
R 46,2,3072
v_fma_f32 v118,v118,v105,v105
v_fma_f32 v119,v119,v105,v105
v_fma_f32 v120,v120,v105,v105
v_fma_f32 v121,v121,v105,v105
v_rcp_f32 v118,v118
v_rcp_f32 v119,v119
v_rcp_f32 v120,v120
v_rcp_f32 v121,v121
v_cmp_eq_u32 vcc,s53,v7
M 18,a[248:251],94
v_cvt_pk_bf16_f32 v123,v28,v29
v_mul_f32 v30,v30,v118
v_mul_f32 v31,v31,v119
v_mul_f32 v32,v32,v120
v_mul_f32 v33,v33,v121
v_cvt_pk_bf16_f32 v124,v30,v31
v_cvt_pk_bf16_f32 v125,v32,v33
M 22,a[244:247],90
L 2
s_cmp_eq_u32 vcc_lo,-1
R 50,2,4096
buffer_store_dwordx4 v[122:125],v0,s[36:39],s67 offen offset:0 sc0 sc1
M 22,a[252:255],94
s_cbranch_scc0 Lg3o_K1_%=
Lg3o_F1_%=:
v_mfma_f32_16x16x32_bf16 v[30:33],a[4:7],v[34:37],0
s_add_u32 s58,s58,s61
s_add_u32 m0,s59,1024
R 54,2,5120
M 30,a[12:15],38
buffer_load_dword v8,s[36:39],s58 offen offset:0 sc1 lds
v_mfma_f32_16x16x32_bf16 v[26:29],a[0:3],v[34:37],0
s_add_u32 m0,s57,0
R 58,2,6144
v_add_f32_dpp v131,v130,v130 quad_perm:[1,0,3,2] row_mask:0xf bank_mask:0xf
M 26,a[8:11],38
L 3
s_add_u32 s56,s55,0
buffer_load_dword v130,v11,s[36:39],s73 offen offset:0 sc1
M 26,a[16:19],42
s_mov_b32 s67,s65
R 62,2,7168
v_add_f32_dpp v131,v131,v131 quad_perm:[2,3,0,1] row_mask:0xf bank_mask:0xf
M 26,a[24:27],46
D 0,56,0
M 30,a[20:23],42
s_add_u32 s65,s65,s62
R 66,2,8192
v_add_f32_dpp v134,v131,v131 row_half_mirror row_mask:0xf bank_mask:0xf
M 30,a[28:31],46
L 3
D 0,56,1024
M 30,a[36:39],50
s_add_u32 s54,s54,s46
R 70,2,9216
v_mov_b32_dpp v131,v134 row_ror:8 row_mask:0xf bank_mask:0xf
M 30,a[44:47],54
s_add_u32 s73,s73,s64
v_fma_f32 v131,-v134,v134,v131
v_max_f32 v131,s87,v131
M 26,a[32:35],50
R 74,2,10240
v_mul_f32 v134,v17,v134
M 26,a[40:43],54
L 3
D 0,56,2048
M 26,a[48:51],58
R 78,2,11264
v_fma_f32 v18,-v98,v106,v18
M 26,a[56:59],62
D 0,56,3072
M 30,a[52:55],58
s_add_u32 m0,s57,16384
R 82,2,12288
v_fma_f32 v19,-v98,v107,v19
M 30,a[60:63],62
L 3
v_rsq_f32 v132,v131
M 30,a[68:71],66
s_add_u32 s56,s55,16384
R 86,2,13312
v_fma_f32 v131,v15,v131,v16
M 30,a[76:79],70
s_add_u32 s55,s55,s60
v_mul_f32 v135,v132,v132
v_fma_f32 v131,v14,v135,v131
M 26,a[64:67],66
s_sub_u32 s57,s84,s57
R 90,2,14336
v_fma_f32 v134,v132,v131,v134
M 26,a[72:75],70
L 3
ds_write_b32 v12,v134 offset:2048
M 26,a[80:83],74
s_waitcnt vmcnt(8)
R 94,2,15360
v_fma_f32 v20,-v98,v108,v20
M 26,a[88:91],78
s_barrier
v_fma_f32 v21,-v98,v109,v21
)"
R"(v_mul_f32 v114,v18,v18
M 30,a[84:87],74
R 34,2,16384
v_mul_f32 v115,v19,v19
M 30,a[92:95],78
L 4
v_mul_f32 v116,v20,v20
v_mul_f32 v117,v21,v21
M 30,a[100:103],82
R 38,2,17408
v_fma_f32 v114,v100,v114,v99
M 30,a[108:111],86
D 0,56,0
M 26,a[96:99],82
R 42,2,18432
v_fma_f32 v115,v100,v115,v99
M 26,a[104:107],86
L 3
v_fma_f32 v116,v100,v116,v99
v_fma_f32 v117,v100,v117,v99
M 26,a[112:115],90
R 46,2,19456
v_mul_f32 v114,v18,v114
M 26,a[120:123],94
v_exp_f32 v114,v114
M 30,a[116:119],90
R 50,2,20480
v_mul_f32 v115,v19,v115
M 30,a[124:127],94
L 3
v_exp_f32 v115,v115
M 30,a[132:135],34
R 54,2,21504
v_mul_f32 v116,v20,v116
M 30,a[140:143],38
D 0,56,1024
M 26,a[128:131],34
R 58,2,22528
v_mul_f32 v117,v21,v117
M 26,a[136:139],38
L 3
v_exp_f32 v116,v116
M 26,a[144:147],42
R 62,2,23552
v_fma_f32 v114,v114,v101,v101
M 26,a[152:155],46
v_exp_f32 v117,v117
M 30,a[148:151],42
R 66,2,24576
v_fma_f32 v115,v115,v101,v101
M 30,a[156:159],46
L 3
D 0,56,2048
M 30,a[164:167],50
R 70,2,25600
v_fma_f32 v116,v116,v101,v101
M 30,a[172:175],54
v_rcp_f32 v114,v114
M 26,a[160:163],50
R 74,2,26624
v_fma_f32 v117,v117,v101,v101
M 26,a[168:171],54
L 3
v_rcp_f32 v115,v115
M 26,a[176:179],58
R 78,2,27648
v_mul_f32 v18,v18,v114
M 26,a[184:187],62
v_rcp_f32 v116,v116
M 30,a[180:183],58
R 82,2,28672
v_mul_f32 v19,v19,v115
M 30,a[188:191],62
L 3
D 0,56,3072
M 30,a[196:199],66
R 86,2,29696
v_mul_f32 v20,v20,v116
M 30,a[204:207],70
v_rcp_f32 v117,v117
M 26,a[192:195],66
R 90,2,30720
v_mul_f32 v21,v21,v117
M 26,a[200:203],70
L 3
s_nop 0
v_cvt_pk_bf16_f32 v122,v18,v19
M 26,a[208:211],74
s_waitcnt vmcnt(4)
R 94,2,31744
v_fma_f32 v22,-v98,v110,v22
M 26,a[216:219],78
s_barrier
ds_read_b32 v7,v6 offset:1024
R 102,13,2048
M 30,a[212:215],74
R 34,1,0
v_fma_f32 v23,-v98,v111,v23
M 30,a[220:223],78
L 5
buffer_store_dword v4,v3,s[36:39],s70 offen offset:0 sc0 sc1
M 30,a[228:231],82
s_mov_b32 s70,s69
R 38,1,1024
v_fma_f32 v24,-v98,v112,v24
M 30,a[236:239],86
s_mov_b32 s69,s68
v_fma_f32 v25,-v98,v113,v25
v_mul_f32 v118,v22,v22
M 26,a[224:227],82
s_add_u32 s68,s68,s63
R 42,1,2048
v_mul_f32 v119,v23,v23
M 26,a[232:235],86
L 5
v_mul_f32 v120,v24,v24
v_mul_f32 v121,v25,v25
v_fma_f32 v118,v100,v118,v99
v_fma_f32 v119,v100,v119,v99
v_fma_f32 v120,v100,v120,v99
v_fma_f32 v121,v100,v121,v99
v_mul_f32 v118,v22,v118
v_mul_f32 v119,v23,v119
v_mul_f32 v120,v24,v120
v_mul_f32 v121,v25,v121
v_exp_f32 v118,v118
v_exp_f32 v119,v119
v_exp_f32 v120,v120
v_exp_f32 v121,v121
M 26,a[240:243],90
L 4
R 46,1,3072
v_fma_f32 v118,v118,v101,v101
v_fma_f32 v119,v119,v101,v101
v_fma_f32 v120,v120,v101,v101
v_fma_f32 v121,v121,v101,v101
v_rcp_f32 v118,v118
v_rcp_f32 v119,v119
v_rcp_f32 v120,v120
v_rcp_f32 v121,v121
v_cmp_eq_u32 vcc,s53,v7
M 26,a[248:251],94
v_cvt_pk_bf16_f32 v123,v20,v21
v_mul_f32 v22,v22,v118
v_mul_f32 v23,v23,v119
v_mul_f32 v24,v24,v120
v_mul_f32 v25,v25,v121
v_cvt_pk_bf16_f32 v124,v22,v23
v_cvt_pk_bf16_f32 v125,v24,v25
M 30,a[244:247],90
L 2
s_cmp_eq_u32 vcc_lo,-1
R 50,1,4096
buffer_store_dwordx4 v[122:125],v0,s[36:39],s66 offen offset:0 sc0 sc1
M 30,a[252:255],94
s_cbranch_scc1 Lg3o_F0_%=
Lg3o_K0_%=:
Lg3o_L0_%=:
v_mfma_f32_16x16x32_bf16 v[22:25],a[4:7],v[34:37],0
s_add_u32 m0,s59,3072
R 54,1,5120
v_add_f32_dpp v131,v130,v130 quad_perm:[1,0,3,2] row_mask:0xf bank_mask:0xf
M 22,a[12:15],38
buffer_load_dword v8,s[36:39],s58 offen offset:0 sc1 lds
v_mfma_f32_16x16x32_bf16 v[18:21],a[0:3],v[34:37],0
s_mov_b32 s66,s65
R 58,1,6144
v_add_f32_dpp v131,v131,v131 quad_perm:[2,3,0,1] row_mask:0xf bank_mask:0xf
M 18,a[8:11],38
L 3
v_fma_f32 v26,-v102,v106,v26
v_fma_f32 v27,-v102,v107,v27
M 18,a[16:19],42
s_add_u32 s65,s65,s62
R 62,1,7168
v_add_f32_dpp v134,v131,v131 row_half_mirror row_mask:0xf bank_mask:0xf
M 18,a[24:27],46
v_fma_f32 v28,-v102,v108,v28
v_fma_f32 v29,-v102,v109,v29
M 22,a[20:23],42
R 66,1,8192
v_mov_b32_dpp v131,v134 row_ror:8 row_mask:0xf bank_mask:0xf
M 22,a[28:31],46
L 3
v_fma_f32 v131,-v134,v134,v131
v_max_f32 v131,s87,v131
M 22,a[36:39],50
R 70,1,9216
v_mul_f32 v134,v17,v134
M 22,a[44:47],54
v_rsq_f32 v132,v131
M 18,a[32:35],50
R 74,1,10240
v_fma_f32 v131,v15,v131,v16
M 18,a[40:43],54
L 3
v_mul_f32 v135,v132,v132
v_fma_f32 v131,v14,v135,v131
M 18,a[48:51],58
R 78,1,11264
v_fma_f32 v134,v132,v131,v134
M 18,a[56:59],62
ds_write_b32 v12,v134 offset:0
M 22,a[52:55],58
R 82,1,12288
v_mul_f32 v114,v26,v26
M 22,a[60:63],62
L 4
v_mul_f32 v115,v27,v27
v_mul_f32 v116,v28,v28
M 22,a[68:71],66
R 86,1,13312
v_mul_f32 v117,v29,v29
M 22,a[76:79],70
v_fma_f32 v114,v104,v114,v103
v_fma_f32 v115,v104,v115,v103
M 18,a[64:67],66
R 90,1,14336
v_fma_f32 v116,v104,v116,v103
M 18,a[72:75],70
L 4
v_fma_f32 v117,v104,v117,v103
v_mul_f32 v114,v26,v114
M 18,a[80:83],74
s_waitcnt vmcnt(0)
R 94,1,15360
v_mul_f32 v115,v27,v115
M 18,a[88:91],78
s_barrier
ds_read_b32 v7,v6 offset:3072
v_mul_f32 v116,v28,v116
M 22,a[84:87],74
R 34,1,16384
v_mul_f32 v117,v29,v117
M 22,a[92:95],78
L 4
buffer_store_dword v4,v3,s[36:39],s70 offen offset:0 sc0 sc1
M 22,a[100:103],82
s_mov_b32 s70,0x7fff0000
R 38,1,17408
v_fma_f32 v30,-v102,v110,v30
M 22,a[108:111],86
v_exp_f32 v114,v114
M 18,a[96:99],82
R 42,1,18432
v_fma_f32 v114,v114,v105,v105
M 18,a[104:107],86
L 4
L 3
v_cmp_eq_u32 vcc,s53,v7
M 18,a[112:115],90
R 46,1,19456
v_fma_f32 v31,-v102,v111,v31
M 18,a[120:123],94
v_exp_f32 v115,v115
M 22,a[116:119],90
s_cmp_eq_u32 vcc_lo,-1
R 50,1,20480
v_fma_f32 v115,v115,v105,v105
s_cbranch_scc0 Lg3o_X0_%=
M 22,a[124:127],94
L 3
s_add_u32 m0,s57,0
s_add_u32 s56,s55,0
buffer_load_dword v130,v11,s[36:39],s73 offen offset:0 sc1
M 22,a[132:135],34
s_add_u32 s58,s58,s61
R 54,1,21504
v_fma_f32 v32,-v102,v112,v32
M 22,a[140:143],38
D 0,56,0
M 18,a[128:131],34
s_add_u32 s54,s54,s46
R 58,1,22528
v_fma_f32 v33,-v102,v113,v33
M 18,a[136:139],38
L 3
v_exp_f32 v116,v116
M 18,a[144:147],42
s_add_u32 s73,s73,s64
R 62,1,23552
v_fma_f32 v116,v116,v105,v105
M 18,a[152:155],46
D 0,56,1024
M 22,a[148:151],42
R 66,1,24576
v_mul_f32 v118,v30,v30
M 22,a[156:159],46
L 3
v_exp_f32 v117,v117
M 22,a[164:167],50
R 70,1,25600
v_fma_f32 v117,v117,v105,v105
M 22,a[172:175],54
D 0,56,2048
M 18,a[160:163],50
R 74,1,26624
v_mul_f32 v119,v31,v31
M 18,a[168:171],54
L 3
v_rcp_f32 v114,v114
M 18,a[176:179],58
R 78,1,27648
v_mul_f32 v26,v26,v114
M 18,a[184:187],62
D 0,56,3072
M 22,a[180:183],58
s_add_u32 m0,s59,0
R 82,1,28672
v_mul_f32 v120,v32,v32
M 22,a[188:191],62
L 3
buffer_load_dword v8,s[36:39],s58 offen offset:0 sc1 lds
M 22,a[196:199],66
s_add_u32 m0,s57,16384
R 86,1,29696
v_mul_f32 v121,v33,v33
M 22,a[204:207],70
s_add_u32 s56,s55,16384
v_rcp_f32 v115,v115
M 18,a[192:195],66
s_add_u32 s55,s55,s60
R 90,1,30720
v_mul_f32 v27,v27,v115
M 18,a[200:203],70
L 3
s_nop 0
D 0,56,0
M 18,a[208:211],74
s_waitcnt vmcnt(1)
R 94,1,31744
v_cvt_pk_bf16_f32 v122,v26,v27
M 18,a[216:219],78
s_barrier
ds_read_b32 v7,v6 offset:0
R 98,13,0
M 22,a[212:215],74
s_sub_u32 s57,s84,s57
R 34,2,0
v_fma_f32 v118,v104,v118,v103
M 22,a[220:223],78
L 5
buffer_store_dword v4,v3,s[36:39],s70 offen offset:0 sc0 sc1
M 22,a[228:231],82
s_mov_b32 s70,s69
R 38,2,1024
v_fma_f32 v119,v104,v119,v103
M 22,a[236:239],86
D 0,56,1024
M 18,a[224:227],82
s_mov_b32 s69,s68
R 42,2,2048
v_fma_f32 v120,v104,v120,v103
M 18,a[232:235],86
L 5
v_fma_f32 v121,v104,v121,v103
v_mul_f32 v118,v30,v118
v_mul_f32 v119,v31,v119
v_mul_f32 v120,v32,v120
v_mul_f32 v121,v33,v121
v_exp_f32 v118,v118
v_exp_f32 v119,v119
v_exp_f32 v120,v120
v_exp_f32 v121,v121
M 18,a[240:243],90
L 4
R 46,2,3072
v_rcp_f32 v116,v116
v_rcp_f32 v117,v117
v_fma_f32 v118,v118,v105,v105
v_fma_f32 v119,v119,v105,v105
v_fma_f32 v120,v120,v105,v105
v_fma_f32 v121,v121,v105,v105
v_rcp_f32 v118,v118
v_rcp_f32 v119,v119
v_rcp_f32 v120,v120
v_rcp_f32 v121,v121
v_cmp_eq_u32 vcc,s53,v7
M 18,a[248:251],94
s_add_u32 s68,s68,s63
v_mul_f32 v28,v28,v116
v_mul_f32 v29,v29,v117
v_cvt_pk_bf16_f32 v123,v28,v29
v_mul_f32 v30,v30,v118
v_mul_f32 v31,v31,v119
v_mul_f32 v32,v32,v120
v_mul_f32 v33,v33,v121
v_cvt_pk_bf16_f32 v124,v30,v31
v_cvt_pk_bf16_f32 v125,v32,v33
M 22,a[244:247],90
L 2
s_cmp_eq_u32 vcc_lo,-1
R 50,2,4096
buffer_store_dwordx4 v[122:125],v0,s[36:39],s67 offen offset:0 sc0 sc1
M 22,a[252:255],94
D 0,56,2048
D 0,56,3072
buffer_store_dword v4,v3,s[36:39],s71 offen offset:0 sc0 sc1
buffer_store_dword v4,v3,s[36:39],s71 offen offset:0 sc0 sc1
s_cbranch_scc1 Lg3o_F1_%=
s_branch Lg3o_K1_%=
Lg3o_X0_%=:
M 22,a[124:127],94
L 3
s_add_u32 m0,s59,0
v_fma_f32 v32,-v102,v112,v32
M 22,a[132:135],34
R 54,1,21504
v_fma_f32 v33,-v102,v113,v33
M 22,a[140:143],38
buffer_load_dword v8,s[36:39],s58 offen offset:0 sc1 lds
M 18,a[128:131],34
R 58,1,22528
v_mul_f32 v118,v30,v30
M 18,a[136:139],38
L 3
v_exp_f32 v116,v116
M 18,a[144:147],42
R 62,1,23552
v_fma_f32 v116,v116,v105,v105
M 18,a[152:155],46
v_exp_f32 v117,v117
M 22,a[148:151],42
R 66,1,24576
v_fma_f32 v117,v117,v105,v105
M 22,a[156:159],46
L 3
v_rcp_f32 v114,v114
v_mul_f32 v119,v31,v31
M 22,a[164:167],50
R 70,1,25600
v_mul_f32 v26,v26,v114
M 22,a[172:175],54
v_rcp_f32 v115,v115
M 18,a[160:163],50
R 74,1,26624
v_mul_f32 v27,v27,v115
M 18,a[168:171],54
L 3
v_rcp_f32 v116,v116
M 18,a[176:179],58
R 78,1,27648
v_mul_f32 v28,v28,v116
M 18,a[184:187],62
v_rcp_f32 v117,v117
M 22,a[180:183],58
R 82,1,28672
v_mul_f32 v29,v29,v117
M 22,a[188:191],62
L 3
v_cvt_pk_bf16_f32 v122,v26,v27
v_cvt_pk_bf16_f32 v123,v28,v29
M 22,a[196:199],66
R 86,1,29696
v_mul_f32 v120,v32,v32
M 22,a[204:207],70
v_mul_f32 v121,v33,v33
v_fma_f32 v118,v104,v118,v103
M 18,a[192:195],66
R 90,1,30720
v_fma_f32 v119,v104,v119,v103
M 18,a[200:203],70
L 3
s_nop 0
v_fma_f32 v120,v104,v120,v103
M 18,a[208:211],74
s_waitcnt vmcnt(0)
R 94,1,31744
v_fma_f32 v121,v104,v121,v103
M 18,a[216:219],78
s_barrier
buffer_store_dword v4,v3,s[36:39],s70 offen offset:0 sc0 sc1
M 22,a[212:215],74
s_mov_b32 s70,s69
ds_read_b32 v7,v6 offset:0
R 98,13,0
M 22,a[220:223],78
L 4
v_mul_f32 v118,v30,v118
v_mul_f32 v119,v31,v119
M 22,a[228:231],82
s_mov_b32 s69,s68
v_mul_f32 v120,v32,v120
v_mul_f32 v121,v33,v121
M 22,a[236:239],86
s_add_u32 s68,s68,s63
v_exp_f32 v118,v118
M 18,a[224:227],82
v_exp_f32 v119,v119
M 18,a[232:235],86
L 2
v_exp_f32 v120,v120
v_exp_f32 v121,v121
M 18,a[240:243],90
L 1
v_fma_f32 v118,v118,v105,v105
v_fma_f32 v119,v119,v105,v105
v_fma_f32 v120,v120,v105,v105
v_fma_f32 v121,v121,v105,v105
v_rcp_f32 v118,v118
)"
R"(v_rcp_f32 v119,v119
v_rcp_f32 v120,v120
v_rcp_f32 v121,v121
M 18,a[248:251],94
v_mul_f32 v30,v30,v118
v_mul_f32 v31,v31,v119
v_mul_f32 v32,v32,v120
v_mul_f32 v33,v33,v121
v_cvt_pk_bf16_f32 v124,v30,v31
v_cvt_pk_bf16_f32 v125,v32,v33
M 22,a[244:247],90
buffer_store_dwordx4 v[122:125],v0,s[36:39],s67 offen offset:0 sc0 sc1
v_cmp_eq_u32 vcc,s53,v7
M 22,a[252:255],94
s_mov_b32 s80,1
s_cmp_eq_u32 vcc_lo,-1
s_cbranch_scc0 Lg3o_xn0_%=
buffer_load_dword v130,v11,s[36:39],s73 offen offset:0 sc1
L 0
v_fma_f32 v18,-v98,v106,v18
v_fma_f32 v19,-v98,v107,v19
v_fma_f32 v20,-v98,v108,v20
v_fma_f32 v21,-v98,v109,v21
v_mul_f32 v114,v18,v18
v_mul_f32 v115,v19,v19
s_add_u32 s73,s73,s64
s_add_u32 m0,s57,0
s_add_u32 s56,s55,0
D 0,56,0
v_mul_f32 v116,v20,v20
v_mul_f32 v117,v21,v21
v_fma_f32 v114,v100,v114,v99
v_fma_f32 v115,v100,v115,v99
v_fma_f32 v116,v100,v116,v99
v_fma_f32 v117,v100,v117,v99
D 0,56,1024
v_mul_f32 v114,v18,v114
v_mul_f32 v115,v19,v115
v_mul_f32 v116,v20,v116
v_mul_f32 v117,v21,v117
v_exp_f32 v114,v114
v_exp_f32 v115,v115
D 0,56,2048
v_exp_f32 v116,v116
v_exp_f32 v117,v117
v_fma_f32 v114,v114,v101,v101
v_fma_f32 v115,v115,v101,v101
v_fma_f32 v116,v116,v101,v101
v_fma_f32 v117,v117,v101,v101
D 0,56,3072
v_rcp_f32 v114,v114
v_rcp_f32 v115,v115
v_rcp_f32 v116,v116
v_rcp_f32 v117,v117
v_mul_f32 v18,v18,v114
v_mul_f32 v19,v19,v115
s_add_u32 s58,s58,s61
s_add_u32 m0,s57,16384
s_add_u32 s56,s55,16384
D 0,56,0
v_mul_f32 v20,v20,v116
v_mul_f32 v21,v21,v117
v_cvt_pk_bf16_f32 v122,v18,v19
v_cvt_pk_bf16_f32 v123,v20,v21
v_fma_f32 v22,-v98,v110,v22
v_fma_f32 v23,-v98,v111,v23
D 0,56,1024
v_fma_f32 v24,-v98,v112,v24
v_fma_f32 v25,-v98,v113,v25
v_mul_f32 v118,v22,v22
v_mul_f32 v119,v23,v23
v_mul_f32 v120,v24,v24
v_mul_f32 v121,v25,v25
D 0,56,2048
v_fma_f32 v118,v100,v118,v99
v_fma_f32 v119,v100,v119,v99
v_fma_f32 v120,v100,v120,v99
v_fma_f32 v121,v100,v121,v99
v_mul_f32 v118,v22,v118
v_mul_f32 v119,v23,v119
D 0,56,3072
v_mul_f32 v120,v24,v120
v_mul_f32 v121,v25,v121
v_exp_f32 v118,v118
v_exp_f32 v119,v119
v_exp_f32 v120,v120
v_exp_f32 v121,v121
s_add_u32 s55,s55,s60
s_sub_u32 s57,s84,s57
s_add_u32 s54,s54,s46
v_fma_f32 v118,v118,v101,v101
v_fma_f32 v119,v119,v101,v101
v_fma_f32 v120,v120,v101,v101
v_fma_f32 v121,v121,v101,v101
v_rcp_f32 v118,v118
v_rcp_f32 v119,v119
v_rcp_f32 v120,v120
v_rcp_f32 v121,v121
v_mul_f32 v22,v22,v118
v_mul_f32 v23,v23,v119
v_mul_f32 v24,v24,v120
v_mul_f32 v25,v25,v121
v_cvt_pk_bf16_f32 v124,v22,v23
v_cvt_pk_bf16_f32 v125,v24,v25
buffer_store_dwordx4 v[122:125],v0,s[36:39],s66 offen offset:0 sc0 sc1
s_mov_b32 s72,s69
s_mov_b32 s69,0x7fff0000
s_mov_b32 s66,0x7fff0000
s_mov_b32 s76,0x7fff0000
s_branch Lg3o_gox_%=
Lg3o_xn0_%=:
s_cmp_ge_u32 s54,256
s_cbranch_scc1 Lg3o_fin0_%=
s_add_u32 m0,s59,4096
s_nop 0
buffer_load_dword v8,s[36:39],s58 offen offset:0 sc1 lds
buffer_store_dword v4,v3,s[36:39],s71 offen offset:0 sc0 sc1
s_add_u32 m0,s59,5120
s_nop 0
buffer_load_dword v8,s[36:39],s58 offen offset:0 sc1 lds
buffer_store_dword v4,v3,s[36:39],s71 offen offset:0 sc0 sc1
s_mov_b32 s83,0
L 0
v_fma_f32 v18,-v98,v106,v18
v_fma_f32 v19,-v98,v107,v19
v_fma_f32 v20,-v98,v108,v20
v_fma_f32 v21,-v98,v109,v21
v_mul_f32 v114,v18,v18
v_mul_f32 v115,v19,v19
v_mul_f32 v116,v20,v20
v_mul_f32 v117,v21,v21
v_fma_f32 v114,v100,v114,v99
v_fma_f32 v115,v100,v115,v99
v_fma_f32 v116,v100,v116,v99
v_fma_f32 v117,v100,v117,v99
v_mul_f32 v114,v18,v114
v_mul_f32 v115,v19,v115
v_mul_f32 v116,v20,v116
v_mul_f32 v117,v21,v117
v_exp_f32 v114,v114
v_exp_f32 v115,v115
v_exp_f32 v116,v116
v_exp_f32 v117,v117
v_fma_f32 v114,v114,v101,v101
v_fma_f32 v115,v115,v101,v101
v_fma_f32 v116,v116,v101,v101
v_fma_f32 v117,v117,v101,v101
v_rcp_f32 v114,v114
v_rcp_f32 v115,v115
v_rcp_f32 v116,v116
v_rcp_f32 v117,v117
v_mul_f32 v18,v18,v114
v_mul_f32 v19,v19,v115
v_mul_f32 v20,v20,v116
v_mul_f32 v21,v21,v117
v_cvt_pk_bf16_f32 v122,v18,v19
v_cvt_pk_bf16_f32 v123,v20,v21
v_fma_f32 v22,-v98,v110,v22
v_fma_f32 v23,-v98,v111,v23
v_fma_f32 v24,-v98,v112,v24
v_fma_f32 v25,-v98,v113,v25
v_mul_f32 v118,v22,v22
v_mul_f32 v119,v23,v23
v_mul_f32 v120,v24,v24
v_mul_f32 v121,v25,v25
v_fma_f32 v118,v100,v118,v99
v_fma_f32 v119,v100,v119,v99
v_fma_f32 v120,v100,v120,v99
v_fma_f32 v121,v100,v121,v99
v_mul_f32 v118,v22,v118
v_mul_f32 v119,v23,v119
v_mul_f32 v120,v24,v120
v_mul_f32 v121,v25,v121
v_exp_f32 v118,v118
v_exp_f32 v119,v119
v_exp_f32 v120,v120
v_exp_f32 v121,v121
v_fma_f32 v118,v118,v101,v101
v_fma_f32 v119,v119,v101,v101
v_fma_f32 v120,v120,v101,v101
v_fma_f32 v121,v121,v101,v101
v_rcp_f32 v118,v118
v_rcp_f32 v119,v119
v_rcp_f32 v120,v120
v_rcp_f32 v121,v121
v_mul_f32 v22,v22,v118
v_mul_f32 v23,v23,v119
v_mul_f32 v24,v24,v120
v_mul_f32 v25,v25,v121
v_cvt_pk_bf16_f32 v124,v22,v23
v_cvt_pk_bf16_f32 v125,v24,v25
buffer_store_dwordx4 v[122:125],v0,s[36:39],s66 offen offset:0 sc0 sc1
s_mov_b32 s72,s69
s_mov_b32 s69,0x7fff0000
s_mov_b32 s66,0x7fff0000
s_mov_b32 s76,0x7fff0000
s_branch Lg3o_poll_%=
Lg3o_fin0_%=:
L 0
v_fma_f32 v18,-v98,v106,v18
v_fma_f32 v19,-v98,v107,v19
v_fma_f32 v20,-v98,v108,v20
v_fma_f32 v21,-v98,v109,v21
v_mul_f32 v114,v18,v18
v_mul_f32 v115,v19,v19
v_mul_f32 v116,v20,v20
v_mul_f32 v117,v21,v21
v_fma_f32 v114,v100,v114,v99
v_fma_f32 v115,v100,v115,v99
v_fma_f32 v116,v100,v116,v99
v_fma_f32 v117,v100,v117,v99
v_mul_f32 v114,v18,v114
v_mul_f32 v115,v19,v115
v_mul_f32 v116,v20,v116
v_mul_f32 v117,v21,v117
v_exp_f32 v114,v114
v_exp_f32 v115,v115
v_exp_f32 v116,v116
v_exp_f32 v117,v117
v_fma_f32 v114,v114,v101,v101
v_fma_f32 v115,v115,v101,v101
v_fma_f32 v116,v116,v101,v101
v_fma_f32 v117,v117,v101,v101
v_rcp_f32 v114,v114
v_rcp_f32 v115,v115
v_rcp_f32 v116,v116
v_rcp_f32 v117,v117
v_mul_f32 v18,v18,v114
v_mul_f32 v19,v19,v115
v_mul_f32 v20,v20,v116
v_mul_f32 v21,v21,v117
v_cvt_pk_bf16_f32 v122,v18,v19
v_cvt_pk_bf16_f32 v123,v20,v21
v_fma_f32 v22,-v98,v110,v22
v_fma_f32 v23,-v98,v111,v23
v_fma_f32 v24,-v98,v112,v24
v_fma_f32 v25,-v98,v113,v25
v_mul_f32 v118,v22,v22
v_mul_f32 v119,v23,v23
v_mul_f32 v120,v24,v24
v_mul_f32 v121,v25,v25
v_fma_f32 v118,v100,v118,v99
v_fma_f32 v119,v100,v119,v99
v_fma_f32 v120,v100,v120,v99
v_fma_f32 v121,v100,v121,v99
v_mul_f32 v118,v22,v118
v_mul_f32 v119,v23,v119
v_mul_f32 v120,v24,v120
v_mul_f32 v121,v25,v121
v_exp_f32 v118,v118
v_exp_f32 v119,v119
v_exp_f32 v120,v120
v_exp_f32 v121,v121
v_fma_f32 v118,v118,v101,v101
v_fma_f32 v119,v119,v101,v101
v_fma_f32 v120,v120,v101,v101
v_fma_f32 v121,v121,v101,v101
v_rcp_f32 v118,v118
v_rcp_f32 v119,v119
v_rcp_f32 v120,v120
v_rcp_f32 v121,v121
v_mul_f32 v22,v22,v118
v_mul_f32 v23,v23,v119
v_mul_f32 v24,v24,v120
v_mul_f32 v25,v25,v121
v_cvt_pk_bf16_f32 v124,v22,v23
v_cvt_pk_bf16_f32 v125,v24,v25
buffer_store_dwordx4 v[122:125],v0,s[36:39],s66 offen offset:0 sc0 sc1
s_mov_b32 s72,s69
s_mov_b32 s69,0x7fff0000
s_mov_b32 s66,0x7fff0000
s_mov_b32 s76,0x7fff0000
s_branch Lg3o_done_%=
Lg3o_K1_%=:
Lg3o_L1_%=:
v_mfma_f32_16x16x32_bf16 v[30:33],a[4:7],v[34:37],0
s_add_u32 m0,s59,3072
R 54,2,5120
v_add_f32_dpp v131,v130,v130 quad_perm:[1,0,3,2] row_mask:0xf bank_mask:0xf
M 30,a[12:15],38
buffer_load_dword v8,s[36:39],s58 offen offset:0 sc1 lds
v_mfma_f32_16x16x32_bf16 v[26:29],a[0:3],v[34:37],0
s_mov_b32 s67,s65
R 58,2,6144
v_add_f32_dpp v131,v131,v131 quad_perm:[2,3,0,1] row_mask:0xf bank_mask:0xf
M 26,a[8:11],38
L 3
v_fma_f32 v18,-v98,v106,v18
v_fma_f32 v19,-v98,v107,v19
M 26,a[16:19],42
s_add_u32 s65,s65,s62
R 62,2,7168
v_add_f32_dpp v134,v131,v131 row_half_mirror row_mask:0xf bank_mask:0xf
M 26,a[24:27],46
v_fma_f32 v20,-v98,v108,v20
v_fma_f32 v21,-v98,v109,v21
M 30,a[20:23],42
R 66,2,8192
v_mov_b32_dpp v131,v134 row_ror:8 row_mask:0xf bank_mask:0xf
M 30,a[28:31],46
L 3
v_fma_f32 v131,-v134,v134,v131
v_max_f32 v131,s87,v131
M 30,a[36:39],50
R 70,2,9216
v_mul_f32 v134,v17,v134
M 30,a[44:47],54
v_rsq_f32 v132,v131
M 26,a[32:35],50
R 74,2,10240
v_fma_f32 v131,v15,v131,v16
M 26,a[40:43],54
L 3
v_mul_f32 v135,v132,v132
v_fma_f32 v131,v14,v135,v131
M 26,a[48:51],58
R 78,2,11264
v_fma_f32 v134,v132,v131,v134
M 26,a[56:59],62
ds_write_b32 v12,v134 offset:2048
M 30,a[52:55],58
R 82,2,12288
v_mul_f32 v114,v18,v18
M 30,a[60:63],62
L 4
v_mul_f32 v115,v19,v19
v_mul_f32 v116,v20,v20
M 30,a[68:71],66
R 86,2,13312
v_mul_f32 v117,v21,v21
M 30,a[76:79],70
v_fma_f32 v114,v100,v114,v99
v_fma_f32 v115,v100,v115,v99
M 26,a[64:67],66
R 90,2,14336
v_fma_f32 v116,v100,v116,v99
M 26,a[72:75],70
L 4
v_fma_f32 v117,v100,v117,v99
v_mul_f32 v114,v18,v114
M 26,a[80:83],74
s_waitcnt vmcnt(0)
R 94,2,15360
v_mul_f32 v115,v19,v115
M 26,a[88:91],78
s_barrier
ds_read_b32 v7,v6 offset:3072
v_mul_f32 v116,v20,v116
M 30,a[84:87],74
R 34,2,16384
v_mul_f32 v117,v21,v117
M 30,a[92:95],78
L 4
buffer_store_dword v4,v3,s[36:39],s70 offen offset:0 sc0 sc1
M 30,a[100:103],82
s_mov_b32 s70,0x7fff0000
R 38,2,17408
v_fma_f32 v22,-v98,v110,v22
M 30,a[108:111],86
v_exp_f32 v114,v114
M 26,a[96:99],82
R 42,2,18432
v_fma_f32 v114,v114,v101,v101
M 26,a[104:107],86
L 4
L 3
v_cmp_eq_u32 vcc,s53,v7
M 26,a[112:115],90
R 46,2,19456
v_fma_f32 v23,-v98,v111,v23
M 26,a[120:123],94
v_exp_f32 v115,v115
M 30,a[116:119],90
s_cmp_eq_u32 vcc_lo,-1
R 50,2,20480
v_fma_f32 v115,v115,v101,v101
s_cbranch_scc0 Lg3o_X1_%=
M 30,a[124:127],94
L 3
s_add_u32 m0,s57,0
s_add_u32 s56,s55,0
buffer_load_dword v130,v11,s[36:39],s73 offen offset:0 sc1
M 30,a[132:135],34
s_add_u32 s58,s58,s61
R 54,2,21504
v_fma_f32 v24,-v98,v112,v24
M 30,a[140:143],38
D 0,56,0
M 26,a[128:131],34
s_add_u32 s54,s54,s46
R 58,2,22528
v_fma_f32 v25,-v98,v113,v25
M 26,a[136:139],38
L 3
v_exp_f32 v116,v116
M 26,a[144:147],42
s_add_u32 s73,s73,s64
R 62,2,23552
v_fma_f32 v116,v116,v101,v101
M 26,a[152:155],46
D 0,56,1024
M 30,a[148:151],42
R 66,2,24576
v_mul_f32 v118,v22,v22
M 30,a[156:159],46
L 3
v_exp_f32 v117,v117
M 30,a[164:167],50
R 70,2,25600
v_fma_f32 v117,v117,v101,v101
M 30,a[172:175],54
D 0,56,2048
M 26,a[160:163],50
R 74,2,26624
v_mul_f32 v119,v23,v23
M 26,a[168:171],54
L 3
v_rcp_f32 v114,v114
M 26,a[176:179],58
R 78,2,27648
v_mul_f32 v18,v18,v114
M 26,a[184:187],62
D 0,56,3072
M 30,a[180:183],58
s_add_u32 m0,s59,1024
R 82,2,28672
v_mul_f32 v120,v24,v24
M 30,a[188:191],62
L 3
buffer_load_dword v8,s[36:39],s58 offen offset:0 sc1 lds
M 30,a[196:199],66
s_add_u32 m0,s57,16384
R 86,2,29696
v_mul_f32 v121,v25,v25
M 30,a[204:207],70
s_add_u32 s56,s55,16384
v_rcp_f32 v115,v115
M 26,a[192:195],66
s_add_u32 s55,s55,s60
R 90,2,30720
v_mul_f32 v19,v19,v115
M 26,a[200:203],70
L 3
s_nop 0
D 0,56,0
M 26,a[208:211],74
s_waitcnt vmcnt(1)
R 94,2,31744
v_cvt_pk_bf16_f32 v122,v18,v19
M 26,a[216:219],78
s_barrier
ds_read_b32 v7,v6 offset:1024
R 102,13,2048
M 30,a[212:215],74
s_sub_u32 s57,s84,s57
R 34,1,0
v_fma_f32 v118,v100,v118,v99
M 30,a[220:223],78
L 5
buffer_store_dword v4,v3,s[36:39],s70 offen offset:0 sc0 sc1
M 30,a[228:231],82
s_mov_b32 s70,s69
R 38,1,1024
v_fma_f32 v119,v100,v119,v99
M 30,a[236:239],86
D 0,56,1024
M 26,a[224:227],82
s_mov_b32 s69,s68
R 42,1,2048
v_fma_f32 v120,v100,v120,v99
M 26,a[232:235],86
L 5
v_fma_f32 v121,v100,v121,v99
v_mul_f32 v118,v22,v118
v_mul_f32 v119,v23,v119
v_mul_f32 v120,v24,v120
v_mul_f32 v121,v25,v121
v_exp_f32 v118,v118
v_exp_f32 v119,v119
v_exp_f32 v120,v120
v_exp_f32 v121,v121
M 26,a[240:243],90
L 4
R 46,1,3072
v_rcp_f32 v116,v116
v_rcp_f32 v117,v117
v_fma_f32 v118,v118,v101,v101
v_fma_f32 v119,v119,v101,v101
v_fma_f32 v120,v120,v101,v101
v_fma_f32 v121,v121,v101,v101
v_rcp_f32 v118,v118
v_rcp_f32 v119,v119
v_rcp_f32 v120,v120
v_rcp_f32 v121,v121
v_cmp_eq_u32 vcc,s53,v7
M 26,a[248:251],94
s_add_u32 s68,s68,s63
v_mul_f32 v20,v20,v116
v_mul_f32 v21,v21,v117
v_cvt_pk_bf16_f32 v123,v20,v21
)"
R"(v_mul_f32 v22,v22,v118
v_mul_f32 v23,v23,v119
v_mul_f32 v24,v24,v120
v_mul_f32 v25,v25,v121
v_cvt_pk_bf16_f32 v124,v22,v23
v_cvt_pk_bf16_f32 v125,v24,v25
M 30,a[244:247],90
L 2
s_cmp_eq_u32 vcc_lo,-1
R 50,1,4096
buffer_store_dwordx4 v[122:125],v0,s[36:39],s66 offen offset:0 sc0 sc1
M 30,a[252:255],94
D 0,56,2048
D 0,56,3072
buffer_store_dword v4,v3,s[36:39],s71 offen offset:0 sc0 sc1
buffer_store_dword v4,v3,s[36:39],s71 offen offset:0 sc0 sc1
s_cbranch_scc1 Lg3o_F0_%=
s_branch Lg3o_K0_%=
Lg3o_X1_%=:
M 30,a[124:127],94
L 3
s_add_u32 m0,s59,1024
v_fma_f32 v24,-v98,v112,v24
M 30,a[132:135],34
R 54,2,21504
v_fma_f32 v25,-v98,v113,v25
M 30,a[140:143],38
buffer_load_dword v8,s[36:39],s58 offen offset:0 sc1 lds
M 26,a[128:131],34
R 58,2,22528
v_mul_f32 v118,v22,v22
M 26,a[136:139],38
L 3
v_exp_f32 v116,v116
M 26,a[144:147],42
R 62,2,23552
v_fma_f32 v116,v116,v101,v101
M 26,a[152:155],46
v_exp_f32 v117,v117
M 30,a[148:151],42
R 66,2,24576
v_fma_f32 v117,v117,v101,v101
M 30,a[156:159],46
L 3
v_rcp_f32 v114,v114
v_mul_f32 v119,v23,v23
M 30,a[164:167],50
R 70,2,25600
v_mul_f32 v18,v18,v114
M 30,a[172:175],54
v_rcp_f32 v115,v115
M 26,a[160:163],50
R 74,2,26624
v_mul_f32 v19,v19,v115
M 26,a[168:171],54
L 3
v_rcp_f32 v116,v116
M 26,a[176:179],58
R 78,2,27648
v_mul_f32 v20,v20,v116
M 26,a[184:187],62
v_rcp_f32 v117,v117
M 30,a[180:183],58
R 82,2,28672
v_mul_f32 v21,v21,v117
M 30,a[188:191],62
L 3
v_cvt_pk_bf16_f32 v122,v18,v19
v_cvt_pk_bf16_f32 v123,v20,v21
M 30,a[196:199],66
R 86,2,29696
v_mul_f32 v120,v24,v24
M 30,a[204:207],70
v_mul_f32 v121,v25,v25
v_fma_f32 v118,v100,v118,v99
M 26,a[192:195],66
R 90,2,30720
v_fma_f32 v119,v100,v119,v99
M 26,a[200:203],70
L 3
s_nop 0
v_fma_f32 v120,v100,v120,v99
M 26,a[208:211],74
s_waitcnt vmcnt(0)
R 94,2,31744
v_fma_f32 v121,v100,v121,v99
M 26,a[216:219],78
s_barrier
buffer_store_dword v4,v3,s[36:39],s70 offen offset:0 sc0 sc1
M 30,a[212:215],74
s_mov_b32 s70,s69
ds_read_b32 v7,v6 offset:1024
R 102,13,2048
M 30,a[220:223],78
L 4
v_mul_f32 v118,v22,v118
v_mul_f32 v119,v23,v119
M 30,a[228:231],82
s_mov_b32 s69,s68
v_mul_f32 v120,v24,v120
v_mul_f32 v121,v25,v121
M 30,a[236:239],86
s_add_u32 s68,s68,s63
v_exp_f32 v118,v118
M 26,a[224:227],82
v_exp_f32 v119,v119
M 26,a[232:235],86
L 2
v_exp_f32 v120,v120
v_exp_f32 v121,v121
M 26,a[240:243],90
L 1
v_fma_f32 v118,v118,v101,v101
v_fma_f32 v119,v119,v101,v101
v_fma_f32 v120,v120,v101,v101
v_fma_f32 v121,v121,v101,v101
v_rcp_f32 v118,v118
v_rcp_f32 v119,v119
v_rcp_f32 v120,v120
v_rcp_f32 v121,v121
M 26,a[248:251],94
v_mul_f32 v22,v22,v118
v_mul_f32 v23,v23,v119
v_mul_f32 v24,v24,v120
v_mul_f32 v25,v25,v121
v_cvt_pk_bf16_f32 v124,v22,v23
v_cvt_pk_bf16_f32 v125,v24,v25
M 30,a[244:247],90
buffer_store_dwordx4 v[122:125],v0,s[36:39],s66 offen offset:0 sc0 sc1
v_cmp_eq_u32 vcc,s53,v7
M 30,a[252:255],94
s_mov_b32 s80,0
s_cmp_eq_u32 vcc_lo,-1
s_cbranch_scc0 Lg3o_xn1_%=
buffer_load_dword v130,v11,s[36:39],s73 offen offset:0 sc1
L 0
v_fma_f32 v26,-v102,v106,v26
v_fma_f32 v27,-v102,v107,v27
v_fma_f32 v28,-v102,v108,v28
v_fma_f32 v29,-v102,v109,v29
v_mul_f32 v114,v26,v26
v_mul_f32 v115,v27,v27
s_add_u32 s73,s73,s64
s_add_u32 m0,s57,0
s_add_u32 s56,s55,0
D 0,56,0
v_mul_f32 v116,v28,v28
v_mul_f32 v117,v29,v29
v_fma_f32 v114,v104,v114,v103
v_fma_f32 v115,v104,v115,v103
v_fma_f32 v116,v104,v116,v103
v_fma_f32 v117,v104,v117,v103
D 0,56,1024
v_mul_f32 v114,v26,v114
v_mul_f32 v115,v27,v115
v_mul_f32 v116,v28,v116
v_mul_f32 v117,v29,v117
v_exp_f32 v114,v114
v_exp_f32 v115,v115
D 0,56,2048
v_exp_f32 v116,v116
v_exp_f32 v117,v117
v_fma_f32 v114,v114,v105,v105
v_fma_f32 v115,v115,v105,v105
v_fma_f32 v116,v116,v105,v105
v_fma_f32 v117,v117,v105,v105
D 0,56,3072
v_rcp_f32 v114,v114
v_rcp_f32 v115,v115
v_rcp_f32 v116,v116
v_rcp_f32 v117,v117
v_mul_f32 v26,v26,v114
v_mul_f32 v27,v27,v115
s_add_u32 s58,s58,s61
s_add_u32 m0,s57,16384
s_add_u32 s56,s55,16384
D 0,56,0
v_mul_f32 v28,v28,v116
v_mul_f32 v29,v29,v117
v_cvt_pk_bf16_f32 v122,v26,v27
v_cvt_pk_bf16_f32 v123,v28,v29
v_fma_f32 v30,-v102,v110,v30
v_fma_f32 v31,-v102,v111,v31
D 0,56,1024
v_fma_f32 v32,-v102,v112,v32
v_fma_f32 v33,-v102,v113,v33
v_mul_f32 v118,v30,v30
v_mul_f32 v119,v31,v31
v_mul_f32 v120,v32,v32
v_mul_f32 v121,v33,v33
D 0,56,2048
v_fma_f32 v118,v104,v118,v103
v_fma_f32 v119,v104,v119,v103
v_fma_f32 v120,v104,v120,v103
v_fma_f32 v121,v104,v121,v103
v_mul_f32 v118,v30,v118
v_mul_f32 v119,v31,v119
D 0,56,3072
v_mul_f32 v120,v32,v120
v_mul_f32 v121,v33,v121
v_exp_f32 v118,v118
v_exp_f32 v119,v119
v_exp_f32 v120,v120
v_exp_f32 v121,v121
s_add_u32 s55,s55,s60
s_sub_u32 s57,s84,s57
s_add_u32 s54,s54,s46
v_fma_f32 v118,v118,v105,v105
v_fma_f32 v119,v119,v105,v105
v_fma_f32 v120,v120,v105,v105
v_fma_f32 v121,v121,v105,v105
v_rcp_f32 v118,v118
v_rcp_f32 v119,v119
v_rcp_f32 v120,v120
v_rcp_f32 v121,v121
v_mul_f32 v30,v30,v118
v_mul_f32 v31,v31,v119
v_mul_f32 v32,v32,v120
v_mul_f32 v33,v33,v121
v_cvt_pk_bf16_f32 v124,v30,v31
v_cvt_pk_bf16_f32 v125,v32,v33
buffer_store_dwordx4 v[122:125],v0,s[36:39],s67 offen offset:0 sc0 sc1
s_mov_b32 s72,s69
s_mov_b32 s69,0x7fff0000
s_mov_b32 s67,0x7fff0000
s_mov_b32 s77,0x7fff0000
s_branch Lg3o_gox_%=
Lg3o_xn1_%=:
s_cmp_ge_u32 s54,256
s_cbranch_scc1 Lg3o_fin1_%=
s_add_u32 m0,s59,4096
s_nop 0
buffer_load_dword v8,s[36:39],s58 offen offset:0 sc1 lds
buffer_store_dword v4,v3,s[36:39],s71 offen offset:0 sc0 sc1
s_add_u32 m0,s59,5120
s_nop 0
buffer_load_dword v8,s[36:39],s58 offen offset:0 sc1 lds
buffer_store_dword v4,v3,s[36:39],s71 offen offset:0 sc0 sc1
s_mov_b32 s83,0
L 0
v_fma_f32 v26,-v102,v106,v26
v_fma_f32 v27,-v102,v107,v27
v_fma_f32 v28,-v102,v108,v28
v_fma_f32 v29,-v102,v109,v29
v_mul_f32 v114,v26,v26
v_mul_f32 v115,v27,v27
v_mul_f32 v116,v28,v28
v_mul_f32 v117,v29,v29
v_fma_f32 v114,v104,v114,v103
v_fma_f32 v115,v104,v115,v103
v_fma_f32 v116,v104,v116,v103
v_fma_f32 v117,v104,v117,v103
v_mul_f32 v114,v26,v114
v_mul_f32 v115,v27,v115
v_mul_f32 v116,v28,v116
v_mul_f32 v117,v29,v117
v_exp_f32 v114,v114
v_exp_f32 v115,v115
v_exp_f32 v116,v116
v_exp_f32 v117,v117
v_fma_f32 v114,v114,v105,v105
v_fma_f32 v115,v115,v105,v105
v_fma_f32 v116,v116,v105,v105
v_fma_f32 v117,v117,v105,v105
v_rcp_f32 v114,v114
v_rcp_f32 v115,v115
v_rcp_f32 v116,v116
v_rcp_f32 v117,v117
v_mul_f32 v26,v26,v114
v_mul_f32 v27,v27,v115
v_mul_f32 v28,v28,v116
v_mul_f32 v29,v29,v117
v_cvt_pk_bf16_f32 v122,v26,v27
v_cvt_pk_bf16_f32 v123,v28,v29
v_fma_f32 v30,-v102,v110,v30
v_fma_f32 v31,-v102,v111,v31
v_fma_f32 v32,-v102,v112,v32
v_fma_f32 v33,-v102,v113,v33
v_mul_f32 v118,v30,v30
v_mul_f32 v119,v31,v31
v_mul_f32 v120,v32,v32
v_mul_f32 v121,v33,v33
v_fma_f32 v118,v104,v118,v103
v_fma_f32 v119,v104,v119,v103
v_fma_f32 v120,v104,v120,v103
v_fma_f32 v121,v104,v121,v103
v_mul_f32 v118,v30,v118
v_mul_f32 v119,v31,v119
v_mul_f32 v120,v32,v120
v_mul_f32 v121,v33,v121
v_exp_f32 v118,v118
v_exp_f32 v119,v119
v_exp_f32 v120,v120
v_exp_f32 v121,v121
v_fma_f32 v118,v118,v105,v105
v_fma_f32 v119,v119,v105,v105
v_fma_f32 v120,v120,v105,v105
v_fma_f32 v121,v121,v105,v105
v_rcp_f32 v118,v118
v_rcp_f32 v119,v119
v_rcp_f32 v120,v120
v_rcp_f32 v121,v121
v_mul_f32 v30,v30,v118
v_mul_f32 v31,v31,v119
v_mul_f32 v32,v32,v120
v_mul_f32 v33,v33,v121
v_cvt_pk_bf16_f32 v124,v30,v31
v_cvt_pk_bf16_f32 v125,v32,v33
buffer_store_dwordx4 v[122:125],v0,s[36:39],s67 offen offset:0 sc0 sc1
s_mov_b32 s72,s69
s_mov_b32 s69,0x7fff0000
s_mov_b32 s67,0x7fff0000
s_mov_b32 s77,0x7fff0000
s_branch Lg3o_poll_%=
Lg3o_fin1_%=:
L 0
v_fma_f32 v26,-v102,v106,v26
v_fma_f32 v27,-v102,v107,v27
v_fma_f32 v28,-v102,v108,v28
v_fma_f32 v29,-v102,v109,v29
v_mul_f32 v114,v26,v26
v_mul_f32 v115,v27,v27
v_mul_f32 v116,v28,v28
v_mul_f32 v117,v29,v29
v_fma_f32 v114,v104,v114,v103
v_fma_f32 v115,v104,v115,v103
v_fma_f32 v116,v104,v116,v103
v_fma_f32 v117,v104,v117,v103
v_mul_f32 v114,v26,v114
v_mul_f32 v115,v27,v115
v_mul_f32 v116,v28,v116
v_mul_f32 v117,v29,v117
v_exp_f32 v114,v114
v_exp_f32 v115,v115
v_exp_f32 v116,v116
v_exp_f32 v117,v117
v_fma_f32 v114,v114,v105,v105
v_fma_f32 v115,v115,v105,v105
v_fma_f32 v116,v116,v105,v105
v_fma_f32 v117,v117,v105,v105
v_rcp_f32 v114,v114
v_rcp_f32 v115,v115
v_rcp_f32 v116,v116
v_rcp_f32 v117,v117
v_mul_f32 v26,v26,v114
v_mul_f32 v27,v27,v115
v_mul_f32 v28,v28,v116
v_mul_f32 v29,v29,v117
v_cvt_pk_bf16_f32 v122,v26,v27
v_cvt_pk_bf16_f32 v123,v28,v29
v_fma_f32 v30,-v102,v110,v30
v_fma_f32 v31,-v102,v111,v31
v_fma_f32 v32,-v102,v112,v32
v_fma_f32 v33,-v102,v113,v33
v_mul_f32 v118,v30,v30
v_mul_f32 v119,v31,v31
v_mul_f32 v120,v32,v32
v_mul_f32 v121,v33,v33
v_fma_f32 v118,v104,v118,v103
v_fma_f32 v119,v104,v119,v103
v_fma_f32 v120,v104,v120,v103
v_fma_f32 v121,v104,v121,v103
v_mul_f32 v118,v30,v118
v_mul_f32 v119,v31,v119
v_mul_f32 v120,v32,v120
v_mul_f32 v121,v33,v121
v_exp_f32 v118,v118
v_exp_f32 v119,v119
v_exp_f32 v120,v120
v_exp_f32 v121,v121
v_fma_f32 v118,v118,v105,v105
v_fma_f32 v119,v119,v105,v105
v_fma_f32 v120,v120,v105,v105
v_fma_f32 v121,v121,v105,v105
v_rcp_f32 v118,v118
v_rcp_f32 v119,v119
v_rcp_f32 v120,v120
v_rcp_f32 v121,v121
v_mul_f32 v30,v30,v118
v_mul_f32 v31,v31,v119
v_mul_f32 v32,v32,v120
v_mul_f32 v33,v33,v121
v_cvt_pk_bf16_f32 v124,v30,v31
v_cvt_pk_bf16_f32 v125,v32,v33
buffer_store_dwordx4 v[122:125],v0,s[36:39],s67 offen offset:0 sc0 sc1
s_mov_b32 s72,s69
s_mov_b32 s69,0x7fff0000
s_mov_b32 s67,0x7fff0000
s_mov_b32 s77,0x7fff0000
s_branch Lg3o_done_%=
Lg3o_done_%=:
s_waitcnt vmcnt(1)
s_barrier
buffer_store_dword v4,v3,s[36:39],s70 offen offset:0 sc0 sc1
s_waitcnt vmcnt(1)
s_barrier
buffer_store_dword v4,v3,s[36:39],s72 offen offset:0 sc0 sc1
s_waitcnt vmcnt(0) lgkmcnt(0)
s_barrier
s_endpgm
Lcode_g3o_e:
.purgem M
.purgem R
.purgem T
.purgem D
.purgem L
)"
  :: "s"(ws), "s"(__builtin_amdgcn_readfirstlane(sWave)), "s"(__builtin_amdgcn_readfirstlane(sG0)), "s"(__builtin_amdgcn_readfirstlane(sStep)), "s"(__builtin_amdgcn_readfirstlane(sOutOff)), "s"(__builtin_amdgcn_readfirstlane(sFoutOff)), "s"(__builtin_amdgcn_readfirstlane(sAuxOff)), "s"(__builtin_amdgcn_readfirstlane(sWw)), "s"(__builtin_amdgcn_readfirstlane(sBuf)), "s"(__builtin_amdgcn_readfirstlane(sExp)), "v"(vL16), "v"(vFlag), "v"(vSnapOff), "v"(vSnapRd), "v"(vWd0), "v"(vWd1), "v"(vWr0), "v"(vWr1), "v"(vWr2), "v"(vOutS), "v"(vAux0), "v"(vAux1), "v"(vAux2), "v"(vAux3), "v"(vAn), "v"(vAe), "s"(w3), "s"(out) : "v255", "v0", "v1", "v2", "v3", "v4", "v5", "v6", "v7", "v8", "v9", "v10", "v11", "v12", "v13", "v14", "v15", "v16", "v17", "v18", "v19", "v20", "v21", "v22", "v23", "v24", "v25", "v26", "v27", "v28", "v29", "v30", "v31", "v32", "v33", "v34", "v35", "v36", "v37", "v38", "v39", "v40", "v41", "v42", "v43", "v44", "v45", "v46", "v47", "v48", "v49", "v50", "v51", "v52", "v53", "v54", "v55", "v56", "v57", "v58", "v59", "v60", "v61", "v62", "v63", "v64", "v65", "v66", "v67", "v68", "v69", "v70", "v71", "v72", "v73", "v74", "v75", "v76", "v77", "v78", "v79", "v80", "v81", "v82", "v83", "v84", "v85", "v86", "v87", "v88", "v89", "v90", "v91", "v92", "v93", "v94", "v95", "v96", "v97", "v98", "v99", "v100", "v101", "v102", "v103", "v104", "v105", "v106", "v107", "v108", "v109", "v110", "v111", "v112", "v113", "v114", "v115", "v116", "v117", "v118", "v119", "v120", "v121", "v122", "v123", "v124", "v125", "v126", "v127", "v128", "v129", "v130", "v131", "v132", "v133", "v134", "v135", "v136", "v137", "v138", "v139", "v140", "v141", "v142", "v143", "v144", "v145", "v146", "v147", "v148", "v149", "v150", "v151", "v152", "v153", "v154", "v155", "v156", "v157", "v158", "v159", "v160", "v161", "v162", "v163", "v164", "v165", "v166", "v167", "v168", "v169", "v170", "v171", "v172", "v173", "v174", "v175", "v176", "v177", "v178", "v179", "v180", "v181", "v182", "v183", "v184", "v185", "v186", "v187", "v188", "v189", "v190", "v191", "v192", "v193", "v194", "v195", "v196", "v197", "v198", "v199", "v200", "v201", "v202", "v203", "v204", "v205", "v206", "v207", "v208", "v209", "v210", "v211", "v212", "v213", "v214", "v215", "v216", "v217", "v218", "v219", "v220", "v221", "v222", "v223", "v224", "v225", "v226", "v227", "v228", "v229", "v230", "v231", "a0", "a1", "a2", "a3", "a4", "a5", "a6", "a7", "a8", "a9", "a10", "a11", "a12", "a13", "a14", "a15", "a16", "a17", "a18", "a19", "a20", "a21", "a22", "a23", "a24", "a25", "a26", "a27", "a28", "a29", "a30", "a31", "a32", "a33", "a34", "a35", "a36", "a37", "a38", "a39", "a40", "a41", "a42", "a43", "a44", "a45", "a46", "a47", "a48", "a49", "a50", "a51", "a52", "a53", "a54", "a55", "a56", "a57", "a58", "a59", "a60", "a61", "a62", "a63", "a64", "a65", "a66", "a67", "a68", "a69", "a70", "a71", "a72", "a73", "a74", "a75", "a76", "a77", "a78", "a79", "a80", "a81", "a82", "a83", "a84", "a85", "a86", "a87", "a88", "a89", "a90", "a91", "a92", "a93", "a94", "a95", "a96", "a97", "a98", "a99", "a100", "a101", "a102", "a103", "a104", "a105", "a106", "a107", "a108", "a109", "a110", "a111", "a112", "a113", "a114", "a115", "a116", "a117", "a118", "a119", "a120", "a121", "a122", "a123", "a124", "a125", "a126", "a127", "a128", "a129", "a130", "a131", "a132", "a133", "a134", "a135", "a136", "a137", "a138", "a139", "a140", "a141", "a142", "a143", "a144", "a145", "a146", "a147", "a148", "a149", "a150", "a151", "a152", "a153", "a154", "a155", "a156", "a157", "a158", "a159", "a160", "a161", "a162", "a163", "a164", "a165", "a166", "a167", "a168", "a169", "a170", "a171", "a172", "a173", "a174", "a175", "a176", "a177", "a178", "a179", "a180", "a181", "a182", "a183", "a184", "a185", "a186", "a187", "a188", "a189", "a190", "a191", "a192", "a193", "a194", "a195", "a196", "a197", "a198", "a199", "a200", "a201", "a202", "a203", "a204", "a205", "a206", "a207", "a208", "a209", "a210", "a211", "a212", "a213", "a214", "a215", "a216", "a217", "a218", "a219", "a220", "a221", "a222", "a223", "a224", "a225", "a226", "a227", "a228", "a229", "a230", "a231", "a232", "a233", "a234", "a235", "a236", "a237", "a238", "a239", "a240", "a241", "a242", "a243", "a244", "a245", "a246", "a247", "a248", "a249", "a250", "a251", "a252", "a253", "a254", "a255", "s36", "s37", "s38", "s39", "s40", "s41", "s42", "s43", "s44", "s45", "s46", "s47", "s48", "s49", "s50", "s51", "s52", "s53", "s54", "s55", "s56", "s57", "s58", "s59", "s60", "s61", "s62", "s63", "s64", "s65", "s66", "s67", "s68", "s69", "s70", "s71", "s72", "s73", "s74", "s75", "s76", "s77", "s78", "s79", "s80", "s81", "s82", "s83", "s84", "s85", "s86", "s87", "s88", "s89", "s90", "s91", "s92", "s93", "s94", "s95", "s96", "s97", "vcc", "scc", "memory"); }
    __builtin_unreachable();
  }
  if (kind < 3) {
    const unsigned sStep = 4;
    COMMON(kind == 1 ? 8 : 16, kind == 1 ? fj * 128 : fj < 8 ? 1024 + fj * 128 : (fj & 2) * 1024 + (fj - 8) * 128, 1024, 131072, 2048, 256, kind == 1 ? 0 : 32768);
    const int sr = l15 - ((wave & 2) ? -1 : 1), sr2 = l15 - ((wave & 2) ? -2 : 2), nx = (slice >> 1) & 1;
    auto src = [&](int r_) { return (unsigned)((r_ & 16) ? 1024 + lg * 64 + (r_ & 3) * 16 : (lg * 16 + r_) * 16); };
    const unsigned vOutS = src(sr2), vSp = (sr2 & 16) ? OOB : vOutS;
    (void)vWr2;
    const unsigned vAn = kind == 1 ? HU + vL16 : src(sr);
    const unsigned vAe = kind == 1 ? ((l15 >> 2) == (nx ? 0 : 3) ? (nx ? 0 : 2 * HU) + 1024 + lg * 64 + (l15 & 3) * 16 : OOB) : (sr & 16) ? OOB : vAn;
    const unsigned vAux0 = l15 * 16, vAux1 = 139264 + (l15 * 2 + (lg & 1)) * 16 + wave * 4 + (lg >> 1) * 512, vAux2 = 139264 + (l15 * 2 + (lg & 1)) * 16;
    const unsigned vAux3 = (wave == 0 && lg < 2) ? slice * 128 + l15 * 8 + lg * 4 : OOB;
    const unsigned sOutOff = kind == 1 ? OFF_HS - HU + (slice * 4 + wave) * 1280 : OFF_X1 + (slice * 4 + wave) * 1024, sFoutOff = (kind == 1 ? OFF_FH : OFF_FX1) + slice * 128;
    const unsigned sAuxOff = OFF_XP + (slice * 4 + wave) * 1024, sBuf = kind, sWw = 16 * wave * 2048 + slice * 256;
    const u16* w12 = kind == 1 ? w1 : w2;
    const unsigned sExp = __builtin_amdgcn_readfirstlane(ep);
    unsigned epOut;
    asm volatile(
R"(.macro M d,w,x
v_mfma_f32_16x16x32_bf16 v[\d:\d+3], \w, v[\x:\x+3], v[\d:\d+3]
.endm
.macro R d,a,o
ds_read_b128 v[\d:\d+3], v\a offset:\o
.endm
.macro T d,a,o
ds_read_b64_tr_b16 \d, v\a offset:\o
.endm
.macro D a,s,o
buffer_load_dwordx4 v\a, s[36:39], s\s offen offset:\o sc1 lds
.endm
.macro L c
s_waitcnt lgkmcnt(\c)
.endm
Lcode_g12_b:
s_cmp_eq_u32 %9,2
s_cbranch_scc1 Lg2_begin_%=
s_mov_b64 s[36:37],%1
s_mov_b32 s38,111243392
s_mov_b32 s39,0x00020000
s_mov_b32 s44,%2
s_mov_b32 s45,%3
s_mov_b32 s46,%4
s_mov_b32 s47,%5
s_mov_b32 s48,%6
s_mov_b32 s49,%7
s_mov_b32 s50,%8
s_mov_b32 s51,%9
s_mov_b32 s53,%10
v_mov_b32 v0,%11
v_mov_b32 v3,%12
v_mov_b32 v5,%13
v_mov_b32 v6,%14
v_mov_b32 v18,%15
v_mov_b32 v19,%16
v_mov_b32 v20,%17
v_mov_b32 v21,%18
v_mov_b32 v9,%19
v_mov_b32 v10,%20
v_mov_b32 v11,%21
v_mov_b32 v12,%22
v_mov_b32 v13,%23
v_mov_b32 v14,%24
v_mov_b32 v15,%25
v_mov_b32 v16,%26
s_mov_b64 s[40:41],%27
s_mov_b32 s42,2097152
s_mov_b32 s43,0x00020000
s_lshl_b32 s57,s44,11
s_waitcnt vmcnt(20) lgkmcnt(0)
s_barrier
s_add_u32 s55,s50,786432
s_add_u32 m0,s57,98304
s_nop 0
buffer_load_dwordx4 v18,s[40:43],s55 offen offset:0  lds
s_add_u32 m0,s57,99328
s_nop 0
buffer_load_dwordx4 v19,s[40:43],s55 offen offset:0  lds
s_add_u32 m0,s57,106368
s_nop 0
buffer_load_dwordx4 v18,s[40:43],s55 offen offset:128  lds
s_add_u32 m0,s57,107392
s_nop 0
buffer_load_dwordx4 v19,s[40:43],s55 offen offset:128  lds
v_add_u32 v26,0,v20
v_add_u32 v27,0,v21
T a[0:1],26,0
T a[2:3],26,512
T a[4:5],27,0
T a[6:7],27,512
T a[8:9],26,4096
T a[10:11],26,4608
T a[12:13],27,4096
T a[14:15],27,4608
s_waitcnt vmcnt(20) lgkmcnt(0)
s_barrier
s_add_u32 s55,s50,917504
s_add_u32 m0,s57,0
s_nop 0
buffer_load_dwordx4 v18,s[40:43],s55 offen offset:0  lds
s_add_u32 m0,s57,1024
s_nop 0
buffer_load_dwordx4 v19,s[40:43],s55 offen offset:0  lds
s_add_u32 m0,s57,8064
s_nop 0
buffer_load_dwordx4 v18,s[40:43],s55 offen offset:128  lds
s_add_u32 m0,s57,9088
s_nop 0
buffer_load_dwordx4 v19,s[40:43],s55 offen offset:128  lds
v_add_u32 v26,16384,v20
v_add_u32 v27,16384,v21
T a[16:17],26,0
T a[18:19],26,512
T a[20:21],27,0
T a[22:23],27,512
T a[24:25],26,4096
T a[26:27],26,4608
T a[28:29],27,4096
T a[30:31],27,4608
s_waitcnt vmcnt(20) lgkmcnt(0)
s_barrier
s_add_u32 s55,s50,1048576
s_add_u32 m0,s57,16384
s_nop 0
buffer_load_dwordx4 v18,s[40:43],s55 offen offset:0  lds
s_add_u32 m0,s57,17408
s_nop 0
buffer_load_dwordx4 v19,s[40:43],s55 offen offset:0  lds
s_add_u32 m0,s57,24448
s_nop 0
buffer_load_dwordx4 v18,s[40:43],s55 offen offset:128  lds
s_add_u32 m0,s57,25472
s_nop 0
buffer_load_dwordx4 v19,s[40:43],s55 offen offset:128  lds
v_add_u32 v26,32768,v20
v_add_u32 v27,32768,v21
T a[32:33],26,0
T a[34:35],26,512
T a[36:37],27,0
T a[38:39],27,512
T a[40:41],26,4096
T a[42:43],26,4608
T a[44:45],27,4096
T a[46:47],27,4608
s_waitcnt vmcnt(20) lgkmcnt(0)
s_barrier
s_add_u32 s55,s50,1179648
s_add_u32 m0,s57,32768
s_nop 0
buffer_load_dwordx4 v18,s[40:43],s55 offen offset:0  lds
s_add_u32 m0,s57,33792
s_nop 0
buffer_load_dwordx4 v19,s[40:43],s55 offen offset:0  lds
s_add_u32 m0,s57,40832
s_nop 0
buffer_load_dwordx4 v18,s[40:43],s55 offen offset:128  lds
s_add_u32 m0,s57,41856
s_nop 0
buffer_load_dwordx4 v19,s[40:43],s55 offen offset:128  lds
v_add_u32 v26,49152,v20
v_add_u32 v27,49152,v21
T a[48:49],26,0
T a[50:51],26,512
T a[52:53],27,0
T a[54:55],27,512
T a[56:57],26,4096
T a[58:59],26,4608
T a[60:61],27,4096
T a[62:63],27,4608
s_waitcnt vmcnt(20) lgkmcnt(0)
s_barrier
s_add_u32 s55,s50,1310720
s_add_u32 m0,s57,49152
s_nop 0
buffer_load_dwordx4 v18,s[40:43],s55 offen offset:0  lds
s_add_u32 m0,s57,50176
s_nop 0
buffer_load_dwordx4 v19,s[40:43],s55 offen offset:0  lds
s_add_u32 m0,s57,57216
s_nop 0
buffer_load_dwordx4 v18,s[40:43],s55 offen offset:128  lds
s_add_u32 m0,s57,58240
s_nop 0
buffer_load_dwordx4 v19,s[40:43],s55 offen offset:128  lds
v_add_u32 v26,65536,v20
v_add_u32 v27,65536,v21
T a[64:65],26,0
T a[66:67],26,512
T a[68:69],27,0
T a[70:71],27,512
T a[72:73],26,4096
T a[74:75],26,4608
T a[76:77],27,4096
T a[78:79],27,4608
s_waitcnt vmcnt(20) lgkmcnt(0)
s_barrier
s_add_u32 s55,s50,1441792
s_add_u32 m0,s57,65536
s_nop 0
buffer_load_dwordx4 v18,s[40:43],s55 offen offset:0  lds
s_add_u32 m0,s57,66560
s_nop 0
buffer_load_dwordx4 v19,s[40:43],s55 offen offset:0  lds
s_add_u32 m0,s57,73600
s_nop 0
buffer_load_dwordx4 v18,s[40:43],s55 offen offset:128  lds
s_add_u32 m0,s57,74624
s_nop 0
buffer_load_dwordx4 v19,s[40:43],s55 offen offset:128  lds
v_add_u32 v26,81920,v20
v_add_u32 v27,81920,v21
T a[80:81],26,0
T a[82:83],26,512
T a[84:85],27,0
T a[86:87],27,512
T a[88:89],26,4096
T a[90:91],26,4608
T a[92:93],27,4096
T a[94:95],27,4608
s_waitcnt vmcnt(20) lgkmcnt(0)
s_barrier
s_add_u32 s55,s50,1572864
s_add_u32 m0,s57,81920
s_nop 0
buffer_load_dwordx4 v18,s[40:43],s55 offen offset:0  lds
s_add_u32 m0,s57,82944
s_nop 0
buffer_load_dwordx4 v19,s[40:43],s55 offen offset:0  lds
s_add_u32 m0,s57,89984
s_nop 0
buffer_load_dwordx4 v18,s[40:43],s55 offen offset:128  lds
s_add_u32 m0,s57,91008
s_nop 0
buffer_load_dwordx4 v19,s[40:43],s55 offen offset:128  lds
v_add_u32 v26,98304,v20
v_add_u32 v27,98304,v21
T a[96:97],26,0
T a[98:99],26,512
T a[100:101],27,0
T a[102:103],27,512
T a[104:105],26,4096
T a[106:107],26,4608
T a[108:109],27,4096
T a[110:111],27,4608
s_waitcnt vmcnt(20) lgkmcnt(0)
s_barrier
s_add_u32 s55,s50,1703936
s_add_u32 m0,s57,98304
s_nop 0
buffer_load_dwordx4 v18,s[40:43],s55 offen offset:0  lds
s_add_u32 m0,s57,99328
s_nop 0
buffer_load_dwordx4 v19,s[40:43],s55 offen offset:0  lds
s_add_u32 m0,s57,106368
s_nop 0
buffer_load_dwordx4 v18,s[40:43],s55 offen offset:128  lds
s_add_u32 m0,s57,107392
s_nop 0
buffer_load_dwordx4 v19,s[40:43],s55 offen offset:128  lds
v_add_u32 v26,0,v20
v_add_u32 v27,0,v21
T a[112:113],26,0
T a[114:115],26,512
T a[116:117],27,0
T a[118:119],27,512
T a[120:121],26,4096
T a[122:123],26,4608
T a[124:125],27,4096
T a[126:127],27,4608
s_waitcnt vmcnt(20) lgkmcnt(0)
s_barrier
s_add_u32 s55,s50,1835008
s_add_u32 m0,s57,0
s_nop 0
buffer_load_dwordx4 v18,s[40:43],s55 offen offset:0  lds
s_add_u32 m0,s57,1024
s_nop 0
buffer_load_dwordx4 v19,s[40:43],s55 offen offset:0  lds
s_add_u32 m0,s57,8064
s_nop 0
buffer_load_dwordx4 v18,s[40:43],s55 offen offset:128  lds
s_add_u32 m0,s57,9088
s_nop 0
buffer_load_dwordx4 v19,s[40:43],s55 offen offset:128  lds
v_add_u32 v26,16384,v20
v_add_u32 v27,16384,v21
T a[128:129],26,0
T a[130:131],26,512
T a[132:133],27,0
T a[134:135],27,512
T a[136:137],26,4096
T a[138:139],26,4608
T a[140:141],27,4096
T a[142:143],27,4608
s_waitcnt vmcnt(20) lgkmcnt(0)
s_barrier
s_add_u32 s55,s50,1966080
s_add_u32 m0,s57,16384
s_nop 0
buffer_load_dwordx4 v18,s[40:43],s55 offen offset:0  lds
s_add_u32 m0,s57,17408
s_nop 0
buffer_load_dwordx4 v19,s[40:43],s55 offen offset:0  lds
s_add_u32 m0,s57,24448
s_nop 0
buffer_load_dwordx4 v18,s[40:43],s55 offen offset:128  lds
s_add_u32 m0,s57,25472
s_nop 0
buffer_load_dwordx4 v19,s[40:43],s55 offen offset:128  lds
v_add_u32 v26,32768,v20
v_add_u32 v27,32768,v21
T a[144:145],26,0
T a[146:147],26,512
T a[148:149],27,0
T a[150:151],27,512
T a[152:153],26,4096
T a[154:155],26,4608
T a[156:157],27,4096
T a[158:159],27,4608
s_waitcnt vmcnt(20) lgkmcnt(0)
s_barrier
v_add_u32 v26,49152,v20
v_add_u32 v27,49152,v21
T a[160:161],26,0
T a[162:163],26,512
T a[164:165],27,0
T a[166:167],27,512
T a[168:169],26,4096
T a[170:171],26,4608
T a[172:173],27,4096
T a[174:175],27,4608
s_waitcnt vmcnt(16) lgkmcnt(0)
s_barrier
v_add_u32 v26,65536,v20
v_add_u32 v27,65536,v21
T a[176:177],26,0
T a[178:179],26,512
T a[180:181],27,0
T a[182:183],27,512
T a[184:185],26,4096
T a[186:187],26,4608
T a[188:189],27,4096
T a[190:191],27,4608
s_waitcnt vmcnt(12) lgkmcnt(0)
s_barrier
v_add_u32 v26,81920,v20
v_add_u32 v27,81920,v21
T a[192:193],26,0
T a[194:195],26,512
T a[196:197],27,0
T a[198:199],27,512
T a[200:201],26,4096
T a[202:203],26,4608
T a[204:205],27,4096
T a[206:207],27,4608
s_waitcnt vmcnt(8) lgkmcnt(0)
s_barrier
v_add_u32 v26,98304,v20
v_add_u32 v27,98304,v21
T a[208:209],26,0
T a[210:211],26,512
T a[212:213],27,0
T a[214:215],27,512
T a[216:217],26,4096
T a[218:219],26,4608
T a[220:221],27,4096
T a[222:223],27,4608
s_waitcnt vmcnt(4) lgkmcnt(0)
s_barrier
v_add_u32 v26,0,v20
v_add_u32 v27,0,v21
T a[224:225],26,0
T a[226:227],26,512
T a[228:229],27,0
T a[230:231],27,512
T a[232:233],26,4096
T a[234:235],26,4608
T a[236:237],27,4096
T a[238:239],27,4608
s_waitcnt vmcnt(0) lgkmcnt(0)
s_barrier
v_add_u32 v26,16384,v20
v_add_u32 v27,16384,v21
T a[240:241],26,0
T a[242:243],26,512
T a[244:245],27,0
T a[246:247],27,512
T a[248:249],26,4096
T a[250:251],26,4608
T a[252:253],27,4096
T a[254:255],27,4608
L 0
s_barrier
v_mov_b32 v34,0x3f803f80
v_mov_b32 v35,0x3f803f80
v_mov_b32 v36,0x3f803f80
v_mov_b32 v37,0x3f803f80
s_nop 1
v_mfma_f32_16x16x32_bf16 v[106:109],a[0:3],v[34:37],0
v_mfma_f32_16x16x32_bf16 v[110:113],a[4:7],v[34:37],0
M 106,a[8:11],34
M 110,a[12:15],34
M 106,a[16:19],34
M 110,a[20:23],34
M 106,a[24:27],34
M 110,a[28:31],34
M 106,a[32:35],34
M 110,a[36:39],34
M 106,a[40:43],34
M 110,a[44:47],34
M 106,a[48:51],34
M 110,a[52:55],34
M 106,a[56:59],34
M 110,a[60:63],34
M 106,a[64:67],34
M 110,a[68:71],34
M 106,a[72:75],34
M 110,a[76:79],34
M 106,a[80:83],34
M 110,a[84:87],34
M 106,a[88:91],34
M 110,a[92:95],34
M 106,a[96:99],34
M 110,a[100:103],34
M 106,a[104:107],34
M 110,a[108:111],34
M 106,a[112:115],34
M 110,a[116:119],34
M 106,a[120:123],34
M 110,a[124:127],34
M 106,a[128:131],34
M 110,a[132:135],34
M 106,a[136:139],34
M 110,a[140:143],34
M 106,a[144:147],34
M 110,a[148:151],34
M 106,a[152:155],34
M 110,a[156:159],34
M 106,a[160:163],34
M 110,a[164:167],34
M 106,a[168:171],34
M 110,a[172:175],34
M 106,a[176:179],34
M 110,a[180:183],34
M 106,a[184:187],34
M 110,a[188:191],34
M 106,a[192:195],34
M 110,a[196:199],34
M 106,a[200:203],34
M 110,a[204:207],34
M 106,a[208:211],34
M 110,a[212:215],34
M 106,a[216:219],34
M 110,a[220:223],34
M 106,a[224:227],34
M 110,a[228:231],34
M 106,a[232:235],34
M 110,a[236:239],34
M 106,a[240:243],34
M 110,a[244:247],34
M 106,a[248:251],34
M 110,a[252:255],34
v_mov_b32 v4,s53
s_mov_b32 s82,0
s_mov_b32 s80,0
s_mov_b32 s54,s45
s_mul_i32 s57,s44,4096
s_mul_i32 s55,s45,32768
s_add_u32 s55,s55,s57
s_add_u32 s55,s55,8388608
s_mul_i32 s57,s44,4096
s_mul_i32 s60,s46,32768
s_mul_i32 s61,s46,1024
s_mul_i32 s62,s46,40960
s_mul_i32 s63,s46,1024
s_mul_i32 s64,s46,512
s_lshl_b32 s59,s44,8
s_add_u32 s59,s59,131072
s_mul_i32 s58,s45,1024
s_add_u32 s58,s58,109056000
s_mul_i32 s65,s45,40960
s_add_u32 s65,s65,s47
s_mul_i32 s68,s45,1024
s_add_u32 s68,s68,s48
s_mul_i32 s73,s45,512
s_add_u32 s73,s73,106954752
s_mul_i32 s74,s45,32768
s_add_u32 s74,s74,s49
s_mul_i32 s75,s45,1024
s_add_u32 s75,s75,107479040
s_mov_b32 s66,0x7fff0000
s_mov_b32 s67,0x7fff0000
s_mov_b32 s69,0x7fff0000
s_mov_b32 s76,0x7fff0000
s_mov_b32 s77,0x7fff0000
s_mov_b32 s70,0x7fff0000
s_mov_b32 s71,0x7fff0000
s_mov_b32 s72,0x7fff0000
v_mov_b32 v1,v0
v_add_u32 v2,16384,v0
s_mov_b32 s84,16384
s_mov_b32 s86,0x3a800000
s_mov_b32 s87,0x3727c5ac
s_mov_b32 s88,0xc0135761
s_mov_b32 s89,0x3d372713
s_lshr_b32 s85,s44,1
s_mul_i32 s85,s85,127
v_lshlrev_b32 v8,22,v0
v_and_b32 v8,0x80000000,v8
v_or_b32 v8,v8,v5
v_add_u32 v2,32768,v0
s_lshl_b32 s84,s57,1
s_add_u32 s84,s84,32768
s_getpc_b64 s[40:41]
Lg1_pc_%=:
s_lshl_b32 s78,s44,13
v_lshlrev_b32 v114,3,v0
v_add_u32 v114,s78,v114
v_min_u32 v114,Lcode_g2_b-Lg1_pc_%=-4,v114
)"
R"(s_mov_b32 m0,163584
s_nop 0
global_load_lds_dword v114,s[40:41]
s_add_u32 m0,s59,4096
s_nop 0
buffer_load_dword v8,s[36:39],s58 offen offset:0 sc1 lds
buffer_store_dword v4,v3,s[36:39],s71 offen offset:0 sc0 sc1
s_add_u32 m0,s59,5120
s_nop 0
buffer_load_dword v8,s[36:39],s58 offen offset:0 sc1 lds
buffer_store_dword v4,v3,s[36:39],s71 offen offset:0 sc0 sc1
s_mov_b32 s83,0
Lg1_poll_%=:
s_add_u32 s78,s83,2
s_and_b32 s78,s78,3
s_lshl_b32 s78,s78,10
s_add_u32 s78,s78,4096
s_add_u32 m0,s59,s78
s_nop 0
buffer_load_dword v8,s[36:39],s58 offen offset:0 sc1 lds
s_and_b32 s78,s83,3
s_lshl_b32 s78,s78,10
v_add_u32 v114,s78,v6
s_waitcnt vmcnt(4)
s_barrier
ds_read_b32 v7,v114 offset:4096
buffer_store_dword v4,v3,s[36:39],s70 offen offset:0 sc0 sc1
s_mov_b32 s70,s71
s_mov_b32 s71,s72
s_mov_b32 s72,0x7fff0000
s_add_u32 s83,s83,1
s_add_u32 s82,s82,1
L 0
v_cmp_eq_u32 vcc,s53,v7
s_nop 0
s_cmp_eq_u32 vcc_lo,-1
s_cbranch_scc1 Lg1_go_%=
s_cmp_lt_u32 s82,20000
s_cbranch_scc1 Lg1_poll_%=
Lg1_go_%=:
s_add_u32 m0,s57,0
s_add_u32 s56,s55,0
D 0,56,0
D 0,56,1024
D 0,56,2048
D 0,56,3072
s_add_u32 s58,s58,s61
s_add_u32 m0,s59,2048
s_nop 0
buffer_load_dword v8,s[36:39],s58 offen offset:0 sc1 lds
s_add_u32 m0,s57,16384
s_add_u32 s56,s55,16384
D 0,56,0
D 0,56,1024
D 0,56,2048
D 0,56,3072
s_add_u32 s55,s55,s60
s_sub_u32 s57,s84,s57
s_add_u32 s54,s54,s46
s_waitcnt vmcnt(4)
s_barrier
buffer_store_dword v4,v3,s[36:39],s70 offen offset:0 sc0 sc1
s_min_u32 s71,s71,s72
buffer_store_dword v4,v3,s[36:39],s71 offen offset:0 sc0 sc1
s_mov_b32 s70,0x7fff0000
s_mov_b32 s71,0x7fff0000
s_mov_b32 s72,0x7fff0000
s_cmp_eq_u32 s80,1
buffer_store_dword v4,v3,s[36:39],s71 offen offset:0 sc0 sc1
s_cbranch_scc1 Lg1_st1_%=
s_branch Lg1_st0_%=
Lg1_gox_%=:
s_add_u32 m0,s59,2048
s_nop 0
buffer_load_dword v8,s[36:39],s58 offen offset:0 sc1 lds
s_waitcnt vmcnt(0)
s_barrier
buffer_store_dword v4,v3,s[36:39],s70 offen offset:0 sc0 sc1
s_min_u32 s71,s71,s72
buffer_store_dword v4,v3,s[36:39],s71 offen offset:0 sc0 sc1
s_mov_b32 s70,0x7fff0000
s_mov_b32 s71,0x7fff0000
s_mov_b32 s72,0x7fff0000
s_cmp_eq_u32 s80,1
s_cbranch_scc1 Lg1_st1_%=
Lg1_st0_%=:
ds_read_b32 v7,v6 offset:2048
R 34,1,0
R 38,1,1024
R 42,1,2048
R 46,1,3072
R 50,1,4096
L 5
v_cmp_eq_u32 vcc,s53,v7
L 3
s_cmp_eq_u32 vcc_lo,-1
s_cbranch_scc1 Lg1_F0_%=
s_branch Lg1_L0_%=
Lg1_st1_%=:
ds_read_b32 v7,v6 offset:2048
R 34,2,0
R 38,2,1024
R 42,2,2048
R 46,2,3072
R 50,2,4096
L 5
v_cmp_eq_u32 vcc,s53,v7
L 3
s_cmp_eq_u32 vcc_lo,-1
s_cbranch_scc1 Lg1_F1_%=
s_branch Lg1_L1_%=
Lg1_F0_%=:
v_mfma_f32_16x16x32_bf16 v[22:25],a[4:7],v[34:37],0
s_add_u32 s58,s58,s61
s_add_u32 m0,s59,0
R 54,1,5120
M 22,a[12:15],38
buffer_load_dword v8,s[36:39],s58 offen offset:0 sc1 lds
v_mfma_f32_16x16x32_bf16 v[18:21],a[0:3],v[34:37],0
s_add_u32 m0,s57,0
R 58,1,6144
v_fma_f32 v26,-v102,v106,v26
M 18,a[8:11],38
L 3
s_add_u32 s56,s55,0
v_fma_f32 v27,-v102,v107,v27
M 18,a[16:19],42
s_mov_b32 s66,s65
R 62,1,7168
v_fma_f32 v28,-v102,v108,v28
M 18,a[24:27],46
D 0,56,0
M 22,a[20:23],42
s_add_u32 s65,s65,s62
R 66,1,8192
v_fma_f32 v29,-v102,v109,v29
v_mul_f32 v114,v26,v26
M 22,a[28:31],46
L 3
D 0,56,1024
M 22,a[36:39],50
s_add_u32 s54,s54,s46
R 70,1,9216
v_mul_f32 v115,v27,v27
M 22,a[44:47],54
v_mul_f32 v116,v28,v28
v_mul_f32 v117,v29,v29
M 18,a[32:35],50
R 74,1,10240
v_fma_f32 v114,v104,v114,v103
M 18,a[40:43],54
L 3
D 0,56,2048
M 18,a[48:51],58
R 78,1,11264
v_fma_f32 v115,v104,v115,v103
M 18,a[56:59],62
D 0,56,3072
v_fma_f32 v116,v104,v116,v103
M 22,a[52:55],58
s_add_u32 m0,s57,16384
R 82,1,12288
v_fma_f32 v117,v104,v117,v103
M 22,a[60:63],62
L 3
v_mul_f32 v114,v26,v114
v_mul_f32 v115,v27,v115
M 22,a[68:71],66
s_add_u32 s56,s55,16384
R 86,1,13312
v_mul_f32 v116,v28,v116
M 22,a[76:79],70
buffer_load_dwordx4 v[98:101],v11,s[36:39],s73 offen offset:0 sc1
M 18,a[64:67],66
s_add_u32 s73,s73,s64
R 90,1,14336
v_mul_f32 v117,v29,v117
M 18,a[72:75],70
L 3
v_exp_f32 v114,v114
v_fma_f32 v30,-v102,v110,v30
M 18,a[80:83],74
s_waitcnt vmcnt(9)
R 94,1,15360
v_fma_f32 v114,v114,v105,v105
M 18,a[88:91],78
s_barrier
v_exp_f32 v115,v115
M 22,a[84:87],74
s_add_u32 s55,s55,s60
R 34,1,16384
v_fma_f32 v115,v115,v105,v105
M 22,a[92:95],78
L 3
v_exp_f32 v116,v116
M 22,a[100:103],82
s_sub_u32 s57,s84,s57
R 38,1,17408
v_fma_f32 v116,v116,v105,v105
M 22,a[108:111],86
D 0,56,0
M 18,a[96:99],82
R 42,1,18432
v_exp_f32 v117,v117
M 18,a[104:107],86
L 3
v_rcp_f32 v114,v114
M 18,a[112:115],90
R 46,1,19456
v_fma_f32 v117,v117,v105,v105
M 18,a[120:123],94
v_rcp_f32 v115,v115
M 22,a[116:119],90
R 50,1,20480
v_mul_f32 v26,v26,v114
M 22,a[124:127],94
L 3
v_rcp_f32 v116,v116
M 22,a[132:135],34
R 54,1,21504
v_mul_f32 v27,v27,v115
M 22,a[140:143],38
D 0,56,1024
v_mul_f32 v28,v28,v116
M 18,a[128:131],34
R 58,1,22528
v_cvt_pk_bf16_f32 v122,v26,v27
M 18,a[136:139],38
L 3
v_rcp_f32 v117,v117
M 18,a[144:147],42
R 62,1,23552
v_mul_f32 v29,v29,v117
M 18,a[152:155],46
v_cvt_pk_bf16_f32 v123,v28,v29
v_fma_f32 v31,-v102,v111,v31
M 22,a[148:151],42
R 66,1,24576
v_fma_f32 v32,-v102,v112,v32
M 22,a[156:159],46
L 3
D 0,56,2048
v_fma_f32 v33,-v102,v113,v33
M 22,a[164:167],50
R 70,1,25600
v_mul_f32 v118,v30,v30
M 22,a[172:175],54
v_mul_f32 v119,v31,v31
v_mul_f32 v120,v32,v32
M 18,a[160:163],50
R 74,1,26624
v_mul_f32 v121,v33,v33
M 18,a[168:171],54
L 3
v_fma_f32 v118,v104,v118,v103
v_fma_f32 v119,v104,v119,v103
M 18,a[176:179],58
R 78,1,27648
v_fma_f32 v120,v104,v120,v103
M 18,a[184:187],62
v_fma_f32 v121,v104,v121,v103
v_mul_f32 v118,v30,v118
M 22,a[180:183],58
R 82,1,28672
v_exp_f32 v118,v118
M 22,a[188:191],62
L 3
D 0,56,3072
M 22,a[196:199],66
R 86,1,29696
v_mul_f32 v119,v31,v119
M 22,a[204:207],70
v_exp_f32 v119,v119
M 18,a[192:195],66
R 90,1,30720
v_mul_f32 v120,v32,v120
M 18,a[200:203],70
L 3
v_mul_f32 v121,v33,v121
v_fma_f32 v118,v118,v105,v105
M 18,a[208:211],74
s_waitcnt vmcnt(5)
R 94,1,31744
v_fma_f32 v119,v119,v105,v105
M 18,a[216:219],78
s_barrier
ds_read_b32 v7,v6 offset:0
v_exp_f32 v120,v120
M 22,a[212:215],74
R 34,2,0
v_fma_f32 v120,v120,v105,v105
M 22,a[220:223],78
L 4
buffer_store_dword v4,v3,s[36:39],s70 offen offset:0 sc0 sc1
v_exp_f32 v121,v121
M 22,a[228:231],82
s_mov_b32 s70,s69
R 38,2,1024
v_fma_f32 v121,v121,v105,v105
v_rcp_f32 v118,v118
v_rcp_f32 v119,v119
v_rcp_f32 v120,v120
v_rcp_f32 v121,v121
M 22,a[236:239],86
s_mov_b32 s69,s68
v_mul_f32 v30,v30,v118
v_mul_f32 v31,v31,v119
v_mul_f32 v32,v32,v120
v_mul_f32 v33,v33,v121
v_cvt_pk_bf16_f32 v124,v30,v31
v_cvt_pk_bf16_f32 v125,v32,v33
M 18,a[224:227],82
s_add_u32 s68,s68,s63
R 42,2,2048
buffer_store_dwordx4 v[122:125],v15,s[36:39],s67 offen offset:0 sc0 sc1
M 18,a[232:235],86
L 4
L 3
M 18,a[240:243],90
R 46,2,3072
v_cmp_eq_u32 vcc,s53,v7
M 18,a[248:251],94
buffer_store_dwordx4 v[122:125],v16,s[36:39],s67 offen offset:0 sc0 sc1
M 22,a[244:247],90
s_waitcnt vmcnt(7) lgkmcnt(2)
s_cmp_eq_u32 vcc_lo,-1
R 50,2,4096
M 22,a[252:255],94
s_cbranch_scc0 Lg1_K1_%=
Lg1_F1_%=:
v_mfma_f32_16x16x32_bf16 v[30:33],a[4:7],v[34:37],0
s_add_u32 s58,s58,s61
s_add_u32 m0,s59,1024
R 54,2,5120
M 30,a[12:15],38
buffer_load_dword v8,s[36:39],s58 offen offset:0 sc1 lds
v_mfma_f32_16x16x32_bf16 v[26:29],a[0:3],v[34:37],0
s_add_u32 m0,s57,0
R 58,2,6144
v_fma_f32 v18,-v98,v106,v18
M 26,a[8:11],38
L 3
s_add_u32 s56,s55,0
v_fma_f32 v19,-v98,v107,v19
M 26,a[16:19],42
s_mov_b32 s67,s65
R 62,2,7168
v_fma_f32 v20,-v98,v108,v20
M 26,a[24:27],46
D 0,56,0
M 30,a[20:23],42
s_add_u32 s65,s65,s62
R 66,2,8192
v_fma_f32 v21,-v98,v109,v21
v_mul_f32 v114,v18,v18
M 30,a[28:31],46
L 3
D 0,56,1024
M 30,a[36:39],50
s_add_u32 s54,s54,s46
R 70,2,9216
v_mul_f32 v115,v19,v19
M 30,a[44:47],54
v_mul_f32 v116,v20,v20
v_mul_f32 v117,v21,v21
M 26,a[32:35],50
R 74,2,10240
v_fma_f32 v114,v100,v114,v99
M 26,a[40:43],54
L 3
D 0,56,2048
M 26,a[48:51],58
R 78,2,11264
v_fma_f32 v115,v100,v115,v99
M 26,a[56:59],62
D 0,56,3072
v_fma_f32 v116,v100,v116,v99
M 30,a[52:55],58
s_add_u32 m0,s57,16384
R 82,2,12288
v_fma_f32 v117,v100,v117,v99
M 30,a[60:63],62
L 3
v_mul_f32 v114,v18,v114
v_mul_f32 v115,v19,v115
M 30,a[68:71],66
s_add_u32 s56,s55,16384
R 86,2,13312
v_mul_f32 v116,v20,v116
M 30,a[76:79],70
buffer_load_dwordx4 v[102:105],v11,s[36:39],s73 offen offset:0 sc1
M 26,a[64:67],66
s_add_u32 s73,s73,s64
R 90,2,14336
v_mul_f32 v117,v21,v117
M 26,a[72:75],70
L 3
v_exp_f32 v114,v114
v_fma_f32 v22,-v98,v110,v22
M 26,a[80:83],74
s_waitcnt vmcnt(9)
R 94,2,15360
v_fma_f32 v114,v114,v101,v101
M 26,a[88:91],78
s_barrier
v_exp_f32 v115,v115
M 30,a[84:87],74
s_add_u32 s55,s55,s60
R 34,2,16384
v_fma_f32 v115,v115,v101,v101
M 30,a[92:95],78
L 3
v_exp_f32 v116,v116
M 30,a[100:103],82
s_sub_u32 s57,s84,s57
R 38,2,17408
v_fma_f32 v116,v116,v101,v101
M 30,a[108:111],86
D 0,56,0
M 26,a[96:99],82
R 42,2,18432
v_exp_f32 v117,v117
M 26,a[104:107],86
L 3
v_rcp_f32 v114,v114
M 26,a[112:115],90
R 46,2,19456
v_fma_f32 v117,v117,v101,v101
M 26,a[120:123],94
v_rcp_f32 v115,v115
M 30,a[116:119],90
R 50,2,20480
v_mul_f32 v18,v18,v114
M 30,a[124:127],94
L 3
v_rcp_f32 v116,v116
M 30,a[132:135],34
R 54,2,21504
v_mul_f32 v19,v19,v115
M 30,a[140:143],38
D 0,56,1024
v_mul_f32 v20,v20,v116
M 26,a[128:131],34
R 58,2,22528
v_cvt_pk_bf16_f32 v122,v18,v19
M 26,a[136:139],38
L 3
v_rcp_f32 v117,v117
M 26,a[144:147],42
R 62,2,23552
v_mul_f32 v21,v21,v117
M 26,a[152:155],46
v_cvt_pk_bf16_f32 v123,v20,v21
v_fma_f32 v23,-v98,v111,v23
M 30,a[148:151],42
R 66,2,24576
v_fma_f32 v24,-v98,v112,v24
M 30,a[156:159],46
L 3
D 0,56,2048
v_fma_f32 v25,-v98,v113,v25
M 30,a[164:167],50
R 70,2,25600
v_mul_f32 v118,v22,v22
M 30,a[172:175],54
v_mul_f32 v119,v23,v23
v_mul_f32 v120,v24,v24
M 26,a[160:163],50
R 74,2,26624
v_mul_f32 v121,v25,v25
M 26,a[168:171],54
L 3
v_fma_f32 v118,v100,v118,v99
v_fma_f32 v119,v100,v119,v99
M 26,a[176:179],58
R 78,2,27648
v_fma_f32 v120,v100,v120,v99
M 26,a[184:187],62
v_fma_f32 v121,v100,v121,v99
v_mul_f32 v118,v22,v118
M 30,a[180:183],58
R 82,2,28672
v_exp_f32 v118,v118
M 30,a[188:191],62
L 3
D 0,56,3072
M 30,a[196:199],66
R 86,2,29696
v_mul_f32 v119,v23,v119
M 30,a[204:207],70
v_exp_f32 v119,v119
M 26,a[192:195],66
R 90,2,30720
v_mul_f32 v120,v24,v120
M 26,a[200:203],70
L 3
v_mul_f32 v121,v25,v121
v_fma_f32 v118,v118,v101,v101
M 26,a[208:211],74
s_waitcnt vmcnt(5)
)"
R"(R 94,2,31744
v_fma_f32 v119,v119,v101,v101
M 26,a[216:219],78
s_barrier
ds_read_b32 v7,v6 offset:1024
v_exp_f32 v120,v120
M 30,a[212:215],74
R 34,1,0
v_fma_f32 v120,v120,v101,v101
M 30,a[220:223],78
L 4
buffer_store_dword v4,v3,s[36:39],s70 offen offset:0 sc0 sc1
v_exp_f32 v121,v121
M 30,a[228:231],82
s_mov_b32 s70,s69
R 38,1,1024
v_fma_f32 v121,v121,v101,v101
v_rcp_f32 v118,v118
v_rcp_f32 v119,v119
v_rcp_f32 v120,v120
v_rcp_f32 v121,v121
M 30,a[236:239],86
s_mov_b32 s69,s68
v_mul_f32 v22,v22,v118
v_mul_f32 v23,v23,v119
v_mul_f32 v24,v24,v120
v_mul_f32 v25,v25,v121
v_cvt_pk_bf16_f32 v124,v22,v23
v_cvt_pk_bf16_f32 v125,v24,v25
M 26,a[224:227],82
s_add_u32 s68,s68,s63
R 42,1,2048
buffer_store_dwordx4 v[122:125],v15,s[36:39],s66 offen offset:0 sc0 sc1
M 26,a[232:235],86
L 4
L 3
M 26,a[240:243],90
R 46,1,3072
v_cmp_eq_u32 vcc,s53,v7
M 26,a[248:251],94
buffer_store_dwordx4 v[122:125],v16,s[36:39],s66 offen offset:0 sc0 sc1
M 30,a[244:247],90
s_waitcnt vmcnt(7) lgkmcnt(2)
s_cmp_eq_u32 vcc_lo,-1
R 50,1,4096
M 30,a[252:255],94
s_cbranch_scc1 Lg1_F0_%=
Lg1_K0_%=:
Lg1_L0_%=:
v_mfma_f32_16x16x32_bf16 v[22:25],a[4:7],v[34:37],0
s_add_u32 m0,s59,3072
s_mov_b32 s66,s65
R 54,1,5120
M 22,a[12:15],38
buffer_load_dword v8,s[36:39],s58 offen offset:0 sc1 lds
v_mfma_f32_16x16x32_bf16 v[18:21],a[0:3],v[34:37],0
s_add_u32 s65,s65,s62
R 58,1,6144
v_fma_f32 v26,-v102,v106,v26
M 18,a[8:11],38
L 3
v_fma_f32 v27,-v102,v107,v27
v_fma_f32 v28,-v102,v108,v28
M 18,a[16:19],42
R 62,1,7168
v_fma_f32 v29,-v102,v109,v29
M 18,a[24:27],46
v_mul_f32 v114,v26,v26
v_mul_f32 v115,v27,v27
M 22,a[20:23],42
R 66,1,8192
v_mul_f32 v116,v28,v28
M 22,a[28:31],46
L 3
v_mul_f32 v117,v29,v29
v_fma_f32 v114,v104,v114,v103
M 22,a[36:39],50
R 70,1,9216
v_fma_f32 v115,v104,v115,v103
M 22,a[44:47],54
v_fma_f32 v116,v104,v116,v103
v_fma_f32 v117,v104,v117,v103
M 18,a[32:35],50
R 74,1,10240
v_mul_f32 v114,v26,v114
M 18,a[40:43],54
L 3
v_exp_f32 v114,v114
M 18,a[48:51],58
R 78,1,11264
v_mul_f32 v115,v27,v115
M 18,a[56:59],62
v_exp_f32 v115,v115
M 22,a[52:55],58
R 82,1,12288
v_mul_f32 v116,v28,v116
M 22,a[60:63],62
L 3
v_exp_f32 v116,v116
M 22,a[68:71],66
R 86,1,13312
v_mul_f32 v117,v29,v117
M 22,a[76:79],70
buffer_load_dwordx4 v[98:101],v11,s[36:39],s73 offen offset:0 sc1
M 18,a[64:67],66
s_add_u32 s73,s73,s64
R 90,1,14336
v_fma_f32 v114,v114,v105,v105
M 18,a[72:75],70
L 3
v_exp_f32 v117,v117
M 18,a[80:83],74
s_waitcnt vmcnt(1)
R 94,1,15360
v_fma_f32 v115,v115,v105,v105
M 18,a[88:91],78
s_barrier
ds_read_b32 v7,v6 offset:3072
v_fma_f32 v116,v116,v105,v105
M 22,a[84:87],74
R 34,1,16384
v_fma_f32 v117,v117,v105,v105
M 22,a[92:95],78
L 4
buffer_store_dword v4,v3,s[36:39],s70 offen offset:0 sc0 sc1
M 22,a[100:103],82
s_mov_b32 s70,0x7fff0000
R 38,1,17408
v_fma_f32 v30,-v102,v110,v30
M 22,a[108:111],86
v_rcp_f32 v114,v114
M 18,a[96:99],82
R 42,1,18432
v_mul_f32 v26,v26,v114
M 18,a[104:107],86
L 4
L 3
v_cmp_eq_u32 vcc,s53,v7
M 18,a[112:115],90
R 46,1,19456
v_fma_f32 v31,-v102,v111,v31
M 18,a[120:123],94
v_rcp_f32 v115,v115
M 22,a[116:119],90
s_cmp_eq_u32 vcc_lo,-1
R 50,1,20480
v_mul_f32 v27,v27,v115
s_cbranch_scc0 Lg1_X0_%=
M 22,a[124:127],94
L 3
s_add_u32 m0,s57,0
s_add_u32 s56,s55,0
M 22,a[132:135],34
s_add_u32 s58,s58,s61
R 54,1,21504
v_cvt_pk_bf16_f32 v122,v26,v27
M 22,a[140:143],38
D 0,56,0
M 18,a[128:131],34
s_add_u32 s54,s54,s46
R 58,1,22528
v_fma_f32 v32,-v102,v112,v32
M 18,a[136:139],38
L 3
v_rcp_f32 v116,v116
M 18,a[144:147],42
R 62,1,23552
v_mul_f32 v28,v28,v116
M 18,a[152:155],46
D 0,56,1024
M 22,a[148:151],42
R 66,1,24576
v_fma_f32 v33,-v102,v113,v33
M 22,a[156:159],46
L 3
v_rcp_f32 v117,v117
M 22,a[164:167],50
R 70,1,25600
v_mul_f32 v29,v29,v117
M 22,a[172:175],54
D 0,56,2048
M 18,a[160:163],50
R 74,1,26624
v_cvt_pk_bf16_f32 v123,v28,v29
M 18,a[168:171],54
L 3
v_mul_f32 v118,v30,v30
v_mul_f32 v119,v31,v31
M 18,a[176:179],58
R 78,1,27648
v_mul_f32 v120,v32,v32
M 18,a[184:187],62
D 0,56,3072
M 22,a[180:183],58
s_add_u32 m0,s59,0
R 82,1,28672
v_mul_f32 v121,v33,v33
M 22,a[188:191],62
L 3
buffer_load_dword v8,s[36:39],s58 offen offset:0 sc1 lds
M 22,a[196:199],66
s_add_u32 m0,s57,16384
R 86,1,29696
v_fma_f32 v118,v104,v118,v103
M 22,a[204:207],70
s_add_u32 s56,s55,16384
v_fma_f32 v119,v104,v119,v103
v_fma_f32 v120,v104,v120,v103
M 18,a[192:195],66
s_add_u32 s55,s55,s60
R 90,1,30720
v_fma_f32 v121,v104,v121,v103
M 18,a[200:203],70
L 3
D 0,56,0
M 18,a[208:211],74
s_waitcnt vmcnt(1)
R 94,1,31744
v_mul_f32 v118,v30,v118
M 18,a[216:219],78
s_barrier
ds_read_b32 v7,v6 offset:0
v_mul_f32 v119,v31,v119
M 22,a[212:215],74
s_sub_u32 s57,s84,s57
R 34,2,0
v_mul_f32 v120,v32,v120
M 22,a[220:223],78
L 4
buffer_store_dword v4,v3,s[36:39],s70 offen offset:0 sc0 sc1
v_mul_f32 v121,v33,v121
v_exp_f32 v118,v118
v_exp_f32 v119,v119
v_exp_f32 v120,v120
v_exp_f32 v121,v121
M 22,a[228:231],82
s_mov_b32 s70,s69
R 38,2,1024
v_fma_f32 v118,v118,v105,v105
v_fma_f32 v119,v119,v105,v105
v_fma_f32 v120,v120,v105,v105
v_fma_f32 v121,v121,v105,v105
v_rcp_f32 v118,v118
v_rcp_f32 v119,v119
v_rcp_f32 v120,v120
v_rcp_f32 v121,v121
M 22,a[236:239],86
s_mov_b32 s69,s68
v_mul_f32 v30,v30,v118
v_mul_f32 v31,v31,v119
v_mul_f32 v32,v32,v120
v_mul_f32 v33,v33,v121
v_cvt_pk_bf16_f32 v124,v30,v31
v_cvt_pk_bf16_f32 v125,v32,v33
M 18,a[224:227],82
s_add_u32 s68,s68,s63
R 42,2,2048
buffer_store_dwordx4 v[122:125],v15,s[36:39],s67 offen offset:0 sc0 sc1
M 18,a[232:235],86
L 4
L 3
M 18,a[240:243],90
R 46,2,3072
v_cmp_eq_u32 vcc,s53,v7
M 18,a[248:251],94
D 0,56,1024
M 22,a[244:247],90
L 2
s_cmp_eq_u32 vcc_lo,-1
R 50,2,4096
buffer_store_dwordx4 v[122:125],v16,s[36:39],s67 offen offset:0 sc0 sc1
M 22,a[252:255],94
D 0,56,2048
D 0,56,3072
buffer_store_dword v4,v3,s[36:39],s71 offen offset:0 sc0 sc1
buffer_store_dword v4,v3,s[36:39],s71 offen offset:0 sc0 sc1
buffer_store_dword v4,v3,s[36:39],s71 offen offset:0 sc0 sc1
s_cbranch_scc1 Lg1_F1_%=
s_branch Lg1_K1_%=
Lg1_X0_%=:
M 22,a[124:127],94
L 3
s_add_u32 m0,s59,0
v_cvt_pk_bf16_f32 v122,v26,v27
M 22,a[132:135],34
R 54,1,21504
v_fma_f32 v32,-v102,v112,v32
M 22,a[140:143],38
buffer_load_dword v8,s[36:39],s58 offen offset:0 sc1 lds
M 18,a[128:131],34
R 58,1,22528
v_fma_f32 v33,-v102,v113,v33
M 18,a[136:139],38
L 3
v_rcp_f32 v116,v116
M 18,a[144:147],42
R 62,1,23552
v_mul_f32 v28,v28,v116
M 18,a[152:155],46
v_rcp_f32 v117,v117
M 22,a[148:151],42
R 66,1,24576
v_mul_f32 v29,v29,v117
M 22,a[156:159],46
L 3
v_cvt_pk_bf16_f32 v123,v28,v29
v_mul_f32 v118,v30,v30
M 22,a[164:167],50
R 70,1,25600
v_mul_f32 v119,v31,v31
M 22,a[172:175],54
v_mul_f32 v120,v32,v32
v_mul_f32 v121,v33,v33
M 18,a[160:163],50
R 74,1,26624
v_fma_f32 v118,v104,v118,v103
M 18,a[168:171],54
L 3
v_fma_f32 v119,v104,v119,v103
v_fma_f32 v120,v104,v120,v103
M 18,a[176:179],58
R 78,1,27648
v_fma_f32 v121,v104,v121,v103
M 18,a[184:187],62
v_mul_f32 v118,v30,v118
v_mul_f32 v119,v31,v119
M 22,a[180:183],58
R 82,1,28672
v_mul_f32 v120,v32,v120
M 22,a[188:191],62
L 3
v_exp_f32 v118,v118
M 22,a[196:199],66
R 86,1,29696
v_mul_f32 v121,v33,v121
M 22,a[204:207],70
v_exp_f32 v119,v119
M 18,a[192:195],66
R 90,1,30720
v_fma_f32 v118,v118,v105,v105
M 18,a[200:203],70
L 3
v_exp_f32 v120,v120
M 18,a[208:211],74
s_waitcnt vmcnt(0)
R 94,1,31744
v_fma_f32 v119,v119,v105,v105
M 18,a[216:219],78
s_barrier
buffer_store_dword v4,v3,s[36:39],s70 offen offset:0 sc0 sc1
M 22,a[212:215],74
s_mov_b32 s70,s69
ds_read_b32 v7,v6 offset:0
v_fma_f32 v120,v120,v105,v105
M 22,a[220:223],78
L 3
v_exp_f32 v121,v121
M 22,a[228:231],82
s_mov_b32 s69,s68
v_fma_f32 v121,v121,v105,v105
v_rcp_f32 v118,v118
v_rcp_f32 v119,v119
v_rcp_f32 v120,v120
v_rcp_f32 v121,v121
M 22,a[236:239],86
s_add_u32 s68,s68,s63
v_mul_f32 v30,v30,v118
v_mul_f32 v31,v31,v119
v_mul_f32 v32,v32,v120
v_mul_f32 v33,v33,v121
v_cvt_pk_bf16_f32 v124,v30,v31
v_cvt_pk_bf16_f32 v125,v32,v33
M 18,a[224:227],82
buffer_store_dwordx4 v[122:125],v15,s[36:39],s67 offen offset:0 sc0 sc1
M 18,a[232:235],86
L 1
M 18,a[240:243],90
L 0
v_cmp_eq_u32 vcc,s53,v7
M 18,a[248:251],94
buffer_store_dwordx4 v[122:125],v16,s[36:39],s67 offen offset:0 sc0 sc1
M 22,a[244:247],90
s_nop 0
M 22,a[252:255],94
s_mov_b32 s80,1
s_cmp_eq_u32 vcc_lo,-1
s_cbranch_scc0 Lg1_xn0_%=
s_add_u32 m0,s57,0
s_add_u32 s56,s55,0
D 0,56,0
v_fma_f32 v18,-v98,v106,v18
v_fma_f32 v19,-v98,v107,v19
v_fma_f32 v20,-v98,v108,v20
v_fma_f32 v21,-v98,v109,v21
v_mul_f32 v114,v18,v18
v_mul_f32 v115,v19,v19
v_mul_f32 v116,v20,v20
D 0,56,1024
v_mul_f32 v117,v21,v21
v_fma_f32 v114,v100,v114,v99
v_fma_f32 v115,v100,v115,v99
v_fma_f32 v116,v100,v116,v99
v_fma_f32 v117,v100,v117,v99
v_mul_f32 v114,v18,v114
v_mul_f32 v115,v19,v115
D 0,56,2048
v_mul_f32 v116,v20,v116
v_mul_f32 v117,v21,v117
v_exp_f32 v114,v114
v_exp_f32 v115,v115
v_exp_f32 v116,v116
v_exp_f32 v117,v117
v_fma_f32 v114,v114,v101,v101
D 0,56,3072
v_fma_f32 v115,v115,v101,v101
v_fma_f32 v116,v116,v101,v101
v_fma_f32 v117,v117,v101,v101
v_rcp_f32 v114,v114
v_rcp_f32 v115,v115
v_rcp_f32 v116,v116
v_rcp_f32 v117,v117
s_add_u32 s58,s58,s61
s_add_u32 m0,s57,16384
s_add_u32 s56,s55,16384
D 0,56,0
v_mul_f32 v18,v18,v114
v_mul_f32 v19,v19,v115
v_mul_f32 v20,v20,v116
v_mul_f32 v21,v21,v117
v_cvt_pk_bf16_f32 v122,v18,v19
v_cvt_pk_bf16_f32 v123,v20,v21
v_fma_f32 v22,-v98,v110,v22
D 0,56,1024
v_fma_f32 v23,-v98,v111,v23
v_fma_f32 v24,-v98,v112,v24
v_fma_f32 v25,-v98,v113,v25
v_mul_f32 v118,v22,v22
v_mul_f32 v119,v23,v23
v_mul_f32 v120,v24,v24
v_mul_f32 v121,v25,v25
D 0,56,2048
v_fma_f32 v118,v100,v118,v99
v_fma_f32 v119,v100,v119,v99
v_fma_f32 v120,v100,v120,v99
v_fma_f32 v121,v100,v121,v99
v_mul_f32 v118,v22,v118
v_mul_f32 v119,v23,v119
v_mul_f32 v120,v24,v120
D 0,56,3072
v_mul_f32 v121,v25,v121
v_exp_f32 v118,v118
v_exp_f32 v119,v119
v_exp_f32 v120,v120
v_exp_f32 v121,v121
v_fma_f32 v118,v118,v101,v101
v_fma_f32 v119,v119,v101,v101
s_add_u32 s55,s55,s60
s_sub_u32 s57,s84,s57
s_add_u32 s54,s54,s46
v_fma_f32 v120,v120,v101,v101
v_fma_f32 v121,v121,v101,v101
v_rcp_f32 v118,v118
v_rcp_f32 v119,v119
v_rcp_f32 v120,v120
v_rcp_f32 v121,v121
v_mul_f32 v22,v22,v118
v_mul_f32 v23,v23,v119
v_mul_f32 v24,v24,v120
v_mul_f32 v25,v25,v121
v_cvt_pk_bf16_f32 v124,v22,v23
v_cvt_pk_bf16_f32 v125,v24,v25
buffer_store_dwordx4 v[122:125],v15,s[36:39],s66 offen offset:0 sc0 sc1
buffer_store_dwordx4 v[122:125],v16,s[36:39],s66 offen offset:0 sc0 sc1
s_mov_b32 s72,s69
s_mov_b32 s69,0x7fff0000
s_mov_b32 s66,0x7fff0000
s_mov_b32 s76,0x7fff0000
s_branch Lg1_gox_%=
Lg1_xn0_%=:
s_cmp_ge_u32 s54,256
s_cbranch_scc1 Lg1_fin0_%=
s_add_u32 m0,s59,4096
s_nop 0
buffer_load_dword v8,s[36:39],s58 offen offset:0 sc1 lds
buffer_store_dword v4,v3,s[36:39],s71 offen offset:0 sc0 sc1
s_add_u32 m0,s59,5120
s_nop 0
buffer_load_dword v8,s[36:39],s58 offen offset:0 sc1 lds
buffer_store_dword v4,v3,s[36:39],s71 offen offset:0 sc0 sc1
s_mov_b32 s83,0
v_fma_f32 v18,-v98,v106,v18
v_fma_f32 v19,-v98,v107,v19
v_fma_f32 v20,-v98,v108,v20
v_fma_f32 v21,-v98,v109,v21
v_mul_f32 v114,v18,v18
v_mul_f32 v115,v19,v19
v_mul_f32 v116,v20,v20
v_mul_f32 v117,v21,v21
v_fma_f32 v114,v100,v114,v99
v_fma_f32 v115,v100,v115,v99
)"
R"(v_fma_f32 v116,v100,v116,v99
v_fma_f32 v117,v100,v117,v99
v_mul_f32 v114,v18,v114
v_mul_f32 v115,v19,v115
v_mul_f32 v116,v20,v116
v_mul_f32 v117,v21,v117
v_exp_f32 v114,v114
v_exp_f32 v115,v115
v_exp_f32 v116,v116
v_exp_f32 v117,v117
v_fma_f32 v114,v114,v101,v101
v_fma_f32 v115,v115,v101,v101
v_fma_f32 v116,v116,v101,v101
v_fma_f32 v117,v117,v101,v101
v_rcp_f32 v114,v114
v_rcp_f32 v115,v115
v_rcp_f32 v116,v116
v_rcp_f32 v117,v117
v_mul_f32 v18,v18,v114
v_mul_f32 v19,v19,v115
v_mul_f32 v20,v20,v116
v_mul_f32 v21,v21,v117
v_cvt_pk_bf16_f32 v122,v18,v19
v_cvt_pk_bf16_f32 v123,v20,v21
v_fma_f32 v22,-v98,v110,v22
v_fma_f32 v23,-v98,v111,v23
v_fma_f32 v24,-v98,v112,v24
v_fma_f32 v25,-v98,v113,v25
v_mul_f32 v118,v22,v22
v_mul_f32 v119,v23,v23
v_mul_f32 v120,v24,v24
v_mul_f32 v121,v25,v25
v_fma_f32 v118,v100,v118,v99
v_fma_f32 v119,v100,v119,v99
v_fma_f32 v120,v100,v120,v99
v_fma_f32 v121,v100,v121,v99
v_mul_f32 v118,v22,v118
v_mul_f32 v119,v23,v119
v_mul_f32 v120,v24,v120
v_mul_f32 v121,v25,v121
v_exp_f32 v118,v118
v_exp_f32 v119,v119
v_exp_f32 v120,v120
v_exp_f32 v121,v121
v_fma_f32 v118,v118,v101,v101
v_fma_f32 v119,v119,v101,v101
v_fma_f32 v120,v120,v101,v101
v_fma_f32 v121,v121,v101,v101
v_rcp_f32 v118,v118
v_rcp_f32 v119,v119
v_rcp_f32 v120,v120
v_rcp_f32 v121,v121
v_mul_f32 v22,v22,v118
v_mul_f32 v23,v23,v119
v_mul_f32 v24,v24,v120
v_mul_f32 v25,v25,v121
v_cvt_pk_bf16_f32 v124,v22,v23
v_cvt_pk_bf16_f32 v125,v24,v25
buffer_store_dwordx4 v[122:125],v15,s[36:39],s66 offen offset:0 sc0 sc1
buffer_store_dwordx4 v[122:125],v16,s[36:39],s66 offen offset:0 sc0 sc1
s_mov_b32 s72,s69
s_mov_b32 s69,0x7fff0000
s_mov_b32 s66,0x7fff0000
s_mov_b32 s76,0x7fff0000
s_branch Lg1_poll_%=
Lg1_fin0_%=:
v_fma_f32 v18,-v98,v106,v18
v_fma_f32 v19,-v98,v107,v19
v_fma_f32 v20,-v98,v108,v20
v_fma_f32 v21,-v98,v109,v21
v_mul_f32 v114,v18,v18
v_mul_f32 v115,v19,v19
v_mul_f32 v116,v20,v20
v_mul_f32 v117,v21,v21
v_fma_f32 v114,v100,v114,v99
v_fma_f32 v115,v100,v115,v99
v_fma_f32 v116,v100,v116,v99
v_fma_f32 v117,v100,v117,v99
v_mul_f32 v114,v18,v114
v_mul_f32 v115,v19,v115
v_mul_f32 v116,v20,v116
v_mul_f32 v117,v21,v117
v_exp_f32 v114,v114
v_exp_f32 v115,v115
v_exp_f32 v116,v116
v_exp_f32 v117,v117
v_fma_f32 v114,v114,v101,v101
v_fma_f32 v115,v115,v101,v101
v_fma_f32 v116,v116,v101,v101
v_fma_f32 v117,v117,v101,v101
v_rcp_f32 v114,v114
v_rcp_f32 v115,v115
v_rcp_f32 v116,v116
v_rcp_f32 v117,v117
v_mul_f32 v18,v18,v114
v_mul_f32 v19,v19,v115
v_mul_f32 v20,v20,v116
v_mul_f32 v21,v21,v117
v_cvt_pk_bf16_f32 v122,v18,v19
v_cvt_pk_bf16_f32 v123,v20,v21
v_fma_f32 v22,-v98,v110,v22
v_fma_f32 v23,-v98,v111,v23
v_fma_f32 v24,-v98,v112,v24
v_fma_f32 v25,-v98,v113,v25
v_mul_f32 v118,v22,v22
v_mul_f32 v119,v23,v23
v_mul_f32 v120,v24,v24
v_mul_f32 v121,v25,v25
v_fma_f32 v118,v100,v118,v99
v_fma_f32 v119,v100,v119,v99
v_fma_f32 v120,v100,v120,v99
v_fma_f32 v121,v100,v121,v99
v_mul_f32 v118,v22,v118
v_mul_f32 v119,v23,v119
v_mul_f32 v120,v24,v120
v_mul_f32 v121,v25,v121
v_exp_f32 v118,v118
v_exp_f32 v119,v119
v_exp_f32 v120,v120
v_exp_f32 v121,v121
v_fma_f32 v118,v118,v101,v101
v_fma_f32 v119,v119,v101,v101
v_fma_f32 v120,v120,v101,v101
v_fma_f32 v121,v121,v101,v101
v_rcp_f32 v118,v118
v_rcp_f32 v119,v119
v_rcp_f32 v120,v120
v_rcp_f32 v121,v121
v_mul_f32 v22,v22,v118
v_mul_f32 v23,v23,v119
v_mul_f32 v24,v24,v120
v_mul_f32 v25,v25,v121
v_cvt_pk_bf16_f32 v124,v22,v23
v_cvt_pk_bf16_f32 v125,v24,v25
buffer_store_dwordx4 v[122:125],v15,s[36:39],s66 offen offset:0 sc0 sc1
buffer_store_dwordx4 v[122:125],v16,s[36:39],s66 offen offset:0 sc0 sc1
s_mov_b32 s72,s69
s_mov_b32 s69,0x7fff0000
s_mov_b32 s66,0x7fff0000
s_mov_b32 s76,0x7fff0000
s_branch Lg1_done_%=
Lg1_K1_%=:
Lg1_L1_%=:
v_mfma_f32_16x16x32_bf16 v[30:33],a[4:7],v[34:37],0
s_add_u32 m0,s59,3072
s_mov_b32 s67,s65
R 54,2,5120
M 30,a[12:15],38
buffer_load_dword v8,s[36:39],s58 offen offset:0 sc1 lds
v_mfma_f32_16x16x32_bf16 v[26:29],a[0:3],v[34:37],0
s_add_u32 s65,s65,s62
R 58,2,6144
v_fma_f32 v18,-v98,v106,v18
M 26,a[8:11],38
L 3
v_fma_f32 v19,-v98,v107,v19
v_fma_f32 v20,-v98,v108,v20
M 26,a[16:19],42
R 62,2,7168
v_fma_f32 v21,-v98,v109,v21
M 26,a[24:27],46
v_mul_f32 v114,v18,v18
v_mul_f32 v115,v19,v19
M 30,a[20:23],42
R 66,2,8192
v_mul_f32 v116,v20,v20
M 30,a[28:31],46
L 3
v_mul_f32 v117,v21,v21
v_fma_f32 v114,v100,v114,v99
M 30,a[36:39],50
R 70,2,9216
v_fma_f32 v115,v100,v115,v99
M 30,a[44:47],54
v_fma_f32 v116,v100,v116,v99
v_fma_f32 v117,v100,v117,v99
M 26,a[32:35],50
R 74,2,10240
v_mul_f32 v114,v18,v114
M 26,a[40:43],54
L 3
v_exp_f32 v114,v114
M 26,a[48:51],58
R 78,2,11264
v_mul_f32 v115,v19,v115
M 26,a[56:59],62
v_exp_f32 v115,v115
M 30,a[52:55],58
R 82,2,12288
v_mul_f32 v116,v20,v116
M 30,a[60:63],62
L 3
v_exp_f32 v116,v116
M 30,a[68:71],66
R 86,2,13312
v_mul_f32 v117,v21,v117
M 30,a[76:79],70
buffer_load_dwordx4 v[102:105],v11,s[36:39],s73 offen offset:0 sc1
M 26,a[64:67],66
s_add_u32 s73,s73,s64
R 90,2,14336
v_fma_f32 v114,v114,v101,v101
M 26,a[72:75],70
L 3
v_exp_f32 v117,v117
M 26,a[80:83],74
s_waitcnt vmcnt(1)
R 94,2,15360
v_fma_f32 v115,v115,v101,v101
M 26,a[88:91],78
s_barrier
ds_read_b32 v7,v6 offset:3072
v_fma_f32 v116,v116,v101,v101
M 30,a[84:87],74
R 34,2,16384
v_fma_f32 v117,v117,v101,v101
M 30,a[92:95],78
L 4
buffer_store_dword v4,v3,s[36:39],s70 offen offset:0 sc0 sc1
M 30,a[100:103],82
s_mov_b32 s70,0x7fff0000
R 38,2,17408
v_fma_f32 v22,-v98,v110,v22
M 30,a[108:111],86
v_rcp_f32 v114,v114
M 26,a[96:99],82
R 42,2,18432
v_mul_f32 v18,v18,v114
M 26,a[104:107],86
L 4
L 3
v_cmp_eq_u32 vcc,s53,v7
M 26,a[112:115],90
R 46,2,19456
v_fma_f32 v23,-v98,v111,v23
M 26,a[120:123],94
v_rcp_f32 v115,v115
M 30,a[116:119],90
s_cmp_eq_u32 vcc_lo,-1
R 50,2,20480
v_mul_f32 v19,v19,v115
s_cbranch_scc0 Lg1_X1_%=
M 30,a[124:127],94
L 3
s_add_u32 m0,s57,0
s_add_u32 s56,s55,0
M 30,a[132:135],34
s_add_u32 s58,s58,s61
R 54,2,21504
v_cvt_pk_bf16_f32 v122,v18,v19
M 30,a[140:143],38
D 0,56,0
M 26,a[128:131],34
s_add_u32 s54,s54,s46
R 58,2,22528
v_fma_f32 v24,-v98,v112,v24
M 26,a[136:139],38
L 3
v_rcp_f32 v116,v116
M 26,a[144:147],42
R 62,2,23552
v_mul_f32 v20,v20,v116
M 26,a[152:155],46
D 0,56,1024
M 30,a[148:151],42
R 66,2,24576
v_fma_f32 v25,-v98,v113,v25
M 30,a[156:159],46
L 3
v_rcp_f32 v117,v117
M 30,a[164:167],50
R 70,2,25600
v_mul_f32 v21,v21,v117
M 30,a[172:175],54
D 0,56,2048
M 26,a[160:163],50
R 74,2,26624
v_cvt_pk_bf16_f32 v123,v20,v21
M 26,a[168:171],54
L 3
v_mul_f32 v118,v22,v22
v_mul_f32 v119,v23,v23
M 26,a[176:179],58
R 78,2,27648
v_mul_f32 v120,v24,v24
M 26,a[184:187],62
D 0,56,3072
M 30,a[180:183],58
s_add_u32 m0,s59,1024
R 82,2,28672
v_mul_f32 v121,v25,v25
M 30,a[188:191],62
L 3
buffer_load_dword v8,s[36:39],s58 offen offset:0 sc1 lds
M 30,a[196:199],66
s_add_u32 m0,s57,16384
R 86,2,29696
v_fma_f32 v118,v100,v118,v99
M 30,a[204:207],70
s_add_u32 s56,s55,16384
v_fma_f32 v119,v100,v119,v99
v_fma_f32 v120,v100,v120,v99
M 26,a[192:195],66
s_add_u32 s55,s55,s60
R 90,2,30720
v_fma_f32 v121,v100,v121,v99
M 26,a[200:203],70
L 3
D 0,56,0
M 26,a[208:211],74
s_waitcnt vmcnt(1)
R 94,2,31744
v_mul_f32 v118,v22,v118
M 26,a[216:219],78
s_barrier
ds_read_b32 v7,v6 offset:1024
v_mul_f32 v119,v23,v119
M 30,a[212:215],74
s_sub_u32 s57,s84,s57
R 34,1,0
v_mul_f32 v120,v24,v120
M 30,a[220:223],78
L 4
buffer_store_dword v4,v3,s[36:39],s70 offen offset:0 sc0 sc1
v_mul_f32 v121,v25,v121
v_exp_f32 v118,v118
v_exp_f32 v119,v119
v_exp_f32 v120,v120
v_exp_f32 v121,v121
M 30,a[228:231],82
s_mov_b32 s70,s69
R 38,1,1024
v_fma_f32 v118,v118,v101,v101
v_fma_f32 v119,v119,v101,v101
v_fma_f32 v120,v120,v101,v101
v_fma_f32 v121,v121,v101,v101
v_rcp_f32 v118,v118
v_rcp_f32 v119,v119
v_rcp_f32 v120,v120
v_rcp_f32 v121,v121
M 30,a[236:239],86
s_mov_b32 s69,s68
v_mul_f32 v22,v22,v118
v_mul_f32 v23,v23,v119
v_mul_f32 v24,v24,v120
v_mul_f32 v25,v25,v121
v_cvt_pk_bf16_f32 v124,v22,v23
v_cvt_pk_bf16_f32 v125,v24,v25
M 26,a[224:227],82
s_add_u32 s68,s68,s63
R 42,1,2048
buffer_store_dwordx4 v[122:125],v15,s[36:39],s66 offen offset:0 sc0 sc1
M 26,a[232:235],86
L 4
L 3
M 26,a[240:243],90
R 46,1,3072
v_cmp_eq_u32 vcc,s53,v7
M 26,a[248:251],94
D 0,56,1024
M 30,a[244:247],90
L 2
s_cmp_eq_u32 vcc_lo,-1
R 50,1,4096
buffer_store_dwordx4 v[122:125],v16,s[36:39],s66 offen offset:0 sc0 sc1
M 30,a[252:255],94
D 0,56,2048
D 0,56,3072
buffer_store_dword v4,v3,s[36:39],s71 offen offset:0 sc0 sc1
buffer_store_dword v4,v3,s[36:39],s71 offen offset:0 sc0 sc1
buffer_store_dword v4,v3,s[36:39],s71 offen offset:0 sc0 sc1
s_cbranch_scc1 Lg1_F0_%=
s_branch Lg1_K0_%=
Lg1_X1_%=:
M 30,a[124:127],94
L 3
s_add_u32 m0,s59,1024
v_cvt_pk_bf16_f32 v122,v18,v19
M 30,a[132:135],34
R 54,2,21504
v_fma_f32 v24,-v98,v112,v24
M 30,a[140:143],38
buffer_load_dword v8,s[36:39],s58 offen offset:0 sc1 lds
M 26,a[128:131],34
R 58,2,22528
v_fma_f32 v25,-v98,v113,v25
M 26,a[136:139],38
L 3
v_rcp_f32 v116,v116
M 26,a[144:147],42
R 62,2,23552
v_mul_f32 v20,v20,v116
M 26,a[152:155],46
v_rcp_f32 v117,v117
M 30,a[148:151],42
R 66,2,24576
v_mul_f32 v21,v21,v117
M 30,a[156:159],46
L 3
v_cvt_pk_bf16_f32 v123,v20,v21
v_mul_f32 v118,v22,v22
M 30,a[164:167],50
R 70,2,25600
v_mul_f32 v119,v23,v23
M 30,a[172:175],54
v_mul_f32 v120,v24,v24
v_mul_f32 v121,v25,v25
M 26,a[160:163],50
R 74,2,26624
v_fma_f32 v118,v100,v118,v99
M 26,a[168:171],54
L 3
v_fma_f32 v119,v100,v119,v99
v_fma_f32 v120,v100,v120,v99
M 26,a[176:179],58
R 78,2,27648
v_fma_f32 v121,v100,v121,v99
M 26,a[184:187],62
v_mul_f32 v118,v22,v118
v_mul_f32 v119,v23,v119
M 30,a[180:183],58
R 82,2,28672
v_mul_f32 v120,v24,v120
M 30,a[188:191],62
L 3
v_exp_f32 v118,v118
M 30,a[196:199],66
R 86,2,29696
v_mul_f32 v121,v25,v121
M 30,a[204:207],70
v_exp_f32 v119,v119
M 26,a[192:195],66
R 90,2,30720
v_fma_f32 v118,v118,v101,v101
M 26,a[200:203],70
L 3
v_exp_f32 v120,v120
M 26,a[208:211],74
s_waitcnt vmcnt(0)
R 94,2,31744
v_fma_f32 v119,v119,v101,v101
M 26,a[216:219],78
s_barrier
buffer_store_dword v4,v3,s[36:39],s70 offen offset:0 sc0 sc1
M 30,a[212:215],74
s_mov_b32 s70,s69
ds_read_b32 v7,v6 offset:1024
v_fma_f32 v120,v120,v101,v101
M 30,a[220:223],78
L 3
v_exp_f32 v121,v121
M 30,a[228:231],82
s_mov_b32 s69,s68
v_fma_f32 v121,v121,v101,v101
v_rcp_f32 v118,v118
v_rcp_f32 v119,v119
v_rcp_f32 v120,v120
v_rcp_f32 v121,v121
M 30,a[236:239],86
s_add_u32 s68,s68,s63
v_mul_f32 v22,v22,v118
v_mul_f32 v23,v23,v119
v_mul_f32 v24,v24,v120
v_mul_f32 v25,v25,v121
v_cvt_pk_bf16_f32 v124,v22,v23
v_cvt_pk_bf16_f32 v125,v24,v25
M 26,a[224:227],82
buffer_store_dwordx4 v[122:125],v15,s[36:39],s66 offen offset:0 sc0 sc1
M 26,a[232:235],86
L 1
M 26,a[240:243],90
L 0
v_cmp_eq_u32 vcc,s53,v7
M 26,a[248:251],94
buffer_store_dwordx4 v[122:125],v16,s[36:39],s66 offen offset:0 sc0 sc1
M 30,a[244:247],90
s_nop 0
M 30,a[252:255],94
s_mov_b32 s80,0
s_cmp_eq_u32 vcc_lo,-1
s_cbranch_scc0 Lg1_xn1_%=
s_add_u32 m0,s57,0
s_add_u32 s56,s55,0
D 0,56,0
v_fma_f32 v26,-v102,v106,v26
v_fma_f32 v27,-v102,v107,v27
v_fma_f32 v28,-v102,v108,v28
v_fma_f32 v29,-v102,v109,v29
v_mul_f32 v114,v26,v26
v_mul_f32 v115,v27,v27
v_mul_f32 v116,v28,v28
D 0,56,1024
v_mul_f32 v117,v29,v29
v_fma_f32 v114,v104,v114,v103
v_fma_f32 v115,v104,v115,v103
v_fma_f32 v116,v104,v116,v103
v_fma_f32 v117,v104,v117,v103
v_mul_f32 v114,v26,v114
v_mul_f32 v115,v27,v115
D 0,56,2048
)"
R"(v_mul_f32 v116,v28,v116
v_mul_f32 v117,v29,v117
v_exp_f32 v114,v114
v_exp_f32 v115,v115
v_exp_f32 v116,v116
v_exp_f32 v117,v117
v_fma_f32 v114,v114,v105,v105
D 0,56,3072
v_fma_f32 v115,v115,v105,v105
v_fma_f32 v116,v116,v105,v105
v_fma_f32 v117,v117,v105,v105
v_rcp_f32 v114,v114
v_rcp_f32 v115,v115
v_rcp_f32 v116,v116
v_rcp_f32 v117,v117
s_add_u32 s58,s58,s61
s_add_u32 m0,s57,16384
s_add_u32 s56,s55,16384
D 0,56,0
v_mul_f32 v26,v26,v114
v_mul_f32 v27,v27,v115
v_mul_f32 v28,v28,v116
v_mul_f32 v29,v29,v117
v_cvt_pk_bf16_f32 v122,v26,v27
v_cvt_pk_bf16_f32 v123,v28,v29
v_fma_f32 v30,-v102,v110,v30
D 0,56,1024
v_fma_f32 v31,-v102,v111,v31
v_fma_f32 v32,-v102,v112,v32
v_fma_f32 v33,-v102,v113,v33
v_mul_f32 v118,v30,v30
v_mul_f32 v119,v31,v31
v_mul_f32 v120,v32,v32
v_mul_f32 v121,v33,v33
D 0,56,2048
v_fma_f32 v118,v104,v118,v103
v_fma_f32 v119,v104,v119,v103
v_fma_f32 v120,v104,v120,v103
v_fma_f32 v121,v104,v121,v103
v_mul_f32 v118,v30,v118
v_mul_f32 v119,v31,v119
v_mul_f32 v120,v32,v120
D 0,56,3072
v_mul_f32 v121,v33,v121
v_exp_f32 v118,v118
v_exp_f32 v119,v119
v_exp_f32 v120,v120
v_exp_f32 v121,v121
v_fma_f32 v118,v118,v105,v105
v_fma_f32 v119,v119,v105,v105
s_add_u32 s55,s55,s60
s_sub_u32 s57,s84,s57
s_add_u32 s54,s54,s46
v_fma_f32 v120,v120,v105,v105
v_fma_f32 v121,v121,v105,v105
v_rcp_f32 v118,v118
v_rcp_f32 v119,v119
v_rcp_f32 v120,v120
v_rcp_f32 v121,v121
v_mul_f32 v30,v30,v118
v_mul_f32 v31,v31,v119
v_mul_f32 v32,v32,v120
v_mul_f32 v33,v33,v121
v_cvt_pk_bf16_f32 v124,v30,v31
v_cvt_pk_bf16_f32 v125,v32,v33
buffer_store_dwordx4 v[122:125],v15,s[36:39],s67 offen offset:0 sc0 sc1
buffer_store_dwordx4 v[122:125],v16,s[36:39],s67 offen offset:0 sc0 sc1
s_mov_b32 s72,s69
s_mov_b32 s69,0x7fff0000
s_mov_b32 s67,0x7fff0000
s_mov_b32 s77,0x7fff0000
s_branch Lg1_gox_%=
Lg1_xn1_%=:
s_cmp_ge_u32 s54,256
s_cbranch_scc1 Lg1_fin1_%=
s_add_u32 m0,s59,4096
s_nop 0
buffer_load_dword v8,s[36:39],s58 offen offset:0 sc1 lds
buffer_store_dword v4,v3,s[36:39],s71 offen offset:0 sc0 sc1
s_add_u32 m0,s59,5120
s_nop 0
buffer_load_dword v8,s[36:39],s58 offen offset:0 sc1 lds
buffer_store_dword v4,v3,s[36:39],s71 offen offset:0 sc0 sc1
s_mov_b32 s83,0
v_fma_f32 v26,-v102,v106,v26
v_fma_f32 v27,-v102,v107,v27
v_fma_f32 v28,-v102,v108,v28
v_fma_f32 v29,-v102,v109,v29
v_mul_f32 v114,v26,v26
v_mul_f32 v115,v27,v27
v_mul_f32 v116,v28,v28
v_mul_f32 v117,v29,v29
v_fma_f32 v114,v104,v114,v103
v_fma_f32 v115,v104,v115,v103
v_fma_f32 v116,v104,v116,v103
v_fma_f32 v117,v104,v117,v103
v_mul_f32 v114,v26,v114
v_mul_f32 v115,v27,v115
v_mul_f32 v116,v28,v116
v_mul_f32 v117,v29,v117
v_exp_f32 v114,v114
v_exp_f32 v115,v115
v_exp_f32 v116,v116
v_exp_f32 v117,v117
v_fma_f32 v114,v114,v105,v105
v_fma_f32 v115,v115,v105,v105
v_fma_f32 v116,v116,v105,v105
v_fma_f32 v117,v117,v105,v105
v_rcp_f32 v114,v114
v_rcp_f32 v115,v115
v_rcp_f32 v116,v116
v_rcp_f32 v117,v117
v_mul_f32 v26,v26,v114
v_mul_f32 v27,v27,v115
v_mul_f32 v28,v28,v116
v_mul_f32 v29,v29,v117
v_cvt_pk_bf16_f32 v122,v26,v27
v_cvt_pk_bf16_f32 v123,v28,v29
v_fma_f32 v30,-v102,v110,v30
v_fma_f32 v31,-v102,v111,v31
v_fma_f32 v32,-v102,v112,v32
v_fma_f32 v33,-v102,v113,v33
v_mul_f32 v118,v30,v30
v_mul_f32 v119,v31,v31
v_mul_f32 v120,v32,v32
v_mul_f32 v121,v33,v33
v_fma_f32 v118,v104,v118,v103
v_fma_f32 v119,v104,v119,v103
v_fma_f32 v120,v104,v120,v103
v_fma_f32 v121,v104,v121,v103
v_mul_f32 v118,v30,v118
v_mul_f32 v119,v31,v119
v_mul_f32 v120,v32,v120
v_mul_f32 v121,v33,v121
v_exp_f32 v118,v118
v_exp_f32 v119,v119
v_exp_f32 v120,v120
v_exp_f32 v121,v121
v_fma_f32 v118,v118,v105,v105
v_fma_f32 v119,v119,v105,v105
v_fma_f32 v120,v120,v105,v105
v_fma_f32 v121,v121,v105,v105
v_rcp_f32 v118,v118
v_rcp_f32 v119,v119
v_rcp_f32 v120,v120
v_rcp_f32 v121,v121
v_mul_f32 v30,v30,v118
v_mul_f32 v31,v31,v119
v_mul_f32 v32,v32,v120
v_mul_f32 v33,v33,v121
v_cvt_pk_bf16_f32 v124,v30,v31
v_cvt_pk_bf16_f32 v125,v32,v33
buffer_store_dwordx4 v[122:125],v15,s[36:39],s67 offen offset:0 sc0 sc1
buffer_store_dwordx4 v[122:125],v16,s[36:39],s67 offen offset:0 sc0 sc1
s_mov_b32 s72,s69
s_mov_b32 s69,0x7fff0000
s_mov_b32 s67,0x7fff0000
s_mov_b32 s77,0x7fff0000
s_branch Lg1_poll_%=
Lg1_fin1_%=:
v_fma_f32 v26,-v102,v106,v26
v_fma_f32 v27,-v102,v107,v27
v_fma_f32 v28,-v102,v108,v28
v_fma_f32 v29,-v102,v109,v29
v_mul_f32 v114,v26,v26
v_mul_f32 v115,v27,v27
v_mul_f32 v116,v28,v28
v_mul_f32 v117,v29,v29
v_fma_f32 v114,v104,v114,v103
v_fma_f32 v115,v104,v115,v103
v_fma_f32 v116,v104,v116,v103
v_fma_f32 v117,v104,v117,v103
v_mul_f32 v114,v26,v114
v_mul_f32 v115,v27,v115
v_mul_f32 v116,v28,v116
v_mul_f32 v117,v29,v117
v_exp_f32 v114,v114
v_exp_f32 v115,v115
v_exp_f32 v116,v116
v_exp_f32 v117,v117
v_fma_f32 v114,v114,v105,v105
v_fma_f32 v115,v115,v105,v105
v_fma_f32 v116,v116,v105,v105
v_fma_f32 v117,v117,v105,v105
v_rcp_f32 v114,v114
v_rcp_f32 v115,v115
v_rcp_f32 v116,v116
v_rcp_f32 v117,v117
v_mul_f32 v26,v26,v114
v_mul_f32 v27,v27,v115
v_mul_f32 v28,v28,v116
v_mul_f32 v29,v29,v117
v_cvt_pk_bf16_f32 v122,v26,v27
v_cvt_pk_bf16_f32 v123,v28,v29
v_fma_f32 v30,-v102,v110,v30
v_fma_f32 v31,-v102,v111,v31
v_fma_f32 v32,-v102,v112,v32
v_fma_f32 v33,-v102,v113,v33
v_mul_f32 v118,v30,v30
v_mul_f32 v119,v31,v31
v_mul_f32 v120,v32,v32
v_mul_f32 v121,v33,v33
v_fma_f32 v118,v104,v118,v103
v_fma_f32 v119,v104,v119,v103
v_fma_f32 v120,v104,v120,v103
v_fma_f32 v121,v104,v121,v103
v_mul_f32 v118,v30,v118
v_mul_f32 v119,v31,v119
v_mul_f32 v120,v32,v120
v_mul_f32 v121,v33,v121
v_exp_f32 v118,v118
v_exp_f32 v119,v119
v_exp_f32 v120,v120
v_exp_f32 v121,v121
v_fma_f32 v118,v118,v105,v105
v_fma_f32 v119,v119,v105,v105
v_fma_f32 v120,v120,v105,v105
v_fma_f32 v121,v121,v105,v105
v_rcp_f32 v118,v118
v_rcp_f32 v119,v119
v_rcp_f32 v120,v120
v_rcp_f32 v121,v121
v_mul_f32 v30,v30,v118
v_mul_f32 v31,v31,v119
v_mul_f32 v32,v32,v120
v_mul_f32 v33,v33,v121
v_cvt_pk_bf16_f32 v124,v30,v31
v_cvt_pk_bf16_f32 v125,v32,v33
buffer_store_dwordx4 v[122:125],v15,s[36:39],s67 offen offset:0 sc0 sc1
buffer_store_dwordx4 v[122:125],v16,s[36:39],s67 offen offset:0 sc0 sc1
s_mov_b32 s72,s69
s_mov_b32 s69,0x7fff0000
s_mov_b32 s67,0x7fff0000
s_mov_b32 s77,0x7fff0000
s_branch Lg1_done_%=
Lg1_done_%=:
s_waitcnt vmcnt(2)
s_barrier
buffer_store_dword v4,v3,s[36:39],s70 offen offset:0 sc0 sc1
s_waitcnt vmcnt(1)
s_barrier
buffer_store_dword v4,v3,s[36:39],s72 offen offset:0 sc0 sc1
s_waitcnt vmcnt(0) lgkmcnt(0)
s_barrier
s_branch Lg2_end_%=
Lg2_begin_%=:
Lcode_g2_b:
s_mov_b64 s[36:37],%1
s_mov_b32 s38,111243392
s_mov_b32 s39,0x00020000
s_mov_b32 s44,%2
s_mov_b32 s45,%3
s_mov_b32 s46,%4
s_mov_b32 s47,%5
s_mov_b32 s48,%6
s_mov_b32 s49,%7
s_mov_b32 s50,%8
s_mov_b32 s51,%9
s_mov_b32 s53,%10
v_mov_b32 v0,%11
v_mov_b32 v3,%12
v_mov_b32 v5,%13
v_mov_b32 v6,%14
v_mov_b32 v18,%15
v_mov_b32 v19,%16
v_mov_b32 v20,%17
v_mov_b32 v21,%18
v_mov_b32 v9,%19
v_mov_b32 v10,%20
v_mov_b32 v11,%21
v_mov_b32 v12,%22
v_mov_b32 v13,%23
v_mov_b32 v14,%24
v_mov_b32 v15,%25
v_mov_b32 v16,%26
s_mov_b64 s[40:41],%27
s_mov_b32 s42,2097152
s_mov_b32 s43,0x00020000
s_lshl_b32 s57,s44,11
s_cmp_ge_u32 s45,8
s_cbranch_scc1 Lg2_dly_%=
s_sleep 16
s_sleep 16
s_sleep 16
s_sleep 16
Lg2_dly_%=:
s_add_u32 s55,s50,0
s_add_u32 m0,s57,0
s_nop 0
buffer_load_dwordx4 v18,s[40:43],s55 offen offset:0  lds
s_add_u32 m0,s57,1024
s_nop 0
buffer_load_dwordx4 v19,s[40:43],s55 offen offset:0  lds
s_add_u32 m0,s57,8064
s_nop 0
buffer_load_dwordx4 v18,s[40:43],s55 offen offset:128  lds
s_add_u32 m0,s57,9088
s_nop 0
buffer_load_dwordx4 v19,s[40:43],s55 offen offset:128  lds
s_add_u32 s55,s50,131072
s_add_u32 m0,s57,16384
s_nop 0
buffer_load_dwordx4 v18,s[40:43],s55 offen offset:0  lds
s_add_u32 m0,s57,17408
s_nop 0
buffer_load_dwordx4 v19,s[40:43],s55 offen offset:0  lds
s_add_u32 m0,s57,24448
s_nop 0
buffer_load_dwordx4 v18,s[40:43],s55 offen offset:128  lds
s_add_u32 m0,s57,25472
s_nop 0
buffer_load_dwordx4 v19,s[40:43],s55 offen offset:128  lds
s_add_u32 s55,s50,262144
s_add_u32 m0,s57,32768
s_nop 0
buffer_load_dwordx4 v18,s[40:43],s55 offen offset:0  lds
s_add_u32 m0,s57,33792
s_nop 0
buffer_load_dwordx4 v19,s[40:43],s55 offen offset:0  lds
s_add_u32 m0,s57,40832
s_nop 0
buffer_load_dwordx4 v18,s[40:43],s55 offen offset:128  lds
s_add_u32 m0,s57,41856
s_nop 0
buffer_load_dwordx4 v19,s[40:43],s55 offen offset:128  lds
s_add_u32 s55,s50,393216
s_add_u32 m0,s57,49152
s_nop 0
buffer_load_dwordx4 v18,s[40:43],s55 offen offset:0  lds
s_add_u32 m0,s57,50176
s_nop 0
buffer_load_dwordx4 v19,s[40:43],s55 offen offset:0  lds
s_add_u32 m0,s57,57216
s_nop 0
buffer_load_dwordx4 v18,s[40:43],s55 offen offset:128  lds
s_add_u32 m0,s57,58240
s_nop 0
buffer_load_dwordx4 v19,s[40:43],s55 offen offset:128  lds
s_add_u32 s55,s50,524288
s_add_u32 m0,s57,65536
s_nop 0
buffer_load_dwordx4 v18,s[40:43],s55 offen offset:0  lds
s_add_u32 m0,s57,66560
s_nop 0
buffer_load_dwordx4 v19,s[40:43],s55 offen offset:0  lds
s_add_u32 m0,s57,73600
s_nop 0
buffer_load_dwordx4 v18,s[40:43],s55 offen offset:128  lds
s_add_u32 m0,s57,74624
s_nop 0
buffer_load_dwordx4 v19,s[40:43],s55 offen offset:128  lds
s_add_u32 s55,s50,655360
s_add_u32 m0,s57,81920
s_nop 0
buffer_load_dwordx4 v18,s[40:43],s55 offen offset:0  lds
s_add_u32 m0,s57,82944
s_nop 0
buffer_load_dwordx4 v19,s[40:43],s55 offen offset:0  lds
s_add_u32 m0,s57,89984
s_nop 0
buffer_load_dwordx4 v18,s[40:43],s55 offen offset:128  lds
s_add_u32 m0,s57,91008
s_nop 0
buffer_load_dwordx4 v19,s[40:43],s55 offen offset:128  lds
s_waitcnt vmcnt(20) lgkmcnt(0)
s_barrier
s_add_u32 s55,s50,786432
s_add_u32 m0,s57,98304
s_nop 0
buffer_load_dwordx4 v18,s[40:43],s55 offen offset:0  lds
s_add_u32 m0,s57,99328
s_nop 0
buffer_load_dwordx4 v19,s[40:43],s55 offen offset:0  lds
s_add_u32 m0,s57,106368
s_nop 0
buffer_load_dwordx4 v18,s[40:43],s55 offen offset:128  lds
s_add_u32 m0,s57,107392
s_nop 0
buffer_load_dwordx4 v19,s[40:43],s55 offen offset:128  lds
v_add_u32 v26,0,v20
v_add_u32 v27,0,v21
T a[0:1],26,0
T a[2:3],26,512
T a[4:5],27,0
T a[6:7],27,512
T a[8:9],26,4096
T a[10:11],26,4608
T a[12:13],27,4096
T a[14:15],27,4608
s_waitcnt vmcnt(20) lgkmcnt(0)
s_barrier
s_add_u32 s55,s50,917504
s_add_u32 m0,s57,0
s_nop 0
buffer_load_dwordx4 v18,s[40:43],s55 offen offset:0  lds
s_add_u32 m0,s57,1024
s_nop 0
buffer_load_dwordx4 v19,s[40:43],s55 offen offset:0  lds
s_add_u32 m0,s57,8064
s_nop 0
buffer_load_dwordx4 v18,s[40:43],s55 offen offset:128  lds
s_add_u32 m0,s57,9088
s_nop 0
buffer_load_dwordx4 v19,s[40:43],s55 offen offset:128  lds
v_add_u32 v26,16384,v20
v_add_u32 v27,16384,v21
T a[16:17],26,0
T a[18:19],26,512
T a[20:21],27,0
T a[22:23],27,512
T a[24:25],26,4096
T a[26:27],26,4608
T a[28:29],27,4096
T a[30:31],27,4608
s_waitcnt vmcnt(20) lgkmcnt(0)
s_barrier
s_add_u32 s55,s50,1048576
s_add_u32 m0,s57,16384
s_nop 0
buffer_load_dwordx4 v18,s[40:43],s55 offen offset:0  lds
s_add_u32 m0,s57,17408
s_nop 0
buffer_load_dwordx4 v19,s[40:43],s55 offen offset:0  lds
s_add_u32 m0,s57,24448
s_nop 0
buffer_load_dwordx4 v18,s[40:43],s55 offen offset:128  lds
s_add_u32 m0,s57,25472
s_nop 0
buffer_load_dwordx4 v19,s[40:43],s55 offen offset:128  lds
v_add_u32 v26,32768,v20
v_add_u32 v27,32768,v21
T a[32:33],26,0
T a[34:35],26,512
T a[36:37],27,0
T a[38:39],27,512
T a[40:41],26,4096
T a[42:43],26,4608
T a[44:45],27,4096
T a[46:47],27,4608
s_waitcnt vmcnt(20) lgkmcnt(0)
s_barrier
s_add_u32 s55,s50,1179648
s_add_u32 m0,s57,32768
s_nop 0
buffer_load_dwordx4 v18,s[40:43],s55 offen offset:0  lds
s_add_u32 m0,s57,33792
s_nop 0
buffer_load_dwordx4 v19,s[40:43],s55 offen offset:0  lds
s_add_u32 m0,s57,40832
s_nop 0
buffer_load_dwordx4 v18,s[40:43],s55 offen offset:128  lds
s_add_u32 m0,s57,41856
s_nop 0
buffer_load_dwordx4 v19,s[40:43],s55 offen offset:128  lds
v_add_u32 v26,49152,v20
v_add_u32 v27,49152,v21
T a[48:49],26,0
T a[50:51],26,512
T a[52:53],27,0
T a[54:55],27,512
T a[56:57],26,4096
T a[58:59],26,4608
T a[60:61],27,4096
T a[62:63],27,4608
s_waitcnt vmcnt(20) lgkmcnt(0)
s_barrier
s_add_u32 s55,s50,1310720
s_add_u32 m0,s57,49152
s_nop 0
buffer_load_dwordx4 v18,s[40:43],s55 offen offset:0  lds
s_add_u32 m0,s57,50176
s_nop 0
buffer_load_dwordx4 v19,s[40:43],s55 offen offset:0  lds
s_add_u32 m0,s57,57216
s_nop 0
buffer_load_dwordx4 v18,s[40:43],s55 offen offset:128  lds
s_add_u32 m0,s57,58240
s_nop 0
buffer_load_dwordx4 v19,s[40:43],s55 offen offset:128  lds
v_add_u32 v26,65536,v20
v_add_u32 v27,65536,v21
T a[64:65],26,0
T a[66:67],26,512
T a[68:69],27,0
T a[70:71],27,512
T a[72:73],26,4096
T a[74:75],26,4608
T a[76:77],27,4096
T a[78:79],27,4608
s_waitcnt vmcnt(20) lgkmcnt(0)
s_barrier
s_add_u32 s55,s50,1441792
s_add_u32 m0,s57,65536
s_nop 0
buffer_load_dwordx4 v18,s[40:43],s55 offen offset:0  lds
s_add_u32 m0,s57,66560
s_nop 0
buffer_load_dwordx4 v19,s[40:43],s55 offen offset:0  lds
s_add_u32 m0,s57,73600
s_nop 0
)"
R"(buffer_load_dwordx4 v18,s[40:43],s55 offen offset:128  lds
s_add_u32 m0,s57,74624
s_nop 0
buffer_load_dwordx4 v19,s[40:43],s55 offen offset:128  lds
v_add_u32 v26,81920,v20
v_add_u32 v27,81920,v21
T a[80:81],26,0
T a[82:83],26,512
T a[84:85],27,0
T a[86:87],27,512
T a[88:89],26,4096
T a[90:91],26,4608
T a[92:93],27,4096
T a[94:95],27,4608
s_waitcnt vmcnt(20) lgkmcnt(0)
s_barrier
s_add_u32 s55,s50,1572864
s_add_u32 m0,s57,81920
s_nop 0
buffer_load_dwordx4 v18,s[40:43],s55 offen offset:0  lds
s_add_u32 m0,s57,82944
s_nop 0
buffer_load_dwordx4 v19,s[40:43],s55 offen offset:0  lds
s_add_u32 m0,s57,89984
s_nop 0
buffer_load_dwordx4 v18,s[40:43],s55 offen offset:128  lds
s_add_u32 m0,s57,91008
s_nop 0
buffer_load_dwordx4 v19,s[40:43],s55 offen offset:128  lds
v_add_u32 v26,98304,v20
v_add_u32 v27,98304,v21
T a[96:97],26,0
T a[98:99],26,512
T a[100:101],27,0
T a[102:103],27,512
T a[104:105],26,4096
T a[106:107],26,4608
T a[108:109],27,4096
T a[110:111],27,4608
s_waitcnt vmcnt(20) lgkmcnt(0)
s_barrier
s_add_u32 s55,s50,1703936
s_add_u32 m0,s57,98304
s_nop 0
buffer_load_dwordx4 v18,s[40:43],s55 offen offset:0  lds
s_add_u32 m0,s57,99328
s_nop 0
buffer_load_dwordx4 v19,s[40:43],s55 offen offset:0  lds
s_add_u32 m0,s57,106368
s_nop 0
buffer_load_dwordx4 v18,s[40:43],s55 offen offset:128  lds
s_add_u32 m0,s57,107392
s_nop 0
buffer_load_dwordx4 v19,s[40:43],s55 offen offset:128  lds
v_add_u32 v26,0,v20
v_add_u32 v27,0,v21
T a[112:113],26,0
T a[114:115],26,512
T a[116:117],27,0
T a[118:119],27,512
T a[120:121],26,4096
T a[122:123],26,4608
T a[124:125],27,4096
T a[126:127],27,4608
s_waitcnt vmcnt(20) lgkmcnt(0)
s_barrier
s_add_u32 s55,s50,1835008
s_add_u32 m0,s57,0
s_nop 0
buffer_load_dwordx4 v18,s[40:43],s55 offen offset:0  lds
s_add_u32 m0,s57,1024
s_nop 0
buffer_load_dwordx4 v19,s[40:43],s55 offen offset:0  lds
s_add_u32 m0,s57,8064
s_nop 0
buffer_load_dwordx4 v18,s[40:43],s55 offen offset:128  lds
s_add_u32 m0,s57,9088
s_nop 0
buffer_load_dwordx4 v19,s[40:43],s55 offen offset:128  lds
v_add_u32 v26,16384,v20
v_add_u32 v27,16384,v21
T a[128:129],26,0
T a[130:131],26,512
T a[132:133],27,0
T a[134:135],27,512
T a[136:137],26,4096
T a[138:139],26,4608
T a[140:141],27,4096
T a[142:143],27,4608
s_waitcnt vmcnt(20) lgkmcnt(0)
s_barrier
s_add_u32 s55,s50,1966080
s_add_u32 m0,s57,16384
s_nop 0
buffer_load_dwordx4 v18,s[40:43],s55 offen offset:0  lds
s_add_u32 m0,s57,17408
s_nop 0
buffer_load_dwordx4 v19,s[40:43],s55 offen offset:0  lds
s_add_u32 m0,s57,24448
s_nop 0
buffer_load_dwordx4 v18,s[40:43],s55 offen offset:128  lds
s_add_u32 m0,s57,25472
s_nop 0
buffer_load_dwordx4 v19,s[40:43],s55 offen offset:128  lds
v_add_u32 v26,32768,v20
v_add_u32 v27,32768,v21
T a[144:145],26,0
T a[146:147],26,512
T a[148:149],27,0
T a[150:151],27,512
T a[152:153],26,4096
T a[154:155],26,4608
T a[156:157],27,4096
T a[158:159],27,4608
s_waitcnt vmcnt(20) lgkmcnt(0)
s_barrier
v_add_u32 v26,49152,v20
v_add_u32 v27,49152,v21
T a[160:161],26,0
T a[162:163],26,512
T a[164:165],27,0
T a[166:167],27,512
T a[168:169],26,4096
T a[170:171],26,4608
T a[172:173],27,4096
T a[174:175],27,4608
s_waitcnt vmcnt(16) lgkmcnt(0)
s_barrier
v_add_u32 v26,65536,v20
v_add_u32 v27,65536,v21
T a[176:177],26,0
T a[178:179],26,512
T a[180:181],27,0
T a[182:183],27,512
T a[184:185],26,4096
T a[186:187],26,4608
T a[188:189],27,4096
T a[190:191],27,4608
s_waitcnt vmcnt(12) lgkmcnt(0)
s_barrier
v_add_u32 v26,81920,v20
v_add_u32 v27,81920,v21
T a[192:193],26,0
T a[194:195],26,512
T a[196:197],27,0
T a[198:199],27,512
T a[200:201],26,4096
T a[202:203],26,4608
T a[204:205],27,4096
T a[206:207],27,4608
s_waitcnt vmcnt(8) lgkmcnt(0)
s_barrier
v_add_u32 v26,98304,v20
v_add_u32 v27,98304,v21
T a[208:209],26,0
T a[210:211],26,512
T a[212:213],27,0
T a[214:215],27,512
T a[216:217],26,4096
T a[218:219],26,4608
T a[220:221],27,4096
T a[222:223],27,4608
s_waitcnt vmcnt(4) lgkmcnt(0)
s_barrier
v_add_u32 v26,0,v20
v_add_u32 v27,0,v21
T a[224:225],26,0
T a[226:227],26,512
T a[228:229],27,0
T a[230:231],27,512
T a[232:233],26,4096
T a[234:235],26,4608
T a[236:237],27,4096
T a[238:239],27,4608
s_waitcnt vmcnt(0) lgkmcnt(0)
s_barrier
v_add_u32 v26,16384,v20
v_add_u32 v27,16384,v21
T a[240:241],26,0
T a[242:243],26,512
T a[244:245],27,0
T a[246:247],27,512
T a[248:249],26,4096
T a[250:251],26,4608
T a[252:253],27,4096
T a[254:255],27,4608
L 0
s_barrier
v_mov_b32 v4,s53
s_mov_b32 s82,0
s_mov_b32 s80,0
s_mov_b32 s54,s45
s_mul_i32 s57,s44,5120
s_mul_i32 s55,s45,40960
s_add_u32 s55,s55,s57
s_add_u32 s55,s55,17825792
s_mul_i32 s57,s44,4096
s_mul_i32 s60,s46,40960
s_mul_i32 s61,s46,1024
s_mul_i32 s62,s46,32768
s_mul_i32 s63,s46,1024
s_mul_i32 s64,s46,512
s_lshl_b32 s59,s44,8
s_add_u32 s59,s59,131072
s_mul_i32 s58,s45,1024
s_add_u32 s58,s58,109333504
s_mul_i32 s65,s45,32768
s_add_u32 s65,s65,s47
s_mul_i32 s68,s45,1024
s_add_u32 s68,s68,s48
s_mul_i32 s73,s45,512
s_add_u32 s73,s73,106954752
s_mul_i32 s74,s45,32768
s_add_u32 s74,s74,s49
s_mul_i32 s75,s45,1024
s_add_u32 s75,s75,107479040
s_mov_b32 s66,0x7fff0000
s_mov_b32 s67,0x7fff0000
s_mov_b32 s69,0x7fff0000
s_mov_b32 s76,0x7fff0000
s_mov_b32 s77,0x7fff0000
s_mov_b32 s70,0x7fff0000
s_mov_b32 s71,0x7fff0000
s_mov_b32 s72,0x7fff0000
v_mov_b32 v1,v0
v_add_u32 v2,16384,v0
s_mov_b32 s84,16384
s_mov_b32 s86,0x3a800000
s_mov_b32 s87,0x38a7c5ac
s_mov_b32 s88,0xc0135761
s_mov_b32 s89,0x3d372713
s_lshr_b32 s85,s44,1
s_mul_i32 s85,s85,127
v_lshlrev_b32 v8,22,v0
v_and_b32 v8,0x80000000,v8
v_or_b32 v8,v8,v5
v_add_u32 v2,32768,v0
s_lshl_b32 s84,s57,1
s_add_u32 s84,s84,32768
s_getpc_b64 s[40:41]
Lg2_pc_%=:
s_lshl_b32 s78,s44,13
v_lshlrev_b32 v106,3,v0
v_add_u32 v106,s78,v106
v_min_u32 v106,Lcode_g12_e-Lg2_pc_%=-4,v106
s_mov_b32 m0,163584
s_nop 0
global_load_lds_dword v106,s[40:41]
s_and_b32 s79,s54,127
s_cmp_eq_u32 s79,0
s_cselect_b32 s94,0x3300,0
s_cmp_eq_u32 s79,127
s_cselect_b32 s94,0xcc00,s94
s_add_u32 m0,s59,4096
s_nop 0
buffer_load_dword v8,s[36:39],s58 offen offset:0 sc1 lds
buffer_store_dword v4,v3,s[36:39],s71 offen offset:0 sc0 sc1
s_add_u32 m0,s59,5120
s_nop 0
buffer_load_dword v8,s[36:39],s58 offen offset:0 sc1 lds
buffer_store_dword v4,v3,s[36:39],s71 offen offset:0 sc0 sc1
s_mov_b32 s83,0
Lg2_poll_%=:
s_add_u32 s78,s83,2
s_and_b32 s78,s78,3
s_lshl_b32 s78,s78,10
s_add_u32 s78,s78,4096
s_add_u32 m0,s59,s78
s_nop 0
buffer_load_dword v8,s[36:39],s58 offen offset:0 sc1 lds
s_and_b32 s78,s83,3
s_lshl_b32 s78,s78,10
v_add_u32 v106,s78,v6
s_waitcnt vmcnt(4)
s_barrier
ds_read_b32 v7,v106 offset:4096
buffer_store_dword v4,v3,s[36:39],s70 offen offset:0 sc0 sc1
s_mov_b32 s70,s71
s_mov_b32 s71,s72
s_mov_b32 s72,0x7fff0000
s_add_u32 s83,s83,1
s_add_u32 s82,s82,1
L 0
v_cmp_eq_u32 vcc,s53,v7
s_nop 0
s_or_b64 vcc,vcc,s[94:95]
s_cmp_eq_u32 vcc_lo,-1
s_cbranch_scc1 Lg2_go_%=
s_cmp_lt_u32 s82,20000
s_cbranch_scc1 Lg2_poll_%=
Lg2_go_%=:
s_and_b32 s78,s54,127
s_cmp_eq_u32 s78,s85
s_cselect_b64 s[40:41],-1,0
v_cndmask_b32 v17,v15,v16,s[40:41]
s_add_u32 m0,s57,0
s_add_u32 s56,s55,0
D 17,56,0
s_add_u32 s56,s55,256
D 17,56,1024
s_add_u32 s56,s55,512
D 17,56,2048
s_add_u32 s56,s55,768
D 17,56,3072
s_add_u32 s58,s58,s61
s_add_u32 m0,s59,2048
s_nop 0
buffer_load_dword v8,s[36:39],s58 offen offset:0 sc1 lds
v_cndmask_b32 v17,v10,v9,s[40:41]
s_add_u32 m0,s57,16384
s_add_u32 s56,s55,20480
D 17,56,0
s_add_u32 s56,s55,20736
D 17,56,1024
s_add_u32 s56,s55,20992
D 17,56,2048
s_add_u32 s56,s55,21248
D 17,56,3072
s_add_u32 s55,s55,s60
s_sub_u32 s57,s84,s57
s_add_u32 s54,s54,s46
s_and_b32 s79,s54,127
s_cmp_eq_u32 s79,0
s_cselect_b32 s94,0x3300,0
s_cmp_eq_u32 s79,127
s_cselect_b32 s94,0xcc00,s94
s_waitcnt vmcnt(4)
s_barrier
buffer_store_dword v4,v3,s[36:39],s70 offen offset:0 sc0 sc1
s_min_u32 s71,s71,s72
buffer_store_dword v4,v3,s[36:39],s71 offen offset:0 sc0 sc1
s_mov_b32 s70,0x7fff0000
s_mov_b32 s71,0x7fff0000
s_mov_b32 s72,0x7fff0000
s_cmp_eq_u32 s80,1
buffer_store_dword v4,v3,s[36:39],s71 offen offset:0 sc0 sc1
s_cbranch_scc1 Lg2_st1_%=
s_branch Lg2_st0_%=
Lg2_gox_%=:
s_add_u32 m0,s59,2048
s_nop 0
buffer_load_dword v8,s[36:39],s58 offen offset:0 sc1 lds
s_and_b32 s79,s54,127
s_cmp_eq_u32 s79,0
s_cselect_b32 s94,0x3300,0
s_cmp_eq_u32 s79,127
s_cselect_b32 s94,0xcc00,s94
s_waitcnt vmcnt(0)
s_barrier
buffer_store_dword v4,v3,s[36:39],s70 offen offset:0 sc0 sc1
s_min_u32 s71,s71,s72
buffer_store_dword v4,v3,s[36:39],s71 offen offset:0 sc0 sc1
s_mov_b32 s70,0x7fff0000
s_mov_b32 s71,0x7fff0000
s_mov_b32 s72,0x7fff0000
s_cmp_eq_u32 s80,1
s_cbranch_scc1 Lg2_st1_%=
Lg2_st0_%=:
ds_read_b32 v7,v6 offset:2048
R 34,1,0
R 38,1,1024
R 42,1,2048
R 46,1,3072
R 50,1,4096
L 5
v_cmp_eq_u32 vcc,s53,v7
s_nop 0
s_or_b64 vcc,vcc,s[94:95]
L 3
s_cmp_eq_u32 vcc_lo,-1
s_cbranch_scc1 Lg2_F0_%=
s_branch Lg2_L0_%=
Lg2_st1_%=:
ds_read_b32 v7,v6 offset:2048
R 34,2,0
R 38,2,1024
R 42,2,2048
R 46,2,3072
R 50,2,4096
L 5
v_cmp_eq_u32 vcc,s53,v7
s_nop 0
s_or_b64 vcc,vcc,s[94:95]
L 3
s_cmp_eq_u32 vcc_lo,-1
s_cbranch_scc1 Lg2_F1_%=
s_branch Lg2_L1_%=
Lg2_F0_%=:
v_mfma_f32_16x16x32_bf16 v[22:25],a[4:7],v[34:37],0
s_add_u32 s58,s58,s61
s_add_u32 m0,s59,0
s_and_b32 s78,s54,127
R 54,1,5120
M 22,a[12:15],38
s_cmp_eq_u32 s78,s85
s_cselect_b64 s[40:41],-1,0
s_add_u32 s56,s55,0
buffer_load_dword v8,s[36:39],s58 offen offset:0 sc1 lds
v_mfma_f32_16x16x32_bf16 v[18:21],a[0:3],v[34:37],0
s_add_u32 m0,s57,0
R 58,1,6144
v_cndmask_b32 v17,v15,v16,s[40:41]
M 18,a[8:11],38
L 3
D 17,56,0
M 18,a[16:19],42
s_add_u32 s56,s55,256
R 62,1,7168
v_lshlrev_b32 v106,16,v126
M 18,a[24:27],46
s_add_u32 s54,s54,s46
v_and_b32 v107,0xffff0000,v126
v_lshlrev_b32 v108,16,v127
M 22,a[20:23],42
s_and_b32 s79,s54,127
R 66,1,8192
v_and_b32 v109,0xffff0000,v127
M 22,a[28:31],46
L 3
D 17,56,1024
M 22,a[36:39],50
s_add_u32 s56,s55,512
R 70,1,9216
v_lshlrev_b32 v110,16,v128
M 22,a[44:47],54
s_cmp_eq_u32 s79,0
v_and_b32 v111,0xffff0000,v128
v_lshlrev_b32 v112,16,v129
M 18,a[32:35],50
s_cselect_b32 s94,0x3300,0
R 74,1,10240
v_and_b32 v113,0xffff0000,v129
M 18,a[40:43],54
L 3
D 17,56,2048
M 18,a[48:51],58
s_add_u32 s56,s55,768
R 78,1,11264
v_fma_f32 v106,v106,v103,v102
M 18,a[56:59],62
D 17,56,3072
M 22,a[52:55],58
s_add_u32 m0,s57,16384
R 82,1,12288
v_fma_f32 v107,v107,v103,v102
M 22,a[60:63],62
L 3
v_fma_f32 v108,v108,v103,v102
v_fma_f32 v109,v109,v103,v102
M 22,a[68:71],66
s_add_u32 s56,s55,20480
R 86,1,13312
v_fma_f32 v110,v110,v103,v102
M 22,a[76:79],70
buffer_load_dwordx4 v[122:125],v0,s[36:39],s74 offen offset:0 sc1
M 18,a[64:67],66
s_cmp_eq_u32 s79,127
R 90,1,14336
v_fma_f32 v111,v111,v103,v102
M 18,a[72:75],70
L 3
buffer_load_dwordx2 v[98:99],v11,s[36:39],s73 offen offset:256 sc1
M 18,a[80:83],74
s_waitcnt vmcnt(10)
R 94,1,15360
v_fma_f32 v112,v112,v103,v102
M 18,a[88:91],78
s_barrier
v_fma_f32 v113,v113,v103,v102
v_add_f32 v26,v26,v106
M 22,a[84:87],74
s_cselect_b32 s94,0xcc00,s94
R 34,1,16384
v_add_f32 v27,v27,v107
M 22,a[92:95],78
L 3
v_add_f32 v28,v28,v108
v_add_f32 v29,v29,v109
M 22,a[100:103],82
s_mov_b32 s66,s65
R 38,1,17408
v_add_f32 v30,v30,v110
M 22,a[108:111],86
s_mov_b32 s76,s75
v_cndmask_b32 v17,v10,v9,s[40:41]
v_add_f32 v31,v31,v111
M 18,a[96:99],82
s_add_u32 s73,s73,s64
R 42,1,18432
v_add_f32 v32,v32,v112
M 18,a[104:107],86
L 3
D 17,56,0
M 18,a[112:115],90
s_add_u32 s56,s55,20736
R 46,1,19456
v_add_f32 v33,v33,v113
M 18,a[120:123],94
s_add_u32 s74,s74,s62
v_cvt_pk_bf16_f32 v114,v26,v27
)"
R"(v_cvt_pk_bf16_f32 v115,v28,v29
M 22,a[116:119],90
s_add_u32 s65,s65,s62
R 50,1,20480
v_fma_f32 v107,v26,v26,s87
M 22,a[124:127],94
L 3
v_add_f32 v106,v26,v27
v_fma_f32 v107,v27,v27,v107
M 22,a[132:135],34
s_add_u32 s75,s75,s63
R 54,1,21504
v_add_f32 v106,v106,v28
M 22,a[140:143],38
D 17,56,1024
M 18,a[128:131],34
s_add_u32 s56,s55,20992
R 58,1,22528
v_fma_f32 v107,v28,v28,v107
M 18,a[136:139],38
L 3
v_add_f32 v106,v106,v29
v_fma_f32 v107,v29,v29,v107
M 18,a[144:147],42
s_sub_u32 s57,s84,s57
R 62,1,23552
v_add_f32 v106,v106,v30
M 18,a[152:155],46
v_fma_f32 v107,v30,v30,v107
v_add_f32 v106,v106,v31
M 22,a[148:151],42
R 66,1,24576
v_fma_f32 v107,v31,v31,v107
M 22,a[156:159],46
L 3
v_add_f32 v106,v106,v32
v_fma_f32 v107,v32,v32,v107
M 22,a[164:167],50
R 70,1,25600
v_add_f32 v106,v106,v33
M 22,a[172:175],54
v_fma_f32 v107,v33,v33,v107
v_cvt_pk_bf16_f32 v116,v30,v31
M 18,a[160:163],50
R 74,1,26624
v_cvt_pk_bf16_f32 v117,v32,v33
M 18,a[168:171],54
L 3
v_permlane16_swap_b32 v106,v107
v_add_f32 v106,v106,v107
v_mov_b32 v107,v106
M 18,a[176:179],58
R 78,1,27648
M 18,a[184:187],62
v_permlane32_swap_b32 v106,v107
v_add_f32 v106,v106,v107
M 22,a[180:183],58
R 82,1,28672
ds_write_b32 v12,v106 offset:2048
M 22,a[188:191],62
L 4
D 17,56,2048
M 22,a[196:199],66
s_add_u32 s56,s55,21248
s_add_u32 s55,s55,s60
R 86,1,29696
M 22,a[204:207],70
D 17,56,3072
M 18,a[192:195],66
R 90,1,30720
M 18,a[200:203],70
L 4
L 2
buffer_store_dwordx4 v[114:117],v0,s[36:39],s67 offen offset:0 sc0 sc1
M 18,a[208:211],74
s_waitcnt vmcnt(7)
R 94,1,31744
M 18,a[216:219],78
s_barrier
ds_read_b32 v7,v6 offset:0
R 118,13,2048
M 22,a[212:215],74
R 34,2,0
M 22,a[220:223],78
L 5
buffer_store_dword v4,v3,s[36:39],s70 offen offset:0 sc0 sc1
M 22,a[228:231],82
s_mov_b32 s70,s69
s_mov_b32 s69,s68
R 38,2,1024
M 22,a[236:239],86
s_add_u32 s68,s68,s63
M 18,a[224:227],82
R 42,2,2048
M 18,a[232:235],86
L 5
L 4
v_cmp_eq_u32 vcc,s53,v7
M 18,a[240:243],90
L 3
R 46,2,3072
v_add_f32 v118,v118,v119
M 18,a[248:251],94
s_or_b64 vcc,vcc,s[94:95]
v_add_f32 v120,v120,v121
v_add_f32 v118,v118,v120
M 22,a[244:247],90
s_cmp_eq_u32 vcc_lo,-1
R 50,2,4096
v_mul_f32 v118,s86,v118
M 22,a[252:255],94
s_waitcnt vmcnt(6) lgkmcnt(3)
buffer_store_dword v118,v14,s[36:39],s77 offen offset:0 sc0 sc1
s_cbranch_scc0 Lg2_K1_%=
Lg2_F1_%=:
v_mfma_f32_16x16x32_bf16 v[30:33],a[4:7],v[34:37],0
s_add_u32 s58,s58,s61
s_add_u32 m0,s59,1024
s_and_b32 s78,s54,127
R 54,2,5120
M 30,a[12:15],38
s_cmp_eq_u32 s78,s85
s_cselect_b64 s[40:41],-1,0
s_add_u32 s56,s55,0
buffer_load_dword v8,s[36:39],s58 offen offset:0 sc1 lds
v_mfma_f32_16x16x32_bf16 v[26:29],a[0:3],v[34:37],0
s_add_u32 m0,s57,0
R 58,2,6144
v_cndmask_b32 v17,v15,v16,s[40:41]
M 26,a[8:11],38
L 3
D 17,56,0
M 26,a[16:19],42
s_add_u32 s56,s55,256
R 62,2,7168
v_lshlrev_b32 v106,16,v122
M 26,a[24:27],46
s_add_u32 s54,s54,s46
v_and_b32 v107,0xffff0000,v122
v_lshlrev_b32 v108,16,v123
M 30,a[20:23],42
s_and_b32 s79,s54,127
R 66,2,8192
v_and_b32 v109,0xffff0000,v123
M 30,a[28:31],46
L 3
D 17,56,1024
M 30,a[36:39],50
s_add_u32 s56,s55,512
R 70,2,9216
v_lshlrev_b32 v110,16,v124
M 30,a[44:47],54
s_cmp_eq_u32 s79,0
v_and_b32 v111,0xffff0000,v124
v_lshlrev_b32 v112,16,v125
M 26,a[32:35],50
s_cselect_b32 s94,0x3300,0
R 74,2,10240
v_and_b32 v113,0xffff0000,v125
M 26,a[40:43],54
L 3
D 17,56,2048
M 26,a[48:51],58
s_add_u32 s56,s55,768
R 78,2,11264
v_fma_f32 v106,v106,v99,v98
M 26,a[56:59],62
D 17,56,3072
M 30,a[52:55],58
s_add_u32 m0,s57,16384
R 82,2,12288
v_fma_f32 v107,v107,v99,v98
M 30,a[60:63],62
L 3
v_fma_f32 v108,v108,v99,v98
v_fma_f32 v109,v109,v99,v98
M 30,a[68:71],66
s_add_u32 s56,s55,20480
R 86,2,13312
v_fma_f32 v110,v110,v99,v98
M 30,a[76:79],70
buffer_load_dwordx4 v[126:129],v0,s[36:39],s74 offen offset:0 sc1
M 26,a[64:67],66
s_cmp_eq_u32 s79,127
R 90,2,14336
v_fma_f32 v111,v111,v99,v98
M 26,a[72:75],70
L 3
buffer_load_dwordx2 v[102:103],v11,s[36:39],s73 offen offset:256 sc1
M 26,a[80:83],74
s_waitcnt vmcnt(10)
R 94,2,15360
v_fma_f32 v112,v112,v99,v98
M 26,a[88:91],78
s_barrier
v_fma_f32 v113,v113,v99,v98
v_add_f32 v18,v18,v106
M 30,a[84:87],74
s_cselect_b32 s94,0xcc00,s94
R 34,2,16384
v_add_f32 v19,v19,v107
M 30,a[92:95],78
L 3
v_add_f32 v20,v20,v108
v_add_f32 v21,v21,v109
M 30,a[100:103],82
s_mov_b32 s67,s65
R 38,2,17408
v_add_f32 v22,v22,v110
M 30,a[108:111],86
s_mov_b32 s77,s75
v_cndmask_b32 v17,v10,v9,s[40:41]
v_add_f32 v23,v23,v111
M 26,a[96:99],82
s_add_u32 s73,s73,s64
R 42,2,18432
v_add_f32 v24,v24,v112
M 26,a[104:107],86
L 3
D 17,56,0
M 26,a[112:115],90
s_add_u32 s56,s55,20736
R 46,2,19456
v_add_f32 v25,v25,v113
M 26,a[120:123],94
s_add_u32 s74,s74,s62
v_cvt_pk_bf16_f32 v114,v18,v19
v_cvt_pk_bf16_f32 v115,v20,v21
M 30,a[116:119],90
s_add_u32 s65,s65,s62
R 50,2,20480
v_fma_f32 v107,v18,v18,s87
M 30,a[124:127],94
L 3
v_add_f32 v106,v18,v19
v_fma_f32 v107,v19,v19,v107
M 30,a[132:135],34
s_add_u32 s75,s75,s63
R 54,2,21504
v_add_f32 v106,v106,v20
M 30,a[140:143],38
D 17,56,1024
M 26,a[128:131],34
s_add_u32 s56,s55,20992
R 58,2,22528
v_fma_f32 v107,v20,v20,v107
M 26,a[136:139],38
L 3
v_add_f32 v106,v106,v21
v_fma_f32 v107,v21,v21,v107
M 26,a[144:147],42
s_sub_u32 s57,s84,s57
R 62,2,23552
v_add_f32 v106,v106,v22
M 26,a[152:155],46
v_fma_f32 v107,v22,v22,v107
v_add_f32 v106,v106,v23
M 30,a[148:151],42
R 66,2,24576
v_fma_f32 v107,v23,v23,v107
M 30,a[156:159],46
L 3
v_add_f32 v106,v106,v24
v_fma_f32 v107,v24,v24,v107
M 30,a[164:167],50
R 70,2,25600
v_add_f32 v106,v106,v25
M 30,a[172:175],54
v_fma_f32 v107,v25,v25,v107
v_cvt_pk_bf16_f32 v116,v22,v23
M 26,a[160:163],50
R 74,2,26624
v_cvt_pk_bf16_f32 v117,v24,v25
M 26,a[168:171],54
L 3
v_permlane16_swap_b32 v106,v107
v_add_f32 v106,v106,v107
v_mov_b32 v107,v106
M 26,a[176:179],58
R 78,2,27648
M 26,a[184:187],62
v_permlane32_swap_b32 v106,v107
v_add_f32 v106,v106,v107
M 30,a[180:183],58
R 82,2,28672
ds_write_b32 v12,v106 offset:0
M 30,a[188:191],62
L 4
D 17,56,2048
M 30,a[196:199],66
s_add_u32 s56,s55,21248
s_add_u32 s55,s55,s60
R 86,2,29696
M 30,a[204:207],70
D 17,56,3072
M 26,a[192:195],66
R 90,2,30720
M 26,a[200:203],70
L 4
L 2
buffer_store_dwordx4 v[114:117],v0,s[36:39],s66 offen offset:0 sc0 sc1
M 26,a[208:211],74
s_waitcnt vmcnt(7)
R 94,2,31744
M 26,a[216:219],78
s_barrier
ds_read_b32 v7,v6 offset:1024
R 118,13,0
M 30,a[212:215],74
R 34,1,0
M 30,a[220:223],78
L 5
buffer_store_dword v4,v3,s[36:39],s70 offen offset:0 sc0 sc1
M 30,a[228:231],82
s_mov_b32 s70,s69
s_mov_b32 s69,s68
R 38,1,1024
M 30,a[236:239],86
s_add_u32 s68,s68,s63
M 26,a[224:227],82
R 42,1,2048
M 26,a[232:235],86
L 5
L 4
v_cmp_eq_u32 vcc,s53,v7
M 26,a[240:243],90
L 3
R 46,1,3072
v_add_f32 v118,v118,v119
M 26,a[248:251],94
s_or_b64 vcc,vcc,s[94:95]
v_add_f32 v120,v120,v121
v_add_f32 v118,v118,v120
M 30,a[244:247],90
s_cmp_eq_u32 vcc_lo,-1
R 50,1,4096
v_mul_f32 v118,s86,v118
M 30,a[252:255],94
s_waitcnt vmcnt(6) lgkmcnt(3)
buffer_store_dword v118,v14,s[36:39],s76 offen offset:0 sc0 sc1
s_cbranch_scc1 Lg2_F0_%=
Lg2_K0_%=:
Lg2_L0_%=:
v_mfma_f32_16x16x32_bf16 v[22:25],a[4:7],v[34:37],0
s_add_u32 m0,s59,3072
R 54,1,5120
v_lshlrev_b32 v106,16,v126
M 22,a[12:15],38
buffer_load_dword v8,s[36:39],s58 offen offset:0 sc1 lds
v_mfma_f32_16x16x32_bf16 v[18:21],a[0:3],v[34:37],0
s_mov_b32 s66,s65
R 58,1,6144
v_and_b32 v107,0xffff0000,v126
M 18,a[8:11],38
L 3
v_lshlrev_b32 v108,16,v127
v_and_b32 v109,0xffff0000,v127
M 18,a[16:19],42
s_mov_b32 s76,s75
R 62,1,7168
v_lshlrev_b32 v110,16,v128
M 18,a[24:27],46
s_add_u32 s65,s65,s62
v_and_b32 v111,0xffff0000,v128
v_lshlrev_b32 v112,16,v129
M 22,a[20:23],42
s_add_u32 s75,s75,s63
R 66,1,8192
v_and_b32 v113,0xffff0000,v129
v_fma_f32 v106,v106,v103,v102
M 22,a[28:31],46
L 3
v_fma_f32 v107,v107,v103,v102
v_fma_f32 v108,v108,v103,v102
M 22,a[36:39],50
R 70,1,9216
v_fma_f32 v109,v109,v103,v102
M 22,a[44:47],54
v_fma_f32 v110,v110,v103,v102
v_fma_f32 v111,v111,v103,v102
M 18,a[32:35],50
R 74,1,10240
v_fma_f32 v112,v112,v103,v102
M 18,a[40:43],54
L 3
v_fma_f32 v113,v113,v103,v102
v_add_f32 v26,v26,v106
M 18,a[48:51],58
R 78,1,11264
v_add_f32 v27,v27,v107
M 18,a[56:59],62
v_add_f32 v28,v28,v108
v_add_f32 v29,v29,v109
v_add_f32 v30,v30,v110
M 22,a[52:55],58
R 82,1,12288
v_add_f32 v31,v31,v111
M 22,a[60:63],62
L 3
v_add_f32 v32,v32,v112
v_add_f32 v33,v33,v113
M 22,a[68:71],66
R 86,1,13312
v_cvt_pk_bf16_f32 v114,v26,v27
M 22,a[76:79],70
buffer_load_dwordx4 v[122:125],v0,s[36:39],s74 offen offset:0 sc1
M 18,a[64:67],66
s_add_u32 s74,s74,s62
R 90,1,14336
v_cvt_pk_bf16_f32 v115,v28,v29
M 18,a[72:75],70
L 3
buffer_load_dwordx2 v[98:99],v11,s[36:39],s73 offen offset:256 sc1
v_cvt_pk_bf16_f32 v116,v30,v31
M 18,a[80:83],74
s_waitcnt vmcnt(2)
s_add_u32 s73,s73,s64
R 94,1,15360
M 18,a[88:91],78
s_barrier
ds_read_b32 v7,v6 offset:3072
v_cvt_pk_bf16_f32 v117,v32,v33
M 22,a[84:87],74
R 34,1,16384
v_add_f32 v106,v26,v27
M 22,a[92:95],78
L 4
buffer_store_dword v4,v3,s[36:39],s70 offen offset:0 sc0 sc1
M 22,a[100:103],82
s_mov_b32 s70,0x7fff0000
R 38,1,17408
v_fma_f32 v107,v26,v26,s87
M 22,a[108:111],86
buffer_store_dwordx4 v[114:117],v0,s[36:39],s67 offen offset:0 sc0 sc1
v_fma_f32 v107,v27,v27,v107
M 18,a[96:99],82
R 42,1,18432
v_add_f32 v106,v106,v28
v_fma_f32 v107,v28,v28,v107
M 18,a[104:107],86
L 4
L 3
v_cmp_eq_u32 vcc,s53,v7
M 18,a[112:115],90
R 46,1,19456
v_add_f32 v106,v106,v29
M 18,a[120:123],94
s_or_b64 vcc,vcc,s[94:95]
v_fma_f32 v107,v29,v29,v107
v_add_f32 v106,v106,v30
M 22,a[116:119],90
R 50,1,20480
v_fma_f32 v107,v30,v30,v107
M 22,a[124:127],94
s_cmp_eq_u32 vcc_lo,-1
L 3
v_add_f32 v106,v106,v31
s_cbranch_scc0 Lg2_X0_%=
M 22,a[132:135],34
s_and_b32 s78,s54,127
s_cmp_eq_u32 s78,s85
s_cselect_b64 s[40:41],-1,0
R 54,1,21504
M 22,a[140:143],38
s_add_u32 m0,s57,0
s_add_u32 s56,s55,0
s_add_u32 s58,s58,s61
v_cndmask_b32 v17,v15,v16,s[40:41]
M 18,a[128:131],34
s_add_u32 s54,s54,s46
R 58,1,22528
v_fma_f32 v107,v31,v31,v107
M 18,a[136:139],38
L 3
D 17,56,0
M 18,a[144:147],42
s_add_u32 s56,s55,256
R 62,1,23552
v_add_f32 v106,v106,v32
M 18,a[152:155],46
s_and_b32 s79,s54,127
v_fma_f32 v107,v32,v32,v107
v_add_f32 v106,v106,v33
M 22,a[148:151],42
s_cmp_eq_u32 s79,0
R 66,1,24576
v_fma_f32 v107,v33,v33,v107
M 22,a[156:159],46
L 3
D 17,56,1024
M 22,a[164:167],50
s_cselect_b32 s94,0x3300,0
s_add_u32 s56,s55,512
R 70,1,25600
M 22,a[172:175],54
s_cmp_eq_u32 s79,127
v_permlane16_swap_b32 v106,v107
v_add_f32 v106,v106,v107
M 18,a[160:163],50
s_cselect_b32 s94,0xcc00,s94
)"
R"(R 74,1,26624
v_mov_b32 v107,v106
M 18,a[168:171],54
L 3
D 17,56,2048
M 18,a[176:179],58
s_add_u32 s56,s55,768
R 78,1,27648
M 18,a[184:187],62
v_permlane32_swap_b32 v106,v107
v_add_f32 v106,v106,v107
M 22,a[180:183],58
R 82,1,28672
ds_write_b32 v12,v106 offset:2048
M 22,a[188:191],62
L 4
D 17,56,3072
M 22,a[196:199],66
s_add_u32 m0,s59,0
s_add_u32 s56,s55,20480
R 86,1,29696
M 22,a[204:207],70
buffer_load_dword v8,s[36:39],s58 offen offset:0 sc1 lds
M 18,a[192:195],66
s_add_u32 m0,s57,16384
R 90,1,30720
v_cndmask_b32 v17,v10,v9,s[40:41]
M 18,a[200:203],70
L 4
L 2
s_sub_u32 s57,s84,s57
M 18,a[208:211],74
s_waitcnt vmcnt(0)
R 94,1,31744
M 18,a[216:219],78
s_barrier
D 17,56,0
M 22,a[212:215],74
s_add_u32 s56,s55,20736
R 34,2,0
ds_read_b32 v7,v6 offset:0
M 22,a[220:223],78
L 4
buffer_store_dword v4,v3,s[36:39],s70 offen offset:0 sc0 sc1
M 22,a[228:231],82
s_mov_b32 s70,s69
R 38,2,1024
R 118,13,2048
M 22,a[236:239],86
D 17,56,1024
M 18,a[224:227],82
s_add_u32 s56,s55,20992
s_mov_b32 s69,s68
R 42,2,2048
M 18,a[232:235],86
L 5
D 17,56,2048
M 18,a[240:243],90
L 3
s_add_u32 s56,s55,21248
R 46,2,3072
v_cmp_eq_u32 vcc,s53,v7
M 18,a[248:251],94
D 17,56,3072
M 22,a[244:247],90
s_add_u32 s55,s55,s60
s_add_u32 s68,s68,s63
L 2
s_or_b64 vcc,vcc,s[94:95]
s_cmp_eq_u32 vcc_lo,-1
R 50,2,4096
v_add_f32 v118,v118,v119
v_add_f32 v120,v120,v121
v_add_f32 v118,v118,v120
v_mul_f32 v118,s86,v118
M 22,a[252:255],94
s_nop 0
buffer_store_dword v118,v14,s[36:39],s77 offen offset:0 sc0 sc1
buffer_store_dword v4,v3,s[36:39],s71 offen offset:0 sc0 sc1
buffer_store_dword v4,v3,s[36:39],s71 offen offset:0 sc0 sc1
s_cbranch_scc1 Lg2_F1_%=
s_branch Lg2_K1_%=
Lg2_X0_%=:
M 22,a[132:135],34
s_add_u32 m0,s59,0
R 54,1,21504
v_fma_f32 v107,v31,v31,v107
M 22,a[140:143],38
buffer_load_dword v8,s[36:39],s58 offen offset:0 sc1 lds
M 18,a[128:131],34
R 58,1,22528
v_add_f32 v106,v106,v32
M 18,a[136:139],38
L 3
v_fma_f32 v107,v32,v32,v107
v_add_f32 v106,v106,v33
M 18,a[144:147],42
R 62,1,23552
v_fma_f32 v107,v33,v33,v107
M 18,a[152:155],46
M 22,a[148:151],42
R 66,1,24576
M 22,a[156:159],46
L 3
v_permlane16_swap_b32 v106,v107
v_add_f32 v106,v106,v107
M 22,a[164:167],50
R 70,1,25600
v_mov_b32 v107,v106
M 22,a[172:175],54
M 18,a[160:163],50
R 74,1,26624
M 18,a[168:171],54
L 3
v_permlane32_swap_b32 v106,v107
M 18,a[176:179],58
R 78,1,27648
v_add_f32 v106,v106,v107
M 18,a[184:187],62
ds_write_b32 v12,v106 offset:2048
M 22,a[180:183],58
R 82,1,28672
M 22,a[188:191],62
L 4
M 22,a[196:199],66
R 86,1,29696
M 22,a[204:207],70
M 18,a[192:195],66
R 90,1,30720
M 18,a[200:203],70
L 4
L 3
M 18,a[208:211],74
s_waitcnt vmcnt(0)
R 94,1,31744
M 18,a[216:219],78
s_barrier
R 118,13,2048
ds_read_b32 v7,v6 offset:0
M 22,a[212:215],74
buffer_store_dword v4,v3,s[36:39],s70 offen offset:0 sc0 sc1
M 22,a[220:223],78
L 4
s_mov_b32 s70,s69
s_mov_b32 s69,s68
M 22,a[228:231],82
s_add_u32 s68,s68,s63
M 22,a[236:239],86
M 18,a[224:227],82
M 18,a[232:235],86
L 2
L 1
L 0
M 18,a[240:243],90
v_add_f32 v118,v118,v119
v_add_f32 v120,v120,v121
v_add_f32 v118,v118,v120
M 18,a[248:251],94
v_cmp_eq_u32 vcc,s53,v7
v_mul_f32 v118,s86,v118
M 22,a[244:247],90
buffer_store_dword v118,v14,s[36:39],s77 offen offset:0 sc0 sc1
M 22,a[252:255],94
s_nop 0
s_or_b64 vcc,vcc,s[94:95]
s_mov_b32 s80,1
s_cmp_eq_u32 vcc_lo,-1
s_cbranch_scc0 Lg2_xn0_%=
s_and_b32 s78,s54,127
s_cmp_eq_u32 s78,s85
s_cselect_b64 s[40:41],-1,0
v_cndmask_b32 v17,v15,v16,s[40:41]
s_add_u32 m0,s57,0
s_add_u32 s56,s55,0
D 17,56,0
v_lshlrev_b32 v106,16,v122
v_and_b32 v107,0xffff0000,v122
v_lshlrev_b32 v108,16,v123
v_and_b32 v109,0xffff0000,v123
v_lshlrev_b32 v110,16,v124
s_add_u32 s56,s55,256
D 17,56,1024
v_and_b32 v111,0xffff0000,v124
v_lshlrev_b32 v112,16,v125
v_and_b32 v113,0xffff0000,v125
v_fma_f32 v106,v106,v99,v98
v_fma_f32 v107,v107,v99,v98
s_add_u32 s56,s55,512
D 17,56,2048
v_fma_f32 v108,v108,v99,v98
v_fma_f32 v109,v109,v99,v98
v_fma_f32 v110,v110,v99,v98
v_fma_f32 v111,v111,v99,v98
v_fma_f32 v112,v112,v99,v98
s_add_u32 s56,s55,768
D 17,56,3072
v_fma_f32 v113,v113,v99,v98
v_add_f32 v18,v18,v106
v_add_f32 v19,v19,v107
v_add_f32 v20,v20,v108
v_add_f32 v21,v21,v109
s_add_u32 s58,s58,s61
v_cndmask_b32 v17,v10,v9,s[40:41]
s_add_u32 m0,s57,16384
s_add_u32 s56,s55,20480
D 17,56,0
v_add_f32 v22,v22,v110
v_add_f32 v23,v23,v111
v_add_f32 v24,v24,v112
v_add_f32 v25,v25,v113
v_cvt_pk_bf16_f32 v114,v18,v19
s_add_u32 s56,s55,20736
D 17,56,1024
v_cvt_pk_bf16_f32 v115,v20,v21
v_cvt_pk_bf16_f32 v116,v22,v23
v_cvt_pk_bf16_f32 v117,v24,v25
s_add_u32 s56,s55,20992
D 17,56,2048
s_add_u32 s56,s55,21248
D 17,56,3072
buffer_store_dwordx4 v[114:117],v0,s[36:39],s66 offen offset:0 sc0 sc1
v_add_f32 v106,v18,v19
v_fma_f32 v107,v18,v18,s87
s_add_u32 s55,s55,s60
s_sub_u32 s57,s84,s57
s_add_u32 s54,s54,s46
v_fma_f32 v107,v19,v19,v107
v_add_f32 v106,v106,v20
v_fma_f32 v107,v20,v20,v107
v_add_f32 v106,v106,v21
v_fma_f32 v107,v21,v21,v107
v_add_f32 v106,v106,v22
v_fma_f32 v107,v22,v22,v107
v_add_f32 v106,v106,v23
v_fma_f32 v107,v23,v23,v107
v_add_f32 v106,v106,v24
v_fma_f32 v107,v24,v24,v107
v_add_f32 v106,v106,v25
v_fma_f32 v107,v25,v25,v107
s_nop 1
v_permlane16_swap_b32 v106,v107
v_add_f32 v106,v106,v107
v_mov_b32 v107,v106
s_nop 1
v_permlane32_swap_b32 v106,v107
v_add_f32 v106,v106,v107
ds_write_b32 v12,v106 offset:0
L 0
s_barrier
R 118,13,0
L 0
v_add_f32 v118,v118,v119
v_add_f32 v120,v120,v121
v_add_f32 v118,v118,v120
v_mul_f32 v118,s86,v118
buffer_store_dword v118,v14,s[36:39],s76 offen offset:0 sc0 sc1
s_mov_b32 s72,s69
s_mov_b32 s69,0x7fff0000
s_mov_b32 s66,0x7fff0000
s_mov_b32 s76,0x7fff0000
s_branch Lg2_gox_%=
Lg2_xn0_%=:
s_cmp_ge_u32 s54,256
s_cbranch_scc1 Lg2_fin0_%=
s_add_u32 m0,s59,4096
s_nop 0
buffer_load_dword v8,s[36:39],s58 offen offset:0 sc1 lds
buffer_store_dword v4,v3,s[36:39],s71 offen offset:0 sc0 sc1
s_add_u32 m0,s59,5120
s_nop 0
buffer_load_dword v8,s[36:39],s58 offen offset:0 sc1 lds
buffer_store_dword v4,v3,s[36:39],s71 offen offset:0 sc0 sc1
s_mov_b32 s83,0
v_lshlrev_b32 v106,16,v122
v_and_b32 v107,0xffff0000,v122
v_lshlrev_b32 v108,16,v123
v_and_b32 v109,0xffff0000,v123
v_lshlrev_b32 v110,16,v124
v_and_b32 v111,0xffff0000,v124
v_lshlrev_b32 v112,16,v125
v_and_b32 v113,0xffff0000,v125
v_fma_f32 v106,v106,v99,v98
v_fma_f32 v107,v107,v99,v98
v_fma_f32 v108,v108,v99,v98
v_fma_f32 v109,v109,v99,v98
v_fma_f32 v110,v110,v99,v98
v_fma_f32 v111,v111,v99,v98
v_fma_f32 v112,v112,v99,v98
v_fma_f32 v113,v113,v99,v98
v_add_f32 v18,v18,v106
v_add_f32 v19,v19,v107
v_add_f32 v20,v20,v108
v_add_f32 v21,v21,v109
v_add_f32 v22,v22,v110
v_add_f32 v23,v23,v111
v_add_f32 v24,v24,v112
v_add_f32 v25,v25,v113
v_cvt_pk_bf16_f32 v114,v18,v19
v_cvt_pk_bf16_f32 v115,v20,v21
v_cvt_pk_bf16_f32 v116,v22,v23
v_cvt_pk_bf16_f32 v117,v24,v25
buffer_store_dwordx4 v[114:117],v0,s[36:39],s66 offen offset:0 sc0 sc1
v_add_f32 v106,v18,v19
v_fma_f32 v107,v18,v18,s87
v_fma_f32 v107,v19,v19,v107
v_add_f32 v106,v106,v20
v_fma_f32 v107,v20,v20,v107
v_add_f32 v106,v106,v21
v_fma_f32 v107,v21,v21,v107
v_add_f32 v106,v106,v22
v_fma_f32 v107,v22,v22,v107
v_add_f32 v106,v106,v23
v_fma_f32 v107,v23,v23,v107
v_add_f32 v106,v106,v24
v_fma_f32 v107,v24,v24,v107
v_add_f32 v106,v106,v25
v_fma_f32 v107,v25,v25,v107
s_nop 1
v_permlane16_swap_b32 v106,v107
v_add_f32 v106,v106,v107
v_mov_b32 v107,v106
s_nop 1
v_permlane32_swap_b32 v106,v107
v_add_f32 v106,v106,v107
ds_write_b32 v12,v106 offset:0
L 0
s_barrier
R 118,13,0
L 0
v_add_f32 v118,v118,v119
v_add_f32 v120,v120,v121
v_add_f32 v118,v118,v120
v_mul_f32 v118,s86,v118
buffer_store_dword v118,v14,s[36:39],s76 offen offset:0 sc0 sc1
s_mov_b32 s72,s69
s_mov_b32 s69,0x7fff0000
s_mov_b32 s66,0x7fff0000
s_mov_b32 s76,0x7fff0000
s_branch Lg2_poll_%=
Lg2_fin0_%=:
v_lshlrev_b32 v106,16,v122
v_and_b32 v107,0xffff0000,v122
v_lshlrev_b32 v108,16,v123
v_and_b32 v109,0xffff0000,v123
v_lshlrev_b32 v110,16,v124
v_and_b32 v111,0xffff0000,v124
v_lshlrev_b32 v112,16,v125
v_and_b32 v113,0xffff0000,v125
v_fma_f32 v106,v106,v99,v98
v_fma_f32 v107,v107,v99,v98
v_fma_f32 v108,v108,v99,v98
v_fma_f32 v109,v109,v99,v98
v_fma_f32 v110,v110,v99,v98
v_fma_f32 v111,v111,v99,v98
v_fma_f32 v112,v112,v99,v98
v_fma_f32 v113,v113,v99,v98
v_add_f32 v18,v18,v106
v_add_f32 v19,v19,v107
v_add_f32 v20,v20,v108
v_add_f32 v21,v21,v109
v_add_f32 v22,v22,v110
v_add_f32 v23,v23,v111
v_add_f32 v24,v24,v112
v_add_f32 v25,v25,v113
v_cvt_pk_bf16_f32 v114,v18,v19
v_cvt_pk_bf16_f32 v115,v20,v21
v_cvt_pk_bf16_f32 v116,v22,v23
v_cvt_pk_bf16_f32 v117,v24,v25
buffer_store_dwordx4 v[114:117],v0,s[36:39],s66 offen offset:0 sc0 sc1
v_add_f32 v106,v18,v19
v_fma_f32 v107,v18,v18,s87
v_fma_f32 v107,v19,v19,v107
v_add_f32 v106,v106,v20
v_fma_f32 v107,v20,v20,v107
v_add_f32 v106,v106,v21
v_fma_f32 v107,v21,v21,v107
v_add_f32 v106,v106,v22
v_fma_f32 v107,v22,v22,v107
v_add_f32 v106,v106,v23
v_fma_f32 v107,v23,v23,v107
v_add_f32 v106,v106,v24
v_fma_f32 v107,v24,v24,v107
v_add_f32 v106,v106,v25
v_fma_f32 v107,v25,v25,v107
s_nop 1
v_permlane16_swap_b32 v106,v107
v_add_f32 v106,v106,v107
v_mov_b32 v107,v106
s_nop 1
v_permlane32_swap_b32 v106,v107
v_add_f32 v106,v106,v107
ds_write_b32 v12,v106 offset:0
L 0
s_barrier
R 118,13,0
L 0
v_add_f32 v118,v118,v119
v_add_f32 v120,v120,v121
v_add_f32 v118,v118,v120
v_mul_f32 v118,s86,v118
buffer_store_dword v118,v14,s[36:39],s76 offen offset:0 sc0 sc1
s_mov_b32 s72,s69
s_mov_b32 s69,0x7fff0000
s_mov_b32 s66,0x7fff0000
s_mov_b32 s76,0x7fff0000
s_branch Lg2_done_%=
Lg2_K1_%=:
Lg2_L1_%=:
v_mfma_f32_16x16x32_bf16 v[30:33],a[4:7],v[34:37],0
s_add_u32 m0,s59,3072
R 54,2,5120
v_lshlrev_b32 v106,16,v122
M 30,a[12:15],38
buffer_load_dword v8,s[36:39],s58 offen offset:0 sc1 lds
v_mfma_f32_16x16x32_bf16 v[26:29],a[0:3],v[34:37],0
s_mov_b32 s67,s65
R 58,2,6144
v_and_b32 v107,0xffff0000,v122
M 26,a[8:11],38
L 3
v_lshlrev_b32 v108,16,v123
v_and_b32 v109,0xffff0000,v123
M 26,a[16:19],42
s_mov_b32 s77,s75
R 62,2,7168
v_lshlrev_b32 v110,16,v124
M 26,a[24:27],46
s_add_u32 s65,s65,s62
v_and_b32 v111,0xffff0000,v124
v_lshlrev_b32 v112,16,v125
M 30,a[20:23],42
s_add_u32 s75,s75,s63
R 66,2,8192
v_and_b32 v113,0xffff0000,v125
v_fma_f32 v106,v106,v99,v98
M 30,a[28:31],46
L 3
v_fma_f32 v107,v107,v99,v98
v_fma_f32 v108,v108,v99,v98
M 30,a[36:39],50
R 70,2,9216
v_fma_f32 v109,v109,v99,v98
M 30,a[44:47],54
v_fma_f32 v110,v110,v99,v98
v_fma_f32 v111,v111,v99,v98
M 26,a[32:35],50
R 74,2,10240
v_fma_f32 v112,v112,v99,v98
M 26,a[40:43],54
L 3
v_fma_f32 v113,v113,v99,v98
v_add_f32 v18,v18,v106
M 26,a[48:51],58
R 78,2,11264
v_add_f32 v19,v19,v107
M 26,a[56:59],62
v_add_f32 v20,v20,v108
v_add_f32 v21,v21,v109
v_add_f32 v22,v22,v110
M 30,a[52:55],58
R 82,2,12288
v_add_f32 v23,v23,v111
M 30,a[60:63],62
L 3
v_add_f32 v24,v24,v112
v_add_f32 v25,v25,v113
M 30,a[68:71],66
R 86,2,13312
v_cvt_pk_bf16_f32 v114,v18,v19
M 30,a[76:79],70
buffer_load_dwordx4 v[126:129],v0,s[36:39],s74 offen offset:0 sc1
M 26,a[64:67],66
s_add_u32 s74,s74,s62
R 90,2,14336
v_cvt_pk_bf16_f32 v115,v20,v21
M 26,a[72:75],70
L 3
buffer_load_dwordx2 v[102:103],v11,s[36:39],s73 offen offset:256 sc1
v_cvt_pk_bf16_f32 v116,v22,v23
M 26,a[80:83],74
s_waitcnt vmcnt(2)
s_add_u32 s73,s73,s64
R 94,2,15360
M 26,a[88:91],78
s_barrier
ds_read_b32 v7,v6 offset:3072
v_cvt_pk_bf16_f32 v117,v24,v25
M 30,a[84:87],74
R 34,2,16384
v_add_f32 v106,v18,v19
M 30,a[92:95],78
L 4
buffer_store_dword v4,v3,s[36:39],s70 offen offset:0 sc0 sc1
M 30,a[100:103],82
s_mov_b32 s70,0x7fff0000
R 38,2,17408
v_fma_f32 v107,v18,v18,s87
M 30,a[108:111],86
buffer_store_dwordx4 v[114:117],v0,s[36:39],s66 offen offset:0 sc0 sc1
v_fma_f32 v107,v19,v19,v107
)"
R"(M 26,a[96:99],82
R 42,2,18432
v_add_f32 v106,v106,v20
v_fma_f32 v107,v20,v20,v107
M 26,a[104:107],86
L 4
L 3
v_cmp_eq_u32 vcc,s53,v7
M 26,a[112:115],90
R 46,2,19456
v_add_f32 v106,v106,v21
M 26,a[120:123],94
s_or_b64 vcc,vcc,s[94:95]
v_fma_f32 v107,v21,v21,v107
v_add_f32 v106,v106,v22
M 30,a[116:119],90
R 50,2,20480
v_fma_f32 v107,v22,v22,v107
M 30,a[124:127],94
s_cmp_eq_u32 vcc_lo,-1
L 3
v_add_f32 v106,v106,v23
s_cbranch_scc0 Lg2_X1_%=
M 30,a[132:135],34
s_and_b32 s78,s54,127
s_cmp_eq_u32 s78,s85
s_cselect_b64 s[40:41],-1,0
R 54,2,21504
M 30,a[140:143],38
s_add_u32 m0,s57,0
s_add_u32 s56,s55,0
s_add_u32 s58,s58,s61
v_cndmask_b32 v17,v15,v16,s[40:41]
M 26,a[128:131],34
s_add_u32 s54,s54,s46
R 58,2,22528
v_fma_f32 v107,v23,v23,v107
M 26,a[136:139],38
L 3
D 17,56,0
M 26,a[144:147],42
s_add_u32 s56,s55,256
R 62,2,23552
v_add_f32 v106,v106,v24
M 26,a[152:155],46
s_and_b32 s79,s54,127
v_fma_f32 v107,v24,v24,v107
v_add_f32 v106,v106,v25
M 30,a[148:151],42
s_cmp_eq_u32 s79,0
R 66,2,24576
v_fma_f32 v107,v25,v25,v107
M 30,a[156:159],46
L 3
D 17,56,1024
M 30,a[164:167],50
s_cselect_b32 s94,0x3300,0
s_add_u32 s56,s55,512
R 70,2,25600
M 30,a[172:175],54
s_cmp_eq_u32 s79,127
v_permlane16_swap_b32 v106,v107
v_add_f32 v106,v106,v107
M 26,a[160:163],50
s_cselect_b32 s94,0xcc00,s94
R 74,2,26624
v_mov_b32 v107,v106
M 26,a[168:171],54
L 3
D 17,56,2048
M 26,a[176:179],58
s_add_u32 s56,s55,768
R 78,2,27648
M 26,a[184:187],62
v_permlane32_swap_b32 v106,v107
v_add_f32 v106,v106,v107
M 30,a[180:183],58
R 82,2,28672
ds_write_b32 v12,v106 offset:0
M 30,a[188:191],62
L 4
D 17,56,3072
M 30,a[196:199],66
s_add_u32 m0,s59,1024
s_add_u32 s56,s55,20480
R 86,2,29696
M 30,a[204:207],70
buffer_load_dword v8,s[36:39],s58 offen offset:0 sc1 lds
M 26,a[192:195],66
s_add_u32 m0,s57,16384
R 90,2,30720
v_cndmask_b32 v17,v10,v9,s[40:41]
M 26,a[200:203],70
L 4
L 2
s_sub_u32 s57,s84,s57
M 26,a[208:211],74
s_waitcnt vmcnt(0)
R 94,2,31744
M 26,a[216:219],78
s_barrier
D 17,56,0
M 30,a[212:215],74
s_add_u32 s56,s55,20736
R 34,1,0
ds_read_b32 v7,v6 offset:1024
M 30,a[220:223],78
L 4
buffer_store_dword v4,v3,s[36:39],s70 offen offset:0 sc0 sc1
M 30,a[228:231],82
s_mov_b32 s70,s69
R 38,1,1024
R 118,13,0
M 30,a[236:239],86
D 17,56,1024
M 26,a[224:227],82
s_add_u32 s56,s55,20992
s_mov_b32 s69,s68
R 42,1,2048
M 26,a[232:235],86
L 5
D 17,56,2048
M 26,a[240:243],90
L 3
s_add_u32 s56,s55,21248
R 46,1,3072
v_cmp_eq_u32 vcc,s53,v7
M 26,a[248:251],94
D 17,56,3072
M 30,a[244:247],90
s_add_u32 s55,s55,s60
s_add_u32 s68,s68,s63
L 2
s_or_b64 vcc,vcc,s[94:95]
s_cmp_eq_u32 vcc_lo,-1
R 50,1,4096
v_add_f32 v118,v118,v119
v_add_f32 v120,v120,v121
v_add_f32 v118,v118,v120
v_mul_f32 v118,s86,v118
M 30,a[252:255],94
s_nop 0
buffer_store_dword v118,v14,s[36:39],s76 offen offset:0 sc0 sc1
buffer_store_dword v4,v3,s[36:39],s71 offen offset:0 sc0 sc1
buffer_store_dword v4,v3,s[36:39],s71 offen offset:0 sc0 sc1
s_cbranch_scc1 Lg2_F0_%=
s_branch Lg2_K0_%=
Lg2_X1_%=:
M 30,a[132:135],34
s_add_u32 m0,s59,1024
R 54,2,21504
v_fma_f32 v107,v23,v23,v107
M 30,a[140:143],38
buffer_load_dword v8,s[36:39],s58 offen offset:0 sc1 lds
M 26,a[128:131],34
R 58,2,22528
v_add_f32 v106,v106,v24
M 26,a[136:139],38
L 3
v_fma_f32 v107,v24,v24,v107
v_add_f32 v106,v106,v25
M 26,a[144:147],42
R 62,2,23552
v_fma_f32 v107,v25,v25,v107
M 26,a[152:155],46
M 30,a[148:151],42
R 66,2,24576
M 30,a[156:159],46
L 3
v_permlane16_swap_b32 v106,v107
v_add_f32 v106,v106,v107
M 30,a[164:167],50
R 70,2,25600
v_mov_b32 v107,v106
M 30,a[172:175],54
M 26,a[160:163],50
R 74,2,26624
M 26,a[168:171],54
L 3
v_permlane32_swap_b32 v106,v107
M 26,a[176:179],58
R 78,2,27648
v_add_f32 v106,v106,v107
M 26,a[184:187],62
ds_write_b32 v12,v106 offset:0
M 30,a[180:183],58
R 82,2,28672
M 30,a[188:191],62
L 4
M 30,a[196:199],66
R 86,2,29696
M 30,a[204:207],70
M 26,a[192:195],66
R 90,2,30720
M 26,a[200:203],70
L 4
L 3
M 26,a[208:211],74
s_waitcnt vmcnt(0)
R 94,2,31744
M 26,a[216:219],78
s_barrier
R 118,13,0
ds_read_b32 v7,v6 offset:1024
M 30,a[212:215],74
buffer_store_dword v4,v3,s[36:39],s70 offen offset:0 sc0 sc1
M 30,a[220:223],78
L 4
s_mov_b32 s70,s69
s_mov_b32 s69,s68
M 30,a[228:231],82
s_add_u32 s68,s68,s63
M 30,a[236:239],86
M 26,a[224:227],82
M 26,a[232:235],86
L 2
L 1
L 0
M 26,a[240:243],90
v_add_f32 v118,v118,v119
v_add_f32 v120,v120,v121
v_add_f32 v118,v118,v120
M 26,a[248:251],94
v_cmp_eq_u32 vcc,s53,v7
v_mul_f32 v118,s86,v118
M 30,a[244:247],90
buffer_store_dword v118,v14,s[36:39],s76 offen offset:0 sc0 sc1
M 30,a[252:255],94
s_nop 0
s_or_b64 vcc,vcc,s[94:95]
s_mov_b32 s80,0
s_cmp_eq_u32 vcc_lo,-1
s_cbranch_scc0 Lg2_xn1_%=
s_and_b32 s78,s54,127
s_cmp_eq_u32 s78,s85
s_cselect_b64 s[40:41],-1,0
v_cndmask_b32 v17,v15,v16,s[40:41]
s_add_u32 m0,s57,0
s_add_u32 s56,s55,0
D 17,56,0
v_lshlrev_b32 v106,16,v126
v_and_b32 v107,0xffff0000,v126
v_lshlrev_b32 v108,16,v127
v_and_b32 v109,0xffff0000,v127
v_lshlrev_b32 v110,16,v128
s_add_u32 s56,s55,256
D 17,56,1024
v_and_b32 v111,0xffff0000,v128
v_lshlrev_b32 v112,16,v129
v_and_b32 v113,0xffff0000,v129
v_fma_f32 v106,v106,v103,v102
v_fma_f32 v107,v107,v103,v102
s_add_u32 s56,s55,512
D 17,56,2048
v_fma_f32 v108,v108,v103,v102
v_fma_f32 v109,v109,v103,v102
v_fma_f32 v110,v110,v103,v102
v_fma_f32 v111,v111,v103,v102
v_fma_f32 v112,v112,v103,v102
s_add_u32 s56,s55,768
D 17,56,3072
v_fma_f32 v113,v113,v103,v102
v_add_f32 v26,v26,v106
v_add_f32 v27,v27,v107
v_add_f32 v28,v28,v108
v_add_f32 v29,v29,v109
s_add_u32 s58,s58,s61
v_cndmask_b32 v17,v10,v9,s[40:41]
s_add_u32 m0,s57,16384
s_add_u32 s56,s55,20480
D 17,56,0
v_add_f32 v30,v30,v110
v_add_f32 v31,v31,v111
v_add_f32 v32,v32,v112
v_add_f32 v33,v33,v113
v_cvt_pk_bf16_f32 v114,v26,v27
s_add_u32 s56,s55,20736
D 17,56,1024
v_cvt_pk_bf16_f32 v115,v28,v29
v_cvt_pk_bf16_f32 v116,v30,v31
v_cvt_pk_bf16_f32 v117,v32,v33
s_add_u32 s56,s55,20992
D 17,56,2048
s_add_u32 s56,s55,21248
D 17,56,3072
buffer_store_dwordx4 v[114:117],v0,s[36:39],s67 offen offset:0 sc0 sc1
v_add_f32 v106,v26,v27
v_fma_f32 v107,v26,v26,s87
s_add_u32 s55,s55,s60
s_sub_u32 s57,s84,s57
s_add_u32 s54,s54,s46
v_fma_f32 v107,v27,v27,v107
v_add_f32 v106,v106,v28
v_fma_f32 v107,v28,v28,v107
v_add_f32 v106,v106,v29
v_fma_f32 v107,v29,v29,v107
v_add_f32 v106,v106,v30
v_fma_f32 v107,v30,v30,v107
v_add_f32 v106,v106,v31
v_fma_f32 v107,v31,v31,v107
v_add_f32 v106,v106,v32
v_fma_f32 v107,v32,v32,v107
v_add_f32 v106,v106,v33
v_fma_f32 v107,v33,v33,v107
s_nop 1
v_permlane16_swap_b32 v106,v107
v_add_f32 v106,v106,v107
v_mov_b32 v107,v106
s_nop 1
v_permlane32_swap_b32 v106,v107
v_add_f32 v106,v106,v107
ds_write_b32 v12,v106 offset:2048
L 0
s_barrier
R 118,13,2048
L 0
v_add_f32 v118,v118,v119
v_add_f32 v120,v120,v121
v_add_f32 v118,v118,v120
v_mul_f32 v118,s86,v118
buffer_store_dword v118,v14,s[36:39],s77 offen offset:0 sc0 sc1
s_mov_b32 s72,s69
s_mov_b32 s69,0x7fff0000
s_mov_b32 s67,0x7fff0000
s_mov_b32 s77,0x7fff0000
s_branch Lg2_gox_%=
Lg2_xn1_%=:
s_cmp_ge_u32 s54,256
s_cbranch_scc1 Lg2_fin1_%=
s_add_u32 m0,s59,4096
s_nop 0
buffer_load_dword v8,s[36:39],s58 offen offset:0 sc1 lds
buffer_store_dword v4,v3,s[36:39],s71 offen offset:0 sc0 sc1
s_add_u32 m0,s59,5120
s_nop 0
buffer_load_dword v8,s[36:39],s58 offen offset:0 sc1 lds
buffer_store_dword v4,v3,s[36:39],s71 offen offset:0 sc0 sc1
s_mov_b32 s83,0
v_lshlrev_b32 v106,16,v126
v_and_b32 v107,0xffff0000,v126
v_lshlrev_b32 v108,16,v127
v_and_b32 v109,0xffff0000,v127
v_lshlrev_b32 v110,16,v128
v_and_b32 v111,0xffff0000,v128
v_lshlrev_b32 v112,16,v129
v_and_b32 v113,0xffff0000,v129
v_fma_f32 v106,v106,v103,v102
v_fma_f32 v107,v107,v103,v102
v_fma_f32 v108,v108,v103,v102
v_fma_f32 v109,v109,v103,v102
v_fma_f32 v110,v110,v103,v102
v_fma_f32 v111,v111,v103,v102
v_fma_f32 v112,v112,v103,v102
v_fma_f32 v113,v113,v103,v102
v_add_f32 v26,v26,v106
v_add_f32 v27,v27,v107
v_add_f32 v28,v28,v108
v_add_f32 v29,v29,v109
v_add_f32 v30,v30,v110
v_add_f32 v31,v31,v111
v_add_f32 v32,v32,v112
v_add_f32 v33,v33,v113
v_cvt_pk_bf16_f32 v114,v26,v27
v_cvt_pk_bf16_f32 v115,v28,v29
v_cvt_pk_bf16_f32 v116,v30,v31
v_cvt_pk_bf16_f32 v117,v32,v33
buffer_store_dwordx4 v[114:117],v0,s[36:39],s67 offen offset:0 sc0 sc1
v_add_f32 v106,v26,v27
v_fma_f32 v107,v26,v26,s87
v_fma_f32 v107,v27,v27,v107
v_add_f32 v106,v106,v28
v_fma_f32 v107,v28,v28,v107
v_add_f32 v106,v106,v29
v_fma_f32 v107,v29,v29,v107
v_add_f32 v106,v106,v30
v_fma_f32 v107,v30,v30,v107
v_add_f32 v106,v106,v31
v_fma_f32 v107,v31,v31,v107
v_add_f32 v106,v106,v32
v_fma_f32 v107,v32,v32,v107
v_add_f32 v106,v106,v33
v_fma_f32 v107,v33,v33,v107
s_nop 1
v_permlane16_swap_b32 v106,v107
v_add_f32 v106,v106,v107
v_mov_b32 v107,v106
s_nop 1
v_permlane32_swap_b32 v106,v107
v_add_f32 v106,v106,v107
ds_write_b32 v12,v106 offset:2048
L 0
s_barrier
R 118,13,2048
L 0
v_add_f32 v118,v118,v119
v_add_f32 v120,v120,v121
v_add_f32 v118,v118,v120
v_mul_f32 v118,s86,v118
buffer_store_dword v118,v14,s[36:39],s77 offen offset:0 sc0 sc1
s_mov_b32 s72,s69
s_mov_b32 s69,0x7fff0000
s_mov_b32 s67,0x7fff0000
s_mov_b32 s77,0x7fff0000
s_branch Lg2_poll_%=
Lg2_fin1_%=:
v_lshlrev_b32 v106,16,v126
v_and_b32 v107,0xffff0000,v126
v_lshlrev_b32 v108,16,v127
v_and_b32 v109,0xffff0000,v127
v_lshlrev_b32 v110,16,v128
v_and_b32 v111,0xffff0000,v128
v_lshlrev_b32 v112,16,v129
v_and_b32 v113,0xffff0000,v129
v_fma_f32 v106,v106,v103,v102
v_fma_f32 v107,v107,v103,v102
v_fma_f32 v108,v108,v103,v102
v_fma_f32 v109,v109,v103,v102
v_fma_f32 v110,v110,v103,v102
v_fma_f32 v111,v111,v103,v102
v_fma_f32 v112,v112,v103,v102
v_fma_f32 v113,v113,v103,v102
v_add_f32 v26,v26,v106
v_add_f32 v27,v27,v107
v_add_f32 v28,v28,v108
v_add_f32 v29,v29,v109
v_add_f32 v30,v30,v110
v_add_f32 v31,v31,v111
v_add_f32 v32,v32,v112
v_add_f32 v33,v33,v113
v_cvt_pk_bf16_f32 v114,v26,v27
v_cvt_pk_bf16_f32 v115,v28,v29
v_cvt_pk_bf16_f32 v116,v30,v31
v_cvt_pk_bf16_f32 v117,v32,v33
buffer_store_dwordx4 v[114:117],v0,s[36:39],s67 offen offset:0 sc0 sc1
v_add_f32 v106,v26,v27
v_fma_f32 v107,v26,v26,s87
v_fma_f32 v107,v27,v27,v107
v_add_f32 v106,v106,v28
v_fma_f32 v107,v28,v28,v107
v_add_f32 v106,v106,v29
v_fma_f32 v107,v29,v29,v107
v_add_f32 v106,v106,v30
v_fma_f32 v107,v30,v30,v107
v_add_f32 v106,v106,v31
v_fma_f32 v107,v31,v31,v107
v_add_f32 v106,v106,v32
v_fma_f32 v107,v32,v32,v107
v_add_f32 v106,v106,v33
v_fma_f32 v107,v33,v33,v107
s_nop 1
v_permlane16_swap_b32 v106,v107
v_add_f32 v106,v106,v107
v_mov_b32 v107,v106
s_nop 1
v_permlane32_swap_b32 v106,v107
v_add_f32 v106,v106,v107
ds_write_b32 v12,v106 offset:2048
L 0
s_barrier
R 118,13,2048
L 0
v_add_f32 v118,v118,v119
v_add_f32 v120,v120,v121
v_add_f32 v118,v118,v120
v_mul_f32 v118,s86,v118
buffer_store_dword v118,v14,s[36:39],s77 offen offset:0 sc0 sc1
s_mov_b32 s72,s69
s_mov_b32 s69,0x7fff0000
s_mov_b32 s67,0x7fff0000
s_mov_b32 s77,0x7fff0000
s_branch Lg2_done_%=
Lg2_done_%=:
s_waitcnt vmcnt(2)
s_barrier
buffer_store_dword v4,v3,s[36:39],s70 offen offset:0 sc0 sc1
s_waitcnt vmcnt(1)
s_barrier
buffer_store_dword v4,v3,s[36:39],s72 offen offset:0 sc0 sc1
s_waitcnt vmcnt(0) lgkmcnt(0)
s_barrier
Lg2_end_%=:
s_mov_b32 %0,s53
Lcode_g12_e:
.purgem M
.purgem R
.purgem T
.purgem D
.purgem L
)"
  : "=&s"(epOut) : "s"(ws), "s"(__builtin_amdgcn_readfirstlane(sWave)), "s"(__builtin_amdgcn_readfirstlane(sG0)), "s"(__builtin_amdgcn_readfirstlane(sStep)), "s"(__builtin_amdgcn_readfirstlane(sOutOff)), "s"(__builtin_amdgcn_readfirstlane(sFoutOff)), "s"(__builtin_amdgcn_readfirstlane(sAuxOff)), "s"(__builtin_amdgcn_readfirstlane(sWw)), "s"(__builtin_amdgcn_readfirstlane(sBuf)), "s"(__builtin_amdgcn_readfirstlane(sExp)), "v"(vL16), "v"(vFlag), "v"(vSnapOff), "v"(vSnapRd), "v"(vWd0), "v"(vWd1), "v"(vWr0), "v"(vWr1), "v"(vSp), "v"(vOutS), "v"(vAux0), "v"(vAux1), "v"(vAux2), "v"(vAux3), "v"(vAn), "v"(vAe), "s"(w12) : "v0", "v1", "v2", "v3", "v4", "v5", "v6", "v7", "v8", "v9", "v10", "v11", "v12", "v13", "v14", "v15", "v16", "v17", "v18", "v19", "v20", "v21", "v22", "v23", "v24", "v25", "v26", "v27", "v28", "v29", "v30", "v31", "v32", "v33", "v34", "v35", "v36", "v37", "v38", "v39", "v40", "v41", "v42", "v43", "v44", "v45", "v46", "v47", "v48", "v49", "v50", "v51", "v52", "v53", "v54", "v55", "v56", "v57", "v58", "v59", "v60", "v61", "v62", "v63", "v64", "v65", "v66", "v67", "v68", "v69", "v70", "v71", "v72", "v73", "v74", "v75", "v76", "v77", "v78", "v79", "v80", "v81", "v82", "v83", "v84", "v85", "v86", "v87", "v88", "v89", "v90", "v91", "v92", "v93", "v94", "v95", "v96", "v97", "v98", "v99", "v100", "v101", "v102", "v103", "v104", "v105", "v106", "v107", "v108", "v109", "v110", "v111", "v112", "v113", "v114", "v115", "v116", "v117", "v118", "v119", "v120", "v121", "v122", "v123", "v124", "v125", "v126", "v127", "v128", "v129", "v130", "v131", "v132", "v133", "v134", "v135", "v136", "v137", "v138", "v139", "v140", "v141", "v142", "v143", "v144", "v145", "v146", "v147", "v148", "v149", "v150", "v151", "v152", "v153", "v154", "v155", "v156", "v157", "v158", "v159", "v160", "v161", "v162", "v163", "v164", "v165", "v166", "v167", "v168", "v169", "v170", "v171", "v172", "v173", "v174", "v175", "v176", "v177", "v178", "v179", "v180", "v181", "v182", "v183", "v184", "v185", "v186", "v187", "v188", "v189", "v190", "v191", "v192", "v193", "v194", "v195", "v196", "v197", "v198", "v199", "v200", "v201", "v202", "v203", "v204", "v205", "v206", "v207", "v208", "v209", "v210", "v211", "v212", "v213", "v214", "v215", "v216", "v217", "v218", "v219", "v220", "v221", "v222", "v223", "v224", "v225", "v226", "v227", "v228", "v229", "v230", "v231", "a0", "a1", "a2", "a3", "a4", "a5", "a6", "a7", "a8", "a9", "a10", "a11", "a12", "a13", "a14", "a15", "a16", "a17", "a18", "a19", "a20", "a21", "a22", "a23", "a24", "a25", "a26", "a27", "a28", "a29", "a30", "a31", "a32", "a33", "a34", "a35", "a36", "a37", "a38", "a39", "a40", "a41", "a42", "a43", "a44", "a45", "a46", "a47", "a48", "a49", "a50", "a51", "a52", "a53", "a54", "a55", "a56", "a57", "a58", "a59", "a60", "a61", "a62", "a63", "a64", "a65", "a66", "a67", "a68", "a69", "a70", "a71", "a72", "a73", "a74", "a75", "a76", "a77", "a78", "a79", "a80", "a81", "a82", "a83", "a84", "a85", "a86", "a87", "a88", "a89", "a90", "a91", "a92", "a93", "a94", "a95", "a96", "a97", "a98", "a99", "a100", "a101", "a102", "a103", "a104", "a105", "a106", "a107", "a108", "a109", "a110", "a111", "a112", "a113", "a114", "a115", "a116", "a117", "a118", "a119", "a120", "a121", "a122", "a123", "a124", "a125", "a126", "a127", "a128", "a129", "a130", "a131", "a132", "a133", "a134", "a135", "a136", "a137", "a138", "a139", "a140", "a141", "a142", "a143", "a144", "a145", "a146", "a147", "a148", "a149", "a150", "a151", "a152", "a153", "a154", "a155", "a156", "a157", "a158", "a159", "a160", "a161", "a162", "a163", "a164", "a165", "a166", "a167", "a168", "a169", "a170", "a171", "a172", "a173", "a174", "a175", "a176", "a177", "a178", "a179", "a180", "a181", "a182", "a183", "a184", "a185", "a186", "a187", "a188", "a189", "a190", "a191", "a192", "a193", "a194", "a195", "a196", "a197", "a198", "a199", "a200", "a201", "a202", "a203", "a204", "a205", "a206", "a207", "a208", "a209", "a210", "a211", "a212", "a213", "a214", "a215", "a216", "a217", "a218", "a219", "a220", "a221", "a222", "a223", "a224", "a225", "a226", "a227", "a228", "a229", "a230", "a231", "a232", "a233", "a234", "a235", "a236", "a237", "a238", "a239", "a240", "a241", "a242", "a243", "a244", "a245", "a246", "a247", "a248", "a249", "a250", "a251", "a252", "a253", "a254", "a255", "s36", "s37", "s38", "s39", "s40", "s41", "s42", "s43", "s44", "s45", "s46", "s47", "s48", "s49", "s50", "s51", "s52", "s53", "s54", "s55", "s56", "s57", "s58", "s59", "s60", "s61", "s62", "s63", "s64", "s65", "s66", "s67", "s68", "s69", "s70", "s71", "s72", "s73", "s74", "s75", "s76", "s77", "s78", "s79", "s80", "s81", "s82", "s83", "s84", "s85", "s86", "s87", "s88", "s89", "s90", "s91", "s92", "s93", "s94", "s95", "s96", "s97", "vcc", "scc", "memory");
    ep = epOut;
    if (kind == 1 && 5 + RHELP + (rep >> 1) < NR4) { kind = 4; kh = rep & 1; rep = 5 + RHELP + (rep >> 1); }
  }
  if (kind == 4) {
    const unsigned sStep = rep < 5 ? 5 : rep == 5 ? 6 : NR4;
    COMMON(10, (kh * 9 + fj) * 128, 2560, 143360, 2048, 448, 0);
    const int cs = slice;
    const unsigned sOutOff = (kh ? OFF_P1 : OFF_P0) + (cs * 4 + wave) * 1024, sFoutOff = OFF_FP + (kh * 8 + cs) * 128;
    const unsigned sAuxOff = (kh ? OFF_XP : OFF_X1) + (cs * 4 + wave) * 1024, sWw = (kh * 1792 + 16 * wave) * 2048 + cs * 256, sBuf = kh * 57344;
    const int zu = 108 + (rep + 2) % 5;
    const unsigned vSp = lg * 2048 + l15 * 4, vSn = rep >= 5 ? OOB : !ALLZ ? (zu * 16 + wave * 4 + lg) * 2048 + (cs * 2 + kh) * 128 + l15 * 8 :
                         ((rep + 5 * (wave >> 1)) * 16 + (wave & 1) * 8 + (ln >> 3)) * 2048 + (cs * 2 + kh) * 128 + (ln & 7) * 16;
    (void)vWr2;
    const unsigned sExp = __builtin_amdgcn_readfirstlane(ep);
    asm volatile(
R"(.macro M d,w,x
v_mfma_f32_16x16x32_bf16 v[\d:\d+3], \w, v[\x:\x+3], v[\d:\d+3]
.endm
.macro R d,a,o
ds_read_b128 v[\d:\d+3], v\a offset:\o
.endm
.macro T d,a,o
ds_read_b64_tr_b16 \d, v\a offset:\o
.endm
.macro D a,s,o
buffer_load_dwordx4 v\a, s[36:39], s\s offen offset:\o sc1 lds
.endm
.macro L c
s_waitcnt lgkmcnt(\c)
.endm
Lcode_g4_b:
s_mov_b64 s[36:37],%0
s_mov_b32 s38,111243392
s_mov_b32 s39,0x00020000
s_mov_b32 s44,%1
s_mov_b32 s45,%2
s_mov_b32 s46,%3
s_mov_b32 s47,%4
s_mov_b32 s48,%5
s_mov_b32 s49,%6
s_mov_b32 s50,%7
s_mov_b32 s51,%8
s_mov_b32 s93,%9
s_mov_b32 s53,%10
v_mov_b32 v0,%11
v_mov_b32 v3,%12
v_mov_b32 v5,%13
v_mov_b32 v6,%14
v_mov_b32 v10,%15
v_mov_b32 v11,%16
v_mov_b32 v12,%17
v_mov_b32 v13,%18
v_mov_b32 v9,%19
v_mov_b32 v7,%20
s_mov_b64 s[40:41],%21
s_mov_b32 s42,7340032
s_mov_b32 s43,0x00020000
s_lshl_b32 s57,s44,11
s_cmp_eq_u32 s45,175
s_cbranch_scc1 Lg4_pre_%=
s_add_u32 s55,s50,0
s_add_u32 m0,s57,40960
s_nop 0
buffer_load_dwordx4 v10,s[40:43],s55 offen offset:0  lds
s_add_u32 m0,s57,41984
s_nop 0
buffer_load_dwordx4 v11,s[40:43],s55 offen offset:0  lds
s_add_u32 m0,s57,49024
s_nop 0
buffer_load_dwordx4 v10,s[40:43],s55 offen offset:128  lds
s_add_u32 m0,s57,50048
s_nop 0
buffer_load_dwordx4 v11,s[40:43],s55 offen offset:128  lds
s_add_u32 s55,s50,131072
s_add_u32 m0,s57,57344
s_nop 0
buffer_load_dwordx4 v10,s[40:43],s55 offen offset:0  lds
s_add_u32 m0,s57,58368
s_nop 0
buffer_load_dwordx4 v11,s[40:43],s55 offen offset:0  lds
s_add_u32 m0,s57,65408
s_nop 0
buffer_load_dwordx4 v10,s[40:43],s55 offen offset:128  lds
s_add_u32 m0,s57,66432
s_nop 0
buffer_load_dwordx4 v11,s[40:43],s55 offen offset:128  lds
s_add_u32 s55,s50,262144
s_add_u32 m0,s57,73728
s_nop 0
buffer_load_dwordx4 v10,s[40:43],s55 offen offset:0  lds
s_add_u32 m0,s57,74752
s_nop 0
buffer_load_dwordx4 v11,s[40:43],s55 offen offset:0  lds
s_add_u32 m0,s57,81792
s_nop 0
buffer_load_dwordx4 v10,s[40:43],s55 offen offset:128  lds
s_add_u32 m0,s57,82816
s_nop 0
buffer_load_dwordx4 v11,s[40:43],s55 offen offset:128  lds
s_add_u32 s55,s50,393216
s_add_u32 m0,s57,90112
s_nop 0
buffer_load_dwordx4 v10,s[40:43],s55 offen offset:0  lds
s_add_u32 m0,s57,91136
s_nop 0
buffer_load_dwordx4 v11,s[40:43],s55 offen offset:0  lds
s_add_u32 m0,s57,98176
s_nop 0
buffer_load_dwordx4 v10,s[40:43],s55 offen offset:128  lds
s_add_u32 m0,s57,99200
s_nop 0
buffer_load_dwordx4 v11,s[40:43],s55 offen offset:128  lds
s_add_u32 s55,s50,524288
s_add_u32 m0,s57,106496
s_nop 0
buffer_load_dwordx4 v10,s[40:43],s55 offen offset:0  lds
s_add_u32 m0,s57,107520
s_nop 0
buffer_load_dwordx4 v11,s[40:43],s55 offen offset:0  lds
s_add_u32 m0,s57,114560
s_nop 0
buffer_load_dwordx4 v10,s[40:43],s55 offen offset:128  lds
s_add_u32 m0,s57,115584
s_nop 0
buffer_load_dwordx4 v11,s[40:43],s55 offen offset:128  lds
s_add_u32 s55,s50,655360
s_add_u32 m0,s57,122880
s_nop 0
buffer_load_dwordx4 v10,s[40:43],s55 offen offset:0  lds
s_add_u32 m0,s57,123904
s_nop 0
buffer_load_dwordx4 v11,s[40:43],s55 offen offset:0  lds
s_add_u32 m0,s57,130944
s_nop 0
buffer_load_dwordx4 v10,s[40:43],s55 offen offset:128  lds
s_add_u32 m0,s57,131968
s_nop 0
buffer_load_dwordx4 v11,s[40:43],s55 offen offset:128  lds
s_waitcnt vmcnt(20) lgkmcnt(0)
s_barrier
s_add_u32 s55,s50,786432
s_add_u32 m0,s57,139264
s_nop 0
buffer_load_dwordx4 v10,s[40:43],s55 offen offset:0  lds
s_add_u32 m0,s57,140288
s_nop 0
buffer_load_dwordx4 v11,s[40:43],s55 offen offset:0  lds
s_add_u32 m0,s57,147328
s_nop 0
buffer_load_dwordx4 v10,s[40:43],s55 offen offset:128  lds
s_add_u32 m0,s57,148352
s_nop 0
buffer_load_dwordx4 v11,s[40:43],s55 offen offset:128  lds
v_add_u32 v18,40960,v12
v_add_u32 v19,40960,v13
T a[0:1],18,0
T a[2:3],18,512
T a[4:5],19,0
T a[6:7],19,512
T a[8:9],18,4096
T a[10:11],18,4608
T a[12:13],19,4096
T a[14:15],19,4608
s_waitcnt vmcnt(20) lgkmcnt(0)
s_barrier
s_add_u32 s55,s50,917504
s_add_u32 m0,s57,40960
s_nop 0
buffer_load_dwordx4 v10,s[40:43],s55 offen offset:0  lds
s_add_u32 m0,s57,41984
s_nop 0
buffer_load_dwordx4 v11,s[40:43],s55 offen offset:0  lds
s_add_u32 m0,s57,49024
s_nop 0
buffer_load_dwordx4 v10,s[40:43],s55 offen offset:128  lds
s_add_u32 m0,s57,50048
s_nop 0
buffer_load_dwordx4 v11,s[40:43],s55 offen offset:128  lds
v_add_u32 v18,57344,v12
v_add_u32 v19,57344,v13
T a[16:17],18,0
T a[18:19],18,512
T a[20:21],19,0
T a[22:23],19,512
T a[24:25],18,4096
T a[26:27],18,4608
T a[28:29],19,4096
T a[30:31],19,4608
s_waitcnt vmcnt(20) lgkmcnt(0)
s_barrier
s_add_u32 s55,s50,1048576
s_add_u32 m0,s57,57344
s_nop 0
buffer_load_dwordx4 v10,s[40:43],s55 offen offset:0  lds
s_add_u32 m0,s57,58368
s_nop 0
buffer_load_dwordx4 v11,s[40:43],s55 offen offset:0  lds
s_add_u32 m0,s57,65408
s_nop 0
buffer_load_dwordx4 v10,s[40:43],s55 offen offset:128  lds
s_add_u32 m0,s57,66432
s_nop 0
buffer_load_dwordx4 v11,s[40:43],s55 offen offset:128  lds
v_add_u32 v18,73728,v12
v_add_u32 v19,73728,v13
T a[32:33],18,0
T a[34:35],18,512
T a[36:37],19,0
T a[38:39],19,512
T a[40:41],18,4096
T a[42:43],18,4608
T a[44:45],19,4096
T a[46:47],19,4608
s_waitcnt vmcnt(20) lgkmcnt(0)
s_barrier
s_add_u32 s55,s50,1179648
s_add_u32 m0,s57,73728
s_nop 0
buffer_load_dwordx4 v10,s[40:43],s55 offen offset:0  lds
s_add_u32 m0,s57,74752
s_nop 0
buffer_load_dwordx4 v11,s[40:43],s55 offen offset:0  lds
s_add_u32 m0,s57,81792
s_nop 0
buffer_load_dwordx4 v10,s[40:43],s55 offen offset:128  lds
s_add_u32 m0,s57,82816
s_nop 0
buffer_load_dwordx4 v11,s[40:43],s55 offen offset:128  lds
v_add_u32 v18,90112,v12
v_add_u32 v19,90112,v13
T a[48:49],18,0
T a[50:51],18,512
T a[52:53],19,0
T a[54:55],19,512
T a[56:57],18,4096
T a[58:59],18,4608
T a[60:61],19,4096
T a[62:63],19,4608
s_waitcnt vmcnt(20) lgkmcnt(0)
s_barrier
s_add_u32 s55,s50,1310720
s_add_u32 m0,s57,90112
s_nop 0
buffer_load_dwordx4 v10,s[40:43],s55 offen offset:0  lds
s_add_u32 m0,s57,91136
s_nop 0
buffer_load_dwordx4 v11,s[40:43],s55 offen offset:0  lds
s_add_u32 m0,s57,98176
s_nop 0
buffer_load_dwordx4 v10,s[40:43],s55 offen offset:128  lds
s_add_u32 m0,s57,99200
s_nop 0
buffer_load_dwordx4 v11,s[40:43],s55 offen offset:128  lds
v_add_u32 v18,106496,v12
v_add_u32 v19,106496,v13
T a[64:65],18,0
T a[66:67],18,512
T a[68:69],19,0
T a[70:71],19,512
T a[72:73],18,4096
T a[74:75],18,4608
T a[76:77],19,4096
T a[78:79],19,4608
s_waitcnt vmcnt(20) lgkmcnt(0)
s_barrier
s_add_u32 s55,s50,1441792
s_add_u32 m0,s57,106496
s_nop 0
buffer_load_dwordx4 v10,s[40:43],s55 offen offset:0  lds
s_add_u32 m0,s57,107520
s_nop 0
buffer_load_dwordx4 v11,s[40:43],s55 offen offset:0  lds
s_add_u32 m0,s57,114560
s_nop 0
buffer_load_dwordx4 v10,s[40:43],s55 offen offset:128  lds
s_add_u32 m0,s57,115584
s_nop 0
buffer_load_dwordx4 v11,s[40:43],s55 offen offset:128  lds
v_add_u32 v18,122880,v12
v_add_u32 v19,122880,v13
T a[80:81],18,0
T a[82:83],18,512
T a[84:85],19,0
T a[86:87],19,512
T a[88:89],18,4096
T a[90:91],18,4608
T a[92:93],19,4096
T a[94:95],19,4608
s_waitcnt vmcnt(20) lgkmcnt(0)
s_barrier
s_add_u32 s55,s50,1572864
s_add_u32 m0,s57,122880
s_nop 0
buffer_load_dwordx4 v10,s[40:43],s55 offen offset:0  lds
s_add_u32 m0,s57,123904
s_nop 0
buffer_load_dwordx4 v11,s[40:43],s55 offen offset:0  lds
s_add_u32 m0,s57,130944
s_nop 0
buffer_load_dwordx4 v10,s[40:43],s55 offen offset:128  lds
s_add_u32 m0,s57,131968
s_nop 0
buffer_load_dwordx4 v11,s[40:43],s55 offen offset:128  lds
v_add_u32 v18,139264,v12
v_add_u32 v19,139264,v13
T a[96:97],18,0
T a[98:99],18,512
T a[100:101],19,0
T a[102:103],19,512
T a[104:105],18,4096
T a[106:107],18,4608
T a[108:109],19,4096
T a[110:111],19,4608
s_waitcnt vmcnt(20) lgkmcnt(0)
s_barrier
s_add_u32 s55,s50,1703936
s_add_u32 m0,s57,139264
s_nop 0
buffer_load_dwordx4 v10,s[40:43],s55 offen offset:0  lds
s_add_u32 m0,s57,140288
s_nop 0
buffer_load_dwordx4 v11,s[40:43],s55 offen offset:0  lds
s_add_u32 m0,s57,147328
s_nop 0
buffer_load_dwordx4 v10,s[40:43],s55 offen offset:128  lds
s_add_u32 m0,s57,148352
s_nop 0
buffer_load_dwordx4 v11,s[40:43],s55 offen offset:128  lds
v_add_u32 v18,40960,v12
v_add_u32 v19,40960,v13
T a[112:113],18,0
T a[114:115],18,512
T a[116:117],19,0
T a[118:119],19,512
T a[120:121],18,4096
T a[122:123],18,4608
T a[124:125],19,4096
T a[126:127],19,4608
s_waitcnt vmcnt(20) lgkmcnt(0)
s_barrier
s_add_u32 s55,s50,1835008
s_add_u32 m0,s57,40960
s_nop 0
buffer_load_dwordx4 v10,s[40:43],s55 offen offset:0  lds
s_add_u32 m0,s57,41984
s_nop 0
buffer_load_dwordx4 v11,s[40:43],s55 offen offset:0  lds
s_add_u32 m0,s57,49024
s_nop 0
buffer_load_dwordx4 v10,s[40:43],s55 offen offset:128  lds
s_add_u32 m0,s57,50048
s_nop 0
buffer_load_dwordx4 v11,s[40:43],s55 offen offset:128  lds
v_add_u32 v18,57344,v12
v_add_u32 v19,57344,v13
T a[128:129],18,0
T a[130:131],18,512
T a[132:133],19,0
T a[134:135],19,512
T a[136:137],18,4096
T a[138:139],18,4608
T a[140:141],19,4096
T a[142:143],19,4608
s_waitcnt vmcnt(20) lgkmcnt(0)
s_barrier
s_add_u32 s55,s50,1966080
s_add_u32 m0,s57,57344
s_nop 0
buffer_load_dwordx4 v10,s[40:43],s55 offen offset:0  lds
s_add_u32 m0,s57,58368
s_nop 0
buffer_load_dwordx4 v11,s[40:43],s55 offen offset:0  lds
s_add_u32 m0,s57,65408
s_nop 0
buffer_load_dwordx4 v10,s[40:43],s55 offen offset:128  lds
s_add_u32 m0,s57,66432
s_nop 0
buffer_load_dwordx4 v11,s[40:43],s55 offen offset:128  lds
v_add_u32 v18,73728,v12
v_add_u32 v19,73728,v13
T a[144:145],18,0
T a[146:147],18,512
T a[148:149],19,0
T a[150:151],19,512
T a[152:153],18,4096
T a[154:155],18,4608
T a[156:157],19,4096
T a[158:159],19,4608
s_waitcnt vmcnt(20) lgkmcnt(0)
s_barrier
s_add_u32 s55,s50,2097152
s_add_u32 m0,s57,73728
s_nop 0
buffer_load_dwordx4 v10,s[40:43],s55 offen offset:0  lds
s_add_u32 m0,s57,74752
s_nop 0
buffer_load_dwordx4 v11,s[40:43],s55 offen offset:0  lds
s_add_u32 m0,s57,81792
s_nop 0
buffer_load_dwordx4 v10,s[40:43],s55 offen offset:128  lds
s_add_u32 m0,s57,82816
s_nop 0
buffer_load_dwordx4 v11,s[40:43],s55 offen offset:128  lds
v_add_u32 v18,90112,v12
v_add_u32 v19,90112,v13
T a[160:161],18,0
T a[162:163],18,512
T a[164:165],19,0
T a[166:167],19,512
T a[168:169],18,4096
T a[170:171],18,4608
T a[172:173],19,4096
T a[174:175],19,4608
s_waitcnt vmcnt(20) lgkmcnt(0)
s_barrier
s_add_u32 s55,s50,2228224
s_add_u32 m0,s57,90112
s_nop 0
buffer_load_dwordx4 v10,s[40:43],s55 offen offset:0  lds
s_add_u32 m0,s57,91136
s_nop 0
buffer_load_dwordx4 v11,s[40:43],s55 offen offset:0  lds
s_add_u32 m0,s57,98176
s_nop 0
buffer_load_dwordx4 v10,s[40:43],s55 offen offset:128  lds
s_add_u32 m0,s57,99200
s_nop 0
buffer_load_dwordx4 v11,s[40:43],s55 offen offset:128  lds
v_add_u32 v18,106496,v12
v_add_u32 v19,106496,v13
T a[176:177],18,0
T a[178:179],18,512
T a[180:181],19,0
T a[182:183],19,512
T a[184:185],18,4096
T a[186:187],18,4608
T a[188:189],19,4096
T a[190:191],19,4608
s_waitcnt vmcnt(20) lgkmcnt(0)
s_barrier
s_add_u32 s55,s50,2359296
s_add_u32 m0,s57,106496
s_nop 0
buffer_load_dwordx4 v10,s[40:43],s55 offen offset:0  lds
s_add_u32 m0,s57,107520
s_nop 0
buffer_load_dwordx4 v11,s[40:43],s55 offen offset:0  lds
s_add_u32 m0,s57,114560
s_nop 0
buffer_load_dwordx4 v10,s[40:43],s55 offen offset:128  lds
s_add_u32 m0,s57,115584
s_nop 0
buffer_load_dwordx4 v11,s[40:43],s55 offen offset:128  lds
v_add_u32 v18,122880,v12
v_add_u32 v19,122880,v13
T a[192:193],18,0
T a[194:195],18,512
T a[196:197],19,0
T a[198:199],19,512
T a[200:201],18,4096
T a[202:203],18,4608
T a[204:205],19,4096
T a[206:207],19,4608
s_waitcnt vmcnt(20) lgkmcnt(0)
s_barrier
s_add_u32 s55,s50,2490368
s_add_u32 m0,s57,122880
s_nop 0
buffer_load_dwordx4 v10,s[40:43],s55 offen offset:0  lds
s_add_u32 m0,s57,123904
s_nop 0
buffer_load_dwordx4 v11,s[40:43],s55 offen offset:0  lds
s_add_u32 m0,s57,130944
s_nop 0
buffer_load_dwordx4 v10,s[40:43],s55 offen offset:128  lds
s_add_u32 m0,s57,131968
s_nop 0
buffer_load_dwordx4 v11,s[40:43],s55 offen offset:128  lds
v_add_u32 v18,139264,v12
v_add_u32 v19,139264,v13
T a[208:209],18,0
T a[210:211],18,512
T a[212:213],19,0
T a[214:215],19,512
T a[216:217],18,4096
T a[218:219],18,4608
T a[220:221],19,4096
T a[222:223],19,4608
s_waitcnt vmcnt(20) lgkmcnt(0)
s_barrier
s_add_u32 s55,s50,2621440
s_add_u32 m0,s57,139264
s_nop 0
buffer_load_dwordx4 v10,s[40:43],s55 offen offset:0  lds
s_add_u32 m0,s57,140288
s_nop 0
buffer_load_dwordx4 v11,s[40:43],s55 offen offset:0  lds
s_add_u32 m0,s57,147328
s_nop 0
buffer_load_dwordx4 v10,s[40:43],s55 offen offset:128  lds
s_add_u32 m0,s57,148352
s_nop 0
buffer_load_dwordx4 v11,s[40:43],s55 offen offset:128  lds
v_add_u32 v18,40960,v12
v_add_u32 v19,40960,v13
T a[224:225],18,0
T a[226:227],18,512
T a[228:229],19,0
T a[230:231],19,512
T a[232:233],18,4096
T a[234:235],18,4608
T a[236:237],19,4096
T a[238:239],19,4608
s_waitcnt vmcnt(20) lgkmcnt(0)
s_barrier
)"
R"(s_add_u32 s55,s50,2752512
s_add_u32 m0,s57,40960
s_nop 0
buffer_load_dwordx4 v10,s[40:43],s55 offen offset:0  lds
s_add_u32 m0,s57,41984
s_nop 0
buffer_load_dwordx4 v11,s[40:43],s55 offen offset:0  lds
s_add_u32 m0,s57,49024
s_nop 0
buffer_load_dwordx4 v10,s[40:43],s55 offen offset:128  lds
s_add_u32 m0,s57,50048
s_nop 0
buffer_load_dwordx4 v11,s[40:43],s55 offen offset:128  lds
v_add_u32 v18,57344,v12
v_add_u32 v19,57344,v13
T a[240:241],18,0
T a[242:243],18,512
T a[244:245],19,0
T a[246:247],19,512
T a[248:249],18,4096
T a[250:251],18,4608
T a[252:253],19,4096
T a[254:255],19,4608
Lg4_pre_%=:
s_waitcnt vmcnt(20) lgkmcnt(0)
s_barrier
s_add_u32 s55,s50,2883584
s_add_u32 m0,s57,57344
s_nop 0
buffer_load_dwordx4 v10,s[40:43],s55 offen offset:0  lds
s_add_u32 m0,s57,58368
s_nop 0
buffer_load_dwordx4 v11,s[40:43],s55 offen offset:0  lds
s_add_u32 m0,s57,65408
s_nop 0
buffer_load_dwordx4 v10,s[40:43],s55 offen offset:128  lds
s_add_u32 m0,s57,66432
s_nop 0
buffer_load_dwordx4 v11,s[40:43],s55 offen offset:128  lds
v_add_u32 v18,73728,v12
v_add_u32 v19,73728,v13
T v[64:65],18,0
T v[66:67],18,512
T v[68:69],19,0
T v[70:71],19,512
T v[72:73],18,4096
T v[74:75],18,4608
T v[76:77],19,4096
T v[78:79],19,4608
s_waitcnt vmcnt(20) lgkmcnt(0)
s_barrier
s_add_u32 s55,s50,3014656
s_add_u32 m0,s57,73728
s_nop 0
buffer_load_dwordx4 v10,s[40:43],s55 offen offset:0  lds
s_add_u32 m0,s57,74752
s_nop 0
buffer_load_dwordx4 v11,s[40:43],s55 offen offset:0  lds
s_add_u32 m0,s57,81792
s_nop 0
buffer_load_dwordx4 v10,s[40:43],s55 offen offset:128  lds
s_add_u32 m0,s57,82816
s_nop 0
buffer_load_dwordx4 v11,s[40:43],s55 offen offset:128  lds
v_add_u32 v18,90112,v12
v_add_u32 v19,90112,v13
T v[80:81],18,0
T v[82:83],18,512
T v[84:85],19,0
T v[86:87],19,512
T v[88:89],18,4096
T v[90:91],18,4608
T v[92:93],19,4096
T v[94:95],19,4608
s_waitcnt vmcnt(20) lgkmcnt(0)
s_barrier
s_add_u32 s55,s50,3145728
s_add_u32 m0,s57,90112
s_nop 0
buffer_load_dwordx4 v10,s[40:43],s55 offen offset:0  lds
s_add_u32 m0,s57,91136
s_nop 0
buffer_load_dwordx4 v11,s[40:43],s55 offen offset:0  lds
s_add_u32 m0,s57,98176
s_nop 0
buffer_load_dwordx4 v10,s[40:43],s55 offen offset:128  lds
s_add_u32 m0,s57,99200
s_nop 0
buffer_load_dwordx4 v11,s[40:43],s55 offen offset:128  lds
v_add_u32 v18,106496,v12
v_add_u32 v19,106496,v13
T v[96:97],18,0
T v[98:99],18,512
T v[100:101],19,0
T v[102:103],19,512
T v[104:105],18,4096
T v[106:107],18,4608
T v[108:109],19,4096
T v[110:111],19,4608
s_waitcnt vmcnt(20) lgkmcnt(0)
s_barrier
s_add_u32 s55,s50,3276800
s_add_u32 m0,s57,106496
s_nop 0
buffer_load_dwordx4 v10,s[40:43],s55 offen offset:0  lds
s_add_u32 m0,s57,107520
s_nop 0
buffer_load_dwordx4 v11,s[40:43],s55 offen offset:0  lds
s_add_u32 m0,s57,114560
s_nop 0
buffer_load_dwordx4 v10,s[40:43],s55 offen offset:128  lds
s_add_u32 m0,s57,115584
s_nop 0
buffer_load_dwordx4 v11,s[40:43],s55 offen offset:128  lds
v_add_u32 v18,122880,v12
v_add_u32 v19,122880,v13
T v[112:113],18,0
T v[114:115],18,512
T v[116:117],19,0
T v[118:119],19,512
T v[120:121],18,4096
T v[122:123],18,4608
T v[124:125],19,4096
T v[126:127],19,4608
s_waitcnt vmcnt(20) lgkmcnt(0)
s_barrier
s_add_u32 s55,s50,3407872
s_add_u32 m0,s57,122880
s_nop 0
buffer_load_dwordx4 v10,s[40:43],s55 offen offset:0  lds
s_add_u32 m0,s57,123904
s_nop 0
buffer_load_dwordx4 v11,s[40:43],s55 offen offset:0  lds
s_add_u32 m0,s57,130944
s_nop 0
buffer_load_dwordx4 v10,s[40:43],s55 offen offset:128  lds
s_add_u32 m0,s57,131968
s_nop 0
buffer_load_dwordx4 v11,s[40:43],s55 offen offset:128  lds
v_add_u32 v18,139264,v12
v_add_u32 v19,139264,v13
T v[128:129],18,0
T v[130:131],18,512
T v[132:133],19,0
T v[134:135],19,512
T v[136:137],18,4096
T v[138:139],18,4608
T v[140:141],19,4096
T v[142:143],19,4608
s_waitcnt vmcnt(20) lgkmcnt(0)
s_barrier
s_add_u32 s55,s50,3538944
s_add_u32 m0,s57,139264
s_nop 0
buffer_load_dwordx4 v10,s[40:43],s55 offen offset:0  lds
s_add_u32 m0,s57,140288
s_nop 0
buffer_load_dwordx4 v11,s[40:43],s55 offen offset:0  lds
s_add_u32 m0,s57,147328
s_nop 0
buffer_load_dwordx4 v10,s[40:43],s55 offen offset:128  lds
s_add_u32 m0,s57,148352
s_nop 0
buffer_load_dwordx4 v11,s[40:43],s55 offen offset:128  lds
v_add_u32 v18,40960,v12
v_add_u32 v19,40960,v13
T v[144:145],18,0
T v[146:147],18,512
T v[148:149],19,0
T v[150:151],19,512
T v[152:153],18,4096
T v[154:155],18,4608
T v[156:157],19,4096
T v[158:159],19,4608
s_waitcnt vmcnt(20) lgkmcnt(0)
s_barrier
v_add_u32 v18,57344,v12
v_add_u32 v19,57344,v13
T v[160:161],18,0
T v[162:163],18,512
T v[164:165],19,0
T v[166:167],19,512
T v[168:169],18,4096
T v[170:171],18,4608
T v[172:173],19,4096
T v[174:175],19,4608
s_waitcnt vmcnt(16) lgkmcnt(0)
s_barrier
v_add_u32 v18,73728,v12
v_add_u32 v19,73728,v13
T v[176:177],18,0
T v[178:179],18,512
T v[180:181],19,0
T v[182:183],19,512
T v[184:185],18,4096
T v[186:187],18,4608
T v[188:189],19,4096
T v[190:191],19,4608
s_waitcnt vmcnt(12) lgkmcnt(0)
s_barrier
v_add_u32 v18,90112,v12
v_add_u32 v19,90112,v13
T v[192:193],18,0
T v[194:195],18,512
T v[196:197],19,0
T v[198:199],19,512
T v[200:201],18,4096
T v[202:203],18,4608
T v[204:205],19,4096
T v[206:207],19,4608
s_waitcnt vmcnt(8) lgkmcnt(0)
s_barrier
v_add_u32 v18,106496,v12
v_add_u32 v19,106496,v13
T v[208:209],18,0
T v[210:211],18,512
T v[212:213],19,0
T v[214:215],19,512
T v[216:217],18,4096
T v[218:219],18,4608
T v[220:221],19,4096
T v[222:223],19,4608
s_waitcnt vmcnt(4) lgkmcnt(0)
s_barrier
v_add_u32 v18,122880,v12
v_add_u32 v19,122880,v13
T v[224:225],18,0
T v[226:227],18,512
T v[228:229],19,0
T v[230:231],19,512
T v[232:233],18,4096
T v[234:235],18,4608
T v[236:237],19,4096
T v[238:239],19,4608
s_waitcnt vmcnt(0) lgkmcnt(0)
s_barrier
v_add_u32 v18,139264,v12
v_add_u32 v19,139264,v13
T v[240:241],18,0
T v[242:243],18,512
T v[244:245],19,0
T v[246:247],19,512
T v[248:249],18,4096
T v[250:251],18,4608
T v[252:253],19,4096
T v[254:255],19,4608
L 0
s_barrier
v_mov_b32 v4,s53
s_mov_b32 s82,0
s_mov_b32 s80,0
s_mov_b32 s54,s45
s_mul_i32 s57,s44,7168
s_mul_i32 s55,s45,114688
s_add_u32 s55,s55,s57
s_add_u32 s55,s55,40894464
s_mul_i32 s57,s44,7168
s_mul_i32 s60,s46,114688
s_mul_i32 s61,s46,2560
s_mul_i32 s62,s46,32768
s_mul_i32 s63,s46,2048
s_mul_i32 s64,s46,512
s_lshl_b32 s59,s44,8
s_add_u32 s59,s59,143360
s_mul_i32 s58,s45,2560
s_add_u32 s58,s58,109891584
s_mul_i32 s65,s45,32768
s_add_u32 s65,s65,s47
s_mul_i32 s68,s45,2048
s_add_u32 s68,s68,s48
s_mul_i32 s73,s45,512
s_add_u32 s73,s73,106954752
s_mul_i32 s74,s45,32768
s_add_u32 s74,s74,s49
s_mul_i32 s75,s45,1024
s_add_u32 s75,s75,107479040
s_mov_b32 s66,0x7fff0000
s_mov_b32 s67,0x7fff0000
s_mov_b32 s69,0x7fff0000
s_mov_b32 s76,0x7fff0000
s_mov_b32 s77,0x7fff0000
s_mov_b32 s70,0x7fff0000
s_mov_b32 s71,0x7fff0000
s_mov_b32 s72,0x7fff0000
v_mov_b32 v1,v0
v_add_u32 v2,28672,v0
s_mov_b32 s84,28672
s_mov_b32 s86,0x3a800000
s_mov_b32 s87,0x3727c5ac
s_mov_b32 s88,0xc0135761
s_mov_b32 s89,0x3d372713
s_lshr_b32 s85,s44,1
s_mul_i32 s85,s85,127
s_add_u32 s55,s55,s51
s_mul_i32 s78,s45,114688
s_sub_u32 s60,s55,s78
s_add_u32 s50,s93,224
s_add_u32 s93,s93,170
s_cmp_lt_u32 s45,224
s_cselect_b32 s63,224,-1
s_cmp_lt_u32 s45,170
s_cselect_b32 s63,170,s63
s_cselect_b32 s93,s93,s50
s_cselect_b32 s88,6,8
s_cselect_b32 s89,224,-1
v_lshlrev_b32 v8,22,v0
v_and_b32 v8,0x80000000,v8
v_or_b32 v8,v8,v5
v_add_u32 v2,57344,v0
s_lshl_b32 s84,s57,1
s_add_u32 s84,s84,57344
s_mov_b64 s[86:87],%22
s_mov_b64 s[40:41],%22
s_mov_b32 s42,4194304
s_and_b32 s78,s47,65535
s_lshr_b32 s78,s78,4
s_sub_u32 s85,s47,s78
s_add_u32 s85,s85,4194304
s_add_u32 s45,s47,5832704
s_mul_i32 s73,s44,1280
s_add_u32 s73,s73,114688
v_lshrrev_b32 v62,4,v9
v_and_b32 v62,3,v62
v_mul_u32_u24 v62,288,v62
v_lshrrev_b32 v63,7,v9
v_and_b32 v5,12,v9
v_add3_u32 v5,v5,v62,v63
v_add_u32 v5,s73,v5
v_mov_b32 v10,0
v_mov_b32 v11,0
v_mov_b32 v12,0
v_mov_b32 v13,0
s_mov_b32 s79,1638400
buffer_store_dwordx4 v[10:13],v7,s[40:43],s79 offen offset:0 sc0 sc1
s_mov_b32 s79,1966080
buffer_store_dwordx4 v[10:13],v7,s[40:43],s79 offen offset:0 sc0 sc1
s_mov_b32 s79,2293760
buffer_store_dwordx4 v[10:13],v7,s[40:43],s79 offen offset:0 sc0 sc1
s_mov_b32 s79,2621440
buffer_store_dwordx4 v[10:13],v7,s[40:43],s79 offen offset:0 sc0 sc1
s_mov_b32 s79,2949120
buffer_store_dwordx4 v[10:13],v7,s[40:43],s79 offen offset:0 sc0 sc1
s_mov_b32 s79,3276800
buffer_store_dwordx4 v[10:13],v7,s[40:43],s79 offen offset:0 sc0 sc1
s_mov_b32 s79,3604480
buffer_store_dwordx4 v[10:13],v7,s[40:43],s79 offen offset:0 sc0 sc1
s_mov_b32 s79,3932160
buffer_store_dwordx4 v[10:13],v7,s[40:43],s79 offen offset:0 sc0 sc1
s_getpc_b64 s[40:41]
Lg4_pc_%=:
s_lshl_b32 s78,s44,13
v_lshlrev_b32 v62,3,v0
v_add_u32 v62,s78,v62
v_min_u32 v62,Lcode_g4_e-Lg4_pc_%=-4,v62
s_mov_b32 m0,163584
s_nop 0
global_load_lds_dword v62,s[40:41]
s_add_u32 m0,s59,4096
s_nop 0
buffer_load_dword v8,s[36:39],s58 offen offset:0 sc1 lds
buffer_store_dword v4,v3,s[36:39],s71 offen offset:0 sc0 sc1
s_add_u32 m0,s59,5120
s_nop 0
buffer_load_dword v8,s[36:39],s58 offen offset:0 sc1 lds
buffer_store_dword v4,v3,s[36:39],s71 offen offset:0 sc0 sc1
s_mov_b32 s83,0
Lg4_poll_%=:
s_add_u32 s78,s83,2
s_and_b32 s78,s78,3
s_lshl_b32 s78,s78,10
s_add_u32 s78,s78,4096
s_add_u32 m0,s59,s78
s_nop 0
buffer_load_dword v8,s[36:39],s58 offen offset:0 sc1 lds
s_and_b32 s78,s83,3
s_lshl_b32 s78,s78,10
v_add_u32 v62,s78,v6
s_waitcnt vmcnt(4)
s_barrier
ds_read_b32 v7,v62 offset:4096
buffer_store_dword v4,v3,s[36:39],s70 offen offset:0 sc0 sc1
s_mov_b32 s70,s71
s_mov_b32 s71,s72
s_mov_b32 s72,0x7fff0000
s_add_u32 s83,s83,1
s_add_u32 s82,s82,1
L 0
v_cmp_eq_u32 vcc,s53,v7
s_nop 0
s_cmp_eq_u32 vcc_lo,-1
s_cbranch_scc1 Lg4_go_%=
s_cmp_lt_u32 s82,20000
s_cbranch_scc1 Lg4_poll_%=
Lg4_go_%=:
s_add_u32 m0,s57,0
s_add_u32 s56,s55,0
D 0,56,0
D 0,56,1024
D 0,56,2048
D 0,56,3072
s_add_u32 m0,s57,4096
s_add_u32 s56,s56,4096
D 0,56,0
D 0,56,1024
D 0,56,2048
s_add_u32 s61,s54,s46
s_cmp_ge_u32 s61,s63
s_cselect_b32 s61,s93,s61
s_cselect_b32 s46,s88,s46
s_cselect_b32 s63,s89,s63
s_cselect_b32 s93,s50,s93
s_cselect_b32 s88,8,s88
s_cselect_b32 s89,-1,s89
s_mul_i32 s58,s61,2560
s_add_u32 s58,s58,109891584
s_add_u32 m0,s59,2048
s_nop 0
buffer_load_dword v8,s[36:39],s58 offen offset:0 sc1 lds
s_add_u32 m0,s57,28672
s_add_u32 s56,s55,28672
D 0,56,0
D 0,56,1024
D 0,56,2048
D 0,56,3072
s_add_u32 m0,s57,32768
s_add_u32 s56,s56,4096
D 0,56,0
D 0,56,1024
D 0,56,2048
s_mul_i32 s55,s61,114688
s_add_u32 s55,s55,s60
s_sub_u32 s57,s84,s57
s_mov_b32 s54,s61
s_waitcnt vmcnt(7)
s_barrier
buffer_store_dword v4,v3,s[36:39],s70 offen offset:0 sc0 sc1
s_min_u32 s71,s71,s72
buffer_store_dword v4,v3,s[36:39],s71 offen offset:0 sc0 sc1
s_mov_b32 s70,0x7fff0000
s_mov_b32 s71,0x7fff0000
s_mov_b32 s72,0x7fff0000
s_cmp_eq_u32 s80,1
s_cbranch_scc1 Lg4_st1_%=
s_branch Lg4_st0_%=
Lg4_gox_%=:
s_add_u32 m0,s59,2048
s_nop 0
buffer_load_dword v8,s[36:39],s58 offen offset:0 sc1 lds
s_waitcnt vmcnt(0)
s_barrier
buffer_store_dword v4,v3,s[36:39],s70 offen offset:0 sc0 sc1
s_min_u32 s71,s71,s72
buffer_store_dword v4,v3,s[36:39],s71 offen offset:0 sc0 sc1
s_mov_b32 s70,0x7fff0000
s_mov_b32 s71,0x7fff0000
s_mov_b32 s72,0x7fff0000
s_cmp_eq_u32 s80,1
s_cbranch_scc1 Lg4_st1_%=
Lg4_st0_%=:
ds_read_b32 v7,v6 offset:2048
R 26,1,0
R 30,1,1024
R 34,1,2048
R 38,1,3072
R 42,1,4096
L 5
v_cmp_eq_u32 vcc,s53,v7
L 3
s_cmp_eq_u32 vcc_lo,-1
s_cbranch_scc1 Lg4_F0_%=
s_branch Lg4_L0_%=
Lg4_st1_%=:
ds_read_b32 v7,v6 offset:2048
R 26,2,0
R 30,2,1024
R 34,2,2048
R 38,2,3072
R 42,2,4096
L 5
v_cmp_eq_u32 vcc,s53,v7
L 3
s_cmp_eq_u32 vcc_lo,-1
s_cbranch_scc1 Lg4_F1_%=
s_branch Lg4_L1_%=
Lg4_F0_%=:
v_mfma_f32_16x16x32_bf16 v[14:17],a[4:7],v[26:29],0
s_add_u32 m0,s57,0
R 46,1,5120
v_lshlrev_b32 v62,16,v58
M 14,a[12:15],30
s_add_u32 s56,s55,0
v_add_f32 v18,v18,v62
v_and_b32 v63,0xffff0000,v58
v_mfma_f32_16x16x32_bf16 v[10:13],a[0:3],v[26:29],0
s_add_u32 s61,s54,s46
R 50,1,6144
v_add_f32 v19,v19,v63
M 10,a[8:11],30
L 3
D 0,56,0
)"
R"(M 10,a[16:19],34
s_cmp_ge_u32 s61,s63
R 54,1,7168
v_lshlrev_b32 v62,16,v59
M 10,a[24:27],38
s_cselect_b32 s61,s93,s61
v_add_f32 v20,v20,v62
v_and_b32 v63,0xffff0000,v59
M 14,a[20:23],34
s_cselect_b32 s46,s88,s46
R 26,1,8192
v_add_f32 v21,v21,v63
M 14,a[28:31],38
L 3
v_lshlrev_b32 v62,16,v60
v_add_f32 v22,v22,v62
M 14,a[36:39],42
s_cselect_b32 s63,s89,s63
R 30,1,9216
v_and_b32 v63,0xffff0000,v60
M 14,a[44:47],46
D 0,56,1024
M 10,a[32:35],42
s_cselect_b32 s93,s50,s93
R 34,1,10240
v_add_f32 v23,v23,v63
M 10,a[40:43],46
L 3
v_lshlrev_b32 v62,16,v61
v_add_f32 v24,v24,v62
M 10,a[48:51],50
s_cselect_b32 s88,8,s88
R 38,1,11264
v_and_b32 v63,0xffff0000,v61
M 10,a[56:59],54
buffer_load_dwordx4 v[58:61],v0,s[36:39],s74 offen offset:0 sc1
M 14,a[52:55],50
s_cselect_b32 s89,-1,s89
R 42,1,12288
v_add_f32 v25,v25,v63
M 14,a[60:63],54
L 3
v_cvt_pk_bf16_f32 v18,v18,v19
v_cvt_pk_bf16_f32 v19,v20,v21
M 14,a[68:71],26
s_mul_i32 s58,s61,2560
R 46,1,13312
v_cvt_pk_bf16_f32 v20,v22,v23
M 14,a[76:79],30
D 0,56,2048
M 10,a[64:67],26
s_add_u32 s58,s58,109891584
R 50,1,14336
v_cvt_pk_bf16_f32 v21,v24,v25
M 10,a[72:75],30
L 3
buffer_store_dwordx4 v[18:21],v0,s[36:39],s67 offen offset:0 sc0 sc1
M 10,a[80:83],34
s_lshl_b32 s62,s54,15
s_mov_b32 s66,s65
R 54,1,15360
M 10,a[88:91],38
s_cmp_ge_u32 s66,s45
s_cselect_b32 s51,1,-1
s_add_u32 s74,s49,s62
M 14,a[84:87],34
s_add_u32 s65,s47,s62
s_mov_b32 s54,s61
R 26,1,16384
M 14,a[92:95],38
L 3
M 14,a[100:103],42
R 30,1,17408
M 14,a[108:111],46
D 0,56,3072
M 10,a[96:99],42
s_add_u32 m0,s57,4096
s_add_u32 s56,s56,4096
R 34,1,18432
M 10,a[104:107],46
L 3
M 10,a[112:115],50
R 38,1,19456
M 10,a[120:123],54
M 14,a[116:119],50
R 42,1,20480
M 14,a[124:127],54
L 3
D 0,56,0
M 14,a[132:135],26
R 46,1,21504
M 14,a[140:143],30
M 10,a[128:131],26
R 50,1,22528
M 10,a[136:139],30
L 3
M 10,a[144:147],34
R 54,1,23552
M 10,a[152:155],38
M 14,a[148:151],34
R 26,1,24576
M 14,a[156:159],38
L 3
D 0,56,1024
M 14,a[164:167],42
R 30,1,25600
M 14,a[172:175],46
M 10,a[160:163],42
R 34,1,26624
M 10,a[168:171],46
L 3
M 10,a[176:179],50
s_waitcnt vmcnt(9)
R 38,1,27648
M 10,a[184:187],54
s_barrier
M 14,a[180:183],50
R 42,1,28672
M 14,a[188:191],54
L 3
D 0,56,2048
M 14,a[196:199],26
s_add_u32 m0,s59,0
s_add_u32 s56,s55,28672
R 46,1,29696
M 14,a[204:207],30
s_mul_i32 s55,s61,114688
s_add_u32 s55,s55,s60
M 10,a[192:195],26
R 50,1,30720
M 10,a[200:203],30
L 3
M 10,a[208:211],34
R 54,1,31744
M 10,a[216:219],38
M 14,a[212:215],34
R 26,1,32768
M 14,a[220:223],38
L 3
buffer_load_dword v8,s[36:39],s58 offen offset:0 sc1 lds
M 14,a[228:231],42
s_add_u32 m0,s57,28672
R 30,1,33792
M 14,a[236:239],46
D 0,56,0
M 10,a[224:227],42
R 34,1,34816
M 10,a[232:235],46
L 3
M 10,a[240:243],50
R 38,1,35840
M 10,a[248:251],54
M 14,a[244:247],50
R 42,1,36864
M 14,a[252:255],54
L 3
D 0,56,1024
M 14,v[68:71],26
R 46,1,37888
M 14,v[76:79],30
M 10,v[64:67],26
R 50,1,38912
M 10,v[72:75],30
L 3
M 10,v[80:83],34
R 54,1,39936
M 10,v[88:91],38
D 0,56,2048
M 14,v[84:87],34
R 26,1,40960
M 14,v[92:95],38
L 3
M 14,v[100:103],42
R 30,1,41984
M 14,v[108:111],46
M 10,v[96:99],42
R 34,1,43008
M 10,v[104:107],46
L 3
D 0,56,3072
M 10,v[112:115],50
s_add_u32 m0,s57,32768
s_add_u32 s56,s56,4096
R 38,1,44032
M 10,v[120:123],54
s_sub_u32 s57,s84,s57
M 14,v[116:119],50
R 42,1,45056
M 14,v[124:127],54
L 3
M 14,v[132:135],26
R 46,1,46080
M 14,v[140:143],30
D 0,56,0
M 10,v[128:131],26
R 50,1,47104
M 10,v[136:139],30
L 3
M 10,v[144:147],34
R 54,1,48128
M 10,v[152:155],38
M 14,v[148:151],34
R 26,1,49152
M 14,v[156:159],38
L 3
D 0,56,1024
M 14,v[164:167],42
R 30,1,50176
M 14,v[172:175],46
M 10,v[160:163],42
R 34,1,51200
M 10,v[168:171],46
L 3
M 10,v[176:179],50
R 38,1,52224
M 10,v[184:187],54
D 0,56,2048
M 14,v[180:183],50
R 42,1,53248
M 14,v[188:191],54
L 3
M 14,v[196:199],26
R 46,1,54272
M 14,v[204:207],30
M 10,v[192:195],26
R 50,1,55296
M 10,v[200:203],30
L 3
M 10,v[208:211],34
s_waitcnt vmcnt(7)
R 54,1,56320
M 10,v[216:219],38
s_barrier
ds_read_b32 v7,v6 offset:0
M 14,v[212:215],34
R 26,2,0
M 14,v[220:223],38
L 4
buffer_store_dword v4,v3,s[36:39],s69 offen offset:0 sc0 sc1
M 14,v[228:231],42
s_mov_b32 s69,s68
s_lshr_b32 s68,s62,4
R 30,2,1024
M 14,v[236:239],46
s_add_u32 s68,s68,s48
M 10,v[224:227],42
R 34,2,2048
M 10,v[232:235],46
L 4
L 3
M 10,v[240:243],50
R 38,2,3072
v_cmp_eq_u32 vcc,s53,v7
M 10,v[248:251],54
M 14,v[244:247],50
L 2
s_cmp_eq_u32 vcc_lo,s51
R 42,2,4096
M 14,v[252:255],54
s_cbranch_scc0 Lg4_K1_%=
Lg4_F1_%=:
v_mfma_f32_16x16x32_bf16 v[22:25],a[4:7],v[26:29],0
s_add_u32 m0,s57,0
R 46,2,5120
v_lshlrev_b32 v62,16,v58
M 22,a[12:15],30
s_add_u32 s56,s55,0
v_add_f32 v10,v10,v62
v_and_b32 v63,0xffff0000,v58
v_mfma_f32_16x16x32_bf16 v[18:21],a[0:3],v[26:29],0
s_add_u32 s61,s54,s46
R 50,2,6144
v_add_f32 v11,v11,v63
M 18,a[8:11],30
L 3
D 0,56,0
M 18,a[16:19],34
s_cmp_ge_u32 s61,s63
R 54,2,7168
v_lshlrev_b32 v62,16,v59
M 18,a[24:27],38
s_cselect_b32 s61,s93,s61
v_add_f32 v12,v12,v62
v_and_b32 v63,0xffff0000,v59
M 22,a[20:23],34
s_cselect_b32 s46,s88,s46
R 26,2,8192
v_add_f32 v13,v13,v63
M 22,a[28:31],38
L 3
v_lshlrev_b32 v62,16,v60
v_add_f32 v14,v14,v62
M 22,a[36:39],42
s_cselect_b32 s63,s89,s63
R 30,2,9216
v_and_b32 v63,0xffff0000,v60
M 22,a[44:47],46
D 0,56,1024
M 18,a[32:35],42
s_cselect_b32 s93,s50,s93
R 34,2,10240
v_add_f32 v15,v15,v63
M 18,a[40:43],46
L 3
v_lshlrev_b32 v62,16,v61
v_add_f32 v16,v16,v62
M 18,a[48:51],50
s_cselect_b32 s88,8,s88
R 38,2,11264
v_and_b32 v63,0xffff0000,v61
M 18,a[56:59],54
buffer_load_dwordx4 v[58:61],v0,s[36:39],s74 offen offset:0 sc1
M 22,a[52:55],50
s_cselect_b32 s89,-1,s89
R 42,2,12288
v_add_f32 v17,v17,v63
M 22,a[60:63],54
L 3
v_cvt_pk_bf16_f32 v10,v10,v11
v_cvt_pk_bf16_f32 v11,v12,v13
M 22,a[68:71],26
s_mul_i32 s58,s61,2560
R 46,2,13312
v_cvt_pk_bf16_f32 v12,v14,v15
M 22,a[76:79],30
D 0,56,2048
M 18,a[64:67],26
s_add_u32 s58,s58,109891584
R 50,2,14336
v_cvt_pk_bf16_f32 v13,v16,v17
M 18,a[72:75],30
L 3
buffer_store_dwordx4 v[10:13],v0,s[36:39],s66 offen offset:0 sc0 sc1
M 18,a[80:83],34
s_lshl_b32 s62,s54,15
s_mov_b32 s67,s65
R 54,2,15360
M 18,a[88:91],38
s_cmp_ge_u32 s67,s45
s_cselect_b32 s51,1,-1
s_add_u32 s74,s49,s62
M 22,a[84:87],34
s_add_u32 s65,s47,s62
s_mov_b32 s54,s61
R 26,2,16384
M 22,a[92:95],38
L 3
M 22,a[100:103],42
R 30,2,17408
M 22,a[108:111],46
D 0,56,3072
M 18,a[96:99],42
s_add_u32 m0,s57,4096
s_add_u32 s56,s56,4096
R 34,2,18432
M 18,a[104:107],46
L 3
M 18,a[112:115],50
R 38,2,19456
M 18,a[120:123],54
M 22,a[116:119],50
R 42,2,20480
M 22,a[124:127],54
L 3
D 0,56,0
M 22,a[132:135],26
R 46,2,21504
M 22,a[140:143],30
M 18,a[128:131],26
R 50,2,22528
M 18,a[136:139],30
L 3
M 18,a[144:147],34
R 54,2,23552
M 18,a[152:155],38
M 22,a[148:151],34
R 26,2,24576
M 22,a[156:159],38
L 3
D 0,56,1024
M 22,a[164:167],42
R 30,2,25600
M 22,a[172:175],46
M 18,a[160:163],42
R 34,2,26624
M 18,a[168:171],46
L 3
M 18,a[176:179],50
s_waitcnt vmcnt(9)
R 38,2,27648
M 18,a[184:187],54
s_barrier
M 22,a[180:183],50
R 42,2,28672
M 22,a[188:191],54
L 3
D 0,56,2048
M 22,a[196:199],26
s_add_u32 m0,s59,1024
s_add_u32 s56,s55,28672
R 46,2,29696
M 22,a[204:207],30
s_mul_i32 s55,s61,114688
s_add_u32 s55,s55,s60
M 18,a[192:195],26
R 50,2,30720
M 18,a[200:203],30
L 3
M 18,a[208:211],34
R 54,2,31744
M 18,a[216:219],38
M 22,a[212:215],34
R 26,2,32768
M 22,a[220:223],38
L 3
buffer_load_dword v8,s[36:39],s58 offen offset:0 sc1 lds
M 22,a[228:231],42
s_add_u32 m0,s57,28672
R 30,2,33792
M 22,a[236:239],46
D 0,56,0
M 18,a[224:227],42
R 34,2,34816
M 18,a[232:235],46
L 3
M 18,a[240:243],50
R 38,2,35840
M 18,a[248:251],54
M 22,a[244:247],50
R 42,2,36864
M 22,a[252:255],54
L 3
D 0,56,1024
M 22,v[68:71],26
R 46,2,37888
M 22,v[76:79],30
M 18,v[64:67],26
R 50,2,38912
M 18,v[72:75],30
L 3
M 18,v[80:83],34
R 54,2,39936
M 18,v[88:91],38
D 0,56,2048
M 22,v[84:87],34
R 26,2,40960
M 22,v[92:95],38
L 3
M 22,v[100:103],42
R 30,2,41984
M 22,v[108:111],46
M 18,v[96:99],42
R 34,2,43008
M 18,v[104:107],46
L 3
D 0,56,3072
M 18,v[112:115],50
s_add_u32 m0,s57,32768
s_add_u32 s56,s56,4096
R 38,2,44032
M 18,v[120:123],54
s_sub_u32 s57,s84,s57
M 22,v[116:119],50
R 42,2,45056
M 22,v[124:127],54
L 3
M 22,v[132:135],26
R 46,2,46080
M 22,v[140:143],30
D 0,56,0
M 18,v[128:131],26
R 50,2,47104
M 18,v[136:139],30
L 3
M 18,v[144:147],34
R 54,2,48128
M 18,v[152:155],38
M 22,v[148:151],34
R 26,2,49152
M 22,v[156:159],38
L 3
D 0,56,1024
M 22,v[164:167],42
R 30,2,50176
M 22,v[172:175],46
M 18,v[160:163],42
R 34,2,51200
M 18,v[168:171],46
L 3
M 18,v[176:179],50
R 38,2,52224
M 18,v[184:187],54
D 0,56,2048
M 22,v[180:183],50
R 42,2,53248
M 22,v[188:191],54
L 3
M 22,v[196:199],26
R 46,2,54272
M 22,v[204:207],30
)"
R"(M 18,v[192:195],26
R 50,2,55296
M 18,v[200:203],30
L 3
M 18,v[208:211],34
s_waitcnt vmcnt(7)
R 54,2,56320
M 18,v[216:219],38
s_barrier
ds_read_b32 v7,v6 offset:1024
M 22,v[212:215],34
R 26,1,0
M 22,v[220:223],38
L 4
buffer_store_dword v4,v3,s[36:39],s69 offen offset:0 sc0 sc1
M 22,v[228:231],42
s_mov_b32 s69,s68
s_lshr_b32 s68,s62,4
R 30,1,1024
M 22,v[236:239],46
s_add_u32 s68,s68,s48
M 18,v[224:227],42
R 34,1,2048
M 18,v[232:235],46
L 4
L 3
M 18,v[240:243],50
R 38,1,3072
v_cmp_eq_u32 vcc,s53,v7
M 18,v[248:251],54
M 22,v[244:247],50
L 2
s_cmp_eq_u32 vcc_lo,s51
R 42,1,4096
M 22,v[252:255],54
s_cbranch_scc1 Lg4_F0_%=
Lg4_K0_%=:
s_cmp_eq_u32 s51,1
s_cbranch_scc0 Lg4_L0_%=
s_cmp_eq_u32 vcc_lo,-1
s_cbranch_scc1 Lg4_Fa0_%=
s_branch Lg4_La0_%=
Lg4_L0_%=:
v_mfma_f32_16x16x32_bf16 v[14:17],a[4:7],v[26:29],0
s_add_u32 m0,s59,3072
R 46,1,5120
v_lshlrev_b32 v62,16,v58
M 14,a[12:15],30
buffer_load_dword v8,s[36:39],s58 offen offset:0 sc1 lds
v_mfma_f32_16x16x32_bf16 v[10:13],a[0:3],v[26:29],0
s_mov_b32 s66,s65
R 50,1,6144
v_add_f32 v18,v18,v62
M 10,a[8:11],30
L 3
v_and_b32 v63,0xffff0000,v58
v_add_f32 v19,v19,v63
M 10,a[16:19],34
s_lshl_b32 s62,s54,15
R 54,1,7168
v_lshlrev_b32 v62,16,v59
M 10,a[24:27],38
s_cmp_ge_u32 s66,s45
v_add_f32 v20,v20,v62
v_and_b32 v63,0xffff0000,v59
M 14,a[20:23],34
s_cselect_b32 s51,1,-1
R 26,1,8192
v_add_f32 v21,v21,v63
M 14,a[28:31],38
L 3
v_lshlrev_b32 v62,16,v60
v_add_f32 v22,v22,v62
M 14,a[36:39],42
s_add_u32 s65,s47,s62
R 30,1,9216
v_and_b32 v63,0xffff0000,v60
M 14,a[44:47],46
v_add_f32 v23,v23,v63
v_lshlrev_b32 v62,16,v61
M 10,a[32:35],42
R 34,1,10240
v_add_f32 v24,v24,v62
M 10,a[40:43],46
L 3
v_and_b32 v63,0xffff0000,v61
v_add_f32 v25,v25,v63
M 10,a[48:51],50
R 38,1,11264
v_cvt_pk_bf16_f32 v18,v18,v19
M 10,a[56:59],54
buffer_load_dwordx4 v[58:61],v0,s[36:39],s74 offen offset:0 sc1
M 14,a[52:55],50
s_add_u32 s74,s49,s62
R 42,1,12288
v_cvt_pk_bf16_f32 v19,v20,v21
M 14,a[60:63],54
L 3
v_cvt_pk_bf16_f32 v20,v22,v23
v_cvt_pk_bf16_f32 v21,v24,v25
M 14,a[68:71],26
R 46,1,13312
M 14,a[76:79],30
buffer_store_dwordx4 v[18:21],v0,s[36:39],s67 offen offset:0 sc0 sc1
M 10,a[64:67],26
R 50,1,14336
M 10,a[72:75],30
L 3
M 10,a[80:83],34
R 54,1,15360
M 10,a[88:91],38
M 14,a[84:87],34
R 26,1,16384
M 14,a[92:95],38
L 3
M 14,a[100:103],42
R 30,1,17408
M 14,a[108:111],46
M 10,a[96:99],42
R 34,1,18432
M 10,a[104:107],46
L 3
M 10,a[112:115],50
R 38,1,19456
M 10,a[120:123],54
M 14,a[116:119],50
R 42,1,20480
M 14,a[124:127],54
L 3
M 14,a[132:135],26
R 46,1,21504
M 14,a[140:143],30
M 10,a[128:131],26
R 50,1,22528
M 10,a[136:139],30
L 3
M 10,a[144:147],34
R 54,1,23552
M 10,a[152:155],38
M 14,a[148:151],34
R 26,1,24576
M 14,a[156:159],38
L 3
M 14,a[164:167],42
R 30,1,25600
M 14,a[172:175],46
M 10,a[160:163],42
R 34,1,26624
M 10,a[168:171],46
L 3
M 10,a[176:179],50
s_waitcnt vmcnt(2)
R 38,1,27648
M 10,a[184:187],54
s_barrier
ds_read_b32 v7,v6 offset:3072
M 14,a[180:183],50
R 42,1,28672
M 14,a[188:191],54
L 4
M 14,a[196:199],26
R 46,1,29696
M 14,a[204:207],30
M 10,a[192:195],26
R 50,1,30720
M 10,a[200:203],30
L 4
L 3
v_cmp_eq_u32 vcc,s53,v7
M 10,a[208:211],34
R 54,1,31744
M 10,a[216:219],38
M 14,a[212:215],34
s_cmp_eq_u32 vcc_lo,-1
R 26,1,32768
s_cbranch_scc0 Lg4_X0_%=
M 14,a[220:223],38
L 3
s_add_u32 m0,s57,0
s_add_u32 s56,s55,0
M 14,a[228:231],42
s_add_u32 s61,s54,s46
s_cmp_ge_u32 s61,s63
s_cselect_b32 s61,s93,s61
R 30,1,33792
M 14,a[236:239],46
s_mul_i32 s58,s61,2560
D 0,56,0
M 10,a[224:227],42
s_cselect_b32 s46,s88,s46
s_cselect_b32 s63,s89,s63
R 34,1,34816
M 10,a[232:235],46
L 3
s_cselect_b32 s93,s50,s93
s_cselect_b32 s88,8,s88
M 10,a[240:243],50
s_cselect_b32 s89,-1,s89
s_add_u32 s58,s58,109891584
R 38,1,35840
M 10,a[248:251],54
D 0,56,1024
M 14,a[244:247],50
s_mov_b32 s54,s61
R 42,1,36864
M 14,a[252:255],54
L 3
M 14,v[68:71],26
R 46,1,37888
M 14,v[76:79],30
D 0,56,2048
M 10,v[64:67],26
R 50,1,38912
M 10,v[72:75],30
L 3
M 10,v[80:83],34
R 54,1,39936
M 10,v[88:91],38
D 0,56,3072
M 14,v[84:87],34
s_add_u32 m0,s57,4096
s_add_u32 s56,s56,4096
R 26,1,40960
M 14,v[92:95],38
L 3
M 14,v[100:103],42
R 30,1,41984
M 14,v[108:111],46
D 0,56,0
M 10,v[96:99],42
R 34,1,43008
M 10,v[104:107],46
L 3
M 10,v[112:115],50
R 38,1,44032
M 10,v[120:123],54
D 0,56,1024
M 14,v[116:119],50
R 42,1,45056
M 14,v[124:127],54
L 3
M 14,v[132:135],26
R 46,1,46080
M 14,v[140:143],30
D 0,56,2048
M 10,v[128:131],26
s_add_u32 m0,s59,0
s_add_u32 s56,s55,28672
R 50,1,47104
M 10,v[136:139],30
L 3
buffer_load_dword v8,s[36:39],s58 offen offset:0 sc1 lds
M 10,v[144:147],34
s_add_u32 m0,s57,28672
s_mul_i32 s55,s61,114688
R 54,1,48128
M 10,v[152:155],38
s_add_u32 s55,s55,s60
M 14,v[148:151],34
R 26,1,49152
M 14,v[156:159],38
L 3
D 0,56,0
M 14,v[164:167],42
R 30,1,50176
M 14,v[172:175],46
M 10,v[160:163],42
R 34,1,51200
M 10,v[168:171],46
L 3
D 0,56,1024
M 10,v[176:179],50
R 38,1,52224
M 10,v[184:187],54
D 0,56,2048
M 14,v[180:183],50
R 42,1,53248
M 14,v[188:191],54
L 3
M 14,v[196:199],26
R 46,1,54272
M 14,v[204:207],30
D 0,56,3072
M 10,v[192:195],26
s_add_u32 m0,s57,32768
s_add_u32 s56,s56,4096
R 50,1,55296
M 10,v[200:203],30
L 3
s_sub_u32 s57,s84,s57
M 10,v[208:211],34
s_waitcnt vmcnt(4)
R 54,1,56320
M 10,v[216:219],38
s_barrier
ds_read_b32 v7,v6 offset:0
M 14,v[212:215],34
R 26,2,0
M 14,v[220:223],38
L 4
buffer_store_dword v4,v3,s[36:39],s69 offen offset:0 sc0 sc1
M 14,v[228:231],42
s_mov_b32 s69,s68
s_lshr_b32 s68,s62,4
R 30,2,1024
M 14,v[236:239],46
D 0,56,0
M 10,v[224:227],42
s_add_u32 s68,s68,s48
R 34,2,2048
M 10,v[232:235],46
L 4
D 0,56,1024
M 10,v[240:243],50
L 3
R 38,2,3072
v_cmp_eq_u32 vcc,s53,v7
M 10,v[248:251],54
D 0,56,2048
M 14,v[244:247],50
L 2
s_cmp_eq_u32 vcc_lo,s51
R 42,2,4096
M 14,v[252:255],54
buffer_store_dword v4,v3,s[36:39],s71 offen offset:0 sc0 sc1
s_cbranch_scc1 Lg4_F1_%=
s_branch Lg4_K1_%=
Lg4_X0_%=:
M 14,a[220:223],38
L 3
s_add_u32 m0,s59,0
M 14,a[228:231],42
R 30,1,33792
M 14,a[236:239],46
buffer_load_dword v8,s[36:39],s58 offen offset:0 sc1 lds
M 10,a[224:227],42
R 34,1,34816
M 10,a[232:235],46
L 3
M 10,a[240:243],50
R 38,1,35840
M 10,a[248:251],54
M 14,a[244:247],50
R 42,1,36864
M 14,a[252:255],54
L 3
M 14,v[68:71],26
R 46,1,37888
M 14,v[76:79],30
M 10,v[64:67],26
R 50,1,38912
M 10,v[72:75],30
L 3
M 10,v[80:83],34
R 54,1,39936
M 10,v[88:91],38
M 14,v[84:87],34
R 26,1,40960
M 14,v[92:95],38
L 3
M 14,v[100:103],42
R 30,1,41984
M 14,v[108:111],46
M 10,v[96:99],42
R 34,1,43008
M 10,v[104:107],46
L 3
M 10,v[112:115],50
R 38,1,44032
M 10,v[120:123],54
M 14,v[116:119],50
R 42,1,45056
M 14,v[124:127],54
L 3
M 14,v[132:135],26
R 46,1,46080
M 14,v[140:143],30
M 10,v[128:131],26
R 50,1,47104
M 10,v[136:139],30
L 3
M 10,v[144:147],34
R 54,1,48128
M 10,v[152:155],38
M 14,v[148:151],34
R 26,1,49152
M 14,v[156:159],38
L 3
M 14,v[164:167],42
R 30,1,50176
M 14,v[172:175],46
M 10,v[160:163],42
R 34,1,51200
M 10,v[168:171],46
L 3
M 10,v[176:179],50
R 38,1,52224
M 10,v[184:187],54
M 14,v[180:183],50
R 42,1,53248
M 14,v[188:191],54
L 3
M 14,v[196:199],26
R 46,1,54272
M 14,v[204:207],30
M 10,v[192:195],26
R 50,1,55296
M 10,v[200:203],30
L 3
M 10,v[208:211],34
s_waitcnt vmcnt(0)
R 54,1,56320
M 10,v[216:219],38
s_barrier
buffer_store_dword v4,v3,s[36:39],s69 offen offset:0 sc0 sc1
M 14,v[212:215],34
s_mov_b32 s69,s68
s_lshr_b32 s68,s62,4
ds_read_b32 v7,v6 offset:0
M 14,v[220:223],38
L 3
s_add_u32 s68,s68,s48
M 14,v[228:231],42
M 14,v[236:239],46
M 10,v[224:227],42
M 10,v[232:235],46
L 1
M 10,v[240:243],50
L 0
v_cmp_eq_u32 vcc,s53,v7
M 10,v[248:251],54
M 14,v[244:247],50
M 14,v[252:255],54
s_cmp_eq_u32 s51,1
s_cbranch_scc1 Lg4_pa0_%=
s_mov_b32 s80,1
s_cmp_eq_u32 vcc_lo,-1
s_cbranch_scc0 Lg4_xn0_%=
s_add_u32 m0,s57,0
s_add_u32 s56,s55,0
D 0,56,0
v_lshlrev_b32 v62,16,v58
D 0,56,1024
v_add_f32 v10,v10,v62
D 0,56,2048
v_and_b32 v63,0xffff0000,v58
D 0,56,3072
v_add_f32 v11,v11,v63
s_add_u32 m0,s57,4096
s_add_u32 s56,s56,4096
D 0,56,0
v_lshlrev_b32 v62,16,v59
D 0,56,1024
v_add_f32 v12,v12,v62
D 0,56,2048
v_and_b32 v63,0xffff0000,v59
s_add_u32 s61,s54,s46
s_cmp_ge_u32 s61,s63
s_cselect_b32 s61,s93,s61
s_cselect_b32 s46,s88,s46
s_cselect_b32 s63,s89,s63
s_cselect_b32 s93,s50,s93
s_cselect_b32 s88,8,s88
s_cselect_b32 s89,-1,s89
s_mul_i32 s58,s61,2560
s_add_u32 s58,s58,109891584
s_add_u32 m0,s57,28672
s_add_u32 s56,s55,28672
D 0,56,0
v_add_f32 v13,v13,v63
D 0,56,1024
v_lshlrev_b32 v62,16,v60
D 0,56,2048
v_add_f32 v14,v14,v62
D 0,56,3072
v_and_b32 v63,0xffff0000,v60
s_add_u32 m0,s57,32768
s_add_u32 s56,s56,4096
D 0,56,0
v_add_f32 v15,v15,v63
D 0,56,1024
v_lshlrev_b32 v62,16,v61
D 0,56,2048
v_add_f32 v16,v16,v62
s_mul_i32 s55,s61,114688
s_add_u32 s55,s55,s60
s_sub_u32 s57,s84,s57
s_mov_b32 s54,s61
v_and_b32 v63,0xffff0000,v61
v_add_f32 v17,v17,v63
v_cvt_pk_bf16_f32 v10,v10,v11
v_cvt_pk_bf16_f32 v11,v12,v13
v_cvt_pk_bf16_f32 v12,v14,v15
v_cvt_pk_bf16_f32 v13,v16,v17
buffer_store_dwordx4 v[10:13],v0,s[36:39],s66 offen offset:0 sc0 sc1
s_mov_b32 s72,s69
s_mov_b32 s69,0x7fff0000
s_mov_b32 s66,0x7fff0000
s_mov_b32 s76,0x7fff0000
s_branch Lg4_gox_%=
)"
R"(Lg4_xn0_%=:
s_cmp_ge_u32 s54,256
s_cbranch_scc1 Lg4_fin0_%=
s_add_u32 m0,s59,4096
s_nop 0
buffer_load_dword v8,s[36:39],s58 offen offset:0 sc1 lds
buffer_store_dword v4,v3,s[36:39],s71 offen offset:0 sc0 sc1
s_add_u32 m0,s59,5120
s_nop 0
buffer_load_dword v8,s[36:39],s58 offen offset:0 sc1 lds
buffer_store_dword v4,v3,s[36:39],s71 offen offset:0 sc0 sc1
s_mov_b32 s83,0
v_lshlrev_b32 v62,16,v58
v_add_f32 v10,v10,v62
v_and_b32 v63,0xffff0000,v58
v_add_f32 v11,v11,v63
v_lshlrev_b32 v62,16,v59
v_add_f32 v12,v12,v62
v_and_b32 v63,0xffff0000,v59
v_add_f32 v13,v13,v63
v_lshlrev_b32 v62,16,v60
v_add_f32 v14,v14,v62
v_and_b32 v63,0xffff0000,v60
v_add_f32 v15,v15,v63
v_lshlrev_b32 v62,16,v61
v_add_f32 v16,v16,v62
v_and_b32 v63,0xffff0000,v61
v_add_f32 v17,v17,v63
v_cvt_pk_bf16_f32 v10,v10,v11
v_cvt_pk_bf16_f32 v11,v12,v13
v_cvt_pk_bf16_f32 v12,v14,v15
v_cvt_pk_bf16_f32 v13,v16,v17
buffer_store_dwordx4 v[10:13],v0,s[36:39],s66 offen offset:0 sc0 sc1
s_mov_b32 s72,s69
s_mov_b32 s69,0x7fff0000
s_mov_b32 s66,0x7fff0000
s_mov_b32 s76,0x7fff0000
s_branch Lg4_poll_%=
Lg4_fin0_%=:
v_lshlrev_b32 v62,16,v58
v_add_f32 v10,v10,v62
v_and_b32 v63,0xffff0000,v58
v_add_f32 v11,v11,v63
v_lshlrev_b32 v62,16,v59
v_add_f32 v12,v12,v62
v_and_b32 v63,0xffff0000,v59
v_add_f32 v13,v13,v63
v_lshlrev_b32 v62,16,v60
v_add_f32 v14,v14,v62
v_and_b32 v63,0xffff0000,v60
v_add_f32 v15,v15,v63
v_lshlrev_b32 v62,16,v61
v_add_f32 v16,v16,v62
v_and_b32 v63,0xffff0000,v61
v_add_f32 v17,v17,v63
v_cvt_pk_bf16_f32 v10,v10,v11
v_cvt_pk_bf16_f32 v11,v12,v13
v_cvt_pk_bf16_f32 v12,v14,v15
v_cvt_pk_bf16_f32 v13,v16,v17
buffer_store_dwordx4 v[10:13],v0,s[36:39],s66 offen offset:0 sc0 sc1
s_mov_b32 s72,s69
s_mov_b32 s69,0x7fff0000
s_mov_b32 s66,0x7fff0000
s_mov_b32 s76,0x7fff0000
s_branch Lg4_done_%=
Lg4_K1_%=:
s_cmp_eq_u32 s51,1
s_cbranch_scc0 Lg4_L1_%=
s_cmp_eq_u32 vcc_lo,-1
s_cbranch_scc1 Lg4_Fa1_%=
s_branch Lg4_La1_%=
Lg4_L1_%=:
v_mfma_f32_16x16x32_bf16 v[22:25],a[4:7],v[26:29],0
s_add_u32 m0,s59,3072
R 46,2,5120
v_lshlrev_b32 v62,16,v58
M 22,a[12:15],30
buffer_load_dword v8,s[36:39],s58 offen offset:0 sc1 lds
v_mfma_f32_16x16x32_bf16 v[18:21],a[0:3],v[26:29],0
s_mov_b32 s67,s65
R 50,2,6144
v_add_f32 v10,v10,v62
M 18,a[8:11],30
L 3
v_and_b32 v63,0xffff0000,v58
v_add_f32 v11,v11,v63
M 18,a[16:19],34
s_lshl_b32 s62,s54,15
R 54,2,7168
v_lshlrev_b32 v62,16,v59
M 18,a[24:27],38
s_cmp_ge_u32 s67,s45
v_add_f32 v12,v12,v62
v_and_b32 v63,0xffff0000,v59
M 22,a[20:23],34
s_cselect_b32 s51,1,-1
R 26,2,8192
v_add_f32 v13,v13,v63
M 22,a[28:31],38
L 3
v_lshlrev_b32 v62,16,v60
v_add_f32 v14,v14,v62
M 22,a[36:39],42
s_add_u32 s65,s47,s62
R 30,2,9216
v_and_b32 v63,0xffff0000,v60
M 22,a[44:47],46
v_add_f32 v15,v15,v63
v_lshlrev_b32 v62,16,v61
M 18,a[32:35],42
R 34,2,10240
v_add_f32 v16,v16,v62
M 18,a[40:43],46
L 3
v_and_b32 v63,0xffff0000,v61
v_add_f32 v17,v17,v63
M 18,a[48:51],50
R 38,2,11264
v_cvt_pk_bf16_f32 v10,v10,v11
M 18,a[56:59],54
buffer_load_dwordx4 v[58:61],v0,s[36:39],s74 offen offset:0 sc1
M 22,a[52:55],50
s_add_u32 s74,s49,s62
R 42,2,12288
v_cvt_pk_bf16_f32 v11,v12,v13
M 22,a[60:63],54
L 3
v_cvt_pk_bf16_f32 v12,v14,v15
v_cvt_pk_bf16_f32 v13,v16,v17
M 22,a[68:71],26
R 46,2,13312
M 22,a[76:79],30
buffer_store_dwordx4 v[10:13],v0,s[36:39],s66 offen offset:0 sc0 sc1
M 18,a[64:67],26
R 50,2,14336
M 18,a[72:75],30
L 3
M 18,a[80:83],34
R 54,2,15360
M 18,a[88:91],38
M 22,a[84:87],34
R 26,2,16384
M 22,a[92:95],38
L 3
M 22,a[100:103],42
R 30,2,17408
M 22,a[108:111],46
M 18,a[96:99],42
R 34,2,18432
M 18,a[104:107],46
L 3
M 18,a[112:115],50
R 38,2,19456
M 18,a[120:123],54
M 22,a[116:119],50
R 42,2,20480
M 22,a[124:127],54
L 3
M 22,a[132:135],26
R 46,2,21504
M 22,a[140:143],30
M 18,a[128:131],26
R 50,2,22528
M 18,a[136:139],30
L 3
M 18,a[144:147],34
R 54,2,23552
M 18,a[152:155],38
M 22,a[148:151],34
R 26,2,24576
M 22,a[156:159],38
L 3
M 22,a[164:167],42
R 30,2,25600
M 22,a[172:175],46
M 18,a[160:163],42
R 34,2,26624
M 18,a[168:171],46
L 3
M 18,a[176:179],50
s_waitcnt vmcnt(2)
R 38,2,27648
M 18,a[184:187],54
s_barrier
ds_read_b32 v7,v6 offset:3072
M 22,a[180:183],50
R 42,2,28672
M 22,a[188:191],54
L 4
M 22,a[196:199],26
R 46,2,29696
M 22,a[204:207],30
M 18,a[192:195],26
R 50,2,30720
M 18,a[200:203],30
L 4
L 3
v_cmp_eq_u32 vcc,s53,v7
M 18,a[208:211],34
R 54,2,31744
M 18,a[216:219],38
M 22,a[212:215],34
s_cmp_eq_u32 vcc_lo,-1
R 26,2,32768
s_cbranch_scc0 Lg4_X1_%=
M 22,a[220:223],38
L 3
s_add_u32 m0,s57,0
s_add_u32 s56,s55,0
M 22,a[228:231],42
s_add_u32 s61,s54,s46
s_cmp_ge_u32 s61,s63
s_cselect_b32 s61,s93,s61
R 30,2,33792
M 22,a[236:239],46
s_mul_i32 s58,s61,2560
D 0,56,0
M 18,a[224:227],42
s_cselect_b32 s46,s88,s46
s_cselect_b32 s63,s89,s63
R 34,2,34816
M 18,a[232:235],46
L 3
s_cselect_b32 s93,s50,s93
s_cselect_b32 s88,8,s88
M 18,a[240:243],50
s_cselect_b32 s89,-1,s89
s_add_u32 s58,s58,109891584
R 38,2,35840
M 18,a[248:251],54
D 0,56,1024
M 22,a[244:247],50
s_mov_b32 s54,s61
R 42,2,36864
M 22,a[252:255],54
L 3
M 22,v[68:71],26
R 46,2,37888
M 22,v[76:79],30
D 0,56,2048
M 18,v[64:67],26
R 50,2,38912
M 18,v[72:75],30
L 3
M 18,v[80:83],34
R 54,2,39936
M 18,v[88:91],38
D 0,56,3072
M 22,v[84:87],34
s_add_u32 m0,s57,4096
s_add_u32 s56,s56,4096
R 26,2,40960
M 22,v[92:95],38
L 3
M 22,v[100:103],42
R 30,2,41984
M 22,v[108:111],46
D 0,56,0
M 18,v[96:99],42
R 34,2,43008
M 18,v[104:107],46
L 3
M 18,v[112:115],50
R 38,2,44032
M 18,v[120:123],54
D 0,56,1024
M 22,v[116:119],50
R 42,2,45056
M 22,v[124:127],54
L 3
M 22,v[132:135],26
R 46,2,46080
M 22,v[140:143],30
D 0,56,2048
M 18,v[128:131],26
s_add_u32 m0,s59,1024
s_add_u32 s56,s55,28672
R 50,2,47104
M 18,v[136:139],30
L 3
buffer_load_dword v8,s[36:39],s58 offen offset:0 sc1 lds
M 18,v[144:147],34
s_add_u32 m0,s57,28672
s_mul_i32 s55,s61,114688
R 54,2,48128
M 18,v[152:155],38
s_add_u32 s55,s55,s60
M 22,v[148:151],34
R 26,2,49152
M 22,v[156:159],38
L 3
D 0,56,0
M 22,v[164:167],42
R 30,2,50176
M 22,v[172:175],46
M 18,v[160:163],42
R 34,2,51200
M 18,v[168:171],46
L 3
D 0,56,1024
M 18,v[176:179],50
R 38,2,52224
M 18,v[184:187],54
D 0,56,2048
M 22,v[180:183],50
R 42,2,53248
M 22,v[188:191],54
L 3
M 22,v[196:199],26
R 46,2,54272
M 22,v[204:207],30
D 0,56,3072
M 18,v[192:195],26
s_add_u32 m0,s57,32768
s_add_u32 s56,s56,4096
R 50,2,55296
M 18,v[200:203],30
L 3
s_sub_u32 s57,s84,s57
M 18,v[208:211],34
s_waitcnt vmcnt(4)
R 54,2,56320
M 18,v[216:219],38
s_barrier
ds_read_b32 v7,v6 offset:1024
M 22,v[212:215],34
R 26,1,0
M 22,v[220:223],38
L 4
buffer_store_dword v4,v3,s[36:39],s69 offen offset:0 sc0 sc1
M 22,v[228:231],42
s_mov_b32 s69,s68
s_lshr_b32 s68,s62,4
R 30,1,1024
M 22,v[236:239],46
D 0,56,0
M 18,v[224:227],42
s_add_u32 s68,s68,s48
R 34,1,2048
M 18,v[232:235],46
L 4
D 0,56,1024
M 18,v[240:243],50
L 3
R 38,1,3072
v_cmp_eq_u32 vcc,s53,v7
M 18,v[248:251],54
D 0,56,2048
M 22,v[244:247],50
L 2
s_cmp_eq_u32 vcc_lo,s51
R 42,1,4096
M 22,v[252:255],54
buffer_store_dword v4,v3,s[36:39],s71 offen offset:0 sc0 sc1
s_cbranch_scc1 Lg4_F0_%=
s_branch Lg4_K0_%=
Lg4_X1_%=:
M 22,a[220:223],38
L 3
s_add_u32 m0,s59,1024
M 22,a[228:231],42
R 30,2,33792
M 22,a[236:239],46
buffer_load_dword v8,s[36:39],s58 offen offset:0 sc1 lds
M 18,a[224:227],42
R 34,2,34816
M 18,a[232:235],46
L 3
M 18,a[240:243],50
R 38,2,35840
M 18,a[248:251],54
M 22,a[244:247],50
R 42,2,36864
M 22,a[252:255],54
L 3
M 22,v[68:71],26
R 46,2,37888
M 22,v[76:79],30
M 18,v[64:67],26
R 50,2,38912
M 18,v[72:75],30
L 3
M 18,v[80:83],34
R 54,2,39936
M 18,v[88:91],38
M 22,v[84:87],34
R 26,2,40960
M 22,v[92:95],38
L 3
M 22,v[100:103],42
R 30,2,41984
M 22,v[108:111],46
M 18,v[96:99],42
R 34,2,43008
M 18,v[104:107],46
L 3
M 18,v[112:115],50
R 38,2,44032
M 18,v[120:123],54
M 22,v[116:119],50
R 42,2,45056
M 22,v[124:127],54
L 3
M 22,v[132:135],26
R 46,2,46080
M 22,v[140:143],30
M 18,v[128:131],26
R 50,2,47104
M 18,v[136:139],30
L 3
M 18,v[144:147],34
R 54,2,48128
M 18,v[152:155],38
M 22,v[148:151],34
R 26,2,49152
M 22,v[156:159],38
L 3
M 22,v[164:167],42
R 30,2,50176
M 22,v[172:175],46
M 18,v[160:163],42
R 34,2,51200
M 18,v[168:171],46
L 3
M 18,v[176:179],50
R 38,2,52224
M 18,v[184:187],54
M 22,v[180:183],50
R 42,2,53248
M 22,v[188:191],54
L 3
M 22,v[196:199],26
R 46,2,54272
M 22,v[204:207],30
M 18,v[192:195],26
R 50,2,55296
M 18,v[200:203],30
L 3
M 18,v[208:211],34
s_waitcnt vmcnt(0)
R 54,2,56320
M 18,v[216:219],38
s_barrier
buffer_store_dword v4,v3,s[36:39],s69 offen offset:0 sc0 sc1
M 22,v[212:215],34
s_mov_b32 s69,s68
s_lshr_b32 s68,s62,4
ds_read_b32 v7,v6 offset:1024
M 22,v[220:223],38
L 3
s_add_u32 s68,s68,s48
M 22,v[228:231],42
M 22,v[236:239],46
M 18,v[224:227],42
M 18,v[232:235],46
L 1
M 18,v[240:243],50
L 0
v_cmp_eq_u32 vcc,s53,v7
M 18,v[248:251],54
M 22,v[244:247],50
M 22,v[252:255],54
s_cmp_eq_u32 s51,1
s_cbranch_scc1 Lg4_pa1_%=
s_mov_b32 s80,0
s_cmp_eq_u32 vcc_lo,-1
s_cbranch_scc0 Lg4_xn1_%=
s_add_u32 m0,s57,0
s_add_u32 s56,s55,0
D 0,56,0
v_lshlrev_b32 v62,16,v58
D 0,56,1024
v_add_f32 v18,v18,v62
D 0,56,2048
v_and_b32 v63,0xffff0000,v58
D 0,56,3072
v_add_f32 v19,v19,v63
s_add_u32 m0,s57,4096
s_add_u32 s56,s56,4096
D 0,56,0
v_lshlrev_b32 v62,16,v59
D 0,56,1024
v_add_f32 v20,v20,v62
D 0,56,2048
v_and_b32 v63,0xffff0000,v59
s_add_u32 s61,s54,s46
s_cmp_ge_u32 s61,s63
s_cselect_b32 s61,s93,s61
s_cselect_b32 s46,s88,s46
s_cselect_b32 s63,s89,s63
s_cselect_b32 s93,s50,s93
s_cselect_b32 s88,8,s88
s_cselect_b32 s89,-1,s89
s_mul_i32 s58,s61,2560
s_add_u32 s58,s58,109891584
s_add_u32 m0,s57,28672
s_add_u32 s56,s55,28672
D 0,56,0
v_add_f32 v21,v21,v63
D 0,56,1024
)"
R"(v_lshlrev_b32 v62,16,v60
D 0,56,2048
v_add_f32 v22,v22,v62
D 0,56,3072
v_and_b32 v63,0xffff0000,v60
s_add_u32 m0,s57,32768
s_add_u32 s56,s56,4096
D 0,56,0
v_add_f32 v23,v23,v63
D 0,56,1024
v_lshlrev_b32 v62,16,v61
D 0,56,2048
v_add_f32 v24,v24,v62
s_mul_i32 s55,s61,114688
s_add_u32 s55,s55,s60
s_sub_u32 s57,s84,s57
s_mov_b32 s54,s61
v_and_b32 v63,0xffff0000,v61
v_add_f32 v25,v25,v63
v_cvt_pk_bf16_f32 v18,v18,v19
v_cvt_pk_bf16_f32 v19,v20,v21
v_cvt_pk_bf16_f32 v20,v22,v23
v_cvt_pk_bf16_f32 v21,v24,v25
buffer_store_dwordx4 v[18:21],v0,s[36:39],s67 offen offset:0 sc0 sc1
s_mov_b32 s72,s69
s_mov_b32 s69,0x7fff0000
s_mov_b32 s67,0x7fff0000
s_mov_b32 s77,0x7fff0000
s_branch Lg4_gox_%=
Lg4_xn1_%=:
s_cmp_ge_u32 s54,256
s_cbranch_scc1 Lg4_fin1_%=
s_add_u32 m0,s59,4096
s_nop 0
buffer_load_dword v8,s[36:39],s58 offen offset:0 sc1 lds
buffer_store_dword v4,v3,s[36:39],s71 offen offset:0 sc0 sc1
s_add_u32 m0,s59,5120
s_nop 0
buffer_load_dword v8,s[36:39],s58 offen offset:0 sc1 lds
buffer_store_dword v4,v3,s[36:39],s71 offen offset:0 sc0 sc1
s_mov_b32 s83,0
v_lshlrev_b32 v62,16,v58
v_add_f32 v18,v18,v62
v_and_b32 v63,0xffff0000,v58
v_add_f32 v19,v19,v63
v_lshlrev_b32 v62,16,v59
v_add_f32 v20,v20,v62
v_and_b32 v63,0xffff0000,v59
v_add_f32 v21,v21,v63
v_lshlrev_b32 v62,16,v60
v_add_f32 v22,v22,v62
v_and_b32 v63,0xffff0000,v60
v_add_f32 v23,v23,v63
v_lshlrev_b32 v62,16,v61
v_add_f32 v24,v24,v62
v_and_b32 v63,0xffff0000,v61
v_add_f32 v25,v25,v63
v_cvt_pk_bf16_f32 v18,v18,v19
v_cvt_pk_bf16_f32 v19,v20,v21
v_cvt_pk_bf16_f32 v20,v22,v23
v_cvt_pk_bf16_f32 v21,v24,v25
buffer_store_dwordx4 v[18:21],v0,s[36:39],s67 offen offset:0 sc0 sc1
s_mov_b32 s72,s69
s_mov_b32 s69,0x7fff0000
s_mov_b32 s67,0x7fff0000
s_mov_b32 s77,0x7fff0000
s_branch Lg4_poll_%=
Lg4_fin1_%=:
v_lshlrev_b32 v62,16,v58
v_add_f32 v18,v18,v62
v_and_b32 v63,0xffff0000,v58
v_add_f32 v19,v19,v63
v_lshlrev_b32 v62,16,v59
v_add_f32 v20,v20,v62
v_and_b32 v63,0xffff0000,v59
v_add_f32 v21,v21,v63
v_lshlrev_b32 v62,16,v60
v_add_f32 v22,v22,v62
v_and_b32 v63,0xffff0000,v60
v_add_f32 v23,v23,v63
v_lshlrev_b32 v62,16,v61
v_add_f32 v24,v24,v62
v_and_b32 v63,0xffff0000,v61
v_add_f32 v25,v25,v63
v_cvt_pk_bf16_f32 v18,v18,v19
v_cvt_pk_bf16_f32 v19,v20,v21
v_cvt_pk_bf16_f32 v20,v22,v23
v_cvt_pk_bf16_f32 v21,v24,v25
buffer_store_dwordx4 v[18:21],v0,s[36:39],s67 offen offset:0 sc0 sc1
s_mov_b32 s72,s69
s_mov_b32 s69,0x7fff0000
s_mov_b32 s67,0x7fff0000
s_mov_b32 s77,0x7fff0000
s_branch Lg4_done_%=
Lg4_Fa0_%=:
s_nop 0
v_mfma_f32_16x16x32_bf16 v[14:17],a[4:7],v[26:29],0
s_add_u32 m0,s57,0
R 46,1,5120
v_lshlrev_b32 v62,16,v58
M 14,a[12:15],30
s_add_u32 s56,s55,0
v_add_f32 v18,v18,v62
v_and_b32 v63,0xffff0000,v58
v_mfma_f32_16x16x32_bf16 v[10:13],a[0:3],v[26:29],0
s_add_u32 s61,s54,s46
R 50,1,6144
v_add_f32 v19,v19,v63
M 10,a[8:11],30
L 3
D 0,56,0
M 10,a[16:19],34
s_cmp_ge_u32 s61,s63
R 54,1,7168
v_lshlrev_b32 v62,16,v59
M 10,a[24:27],38
s_cselect_b32 s61,s93,s61
v_add_f32 v20,v20,v62
v_and_b32 v63,0xffff0000,v59
M 14,a[20:23],34
s_cselect_b32 s46,s88,s46
R 26,1,8192
v_add_f32 v21,v21,v63
M 14,a[28:31],38
L 3
v_lshlrev_b32 v62,16,v60
v_add_f32 v22,v22,v62
M 14,a[36:39],42
s_cselect_b32 s63,s89,s63
R 30,1,9216
v_and_b32 v63,0xffff0000,v60
M 14,a[44:47],46
D 0,56,1024
M 10,a[32:35],42
s_cselect_b32 s93,s50,s93
R 34,1,10240
v_add_f32 v23,v23,v63
M 10,a[40:43],46
L 3
v_lshlrev_b32 v62,16,v61
v_add_f32 v24,v24,v62
M 10,a[48:51],50
s_cselect_b32 s88,8,s88
R 38,1,11264
v_and_b32 v63,0xffff0000,v61
M 10,a[56:59],54
buffer_load_dwordx4 v[58:61],v0,s[36:39],s74 offen offset:0 sc1
M 14,a[52:55],50
s_cselect_b32 s89,-1,s89
R 42,1,12288
v_add_f32 v25,v25,v63
M 14,a[60:63],54
L 3
v_cvt_pk_bf16_f32 v18,v18,v19
v_cvt_pk_bf16_f32 v19,v20,v21
M 14,a[68:71],26
s_mul_i32 s58,s61,2560
R 46,1,13312
v_cvt_pk_bf16_f32 v20,v22,v23
M 14,a[76:79],30
D 0,56,2048
M 10,a[64:67],26
s_add_u32 s58,s58,109891584
R 50,1,14336
v_cvt_pk_bf16_f32 v21,v24,v25
M 10,a[72:75],30
L 3
v_lshrrev_b32 v62,8,v0
v_lshl_add_u32 v62,v62,5,v0
M 10,a[80:83],34
s_sub_u32 s75,s67,s85
R 54,1,15360
v_add_u32 v62,s73,v62
M 10,a[88:91],38
s_mov_b64 s[40:41],s[86:87]
ds_write_b128 v62,v[18:21] offset:0
M 14,a[84:87],34
s_mov_b32 s42,4194304
R 26,1,16384
ds_read2_b32 v[22:23],v5 offset0:0 offset1:16
M 14,a[92:95],38
L 5
s_mov_b32 s43,0x00020000
ds_read2_b32 v[24:25],v5 offset0:32 offset1:48
M 14,a[100:103],42
s_add_u32 s64,s75,0
s_lshl_b32 s62,s54,15
R 30,1,17408
M 14,a[108:111],46
D 0,56,3072
M 10,a[96:99],42
s_add_u32 m0,s57,4096
s_add_u32 s56,s56,4096
R 34,1,18432
M 10,a[104:107],46
L 6
s_mov_b32 s66,s65
s_add_u32 s74,s49,s62
M 10,a[112:115],50
s_add_u32 s65,s47,s62
s_mov_b32 s54,s61
R 38,1,19456
M 10,a[120:123],54
L 3
M 14,a[116:119],50
R 42,1,20480
M 14,a[124:127],54
L 3
D 0,56,0
M 14,a[132:135],26
R 46,1,21504
M 14,a[140:143],30
M 10,a[128:131],26
R 50,1,22528
M 10,a[136:139],30
L 3
M 10,a[144:147],34
R 54,1,23552
M 10,a[152:155],38
M 14,a[148:151],34
R 26,1,24576
M 14,a[156:159],38
L 3
D 0,56,1024
M 14,a[164:167],42
R 30,1,25600
M 14,a[172:175],46
M 10,a[160:163],42
R 34,1,26624
M 10,a[168:171],46
L 3
M 10,a[176:179],50
s_waitcnt vmcnt(8)
R 38,1,27648
M 10,a[184:187],54
s_barrier
M 14,a[180:183],50
R 42,1,28672
M 14,a[188:191],54
L 3
D 0,56,2048
M 14,a[196:199],26
s_add_u32 m0,s59,0
s_add_u32 s56,s55,28672
R 46,1,29696
M 14,a[204:207],30
s_mul_i32 s55,s61,114688
s_add_u32 s55,s55,s60
M 10,a[192:195],26
R 50,1,30720
M 10,a[200:203],30
L 3
M 10,a[208:211],34
R 54,1,31744
M 10,a[216:219],38
M 14,a[212:215],34
R 26,1,32768
M 14,a[220:223],38
L 3
buffer_load_dword v8,s[36:39],s58 offen offset:0 sc1 lds
M 14,a[228:231],42
s_add_u32 m0,s57,28672
R 30,1,33792
M 14,a[236:239],46
D 0,56,0
M 10,a[224:227],42
R 34,1,34816
M 10,a[232:235],46
L 3
M 10,a[240:243],50
R 38,1,35840
M 10,a[248:251],54
M 14,a[244:247],50
R 42,1,36864
M 14,a[252:255],54
L 3
D 0,56,1024
M 14,v[68:71],26
R 46,1,37888
M 14,v[76:79],30
M 10,v[64:67],26
R 50,1,38912
M 10,v[72:75],30
L 3
M 10,v[80:83],34
R 54,1,39936
M 10,v[88:91],38
D 0,56,2048
M 14,v[84:87],34
R 26,1,40960
M 14,v[92:95],38
L 3
M 14,v[100:103],42
R 30,1,41984
M 14,v[108:111],46
M 10,v[96:99],42
R 34,1,43008
M 10,v[104:107],46
L 3
D 0,56,3072
M 10,v[112:115],50
s_add_u32 m0,s57,32768
s_add_u32 s56,s56,4096
R 38,1,44032
M 10,v[120:123],54
s_sub_u32 s57,s84,s57
M 14,v[116:119],50
R 42,1,45056
M 14,v[124:127],54
L 3
M 14,v[132:135],26
R 46,1,46080
M 14,v[140:143],30
D 0,56,0
M 10,v[128:131],26
R 50,1,47104
M 10,v[136:139],30
L 3
M 10,v[144:147],34
R 54,1,48128
M 10,v[152:155],38
M 14,v[148:151],34
R 26,1,49152
M 14,v[156:159],38
L 3
D 0,56,1024
M 14,v[164:167],42
R 30,1,50176
M 14,v[172:175],46
M 10,v[160:163],42
R 34,1,51200
M 10,v[168:171],46
L 3
M 10,v[176:179],50
R 38,1,52224
M 10,v[184:187],54
D 0,56,2048
M 14,v[180:183],50
R 42,1,53248
M 14,v[188:191],54
L 3
M 14,v[196:199],26
R 46,1,54272
M 14,v[204:207],30
M 10,v[192:195],26
R 50,1,55296
M 10,v[200:203],30
L 3
M 10,v[208:211],34
s_waitcnt vmcnt(7)
R 54,1,56320
M 10,v[216:219],38
s_barrier
ds_read_b32 v7,v6 offset:0
M 14,v[212:215],34
R 26,2,0
M 14,v[220:223],38
L 4
M 14,v[228:231],42
R 30,2,1024
M 14,v[236:239],46
buffer_atomic_pk_add_bf16 v22,v9,s[40:43],s64 offen sc1
M 10,v[224:227],42
s_add_u32 s64,s75,8192
R 34,2,2048
M 10,v[232:235],46
L 4
buffer_atomic_pk_add_bf16 v23,v9,s[40:43],s64 offen sc1
M 10,v[240:243],50
s_add_u32 s64,s75,16384
L 3
R 38,2,3072
v_cmp_eq_u32 vcc,s53,v7
M 10,v[248:251],54
buffer_atomic_pk_add_bf16 v24,v9,s[40:43],s64 offen sc1
M 14,v[244:247],50
L 2
s_add_u32 s64,s75,24576
s_cmp_eq_u32 vcc_lo,-1
R 42,2,4096
M 14,v[252:255],54
buffer_atomic_pk_add_bf16 v25,v9,s[40:43],s64 offen sc1
s_cbranch_scc0 Lg4_Ka1_%=
Lg4_Fa1_%=:
s_nop 0
v_mfma_f32_16x16x32_bf16 v[22:25],a[4:7],v[26:29],0
s_add_u32 m0,s57,0
R 46,2,5120
v_lshlrev_b32 v62,16,v58
M 22,a[12:15],30
s_add_u32 s56,s55,0
v_add_f32 v10,v10,v62
v_and_b32 v63,0xffff0000,v58
v_mfma_f32_16x16x32_bf16 v[18:21],a[0:3],v[26:29],0
s_add_u32 s61,s54,s46
R 50,2,6144
v_add_f32 v11,v11,v63
M 18,a[8:11],30
L 3
D 0,56,0
M 18,a[16:19],34
s_cmp_ge_u32 s61,s63
R 54,2,7168
v_lshlrev_b32 v62,16,v59
M 18,a[24:27],38
s_cselect_b32 s61,s93,s61
v_add_f32 v12,v12,v62
v_and_b32 v63,0xffff0000,v59
M 22,a[20:23],34
s_cselect_b32 s46,s88,s46
R 26,2,8192
v_add_f32 v13,v13,v63
M 22,a[28:31],38
L 3
v_lshlrev_b32 v62,16,v60
v_add_f32 v14,v14,v62
M 22,a[36:39],42
s_cselect_b32 s63,s89,s63
R 30,2,9216
v_and_b32 v63,0xffff0000,v60
M 22,a[44:47],46
D 0,56,1024
M 18,a[32:35],42
s_cselect_b32 s93,s50,s93
R 34,2,10240
v_add_f32 v15,v15,v63
M 18,a[40:43],46
L 3
v_lshlrev_b32 v62,16,v61
v_add_f32 v16,v16,v62
M 18,a[48:51],50
s_cselect_b32 s88,8,s88
R 38,2,11264
v_and_b32 v63,0xffff0000,v61
M 18,a[56:59],54
buffer_load_dwordx4 v[58:61],v0,s[36:39],s74 offen offset:0 sc1
M 22,a[52:55],50
s_cselect_b32 s89,-1,s89
R 42,2,12288
v_add_f32 v17,v17,v63
M 22,a[60:63],54
L 3
v_cvt_pk_bf16_f32 v10,v10,v11
v_cvt_pk_bf16_f32 v11,v12,v13
M 22,a[68:71],26
s_mul_i32 s58,s61,2560
R 46,2,13312
v_cvt_pk_bf16_f32 v12,v14,v15
M 22,a[76:79],30
D 0,56,2048
M 18,a[64:67],26
s_add_u32 s58,s58,109891584
R 50,2,14336
v_cvt_pk_bf16_f32 v13,v16,v17
M 18,a[72:75],30
L 3
v_lshrrev_b32 v62,8,v0
v_lshl_add_u32 v62,v62,5,v0
M 18,a[80:83],34
s_sub_u32 s75,s66,s85
R 54,2,15360
v_add_u32 v62,s73,v62
M 18,a[88:91],38
s_mov_b64 s[40:41],s[86:87]
ds_write_b128 v62,v[10:13] offset:0
M 22,a[84:87],34
s_mov_b32 s42,4194304
R 26,2,16384
ds_read2_b32 v[14:15],v5 offset0:0 offset1:16
M 22,a[92:95],38
L 5
s_mov_b32 s43,0x00020000
ds_read2_b32 v[16:17],v5 offset0:32 offset1:48
M 22,a[100:103],42
s_add_u32 s64,s75,0
s_lshl_b32 s62,s54,15
R 30,2,17408
M 22,a[108:111],46
D 0,56,3072
M 18,a[96:99],42
s_add_u32 m0,s57,4096
s_add_u32 s56,s56,4096
R 34,2,18432
M 18,a[104:107],46
L 6
s_mov_b32 s67,s65
s_add_u32 s74,s49,s62
M 18,a[112:115],50
s_add_u32 s65,s47,s62
s_mov_b32 s54,s61
R 38,2,19456
M 18,a[120:123],54
L 3
M 22,a[116:119],50
R 42,2,20480
M 22,a[124:127],54
L 3
D 0,56,0
M 22,a[132:135],26
R 46,2,21504
)"
R"(M 22,a[140:143],30
M 18,a[128:131],26
R 50,2,22528
M 18,a[136:139],30
L 3
M 18,a[144:147],34
R 54,2,23552
M 18,a[152:155],38
M 22,a[148:151],34
R 26,2,24576
M 22,a[156:159],38
L 3
D 0,56,1024
M 22,a[164:167],42
R 30,2,25600
M 22,a[172:175],46
M 18,a[160:163],42
R 34,2,26624
M 18,a[168:171],46
L 3
M 18,a[176:179],50
s_waitcnt vmcnt(8)
R 38,2,27648
M 18,a[184:187],54
s_barrier
M 22,a[180:183],50
R 42,2,28672
M 22,a[188:191],54
L 3
D 0,56,2048
M 22,a[196:199],26
s_add_u32 m0,s59,1024
s_add_u32 s56,s55,28672
R 46,2,29696
M 22,a[204:207],30
s_mul_i32 s55,s61,114688
s_add_u32 s55,s55,s60
M 18,a[192:195],26
R 50,2,30720
M 18,a[200:203],30
L 3
M 18,a[208:211],34
R 54,2,31744
M 18,a[216:219],38
M 22,a[212:215],34
R 26,2,32768
M 22,a[220:223],38
L 3
buffer_load_dword v8,s[36:39],s58 offen offset:0 sc1 lds
M 22,a[228:231],42
s_add_u32 m0,s57,28672
R 30,2,33792
M 22,a[236:239],46
D 0,56,0
M 18,a[224:227],42
R 34,2,34816
M 18,a[232:235],46
L 3
M 18,a[240:243],50
R 38,2,35840
M 18,a[248:251],54
M 22,a[244:247],50
R 42,2,36864
M 22,a[252:255],54
L 3
D 0,56,1024
M 22,v[68:71],26
R 46,2,37888
M 22,v[76:79],30
M 18,v[64:67],26
R 50,2,38912
M 18,v[72:75],30
L 3
M 18,v[80:83],34
R 54,2,39936
M 18,v[88:91],38
D 0,56,2048
M 22,v[84:87],34
R 26,2,40960
M 22,v[92:95],38
L 3
M 22,v[100:103],42
R 30,2,41984
M 22,v[108:111],46
M 18,v[96:99],42
R 34,2,43008
M 18,v[104:107],46
L 3
D 0,56,3072
M 18,v[112:115],50
s_add_u32 m0,s57,32768
s_add_u32 s56,s56,4096
R 38,2,44032
M 18,v[120:123],54
s_sub_u32 s57,s84,s57
M 22,v[116:119],50
R 42,2,45056
M 22,v[124:127],54
L 3
M 22,v[132:135],26
R 46,2,46080
M 22,v[140:143],30
D 0,56,0
M 18,v[128:131],26
R 50,2,47104
M 18,v[136:139],30
L 3
M 18,v[144:147],34
R 54,2,48128
M 18,v[152:155],38
M 22,v[148:151],34
R 26,2,49152
M 22,v[156:159],38
L 3
D 0,56,1024
M 22,v[164:167],42
R 30,2,50176
M 22,v[172:175],46
M 18,v[160:163],42
R 34,2,51200
M 18,v[168:171],46
L 3
M 18,v[176:179],50
R 38,2,52224
M 18,v[184:187],54
D 0,56,2048
M 22,v[180:183],50
R 42,2,53248
M 22,v[188:191],54
L 3
M 22,v[196:199],26
R 46,2,54272
M 22,v[204:207],30
M 18,v[192:195],26
R 50,2,55296
M 18,v[200:203],30
L 3
M 18,v[208:211],34
s_waitcnt vmcnt(7)
R 54,2,56320
M 18,v[216:219],38
s_barrier
ds_read_b32 v7,v6 offset:1024
M 22,v[212:215],34
R 26,1,0
M 22,v[220:223],38
L 4
M 22,v[228:231],42
R 30,1,1024
M 22,v[236:239],46
buffer_atomic_pk_add_bf16 v14,v9,s[40:43],s64 offen sc1
M 18,v[224:227],42
s_add_u32 s64,s75,8192
R 34,1,2048
M 18,v[232:235],46
L 4
buffer_atomic_pk_add_bf16 v15,v9,s[40:43],s64 offen sc1
M 18,v[240:243],50
s_add_u32 s64,s75,16384
L 3
R 38,1,3072
v_cmp_eq_u32 vcc,s53,v7
M 18,v[248:251],54
buffer_atomic_pk_add_bf16 v16,v9,s[40:43],s64 offen sc1
M 22,v[244:247],50
L 2
s_add_u32 s64,s75,24576
s_cmp_eq_u32 vcc_lo,-1
R 42,1,4096
M 22,v[252:255],54
buffer_atomic_pk_add_bf16 v17,v9,s[40:43],s64 offen sc1
s_cbranch_scc1 Lg4_Fa0_%=
Lg4_Ka0_%=:
Lg4_La0_%=:
s_nop 0
v_mfma_f32_16x16x32_bf16 v[14:17],a[4:7],v[26:29],0
s_add_u32 m0,s59,3072
R 46,1,5120
v_lshlrev_b32 v62,16,v58
M 14,a[12:15],30
buffer_load_dword v8,s[36:39],s58 offen offset:0 sc1 lds
v_mfma_f32_16x16x32_bf16 v[10:13],a[0:3],v[26:29],0
s_sub_u32 s75,s67,s85
R 50,1,6144
v_add_f32 v18,v18,v62
M 10,a[8:11],30
L 3
v_and_b32 v63,0xffff0000,v58
v_add_f32 v19,v19,v63
M 10,a[16:19],34
s_mov_b64 s[40:41],s[86:87]
R 54,1,7168
v_lshlrev_b32 v62,16,v59
M 10,a[24:27],38
s_mov_b32 s42,4194304
v_add_f32 v20,v20,v62
v_and_b32 v63,0xffff0000,v59
M 14,a[20:23],34
s_mov_b32 s43,0x00020000
R 26,1,8192
v_add_f32 v21,v21,v63
M 14,a[28:31],38
L 3
v_lshlrev_b32 v62,16,v60
v_add_f32 v22,v22,v62
M 14,a[36:39],42
s_add_u32 s64,s75,0
R 30,1,9216
v_and_b32 v63,0xffff0000,v60
M 14,a[44:47],46
s_lshl_b32 s62,s54,15
v_add_f32 v23,v23,v63
v_lshlrev_b32 v62,16,v61
M 10,a[32:35],42
s_mov_b32 s66,s65
R 34,1,10240
v_add_f32 v24,v24,v62
M 10,a[40:43],46
L 3
v_and_b32 v63,0xffff0000,v61
v_add_f32 v25,v25,v63
M 10,a[48:51],50
s_add_u32 s65,s47,s62
R 38,1,11264
v_cvt_pk_bf16_f32 v18,v18,v19
M 10,a[56:59],54
buffer_load_dwordx4 v[58:61],v0,s[36:39],s74 offen offset:0 sc1
M 14,a[52:55],50
s_add_u32 s74,s49,s62
R 42,1,12288
v_cvt_pk_bf16_f32 v19,v20,v21
M 14,a[60:63],54
L 3
v_cvt_pk_bf16_f32 v20,v22,v23
v_cvt_pk_bf16_f32 v21,v24,v25
M 14,a[68:71],26
R 46,1,13312
v_lshrrev_b32 v62,8,v0
M 14,a[76:79],30
v_lshl_add_u32 v62,v62,5,v0
v_add_u32 v62,s73,v62
M 10,a[64:67],26
R 50,1,14336
M 10,a[72:75],30
L 3
ds_write_b128 v62,v[18:21] offset:0
M 10,a[80:83],34
R 54,1,15360
ds_read2_b32 v[22:23],v5 offset0:0 offset1:16
M 10,a[88:91],38
ds_read2_b32 v[24:25],v5 offset0:32 offset1:48
M 14,a[84:87],34
R 26,1,16384
M 14,a[92:95],38
L 6
M 14,a[100:103],42
R 30,1,17408
M 14,a[108:111],46
M 10,a[96:99],42
R 34,1,18432
M 10,a[104:107],46
L 5
L 3
M 10,a[112:115],50
R 38,1,19456
M 10,a[120:123],54
buffer_atomic_pk_add_bf16 v22,v9,s[40:43],s64 offen sc1
M 14,a[116:119],50
s_add_u32 s64,s75,8192
R 42,1,20480
M 14,a[124:127],54
L 3
buffer_atomic_pk_add_bf16 v23,v9,s[40:43],s64 offen sc1
M 14,a[132:135],26
s_add_u32 s64,s75,16384
R 46,1,21504
M 14,a[140:143],30
buffer_atomic_pk_add_bf16 v24,v9,s[40:43],s64 offen sc1
M 10,a[128:131],26
s_add_u32 s64,s75,24576
R 50,1,22528
M 10,a[136:139],30
L 3
buffer_atomic_pk_add_bf16 v25,v9,s[40:43],s64 offen sc1
M 10,a[144:147],34
R 54,1,23552
M 10,a[152:155],38
M 14,a[148:151],34
R 26,1,24576
M 14,a[156:159],38
L 3
M 14,a[164:167],42
R 30,1,25600
M 14,a[172:175],46
M 10,a[160:163],42
R 34,1,26624
M 10,a[168:171],46
L 3
M 10,a[176:179],50
s_waitcnt vmcnt(5)
R 38,1,27648
M 10,a[184:187],54
s_barrier
ds_read_b32 v7,v6 offset:3072
M 14,a[180:183],50
R 42,1,28672
M 14,a[188:191],54
L 4
M 14,a[196:199],26
R 46,1,29696
M 14,a[204:207],30
M 10,a[192:195],26
R 50,1,30720
M 10,a[200:203],30
L 4
L 3
v_cmp_eq_u32 vcc,s53,v7
M 10,a[208:211],34
R 54,1,31744
M 10,a[216:219],38
M 14,a[212:215],34
s_cmp_eq_u32 vcc_lo,-1
R 26,1,32768
s_cbranch_scc0 Lg4_Xa0_%=
M 14,a[220:223],38
L 3
s_add_u32 m0,s57,0
s_add_u32 s56,s55,0
M 14,a[228:231],42
s_add_u32 s61,s54,s46
s_cmp_ge_u32 s61,s63
s_cselect_b32 s61,s93,s61
R 30,1,33792
M 14,a[236:239],46
s_mul_i32 s58,s61,2560
D 0,56,0
M 10,a[224:227],42
s_cselect_b32 s46,s88,s46
s_cselect_b32 s63,s89,s63
R 34,1,34816
M 10,a[232:235],46
L 3
s_cselect_b32 s93,s50,s93
s_cselect_b32 s88,8,s88
M 10,a[240:243],50
s_cselect_b32 s89,-1,s89
s_add_u32 s58,s58,109891584
R 38,1,35840
M 10,a[248:251],54
D 0,56,1024
M 14,a[244:247],50
s_mov_b32 s54,s61
R 42,1,36864
M 14,a[252:255],54
L 3
M 14,v[68:71],26
R 46,1,37888
M 14,v[76:79],30
D 0,56,2048
M 10,v[64:67],26
R 50,1,38912
M 10,v[72:75],30
L 3
M 10,v[80:83],34
R 54,1,39936
M 10,v[88:91],38
D 0,56,3072
M 14,v[84:87],34
s_add_u32 m0,s57,4096
s_add_u32 s56,s56,4096
R 26,1,40960
M 14,v[92:95],38
L 3
M 14,v[100:103],42
R 30,1,41984
M 14,v[108:111],46
D 0,56,0
M 10,v[96:99],42
R 34,1,43008
M 10,v[104:107],46
L 3
M 10,v[112:115],50
R 38,1,44032
M 10,v[120:123],54
D 0,56,1024
M 14,v[116:119],50
R 42,1,45056
M 14,v[124:127],54
L 3
M 14,v[132:135],26
R 46,1,46080
M 14,v[140:143],30
D 0,56,2048
M 10,v[128:131],26
s_add_u32 m0,s59,0
s_add_u32 s56,s55,28672
R 50,1,47104
M 10,v[136:139],30
L 3
buffer_load_dword v8,s[36:39],s58 offen offset:0 sc1 lds
M 10,v[144:147],34
s_add_u32 m0,s57,28672
s_mul_i32 s55,s61,114688
R 54,1,48128
M 10,v[152:155],38
s_add_u32 s55,s55,s60
M 14,v[148:151],34
R 26,1,49152
M 14,v[156:159],38
L 3
D 0,56,0
M 14,v[164:167],42
R 30,1,50176
M 14,v[172:175],46
M 10,v[160:163],42
R 34,1,51200
M 10,v[168:171],46
L 3
D 0,56,1024
M 10,v[176:179],50
R 38,1,52224
M 10,v[184:187],54
D 0,56,2048
M 14,v[180:183],50
R 42,1,53248
M 14,v[188:191],54
L 3
M 14,v[196:199],26
R 46,1,54272
M 14,v[204:207],30
D 0,56,3072
M 10,v[192:195],26
s_add_u32 m0,s57,32768
s_add_u32 s56,s56,4096
R 50,1,55296
M 10,v[200:203],30
L 3
s_sub_u32 s57,s84,s57
M 10,v[208:211],34
s_waitcnt vmcnt(4)
R 54,1,56320
M 10,v[216:219],38
s_barrier
ds_read_b32 v7,v6 offset:0
M 14,v[212:215],34
R 26,2,0
M 14,v[220:223],38
L 4
D 0,56,0
M 14,v[228:231],42
R 30,2,1024
M 14,v[236:239],46
D 0,56,1024
M 10,v[224:227],42
R 34,2,2048
M 10,v[232:235],46
L 4
D 0,56,2048
M 10,v[240:243],50
L 3
R 38,2,3072
v_cmp_eq_u32 vcc,s53,v7
M 10,v[248:251],54
M 14,v[244:247],50
L 2
s_cmp_eq_u32 vcc_lo,-1
R 42,2,4096
M 14,v[252:255],54
buffer_store_dword v4,v3,s[36:39],s71 offen offset:0 sc0 sc1
buffer_store_dword v4,v3,s[36:39],s71 offen offset:0 sc0 sc1
buffer_store_dword v4,v3,s[36:39],s71 offen offset:0 sc0 sc1
buffer_store_dword v4,v3,s[36:39],s71 offen offset:0 sc0 sc1
s_cbranch_scc1 Lg4_Fa1_%=
s_branch Lg4_Ka1_%=
Lg4_Xa0_%=:
M 14,a[220:223],38
L 3
s_add_u32 m0,s59,0
M 14,a[228:231],42
R 30,1,33792
M 14,a[236:239],46
buffer_load_dword v8,s[36:39],s58 offen offset:0 sc1 lds
M 10,a[224:227],42
R 34,1,34816
M 10,a[232:235],46
L 3
M 10,a[240:243],50
R 38,1,35840
M 10,a[248:251],54
M 14,a[244:247],50
R 42,1,36864
M 14,a[252:255],54
L 3
M 14,v[68:71],26
R 46,1,37888
M 14,v[76:79],30
M 10,v[64:67],26
R 50,1,38912
M 10,v[72:75],30
L 3
)"
R"(M 10,v[80:83],34
R 54,1,39936
M 10,v[88:91],38
M 14,v[84:87],34
R 26,1,40960
M 14,v[92:95],38
L 3
M 14,v[100:103],42
R 30,1,41984
M 14,v[108:111],46
M 10,v[96:99],42
R 34,1,43008
M 10,v[104:107],46
L 3
M 10,v[112:115],50
R 38,1,44032
M 10,v[120:123],54
M 14,v[116:119],50
R 42,1,45056
M 14,v[124:127],54
L 3
M 14,v[132:135],26
R 46,1,46080
M 14,v[140:143],30
M 10,v[128:131],26
R 50,1,47104
M 10,v[136:139],30
L 3
M 10,v[144:147],34
R 54,1,48128
M 10,v[152:155],38
M 14,v[148:151],34
R 26,1,49152
M 14,v[156:159],38
L 3
M 14,v[164:167],42
R 30,1,50176
M 14,v[172:175],46
M 10,v[160:163],42
R 34,1,51200
M 10,v[168:171],46
L 3
M 10,v[176:179],50
R 38,1,52224
M 10,v[184:187],54
M 14,v[180:183],50
R 42,1,53248
M 14,v[188:191],54
L 3
M 14,v[196:199],26
R 46,1,54272
M 14,v[204:207],30
M 10,v[192:195],26
R 50,1,55296
M 10,v[200:203],30
L 3
M 10,v[208:211],34
s_waitcnt vmcnt(0)
R 54,1,56320
M 10,v[216:219],38
s_barrier
ds_read_b32 v7,v6 offset:0
M 14,v[212:215],34
M 14,v[220:223],38
L 3
M 14,v[228:231],42
M 14,v[236:239],46
M 10,v[224:227],42
M 10,v[232:235],46
L 1
L 0
M 10,v[240:243],50
v_cmp_eq_u32 vcc,s53,v7
M 10,v[248:251],54
M 14,v[244:247],50
M 14,v[252:255],54
Lg4_pa0_%=:
s_mov_b32 s80,1
s_cmp_eq_u32 vcc_lo,-1
s_cbranch_scc0 Lg4_xna0_%=
s_add_u32 m0,s57,0
s_add_u32 s56,s55,0
D 0,56,0
s_sub_u32 s75,s66,s85
v_lshlrev_b32 v62,16,v58
D 0,56,1024
v_add_f32 v10,v10,v62
D 0,56,2048
v_and_b32 v63,0xffff0000,v58
D 0,56,3072
v_add_f32 v11,v11,v63
s_add_u32 m0,s57,4096
s_add_u32 s56,s56,4096
D 0,56,0
v_lshlrev_b32 v62,16,v59
D 0,56,1024
v_add_f32 v12,v12,v62
D 0,56,2048
v_and_b32 v63,0xffff0000,v59
s_add_u32 s61,s54,s46
s_cmp_ge_u32 s61,s63
s_cselect_b32 s61,s93,s61
s_cselect_b32 s46,s88,s46
s_cselect_b32 s63,s89,s63
s_cselect_b32 s93,s50,s93
s_cselect_b32 s88,8,s88
s_cselect_b32 s89,-1,s89
s_mul_i32 s58,s61,2560
s_add_u32 s58,s58,109891584
s_add_u32 m0,s57,28672
s_add_u32 s56,s55,28672
D 0,56,0
v_add_f32 v13,v13,v63
D 0,56,1024
v_lshlrev_b32 v62,16,v60
D 0,56,2048
v_add_f32 v14,v14,v62
D 0,56,3072
v_and_b32 v63,0xffff0000,v60
s_add_u32 m0,s57,32768
s_add_u32 s56,s56,4096
D 0,56,0
v_add_f32 v15,v15,v63
D 0,56,1024
v_lshlrev_b32 v62,16,v61
D 0,56,2048
v_add_f32 v16,v16,v62
s_mul_i32 s55,s61,114688
s_add_u32 s55,s55,s60
s_sub_u32 s57,s84,s57
s_mov_b32 s54,s61
v_and_b32 v63,0xffff0000,v61
v_add_f32 v17,v17,v63
v_cvt_pk_bf16_f32 v10,v10,v11
v_cvt_pk_bf16_f32 v11,v12,v13
v_cvt_pk_bf16_f32 v12,v14,v15
v_cvt_pk_bf16_f32 v13,v16,v17
v_lshrrev_b32 v62,8,v0
v_lshl_add_u32 v62,v62,5,v0
v_add_u32 v62,s73,v62
ds_write_b128 v62,v[10:13] offset:0
ds_read2_b32 v[14:15],v5 offset0:0 offset1:16
ds_read2_b32 v[16:17],v5 offset0:32 offset1:48
s_mov_b64 s[40:41],s[86:87]
s_mov_b32 s42,4194304
s_mov_b32 s43,0x00020000
L 0
s_add_u32 s64,s75,0
buffer_atomic_pk_add_bf16 v14,v9,s[40:43],s64 offen sc1
s_add_u32 s64,s75,8192
buffer_atomic_pk_add_bf16 v15,v9,s[40:43],s64 offen sc1
s_add_u32 s64,s75,16384
buffer_atomic_pk_add_bf16 v16,v9,s[40:43],s64 offen sc1
s_add_u32 s64,s75,24576
buffer_atomic_pk_add_bf16 v17,v9,s[40:43],s64 offen sc1
s_mov_b32 s72,0x7fff0000
s_mov_b32 s69,0x7fff0000
s_mov_b32 s66,0x7fff0000
s_mov_b32 s76,0x7fff0000
s_branch Lg4_gox_%=
Lg4_xna0_%=:
s_cmp_ge_u32 s54,256
s_cbranch_scc1 Lg4_fina0_%=
s_add_u32 m0,s59,4096
s_nop 0
buffer_load_dword v8,s[36:39],s58 offen offset:0 sc1 lds
buffer_store_dword v4,v3,s[36:39],s71 offen offset:0 sc0 sc1
s_add_u32 m0,s59,5120
s_nop 0
buffer_load_dword v8,s[36:39],s58 offen offset:0 sc1 lds
buffer_store_dword v4,v3,s[36:39],s71 offen offset:0 sc0 sc1
s_mov_b32 s83,0
s_sub_u32 s75,s66,s85
v_lshlrev_b32 v62,16,v58
v_add_f32 v10,v10,v62
v_and_b32 v63,0xffff0000,v58
v_add_f32 v11,v11,v63
v_lshlrev_b32 v62,16,v59
v_add_f32 v12,v12,v62
v_and_b32 v63,0xffff0000,v59
v_add_f32 v13,v13,v63
v_lshlrev_b32 v62,16,v60
v_add_f32 v14,v14,v62
v_and_b32 v63,0xffff0000,v60
v_add_f32 v15,v15,v63
v_lshlrev_b32 v62,16,v61
v_add_f32 v16,v16,v62
v_and_b32 v63,0xffff0000,v61
v_add_f32 v17,v17,v63
v_cvt_pk_bf16_f32 v10,v10,v11
v_cvt_pk_bf16_f32 v11,v12,v13
v_cvt_pk_bf16_f32 v12,v14,v15
v_cvt_pk_bf16_f32 v13,v16,v17
v_lshrrev_b32 v62,8,v0
v_lshl_add_u32 v62,v62,5,v0
v_add_u32 v62,s73,v62
ds_write_b128 v62,v[10:13] offset:0
ds_read2_b32 v[14:15],v5 offset0:0 offset1:16
ds_read2_b32 v[16:17],v5 offset0:32 offset1:48
s_mov_b64 s[40:41],s[86:87]
s_mov_b32 s42,4194304
s_mov_b32 s43,0x00020000
L 0
s_add_u32 s64,s75,0
buffer_atomic_pk_add_bf16 v14,v9,s[40:43],s64 offen sc1
s_add_u32 s64,s75,8192
buffer_atomic_pk_add_bf16 v15,v9,s[40:43],s64 offen sc1
s_add_u32 s64,s75,16384
buffer_atomic_pk_add_bf16 v16,v9,s[40:43],s64 offen sc1
s_add_u32 s64,s75,24576
buffer_atomic_pk_add_bf16 v17,v9,s[40:43],s64 offen sc1
s_mov_b32 s72,0x7fff0000
s_mov_b32 s69,0x7fff0000
s_mov_b32 s66,0x7fff0000
s_mov_b32 s76,0x7fff0000
s_branch Lg4_poll_%=
Lg4_fina0_%=:
s_sub_u32 s75,s66,s85
v_lshlrev_b32 v62,16,v58
v_add_f32 v10,v10,v62
v_and_b32 v63,0xffff0000,v58
v_add_f32 v11,v11,v63
v_lshlrev_b32 v62,16,v59
v_add_f32 v12,v12,v62
v_and_b32 v63,0xffff0000,v59
v_add_f32 v13,v13,v63
v_lshlrev_b32 v62,16,v60
v_add_f32 v14,v14,v62
v_and_b32 v63,0xffff0000,v60
v_add_f32 v15,v15,v63
v_lshlrev_b32 v62,16,v61
v_add_f32 v16,v16,v62
v_and_b32 v63,0xffff0000,v61
v_add_f32 v17,v17,v63
v_cvt_pk_bf16_f32 v10,v10,v11
v_cvt_pk_bf16_f32 v11,v12,v13
v_cvt_pk_bf16_f32 v12,v14,v15
v_cvt_pk_bf16_f32 v13,v16,v17
v_lshrrev_b32 v62,8,v0
v_lshl_add_u32 v62,v62,5,v0
v_add_u32 v62,s73,v62
ds_write_b128 v62,v[10:13] offset:0
ds_read2_b32 v[14:15],v5 offset0:0 offset1:16
ds_read2_b32 v[16:17],v5 offset0:32 offset1:48
s_mov_b64 s[40:41],s[86:87]
s_mov_b32 s42,4194304
s_mov_b32 s43,0x00020000
L 0
s_add_u32 s64,s75,0
buffer_atomic_pk_add_bf16 v14,v9,s[40:43],s64 offen sc1
s_add_u32 s64,s75,8192
buffer_atomic_pk_add_bf16 v15,v9,s[40:43],s64 offen sc1
s_add_u32 s64,s75,16384
buffer_atomic_pk_add_bf16 v16,v9,s[40:43],s64 offen sc1
s_add_u32 s64,s75,24576
buffer_atomic_pk_add_bf16 v17,v9,s[40:43],s64 offen sc1
s_mov_b32 s72,0x7fff0000
s_mov_b32 s69,0x7fff0000
s_mov_b32 s66,0x7fff0000
s_mov_b32 s76,0x7fff0000
s_branch Lg4_done_%=
Lg4_Ka1_%=:
Lg4_La1_%=:
s_nop 0
v_mfma_f32_16x16x32_bf16 v[22:25],a[4:7],v[26:29],0
s_add_u32 m0,s59,3072
R 46,2,5120
v_lshlrev_b32 v62,16,v58
M 22,a[12:15],30
buffer_load_dword v8,s[36:39],s58 offen offset:0 sc1 lds
v_mfma_f32_16x16x32_bf16 v[18:21],a[0:3],v[26:29],0
s_sub_u32 s75,s66,s85
R 50,2,6144
v_add_f32 v10,v10,v62
M 18,a[8:11],30
L 3
v_and_b32 v63,0xffff0000,v58
v_add_f32 v11,v11,v63
M 18,a[16:19],34
s_mov_b64 s[40:41],s[86:87]
R 54,2,7168
v_lshlrev_b32 v62,16,v59
M 18,a[24:27],38
s_mov_b32 s42,4194304
v_add_f32 v12,v12,v62
v_and_b32 v63,0xffff0000,v59
M 22,a[20:23],34
s_mov_b32 s43,0x00020000
R 26,2,8192
v_add_f32 v13,v13,v63
M 22,a[28:31],38
L 3
v_lshlrev_b32 v62,16,v60
v_add_f32 v14,v14,v62
M 22,a[36:39],42
s_add_u32 s64,s75,0
R 30,2,9216
v_and_b32 v63,0xffff0000,v60
M 22,a[44:47],46
s_lshl_b32 s62,s54,15
v_add_f32 v15,v15,v63
v_lshlrev_b32 v62,16,v61
M 18,a[32:35],42
s_mov_b32 s67,s65
R 34,2,10240
v_add_f32 v16,v16,v62
M 18,a[40:43],46
L 3
v_and_b32 v63,0xffff0000,v61
v_add_f32 v17,v17,v63
M 18,a[48:51],50
s_add_u32 s65,s47,s62
R 38,2,11264
v_cvt_pk_bf16_f32 v10,v10,v11
M 18,a[56:59],54
buffer_load_dwordx4 v[58:61],v0,s[36:39],s74 offen offset:0 sc1
M 22,a[52:55],50
s_add_u32 s74,s49,s62
R 42,2,12288
v_cvt_pk_bf16_f32 v11,v12,v13
M 22,a[60:63],54
L 3
v_cvt_pk_bf16_f32 v12,v14,v15
v_cvt_pk_bf16_f32 v13,v16,v17
M 22,a[68:71],26
R 46,2,13312
v_lshrrev_b32 v62,8,v0
M 22,a[76:79],30
v_lshl_add_u32 v62,v62,5,v0
v_add_u32 v62,s73,v62
M 18,a[64:67],26
R 50,2,14336
M 18,a[72:75],30
L 3
ds_write_b128 v62,v[10:13] offset:0
M 18,a[80:83],34
R 54,2,15360
ds_read2_b32 v[14:15],v5 offset0:0 offset1:16
M 18,a[88:91],38
ds_read2_b32 v[16:17],v5 offset0:32 offset1:48
M 22,a[84:87],34
R 26,2,16384
M 22,a[92:95],38
L 6
M 22,a[100:103],42
R 30,2,17408
M 22,a[108:111],46
M 18,a[96:99],42
R 34,2,18432
M 18,a[104:107],46
L 5
L 3
M 18,a[112:115],50
R 38,2,19456
M 18,a[120:123],54
buffer_atomic_pk_add_bf16 v14,v9,s[40:43],s64 offen sc1
M 22,a[116:119],50
s_add_u32 s64,s75,8192
R 42,2,20480
M 22,a[124:127],54
L 3
buffer_atomic_pk_add_bf16 v15,v9,s[40:43],s64 offen sc1
M 22,a[132:135],26
s_add_u32 s64,s75,16384
R 46,2,21504
M 22,a[140:143],30
buffer_atomic_pk_add_bf16 v16,v9,s[40:43],s64 offen sc1
M 18,a[128:131],26
s_add_u32 s64,s75,24576
R 50,2,22528
M 18,a[136:139],30
L 3
buffer_atomic_pk_add_bf16 v17,v9,s[40:43],s64 offen sc1
M 18,a[144:147],34
R 54,2,23552
M 18,a[152:155],38
M 22,a[148:151],34
R 26,2,24576
M 22,a[156:159],38
L 3
M 22,a[164:167],42
R 30,2,25600
M 22,a[172:175],46
M 18,a[160:163],42
R 34,2,26624
M 18,a[168:171],46
L 3
M 18,a[176:179],50
s_waitcnt vmcnt(5)
R 38,2,27648
M 18,a[184:187],54
s_barrier
ds_read_b32 v7,v6 offset:3072
M 22,a[180:183],50
R 42,2,28672
M 22,a[188:191],54
L 4
M 22,a[196:199],26
R 46,2,29696
M 22,a[204:207],30
M 18,a[192:195],26
R 50,2,30720
M 18,a[200:203],30
L 4
L 3
v_cmp_eq_u32 vcc,s53,v7
M 18,a[208:211],34
R 54,2,31744
M 18,a[216:219],38
M 22,a[212:215],34
s_cmp_eq_u32 vcc_lo,-1
R 26,2,32768
s_cbranch_scc0 Lg4_Xa1_%=
M 22,a[220:223],38
L 3
s_add_u32 m0,s57,0
s_add_u32 s56,s55,0
M 22,a[228:231],42
s_add_u32 s61,s54,s46
s_cmp_ge_u32 s61,s63
s_cselect_b32 s61,s93,s61
R 30,2,33792
M 22,a[236:239],46
s_mul_i32 s58,s61,2560
D 0,56,0
M 18,a[224:227],42
s_cselect_b32 s46,s88,s46
s_cselect_b32 s63,s89,s63
R 34,2,34816
M 18,a[232:235],46
L 3
s_cselect_b32 s93,s50,s93
s_cselect_b32 s88,8,s88
M 18,a[240:243],50
s_cselect_b32 s89,-1,s89
s_add_u32 s58,s58,109891584
R 38,2,35840
M 18,a[248:251],54
D 0,56,1024
M 22,a[244:247],50
s_mov_b32 s54,s61
R 42,2,36864
M 22,a[252:255],54
L 3
M 22,v[68:71],26
R 46,2,37888
M 22,v[76:79],30
D 0,56,2048
M 18,v[64:67],26
R 50,2,38912
M 18,v[72:75],30
L 3
M 18,v[80:83],34
R 54,2,39936
M 18,v[88:91],38
D 0,56,3072
M 22,v[84:87],34
s_add_u32 m0,s57,4096
s_add_u32 s56,s56,4096
R 26,2,40960
M 22,v[92:95],38
L 3
M 22,v[100:103],42
R 30,2,41984
M 22,v[108:111],46
D 0,56,0
M 18,v[96:99],42
R 34,2,43008
M 18,v[104:107],46
L 3
M 18,v[112:115],50
R 38,2,44032
M 18,v[120:123],54
D 0,56,1024
M 22,v[116:119],50
R 42,2,45056
M 22,v[124:127],54
L 3
M 22,v[132:135],26
R 46,2,46080
M 22,v[140:143],30
D 0,56,2048
M 18,v[128:131],26
s_add_u32 m0,s59,1024
s_add_u32 s56,s55,28672
R 50,2,47104
M 18,v[136:139],30
L 3
buffer_load_dword v8,s[36:39],s58 offen offset:0 sc1 lds
M 18,v[144:147],34
s_add_u32 m0,s57,28672
s_mul_i32 s55,s61,114688
R 54,2,48128
M 18,v[152:155],38
s_add_u32 s55,s55,s60
M 22,v[148:151],34
R 26,2,49152
M 22,v[156:159],38
L 3
D 0,56,0
)"
R"(M 22,v[164:167],42
R 30,2,50176
M 22,v[172:175],46
M 18,v[160:163],42
R 34,2,51200
M 18,v[168:171],46
L 3
D 0,56,1024
M 18,v[176:179],50
R 38,2,52224
M 18,v[184:187],54
D 0,56,2048
M 22,v[180:183],50
R 42,2,53248
M 22,v[188:191],54
L 3
M 22,v[196:199],26
R 46,2,54272
M 22,v[204:207],30
D 0,56,3072
M 18,v[192:195],26
s_add_u32 m0,s57,32768
s_add_u32 s56,s56,4096
R 50,2,55296
M 18,v[200:203],30
L 3
s_sub_u32 s57,s84,s57
M 18,v[208:211],34
s_waitcnt vmcnt(4)
R 54,2,56320
M 18,v[216:219],38
s_barrier
ds_read_b32 v7,v6 offset:1024
M 22,v[212:215],34
R 26,1,0
M 22,v[220:223],38
L 4
D 0,56,0
M 22,v[228:231],42
R 30,1,1024
M 22,v[236:239],46
D 0,56,1024
M 18,v[224:227],42
R 34,1,2048
M 18,v[232:235],46
L 4
D 0,56,2048
M 18,v[240:243],50
L 3
R 38,1,3072
v_cmp_eq_u32 vcc,s53,v7
M 18,v[248:251],54
M 22,v[244:247],50
L 2
s_cmp_eq_u32 vcc_lo,-1
R 42,1,4096
M 22,v[252:255],54
buffer_store_dword v4,v3,s[36:39],s71 offen offset:0 sc0 sc1
buffer_store_dword v4,v3,s[36:39],s71 offen offset:0 sc0 sc1
buffer_store_dword v4,v3,s[36:39],s71 offen offset:0 sc0 sc1
buffer_store_dword v4,v3,s[36:39],s71 offen offset:0 sc0 sc1
s_cbranch_scc1 Lg4_Fa0_%=
s_branch Lg4_Ka0_%=
Lg4_Xa1_%=:
M 22,a[220:223],38
L 3
s_add_u32 m0,s59,1024
M 22,a[228:231],42
R 30,2,33792
M 22,a[236:239],46
buffer_load_dword v8,s[36:39],s58 offen offset:0 sc1 lds
M 18,a[224:227],42
R 34,2,34816
M 18,a[232:235],46
L 3
M 18,a[240:243],50
R 38,2,35840
M 18,a[248:251],54
M 22,a[244:247],50
R 42,2,36864
M 22,a[252:255],54
L 3
M 22,v[68:71],26
R 46,2,37888
M 22,v[76:79],30
M 18,v[64:67],26
R 50,2,38912
M 18,v[72:75],30
L 3
M 18,v[80:83],34
R 54,2,39936
M 18,v[88:91],38
M 22,v[84:87],34
R 26,2,40960
M 22,v[92:95],38
L 3
M 22,v[100:103],42
R 30,2,41984
M 22,v[108:111],46
M 18,v[96:99],42
R 34,2,43008
M 18,v[104:107],46
L 3
M 18,v[112:115],50
R 38,2,44032
M 18,v[120:123],54
M 22,v[116:119],50
R 42,2,45056
M 22,v[124:127],54
L 3
M 22,v[132:135],26
R 46,2,46080
M 22,v[140:143],30
M 18,v[128:131],26
R 50,2,47104
M 18,v[136:139],30
L 3
M 18,v[144:147],34
R 54,2,48128
M 18,v[152:155],38
M 22,v[148:151],34
R 26,2,49152
M 22,v[156:159],38
L 3
M 22,v[164:167],42
R 30,2,50176
M 22,v[172:175],46
M 18,v[160:163],42
R 34,2,51200
M 18,v[168:171],46
L 3
M 18,v[176:179],50
R 38,2,52224
M 18,v[184:187],54
M 22,v[180:183],50
R 42,2,53248
M 22,v[188:191],54
L 3
M 22,v[196:199],26
R 46,2,54272
M 22,v[204:207],30
M 18,v[192:195],26
R 50,2,55296
M 18,v[200:203],30
L 3
M 18,v[208:211],34
s_waitcnt vmcnt(0)
R 54,2,56320
M 18,v[216:219],38
s_barrier
ds_read_b32 v7,v6 offset:1024
M 22,v[212:215],34
M 22,v[220:223],38
L 3
M 22,v[228:231],42
M 22,v[236:239],46
M 18,v[224:227],42
M 18,v[232:235],46
L 1
L 0
M 18,v[240:243],50
v_cmp_eq_u32 vcc,s53,v7
M 18,v[248:251],54
M 22,v[244:247],50
M 22,v[252:255],54
Lg4_pa1_%=:
s_mov_b32 s80,0
s_cmp_eq_u32 vcc_lo,-1
s_cbranch_scc0 Lg4_xna1_%=
s_add_u32 m0,s57,0
s_add_u32 s56,s55,0
D 0,56,0
s_sub_u32 s75,s67,s85
v_lshlrev_b32 v62,16,v58
D 0,56,1024
v_add_f32 v18,v18,v62
D 0,56,2048
v_and_b32 v63,0xffff0000,v58
D 0,56,3072
v_add_f32 v19,v19,v63
s_add_u32 m0,s57,4096
s_add_u32 s56,s56,4096
D 0,56,0
v_lshlrev_b32 v62,16,v59
D 0,56,1024
v_add_f32 v20,v20,v62
D 0,56,2048
v_and_b32 v63,0xffff0000,v59
s_add_u32 s61,s54,s46
s_cmp_ge_u32 s61,s63
s_cselect_b32 s61,s93,s61
s_cselect_b32 s46,s88,s46
s_cselect_b32 s63,s89,s63
s_cselect_b32 s93,s50,s93
s_cselect_b32 s88,8,s88
s_cselect_b32 s89,-1,s89
s_mul_i32 s58,s61,2560
s_add_u32 s58,s58,109891584
s_add_u32 m0,s57,28672
s_add_u32 s56,s55,28672
D 0,56,0
v_add_f32 v21,v21,v63
D 0,56,1024
v_lshlrev_b32 v62,16,v60
D 0,56,2048
v_add_f32 v22,v22,v62
D 0,56,3072
v_and_b32 v63,0xffff0000,v60
s_add_u32 m0,s57,32768
s_add_u32 s56,s56,4096
D 0,56,0
v_add_f32 v23,v23,v63
D 0,56,1024
v_lshlrev_b32 v62,16,v61
D 0,56,2048
v_add_f32 v24,v24,v62
s_mul_i32 s55,s61,114688
s_add_u32 s55,s55,s60
s_sub_u32 s57,s84,s57
s_mov_b32 s54,s61
v_and_b32 v63,0xffff0000,v61
v_add_f32 v25,v25,v63
v_cvt_pk_bf16_f32 v18,v18,v19
v_cvt_pk_bf16_f32 v19,v20,v21
v_cvt_pk_bf16_f32 v20,v22,v23
v_cvt_pk_bf16_f32 v21,v24,v25
v_lshrrev_b32 v62,8,v0
v_lshl_add_u32 v62,v62,5,v0
v_add_u32 v62,s73,v62
ds_write_b128 v62,v[18:21] offset:0
ds_read2_b32 v[22:23],v5 offset0:0 offset1:16
ds_read2_b32 v[24:25],v5 offset0:32 offset1:48
s_mov_b64 s[40:41],s[86:87]
s_mov_b32 s42,4194304
s_mov_b32 s43,0x00020000
L 0
s_add_u32 s64,s75,0
buffer_atomic_pk_add_bf16 v22,v9,s[40:43],s64 offen sc1
s_add_u32 s64,s75,8192
buffer_atomic_pk_add_bf16 v23,v9,s[40:43],s64 offen sc1
s_add_u32 s64,s75,16384
buffer_atomic_pk_add_bf16 v24,v9,s[40:43],s64 offen sc1
s_add_u32 s64,s75,24576
buffer_atomic_pk_add_bf16 v25,v9,s[40:43],s64 offen sc1
s_mov_b32 s72,0x7fff0000
s_mov_b32 s69,0x7fff0000
s_mov_b32 s67,0x7fff0000
s_mov_b32 s77,0x7fff0000
s_branch Lg4_gox_%=
Lg4_xna1_%=:
s_cmp_ge_u32 s54,256
s_cbranch_scc1 Lg4_fina1_%=
s_add_u32 m0,s59,4096
s_nop 0
buffer_load_dword v8,s[36:39],s58 offen offset:0 sc1 lds
buffer_store_dword v4,v3,s[36:39],s71 offen offset:0 sc0 sc1
s_add_u32 m0,s59,5120
s_nop 0
buffer_load_dword v8,s[36:39],s58 offen offset:0 sc1 lds
buffer_store_dword v4,v3,s[36:39],s71 offen offset:0 sc0 sc1
s_mov_b32 s83,0
s_sub_u32 s75,s67,s85
v_lshlrev_b32 v62,16,v58
v_add_f32 v18,v18,v62
v_and_b32 v63,0xffff0000,v58
v_add_f32 v19,v19,v63
v_lshlrev_b32 v62,16,v59
v_add_f32 v20,v20,v62
v_and_b32 v63,0xffff0000,v59
v_add_f32 v21,v21,v63
v_lshlrev_b32 v62,16,v60
v_add_f32 v22,v22,v62
v_and_b32 v63,0xffff0000,v60
v_add_f32 v23,v23,v63
v_lshlrev_b32 v62,16,v61
v_add_f32 v24,v24,v62
v_and_b32 v63,0xffff0000,v61
v_add_f32 v25,v25,v63
v_cvt_pk_bf16_f32 v18,v18,v19
v_cvt_pk_bf16_f32 v19,v20,v21
v_cvt_pk_bf16_f32 v20,v22,v23
v_cvt_pk_bf16_f32 v21,v24,v25
v_lshrrev_b32 v62,8,v0
v_lshl_add_u32 v62,v62,5,v0
v_add_u32 v62,s73,v62
ds_write_b128 v62,v[18:21] offset:0
ds_read2_b32 v[22:23],v5 offset0:0 offset1:16
ds_read2_b32 v[24:25],v5 offset0:32 offset1:48
s_mov_b64 s[40:41],s[86:87]
s_mov_b32 s42,4194304
s_mov_b32 s43,0x00020000
L 0
s_add_u32 s64,s75,0
buffer_atomic_pk_add_bf16 v22,v9,s[40:43],s64 offen sc1
s_add_u32 s64,s75,8192
buffer_atomic_pk_add_bf16 v23,v9,s[40:43],s64 offen sc1
s_add_u32 s64,s75,16384
buffer_atomic_pk_add_bf16 v24,v9,s[40:43],s64 offen sc1
s_add_u32 s64,s75,24576
buffer_atomic_pk_add_bf16 v25,v9,s[40:43],s64 offen sc1
s_mov_b32 s72,0x7fff0000
s_mov_b32 s69,0x7fff0000
s_mov_b32 s67,0x7fff0000
s_mov_b32 s77,0x7fff0000
s_branch Lg4_poll_%=
Lg4_fina1_%=:
s_sub_u32 s75,s67,s85
v_lshlrev_b32 v62,16,v58
v_add_f32 v18,v18,v62
v_and_b32 v63,0xffff0000,v58
v_add_f32 v19,v19,v63
v_lshlrev_b32 v62,16,v59
v_add_f32 v20,v20,v62
v_and_b32 v63,0xffff0000,v59
v_add_f32 v21,v21,v63
v_lshlrev_b32 v62,16,v60
v_add_f32 v22,v22,v62
v_and_b32 v63,0xffff0000,v60
v_add_f32 v23,v23,v63
v_lshlrev_b32 v62,16,v61
v_add_f32 v24,v24,v62
v_and_b32 v63,0xffff0000,v61
v_add_f32 v25,v25,v63
v_cvt_pk_bf16_f32 v18,v18,v19
v_cvt_pk_bf16_f32 v19,v20,v21
v_cvt_pk_bf16_f32 v20,v22,v23
v_cvt_pk_bf16_f32 v21,v24,v25
v_lshrrev_b32 v62,8,v0
v_lshl_add_u32 v62,v62,5,v0
v_add_u32 v62,s73,v62
ds_write_b128 v62,v[18:21] offset:0
ds_read2_b32 v[22:23],v5 offset0:0 offset1:16
ds_read2_b32 v[24:25],v5 offset0:32 offset1:48
s_mov_b64 s[40:41],s[86:87]
s_mov_b32 s42,4194304
s_mov_b32 s43,0x00020000
L 0
s_add_u32 s64,s75,0
buffer_atomic_pk_add_bf16 v22,v9,s[40:43],s64 offen sc1
s_add_u32 s64,s75,8192
buffer_atomic_pk_add_bf16 v23,v9,s[40:43],s64 offen sc1
s_add_u32 s64,s75,16384
buffer_atomic_pk_add_bf16 v24,v9,s[40:43],s64 offen sc1
s_add_u32 s64,s75,24576
buffer_atomic_pk_add_bf16 v25,v9,s[40:43],s64 offen sc1
s_mov_b32 s72,0x7fff0000
s_mov_b32 s69,0x7fff0000
s_mov_b32 s67,0x7fff0000
s_mov_b32 s77,0x7fff0000
s_branch Lg4_done_%=
Lg4_done_%=:
s_nop 0
s_barrier
buffer_store_dword v4,v3,s[36:39],s70 offen offset:0 sc0 sc1
s_waitcnt vmcnt(1)
s_barrier
buffer_store_dword v4,v3,s[36:39],s72 offen offset:0 sc0 sc1
s_waitcnt vmcnt(0) lgkmcnt(0)
s_barrier
s_endpgm
Lcode_g4_e:
.purgem M
.purgem R
.purgem T
.purgem D
.purgem L
)"
  :: "s"(ws), "s"(__builtin_amdgcn_readfirstlane(sWave)), "s"(__builtin_amdgcn_readfirstlane(sG0)), "s"(__builtin_amdgcn_readfirstlane(sStep)), "s"(__builtin_amdgcn_readfirstlane(sOutOff)), "s"(__builtin_amdgcn_readfirstlane(sFoutOff)), "s"(__builtin_amdgcn_readfirstlane(sAuxOff)), "s"(__builtin_amdgcn_readfirstlane(sWw)), "s"(__builtin_amdgcn_readfirstlane(sBuf)), "s"(__builtin_amdgcn_readfirstlane(sRk)), "s"(__builtin_amdgcn_readfirstlane(sExp)), "v"(vL16), "v"(vFlag), "v"(vSnapOff), "v"(vSnapRd), "v"(vWd0), "v"(vWd1), "v"(vWr0), "v"(vWr1), "v"(vSp), "v"(vSn), "s"(w4), "s"(out) : "v255", "v0", "v1", "v2", "v3", "v4", "v5", "v6", "v7", "v8", "v9", "v10", "v11", "v12", "v13", "v14", "v15", "v16", "v17", "v18", "v19", "v20", "v21", "v22", "v23", "v24", "v25", "v26", "v27", "v28", "v29", "v30", "v31", "v32", "v33", "v34", "v35", "v36", "v37", "v38", "v39", "v40", "v41", "v42", "v43", "v44", "v45", "v46", "v47", "v48", "v49", "v50", "v51", "v52", "v53", "v54", "v55", "v56", "v57", "v58", "v59", "v60", "v61", "v62", "v63", "v64", "v65", "v66", "v67", "v68", "v69", "v70", "v71", "v72", "v73", "v74", "v75", "v76", "v77", "v78", "v79", "v80", "v81", "v82", "v83", "v84", "v85", "v86", "v87", "v88", "v89", "v90", "v91", "v92", "v93", "v94", "v95", "v96", "v97", "v98", "v99", "v100", "v101", "v102", "v103", "v104", "v105", "v106", "v107", "v108", "v109", "v110", "v111", "v112", "v113", "v114", "v115", "v116", "v117", "v118", "v119", "v120", "v121", "v122", "v123", "v124", "v125", "v126", "v127", "v128", "v129", "v130", "v131", "v132", "v133", "v134", "v135", "v136", "v137", "v138", "v139", "v140", "v141", "v142", "v143", "v144", "v145", "v146", "v147", "v148", "v149", "v150", "v151", "v152", "v153", "v154", "v155", "v156", "v157", "v158", "v159", "v160", "v161", "v162", "v163", "v164", "v165", "v166", "v167", "v168", "v169", "v170", "v171", "v172", "v173", "v174", "v175", "v176", "v177", "v178", "v179", "v180", "v181", "v182", "v183", "v184", "v185", "v186", "v187", "v188", "v189", "v190", "v191", "v192", "v193", "v194", "v195", "v196", "v197", "v198", "v199", "v200", "v201", "v202", "v203", "v204", "v205", "v206", "v207", "v208", "v209", "v210", "v211", "v212", "v213", "v214", "v215", "v216", "v217", "v218", "v219", "v220", "v221", "v222", "v223", "v224", "v225", "v226", "v227", "v228", "v229", "v230", "v231", "a0", "a1", "a2", "a3", "a4", "a5", "a6", "a7", "a8", "a9", "a10", "a11", "a12", "a13", "a14", "a15", "a16", "a17", "a18", "a19", "a20", "a21", "a22", "a23", "a24", "a25", "a26", "a27", "a28", "a29", "a30", "a31", "a32", "a33", "a34", "a35", "a36", "a37", "a38", "a39", "a40", "a41", "a42", "a43", "a44", "a45", "a46", "a47", "a48", "a49", "a50", "a51", "a52", "a53", "a54", "a55", "a56", "a57", "a58", "a59", "a60", "a61", "a62", "a63", "a64", "a65", "a66", "a67", "a68", "a69", "a70", "a71", "a72", "a73", "a74", "a75", "a76", "a77", "a78", "a79", "a80", "a81", "a82", "a83", "a84", "a85", "a86", "a87", "a88", "a89", "a90", "a91", "a92", "a93", "a94", "a95", "a96", "a97", "a98", "a99", "a100", "a101", "a102", "a103", "a104", "a105", "a106", "a107", "a108", "a109", "a110", "a111", "a112", "a113", "a114", "a115", "a116", "a117", "a118", "a119", "a120", "a121", "a122", "a123", "a124", "a125", "a126", "a127", "a128", "a129", "a130", "a131", "a132", "a133", "a134", "a135", "a136", "a137", "a138", "a139", "a140", "a141", "a142", "a143", "a144", "a145", "a146", "a147", "a148", "a149", "a150", "a151", "a152", "a153", "a154", "a155", "a156", "a157", "a158", "a159", "a160", "a161", "a162", "a163", "a164", "a165", "a166", "a167", "a168", "a169", "a170", "a171", "a172", "a173", "a174", "a175", "a176", "a177", "a178", "a179", "a180", "a181", "a182", "a183", "a184", "a185", "a186", "a187", "a188", "a189", "a190", "a191", "a192", "a193", "a194", "a195", "a196", "a197", "a198", "a199", "a200", "a201", "a202", "a203", "a204", "a205", "a206", "a207", "a208", "a209", "a210", "a211", "a212", "a213", "a214", "a215", "a216", "a217", "a218", "a219", "a220", "a221", "a222", "a223", "a224", "a225", "a226", "a227", "a228", "a229", "a230", "a231", "a232", "a233", "a234", "a235", "a236", "a237", "a238", "a239", "a240", "a241", "a242", "a243", "a244", "a245", "a246", "a247", "a248", "a249", "a250", "a251", "a252", "a253", "a254", "a255", "s36", "s37", "s38", "s39", "s40", "s41", "s42", "s43", "s44", "s45", "s46", "s47", "s48", "s49", "s50", "s51", "s52", "s53", "s54", "s55", "s56", "s57", "s58", "s59", "s60", "s61", "s62", "s63", "s64", "s65", "s66", "s67", "s68", "s69", "s70", "s71", "s72", "s73", "s74", "s75", "s76", "s77", "s78", "s79", "s80", "s81", "s82", "s83", "s84", "s85", "s86", "s87", "s88", "s89", "s90", "s91", "s92", "s93", "s94", "s95", "s96", "s97", "vcc", "scc", "memory");
    __builtin_unreachable();
  } else {
    LANE_ID(lane2);
    constexpr int N1 = 33 - (NR4 - 6) * 16, T1 = LAST_TASKS > RLN * N1 ? RLN * N1 : LAST_TASKS;
    const int w = kind == 6 ? 0 : kind == 1 ? 1 + (3 - rep) * 8 + slice : N1 + rep * 8 + slice;
    if (kind != 2) for (int t = w; t < T1; t += N1) row_last(ws, out, t, lane2, wave, ep);
    for (int t = T1 + (w + 32) % (N1 + 32); t < LAST_TASKS; t += N1 + 32) row_last(ws, out, t, lane2, wave, ep);
    if (kind == 2 && rep == 0 && slice == 0 && wave == 0) set_flag(ws + OFF_EP, lane2, ep);
    CTS(14);
    asm volatile("s_endpgm\nLcode_end:" ::: "memory");
    __builtin_unreachable();
  }
}

extern "C" void kernel_launch(void* const* d_in, const int* in_sizes, int n_in, void* d_out, int out_size,
                              void* d_ws, size_t ws_size, hipStream_t stream) {
  const u16* q = (const u16*)d_in[0];
  const u16* k = (const u16*)d_in[1];
  const u16* v = (const u16*)d_in[2];
  const u16* shift_k = (const u16*)d_in[3];
  const u16* ssm_k = (const u16*)d_in[4];
  const u16* ssm_D = (const u16*)d_in[5];
  const u16* w1 = (const u16*)d_in[7];
  const u16* w2 = (const u16*)d_in[8];
  const u16* w3 = (const u16*)d_in[9];
  const u16* w4 = (const u16*)d_in[10];
  flow_kernel<<<256, 256, 0, stream>>>(q, k, v, shift_k, ssm_k, ssm_D, w1, w2, w3, w4, (char*)d_ws, (u16*)d_out);
}
```

```cpp
#include <hip/hip_runtime.h>
#include <hip/hip_bf16.h>
#include <stdint.h>
#include <type_traits>

typedef short s16x8 __attribute__((ext_vector_type(8)));
typedef short s16x4 __attribute__((ext_vector_type(4)));
typedef float f32x4 __attribute__((ext_vector_type(4)));
typedef unsigned short u16;
typedef unsigned short u16x8 __attribute__((ext_vector_type(8)));
typedef unsigned short u16x4 __attribute__((ext_vector_type(4)));
typedef float f32x2 __attribute__((ext_vector_type(2)));
typedef __bf16 bf16x2 __attribute__((ext_vector_type(2)));
typedef unsigned u32x4v __attribute__((ext_vector_type(4)));
typedef __attribute__((address_space(1))) unsigned gu32;

constexpr int S_ = 2048, D_ = 1024, F_ = 3584;

constexpr int WS_USED = 111243392, LAST_TASKS = 100, ALLZ = 1, U0 = 170, U1 = 224, NR4 = 8, RHELP = 1, PRE1 = 1;
constexpr int OFF_KVR = 4194304, OFF_XP = 8388608, OFF_HS = 17825792, OFF_X1 = 31457280, OFF_G = 40894464, OFF_P0 = 71303168, OFF_P1 = 89128960, OFF_Y = 98566144, OFF_S1 = 106954752, OFF_ST = 107479040, OFF_FL = 109051904, OFF_FXP = 109056000, OFF_FH = 109334528, OFF_FX1 = 109613056, OFF_FG = 109891584, OFF_FP = 110587904, OFF_FKV = 111144960, OFF_FY = 111177728, OFF_EP = 111243264, OFF_DBG = 115343360;

#define WS_DIAG 0

__device__ __forceinline__ float bf2f(u16 v) { return __uint_as_float(((unsigned)v) << 16); }
__device__ __forceinline__ u16 f2bf(float f) { return __builtin_bit_cast(u16, (__bf16)f); }
__device__ __forceinline__ float rbf(float f) { return bf2f(f2bf(f)); }
__device__ __forceinline__ void st16(void* p, u16x8 v) {
  asm volatile("global_store_dwordx4 %0, %1, off sc0 sc1\n\ts_nop 1" :: "v"(p), "v"(v) : "memory");
}
#define WAIT_VM(n) asm volatile("s_waitcnt vmcnt(%0)" ::"n"(n) : "memory")
#define WAIT_LGKM0() asm volatile("s_waitcnt lgkmcnt(0)" ::: "memory")

__device__ __forceinline__ void touch_lds(const char* p, unsigned a) {
  asm volatile("s_mov_b32 m0, %1\n\ts_nop 0\n\tglobal_load_lds_dword %0, off" :: "v"(p), "s"(a) : "memory");
}
#define CTS(i)
__device__ __forceinline__ f32x2 up2(unsigned p) { return (f32x2){__uint_as_float(p << 16), __uint_as_float(p & 0xffff0000u)}; }
__device__ __forceinline__ unsigned pk2(f32x2 v) { return __builtin_bit_cast(unsigned, __builtin_convertvector(v, bf16x2)); }
__device__ __forceinline__ f32x2 rbf2(f32x2 v) { return up2(pk2(v)); }
__device__ __forceinline__ void set_flag(char* p, int i, unsigned ep) {
  if (i < 32) __hip_atomic_store((gu32*)p + i, ep, __ATOMIC_RELAXED, __HIP_MEMORY_SCOPE_AGENT);
}
constexpr int CV_HP = (2048 + 32) * 2, CV_UR = (2048 + 256) * 2, CV_CH = 2 * CV_HP + CV_UR;
constexpr int NCV = 174;
constexpr int CV_STG = 8 * CV_CH;
constexpr int YP = D_ * 16;

struct KvRegs { u32x4v k0, k1, k2, k3, k4, k5, k6, v0, v1, v2, v3, s0, s1, s2, s3; };
#define KV_LOADS                                                                                                                       \
  "v_lshrrev_b32 %[a], 3, %[tid]\n\tv_and_b32 %[c], 7, %[tid]\n\ts_lshr_b32 s90, %[tile], 4\n\ts_and_b32 s91, %[tile], 15\n\t"          \
  "v_lshlrev_b32 %[a], 13, %[a]\n\tv_lshlrev_b32 %[c], 4, %[c]\n\ts_lshl_b32 s90, s90, 18\n\ts_lshl_b32 s91, s91, 7\n\t"                 \
  "v_add_u32 %[a], s90, %[a]\n\tv_add_u32 %[c], s91, %[c]\n\tv_subrev_u32 %[h], 0x1000, %[a]\n\tv_max_i32 %[h], 0x800, %[h]\n\t"         \
  "v_add3_u32 %[a], %[a], %[c], s92\n\tv_add_u32 %[h], %[h], %[c]\n\tv_lshlrev_b32 %[c], 2, %[c]\n\ts_waitcnt lgkmcnt(0)\n\t"            \
  "global_load_dwordx4 %3, %[a], s[76:77] offset:-4096 nt\n\tglobal_load_dwordx4 %4, %[a], s[76:77] offset:-2048 nt\n\t"                \
  "global_load_dwordx4 %5, %[a], s[76:77] nt\n\tglobal_load_dwordx4 %6, %[a], s[76:77] offset:2048 nt\n\t"                              \
  "global_load_dwordx4 %7, %[a], s[78:79] offset:-4096 nt\n\tglobal_load_dwordx4 %8, %[a], s[78:79] offset:-2048 nt\n\t"                \
  "global_load_dwordx4 %9, %[a], s[78:79] nt\n\tglobal_load_dwordx4 %10, %[a], s[78:79] offset:2048 nt\n\t"                             \
  "global_load_dwordx4 %0, %[h], s[76:77] offset:-2048 nt\n\tglobal_load_dwordx4 %1, %[h], s[76:77] nt\n\t"                             \
  "global_load_dwordx4 %2, %[h], s[76:77] offset:2048 nt\n\t"                                                                           \
  "global_load_dwordx4 %11, %[c], s[80:81]\n\tglobal_load_dwordx4 %12, %[c], s[80:81] offset:16\n\t"                                    \
  "global_load_dwordx4 %13, %[c], s[80:81] offset:32\n\tglobal_load_dwordx4 %14, %[c], s[80:81] offset:48\n\t"
#define KV_OUTS(R) "=&v"(R.k0), "=&v"(R.k1), "=&v"(R.k2), "=&v"(R.k3), "=&v"(R.k4), "=&v"(R.k5), "=&v"(R.k6), "=&v"(R.v0), "=&v"(R.v1), "=&v"(R.v2), \
                   "=&v"(R.v3), "=&v"(R.s0), "=&v"(R.s1), "=&v"(R.s2), "=&v"(R.s3), [a] "=&v"(ta), [h] "=&v"(th), [c] "=&v"(tc)
#define KV_CLOB "s74", "s75", "s76", "s77", "s78", "s79", "s80", "s81", "s82", "s83", "s90", "s91", "s92", "scc", "vcc", "memory"
template <int N>
__device__ __forceinline__ void kv_wait(KvRegs& R) {
  asm volatile("s_waitcnt vmcnt(%15)" : "+v"(R.k0), "+v"(R.k1), "+v"(R.k2), "+v"(R.k3), "+v"(R.k4), "+v"(R.k5), "+v"(R.k6), "+v"(R.v0), "+v"(R.v1), "+v"(R.v2),
               "+v"(R.v3), "+v"(R.s0), "+v"(R.s1), "+v"(R.s2), "+v"(R.s3) : "n"(N) : "memory");
}
__device__ __forceinline__ void kv_store(KvRegs& R, char* __restrict__ ws, char* T, int b, int tid) {
  const int t0 = (b >> 4) * 128, c0 = (b & 15) * 64, ch8 = tid & 7, rg = tid >> 3;
  if (t0 + 4 * rg < 3) R.k0 = R.k1 = R.k2 = (u32x4v){0u, 0u, 0u, 0u};
  typedef unsigned u32x2 __attribute__((ext_vector_type(2)));
  const u32x4v kr[7] = {R.k0, R.k1, R.k2, R.k3, R.k4, R.k5, R.k6}, vr[4] = {R.v0, R.v1, R.v2, R.v3}, sk[4] = {R.s0, R.s1, R.s2, R.s3};
#pragma unroll
  for (int j2 = 0; j2 < 4; ++j2) {
    const f32x2 sa = up2(sk[j2][0]), sb = up2(sk[j2][1]), sc = up2(sk[j2][2]), sd = up2(sk[j2][3]);
    const f32x2 s0 = {sa[0], sc[0]}, s1 = {sa[1], sc[1]}, s2 = {sb[0], sd[0]}, s3 = {sb[1], sd[1]};
    f32x2 kf[7], o[4];
#pragma unroll
    for (int r = 0; r < 7; ++r) kf[r] = up2(kr[r][j2]);
#pragma unroll
    for (int r = 0; r < 4; ++r)
      o[r] = __builtin_elementwise_fma(kf[r + 3], s0, __builtin_elementwise_fma(kf[r + 2], s1, __builtin_elementwise_fma(kf[r + 1], s2, kf[r] * s3))) * up2(vr[r][j2]);
#pragma unroll
    for (int e = 0; e < 2; ++e)
      *(u32x2*)(T + (8 * ch8 + 2 * j2 + e) * 264 + (124 - 4 * rg) * 2) = (u32x2){pk2((f32x2){o[3][e], o[2][e]}), pk2((f32x2){o[1][e], o[0][e]})};
  }
  __syncthreads();
  u16* kvr = (u16*)(ws + OFF_KVR);
#pragma unroll
  for (int i = 0; i < 4; ++i) {
    const int idx = tid + 256 * i, ch = idx >> 4, cb = idx & 15;
    const u16x4 lo = *(const u16x4*)(T + ch * 264 + cb * 16), hi = *(const u16x4*)(T + ch * 264 + cb * 16 + 8);
    st16(kvr + (size_t)(c0 + ch) * S_ + (1920 - t0) + cb * 8, __builtin_shufflevector(lo, hi, 0, 1, 2, 3, 4, 5, 6, 7));
  }
}

#define LASTSLEEP 4
__device__ __forceinline__ void wait_flags(const char* p, unsigned ep) {
  gu32* f = (gu32*)p;
  for (int it = 0; it < 20000; ++it) {
    if (__all(__hip_atomic_load(f, __ATOMIC_RELAXED, __HIP_MEMORY_SCOPE_AGENT) == ep)) break;
    __builtin_amdgcn_s_sleep(LASTSLEEP);
  }
  asm volatile("" ::: "memory");
}
__device__ __forceinline__ void row_last(char* __restrict__ ws, u16* __restrict__ out, int t, int lane, int wave, unsigned ep) {
  const __amdgpu_buffer_rsrc_t rs = __builtin_amdgcn_make_buffer_rsrc(ws, 0, WS_USED, 0x00020000);
  const int r8 = lane & 7, seg = lane >> 3, un = 128 + (t >> 1);
  const unsigned fo = un * 32768 + (wave * 8 + (seg >> 2)) * 1024 + ((seg & 3) * 16 + (t & 1) * 8 + r8) * 16;
  wait_flags(ws + OFF_FP + un * 2048 + ((lane & 2) * 4 + wave * 2 + (lane & 1)) * 128, ep);
  u32x4v p0[4], p1[4];
#pragma unroll
  for (int i = 0; i < 4; ++i) {
    p0[i] = __builtin_bit_cast(u32x4v, __builtin_amdgcn_raw_buffer_load_b128(rs, fo + i * 2048, OFF_P0, 16));
    p1[i] = __builtin_bit_cast(u32x4v, __builtin_amdgcn_raw_buffer_load_b128(rs, fo + i * 2048, OFF_P1, 16));
  }
#pragma unroll
  for (int i = 0; i < 4; ++i) {
    u32x4v o;
#pragma unroll
    for (int e = 0; e < 4; ++e) o[e] = pk2(up2(p0[i][e]) + up2(p1[i][e]));
    st16(out + (size_t)(t * 8 + r8) * D_ + wave * 256 + i * 64 + seg * 8, __builtin_bit_cast(u16x8, o));
  }
}

__device__ __forceinline__ int swzB(int r) { return ((((r >> 1) & 1) | (((r >> 3) & 1) << 1)) << 1); }
#define LANE_ID(x) int x; asm volatile("v_mbcnt_lo_u32_b32 %0, -1, 0\n\tv_mbcnt_hi_u32_b32 %0, -1, %0" : "=v"(x))
#define W12 6
#define RLN 3
#define QTB 1
constexpr int QT0 = 34 * 128;
#define CODE_MAX(x, y) "(((" x ">" y ")&" x ")|((" x "<=" y ")&" y "))"
constexpr int HU = 40960;
#define G3OR 5
constexpr int NRW = 22 - G3OR;
__global__ __launch_bounds__(256, 1) void flow_kernel(const u16* __restrict__ q, const u16* __restrict__ kin, const u16* __restrict__ vin,
                                                      const u16* __restrict__ shift_k, const u16* __restrict__ ssm_k, const u16* __restrict__ ssm_D,
                                                      const u16* __restrict__ w1, const u16* __restrict__ w2,
                                                      const u16* __restrict__ w3, const u16* __restrict__ w4, char* __restrict__ ws,
                                                      u16* __restrict__ out) {
  __shared__ __attribute__((aligned(16))) char lds[163840];
  KvRegs KR;
  unsigned ew, ta, th, tc;
  int b, tile;
  asm volatile("Lcode_b:\n\ts_load_dwordx4 s[76:79], %[ka], 0x8\n\ts_load_dwordx2 s[82:83], %[ka], 0x50\n\t"
               "s_getpc_b64 s[74:75]\n\ts_lshr_b32 s90, %[bi], 3\n\ts_mov_b32 m0, 0x27f00\n\tv_lshlrev_b32 %[a], 5, %[tid]\n\tv_add_lshl_u32 %[a], %[a], s90, 7\n\t"
               "v_min_u32 %[a], " CODE_MAX(CODE_MAX(CODE_MAX("(Lcode_g3_e-Lcode_b)", "(Lcode_g3o_e-Lcode_b)"), CODE_MAX("(Lcode_g4_e-Lcode_b)", "(Lcode_g12_e-Lcode_b)")), "(Lcode_end-Lcode_b)") "-64, %[a]\n\t"
               "global_load_lds_dword %[a], s[74:75]\n\ts_load_dwordx2 s[80:81], %[ka], 0x18\n\t"
               "s_mov_b32 %[b], %[bi]\n\ts_bfe_u32 s91, %[bi], 0x20001\n\ts_lshl2_add_u32 %[tile], s90, s91\n\ts_movk_i32 s92, 0x1000\n\t"
               "s_lshl_b32 s90, %[bi], 7\n\ts_add_u32 %[tile], %[tile], s90\n\ts_add_u32 %[tile], %[tile], 64\n\ts_and_b32 %[tile], %[tile], 255\n\t"
               KV_LOADS "v_mov_b32 %[a], %[eo]\n\tglobal_load_dword %[ew], %[a], s[82:83] sc1"
               : KV_OUTS(KR), [ew] "=&v"(ew), [b] "=&s"(b), [tile] "=&s"(tile)
               : [ka] "s"(__builtin_amdgcn_kernarg_segment_ptr()), [bi] "s"(blockIdx.x), [tid] "v"(threadIdx.x), [eo] "n"(OFF_EP) : KV_CLOB);
  const unsigned t_start = 0;
  const int tid = threadIdx.x, lane = tid & 63, wave = __builtin_amdgcn_readfirstlane(tid >> 6);
  const int xcd = b & 7, idx = b >> 3;
  const unsigned sLds = (unsigned)(size_t)lds;
  const int o8 = idx & 7;
  int kind, rep, slice, kh = 0;
  int j3 = 0, j4 = 0;
  const int xp = xcd >> 1, pr = xp & 1;
  if (!(xcd & 1)) {
    if (idx < 20) { kind = 4; rep = idx < 16 ? (xp >> 1) * 3 + (idx >> 3) : 2; kh = pr; slice = idx < 16 ? o8 : (xp >> 1) * 4 + o8; j4 = rep * 16 + kh * 8 + slice; }
    else if (idx >= 24) { kind = 1; rep = xp; slice = o8; }
    else { kind = 3; rep = 4; slice = xp * 4 + idx - 20; j3 = 72 + slice; }
  } else if (idx >= 24) { kind = 2; rep = xp; slice = o8; }
  else if (idx < 18) { kind = 3; rep = pr * 2 + (idx >= 9); slice = (xp >> 1) * 9 + idx - 9 * (idx >= 9); j3 = rep * 18 + slice; }
  else if (idx < 22 || b == 191) { kind = 6; rep = 0; slice = idx < 22 ? xp * 4 + idx - 18 : 16; }
  else if (idx == 22 || xp == 0) { kind = 5; rep = idx == 22 ? pr * 2 + (xp >> 1) : 4; slice = 18; j3 = 90 + rep; }
  else { kind = 3; rep = 4; slice = 15 + xp; j3 = 72 + slice; }
  CTS(0);
  const unsigned sScr = sLds + 163584;
  unsigned ep = 0;
  auto w_touch = [&](const u16* w) {
#pragma unroll
    for (int i = 0; i < 2; ++i) touch_lds((const char*)w + (size_t)(rep * 256 + i * 128 + (tid >> 1)) * 2048 + slice * 256 + (tid & 1) * 128, sScr);
  };
  {
  const bool isconv = kind >= 3 && kind != 6 && j3 < 94;
  const int nw = (kind == 4 || j3 >= 92) ? 2 : 1, c0 = kind == 4 ? 8 * j4 : j3 < 92 ? 640 + 4 * j3 : j3 < 94 ? 272 + 8 * j3 : 0, cw = c0 + nw * wave;
  const unsigned hd = isconv ? sLds + 2 * wave * CV_CH + 32 : 0x30000u;
  auto h_dma = [&](bool first) {
#pragma unroll
    for (int j = 0; j < 2; ++j) {
      const u16* hp = ssm_k + (size_t)cw * S_ + (isconv && j < nw ? j * S_ + lane * 8 : 0);
      if (first) asm volatile("s_mov_b32 m0, %1\n\ts_nop 0\n\tglobal_load_lds_dwordx4 %0, off nt" :: "v"(hp), "s"(hd + j * CV_CH) : "memory");
      else asm volatile("s_mov_b32 m0, %1\n\ts_nop 0\n\tglobal_load_lds_dwordx4 %0, off offset:1024 nt\n\t"
                        "global_load_lds_dwordx4 %0, off offset:2048 nt\n\tglobal_load_lds_dwordx4 %0, off offset:3072 nt" :: "v"(hp), "s"(hd + j * CV_CH) : "memory");
    }
  };
  auto w_dma = [&](int t0, int t1) {
    const char* wp = (const char*)w1 + (16 * wave + (lane >> 3)) * 2048 + slice * 256;
    const unsigned c0_ = ((lane & 7) ^ swzB(lane >> 3)) * 16, c1_ = 8 * 2048 + ((lane & 7) ^ swzB(8 + (lane >> 3))) * 16;
#pragma unroll
    for (int t = t0; t < t1; ++t)
#pragma unroll
      for (int i = 0; i < 2; ++i)
        asm volatile("s_mov_b32 m0, %1\n\ts_nop 0\n\tglobal_load_lds_dwordx4 %0, off\n\ts_add_u32 m0, m0, 8064\n\ts_nop 0\n\tglobal_load_lds_dwordx4 %0, off offset:128"
                     :: "v"(wp + t * 131072 + (i ? c1_ : c0_)), "s"(sLds + t * 16384 + wave * 2048 + i * 1024) : "scc", "memory");
  };
  if (PRE1 && kind == 1) w_dma(0, 2);
  else { h_dma(true); h_dma(false); }
  if (isconv) *(u32x4v*)(lds + (2 * wave + (lane >> 5)) * CV_CH + 2 * CV_HP + 4096 + (lane & 31) * 16) = (u32x4v){0u, 0u, 0u, 0u};
  if (isconv && lane < 8) *(u32x4v*)(lds + (2 * wave + (lane >> 2)) * CV_CH + ((lane >> 1) & 1) * CV_HP + (lane & 1) * 16) = (u32x4v){0u, 0u, 0u, 0u};
  kv_wait<9>(KR);
  CTS(12);
  kv_store(KR, ws, lds + CV_STG, tile, tid);
  CTS(1);
  if (PRE1 && kind == 1) w_dma(2, 6);
  const unsigned dw = ((const unsigned*)ssm_D)[cw >> 1];
  asm volatile("s_cmp_eq_u32 %1, %2\n\ts_cbranch_scc1 Lew%=\n\ts_waitcnt vmcnt(0)\nLew%=:\n\ts_waitcnt vmcnt(16)" : "+v"(ew) : "s"(__builtin_amdgcn_readfirstlane(kind)), "n"(PRE1 ? 1 : 99) : "scc", "memory");
  ep = __builtin_amdgcn_readfirstlane(ew) + 1u;
  if (ep == 0u) ep = 1u;
  __builtin_amdgcn_s_barrier();
  set_flag(ws + OFF_FKV + tile * 128, tid, ep);
  CTS(2);
  if (kind < 3 && (W12 & kind) != 0) w_touch(kind == 1 ? w1 : w2);
  if (kind == 2 && (W12 & 4) != 0) w_touch(w1);
  if (isconv) {
    LANE_ID(lane);
    const int tid = wave * 64 + lane;
    const unsigned sC0 = (cw & 1) ? dw & 0xffff0000u : dw << 16, sC1 = dw & 0xffff0000u;
    const int l15 = lane & 15, lg = lane >> 4, cp = (l15 + 1) & 1, iw = kind == 4 ? j4 : 80 + j3, own = 2 * wave * CV_CH;
    const int ch = tid & (4 * nw - 1), tb = nw == 2 ? tid >> 3 : tid >> 2, slot = nw == 2 ? ch : 2 * ch;
    const unsigned sWave = wave, sBuf = nw, sAuxOff = OFF_KVR + cw * 4096, sFoutOff = OFF_FY + iw * 128, sExp = __builtin_amdgcn_readfirstlane(ep), sDbg = 110u * 1048576u + (b * 4 + wave) * 8192;
    const unsigned vL16 = lane * 16, vPA = own + cp * CV_HP + 2 * (8 * lg + l15 + 1 - cp), vPB = own + 2 * CV_HP + 2 * (2032 - 16 * l15 + 8 * lg) - 512 * 7;
    const unsigned vPU = own + 2 * CV_HP + 2 * (2044 - 16 * l15 - 4 * lg) - 512 * 7, vPW = 32 * l15 + 8 * lg;
    const unsigned vOR = slot * CV_CH + tb * 16, vORA = CV_STG + slot * 1024 + tb * 16, vOG = tb * YP + (c0 + ch) * 16;
    const unsigned vFA = ((lane & 3) * 16 + (c0 >> 6)) * 128, vFB = ((4 + lane % 12) * 16 + (c0 >> 6)) * 128, vFlag = tid < 32 ? tid * 4 : 0x80000000u;
    const unsigned vTW = lane < 42 ? (iw * 168 + wave * 42 + lane) * 128 : 0x80000000u;
    const unsigned vTX = lane < 44 ? ((kind == 4 ? j4 : j3 - 12) * 176 + wave * 44 + lane) * 128 : 0x80000000u;
    (void)sDbg;
    asm volatile(
R"(.macro M d,w,x
v_mfma_f32_16x16x32_bf16 v[\d:\d+3], \w, v[\x:\x+3], v[\d:\d+3]
.endm
.macro R d,a,o
ds_read_b128 v[\d:\d+3], v\a offset:\o
.endm
.macro T d,a,o
ds_read_b64_tr_b16 \d, v\a offset:\o
.endm
.macro D a,s,o
buffer_load_dwordx4 v\a, s[36:39], s\s offen offset:\o sc1 lds
.endm
.macro L c
s_waitcnt lgkmcnt(\c)
.endm
Lcode_cv_b:
s_mov_b64 s[36:37],%0
s_mov_b32 s38,111243392
s_mov_b32 s39,0x00020000
s_mov_b32 s44,%1
s_mov_b32 s51,%2
s_mov_b32 s49,%3
s_mov_b32 s48,%4
s_mov_b32 s86,%5
s_mov_b32 s87,%6
s_mov_b32 s53,%7
v_mov_b32 v0,%8
v_mov_b32 v1,%9
v_mov_b32 v2,%10
v_mov_b32 v3,%11
v_mov_b32 v4,%12
v_mov_b32 v5,%13
v_mov_b32 v6,%14
v_mov_b32 v7,%15
v_mov_b32 v8,%16
v_mov_b32 v9,%17
v_mov_b32 v10,%18
v_mov_b32 v11,%19
v_mov_b32 v12,%20
s_mov_b32 s58,111144960
buffer_load_dword v13,v8,s[36:39],s58 offen offset:0 sc1
s_mul_i32 s57,s44,25856
v_add_u32 v18,s57,v0
v_add_u32 v18,32,v18
v_mov_b32 v19,s57
R 88,18,0
ds_read_b32 v92,v18 offset:16
R 152,18,12928
ds_read_b32 v156,v18 offset:12944
ds_read_b32 v24,v19 offset:32
ds_read_b32 v25,v19 offset:12960
s_mov_b64 s[40:41],%21
s_mov_b32 s42,7340032
s_mov_b32 s43,0x00020000
s_mov_b64 s[60:61],%22
s_mov_b32 s62,7340032
s_mov_b32 s63,0x00020000
v_mov_b32 v23,s53
s_mov_b32 s82,0
s_add_u32 s55,s49,4096
s_cmp_eq_u32 s51,2
s_cselect_b32 s55,s55,0x7fff0000
s_cselect_b32 s56,0,0x70000000
s_add_u32 s57,s57,8320
v_add_u32 v14,1024,v1
v_add_u32 v15,2048,v1
v_add_u32 v16,3072,v1
v_add_u32 v17,12928,v1
L 4
v_alignbyte_b32 v96,v89,v88,2
v_alignbyte_b32 v97,v90,v89,2
v_alignbyte_b32 v98,v91,v90,2
v_alignbyte_b32 v99,v92,v91,2
ds_write_b128 v18,v[96:99] offset:4160
L 3
v_alignbyte_b32 v160,v153,v152,2
v_alignbyte_b32 v161,v154,v153,2
v_alignbyte_b32 v162,v155,v154,2
v_alignbyte_b32 v163,v156,v155,2
ds_write_b128 v18,v[160:163] offset:17088
L 2
v_lshlrev_b32 v24,16,v24
v_lshlrev_b32 v25,16,v25
s_mov_b64 exec,1
ds_write_b32 v19,v24 offset:4188
ds_write_b32 v19,v25 offset:17116
s_mov_b64 exec,-1
ds_read2_b32 a[0:1],v1 offset0:0 offset1:1
ds_read2_b32 a[2:3],v1 offset0:2 offset1:3
ds_read2_b32 a[4:5],v1 offset0:128 offset1:129
ds_read2_b32 a[6:7],v1 offset0:130 offset1:131
ds_read2_b32 a[8:9],v1 offset0:16 offset1:17
ds_read2_b32 a[10:11],v1 offset0:18 offset1:19
ds_read2_b32 a[12:13],v1 offset0:144 offset1:145
ds_read2_b32 a[14:15],v1 offset0:146 offset1:147
ds_read2_b32 a[16:17],v1 offset0:32 offset1:33
ds_read2_b32 a[18:19],v1 offset0:34 offset1:35
ds_read2_b32 a[20:21],v1 offset0:160 offset1:161
ds_read2_b32 a[22:23],v1 offset0:162 offset1:163
ds_read2_b32 a[24:25],v1 offset0:48 offset1:49
ds_read2_b32 a[26:27],v1 offset0:50 offset1:51
ds_read2_b32 a[28:29],v1 offset0:176 offset1:177
ds_read2_b32 a[30:31],v1 offset0:178 offset1:179
ds_read2_b32 a[32:33],v1 offset0:64 offset1:65
ds_read2_b32 a[34:35],v1 offset0:66 offset1:67
ds_read2_b32 a[36:37],v1 offset0:192 offset1:193
ds_read2_b32 a[38:39],v1 offset0:194 offset1:195
ds_read2_b32 a[40:41],v1 offset0:80 offset1:81
ds_read2_b32 a[42:43],v1 offset0:82 offset1:83
ds_read2_b32 a[44:45],v1 offset0:208 offset1:209
ds_read2_b32 a[46:47],v1 offset0:210 offset1:211
ds_read2_b32 a[48:49],v1 offset0:96 offset1:97
ds_read2_b32 a[50:51],v1 offset0:98 offset1:99
ds_read2_b32 a[52:53],v1 offset0:224 offset1:225
ds_read2_b32 a[54:55],v1 offset0:226 offset1:227
ds_read2_b32 a[56:57],v1 offset0:112 offset1:113
ds_read2_b32 a[58:59],v1 offset0:114 offset1:115
ds_read2_b32 a[60:61],v1 offset0:240 offset1:241
ds_read2_b32 a[62:63],v1 offset0:242 offset1:243
ds_read2_b32 a[64:65],v17 offset0:0 offset1:1
ds_read2_b32 a[66:67],v17 offset0:2 offset1:3
ds_read2_b32 a[68:69],v17 offset0:128 offset1:129
ds_read2_b32 a[70:71],v17 offset0:130 offset1:131
ds_read2_b32 a[72:73],v17 offset0:16 offset1:17
ds_read2_b32 a[74:75],v17 offset0:18 offset1:19
ds_read2_b32 a[76:77],v17 offset0:144 offset1:145
ds_read2_b32 a[78:79],v17 offset0:146 offset1:147
ds_read2_b32 a[80:81],v17 offset0:32 offset1:33
ds_read2_b32 a[82:83],v17 offset0:34 offset1:35
ds_read2_b32 a[84:85],v17 offset0:160 offset1:161
ds_read2_b32 a[86:87],v17 offset0:162 offset1:163
ds_read2_b32 a[88:89],v17 offset0:48 offset1:49
ds_read2_b32 a[90:91],v17 offset0:50 offset1:51
ds_read2_b32 a[92:93],v17 offset0:176 offset1:177
ds_read2_b32 a[94:95],v17 offset0:178 offset1:179
ds_read2_b32 a[96:97],v17 offset0:64 offset1:65
ds_read2_b32 a[98:99],v17 offset0:66 offset1:67
ds_read2_b32 a[100:101],v17 offset0:192 offset1:193
ds_read2_b32 a[102:103],v17 offset0:194 offset1:195
ds_read2_b32 a[104:105],v17 offset0:80 offset1:81
ds_read2_b32 a[106:107],v17 offset0:82 offset1:83
ds_read2_b32 a[108:109],v17 offset0:208 offset1:209
ds_read2_b32 a[110:111],v17 offset0:210 offset1:211
ds_read2_b32 a[112:113],v17 offset0:96 offset1:97
ds_read2_b32 a[114:115],v17 offset0:98 offset1:99
ds_read2_b32 a[116:117],v17 offset0:224 offset1:225
ds_read2_b32 a[118:119],v17 offset0:226 offset1:227
ds_read2_b32 a[120:121],v17 offset0:112 offset1:113
ds_read2_b32 a[122:123],v17 offset0:114 offset1:115
ds_read2_b32 a[124:125],v17 offset0:240 offset1:241
ds_read2_b32 a[126:127],v17 offset0:242 offset1:243
Lcv_bpa_%=:
s_waitcnt vmcnt(0)
v_cmp_eq_u32 vcc,s53,v13
s_add_u32 s82,s82,1
s_cmp_eq_u64 vcc,-1
s_cbranch_scc1 Lcv_bqa_%=
s_sleep 1
buffer_load_dword v13,v8,s[36:39],s58 offen offset:0 sc1
s_cmp_lt_u32 s82,20000
s_cbranch_scc1 Lcv_bpa_%=
s_waitcnt vmcnt(0)
Lcv_bqa_%=:
s_add_u32 m0,s57,0
s_nop 0
D 0,49,3072
s_add_u32 m0,s57,12928
s_nop 0
D 0,55,3072
buffer_load_dword v13,v9,s[36:39],s58 offen offset:0 sc1
R 88,18,0
ds_read_b32 v92,v18 offset:16
R 104,18,1024
ds_read_b32 v108,v18 offset:1040
R 120,18,2048
ds_read_b32 v124,v18 offset:2064
R 136,18,3072
ds_read_b32 v140,v18 offset:3088
L 6
v_alignbyte_b32 v96,v89,v88,2
v_alignbyte_b32 v97,v90,v89,2
v_alignbyte_b32 v98,v91,v90,2
v_alignbyte_b32 v99,v92,v91,2
ds_write_b128 v18,v[96:99] offset:4160
L 5
v_alignbyte_b32 v112,v105,v104,2
v_alignbyte_b32 v113,v106,v105,2
v_alignbyte_b32 v114,v107,v106,2
v_alignbyte_b32 v115,v108,v107,2
ds_write_b128 v18,v[112:115] offset:5184
L 4
v_alignbyte_b32 v128,v121,v120,2
v_alignbyte_b32 v129,v122,v121,2
v_alignbyte_b32 v130,v123,v122,2
v_alignbyte_b32 v131,v124,v123,2
ds_write_b128 v18,v[128:131] offset:6208
L 3
v_alignbyte_b32 v144,v137,v136,2
v_alignbyte_b32 v145,v138,v137,2
v_alignbyte_b32 v146,v139,v138,2
v_alignbyte_b32 v147,v140,v139,2
ds_write_b128 v18,v[144:147] offset:7232
R 152,18,12928
ds_read_b32 v156,v18 offset:12944
R 168,18,13952
ds_read_b32 v172,v18 offset:13968
R 184,18,14976
ds_read_b32 v188,v18 offset:14992
R 200,18,16000
ds_read_b32 v204,v18 offset:16016
L 6
v_alignbyte_b32 v160,v153,v152,2
v_alignbyte_b32 v161,v154,v153,2
v_alignbyte_b32 v162,v155,v154,2
v_alignbyte_b32 v163,v156,v155,2
ds_write_b128 v18,v[160:163] offset:17088
L 5
v_alignbyte_b32 v176,v169,v168,2
v_alignbyte_b32 v177,v170,v169,2
v_alignbyte_b32 v178,v171,v170,2
v_alignbyte_b32 v179,v172,v171,2
ds_write_b128 v18,v[176:179] offset:18112
L 4
v_alignbyte_b32 v192,v185,v184,2
v_alignbyte_b32 v193,v186,v185,2
v_alignbyte_b32 v194,v187,v186,2
v_alignbyte_b32 v195,v188,v187,2
ds_write_b128 v18,v[192:195] offset:19136
L 3
v_alignbyte_b32 v208,v201,v200,2
v_alignbyte_b32 v209,v202,v201,2
v_alignbyte_b32 v210,v203,v202,2
v_alignbyte_b32 v211,v204,v203,2
ds_write_b128 v18,v[208:211] offset:20160
s_lshl_b32 s78,s44,11
s_add_u32 s78,s78,103424
v_add_u32 v18,s78,v4
v_add_u32 v4,v19,v4
s_waitcnt vmcnt(0)
v_cmp_eq_u32 vcc,s53,v13
s_mov_b32 s83,0
s_cmp_eq_u64 vcc,-1
s_cbranch_scc0 Lcv_a_%=
s_mov_b32 s83,1
s_add_u32 m0,s57,0
s_nop 0
D 0,49,2048
D 0,49,1024
D 0,49,0
s_add_u32 m0,s57,12928
s_nop 0
D 0,55,2048
D 0,55,1024
D 0,55,0
Lcv_a_%=:
s_cmp_eq_u32 s51,2
s_cbranch_scc1 Lcv_a2_%=
L 0
R 88,2,3584
R 92,2,3072
R 96,2,3648
R 100,2,3136
R 104,2,3712
R 108,2,3200
R 112,2,3776
R 116,2,3264
L 6
v_mfma_f32_16x16x32_bf16 v[40:43],a[0:3],v[88:91],0
R 120,2,3840
R 124,2,3328
v_mfma_f32_16x16x32_bf16 v[44:47],a[0:3],v[92:95],0
M 44,a[4:7],88
L 6
M 40,a[8:11],96
R 128,2,3904
R 132,2,3392
M 44,a[8:11],100
M 44,a[12:15],96
L 6
M 40,a[16:19],104
R 136,2,3968
R 140,2,3456
M 44,a[16:19],108
M 44,a[20:23],104
L 6
M 40,a[24:27],112
R 144,2,4032
R 148,2,3520
M 44,a[24:27],116
M 44,a[28:31],112
L 6
M 40,a[32:35],120
M 44,a[32:35],124
M 44,a[36:39],120
L 4
M 40,a[40:43],128
M 44,a[40:43],132
M 44,a[44:47],128
L 2
M 40,a[48:51],136
ds_read_b64 v[24:25],v3 offset:3584
ds_read_b64 v[26:27],v3 offset:3072
M 44,a[48:51],140
M 44,a[52:55],136
L 2
M 40,a[56:59],144
M 44,a[56:59],148
M 44,a[60:63],144
L 0
v_and_b32 v36,0xffff0000,v25
v_lshlrev_b32 v37,16,v25
v_and_b32 v38,0xffff0000,v24
v_lshlrev_b32 v39,16,v24
s_nop 0
v_fma_f32 v40,v36,s86,v40
v_fma_f32 v41,v37,s86,v41
v_fma_f32 v42,v38,s86,v42
v_fma_f32 v43,v39,s86,v43
v_cvt_pk_bf16_f32 v40,v40,v41
v_cvt_pk_bf16_f32 v41,v42,v43
ds_write_b64 v18,v[40:41] offset:0
v_and_b32 v36,0xffff0000,v27
v_lshlrev_b32 v37,16,v27
v_and_b32 v38,0xffff0000,v26
v_lshlrev_b32 v39,16,v26
v_fma_f32 v44,v36,s86,v44
v_fma_f32 v45,v37,s86,v45
v_fma_f32 v46,v38,s86,v46
v_fma_f32 v47,v39,s86,v47
v_cvt_pk_bf16_f32 v44,v44,v45
v_cvt_pk_bf16_f32 v45,v46,v47
ds_write_b64 v18,v[44:45] offset:512
s_branch Lcv_a3_%=
Lcv_a2_%=:
L 0
R 88,2,3584
R 92,2,3072
R 96,2,3648
R 100,2,3136
R 104,2,3712
R 108,2,3200
R 112,2,3776
R 116,2,3264
L 6
v_mfma_f32_16x16x32_bf16 v[40:43],a[0:3],v[88:91],0
R 120,2,3840
R 124,2,3328
v_mfma_f32_16x16x32_bf16 v[44:47],a[0:3],v[92:95],0
M 44,a[4:7],88
L 6
M 40,a[8:11],96
R 128,2,3904
R 132,2,3392
M 44,a[8:11],100
M 44,a[12:15],96
L 6
M 40,a[16:19],104
R 136,2,3968
R 140,2,3456
M 44,a[16:19],108
M 44,a[20:23],104
L 6
M 40,a[24:27],112
R 144,2,4032
R 148,2,3520
M 44,a[24:27],116
M 44,a[28:31],112
L 6
M 40,a[32:35],120
R 152,2,16512
R 156,2,16000
M 44,a[32:35],124
M 44,a[36:39],120
L 6
M 40,a[40:43],128
R 160,2,16576
R 164,2,16064
M 44,a[40:43],132
M 44,a[44:47],128
L 6
M 40,a[48:51],136
R 168,2,16640
R 172,2,16128
M 44,a[48:51],140
ds_read_b64 v[24:25],v3 offset:3584
ds_read_b64 v[26:27],v3 offset:3072
M 44,a[52:55],136
L 8
M 40,a[56:59],144
R 176,2,16704
R 180,2,16192
M 44,a[56:59],148
M 44,a[60:63],144
L 8
v_mfma_f32_16x16x32_bf16 v[48:51],a[64:67],v[152:155],0
R 184,2,16768
R 188,2,16256
v_mfma_f32_16x16x32_bf16 v[52:55],a[64:67],v[156:159],0
M 52,a[68:71],152
L 8
M 48,a[72:75],160
R 192,2,16832
R 196,2,16320
M 52,a[72:75],164
L 6
v_and_b32 v36,0xffff0000,v25
M 52,a[76:79],160
v_lshlrev_b32 v37,16,v25
v_and_b32 v38,0xffff0000,v24
v_lshlrev_b32 v39,16,v24
s_nop 0
M 48,a[80:83],168
R 200,2,16896
R 204,2,16384
M 52,a[80:83],172
v_fma_f32 v40,v36,s86,v40
v_fma_f32 v41,v37,s86,v41
M 52,a[84:87],168
v_fma_f32 v42,v38,s86,v42
v_fma_f32 v43,v39,s86,v43
v_cvt_pk_bf16_f32 v40,v40,v41
L 6
M 48,a[88:91],176
R 208,2,16960
R 212,2,16448
M 52,a[88:91],180
v_cvt_pk_bf16_f32 v41,v42,v43
ds_write_b64 v18,v[40:41] offset:0
M 52,a[92:95],176
v_and_b32 v36,0xffff0000,v27
v_lshlrev_b32 v37,16,v27
v_and_b32 v38,0xffff0000,v26
L 7
M 48,a[96:99],184
M 52,a[96:99],188
v_lshlrev_b32 v39,16,v26
v_fma_f32 v44,v36,s86,v44
M 52,a[100:103],184
v_fma_f32 v45,v37,s86,v45
v_fma_f32 v46,v38,s86,v46
v_fma_f32 v47,v39,s86,v47
L 5
M 48,a[104:107],192
M 52,a[104:107],196
v_cvt_pk_bf16_f32 v44,v44,v45
v_cvt_pk_bf16_f32 v45,v46,v47
M 52,a[108:111],192
ds_write_b64 v18,v[44:45] offset:512
L 4
M 48,a[112:115],200
ds_read_b64 v[24:25],v3 offset:16512
ds_read_b64 v[26:27],v3 offset:16000
M 52,a[112:115],204
M 52,a[116:119],200
L 4
M 48,a[120:123],208
M 52,a[120:123],212
M 52,a[124:127],208
L 0
v_and_b32 v36,0xffff0000,v25
v_lshlrev_b32 v37,16,v25
v_and_b32 v38,0xffff0000,v24
v_lshlrev_b32 v39,16,v24
s_nop 0
v_fma_f32 v48,v36,s87,v48
v_fma_f32 v49,v37,s87,v49
v_fma_f32 v50,v38,s87,v50
v_fma_f32 v51,v39,s87,v51
v_cvt_pk_bf16_f32 v48,v48,v49
v_cvt_pk_bf16_f32 v49,v50,v51
ds_write_b64 v18,v[48:49] offset:1024
v_and_b32 v36,0xffff0000,v27
v_lshlrev_b32 v37,16,v27
v_and_b32 v38,0xffff0000,v26
v_lshlrev_b32 v39,16,v26
v_fma_f32 v52,v36,s87,v52
v_fma_f32 v53,v37,s87,v53
v_fma_f32 v54,v38,s87,v54
v_fma_f32 v55,v39,s87,v55
v_cvt_pk_bf16_f32 v52,v52,v53
v_cvt_pk_bf16_f32 v53,v54,v55
ds_write_b64 v18,v[52:53] offset:1536
Lcv_a3_%=:
L 0
s_barrier
R 152,6,0
R 156,6,512
s_mov_b32 s78,98566144
L 1
buffer_store_dwordx4 v[152:155],v7,s[36:39],s78 offen offset:0 sc0 sc1
s_add_u32 s78,s56,99090432
L 0
buffer_store_dwordx4 v[156:159],v7,s[36:39],s78 offen offset:0 sc0 sc1
s_cmp_eq_u32 s83,1
s_cbranch_scc1 Lcv_b_%=
s_waitcnt vmcnt(0)
s_barrier
buffer_store_dword v23,v10,s[36:39],s48 offen offset:0 sc0 sc1
buffer_load_dword v13,v9,s[36:39],s58 offen offset:0 sc1
Lcv_bpb_%=:
)"
R"(s_waitcnt vmcnt(0)
v_cmp_eq_u32 vcc,s53,v13
s_add_u32 s82,s82,1
s_cmp_eq_u64 vcc,-1
s_cbranch_scc1 Lcv_bqb_%=
s_sleep 1
buffer_load_dword v13,v9,s[36:39],s58 offen offset:0 sc1
s_cmp_lt_u32 s82,20000
s_cbranch_scc1 Lcv_bpb_%=
s_waitcnt vmcnt(0)
Lcv_bqb_%=:
s_add_u32 m0,s57,0
s_nop 0
D 0,49,2048
D 0,49,1024
D 0,49,0
s_add_u32 m0,s57,12928
s_nop 0
D 0,55,2048
D 0,55,1024
D 0,55,0
s_waitcnt vmcnt(0)
Lcv_b_%=:
s_waitcnt vmcnt(2)
ds_read2_b32 v[88:89],v1 offset0:0 offset1:1
ds_read2_b32 v[90:91],v1 offset0:2 offset1:3
R 120,2,3584
ds_read2_b32 v[92:93],v1 offset0:128 offset1:129
ds_read2_b32 v[94:95],v1 offset0:130 offset1:131
R 124,2,3072
ds_read2_b32 v[96:97],v14 offset0:0 offset1:1
ds_read2_b32 v[98:99],v14 offset0:2 offset1:3
R 128,2,2560
ds_read2_b32 v[100:101],v14 offset0:128 offset1:129
ds_read2_b32 v[102:103],v14 offset0:130 offset1:131
R 132,2,2048
ds_read2_b32 v[104:105],v15 offset0:0 offset1:1
ds_read2_b32 v[106:107],v15 offset0:2 offset1:3
R 136,2,1536
ds_read2_b32 v[108:109],v15 offset0:128 offset1:129
ds_read2_b32 v[110:111],v15 offset0:130 offset1:131
R 140,2,1024
ds_read2_b32 v[112:113],v16 offset0:0 offset1:1
ds_read2_b32 v[114:115],v16 offset0:2 offset1:3
R 144,2,512
ds_read2_b32 v[116:117],v16 offset0:128 offset1:129
ds_read2_b32 v[118:119],v16 offset0:130 offset1:131
R 148,2,0
L 15
v_mfma_f32_16x16x32_bf16 v[40:43],v[88:91],v[128:131],0
ds_read2_b32 v[152:153],v1 offset0:16 offset1:17
M 40,v[92:95],124
ds_read2_b32 v[154:155],v1 offset0:18 offset1:19
M 40,v[96:99],120
L 14
R 184,2,3648
v_mfma_f32_16x16x32_bf16 v[44:47],v[88:91],v[132:135],0
ds_read2_b32 v[156:157],v1 offset0:144 offset1:145
M 44,v[92:95],128
M 44,v[96:99],124
ds_read2_b32 v[158:159],v1 offset0:146 offset1:147
M 44,v[100:103],120
L 14
R 188,2,3136
v_mfma_f32_16x16x32_bf16 v[48:51],v[88:91],v[136:139],0
ds_read2_b32 v[160:161],v14 offset0:16 offset1:17
M 48,v[92:95],132
ds_read2_b32 v[162:163],v14 offset0:18 offset1:19
M 48,v[96:99],128
M 48,v[100:103],124
R 192,2,2624
M 48,v[104:107],120
L 15
ds_read2_b32 v[164:165],v14 offset0:144 offset1:145
v_mfma_f32_16x16x32_bf16 v[52:55],v[88:91],v[140:143],0
ds_read2_b32 v[166:167],v14 offset0:146 offset1:147
M 52,v[92:95],136
R 196,2,2112
M 52,v[96:99],132
M 52,v[100:103],128
ds_read2_b32 v[168:169],v15 offset0:16 offset1:17
M 52,v[104:107],124
ds_read2_b32 v[170:171],v15 offset0:18 offset1:19
M 52,v[108:111],120
L 15
R 200,2,1600
v_mfma_f32_16x16x32_bf16 v[56:59],v[88:91],v[144:147],0
ds_read2_b32 v[172:173],v15 offset0:144 offset1:145
M 56,v[92:95],140
M 56,v[96:99],136
ds_read2_b32 v[174:175],v15 offset0:146 offset1:147
M 56,v[100:103],132
R 204,2,1088
M 56,v[104:107],128
ds_read2_b32 v[176:177],v16 offset0:16 offset1:17
M 56,v[108:111],124
ds_read2_b32 v[178:179],v16 offset0:18 offset1:19
M 56,v[112:115],120
L 15
v_mfma_f32_16x16x32_bf16 v[60:63],v[88:91],v[148:151],0
R 208,2,576
M 60,v[92:95],144
ds_read2_b32 v[180:181],v16 offset0:144 offset1:145
M 60,v[96:99],140
ds_read2_b32 v[182:183],v16 offset0:146 offset1:147
M 60,v[100:103],136
R 212,2,64
M 60,v[104:107],132
s_cmp_eq_u32 s83,0
s_cbranch_scc1 Lcv_c_%=
s_waitcnt vmcnt(0)
s_barrier
buffer_store_dword v23,v10,s[36:39],s48 offen offset:0 sc0 sc1
Lcv_c_%=:
M 60,v[108:111],128
M 60,v[112:115],124
M 60,v[116:119],120
L 15
M 40,v[152:155],192
ds_read2_b32 v[88:89],v1 offset0:32 offset1:33
M 40,v[156:159],188
ds_read2_b32 v[90:91],v1 offset0:34 offset1:35
M 40,v[160:163],184
L 14
R 120,2,3712
M 44,v[152:155],196
ds_read2_b32 v[92:93],v1 offset0:160 offset1:161
M 44,v[156:159],192
M 44,v[160:163],188
ds_read2_b32 v[94:95],v1 offset0:162 offset1:163
M 44,v[164:167],184
L 14
R 124,2,3200
M 48,v[152:155],200
ds_read2_b32 v[96:97],v14 offset0:32 offset1:33
M 48,v[156:159],196
ds_read2_b32 v[98:99],v14 offset0:34 offset1:35
M 48,v[160:163],192
M 48,v[164:167],188
R 128,2,2688
M 48,v[168:171],184
L 15
ds_read2_b32 v[100:101],v14 offset0:160 offset1:161
M 52,v[152:155],204
ds_read2_b32 v[102:103],v14 offset0:162 offset1:163
M 52,v[156:159],200
R 132,2,2176
M 52,v[160:163],196
M 52,v[164:167],192
ds_read2_b32 v[104:105],v15 offset0:32 offset1:33
M 52,v[168:171],188
ds_read2_b32 v[106:107],v15 offset0:34 offset1:35
M 52,v[172:175],184
L 15
R 136,2,1664
M 56,v[152:155],208
ds_read2_b32 v[108:109],v15 offset0:160 offset1:161
M 56,v[156:159],204
M 56,v[160:163],200
ds_read2_b32 v[110:111],v15 offset0:162 offset1:163
M 56,v[164:167],196
R 140,2,1152
M 56,v[168:171],192
ds_read2_b32 v[112:113],v16 offset0:32 offset1:33
M 56,v[172:175],188
ds_read2_b32 v[114:115],v16 offset0:34 offset1:35
M 56,v[176:179],184
L 15
M 60,v[152:155],212
R 144,2,640
M 60,v[156:159],208
ds_read2_b32 v[116:117],v16 offset0:160 offset1:161
M 60,v[160:163],204
ds_read2_b32 v[118:119],v16 offset0:162 offset1:163
M 60,v[164:167],200
R 148,2,128
M 60,v[168:171],196
M 60,v[172:175],192
M 60,v[176:179],188
M 60,v[180:183],184
L 15
M 40,v[88:91],128
ds_read2_b32 v[152:153],v1 offset0:48 offset1:49
M 40,v[92:95],124
ds_read2_b32 v[154:155],v1 offset0:50 offset1:51
M 40,v[96:99],120
L 14
R 184,2,3776
M 44,v[88:91],132
ds_read2_b32 v[156:157],v1 offset0:176 offset1:177
M 44,v[92:95],128
M 44,v[96:99],124
ds_read2_b32 v[158:159],v1 offset0:178 offset1:179
M 44,v[100:103],120
L 14
R 188,2,3264
M 48,v[88:91],136
ds_read2_b32 v[160:161],v14 offset0:48 offset1:49
M 48,v[92:95],132
ds_read2_b32 v[162:163],v14 offset0:50 offset1:51
M 48,v[96:99],128
M 48,v[100:103],124
R 192,2,2752
M 48,v[104:107],120
L 15
ds_read2_b32 v[164:165],v14 offset0:176 offset1:177
M 52,v[88:91],140
s_mov_b32 m0,163584
s_mov_b32 s78,0
ds_read2_b32 v[166:167],v14 offset0:178 offset1:179
M 52,v[92:95],136
R 196,2,2240
M 52,v[96:99],132
buffer_load_dword v11,s[40:43],s78 offen offset:0  lds
M 52,v[100:103],128
ds_read2_b32 v[168:169],v15 offset0:48 offset1:49
M 52,v[104:107],124
ds_read2_b32 v[170:171],v15 offset0:50 offset1:51
M 52,v[108:111],120
L 15
R 200,2,1728
M 56,v[88:91],144
ds_read2_b32 v[172:173],v15 offset0:176 offset1:177
M 56,v[92:95],140
M 56,v[96:99],136
ds_read2_b32 v[174:175],v15 offset0:178 offset1:179
M 56,v[100:103],132
R 204,2,1216
M 56,v[104:107],128
ds_read2_b32 v[176:177],v16 offset0:48 offset1:49
M 56,v[108:111],124
ds_read2_b32 v[178:179],v16 offset0:50 offset1:51
M 56,v[112:115],120
L 15
M 60,v[88:91],148
R 208,2,704
M 60,v[92:95],144
ds_read2_b32 v[180:181],v16 offset0:176 offset1:177
M 60,v[96:99],140
ds_read2_b32 v[182:183],v16 offset0:178 offset1:179
M 60,v[100:103],136
R 212,2,192
M 60,v[104:107],132
M 60,v[108:111],128
M 60,v[112:115],124
M 60,v[116:119],120
L 15
M 40,v[152:155],192
ds_read2_b32 v[88:89],v1 offset0:64 offset1:65
M 40,v[156:159],188
ds_read2_b32 v[90:91],v1 offset0:66 offset1:67
M 40,v[160:163],184
L 14
R 120,2,3840
M 44,v[152:155],196
ds_read2_b32 v[92:93],v1 offset0:192 offset1:193
M 44,v[156:159],192
M 44,v[160:163],188
ds_read2_b32 v[94:95],v1 offset0:194 offset1:195
M 44,v[164:167],184
L 14
R 124,2,3328
M 48,v[152:155],200
ds_read2_b32 v[96:97],v14 offset0:64 offset1:65
M 48,v[156:159],196
ds_read2_b32 v[98:99],v14 offset0:66 offset1:67
M 48,v[160:163],192
M 48,v[164:167],188
R 128,2,2816
M 48,v[168:171],184
L 15
ds_read2_b32 v[100:101],v14 offset0:192 offset1:193
M 52,v[152:155],204
ds_read2_b32 v[102:103],v14 offset0:194 offset1:195
M 52,v[156:159],200
R 132,2,2304
M 52,v[160:163],196
M 52,v[164:167],192
ds_read2_b32 v[104:105],v15 offset0:64 offset1:65
M 52,v[168:171],188
ds_read2_b32 v[106:107],v15 offset0:66 offset1:67
M 52,v[172:175],184
L 15
R 136,2,1792
M 56,v[152:155],208
ds_read2_b32 v[108:109],v15 offset0:192 offset1:193
M 56,v[156:159],204
M 56,v[160:163],200
ds_read2_b32 v[110:111],v15 offset0:194 offset1:195
M 56,v[164:167],196
R 140,2,1280
M 56,v[168:171],192
ds_read2_b32 v[112:113],v16 offset0:64 offset1:65
M 56,v[172:175],188
ds_read2_b32 v[114:115],v16 offset0:66 offset1:67
M 56,v[176:179],184
L 15
M 60,v[152:155],212
R 144,2,768
M 60,v[156:159],208
ds_read2_b32 v[116:117],v16 offset0:192 offset1:193
M 60,v[160:163],204
ds_read2_b32 v[118:119],v16 offset0:194 offset1:195
M 60,v[164:167],200
R 148,2,256
M 60,v[168:171],196
M 60,v[172:175],192
M 60,v[176:179],188
M 60,v[180:183],184
L 15
M 40,v[88:91],128
ds_read2_b32 v[152:153],v1 offset0:80 offset1:81
M 40,v[92:95],124
ds_read2_b32 v[154:155],v1 offset0:82 offset1:83
M 40,v[96:99],120
L 14
R 184,2,3904
M 44,v[88:91],132
ds_read2_b32 v[156:157],v1 offset0:208 offset1:209
M 44,v[92:95],128
M 44,v[96:99],124
ds_read2_b32 v[158:159],v1 offset0:210 offset1:211
M 44,v[100:103],120
L 14
R 188,2,3392
M 48,v[88:91],136
ds_read2_b32 v[160:161],v14 offset0:80 offset1:81
M 48,v[92:95],132
ds_read2_b32 v[162:163],v14 offset0:82 offset1:83
M 48,v[96:99],128
M 48,v[100:103],124
R 192,2,2880
M 48,v[104:107],120
L 15
ds_read2_b32 v[164:165],v14 offset0:208 offset1:209
M 52,v[88:91],140
ds_read2_b32 v[166:167],v14 offset0:210 offset1:211
M 52,v[92:95],136
R 196,2,2368
M 52,v[96:99],132
M 52,v[100:103],128
ds_read2_b32 v[168:169],v15 offset0:80 offset1:81
M 52,v[104:107],124
ds_read2_b32 v[170:171],v15 offset0:82 offset1:83
M 52,v[108:111],120
L 15
R 200,2,1856
M 56,v[88:91],144
ds_read2_b32 v[172:173],v15 offset0:208 offset1:209
M 56,v[92:95],140
M 56,v[96:99],136
ds_read2_b32 v[174:175],v15 offset0:210 offset1:211
M 56,v[100:103],132
R 204,2,1344
M 56,v[104:107],128
ds_read2_b32 v[176:177],v16 offset0:80 offset1:81
M 56,v[108:111],124
ds_read2_b32 v[178:179],v16 offset0:82 offset1:83
M 56,v[112:115],120
L 15
M 60,v[88:91],148
R 208,2,832
M 60,v[92:95],144
ds_read2_b32 v[180:181],v16 offset0:208 offset1:209
M 60,v[96:99],140
ds_read2_b32 v[182:183],v16 offset0:210 offset1:211
M 60,v[100:103],136
R 212,2,320
M 60,v[104:107],132
M 60,v[108:111],128
M 60,v[112:115],124
M 60,v[116:119],120
L 15
M 40,v[152:155],192
ds_read2_b32 v[88:89],v1 offset0:96 offset1:97
M 40,v[156:159],188
ds_read2_b32 v[90:91],v1 offset0:98 offset1:99
M 40,v[160:163],184
L 14
R 120,2,3968
M 44,v[152:155],196
s_mov_b32 m0,163584
s_mov_b32 s78,3741696
ds_read2_b32 v[92:93],v1 offset0:224 offset1:225
M 44,v[156:159],192
M 44,v[160:163],188
ds_read2_b32 v[94:95],v1 offset0:226 offset1:227
M 44,v[164:167],184
L 14
R 124,2,3456
M 48,v[152:155],200
ds_read2_b32 v[96:97],v14 offset0:96 offset1:97
M 48,v[156:159],196
ds_read2_b32 v[98:99],v14 offset0:98 offset1:99
M 48,v[160:163],192
buffer_load_dword v11,s[40:43],s78 offen offset:0  lds
M 48,v[164:167],188
R 128,2,2944
M 48,v[168:171],184
L 15
ds_read2_b32 v[100:101],v14 offset0:224 offset1:225
M 52,v[152:155],204
ds_read2_b32 v[102:103],v14 offset0:226 offset1:227
M 52,v[156:159],200
R 132,2,2432
M 52,v[160:163],196
M 52,v[164:167],192
ds_read2_b32 v[104:105],v15 offset0:96 offset1:97
M 52,v[168:171],188
ds_read2_b32 v[106:107],v15 offset0:98 offset1:99
M 52,v[172:175],184
L 15
R 136,2,1920
M 56,v[152:155],208
ds_read2_b32 v[108:109],v15 offset0:224 offset1:225
M 56,v[156:159],204
M 56,v[160:163],200
ds_read2_b32 v[110:111],v15 offset0:226 offset1:227
M 56,v[164:167],196
R 140,2,1408
M 56,v[168:171],192
ds_read2_b32 v[112:113],v16 offset0:96 offset1:97
M 56,v[172:175],188
ds_read2_b32 v[114:115],v16 offset0:98 offset1:99
M 56,v[176:179],184
L 15
M 60,v[152:155],212
R 144,2,896
M 60,v[156:159],208
ds_read2_b32 v[116:117],v16 offset0:224 offset1:225
M 60,v[160:163],204
ds_read2_b32 v[118:119],v16 offset0:226 offset1:227
M 60,v[164:167],200
R 148,2,384
M 60,v[168:171],196
M 60,v[172:175],192
M 60,v[176:179],188
M 60,v[180:183],184
L 15
M 40,v[88:91],128
ds_read2_b32 v[152:153],v1 offset0:112 offset1:113
M 40,v[92:95],124
ds_read2_b32 v[154:155],v1 offset0:114 offset1:115
M 40,v[96:99],120
L 14
R 184,2,4032
M 44,v[88:91],132
ds_read2_b32 v[156:157],v1 offset0:240 offset1:241
M 44,v[92:95],128
M 44,v[96:99],124
ds_read2_b32 v[158:159],v1 offset0:242 offset1:243
v_add_u32 v1,12928,v1
M 44,v[100:103],120
L 14
R 188,2,3520
M 48,v[88:91],136
ds_read2_b32 v[160:161],v14 offset0:112 offset1:113
M 48,v[92:95],132
ds_read2_b32 v[162:163],v14 offset0:114 offset1:115
M 48,v[96:99],128
M 48,v[100:103],124
R 192,2,3008
M 48,v[104:107],120
L 15
ds_read2_b32 v[164:165],v14 offset0:240 offset1:241
M 52,v[88:91],140
ds_read2_b32 v[166:167],v14 offset0:242 offset1:243
v_add_u32 v14,12928,v14
M 52,v[92:95],136
R 196,2,2496
M 52,v[96:99],132
M 52,v[100:103],128
ds_read2_b32 v[168:169],v15 offset0:112 offset1:113
M 52,v[104:107],124
ds_read2_b32 v[170:171],v15 offset0:114 offset1:115
M 52,v[108:111],120
L 15
R 200,2,1984
M 56,v[88:91],144
ds_read2_b32 v[172:173],v15 offset0:240 offset1:241
M 56,v[92:95],140
M 56,v[96:99],136
ds_read2_b32 v[174:175],v15 offset0:242 offset1:243
v_add_u32 v15,12928,v15
M 56,v[100:103],132
R 204,2,1472
M 56,v[104:107],128
ds_read2_b32 v[176:177],v16 offset0:112 offset1:113
M 56,v[108:111],124
ds_read2_b32 v[178:179],v16 offset0:114 offset1:115
M 56,v[112:115],120
L 15
M 60,v[88:91],148
R 208,2,960
M 60,v[92:95],144
ds_read2_b32 v[180:181],v16 offset0:240 offset1:241
M 60,v[96:99],140
ds_read2_b32 v[182:183],v16 offset0:242 offset1:243
v_add_u32 v16,12928,v16
M 60,v[100:103],136
)"
R"(R 212,2,448
M 60,v[104:107],132
M 60,v[108:111],128
M 60,v[112:115],124
M 60,v[116:119],120
L 15
M 40,v[152:155],192
ds_read2_b32 v[88:89],v1 offset0:0 offset1:1
M 40,v[156:159],188
ds_read2_b32 v[90:91],v1 offset0:2 offset1:3
M 40,v[160:163],184
L 14
R 120,2,16512
M 44,v[152:155],196
ds_read2_b32 v[92:93],v1 offset0:128 offset1:129
M 44,v[156:159],192
M 44,v[160:163],188
ds_read2_b32 v[94:95],v1 offset0:130 offset1:131
M 44,v[164:167],184
L 14
R 124,2,16000
M 48,v[152:155],200
ds_read2_b32 v[96:97],v14 offset0:0 offset1:1
M 48,v[156:159],196
ds_read2_b32 v[98:99],v14 offset0:2 offset1:3
M 48,v[160:163],192
M 48,v[164:167],188
R 128,2,15488
M 48,v[168:171],184
L 15
ds_read2_b32 v[100:101],v14 offset0:128 offset1:129
M 52,v[152:155],204
ds_read2_b32 v[102:103],v14 offset0:130 offset1:131
M 52,v[156:159],200
R 132,2,14976
M 52,v[160:163],196
M 52,v[164:167],192
ds_read2_b32 v[104:105],v15 offset0:0 offset1:1
M 52,v[168:171],188
ds_read2_b32 v[106:107],v15 offset0:2 offset1:3
M 52,v[172:175],184
L 15
R 136,2,14464
M 56,v[152:155],208
ds_read2_b32 v[108:109],v15 offset0:128 offset1:129
M 56,v[156:159],204
M 56,v[160:163],200
ds_read2_b32 v[110:111],v15 offset0:130 offset1:131
M 56,v[164:167],196
R 140,2,13952
M 56,v[168:171],192
ds_read2_b32 v[112:113],v16 offset0:0 offset1:1
M 56,v[172:175],188
ds_read2_b32 v[114:115],v16 offset0:2 offset1:3
M 56,v[176:179],184
L 15
M 60,v[152:155],212
R 144,2,13440
M 60,v[156:159],208
ds_read2_b32 v[116:117],v16 offset0:128 offset1:129
M 60,v[160:163],204
ds_read2_b32 v[118:119],v16 offset0:130 offset1:131
M 60,v[164:167],200
R 148,2,12928
M 60,v[168:171],196
M 60,v[172:175],192
M 60,v[176:179],188
M 60,v[180:183],184
s_cmp_eq_u32 s51,2
s_cbranch_scc1 Lcv_d_%=
ds_read_b64 v[24:25],v3 offset:2560
ds_read_b64 v[26:27],v3 offset:2048
ds_read_b64 v[28:29],v3 offset:1536
ds_read_b64 v[30:31],v3 offset:1024
ds_read_b64 v[32:33],v3 offset:512
ds_read_b64 v[34:35],v3 offset:0
L 0
v_and_b32 v36,0xffff0000,v25
v_lshlrev_b32 v37,16,v25
v_and_b32 v38,0xffff0000,v24
v_lshlrev_b32 v39,16,v24
v_fma_f32 v40,v36,s86,v40
v_fma_f32 v41,v37,s86,v41
v_fma_f32 v42,v38,s86,v42
v_fma_f32 v43,v39,s86,v43
v_cvt_pk_bf16_f32 v40,v40,v41
v_cvt_pk_bf16_f32 v41,v42,v43
ds_write_b64 v4,v[40:41] offset:0
v_and_b32 v36,0xffff0000,v27
v_lshlrev_b32 v37,16,v27
v_and_b32 v38,0xffff0000,v26
v_lshlrev_b32 v39,16,v26
v_fma_f32 v44,v36,s86,v44
v_fma_f32 v45,v37,s86,v45
v_fma_f32 v46,v38,s86,v46
v_fma_f32 v47,v39,s86,v47
v_cvt_pk_bf16_f32 v44,v44,v45
v_cvt_pk_bf16_f32 v45,v46,v47
ds_write_b64 v4,v[44:45] offset:512
v_and_b32 v36,0xffff0000,v29
v_lshlrev_b32 v37,16,v29
v_and_b32 v38,0xffff0000,v28
v_lshlrev_b32 v39,16,v28
v_fma_f32 v48,v36,s86,v48
v_fma_f32 v49,v37,s86,v49
v_fma_f32 v50,v38,s86,v50
v_fma_f32 v51,v39,s86,v51
v_cvt_pk_bf16_f32 v48,v48,v49
v_cvt_pk_bf16_f32 v49,v50,v51
ds_write_b64 v4,v[48:49] offset:1024
v_and_b32 v36,0xffff0000,v31
v_lshlrev_b32 v37,16,v31
v_and_b32 v38,0xffff0000,v30
v_lshlrev_b32 v39,16,v30
v_fma_f32 v52,v36,s86,v52
v_fma_f32 v53,v37,s86,v53
v_fma_f32 v54,v38,s86,v54
v_fma_f32 v55,v39,s86,v55
v_cvt_pk_bf16_f32 v52,v52,v53
v_cvt_pk_bf16_f32 v53,v54,v55
ds_write_b64 v4,v[52:53] offset:1536
v_and_b32 v36,0xffff0000,v33
v_lshlrev_b32 v37,16,v33
v_and_b32 v38,0xffff0000,v32
v_lshlrev_b32 v39,16,v32
v_fma_f32 v56,v36,s86,v56
v_fma_f32 v57,v37,s86,v57
v_fma_f32 v58,v38,s86,v58
v_fma_f32 v59,v39,s86,v59
v_cvt_pk_bf16_f32 v56,v56,v57
v_cvt_pk_bf16_f32 v57,v58,v59
ds_write_b64 v4,v[56:57] offset:2048
v_and_b32 v36,0xffff0000,v35
v_lshlrev_b32 v37,16,v35
v_and_b32 v38,0xffff0000,v34
v_lshlrev_b32 v39,16,v34
v_fma_f32 v60,v36,s86,v60
v_fma_f32 v61,v37,s86,v61
v_fma_f32 v62,v38,s86,v62
v_fma_f32 v63,v39,s86,v63
v_cvt_pk_bf16_f32 v60,v60,v61
v_cvt_pk_bf16_f32 v61,v62,v63
ds_write_b64 v4,v[60:61] offset:2560
s_branch Lcv_e_%=
Lcv_d_%=:
L 15
v_mfma_f32_16x16x32_bf16 v[64:67],v[88:91],v[128:131],0
ds_read2_b32 v[152:153],v1 offset0:16 offset1:17
ds_read_b64 v[24:25],v3 offset:2560
M 64,v[92:95],124
ds_read2_b32 v[154:155],v1 offset0:18 offset1:19
ds_read_b64 v[26:27],v3 offset:2048
M 64,v[96:99],120
L 15
R 184,2,16576
ds_read_b64 v[28:29],v3 offset:1536
v_mfma_f32_16x16x32_bf16 v[68:71],v[88:91],v[132:135],0
ds_read2_b32 v[156:157],v1 offset0:144 offset1:145
ds_read_b64 v[30:31],v3 offset:1024
M 68,v[92:95],128
ds_read_b64 v[32:33],v3 offset:512
ds_read_b64 v[34:35],v3 offset:0
M 68,v[96:99],124
ds_read2_b32 v[158:159],v1 offset0:146 offset1:147
M 68,v[100:103],120
L 15
R 188,2,16064
v_mfma_f32_16x16x32_bf16 v[72:75],v[88:91],v[136:139],0
ds_read2_b32 v[160:161],v14 offset0:16 offset1:17
M 72,v[92:95],132
s_mov_b32 m0,163584
s_mov_b32 s78,0
ds_read2_b32 v[162:163],v14 offset0:18 offset1:19
M 72,v[96:99],128
M 72,v[100:103],124
L 4
R 192,2,15552
v_and_b32 v36,0xffff0000,v25
M 72,v[104:107],120
s_nop 0
ds_read2_b32 v[164:165],v14 offset0:144 offset1:145
v_lshlrev_b32 v37,16,v25
v_mfma_f32_16x16x32_bf16 v[76:79],v[88:91],v[140:143],0
ds_read2_b32 v[166:167],v14 offset0:146 offset1:147
v_and_b32 v38,0xffff0000,v24
M 76,v[92:95],136
R 196,2,15040
v_lshlrev_b32 v39,16,v24
M 76,v[96:99],132
buffer_load_dword v12,s[60:63],s78 offen offset:0  lds
M 76,v[100:103],128
ds_read2_b32 v[168:169],v15 offset0:16 offset1:17
v_fma_f32 v40,v36,s86,v40
M 76,v[104:107],124
ds_read2_b32 v[170:171],v15 offset0:18 offset1:19
v_fma_f32 v41,v37,s86,v41
M 76,v[108:111],120
s_nop 0
R 200,2,14528
v_fma_f32 v42,v38,s86,v42
v_mfma_f32_16x16x32_bf16 v[80:83],v[88:91],v[144:147],0
ds_read2_b32 v[172:173],v15 offset0:144 offset1:145
v_fma_f32 v43,v39,s86,v43
M 80,v[92:95],140
v_cvt_pk_bf16_f32 v40,v40,v41
v_cvt_pk_bf16_f32 v41,v42,v43
M 80,v[96:99],136
ds_read2_b32 v[174:175],v15 offset0:146 offset1:147
v_and_b32 v36,0xffff0000,v27
M 80,v[100:103],132
R 204,2,14016
v_lshlrev_b32 v37,16,v27
M 80,v[104:107],128
ds_read2_b32 v[176:177],v16 offset0:16 offset1:17
v_and_b32 v38,0xffff0000,v26
M 80,v[108:111],124
ds_read2_b32 v[178:179],v16 offset0:18 offset1:19
v_lshlrev_b32 v39,16,v26
M 80,v[112:115],120
s_nop 0
ds_write_b64 v4,v[40:41] offset:0
v_mfma_f32_16x16x32_bf16 v[84:87],v[88:91],v[148:151],0
R 208,2,13504
v_fma_f32 v44,v36,s86,v44
M 84,v[92:95],144
ds_read2_b32 v[180:181],v16 offset0:144 offset1:145
v_fma_f32 v45,v37,s86,v45
M 84,v[96:99],140
ds_read2_b32 v[182:183],v16 offset0:146 offset1:147
v_fma_f32 v46,v38,s86,v46
M 84,v[100:103],136
R 212,2,12992
v_fma_f32 v47,v39,s86,v47
M 84,v[104:107],132
v_cvt_pk_bf16_f32 v44,v44,v45
v_cvt_pk_bf16_f32 v45,v46,v47
M 84,v[108:111],128
ds_write_b64 v4,v[44:45] offset:512
M 84,v[112:115],124
v_and_b32 v36,0xffff0000,v29
v_lshlrev_b32 v37,16,v29
M 84,v[116:119],120
L 15
v_and_b32 v38,0xffff0000,v28
v_lshlrev_b32 v39,16,v28
M 64,v[152:155],192
ds_read2_b32 v[88:89],v1 offset0:32 offset1:33
v_fma_f32 v48,v36,s86,v48
M 64,v[156:159],188
ds_read2_b32 v[90:91],v1 offset0:34 offset1:35
v_fma_f32 v49,v37,s86,v49
M 64,v[160:163],184
L 15
R 120,2,16640
v_fma_f32 v50,v38,s86,v50
M 68,v[152:155],196
ds_read2_b32 v[92:93],v1 offset0:160 offset1:161
v_fma_f32 v51,v39,s86,v51
M 68,v[156:159],192
v_cvt_pk_bf16_f32 v48,v48,v49
v_cvt_pk_bf16_f32 v49,v50,v51
M 68,v[160:163],188
ds_read2_b32 v[94:95],v1 offset0:162 offset1:163
v_and_b32 v36,0xffff0000,v31
M 68,v[164:167],184
L 15
R 124,2,16128
v_lshlrev_b32 v37,16,v31
M 72,v[152:155],200
ds_read2_b32 v[96:97],v14 offset0:32 offset1:33
v_and_b32 v38,0xffff0000,v30
M 72,v[156:159],196
ds_read2_b32 v[98:99],v14 offset0:34 offset1:35
v_lshlrev_b32 v39,16,v30
M 72,v[160:163],192
ds_write_b64 v4,v[48:49] offset:1024
M 72,v[164:167],188
R 128,2,15616
v_fma_f32 v52,v36,s86,v52
M 72,v[168:171],184
L 15
ds_read2_b32 v[100:101],v14 offset0:160 offset1:161
v_fma_f32 v53,v37,s86,v53
M 76,v[152:155],204
ds_read2_b32 v[102:103],v14 offset0:162 offset1:163
v_fma_f32 v54,v38,s86,v54
M 76,v[156:159],200
R 132,2,15104
v_fma_f32 v55,v39,s86,v55
M 76,v[160:163],196
v_cvt_pk_bf16_f32 v52,v52,v53
v_cvt_pk_bf16_f32 v53,v54,v55
M 76,v[164:167],192
ds_read2_b32 v[104:105],v15 offset0:32 offset1:33
v_and_b32 v36,0xffff0000,v33
M 76,v[168:171],188
ds_read2_b32 v[106:107],v15 offset0:34 offset1:35
v_lshlrev_b32 v37,16,v33
M 76,v[172:175],184
L 15
R 136,2,14592
v_and_b32 v38,0xffff0000,v32
M 80,v[152:155],208
ds_read2_b32 v[108:109],v15 offset0:160 offset1:161
v_lshlrev_b32 v39,16,v32
M 80,v[156:159],204
ds_write_b64 v4,v[52:53] offset:1536
M 80,v[160:163],200
ds_read2_b32 v[110:111],v15 offset0:162 offset1:163
v_fma_f32 v56,v36,s86,v56
M 80,v[164:167],196
R 140,2,14080
v_fma_f32 v57,v37,s86,v57
M 80,v[168:171],192
ds_read2_b32 v[112:113],v16 offset0:32 offset1:33
v_fma_f32 v58,v38,s86,v58
M 80,v[172:175],188
ds_read2_b32 v[114:115],v16 offset0:34 offset1:35
v_fma_f32 v59,v39,s86,v59
M 80,v[176:179],184
L 15
v_cvt_pk_bf16_f32 v56,v56,v57
v_cvt_pk_bf16_f32 v57,v58,v59
M 84,v[152:155],212
R 144,2,13568
v_and_b32 v36,0xffff0000,v35
M 84,v[156:159],208
ds_read2_b32 v[116:117],v16 offset0:160 offset1:161
v_lshlrev_b32 v37,16,v35
M 84,v[160:163],204
ds_read2_b32 v[118:119],v16 offset0:162 offset1:163
v_and_b32 v38,0xffff0000,v34
M 84,v[164:167],200
R 148,2,13056
v_lshlrev_b32 v39,16,v34
M 84,v[168:171],196
ds_write_b64 v4,v[56:57] offset:2048
M 84,v[172:175],192
v_fma_f32 v60,v36,s86,v60
v_fma_f32 v61,v37,s86,v61
M 84,v[176:179],188
v_fma_f32 v62,v38,s86,v62
v_fma_f32 v63,v39,s86,v63
M 84,v[180:183],184
L 15
v_cvt_pk_bf16_f32 v60,v60,v61
v_cvt_pk_bf16_f32 v61,v62,v63
M 64,v[88:91],128
ds_read2_b32 v[152:153],v1 offset0:48 offset1:49
M 64,v[92:95],124
ds_read2_b32 v[154:155],v1 offset0:50 offset1:51
M 64,v[96:99],120
L 15
R 184,2,16704
M 68,v[88:91],132
ds_read2_b32 v[156:157],v1 offset0:176 offset1:177
M 68,v[92:95],128
ds_write_b64 v4,v[60:61] offset:2560
M 68,v[96:99],124
ds_read2_b32 v[158:159],v1 offset0:178 offset1:179
M 68,v[100:103],120
L 15
R 188,2,16192
M 72,v[88:91],136
ds_read2_b32 v[160:161],v14 offset0:48 offset1:49
M 72,v[92:95],132
ds_read2_b32 v[162:163],v14 offset0:50 offset1:51
M 72,v[96:99],128
M 72,v[100:103],124
R 192,2,15680
M 72,v[104:107],120
L 15
ds_read2_b32 v[164:165],v14 offset0:176 offset1:177
M 76,v[88:91],140
ds_read2_b32 v[166:167],v14 offset0:178 offset1:179
M 76,v[92:95],136
R 196,2,15168
M 76,v[96:99],132
M 76,v[100:103],128
ds_read2_b32 v[168:169],v15 offset0:48 offset1:49
M 76,v[104:107],124
ds_read2_b32 v[170:171],v15 offset0:50 offset1:51
M 76,v[108:111],120
L 15
R 200,2,14656
M 80,v[88:91],144
ds_read2_b32 v[172:173],v15 offset0:176 offset1:177
M 80,v[92:95],140
M 80,v[96:99],136
ds_read2_b32 v[174:175],v15 offset0:178 offset1:179
M 80,v[100:103],132
R 204,2,14144
M 80,v[104:107],128
s_mov_b32 m0,163584
s_mov_b32 s78,1847296
ds_read2_b32 v[176:177],v16 offset0:48 offset1:49
M 80,v[108:111],124
ds_read2_b32 v[178:179],v16 offset0:50 offset1:51
M 80,v[112:115],120
L 15
buffer_load_dword v12,s[60:63],s78 offen offset:0  lds
M 84,v[88:91],148
R 208,2,13632
M 84,v[92:95],144
ds_read2_b32 v[180:181],v16 offset0:176 offset1:177
M 84,v[96:99],140
ds_read2_b32 v[182:183],v16 offset0:178 offset1:179
M 84,v[100:103],136
R 212,2,13120
M 84,v[104:107],132
M 84,v[108:111],128
M 84,v[112:115],124
M 84,v[116:119],120
L 15
M 64,v[152:155],192
ds_read2_b32 v[88:89],v1 offset0:64 offset1:65
M 64,v[156:159],188
ds_read2_b32 v[90:91],v1 offset0:66 offset1:67
M 64,v[160:163],184
L 14
R 120,2,16768
M 68,v[152:155],196
ds_read2_b32 v[92:93],v1 offset0:192 offset1:193
M 68,v[156:159],192
M 68,v[160:163],188
ds_read2_b32 v[94:95],v1 offset0:194 offset1:195
M 68,v[164:167],184
L 14
R 124,2,16256
M 72,v[152:155],200
ds_read2_b32 v[96:97],v14 offset0:64 offset1:65
M 72,v[156:159],196
ds_read2_b32 v[98:99],v14 offset0:66 offset1:67
M 72,v[160:163],192
M 72,v[164:167],188
R 128,2,15744
M 72,v[168:171],184
L 15
ds_read2_b32 v[100:101],v14 offset0:192 offset1:193
M 76,v[152:155],204
ds_read2_b32 v[102:103],v14 offset0:194 offset1:195
M 76,v[156:159],200
R 132,2,15232
M 76,v[160:163],196
M 76,v[164:167],192
ds_read2_b32 v[104:105],v15 offset0:64 offset1:65
M 76,v[168:171],188
ds_read2_b32 v[106:107],v15 offset0:66 offset1:67
M 76,v[172:175],184
L 15
R 136,2,14720
M 80,v[152:155],208
ds_read2_b32 v[108:109],v15 offset0:192 offset1:193
M 80,v[156:159],204
M 80,v[160:163],200
ds_read2_b32 v[110:111],v15 offset0:194 offset1:195
M 80,v[164:167],196
R 140,2,14208
M 80,v[168:171],192
ds_read2_b32 v[112:113],v16 offset0:64 offset1:65
M 80,v[172:175],188
ds_read2_b32 v[114:115],v16 offset0:66 offset1:67
M 80,v[176:179],184
L 15
M 84,v[152:155],212
R 144,2,13696
M 84,v[156:159],208
ds_read2_b32 v[116:117],v16 offset0:192 offset1:193
M 84,v[160:163],204
ds_read2_b32 v[118:119],v16 offset0:194 offset1:195
M 84,v[164:167],200
R 148,2,13184
M 84,v[168:171],196
M 84,v[172:175],192
M 84,v[176:179],188
M 84,v[180:183],184
L 15
M 64,v[88:91],128
ds_read2_b32 v[152:153],v1 offset0:80 offset1:81
M 64,v[92:95],124
ds_read2_b32 v[154:155],v1 offset0:82 offset1:83
M 64,v[96:99],120
L 14
R 184,2,16832
M 68,v[88:91],132
ds_read2_b32 v[156:157],v1 offset0:208 offset1:209
M 68,v[92:95],128
M 68,v[96:99],124
ds_read2_b32 v[158:159],v1 offset0:210 offset1:211
M 68,v[100:103],120
L 14
R 188,2,16320
M 72,v[88:91],136
ds_read2_b32 v[160:161],v14 offset0:80 offset1:81
M 72,v[92:95],132
ds_read2_b32 v[162:163],v14 offset0:82 offset1:83
M 72,v[96:99],128
M 72,v[100:103],124
R 192,2,15808
M 72,v[104:107],120
)"
R"(L 15
ds_read2_b32 v[164:165],v14 offset0:208 offset1:209
M 76,v[88:91],140
ds_read2_b32 v[166:167],v14 offset0:210 offset1:211
M 76,v[92:95],136
R 196,2,15296
M 76,v[96:99],132
M 76,v[100:103],128
ds_read2_b32 v[168:169],v15 offset0:80 offset1:81
M 76,v[104:107],124
ds_read2_b32 v[170:171],v15 offset0:82 offset1:83
M 76,v[108:111],120
L 15
R 200,2,14784
M 80,v[88:91],144
ds_read2_b32 v[172:173],v15 offset0:208 offset1:209
M 80,v[92:95],140
M 80,v[96:99],136
ds_read2_b32 v[174:175],v15 offset0:210 offset1:211
M 80,v[100:103],132
R 204,2,14272
M 80,v[104:107],128
ds_read2_b32 v[176:177],v16 offset0:80 offset1:81
M 80,v[108:111],124
ds_read2_b32 v[178:179],v16 offset0:82 offset1:83
M 80,v[112:115],120
L 15
M 84,v[88:91],148
R 208,2,13760
M 84,v[92:95],144
ds_read2_b32 v[180:181],v16 offset0:208 offset1:209
M 84,v[96:99],140
ds_read2_b32 v[182:183],v16 offset0:210 offset1:211
M 84,v[100:103],136
R 212,2,13248
M 84,v[104:107],132
M 84,v[108:111],128
M 84,v[112:115],124
M 84,v[116:119],120
L 15
M 64,v[152:155],192
ds_read2_b32 v[88:89],v1 offset0:96 offset1:97
M 64,v[156:159],188
ds_read2_b32 v[90:91],v1 offset0:98 offset1:99
M 64,v[160:163],184
L 14
R 120,2,16896
M 68,v[152:155],196
ds_read2_b32 v[92:93],v1 offset0:224 offset1:225
M 68,v[156:159],192
M 68,v[160:163],188
ds_read2_b32 v[94:95],v1 offset0:226 offset1:227
M 68,v[164:167],184
L 14
R 124,2,16384
M 72,v[152:155],200
ds_read2_b32 v[96:97],v14 offset0:96 offset1:97
M 72,v[156:159],196
ds_read2_b32 v[98:99],v14 offset0:98 offset1:99
M 72,v[160:163],192
M 72,v[164:167],188
R 128,2,15872
M 72,v[168:171],184
L 15
ds_read2_b32 v[100:101],v14 offset0:224 offset1:225
M 76,v[152:155],204
ds_read2_b32 v[102:103],v14 offset0:226 offset1:227
M 76,v[156:159],200
R 132,2,15360
M 76,v[160:163],196
M 76,v[164:167],192
ds_read2_b32 v[104:105],v15 offset0:96 offset1:97
M 76,v[168:171],188
ds_read2_b32 v[106:107],v15 offset0:98 offset1:99
M 76,v[172:175],184
L 15
R 136,2,14848
M 80,v[152:155],208
ds_read2_b32 v[108:109],v15 offset0:224 offset1:225
M 80,v[156:159],204
M 80,v[160:163],200
ds_read2_b32 v[110:111],v15 offset0:226 offset1:227
M 80,v[164:167],196
R 140,2,14336
M 80,v[168:171],192
ds_read2_b32 v[112:113],v16 offset0:96 offset1:97
M 80,v[172:175],188
ds_read2_b32 v[114:115],v16 offset0:98 offset1:99
M 80,v[176:179],184
L 15
M 84,v[152:155],212
R 144,2,13824
M 84,v[156:159],208
ds_read2_b32 v[116:117],v16 offset0:224 offset1:225
M 84,v[160:163],204
ds_read2_b32 v[118:119],v16 offset0:226 offset1:227
M 84,v[164:167],200
R 148,2,13312
M 84,v[168:171],196
M 84,v[172:175],192
M 84,v[176:179],188
M 84,v[180:183],184
L 15
M 64,v[88:91],128
ds_read2_b32 v[152:153],v1 offset0:112 offset1:113
M 64,v[92:95],124
ds_read2_b32 v[154:155],v1 offset0:114 offset1:115
M 64,v[96:99],120
L 14
R 184,2,16960
M 68,v[88:91],132
ds_read2_b32 v[156:157],v1 offset0:240 offset1:241
M 68,v[92:95],128
M 68,v[96:99],124
ds_read2_b32 v[158:159],v1 offset0:242 offset1:243
M 68,v[100:103],120
L 14
R 188,2,16448
M 72,v[88:91],136
ds_read2_b32 v[160:161],v14 offset0:112 offset1:113
M 72,v[92:95],132
ds_read2_b32 v[162:163],v14 offset0:114 offset1:115
M 72,v[96:99],128
M 72,v[100:103],124
R 192,2,15936
M 72,v[104:107],120
L 15
ds_read2_b32 v[164:165],v14 offset0:240 offset1:241
M 76,v[88:91],140
ds_read2_b32 v[166:167],v14 offset0:242 offset1:243
M 76,v[92:95],136
R 196,2,15424
M 76,v[96:99],132
M 76,v[100:103],128
ds_read2_b32 v[168:169],v15 offset0:112 offset1:113
M 76,v[104:107],124
ds_read2_b32 v[170:171],v15 offset0:114 offset1:115
M 76,v[108:111],120
L 15
R 200,2,14912
M 80,v[88:91],144
ds_read2_b32 v[172:173],v15 offset0:240 offset1:241
M 80,v[92:95],140
M 80,v[96:99],136
ds_read2_b32 v[174:175],v15 offset0:242 offset1:243
M 80,v[100:103],132
R 204,2,14400
M 80,v[104:107],128
ds_read2_b32 v[176:177],v16 offset0:112 offset1:113
M 80,v[108:111],124
ds_read2_b32 v[178:179],v16 offset0:114 offset1:115
M 80,v[112:115],120
L 15
M 84,v[88:91],148
R 208,2,13888
M 84,v[92:95],144
ds_read2_b32 v[180:181],v16 offset0:240 offset1:241
M 84,v[96:99],140
ds_read2_b32 v[182:183],v16 offset0:242 offset1:243
M 84,v[100:103],136
R 212,2,13376
M 84,v[104:107],132
M 84,v[108:111],128
M 84,v[112:115],124
M 84,v[116:119],120
L 15
M 64,v[152:155],192
M 64,v[156:159],188
M 64,v[160:163],184
L 12
M 68,v[152:155],196
M 68,v[156:159],192
M 68,v[160:163],188
M 68,v[164:167],184
L 9
M 72,v[152:155],200
M 72,v[156:159],196
M 72,v[160:163],192
ds_read_b64 v[24:25],v3 offset:15488
ds_read_b64 v[26:27],v3 offset:14976
M 72,v[164:167],188
ds_read_b64 v[28:29],v3 offset:14464
ds_read_b64 v[30:31],v3 offset:13952
M 72,v[168:171],184
L 10
ds_read_b64 v[32:33],v3 offset:13440
ds_read_b64 v[34:35],v3 offset:12928
M 76,v[152:155],204
M 76,v[156:159],200
M 76,v[160:163],196
M 76,v[164:167],192
M 76,v[168:171],188
M 76,v[172:175],184
L 9
M 80,v[152:155],208
M 80,v[156:159],204
M 80,v[160:163],200
M 80,v[164:167],196
M 80,v[168:171],192
M 80,v[172:175],188
M 80,v[176:179],184
L 6
M 84,v[152:155],212
M 84,v[156:159],208
M 84,v[160:163],204
M 84,v[164:167],200
M 84,v[168:171],196
M 84,v[172:175],192
M 84,v[176:179],188
M 84,v[180:183],184
L 0
v_and_b32 v36,0xffff0000,v25
v_lshlrev_b32 v37,16,v25
v_and_b32 v38,0xffff0000,v24
v_lshlrev_b32 v39,16,v24
v_fma_f32 v64,v36,s87,v64
v_fma_f32 v65,v37,s87,v65
v_fma_f32 v66,v38,s87,v66
v_fma_f32 v67,v39,s87,v67
v_cvt_pk_bf16_f32 v64,v64,v65
v_cvt_pk_bf16_f32 v65,v66,v67
ds_write_b64 v4,v[64:65] offset:12928
v_and_b32 v36,0xffff0000,v27
v_lshlrev_b32 v37,16,v27
v_and_b32 v38,0xffff0000,v26
v_lshlrev_b32 v39,16,v26
v_fma_f32 v68,v36,s87,v68
v_fma_f32 v69,v37,s87,v69
v_fma_f32 v70,v38,s87,v70
v_fma_f32 v71,v39,s87,v71
v_cvt_pk_bf16_f32 v68,v68,v69
v_cvt_pk_bf16_f32 v69,v70,v71
ds_write_b64 v4,v[68:69] offset:13440
v_and_b32 v36,0xffff0000,v29
v_lshlrev_b32 v37,16,v29
v_and_b32 v38,0xffff0000,v28
v_lshlrev_b32 v39,16,v28
v_fma_f32 v72,v36,s87,v72
v_fma_f32 v73,v37,s87,v73
v_fma_f32 v74,v38,s87,v74
v_fma_f32 v75,v39,s87,v75
v_cvt_pk_bf16_f32 v72,v72,v73
v_cvt_pk_bf16_f32 v73,v74,v75
ds_write_b64 v4,v[72:73] offset:13952
v_and_b32 v36,0xffff0000,v31
v_lshlrev_b32 v37,16,v31
v_and_b32 v38,0xffff0000,v30
v_lshlrev_b32 v39,16,v30
v_fma_f32 v76,v36,s87,v76
v_fma_f32 v77,v37,s87,v77
v_fma_f32 v78,v38,s87,v78
v_fma_f32 v79,v39,s87,v79
v_cvt_pk_bf16_f32 v76,v76,v77
v_cvt_pk_bf16_f32 v77,v78,v79
ds_write_b64 v4,v[76:77] offset:14464
v_and_b32 v36,0xffff0000,v33
v_lshlrev_b32 v37,16,v33
v_and_b32 v38,0xffff0000,v32
v_lshlrev_b32 v39,16,v32
v_fma_f32 v80,v36,s87,v80
v_fma_f32 v81,v37,s87,v81
v_fma_f32 v82,v38,s87,v82
v_fma_f32 v83,v39,s87,v83
v_cvt_pk_bf16_f32 v80,v80,v81
v_cvt_pk_bf16_f32 v81,v82,v83
ds_write_b64 v4,v[80:81] offset:14976
v_and_b32 v36,0xffff0000,v35
v_lshlrev_b32 v37,16,v35
v_and_b32 v38,0xffff0000,v34
v_lshlrev_b32 v39,16,v34
v_fma_f32 v84,v36,s87,v84
v_fma_f32 v85,v37,s87,v85
v_fma_f32 v86,v38,s87,v86
v_fma_f32 v87,v39,s87,v87
v_cvt_pk_bf16_f32 v84,v84,v85
v_cvt_pk_bf16_f32 v85,v86,v87
ds_write_b64 v4,v[84:85] offset:15488
Lcv_e_%=:
L 0
s_barrier
R 152,5,0
R 156,5,512
R 160,5,1024
R 164,5,1536
R 168,5,2048
R 172,5,2560
s_mov_b32 s78,99614720
L 5
buffer_store_dwordx4 v[152:155],v7,s[36:39],s78 offen offset:0 sc0 sc1
s_add_u32 s78,s56,100139008
L 4
buffer_store_dwordx4 v[156:159],v7,s[36:39],s78 offen offset:0 sc0 sc1
s_mov_b32 s78,100663296
L 3
buffer_store_dwordx4 v[160:163],v7,s[36:39],s78 offen offset:0 sc0 sc1
s_add_u32 s78,s56,101187584
L 2
buffer_store_dwordx4 v[164:167],v7,s[36:39],s78 offen offset:0 sc0 sc1
s_mov_b32 s78,101711872
L 1
buffer_store_dwordx4 v[168:171],v7,s[36:39],s78 offen offset:0 sc0 sc1
s_add_u32 s78,s56,102236160
L 0
buffer_store_dwordx4 v[172:175],v7,s[36:39],s78 offen offset:0 sc0 sc1
s_waitcnt vmcnt(0)
s_barrier
s_add_u32 s48,s48,32768
buffer_store_dword v23,v10,s[36:39],s48 offen offset:0 sc0 sc1
s_cmp_eq_u32 s51,2
s_cbranch_scc0 Lcv_f_%=
s_mov_b32 m0,163584
s_mov_b32 s78,3694592
buffer_load_dword v12,s[60:63],s78 offen offset:0  lds
s_mov_b32 m0,163584
s_mov_b32 s78,5541888
buffer_load_dword v12,s[60:63],s78 offen offset:0  lds
Lcv_f_%=:
Lcode_cv_e:
.purgem M
.purgem R
.purgem T
.purgem D
.purgem L
)"
  :: "s"(ws), "s"(__builtin_amdgcn_readfirstlane(sWave)), "s"(__builtin_amdgcn_readfirstlane(sBuf)), "s"(__builtin_amdgcn_readfirstlane(sAuxOff)), "s"(__builtin_amdgcn_readfirstlane(sFoutOff)), "s"(__builtin_amdgcn_readfirstlane(sC0)), "s"(__builtin_amdgcn_readfirstlane(sC1)), "s"(__builtin_amdgcn_readfirstlane(sExp)), "v"(vL16), "v"(vPA), "v"(vPB), "v"(vPU), "v"(vPW), "v"(vOR), "v"(vORA), "v"(vOG), "v"(vFA), "v"(vFB), "v"(vFlag), "v"(vTW), "v"(vTX), "s"(w3), "s"(w4) : "v0", "v1", "v2", "v3", "v4", "v5", "v6", "v7", "v8", "v9", "v10", "v11", "v12", "v13", "v14", "v15", "v16", "v17", "v18", "v19", "v20", "v21", "v22", "v23", "v24", "v25", "v26", "v27", "v28", "v29", "v30", "v31", "v32", "v33", "v34", "v35", "v36", "v37", "v38", "v39", "v40", "v41", "v42", "v43", "v44", "v45", "v46", "v47", "v48", "v49", "v50", "v51", "v52", "v53", "v54", "v55", "v56", "v57", "v58", "v59", "v60", "v61", "v62", "v63", "v64", "v65", "v66", "v67", "v68", "v69", "v70", "v71", "v72", "v73", "v74", "v75", "v76", "v77", "v78", "v79", "v80", "v81", "v82", "v83", "v84", "v85", "v86", "v87", "v88", "v89", "v90", "v91", "v92", "v93", "v94", "v95", "v96", "v97", "v98", "v99", "v100", "v101", "v102", "v103", "v104", "v105", "v106", "v107", "v108", "v109", "v110", "v111", "v112", "v113", "v114", "v115", "v116", "v117", "v118", "v119", "v120", "v121", "v122", "v123", "v124", "v125", "v126", "v127", "v128", "v129", "v130", "v131", "v132", "v133", "v134", "v135", "v136", "v137", "v138", "v139", "v140", "v141", "v142", "v143", "v144", "v145", "v146", "v147", "v148", "v149", "v150", "v151", "v152", "v153", "v154", "v155", "v156", "v157", "v158", "v159", "v160", "v161", "v162", "v163", "v164", "v165", "v166", "v167", "v168", "v169", "v170", "v171", "v172", "v173", "v174", "v175", "v176", "v177", "v178", "v179", "v180", "v181", "v182", "v183", "v184", "v185", "v186", "v187", "v188", "v189", "v190", "v191", "v192", "v193", "v194", "v195", "v196", "v197", "v198", "v199", "v200", "v201", "v202", "v203", "v204", "v205", "v206", "v207", "v208", "v209", "v210", "v211", "v212", "v213", "v214", "v215", "v216", "v217", "v218", "v219", "v220", "v221", "v222", "v223", "v224", "v225", "v226", "v227", "v228", "v229", "v230", "v231", "a0", "a1", "a2", "a3", "a4", "a5", "a6", "a7", "a8", "a9", "a10", "a11", "a12", "a13", "a14", "a15", "a16", "a17", "a18", "a19", "a20", "a21", "a22", "a23", "a24", "a25", "a26", "a27", "a28", "a29", "a30", "a31", "a32", "a33", "a34", "a35", "a36", "a37", "a38", "a39", "a40", "a41", "a42", "a43", "a44", "a45", "a46", "a47", "a48", "a49", "a50", "a51", "a52", "a53", "a54", "a55", "a56", "a57", "a58", "a59", "a60", "a61", "a62", "a63", "a64", "a65", "a66", "a67", "a68", "a69", "a70", "a71", "a72", "a73", "a74", "a75", "a76", "a77", "a78", "a79", "a80", "a81", "a82", "a83", "a84", "a85", "a86", "a87", "a88", "a89", "a90", "a91", "a92", "a93", "a94", "a95", "a96", "a97", "a98", "a99", "a100", "a101", "a102", "a103", "a104", "a105", "a106", "a107", "a108", "a109", "a110", "a111", "a112", "a113", "a114", "a115", "a116", "a117", "a118", "a119", "a120", "a121", "a122", "a123", "a124", "a125", "a126", "a127", "a128", "a129", "a130", "a131", "a132", "a133", "a134", "a135", "a136", "a137", "a138", "a139", "a140", "a141", "a142", "a143", "a144", "a145", "a146", "a147", "a148", "a149", "a150", "a151", "a152", "a153", "a154", "a155", "a156", "a157", "a158", "a159", "a160", "a161", "a162", "a163", "a164", "a165", "a166", "a167", "a168", "a169", "a170", "a171", "a172", "a173", "a174", "a175", "a176", "a177", "a178", "a179", "a180", "a181", "a182", "a183", "a184", "a185", "a186", "a187", "a188", "a189", "a190", "a191", "a192", "a193", "a194", "a195", "a196", "a197", "a198", "a199", "a200", "a201", "a202", "a203", "a204", "a205", "a206", "a207", "a208", "a209", "a210", "a211", "a212", "a213", "a214", "a215", "a216", "a217", "a218", "a219", "a220", "a221", "a222", "a223", "a224", "a225", "a226", "a227", "a228", "a229", "a230", "a231", "a232", "a233", "a234", "a235", "a236", "a237", "a238", "a239", "a240", "a241", "a242", "a243", "a244", "a245", "a246", "a247", "a248", "a249", "a250", "a251", "a252", "a253", "a254", "a255", "s36", "s37", "s38", "s39", "s40", "s41", "s42", "s43", "s44", "s45", "s46", "s47", "s48", "s49", "s50", "s51", "s52", "s53", "s54", "s55", "s56", "s57", "s58", "s59", "s60", "s61", "s62", "s63", "s64", "s65", "s66", "s67", "s68", "s69", "s70", "s71", "s72", "s73", "s74", "s75", "s76", "s77", "s78", "s79", "s80", "s81", "s82", "s83", "s84", "s85", "s86", "s87", "s88", "s89", "s90", "s91", "s92", "s93", "s94", "s95", "s96", "s97", "vcc", "scc", "memory");
    LANE_ID(lq_);
    const int g = QT0 + (NCV - 1 - iw) * 164 + min(wave * 64 + lq_, 163);
    if (QTB) touch_lds((const char*)q + (size_t)(g < 32768 ? g : 0) * 128, sScr);
  }
  WAIT_LGKM0();
  __builtin_amdgcn_s_barrier();
  CTS(10);
  }
  const unsigned sDbg0 = 110u * 1048576u + (b * 4 + wave) * 8192;
  constexpr unsigned OOB = 0x80000000u;
  if (kind == 6) {
    LANE_ID(lr);
    const int r8 = lr & 7, seg = lr >> 3, j = lr & 15, g = lr >> 4, rr = 8 * g + (j >> 2);
    const unsigned sWave = wave, sExp = __builtin_amdgcn_readfirstlane(ep), sG0 = slice < 16 ? slice + 1 : 0, sStep = NRW, sDbg = sDbg0;
    const unsigned sWw = ((slice >> 3) * 1792 + 16 * wave) * 2048 + (slice & 7) * 256;
    const unsigned vWd0 = seg * 2048 + (r8 ^ swzB(seg)) * 16, vWd1 = (seg + 8) * 2048 + (r8 ^ swzB(8 + seg)) * 16;
    const unsigned vWr0 = (wave >> 1) * 8192 + rr * 128 + (((wave & 1) * 4 + (lr & 3)) ^ swzB(rr)) * 16, vWr1 = vWr0 + 8;
    const unsigned vRF = (wave * 8 + (seg >> 2)) * 1024 + ((seg & 3) * 16 + r8) * 16;
    const unsigned vRwr = seg == 0 ? r8 * 64 + wave * 4 : 512 + lr * 4, vRrd = r8 * 64;
    const unsigned vRP = g == (j >> 2) ? r8 * 64 + 16 + wave * 8 + (seg & 1) * 4 : 768 + lr * 4;
    const unsigned vRS = (wave == 0 && seg < 2) ? r8 * 16 + seg * 256 : OOB, vFlag = lr < 32 ? lr * 4 : OOB;
    const unsigned vL4 = ((lr & 2) * 4 + wave * 2 + (lr & 1)) * 128, vLY = min((wave < 3 ? wave * 32 : 112) + lr, wave < 2 ? wave * 32 + 31 : wave == 2 ? 111 : NCV - 1) * 128;
    const unsigned vQ = r8 * 2048 + wave * 512 + seg * 16;
    const unsigned vY0 = wave * 4096 + lr * 16;
    const unsigned vTr = 4096 + wave * 4096 + ((2 * g + ((j & 3) >> 1)) * 8 + (j >> 2)) * 16 + (j & 1) * 8;
    const unsigned one = g == (j >> 2) ? 0x3f80u << (16 * (j & 1)) : 0u, vSa = (j & 2) ? 0u : one, vSb = (j & 2) ? one : 0u;
    (void)sDbg;
    asm volatile(
R"(.macro M d,w,x
v_mfma_f32_16x16x32_bf16 v[\d:\d+3], \w, v[\x:\x+3], v[\d:\d+3]
.endm
.macro R d,a,o
ds_read_b128 v[\d:\d+3], v\a offset:\o
.endm
.macro T d,a,o
ds_read_b64_tr_b16 \d, v\a offset:\o
.endm
.macro D a,s,o
buffer_load_dwordx4 v\a, s[36:39], s\s offen offset:\o sc1 lds
.endm
.macro L c
s_waitcnt lgkmcnt(\c)
.endm
Lcode_r_b:
s_mov_b64 s[36:37],%0
s_mov_b32 s38,111243392
s_mov_b32 s39,0x00020000
s_mov_b32 s44,%1
s_mov_b32 s45,%2
s_mov_b32 s46,%3
s_mov_b32 s50,%4
s_mov_b32 s53,%5
v_mov_b32 v0,%6
v_mov_b32 v1,%7
v_mov_b32 v2,%8
v_mov_b32 v3,%9
v_mov_b32 v4,%10
v_mov_b32 v5,%11
v_mov_b32 v6,%12
v_mov_b32 v7,%13
v_mov_b32 v8,%14
v_mov_b32 v9,%15
v_mov_b32 v10,%16
v_mov_b32 v11,%17
v_mov_b32 v12,%18
v_mov_b32 v13,%19
v_mov_b32 v14,%20
v_mov_b32 v15,%21
v_mov_b32 v16,%22
s_mov_b32 s47,256
s_mov_b32 s49,0
s_mov_b32 s93,s45
s_mov_b64 s[84:85],%23
s_mov_b32 s86,4194304
s_mov_b32 s87,0x00020000
s_lshl_b32 s55,s45,14
buffer_load_dwordx4 v[164:167],v8,s[84:87],s55 offen offset:0 nt
buffer_load_dwordx4 v[168:171],v8,s[84:87],s55 offen offset:128 nt
buffer_load_dwordx4 v[172:175],v8,s[84:87],s55 offen offset:256 nt
buffer_load_dwordx4 v[176:179],v8,s[84:87],s55 offen offset:384 nt
v_mov_b32 v17,s53
v_mbcnt_lo_u32_b32 v19,-1,0
v_mbcnt_hi_u32_b32 v19,-1,v19
v_lshlrev_b32 v19,4,v19
s_mov_b32 s82,0
s_mov_b32 s63,0x7fff0000
s_mov_b32 s64,0
s_mov_b32 s67,0
s_lshl_b32 s65,s44,12
s_add_u32 s65,s65,4096
s_lshl_b32 s66,s44,7
s_add_u32 s66,s66,109056000
s_mov_b32 s70,0xaaaaaaaa
s_mov_b32 s71,0xaaaaaaaa
s_mov_b32 s72,0xcccccccc
s_mov_b32 s73,0xcccccccc
s_mov_b32 s74,0xff00ff00
s_mov_b32 s75,0xff00ff00
s_mov_b32 s68,0x3a800000
s_mov_b32 s69,0x3727c5ac
s_mov_b32 s76,0xc0135761
s_mov_b32 s77,0x3d372713
v_mov_b32 v32,0x3f803f80
v_mov_b32 v33,0x3f803f80
v_mov_b32 v34,0x3f803f80
v_mov_b32 v35,0x3f803f80
v_mov_b32 v24,0
v_mov_b32 v25,0
v_mov_b32 v26,v11
v_mov_b32 v27,v12
v_mov_b32 v28,0
v_mov_b32 v29,0
s_getpc_b64 s[94:95]
Lr_pc_%=:
s_add_u32 s94,s94,Lr_tab_%=-Lr_pc_%=
s_addc_u32 s95,s95,0
s_lshl_b32 s54,s44,11
s_add_u32 s80,s45,85
s_cmp_eq_u32 s45,0
s_cselect_b32 s80,-1,s80
s_waitcnt vmcnt(0)
Lr_a0_%=:
s_waitcnt vmcnt(10)
v_mfma_f32_16x16x32_bf16 v[36:39],v[32:35],v[164:167],0
s_add_u32 s79,s45,s46
s_cmp_lt_u32 s45,64
s_cselect_b32 s78,s49,0
v_mfma_f32_16x16x32_bf16 v[40:43],v[164:167],v[164:167],0
s_cmp_ge_u32 s79,64
s_cselect_b32 s78,s78,0
s_add_u32 s62,s79,s78
M 36,v[32:35],168
s_lshr_b32 s78,s45,1
s_and_b32 s79,s45,1
s_lshl_b32 s79,s79,7
M 40,v[168:171],168
s_lshl_b32 s57,s78,15
s_lshl_b32 s55,s62,14
s_cmp_lt_u32 s62,s47
M 36,v[32:35],172
s_cselect_b32 s55,s55,0x7fff0000
s_add_u32 s57,s57,s79
s_lshl_b32 s59,s78,9
M 40,v[172:175],172
buffer_load_dwordx4 v[196:199],v8,s[84:87],s55 offen offset:0 nt
M 36,v[32:35],176
buffer_load_dwordx4 v[200:203],v8,s[84:87],s55 offen offset:128 nt
M 40,v[176:179],176
buffer_load_dwordx4 v[204:207],v8,s[84:87],s55 offen offset:256 nt
v_mfma_f32_16x16x32_bf16 v[116:119],v[26:29],v[164:167],0
buffer_load_dwordx4 v[208:211],v8,s[84:87],s55 offen offset:384 nt
v_mfma_f32_16x16x32_bf16 v[120:123],v[24:27],v[164:167],0
s_add_u32 s57,s57,8388608
s_add_u32 s59,s59,s79
s_lshl_b32 s60,s45,9
v_mfma_f32_16x16x32_bf16 v[124:127],v[26:29],v[168:171],0
s_add_u32 s58,s57,4096
s_add_u32 s59,s59,106954752
s_add_u32 s60,s60,s66
v_mfma_f32_16x16x32_bf16 v[128:131],v[24:27],v[168:171],0
v_add_f32_dpp v44,v36,v36 row_ror:8 row_mask:0xf bank_mask:0xf
v_cndmask_b32 v45,v40,v41,s[70:71]
v_mfma_f32_16x16x32_bf16 v[132:135],v[26:29],v[172:175],0
ds_write_b32 v2,v44 offset:0
v_mfma_f32_16x16x32_bf16 v[136:139],v[24:27],v[172:175],0
v_cndmask_b32 v46,v42,v43,s[70:71]
v_cndmask_b32 v45,v45,v46,s[72:73]
v_mfma_f32_16x16x32_bf16 v[140:143],v[26:29],v[176:179],0
ds_write_b32 v0,v45 offset:0
v_mfma_f32_16x16x32_bf16 v[144:147],v[24:27],v[176:179],0
L 0
s_barrier
R 52,3,0
R 56,3,16
R 60,3,32
s_cmp_lg_u32 s67,0
s_cbranch_scc1 Lr_b0_%=
s_waitcnt vmcnt(4)
buffer_store_dword v17,v5,s[36:39],s63 offen offset:0 sc0 sc1
s_mov_b32 s63,0x7fff0000
s_mov_b32 s38,111243392
s_cmp_lt_u32 s45,64
s_cselect_b32 s64,-192,0
s_add_u32 s64,s64,256
s_cmp_lt_u32 s45,64
s_cselect_b32 s61,0,32768
s_add_u32 s61,s61,111177728
s_mul_i32 s56,s45,16384
s_add_u32 s56,s56,98566144
Lr_bpy0_%=:
buffer_load_dword v18,v7,s[36:39],s61 offen offset:0 sc1
s_add_u32 s82,s82,1
s_waitcnt vmcnt(0)
v_cmp_eq_u32 vcc,s53,v18
s_nop 0
s_cmp_eq_u64 vcc,-1
s_cbranch_scc1 Lr_bqy0_%=
s_sleep 2
s_cmp_lt_u32 s82,20000
s_cbranch_scc1 Lr_bpy0_%=
Lr_bqy0_%=:
s_add_u32 m0,s65,0
s_nop 0
D 9,56,0
D 9,56,1024
D 9,56,2048
D 9,56,3072
s_waitcnt vmcnt(0)
Lr_b0_%=:
s_waitcnt vmcnt(10)
T v[68:69],10,0
T v[70:71],10,64
T v[72:73],10,1024
T v[74:75],10,1088
T v[76:77],10,2048
T v[78:79],10,2112
T v[80:81],10,3072
T v[82:83],10,3136
s_mov_b32 s45,s62
s_min_u32 s78,s64,s47
s_mul_i32 s56,s45,16384
s_add_u32 s56,s56,98566144
s_cmp_lt_i32 s45,s78
s_cselect_b32 s67,1,0
s_cselect_b32 s56,s56,0x7fff0000
s_add_u32 m0,s65,16384
s_nop 0
D 9,56,0
D 9,56,1024
D 9,56,2048
D 9,56,3072
L 8
v_pk_add_f32 v[52:53],v[52:53],v[54:55]
v_pk_add_f32 v[56:57],v[56:57],v[58:59]
v_pk_add_f32 v[60:61],v[60:61],v[62:63]
v_add_f32 v52,v52,v53
v_pk_add_f32 v[56:57],v[56:57],v[60:61]
v_mul_f32 v52,s68,v52
v_add_f32 v53,v56,v57
v_mul_f32 v53,s68,v53
v_fma_f32 v53,-v52,v52,v53
v_max_f32 v53,0,v53
s_waitcnt vmcnt(8)
buffer_store_dword v17,v5,s[36:39],s63 offen offset:0 sc0 sc1
s_mov_b32 s63,0x7fff0000
L 6
v_lshlrev_b32 v84,16,v68
v_and_b32 v85,0xffff0000,v68
v_lshlrev_b32 v86,16,v69
v_and_b32 v87,0xffff0000,v69
v_lshlrev_b32 v88,16,v70
v_and_b32 v89,0xffff0000,v70
v_lshlrev_b32 v90,16,v71
v_and_b32 v91,0xffff0000,v71
L 4
v_lshlrev_b32 v92,16,v72
v_and_b32 v93,0xffff0000,v72
v_lshlrev_b32 v94,16,v73
v_and_b32 v95,0xffff0000,v73
v_lshlrev_b32 v96,16,v74
v_and_b32 v97,0xffff0000,v74
v_lshlrev_b32 v98,16,v75
v_and_b32 v99,0xffff0000,v75
L 2
v_lshlrev_b32 v100,16,v76
v_and_b32 v101,0xffff0000,v76
v_lshlrev_b32 v102,16,v77
v_and_b32 v103,0xffff0000,v77
v_lshlrev_b32 v104,16,v78
v_and_b32 v105,0xffff0000,v78
v_lshlrev_b32 v106,16,v79
v_and_b32 v107,0xffff0000,v79
L 0
v_lshlrev_b32 v108,16,v80
v_and_b32 v109,0xffff0000,v80
v_lshlrev_b32 v110,16,v81
v_and_b32 v111,0xffff0000,v81
v_lshlrev_b32 v112,16,v82
v_and_b32 v113,0xffff0000,v82
v_lshlrev_b32 v114,16,v83
v_and_b32 v115,0xffff0000,v83
v_add_f32 v53,s69,v53
v_rsq_f32 v48,v53
s_nop 0
v_mul_f32_e64 v50,-v52,v48
v_mov_b32 v49,v48
v_mov_b32 v51,v50
v_pk_fma_f32 v[52:53],v[116:117],v[48:49],v[50:51]
v_pk_fma_f32 v[54:55],v[118:119],v[48:49],v[50:51]
v_pk_fma_f32 v[56:57],v[120:121],v[48:49],v[50:51]
v_pk_fma_f32 v[58:59],v[122:123],v[48:49],v[50:51]
v_pk_fma_f32 v[116:117],v[52:53],v[84:85],v[116:117]
v_pk_fma_f32 v[118:119],v[54:55],v[86:87],v[118:119]
v_pk_fma_f32 v[120:121],v[56:57],v[88:89],v[120:121]
v_pk_fma_f32 v[122:123],v[58:59],v[90:91],v[122:123]
v_cvt_pk_bf16_f32 v148,v116,v117
v_cvt_pk_bf16_f32 v149,v118,v119
v_cvt_pk_bf16_f32 v150,v120,v121
v_cvt_pk_bf16_f32 v151,v122,v123
v_pk_fma_f32 v[52:53],v[124:125],v[48:49],v[50:51]
v_pk_fma_f32 v[54:55],v[126:127],v[48:49],v[50:51]
v_pk_fma_f32 v[56:57],v[128:129],v[48:49],v[50:51]
v_pk_fma_f32 v[58:59],v[130:131],v[48:49],v[50:51]
v_pk_fma_f32 v[124:125],v[52:53],v[92:93],v[124:125]
v_pk_fma_f32 v[126:127],v[54:55],v[94:95],v[126:127]
v_pk_fma_f32 v[128:129],v[56:57],v[96:97],v[128:129]
v_pk_fma_f32 v[130:131],v[58:59],v[98:99],v[130:131]
v_cvt_pk_bf16_f32 v152,v124,v125
v_cvt_pk_bf16_f32 v153,v126,v127
v_cvt_pk_bf16_f32 v154,v128,v129
v_cvt_pk_bf16_f32 v155,v130,v131
v_pk_fma_f32 v[52:53],v[132:133],v[48:49],v[50:51]
v_pk_fma_f32 v[54:55],v[134:135],v[48:49],v[50:51]
v_pk_fma_f32 v[56:57],v[136:137],v[48:49],v[50:51]
v_pk_fma_f32 v[58:59],v[138:139],v[48:49],v[50:51]
v_pk_fma_f32 v[132:133],v[52:53],v[100:101],v[132:133]
v_pk_fma_f32 v[134:135],v[54:55],v[102:103],v[134:135]
v_pk_fma_f32 v[136:137],v[56:57],v[104:105],v[136:137]
v_pk_fma_f32 v[138:139],v[58:59],v[106:107],v[138:139]
v_cvt_pk_bf16_f32 v156,v132,v133
v_cvt_pk_bf16_f32 v157,v134,v135
v_cvt_pk_bf16_f32 v158,v136,v137
v_cvt_pk_bf16_f32 v159,v138,v139
v_pk_fma_f32 v[52:53],v[140:141],v[48:49],v[50:51]
v_pk_fma_f32 v[54:55],v[142:143],v[48:49],v[50:51]
v_pk_fma_f32 v[56:57],v[144:145],v[48:49],v[50:51]
v_pk_fma_f32 v[58:59],v[146:147],v[48:49],v[50:51]
v_pk_fma_f32 v[140:141],v[52:53],v[108:109],v[140:141]
v_pk_fma_f32 v[142:143],v[54:55],v[110:111],v[142:143]
v_pk_fma_f32 v[144:145],v[56:57],v[112:113],v[144:145]
v_pk_fma_f32 v[146:147],v[58:59],v[114:115],v[146:147]
v_cvt_pk_bf16_f32 v160,v140,v141
v_cvt_pk_bf16_f32 v161,v142,v143
v_cvt_pk_bf16_f32 v162,v144,v145
v_cvt_pk_bf16_f32 v163,v146,v147
v_mfma_f32_16x16x32_bf16 v[36:39],v[32:35],v[148:151],0
buffer_store_dwordx4 v[148:151],v1,s[36:39],s57 offen offset:0 sc0 sc1
v_mfma_f32_16x16x32_bf16 v[40:43],v[148:151],v[148:151],0
M 36,v[32:35],152
buffer_store_dwordx4 v[152:155],v1,s[36:39],s57 offen offset:2048 sc0 sc1
M 40,v[152:155],152
M 36,v[32:35],156
buffer_store_dwordx4 v[156:159],v1,s[36:39],s58 offen offset:0 sc0 sc1
M 40,v[156:159],156
M 36,v[32:35],160
buffer_store_dwordx4 v[160:163],v1,s[36:39],s58 offen offset:2048 sc0 sc1
M 40,v[160:163],160
s_nop 5
v_add_f32_dpp v44,v36,v36 row_ror:8 row_mask:0xf bank_mask:0xf
s_nop 0
v_cndmask_b32 v45,v40,v41,s[70:71]
v_cndmask_b32 v46,v42,v43,s[70:71]
ds_write_b32 v2,v44 offset:1024
v_cndmask_b32 v45,v45,v46,s[72:73]
ds_write_b32 v0,v45 offset:1024
L 0
s_barrier
s_cmp_lt_u32 s45,s80
s_cbranch_scc1 Lr_bg0_%=
s_swappc_b64 s[88:89],s[94:95]
s_add_u32 s94,s94,256
s_addc_u32 s95,s95,0
Lr_bg0_%=:
R 52,3,1024
R 56,3,1040
R 60,3,1056
L 0
v_pk_add_f32 v[52:53],v[52:53],v[54:55]
v_pk_add_f32 v[56:57],v[56:57],v[58:59]
v_pk_add_f32 v[60:61],v[60:61],v[62:63]
v_add_f32 v52,v52,v53
v_pk_add_f32 v[56:57],v[56:57],v[60:61]
v_mul_f32 v52,s68,v52
v_add_f32 v53,v56,v57
v_mul_f32 v53,s68,v53
v_fma_f32 v53,-v52,v52,v53
v_max_f32 v53,0,v53
v_add_f32 v54,s69,v53
v_fma_f32 v55,s69,v54,v53
v_rsq_f32 v65,v54
v_rsq_f32 v54,v55
v_mul_f32_e64 v64,-v52,v65
v_mul_f32 v63,v55,v54
v_mul_f32 v61,s76,v54
v_mul_f32 v55,v54,v54
v_mul_f32 v62,s77,v55
v_mul_f32 v62,v62,v61
v_cndmask_b32 v60,v52,v64,s[74:75]
v_cndmask_b32 v61,v61,v65,s[74:75]
buffer_store_dwordx4 v[60:63],v4,s[36:39],s59 offen offset:0 sc0 sc1
s_mov_b32 s63,s60
s_cmp_lt_i32 s45,s47
s_cbranch_scc0 Lr_mid_%=
Lr_a1_%=:
s_waitcnt vmcnt(10)
v_mfma_f32_16x16x32_bf16 v[36:39],v[32:35],v[196:199],0
s_add_u32 s79,s45,s46
s_cmp_lt_u32 s45,64
s_cselect_b32 s78,s49,0
v_mfma_f32_16x16x32_bf16 v[40:43],v[196:199],v[196:199],0
s_cmp_ge_u32 s79,64
s_cselect_b32 s78,s78,0
s_add_u32 s62,s79,s78
M 36,v[32:35],200
s_lshr_b32 s78,s45,1
s_and_b32 s79,s45,1
s_lshl_b32 s79,s79,7
M 40,v[200:203],200
s_lshl_b32 s57,s78,15
s_lshl_b32 s55,s62,14
s_cmp_lt_u32 s62,s47
M 36,v[32:35],204
s_cselect_b32 s55,s55,0x7fff0000
s_add_u32 s57,s57,s79
s_lshl_b32 s59,s78,9
M 40,v[204:207],204
buffer_load_dwordx4 v[164:167],v8,s[84:87],s55 offen offset:0 nt
M 36,v[32:35],208
buffer_load_dwordx4 v[168:171],v8,s[84:87],s55 offen offset:128 nt
M 40,v[208:211],208
buffer_load_dwordx4 v[172:175],v8,s[84:87],s55 offen offset:256 nt
v_mfma_f32_16x16x32_bf16 v[116:119],v[26:29],v[196:199],0
buffer_load_dwordx4 v[176:179],v8,s[84:87],s55 offen offset:384 nt
v_mfma_f32_16x16x32_bf16 v[120:123],v[24:27],v[196:199],0
s_add_u32 s57,s57,8388608
s_add_u32 s59,s59,s79
s_lshl_b32 s60,s45,9
v_mfma_f32_16x16x32_bf16 v[124:127],v[26:29],v[200:203],0
s_add_u32 s58,s57,4096
s_add_u32 s59,s59,106954752
s_add_u32 s60,s60,s66
v_mfma_f32_16x16x32_bf16 v[128:131],v[24:27],v[200:203],0
v_add_f32_dpp v44,v36,v36 row_ror:8 row_mask:0xf bank_mask:0xf
v_cndmask_b32 v45,v40,v41,s[70:71]
v_mfma_f32_16x16x32_bf16 v[132:135],v[26:29],v[204:207],0
ds_write_b32 v2,v44 offset:0
v_mfma_f32_16x16x32_bf16 v[136:139],v[24:27],v[204:207],0
v_cndmask_b32 v46,v42,v43,s[70:71]
v_cndmask_b32 v45,v45,v46,s[72:73]
v_mfma_f32_16x16x32_bf16 v[140:143],v[26:29],v[208:211],0
ds_write_b32 v0,v45 offset:0
v_mfma_f32_16x16x32_bf16 v[144:147],v[24:27],v[208:211],0
L 0
s_barrier
R 52,3,0
R 56,3,16
R 60,3,32
s_cmp_lg_u32 s67,0
s_cbranch_scc1 Lr_b1_%=
s_waitcnt vmcnt(4)
buffer_store_dword v17,v5,s[36:39],s63 offen offset:0 sc0 sc1
s_mov_b32 s63,0x7fff0000
s_mov_b32 s38,111243392
s_cmp_lt_u32 s45,64
s_cselect_b32 s64,-192,0
s_add_u32 s64,s64,256
s_cmp_lt_u32 s45,64
s_cselect_b32 s61,0,32768
s_add_u32 s61,s61,111177728
s_mul_i32 s56,s45,16384
s_add_u32 s56,s56,98566144
Lr_bpy1_%=:
buffer_load_dword v18,v7,s[36:39],s61 offen offset:0 sc1
s_add_u32 s82,s82,1
s_waitcnt vmcnt(0)
v_cmp_eq_u32 vcc,s53,v18
s_nop 0
s_cmp_eq_u64 vcc,-1
s_cbranch_scc1 Lr_bqy1_%=
s_sleep 2
s_cmp_lt_u32 s82,20000
s_cbranch_scc1 Lr_bpy1_%=
Lr_bqy1_%=:
s_add_u32 m0,s65,16384
s_nop 0
D 9,56,0
D 9,56,1024
D 9,56,2048
D 9,56,3072
s_waitcnt vmcnt(0)
Lr_b1_%=:
s_waitcnt vmcnt(10)
T v[68:69],10,16384
T v[70:71],10,16448
T v[72:73],10,17408
T v[74:75],10,17472
T v[76:77],10,18432
T v[78:79],10,18496
T v[80:81],10,19456
T v[82:83],10,19520
s_mov_b32 s45,s62
s_min_u32 s78,s64,s47
s_mul_i32 s56,s45,16384
s_add_u32 s56,s56,98566144
s_cmp_lt_i32 s45,s78
s_cselect_b32 s67,1,0
s_cselect_b32 s56,s56,0x7fff0000
s_add_u32 m0,s65,0
s_nop 0
D 9,56,0
D 9,56,1024
D 9,56,2048
D 9,56,3072
L 8
v_pk_add_f32 v[52:53],v[52:53],v[54:55]
v_pk_add_f32 v[56:57],v[56:57],v[58:59]
v_pk_add_f32 v[60:61],v[60:61],v[62:63]
v_add_f32 v52,v52,v53
v_pk_add_f32 v[56:57],v[56:57],v[60:61]
v_mul_f32 v52,s68,v52
v_add_f32 v53,v56,v57
v_mul_f32 v53,s68,v53
v_fma_f32 v53,-v52,v52,v53
v_max_f32 v53,0,v53
s_waitcnt vmcnt(8)
buffer_store_dword v17,v5,s[36:39],s63 offen offset:0 sc0 sc1
s_mov_b32 s63,0x7fff0000
L 6
v_lshlrev_b32 v84,16,v68
v_and_b32 v85,0xffff0000,v68
v_lshlrev_b32 v86,16,v69
v_and_b32 v87,0xffff0000,v69
v_lshlrev_b32 v88,16,v70
v_and_b32 v89,0xffff0000,v70
v_lshlrev_b32 v90,16,v71
v_and_b32 v91,0xffff0000,v71
L 4
v_lshlrev_b32 v92,16,v72
)"
R"(v_and_b32 v93,0xffff0000,v72
v_lshlrev_b32 v94,16,v73
v_and_b32 v95,0xffff0000,v73
v_lshlrev_b32 v96,16,v74
v_and_b32 v97,0xffff0000,v74
v_lshlrev_b32 v98,16,v75
v_and_b32 v99,0xffff0000,v75
L 2
v_lshlrev_b32 v100,16,v76
v_and_b32 v101,0xffff0000,v76
v_lshlrev_b32 v102,16,v77
v_and_b32 v103,0xffff0000,v77
v_lshlrev_b32 v104,16,v78
v_and_b32 v105,0xffff0000,v78
v_lshlrev_b32 v106,16,v79
v_and_b32 v107,0xffff0000,v79
L 0
v_lshlrev_b32 v108,16,v80
v_and_b32 v109,0xffff0000,v80
v_lshlrev_b32 v110,16,v81
v_and_b32 v111,0xffff0000,v81
v_lshlrev_b32 v112,16,v82
v_and_b32 v113,0xffff0000,v82
v_lshlrev_b32 v114,16,v83
v_and_b32 v115,0xffff0000,v83
v_add_f32 v53,s69,v53
v_rsq_f32 v48,v53
s_nop 0
v_mul_f32_e64 v50,-v52,v48
v_mov_b32 v49,v48
v_mov_b32 v51,v50
v_pk_fma_f32 v[52:53],v[116:117],v[48:49],v[50:51]
v_pk_fma_f32 v[54:55],v[118:119],v[48:49],v[50:51]
v_pk_fma_f32 v[56:57],v[120:121],v[48:49],v[50:51]
v_pk_fma_f32 v[58:59],v[122:123],v[48:49],v[50:51]
v_pk_fma_f32 v[116:117],v[52:53],v[84:85],v[116:117]
v_pk_fma_f32 v[118:119],v[54:55],v[86:87],v[118:119]
v_pk_fma_f32 v[120:121],v[56:57],v[88:89],v[120:121]
v_pk_fma_f32 v[122:123],v[58:59],v[90:91],v[122:123]
v_cvt_pk_bf16_f32 v148,v116,v117
v_cvt_pk_bf16_f32 v149,v118,v119
v_cvt_pk_bf16_f32 v150,v120,v121
v_cvt_pk_bf16_f32 v151,v122,v123
v_pk_fma_f32 v[52:53],v[124:125],v[48:49],v[50:51]
v_pk_fma_f32 v[54:55],v[126:127],v[48:49],v[50:51]
v_pk_fma_f32 v[56:57],v[128:129],v[48:49],v[50:51]
v_pk_fma_f32 v[58:59],v[130:131],v[48:49],v[50:51]
v_pk_fma_f32 v[124:125],v[52:53],v[92:93],v[124:125]
v_pk_fma_f32 v[126:127],v[54:55],v[94:95],v[126:127]
v_pk_fma_f32 v[128:129],v[56:57],v[96:97],v[128:129]
v_pk_fma_f32 v[130:131],v[58:59],v[98:99],v[130:131]
v_cvt_pk_bf16_f32 v152,v124,v125
v_cvt_pk_bf16_f32 v153,v126,v127
v_cvt_pk_bf16_f32 v154,v128,v129
v_cvt_pk_bf16_f32 v155,v130,v131
v_pk_fma_f32 v[52:53],v[132:133],v[48:49],v[50:51]
v_pk_fma_f32 v[54:55],v[134:135],v[48:49],v[50:51]
v_pk_fma_f32 v[56:57],v[136:137],v[48:49],v[50:51]
v_pk_fma_f32 v[58:59],v[138:139],v[48:49],v[50:51]
v_pk_fma_f32 v[132:133],v[52:53],v[100:101],v[132:133]
v_pk_fma_f32 v[134:135],v[54:55],v[102:103],v[134:135]
v_pk_fma_f32 v[136:137],v[56:57],v[104:105],v[136:137]
v_pk_fma_f32 v[138:139],v[58:59],v[106:107],v[138:139]
v_cvt_pk_bf16_f32 v156,v132,v133
v_cvt_pk_bf16_f32 v157,v134,v135
v_cvt_pk_bf16_f32 v158,v136,v137
v_cvt_pk_bf16_f32 v159,v138,v139
v_pk_fma_f32 v[52:53],v[140:141],v[48:49],v[50:51]
v_pk_fma_f32 v[54:55],v[142:143],v[48:49],v[50:51]
v_pk_fma_f32 v[56:57],v[144:145],v[48:49],v[50:51]
v_pk_fma_f32 v[58:59],v[146:147],v[48:49],v[50:51]
v_pk_fma_f32 v[140:141],v[52:53],v[108:109],v[140:141]
v_pk_fma_f32 v[142:143],v[54:55],v[110:111],v[142:143]
v_pk_fma_f32 v[144:145],v[56:57],v[112:113],v[144:145]
v_pk_fma_f32 v[146:147],v[58:59],v[114:115],v[146:147]
v_cvt_pk_bf16_f32 v160,v140,v141
v_cvt_pk_bf16_f32 v161,v142,v143
v_cvt_pk_bf16_f32 v162,v144,v145
v_cvt_pk_bf16_f32 v163,v146,v147
v_mfma_f32_16x16x32_bf16 v[36:39],v[32:35],v[148:151],0
buffer_store_dwordx4 v[148:151],v1,s[36:39],s57 offen offset:0 sc0 sc1
v_mfma_f32_16x16x32_bf16 v[40:43],v[148:151],v[148:151],0
M 36,v[32:35],152
buffer_store_dwordx4 v[152:155],v1,s[36:39],s57 offen offset:2048 sc0 sc1
M 40,v[152:155],152
M 36,v[32:35],156
buffer_store_dwordx4 v[156:159],v1,s[36:39],s58 offen offset:0 sc0 sc1
M 40,v[156:159],156
M 36,v[32:35],160
buffer_store_dwordx4 v[160:163],v1,s[36:39],s58 offen offset:2048 sc0 sc1
M 40,v[160:163],160
s_nop 5
v_add_f32_dpp v44,v36,v36 row_ror:8 row_mask:0xf bank_mask:0xf
s_nop 0
v_cndmask_b32 v45,v40,v41,s[70:71]
v_cndmask_b32 v46,v42,v43,s[70:71]
ds_write_b32 v2,v44 offset:1024
v_cndmask_b32 v45,v45,v46,s[72:73]
ds_write_b32 v0,v45 offset:1024
L 0
s_barrier
s_cmp_lt_u32 s45,s80
s_cbranch_scc1 Lr_bg1_%=
s_swappc_b64 s[88:89],s[94:95]
s_add_u32 s94,s94,256
s_addc_u32 s95,s95,0
Lr_bg1_%=:
R 52,3,1024
R 56,3,1040
R 60,3,1056
L 0
v_pk_add_f32 v[52:53],v[52:53],v[54:55]
v_pk_add_f32 v[56:57],v[56:57],v[58:59]
v_pk_add_f32 v[60:61],v[60:61],v[62:63]
v_add_f32 v52,v52,v53
v_pk_add_f32 v[56:57],v[56:57],v[60:61]
v_mul_f32 v52,s68,v52
v_add_f32 v53,v56,v57
v_mul_f32 v53,s68,v53
v_fma_f32 v53,-v52,v52,v53
v_max_f32 v53,0,v53
v_add_f32 v54,s69,v53
v_fma_f32 v55,s69,v54,v53
v_rsq_f32 v65,v54
v_rsq_f32 v54,v55
v_mul_f32_e64 v64,-v52,v65
v_mul_f32 v63,v55,v54
v_mul_f32 v61,s76,v54
v_mul_f32 v55,v54,v54
v_mul_f32 v62,s77,v55
v_mul_f32 v62,v62,v61
v_cndmask_b32 v60,v52,v64,s[74:75]
v_cndmask_b32 v61,v61,v65,s[74:75]
buffer_store_dwordx4 v[60:63],v4,s[36:39],s59 offen offset:0 sc0 sc1
s_mov_b32 s63,s60
s_cmp_lt_i32 s45,s47
s_cbranch_scc1 Lr_a0_%=
Lr_mid_%=:
s_waitcnt vmcnt(0)
buffer_store_dword v17,v5,s[36:39],s63 offen offset:0 sc0 sc1
s_mov_b32 s63,0x7fff0000
s_mov_b32 s45,s93
s_cmp_eq_u32 s93,0
s_cbranch_scc1 Lr_h0_%=
s_mov_b64 s[40:41],%24
s_mov_b32 s42,7340032
s_mov_b32 s43,0x00020000
s_lshl_b32 s57,s44,11
s_add_u32 s55,s50,1441792
s_add_u32 m0,s57,106496
s_nop 0
buffer_load_dwordx4 v13,s[40:43],s55 offen offset:0  lds
s_add_u32 m0,s57,107520
s_nop 0
buffer_load_dwordx4 v14,s[40:43],s55 offen offset:0  lds
s_add_u32 m0,s57,114560
s_nop 0
buffer_load_dwordx4 v13,s[40:43],s55 offen offset:128  lds
s_add_u32 m0,s57,115584
s_nop 0
buffer_load_dwordx4 v14,s[40:43],s55 offen offset:128  lds
s_add_u32 s55,s50,1572864
s_add_u32 m0,s57,122880
s_nop 0
buffer_load_dwordx4 v13,s[40:43],s55 offen offset:0  lds
s_add_u32 m0,s57,123904
s_nop 0
buffer_load_dwordx4 v14,s[40:43],s55 offen offset:0  lds
s_add_u32 m0,s57,130944
s_nop 0
buffer_load_dwordx4 v13,s[40:43],s55 offen offset:128  lds
s_add_u32 m0,s57,131968
s_nop 0
buffer_load_dwordx4 v14,s[40:43],s55 offen offset:128  lds
s_add_u32 s55,s50,1703936
s_add_u32 m0,s57,139264
s_nop 0
buffer_load_dwordx4 v13,s[40:43],s55 offen offset:0  lds
s_add_u32 m0,s57,140288
s_nop 0
buffer_load_dwordx4 v14,s[40:43],s55 offen offset:0  lds
s_add_u32 m0,s57,147328
s_nop 0
buffer_load_dwordx4 v13,s[40:43],s55 offen offset:128  lds
s_add_u32 m0,s57,148352
s_nop 0
buffer_load_dwordx4 v14,s[40:43],s55 offen offset:128  lds
s_add_u32 s55,s50,1835008
s_add_u32 m0,s57,40960
s_nop 0
buffer_load_dwordx4 v13,s[40:43],s55 offen offset:0  lds
s_add_u32 m0,s57,41984
s_nop 0
buffer_load_dwordx4 v14,s[40:43],s55 offen offset:0  lds
s_add_u32 m0,s57,49024
s_nop 0
buffer_load_dwordx4 v13,s[40:43],s55 offen offset:128  lds
s_add_u32 m0,s57,50048
s_nop 0
buffer_load_dwordx4 v14,s[40:43],s55 offen offset:128  lds
s_add_u32 s55,s50,1966080
s_add_u32 m0,s57,57344
s_nop 0
buffer_load_dwordx4 v13,s[40:43],s55 offen offset:0  lds
s_add_u32 m0,s57,58368
s_nop 0
buffer_load_dwordx4 v14,s[40:43],s55 offen offset:0  lds
s_add_u32 m0,s57,65408
s_nop 0
buffer_load_dwordx4 v13,s[40:43],s55 offen offset:128  lds
s_add_u32 m0,s57,66432
s_nop 0
buffer_load_dwordx4 v14,s[40:43],s55 offen offset:128  lds
Lr_h0_%=:
s_lshr_b32 s78,s45,1
s_and_b32 s79,s45,1
s_lshl_b32 s79,s79,7
s_lshl_b32 s55,s78,15
s_add_u32 s55,s55,s79
s_mul_i32 s56,s45,16384
s_add_u32 s56,s56,98566144
s_lshl_b32 s61,s78,11
s_add_u32 s61,s61,110587904
s_add_u32 s78,s45,s46
s_lshr_b32 s78,s78,1
s_lshl_b32 s62,s78,11
s_add_u32 s62,s62,110587904
s_add_u32 m0,s65,0
s_nop 0
D 9,56,0
D 9,56,1024
D 9,56,2048
D 9,56,3072
Lr_bps_%=:
buffer_load_dword v18,v6,s[36:39],s61 offen offset:0 sc1
s_add_u32 s82,s82,1
s_waitcnt vmcnt(0)
v_cmp_eq_u32 vcc,s53,v18
s_nop 0
s_cmp_eq_u64 vcc,-1
s_cbranch_scc1 Lr_bqs_%=
s_sleep 2
s_cmp_lt_u32 s82,20000
s_cbranch_scc1 Lr_bps_%=
Lr_bqs_%=:
s_add_u32 s78,s55,71303168
buffer_load_dwordx4 v[164:167],v1,s[36:39],s78 offen offset:0 sc1
buffer_load_dwordx4 v[168:171],v1,s[36:39],s78 offen offset:2048 sc1
s_add_u32 s78,s55,71307264
buffer_load_dwordx4 v[172:175],v1,s[36:39],s78 offen offset:0 sc1
buffer_load_dwordx4 v[176:179],v1,s[36:39],s78 offen offset:2048 sc1
s_add_u32 s78,s55,89128960
buffer_load_dwordx4 v[180:183],v1,s[36:39],s78 offen offset:0 sc1
buffer_load_dwordx4 v[184:187],v1,s[36:39],s78 offen offset:2048 sc1
s_add_u32 s78,s55,89133056
buffer_load_dwordx4 v[188:191],v1,s[36:39],s78 offen offset:0 sc1
buffer_load_dwordx4 v[192:195],v1,s[36:39],s78 offen offset:2048 sc1
buffer_load_dword v18,v6,s[36:39],s62 offen offset:0 sc1
s_cmp_eq_u32 s93,0
s_cbranch_scc1 Lr_h2_%=
s_mov_b64 s[40:41],%24
s_mov_b32 s42,7340032
s_mov_b32 s43,0x00020000
s_lshl_b32 s57,s44,11
L 0
s_barrier
s_add_u32 s55,s50,2097152
s_add_u32 m0,s57,73728
s_nop 0
buffer_load_dwordx4 v13,s[40:43],s55 offen offset:0  lds
s_add_u32 m0,s57,74752
s_nop 0
buffer_load_dwordx4 v14,s[40:43],s55 offen offset:0  lds
s_add_u32 m0,s57,81792
s_nop 0
buffer_load_dwordx4 v13,s[40:43],s55 offen offset:128  lds
s_add_u32 m0,s57,82816
s_nop 0
buffer_load_dwordx4 v14,s[40:43],s55 offen offset:128  lds
v_add_u32 v44,90112,v15
v_add_u32 v45,90112,v16
T a[160:161],44,0
T a[162:163],44,512
T a[164:165],45,0
T a[166:167],45,512
T a[168:169],44,4096
T a[170:171],44,4608
T a[172:173],45,4096
T a[174:175],45,4608
L 0
s_barrier
s_add_u32 s55,s50,2228224
s_add_u32 m0,s57,90112
s_nop 0
buffer_load_dwordx4 v13,s[40:43],s55 offen offset:0  lds
s_add_u32 m0,s57,91136
s_nop 0
buffer_load_dwordx4 v14,s[40:43],s55 offen offset:0  lds
s_add_u32 m0,s57,98176
s_nop 0
buffer_load_dwordx4 v13,s[40:43],s55 offen offset:128  lds
s_add_u32 m0,s57,99200
s_nop 0
buffer_load_dwordx4 v14,s[40:43],s55 offen offset:128  lds
v_add_u32 v44,106496,v15
v_add_u32 v45,106496,v16
T a[176:177],44,0
T a[178:179],44,512
T a[180:181],45,0
T a[182:183],45,512
T a[184:185],44,4096
T a[186:187],44,4608
T a[188:189],45,4096
T a[190:191],45,4608
L 0
s_barrier
s_add_u32 s55,s50,2359296
s_add_u32 m0,s57,106496
s_nop 0
buffer_load_dwordx4 v13,s[40:43],s55 offen offset:0  lds
s_add_u32 m0,s57,107520
s_nop 0
buffer_load_dwordx4 v14,s[40:43],s55 offen offset:0  lds
s_add_u32 m0,s57,114560
s_nop 0
buffer_load_dwordx4 v13,s[40:43],s55 offen offset:128  lds
s_add_u32 m0,s57,115584
s_nop 0
buffer_load_dwordx4 v14,s[40:43],s55 offen offset:128  lds
v_add_u32 v44,122880,v15
v_add_u32 v45,122880,v16
T a[192:193],44,0
T a[194:195],44,512
T a[196:197],45,0
T a[198:199],45,512
T a[200:201],44,4096
T a[202:203],44,4608
T a[204:205],45,4096
T a[206:207],45,4608
L 0
s_barrier
s_add_u32 s55,s50,2490368
s_add_u32 m0,s57,122880
s_nop 0
buffer_load_dwordx4 v13,s[40:43],s55 offen offset:0  lds
s_add_u32 m0,s57,123904
s_nop 0
buffer_load_dwordx4 v14,s[40:43],s55 offen offset:0  lds
s_add_u32 m0,s57,130944
s_nop 0
buffer_load_dwordx4 v13,s[40:43],s55 offen offset:128  lds
s_add_u32 m0,s57,131968
s_nop 0
buffer_load_dwordx4 v14,s[40:43],s55 offen offset:128  lds
v_add_u32 v44,139264,v15
v_add_u32 v45,139264,v16
T a[208:209],44,0
T a[210:211],44,512
T a[212:213],45,0
T a[214:215],45,512
T a[216:217],44,4096
T a[218:219],44,4608
T a[220:221],45,4096
T a[222:223],45,4608
L 0
s_barrier
s_add_u32 s55,s50,2621440
s_add_u32 m0,s57,139264
s_nop 0
buffer_load_dwordx4 v13,s[40:43],s55 offen offset:0  lds
s_add_u32 m0,s57,140288
s_nop 0
buffer_load_dwordx4 v14,s[40:43],s55 offen offset:0  lds
s_add_u32 m0,s57,147328
s_nop 0
buffer_load_dwordx4 v13,s[40:43],s55 offen offset:128  lds
s_add_u32 m0,s57,148352
s_nop 0
buffer_load_dwordx4 v14,s[40:43],s55 offen offset:128  lds
v_add_u32 v44,40960,v15
v_add_u32 v45,40960,v16
T a[224:225],44,0
T a[226:227],44,512
T a[228:229],45,0
T a[230:231],45,512
T a[232:233],44,4096
T a[234:235],44,4608
T a[236:237],45,4096
T a[238:239],45,4608
L 0
s_barrier
s_add_u32 s55,s50,2752512
s_add_u32 m0,s57,40960
s_nop 0
buffer_load_dwordx4 v13,s[40:43],s55 offen offset:0  lds
s_add_u32 m0,s57,41984
s_nop 0
buffer_load_dwordx4 v14,s[40:43],s55 offen offset:0  lds
s_add_u32 m0,s57,49024
s_nop 0
buffer_load_dwordx4 v13,s[40:43],s55 offen offset:128  lds
s_add_u32 m0,s57,50048
s_nop 0
buffer_load_dwordx4 v14,s[40:43],s55 offen offset:128  lds
v_add_u32 v44,57344,v15
v_add_u32 v45,57344,v16
T a[240:241],44,0
T a[242:243],44,512
T a[244:245],45,0
T a[246:247],45,512
T a[248:249],44,4096
T a[250:251],44,4608
T a[252:253],45,4096
T a[254:255],45,4608
L 0
s_barrier
s_waitcnt vmcnt(24)
Lr_h2_%=:
s_lshr_b32 s78,s45,1
s_and_b32 s79,s45,1
s_lshl_b32 s79,s79,7
s_lshl_b32 s57,s78,15
s_add_u32 s57,s57,s79
s_add_u32 s57,s57,12582912
s_add_u32 s58,s57,4096
s_lshl_b32 s59,s78,9
s_add_u32 s59,s59,s79
s_add_u32 s59,s59,107020288
s_lshl_b32 s60,s45,9
s_add_u32 s60,s60,s66
s_add_u32 s60,s60,131072
s_add_u32 s45,s45,s46
s_lshr_b32 s78,s45,1
s_and_b32 s79,s45,1
s_lshl_b32 s79,s79,7
s_lshl_b32 s55,s78,15
s_add_u32 s55,s55,s79
s_mul_i32 s56,s45,16384
s_add_u32 s56,s56,98566144
s_lshl_b32 s61,s78,11
s_add_u32 s61,s61,110587904
s_add_u32 s78,s45,s46
s_lshr_b32 s78,s78,1
s_lshl_b32 s62,s78,11
s_add_u32 s62,s62,110587904
s_waitcnt vmcnt(0)
Lr_it0_%=:
T v[68:69],10,0
T v[70:71],10,64
T v[72:73],10,1024
T v[74:75],10,1088
T v[76:77],10,2048
T v[78:79],10,2112
T v[80:81],10,3072
T v[82:83],10,3136
s_nop 0
v_cmp_eq_u32 vcc,s53,v18
s_nop 0
s_cmp_eq_u64 vcc,-1
s_cselect_b32 s78,s45,256
s_cmp_lt_u32 s78,256
s_cbranch_scc1 Lr_ta0_%=
buffer_load_dword v18,v6,s[36:39],s61 offen offset:0 sc1
v_mfma_f32_16x16x32_bf16 v[116:119],v[26:29],v[164:167],0
L 6
v_lshlrev_b32 v84,16,v68
v_and_b32 v85,0xffff0000,v68
v_mfma_f32_16x16x32_bf16 v[120:123],v[24:27],v[164:167],0
L 4
v_lshlrev_b32 v86,16,v69
v_and_b32 v87,0xffff0000,v69
M 116,v[26:29],180
L 2
v_lshlrev_b32 v88,16,v70
v_and_b32 v89,0xffff0000,v70
M 120,v[24:27],180
L 0
v_lshlrev_b32 v90,16,v71
v_and_b32 v91,0xffff0000,v71
v_mfma_f32_16x16x32_bf16 v[124:127],v[26:29],v[168:171],0
s_add_u32 m0,s65,16384
v_lshlrev_b32 v92,16,v72
v_and_b32 v93,0xffff0000,v72
v_mfma_f32_16x16x32_bf16 v[128:131],v[24:27],v[168:171],0
D 9,56,0
M 124,v[26:29],184
D 9,56,1024
M 128,v[24:27],184
D 9,56,2048
v_mfma_f32_16x16x32_bf16 v[132:135],v[26:29],v[172:175],0
D 9,56,3072
v_mfma_f32_16x16x32_bf16 v[136:139],v[24:27],v[172:175],0
v_cvt_pk_bf16_f32 v148,v116,v117
v_cvt_pk_bf16_f32 v149,v118,v119
M 132,v[26:29],188
v_cvt_pk_bf16_f32 v150,v120,v121
v_cvt_pk_bf16_f32 v151,v122,v123
M 136,v[24:27],188
v_cvt_pk_bf16_f32 v152,v124,v125
v_cvt_pk_bf16_f32 v153,v126,v127
)"
R"(v_mfma_f32_16x16x32_bf16 v[140:143],v[26:29],v[176:179],0
v_cvt_pk_bf16_f32 v154,v128,v129
v_cvt_pk_bf16_f32 v155,v130,v131
v_mfma_f32_16x16x32_bf16 v[144:147],v[24:27],v[176:179],0
v_lshlrev_b32 v94,16,v73
v_and_b32 v95,0xffff0000,v73
M 140,v[26:29],192
v_cvt_pk_bf16_f32 v156,v132,v133
v_cvt_pk_bf16_f32 v157,v134,v135
M 144,v[24:27],192
v_cvt_pk_bf16_f32 v158,v136,v137
v_cvt_pk_bf16_f32 v159,v138,v139
v_mfma_f32_16x16x32_bf16 v[36:39],v[32:35],v[148:151],0
v_lshlrev_b32 v96,16,v74
v_and_b32 v97,0xffff0000,v74
v_mfma_f32_16x16x32_bf16 v[40:43],v[148:151],v[148:151],0
v_lshlrev_b32 v98,16,v75
v_and_b32 v99,0xffff0000,v75
M 36,v[32:35],152
v_cvt_pk_bf16_f32 v160,v140,v141
v_cvt_pk_bf16_f32 v161,v142,v143
M 40,v[152:155],152
v_cvt_pk_bf16_f32 v162,v144,v145
v_cvt_pk_bf16_f32 v163,v146,v147
M 36,v[32:35],156
v_lshlrev_b32 v100,16,v76
v_and_b32 v101,0xffff0000,v76
M 40,v[156:159],156
v_lshlrev_b32 v102,16,v77
v_and_b32 v103,0xffff0000,v77
M 36,v[32:35],160
v_lshlrev_b32 v104,16,v78
v_and_b32 v105,0xffff0000,v78
M 40,v[160:163],160
v_lshlrev_b32 v106,16,v79
v_and_b32 v107,0xffff0000,v79
v_lshlrev_b32 v108,16,v80
v_and_b32 v109,0xffff0000,v80
v_lshlrev_b32 v110,16,v81
v_and_b32 v111,0xffff0000,v81
v_lshlrev_b32 v112,16,v82
v_and_b32 v113,0xffff0000,v82
v_lshlrev_b32 v114,16,v83
v_and_b32 v115,0xffff0000,v83
v_add_f32_dpp v44,v36,v36 row_ror:8 row_mask:0xf bank_mask:0xf
v_cndmask_b32 v45,v40,v41,s[70:71]
v_cndmask_b32 v46,v42,v43,s[70:71]
ds_write_b32 v2,v44 offset:0
v_cndmask_b32 v45,v45,v46,s[72:73]
ds_write_b32 v0,v45 offset:0
L 0
s_barrier
R 52,3,0
R 56,3,16
R 60,3,32
L 0
v_pk_add_f32 v[52:53],v[52:53],v[54:55]
v_pk_add_f32 v[56:57],v[56:57],v[58:59]
v_pk_add_f32 v[60:61],v[60:61],v[62:63]
v_add_f32 v52,v52,v53
v_pk_add_f32 v[56:57],v[56:57],v[60:61]
v_mul_f32 v52,s68,v52
v_add_f32 v53,v56,v57
v_mul_f32 v53,s68,v53
v_fma_f32 v53,-v52,v52,v53
v_max_f32 v53,0,v53
s_waitcnt vmcnt(5)
buffer_store_dword v17,v5,s[36:39],s63 offen offset:0 sc0 sc1
s_mov_b32 s63,0x7fff0000
s_waitcnt vmcnt(5)
v_cmp_eq_u32 vcc,s53,v18
s_nop 0
s_cmp_eq_u64 vcc,-1
s_cselect_b32 s78,s45,256
s_cmp_lt_u32 s78,256
s_cbranch_scc1 Lr_tb0_%=
buffer_load_dword v18,v6,s[36:39],s61 offen offset:0 sc1
v_add_f32 v53,s69,v53
v_rsq_f32 v48,v53
s_nop 0
v_mul_f32_e64 v50,-v52,v48
v_mov_b32 v49,v48
v_mov_b32 v51,v50
v_pk_fma_f32 v[52:53],v[116:117],v[48:49],v[50:51]
v_pk_fma_f32 v[54:55],v[118:119],v[48:49],v[50:51]
v_pk_fma_f32 v[56:57],v[120:121],v[48:49],v[50:51]
v_pk_fma_f32 v[58:59],v[122:123],v[48:49],v[50:51]
v_pk_fma_f32 v[116:117],v[52:53],v[84:85],v[116:117]
v_pk_fma_f32 v[118:119],v[54:55],v[86:87],v[118:119]
v_pk_fma_f32 v[120:121],v[56:57],v[88:89],v[120:121]
v_pk_fma_f32 v[122:123],v[58:59],v[90:91],v[122:123]
v_cvt_pk_bf16_f32 v148,v116,v117
v_cvt_pk_bf16_f32 v149,v118,v119
v_cvt_pk_bf16_f32 v150,v120,v121
v_cvt_pk_bf16_f32 v151,v122,v123
v_pk_fma_f32 v[52:53],v[124:125],v[48:49],v[50:51]
v_pk_fma_f32 v[54:55],v[126:127],v[48:49],v[50:51]
v_pk_fma_f32 v[56:57],v[128:129],v[48:49],v[50:51]
v_pk_fma_f32 v[58:59],v[130:131],v[48:49],v[50:51]
v_pk_fma_f32 v[124:125],v[52:53],v[92:93],v[124:125]
v_pk_fma_f32 v[126:127],v[54:55],v[94:95],v[126:127]
v_pk_fma_f32 v[128:129],v[56:57],v[96:97],v[128:129]
v_pk_fma_f32 v[130:131],v[58:59],v[98:99],v[130:131]
v_cvt_pk_bf16_f32 v152,v124,v125
v_cvt_pk_bf16_f32 v153,v126,v127
v_cvt_pk_bf16_f32 v154,v128,v129
v_cvt_pk_bf16_f32 v155,v130,v131
v_pk_fma_f32 v[52:53],v[132:133],v[48:49],v[50:51]
v_pk_fma_f32 v[54:55],v[134:135],v[48:49],v[50:51]
v_pk_fma_f32 v[56:57],v[136:137],v[48:49],v[50:51]
v_pk_fma_f32 v[58:59],v[138:139],v[48:49],v[50:51]
v_pk_fma_f32 v[132:133],v[52:53],v[100:101],v[132:133]
v_pk_fma_f32 v[134:135],v[54:55],v[102:103],v[134:135]
v_pk_fma_f32 v[136:137],v[56:57],v[104:105],v[136:137]
v_pk_fma_f32 v[138:139],v[58:59],v[106:107],v[138:139]
v_cvt_pk_bf16_f32 v156,v132,v133
v_cvt_pk_bf16_f32 v157,v134,v135
v_cvt_pk_bf16_f32 v158,v136,v137
v_cvt_pk_bf16_f32 v159,v138,v139
v_pk_fma_f32 v[52:53],v[140:141],v[48:49],v[50:51]
v_pk_fma_f32 v[54:55],v[142:143],v[48:49],v[50:51]
v_pk_fma_f32 v[56:57],v[144:145],v[48:49],v[50:51]
v_pk_fma_f32 v[58:59],v[146:147],v[48:49],v[50:51]
v_pk_fma_f32 v[140:141],v[52:53],v[108:109],v[140:141]
v_pk_fma_f32 v[142:143],v[54:55],v[110:111],v[142:143]
v_pk_fma_f32 v[144:145],v[56:57],v[112:113],v[144:145]
v_pk_fma_f32 v[146:147],v[58:59],v[114:115],v[146:147]
v_cvt_pk_bf16_f32 v160,v140,v141
v_cvt_pk_bf16_f32 v161,v142,v143
v_cvt_pk_bf16_f32 v162,v144,v145
v_cvt_pk_bf16_f32 v163,v146,v147
v_mfma_f32_16x16x32_bf16 v[36:39],v[32:35],v[148:151],0
buffer_store_dwordx4 v[148:151],v1,s[36:39],s57 offen offset:0 sc0 sc1
v_mfma_f32_16x16x32_bf16 v[40:43],v[148:151],v[148:151],0
M 36,v[32:35],152
buffer_store_dwordx4 v[152:155],v1,s[36:39],s57 offen offset:2048 sc0 sc1
M 40,v[152:155],152
M 36,v[32:35],156
buffer_store_dwordx4 v[156:159],v1,s[36:39],s58 offen offset:0 sc0 sc1
M 40,v[156:159],156
M 36,v[32:35],160
buffer_store_dwordx4 v[160:163],v1,s[36:39],s58 offen offset:2048 sc0 sc1
M 40,v[160:163],160
s_nop 5
v_add_f32_dpp v44,v36,v36 row_ror:8 row_mask:0xf bank_mask:0xf
s_nop 0
v_cndmask_b32 v45,v40,v41,s[70:71]
v_cndmask_b32 v46,v42,v43,s[70:71]
ds_write_b32 v2,v44 offset:1024
v_cndmask_b32 v45,v45,v46,s[72:73]
ds_write_b32 v0,v45 offset:1024
L 0
s_barrier
R 52,3,1024
R 56,3,1040
R 60,3,1056
L 0
v_pk_add_f32 v[52:53],v[52:53],v[54:55]
v_pk_add_f32 v[56:57],v[56:57],v[58:59]
v_pk_add_f32 v[60:61],v[60:61],v[62:63]
v_add_f32 v52,v52,v53
v_pk_add_f32 v[56:57],v[56:57],v[60:61]
v_mul_f32 v52,s68,v52
v_add_f32 v53,v56,v57
v_mul_f32 v53,s68,v53
v_fma_f32 v53,-v52,v52,v53
v_max_f32 v53,0,v53
v_add_f32 v54,s69,v53
v_fma_f32 v55,s69,v54,v53
v_rsq_f32 v65,v54
v_rsq_f32 v54,v55
v_mul_f32_e64 v64,-v52,v65
v_mul_f32 v63,v55,v54
v_mul_f32 v61,s76,v54
v_mul_f32 v55,v54,v54
v_mul_f32 v62,s77,v55
v_mul_f32 v62,v62,v61
v_cndmask_b32 v60,v52,v64,s[74:75]
v_cndmask_b32 v61,v61,v65,s[74:75]
buffer_store_dwordx4 v[60:63],v4,s[36:39],s59 offen offset:0 sc0 sc1
s_mov_b32 s63,s60
s_waitcnt vmcnt(5)
v_cmp_eq_u32 vcc,s53,v18
s_nop 0
s_cmp_eq_u64 vcc,-1
s_cselect_b32 s78,s45,256
s_cmp_lt_u32 s78,256
s_cbranch_scc1 Lr_tc0_%=
s_waitcnt vmcnt(0)
buffer_store_dword v17,v5,s[36:39],s63 offen offset:0 sc0 sc1
s_mov_b32 s63,0x7fff0000
s_cmp_ge_u32 s45,256
s_cbranch_scc1 Lr_done_%=
Lr_bp0_%=:
buffer_load_dword v18,v6,s[36:39],s61 offen offset:0 sc1
s_add_u32 s82,s82,1
s_waitcnt vmcnt(0)
v_cmp_eq_u32 vcc,s53,v18
s_nop 0
s_cmp_eq_u64 vcc,-1
s_cbranch_scc1 Lr_bq0_%=
s_sleep 2
s_cmp_lt_u32 s82,20000
s_cbranch_scc1 Lr_bp0_%=
Lr_bq0_%=:
s_add_u32 s78,s55,71303168
buffer_load_dwordx4 v[196:199],v1,s[36:39],s78 offen offset:0 sc1
buffer_load_dwordx4 v[200:203],v1,s[36:39],s78 offen offset:2048 sc1
s_add_u32 s78,s55,71307264
buffer_load_dwordx4 v[204:207],v1,s[36:39],s78 offen offset:0 sc1
buffer_load_dwordx4 v[208:211],v1,s[36:39],s78 offen offset:2048 sc1
s_add_u32 s78,s55,89128960
buffer_load_dwordx4 v[212:215],v1,s[36:39],s78 offen offset:0 sc1
buffer_load_dwordx4 v[216:219],v1,s[36:39],s78 offen offset:2048 sc1
s_add_u32 s78,s55,89133056
buffer_load_dwordx4 v[220:223],v1,s[36:39],s78 offen offset:0 sc1
buffer_load_dwordx4 v[224:227],v1,s[36:39],s78 offen offset:2048 sc1
buffer_load_dword v18,v6,s[36:39],s62 offen offset:0 sc1
s_lshr_b32 s78,s45,1
s_and_b32 s79,s45,1
s_lshl_b32 s79,s79,7
s_lshl_b32 s57,s78,15
s_add_u32 s57,s57,s79
s_add_u32 s57,s57,12582912
s_add_u32 s58,s57,4096
s_lshl_b32 s59,s78,9
s_add_u32 s59,s59,s79
s_add_u32 s59,s59,107020288
s_lshl_b32 s60,s45,9
s_add_u32 s60,s60,s66
s_add_u32 s60,s60,131072
s_add_u32 s45,s45,s46
s_lshr_b32 s78,s45,1
s_and_b32 s79,s45,1
s_lshl_b32 s79,s79,7
s_lshl_b32 s55,s78,15
s_add_u32 s55,s55,s79
s_mul_i32 s56,s45,16384
s_add_u32 s56,s56,98566144
s_lshl_b32 s61,s78,11
s_add_u32 s61,s61,110587904
s_add_u32 s78,s45,s46
s_lshr_b32 s78,s78,1
s_lshl_b32 s62,s78,11
s_add_u32 s62,s62,110587904
s_waitcnt vmcnt(0)
s_branch Lr_it1_%=
Lr_ta0_%=:
v_mfma_f32_16x16x32_bf16 v[116:119],v[26:29],v[164:167],0
s_add_u32 s78,s55,71303168
L 6
L 4
v_mfma_f32_16x16x32_bf16 v[120:123],v[24:27],v[164:167],0
L 2
buffer_load_dwordx4 v[196:199],v1,s[36:39],s78 offen offset:0 sc1
M 116,v[26:29],180
L 0
buffer_load_dwordx4 v[200:203],v1,s[36:39],s78 offen offset:2048 sc1
M 120,v[24:27],180
s_add_u32 s78,s55,71307264
v_lshlrev_b32 v84,16,v68
v_and_b32 v85,0xffff0000,v68
v_mfma_f32_16x16x32_bf16 v[124:127],v[26:29],v[168:171],0
buffer_load_dwordx4 v[204:207],v1,s[36:39],s78 offen offset:0 sc1
v_mfma_f32_16x16x32_bf16 v[128:131],v[24:27],v[168:171],0
buffer_load_dwordx4 v[208:211],v1,s[36:39],s78 offen offset:2048 sc1
M 124,v[26:29],184
s_add_u32 s78,s55,89128960
v_cvt_pk_bf16_f32 v148,v116,v117
v_cvt_pk_bf16_f32 v149,v118,v119
M 128,v[24:27],184
buffer_load_dwordx4 v[212:215],v1,s[36:39],s78 offen offset:0 sc1
v_mfma_f32_16x16x32_bf16 v[132:135],v[26:29],v[172:175],0
buffer_load_dwordx4 v[216:219],v1,s[36:39],s78 offen offset:2048 sc1
v_mfma_f32_16x16x32_bf16 v[136:139],v[24:27],v[172:175],0
s_add_u32 s78,s55,89133056
v_cvt_pk_bf16_f32 v150,v120,v121
v_cvt_pk_bf16_f32 v151,v122,v123
M 132,v[26:29],188
buffer_load_dwordx4 v[220:223],v1,s[36:39],s78 offen offset:0 sc1
M 136,v[24:27],188
buffer_load_dwordx4 v[224:227],v1,s[36:39],s78 offen offset:2048 sc1
v_mfma_f32_16x16x32_bf16 v[140:143],v[26:29],v[176:179],0
s_add_u32 m0,s65,16384
v_cvt_pk_bf16_f32 v152,v124,v125
v_cvt_pk_bf16_f32 v153,v126,v127
v_mfma_f32_16x16x32_bf16 v[144:147],v[24:27],v[176:179],0
D 9,56,0
M 140,v[26:29],192
D 9,56,1024
M 144,v[24:27],192
D 9,56,2048
v_mfma_f32_16x16x32_bf16 v[36:39],v[32:35],v[148:151],0
v_cvt_pk_bf16_f32 v154,v128,v129
v_cvt_pk_bf16_f32 v155,v130,v131
v_mfma_f32_16x16x32_bf16 v[40:43],v[148:151],v[148:151],0
D 9,56,3072
M 36,v[32:35],152
v_cvt_pk_bf16_f32 v156,v132,v133
v_cvt_pk_bf16_f32 v157,v134,v135
v_cvt_pk_bf16_f32 v158,v136,v137
v_cvt_pk_bf16_f32 v159,v138,v139
M 40,v[152:155],152
v_cvt_pk_bf16_f32 v160,v140,v141
v_cvt_pk_bf16_f32 v161,v142,v143
M 36,v[32:35],156
v_cvt_pk_bf16_f32 v162,v144,v145
v_cvt_pk_bf16_f32 v163,v146,v147
M 40,v[156:159],156
v_lshlrev_b32 v86,16,v69
v_and_b32 v87,0xffff0000,v69
M 36,v[32:35],160
v_lshlrev_b32 v88,16,v70
v_and_b32 v89,0xffff0000,v70
M 40,v[160:163],160
v_lshlrev_b32 v90,16,v71
v_and_b32 v91,0xffff0000,v71
v_lshlrev_b32 v92,16,v72
v_and_b32 v93,0xffff0000,v72
v_lshlrev_b32 v94,16,v73
v_and_b32 v95,0xffff0000,v73
v_lshlrev_b32 v96,16,v74
v_and_b32 v97,0xffff0000,v74
v_lshlrev_b32 v98,16,v75
v_and_b32 v99,0xffff0000,v75
v_lshlrev_b32 v100,16,v76
v_and_b32 v101,0xffff0000,v76
v_lshlrev_b32 v102,16,v77
v_and_b32 v103,0xffff0000,v77
v_lshlrev_b32 v104,16,v78
v_and_b32 v105,0xffff0000,v78
v_lshlrev_b32 v106,16,v79
v_and_b32 v107,0xffff0000,v79
v_lshlrev_b32 v108,16,v80
v_and_b32 v109,0xffff0000,v80
v_lshlrev_b32 v110,16,v81
v_and_b32 v111,0xffff0000,v81
v_lshlrev_b32 v112,16,v82
v_and_b32 v113,0xffff0000,v82
v_lshlrev_b32 v114,16,v83
v_and_b32 v115,0xffff0000,v83
v_add_f32_dpp v44,v36,v36 row_ror:8 row_mask:0xf bank_mask:0xf
v_cndmask_b32 v45,v40,v41,s[70:71]
v_cndmask_b32 v46,v42,v43,s[70:71]
ds_write_b32 v2,v44 offset:0
v_cndmask_b32 v45,v45,v46,s[72:73]
ds_write_b32 v0,v45 offset:0
L 0
s_barrier
R 52,3,0
R 56,3,16
R 60,3,32
L 0
v_pk_add_f32 v[52:53],v[52:53],v[54:55]
v_pk_add_f32 v[56:57],v[56:57],v[58:59]
v_pk_add_f32 v[60:61],v[60:61],v[62:63]
v_add_f32 v52,v52,v53
v_pk_add_f32 v[56:57],v[56:57],v[60:61]
v_mul_f32 v52,s68,v52
v_add_f32 v53,v56,v57
v_mul_f32 v53,s68,v53
v_fma_f32 v53,-v52,v52,v53
v_max_f32 v53,0,v53
buffer_load_dword v18,v6,s[36:39],s62 offen offset:0 sc1
v_add_f32 v53,s69,v53
v_rsq_f32 v48,v53
s_nop 0
v_mul_f32_e64 v50,-v52,v48
v_mov_b32 v49,v48
v_mov_b32 v51,v50
v_pk_fma_f32 v[52:53],v[116:117],v[48:49],v[50:51]
v_pk_fma_f32 v[54:55],v[118:119],v[48:49],v[50:51]
v_pk_fma_f32 v[56:57],v[120:121],v[48:49],v[50:51]
v_pk_fma_f32 v[58:59],v[122:123],v[48:49],v[50:51]
v_pk_fma_f32 v[116:117],v[52:53],v[84:85],v[116:117]
v_pk_fma_f32 v[118:119],v[54:55],v[86:87],v[118:119]
v_pk_fma_f32 v[120:121],v[56:57],v[88:89],v[120:121]
v_pk_fma_f32 v[122:123],v[58:59],v[90:91],v[122:123]
v_cvt_pk_bf16_f32 v148,v116,v117
v_cvt_pk_bf16_f32 v149,v118,v119
v_cvt_pk_bf16_f32 v150,v120,v121
v_cvt_pk_bf16_f32 v151,v122,v123
v_pk_fma_f32 v[52:53],v[124:125],v[48:49],v[50:51]
v_pk_fma_f32 v[54:55],v[126:127],v[48:49],v[50:51]
v_pk_fma_f32 v[56:57],v[128:129],v[48:49],v[50:51]
v_pk_fma_f32 v[58:59],v[130:131],v[48:49],v[50:51]
v_pk_fma_f32 v[124:125],v[52:53],v[92:93],v[124:125]
v_pk_fma_f32 v[126:127],v[54:55],v[94:95],v[126:127]
v_pk_fma_f32 v[128:129],v[56:57],v[96:97],v[128:129]
v_pk_fma_f32 v[130:131],v[58:59],v[98:99],v[130:131]
v_cvt_pk_bf16_f32 v152,v124,v125
v_cvt_pk_bf16_f32 v153,v126,v127
v_cvt_pk_bf16_f32 v154,v128,v129
v_cvt_pk_bf16_f32 v155,v130,v131
v_pk_fma_f32 v[52:53],v[132:133],v[48:49],v[50:51]
v_pk_fma_f32 v[54:55],v[134:135],v[48:49],v[50:51]
v_pk_fma_f32 v[56:57],v[136:137],v[48:49],v[50:51]
v_pk_fma_f32 v[58:59],v[138:139],v[48:49],v[50:51]
v_pk_fma_f32 v[132:133],v[52:53],v[100:101],v[132:133]
v_pk_fma_f32 v[134:135],v[54:55],v[102:103],v[134:135]
v_pk_fma_f32 v[136:137],v[56:57],v[104:105],v[136:137]
v_pk_fma_f32 v[138:139],v[58:59],v[106:107],v[138:139]
v_cvt_pk_bf16_f32 v156,v132,v133
v_cvt_pk_bf16_f32 v157,v134,v135
v_cvt_pk_bf16_f32 v158,v136,v137
v_cvt_pk_bf16_f32 v159,v138,v139
v_pk_fma_f32 v[52:53],v[140:141],v[48:49],v[50:51]
v_pk_fma_f32 v[54:55],v[142:143],v[48:49],v[50:51]
v_pk_fma_f32 v[56:57],v[144:145],v[48:49],v[50:51]
v_pk_fma_f32 v[58:59],v[146:147],v[48:49],v[50:51]
v_pk_fma_f32 v[140:141],v[52:53],v[108:109],v[140:141]
v_pk_fma_f32 v[142:143],v[54:55],v[110:111],v[142:143]
v_pk_fma_f32 v[144:145],v[56:57],v[112:113],v[144:145]
v_pk_fma_f32 v[146:147],v[58:59],v[114:115],v[146:147]
v_cvt_pk_bf16_f32 v160,v140,v141
v_cvt_pk_bf16_f32 v161,v142,v143
v_cvt_pk_bf16_f32 v162,v144,v145
v_cvt_pk_bf16_f32 v163,v146,v147
s_waitcnt vmcnt(13)
buffer_store_dword v17,v5,s[36:39],s63 offen offset:0 sc0 sc1
s_mov_b32 s63,0x7fff0000
v_mfma_f32_16x16x32_bf16 v[36:39],v[32:35],v[148:151],0
buffer_store_dwordx4 v[148:151],v1,s[36:39],s57 offen offset:0 sc0 sc1
v_mfma_f32_16x16x32_bf16 v[40:43],v[148:151],v[148:151],0
M 36,v[32:35],152
buffer_store_dwordx4 v[152:155],v1,s[36:39],s57 offen offset:2048 sc0 sc1
M 40,v[152:155],152
M 36,v[32:35],156
buffer_store_dwordx4 v[156:159],v1,s[36:39],s58 offen offset:0 sc0 sc1
M 40,v[156:159],156
M 36,v[32:35],160
buffer_store_dwordx4 v[160:163],v1,s[36:39],s58 offen offset:2048 sc0 sc1
M 40,v[160:163],160
s_nop 5
v_add_f32_dpp v44,v36,v36 row_ror:8 row_mask:0xf bank_mask:0xf
s_nop 0
v_cndmask_b32 v45,v40,v41,s[70:71]
v_cndmask_b32 v46,v42,v43,s[70:71]
ds_write_b32 v2,v44 offset:1024
v_cndmask_b32 v45,v45,v46,s[72:73]
ds_write_b32 v0,v45 offset:1024
L 0
s_barrier
R 52,3,1024
R 56,3,1040
R 60,3,1056
L 0
v_pk_add_f32 v[52:53],v[52:53],v[54:55]
v_pk_add_f32 v[56:57],v[56:57],v[58:59]
v_pk_add_f32 v[60:61],v[60:61],v[62:63]
v_add_f32 v52,v52,v53
v_pk_add_f32 v[56:57],v[56:57],v[60:61]
v_mul_f32 v52,s68,v52
v_add_f32 v53,v56,v57
v_mul_f32 v53,s68,v53
v_fma_f32 v53,-v52,v52,v53
v_max_f32 v53,0,v53
v_add_f32 v54,s69,v53
v_fma_f32 v55,s69,v54,v53
v_rsq_f32 v65,v54
v_rsq_f32 v54,v55
v_mul_f32_e64 v64,-v52,v65
v_mul_f32 v63,v55,v54
v_mul_f32 v61,s76,v54
v_mul_f32 v55,v54,v54
v_mul_f32 v62,s77,v55
v_mul_f32 v62,v62,v61
v_cndmask_b32 v60,v52,v64,s[74:75]
v_cndmask_b32 v61,v61,v65,s[74:75]
buffer_store_dwordx4 v[60:63],v4,s[36:39],s59 offen offset:0 sc0 sc1
s_mov_b32 s63,s60
s_lshr_b32 s78,s45,1
s_and_b32 s79,s45,1
s_lshl_b32 s79,s79,7
s_lshl_b32 s57,s78,15
s_add_u32 s57,s57,s79
s_add_u32 s57,s57,12582912
s_add_u32 s58,s57,4096
s_lshl_b32 s59,s78,9
s_add_u32 s59,s59,s79
s_add_u32 s59,s59,107020288
s_lshl_b32 s60,s45,9
s_add_u32 s60,s60,s66
s_add_u32 s60,s60,131072
s_add_u32 s45,s45,s46
s_lshr_b32 s78,s45,1
s_and_b32 s79,s45,1
s_lshl_b32 s79,s79,7
s_lshl_b32 s55,s78,15
s_add_u32 s55,s55,s79
s_mul_i32 s56,s45,16384
s_add_u32 s56,s56,98566144
s_lshl_b32 s61,s78,11
s_add_u32 s61,s61,110587904
s_add_u32 s78,s45,s46
s_lshr_b32 s78,s78,1
s_lshl_b32 s62,s78,11
s_add_u32 s62,s62,110587904
s_waitcnt vmcnt(6)
s_branch Lr_it1_%=
)"
R"(Lr_tb0_%=:
v_add_f32 v53,s69,v53
v_rsq_f32 v48,v53
s_nop 0
v_mul_f32_e64 v50,-v52,v48
v_mov_b32 v49,v48
v_mov_b32 v51,v50
v_pk_fma_f32 v[52:53],v[116:117],v[48:49],v[50:51]
v_pk_fma_f32 v[54:55],v[118:119],v[48:49],v[50:51]
v_pk_fma_f32 v[56:57],v[120:121],v[48:49],v[50:51]
v_pk_fma_f32 v[58:59],v[122:123],v[48:49],v[50:51]
s_add_u32 s78,s55,71303168
buffer_load_dwordx4 v[196:199],v1,s[36:39],s78 offen offset:0 sc1
buffer_load_dwordx4 v[200:203],v1,s[36:39],s78 offen offset:2048 sc1
s_add_u32 s78,s55,71307264
buffer_load_dwordx4 v[204:207],v1,s[36:39],s78 offen offset:0 sc1
buffer_load_dwordx4 v[208:211],v1,s[36:39],s78 offen offset:2048 sc1
v_pk_fma_f32 v[116:117],v[52:53],v[84:85],v[116:117]
v_pk_fma_f32 v[118:119],v[54:55],v[86:87],v[118:119]
v_pk_fma_f32 v[120:121],v[56:57],v[88:89],v[120:121]
v_pk_fma_f32 v[122:123],v[58:59],v[90:91],v[122:123]
v_cvt_pk_bf16_f32 v148,v116,v117
v_cvt_pk_bf16_f32 v149,v118,v119
v_cvt_pk_bf16_f32 v150,v120,v121
v_cvt_pk_bf16_f32 v151,v122,v123
v_pk_fma_f32 v[52:53],v[124:125],v[48:49],v[50:51]
v_pk_fma_f32 v[54:55],v[126:127],v[48:49],v[50:51]
v_pk_fma_f32 v[56:57],v[128:129],v[48:49],v[50:51]
v_pk_fma_f32 v[58:59],v[130:131],v[48:49],v[50:51]
s_add_u32 s78,s55,89128960
buffer_load_dwordx4 v[212:215],v1,s[36:39],s78 offen offset:0 sc1
buffer_load_dwordx4 v[216:219],v1,s[36:39],s78 offen offset:2048 sc1
s_add_u32 s78,s55,89133056
buffer_load_dwordx4 v[220:223],v1,s[36:39],s78 offen offset:0 sc1
buffer_load_dwordx4 v[224:227],v1,s[36:39],s78 offen offset:2048 sc1
v_pk_fma_f32 v[124:125],v[52:53],v[92:93],v[124:125]
v_pk_fma_f32 v[126:127],v[54:55],v[94:95],v[126:127]
v_pk_fma_f32 v[128:129],v[56:57],v[96:97],v[128:129]
v_pk_fma_f32 v[130:131],v[58:59],v[98:99],v[130:131]
v_cvt_pk_bf16_f32 v152,v124,v125
v_cvt_pk_bf16_f32 v153,v126,v127
v_cvt_pk_bf16_f32 v154,v128,v129
v_cvt_pk_bf16_f32 v155,v130,v131
v_pk_fma_f32 v[52:53],v[132:133],v[48:49],v[50:51]
v_pk_fma_f32 v[54:55],v[134:135],v[48:49],v[50:51]
v_pk_fma_f32 v[56:57],v[136:137],v[48:49],v[50:51]
v_pk_fma_f32 v[58:59],v[138:139],v[48:49],v[50:51]
v_pk_fma_f32 v[132:133],v[52:53],v[100:101],v[132:133]
v_pk_fma_f32 v[134:135],v[54:55],v[102:103],v[134:135]
v_pk_fma_f32 v[136:137],v[56:57],v[104:105],v[136:137]
v_pk_fma_f32 v[138:139],v[58:59],v[106:107],v[138:139]
v_cvt_pk_bf16_f32 v156,v132,v133
v_cvt_pk_bf16_f32 v157,v134,v135
v_cvt_pk_bf16_f32 v158,v136,v137
v_cvt_pk_bf16_f32 v159,v138,v139
v_pk_fma_f32 v[52:53],v[140:141],v[48:49],v[50:51]
v_pk_fma_f32 v[54:55],v[142:143],v[48:49],v[50:51]
v_pk_fma_f32 v[56:57],v[144:145],v[48:49],v[50:51]
v_pk_fma_f32 v[58:59],v[146:147],v[48:49],v[50:51]
v_pk_fma_f32 v[140:141],v[52:53],v[108:109],v[140:141]
v_pk_fma_f32 v[142:143],v[54:55],v[110:111],v[142:143]
v_pk_fma_f32 v[144:145],v[56:57],v[112:113],v[144:145]
v_pk_fma_f32 v[146:147],v[58:59],v[114:115],v[146:147]
v_cvt_pk_bf16_f32 v160,v140,v141
v_cvt_pk_bf16_f32 v161,v142,v143
v_cvt_pk_bf16_f32 v162,v144,v145
v_cvt_pk_bf16_f32 v163,v146,v147
v_mfma_f32_16x16x32_bf16 v[36:39],v[32:35],v[148:151],0
buffer_load_dword v18,v6,s[36:39],s62 offen offset:0 sc1
buffer_store_dwordx4 v[148:151],v1,s[36:39],s57 offen offset:0 sc0 sc1
v_mfma_f32_16x16x32_bf16 v[40:43],v[148:151],v[148:151],0
M 36,v[32:35],152
buffer_store_dwordx4 v[152:155],v1,s[36:39],s57 offen offset:2048 sc0 sc1
M 40,v[152:155],152
M 36,v[32:35],156
buffer_store_dwordx4 v[156:159],v1,s[36:39],s58 offen offset:0 sc0 sc1
M 40,v[156:159],156
M 36,v[32:35],160
buffer_store_dwordx4 v[160:163],v1,s[36:39],s58 offen offset:2048 sc0 sc1
M 40,v[160:163],160
s_nop 5
v_add_f32_dpp v44,v36,v36 row_ror:8 row_mask:0xf bank_mask:0xf
s_nop 0
v_cndmask_b32 v45,v40,v41,s[70:71]
v_cndmask_b32 v46,v42,v43,s[70:71]
ds_write_b32 v2,v44 offset:1024
v_cndmask_b32 v45,v45,v46,s[72:73]
ds_write_b32 v0,v45 offset:1024
L 0
s_barrier
R 52,3,1024
R 56,3,1040
R 60,3,1056
L 0
v_pk_add_f32 v[52:53],v[52:53],v[54:55]
v_pk_add_f32 v[56:57],v[56:57],v[58:59]
v_pk_add_f32 v[60:61],v[60:61],v[62:63]
v_add_f32 v52,v52,v53
v_pk_add_f32 v[56:57],v[56:57],v[60:61]
v_mul_f32 v52,s68,v52
v_add_f32 v53,v56,v57
v_mul_f32 v53,s68,v53
v_fma_f32 v53,-v52,v52,v53
v_max_f32 v53,0,v53
v_add_f32 v54,s69,v53
v_fma_f32 v55,s69,v54,v53
v_rsq_f32 v65,v54
v_rsq_f32 v54,v55
v_mul_f32_e64 v64,-v52,v65
v_mul_f32 v63,v55,v54
v_mul_f32 v61,s76,v54
v_mul_f32 v55,v54,v54
v_mul_f32 v62,s77,v55
v_mul_f32 v62,v62,v61
v_cndmask_b32 v60,v52,v64,s[74:75]
v_cndmask_b32 v61,v61,v65,s[74:75]
buffer_store_dwordx4 v[60:63],v4,s[36:39],s59 offen offset:0 sc0 sc1
s_mov_b32 s63,s60
s_lshr_b32 s78,s45,1
s_and_b32 s79,s45,1
s_lshl_b32 s79,s79,7
s_lshl_b32 s57,s78,15
s_add_u32 s57,s57,s79
s_add_u32 s57,s57,12582912
s_add_u32 s58,s57,4096
s_lshl_b32 s59,s78,9
s_add_u32 s59,s59,s79
s_add_u32 s59,s59,107020288
s_lshl_b32 s60,s45,9
s_add_u32 s60,s60,s66
s_add_u32 s60,s60,131072
s_add_u32 s45,s45,s46
s_lshr_b32 s78,s45,1
s_and_b32 s79,s45,1
s_lshl_b32 s79,s79,7
s_lshl_b32 s55,s78,15
s_add_u32 s55,s55,s79
s_mul_i32 s56,s45,16384
s_add_u32 s56,s56,98566144
s_lshl_b32 s61,s78,11
s_add_u32 s61,s61,110587904
s_add_u32 s78,s45,s46
s_lshr_b32 s78,s78,1
s_lshl_b32 s62,s78,11
s_add_u32 s62,s62,110587904
s_waitcnt vmcnt(5)
s_branch Lr_it1_%=
Lr_tc0_%=:
s_add_u32 s78,s55,71303168
buffer_load_dwordx4 v[196:199],v1,s[36:39],s78 offen offset:0 sc1
buffer_load_dwordx4 v[200:203],v1,s[36:39],s78 offen offset:2048 sc1
s_add_u32 s78,s55,71307264
buffer_load_dwordx4 v[204:207],v1,s[36:39],s78 offen offset:0 sc1
buffer_load_dwordx4 v[208:211],v1,s[36:39],s78 offen offset:2048 sc1
s_add_u32 s78,s55,89128960
buffer_load_dwordx4 v[212:215],v1,s[36:39],s78 offen offset:0 sc1
buffer_load_dwordx4 v[216:219],v1,s[36:39],s78 offen offset:2048 sc1
s_add_u32 s78,s55,89133056
buffer_load_dwordx4 v[220:223],v1,s[36:39],s78 offen offset:0 sc1
buffer_load_dwordx4 v[224:227],v1,s[36:39],s78 offen offset:2048 sc1
buffer_load_dword v18,v6,s[36:39],s62 offen offset:0 sc1
s_waitcnt vmcnt(9)
buffer_store_dword v17,v5,s[36:39],s63 offen offset:0 sc0 sc1
s_mov_b32 s63,0x7fff0000
s_lshr_b32 s78,s45,1
s_and_b32 s79,s45,1
s_lshl_b32 s79,s79,7
s_lshl_b32 s57,s78,15
s_add_u32 s57,s57,s79
s_add_u32 s57,s57,12582912
s_add_u32 s58,s57,4096
s_lshl_b32 s59,s78,9
s_add_u32 s59,s59,s79
s_add_u32 s59,s59,107020288
s_lshl_b32 s60,s45,9
s_add_u32 s60,s60,s66
s_add_u32 s60,s60,131072
s_add_u32 s45,s45,s46
s_lshr_b32 s78,s45,1
s_and_b32 s79,s45,1
s_lshl_b32 s79,s79,7
s_lshl_b32 s55,s78,15
s_add_u32 s55,s55,s79
s_mul_i32 s56,s45,16384
s_add_u32 s56,s56,98566144
s_lshl_b32 s61,s78,11
s_add_u32 s61,s61,110587904
s_add_u32 s78,s45,s46
s_lshr_b32 s78,s78,1
s_lshl_b32 s62,s78,11
s_add_u32 s62,s62,110587904
s_waitcnt vmcnt(1)
s_branch Lr_it1_%=
Lr_it1_%=:
T v[68:69],10,16384
T v[70:71],10,16448
T v[72:73],10,17408
T v[74:75],10,17472
T v[76:77],10,18432
T v[78:79],10,18496
T v[80:81],10,19456
T v[82:83],10,19520
s_nop 0
v_cmp_eq_u32 vcc,s53,v18
s_nop 0
s_cmp_eq_u64 vcc,-1
s_cselect_b32 s78,s45,256
s_cmp_lt_u32 s78,256
s_cbranch_scc1 Lr_ta1_%=
buffer_load_dword v18,v6,s[36:39],s61 offen offset:0 sc1
v_mfma_f32_16x16x32_bf16 v[116:119],v[26:29],v[196:199],0
L 6
v_lshlrev_b32 v84,16,v68
v_and_b32 v85,0xffff0000,v68
v_mfma_f32_16x16x32_bf16 v[120:123],v[24:27],v[196:199],0
L 4
v_lshlrev_b32 v86,16,v69
v_and_b32 v87,0xffff0000,v69
M 116,v[26:29],212
L 2
v_lshlrev_b32 v88,16,v70
v_and_b32 v89,0xffff0000,v70
M 120,v[24:27],212
L 0
v_lshlrev_b32 v90,16,v71
v_and_b32 v91,0xffff0000,v71
v_mfma_f32_16x16x32_bf16 v[124:127],v[26:29],v[200:203],0
s_add_u32 m0,s65,0
v_lshlrev_b32 v92,16,v72
v_and_b32 v93,0xffff0000,v72
v_mfma_f32_16x16x32_bf16 v[128:131],v[24:27],v[200:203],0
D 9,56,0
M 124,v[26:29],216
D 9,56,1024
M 128,v[24:27],216
D 9,56,2048
v_mfma_f32_16x16x32_bf16 v[132:135],v[26:29],v[204:207],0
D 9,56,3072
v_mfma_f32_16x16x32_bf16 v[136:139],v[24:27],v[204:207],0
v_cvt_pk_bf16_f32 v148,v116,v117
v_cvt_pk_bf16_f32 v149,v118,v119
M 132,v[26:29],220
v_cvt_pk_bf16_f32 v150,v120,v121
v_cvt_pk_bf16_f32 v151,v122,v123
M 136,v[24:27],220
v_cvt_pk_bf16_f32 v152,v124,v125
v_cvt_pk_bf16_f32 v153,v126,v127
v_mfma_f32_16x16x32_bf16 v[140:143],v[26:29],v[208:211],0
v_cvt_pk_bf16_f32 v154,v128,v129
v_cvt_pk_bf16_f32 v155,v130,v131
v_mfma_f32_16x16x32_bf16 v[144:147],v[24:27],v[208:211],0
v_lshlrev_b32 v94,16,v73
v_and_b32 v95,0xffff0000,v73
M 140,v[26:29],224
v_cvt_pk_bf16_f32 v156,v132,v133
v_cvt_pk_bf16_f32 v157,v134,v135
M 144,v[24:27],224
v_cvt_pk_bf16_f32 v158,v136,v137
v_cvt_pk_bf16_f32 v159,v138,v139
v_mfma_f32_16x16x32_bf16 v[36:39],v[32:35],v[148:151],0
v_lshlrev_b32 v96,16,v74
v_and_b32 v97,0xffff0000,v74
v_mfma_f32_16x16x32_bf16 v[40:43],v[148:151],v[148:151],0
v_lshlrev_b32 v98,16,v75
v_and_b32 v99,0xffff0000,v75
M 36,v[32:35],152
v_cvt_pk_bf16_f32 v160,v140,v141
v_cvt_pk_bf16_f32 v161,v142,v143
M 40,v[152:155],152
v_cvt_pk_bf16_f32 v162,v144,v145
v_cvt_pk_bf16_f32 v163,v146,v147
M 36,v[32:35],156
v_lshlrev_b32 v100,16,v76
v_and_b32 v101,0xffff0000,v76
M 40,v[156:159],156
v_lshlrev_b32 v102,16,v77
v_and_b32 v103,0xffff0000,v77
M 36,v[32:35],160
v_lshlrev_b32 v104,16,v78
v_and_b32 v105,0xffff0000,v78
M 40,v[160:163],160
v_lshlrev_b32 v106,16,v79
v_and_b32 v107,0xffff0000,v79
v_lshlrev_b32 v108,16,v80
v_and_b32 v109,0xffff0000,v80
v_lshlrev_b32 v110,16,v81
v_and_b32 v111,0xffff0000,v81
v_lshlrev_b32 v112,16,v82
v_and_b32 v113,0xffff0000,v82
v_lshlrev_b32 v114,16,v83
v_and_b32 v115,0xffff0000,v83
v_add_f32_dpp v44,v36,v36 row_ror:8 row_mask:0xf bank_mask:0xf
v_cndmask_b32 v45,v40,v41,s[70:71]
v_cndmask_b32 v46,v42,v43,s[70:71]
ds_write_b32 v2,v44 offset:0
v_cndmask_b32 v45,v45,v46,s[72:73]
ds_write_b32 v0,v45 offset:0
L 0
s_barrier
R 52,3,0
R 56,3,16
R 60,3,32
L 0
v_pk_add_f32 v[52:53],v[52:53],v[54:55]
v_pk_add_f32 v[56:57],v[56:57],v[58:59]
v_pk_add_f32 v[60:61],v[60:61],v[62:63]
v_add_f32 v52,v52,v53
v_pk_add_f32 v[56:57],v[56:57],v[60:61]
v_mul_f32 v52,s68,v52
v_add_f32 v53,v56,v57
v_mul_f32 v53,s68,v53
v_fma_f32 v53,-v52,v52,v53
v_max_f32 v53,0,v53
s_waitcnt vmcnt(5)
buffer_store_dword v17,v5,s[36:39],s63 offen offset:0 sc0 sc1
s_mov_b32 s63,0x7fff0000
s_waitcnt vmcnt(5)
v_cmp_eq_u32 vcc,s53,v18
s_nop 0
s_cmp_eq_u64 vcc,-1
s_cselect_b32 s78,s45,256
s_cmp_lt_u32 s78,256
s_cbranch_scc1 Lr_tb1_%=
buffer_load_dword v18,v6,s[36:39],s61 offen offset:0 sc1
v_add_f32 v53,s69,v53
v_rsq_f32 v48,v53
s_nop 0
v_mul_f32_e64 v50,-v52,v48
v_mov_b32 v49,v48
v_mov_b32 v51,v50
v_pk_fma_f32 v[52:53],v[116:117],v[48:49],v[50:51]
v_pk_fma_f32 v[54:55],v[118:119],v[48:49],v[50:51]
v_pk_fma_f32 v[56:57],v[120:121],v[48:49],v[50:51]
v_pk_fma_f32 v[58:59],v[122:123],v[48:49],v[50:51]
v_pk_fma_f32 v[116:117],v[52:53],v[84:85],v[116:117]
v_pk_fma_f32 v[118:119],v[54:55],v[86:87],v[118:119]
v_pk_fma_f32 v[120:121],v[56:57],v[88:89],v[120:121]
v_pk_fma_f32 v[122:123],v[58:59],v[90:91],v[122:123]
v_cvt_pk_bf16_f32 v148,v116,v117
v_cvt_pk_bf16_f32 v149,v118,v119
v_cvt_pk_bf16_f32 v150,v120,v121
v_cvt_pk_bf16_f32 v151,v122,v123
v_pk_fma_f32 v[52:53],v[124:125],v[48:49],v[50:51]
v_pk_fma_f32 v[54:55],v[126:127],v[48:49],v[50:51]
v_pk_fma_f32 v[56:57],v[128:129],v[48:49],v[50:51]
v_pk_fma_f32 v[58:59],v[130:131],v[48:49],v[50:51]
v_pk_fma_f32 v[124:125],v[52:53],v[92:93],v[124:125]
v_pk_fma_f32 v[126:127],v[54:55],v[94:95],v[126:127]
v_pk_fma_f32 v[128:129],v[56:57],v[96:97],v[128:129]
v_pk_fma_f32 v[130:131],v[58:59],v[98:99],v[130:131]
v_cvt_pk_bf16_f32 v152,v124,v125
v_cvt_pk_bf16_f32 v153,v126,v127
v_cvt_pk_bf16_f32 v154,v128,v129
v_cvt_pk_bf16_f32 v155,v130,v131
v_pk_fma_f32 v[52:53],v[132:133],v[48:49],v[50:51]
v_pk_fma_f32 v[54:55],v[134:135],v[48:49],v[50:51]
v_pk_fma_f32 v[56:57],v[136:137],v[48:49],v[50:51]
v_pk_fma_f32 v[58:59],v[138:139],v[48:49],v[50:51]
v_pk_fma_f32 v[132:133],v[52:53],v[100:101],v[132:133]
v_pk_fma_f32 v[134:135],v[54:55],v[102:103],v[134:135]
v_pk_fma_f32 v[136:137],v[56:57],v[104:105],v[136:137]
v_pk_fma_f32 v[138:139],v[58:59],v[106:107],v[138:139]
v_cvt_pk_bf16_f32 v156,v132,v133
v_cvt_pk_bf16_f32 v157,v134,v135
v_cvt_pk_bf16_f32 v158,v136,v137
v_cvt_pk_bf16_f32 v159,v138,v139
v_pk_fma_f32 v[52:53],v[140:141],v[48:49],v[50:51]
v_pk_fma_f32 v[54:55],v[142:143],v[48:49],v[50:51]
v_pk_fma_f32 v[56:57],v[144:145],v[48:49],v[50:51]
v_pk_fma_f32 v[58:59],v[146:147],v[48:49],v[50:51]
v_pk_fma_f32 v[140:141],v[52:53],v[108:109],v[140:141]
v_pk_fma_f32 v[142:143],v[54:55],v[110:111],v[142:143]
v_pk_fma_f32 v[144:145],v[56:57],v[112:113],v[144:145]
v_pk_fma_f32 v[146:147],v[58:59],v[114:115],v[146:147]
v_cvt_pk_bf16_f32 v160,v140,v141
v_cvt_pk_bf16_f32 v161,v142,v143
v_cvt_pk_bf16_f32 v162,v144,v145
v_cvt_pk_bf16_f32 v163,v146,v147
v_mfma_f32_16x16x32_bf16 v[36:39],v[32:35],v[148:151],0
buffer_store_dwordx4 v[148:151],v1,s[36:39],s57 offen offset:0 sc0 sc1
v_mfma_f32_16x16x32_bf16 v[40:43],v[148:151],v[148:151],0
M 36,v[32:35],152
buffer_store_dwordx4 v[152:155],v1,s[36:39],s57 offen offset:2048 sc0 sc1
M 40,v[152:155],152
M 36,v[32:35],156
buffer_store_dwordx4 v[156:159],v1,s[36:39],s58 offen offset:0 sc0 sc1
M 40,v[156:159],156
M 36,v[32:35],160
buffer_store_dwordx4 v[160:163],v1,s[36:39],s58 offen offset:2048 sc0 sc1
M 40,v[160:163],160
s_nop 5
v_add_f32_dpp v44,v36,v36 row_ror:8 row_mask:0xf bank_mask:0xf
s_nop 0
v_cndmask_b32 v45,v40,v41,s[70:71]
v_cndmask_b32 v46,v42,v43,s[70:71]
ds_write_b32 v2,v44 offset:1024
v_cndmask_b32 v45,v45,v46,s[72:73]
ds_write_b32 v0,v45 offset:1024
L 0
s_barrier
R 52,3,1024
R 56,3,1040
R 60,3,1056
L 0
v_pk_add_f32 v[52:53],v[52:53],v[54:55]
v_pk_add_f32 v[56:57],v[56:57],v[58:59]
v_pk_add_f32 v[60:61],v[60:61],v[62:63]
v_add_f32 v52,v52,v53
v_pk_add_f32 v[56:57],v[56:57],v[60:61]
v_mul_f32 v52,s68,v52
v_add_f32 v53,v56,v57
v_mul_f32 v53,s68,v53
v_fma_f32 v53,-v52,v52,v53
v_max_f32 v53,0,v53
v_add_f32 v54,s69,v53
v_fma_f32 v55,s69,v54,v53
v_rsq_f32 v65,v54
v_rsq_f32 v54,v55
v_mul_f32_e64 v64,-v52,v65
v_mul_f32 v63,v55,v54
v_mul_f32 v61,s76,v54
v_mul_f32 v55,v54,v54
v_mul_f32 v62,s77,v55
v_mul_f32 v62,v62,v61
v_cndmask_b32 v60,v52,v64,s[74:75]
v_cndmask_b32 v61,v61,v65,s[74:75]
buffer_store_dwordx4 v[60:63],v4,s[36:39],s59 offen offset:0 sc0 sc1
s_mov_b32 s63,s60
s_waitcnt vmcnt(5)
v_cmp_eq_u32 vcc,s53,v18
s_nop 0
s_cmp_eq_u64 vcc,-1
s_cselect_b32 s78,s45,256
s_cmp_lt_u32 s78,256
s_cbranch_scc1 Lr_tc1_%=
s_waitcnt vmcnt(0)
buffer_store_dword v17,v5,s[36:39],s63 offen offset:0 sc0 sc1
s_mov_b32 s63,0x7fff0000
s_cmp_ge_u32 s45,256
s_cbranch_scc1 Lr_done_%=
Lr_bp1_%=:
buffer_load_dword v18,v6,s[36:39],s61 offen offset:0 sc1
s_add_u32 s82,s82,1
s_waitcnt vmcnt(0)
v_cmp_eq_u32 vcc,s53,v18
s_nop 0
s_cmp_eq_u64 vcc,-1
s_cbranch_scc1 Lr_bq1_%=
s_sleep 2
s_cmp_lt_u32 s82,20000
s_cbranch_scc1 Lr_bp1_%=
Lr_bq1_%=:
s_add_u32 s78,s55,71303168
buffer_load_dwordx4 v[164:167],v1,s[36:39],s78 offen offset:0 sc1
buffer_load_dwordx4 v[168:171],v1,s[36:39],s78 offen offset:2048 sc1
s_add_u32 s78,s55,71307264
buffer_load_dwordx4 v[172:175],v1,s[36:39],s78 offen offset:0 sc1
buffer_load_dwordx4 v[176:179],v1,s[36:39],s78 offen offset:2048 sc1
s_add_u32 s78,s55,89128960
buffer_load_dwordx4 v[180:183],v1,s[36:39],s78 offen offset:0 sc1
buffer_load_dwordx4 v[184:187],v1,s[36:39],s78 offen offset:2048 sc1
s_add_u32 s78,s55,89133056
buffer_load_dwordx4 v[188:191],v1,s[36:39],s78 offen offset:0 sc1
buffer_load_dwordx4 v[192:195],v1,s[36:39],s78 offen offset:2048 sc1
buffer_load_dword v18,v6,s[36:39],s62 offen offset:0 sc1
s_lshr_b32 s78,s45,1
s_and_b32 s79,s45,1
s_lshl_b32 s79,s79,7
s_lshl_b32 s57,s78,15
s_add_u32 s57,s57,s79
s_add_u32 s57,s57,12582912
s_add_u32 s58,s57,4096
s_lshl_b32 s59,s78,9
s_add_u32 s59,s59,s79
s_add_u32 s59,s59,107020288
s_lshl_b32 s60,s45,9
s_add_u32 s60,s60,s66
s_add_u32 s60,s60,131072
s_add_u32 s45,s45,s46
s_lshr_b32 s78,s45,1
s_and_b32 s79,s45,1
s_lshl_b32 s79,s79,7
s_lshl_b32 s55,s78,15
s_add_u32 s55,s55,s79
s_mul_i32 s56,s45,16384
s_add_u32 s56,s56,98566144
s_lshl_b32 s61,s78,11
s_add_u32 s61,s61,110587904
s_add_u32 s78,s45,s46
s_lshr_b32 s78,s78,1
s_lshl_b32 s62,s78,11
s_add_u32 s62,s62,110587904
s_waitcnt vmcnt(0)
s_branch Lr_it0_%=
Lr_ta1_%=:
v_mfma_f32_16x16x32_bf16 v[116:119],v[26:29],v[196:199],0
s_add_u32 s78,s55,71303168
L 6
L 4
v_mfma_f32_16x16x32_bf16 v[120:123],v[24:27],v[196:199],0
)"
R"(L 2
buffer_load_dwordx4 v[164:167],v1,s[36:39],s78 offen offset:0 sc1
M 116,v[26:29],212
L 0
buffer_load_dwordx4 v[168:171],v1,s[36:39],s78 offen offset:2048 sc1
M 120,v[24:27],212
s_add_u32 s78,s55,71307264
v_lshlrev_b32 v84,16,v68
v_and_b32 v85,0xffff0000,v68
v_mfma_f32_16x16x32_bf16 v[124:127],v[26:29],v[200:203],0
buffer_load_dwordx4 v[172:175],v1,s[36:39],s78 offen offset:0 sc1
v_mfma_f32_16x16x32_bf16 v[128:131],v[24:27],v[200:203],0
buffer_load_dwordx4 v[176:179],v1,s[36:39],s78 offen offset:2048 sc1
M 124,v[26:29],216
s_add_u32 s78,s55,89128960
v_cvt_pk_bf16_f32 v148,v116,v117
v_cvt_pk_bf16_f32 v149,v118,v119
M 128,v[24:27],216
buffer_load_dwordx4 v[180:183],v1,s[36:39],s78 offen offset:0 sc1
v_mfma_f32_16x16x32_bf16 v[132:135],v[26:29],v[204:207],0
buffer_load_dwordx4 v[184:187],v1,s[36:39],s78 offen offset:2048 sc1
v_mfma_f32_16x16x32_bf16 v[136:139],v[24:27],v[204:207],0
s_add_u32 s78,s55,89133056
v_cvt_pk_bf16_f32 v150,v120,v121
v_cvt_pk_bf16_f32 v151,v122,v123
M 132,v[26:29],220
buffer_load_dwordx4 v[188:191],v1,s[36:39],s78 offen offset:0 sc1
M 136,v[24:27],220
buffer_load_dwordx4 v[192:195],v1,s[36:39],s78 offen offset:2048 sc1
v_mfma_f32_16x16x32_bf16 v[140:143],v[26:29],v[208:211],0
s_add_u32 m0,s65,0
v_cvt_pk_bf16_f32 v152,v124,v125
v_cvt_pk_bf16_f32 v153,v126,v127
v_mfma_f32_16x16x32_bf16 v[144:147],v[24:27],v[208:211],0
D 9,56,0
M 140,v[26:29],224
D 9,56,1024
M 144,v[24:27],224
D 9,56,2048
v_mfma_f32_16x16x32_bf16 v[36:39],v[32:35],v[148:151],0
v_cvt_pk_bf16_f32 v154,v128,v129
v_cvt_pk_bf16_f32 v155,v130,v131
v_mfma_f32_16x16x32_bf16 v[40:43],v[148:151],v[148:151],0
D 9,56,3072
M 36,v[32:35],152
v_cvt_pk_bf16_f32 v156,v132,v133
v_cvt_pk_bf16_f32 v157,v134,v135
v_cvt_pk_bf16_f32 v158,v136,v137
v_cvt_pk_bf16_f32 v159,v138,v139
M 40,v[152:155],152
v_cvt_pk_bf16_f32 v160,v140,v141
v_cvt_pk_bf16_f32 v161,v142,v143
M 36,v[32:35],156
v_cvt_pk_bf16_f32 v162,v144,v145
v_cvt_pk_bf16_f32 v163,v146,v147
M 40,v[156:159],156
v_lshlrev_b32 v86,16,v69
v_and_b32 v87,0xffff0000,v69
M 36,v[32:35],160
v_lshlrev_b32 v88,16,v70
v_and_b32 v89,0xffff0000,v70
M 40,v[160:163],160
v_lshlrev_b32 v90,16,v71
v_and_b32 v91,0xffff0000,v71
v_lshlrev_b32 v92,16,v72
v_and_b32 v93,0xffff0000,v72
v_lshlrev_b32 v94,16,v73
v_and_b32 v95,0xffff0000,v73
v_lshlrev_b32 v96,16,v74
v_and_b32 v97,0xffff0000,v74
v_lshlrev_b32 v98,16,v75
v_and_b32 v99,0xffff0000,v75
v_lshlrev_b32 v100,16,v76
v_and_b32 v101,0xffff0000,v76
v_lshlrev_b32 v102,16,v77
v_and_b32 v103,0xffff0000,v77
v_lshlrev_b32 v104,16,v78
v_and_b32 v105,0xffff0000,v78
v_lshlrev_b32 v106,16,v79
v_and_b32 v107,0xffff0000,v79
v_lshlrev_b32 v108,16,v80
v_and_b32 v109,0xffff0000,v80
v_lshlrev_b32 v110,16,v81
v_and_b32 v111,0xffff0000,v81
v_lshlrev_b32 v112,16,v82
v_and_b32 v113,0xffff0000,v82
v_lshlrev_b32 v114,16,v83
v_and_b32 v115,0xffff0000,v83
v_add_f32_dpp v44,v36,v36 row_ror:8 row_mask:0xf bank_mask:0xf
v_cndmask_b32 v45,v40,v41,s[70:71]
v_cndmask_b32 v46,v42,v43,s[70:71]
ds_write_b32 v2,v44 offset:0
v_cndmask_b32 v45,v45,v46,s[72:73]
ds_write_b32 v0,v45 offset:0
L 0
s_barrier
R 52,3,0
R 56,3,16
R 60,3,32
L 0
v_pk_add_f32 v[52:53],v[52:53],v[54:55]
v_pk_add_f32 v[56:57],v[56:57],v[58:59]
v_pk_add_f32 v[60:61],v[60:61],v[62:63]
v_add_f32 v52,v52,v53
v_pk_add_f32 v[56:57],v[56:57],v[60:61]
v_mul_f32 v52,s68,v52
v_add_f32 v53,v56,v57
v_mul_f32 v53,s68,v53
v_fma_f32 v53,-v52,v52,v53
v_max_f32 v53,0,v53
buffer_load_dword v18,v6,s[36:39],s62 offen offset:0 sc1
v_add_f32 v53,s69,v53
v_rsq_f32 v48,v53
s_nop 0
v_mul_f32_e64 v50,-v52,v48
v_mov_b32 v49,v48
v_mov_b32 v51,v50
v_pk_fma_f32 v[52:53],v[116:117],v[48:49],v[50:51]
v_pk_fma_f32 v[54:55],v[118:119],v[48:49],v[50:51]
v_pk_fma_f32 v[56:57],v[120:121],v[48:49],v[50:51]
v_pk_fma_f32 v[58:59],v[122:123],v[48:49],v[50:51]
v_pk_fma_f32 v[116:117],v[52:53],v[84:85],v[116:117]
v_pk_fma_f32 v[118:119],v[54:55],v[86:87],v[118:119]
v_pk_fma_f32 v[120:121],v[56:57],v[88:89],v[120:121]
v_pk_fma_f32 v[122:123],v[58:59],v[90:91],v[122:123]
v_cvt_pk_bf16_f32 v148,v116,v117
v_cvt_pk_bf16_f32 v149,v118,v119
v_cvt_pk_bf16_f32 v150,v120,v121
v_cvt_pk_bf16_f32 v151,v122,v123
v_pk_fma_f32 v[52:53],v[124:125],v[48:49],v[50:51]
v_pk_fma_f32 v[54:55],v[126:127],v[48:49],v[50:51]
v_pk_fma_f32 v[56:57],v[128:129],v[48:49],v[50:51]
v_pk_fma_f32 v[58:59],v[130:131],v[48:49],v[50:51]
v_pk_fma_f32 v[124:125],v[52:53],v[92:93],v[124:125]
v_pk_fma_f32 v[126:127],v[54:55],v[94:95],v[126:127]
v_pk_fma_f32 v[128:129],v[56:57],v[96:97],v[128:129]
v_pk_fma_f32 v[130:131],v[58:59],v[98:99],v[130:131]
v_cvt_pk_bf16_f32 v152,v124,v125
v_cvt_pk_bf16_f32 v153,v126,v127
v_cvt_pk_bf16_f32 v154,v128,v129
v_cvt_pk_bf16_f32 v155,v130,v131
v_pk_fma_f32 v[52:53],v[132:133],v[48:49],v[50:51]
v_pk_fma_f32 v[54:55],v[134:135],v[48:49],v[50:51]
v_pk_fma_f32 v[56:57],v[136:137],v[48:49],v[50:51]
v_pk_fma_f32 v[58:59],v[138:139],v[48:49],v[50:51]
v_pk_fma_f32 v[132:133],v[52:53],v[100:101],v[132:133]
v_pk_fma_f32 v[134:135],v[54:55],v[102:103],v[134:135]
v_pk_fma_f32 v[136:137],v[56:57],v[104:105],v[136:137]
v_pk_fma_f32 v[138:139],v[58:59],v[106:107],v[138:139]
v_cvt_pk_bf16_f32 v156,v132,v133
v_cvt_pk_bf16_f32 v157,v134,v135
v_cvt_pk_bf16_f32 v158,v136,v137
v_cvt_pk_bf16_f32 v159,v138,v139
v_pk_fma_f32 v[52:53],v[140:141],v[48:49],v[50:51]
v_pk_fma_f32 v[54:55],v[142:143],v[48:49],v[50:51]
v_pk_fma_f32 v[56:57],v[144:145],v[48:49],v[50:51]
v_pk_fma_f32 v[58:59],v[146:147],v[48:49],v[50:51]
v_pk_fma_f32 v[140:141],v[52:53],v[108:109],v[140:141]
v_pk_fma_f32 v[142:143],v[54:55],v[110:111],v[142:143]
v_pk_fma_f32 v[144:145],v[56:57],v[112:113],v[144:145]
v_pk_fma_f32 v[146:147],v[58:59],v[114:115],v[146:147]
v_cvt_pk_bf16_f32 v160,v140,v141
v_cvt_pk_bf16_f32 v161,v142,v143
v_cvt_pk_bf16_f32 v162,v144,v145
v_cvt_pk_bf16_f32 v163,v146,v147
s_waitcnt vmcnt(13)
buffer_store_dword v17,v5,s[36:39],s63 offen offset:0 sc0 sc1
s_mov_b32 s63,0x7fff0000
v_mfma_f32_16x16x32_bf16 v[36:39],v[32:35],v[148:151],0
buffer_store_dwordx4 v[148:151],v1,s[36:39],s57 offen offset:0 sc0 sc1
v_mfma_f32_16x16x32_bf16 v[40:43],v[148:151],v[148:151],0
M 36,v[32:35],152
buffer_store_dwordx4 v[152:155],v1,s[36:39],s57 offen offset:2048 sc0 sc1
M 40,v[152:155],152
M 36,v[32:35],156
buffer_store_dwordx4 v[156:159],v1,s[36:39],s58 offen offset:0 sc0 sc1
M 40,v[156:159],156
M 36,v[32:35],160
buffer_store_dwordx4 v[160:163],v1,s[36:39],s58 offen offset:2048 sc0 sc1
M 40,v[160:163],160
s_nop 5
v_add_f32_dpp v44,v36,v36 row_ror:8 row_mask:0xf bank_mask:0xf
s_nop 0
v_cndmask_b32 v45,v40,v41,s[70:71]
v_cndmask_b32 v46,v42,v43,s[70:71]
ds_write_b32 v2,v44 offset:1024
v_cndmask_b32 v45,v45,v46,s[72:73]
ds_write_b32 v0,v45 offset:1024
L 0
s_barrier
R 52,3,1024
R 56,3,1040
R 60,3,1056
L 0
v_pk_add_f32 v[52:53],v[52:53],v[54:55]
v_pk_add_f32 v[56:57],v[56:57],v[58:59]
v_pk_add_f32 v[60:61],v[60:61],v[62:63]
v_add_f32 v52,v52,v53
v_pk_add_f32 v[56:57],v[56:57],v[60:61]
v_mul_f32 v52,s68,v52
v_add_f32 v53,v56,v57
v_mul_f32 v53,s68,v53
v_fma_f32 v53,-v52,v52,v53
v_max_f32 v53,0,v53
v_add_f32 v54,s69,v53
v_fma_f32 v55,s69,v54,v53
v_rsq_f32 v65,v54
v_rsq_f32 v54,v55
v_mul_f32_e64 v64,-v52,v65
v_mul_f32 v63,v55,v54
v_mul_f32 v61,s76,v54
v_mul_f32 v55,v54,v54
v_mul_f32 v62,s77,v55
v_mul_f32 v62,v62,v61
v_cndmask_b32 v60,v52,v64,s[74:75]
v_cndmask_b32 v61,v61,v65,s[74:75]
buffer_store_dwordx4 v[60:63],v4,s[36:39],s59 offen offset:0 sc0 sc1
s_mov_b32 s63,s60
s_lshr_b32 s78,s45,1
s_and_b32 s79,s45,1
s_lshl_b32 s79,s79,7
s_lshl_b32 s57,s78,15
s_add_u32 s57,s57,s79
s_add_u32 s57,s57,12582912
s_add_u32 s58,s57,4096
s_lshl_b32 s59,s78,9
s_add_u32 s59,s59,s79
s_add_u32 s59,s59,107020288
s_lshl_b32 s60,s45,9
s_add_u32 s60,s60,s66
s_add_u32 s60,s60,131072
s_add_u32 s45,s45,s46
s_lshr_b32 s78,s45,1
s_and_b32 s79,s45,1
s_lshl_b32 s79,s79,7
s_lshl_b32 s55,s78,15
s_add_u32 s55,s55,s79
s_mul_i32 s56,s45,16384
s_add_u32 s56,s56,98566144
s_lshl_b32 s61,s78,11
s_add_u32 s61,s61,110587904
s_add_u32 s78,s45,s46
s_lshr_b32 s78,s78,1
s_lshl_b32 s62,s78,11
s_add_u32 s62,s62,110587904
s_waitcnt vmcnt(6)
s_branch Lr_it0_%=
Lr_tb1_%=:
v_add_f32 v53,s69,v53
v_rsq_f32 v48,v53
s_nop 0
v_mul_f32_e64 v50,-v52,v48
v_mov_b32 v49,v48
v_mov_b32 v51,v50
v_pk_fma_f32 v[52:53],v[116:117],v[48:49],v[50:51]
v_pk_fma_f32 v[54:55],v[118:119],v[48:49],v[50:51]
v_pk_fma_f32 v[56:57],v[120:121],v[48:49],v[50:51]
v_pk_fma_f32 v[58:59],v[122:123],v[48:49],v[50:51]
s_add_u32 s78,s55,71303168
buffer_load_dwordx4 v[164:167],v1,s[36:39],s78 offen offset:0 sc1
buffer_load_dwordx4 v[168:171],v1,s[36:39],s78 offen offset:2048 sc1
s_add_u32 s78,s55,71307264
buffer_load_dwordx4 v[172:175],v1,s[36:39],s78 offen offset:0 sc1
buffer_load_dwordx4 v[176:179],v1,s[36:39],s78 offen offset:2048 sc1
v_pk_fma_f32 v[116:117],v[52:53],v[84:85],v[116:117]
v_pk_fma_f32 v[118:119],v[54:55],v[86:87],v[118:119]
v_pk_fma_f32 v[120:121],v[56:57],v[88:89],v[120:121]
v_pk_fma_f32 v[122:123],v[58:59],v[90:91],v[122:123]
v_cvt_pk_bf16_f32 v148,v116,v117
v_cvt_pk_bf16_f32 v149,v118,v119
v_cvt_pk_bf16_f32 v150,v120,v121
v_cvt_pk_bf16_f32 v151,v122,v123
v_pk_fma_f32 v[52:53],v[124:125],v[48:49],v[50:51]
v_pk_fma_f32 v[54:55],v[126:127],v[48:49],v[50:51]
v_pk_fma_f32 v[56:57],v[128:129],v[48:49],v[50:51]
v_pk_fma_f32 v[58:59],v[130:131],v[48:49],v[50:51]
s_add_u32 s78,s55,89128960
buffer_load_dwordx4 v[180:183],v1,s[36:39],s78 offen offset:0 sc1
buffer_load_dwordx4 v[184:187],v1,s[36:39],s78 offen offset:2048 sc1
s_add_u32 s78,s55,89133056
buffer_load_dwordx4 v[188:191],v1,s[36:39],s78 offen offset:0 sc1
buffer_load_dwordx4 v[192:195],v1,s[36:39],s78 offen offset:2048 sc1
v_pk_fma_f32 v[124:125],v[52:53],v[92:93],v[124:125]
v_pk_fma_f32 v[126:127],v[54:55],v[94:95],v[126:127]
v_pk_fma_f32 v[128:129],v[56:57],v[96:97],v[128:129]
v_pk_fma_f32 v[130:131],v[58:59],v[98:99],v[130:131]
v_cvt_pk_bf16_f32 v152,v124,v125
v_cvt_pk_bf16_f32 v153,v126,v127
v_cvt_pk_bf16_f32 v154,v128,v129
v_cvt_pk_bf16_f32 v155,v130,v131
v_pk_fma_f32 v[52:53],v[132:133],v[48:49],v[50:51]
v_pk_fma_f32 v[54:55],v[134:135],v[48:49],v[50:51]
v_pk_fma_f32 v[56:57],v[136:137],v[48:49],v[50:51]
v_pk_fma_f32 v[58:59],v[138:139],v[48:49],v[50:51]
v_pk_fma_f32 v[132:133],v[52:53],v[100:101],v[132:133]
v_pk_fma_f32 v[134:135],v[54:55],v[102:103],v[134:135]
v_pk_fma_f32 v[136:137],v[56:57],v[104:105],v[136:137]
v_pk_fma_f32 v[138:139],v[58:59],v[106:107],v[138:139]
v_cvt_pk_bf16_f32 v156,v132,v133
v_cvt_pk_bf16_f32 v157,v134,v135
v_cvt_pk_bf16_f32 v158,v136,v137
v_cvt_pk_bf16_f32 v159,v138,v139
v_pk_fma_f32 v[52:53],v[140:141],v[48:49],v[50:51]
v_pk_fma_f32 v[54:55],v[142:143],v[48:49],v[50:51]
v_pk_fma_f32 v[56:57],v[144:145],v[48:49],v[50:51]
v_pk_fma_f32 v[58:59],v[146:147],v[48:49],v[50:51]
v_pk_fma_f32 v[140:141],v[52:53],v[108:109],v[140:141]
v_pk_fma_f32 v[142:143],v[54:55],v[110:111],v[142:143]
v_pk_fma_f32 v[144:145],v[56:57],v[112:113],v[144:145]
v_pk_fma_f32 v[146:147],v[58:59],v[114:115],v[146:147]
v_cvt_pk_bf16_f32 v160,v140,v141
v_cvt_pk_bf16_f32 v161,v142,v143
v_cvt_pk_bf16_f32 v162,v144,v145
v_cvt_pk_bf16_f32 v163,v146,v147
v_mfma_f32_16x16x32_bf16 v[36:39],v[32:35],v[148:151],0
buffer_load_dword v18,v6,s[36:39],s62 offen offset:0 sc1
buffer_store_dwordx4 v[148:151],v1,s[36:39],s57 offen offset:0 sc0 sc1
v_mfma_f32_16x16x32_bf16 v[40:43],v[148:151],v[148:151],0
M 36,v[32:35],152
buffer_store_dwordx4 v[152:155],v1,s[36:39],s57 offen offset:2048 sc0 sc1
M 40,v[152:155],152
M 36,v[32:35],156
buffer_store_dwordx4 v[156:159],v1,s[36:39],s58 offen offset:0 sc0 sc1
M 40,v[156:159],156
M 36,v[32:35],160
buffer_store_dwordx4 v[160:163],v1,s[36:39],s58 offen offset:2048 sc0 sc1
M 40,v[160:163],160
s_nop 5
v_add_f32_dpp v44,v36,v36 row_ror:8 row_mask:0xf bank_mask:0xf
s_nop 0
v_cndmask_b32 v45,v40,v41,s[70:71]
v_cndmask_b32 v46,v42,v43,s[70:71]
ds_write_b32 v2,v44 offset:1024
v_cndmask_b32 v45,v45,v46,s[72:73]
ds_write_b32 v0,v45 offset:1024
L 0
s_barrier
R 52,3,1024
R 56,3,1040
R 60,3,1056
L 0
v_pk_add_f32 v[52:53],v[52:53],v[54:55]
v_pk_add_f32 v[56:57],v[56:57],v[58:59]
v_pk_add_f32 v[60:61],v[60:61],v[62:63]
v_add_f32 v52,v52,v53
v_pk_add_f32 v[56:57],v[56:57],v[60:61]
v_mul_f32 v52,s68,v52
v_add_f32 v53,v56,v57
v_mul_f32 v53,s68,v53
v_fma_f32 v53,-v52,v52,v53
v_max_f32 v53,0,v53
v_add_f32 v54,s69,v53
v_fma_f32 v55,s69,v54,v53
v_rsq_f32 v65,v54
v_rsq_f32 v54,v55
v_mul_f32_e64 v64,-v52,v65
v_mul_f32 v63,v55,v54
v_mul_f32 v61,s76,v54
v_mul_f32 v55,v54,v54
v_mul_f32 v62,s77,v55
v_mul_f32 v62,v62,v61
v_cndmask_b32 v60,v52,v64,s[74:75]
v_cndmask_b32 v61,v61,v65,s[74:75]
buffer_store_dwordx4 v[60:63],v4,s[36:39],s59 offen offset:0 sc0 sc1
s_mov_b32 s63,s60
s_lshr_b32 s78,s45,1
s_and_b32 s79,s45,1
s_lshl_b32 s79,s79,7
s_lshl_b32 s57,s78,15
s_add_u32 s57,s57,s79
s_add_u32 s57,s57,12582912
s_add_u32 s58,s57,4096
s_lshl_b32 s59,s78,9
s_add_u32 s59,s59,s79
s_add_u32 s59,s59,107020288
s_lshl_b32 s60,s45,9
s_add_u32 s60,s60,s66
s_add_u32 s60,s60,131072
s_add_u32 s45,s45,s46
s_lshr_b32 s78,s45,1
s_and_b32 s79,s45,1
s_lshl_b32 s79,s79,7
s_lshl_b32 s55,s78,15
s_add_u32 s55,s55,s79
s_mul_i32 s56,s45,16384
s_add_u32 s56,s56,98566144
s_lshl_b32 s61,s78,11
s_add_u32 s61,s61,110587904
s_add_u32 s78,s45,s46
s_lshr_b32 s78,s78,1
s_lshl_b32 s62,s78,11
s_add_u32 s62,s62,110587904
s_waitcnt vmcnt(5)
s_branch Lr_it0_%=
Lr_tc1_%=:
s_add_u32 s78,s55,71303168
buffer_load_dwordx4 v[164:167],v1,s[36:39],s78 offen offset:0 sc1
buffer_load_dwordx4 v[168:171],v1,s[36:39],s78 offen offset:2048 sc1
s_add_u32 s78,s55,71307264
buffer_load_dwordx4 v[172:175],v1,s[36:39],s78 offen offset:0 sc1
buffer_load_dwordx4 v[176:179],v1,s[36:39],s78 offen offset:2048 sc1
s_add_u32 s78,s55,89128960
buffer_load_dwordx4 v[180:183],v1,s[36:39],s78 offen offset:0 sc1
buffer_load_dwordx4 v[184:187],v1,s[36:39],s78 offen offset:2048 sc1
s_add_u32 s78,s55,89133056
buffer_load_dwordx4 v[188:191],v1,s[36:39],s78 offen offset:0 sc1
buffer_load_dwordx4 v[192:195],v1,s[36:39],s78 offen offset:2048 sc1
buffer_load_dword v18,v6,s[36:39],s62 offen offset:0 sc1
s_waitcnt vmcnt(9)
buffer_store_dword v17,v5,s[36:39],s63 offen offset:0 sc0 sc1
s_mov_b32 s63,0x7fff0000
s_lshr_b32 s78,s45,1
s_and_b32 s79,s45,1
s_lshl_b32 s79,s79,7
s_lshl_b32 s57,s78,15
s_add_u32 s57,s57,s79
s_add_u32 s57,s57,12582912
s_add_u32 s58,s57,4096
s_lshl_b32 s59,s78,9
s_add_u32 s59,s59,s79
s_add_u32 s59,s59,107020288
s_lshl_b32 s60,s45,9
s_add_u32 s60,s60,s66
s_add_u32 s60,s60,131072
s_add_u32 s45,s45,s46
s_lshr_b32 s78,s45,1
s_and_b32 s79,s45,1
s_lshl_b32 s79,s79,7
s_lshl_b32 s55,s78,15
s_add_u32 s55,s55,s79
s_mul_i32 s56,s45,16384
s_add_u32 s56,s56,98566144
s_lshl_b32 s61,s78,11
s_add_u32 s61,s61,110587904
s_add_u32 s78,s45,s46
s_lshr_b32 s78,s78,1
s_lshl_b32 s62,s78,11
s_add_u32 s62,s62,110587904
s_waitcnt vmcnt(1)
s_branch Lr_it0_%=
Lr_done_%=:
s_waitcnt vmcnt(0) lgkmcnt(0)
s_barrier
s_branch Lr_end_%=
.p2align 8
Lr_tab_%=:
s_mov_b64 s[40:41],%24
s_mov_b32 s42,7340032
s_mov_b32 s43,0x00020000
s_add_u32 s83,s50,0
s_add_u32 m0,s54,40960
s_nop 0
buffer_load_dwordx4 v13,s[40:43],s83 offen offset:0  lds
s_add_u32 m0,s54,41984
s_nop 0
buffer_load_dwordx4 v14,s[40:43],s83 offen offset:0  lds
s_add_u32 m0,s54,49024
s_nop 0
buffer_load_dwordx4 v13,s[40:43],s83 offen offset:128  lds
s_add_u32 m0,s54,50048
s_nop 0
buffer_load_dwordx4 v14,s[40:43],s83 offen offset:128  lds
s_setpc_b64 s[88:89]
.p2align 8
s_mov_b64 s[40:41],%24
s_mov_b32 s42,7340032
s_mov_b32 s43,0x00020000
v_add_u32 v230,40960,v15
v_add_u32 v231,40960,v16
T a[0:1],230,0
T a[2:3],230,512
T a[4:5],231,0
T a[6:7],231,512
T a[8:9],230,4096
T a[10:11],230,4608
T a[12:13],231,4096
T a[14:15],231,4608
s_add_u32 s83,s50,131072
s_add_u32 m0,s54,57344
s_nop 0
buffer_load_dwordx4 v13,s[40:43],s83 offen offset:0  lds
s_add_u32 m0,s54,58368
s_nop 0
buffer_load_dwordx4 v14,s[40:43],s83 offen offset:0  lds
s_add_u32 m0,s54,65408
s_nop 0
buffer_load_dwordx4 v13,s[40:43],s83 offen offset:128  lds
s_add_u32 m0,s54,66432
s_nop 0
buffer_load_dwordx4 v14,s[40:43],s83 offen offset:128  lds
s_setpc_b64 s[88:89]
.p2align 8
s_mov_b64 s[40:41],%24
s_mov_b32 s42,7340032
s_mov_b32 s43,0x00020000
v_add_u32 v230,57344,v15
v_add_u32 v231,57344,v16
T a[16:17],230,0
T a[18:19],230,512
T a[20:21],231,0
)"
R"(T a[22:23],231,512
T a[24:25],230,4096
T a[26:27],230,4608
T a[28:29],231,4096
T a[30:31],231,4608
s_add_u32 s83,s50,262144
s_add_u32 m0,s54,73728
s_nop 0
buffer_load_dwordx4 v13,s[40:43],s83 offen offset:0  lds
s_add_u32 m0,s54,74752
s_nop 0
buffer_load_dwordx4 v14,s[40:43],s83 offen offset:0  lds
s_add_u32 m0,s54,81792
s_nop 0
buffer_load_dwordx4 v13,s[40:43],s83 offen offset:128  lds
s_add_u32 m0,s54,82816
s_nop 0
buffer_load_dwordx4 v14,s[40:43],s83 offen offset:128  lds
s_setpc_b64 s[88:89]
.p2align 8
s_mov_b64 s[40:41],%24
s_mov_b32 s42,7340032
s_mov_b32 s43,0x00020000
v_add_u32 v230,73728,v15
v_add_u32 v231,73728,v16
T a[32:33],230,0
T a[34:35],230,512
T a[36:37],231,0
T a[38:39],231,512
T a[40:41],230,4096
T a[42:43],230,4608
T a[44:45],231,4096
T a[46:47],231,4608
s_add_u32 s83,s50,393216
s_add_u32 m0,s54,90112
s_nop 0
buffer_load_dwordx4 v13,s[40:43],s83 offen offset:0  lds
s_add_u32 m0,s54,91136
s_nop 0
buffer_load_dwordx4 v14,s[40:43],s83 offen offset:0  lds
s_add_u32 m0,s54,98176
s_nop 0
buffer_load_dwordx4 v13,s[40:43],s83 offen offset:128  lds
s_add_u32 m0,s54,99200
s_nop 0
buffer_load_dwordx4 v14,s[40:43],s83 offen offset:128  lds
s_setpc_b64 s[88:89]
.p2align 8
s_mov_b64 s[40:41],%24
s_mov_b32 s42,7340032
s_mov_b32 s43,0x00020000
v_add_u32 v230,90112,v15
v_add_u32 v231,90112,v16
T a[48:49],230,0
T a[50:51],230,512
T a[52:53],231,0
T a[54:55],231,512
T a[56:57],230,4096
T a[58:59],230,4608
T a[60:61],231,4096
T a[62:63],231,4608
s_add_u32 s83,s50,524288
s_add_u32 m0,s54,106496
s_nop 0
buffer_load_dwordx4 v13,s[40:43],s83 offen offset:0  lds
s_add_u32 m0,s54,107520
s_nop 0
buffer_load_dwordx4 v14,s[40:43],s83 offen offset:0  lds
s_add_u32 m0,s54,114560
s_nop 0
buffer_load_dwordx4 v13,s[40:43],s83 offen offset:128  lds
s_add_u32 m0,s54,115584
s_nop 0
buffer_load_dwordx4 v14,s[40:43],s83 offen offset:128  lds
s_setpc_b64 s[88:89]
.p2align 8
s_mov_b64 s[40:41],%24
s_mov_b32 s42,7340032
s_mov_b32 s43,0x00020000
v_add_u32 v230,106496,v15
v_add_u32 v231,106496,v16
T a[64:65],230,0
T a[66:67],230,512
T a[68:69],231,0
T a[70:71],231,512
T a[72:73],230,4096
T a[74:75],230,4608
T a[76:77],231,4096
T a[78:79],231,4608
s_add_u32 s83,s50,655360
s_add_u32 m0,s54,122880
s_nop 0
buffer_load_dwordx4 v13,s[40:43],s83 offen offset:0  lds
s_add_u32 m0,s54,123904
s_nop 0
buffer_load_dwordx4 v14,s[40:43],s83 offen offset:0  lds
s_add_u32 m0,s54,130944
s_nop 0
buffer_load_dwordx4 v13,s[40:43],s83 offen offset:128  lds
s_add_u32 m0,s54,131968
s_nop 0
buffer_load_dwordx4 v14,s[40:43],s83 offen offset:128  lds
s_setpc_b64 s[88:89]
.p2align 8
s_mov_b64 s[40:41],%24
s_mov_b32 s42,7340032
s_mov_b32 s43,0x00020000
v_add_u32 v230,122880,v15
v_add_u32 v231,122880,v16
T a[80:81],230,0
T a[82:83],230,512
T a[84:85],231,0
T a[86:87],231,512
T a[88:89],230,4096
T a[90:91],230,4608
T a[92:93],231,4096
T a[94:95],231,4608
s_add_u32 s83,s50,786432
s_add_u32 m0,s54,139264
s_nop 0
buffer_load_dwordx4 v13,s[40:43],s83 offen offset:0  lds
s_add_u32 m0,s54,140288
s_nop 0
buffer_load_dwordx4 v14,s[40:43],s83 offen offset:0  lds
s_add_u32 m0,s54,147328
s_nop 0
buffer_load_dwordx4 v13,s[40:43],s83 offen offset:128  lds
s_add_u32 m0,s54,148352
s_nop 0
buffer_load_dwordx4 v14,s[40:43],s83 offen offset:128  lds
s_setpc_b64 s[88:89]
.p2align 8
s_mov_b64 s[40:41],%24
s_mov_b32 s42,7340032
s_mov_b32 s43,0x00020000
v_add_u32 v230,139264,v15
v_add_u32 v231,139264,v16
T a[96:97],230,0
T a[98:99],230,512
T a[100:101],231,0
T a[102:103],231,512
T a[104:105],230,4096
T a[106:107],230,4608
T a[108:109],231,4096
T a[110:111],231,4608
s_add_u32 s83,s50,917504
s_add_u32 m0,s54,40960
s_nop 0
buffer_load_dwordx4 v13,s[40:43],s83 offen offset:0  lds
s_add_u32 m0,s54,41984
s_nop 0
buffer_load_dwordx4 v14,s[40:43],s83 offen offset:0  lds
s_add_u32 m0,s54,49024
s_nop 0
buffer_load_dwordx4 v13,s[40:43],s83 offen offset:128  lds
s_add_u32 m0,s54,50048
s_nop 0
buffer_load_dwordx4 v14,s[40:43],s83 offen offset:128  lds
s_setpc_b64 s[88:89]
.p2align 8
s_mov_b64 s[40:41],%24
s_mov_b32 s42,7340032
s_mov_b32 s43,0x00020000
v_add_u32 v230,40960,v15
v_add_u32 v231,40960,v16
T a[112:113],230,0
T a[114:115],230,512
T a[116:117],231,0
T a[118:119],231,512
T a[120:121],230,4096
T a[122:123],230,4608
T a[124:125],231,4096
T a[126:127],231,4608
s_add_u32 s83,s50,1048576
s_add_u32 m0,s54,57344
s_nop 0
buffer_load_dwordx4 v13,s[40:43],s83 offen offset:0  lds
s_add_u32 m0,s54,58368
s_nop 0
buffer_load_dwordx4 v14,s[40:43],s83 offen offset:0  lds
s_add_u32 m0,s54,65408
s_nop 0
buffer_load_dwordx4 v13,s[40:43],s83 offen offset:128  lds
s_add_u32 m0,s54,66432
s_nop 0
buffer_load_dwordx4 v14,s[40:43],s83 offen offset:128  lds
s_setpc_b64 s[88:89]
.p2align 8
s_mov_b64 s[40:41],%24
s_mov_b32 s42,7340032
s_mov_b32 s43,0x00020000
v_add_u32 v230,57344,v15
v_add_u32 v231,57344,v16
T a[128:129],230,0
T a[130:131],230,512
T a[132:133],231,0
T a[134:135],231,512
T a[136:137],230,4096
T a[138:139],230,4608
T a[140:141],231,4096
T a[142:143],231,4608
s_add_u32 s83,s50,1179648
s_add_u32 m0,s54,73728
s_nop 0
buffer_load_dwordx4 v13,s[40:43],s83 offen offset:0  lds
s_add_u32 m0,s54,74752
s_nop 0
buffer_load_dwordx4 v14,s[40:43],s83 offen offset:0  lds
s_add_u32 m0,s54,81792
s_nop 0
buffer_load_dwordx4 v13,s[40:43],s83 offen offset:128  lds
s_add_u32 m0,s54,82816
s_nop 0
buffer_load_dwordx4 v14,s[40:43],s83 offen offset:128  lds
s_setpc_b64 s[88:89]
.p2align 8
s_mov_b64 s[40:41],%24
s_mov_b32 s42,7340032
s_mov_b32 s43,0x00020000
v_add_u32 v230,73728,v15
v_add_u32 v231,73728,v16
T a[144:145],230,0
T a[146:147],230,512
T a[148:149],231,0
T a[150:151],231,512
T a[152:153],230,4096
T a[154:155],230,4608
T a[156:157],231,4096
T a[158:159],231,4608
s_add_u32 s83,s50,1310720
s_add_u32 m0,s54,90112
s_nop 0
buffer_load_dwordx4 v13,s[40:43],s83 offen offset:0  lds
s_add_u32 m0,s54,91136
s_nop 0
buffer_load_dwordx4 v14,s[40:43],s83 offen offset:0  lds
s_add_u32 m0,s54,98176
s_nop 0
buffer_load_dwordx4 v13,s[40:43],s83 offen offset:128  lds
s_add_u32 m0,s54,99200
s_nop 0
buffer_load_dwordx4 v14,s[40:43],s83 offen offset:128  lds
s_setpc_b64 s[88:89]
.p2align 8
Lr_end_%=:
Lcode_r_e:
.purgem M
.purgem R
.purgem T
.purgem D
.purgem L
)"
  :: "s"(ws), "s"(__builtin_amdgcn_readfirstlane(sWave)), "s"(__builtin_amdgcn_readfirstlane(sG0)), "s"(__builtin_amdgcn_readfirstlane(sStep)), "s"(__builtin_amdgcn_readfirstlane(sWw)), "s"(__builtin_amdgcn_readfirstlane(sExp)), "v"(vRP), "v"(vRF), "v"(vRwr), "v"(vRrd), "v"(vRS), "v"(vFlag), "v"(vL4), "v"(vLY), "v"(vQ), "v"(vY0), "v"(vTr), "v"(vSa), "v"(vSb), "v"(vWd0), "v"(vWd1), "v"(vWr0), "v"(vWr1), "s"(q), "s"(w4) : "v0", "v1", "v2", "v3", "v4", "v5", "v6", "v7", "v8", "v9", "v10", "v11", "v12", "v13", "v14", "v15", "v16", "v17", "v18", "v19", "v20", "v21", "v22", "v23", "v24", "v25", "v26", "v27", "v28", "v29", "v30", "v31", "v32", "v33", "v34", "v35", "v36", "v37", "v38", "v39", "v40", "v41", "v42", "v43", "v44", "v45", "v46", "v47", "v48", "v49", "v50", "v51", "v52", "v53", "v54", "v55", "v56", "v57", "v58", "v59", "v60", "v61", "v62", "v63", "v64", "v65", "v66", "v67", "v68", "v69", "v70", "v71", "v72", "v73", "v74", "v75", "v76", "v77", "v78", "v79", "v80", "v81", "v82", "v83", "v84", "v85", "v86", "v87", "v88", "v89", "v90", "v91", "v92", "v93", "v94", "v95", "v96", "v97", "v98", "v99", "v100", "v101", "v102", "v103", "v104", "v105", "v106", "v107", "v108", "v109", "v110", "v111", "v112", "v113", "v114", "v115", "v116", "v117", "v118", "v119", "v120", "v121", "v122", "v123", "v124", "v125", "v126", "v127", "v128", "v129", "v130", "v131", "v132", "v133", "v134", "v135", "v136", "v137", "v138", "v139", "v140", "v141", "v142", "v143", "v144", "v145", "v146", "v147", "v148", "v149", "v150", "v151", "v152", "v153", "v154", "v155", "v156", "v157", "v158", "v159", "v160", "v161", "v162", "v163", "v164", "v165", "v166", "v167", "v168", "v169", "v170", "v171", "v172", "v173", "v174", "v175", "v176", "v177", "v178", "v179", "v180", "v181", "v182", "v183", "v184", "v185", "v186", "v187", "v188", "v189", "v190", "v191", "v192", "v193", "v194", "v195", "v196", "v197", "v198", "v199", "v200", "v201", "v202", "v203", "v204", "v205", "v206", "v207", "v208", "v209", "v210", "v211", "v212", "v213", "v214", "v215", "v216", "v217", "v218", "v219", "v220", "v221", "v222", "v223", "v224", "v225", "v226", "v227", "v228", "v229", "v230", "v231", "a0", "a1", "a2", "a3", "a4", "a5", "a6", "a7", "a8", "a9", "a10", "a11", "a12", "a13", "a14", "a15", "a16", "a17", "a18", "a19", "a20", "a21", "a22", "a23", "a24", "a25", "a26", "a27", "a28", "a29", "a30", "a31", "a32", "a33", "a34", "a35", "a36", "a37", "a38", "a39", "a40", "a41", "a42", "a43", "a44", "a45", "a46", "a47", "a48", "a49", "a50", "a51", "a52", "a53", "a54", "a55", "a56", "a57", "a58", "a59", "a60", "a61", "a62", "a63", "a64", "a65", "a66", "a67", "a68", "a69", "a70", "a71", "a72", "a73", "a74", "a75", "a76", "a77", "a78", "a79", "a80", "a81", "a82", "a83", "a84", "a85", "a86", "a87", "a88", "a89", "a90", "a91", "a92", "a93", "a94", "a95", "a96", "a97", "a98", "a99", "a100", "a101", "a102", "a103", "a104", "a105", "a106", "a107", "a108", "a109", "a110", "a111", "a112", "a113", "a114", "a115", "a116", "a117", "a118", "a119", "a120", "a121", "a122", "a123", "a124", "a125", "a126", "a127", "a128", "a129", "a130", "a131", "a132", "a133", "a134", "a135", "a136", "a137", "a138", "a139", "a140", "a141", "a142", "a143", "a144", "a145", "a146", "a147", "a148", "a149", "a150", "a151", "a152", "a153", "a154", "a155", "a156", "a157", "a158", "a159", "a160", "a161", "a162", "a163", "a164", "a165", "a166", "a167", "a168", "a169", "a170", "a171", "a172", "a173", "a174", "a175", "a176", "a177", "a178", "a179", "a180", "a181", "a182", "a183", "a184", "a185", "a186", "a187", "a188", "a189", "a190", "a191", "a192", "a193", "a194", "a195", "a196", "a197", "a198", "a199", "a200", "a201", "a202", "a203", "a204", "a205", "a206", "a207", "a208", "a209", "a210", "a211", "a212", "a213", "a214", "a215", "a216", "a217", "a218", "a219", "a220", "a221", "a222", "a223", "a224", "a225", "a226", "a227", "a228", "a229", "a230", "a231", "a232", "a233", "a234", "a235", "a236", "a237", "a238", "a239", "a240", "a241", "a242", "a243", "a244", "a245", "a246", "a247", "a248", "a249", "a250", "a251", "a252", "a253", "a254", "a255", "s36", "s37", "s38", "s39", "s40", "s41", "s42", "s43", "s44", "s45", "s46", "s47", "s48", "s49", "s50", "s51", "s52", "s53", "s54", "s55", "s56", "s57", "s58", "s59", "s60", "s61", "s62", "s63", "s64", "s65", "s66", "s67", "s68", "s69", "s70", "s71", "s72", "s73", "s74", "s75", "s76", "s77", "s78", "s79", "s80", "s81", "s82", "s83", "s84", "s85", "s86", "s87", "s88", "s89", "s90", "s91", "s92", "s93", "s94", "s95", "s96", "s97", "vcc", "scc", "memory");
    if (RHELP && slice < 16) { kind = 4; rep = 5; kh = slice >> 3; slice &= 7; }
  }
  CTS(9);
#define COMMON(NF, FOFF, FSTR, SNAP, LDW, NL, TOFF)                                                                                            \
  LANE_ID(ln);                                                                                                                       \
  const int l15 = ln & 15, lg = ln >> 4, lq = l15 >> 2, lp = ln & 3, rr = 8 * lg + lq, fj = ln & 31;                                 \
  const unsigned vL16 = ln * 16, vFlag = (ln < 32 && wave == 0) ? ln * 4 : OOB;                                                      \
  const unsigned vSnapOff = (wave == 0 && fj < (NF)) ? (FOFF) + (ln >> 5) * sStep * (FSTR) : OOB;                                    \
  const unsigned vSnapRd = (SNAP) + (fj < (NF) ? ln : ln & 32) * 4;                                                                  \
  const unsigned vWd0 = (ln >> 3) * (LDW) + ((ln & 7) ^ swzB(ln >> 3)) * 16, vWd1 = ((ln >> 3) + 8) * (LDW) + ((ln & 7) ^ swzB(8 + (ln >> 3))) * 16; \
  const unsigned vWr0 = (wave >> 1) * 8192 + rr * 128 + (((wave & 1) * 4 + lp) ^ swzB(rr)) * 16, vWr1 = vWr0 + 8;                    \
  const unsigned vWr2 = 16384 + rr * 128 + ((2 * wave + (lp >> 1)) ^ swzB(rr)) * 16 + (lp & 1) * 8;                                  \
  const unsigned sWave = wave, sRk = kind == 4 ? rep : t_start, sG0 = rep < 5 ? rep : rep == 5 ? U0 + 5 : U1 + rep, sDbg = sDbg0 + (rep < 5 ? 0 : 2048)
  if (kind == 3 || kind == 5) {
    const unsigned sStep = kind == 3 ? 5 : G3OR;
    COMMON(8, fj * 128, 1024, 131072, 7168, 256, 0);
    const unsigned vOutS = (lg & 1) * 8 + (((wave & 1) * 2 + (lg >> 1)) * 16 + l15) * 16 + (4 + (wave >> 1) - wave) * 1024;
    const unsigned vAux0 = (ln & 7) * 128 + (4 * wave + lg) * 8 + ((ln >> 3) & 1) * 4;
    const float k1 = -2.0f * 0.7978845608028654f * 1.4426950408889634f;
    const unsigned vAux1 = 139264 + (l15 < 4 ? (4 * wave + lg) * 16 + l15 * 4 : 512 + ln * 4), vAux2 = 139264 + l15 * 16;
    const unsigned vAux3 = l15 == 2 ? __float_as_uint(k1 * 0.044715f) : 0u, vAn = l15 == 3 ? 0x3f800000u : 0u, vAe = l15 == 1 ? __float_as_uint(k1) : 0u;
    const unsigned sOutOff = OFF_G + (slice * 6 + wave) * 1024, sFoutOff = OFF_FG + slice * 128, sAuxOff = 0, sWw = 16 * wave * 7168 + slice * 384, sBuf = 0;
    const unsigned sExp = __builtin_amdgcn_readfirstlane(ep);
    if (kind == 3) { asm volatile(
R"(.macro M d,w,x
v_mfma_f32_16x16x32_bf16 v[\d:\d+3], \w, v[\x:\x+3], v[\d:\d+3]
.endm
.macro R d,a,o
ds_read_b128 v[\d:\d+3], v\a offset:\o
.endm
.macro T d,a,o
ds_read_b64_tr_b16 \d, v\a offset:\o
.endm
.macro D a,s,o
buffer_load_dwordx4 v\a, s[36:39], s\s offen offset:\o sc1 lds
.endm
.macro L c
s_waitcnt lgkmcnt(\c)
.endm
Lcode_g3_b:
s_mov_b64 s[36:37],%0
s_mov_b32 s38,111243392
s_mov_b32 s39,0x00020000
s_mov_b32 s44,%1
s_mov_b32 s45,%2
s_mov_b32 s46,%3
s_mov_b32 s47,%4
s_mov_b32 s48,%5
s_mov_b32 s49,%6
s_mov_b32 s50,%7
s_mov_b32 s51,%8
s_mov_b32 s53,%9
v_mov_b32 v0,%10
v_mov_b32 v3,%11
v_mov_b32 v5,%12
v_mov_b32 v6,%13
v_mov_b32 v18,%14
v_mov_b32 v19,%15
v_mov_b32 v20,%16
v_mov_b32 v21,%17
v_mov_b32 v22,%18
v_mov_b32 v10,%19
v_mov_b32 v11,%20
v_mov_b32 v12,%21
v_mov_b32 v13,%22
v_mov_b32 v14,%23
v_mov_b32 v15,%24
v_mov_b32 v16,%25
s_mov_b64 s[40:41],%26
s_mov_b32 s42,7340032
s_mov_b32 s43,0x00020000
s_lshl_b32 s57,s44,11
s_cmp_ge_u32 s45,8
s_cbranch_scc1 Lg3_dly_%=
s_sleep 16
s_sleep 16
s_sleep 16
Lg3_dly_%=:
s_add_u32 s55,s50,0
s_add_u32 m0,s57,0
s_nop 0
buffer_load_dwordx4 v18,s[40:43],s55 offen offset:0  lds
s_add_u32 m0,s57,1024
s_nop 0
buffer_load_dwordx4 v19,s[40:43],s55 offen offset:0  lds
s_add_u32 m0,s57,8064
s_nop 0
buffer_load_dwordx4 v18,s[40:43],s55 offen offset:128  lds
s_add_u32 m0,s57,9088
s_nop 0
buffer_load_dwordx4 v19,s[40:43],s55 offen offset:128  lds
s_add_u32 m0,s57,16128
s_nop 0
buffer_load_dwordx4 v18,s[40:43],s55 offen offset:256  lds
s_add_u32 m0,s57,17152
s_nop 0
buffer_load_dwordx4 v19,s[40:43],s55 offen offset:256  lds
s_add_u32 s55,s50,458752
s_add_u32 m0,s57,24576
s_nop 0
buffer_load_dwordx4 v18,s[40:43],s55 offen offset:0  lds
s_add_u32 m0,s57,25600
s_nop 0
buffer_load_dwordx4 v19,s[40:43],s55 offen offset:0  lds
s_add_u32 m0,s57,32640
s_nop 0
buffer_load_dwordx4 v18,s[40:43],s55 offen offset:128  lds
s_add_u32 m0,s57,33664
s_nop 0
buffer_load_dwordx4 v19,s[40:43],s55 offen offset:128  lds
s_add_u32 m0,s57,40704
s_nop 0
buffer_load_dwordx4 v18,s[40:43],s55 offen offset:256  lds
s_add_u32 m0,s57,41728
s_nop 0
buffer_load_dwordx4 v19,s[40:43],s55 offen offset:256  lds
s_add_u32 s55,s50,917504
s_add_u32 m0,s57,49152
s_nop 0
buffer_load_dwordx4 v18,s[40:43],s55 offen offset:0  lds
s_add_u32 m0,s57,50176
s_nop 0
buffer_load_dwordx4 v19,s[40:43],s55 offen offset:0  lds
s_add_u32 m0,s57,57216
s_nop 0
buffer_load_dwordx4 v18,s[40:43],s55 offen offset:128  lds
s_add_u32 m0,s57,58240
s_nop 0
buffer_load_dwordx4 v19,s[40:43],s55 offen offset:128  lds
s_add_u32 m0,s57,65280
s_nop 0
buffer_load_dwordx4 v18,s[40:43],s55 offen offset:256  lds
s_add_u32 m0,s57,66304
s_nop 0
buffer_load_dwordx4 v19,s[40:43],s55 offen offset:256  lds
s_add_u32 s55,s50,1376256
s_add_u32 m0,s57,73728
s_nop 0
buffer_load_dwordx4 v18,s[40:43],s55 offen offset:0  lds
s_add_u32 m0,s57,74752
s_nop 0
buffer_load_dwordx4 v19,s[40:43],s55 offen offset:0  lds
s_add_u32 m0,s57,81792
s_nop 0
buffer_load_dwordx4 v18,s[40:43],s55 offen offset:128  lds
s_add_u32 m0,s57,82816
s_nop 0
buffer_load_dwordx4 v19,s[40:43],s55 offen offset:128  lds
s_add_u32 m0,s57,89856
s_nop 0
buffer_load_dwordx4 v18,s[40:43],s55 offen offset:256  lds
s_add_u32 m0,s57,90880
s_nop 0
buffer_load_dwordx4 v19,s[40:43],s55 offen offset:256  lds
s_add_u32 s55,s50,1835008
s_add_u32 m0,s57,98304
s_nop 0
buffer_load_dwordx4 v18,s[40:43],s55 offen offset:0  lds
s_add_u32 m0,s57,99328
s_nop 0
buffer_load_dwordx4 v19,s[40:43],s55 offen offset:0  lds
s_add_u32 m0,s57,106368
s_nop 0
buffer_load_dwordx4 v18,s[40:43],s55 offen offset:128  lds
s_add_u32 m0,s57,107392
s_nop 0
buffer_load_dwordx4 v19,s[40:43],s55 offen offset:128  lds
s_add_u32 m0,s57,114432
s_nop 0
buffer_load_dwordx4 v18,s[40:43],s55 offen offset:256  lds
s_add_u32 m0,s57,115456
s_nop 0
buffer_load_dwordx4 v19,s[40:43],s55 offen offset:256  lds
s_waitcnt vmcnt(24) lgkmcnt(0)
s_barrier
s_add_u32 s55,s50,2293760
s_add_u32 m0,s57,122880
s_nop 0
buffer_load_dwordx4 v18,s[40:43],s55 offen offset:0  lds
s_add_u32 m0,s57,123904
s_nop 0
buffer_load_dwordx4 v19,s[40:43],s55 offen offset:0  lds
s_add_u32 m0,s57,130944
s_nop 0
buffer_load_dwordx4 v18,s[40:43],s55 offen offset:128  lds
s_add_u32 m0,s57,131968
s_nop 0
buffer_load_dwordx4 v19,s[40:43],s55 offen offset:128  lds
s_add_u32 m0,s57,139008
s_nop 0
buffer_load_dwordx4 v18,s[40:43],s55 offen offset:256  lds
s_add_u32 m0,s57,140032
s_nop 0
buffer_load_dwordx4 v19,s[40:43],s55 offen offset:256  lds
v_add_u32 v26,0,v20
v_add_u32 v27,0,v21
v_add_u32 v28,0,v22
T a[0:1],26,0
T a[2:3],26,512
T a[4:5],27,0
T a[6:7],27,512
T a[8:9],28,0
T a[10:11],28,512
T a[12:13],26,4096
T a[14:15],26,4608
T a[16:17],27,4096
T a[18:19],27,4608
T a[20:21],28,4096
T a[22:23],28,4608
s_waitcnt vmcnt(24) lgkmcnt(0)
s_barrier
s_add_u32 s55,s50,2752512
s_add_u32 m0,s57,0
s_nop 0
buffer_load_dwordx4 v18,s[40:43],s55 offen offset:0  lds
s_add_u32 m0,s57,1024
s_nop 0
buffer_load_dwordx4 v19,s[40:43],s55 offen offset:0  lds
s_add_u32 m0,s57,8064
s_nop 0
buffer_load_dwordx4 v18,s[40:43],s55 offen offset:128  lds
s_add_u32 m0,s57,9088
s_nop 0
buffer_load_dwordx4 v19,s[40:43],s55 offen offset:128  lds
s_add_u32 m0,s57,16128
s_nop 0
buffer_load_dwordx4 v18,s[40:43],s55 offen offset:256  lds
s_add_u32 m0,s57,17152
s_nop 0
buffer_load_dwordx4 v19,s[40:43],s55 offen offset:256  lds
v_add_u32 v26,24576,v20
v_add_u32 v27,24576,v21
v_add_u32 v28,24576,v22
T a[24:25],26,0
T a[26:27],26,512
T a[28:29],27,0
T a[30:31],27,512
T a[32:33],28,0
T a[34:35],28,512
T a[36:37],26,4096
T a[38:39],26,4608
T a[40:41],27,4096
T a[42:43],27,4608
T a[44:45],28,4096
T a[46:47],28,4608
s_waitcnt vmcnt(24) lgkmcnt(0)
s_barrier
s_add_u32 s55,s50,3211264
s_add_u32 m0,s57,24576
s_nop 0
buffer_load_dwordx4 v18,s[40:43],s55 offen offset:0  lds
s_add_u32 m0,s57,25600
s_nop 0
buffer_load_dwordx4 v19,s[40:43],s55 offen offset:0  lds
s_add_u32 m0,s57,32640
s_nop 0
buffer_load_dwordx4 v18,s[40:43],s55 offen offset:128  lds
s_add_u32 m0,s57,33664
s_nop 0
buffer_load_dwordx4 v19,s[40:43],s55 offen offset:128  lds
s_add_u32 m0,s57,40704
s_nop 0
buffer_load_dwordx4 v18,s[40:43],s55 offen offset:256  lds
s_add_u32 m0,s57,41728
s_nop 0
buffer_load_dwordx4 v19,s[40:43],s55 offen offset:256  lds
v_add_u32 v26,49152,v20
v_add_u32 v27,49152,v21
v_add_u32 v28,49152,v22
T a[48:49],26,0
T a[50:51],26,512
T a[52:53],27,0
T a[54:55],27,512
T a[56:57],28,0
T a[58:59],28,512
T a[60:61],26,4096
T a[62:63],26,4608
T a[64:65],27,4096
T a[66:67],27,4608
T a[68:69],28,4096
T a[70:71],28,4608
s_waitcnt vmcnt(24) lgkmcnt(0)
s_barrier
s_add_u32 s55,s50,3670016
s_add_u32 m0,s57,49152
s_nop 0
buffer_load_dwordx4 v18,s[40:43],s55 offen offset:0  lds
s_add_u32 m0,s57,50176
s_nop 0
buffer_load_dwordx4 v19,s[40:43],s55 offen offset:0  lds
s_add_u32 m0,s57,57216
s_nop 0
buffer_load_dwordx4 v18,s[40:43],s55 offen offset:128  lds
s_add_u32 m0,s57,58240
s_nop 0
buffer_load_dwordx4 v19,s[40:43],s55 offen offset:128  lds
s_add_u32 m0,s57,65280
s_nop 0
buffer_load_dwordx4 v18,s[40:43],s55 offen offset:256  lds
s_add_u32 m0,s57,66304
s_nop 0
buffer_load_dwordx4 v19,s[40:43],s55 offen offset:256  lds
v_add_u32 v26,73728,v20
v_add_u32 v27,73728,v21
v_add_u32 v28,73728,v22
T a[72:73],26,0
T a[74:75],26,512
T a[76:77],27,0
T a[78:79],27,512
T a[80:81],28,0
T a[82:83],28,512
T a[84:85],26,4096
T a[86:87],26,4608
T a[88:89],27,4096
T a[90:91],27,4608
T a[92:93],28,4096
T a[94:95],28,4608
s_waitcnt vmcnt(24) lgkmcnt(0)
s_barrier
s_add_u32 s55,s50,4128768
s_add_u32 m0,s57,73728
s_nop 0
buffer_load_dwordx4 v18,s[40:43],s55 offen offset:0  lds
s_add_u32 m0,s57,74752
s_nop 0
buffer_load_dwordx4 v19,s[40:43],s55 offen offset:0  lds
s_add_u32 m0,s57,81792
s_nop 0
buffer_load_dwordx4 v18,s[40:43],s55 offen offset:128  lds
s_add_u32 m0,s57,82816
s_nop 0
buffer_load_dwordx4 v19,s[40:43],s55 offen offset:128  lds
s_add_u32 m0,s57,89856
s_nop 0
buffer_load_dwordx4 v18,s[40:43],s55 offen offset:256  lds
s_add_u32 m0,s57,90880
s_nop 0
buffer_load_dwordx4 v19,s[40:43],s55 offen offset:256  lds
v_add_u32 v26,98304,v20
v_add_u32 v27,98304,v21
v_add_u32 v28,98304,v22
T a[96:97],26,0
T a[98:99],26,512
T a[100:101],27,0
T a[102:103],27,512
T a[104:105],28,0
T a[106:107],28,512
T a[108:109],26,4096
T a[110:111],26,4608
T a[112:113],27,4096
T a[114:115],27,4608
T a[116:117],28,4096
T a[118:119],28,4608
s_waitcnt vmcnt(24) lgkmcnt(0)
s_barrier
s_add_u32 s55,s50,4587520
s_add_u32 m0,s57,98304
s_nop 0
buffer_load_dwordx4 v18,s[40:43],s55 offen offset:0  lds
s_add_u32 m0,s57,99328
s_nop 0
buffer_load_dwordx4 v19,s[40:43],s55 offen offset:0  lds
s_add_u32 m0,s57,106368
s_nop 0
buffer_load_dwordx4 v18,s[40:43],s55 offen offset:128  lds
s_add_u32 m0,s57,107392
s_nop 0
buffer_load_dwordx4 v19,s[40:43],s55 offen offset:128  lds
s_add_u32 m0,s57,114432
s_nop 0
buffer_load_dwordx4 v18,s[40:43],s55 offen offset:256  lds
s_add_u32 m0,s57,115456
s_nop 0
buffer_load_dwordx4 v19,s[40:43],s55 offen offset:256  lds
v_add_u32 v26,122880,v20
v_add_u32 v27,122880,v21
v_add_u32 v28,122880,v22
T a[120:121],26,0
T a[122:123],26,512
T a[124:125],27,0
T a[126:127],27,512
T a[128:129],28,0
T a[130:131],28,512
T a[132:133],26,4096
T a[134:135],26,4608
T a[136:137],27,4096
T a[138:139],27,4608
T a[140:141],28,4096
T a[142:143],28,4608
s_waitcnt vmcnt(24) lgkmcnt(0)
s_barrier
s_add_u32 s55,s50,5046272
s_add_u32 m0,s57,122880
s_nop 0
buffer_load_dwordx4 v18,s[40:43],s55 offen offset:0  lds
s_add_u32 m0,s57,123904
s_nop 0
buffer_load_dwordx4 v19,s[40:43],s55 offen offset:0  lds
s_add_u32 m0,s57,130944
s_nop 0
buffer_load_dwordx4 v18,s[40:43],s55 offen offset:128  lds
s_add_u32 m0,s57,131968
s_nop 0
buffer_load_dwordx4 v19,s[40:43],s55 offen offset:128  lds
s_add_u32 m0,s57,139008
s_nop 0
buffer_load_dwordx4 v18,s[40:43],s55 offen offset:256  lds
s_add_u32 m0,s57,140032
s_nop 0
buffer_load_dwordx4 v19,s[40:43],s55 offen offset:256  lds
v_add_u32 v26,0,v20
v_add_u32 v27,0,v21
v_add_u32 v28,0,v22
T a[144:145],26,0
T a[146:147],26,512
T a[148:149],27,0
T a[150:151],27,512
T a[152:153],28,0
T a[154:155],28,512
T a[156:157],26,4096
T a[158:159],26,4608
T a[160:161],27,4096
T a[162:163],27,4608
T a[164:165],28,4096
T a[166:167],28,4608
s_waitcnt vmcnt(24) lgkmcnt(0)
s_barrier
s_add_u32 s55,s50,5505024
s_add_u32 m0,s57,0
s_nop 0
buffer_load_dwordx4 v18,s[40:43],s55 offen offset:0  lds
s_add_u32 m0,s57,1024
s_nop 0
buffer_load_dwordx4 v19,s[40:43],s55 offen offset:0  lds
s_add_u32 m0,s57,8064
s_nop 0
buffer_load_dwordx4 v18,s[40:43],s55 offen offset:128  lds
s_add_u32 m0,s57,9088
s_nop 0
buffer_load_dwordx4 v19,s[40:43],s55 offen offset:128  lds
s_add_u32 m0,s57,16128
s_nop 0
buffer_load_dwordx4 v18,s[40:43],s55 offen offset:256  lds
s_add_u32 m0,s57,17152
s_nop 0
buffer_load_dwordx4 v19,s[40:43],s55 offen offset:256  lds
v_add_u32 v26,24576,v20
v_add_u32 v27,24576,v21
v_add_u32 v28,24576,v22
T a[168:169],26,0
T a[170:171],26,512
T a[172:173],27,0
T a[174:175],27,512
T a[176:177],28,0
T a[178:179],28,512
T a[180:181],26,4096
T a[182:183],26,4608
T a[184:185],27,4096
T a[186:187],27,4608
T a[188:189],28,4096
T a[190:191],28,4608
s_waitcnt vmcnt(24) lgkmcnt(0)
s_barrier
s_add_u32 s55,s50,5963776
s_add_u32 m0,s57,24576
s_nop 0
buffer_load_dwordx4 v18,s[40:43],s55 offen offset:0  lds
s_add_u32 m0,s57,25600
s_nop 0
buffer_load_dwordx4 v19,s[40:43],s55 offen offset:0  lds
s_add_u32 m0,s57,32640
s_nop 0
buffer_load_dwordx4 v18,s[40:43],s55 offen offset:128  lds
s_add_u32 m0,s57,33664
s_nop 0
buffer_load_dwordx4 v19,s[40:43],s55 offen offset:128  lds
s_add_u32 m0,s57,40704
s_nop 0
buffer_load_dwordx4 v18,s[40:43],s55 offen offset:256  lds
s_add_u32 m0,s57,41728
s_nop 0
buffer_load_dwordx4 v19,s[40:43],s55 offen offset:256  lds
v_add_u32 v26,49152,v20
v_add_u32 v27,49152,v21
v_add_u32 v28,49152,v22
T a[192:193],26,0
T a[194:195],26,512
T a[196:197],27,0
T a[198:199],27,512
T a[200:201],28,0
T a[202:203],28,512
T a[204:205],26,4096
T a[206:207],26,4608
T a[208:209],27,4096
T a[210:211],27,4608
T a[212:213],28,4096
T a[214:215],28,4608
s_waitcnt vmcnt(24) lgkmcnt(0)
s_barrier
s_add_u32 s55,s50,6422528
s_add_u32 m0,s57,49152
s_nop 0
buffer_load_dwordx4 v18,s[40:43],s55 offen offset:0  lds
s_add_u32 m0,s57,50176
s_nop 0
buffer_load_dwordx4 v19,s[40:43],s55 offen offset:0  lds
s_add_u32 m0,s57,57216
s_nop 0
buffer_load_dwordx4 v18,s[40:43],s55 offen offset:128  lds
s_add_u32 m0,s57,58240
s_nop 0
buffer_load_dwordx4 v19,s[40:43],s55 offen offset:128  lds
s_add_u32 m0,s57,65280
s_nop 0
buffer_load_dwordx4 v18,s[40:43],s55 offen offset:256  lds
s_add_u32 m0,s57,66304
s_nop 0
buffer_load_dwordx4 v19,s[40:43],s55 offen offset:256  lds
v_add_u32 v26,73728,v20
v_add_u32 v27,73728,v21
v_add_u32 v28,73728,v22
T a[216:217],26,0
T a[218:219],26,512
T a[220:221],27,0
)"
R"(T a[222:223],27,512
T a[224:225],28,0
T a[226:227],28,512
T a[228:229],26,4096
T a[230:231],26,4608
T a[232:233],27,4096
T a[234:235],27,4608
T a[236:237],28,4096
T a[238:239],28,4608
s_waitcnt vmcnt(24) lgkmcnt(0)
s_barrier
s_add_u32 s55,s50,6881280
s_add_u32 m0,s57,73728
s_nop 0
buffer_load_dwordx4 v18,s[40:43],s55 offen offset:0  lds
s_add_u32 m0,s57,74752
s_nop 0
buffer_load_dwordx4 v19,s[40:43],s55 offen offset:0  lds
s_add_u32 m0,s57,81792
s_nop 0
buffer_load_dwordx4 v18,s[40:43],s55 offen offset:128  lds
s_add_u32 m0,s57,82816
s_nop 0
buffer_load_dwordx4 v19,s[40:43],s55 offen offset:128  lds
s_add_u32 m0,s57,89856
s_nop 0
buffer_load_dwordx4 v18,s[40:43],s55 offen offset:256  lds
s_add_u32 m0,s57,90880
s_nop 0
buffer_load_dwordx4 v19,s[40:43],s55 offen offset:256  lds
v_add_u32 v26,98304,v20
v_add_u32 v27,98304,v21
v_add_u32 v28,98304,v22
T a[240:241],26,0
T a[242:243],26,512
T a[244:245],27,0
T a[246:247],27,512
T a[248:249],28,0
T a[250:251],28,512
T a[252:253],26,4096
T a[254:255],26,4608
T v[128:129],27,4096
T v[130:131],27,4608
T v[132:133],28,4096
T v[134:135],28,4608
s_waitcnt vmcnt(24) lgkmcnt(0)
s_barrier
v_add_u32 v26,122880,v20
v_add_u32 v27,122880,v21
v_add_u32 v28,122880,v22
T v[136:137],26,0
T v[138:139],26,512
T v[140:141],27,0
T v[142:143],27,512
T v[144:145],28,0
T v[146:147],28,512
T v[148:149],26,4096
T v[150:151],26,4608
T v[152:153],27,4096
T v[154:155],27,4608
T v[156:157],28,4096
T v[158:159],28,4608
s_waitcnt vmcnt(18) lgkmcnt(0)
s_barrier
v_add_u32 v26,0,v20
v_add_u32 v27,0,v21
v_add_u32 v28,0,v22
T v[160:161],26,0
T v[162:163],26,512
T v[164:165],27,0
T v[166:167],27,512
T v[168:169],28,0
T v[170:171],28,512
T v[172:173],26,4096
T v[174:175],26,4608
T v[176:177],27,4096
T v[178:179],27,4608
T v[180:181],28,4096
T v[182:183],28,4608
s_waitcnt vmcnt(12) lgkmcnt(0)
s_barrier
v_add_u32 v26,24576,v20
v_add_u32 v27,24576,v21
v_add_u32 v28,24576,v22
T v[184:185],26,0
T v[186:187],26,512
T v[188:189],27,0
T v[190:191],27,512
T v[192:193],28,0
T v[194:195],28,512
T v[196:197],26,4096
T v[198:199],26,4608
T v[200:201],27,4096
T v[202:203],27,4608
T v[204:205],28,4096
T v[206:207],28,4608
s_waitcnt vmcnt(6) lgkmcnt(0)
s_barrier
v_add_u32 v26,49152,v20
v_add_u32 v27,49152,v21
v_add_u32 v28,49152,v22
T v[208:209],26,0
T v[210:211],26,512
T v[212:213],27,0
T v[214:215],27,512
T v[216:217],28,0
T v[218:219],28,512
T v[220:221],26,4096
T v[222:223],26,4608
T v[224:225],27,4096
T v[226:227],27,4608
T v[228:229],28,4096
T v[230:231],28,4608
s_waitcnt vmcnt(0) lgkmcnt(0)
s_barrier
v_add_u32 v26,73728,v20
v_add_u32 v27,73728,v21
v_add_u32 v28,73728,v22
T v[232:233],26,0
T v[234:235],26,512
T v[236:237],27,0
T v[238:239],27,512
T v[240:241],28,0
T v[242:243],28,512
T v[244:245],26,4096
T v[246:247],26,4608
T v[248:249],27,4096
T v[250:251],27,4608
T v[252:253],28,4096
T v[254:255],28,4608
L 0
s_barrier
v_mov_b32 v42,0x3f803f80
v_mov_b32 v43,0x3f803f80
v_mov_b32 v44,0x3f803f80
v_mov_b32 v45,0x3f803f80
s_nop 1
v_mfma_f32_16x16x32_bf16 v[82:85],a[0:3],v[42:45],0
v_mfma_f32_16x16x32_bf16 v[86:89],a[4:7],v[42:45],0
v_mfma_f32_16x16x32_bf16 v[90:93],a[8:11],v[42:45],0
M 82,a[12:15],42
M 86,a[16:19],42
M 90,a[20:23],42
M 82,a[24:27],42
M 86,a[28:31],42
M 90,a[32:35],42
M 82,a[36:39],42
M 86,a[40:43],42
M 90,a[44:47],42
M 82,a[48:51],42
M 86,a[52:55],42
M 90,a[56:59],42
M 82,a[60:63],42
M 86,a[64:67],42
M 90,a[68:71],42
M 82,a[72:75],42
M 86,a[76:79],42
M 90,a[80:83],42
M 82,a[84:87],42
M 86,a[88:91],42
M 90,a[92:95],42
M 82,a[96:99],42
M 86,a[100:103],42
M 90,a[104:107],42
M 82,a[108:111],42
M 86,a[112:115],42
M 90,a[116:119],42
M 82,a[120:123],42
M 86,a[124:127],42
M 90,a[128:131],42
M 82,a[132:135],42
M 86,a[136:139],42
M 90,a[140:143],42
M 82,a[144:147],42
M 86,a[148:151],42
M 90,a[152:155],42
M 82,a[156:159],42
M 86,a[160:163],42
M 90,a[164:167],42
M 82,a[168:171],42
M 86,a[172:175],42
M 90,a[176:179],42
M 82,a[180:183],42
M 86,a[184:187],42
M 90,a[188:191],42
M 82,a[192:195],42
M 86,a[196:199],42
M 90,a[200:203],42
M 82,a[204:207],42
M 86,a[208:211],42
M 90,a[212:215],42
M 82,a[216:219],42
M 86,a[220:223],42
M 90,a[224:227],42
M 82,a[228:231],42
M 86,a[232:235],42
M 90,a[236:239],42
M 82,a[240:243],42
M 86,a[244:247],42
M 90,a[248:251],42
M 82,a[252:255],42
M 86,v[128:131],42
M 90,v[132:135],42
M 82,v[136:139],42
M 86,v[140:143],42
M 90,v[144:147],42
M 82,v[148:151],42
M 86,v[152:155],42
M 90,v[156:159],42
M 82,v[160:163],42
M 86,v[164:167],42
M 90,v[168:171],42
M 82,v[172:175],42
M 86,v[176:179],42
M 90,v[180:183],42
M 82,v[184:187],42
M 86,v[188:191],42
M 90,v[192:195],42
M 82,v[196:199],42
M 86,v[200:203],42
M 90,v[204:207],42
M 82,v[208:211],42
M 86,v[212:215],42
M 90,v[216:219],42
M 82,v[220:223],42
M 86,v[224:227],42
M 90,v[228:231],42
M 82,v[232:235],42
M 86,v[236:239],42
M 90,v[240:243],42
M 82,v[244:247],42
M 86,v[248:251],42
M 90,v[252:255],42
v_mov_b32 v4,s53
s_mov_b32 s82,0
s_mov_b32 s80,0
s_mov_b32 s54,s45
s_mul_i32 s57,s44,4096
s_mul_i32 s55,s45,32768
s_add_u32 s55,s55,s57
s_add_u32 s55,s55,31457280
s_mul_i32 s57,s44,4096
s_mul_i32 s60,s46,32768
s_mul_i32 s61,s46,1024
s_mul_i32 s62,s46,114688
s_mul_i32 s63,s46,2560
s_mul_i32 s64,s46,1024
s_lshl_b32 s59,s44,8
s_add_u32 s59,s59,131072
s_mul_i32 s58,s45,1024
s_add_u32 s58,s58,109613056
s_mul_i32 s65,s45,114688
s_add_u32 s65,s65,s47
s_mul_i32 s68,s45,2560
s_add_u32 s68,s68,s48
s_mul_i32 s73,s45,1024
s_add_u32 s73,s73,107479040
s_mul_i32 s74,s45,32768
s_add_u32 s74,s74,s49
s_mul_i32 s75,s45,1024
s_add_u32 s75,s75,107479040
s_mov_b32 s66,0x7fff0000
s_mov_b32 s67,0x7fff0000
s_mov_b32 s69,0x7fff0000
s_mov_b32 s76,0x7fff0000
s_mov_b32 s77,0x7fff0000
s_mov_b32 s70,0x7fff0000
s_mov_b32 s71,0x7fff0000
s_mov_b32 s72,0x7fff0000
v_mov_b32 v1,v0
v_add_u32 v2,16384,v0
s_mov_b32 s84,16384
s_mov_b32 s86,0x3a800000
s_mov_b32 s87,0x3727c5ac
s_mov_b32 s88,0xc0135761
s_mov_b32 s89,0x3d372713
s_lshr_b32 s85,s44,1
s_mul_i32 s85,s85,127
v_lshlrev_b32 v8,22,v0
v_and_b32 v8,0x80000000,v8
v_or_b32 v8,v8,v5
v_add_u32 v2,32768,v0
s_lshl_b32 s84,s57,1
s_add_u32 s84,s84,32768
v_and_b32 v94,240,v0
v_cmp_eq_u32 vcc,0,v94
s_nop 1
v_cndmask_b32 v17,0,1.0,vcc
s_getpc_b64 s[40:41]
Lg3_pc_%=:
s_lshl_b32 s78,s44,13
v_lshlrev_b32 v94,3,v0
v_add_u32 v94,s78,v94
v_min_u32 v94,Lcode_g3_e-Lg3_pc_%=-4,v94
s_mov_b32 m0,163584
s_nop 0
global_load_lds_dword v94,s[40:41]
s_add_u32 m0,s59,4096
s_nop 0
buffer_load_dword v8,s[36:39],s58 offen offset:0 sc1 lds
buffer_store_dword v4,v3,s[36:39],s71 offen offset:0 sc0 sc1
s_add_u32 m0,s59,5120
s_nop 0
buffer_load_dword v8,s[36:39],s58 offen offset:0 sc1 lds
buffer_store_dword v4,v3,s[36:39],s71 offen offset:0 sc0 sc1
s_mov_b32 s83,0
Lg3_poll_%=:
s_add_u32 s78,s83,2
s_and_b32 s78,s78,3
s_lshl_b32 s78,s78,10
s_add_u32 s78,s78,4096
s_add_u32 m0,s59,s78
s_nop 0
buffer_load_dword v8,s[36:39],s58 offen offset:0 sc1 lds
s_and_b32 s78,s83,3
s_lshl_b32 s78,s78,10
v_add_u32 v94,s78,v6
s_waitcnt vmcnt(4)
s_barrier
ds_read_b32 v7,v94 offset:4096
buffer_store_dword v4,v3,s[36:39],s70 offen offset:0 sc0 sc1
s_mov_b32 s70,s71
s_mov_b32 s71,s72
s_mov_b32 s72,0x7fff0000
s_add_u32 s83,s83,1
s_add_u32 s82,s82,1
L 0
v_cmp_eq_u32 vcc,s53,v7
s_nop 0
s_cmp_eq_u32 vcc_lo,-1
s_cbranch_scc1 Lg3_go_%=
s_cmp_lt_u32 s82,20000
s_cbranch_scc1 Lg3_poll_%=
Lg3_go_%=:
buffer_load_dword v114,v11,s[36:39],s73 offen offset:0 sc1
s_add_u32 s73,s73,s64
s_add_u32 m0,s57,0
s_add_u32 s56,s55,0
D 0,56,0
D 0,56,1024
D 0,56,2048
D 0,56,3072
s_add_u32 s58,s58,s61
s_add_u32 m0,s59,2048
s_nop 0
buffer_load_dword v8,s[36:39],s58 offen offset:0 sc1 lds
s_add_u32 m0,s57,16384
s_add_u32 s56,s55,16384
D 0,56,0
D 0,56,1024
D 0,56,2048
D 0,56,3072
s_add_u32 s55,s55,s60
s_sub_u32 s57,s84,s57
s_add_u32 s54,s54,s46
s_waitcnt vmcnt(4)
s_barrier
buffer_store_dword v4,v3,s[36:39],s70 offen offset:0 sc0 sc1
s_min_u32 s71,s71,s72
buffer_store_dword v4,v3,s[36:39],s71 offen offset:0 sc0 sc1
s_mov_b32 s70,0x7fff0000
s_mov_b32 s71,0x7fff0000
s_mov_b32 s72,0x7fff0000
s_cmp_eq_u32 s80,1
buffer_store_dword v4,v3,s[36:39],s71 offen offset:0 sc0 sc1
s_cbranch_scc1 Lg3_st1_%=
s_branch Lg3_st0_%=
Lg3_gox_%=:
s_add_u32 m0,s59,2048
s_nop 0
buffer_load_dword v8,s[36:39],s58 offen offset:0 sc1 lds
s_waitcnt vmcnt(0)
s_barrier
buffer_store_dword v4,v3,s[36:39],s70 offen offset:0 sc0 sc1
s_min_u32 s71,s71,s72
buffer_store_dword v4,v3,s[36:39],s71 offen offset:0 sc0 sc1
s_mov_b32 s70,0x7fff0000
s_mov_b32 s71,0x7fff0000
s_mov_b32 s72,0x7fff0000
s_cmp_eq_u32 s80,1
s_cbranch_scc1 Lg3_st1_%=
Lg3_st0_%=:
ds_read_b32 v7,v6 offset:2048
R 42,1,0
R 46,1,1024
R 50,1,2048
R 54,1,3072
R 58,1,4096
L 5
v_cmp_eq_u32 vcc,s53,v7
L 3
s_cmp_eq_u32 vcc_lo,-1
s_cbranch_scc1 Lg3_F0_%=
s_branch Lg3_L0_%=
Lg3_st1_%=:
ds_read_b32 v7,v6 offset:2048
R 42,2,0
R 46,2,1024
R 50,2,2048
R 54,2,3072
R 58,2,4096
L 5
v_cmp_eq_u32 vcc,s53,v7
L 3
s_cmp_eq_u32 vcc_lo,-1
s_cbranch_scc1 Lg3_F1_%=
s_branch Lg3_L1_%=
Lg3_F0_%=:
v_mfma_f32_16x16x32_bf16 v[26:29],a[8:11],v[42:45],0
s_add_u32 m0,s57,0
R 62,1,5120
v_add_f32_dpp v115,v114,v114 quad_perm:[1,0,3,2] row_mask:0xf bank_mask:0xf
M 26,a[20:23],46
buffer_load_dword v114,v11,s[36:39],s73 offen offset:0 sc1
v_mfma_f32_16x16x32_bf16 v[22:25],a[4:7],v[42:45],0
s_add_u32 s56,s55,0
v_add_f32_dpp v115,v115,v115 quad_perm:[2,3,0,1] row_mask:0xf bank_mask:0xf
v_fma_f32 v30,-v78,v82,v30
M 22,a[16:19],46
s_add_u32 s58,s58,s61
R 66,1,6144
v_fma_f32 v31,-v78,v83,v31
v_mfma_f32_16x16x32_bf16 v[18:21],a[0:3],v[42:45],0
D 0,56,0
M 18,a[12:15],46
L 3
v_add_f32_dpp v118,v115,v115 row_half_mirror row_mask:0xf bank_mask:0xf
v_fma_f32 v32,-v78,v84,v32
M 18,a[24:27],50
s_mov_b32 s66,s65
R 70,1,7168
v_fma_f32 v33,-v78,v85,v33
M 18,a[36:39],54
s_add_u32 s65,s65,s62
v_mov_b32_dpp v115,v118 row_ror:8 row_mask:0xf bank_mask:0xf
v_fma_f32 v115,-v118,v118,v115
M 22,a[28:31],50
s_add_u32 s54,s54,s46
v_max_f32 v115,s87,v115
v_mul_f32 v118,v17,v118
M 22,a[40:43],54
s_add_u32 s73,s73,s64
R 42,1,8192
v_mul_f32 v94,v30,v30
M 26,a[32:35],50
v_rsq_f32 v116,v115
M 26,a[44:47],54
L 3
D 0,56,1024
M 26,a[56:59],58
R 46,1,9216
v_fma_f32 v115,v15,v115,v16
M 26,a[68:71],62
v_mul_f32 v119,v116,v116
v_fma_f32 v115,v14,v119,v115
M 22,a[52:55],58
v_fma_f32 v118,v116,v115,v118
v_mul_f32 v95,v31,v31
M 22,a[64:67],62
R 50,1,10240
v_mul_f32 v96,v32,v32
M 18,a[48:51],58
ds_write_b32 v12,v118 offset:0
M 18,a[60:63],62
L 4
v_mul_f32 v97,v33,v33
v_fma_f32 v94,v80,v94,v79
M 18,a[72:75],66
R 54,1,11264
v_fma_f32 v95,v80,v95,v79
M 18,a[84:87],70
v_fma_f32 v96,v80,v96,v79
v_fma_f32 v97,v80,v97,v79
M 22,a[76:79],66
v_mul_f32 v94,v30,v94
v_mul_f32 v95,v31,v95
M 22,a[88:91],70
R 58,1,12288
v_mul_f32 v96,v32,v96
M 26,a[80:83],66
D 0,56,2048
M 26,a[92:95],70
L 4
v_exp_f32 v94,v94
M 26,a[104:107],42
R 62,1,13312
v_mul_f32 v97,v33,v97
M 26,a[116:119],46
v_exp_f32 v95,v95
M 22,a[100:103],42
v_exp_f32 v96,v96
M 22,a[112:115],46
R 66,1,14336
v_fma_f32 v94,v94,v81,v81
M 18,a[96:99],42
v_exp_f32 v97,v97
M 18,a[108:111],46
L 3
v_rcp_f32 v94,v94
M 18,a[120:123],50
)"
R"(R 70,1,15360
v_fma_f32 v95,v95,v81,v81
M 18,a[132:135],54
s_waitcnt vmcnt(7)
D 0,56,3072
M 22,a[124:127],50
s_barrier
v_rcp_f32 v95,v95
M 22,a[136:139],54
s_add_u32 m0,s59,0
R 42,1,16384
v_fma_f32 v96,v96,v81,v81
M 26,a[128:131],50
s_add_u32 s56,s55,16384
v_rcp_f32 v96,v96
M 26,a[140:143],54
L 3
v_fma_f32 v97,v97,v81,v81
v_mul_f32 v30,v30,v94
M 26,a[152:155],58
s_add_u32 s55,s55,s60
R 46,1,17408
v_mul_f32 v31,v31,v95
M 26,a[164:167],62
v_rcp_f32 v97,v97
M 22,a[148:151],58
v_mul_f32 v32,v32,v96
v_mul_f32 v33,v33,v97
M 22,a[160:163],62
R 50,1,18432
v_cvt_pk_bf16_f32 v106,v30,v31
M 18,a[144:147],58
buffer_load_dword v8,s[36:39],s58 offen offset:0 sc1 lds
M 18,a[156:159],62
L 3
v_cvt_pk_bf16_f32 v107,v32,v33
v_fma_f32 v34,-v78,v86,v34
M 18,a[168:171],66
s_add_u32 m0,s57,16384
R 54,1,19456
v_fma_f32 v35,-v78,v87,v35
M 18,a[180:183],70
s_sub_u32 s57,s84,s57
v_fma_f32 v36,-v78,v88,v36
v_fma_f32 v37,-v78,v89,v37
M 22,a[172:175],66
v_mul_f32 v98,v34,v34
v_mul_f32 v99,v35,v35
M 22,a[184:187],70
R 58,1,20480
v_mul_f32 v100,v36,v36
M 26,a[176:179],66
D 0,56,0
M 26,a[188:191],70
L 3
v_mul_f32 v101,v37,v37
v_fma_f32 v98,v80,v98,v79
M 26,a[200:203],42
R 62,1,21504
v_fma_f32 v99,v80,v99,v79
M 26,a[212:215],46
v_fma_f32 v100,v80,v100,v79
v_fma_f32 v101,v80,v101,v79
M 22,a[196:199],42
v_mul_f32 v98,v34,v98
v_mul_f32 v99,v35,v99
M 22,a[208:211],46
R 66,1,22528
v_mul_f32 v100,v36,v100
M 18,a[192:195],42
v_exp_f32 v98,v98
M 18,a[204:207],46
L 3
v_exp_f32 v99,v99
M 18,a[216:219],50
R 70,1,23552
v_mul_f32 v101,v37,v101
M 18,a[228:231],54
D 0,56,1024
M 22,a[220:223],50
v_exp_f32 v100,v100
M 22,a[232:235],54
R 42,1,24576
v_fma_f32 v98,v98,v81,v81
M 26,a[224:227],50
v_exp_f32 v101,v101
M 26,a[236:239],54
L 3
v_rcp_f32 v98,v98
M 26,a[248:251],58
R 46,1,25600
v_fma_f32 v99,v99,v81,v81
M 26,v[132:135],62
v_rcp_f32 v99,v99
M 22,a[244:247],58
v_fma_f32 v100,v100,v81,v81
v_fma_f32 v101,v101,v81,v81
M 22,v[128:131],62
R 50,1,26624
v_mul_f32 v34,v34,v98
M 18,a[240:243],58
v_rcp_f32 v100,v100
M 18,a[252:255],62
L 3
v_rcp_f32 v101,v101
M 18,v[136:139],66
R 54,1,27648
v_mul_f32 v35,v35,v99
M 18,v[148:151],70
D 0,56,2048
M 22,v[140:143],66
v_mul_f32 v36,v36,v100
v_mul_f32 v37,v37,v101
M 22,v[152:155],70
R 58,1,28672
v_cvt_pk_bf16_f32 v108,v34,v35
M 26,v[144:147],66
v_cvt_pk_bf16_f32 v109,v36,v37
v_fma_f32 v38,-v78,v90,v38
M 26,v[156:159],70
L 3
v_fma_f32 v39,-v78,v91,v39
v_fma_f32 v40,-v78,v92,v40
M 26,v[168:171],42
R 62,1,29696
v_fma_f32 v41,-v78,v93,v41
M 26,v[180:183],46
v_mul_f32 v102,v38,v38
v_mul_f32 v103,v39,v39
M 22,v[164:167],42
v_mul_f32 v104,v40,v40
v_mul_f32 v105,v41,v41
M 22,v[176:179],46
R 66,1,30720
v_fma_f32 v102,v80,v102,v79
M 18,v[160:163],42
v_fma_f32 v103,v80,v103,v79
v_fma_f32 v104,v80,v104,v79
M 18,v[172:175],46
L 3
D 0,56,3072
M 18,v[184:187],50
s_nop 0
R 70,1,31744
v_fma_f32 v105,v80,v105,v79
M 18,v[196:199],54
s_waitcnt vmcnt(4)
v_mul_f32 v102,v38,v102
v_mul_f32 v103,v39,v103
M 22,v[188:191],50
s_barrier
buffer_store_dword v4,v3,s[36:39],s70 offen offset:0 sc0 sc1
M 22,v[200:203],54
s_mov_b32 s70,s69
R 42,2,0
ds_read_b32 v7,v6 offset:0
M 26,v[192:195],50
s_mov_b32 s69,s68
R 74,13,0
v_mul_f32 v104,v40,v104
M 26,v[204:207],54
L 5
v_exp_f32 v102,v102
M 26,v[216:219],58
s_add_u32 s68,s68,s63
R 46,2,1024
v_mul_f32 v105,v41,v105
M 26,v[228:231],62
v_exp_f32 v103,v103
M 22,v[212:215],58
buffer_store_dwordx4 v[106:109],v0,s[36:39],s67 offen offset:0 sc0 sc1
v_fma_f32 v102,v102,v81,v81
M 22,v[224:227],62
L 2
R 50,2,2048
v_fma_f32 v103,v103,v81,v81
M 18,v[208:211],58
v_exp_f32 v104,v104
M 18,v[220:223],62
s_nop 0
v_exp_f32 v105,v105
M 18,v[232:235],66
R 54,2,3072
v_fma_f32 v104,v104,v81,v81
M 18,v[244:247],70
v_fma_f32 v105,v105,v81,v81
v_cmp_eq_u32 vcc,s53,v7
M 22,v[236:239],66
v_rcp_f32 v102,v102
v_rcp_f32 v103,v103
v_rcp_f32 v104,v104
v_rcp_f32 v105,v105
M 22,v[248:251],70
s_cmp_eq_u32 vcc_lo,-1
R 58,2,4096
v_mul_f32 v38,v38,v102
v_mul_f32 v39,v39,v103
v_mul_f32 v40,v40,v104
v_mul_f32 v41,v41,v105
v_cvt_pk_bf16_f32 v110,v38,v39
v_cvt_pk_bf16_f32 v111,v40,v41
M 26,v[240:243],66
L 3
buffer_store_dwordx2 v[110:111],v10,s[36:39],s67 offen offset:0 sc0 sc1
M 26,v[252:255],70
s_cbranch_scc0 Lg3_K1_%=
Lg3_F1_%=:
v_mfma_f32_16x16x32_bf16 v[38:41],a[8:11],v[42:45],0
s_add_u32 m0,s57,0
R 62,2,5120
v_add_f32_dpp v115,v114,v114 quad_perm:[1,0,3,2] row_mask:0xf bank_mask:0xf
M 38,a[20:23],46
buffer_load_dword v114,v11,s[36:39],s73 offen offset:0 sc1
v_mfma_f32_16x16x32_bf16 v[34:37],a[4:7],v[42:45],0
s_add_u32 s56,s55,0
v_add_f32_dpp v115,v115,v115 quad_perm:[2,3,0,1] row_mask:0xf bank_mask:0xf
v_fma_f32 v18,-v74,v82,v18
M 34,a[16:19],46
s_add_u32 s58,s58,s61
R 66,2,6144
v_fma_f32 v19,-v74,v83,v19
v_mfma_f32_16x16x32_bf16 v[30:33],a[0:3],v[42:45],0
D 0,56,0
M 30,a[12:15],46
L 3
v_add_f32_dpp v118,v115,v115 row_half_mirror row_mask:0xf bank_mask:0xf
v_fma_f32 v20,-v74,v84,v20
M 30,a[24:27],50
s_mov_b32 s67,s65
R 70,2,7168
v_fma_f32 v21,-v74,v85,v21
M 30,a[36:39],54
s_add_u32 s65,s65,s62
v_mov_b32_dpp v115,v118 row_ror:8 row_mask:0xf bank_mask:0xf
v_fma_f32 v115,-v118,v118,v115
M 34,a[28:31],50
s_add_u32 s54,s54,s46
v_max_f32 v115,s87,v115
v_mul_f32 v118,v17,v118
M 34,a[40:43],54
s_add_u32 s73,s73,s64
R 42,2,8192
v_mul_f32 v94,v18,v18
M 38,a[32:35],50
v_rsq_f32 v116,v115
M 38,a[44:47],54
L 3
D 0,56,1024
M 38,a[56:59],58
R 46,2,9216
v_fma_f32 v115,v15,v115,v16
M 38,a[68:71],62
v_mul_f32 v119,v116,v116
v_fma_f32 v115,v14,v119,v115
M 34,a[52:55],58
v_fma_f32 v118,v116,v115,v118
v_mul_f32 v95,v19,v19
M 34,a[64:67],62
R 50,2,10240
v_mul_f32 v96,v20,v20
M 30,a[48:51],58
ds_write_b32 v12,v118 offset:2048
M 30,a[60:63],62
L 4
v_mul_f32 v97,v21,v21
v_fma_f32 v94,v76,v94,v75
M 30,a[72:75],66
R 54,2,11264
v_fma_f32 v95,v76,v95,v75
M 30,a[84:87],70
v_fma_f32 v96,v76,v96,v75
v_fma_f32 v97,v76,v97,v75
M 34,a[76:79],66
v_mul_f32 v94,v18,v94
v_mul_f32 v95,v19,v95
M 34,a[88:91],70
R 58,2,12288
v_mul_f32 v96,v20,v96
M 38,a[80:83],66
D 0,56,2048
M 38,a[92:95],70
L 4
v_exp_f32 v94,v94
M 38,a[104:107],42
R 62,2,13312
v_mul_f32 v97,v21,v97
M 38,a[116:119],46
v_exp_f32 v95,v95
M 34,a[100:103],42
v_exp_f32 v96,v96
M 34,a[112:115],46
R 66,2,14336
v_fma_f32 v94,v94,v77,v77
M 30,a[96:99],42
v_exp_f32 v97,v97
M 30,a[108:111],46
L 3
v_rcp_f32 v94,v94
M 30,a[120:123],50
R 70,2,15360
v_fma_f32 v95,v95,v77,v77
M 30,a[132:135],54
s_waitcnt vmcnt(7)
D 0,56,3072
M 34,a[124:127],50
s_barrier
v_rcp_f32 v95,v95
M 34,a[136:139],54
s_add_u32 m0,s59,1024
R 42,2,16384
v_fma_f32 v96,v96,v77,v77
M 38,a[128:131],50
s_add_u32 s56,s55,16384
v_rcp_f32 v96,v96
M 38,a[140:143],54
L 3
v_fma_f32 v97,v97,v77,v77
v_mul_f32 v18,v18,v94
M 38,a[152:155],58
s_add_u32 s55,s55,s60
R 46,2,17408
v_mul_f32 v19,v19,v95
M 38,a[164:167],62
v_rcp_f32 v97,v97
M 34,a[148:151],58
v_mul_f32 v20,v20,v96
v_mul_f32 v21,v21,v97
M 34,a[160:163],62
R 50,2,18432
v_cvt_pk_bf16_f32 v106,v18,v19
M 30,a[144:147],58
buffer_load_dword v8,s[36:39],s58 offen offset:0 sc1 lds
M 30,a[156:159],62
L 3
v_cvt_pk_bf16_f32 v107,v20,v21
v_fma_f32 v22,-v74,v86,v22
M 30,a[168:171],66
s_add_u32 m0,s57,16384
R 54,2,19456
v_fma_f32 v23,-v74,v87,v23
M 30,a[180:183],70
s_sub_u32 s57,s84,s57
v_fma_f32 v24,-v74,v88,v24
v_fma_f32 v25,-v74,v89,v25
M 34,a[172:175],66
v_mul_f32 v98,v22,v22
v_mul_f32 v99,v23,v23
M 34,a[184:187],70
R 58,2,20480
v_mul_f32 v100,v24,v24
M 38,a[176:179],66
D 0,56,0
M 38,a[188:191],70
L 3
v_mul_f32 v101,v25,v25
v_fma_f32 v98,v76,v98,v75
M 38,a[200:203],42
R 62,2,21504
v_fma_f32 v99,v76,v99,v75
M 38,a[212:215],46
v_fma_f32 v100,v76,v100,v75
v_fma_f32 v101,v76,v101,v75
M 34,a[196:199],42
v_mul_f32 v98,v22,v98
v_mul_f32 v99,v23,v99
M 34,a[208:211],46
R 66,2,22528
v_mul_f32 v100,v24,v100
M 30,a[192:195],42
v_exp_f32 v98,v98
M 30,a[204:207],46
L 3
v_exp_f32 v99,v99
M 30,a[216:219],50
R 70,2,23552
v_mul_f32 v101,v25,v101
M 30,a[228:231],54
D 0,56,1024
M 34,a[220:223],50
v_exp_f32 v100,v100
M 34,a[232:235],54
R 42,2,24576
v_fma_f32 v98,v98,v77,v77
M 38,a[224:227],50
v_exp_f32 v101,v101
M 38,a[236:239],54
L 3
v_rcp_f32 v98,v98
M 38,a[248:251],58
R 46,2,25600
v_fma_f32 v99,v99,v77,v77
M 38,v[132:135],62
v_rcp_f32 v99,v99
M 34,a[244:247],58
v_fma_f32 v100,v100,v77,v77
v_fma_f32 v101,v101,v77,v77
M 34,v[128:131],62
R 50,2,26624
v_mul_f32 v22,v22,v98
M 30,a[240:243],58
v_rcp_f32 v100,v100
M 30,a[252:255],62
L 3
v_rcp_f32 v101,v101
M 30,v[136:139],66
R 54,2,27648
v_mul_f32 v23,v23,v99
M 30,v[148:151],70
D 0,56,2048
M 34,v[140:143],66
v_mul_f32 v24,v24,v100
v_mul_f32 v25,v25,v101
M 34,v[152:155],70
R 58,2,28672
v_cvt_pk_bf16_f32 v108,v22,v23
M 38,v[144:147],66
v_cvt_pk_bf16_f32 v109,v24,v25
v_fma_f32 v26,-v74,v90,v26
M 38,v[156:159],70
L 3
v_fma_f32 v27,-v74,v91,v27
v_fma_f32 v28,-v74,v92,v28
M 38,v[168:171],42
R 62,2,29696
v_fma_f32 v29,-v74,v93,v29
M 38,v[180:183],46
v_mul_f32 v102,v26,v26
v_mul_f32 v103,v27,v27
M 34,v[164:167],42
v_mul_f32 v104,v28,v28
v_mul_f32 v105,v29,v29
M 34,v[176:179],46
R 66,2,30720
v_fma_f32 v102,v76,v102,v75
M 30,v[160:163],42
v_fma_f32 v103,v76,v103,v75
v_fma_f32 v104,v76,v104,v75
M 30,v[172:175],46
L 3
D 0,56,3072
M 30,v[184:187],50
s_nop 0
R 70,2,31744
v_fma_f32 v105,v76,v105,v75
M 30,v[196:199],54
s_waitcnt vmcnt(4)
v_mul_f32 v102,v26,v102
v_mul_f32 v103,v27,v103
M 34,v[188:191],50
s_barrier
buffer_store_dword v4,v3,s[36:39],s70 offen offset:0 sc0 sc1
M 34,v[200:203],54
s_mov_b32 s70,s69
R 42,1,0
ds_read_b32 v7,v6 offset:1024
M 38,v[192:195],50
s_mov_b32 s69,s68
R 78,13,2048
v_mul_f32 v104,v28,v104
M 38,v[204:207],54
L 5
v_exp_f32 v102,v102
M 38,v[216:219],58
s_add_u32 s68,s68,s63
R 46,1,1024
v_mul_f32 v105,v29,v105
M 38,v[228:231],62
v_exp_f32 v103,v103
M 34,v[212:215],58
buffer_store_dwordx4 v[106:109],v0,s[36:39],s66 offen offset:0 sc0 sc1
v_fma_f32 v102,v102,v77,v77
M 34,v[224:227],62
L 2
R 50,1,2048
v_fma_f32 v103,v103,v77,v77
M 30,v[208:211],58
v_exp_f32 v104,v104
M 30,v[220:223],62
s_nop 0
v_exp_f32 v105,v105
M 30,v[232:235],66
R 54,1,3072
v_fma_f32 v104,v104,v77,v77
M 30,v[244:247],70
v_fma_f32 v105,v105,v77,v77
v_cmp_eq_u32 vcc,s53,v7
M 34,v[236:239],66
v_rcp_f32 v102,v102
v_rcp_f32 v103,v103
v_rcp_f32 v104,v104
v_rcp_f32 v105,v105
M 34,v[248:251],70
s_cmp_eq_u32 vcc_lo,-1
R 58,1,4096
v_mul_f32 v26,v26,v102
v_mul_f32 v27,v27,v103
v_mul_f32 v28,v28,v104
v_mul_f32 v29,v29,v105
v_cvt_pk_bf16_f32 v110,v26,v27
v_cvt_pk_bf16_f32 v111,v28,v29
)"
R"(M 38,v[240:243],66
L 3
buffer_store_dwordx2 v[110:111],v10,s[36:39],s66 offen offset:0 sc0 sc1
M 38,v[252:255],70
s_cbranch_scc1 Lg3_F0_%=
Lg3_K0_%=:
Lg3_L0_%=:
v_mfma_f32_16x16x32_bf16 v[26:29],a[8:11],v[42:45],0
s_add_u32 m0,s59,3072
R 62,1,5120
v_add_f32_dpp v115,v114,v114 quad_perm:[1,0,3,2] row_mask:0xf bank_mask:0xf
M 26,a[20:23],46
buffer_load_dword v8,s[36:39],s58 offen offset:0 sc1 lds
v_mfma_f32_16x16x32_bf16 v[22:25],a[4:7],v[42:45],0
s_mov_b32 s66,s65
v_add_f32_dpp v115,v115,v115 quad_perm:[2,3,0,1] row_mask:0xf bank_mask:0xf
v_fma_f32 v30,-v78,v82,v30
M 22,a[16:19],46
s_add_u32 s65,s65,s62
R 66,1,6144
v_fma_f32 v31,-v78,v83,v31
v_mfma_f32_16x16x32_bf16 v[18:21],a[0:3],v[42:45],0
v_add_f32_dpp v118,v115,v115 row_half_mirror row_mask:0xf bank_mask:0xf
v_fma_f32 v32,-v78,v84,v32
v_fma_f32 v33,-v78,v85,v33
M 18,a[12:15],46
L 3
v_mul_f32 v94,v30,v30
v_mul_f32 v95,v31,v31
M 18,a[24:27],50
R 70,1,7168
v_mov_b32_dpp v115,v118 row_ror:8 row_mask:0xf bank_mask:0xf
M 18,a[36:39],54
v_fma_f32 v115,-v118,v118,v115
v_max_f32 v115,s87,v115
M 22,a[28:31],50
v_rsq_f32 v116,v115
M 22,a[40:43],54
R 42,1,8192
v_fma_f32 v115,v15,v115,v16
v_mul_f32 v118,v17,v118
M 26,a[32:35],50
v_mul_f32 v119,v116,v116
v_fma_f32 v115,v14,v119,v115
M 26,a[44:47],54
L 3
v_fma_f32 v118,v116,v115,v118
v_mul_f32 v96,v32,v32
M 26,a[56:59],58
R 46,1,9216
v_mul_f32 v97,v33,v33
M 26,a[68:71],62
ds_write_b32 v12,v118 offset:0
M 22,a[52:55],58
v_fma_f32 v94,v80,v94,v79
v_fma_f32 v95,v80,v95,v79
v_fma_f32 v96,v80,v96,v79
M 22,a[64:67],62
R 50,1,10240
v_fma_f32 v97,v80,v97,v79
M 18,a[48:51],58
v_mul_f32 v94,v30,v94
v_mul_f32 v95,v31,v95
M 18,a[60:63],62
L 4
v_exp_f32 v94,v94
M 18,a[72:75],66
R 54,1,11264
v_mul_f32 v96,v32,v96
M 18,a[84:87],70
v_exp_f32 v95,v95
v_mul_f32 v97,v33,v97
M 22,a[76:79],66
v_exp_f32 v96,v96
M 22,a[88:91],70
R 58,1,12288
v_fma_f32 v94,v94,v81,v81
M 26,a[80:83],66
v_exp_f32 v97,v97
M 26,a[92:95],70
L 4
v_rcp_f32 v94,v94
M 26,a[104:107],42
R 62,1,13312
v_fma_f32 v95,v95,v81,v81
v_fma_f32 v96,v96,v81,v81
M 26,a[116:119],46
v_rcp_f32 v95,v95
M 22,a[100:103],42
v_rcp_f32 v96,v96
M 22,a[112:115],46
R 66,1,14336
v_fma_f32 v97,v97,v81,v81
M 18,a[96:99],42
v_rcp_f32 v97,v97
M 18,a[108:111],46
L 3
v_mul_f32 v30,v30,v94
v_mul_f32 v31,v31,v95
v_mul_f32 v32,v32,v96
M 18,a[120:123],50
R 70,1,15360
v_mul_f32 v33,v33,v97
M 18,a[132:135],54
s_waitcnt vmcnt(0)
v_cvt_pk_bf16_f32 v106,v30,v31
v_cvt_pk_bf16_f32 v107,v32,v33
M 22,a[124:127],50
s_barrier
ds_read_b32 v7,v6 offset:3072
v_fma_f32 v34,-v78,v86,v34
M 22,a[136:139],54
R 42,1,16384
v_fma_f32 v35,-v78,v87,v35
M 26,a[128:131],50
buffer_store_dword v4,v3,s[36:39],s70 offen offset:0 sc0 sc1
v_fma_f32 v36,-v78,v88,v36
M 26,a[140:143],54
L 4
v_fma_f32 v37,-v78,v89,v37
v_mul_f32 v98,v34,v34
M 26,a[152:155],58
s_mov_b32 s70,0x7fff0000
R 46,1,17408
v_mul_f32 v99,v35,v35
M 26,a[164:167],62
v_mul_f32 v100,v36,v36
v_mul_f32 v101,v37,v37
M 22,a[148:151],58
L 2
v_cmp_eq_u32 vcc,s53,v7
v_fma_f32 v98,v80,v98,v79
M 22,a[160:163],62
R 50,1,18432
v_fma_f32 v99,v80,v99,v79
v_fma_f32 v100,v80,v100,v79
M 18,a[144:147],58
v_fma_f32 v101,v80,v101,v79
v_mul_f32 v98,v34,v98
M 18,a[156:159],62
s_cmp_eq_u32 vcc_lo,-1
s_nop 0
v_mul_f32 v99,v35,v99
s_cbranch_scc0 Lg3_X0_%=
M 18,a[168:171],66
s_add_u32 m0,s57,0
s_add_u32 s56,s55,0
s_add_u32 s58,s58,s61
R 54,1,19456
M 18,a[180:183],70
buffer_load_dword v114,v11,s[36:39],s73 offen offset:0 sc1
M 22,a[172:175],66
D 0,56,0
v_mul_f32 v100,v36,v100
M 22,a[184:187],70
s_add_u32 s54,s54,s46
R 58,1,20480
v_mul_f32 v101,v37,v101
M 26,a[176:179],66
s_add_u32 s73,s73,s64
v_exp_f32 v98,v98
M 26,a[188:191],70
L 3
v_exp_f32 v99,v99
M 26,a[200:203],42
R 62,1,21504
v_fma_f32 v98,v98,v81,v81
M 26,a[212:215],46
D 0,56,1024
v_fma_f32 v99,v99,v81,v81
M 22,a[196:199],42
v_exp_f32 v100,v100
M 22,a[208:211],46
R 66,1,22528
v_fma_f32 v100,v100,v81,v81
M 18,a[192:195],42
D 0,56,2048
M 18,a[204:207],46
L 3
v_exp_f32 v101,v101
M 18,a[216:219],50
R 70,1,23552
v_rcp_f32 v98,v98
M 18,a[228:231],54
v_rcp_f32 v99,v99
M 22,a[220:223],50
D 0,56,3072
M 22,a[232:235],54
s_add_u32 m0,s59,0
R 42,1,24576
v_fma_f32 v101,v101,v81,v81
M 26,a[224:227],50
buffer_load_dword v8,s[36:39],s58 offen offset:0 sc1 lds
M 26,a[236:239],54
L 3
v_rcp_f32 v100,v100
v_mul_f32 v34,v34,v98
M 26,a[248:251],58
s_add_u32 m0,s57,16384
R 46,1,25600
v_mul_f32 v35,v35,v99
M 26,v[132:135],62
s_add_u32 s56,s55,16384
v_rcp_f32 v101,v101
M 22,a[244:247],58
D 0,56,0
M 22,v[128:131],62
s_add_u32 s55,s55,s60
R 50,1,26624
v_mul_f32 v36,v36,v100
M 18,a[240:243],58
s_sub_u32 s57,s84,s57
v_mul_f32 v37,v37,v101
v_cvt_pk_bf16_f32 v108,v34,v35
v_cvt_pk_bf16_f32 v109,v36,v37
M 18,a[252:255],62
L 3
buffer_store_dwordx4 v[106:109],v0,s[36:39],s67 offen offset:0 sc0 sc1
v_fma_f32 v38,-v78,v90,v38
M 18,v[136:139],66
R 54,1,27648
v_fma_f32 v39,-v78,v91,v39
M 18,v[148:151],70
v_fma_f32 v40,-v78,v92,v40
v_fma_f32 v41,-v78,v93,v41
M 22,v[140:143],66
D 0,56,1024
M 22,v[152:155],70
R 58,1,28672
v_mul_f32 v102,v38,v38
v_mul_f32 v103,v39,v39
M 26,v[144:147],66
v_mul_f32 v104,v40,v40
v_mul_f32 v105,v41,v41
M 26,v[156:159],70
L 3
v_fma_f32 v102,v80,v102,v79
v_fma_f32 v103,v80,v103,v79
M 26,v[168:171],42
R 62,1,29696
v_fma_f32 v104,v80,v104,v79
M 26,v[180:183],46
D 0,56,2048
M 22,v[164:167],42
v_fma_f32 v105,v80,v105,v79
v_mul_f32 v102,v38,v102
v_mul_f32 v103,v39,v103
M 22,v[176:179],46
R 66,1,30720
v_mul_f32 v104,v40,v104
M 18,v[160:163],42
v_exp_f32 v102,v102
M 18,v[172:175],46
L 3
D 0,56,3072
M 18,v[184:187],50
s_nop 0
R 70,1,31744
v_mul_f32 v105,v41,v105
M 18,v[196:199],54
s_waitcnt vmcnt(5)
v_exp_f32 v103,v103
v_fma_f32 v102,v102,v81,v81
M 22,v[188:191],50
s_barrier
buffer_store_dword v4,v3,s[36:39],s70 offen offset:0 sc0 sc1
M 22,v[200:203],54
s_mov_b32 s70,s69
R 42,2,0
ds_read_b32 v7,v6 offset:0
M 26,v[192:195],50
s_mov_b32 s69,s68
R 74,13,0
v_fma_f32 v103,v103,v81,v81
M 26,v[204:207],54
L 5
v_exp_f32 v104,v104
M 26,v[216:219],58
s_add_u32 s68,s68,s63
R 46,2,1024
v_exp_f32 v105,v105
M 26,v[228:231],62
v_rcp_f32 v102,v102
M 22,v[212:215],58
v_rcp_f32 v103,v103
M 22,v[224:227],62
L 2
R 50,2,2048
v_fma_f32 v104,v104,v81,v81
M 18,v[208:211],58
v_rcp_f32 v104,v104
M 18,v[220:223],62
s_nop 0
v_fma_f32 v105,v105,v81,v81
v_rcp_f32 v105,v105
M 18,v[232:235],66
R 54,2,3072
v_mul_f32 v38,v38,v102
M 18,v[244:247],70
v_mul_f32 v39,v39,v103
v_mul_f32 v40,v40,v104
M 22,v[236:239],66
v_cmp_eq_u32 vcc,s53,v7
v_mul_f32 v41,v41,v105
M 22,v[248:251],70
R 58,2,4096
v_cvt_pk_bf16_f32 v110,v38,v39
v_cvt_pk_bf16_f32 v111,v40,v41
M 26,v[240:243],66
L 3
s_cmp_eq_u32 vcc_lo,-1
buffer_store_dwordx2 v[110:111],v10,s[36:39],s67 offen offset:0 sc0 sc1
M 26,v[252:255],70
buffer_store_dword v4,v3,s[36:39],s71 offen offset:0 sc0 sc1
s_cbranch_scc1 Lg3_F1_%=
s_branch Lg3_K1_%=
Lg3_X0_%=:
M 18,a[168:171],66
s_add_u32 m0,s59,0
R 54,1,19456
v_mul_f32 v100,v36,v100
M 18,a[180:183],70
buffer_load_dword v8,s[36:39],s58 offen offset:0 sc1 lds
M 22,a[172:175],66
v_exp_f32 v98,v98
M 22,a[184:187],70
R 58,1,20480
v_mul_f32 v101,v37,v101
M 26,a[176:179],66
v_exp_f32 v99,v99
M 26,a[188:191],70
L 3
v_exp_f32 v100,v100
M 26,a[200:203],42
R 62,1,21504
v_fma_f32 v98,v98,v81,v81
M 26,a[212:215],46
v_exp_f32 v101,v101
M 22,a[196:199],42
v_rcp_f32 v98,v98
M 22,a[208:211],46
R 66,1,22528
v_fma_f32 v99,v99,v81,v81
M 18,a[192:195],42
v_rcp_f32 v99,v99
M 18,a[204:207],46
L 3
v_fma_f32 v100,v100,v81,v81
v_fma_f32 v101,v101,v81,v81
M 18,a[216:219],50
R 70,1,23552
v_mul_f32 v34,v34,v98
M 18,a[228:231],54
v_rcp_f32 v100,v100
M 22,a[220:223],50
v_rcp_f32 v101,v101
M 22,a[232:235],54
R 42,1,24576
v_mul_f32 v35,v35,v99
M 26,a[224:227],50
v_mul_f32 v36,v36,v100
v_mul_f32 v37,v37,v101
M 26,a[236:239],54
L 3
v_cvt_pk_bf16_f32 v108,v34,v35
v_cvt_pk_bf16_f32 v109,v36,v37
M 26,a[248:251],58
R 46,1,25600
v_fma_f32 v38,-v78,v90,v38
M 26,v[132:135],62
buffer_store_dwordx4 v[106:109],v0,s[36:39],s67 offen offset:0 sc0 sc1
v_fma_f32 v39,-v78,v91,v39
M 22,a[244:247],58
v_fma_f32 v40,-v78,v92,v40
v_fma_f32 v41,-v78,v93,v41
M 22,v[128:131],62
R 50,1,26624
v_mul_f32 v102,v38,v38
M 18,a[240:243],58
v_mul_f32 v103,v39,v39
v_mul_f32 v104,v40,v40
M 18,a[252:255],62
L 3
v_mul_f32 v105,v41,v41
v_fma_f32 v102,v80,v102,v79
M 18,v[136:139],66
R 54,1,27648
v_fma_f32 v103,v80,v103,v79
M 18,v[148:151],70
v_fma_f32 v104,v80,v104,v79
v_fma_f32 v105,v80,v105,v79
M 22,v[140:143],66
v_mul_f32 v102,v38,v102
v_mul_f32 v103,v39,v103
M 22,v[152:155],70
R 58,1,28672
v_mul_f32 v104,v40,v104
M 26,v[144:147],66
v_exp_f32 v102,v102
M 26,v[156:159],70
L 3
v_exp_f32 v103,v103
M 26,v[168:171],42
R 62,1,29696
v_mul_f32 v105,v41,v105
M 26,v[180:183],46
v_exp_f32 v104,v104
M 22,v[164:167],42
v_exp_f32 v105,v105
M 22,v[176:179],46
R 66,1,30720
v_fma_f32 v102,v102,v81,v81
M 18,v[160:163],42
v_rcp_f32 v102,v102
M 18,v[172:175],46
L 3
v_fma_f32 v103,v103,v81,v81
v_fma_f32 v104,v104,v81,v81
M 18,v[184:187],50
s_nop 0
R 70,1,31744
v_fma_f32 v105,v105,v81,v81
M 18,v[196:199],54
s_waitcnt vmcnt(1)
v_rcp_f32 v103,v103
M 22,v[188:191],50
s_barrier
buffer_store_dword v4,v3,s[36:39],s70 offen offset:0 sc0 sc1
M 22,v[200:203],54
s_mov_b32 s70,s69
ds_read_b32 v7,v6 offset:0
R 74,13,0
M 26,v[192:195],50
s_mov_b32 s69,s68
v_rcp_f32 v104,v104
M 26,v[204:207],54
L 4
v_rcp_f32 v105,v105
M 26,v[216:219],58
s_add_u32 s68,s68,s63
v_mul_f32 v38,v38,v102
v_mul_f32 v39,v39,v103
M 26,v[228:231],62
v_mul_f32 v40,v40,v104
v_mul_f32 v41,v41,v105
M 22,v[212:215],58
v_cvt_pk_bf16_f32 v110,v38,v39
v_cvt_pk_bf16_f32 v111,v40,v41
M 22,v[224:227],62
L 1
buffer_store_dwordx2 v[110:111],v10,s[36:39],s67 offen offset:0 sc0 sc1
M 18,v[208:211],58
v_cmp_eq_u32 vcc,s53,v7
M 18,v[220:223],62
s_nop 0
M 18,v[232:235],66
M 18,v[244:247],70
M 22,v[236:239],66
M 22,v[248:251],70
M 26,v[240:243],66
M 26,v[252:255],70
s_mov_b32 s80,1
s_cmp_eq_u32 vcc_lo,-1
s_cbranch_scc0 Lg3_xn0_%=
buffer_load_dword v114,v11,s[36:39],s73 offen offset:0 sc1
L 0
v_fma_f32 v18,-v74,v82,v18
v_fma_f32 v19,-v74,v83,v19
v_fma_f32 v20,-v74,v84,v20
v_fma_f32 v21,-v74,v85,v21
v_mul_f32 v94,v18,v18
v_mul_f32 v95,v19,v19
v_mul_f32 v96,v20,v20
v_mul_f32 v97,v21,v21
s_add_u32 s73,s73,s64
s_add_u32 m0,s57,0
s_add_u32 s56,s55,0
D 0,56,0
v_fma_f32 v94,v76,v94,v75
v_fma_f32 v95,v76,v95,v75
v_fma_f32 v96,v76,v96,v75
v_fma_f32 v97,v76,v97,v75
v_mul_f32 v94,v18,v94
v_mul_f32 v95,v19,v95
v_mul_f32 v96,v20,v96
v_mul_f32 v97,v21,v97
D 0,56,1024
v_exp_f32 v94,v94
v_exp_f32 v95,v95
v_exp_f32 v96,v96
v_exp_f32 v97,v97
v_fma_f32 v94,v94,v77,v77
v_fma_f32 v95,v95,v77,v77
v_fma_f32 v96,v96,v77,v77
v_fma_f32 v97,v97,v77,v77
D 0,56,2048
v_rcp_f32 v94,v94
v_rcp_f32 v95,v95
v_rcp_f32 v96,v96
v_rcp_f32 v97,v97
)"
R"(v_mul_f32 v18,v18,v94
v_mul_f32 v19,v19,v95
v_mul_f32 v20,v20,v96
v_mul_f32 v21,v21,v97
D 0,56,3072
v_cvt_pk_bf16_f32 v106,v18,v19
v_cvt_pk_bf16_f32 v107,v20,v21
v_fma_f32 v22,-v74,v86,v22
v_fma_f32 v23,-v74,v87,v23
v_fma_f32 v24,-v74,v88,v24
v_fma_f32 v25,-v74,v89,v25
v_mul_f32 v98,v22,v22
v_mul_f32 v99,v23,v23
s_add_u32 s58,s58,s61
s_add_u32 m0,s57,16384
s_add_u32 s56,s55,16384
D 0,56,0
v_mul_f32 v100,v24,v24
v_mul_f32 v101,v25,v25
v_fma_f32 v98,v76,v98,v75
v_fma_f32 v99,v76,v99,v75
v_fma_f32 v100,v76,v100,v75
v_fma_f32 v101,v76,v101,v75
v_mul_f32 v98,v22,v98
v_mul_f32 v99,v23,v99
D 0,56,1024
v_mul_f32 v100,v24,v100
v_mul_f32 v101,v25,v101
v_exp_f32 v98,v98
v_exp_f32 v99,v99
v_exp_f32 v100,v100
v_exp_f32 v101,v101
v_fma_f32 v98,v98,v77,v77
v_fma_f32 v99,v99,v77,v77
D 0,56,2048
v_fma_f32 v100,v100,v77,v77
v_fma_f32 v101,v101,v77,v77
v_rcp_f32 v98,v98
v_rcp_f32 v99,v99
v_rcp_f32 v100,v100
v_rcp_f32 v101,v101
v_mul_f32 v22,v22,v98
v_mul_f32 v23,v23,v99
D 0,56,3072
v_mul_f32 v24,v24,v100
v_mul_f32 v25,v25,v101
v_cvt_pk_bf16_f32 v108,v22,v23
v_cvt_pk_bf16_f32 v109,v24,v25
buffer_store_dwordx4 v[106:109],v0,s[36:39],s66 offen offset:0 sc0 sc1
v_fma_f32 v26,-v74,v90,v26
v_fma_f32 v27,-v74,v91,v27
v_fma_f32 v28,-v74,v92,v28
v_fma_f32 v29,-v74,v93,v29
s_add_u32 s55,s55,s60
s_sub_u32 s57,s84,s57
s_add_u32 s54,s54,s46
v_mul_f32 v102,v26,v26
v_mul_f32 v103,v27,v27
v_mul_f32 v104,v28,v28
v_mul_f32 v105,v29,v29
v_fma_f32 v102,v76,v102,v75
v_fma_f32 v103,v76,v103,v75
v_fma_f32 v104,v76,v104,v75
v_fma_f32 v105,v76,v105,v75
v_mul_f32 v102,v26,v102
v_mul_f32 v103,v27,v103
v_mul_f32 v104,v28,v104
v_mul_f32 v105,v29,v105
v_exp_f32 v102,v102
v_exp_f32 v103,v103
v_exp_f32 v104,v104
v_exp_f32 v105,v105
v_fma_f32 v102,v102,v77,v77
v_fma_f32 v103,v103,v77,v77
v_fma_f32 v104,v104,v77,v77
v_fma_f32 v105,v105,v77,v77
v_rcp_f32 v102,v102
v_rcp_f32 v103,v103
v_rcp_f32 v104,v104
v_rcp_f32 v105,v105
v_mul_f32 v26,v26,v102
v_mul_f32 v27,v27,v103
v_mul_f32 v28,v28,v104
v_mul_f32 v29,v29,v105
v_cvt_pk_bf16_f32 v110,v26,v27
v_cvt_pk_bf16_f32 v111,v28,v29
buffer_store_dwordx2 v[110:111],v10,s[36:39],s66 offen offset:0 sc0 sc1
s_mov_b32 s72,s69
s_mov_b32 s69,0x7fff0000
s_mov_b32 s66,0x7fff0000
s_mov_b32 s76,0x7fff0000
s_branch Lg3_gox_%=
Lg3_xn0_%=:
s_cmp_ge_u32 s54,256
s_cbranch_scc1 Lg3_fin0_%=
s_add_u32 m0,s59,4096
s_nop 0
buffer_load_dword v8,s[36:39],s58 offen offset:0 sc1 lds
buffer_store_dword v4,v3,s[36:39],s71 offen offset:0 sc0 sc1
s_add_u32 m0,s59,5120
s_nop 0
buffer_load_dword v8,s[36:39],s58 offen offset:0 sc1 lds
buffer_store_dword v4,v3,s[36:39],s71 offen offset:0 sc0 sc1
s_mov_b32 s83,0
L 0
v_fma_f32 v18,-v74,v82,v18
v_fma_f32 v19,-v74,v83,v19
v_fma_f32 v20,-v74,v84,v20
v_fma_f32 v21,-v74,v85,v21
v_mul_f32 v94,v18,v18
v_mul_f32 v95,v19,v19
v_mul_f32 v96,v20,v20
v_mul_f32 v97,v21,v21
v_fma_f32 v94,v76,v94,v75
v_fma_f32 v95,v76,v95,v75
v_fma_f32 v96,v76,v96,v75
v_fma_f32 v97,v76,v97,v75
v_mul_f32 v94,v18,v94
v_mul_f32 v95,v19,v95
v_mul_f32 v96,v20,v96
v_mul_f32 v97,v21,v97
v_exp_f32 v94,v94
v_exp_f32 v95,v95
v_exp_f32 v96,v96
v_exp_f32 v97,v97
v_fma_f32 v94,v94,v77,v77
v_fma_f32 v95,v95,v77,v77
v_fma_f32 v96,v96,v77,v77
v_fma_f32 v97,v97,v77,v77
v_rcp_f32 v94,v94
v_rcp_f32 v95,v95
v_rcp_f32 v96,v96
v_rcp_f32 v97,v97
v_mul_f32 v18,v18,v94
v_mul_f32 v19,v19,v95
v_mul_f32 v20,v20,v96
v_mul_f32 v21,v21,v97
v_cvt_pk_bf16_f32 v106,v18,v19
v_cvt_pk_bf16_f32 v107,v20,v21
v_fma_f32 v22,-v74,v86,v22
v_fma_f32 v23,-v74,v87,v23
v_fma_f32 v24,-v74,v88,v24
v_fma_f32 v25,-v74,v89,v25
v_mul_f32 v98,v22,v22
v_mul_f32 v99,v23,v23
v_mul_f32 v100,v24,v24
v_mul_f32 v101,v25,v25
v_fma_f32 v98,v76,v98,v75
v_fma_f32 v99,v76,v99,v75
v_fma_f32 v100,v76,v100,v75
v_fma_f32 v101,v76,v101,v75
v_mul_f32 v98,v22,v98
v_mul_f32 v99,v23,v99
v_mul_f32 v100,v24,v100
v_mul_f32 v101,v25,v101
v_exp_f32 v98,v98
v_exp_f32 v99,v99
v_exp_f32 v100,v100
v_exp_f32 v101,v101
v_fma_f32 v98,v98,v77,v77
v_fma_f32 v99,v99,v77,v77
v_fma_f32 v100,v100,v77,v77
v_fma_f32 v101,v101,v77,v77
v_rcp_f32 v98,v98
v_rcp_f32 v99,v99
v_rcp_f32 v100,v100
v_rcp_f32 v101,v101
v_mul_f32 v22,v22,v98
v_mul_f32 v23,v23,v99
v_mul_f32 v24,v24,v100
v_mul_f32 v25,v25,v101
v_cvt_pk_bf16_f32 v108,v22,v23
v_cvt_pk_bf16_f32 v109,v24,v25
buffer_store_dwordx4 v[106:109],v0,s[36:39],s66 offen offset:0 sc0 sc1
v_fma_f32 v26,-v74,v90,v26
v_fma_f32 v27,-v74,v91,v27
v_fma_f32 v28,-v74,v92,v28
v_fma_f32 v29,-v74,v93,v29
v_mul_f32 v102,v26,v26
v_mul_f32 v103,v27,v27
v_mul_f32 v104,v28,v28
v_mul_f32 v105,v29,v29
v_fma_f32 v102,v76,v102,v75
v_fma_f32 v103,v76,v103,v75
v_fma_f32 v104,v76,v104,v75
v_fma_f32 v105,v76,v105,v75
v_mul_f32 v102,v26,v102
v_mul_f32 v103,v27,v103
v_mul_f32 v104,v28,v104
v_mul_f32 v105,v29,v105
v_exp_f32 v102,v102
v_exp_f32 v103,v103
v_exp_f32 v104,v104
v_exp_f32 v105,v105
v_fma_f32 v102,v102,v77,v77
v_fma_f32 v103,v103,v77,v77
v_fma_f32 v104,v104,v77,v77
v_fma_f32 v105,v105,v77,v77
v_rcp_f32 v102,v102
v_rcp_f32 v103,v103
v_rcp_f32 v104,v104
v_rcp_f32 v105,v105
v_mul_f32 v26,v26,v102
v_mul_f32 v27,v27,v103
v_mul_f32 v28,v28,v104
v_mul_f32 v29,v29,v105
v_cvt_pk_bf16_f32 v110,v26,v27
v_cvt_pk_bf16_f32 v111,v28,v29
buffer_store_dwordx2 v[110:111],v10,s[36:39],s66 offen offset:0 sc0 sc1
s_mov_b32 s72,s69
s_mov_b32 s69,0x7fff0000
s_mov_b32 s66,0x7fff0000
s_mov_b32 s76,0x7fff0000
s_branch Lg3_poll_%=
Lg3_fin0_%=:
L 0
v_fma_f32 v18,-v74,v82,v18
v_fma_f32 v19,-v74,v83,v19
v_fma_f32 v20,-v74,v84,v20
v_fma_f32 v21,-v74,v85,v21
v_mul_f32 v94,v18,v18
v_mul_f32 v95,v19,v19
v_mul_f32 v96,v20,v20
v_mul_f32 v97,v21,v21
v_fma_f32 v94,v76,v94,v75
v_fma_f32 v95,v76,v95,v75
v_fma_f32 v96,v76,v96,v75
v_fma_f32 v97,v76,v97,v75
v_mul_f32 v94,v18,v94
v_mul_f32 v95,v19,v95
v_mul_f32 v96,v20,v96
v_mul_f32 v97,v21,v97
v_exp_f32 v94,v94
v_exp_f32 v95,v95
v_exp_f32 v96,v96
v_exp_f32 v97,v97
v_fma_f32 v94,v94,v77,v77
v_fma_f32 v95,v95,v77,v77
v_fma_f32 v96,v96,v77,v77
v_fma_f32 v97,v97,v77,v77
v_rcp_f32 v94,v94
v_rcp_f32 v95,v95
v_rcp_f32 v96,v96
v_rcp_f32 v97,v97
v_mul_f32 v18,v18,v94
v_mul_f32 v19,v19,v95
v_mul_f32 v20,v20,v96
v_mul_f32 v21,v21,v97
v_cvt_pk_bf16_f32 v106,v18,v19
v_cvt_pk_bf16_f32 v107,v20,v21
v_fma_f32 v22,-v74,v86,v22
v_fma_f32 v23,-v74,v87,v23
v_fma_f32 v24,-v74,v88,v24
v_fma_f32 v25,-v74,v89,v25
v_mul_f32 v98,v22,v22
v_mul_f32 v99,v23,v23
v_mul_f32 v100,v24,v24
v_mul_f32 v101,v25,v25
v_fma_f32 v98,v76,v98,v75
v_fma_f32 v99,v76,v99,v75
v_fma_f32 v100,v76,v100,v75
v_fma_f32 v101,v76,v101,v75
v_mul_f32 v98,v22,v98
v_mul_f32 v99,v23,v99
v_mul_f32 v100,v24,v100
v_mul_f32 v101,v25,v101
v_exp_f32 v98,v98
v_exp_f32 v99,v99
v_exp_f32 v100,v100
v_exp_f32 v101,v101
v_fma_f32 v98,v98,v77,v77
v_fma_f32 v99,v99,v77,v77
v_fma_f32 v100,v100,v77,v77
v_fma_f32 v101,v101,v77,v77
v_rcp_f32 v98,v98
v_rcp_f32 v99,v99
v_rcp_f32 v100,v100
v_rcp_f32 v101,v101
v_mul_f32 v22,v22,v98
v_mul_f32 v23,v23,v99
v_mul_f32 v24,v24,v100
v_mul_f32 v25,v25,v101
v_cvt_pk_bf16_f32 v108,v22,v23
v_cvt_pk_bf16_f32 v109,v24,v25
buffer_store_dwordx4 v[106:109],v0,s[36:39],s66 offen offset:0 sc0 sc1
v_fma_f32 v26,-v74,v90,v26
v_fma_f32 v27,-v74,v91,v27
v_fma_f32 v28,-v74,v92,v28
v_fma_f32 v29,-v74,v93,v29
v_mul_f32 v102,v26,v26
v_mul_f32 v103,v27,v27
v_mul_f32 v104,v28,v28
v_mul_f32 v105,v29,v29
v_fma_f32 v102,v76,v102,v75
v_fma_f32 v103,v76,v103,v75
v_fma_f32 v104,v76,v104,v75
v_fma_f32 v105,v76,v105,v75
v_mul_f32 v102,v26,v102
v_mul_f32 v103,v27,v103
v_mul_f32 v104,v28,v104
v_mul_f32 v105,v29,v105
v_exp_f32 v102,v102
v_exp_f32 v103,v103
v_exp_f32 v104,v104
v_exp_f32 v105,v105
v_fma_f32 v102,v102,v77,v77
v_fma_f32 v103,v103,v77,v77
v_fma_f32 v104,v104,v77,v77
v_fma_f32 v105,v105,v77,v77
v_rcp_f32 v102,v102
v_rcp_f32 v103,v103
v_rcp_f32 v104,v104
v_rcp_f32 v105,v105
v_mul_f32 v26,v26,v102
v_mul_f32 v27,v27,v103
v_mul_f32 v28,v28,v104
v_mul_f32 v29,v29,v105
v_cvt_pk_bf16_f32 v110,v26,v27
v_cvt_pk_bf16_f32 v111,v28,v29
buffer_store_dwordx2 v[110:111],v10,s[36:39],s66 offen offset:0 sc0 sc1
s_mov_b32 s72,s69
s_mov_b32 s69,0x7fff0000
s_mov_b32 s66,0x7fff0000
s_mov_b32 s76,0x7fff0000
s_branch Lg3_done_%=
Lg3_K1_%=:
Lg3_L1_%=:
v_mfma_f32_16x16x32_bf16 v[38:41],a[8:11],v[42:45],0
s_add_u32 m0,s59,3072
R 62,2,5120
v_add_f32_dpp v115,v114,v114 quad_perm:[1,0,3,2] row_mask:0xf bank_mask:0xf
M 38,a[20:23],46
buffer_load_dword v8,s[36:39],s58 offen offset:0 sc1 lds
v_mfma_f32_16x16x32_bf16 v[34:37],a[4:7],v[42:45],0
s_mov_b32 s67,s65
v_add_f32_dpp v115,v115,v115 quad_perm:[2,3,0,1] row_mask:0xf bank_mask:0xf
v_fma_f32 v18,-v74,v82,v18
M 34,a[16:19],46
s_add_u32 s65,s65,s62
R 66,2,6144
v_fma_f32 v19,-v74,v83,v19
v_mfma_f32_16x16x32_bf16 v[30:33],a[0:3],v[42:45],0
v_add_f32_dpp v118,v115,v115 row_half_mirror row_mask:0xf bank_mask:0xf
v_fma_f32 v20,-v74,v84,v20
v_fma_f32 v21,-v74,v85,v21
M 30,a[12:15],46
L 3
v_mul_f32 v94,v18,v18
v_mul_f32 v95,v19,v19
M 30,a[24:27],50
R 70,2,7168
v_mov_b32_dpp v115,v118 row_ror:8 row_mask:0xf bank_mask:0xf
M 30,a[36:39],54
v_fma_f32 v115,-v118,v118,v115
v_max_f32 v115,s87,v115
M 34,a[28:31],50
v_rsq_f32 v116,v115
M 34,a[40:43],54
R 42,2,8192
v_fma_f32 v115,v15,v115,v16
v_mul_f32 v118,v17,v118
M 38,a[32:35],50
v_mul_f32 v119,v116,v116
v_fma_f32 v115,v14,v119,v115
M 38,a[44:47],54
L 3
v_fma_f32 v118,v116,v115,v118
v_mul_f32 v96,v20,v20
M 38,a[56:59],58
R 46,2,9216
v_mul_f32 v97,v21,v21
M 38,a[68:71],62
ds_write_b32 v12,v118 offset:2048
M 34,a[52:55],58
v_fma_f32 v94,v76,v94,v75
v_fma_f32 v95,v76,v95,v75
v_fma_f32 v96,v76,v96,v75
M 34,a[64:67],62
R 50,2,10240
v_fma_f32 v97,v76,v97,v75
M 30,a[48:51],58
v_mul_f32 v94,v18,v94
v_mul_f32 v95,v19,v95
M 30,a[60:63],62
L 4
v_exp_f32 v94,v94
M 30,a[72:75],66
R 54,2,11264
v_mul_f32 v96,v20,v96
M 30,a[84:87],70
v_exp_f32 v95,v95
v_mul_f32 v97,v21,v97
M 34,a[76:79],66
v_exp_f32 v96,v96
M 34,a[88:91],70
R 58,2,12288
v_fma_f32 v94,v94,v77,v77
M 38,a[80:83],66
v_exp_f32 v97,v97
M 38,a[92:95],70
L 4
v_rcp_f32 v94,v94
M 38,a[104:107],42
R 62,2,13312
v_fma_f32 v95,v95,v77,v77
v_fma_f32 v96,v96,v77,v77
M 38,a[116:119],46
v_rcp_f32 v95,v95
M 34,a[100:103],42
v_rcp_f32 v96,v96
M 34,a[112:115],46
R 66,2,14336
v_fma_f32 v97,v97,v77,v77
M 30,a[96:99],42
v_rcp_f32 v97,v97
M 30,a[108:111],46
L 3
v_mul_f32 v18,v18,v94
v_mul_f32 v19,v19,v95
v_mul_f32 v20,v20,v96
M 30,a[120:123],50
R 70,2,15360
v_mul_f32 v21,v21,v97
M 30,a[132:135],54
s_waitcnt vmcnt(0)
v_cvt_pk_bf16_f32 v106,v18,v19
v_cvt_pk_bf16_f32 v107,v20,v21
M 34,a[124:127],50
s_barrier
ds_read_b32 v7,v6 offset:3072
v_fma_f32 v22,-v74,v86,v22
M 34,a[136:139],54
R 42,2,16384
v_fma_f32 v23,-v74,v87,v23
M 38,a[128:131],50
buffer_store_dword v4,v3,s[36:39],s70 offen offset:0 sc0 sc1
v_fma_f32 v24,-v74,v88,v24
M 38,a[140:143],54
L 4
v_fma_f32 v25,-v74,v89,v25
v_mul_f32 v98,v22,v22
M 38,a[152:155],58
s_mov_b32 s70,0x7fff0000
R 46,2,17408
v_mul_f32 v99,v23,v23
M 38,a[164:167],62
v_mul_f32 v100,v24,v24
v_mul_f32 v101,v25,v25
M 34,a[148:151],58
L 2
v_cmp_eq_u32 vcc,s53,v7
v_fma_f32 v98,v76,v98,v75
M 34,a[160:163],62
R 50,2,18432
v_fma_f32 v99,v76,v99,v75
v_fma_f32 v100,v76,v100,v75
M 30,a[144:147],58
v_fma_f32 v101,v76,v101,v75
v_mul_f32 v98,v22,v98
M 30,a[156:159],62
s_cmp_eq_u32 vcc_lo,-1
s_nop 0
v_mul_f32 v99,v23,v99
s_cbranch_scc0 Lg3_X1_%=
M 30,a[168:171],66
s_add_u32 m0,s57,0
s_add_u32 s56,s55,0
s_add_u32 s58,s58,s61
R 54,2,19456
M 30,a[180:183],70
buffer_load_dword v114,v11,s[36:39],s73 offen offset:0 sc1
M 34,a[172:175],66
D 0,56,0
v_mul_f32 v100,v24,v100
M 34,a[184:187],70
s_add_u32 s54,s54,s46
R 58,2,20480
v_mul_f32 v101,v25,v101
M 38,a[176:179],66
s_add_u32 s73,s73,s64
v_exp_f32 v98,v98
M 38,a[188:191],70
L 3
v_exp_f32 v99,v99
M 38,a[200:203],42
R 62,2,21504
v_fma_f32 v98,v98,v77,v77
M 38,a[212:215],46
D 0,56,1024
v_fma_f32 v99,v99,v77,v77
M 34,a[196:199],42
v_exp_f32 v100,v100
M 34,a[208:211],46
R 66,2,22528
v_fma_f32 v100,v100,v77,v77
M 30,a[192:195],42
D 0,56,2048
M 30,a[204:207],46
L 3
v_exp_f32 v101,v101
)"
R"(M 30,a[216:219],50
R 70,2,23552
v_rcp_f32 v98,v98
M 30,a[228:231],54
v_rcp_f32 v99,v99
M 34,a[220:223],50
D 0,56,3072
M 34,a[232:235],54
s_add_u32 m0,s59,1024
R 42,2,24576
v_fma_f32 v101,v101,v77,v77
M 38,a[224:227],50
buffer_load_dword v8,s[36:39],s58 offen offset:0 sc1 lds
M 38,a[236:239],54
L 3
v_rcp_f32 v100,v100
v_mul_f32 v22,v22,v98
M 38,a[248:251],58
s_add_u32 m0,s57,16384
R 46,2,25600
v_mul_f32 v23,v23,v99
M 38,v[132:135],62
s_add_u32 s56,s55,16384
v_rcp_f32 v101,v101
M 34,a[244:247],58
D 0,56,0
M 34,v[128:131],62
s_add_u32 s55,s55,s60
R 50,2,26624
v_mul_f32 v24,v24,v100
M 30,a[240:243],58
s_sub_u32 s57,s84,s57
v_mul_f32 v25,v25,v101
v_cvt_pk_bf16_f32 v108,v22,v23
v_cvt_pk_bf16_f32 v109,v24,v25
M 30,a[252:255],62
L 3
buffer_store_dwordx4 v[106:109],v0,s[36:39],s66 offen offset:0 sc0 sc1
v_fma_f32 v26,-v74,v90,v26
M 30,v[136:139],66
R 54,2,27648
v_fma_f32 v27,-v74,v91,v27
M 30,v[148:151],70
v_fma_f32 v28,-v74,v92,v28
v_fma_f32 v29,-v74,v93,v29
M 34,v[140:143],66
D 0,56,1024
M 34,v[152:155],70
R 58,2,28672
v_mul_f32 v102,v26,v26
v_mul_f32 v103,v27,v27
M 38,v[144:147],66
v_mul_f32 v104,v28,v28
v_mul_f32 v105,v29,v29
M 38,v[156:159],70
L 3
v_fma_f32 v102,v76,v102,v75
v_fma_f32 v103,v76,v103,v75
M 38,v[168:171],42
R 62,2,29696
v_fma_f32 v104,v76,v104,v75
M 38,v[180:183],46
D 0,56,2048
M 34,v[164:167],42
v_fma_f32 v105,v76,v105,v75
v_mul_f32 v102,v26,v102
v_mul_f32 v103,v27,v103
M 34,v[176:179],46
R 66,2,30720
v_mul_f32 v104,v28,v104
M 30,v[160:163],42
v_exp_f32 v102,v102
M 30,v[172:175],46
L 3
D 0,56,3072
M 30,v[184:187],50
s_nop 0
R 70,2,31744
v_mul_f32 v105,v29,v105
M 30,v[196:199],54
s_waitcnt vmcnt(5)
v_exp_f32 v103,v103
v_fma_f32 v102,v102,v77,v77
M 34,v[188:191],50
s_barrier
buffer_store_dword v4,v3,s[36:39],s70 offen offset:0 sc0 sc1
M 34,v[200:203],54
s_mov_b32 s70,s69
R 42,1,0
ds_read_b32 v7,v6 offset:1024
M 38,v[192:195],50
s_mov_b32 s69,s68
R 78,13,2048
v_fma_f32 v103,v103,v77,v77
M 38,v[204:207],54
L 5
v_exp_f32 v104,v104
M 38,v[216:219],58
s_add_u32 s68,s68,s63
R 46,1,1024
v_exp_f32 v105,v105
M 38,v[228:231],62
v_rcp_f32 v102,v102
M 34,v[212:215],58
v_rcp_f32 v103,v103
M 34,v[224:227],62
L 2
R 50,1,2048
v_fma_f32 v104,v104,v77,v77
M 30,v[208:211],58
v_rcp_f32 v104,v104
M 30,v[220:223],62
s_nop 0
v_fma_f32 v105,v105,v77,v77
v_rcp_f32 v105,v105
M 30,v[232:235],66
R 54,1,3072
v_mul_f32 v26,v26,v102
M 30,v[244:247],70
v_mul_f32 v27,v27,v103
v_mul_f32 v28,v28,v104
M 34,v[236:239],66
v_cmp_eq_u32 vcc,s53,v7
v_mul_f32 v29,v29,v105
M 34,v[248:251],70
R 58,1,4096
v_cvt_pk_bf16_f32 v110,v26,v27
v_cvt_pk_bf16_f32 v111,v28,v29
M 38,v[240:243],66
L 3
s_cmp_eq_u32 vcc_lo,-1
buffer_store_dwordx2 v[110:111],v10,s[36:39],s66 offen offset:0 sc0 sc1
M 38,v[252:255],70
buffer_store_dword v4,v3,s[36:39],s71 offen offset:0 sc0 sc1
s_cbranch_scc1 Lg3_F0_%=
s_branch Lg3_K0_%=
Lg3_X1_%=:
M 30,a[168:171],66
s_add_u32 m0,s59,1024
R 54,2,19456
v_mul_f32 v100,v24,v100
M 30,a[180:183],70
buffer_load_dword v8,s[36:39],s58 offen offset:0 sc1 lds
M 34,a[172:175],66
v_exp_f32 v98,v98
M 34,a[184:187],70
R 58,2,20480
v_mul_f32 v101,v25,v101
M 38,a[176:179],66
v_exp_f32 v99,v99
M 38,a[188:191],70
L 3
v_exp_f32 v100,v100
M 38,a[200:203],42
R 62,2,21504
v_fma_f32 v98,v98,v77,v77
M 38,a[212:215],46
v_exp_f32 v101,v101
M 34,a[196:199],42
v_rcp_f32 v98,v98
M 34,a[208:211],46
R 66,2,22528
v_fma_f32 v99,v99,v77,v77
M 30,a[192:195],42
v_rcp_f32 v99,v99
M 30,a[204:207],46
L 3
v_fma_f32 v100,v100,v77,v77
v_fma_f32 v101,v101,v77,v77
M 30,a[216:219],50
R 70,2,23552
v_mul_f32 v22,v22,v98
M 30,a[228:231],54
v_rcp_f32 v100,v100
M 34,a[220:223],50
v_rcp_f32 v101,v101
M 34,a[232:235],54
R 42,2,24576
v_mul_f32 v23,v23,v99
M 38,a[224:227],50
v_mul_f32 v24,v24,v100
v_mul_f32 v25,v25,v101
M 38,a[236:239],54
L 3
v_cvt_pk_bf16_f32 v108,v22,v23
v_cvt_pk_bf16_f32 v109,v24,v25
M 38,a[248:251],58
R 46,2,25600
v_fma_f32 v26,-v74,v90,v26
M 38,v[132:135],62
buffer_store_dwordx4 v[106:109],v0,s[36:39],s66 offen offset:0 sc0 sc1
v_fma_f32 v27,-v74,v91,v27
M 34,a[244:247],58
v_fma_f32 v28,-v74,v92,v28
v_fma_f32 v29,-v74,v93,v29
M 34,v[128:131],62
R 50,2,26624
v_mul_f32 v102,v26,v26
M 30,a[240:243],58
v_mul_f32 v103,v27,v27
v_mul_f32 v104,v28,v28
M 30,a[252:255],62
L 3
v_mul_f32 v105,v29,v29
v_fma_f32 v102,v76,v102,v75
M 30,v[136:139],66
R 54,2,27648
v_fma_f32 v103,v76,v103,v75
M 30,v[148:151],70
v_fma_f32 v104,v76,v104,v75
v_fma_f32 v105,v76,v105,v75
M 34,v[140:143],66
v_mul_f32 v102,v26,v102
v_mul_f32 v103,v27,v103
M 34,v[152:155],70
R 58,2,28672
v_mul_f32 v104,v28,v104
M 38,v[144:147],66
v_exp_f32 v102,v102
M 38,v[156:159],70
L 3
v_exp_f32 v103,v103
M 38,v[168:171],42
R 62,2,29696
v_mul_f32 v105,v29,v105
M 38,v[180:183],46
v_exp_f32 v104,v104
M 34,v[164:167],42
v_exp_f32 v105,v105
M 34,v[176:179],46
R 66,2,30720
v_fma_f32 v102,v102,v77,v77
M 30,v[160:163],42
v_rcp_f32 v102,v102
M 30,v[172:175],46
L 3
v_fma_f32 v103,v103,v77,v77
v_fma_f32 v104,v104,v77,v77
M 30,v[184:187],50
s_nop 0
R 70,2,31744
v_fma_f32 v105,v105,v77,v77
M 30,v[196:199],54
s_waitcnt vmcnt(1)
v_rcp_f32 v103,v103
M 34,v[188:191],50
s_barrier
buffer_store_dword v4,v3,s[36:39],s70 offen offset:0 sc0 sc1
M 34,v[200:203],54
s_mov_b32 s70,s69
ds_read_b32 v7,v6 offset:1024
R 78,13,2048
M 38,v[192:195],50
s_mov_b32 s69,s68
v_rcp_f32 v104,v104
M 38,v[204:207],54
L 4
v_rcp_f32 v105,v105
M 38,v[216:219],58
s_add_u32 s68,s68,s63
v_mul_f32 v26,v26,v102
v_mul_f32 v27,v27,v103
M 38,v[228:231],62
v_mul_f32 v28,v28,v104
v_mul_f32 v29,v29,v105
M 34,v[212:215],58
v_cvt_pk_bf16_f32 v110,v26,v27
v_cvt_pk_bf16_f32 v111,v28,v29
M 34,v[224:227],62
L 1
buffer_store_dwordx2 v[110:111],v10,s[36:39],s66 offen offset:0 sc0 sc1
M 30,v[208:211],58
v_cmp_eq_u32 vcc,s53,v7
M 30,v[220:223],62
s_nop 0
M 30,v[232:235],66
M 30,v[244:247],70
M 34,v[236:239],66
M 34,v[248:251],70
M 38,v[240:243],66
M 38,v[252:255],70
s_mov_b32 s80,0
s_cmp_eq_u32 vcc_lo,-1
s_cbranch_scc0 Lg3_xn1_%=
buffer_load_dword v114,v11,s[36:39],s73 offen offset:0 sc1
L 0
v_fma_f32 v30,-v78,v82,v30
v_fma_f32 v31,-v78,v83,v31
v_fma_f32 v32,-v78,v84,v32
v_fma_f32 v33,-v78,v85,v33
v_mul_f32 v94,v30,v30
v_mul_f32 v95,v31,v31
v_mul_f32 v96,v32,v32
v_mul_f32 v97,v33,v33
s_add_u32 s73,s73,s64
s_add_u32 m0,s57,0
s_add_u32 s56,s55,0
D 0,56,0
v_fma_f32 v94,v80,v94,v79
v_fma_f32 v95,v80,v95,v79
v_fma_f32 v96,v80,v96,v79
v_fma_f32 v97,v80,v97,v79
v_mul_f32 v94,v30,v94
v_mul_f32 v95,v31,v95
v_mul_f32 v96,v32,v96
v_mul_f32 v97,v33,v97
D 0,56,1024
v_exp_f32 v94,v94
v_exp_f32 v95,v95
v_exp_f32 v96,v96
v_exp_f32 v97,v97
v_fma_f32 v94,v94,v81,v81
v_fma_f32 v95,v95,v81,v81
v_fma_f32 v96,v96,v81,v81
v_fma_f32 v97,v97,v81,v81
D 0,56,2048
v_rcp_f32 v94,v94
v_rcp_f32 v95,v95
v_rcp_f32 v96,v96
v_rcp_f32 v97,v97
v_mul_f32 v30,v30,v94
v_mul_f32 v31,v31,v95
v_mul_f32 v32,v32,v96
v_mul_f32 v33,v33,v97
D 0,56,3072
v_cvt_pk_bf16_f32 v106,v30,v31
v_cvt_pk_bf16_f32 v107,v32,v33
v_fma_f32 v34,-v78,v86,v34
v_fma_f32 v35,-v78,v87,v35
v_fma_f32 v36,-v78,v88,v36
v_fma_f32 v37,-v78,v89,v37
v_mul_f32 v98,v34,v34
v_mul_f32 v99,v35,v35
s_add_u32 s58,s58,s61
s_add_u32 m0,s57,16384
s_add_u32 s56,s55,16384
D 0,56,0
v_mul_f32 v100,v36,v36
v_mul_f32 v101,v37,v37
v_fma_f32 v98,v80,v98,v79
v_fma_f32 v99,v80,v99,v79
v_fma_f32 v100,v80,v100,v79
v_fma_f32 v101,v80,v101,v79
v_mul_f32 v98,v34,v98
v_mul_f32 v99,v35,v99
D 0,56,1024
v_mul_f32 v100,v36,v100
v_mul_f32 v101,v37,v101
v_exp_f32 v98,v98
v_exp_f32 v99,v99
v_exp_f32 v100,v100
v_exp_f32 v101,v101
v_fma_f32 v98,v98,v81,v81
v_fma_f32 v99,v99,v81,v81
D 0,56,2048
v_fma_f32 v100,v100,v81,v81
v_fma_f32 v101,v101,v81,v81
v_rcp_f32 v98,v98
v_rcp_f32 v99,v99
v_rcp_f32 v100,v100
v_rcp_f32 v101,v101
v_mul_f32 v34,v34,v98
v_mul_f32 v35,v35,v99
D 0,56,3072
v_mul_f32 v36,v36,v100
v_mul_f32 v37,v37,v101
v_cvt_pk_bf16_f32 v108,v34,v35
v_cvt_pk_bf16_f32 v109,v36,v37
buffer_store_dwordx4 v[106:109],v0,s[36:39],s67 offen offset:0 sc0 sc1
v_fma_f32 v38,-v78,v90,v38
v_fma_f32 v39,-v78,v91,v39
v_fma_f32 v40,-v78,v92,v40
v_fma_f32 v41,-v78,v93,v41
s_add_u32 s55,s55,s60
s_sub_u32 s57,s84,s57
s_add_u32 s54,s54,s46
v_mul_f32 v102,v38,v38
v_mul_f32 v103,v39,v39
v_mul_f32 v104,v40,v40
v_mul_f32 v105,v41,v41
v_fma_f32 v102,v80,v102,v79
v_fma_f32 v103,v80,v103,v79
v_fma_f32 v104,v80,v104,v79
v_fma_f32 v105,v80,v105,v79
v_mul_f32 v102,v38,v102
v_mul_f32 v103,v39,v103
v_mul_f32 v104,v40,v104
v_mul_f32 v105,v41,v105
v_exp_f32 v102,v102
v_exp_f32 v103,v103
v_exp_f32 v104,v104
v_exp_f32 v105,v105
v_fma_f32 v102,v102,v81,v81
v_fma_f32 v103,v103,v81,v81
v_fma_f32 v104,v104,v81,v81
v_fma_f32 v105,v105,v81,v81
v_rcp_f32 v102,v102
v_rcp_f32 v103,v103
v_rcp_f32 v104,v104
v_rcp_f32 v105,v105
v_mul_f32 v38,v38,v102
v_mul_f32 v39,v39,v103
v_mul_f32 v40,v40,v104
v_mul_f32 v41,v41,v105
v_cvt_pk_bf16_f32 v110,v38,v39
v_cvt_pk_bf16_f32 v111,v40,v41
buffer_store_dwordx2 v[110:111],v10,s[36:39],s67 offen offset:0 sc0 sc1
s_mov_b32 s72,s69
s_mov_b32 s69,0x7fff0000
s_mov_b32 s67,0x7fff0000
s_mov_b32 s77,0x7fff0000
s_branch Lg3_gox_%=
Lg3_xn1_%=:
s_cmp_ge_u32 s54,256
s_cbranch_scc1 Lg3_fin1_%=
s_add_u32 m0,s59,4096
s_nop 0
buffer_load_dword v8,s[36:39],s58 offen offset:0 sc1 lds
buffer_store_dword v4,v3,s[36:39],s71 offen offset:0 sc0 sc1
s_add_u32 m0,s59,5120
s_nop 0
buffer_load_dword v8,s[36:39],s58 offen offset:0 sc1 lds
buffer_store_dword v4,v3,s[36:39],s71 offen offset:0 sc0 sc1
s_mov_b32 s83,0
L 0
v_fma_f32 v30,-v78,v82,v30
v_fma_f32 v31,-v78,v83,v31
v_fma_f32 v32,-v78,v84,v32
v_fma_f32 v33,-v78,v85,v33
v_mul_f32 v94,v30,v30
v_mul_f32 v95,v31,v31
v_mul_f32 v96,v32,v32
v_mul_f32 v97,v33,v33
v_fma_f32 v94,v80,v94,v79
v_fma_f32 v95,v80,v95,v79
v_fma_f32 v96,v80,v96,v79
v_fma_f32 v97,v80,v97,v79
v_mul_f32 v94,v30,v94
v_mul_f32 v95,v31,v95
v_mul_f32 v96,v32,v96
v_mul_f32 v97,v33,v97
v_exp_f32 v94,v94
v_exp_f32 v95,v95
v_exp_f32 v96,v96
v_exp_f32 v97,v97
v_fma_f32 v94,v94,v81,v81
v_fma_f32 v95,v95,v81,v81
v_fma_f32 v96,v96,v81,v81
v_fma_f32 v97,v97,v81,v81
v_rcp_f32 v94,v94
v_rcp_f32 v95,v95
v_rcp_f32 v96,v96
v_rcp_f32 v97,v97
v_mul_f32 v30,v30,v94
v_mul_f32 v31,v31,v95
v_mul_f32 v32,v32,v96
v_mul_f32 v33,v33,v97
v_cvt_pk_bf16_f32 v106,v30,v31
v_cvt_pk_bf16_f32 v107,v32,v33
v_fma_f32 v34,-v78,v86,v34
v_fma_f32 v35,-v78,v87,v35
v_fma_f32 v36,-v78,v88,v36
v_fma_f32 v37,-v78,v89,v37
v_mul_f32 v98,v34,v34
v_mul_f32 v99,v35,v35
v_mul_f32 v100,v36,v36
v_mul_f32 v101,v37,v37
v_fma_f32 v98,v80,v98,v79
v_fma_f32 v99,v80,v99,v79
v_fma_f32 v100,v80,v100,v79
v_fma_f32 v101,v80,v101,v79
v_mul_f32 v98,v34,v98
v_mul_f32 v99,v35,v99
v_mul_f32 v100,v36,v100
v_mul_f32 v101,v37,v101
v_exp_f32 v98,v98
v_exp_f32 v99,v99
v_exp_f32 v100,v100
v_exp_f32 v101,v101
v_fma_f32 v98,v98,v81,v81
v_fma_f32 v99,v99,v81,v81
v_fma_f32 v100,v100,v81,v81
v_fma_f32 v101,v101,v81,v81
v_rcp_f32 v98,v98
v_rcp_f32 v99,v99
v_rcp_f32 v100,v100
v_rcp_f32 v101,v101
v_mul_f32 v34,v34,v98
v_mul_f32 v35,v35,v99
v_mul_f32 v36,v36,v100
v_mul_f32 v37,v37,v101
v_cvt_pk_bf16_f32 v108,v34,v35
v_cvt_pk_bf16_f32 v109,v36,v37
buffer_store_dwordx4 v[106:109],v0,s[36:39],s67 offen offset:0 sc0 sc1
v_fma_f32 v38,-v78,v90,v38
v_fma_f32 v39,-v78,v91,v39
v_fma_f32 v40,-v78,v92,v40
v_fma_f32 v41,-v78,v93,v41
v_mul_f32 v102,v38,v38
v_mul_f32 v103,v39,v39
)"
R"(v_mul_f32 v104,v40,v40
v_mul_f32 v105,v41,v41
v_fma_f32 v102,v80,v102,v79
v_fma_f32 v103,v80,v103,v79
v_fma_f32 v104,v80,v104,v79
v_fma_f32 v105,v80,v105,v79
v_mul_f32 v102,v38,v102
v_mul_f32 v103,v39,v103
v_mul_f32 v104,v40,v104
v_mul_f32 v105,v41,v105
v_exp_f32 v102,v102
v_exp_f32 v103,v103
v_exp_f32 v104,v104
v_exp_f32 v105,v105
v_fma_f32 v102,v102,v81,v81
v_fma_f32 v103,v103,v81,v81
v_fma_f32 v104,v104,v81,v81
v_fma_f32 v105,v105,v81,v81
v_rcp_f32 v102,v102
v_rcp_f32 v103,v103
v_rcp_f32 v104,v104
v_rcp_f32 v105,v105
v_mul_f32 v38,v38,v102
v_mul_f32 v39,v39,v103
v_mul_f32 v40,v40,v104
v_mul_f32 v41,v41,v105
v_cvt_pk_bf16_f32 v110,v38,v39
v_cvt_pk_bf16_f32 v111,v40,v41
buffer_store_dwordx2 v[110:111],v10,s[36:39],s67 offen offset:0 sc0 sc1
s_mov_b32 s72,s69
s_mov_b32 s69,0x7fff0000
s_mov_b32 s67,0x7fff0000
s_mov_b32 s77,0x7fff0000
s_branch Lg3_poll_%=
Lg3_fin1_%=:
L 0
v_fma_f32 v30,-v78,v82,v30
v_fma_f32 v31,-v78,v83,v31
v_fma_f32 v32,-v78,v84,v32
v_fma_f32 v33,-v78,v85,v33
v_mul_f32 v94,v30,v30
v_mul_f32 v95,v31,v31
v_mul_f32 v96,v32,v32
v_mul_f32 v97,v33,v33
v_fma_f32 v94,v80,v94,v79
v_fma_f32 v95,v80,v95,v79
v_fma_f32 v96,v80,v96,v79
v_fma_f32 v97,v80,v97,v79
v_mul_f32 v94,v30,v94
v_mul_f32 v95,v31,v95
v_mul_f32 v96,v32,v96
v_mul_f32 v97,v33,v97
v_exp_f32 v94,v94
v_exp_f32 v95,v95
v_exp_f32 v96,v96
v_exp_f32 v97,v97
v_fma_f32 v94,v94,v81,v81
v_fma_f32 v95,v95,v81,v81
v_fma_f32 v96,v96,v81,v81
v_fma_f32 v97,v97,v81,v81
v_rcp_f32 v94,v94
v_rcp_f32 v95,v95
v_rcp_f32 v96,v96
v_rcp_f32 v97,v97
v_mul_f32 v30,v30,v94
v_mul_f32 v31,v31,v95
v_mul_f32 v32,v32,v96
v_mul_f32 v33,v33,v97
v_cvt_pk_bf16_f32 v106,v30,v31
v_cvt_pk_bf16_f32 v107,v32,v33
v_fma_f32 v34,-v78,v86,v34
v_fma_f32 v35,-v78,v87,v35
v_fma_f32 v36,-v78,v88,v36
v_fma_f32 v37,-v78,v89,v37
v_mul_f32 v98,v34,v34
v_mul_f32 v99,v35,v35
v_mul_f32 v100,v36,v36
v_mul_f32 v101,v37,v37
v_fma_f32 v98,v80,v98,v79
v_fma_f32 v99,v80,v99,v79
v_fma_f32 v100,v80,v100,v79
v_fma_f32 v101,v80,v101,v79
v_mul_f32 v98,v34,v98
v_mul_f32 v99,v35,v99
v_mul_f32 v100,v36,v100
v_mul_f32 v101,v37,v101
v_exp_f32 v98,v98
v_exp_f32 v99,v99
v_exp_f32 v100,v100
v_exp_f32 v101,v101
v_fma_f32 v98,v98,v81,v81
v_fma_f32 v99,v99,v81,v81
v_fma_f32 v100,v100,v81,v81
v_fma_f32 v101,v101,v81,v81
v_rcp_f32 v98,v98
v_rcp_f32 v99,v99
v_rcp_f32 v100,v100
v_rcp_f32 v101,v101
v_mul_f32 v34,v34,v98
v_mul_f32 v35,v35,v99
v_mul_f32 v36,v36,v100
v_mul_f32 v37,v37,v101
v_cvt_pk_bf16_f32 v108,v34,v35
v_cvt_pk_bf16_f32 v109,v36,v37
buffer_store_dwordx4 v[106:109],v0,s[36:39],s67 offen offset:0 sc0 sc1
v_fma_f32 v38,-v78,v90,v38
v_fma_f32 v39,-v78,v91,v39
v_fma_f32 v40,-v78,v92,v40
v_fma_f32 v41,-v78,v93,v41
v_mul_f32 v102,v38,v38
v_mul_f32 v103,v39,v39
v_mul_f32 v104,v40,v40
v_mul_f32 v105,v41,v41
v_fma_f32 v102,v80,v102,v79
v_fma_f32 v103,v80,v103,v79
v_fma_f32 v104,v80,v104,v79
v_fma_f32 v105,v80,v105,v79
v_mul_f32 v102,v38,v102
v_mul_f32 v103,v39,v103
v_mul_f32 v104,v40,v104
v_mul_f32 v105,v41,v105
v_exp_f32 v102,v102
v_exp_f32 v103,v103
v_exp_f32 v104,v104
v_exp_f32 v105,v105
v_fma_f32 v102,v102,v81,v81
v_fma_f32 v103,v103,v81,v81
v_fma_f32 v104,v104,v81,v81
v_fma_f32 v105,v105,v81,v81
v_rcp_f32 v102,v102
v_rcp_f32 v103,v103
v_rcp_f32 v104,v104
v_rcp_f32 v105,v105
v_mul_f32 v38,v38,v102
v_mul_f32 v39,v39,v103
v_mul_f32 v40,v40,v104
v_mul_f32 v41,v41,v105
v_cvt_pk_bf16_f32 v110,v38,v39
v_cvt_pk_bf16_f32 v111,v40,v41
buffer_store_dwordx2 v[110:111],v10,s[36:39],s67 offen offset:0 sc0 sc1
s_mov_b32 s72,s69
s_mov_b32 s69,0x7fff0000
s_mov_b32 s67,0x7fff0000
s_mov_b32 s77,0x7fff0000
s_branch Lg3_done_%=
Lg3_done_%=:
s_waitcnt vmcnt(2)
s_barrier
buffer_store_dword v4,v3,s[36:39],s70 offen offset:0 sc0 sc1
s_waitcnt vmcnt(1)
s_barrier
buffer_store_dword v4,v3,s[36:39],s72 offen offset:0 sc0 sc1
s_waitcnt vmcnt(0) lgkmcnt(0)
s_barrier
s_endpgm
Lcode_g3_e:
.purgem M
.purgem R
.purgem T
.purgem D
.purgem L
)"
  :: "s"(ws), "s"(__builtin_amdgcn_readfirstlane(sWave)), "s"(__builtin_amdgcn_readfirstlane(sG0)), "s"(__builtin_amdgcn_readfirstlane(sStep)), "s"(__builtin_amdgcn_readfirstlane(sOutOff)), "s"(__builtin_amdgcn_readfirstlane(sFoutOff)), "s"(__builtin_amdgcn_readfirstlane(sAuxOff)), "s"(__builtin_amdgcn_readfirstlane(sWw)), "s"(__builtin_amdgcn_readfirstlane(sBuf)), "s"(__builtin_amdgcn_readfirstlane(sExp)), "v"(vL16), "v"(vFlag), "v"(vSnapOff), "v"(vSnapRd), "v"(vWd0), "v"(vWd1), "v"(vWr0), "v"(vWr1), "v"(vWr2), "v"(vOutS), "v"(vAux0), "v"(vAux1), "v"(vAux2), "v"(vAux3), "v"(vAn), "v"(vAe), "s"(w3), "s"(out) : "v255", "v0", "v1", "v2", "v3", "v4", "v5", "v6", "v7", "v8", "v9", "v10", "v11", "v12", "v13", "v14", "v15", "v16", "v17", "v18", "v19", "v20", "v21", "v22", "v23", "v24", "v25", "v26", "v27", "v28", "v29", "v30", "v31", "v32", "v33", "v34", "v35", "v36", "v37", "v38", "v39", "v40", "v41", "v42", "v43", "v44", "v45", "v46", "v47", "v48", "v49", "v50", "v51", "v52", "v53", "v54", "v55", "v56", "v57", "v58", "v59", "v60", "v61", "v62", "v63", "v64", "v65", "v66", "v67", "v68", "v69", "v70", "v71", "v72", "v73", "v74", "v75", "v76", "v77", "v78", "v79", "v80", "v81", "v82", "v83", "v84", "v85", "v86", "v87", "v88", "v89", "v90", "v91", "v92", "v93", "v94", "v95", "v96", "v97", "v98", "v99", "v100", "v101", "v102", "v103", "v104", "v105", "v106", "v107", "v108", "v109", "v110", "v111", "v112", "v113", "v114", "v115", "v116", "v117", "v118", "v119", "v120", "v121", "v122", "v123", "v124", "v125", "v126", "v127", "v128", "v129", "v130", "v131", "v132", "v133", "v134", "v135", "v136", "v137", "v138", "v139", "v140", "v141", "v142", "v143", "v144", "v145", "v146", "v147", "v148", "v149", "v150", "v151", "v152", "v153", "v154", "v155", "v156", "v157", "v158", "v159", "v160", "v161", "v162", "v163", "v164", "v165", "v166", "v167", "v168", "v169", "v170", "v171", "v172", "v173", "v174", "v175", "v176", "v177", "v178", "v179", "v180", "v181", "v182", "v183", "v184", "v185", "v186", "v187", "v188", "v189", "v190", "v191", "v192", "v193", "v194", "v195", "v196", "v197", "v198", "v199", "v200", "v201", "v202", "v203", "v204", "v205", "v206", "v207", "v208", "v209", "v210", "v211", "v212", "v213", "v214", "v215", "v216", "v217", "v218", "v219", "v220", "v221", "v222", "v223", "v224", "v225", "v226", "v227", "v228", "v229", "v230", "v231", "a0", "a1", "a2", "a3", "a4", "a5", "a6", "a7", "a8", "a9", "a10", "a11", "a12", "a13", "a14", "a15", "a16", "a17", "a18", "a19", "a20", "a21", "a22", "a23", "a24", "a25", "a26", "a27", "a28", "a29", "a30", "a31", "a32", "a33", "a34", "a35", "a36", "a37", "a38", "a39", "a40", "a41", "a42", "a43", "a44", "a45", "a46", "a47", "a48", "a49", "a50", "a51", "a52", "a53", "a54", "a55", "a56", "a57", "a58", "a59", "a60", "a61", "a62", "a63", "a64", "a65", "a66", "a67", "a68", "a69", "a70", "a71", "a72", "a73", "a74", "a75", "a76", "a77", "a78", "a79", "a80", "a81", "a82", "a83", "a84", "a85", "a86", "a87", "a88", "a89", "a90", "a91", "a92", "a93", "a94", "a95", "a96", "a97", "a98", "a99", "a100", "a101", "a102", "a103", "a104", "a105", "a106", "a107", "a108", "a109", "a110", "a111", "a112", "a113", "a114", "a115", "a116", "a117", "a118", "a119", "a120", "a121", "a122", "a123", "a124", "a125", "a126", "a127", "a128", "a129", "a130", "a131", "a132", "a133", "a134", "a135", "a136", "a137", "a138", "a139", "a140", "a141", "a142", "a143", "a144", "a145", "a146", "a147", "a148", "a149", "a150", "a151", "a152", "a153", "a154", "a155", "a156", "a157", "a158", "a159", "a160", "a161", "a162", "a163", "a164", "a165", "a166", "a167", "a168", "a169", "a170", "a171", "a172", "a173", "a174", "a175", "a176", "a177", "a178", "a179", "a180", "a181", "a182", "a183", "a184", "a185", "a186", "a187", "a188", "a189", "a190", "a191", "a192", "a193", "a194", "a195", "a196", "a197", "a198", "a199", "a200", "a201", "a202", "a203", "a204", "a205", "a206", "a207", "a208", "a209", "a210", "a211", "a212", "a213", "a214", "a215", "a216", "a217", "a218", "a219", "a220", "a221", "a222", "a223", "a224", "a225", "a226", "a227", "a228", "a229", "a230", "a231", "a232", "a233", "a234", "a235", "a236", "a237", "a238", "a239", "a240", "a241", "a242", "a243", "a244", "a245", "a246", "a247", "a248", "a249", "a250", "a251", "a252", "a253", "a254", "a255", "s36", "s37", "s38", "s39", "s40", "s41", "s42", "s43", "s44", "s45", "s46", "s47", "s48", "s49", "s50", "s51", "s52", "s53", "s54", "s55", "s56", "s57", "s58", "s59", "s60", "s61", "s62", "s63", "s64", "s65", "s66", "s67", "s68", "s69", "s70", "s71", "s72", "s73", "s74", "s75", "s76", "s77", "s78", "s79", "s80", "s81", "s82", "s83", "s84", "s85", "s86", "s87", "s88", "s89", "s90", "s91", "s92", "s93", "s94", "s95", "s96", "s97", "vcc", "scc", "memory"); } else { asm volatile(
R"(.macro M d,w,x
v_mfma_f32_16x16x32_bf16 v[\d:\d+3], \w, v[\x:\x+3], v[\d:\d+3]
.endm
.macro R d,a,o
ds_read_b128 v[\d:\d+3], v\a offset:\o
.endm
.macro T d,a,o
ds_read_b64_tr_b16 \d, v\a offset:\o
.endm
.macro D a,s,o
buffer_load_dwordx4 v\a, s[36:39], s\s offen offset:\o sc1 lds
.endm
.macro L c
s_waitcnt lgkmcnt(\c)
.endm
Lcode_g3o_b:
s_mov_b64 s[36:37],%0
s_mov_b32 s38,111243392
s_mov_b32 s39,0x00020000
s_mov_b32 s44,%1
s_mov_b32 s45,%2
s_mov_b32 s46,%3
s_mov_b32 s47,%4
s_mov_b32 s48,%5
s_mov_b32 s49,%6
s_mov_b32 s50,%7
s_mov_b32 s51,%8
s_mov_b32 s53,%9
v_mov_b32 v0,%10
v_mov_b32 v3,%11
v_mov_b32 v5,%12
v_mov_b32 v6,%13
v_mov_b32 v18,%14
v_mov_b32 v19,%15
v_mov_b32 v20,%16
v_mov_b32 v21,%17
v_mov_b32 v22,%18
v_mov_b32 v10,%19
v_mov_b32 v11,%20
v_mov_b32 v12,%21
v_mov_b32 v13,%22
v_mov_b32 v14,%23
v_mov_b32 v15,%24
v_mov_b32 v16,%25
s_mov_b64 s[40:41],%26
s_mov_b32 s42,7340032
s_mov_b32 s43,0x00020000
s_lshl_b32 s57,s44,11
s_add_u32 s55,s50,0
s_add_u32 m0,s57,0
s_nop 0
buffer_load_dwordx4 v18,s[40:43],s55 offen offset:0  lds
s_add_u32 m0,s57,1024
s_nop 0
buffer_load_dwordx4 v19,s[40:43],s55 offen offset:0  lds
s_add_u32 m0,s57,8064
s_nop 0
buffer_load_dwordx4 v18,s[40:43],s55 offen offset:128  lds
s_add_u32 m0,s57,9088
s_nop 0
buffer_load_dwordx4 v19,s[40:43],s55 offen offset:128  lds
s_add_u32 s55,s50,458752
s_add_u32 m0,s57,16384
s_nop 0
buffer_load_dwordx4 v18,s[40:43],s55 offen offset:0  lds
s_add_u32 m0,s57,17408
s_nop 0
buffer_load_dwordx4 v19,s[40:43],s55 offen offset:0  lds
s_add_u32 m0,s57,24448
s_nop 0
buffer_load_dwordx4 v18,s[40:43],s55 offen offset:128  lds
s_add_u32 m0,s57,25472
s_nop 0
buffer_load_dwordx4 v19,s[40:43],s55 offen offset:128  lds
s_add_u32 s55,s50,917504
s_add_u32 m0,s57,32768
s_nop 0
buffer_load_dwordx4 v18,s[40:43],s55 offen offset:0  lds
s_add_u32 m0,s57,33792
s_nop 0
buffer_load_dwordx4 v19,s[40:43],s55 offen offset:0  lds
s_add_u32 m0,s57,40832
s_nop 0
buffer_load_dwordx4 v18,s[40:43],s55 offen offset:128  lds
s_add_u32 m0,s57,41856
s_nop 0
buffer_load_dwordx4 v19,s[40:43],s55 offen offset:128  lds
s_add_u32 s55,s50,1376256
s_add_u32 m0,s57,49152
s_nop 0
buffer_load_dwordx4 v18,s[40:43],s55 offen offset:0  lds
s_add_u32 m0,s57,50176
s_nop 0
buffer_load_dwordx4 v19,s[40:43],s55 offen offset:0  lds
s_add_u32 m0,s57,57216
s_nop 0
buffer_load_dwordx4 v18,s[40:43],s55 offen offset:128  lds
s_add_u32 m0,s57,58240
s_nop 0
buffer_load_dwordx4 v19,s[40:43],s55 offen offset:128  lds
s_add_u32 s55,s50,1835008
s_add_u32 m0,s57,65536
s_nop 0
buffer_load_dwordx4 v18,s[40:43],s55 offen offset:0  lds
s_add_u32 m0,s57,66560
s_nop 0
buffer_load_dwordx4 v19,s[40:43],s55 offen offset:0  lds
s_add_u32 m0,s57,73600
s_nop 0
buffer_load_dwordx4 v18,s[40:43],s55 offen offset:128  lds
s_add_u32 m0,s57,74624
s_nop 0
buffer_load_dwordx4 v19,s[40:43],s55 offen offset:128  lds
s_add_u32 s55,s50,2293760
s_add_u32 m0,s57,81920
s_nop 0
buffer_load_dwordx4 v18,s[40:43],s55 offen offset:0  lds
s_add_u32 m0,s57,82944
s_nop 0
buffer_load_dwordx4 v19,s[40:43],s55 offen offset:0  lds
s_add_u32 m0,s57,89984
s_nop 0
buffer_load_dwordx4 v18,s[40:43],s55 offen offset:128  lds
s_add_u32 m0,s57,91008
s_nop 0
buffer_load_dwordx4 v19,s[40:43],s55 offen offset:128  lds
s_waitcnt vmcnt(20) lgkmcnt(0)
s_barrier
s_add_u32 s55,s50,2752512
s_add_u32 m0,s57,98304
s_nop 0
buffer_load_dwordx4 v18,s[40:43],s55 offen offset:0  lds
s_add_u32 m0,s57,99328
s_nop 0
buffer_load_dwordx4 v19,s[40:43],s55 offen offset:0  lds
s_add_u32 m0,s57,106368
s_nop 0
buffer_load_dwordx4 v18,s[40:43],s55 offen offset:128  lds
s_add_u32 m0,s57,107392
s_nop 0
buffer_load_dwordx4 v19,s[40:43],s55 offen offset:128  lds
v_add_u32 v26,0,v20
v_add_u32 v27,0,v21
T a[0:1],26,0
T a[2:3],26,512
T a[4:5],27,0
T a[6:7],27,512
T a[8:9],26,4096
T a[10:11],26,4608
T a[12:13],27,4096
T a[14:15],27,4608
s_waitcnt vmcnt(20) lgkmcnt(0)
s_barrier
s_add_u32 s55,s50,3211264
s_add_u32 m0,s57,0
s_nop 0
buffer_load_dwordx4 v18,s[40:43],s55 offen offset:0  lds
s_add_u32 m0,s57,1024
s_nop 0
buffer_load_dwordx4 v19,s[40:43],s55 offen offset:0  lds
s_add_u32 m0,s57,8064
s_nop 0
buffer_load_dwordx4 v18,s[40:43],s55 offen offset:128  lds
s_add_u32 m0,s57,9088
s_nop 0
buffer_load_dwordx4 v19,s[40:43],s55 offen offset:128  lds
v_add_u32 v26,16384,v20
v_add_u32 v27,16384,v21
T a[16:17],26,0
T a[18:19],26,512
T a[20:21],27,0
T a[22:23],27,512
T a[24:25],26,4096
T a[26:27],26,4608
T a[28:29],27,4096
T a[30:31],27,4608
s_waitcnt vmcnt(20) lgkmcnt(0)
s_barrier
s_add_u32 s55,s50,3670016
s_add_u32 m0,s57,16384
s_nop 0
buffer_load_dwordx4 v18,s[40:43],s55 offen offset:0  lds
s_add_u32 m0,s57,17408
s_nop 0
buffer_load_dwordx4 v19,s[40:43],s55 offen offset:0  lds
s_add_u32 m0,s57,24448
s_nop 0
buffer_load_dwordx4 v18,s[40:43],s55 offen offset:128  lds
s_add_u32 m0,s57,25472
s_nop 0
buffer_load_dwordx4 v19,s[40:43],s55 offen offset:128  lds
v_add_u32 v26,32768,v20
v_add_u32 v27,32768,v21
T a[32:33],26,0
T a[34:35],26,512
T a[36:37],27,0
T a[38:39],27,512
T a[40:41],26,4096
T a[42:43],26,4608
T a[44:45],27,4096
T a[46:47],27,4608
s_waitcnt vmcnt(20) lgkmcnt(0)
s_barrier
s_add_u32 s55,s50,4128768
s_add_u32 m0,s57,32768
s_nop 0
buffer_load_dwordx4 v18,s[40:43],s55 offen offset:0  lds
s_add_u32 m0,s57,33792
s_nop 0
buffer_load_dwordx4 v19,s[40:43],s55 offen offset:0  lds
s_add_u32 m0,s57,40832
s_nop 0
buffer_load_dwordx4 v18,s[40:43],s55 offen offset:128  lds
s_add_u32 m0,s57,41856
s_nop 0
buffer_load_dwordx4 v19,s[40:43],s55 offen offset:128  lds
v_add_u32 v26,49152,v20
v_add_u32 v27,49152,v21
T a[48:49],26,0
T a[50:51],26,512
T a[52:53],27,0
T a[54:55],27,512
T a[56:57],26,4096
T a[58:59],26,4608
T a[60:61],27,4096
T a[62:63],27,4608
s_waitcnt vmcnt(20) lgkmcnt(0)
s_barrier
s_add_u32 s55,s50,4587520
s_add_u32 m0,s57,49152
s_nop 0
buffer_load_dwordx4 v18,s[40:43],s55 offen offset:0  lds
s_add_u32 m0,s57,50176
s_nop 0
buffer_load_dwordx4 v19,s[40:43],s55 offen offset:0  lds
s_add_u32 m0,s57,57216
s_nop 0
buffer_load_dwordx4 v18,s[40:43],s55 offen offset:128  lds
s_add_u32 m0,s57,58240
s_nop 0
buffer_load_dwordx4 v19,s[40:43],s55 offen offset:128  lds
v_add_u32 v26,65536,v20
v_add_u32 v27,65536,v21
T a[64:65],26,0
T a[66:67],26,512
T a[68:69],27,0
T a[70:71],27,512
T a[72:73],26,4096
T a[74:75],26,4608
T a[76:77],27,4096
T a[78:79],27,4608
s_waitcnt vmcnt(20) lgkmcnt(0)
s_barrier
s_add_u32 s55,s50,5046272
s_add_u32 m0,s57,65536
s_nop 0
buffer_load_dwordx4 v18,s[40:43],s55 offen offset:0  lds
s_add_u32 m0,s57,66560
s_nop 0
buffer_load_dwordx4 v19,s[40:43],s55 offen offset:0  lds
s_add_u32 m0,s57,73600
s_nop 0
buffer_load_dwordx4 v18,s[40:43],s55 offen offset:128  lds
s_add_u32 m0,s57,74624
s_nop 0
buffer_load_dwordx4 v19,s[40:43],s55 offen offset:128  lds
v_add_u32 v26,81920,v20
v_add_u32 v27,81920,v21
T a[80:81],26,0
T a[82:83],26,512
T a[84:85],27,0
T a[86:87],27,512
T a[88:89],26,4096
T a[90:91],26,4608
T a[92:93],27,4096
T a[94:95],27,4608
s_waitcnt vmcnt(20) lgkmcnt(0)
s_barrier
s_add_u32 s55,s50,5505024
s_add_u32 m0,s57,81920
s_nop 0
buffer_load_dwordx4 v18,s[40:43],s55 offen offset:0  lds
s_add_u32 m0,s57,82944
s_nop 0
buffer_load_dwordx4 v19,s[40:43],s55 offen offset:0  lds
s_add_u32 m0,s57,89984
s_nop 0
buffer_load_dwordx4 v18,s[40:43],s55 offen offset:128  lds
s_add_u32 m0,s57,91008
s_nop 0
buffer_load_dwordx4 v19,s[40:43],s55 offen offset:128  lds
v_add_u32 v26,98304,v20
v_add_u32 v27,98304,v21
T a[96:97],26,0
T a[98:99],26,512
T a[100:101],27,0
T a[102:103],27,512
T a[104:105],26,4096
T a[106:107],26,4608
T a[108:109],27,4096
T a[110:111],27,4608
s_waitcnt vmcnt(20) lgkmcnt(0)
s_barrier
s_add_u32 s55,s50,5963776
s_add_u32 m0,s57,98304
s_nop 0
buffer_load_dwordx4 v18,s[40:43],s55 offen offset:0  lds
s_add_u32 m0,s57,99328
s_nop 0
buffer_load_dwordx4 v19,s[40:43],s55 offen offset:0  lds
s_add_u32 m0,s57,106368
s_nop 0
buffer_load_dwordx4 v18,s[40:43],s55 offen offset:128  lds
s_add_u32 m0,s57,107392
s_nop 0
buffer_load_dwordx4 v19,s[40:43],s55 offen offset:128  lds
v_add_u32 v26,0,v20
v_add_u32 v27,0,v21
T a[112:113],26,0
T a[114:115],26,512
T a[116:117],27,0
T a[118:119],27,512
T a[120:121],26,4096
T a[122:123],26,4608
T a[124:125],27,4096
T a[126:127],27,4608
s_waitcnt vmcnt(20) lgkmcnt(0)
s_barrier
s_add_u32 s55,s50,6422528
s_add_u32 m0,s57,0
s_nop 0
buffer_load_dwordx4 v18,s[40:43],s55 offen offset:0  lds
s_add_u32 m0,s57,1024
s_nop 0
buffer_load_dwordx4 v19,s[40:43],s55 offen offset:0  lds
s_add_u32 m0,s57,8064
s_nop 0
buffer_load_dwordx4 v18,s[40:43],s55 offen offset:128  lds
s_add_u32 m0,s57,9088
s_nop 0
buffer_load_dwordx4 v19,s[40:43],s55 offen offset:128  lds
v_add_u32 v26,16384,v20
v_add_u32 v27,16384,v21
T a[128:129],26,0
T a[130:131],26,512
T a[132:133],27,0
T a[134:135],27,512
T a[136:137],26,4096
T a[138:139],26,4608
T a[140:141],27,4096
T a[142:143],27,4608
s_waitcnt vmcnt(20) lgkmcnt(0)
s_barrier
s_add_u32 s55,s50,6881280
s_add_u32 m0,s57,16384
s_nop 0
buffer_load_dwordx4 v18,s[40:43],s55 offen offset:0  lds
s_add_u32 m0,s57,17408
s_nop 0
buffer_load_dwordx4 v19,s[40:43],s55 offen offset:0  lds
s_add_u32 m0,s57,24448
s_nop 0
buffer_load_dwordx4 v18,s[40:43],s55 offen offset:128  lds
s_add_u32 m0,s57,25472
s_nop 0
buffer_load_dwordx4 v19,s[40:43],s55 offen offset:128  lds
v_add_u32 v26,32768,v20
v_add_u32 v27,32768,v21
T a[144:145],26,0
T a[146:147],26,512
T a[148:149],27,0
T a[150:151],27,512
T a[152:153],26,4096
T a[154:155],26,4608
T a[156:157],27,4096
T a[158:159],27,4608
s_waitcnt vmcnt(20) lgkmcnt(0)
s_barrier
v_add_u32 v26,49152,v20
v_add_u32 v27,49152,v21
T a[160:161],26,0
T a[162:163],26,512
T a[164:165],27,0
T a[166:167],27,512
T a[168:169],26,4096
T a[170:171],26,4608
T a[172:173],27,4096
T a[174:175],27,4608
s_waitcnt vmcnt(16) lgkmcnt(0)
s_barrier
v_add_u32 v26,65536,v20
v_add_u32 v27,65536,v21
T a[176:177],26,0
T a[178:179],26,512
T a[180:181],27,0
T a[182:183],27,512
T a[184:185],26,4096
T a[186:187],26,4608
T a[188:189],27,4096
T a[190:191],27,4608
s_waitcnt vmcnt(12) lgkmcnt(0)
s_barrier
v_add_u32 v26,81920,v20
v_add_u32 v27,81920,v21
T a[192:193],26,0
T a[194:195],26,512
T a[196:197],27,0
T a[198:199],27,512
T a[200:201],26,4096
T a[202:203],26,4608
T a[204:205],27,4096
T a[206:207],27,4608
s_waitcnt vmcnt(8) lgkmcnt(0)
s_barrier
v_add_u32 v26,98304,v20
v_add_u32 v27,98304,v21
T a[208:209],26,0
T a[210:211],26,512
T a[212:213],27,0
T a[214:215],27,512
T a[216:217],26,4096
T a[218:219],26,4608
T a[220:221],27,4096
T a[222:223],27,4608
s_waitcnt vmcnt(4) lgkmcnt(0)
s_barrier
v_add_u32 v26,0,v20
v_add_u32 v27,0,v21
T a[224:225],26,0
T a[226:227],26,512
T a[228:229],27,0
T a[230:231],27,512
T a[232:233],26,4096
T a[234:235],26,4608
T a[236:237],27,4096
T a[238:239],27,4608
s_waitcnt vmcnt(0) lgkmcnt(0)
s_barrier
v_add_u32 v26,16384,v20
v_add_u32 v27,16384,v21
T a[240:241],26,0
T a[242:243],26,512
T a[244:245],27,0
T a[246:247],27,512
T a[248:249],26,4096
T a[250:251],26,4608
T a[252:253],27,4096
T a[254:255],27,4608
L 0
s_barrier
v_mov_b32 v34,0x3f803f80
v_mov_b32 v35,0x3f803f80
v_mov_b32 v36,0x3f803f80
v_mov_b32 v37,0x3f803f80
s_nop 1
v_mfma_f32_16x16x32_bf16 v[106:109],a[0:3],v[34:37],0
v_mfma_f32_16x16x32_bf16 v[110:113],a[4:7],v[34:37],0
M 106,a[8:11],34
M 110,a[12:15],34
M 106,a[16:19],34
M 110,a[20:23],34
M 106,a[24:27],34
M 110,a[28:31],34
M 106,a[32:35],34
M 110,a[36:39],34
M 106,a[40:43],34
M 110,a[44:47],34
M 106,a[48:51],34
M 110,a[52:55],34
M 106,a[56:59],34
M 110,a[60:63],34
M 106,a[64:67],34
M 110,a[68:71],34
M 106,a[72:75],34
M 110,a[76:79],34
M 106,a[80:83],34
M 110,a[84:87],34
M 106,a[88:91],34
M 110,a[92:95],34
M 106,a[96:99],34
M 110,a[100:103],34
M 106,a[104:107],34
M 110,a[108:111],34
M 106,a[112:115],34
M 110,a[116:119],34
M 106,a[120:123],34
M 110,a[124:127],34
M 106,a[128:131],34
M 110,a[132:135],34
M 106,a[136:139],34
M 110,a[140:143],34
M 106,a[144:147],34
M 110,a[148:151],34
M 106,a[152:155],34
M 110,a[156:159],34
M 106,a[160:163],34
M 110,a[164:167],34
M 106,a[168:171],34
M 110,a[172:175],34
M 106,a[176:179],34
)"
R"(M 110,a[180:183],34
M 106,a[184:187],34
M 110,a[188:191],34
M 106,a[192:195],34
M 110,a[196:199],34
M 106,a[200:203],34
M 110,a[204:207],34
M 106,a[208:211],34
M 110,a[212:215],34
M 106,a[216:219],34
M 110,a[220:223],34
M 106,a[224:227],34
M 110,a[228:231],34
M 106,a[232:235],34
M 110,a[236:239],34
M 106,a[240:243],34
M 110,a[244:247],34
M 106,a[248:251],34
M 110,a[252:255],34
v_mov_b32 v4,s53
s_mov_b32 s82,0
s_mov_b32 s80,0
s_mov_b32 s54,s45
s_mul_i32 s57,s44,4096
s_mul_i32 s55,s45,32768
s_add_u32 s55,s55,s57
s_add_u32 s55,s55,31457280
s_mul_i32 s57,s44,4096
s_mul_i32 s60,s46,32768
s_mul_i32 s61,s46,1024
s_mul_i32 s62,s46,114688
s_mul_i32 s63,s46,2560
s_mul_i32 s64,s46,1024
s_lshl_b32 s59,s44,8
s_add_u32 s59,s59,131072
s_mul_i32 s58,s45,1024
s_add_u32 s58,s58,109613056
s_mul_i32 s65,s45,114688
s_add_u32 s65,s65,s47
s_mul_i32 s68,s45,2560
s_add_u32 s68,s68,s48
s_mul_i32 s73,s45,1024
s_add_u32 s73,s73,107479040
s_mul_i32 s74,s45,32768
s_add_u32 s74,s74,s49
s_mul_i32 s75,s45,1024
s_add_u32 s75,s75,107479040
s_mov_b32 s66,0x7fff0000
s_mov_b32 s67,0x7fff0000
s_mov_b32 s69,0x7fff0000
s_mov_b32 s76,0x7fff0000
s_mov_b32 s77,0x7fff0000
s_mov_b32 s70,0x7fff0000
s_mov_b32 s71,0x7fff0000
s_mov_b32 s72,0x7fff0000
v_mov_b32 v1,v0
v_add_u32 v2,16384,v0
s_mov_b32 s84,16384
s_mov_b32 s86,0x3a800000
s_mov_b32 s87,0x3727c5ac
s_mov_b32 s88,0xc0135761
s_mov_b32 s89,0x3d372713
s_lshr_b32 s85,s44,1
s_mul_i32 s85,s85,127
v_lshlrev_b32 v8,22,v0
v_and_b32 v8,0x80000000,v8
v_or_b32 v8,v8,v5
v_add_u32 v2,32768,v0
s_lshl_b32 s84,s57,1
s_add_u32 s84,s84,32768
v_and_b32 v114,240,v0
v_cmp_eq_u32 vcc,0,v114
s_nop 1
v_cndmask_b32 v17,0,1.0,vcc
s_getpc_b64 s[40:41]
Lg3o_pc_%=:
s_lshl_b32 s78,s44,13
v_lshlrev_b32 v114,3,v0
v_add_u32 v114,s78,v114
v_min_u32 v114,Lcode_g3o_e-Lg3o_pc_%=-4,v114
s_mov_b32 m0,163584
s_nop 0
global_load_lds_dword v114,s[40:41]
s_add_u32 m0,s59,4096
s_nop 0
buffer_load_dword v8,s[36:39],s58 offen offset:0 sc1 lds
buffer_store_dword v4,v3,s[36:39],s71 offen offset:0 sc0 sc1
s_add_u32 m0,s59,5120
s_nop 0
buffer_load_dword v8,s[36:39],s58 offen offset:0 sc1 lds
buffer_store_dword v4,v3,s[36:39],s71 offen offset:0 sc0 sc1
s_mov_b32 s83,0
Lg3o_poll_%=:
s_add_u32 s78,s83,2
s_and_b32 s78,s78,3
s_lshl_b32 s78,s78,10
s_add_u32 s78,s78,4096
s_add_u32 m0,s59,s78
s_nop 0
buffer_load_dword v8,s[36:39],s58 offen offset:0 sc1 lds
s_and_b32 s78,s83,3
s_lshl_b32 s78,s78,10
v_add_u32 v114,s78,v6
s_waitcnt vmcnt(4)
s_barrier
ds_read_b32 v7,v114 offset:4096
buffer_store_dword v4,v3,s[36:39],s70 offen offset:0 sc0 sc1
s_mov_b32 s70,s71
s_mov_b32 s71,s72
s_mov_b32 s72,0x7fff0000
s_add_u32 s83,s83,1
s_add_u32 s82,s82,1
L 0
v_cmp_eq_u32 vcc,s53,v7
s_nop 0
s_cmp_eq_u32 vcc_lo,-1
s_cbranch_scc1 Lg3o_go_%=
s_cmp_lt_u32 s82,20000
s_cbranch_scc1 Lg3o_poll_%=
Lg3o_go_%=:
buffer_load_dword v130,v11,s[36:39],s73 offen offset:0 sc1
s_add_u32 s73,s73,s64
s_add_u32 m0,s57,0
s_add_u32 s56,s55,0
D 0,56,0
D 0,56,1024
D 0,56,2048
D 0,56,3072
s_add_u32 s58,s58,s61
s_add_u32 m0,s59,2048
s_nop 0
buffer_load_dword v8,s[36:39],s58 offen offset:0 sc1 lds
s_add_u32 m0,s57,16384
s_add_u32 s56,s55,16384
D 0,56,0
D 0,56,1024
D 0,56,2048
D 0,56,3072
s_add_u32 s55,s55,s60
s_sub_u32 s57,s84,s57
s_add_u32 s54,s54,s46
s_waitcnt vmcnt(4)
s_barrier
buffer_store_dword v4,v3,s[36:39],s70 offen offset:0 sc0 sc1
s_min_u32 s71,s71,s72
buffer_store_dword v4,v3,s[36:39],s71 offen offset:0 sc0 sc1
s_mov_b32 s70,0x7fff0000
s_mov_b32 s71,0x7fff0000
s_mov_b32 s72,0x7fff0000
s_cmp_eq_u32 s80,1
s_cbranch_scc1 Lg3o_st1_%=
s_branch Lg3o_st0_%=
Lg3o_gox_%=:
s_add_u32 m0,s59,2048
s_nop 0
buffer_load_dword v8,s[36:39],s58 offen offset:0 sc1 lds
s_waitcnt vmcnt(0)
s_barrier
buffer_store_dword v4,v3,s[36:39],s70 offen offset:0 sc0 sc1
s_min_u32 s71,s71,s72
buffer_store_dword v4,v3,s[36:39],s71 offen offset:0 sc0 sc1
s_mov_b32 s70,0x7fff0000
s_mov_b32 s71,0x7fff0000
s_mov_b32 s72,0x7fff0000
s_cmp_eq_u32 s80,1
s_cbranch_scc1 Lg3o_st1_%=
Lg3o_st0_%=:
ds_read_b32 v7,v6 offset:2048
R 34,1,0
R 38,1,1024
R 42,1,2048
R 46,1,3072
R 50,1,4096
L 5
v_cmp_eq_u32 vcc,s53,v7
L 3
s_cmp_eq_u32 vcc_lo,-1
s_cbranch_scc1 Lg3o_F0_%=
s_branch Lg3o_L0_%=
Lg3o_st1_%=:
ds_read_b32 v7,v6 offset:2048
R 34,2,0
R 38,2,1024
R 42,2,2048
R 46,2,3072
R 50,2,4096
L 5
v_cmp_eq_u32 vcc,s53,v7
L 3
s_cmp_eq_u32 vcc_lo,-1
s_cbranch_scc1 Lg3o_F1_%=
s_branch Lg3o_L1_%=
Lg3o_F0_%=:
v_mfma_f32_16x16x32_bf16 v[22:25],a[4:7],v[34:37],0
s_add_u32 s58,s58,s61
s_add_u32 m0,s59,0
R 54,1,5120
M 22,a[12:15],38
buffer_load_dword v8,s[36:39],s58 offen offset:0 sc1 lds
v_mfma_f32_16x16x32_bf16 v[18:21],a[0:3],v[34:37],0
s_add_u32 m0,s57,0
R 58,1,6144
v_add_f32_dpp v131,v130,v130 quad_perm:[1,0,3,2] row_mask:0xf bank_mask:0xf
M 18,a[8:11],38
L 3
s_add_u32 s56,s55,0
buffer_load_dword v130,v11,s[36:39],s73 offen offset:0 sc1
M 18,a[16:19],42
s_mov_b32 s66,s65
R 62,1,7168
v_add_f32_dpp v131,v131,v131 quad_perm:[2,3,0,1] row_mask:0xf bank_mask:0xf
M 18,a[24:27],46
D 0,56,0
M 22,a[20:23],42
s_add_u32 s65,s65,s62
R 66,1,8192
v_add_f32_dpp v134,v131,v131 row_half_mirror row_mask:0xf bank_mask:0xf
M 22,a[28:31],46
L 3
D 0,56,1024
M 22,a[36:39],50
s_add_u32 s54,s54,s46
R 70,1,9216
v_mov_b32_dpp v131,v134 row_ror:8 row_mask:0xf bank_mask:0xf
M 22,a[44:47],54
s_add_u32 s73,s73,s64
v_fma_f32 v131,-v134,v134,v131
v_max_f32 v131,s87,v131
M 18,a[32:35],50
R 74,1,10240
v_mul_f32 v134,v17,v134
M 18,a[40:43],54
L 3
D 0,56,2048
M 18,a[48:51],58
R 78,1,11264
v_fma_f32 v26,-v102,v106,v26
M 18,a[56:59],62
D 0,56,3072
M 22,a[52:55],58
s_add_u32 m0,s57,16384
R 82,1,12288
v_fma_f32 v27,-v102,v107,v27
M 22,a[60:63],62
L 3
v_rsq_f32 v132,v131
M 22,a[68:71],66
s_add_u32 s56,s55,16384
R 86,1,13312
v_fma_f32 v131,v15,v131,v16
M 22,a[76:79],70
s_add_u32 s55,s55,s60
v_mul_f32 v135,v132,v132
v_fma_f32 v131,v14,v135,v131
M 18,a[64:67],66
s_sub_u32 s57,s84,s57
R 90,1,14336
v_fma_f32 v134,v132,v131,v134
M 18,a[72:75],70
L 3
ds_write_b32 v12,v134 offset:0
M 18,a[80:83],74
s_waitcnt vmcnt(8)
R 94,1,15360
v_fma_f32 v28,-v102,v108,v28
M 18,a[88:91],78
s_barrier
v_fma_f32 v29,-v102,v109,v29
v_mul_f32 v114,v26,v26
M 22,a[84:87],74
R 34,1,16384
v_mul_f32 v115,v27,v27
M 22,a[92:95],78
L 4
v_mul_f32 v116,v28,v28
v_mul_f32 v117,v29,v29
M 22,a[100:103],82
R 38,1,17408
v_fma_f32 v114,v104,v114,v103
M 22,a[108:111],86
D 0,56,0
M 18,a[96:99],82
R 42,1,18432
v_fma_f32 v115,v104,v115,v103
M 18,a[104:107],86
L 3
v_fma_f32 v116,v104,v116,v103
v_fma_f32 v117,v104,v117,v103
M 18,a[112:115],90
R 46,1,19456
v_mul_f32 v114,v26,v114
M 18,a[120:123],94
v_exp_f32 v114,v114
M 22,a[116:119],90
R 50,1,20480
v_mul_f32 v115,v27,v115
M 22,a[124:127],94
L 3
v_exp_f32 v115,v115
M 22,a[132:135],34
R 54,1,21504
v_mul_f32 v116,v28,v116
M 22,a[140:143],38
D 0,56,1024
M 18,a[128:131],34
R 58,1,22528
v_mul_f32 v117,v29,v117
M 18,a[136:139],38
L 3
v_exp_f32 v116,v116
M 18,a[144:147],42
R 62,1,23552
v_fma_f32 v114,v114,v105,v105
M 18,a[152:155],46
v_exp_f32 v117,v117
M 22,a[148:151],42
R 66,1,24576
v_fma_f32 v115,v115,v105,v105
M 22,a[156:159],46
L 3
D 0,56,2048
M 22,a[164:167],50
R 70,1,25600
v_fma_f32 v116,v116,v105,v105
M 22,a[172:175],54
v_rcp_f32 v114,v114
M 18,a[160:163],50
R 74,1,26624
v_fma_f32 v117,v117,v105,v105
M 18,a[168:171],54
L 3
v_rcp_f32 v115,v115
M 18,a[176:179],58
R 78,1,27648
v_mul_f32 v26,v26,v114
M 18,a[184:187],62
v_rcp_f32 v116,v116
M 22,a[180:183],58
R 82,1,28672
v_mul_f32 v27,v27,v115
M 22,a[188:191],62
L 3
D 0,56,3072
M 22,a[196:199],66
R 86,1,29696
v_mul_f32 v28,v28,v116
M 22,a[204:207],70
v_rcp_f32 v117,v117
M 18,a[192:195],66
R 90,1,30720
v_mul_f32 v29,v29,v117
M 18,a[200:203],70
L 3
s_nop 0
v_cvt_pk_bf16_f32 v122,v26,v27
M 18,a[208:211],74
s_waitcnt vmcnt(4)
R 94,1,31744
v_fma_f32 v30,-v102,v110,v30
M 18,a[216:219],78
s_barrier
ds_read_b32 v7,v6 offset:0
R 98,13,0
M 22,a[212:215],74
R 34,2,0
v_fma_f32 v31,-v102,v111,v31
M 22,a[220:223],78
L 5
buffer_store_dword v4,v3,s[36:39],s70 offen offset:0 sc0 sc1
M 22,a[228:231],82
s_mov_b32 s70,s69
R 38,2,1024
v_fma_f32 v32,-v102,v112,v32
M 22,a[236:239],86
s_mov_b32 s69,s68
v_fma_f32 v33,-v102,v113,v33
v_mul_f32 v118,v30,v30
M 18,a[224:227],82
s_add_u32 s68,s68,s63
R 42,2,2048
v_mul_f32 v119,v31,v31
M 18,a[232:235],86
L 5
v_mul_f32 v120,v32,v32
v_mul_f32 v121,v33,v33
v_fma_f32 v118,v104,v118,v103
v_fma_f32 v119,v104,v119,v103
v_fma_f32 v120,v104,v120,v103
v_fma_f32 v121,v104,v121,v103
v_mul_f32 v118,v30,v118
v_mul_f32 v119,v31,v119
v_mul_f32 v120,v32,v120
v_mul_f32 v121,v33,v121
v_exp_f32 v118,v118
v_exp_f32 v119,v119
v_exp_f32 v120,v120
v_exp_f32 v121,v121
M 18,a[240:243],90
L 4
R 46,2,3072
v_fma_f32 v118,v118,v105,v105
v_fma_f32 v119,v119,v105,v105
v_fma_f32 v120,v120,v105,v105
v_fma_f32 v121,v121,v105,v105
v_rcp_f32 v118,v118
v_rcp_f32 v119,v119
v_rcp_f32 v120,v120
v_rcp_f32 v121,v121
v_cmp_eq_u32 vcc,s53,v7
M 18,a[248:251],94
v_cvt_pk_bf16_f32 v123,v28,v29
v_mul_f32 v30,v30,v118
v_mul_f32 v31,v31,v119
v_mul_f32 v32,v32,v120
v_mul_f32 v33,v33,v121
v_cvt_pk_bf16_f32 v124,v30,v31
v_cvt_pk_bf16_f32 v125,v32,v33
M 22,a[244:247],90
L 2
s_cmp_eq_u32 vcc_lo,-1
R 50,2,4096
buffer_store_dwordx4 v[122:125],v0,s[36:39],s67 offen offset:0 sc0 sc1
M 22,a[252:255],94
s_cbranch_scc0 Lg3o_K1_%=
Lg3o_F1_%=:
v_mfma_f32_16x16x32_bf16 v[30:33],a[4:7],v[34:37],0
s_add_u32 s58,s58,s61
s_add_u32 m0,s59,1024
R 54,2,5120
M 30,a[12:15],38
buffer_load_dword v8,s[36:39],s58 offen offset:0 sc1 lds
v_mfma_f32_16x16x32_bf16 v[26:29],a[0:3],v[34:37],0
s_add_u32 m0,s57,0
R 58,2,6144
v_add_f32_dpp v131,v130,v130 quad_perm:[1,0,3,2] row_mask:0xf bank_mask:0xf
M 26,a[8:11],38
L 3
s_add_u32 s56,s55,0
buffer_load_dword v130,v11,s[36:39],s73 offen offset:0 sc1
M 26,a[16:19],42
s_mov_b32 s67,s65
R 62,2,7168
v_add_f32_dpp v131,v131,v131 quad_perm:[2,3,0,1] row_mask:0xf bank_mask:0xf
M 26,a[24:27],46
D 0,56,0
M 30,a[20:23],42
s_add_u32 s65,s65,s62
R 66,2,8192
v_add_f32_dpp v134,v131,v131 row_half_mirror row_mask:0xf bank_mask:0xf
M 30,a[28:31],46
L 3
D 0,56,1024
M 30,a[36:39],50
s_add_u32 s54,s54,s46
R 70,2,9216
v_mov_b32_dpp v131,v134 row_ror:8 row_mask:0xf bank_mask:0xf
M 30,a[44:47],54
s_add_u32 s73,s73,s64
v_fma_f32 v131,-v134,v134,v131
v_max_f32 v131,s87,v131
M 26,a[32:35],50
R 74,2,10240
v_mul_f32 v134,v17,v134
M 26,a[40:43],54
L 3
D 0,56,2048
M 26,a[48:51],58
R 78,2,11264
v_fma_f32 v18,-v98,v106,v18
M 26,a[56:59],62
D 0,56,3072
M 30,a[52:55],58
s_add_u32 m0,s57,16384
R 82,2,12288
v_fma_f32 v19,-v98,v107,v19
M 30,a[60:63],62
L 3
v_rsq_f32 v132,v131
M 30,a[68:71],66
s_add_u32 s56,s55,16384
R 86,2,13312
v_fma_f32 v131,v15,v131,v16
M 30,a[76:79],70
s_add_u32 s55,s55,s60
v_mul_f32 v135,v132,v132
v_fma_f32 v131,v14,v135,v131
M 26,a[64:67],66
s_sub_u32 s57,s84,s57
R 90,2,14336
v_fma_f32 v134,v132,v131,v134
M 26,a[72:75],70
L 3
ds_write_b32 v12,v134 offset:2048
M 26,a[80:83],74
s_waitcnt vmcnt(8)
R 94,2,15360
v_fma_f32 v20,-v98,v108,v20
M 26,a[88:91],78
s_barrier
v_fma_f32 v21,-v98,v109,v21
)"
R"(v_mul_f32 v114,v18,v18
M 30,a[84:87],74
R 34,2,16384
v_mul_f32 v115,v19,v19
M 30,a[92:95],78
L 4
v_mul_f32 v116,v20,v20
v_mul_f32 v117,v21,v21
M 30,a[100:103],82
R 38,2,17408
v_fma_f32 v114,v100,v114,v99
M 30,a[108:111],86
D 0,56,0
M 26,a[96:99],82
R 42,2,18432
v_fma_f32 v115,v100,v115,v99
M 26,a[104:107],86
L 3
v_fma_f32 v116,v100,v116,v99
v_fma_f32 v117,v100,v117,v99
M 26,a[112:115],90
R 46,2,19456
v_mul_f32 v114,v18,v114
M 26,a[120:123],94
v_exp_f32 v114,v114
M 30,a[116:119],90
R 50,2,20480
v_mul_f32 v115,v19,v115
M 30,a[124:127],94
L 3
v_exp_f32 v115,v115
M 30,a[132:135],34
R 54,2,21504
v_mul_f32 v116,v20,v116
M 30,a[140:143],38
D 0,56,1024
M 26,a[128:131],34
R 58,2,22528
v_mul_f32 v117,v21,v117
M 26,a[136:139],38
L 3
v_exp_f32 v116,v116
M 26,a[144:147],42
R 62,2,23552
v_fma_f32 v114,v114,v101,v101
M 26,a[152:155],46
v_exp_f32 v117,v117
M 30,a[148:151],42
R 66,2,24576
v_fma_f32 v115,v115,v101,v101
M 30,a[156:159],46
L 3
D 0,56,2048
M 30,a[164:167],50
R 70,2,25600
v_fma_f32 v116,v116,v101,v101
M 30,a[172:175],54
v_rcp_f32 v114,v114
M 26,a[160:163],50
R 74,2,26624
v_fma_f32 v117,v117,v101,v101
M 26,a[168:171],54
L 3
v_rcp_f32 v115,v115
M 26,a[176:179],58
R 78,2,27648
v_mul_f32 v18,v18,v114
M 26,a[184:187],62
v_rcp_f32 v116,v116
M 30,a[180:183],58
R 82,2,28672
v_mul_f32 v19,v19,v115
M 30,a[188:191],62
L 3
D 0,56,3072
M 30,a[196:199],66
R 86,2,29696
v_mul_f32 v20,v20,v116
M 30,a[204:207],70
v_rcp_f32 v117,v117
M 26,a[192:195],66
R 90,2,30720
v_mul_f32 v21,v21,v117
M 26,a[200:203],70
L 3
s_nop 0
v_cvt_pk_bf16_f32 v122,v18,v19
M 26,a[208:211],74
s_waitcnt vmcnt(4)
R 94,2,31744
v_fma_f32 v22,-v98,v110,v22
M 26,a[216:219],78
s_barrier
ds_read_b32 v7,v6 offset:1024
R 102,13,2048
M 30,a[212:215],74
R 34,1,0
v_fma_f32 v23,-v98,v111,v23
M 30,a[220:223],78
L 5
buffer_store_dword v4,v3,s[36:39],s70 offen offset:0 sc0 sc1
M 30,a[228:231],82
s_mov_b32 s70,s69
R 38,1,1024
v_fma_f32 v24,-v98,v112,v24
M 30,a[236:239],86
s_mov_b32 s69,s68
v_fma_f32 v25,-v98,v113,v25
v_mul_f32 v118,v22,v22
M 26,a[224:227],82
s_add_u32 s68,s68,s63
R 42,1,2048
v_mul_f32 v119,v23,v23
M 26,a[232:235],86
L 5
v_mul_f32 v120,v24,v24
v_mul_f32 v121,v25,v25
v_fma_f32 v118,v100,v118,v99
v_fma_f32 v119,v100,v119,v99
v_fma_f32 v120,v100,v120,v99
v_fma_f32 v121,v100,v121,v99
v_mul_f32 v118,v22,v118
v_mul_f32 v119,v23,v119
v_mul_f32 v120,v24,v120
v_mul_f32 v121,v25,v121
v_exp_f32 v118,v118
v_exp_f32 v119,v119
v_exp_f32 v120,v120
v_exp_f32 v121,v121
M 26,a[240:243],90
L 4
R 46,1,3072
v_fma_f32 v118,v118,v101,v101
v_fma_f32 v119,v119,v101,v101
v_fma_f32 v120,v120,v101,v101
v_fma_f32 v121,v121,v101,v101
v_rcp_f32 v118,v118
v_rcp_f32 v119,v119
v_rcp_f32 v120,v120
v_rcp_f32 v121,v121
v_cmp_eq_u32 vcc,s53,v7
M 26,a[248:251],94
v_cvt_pk_bf16_f32 v123,v20,v21
v_mul_f32 v22,v22,v118
v_mul_f32 v23,v23,v119
v_mul_f32 v24,v24,v120
v_mul_f32 v25,v25,v121
v_cvt_pk_bf16_f32 v124,v22,v23
v_cvt_pk_bf16_f32 v125,v24,v25
M 30,a[244:247],90
L 2
s_cmp_eq_u32 vcc_lo,-1
R 50,1,4096
buffer_store_dwordx4 v[122:125],v0,s[36:39],s66 offen offset:0 sc0 sc1
M 30,a[252:255],94
s_cbranch_scc1 Lg3o_F0_%=
Lg3o_K0_%=:
Lg3o_L0_%=:
v_mfma_f32_16x16x32_bf16 v[22:25],a[4:7],v[34:37],0
s_add_u32 m0,s59,3072
R 54,1,5120
v_add_f32_dpp v131,v130,v130 quad_perm:[1,0,3,2] row_mask:0xf bank_mask:0xf
M 22,a[12:15],38
buffer_load_dword v8,s[36:39],s58 offen offset:0 sc1 lds
v_mfma_f32_16x16x32_bf16 v[18:21],a[0:3],v[34:37],0
s_mov_b32 s66,s65
R 58,1,6144
v_add_f32_dpp v131,v131,v131 quad_perm:[2,3,0,1] row_mask:0xf bank_mask:0xf
M 18,a[8:11],38
L 3
v_fma_f32 v26,-v102,v106,v26
v_fma_f32 v27,-v102,v107,v27
M 18,a[16:19],42
s_add_u32 s65,s65,s62
R 62,1,7168
v_add_f32_dpp v134,v131,v131 row_half_mirror row_mask:0xf bank_mask:0xf
M 18,a[24:27],46
v_fma_f32 v28,-v102,v108,v28
v_fma_f32 v29,-v102,v109,v29
M 22,a[20:23],42
R 66,1,8192
v_mov_b32_dpp v131,v134 row_ror:8 row_mask:0xf bank_mask:0xf
M 22,a[28:31],46
L 3
v_fma_f32 v131,-v134,v134,v131
v_max_f32 v131,s87,v131
M 22,a[36:39],50
R 70,1,9216
v_mul_f32 v134,v17,v134
M 22,a[44:47],54
v_rsq_f32 v132,v131
M 18,a[32:35],50
R 74,1,10240
v_fma_f32 v131,v15,v131,v16
M 18,a[40:43],54
L 3
v_mul_f32 v135,v132,v132
v_fma_f32 v131,v14,v135,v131
M 18,a[48:51],58
R 78,1,11264
v_fma_f32 v134,v132,v131,v134
M 18,a[56:59],62
ds_write_b32 v12,v134 offset:0
M 22,a[52:55],58
R 82,1,12288
v_mul_f32 v114,v26,v26
M 22,a[60:63],62
L 4
v_mul_f32 v115,v27,v27
v_mul_f32 v116,v28,v28
M 22,a[68:71],66
R 86,1,13312
v_mul_f32 v117,v29,v29
M 22,a[76:79],70
v_fma_f32 v114,v104,v114,v103
v_fma_f32 v115,v104,v115,v103
M 18,a[64:67],66
R 90,1,14336
v_fma_f32 v116,v104,v116,v103
M 18,a[72:75],70
L 4
v_fma_f32 v117,v104,v117,v103
v_mul_f32 v114,v26,v114
M 18,a[80:83],74
s_waitcnt vmcnt(0)
R 94,1,15360
v_mul_f32 v115,v27,v115
M 18,a[88:91],78
s_barrier
ds_read_b32 v7,v6 offset:3072
v_mul_f32 v116,v28,v116
M 22,a[84:87],74
R 34,1,16384
v_mul_f32 v117,v29,v117
M 22,a[92:95],78
L 4
buffer_store_dword v4,v3,s[36:39],s70 offen offset:0 sc0 sc1
M 22,a[100:103],82
s_mov_b32 s70,0x7fff0000
R 38,1,17408
v_fma_f32 v30,-v102,v110,v30
M 22,a[108:111],86
v_exp_f32 v114,v114
M 18,a[96:99],82
R 42,1,18432
v_fma_f32 v114,v114,v105,v105
M 18,a[104:107],86
L 4
L 3
v_cmp_eq_u32 vcc,s53,v7
M 18,a[112:115],90
R 46,1,19456
v_fma_f32 v31,-v102,v111,v31
M 18,a[120:123],94
v_exp_f32 v115,v115
M 22,a[116:119],90
s_cmp_eq_u32 vcc_lo,-1
R 50,1,20480
v_fma_f32 v115,v115,v105,v105
s_cbranch_scc0 Lg3o_X0_%=
M 22,a[124:127],94
L 3
s_add_u32 m0,s57,0
s_add_u32 s56,s55,0
buffer_load_dword v130,v11,s[36:39],s73 offen offset:0 sc1
M 22,a[132:135],34
s_add_u32 s58,s58,s61
R 54,1,21504
v_fma_f32 v32,-v102,v112,v32
M 22,a[140:143],38
D 0,56,0
M 18,a[128:131],34
s_add_u32 s54,s54,s46
R 58,1,22528
v_fma_f32 v33,-v102,v113,v33
M 18,a[136:139],38
L 3
v_exp_f32 v116,v116
M 18,a[144:147],42
s_add_u32 s73,s73,s64
R 62,1,23552
v_fma_f32 v116,v116,v105,v105
M 18,a[152:155],46
D 0,56,1024
M 22,a[148:151],42
R 66,1,24576
v_mul_f32 v118,v30,v30
M 22,a[156:159],46
L 3
v_exp_f32 v117,v117
M 22,a[164:167],50
R 70,1,25600
v_fma_f32 v117,v117,v105,v105
M 22,a[172:175],54
D 0,56,2048
M 18,a[160:163],50
R 74,1,26624
v_mul_f32 v119,v31,v31
M 18,a[168:171],54
L 3
v_rcp_f32 v114,v114
M 18,a[176:179],58
R 78,1,27648
v_mul_f32 v26,v26,v114
M 18,a[184:187],62
D 0,56,3072
M 22,a[180:183],58
s_add_u32 m0,s59,0
R 82,1,28672
v_mul_f32 v120,v32,v32
M 22,a[188:191],62
L 3
buffer_load_dword v8,s[36:39],s58 offen offset:0 sc1 lds
M 22,a[196:199],66
s_add_u32 m0,s57,16384
R 86,1,29696
v_mul_f32 v121,v33,v33
M 22,a[204:207],70
s_add_u32 s56,s55,16384
v_rcp_f32 v115,v115
M 18,a[192:195],66
s_add_u32 s55,s55,s60
R 90,1,30720
v_mul_f32 v27,v27,v115
M 18,a[200:203],70
L 3
s_nop 0
D 0,56,0
M 18,a[208:211],74
s_waitcnt vmcnt(1)
R 94,1,31744
v_cvt_pk_bf16_f32 v122,v26,v27
M 18,a[216:219],78
s_barrier
ds_read_b32 v7,v6 offset:0
R 98,13,0
M 22,a[212:215],74
s_sub_u32 s57,s84,s57
R 34,2,0
v_fma_f32 v118,v104,v118,v103
M 22,a[220:223],78
L 5
buffer_store_dword v4,v3,s[36:39],s70 offen offset:0 sc0 sc1
M 22,a[228:231],82
s_mov_b32 s70,s69
R 38,2,1024
v_fma_f32 v119,v104,v119,v103
M 22,a[236:239],86
D 0,56,1024
M 18,a[224:227],82
s_mov_b32 s69,s68
R 42,2,2048
v_fma_f32 v120,v104,v120,v103
M 18,a[232:235],86
L 5
v_fma_f32 v121,v104,v121,v103
v_mul_f32 v118,v30,v118
v_mul_f32 v119,v31,v119
v_mul_f32 v120,v32,v120
v_mul_f32 v121,v33,v121
v_exp_f32 v118,v118
v_exp_f32 v119,v119
v_exp_f32 v120,v120
v_exp_f32 v121,v121
M 18,a[240:243],90
L 4
R 46,2,3072
v_rcp_f32 v116,v116
v_rcp_f32 v117,v117
v_fma_f32 v118,v118,v105,v105
v_fma_f32 v119,v119,v105,v105
v_fma_f32 v120,v120,v105,v105
v_fma_f32 v121,v121,v105,v105
v_rcp_f32 v118,v118
v_rcp_f32 v119,v119
v_rcp_f32 v120,v120
v_rcp_f32 v121,v121
v_cmp_eq_u32 vcc,s53,v7
M 18,a[248:251],94
s_add_u32 s68,s68,s63
v_mul_f32 v28,v28,v116
v_mul_f32 v29,v29,v117
v_cvt_pk_bf16_f32 v123,v28,v29
v_mul_f32 v30,v30,v118
v_mul_f32 v31,v31,v119
v_mul_f32 v32,v32,v120
v_mul_f32 v33,v33,v121
v_cvt_pk_bf16_f32 v124,v30,v31
v_cvt_pk_bf16_f32 v125,v32,v33
M 22,a[244:247],90
L 2
s_cmp_eq_u32 vcc_lo,-1
R 50,2,4096
buffer_store_dwordx4 v[122:125],v0,s[36:39],s67 offen offset:0 sc0 sc1
M 22,a[252:255],94
D 0,56,2048
D 0,56,3072
buffer_store_dword v4,v3,s[36:39],s71 offen offset:0 sc0 sc1
buffer_store_dword v4,v3,s[36:39],s71 offen offset:0 sc0 sc1
s_cbranch_scc1 Lg3o_F1_%=
s_branch Lg3o_K1_%=
Lg3o_X0_%=:
M 22,a[124:127],94
L 3
s_add_u32 m0,s59,0
v_fma_f32 v32,-v102,v112,v32
M 22,a[132:135],34
R 54,1,21504
v_fma_f32 v33,-v102,v113,v33
M 22,a[140:143],38
buffer_load_dword v8,s[36:39],s58 offen offset:0 sc1 lds
M 18,a[128:131],34
R 58,1,22528
v_mul_f32 v118,v30,v30
M 18,a[136:139],38
L 3
v_exp_f32 v116,v116
M 18,a[144:147],42
R 62,1,23552
v_fma_f32 v116,v116,v105,v105
M 18,a[152:155],46
v_exp_f32 v117,v117
M 22,a[148:151],42
R 66,1,24576
v_fma_f32 v117,v117,v105,v105
M 22,a[156:159],46
L 3
v_rcp_f32 v114,v114
v_mul_f32 v119,v31,v31
M 22,a[164:167],50
R 70,1,25600
v_mul_f32 v26,v26,v114
M 22,a[172:175],54
v_rcp_f32 v115,v115
M 18,a[160:163],50
R 74,1,26624
v_mul_f32 v27,v27,v115
M 18,a[168:171],54
L 3
v_rcp_f32 v116,v116
M 18,a[176:179],58
R 78,1,27648
v_mul_f32 v28,v28,v116
M 18,a[184:187],62
v_rcp_f32 v117,v117
M 22,a[180:183],58
R 82,1,28672
v_mul_f32 v29,v29,v117
M 22,a[188:191],62
L 3
v_cvt_pk_bf16_f32 v122,v26,v27
v_cvt_pk_bf16_f32 v123,v28,v29
M 22,a[196:199],66
R 86,1,29696
v_mul_f32 v120,v32,v32
M 22,a[204:207],70
v_mul_f32 v121,v33,v33
v_fma_f32 v118,v104,v118,v103
M 18,a[192:195],66
R 90,1,30720
v_fma_f32 v119,v104,v119,v103
M 18,a[200:203],70
L 3
s_nop 0
v_fma_f32 v120,v104,v120,v103
M 18,a[208:211],74
s_waitcnt vmcnt(0)
R 94,1,31744
v_fma_f32 v121,v104,v121,v103
M 18,a[216:219],78
s_barrier
buffer_store_dword v4,v3,s[36:39],s70 offen offset:0 sc0 sc1
M 22,a[212:215],74
s_mov_b32 s70,s69
ds_read_b32 v7,v6 offset:0
R 98,13,0
M 22,a[220:223],78
L 4
v_mul_f32 v118,v30,v118
v_mul_f32 v119,v31,v119
M 22,a[228:231],82
s_mov_b32 s69,s68
v_mul_f32 v120,v32,v120
v_mul_f32 v121,v33,v121
M 22,a[236:239],86
s_add_u32 s68,s68,s63
v_exp_f32 v118,v118
M 18,a[224:227],82
v_exp_f32 v119,v119
M 18,a[232:235],86
L 2
v_exp_f32 v120,v120
v_exp_f32 v121,v121
M 18,a[240:243],90
L 1
v_fma_f32 v118,v118,v105,v105
v_fma_f32 v119,v119,v105,v105
v_fma_f32 v120,v120,v105,v105
v_fma_f32 v121,v121,v105,v105
v_rcp_f32 v118,v118
)"
R"(v_rcp_f32 v119,v119
v_rcp_f32 v120,v120
v_rcp_f32 v121,v121
M 18,a[248:251],94
v_mul_f32 v30,v30,v118
v_mul_f32 v31,v31,v119
v_mul_f32 v32,v32,v120
v_mul_f32 v33,v33,v121
v_cvt_pk_bf16_f32 v124,v30,v31
v_cvt_pk_bf16_f32 v125,v32,v33
M 22,a[244:247],90
buffer_store_dwordx4 v[122:125],v0,s[36:39],s67 offen offset:0 sc0 sc1
v_cmp_eq_u32 vcc,s53,v7
M 22,a[252:255],94
s_mov_b32 s80,1
s_cmp_eq_u32 vcc_lo,-1
s_cbranch_scc0 Lg3o_xn0_%=
buffer_load_dword v130,v11,s[36:39],s73 offen offset:0 sc1
L 0
v_fma_f32 v18,-v98,v106,v18
v_fma_f32 v19,-v98,v107,v19
v_fma_f32 v20,-v98,v108,v20
v_fma_f32 v21,-v98,v109,v21
v_mul_f32 v114,v18,v18
v_mul_f32 v115,v19,v19
s_add_u32 s73,s73,s64
s_add_u32 m0,s57,0
s_add_u32 s56,s55,0
D 0,56,0
v_mul_f32 v116,v20,v20
v_mul_f32 v117,v21,v21
v_fma_f32 v114,v100,v114,v99
v_fma_f32 v115,v100,v115,v99
v_fma_f32 v116,v100,v116,v99
v_fma_f32 v117,v100,v117,v99
D 0,56,1024
v_mul_f32 v114,v18,v114
v_mul_f32 v115,v19,v115
v_mul_f32 v116,v20,v116
v_mul_f32 v117,v21,v117
v_exp_f32 v114,v114
v_exp_f32 v115,v115
D 0,56,2048
v_exp_f32 v116,v116
v_exp_f32 v117,v117
v_fma_f32 v114,v114,v101,v101
v_fma_f32 v115,v115,v101,v101
v_fma_f32 v116,v116,v101,v101
v_fma_f32 v117,v117,v101,v101
D 0,56,3072
v_rcp_f32 v114,v114
v_rcp_f32 v115,v115
v_rcp_f32 v116,v116
v_rcp_f32 v117,v117
v_mul_f32 v18,v18,v114
v_mul_f32 v19,v19,v115
s_add_u32 s58,s58,s61
s_add_u32 m0,s57,16384
s_add_u32 s56,s55,16384
D 0,56,0
v_mul_f32 v20,v20,v116
v_mul_f32 v21,v21,v117
v_cvt_pk_bf16_f32 v122,v18,v19
v_cvt_pk_bf16_f32 v123,v20,v21
v_fma_f32 v22,-v98,v110,v22
v_fma_f32 v23,-v98,v111,v23
D 0,56,1024
v_fma_f32 v24,-v98,v112,v24
v_fma_f32 v25,-v98,v113,v25
v_mul_f32 v118,v22,v22
v_mul_f32 v119,v23,v23
v_mul_f32 v120,v24,v24
v_mul_f32 v121,v25,v25
D 0,56,2048
v_fma_f32 v118,v100,v118,v99
v_fma_f32 v119,v100,v119,v99
v_fma_f32 v120,v100,v120,v99
v_fma_f32 v121,v100,v121,v99
v_mul_f32 v118,v22,v118
v_mul_f32 v119,v23,v119
D 0,56,3072
v_mul_f32 v120,v24,v120
v_mul_f32 v121,v25,v121
v_exp_f32 v118,v118
v_exp_f32 v119,v119
v_exp_f32 v120,v120
v_exp_f32 v121,v121
s_add_u32 s55,s55,s60
s_sub_u32 s57,s84,s57
s_add_u32 s54,s54,s46
v_fma_f32 v118,v118,v101,v101
v_fma_f32 v119,v119,v101,v101
v_fma_f32 v120,v120,v101,v101
v_fma_f32 v121,v121,v101,v101
v_rcp_f32 v118,v118
v_rcp_f32 v119,v119
v_rcp_f32 v120,v120
v_rcp_f32 v121,v121
v_mul_f32 v22,v22,v118
v_mul_f32 v23,v23,v119
v_mul_f32 v24,v24,v120
v_mul_f32 v25,v25,v121
v_cvt_pk_bf16_f32 v124,v22,v23
v_cvt_pk_bf16_f32 v125,v24,v25
buffer_store_dwordx4 v[122:125],v0,s[36:39],s66 offen offset:0 sc0 sc1
s_mov_b32 s72,s69
s_mov_b32 s69,0x7fff0000
s_mov_b32 s66,0x7fff0000
s_mov_b32 s76,0x7fff0000
s_branch Lg3o_gox_%=
Lg3o_xn0_%=:
s_cmp_ge_u32 s54,256
s_cbranch_scc1 Lg3o_fin0_%=
s_add_u32 m0,s59,4096
s_nop 0
buffer_load_dword v8,s[36:39],s58 offen offset:0 sc1 lds
buffer_store_dword v4,v3,s[36:39],s71 offen offset:0 sc0 sc1
s_add_u32 m0,s59,5120
s_nop 0
buffer_load_dword v8,s[36:39],s58 offen offset:0 sc1 lds
buffer_store_dword v4,v3,s[36:39],s71 offen offset:0 sc0 sc1
s_mov_b32 s83,0
L 0
v_fma_f32 v18,-v98,v106,v18
v_fma_f32 v19,-v98,v107,v19
v_fma_f32 v20,-v98,v108,v20
v_fma_f32 v21,-v98,v109,v21
v_mul_f32 v114,v18,v18
v_mul_f32 v115,v19,v19
v_mul_f32 v116,v20,v20
v_mul_f32 v117,v21,v21
v_fma_f32 v114,v100,v114,v99
v_fma_f32 v115,v100,v115,v99
v_fma_f32 v116,v100,v116,v99
v_fma_f32 v117,v100,v117,v99
v_mul_f32 v114,v18,v114
v_mul_f32 v115,v19,v115
v_mul_f32 v116,v20,v116
v_mul_f32 v117,v21,v117
v_exp_f32 v114,v114
v_exp_f32 v115,v115
v_exp_f32 v116,v116
v_exp_f32 v117,v117
v_fma_f32 v114,v114,v101,v101
v_fma_f32 v115,v115,v101,v101
v_fma_f32 v116,v116,v101,v101
v_fma_f32 v117,v117,v101,v101
v_rcp_f32 v114,v114
v_rcp_f32 v115,v115
v_rcp_f32 v116,v116
v_rcp_f32 v117,v117
v_mul_f32 v18,v18,v114
v_mul_f32 v19,v19,v115
v_mul_f32 v20,v20,v116
v_mul_f32 v21,v21,v117
v_cvt_pk_bf16_f32 v122,v18,v19
v_cvt_pk_bf16_f32 v123,v20,v21
v_fma_f32 v22,-v98,v110,v22
v_fma_f32 v23,-v98,v111,v23
v_fma_f32 v24,-v98,v112,v24
v_fma_f32 v25,-v98,v113,v25
v_mul_f32 v118,v22,v22
v_mul_f32 v119,v23,v23
v_mul_f32 v120,v24,v24
v_mul_f32 v121,v25,v25
v_fma_f32 v118,v100,v118,v99
v_fma_f32 v119,v100,v119,v99
v_fma_f32 v120,v100,v120,v99
v_fma_f32 v121,v100,v121,v99
v_mul_f32 v118,v22,v118
v_mul_f32 v119,v23,v119
v_mul_f32 v120,v24,v120
v_mul_f32 v121,v25,v121
v_exp_f32 v118,v118
v_exp_f32 v119,v119
v_exp_f32 v120,v120
v_exp_f32 v121,v121
v_fma_f32 v118,v118,v101,v101
v_fma_f32 v119,v119,v101,v101
v_fma_f32 v120,v120,v101,v101
v_fma_f32 v121,v121,v101,v101
v_rcp_f32 v118,v118
v_rcp_f32 v119,v119
v_rcp_f32 v120,v120
v_rcp_f32 v121,v121
v_mul_f32 v22,v22,v118
v_mul_f32 v23,v23,v119
v_mul_f32 v24,v24,v120
v_mul_f32 v25,v25,v121
v_cvt_pk_bf16_f32 v124,v22,v23
v_cvt_pk_bf16_f32 v125,v24,v25
buffer_store_dwordx4 v[122:125],v0,s[36:39],s66 offen offset:0 sc0 sc1
s_mov_b32 s72,s69
s_mov_b32 s69,0x7fff0000
s_mov_b32 s66,0x7fff0000
s_mov_b32 s76,0x7fff0000
s_branch Lg3o_poll_%=
Lg3o_fin0_%=:
L 0
v_fma_f32 v18,-v98,v106,v18
v_fma_f32 v19,-v98,v107,v19
v_fma_f32 v20,-v98,v108,v20
v_fma_f32 v21,-v98,v109,v21
v_mul_f32 v114,v18,v18
v_mul_f32 v115,v19,v19
v_mul_f32 v116,v20,v20
v_mul_f32 v117,v21,v21
v_fma_f32 v114,v100,v114,v99
v_fma_f32 v115,v100,v115,v99
v_fma_f32 v116,v100,v116,v99
v_fma_f32 v117,v100,v117,v99
v_mul_f32 v114,v18,v114
v_mul_f32 v115,v19,v115
v_mul_f32 v116,v20,v116
v_mul_f32 v117,v21,v117
v_exp_f32 v114,v114
v_exp_f32 v115,v115
v_exp_f32 v116,v116
v_exp_f32 v117,v117
v_fma_f32 v114,v114,v101,v101
v_fma_f32 v115,v115,v101,v101
v_fma_f32 v116,v116,v101,v101
v_fma_f32 v117,v117,v101,v101
v_rcp_f32 v114,v114
v_rcp_f32 v115,v115
v_rcp_f32 v116,v116
v_rcp_f32 v117,v117
v_mul_f32 v18,v18,v114
v_mul_f32 v19,v19,v115
v_mul_f32 v20,v20,v116
v_mul_f32 v21,v21,v117
v_cvt_pk_bf16_f32 v122,v18,v19
v_cvt_pk_bf16_f32 v123,v20,v21
v_fma_f32 v22,-v98,v110,v22
v_fma_f32 v23,-v98,v111,v23
v_fma_f32 v24,-v98,v112,v24
v_fma_f32 v25,-v98,v113,v25
v_mul_f32 v118,v22,v22
v_mul_f32 v119,v23,v23
v_mul_f32 v120,v24,v24
v_mul_f32 v121,v25,v25
v_fma_f32 v118,v100,v118,v99
v_fma_f32 v119,v100,v119,v99
v_fma_f32 v120,v100,v120,v99
v_fma_f32 v121,v100,v121,v99
v_mul_f32 v118,v22,v118
v_mul_f32 v119,v23,v119
v_mul_f32 v120,v24,v120
v_mul_f32 v121,v25,v121
v_exp_f32 v118,v118
v_exp_f32 v119,v119
v_exp_f32 v120,v120
v_exp_f32 v121,v121
v_fma_f32 v118,v118,v101,v101
v_fma_f32 v119,v119,v101,v101
v_fma_f32 v120,v120,v101,v101
v_fma_f32 v121,v121,v101,v101
v_rcp_f32 v118,v118
v_rcp_f32 v119,v119
v_rcp_f32 v120,v120
v_rcp_f32 v121,v121
v_mul_f32 v22,v22,v118
v_mul_f32 v23,v23,v119
v_mul_f32 v24,v24,v120
v_mul_f32 v25,v25,v121
v_cvt_pk_bf16_f32 v124,v22,v23
v_cvt_pk_bf16_f32 v125,v24,v25
buffer_store_dwordx4 v[122:125],v0,s[36:39],s66 offen offset:0 sc0 sc1
s_mov_b32 s72,s69
s_mov_b32 s69,0x7fff0000
s_mov_b32 s66,0x7fff0000
s_mov_b32 s76,0x7fff0000
s_branch Lg3o_done_%=
Lg3o_K1_%=:
Lg3o_L1_%=:
v_mfma_f32_16x16x32_bf16 v[30:33],a[4:7],v[34:37],0
s_add_u32 m0,s59,3072
R 54,2,5120
v_add_f32_dpp v131,v130,v130 quad_perm:[1,0,3,2] row_mask:0xf bank_mask:0xf
M 30,a[12:15],38
buffer_load_dword v8,s[36:39],s58 offen offset:0 sc1 lds
v_mfma_f32_16x16x32_bf16 v[26:29],a[0:3],v[34:37],0
s_mov_b32 s67,s65
R 58,2,6144
v_add_f32_dpp v131,v131,v131 quad_perm:[2,3,0,1] row_mask:0xf bank_mask:0xf
M 26,a[8:11],38
L 3
v_fma_f32 v18,-v98,v106,v18
v_fma_f32 v19,-v98,v107,v19
M 26,a[16:19],42
s_add_u32 s65,s65,s62
R 62,2,7168
v_add_f32_dpp v134,v131,v131 row_half_mirror row_mask:0xf bank_mask:0xf
M 26,a[24:27],46
v_fma_f32 v20,-v98,v108,v20
v_fma_f32 v21,-v98,v109,v21
M 30,a[20:23],42
R 66,2,8192
v_mov_b32_dpp v131,v134 row_ror:8 row_mask:0xf bank_mask:0xf
M 30,a[28:31],46
L 3
v_fma_f32 v131,-v134,v134,v131
v_max_f32 v131,s87,v131
M 30,a[36:39],50
R 70,2,9216
v_mul_f32 v134,v17,v134
M 30,a[44:47],54
v_rsq_f32 v132,v131
M 26,a[32:35],50
R 74,2,10240
v_fma_f32 v131,v15,v131,v16
M 26,a[40:43],54
L 3
v_mul_f32 v135,v132,v132
v_fma_f32 v131,v14,v135,v131
M 26,a[48:51],58
R 78,2,11264
v_fma_f32 v134,v132,v131,v134
M 26,a[56:59],62
ds_write_b32 v12,v134 offset:2048
M 30,a[52:55],58
R 82,2,12288
v_mul_f32 v114,v18,v18
M 30,a[60:63],62
L 4
v_mul_f32 v115,v19,v19
v_mul_f32 v116,v20,v20
M 30,a[68:71],66
R 86,2,13312
v_mul_f32 v117,v21,v21
M 30,a[76:79],70
v_fma_f32 v114,v100,v114,v99
v_fma_f32 v115,v100,v115,v99
M 26,a[64:67],66
R 90,2,14336
v_fma_f32 v116,v100,v116,v99
M 26,a[72:75],70
L 4
v_fma_f32 v117,v100,v117,v99
v_mul_f32 v114,v18,v114
M 26,a[80:83],74
s_waitcnt vmcnt(0)
R 94,2,15360
v_mul_f32 v115,v19,v115
M 26,a[88:91],78
s_barrier
ds_read_b32 v7,v6 offset:3072
v_mul_f32 v116,v20,v116
M 30,a[84:87],74
R 34,2,16384
v_mul_f32 v117,v21,v117
M 30,a[92:95],78
L 4
buffer_store_dword v4,v3,s[36:39],s70 offen offset:0 sc0 sc1
M 30,a[100:103],82
s_mov_b32 s70,0x7fff0000
R 38,2,17408
v_fma_f32 v22,-v98,v110,v22
M 30,a[108:111],86
v_exp_f32 v114,v114
M 26,a[96:99],82
R 42,2,18432
v_fma_f32 v114,v114,v101,v101
M 26,a[104:107],86
L 4
L 3
v_cmp_eq_u32 vcc,s53,v7
M 26,a[112:115],90
R 46,2,19456
v_fma_f32 v23,-v98,v111,v23
M 26,a[120:123],94
v_exp_f32 v115,v115
M 30,a[116:119],90
s_cmp_eq_u32 vcc_lo,-1
R 50,2,20480
v_fma_f32 v115,v115,v101,v101
s_cbranch_scc0 Lg3o_X1_%=
M 30,a[124:127],94
L 3
s_add_u32 m0,s57,0
s_add_u32 s56,s55,0
buffer_load_dword v130,v11,s[36:39],s73 offen offset:0 sc1
M 30,a[132:135],34
s_add_u32 s58,s58,s61
R 54,2,21504
v_fma_f32 v24,-v98,v112,v24
M 30,a[140:143],38
D 0,56,0
M 26,a[128:131],34
s_add_u32 s54,s54,s46
R 58,2,22528
v_fma_f32 v25,-v98,v113,v25
M 26,a[136:139],38
L 3
v_exp_f32 v116,v116
M 26,a[144:147],42
s_add_u32 s73,s73,s64
R 62,2,23552
v_fma_f32 v116,v116,v101,v101
M 26,a[152:155],46
D 0,56,1024
M 30,a[148:151],42
R 66,2,24576
v_mul_f32 v118,v22,v22
M 30,a[156:159],46
L 3
v_exp_f32 v117,v117
M 30,a[164:167],50
R 70,2,25600
v_fma_f32 v117,v117,v101,v101
M 30,a[172:175],54
D 0,56,2048
M 26,a[160:163],50
R 74,2,26624
v_mul_f32 v119,v23,v23
M 26,a[168:171],54
L 3
v_rcp_f32 v114,v114
M 26,a[176:179],58
R 78,2,27648
v_mul_f32 v18,v18,v114
M 26,a[184:187],62
D 0,56,3072
M 30,a[180:183],58
s_add_u32 m0,s59,1024
R 82,2,28672
v_mul_f32 v120,v24,v24
M 30,a[188:191],62
L 3
buffer_load_dword v8,s[36:39],s58 offen offset:0 sc1 lds
M 30,a[196:199],66
s_add_u32 m0,s57,16384
R 86,2,29696
v_mul_f32 v121,v25,v25
M 30,a[204:207],70
s_add_u32 s56,s55,16384
v_rcp_f32 v115,v115
M 26,a[192:195],66
s_add_u32 s55,s55,s60
R 90,2,30720
v_mul_f32 v19,v19,v115
M 26,a[200:203],70
L 3
s_nop 0
D 0,56,0
M 26,a[208:211],74
s_waitcnt vmcnt(1)
R 94,2,31744
v_cvt_pk_bf16_f32 v122,v18,v19
M 26,a[216:219],78
s_barrier
ds_read_b32 v7,v6 offset:1024
R 102,13,2048
M 30,a[212:215],74
s_sub_u32 s57,s84,s57
R 34,1,0
v_fma_f32 v118,v100,v118,v99
M 30,a[220:223],78
L 5
buffer_store_dword v4,v3,s[36:39],s70 offen offset:0 sc0 sc1
M 30,a[228:231],82
s_mov_b32 s70,s69
R 38,1,1024
v_fma_f32 v119,v100,v119,v99
M 30,a[236:239],86
D 0,56,1024
M 26,a[224:227],82
s_mov_b32 s69,s68
R 42,1,2048
v_fma_f32 v120,v100,v120,v99
M 26,a[232:235],86
L 5
v_fma_f32 v121,v100,v121,v99
v_mul_f32 v118,v22,v118
v_mul_f32 v119,v23,v119
v_mul_f32 v120,v24,v120
v_mul_f32 v121,v25,v121
v_exp_f32 v118,v118
v_exp_f32 v119,v119
v_exp_f32 v120,v120
v_exp_f32 v121,v121
M 26,a[240:243],90
L 4
R 46,1,3072
v_rcp_f32 v116,v116
v_rcp_f32 v117,v117
v_fma_f32 v118,v118,v101,v101
v_fma_f32 v119,v119,v101,v101
v_fma_f32 v120,v120,v101,v101
v_fma_f32 v121,v121,v101,v101
v_rcp_f32 v118,v118
v_rcp_f32 v119,v119
v_rcp_f32 v120,v120
v_rcp_f32 v121,v121
v_cmp_eq_u32 vcc,s53,v7
M 26,a[248:251],94
s_add_u32 s68,s68,s63
v_mul_f32 v20,v20,v116
v_mul_f32 v21,v21,v117
v_cvt_pk_bf16_f32 v123,v20,v21
)"
R"(v_mul_f32 v22,v22,v118
v_mul_f32 v23,v23,v119
v_mul_f32 v24,v24,v120
v_mul_f32 v25,v25,v121
v_cvt_pk_bf16_f32 v124,v22,v23
v_cvt_pk_bf16_f32 v125,v24,v25
M 30,a[244:247],90
L 2
s_cmp_eq_u32 vcc_lo,-1
R 50,1,4096
buffer_store_dwordx4 v[122:125],v0,s[36:39],s66 offen offset:0 sc0 sc1
M 30,a[252:255],94
D 0,56,2048
D 0,56,3072
buffer_store_dword v4,v3,s[36:39],s71 offen offset:0 sc0 sc1
buffer_store_dword v4,v3,s[36:39],s71 offen offset:0 sc0 sc1
s_cbranch_scc1 Lg3o_F0_%=
s_branch Lg3o_K0_%=
Lg3o_X1_%=:
M 30,a[124:127],94
L 3
s_add_u32 m0,s59,1024
v_fma_f32 v24,-v98,v112,v24
M 30,a[132:135],34
R 54,2,21504
v_fma_f32 v25,-v98,v113,v25
M 30,a[140:143],38
buffer_load_dword v8,s[36:39],s58 offen offset:0 sc1 lds
M 26,a[128:131],34
R 58,2,22528
v_mul_f32 v118,v22,v22
M 26,a[136:139],38
L 3
v_exp_f32 v116,v116
M 26,a[144:147],42
R 62,2,23552
v_fma_f32 v116,v116,v101,v101
M 26,a[152:155],46
v_exp_f32 v117,v117
M 30,a[148:151],42
R 66,2,24576
v_fma_f32 v117,v117,v101,v101
M 30,a[156:159],46
L 3
v_rcp_f32 v114,v114
v_mul_f32 v119,v23,v23
M 30,a[164:167],50
R 70,2,25600
v_mul_f32 v18,v18,v114
M 30,a[172:175],54
v_rcp_f32 v115,v115
M 26,a[160:163],50
R 74,2,26624
v_mul_f32 v19,v19,v115
M 26,a[168:171],54
L 3
v_rcp_f32 v116,v116
M 26,a[176:179],58
R 78,2,27648
v_mul_f32 v20,v20,v116
M 26,a[184:187],62
v_rcp_f32 v117,v117
M 30,a[180:183],58
R 82,2,28672
v_mul_f32 v21,v21,v117
M 30,a[188:191],62
L 3
v_cvt_pk_bf16_f32 v122,v18,v19
v_cvt_pk_bf16_f32 v123,v20,v21
M 30,a[196:199],66
R 86,2,29696
v_mul_f32 v120,v24,v24
M 30,a[204:207],70
v_mul_f32 v121,v25,v25
v_fma_f32 v118,v100,v118,v99
M 26,a[192:195],66
R 90,2,30720
v_fma_f32 v119,v100,v119,v99
M 26,a[200:203],70
L 3
s_nop 0
v_fma_f32 v120,v100,v120,v99
M 26,a[208:211],74
s_waitcnt vmcnt(0)
R 94,2,31744
v_fma_f32 v121,v100,v121,v99
M 26,a[216:219],78
s_barrier
buffer_store_dword v4,v3,s[36:39],s70 offen offset:0 sc0 sc1
M 30,a[212:215],74
s_mov_b32 s70,s69
ds_read_b32 v7,v6 offset:1024
R 102,13,2048
M 30,a[220:223],78
L 4
v_mul_f32 v118,v22,v118
v_mul_f32 v119,v23,v119
M 30,a[228:231],82
s_mov_b32 s69,s68
v_mul_f32 v120,v24,v120
v_mul_f32 v121,v25,v121
M 30,a[236:239],86
s_add_u32 s68,s68,s63
v_exp_f32 v118,v118
M 26,a[224:227],82
v_exp_f32 v119,v119
M 26,a[232:235],86
L 2
v_exp_f32 v120,v120
v_exp_f32 v121,v121
M 26,a[240:243],90
L 1
v_fma_f32 v118,v118,v101,v101
v_fma_f32 v119,v119,v101,v101
v_fma_f32 v120,v120,v101,v101
v_fma_f32 v121,v121,v101,v101
v_rcp_f32 v118,v118
v_rcp_f32 v119,v119
v_rcp_f32 v120,v120
v_rcp_f32 v121,v121
M 26,a[248:251],94
v_mul_f32 v22,v22,v118
v_mul_f32 v23,v23,v119
v_mul_f32 v24,v24,v120
v_mul_f32 v25,v25,v121
v_cvt_pk_bf16_f32 v124,v22,v23
v_cvt_pk_bf16_f32 v125,v24,v25
M 30,a[244:247],90
buffer_store_dwordx4 v[122:125],v0,s[36:39],s66 offen offset:0 sc0 sc1
v_cmp_eq_u32 vcc,s53,v7
M 30,a[252:255],94
s_mov_b32 s80,0
s_cmp_eq_u32 vcc_lo,-1
s_cbranch_scc0 Lg3o_xn1_%=
buffer_load_dword v130,v11,s[36:39],s73 offen offset:0 sc1
L 0
v_fma_f32 v26,-v102,v106,v26
v_fma_f32 v27,-v102,v107,v27
v_fma_f32 v28,-v102,v108,v28
v_fma_f32 v29,-v102,v109,v29
v_mul_f32 v114,v26,v26
v_mul_f32 v115,v27,v27
s_add_u32 s73,s73,s64
s_add_u32 m0,s57,0
s_add_u32 s56,s55,0
D 0,56,0
v_mul_f32 v116,v28,v28
v_mul_f32 v117,v29,v29
v_fma_f32 v114,v104,v114,v103
v_fma_f32 v115,v104,v115,v103
v_fma_f32 v116,v104,v116,v103
v_fma_f32 v117,v104,v117,v103
D 0,56,1024
v_mul_f32 v114,v26,v114
v_mul_f32 v115,v27,v115
v_mul_f32 v116,v28,v116
v_mul_f32 v117,v29,v117
v_exp_f32 v114,v114
v_exp_f32 v115,v115
D 0,56,2048
v_exp_f32 v116,v116
v_exp_f32 v117,v117
v_fma_f32 v114,v114,v105,v105
v_fma_f32 v115,v115,v105,v105
v_fma_f32 v116,v116,v105,v105
v_fma_f32 v117,v117,v105,v105
D 0,56,3072
v_rcp_f32 v114,v114
v_rcp_f32 v115,v115
v_rcp_f32 v116,v116
v_rcp_f32 v117,v117
v_mul_f32 v26,v26,v114
v_mul_f32 v27,v27,v115
s_add_u32 s58,s58,s61
s_add_u32 m0,s57,16384
s_add_u32 s56,s55,16384
D 0,56,0
v_mul_f32 v28,v28,v116
v_mul_f32 v29,v29,v117
v_cvt_pk_bf16_f32 v122,v26,v27
v_cvt_pk_bf16_f32 v123,v28,v29
v_fma_f32 v30,-v102,v110,v30
v_fma_f32 v31,-v102,v111,v31
D 0,56,1024
v_fma_f32 v32,-v102,v112,v32
v_fma_f32 v33,-v102,v113,v33
v_mul_f32 v118,v30,v30
v_mul_f32 v119,v31,v31
v_mul_f32 v120,v32,v32
v_mul_f32 v121,v33,v33
D 0,56,2048
v_fma_f32 v118,v104,v118,v103
v_fma_f32 v119,v104,v119,v103
v_fma_f32 v120,v104,v120,v103
v_fma_f32 v121,v104,v121,v103
v_mul_f32 v118,v30,v118
v_mul_f32 v119,v31,v119
D 0,56,3072
v_mul_f32 v120,v32,v120
v_mul_f32 v121,v33,v121
v_exp_f32 v118,v118
v_exp_f32 v119,v119
v_exp_f32 v120,v120
v_exp_f32 v121,v121
s_add_u32 s55,s55,s60
s_sub_u32 s57,s84,s57
s_add_u32 s54,s54,s46
v_fma_f32 v118,v118,v105,v105
v_fma_f32 v119,v119,v105,v105
v_fma_f32 v120,v120,v105,v105
v_fma_f32 v121,v121,v105,v105
v_rcp_f32 v118,v118
v_rcp_f32 v119,v119
v_rcp_f32 v120,v120
v_rcp_f32 v121,v121
v_mul_f32 v30,v30,v118
v_mul_f32 v31,v31,v119
v_mul_f32 v32,v32,v120
v_mul_f32 v33,v33,v121
v_cvt_pk_bf16_f32 v124,v30,v31
v_cvt_pk_bf16_f32 v125,v32,v33
buffer_store_dwordx4 v[122:125],v0,s[36:39],s67 offen offset:0 sc0 sc1
s_mov_b32 s72,s69
s_mov_b32 s69,0x7fff0000
s_mov_b32 s67,0x7fff0000
s_mov_b32 s77,0x7fff0000
s_branch Lg3o_gox_%=
Lg3o_xn1_%=:
s_cmp_ge_u32 s54,256
s_cbranch_scc1 Lg3o_fin1_%=
s_add_u32 m0,s59,4096
s_nop 0
buffer_load_dword v8,s[36:39],s58 offen offset:0 sc1 lds
buffer_store_dword v4,v3,s[36:39],s71 offen offset:0 sc0 sc1
s_add_u32 m0,s59,5120
s_nop 0
buffer_load_dword v8,s[36:39],s58 offen offset:0 sc1 lds
buffer_store_dword v4,v3,s[36:39],s71 offen offset:0 sc0 sc1
s_mov_b32 s83,0
L 0
v_fma_f32 v26,-v102,v106,v26
v_fma_f32 v27,-v102,v107,v27
v_fma_f32 v28,-v102,v108,v28
v_fma_f32 v29,-v102,v109,v29
v_mul_f32 v114,v26,v26
v_mul_f32 v115,v27,v27
v_mul_f32 v116,v28,v28
v_mul_f32 v117,v29,v29
v_fma_f32 v114,v104,v114,v103
v_fma_f32 v115,v104,v115,v103
v_fma_f32 v116,v104,v116,v103
v_fma_f32 v117,v104,v117,v103
v_mul_f32 v114,v26,v114
v_mul_f32 v115,v27,v115
v_mul_f32 v116,v28,v116
v_mul_f32 v117,v29,v117
v_exp_f32 v114,v114
v_exp_f32 v115,v115
v_exp_f32 v116,v116
v_exp_f32 v117,v117
v_fma_f32 v114,v114,v105,v105
v_fma_f32 v115,v115,v105,v105
v_fma_f32 v116,v116,v105,v105
v_fma_f32 v117,v117,v105,v105
v_rcp_f32 v114,v114
v_rcp_f32 v115,v115
v_rcp_f32 v116,v116
v_rcp_f32 v117,v117
v_mul_f32 v26,v26,v114
v_mul_f32 v27,v27,v115
v_mul_f32 v28,v28,v116
v_mul_f32 v29,v29,v117
v_cvt_pk_bf16_f32 v122,v26,v27
v_cvt_pk_bf16_f32 v123,v28,v29
v_fma_f32 v30,-v102,v110,v30
v_fma_f32 v31,-v102,v111,v31
v_fma_f32 v32,-v102,v112,v32
v_fma_f32 v33,-v102,v113,v33
v_mul_f32 v118,v30,v30
v_mul_f32 v119,v31,v31
v_mul_f32 v120,v32,v32
v_mul_f32 v121,v33,v33
v_fma_f32 v118,v104,v118,v103
v_fma_f32 v119,v104,v119,v103
v_fma_f32 v120,v104,v120,v103
v_fma_f32 v121,v104,v121,v103
v_mul_f32 v118,v30,v118
v_mul_f32 v119,v31,v119
v_mul_f32 v120,v32,v120
v_mul_f32 v121,v33,v121
v_exp_f32 v118,v118
v_exp_f32 v119,v119
v_exp_f32 v120,v120
v_exp_f32 v121,v121
v_fma_f32 v118,v118,v105,v105
v_fma_f32 v119,v119,v105,v105
v_fma_f32 v120,v120,v105,v105
v_fma_f32 v121,v121,v105,v105
v_rcp_f32 v118,v118
v_rcp_f32 v119,v119
v_rcp_f32 v120,v120
v_rcp_f32 v121,v121
v_mul_f32 v30,v30,v118
v_mul_f32 v31,v31,v119
v_mul_f32 v32,v32,v120
v_mul_f32 v33,v33,v121
v_cvt_pk_bf16_f32 v124,v30,v31
v_cvt_pk_bf16_f32 v125,v32,v33
buffer_store_dwordx4 v[122:125],v0,s[36:39],s67 offen offset:0 sc0 sc1
s_mov_b32 s72,s69
s_mov_b32 s69,0x7fff0000
s_mov_b32 s67,0x7fff0000
s_mov_b32 s77,0x7fff0000
s_branch Lg3o_poll_%=
Lg3o_fin1_%=:
L 0
v_fma_f32 v26,-v102,v106,v26
v_fma_f32 v27,-v102,v107,v27
v_fma_f32 v28,-v102,v108,v28
v_fma_f32 v29,-v102,v109,v29
v_mul_f32 v114,v26,v26
v_mul_f32 v115,v27,v27
v_mul_f32 v116,v28,v28
v_mul_f32 v117,v29,v29
v_fma_f32 v114,v104,v114,v103
v_fma_f32 v115,v104,v115,v103
v_fma_f32 v116,v104,v116,v103
v_fma_f32 v117,v104,v117,v103
v_mul_f32 v114,v26,v114
v_mul_f32 v115,v27,v115
v_mul_f32 v116,v28,v116
v_mul_f32 v117,v29,v117
v_exp_f32 v114,v114
v_exp_f32 v115,v115
v_exp_f32 v116,v116
v_exp_f32 v117,v117
v_fma_f32 v114,v114,v105,v105
v_fma_f32 v115,v115,v105,v105
v_fma_f32 v116,v116,v105,v105
v_fma_f32 v117,v117,v105,v105
v_rcp_f32 v114,v114
v_rcp_f32 v115,v115
v_rcp_f32 v116,v116
v_rcp_f32 v117,v117
v_mul_f32 v26,v26,v114
v_mul_f32 v27,v27,v115
v_mul_f32 v28,v28,v116
v_mul_f32 v29,v29,v117
v_cvt_pk_bf16_f32 v122,v26,v27
v_cvt_pk_bf16_f32 v123,v28,v29
v_fma_f32 v30,-v102,v110,v30
v_fma_f32 v31,-v102,v111,v31
v_fma_f32 v32,-v102,v112,v32
v_fma_f32 v33,-v102,v113,v33
v_mul_f32 v118,v30,v30
v_mul_f32 v119,v31,v31
v_mul_f32 v120,v32,v32
v_mul_f32 v121,v33,v33
v_fma_f32 v118,v104,v118,v103
v_fma_f32 v119,v104,v119,v103
v_fma_f32 v120,v104,v120,v103
v_fma_f32 v121,v104,v121,v103
v_mul_f32 v118,v30,v118
v_mul_f32 v119,v31,v119
v_mul_f32 v120,v32,v120
v_mul_f32 v121,v33,v121
v_exp_f32 v118,v118
v_exp_f32 v119,v119
v_exp_f32 v120,v120
v_exp_f32 v121,v121
v_fma_f32 v118,v118,v105,v105
v_fma_f32 v119,v119,v105,v105
v_fma_f32 v120,v120,v105,v105
v_fma_f32 v121,v121,v105,v105
v_rcp_f32 v118,v118
v_rcp_f32 v119,v119
v_rcp_f32 v120,v120
v_rcp_f32 v121,v121
v_mul_f32 v30,v30,v118
v_mul_f32 v31,v31,v119
v_mul_f32 v32,v32,v120
v_mul_f32 v33,v33,v121
v_cvt_pk_bf16_f32 v124,v30,v31
v_cvt_pk_bf16_f32 v125,v32,v33
buffer_store_dwordx4 v[122:125],v0,s[36:39],s67 offen offset:0 sc0 sc1
s_mov_b32 s72,s69
s_mov_b32 s69,0x7fff0000
s_mov_b32 s67,0x7fff0000
s_mov_b32 s77,0x7fff0000
s_branch Lg3o_done_%=
Lg3o_done_%=:
s_waitcnt vmcnt(1)
s_barrier
buffer_store_dword v4,v3,s[36:39],s70 offen offset:0 sc0 sc1
s_waitcnt vmcnt(1)
s_barrier
buffer_store_dword v4,v3,s[36:39],s72 offen offset:0 sc0 sc1
s_waitcnt vmcnt(0) lgkmcnt(0)
s_barrier
s_endpgm
Lcode_g3o_e:
.purgem M
.purgem R
.purgem T
.purgem D
.purgem L
)"
  :: "s"(ws), "s"(__builtin_amdgcn_readfirstlane(sWave)), "s"(__builtin_amdgcn_readfirstlane(sG0)), "s"(__builtin_amdgcn_readfirstlane(sStep)), "s"(__builtin_amdgcn_readfirstlane(sOutOff)), "s"(__builtin_amdgcn_readfirstlane(sFoutOff)), "s"(__builtin_amdgcn_readfirstlane(sAuxOff)), "s"(__builtin_amdgcn_readfirstlane(sWw)), "s"(__builtin_amdgcn_readfirstlane(sBuf)), "s"(__builtin_amdgcn_readfirstlane(sExp)), "v"(vL16), "v"(vFlag), "v"(vSnapOff), "v"(vSnapRd), "v"(vWd0), "v"(vWd1), "v"(vWr0), "v"(vWr1), "v"(vWr2), "v"(vOutS), "v"(vAux0), "v"(vAux1), "v"(vAux2), "v"(vAux3), "v"(vAn), "v"(vAe), "s"(w3), "s"(out) : "v255", "v0", "v1", "v2", "v3", "v4", "v5", "v6", "v7", "v8", "v9", "v10", "v11", "v12", "v13", "v14", "v15", "v16", "v17", "v18", "v19", "v20", "v21", "v22", "v23", "v24", "v25", "v26", "v27", "v28", "v29", "v30", "v31", "v32", "v33", "v34", "v35", "v36", "v37", "v38", "v39", "v40", "v41", "v42", "v43", "v44", "v45", "v46", "v47", "v48", "v49", "v50", "v51", "v52", "v53", "v54", "v55", "v56", "v57", "v58", "v59", "v60", "v61", "v62", "v63", "v64", "v65", "v66", "v67", "v68", "v69", "v70", "v71", "v72", "v73", "v74", "v75", "v76", "v77", "v78", "v79", "v80", "v81", "v82", "v83", "v84", "v85", "v86", "v87", "v88", "v89", "v90", "v91", "v92", "v93", "v94", "v95", "v96", "v97", "v98", "v99", "v100", "v101", "v102", "v103", "v104", "v105", "v106", "v107", "v108", "v109", "v110", "v111", "v112", "v113", "v114", "v115", "v116", "v117", "v118", "v119", "v120", "v121", "v122", "v123", "v124", "v125", "v126", "v127", "v128", "v129", "v130", "v131", "v132", "v133", "v134", "v135", "v136", "v137", "v138", "v139", "v140", "v141", "v142", "v143", "v144", "v145", "v146", "v147", "v148", "v149", "v150", "v151", "v152", "v153", "v154", "v155", "v156", "v157", "v158", "v159", "v160", "v161", "v162", "v163", "v164", "v165", "v166", "v167", "v168", "v169", "v170", "v171", "v172", "v173", "v174", "v175", "v176", "v177", "v178", "v179", "v180", "v181", "v182", "v183", "v184", "v185", "v186", "v187", "v188", "v189", "v190", "v191", "v192", "v193", "v194", "v195", "v196", "v197", "v198", "v199", "v200", "v201", "v202", "v203", "v204", "v205", "v206", "v207", "v208", "v209", "v210", "v211", "v212", "v213", "v214", "v215", "v216", "v217", "v218", "v219", "v220", "v221", "v222", "v223", "v224", "v225", "v226", "v227", "v228", "v229", "v230", "v231", "a0", "a1", "a2", "a3", "a4", "a5", "a6", "a7", "a8", "a9", "a10", "a11", "a12", "a13", "a14", "a15", "a16", "a17", "a18", "a19", "a20", "a21", "a22", "a23", "a24", "a25", "a26", "a27", "a28", "a29", "a30", "a31", "a32", "a33", "a34", "a35", "a36", "a37", "a38", "a39", "a40", "a41", "a42", "a43", "a44", "a45", "a46", "a47", "a48", "a49", "a50", "a51", "a52", "a53", "a54", "a55", "a56", "a57", "a58", "a59", "a60", "a61", "a62", "a63", "a64", "a65", "a66", "a67", "a68", "a69", "a70", "a71", "a72", "a73", "a74", "a75", "a76", "a77", "a78", "a79", "a80", "a81", "a82", "a83", "a84", "a85", "a86", "a87", "a88", "a89", "a90", "a91", "a92", "a93", "a94", "a95", "a96", "a97", "a98", "a99", "a100", "a101", "a102", "a103", "a104", "a105", "a106", "a107", "a108", "a109", "a110", "a111", "a112", "a113", "a114", "a115", "a116", "a117", "a118", "a119", "a120", "a121", "a122", "a123", "a124", "a125", "a126", "a127", "a128", "a129", "a130", "a131", "a132", "a133", "a134", "a135", "a136", "a137", "a138", "a139", "a140", "a141", "a142", "a143", "a144", "a145", "a146", "a147", "a148", "a149", "a150", "a151", "a152", "a153", "a154", "a155", "a156", "a157", "a158", "a159", "a160", "a161", "a162", "a163", "a164", "a165", "a166", "a167", "a168", "a169", "a170", "a171", "a172", "a173", "a174", "a175", "a176", "a177", "a178", "a179", "a180", "a181", "a182", "a183", "a184", "a185", "a186", "a187", "a188", "a189", "a190", "a191", "a192", "a193", "a194", "a195", "a196", "a197", "a198", "a199", "a200", "a201", "a202", "a203", "a204", "a205", "a206", "a207", "a208", "a209", "a210", "a211", "a212", "a213", "a214", "a215", "a216", "a217", "a218", "a219", "a220", "a221", "a222", "a223", "a224", "a225", "a226", "a227", "a228", "a229", "a230", "a231", "a232", "a233", "a234", "a235", "a236", "a237", "a238", "a239", "a240", "a241", "a242", "a243", "a244", "a245", "a246", "a247", "a248", "a249", "a250", "a251", "a252", "a253", "a254", "a255", "s36", "s37", "s38", "s39", "s40", "s41", "s42", "s43", "s44", "s45", "s46", "s47", "s48", "s49", "s50", "s51", "s52", "s53", "s54", "s55", "s56", "s57", "s58", "s59", "s60", "s61", "s62", "s63", "s64", "s65", "s66", "s67", "s68", "s69", "s70", "s71", "s72", "s73", "s74", "s75", "s76", "s77", "s78", "s79", "s80", "s81", "s82", "s83", "s84", "s85", "s86", "s87", "s88", "s89", "s90", "s91", "s92", "s93", "s94", "s95", "s96", "s97", "vcc", "scc", "memory"); }
    __builtin_unreachable();
  }
  if (kind < 3) {
    const unsigned sStep = 4;
    COMMON(kind == 1 ? 8 : 16, kind == 1 ? fj * 128 : fj < 8 ? 1024 + fj * 128 : (fj & 2) * 1024 + (fj - 8) * 128, 1024, 131072, 2048, 256, kind == 1 ? 0 : 32768);
    const int sr = l15 - ((wave & 2) ? -1 : 1), sr2 = l15 - ((wave & 2) ? -2 : 2), nx = (slice >> 1) & 1;
    auto src = [&](int r_) { return (unsigned)((r_ & 16) ? 1024 + lg * 64 + (r_ & 3) * 16 : (lg * 16 + r_) * 16); };
    const unsigned vOutS = src(sr2), vSp = (sr2 & 16) ? OOB : vOutS;
    (void)vWr2;
    const unsigned vAn = kind == 1 ? HU + vL16 : src(sr);
    const unsigned vAe = kind == 1 ? ((l15 >> 2) == (nx ? 0 : 3) ? (nx ? 0 : 2 * HU) + 1024 + lg * 64 + (l15 & 3) * 16 : OOB) : (sr & 16) ? OOB : vAn;
    const unsigned vAux0 = l15 * 16, vAux1 = 139264 + (l15 * 2 + (lg & 1)) * 16 + wave * 4 + (lg >> 1) * 512, vAux2 = 139264 + (l15 * 2 + (lg & 1)) * 16;
    const unsigned vAux3 = (wave == 0 && lg < 2) ? slice * 128 + l15 * 8 + lg * 4 : OOB;
    const unsigned sOutOff = kind == 1 ? OFF_HS - HU + (slice * 4 + wave) * 1280 : OFF_X1 + (slice * 4 + wave) * 1024, sFoutOff = (kind == 1 ? OFF_FH : OFF_FX1) + slice * 128;
    const unsigned sAuxOff = OFF_XP + (slice * 4 + wave) * 1024, sBuf = kind, sWw = 16 * wave * 2048 + slice * 256;
    const u16* w12 = kind == 1 ? w1 : w2;
    const unsigned sExp = __builtin_amdgcn_readfirstlane(ep);
    unsigned epOut;
    asm volatile(
R"(.macro M d,w,x
v_mfma_f32_16x16x32_bf16 v[\d:\d+3], \w, v[\x:\x+3], v[\d:\d+3]
.endm
.macro R d,a,o
ds_read_b128 v[\d:\d+3], v\a offset:\o
.endm
.macro T d,a,o
ds_read_b64_tr_b16 \d, v\a offset:\o
.endm
.macro D a,s,o
buffer_load_dwordx4 v\a, s[36:39], s\s offen offset:\o sc1 lds
.endm
.macro L c
s_waitcnt lgkmcnt(\c)
.endm
Lcode_g12_b:
s_cmp_eq_u32 %9,2
s_cbranch_scc1 Lg2_begin_%=
s_mov_b64 s[36:37],%1
s_mov_b32 s38,111243392
s_mov_b32 s39,0x00020000
s_mov_b32 s44,%2
s_mov_b32 s45,%3
s_mov_b32 s46,%4
s_mov_b32 s47,%5
s_mov_b32 s48,%6
s_mov_b32 s49,%7
s_mov_b32 s50,%8
s_mov_b32 s51,%9
s_mov_b32 s53,%10
v_mov_b32 v0,%11
v_mov_b32 v3,%12
v_mov_b32 v5,%13
v_mov_b32 v6,%14
v_mov_b32 v18,%15
v_mov_b32 v19,%16
v_mov_b32 v20,%17
v_mov_b32 v21,%18
v_mov_b32 v9,%19
v_mov_b32 v10,%20
v_mov_b32 v11,%21
v_mov_b32 v12,%22
v_mov_b32 v13,%23
v_mov_b32 v14,%24
v_mov_b32 v15,%25
v_mov_b32 v16,%26
s_mov_b64 s[40:41],%27
s_mov_b32 s42,2097152
s_mov_b32 s43,0x00020000
s_lshl_b32 s57,s44,11
s_waitcnt vmcnt(20) lgkmcnt(0)
s_barrier
s_add_u32 s55,s50,786432
s_add_u32 m0,s57,98304
s_nop 0
buffer_load_dwordx4 v18,s[40:43],s55 offen offset:0  lds
s_add_u32 m0,s57,99328
s_nop 0
buffer_load_dwordx4 v19,s[40:43],s55 offen offset:0  lds
s_add_u32 m0,s57,106368
s_nop 0
buffer_load_dwordx4 v18,s[40:43],s55 offen offset:128  lds
s_add_u32 m0,s57,107392
s_nop 0
buffer_load_dwordx4 v19,s[40:43],s55 offen offset:128  lds
v_add_u32 v26,0,v20
v_add_u32 v27,0,v21
T a[0:1],26,0
T a[2:3],26,512
T a[4:5],27,0
T a[6:7],27,512
T a[8:9],26,4096
T a[10:11],26,4608
T a[12:13],27,4096
T a[14:15],27,4608
s_waitcnt vmcnt(20) lgkmcnt(0)
s_barrier
s_add_u32 s55,s50,917504
s_add_u32 m0,s57,0
s_nop 0
buffer_load_dwordx4 v18,s[40:43],s55 offen offset:0  lds
s_add_u32 m0,s57,1024
s_nop 0
buffer_load_dwordx4 v19,s[40:43],s55 offen offset:0  lds
s_add_u32 m0,s57,8064
s_nop 0
buffer_load_dwordx4 v18,s[40:43],s55 offen offset:128  lds
s_add_u32 m0,s57,9088
s_nop 0
buffer_load_dwordx4 v19,s[40:43],s55 offen offset:128  lds
v_add_u32 v26,16384,v20
v_add_u32 v27,16384,v21
T a[16:17],26,0
T a[18:19],26,512
T a[20:21],27,0
T a[22:23],27,512
T a[24:25],26,4096
T a[26:27],26,4608
T a[28:29],27,4096
T a[30:31],27,4608
s_waitcnt vmcnt(20) lgkmcnt(0)
s_barrier
s_add_u32 s55,s50,1048576
s_add_u32 m0,s57,16384
s_nop 0
buffer_load_dwordx4 v18,s[40:43],s55 offen offset:0  lds
s_add_u32 m0,s57,17408
s_nop 0
buffer_load_dwordx4 v19,s[40:43],s55 offen offset:0  lds
s_add_u32 m0,s57,24448
s_nop 0
buffer_load_dwordx4 v18,s[40:43],s55 offen offset:128  lds
s_add_u32 m0,s57,25472
s_nop 0
buffer_load_dwordx4 v19,s[40:43],s55 offen offset:128  lds
v_add_u32 v26,32768,v20
v_add_u32 v27,32768,v21
T a[32:33],26,0
T a[34:35],26,512
T a[36:37],27,0
T a[38:39],27,512
T a[40:41],26,4096
T a[42:43],26,4608
T a[44:45],27,4096
T a[46:47],27,4608
s_waitcnt vmcnt(20) lgkmcnt(0)
s_barrier
s_add_u32 s55,s50,1179648
s_add_u32 m0,s57,32768
s_nop 0
buffer_load_dwordx4 v18,s[40:43],s55 offen offset:0  lds
s_add_u32 m0,s57,33792
s_nop 0
buffer_load_dwordx4 v19,s[40:43],s55 offen offset:0  lds
s_add_u32 m0,s57,40832
s_nop 0
buffer_load_dwordx4 v18,s[40:43],s55 offen offset:128  lds
s_add_u32 m0,s57,41856
s_nop 0
buffer_load_dwordx4 v19,s[40:43],s55 offen offset:128  lds
v_add_u32 v26,49152,v20
v_add_u32 v27,49152,v21
T a[48:49],26,0
T a[50:51],26,512
T a[52:53],27,0
T a[54:55],27,512
T a[56:57],26,4096
T a[58:59],26,4608
T a[60:61],27,4096
T a[62:63],27,4608
s_waitcnt vmcnt(20) lgkmcnt(0)
s_barrier
s_add_u32 s55,s50,1310720
s_add_u32 m0,s57,49152
s_nop 0
buffer_load_dwordx4 v18,s[40:43],s55 offen offset:0  lds
s_add_u32 m0,s57,50176
s_nop 0
buffer_load_dwordx4 v19,s[40:43],s55 offen offset:0  lds
s_add_u32 m0,s57,57216
s_nop 0
buffer_load_dwordx4 v18,s[40:43],s55 offen offset:128  lds
s_add_u32 m0,s57,58240
s_nop 0
buffer_load_dwordx4 v19,s[40:43],s55 offen offset:128  lds
v_add_u32 v26,65536,v20
v_add_u32 v27,65536,v21
T a[64:65],26,0
T a[66:67],26,512
T a[68:69],27,0
T a[70:71],27,512
T a[72:73],26,4096
T a[74:75],26,4608
T a[76:77],27,4096
T a[78:79],27,4608
s_waitcnt vmcnt(20) lgkmcnt(0)
s_barrier
s_add_u32 s55,s50,1441792
s_add_u32 m0,s57,65536
s_nop 0
buffer_load_dwordx4 v18,s[40:43],s55 offen offset:0  lds
s_add_u32 m0,s57,66560
s_nop 0
buffer_load_dwordx4 v19,s[40:43],s55 offen offset:0  lds
s_add_u32 m0,s57,73600
s_nop 0
buffer_load_dwordx4 v18,s[40:43],s55 offen offset:128  lds
s_add_u32 m0,s57,74624
s_nop 0
buffer_load_dwordx4 v19,s[40:43],s55 offen offset:128  lds
v_add_u32 v26,81920,v20
v_add_u32 v27,81920,v21
T a[80:81],26,0
T a[82:83],26,512
T a[84:85],27,0
T a[86:87],27,512
T a[88:89],26,4096
T a[90:91],26,4608
T a[92:93],27,4096
T a[94:95],27,4608
s_waitcnt vmcnt(20) lgkmcnt(0)
s_barrier
s_add_u32 s55,s50,1572864
s_add_u32 m0,s57,81920
s_nop 0
buffer_load_dwordx4 v18,s[40:43],s55 offen offset:0  lds
s_add_u32 m0,s57,82944
s_nop 0
buffer_load_dwordx4 v19,s[40:43],s55 offen offset:0  lds
s_add_u32 m0,s57,89984
s_nop 0
buffer_load_dwordx4 v18,s[40:43],s55 offen offset:128  lds
s_add_u32 m0,s57,91008
s_nop 0
buffer_load_dwordx4 v19,s[40:43],s55 offen offset:128  lds
v_add_u32 v26,98304,v20
v_add_u32 v27,98304,v21
T a[96:97],26,0
T a[98:99],26,512
T a[100:101],27,0
T a[102:103],27,512
T a[104:105],26,4096
T a[106:107],26,4608
T a[108:109],27,4096
T a[110:111],27,4608
s_waitcnt vmcnt(20) lgkmcnt(0)
s_barrier
s_add_u32 s55,s50,1703936
s_add_u32 m0,s57,98304
s_nop 0
buffer_load_dwordx4 v18,s[40:43],s55 offen offset:0  lds
s_add_u32 m0,s57,99328
s_nop 0
buffer_load_dwordx4 v19,s[40:43],s55 offen offset:0  lds
s_add_u32 m0,s57,106368
s_nop 0
buffer_load_dwordx4 v18,s[40:43],s55 offen offset:128  lds
s_add_u32 m0,s57,107392
s_nop 0
buffer_load_dwordx4 v19,s[40:43],s55 offen offset:128  lds
v_add_u32 v26,0,v20
v_add_u32 v27,0,v21
T a[112:113],26,0
T a[114:115],26,512
T a[116:117],27,0
T a[118:119],27,512
T a[120:121],26,4096
T a[122:123],26,4608
T a[124:125],27,4096
T a[126:127],27,4608
s_waitcnt vmcnt(20) lgkmcnt(0)
s_barrier
s_add_u32 s55,s50,1835008
s_add_u32 m0,s57,0
s_nop 0
buffer_load_dwordx4 v18,s[40:43],s55 offen offset:0  lds
s_add_u32 m0,s57,1024
s_nop 0
buffer_load_dwordx4 v19,s[40:43],s55 offen offset:0  lds
s_add_u32 m0,s57,8064
s_nop 0
buffer_load_dwordx4 v18,s[40:43],s55 offen offset:128  lds
s_add_u32 m0,s57,9088
s_nop 0
buffer_load_dwordx4 v19,s[40:43],s55 offen offset:128  lds
v_add_u32 v26,16384,v20
v_add_u32 v27,16384,v21
T a[128:129],26,0
T a[130:131],26,512
T a[132:133],27,0
T a[134:135],27,512
T a[136:137],26,4096
T a[138:139],26,4608
T a[140:141],27,4096
T a[142:143],27,4608
s_waitcnt vmcnt(20) lgkmcnt(0)
s_barrier
s_add_u32 s55,s50,1966080
s_add_u32 m0,s57,16384
s_nop 0
buffer_load_dwordx4 v18,s[40:43],s55 offen offset:0  lds
s_add_u32 m0,s57,17408
s_nop 0
buffer_load_dwordx4 v19,s[40:43],s55 offen offset:0  lds
s_add_u32 m0,s57,24448
s_nop 0
buffer_load_dwordx4 v18,s[40:43],s55 offen offset:128  lds
s_add_u32 m0,s57,25472
s_nop 0
buffer_load_dwordx4 v19,s[40:43],s55 offen offset:128  lds
v_add_u32 v26,32768,v20
v_add_u32 v27,32768,v21
T a[144:145],26,0
T a[146:147],26,512
T a[148:149],27,0
T a[150:151],27,512
T a[152:153],26,4096
T a[154:155],26,4608
T a[156:157],27,4096
T a[158:159],27,4608
s_waitcnt vmcnt(20) lgkmcnt(0)
s_barrier
v_add_u32 v26,49152,v20
v_add_u32 v27,49152,v21
T a[160:161],26,0
T a[162:163],26,512
T a[164:165],27,0
T a[166:167],27,512
T a[168:169],26,4096
T a[170:171],26,4608
T a[172:173],27,4096
T a[174:175],27,4608
s_waitcnt vmcnt(16) lgkmcnt(0)
s_barrier
v_add_u32 v26,65536,v20
v_add_u32 v27,65536,v21
T a[176:177],26,0
T a[178:179],26,512
T a[180:181],27,0
T a[182:183],27,512
T a[184:185],26,4096
T a[186:187],26,4608
T a[188:189],27,4096
T a[190:191],27,4608
s_waitcnt vmcnt(12) lgkmcnt(0)
s_barrier
v_add_u32 v26,81920,v20
v_add_u32 v27,81920,v21
T a[192:193],26,0
T a[194:195],26,512
T a[196:197],27,0
T a[198:199],27,512
T a[200:201],26,4096
T a[202:203],26,4608
T a[204:205],27,4096
T a[206:207],27,4608
s_waitcnt vmcnt(8) lgkmcnt(0)
s_barrier
v_add_u32 v26,98304,v20
v_add_u32 v27,98304,v21
T a[208:209],26,0
T a[210:211],26,512
T a[212:213],27,0
T a[214:215],27,512
T a[216:217],26,4096
T a[218:219],26,4608
T a[220:221],27,4096
T a[222:223],27,4608
s_waitcnt vmcnt(4) lgkmcnt(0)
s_barrier
v_add_u32 v26,0,v20
v_add_u32 v27,0,v21
T a[224:225],26,0
T a[226:227],26,512
T a[228:229],27,0
T a[230:231],27,512
T a[232:233],26,4096
T a[234:235],26,4608
T a[236:237],27,4096
T a[238:239],27,4608
s_waitcnt vmcnt(0) lgkmcnt(0)
s_barrier
v_add_u32 v26,16384,v20
v_add_u32 v27,16384,v21
T a[240:241],26,0
T a[242:243],26,512
T a[244:245],27,0
T a[246:247],27,512
T a[248:249],26,4096
T a[250:251],26,4608
T a[252:253],27,4096
T a[254:255],27,4608
L 0
s_barrier
v_mov_b32 v34,0x3f803f80
v_mov_b32 v35,0x3f803f80
v_mov_b32 v36,0x3f803f80
v_mov_b32 v37,0x3f803f80
s_nop 1
v_mfma_f32_16x16x32_bf16 v[106:109],a[0:3],v[34:37],0
v_mfma_f32_16x16x32_bf16 v[110:113],a[4:7],v[34:37],0
M 106,a[8:11],34
M 110,a[12:15],34
M 106,a[16:19],34
M 110,a[20:23],34
M 106,a[24:27],34
M 110,a[28:31],34
M 106,a[32:35],34
M 110,a[36:39],34
M 106,a[40:43],34
M 110,a[44:47],34
M 106,a[48:51],34
M 110,a[52:55],34
M 106,a[56:59],34
M 110,a[60:63],34
M 106,a[64:67],34
M 110,a[68:71],34
M 106,a[72:75],34
M 110,a[76:79],34
M 106,a[80:83],34
M 110,a[84:87],34
M 106,a[88:91],34
M 110,a[92:95],34
M 106,a[96:99],34
M 110,a[100:103],34
M 106,a[104:107],34
M 110,a[108:111],34
M 106,a[112:115],34
M 110,a[116:119],34
M 106,a[120:123],34
M 110,a[124:127],34
M 106,a[128:131],34
M 110,a[132:135],34
M 106,a[136:139],34
M 110,a[140:143],34
M 106,a[144:147],34
M 110,a[148:151],34
M 106,a[152:155],34
M 110,a[156:159],34
M 106,a[160:163],34
M 110,a[164:167],34
M 106,a[168:171],34
M 110,a[172:175],34
M 106,a[176:179],34
M 110,a[180:183],34
M 106,a[184:187],34
M 110,a[188:191],34
M 106,a[192:195],34
M 110,a[196:199],34
M 106,a[200:203],34
M 110,a[204:207],34
M 106,a[208:211],34
M 110,a[212:215],34
M 106,a[216:219],34
M 110,a[220:223],34
M 106,a[224:227],34
M 110,a[228:231],34
M 106,a[232:235],34
M 110,a[236:239],34
M 106,a[240:243],34
M 110,a[244:247],34
M 106,a[248:251],34
M 110,a[252:255],34
v_mov_b32 v4,s53
s_mov_b32 s82,0
s_mov_b32 s80,0
s_mov_b32 s54,s45
s_mul_i32 s57,s44,4096
s_mul_i32 s55,s45,32768
s_add_u32 s55,s55,s57
s_add_u32 s55,s55,8388608
s_mul_i32 s57,s44,4096
s_mul_i32 s60,s46,32768
s_mul_i32 s61,s46,1024
s_mul_i32 s62,s46,40960
s_mul_i32 s63,s46,1024
s_mul_i32 s64,s46,512
s_lshl_b32 s59,s44,8
s_add_u32 s59,s59,131072
s_mul_i32 s58,s45,1024
s_add_u32 s58,s58,109056000
s_mul_i32 s65,s45,40960
s_add_u32 s65,s65,s47
s_mul_i32 s68,s45,1024
s_add_u32 s68,s68,s48
s_mul_i32 s73,s45,512
s_add_u32 s73,s73,106954752
s_mul_i32 s74,s45,32768
s_add_u32 s74,s74,s49
s_mul_i32 s75,s45,1024
s_add_u32 s75,s75,107479040
s_mov_b32 s66,0x7fff0000
s_mov_b32 s67,0x7fff0000
s_mov_b32 s69,0x7fff0000
s_mov_b32 s76,0x7fff0000
s_mov_b32 s77,0x7fff0000
s_mov_b32 s70,0x7fff0000
s_mov_b32 s71,0x7fff0000
s_mov_b32 s72,0x7fff0000
v_mov_b32 v1,v0
v_add_u32 v2,16384,v0
s_mov_b32 s84,16384
s_mov_b32 s86,0x3a800000
s_mov_b32 s87,0x3727c5ac
s_mov_b32 s88,0xc0135761
s_mov_b32 s89,0x3d372713
s_lshr_b32 s85,s44,1
s_mul_i32 s85,s85,127
v_lshlrev_b32 v8,22,v0
v_and_b32 v8,0x80000000,v8
v_or_b32 v8,v8,v5
v_add_u32 v2,32768,v0
s_lshl_b32 s84,s57,1
s_add_u32 s84,s84,32768
s_getpc_b64 s[40:41]
Lg1_pc_%=:
s_lshl_b32 s78,s44,13
v_lshlrev_b32 v114,3,v0
v_add_u32 v114,s78,v114
v_min_u32 v114,Lcode_g2_b-Lg1_pc_%=-4,v114
)"
R"(s_mov_b32 m0,163584
s_nop 0
global_load_lds_dword v114,s[40:41]
s_add_u32 m0,s59,4096
s_nop 0
buffer_load_dword v8,s[36:39],s58 offen offset:0 sc1 lds
buffer_store_dword v4,v3,s[36:39],s71 offen offset:0 sc0 sc1
s_add_u32 m0,s59,5120
s_nop 0
buffer_load_dword v8,s[36:39],s58 offen offset:0 sc1 lds
buffer_store_dword v4,v3,s[36:39],s71 offen offset:0 sc0 sc1
s_mov_b32 s83,0
Lg1_poll_%=:
s_add_u32 s78,s83,2
s_and_b32 s78,s78,3
s_lshl_b32 s78,s78,10
s_add_u32 s78,s78,4096
s_add_u32 m0,s59,s78
s_nop 0
buffer_load_dword v8,s[36:39],s58 offen offset:0 sc1 lds
s_and_b32 s78,s83,3
s_lshl_b32 s78,s78,10
v_add_u32 v114,s78,v6
s_waitcnt vmcnt(4)
s_barrier
ds_read_b32 v7,v114 offset:4096
buffer_store_dword v4,v3,s[36:39],s70 offen offset:0 sc0 sc1
s_mov_b32 s70,s71
s_mov_b32 s71,s72
s_mov_b32 s72,0x7fff0000
s_add_u32 s83,s83,1
s_add_u32 s82,s82,1
L 0
v_cmp_eq_u32 vcc,s53,v7
s_nop 0
s_cmp_eq_u32 vcc_lo,-1
s_cbranch_scc1 Lg1_go_%=
s_cmp_lt_u32 s82,20000
s_cbranch_scc1 Lg1_poll_%=
Lg1_go_%=:
s_add_u32 m0,s57,0
s_add_u32 s56,s55,0
D 0,56,0
D 0,56,1024
D 0,56,2048
D 0,56,3072
s_add_u32 s58,s58,s61
s_add_u32 m0,s59,2048
s_nop 0
buffer_load_dword v8,s[36:39],s58 offen offset:0 sc1 lds
s_add_u32 m0,s57,16384
s_add_u32 s56,s55,16384
D 0,56,0
D 0,56,1024
D 0,56,2048
D 0,56,3072
s_add_u32 s55,s55,s60
s_sub_u32 s57,s84,s57
s_add_u32 s54,s54,s46
s_waitcnt vmcnt(4)
s_barrier
buffer_store_dword v4,v3,s[36:39],s70 offen offset:0 sc0 sc1
s_min_u32 s71,s71,s72
buffer_store_dword v4,v3,s[36:39],s71 offen offset:0 sc0 sc1
s_mov_b32 s70,0x7fff0000
s_mov_b32 s71,0x7fff0000
s_mov_b32 s72,0x7fff0000
s_cmp_eq_u32 s80,1
buffer_store_dword v4,v3,s[36:39],s71 offen offset:0 sc0 sc1
s_cbranch_scc1 Lg1_st1_%=
s_branch Lg1_st0_%=
Lg1_gox_%=:
s_add_u32 m0,s59,2048
s_nop 0
buffer_load_dword v8,s[36:39],s58 offen offset:0 sc1 lds
s_waitcnt vmcnt(0)
s_barrier
buffer_store_dword v4,v3,s[36:39],s70 offen offset:0 sc0 sc1
s_min_u32 s71,s71,s72
buffer_store_dword v4,v3,s[36:39],s71 offen offset:0 sc0 sc1
s_mov_b32 s70,0x7fff0000
s_mov_b32 s71,0x7fff0000
s_mov_b32 s72,0x7fff0000
s_cmp_eq_u32 s80,1
s_cbranch_scc1 Lg1_st1_%=
Lg1_st0_%=:
ds_read_b32 v7,v6 offset:2048
R 34,1,0
R 38,1,1024
R 42,1,2048
R 46,1,3072
R 50,1,4096
L 5
v_cmp_eq_u32 vcc,s53,v7
L 3
s_cmp_eq_u32 vcc_lo,-1
s_cbranch_scc1 Lg1_F0_%=
s_branch Lg1_L0_%=
Lg1_st1_%=:
ds_read_b32 v7,v6 offset:2048
R 34,2,0
R 38,2,1024
R 42,2,2048
R 46,2,3072
R 50,2,4096
L 5
v_cmp_eq_u32 vcc,s53,v7
L 3
s_cmp_eq_u32 vcc_lo,-1
s_cbranch_scc1 Lg1_F1_%=
s_branch Lg1_L1_%=
Lg1_F0_%=:
v_mfma_f32_16x16x32_bf16 v[22:25],a[4:7],v[34:37],0
s_add_u32 s58,s58,s61
s_add_u32 m0,s59,0
R 54,1,5120
M 22,a[12:15],38
buffer_load_dword v8,s[36:39],s58 offen offset:0 sc1 lds
v_mfma_f32_16x16x32_bf16 v[18:21],a[0:3],v[34:37],0
s_add_u32 m0,s57,0
R 58,1,6144
v_fma_f32 v26,-v102,v106,v26
M 18,a[8:11],38
L 3
s_add_u32 s56,s55,0
v_fma_f32 v27,-v102,v107,v27
M 18,a[16:19],42
s_mov_b32 s66,s65
R 62,1,7168
v_fma_f32 v28,-v102,v108,v28
M 18,a[24:27],46
D 0,56,0
M 22,a[20:23],42
s_add_u32 s65,s65,s62
R 66,1,8192
v_fma_f32 v29,-v102,v109,v29
v_mul_f32 v114,v26,v26
M 22,a[28:31],46
L 3
D 0,56,1024
M 22,a[36:39],50
s_add_u32 s54,s54,s46
R 70,1,9216
v_mul_f32 v115,v27,v27
M 22,a[44:47],54
v_mul_f32 v116,v28,v28
v_mul_f32 v117,v29,v29
M 18,a[32:35],50
R 74,1,10240
v_fma_f32 v114,v104,v114,v103
M 18,a[40:43],54
L 3
D 0,56,2048
M 18,a[48:51],58
R 78,1,11264
v_fma_f32 v115,v104,v115,v103
M 18,a[56:59],62
D 0,56,3072
v_fma_f32 v116,v104,v116,v103
M 22,a[52:55],58
s_add_u32 m0,s57,16384
R 82,1,12288
v_fma_f32 v117,v104,v117,v103
M 22,a[60:63],62
L 3
v_mul_f32 v114,v26,v114
v_mul_f32 v115,v27,v115
M 22,a[68:71],66
s_add_u32 s56,s55,16384
R 86,1,13312
v_mul_f32 v116,v28,v116
M 22,a[76:79],70
buffer_load_dwordx4 v[98:101],v11,s[36:39],s73 offen offset:0 sc1
M 18,a[64:67],66
s_add_u32 s73,s73,s64
R 90,1,14336
v_mul_f32 v117,v29,v117
M 18,a[72:75],70
L 3
v_exp_f32 v114,v114
v_fma_f32 v30,-v102,v110,v30
M 18,a[80:83],74
s_waitcnt vmcnt(9)
R 94,1,15360
v_fma_f32 v114,v114,v105,v105
M 18,a[88:91],78
s_barrier
v_exp_f32 v115,v115
M 22,a[84:87],74
s_add_u32 s55,s55,s60
R 34,1,16384
v_fma_f32 v115,v115,v105,v105
M 22,a[92:95],78
L 3
v_exp_f32 v116,v116
M 22,a[100:103],82
s_sub_u32 s57,s84,s57
R 38,1,17408
v_fma_f32 v116,v116,v105,v105
M 22,a[108:111],86
D 0,56,0
M 18,a[96:99],82
R 42,1,18432
v_exp_f32 v117,v117
M 18,a[104:107],86
L 3
v_rcp_f32 v114,v114
M 18,a[112:115],90
R 46,1,19456
v_fma_f32 v117,v117,v105,v105
M 18,a[120:123],94
v_rcp_f32 v115,v115
M 22,a[116:119],90
R 50,1,20480
v_mul_f32 v26,v26,v114
M 22,a[124:127],94
L 3
v_rcp_f32 v116,v116
M 22,a[132:135],34
R 54,1,21504
v_mul_f32 v27,v27,v115
M 22,a[140:143],38
D 0,56,1024
v_mul_f32 v28,v28,v116
M 18,a[128:131],34
R 58,1,22528
v_cvt_pk_bf16_f32 v122,v26,v27
M 18,a[136:139],38
L 3
v_rcp_f32 v117,v117
M 18,a[144:147],42
R 62,1,23552
v_mul_f32 v29,v29,v117
M 18,a[152:155],46
v_cvt_pk_bf16_f32 v123,v28,v29
v_fma_f32 v31,-v102,v111,v31
M 22,a[148:151],42
R 66,1,24576
v_fma_f32 v32,-v102,v112,v32
M 22,a[156:159],46
L 3
D 0,56,2048
v_fma_f32 v33,-v102,v113,v33
M 22,a[164:167],50
R 70,1,25600
v_mul_f32 v118,v30,v30
M 22,a[172:175],54
v_mul_f32 v119,v31,v31
v_mul_f32 v120,v32,v32
M 18,a[160:163],50
R 74,1,26624
v_mul_f32 v121,v33,v33
M 18,a[168:171],54
L 3
v_fma_f32 v118,v104,v118,v103
v_fma_f32 v119,v104,v119,v103
M 18,a[176:179],58
R 78,1,27648
v_fma_f32 v120,v104,v120,v103
M 18,a[184:187],62
v_fma_f32 v121,v104,v121,v103
v_mul_f32 v118,v30,v118
M 22,a[180:183],58
R 82,1,28672
v_exp_f32 v118,v118
M 22,a[188:191],62
L 3
D 0,56,3072
M 22,a[196:199],66
R 86,1,29696
v_mul_f32 v119,v31,v119
M 22,a[204:207],70
v_exp_f32 v119,v119
M 18,a[192:195],66
R 90,1,30720
v_mul_f32 v120,v32,v120
M 18,a[200:203],70
L 3
v_mul_f32 v121,v33,v121
v_fma_f32 v118,v118,v105,v105
M 18,a[208:211],74
s_waitcnt vmcnt(5)
R 94,1,31744
v_fma_f32 v119,v119,v105,v105
M 18,a[216:219],78
s_barrier
ds_read_b32 v7,v6 offset:0
v_exp_f32 v120,v120
M 22,a[212:215],74
R 34,2,0
v_fma_f32 v120,v120,v105,v105
M 22,a[220:223],78
L 4
buffer_store_dword v4,v3,s[36:39],s70 offen offset:0 sc0 sc1
v_exp_f32 v121,v121
M 22,a[228:231],82
s_mov_b32 s70,s69
R 38,2,1024
v_fma_f32 v121,v121,v105,v105
v_rcp_f32 v118,v118
v_rcp_f32 v119,v119
v_rcp_f32 v120,v120
v_rcp_f32 v121,v121
M 22,a[236:239],86
s_mov_b32 s69,s68
v_mul_f32 v30,v30,v118
v_mul_f32 v31,v31,v119
v_mul_f32 v32,v32,v120
v_mul_f32 v33,v33,v121
v_cvt_pk_bf16_f32 v124,v30,v31
v_cvt_pk_bf16_f32 v125,v32,v33
M 18,a[224:227],82
s_add_u32 s68,s68,s63
R 42,2,2048
buffer_store_dwordx4 v[122:125],v15,s[36:39],s67 offen offset:0 sc0 sc1
M 18,a[232:235],86
L 4
L 3
M 18,a[240:243],90
R 46,2,3072
v_cmp_eq_u32 vcc,s53,v7
M 18,a[248:251],94
buffer_store_dwordx4 v[122:125],v16,s[36:39],s67 offen offset:0 sc0 sc1
M 22,a[244:247],90
s_waitcnt vmcnt(7) lgkmcnt(2)
s_cmp_eq_u32 vcc_lo,-1
R 50,2,4096
M 22,a[252:255],94
s_cbranch_scc0 Lg1_K1_%=
Lg1_F1_%=:
v_mfma_f32_16x16x32_bf16 v[30:33],a[4:7],v[34:37],0
s_add_u32 s58,s58,s61
s_add_u32 m0,s59,1024
R 54,2,5120
M 30,a[12:15],38
buffer_load_dword v8,s[36:39],s58 offen offset:0 sc1 lds
v_mfma_f32_16x16x32_bf16 v[26:29],a[0:3],v[34:37],0
s_add_u32 m0,s57,0
R 58,2,6144
v_fma_f32 v18,-v98,v106,v18
M 26,a[8:11],38
L 3
s_add_u32 s56,s55,0
v_fma_f32 v19,-v98,v107,v19
M 26,a[16:19],42
s_mov_b32 s67,s65
R 62,2,7168
v_fma_f32 v20,-v98,v108,v20
M 26,a[24:27],46
D 0,56,0
M 30,a[20:23],42
s_add_u32 s65,s65,s62
R 66,2,8192
v_fma_f32 v21,-v98,v109,v21
v_mul_f32 v114,v18,v18
M 30,a[28:31],46
L 3
D 0,56,1024
M 30,a[36:39],50
s_add_u32 s54,s54,s46
R 70,2,9216
v_mul_f32 v115,v19,v19
M 30,a[44:47],54
v_mul_f32 v116,v20,v20
v_mul_f32 v117,v21,v21
M 26,a[32:35],50
R 74,2,10240
v_fma_f32 v114,v100,v114,v99
M 26,a[40:43],54
L 3
D 0,56,2048
M 26,a[48:51],58
R 78,2,11264
v_fma_f32 v115,v100,v115,v99
M 26,a[56:59],62
D 0,56,3072
v_fma_f32 v116,v100,v116,v99
M 30,a[52:55],58
s_add_u32 m0,s57,16384
R 82,2,12288
v_fma_f32 v117,v100,v117,v99
M 30,a[60:63],62
L 3
v_mul_f32 v114,v18,v114
v_mul_f32 v115,v19,v115
M 30,a[68:71],66
s_add_u32 s56,s55,16384
R 86,2,13312
v_mul_f32 v116,v20,v116
M 30,a[76:79],70
buffer_load_dwordx4 v[102:105],v11,s[36:39],s73 offen offset:0 sc1
M 26,a[64:67],66
s_add_u32 s73,s73,s64
R 90,2,14336
v_mul_f32 v117,v21,v117
M 26,a[72:75],70
L 3
v_exp_f32 v114,v114
v_fma_f32 v22,-v98,v110,v22
M 26,a[80:83],74
s_waitcnt vmcnt(9)
R 94,2,15360
v_fma_f32 v114,v114,v101,v101
M 26,a[88:91],78
s_barrier
v_exp_f32 v115,v115
M 30,a[84:87],74
s_add_u32 s55,s55,s60
R 34,2,16384
v_fma_f32 v115,v115,v101,v101
M 30,a[92:95],78
L 3
v_exp_f32 v116,v116
M 30,a[100:103],82
s_sub_u32 s57,s84,s57
R 38,2,17408
v_fma_f32 v116,v116,v101,v101
M 30,a[108:111],86
D 0,56,0
M 26,a[96:99],82
R 42,2,18432
v_exp_f32 v117,v117
M 26,a[104:107],86
L 3
v_rcp_f32 v114,v114
M 26,a[112:115],90
R 46,2,19456
v_fma_f32 v117,v117,v101,v101
M 26,a[120:123],94
v_rcp_f32 v115,v115
M 30,a[116:119],90
R 50,2,20480
v_mul_f32 v18,v18,v114
M 30,a[124:127],94
L 3
v_rcp_f32 v116,v116
M 30,a[132:135],34
R 54,2,21504
v_mul_f32 v19,v19,v115
M 30,a[140:143],38
D 0,56,1024
v_mul_f32 v20,v20,v116
M 26,a[128:131],34
R 58,2,22528
v_cvt_pk_bf16_f32 v122,v18,v19
M 26,a[136:139],38
L 3
v_rcp_f32 v117,v117
M 26,a[144:147],42
R 62,2,23552
v_mul_f32 v21,v21,v117
M 26,a[152:155],46
v_cvt_pk_bf16_f32 v123,v20,v21
v_fma_f32 v23,-v98,v111,v23
M 30,a[148:151],42
R 66,2,24576
v_fma_f32 v24,-v98,v112,v24
M 30,a[156:159],46
L 3
D 0,56,2048
v_fma_f32 v25,-v98,v113,v25
M 30,a[164:167],50
R 70,2,25600
v_mul_f32 v118,v22,v22
M 30,a[172:175],54
v_mul_f32 v119,v23,v23
v_mul_f32 v120,v24,v24
M 26,a[160:163],50
R 74,2,26624
v_mul_f32 v121,v25,v25
M 26,a[168:171],54
L 3
v_fma_f32 v118,v100,v118,v99
v_fma_f32 v119,v100,v119,v99
M 26,a[176:179],58
R 78,2,27648
v_fma_f32 v120,v100,v120,v99
M 26,a[184:187],62
v_fma_f32 v121,v100,v121,v99
v_mul_f32 v118,v22,v118
M 30,a[180:183],58
R 82,2,28672
v_exp_f32 v118,v118
M 30,a[188:191],62
L 3
D 0,56,3072
M 30,a[196:199],66
R 86,2,29696
v_mul_f32 v119,v23,v119
M 30,a[204:207],70
v_exp_f32 v119,v119
M 26,a[192:195],66
R 90,2,30720
v_mul_f32 v120,v24,v120
M 26,a[200:203],70
L 3
v_mul_f32 v121,v25,v121
v_fma_f32 v118,v118,v101,v101
M 26,a[208:211],74
s_waitcnt vmcnt(5)
)"
R"(R 94,2,31744
v_fma_f32 v119,v119,v101,v101
M 26,a[216:219],78
s_barrier
ds_read_b32 v7,v6 offset:1024
v_exp_f32 v120,v120
M 30,a[212:215],74
R 34,1,0
v_fma_f32 v120,v120,v101,v101
M 30,a[220:223],78
L 4
buffer_store_dword v4,v3,s[36:39],s70 offen offset:0 sc0 sc1
v_exp_f32 v121,v121
M 30,a[228:231],82
s_mov_b32 s70,s69
R 38,1,1024
v_fma_f32 v121,v121,v101,v101
v_rcp_f32 v118,v118
v_rcp_f32 v119,v119
v_rcp_f32 v120,v120
v_rcp_f32 v121,v121
M 30,a[236:239],86
s_mov_b32 s69,s68
v_mul_f32 v22,v22,v118
v_mul_f32 v23,v23,v119
v_mul_f32 v24,v24,v120
v_mul_f32 v25,v25,v121
v_cvt_pk_bf16_f32 v124,v22,v23
v_cvt_pk_bf16_f32 v125,v24,v25
M 26,a[224:227],82
s_add_u32 s68,s68,s63
R 42,1,2048
buffer_store_dwordx4 v[122:125],v15,s[36:39],s66 offen offset:0 sc0 sc1
M 26,a[232:235],86
L 4
L 3
M 26,a[240:243],90
R 46,1,3072
v_cmp_eq_u32 vcc,s53,v7
M 26,a[248:251],94
buffer_store_dwordx4 v[122:125],v16,s[36:39],s66 offen offset:0 sc0 sc1
M 30,a[244:247],90
s_waitcnt vmcnt(7) lgkmcnt(2)
s_cmp_eq_u32 vcc_lo,-1
R 50,1,4096
M 30,a[252:255],94
s_cbranch_scc1 Lg1_F0_%=
Lg1_K0_%=:
Lg1_L0_%=:
v_mfma_f32_16x16x32_bf16 v[22:25],a[4:7],v[34:37],0
s_add_u32 m0,s59,3072
s_mov_b32 s66,s65
R 54,1,5120
M 22,a[12:15],38
buffer_load_dword v8,s[36:39],s58 offen offset:0 sc1 lds
v_mfma_f32_16x16x32_bf16 v[18:21],a[0:3],v[34:37],0
s_add_u32 s65,s65,s62
R 58,1,6144
v_fma_f32 v26,-v102,v106,v26
M 18,a[8:11],38
L 3
v_fma_f32 v27,-v102,v107,v27
v_fma_f32 v28,-v102,v108,v28
M 18,a[16:19],42
R 62,1,7168
v_fma_f32 v29,-v102,v109,v29
M 18,a[24:27],46
v_mul_f32 v114,v26,v26
v_mul_f32 v115,v27,v27
M 22,a[20:23],42
R 66,1,8192
v_mul_f32 v116,v28,v28
M 22,a[28:31],46
L 3
v_mul_f32 v117,v29,v29
v_fma_f32 v114,v104,v114,v103
M 22,a[36:39],50
R 70,1,9216
v_fma_f32 v115,v104,v115,v103
M 22,a[44:47],54
v_fma_f32 v116,v104,v116,v103
v_fma_f32 v117,v104,v117,v103
M 18,a[32:35],50
R 74,1,10240
v_mul_f32 v114,v26,v114
M 18,a[40:43],54
L 3
v_exp_f32 v114,v114
M 18,a[48:51],58
R 78,1,11264
v_mul_f32 v115,v27,v115
M 18,a[56:59],62
v_exp_f32 v115,v115
M 22,a[52:55],58
R 82,1,12288
v_mul_f32 v116,v28,v116
M 22,a[60:63],62
L 3
v_exp_f32 v116,v116
M 22,a[68:71],66
R 86,1,13312
v_mul_f32 v117,v29,v117
M 22,a[76:79],70
buffer_load_dwordx4 v[98:101],v11,s[36:39],s73 offen offset:0 sc1
M 18,a[64:67],66
s_add_u32 s73,s73,s64
R 90,1,14336
v_fma_f32 v114,v114,v105,v105
M 18,a[72:75],70
L 3
v_exp_f32 v117,v117
M 18,a[80:83],74
s_waitcnt vmcnt(1)
R 94,1,15360
v_fma_f32 v115,v115,v105,v105
M 18,a[88:91],78
s_barrier
ds_read_b32 v7,v6 offset:3072
v_fma_f32 v116,v116,v105,v105
M 22,a[84:87],74
R 34,1,16384
v_fma_f32 v117,v117,v105,v105
M 22,a[92:95],78
L 4
buffer_store_dword v4,v3,s[36:39],s70 offen offset:0 sc0 sc1
M 22,a[100:103],82
s_mov_b32 s70,0x7fff0000
R 38,1,17408
v_fma_f32 v30,-v102,v110,v30
M 22,a[108:111],86
v_rcp_f32 v114,v114
M 18,a[96:99],82
R 42,1,18432
v_mul_f32 v26,v26,v114
M 18,a[104:107],86
L 4
L 3
v_cmp_eq_u32 vcc,s53,v7
M 18,a[112:115],90
R 46,1,19456
v_fma_f32 v31,-v102,v111,v31
M 18,a[120:123],94
v_rcp_f32 v115,v115
M 22,a[116:119],90
s_cmp_eq_u32 vcc_lo,-1
R 50,1,20480
v_mul_f32 v27,v27,v115
s_cbranch_scc0 Lg1_X0_%=
M 22,a[124:127],94
L 3
s_add_u32 m0,s57,0
s_add_u32 s56,s55,0
M 22,a[132:135],34
s_add_u32 s58,s58,s61
R 54,1,21504
v_cvt_pk_bf16_f32 v122,v26,v27
M 22,a[140:143],38
D 0,56,0
M 18,a[128:131],34
s_add_u32 s54,s54,s46
R 58,1,22528
v_fma_f32 v32,-v102,v112,v32
M 18,a[136:139],38
L 3
v_rcp_f32 v116,v116
M 18,a[144:147],42
R 62,1,23552
v_mul_f32 v28,v28,v116
M 18,a[152:155],46
D 0,56,1024
M 22,a[148:151],42
R 66,1,24576
v_fma_f32 v33,-v102,v113,v33
M 22,a[156:159],46
L 3
v_rcp_f32 v117,v117
M 22,a[164:167],50
R 70,1,25600
v_mul_f32 v29,v29,v117
M 22,a[172:175],54
D 0,56,2048
M 18,a[160:163],50
R 74,1,26624
v_cvt_pk_bf16_f32 v123,v28,v29
M 18,a[168:171],54
L 3
v_mul_f32 v118,v30,v30
v_mul_f32 v119,v31,v31
M 18,a[176:179],58
R 78,1,27648
v_mul_f32 v120,v32,v32
M 18,a[184:187],62
D 0,56,3072
M 22,a[180:183],58
s_add_u32 m0,s59,0
R 82,1,28672
v_mul_f32 v121,v33,v33
M 22,a[188:191],62
L 3
buffer_load_dword v8,s[36:39],s58 offen offset:0 sc1 lds
M 22,a[196:199],66
s_add_u32 m0,s57,16384
R 86,1,29696
v_fma_f32 v118,v104,v118,v103
M 22,a[204:207],70
s_add_u32 s56,s55,16384
v_fma_f32 v119,v104,v119,v103
v_fma_f32 v120,v104,v120,v103
M 18,a[192:195],66
s_add_u32 s55,s55,s60
R 90,1,30720
v_fma_f32 v121,v104,v121,v103
M 18,a[200:203],70
L 3
D 0,56,0
M 18,a[208:211],74
s_waitcnt vmcnt(1)
R 94,1,31744
v_mul_f32 v118,v30,v118
M 18,a[216:219],78
s_barrier
ds_read_b32 v7,v6 offset:0
v_mul_f32 v119,v31,v119
M 22,a[212:215],74
s_sub_u32 s57,s84,s57
R 34,2,0
v_mul_f32 v120,v32,v120
M 22,a[220:223],78
L 4
buffer_store_dword v4,v3,s[36:39],s70 offen offset:0 sc0 sc1
v_mul_f32 v121,v33,v121
v_exp_f32 v118,v118
v_exp_f32 v119,v119
v_exp_f32 v120,v120
v_exp_f32 v121,v121
M 22,a[228:231],82
s_mov_b32 s70,s69
R 38,2,1024
v_fma_f32 v118,v118,v105,v105
v_fma_f32 v119,v119,v105,v105
v_fma_f32 v120,v120,v105,v105
v_fma_f32 v121,v121,v105,v105
v_rcp_f32 v118,v118
v_rcp_f32 v119,v119
v_rcp_f32 v120,v120
v_rcp_f32 v121,v121
M 22,a[236:239],86
s_mov_b32 s69,s68
v_mul_f32 v30,v30,v118
v_mul_f32 v31,v31,v119
v_mul_f32 v32,v32,v120
v_mul_f32 v33,v33,v121
v_cvt_pk_bf16_f32 v124,v30,v31
v_cvt_pk_bf16_f32 v125,v32,v33
M 18,a[224:227],82
s_add_u32 s68,s68,s63
R 42,2,2048
buffer_store_dwordx4 v[122:125],v15,s[36:39],s67 offen offset:0 sc0 sc1
M 18,a[232:235],86
L 4
L 3
M 18,a[240:243],90
R 46,2,3072
v_cmp_eq_u32 vcc,s53,v7
M 18,a[248:251],94
D 0,56,1024
M 22,a[244:247],90
L 2
s_cmp_eq_u32 vcc_lo,-1
R 50,2,4096
buffer_store_dwordx4 v[122:125],v16,s[36:39],s67 offen offset:0 sc0 sc1
M 22,a[252:255],94
D 0,56,2048
D 0,56,3072
buffer_store_dword v4,v3,s[36:39],s71 offen offset:0 sc0 sc1
buffer_store_dword v4,v3,s[36:39],s71 offen offset:0 sc0 sc1
buffer_store_dword v4,v3,s[36:39],s71 offen offset:0 sc0 sc1
s_cbranch_scc1 Lg1_F1_%=
s_branch Lg1_K1_%=
Lg1_X0_%=:
M 22,a[124:127],94
L 3
s_add_u32 m0,s59,0
v_cvt_pk_bf16_f32 v122,v26,v27
M 22,a[132:135],34
R 54,1,21504
v_fma_f32 v32,-v102,v112,v32
M 22,a[140:143],38
buffer_load_dword v8,s[36:39],s58 offen offset:0 sc1 lds
M 18,a[128:131],34
R 58,1,22528
v_fma_f32 v33,-v102,v113,v33
M 18,a[136:139],38
L 3
v_rcp_f32 v116,v116
M 18,a[144:147],42
R 62,1,23552
v_mul_f32 v28,v28,v116
M 18,a[152:155],46
v_rcp_f32 v117,v117
M 22,a[148:151],42
R 66,1,24576
v_mul_f32 v29,v29,v117
M 22,a[156:159],46
L 3
v_cvt_pk_bf16_f32 v123,v28,v29
v_mul_f32 v118,v30,v30
M 22,a[164:167],50
R 70,1,25600
v_mul_f32 v119,v31,v31
M 22,a[172:175],54
v_mul_f32 v120,v32,v32
v_mul_f32 v121,v33,v33
M 18,a[160:163],50
R 74,1,26624
v_fma_f32 v118,v104,v118,v103
M 18,a[168:171],54
L 3
v_fma_f32 v119,v104,v119,v103
v_fma_f32 v120,v104,v120,v103
M 18,a[176:179],58
R 78,1,27648
v_fma_f32 v121,v104,v121,v103
M 18,a[184:187],62
v_mul_f32 v118,v30,v118
v_mul_f32 v119,v31,v119
M 22,a[180:183],58
R 82,1,28672
v_mul_f32 v120,v32,v120
M 22,a[188:191],62
L 3
v_exp_f32 v118,v118
M 22,a[196:199],66
R 86,1,29696
v_mul_f32 v121,v33,v121
M 22,a[204:207],70
v_exp_f32 v119,v119
M 18,a[192:195],66
R 90,1,30720
v_fma_f32 v118,v118,v105,v105
M 18,a[200:203],70
L 3
v_exp_f32 v120,v120
M 18,a[208:211],74
s_waitcnt vmcnt(0)
R 94,1,31744
v_fma_f32 v119,v119,v105,v105
M 18,a[216:219],78
s_barrier
buffer_store_dword v4,v3,s[36:39],s70 offen offset:0 sc0 sc1
M 22,a[212:215],74
s_mov_b32 s70,s69
ds_read_b32 v7,v6 offset:0
v_fma_f32 v120,v120,v105,v105
M 22,a[220:223],78
L 3
v_exp_f32 v121,v121
M 22,a[228:231],82
s_mov_b32 s69,s68
v_fma_f32 v121,v121,v105,v105
v_rcp_f32 v118,v118
v_rcp_f32 v119,v119
v_rcp_f32 v120,v120
v_rcp_f32 v121,v121
M 22,a[236:239],86
s_add_u32 s68,s68,s63
v_mul_f32 v30,v30,v118
v_mul_f32 v31,v31,v119
v_mul_f32 v32,v32,v120
v_mul_f32 v33,v33,v121
v_cvt_pk_bf16_f32 v124,v30,v31
v_cvt_pk_bf16_f32 v125,v32,v33
M 18,a[224:227],82
buffer_store_dwordx4 v[122:125],v15,s[36:39],s67 offen offset:0 sc0 sc1
M 18,a[232:235],86
L 1
M 18,a[240:243],90
L 0
v_cmp_eq_u32 vcc,s53,v7
M 18,a[248:251],94
buffer_store_dwordx4 v[122:125],v16,s[36:39],s67 offen offset:0 sc0 sc1
M 22,a[244:247],90
s_nop 0
M 22,a[252:255],94
s_mov_b32 s80,1
s_cmp_eq_u32 vcc_lo,-1
s_cbranch_scc0 Lg1_xn0_%=
s_add_u32 m0,s57,0
s_add_u32 s56,s55,0
D 0,56,0
v_fma_f32 v18,-v98,v106,v18
v_fma_f32 v19,-v98,v107,v19
v_fma_f32 v20,-v98,v108,v20
v_fma_f32 v21,-v98,v109,v21
v_mul_f32 v114,v18,v18
v_mul_f32 v115,v19,v19
v_mul_f32 v116,v20,v20
D 0,56,1024
v_mul_f32 v117,v21,v21
v_fma_f32 v114,v100,v114,v99
v_fma_f32 v115,v100,v115,v99
v_fma_f32 v116,v100,v116,v99
v_fma_f32 v117,v100,v117,v99
v_mul_f32 v114,v18,v114
v_mul_f32 v115,v19,v115
D 0,56,2048
v_mul_f32 v116,v20,v116
v_mul_f32 v117,v21,v117
v_exp_f32 v114,v114
v_exp_f32 v115,v115
v_exp_f32 v116,v116
v_exp_f32 v117,v117
v_fma_f32 v114,v114,v101,v101
D 0,56,3072
v_fma_f32 v115,v115,v101,v101
v_fma_f32 v116,v116,v101,v101
v_fma_f32 v117,v117,v101,v101
v_rcp_f32 v114,v114
v_rcp_f32 v115,v115
v_rcp_f32 v116,v116
v_rcp_f32 v117,v117
s_add_u32 s58,s58,s61
s_add_u32 m0,s57,16384
s_add_u32 s56,s55,16384
D 0,56,0
v_mul_f32 v18,v18,v114
v_mul_f32 v19,v19,v115
v_mul_f32 v20,v20,v116
v_mul_f32 v21,v21,v117
v_cvt_pk_bf16_f32 v122,v18,v19
v_cvt_pk_bf16_f32 v123,v20,v21
v_fma_f32 v22,-v98,v110,v22
D 0,56,1024
v_fma_f32 v23,-v98,v111,v23
v_fma_f32 v24,-v98,v112,v24
v_fma_f32 v25,-v98,v113,v25
v_mul_f32 v118,v22,v22
v_mul_f32 v119,v23,v23
v_mul_f32 v120,v24,v24
v_mul_f32 v121,v25,v25
D 0,56,2048
v_fma_f32 v118,v100,v118,v99
v_fma_f32 v119,v100,v119,v99
v_fma_f32 v120,v100,v120,v99
v_fma_f32 v121,v100,v121,v99
v_mul_f32 v118,v22,v118
v_mul_f32 v119,v23,v119
v_mul_f32 v120,v24,v120
D 0,56,3072
v_mul_f32 v121,v25,v121
v_exp_f32 v118,v118
v_exp_f32 v119,v119
v_exp_f32 v120,v120
v_exp_f32 v121,v121
v_fma_f32 v118,v118,v101,v101
v_fma_f32 v119,v119,v101,v101
s_add_u32 s55,s55,s60
s_sub_u32 s57,s84,s57
s_add_u32 s54,s54,s46
v_fma_f32 v120,v120,v101,v101
v_fma_f32 v121,v121,v101,v101
v_rcp_f32 v118,v118
v_rcp_f32 v119,v119
v_rcp_f32 v120,v120
v_rcp_f32 v121,v121
v_mul_f32 v22,v22,v118
v_mul_f32 v23,v23,v119
v_mul_f32 v24,v24,v120
v_mul_f32 v25,v25,v121
v_cvt_pk_bf16_f32 v124,v22,v23
v_cvt_pk_bf16_f32 v125,v24,v25
buffer_store_dwordx4 v[122:125],v15,s[36:39],s66 offen offset:0 sc0 sc1
buffer_store_dwordx4 v[122:125],v16,s[36:39],s66 offen offset:0 sc0 sc1
s_mov_b32 s72,s69
s_mov_b32 s69,0x7fff0000
s_mov_b32 s66,0x7fff0000
s_mov_b32 s76,0x7fff0000
s_branch Lg1_gox_%=
Lg1_xn0_%=:
s_cmp_ge_u32 s54,256
s_cbranch_scc1 Lg1_fin0_%=
s_add_u32 m0,s59,4096
s_nop 0
buffer_load_dword v8,s[36:39],s58 offen offset:0 sc1 lds
buffer_store_dword v4,v3,s[36:39],s71 offen offset:0 sc0 sc1
s_add_u32 m0,s59,5120
s_nop 0
buffer_load_dword v8,s[36:39],s58 offen offset:0 sc1 lds
buffer_store_dword v4,v3,s[36:39],s71 offen offset:0 sc0 sc1
s_mov_b32 s83,0
v_fma_f32 v18,-v98,v106,v18
v_fma_f32 v19,-v98,v107,v19
v_fma_f32 v20,-v98,v108,v20
v_fma_f32 v21,-v98,v109,v21
v_mul_f32 v114,v18,v18
v_mul_f32 v115,v19,v19
v_mul_f32 v116,v20,v20
v_mul_f32 v117,v21,v21
v_fma_f32 v114,v100,v114,v99
v_fma_f32 v115,v100,v115,v99
)"
R"(v_fma_f32 v116,v100,v116,v99
v_fma_f32 v117,v100,v117,v99
v_mul_f32 v114,v18,v114
v_mul_f32 v115,v19,v115
v_mul_f32 v116,v20,v116
v_mul_f32 v117,v21,v117
v_exp_f32 v114,v114
v_exp_f32 v115,v115
v_exp_f32 v116,v116
v_exp_f32 v117,v117
v_fma_f32 v114,v114,v101,v101
v_fma_f32 v115,v115,v101,v101
v_fma_f32 v116,v116,v101,v101
v_fma_f32 v117,v117,v101,v101
v_rcp_f32 v114,v114
v_rcp_f32 v115,v115
v_rcp_f32 v116,v116
v_rcp_f32 v117,v117
v_mul_f32 v18,v18,v114
v_mul_f32 v19,v19,v115
v_mul_f32 v20,v20,v116
v_mul_f32 v21,v21,v117
v_cvt_pk_bf16_f32 v122,v18,v19
v_cvt_pk_bf16_f32 v123,v20,v21
v_fma_f32 v22,-v98,v110,v22
v_fma_f32 v23,-v98,v111,v23
v_fma_f32 v24,-v98,v112,v24
v_fma_f32 v25,-v98,v113,v25
v_mul_f32 v118,v22,v22
v_mul_f32 v119,v23,v23
v_mul_f32 v120,v24,v24
v_mul_f32 v121,v25,v25
v_fma_f32 v118,v100,v118,v99
v_fma_f32 v119,v100,v119,v99
v_fma_f32 v120,v100,v120,v99
v_fma_f32 v121,v100,v121,v99
v_mul_f32 v118,v22,v118
v_mul_f32 v119,v23,v119
v_mul_f32 v120,v24,v120
v_mul_f32 v121,v25,v121
v_exp_f32 v118,v118
v_exp_f32 v119,v119
v_exp_f32 v120,v120
v_exp_f32 v121,v121
v_fma_f32 v118,v118,v101,v101
v_fma_f32 v119,v119,v101,v101
v_fma_f32 v120,v120,v101,v101
v_fma_f32 v121,v121,v101,v101
v_rcp_f32 v118,v118
v_rcp_f32 v119,v119
v_rcp_f32 v120,v120
v_rcp_f32 v121,v121
v_mul_f32 v22,v22,v118
v_mul_f32 v23,v23,v119
v_mul_f32 v24,v24,v120
v_mul_f32 v25,v25,v121
v_cvt_pk_bf16_f32 v124,v22,v23
v_cvt_pk_bf16_f32 v125,v24,v25
buffer_store_dwordx4 v[122:125],v15,s[36:39],s66 offen offset:0 sc0 sc1
buffer_store_dwordx4 v[122:125],v16,s[36:39],s66 offen offset:0 sc0 sc1
s_mov_b32 s72,s69
s_mov_b32 s69,0x7fff0000
s_mov_b32 s66,0x7fff0000
s_mov_b32 s76,0x7fff0000
s_branch Lg1_poll_%=
Lg1_fin0_%=:
v_fma_f32 v18,-v98,v106,v18
v_fma_f32 v19,-v98,v107,v19
v_fma_f32 v20,-v98,v108,v20
v_fma_f32 v21,-v98,v109,v21
v_mul_f32 v114,v18,v18
v_mul_f32 v115,v19,v19
v_mul_f32 v116,v20,v20
v_mul_f32 v117,v21,v21
v_fma_f32 v114,v100,v114,v99
v_fma_f32 v115,v100,v115,v99
v_fma_f32 v116,v100,v116,v99
v_fma_f32 v117,v100,v117,v99
v_mul_f32 v114,v18,v114
v_mul_f32 v115,v19,v115
v_mul_f32 v116,v20,v116
v_mul_f32 v117,v21,v117
v_exp_f32 v114,v114
v_exp_f32 v115,v115
v_exp_f32 v116,v116
v_exp_f32 v117,v117
v_fma_f32 v114,v114,v101,v101
v_fma_f32 v115,v115,v101,v101
v_fma_f32 v116,v116,v101,v101
v_fma_f32 v117,v117,v101,v101
v_rcp_f32 v114,v114
v_rcp_f32 v115,v115
v_rcp_f32 v116,v116
v_rcp_f32 v117,v117
v_mul_f32 v18,v18,v114
v_mul_f32 v19,v19,v115
v_mul_f32 v20,v20,v116
v_mul_f32 v21,v21,v117
v_cvt_pk_bf16_f32 v122,v18,v19
v_cvt_pk_bf16_f32 v123,v20,v21
v_fma_f32 v22,-v98,v110,v22
v_fma_f32 v23,-v98,v111,v23
v_fma_f32 v24,-v98,v112,v24
v_fma_f32 v25,-v98,v113,v25
v_mul_f32 v118,v22,v22
v_mul_f32 v119,v23,v23
v_mul_f32 v120,v24,v24
v_mul_f32 v121,v25,v25
v_fma_f32 v118,v100,v118,v99
v_fma_f32 v119,v100,v119,v99
v_fma_f32 v120,v100,v120,v99
v_fma_f32 v121,v100,v121,v99
v_mul_f32 v118,v22,v118
v_mul_f32 v119,v23,v119
v_mul_f32 v120,v24,v120
v_mul_f32 v121,v25,v121
v_exp_f32 v118,v118
v_exp_f32 v119,v119
v_exp_f32 v120,v120
v_exp_f32 v121,v121
v_fma_f32 v118,v118,v101,v101
v_fma_f32 v119,v119,v101,v101
v_fma_f32 v120,v120,v101,v101
v_fma_f32 v121,v121,v101,v101
v_rcp_f32 v118,v118
v_rcp_f32 v119,v119
v_rcp_f32 v120,v120
v_rcp_f32 v121,v121
v_mul_f32 v22,v22,v118
v_mul_f32 v23,v23,v119
v_mul_f32 v24,v24,v120
v_mul_f32 v25,v25,v121
v_cvt_pk_bf16_f32 v124,v22,v23
v_cvt_pk_bf16_f32 v125,v24,v25
buffer_store_dwordx4 v[122:125],v15,s[36:39],s66 offen offset:0 sc0 sc1
buffer_store_dwordx4 v[122:125],v16,s[36:39],s66 offen offset:0 sc0 sc1
s_mov_b32 s72,s69
s_mov_b32 s69,0x7fff0000
s_mov_b32 s66,0x7fff0000
s_mov_b32 s76,0x7fff0000
s_branch Lg1_done_%=
Lg1_K1_%=:
Lg1_L1_%=:
v_mfma_f32_16x16x32_bf16 v[30:33],a[4:7],v[34:37],0
s_add_u32 m0,s59,3072
s_mov_b32 s67,s65
R 54,2,5120
M 30,a[12:15],38
buffer_load_dword v8,s[36:39],s58 offen offset:0 sc1 lds
v_mfma_f32_16x16x32_bf16 v[26:29],a[0:3],v[34:37],0
s_add_u32 s65,s65,s62
R 58,2,6144
v_fma_f32 v18,-v98,v106,v18
M 26,a[8:11],38
L 3
v_fma_f32 v19,-v98,v107,v19
v_fma_f32 v20,-v98,v108,v20
M 26,a[16:19],42
R 62,2,7168
v_fma_f32 v21,-v98,v109,v21
M 26,a[24:27],46
v_mul_f32 v114,v18,v18
v_mul_f32 v115,v19,v19
M 30,a[20:23],42
R 66,2,8192
v_mul_f32 v116,v20,v20
M 30,a[28:31],46
L 3
v_mul_f32 v117,v21,v21
v_fma_f32 v114,v100,v114,v99
M 30,a[36:39],50
R 70,2,9216
v_fma_f32 v115,v100,v115,v99
M 30,a[44:47],54
v_fma_f32 v116,v100,v116,v99
v_fma_f32 v117,v100,v117,v99
M 26,a[32:35],50
R 74,2,10240
v_mul_f32 v114,v18,v114
M 26,a[40:43],54
L 3
v_exp_f32 v114,v114
M 26,a[48:51],58
R 78,2,11264
v_mul_f32 v115,v19,v115
M 26,a[56:59],62
v_exp_f32 v115,v115
M 30,a[52:55],58
R 82,2,12288
v_mul_f32 v116,v20,v116
M 30,a[60:63],62
L 3
v_exp_f32 v116,v116
M 30,a[68:71],66
R 86,2,13312
v_mul_f32 v117,v21,v117
M 30,a[76:79],70
buffer_load_dwordx4 v[102:105],v11,s[36:39],s73 offen offset:0 sc1
M 26,a[64:67],66
s_add_u32 s73,s73,s64
R 90,2,14336
v_fma_f32 v114,v114,v101,v101
M 26,a[72:75],70
L 3
v_exp_f32 v117,v117
M 26,a[80:83],74
s_waitcnt vmcnt(1)
R 94,2,15360
v_fma_f32 v115,v115,v101,v101
M 26,a[88:91],78
s_barrier
ds_read_b32 v7,v6 offset:3072
v_fma_f32 v116,v116,v101,v101
M 30,a[84:87],74
R 34,2,16384
v_fma_f32 v117,v117,v101,v101
M 30,a[92:95],78
L 4
buffer_store_dword v4,v3,s[36:39],s70 offen offset:0 sc0 sc1
M 30,a[100:103],82
s_mov_b32 s70,0x7fff0000
R 38,2,17408
v_fma_f32 v22,-v98,v110,v22
M 30,a[108:111],86
v_rcp_f32 v114,v114
M 26,a[96:99],82
R 42,2,18432
v_mul_f32 v18,v18,v114
M 26,a[104:107],86
L 4
L 3
v_cmp_eq_u32 vcc,s53,v7
M 26,a[112:115],90
R 46,2,19456
v_fma_f32 v23,-v98,v111,v23
M 26,a[120:123],94
v_rcp_f32 v115,v115
M 30,a[116:119],90
s_cmp_eq_u32 vcc_lo,-1
R 50,2,20480
v_mul_f32 v19,v19,v115
s_cbranch_scc0 Lg1_X1_%=
M 30,a[124:127],94
L 3
s_add_u32 m0,s57,0
s_add_u32 s56,s55,0
M 30,a[132:135],34
s_add_u32 s58,s58,s61
R 54,2,21504
v_cvt_pk_bf16_f32 v122,v18,v19
M 30,a[140:143],38
D 0,56,0
M 26,a[128:131],34
s_add_u32 s54,s54,s46
R 58,2,22528
v_fma_f32 v24,-v98,v112,v24
M 26,a[136:139],38
L 3
v_rcp_f32 v116,v116
M 26,a[144:147],42
R 62,2,23552
v_mul_f32 v20,v20,v116
M 26,a[152:155],46
D 0,56,1024
M 30,a[148:151],42
R 66,2,24576
v_fma_f32 v25,-v98,v113,v25
M 30,a[156:159],46
L 3
v_rcp_f32 v117,v117
M 30,a[164:167],50
R 70,2,25600
v_mul_f32 v21,v21,v117
M 30,a[172:175],54
D 0,56,2048
M 26,a[160:163],50
R 74,2,26624
v_cvt_pk_bf16_f32 v123,v20,v21
M 26,a[168:171],54
L 3
v_mul_f32 v118,v22,v22
v_mul_f32 v119,v23,v23
M 26,a[176:179],58
R 78,2,27648
v_mul_f32 v120,v24,v24
M 26,a[184:187],62
D 0,56,3072
M 30,a[180:183],58
s_add_u32 m0,s59,1024
R 82,2,28672
v_mul_f32 v121,v25,v25
M 30,a[188:191],62
L 3
buffer_load_dword v8,s[36:39],s58 offen offset:0 sc1 lds
M 30,a[196:199],66
s_add_u32 m0,s57,16384
R 86,2,29696
v_fma_f32 v118,v100,v118,v99
M 30,a[204:207],70
s_add_u32 s56,s55,16384
v_fma_f32 v119,v100,v119,v99
v_fma_f32 v120,v100,v120,v99
M 26,a[192:195],66
s_add_u32 s55,s55,s60
R 90,2,30720
v_fma_f32 v121,v100,v121,v99
M 26,a[200:203],70
L 3
D 0,56,0
M 26,a[208:211],74
s_waitcnt vmcnt(1)
R 94,2,31744
v_mul_f32 v118,v22,v118
M 26,a[216:219],78
s_barrier
ds_read_b32 v7,v6 offset:1024
v_mul_f32 v119,v23,v119
M 30,a[212:215],74
s_sub_u32 s57,s84,s57
R 34,1,0
v_mul_f32 v120,v24,v120
M 30,a[220:223],78
L 4
buffer_store_dword v4,v3,s[36:39],s70 offen offset:0 sc0 sc1
v_mul_f32 v121,v25,v121
v_exp_f32 v118,v118
v_exp_f32 v119,v119
v_exp_f32 v120,v120
v_exp_f32 v121,v121
M 30,a[228:231],82
s_mov_b32 s70,s69
R 38,1,1024
v_fma_f32 v118,v118,v101,v101
v_fma_f32 v119,v119,v101,v101
v_fma_f32 v120,v120,v101,v101
v_fma_f32 v121,v121,v101,v101
v_rcp_f32 v118,v118
v_rcp_f32 v119,v119
v_rcp_f32 v120,v120
v_rcp_f32 v121,v121
M 30,a[236:239],86
s_mov_b32 s69,s68
v_mul_f32 v22,v22,v118
v_mul_f32 v23,v23,v119
v_mul_f32 v24,v24,v120
v_mul_f32 v25,v25,v121
v_cvt_pk_bf16_f32 v124,v22,v23
v_cvt_pk_bf16_f32 v125,v24,v25
M 26,a[224:227],82
s_add_u32 s68,s68,s63
R 42,1,2048
buffer_store_dwordx4 v[122:125],v15,s[36:39],s66 offen offset:0 sc0 sc1
M 26,a[232:235],86
L 4
L 3
M 26,a[240:243],90
R 46,1,3072
v_cmp_eq_u32 vcc,s53,v7
M 26,a[248:251],94
D 0,56,1024
M 30,a[244:247],90
L 2
s_cmp_eq_u32 vcc_lo,-1
R 50,1,4096
buffer_store_dwordx4 v[122:125],v16,s[36:39],s66 offen offset:0 sc0 sc1
M 30,a[252:255],94
D 0,56,2048
D 0,56,3072
buffer_store_dword v4,v3,s[36:39],s71 offen offset:0 sc0 sc1
buffer_store_dword v4,v3,s[36:39],s71 offen offset:0 sc0 sc1
buffer_store_dword v4,v3,s[36:39],s71 offen offset:0 sc0 sc1
s_cbranch_scc1 Lg1_F0_%=
s_branch Lg1_K0_%=
Lg1_X1_%=:
M 30,a[124:127],94
L 3
s_add_u32 m0,s59,1024
v_cvt_pk_bf16_f32 v122,v18,v19
M 30,a[132:135],34
R 54,2,21504
v_fma_f32 v24,-v98,v112,v24
M 30,a[140:143],38
buffer_load_dword v8,s[36:39],s58 offen offset:0 sc1 lds
M 26,a[128:131],34
R 58,2,22528
v_fma_f32 v25,-v98,v113,v25
M 26,a[136:139],38
L 3
v_rcp_f32 v116,v116
M 26,a[144:147],42
R 62,2,23552
v_mul_f32 v20,v20,v116
M 26,a[152:155],46
v_rcp_f32 v117,v117
M 30,a[148:151],42
R 66,2,24576
v_mul_f32 v21,v21,v117
M 30,a[156:159],46
L 3
v_cvt_pk_bf16_f32 v123,v20,v21
v_mul_f32 v118,v22,v22
M 30,a[164:167],50
R 70,2,25600
v_mul_f32 v119,v23,v23
M 30,a[172:175],54
v_mul_f32 v120,v24,v24
v_mul_f32 v121,v25,v25
M 26,a[160:163],50
R 74,2,26624
v_fma_f32 v118,v100,v118,v99
M 26,a[168:171],54
L 3
v_fma_f32 v119,v100,v119,v99
v_fma_f32 v120,v100,v120,v99
M 26,a[176:179],58
R 78,2,27648
v_fma_f32 v121,v100,v121,v99
M 26,a[184:187],62
v_mul_f32 v118,v22,v118
v_mul_f32 v119,v23,v119
M 30,a[180:183],58
R 82,2,28672
v_mul_f32 v120,v24,v120
M 30,a[188:191],62
L 3
v_exp_f32 v118,v118
M 30,a[196:199],66
R 86,2,29696
v_mul_f32 v121,v25,v121
M 30,a[204:207],70
v_exp_f32 v119,v119
M 26,a[192:195],66
R 90,2,30720
v_fma_f32 v118,v118,v101,v101
M 26,a[200:203],70
L 3
v_exp_f32 v120,v120
M 26,a[208:211],74
s_waitcnt vmcnt(0)
R 94,2,31744
v_fma_f32 v119,v119,v101,v101
M 26,a[216:219],78
s_barrier
buffer_store_dword v4,v3,s[36:39],s70 offen offset:0 sc0 sc1
M 30,a[212:215],74
s_mov_b32 s70,s69
ds_read_b32 v7,v6 offset:1024
v_fma_f32 v120,v120,v101,v101
M 30,a[220:223],78
L 3
v_exp_f32 v121,v121
M 30,a[228:231],82
s_mov_b32 s69,s68
v_fma_f32 v121,v121,v101,v101
v_rcp_f32 v118,v118
v_rcp_f32 v119,v119
v_rcp_f32 v120,v120
v_rcp_f32 v121,v121
M 30,a[236:239],86
s_add_u32 s68,s68,s63
v_mul_f32 v22,v22,v118
v_mul_f32 v23,v23,v119
v_mul_f32 v24,v24,v120
v_mul_f32 v25,v25,v121
v_cvt_pk_bf16_f32 v124,v22,v23
v_cvt_pk_bf16_f32 v125,v24,v25
M 26,a[224:227],82
buffer_store_dwordx4 v[122:125],v15,s[36:39],s66 offen offset:0 sc0 sc1
M 26,a[232:235],86
L 1
M 26,a[240:243],90
L 0
v_cmp_eq_u32 vcc,s53,v7
M 26,a[248:251],94
buffer_store_dwordx4 v[122:125],v16,s[36:39],s66 offen offset:0 sc0 sc1
M 30,a[244:247],90
s_nop 0
M 30,a[252:255],94
s_mov_b32 s80,0
s_cmp_eq_u32 vcc_lo,-1
s_cbranch_scc0 Lg1_xn1_%=
s_add_u32 m0,s57,0
s_add_u32 s56,s55,0
D 0,56,0
v_fma_f32 v26,-v102,v106,v26
v_fma_f32 v27,-v102,v107,v27
v_fma_f32 v28,-v102,v108,v28
v_fma_f32 v29,-v102,v109,v29
v_mul_f32 v114,v26,v26
v_mul_f32 v115,v27,v27
v_mul_f32 v116,v28,v28
D 0,56,1024
v_mul_f32 v117,v29,v29
v_fma_f32 v114,v104,v114,v103
v_fma_f32 v115,v104,v115,v103
v_fma_f32 v116,v104,v116,v103
v_fma_f32 v117,v104,v117,v103
v_mul_f32 v114,v26,v114
v_mul_f32 v115,v27,v115
D 0,56,2048
)"
R"(v_mul_f32 v116,v28,v116
v_mul_f32 v117,v29,v117
v_exp_f32 v114,v114
v_exp_f32 v115,v115
v_exp_f32 v116,v116
v_exp_f32 v117,v117
v_fma_f32 v114,v114,v105,v105
D 0,56,3072
v_fma_f32 v115,v115,v105,v105
v_fma_f32 v116,v116,v105,v105
v_fma_f32 v117,v117,v105,v105
v_rcp_f32 v114,v114
v_rcp_f32 v115,v115
v_rcp_f32 v116,v116
v_rcp_f32 v117,v117
s_add_u32 s58,s58,s61
s_add_u32 m0,s57,16384
s_add_u32 s56,s55,16384
D 0,56,0
v_mul_f32 v26,v26,v114
v_mul_f32 v27,v27,v115
v_mul_f32 v28,v28,v116
v_mul_f32 v29,v29,v117
v_cvt_pk_bf16_f32 v122,v26,v27
v_cvt_pk_bf16_f32 v123,v28,v29
v_fma_f32 v30,-v102,v110,v30
D 0,56,1024
v_fma_f32 v31,-v102,v111,v31
v_fma_f32 v32,-v102,v112,v32
v_fma_f32 v33,-v102,v113,v33
v_mul_f32 v118,v30,v30
v_mul_f32 v119,v31,v31
v_mul_f32 v120,v32,v32
v_mul_f32 v121,v33,v33
D 0,56,2048
v_fma_f32 v118,v104,v118,v103
v_fma_f32 v119,v104,v119,v103
v_fma_f32 v120,v104,v120,v103
v_fma_f32 v121,v104,v121,v103
v_mul_f32 v118,v30,v118
v_mul_f32 v119,v31,v119
v_mul_f32 v120,v32,v120
D 0,56,3072
v_mul_f32 v121,v33,v121
v_exp_f32 v118,v118
v_exp_f32 v119,v119
v_exp_f32 v120,v120
v_exp_f32 v121,v121
v_fma_f32 v118,v118,v105,v105
v_fma_f32 v119,v119,v105,v105
s_add_u32 s55,s55,s60
s_sub_u32 s57,s84,s57
s_add_u32 s54,s54,s46
v_fma_f32 v120,v120,v105,v105
v_fma_f32 v121,v121,v105,v105
v_rcp_f32 v118,v118
v_rcp_f32 v119,v119
v_rcp_f32 v120,v120
v_rcp_f32 v121,v121
v_mul_f32 v30,v30,v118
v_mul_f32 v31,v31,v119
v_mul_f32 v32,v32,v120
v_mul_f32 v33,v33,v121
v_cvt_pk_bf16_f32 v124,v30,v31
v_cvt_pk_bf16_f32 v125,v32,v33
buffer_store_dwordx4 v[122:125],v15,s[36:39],s67 offen offset:0 sc0 sc1
buffer_store_dwordx4 v[122:125],v16,s[36:39],s67 offen offset:0 sc0 sc1
s_mov_b32 s72,s69
s_mov_b32 s69,0x7fff0000
s_mov_b32 s67,0x7fff0000
s_mov_b32 s77,0x7fff0000
s_branch Lg1_gox_%=
Lg1_xn1_%=:
s_cmp_ge_u32 s54,256
s_cbranch_scc1 Lg1_fin1_%=
s_add_u32 m0,s59,4096
s_nop 0
buffer_load_dword v8,s[36:39],s58 offen offset:0 sc1 lds
buffer_store_dword v4,v3,s[36:39],s71 offen offset:0 sc0 sc1
s_add_u32 m0,s59,5120
s_nop 0
buffer_load_dword v8,s[36:39],s58 offen offset:0 sc1 lds
buffer_store_dword v4,v3,s[36:39],s71 offen offset:0 sc0 sc1
s_mov_b32 s83,0
v_fma_f32 v26,-v102,v106,v26
v_fma_f32 v27,-v102,v107,v27
v_fma_f32 v28,-v102,v108,v28
v_fma_f32 v29,-v102,v109,v29
v_mul_f32 v114,v26,v26
v_mul_f32 v115,v27,v27
v_mul_f32 v116,v28,v28
v_mul_f32 v117,v29,v29
v_fma_f32 v114,v104,v114,v103
v_fma_f32 v115,v104,v115,v103
v_fma_f32 v116,v104,v116,v103
v_fma_f32 v117,v104,v117,v103
v_mul_f32 v114,v26,v114
v_mul_f32 v115,v27,v115
v_mul_f32 v116,v28,v116
v_mul_f32 v117,v29,v117
v_exp_f32 v114,v114
v_exp_f32 v115,v115
v_exp_f32 v116,v116
v_exp_f32 v117,v117
v_fma_f32 v114,v114,v105,v105
v_fma_f32 v115,v115,v105,v105
v_fma_f32 v116,v116,v105,v105
v_fma_f32 v117,v117,v105,v105
v_rcp_f32 v114,v114
v_rcp_f32 v115,v115
v_rcp_f32 v116,v116
v_rcp_f32 v117,v117
v_mul_f32 v26,v26,v114
v_mul_f32 v27,v27,v115
v_mul_f32 v28,v28,v116
v_mul_f32 v29,v29,v117
v_cvt_pk_bf16_f32 v122,v26,v27
v_cvt_pk_bf16_f32 v123,v28,v29
v_fma_f32 v30,-v102,v110,v30
v_fma_f32 v31,-v102,v111,v31
v_fma_f32 v32,-v102,v112,v32
v_fma_f32 v33,-v102,v113,v33
v_mul_f32 v118,v30,v30
v_mul_f32 v119,v31,v31
v_mul_f32 v120,v32,v32
v_mul_f32 v121,v33,v33
v_fma_f32 v118,v104,v118,v103
v_fma_f32 v119,v104,v119,v103
v_fma_f32 v120,v104,v120,v103
v_fma_f32 v121,v104,v121,v103
v_mul_f32 v118,v30,v118
v_mul_f32 v119,v31,v119
v_mul_f32 v120,v32,v120
v_mul_f32 v121,v33,v121
v_exp_f32 v118,v118
v_exp_f32 v119,v119
v_exp_f32 v120,v120
v_exp_f32 v121,v121
v_fma_f32 v118,v118,v105,v105
v_fma_f32 v119,v119,v105,v105
v_fma_f32 v120,v120,v105,v105
v_fma_f32 v121,v121,v105,v105
v_rcp_f32 v118,v118
v_rcp_f32 v119,v119
v_rcp_f32 v120,v120
v_rcp_f32 v121,v121
v_mul_f32 v30,v30,v118
v_mul_f32 v31,v31,v119
v_mul_f32 v32,v32,v120
v_mul_f32 v33,v33,v121
v_cvt_pk_bf16_f32 v124,v30,v31
v_cvt_pk_bf16_f32 v125,v32,v33
buffer_store_dwordx4 v[122:125],v15,s[36:39],s67 offen offset:0 sc0 sc1
buffer_store_dwordx4 v[122:125],v16,s[36:39],s67 offen offset:0 sc0 sc1
s_mov_b32 s72,s69
s_mov_b32 s69,0x7fff0000
s_mov_b32 s67,0x7fff0000
s_mov_b32 s77,0x7fff0000
s_branch Lg1_poll_%=
Lg1_fin1_%=:
v_fma_f32 v26,-v102,v106,v26
v_fma_f32 v27,-v102,v107,v27
v_fma_f32 v28,-v102,v108,v28
v_fma_f32 v29,-v102,v109,v29
v_mul_f32 v114,v26,v26
v_mul_f32 v115,v27,v27
v_mul_f32 v116,v28,v28
v_mul_f32 v117,v29,v29
v_fma_f32 v114,v104,v114,v103
v_fma_f32 v115,v104,v115,v103
v_fma_f32 v116,v104,v116,v103
v_fma_f32 v117,v104,v117,v103
v_mul_f32 v114,v26,v114
v_mul_f32 v115,v27,v115
v_mul_f32 v116,v28,v116
v_mul_f32 v117,v29,v117
v_exp_f32 v114,v114
v_exp_f32 v115,v115
v_exp_f32 v116,v116
v_exp_f32 v117,v117
v_fma_f32 v114,v114,v105,v105
v_fma_f32 v115,v115,v105,v105
v_fma_f32 v116,v116,v105,v105
v_fma_f32 v117,v117,v105,v105
v_rcp_f32 v114,v114
v_rcp_f32 v115,v115
v_rcp_f32 v116,v116
v_rcp_f32 v117,v117
v_mul_f32 v26,v26,v114
v_mul_f32 v27,v27,v115
v_mul_f32 v28,v28,v116
v_mul_f32 v29,v29,v117
v_cvt_pk_bf16_f32 v122,v26,v27
v_cvt_pk_bf16_f32 v123,v28,v29
v_fma_f32 v30,-v102,v110,v30
v_fma_f32 v31,-v102,v111,v31
v_fma_f32 v32,-v102,v112,v32
v_fma_f32 v33,-v102,v113,v33
v_mul_f32 v118,v30,v30
v_mul_f32 v119,v31,v31
v_mul_f32 v120,v32,v32
v_mul_f32 v121,v33,v33
v_fma_f32 v118,v104,v118,v103
v_fma_f32 v119,v104,v119,v103
v_fma_f32 v120,v104,v120,v103
v_fma_f32 v121,v104,v121,v103
v_mul_f32 v118,v30,v118
v_mul_f32 v119,v31,v119
v_mul_f32 v120,v32,v120
v_mul_f32 v121,v33,v121
v_exp_f32 v118,v118
v_exp_f32 v119,v119
v_exp_f32 v120,v120
v_exp_f32 v121,v121
v_fma_f32 v118,v118,v105,v105
v_fma_f32 v119,v119,v105,v105
v_fma_f32 v120,v120,v105,v105
v_fma_f32 v121,v121,v105,v105
v_rcp_f32 v118,v118
v_rcp_f32 v119,v119
v_rcp_f32 v120,v120
v_rcp_f32 v121,v121
v_mul_f32 v30,v30,v118
v_mul_f32 v31,v31,v119
v_mul_f32 v32,v32,v120
v_mul_f32 v33,v33,v121
v_cvt_pk_bf16_f32 v124,v30,v31
v_cvt_pk_bf16_f32 v125,v32,v33
buffer_store_dwordx4 v[122:125],v15,s[36:39],s67 offen offset:0 sc0 sc1
buffer_store_dwordx4 v[122:125],v16,s[36:39],s67 offen offset:0 sc0 sc1
s_mov_b32 s72,s69
s_mov_b32 s69,0x7fff0000
s_mov_b32 s67,0x7fff0000
s_mov_b32 s77,0x7fff0000
s_branch Lg1_done_%=
Lg1_done_%=:
s_waitcnt vmcnt(2)
s_barrier
buffer_store_dword v4,v3,s[36:39],s70 offen offset:0 sc0 sc1
s_waitcnt vmcnt(1)
s_barrier
buffer_store_dword v4,v3,s[36:39],s72 offen offset:0 sc0 sc1
s_waitcnt vmcnt(0) lgkmcnt(0)
s_barrier
s_branch Lg2_end_%=
Lg2_begin_%=:
Lcode_g2_b:
s_mov_b64 s[36:37],%1
s_mov_b32 s38,111243392
s_mov_b32 s39,0x00020000
s_mov_b32 s44,%2
s_mov_b32 s45,%3
s_mov_b32 s46,%4
s_mov_b32 s47,%5
s_mov_b32 s48,%6
s_mov_b32 s49,%7
s_mov_b32 s50,%8
s_mov_b32 s51,%9
s_mov_b32 s53,%10
v_mov_b32 v0,%11
v_mov_b32 v3,%12
v_mov_b32 v5,%13
v_mov_b32 v6,%14
v_mov_b32 v18,%15
v_mov_b32 v19,%16
v_mov_b32 v20,%17
v_mov_b32 v21,%18
v_mov_b32 v9,%19
v_mov_b32 v10,%20
v_mov_b32 v11,%21
v_mov_b32 v12,%22
v_mov_b32 v13,%23
v_mov_b32 v14,%24
v_mov_b32 v15,%25
v_mov_b32 v16,%26
s_mov_b64 s[40:41],%27
s_mov_b32 s42,2097152
s_mov_b32 s43,0x00020000
s_lshl_b32 s57,s44,11
s_cmp_ge_u32 s45,8
s_cbranch_scc1 Lg2_dly_%=
s_sleep 16
s_sleep 16
s_sleep 16
s_sleep 16
Lg2_dly_%=:
s_add_u32 s55,s50,0
s_add_u32 m0,s57,0
s_nop 0
buffer_load_dwordx4 v18,s[40:43],s55 offen offset:0  lds
s_add_u32 m0,s57,1024
s_nop 0
buffer_load_dwordx4 v19,s[40:43],s55 offen offset:0  lds
s_add_u32 m0,s57,8064
s_nop 0
buffer_load_dwordx4 v18,s[40:43],s55 offen offset:128  lds
s_add_u32 m0,s57,9088
s_nop 0
buffer_load_dwordx4 v19,s[40:43],s55 offen offset:128  lds
s_add_u32 s55,s50,131072
s_add_u32 m0,s57,16384
s_nop 0
buffer_load_dwordx4 v18,s[40:43],s55 offen offset:0  lds
s_add_u32 m0,s57,17408
s_nop 0
buffer_load_dwordx4 v19,s[40:43],s55 offen offset:0  lds
s_add_u32 m0,s57,24448
s_nop 0
buffer_load_dwordx4 v18,s[40:43],s55 offen offset:128  lds
s_add_u32 m0,s57,25472
s_nop 0
buffer_load_dwordx4 v19,s[40:43],s55 offen offset:128  lds
s_add_u32 s55,s50,262144
s_add_u32 m0,s57,32768
s_nop 0
buffer_load_dwordx4 v18,s[40:43],s55 offen offset:0  lds
s_add_u32 m0,s57,33792
s_nop 0
buffer_load_dwordx4 v19,s[40:43],s55 offen offset:0  lds
s_add_u32 m0,s57,40832
s_nop 0
buffer_load_dwordx4 v18,s[40:43],s55 offen offset:128  lds
s_add_u32 m0,s57,41856
s_nop 0
buffer_load_dwordx4 v19,s[40:43],s55 offen offset:128  lds
s_add_u32 s55,s50,393216
s_add_u32 m0,s57,49152
s_nop 0
buffer_load_dwordx4 v18,s[40:43],s55 offen offset:0  lds
s_add_u32 m0,s57,50176
s_nop 0
buffer_load_dwordx4 v19,s[40:43],s55 offen offset:0  lds
s_add_u32 m0,s57,57216
s_nop 0
buffer_load_dwordx4 v18,s[40:43],s55 offen offset:128  lds
s_add_u32 m0,s57,58240
s_nop 0
buffer_load_dwordx4 v19,s[40:43],s55 offen offset:128  lds
s_add_u32 s55,s50,524288
s_add_u32 m0,s57,65536
s_nop 0
buffer_load_dwordx4 v18,s[40:43],s55 offen offset:0  lds
s_add_u32 m0,s57,66560
s_nop 0
buffer_load_dwordx4 v19,s[40:43],s55 offen offset:0  lds
s_add_u32 m0,s57,73600
s_nop 0
buffer_load_dwordx4 v18,s[40:43],s55 offen offset:128  lds
s_add_u32 m0,s57,74624
s_nop 0
buffer_load_dwordx4 v19,s[40:43],s55 offen offset:128  lds
s_add_u32 s55,s50,655360
s_add_u32 m0,s57,81920
s_nop 0
buffer_load_dwordx4 v18,s[40:43],s55 offen offset:0  lds
s_add_u32 m0,s57,82944
s_nop 0
buffer_load_dwordx4 v19,s[40:43],s55 offen offset:0  lds
s_add_u32 m0,s57,89984
s_nop 0
buffer_load_dwordx4 v18,s[40:43],s55 offen offset:128  lds
s_add_u32 m0,s57,91008
s_nop 0
buffer_load_dwordx4 v19,s[40:43],s55 offen offset:128  lds
s_waitcnt vmcnt(20) lgkmcnt(0)
s_barrier
s_add_u32 s55,s50,786432
s_add_u32 m0,s57,98304
s_nop 0
buffer_load_dwordx4 v18,s[40:43],s55 offen offset:0  lds
s_add_u32 m0,s57,99328
s_nop 0
buffer_load_dwordx4 v19,s[40:43],s55 offen offset:0  lds
s_add_u32 m0,s57,106368
s_nop 0
buffer_load_dwordx4 v18,s[40:43],s55 offen offset:128  lds
s_add_u32 m0,s57,107392
s_nop 0
buffer_load_dwordx4 v19,s[40:43],s55 offen offset:128  lds
v_add_u32 v26,0,v20
v_add_u32 v27,0,v21
T a[0:1],26,0
T a[2:3],26,512
T a[4:5],27,0
T a[6:7],27,512
T a[8:9],26,4096
T a[10:11],26,4608
T a[12:13],27,4096
T a[14:15],27,4608
s_waitcnt vmcnt(20) lgkmcnt(0)
s_barrier
s_add_u32 s55,s50,917504
s_add_u32 m0,s57,0
s_nop 0
buffer_load_dwordx4 v18,s[40:43],s55 offen offset:0  lds
s_add_u32 m0,s57,1024
s_nop 0
buffer_load_dwordx4 v19,s[40:43],s55 offen offset:0  lds
s_add_u32 m0,s57,8064
s_nop 0
buffer_load_dwordx4 v18,s[40:43],s55 offen offset:128  lds
s_add_u32 m0,s57,9088
s_nop 0
buffer_load_dwordx4 v19,s[40:43],s55 offen offset:128  lds
v_add_u32 v26,16384,v20
v_add_u32 v27,16384,v21
T a[16:17],26,0
T a[18:19],26,512
T a[20:21],27,0
T a[22:23],27,512
T a[24:25],26,4096
T a[26:27],26,4608
T a[28:29],27,4096
T a[30:31],27,4608
s_waitcnt vmcnt(20) lgkmcnt(0)
s_barrier
s_add_u32 s55,s50,1048576
s_add_u32 m0,s57,16384
s_nop 0
buffer_load_dwordx4 v18,s[40:43],s55 offen offset:0  lds
s_add_u32 m0,s57,17408
s_nop 0
buffer_load_dwordx4 v19,s[40:43],s55 offen offset:0  lds
s_add_u32 m0,s57,24448
s_nop 0
buffer_load_dwordx4 v18,s[40:43],s55 offen offset:128  lds
s_add_u32 m0,s57,25472
s_nop 0
buffer_load_dwordx4 v19,s[40:43],s55 offen offset:128  lds
v_add_u32 v26,32768,v20
v_add_u32 v27,32768,v21
T a[32:33],26,0
T a[34:35],26,512
T a[36:37],27,0
T a[38:39],27,512
T a[40:41],26,4096
T a[42:43],26,4608
T a[44:45],27,4096
T a[46:47],27,4608
s_waitcnt vmcnt(20) lgkmcnt(0)
s_barrier
s_add_u32 s55,s50,1179648
s_add_u32 m0,s57,32768
s_nop 0
buffer_load_dwordx4 v18,s[40:43],s55 offen offset:0  lds
s_add_u32 m0,s57,33792
s_nop 0
buffer_load_dwordx4 v19,s[40:43],s55 offen offset:0  lds
s_add_u32 m0,s57,40832
s_nop 0
buffer_load_dwordx4 v18,s[40:43],s55 offen offset:128  lds
s_add_u32 m0,s57,41856
s_nop 0
buffer_load_dwordx4 v19,s[40:43],s55 offen offset:128  lds
v_add_u32 v26,49152,v20
v_add_u32 v27,49152,v21
T a[48:49],26,0
T a[50:51],26,512
T a[52:53],27,0
T a[54:55],27,512
T a[56:57],26,4096
T a[58:59],26,4608
T a[60:61],27,4096
T a[62:63],27,4608
s_waitcnt vmcnt(20) lgkmcnt(0)
s_barrier
s_add_u32 s55,s50,1310720
s_add_u32 m0,s57,49152
s_nop 0
buffer_load_dwordx4 v18,s[40:43],s55 offen offset:0  lds
s_add_u32 m0,s57,50176
s_nop 0
buffer_load_dwordx4 v19,s[40:43],s55 offen offset:0  lds
s_add_u32 m0,s57,57216
s_nop 0
buffer_load_dwordx4 v18,s[40:43],s55 offen offset:128  lds
s_add_u32 m0,s57,58240
s_nop 0
buffer_load_dwordx4 v19,s[40:43],s55 offen offset:128  lds
v_add_u32 v26,65536,v20
v_add_u32 v27,65536,v21
T a[64:65],26,0
T a[66:67],26,512
T a[68:69],27,0
T a[70:71],27,512
T a[72:73],26,4096
T a[74:75],26,4608
T a[76:77],27,4096
T a[78:79],27,4608
s_waitcnt vmcnt(20) lgkmcnt(0)
s_barrier
s_add_u32 s55,s50,1441792
s_add_u32 m0,s57,65536
s_nop 0
buffer_load_dwordx4 v18,s[40:43],s55 offen offset:0  lds
s_add_u32 m0,s57,66560
s_nop 0
buffer_load_dwordx4 v19,s[40:43],s55 offen offset:0  lds
s_add_u32 m0,s57,73600
s_nop 0
)"
R"(buffer_load_dwordx4 v18,s[40:43],s55 offen offset:128  lds
s_add_u32 m0,s57,74624
s_nop 0
buffer_load_dwordx4 v19,s[40:43],s55 offen offset:128  lds
v_add_u32 v26,81920,v20
v_add_u32 v27,81920,v21
T a[80:81],26,0
T a[82:83],26,512
T a[84:85],27,0
T a[86:87],27,512
T a[88:89],26,4096
T a[90:91],26,4608
T a[92:93],27,4096
T a[94:95],27,4608
s_waitcnt vmcnt(20) lgkmcnt(0)
s_barrier
s_add_u32 s55,s50,1572864
s_add_u32 m0,s57,81920
s_nop 0
buffer_load_dwordx4 v18,s[40:43],s55 offen offset:0  lds
s_add_u32 m0,s57,82944
s_nop 0
buffer_load_dwordx4 v19,s[40:43],s55 offen offset:0  lds
s_add_u32 m0,s57,89984
s_nop 0
buffer_load_dwordx4 v18,s[40:43],s55 offen offset:128  lds
s_add_u32 m0,s57,91008
s_nop 0
buffer_load_dwordx4 v19,s[40:43],s55 offen offset:128  lds
v_add_u32 v26,98304,v20
v_add_u32 v27,98304,v21
T a[96:97],26,0
T a[98:99],26,512
T a[100:101],27,0
T a[102:103],27,512
T a[104:105],26,4096
T a[106:107],26,4608
T a[108:109],27,4096
T a[110:111],27,4608
s_waitcnt vmcnt(20) lgkmcnt(0)
s_barrier
s_add_u32 s55,s50,1703936
s_add_u32 m0,s57,98304
s_nop 0
buffer_load_dwordx4 v18,s[40:43],s55 offen offset:0  lds
s_add_u32 m0,s57,99328
s_nop 0
buffer_load_dwordx4 v19,s[40:43],s55 offen offset:0  lds
s_add_u32 m0,s57,106368
s_nop 0
buffer_load_dwordx4 v18,s[40:43],s55 offen offset:128  lds
s_add_u32 m0,s57,107392
s_nop 0
buffer_load_dwordx4 v19,s[40:43],s55 offen offset:128  lds
v_add_u32 v26,0,v20
v_add_u32 v27,0,v21
T a[112:113],26,0
T a[114:115],26,512
T a[116:117],27,0
T a[118:119],27,512
T a[120:121],26,4096
T a[122:123],26,4608
T a[124:125],27,4096
T a[126:127],27,4608
s_waitcnt vmcnt(20) lgkmcnt(0)
s_barrier
s_add_u32 s55,s50,1835008
s_add_u32 m0,s57,0
s_nop 0
buffer_load_dwordx4 v18,s[40:43],s55 offen offset:0  lds
s_add_u32 m0,s57,1024
s_nop 0
buffer_load_dwordx4 v19,s[40:43],s55 offen offset:0  lds
s_add_u32 m0,s57,8064
s_nop 0
buffer_load_dwordx4 v18,s[40:43],s55 offen offset:128  lds
s_add_u32 m0,s57,9088
s_nop 0
buffer_load_dwordx4 v19,s[40:43],s55 offen offset:128  lds
v_add_u32 v26,16384,v20
v_add_u32 v27,16384,v21
T a[128:129],26,0
T a[130:131],26,512
T a[132:133],27,0
T a[134:135],27,512
T a[136:137],26,4096
T a[138:139],26,4608
T a[140:141],27,4096
T a[142:143],27,4608
s_waitcnt vmcnt(20) lgkmcnt(0)
s_barrier
s_add_u32 s55,s50,1966080
s_add_u32 m0,s57,16384
s_nop 0
buffer_load_dwordx4 v18,s[40:43],s55 offen offset:0  lds
s_add_u32 m0,s57,17408
s_nop 0
buffer_load_dwordx4 v19,s[40:43],s55 offen offset:0  lds
s_add_u32 m0,s57,24448
s_nop 0
buffer_load_dwordx4 v18,s[40:43],s55 offen offset:128  lds
s_add_u32 m0,s57,25472
s_nop 0
buffer_load_dwordx4 v19,s[40:43],s55 offen offset:128  lds
v_add_u32 v26,32768,v20
v_add_u32 v27,32768,v21
T a[144:145],26,0
T a[146:147],26,512
T a[148:149],27,0
T a[150:151],27,512
T a[152:153],26,4096
T a[154:155],26,4608
T a[156:157],27,4096
T a[158:159],27,4608
s_waitcnt vmcnt(20) lgkmcnt(0)
s_barrier
v_add_u32 v26,49152,v20
v_add_u32 v27,49152,v21
T a[160:161],26,0
T a[162:163],26,512
T a[164:165],27,0
T a[166:167],27,512
T a[168:169],26,4096
T a[170:171],26,4608
T a[172:173],27,4096
T a[174:175],27,4608
s_waitcnt vmcnt(16) lgkmcnt(0)
s_barrier
v_add_u32 v26,65536,v20
v_add_u32 v27,65536,v21
T a[176:177],26,0
T a[178:179],26,512
T a[180:181],27,0
T a[182:183],27,512
T a[184:185],26,4096
T a[186:187],26,4608
T a[188:189],27,4096
T a[190:191],27,4608
s_waitcnt vmcnt(12) lgkmcnt(0)
s_barrier
v_add_u32 v26,81920,v20
v_add_u32 v27,81920,v21
T a[192:193],26,0
T a[194:195],26,512
T a[196:197],27,0
T a[198:199],27,512
T a[200:201],26,4096
T a[202:203],26,4608
T a[204:205],27,4096
T a[206:207],27,4608
s_waitcnt vmcnt(8) lgkmcnt(0)
s_barrier
v_add_u32 v26,98304,v20
v_add_u32 v27,98304,v21
T a[208:209],26,0
T a[210:211],26,512
T a[212:213],27,0
T a[214:215],27,512
T a[216:217],26,4096
T a[218:219],26,4608
T a[220:221],27,4096
T a[222:223],27,4608
s_waitcnt vmcnt(4) lgkmcnt(0)
s_barrier
v_add_u32 v26,0,v20
v_add_u32 v27,0,v21
T a[224:225],26,0
T a[226:227],26,512
T a[228:229],27,0
T a[230:231],27,512
T a[232:233],26,4096
T a[234:235],26,4608
T a[236:237],27,4096
T a[238:239],27,4608
s_waitcnt vmcnt(0) lgkmcnt(0)
s_barrier
v_add_u32 v26,16384,v20
v_add_u32 v27,16384,v21
T a[240:241],26,0
T a[242:243],26,512
T a[244:245],27,0
T a[246:247],27,512
T a[248:249],26,4096
T a[250:251],26,4608
T a[252:253],27,4096
T a[254:255],27,4608
L 0
s_barrier
v_mov_b32 v4,s53
s_mov_b32 s82,0
s_mov_b32 s80,0
s_mov_b32 s54,s45
s_mul_i32 s57,s44,5120
s_mul_i32 s55,s45,40960
s_add_u32 s55,s55,s57
s_add_u32 s55,s55,17825792
s_mul_i32 s57,s44,4096
s_mul_i32 s60,s46,40960
s_mul_i32 s61,s46,1024
s_mul_i32 s62,s46,32768
s_mul_i32 s63,s46,1024
s_mul_i32 s64,s46,512
s_lshl_b32 s59,s44,8
s_add_u32 s59,s59,131072
s_mul_i32 s58,s45,1024
s_add_u32 s58,s58,109333504
s_mul_i32 s65,s45,32768
s_add_u32 s65,s65,s47
s_mul_i32 s68,s45,1024
s_add_u32 s68,s68,s48
s_mul_i32 s73,s45,512
s_add_u32 s73,s73,106954752
s_mul_i32 s74,s45,32768
s_add_u32 s74,s74,s49
s_mul_i32 s75,s45,1024
s_add_u32 s75,s75,107479040
s_mov_b32 s66,0x7fff0000
s_mov_b32 s67,0x7fff0000
s_mov_b32 s69,0x7fff0000
s_mov_b32 s76,0x7fff0000
s_mov_b32 s77,0x7fff0000
s_mov_b32 s70,0x7fff0000
s_mov_b32 s71,0x7fff0000
s_mov_b32 s72,0x7fff0000
v_mov_b32 v1,v0
v_add_u32 v2,16384,v0
s_mov_b32 s84,16384
s_mov_b32 s86,0x3a800000
s_mov_b32 s87,0x38a7c5ac
s_mov_b32 s88,0xc0135761
s_mov_b32 s89,0x3d372713
s_lshr_b32 s85,s44,1
s_mul_i32 s85,s85,127
v_lshlrev_b32 v8,22,v0
v_and_b32 v8,0x80000000,v8
v_or_b32 v8,v8,v5
v_add_u32 v2,32768,v0
s_lshl_b32 s84,s57,1
s_add_u32 s84,s84,32768
s_getpc_b64 s[40:41]
Lg2_pc_%=:
s_lshl_b32 s78,s44,13
v_lshlrev_b32 v106,3,v0
v_add_u32 v106,s78,v106
v_min_u32 v106,Lcode_g12_e-Lg2_pc_%=-4,v106
s_mov_b32 m0,163584
s_nop 0
global_load_lds_dword v106,s[40:41]
s_and_b32 s79,s54,127
s_cmp_eq_u32 s79,0
s_cselect_b32 s94,0x3300,0
s_cmp_eq_u32 s79,127
s_cselect_b32 s94,0xcc00,s94
s_add_u32 m0,s59,4096
s_nop 0
buffer_load_dword v8,s[36:39],s58 offen offset:0 sc1 lds
buffer_store_dword v4,v3,s[36:39],s71 offen offset:0 sc0 sc1
s_add_u32 m0,s59,5120
s_nop 0
buffer_load_dword v8,s[36:39],s58 offen offset:0 sc1 lds
buffer_store_dword v4,v3,s[36:39],s71 offen offset:0 sc0 sc1
s_mov_b32 s83,0
Lg2_poll_%=:
s_add_u32 s78,s83,2
s_and_b32 s78,s78,3
s_lshl_b32 s78,s78,10
s_add_u32 s78,s78,4096
s_add_u32 m0,s59,s78
s_nop 0
buffer_load_dword v8,s[36:39],s58 offen offset:0 sc1 lds
s_and_b32 s78,s83,3
s_lshl_b32 s78,s78,10
v_add_u32 v106,s78,v6
s_waitcnt vmcnt(4)
s_barrier
ds_read_b32 v7,v106 offset:4096
buffer_store_dword v4,v3,s[36:39],s70 offen offset:0 sc0 sc1
s_mov_b32 s70,s71
s_mov_b32 s71,s72
s_mov_b32 s72,0x7fff0000
s_add_u32 s83,s83,1
s_add_u32 s82,s82,1
L 0
v_cmp_eq_u32 vcc,s53,v7
s_nop 0
s_or_b64 vcc,vcc,s[94:95]
s_cmp_eq_u32 vcc_lo,-1
s_cbranch_scc1 Lg2_go_%=
s_cmp_lt_u32 s82,20000
s_cbranch_scc1 Lg2_poll_%=
Lg2_go_%=:
s_and_b32 s78,s54,127
s_cmp_eq_u32 s78,s85
s_cselect_b64 s[40:41],-1,0
v_cndmask_b32 v17,v15,v16,s[40:41]
s_add_u32 m0,s57,0
s_add_u32 s56,s55,0
D 17,56,0
s_add_u32 s56,s55,256
D 17,56,1024
s_add_u32 s56,s55,512
D 17,56,2048
s_add_u32 s56,s55,768
D 17,56,3072
s_add_u32 s58,s58,s61
s_add_u32 m0,s59,2048
s_nop 0
buffer_load_dword v8,s[36:39],s58 offen offset:0 sc1 lds
v_cndmask_b32 v17,v10,v9,s[40:41]
s_add_u32 m0,s57,16384
s_add_u32 s56,s55,20480
D 17,56,0
s_add_u32 s56,s55,20736
D 17,56,1024
s_add_u32 s56,s55,20992
D 17,56,2048
s_add_u32 s56,s55,21248
D 17,56,3072
s_add_u32 s55,s55,s60
s_sub_u32 s57,s84,s57
s_add_u32 s54,s54,s46
s_and_b32 s79,s54,127
s_cmp_eq_u32 s79,0
s_cselect_b32 s94,0x3300,0
s_cmp_eq_u32 s79,127
s_cselect_b32 s94,0xcc00,s94
s_waitcnt vmcnt(4)
s_barrier
buffer_store_dword v4,v3,s[36:39],s70 offen offset:0 sc0 sc1
s_min_u32 s71,s71,s72
buffer_store_dword v4,v3,s[36:39],s71 offen offset:0 sc0 sc1
s_mov_b32 s70,0x7fff0000
s_mov_b32 s71,0x7fff0000
s_mov_b32 s72,0x7fff0000
s_cmp_eq_u32 s80,1
buffer_store_dword v4,v3,s[36:39],s71 offen offset:0 sc0 sc1
s_cbranch_scc1 Lg2_st1_%=
s_branch Lg2_st0_%=
Lg2_gox_%=:
s_add_u32 m0,s59,2048
s_nop 0
buffer_load_dword v8,s[36:39],s58 offen offset:0 sc1 lds
s_and_b32 s79,s54,127
s_cmp_eq_u32 s79,0
s_cselect_b32 s94,0x3300,0
s_cmp_eq_u32 s79,127
s_cselect_b32 s94,0xcc00,s94
s_waitcnt vmcnt(0)
s_barrier
buffer_store_dword v4,v3,s[36:39],s70 offen offset:0 sc0 sc1
s_min_u32 s71,s71,s72
buffer_store_dword v4,v3,s[36:39],s71 offen offset:0 sc0 sc1
s_mov_b32 s70,0x7fff0000
s_mov_b32 s71,0x7fff0000
s_mov_b32 s72,0x7fff0000
s_cmp_eq_u32 s80,1
s_cbranch_scc1 Lg2_st1_%=
Lg2_st0_%=:
ds_read_b32 v7,v6 offset:2048
R 34,1,0
R 38,1,1024
R 42,1,2048
R 46,1,3072
R 50,1,4096
L 5
v_cmp_eq_u32 vcc,s53,v7
s_nop 0
s_or_b64 vcc,vcc,s[94:95]
L 3
s_cmp_eq_u32 vcc_lo,-1
s_cbranch_scc1 Lg2_F0_%=
s_branch Lg2_L0_%=
Lg2_st1_%=:
ds_read_b32 v7,v6 offset:2048
R 34,2,0
R 38,2,1024
R 42,2,2048
R 46,2,3072
R 50,2,4096
L 5
v_cmp_eq_u32 vcc,s53,v7
s_nop 0
s_or_b64 vcc,vcc,s[94:95]
L 3
s_cmp_eq_u32 vcc_lo,-1
s_cbranch_scc1 Lg2_F1_%=
s_branch Lg2_L1_%=
Lg2_F0_%=:
v_mfma_f32_16x16x32_bf16 v[22:25],a[4:7],v[34:37],0
s_add_u32 s58,s58,s61
s_add_u32 m0,s59,0
s_and_b32 s78,s54,127
R 54,1,5120
M 22,a[12:15],38
s_cmp_eq_u32 s78,s85
s_cselect_b64 s[40:41],-1,0
s_add_u32 s56,s55,0
buffer_load_dword v8,s[36:39],s58 offen offset:0 sc1 lds
v_mfma_f32_16x16x32_bf16 v[18:21],a[0:3],v[34:37],0
s_add_u32 m0,s57,0
R 58,1,6144
v_cndmask_b32 v17,v15,v16,s[40:41]
M 18,a[8:11],38
L 3
D 17,56,0
M 18,a[16:19],42
s_add_u32 s56,s55,256
R 62,1,7168
v_lshlrev_b32 v106,16,v126
M 18,a[24:27],46
s_add_u32 s54,s54,s46
v_and_b32 v107,0xffff0000,v126
v_lshlrev_b32 v108,16,v127
M 22,a[20:23],42
s_and_b32 s79,s54,127
R 66,1,8192
v_and_b32 v109,0xffff0000,v127
M 22,a[28:31],46
L 3
D 17,56,1024
M 22,a[36:39],50
s_add_u32 s56,s55,512
R 70,1,9216
v_lshlrev_b32 v110,16,v128
M 22,a[44:47],54
s_cmp_eq_u32 s79,0
v_and_b32 v111,0xffff0000,v128
v_lshlrev_b32 v112,16,v129
M 18,a[32:35],50
s_cselect_b32 s94,0x3300,0
R 74,1,10240
v_and_b32 v113,0xffff0000,v129
M 18,a[40:43],54
L 3
D 17,56,2048
M 18,a[48:51],58
s_add_u32 s56,s55,768
R 78,1,11264
v_fma_f32 v106,v106,v103,v102
M 18,a[56:59],62
D 17,56,3072
M 22,a[52:55],58
s_add_u32 m0,s57,16384
R 82,1,12288
v_fma_f32 v107,v107,v103,v102
M 22,a[60:63],62
L 3
v_fma_f32 v108,v108,v103,v102
v_fma_f32 v109,v109,v103,v102
M 22,a[68:71],66
s_add_u32 s56,s55,20480
R 86,1,13312
v_fma_f32 v110,v110,v103,v102
M 22,a[76:79],70
buffer_load_dwordx4 v[122:125],v0,s[36:39],s74 offen offset:0 sc1
M 18,a[64:67],66
s_cmp_eq_u32 s79,127
R 90,1,14336
v_fma_f32 v111,v111,v103,v102
M 18,a[72:75],70
L 3
buffer_load_dwordx2 v[98:99],v11,s[36:39],s73 offen offset:256 sc1
M 18,a[80:83],74
s_waitcnt vmcnt(10)
R 94,1,15360
v_fma_f32 v112,v112,v103,v102
M 18,a[88:91],78
s_barrier
v_fma_f32 v113,v113,v103,v102
v_add_f32 v26,v26,v106
M 22,a[84:87],74
s_cselect_b32 s94,0xcc00,s94
R 34,1,16384
v_add_f32 v27,v27,v107
M 22,a[92:95],78
L 3
v_add_f32 v28,v28,v108
v_add_f32 v29,v29,v109
M 22,a[100:103],82
s_mov_b32 s66,s65
R 38,1,17408
v_add_f32 v30,v30,v110
M 22,a[108:111],86
s_mov_b32 s76,s75
v_cndmask_b32 v17,v10,v9,s[40:41]
v_add_f32 v31,v31,v111
M 18,a[96:99],82
s_add_u32 s73,s73,s64
R 42,1,18432
v_add_f32 v32,v32,v112
M 18,a[104:107],86
L 3
D 17,56,0
M 18,a[112:115],90
s_add_u32 s56,s55,20736
R 46,1,19456
v_add_f32 v33,v33,v113
M 18,a[120:123],94
s_add_u32 s74,s74,s62
v_cvt_pk_bf16_f32 v114,v26,v27
)"
R"(v_cvt_pk_bf16_f32 v115,v28,v29
M 22,a[116:119],90
s_add_u32 s65,s65,s62
R 50,1,20480
v_fma_f32 v107,v26,v26,s87
M 22,a[124:127],94
L 3
v_add_f32 v106,v26,v27
v_fma_f32 v107,v27,v27,v107
M 22,a[132:135],34
s_add_u32 s75,s75,s63
R 54,1,21504
v_add_f32 v106,v106,v28
M 22,a[140:143],38
D 17,56,1024
M 18,a[128:131],34
s_add_u32 s56,s55,20992
R 58,1,22528
v_fma_f32 v107,v28,v28,v107
M 18,a[136:139],38
L 3
v_add_f32 v106,v106,v29
v_fma_f32 v107,v29,v29,v107
M 18,a[144:147],42
s_sub_u32 s57,s84,s57
R 62,1,23552
v_add_f32 v106,v106,v30
M 18,a[152:155],46
v_fma_f32 v107,v30,v30,v107
v_add_f32 v106,v106,v31
M 22,a[148:151],42
R 66,1,24576
v_fma_f32 v107,v31,v31,v107
M 22,a[156:159],46
L 3
v_add_f32 v106,v106,v32
v_fma_f32 v107,v32,v32,v107
M 22,a[164:167],50
R 70,1,25600
v_add_f32 v106,v106,v33
M 22,a[172:175],54
v_fma_f32 v107,v33,v33,v107
v_cvt_pk_bf16_f32 v116,v30,v31
M 18,a[160:163],50
R 74,1,26624
v_cvt_pk_bf16_f32 v117,v32,v33
M 18,a[168:171],54
L 3
v_permlane16_swap_b32 v106,v107
v_add_f32 v106,v106,v107
v_mov_b32 v107,v106
M 18,a[176:179],58
R 78,1,27648
M 18,a[184:187],62
v_permlane32_swap_b32 v106,v107
v_add_f32 v106,v106,v107
M 22,a[180:183],58
R 82,1,28672
ds_write_b32 v12,v106 offset:2048
M 22,a[188:191],62
L 4
D 17,56,2048
M 22,a[196:199],66
s_add_u32 s56,s55,21248
s_add_u32 s55,s55,s60
R 86,1,29696
M 22,a[204:207],70
D 17,56,3072
M 18,a[192:195],66
R 90,1,30720
M 18,a[200:203],70
L 4
L 2
buffer_store_dwordx4 v[114:117],v0,s[36:39],s67 offen offset:0 sc0 sc1
M 18,a[208:211],74
s_waitcnt vmcnt(7)
R 94,1,31744
M 18,a[216:219],78
s_barrier
ds_read_b32 v7,v6 offset:0
R 118,13,2048
M 22,a[212:215],74
R 34,2,0
M 22,a[220:223],78
L 5
buffer_store_dword v4,v3,s[36:39],s70 offen offset:0 sc0 sc1
M 22,a[228:231],82
s_mov_b32 s70,s69
s_mov_b32 s69,s68
R 38,2,1024
M 22,a[236:239],86
s_add_u32 s68,s68,s63
M 18,a[224:227],82
R 42,2,2048
M 18,a[232:235],86
L 5
L 4
v_cmp_eq_u32 vcc,s53,v7
M 18,a[240:243],90
L 3
R 46,2,3072
v_add_f32 v118,v118,v119
M 18,a[248:251],94
s_or_b64 vcc,vcc,s[94:95]
v_add_f32 v120,v120,v121
v_add_f32 v118,v118,v120
M 22,a[244:247],90
s_cmp_eq_u32 vcc_lo,-1
R 50,2,4096
v_mul_f32 v118,s86,v118
M 22,a[252:255],94
s_waitcnt vmcnt(6) lgkmcnt(3)
buffer_store_dword v118,v14,s[36:39],s77 offen offset:0 sc0 sc1
s_cbranch_scc0 Lg2_K1_%=
Lg2_F1_%=:
v_mfma_f32_16x16x32_bf16 v[30:33],a[4:7],v[34:37],0
s_add_u32 s58,s58,s61
s_add_u32 m0,s59,1024
s_and_b32 s78,s54,127
R 54,2,5120
M 30,a[12:15],38
s_cmp_eq_u32 s78,s85
s_cselect_b64 s[40:41],-1,0
s_add_u32 s56,s55,0
buffer_load_dword v8,s[36:39],s58 offen offset:0 sc1 lds
v_mfma_f32_16x16x32_bf16 v[26:29],a[0:3],v[34:37],0
s_add_u32 m0,s57,0
R 58,2,6144
v_cndmask_b32 v17,v15,v16,s[40:41]
M 26,a[8:11],38
L 3
D 17,56,0
M 26,a[16:19],42
s_add_u32 s56,s55,256
R 62,2,7168
v_lshlrev_b32 v106,16,v122
M 26,a[24:27],46
s_add_u32 s54,s54,s46
v_and_b32 v107,0xffff0000,v122
v_lshlrev_b32 v108,16,v123
M 30,a[20:23],42
s_and_b32 s79,s54,127
R 66,2,8192
v_and_b32 v109,0xffff0000,v123
M 30,a[28:31],46
L 3
D 17,56,1024
M 30,a[36:39],50
s_add_u32 s56,s55,512
R 70,2,9216
v_lshlrev_b32 v110,16,v124
M 30,a[44:47],54
s_cmp_eq_u32 s79,0
v_and_b32 v111,0xffff0000,v124
v_lshlrev_b32 v112,16,v125
M 26,a[32:35],50
s_cselect_b32 s94,0x3300,0
R 74,2,10240
v_and_b32 v113,0xffff0000,v125
M 26,a[40:43],54
L 3
D 17,56,2048
M 26,a[48:51],58
s_add_u32 s56,s55,768
R 78,2,11264
v_fma_f32 v106,v106,v99,v98
M 26,a[56:59],62
D 17,56,3072
M 30,a[52:55],58
s_add_u32 m0,s57,16384
R 82,2,12288
v_fma_f32 v107,v107,v99,v98
M 30,a[60:63],62
L 3
v_fma_f32 v108,v108,v99,v98
v_fma_f32 v109,v109,v99,v98
M 30,a[68:71],66
s_add_u32 s56,s55,20480
R 86,2,13312
v_fma_f32 v110,v110,v99,v98
M 30,a[76:79],70
buffer_load_dwordx4 v[126:129],v0,s[36:39],s74 offen offset:0 sc1
M 26,a[64:67],66
s_cmp_eq_u32 s79,127
R 90,2,14336
v_fma_f32 v111,v111,v99,v98
M 26,a[72:75],70
L 3
buffer_load_dwordx2 v[102:103],v11,s[36:39],s73 offen offset:256 sc1
M 26,a[80:83],74
s_waitcnt vmcnt(10)
R 94,2,15360
v_fma_f32 v112,v112,v99,v98
M 26,a[88:91],78
s_barrier
v_fma_f32 v113,v113,v99,v98
v_add_f32 v18,v18,v106
M 30,a[84:87],74
s_cselect_b32 s94,0xcc00,s94
R 34,2,16384
v_add_f32 v19,v19,v107
M 30,a[92:95],78
L 3
v_add_f32 v20,v20,v108
v_add_f32 v21,v21,v109
M 30,a[100:103],82
s_mov_b32 s67,s65
R 38,2,17408
v_add_f32 v22,v22,v110
M 30,a[108:111],86
s_mov_b32 s77,s75
v_cndmask_b32 v17,v10,v9,s[40:41]
v_add_f32 v23,v23,v111
M 26,a[96:99],82
s_add_u32 s73,s73,s64
R 42,2,18432
v_add_f32 v24,v24,v112
M 26,a[104:107],86
L 3
D 17,56,0
M 26,a[112:115],90
s_add_u32 s56,s55,20736
R 46,2,19456
v_add_f32 v25,v25,v113
M 26,a[120:123],94
s_add_u32 s74,s74,s62
v_cvt_pk_bf16_f32 v114,v18,v19
v_cvt_pk_bf16_f32 v115,v20,v21
M 30,a[116:119],90
s_add_u32 s65,s65,s62
R 50,2,20480
v_fma_f32 v107,v18,v18,s87
M 30,a[124:127],94
L 3
v_add_f32 v106,v18,v19
v_fma_f32 v107,v19,v19,v107
M 30,a[132:135],34
s_add_u32 s75,s75,s63
R 54,2,21504
v_add_f32 v106,v106,v20
M 30,a[140:143],38
D 17,56,1024
M 26,a[128:131],34
s_add_u32 s56,s55,20992
R 58,2,22528
v_fma_f32 v107,v20,v20,v107
M 26,a[136:139],38
L 3
v_add_f32 v106,v106,v21
v_fma_f32 v107,v21,v21,v107
M 26,a[144:147],42
s_sub_u32 s57,s84,s57
R 62,2,23552
v_add_f32 v106,v106,v22
M 26,a[152:155],46
v_fma_f32 v107,v22,v22,v107
v_add_f32 v106,v106,v23
M 30,a[148:151],42
R 66,2,24576
v_fma_f32 v107,v23,v23,v107
M 30,a[156:159],46
L 3
v_add_f32 v106,v106,v24
v_fma_f32 v107,v24,v24,v107
M 30,a[164:167],50
R 70,2,25600
v_add_f32 v106,v106,v25
M 30,a[172:175],54
v_fma_f32 v107,v25,v25,v107
v_cvt_pk_bf16_f32 v116,v22,v23
M 26,a[160:163],50
R 74,2,26624
v_cvt_pk_bf16_f32 v117,v24,v25
M 26,a[168:171],54
L 3
v_permlane16_swap_b32 v106,v107
v_add_f32 v106,v106,v107
v_mov_b32 v107,v106
M 26,a[176:179],58
R 78,2,27648
M 26,a[184:187],62
v_permlane32_swap_b32 v106,v107
v_add_f32 v106,v106,v107
M 30,a[180:183],58
R 82,2,28672
ds_write_b32 v12,v106 offset:0
M 30,a[188:191],62
L 4
D 17,56,2048
M 30,a[196:199],66
s_add_u32 s56,s55,21248
s_add_u32 s55,s55,s60
R 86,2,29696
M 30,a[204:207],70
D 17,56,3072
M 26,a[192:195],66
R 90,2,30720
M 26,a[200:203],70
L 4
L 2
buffer_store_dwordx4 v[114:117],v0,s[36:39],s66 offen offset:0 sc0 sc1
M 26,a[208:211],74
s_waitcnt vmcnt(7)
R 94,2,31744
M 26,a[216:219],78
s_barrier
ds_read_b32 v7,v6 offset:1024
R 118,13,0
M 30,a[212:215],74
R 34,1,0
M 30,a[220:223],78
L 5
buffer_store_dword v4,v3,s[36:39],s70 offen offset:0 sc0 sc1
M 30,a[228:231],82
s_mov_b32 s70,s69
s_mov_b32 s69,s68
R 38,1,1024
M 30,a[236:239],86
s_add_u32 s68,s68,s63
M 26,a[224:227],82
R 42,1,2048
M 26,a[232:235],86
L 5
L 4
v_cmp_eq_u32 vcc,s53,v7
M 26,a[240:243],90
L 3
R 46,1,3072
v_add_f32 v118,v118,v119
M 26,a[248:251],94
s_or_b64 vcc,vcc,s[94:95]
v_add_f32 v120,v120,v121
v_add_f32 v118,v118,v120
M 30,a[244:247],90
s_cmp_eq_u32 vcc_lo,-1
R 50,1,4096
v_mul_f32 v118,s86,v118
M 30,a[252:255],94
s_waitcnt vmcnt(6) lgkmcnt(3)
buffer_store_dword v118,v14,s[36:39],s76 offen offset:0 sc0 sc1
s_cbranch_scc1 Lg2_F0_%=
Lg2_K0_%=:
Lg2_L0_%=:
v_mfma_f32_16x16x32_bf16 v[22:25],a[4:7],v[34:37],0
s_add_u32 m0,s59,3072
R 54,1,5120
v_lshlrev_b32 v106,16,v126
M 22,a[12:15],38
buffer_load_dword v8,s[36:39],s58 offen offset:0 sc1 lds
v_mfma_f32_16x16x32_bf16 v[18:21],a[0:3],v[34:37],0
s_mov_b32 s66,s65
R 58,1,6144
v_and_b32 v107,0xffff0000,v126
M 18,a[8:11],38
L 3
v_lshlrev_b32 v108,16,v127
v_and_b32 v109,0xffff0000,v127
M 18,a[16:19],42
s_mov_b32 s76,s75
R 62,1,7168
v_lshlrev_b32 v110,16,v128
M 18,a[24:27],46
s_add_u32 s65,s65,s62
v_and_b32 v111,0xffff0000,v128
v_lshlrev_b32 v112,16,v129
M 22,a[20:23],42
s_add_u32 s75,s75,s63
R 66,1,8192
v_and_b32 v113,0xffff0000,v129
v_fma_f32 v106,v106,v103,v102
M 22,a[28:31],46
L 3
v_fma_f32 v107,v107,v103,v102
v_fma_f32 v108,v108,v103,v102
M 22,a[36:39],50
R 70,1,9216
v_fma_f32 v109,v109,v103,v102
M 22,a[44:47],54
v_fma_f32 v110,v110,v103,v102
v_fma_f32 v111,v111,v103,v102
M 18,a[32:35],50
R 74,1,10240
v_fma_f32 v112,v112,v103,v102
M 18,a[40:43],54
L 3
v_fma_f32 v113,v113,v103,v102
v_add_f32 v26,v26,v106
M 18,a[48:51],58
R 78,1,11264
v_add_f32 v27,v27,v107
M 18,a[56:59],62
v_add_f32 v28,v28,v108
v_add_f32 v29,v29,v109
v_add_f32 v30,v30,v110
M 22,a[52:55],58
R 82,1,12288
v_add_f32 v31,v31,v111
M 22,a[60:63],62
L 3
v_add_f32 v32,v32,v112
v_add_f32 v33,v33,v113
M 22,a[68:71],66
R 86,1,13312
v_cvt_pk_bf16_f32 v114,v26,v27
M 22,a[76:79],70
buffer_load_dwordx4 v[122:125],v0,s[36:39],s74 offen offset:0 sc1
M 18,a[64:67],66
s_add_u32 s74,s74,s62
R 90,1,14336
v_cvt_pk_bf16_f32 v115,v28,v29
M 18,a[72:75],70
L 3
buffer_load_dwordx2 v[98:99],v11,s[36:39],s73 offen offset:256 sc1
v_cvt_pk_bf16_f32 v116,v30,v31
M 18,a[80:83],74
s_waitcnt vmcnt(2)
s_add_u32 s73,s73,s64
R 94,1,15360
M 18,a[88:91],78
s_barrier
ds_read_b32 v7,v6 offset:3072
v_cvt_pk_bf16_f32 v117,v32,v33
M 22,a[84:87],74
R 34,1,16384
v_add_f32 v106,v26,v27
M 22,a[92:95],78
L 4
buffer_store_dword v4,v3,s[36:39],s70 offen offset:0 sc0 sc1
M 22,a[100:103],82
s_mov_b32 s70,0x7fff0000
R 38,1,17408
v_fma_f32 v107,v26,v26,s87
M 22,a[108:111],86
buffer_store_dwordx4 v[114:117],v0,s[36:39],s67 offen offset:0 sc0 sc1
v_fma_f32 v107,v27,v27,v107
M 18,a[96:99],82
R 42,1,18432
v_add_f32 v106,v106,v28
v_fma_f32 v107,v28,v28,v107
M 18,a[104:107],86
L 4
L 3
v_cmp_eq_u32 vcc,s53,v7
M 18,a[112:115],90
R 46,1,19456
v_add_f32 v106,v106,v29
M 18,a[120:123],94
s_or_b64 vcc,vcc,s[94:95]
v_fma_f32 v107,v29,v29,v107
v_add_f32 v106,v106,v30
M 22,a[116:119],90
R 50,1,20480
v_fma_f32 v107,v30,v30,v107
M 22,a[124:127],94
s_cmp_eq_u32 vcc_lo,-1
L 3
v_add_f32 v106,v106,v31
s_cbranch_scc0 Lg2_X0_%=
M 22,a[132:135],34
s_and_b32 s78,s54,127
s_cmp_eq_u32 s78,s85
s_cselect_b64 s[40:41],-1,0
R 54,1,21504
M 22,a[140:143],38
s_add_u32 m0,s57,0
s_add_u32 s56,s55,0
s_add_u32 s58,s58,s61
v_cndmask_b32 v17,v15,v16,s[40:41]
M 18,a[128:131],34
s_add_u32 s54,s54,s46
R 58,1,22528
v_fma_f32 v107,v31,v31,v107
M 18,a[136:139],38
L 3
D 17,56,0
M 18,a[144:147],42
s_add_u32 s56,s55,256
R 62,1,23552
v_add_f32 v106,v106,v32
M 18,a[152:155],46
s_and_b32 s79,s54,127
v_fma_f32 v107,v32,v32,v107
v_add_f32 v106,v106,v33
M 22,a[148:151],42
s_cmp_eq_u32 s79,0
R 66,1,24576
v_fma_f32 v107,v33,v33,v107
M 22,a[156:159],46
L 3
D 17,56,1024
M 22,a[164:167],50
s_cselect_b32 s94,0x3300,0
s_add_u32 s56,s55,512
R 70,1,25600
M 22,a[172:175],54
s_cmp_eq_u32 s79,127
v_permlane16_swap_b32 v106,v107
v_add_f32 v106,v106,v107
M 18,a[160:163],50
s_cselect_b32 s94,0xcc00,s94
)"
R"(R 74,1,26624
v_mov_b32 v107,v106
M 18,a[168:171],54
L 3
D 17,56,2048
M 18,a[176:179],58
s_add_u32 s56,s55,768
R 78,1,27648
M 18,a[184:187],62
v_permlane32_swap_b32 v106,v107
v_add_f32 v106,v106,v107
M 22,a[180:183],58
R 82,1,28672
ds_write_b32 v12,v106 offset:2048
M 22,a[188:191],62
L 4
D 17,56,3072
M 22,a[196:199],66
s_add_u32 m0,s59,0
s_add_u32 s56,s55,20480
R 86,1,29696
M 22,a[204:207],70
buffer_load_dword v8,s[36:39],s58 offen offset:0 sc1 lds
M 18,a[192:195],66
s_add_u32 m0,s57,16384
R 90,1,30720
v_cndmask_b32 v17,v10,v9,s[40:41]
M 18,a[200:203],70
L 4
L 2
s_sub_u32 s57,s84,s57
M 18,a[208:211],74
s_waitcnt vmcnt(0)
R 94,1,31744
M 18,a[216:219],78
s_barrier
D 17,56,0
M 22,a[212:215],74
s_add_u32 s56,s55,20736
R 34,2,0
ds_read_b32 v7,v6 offset:0
M 22,a[220:223],78
L 4
buffer_store_dword v4,v3,s[36:39],s70 offen offset:0 sc0 sc1
M 22,a[228:231],82
s_mov_b32 s70,s69
R 38,2,1024
R 118,13,2048
M 22,a[236:239],86
D 17,56,1024
M 18,a[224:227],82
s_add_u32 s56,s55,20992
s_mov_b32 s69,s68
R 42,2,2048
M 18,a[232:235],86
L 5
D 17,56,2048
M 18,a[240:243],90
L 3
s_add_u32 s56,s55,21248
R 46,2,3072
v_cmp_eq_u32 vcc,s53,v7
M 18,a[248:251],94
D 17,56,3072
M 22,a[244:247],90
s_add_u32 s55,s55,s60
s_add_u32 s68,s68,s63
L 2
s_or_b64 vcc,vcc,s[94:95]
s_cmp_eq_u32 vcc_lo,-1
R 50,2,4096
v_add_f32 v118,v118,v119
v_add_f32 v120,v120,v121
v_add_f32 v118,v118,v120
v_mul_f32 v118,s86,v118
M 22,a[252:255],94
s_nop 0
buffer_store_dword v118,v14,s[36:39],s77 offen offset:0 sc0 sc1
buffer_store_dword v4,v3,s[36:39],s71 offen offset:0 sc0 sc1
buffer_store_dword v4,v3,s[36:39],s71 offen offset:0 sc0 sc1
s_cbranch_scc1 Lg2_F1_%=
s_branch Lg2_K1_%=
Lg2_X0_%=:
M 22,a[132:135],34
s_add_u32 m0,s59,0
R 54,1,21504
v_fma_f32 v107,v31,v31,v107
M 22,a[140:143],38
buffer_load_dword v8,s[36:39],s58 offen offset:0 sc1 lds
M 18,a[128:131],34
R 58,1,22528
v_add_f32 v106,v106,v32
M 18,a[136:139],38
L 3
v_fma_f32 v107,v32,v32,v107
v_add_f32 v106,v106,v33
M 18,a[144:147],42
R 62,1,23552
v_fma_f32 v107,v33,v33,v107
M 18,a[152:155],46
M 22,a[148:151],42
R 66,1,24576
M 22,a[156:159],46
L 3
v_permlane16_swap_b32 v106,v107
v_add_f32 v106,v106,v107
M 22,a[164:167],50
R 70,1,25600
v_mov_b32 v107,v106
M 22,a[172:175],54
M 18,a[160:163],50
R 74,1,26624
M 18,a[168:171],54
L 3
v_permlane32_swap_b32 v106,v107
M 18,a[176:179],58
R 78,1,27648
v_add_f32 v106,v106,v107
M 18,a[184:187],62
ds_write_b32 v12,v106 offset:2048
M 22,a[180:183],58
R 82,1,28672
M 22,a[188:191],62
L 4
M 22,a[196:199],66
R 86,1,29696
M 22,a[204:207],70
M 18,a[192:195],66
R 90,1,30720
M 18,a[200:203],70
L 4
L 3
M 18,a[208:211],74
s_waitcnt vmcnt(0)
R 94,1,31744
M 18,a[216:219],78
s_barrier
R 118,13,2048
ds_read_b32 v7,v6 offset:0
M 22,a[212:215],74
buffer_store_dword v4,v3,s[36:39],s70 offen offset:0 sc0 sc1
M 22,a[220:223],78
L 4
s_mov_b32 s70,s69
s_mov_b32 s69,s68
M 22,a[228:231],82
s_add_u32 s68,s68,s63
M 22,a[236:239],86
M 18,a[224:227],82
M 18,a[232:235],86
L 2
L 1
L 0
M 18,a[240:243],90
v_add_f32 v118,v118,v119
v_add_f32 v120,v120,v121
v_add_f32 v118,v118,v120
M 18,a[248:251],94
v_cmp_eq_u32 vcc,s53,v7
v_mul_f32 v118,s86,v118
M 22,a[244:247],90
buffer_store_dword v118,v14,s[36:39],s77 offen offset:0 sc0 sc1
M 22,a[252:255],94
s_nop 0
s_or_b64 vcc,vcc,s[94:95]
s_mov_b32 s80,1
s_cmp_eq_u32 vcc_lo,-1
s_cbranch_scc0 Lg2_xn0_%=
s_and_b32 s78,s54,127
s_cmp_eq_u32 s78,s85
s_cselect_b64 s[40:41],-1,0
v_cndmask_b32 v17,v15,v16,s[40:41]
s_add_u32 m0,s57,0
s_add_u32 s56,s55,0
D 17,56,0
v_lshlrev_b32 v106,16,v122
v_and_b32 v107,0xffff0000,v122
v_lshlrev_b32 v108,16,v123
v_and_b32 v109,0xffff0000,v123
v_lshlrev_b32 v110,16,v124
s_add_u32 s56,s55,256
D 17,56,1024
v_and_b32 v111,0xffff0000,v124
v_lshlrev_b32 v112,16,v125
v_and_b32 v113,0xffff0000,v125
v_fma_f32 v106,v106,v99,v98
v_fma_f32 v107,v107,v99,v98
s_add_u32 s56,s55,512
D 17,56,2048
v_fma_f32 v108,v108,v99,v98
v_fma_f32 v109,v109,v99,v98
v_fma_f32 v110,v110,v99,v98
v_fma_f32 v111,v111,v99,v98
v_fma_f32 v112,v112,v99,v98
s_add_u32 s56,s55,768
D 17,56,3072
v_fma_f32 v113,v113,v99,v98
v_add_f32 v18,v18,v106
v_add_f32 v19,v19,v107
v_add_f32 v20,v20,v108
v_add_f32 v21,v21,v109
s_add_u32 s58,s58,s61
v_cndmask_b32 v17,v10,v9,s[40:41]
s_add_u32 m0,s57,16384
s_add_u32 s56,s55,20480
D 17,56,0
v_add_f32 v22,v22,v110
v_add_f32 v23,v23,v111
v_add_f32 v24,v24,v112
v_add_f32 v25,v25,v113
v_cvt_pk_bf16_f32 v114,v18,v19
s_add_u32 s56,s55,20736
D 17,56,1024
v_cvt_pk_bf16_f32 v115,v20,v21
v_cvt_pk_bf16_f32 v116,v22,v23
v_cvt_pk_bf16_f32 v117,v24,v25
s_add_u32 s56,s55,20992
D 17,56,2048
s_add_u32 s56,s55,21248
D 17,56,3072
buffer_store_dwordx4 v[114:117],v0,s[36:39],s66 offen offset:0 sc0 sc1
v_add_f32 v106,v18,v19
v_fma_f32 v107,v18,v18,s87
s_add_u32 s55,s55,s60
s_sub_u32 s57,s84,s57
s_add_u32 s54,s54,s46
v_fma_f32 v107,v19,v19,v107
v_add_f32 v106,v106,v20
v_fma_f32 v107,v20,v20,v107
v_add_f32 v106,v106,v21
v_fma_f32 v107,v21,v21,v107
v_add_f32 v106,v106,v22
v_fma_f32 v107,v22,v22,v107
v_add_f32 v106,v106,v23
v_fma_f32 v107,v23,v23,v107
v_add_f32 v106,v106,v24
v_fma_f32 v107,v24,v24,v107
v_add_f32 v106,v106,v25
v_fma_f32 v107,v25,v25,v107
s_nop 1
v_permlane16_swap_b32 v106,v107
v_add_f32 v106,v106,v107
v_mov_b32 v107,v106
s_nop 1
v_permlane32_swap_b32 v106,v107
v_add_f32 v106,v106,v107
ds_write_b32 v12,v106 offset:0
L 0
s_barrier
R 118,13,0
L 0
v_add_f32 v118,v118,v119
v_add_f32 v120,v120,v121
v_add_f32 v118,v118,v120
v_mul_f32 v118,s86,v118
buffer_store_dword v118,v14,s[36:39],s76 offen offset:0 sc0 sc1
s_mov_b32 s72,s69
s_mov_b32 s69,0x7fff0000
s_mov_b32 s66,0x7fff0000
s_mov_b32 s76,0x7fff0000
s_branch Lg2_gox_%=
Lg2_xn0_%=:
s_cmp_ge_u32 s54,256
s_cbranch_scc1 Lg2_fin0_%=
s_add_u32 m0,s59,4096
s_nop 0
buffer_load_dword v8,s[36:39],s58 offen offset:0 sc1 lds
buffer_store_dword v4,v3,s[36:39],s71 offen offset:0 sc0 sc1
s_add_u32 m0,s59,5120
s_nop 0
buffer_load_dword v8,s[36:39],s58 offen offset:0 sc1 lds
buffer_store_dword v4,v3,s[36:39],s71 offen offset:0 sc0 sc1
s_mov_b32 s83,0
v_lshlrev_b32 v106,16,v122
v_and_b32 v107,0xffff0000,v122
v_lshlrev_b32 v108,16,v123
v_and_b32 v109,0xffff0000,v123
v_lshlrev_b32 v110,16,v124
v_and_b32 v111,0xffff0000,v124
v_lshlrev_b32 v112,16,v125
v_and_b32 v113,0xffff0000,v125
v_fma_f32 v106,v106,v99,v98
v_fma_f32 v107,v107,v99,v98
v_fma_f32 v108,v108,v99,v98
v_fma_f32 v109,v109,v99,v98
v_fma_f32 v110,v110,v99,v98
v_fma_f32 v111,v111,v99,v98
v_fma_f32 v112,v112,v99,v98
v_fma_f32 v113,v113,v99,v98
v_add_f32 v18,v18,v106
v_add_f32 v19,v19,v107
v_add_f32 v20,v20,v108
v_add_f32 v21,v21,v109
v_add_f32 v22,v22,v110
v_add_f32 v23,v23,v111
v_add_f32 v24,v24,v112
v_add_f32 v25,v25,v113
v_cvt_pk_bf16_f32 v114,v18,v19
v_cvt_pk_bf16_f32 v115,v20,v21
v_cvt_pk_bf16_f32 v116,v22,v23
v_cvt_pk_bf16_f32 v117,v24,v25
buffer_store_dwordx4 v[114:117],v0,s[36:39],s66 offen offset:0 sc0 sc1
v_add_f32 v106,v18,v19
v_fma_f32 v107,v18,v18,s87
v_fma_f32 v107,v19,v19,v107
v_add_f32 v106,v106,v20
v_fma_f32 v107,v20,v20,v107
v_add_f32 v106,v106,v21
v_fma_f32 v107,v21,v21,v107
v_add_f32 v106,v106,v22
v_fma_f32 v107,v22,v22,v107
v_add_f32 v106,v106,v23
v_fma_f32 v107,v23,v23,v107
v_add_f32 v106,v106,v24
v_fma_f32 v107,v24,v24,v107
v_add_f32 v106,v106,v25
v_fma_f32 v107,v25,v25,v107
s_nop 1
v_permlane16_swap_b32 v106,v107
v_add_f32 v106,v106,v107
v_mov_b32 v107,v106
s_nop 1
v_permlane32_swap_b32 v106,v107
v_add_f32 v106,v106,v107
ds_write_b32 v12,v106 offset:0
L 0
s_barrier
R 118,13,0
L 0
v_add_f32 v118,v118,v119
v_add_f32 v120,v120,v121
v_add_f32 v118,v118,v120
v_mul_f32 v118,s86,v118
buffer_store_dword v118,v14,s[36:39],s76 offen offset:0 sc0 sc1
s_mov_b32 s72,s69
s_mov_b32 s69,0x7fff0000
s_mov_b32 s66,0x7fff0000
s_mov_b32 s76,0x7fff0000
s_branch Lg2_poll_%=
Lg2_fin0_%=:
v_lshlrev_b32 v106,16,v122
v_and_b32 v107,0xffff0000,v122
v_lshlrev_b32 v108,16,v123
v_and_b32 v109,0xffff0000,v123
v_lshlrev_b32 v110,16,v124
v_and_b32 v111,0xffff0000,v124
v_lshlrev_b32 v112,16,v125
v_and_b32 v113,0xffff0000,v125
v_fma_f32 v106,v106,v99,v98
v_fma_f32 v107,v107,v99,v98
v_fma_f32 v108,v108,v99,v98
v_fma_f32 v109,v109,v99,v98
v_fma_f32 v110,v110,v99,v98
v_fma_f32 v111,v111,v99,v98
v_fma_f32 v112,v112,v99,v98
v_fma_f32 v113,v113,v99,v98
v_add_f32 v18,v18,v106
v_add_f32 v19,v19,v107
v_add_f32 v20,v20,v108
v_add_f32 v21,v21,v109
v_add_f32 v22,v22,v110
v_add_f32 v23,v23,v111
v_add_f32 v24,v24,v112
v_add_f32 v25,v25,v113
v_cvt_pk_bf16_f32 v114,v18,v19
v_cvt_pk_bf16_f32 v115,v20,v21
v_cvt_pk_bf16_f32 v116,v22,v23
v_cvt_pk_bf16_f32 v117,v24,v25
buffer_store_dwordx4 v[114:117],v0,s[36:39],s66 offen offset:0 sc0 sc1
v_add_f32 v106,v18,v19
v_fma_f32 v107,v18,v18,s87
v_fma_f32 v107,v19,v19,v107
v_add_f32 v106,v106,v20
v_fma_f32 v107,v20,v20,v107
v_add_f32 v106,v106,v21
v_fma_f32 v107,v21,v21,v107
v_add_f32 v106,v106,v22
v_fma_f32 v107,v22,v22,v107
v_add_f32 v106,v106,v23
v_fma_f32 v107,v23,v23,v107
v_add_f32 v106,v106,v24
v_fma_f32 v107,v24,v24,v107
v_add_f32 v106,v106,v25
v_fma_f32 v107,v25,v25,v107
s_nop 1
v_permlane16_swap_b32 v106,v107
v_add_f32 v106,v106,v107
v_mov_b32 v107,v106
s_nop 1
v_permlane32_swap_b32 v106,v107
v_add_f32 v106,v106,v107
ds_write_b32 v12,v106 offset:0
L 0
s_barrier
R 118,13,0
L 0
v_add_f32 v118,v118,v119
v_add_f32 v120,v120,v121
v_add_f32 v118,v118,v120
v_mul_f32 v118,s86,v118
buffer_store_dword v118,v14,s[36:39],s76 offen offset:0 sc0 sc1
s_mov_b32 s72,s69
s_mov_b32 s69,0x7fff0000
s_mov_b32 s66,0x7fff0000
s_mov_b32 s76,0x7fff0000
s_branch Lg2_done_%=
Lg2_K1_%=:
Lg2_L1_%=:
v_mfma_f32_16x16x32_bf16 v[30:33],a[4:7],v[34:37],0
s_add_u32 m0,s59,3072
R 54,2,5120
v_lshlrev_b32 v106,16,v122
M 30,a[12:15],38
buffer_load_dword v8,s[36:39],s58 offen offset:0 sc1 lds
v_mfma_f32_16x16x32_bf16 v[26:29],a[0:3],v[34:37],0
s_mov_b32 s67,s65
R 58,2,6144
v_and_b32 v107,0xffff0000,v122
M 26,a[8:11],38
L 3
v_lshlrev_b32 v108,16,v123
v_and_b32 v109,0xffff0000,v123
M 26,a[16:19],42
s_mov_b32 s77,s75
R 62,2,7168
v_lshlrev_b32 v110,16,v124
M 26,a[24:27],46
s_add_u32 s65,s65,s62
v_and_b32 v111,0xffff0000,v124
v_lshlrev_b32 v112,16,v125
M 30,a[20:23],42
s_add_u32 s75,s75,s63
R 66,2,8192
v_and_b32 v113,0xffff0000,v125
v_fma_f32 v106,v106,v99,v98
M 30,a[28:31],46
L 3
v_fma_f32 v107,v107,v99,v98
v_fma_f32 v108,v108,v99,v98
M 30,a[36:39],50
R 70,2,9216
v_fma_f32 v109,v109,v99,v98
M 30,a[44:47],54
v_fma_f32 v110,v110,v99,v98
v_fma_f32 v111,v111,v99,v98
M 26,a[32:35],50
R 74,2,10240
v_fma_f32 v112,v112,v99,v98
M 26,a[40:43],54
L 3
v_fma_f32 v113,v113,v99,v98
v_add_f32 v18,v18,v106
M 26,a[48:51],58
R 78,2,11264
v_add_f32 v19,v19,v107
M 26,a[56:59],62
v_add_f32 v20,v20,v108
v_add_f32 v21,v21,v109
v_add_f32 v22,v22,v110
M 30,a[52:55],58
R 82,2,12288
v_add_f32 v23,v23,v111
M 30,a[60:63],62
L 3
v_add_f32 v24,v24,v112
v_add_f32 v25,v25,v113
M 30,a[68:71],66
R 86,2,13312
v_cvt_pk_bf16_f32 v114,v18,v19
M 30,a[76:79],70
buffer_load_dwordx4 v[126:129],v0,s[36:39],s74 offen offset:0 sc1
M 26,a[64:67],66
s_add_u32 s74,s74,s62
R 90,2,14336
v_cvt_pk_bf16_f32 v115,v20,v21
M 26,a[72:75],70
L 3
buffer_load_dwordx2 v[102:103],v11,s[36:39],s73 offen offset:256 sc1
v_cvt_pk_bf16_f32 v116,v22,v23
M 26,a[80:83],74
s_waitcnt vmcnt(2)
s_add_u32 s73,s73,s64
R 94,2,15360
M 26,a[88:91],78
s_barrier
ds_read_b32 v7,v6 offset:3072
v_cvt_pk_bf16_f32 v117,v24,v25
M 30,a[84:87],74
R 34,2,16384
v_add_f32 v106,v18,v19
M 30,a[92:95],78
L 4
buffer_store_dword v4,v3,s[36:39],s70 offen offset:0 sc0 sc1
M 30,a[100:103],82
s_mov_b32 s70,0x7fff0000
R 38,2,17408
v_fma_f32 v107,v18,v18,s87
M 30,a[108:111],86
buffer_store_dwordx4 v[114:117],v0,s[36:39],s66 offen offset:0 sc0 sc1
v_fma_f32 v107,v19,v19,v107
)"
R"(M 26,a[96:99],82
R 42,2,18432
v_add_f32 v106,v106,v20
v_fma_f32 v107,v20,v20,v107
M 26,a[104:107],86
L 4
L 3
v_cmp_eq_u32 vcc,s53,v7
M 26,a[112:115],90
R 46,2,19456
v_add_f32 v106,v106,v21
M 26,a[120:123],94
s_or_b64 vcc,vcc,s[94:95]
v_fma_f32 v107,v21,v21,v107
v_add_f32 v106,v106,v22
M 30,a[116:119],90
R 50,2,20480
v_fma_f32 v107,v22,v22,v107
M 30,a[124:127],94
s_cmp_eq_u32 vcc_lo,-1
L 3
v_add_f32 v106,v106,v23
s_cbranch_scc0 Lg2_X1_%=
M 30,a[132:135],34
s_and_b32 s78,s54,127
s_cmp_eq_u32 s78,s85
s_cselect_b64 s[40:41],-1,0
R 54,2,21504
M 30,a[140:143],38
s_add_u32 m0,s57,0
s_add_u32 s56,s55,0
s_add_u32 s58,s58,s61
v_cndmask_b32 v17,v15,v16,s[40:41]
M 26,a[128:131],34
s_add_u32 s54,s54,s46
R 58,2,22528
v_fma_f32 v107,v23,v23,v107
M 26,a[136:139],38
L 3
D 17,56,0
M 26,a[144:147],42
s_add_u32 s56,s55,256
R 62,2,23552
v_add_f32 v106,v106,v24
M 26,a[152:155],46
s_and_b32 s79,s54,127
v_fma_f32 v107,v24,v24,v107
v_add_f32 v106,v106,v25
M 30,a[148:151],42
s_cmp_eq_u32 s79,0
R 66,2,24576
v_fma_f32 v107,v25,v25,v107
M 30,a[156:159],46
L 3
D 17,56,1024
M 30,a[164:167],50
s_cselect_b32 s94,0x3300,0
s_add_u32 s56,s55,512
R 70,2,25600
M 30,a[172:175],54
s_cmp_eq_u32 s79,127
v_permlane16_swap_b32 v106,v107
v_add_f32 v106,v106,v107
M 26,a[160:163],50
s_cselect_b32 s94,0xcc00,s94
R 74,2,26624
v_mov_b32 v107,v106
M 26,a[168:171],54
L 3
D 17,56,2048
M 26,a[176:179],58
s_add_u32 s56,s55,768
R 78,2,27648
M 26,a[184:187],62
v_permlane32_swap_b32 v106,v107
v_add_f32 v106,v106,v107
M 30,a[180:183],58
R 82,2,28672
ds_write_b32 v12,v106 offset:0
M 30,a[188:191],62
L 4
D 17,56,3072
M 30,a[196:199],66
s_add_u32 m0,s59,1024
s_add_u32 s56,s55,20480
R 86,2,29696
M 30,a[204:207],70
buffer_load_dword v8,s[36:39],s58 offen offset:0 sc1 lds
M 26,a[192:195],66
s_add_u32 m0,s57,16384
R 90,2,30720
v_cndmask_b32 v17,v10,v9,s[40:41]
M 26,a[200:203],70
L 4
L 2
s_sub_u32 s57,s84,s57
M 26,a[208:211],74
s_waitcnt vmcnt(0)
R 94,2,31744
M 26,a[216:219],78
s_barrier
D 17,56,0
M 30,a[212:215],74
s_add_u32 s56,s55,20736
R 34,1,0
ds_read_b32 v7,v6 offset:1024
M 30,a[220:223],78
L 4
buffer_store_dword v4,v3,s[36:39],s70 offen offset:0 sc0 sc1
M 30,a[228:231],82
s_mov_b32 s70,s69
R 38,1,1024
R 118,13,0
M 30,a[236:239],86
D 17,56,1024
M 26,a[224:227],82
s_add_u32 s56,s55,20992
s_mov_b32 s69,s68
R 42,1,2048
M 26,a[232:235],86
L 5
D 17,56,2048
M 26,a[240:243],90
L 3
s_add_u32 s56,s55,21248
R 46,1,3072
v_cmp_eq_u32 vcc,s53,v7
M 26,a[248:251],94
D 17,56,3072
M 30,a[244:247],90
s_add_u32 s55,s55,s60
s_add_u32 s68,s68,s63
L 2
s_or_b64 vcc,vcc,s[94:95]
s_cmp_eq_u32 vcc_lo,-1
R 50,1,4096
v_add_f32 v118,v118,v119
v_add_f32 v120,v120,v121
v_add_f32 v118,v118,v120
v_mul_f32 v118,s86,v118
M 30,a[252:255],94
s_nop 0
buffer_store_dword v118,v14,s[36:39],s76 offen offset:0 sc0 sc1
buffer_store_dword v4,v3,s[36:39],s71 offen offset:0 sc0 sc1
buffer_store_dword v4,v3,s[36:39],s71 offen offset:0 sc0 sc1
s_cbranch_scc1 Lg2_F0_%=
s_branch Lg2_K0_%=
Lg2_X1_%=:
M 30,a[132:135],34
s_add_u32 m0,s59,1024
R 54,2,21504
v_fma_f32 v107,v23,v23,v107
M 30,a[140:143],38
buffer_load_dword v8,s[36:39],s58 offen offset:0 sc1 lds
M 26,a[128:131],34
R 58,2,22528
v_add_f32 v106,v106,v24
M 26,a[136:139],38
L 3
v_fma_f32 v107,v24,v24,v107
v_add_f32 v106,v106,v25
M 26,a[144:147],42
R 62,2,23552
v_fma_f32 v107,v25,v25,v107
M 26,a[152:155],46
M 30,a[148:151],42
R 66,2,24576
M 30,a[156:159],46
L 3
v_permlane16_swap_b32 v106,v107
v_add_f32 v106,v106,v107
M 30,a[164:167],50
R 70,2,25600
v_mov_b32 v107,v106
M 30,a[172:175],54
M 26,a[160:163],50
R 74,2,26624
M 26,a[168:171],54
L 3
v_permlane32_swap_b32 v106,v107
M 26,a[176:179],58
R 78,2,27648
v_add_f32 v106,v106,v107
M 26,a[184:187],62
ds_write_b32 v12,v106 offset:0
M 30,a[180:183],58
R 82,2,28672
M 30,a[188:191],62
L 4
M 30,a[196:199],66
R 86,2,29696
M 30,a[204:207],70
M 26,a[192:195],66
R 90,2,30720
M 26,a[200:203],70
L 4
L 3
M 26,a[208:211],74
s_waitcnt vmcnt(0)
R 94,2,31744
M 26,a[216:219],78
s_barrier
R 118,13,0
ds_read_b32 v7,v6 offset:1024
M 30,a[212:215],74
buffer_store_dword v4,v3,s[36:39],s70 offen offset:0 sc0 sc1
M 30,a[220:223],78
L 4
s_mov_b32 s70,s69
s_mov_b32 s69,s68
M 30,a[228:231],82
s_add_u32 s68,s68,s63
M 30,a[236:239],86
M 26,a[224:227],82
M 26,a[232:235],86
L 2
L 1
L 0
M 26,a[240:243],90
v_add_f32 v118,v118,v119
v_add_f32 v120,v120,v121
v_add_f32 v118,v118,v120
M 26,a[248:251],94
v_cmp_eq_u32 vcc,s53,v7
v_mul_f32 v118,s86,v118
M 30,a[244:247],90
buffer_store_dword v118,v14,s[36:39],s76 offen offset:0 sc0 sc1
M 30,a[252:255],94
s_nop 0
s_or_b64 vcc,vcc,s[94:95]
s_mov_b32 s80,0
s_cmp_eq_u32 vcc_lo,-1
s_cbranch_scc0 Lg2_xn1_%=
s_and_b32 s78,s54,127
s_cmp_eq_u32 s78,s85
s_cselect_b64 s[40:41],-1,0
v_cndmask_b32 v17,v15,v16,s[40:41]
s_add_u32 m0,s57,0
s_add_u32 s56,s55,0
D 17,56,0
v_lshlrev_b32 v106,16,v126
v_and_b32 v107,0xffff0000,v126
v_lshlrev_b32 v108,16,v127
v_and_b32 v109,0xffff0000,v127
v_lshlrev_b32 v110,16,v128
s_add_u32 s56,s55,256
D 17,56,1024
v_and_b32 v111,0xffff0000,v128
v_lshlrev_b32 v112,16,v129
v_and_b32 v113,0xffff0000,v129
v_fma_f32 v106,v106,v103,v102
v_fma_f32 v107,v107,v103,v102
s_add_u32 s56,s55,512
D 17,56,2048
v_fma_f32 v108,v108,v103,v102
v_fma_f32 v109,v109,v103,v102
v_fma_f32 v110,v110,v103,v102
v_fma_f32 v111,v111,v103,v102
v_fma_f32 v112,v112,v103,v102
s_add_u32 s56,s55,768
D 17,56,3072
v_fma_f32 v113,v113,v103,v102
v_add_f32 v26,v26,v106
v_add_f32 v27,v27,v107
v_add_f32 v28,v28,v108
v_add_f32 v29,v29,v109
s_add_u32 s58,s58,s61
v_cndmask_b32 v17,v10,v9,s[40:41]
s_add_u32 m0,s57,16384
s_add_u32 s56,s55,20480
D 17,56,0
v_add_f32 v30,v30,v110
v_add_f32 v31,v31,v111
v_add_f32 v32,v32,v112
v_add_f32 v33,v33,v113
v_cvt_pk_bf16_f32 v114,v26,v27
s_add_u32 s56,s55,20736
D 17,56,1024
v_cvt_pk_bf16_f32 v115,v28,v29
v_cvt_pk_bf16_f32 v116,v30,v31
v_cvt_pk_bf16_f32 v117,v32,v33
s_add_u32 s56,s55,20992
D 17,56,2048
s_add_u32 s56,s55,21248
D 17,56,3072
buffer_store_dwordx4 v[114:117],v0,s[36:39],s67 offen offset:0 sc0 sc1
v_add_f32 v106,v26,v27
v_fma_f32 v107,v26,v26,s87
s_add_u32 s55,s55,s60
s_sub_u32 s57,s84,s57
s_add_u32 s54,s54,s46
v_fma_f32 v107,v27,v27,v107
v_add_f32 v106,v106,v28
v_fma_f32 v107,v28,v28,v107
v_add_f32 v106,v106,v29
v_fma_f32 v107,v29,v29,v107
v_add_f32 v106,v106,v30
v_fma_f32 v107,v30,v30,v107
v_add_f32 v106,v106,v31
v_fma_f32 v107,v31,v31,v107
v_add_f32 v106,v106,v32
v_fma_f32 v107,v32,v32,v107
v_add_f32 v106,v106,v33
v_fma_f32 v107,v33,v33,v107
s_nop 1
v_permlane16_swap_b32 v106,v107
v_add_f32 v106,v106,v107
v_mov_b32 v107,v106
s_nop 1
v_permlane32_swap_b32 v106,v107
v_add_f32 v106,v106,v107
ds_write_b32 v12,v106 offset:2048
L 0
s_barrier
R 118,13,2048
L 0
v_add_f32 v118,v118,v119
v_add_f32 v120,v120,v121
v_add_f32 v118,v118,v120
v_mul_f32 v118,s86,v118
buffer_store_dword v118,v14,s[36:39],s77 offen offset:0 sc0 sc1
s_mov_b32 s72,s69
s_mov_b32 s69,0x7fff0000
s_mov_b32 s67,0x7fff0000
s_mov_b32 s77,0x7fff0000
s_branch Lg2_gox_%=
Lg2_xn1_%=:
s_cmp_ge_u32 s54,256
s_cbranch_scc1 Lg2_fin1_%=
s_add_u32 m0,s59,4096
s_nop 0
buffer_load_dword v8,s[36:39],s58 offen offset:0 sc1 lds
buffer_store_dword v4,v3,s[36:39],s71 offen offset:0 sc0 sc1
s_add_u32 m0,s59,5120
s_nop 0
buffer_load_dword v8,s[36:39],s58 offen offset:0 sc1 lds
buffer_store_dword v4,v3,s[36:39],s71 offen offset:0 sc0 sc1
s_mov_b32 s83,0
v_lshlrev_b32 v106,16,v126
v_and_b32 v107,0xffff0000,v126
v_lshlrev_b32 v108,16,v127
v_and_b32 v109,0xffff0000,v127
v_lshlrev_b32 v110,16,v128
v_and_b32 v111,0xffff0000,v128
v_lshlrev_b32 v112,16,v129
v_and_b32 v113,0xffff0000,v129
v_fma_f32 v106,v106,v103,v102
v_fma_f32 v107,v107,v103,v102
v_fma_f32 v108,v108,v103,v102
v_fma_f32 v109,v109,v103,v102
v_fma_f32 v110,v110,v103,v102
v_fma_f32 v111,v111,v103,v102
v_fma_f32 v112,v112,v103,v102
v_fma_f32 v113,v113,v103,v102
v_add_f32 v26,v26,v106
v_add_f32 v27,v27,v107
v_add_f32 v28,v28,v108
v_add_f32 v29,v29,v109
v_add_f32 v30,v30,v110
v_add_f32 v31,v31,v111
v_add_f32 v32,v32,v112
v_add_f32 v33,v33,v113
v_cvt_pk_bf16_f32 v114,v26,v27
v_cvt_pk_bf16_f32 v115,v28,v29
v_cvt_pk_bf16_f32 v116,v30,v31
v_cvt_pk_bf16_f32 v117,v32,v33
buffer_store_dwordx4 v[114:117],v0,s[36:39],s67 offen offset:0 sc0 sc1
v_add_f32 v106,v26,v27
v_fma_f32 v107,v26,v26,s87
v_fma_f32 v107,v27,v27,v107
v_add_f32 v106,v106,v28
v_fma_f32 v107,v28,v28,v107
v_add_f32 v106,v106,v29
v_fma_f32 v107,v29,v29,v107
v_add_f32 v106,v106,v30
v_fma_f32 v107,v30,v30,v107
v_add_f32 v106,v106,v31
v_fma_f32 v107,v31,v31,v107
v_add_f32 v106,v106,v32
v_fma_f32 v107,v32,v32,v107
v_add_f32 v106,v106,v33
v_fma_f32 v107,v33,v33,v107
s_nop 1
v_permlane16_swap_b32 v106,v107
v_add_f32 v106,v106,v107
v_mov_b32 v107,v106
s_nop 1
v_permlane32_swap_b32 v106,v107
v_add_f32 v106,v106,v107
ds_write_b32 v12,v106 offset:2048
L 0
s_barrier
R 118,13,2048
L 0
v_add_f32 v118,v118,v119
v_add_f32 v120,v120,v121
v_add_f32 v118,v118,v120
v_mul_f32 v118,s86,v118
buffer_store_dword v118,v14,s[36:39],s77 offen offset:0 sc0 sc1
s_mov_b32 s72,s69
s_mov_b32 s69,0x7fff0000
s_mov_b32 s67,0x7fff0000
s_mov_b32 s77,0x7fff0000
s_branch Lg2_poll_%=
Lg2_fin1_%=:
v_lshlrev_b32 v106,16,v126
v_and_b32 v107,0xffff0000,v126
v_lshlrev_b32 v108,16,v127
v_and_b32 v109,0xffff0000,v127
v_lshlrev_b32 v110,16,v128
v_and_b32 v111,0xffff0000,v128
v_lshlrev_b32 v112,16,v129
v_and_b32 v113,0xffff0000,v129
v_fma_f32 v106,v106,v103,v102
v_fma_f32 v107,v107,v103,v102
v_fma_f32 v108,v108,v103,v102
v_fma_f32 v109,v109,v103,v102
v_fma_f32 v110,v110,v103,v102
v_fma_f32 v111,v111,v103,v102
v_fma_f32 v112,v112,v103,v102
v_fma_f32 v113,v113,v103,v102
v_add_f32 v26,v26,v106
v_add_f32 v27,v27,v107
v_add_f32 v28,v28,v108
v_add_f32 v29,v29,v109
v_add_f32 v30,v30,v110
v_add_f32 v31,v31,v111
v_add_f32 v32,v32,v112
v_add_f32 v33,v33,v113
v_cvt_pk_bf16_f32 v114,v26,v27
v_cvt_pk_bf16_f32 v115,v28,v29
v_cvt_pk_bf16_f32 v116,v30,v31
v_cvt_pk_bf16_f32 v117,v32,v33
buffer_store_dwordx4 v[114:117],v0,s[36:39],s67 offen offset:0 sc0 sc1
v_add_f32 v106,v26,v27
v_fma_f32 v107,v26,v26,s87
v_fma_f32 v107,v27,v27,v107
v_add_f32 v106,v106,v28
v_fma_f32 v107,v28,v28,v107
v_add_f32 v106,v106,v29
v_fma_f32 v107,v29,v29,v107
v_add_f32 v106,v106,v30
v_fma_f32 v107,v30,v30,v107
v_add_f32 v106,v106,v31
v_fma_f32 v107,v31,v31,v107
v_add_f32 v106,v106,v32
v_fma_f32 v107,v32,v32,v107
v_add_f32 v106,v106,v33
v_fma_f32 v107,v33,v33,v107
s_nop 1
v_permlane16_swap_b32 v106,v107
v_add_f32 v106,v106,v107
v_mov_b32 v107,v106
s_nop 1
v_permlane32_swap_b32 v106,v107
v_add_f32 v106,v106,v107
ds_write_b32 v12,v106 offset:2048
L 0
s_barrier
R 118,13,2048
L 0
v_add_f32 v118,v118,v119
v_add_f32 v120,v120,v121
v_add_f32 v118,v118,v120
v_mul_f32 v118,s86,v118
buffer_store_dword v118,v14,s[36:39],s77 offen offset:0 sc0 sc1
s_mov_b32 s72,s69
s_mov_b32 s69,0x7fff0000
s_mov_b32 s67,0x7fff0000
s_mov_b32 s77,0x7fff0000
s_branch Lg2_done_%=
Lg2_done_%=:
s_waitcnt vmcnt(2)
s_barrier
buffer_store_dword v4,v3,s[36:39],s70 offen offset:0 sc0 sc1
s_waitcnt vmcnt(1)
s_barrier
buffer_store_dword v4,v3,s[36:39],s72 offen offset:0 sc0 sc1
s_waitcnt vmcnt(0) lgkmcnt(0)
s_barrier
Lg2_end_%=:
s_mov_b32 %0,s53
Lcode_g12_e:
.purgem M
.purgem R
.purgem T
.purgem D
.purgem L
)"
  : "=&s"(epOut) : "s"(ws), "s"(__builtin_amdgcn_readfirstlane(sWave)), "s"(__builtin_amdgcn_readfirstlane(sG0)), "s"(__builtin_amdgcn_readfirstlane(sStep)), "s"(__builtin_amdgcn_readfirstlane(sOutOff)), "s"(__builtin_amdgcn_readfirstlane(sFoutOff)), "s"(__builtin_amdgcn_readfirstlane(sAuxOff)), "s"(__builtin_amdgcn_readfirstlane(sWw)), "s"(__builtin_amdgcn_readfirstlane(sBuf)), "s"(__builtin_amdgcn_readfirstlane(sExp)), "v"(vL16), "v"(vFlag), "v"(vSnapOff), "v"(vSnapRd), "v"(vWd0), "v"(vWd1), "v"(vWr0), "v"(vWr1), "v"(vSp), "v"(vOutS), "v"(vAux0), "v"(vAux1), "v"(vAux2), "v"(vAux3), "v"(vAn), "v"(vAe), "s"(w12) : "v0", "v1", "v2", "v3", "v4", "v5", "v6", "v7", "v8", "v9", "v10", "v11", "v12", "v13", "v14", "v15", "v16", "v17", "v18", "v19", "v20", "v21", "v22", "v23", "v24", "v25", "v26", "v27", "v28", "v29", "v30", "v31", "v32", "v33", "v34", "v35", "v36", "v37", "v38", "v39", "v40", "v41", "v42", "v43", "v44", "v45", "v46", "v47", "v48", "v49", "v50", "v51", "v52", "v53", "v54", "v55", "v56", "v57", "v58", "v59", "v60", "v61", "v62", "v63", "v64", "v65", "v66", "v67", "v68", "v69", "v70", "v71", "v72", "v73", "v74", "v75", "v76", "v77", "v78", "v79", "v80", "v81", "v82", "v83", "v84", "v85", "v86", "v87", "v88", "v89", "v90", "v91", "v92", "v93", "v94", "v95", "v96", "v97", "v98", "v99", "v100", "v101", "v102", "v103", "v104", "v105", "v106", "v107", "v108", "v109", "v110", "v111", "v112", "v113", "v114", "v115", "v116", "v117", "v118", "v119", "v120", "v121", "v122", "v123", "v124", "v125", "v126", "v127", "v128", "v129", "v130", "v131", "v132", "v133", "v134", "v135", "v136", "v137", "v138", "v139", "v140", "v141", "v142", "v143", "v144", "v145", "v146", "v147", "v148", "v149", "v150", "v151", "v152", "v153", "v154", "v155", "v156", "v157", "v158", "v159", "v160", "v161", "v162", "v163", "v164", "v165", "v166", "v167", "v168", "v169", "v170", "v171", "v172", "v173", "v174", "v175", "v176", "v177", "v178", "v179", "v180", "v181", "v182", "v183", "v184", "v185", "v186", "v187", "v188", "v189", "v190", "v191", "v192", "v193", "v194", "v195", "v196", "v197", "v198", "v199", "v200", "v201", "v202", "v203", "v204", "v205", "v206", "v207", "v208", "v209", "v210", "v211", "v212", "v213", "v214", "v215", "v216", "v217", "v218", "v219", "v220", "v221", "v222", "v223", "v224", "v225", "v226", "v227", "v228", "v229", "v230", "v231", "a0", "a1", "a2", "a3", "a4", "a5", "a6", "a7", "a8", "a9", "a10", "a11", "a12", "a13", "a14", "a15", "a16", "a17", "a18", "a19", "a20", "a21", "a22", "a23", "a24", "a25", "a26", "a27", "a28", "a29", "a30", "a31", "a32", "a33", "a34", "a35", "a36", "a37", "a38", "a39", "a40", "a41", "a42", "a43", "a44", "a45", "a46", "a47", "a48", "a49", "a50", "a51", "a52", "a53", "a54", "a55", "a56", "a57", "a58", "a59", "a60", "a61", "a62", "a63", "a64", "a65", "a66", "a67", "a68", "a69", "a70", "a71", "a72", "a73", "a74", "a75", "a76", "a77", "a78", "a79", "a80", "a81", "a82", "a83", "a84", "a85", "a86", "a87", "a88", "a89", "a90", "a91", "a92", "a93", "a94", "a95", "a96", "a97", "a98", "a99", "a100", "a101", "a102", "a103", "a104", "a105", "a106", "a107", "a108", "a109", "a110", "a111", "a112", "a113", "a114", "a115", "a116", "a117", "a118", "a119", "a120", "a121", "a122", "a123", "a124", "a125", "a126", "a127", "a128", "a129", "a130", "a131", "a132", "a133", "a134", "a135", "a136", "a137", "a138", "a139", "a140", "a141", "a142", "a143", "a144", "a145", "a146", "a147", "a148", "a149", "a150", "a151", "a152", "a153", "a154", "a155", "a156", "a157", "a158", "a159", "a160", "a161", "a162", "a163", "a164", "a165", "a166", "a167", "a168", "a169", "a170", "a171", "a172", "a173", "a174", "a175", "a176", "a177", "a178", "a179", "a180", "a181", "a182", "a183", "a184", "a185", "a186", "a187", "a188", "a189", "a190", "a191", "a192", "a193", "a194", "a195", "a196", "a197", "a198", "a199", "a200", "a201", "a202", "a203", "a204", "a205", "a206", "a207", "a208", "a209", "a210", "a211", "a212", "a213", "a214", "a215", "a216", "a217", "a218", "a219", "a220", "a221", "a222", "a223", "a224", "a225", "a226", "a227", "a228", "a229", "a230", "a231", "a232", "a233", "a234", "a235", "a236", "a237", "a238", "a239", "a240", "a241", "a242", "a243", "a244", "a245", "a246", "a247", "a248", "a249", "a250", "a251", "a252", "a253", "a254", "a255", "s36", "s37", "s38", "s39", "s40", "s41", "s42", "s43", "s44", "s45", "s46", "s47", "s48", "s49", "s50", "s51", "s52", "s53", "s54", "s55", "s56", "s57", "s58", "s59", "s60", "s61", "s62", "s63", "s64", "s65", "s66", "s67", "s68", "s69", "s70", "s71", "s72", "s73", "s74", "s75", "s76", "s77", "s78", "s79", "s80", "s81", "s82", "s83", "s84", "s85", "s86", "s87", "s88", "s89", "s90", "s91", "s92", "s93", "s94", "s95", "s96", "s97", "vcc", "scc", "memory");
    ep = epOut;
    if (kind == 1 && 5 + RHELP + (rep >> 1) < NR4) { kind = 4; kh = rep & 1; rep = 5 + RHELP + (rep >> 1); }
  }
  if (kind == 4) {
    const unsigned sStep = rep < 5 ? 5 : rep == 5 ? 6 : NR4;
    COMMON(10, (kh * 9 + fj) * 128, 2560, 143360, 2048, 448, 0);
    const int cs = slice;
    const unsigned sOutOff = (kh ? OFF_P1 : OFF_P0) + (cs * 4 + wave) * 1024, sFoutOff = OFF_FP + (kh * 8 + cs) * 128;
    const unsigned sAuxOff = (kh ? OFF_XP : OFF_X1) + (cs * 4 + wave) * 1024, sWw = (kh * 1792 + 16 * wave) * 2048 + cs * 256, sBuf = kh * 57344;
    const int zu = 108 + (rep + 2) % 5;
    const unsigned vSp = lg * 2048 + l15 * 4, vSn = rep >= 5 ? OOB : !ALLZ ? (zu * 16 + wave * 4 + lg) * 2048 + (cs * 2 + kh) * 128 + l15 * 8 :
                         ((rep + 5 * (wave >> 1)) * 16 + (wave & 1) * 8 + (ln >> 3)) * 2048 + (cs * 2 + kh) * 128 + (ln & 7) * 16;
    (void)vWr2;
    const unsigned sExp = __builtin_amdgcn_readfirstlane(ep);
    asm volatile(
R"(.macro M d,w,x
v_mfma_f32_16x16x32_bf16 v[\d:\d+3], \w, v[\x:\x+3], v[\d:\d+3]
.endm
.macro R d,a,o
ds_read_b128 v[\d:\d+3], v\a offset:\o
.endm
.macro T d,a,o
ds_read_b64_tr_b16 \d, v\a offset:\o
.endm
.macro D a,s,o
buffer_load_dwordx4 v\a, s[36:39], s\s offen offset:\o sc1 lds
.endm
.macro L c
s_waitcnt lgkmcnt(\c)
.endm
Lcode_g4_b:
s_mov_b64 s[36:37],%0
s_mov_b32 s38,111243392
s_mov_b32 s39,0x00020000
s_mov_b32 s44,%1
s_mov_b32 s45,%2
s_mov_b32 s46,%3
s_mov_b32 s47,%4
s_mov_b32 s48,%5
s_mov_b32 s49,%6
s_mov_b32 s50,%7
s_mov_b32 s51,%8
s_mov_b32 s93,%9
s_mov_b32 s53,%10
v_mov_b32 v0,%11
v_mov_b32 v3,%12
v_mov_b32 v5,%13
v_mov_b32 v6,%14
v_mov_b32 v10,%15
v_mov_b32 v11,%16
v_mov_b32 v12,%17
v_mov_b32 v13,%18
v_mov_b32 v9,%19
v_mov_b32 v7,%20
s_mov_b64 s[40:41],%21
s_mov_b32 s42,7340032
s_mov_b32 s43,0x00020000
s_lshl_b32 s57,s44,11
s_cmp_eq_u32 s45,175
s_cbranch_scc1 Lg4_pre_%=
s_add_u32 s55,s50,0
s_add_u32 m0,s57,40960
s_nop 0
buffer_load_dwordx4 v10,s[40:43],s55 offen offset:0  lds
s_add_u32 m0,s57,41984
s_nop 0
buffer_load_dwordx4 v11,s[40:43],s55 offen offset:0  lds
s_add_u32 m0,s57,49024
s_nop 0
buffer_load_dwordx4 v10,s[40:43],s55 offen offset:128  lds
s_add_u32 m0,s57,50048
s_nop 0
buffer_load_dwordx4 v11,s[40:43],s55 offen offset:128  lds
s_add_u32 s55,s50,131072
s_add_u32 m0,s57,57344
s_nop 0
buffer_load_dwordx4 v10,s[40:43],s55 offen offset:0  lds
s_add_u32 m0,s57,58368
s_nop 0
buffer_load_dwordx4 v11,s[40:43],s55 offen offset:0  lds
s_add_u32 m0,s57,65408
s_nop 0
buffer_load_dwordx4 v10,s[40:43],s55 offen offset:128  lds
s_add_u32 m0,s57,66432
s_nop 0
buffer_load_dwordx4 v11,s[40:43],s55 offen offset:128  lds
s_add_u32 s55,s50,262144
s_add_u32 m0,s57,73728
s_nop 0
buffer_load_dwordx4 v10,s[40:43],s55 offen offset:0  lds
s_add_u32 m0,s57,74752
s_nop 0
buffer_load_dwordx4 v11,s[40:43],s55 offen offset:0  lds
s_add_u32 m0,s57,81792
s_nop 0
buffer_load_dwordx4 v10,s[40:43],s55 offen offset:128  lds
s_add_u32 m0,s57,82816
s_nop 0
buffer_load_dwordx4 v11,s[40:43],s55 offen offset:128  lds
s_add_u32 s55,s50,393216
s_add_u32 m0,s57,90112
s_nop 0
buffer_load_dwordx4 v10,s[40:43],s55 offen offset:0  lds
s_add_u32 m0,s57,91136
s_nop 0
buffer_load_dwordx4 v11,s[40:43],s55 offen offset:0  lds
s_add_u32 m0,s57,98176
s_nop 0
buffer_load_dwordx4 v10,s[40:43],s55 offen offset:128  lds
s_add_u32 m0,s57,99200
s_nop 0
buffer_load_dwordx4 v11,s[40:43],s55 offen offset:128  lds
s_add_u32 s55,s50,524288
s_add_u32 m0,s57,106496
s_nop 0
buffer_load_dwordx4 v10,s[40:43],s55 offen offset:0  lds
s_add_u32 m0,s57,107520
s_nop 0
buffer_load_dwordx4 v11,s[40:43],s55 offen offset:0  lds
s_add_u32 m0,s57,114560
s_nop 0
buffer_load_dwordx4 v10,s[40:43],s55 offen offset:128  lds
s_add_u32 m0,s57,115584
s_nop 0
buffer_load_dwordx4 v11,s[40:43],s55 offen offset:128  lds
s_add_u32 s55,s50,655360
s_add_u32 m0,s57,122880
s_nop 0
buffer_load_dwordx4 v10,s[40:43],s55 offen offset:0  lds
s_add_u32 m0,s57,123904
s_nop 0
buffer_load_dwordx4 v11,s[40:43],s55 offen offset:0  lds
s_add_u32 m0,s57,130944
s_nop 0
buffer_load_dwordx4 v10,s[40:43],s55 offen offset:128  lds
s_add_u32 m0,s57,131968
s_nop 0
buffer_load_dwordx4 v11,s[40:43],s55 offen offset:128  lds
s_waitcnt vmcnt(20) lgkmcnt(0)
s_barrier
s_add_u32 s55,s50,786432
s_add_u32 m0,s57,139264
s_nop 0
buffer_load_dwordx4 v10,s[40:43],s55 offen offset:0  lds
s_add_u32 m0,s57,140288
s_nop 0
buffer_load_dwordx4 v11,s[40:43],s55 offen offset:0  lds
s_add_u32 m0,s57,147328
s_nop 0
buffer_load_dwordx4 v10,s[40:43],s55 offen offset:128  lds
s_add_u32 m0,s57,148352
s_nop 0
buffer_load_dwordx4 v11,s[40:43],s55 offen offset:128  lds
v_add_u32 v18,40960,v12
v_add_u32 v19,40960,v13
T a[0:1],18,0
T a[2:3],18,512
T a[4:5],19,0
T a[6:7],19,512
T a[8:9],18,4096
T a[10:11],18,4608
T a[12:13],19,4096
T a[14:15],19,4608
s_waitcnt vmcnt(20) lgkmcnt(0)
s_barrier
s_add_u32 s55,s50,917504
s_add_u32 m0,s57,40960
s_nop 0
buffer_load_dwordx4 v10,s[40:43],s55 offen offset:0  lds
s_add_u32 m0,s57,41984
s_nop 0
buffer_load_dwordx4 v11,s[40:43],s55 offen offset:0  lds
s_add_u32 m0,s57,49024
s_nop 0
buffer_load_dwordx4 v10,s[40:43],s55 offen offset:128  lds
s_add_u32 m0,s57,50048
s_nop 0
buffer_load_dwordx4 v11,s[40:43],s55 offen offset:128  lds
v_add_u32 v18,57344,v12
v_add_u32 v19,57344,v13
T a[16:17],18,0
T a[18:19],18,512
T a[20:21],19,0
T a[22:23],19,512
T a[24:25],18,4096
T a[26:27],18,4608
T a[28:29],19,4096
T a[30:31],19,4608
s_waitcnt vmcnt(20) lgkmcnt(0)
s_barrier
s_add_u32 s55,s50,1048576
s_add_u32 m0,s57,57344
s_nop 0
buffer_load_dwordx4 v10,s[40:43],s55 offen offset:0  lds
s_add_u32 m0,s57,58368
s_nop 0
buffer_load_dwordx4 v11,s[40:43],s55 offen offset:0  lds
s_add_u32 m0,s57,65408
s_nop 0
buffer_load_dwordx4 v10,s[40:43],s55 offen offset:128  lds
s_add_u32 m0,s57,66432
s_nop 0
buffer_load_dwordx4 v11,s[40:43],s55 offen offset:128  lds
v_add_u32 v18,73728,v12
v_add_u32 v19,73728,v13
T a[32:33],18,0
T a[34:35],18,512
T a[36:37],19,0
T a[38:39],19,512
T a[40:41],18,4096
T a[42:43],18,4608
T a[44:45],19,4096
T a[46:47],19,4608
s_waitcnt vmcnt(20) lgkmcnt(0)
s_barrier
s_add_u32 s55,s50,1179648
s_add_u32 m0,s57,73728
s_nop 0
buffer_load_dwordx4 v10,s[40:43],s55 offen offset:0  lds
s_add_u32 m0,s57,74752
s_nop 0
buffer_load_dwordx4 v11,s[40:43],s55 offen offset:0  lds
s_add_u32 m0,s57,81792
s_nop 0
buffer_load_dwordx4 v10,s[40:43],s55 offen offset:128  lds
s_add_u32 m0,s57,82816
s_nop 0
buffer_load_dwordx4 v11,s[40:43],s55 offen offset:128  lds
v_add_u32 v18,90112,v12
v_add_u32 v19,90112,v13
T a[48:49],18,0
T a[50:51],18,512
T a[52:53],19,0
T a[54:55],19,512
T a[56:57],18,4096
T a[58:59],18,4608
T a[60:61],19,4096
T a[62:63],19,4608
s_waitcnt vmcnt(20) lgkmcnt(0)
s_barrier
s_add_u32 s55,s50,1310720
s_add_u32 m0,s57,90112
s_nop 0
buffer_load_dwordx4 v10,s[40:43],s55 offen offset:0  lds
s_add_u32 m0,s57,91136
s_nop 0
buffer_load_dwordx4 v11,s[40:43],s55 offen offset:0  lds
s_add_u32 m0,s57,98176
s_nop 0
buffer_load_dwordx4 v10,s[40:43],s55 offen offset:128  lds
s_add_u32 m0,s57,99200
s_nop 0
buffer_load_dwordx4 v11,s[40:43],s55 offen offset:128  lds
v_add_u32 v18,106496,v12
v_add_u32 v19,106496,v13
T a[64:65],18,0
T a[66:67],18,512
T a[68:69],19,0
T a[70:71],19,512
T a[72:73],18,4096
T a[74:75],18,4608
T a[76:77],19,4096
T a[78:79],19,4608
s_waitcnt vmcnt(20) lgkmcnt(0)
s_barrier
s_add_u32 s55,s50,1441792
s_add_u32 m0,s57,106496
s_nop 0
buffer_load_dwordx4 v10,s[40:43],s55 offen offset:0  lds
s_add_u32 m0,s57,107520
s_nop 0
buffer_load_dwordx4 v11,s[40:43],s55 offen offset:0  lds
s_add_u32 m0,s57,114560
s_nop 0
buffer_load_dwordx4 v10,s[40:43],s55 offen offset:128  lds
s_add_u32 m0,s57,115584
s_nop 0
buffer_load_dwordx4 v11,s[40:43],s55 offen offset:128  lds
v_add_u32 v18,122880,v12
v_add_u32 v19,122880,v13
T a[80:81],18,0
T a[82:83],18,512
T a[84:85],19,0
T a[86:87],19,512
T a[88:89],18,4096
T a[90:91],18,4608
T a[92:93],19,4096
T a[94:95],19,4608
s_waitcnt vmcnt(20) lgkmcnt(0)
s_barrier
s_add_u32 s55,s50,1572864
s_add_u32 m0,s57,122880
s_nop 0
buffer_load_dwordx4 v10,s[40:43],s55 offen offset:0  lds
s_add_u32 m0,s57,123904
s_nop 0
buffer_load_dwordx4 v11,s[40:43],s55 offen offset:0  lds
s_add_u32 m0,s57,130944
s_nop 0
buffer_load_dwordx4 v10,s[40:43],s55 offen offset:128  lds
s_add_u32 m0,s57,131968
s_nop 0
buffer_load_dwordx4 v11,s[40:43],s55 offen offset:128  lds
v_add_u32 v18,139264,v12
v_add_u32 v19,139264,v13
T a[96:97],18,0
T a[98:99],18,512
T a[100:101],19,0
T a[102:103],19,512
T a[104:105],18,4096
T a[106:107],18,4608
T a[108:109],19,4096
T a[110:111],19,4608
s_waitcnt vmcnt(20) lgkmcnt(0)
s_barrier
s_add_u32 s55,s50,1703936
s_add_u32 m0,s57,139264
s_nop 0
buffer_load_dwordx4 v10,s[40:43],s55 offen offset:0  lds
s_add_u32 m0,s57,140288
s_nop 0
buffer_load_dwordx4 v11,s[40:43],s55 offen offset:0  lds
s_add_u32 m0,s57,147328
s_nop 0
buffer_load_dwordx4 v10,s[40:43],s55 offen offset:128  lds
s_add_u32 m0,s57,148352
s_nop 0
buffer_load_dwordx4 v11,s[40:43],s55 offen offset:128  lds
v_add_u32 v18,40960,v12
v_add_u32 v19,40960,v13
T a[112:113],18,0
T a[114:115],18,512
T a[116:117],19,0
T a[118:119],19,512
T a[120:121],18,4096
T a[122:123],18,4608
T a[124:125],19,4096
T a[126:127],19,4608
s_waitcnt vmcnt(20) lgkmcnt(0)
s_barrier
s_add_u32 s55,s50,1835008
s_add_u32 m0,s57,40960
s_nop 0
buffer_load_dwordx4 v10,s[40:43],s55 offen offset:0  lds
s_add_u32 m0,s57,41984
s_nop 0
buffer_load_dwordx4 v11,s[40:43],s55 offen offset:0  lds
s_add_u32 m0,s57,49024
s_nop 0
buffer_load_dwordx4 v10,s[40:43],s55 offen offset:128  lds
s_add_u32 m0,s57,50048
s_nop 0
buffer_load_dwordx4 v11,s[40:43],s55 offen offset:128  lds
v_add_u32 v18,57344,v12
v_add_u32 v19,57344,v13
T a[128:129],18,0
T a[130:131],18,512
T a[132:133],19,0
T a[134:135],19,512
T a[136:137],18,4096
T a[138:139],18,4608
T a[140:141],19,4096
T a[142:143],19,4608
s_waitcnt vmcnt(20) lgkmcnt(0)
s_barrier
s_add_u32 s55,s50,1966080
s_add_u32 m0,s57,57344
s_nop 0
buffer_load_dwordx4 v10,s[40:43],s55 offen offset:0  lds
s_add_u32 m0,s57,58368
s_nop 0
buffer_load_dwordx4 v11,s[40:43],s55 offen offset:0  lds
s_add_u32 m0,s57,65408
s_nop 0
buffer_load_dwordx4 v10,s[40:43],s55 offen offset:128  lds
s_add_u32 m0,s57,66432
s_nop 0
buffer_load_dwordx4 v11,s[40:43],s55 offen offset:128  lds
v_add_u32 v18,73728,v12
v_add_u32 v19,73728,v13
T a[144:145],18,0
T a[146:147],18,512
T a[148:149],19,0
T a[150:151],19,512
T a[152:153],18,4096
T a[154:155],18,4608
T a[156:157],19,4096
T a[158:159],19,4608
s_waitcnt vmcnt(20) lgkmcnt(0)
s_barrier
s_add_u32 s55,s50,2097152
s_add_u32 m0,s57,73728
s_nop 0
buffer_load_dwordx4 v10,s[40:43],s55 offen offset:0  lds
s_add_u32 m0,s57,74752
s_nop 0
buffer_load_dwordx4 v11,s[40:43],s55 offen offset:0  lds
s_add_u32 m0,s57,81792
s_nop 0
buffer_load_dwordx4 v10,s[40:43],s55 offen offset:128  lds
s_add_u32 m0,s57,82816
s_nop 0
buffer_load_dwordx4 v11,s[40:43],s55 offen offset:128  lds
v_add_u32 v18,90112,v12
v_add_u32 v19,90112,v13
T a[160:161],18,0
T a[162:163],18,512
T a[164:165],19,0
T a[166:167],19,512
T a[168:169],18,4096
T a[170:171],18,4608
T a[172:173],19,4096
T a[174:175],19,4608
s_waitcnt vmcnt(20) lgkmcnt(0)
s_barrier
s_add_u32 s55,s50,2228224
s_add_u32 m0,s57,90112
s_nop 0
buffer_load_dwordx4 v10,s[40:43],s55 offen offset:0  lds
s_add_u32 m0,s57,91136
s_nop 0
buffer_load_dwordx4 v11,s[40:43],s55 offen offset:0  lds
s_add_u32 m0,s57,98176
s_nop 0
buffer_load_dwordx4 v10,s[40:43],s55 offen offset:128  lds
s_add_u32 m0,s57,99200
s_nop 0
buffer_load_dwordx4 v11,s[40:43],s55 offen offset:128  lds
v_add_u32 v18,106496,v12
v_add_u32 v19,106496,v13
T a[176:177],18,0
T a[178:179],18,512
T a[180:181],19,0
T a[182:183],19,512
T a[184:185],18,4096
T a[186:187],18,4608
T a[188:189],19,4096
T a[190:191],19,4608
s_waitcnt vmcnt(20) lgkmcnt(0)
s_barrier
s_add_u32 s55,s50,2359296
s_add_u32 m0,s57,106496
s_nop 0
buffer_load_dwordx4 v10,s[40:43],s55 offen offset:0  lds
s_add_u32 m0,s57,107520
s_nop 0
buffer_load_dwordx4 v11,s[40:43],s55 offen offset:0  lds
s_add_u32 m0,s57,114560
s_nop 0
buffer_load_dwordx4 v10,s[40:43],s55 offen offset:128  lds
s_add_u32 m0,s57,115584
s_nop 0
buffer_load_dwordx4 v11,s[40:43],s55 offen offset:128  lds
v_add_u32 v18,122880,v12
v_add_u32 v19,122880,v13
T a[192:193],18,0
T a[194:195],18,512
T a[196:197],19,0
T a[198:199],19,512
T a[200:201],18,4096
T a[202:203],18,4608
T a[204:205],19,4096
T a[206:207],19,4608
s_waitcnt vmcnt(20) lgkmcnt(0)
s_barrier
s_add_u32 s55,s50,2490368
s_add_u32 m0,s57,122880
s_nop 0
buffer_load_dwordx4 v10,s[40:43],s55 offen offset:0  lds
s_add_u32 m0,s57,123904
s_nop 0
buffer_load_dwordx4 v11,s[40:43],s55 offen offset:0  lds
s_add_u32 m0,s57,130944
s_nop 0
buffer_load_dwordx4 v10,s[40:43],s55 offen offset:128  lds
s_add_u32 m0,s57,131968
s_nop 0
buffer_load_dwordx4 v11,s[40:43],s55 offen offset:128  lds
v_add_u32 v18,139264,v12
v_add_u32 v19,139264,v13
T a[208:209],18,0
T a[210:211],18,512
T a[212:213],19,0
T a[214:215],19,512
T a[216:217],18,4096
T a[218:219],18,4608
T a[220:221],19,4096
T a[222:223],19,4608
s_waitcnt vmcnt(20) lgkmcnt(0)
s_barrier
s_add_u32 s55,s50,2621440
s_add_u32 m0,s57,139264
s_nop 0
buffer_load_dwordx4 v10,s[40:43],s55 offen offset:0  lds
s_add_u32 m0,s57,140288
s_nop 0
buffer_load_dwordx4 v11,s[40:43],s55 offen offset:0  lds
s_add_u32 m0,s57,147328
s_nop 0
buffer_load_dwordx4 v10,s[40:43],s55 offen offset:128  lds
s_add_u32 m0,s57,148352
s_nop 0
buffer_load_dwordx4 v11,s[40:43],s55 offen offset:128  lds
v_add_u32 v18,40960,v12
v_add_u32 v19,40960,v13
T a[224:225],18,0
T a[226:227],18,512
T a[228:229],19,0
T a[230:231],19,512
T a[232:233],18,4096
T a[234:235],18,4608
T a[236:237],19,4096
T a[238:239],19,4608
s_waitcnt vmcnt(20) lgkmcnt(0)
s_barrier
)"
R"(s_add_u32 s55,s50,2752512
s_add_u32 m0,s57,40960
s_nop 0
buffer_load_dwordx4 v10,s[40:43],s55 offen offset:0  lds
s_add_u32 m0,s57,41984
s_nop 0
buffer_load_dwordx4 v11,s[40:43],s55 offen offset:0  lds
s_add_u32 m0,s57,49024
s_nop 0
buffer_load_dwordx4 v10,s[40:43],s55 offen offset:128  lds
s_add_u32 m0,s57,50048
s_nop 0
buffer_load_dwordx4 v11,s[40:43],s55 offen offset:128  lds
v_add_u32 v18,57344,v12
v_add_u32 v19,57344,v13
T a[240:241],18,0
T a[242:243],18,512
T a[244:245],19,0
T a[246:247],19,512
T a[248:249],18,4096
T a[250:251],18,4608
T a[252:253],19,4096
T a[254:255],19,4608
Lg4_pre_%=:
s_waitcnt vmcnt(20) lgkmcnt(0)
s_barrier
s_add_u32 s55,s50,2883584
s_add_u32 m0,s57,57344
s_nop 0
buffer_load_dwordx4 v10,s[40:43],s55 offen offset:0  lds
s_add_u32 m0,s57,58368
s_nop 0
buffer_load_dwordx4 v11,s[40:43],s55 offen offset:0  lds
s_add_u32 m0,s57,65408
s_nop 0
buffer_load_dwordx4 v10,s[40:43],s55 offen offset:128  lds
s_add_u32 m0,s57,66432
s_nop 0
buffer_load_dwordx4 v11,s[40:43],s55 offen offset:128  lds
v_add_u32 v18,73728,v12
v_add_u32 v19,73728,v13
T v[64:65],18,0
T v[66:67],18,512
T v[68:69],19,0
T v[70:71],19,512
T v[72:73],18,4096
T v[74:75],18,4608
T v[76:77],19,4096
T v[78:79],19,4608
s_waitcnt vmcnt(20) lgkmcnt(0)
s_barrier
s_add_u32 s55,s50,3014656
s_add_u32 m0,s57,73728
s_nop 0
buffer_load_dwordx4 v10,s[40:43],s55 offen offset:0  lds
s_add_u32 m0,s57,74752
s_nop 0
buffer_load_dwordx4 v11,s[40:43],s55 offen offset:0  lds
s_add_u32 m0,s57,81792
s_nop 0
buffer_load_dwordx4 v10,s[40:43],s55 offen offset:128  lds
s_add_u32 m0,s57,82816
s_nop 0
buffer_load_dwordx4 v11,s[40:43],s55 offen offset:128  lds
v_add_u32 v18,90112,v12
v_add_u32 v19,90112,v13
T v[80:81],18,0
T v[82:83],18,512
T v[84:85],19,0
T v[86:87],19,512
T v[88:89],18,4096
T v[90:91],18,4608
T v[92:93],19,4096
T v[94:95],19,4608
s_waitcnt vmcnt(20) lgkmcnt(0)
s_barrier
s_add_u32 s55,s50,3145728
s_add_u32 m0,s57,90112
s_nop 0
buffer_load_dwordx4 v10,s[40:43],s55 offen offset:0  lds
s_add_u32 m0,s57,91136
s_nop 0
buffer_load_dwordx4 v11,s[40:43],s55 offen offset:0  lds
s_add_u32 m0,s57,98176
s_nop 0
buffer_load_dwordx4 v10,s[40:43],s55 offen offset:128  lds
s_add_u32 m0,s57,99200
s_nop 0
buffer_load_dwordx4 v11,s[40:43],s55 offen offset:128  lds
v_add_u32 v18,106496,v12
v_add_u32 v19,106496,v13
T v[96:97],18,0
T v[98:99],18,512
T v[100:101],19,0
T v[102:103],19,512
T v[104:105],18,4096
T v[106:107],18,4608
T v[108:109],19,4096
T v[110:111],19,4608
s_waitcnt vmcnt(20) lgkmcnt(0)
s_barrier
s_add_u32 s55,s50,3276800
s_add_u32 m0,s57,106496
s_nop 0
buffer_load_dwordx4 v10,s[40:43],s55 offen offset:0  lds
s_add_u32 m0,s57,107520
s_nop 0
buffer_load_dwordx4 v11,s[40:43],s55 offen offset:0  lds
s_add_u32 m0,s57,114560
s_nop 0
buffer_load_dwordx4 v10,s[40:43],s55 offen offset:128  lds
s_add_u32 m0,s57,115584
s_nop 0
buffer_load_dwordx4 v11,s[40:43],s55 offen offset:128  lds
v_add_u32 v18,122880,v12
v_add_u32 v19,122880,v13
T v[112:113],18,0
T v[114:115],18,512
T v[116:117],19,0
T v[118:119],19,512
T v[120:121],18,4096
T v[122:123],18,4608
T v[124:125],19,4096
T v[126:127],19,4608
s_waitcnt vmcnt(20) lgkmcnt(0)
s_barrier
s_add_u32 s55,s50,3407872
s_add_u32 m0,s57,122880
s_nop 0
buffer_load_dwordx4 v10,s[40:43],s55 offen offset:0  lds
s_add_u32 m0,s57,123904
s_nop 0
buffer_load_dwordx4 v11,s[40:43],s55 offen offset:0  lds
s_add_u32 m0,s57,130944
s_nop 0
buffer_load_dwordx4 v10,s[40:43],s55 offen offset:128  lds
s_add_u32 m0,s57,131968
s_nop 0
buffer_load_dwordx4 v11,s[40:43],s55 offen offset:128  lds
v_add_u32 v18,139264,v12
v_add_u32 v19,139264,v13
T v[128:129],18,0
T v[130:131],18,512
T v[132:133],19,0
T v[134:135],19,512
T v[136:137],18,4096
T v[138:139],18,4608
T v[140:141],19,4096
T v[142:143],19,4608
s_waitcnt vmcnt(20) lgkmcnt(0)
s_barrier
s_add_u32 s55,s50,3538944
s_add_u32 m0,s57,139264
s_nop 0
buffer_load_dwordx4 v10,s[40:43],s55 offen offset:0  lds
s_add_u32 m0,s57,140288
s_nop 0
buffer_load_dwordx4 v11,s[40:43],s55 offen offset:0  lds
s_add_u32 m0,s57,147328
s_nop 0
buffer_load_dwordx4 v10,s[40:43],s55 offen offset:128  lds
s_add_u32 m0,s57,148352
s_nop 0
buffer_load_dwordx4 v11,s[40:43],s55 offen offset:128  lds
v_add_u32 v18,40960,v12
v_add_u32 v19,40960,v13
T v[144:145],18,0
T v[146:147],18,512
T v[148:149],19,0
T v[150:151],19,512
T v[152:153],18,4096
T v[154:155],18,4608
T v[156:157],19,4096
T v[158:159],19,4608
s_waitcnt vmcnt(20) lgkmcnt(0)
s_barrier
v_add_u32 v18,57344,v12
v_add_u32 v19,57344,v13
T v[160:161],18,0
T v[162:163],18,512
T v[164:165],19,0
T v[166:167],19,512
T v[168:169],18,4096
T v[170:171],18,4608
T v[172:173],19,4096
T v[174:175],19,4608
s_waitcnt vmcnt(16) lgkmcnt(0)
s_barrier
v_add_u32 v18,73728,v12
v_add_u32 v19,73728,v13
T v[176:177],18,0
T v[178:179],18,512
T v[180:181],19,0
T v[182:183],19,512
T v[184:185],18,4096
T v[186:187],18,4608
T v[188:189],19,4096
T v[190:191],19,4608
s_waitcnt vmcnt(12) lgkmcnt(0)
s_barrier
v_add_u32 v18,90112,v12
v_add_u32 v19,90112,v13
T v[192:193],18,0
T v[194:195],18,512
T v[196:197],19,0
T v[198:199],19,512
T v[200:201],18,4096
T v[202:203],18,4608
T v[204:205],19,4096
T v[206:207],19,4608
s_waitcnt vmcnt(8) lgkmcnt(0)
s_barrier
v_add_u32 v18,106496,v12
v_add_u32 v19,106496,v13
T v[208:209],18,0
T v[210:211],18,512
T v[212:213],19,0
T v[214:215],19,512
T v[216:217],18,4096
T v[218:219],18,4608
T v[220:221],19,4096
T v[222:223],19,4608
s_waitcnt vmcnt(4) lgkmcnt(0)
s_barrier
v_add_u32 v18,122880,v12
v_add_u32 v19,122880,v13
T v[224:225],18,0
T v[226:227],18,512
T v[228:229],19,0
T v[230:231],19,512
T v[232:233],18,4096
T v[234:235],18,4608
T v[236:237],19,4096
T v[238:239],19,4608
s_waitcnt vmcnt(0) lgkmcnt(0)
s_barrier
v_add_u32 v18,139264,v12
v_add_u32 v19,139264,v13
T v[240:241],18,0
T v[242:243],18,512
T v[244:245],19,0
T v[246:247],19,512
T v[248:249],18,4096
T v[250:251],18,4608
T v[252:253],19,4096
T v[254:255],19,4608
L 0
s_barrier
v_mov_b32 v4,s53
s_mov_b32 s82,0
s_mov_b32 s80,0
s_mov_b32 s54,s45
s_mul_i32 s57,s44,7168
s_mul_i32 s55,s45,114688
s_add_u32 s55,s55,s57
s_add_u32 s55,s55,40894464
s_mul_i32 s57,s44,7168
s_mul_i32 s60,s46,114688
s_mul_i32 s61,s46,2560
s_mul_i32 s62,s46,32768
s_mul_i32 s63,s46,2048
s_mul_i32 s64,s46,512
s_lshl_b32 s59,s44,8
s_add_u32 s59,s59,143360
s_mul_i32 s58,s45,2560
s_add_u32 s58,s58,109891584
s_mul_i32 s65,s45,32768
s_add_u32 s65,s65,s47
s_mul_i32 s68,s45,2048
s_add_u32 s68,s68,s48
s_mul_i32 s73,s45,512
s_add_u32 s73,s73,106954752
s_mul_i32 s74,s45,32768
s_add_u32 s74,s74,s49
s_mul_i32 s75,s45,1024
s_add_u32 s75,s75,107479040
s_mov_b32 s66,0x7fff0000
s_mov_b32 s67,0x7fff0000
s_mov_b32 s69,0x7fff0000
s_mov_b32 s76,0x7fff0000
s_mov_b32 s77,0x7fff0000
s_mov_b32 s70,0x7fff0000
s_mov_b32 s71,0x7fff0000
s_mov_b32 s72,0x7fff0000
v_mov_b32 v1,v0
v_add_u32 v2,28672,v0
s_mov_b32 s84,28672
s_mov_b32 s86,0x3a800000
s_mov_b32 s87,0x3727c5ac
s_mov_b32 s88,0xc0135761
s_mov_b32 s89,0x3d372713
s_lshr_b32 s85,s44,1
s_mul_i32 s85,s85,127
s_add_u32 s55,s55,s51
s_mul_i32 s78,s45,114688
s_sub_u32 s60,s55,s78
s_add_u32 s50,s93,224
s_add_u32 s93,s93,170
s_cmp_lt_u32 s45,224
s_cselect_b32 s63,224,-1
s_cmp_lt_u32 s45,170
s_cselect_b32 s63,170,s63
s_cselect_b32 s93,s93,s50
s_cselect_b32 s88,6,8
s_cselect_b32 s89,224,-1
v_lshlrev_b32 v8,22,v0
v_and_b32 v8,0x80000000,v8
v_or_b32 v8,v8,v5
v_add_u32 v2,57344,v0
s_lshl_b32 s84,s57,1
s_add_u32 s84,s84,57344
s_mov_b64 s[86:87],%22
s_mov_b64 s[40:41],%22
s_mov_b32 s42,4194304
s_and_b32 s78,s47,65535
s_lshr_b32 s78,s78,4
s_sub_u32 s85,s47,s78
s_add_u32 s85,s85,4194304
s_add_u32 s45,s47,5832704
s_mul_i32 s73,s44,1280
s_add_u32 s73,s73,114688
v_lshrrev_b32 v62,4,v9
v_and_b32 v62,3,v62
v_mul_u32_u24 v62,288,v62
v_lshrrev_b32 v63,7,v9
v_and_b32 v5,12,v9
v_add3_u32 v5,v5,v62,v63
v_add_u32 v5,s73,v5
v_mov_b32 v10,0
v_mov_b32 v11,0
v_mov_b32 v12,0
v_mov_b32 v13,0
s_mov_b32 s79,1638400
buffer_store_dwordx4 v[10:13],v7,s[40:43],s79 offen offset:0 sc0 sc1
s_mov_b32 s79,1966080
buffer_store_dwordx4 v[10:13],v7,s[40:43],s79 offen offset:0 sc0 sc1
s_mov_b32 s79,2293760
buffer_store_dwordx4 v[10:13],v7,s[40:43],s79 offen offset:0 sc0 sc1
s_mov_b32 s79,2621440
buffer_store_dwordx4 v[10:13],v7,s[40:43],s79 offen offset:0 sc0 sc1
s_mov_b32 s79,2949120
buffer_store_dwordx4 v[10:13],v7,s[40:43],s79 offen offset:0 sc0 sc1
s_mov_b32 s79,3276800
buffer_store_dwordx4 v[10:13],v7,s[40:43],s79 offen offset:0 sc0 sc1
s_mov_b32 s79,3604480
buffer_store_dwordx4 v[10:13],v7,s[40:43],s79 offen offset:0 sc0 sc1
s_mov_b32 s79,3932160
buffer_store_dwordx4 v[10:13],v7,s[40:43],s79 offen offset:0 sc0 sc1
s_getpc_b64 s[40:41]
Lg4_pc_%=:
s_lshl_b32 s78,s44,13
v_lshlrev_b32 v62,3,v0
v_add_u32 v62,s78,v62
v_min_u32 v62,Lcode_g4_e-Lg4_pc_%=-4,v62
s_mov_b32 m0,163584
s_nop 0
global_load_lds_dword v62,s[40:41]
s_add_u32 m0,s59,4096
s_nop 0
buffer_load_dword v8,s[36:39],s58 offen offset:0 sc1 lds
buffer_store_dword v4,v3,s[36:39],s71 offen offset:0 sc0 sc1
s_add_u32 m0,s59,5120
s_nop 0
buffer_load_dword v8,s[36:39],s58 offen offset:0 sc1 lds
buffer_store_dword v4,v3,s[36:39],s71 offen offset:0 sc0 sc1
s_mov_b32 s83,0
Lg4_poll_%=:
s_add_u32 s78,s83,2
s_and_b32 s78,s78,3
s_lshl_b32 s78,s78,10
s_add_u32 s78,s78,4096
s_add_u32 m0,s59,s78
s_nop 0
buffer_load_dword v8,s[36:39],s58 offen offset:0 sc1 lds
s_and_b32 s78,s83,3
s_lshl_b32 s78,s78,10
v_add_u32 v62,s78,v6
s_waitcnt vmcnt(4)
s_barrier
ds_read_b32 v7,v62 offset:4096
buffer_store_dword v4,v3,s[36:39],s70 offen offset:0 sc0 sc1
s_mov_b32 s70,s71
s_mov_b32 s71,s72
s_mov_b32 s72,0x7fff0000
s_add_u32 s83,s83,1
s_add_u32 s82,s82,1
L 0
v_cmp_eq_u32 vcc,s53,v7
s_nop 0
s_cmp_eq_u32 vcc_lo,-1
s_cbranch_scc1 Lg4_go_%=
s_cmp_lt_u32 s82,20000
s_cbranch_scc1 Lg4_poll_%=
Lg4_go_%=:
s_add_u32 m0,s57,0
s_add_u32 s56,s55,0
D 0,56,0
D 0,56,1024
D 0,56,2048
D 0,56,3072
s_add_u32 m0,s57,4096
s_add_u32 s56,s56,4096
D 0,56,0
D 0,56,1024
D 0,56,2048
s_add_u32 s61,s54,s46
s_cmp_ge_u32 s61,s63
s_cselect_b32 s61,s93,s61
s_cselect_b32 s46,s88,s46
s_cselect_b32 s63,s89,s63
s_cselect_b32 s93,s50,s93
s_cselect_b32 s88,8,s88
s_cselect_b32 s89,-1,s89
s_mul_i32 s58,s61,2560
s_add_u32 s58,s58,109891584
s_add_u32 m0,s59,2048
s_nop 0
buffer_load_dword v8,s[36:39],s58 offen offset:0 sc1 lds
s_add_u32 m0,s57,28672
s_add_u32 s56,s55,28672
D 0,56,0
D 0,56,1024
D 0,56,2048
D 0,56,3072
s_add_u32 m0,s57,32768
s_add_u32 s56,s56,4096
D 0,56,0
D 0,56,1024
D 0,56,2048
s_mul_i32 s55,s61,114688
s_add_u32 s55,s55,s60
s_sub_u32 s57,s84,s57
s_mov_b32 s54,s61
s_waitcnt vmcnt(7)
s_barrier
buffer_store_dword v4,v3,s[36:39],s70 offen offset:0 sc0 sc1
s_min_u32 s71,s71,s72
buffer_store_dword v4,v3,s[36:39],s71 offen offset:0 sc0 sc1
s_mov_b32 s70,0x7fff0000
s_mov_b32 s71,0x7fff0000
s_mov_b32 s72,0x7fff0000
s_cmp_eq_u32 s80,1
s_cbranch_scc1 Lg4_st1_%=
s_branch Lg4_st0_%=
Lg4_gox_%=:
s_add_u32 m0,s59,2048
s_nop 0
buffer_load_dword v8,s[36:39],s58 offen offset:0 sc1 lds
s_waitcnt vmcnt(0)
s_barrier
buffer_store_dword v4,v3,s[36:39],s70 offen offset:0 sc0 sc1
s_min_u32 s71,s71,s72
buffer_store_dword v4,v3,s[36:39],s71 offen offset:0 sc0 sc1
s_mov_b32 s70,0x7fff0000
s_mov_b32 s71,0x7fff0000
s_mov_b32 s72,0x7fff0000
s_cmp_eq_u32 s80,1
s_cbranch_scc1 Lg4_st1_%=
Lg4_st0_%=:
ds_read_b32 v7,v6 offset:2048
R 26,1,0
R 30,1,1024
R 34,1,2048
R 38,1,3072
R 42,1,4096
L 5
v_cmp_eq_u32 vcc,s53,v7
L 3
s_cmp_eq_u32 vcc_lo,-1
s_cbranch_scc1 Lg4_F0_%=
s_branch Lg4_L0_%=
Lg4_st1_%=:
ds_read_b32 v7,v6 offset:2048
R 26,2,0
R 30,2,1024
R 34,2,2048
R 38,2,3072
R 42,2,4096
L 5
v_cmp_eq_u32 vcc,s53,v7
L 3
s_cmp_eq_u32 vcc_lo,-1
s_cbranch_scc1 Lg4_F1_%=
s_branch Lg4_L1_%=
Lg4_F0_%=:
v_mfma_f32_16x16x32_bf16 v[14:17],a[4:7],v[26:29],0
s_add_u32 m0,s57,0
R 46,1,5120
v_lshlrev_b32 v62,16,v58
M 14,a[12:15],30
s_add_u32 s56,s55,0
v_add_f32 v18,v18,v62
v_and_b32 v63,0xffff0000,v58
v_mfma_f32_16x16x32_bf16 v[10:13],a[0:3],v[26:29],0
s_add_u32 s61,s54,s46
R 50,1,6144
v_add_f32 v19,v19,v63
M 10,a[8:11],30
L 3
D 0,56,0
)"
R"(M 10,a[16:19],34
s_cmp_ge_u32 s61,s63
R 54,1,7168
v_lshlrev_b32 v62,16,v59
M 10,a[24:27],38
s_cselect_b32 s61,s93,s61
v_add_f32 v20,v20,v62
v_and_b32 v63,0xffff0000,v59
M 14,a[20:23],34
s_cselect_b32 s46,s88,s46
R 26,1,8192
v_add_f32 v21,v21,v63
M 14,a[28:31],38
L 3
v_lshlrev_b32 v62,16,v60
v_add_f32 v22,v22,v62
M 14,a[36:39],42
s_cselect_b32 s63,s89,s63
R 30,1,9216
v_and_b32 v63,0xffff0000,v60
M 14,a[44:47],46
D 0,56,1024
M 10,a[32:35],42
s_cselect_b32 s93,s50,s93
R 34,1,10240
v_add_f32 v23,v23,v63
M 10,a[40:43],46
L 3
v_lshlrev_b32 v62,16,v61
v_add_f32 v24,v24,v62
M 10,a[48:51],50
s_cselect_b32 s88,8,s88
R 38,1,11264
v_and_b32 v63,0xffff0000,v61
M 10,a[56:59],54
buffer_load_dwordx4 v[58:61],v0,s[36:39],s74 offen offset:0 sc1
M 14,a[52:55],50
s_cselect_b32 s89,-1,s89
R 42,1,12288
v_add_f32 v25,v25,v63
M 14,a[60:63],54
L 3
v_cvt_pk_bf16_f32 v18,v18,v19
v_cvt_pk_bf16_f32 v19,v20,v21
M 14,a[68:71],26
s_mul_i32 s58,s61,2560
R 46,1,13312
v_cvt_pk_bf16_f32 v20,v22,v23
M 14,a[76:79],30
D 0,56,2048
M 10,a[64:67],26
s_add_u32 s58,s58,109891584
R 50,1,14336
v_cvt_pk_bf16_f32 v21,v24,v25
M 10,a[72:75],30
L 3
buffer_store_dwordx4 v[18:21],v0,s[36:39],s67 offen offset:0 sc0 sc1
M 10,a[80:83],34
s_lshl_b32 s62,s54,15
s_mov_b32 s66,s65
R 54,1,15360
M 10,a[88:91],38
s_cmp_ge_u32 s66,s45
s_cselect_b32 s51,1,-1
s_add_u32 s74,s49,s62
M 14,a[84:87],34
s_add_u32 s65,s47,s62
s_mov_b32 s54,s61
R 26,1,16384
M 14,a[92:95],38
L 3
M 14,a[100:103],42
R 30,1,17408
M 14,a[108:111],46
D 0,56,3072
M 10,a[96:99],42
s_add_u32 m0,s57,4096
s_add_u32 s56,s56,4096
R 34,1,18432
M 10,a[104:107],46
L 3
M 10,a[112:115],50
R 38,1,19456
M 10,a[120:123],54
M 14,a[116:119],50
R 42,1,20480
M 14,a[124:127],54
L 3
D 0,56,0
M 14,a[132:135],26
R 46,1,21504
M 14,a[140:143],30
M 10,a[128:131],26
R 50,1,22528
M 10,a[136:139],30
L 3
M 10,a[144:147],34
R 54,1,23552
M 10,a[152:155],38
M 14,a[148:151],34
R 26,1,24576
M 14,a[156:159],38
L 3
D 0,56,1024
M 14,a[164:167],42
R 30,1,25600
M 14,a[172:175],46
M 10,a[160:163],42
R 34,1,26624
M 10,a[168:171],46
L 3
M 10,a[176:179],50
s_waitcnt vmcnt(9)
R 38,1,27648
M 10,a[184:187],54
s_barrier
M 14,a[180:183],50
R 42,1,28672
M 14,a[188:191],54
L 3
D 0,56,2048
M 14,a[196:199],26
s_add_u32 m0,s59,0
s_add_u32 s56,s55,28672
R 46,1,29696
M 14,a[204:207],30
s_mul_i32 s55,s61,114688
s_add_u32 s55,s55,s60
M 10,a[192:195],26
R 50,1,30720
M 10,a[200:203],30
L 3
M 10,a[208:211],34
R 54,1,31744
M 10,a[216:219],38
M 14,a[212:215],34
R 26,1,32768
M 14,a[220:223],38
L 3
buffer_load_dword v8,s[36:39],s58 offen offset:0 sc1 lds
M 14,a[228:231],42
s_add_u32 m0,s57,28672
R 30,1,33792
M 14,a[236:239],46
D 0,56,0
M 10,a[224:227],42
R 34,1,34816
M 10,a[232:235],46
L 3
M 10,a[240:243],50
R 38,1,35840
M 10,a[248:251],54
M 14,a[244:247],50
R 42,1,36864
M 14,a[252:255],54
L 3
D 0,56,1024
M 14,v[68:71],26
R 46,1,37888
M 14,v[76:79],30
M 10,v[64:67],26
R 50,1,38912
M 10,v[72:75],30
L 3
M 10,v[80:83],34
R 54,1,39936
M 10,v[88:91],38
D 0,56,2048
M 14,v[84:87],34
R 26,1,40960
M 14,v[92:95],38
L 3
M 14,v[100:103],42
R 30,1,41984
M 14,v[108:111],46
M 10,v[96:99],42
R 34,1,43008
M 10,v[104:107],46
L 3
D 0,56,3072
M 10,v[112:115],50
s_add_u32 m0,s57,32768
s_add_u32 s56,s56,4096
R 38,1,44032
M 10,v[120:123],54
s_sub_u32 s57,s84,s57
M 14,v[116:119],50
R 42,1,45056
M 14,v[124:127],54
L 3
M 14,v[132:135],26
R 46,1,46080
M 14,v[140:143],30
D 0,56,0
M 10,v[128:131],26
R 50,1,47104
M 10,v[136:139],30
L 3
M 10,v[144:147],34
R 54,1,48128
M 10,v[152:155],38
M 14,v[148:151],34
R 26,1,49152
M 14,v[156:159],38
L 3
D 0,56,1024
M 14,v[164:167],42
R 30,1,50176
M 14,v[172:175],46
M 10,v[160:163],42
R 34,1,51200
M 10,v[168:171],46
L 3
M 10,v[176:179],50
R 38,1,52224
M 10,v[184:187],54
D 0,56,2048
M 14,v[180:183],50
R 42,1,53248
M 14,v[188:191],54
L 3
M 14,v[196:199],26
R 46,1,54272
M 14,v[204:207],30
M 10,v[192:195],26
R 50,1,55296
M 10,v[200:203],30
L 3
M 10,v[208:211],34
s_waitcnt vmcnt(7)
R 54,1,56320
M 10,v[216:219],38
s_barrier
ds_read_b32 v7,v6 offset:0
M 14,v[212:215],34
R 26,2,0
M 14,v[220:223],38
L 4
buffer_store_dword v4,v3,s[36:39],s69 offen offset:0 sc0 sc1
M 14,v[228:231],42
s_mov_b32 s69,s68
s_lshr_b32 s68,s62,4
R 30,2,1024
M 14,v[236:239],46
s_add_u32 s68,s68,s48
M 10,v[224:227],42
R 34,2,2048
M 10,v[232:235],46
L 4
L 3
M 10,v[240:243],50
R 38,2,3072
v_cmp_eq_u32 vcc,s53,v7
M 10,v[248:251],54
M 14,v[244:247],50
L 2
s_cmp_eq_u32 vcc_lo,s51
R 42,2,4096
M 14,v[252:255],54
s_cbranch_scc0 Lg4_K1_%=
Lg4_F1_%=:
v_mfma_f32_16x16x32_bf16 v[22:25],a[4:7],v[26:29],0
s_add_u32 m0,s57,0
R 46,2,5120
v_lshlrev_b32 v62,16,v58
M 22,a[12:15],30
s_add_u32 s56,s55,0
v_add_f32 v10,v10,v62
v_and_b32 v63,0xffff0000,v58
v_mfma_f32_16x16x32_bf16 v[18:21],a[0:3],v[26:29],0
s_add_u32 s61,s54,s46
R 50,2,6144
v_add_f32 v11,v11,v63
M 18,a[8:11],30
L 3
D 0,56,0
M 18,a[16:19],34
s_cmp_ge_u32 s61,s63
R 54,2,7168
v_lshlrev_b32 v62,16,v59
M 18,a[24:27],38
s_cselect_b32 s61,s93,s61
v_add_f32 v12,v12,v62
v_and_b32 v63,0xffff0000,v59
M 22,a[20:23],34
s_cselect_b32 s46,s88,s46
R 26,2,8192
v_add_f32 v13,v13,v63
M 22,a[28:31],38
L 3
v_lshlrev_b32 v62,16,v60
v_add_f32 v14,v14,v62
M 22,a[36:39],42
s_cselect_b32 s63,s89,s63
R 30,2,9216
v_and_b32 v63,0xffff0000,v60
M 22,a[44:47],46
D 0,56,1024
M 18,a[32:35],42
s_cselect_b32 s93,s50,s93
R 34,2,10240
v_add_f32 v15,v15,v63
M 18,a[40:43],46
L 3
v_lshlrev_b32 v62,16,v61
v_add_f32 v16,v16,v62
M 18,a[48:51],50
s_cselect_b32 s88,8,s88
R 38,2,11264
v_and_b32 v63,0xffff0000,v61
M 18,a[56:59],54
buffer_load_dwordx4 v[58:61],v0,s[36:39],s74 offen offset:0 sc1
M 22,a[52:55],50
s_cselect_b32 s89,-1,s89
R 42,2,12288
v_add_f32 v17,v17,v63
M 22,a[60:63],54
L 3
v_cvt_pk_bf16_f32 v10,v10,v11
v_cvt_pk_bf16_f32 v11,v12,v13
M 22,a[68:71],26
s_mul_i32 s58,s61,2560
R 46,2,13312
v_cvt_pk_bf16_f32 v12,v14,v15
M 22,a[76:79],30
D 0,56,2048
M 18,a[64:67],26
s_add_u32 s58,s58,109891584
R 50,2,14336
v_cvt_pk_bf16_f32 v13,v16,v17
M 18,a[72:75],30
L 3
buffer_store_dwordx4 v[10:13],v0,s[36:39],s66 offen offset:0 sc0 sc1
M 18,a[80:83],34
s_lshl_b32 s62,s54,15
s_mov_b32 s67,s65
R 54,2,15360
M 18,a[88:91],38
s_cmp_ge_u32 s67,s45
s_cselect_b32 s51,1,-1
s_add_u32 s74,s49,s62
M 22,a[84:87],34
s_add_u32 s65,s47,s62
s_mov_b32 s54,s61
R 26,2,16384
M 22,a[92:95],38
L 3
M 22,a[100:103],42
R 30,2,17408
M 22,a[108:111],46
D 0,56,3072
M 18,a[96:99],42
s_add_u32 m0,s57,4096
s_add_u32 s56,s56,4096
R 34,2,18432
M 18,a[104:107],46
L 3
M 18,a[112:115],50
R 38,2,19456
M 18,a[120:123],54
M 22,a[116:119],50
R 42,2,20480
M 22,a[124:127],54
L 3
D 0,56,0
M 22,a[132:135],26
R 46,2,21504
M 22,a[140:143],30
M 18,a[128:131],26
R 50,2,22528
M 18,a[136:139],30
L 3
M 18,a[144:147],34
R 54,2,23552
M 18,a[152:155],38
M 22,a[148:151],34
R 26,2,24576
M 22,a[156:159],38
L 3
D 0,56,1024
M 22,a[164:167],42
R 30,2,25600
M 22,a[172:175],46
M 18,a[160:163],42
R 34,2,26624
M 18,a[168:171],46
L 3
M 18,a[176:179],50
s_waitcnt vmcnt(9)
R 38,2,27648
M 18,a[184:187],54
s_barrier
M 22,a[180:183],50
R 42,2,28672
M 22,a[188:191],54
L 3
D 0,56,2048
M 22,a[196:199],26
s_add_u32 m0,s59,1024
s_add_u32 s56,s55,28672
R 46,2,29696
M 22,a[204:207],30
s_mul_i32 s55,s61,114688
s_add_u32 s55,s55,s60
M 18,a[192:195],26
R 50,2,30720
M 18,a[200:203],30
L 3
M 18,a[208:211],34
R 54,2,31744
M 18,a[216:219],38
M 22,a[212:215],34
R 26,2,32768
M 22,a[220:223],38
L 3
buffer_load_dword v8,s[36:39],s58 offen offset:0 sc1 lds
M 22,a[228:231],42
s_add_u32 m0,s57,28672
R 30,2,33792
M 22,a[236:239],46
D 0,56,0
M 18,a[224:227],42
R 34,2,34816
M 18,a[232:235],46
L 3
M 18,a[240:243],50
R 38,2,35840
M 18,a[248:251],54
M 22,a[244:247],50
R 42,2,36864
M 22,a[252:255],54
L 3
D 0,56,1024
M 22,v[68:71],26
R 46,2,37888
M 22,v[76:79],30
M 18,v[64:67],26
R 50,2,38912
M 18,v[72:75],30
L 3
M 18,v[80:83],34
R 54,2,39936
M 18,v[88:91],38
D 0,56,2048
M 22,v[84:87],34
R 26,2,40960
M 22,v[92:95],38
L 3
M 22,v[100:103],42
R 30,2,41984
M 22,v[108:111],46
M 18,v[96:99],42
R 34,2,43008
M 18,v[104:107],46
L 3
D 0,56,3072
M 18,v[112:115],50
s_add_u32 m0,s57,32768
s_add_u32 s56,s56,4096
R 38,2,44032
M 18,v[120:123],54
s_sub_u32 s57,s84,s57
M 22,v[116:119],50
R 42,2,45056
M 22,v[124:127],54
L 3
M 22,v[132:135],26
R 46,2,46080
M 22,v[140:143],30
D 0,56,0
M 18,v[128:131],26
R 50,2,47104
M 18,v[136:139],30
L 3
M 18,v[144:147],34
R 54,2,48128
M 18,v[152:155],38
M 22,v[148:151],34
R 26,2,49152
M 22,v[156:159],38
L 3
D 0,56,1024
M 22,v[164:167],42
R 30,2,50176
M 22,v[172:175],46
M 18,v[160:163],42
R 34,2,51200
M 18,v[168:171],46
L 3
M 18,v[176:179],50
R 38,2,52224
M 18,v[184:187],54
D 0,56,2048
M 22,v[180:183],50
R 42,2,53248
M 22,v[188:191],54
L 3
M 22,v[196:199],26
R 46,2,54272
M 22,v[204:207],30
)"
R"(M 18,v[192:195],26
R 50,2,55296
M 18,v[200:203],30
L 3
M 18,v[208:211],34
s_waitcnt vmcnt(7)
R 54,2,56320
M 18,v[216:219],38
s_barrier
ds_read_b32 v7,v6 offset:1024
M 22,v[212:215],34
R 26,1,0
M 22,v[220:223],38
L 4
buffer_store_dword v4,v3,s[36:39],s69 offen offset:0 sc0 sc1
M 22,v[228:231],42
s_mov_b32 s69,s68
s_lshr_b32 s68,s62,4
R 30,1,1024
M 22,v[236:239],46
s_add_u32 s68,s68,s48
M 18,v[224:227],42
R 34,1,2048
M 18,v[232:235],46
L 4
L 3
M 18,v[240:243],50
R 38,1,3072
v_cmp_eq_u32 vcc,s53,v7
M 18,v[248:251],54
M 22,v[244:247],50
L 2
s_cmp_eq_u32 vcc_lo,s51
R 42,1,4096
M 22,v[252:255],54
s_cbranch_scc1 Lg4_F0_%=
Lg4_K0_%=:
s_cmp_eq_u32 s51,1
s_cbranch_scc0 Lg4_L0_%=
s_cmp_eq_u32 vcc_lo,-1
s_cbranch_scc1 Lg4_Fa0_%=
s_branch Lg4_La0_%=
Lg4_L0_%=:
v_mfma_f32_16x16x32_bf16 v[14:17],a[4:7],v[26:29],0
s_add_u32 m0,s59,3072
R 46,1,5120
v_lshlrev_b32 v62,16,v58
M 14,a[12:15],30
buffer_load_dword v8,s[36:39],s58 offen offset:0 sc1 lds
v_mfma_f32_16x16x32_bf16 v[10:13],a[0:3],v[26:29],0
s_mov_b32 s66,s65
R 50,1,6144
v_add_f32 v18,v18,v62
M 10,a[8:11],30
L 3
v_and_b32 v63,0xffff0000,v58
v_add_f32 v19,v19,v63
M 10,a[16:19],34
s_lshl_b32 s62,s54,15
R 54,1,7168
v_lshlrev_b32 v62,16,v59
M 10,a[24:27],38
s_cmp_ge_u32 s66,s45
v_add_f32 v20,v20,v62
v_and_b32 v63,0xffff0000,v59
M 14,a[20:23],34
s_cselect_b32 s51,1,-1
R 26,1,8192
v_add_f32 v21,v21,v63
M 14,a[28:31],38
L 3
v_lshlrev_b32 v62,16,v60
v_add_f32 v22,v22,v62
M 14,a[36:39],42
s_add_u32 s65,s47,s62
R 30,1,9216
v_and_b32 v63,0xffff0000,v60
M 14,a[44:47],46
v_add_f32 v23,v23,v63
v_lshlrev_b32 v62,16,v61
M 10,a[32:35],42
R 34,1,10240
v_add_f32 v24,v24,v62
M 10,a[40:43],46
L 3
v_and_b32 v63,0xffff0000,v61
v_add_f32 v25,v25,v63
M 10,a[48:51],50
R 38,1,11264
v_cvt_pk_bf16_f32 v18,v18,v19
M 10,a[56:59],54
buffer_load_dwordx4 v[58:61],v0,s[36:39],s74 offen offset:0 sc1
M 14,a[52:55],50
s_add_u32 s74,s49,s62
R 42,1,12288
v_cvt_pk_bf16_f32 v19,v20,v21
M 14,a[60:63],54
L 3
v_cvt_pk_bf16_f32 v20,v22,v23
v_cvt_pk_bf16_f32 v21,v24,v25
M 14,a[68:71],26
R 46,1,13312
M 14,a[76:79],30
buffer_store_dwordx4 v[18:21],v0,s[36:39],s67 offen offset:0 sc0 sc1
M 10,a[64:67],26
R 50,1,14336
M 10,a[72:75],30
L 3
M 10,a[80:83],34
R 54,1,15360
M 10,a[88:91],38
M 14,a[84:87],34
R 26,1,16384
M 14,a[92:95],38
L 3
M 14,a[100:103],42
R 30,1,17408
M 14,a[108:111],46
M 10,a[96:99],42
R 34,1,18432
M 10,a[104:107],46
L 3
M 10,a[112:115],50
R 38,1,19456
M 10,a[120:123],54
M 14,a[116:119],50
R 42,1,20480
M 14,a[124:127],54
L 3
M 14,a[132:135],26
R 46,1,21504
M 14,a[140:143],30
M 10,a[128:131],26
R 50,1,22528
M 10,a[136:139],30
L 3
M 10,a[144:147],34
R 54,1,23552
M 10,a[152:155],38
M 14,a[148:151],34
R 26,1,24576
M 14,a[156:159],38
L 3
M 14,a[164:167],42
R 30,1,25600
M 14,a[172:175],46
M 10,a[160:163],42
R 34,1,26624
M 10,a[168:171],46
L 3
M 10,a[176:179],50
s_waitcnt vmcnt(2)
R 38,1,27648
M 10,a[184:187],54
s_barrier
ds_read_b32 v7,v6 offset:3072
M 14,a[180:183],50
R 42,1,28672
M 14,a[188:191],54
L 4
M 14,a[196:199],26
R 46,1,29696
M 14,a[204:207],30
M 10,a[192:195],26
R 50,1,30720
M 10,a[200:203],30
L 4
L 3
v_cmp_eq_u32 vcc,s53,v7
M 10,a[208:211],34
R 54,1,31744
M 10,a[216:219],38
M 14,a[212:215],34
s_cmp_eq_u32 vcc_lo,-1
R 26,1,32768
s_cbranch_scc0 Lg4_X0_%=
M 14,a[220:223],38
L 3
s_add_u32 m0,s57,0
s_add_u32 s56,s55,0
M 14,a[228:231],42
s_add_u32 s61,s54,s46
s_cmp_ge_u32 s61,s63
s_cselect_b32 s61,s93,s61
R 30,1,33792
M 14,a[236:239],46
s_mul_i32 s58,s61,2560
D 0,56,0
M 10,a[224:227],42
s_cselect_b32 s46,s88,s46
s_cselect_b32 s63,s89,s63
R 34,1,34816
M 10,a[232:235],46
L 3
s_cselect_b32 s93,s50,s93
s_cselect_b32 s88,8,s88
M 10,a[240:243],50
s_cselect_b32 s89,-1,s89
s_add_u32 s58,s58,109891584
R 38,1,35840
M 10,a[248:251],54
D 0,56,1024
M 14,a[244:247],50
s_mov_b32 s54,s61
R 42,1,36864
M 14,a[252:255],54
L 3
M 14,v[68:71],26
R 46,1,37888
M 14,v[76:79],30
D 0,56,2048
M 10,v[64:67],26
R 50,1,38912
M 10,v[72:75],30
L 3
M 10,v[80:83],34
R 54,1,39936
M 10,v[88:91],38
D 0,56,3072
M 14,v[84:87],34
s_add_u32 m0,s57,4096
s_add_u32 s56,s56,4096
R 26,1,40960
M 14,v[92:95],38
L 3
M 14,v[100:103],42
R 30,1,41984
M 14,v[108:111],46
D 0,56,0
M 10,v[96:99],42
R 34,1,43008
M 10,v[104:107],46
L 3
M 10,v[112:115],50
R 38,1,44032
M 10,v[120:123],54
D 0,56,1024
M 14,v[116:119],50
R 42,1,45056
M 14,v[124:127],54
L 3
M 14,v[132:135],26
R 46,1,46080
M 14,v[140:143],30
D 0,56,2048
M 10,v[128:131],26
s_add_u32 m0,s59,0
s_add_u32 s56,s55,28672
R 50,1,47104
M 10,v[136:139],30
L 3
buffer_load_dword v8,s[36:39],s58 offen offset:0 sc1 lds
M 10,v[144:147],34
s_add_u32 m0,s57,28672
s_mul_i32 s55,s61,114688
R 54,1,48128
M 10,v[152:155],38
s_add_u32 s55,s55,s60
M 14,v[148:151],34
R 26,1,49152
M 14,v[156:159],38
L 3
D 0,56,0
M 14,v[164:167],42
R 30,1,50176
M 14,v[172:175],46
M 10,v[160:163],42
R 34,1,51200
M 10,v[168:171],46
L 3
D 0,56,1024
M 10,v[176:179],50
R 38,1,52224
M 10,v[184:187],54
D 0,56,2048
M 14,v[180:183],50
R 42,1,53248
M 14,v[188:191],54
L 3
M 14,v[196:199],26
R 46,1,54272
M 14,v[204:207],30
D 0,56,3072
M 10,v[192:195],26
s_add_u32 m0,s57,32768
s_add_u32 s56,s56,4096
R 50,1,55296
M 10,v[200:203],30
L 3
s_sub_u32 s57,s84,s57
M 10,v[208:211],34
s_waitcnt vmcnt(4)
R 54,1,56320
M 10,v[216:219],38
s_barrier
ds_read_b32 v7,v6 offset:0
M 14,v[212:215],34
R 26,2,0
M 14,v[220:223],38
L 4
buffer_store_dword v4,v3,s[36:39],s69 offen offset:0 sc0 sc1
M 14,v[228:231],42
s_mov_b32 s69,s68
s_lshr_b32 s68,s62,4
R 30,2,1024
M 14,v[236:239],46
D 0,56,0
M 10,v[224:227],42
s_add_u32 s68,s68,s48
R 34,2,2048
M 10,v[232:235],46
L 4
D 0,56,1024
M 10,v[240:243],50
L 3
R 38,2,3072
v_cmp_eq_u32 vcc,s53,v7
M 10,v[248:251],54
D 0,56,2048
M 14,v[244:247],50
L 2
s_cmp_eq_u32 vcc_lo,s51
R 42,2,4096
M 14,v[252:255],54
buffer_store_dword v4,v3,s[36:39],s71 offen offset:0 sc0 sc1
s_cbranch_scc1 Lg4_F1_%=
s_branch Lg4_K1_%=
Lg4_X0_%=:
M 14,a[220:223],38
L 3
s_add_u32 m0,s59,0
M 14,a[228:231],42
R 30,1,33792
M 14,a[236:239],46
buffer_load_dword v8,s[36:39],s58 offen offset:0 sc1 lds
M 10,a[224:227],42
R 34,1,34816
M 10,a[232:235],46
L 3
M 10,a[240:243],50
R 38,1,35840
M 10,a[248:251],54
M 14,a[244:247],50
R 42,1,36864
M 14,a[252:255],54
L 3
M 14,v[68:71],26
R 46,1,37888
M 14,v[76:79],30
M 10,v[64:67],26
R 50,1,38912
M 10,v[72:75],30
L 3
M 10,v[80:83],34
R 54,1,39936
M 10,v[88:91],38
M 14,v[84:87],34
R 26,1,40960
M 14,v[92:95],38
L 3
M 14,v[100:103],42
R 30,1,41984
M 14,v[108:111],46
M 10,v[96:99],42
R 34,1,43008
M 10,v[104:107],46
L 3
M 10,v[112:115],50
R 38,1,44032
M 10,v[120:123],54
M 14,v[116:119],50
R 42,1,45056
M 14,v[124:127],54
L 3
M 14,v[132:135],26
R 46,1,46080
M 14,v[140:143],30
M 10,v[128:131],26
R 50,1,47104
M 10,v[136:139],30
L 3
M 10,v[144:147],34
R 54,1,48128
M 10,v[152:155],38
M 14,v[148:151],34
R 26,1,49152
M 14,v[156:159],38
L 3
M 14,v[164:167],42
R 30,1,50176
M 14,v[172:175],46
M 10,v[160:163],42
R 34,1,51200
M 10,v[168:171],46
L 3
M 10,v[176:179],50
R 38,1,52224
M 10,v[184:187],54
M 14,v[180:183],50
R 42,1,53248
M 14,v[188:191],54
L 3
M 14,v[196:199],26
R 46,1,54272
M 14,v[204:207],30
M 10,v[192:195],26
R 50,1,55296
M 10,v[200:203],30
L 3
M 10,v[208:211],34
s_waitcnt vmcnt(0)
R 54,1,56320
M 10,v[216:219],38
s_barrier
buffer_store_dword v4,v3,s[36:39],s69 offen offset:0 sc0 sc1
M 14,v[212:215],34
s_mov_b32 s69,s68
s_lshr_b32 s68,s62,4
ds_read_b32 v7,v6 offset:0
M 14,v[220:223],38
L 3
s_add_u32 s68,s68,s48
M 14,v[228:231],42
M 14,v[236:239],46
M 10,v[224:227],42
M 10,v[232:235],46
L 1
M 10,v[240:243],50
L 0
v_cmp_eq_u32 vcc,s53,v7
M 10,v[248:251],54
M 14,v[244:247],50
M 14,v[252:255],54
s_cmp_eq_u32 s51,1
s_cbranch_scc1 Lg4_pa0_%=
s_mov_b32 s80,1
s_cmp_eq_u32 vcc_lo,-1
s_cbranch_scc0 Lg4_xn0_%=
s_add_u32 m0,s57,0
s_add_u32 s56,s55,0
D 0,56,0
v_lshlrev_b32 v62,16,v58
D 0,56,1024
v_add_f32 v10,v10,v62
D 0,56,2048
v_and_b32 v63,0xffff0000,v58
D 0,56,3072
v_add_f32 v11,v11,v63
s_add_u32 m0,s57,4096
s_add_u32 s56,s56,4096
D 0,56,0
v_lshlrev_b32 v62,16,v59
D 0,56,1024
v_add_f32 v12,v12,v62
D 0,56,2048
v_and_b32 v63,0xffff0000,v59
s_add_u32 s61,s54,s46
s_cmp_ge_u32 s61,s63
s_cselect_b32 s61,s93,s61
s_cselect_b32 s46,s88,s46
s_cselect_b32 s63,s89,s63
s_cselect_b32 s93,s50,s93
s_cselect_b32 s88,8,s88
s_cselect_b32 s89,-1,s89
s_mul_i32 s58,s61,2560
s_add_u32 s58,s58,109891584
s_add_u32 m0,s57,28672
s_add_u32 s56,s55,28672
D 0,56,0
v_add_f32 v13,v13,v63
D 0,56,1024
v_lshlrev_b32 v62,16,v60
D 0,56,2048
v_add_f32 v14,v14,v62
D 0,56,3072
v_and_b32 v63,0xffff0000,v60
s_add_u32 m0,s57,32768
s_add_u32 s56,s56,4096
D 0,56,0
v_add_f32 v15,v15,v63
D 0,56,1024
v_lshlrev_b32 v62,16,v61
D 0,56,2048
v_add_f32 v16,v16,v62
s_mul_i32 s55,s61,114688
s_add_u32 s55,s55,s60
s_sub_u32 s57,s84,s57
s_mov_b32 s54,s61
v_and_b32 v63,0xffff0000,v61
v_add_f32 v17,v17,v63
v_cvt_pk_bf16_f32 v10,v10,v11
v_cvt_pk_bf16_f32 v11,v12,v13
v_cvt_pk_bf16_f32 v12,v14,v15
v_cvt_pk_bf16_f32 v13,v16,v17
buffer_store_dwordx4 v[10:13],v0,s[36:39],s66 offen offset:0 sc0 sc1
s_mov_b32 s72,s69
s_mov_b32 s69,0x7fff0000
s_mov_b32 s66,0x7fff0000
s_mov_b32 s76,0x7fff0000
s_branch Lg4_gox_%=
)"
R"(Lg4_xn0_%=:
s_cmp_ge_u32 s54,256
s_cbranch_scc1 Lg4_fin0_%=
s_add_u32 m0,s59,4096
s_nop 0
buffer_load_dword v8,s[36:39],s58 offen offset:0 sc1 lds
buffer_store_dword v4,v3,s[36:39],s71 offen offset:0 sc0 sc1
s_add_u32 m0,s59,5120
s_nop 0
buffer_load_dword v8,s[36:39],s58 offen offset:0 sc1 lds
buffer_store_dword v4,v3,s[36:39],s71 offen offset:0 sc0 sc1
s_mov_b32 s83,0
v_lshlrev_b32 v62,16,v58
v_add_f32 v10,v10,v62
v_and_b32 v63,0xffff0000,v58
v_add_f32 v11,v11,v63
v_lshlrev_b32 v62,16,v59
v_add_f32 v12,v12,v62
v_and_b32 v63,0xffff0000,v59
v_add_f32 v13,v13,v63
v_lshlrev_b32 v62,16,v60
v_add_f32 v14,v14,v62
v_and_b32 v63,0xffff0000,v60
v_add_f32 v15,v15,v63
v_lshlrev_b32 v62,16,v61
v_add_f32 v16,v16,v62
v_and_b32 v63,0xffff0000,v61
v_add_f32 v17,v17,v63
v_cvt_pk_bf16_f32 v10,v10,v11
v_cvt_pk_bf16_f32 v11,v12,v13
v_cvt_pk_bf16_f32 v12,v14,v15
v_cvt_pk_bf16_f32 v13,v16,v17
buffer_store_dwordx4 v[10:13],v0,s[36:39],s66 offen offset:0 sc0 sc1
s_mov_b32 s72,s69
s_mov_b32 s69,0x7fff0000
s_mov_b32 s66,0x7fff0000
s_mov_b32 s76,0x7fff0000
s_branch Lg4_poll_%=
Lg4_fin0_%=:
v_lshlrev_b32 v62,16,v58
v_add_f32 v10,v10,v62
v_and_b32 v63,0xffff0000,v58
v_add_f32 v11,v11,v63
v_lshlrev_b32 v62,16,v59
v_add_f32 v12,v12,v62
v_and_b32 v63,0xffff0000,v59
v_add_f32 v13,v13,v63
v_lshlrev_b32 v62,16,v60
v_add_f32 v14,v14,v62
v_and_b32 v63,0xffff0000,v60
v_add_f32 v15,v15,v63
v_lshlrev_b32 v62,16,v61
v_add_f32 v16,v16,v62
v_and_b32 v63,0xffff0000,v61
v_add_f32 v17,v17,v63
v_cvt_pk_bf16_f32 v10,v10,v11
v_cvt_pk_bf16_f32 v11,v12,v13
v_cvt_pk_bf16_f32 v12,v14,v15
v_cvt_pk_bf16_f32 v13,v16,v17
buffer_store_dwordx4 v[10:13],v0,s[36:39],s66 offen offset:0 sc0 sc1
s_mov_b32 s72,s69
s_mov_b32 s69,0x7fff0000
s_mov_b32 s66,0x7fff0000
s_mov_b32 s76,0x7fff0000
s_branch Lg4_done_%=
Lg4_K1_%=:
s_cmp_eq_u32 s51,1
s_cbranch_scc0 Lg4_L1_%=
s_cmp_eq_u32 vcc_lo,-1
s_cbranch_scc1 Lg4_Fa1_%=
s_branch Lg4_La1_%=
Lg4_L1_%=:
v_mfma_f32_16x16x32_bf16 v[22:25],a[4:7],v[26:29],0
s_add_u32 m0,s59,3072
R 46,2,5120
v_lshlrev_b32 v62,16,v58
M 22,a[12:15],30
buffer_load_dword v8,s[36:39],s58 offen offset:0 sc1 lds
v_mfma_f32_16x16x32_bf16 v[18:21],a[0:3],v[26:29],0
s_mov_b32 s67,s65
R 50,2,6144
v_add_f32 v10,v10,v62
M 18,a[8:11],30
L 3
v_and_b32 v63,0xffff0000,v58
v_add_f32 v11,v11,v63
M 18,a[16:19],34
s_lshl_b32 s62,s54,15
R 54,2,7168
v_lshlrev_b32 v62,16,v59
M 18,a[24:27],38
s_cmp_ge_u32 s67,s45
v_add_f32 v12,v12,v62
v_and_b32 v63,0xffff0000,v59
M 22,a[20:23],34
s_cselect_b32 s51,1,-1
R 26,2,8192
v_add_f32 v13,v13,v63
M 22,a[28:31],38
L 3
v_lshlrev_b32 v62,16,v60
v_add_f32 v14,v14,v62
M 22,a[36:39],42
s_add_u32 s65,s47,s62
R 30,2,9216
v_and_b32 v63,0xffff0000,v60
M 22,a[44:47],46
v_add_f32 v15,v15,v63
v_lshlrev_b32 v62,16,v61
M 18,a[32:35],42
R 34,2,10240
v_add_f32 v16,v16,v62
M 18,a[40:43],46
L 3
v_and_b32 v63,0xffff0000,v61
v_add_f32 v17,v17,v63
M 18,a[48:51],50
R 38,2,11264
v_cvt_pk_bf16_f32 v10,v10,v11
M 18,a[56:59],54
buffer_load_dwordx4 v[58:61],v0,s[36:39],s74 offen offset:0 sc1
M 22,a[52:55],50
s_add_u32 s74,s49,s62
R 42,2,12288
v_cvt_pk_bf16_f32 v11,v12,v13
M 22,a[60:63],54
L 3
v_cvt_pk_bf16_f32 v12,v14,v15
v_cvt_pk_bf16_f32 v13,v16,v17
M 22,a[68:71],26
R 46,2,13312
M 22,a[76:79],30
buffer_store_dwordx4 v[10:13],v0,s[36:39],s66 offen offset:0 sc0 sc1
M 18,a[64:67],26
R 50,2,14336
M 18,a[72:75],30
L 3
M 18,a[80:83],34
R 54,2,15360
M 18,a[88:91],38
M 22,a[84:87],34
R 26,2,16384
M 22,a[92:95],38
L 3
M 22,a[100:103],42
R 30,2,17408
M 22,a[108:111],46
M 18,a[96:99],42
R 34,2,18432
M 18,a[104:107],46
L 3
M 18,a[112:115],50
R 38,2,19456
M 18,a[120:123],54
M 22,a[116:119],50
R 42,2,20480
M 22,a[124:127],54
L 3
M 22,a[132:135],26
R 46,2,21504
M 22,a[140:143],30
M 18,a[128:131],26
R 50,2,22528
M 18,a[136:139],30
L 3
M 18,a[144:147],34
R 54,2,23552
M 18,a[152:155],38
M 22,a[148:151],34
R 26,2,24576
M 22,a[156:159],38
L 3
M 22,a[164:167],42
R 30,2,25600
M 22,a[172:175],46
M 18,a[160:163],42
R 34,2,26624
M 18,a[168:171],46
L 3
M 18,a[176:179],50
s_waitcnt vmcnt(2)
R 38,2,27648
M 18,a[184:187],54
s_barrier
ds_read_b32 v7,v6 offset:3072
M 22,a[180:183],50
R 42,2,28672
M 22,a[188:191],54
L 4
M 22,a[196:199],26
R 46,2,29696
M 22,a[204:207],30
M 18,a[192:195],26
R 50,2,30720
M 18,a[200:203],30
L 4
L 3
v_cmp_eq_u32 vcc,s53,v7
M 18,a[208:211],34
R 54,2,31744
M 18,a[216:219],38
M 22,a[212:215],34
s_cmp_eq_u32 vcc_lo,-1
R 26,2,32768
s_cbranch_scc0 Lg4_X1_%=
M 22,a[220:223],38
L 3
s_add_u32 m0,s57,0
s_add_u32 s56,s55,0
M 22,a[228:231],42
s_add_u32 s61,s54,s46
s_cmp_ge_u32 s61,s63
s_cselect_b32 s61,s93,s61
R 30,2,33792
M 22,a[236:239],46
s_mul_i32 s58,s61,2560
D 0,56,0
M 18,a[224:227],42
s_cselect_b32 s46,s88,s46
s_cselect_b32 s63,s89,s63
R 34,2,34816
M 18,a[232:235],46
L 3
s_cselect_b32 s93,s50,s93
s_cselect_b32 s88,8,s88
M 18,a[240:243],50
s_cselect_b32 s89,-1,s89
s_add_u32 s58,s58,109891584
R 38,2,35840
M 18,a[248:251],54
D 0,56,1024
M 22,a[244:247],50
s_mov_b32 s54,s61
R 42,2,36864
M 22,a[252:255],54
L 3
M 22,v[68:71],26
R 46,2,37888
M 22,v[76:79],30
D 0,56,2048
M 18,v[64:67],26
R 50,2,38912
M 18,v[72:75],30
L 3
M 18,v[80:83],34
R 54,2,39936
M 18,v[88:91],38
D 0,56,3072
M 22,v[84:87],34
s_add_u32 m0,s57,4096
s_add_u32 s56,s56,4096
R 26,2,40960
M 22,v[92:95],38
L 3
M 22,v[100:103],42
R 30,2,41984
M 22,v[108:111],46
D 0,56,0
M 18,v[96:99],42
R 34,2,43008
M 18,v[104:107],46
L 3
M 18,v[112:115],50
R 38,2,44032
M 18,v[120:123],54
D 0,56,1024
M 22,v[116:119],50
R 42,2,45056
M 22,v[124:127],54
L 3
M 22,v[132:135],26
R 46,2,46080
M 22,v[140:143],30
D 0,56,2048
M 18,v[128:131],26
s_add_u32 m0,s59,1024
s_add_u32 s56,s55,28672
R 50,2,47104
M 18,v[136:139],30
L 3
buffer_load_dword v8,s[36:39],s58 offen offset:0 sc1 lds
M 18,v[144:147],34
s_add_u32 m0,s57,28672
s_mul_i32 s55,s61,114688
R 54,2,48128
M 18,v[152:155],38
s_add_u32 s55,s55,s60
M 22,v[148:151],34
R 26,2,49152
M 22,v[156:159],38
L 3
D 0,56,0
M 22,v[164:167],42
R 30,2,50176
M 22,v[172:175],46
M 18,v[160:163],42
R 34,2,51200
M 18,v[168:171],46
L 3
D 0,56,1024
M 18,v[176:179],50
R 38,2,52224
M 18,v[184:187],54
D 0,56,2048
M 22,v[180:183],50
R 42,2,53248
M 22,v[188:191],54
L 3
M 22,v[196:199],26
R 46,2,54272
M 22,v[204:207],30
D 0,56,3072
M 18,v[192:195],26
s_add_u32 m0,s57,32768
s_add_u32 s56,s56,4096
R 50,2,55296
M 18,v[200:203],30
L 3
s_sub_u32 s57,s84,s57
M 18,v[208:211],34
s_waitcnt vmcnt(4)
R 54,2,56320
M 18,v[216:219],38
s_barrier
ds_read_b32 v7,v6 offset:1024
M 22,v[212:215],34
R 26,1,0
M 22,v[220:223],38
L 4
buffer_store_dword v4,v3,s[36:39],s69 offen offset:0 sc0 sc1
M 22,v[228:231],42
s_mov_b32 s69,s68
s_lshr_b32 s68,s62,4
R 30,1,1024
M 22,v[236:239],46
D 0,56,0
M 18,v[224:227],42
s_add_u32 s68,s68,s48
R 34,1,2048
M 18,v[232:235],46
L 4
D 0,56,1024
M 18,v[240:243],50
L 3
R 38,1,3072
v_cmp_eq_u32 vcc,s53,v7
M 18,v[248:251],54
D 0,56,2048
M 22,v[244:247],50
L 2
s_cmp_eq_u32 vcc_lo,s51
R 42,1,4096
M 22,v[252:255],54
buffer_store_dword v4,v3,s[36:39],s71 offen offset:0 sc0 sc1
s_cbranch_scc1 Lg4_F0_%=
s_branch Lg4_K0_%=
Lg4_X1_%=:
M 22,a[220:223],38
L 3
s_add_u32 m0,s59,1024
M 22,a[228:231],42
R 30,2,33792
M 22,a[236:239],46
buffer_load_dword v8,s[36:39],s58 offen offset:0 sc1 lds
M 18,a[224:227],42
R 34,2,34816
M 18,a[232:235],46
L 3
M 18,a[240:243],50
R 38,2,35840
M 18,a[248:251],54
M 22,a[244:247],50
R 42,2,36864
M 22,a[252:255],54
L 3
M 22,v[68:71],26
R 46,2,37888
M 22,v[76:79],30
M 18,v[64:67],26
R 50,2,38912
M 18,v[72:75],30
L 3
M 18,v[80:83],34
R 54,2,39936
M 18,v[88:91],38
M 22,v[84:87],34
R 26,2,40960
M 22,v[92:95],38
L 3
M 22,v[100:103],42
R 30,2,41984
M 22,v[108:111],46
M 18,v[96:99],42
R 34,2,43008
M 18,v[104:107],46
L 3
M 18,v[112:115],50
R 38,2,44032
M 18,v[120:123],54
M 22,v[116:119],50
R 42,2,45056
M 22,v[124:127],54
L 3
M 22,v[132:135],26
R 46,2,46080
M 22,v[140:143],30
M 18,v[128:131],26
R 50,2,47104
M 18,v[136:139],30
L 3
M 18,v[144:147],34
R 54,2,48128
M 18,v[152:155],38
M 22,v[148:151],34
R 26,2,49152
M 22,v[156:159],38
L 3
M 22,v[164:167],42
R 30,2,50176
M 22,v[172:175],46
M 18,v[160:163],42
R 34,2,51200
M 18,v[168:171],46
L 3
M 18,v[176:179],50
R 38,2,52224
M 18,v[184:187],54
M 22,v[180:183],50
R 42,2,53248
M 22,v[188:191],54
L 3
M 22,v[196:199],26
R 46,2,54272
M 22,v[204:207],30
M 18,v[192:195],26
R 50,2,55296
M 18,v[200:203],30
L 3
M 18,v[208:211],34
s_waitcnt vmcnt(0)
R 54,2,56320
M 18,v[216:219],38
s_barrier
buffer_store_dword v4,v3,s[36:39],s69 offen offset:0 sc0 sc1
M 22,v[212:215],34
s_mov_b32 s69,s68
s_lshr_b32 s68,s62,4
ds_read_b32 v7,v6 offset:1024
M 22,v[220:223],38
L 3
s_add_u32 s68,s68,s48
M 22,v[228:231],42
M 22,v[236:239],46
M 18,v[224:227],42
M 18,v[232:235],46
L 1
M 18,v[240:243],50
L 0
v_cmp_eq_u32 vcc,s53,v7
M 18,v[248:251],54
M 22,v[244:247],50
M 22,v[252:255],54
s_cmp_eq_u32 s51,1
s_cbranch_scc1 Lg4_pa1_%=
s_mov_b32 s80,0
s_cmp_eq_u32 vcc_lo,-1
s_cbranch_scc0 Lg4_xn1_%=
s_add_u32 m0,s57,0
s_add_u32 s56,s55,0
D 0,56,0
v_lshlrev_b32 v62,16,v58
D 0,56,1024
v_add_f32 v18,v18,v62
D 0,56,2048
v_and_b32 v63,0xffff0000,v58
D 0,56,3072
v_add_f32 v19,v19,v63
s_add_u32 m0,s57,4096
s_add_u32 s56,s56,4096
D 0,56,0
v_lshlrev_b32 v62,16,v59
D 0,56,1024
v_add_f32 v20,v20,v62
D 0,56,2048
v_and_b32 v63,0xffff0000,v59
s_add_u32 s61,s54,s46
s_cmp_ge_u32 s61,s63
s_cselect_b32 s61,s93,s61
s_cselect_b32 s46,s88,s46
s_cselect_b32 s63,s89,s63
s_cselect_b32 s93,s50,s93
s_cselect_b32 s88,8,s88
s_cselect_b32 s89,-1,s89
s_mul_i32 s58,s61,2560
s_add_u32 s58,s58,109891584
s_add_u32 m0,s57,28672
s_add_u32 s56,s55,28672
D 0,56,0
v_add_f32 v21,v21,v63
D 0,56,1024
)"
R"(v_lshlrev_b32 v62,16,v60
D 0,56,2048
v_add_f32 v22,v22,v62
D 0,56,3072
v_and_b32 v63,0xffff0000,v60
s_add_u32 m0,s57,32768
s_add_u32 s56,s56,4096
D 0,56,0
v_add_f32 v23,v23,v63
D 0,56,1024
v_lshlrev_b32 v62,16,v61
D 0,56,2048
v_add_f32 v24,v24,v62
s_mul_i32 s55,s61,114688
s_add_u32 s55,s55,s60
s_sub_u32 s57,s84,s57
s_mov_b32 s54,s61
v_and_b32 v63,0xffff0000,v61
v_add_f32 v25,v25,v63
v_cvt_pk_bf16_f32 v18,v18,v19
v_cvt_pk_bf16_f32 v19,v20,v21
v_cvt_pk_bf16_f32 v20,v22,v23
v_cvt_pk_bf16_f32 v21,v24,v25
buffer_store_dwordx4 v[18:21],v0,s[36:39],s67 offen offset:0 sc0 sc1
s_mov_b32 s72,s69
s_mov_b32 s69,0x7fff0000
s_mov_b32 s67,0x7fff0000
s_mov_b32 s77,0x7fff0000
s_branch Lg4_gox_%=
Lg4_xn1_%=:
s_cmp_ge_u32 s54,256
s_cbranch_scc1 Lg4_fin1_%=
s_add_u32 m0,s59,4096
s_nop 0
buffer_load_dword v8,s[36:39],s58 offen offset:0 sc1 lds
buffer_store_dword v4,v3,s[36:39],s71 offen offset:0 sc0 sc1
s_add_u32 m0,s59,5120
s_nop 0
buffer_load_dword v8,s[36:39],s58 offen offset:0 sc1 lds
buffer_store_dword v4,v3,s[36:39],s71 offen offset:0 sc0 sc1
s_mov_b32 s83,0
v_lshlrev_b32 v62,16,v58
v_add_f32 v18,v18,v62
v_and_b32 v63,0xffff0000,v58
v_add_f32 v19,v19,v63
v_lshlrev_b32 v62,16,v59
v_add_f32 v20,v20,v62
v_and_b32 v63,0xffff0000,v59
v_add_f32 v21,v21,v63
v_lshlrev_b32 v62,16,v60
v_add_f32 v22,v22,v62
v_and_b32 v63,0xffff0000,v60
v_add_f32 v23,v23,v63
v_lshlrev_b32 v62,16,v61
v_add_f32 v24,v24,v62
v_and_b32 v63,0xffff0000,v61
v_add_f32 v25,v25,v63
v_cvt_pk_bf16_f32 v18,v18,v19
v_cvt_pk_bf16_f32 v19,v20,v21
v_cvt_pk_bf16_f32 v20,v22,v23
v_cvt_pk_bf16_f32 v21,v24,v25
buffer_store_dwordx4 v[18:21],v0,s[36:39],s67 offen offset:0 sc0 sc1
s_mov_b32 s72,s69
s_mov_b32 s69,0x7fff0000
s_mov_b32 s67,0x7fff0000
s_mov_b32 s77,0x7fff0000
s_branch Lg4_poll_%=
Lg4_fin1_%=:
v_lshlrev_b32 v62,16,v58
v_add_f32 v18,v18,v62
v_and_b32 v63,0xffff0000,v58
v_add_f32 v19,v19,v63
v_lshlrev_b32 v62,16,v59
v_add_f32 v20,v20,v62
v_and_b32 v63,0xffff0000,v59
v_add_f32 v21,v21,v63
v_lshlrev_b32 v62,16,v60
v_add_f32 v22,v22,v62
v_and_b32 v63,0xffff0000,v60
v_add_f32 v23,v23,v63
v_lshlrev_b32 v62,16,v61
v_add_f32 v24,v24,v62
v_and_b32 v63,0xffff0000,v61
v_add_f32 v25,v25,v63
v_cvt_pk_bf16_f32 v18,v18,v19
v_cvt_pk_bf16_f32 v19,v20,v21
v_cvt_pk_bf16_f32 v20,v22,v23
v_cvt_pk_bf16_f32 v21,v24,v25
buffer_store_dwordx4 v[18:21],v0,s[36:39],s67 offen offset:0 sc0 sc1
s_mov_b32 s72,s69
s_mov_b32 s69,0x7fff0000
s_mov_b32 s67,0x7fff0000
s_mov_b32 s77,0x7fff0000
s_branch Lg4_done_%=
Lg4_Fa0_%=:
s_nop 0
v_mfma_f32_16x16x32_bf16 v[14:17],a[4:7],v[26:29],0
s_add_u32 m0,s57,0
R 46,1,5120
v_lshlrev_b32 v62,16,v58
M 14,a[12:15],30
s_add_u32 s56,s55,0
v_add_f32 v18,v18,v62
v_and_b32 v63,0xffff0000,v58
v_mfma_f32_16x16x32_bf16 v[10:13],a[0:3],v[26:29],0
s_add_u32 s61,s54,s46
R 50,1,6144
v_add_f32 v19,v19,v63
M 10,a[8:11],30
L 3
D 0,56,0
M 10,a[16:19],34
s_cmp_ge_u32 s61,s63
R 54,1,7168
v_lshlrev_b32 v62,16,v59
M 10,a[24:27],38
s_cselect_b32 s61,s93,s61
v_add_f32 v20,v20,v62
v_and_b32 v63,0xffff0000,v59
M 14,a[20:23],34
s_cselect_b32 s46,s88,s46
R 26,1,8192
v_add_f32 v21,v21,v63
M 14,a[28:31],38
L 3
v_lshlrev_b32 v62,16,v60
v_add_f32 v22,v22,v62
M 14,a[36:39],42
s_cselect_b32 s63,s89,s63
R 30,1,9216
v_and_b32 v63,0xffff0000,v60
M 14,a[44:47],46
D 0,56,1024
M 10,a[32:35],42
s_cselect_b32 s93,s50,s93
R 34,1,10240
v_add_f32 v23,v23,v63
M 10,a[40:43],46
L 3
v_lshlrev_b32 v62,16,v61
v_add_f32 v24,v24,v62
M 10,a[48:51],50
s_cselect_b32 s88,8,s88
R 38,1,11264
v_and_b32 v63,0xffff0000,v61
M 10,a[56:59],54
buffer_load_dwordx4 v[58:61],v0,s[36:39],s74 offen offset:0 sc1
M 14,a[52:55],50
s_cselect_b32 s89,-1,s89
R 42,1,12288
v_add_f32 v25,v25,v63
M 14,a[60:63],54
L 3
v_cvt_pk_bf16_f32 v18,v18,v19
v_cvt_pk_bf16_f32 v19,v20,v21
M 14,a[68:71],26
s_mul_i32 s58,s61,2560
R 46,1,13312
v_cvt_pk_bf16_f32 v20,v22,v23
M 14,a[76:79],30
D 0,56,2048
M 10,a[64:67],26
s_add_u32 s58,s58,109891584
R 50,1,14336
v_cvt_pk_bf16_f32 v21,v24,v25
M 10,a[72:75],30
L 3
v_lshrrev_b32 v62,8,v0
v_lshl_add_u32 v62,v62,5,v0
M 10,a[80:83],34
s_sub_u32 s75,s67,s85
R 54,1,15360
v_add_u32 v62,s73,v62
M 10,a[88:91],38
s_mov_b64 s[40:41],s[86:87]
ds_write_b128 v62,v[18:21] offset:0
M 14,a[84:87],34
s_mov_b32 s42,4194304
R 26,1,16384
ds_read2_b32 v[22:23],v5 offset0:0 offset1:16
M 14,a[92:95],38
L 5
s_mov_b32 s43,0x00020000
ds_read2_b32 v[24:25],v5 offset0:32 offset1:48
M 14,a[100:103],42
s_add_u32 s64,s75,0
s_lshl_b32 s62,s54,15
R 30,1,17408
M 14,a[108:111],46
D 0,56,3072
M 10,a[96:99],42
s_add_u32 m0,s57,4096
s_add_u32 s56,s56,4096
R 34,1,18432
M 10,a[104:107],46
L 6
s_mov_b32 s66,s65
s_add_u32 s74,s49,s62
M 10,a[112:115],50
s_add_u32 s65,s47,s62
s_mov_b32 s54,s61
R 38,1,19456
M 10,a[120:123],54
L 3
M 14,a[116:119],50
R 42,1,20480
M 14,a[124:127],54
L 3
D 0,56,0
M 14,a[132:135],26
R 46,1,21504
M 14,a[140:143],30
M 10,a[128:131],26
R 50,1,22528
M 10,a[136:139],30
L 3
M 10,a[144:147],34
R 54,1,23552
M 10,a[152:155],38
M 14,a[148:151],34
R 26,1,24576
M 14,a[156:159],38
L 3
D 0,56,1024
M 14,a[164:167],42
R 30,1,25600
M 14,a[172:175],46
M 10,a[160:163],42
R 34,1,26624
M 10,a[168:171],46
L 3
M 10,a[176:179],50
s_waitcnt vmcnt(8)
R 38,1,27648
M 10,a[184:187],54
s_barrier
M 14,a[180:183],50
R 42,1,28672
M 14,a[188:191],54
L 3
D 0,56,2048
M 14,a[196:199],26
s_add_u32 m0,s59,0
s_add_u32 s56,s55,28672
R 46,1,29696
M 14,a[204:207],30
s_mul_i32 s55,s61,114688
s_add_u32 s55,s55,s60
M 10,a[192:195],26
R 50,1,30720
M 10,a[200:203],30
L 3
M 10,a[208:211],34
R 54,1,31744
M 10,a[216:219],38
M 14,a[212:215],34
R 26,1,32768
M 14,a[220:223],38
L 3
buffer_load_dword v8,s[36:39],s58 offen offset:0 sc1 lds
M 14,a[228:231],42
s_add_u32 m0,s57,28672
R 30,1,33792
M 14,a[236:239],46
D 0,56,0
M 10,a[224:227],42
R 34,1,34816
M 10,a[232:235],46
L 3
M 10,a[240:243],50
R 38,1,35840
M 10,a[248:251],54
M 14,a[244:247],50
R 42,1,36864
M 14,a[252:255],54
L 3
D 0,56,1024
M 14,v[68:71],26
R 46,1,37888
M 14,v[76:79],30
M 10,v[64:67],26
R 50,1,38912
M 10,v[72:75],30
L 3
M 10,v[80:83],34
R 54,1,39936
M 10,v[88:91],38
D 0,56,2048
M 14,v[84:87],34
R 26,1,40960
M 14,v[92:95],38
L 3
M 14,v[100:103],42
R 30,1,41984
M 14,v[108:111],46
M 10,v[96:99],42
R 34,1,43008
M 10,v[104:107],46
L 3
D 0,56,3072
M 10,v[112:115],50
s_add_u32 m0,s57,32768
s_add_u32 s56,s56,4096
R 38,1,44032
M 10,v[120:123],54
s_sub_u32 s57,s84,s57
M 14,v[116:119],50
R 42,1,45056
M 14,v[124:127],54
L 3
M 14,v[132:135],26
R 46,1,46080
M 14,v[140:143],30
D 0,56,0
M 10,v[128:131],26
R 50,1,47104
M 10,v[136:139],30
L 3
M 10,v[144:147],34
R 54,1,48128
M 10,v[152:155],38
M 14,v[148:151],34
R 26,1,49152
M 14,v[156:159],38
L 3
D 0,56,1024
M 14,v[164:167],42
R 30,1,50176
M 14,v[172:175],46
M 10,v[160:163],42
R 34,1,51200
M 10,v[168:171],46
L 3
M 10,v[176:179],50
R 38,1,52224
M 10,v[184:187],54
D 0,56,2048
M 14,v[180:183],50
R 42,1,53248
M 14,v[188:191],54
L 3
M 14,v[196:199],26
R 46,1,54272
M 14,v[204:207],30
M 10,v[192:195],26
R 50,1,55296
M 10,v[200:203],30
L 3
M 10,v[208:211],34
s_waitcnt vmcnt(7)
R 54,1,56320
M 10,v[216:219],38
s_barrier
ds_read_b32 v7,v6 offset:0
M 14,v[212:215],34
R 26,2,0
M 14,v[220:223],38
L 4
M 14,v[228:231],42
R 30,2,1024
M 14,v[236:239],46
buffer_atomic_pk_add_bf16 v22,v9,s[40:43],s64 offen sc1
M 10,v[224:227],42
s_add_u32 s64,s75,8192
R 34,2,2048
M 10,v[232:235],46
L 4
buffer_atomic_pk_add_bf16 v23,v9,s[40:43],s64 offen sc1
M 10,v[240:243],50
s_add_u32 s64,s75,16384
L 3
R 38,2,3072
v_cmp_eq_u32 vcc,s53,v7
M 10,v[248:251],54
buffer_atomic_pk_add_bf16 v24,v9,s[40:43],s64 offen sc1
M 14,v[244:247],50
L 2
s_add_u32 s64,s75,24576
s_cmp_eq_u32 vcc_lo,-1
R 42,2,4096
M 14,v[252:255],54
buffer_atomic_pk_add_bf16 v25,v9,s[40:43],s64 offen sc1
s_cbranch_scc0 Lg4_Ka1_%=
Lg4_Fa1_%=:
s_nop 0
v_mfma_f32_16x16x32_bf16 v[22:25],a[4:7],v[26:29],0
s_add_u32 m0,s57,0
R 46,2,5120
v_lshlrev_b32 v62,16,v58
M 22,a[12:15],30
s_add_u32 s56,s55,0
v_add_f32 v10,v10,v62
v_and_b32 v63,0xffff0000,v58
v_mfma_f32_16x16x32_bf16 v[18:21],a[0:3],v[26:29],0
s_add_u32 s61,s54,s46
R 50,2,6144
v_add_f32 v11,v11,v63
M 18,a[8:11],30
L 3
D 0,56,0
M 18,a[16:19],34
s_cmp_ge_u32 s61,s63
R 54,2,7168
v_lshlrev_b32 v62,16,v59
M 18,a[24:27],38
s_cselect_b32 s61,s93,s61
v_add_f32 v12,v12,v62
v_and_b32 v63,0xffff0000,v59
M 22,a[20:23],34
s_cselect_b32 s46,s88,s46
R 26,2,8192
v_add_f32 v13,v13,v63
M 22,a[28:31],38
L 3
v_lshlrev_b32 v62,16,v60
v_add_f32 v14,v14,v62
M 22,a[36:39],42
s_cselect_b32 s63,s89,s63
R 30,2,9216
v_and_b32 v63,0xffff0000,v60
M 22,a[44:47],46
D 0,56,1024
M 18,a[32:35],42
s_cselect_b32 s93,s50,s93
R 34,2,10240
v_add_f32 v15,v15,v63
M 18,a[40:43],46
L 3
v_lshlrev_b32 v62,16,v61
v_add_f32 v16,v16,v62
M 18,a[48:51],50
s_cselect_b32 s88,8,s88
R 38,2,11264
v_and_b32 v63,0xffff0000,v61
M 18,a[56:59],54
buffer_load_dwordx4 v[58:61],v0,s[36:39],s74 offen offset:0 sc1
M 22,a[52:55],50
s_cselect_b32 s89,-1,s89
R 42,2,12288
v_add_f32 v17,v17,v63
M 22,a[60:63],54
L 3
v_cvt_pk_bf16_f32 v10,v10,v11
v_cvt_pk_bf16_f32 v11,v12,v13
M 22,a[68:71],26
s_mul_i32 s58,s61,2560
R 46,2,13312
v_cvt_pk_bf16_f32 v12,v14,v15
M 22,a[76:79],30
D 0,56,2048
M 18,a[64:67],26
s_add_u32 s58,s58,109891584
R 50,2,14336
v_cvt_pk_bf16_f32 v13,v16,v17
M 18,a[72:75],30
L 3
v_lshrrev_b32 v62,8,v0
v_lshl_add_u32 v62,v62,5,v0
M 18,a[80:83],34
s_sub_u32 s75,s66,s85
R 54,2,15360
v_add_u32 v62,s73,v62
M 18,a[88:91],38
s_mov_b64 s[40:41],s[86:87]
ds_write_b128 v62,v[10:13] offset:0
M 22,a[84:87],34
s_mov_b32 s42,4194304
R 26,2,16384
ds_read2_b32 v[14:15],v5 offset0:0 offset1:16
M 22,a[92:95],38
L 5
s_mov_b32 s43,0x00020000
ds_read2_b32 v[16:17],v5 offset0:32 offset1:48
M 22,a[100:103],42
s_add_u32 s64,s75,0
s_lshl_b32 s62,s54,15
R 30,2,17408
M 22,a[108:111],46
D 0,56,3072
M 18,a[96:99],42
s_add_u32 m0,s57,4096
s_add_u32 s56,s56,4096
R 34,2,18432
M 18,a[104:107],46
L 6
s_mov_b32 s67,s65
s_add_u32 s74,s49,s62
M 18,a[112:115],50
s_add_u32 s65,s47,s62
s_mov_b32 s54,s61
R 38,2,19456
M 18,a[120:123],54
L 3
M 22,a[116:119],50
R 42,2,20480
M 22,a[124:127],54
L 3
D 0,56,0
M 22,a[132:135],26
R 46,2,21504
)"
R"(M 22,a[140:143],30
M 18,a[128:131],26
R 50,2,22528
M 18,a[136:139],30
L 3
M 18,a[144:147],34
R 54,2,23552
M 18,a[152:155],38
M 22,a[148:151],34
R 26,2,24576
M 22,a[156:159],38
L 3
D 0,56,1024
M 22,a[164:167],42
R 30,2,25600
M 22,a[172:175],46
M 18,a[160:163],42
R 34,2,26624
M 18,a[168:171],46
L 3
M 18,a[176:179],50
s_waitcnt vmcnt(8)
R 38,2,27648
M 18,a[184:187],54
s_barrier
M 22,a[180:183],50
R 42,2,28672
M 22,a[188:191],54
L 3
D 0,56,2048
M 22,a[196:199],26
s_add_u32 m0,s59,1024
s_add_u32 s56,s55,28672
R 46,2,29696
M 22,a[204:207],30
s_mul_i32 s55,s61,114688
s_add_u32 s55,s55,s60
M 18,a[192:195],26
R 50,2,30720
M 18,a[200:203],30
L 3
M 18,a[208:211],34
R 54,2,31744
M 18,a[216:219],38
M 22,a[212:215],34
R 26,2,32768
M 22,a[220:223],38
L 3
buffer_load_dword v8,s[36:39],s58 offen offset:0 sc1 lds
M 22,a[228:231],42
s_add_u32 m0,s57,28672
R 30,2,33792
M 22,a[236:239],46
D 0,56,0
M 18,a[224:227],42
R 34,2,34816
M 18,a[232:235],46
L 3
M 18,a[240:243],50
R 38,2,35840
M 18,a[248:251],54
M 22,a[244:247],50
R 42,2,36864
M 22,a[252:255],54
L 3
D 0,56,1024
M 22,v[68:71],26
R 46,2,37888
M 22,v[76:79],30
M 18,v[64:67],26
R 50,2,38912
M 18,v[72:75],30
L 3
M 18,v[80:83],34
R 54,2,39936
M 18,v[88:91],38
D 0,56,2048
M 22,v[84:87],34
R 26,2,40960
M 22,v[92:95],38
L 3
M 22,v[100:103],42
R 30,2,41984
M 22,v[108:111],46
M 18,v[96:99],42
R 34,2,43008
M 18,v[104:107],46
L 3
D 0,56,3072
M 18,v[112:115],50
s_add_u32 m0,s57,32768
s_add_u32 s56,s56,4096
R 38,2,44032
M 18,v[120:123],54
s_sub_u32 s57,s84,s57
M 22,v[116:119],50
R 42,2,45056
M 22,v[124:127],54
L 3
M 22,v[132:135],26
R 46,2,46080
M 22,v[140:143],30
D 0,56,0
M 18,v[128:131],26
R 50,2,47104
M 18,v[136:139],30
L 3
M 18,v[144:147],34
R 54,2,48128
M 18,v[152:155],38
M 22,v[148:151],34
R 26,2,49152
M 22,v[156:159],38
L 3
D 0,56,1024
M 22,v[164:167],42
R 30,2,50176
M 22,v[172:175],46
M 18,v[160:163],42
R 34,2,51200
M 18,v[168:171],46
L 3
M 18,v[176:179],50
R 38,2,52224
M 18,v[184:187],54
D 0,56,2048
M 22,v[180:183],50
R 42,2,53248
M 22,v[188:191],54
L 3
M 22,v[196:199],26
R 46,2,54272
M 22,v[204:207],30
M 18,v[192:195],26
R 50,2,55296
M 18,v[200:203],30
L 3
M 18,v[208:211],34
s_waitcnt vmcnt(7)
R 54,2,56320
M 18,v[216:219],38
s_barrier
ds_read_b32 v7,v6 offset:1024
M 22,v[212:215],34
R 26,1,0
M 22,v[220:223],38
L 4
M 22,v[228:231],42
R 30,1,1024
M 22,v[236:239],46
buffer_atomic_pk_add_bf16 v14,v9,s[40:43],s64 offen sc1
M 18,v[224:227],42
s_add_u32 s64,s75,8192
R 34,1,2048
M 18,v[232:235],46
L 4
buffer_atomic_pk_add_bf16 v15,v9,s[40:43],s64 offen sc1
M 18,v[240:243],50
s_add_u32 s64,s75,16384
L 3
R 38,1,3072
v_cmp_eq_u32 vcc,s53,v7
M 18,v[248:251],54
buffer_atomic_pk_add_bf16 v16,v9,s[40:43],s64 offen sc1
M 22,v[244:247],50
L 2
s_add_u32 s64,s75,24576
s_cmp_eq_u32 vcc_lo,-1
R 42,1,4096
M 22,v[252:255],54
buffer_atomic_pk_add_bf16 v17,v9,s[40:43],s64 offen sc1
s_cbranch_scc1 Lg4_Fa0_%=
Lg4_Ka0_%=:
Lg4_La0_%=:
s_nop 0
v_mfma_f32_16x16x32_bf16 v[14:17],a[4:7],v[26:29],0
s_add_u32 m0,s59,3072
R 46,1,5120
v_lshlrev_b32 v62,16,v58
M 14,a[12:15],30
buffer_load_dword v8,s[36:39],s58 offen offset:0 sc1 lds
v_mfma_f32_16x16x32_bf16 v[10:13],a[0:3],v[26:29],0
s_sub_u32 s75,s67,s85
R 50,1,6144
v_add_f32 v18,v18,v62
M 10,a[8:11],30
L 3
v_and_b32 v63,0xffff0000,v58
v_add_f32 v19,v19,v63
M 10,a[16:19],34
s_mov_b64 s[40:41],s[86:87]
R 54,1,7168
v_lshlrev_b32 v62,16,v59
M 10,a[24:27],38
s_mov_b32 s42,4194304
v_add_f32 v20,v20,v62
v_and_b32 v63,0xffff0000,v59
M 14,a[20:23],34
s_mov_b32 s43,0x00020000
R 26,1,8192
v_add_f32 v21,v21,v63
M 14,a[28:31],38
L 3
v_lshlrev_b32 v62,16,v60
v_add_f32 v22,v22,v62
M 14,a[36:39],42
s_add_u32 s64,s75,0
R 30,1,9216
v_and_b32 v63,0xffff0000,v60
M 14,a[44:47],46
s_lshl_b32 s62,s54,15
v_add_f32 v23,v23,v63
v_lshlrev_b32 v62,16,v61
M 10,a[32:35],42
s_mov_b32 s66,s65
R 34,1,10240
v_add_f32 v24,v24,v62
M 10,a[40:43],46
L 3
v_and_b32 v63,0xffff0000,v61
v_add_f32 v25,v25,v63
M 10,a[48:51],50
s_add_u32 s65,s47,s62
R 38,1,11264
v_cvt_pk_bf16_f32 v18,v18,v19
M 10,a[56:59],54
buffer_load_dwordx4 v[58:61],v0,s[36:39],s74 offen offset:0 sc1
M 14,a[52:55],50
s_add_u32 s74,s49,s62
R 42,1,12288
v_cvt_pk_bf16_f32 v19,v20,v21
M 14,a[60:63],54
L 3
v_cvt_pk_bf16_f32 v20,v22,v23
v_cvt_pk_bf16_f32 v21,v24,v25
M 14,a[68:71],26
R 46,1,13312
v_lshrrev_b32 v62,8,v0
M 14,a[76:79],30
v_lshl_add_u32 v62,v62,5,v0
v_add_u32 v62,s73,v62
M 10,a[64:67],26
R 50,1,14336
M 10,a[72:75],30
L 3
ds_write_b128 v62,v[18:21] offset:0
M 10,a[80:83],34
R 54,1,15360
ds_read2_b32 v[22:23],v5 offset0:0 offset1:16
M 10,a[88:91],38
ds_read2_b32 v[24:25],v5 offset0:32 offset1:48
M 14,a[84:87],34
R 26,1,16384
M 14,a[92:95],38
L 6
M 14,a[100:103],42
R 30,1,17408
M 14,a[108:111],46
M 10,a[96:99],42
R 34,1,18432
M 10,a[104:107],46
L 5
L 3
M 10,a[112:115],50
R 38,1,19456
M 10,a[120:123],54
buffer_atomic_pk_add_bf16 v22,v9,s[40:43],s64 offen sc1
M 14,a[116:119],50
s_add_u32 s64,s75,8192
R 42,1,20480
M 14,a[124:127],54
L 3
buffer_atomic_pk_add_bf16 v23,v9,s[40:43],s64 offen sc1
M 14,a[132:135],26
s_add_u32 s64,s75,16384
R 46,1,21504
M 14,a[140:143],30
buffer_atomic_pk_add_bf16 v24,v9,s[40:43],s64 offen sc1
M 10,a[128:131],26
s_add_u32 s64,s75,24576
R 50,1,22528
M 10,a[136:139],30
L 3
buffer_atomic_pk_add_bf16 v25,v9,s[40:43],s64 offen sc1
M 10,a[144:147],34
R 54,1,23552
M 10,a[152:155],38
M 14,a[148:151],34
R 26,1,24576
M 14,a[156:159],38
L 3
M 14,a[164:167],42
R 30,1,25600
M 14,a[172:175],46
M 10,a[160:163],42
R 34,1,26624
M 10,a[168:171],46
L 3
M 10,a[176:179],50
s_waitcnt vmcnt(5)
R 38,1,27648
M 10,a[184:187],54
s_barrier
ds_read_b32 v7,v6 offset:3072
M 14,a[180:183],50
R 42,1,28672
M 14,a[188:191],54
L 4
M 14,a[196:199],26
R 46,1,29696
M 14,a[204:207],30
M 10,a[192:195],26
R 50,1,30720
M 10,a[200:203],30
L 4
L 3
v_cmp_eq_u32 vcc,s53,v7
M 10,a[208:211],34
R 54,1,31744
M 10,a[216:219],38
M 14,a[212:215],34
s_cmp_eq_u32 vcc_lo,-1
R 26,1,32768
s_cbranch_scc0 Lg4_Xa0_%=
M 14,a[220:223],38
L 3
s_add_u32 m0,s57,0
s_add_u32 s56,s55,0
M 14,a[228:231],42
s_add_u32 s61,s54,s46
s_cmp_ge_u32 s61,s63
s_cselect_b32 s61,s93,s61
R 30,1,33792
M 14,a[236:239],46
s_mul_i32 s58,s61,2560
D 0,56,0
M 10,a[224:227],42
s_cselect_b32 s46,s88,s46
s_cselect_b32 s63,s89,s63
R 34,1,34816
M 10,a[232:235],46
L 3
s_cselect_b32 s93,s50,s93
s_cselect_b32 s88,8,s88
M 10,a[240:243],50
s_cselect_b32 s89,-1,s89
s_add_u32 s58,s58,109891584
R 38,1,35840
M 10,a[248:251],54
D 0,56,1024
M 14,a[244:247],50
s_mov_b32 s54,s61
R 42,1,36864
M 14,a[252:255],54
L 3
M 14,v[68:71],26
R 46,1,37888
M 14,v[76:79],30
D 0,56,2048
M 10,v[64:67],26
R 50,1,38912
M 10,v[72:75],30
L 3
M 10,v[80:83],34
R 54,1,39936
M 10,v[88:91],38
D 0,56,3072
M 14,v[84:87],34
s_add_u32 m0,s57,4096
s_add_u32 s56,s56,4096
R 26,1,40960
M 14,v[92:95],38
L 3
M 14,v[100:103],42
R 30,1,41984
M 14,v[108:111],46
D 0,56,0
M 10,v[96:99],42
R 34,1,43008
M 10,v[104:107],46
L 3
M 10,v[112:115],50
R 38,1,44032
M 10,v[120:123],54
D 0,56,1024
M 14,v[116:119],50
R 42,1,45056
M 14,v[124:127],54
L 3
M 14,v[132:135],26
R 46,1,46080
M 14,v[140:143],30
D 0,56,2048
M 10,v[128:131],26
s_add_u32 m0,s59,0
s_add_u32 s56,s55,28672
R 50,1,47104
M 10,v[136:139],30
L 3
buffer_load_dword v8,s[36:39],s58 offen offset:0 sc1 lds
M 10,v[144:147],34
s_add_u32 m0,s57,28672
s_mul_i32 s55,s61,114688
R 54,1,48128
M 10,v[152:155],38
s_add_u32 s55,s55,s60
M 14,v[148:151],34
R 26,1,49152
M 14,v[156:159],38
L 3
D 0,56,0
M 14,v[164:167],42
R 30,1,50176
M 14,v[172:175],46
M 10,v[160:163],42
R 34,1,51200
M 10,v[168:171],46
L 3
D 0,56,1024
M 10,v[176:179],50
R 38,1,52224
M 10,v[184:187],54
D 0,56,2048
M 14,v[180:183],50
R 42,1,53248
M 14,v[188:191],54
L 3
M 14,v[196:199],26
R 46,1,54272
M 14,v[204:207],30
D 0,56,3072
M 10,v[192:195],26
s_add_u32 m0,s57,32768
s_add_u32 s56,s56,4096
R 50,1,55296
M 10,v[200:203],30
L 3
s_sub_u32 s57,s84,s57
M 10,v[208:211],34
s_waitcnt vmcnt(4)
R 54,1,56320
M 10,v[216:219],38
s_barrier
ds_read_b32 v7,v6 offset:0
M 14,v[212:215],34
R 26,2,0
M 14,v[220:223],38
L 4
D 0,56,0
M 14,v[228:231],42
R 30,2,1024
M 14,v[236:239],46
D 0,56,1024
M 10,v[224:227],42
R 34,2,2048
M 10,v[232:235],46
L 4
D 0,56,2048
M 10,v[240:243],50
L 3
R 38,2,3072
v_cmp_eq_u32 vcc,s53,v7
M 10,v[248:251],54
M 14,v[244:247],50
L 2
s_cmp_eq_u32 vcc_lo,-1
R 42,2,4096
M 14,v[252:255],54
buffer_store_dword v4,v3,s[36:39],s71 offen offset:0 sc0 sc1
buffer_store_dword v4,v3,s[36:39],s71 offen offset:0 sc0 sc1
buffer_store_dword v4,v3,s[36:39],s71 offen offset:0 sc0 sc1
buffer_store_dword v4,v3,s[36:39],s71 offen offset:0 sc0 sc1
s_cbranch_scc1 Lg4_Fa1_%=
s_branch Lg4_Ka1_%=
Lg4_Xa0_%=:
M 14,a[220:223],38
L 3
s_add_u32 m0,s59,0
M 14,a[228:231],42
R 30,1,33792
M 14,a[236:239],46
buffer_load_dword v8,s[36:39],s58 offen offset:0 sc1 lds
M 10,a[224:227],42
R 34,1,34816
M 10,a[232:235],46
L 3
M 10,a[240:243],50
R 38,1,35840
M 10,a[248:251],54
M 14,a[244:247],50
R 42,1,36864
M 14,a[252:255],54
L 3
M 14,v[68:71],26
R 46,1,37888
M 14,v[76:79],30
M 10,v[64:67],26
R 50,1,38912
M 10,v[72:75],30
L 3
)"
R"(M 10,v[80:83],34
R 54,1,39936
M 10,v[88:91],38
M 14,v[84:87],34
R 26,1,40960
M 14,v[92:95],38
L 3
M 14,v[100:103],42
R 30,1,41984
M 14,v[108:111],46
M 10,v[96:99],42
R 34,1,43008
M 10,v[104:107],46
L 3
M 10,v[112:115],50
R 38,1,44032
M 10,v[120:123],54
M 14,v[116:119],50
R 42,1,45056
M 14,v[124:127],54
L 3
M 14,v[132:135],26
R 46,1,46080
M 14,v[140:143],30
M 10,v[128:131],26
R 50,1,47104
M 10,v[136:139],30
L 3
M 10,v[144:147],34
R 54,1,48128
M 10,v[152:155],38
M 14,v[148:151],34
R 26,1,49152
M 14,v[156:159],38
L 3
M 14,v[164:167],42
R 30,1,50176
M 14,v[172:175],46
M 10,v[160:163],42
R 34,1,51200
M 10,v[168:171],46
L 3
M 10,v[176:179],50
R 38,1,52224
M 10,v[184:187],54
M 14,v[180:183],50
R 42,1,53248
M 14,v[188:191],54
L 3
M 14,v[196:199],26
R 46,1,54272
M 14,v[204:207],30
M 10,v[192:195],26
R 50,1,55296
M 10,v[200:203],30
L 3
M 10,v[208:211],34
s_waitcnt vmcnt(0)
R 54,1,56320
M 10,v[216:219],38
s_barrier
ds_read_b32 v7,v6 offset:0
M 14,v[212:215],34
M 14,v[220:223],38
L 3
M 14,v[228:231],42
M 14,v[236:239],46
M 10,v[224:227],42
M 10,v[232:235],46
L 1
L 0
M 10,v[240:243],50
v_cmp_eq_u32 vcc,s53,v7
M 10,v[248:251],54
M 14,v[244:247],50
M 14,v[252:255],54
Lg4_pa0_%=:
s_mov_b32 s80,1
s_cmp_eq_u32 vcc_lo,-1
s_cbranch_scc0 Lg4_xna0_%=
s_add_u32 m0,s57,0
s_add_u32 s56,s55,0
D 0,56,0
s_sub_u32 s75,s66,s85
v_lshlrev_b32 v62,16,v58
D 0,56,1024
v_add_f32 v10,v10,v62
D 0,56,2048
v_and_b32 v63,0xffff0000,v58
D 0,56,3072
v_add_f32 v11,v11,v63
s_add_u32 m0,s57,4096
s_add_u32 s56,s56,4096
D 0,56,0
v_lshlrev_b32 v62,16,v59
D 0,56,1024
v_add_f32 v12,v12,v62
D 0,56,2048
v_and_b32 v63,0xffff0000,v59
s_add_u32 s61,s54,s46
s_cmp_ge_u32 s61,s63
s_cselect_b32 s61,s93,s61
s_cselect_b32 s46,s88,s46
s_cselect_b32 s63,s89,s63
s_cselect_b32 s93,s50,s93
s_cselect_b32 s88,8,s88
s_cselect_b32 s89,-1,s89
s_mul_i32 s58,s61,2560
s_add_u32 s58,s58,109891584
s_add_u32 m0,s57,28672
s_add_u32 s56,s55,28672
D 0,56,0
v_add_f32 v13,v13,v63
D 0,56,1024
v_lshlrev_b32 v62,16,v60
D 0,56,2048
v_add_f32 v14,v14,v62
D 0,56,3072
v_and_b32 v63,0xffff0000,v60
s_add_u32 m0,s57,32768
s_add_u32 s56,s56,4096
D 0,56,0
v_add_f32 v15,v15,v63
D 0,56,1024
v_lshlrev_b32 v62,16,v61
D 0,56,2048
v_add_f32 v16,v16,v62
s_mul_i32 s55,s61,114688
s_add_u32 s55,s55,s60
s_sub_u32 s57,s84,s57
s_mov_b32 s54,s61
v_and_b32 v63,0xffff0000,v61
v_add_f32 v17,v17,v63
v_cvt_pk_bf16_f32 v10,v10,v11
v_cvt_pk_bf16_f32 v11,v12,v13
v_cvt_pk_bf16_f32 v12,v14,v15
v_cvt_pk_bf16_f32 v13,v16,v17
v_lshrrev_b32 v62,8,v0
v_lshl_add_u32 v62,v62,5,v0
v_add_u32 v62,s73,v62
ds_write_b128 v62,v[10:13] offset:0
ds_read2_b32 v[14:15],v5 offset0:0 offset1:16
ds_read2_b32 v[16:17],v5 offset0:32 offset1:48
s_mov_b64 s[40:41],s[86:87]
s_mov_b32 s42,4194304
s_mov_b32 s43,0x00020000
L 0
s_add_u32 s64,s75,0
buffer_atomic_pk_add_bf16 v14,v9,s[40:43],s64 offen sc1
s_add_u32 s64,s75,8192
buffer_atomic_pk_add_bf16 v15,v9,s[40:43],s64 offen sc1
s_add_u32 s64,s75,16384
buffer_atomic_pk_add_bf16 v16,v9,s[40:43],s64 offen sc1
s_add_u32 s64,s75,24576
buffer_atomic_pk_add_bf16 v17,v9,s[40:43],s64 offen sc1
s_mov_b32 s72,0x7fff0000
s_mov_b32 s69,0x7fff0000
s_mov_b32 s66,0x7fff0000
s_mov_b32 s76,0x7fff0000
s_branch Lg4_gox_%=
Lg4_xna0_%=:
s_cmp_ge_u32 s54,256
s_cbranch_scc1 Lg4_fina0_%=
s_add_u32 m0,s59,4096
s_nop 0
buffer_load_dword v8,s[36:39],s58 offen offset:0 sc1 lds
buffer_store_dword v4,v3,s[36:39],s71 offen offset:0 sc0 sc1
s_add_u32 m0,s59,5120
s_nop 0
buffer_load_dword v8,s[36:39],s58 offen offset:0 sc1 lds
buffer_store_dword v4,v3,s[36:39],s71 offen offset:0 sc0 sc1
s_mov_b32 s83,0
s_sub_u32 s75,s66,s85
v_lshlrev_b32 v62,16,v58
v_add_f32 v10,v10,v62
v_and_b32 v63,0xffff0000,v58
v_add_f32 v11,v11,v63
v_lshlrev_b32 v62,16,v59
v_add_f32 v12,v12,v62
v_and_b32 v63,0xffff0000,v59
v_add_f32 v13,v13,v63
v_lshlrev_b32 v62,16,v60
v_add_f32 v14,v14,v62
v_and_b32 v63,0xffff0000,v60
v_add_f32 v15,v15,v63
v_lshlrev_b32 v62,16,v61
v_add_f32 v16,v16,v62
v_and_b32 v63,0xffff0000,v61
v_add_f32 v17,v17,v63
v_cvt_pk_bf16_f32 v10,v10,v11
v_cvt_pk_bf16_f32 v11,v12,v13
v_cvt_pk_bf16_f32 v12,v14,v15
v_cvt_pk_bf16_f32 v13,v16,v17
v_lshrrev_b32 v62,8,v0
v_lshl_add_u32 v62,v62,5,v0
v_add_u32 v62,s73,v62
ds_write_b128 v62,v[10:13] offset:0
ds_read2_b32 v[14:15],v5 offset0:0 offset1:16
ds_read2_b32 v[16:17],v5 offset0:32 offset1:48
s_mov_b64 s[40:41],s[86:87]
s_mov_b32 s42,4194304
s_mov_b32 s43,0x00020000
L 0
s_add_u32 s64,s75,0
buffer_atomic_pk_add_bf16 v14,v9,s[40:43],s64 offen sc1
s_add_u32 s64,s75,8192
buffer_atomic_pk_add_bf16 v15,v9,s[40:43],s64 offen sc1
s_add_u32 s64,s75,16384
buffer_atomic_pk_add_bf16 v16,v9,s[40:43],s64 offen sc1
s_add_u32 s64,s75,24576
buffer_atomic_pk_add_bf16 v17,v9,s[40:43],s64 offen sc1
s_mov_b32 s72,0x7fff0000
s_mov_b32 s69,0x7fff0000
s_mov_b32 s66,0x7fff0000
s_mov_b32 s76,0x7fff0000
s_branch Lg4_poll_%=
Lg4_fina0_%=:
s_sub_u32 s75,s66,s85
v_lshlrev_b32 v62,16,v58
v_add_f32 v10,v10,v62
v_and_b32 v63,0xffff0000,v58
v_add_f32 v11,v11,v63
v_lshlrev_b32 v62,16,v59
v_add_f32 v12,v12,v62
v_and_b32 v63,0xffff0000,v59
v_add_f32 v13,v13,v63
v_lshlrev_b32 v62,16,v60
v_add_f32 v14,v14,v62
v_and_b32 v63,0xffff0000,v60
v_add_f32 v15,v15,v63
v_lshlrev_b32 v62,16,v61
v_add_f32 v16,v16,v62
v_and_b32 v63,0xffff0000,v61
v_add_f32 v17,v17,v63
v_cvt_pk_bf16_f32 v10,v10,v11
v_cvt_pk_bf16_f32 v11,v12,v13
v_cvt_pk_bf16_f32 v12,v14,v15
v_cvt_pk_bf16_f32 v13,v16,v17
v_lshrrev_b32 v62,8,v0
v_lshl_add_u32 v62,v62,5,v0
v_add_u32 v62,s73,v62
ds_write_b128 v62,v[10:13] offset:0
ds_read2_b32 v[14:15],v5 offset0:0 offset1:16
ds_read2_b32 v[16:17],v5 offset0:32 offset1:48
s_mov_b64 s[40:41],s[86:87]
s_mov_b32 s42,4194304
s_mov_b32 s43,0x00020000
L 0
s_add_u32 s64,s75,0
buffer_atomic_pk_add_bf16 v14,v9,s[40:43],s64 offen sc1
s_add_u32 s64,s75,8192
buffer_atomic_pk_add_bf16 v15,v9,s[40:43],s64 offen sc1
s_add_u32 s64,s75,16384
buffer_atomic_pk_add_bf16 v16,v9,s[40:43],s64 offen sc1
s_add_u32 s64,s75,24576
buffer_atomic_pk_add_bf16 v17,v9,s[40:43],s64 offen sc1
s_mov_b32 s72,0x7fff0000
s_mov_b32 s69,0x7fff0000
s_mov_b32 s66,0x7fff0000
s_mov_b32 s76,0x7fff0000
s_branch Lg4_done_%=
Lg4_Ka1_%=:
Lg4_La1_%=:
s_nop 0
v_mfma_f32_16x16x32_bf16 v[22:25],a[4:7],v[26:29],0
s_add_u32 m0,s59,3072
R 46,2,5120
v_lshlrev_b32 v62,16,v58
M 22,a[12:15],30
buffer_load_dword v8,s[36:39],s58 offen offset:0 sc1 lds
v_mfma_f32_16x16x32_bf16 v[18:21],a[0:3],v[26:29],0
s_sub_u32 s75,s66,s85
R 50,2,6144
v_add_f32 v10,v10,v62
M 18,a[8:11],30
L 3
v_and_b32 v63,0xffff0000,v58
v_add_f32 v11,v11,v63
M 18,a[16:19],34
s_mov_b64 s[40:41],s[86:87]
R 54,2,7168
v_lshlrev_b32 v62,16,v59
M 18,a[24:27],38
s_mov_b32 s42,4194304
v_add_f32 v12,v12,v62
v_and_b32 v63,0xffff0000,v59
M 22,a[20:23],34
s_mov_b32 s43,0x00020000
R 26,2,8192
v_add_f32 v13,v13,v63
M 22,a[28:31],38
L 3
v_lshlrev_b32 v62,16,v60
v_add_f32 v14,v14,v62
M 22,a[36:39],42
s_add_u32 s64,s75,0
R 30,2,9216
v_and_b32 v63,0xffff0000,v60
M 22,a[44:47],46
s_lshl_b32 s62,s54,15
v_add_f32 v15,v15,v63
v_lshlrev_b32 v62,16,v61
M 18,a[32:35],42
s_mov_b32 s67,s65
R 34,2,10240
v_add_f32 v16,v16,v62
M 18,a[40:43],46
L 3
v_and_b32 v63,0xffff0000,v61
v_add_f32 v17,v17,v63
M 18,a[48:51],50
s_add_u32 s65,s47,s62
R 38,2,11264
v_cvt_pk_bf16_f32 v10,v10,v11
M 18,a[56:59],54
buffer_load_dwordx4 v[58:61],v0,s[36:39],s74 offen offset:0 sc1
M 22,a[52:55],50
s_add_u32 s74,s49,s62
R 42,2,12288
v_cvt_pk_bf16_f32 v11,v12,v13
M 22,a[60:63],54
L 3
v_cvt_pk_bf16_f32 v12,v14,v15
v_cvt_pk_bf16_f32 v13,v16,v17
M 22,a[68:71],26
R 46,2,13312
v_lshrrev_b32 v62,8,v0
M 22,a[76:79],30
v_lshl_add_u32 v62,v62,5,v0
v_add_u32 v62,s73,v62
M 18,a[64:67],26
R 50,2,14336
M 18,a[72:75],30
L 3
ds_write_b128 v62,v[10:13] offset:0
M 18,a[80:83],34
R 54,2,15360
ds_read2_b32 v[14:15],v5 offset0:0 offset1:16
M 18,a[88:91],38
ds_read2_b32 v[16:17],v5 offset0:32 offset1:48
M 22,a[84:87],34
R 26,2,16384
M 22,a[92:95],38
L 6
M 22,a[100:103],42
R 30,2,17408
M 22,a[108:111],46
M 18,a[96:99],42
R 34,2,18432
M 18,a[104:107],46
L 5
L 3
M 18,a[112:115],50
R 38,2,19456
M 18,a[120:123],54
buffer_atomic_pk_add_bf16 v14,v9,s[40:43],s64 offen sc1
M 22,a[116:119],50
s_add_u32 s64,s75,8192
R 42,2,20480
M 22,a[124:127],54
L 3
buffer_atomic_pk_add_bf16 v15,v9,s[40:43],s64 offen sc1
M 22,a[132:135],26
s_add_u32 s64,s75,16384
R 46,2,21504
M 22,a[140:143],30
buffer_atomic_pk_add_bf16 v16,v9,s[40:43],s64 offen sc1
M 18,a[128:131],26
s_add_u32 s64,s75,24576
R 50,2,22528
M 18,a[136:139],30
L 3
buffer_atomic_pk_add_bf16 v17,v9,s[40:43],s64 offen sc1
M 18,a[144:147],34
R 54,2,23552
M 18,a[152:155],38
M 22,a[148:151],34
R 26,2,24576
M 22,a[156:159],38
L 3
M 22,a[164:167],42
R 30,2,25600
M 22,a[172:175],46
M 18,a[160:163],42
R 34,2,26624
M 18,a[168:171],46
L 3
M 18,a[176:179],50
s_waitcnt vmcnt(5)
R 38,2,27648
M 18,a[184:187],54
s_barrier
ds_read_b32 v7,v6 offset:3072
M 22,a[180:183],50
R 42,2,28672
M 22,a[188:191],54
L 4
M 22,a[196:199],26
R 46,2,29696
M 22,a[204:207],30
M 18,a[192:195],26
R 50,2,30720
M 18,a[200:203],30
L 4
L 3
v_cmp_eq_u32 vcc,s53,v7
M 18,a[208:211],34
R 54,2,31744
M 18,a[216:219],38
M 22,a[212:215],34
s_cmp_eq_u32 vcc_lo,-1
R 26,2,32768
s_cbranch_scc0 Lg4_Xa1_%=
M 22,a[220:223],38
L 3
s_add_u32 m0,s57,0
s_add_u32 s56,s55,0
M 22,a[228:231],42
s_add_u32 s61,s54,s46
s_cmp_ge_u32 s61,s63
s_cselect_b32 s61,s93,s61
R 30,2,33792
M 22,a[236:239],46
s_mul_i32 s58,s61,2560
D 0,56,0
M 18,a[224:227],42
s_cselect_b32 s46,s88,s46
s_cselect_b32 s63,s89,s63
R 34,2,34816
M 18,a[232:235],46
L 3
s_cselect_b32 s93,s50,s93
s_cselect_b32 s88,8,s88
M 18,a[240:243],50
s_cselect_b32 s89,-1,s89
s_add_u32 s58,s58,109891584
R 38,2,35840
M 18,a[248:251],54
D 0,56,1024
M 22,a[244:247],50
s_mov_b32 s54,s61
R 42,2,36864
M 22,a[252:255],54
L 3
M 22,v[68:71],26
R 46,2,37888
M 22,v[76:79],30
D 0,56,2048
M 18,v[64:67],26
R 50,2,38912
M 18,v[72:75],30
L 3
M 18,v[80:83],34
R 54,2,39936
M 18,v[88:91],38
D 0,56,3072
M 22,v[84:87],34
s_add_u32 m0,s57,4096
s_add_u32 s56,s56,4096
R 26,2,40960
M 22,v[92:95],38
L 3
M 22,v[100:103],42
R 30,2,41984
M 22,v[108:111],46
D 0,56,0
M 18,v[96:99],42
R 34,2,43008
M 18,v[104:107],46
L 3
M 18,v[112:115],50
R 38,2,44032
M 18,v[120:123],54
D 0,56,1024
M 22,v[116:119],50
R 42,2,45056
M 22,v[124:127],54
L 3
M 22,v[132:135],26
R 46,2,46080
M 22,v[140:143],30
D 0,56,2048
M 18,v[128:131],26
s_add_u32 m0,s59,1024
s_add_u32 s56,s55,28672
R 50,2,47104
M 18,v[136:139],30
L 3
buffer_load_dword v8,s[36:39],s58 offen offset:0 sc1 lds
M 18,v[144:147],34
s_add_u32 m0,s57,28672
s_mul_i32 s55,s61,114688
R 54,2,48128
M 18,v[152:155],38
s_add_u32 s55,s55,s60
M 22,v[148:151],34
R 26,2,49152
M 22,v[156:159],38
L 3
D 0,56,0
)"
R"(M 22,v[164:167],42
R 30,2,50176
M 22,v[172:175],46
M 18,v[160:163],42
R 34,2,51200
M 18,v[168:171],46
L 3
D 0,56,1024
M 18,v[176:179],50
R 38,2,52224
M 18,v[184:187],54
D 0,56,2048
M 22,v[180:183],50
R 42,2,53248
M 22,v[188:191],54
L 3
M 22,v[196:199],26
R 46,2,54272
M 22,v[204:207],30
D 0,56,3072
M 18,v[192:195],26
s_add_u32 m0,s57,32768
s_add_u32 s56,s56,4096
R 50,2,55296
M 18,v[200:203],30
L 3
s_sub_u32 s57,s84,s57
M 18,v[208:211],34
s_waitcnt vmcnt(4)
R 54,2,56320
M 18,v[216:219],38
s_barrier
ds_read_b32 v7,v6 offset:1024
M 22,v[212:215],34
R 26,1,0
M 22,v[220:223],38
L 4
D 0,56,0
M 22,v[228:231],42
R 30,1,1024
M 22,v[236:239],46
D 0,56,1024
M 18,v[224:227],42
R 34,1,2048
M 18,v[232:235],46
L 4
D 0,56,2048
M 18,v[240:243],50
L 3
R 38,1,3072
v_cmp_eq_u32 vcc,s53,v7
M 18,v[248:251],54
M 22,v[244:247],50
L 2
s_cmp_eq_u32 vcc_lo,-1
R 42,1,4096
M 22,v[252:255],54
buffer_store_dword v4,v3,s[36:39],s71 offen offset:0 sc0 sc1
buffer_store_dword v4,v3,s[36:39],s71 offen offset:0 sc0 sc1
buffer_store_dword v4,v3,s[36:39],s71 offen offset:0 sc0 sc1
buffer_store_dword v4,v3,s[36:39],s71 offen offset:0 sc0 sc1
s_cbranch_scc1 Lg4_Fa0_%=
s_branch Lg4_Ka0_%=
Lg4_Xa1_%=:
M 22,a[220:223],38
L 3
s_add_u32 m0,s59,1024
M 22,a[228:231],42
R 30,2,33792
M 22,a[236:239],46
buffer_load_dword v8,s[36:39],s58 offen offset:0 sc1 lds
M 18,a[224:227],42
R 34,2,34816
M 18,a[232:235],46
L 3
M 18,a[240:243],50
R 38,2,35840
M 18,a[248:251],54
M 22,a[244:247],50
R 42,2,36864
M 22,a[252:255],54
L 3
M 22,v[68:71],26
R 46,2,37888
M 22,v[76:79],30
M 18,v[64:67],26
R 50,2,38912
M 18,v[72:75],30
L 3
M 18,v[80:83],34
R 54,2,39936
M 18,v[88:91],38
M 22,v[84:87],34
R 26,2,40960
M 22,v[92:95],38
L 3
M 22,v[100:103],42
R 30,2,41984
M 22,v[108:111],46
M 18,v[96:99],42
R 34,2,43008
M 18,v[104:107],46
L 3
M 18,v[112:115],50
R 38,2,44032
M 18,v[120:123],54
M 22,v[116:119],50
R 42,2,45056
M 22,v[124:127],54
L 3
M 22,v[132:135],26
R 46,2,46080
M 22,v[140:143],30
M 18,v[128:131],26
R 50,2,47104
M 18,v[136:139],30
L 3
M 18,v[144:147],34
R 54,2,48128
M 18,v[152:155],38
M 22,v[148:151],34
R 26,2,49152
M 22,v[156:159],38
L 3
M 22,v[164:167],42
R 30,2,50176
M 22,v[172:175],46
M 18,v[160:163],42
R 34,2,51200
M 18,v[168:171],46
L 3
M 18,v[176:179],50
R 38,2,52224
M 18,v[184:187],54
M 22,v[180:183],50
R 42,2,53248
M 22,v[188:191],54
L 3
M 22,v[196:199],26
R 46,2,54272
M 22,v[204:207],30
M 18,v[192:195],26
R 50,2,55296
M 18,v[200:203],30
L 3
M 18,v[208:211],34
s_waitcnt vmcnt(0)
R 54,2,56320
M 18,v[216:219],38
s_barrier
ds_read_b32 v7,v6 offset:1024
M 22,v[212:215],34
M 22,v[220:223],38
L 3
M 22,v[228:231],42
M 22,v[236:239],46
M 18,v[224:227],42
M 18,v[232:235],46
L 1
L 0
M 18,v[240:243],50
v_cmp_eq_u32 vcc,s53,v7
M 18,v[248:251],54
M 22,v[244:247],50
M 22,v[252:255],54
Lg4_pa1_%=:
s_mov_b32 s80,0
s_cmp_eq_u32 vcc_lo,-1
s_cbranch_scc0 Lg4_xna1_%=
s_add_u32 m0,s57,0
s_add_u32 s56,s55,0
D 0,56,0
s_sub_u32 s75,s67,s85
v_lshlrev_b32 v62,16,v58
D 0,56,1024
v_add_f32 v18,v18,v62
D 0,56,2048
v_and_b32 v63,0xffff0000,v58
D 0,56,3072
v_add_f32 v19,v19,v63
s_add_u32 m0,s57,4096
s_add_u32 s56,s56,4096
D 0,56,0
v_lshlrev_b32 v62,16,v59
D 0,56,1024
v_add_f32 v20,v20,v62
D 0,56,2048
v_and_b32 v63,0xffff0000,v59
s_add_u32 s61,s54,s46
s_cmp_ge_u32 s61,s63
s_cselect_b32 s61,s93,s61
s_cselect_b32 s46,s88,s46
s_cselect_b32 s63,s89,s63
s_cselect_b32 s93,s50,s93
s_cselect_b32 s88,8,s88
s_cselect_b32 s89,-1,s89
s_mul_i32 s58,s61,2560
s_add_u32 s58,s58,109891584
s_add_u32 m0,s57,28672
s_add_u32 s56,s55,28672
D 0,56,0
v_add_f32 v21,v21,v63
D 0,56,1024
v_lshlrev_b32 v62,16,v60
D 0,56,2048
v_add_f32 v22,v22,v62
D 0,56,3072
v_and_b32 v63,0xffff0000,v60
s_add_u32 m0,s57,32768
s_add_u32 s56,s56,4096
D 0,56,0
v_add_f32 v23,v23,v63
D 0,56,1024
v_lshlrev_b32 v62,16,v61
D 0,56,2048
v_add_f32 v24,v24,v62
s_mul_i32 s55,s61,114688
s_add_u32 s55,s55,s60
s_sub_u32 s57,s84,s57
s_mov_b32 s54,s61
v_and_b32 v63,0xffff0000,v61
v_add_f32 v25,v25,v63
v_cvt_pk_bf16_f32 v18,v18,v19
v_cvt_pk_bf16_f32 v19,v20,v21
v_cvt_pk_bf16_f32 v20,v22,v23
v_cvt_pk_bf16_f32 v21,v24,v25
v_lshrrev_b32 v62,8,v0
v_lshl_add_u32 v62,v62,5,v0
v_add_u32 v62,s73,v62
ds_write_b128 v62,v[18:21] offset:0
ds_read2_b32 v[22:23],v5 offset0:0 offset1:16
ds_read2_b32 v[24:25],v5 offset0:32 offset1:48
s_mov_b64 s[40:41],s[86:87]
s_mov_b32 s42,4194304
s_mov_b32 s43,0x00020000
L 0
s_add_u32 s64,s75,0
buffer_atomic_pk_add_bf16 v22,v9,s[40:43],s64 offen sc1
s_add_u32 s64,s75,8192
buffer_atomic_pk_add_bf16 v23,v9,s[40:43],s64 offen sc1
s_add_u32 s64,s75,16384
buffer_atomic_pk_add_bf16 v24,v9,s[40:43],s64 offen sc1
s_add_u32 s64,s75,24576
buffer_atomic_pk_add_bf16 v25,v9,s[40:43],s64 offen sc1
s_mov_b32 s72,0x7fff0000
s_mov_b32 s69,0x7fff0000
s_mov_b32 s67,0x7fff0000
s_mov_b32 s77,0x7fff0000
s_branch Lg4_gox_%=
Lg4_xna1_%=:
s_cmp_ge_u32 s54,256
s_cbranch_scc1 Lg4_fina1_%=
s_add_u32 m0,s59,4096
s_nop 0
buffer_load_dword v8,s[36:39],s58 offen offset:0 sc1 lds
buffer_store_dword v4,v3,s[36:39],s71 offen offset:0 sc0 sc1
s_add_u32 m0,s59,5120
s_nop 0
buffer_load_dword v8,s[36:39],s58 offen offset:0 sc1 lds
buffer_store_dword v4,v3,s[36:39],s71 offen offset:0 sc0 sc1
s_mov_b32 s83,0
s_sub_u32 s75,s67,s85
v_lshlrev_b32 v62,16,v58
v_add_f32 v18,v18,v62
v_and_b32 v63,0xffff0000,v58
v_add_f32 v19,v19,v63
v_lshlrev_b32 v62,16,v59
v_add_f32 v20,v20,v62
v_and_b32 v63,0xffff0000,v59
v_add_f32 v21,v21,v63
v_lshlrev_b32 v62,16,v60
v_add_f32 v22,v22,v62
v_and_b32 v63,0xffff0000,v60
v_add_f32 v23,v23,v63
v_lshlrev_b32 v62,16,v61
v_add_f32 v24,v24,v62
v_and_b32 v63,0xffff0000,v61
v_add_f32 v25,v25,v63
v_cvt_pk_bf16_f32 v18,v18,v19
v_cvt_pk_bf16_f32 v19,v20,v21
v_cvt_pk_bf16_f32 v20,v22,v23
v_cvt_pk_bf16_f32 v21,v24,v25
v_lshrrev_b32 v62,8,v0
v_lshl_add_u32 v62,v62,5,v0
v_add_u32 v62,s73,v62
ds_write_b128 v62,v[18:21] offset:0
ds_read2_b32 v[22:23],v5 offset0:0 offset1:16
ds_read2_b32 v[24:25],v5 offset0:32 offset1:48
s_mov_b64 s[40:41],s[86:87]
s_mov_b32 s42,4194304
s_mov_b32 s43,0x00020000
L 0
s_add_u32 s64,s75,0
buffer_atomic_pk_add_bf16 v22,v9,s[40:43],s64 offen sc1
s_add_u32 s64,s75,8192
buffer_atomic_pk_add_bf16 v23,v9,s[40:43],s64 offen sc1
s_add_u32 s64,s75,16384
buffer_atomic_pk_add_bf16 v24,v9,s[40:43],s64 offen sc1
s_add_u32 s64,s75,24576
buffer_atomic_pk_add_bf16 v25,v9,s[40:43],s64 offen sc1
s_mov_b32 s72,0x7fff0000
s_mov_b32 s69,0x7fff0000
s_mov_b32 s67,0x7fff0000
s_mov_b32 s77,0x7fff0000
s_branch Lg4_poll_%=
Lg4_fina1_%=:
s_sub_u32 s75,s67,s85
v_lshlrev_b32 v62,16,v58
v_add_f32 v18,v18,v62
v_and_b32 v63,0xffff0000,v58
v_add_f32 v19,v19,v63
v_lshlrev_b32 v62,16,v59
v_add_f32 v20,v20,v62
v_and_b32 v63,0xffff0000,v59
v_add_f32 v21,v21,v63
v_lshlrev_b32 v62,16,v60
v_add_f32 v22,v22,v62
v_and_b32 v63,0xffff0000,v60
v_add_f32 v23,v23,v63
v_lshlrev_b32 v62,16,v61
v_add_f32 v24,v24,v62
v_and_b32 v63,0xffff0000,v61
v_add_f32 v25,v25,v63
v_cvt_pk_bf16_f32 v18,v18,v19
v_cvt_pk_bf16_f32 v19,v20,v21
v_cvt_pk_bf16_f32 v20,v22,v23
v_cvt_pk_bf16_f32 v21,v24,v25
v_lshrrev_b32 v62,8,v0
v_lshl_add_u32 v62,v62,5,v0
v_add_u32 v62,s73,v62
ds_write_b128 v62,v[18:21] offset:0
ds_read2_b32 v[22:23],v5 offset0:0 offset1:16
ds_read2_b32 v[24:25],v5 offset0:32 offset1:48
s_mov_b64 s[40:41],s[86:87]
s_mov_b32 s42,4194304
s_mov_b32 s43,0x00020000
L 0
s_add_u32 s64,s75,0
buffer_atomic_pk_add_bf16 v22,v9,s[40:43],s64 offen sc1
s_add_u32 s64,s75,8192
buffer_atomic_pk_add_bf16 v23,v9,s[40:43],s64 offen sc1
s_add_u32 s64,s75,16384
buffer_atomic_pk_add_bf16 v24,v9,s[40:43],s64 offen sc1
s_add_u32 s64,s75,24576
buffer_atomic_pk_add_bf16 v25,v9,s[40:43],s64 offen sc1
s_mov_b32 s72,0x7fff0000
s_mov_b32 s69,0x7fff0000
s_mov_b32 s67,0x7fff0000
s_mov_b32 s77,0x7fff0000
s_branch Lg4_done_%=
Lg4_done_%=:
s_nop 0
s_barrier
buffer_store_dword v4,v3,s[36:39],s70 offen offset:0 sc0 sc1
s_waitcnt vmcnt(1)
s_barrier
buffer_store_dword v4,v3,s[36:39],s72 offen offset:0 sc0 sc1
s_waitcnt vmcnt(0) lgkmcnt(0)
s_barrier
s_endpgm
Lcode_g4_e:
.purgem M
.purgem R
.purgem T
.purgem D
.purgem L
)"
  :: "s"(ws), "s"(__builtin_amdgcn_readfirstlane(sWave)), "s"(__builtin_amdgcn_readfirstlane(sG0)), "s"(__builtin_amdgcn_readfirstlane(sStep)), "s"(__builtin_amdgcn_readfirstlane(sOutOff)), "s"(__builtin_amdgcn_readfirstlane(sFoutOff)), "s"(__builtin_amdgcn_readfirstlane(sAuxOff)), "s"(__builtin_amdgcn_readfirstlane(sWw)), "s"(__builtin_amdgcn_readfirstlane(sBuf)), "s"(__builtin_amdgcn_readfirstlane(sRk)), "s"(__builtin_amdgcn_readfirstlane(sExp)), "v"(vL16), "v"(vFlag), "v"(vSnapOff), "v"(vSnapRd), "v"(vWd0), "v"(vWd1), "v"(vWr0), "v"(vWr1), "v"(vSp), "v"(vSn), "s"(w4), "s"(out) : "v255", "v0", "v1", "v2", "v3", "v4", "v5", "v6", "v7", "v8", "v9", "v10", "v11", "v12", "v13", "v14", "v15", "v16", "v17", "v18", "v19", "v20", "v21", "v22", "v23", "v24", "v25", "v26", "v27", "v28", "v29", "v30", "v31", "v32", "v33", "v34", "v35", "v36", "v37", "v38", "v39", "v40", "v41", "v42", "v43", "v44", "v45", "v46", "v47", "v48", "v49", "v50", "v51", "v52", "v53", "v54", "v55", "v56", "v57", "v58", "v59", "v60", "v61", "v62", "v63", "v64", "v65", "v66", "v67", "v68", "v69", "v70", "v71", "v72", "v73", "v74", "v75", "v76", "v77", "v78", "v79", "v80", "v81", "v82", "v83", "v84", "v85", "v86", "v87", "v88", "v89", "v90", "v91", "v92", "v93", "v94", "v95", "v96", "v97", "v98", "v99", "v100", "v101", "v102", "v103", "v104", "v105", "v106", "v107", "v108", "v109", "v110", "v111", "v112", "v113", "v114", "v115", "v116", "v117", "v118", "v119", "v120", "v121", "v122", "v123", "v124", "v125", "v126", "v127", "v128", "v129", "v130", "v131", "v132", "v133", "v134", "v135", "v136", "v137", "v138", "v139", "v140", "v141", "v142", "v143", "v144", "v145", "v146", "v147", "v148", "v149", "v150", "v151", "v152", "v153", "v154", "v155", "v156", "v157", "v158", "v159", "v160", "v161", "v162", "v163", "v164", "v165", "v166", "v167", "v168", "v169", "v170", "v171", "v172", "v173", "v174", "v175", "v176", "v177", "v178", "v179", "v180", "v181", "v182", "v183", "v184", "v185", "v186", "v187", "v188", "v189", "v190", "v191", "v192", "v193", "v194", "v195", "v196", "v197", "v198", "v199", "v200", "v201", "v202", "v203", "v204", "v205", "v206", "v207", "v208", "v209", "v210", "v211", "v212", "v213", "v214", "v215", "v216", "v217", "v218", "v219", "v220", "v221", "v222", "v223", "v224", "v225", "v226", "v227", "v228", "v229", "v230", "v231", "a0", "a1", "a2", "a3", "a4", "a5", "a6", "a7", "a8", "a9", "a10", "a11", "a12", "a13", "a14", "a15", "a16", "a17", "a18", "a19", "a20", "a21", "a22", "a23", "a24", "a25", "a26", "a27", "a28", "a29", "a30", "a31", "a32", "a33", "a34", "a35", "a36", "a37", "a38", "a39", "a40", "a41", "a42", "a43", "a44", "a45", "a46", "a47", "a48", "a49", "a50", "a51", "a52", "a53", "a54", "a55", "a56", "a57", "a58", "a59", "a60", "a61", "a62", "a63", "a64", "a65", "a66", "a67", "a68", "a69", "a70", "a71", "a72", "a73", "a74", "a75", "a76", "a77", "a78", "a79", "a80", "a81", "a82", "a83", "a84", "a85", "a86", "a87", "a88", "a89", "a90", "a91", "a92", "a93", "a94", "a95", "a96", "a97", "a98", "a99", "a100", "a101", "a102", "a103", "a104", "a105", "a106", "a107", "a108", "a109", "a110", "a111", "a112", "a113", "a114", "a115", "a116", "a117", "a118", "a119", "a120", "a121", "a122", "a123", "a124", "a125", "a126", "a127", "a128", "a129", "a130", "a131", "a132", "a133", "a134", "a135", "a136", "a137", "a138", "a139", "a140", "a141", "a142", "a143", "a144", "a145", "a146", "a147", "a148", "a149", "a150", "a151", "a152", "a153", "a154", "a155", "a156", "a157", "a158", "a159", "a160", "a161", "a162", "a163", "a164", "a165", "a166", "a167", "a168", "a169", "a170", "a171", "a172", "a173", "a174", "a175", "a176", "a177", "a178", "a179", "a180", "a181", "a182", "a183", "a184", "a185", "a186", "a187", "a188", "a189", "a190", "a191", "a192", "a193", "a194", "a195", "a196", "a197", "a198", "a199", "a200", "a201", "a202", "a203", "a204", "a205", "a206", "a207", "a208", "a209", "a210", "a211", "a212", "a213", "a214", "a215", "a216", "a217", "a218", "a219", "a220", "a221", "a222", "a223", "a224", "a225", "a226", "a227", "a228", "a229", "a230", "a231", "a232", "a233", "a234", "a235", "a236", "a237", "a238", "a239", "a240", "a241", "a242", "a243", "a244", "a245", "a246", "a247", "a248", "a249", "a250", "a251", "a252", "a253", "a254", "a255", "s36", "s37", "s38", "s39", "s40", "s41", "s42", "s43", "s44", "s45", "s46", "s47", "s48", "s49", "s50", "s51", "s52", "s53", "s54", "s55", "s56", "s57", "s58", "s59", "s60", "s61", "s62", "s63", "s64", "s65", "s66", "s67", "s68", "s69", "s70", "s71", "s72", "s73", "s74", "s75", "s76", "s77", "s78", "s79", "s80", "s81", "s82", "s83", "s84", "s85", "s86", "s87", "s88", "s89", "s90", "s91", "s92", "s93", "s94", "s95", "s96", "s97", "vcc", "scc", "memory");
    __builtin_unreachable();
  } else {
    LANE_ID(lane2);
    constexpr int N1 = 33 - (NR4 - 6) * 16, T1 = LAST_TASKS > RLN * N1 ? RLN * N1 : LAST_TASKS;
    const int w = kind == 6 ? 0 : kind == 1 ? 1 + (3 - rep) * 8 + slice : N1 + rep * 8 + slice;
    if (kind != 2) for (int t = w; t < T1; t += N1) row_last(ws, out, t, lane2, wave, ep);
    for (int t = T1 + (w + 32) % (N1 + 32); t < LAST_TASKS; t += N1 + 32) row_last(ws, out, t, lane2, wave, ep);
    if (kind == 2 && rep == 0 && slice == 0 && wave == 0) set_flag(ws + OFF_EP, lane2, ep);
    CTS(14);
    asm volatile("s_endpgm\nLcode_end:" ::: "memory");
    __builtin_unreachable();
  }
}

extern "C" void kernel_launch(void* const* d_in, const int* in_sizes, int n_in, void* d_out, int out_size,
                              void* d_ws, size_t ws_size, hipStream_t stream) {
  const u16* q = (const u16*)d_in[0];
  const u16* k = (const u16*)d_in[1];
  const u16* v = (const u16*)d_in[2];
  const u16* shift_k = (const u16*)d_in[3];
  const u16* ssm_k = (const u16*)d_in[4];
  const u16* ssm_D = (const u16*)d_in[5];
  const u16* w1 = (const u16*)d_in[7];
  const u16* w2 = (const u16*)d_in[8];
  const u16* w3 = (const u16*)d_in[9];
  const u16* w4 = (const u16*)d_in[10];
  flow_kernel<<<256, 256, 0, stream>>>(q, k, v, shift_k, ssm_k, ssm_D, w1, w2, w3, w4, (char*)d_ws, (u16*)d_out);
}
```

```cpp
#include <hip/hip_runtime.h>
#include <hip/hip_bf16.h>
#include <stdint.h>
#include <type_traits>

typedef short s16x8 __attribute__((ext_vector_type(8)));
typedef short s16x4 __attribute__((ext_vector_type(4)));
typedef float f32x4 __attribute__((ext_vector_type(4)));
typedef unsigned short u16;
typedef unsigned short u16x8 __attribute__((ext_vector_type(8)));
typedef unsigned short u16x4 __attribute__((ext_vector_type(4)));
typedef float f32x2 __attribute__((ext_vector_type(2)));
typedef __bf16 bf16x2 __attribute__((ext_vector_type(2)));
typedef unsigned u32x4v __attribute__((ext_vector_type(4)));
typedef __attribute__((address_space(1))) unsigned gu32;

constexpr int S_ = 2048, D_ = 1024, F_ = 3584;

constexpr int WS_USED = 111243392, LAST_TASKS = 100, ALLZ = 1, U0 = 170, U1 = 224, NR4 = 8, RHELP = 1, PRE1 = 1;
constexpr int OFF_KVR = 4194304, OFF_XP = 8388608, OFF_HS = 17825792, OFF_X1 = 31457280, OFF_G = 40894464, OFF_P0 = 71303168, OFF_P1 = 89128960, OFF_Y = 98566144, OFF_S1 = 106954752, OFF_ST = 107479040, OFF_FL = 109051904, OFF_FXP = 109056000, OFF_FH = 109334528, OFF_FX1 = 109613056, OFF_FG = 109891584, OFF_FP = 110587904, OFF_FKV = 111144960, OFF_FY = 111177728, OFF_EP = 111243264, OFF_DBG = 115343360;

#define WS_DIAG 0

__device__ __forceinline__ float bf2f(u16 v) { return __uint_as_float(((unsigned)v) << 16); }
__device__ __forceinline__ u16 f2bf(float f) { return __builtin_bit_cast(u16, (__bf16)f); }
__device__ __forceinline__ float rbf(float f) { return bf2f(f2bf(f)); }
__device__ __forceinline__ void st16(void* p, u16x8 v) {
  asm volatile("global_store_dwordx4 %0, %1, off sc0 sc1\n\ts_nop 1" :: "v"(p), "v"(v) : "memory");
}
#define WAIT_VM(n) asm volatile("s_waitcnt vmcnt(%0)" ::"n"(n) : "memory")
#define WAIT_LGKM0() asm volatile("s_waitcnt lgkmcnt(0)" ::: "memory")

__device__ __forceinline__ void touch_lds(const char* p, unsigned a) {
  asm volatile("s_mov_b32 m0, %1\n\ts_nop 0\n\tglobal_load_lds_dword %0, off" :: "v"(p), "s"(a) : "memory");
}
#define CTS(i)
__device__ __forceinline__ f32x2 up2(unsigned p) { return (f32x2){__uint_as_float(p << 16), __uint_as_float(p & 0xffff0000u)}; }
__device__ __forceinline__ unsigned pk2(f32x2 v) { return __builtin_bit_cast(unsigned, __builtin_convertvector(v, bf16x2)); }
__device__ __forceinline__ f32x2 rbf2(f32x2 v) { return up2(pk2(v)); }
__device__ __forceinline__ void set_flag(char* p, int i, unsigned ep) {
  if (i < 32) __hip_atomic_store((gu32*)p + i, ep, __ATOMIC_RELAXED, __HIP_MEMORY_SCOPE_AGENT);
}
constexpr int CV_HP = (2048 + 32) * 2, CV_UR = (2048 + 256) * 2, CV_CH = 2 * CV_HP + CV_UR;
constexpr int NCV = 174;
constexpr int CV_STG = 8 * CV_CH;
constexpr int YP = D_ * 16;

struct KvRegs { u32x4v k0, k1, k2, k3, k4, k5, k6, v0, v1, v2, v3, s0, s1, s2, s3; };
#define KV_LOADS                                                                                                                       \
  "v_lshrrev_b32 %[a], 3, %[tid]\n\tv_and_b32 %[c], 7, %[tid]\n\ts_lshr_b32 s90, %[tile], 4\n\ts_and_b32 s91, %[tile], 15\n\t"          \
  "v_lshlrev_b32 %[a], 13, %[a]\n\tv_lshlrev_b32 %[c], 4, %[c]\n\ts_lshl_b32 s90, s90, 18\n\ts_lshl_b32 s91, s91, 7\n\t"                 \
  "v_add_u32 %[a], s90, %[a]\n\tv_add_u32 %[c], s91, %[c]\n\tv_subrev_u32 %[h], 0x1000, %[a]\n\tv_max_i32 %[h], 0x800, %[h]\n\t"         \
  "v_add3_u32 %[a], %[a], %[c], s92\n\tv_add_u32 %[h], %[h], %[c]\n\tv_lshlrev_b32 %[c], 2, %[c]\n\ts_waitcnt lgkmcnt(0)\n\t"            \
  "global_load_dwordx4 %3, %[a], s[76:77] offset:-4096 nt\n\tglobal_load_dwordx4 %4, %[a], s[76:77] offset:-2048 nt\n\t"                \
  "global_load_dwordx4 %5, %[a], s[76:77] nt\n\tglobal_load_dwordx4 %6, %[a], s[76:77] offset:2048 nt\n\t"                              \
  "global_load_dwordx4 %7, %[a], s[78:79] offset:-4096 nt\n\tglobal_load_dwordx4 %8, %[a], s[78:79] offset:-2048 nt\n\t"                \
  "global_load_dwordx4 %9, %[a], s[78:79] nt\n\tglobal_load_dwordx4 %10, %[a], s[78:79] offset:2048 nt\n\t"                             \
  "global_load_dwordx4 %0, %[h], s[76:77] offset:-2048 nt\n\tglobal_load_dwordx4 %1, %[h], s[76:77] nt\n\t"                             \
  "global_load_dwordx4 %2, %[h], s[76:77] offset:2048 nt\n\t"                                                                           \
  "global_load_dwordx4 %11, %[c], s[80:81]\n\tglobal_load_dwordx4 %12, %[c], s[80:81] offset:16\n\t"                                    \
  "global_load_dwordx4 %13, %[c], s[80:81] offset:32\n\tglobal_load_dwordx4 %14, %[c], s[80:81] offset:48\n\t"
#define KV_OUTS(R) "=&v"(R.k0), "=&v"(R.k1), "=&v"(R.k2), "=&v"(R.k3), "=&v"(R.k4), "=&v"(R.k5), "=&v"(R.k6), "=&v"(R.v0), "=&v"(R.v1), "=&v"(R.v2), \
                   "=&v"(R.v3), "=&v"(R.s0), "=&v"(R.s1), "=&v"(R.s2), "=&v"(R.s3), [a] "=&v"(ta), [h] "=&v"(th), [c] "=&v"(tc)
#define KV_CLOB "s74", "s75", "s76", "s77", "s78", "s79", "s80", "s81", "s82", "s83", "s90", "s91", "s92", "scc", "vcc", "memory"
template <int N>
__device__ __forceinline__ void kv_wait(KvRegs& R) {
  asm volatile("s_waitcnt vmcnt(%15)" : "+v"(R.k0), "+v"(R.k1), "+v"(R.k2), "+v"(R.k3), "+v"(R.k4), "+v"(R.k5), "+v"(R.k6), "+v"(R.v0), "+v"(R.v1), "+v"(R.v2),
               "+v"(R.v3), "+v"(R.s0), "+v"(R.s1), "+v"(R.s2), "+v"(R.s3) : "n"(N) : "memory");
}
__device__ __forceinline__ void kv_store(KvRegs& R, char* __restrict__ ws, char* T, int b, int tid) {
  const int t0 = (b >> 4) * 128, c0 = (b & 15) * 64, ch8 = tid & 7, rg = tid >> 3;
  if (t0 + 4 * rg < 3) R.k0 = R.k1 = R.k2 = (u32x4v){0u, 0u, 0u, 0u};
  typedef unsigned u32x2 __attribute__((ext_vector_type(2)));
  const u32x4v kr[7] = {R.k0, R.k1, R.k2, R.k3, R.k4, R.k5, R.k6}, vr[4] = {R.v0, R.v1, R.v2, R.v3}, sk[4] = {R.s0, R.s1, R.s2, R.s3};
#pragma unroll
  for (int j2 = 0; j2 < 4; ++j2) {
    const f32x2 sa = up2(sk[j2][0]), sb = up2(sk[j2][1]), sc = up2(sk[j2][2]), sd = up2(sk[j2][3]);
    const f32x2 s0 = {sa[0], sc[0]}, s1 = {sa[1], sc[1]}, s2 = {sb[0], sd[0]}, s3 = {sb[1], sd[1]};
    f32x2 kf[7], o[4];
#pragma unroll
    for (int r = 0; r < 7; ++r) kf[r] = up2(kr[r][j2]);
#pragma unroll
    for (int r = 0; r < 4; ++r)
      o[r] = __builtin_elementwise_fma(kf[r + 3], s0, __builtin_elementwise_fma(kf[r + 2], s1, __builtin_elementwise_fma(kf[r + 1], s2, kf[r] * s3))) * up2(vr[r][j2]);
#pragma unroll
    for (int e = 0; e < 2; ++e)
      *(u32x2*)(T + (8 * ch8 + 2 * j2 + e) * 264 + (124 - 4 * rg) * 2) = (u32x2){pk2((f32x2){o[3][e], o[2][e]}), pk2((f32x2){o[1][e], o[0][e]})};
  }
  __syncthreads();
  u16* kvr = (u16*)(ws + OFF_KVR);
#pragma unroll
  for (int i = 0; i < 4; ++i) {
    const int idx = tid + 256 * i, ch = idx >> 4, cb = idx & 15;
    const u16x4 lo = *(const u16x4*)(T + ch * 264 + cb * 16), hi = *(const u16x4*)(T + ch * 264 + cb * 16 + 8);
    st16(kvr + (size_t)(c0 + ch) * S_ + (1920 - t0) + cb * 8, __builtin_shufflevector(lo, hi, 0, 1, 2, 3, 4, 5, 6, 7));
  }
}

#define LASTSLEEP 4
__device__ __forceinline__ void wait_flags(const char* p, unsigned ep) {
  gu32* f = (gu32*)p;
  for (int it = 0; it < 20000; ++it) {
    if (__all(__hip_atomic_load(f, __ATOMIC_RELAXED, __HIP_MEMORY_SCOPE_AGENT) == ep)) break;
    __builtin_amdgcn_s_sleep(LASTSLEEP);
  }
  asm volatile("" ::: "memory");
}
__device__ __forceinline__ void row_last(char* __restrict__ ws, u16* __restrict__ out, int t, int lane, int wave, unsigned ep) {
  const __amdgpu_buffer_rsrc_t rs = __builtin_amdgcn_make_buffer_rsrc(ws, 0, WS_USED, 0x00020000);
  const int r8 = lane & 7, seg = lane >> 3, un = 128 + (t >> 1);
  const unsigned fo = un * 32768 + (wave * 8 + (seg >> 2)) * 1024 + ((seg & 3) * 16 + (t & 1) * 8 + r8) * 16;
  wait_flags(ws + OFF_FP + un * 2048 + ((lane & 2) * 4 + wave * 2 + (lane & 1)) * 128, ep);
  u32x4v p0[4], p1[4];
#pragma unroll
  for (int i = 0; i < 4; ++i) {
    p0[i] = __builtin_bit_cast(u32x4v, __builtin_amdgcn_raw_buffer_load_b128(rs, fo + i * 2048, OFF_P0, 16));
    p1[i] = __builtin_bit_cast(u32x4v, __builtin_amdgcn_raw_buffer_load_b128(rs, fo + i * 2048, OFF_P1, 16));
  }
#pragma unroll
  for (int i = 0; i < 4; ++i) {
    u32x4v o;
#pragma unroll
    for (int e = 0; e < 4; ++e) o[e] = pk2(up2(p0[i][e]) + up2(p1[i][e]));
    st16(out + (size_t)(t * 8 + r8) * D_ + wave * 256 + i * 64 + seg * 8, __builtin_bit_cast(u16x8, o));
  }
}

__device__ __forceinline__ int swzB(int r) { return ((((r >> 1) & 1) | (((r >> 3) & 1) << 1)) << 1); }
#define LANE_ID(x) int x; asm volatile("v_mbcnt_lo_u32_b32 %0, -1, 0\n\tv_mbcnt_hi_u32_b32 %0, -1, %0" : "=v"(x))
#define W12 6
#define RLN 3
#define QTB 1
constexpr int QT0 = 34 * 128;
#define CODE_MAX(x, y) "(((" x ">" y ")&" x ")|((" x "<=" y ")&" y "))"
constexpr int HU = 40960;
#define G3OR 5
constexpr int NRW = 22 - G3OR;
__global__ __launch_bounds__(256, 1) void flow_kernel(const u16* __restrict__ q, const u16* __restrict__ kin, const u16* __restrict__ vin,
                                                      const u16* __restrict__ shift_k, const u16* __restrict__ ssm_k, const u16* __restrict__ ssm_D,
                                                      const u16* __restrict__ w1, const u16* __restrict__ w2,
                                                      const u16* __restrict__ w3, const u16* __restrict__ w4, char* __restrict__ ws,
                                                      u16* __restrict__ out) {
  __shared__ __attribute__((aligned(16))) char lds[163840];
  KvRegs KR;
  unsigned ew, ta, th, tc;
  int b, tile;
  asm volatile("Lcode_b:\n\ts_load_dwordx4 s[76:79], %[ka], 0x8\n\ts_load_dwordx2 s[82:83], %[ka], 0x50\n\t"
               "s_getpc_b64 s[74:75]\n\ts_lshr_b32 s90, %[bi], 3\n\ts_mov_b32 m0, 0x27f00\n\tv_lshlrev_b32 %[a], 5, %[tid]\n\tv_add_lshl_u32 %[a], %[a], s90, 7\n\t"
               "v_min_u32 %[a], " CODE_MAX(CODE_MAX(CODE_MAX("(Lcode_g3_e-Lcode_b)", "(Lcode_g3o_e-Lcode_b)"), CODE_MAX("(Lcode_g4_e-Lcode_b)", "(Lcode_g12_e-Lcode_b)")), "(Lcode_end-Lcode_b)") "-64, %[a]\n\t"
               "global_load_lds_dword %[a], s[74:75]\n\ts_load_dwordx2 s[80:81], %[ka], 0x18\n\t"
               "s_mov_b32 %[b], %[bi]\n\ts_bfe_u32 s91, %[bi], 0x20001\n\ts_lshl2_add_u32 %[tile], s90, s91\n\ts_movk_i32 s92, 0x1000\n\t"
               "s_lshl_b32 s90, %[bi], 7\n\ts_add_u32 %[tile], %[tile], s90\n\ts_add_u32 %[tile], %[tile], 64\n\ts_and_b32 %[tile], %[tile], 255\n\t"
               KV_LOADS "v_mov_b32 %[a], %[eo]\n\tglobal_load_dword %[ew], %[a], s[82:83] sc1"
               : KV_OUTS(KR), [ew] "=&v"(ew), [b] "=&s"(b), [tile] "=&s"(tile)
               : [ka] "s"(__builtin_amdgcn_kernarg_segment_ptr()), [bi] "s"(blockIdx.x), [tid] "v"(threadIdx.x), [eo] "n"(OFF_EP) : KV_CLOB);
  const unsigned t_start = 0;
  const int tid = threadIdx.x, lane = tid & 63, wave = __builtin_amdgcn_readfirstlane(tid >> 6);
  const int xcd = b & 7, idx = b >> 3;
  const unsigned sLds = (unsigned)(size_t)lds;
  const int o8 = idx & 7;
  int kind, rep, slice, kh = 0;
  int j3 = 0, j4 = 0;
  const int xp = xcd >> 1, pr = xp & 1;
  if (!(xcd & 1)) {
    if (idx < 20) { kind = 4; rep = idx < 16 ? (xp >> 1) * 3 + (idx >> 3) : 2; kh = pr; slice = idx < 16 ? o8 : (xp >> 1) * 4 + o8; j4 = rep * 16 + kh * 8 + slice; }
    else if (idx >= 24) { kind = 1; rep = xp; slice = o8; }
    else { kind = 3; rep = 4; slice = xp * 4 + idx - 20; j3 = 72 + slice; }
  } else if (idx >= 24) { kind = 2; rep = xp; slice = o8; }
  else if (idx < 18) { kind = 3; rep = pr * 2 + (idx >= 9); slice = (xp >> 1) * 9 + idx - 9 * (idx >= 9); j3 = rep * 18 + slice; }
  else if (idx < 22 || b == 191) { kind = 6; rep = 0; slice = idx < 22 ? xp * 4 + idx - 18 : 16; }
  else if (idx == 22 || xp == 0) { kind = 5; rep = idx == 22 ? pr * 2 + (xp >> 1) : 4; slice = 18; j3 = 90 + rep; }
  else { kind = 3; rep = 4; slice = 15 + xp; j3 = 72 + slice; }
  CTS(0);
  const unsigned sScr = sLds + 163584;
  unsigned ep = 0;
  auto w_touch = [&](const u16* w) {
#pragma unroll
    for (int i = 0; i < 2; ++i) touch_lds((const char*)w + (size_t)(rep * 256 + i * 128 + (tid >> 1)) * 2048 + slice * 256 + (tid & 1) * 128, sScr);
  };
  {
  const bool isconv = kind >= 3 && kind != 6 && j3 < 94;
  const int nw = (kind == 4 || j3 >= 92) ? 2 : 1, c0 = kind == 4 ? 8 * j4 : j3 < 92 ? 640 + 4 * j3 : j3 < 94 ? 272 + 8 * j3 : 0, cw = c0 + nw * wave;
  const unsigned hd = isconv ? sLds + 2 * wave * CV_CH + 32 : 0x30000u;
  auto h_dma = [&](bool first) {
#pragma unroll
    for (int j = 0; j < 2; ++j) {
      const u16* hp = ssm_k + (size_t)cw * S_ + (isconv && j < nw ? j * S_ + lane * 8 : 0);
      if (first) asm volatile("s_mov_b32 m0, %1\n\ts_nop 0\n\tglobal_load_lds_dwordx4 %0, off nt" :: "v"(hp), "s"(hd + j * CV_CH) : "memory");
      else asm volatile("s_mov_b32 m0, %1\n\ts_nop 0\n\tglobal_load_lds_dwordx4 %0, off offset:1024 nt\n\t"
                        "global_load_lds_dwordx4 %0, off offset:2048 nt\n\tglobal_load_lds_dwordx4 %0, off offset:3072 nt" :: "v"(hp), "s"(hd + j * CV_CH) : "memory");
    }
  };
  auto w_dma = [&](int t0, int t1) {
    const char* wp = (const char*)w1 + (16 * wave + (lane >> 3)) * 2048 + slice * 256;
    const unsigned c0_ = ((lane & 7) ^ swzB(lane >> 3)) * 16, c1_ = 8 * 2048 + ((lane & 7) ^ swzB(8 + (lane >> 3))) * 16;
#pragma unroll
    for (int t = t0; t < t1; ++t)
#pragma unroll
      for (int i = 0; i < 2; ++i)
        asm volatile("s_mov_b32 m0, %1\n\ts_nop 0\n\tglobal_load_lds_dwordx4 %0, off\n\ts_add_u32 m0, m0, 8064\n\ts_nop 0\n\tglobal_load_lds_dwordx4 %0, off offset:128"
                     :: "v"(wp + t * 131072 + (i ? c1_ : c0_)), "s"(sLds + t * 16384 + wave * 2048 + i * 1024) : "scc", "memory");
  };
  if (PRE1 && kind == 1) w_dma(0, 2);
  else { h_dma(true); h_dma(false); }
  if (isconv) *(u32x4v*)(lds + (2 * wave + (lane >> 5)) * CV_CH + 2 * CV_HP + 4096 + (lane & 31) * 16) = (u32x4v){0u, 0u, 0u, 0u};
  if (isconv && lane < 8) *(u32x4v*)(lds + (2 * wave + (lane >> 2)) * CV_CH + ((lane >> 1) & 1) * CV_HP + (lane & 1) * 16) = (u32x4v){0u, 0u, 0u, 0u};
  kv_wait<9>(KR);
  CTS(12);
  kv_store(KR, ws, lds + CV_STG, tile, tid);
  CTS(1);
  if (PRE1 && kind == 1) w_dma(2, 6);
  const unsigned dw = ((const unsigned*)ssm_D)[cw >> 1];
  asm volatile("s_cmp_eq_u32 %1, %2\n\ts_cbranch_scc1 Lew%=\n\ts_waitcnt vmcnt(0)\nLew%=:\n\ts_waitcnt vmcnt(16)" : "+v"(ew) : "s"(__builtin_amdgcn_readfirstlane(kind)), "n"(PRE1 ? 1 : 99) : "scc", "memory");
  ep = __builtin_amdgcn_readfirstlane(ew) + 1u;
  if (ep == 0u) ep = 1u;
  __builtin_amdgcn_s_barrier();
  set_flag(ws + OFF_FKV + tile * 128, tid, ep);
  CTS(2);
  if (kind < 3 && (W12 & kind) != 0) w_touch(kind == 1 ? w1 : w2);
  if (kind == 2 && (W12 & 4) != 0) w_touch(w1);
  if (isconv) {
    LANE_ID(lane);
    const int tid = wave * 64 + lane;
    const unsigned sC0 = (cw & 1) ? dw & 0xffff0000u : dw << 16, sC1 = dw & 0xffff0000u;
    const int l15 = lane & 15, lg = lane >> 4, cp = (l15 + 1) & 1, iw = kind == 4 ? j4 : 80 + j3, own = 2 * wave * CV_CH;
    const int ch = tid & (4 * nw - 1), tb = nw == 2 ? tid >> 3 : tid >> 2, slot = nw == 2 ? ch : 2 * ch;
    const unsigned sWave = wave, sBuf = nw, sAuxOff = OFF_KVR + cw * 4096, sFoutOff = OFF_FY + iw * 128, sExp = __builtin_amdgcn_readfirstlane(ep), sDbg = 110u * 1048576u + (b * 4 + wave) * 8192;
    const unsigned vL16 = lane * 16, vPA = own + cp * CV_HP + 2 * (8 * lg + l15 + 1 - cp), vPB = own + 2 * CV_HP + 2 * (2032 - 16 * l15 + 8 * lg) - 512 * 7;
    const unsigned vPU = own + 2 * CV_HP + 2 * (2044 - 16 * l15 - 4 * lg) - 512 * 7, vPW = 32 * l15 + 8 * lg;
    const unsigned vOR = slot * CV_CH + tb * 16, vORA = CV_STG + slot * 1024 + tb * 16, vOG = tb * YP + (c0 + ch) * 16;
    const unsigned vFA = ((lane & 3) * 16 + (c0 >> 6)) * 128, vFB = ((4 + lane % 12) * 16 + (c0 >> 6)) * 128, vFlag = tid < 32 ? tid * 4 : 0x80000000u;
    const unsigned vTW = lane < 42 ? (iw * 168 + wave * 42 + lane) * 128 : 0x80000000u;
    const unsigned vTX = lane < 44 ? ((kind == 4 ? j4 : j3 - 12) * 176 + wave * 44 + lane) * 128 : 0x80000000u;
    (void)sDbg;
    asm volatile(
R"(.macro M d,w,x
v_mfma_f32_16x16x32_bf16 v[\d:\d+3], \w, v[\x:\x+3], v[\d:\d+3]
.endm
.macro R d,a,o
ds_read_b128 v[\d:\d+3], v\a offset:\o
.endm
.macro T d,a,o
ds_read_b64_tr_b16 \d, v\a offset:\o
.endm
.macro D a,s,o
buffer_load_dwordx4 v\a, s[36:39], s\s offen offset:\o sc1 lds
.endm
.macro L c
s_waitcnt lgkmcnt(\c)
.endm
Lcode_cv_b:
s_mov_b64 s[36:37],%0
s_mov_b32 s38,111243392
s_mov_b32 s39,0x00020000
s_mov_b32 s44,%1
s_mov_b32 s51,%2
s_mov_b32 s49,%3
s_mov_b32 s48,%4
s_mov_b32 s86,%5
s_mov_b32 s87,%6
s_mov_b32 s53,%7
v_mov_b32 v0,%8
v_mov_b32 v1,%9
v_mov_b32 v2,%10
v_mov_b32 v3,%11
v_mov_b32 v4,%12
v_mov_b32 v5,%13
v_mov_b32 v6,%14
v_mov_b32 v7,%15
v_mov_b32 v8,%16
v_mov_b32 v9,%17
v_mov_b32 v10,%18
v_mov_b32 v11,%19
v_mov_b32 v12,%20
s_mov_b32 s58,111144960
buffer_load_dword v13,v8,s[36:39],s58 offen offset:0 sc1
s_mul_i32 s57,s44,25856
v_add_u32 v18,s57,v0
v_add_u32 v18,32,v18
v_mov_b32 v19,s57
R 88,18,0
ds_read_b32 v92,v18 offset:16
R 152,18,12928
ds_read_b32 v156,v18 offset:12944
ds_read_b32 v24,v19 offset:32
ds_read_b32 v25,v19 offset:12960
s_mov_b64 s[40:41],%21
s_mov_b32 s42,7340032
s_mov_b32 s43,0x00020000
s_mov_b64 s[60:61],%22
s_mov_b32 s62,7340032
s_mov_b32 s63,0x00020000
v_mov_b32 v23,s53
s_mov_b32 s82,0
s_add_u32 s55,s49,4096
s_cmp_eq_u32 s51,2
s_cselect_b32 s55,s55,0x7fff0000
s_cselect_b32 s56,0,0x70000000
s_add_u32 s57,s57,8320
v_add_u32 v14,1024,v1
v_add_u32 v15,2048,v1
v_add_u32 v16,3072,v1
v_add_u32 v17,12928,v1
L 4
v_alignbyte_b32 v96,v89,v88,2
v_alignbyte_b32 v97,v90,v89,2
v_alignbyte_b32 v98,v91,v90,2
v_alignbyte_b32 v99,v92,v91,2
ds_write_b128 v18,v[96:99] offset:4160
L 3
v_alignbyte_b32 v160,v153,v152,2
v_alignbyte_b32 v161,v154,v153,2
v_alignbyte_b32 v162,v155,v154,2
v_alignbyte_b32 v163,v156,v155,2
ds_write_b128 v18,v[160:163] offset:17088
L 2
v_lshlrev_b32 v24,16,v24
v_lshlrev_b32 v25,16,v25
s_mov_b64 exec,1
ds_write_b32 v19,v24 offset:4188
ds_write_b32 v19,v25 offset:17116
s_mov_b64 exec,-1
ds_read2_b32 a[0:1],v1 offset0:0 offset1:1
ds_read2_b32 a[2:3],v1 offset0:2 offset1:3
ds_read2_b32 a[4:5],v1 offset0:128 offset1:129
ds_read2_b32 a[6:7],v1 offset0:130 offset1:131
ds_read2_b32 a[8:9],v1 offset0:16 offset1:17
ds_read2_b32 a[10:11],v1 offset0:18 offset1:19
ds_read2_b32 a[12:13],v1 offset0:144 offset1:145
ds_read2_b32 a[14:15],v1 offset0:146 offset1:147
ds_read2_b32 a[16:17],v1 offset0:32 offset1:33
ds_read2_b32 a[18:19],v1 offset0:34 offset1:35
ds_read2_b32 a[20:21],v1 offset0:160 offset1:161
ds_read2_b32 a[22:23],v1 offset0:162 offset1:163
ds_read2_b32 a[24:25],v1 offset0:48 offset1:49
ds_read2_b32 a[26:27],v1 offset0:50 offset1:51
ds_read2_b32 a[28:29],v1 offset0:176 offset1:177
ds_read2_b32 a[30:31],v1 offset0:178 offset1:179
ds_read2_b32 a[32:33],v1 offset0:64 offset1:65
ds_read2_b32 a[34:35],v1 offset0:66 offset1:67
ds_read2_b32 a[36:37],v1 offset0:192 offset1:193
ds_read2_b32 a[38:39],v1 offset0:194 offset1:195
ds_read2_b32 a[40:41],v1 offset0:80 offset1:81
ds_read2_b32 a[42:43],v1 offset0:82 offset1:83
ds_read2_b32 a[44:45],v1 offset0:208 offset1:209
ds_read2_b32 a[46:47],v1 offset0:210 offset1:211
ds_read2_b32 a[48:49],v1 offset0:96 offset1:97
ds_read2_b32 a[50:51],v1 offset0:98 offset1:99
ds_read2_b32 a[52:53],v1 offset0:224 offset1:225
ds_read2_b32 a[54:55],v1 offset0:226 offset1:227
ds_read2_b32 a[56:57],v1 offset0:112 offset1:113
ds_read2_b32 a[58:59],v1 offset0:114 offset1:115
ds_read2_b32 a[60:61],v1 offset0:240 offset1:241
ds_read2_b32 a[62:63],v1 offset0:242 offset1:243
ds_read2_b32 a[64:65],v17 offset0:0 offset1:1
ds_read2_b32 a[66:67],v17 offset0:2 offset1:3
ds_read2_b32 a[68:69],v17 offset0:128 offset1:129
ds_read2_b32 a[70:71],v17 offset0:130 offset1:131
ds_read2_b32 a[72:73],v17 offset0:16 offset1:17
ds_read2_b32 a[74:75],v17 offset0:18 offset1:19
ds_read2_b32 a[76:77],v17 offset0:144 offset1:145
ds_read2_b32 a[78:79],v17 offset0:146 offset1:147
ds_read2_b32 a[80:81],v17 offset0:32 offset1:33
ds_read2_b32 a[82:83],v17 offset0:34 offset1:35
ds_read2_b32 a[84:85],v17 offset0:160 offset1:161
ds_read2_b32 a[86:87],v17 offset0:162 offset1:163
ds_read2_b32 a[88:89],v17 offset0:48 offset1:49
ds_read2_b32 a[90:91],v17 offset0:50 offset1:51
ds_read2_b32 a[92:93],v17 offset0:176 offset1:177
ds_read2_b32 a[94:95],v17 offset0:178 offset1:179
ds_read2_b32 a[96:97],v17 offset0:64 offset1:65
ds_read2_b32 a[98:99],v17 offset0:66 offset1:67
ds_read2_b32 a[100:101],v17 offset0:192 offset1:193
ds_read2_b32 a[102:103],v17 offset0:194 offset1:195
ds_read2_b32 a[104:105],v17 offset0:80 offset1:81
ds_read2_b32 a[106:107],v17 offset0:82 offset1:83
ds_read2_b32 a[108:109],v17 offset0:208 offset1:209
ds_read2_b32 a[110:111],v17 offset0:210 offset1:211
ds_read2_b32 a[112:113],v17 offset0:96 offset1:97
ds_read2_b32 a[114:115],v17 offset0:98 offset1:99
ds_read2_b32 a[116:117],v17 offset0:224 offset1:225
ds_read2_b32 a[118:119],v17 offset0:226 offset1:227
ds_read2_b32 a[120:121],v17 offset0:112 offset1:113
ds_read2_b32 a[122:123],v17 offset0:114 offset1:115
ds_read2_b32 a[124:125],v17 offset0:240 offset1:241
ds_read2_b32 a[126:127],v17 offset0:242 offset1:243
Lcv_bpa_%=:
s_waitcnt vmcnt(0)
v_cmp_eq_u32 vcc,s53,v13
s_add_u32 s82,s82,1
s_cmp_eq_u64 vcc,-1
s_cbranch_scc1 Lcv_bqa_%=
s_sleep 1
buffer_load_dword v13,v8,s[36:39],s58 offen offset:0 sc1
s_cmp_lt_u32 s82,20000
s_cbranch_scc1 Lcv_bpa_%=
s_waitcnt vmcnt(0)
Lcv_bqa_%=:
s_add_u32 m0,s57,0
s_nop 0
D 0,49,3072
s_add_u32 m0,s57,12928
s_nop 0
D 0,55,3072
buffer_load_dword v13,v9,s[36:39],s58 offen offset:0 sc1
R 88,18,0
ds_read_b32 v92,v18 offset:16
R 104,18,1024
ds_read_b32 v108,v18 offset:1040
R 120,18,2048
ds_read_b32 v124,v18 offset:2064
R 136,18,3072
ds_read_b32 v140,v18 offset:3088
L 6
v_alignbyte_b32 v96,v89,v88,2
v_alignbyte_b32 v97,v90,v89,2
v_alignbyte_b32 v98,v91,v90,2
v_alignbyte_b32 v99,v92,v91,2
ds_write_b128 v18,v[96:99] offset:4160
L 5
v_alignbyte_b32 v112,v105,v104,2
v_alignbyte_b32 v113,v106,v105,2
v_alignbyte_b32 v114,v107,v106,2
v_alignbyte_b32 v115,v108,v107,2
ds_write_b128 v18,v[112:115] offset:5184
L 4
v_alignbyte_b32 v128,v121,v120,2
v_alignbyte_b32 v129,v122,v121,2
v_alignbyte_b32 v130,v123,v122,2
v_alignbyte_b32 v131,v124,v123,2
ds_write_b128 v18,v[128:131] offset:6208
L 3
v_alignbyte_b32 v144,v137,v136,2
v_alignbyte_b32 v145,v138,v137,2
v_alignbyte_b32 v146,v139,v138,2
v_alignbyte_b32 v147,v140,v139,2
ds_write_b128 v18,v[144:147] offset:7232
R 152,18,12928
ds_read_b32 v156,v18 offset:12944
R 168,18,13952
ds_read_b32 v172,v18 offset:13968
R 184,18,14976
ds_read_b32 v188,v18 offset:14992
R 200,18,16000
ds_read_b32 v204,v18 offset:16016
L 6
v_alignbyte_b32 v160,v153,v152,2
v_alignbyte_b32 v161,v154,v153,2
v_alignbyte_b32 v162,v155,v154,2
v_alignbyte_b32 v163,v156,v155,2
ds_write_b128 v18,v[160:163] offset:17088
L 5
v_alignbyte_b32 v176,v169,v168,2
v_alignbyte_b32 v177,v170,v169,2
v_alignbyte_b32 v178,v171,v170,2
v_alignbyte_b32 v179,v172,v171,2
ds_write_b128 v18,v[176:179] offset:18112
L 4
v_alignbyte_b32 v192,v185,v184,2
v_alignbyte_b32 v193,v186,v185,2
v_alignbyte_b32 v194,v187,v186,2
v_alignbyte_b32 v195,v188,v187,2
ds_write_b128 v18,v[192:195] offset:19136
L 3
v_alignbyte_b32 v208,v201,v200,2
v_alignbyte_b32 v209,v202,v201,2
v_alignbyte_b32 v210,v203,v202,2
v_alignbyte_b32 v211,v204,v203,2
ds_write_b128 v18,v[208:211] offset:20160
s_lshl_b32 s78,s44,11
s_add_u32 s78,s78,103424
v_add_u32 v18,s78,v4
v_add_u32 v4,v19,v4
s_waitcnt vmcnt(0)
v_cmp_eq_u32 vcc,s53,v13
s_mov_b32 s83,0
s_cmp_eq_u64 vcc,-1
s_cbranch_scc0 Lcv_a_%=
s_mov_b32 s83,1
s_add_u32 m0,s57,0
s_nop 0
D 0,49,2048
D 0,49,1024
D 0,49,0
s_add_u32 m0,s57,12928
s_nop 0
D 0,55,2048
D 0,55,1024
D 0,55,0
Lcv_a_%=:
s_cmp_eq_u32 s51,2
s_cbranch_scc1 Lcv_a2_%=
L 0
R 88,2,3584
R 92,2,3072
R 96,2,3648
R 100,2,3136
R 104,2,3712
R 108,2,3200
R 112,2,3776
R 116,2,3264
L 6
v_mfma_f32_16x16x32_bf16 v[40:43],a[0:3],v[88:91],0
R 120,2,3840
R 124,2,3328
v_mfma_f32_16x16x32_bf16 v[44:47],a[0:3],v[92:95],0
M 44,a[4:7],88
L 6
M 40,a[8:11],96
R 128,2,3904
R 132,2,3392
M 44,a[8:11],100
M 44,a[12:15],96
L 6
M 40,a[16:19],104
R 136,2,3968
R 140,2,3456
M 44,a[16:19],108
M 44,a[20:23],104
L 6
M 40,a[24:27],112
R 144,2,4032
R 148,2,3520
M 44,a[24:27],116
M 44,a[28:31],112
L 6
M 40,a[32:35],120
M 44,a[32:35],124
M 44,a[36:39],120
L 4
M 40,a[40:43],128
M 44,a[40:43],132
M 44,a[44:47],128
L 2
M 40,a[48:51],136
ds_read_b64 v[24:25],v3 offset:3584
ds_read_b64 v[26:27],v3 offset:3072
M 44,a[48:51],140
M 44,a[52:55],136
L 2
M 40,a[56:59],144
M 44,a[56:59],148
M 44,a[60:63],144
L 0
v_and_b32 v36,0xffff0000,v25
v_lshlrev_b32 v37,16,v25
v_and_b32 v38,0xffff0000,v24
v_lshlrev_b32 v39,16,v24
s_nop 0
v_fma_f32 v40,v36,s86,v40
v_fma_f32 v41,v37,s86,v41
v_fma_f32 v42,v38,s86,v42
v_fma_f32 v43,v39,s86,v43
v_cvt_pk_bf16_f32 v40,v40,v41
v_cvt_pk_bf16_f32 v41,v42,v43
ds_write_b64 v18,v[40:41] offset:0
v_and_b32 v36,0xffff0000,v27
v_lshlrev_b32 v37,16,v27
v_and_b32 v38,0xffff0000,v26
v_lshlrev_b32 v39,16,v26
v_fma_f32 v44,v36,s86,v44
v_fma_f32 v45,v37,s86,v45
v_fma_f32 v46,v38,s86,v46
v_fma_f32 v47,v39,s86,v47
v_cvt_pk_bf16_f32 v44,v44,v45
v_cvt_pk_bf16_f32 v45,v46,v47
ds_write_b64 v18,v[44:45] offset:512
s_branch Lcv_a3_%=
Lcv_a2_%=:
L 0
R 88,2,3584
R 92,2,3072
R 96,2,3648
R 100,2,3136
R 104,2,3712
R 108,2,3200
R 112,2,3776
R 116,2,3264
L 6
v_mfma_f32_16x16x32_bf16 v[40:43],a[0:3],v[88:91],0
R 120,2,3840
R 124,2,3328
v_mfma_f32_16x16x32_bf16 v[44:47],a[0:3],v[92:95],0
M 44,a[4:7],88
L 6
M 40,a[8:11],96
R 128,2,3904
R 132,2,3392
M 44,a[8:11],100
M 44,a[12:15],96
L 6
M 40,a[16:19],104
R 136,2,3968
R 140,2,3456
M 44,a[16:19],108
M 44,a[20:23],104
L 6
M 40,a[24:27],112
R 144,2,4032
R 148,2,3520
M 44,a[24:27],116
M 44,a[28:31],112
L 6
M 40,a[32:35],120
R 152,2,16512
R 156,2,16000
M 44,a[32:35],124
M 44,a[36:39],120
L 6
M 40,a[40:43],128
R 160,2,16576
R 164,2,16064
M 44,a[40:43],132
M 44,a[44:47],128
L 6
M 40,a[48:51],136
R 168,2,16640
R 172,2,16128
M 44,a[48:51],140
ds_read_b64 v[24:25],v3 offset:3584
ds_read_b64 v[26:27],v3 offset:3072
M 44,a[52:55],136
L 8
M 40,a[56:59],144
R 176,2,16704
R 180,2,16192
M 44,a[56:59],148
M 44,a[60:63],144
L 8
v_mfma_f32_16x16x32_bf16 v[48:51],a[64:67],v[152:155],0
R 184,2,16768
R 188,2,16256
v_mfma_f32_16x16x32_bf16 v[52:55],a[64:67],v[156:159],0
M 52,a[68:71],152
L 8
M 48,a[72:75],160
R 192,2,16832
R 196,2,16320
M 52,a[72:75],164
L 6
v_and_b32 v36,0xffff0000,v25
M 52,a[76:79],160
v_lshlrev_b32 v37,16,v25
v_and_b32 v38,0xffff0000,v24
v_lshlrev_b32 v39,16,v24
s_nop 0
M 48,a[80:83],168
R 200,2,16896
R 204,2,16384
M 52,a[80:83],172
v_fma_f32 v40,v36,s86,v40
v_fma_f32 v41,v37,s86,v41
M 52,a[84:87],168
v_fma_f32 v42,v38,s86,v42
v_fma_f32 v43,v39,s86,v43
v_cvt_pk_bf16_f32 v40,v40,v41
L 6
M 48,a[88:91],176
R 208,2,16960
R 212,2,16448
M 52,a[88:91],180
v_cvt_pk_bf16_f32 v41,v42,v43
ds_write_b64 v18,v[40:41] offset:0
M 52,a[92:95],176
v_and_b32 v36,0xffff0000,v27
v_lshlrev_b32 v37,16,v27
v_and_b32 v38,0xffff0000,v26
L 7
M 48,a[96:99],184
M 52,a[96:99],188
v_lshlrev_b32 v39,16,v26
v_fma_f32 v44,v36,s86,v44
M 52,a[100:103],184
v_fma_f32 v45,v37,s86,v45
v_fma_f32 v46,v38,s86,v46
v_fma_f32 v47,v39,s86,v47
L 5
M 48,a[104:107],192
M 52,a[104:107],196
v_cvt_pk_bf16_f32 v44,v44,v45
v_cvt_pk_bf16_f32 v45,v46,v47
M 52,a[108:111],192
ds_write_b64 v18,v[44:45] offset:512
L 4
M 48,a[112:115],200
ds_read_b64 v[24:25],v3 offset:16512
ds_read_b64 v[26:27],v3 offset:16000
M 52,a[112:115],204
M 52,a[116:119],200
L 4
M 48,a[120:123],208
M 52,a[120:123],212
M 52,a[124:127],208
L 0
v_and_b32 v36,0xffff0000,v25
v_lshlrev_b32 v37,16,v25
v_and_b32 v38,0xffff0000,v24
v_lshlrev_b32 v39,16,v24
s_nop 0
v_fma_f32 v48,v36,s87,v48
v_fma_f32 v49,v37,s87,v49
v_fma_f32 v50,v38,s87,v50
v_fma_f32 v51,v39,s87,v51
v_cvt_pk_bf16_f32 v48,v48,v49
v_cvt_pk_bf16_f32 v49,v50,v51
ds_write_b64 v18,v[48:49] offset:1024
v_and_b32 v36,0xffff0000,v27
v_lshlrev_b32 v37,16,v27
v_and_b32 v38,0xffff0000,v26
v_lshlrev_b32 v39,16,v26
v_fma_f32 v52,v36,s87,v52
v_fma_f32 v53,v37,s87,v53
v_fma_f32 v54,v38,s87,v54
v_fma_f32 v55,v39,s87,v55
v_cvt_pk_bf16_f32 v52,v52,v53
v_cvt_pk_bf16_f32 v53,v54,v55
ds_write_b64 v18,v[52:53] offset:1536
Lcv_a3_%=:
L 0
s_barrier
R 152,6,0
R 156,6,512
s_mov_b32 s78,98566144
L 1
buffer_store_dwordx4 v[152:155],v7,s[36:39],s78 offen offset:0 sc0 sc1
s_add_u32 s78,s56,99090432
L 0
buffer_store_dwordx4 v[156:159],v7,s[36:39],s78 offen offset:0 sc0 sc1
s_cmp_eq_u32 s83,1
s_cbranch_scc1 Lcv_b_%=
s_waitcnt vmcnt(0)
s_barrier
buffer_store_dword v23,v10,s[36:39],s48 offen offset:0 sc0 sc1
buffer_load_dword v13,v9,s[36:39],s58 offen offset:0 sc1
Lcv_bpb_%=:
)"
R"(s_waitcnt vmcnt(0)
v_cmp_eq_u32 vcc,s53,v13
s_add_u32 s82,s82,1
s_cmp_eq_u64 vcc,-1
s_cbranch_scc1 Lcv_bqb_%=
s_sleep 1
buffer_load_dword v13,v9,s[36:39],s58 offen offset:0 sc1
s_cmp_lt_u32 s82,20000
s_cbranch_scc1 Lcv_bpb_%=
s_waitcnt vmcnt(0)
Lcv_bqb_%=:
s_add_u32 m0,s57,0
s_nop 0
D 0,49,2048
D 0,49,1024
D 0,49,0
s_add_u32 m0,s57,12928
s_nop 0
D 0,55,2048
D 0,55,1024
D 0,55,0
s_waitcnt vmcnt(0)
Lcv_b_%=:
s_waitcnt vmcnt(2)
ds_read2_b32 v[88:89],v1 offset0:0 offset1:1
ds_read2_b32 v[90:91],v1 offset0:2 offset1:3
R 120,2,3584
ds_read2_b32 v[92:93],v1 offset0:128 offset1:129
ds_read2_b32 v[94:95],v1 offset0:130 offset1:131
R 124,2,3072
ds_read2_b32 v[96:97],v14 offset0:0 offset1:1
ds_read2_b32 v[98:99],v14 offset0:2 offset1:3
R 128,2,2560
ds_read2_b32 v[100:101],v14 offset0:128 offset1:129
ds_read2_b32 v[102:103],v14 offset0:130 offset1:131
R 132,2,2048
ds_read2_b32 v[104:105],v15 offset0:0 offset1:1
ds_read2_b32 v[106:107],v15 offset0:2 offset1:3
R 136,2,1536
ds_read2_b32 v[108:109],v15 offset0:128 offset1:129
ds_read2_b32 v[110:111],v15 offset0:130 offset1:131
R 140,2,1024
ds_read2_b32 v[112:113],v16 offset0:0 offset1:1
ds_read2_b32 v[114:115],v16 offset0:2 offset1:3
R 144,2,512
ds_read2_b32 v[116:117],v16 offset0:128 offset1:129
ds_read2_b32 v[118:119],v16 offset0:130 offset1:131
R 148,2,0
L 15
v_mfma_f32_16x16x32_bf16 v[40:43],v[88:91],v[128:131],0
ds_read2_b32 v[152:153],v1 offset0:16 offset1:17
M 40,v[92:95],124
ds_read2_b32 v[154:155],v1 offset0:18 offset1:19
M 40,v[96:99],120
L 14
R 184,2,3648
v_mfma_f32_16x16x32_bf16 v[44:47],v[88:91],v[132:135],0
ds_read2_b32 v[156:157],v1 offset0:144 offset1:145
M 44,v[92:95],128
M 44,v[96:99],124
ds_read2_b32 v[158:159],v1 offset0:146 offset1:147
M 44,v[100:103],120
L 14
R 188,2,3136
v_mfma_f32_16x16x32_bf16 v[48:51],v[88:91],v[136:139],0
ds_read2_b32 v[160:161],v14 offset0:16 offset1:17
M 48,v[92:95],132
ds_read2_b32 v[162:163],v14 offset0:18 offset1:19
M 48,v[96:99],128
M 48,v[100:103],124
R 192,2,2624
M 48,v[104:107],120
L 15
ds_read2_b32 v[164:165],v14 offset0:144 offset1:145
v_mfma_f32_16x16x32_bf16 v[52:55],v[88:91],v[140:143],0
ds_read2_b32 v[166:167],v14 offset0:146 offset1:147
M 52,v[92:95],136
R 196,2,2112
M 52,v[96:99],132
M 52,v[100:103],128
ds_read2_b32 v[168:169],v15 offset0:16 offset1:17
M 52,v[104:107],124
ds_read2_b32 v[170:171],v15 offset0:18 offset1:19
M 52,v[108:111],120
L 15
R 200,2,1600
v_mfma_f32_16x16x32_bf16 v[56:59],v[88:91],v[144:147],0
ds_read2_b32 v[172:173],v15 offset0:144 offset1:145
M 56,v[92:95],140
M 56,v[96:99],136
ds_read2_b32 v[174:175],v15 offset0:146 offset1:147
M 56,v[100:103],132
R 204,2,1088
M 56,v[104:107],128
ds_read2_b32 v[176:177],v16 offset0:16 offset1:17
M 56,v[108:111],124
ds_read2_b32 v[178:179],v16 offset0:18 offset1:19
M 56,v[112:115],120
L 15
v_mfma_f32_16x16x32_bf16 v[60:63],v[88:91],v[148:151],0
R 208,2,576
M 60,v[92:95],144
ds_read2_b32 v[180:181],v16 offset0:144 offset1:145
M 60,v[96:99],140
ds_read2_b32 v[182:183],v16 offset0:146 offset1:147
M 60,v[100:103],136
R 212,2,64
M 60,v[104:107],132
s_cmp_eq_u32 s83,0
s_cbranch_scc1 Lcv_c_%=
s_waitcnt vmcnt(0)
s_barrier
buffer_store_dword v23,v10,s[36:39],s48 offen offset:0 sc0 sc1
Lcv_c_%=:
M 60,v[108:111],128
M 60,v[112:115],124
M 60,v[116:119],120
L 15
M 40,v[152:155],192
ds_read2_b32 v[88:89],v1 offset0:32 offset1:33
M 40,v[156:159],188
ds_read2_b32 v[90:91],v1 offset0:34 offset1:35
M 40,v[160:163],184
L 14
R 120,2,3712
M 44,v[152:155],196
ds_read2_b32 v[92:93],v1 offset0:160 offset1:161
M 44,v[156:159],192
M 44,v[160:163],188
ds_read2_b32 v[94:95],v1 offset0:162 offset1:163
M 44,v[164:167],184
L 14
R 124,2,3200
M 48,v[152:155],200
ds_read2_b32 v[96:97],v14 offset0:32 offset1:33
M 48,v[156:159],196
ds_read2_b32 v[98:99],v14 offset0:34 offset1:35
M 48,v[160:163],192
M 48,v[164:167],188
R 128,2,2688
M 48,v[168:171],184
L 15
ds_read2_b32 v[100:101],v14 offset0:160 offset1:161
M 52,v[152:155],204
ds_read2_b32 v[102:103],v14 offset0:162 offset1:163
M 52,v[156:159],200
R 132,2,2176
M 52,v[160:163],196
M 52,v[164:167],192
ds_read2_b32 v[104:105],v15 offset0:32 offset1:33
M 52,v[168:171],188
ds_read2_b32 v[106:107],v15 offset0:34 offset1:35
M 52,v[172:175],184
L 15
R 136,2,1664
M 56,v[152:155],208
ds_read2_b32 v[108:109],v15 offset0:160 offset1:161
M 56,v[156:159],204
M 56,v[160:163],200
ds_read2_b32 v[110:111],v15 offset0:162 offset1:163
M 56,v[164:167],196
R 140,2,1152
M 56,v[168:171],192
ds_read2_b32 v[112:113],v16 offset0:32 offset1:33
M 56,v[172:175],188
ds_read2_b32 v[114:115],v16 offset0:34 offset1:35
M 56,v[176:179],184
L 15
M 60,v[152:155],212
R 144,2,640
M 60,v[156:159],208
ds_read2_b32 v[116:117],v16 offset0:160 offset1:161
M 60,v[160:163],204
ds_read2_b32 v[118:119],v16 offset0:162 offset1:163
M 60,v[164:167],200
R 148,2,128
M 60,v[168:171],196
M 60,v[172:175],192
M 60,v[176:179],188
M 60,v[180:183],184
L 15
M 40,v[88:91],128
ds_read2_b32 v[152:153],v1 offset0:48 offset1:49
M 40,v[92:95],124
ds_read2_b32 v[154:155],v1 offset0:50 offset1:51
M 40,v[96:99],120
L 14
R 184,2,3776
M 44,v[88:91],132
ds_read2_b32 v[156:157],v1 offset0:176 offset1:177
M 44,v[92:95],128
M 44,v[96:99],124
ds_read2_b32 v[158:159],v1 offset0:178 offset1:179
M 44,v[100:103],120
L 14
R 188,2,3264
M 48,v[88:91],136
ds_read2_b32 v[160:161],v14 offset0:48 offset1:49
M 48,v[92:95],132
ds_read2_b32 v[162:163],v14 offset0:50 offset1:51
M 48,v[96:99],128
M 48,v[100:103],124
R 192,2,2752
M 48,v[104:107],120
L 15
ds_read2_b32 v[164:165],v14 offset0:176 offset1:177
M 52,v[88:91],140
s_mov_b32 m0,163584
s_mov_b32 s78,0
ds_read2_b32 v[166:167],v14 offset0:178 offset1:179
M 52,v[92:95],136
R 196,2,2240
M 52,v[96:99],132
buffer_load_dword v11,s[40:43],s78 offen offset:0  lds
M 52,v[100:103],128
ds_read2_b32 v[168:169],v15 offset0:48 offset1:49
M 52,v[104:107],124
ds_read2_b32 v[170:171],v15 offset0:50 offset1:51
M 52,v[108:111],120
L 15
R 200,2,1728
M 56,v[88:91],144
ds_read2_b32 v[172:173],v15 offset0:176 offset1:177
M 56,v[92:95],140
M 56,v[96:99],136
ds_read2_b32 v[174:175],v15 offset0:178 offset1:179
M 56,v[100:103],132
R 204,2,1216
M 56,v[104:107],128
ds_read2_b32 v[176:177],v16 offset0:48 offset1:49
M 56,v[108:111],124
ds_read2_b32 v[178:179],v16 offset0:50 offset1:51
M 56,v[112:115],120
L 15
M 60,v[88:91],148
R 208,2,704
M 60,v[92:95],144
ds_read2_b32 v[180:181],v16 offset0:176 offset1:177
M 60,v[96:99],140
ds_read2_b32 v[182:183],v16 offset0:178 offset1:179
M 60,v[100:103],136
R 212,2,192
M 60,v[104:107],132
M 60,v[108:111],128
M 60,v[112:115],124
M 60,v[116:119],120
L 15
M 40,v[152:155],192
ds_read2_b32 v[88:89],v1 offset0:64 offset1:65
M 40,v[156:159],188
ds_read2_b32 v[90:91],v1 offset0:66 offset1:67
M 40,v[160:163],184
L 14
R 120,2,3840
M 44,v[152:155],196
ds_read2_b32 v[92:93],v1 offset0:192 offset1:193
M 44,v[156:159],192
M 44,v[160:163],188
ds_read2_b32 v[94:95],v1 offset0:194 offset1:195
M 44,v[164:167],184
L 14
R 124,2,3328
M 48,v[152:155],200
ds_read2_b32 v[96:97],v14 offset0:64 offset1:65
M 48,v[156:159],196
ds_read2_b32 v[98:99],v14 offset0:66 offset1:67
M 48,v[160:163],192
M 48,v[164:167],188
R 128,2,2816
M 48,v[168:171],184
L 15
ds_read2_b32 v[100:101],v14 offset0:192 offset1:193
M 52,v[152:155],204
ds_read2_b32 v[102:103],v14 offset0:194 offset1:195
M 52,v[156:159],200
R 132,2,2304
M 52,v[160:163],196
M 52,v[164:167],192
ds_read2_b32 v[104:105],v15 offset0:64 offset1:65
M 52,v[168:171],188
ds_read2_b32 v[106:107],v15 offset0:66 offset1:67
M 52,v[172:175],184
L 15
R 136,2,1792
M 56,v[152:155],208
ds_read2_b32 v[108:109],v15 offset0:192 offset1:193
M 56,v[156:159],204
M 56,v[160:163],200
ds_read2_b32 v[110:111],v15 offset0:194 offset1:195
M 56,v[164:167],196
R 140,2,1280
M 56,v[168:171],192
ds_read2_b32 v[112:113],v16 offset0:64 offset1:65
M 56,v[172:175],188
ds_read2_b32 v[114:115],v16 offset0:66 offset1:67
M 56,v[176:179],184
L 15
M 60,v[152:155],212
R 144,2,768
M 60,v[156:159],208
ds_read2_b32 v[116:117],v16 offset0:192 offset1:193
M 60,v[160:163],204
ds_read2_b32 v[118:119],v16 offset0:194 offset1:195
M 60,v[164:167],200
R 148,2,256
M 60,v[168:171],196
M 60,v[172:175],192
M 60,v[176:179],188
M 60,v[180:183],184
L 15
M 40,v[88:91],128
ds_read2_b32 v[152:153],v1 offset0:80 offset1:81
M 40,v[92:95],124
ds_read2_b32 v[154:155],v1 offset0:82 offset1:83
M 40,v[96:99],120
L 14
R 184,2,3904
M 44,v[88:91],132
ds_read2_b32 v[156:157],v1 offset0:208 offset1:209
M 44,v[92:95],128
M 44,v[96:99],124
ds_read2_b32 v[158:159],v1 offset0:210 offset1:211
M 44,v[100:103],120
L 14
R 188,2,3392
M 48,v[88:91],136
ds_read2_b32 v[160:161],v14 offset0:80 offset1:81
M 48,v[92:95],132
ds_read2_b32 v[162:163],v14 offset0:82 offset1:83
M 48,v[96:99],128
M 48,v[100:103],124
R 192,2,2880
M 48,v[104:107],120
L 15
ds_read2_b32 v[164:165],v14 offset0:208 offset1:209
M 52,v[88:91],140
ds_read2_b32 v[166:167],v14 offset0:210 offset1:211
M 52,v[92:95],136
R 196,2,2368
M 52,v[96:99],132
M 52,v[100:103],128
ds_read2_b32 v[168:169],v15 offset0:80 offset1:81
M 52,v[104:107],124
ds_read2_b32 v[170:171],v15 offset0:82 offset1:83
M 52,v[108:111],120
L 15
R 200,2,1856
M 56,v[88:91],144
ds_read2_b32 v[172:173],v15 offset0:208 offset1:209
M 56,v[92:95],140
M 56,v[96:99],136
ds_read2_b32 v[174:175],v15 offset0:210 offset1:211
M 56,v[100:103],132
R 204,2,1344
M 56,v[104:107],128
ds_read2_b32 v[176:177],v16 offset0:80 offset1:81
M 56,v[108:111],124
ds_read2_b32 v[178:179],v16 offset0:82 offset1:83
M 56,v[112:115],120
L 15
M 60,v[88:91],148
R 208,2,832
M 60,v[92:95],144
ds_read2_b32 v[180:181],v16 offset0:208 offset1:209
M 60,v[96:99],140
ds_read2_b32 v[182:183],v16 offset0:210 offset1:211
M 60,v[100:103],136
R 212,2,320
M 60,v[104:107],132
M 60,v[108:111],128
M 60,v[112:115],124
M 60,v[116:119],120
L 15
M 40,v[152:155],192
ds_read2_b32 v[88:89],v1 offset0:96 offset1:97
M 40,v[156:159],188
ds_read2_b32 v[90:91],v1 offset0:98 offset1:99
M 40,v[160:163],184
L 14
R 120,2,3968
M 44,v[152:155],196
s_mov_b32 m0,163584
s_mov_b32 s78,3741696
ds_read2_b32 v[92:93],v1 offset0:224 offset1:225
M 44,v[156:159],192
M 44,v[160:163],188
ds_read2_b32 v[94:95],v1 offset0:226 offset1:227
M 44,v[164:167],184
L 14
R 124,2,3456
M 48,v[152:155],200
ds_read2_b32 v[96:97],v14 offset0:96 offset1:97
M 48,v[156:159],196
ds_read2_b32 v[98:99],v14 offset0:98 offset1:99
M 48,v[160:163],192
buffer_load_dword v11,s[40:43],s78 offen offset:0  lds
M 48,v[164:167],188
R 128,2,2944
M 48,v[168:171],184
L 15
ds_read2_b32 v[100:101],v14 offset0:224 offset1:225
M 52,v[152:155],204
ds_read2_b32 v[102:103],v14 offset0:226 offset1:227
M 52,v[156:159],200
R 132,2,2432
M 52,v[160:163],196
M 52,v[164:167],192
ds_read2_b32 v[104:105],v15 offset0:96 offset1:97
M 52,v[168:171],188
ds_read2_b32 v[106:107],v15 offset0:98 offset1:99
M 52,v[172:175],184
L 15
R 136,2,1920
M 56,v[152:155],208
ds_read2_b32 v[108:109],v15 offset0:224 offset1:225
M 56,v[156:159],204
M 56,v[160:163],200
ds_read2_b32 v[110:111],v15 offset0:226 offset1:227
M 56,v[164:167],196
R 140,2,1408
M 56,v[168:171],192
ds_read2_b32 v[112:113],v16 offset0:96 offset1:97
M 56,v[172:175],188
ds_read2_b32 v[114:115],v16 offset0:98 offset1:99
M 56,v[176:179],184
L 15
M 60,v[152:155],212
R 144,2,896
M 60,v[156:159],208
ds_read2_b32 v[116:117],v16 offset0:224 offset1:225
M 60,v[160:163],204
ds_read2_b32 v[118:119],v16 offset0:226 offset1:227
M 60,v[164:167],200
R 148,2,384
M 60,v[168:171],196
M 60,v[172:175],192
M 60,v[176:179],188
M 60,v[180:183],184
L 15
M 40,v[88:91],128
ds_read2_b32 v[152:153],v1 offset0:112 offset1:113
M 40,v[92:95],124
ds_read2_b32 v[154:155],v1 offset0:114 offset1:115
M 40,v[96:99],120
L 14
R 184,2,4032
M 44,v[88:91],132
ds_read2_b32 v[156:157],v1 offset0:240 offset1:241
M 44,v[92:95],128
M 44,v[96:99],124
ds_read2_b32 v[158:159],v1 offset0:242 offset1:243
v_add_u32 v1,12928,v1
M 44,v[100:103],120
L 14
R 188,2,3520
M 48,v[88:91],136
ds_read2_b32 v[160:161],v14 offset0:112 offset1:113
M 48,v[92:95],132
ds_read2_b32 v[162:163],v14 offset0:114 offset1:115
M 48,v[96:99],128
M 48,v[100:103],124
R 192,2,3008
M 48,v[104:107],120
L 15
ds_read2_b32 v[164:165],v14 offset0:240 offset1:241
M 52,v[88:91],140
ds_read2_b32 v[166:167],v14 offset0:242 offset1:243
v_add_u32 v14,12928,v14
M 52,v[92:95],136
R 196,2,2496
M 52,v[96:99],132
M 52,v[100:103],128
ds_read2_b32 v[168:169],v15 offset0:112 offset1:113
M 52,v[104:107],124
ds_read2_b32 v[170:171],v15 offset0:114 offset1:115
M 52,v[108:111],120
L 15
R 200,2,1984
M 56,v[88:91],144
ds_read2_b32 v[172:173],v15 offset0:240 offset1:241
M 56,v[92:95],140
M 56,v[96:99],136
ds_read2_b32 v[174:175],v15 offset0:242 offset1:243
v_add_u32 v15,12928,v15
M 56,v[100:103],132
R 204,2,1472
M 56,v[104:107],128
ds_read2_b32 v[176:177],v16 offset0:112 offset1:113
M 56,v[108:111],124
ds_read2_b32 v[178:179],v16 offset0:114 offset1:115
M 56,v[112:115],120
L 15
M 60,v[88:91],148
R 208,2,960
M 60,v[92:95],144
ds_read2_b32 v[180:181],v16 offset0:240 offset1:241
M 60,v[96:99],140
ds_read2_b32 v[182:183],v16 offset0:242 offset1:243
v_add_u32 v16,12928,v16
M 60,v[100:103],136
)"
R"(R 212,2,448
M 60,v[104:107],132
M 60,v[108:111],128
M 60,v[112:115],124
M 60,v[116:119],120
L 15
M 40,v[152:155],192
ds_read2_b32 v[88:89],v1 offset0:0 offset1:1
M 40,v[156:159],188
ds_read2_b32 v[90:91],v1 offset0:2 offset1:3
M 40,v[160:163],184
L 14
R 120,2,16512
M 44,v[152:155],196
ds_read2_b32 v[92:93],v1 offset0:128 offset1:129
M 44,v[156:159],192
M 44,v[160:163],188
ds_read2_b32 v[94:95],v1 offset0:130 offset1:131
M 44,v[164:167],184
L 14
R 124,2,16000
M 48,v[152:155],200
ds_read2_b32 v[96:97],v14 offset0:0 offset1:1
M 48,v[156:159],196
ds_read2_b32 v[98:99],v14 offset0:2 offset1:3
M 48,v[160:163],192
M 48,v[164:167],188
R 128,2,15488
M 48,v[168:171],184
L 15
ds_read2_b32 v[100:101],v14 offset0:128 offset1:129
M 52,v[152:155],204
ds_read2_b32 v[102:103],v14 offset0:130 offset1:131
M 52,v[156:159],200
R 132,2,14976
M 52,v[160:163],196
M 52,v[164:167],192
ds_read2_b32 v[104:105],v15 offset0:0 offset1:1
M 52,v[168:171],188
ds_read2_b32 v[106:107],v15 offset0:2 offset1:3
M 52,v[172:175],184
L 15
R 136,2,14464
M 56,v[152:155],208
ds_read2_b32 v[108:109],v15 offset0:128 offset1:129
M 56,v[156:159],204
M 56,v[160:163],200
ds_read2_b32 v[110:111],v15 offset0:130 offset1:131
M 56,v[164:167],196
R 140,2,13952
M 56,v[168:171],192
ds_read2_b32 v[112:113],v16 offset0:0 offset1:1
M 56,v[172:175],188
ds_read2_b32 v[114:115],v16 offset0:2 offset1:3
M 56,v[176:179],184
L 15
M 60,v[152:155],212
R 144,2,13440
M 60,v[156:159],208
ds_read2_b32 v[116:117],v16 offset0:128 offset1:129
M 60,v[160:163],204
ds_read2_b32 v[118:119],v16 offset0:130 offset1:131
M 60,v[164:167],200
R 148,2,12928
M 60,v[168:171],196
M 60,v[172:175],192
M 60,v[176:179],188
M 60,v[180:183],184
s_cmp_eq_u32 s51,2
s_cbranch_scc1 Lcv_d_%=
ds_read_b64 v[24:25],v3 offset:2560
ds_read_b64 v[26:27],v3 offset:2048
ds_read_b64 v[28:29],v3 offset:1536
ds_read_b64 v[30:31],v3 offset:1024
ds_read_b64 v[32:33],v3 offset:512
ds_read_b64 v[34:35],v3 offset:0
L 0
v_and_b32 v36,0xffff0000,v25
v_lshlrev_b32 v37,16,v25
v_and_b32 v38,0xffff0000,v24
v_lshlrev_b32 v39,16,v24
v_fma_f32 v40,v36,s86,v40
v_fma_f32 v41,v37,s86,v41
v_fma_f32 v42,v38,s86,v42
v_fma_f32 v43,v39,s86,v43
v_cvt_pk_bf16_f32 v40,v40,v41
v_cvt_pk_bf16_f32 v41,v42,v43
ds_write_b64 v4,v[40:41] offset:0
v_and_b32 v36,0xffff0000,v27
v_lshlrev_b32 v37,16,v27
v_and_b32 v38,0xffff0000,v26
v_lshlrev_b32 v39,16,v26
v_fma_f32 v44,v36,s86,v44
v_fma_f32 v45,v37,s86,v45
v_fma_f32 v46,v38,s86,v46
v_fma_f32 v47,v39,s86,v47
v_cvt_pk_bf16_f32 v44,v44,v45
v_cvt_pk_bf16_f32 v45,v46,v47
ds_write_b64 v4,v[44:45] offset:512
v_and_b32 v36,0xffff0000,v29
v_lshlrev_b32 v37,16,v29
v_and_b32 v38,0xffff0000,v28
v_lshlrev_b32 v39,16,v28
v_fma_f32 v48,v36,s86,v48
v_fma_f32 v49,v37,s86,v49
v_fma_f32 v50,v38,s86,v50
v_fma_f32 v51,v39,s86,v51
v_cvt_pk_bf16_f32 v48,v48,v49
v_cvt_pk_bf16_f32 v49,v50,v51
ds_write_b64 v4,v[48:49] offset:1024
v_and_b32 v36,0xffff0000,v31
v_lshlrev_b32 v37,16,v31
v_and_b32 v38,0xffff0000,v30
v_lshlrev_b32 v39,16,v30
v_fma_f32 v52,v36,s86,v52
v_fma_f32 v53,v37,s86,v53
v_fma_f32 v54,v38,s86,v54
v_fma_f32 v55,v39,s86,v55
v_cvt_pk_bf16_f32 v52,v52,v53
v_cvt_pk_bf16_f32 v53,v54,v55
ds_write_b64 v4,v[52:53] offset:1536
v_and_b32 v36,0xffff0000,v33
v_lshlrev_b32 v37,16,v33
v_and_b32 v38,0xffff0000,v32
v_lshlrev_b32 v39,16,v32
v_fma_f32 v56,v36,s86,v56
v_fma_f32 v57,v37,s86,v57
v_fma_f32 v58,v38,s86,v58
v_fma_f32 v59,v39,s86,v59
v_cvt_pk_bf16_f32 v56,v56,v57
v_cvt_pk_bf16_f32 v57,v58,v59
ds_write_b64 v4,v[56:57] offset:2048
v_and_b32 v36,0xffff0000,v35
v_lshlrev_b32 v37,16,v35
v_and_b32 v38,0xffff0000,v34
v_lshlrev_b32 v39,16,v34
v_fma_f32 v60,v36,s86,v60
v_fma_f32 v61,v37,s86,v61
v_fma_f32 v62,v38,s86,v62
v_fma_f32 v63,v39,s86,v63
v_cvt_pk_bf16_f32 v60,v60,v61
v_cvt_pk_bf16_f32 v61,v62,v63
ds_write_b64 v4,v[60:61] offset:2560
s_branch Lcv_e_%=
Lcv_d_%=:
L 15
v_mfma_f32_16x16x32_bf16 v[64:67],v[88:91],v[128:131],0
ds_read2_b32 v[152:153],v1 offset0:16 offset1:17
ds_read_b64 v[24:25],v3 offset:2560
M 64,v[92:95],124
ds_read2_b32 v[154:155],v1 offset0:18 offset1:19
ds_read_b64 v[26:27],v3 offset:2048
M 64,v[96:99],120
L 15
R 184,2,16576
ds_read_b64 v[28:29],v3 offset:1536
v_mfma_f32_16x16x32_bf16 v[68:71],v[88:91],v[132:135],0
ds_read2_b32 v[156:157],v1 offset0:144 offset1:145
ds_read_b64 v[30:31],v3 offset:1024
M 68,v[92:95],128
ds_read_b64 v[32:33],v3 offset:512
ds_read_b64 v[34:35],v3 offset:0
M 68,v[96:99],124
ds_read2_b32 v[158:159],v1 offset0:146 offset1:147
M 68,v[100:103],120
L 15
R 188,2,16064
v_mfma_f32_16x16x32_bf16 v[72:75],v[88:91],v[136:139],0
ds_read2_b32 v[160:161],v14 offset0:16 offset1:17
M 72,v[92:95],132
s_mov_b32 m0,163584
s_mov_b32 s78,0
ds_read2_b32 v[162:163],v14 offset0:18 offset1:19
M 72,v[96:99],128
M 72,v[100:103],124
L 4
R 192,2,15552
v_and_b32 v36,0xffff0000,v25
M 72,v[104:107],120
s_nop 0
ds_read2_b32 v[164:165],v14 offset0:144 offset1:145
v_lshlrev_b32 v37,16,v25
v_mfma_f32_16x16x32_bf16 v[76:79],v[88:91],v[140:143],0
ds_read2_b32 v[166:167],v14 offset0:146 offset1:147
v_and_b32 v38,0xffff0000,v24
M 76,v[92:95],136
R 196,2,15040
v_lshlrev_b32 v39,16,v24
M 76,v[96:99],132
buffer_load_dword v12,s[60:63],s78 offen offset:0  lds
M 76,v[100:103],128
ds_read2_b32 v[168:169],v15 offset0:16 offset1:17
v_fma_f32 v40,v36,s86,v40
M 76,v[104:107],124
ds_read2_b32 v[170:171],v15 offset0:18 offset1:19
v_fma_f32 v41,v37,s86,v41
M 76,v[108:111],120
s_nop 0
R 200,2,14528
v_fma_f32 v42,v38,s86,v42
v_mfma_f32_16x16x32_bf16 v[80:83],v[88:91],v[144:147],0
ds_read2_b32 v[172:173],v15 offset0:144 offset1:145
v_fma_f32 v43,v39,s86,v43
M 80,v[92:95],140
v_cvt_pk_bf16_f32 v40,v40,v41
v_cvt_pk_bf16_f32 v41,v42,v43
M 80,v[96:99],136
ds_read2_b32 v[174:175],v15 offset0:146 offset1:147
v_and_b32 v36,0xffff0000,v27
M 80,v[100:103],132
R 204,2,14016
v_lshlrev_b32 v37,16,v27
M 80,v[104:107],128
ds_read2_b32 v[176:177],v16 offset0:16 offset1:17
v_and_b32 v38,0xffff0000,v26
M 80,v[108:111],124
ds_read2_b32 v[178:179],v16 offset0:18 offset1:19
v_lshlrev_b32 v39,16,v26
M 80,v[112:115],120
s_nop 0
ds_write_b64 v4,v[40:41] offset:0
v_mfma_f32_16x16x32_bf16 v[84:87],v[88:91],v[148:151],0
R 208,2,13504
v_fma_f32 v44,v36,s86,v44
M 84,v[92:95],144
ds_read2_b32 v[180:181],v16 offset0:144 offset1:145
v_fma_f32 v45,v37,s86,v45
M 84,v[96:99],140
ds_read2_b32 v[182:183],v16 offset0:146 offset1:147
v_fma_f32 v46,v38,s86,v46
M 84,v[100:103],136
R 212,2,12992
v_fma_f32 v47,v39,s86,v47
M 84,v[104:107],132
v_cvt_pk_bf16_f32 v44,v44,v45
v_cvt_pk_bf16_f32 v45,v46,v47
M 84,v[108:111],128
ds_write_b64 v4,v[44:45] offset:512
M 84,v[112:115],124
v_and_b32 v36,0xffff0000,v29
v_lshlrev_b32 v37,16,v29
M 84,v[116:119],120
L 15
v_and_b32 v38,0xffff0000,v28
v_lshlrev_b32 v39,16,v28
M 64,v[152:155],192
ds_read2_b32 v[88:89],v1 offset0:32 offset1:33
v_fma_f32 v48,v36,s86,v48
M 64,v[156:159],188
ds_read2_b32 v[90:91],v1 offset0:34 offset1:35
v_fma_f32 v49,v37,s86,v49
M 64,v[160:163],184
L 15
R 120,2,16640
v_fma_f32 v50,v38,s86,v50
M 68,v[152:155],196
ds_read2_b32 v[92:93],v1 offset0:160 offset1:161
v_fma_f32 v51,v39,s86,v51
M 68,v[156:159],192
v_cvt_pk_bf16_f32 v48,v48,v49
v_cvt_pk_bf16_f32 v49,v50,v51
M 68,v[160:163],188
ds_read2_b32 v[94:95],v1 offset0:162 offset1:163
v_and_b32 v36,0xffff0000,v31
M 68,v[164:167],184
L 15
R 124,2,16128
v_lshlrev_b32 v37,16,v31
M 72,v[152:155],200
ds_read2_b32 v[96:97],v14 offset0:32 offset1:33
v_and_b32 v38,0xffff0000,v30
M 72,v[156:159],196
ds_read2_b32 v[98:99],v14 offset0:34 offset1:35
v_lshlrev_b32 v39,16,v30
M 72,v[160:163],192
ds_write_b64 v4,v[48:49] offset:1024
M 72,v[164:167],188
R 128,2,15616
v_fma_f32 v52,v36,s86,v52
M 72,v[168:171],184
L 15
ds_read2_b32 v[100:101],v14 offset0:160 offset1:161
v_fma_f32 v53,v37,s86,v53
M 76,v[152:155],204
ds_read2_b32 v[102:103],v14 offset0:162 offset1:163
v_fma_f32 v54,v38,s86,v54
M 76,v[156:159],200
R 132,2,15104
v_fma_f32 v55,v39,s86,v55
M 76,v[160:163],196
v_cvt_pk_bf16_f32 v52,v52,v53
v_cvt_pk_bf16_f32 v53,v54,v55
M 76,v[164:167],192
ds_read2_b32 v[104:105],v15 offset0:32 offset1:33
v_and_b32 v36,0xffff0000,v33
M 76,v[168:171],188
ds_read2_b32 v[106:107],v15 offset0:34 offset1:35
v_lshlrev_b32 v37,16,v33
M 76,v[172:175],184
L 15
R 136,2,14592
v_and_b32 v38,0xffff0000,v32
M 80,v[152:155],208
ds_read2_b32 v[108:109],v15 offset0:160 offset1:161
v_lshlrev_b32 v39,16,v32
M 80,v[156:159],204
ds_write_b64 v4,v[52:53] offset:1536
M 80,v[160:163],200
ds_read2_b32 v[110:111],v15 offset0:162 offset1:163
v_fma_f32 v56,v36,s86,v56
M 80,v[164:167],196
R 140,2,14080
v_fma_f32 v57,v37,s86,v57
M 80,v[168:171],192
ds_read2_b32 v[112:113],v16 offset0:32 offset1:33
v_fma_f32 v58,v38,s86,v58
M 80,v[172:175],188
ds_read2_b32 v[114:115],v16 offset0:34 offset1:35
v_fma_f32 v59,v39,s86,v59
M 80,v[176:179],184
L 15
v_cvt_pk_bf16_f32 v56,v56,v57
v_cvt_pk_bf16_f32 v57,v58,v59
M 84,v[152:155],212
R 144,2,13568
v_and_b32 v36,0xffff0000,v35
M 84,v[156:159],208
ds_read2_b32 v[116:117],v16 offset0:160 offset1:161
v_lshlrev_b32 v37,16,v35
M 84,v[160:163],204
ds_read2_b32 v[118:119],v16 offset0:162 offset1:163
v_and_b32 v38,0xffff0000,v34
M 84,v[164:167],200
R 148,2,13056
v_lshlrev_b32 v39,16,v34
M 84,v[168:171],196
ds_write_b64 v4,v[56:57] offset:2048
M 84,v[172:175],192
v_fma_f32 v60,v36,s86,v60
v_fma_f32 v61,v37,s86,v61
M 84,v[176:179],188
v_fma_f32 v62,v38,s86,v62
v_fma_f32 v63,v39,s86,v63
M 84,v[180:183],184
L 15
v_cvt_pk_bf16_f32 v60,v60,v61
v_cvt_pk_bf16_f32 v61,v62,v63
M 64,v[88:91],128
ds_read2_b32 v[152:153],v1 offset0:48 offset1:49
M 64,v[92:95],124
ds_read2_b32 v[154:155],v1 offset0:50 offset1:51
M 64,v[96:99],120
L 15
R 184,2,16704
M 68,v[88:91],132
ds_read2_b32 v[156:157],v1 offset0:176 offset1:177
M 68,v[92:95],128
ds_write_b64 v4,v[60:61] offset:2560
M 68,v[96:99],124
ds_read2_b32 v[158:159],v1 offset0:178 offset1:179
M 68,v[100:103],120
L 15
R 188,2,16192
M 72,v[88:91],136
ds_read2_b32 v[160:161],v14 offset0:48 offset1:49
M 72,v[92:95],132
ds_read2_b32 v[162:163],v14 offset0:50 offset1:51
M 72,v[96:99],128
M 72,v[100:103],124
R 192,2,15680
M 72,v[104:107],120
L 15
ds_read2_b32 v[164:165],v14 offset0:176 offset1:177
M 76,v[88:91],140
ds_read2_b32 v[166:167],v14 offset0:178 offset1:179
M 76,v[92:95],136
R 196,2,15168
M 76,v[96:99],132
M 76,v[100:103],128
ds_read2_b32 v[168:169],v15 offset0:48 offset1:49
M 76,v[104:107],124
ds_read2_b32 v[170:171],v15 offset0:50 offset1:51
M 76,v[108:111],120
L 15
R 200,2,14656
M 80,v[88:91],144
ds_read2_b32 v[172:173],v15 offset0:176 offset1:177
M 80,v[92:95],140
M 80,v[96:99],136
ds_read2_b32 v[174:175],v15 offset0:178 offset1:179
M 80,v[100:103],132
R 204,2,14144
M 80,v[104:107],128
s_mov_b32 m0,163584
s_mov_b32 s78,1847296
ds_read2_b32 v[176:177],v16 offset0:48 offset1:49
M 80,v[108:111],124
ds_read2_b32 v[178:179],v16 offset0:50 offset1:51
M 80,v[112:115],120
L 15
buffer_load_dword v12,s[60:63],s78 offen offset:0  lds
M 84,v[88:91],148
R 208,2,13632
M 84,v[92:95],144
ds_read2_b32 v[180:181],v16 offset0:176 offset1:177
M 84,v[96:99],140
ds_read2_b32 v[182:183],v16 offset0:178 offset1:179
M 84,v[100:103],136
R 212,2,13120
M 84,v[104:107],132
M 84,v[108:111],128
M 84,v[112:115],124
M 84,v[116:119],120
L 15
M 64,v[152:155],192
ds_read2_b32 v[88:89],v1 offset0:64 offset1:65
M 64,v[156:159],188
ds_read2_b32 v[90:91],v1 offset0:66 offset1:67
M 64,v[160:163],184
L 14
R 120,2,16768
M 68,v[152:155],196
ds_read2_b32 v[92:93],v1 offset0:192 offset1:193
M 68,v[156:159],192
M 68,v[160:163],188
ds_read2_b32 v[94:95],v1 offset0:194 offset1:195
M 68,v[164:167],184
L 14
R 124,2,16256
M 72,v[152:155],200
ds_read2_b32 v[96:97],v14 offset0:64 offset1:65
M 72,v[156:159],196
ds_read2_b32 v[98:99],v14 offset0:66 offset1:67
M 72,v[160:163],192
M 72,v[164:167],188
R 128,2,15744
M 72,v[168:171],184
L 15
ds_read2_b32 v[100:101],v14 offset0:192 offset1:193
M 76,v[152:155],204
ds_read2_b32 v[102:103],v14 offset0:194 offset1:195
M 76,v[156:159],200
R 132,2,15232
M 76,v[160:163],196
M 76,v[164:167],192
ds_read2_b32 v[104:105],v15 offset0:64 offset1:65
M 76,v[168:171],188
ds_read2_b32 v[106:107],v15 offset0:66 offset1:67
M 76,v[172:175],184
L 15
R 136,2,14720
M 80,v[152:155],208
ds_read2_b32 v[108:109],v15 offset0:192 offset1:193
M 80,v[156:159],204
M 80,v[160:163],200
ds_read2_b32 v[110:111],v15 offset0:194 offset1:195
M 80,v[164:167],196
R 140,2,14208
M 80,v[168:171],192
ds_read2_b32 v[112:113],v16 offset0:64 offset1:65
M 80,v[172:175],188
ds_read2_b32 v[114:115],v16 offset0:66 offset1:67
M 80,v[176:179],184
L 15
M 84,v[152:155],212
R 144,2,13696
M 84,v[156:159],208
ds_read2_b32 v[116:117],v16 offset0:192 offset1:193
M 84,v[160:163],204
ds_read2_b32 v[118:119],v16 offset0:194 offset1:195
M 84,v[164:167],200
R 148,2,13184
M 84,v[168:171],196
M 84,v[172:175],192
M 84,v[176:179],188
M 84,v[180:183],184
L 15
M 64,v[88:91],128
ds_read2_b32 v[152:153],v1 offset0:80 offset1:81
M 64,v[92:95],124
ds_read2_b32 v[154:155],v1 offset0:82 offset1:83
M 64,v[96:99],120
L 14
R 184,2,16832
M 68,v[88:91],132
ds_read2_b32 v[156:157],v1 offset0:208 offset1:209
M 68,v[92:95],128
M 68,v[96:99],124
ds_read2_b32 v[158:159],v1 offset0:210 offset1:211
M 68,v[100:103],120
L 14
R 188,2,16320
M 72,v[88:91],136
ds_read2_b32 v[160:161],v14 offset0:80 offset1:81
M 72,v[92:95],132
ds_read2_b32 v[162:163],v14 offset0:82 offset1:83
M 72,v[96:99],128
M 72,v[100:103],124
R 192,2,15808
M 72,v[104:107],120
)"
R"(L 15
ds_read2_b32 v[164:165],v14 offset0:208 offset1:209
M 76,v[88:91],140
ds_read2_b32 v[166:167],v14 offset0:210 offset1:211
M 76,v[92:95],136
R 196,2,15296
M 76,v[96:99],132
M 76,v[100:103],128
ds_read2_b32 v[168:169],v15 offset0:80 offset1:81
M 76,v[104:107],124
ds_read2_b32 v[170:171],v15 offset0:82 offset1:83
M 76,v[108:111],120
L 15
R 200,2,14784
M 80,v[88:91],144
ds_read2_b32 v[172:173],v15 offset0:208 offset1:209
M 80,v[92:95],140
M 80,v[96:99],136
ds_read2_b32 v[174:175],v15 offset0:210 offset1:211
M 80,v[100:103],132
R 204,2,14272
M 80,v[104:107],128
ds_read2_b32 v[176:177],v16 offset0:80 offset1:81
M 80,v[108:111],124
ds_read2_b32 v[178:179],v16 offset0:82 offset1:83
M 80,v[112:115],120
L 15
M 84,v[88:91],148
R 208,2,13760
M 84,v[92:95],144
ds_read2_b32 v[180:181],v16 offset0:208 offset1:209
M 84,v[96:99],140
ds_read2_b32 v[182:183],v16 offset0:210 offset1:211
M 84,v[100:103],136
R 212,2,13248
M 84,v[104:107],132
M 84,v[108:111],128
M 84,v[112:115],124
M 84,v[116:119],120
L 15
M 64,v[152:155],192
ds_read2_b32 v[88:89],v1 offset0:96 offset1:97
M 64,v[156:159],188
ds_read2_b32 v[90:91],v1 offset0:98 offset1:99
M 64,v[160:163],184
L 14
R 120,2,16896
M 68,v[152:155],196
ds_read2_b32 v[92:93],v1 offset0:224 offset1:225
M 68,v[156:159],192
M 68,v[160:163],188
ds_read2_b32 v[94:95],v1 offset0:226 offset1:227
M 68,v[164:167],184
L 14
R 124,2,16384
M 72,v[152:155],200
ds_read2_b32 v[96:97],v14 offset0:96 offset1:97
M 72,v[156:159],196
ds_read2_b32 v[98:99],v14 offset0:98 offset1:99
M 72,v[160:163],192
M 72,v[164:167],188
R 128,2,15872
M 72,v[168:171],184
L 15
ds_read2_b32 v[100:101],v14 offset0:224 offset1:225
M 76,v[152:155],204
ds_read2_b32 v[102:103],v14 offset0:226 offset1:227
M 76,v[156:159],200
R 132,2,15360
M 76,v[160:163],196
M 76,v[164:167],192
ds_read2_b32 v[104:105],v15 offset0:96 offset1:97
M 76,v[168:171],188
ds_read2_b32 v[106:107],v15 offset0:98 offset1:99
M 76,v[172:175],184
L 15
R 136,2,14848
M 80,v[152:155],208
ds_read2_b32 v[108:109],v15 offset0:224 offset1:225
M 80,v[156:159],204
M 80,v[160:163],200
ds_read2_b32 v[110:111],v15 offset0:226 offset1:227
M 80,v[164:167],196
R 140,2,14336
M 80,v[168:171],192
ds_read2_b32 v[112:113],v16 offset0:96 offset1:97
M 80,v[172:175],188
ds_read2_b32 v[114:115],v16 offset0:98 offset1:99
M 80,v[176:179],184
L 15
M 84,v[152:155],212
R 144,2,13824
M 84,v[156:159],208
ds_read2_b32 v[116:117],v16 offset0:224 offset1:225
M 84,v[160:163],204
ds_read2_b32 v[118:119],v16 offset0:226 offset1:227
M 84,v[164:167],200
R 148,2,13312
M 84,v[168:171],196
M 84,v[172:175],192
M 84,v[176:179],188
M 84,v[180:183],184
L 15
M 64,v[88:91],128
ds_read2_b32 v[152:153],v1 offset0:112 offset1:113
M 64,v[92:95],124
ds_read2_b32 v[154:155],v1 offset0:114 offset1:115
M 64,v[96:99],120
L 14
R 184,2,16960
M 68,v[88:91],132
ds_read2_b32 v[156:157],v1 offset0:240 offset1:241
M 68,v[92:95],128
M 68,v[96:99],124
ds_read2_b32 v[158:159],v1 offset0:242 offset1:243
M 68,v[100:103],120
L 14
R 188,2,16448
M 72,v[88:91],136
ds_read2_b32 v[160:161],v14 offset0:112 offset1:113
M 72,v[92:95],132
ds_read2_b32 v[162:163],v14 offset0:114 offset1:115
M 72,v[96:99],128
M 72,v[100:103],124
R 192,2,15936
M 72,v[104:107],120
L 15
ds_read2_b32 v[164:165],v14 offset0:240 offset1:241
M 76,v[88:91],140
ds_read2_b32 v[166:167],v14 offset0:242 offset1:243
M 76,v[92:95],136
R 196,2,15424
M 76,v[96:99],132
M 76,v[100:103],128
ds_read2_b32 v[168:169],v15 offset0:112 offset1:113
M 76,v[104:107],124
ds_read2_b32 v[170:171],v15 offset0:114 offset1:115
M 76,v[108:111],120
L 15
R 200,2,14912
M 80,v[88:91],144
ds_read2_b32 v[172:173],v15 offset0:240 offset1:241
M 80,v[92:95],140
M 80,v[96:99],136
ds_read2_b32 v[174:175],v15 offset0:242 offset1:243
M 80,v[100:103],132
R 204,2,14400
M 80,v[104:107],128
ds_read2_b32 v[176:177],v16 offset0:112 offset1:113
M 80,v[108:111],124
ds_read2_b32 v[178:179],v16 offset0:114 offset1:115
M 80,v[112:115],120
L 15
M 84,v[88:91],148
R 208,2,13888
M 84,v[92:95],144
ds_read2_b32 v[180:181],v16 offset0:240 offset1:241
M 84,v[96:99],140
ds_read2_b32 v[182:183],v16 offset0:242 offset1:243
M 84,v[100:103],136
R 212,2,13376
M 84,v[104:107],132
M 84,v[108:111],128
M 84,v[112:115],124
M 84,v[116:119],120
L 15
M 64,v[152:155],192
M 64,v[156:159],188
M 64,v[160:163],184
L 12
M 68,v[152:155],196
M 68,v[156:159],192
M 68,v[160:163],188
M 68,v[164:167],184
L 9
M 72,v[152:155],200
M 72,v[156:159],196
M 72,v[160:163],192
ds_read_b64 v[24:25],v3 offset:15488
ds_read_b64 v[26:27],v3 offset:14976
M 72,v[164:167],188
ds_read_b64 v[28:29],v3 offset:14464
ds_read_b64 v[30:31],v3 offset:13952
M 72,v[168:171],184
L 10
ds_read_b64 v[32:33],v3 offset:13440
ds_read_b64 v[34:35],v3 offset:12928
M 76,v[152:155],204
M 76,v[156:159],200
M 76,v[160:163],196
M 76,v[164:167],192
M 76,v[168:171],188
M 76,v[172:175],184
L 9
M 80,v[152:155],208
M 80,v[156:159],204
M 80,v[160:163],200
M 80,v[164:167],196
M 80,v[168:171],192
M 80,v[172:175],188
M 80,v[176:179],184
L 6
M 84,v[152:155],212
M 84,v[156:159],208
M 84,v[160:163],204
M 84,v[164:167],200
M 84,v[168:171],196
M 84,v[172:175],192
M 84,v[176:179],188
M 84,v[180:183],184
L 0
v_and_b32 v36,0xffff0000,v25
v_lshlrev_b32 v37,16,v25
v_and_b32 v38,0xffff0000,v24
v_lshlrev_b32 v39,16,v24
v_fma_f32 v64,v36,s87,v64
v_fma_f32 v65,v37,s87,v65
v_fma_f32 v66,v38,s87,v66
v_fma_f32 v67,v39,s87,v67
v_cvt_pk_bf16_f32 v64,v64,v65
v_cvt_pk_bf16_f32 v65,v66,v67
ds_write_b64 v4,v[64:65] offset:12928
v_and_b32 v36,0xffff0000,v27
v_lshlrev_b32 v37,16,v27
v_and_b32 v38,0xffff0000,v26
v_lshlrev_b32 v39,16,v26
v_fma_f32 v68,v36,s87,v68
v_fma_f32 v69,v37,s87,v69
v_fma_f32 v70,v38,s87,v70
v_fma_f32 v71,v39,s87,v71
v_cvt_pk_bf16_f32 v68,v68,v69
v_cvt_pk_bf16_f32 v69,v70,v71
ds_write_b64 v4,v[68:69] offset:13440
v_and_b32 v36,0xffff0000,v29
v_lshlrev_b32 v37,16,v29
v_and_b32 v38,0xffff0000,v28
v_lshlrev_b32 v39,16,v28
v_fma_f32 v72,v36,s87,v72
v_fma_f32 v73,v37,s87,v73
v_fma_f32 v74,v38,s87,v74
v_fma_f32 v75,v39,s87,v75
v_cvt_pk_bf16_f32 v72,v72,v73
v_cvt_pk_bf16_f32 v73,v74,v75
ds_write_b64 v4,v[72:73] offset:13952
v_and_b32 v36,0xffff0000,v31
v_lshlrev_b32 v37,16,v31
v_and_b32 v38,0xffff0000,v30
v_lshlrev_b32 v39,16,v30
v_fma_f32 v76,v36,s87,v76
v_fma_f32 v77,v37,s87,v77
v_fma_f32 v78,v38,s87,v78
v_fma_f32 v79,v39,s87,v79
v_cvt_pk_bf16_f32 v76,v76,v77
v_cvt_pk_bf16_f32 v77,v78,v79
ds_write_b64 v4,v[76:77] offset:14464
v_and_b32 v36,0xffff0000,v33
v_lshlrev_b32 v37,16,v33
v_and_b32 v38,0xffff0000,v32
v_lshlrev_b32 v39,16,v32
v_fma_f32 v80,v36,s87,v80
v_fma_f32 v81,v37,s87,v81
v_fma_f32 v82,v38,s87,v82
v_fma_f32 v83,v39,s87,v83
v_cvt_pk_bf16_f32 v80,v80,v81
v_cvt_pk_bf16_f32 v81,v82,v83
ds_write_b64 v4,v[80:81] offset:14976
v_and_b32 v36,0xffff0000,v35
v_lshlrev_b32 v37,16,v35
v_and_b32 v38,0xffff0000,v34
v_lshlrev_b32 v39,16,v34
v_fma_f32 v84,v36,s87,v84
v_fma_f32 v85,v37,s87,v85
v_fma_f32 v86,v38,s87,v86
v_fma_f32 v87,v39,s87,v87
v_cvt_pk_bf16_f32 v84,v84,v85
v_cvt_pk_bf16_f32 v85,v86,v87
ds_write_b64 v4,v[84:85] offset:15488
Lcv_e_%=:
L 0
s_barrier
R 152,5,0
R 156,5,512
R 160,5,1024
R 164,5,1536
R 168,5,2048
R 172,5,2560
s_mov_b32 s78,99614720
L 5
buffer_store_dwordx4 v[152:155],v7,s[36:39],s78 offen offset:0 sc0 sc1
s_add_u32 s78,s56,100139008
L 4
buffer_store_dwordx4 v[156:159],v7,s[36:39],s78 offen offset:0 sc0 sc1
s_mov_b32 s78,100663296
L 3
buffer_store_dwordx4 v[160:163],v7,s[36:39],s78 offen offset:0 sc0 sc1
s_add_u32 s78,s56,101187584
L 2
buffer_store_dwordx4 v[164:167],v7,s[36:39],s78 offen offset:0 sc0 sc1
s_mov_b32 s78,101711872
L 1
buffer_store_dwordx4 v[168:171],v7,s[36:39],s78 offen offset:0 sc0 sc1
s_add_u32 s78,s56,102236160
L 0
buffer_store_dwordx4 v[172:175],v7,s[36:39],s78 offen offset:0 sc0 sc1
s_waitcnt vmcnt(0)
s_barrier
s_add_u32 s48,s48,32768
buffer_store_dword v23,v10,s[36:39],s48 offen offset:0 sc0 sc1
s_cmp_eq_u32 s51,2
s_cbranch_scc0 Lcv_f_%=
s_mov_b32 m0,163584
s_mov_b32 s78,3694592
buffer_load_dword v12,s[60:63],s78 offen offset:0  lds
s_mov_b32 m0,163584
s_mov_b32 s78,5541888
buffer_load_dword v12,s[60:63],s78 offen offset:0  lds
Lcv_f_%=:
Lcode_cv_e:
.purgem M
.purgem R
.purgem T
.purgem D
.purgem L
)"
  :: "s"(ws), "s"(__builtin_amdgcn_readfirstlane(sWave)), "s"(__builtin_amdgcn_readfirstlane(sBuf)), "s"(__builtin_amdgcn_readfirstlane(sAuxOff)), "s"(__builtin_amdgcn_readfirstlane(sFoutOff)), "s"(__builtin_amdgcn_readfirstlane(sC0)), "s"(__builtin_amdgcn_readfirstlane(sC1)), "s"(__builtin_amdgcn_readfirstlane(sExp)), "v"(vL16), "v"(vPA), "v"(vPB), "v"(vPU), "v"(vPW), "v"(vOR), "v"(vORA), "v"(vOG), "v"(vFA), "v"(vFB), "v"(vFlag), "v"(vTW), "v"(vTX), "s"(w3), "s"(w4) : "v0", "v1", "v2", "v3", "v4", "v5", "v6", "v7", "v8", "v9", "v10", "v11", "v12", "v13", "v14", "v15", "v16", "v17", "v18", "v19", "v20", "v21", "v22", "v23", "v24", "v25", "v26", "v27", "v28", "v29", "v30", "v31", "v32", "v33", "v34", "v35", "v36", "v37", "v38", "v39", "v40", "v41", "v42", "v43", "v44", "v45", "v46", "v47", "v48", "v49", "v50", "v51", "v52", "v53", "v54", "v55", "v56", "v57", "v58", "v59", "v60", "v61", "v62", "v63", "v64", "v65", "v66", "v67", "v68", "v69", "v70", "v71", "v72", "v73", "v74", "v75", "v76", "v77", "v78", "v79", "v80", "v81", "v82", "v83", "v84", "v85", "v86", "v87", "v88", "v89", "v90", "v91", "v92", "v93", "v94", "v95", "v96", "v97", "v98", "v99", "v100", "v101", "v102", "v103", "v104", "v105", "v106", "v107", "v108", "v109", "v110", "v111", "v112", "v113", "v114", "v115", "v116", "v117", "v118", "v119", "v120", "v121", "v122", "v123", "v124", "v125", "v126", "v127", "v128", "v129", "v130", "v131", "v132", "v133", "v134", "v135", "v136", "v137", "v138", "v139", "v140", "v141", "v142", "v143", "v144", "v145", "v146", "v147", "v148", "v149", "v150", "v151", "v152", "v153", "v154", "v155", "v156", "v157", "v158", "v159", "v160", "v161", "v162", "v163", "v164", "v165", "v166", "v167", "v168", "v169", "v170", "v171", "v172", "v173", "v174", "v175", "v176", "v177", "v178", "v179", "v180", "v181", "v182", "v183", "v184", "v185", "v186", "v187", "v188", "v189", "v190", "v191", "v192", "v193", "v194", "v195", "v196", "v197", "v198", "v199", "v200", "v201", "v202", "v203", "v204", "v205", "v206", "v207", "v208", "v209", "v210", "v211", "v212", "v213", "v214", "v215", "v216", "v217", "v218", "v219", "v220", "v221", "v222", "v223", "v224", "v225", "v226", "v227", "v228", "v229", "v230", "v231", "a0", "a1", "a2", "a3", "a4", "a5", "a6", "a7", "a8", "a9", "a10", "a11", "a12", "a13", "a14", "a15", "a16", "a17", "a18", "a19", "a20", "a21", "a22", "a23", "a24", "a25", "a26", "a27", "a28", "a29", "a30", "a31", "a32", "a33", "a34", "a35", "a36", "a37", "a38", "a39", "a40", "a41", "a42", "a43", "a44", "a45", "a46", "a47", "a48", "a49", "a50", "a51", "a52", "a53", "a54", "a55", "a56", "a57", "a58", "a59", "a60", "a61", "a62", "a63", "a64", "a65", "a66", "a67", "a68", "a69", "a70", "a71", "a72", "a73", "a74", "a75", "a76", "a77", "a78", "a79", "a80", "a81", "a82", "a83", "a84", "a85", "a86", "a87", "a88", "a89", "a90", "a91", "a92", "a93", "a94", "a95", "a96", "a97", "a98", "a99", "a100", "a101", "a102", "a103", "a104", "a105", "a106", "a107", "a108", "a109", "a110", "a111", "a112", "a113", "a114", "a115", "a116", "a117", "a118", "a119", "a120", "a121", "a122", "a123", "a124", "a125", "a126", "a127", "a128", "a129", "a130", "a131", "a132", "a133", "a134", "a135", "a136", "a137", "a138", "a139", "a140", "a141", "a142", "a143", "a144", "a145", "a146", "a147", "a148", "a149", "a150", "a151", "a152", "a153", "a154", "a155", "a156", "a157", "a158", "a159", "a160", "a161", "a162", "a163", "a164", "a165", "a166", "a167", "a168", "a169", "a170", "a171", "a172", "a173", "a174", "a175", "a176", "a177", "a178", "a179", "a180", "a181", "a182", "a183", "a184", "a185", "a186", "a187", "a188", "a189", "a190", "a191", "a192", "a193", "a194", "a195", "a196", "a197", "a198", "a199", "a200", "a201", "a202", "a203", "a204", "a205", "a206", "a207", "a208", "a209", "a210", "a211", "a212", "a213", "a214", "a215", "a216", "a217", "a218", "a219", "a220", "a221", "a222", "a223", "a224", "a225", "a226", "a227", "a228", "a229", "a230", "a231", "a232", "a233", "a234", "a235", "a236", "a237", "a238", "a239", "a240", "a241", "a242", "a243", "a244", "a245", "a246", "a247", "a248", "a249", "a250", "a251", "a252", "a253", "a254", "a255", "s36", "s37", "s38", "s39", "s40", "s41", "s42", "s43", "s44", "s45", "s46", "s47", "s48", "s49", "s50", "s51", "s52", "s53", "s54", "s55", "s56", "s57", "s58", "s59", "s60", "s61", "s62", "s63", "s64", "s65", "s66", "s67", "s68", "s69", "s70", "s71", "s72", "s73", "s74", "s75", "s76", "s77", "s78", "s79", "s80", "s81", "s82", "s83", "s84", "s85", "s86", "s87", "s88", "s89", "s90", "s91", "s92", "s93", "s94", "s95", "s96", "s97", "vcc", "scc", "memory");
    LANE_ID(lq_);
    const int g = QT0 + (NCV - 1 - iw) * 164 + min(wave * 64 + lq_, 163);
    if (QTB) touch_lds((const char*)q + (size_t)(g < 32768 ? g : 0) * 128, sScr);
  }
  WAIT_LGKM0();
  __builtin_amdgcn_s_barrier();
  CTS(10);
  }
  const unsigned sDbg0 = 110u * 1048576u + (b * 4 + wave) * 8192;
  constexpr unsigned OOB = 0x80000000u;
  if (kind == 6) {
    LANE_ID(lr);
    const int r8 = lr & 7, seg = lr >> 3, j = lr & 15, g = lr >> 4, rr = 8 * g + (j >> 2);
    const unsigned sWave = wave, sExp = __builtin_amdgcn_readfirstlane(ep), sG0 = slice < 16 ? slice + 1 : 0, sStep = NRW, sDbg = sDbg0;
    const unsigned sWw = ((slice >> 3) * 1792 + 16 * wave) * 2048 + (slice & 7) * 256;
    const unsigned vWd0 = seg * 2048 + (r8 ^ swzB(seg)) * 16, vWd1 = (seg + 8) * 2048 + (r8 ^ swzB(8 + seg)) * 16;
    const unsigned vWr0 = (wave >> 1) * 8192 + rr * 128 + (((wave & 1) * 4 + (lr & 3)) ^ swzB(rr)) * 16, vWr1 = vWr0 + 8;
    const unsigned vRF = (wave * 8 + (seg >> 2)) * 1024 + ((seg & 3) * 16 + r8) * 16;
    const unsigned vRwr = seg == 0 ? r8 * 64 + wave * 4 : 512 + lr * 4, vRrd = r8 * 64;
    const unsigned vRP = g == (j >> 2) ? r8 * 64 + 16 + wave * 8 + (seg & 1) * 4 : 768 + lr * 4;
    const unsigned vRS = (wave == 0 && seg < 2) ? r8 * 16 + seg * 256 : OOB, vFlag = lr < 32 ? lr * 4 : OOB;
    const unsigned vL4 = ((lr & 2) * 4 + wave * 2 + (lr & 1)) * 128, vLY = min((wave < 3 ? wave * 32 : 112) + lr, wave < 2 ? wave * 32 + 31 : wave == 2 ? 111 : NCV - 1) * 128;
    const unsigned vQ = r8 * 2048 + wave * 512 + seg * 16;
    const unsigned vY0 = wave * 4096 + lr * 16;
    const unsigned vTr = 4096 + wave * 4096 + ((2 * g + ((j & 3) >> 1)) * 8 + (j >> 2)) * 16 + (j & 1) * 8;
    const unsigned one = g == (j >> 2) ? 0x3f80u << (16 * (j & 1)) : 0u, vSa = (j & 2) ? 0u : one, vSb = (j & 2) ? one : 0u;
    (void)sDbg;
    asm volatile(
R"(.macro M d,w,x
v_mfma_f32_16x16x32_bf16 v[\d:\d+3], \w, v[\x:\x+3], v[\d:\d+3]
.endm
.macro R d,a,o
ds_read_b128 v[\d:\d+3], v\a offset:\o
.endm
.macro T d,a,o
ds_read_b64_tr_b16 \d, v\a offset:\o
.endm
.macro D a,s,o
buffer_load_dwordx4 v\a, s[36:39], s\s offen offset:\o sc1 lds
.endm
.macro L c
s_waitcnt lgkmcnt(\c)
.endm
Lcode_r_b:
s_mov_b64 s[36:37],%0
s_mov_b32 s38,111243392
s_mov_b32 s39,0x00020000
s_mov_b32 s44,%1
s_mov_b32 s45,%2
s_mov_b32 s46,%3
s_mov_b32 s50,%4
s_mov_b32 s53,%5
v_mov_b32 v0,%6
v_mov_b32 v1,%7
v_mov_b32 v2,%8
v_mov_b32 v3,%9
v_mov_b32 v4,%10
v_mov_b32 v5,%11
v_mov_b32 v6,%12
v_mov_b32 v7,%13
v_mov_b32 v8,%14
v_mov_b32 v9,%15
v_mov_b32 v10,%16
v_mov_b32 v11,%17
v_mov_b32 v12,%18
v_mov_b32 v13,%19
v_mov_b32 v14,%20
v_mov_b32 v15,%21
v_mov_b32 v16,%22
s_mov_b32 s47,256
s_mov_b32 s49,0
s_mov_b32 s93,s45
s_mov_b64 s[84:85],%23
s_mov_b32 s86,4194304
s_mov_b32 s87,0x00020000
s_lshl_b32 s55,s45,14
buffer_load_dwordx4 v[164:167],v8,s[84:87],s55 offen offset:0 nt
buffer_load_dwordx4 v[168:171],v8,s[84:87],s55 offen offset:128 nt
buffer_load_dwordx4 v[172:175],v8,s[84:87],s55 offen offset:256 nt
buffer_load_dwordx4 v[176:179],v8,s[84:87],s55 offen offset:384 nt
v_mov_b32 v17,s53
v_mbcnt_lo_u32_b32 v19,-1,0
v_mbcnt_hi_u32_b32 v19,-1,v19
v_lshlrev_b32 v19,4,v19
s_mov_b32 s82,0
s_mov_b32 s63,0x7fff0000
s_mov_b32 s64,0
s_mov_b32 s67,0
s_lshl_b32 s65,s44,12
s_add_u32 s65,s65,4096
s_lshl_b32 s66,s44,7
s_add_u32 s66,s66,109056000
s_mov_b32 s70,0xaaaaaaaa
s_mov_b32 s71,0xaaaaaaaa
s_mov_b32 s72,0xcccccccc
s_mov_b32 s73,0xcccccccc
s_mov_b32 s74,0xff00ff00
s_mov_b32 s75,0xff00ff00
s_mov_b32 s68,0x3a800000
s_mov_b32 s69,0x3727c5ac
s_mov_b32 s76,0xc0135761
s_mov_b32 s77,0x3d372713
v_mov_b32 v32,0x3f803f80
v_mov_b32 v33,0x3f803f80
v_mov_b32 v34,0x3f803f80
v_mov_b32 v35,0x3f803f80
v_mov_b32 v24,0
v_mov_b32 v25,0
v_mov_b32 v26,v11
v_mov_b32 v27,v12
v_mov_b32 v28,0
v_mov_b32 v29,0
s_getpc_b64 s[94:95]
Lr_pc_%=:
s_add_u32 s94,s94,Lr_tab_%=-Lr_pc_%=
s_addc_u32 s95,s95,0
s_mov_b32 s81,0x7fff0000
s_lshl_b32 s54,s44,11
s_add_u32 s80,s45,85
s_cmp_eq_u32 s45,0
s_cselect_b32 s80,-1,s80
s_waitcnt vmcnt(0)
Lr_a0_%=:
s_cmp_lg_u32 s67,0
s_cbranch_scc1 Lr_b0_%=
s_waitcnt vmcnt(14)
v_mfma_f32_16x16x32_bf16 v[36:39],v[32:35],v[164:167],0
s_add_u32 s79,s45,s46
s_cmp_lt_u32 s45,64
s_cselect_b32 s78,s49,0
v_mfma_f32_16x16x32_bf16 v[40:43],v[164:167],v[164:167],0
s_cmp_ge_u32 s79,64
s_cselect_b32 s78,s78,0
s_add_u32 s62,s79,s78
M 36,v[32:35],168
s_lshr_b32 s78,s45,1
s_and_b32 s79,s45,1
s_lshl_b32 s79,s79,7
M 40,v[168:171],168
s_lshl_b32 s57,s78,15
s_lshl_b32 s55,s62,14
s_cmp_lt_u32 s62,s47
M 36,v[32:35],172
s_cselect_b32 s55,s55,0x7fff0000
s_add_u32 s57,s57,s79
s_lshl_b32 s59,s78,9
M 40,v[172:175],172
buffer_load_dwordx4 v[196:199],v8,s[84:87],s55 offen offset:0 nt
M 36,v[32:35],176
buffer_load_dwordx4 v[200:203],v8,s[84:87],s55 offen offset:128 nt
M 40,v[176:179],176
buffer_load_dwordx4 v[204:207],v8,s[84:87],s55 offen offset:256 nt
v_mfma_f32_16x16x32_bf16 v[116:119],v[26:29],v[164:167],0
buffer_load_dwordx4 v[208:211],v8,s[84:87],s55 offen offset:384 nt
v_mfma_f32_16x16x32_bf16 v[120:123],v[24:27],v[164:167],0
s_add_u32 s57,s57,8388608
s_add_u32 s59,s59,s79
s_lshl_b32 s60,s45,9
v_mfma_f32_16x16x32_bf16 v[124:127],v[26:29],v[168:171],0
s_add_u32 s58,s57,4096
s_add_u32 s59,s59,106954752
s_add_u32 s60,s60,s66
v_mfma_f32_16x16x32_bf16 v[128:131],v[24:27],v[168:171],0
v_add_f32_dpp v44,v36,v36 row_ror:8 row_mask:0xf bank_mask:0xf
v_cndmask_b32 v45,v40,v41,s[70:71]
v_mfma_f32_16x16x32_bf16 v[132:135],v[26:29],v[172:175],0
ds_write_b32 v2,v44 offset:0
v_mfma_f32_16x16x32_bf16 v[136:139],v[24:27],v[172:175],0
v_cndmask_b32 v46,v42,v43,s[70:71]
v_cndmask_b32 v45,v45,v46,s[72:73]
v_mfma_f32_16x16x32_bf16 v[140:143],v[26:29],v[176:179],0
ds_write_b32 v0,v45 offset:0
v_mfma_f32_16x16x32_bf16 v[144:147],v[24:27],v[176:179],0
L 0
s_barrier
R 52,3,0
R 56,3,16
R 60,3,32
s_waitcnt vmcnt(8)
buffer_store_dword v17,v5,s[36:39],s63 offen offset:0 sc0 sc1
s_mov_b32 s63,0x7fff0000
s_cmp_lt_u32 s45,64
s_cselect_b32 s64,-192,0
s_add_u32 s64,s64,256
s_cmp_lt_u32 s45,64
s_cselect_b32 s61,0,32768
s_add_u32 s61,s61,111177728
s_mul_i32 s56,s45,16384
s_add_u32 s56,s56,98566144
Lr_bpy0_%=:
buffer_load_dword v18,v7,s[36:39],s61 offen offset:0 sc1
s_add_u32 s82,s82,1
s_waitcnt vmcnt(0)
v_cmp_eq_u32 vcc,s53,v18
s_nop 0
s_cmp_eq_u64 vcc,-1
s_cbranch_scc1 Lr_bqy0_%=
s_sleep 2
s_cmp_lt_u32 s82,20000
s_cbranch_scc1 Lr_bpy0_%=
Lr_bqy0_%=:
s_add_u32 m0,s65,0
s_nop 0
D 9,56,0
D 9,56,1024
D 9,56,2048
D 9,56,3072
s_waitcnt vmcnt(0)
T v[68:69],10,0
T v[70:71],10,64
T v[72:73],10,1024
T v[74:75],10,1088
T v[76:77],10,2048
T v[78:79],10,2112
T v[80:81],10,3072
T v[82:83],10,3136
s_mov_b32 s45,s62
s_min_u32 s78,s64,s47
s_mul_i32 s56,s45,16384
s_add_u32 s56,s56,98566144
s_cmp_lt_u32 s45,s78
s_cselect_b32 s67,1,0
s_cselect_b32 s56,s56,0x7fff0000
s_add_u32 m0,s65,16384
s_nop 0
D 9,56,0
D 9,56,1024
D 9,56,2048
D 9,56,3072
L 4
v_lshlrev_b32 v84,16,v68
v_and_b32 v85,0xffff0000,v68
v_lshlrev_b32 v86,16,v69
v_and_b32 v87,0xffff0000,v69
v_lshlrev_b32 v88,16,v70
v_and_b32 v89,0xffff0000,v70
v_lshlrev_b32 v90,16,v71
v_and_b32 v91,0xffff0000,v71
v_lshlrev_b32 v92,16,v72
v_and_b32 v93,0xffff0000,v72
v_lshlrev_b32 v94,16,v73
v_and_b32 v95,0xffff0000,v73
v_lshlrev_b32 v96,16,v74
v_and_b32 v97,0xffff0000,v74
v_lshlrev_b32 v98,16,v75
v_and_b32 v99,0xffff0000,v75
L 0
v_lshlrev_b32 v100,16,v76
v_and_b32 v101,0xffff0000,v76
v_lshlrev_b32 v102,16,v77
v_and_b32 v103,0xffff0000,v77
v_lshlrev_b32 v104,16,v78
v_and_b32 v105,0xffff0000,v78
v_lshlrev_b32 v106,16,v79
v_and_b32 v107,0xffff0000,v79
v_lshlrev_b32 v108,16,v80
v_and_b32 v109,0xffff0000,v80
v_lshlrev_b32 v110,16,v81
v_and_b32 v111,0xffff0000,v81
v_lshlrev_b32 v112,16,v82
v_and_b32 v113,0xffff0000,v82
v_lshlrev_b32 v114,16,v83
v_and_b32 v115,0xffff0000,v83
s_branch Lr_c0_%=
Lr_b0_%=:
s_waitcnt vmcnt(10)
T v[68:69],10,0
T v[70:71],10,64
T v[72:73],10,1024
T v[74:75],10,1088
T v[76:77],10,2048
T v[78:79],10,2112
T v[80:81],10,3072
T v[82:83],10,3136
v_mfma_f32_16x16x32_bf16 v[36:39],v[32:35],v[164:167],0
s_add_u32 s79,s45,s46
s_cmp_lt_u32 s45,64
s_cselect_b32 s78,s49,0
v_mfma_f32_16x16x32_bf16 v[40:43],v[164:167],v[164:167],0
s_cmp_ge_u32 s79,64
s_cselect_b32 s78,s78,0
s_add_u32 s62,s79,s78
M 36,v[32:35],168
s_lshr_b32 s78,s45,1
s_and_b32 s79,s45,1
s_lshl_b32 s60,s45,9
M 40,v[168:171],168
s_lshl_b32 s57,s78,15
s_lshl_b32 s59,s78,9
s_mov_b32 s45,s62
M 36,v[32:35],172
s_lshl_b32 s79,s79,7
s_min_u32 s78,s64,s47
s_mul_i32 s56,s45,16384
M 40,v[172:175],172
s_lshl_b32 s55,s62,14
s_cmp_lt_u32 s62,s47
s_cselect_b32 s55,s55,0x7fff0000
M 36,v[32:35],176
buffer_load_dwordx4 v[196:199],v8,s[84:87],s55 offen offset:0 nt
M 40,v[176:179],176
buffer_load_dwordx4 v[200:203],v8,s[84:87],s55 offen offset:128 nt
v_mfma_f32_16x16x32_bf16 v[116:119],v[26:29],v[164:167],0
buffer_load_dwordx4 v[204:207],v8,s[84:87],s55 offen offset:256 nt
v_mfma_f32_16x16x32_bf16 v[120:123],v[24:27],v[164:167],0
buffer_load_dwordx4 v[208:211],v8,s[84:87],s55 offen offset:384 nt
v_mfma_f32_16x16x32_bf16 v[124:127],v[26:29],v[168:171],0
s_add_u32 s57,s57,s79
s_add_u32 s56,s56,98566144
s_cmp_lt_u32 s45,s78
v_mfma_f32_16x16x32_bf16 v[128:131],v[24:27],v[168:171],0
s_cselect_b32 s56,s56,0x7fff0000
v_add_f32_dpp v44,v36,v36 row_ror:8 row_mask:0xf bank_mask:0xf
v_cndmask_b32 v45,v40,v41,s[70:71]
v_mfma_f32_16x16x32_bf16 v[132:135],v[26:29],v[172:175],0
s_cselect_b32 s67,1,0
ds_write_b32 v2,v44 offset:0
v_mfma_f32_16x16x32_bf16 v[136:139],v[24:27],v[172:175],0
s_add_u32 s57,s57,8388608
v_cndmask_b32 v46,v42,v43,s[70:71]
v_cndmask_b32 v45,v45,v46,s[72:73]
v_mfma_f32_16x16x32_bf16 v[140:143],v[26:29],v[176:179],0
s_add_u32 s59,s59,s79
ds_write_b32 v0,v45 offset:0
v_mfma_f32_16x16x32_bf16 v[144:147],v[24:27],v[176:179],0
L 2
s_add_u32 m0,s65,16384
s_add_u32 s58,s57,4096
v_mfma_f32_16x16x32_bf16 v[84:87],v[26:29],v[68:71],0
D 9,56,0
v_mfma_f32_16x16x32_bf16 v[88:91],v[24:27],v[68:71],0
D 9,56,1024
v_mfma_f32_16x16x32_bf16 v[92:95],v[26:29],v[72:75],0
L 0
D 9,56,2048
v_mfma_f32_16x16x32_bf16 v[96:99],v[24:27],v[72:75],0
s_barrier
R 52,3,0
R 56,3,16
v_mfma_f32_16x16x32_bf16 v[100:103],v[26:29],v[76:79],0
s_add_u32 s59,s59,106954752
s_add_u32 s60,s60,s66
R 60,3,32
v_mfma_f32_16x16x32_bf16 v[104:107],v[24:27],v[76:79],0
D 9,56,3072
v_mfma_f32_16x16x32_bf16 v[108:111],v[26:29],v[80:83],0
v_mfma_f32_16x16x32_bf16 v[112:115],v[24:27],v[80:83],0
Lr_c0_%=:
L 0
v_pk_add_f32 v[52:53],v[52:53],v[54:55]
v_pk_add_f32 v[56:57],v[56:57],v[58:59]
v_pk_add_f32 v[60:61],v[60:61],v[62:63]
v_add_f32 v52,v52,v53
v_pk_add_f32 v[56:57],v[56:57],v[60:61]
v_mul_f32 v52,s68,v52
v_add_f32 v53,v56,v57
v_mul_f32 v53,s68,v53
v_fma_f32 v53,-v52,v52,v53
v_max_f32 v53,0,v53
s_waitcnt vmcnt(12)
buffer_store_dword v17,v5,s[36:39],s63 offen offset:0 sc0 sc1
s_mov_b32 s63,0x7fff0000
v_add_f32 v53,s69,v53
v_rsq_f32 v48,v53
s_nop 0
v_mul_f32_e64 v50,-v52,v48
v_mov_b32 v49,v48
v_mov_b32 v51,v50
v_pk_fma_f32 v[52:53],v[116:117],v[48:49],v[50:51]
v_pk_fma_f32 v[54:55],v[118:119],v[48:49],v[50:51]
v_pk_fma_f32 v[56:57],v[120:121],v[48:49],v[50:51]
v_pk_fma_f32 v[58:59],v[122:123],v[48:49],v[50:51]
v_pk_fma_f32 v[116:117],v[52:53],v[84:85],v[116:117]
v_pk_fma_f32 v[118:119],v[54:55],v[86:87],v[118:119]
v_pk_fma_f32 v[120:121],v[56:57],v[88:89],v[120:121]
v_pk_fma_f32 v[122:123],v[58:59],v[90:91],v[122:123]
v_pk_fma_f32 v[52:53],v[124:125],v[48:49],v[50:51]
v_pk_fma_f32 v[54:55],v[126:127],v[48:49],v[50:51]
v_pk_fma_f32 v[56:57],v[128:129],v[48:49],v[50:51]
v_pk_fma_f32 v[58:59],v[130:131],v[48:49],v[50:51]
v_pk_fma_f32 v[124:125],v[52:53],v[92:93],v[124:125]
v_pk_fma_f32 v[126:127],v[54:55],v[94:95],v[126:127]
v_pk_fma_f32 v[128:129],v[56:57],v[96:97],v[128:129]
v_pk_fma_f32 v[130:131],v[58:59],v[98:99],v[130:131]
v_pk_fma_f32 v[52:53],v[132:133],v[48:49],v[50:51]
v_pk_fma_f32 v[54:55],v[134:135],v[48:49],v[50:51]
v_pk_fma_f32 v[56:57],v[136:137],v[48:49],v[50:51]
v_pk_fma_f32 v[58:59],v[138:139],v[48:49],v[50:51]
v_pk_fma_f32 v[132:133],v[52:53],v[100:101],v[132:133]
v_pk_fma_f32 v[134:135],v[54:55],v[102:103],v[134:135]
v_pk_fma_f32 v[136:137],v[56:57],v[104:105],v[136:137]
v_pk_fma_f32 v[138:139],v[58:59],v[106:107],v[138:139]
v_pk_fma_f32 v[52:53],v[140:141],v[48:49],v[50:51]
v_pk_fma_f32 v[54:55],v[142:143],v[48:49],v[50:51]
v_pk_fma_f32 v[56:57],v[144:145],v[48:49],v[50:51]
v_pk_fma_f32 v[58:59],v[146:147],v[48:49],v[50:51]
v_pk_fma_f32 v[140:141],v[52:53],v[108:109],v[140:141]
v_pk_fma_f32 v[142:143],v[54:55],v[110:111],v[142:143]
v_pk_fma_f32 v[144:145],v[56:57],v[112:113],v[144:145]
v_pk_fma_f32 v[146:147],v[58:59],v[114:115],v[146:147]
v_cvt_pk_bf16_f32 v148,v116,v117
v_cvt_pk_bf16_f32 v149,v118,v119
v_cvt_pk_bf16_f32 v150,v120,v121
v_cvt_pk_bf16_f32 v151,v122,v123
v_cvt_pk_bf16_f32 v152,v124,v125
v_cvt_pk_bf16_f32 v153,v126,v127
v_mfma_f32_16x16x32_bf16 v[36:39],v[32:35],v[148:151],0
v_cvt_pk_bf16_f32 v154,v128,v129
v_cvt_pk_bf16_f32 v155,v130,v131
v_mfma_f32_16x16x32_bf16 v[40:43],v[148:151],v[148:151],0
v_cvt_pk_bf16_f32 v156,v132,v133
v_cvt_pk_bf16_f32 v157,v134,v135
M 36,v[32:35],152
v_cvt_pk_bf16_f32 v158,v136,v137
v_cvt_pk_bf16_f32 v159,v138,v139
M 40,v[152:155],152
v_cvt_pk_bf16_f32 v160,v140,v141
v_cvt_pk_bf16_f32 v161,v142,v143
M 36,v[32:35],156
v_cvt_pk_bf16_f32 v162,v144,v145
v_cvt_pk_bf16_f32 v163,v146,v147
M 40,v[156:159],156
buffer_store_dwordx4 v[148:151],v1,s[36:39],s57 offen offset:0 sc0 sc1
M 36,v[32:35],160
buffer_store_dwordx4 v[152:155],v1,s[36:39],s57 offen offset:2048 sc0 sc1
M 40,v[160:163],160
buffer_store_dwordx4 v[156:159],v1,s[36:39],s58 offen offset:0 sc0 sc1
s_nop 4
v_add_f32_dpp v44,v36,v36 row_ror:8 row_mask:0xf bank_mask:0xf
s_nop 0
v_cndmask_b32 v45,v40,v41,s[70:71]
v_cndmask_b32 v46,v42,v43,s[70:71]
ds_write_b32 v2,v44 offset:1024
v_cndmask_b32 v45,v45,v46,s[72:73]
ds_write_b32 v0,v45 offset:1024
buffer_store_dwordx4 v[160:163],v1,s[36:39],s58 offen offset:2048 sc0 sc1
s_waitcnt vmcnt(13) lgkmcnt(0)
s_barrier
R 52,3,1024
R 56,3,1040
R 60,3,1056
L 0
v_pk_add_f32 v[52:53],v[52:53],v[54:55]
v_pk_add_f32 v[56:57],v[56:57],v[58:59]
v_pk_add_f32 v[60:61],v[60:61],v[62:63]
v_add_f32 v52,v52,v53
v_pk_add_f32 v[56:57],v[56:57],v[60:61]
v_mul_f32 v52,s68,v52
v_add_f32 v53,v56,v57
v_mul_f32 v53,s68,v53
v_fma_f32 v53,-v52,v52,v53
v_max_f32 v53,0,v53
v_add_f32 v54,s69,v53
v_fma_f32 v55,s69,v54,v53
v_rsq_f32 v65,v54
v_rsq_f32 v54,v55
v_mul_f32_e64 v64,-v52,v65
v_mul_f32 v63,v55,v54
v_mul_f32 v61,s76,v54
v_mul_f32 v55,v54,v54
v_mul_f32 v62,s77,v55
v_mul_f32 v62,v62,v61
v_cndmask_b32 v60,v52,v64,s[74:75]
v_cndmask_b32 v61,v61,v65,s[74:75]
buffer_store_dwordx4 v[60:63],v4,s[36:39],s59 offen offset:0 sc0 sc1
s_mov_b32 s63,s60
s_cmp_lt_u32 s45,s80
s_cbranch_scc1 Lr_bg0_%=
s_swappc_b64 s[88:89],s[94:95]
s_add_u32 s94,s94,256
s_addc_u32 s95,s95,0
s_branch Lr_bh0_%=
Lr_bg0_%=:
buffer_store_dword v17,v5,s[36:39],s81 offen offset:0 sc0 sc1
buffer_store_dword v17,v5,s[36:39],s81 offen offset:0 sc0 sc1
buffer_store_dword v17,v5,s[36:39],s81 offen offset:0 sc0 sc1
buffer_store_dword v17,v5,s[36:39],s81 offen offset:0 sc0 sc1
Lr_bh0_%=:
s_cmp_lt_u32 s45,s47
s_cbranch_scc0 Lr_mid_%=
Lr_a1_%=:
s_cmp_lg_u32 s67,0
s_cbranch_scc1 Lr_b1_%=
s_waitcnt vmcnt(14)
v_mfma_f32_16x16x32_bf16 v[36:39],v[32:35],v[196:199],0
s_add_u32 s79,s45,s46
s_cmp_lt_u32 s45,64
s_cselect_b32 s78,s49,0
v_mfma_f32_16x16x32_bf16 v[40:43],v[196:199],v[196:199],0
s_cmp_ge_u32 s79,64
s_cselect_b32 s78,s78,0
s_add_u32 s62,s79,s78
M 36,v[32:35],200
s_lshr_b32 s78,s45,1
s_and_b32 s79,s45,1
s_lshl_b32 s79,s79,7
M 40,v[200:203],200
s_lshl_b32 s57,s78,15
s_lshl_b32 s55,s62,14
s_cmp_lt_u32 s62,s47
M 36,v[32:35],204
s_cselect_b32 s55,s55,0x7fff0000
s_add_u32 s57,s57,s79
s_lshl_b32 s59,s78,9
M 40,v[204:207],204
buffer_load_dwordx4 v[164:167],v8,s[84:87],s55 offen offset:0 nt
M 36,v[32:35],208
buffer_load_dwordx4 v[168:171],v8,s[84:87],s55 offen offset:128 nt
M 40,v[208:211],208
buffer_load_dwordx4 v[172:175],v8,s[84:87],s55 offen offset:256 nt
v_mfma_f32_16x16x32_bf16 v[116:119],v[26:29],v[196:199],0
buffer_load_dwordx4 v[176:179],v8,s[84:87],s55 offen offset:384 nt
v_mfma_f32_16x16x32_bf16 v[120:123],v[24:27],v[196:199],0
s_add_u32 s57,s57,8388608
s_add_u32 s59,s59,s79
s_lshl_b32 s60,s45,9
v_mfma_f32_16x16x32_bf16 v[124:127],v[26:29],v[200:203],0
s_add_u32 s58,s57,4096
s_add_u32 s59,s59,106954752
s_add_u32 s60,s60,s66
v_mfma_f32_16x16x32_bf16 v[128:131],v[24:27],v[200:203],0
v_add_f32_dpp v44,v36,v36 row_ror:8 row_mask:0xf bank_mask:0xf
v_cndmask_b32 v45,v40,v41,s[70:71]
v_mfma_f32_16x16x32_bf16 v[132:135],v[26:29],v[204:207],0
ds_write_b32 v2,v44 offset:0
v_mfma_f32_16x16x32_bf16 v[136:139],v[24:27],v[204:207],0
v_cndmask_b32 v46,v42,v43,s[70:71]
v_cndmask_b32 v45,v45,v46,s[72:73]
)"
R"(v_mfma_f32_16x16x32_bf16 v[140:143],v[26:29],v[208:211],0
ds_write_b32 v0,v45 offset:0
v_mfma_f32_16x16x32_bf16 v[144:147],v[24:27],v[208:211],0
L 0
s_barrier
R 52,3,0
R 56,3,16
R 60,3,32
s_waitcnt vmcnt(8)
buffer_store_dword v17,v5,s[36:39],s63 offen offset:0 sc0 sc1
s_mov_b32 s63,0x7fff0000
s_cmp_lt_u32 s45,64
s_cselect_b32 s64,-192,0
s_add_u32 s64,s64,256
s_cmp_lt_u32 s45,64
s_cselect_b32 s61,0,32768
s_add_u32 s61,s61,111177728
s_mul_i32 s56,s45,16384
s_add_u32 s56,s56,98566144
Lr_bpy1_%=:
buffer_load_dword v18,v7,s[36:39],s61 offen offset:0 sc1
s_add_u32 s82,s82,1
s_waitcnt vmcnt(0)
v_cmp_eq_u32 vcc,s53,v18
s_nop 0
s_cmp_eq_u64 vcc,-1
s_cbranch_scc1 Lr_bqy1_%=
s_sleep 2
s_cmp_lt_u32 s82,20000
s_cbranch_scc1 Lr_bpy1_%=
Lr_bqy1_%=:
s_add_u32 m0,s65,16384
s_nop 0
D 9,56,0
D 9,56,1024
D 9,56,2048
D 9,56,3072
s_waitcnt vmcnt(0)
T v[68:69],10,16384
T v[70:71],10,16448
T v[72:73],10,17408
T v[74:75],10,17472
T v[76:77],10,18432
T v[78:79],10,18496
T v[80:81],10,19456
T v[82:83],10,19520
s_mov_b32 s45,s62
s_min_u32 s78,s64,s47
s_mul_i32 s56,s45,16384
s_add_u32 s56,s56,98566144
s_cmp_lt_u32 s45,s78
s_cselect_b32 s67,1,0
s_cselect_b32 s56,s56,0x7fff0000
s_add_u32 m0,s65,0
s_nop 0
D 9,56,0
D 9,56,1024
D 9,56,2048
D 9,56,3072
L 4
v_lshlrev_b32 v84,16,v68
v_and_b32 v85,0xffff0000,v68
v_lshlrev_b32 v86,16,v69
v_and_b32 v87,0xffff0000,v69
v_lshlrev_b32 v88,16,v70
v_and_b32 v89,0xffff0000,v70
v_lshlrev_b32 v90,16,v71
v_and_b32 v91,0xffff0000,v71
v_lshlrev_b32 v92,16,v72
v_and_b32 v93,0xffff0000,v72
v_lshlrev_b32 v94,16,v73
v_and_b32 v95,0xffff0000,v73
v_lshlrev_b32 v96,16,v74
v_and_b32 v97,0xffff0000,v74
v_lshlrev_b32 v98,16,v75
v_and_b32 v99,0xffff0000,v75
L 0
v_lshlrev_b32 v100,16,v76
v_and_b32 v101,0xffff0000,v76
v_lshlrev_b32 v102,16,v77
v_and_b32 v103,0xffff0000,v77
v_lshlrev_b32 v104,16,v78
v_and_b32 v105,0xffff0000,v78
v_lshlrev_b32 v106,16,v79
v_and_b32 v107,0xffff0000,v79
v_lshlrev_b32 v108,16,v80
v_and_b32 v109,0xffff0000,v80
v_lshlrev_b32 v110,16,v81
v_and_b32 v111,0xffff0000,v81
v_lshlrev_b32 v112,16,v82
v_and_b32 v113,0xffff0000,v82
v_lshlrev_b32 v114,16,v83
v_and_b32 v115,0xffff0000,v83
s_branch Lr_c1_%=
Lr_b1_%=:
s_waitcnt vmcnt(10)
T v[68:69],10,16384
T v[70:71],10,16448
T v[72:73],10,17408
T v[74:75],10,17472
T v[76:77],10,18432
T v[78:79],10,18496
T v[80:81],10,19456
T v[82:83],10,19520
v_mfma_f32_16x16x32_bf16 v[36:39],v[32:35],v[196:199],0
s_add_u32 s79,s45,s46
s_cmp_lt_u32 s45,64
s_cselect_b32 s78,s49,0
v_mfma_f32_16x16x32_bf16 v[40:43],v[196:199],v[196:199],0
s_cmp_ge_u32 s79,64
s_cselect_b32 s78,s78,0
s_add_u32 s62,s79,s78
M 36,v[32:35],200
s_lshr_b32 s78,s45,1
s_and_b32 s79,s45,1
s_lshl_b32 s60,s45,9
M 40,v[200:203],200
s_lshl_b32 s57,s78,15
s_lshl_b32 s59,s78,9
s_mov_b32 s45,s62
M 36,v[32:35],204
s_lshl_b32 s79,s79,7
s_min_u32 s78,s64,s47
s_mul_i32 s56,s45,16384
M 40,v[204:207],204
s_lshl_b32 s55,s62,14
s_cmp_lt_u32 s62,s47
s_cselect_b32 s55,s55,0x7fff0000
M 36,v[32:35],208
buffer_load_dwordx4 v[164:167],v8,s[84:87],s55 offen offset:0 nt
M 40,v[208:211],208
buffer_load_dwordx4 v[168:171],v8,s[84:87],s55 offen offset:128 nt
v_mfma_f32_16x16x32_bf16 v[116:119],v[26:29],v[196:199],0
buffer_load_dwordx4 v[172:175],v8,s[84:87],s55 offen offset:256 nt
v_mfma_f32_16x16x32_bf16 v[120:123],v[24:27],v[196:199],0
buffer_load_dwordx4 v[176:179],v8,s[84:87],s55 offen offset:384 nt
v_mfma_f32_16x16x32_bf16 v[124:127],v[26:29],v[200:203],0
s_add_u32 s57,s57,s79
s_add_u32 s56,s56,98566144
s_cmp_lt_u32 s45,s78
v_mfma_f32_16x16x32_bf16 v[128:131],v[24:27],v[200:203],0
s_cselect_b32 s56,s56,0x7fff0000
v_add_f32_dpp v44,v36,v36 row_ror:8 row_mask:0xf bank_mask:0xf
v_cndmask_b32 v45,v40,v41,s[70:71]
v_mfma_f32_16x16x32_bf16 v[132:135],v[26:29],v[204:207],0
s_cselect_b32 s67,1,0
ds_write_b32 v2,v44 offset:0
v_mfma_f32_16x16x32_bf16 v[136:139],v[24:27],v[204:207],0
s_add_u32 s57,s57,8388608
v_cndmask_b32 v46,v42,v43,s[70:71]
v_cndmask_b32 v45,v45,v46,s[72:73]
v_mfma_f32_16x16x32_bf16 v[140:143],v[26:29],v[208:211],0
s_add_u32 s59,s59,s79
ds_write_b32 v0,v45 offset:0
v_mfma_f32_16x16x32_bf16 v[144:147],v[24:27],v[208:211],0
L 2
s_add_u32 m0,s65,0
s_add_u32 s58,s57,4096
v_mfma_f32_16x16x32_bf16 v[84:87],v[26:29],v[68:71],0
D 9,56,0
v_mfma_f32_16x16x32_bf16 v[88:91],v[24:27],v[68:71],0
D 9,56,1024
v_mfma_f32_16x16x32_bf16 v[92:95],v[26:29],v[72:75],0
L 0
D 9,56,2048
v_mfma_f32_16x16x32_bf16 v[96:99],v[24:27],v[72:75],0
s_barrier
R 52,3,0
R 56,3,16
v_mfma_f32_16x16x32_bf16 v[100:103],v[26:29],v[76:79],0
s_add_u32 s59,s59,106954752
s_add_u32 s60,s60,s66
R 60,3,32
v_mfma_f32_16x16x32_bf16 v[104:107],v[24:27],v[76:79],0
D 9,56,3072
v_mfma_f32_16x16x32_bf16 v[108:111],v[26:29],v[80:83],0
v_mfma_f32_16x16x32_bf16 v[112:115],v[24:27],v[80:83],0
Lr_c1_%=:
L 0
v_pk_add_f32 v[52:53],v[52:53],v[54:55]
v_pk_add_f32 v[56:57],v[56:57],v[58:59]
v_pk_add_f32 v[60:61],v[60:61],v[62:63]
v_add_f32 v52,v52,v53
v_pk_add_f32 v[56:57],v[56:57],v[60:61]
v_mul_f32 v52,s68,v52
v_add_f32 v53,v56,v57
v_mul_f32 v53,s68,v53
v_fma_f32 v53,-v52,v52,v53
v_max_f32 v53,0,v53
s_waitcnt vmcnt(12)
buffer_store_dword v17,v5,s[36:39],s63 offen offset:0 sc0 sc1
s_mov_b32 s63,0x7fff0000
v_add_f32 v53,s69,v53
v_rsq_f32 v48,v53
s_nop 0
v_mul_f32_e64 v50,-v52,v48
v_mov_b32 v49,v48
v_mov_b32 v51,v50
v_pk_fma_f32 v[52:53],v[116:117],v[48:49],v[50:51]
v_pk_fma_f32 v[54:55],v[118:119],v[48:49],v[50:51]
v_pk_fma_f32 v[56:57],v[120:121],v[48:49],v[50:51]
v_pk_fma_f32 v[58:59],v[122:123],v[48:49],v[50:51]
v_pk_fma_f32 v[116:117],v[52:53],v[84:85],v[116:117]
v_pk_fma_f32 v[118:119],v[54:55],v[86:87],v[118:119]
v_pk_fma_f32 v[120:121],v[56:57],v[88:89],v[120:121]
v_pk_fma_f32 v[122:123],v[58:59],v[90:91],v[122:123]
v_pk_fma_f32 v[52:53],v[124:125],v[48:49],v[50:51]
v_pk_fma_f32 v[54:55],v[126:127],v[48:49],v[50:51]
v_pk_fma_f32 v[56:57],v[128:129],v[48:49],v[50:51]
v_pk_fma_f32 v[58:59],v[130:131],v[48:49],v[50:51]
v_pk_fma_f32 v[124:125],v[52:53],v[92:93],v[124:125]
v_pk_fma_f32 v[126:127],v[54:55],v[94:95],v[126:127]
v_pk_fma_f32 v[128:129],v[56:57],v[96:97],v[128:129]
v_pk_fma_f32 v[130:131],v[58:59],v[98:99],v[130:131]
v_pk_fma_f32 v[52:53],v[132:133],v[48:49],v[50:51]
v_pk_fma_f32 v[54:55],v[134:135],v[48:49],v[50:51]
v_pk_fma_f32 v[56:57],v[136:137],v[48:49],v[50:51]
v_pk_fma_f32 v[58:59],v[138:139],v[48:49],v[50:51]
v_pk_fma_f32 v[132:133],v[52:53],v[100:101],v[132:133]
v_pk_fma_f32 v[134:135],v[54:55],v[102:103],v[134:135]
v_pk_fma_f32 v[136:137],v[56:57],v[104:105],v[136:137]
v_pk_fma_f32 v[138:139],v[58:59],v[106:107],v[138:139]
v_pk_fma_f32 v[52:53],v[140:141],v[48:49],v[50:51]
v_pk_fma_f32 v[54:55],v[142:143],v[48:49],v[50:51]
v_pk_fma_f32 v[56:57],v[144:145],v[48:49],v[50:51]
v_pk_fma_f32 v[58:59],v[146:147],v[48:49],v[50:51]
v_pk_fma_f32 v[140:141],v[52:53],v[108:109],v[140:141]
v_pk_fma_f32 v[142:143],v[54:55],v[110:111],v[142:143]
v_pk_fma_f32 v[144:145],v[56:57],v[112:113],v[144:145]
v_pk_fma_f32 v[146:147],v[58:59],v[114:115],v[146:147]
v_cvt_pk_bf16_f32 v148,v116,v117
v_cvt_pk_bf16_f32 v149,v118,v119
v_cvt_pk_bf16_f32 v150,v120,v121
v_cvt_pk_bf16_f32 v151,v122,v123
v_cvt_pk_bf16_f32 v152,v124,v125
v_cvt_pk_bf16_f32 v153,v126,v127
v_mfma_f32_16x16x32_bf16 v[36:39],v[32:35],v[148:151],0
v_cvt_pk_bf16_f32 v154,v128,v129
v_cvt_pk_bf16_f32 v155,v130,v131
v_mfma_f32_16x16x32_bf16 v[40:43],v[148:151],v[148:151],0
v_cvt_pk_bf16_f32 v156,v132,v133
v_cvt_pk_bf16_f32 v157,v134,v135
M 36,v[32:35],152
v_cvt_pk_bf16_f32 v158,v136,v137
v_cvt_pk_bf16_f32 v159,v138,v139
M 40,v[152:155],152
v_cvt_pk_bf16_f32 v160,v140,v141
v_cvt_pk_bf16_f32 v161,v142,v143
M 36,v[32:35],156
v_cvt_pk_bf16_f32 v162,v144,v145
v_cvt_pk_bf16_f32 v163,v146,v147
M 40,v[156:159],156
buffer_store_dwordx4 v[148:151],v1,s[36:39],s57 offen offset:0 sc0 sc1
M 36,v[32:35],160
buffer_store_dwordx4 v[152:155],v1,s[36:39],s57 offen offset:2048 sc0 sc1
M 40,v[160:163],160
buffer_store_dwordx4 v[156:159],v1,s[36:39],s58 offen offset:0 sc0 sc1
s_nop 4
v_add_f32_dpp v44,v36,v36 row_ror:8 row_mask:0xf bank_mask:0xf
s_nop 0
v_cndmask_b32 v45,v40,v41,s[70:71]
v_cndmask_b32 v46,v42,v43,s[70:71]
ds_write_b32 v2,v44 offset:1024
v_cndmask_b32 v45,v45,v46,s[72:73]
ds_write_b32 v0,v45 offset:1024
buffer_store_dwordx4 v[160:163],v1,s[36:39],s58 offen offset:2048 sc0 sc1
s_waitcnt vmcnt(13) lgkmcnt(0)
s_barrier
R 52,3,1024
R 56,3,1040
R 60,3,1056
L 0
v_pk_add_f32 v[52:53],v[52:53],v[54:55]
v_pk_add_f32 v[56:57],v[56:57],v[58:59]
v_pk_add_f32 v[60:61],v[60:61],v[62:63]
v_add_f32 v52,v52,v53
v_pk_add_f32 v[56:57],v[56:57],v[60:61]
v_mul_f32 v52,s68,v52
v_add_f32 v53,v56,v57
v_mul_f32 v53,s68,v53
v_fma_f32 v53,-v52,v52,v53
v_max_f32 v53,0,v53
v_add_f32 v54,s69,v53
v_fma_f32 v55,s69,v54,v53
v_rsq_f32 v65,v54
v_rsq_f32 v54,v55
v_mul_f32_e64 v64,-v52,v65
v_mul_f32 v63,v55,v54
v_mul_f32 v61,s76,v54
v_mul_f32 v55,v54,v54
v_mul_f32 v62,s77,v55
v_mul_f32 v62,v62,v61
v_cndmask_b32 v60,v52,v64,s[74:75]
v_cndmask_b32 v61,v61,v65,s[74:75]
buffer_store_dwordx4 v[60:63],v4,s[36:39],s59 offen offset:0 sc0 sc1
s_mov_b32 s63,s60
s_cmp_lt_u32 s45,s80
s_cbranch_scc1 Lr_bg1_%=
s_swappc_b64 s[88:89],s[94:95]
s_add_u32 s94,s94,256
s_addc_u32 s95,s95,0
s_branch Lr_bh1_%=
Lr_bg1_%=:
buffer_store_dword v17,v5,s[36:39],s81 offen offset:0 sc0 sc1
buffer_store_dword v17,v5,s[36:39],s81 offen offset:0 sc0 sc1
buffer_store_dword v17,v5,s[36:39],s81 offen offset:0 sc0 sc1
buffer_store_dword v17,v5,s[36:39],s81 offen offset:0 sc0 sc1
Lr_bh1_%=:
s_cmp_lt_u32 s45,s47
s_cbranch_scc1 Lr_a0_%=
Lr_mid_%=:
s_waitcnt vmcnt(4)
buffer_store_dword v17,v5,s[36:39],s63 offen offset:0 sc0 sc1
s_mov_b32 s63,0x7fff0000
s_mov_b32 s45,s93
s_cmp_eq_u32 s93,0
s_cbranch_scc1 Lr_h0_%=
s_mov_b64 s[40:41],%24
s_mov_b32 s42,7340032
s_mov_b32 s43,0x00020000
s_lshl_b32 s57,s44,11
s_add_u32 s55,s50,1441792
s_add_u32 m0,s57,106496
s_nop 0
buffer_load_dwordx4 v13,s[40:43],s55 offen offset:0  lds
s_add_u32 m0,s57,107520
s_nop 0
buffer_load_dwordx4 v14,s[40:43],s55 offen offset:0  lds
s_add_u32 m0,s57,114560
s_nop 0
buffer_load_dwordx4 v13,s[40:43],s55 offen offset:128  lds
s_add_u32 m0,s57,115584
s_nop 0
buffer_load_dwordx4 v14,s[40:43],s55 offen offset:128  lds
s_add_u32 s55,s50,1572864
s_add_u32 m0,s57,122880
s_nop 0
buffer_load_dwordx4 v13,s[40:43],s55 offen offset:0  lds
s_add_u32 m0,s57,123904
s_nop 0
buffer_load_dwordx4 v14,s[40:43],s55 offen offset:0  lds
s_add_u32 m0,s57,130944
s_nop 0
buffer_load_dwordx4 v13,s[40:43],s55 offen offset:128  lds
s_add_u32 m0,s57,131968
s_nop 0
buffer_load_dwordx4 v14,s[40:43],s55 offen offset:128  lds
s_add_u32 s55,s50,1703936
s_add_u32 m0,s57,139264
s_nop 0
buffer_load_dwordx4 v13,s[40:43],s55 offen offset:0  lds
s_add_u32 m0,s57,140288
s_nop 0
buffer_load_dwordx4 v14,s[40:43],s55 offen offset:0  lds
s_add_u32 m0,s57,147328
s_nop 0
buffer_load_dwordx4 v13,s[40:43],s55 offen offset:128  lds
s_add_u32 m0,s57,148352
s_nop 0
buffer_load_dwordx4 v14,s[40:43],s55 offen offset:128  lds
s_add_u32 s55,s50,1835008
s_add_u32 m0,s57,40960
s_nop 0
buffer_load_dwordx4 v13,s[40:43],s55 offen offset:0  lds
s_add_u32 m0,s57,41984
s_nop 0
buffer_load_dwordx4 v14,s[40:43],s55 offen offset:0  lds
s_add_u32 m0,s57,49024
s_nop 0
buffer_load_dwordx4 v13,s[40:43],s55 offen offset:128  lds
s_add_u32 m0,s57,50048
s_nop 0
buffer_load_dwordx4 v14,s[40:43],s55 offen offset:128  lds
s_add_u32 s55,s50,1966080
s_add_u32 m0,s57,57344
s_nop 0
buffer_load_dwordx4 v13,s[40:43],s55 offen offset:0  lds
s_add_u32 m0,s57,58368
s_nop 0
buffer_load_dwordx4 v14,s[40:43],s55 offen offset:0  lds
s_add_u32 m0,s57,65408
s_nop 0
buffer_load_dwordx4 v13,s[40:43],s55 offen offset:128  lds
s_add_u32 m0,s57,66432
s_nop 0
buffer_load_dwordx4 v14,s[40:43],s55 offen offset:128  lds
Lr_h0_%=:
s_lshr_b32 s78,s45,1
s_and_b32 s79,s45,1
s_lshl_b32 s79,s79,7
s_lshl_b32 s55,s78,15
s_add_u32 s55,s55,s79
s_mul_i32 s56,s45,16384
s_add_u32 s56,s56,98566144
s_lshl_b32 s61,s78,11
s_add_u32 s61,s61,110587904
s_add_u32 s78,s45,s46
s_lshr_b32 s78,s78,1
s_lshl_b32 s62,s78,11
s_add_u32 s62,s62,110587904
s_add_u32 m0,s65,0
s_nop 0
D 9,56,0
D 9,56,1024
D 9,56,2048
D 9,56,3072
Lr_bps_%=:
buffer_load_dword v18,v6,s[36:39],s61 offen offset:0 sc1
s_add_u32 s82,s82,1
s_waitcnt vmcnt(0)
v_cmp_eq_u32 vcc,s53,v18
s_nop 0
s_cmp_eq_u64 vcc,-1
s_cbranch_scc1 Lr_bqs_%=
s_sleep 2
s_cmp_lt_u32 s82,20000
s_cbranch_scc1 Lr_bps_%=
Lr_bqs_%=:
s_add_u32 s78,s55,71303168
buffer_load_dwordx4 v[164:167],v1,s[36:39],s78 offen offset:0 sc1
buffer_load_dwordx4 v[168:171],v1,s[36:39],s78 offen offset:2048 sc1
s_add_u32 s78,s55,71307264
buffer_load_dwordx4 v[172:175],v1,s[36:39],s78 offen offset:0 sc1
buffer_load_dwordx4 v[176:179],v1,s[36:39],s78 offen offset:2048 sc1
s_add_u32 s78,s55,89128960
buffer_load_dwordx4 v[180:183],v1,s[36:39],s78 offen offset:0 sc1
buffer_load_dwordx4 v[184:187],v1,s[36:39],s78 offen offset:2048 sc1
s_add_u32 s78,s55,89133056
buffer_load_dwordx4 v[188:191],v1,s[36:39],s78 offen offset:0 sc1
buffer_load_dwordx4 v[192:195],v1,s[36:39],s78 offen offset:2048 sc1
buffer_load_dword v18,v6,s[36:39],s62 offen offset:0 sc1
s_cmp_eq_u32 s93,0
s_cbranch_scc1 Lr_h2_%=
s_mov_b64 s[40:41],%24
s_mov_b32 s42,7340032
s_mov_b32 s43,0x00020000
s_lshl_b32 s57,s44,11
L 0
s_barrier
s_add_u32 s55,s50,2097152
s_add_u32 m0,s57,73728
s_nop 0
buffer_load_dwordx4 v13,s[40:43],s55 offen offset:0  lds
s_add_u32 m0,s57,74752
s_nop 0
buffer_load_dwordx4 v14,s[40:43],s55 offen offset:0  lds
s_add_u32 m0,s57,81792
s_nop 0
buffer_load_dwordx4 v13,s[40:43],s55 offen offset:128  lds
s_add_u32 m0,s57,82816
s_nop 0
buffer_load_dwordx4 v14,s[40:43],s55 offen offset:128  lds
v_add_u32 v44,90112,v15
v_add_u32 v45,90112,v16
T a[160:161],44,0
T a[162:163],44,512
T a[164:165],45,0
T a[166:167],45,512
T a[168:169],44,4096
T a[170:171],44,4608
T a[172:173],45,4096
T a[174:175],45,4608
L 0
s_barrier
s_add_u32 s55,s50,2228224
s_add_u32 m0,s57,90112
s_nop 0
buffer_load_dwordx4 v13,s[40:43],s55 offen offset:0  lds
s_add_u32 m0,s57,91136
s_nop 0
buffer_load_dwordx4 v14,s[40:43],s55 offen offset:0  lds
s_add_u32 m0,s57,98176
s_nop 0
buffer_load_dwordx4 v13,s[40:43],s55 offen offset:128  lds
s_add_u32 m0,s57,99200
s_nop 0
buffer_load_dwordx4 v14,s[40:43],s55 offen offset:128  lds
v_add_u32 v44,106496,v15
v_add_u32 v45,106496,v16
T a[176:177],44,0
T a[178:179],44,512
T a[180:181],45,0
T a[182:183],45,512
T a[184:185],44,4096
T a[186:187],44,4608
T a[188:189],45,4096
T a[190:191],45,4608
L 0
s_barrier
s_add_u32 s55,s50,2359296
s_add_u32 m0,s57,106496
s_nop 0
buffer_load_dwordx4 v13,s[40:43],s55 offen offset:0  lds
s_add_u32 m0,s57,107520
s_nop 0
buffer_load_dwordx4 v14,s[40:43],s55 offen offset:0  lds
s_add_u32 m0,s57,114560
s_nop 0
buffer_load_dwordx4 v13,s[40:43],s55 offen offset:128  lds
s_add_u32 m0,s57,115584
)"
R"(s_nop 0
buffer_load_dwordx4 v14,s[40:43],s55 offen offset:128  lds
v_add_u32 v44,122880,v15
v_add_u32 v45,122880,v16
T a[192:193],44,0
T a[194:195],44,512
T a[196:197],45,0
T a[198:199],45,512
T a[200:201],44,4096
T a[202:203],44,4608
T a[204:205],45,4096
T a[206:207],45,4608
L 0
s_barrier
s_add_u32 s55,s50,2490368
s_add_u32 m0,s57,122880
s_nop 0
buffer_load_dwordx4 v13,s[40:43],s55 offen offset:0  lds
s_add_u32 m0,s57,123904
s_nop 0
buffer_load_dwordx4 v14,s[40:43],s55 offen offset:0  lds
s_add_u32 m0,s57,130944
s_nop 0
buffer_load_dwordx4 v13,s[40:43],s55 offen offset:128  lds
s_add_u32 m0,s57,131968
s_nop 0
buffer_load_dwordx4 v14,s[40:43],s55 offen offset:128  lds
v_add_u32 v44,139264,v15
v_add_u32 v45,139264,v16
T a[208:209],44,0
T a[210:211],44,512
T a[212:213],45,0
T a[214:215],45,512
T a[216:217],44,4096
T a[218:219],44,4608
T a[220:221],45,4096
T a[222:223],45,4608
L 0
s_barrier
s_add_u32 s55,s50,2621440
s_add_u32 m0,s57,139264
s_nop 0
buffer_load_dwordx4 v13,s[40:43],s55 offen offset:0  lds
s_add_u32 m0,s57,140288
s_nop 0
buffer_load_dwordx4 v14,s[40:43],s55 offen offset:0  lds
s_add_u32 m0,s57,147328
s_nop 0
buffer_load_dwordx4 v13,s[40:43],s55 offen offset:128  lds
s_add_u32 m0,s57,148352
s_nop 0
buffer_load_dwordx4 v14,s[40:43],s55 offen offset:128  lds
v_add_u32 v44,40960,v15
v_add_u32 v45,40960,v16
T a[224:225],44,0
T a[226:227],44,512
T a[228:229],45,0
T a[230:231],45,512
T a[232:233],44,4096
T a[234:235],44,4608
T a[236:237],45,4096
T a[238:239],45,4608
L 0
s_barrier
s_add_u32 s55,s50,2752512
s_add_u32 m0,s57,40960
s_nop 0
buffer_load_dwordx4 v13,s[40:43],s55 offen offset:0  lds
s_add_u32 m0,s57,41984
s_nop 0
buffer_load_dwordx4 v14,s[40:43],s55 offen offset:0  lds
s_add_u32 m0,s57,49024
s_nop 0
buffer_load_dwordx4 v13,s[40:43],s55 offen offset:128  lds
s_add_u32 m0,s57,50048
s_nop 0
buffer_load_dwordx4 v14,s[40:43],s55 offen offset:128  lds
v_add_u32 v44,57344,v15
v_add_u32 v45,57344,v16
T a[240:241],44,0
T a[242:243],44,512
T a[244:245],45,0
T a[246:247],45,512
T a[248:249],44,4096
T a[250:251],44,4608
T a[252:253],45,4096
T a[254:255],45,4608
L 0
s_barrier
s_waitcnt vmcnt(24)
Lr_h2_%=:
s_lshr_b32 s78,s45,1
s_and_b32 s79,s45,1
s_lshl_b32 s79,s79,7
s_lshl_b32 s57,s78,15
s_add_u32 s57,s57,s79
s_add_u32 s57,s57,12582912
s_add_u32 s58,s57,4096
s_lshl_b32 s59,s78,9
s_add_u32 s59,s59,s79
s_add_u32 s59,s59,107020288
s_lshl_b32 s60,s45,9
s_add_u32 s60,s60,s66
s_add_u32 s60,s60,131072
s_add_u32 s45,s45,s46
s_lshr_b32 s78,s45,1
s_and_b32 s79,s45,1
s_lshl_b32 s79,s79,7
s_lshl_b32 s55,s78,15
s_add_u32 s55,s55,s79
s_mul_i32 s56,s45,16384
s_add_u32 s56,s56,98566144
s_lshl_b32 s61,s78,11
s_add_u32 s61,s61,110587904
s_add_u32 s78,s45,s46
s_lshr_b32 s78,s78,1
s_lshl_b32 s62,s78,11
s_add_u32 s62,s62,110587904
s_waitcnt vmcnt(0)
Lr_it0_%=:
T v[68:69],10,0
T v[70:71],10,64
T v[72:73],10,1024
T v[74:75],10,1088
T v[76:77],10,2048
T v[78:79],10,2112
T v[80:81],10,3072
T v[82:83],10,3136
s_nop 0
v_cmp_eq_u32 vcc,s53,v18
s_nop 0
s_cmp_eq_u64 vcc,-1
s_cselect_b32 s78,s45,256
s_cmp_lt_u32 s78,256
s_cbranch_scc1 Lr_ta0_%=
buffer_load_dword v18,v6,s[36:39],s61 offen offset:0 sc1
v_mfma_f32_16x16x32_bf16 v[148:151],v[164:167],v[180:183],0
s_add_u32 m0,s65,16384
M 148,v[168:171],184
D 9,56,0
M 148,v[172:175],188
D 9,56,1024
M 148,v[176:179],192
L 4
D 9,56,2048
v_mfma_f32_16x16x32_bf16 v[40:43],v[164:167],v[164:167],0
D 9,56,3072
M 40,v[168:171],168
L 0
v_lshlrev_b32 v84,16,v68
v_and_b32 v85,0xffff0000,v68
M 40,v[172:175],172
v_lshlrev_b32 v86,16,v69
v_and_b32 v87,0xffff0000,v69
M 40,v[176:179],176
v_lshlrev_b32 v88,16,v70
v_and_b32 v89,0xffff0000,v70
M 40,v[180:183],180
v_lshlrev_b32 v90,16,v71
v_and_b32 v91,0xffff0000,v71
M 40,v[184:187],184
v_lshlrev_b32 v92,16,v72
v_and_b32 v93,0xffff0000,v72
M 40,v[188:191],188
v_lshlrev_b32 v94,16,v73
v_and_b32 v95,0xffff0000,v73
M 40,v[192:195],192
v_lshlrev_b32 v96,16,v74
v_and_b32 v97,0xffff0000,v74
v_mfma_f32_16x16x32_bf16 v[36:39],v[32:35],v[164:167],0
v_lshlrev_b32 v98,16,v75
v_and_b32 v99,0xffff0000,v75
M 36,v[32:35],168
v_lshlrev_b32 v100,16,v76
v_and_b32 v101,0xffff0000,v76
M 36,v[32:35],172
v_lshlrev_b32 v102,16,v77
v_and_b32 v103,0xffff0000,v77
M 36,v[32:35],176
v_lshlrev_b32 v104,16,v78
v_and_b32 v105,0xffff0000,v78
M 36,v[32:35],180
v_fma_f32 v40,2.0,v148,v40
v_fma_f32 v41,2.0,v149,v41
M 36,v[32:35],184
v_fma_f32 v42,2.0,v150,v42
v_fma_f32 v43,2.0,v151,v43
M 36,v[32:35],188
v_cndmask_b32 v45,v40,v41,s[70:71]
v_cndmask_b32 v46,v42,v43,s[70:71]
M 36,v[32:35],192
v_cndmask_b32 v45,v45,v46,s[72:73]
v_lshlrev_b32 v106,16,v79
v_mfma_f32_16x16x32_bf16 v[116:119],v[26:29],v[164:167],0
v_and_b32 v107,0xffff0000,v79
v_lshlrev_b32 v108,16,v80
M 116,v[26:29],180
v_and_b32 v109,0xffff0000,v80
v_lshlrev_b32 v110,16,v81
v_mfma_f32_16x16x32_bf16 v[120:123],v[24:27],v[164:167],0
v_and_b32 v111,0xffff0000,v81
v_lshlrev_b32 v112,16,v82
M 120,v[24:27],180
v_and_b32 v113,0xffff0000,v82
v_lshlrev_b32 v114,16,v83
v_mfma_f32_16x16x32_bf16 v[124:127],v[26:29],v[168:171],0
v_add_f32_dpp v44,v36,v36 row_ror:8 row_mask:0xf bank_mask:0xf
v_and_b32 v115,0xffff0000,v83
M 124,v[26:29],184
ds_write_b32 v2,v44 offset:0
v_mfma_f32_16x16x32_bf16 v[128:131],v[24:27],v[168:171],0
ds_write_b32 v0,v45 offset:0
M 128,v[24:27],184
v_mfma_f32_16x16x32_bf16 v[132:135],v[26:29],v[172:175],0
M 132,v[26:29],188
L 0
v_mfma_f32_16x16x32_bf16 v[136:139],v[24:27],v[172:175],0
s_barrier
R 52,3,0
R 56,3,16
M 136,v[24:27],188
R 60,3,32
v_mfma_f32_16x16x32_bf16 v[140:143],v[26:29],v[176:179],0
M 140,v[26:29],192
v_mfma_f32_16x16x32_bf16 v[144:147],v[24:27],v[176:179],0
M 144,v[24:27],192
L 0
v_pk_add_f32 v[52:53],v[52:53],v[54:55]
v_pk_add_f32 v[56:57],v[56:57],v[58:59]
v_pk_add_f32 v[60:61],v[60:61],v[62:63]
v_add_f32 v52,v52,v53
v_pk_add_f32 v[56:57],v[56:57],v[60:61]
v_mul_f32 v52,s68,v52
v_add_f32 v53,v56,v57
v_mul_f32 v53,s68,v53
v_fma_f32 v53,-v52,v52,v53
v_max_f32 v53,0,v53
s_waitcnt vmcnt(5)
buffer_store_dword v17,v5,s[36:39],s63 offen offset:0 sc0 sc1
s_mov_b32 s63,0x7fff0000
s_waitcnt vmcnt(5)
v_cmp_eq_u32 vcc,s53,v18
s_nop 0
s_cmp_eq_u64 vcc,-1
s_cselect_b32 s78,s45,256
s_cmp_lt_u32 s78,256
s_cbranch_scc1 Lr_tb0_%=
buffer_load_dword v18,v6,s[36:39],s61 offen offset:0 sc1
v_add_f32 v53,s69,v53
v_rsq_f32 v48,v53
s_nop 0
v_mul_f32_e64 v50,-v52,v48
v_mov_b32 v49,v48
v_mov_b32 v51,v50
v_pk_fma_f32 v[52:53],v[116:117],v[48:49],v[50:51]
v_pk_fma_f32 v[54:55],v[118:119],v[48:49],v[50:51]
v_pk_fma_f32 v[56:57],v[120:121],v[48:49],v[50:51]
v_pk_fma_f32 v[58:59],v[122:123],v[48:49],v[50:51]
v_pk_fma_f32 v[116:117],v[52:53],v[84:85],v[116:117]
v_pk_fma_f32 v[118:119],v[54:55],v[86:87],v[118:119]
v_pk_fma_f32 v[120:121],v[56:57],v[88:89],v[120:121]
v_pk_fma_f32 v[122:123],v[58:59],v[90:91],v[122:123]
v_pk_fma_f32 v[52:53],v[124:125],v[48:49],v[50:51]
v_pk_fma_f32 v[54:55],v[126:127],v[48:49],v[50:51]
v_pk_fma_f32 v[56:57],v[128:129],v[48:49],v[50:51]
v_pk_fma_f32 v[58:59],v[130:131],v[48:49],v[50:51]
v_pk_fma_f32 v[124:125],v[52:53],v[92:93],v[124:125]
v_pk_fma_f32 v[126:127],v[54:55],v[94:95],v[126:127]
v_pk_fma_f32 v[128:129],v[56:57],v[96:97],v[128:129]
v_pk_fma_f32 v[130:131],v[58:59],v[98:99],v[130:131]
v_pk_fma_f32 v[52:53],v[132:133],v[48:49],v[50:51]
v_pk_fma_f32 v[54:55],v[134:135],v[48:49],v[50:51]
v_pk_fma_f32 v[56:57],v[136:137],v[48:49],v[50:51]
v_pk_fma_f32 v[58:59],v[138:139],v[48:49],v[50:51]
v_pk_fma_f32 v[132:133],v[52:53],v[100:101],v[132:133]
v_pk_fma_f32 v[134:135],v[54:55],v[102:103],v[134:135]
v_pk_fma_f32 v[136:137],v[56:57],v[104:105],v[136:137]
v_pk_fma_f32 v[138:139],v[58:59],v[106:107],v[138:139]
v_pk_fma_f32 v[52:53],v[140:141],v[48:49],v[50:51]
v_pk_fma_f32 v[54:55],v[142:143],v[48:49],v[50:51]
v_pk_fma_f32 v[56:57],v[144:145],v[48:49],v[50:51]
v_pk_fma_f32 v[58:59],v[146:147],v[48:49],v[50:51]
v_pk_fma_f32 v[140:141],v[52:53],v[108:109],v[140:141]
v_pk_fma_f32 v[142:143],v[54:55],v[110:111],v[142:143]
v_pk_fma_f32 v[144:145],v[56:57],v[112:113],v[144:145]
v_pk_fma_f32 v[146:147],v[58:59],v[114:115],v[146:147]
v_cvt_pk_bf16_f32 v148,v116,v117
v_cvt_pk_bf16_f32 v149,v118,v119
v_cvt_pk_bf16_f32 v150,v120,v121
v_cvt_pk_bf16_f32 v151,v122,v123
v_cvt_pk_bf16_f32 v152,v124,v125
v_cvt_pk_bf16_f32 v153,v126,v127
v_mfma_f32_16x16x32_bf16 v[36:39],v[32:35],v[148:151],0
v_cvt_pk_bf16_f32 v154,v128,v129
v_cvt_pk_bf16_f32 v155,v130,v131
v_mfma_f32_16x16x32_bf16 v[40:43],v[148:151],v[148:151],0
v_cvt_pk_bf16_f32 v156,v132,v133
v_cvt_pk_bf16_f32 v157,v134,v135
M 36,v[32:35],152
v_cvt_pk_bf16_f32 v158,v136,v137
v_cvt_pk_bf16_f32 v159,v138,v139
M 40,v[152:155],152
v_cvt_pk_bf16_f32 v160,v140,v141
v_cvt_pk_bf16_f32 v161,v142,v143
M 36,v[32:35],156
v_cvt_pk_bf16_f32 v162,v144,v145
v_cvt_pk_bf16_f32 v163,v146,v147
M 40,v[156:159],156
buffer_store_dwordx4 v[148:151],v1,s[36:39],s57 offen offset:0 sc0 sc1
M 36,v[32:35],160
buffer_store_dwordx4 v[152:155],v1,s[36:39],s57 offen offset:2048 sc0 sc1
M 40,v[160:163],160
buffer_store_dwordx4 v[156:159],v1,s[36:39],s58 offen offset:0 sc0 sc1
s_nop 4
v_add_f32_dpp v44,v36,v36 row_ror:8 row_mask:0xf bank_mask:0xf
s_nop 0
v_cndmask_b32 v45,v40,v41,s[70:71]
v_cndmask_b32 v46,v42,v43,s[70:71]
ds_write_b32 v2,v44 offset:1024
v_cndmask_b32 v45,v45,v46,s[72:73]
ds_write_b32 v0,v45 offset:1024
buffer_store_dwordx4 v[160:163],v1,s[36:39],s58 offen offset:2048 sc0 sc1
L 0
s_barrier
R 52,3,1024
R 56,3,1040
R 60,3,1056
L 0
v_pk_add_f32 v[52:53],v[52:53],v[54:55]
v_pk_add_f32 v[56:57],v[56:57],v[58:59]
v_pk_add_f32 v[60:61],v[60:61],v[62:63]
v_add_f32 v52,v52,v53
v_pk_add_f32 v[56:57],v[56:57],v[60:61]
v_mul_f32 v52,s68,v52
v_add_f32 v53,v56,v57
v_mul_f32 v53,s68,v53
v_fma_f32 v53,-v52,v52,v53
v_max_f32 v53,0,v53
v_add_f32 v54,s69,v53
v_fma_f32 v55,s69,v54,v53
v_rsq_f32 v65,v54
v_rsq_f32 v54,v55
v_mul_f32_e64 v64,-v52,v65
v_mul_f32 v63,v55,v54
v_mul_f32 v61,s76,v54
v_mul_f32 v55,v54,v54
v_mul_f32 v62,s77,v55
v_mul_f32 v62,v62,v61
v_cndmask_b32 v60,v52,v64,s[74:75]
v_cndmask_b32 v61,v61,v65,s[74:75]
buffer_store_dwordx4 v[60:63],v4,s[36:39],s59 offen offset:0 sc0 sc1
s_mov_b32 s63,s60
s_waitcnt vmcnt(5)
v_cmp_eq_u32 vcc,s53,v18
s_nop 0
s_cmp_eq_u64 vcc,-1
s_cselect_b32 s78,s45,256
s_cmp_lt_u32 s78,256
s_cbranch_scc1 Lr_tc0_%=
s_waitcnt vmcnt(0)
buffer_store_dword v17,v5,s[36:39],s63 offen offset:0 sc0 sc1
s_mov_b32 s63,0x7fff0000
s_cmp_ge_u32 s45,256
s_cbranch_scc1 Lr_done_%=
Lr_bp0_%=:
buffer_load_dword v18,v6,s[36:39],s61 offen offset:0 sc1
s_add_u32 s82,s82,1
s_waitcnt vmcnt(0)
v_cmp_eq_u32 vcc,s53,v18
s_nop 0
s_cmp_eq_u64 vcc,-1
s_cbranch_scc1 Lr_bq0_%=
s_sleep 2
s_cmp_lt_u32 s82,20000
s_cbranch_scc1 Lr_bp0_%=
Lr_bq0_%=:
s_add_u32 s78,s55,71303168
buffer_load_dwordx4 v[196:199],v1,s[36:39],s78 offen offset:0 sc1
buffer_load_dwordx4 v[200:203],v1,s[36:39],s78 offen offset:2048 sc1
s_add_u32 s78,s55,71307264
buffer_load_dwordx4 v[204:207],v1,s[36:39],s78 offen offset:0 sc1
buffer_load_dwordx4 v[208:211],v1,s[36:39],s78 offen offset:2048 sc1
s_add_u32 s78,s55,89128960
buffer_load_dwordx4 v[212:215],v1,s[36:39],s78 offen offset:0 sc1
buffer_load_dwordx4 v[216:219],v1,s[36:39],s78 offen offset:2048 sc1
s_add_u32 s78,s55,89133056
buffer_load_dwordx4 v[220:223],v1,s[36:39],s78 offen offset:0 sc1
buffer_load_dwordx4 v[224:227],v1,s[36:39],s78 offen offset:2048 sc1
buffer_load_dword v18,v6,s[36:39],s62 offen offset:0 sc1
s_lshr_b32 s78,s45,1
s_and_b32 s79,s45,1
s_lshl_b32 s79,s79,7
s_lshl_b32 s57,s78,15
s_add_u32 s57,s57,s79
s_add_u32 s57,s57,12582912
s_add_u32 s58,s57,4096
s_lshl_b32 s59,s78,9
s_add_u32 s59,s59,s79
s_add_u32 s59,s59,107020288
s_lshl_b32 s60,s45,9
s_add_u32 s60,s60,s66
s_add_u32 s60,s60,131072
s_add_u32 s45,s45,s46
s_lshr_b32 s78,s45,1
s_and_b32 s79,s45,1
s_lshl_b32 s79,s79,7
s_lshl_b32 s55,s78,15
s_add_u32 s55,s55,s79
s_mul_i32 s56,s45,16384
s_add_u32 s56,s56,98566144
s_lshl_b32 s61,s78,11
s_add_u32 s61,s61,110587904
s_add_u32 s78,s45,s46
s_lshr_b32 s78,s78,1
s_lshl_b32 s62,s78,11
s_add_u32 s62,s62,110587904
s_waitcnt vmcnt(0)
s_branch Lr_it1_%=
Lr_ta0_%=:
v_mfma_f32_16x16x32_bf16 v[148:151],v[164:167],v[180:183],0
s_add_u32 s78,s55,71303168
s_add_u32 m0,s65,16384
M 148,v[168:171],184
buffer_load_dwordx4 v[196:199],v1,s[36:39],s78 offen offset:0 sc1
M 148,v[172:175],188
buffer_load_dwordx4 v[200:203],v1,s[36:39],s78 offen offset:2048 sc1
M 148,v[176:179],192
L 4
D 9,56,0
v_mfma_f32_16x16x32_bf16 v[40:43],v[164:167],v[164:167],0
D 9,56,1024
M 40,v[168:171],168
L 0
D 9,56,2048
M 40,v[172:175],172
D 9,56,3072
M 40,v[176:179],176
s_add_u32 s78,s55,71307264
v_lshlrev_b32 v84,16,v68
v_and_b32 v85,0xffff0000,v68
M 40,v[180:183],180
buffer_load_dwordx4 v[204:207],v1,s[36:39],s78 offen offset:0 sc1
M 40,v[184:187],184
buffer_load_dwordx4 v[208:211],v1,s[36:39],s78 offen offset:2048 sc1
M 40,v[188:191],188
s_add_u32 s78,s55,89128960
v_lshlrev_b32 v86,16,v69
v_and_b32 v87,0xffff0000,v69
M 40,v[192:195],192
buffer_load_dwordx4 v[212:215],v1,s[36:39],s78 offen offset:0 sc1
v_mfma_f32_16x16x32_bf16 v[36:39],v[32:35],v[164:167],0
buffer_load_dwordx4 v[216:219],v1,s[36:39],s78 offen offset:2048 sc1
M 36,v[32:35],168
s_add_u32 s78,s55,89133056
v_lshlrev_b32 v88,16,v70
v_and_b32 v89,0xffff0000,v70
M 36,v[32:35],172
buffer_load_dwordx4 v[220:223],v1,s[36:39],s78 offen offset:0 sc1
M 36,v[32:35],176
buffer_load_dwordx4 v[224:227],v1,s[36:39],s78 offen offset:2048 sc1
M 36,v[32:35],180
v_fma_f32 v40,2.0,v148,v40
v_fma_f32 v41,2.0,v149,v41
M 36,v[32:35],184
v_fma_f32 v42,2.0,v150,v42
v_fma_f32 v43,2.0,v151,v43
M 36,v[32:35],188
v_cndmask_b32 v45,v40,v41,s[70:71]
v_cndmask_b32 v46,v42,v43,s[70:71]
M 36,v[32:35],192
v_cndmask_b32 v45,v45,v46,s[72:73]
v_lshlrev_b32 v90,16,v71
v_mfma_f32_16x16x32_bf16 v[116:119],v[26:29],v[164:167],0
v_and_b32 v91,0xffff0000,v71
v_lshlrev_b32 v92,16,v72
M 116,v[26:29],180
v_and_b32 v93,0xffff0000,v72
v_lshlrev_b32 v94,16,v73
v_mfma_f32_16x16x32_bf16 v[120:123],v[24:27],v[164:167],0
v_and_b32 v95,0xffff0000,v73
v_lshlrev_b32 v96,16,v74
M 120,v[24:27],180
v_and_b32 v97,0xffff0000,v74
v_lshlrev_b32 v98,16,v75
v_mfma_f32_16x16x32_bf16 v[124:127],v[26:29],v[168:171],0
v_add_f32_dpp v44,v36,v36 row_ror:8 row_mask:0xf bank_mask:0xf
v_and_b32 v99,0xffff0000,v75
M 124,v[26:29],184
ds_write_b32 v2,v44 offset:0
v_mfma_f32_16x16x32_bf16 v[128:131],v[24:27],v[168:171],0
ds_write_b32 v0,v45 offset:0
M 128,v[24:27],184
v_lshlrev_b32 v100,16,v76
v_and_b32 v101,0xffff0000,v76
v_mfma_f32_16x16x32_bf16 v[132:135],v[26:29],v[172:175],0
v_lshlrev_b32 v102,16,v77
v_and_b32 v103,0xffff0000,v77
M 132,v[26:29],188
L 0
v_lshlrev_b32 v104,16,v78
)"
R"(v_and_b32 v105,0xffff0000,v78
v_mfma_f32_16x16x32_bf16 v[136:139],v[24:27],v[172:175],0
s_barrier
R 52,3,0
R 56,3,16
M 136,v[24:27],188
R 60,3,32
v_lshlrev_b32 v106,16,v79
v_mfma_f32_16x16x32_bf16 v[140:143],v[26:29],v[176:179],0
v_and_b32 v107,0xffff0000,v79
v_lshlrev_b32 v108,16,v80
M 140,v[26:29],192
v_and_b32 v109,0xffff0000,v80
v_lshlrev_b32 v110,16,v81
v_mfma_f32_16x16x32_bf16 v[144:147],v[24:27],v[176:179],0
v_and_b32 v111,0xffff0000,v81
v_lshlrev_b32 v112,16,v82
M 144,v[24:27],192
v_and_b32 v113,0xffff0000,v82
v_lshlrev_b32 v114,16,v83
v_and_b32 v115,0xffff0000,v83
L 0
v_pk_add_f32 v[52:53],v[52:53],v[54:55]
v_pk_add_f32 v[56:57],v[56:57],v[58:59]
v_pk_add_f32 v[60:61],v[60:61],v[62:63]
v_add_f32 v52,v52,v53
v_pk_add_f32 v[56:57],v[56:57],v[60:61]
v_mul_f32 v52,s68,v52
v_add_f32 v53,v56,v57
v_mul_f32 v53,s68,v53
v_fma_f32 v53,-v52,v52,v53
v_max_f32 v53,0,v53
buffer_load_dword v18,v6,s[36:39],s62 offen offset:0 sc1
v_add_f32 v53,s69,v53
v_rsq_f32 v48,v53
s_nop 0
v_mul_f32_e64 v50,-v52,v48
v_mov_b32 v49,v48
v_mov_b32 v51,v50
v_pk_fma_f32 v[52:53],v[116:117],v[48:49],v[50:51]
v_pk_fma_f32 v[54:55],v[118:119],v[48:49],v[50:51]
v_pk_fma_f32 v[56:57],v[120:121],v[48:49],v[50:51]
v_pk_fma_f32 v[58:59],v[122:123],v[48:49],v[50:51]
v_pk_fma_f32 v[116:117],v[52:53],v[84:85],v[116:117]
v_pk_fma_f32 v[118:119],v[54:55],v[86:87],v[118:119]
v_pk_fma_f32 v[120:121],v[56:57],v[88:89],v[120:121]
v_pk_fma_f32 v[122:123],v[58:59],v[90:91],v[122:123]
v_pk_fma_f32 v[52:53],v[124:125],v[48:49],v[50:51]
v_pk_fma_f32 v[54:55],v[126:127],v[48:49],v[50:51]
v_pk_fma_f32 v[56:57],v[128:129],v[48:49],v[50:51]
v_pk_fma_f32 v[58:59],v[130:131],v[48:49],v[50:51]
v_pk_fma_f32 v[124:125],v[52:53],v[92:93],v[124:125]
v_pk_fma_f32 v[126:127],v[54:55],v[94:95],v[126:127]
v_pk_fma_f32 v[128:129],v[56:57],v[96:97],v[128:129]
v_pk_fma_f32 v[130:131],v[58:59],v[98:99],v[130:131]
v_pk_fma_f32 v[52:53],v[132:133],v[48:49],v[50:51]
v_pk_fma_f32 v[54:55],v[134:135],v[48:49],v[50:51]
v_pk_fma_f32 v[56:57],v[136:137],v[48:49],v[50:51]
v_pk_fma_f32 v[58:59],v[138:139],v[48:49],v[50:51]
v_pk_fma_f32 v[132:133],v[52:53],v[100:101],v[132:133]
v_pk_fma_f32 v[134:135],v[54:55],v[102:103],v[134:135]
v_pk_fma_f32 v[136:137],v[56:57],v[104:105],v[136:137]
v_pk_fma_f32 v[138:139],v[58:59],v[106:107],v[138:139]
v_pk_fma_f32 v[52:53],v[140:141],v[48:49],v[50:51]
v_pk_fma_f32 v[54:55],v[142:143],v[48:49],v[50:51]
v_pk_fma_f32 v[56:57],v[144:145],v[48:49],v[50:51]
v_pk_fma_f32 v[58:59],v[146:147],v[48:49],v[50:51]
v_pk_fma_f32 v[140:141],v[52:53],v[108:109],v[140:141]
v_pk_fma_f32 v[142:143],v[54:55],v[110:111],v[142:143]
v_pk_fma_f32 v[144:145],v[56:57],v[112:113],v[144:145]
v_pk_fma_f32 v[146:147],v[58:59],v[114:115],v[146:147]
v_cvt_pk_bf16_f32 v148,v116,v117
v_cvt_pk_bf16_f32 v149,v118,v119
v_cvt_pk_bf16_f32 v150,v120,v121
v_cvt_pk_bf16_f32 v151,v122,v123
v_cvt_pk_bf16_f32 v152,v124,v125
v_cvt_pk_bf16_f32 v153,v126,v127
s_waitcnt vmcnt(13)
buffer_store_dword v17,v5,s[36:39],s63 offen offset:0 sc0 sc1
s_mov_b32 s63,0x7fff0000
v_mfma_f32_16x16x32_bf16 v[36:39],v[32:35],v[148:151],0
v_cvt_pk_bf16_f32 v154,v128,v129
v_cvt_pk_bf16_f32 v155,v130,v131
v_mfma_f32_16x16x32_bf16 v[40:43],v[148:151],v[148:151],0
v_cvt_pk_bf16_f32 v156,v132,v133
v_cvt_pk_bf16_f32 v157,v134,v135
M 36,v[32:35],152
v_cvt_pk_bf16_f32 v158,v136,v137
v_cvt_pk_bf16_f32 v159,v138,v139
M 40,v[152:155],152
v_cvt_pk_bf16_f32 v160,v140,v141
v_cvt_pk_bf16_f32 v161,v142,v143
M 36,v[32:35],156
v_cvt_pk_bf16_f32 v162,v144,v145
v_cvt_pk_bf16_f32 v163,v146,v147
M 40,v[156:159],156
buffer_store_dwordx4 v[148:151],v1,s[36:39],s57 offen offset:0 sc0 sc1
M 36,v[32:35],160
buffer_store_dwordx4 v[152:155],v1,s[36:39],s57 offen offset:2048 sc0 sc1
M 40,v[160:163],160
buffer_store_dwordx4 v[156:159],v1,s[36:39],s58 offen offset:0 sc0 sc1
s_nop 4
v_add_f32_dpp v44,v36,v36 row_ror:8 row_mask:0xf bank_mask:0xf
s_nop 0
v_cndmask_b32 v45,v40,v41,s[70:71]
v_cndmask_b32 v46,v42,v43,s[70:71]
ds_write_b32 v2,v44 offset:1024
v_cndmask_b32 v45,v45,v46,s[72:73]
ds_write_b32 v0,v45 offset:1024
buffer_store_dwordx4 v[160:163],v1,s[36:39],s58 offen offset:2048 sc0 sc1
L 0
s_barrier
R 52,3,1024
R 56,3,1040
R 60,3,1056
L 0
v_pk_add_f32 v[52:53],v[52:53],v[54:55]
v_pk_add_f32 v[56:57],v[56:57],v[58:59]
v_pk_add_f32 v[60:61],v[60:61],v[62:63]
v_add_f32 v52,v52,v53
v_pk_add_f32 v[56:57],v[56:57],v[60:61]
v_mul_f32 v52,s68,v52
v_add_f32 v53,v56,v57
v_mul_f32 v53,s68,v53
v_fma_f32 v53,-v52,v52,v53
v_max_f32 v53,0,v53
v_add_f32 v54,s69,v53
v_fma_f32 v55,s69,v54,v53
v_rsq_f32 v65,v54
v_rsq_f32 v54,v55
v_mul_f32_e64 v64,-v52,v65
v_mul_f32 v63,v55,v54
v_mul_f32 v61,s76,v54
v_mul_f32 v55,v54,v54
v_mul_f32 v62,s77,v55
v_mul_f32 v62,v62,v61
v_cndmask_b32 v60,v52,v64,s[74:75]
v_cndmask_b32 v61,v61,v65,s[74:75]
buffer_store_dwordx4 v[60:63],v4,s[36:39],s59 offen offset:0 sc0 sc1
s_mov_b32 s63,s60
s_lshr_b32 s78,s45,1
s_and_b32 s79,s45,1
s_lshl_b32 s79,s79,7
s_lshl_b32 s57,s78,15
s_add_u32 s57,s57,s79
s_add_u32 s57,s57,12582912
s_add_u32 s58,s57,4096
s_lshl_b32 s59,s78,9
s_add_u32 s59,s59,s79
s_add_u32 s59,s59,107020288
s_lshl_b32 s60,s45,9
s_add_u32 s60,s60,s66
s_add_u32 s60,s60,131072
s_add_u32 s45,s45,s46
s_lshr_b32 s78,s45,1
s_and_b32 s79,s45,1
s_lshl_b32 s79,s79,7
s_lshl_b32 s55,s78,15
s_add_u32 s55,s55,s79
s_mul_i32 s56,s45,16384
s_add_u32 s56,s56,98566144
s_lshl_b32 s61,s78,11
s_add_u32 s61,s61,110587904
s_add_u32 s78,s45,s46
s_lshr_b32 s78,s78,1
s_lshl_b32 s62,s78,11
s_add_u32 s62,s62,110587904
s_waitcnt vmcnt(6)
s_branch Lr_it1_%=
Lr_tb0_%=:
v_add_f32 v53,s69,v53
v_rsq_f32 v48,v53
s_nop 0
v_mul_f32_e64 v50,-v52,v48
v_mov_b32 v49,v48
v_mov_b32 v51,v50
v_pk_fma_f32 v[52:53],v[116:117],v[48:49],v[50:51]
v_pk_fma_f32 v[54:55],v[118:119],v[48:49],v[50:51]
v_pk_fma_f32 v[56:57],v[120:121],v[48:49],v[50:51]
v_pk_fma_f32 v[58:59],v[122:123],v[48:49],v[50:51]
s_add_u32 s78,s55,71303168
buffer_load_dwordx4 v[196:199],v1,s[36:39],s78 offen offset:0 sc1
buffer_load_dwordx4 v[200:203],v1,s[36:39],s78 offen offset:2048 sc1
s_add_u32 s78,s55,71307264
buffer_load_dwordx4 v[204:207],v1,s[36:39],s78 offen offset:0 sc1
buffer_load_dwordx4 v[208:211],v1,s[36:39],s78 offen offset:2048 sc1
v_pk_fma_f32 v[116:117],v[52:53],v[84:85],v[116:117]
v_pk_fma_f32 v[118:119],v[54:55],v[86:87],v[118:119]
v_pk_fma_f32 v[120:121],v[56:57],v[88:89],v[120:121]
v_pk_fma_f32 v[122:123],v[58:59],v[90:91],v[122:123]
v_pk_fma_f32 v[52:53],v[124:125],v[48:49],v[50:51]
v_pk_fma_f32 v[54:55],v[126:127],v[48:49],v[50:51]
v_pk_fma_f32 v[56:57],v[128:129],v[48:49],v[50:51]
v_pk_fma_f32 v[58:59],v[130:131],v[48:49],v[50:51]
s_add_u32 s78,s55,89128960
buffer_load_dwordx4 v[212:215],v1,s[36:39],s78 offen offset:0 sc1
buffer_load_dwordx4 v[216:219],v1,s[36:39],s78 offen offset:2048 sc1
s_add_u32 s78,s55,89133056
buffer_load_dwordx4 v[220:223],v1,s[36:39],s78 offen offset:0 sc1
buffer_load_dwordx4 v[224:227],v1,s[36:39],s78 offen offset:2048 sc1
v_pk_fma_f32 v[124:125],v[52:53],v[92:93],v[124:125]
v_pk_fma_f32 v[126:127],v[54:55],v[94:95],v[126:127]
v_pk_fma_f32 v[128:129],v[56:57],v[96:97],v[128:129]
v_pk_fma_f32 v[130:131],v[58:59],v[98:99],v[130:131]
v_pk_fma_f32 v[52:53],v[132:133],v[48:49],v[50:51]
v_pk_fma_f32 v[54:55],v[134:135],v[48:49],v[50:51]
v_pk_fma_f32 v[56:57],v[136:137],v[48:49],v[50:51]
v_pk_fma_f32 v[58:59],v[138:139],v[48:49],v[50:51]
v_pk_fma_f32 v[132:133],v[52:53],v[100:101],v[132:133]
v_pk_fma_f32 v[134:135],v[54:55],v[102:103],v[134:135]
v_pk_fma_f32 v[136:137],v[56:57],v[104:105],v[136:137]
v_pk_fma_f32 v[138:139],v[58:59],v[106:107],v[138:139]
v_pk_fma_f32 v[52:53],v[140:141],v[48:49],v[50:51]
v_pk_fma_f32 v[54:55],v[142:143],v[48:49],v[50:51]
v_pk_fma_f32 v[56:57],v[144:145],v[48:49],v[50:51]
v_pk_fma_f32 v[58:59],v[146:147],v[48:49],v[50:51]
v_pk_fma_f32 v[140:141],v[52:53],v[108:109],v[140:141]
v_pk_fma_f32 v[142:143],v[54:55],v[110:111],v[142:143]
v_pk_fma_f32 v[144:145],v[56:57],v[112:113],v[144:145]
v_pk_fma_f32 v[146:147],v[58:59],v[114:115],v[146:147]
buffer_load_dword v18,v6,s[36:39],s62 offen offset:0 sc1
v_cvt_pk_bf16_f32 v148,v116,v117
v_cvt_pk_bf16_f32 v149,v118,v119
v_cvt_pk_bf16_f32 v150,v120,v121
v_cvt_pk_bf16_f32 v151,v122,v123
v_cvt_pk_bf16_f32 v152,v124,v125
v_cvt_pk_bf16_f32 v153,v126,v127
v_mfma_f32_16x16x32_bf16 v[36:39],v[32:35],v[148:151],0
v_cvt_pk_bf16_f32 v154,v128,v129
v_cvt_pk_bf16_f32 v155,v130,v131
v_mfma_f32_16x16x32_bf16 v[40:43],v[148:151],v[148:151],0
v_cvt_pk_bf16_f32 v156,v132,v133
v_cvt_pk_bf16_f32 v157,v134,v135
M 36,v[32:35],152
v_cvt_pk_bf16_f32 v158,v136,v137
v_cvt_pk_bf16_f32 v159,v138,v139
M 40,v[152:155],152
v_cvt_pk_bf16_f32 v160,v140,v141
v_cvt_pk_bf16_f32 v161,v142,v143
M 36,v[32:35],156
v_cvt_pk_bf16_f32 v162,v144,v145
v_cvt_pk_bf16_f32 v163,v146,v147
M 40,v[156:159],156
buffer_store_dwordx4 v[148:151],v1,s[36:39],s57 offen offset:0 sc0 sc1
M 36,v[32:35],160
buffer_store_dwordx4 v[152:155],v1,s[36:39],s57 offen offset:2048 sc0 sc1
M 40,v[160:163],160
buffer_store_dwordx4 v[156:159],v1,s[36:39],s58 offen offset:0 sc0 sc1
s_nop 4
v_add_f32_dpp v44,v36,v36 row_ror:8 row_mask:0xf bank_mask:0xf
s_nop 0
v_cndmask_b32 v45,v40,v41,s[70:71]
v_cndmask_b32 v46,v42,v43,s[70:71]
ds_write_b32 v2,v44 offset:1024
v_cndmask_b32 v45,v45,v46,s[72:73]
ds_write_b32 v0,v45 offset:1024
buffer_store_dwordx4 v[160:163],v1,s[36:39],s58 offen offset:2048 sc0 sc1
L 0
s_barrier
R 52,3,1024
R 56,3,1040
R 60,3,1056
L 0
v_pk_add_f32 v[52:53],v[52:53],v[54:55]
v_pk_add_f32 v[56:57],v[56:57],v[58:59]
v_pk_add_f32 v[60:61],v[60:61],v[62:63]
v_add_f32 v52,v52,v53
v_pk_add_f32 v[56:57],v[56:57],v[60:61]
v_mul_f32 v52,s68,v52
v_add_f32 v53,v56,v57
v_mul_f32 v53,s68,v53
v_fma_f32 v53,-v52,v52,v53
v_max_f32 v53,0,v53
v_add_f32 v54,s69,v53
v_fma_f32 v55,s69,v54,v53
v_rsq_f32 v65,v54
v_rsq_f32 v54,v55
v_mul_f32_e64 v64,-v52,v65
v_mul_f32 v63,v55,v54
v_mul_f32 v61,s76,v54
v_mul_f32 v55,v54,v54
v_mul_f32 v62,s77,v55
v_mul_f32 v62,v62,v61
v_cndmask_b32 v60,v52,v64,s[74:75]
v_cndmask_b32 v61,v61,v65,s[74:75]
buffer_store_dwordx4 v[60:63],v4,s[36:39],s59 offen offset:0 sc0 sc1
s_mov_b32 s63,s60
s_lshr_b32 s78,s45,1
s_and_b32 s79,s45,1
s_lshl_b32 s79,s79,7
s_lshl_b32 s57,s78,15
s_add_u32 s57,s57,s79
s_add_u32 s57,s57,12582912
s_add_u32 s58,s57,4096
s_lshl_b32 s59,s78,9
s_add_u32 s59,s59,s79
s_add_u32 s59,s59,107020288
s_lshl_b32 s60,s45,9
s_add_u32 s60,s60,s66
s_add_u32 s60,s60,131072
s_add_u32 s45,s45,s46
s_lshr_b32 s78,s45,1
s_and_b32 s79,s45,1
s_lshl_b32 s79,s79,7
s_lshl_b32 s55,s78,15
s_add_u32 s55,s55,s79
s_mul_i32 s56,s45,16384
s_add_u32 s56,s56,98566144
s_lshl_b32 s61,s78,11
s_add_u32 s61,s61,110587904
s_add_u32 s78,s45,s46
s_lshr_b32 s78,s78,1
s_lshl_b32 s62,s78,11
s_add_u32 s62,s62,110587904
s_waitcnt vmcnt(5)
s_branch Lr_it1_%=
Lr_tc0_%=:
s_add_u32 s78,s55,71303168
buffer_load_dwordx4 v[196:199],v1,s[36:39],s78 offen offset:0 sc1
buffer_load_dwordx4 v[200:203],v1,s[36:39],s78 offen offset:2048 sc1
s_add_u32 s78,s55,71307264
buffer_load_dwordx4 v[204:207],v1,s[36:39],s78 offen offset:0 sc1
buffer_load_dwordx4 v[208:211],v1,s[36:39],s78 offen offset:2048 sc1
s_add_u32 s78,s55,89128960
buffer_load_dwordx4 v[212:215],v1,s[36:39],s78 offen offset:0 sc1
buffer_load_dwordx4 v[216:219],v1,s[36:39],s78 offen offset:2048 sc1
s_add_u32 s78,s55,89133056
buffer_load_dwordx4 v[220:223],v1,s[36:39],s78 offen offset:0 sc1
buffer_load_dwordx4 v[224:227],v1,s[36:39],s78 offen offset:2048 sc1
buffer_load_dword v18,v6,s[36:39],s62 offen offset:0 sc1
s_waitcnt vmcnt(9)
buffer_store_dword v17,v5,s[36:39],s63 offen offset:0 sc0 sc1
s_mov_b32 s63,0x7fff0000
s_lshr_b32 s78,s45,1
s_and_b32 s79,s45,1
s_lshl_b32 s79,s79,7
s_lshl_b32 s57,s78,15
s_add_u32 s57,s57,s79
s_add_u32 s57,s57,12582912
s_add_u32 s58,s57,4096
s_lshl_b32 s59,s78,9
s_add_u32 s59,s59,s79
s_add_u32 s59,s59,107020288
s_lshl_b32 s60,s45,9
s_add_u32 s60,s60,s66
s_add_u32 s60,s60,131072
s_add_u32 s45,s45,s46
s_lshr_b32 s78,s45,1
s_and_b32 s79,s45,1
s_lshl_b32 s79,s79,7
s_lshl_b32 s55,s78,15
s_add_u32 s55,s55,s79
s_mul_i32 s56,s45,16384
s_add_u32 s56,s56,98566144
s_lshl_b32 s61,s78,11
s_add_u32 s61,s61,110587904
s_add_u32 s78,s45,s46
s_lshr_b32 s78,s78,1
s_lshl_b32 s62,s78,11
s_add_u32 s62,s62,110587904
s_waitcnt vmcnt(1)
s_branch Lr_it1_%=
Lr_it1_%=:
T v[68:69],10,16384
T v[70:71],10,16448
T v[72:73],10,17408
T v[74:75],10,17472
T v[76:77],10,18432
T v[78:79],10,18496
T v[80:81],10,19456
T v[82:83],10,19520
s_nop 0
v_cmp_eq_u32 vcc,s53,v18
s_nop 0
s_cmp_eq_u64 vcc,-1
s_cselect_b32 s78,s45,256
s_cmp_lt_u32 s78,256
s_cbranch_scc1 Lr_ta1_%=
buffer_load_dword v18,v6,s[36:39],s61 offen offset:0 sc1
v_mfma_f32_16x16x32_bf16 v[148:151],v[196:199],v[212:215],0
s_add_u32 m0,s65,0
M 148,v[200:203],216
D 9,56,0
M 148,v[204:207],220
D 9,56,1024
M 148,v[208:211],224
L 4
D 9,56,2048
v_mfma_f32_16x16x32_bf16 v[40:43],v[196:199],v[196:199],0
D 9,56,3072
M 40,v[200:203],200
L 0
v_lshlrev_b32 v84,16,v68
v_and_b32 v85,0xffff0000,v68
M 40,v[204:207],204
v_lshlrev_b32 v86,16,v69
v_and_b32 v87,0xffff0000,v69
M 40,v[208:211],208
v_lshlrev_b32 v88,16,v70
v_and_b32 v89,0xffff0000,v70
M 40,v[212:215],212
v_lshlrev_b32 v90,16,v71
v_and_b32 v91,0xffff0000,v71
M 40,v[216:219],216
v_lshlrev_b32 v92,16,v72
v_and_b32 v93,0xffff0000,v72
M 40,v[220:223],220
v_lshlrev_b32 v94,16,v73
v_and_b32 v95,0xffff0000,v73
M 40,v[224:227],224
v_lshlrev_b32 v96,16,v74
v_and_b32 v97,0xffff0000,v74
v_mfma_f32_16x16x32_bf16 v[36:39],v[32:35],v[196:199],0
v_lshlrev_b32 v98,16,v75
v_and_b32 v99,0xffff0000,v75
M 36,v[32:35],200
v_lshlrev_b32 v100,16,v76
v_and_b32 v101,0xffff0000,v76
M 36,v[32:35],204
v_lshlrev_b32 v102,16,v77
v_and_b32 v103,0xffff0000,v77
M 36,v[32:35],208
v_lshlrev_b32 v104,16,v78
v_and_b32 v105,0xffff0000,v78
M 36,v[32:35],212
v_fma_f32 v40,2.0,v148,v40
v_fma_f32 v41,2.0,v149,v41
M 36,v[32:35],216
v_fma_f32 v42,2.0,v150,v42
v_fma_f32 v43,2.0,v151,v43
M 36,v[32:35],220
v_cndmask_b32 v45,v40,v41,s[70:71]
v_cndmask_b32 v46,v42,v43,s[70:71]
M 36,v[32:35],224
v_cndmask_b32 v45,v45,v46,s[72:73]
v_lshlrev_b32 v106,16,v79
v_mfma_f32_16x16x32_bf16 v[116:119],v[26:29],v[196:199],0
v_and_b32 v107,0xffff0000,v79
v_lshlrev_b32 v108,16,v80
M 116,v[26:29],212
v_and_b32 v109,0xffff0000,v80
v_lshlrev_b32 v110,16,v81
v_mfma_f32_16x16x32_bf16 v[120:123],v[24:27],v[196:199],0
v_and_b32 v111,0xffff0000,v81
v_lshlrev_b32 v112,16,v82
M 120,v[24:27],212
v_and_b32 v113,0xffff0000,v82
v_lshlrev_b32 v114,16,v83
v_mfma_f32_16x16x32_bf16 v[124:127],v[26:29],v[200:203],0
v_add_f32_dpp v44,v36,v36 row_ror:8 row_mask:0xf bank_mask:0xf
v_and_b32 v115,0xffff0000,v83
M 124,v[26:29],216
ds_write_b32 v2,v44 offset:0
v_mfma_f32_16x16x32_bf16 v[128:131],v[24:27],v[200:203],0
ds_write_b32 v0,v45 offset:0
M 128,v[24:27],216
v_mfma_f32_16x16x32_bf16 v[132:135],v[26:29],v[204:207],0
M 132,v[26:29],220
L 0
v_mfma_f32_16x16x32_bf16 v[136:139],v[24:27],v[204:207],0
s_barrier
R 52,3,0
R 56,3,16
M 136,v[24:27],220
R 60,3,32
v_mfma_f32_16x16x32_bf16 v[140:143],v[26:29],v[208:211],0
M 140,v[26:29],224
v_mfma_f32_16x16x32_bf16 v[144:147],v[24:27],v[208:211],0
M 144,v[24:27],224
L 0
v_pk_add_f32 v[52:53],v[52:53],v[54:55]
v_pk_add_f32 v[56:57],v[56:57],v[58:59]
v_pk_add_f32 v[60:61],v[60:61],v[62:63]
v_add_f32 v52,v52,v53
v_pk_add_f32 v[56:57],v[56:57],v[60:61]
v_mul_f32 v52,s68,v52
v_add_f32 v53,v56,v57
v_mul_f32 v53,s68,v53
v_fma_f32 v53,-v52,v52,v53
v_max_f32 v53,0,v53
s_waitcnt vmcnt(5)
buffer_store_dword v17,v5,s[36:39],s63 offen offset:0 sc0 sc1
s_mov_b32 s63,0x7fff0000
s_waitcnt vmcnt(5)
v_cmp_eq_u32 vcc,s53,v18
s_nop 0
s_cmp_eq_u64 vcc,-1
s_cselect_b32 s78,s45,256
s_cmp_lt_u32 s78,256
s_cbranch_scc1 Lr_tb1_%=
buffer_load_dword v18,v6,s[36:39],s61 offen offset:0 sc1
v_add_f32 v53,s69,v53
v_rsq_f32 v48,v53
s_nop 0
v_mul_f32_e64 v50,-v52,v48
v_mov_b32 v49,v48
v_mov_b32 v51,v50
v_pk_fma_f32 v[52:53],v[116:117],v[48:49],v[50:51]
v_pk_fma_f32 v[54:55],v[118:119],v[48:49],v[50:51]
v_pk_fma_f32 v[56:57],v[120:121],v[48:49],v[50:51]
)"
R"(v_pk_fma_f32 v[58:59],v[122:123],v[48:49],v[50:51]
v_pk_fma_f32 v[116:117],v[52:53],v[84:85],v[116:117]
v_pk_fma_f32 v[118:119],v[54:55],v[86:87],v[118:119]
v_pk_fma_f32 v[120:121],v[56:57],v[88:89],v[120:121]
v_pk_fma_f32 v[122:123],v[58:59],v[90:91],v[122:123]
v_pk_fma_f32 v[52:53],v[124:125],v[48:49],v[50:51]
v_pk_fma_f32 v[54:55],v[126:127],v[48:49],v[50:51]
v_pk_fma_f32 v[56:57],v[128:129],v[48:49],v[50:51]
v_pk_fma_f32 v[58:59],v[130:131],v[48:49],v[50:51]
v_pk_fma_f32 v[124:125],v[52:53],v[92:93],v[124:125]
v_pk_fma_f32 v[126:127],v[54:55],v[94:95],v[126:127]
v_pk_fma_f32 v[128:129],v[56:57],v[96:97],v[128:129]
v_pk_fma_f32 v[130:131],v[58:59],v[98:99],v[130:131]
v_pk_fma_f32 v[52:53],v[132:133],v[48:49],v[50:51]
v_pk_fma_f32 v[54:55],v[134:135],v[48:49],v[50:51]
v_pk_fma_f32 v[56:57],v[136:137],v[48:49],v[50:51]
v_pk_fma_f32 v[58:59],v[138:139],v[48:49],v[50:51]
v_pk_fma_f32 v[132:133],v[52:53],v[100:101],v[132:133]
v_pk_fma_f32 v[134:135],v[54:55],v[102:103],v[134:135]
v_pk_fma_f32 v[136:137],v[56:57],v[104:105],v[136:137]
v_pk_fma_f32 v[138:139],v[58:59],v[106:107],v[138:139]
v_pk_fma_f32 v[52:53],v[140:141],v[48:49],v[50:51]
v_pk_fma_f32 v[54:55],v[142:143],v[48:49],v[50:51]
v_pk_fma_f32 v[56:57],v[144:145],v[48:49],v[50:51]
v_pk_fma_f32 v[58:59],v[146:147],v[48:49],v[50:51]
v_pk_fma_f32 v[140:141],v[52:53],v[108:109],v[140:141]
v_pk_fma_f32 v[142:143],v[54:55],v[110:111],v[142:143]
v_pk_fma_f32 v[144:145],v[56:57],v[112:113],v[144:145]
v_pk_fma_f32 v[146:147],v[58:59],v[114:115],v[146:147]
v_cvt_pk_bf16_f32 v148,v116,v117
v_cvt_pk_bf16_f32 v149,v118,v119
v_cvt_pk_bf16_f32 v150,v120,v121
v_cvt_pk_bf16_f32 v151,v122,v123
v_cvt_pk_bf16_f32 v152,v124,v125
v_cvt_pk_bf16_f32 v153,v126,v127
v_mfma_f32_16x16x32_bf16 v[36:39],v[32:35],v[148:151],0
v_cvt_pk_bf16_f32 v154,v128,v129
v_cvt_pk_bf16_f32 v155,v130,v131
v_mfma_f32_16x16x32_bf16 v[40:43],v[148:151],v[148:151],0
v_cvt_pk_bf16_f32 v156,v132,v133
v_cvt_pk_bf16_f32 v157,v134,v135
M 36,v[32:35],152
v_cvt_pk_bf16_f32 v158,v136,v137
v_cvt_pk_bf16_f32 v159,v138,v139
M 40,v[152:155],152
v_cvt_pk_bf16_f32 v160,v140,v141
v_cvt_pk_bf16_f32 v161,v142,v143
M 36,v[32:35],156
v_cvt_pk_bf16_f32 v162,v144,v145
v_cvt_pk_bf16_f32 v163,v146,v147
M 40,v[156:159],156
buffer_store_dwordx4 v[148:151],v1,s[36:39],s57 offen offset:0 sc0 sc1
M 36,v[32:35],160
buffer_store_dwordx4 v[152:155],v1,s[36:39],s57 offen offset:2048 sc0 sc1
M 40,v[160:163],160
buffer_store_dwordx4 v[156:159],v1,s[36:39],s58 offen offset:0 sc0 sc1
s_nop 4
v_add_f32_dpp v44,v36,v36 row_ror:8 row_mask:0xf bank_mask:0xf
s_nop 0
v_cndmask_b32 v45,v40,v41,s[70:71]
v_cndmask_b32 v46,v42,v43,s[70:71]
ds_write_b32 v2,v44 offset:1024
v_cndmask_b32 v45,v45,v46,s[72:73]
ds_write_b32 v0,v45 offset:1024
buffer_store_dwordx4 v[160:163],v1,s[36:39],s58 offen offset:2048 sc0 sc1
L 0
s_barrier
R 52,3,1024
R 56,3,1040
R 60,3,1056
L 0
v_pk_add_f32 v[52:53],v[52:53],v[54:55]
v_pk_add_f32 v[56:57],v[56:57],v[58:59]
v_pk_add_f32 v[60:61],v[60:61],v[62:63]
v_add_f32 v52,v52,v53
v_pk_add_f32 v[56:57],v[56:57],v[60:61]
v_mul_f32 v52,s68,v52
v_add_f32 v53,v56,v57
v_mul_f32 v53,s68,v53
v_fma_f32 v53,-v52,v52,v53
v_max_f32 v53,0,v53
v_add_f32 v54,s69,v53
v_fma_f32 v55,s69,v54,v53
v_rsq_f32 v65,v54
v_rsq_f32 v54,v55
v_mul_f32_e64 v64,-v52,v65
v_mul_f32 v63,v55,v54
v_mul_f32 v61,s76,v54
v_mul_f32 v55,v54,v54
v_mul_f32 v62,s77,v55
v_mul_f32 v62,v62,v61
v_cndmask_b32 v60,v52,v64,s[74:75]
v_cndmask_b32 v61,v61,v65,s[74:75]
buffer_store_dwordx4 v[60:63],v4,s[36:39],s59 offen offset:0 sc0 sc1
s_mov_b32 s63,s60
s_waitcnt vmcnt(5)
v_cmp_eq_u32 vcc,s53,v18
s_nop 0
s_cmp_eq_u64 vcc,-1
s_cselect_b32 s78,s45,256
s_cmp_lt_u32 s78,256
s_cbranch_scc1 Lr_tc1_%=
s_waitcnt vmcnt(0)
buffer_store_dword v17,v5,s[36:39],s63 offen offset:0 sc0 sc1
s_mov_b32 s63,0x7fff0000
s_cmp_ge_u32 s45,256
s_cbranch_scc1 Lr_done_%=
Lr_bp1_%=:
buffer_load_dword v18,v6,s[36:39],s61 offen offset:0 sc1
s_add_u32 s82,s82,1
s_waitcnt vmcnt(0)
v_cmp_eq_u32 vcc,s53,v18
s_nop 0
s_cmp_eq_u64 vcc,-1
s_cbranch_scc1 Lr_bq1_%=
s_sleep 2
s_cmp_lt_u32 s82,20000
s_cbranch_scc1 Lr_bp1_%=
Lr_bq1_%=:
s_add_u32 s78,s55,71303168
buffer_load_dwordx4 v[164:167],v1,s[36:39],s78 offen offset:0 sc1
buffer_load_dwordx4 v[168:171],v1,s[36:39],s78 offen offset:2048 sc1
s_add_u32 s78,s55,71307264
buffer_load_dwordx4 v[172:175],v1,s[36:39],s78 offen offset:0 sc1
buffer_load_dwordx4 v[176:179],v1,s[36:39],s78 offen offset:2048 sc1
s_add_u32 s78,s55,89128960
buffer_load_dwordx4 v[180:183],v1,s[36:39],s78 offen offset:0 sc1
buffer_load_dwordx4 v[184:187],v1,s[36:39],s78 offen offset:2048 sc1
s_add_u32 s78,s55,89133056
buffer_load_dwordx4 v[188:191],v1,s[36:39],s78 offen offset:0 sc1
buffer_load_dwordx4 v[192:195],v1,s[36:39],s78 offen offset:2048 sc1
buffer_load_dword v18,v6,s[36:39],s62 offen offset:0 sc1
s_lshr_b32 s78,s45,1
s_and_b32 s79,s45,1
s_lshl_b32 s79,s79,7
s_lshl_b32 s57,s78,15
s_add_u32 s57,s57,s79
s_add_u32 s57,s57,12582912
s_add_u32 s58,s57,4096
s_lshl_b32 s59,s78,9
s_add_u32 s59,s59,s79
s_add_u32 s59,s59,107020288
s_lshl_b32 s60,s45,9
s_add_u32 s60,s60,s66
s_add_u32 s60,s60,131072
s_add_u32 s45,s45,s46
s_lshr_b32 s78,s45,1
s_and_b32 s79,s45,1
s_lshl_b32 s79,s79,7
s_lshl_b32 s55,s78,15
s_add_u32 s55,s55,s79
s_mul_i32 s56,s45,16384
s_add_u32 s56,s56,98566144
s_lshl_b32 s61,s78,11
s_add_u32 s61,s61,110587904
s_add_u32 s78,s45,s46
s_lshr_b32 s78,s78,1
s_lshl_b32 s62,s78,11
s_add_u32 s62,s62,110587904
s_waitcnt vmcnt(0)
s_branch Lr_it0_%=
Lr_ta1_%=:
v_mfma_f32_16x16x32_bf16 v[148:151],v[196:199],v[212:215],0
s_add_u32 s78,s55,71303168
s_add_u32 m0,s65,0
M 148,v[200:203],216
buffer_load_dwordx4 v[164:167],v1,s[36:39],s78 offen offset:0 sc1
M 148,v[204:207],220
buffer_load_dwordx4 v[168:171],v1,s[36:39],s78 offen offset:2048 sc1
M 148,v[208:211],224
L 4
D 9,56,0
v_mfma_f32_16x16x32_bf16 v[40:43],v[196:199],v[196:199],0
D 9,56,1024
M 40,v[200:203],200
L 0
D 9,56,2048
M 40,v[204:207],204
D 9,56,3072
M 40,v[208:211],208
s_add_u32 s78,s55,71307264
v_lshlrev_b32 v84,16,v68
v_and_b32 v85,0xffff0000,v68
M 40,v[212:215],212
buffer_load_dwordx4 v[172:175],v1,s[36:39],s78 offen offset:0 sc1
M 40,v[216:219],216
buffer_load_dwordx4 v[176:179],v1,s[36:39],s78 offen offset:2048 sc1
M 40,v[220:223],220
s_add_u32 s78,s55,89128960
v_lshlrev_b32 v86,16,v69
v_and_b32 v87,0xffff0000,v69
M 40,v[224:227],224
buffer_load_dwordx4 v[180:183],v1,s[36:39],s78 offen offset:0 sc1
v_mfma_f32_16x16x32_bf16 v[36:39],v[32:35],v[196:199],0
buffer_load_dwordx4 v[184:187],v1,s[36:39],s78 offen offset:2048 sc1
M 36,v[32:35],200
s_add_u32 s78,s55,89133056
v_lshlrev_b32 v88,16,v70
v_and_b32 v89,0xffff0000,v70
M 36,v[32:35],204
buffer_load_dwordx4 v[188:191],v1,s[36:39],s78 offen offset:0 sc1
M 36,v[32:35],208
buffer_load_dwordx4 v[192:195],v1,s[36:39],s78 offen offset:2048 sc1
M 36,v[32:35],212
v_fma_f32 v40,2.0,v148,v40
v_fma_f32 v41,2.0,v149,v41
M 36,v[32:35],216
v_fma_f32 v42,2.0,v150,v42
v_fma_f32 v43,2.0,v151,v43
M 36,v[32:35],220
v_cndmask_b32 v45,v40,v41,s[70:71]
v_cndmask_b32 v46,v42,v43,s[70:71]
M 36,v[32:35],224
v_cndmask_b32 v45,v45,v46,s[72:73]
v_lshlrev_b32 v90,16,v71
v_mfma_f32_16x16x32_bf16 v[116:119],v[26:29],v[196:199],0
v_and_b32 v91,0xffff0000,v71
v_lshlrev_b32 v92,16,v72
M 116,v[26:29],212
v_and_b32 v93,0xffff0000,v72
v_lshlrev_b32 v94,16,v73
v_mfma_f32_16x16x32_bf16 v[120:123],v[24:27],v[196:199],0
v_and_b32 v95,0xffff0000,v73
v_lshlrev_b32 v96,16,v74
M 120,v[24:27],212
v_and_b32 v97,0xffff0000,v74
v_lshlrev_b32 v98,16,v75
v_mfma_f32_16x16x32_bf16 v[124:127],v[26:29],v[200:203],0
v_add_f32_dpp v44,v36,v36 row_ror:8 row_mask:0xf bank_mask:0xf
v_and_b32 v99,0xffff0000,v75
M 124,v[26:29],216
ds_write_b32 v2,v44 offset:0
v_mfma_f32_16x16x32_bf16 v[128:131],v[24:27],v[200:203],0
ds_write_b32 v0,v45 offset:0
M 128,v[24:27],216
v_lshlrev_b32 v100,16,v76
v_and_b32 v101,0xffff0000,v76
v_mfma_f32_16x16x32_bf16 v[132:135],v[26:29],v[204:207],0
v_lshlrev_b32 v102,16,v77
v_and_b32 v103,0xffff0000,v77
M 132,v[26:29],220
L 0
v_lshlrev_b32 v104,16,v78
v_and_b32 v105,0xffff0000,v78
v_mfma_f32_16x16x32_bf16 v[136:139],v[24:27],v[204:207],0
s_barrier
R 52,3,0
R 56,3,16
M 136,v[24:27],220
R 60,3,32
v_lshlrev_b32 v106,16,v79
v_mfma_f32_16x16x32_bf16 v[140:143],v[26:29],v[208:211],0
v_and_b32 v107,0xffff0000,v79
v_lshlrev_b32 v108,16,v80
M 140,v[26:29],224
v_and_b32 v109,0xffff0000,v80
v_lshlrev_b32 v110,16,v81
v_mfma_f32_16x16x32_bf16 v[144:147],v[24:27],v[208:211],0
v_and_b32 v111,0xffff0000,v81
v_lshlrev_b32 v112,16,v82
M 144,v[24:27],224
v_and_b32 v113,0xffff0000,v82
v_lshlrev_b32 v114,16,v83
v_and_b32 v115,0xffff0000,v83
L 0
v_pk_add_f32 v[52:53],v[52:53],v[54:55]
v_pk_add_f32 v[56:57],v[56:57],v[58:59]
v_pk_add_f32 v[60:61],v[60:61],v[62:63]
v_add_f32 v52,v52,v53
v_pk_add_f32 v[56:57],v[56:57],v[60:61]
v_mul_f32 v52,s68,v52
v_add_f32 v53,v56,v57
v_mul_f32 v53,s68,v53
v_fma_f32 v53,-v52,v52,v53
v_max_f32 v53,0,v53
buffer_load_dword v18,v6,s[36:39],s62 offen offset:0 sc1
v_add_f32 v53,s69,v53
v_rsq_f32 v48,v53
s_nop 0
v_mul_f32_e64 v50,-v52,v48
v_mov_b32 v49,v48
v_mov_b32 v51,v50
v_pk_fma_f32 v[52:53],v[116:117],v[48:49],v[50:51]
v_pk_fma_f32 v[54:55],v[118:119],v[48:49],v[50:51]
v_pk_fma_f32 v[56:57],v[120:121],v[48:49],v[50:51]
v_pk_fma_f32 v[58:59],v[122:123],v[48:49],v[50:51]
v_pk_fma_f32 v[116:117],v[52:53],v[84:85],v[116:117]
v_pk_fma_f32 v[118:119],v[54:55],v[86:87],v[118:119]
v_pk_fma_f32 v[120:121],v[56:57],v[88:89],v[120:121]
v_pk_fma_f32 v[122:123],v[58:59],v[90:91],v[122:123]
v_pk_fma_f32 v[52:53],v[124:125],v[48:49],v[50:51]
v_pk_fma_f32 v[54:55],v[126:127],v[48:49],v[50:51]
v_pk_fma_f32 v[56:57],v[128:129],v[48:49],v[50:51]
v_pk_fma_f32 v[58:59],v[130:131],v[48:49],v[50:51]
v_pk_fma_f32 v[124:125],v[52:53],v[92:93],v[124:125]
v_pk_fma_f32 v[126:127],v[54:55],v[94:95],v[126:127]
v_pk_fma_f32 v[128:129],v[56:57],v[96:97],v[128:129]
v_pk_fma_f32 v[130:131],v[58:59],v[98:99],v[130:131]
v_pk_fma_f32 v[52:53],v[132:133],v[48:49],v[50:51]
v_pk_fma_f32 v[54:55],v[134:135],v[48:49],v[50:51]
v_pk_fma_f32 v[56:57],v[136:137],v[48:49],v[50:51]
v_pk_fma_f32 v[58:59],v[138:139],v[48:49],v[50:51]
v_pk_fma_f32 v[132:133],v[52:53],v[100:101],v[132:133]
v_pk_fma_f32 v[134:135],v[54:55],v[102:103],v[134:135]
v_pk_fma_f32 v[136:137],v[56:57],v[104:105],v[136:137]
v_pk_fma_f32 v[138:139],v[58:59],v[106:107],v[138:139]
v_pk_fma_f32 v[52:53],v[140:141],v[48:49],v[50:51]
v_pk_fma_f32 v[54:55],v[142:143],v[48:49],v[50:51]
v_pk_fma_f32 v[56:57],v[144:145],v[48:49],v[50:51]
v_pk_fma_f32 v[58:59],v[146:147],v[48:49],v[50:51]
v_pk_fma_f32 v[140:141],v[52:53],v[108:109],v[140:141]
v_pk_fma_f32 v[142:143],v[54:55],v[110:111],v[142:143]
v_pk_fma_f32 v[144:145],v[56:57],v[112:113],v[144:145]
v_pk_fma_f32 v[146:147],v[58:59],v[114:115],v[146:147]
v_cvt_pk_bf16_f32 v148,v116,v117
v_cvt_pk_bf16_f32 v149,v118,v119
v_cvt_pk_bf16_f32 v150,v120,v121
v_cvt_pk_bf16_f32 v151,v122,v123
v_cvt_pk_bf16_f32 v152,v124,v125
v_cvt_pk_bf16_f32 v153,v126,v127
s_waitcnt vmcnt(13)
buffer_store_dword v17,v5,s[36:39],s63 offen offset:0 sc0 sc1
s_mov_b32 s63,0x7fff0000
v_mfma_f32_16x16x32_bf16 v[36:39],v[32:35],v[148:151],0
v_cvt_pk_bf16_f32 v154,v128,v129
v_cvt_pk_bf16_f32 v155,v130,v131
v_mfma_f32_16x16x32_bf16 v[40:43],v[148:151],v[148:151],0
v_cvt_pk_bf16_f32 v156,v132,v133
v_cvt_pk_bf16_f32 v157,v134,v135
M 36,v[32:35],152
v_cvt_pk_bf16_f32 v158,v136,v137
v_cvt_pk_bf16_f32 v159,v138,v139
M 40,v[152:155],152
v_cvt_pk_bf16_f32 v160,v140,v141
v_cvt_pk_bf16_f32 v161,v142,v143
M 36,v[32:35],156
v_cvt_pk_bf16_f32 v162,v144,v145
v_cvt_pk_bf16_f32 v163,v146,v147
M 40,v[156:159],156
buffer_store_dwordx4 v[148:151],v1,s[36:39],s57 offen offset:0 sc0 sc1
M 36,v[32:35],160
buffer_store_dwordx4 v[152:155],v1,s[36:39],s57 offen offset:2048 sc0 sc1
M 40,v[160:163],160
buffer_store_dwordx4 v[156:159],v1,s[36:39],s58 offen offset:0 sc0 sc1
s_nop 4
v_add_f32_dpp v44,v36,v36 row_ror:8 row_mask:0xf bank_mask:0xf
s_nop 0
v_cndmask_b32 v45,v40,v41,s[70:71]
v_cndmask_b32 v46,v42,v43,s[70:71]
ds_write_b32 v2,v44 offset:1024
v_cndmask_b32 v45,v45,v46,s[72:73]
ds_write_b32 v0,v45 offset:1024
buffer_store_dwordx4 v[160:163],v1,s[36:39],s58 offen offset:2048 sc0 sc1
L 0
s_barrier
R 52,3,1024
R 56,3,1040
R 60,3,1056
L 0
v_pk_add_f32 v[52:53],v[52:53],v[54:55]
v_pk_add_f32 v[56:57],v[56:57],v[58:59]
v_pk_add_f32 v[60:61],v[60:61],v[62:63]
v_add_f32 v52,v52,v53
v_pk_add_f32 v[56:57],v[56:57],v[60:61]
v_mul_f32 v52,s68,v52
v_add_f32 v53,v56,v57
v_mul_f32 v53,s68,v53
v_fma_f32 v53,-v52,v52,v53
v_max_f32 v53,0,v53
v_add_f32 v54,s69,v53
v_fma_f32 v55,s69,v54,v53
v_rsq_f32 v65,v54
v_rsq_f32 v54,v55
v_mul_f32_e64 v64,-v52,v65
v_mul_f32 v63,v55,v54
v_mul_f32 v61,s76,v54
v_mul_f32 v55,v54,v54
v_mul_f32 v62,s77,v55
v_mul_f32 v62,v62,v61
v_cndmask_b32 v60,v52,v64,s[74:75]
v_cndmask_b32 v61,v61,v65,s[74:75]
buffer_store_dwordx4 v[60:63],v4,s[36:39],s59 offen offset:0 sc0 sc1
s_mov_b32 s63,s60
s_lshr_b32 s78,s45,1
s_and_b32 s79,s45,1
s_lshl_b32 s79,s79,7
s_lshl_b32 s57,s78,15
s_add_u32 s57,s57,s79
s_add_u32 s57,s57,12582912
s_add_u32 s58,s57,4096
s_lshl_b32 s59,s78,9
s_add_u32 s59,s59,s79
s_add_u32 s59,s59,107020288
s_lshl_b32 s60,s45,9
s_add_u32 s60,s60,s66
s_add_u32 s60,s60,131072
s_add_u32 s45,s45,s46
s_lshr_b32 s78,s45,1
s_and_b32 s79,s45,1
s_lshl_b32 s79,s79,7
s_lshl_b32 s55,s78,15
s_add_u32 s55,s55,s79
s_mul_i32 s56,s45,16384
s_add_u32 s56,s56,98566144
s_lshl_b32 s61,s78,11
s_add_u32 s61,s61,110587904
s_add_u32 s78,s45,s46
s_lshr_b32 s78,s78,1
s_lshl_b32 s62,s78,11
s_add_u32 s62,s62,110587904
s_waitcnt vmcnt(6)
s_branch Lr_it0_%=
Lr_tb1_%=:
v_add_f32 v53,s69,v53
v_rsq_f32 v48,v53
s_nop 0
v_mul_f32_e64 v50,-v52,v48
v_mov_b32 v49,v48
v_mov_b32 v51,v50
v_pk_fma_f32 v[52:53],v[116:117],v[48:49],v[50:51]
v_pk_fma_f32 v[54:55],v[118:119],v[48:49],v[50:51]
v_pk_fma_f32 v[56:57],v[120:121],v[48:49],v[50:51]
v_pk_fma_f32 v[58:59],v[122:123],v[48:49],v[50:51]
s_add_u32 s78,s55,71303168
buffer_load_dwordx4 v[164:167],v1,s[36:39],s78 offen offset:0 sc1
buffer_load_dwordx4 v[168:171],v1,s[36:39],s78 offen offset:2048 sc1
s_add_u32 s78,s55,71307264
buffer_load_dwordx4 v[172:175],v1,s[36:39],s78 offen offset:0 sc1
buffer_load_dwordx4 v[176:179],v1,s[36:39],s78 offen offset:2048 sc1
v_pk_fma_f32 v[116:117],v[52:53],v[84:85],v[116:117]
v_pk_fma_f32 v[118:119],v[54:55],v[86:87],v[118:119]
v_pk_fma_f32 v[120:121],v[56:57],v[88:89],v[120:121]
v_pk_fma_f32 v[122:123],v[58:59],v[90:91],v[122:123]
v_pk_fma_f32 v[52:53],v[124:125],v[48:49],v[50:51]
v_pk_fma_f32 v[54:55],v[126:127],v[48:49],v[50:51]
v_pk_fma_f32 v[56:57],v[128:129],v[48:49],v[50:51]
v_pk_fma_f32 v[58:59],v[130:131],v[48:49],v[50:51]
s_add_u32 s78,s55,89128960
buffer_load_dwordx4 v[180:183],v1,s[36:39],s78 offen offset:0 sc1
buffer_load_dwordx4 v[184:187],v1,s[36:39],s78 offen offset:2048 sc1
s_add_u32 s78,s55,89133056
buffer_load_dwordx4 v[188:191],v1,s[36:39],s78 offen offset:0 sc1
buffer_load_dwordx4 v[192:195],v1,s[36:39],s78 offen offset:2048 sc1
v_pk_fma_f32 v[124:125],v[52:53],v[92:93],v[124:125]
v_pk_fma_f32 v[126:127],v[54:55],v[94:95],v[126:127]
v_pk_fma_f32 v[128:129],v[56:57],v[96:97],v[128:129]
v_pk_fma_f32 v[130:131],v[58:59],v[98:99],v[130:131]
v_pk_fma_f32 v[52:53],v[132:133],v[48:49],v[50:51]
v_pk_fma_f32 v[54:55],v[134:135],v[48:49],v[50:51]
v_pk_fma_f32 v[56:57],v[136:137],v[48:49],v[50:51]
v_pk_fma_f32 v[58:59],v[138:139],v[48:49],v[50:51]
v_pk_fma_f32 v[132:133],v[52:53],v[100:101],v[132:133]
v_pk_fma_f32 v[134:135],v[54:55],v[102:103],v[134:135]
v_pk_fma_f32 v[136:137],v[56:57],v[104:105],v[136:137]
v_pk_fma_f32 v[138:139],v[58:59],v[106:107],v[138:139]
v_pk_fma_f32 v[52:53],v[140:141],v[48:49],v[50:51]
v_pk_fma_f32 v[54:55],v[142:143],v[48:49],v[50:51]
v_pk_fma_f32 v[56:57],v[144:145],v[48:49],v[50:51]
v_pk_fma_f32 v[58:59],v[146:147],v[48:49],v[50:51]
v_pk_fma_f32 v[140:141],v[52:53],v[108:109],v[140:141]
v_pk_fma_f32 v[142:143],v[54:55],v[110:111],v[142:143]
v_pk_fma_f32 v[144:145],v[56:57],v[112:113],v[144:145]
v_pk_fma_f32 v[146:147],v[58:59],v[114:115],v[146:147]
buffer_load_dword v18,v6,s[36:39],s62 offen offset:0 sc1
v_cvt_pk_bf16_f32 v148,v116,v117
v_cvt_pk_bf16_f32 v149,v118,v119
v_cvt_pk_bf16_f32 v150,v120,v121
v_cvt_pk_bf16_f32 v151,v122,v123
v_cvt_pk_bf16_f32 v152,v124,v125
v_cvt_pk_bf16_f32 v153,v126,v127
v_mfma_f32_16x16x32_bf16 v[36:39],v[32:35],v[148:151],0
v_cvt_pk_bf16_f32 v154,v128,v129
v_cvt_pk_bf16_f32 v155,v130,v131
v_mfma_f32_16x16x32_bf16 v[40:43],v[148:151],v[148:151],0
v_cvt_pk_bf16_f32 v156,v132,v133
v_cvt_pk_bf16_f32 v157,v134,v135
M 36,v[32:35],152
v_cvt_pk_bf16_f32 v158,v136,v137
v_cvt_pk_bf16_f32 v159,v138,v139
M 40,v[152:155],152
v_cvt_pk_bf16_f32 v160,v140,v141
v_cvt_pk_bf16_f32 v161,v142,v143
M 36,v[32:35],156
v_cvt_pk_bf16_f32 v162,v144,v145
v_cvt_pk_bf16_f32 v163,v146,v147
M 40,v[156:159],156
buffer_store_dwordx4 v[148:151],v1,s[36:39],s57 offen offset:0 sc0 sc1
M 36,v[32:35],160
buffer_store_dwordx4 v[152:155],v1,s[36:39],s57 offen offset:2048 sc0 sc1
M 40,v[160:163],160
buffer_store_dwordx4 v[156:159],v1,s[36:39],s58 offen offset:0 sc0 sc1
s_nop 4
v_add_f32_dpp v44,v36,v36 row_ror:8 row_mask:0xf bank_mask:0xf
s_nop 0
v_cndmask_b32 v45,v40,v41,s[70:71]
v_cndmask_b32 v46,v42,v43,s[70:71]
ds_write_b32 v2,v44 offset:1024
v_cndmask_b32 v45,v45,v46,s[72:73]
ds_write_b32 v0,v45 offset:1024
buffer_store_dwordx4 v[160:163],v1,s[36:39],s58 offen offset:2048 sc0 sc1
)"
R"(L 0
s_barrier
R 52,3,1024
R 56,3,1040
R 60,3,1056
L 0
v_pk_add_f32 v[52:53],v[52:53],v[54:55]
v_pk_add_f32 v[56:57],v[56:57],v[58:59]
v_pk_add_f32 v[60:61],v[60:61],v[62:63]
v_add_f32 v52,v52,v53
v_pk_add_f32 v[56:57],v[56:57],v[60:61]
v_mul_f32 v52,s68,v52
v_add_f32 v53,v56,v57
v_mul_f32 v53,s68,v53
v_fma_f32 v53,-v52,v52,v53
v_max_f32 v53,0,v53
v_add_f32 v54,s69,v53
v_fma_f32 v55,s69,v54,v53
v_rsq_f32 v65,v54
v_rsq_f32 v54,v55
v_mul_f32_e64 v64,-v52,v65
v_mul_f32 v63,v55,v54
v_mul_f32 v61,s76,v54
v_mul_f32 v55,v54,v54
v_mul_f32 v62,s77,v55
v_mul_f32 v62,v62,v61
v_cndmask_b32 v60,v52,v64,s[74:75]
v_cndmask_b32 v61,v61,v65,s[74:75]
buffer_store_dwordx4 v[60:63],v4,s[36:39],s59 offen offset:0 sc0 sc1
s_mov_b32 s63,s60
s_lshr_b32 s78,s45,1
s_and_b32 s79,s45,1
s_lshl_b32 s79,s79,7
s_lshl_b32 s57,s78,15
s_add_u32 s57,s57,s79
s_add_u32 s57,s57,12582912
s_add_u32 s58,s57,4096
s_lshl_b32 s59,s78,9
s_add_u32 s59,s59,s79
s_add_u32 s59,s59,107020288
s_lshl_b32 s60,s45,9
s_add_u32 s60,s60,s66
s_add_u32 s60,s60,131072
s_add_u32 s45,s45,s46
s_lshr_b32 s78,s45,1
s_and_b32 s79,s45,1
s_lshl_b32 s79,s79,7
s_lshl_b32 s55,s78,15
s_add_u32 s55,s55,s79
s_mul_i32 s56,s45,16384
s_add_u32 s56,s56,98566144
s_lshl_b32 s61,s78,11
s_add_u32 s61,s61,110587904
s_add_u32 s78,s45,s46
s_lshr_b32 s78,s78,1
s_lshl_b32 s62,s78,11
s_add_u32 s62,s62,110587904
s_waitcnt vmcnt(5)
s_branch Lr_it0_%=
Lr_tc1_%=:
s_add_u32 s78,s55,71303168
buffer_load_dwordx4 v[164:167],v1,s[36:39],s78 offen offset:0 sc1
buffer_load_dwordx4 v[168:171],v1,s[36:39],s78 offen offset:2048 sc1
s_add_u32 s78,s55,71307264
buffer_load_dwordx4 v[172:175],v1,s[36:39],s78 offen offset:0 sc1
buffer_load_dwordx4 v[176:179],v1,s[36:39],s78 offen offset:2048 sc1
s_add_u32 s78,s55,89128960
buffer_load_dwordx4 v[180:183],v1,s[36:39],s78 offen offset:0 sc1
buffer_load_dwordx4 v[184:187],v1,s[36:39],s78 offen offset:2048 sc1
s_add_u32 s78,s55,89133056
buffer_load_dwordx4 v[188:191],v1,s[36:39],s78 offen offset:0 sc1
buffer_load_dwordx4 v[192:195],v1,s[36:39],s78 offen offset:2048 sc1
buffer_load_dword v18,v6,s[36:39],s62 offen offset:0 sc1
s_waitcnt vmcnt(9)
buffer_store_dword v17,v5,s[36:39],s63 offen offset:0 sc0 sc1
s_mov_b32 s63,0x7fff0000
s_lshr_b32 s78,s45,1
s_and_b32 s79,s45,1
s_lshl_b32 s79,s79,7
s_lshl_b32 s57,s78,15
s_add_u32 s57,s57,s79
s_add_u32 s57,s57,12582912
s_add_u32 s58,s57,4096
s_lshl_b32 s59,s78,9
s_add_u32 s59,s59,s79
s_add_u32 s59,s59,107020288
s_lshl_b32 s60,s45,9
s_add_u32 s60,s60,s66
s_add_u32 s60,s60,131072
s_add_u32 s45,s45,s46
s_lshr_b32 s78,s45,1
s_and_b32 s79,s45,1
s_lshl_b32 s79,s79,7
s_lshl_b32 s55,s78,15
s_add_u32 s55,s55,s79
s_mul_i32 s56,s45,16384
s_add_u32 s56,s56,98566144
s_lshl_b32 s61,s78,11
s_add_u32 s61,s61,110587904
s_add_u32 s78,s45,s46
s_lshr_b32 s78,s78,1
s_lshl_b32 s62,s78,11
s_add_u32 s62,s62,110587904
s_waitcnt vmcnt(1)
s_branch Lr_it0_%=
Lr_done_%=:
s_waitcnt vmcnt(0) lgkmcnt(0)
s_barrier
s_branch Lr_end_%=
.p2align 8
Lr_tab_%=:
s_mov_b64 s[40:41],%24
s_mov_b32 s42,7340032
s_mov_b32 s43,0x00020000
s_add_u32 s83,s50,0
s_add_u32 m0,s54,40960
s_nop 0
buffer_load_dwordx4 v13,s[40:43],s83 offen offset:0  lds
s_add_u32 m0,s54,41984
s_nop 0
buffer_load_dwordx4 v14,s[40:43],s83 offen offset:0  lds
s_add_u32 m0,s54,49024
s_nop 0
buffer_load_dwordx4 v13,s[40:43],s83 offen offset:128  lds
s_add_u32 m0,s54,50048
s_nop 0
buffer_load_dwordx4 v14,s[40:43],s83 offen offset:128  lds
s_setpc_b64 s[88:89]
.p2align 8
s_mov_b64 s[40:41],%24
s_mov_b32 s42,7340032
s_mov_b32 s43,0x00020000
v_add_u32 v230,40960,v15
v_add_u32 v231,40960,v16
T a[0:1],230,0
T a[2:3],230,512
T a[4:5],231,0
T a[6:7],231,512
T a[8:9],230,4096
T a[10:11],230,4608
T a[12:13],231,4096
T a[14:15],231,4608
s_add_u32 s83,s50,131072
s_add_u32 m0,s54,57344
s_nop 0
buffer_load_dwordx4 v13,s[40:43],s83 offen offset:0  lds
s_add_u32 m0,s54,58368
s_nop 0
buffer_load_dwordx4 v14,s[40:43],s83 offen offset:0  lds
s_add_u32 m0,s54,65408
s_nop 0
buffer_load_dwordx4 v13,s[40:43],s83 offen offset:128  lds
s_add_u32 m0,s54,66432
s_nop 0
buffer_load_dwordx4 v14,s[40:43],s83 offen offset:128  lds
s_setpc_b64 s[88:89]
.p2align 8
s_mov_b64 s[40:41],%24
s_mov_b32 s42,7340032
s_mov_b32 s43,0x00020000
v_add_u32 v230,57344,v15
v_add_u32 v231,57344,v16
T a[16:17],230,0
T a[18:19],230,512
T a[20:21],231,0
T a[22:23],231,512
T a[24:25],230,4096
T a[26:27],230,4608
T a[28:29],231,4096
T a[30:31],231,4608
s_add_u32 s83,s50,262144
s_add_u32 m0,s54,73728
s_nop 0
buffer_load_dwordx4 v13,s[40:43],s83 offen offset:0  lds
s_add_u32 m0,s54,74752
s_nop 0
buffer_load_dwordx4 v14,s[40:43],s83 offen offset:0  lds
s_add_u32 m0,s54,81792
s_nop 0
buffer_load_dwordx4 v13,s[40:43],s83 offen offset:128  lds
s_add_u32 m0,s54,82816
s_nop 0
buffer_load_dwordx4 v14,s[40:43],s83 offen offset:128  lds
s_setpc_b64 s[88:89]
.p2align 8
s_mov_b64 s[40:41],%24
s_mov_b32 s42,7340032
s_mov_b32 s43,0x00020000
v_add_u32 v230,73728,v15
v_add_u32 v231,73728,v16
T a[32:33],230,0
T a[34:35],230,512
T a[36:37],231,0
T a[38:39],231,512
T a[40:41],230,4096
T a[42:43],230,4608
T a[44:45],231,4096
T a[46:47],231,4608
s_add_u32 s83,s50,393216
s_add_u32 m0,s54,90112
s_nop 0
buffer_load_dwordx4 v13,s[40:43],s83 offen offset:0  lds
s_add_u32 m0,s54,91136
s_nop 0
buffer_load_dwordx4 v14,s[40:43],s83 offen offset:0  lds
s_add_u32 m0,s54,98176
s_nop 0
buffer_load_dwordx4 v13,s[40:43],s83 offen offset:128  lds
s_add_u32 m0,s54,99200
s_nop 0
buffer_load_dwordx4 v14,s[40:43],s83 offen offset:128  lds
s_setpc_b64 s[88:89]
.p2align 8
s_mov_b64 s[40:41],%24
s_mov_b32 s42,7340032
s_mov_b32 s43,0x00020000
v_add_u32 v230,90112,v15
v_add_u32 v231,90112,v16
T a[48:49],230,0
T a[50:51],230,512
T a[52:53],231,0
T a[54:55],231,512
T a[56:57],230,4096
T a[58:59],230,4608
T a[60:61],231,4096
T a[62:63],231,4608
s_add_u32 s83,s50,524288
s_add_u32 m0,s54,106496
s_nop 0
buffer_load_dwordx4 v13,s[40:43],s83 offen offset:0  lds
s_add_u32 m0,s54,107520
s_nop 0
buffer_load_dwordx4 v14,s[40:43],s83 offen offset:0  lds
s_add_u32 m0,s54,114560
s_nop 0
buffer_load_dwordx4 v13,s[40:43],s83 offen offset:128  lds
s_add_u32 m0,s54,115584
s_nop 0
buffer_load_dwordx4 v14,s[40:43],s83 offen offset:128  lds
s_setpc_b64 s[88:89]
.p2align 8
s_mov_b64 s[40:41],%24
s_mov_b32 s42,7340032
s_mov_b32 s43,0x00020000
v_add_u32 v230,106496,v15
v_add_u32 v231,106496,v16
T a[64:65],230,0
T a[66:67],230,512
T a[68:69],231,0
T a[70:71],231,512
T a[72:73],230,4096
T a[74:75],230,4608
T a[76:77],231,4096
T a[78:79],231,4608
s_add_u32 s83,s50,655360
s_add_u32 m0,s54,122880
s_nop 0
buffer_load_dwordx4 v13,s[40:43],s83 offen offset:0  lds
s_add_u32 m0,s54,123904
s_nop 0
buffer_load_dwordx4 v14,s[40:43],s83 offen offset:0  lds
s_add_u32 m0,s54,130944
s_nop 0
buffer_load_dwordx4 v13,s[40:43],s83 offen offset:128  lds
s_add_u32 m0,s54,131968
s_nop 0
buffer_load_dwordx4 v14,s[40:43],s83 offen offset:128  lds
s_setpc_b64 s[88:89]
.p2align 8
s_mov_b64 s[40:41],%24
s_mov_b32 s42,7340032
s_mov_b32 s43,0x00020000
v_add_u32 v230,122880,v15
v_add_u32 v231,122880,v16
T a[80:81],230,0
T a[82:83],230,512
T a[84:85],231,0
T a[86:87],231,512
T a[88:89],230,4096
T a[90:91],230,4608
T a[92:93],231,4096
T a[94:95],231,4608
s_add_u32 s83,s50,786432
s_add_u32 m0,s54,139264
s_nop 0
buffer_load_dwordx4 v13,s[40:43],s83 offen offset:0  lds
s_add_u32 m0,s54,140288
s_nop 0
buffer_load_dwordx4 v14,s[40:43],s83 offen offset:0  lds
s_add_u32 m0,s54,147328
s_nop 0
buffer_load_dwordx4 v13,s[40:43],s83 offen offset:128  lds
s_add_u32 m0,s54,148352
s_nop 0
buffer_load_dwordx4 v14,s[40:43],s83 offen offset:128  lds
s_setpc_b64 s[88:89]
.p2align 8
s_mov_b64 s[40:41],%24
s_mov_b32 s42,7340032
s_mov_b32 s43,0x00020000
v_add_u32 v230,139264,v15
v_add_u32 v231,139264,v16
T a[96:97],230,0
T a[98:99],230,512
T a[100:101],231,0
T a[102:103],231,512
T a[104:105],230,4096
T a[106:107],230,4608
T a[108:109],231,4096
T a[110:111],231,4608
s_add_u32 s83,s50,917504
s_add_u32 m0,s54,40960
s_nop 0
buffer_load_dwordx4 v13,s[40:43],s83 offen offset:0  lds
s_add_u32 m0,s54,41984
s_nop 0
buffer_load_dwordx4 v14,s[40:43],s83 offen offset:0  lds
s_add_u32 m0,s54,49024
s_nop 0
buffer_load_dwordx4 v13,s[40:43],s83 offen offset:128  lds
s_add_u32 m0,s54,50048
s_nop 0
buffer_load_dwordx4 v14,s[40:43],s83 offen offset:128  lds
s_setpc_b64 s[88:89]
.p2align 8
s_mov_b64 s[40:41],%24
s_mov_b32 s42,7340032
s_mov_b32 s43,0x00020000
v_add_u32 v230,40960,v15
v_add_u32 v231,40960,v16
T a[112:113],230,0
T a[114:115],230,512
T a[116:117],231,0
T a[118:119],231,512
T a[120:121],230,4096
T a[122:123],230,4608
T a[124:125],231,4096
T a[126:127],231,4608
s_add_u32 s83,s50,1048576
s_add_u32 m0,s54,57344
s_nop 0
buffer_load_dwordx4 v13,s[40:43],s83 offen offset:0  lds
s_add_u32 m0,s54,58368
s_nop 0
buffer_load_dwordx4 v14,s[40:43],s83 offen offset:0  lds
s_add_u32 m0,s54,65408
s_nop 0
buffer_load_dwordx4 v13,s[40:43],s83 offen offset:128  lds
s_add_u32 m0,s54,66432
s_nop 0
buffer_load_dwordx4 v14,s[40:43],s83 offen offset:128  lds
s_setpc_b64 s[88:89]
.p2align 8
s_mov_b64 s[40:41],%24
s_mov_b32 s42,7340032
s_mov_b32 s43,0x00020000
v_add_u32 v230,57344,v15
v_add_u32 v231,57344,v16
T a[128:129],230,0
T a[130:131],230,512
T a[132:133],231,0
T a[134:135],231,512
T a[136:137],230,4096
T a[138:139],230,4608
T a[140:141],231,4096
T a[142:143],231,4608
s_add_u32 s83,s50,1179648
s_add_u32 m0,s54,73728
s_nop 0
buffer_load_dwordx4 v13,s[40:43],s83 offen offset:0  lds
s_add_u32 m0,s54,74752
s_nop 0
buffer_load_dwordx4 v14,s[40:43],s83 offen offset:0  lds
s_add_u32 m0,s54,81792
s_nop 0
buffer_load_dwordx4 v13,s[40:43],s83 offen offset:128  lds
s_add_u32 m0,s54,82816
s_nop 0
buffer_load_dwordx4 v14,s[40:43],s83 offen offset:128  lds
s_setpc_b64 s[88:89]
.p2align 8
s_mov_b64 s[40:41],%24
s_mov_b32 s42,7340032
s_mov_b32 s43,0x00020000
v_add_u32 v230,73728,v15
v_add_u32 v231,73728,v16
T a[144:145],230,0
T a[146:147],230,512
T a[148:149],231,0
T a[150:151],231,512
T a[152:153],230,4096
T a[154:155],230,4608
T a[156:157],231,4096
T a[158:159],231,4608
s_add_u32 s83,s50,1310720
s_add_u32 m0,s54,90112
s_nop 0
buffer_load_dwordx4 v13,s[40:43],s83 offen offset:0  lds
s_add_u32 m0,s54,91136
s_nop 0
buffer_load_dwordx4 v14,s[40:43],s83 offen offset:0  lds
s_add_u32 m0,s54,98176
s_nop 0
buffer_load_dwordx4 v13,s[40:43],s83 offen offset:128  lds
s_add_u32 m0,s54,99200
s_nop 0
buffer_load_dwordx4 v14,s[40:43],s83 offen offset:128  lds
s_setpc_b64 s[88:89]
.p2align 8
Lr_end_%=:
Lcode_r_e:
.purgem M
.purgem R
.purgem T
.purgem D
.purgem L
)"
  :: "s"(ws), "s"(__builtin_amdgcn_readfirstlane(sWave)), "s"(__builtin_amdgcn_readfirstlane(sG0)), "s"(__builtin_amdgcn_readfirstlane(sStep)), "s"(__builtin_amdgcn_readfirstlane(sWw)), "s"(__builtin_amdgcn_readfirstlane(sExp)), "v"(vRP), "v"(vRF), "v"(vRwr), "v"(vRrd), "v"(vRS), "v"(vFlag), "v"(vL4), "v"(vLY), "v"(vQ), "v"(vY0), "v"(vTr), "v"(vSa), "v"(vSb), "v"(vWd0), "v"(vWd1), "v"(vWr0), "v"(vWr1), "s"(q), "s"(w4) : "v0", "v1", "v2", "v3", "v4", "v5", "v6", "v7", "v8", "v9", "v10", "v11", "v12", "v13", "v14", "v15", "v16", "v17", "v18", "v19", "v20", "v21", "v22", "v23", "v24", "v25", "v26", "v27", "v28", "v29", "v30", "v31", "v32", "v33", "v34", "v35", "v36", "v37", "v38", "v39", "v40", "v41", "v42", "v43", "v44", "v45", "v46", "v47", "v48", "v49", "v50", "v51", "v52", "v53", "v54", "v55", "v56", "v57", "v58", "v59", "v60", "v61", "v62", "v63", "v64", "v65", "v66", "v67", "v68", "v69", "v70", "v71", "v72", "v73", "v74", "v75", "v76", "v77", "v78", "v79", "v80", "v81", "v82", "v83", "v84", "v85", "v86", "v87", "v88", "v89", "v90", "v91", "v92", "v93", "v94", "v95", "v96", "v97", "v98", "v99", "v100", "v101", "v102", "v103", "v104", "v105", "v106", "v107", "v108", "v109", "v110", "v111", "v112", "v113", "v114", "v115", "v116", "v117", "v118", "v119", "v120", "v121", "v122", "v123", "v124", "v125", "v126", "v127", "v128", "v129", "v130", "v131", "v132", "v133", "v134", "v135", "v136", "v137", "v138", "v139", "v140", "v141", "v142", "v143", "v144", "v145", "v146", "v147", "v148", "v149", "v150", "v151", "v152", "v153", "v154", "v155", "v156", "v157", "v158", "v159", "v160", "v161", "v162", "v163", "v164", "v165", "v166", "v167", "v168", "v169", "v170", "v171", "v172", "v173", "v174", "v175", "v176", "v177", "v178", "v179", "v180", "v181", "v182", "v183", "v184", "v185", "v186", "v187", "v188", "v189", "v190", "v191", "v192", "v193", "v194", "v195", "v196", "v197", "v198", "v199", "v200", "v201", "v202", "v203", "v204", "v205", "v206", "v207", "v208", "v209", "v210", "v211", "v212", "v213", "v214", "v215", "v216", "v217", "v218", "v219", "v220", "v221", "v222", "v223", "v224", "v225", "v226", "v227", "v228", "v229", "v230", "v231", "a0", "a1", "a2", "a3", "a4", "a5", "a6", "a7", "a8", "a9", "a10", "a11", "a12", "a13", "a14", "a15", "a16", "a17", "a18", "a19", "a20", "a21", "a22", "a23", "a24", "a25", "a26", "a27", "a28", "a29", "a30", "a31", "a32", "a33", "a34", "a35", "a36", "a37", "a38", "a39", "a40", "a41", "a42", "a43", "a44", "a45", "a46", "a47", "a48", "a49", "a50", "a51", "a52", "a53", "a54", "a55", "a56", "a57", "a58", "a59", "a60", "a61", "a62", "a63", "a64", "a65", "a66", "a67", "a68", "a69", "a70", "a71", "a72", "a73", "a74", "a75", "a76", "a77", "a78", "a79", "a80", "a81", "a82", "a83", "a84", "a85", "a86", "a87", "a88", "a89", "a90", "a91", "a92", "a93", "a94", "a95", "a96", "a97", "a98", "a99", "a100", "a101", "a102", "a103", "a104", "a105", "a106", "a107", "a108", "a109", "a110", "a111", "a112", "a113", "a114", "a115", "a116", "a117", "a118", "a119", "a120", "a121", "a122", "a123", "a124", "a125", "a126", "a127", "a128", "a129", "a130", "a131", "a132", "a133", "a134", "a135", "a136", "a137", "a138", "a139", "a140", "a141", "a142", "a143", "a144", "a145", "a146", "a147", "a148", "a149", "a150", "a151", "a152", "a153", "a154", "a155", "a156", "a157", "a158", "a159", "a160", "a161", "a162", "a163", "a164", "a165", "a166", "a167", "a168", "a169", "a170", "a171", "a172", "a173", "a174", "a175", "a176", "a177", "a178", "a179", "a180", "a181", "a182", "a183", "a184", "a185", "a186", "a187", "a188", "a189", "a190", "a191", "a192", "a193", "a194", "a195", "a196", "a197", "a198", "a199", "a200", "a201", "a202", "a203", "a204", "a205", "a206", "a207", "a208", "a209", "a210", "a211", "a212", "a213", "a214", "a215", "a216", "a217", "a218", "a219", "a220", "a221", "a222", "a223", "a224", "a225", "a226", "a227", "a228", "a229", "a230", "a231", "a232", "a233", "a234", "a235", "a236", "a237", "a238", "a239", "a240", "a241", "a242", "a243", "a244", "a245", "a246", "a247", "a248", "a249", "a250", "a251", "a252", "a253", "a254", "a255", "s36", "s37", "s38", "s39", "s40", "s41", "s42", "s43", "s44", "s45", "s46", "s47", "s48", "s49", "s50", "s51", "s52", "s53", "s54", "s55", "s56", "s57", "s58", "s59", "s60", "s61", "s62", "s63", "s64", "s65", "s66", "s67", "s68", "s69", "s70", "s71", "s72", "s73", "s74", "s75", "s76", "s77", "s78", "s79", "s80", "s81", "s82", "s83", "s84", "s85", "s86", "s87", "s88", "s89", "s90", "s91", "s92", "s93", "s94", "s95", "s96", "s97", "vcc", "scc", "memory");
    if (RHELP && slice < 16) { kind = 4; rep = 5; kh = slice >> 3; slice &= 7; }
  }
  CTS(9);
#define COMMON(NF, FOFF, FSTR, SNAP, LDW, NL, TOFF)                                                                                            \
  LANE_ID(ln);                                                                                                                       \
  const int l15 = ln & 15, lg = ln >> 4, lq = l15 >> 2, lp = ln & 3, rr = 8 * lg + lq, fj = ln & 31;                                 \
  const unsigned vL16 = ln * 16, vFlag = (ln < 32 && wave == 0) ? ln * 4 : OOB;                                                      \
  const unsigned vSnapOff = (wave == 0 && fj < (NF)) ? (FOFF) + (ln >> 5) * sStep * (FSTR) : OOB;                                    \
  const unsigned vSnapRd = (SNAP) + (fj < (NF) ? ln : ln & 32) * 4;                                                                  \
  const unsigned vWd0 = (ln >> 3) * (LDW) + ((ln & 7) ^ swzB(ln >> 3)) * 16, vWd1 = ((ln >> 3) + 8) * (LDW) + ((ln & 7) ^ swzB(8 + (ln >> 3))) * 16; \
  const unsigned vWr0 = (wave >> 1) * 8192 + rr * 128 + (((wave & 1) * 4 + lp) ^ swzB(rr)) * 16, vWr1 = vWr0 + 8;                    \
  const unsigned vWr2 = 16384 + rr * 128 + ((2 * wave + (lp >> 1)) ^ swzB(rr)) * 16 + (lp & 1) * 8;                                  \
  const unsigned sWave = wave, sRk = kind == 4 ? rep : t_start, sG0 = rep < 5 ? rep : rep == 5 ? U0 + 5 : U1 + rep, sDbg = sDbg0 + (rep < 5 ? 0 : 2048)
  if (kind == 3 || kind == 5) {
    const unsigned sStep = kind == 3 ? 5 : G3OR;
    COMMON(8, fj * 128, 1024, 131072, 7168, 256, 0);
    const unsigned vOutS = (lg & 1) * 8 + (((wave & 1) * 2 + (lg >> 1)) * 16 + l15) * 16 + (4 + (wave >> 1) - wave) * 1024;
    const unsigned vAux0 = (ln & 7) * 128 + (4 * wave + lg) * 8 + ((ln >> 3) & 1) * 4;
    const float k1 = -2.0f * 0.7978845608028654f * 1.4426950408889634f;
    const unsigned vAux1 = 139264 + (l15 < 4 ? (4 * wave + lg) * 16 + l15 * 4 : 512 + ln * 4), vAux2 = 139264 + l15 * 16;
    const unsigned vAux3 = l15 == 2 ? __float_as_uint(k1 * 0.044715f) : 0u, vAn = l15 == 3 ? 0x3f800000u : 0u, vAe = l15 == 1 ? __float_as_uint(k1) : 0u;
    const unsigned sOutOff = OFF_G + (slice * 6 + wave) * 1024, sFoutOff = OFF_FG + slice * 128, sAuxOff = 0, sWw = 16 * wave * 7168 + slice * 384, sBuf = 0;
    const unsigned sExp = __builtin_amdgcn_readfirstlane(ep);
    if (kind == 3) { asm volatile(
R"(.macro M d,w,x
v_mfma_f32_16x16x32_bf16 v[\d:\d+3], \w, v[\x:\x+3], v[\d:\d+3]
.endm
.macro R d,a,o
ds_read_b128 v[\d:\d+3], v\a offset:\o
.endm
.macro T d,a,o
ds_read_b64_tr_b16 \d, v\a offset:\o
.endm
.macro D a,s,o
buffer_load_dwordx4 v\a, s[36:39], s\s offen offset:\o sc1 lds
.endm
.macro L c
s_waitcnt lgkmcnt(\c)
.endm
Lcode_g3_b:
s_mov_b64 s[36:37],%0
s_mov_b32 s38,111243392
s_mov_b32 s39,0x00020000
s_mov_b32 s44,%1
s_mov_b32 s45,%2
s_mov_b32 s46,%3
s_mov_b32 s47,%4
s_mov_b32 s48,%5
s_mov_b32 s49,%6
s_mov_b32 s50,%7
s_mov_b32 s51,%8
s_mov_b32 s53,%9
v_mov_b32 v0,%10
v_mov_b32 v3,%11
v_mov_b32 v5,%12
v_mov_b32 v6,%13
v_mov_b32 v18,%14
v_mov_b32 v19,%15
v_mov_b32 v20,%16
v_mov_b32 v21,%17
v_mov_b32 v22,%18
v_mov_b32 v10,%19
v_mov_b32 v11,%20
v_mov_b32 v12,%21
v_mov_b32 v13,%22
v_mov_b32 v14,%23
v_mov_b32 v15,%24
v_mov_b32 v16,%25
s_mov_b64 s[40:41],%26
s_mov_b32 s42,7340032
s_mov_b32 s43,0x00020000
s_lshl_b32 s57,s44,11
s_cmp_ge_u32 s45,8
s_cbranch_scc1 Lg3_dly_%=
s_sleep 16
Lg3_dly_%=:
s_add_u32 s55,s50,0
s_add_u32 m0,s57,0
s_nop 0
buffer_load_dwordx4 v18,s[40:43],s55 offen offset:0  lds
s_add_u32 m0,s57,1024
s_nop 0
buffer_load_dwordx4 v19,s[40:43],s55 offen offset:0  lds
s_add_u32 m0,s57,8064
s_nop 0
buffer_load_dwordx4 v18,s[40:43],s55 offen offset:128  lds
s_add_u32 m0,s57,9088
s_nop 0
buffer_load_dwordx4 v19,s[40:43],s55 offen offset:128  lds
s_add_u32 m0,s57,16128
s_nop 0
buffer_load_dwordx4 v18,s[40:43],s55 offen offset:256  lds
s_add_u32 m0,s57,17152
s_nop 0
buffer_load_dwordx4 v19,s[40:43],s55 offen offset:256  lds
s_add_u32 s55,s50,458752
s_add_u32 m0,s57,24576
s_nop 0
buffer_load_dwordx4 v18,s[40:43],s55 offen offset:0  lds
s_add_u32 m0,s57,25600
s_nop 0
buffer_load_dwordx4 v19,s[40:43],s55 offen offset:0  lds
s_add_u32 m0,s57,32640
s_nop 0
buffer_load_dwordx4 v18,s[40:43],s55 offen offset:128  lds
s_add_u32 m0,s57,33664
s_nop 0
buffer_load_dwordx4 v19,s[40:43],s55 offen offset:128  lds
s_add_u32 m0,s57,40704
s_nop 0
buffer_load_dwordx4 v18,s[40:43],s55 offen offset:256  lds
s_add_u32 m0,s57,41728
s_nop 0
buffer_load_dwordx4 v19,s[40:43],s55 offen offset:256  lds
s_add_u32 s55,s50,917504
s_add_u32 m0,s57,49152
s_nop 0
buffer_load_dwordx4 v18,s[40:43],s55 offen offset:0  lds
s_add_u32 m0,s57,50176
s_nop 0
buffer_load_dwordx4 v19,s[40:43],s55 offen offset:0  lds
s_add_u32 m0,s57,57216
s_nop 0
buffer_load_dwordx4 v18,s[40:43],s55 offen offset:128  lds
s_add_u32 m0,s57,58240
s_nop 0
buffer_load_dwordx4 v19,s[40:43],s55 offen offset:128  lds
s_add_u32 m0,s57,65280
s_nop 0
buffer_load_dwordx4 v18,s[40:43],s55 offen offset:256  lds
s_add_u32 m0,s57,66304
s_nop 0
buffer_load_dwordx4 v19,s[40:43],s55 offen offset:256  lds
s_add_u32 s55,s50,1376256
s_add_u32 m0,s57,73728
s_nop 0
buffer_load_dwordx4 v18,s[40:43],s55 offen offset:0  lds
s_add_u32 m0,s57,74752
s_nop 0
buffer_load_dwordx4 v19,s[40:43],s55 offen offset:0  lds
s_add_u32 m0,s57,81792
s_nop 0
buffer_load_dwordx4 v18,s[40:43],s55 offen offset:128  lds
s_add_u32 m0,s57,82816
s_nop 0
buffer_load_dwordx4 v19,s[40:43],s55 offen offset:128  lds
s_add_u32 m0,s57,89856
s_nop 0
buffer_load_dwordx4 v18,s[40:43],s55 offen offset:256  lds
s_add_u32 m0,s57,90880
s_nop 0
buffer_load_dwordx4 v19,s[40:43],s55 offen offset:256  lds
s_add_u32 s55,s50,1835008
s_add_u32 m0,s57,98304
s_nop 0
buffer_load_dwordx4 v18,s[40:43],s55 offen offset:0  lds
s_add_u32 m0,s57,99328
s_nop 0
buffer_load_dwordx4 v19,s[40:43],s55 offen offset:0  lds
s_add_u32 m0,s57,106368
s_nop 0
buffer_load_dwordx4 v18,s[40:43],s55 offen offset:128  lds
s_add_u32 m0,s57,107392
s_nop 0
buffer_load_dwordx4 v19,s[40:43],s55 offen offset:128  lds
s_add_u32 m0,s57,114432
s_nop 0
buffer_load_dwordx4 v18,s[40:43],s55 offen offset:256  lds
s_add_u32 m0,s57,115456
s_nop 0
buffer_load_dwordx4 v19,s[40:43],s55 offen offset:256  lds
s_waitcnt vmcnt(24) lgkmcnt(0)
s_barrier
s_add_u32 s55,s50,2293760
s_add_u32 m0,s57,122880
s_nop 0
buffer_load_dwordx4 v18,s[40:43],s55 offen offset:0  lds
s_add_u32 m0,s57,123904
s_nop 0
buffer_load_dwordx4 v19,s[40:43],s55 offen offset:0  lds
s_add_u32 m0,s57,130944
s_nop 0
buffer_load_dwordx4 v18,s[40:43],s55 offen offset:128  lds
s_add_u32 m0,s57,131968
s_nop 0
buffer_load_dwordx4 v19,s[40:43],s55 offen offset:128  lds
s_add_u32 m0,s57,139008
s_nop 0
buffer_load_dwordx4 v18,s[40:43],s55 offen offset:256  lds
s_add_u32 m0,s57,140032
s_nop 0
buffer_load_dwordx4 v19,s[40:43],s55 offen offset:256  lds
v_add_u32 v26,0,v20
v_add_u32 v27,0,v21
v_add_u32 v28,0,v22
T a[0:1],26,0
T a[2:3],26,512
T a[4:5],27,0
T a[6:7],27,512
T a[8:9],28,0
T a[10:11],28,512
T a[12:13],26,4096
T a[14:15],26,4608
T a[16:17],27,4096
T a[18:19],27,4608
T a[20:21],28,4096
T a[22:23],28,4608
s_waitcnt vmcnt(24) lgkmcnt(0)
s_barrier
s_add_u32 s55,s50,2752512
s_add_u32 m0,s57,0
s_nop 0
buffer_load_dwordx4 v18,s[40:43],s55 offen offset:0  lds
s_add_u32 m0,s57,1024
s_nop 0
buffer_load_dwordx4 v19,s[40:43],s55 offen offset:0  lds
s_add_u32 m0,s57,8064
s_nop 0
buffer_load_dwordx4 v18,s[40:43],s55 offen offset:128  lds
s_add_u32 m0,s57,9088
s_nop 0
buffer_load_dwordx4 v19,s[40:43],s55 offen offset:128  lds
s_add_u32 m0,s57,16128
s_nop 0
buffer_load_dwordx4 v18,s[40:43],s55 offen offset:256  lds
s_add_u32 m0,s57,17152
s_nop 0
buffer_load_dwordx4 v19,s[40:43],s55 offen offset:256  lds
v_add_u32 v26,24576,v20
v_add_u32 v27,24576,v21
v_add_u32 v28,24576,v22
T a[24:25],26,0
T a[26:27],26,512
T a[28:29],27,0
T a[30:31],27,512
T a[32:33],28,0
T a[34:35],28,512
T a[36:37],26,4096
T a[38:39],26,4608
T a[40:41],27,4096
T a[42:43],27,4608
T a[44:45],28,4096
T a[46:47],28,4608
s_waitcnt vmcnt(24) lgkmcnt(0)
s_barrier
s_add_u32 s55,s50,3211264
s_add_u32 m0,s57,24576
s_nop 0
buffer_load_dwordx4 v18,s[40:43],s55 offen offset:0  lds
s_add_u32 m0,s57,25600
s_nop 0
buffer_load_dwordx4 v19,s[40:43],s55 offen offset:0  lds
s_add_u32 m0,s57,32640
s_nop 0
buffer_load_dwordx4 v18,s[40:43],s55 offen offset:128  lds
s_add_u32 m0,s57,33664
s_nop 0
buffer_load_dwordx4 v19,s[40:43],s55 offen offset:128  lds
s_add_u32 m0,s57,40704
s_nop 0
buffer_load_dwordx4 v18,s[40:43],s55 offen offset:256  lds
s_add_u32 m0,s57,41728
s_nop 0
buffer_load_dwordx4 v19,s[40:43],s55 offen offset:256  lds
v_add_u32 v26,49152,v20
v_add_u32 v27,49152,v21
v_add_u32 v28,49152,v22
T a[48:49],26,0
T a[50:51],26,512
T a[52:53],27,0
T a[54:55],27,512
T a[56:57],28,0
T a[58:59],28,512
T a[60:61],26,4096
T a[62:63],26,4608
T a[64:65],27,4096
T a[66:67],27,4608
T a[68:69],28,4096
T a[70:71],28,4608
s_waitcnt vmcnt(24) lgkmcnt(0)
s_barrier
s_add_u32 s55,s50,3670016
s_add_u32 m0,s57,49152
s_nop 0
buffer_load_dwordx4 v18,s[40:43],s55 offen offset:0  lds
s_add_u32 m0,s57,50176
s_nop 0
buffer_load_dwordx4 v19,s[40:43],s55 offen offset:0  lds
s_add_u32 m0,s57,57216
s_nop 0
buffer_load_dwordx4 v18,s[40:43],s55 offen offset:128  lds
s_add_u32 m0,s57,58240
s_nop 0
buffer_load_dwordx4 v19,s[40:43],s55 offen offset:128  lds
s_add_u32 m0,s57,65280
s_nop 0
buffer_load_dwordx4 v18,s[40:43],s55 offen offset:256  lds
s_add_u32 m0,s57,66304
s_nop 0
buffer_load_dwordx4 v19,s[40:43],s55 offen offset:256  lds
v_add_u32 v26,73728,v20
v_add_u32 v27,73728,v21
v_add_u32 v28,73728,v22
T a[72:73],26,0
T a[74:75],26,512
T a[76:77],27,0
T a[78:79],27,512
T a[80:81],28,0
T a[82:83],28,512
T a[84:85],26,4096
T a[86:87],26,4608
T a[88:89],27,4096
T a[90:91],27,4608
T a[92:93],28,4096
T a[94:95],28,4608
s_waitcnt vmcnt(24) lgkmcnt(0)
s_barrier
s_add_u32 s55,s50,4128768
s_add_u32 m0,s57,73728
s_nop 0
buffer_load_dwordx4 v18,s[40:43],s55 offen offset:0  lds
s_add_u32 m0,s57,74752
s_nop 0
buffer_load_dwordx4 v19,s[40:43],s55 offen offset:0  lds
s_add_u32 m0,s57,81792
s_nop 0
buffer_load_dwordx4 v18,s[40:43],s55 offen offset:128  lds
s_add_u32 m0,s57,82816
s_nop 0
buffer_load_dwordx4 v19,s[40:43],s55 offen offset:128  lds
s_add_u32 m0,s57,89856
s_nop 0
buffer_load_dwordx4 v18,s[40:43],s55 offen offset:256  lds
s_add_u32 m0,s57,90880
s_nop 0
buffer_load_dwordx4 v19,s[40:43],s55 offen offset:256  lds
v_add_u32 v26,98304,v20
v_add_u32 v27,98304,v21
v_add_u32 v28,98304,v22
T a[96:97],26,0
T a[98:99],26,512
T a[100:101],27,0
T a[102:103],27,512
T a[104:105],28,0
T a[106:107],28,512
T a[108:109],26,4096
T a[110:111],26,4608
T a[112:113],27,4096
T a[114:115],27,4608
T a[116:117],28,4096
T a[118:119],28,4608
s_waitcnt vmcnt(24) lgkmcnt(0)
s_barrier
s_add_u32 s55,s50,4587520
s_add_u32 m0,s57,98304
s_nop 0
buffer_load_dwordx4 v18,s[40:43],s55 offen offset:0  lds
s_add_u32 m0,s57,99328
s_nop 0
buffer_load_dwordx4 v19,s[40:43],s55 offen offset:0  lds
s_add_u32 m0,s57,106368
s_nop 0
buffer_load_dwordx4 v18,s[40:43],s55 offen offset:128  lds
s_add_u32 m0,s57,107392
s_nop 0
buffer_load_dwordx4 v19,s[40:43],s55 offen offset:128  lds
s_add_u32 m0,s57,114432
s_nop 0
buffer_load_dwordx4 v18,s[40:43],s55 offen offset:256  lds
s_add_u32 m0,s57,115456
s_nop 0
buffer_load_dwordx4 v19,s[40:43],s55 offen offset:256  lds
v_add_u32 v26,122880,v20
v_add_u32 v27,122880,v21
v_add_u32 v28,122880,v22
T a[120:121],26,0
T a[122:123],26,512
T a[124:125],27,0
T a[126:127],27,512
T a[128:129],28,0
T a[130:131],28,512
T a[132:133],26,4096
T a[134:135],26,4608
T a[136:137],27,4096
T a[138:139],27,4608
T a[140:141],28,4096
T a[142:143],28,4608
s_waitcnt vmcnt(24) lgkmcnt(0)
s_barrier
s_add_u32 s55,s50,5046272
s_add_u32 m0,s57,122880
s_nop 0
buffer_load_dwordx4 v18,s[40:43],s55 offen offset:0  lds
s_add_u32 m0,s57,123904
s_nop 0
buffer_load_dwordx4 v19,s[40:43],s55 offen offset:0  lds
s_add_u32 m0,s57,130944
s_nop 0
buffer_load_dwordx4 v18,s[40:43],s55 offen offset:128  lds
s_add_u32 m0,s57,131968
s_nop 0
buffer_load_dwordx4 v19,s[40:43],s55 offen offset:128  lds
s_add_u32 m0,s57,139008
s_nop 0
buffer_load_dwordx4 v18,s[40:43],s55 offen offset:256  lds
s_add_u32 m0,s57,140032
s_nop 0
buffer_load_dwordx4 v19,s[40:43],s55 offen offset:256  lds
v_add_u32 v26,0,v20
v_add_u32 v27,0,v21
v_add_u32 v28,0,v22
T a[144:145],26,0
T a[146:147],26,512
T a[148:149],27,0
T a[150:151],27,512
T a[152:153],28,0
T a[154:155],28,512
T a[156:157],26,4096
T a[158:159],26,4608
T a[160:161],27,4096
T a[162:163],27,4608
T a[164:165],28,4096
T a[166:167],28,4608
s_waitcnt vmcnt(24) lgkmcnt(0)
s_barrier
s_add_u32 s55,s50,5505024
s_add_u32 m0,s57,0
s_nop 0
buffer_load_dwordx4 v18,s[40:43],s55 offen offset:0  lds
s_add_u32 m0,s57,1024
s_nop 0
buffer_load_dwordx4 v19,s[40:43],s55 offen offset:0  lds
s_add_u32 m0,s57,8064
s_nop 0
buffer_load_dwordx4 v18,s[40:43],s55 offen offset:128  lds
s_add_u32 m0,s57,9088
s_nop 0
buffer_load_dwordx4 v19,s[40:43],s55 offen offset:128  lds
s_add_u32 m0,s57,16128
s_nop 0
buffer_load_dwordx4 v18,s[40:43],s55 offen offset:256  lds
s_add_u32 m0,s57,17152
s_nop 0
buffer_load_dwordx4 v19,s[40:43],s55 offen offset:256  lds
v_add_u32 v26,24576,v20
v_add_u32 v27,24576,v21
v_add_u32 v28,24576,v22
T a[168:169],26,0
T a[170:171],26,512
T a[172:173],27,0
T a[174:175],27,512
T a[176:177],28,0
T a[178:179],28,512
T a[180:181],26,4096
T a[182:183],26,4608
T a[184:185],27,4096
T a[186:187],27,4608
T a[188:189],28,4096
T a[190:191],28,4608
s_waitcnt vmcnt(24) lgkmcnt(0)
s_barrier
s_add_u32 s55,s50,5963776
s_add_u32 m0,s57,24576
s_nop 0
buffer_load_dwordx4 v18,s[40:43],s55 offen offset:0  lds
s_add_u32 m0,s57,25600
s_nop 0
buffer_load_dwordx4 v19,s[40:43],s55 offen offset:0  lds
s_add_u32 m0,s57,32640
s_nop 0
buffer_load_dwordx4 v18,s[40:43],s55 offen offset:128  lds
s_add_u32 m0,s57,33664
s_nop 0
buffer_load_dwordx4 v19,s[40:43],s55 offen offset:128  lds
s_add_u32 m0,s57,40704
s_nop 0
buffer_load_dwordx4 v18,s[40:43],s55 offen offset:256  lds
s_add_u32 m0,s57,41728
s_nop 0
buffer_load_dwordx4 v19,s[40:43],s55 offen offset:256  lds
v_add_u32 v26,49152,v20
v_add_u32 v27,49152,v21
v_add_u32 v28,49152,v22
T a[192:193],26,0
T a[194:195],26,512
T a[196:197],27,0
T a[198:199],27,512
T a[200:201],28,0
T a[202:203],28,512
T a[204:205],26,4096
T a[206:207],26,4608
T a[208:209],27,4096
T a[210:211],27,4608
T a[212:213],28,4096
T a[214:215],28,4608
s_waitcnt vmcnt(24) lgkmcnt(0)
s_barrier
s_add_u32 s55,s50,6422528
s_add_u32 m0,s57,49152
s_nop 0
buffer_load_dwordx4 v18,s[40:43],s55 offen offset:0  lds
s_add_u32 m0,s57,50176
s_nop 0
buffer_load_dwordx4 v19,s[40:43],s55 offen offset:0  lds
s_add_u32 m0,s57,57216
s_nop 0
buffer_load_dwordx4 v18,s[40:43],s55 offen offset:128  lds
s_add_u32 m0,s57,58240
s_nop 0
buffer_load_dwordx4 v19,s[40:43],s55 offen offset:128  lds
s_add_u32 m0,s57,65280
s_nop 0
buffer_load_dwordx4 v18,s[40:43],s55 offen offset:256  lds
s_add_u32 m0,s57,66304
s_nop 0
buffer_load_dwordx4 v19,s[40:43],s55 offen offset:256  lds
v_add_u32 v26,73728,v20
v_add_u32 v27,73728,v21
v_add_u32 v28,73728,v22
T a[216:217],26,0
T a[218:219],26,512
T a[220:221],27,0
T a[222:223],27,512
T a[224:225],28,0
)"
R"(T a[226:227],28,512
T a[228:229],26,4096
T a[230:231],26,4608
T a[232:233],27,4096
T a[234:235],27,4608
T a[236:237],28,4096
T a[238:239],28,4608
s_waitcnt vmcnt(24) lgkmcnt(0)
s_barrier
s_add_u32 s55,s50,6881280
s_add_u32 m0,s57,73728
s_nop 0
buffer_load_dwordx4 v18,s[40:43],s55 offen offset:0  lds
s_add_u32 m0,s57,74752
s_nop 0
buffer_load_dwordx4 v19,s[40:43],s55 offen offset:0  lds
s_add_u32 m0,s57,81792
s_nop 0
buffer_load_dwordx4 v18,s[40:43],s55 offen offset:128  lds
s_add_u32 m0,s57,82816
s_nop 0
buffer_load_dwordx4 v19,s[40:43],s55 offen offset:128  lds
s_add_u32 m0,s57,89856
s_nop 0
buffer_load_dwordx4 v18,s[40:43],s55 offen offset:256  lds
s_add_u32 m0,s57,90880
s_nop 0
buffer_load_dwordx4 v19,s[40:43],s55 offen offset:256  lds
v_add_u32 v26,98304,v20
v_add_u32 v27,98304,v21
v_add_u32 v28,98304,v22
T a[240:241],26,0
T a[242:243],26,512
T a[244:245],27,0
T a[246:247],27,512
T a[248:249],28,0
T a[250:251],28,512
T a[252:253],26,4096
T a[254:255],26,4608
T v[128:129],27,4096
T v[130:131],27,4608
T v[132:133],28,4096
T v[134:135],28,4608
s_waitcnt vmcnt(24) lgkmcnt(0)
s_barrier
v_add_u32 v26,122880,v20
v_add_u32 v27,122880,v21
v_add_u32 v28,122880,v22
T v[136:137],26,0
T v[138:139],26,512
T v[140:141],27,0
T v[142:143],27,512
T v[144:145],28,0
T v[146:147],28,512
T v[148:149],26,4096
T v[150:151],26,4608
T v[152:153],27,4096
T v[154:155],27,4608
T v[156:157],28,4096
T v[158:159],28,4608
s_waitcnt vmcnt(18) lgkmcnt(0)
s_barrier
v_add_u32 v26,0,v20
v_add_u32 v27,0,v21
v_add_u32 v28,0,v22
T v[160:161],26,0
T v[162:163],26,512
T v[164:165],27,0
T v[166:167],27,512
T v[168:169],28,0
T v[170:171],28,512
T v[172:173],26,4096
T v[174:175],26,4608
T v[176:177],27,4096
T v[178:179],27,4608
T v[180:181],28,4096
T v[182:183],28,4608
s_waitcnt vmcnt(12) lgkmcnt(0)
s_barrier
v_add_u32 v26,24576,v20
v_add_u32 v27,24576,v21
v_add_u32 v28,24576,v22
T v[184:185],26,0
T v[186:187],26,512
T v[188:189],27,0
T v[190:191],27,512
T v[192:193],28,0
T v[194:195],28,512
T v[196:197],26,4096
T v[198:199],26,4608
T v[200:201],27,4096
T v[202:203],27,4608
T v[204:205],28,4096
T v[206:207],28,4608
s_waitcnt vmcnt(6) lgkmcnt(0)
s_barrier
v_add_u32 v26,49152,v20
v_add_u32 v27,49152,v21
v_add_u32 v28,49152,v22
T v[208:209],26,0
T v[210:211],26,512
T v[212:213],27,0
T v[214:215],27,512
T v[216:217],28,0
T v[218:219],28,512
T v[220:221],26,4096
T v[222:223],26,4608
T v[224:225],27,4096
T v[226:227],27,4608
T v[228:229],28,4096
T v[230:231],28,4608
s_waitcnt vmcnt(0) lgkmcnt(0)
s_barrier
v_add_u32 v26,73728,v20
v_add_u32 v27,73728,v21
v_add_u32 v28,73728,v22
T v[232:233],26,0
T v[234:235],26,512
T v[236:237],27,0
T v[238:239],27,512
T v[240:241],28,0
T v[242:243],28,512
T v[244:245],26,4096
T v[246:247],26,4608
T v[248:249],27,4096
T v[250:251],27,4608
T v[252:253],28,4096
T v[254:255],28,4608
L 0
s_barrier
v_mov_b32 v42,0x3f803f80
v_mov_b32 v43,0x3f803f80
v_mov_b32 v44,0x3f803f80
v_mov_b32 v45,0x3f803f80
s_nop 1
v_mfma_f32_16x16x32_bf16 v[82:85],a[0:3],v[42:45],0
v_mfma_f32_16x16x32_bf16 v[86:89],a[4:7],v[42:45],0
v_mfma_f32_16x16x32_bf16 v[90:93],a[8:11],v[42:45],0
M 82,a[12:15],42
M 86,a[16:19],42
M 90,a[20:23],42
M 82,a[24:27],42
M 86,a[28:31],42
M 90,a[32:35],42
M 82,a[36:39],42
M 86,a[40:43],42
M 90,a[44:47],42
M 82,a[48:51],42
M 86,a[52:55],42
M 90,a[56:59],42
M 82,a[60:63],42
M 86,a[64:67],42
M 90,a[68:71],42
M 82,a[72:75],42
M 86,a[76:79],42
M 90,a[80:83],42
M 82,a[84:87],42
M 86,a[88:91],42
M 90,a[92:95],42
M 82,a[96:99],42
M 86,a[100:103],42
M 90,a[104:107],42
M 82,a[108:111],42
M 86,a[112:115],42
M 90,a[116:119],42
M 82,a[120:123],42
M 86,a[124:127],42
M 90,a[128:131],42
M 82,a[132:135],42
M 86,a[136:139],42
M 90,a[140:143],42
M 82,a[144:147],42
M 86,a[148:151],42
M 90,a[152:155],42
M 82,a[156:159],42
M 86,a[160:163],42
M 90,a[164:167],42
M 82,a[168:171],42
M 86,a[172:175],42
M 90,a[176:179],42
M 82,a[180:183],42
M 86,a[184:187],42
M 90,a[188:191],42
M 82,a[192:195],42
M 86,a[196:199],42
M 90,a[200:203],42
M 82,a[204:207],42
M 86,a[208:211],42
M 90,a[212:215],42
M 82,a[216:219],42
M 86,a[220:223],42
M 90,a[224:227],42
M 82,a[228:231],42
M 86,a[232:235],42
M 90,a[236:239],42
M 82,a[240:243],42
M 86,a[244:247],42
M 90,a[248:251],42
M 82,a[252:255],42
M 86,v[128:131],42
M 90,v[132:135],42
M 82,v[136:139],42
M 86,v[140:143],42
M 90,v[144:147],42
M 82,v[148:151],42
M 86,v[152:155],42
M 90,v[156:159],42
M 82,v[160:163],42
M 86,v[164:167],42
M 90,v[168:171],42
M 82,v[172:175],42
M 86,v[176:179],42
M 90,v[180:183],42
M 82,v[184:187],42
M 86,v[188:191],42
M 90,v[192:195],42
M 82,v[196:199],42
M 86,v[200:203],42
M 90,v[204:207],42
M 82,v[208:211],42
M 86,v[212:215],42
M 90,v[216:219],42
M 82,v[220:223],42
M 86,v[224:227],42
M 90,v[228:231],42
M 82,v[232:235],42
M 86,v[236:239],42
M 90,v[240:243],42
M 82,v[244:247],42
M 86,v[248:251],42
M 90,v[252:255],42
v_mov_b32 v4,s53
s_mov_b32 s82,0
s_mov_b32 s80,0
s_mov_b32 s54,s45
s_mul_i32 s57,s44,4096
s_mul_i32 s55,s45,32768
s_add_u32 s55,s55,s57
s_add_u32 s55,s55,31457280
s_mul_i32 s57,s44,4096
s_mul_i32 s60,s46,32768
s_mul_i32 s61,s46,1024
s_mul_i32 s62,s46,114688
s_mul_i32 s63,s46,2560
s_mul_i32 s64,s46,1024
s_lshl_b32 s59,s44,8
s_add_u32 s59,s59,131072
s_mul_i32 s58,s45,1024
s_add_u32 s58,s58,109613056
s_mul_i32 s65,s45,114688
s_add_u32 s65,s65,s47
s_mul_i32 s68,s45,2560
s_add_u32 s68,s68,s48
s_mul_i32 s73,s45,1024
s_add_u32 s73,s73,107479040
s_mul_i32 s74,s45,32768
s_add_u32 s74,s74,s49
s_mul_i32 s75,s45,1024
s_add_u32 s75,s75,107479040
s_mov_b32 s66,0x7fff0000
s_mov_b32 s67,0x7fff0000
s_mov_b32 s69,0x7fff0000
s_mov_b32 s76,0x7fff0000
s_mov_b32 s77,0x7fff0000
s_mov_b32 s70,0x7fff0000
s_mov_b32 s71,0x7fff0000
s_mov_b32 s72,0x7fff0000
v_mov_b32 v1,v0
v_add_u32 v2,16384,v0
s_mov_b32 s84,16384
s_mov_b32 s86,0x3a800000
s_mov_b32 s87,0x3727c5ac
s_mov_b32 s88,0xc0135761
s_mov_b32 s89,0x3d372713
s_lshr_b32 s85,s44,1
s_mul_i32 s85,s85,127
v_lshlrev_b32 v8,22,v0
v_and_b32 v8,0x80000000,v8
v_or_b32 v8,v8,v5
v_add_u32 v2,32768,v0
s_lshl_b32 s84,s57,1
s_add_u32 s84,s84,32768
v_and_b32 v94,240,v0
v_cmp_eq_u32 vcc,0,v94
s_nop 1
v_cndmask_b32 v17,0,1.0,vcc
s_getpc_b64 s[40:41]
Lg3_pc_%=:
s_lshl_b32 s78,s44,13
v_lshlrev_b32 v94,3,v0
v_add_u32 v94,s78,v94
v_min_u32 v94,Lcode_g3_e-Lg3_pc_%=-4,v94
s_mov_b32 m0,163584
s_nop 0
global_load_lds_dword v94,s[40:41]
s_add_u32 m0,s59,4096
s_nop 0
buffer_load_dword v8,s[36:39],s58 offen offset:0 sc1 lds
buffer_store_dword v4,v3,s[36:39],s71 offen offset:0 sc0 sc1
s_add_u32 m0,s59,5120
s_nop 0
buffer_load_dword v8,s[36:39],s58 offen offset:0 sc1 lds
buffer_store_dword v4,v3,s[36:39],s71 offen offset:0 sc0 sc1
s_mov_b32 s83,0
Lg3_poll_%=:
s_add_u32 s78,s83,2
s_and_b32 s78,s78,3
s_lshl_b32 s78,s78,10
s_add_u32 s78,s78,4096
s_add_u32 m0,s59,s78
s_nop 0
buffer_load_dword v8,s[36:39],s58 offen offset:0 sc1 lds
s_and_b32 s78,s83,3
s_lshl_b32 s78,s78,10
v_add_u32 v94,s78,v6
s_waitcnt vmcnt(4)
s_barrier
ds_read_b32 v7,v94 offset:4096
buffer_store_dword v4,v3,s[36:39],s70 offen offset:0 sc0 sc1
s_mov_b32 s70,s71
s_mov_b32 s71,s72
s_mov_b32 s72,0x7fff0000
s_add_u32 s83,s83,1
s_add_u32 s82,s82,1
L 0
v_cmp_eq_u32 vcc,s53,v7
s_nop 0
s_cmp_eq_u32 vcc_lo,-1
s_cbranch_scc1 Lg3_go_%=
s_cmp_lt_u32 s82,20000
s_cbranch_scc1 Lg3_poll_%=
Lg3_go_%=:
buffer_load_dword v114,v11,s[36:39],s73 offen offset:0 sc1
s_add_u32 s73,s73,s64
s_add_u32 m0,s57,0
s_add_u32 s56,s55,0
D 0,56,0
D 0,56,1024
D 0,56,2048
D 0,56,3072
s_add_u32 s58,s58,s61
s_add_u32 m0,s59,2048
s_nop 0
buffer_load_dword v8,s[36:39],s58 offen offset:0 sc1 lds
s_add_u32 m0,s57,16384
s_add_u32 s56,s55,16384
D 0,56,0
D 0,56,1024
D 0,56,2048
D 0,56,3072
s_add_u32 s55,s55,s60
s_sub_u32 s57,s84,s57
s_add_u32 s54,s54,s46
s_waitcnt vmcnt(4)
s_barrier
buffer_store_dword v4,v3,s[36:39],s70 offen offset:0 sc0 sc1
s_min_u32 s71,s71,s72
buffer_store_dword v4,v3,s[36:39],s71 offen offset:0 sc0 sc1
s_mov_b32 s70,0x7fff0000
s_mov_b32 s71,0x7fff0000
s_mov_b32 s72,0x7fff0000
s_cmp_eq_u32 s80,1
buffer_store_dword v4,v3,s[36:39],s71 offen offset:0 sc0 sc1
s_cbranch_scc1 Lg3_st1_%=
s_branch Lg3_st0_%=
Lg3_gox_%=:
s_add_u32 m0,s59,2048
s_nop 0
buffer_load_dword v8,s[36:39],s58 offen offset:0 sc1 lds
s_waitcnt vmcnt(0)
s_barrier
buffer_store_dword v4,v3,s[36:39],s70 offen offset:0 sc0 sc1
s_min_u32 s71,s71,s72
buffer_store_dword v4,v3,s[36:39],s71 offen offset:0 sc0 sc1
s_mov_b32 s70,0x7fff0000
s_mov_b32 s71,0x7fff0000
s_mov_b32 s72,0x7fff0000
s_cmp_eq_u32 s80,1
s_cbranch_scc1 Lg3_st1_%=
Lg3_st0_%=:
ds_read_b32 v7,v6 offset:2048
R 42,1,0
R 46,1,1024
R 50,1,2048
R 54,1,3072
R 58,1,4096
L 5
v_cmp_eq_u32 vcc,s53,v7
L 3
s_cmp_eq_u32 vcc_lo,-1
s_cbranch_scc1 Lg3_F0_%=
s_branch Lg3_L0_%=
Lg3_st1_%=:
ds_read_b32 v7,v6 offset:2048
R 42,2,0
R 46,2,1024
R 50,2,2048
R 54,2,3072
R 58,2,4096
L 5
v_cmp_eq_u32 vcc,s53,v7
L 3
s_cmp_eq_u32 vcc_lo,-1
s_cbranch_scc1 Lg3_F1_%=
s_branch Lg3_L1_%=
Lg3_F0_%=:
v_mfma_f32_16x16x32_bf16 v[26:29],a[8:11],v[42:45],0
s_add_u32 m0,s57,0
R 62,1,5120
v_add_f32_dpp v115,v114,v114 quad_perm:[1,0,3,2] row_mask:0xf bank_mask:0xf
M 26,a[20:23],46
buffer_load_dword v114,v11,s[36:39],s73 offen offset:0 sc1
v_mfma_f32_16x16x32_bf16 v[22:25],a[4:7],v[42:45],0
s_add_u32 s56,s55,0
v_add_f32_dpp v115,v115,v115 quad_perm:[2,3,0,1] row_mask:0xf bank_mask:0xf
v_fma_f32 v30,-v78,v82,v30
M 22,a[16:19],46
s_add_u32 s58,s58,s61
R 66,1,6144
v_fma_f32 v31,-v78,v83,v31
v_mfma_f32_16x16x32_bf16 v[18:21],a[0:3],v[42:45],0
D 0,56,0
M 18,a[12:15],46
L 3
v_add_f32_dpp v118,v115,v115 row_half_mirror row_mask:0xf bank_mask:0xf
v_fma_f32 v32,-v78,v84,v32
M 18,a[24:27],50
s_mov_b32 s66,s65
R 70,1,7168
v_fma_f32 v33,-v78,v85,v33
M 18,a[36:39],54
s_add_u32 s65,s65,s62
v_mov_b32_dpp v115,v118 row_ror:8 row_mask:0xf bank_mask:0xf
v_fma_f32 v115,-v118,v118,v115
M 22,a[28:31],50
s_add_u32 s54,s54,s46
v_max_f32 v115,s87,v115
v_mul_f32 v118,v17,v118
M 22,a[40:43],54
s_add_u32 s73,s73,s64
R 42,1,8192
v_mul_f32 v94,v30,v30
M 26,a[32:35],50
v_rsq_f32 v116,v115
M 26,a[44:47],54
L 3
D 0,56,1024
M 26,a[56:59],58
R 46,1,9216
v_fma_f32 v115,v15,v115,v16
M 26,a[68:71],62
v_mul_f32 v119,v116,v116
v_fma_f32 v115,v14,v119,v115
M 22,a[52:55],58
v_fma_f32 v118,v116,v115,v118
v_mul_f32 v95,v31,v31
M 22,a[64:67],62
R 50,1,10240
v_mul_f32 v96,v32,v32
M 18,a[48:51],58
ds_write_b32 v12,v118 offset:0
M 18,a[60:63],62
L 4
v_mul_f32 v97,v33,v33
v_fma_f32 v94,v80,v94,v79
M 18,a[72:75],66
R 54,1,11264
v_fma_f32 v95,v80,v95,v79
M 18,a[84:87],70
v_fma_f32 v96,v80,v96,v79
v_fma_f32 v97,v80,v97,v79
M 22,a[76:79],66
v_mul_f32 v94,v30,v94
v_mul_f32 v95,v31,v95
M 22,a[88:91],70
R 58,1,12288
v_mul_f32 v96,v32,v96
M 26,a[80:83],66
D 0,56,2048
M 26,a[92:95],70
L 4
v_exp_f32 v94,v94
M 26,a[104:107],42
R 62,1,13312
v_mul_f32 v97,v33,v97
M 26,a[116:119],46
v_exp_f32 v95,v95
M 22,a[100:103],42
v_exp_f32 v96,v96
M 22,a[112:115],46
R 66,1,14336
v_fma_f32 v94,v94,v81,v81
M 18,a[96:99],42
v_exp_f32 v97,v97
M 18,a[108:111],46
L 3
v_rcp_f32 v94,v94
M 18,a[120:123],50
R 70,1,15360
v_fma_f32 v95,v95,v81,v81
)"
R"(M 18,a[132:135],54
s_waitcnt vmcnt(7)
D 0,56,3072
M 22,a[124:127],50
s_barrier
v_rcp_f32 v95,v95
M 22,a[136:139],54
s_add_u32 m0,s59,0
R 42,1,16384
v_fma_f32 v96,v96,v81,v81
M 26,a[128:131],50
s_add_u32 s56,s55,16384
v_rcp_f32 v96,v96
M 26,a[140:143],54
L 3
v_fma_f32 v97,v97,v81,v81
v_mul_f32 v30,v30,v94
M 26,a[152:155],58
s_add_u32 s55,s55,s60
R 46,1,17408
v_mul_f32 v31,v31,v95
M 26,a[164:167],62
v_rcp_f32 v97,v97
M 22,a[148:151],58
v_mul_f32 v32,v32,v96
v_mul_f32 v33,v33,v97
M 22,a[160:163],62
R 50,1,18432
v_cvt_pk_bf16_f32 v106,v30,v31
M 18,a[144:147],58
buffer_load_dword v8,s[36:39],s58 offen offset:0 sc1 lds
M 18,a[156:159],62
L 3
v_cvt_pk_bf16_f32 v107,v32,v33
v_fma_f32 v34,-v78,v86,v34
M 18,a[168:171],66
s_add_u32 m0,s57,16384
R 54,1,19456
v_fma_f32 v35,-v78,v87,v35
M 18,a[180:183],70
s_sub_u32 s57,s84,s57
v_fma_f32 v36,-v78,v88,v36
v_fma_f32 v37,-v78,v89,v37
M 22,a[172:175],66
v_mul_f32 v98,v34,v34
v_mul_f32 v99,v35,v35
M 22,a[184:187],70
R 58,1,20480
v_mul_f32 v100,v36,v36
M 26,a[176:179],66
D 0,56,0
M 26,a[188:191],70
L 3
v_mul_f32 v101,v37,v37
v_fma_f32 v98,v80,v98,v79
M 26,a[200:203],42
R 62,1,21504
v_fma_f32 v99,v80,v99,v79
M 26,a[212:215],46
v_fma_f32 v100,v80,v100,v79
v_fma_f32 v101,v80,v101,v79
M 22,a[196:199],42
v_mul_f32 v98,v34,v98
v_mul_f32 v99,v35,v99
M 22,a[208:211],46
R 66,1,22528
v_mul_f32 v100,v36,v100
M 18,a[192:195],42
v_exp_f32 v98,v98
M 18,a[204:207],46
L 3
v_exp_f32 v99,v99
M 18,a[216:219],50
R 70,1,23552
v_mul_f32 v101,v37,v101
M 18,a[228:231],54
D 0,56,1024
M 22,a[220:223],50
v_exp_f32 v100,v100
M 22,a[232:235],54
R 42,1,24576
v_fma_f32 v98,v98,v81,v81
M 26,a[224:227],50
v_exp_f32 v101,v101
M 26,a[236:239],54
L 3
v_rcp_f32 v98,v98
M 26,a[248:251],58
R 46,1,25600
v_fma_f32 v99,v99,v81,v81
M 26,v[132:135],62
v_rcp_f32 v99,v99
M 22,a[244:247],58
v_fma_f32 v100,v100,v81,v81
v_fma_f32 v101,v101,v81,v81
M 22,v[128:131],62
R 50,1,26624
v_mul_f32 v34,v34,v98
M 18,a[240:243],58
v_rcp_f32 v100,v100
M 18,a[252:255],62
L 3
v_rcp_f32 v101,v101
M 18,v[136:139],66
R 54,1,27648
v_mul_f32 v35,v35,v99
M 18,v[148:151],70
D 0,56,2048
M 22,v[140:143],66
v_mul_f32 v36,v36,v100
v_mul_f32 v37,v37,v101
M 22,v[152:155],70
R 58,1,28672
v_cvt_pk_bf16_f32 v108,v34,v35
M 26,v[144:147],66
v_cvt_pk_bf16_f32 v109,v36,v37
v_fma_f32 v38,-v78,v90,v38
M 26,v[156:159],70
L 3
v_fma_f32 v39,-v78,v91,v39
v_fma_f32 v40,-v78,v92,v40
M 26,v[168:171],42
R 62,1,29696
v_fma_f32 v41,-v78,v93,v41
M 26,v[180:183],46
v_mul_f32 v102,v38,v38
v_mul_f32 v103,v39,v39
M 22,v[164:167],42
v_mul_f32 v104,v40,v40
v_mul_f32 v105,v41,v41
M 22,v[176:179],46
R 66,1,30720
v_fma_f32 v102,v80,v102,v79
M 18,v[160:163],42
v_fma_f32 v103,v80,v103,v79
v_fma_f32 v104,v80,v104,v79
M 18,v[172:175],46
L 3
D 0,56,3072
M 18,v[184:187],50
s_nop 0
R 70,1,31744
v_fma_f32 v105,v80,v105,v79
M 18,v[196:199],54
s_waitcnt vmcnt(4)
v_mul_f32 v102,v38,v102
v_mul_f32 v103,v39,v103
M 22,v[188:191],50
s_barrier
buffer_store_dword v4,v3,s[36:39],s70 offen offset:0 sc0 sc1
M 22,v[200:203],54
s_mov_b32 s70,s69
R 42,2,0
ds_read_b32 v7,v6 offset:0
M 26,v[192:195],50
s_mov_b32 s69,s68
R 74,13,0
v_mul_f32 v104,v40,v104
M 26,v[204:207],54
L 5
v_exp_f32 v102,v102
M 26,v[216:219],58
s_add_u32 s68,s68,s63
R 46,2,1024
v_mul_f32 v105,v41,v105
M 26,v[228:231],62
v_exp_f32 v103,v103
M 22,v[212:215],58
buffer_store_dwordx4 v[106:109],v0,s[36:39],s67 offen offset:0 sc0 sc1
v_fma_f32 v102,v102,v81,v81
M 22,v[224:227],62
L 2
R 50,2,2048
v_fma_f32 v103,v103,v81,v81
M 18,v[208:211],58
v_exp_f32 v104,v104
M 18,v[220:223],62
s_nop 0
v_exp_f32 v105,v105
M 18,v[232:235],66
R 54,2,3072
v_fma_f32 v104,v104,v81,v81
M 18,v[244:247],70
v_fma_f32 v105,v105,v81,v81
v_cmp_eq_u32 vcc,s53,v7
M 22,v[236:239],66
v_rcp_f32 v102,v102
v_rcp_f32 v103,v103
v_rcp_f32 v104,v104
v_rcp_f32 v105,v105
M 22,v[248:251],70
s_cmp_eq_u32 vcc_lo,-1
R 58,2,4096
v_mul_f32 v38,v38,v102
v_mul_f32 v39,v39,v103
v_mul_f32 v40,v40,v104
v_mul_f32 v41,v41,v105
v_cvt_pk_bf16_f32 v110,v38,v39
v_cvt_pk_bf16_f32 v111,v40,v41
M 26,v[240:243],66
L 3
buffer_store_dwordx2 v[110:111],v10,s[36:39],s67 offen offset:0 sc0 sc1
M 26,v[252:255],70
s_cbranch_scc0 Lg3_K1_%=
Lg3_F1_%=:
v_mfma_f32_16x16x32_bf16 v[38:41],a[8:11],v[42:45],0
s_add_u32 m0,s57,0
R 62,2,5120
v_add_f32_dpp v115,v114,v114 quad_perm:[1,0,3,2] row_mask:0xf bank_mask:0xf
M 38,a[20:23],46
buffer_load_dword v114,v11,s[36:39],s73 offen offset:0 sc1
v_mfma_f32_16x16x32_bf16 v[34:37],a[4:7],v[42:45],0
s_add_u32 s56,s55,0
v_add_f32_dpp v115,v115,v115 quad_perm:[2,3,0,1] row_mask:0xf bank_mask:0xf
v_fma_f32 v18,-v74,v82,v18
M 34,a[16:19],46
s_add_u32 s58,s58,s61
R 66,2,6144
v_fma_f32 v19,-v74,v83,v19
v_mfma_f32_16x16x32_bf16 v[30:33],a[0:3],v[42:45],0
D 0,56,0
M 30,a[12:15],46
L 3
v_add_f32_dpp v118,v115,v115 row_half_mirror row_mask:0xf bank_mask:0xf
v_fma_f32 v20,-v74,v84,v20
M 30,a[24:27],50
s_mov_b32 s67,s65
R 70,2,7168
v_fma_f32 v21,-v74,v85,v21
M 30,a[36:39],54
s_add_u32 s65,s65,s62
v_mov_b32_dpp v115,v118 row_ror:8 row_mask:0xf bank_mask:0xf
v_fma_f32 v115,-v118,v118,v115
M 34,a[28:31],50
s_add_u32 s54,s54,s46
v_max_f32 v115,s87,v115
v_mul_f32 v118,v17,v118
M 34,a[40:43],54
s_add_u32 s73,s73,s64
R 42,2,8192
v_mul_f32 v94,v18,v18
M 38,a[32:35],50
v_rsq_f32 v116,v115
M 38,a[44:47],54
L 3
D 0,56,1024
M 38,a[56:59],58
R 46,2,9216
v_fma_f32 v115,v15,v115,v16
M 38,a[68:71],62
v_mul_f32 v119,v116,v116
v_fma_f32 v115,v14,v119,v115
M 34,a[52:55],58
v_fma_f32 v118,v116,v115,v118
v_mul_f32 v95,v19,v19
M 34,a[64:67],62
R 50,2,10240
v_mul_f32 v96,v20,v20
M 30,a[48:51],58
ds_write_b32 v12,v118 offset:2048
M 30,a[60:63],62
L 4
v_mul_f32 v97,v21,v21
v_fma_f32 v94,v76,v94,v75
M 30,a[72:75],66
R 54,2,11264
v_fma_f32 v95,v76,v95,v75
M 30,a[84:87],70
v_fma_f32 v96,v76,v96,v75
v_fma_f32 v97,v76,v97,v75
M 34,a[76:79],66
v_mul_f32 v94,v18,v94
v_mul_f32 v95,v19,v95
M 34,a[88:91],70
R 58,2,12288
v_mul_f32 v96,v20,v96
M 38,a[80:83],66
D 0,56,2048
M 38,a[92:95],70
L 4
v_exp_f32 v94,v94
M 38,a[104:107],42
R 62,2,13312
v_mul_f32 v97,v21,v97
M 38,a[116:119],46
v_exp_f32 v95,v95
M 34,a[100:103],42
v_exp_f32 v96,v96
M 34,a[112:115],46
R 66,2,14336
v_fma_f32 v94,v94,v77,v77
M 30,a[96:99],42
v_exp_f32 v97,v97
M 30,a[108:111],46
L 3
v_rcp_f32 v94,v94
M 30,a[120:123],50
R 70,2,15360
v_fma_f32 v95,v95,v77,v77
M 30,a[132:135],54
s_waitcnt vmcnt(7)
D 0,56,3072
M 34,a[124:127],50
s_barrier
v_rcp_f32 v95,v95
M 34,a[136:139],54
s_add_u32 m0,s59,1024
R 42,2,16384
v_fma_f32 v96,v96,v77,v77
M 38,a[128:131],50
s_add_u32 s56,s55,16384
v_rcp_f32 v96,v96
M 38,a[140:143],54
L 3
v_fma_f32 v97,v97,v77,v77
v_mul_f32 v18,v18,v94
M 38,a[152:155],58
s_add_u32 s55,s55,s60
R 46,2,17408
v_mul_f32 v19,v19,v95
M 38,a[164:167],62
v_rcp_f32 v97,v97
M 34,a[148:151],58
v_mul_f32 v20,v20,v96
v_mul_f32 v21,v21,v97
M 34,a[160:163],62
R 50,2,18432
v_cvt_pk_bf16_f32 v106,v18,v19
M 30,a[144:147],58
buffer_load_dword v8,s[36:39],s58 offen offset:0 sc1 lds
M 30,a[156:159],62
L 3
v_cvt_pk_bf16_f32 v107,v20,v21
v_fma_f32 v22,-v74,v86,v22
M 30,a[168:171],66
s_add_u32 m0,s57,16384
R 54,2,19456
v_fma_f32 v23,-v74,v87,v23
M 30,a[180:183],70
s_sub_u32 s57,s84,s57
v_fma_f32 v24,-v74,v88,v24
v_fma_f32 v25,-v74,v89,v25
M 34,a[172:175],66
v_mul_f32 v98,v22,v22
v_mul_f32 v99,v23,v23
M 34,a[184:187],70
R 58,2,20480
v_mul_f32 v100,v24,v24
M 38,a[176:179],66
D 0,56,0
M 38,a[188:191],70
L 3
v_mul_f32 v101,v25,v25
v_fma_f32 v98,v76,v98,v75
M 38,a[200:203],42
R 62,2,21504
v_fma_f32 v99,v76,v99,v75
M 38,a[212:215],46
v_fma_f32 v100,v76,v100,v75
v_fma_f32 v101,v76,v101,v75
M 34,a[196:199],42
v_mul_f32 v98,v22,v98
v_mul_f32 v99,v23,v99
M 34,a[208:211],46
R 66,2,22528
v_mul_f32 v100,v24,v100
M 30,a[192:195],42
v_exp_f32 v98,v98
M 30,a[204:207],46
L 3
v_exp_f32 v99,v99
M 30,a[216:219],50
R 70,2,23552
v_mul_f32 v101,v25,v101
M 30,a[228:231],54
D 0,56,1024
M 34,a[220:223],50
v_exp_f32 v100,v100
M 34,a[232:235],54
R 42,2,24576
v_fma_f32 v98,v98,v77,v77
M 38,a[224:227],50
v_exp_f32 v101,v101
M 38,a[236:239],54
L 3
v_rcp_f32 v98,v98
M 38,a[248:251],58
R 46,2,25600
v_fma_f32 v99,v99,v77,v77
M 38,v[132:135],62
v_rcp_f32 v99,v99
M 34,a[244:247],58
v_fma_f32 v100,v100,v77,v77
v_fma_f32 v101,v101,v77,v77
M 34,v[128:131],62
R 50,2,26624
v_mul_f32 v22,v22,v98
M 30,a[240:243],58
v_rcp_f32 v100,v100
M 30,a[252:255],62
L 3
v_rcp_f32 v101,v101
M 30,v[136:139],66
R 54,2,27648
v_mul_f32 v23,v23,v99
M 30,v[148:151],70
D 0,56,2048
M 34,v[140:143],66
v_mul_f32 v24,v24,v100
v_mul_f32 v25,v25,v101
M 34,v[152:155],70
R 58,2,28672
v_cvt_pk_bf16_f32 v108,v22,v23
M 38,v[144:147],66
v_cvt_pk_bf16_f32 v109,v24,v25
v_fma_f32 v26,-v74,v90,v26
M 38,v[156:159],70
L 3
v_fma_f32 v27,-v74,v91,v27
v_fma_f32 v28,-v74,v92,v28
M 38,v[168:171],42
R 62,2,29696
v_fma_f32 v29,-v74,v93,v29
M 38,v[180:183],46
v_mul_f32 v102,v26,v26
v_mul_f32 v103,v27,v27
M 34,v[164:167],42
v_mul_f32 v104,v28,v28
v_mul_f32 v105,v29,v29
M 34,v[176:179],46
R 66,2,30720
v_fma_f32 v102,v76,v102,v75
M 30,v[160:163],42
v_fma_f32 v103,v76,v103,v75
v_fma_f32 v104,v76,v104,v75
M 30,v[172:175],46
L 3
D 0,56,3072
M 30,v[184:187],50
s_nop 0
R 70,2,31744
v_fma_f32 v105,v76,v105,v75
M 30,v[196:199],54
s_waitcnt vmcnt(4)
v_mul_f32 v102,v26,v102
v_mul_f32 v103,v27,v103
M 34,v[188:191],50
s_barrier
buffer_store_dword v4,v3,s[36:39],s70 offen offset:0 sc0 sc1
M 34,v[200:203],54
s_mov_b32 s70,s69
R 42,1,0
ds_read_b32 v7,v6 offset:1024
M 38,v[192:195],50
s_mov_b32 s69,s68
R 78,13,2048
v_mul_f32 v104,v28,v104
M 38,v[204:207],54
L 5
v_exp_f32 v102,v102
M 38,v[216:219],58
s_add_u32 s68,s68,s63
R 46,1,1024
v_mul_f32 v105,v29,v105
M 38,v[228:231],62
v_exp_f32 v103,v103
M 34,v[212:215],58
buffer_store_dwordx4 v[106:109],v0,s[36:39],s66 offen offset:0 sc0 sc1
v_fma_f32 v102,v102,v77,v77
M 34,v[224:227],62
L 2
R 50,1,2048
v_fma_f32 v103,v103,v77,v77
M 30,v[208:211],58
v_exp_f32 v104,v104
M 30,v[220:223],62
s_nop 0
v_exp_f32 v105,v105
M 30,v[232:235],66
R 54,1,3072
v_fma_f32 v104,v104,v77,v77
M 30,v[244:247],70
v_fma_f32 v105,v105,v77,v77
v_cmp_eq_u32 vcc,s53,v7
M 34,v[236:239],66
v_rcp_f32 v102,v102
v_rcp_f32 v103,v103
v_rcp_f32 v104,v104
v_rcp_f32 v105,v105
M 34,v[248:251],70
s_cmp_eq_u32 vcc_lo,-1
R 58,1,4096
v_mul_f32 v26,v26,v102
v_mul_f32 v27,v27,v103
v_mul_f32 v28,v28,v104
v_mul_f32 v29,v29,v105
v_cvt_pk_bf16_f32 v110,v26,v27
v_cvt_pk_bf16_f32 v111,v28,v29
M 38,v[240:243],66
L 3
)"
R"(buffer_store_dwordx2 v[110:111],v10,s[36:39],s66 offen offset:0 sc0 sc1
M 38,v[252:255],70
s_cbranch_scc1 Lg3_F0_%=
Lg3_K0_%=:
Lg3_L0_%=:
v_mfma_f32_16x16x32_bf16 v[26:29],a[8:11],v[42:45],0
s_add_u32 m0,s59,3072
R 62,1,5120
v_add_f32_dpp v115,v114,v114 quad_perm:[1,0,3,2] row_mask:0xf bank_mask:0xf
M 26,a[20:23],46
buffer_load_dword v8,s[36:39],s58 offen offset:0 sc1 lds
v_mfma_f32_16x16x32_bf16 v[22:25],a[4:7],v[42:45],0
s_mov_b32 s66,s65
v_add_f32_dpp v115,v115,v115 quad_perm:[2,3,0,1] row_mask:0xf bank_mask:0xf
v_fma_f32 v30,-v78,v82,v30
M 22,a[16:19],46
s_add_u32 s65,s65,s62
R 66,1,6144
v_fma_f32 v31,-v78,v83,v31
v_mfma_f32_16x16x32_bf16 v[18:21],a[0:3],v[42:45],0
v_add_f32_dpp v118,v115,v115 row_half_mirror row_mask:0xf bank_mask:0xf
v_fma_f32 v32,-v78,v84,v32
v_fma_f32 v33,-v78,v85,v33
M 18,a[12:15],46
L 3
v_mul_f32 v94,v30,v30
v_mul_f32 v95,v31,v31
M 18,a[24:27],50
R 70,1,7168
v_mov_b32_dpp v115,v118 row_ror:8 row_mask:0xf bank_mask:0xf
M 18,a[36:39],54
v_fma_f32 v115,-v118,v118,v115
v_max_f32 v115,s87,v115
M 22,a[28:31],50
v_rsq_f32 v116,v115
M 22,a[40:43],54
R 42,1,8192
v_fma_f32 v115,v15,v115,v16
v_mul_f32 v118,v17,v118
M 26,a[32:35],50
v_mul_f32 v119,v116,v116
v_fma_f32 v115,v14,v119,v115
M 26,a[44:47],54
L 3
v_fma_f32 v118,v116,v115,v118
v_mul_f32 v96,v32,v32
M 26,a[56:59],58
R 46,1,9216
v_mul_f32 v97,v33,v33
M 26,a[68:71],62
ds_write_b32 v12,v118 offset:0
M 22,a[52:55],58
v_fma_f32 v94,v80,v94,v79
v_fma_f32 v95,v80,v95,v79
v_fma_f32 v96,v80,v96,v79
M 22,a[64:67],62
R 50,1,10240
v_fma_f32 v97,v80,v97,v79
M 18,a[48:51],58
v_mul_f32 v94,v30,v94
v_mul_f32 v95,v31,v95
M 18,a[60:63],62
L 4
v_exp_f32 v94,v94
M 18,a[72:75],66
R 54,1,11264
v_mul_f32 v96,v32,v96
M 18,a[84:87],70
v_exp_f32 v95,v95
v_mul_f32 v97,v33,v97
M 22,a[76:79],66
v_exp_f32 v96,v96
M 22,a[88:91],70
R 58,1,12288
v_fma_f32 v94,v94,v81,v81
M 26,a[80:83],66
v_exp_f32 v97,v97
M 26,a[92:95],70
L 4
v_rcp_f32 v94,v94
M 26,a[104:107],42
R 62,1,13312
v_fma_f32 v95,v95,v81,v81
v_fma_f32 v96,v96,v81,v81
M 26,a[116:119],46
v_rcp_f32 v95,v95
M 22,a[100:103],42
v_rcp_f32 v96,v96
M 22,a[112:115],46
R 66,1,14336
v_fma_f32 v97,v97,v81,v81
M 18,a[96:99],42
v_rcp_f32 v97,v97
M 18,a[108:111],46
L 3
v_mul_f32 v30,v30,v94
v_mul_f32 v31,v31,v95
v_mul_f32 v32,v32,v96
M 18,a[120:123],50
R 70,1,15360
v_mul_f32 v33,v33,v97
M 18,a[132:135],54
s_waitcnt vmcnt(0)
v_cvt_pk_bf16_f32 v106,v30,v31
v_cvt_pk_bf16_f32 v107,v32,v33
M 22,a[124:127],50
s_barrier
ds_read_b32 v7,v6 offset:3072
v_fma_f32 v34,-v78,v86,v34
M 22,a[136:139],54
R 42,1,16384
v_fma_f32 v35,-v78,v87,v35
M 26,a[128:131],50
buffer_store_dword v4,v3,s[36:39],s70 offen offset:0 sc0 sc1
v_fma_f32 v36,-v78,v88,v36
M 26,a[140:143],54
L 4
v_fma_f32 v37,-v78,v89,v37
v_mul_f32 v98,v34,v34
M 26,a[152:155],58
s_mov_b32 s70,0x7fff0000
R 46,1,17408
v_mul_f32 v99,v35,v35
M 26,a[164:167],62
v_mul_f32 v100,v36,v36
v_mul_f32 v101,v37,v37
M 22,a[148:151],58
L 2
v_cmp_eq_u32 vcc,s53,v7
v_fma_f32 v98,v80,v98,v79
M 22,a[160:163],62
R 50,1,18432
v_fma_f32 v99,v80,v99,v79
v_fma_f32 v100,v80,v100,v79
M 18,a[144:147],58
v_fma_f32 v101,v80,v101,v79
v_mul_f32 v98,v34,v98
M 18,a[156:159],62
s_cmp_eq_u32 vcc_lo,-1
s_nop 0
v_mul_f32 v99,v35,v99
s_cbranch_scc0 Lg3_X0_%=
M 18,a[168:171],66
s_add_u32 m0,s57,0
s_add_u32 s56,s55,0
s_add_u32 s58,s58,s61
R 54,1,19456
M 18,a[180:183],70
buffer_load_dword v114,v11,s[36:39],s73 offen offset:0 sc1
M 22,a[172:175],66
D 0,56,0
v_mul_f32 v100,v36,v100
M 22,a[184:187],70
s_add_u32 s54,s54,s46
R 58,1,20480
v_mul_f32 v101,v37,v101
M 26,a[176:179],66
s_add_u32 s73,s73,s64
v_exp_f32 v98,v98
M 26,a[188:191],70
L 3
v_exp_f32 v99,v99
M 26,a[200:203],42
R 62,1,21504
v_fma_f32 v98,v98,v81,v81
M 26,a[212:215],46
D 0,56,1024
v_fma_f32 v99,v99,v81,v81
M 22,a[196:199],42
v_exp_f32 v100,v100
M 22,a[208:211],46
R 66,1,22528
v_fma_f32 v100,v100,v81,v81
M 18,a[192:195],42
D 0,56,2048
M 18,a[204:207],46
L 3
v_exp_f32 v101,v101
M 18,a[216:219],50
R 70,1,23552
v_rcp_f32 v98,v98
M 18,a[228:231],54
v_rcp_f32 v99,v99
M 22,a[220:223],50
D 0,56,3072
M 22,a[232:235],54
s_add_u32 m0,s59,0
R 42,1,24576
v_fma_f32 v101,v101,v81,v81
M 26,a[224:227],50
buffer_load_dword v8,s[36:39],s58 offen offset:0 sc1 lds
M 26,a[236:239],54
L 3
v_rcp_f32 v100,v100
v_mul_f32 v34,v34,v98
M 26,a[248:251],58
s_add_u32 m0,s57,16384
R 46,1,25600
v_mul_f32 v35,v35,v99
M 26,v[132:135],62
s_add_u32 s56,s55,16384
v_rcp_f32 v101,v101
M 22,a[244:247],58
D 0,56,0
M 22,v[128:131],62
s_add_u32 s55,s55,s60
R 50,1,26624
v_mul_f32 v36,v36,v100
M 18,a[240:243],58
s_sub_u32 s57,s84,s57
v_mul_f32 v37,v37,v101
v_cvt_pk_bf16_f32 v108,v34,v35
v_cvt_pk_bf16_f32 v109,v36,v37
M 18,a[252:255],62
L 3
buffer_store_dwordx4 v[106:109],v0,s[36:39],s67 offen offset:0 sc0 sc1
v_fma_f32 v38,-v78,v90,v38
M 18,v[136:139],66
R 54,1,27648
v_fma_f32 v39,-v78,v91,v39
M 18,v[148:151],70
v_fma_f32 v40,-v78,v92,v40
v_fma_f32 v41,-v78,v93,v41
M 22,v[140:143],66
D 0,56,1024
M 22,v[152:155],70
R 58,1,28672
v_mul_f32 v102,v38,v38
v_mul_f32 v103,v39,v39
M 26,v[144:147],66
v_mul_f32 v104,v40,v40
v_mul_f32 v105,v41,v41
M 26,v[156:159],70
L 3
v_fma_f32 v102,v80,v102,v79
v_fma_f32 v103,v80,v103,v79
M 26,v[168:171],42
R 62,1,29696
v_fma_f32 v104,v80,v104,v79
M 26,v[180:183],46
D 0,56,2048
M 22,v[164:167],42
v_fma_f32 v105,v80,v105,v79
v_mul_f32 v102,v38,v102
v_mul_f32 v103,v39,v103
M 22,v[176:179],46
R 66,1,30720
v_mul_f32 v104,v40,v104
M 18,v[160:163],42
v_exp_f32 v102,v102
M 18,v[172:175],46
L 3
D 0,56,3072
M 18,v[184:187],50
s_nop 0
R 70,1,31744
v_mul_f32 v105,v41,v105
M 18,v[196:199],54
s_waitcnt vmcnt(5)
v_exp_f32 v103,v103
v_fma_f32 v102,v102,v81,v81
M 22,v[188:191],50
s_barrier
buffer_store_dword v4,v3,s[36:39],s70 offen offset:0 sc0 sc1
M 22,v[200:203],54
s_mov_b32 s70,s69
R 42,2,0
ds_read_b32 v7,v6 offset:0
M 26,v[192:195],50
s_mov_b32 s69,s68
R 74,13,0
v_fma_f32 v103,v103,v81,v81
M 26,v[204:207],54
L 5
v_exp_f32 v104,v104
M 26,v[216:219],58
s_add_u32 s68,s68,s63
R 46,2,1024
v_exp_f32 v105,v105
M 26,v[228:231],62
v_rcp_f32 v102,v102
M 22,v[212:215],58
v_rcp_f32 v103,v103
M 22,v[224:227],62
L 2
R 50,2,2048
v_fma_f32 v104,v104,v81,v81
M 18,v[208:211],58
v_rcp_f32 v104,v104
M 18,v[220:223],62
s_nop 0
v_fma_f32 v105,v105,v81,v81
v_rcp_f32 v105,v105
M 18,v[232:235],66
R 54,2,3072
v_mul_f32 v38,v38,v102
M 18,v[244:247],70
v_mul_f32 v39,v39,v103
v_mul_f32 v40,v40,v104
M 22,v[236:239],66
v_cmp_eq_u32 vcc,s53,v7
v_mul_f32 v41,v41,v105
M 22,v[248:251],70
R 58,2,4096
v_cvt_pk_bf16_f32 v110,v38,v39
v_cvt_pk_bf16_f32 v111,v40,v41
M 26,v[240:243],66
L 3
s_cmp_eq_u32 vcc_lo,-1
buffer_store_dwordx2 v[110:111],v10,s[36:39],s67 offen offset:0 sc0 sc1
M 26,v[252:255],70
buffer_store_dword v4,v3,s[36:39],s71 offen offset:0 sc0 sc1
s_cbranch_scc1 Lg3_F1_%=
s_branch Lg3_K1_%=
Lg3_X0_%=:
M 18,a[168:171],66
s_add_u32 m0,s59,0
R 54,1,19456
v_mul_f32 v100,v36,v100
M 18,a[180:183],70
buffer_load_dword v8,s[36:39],s58 offen offset:0 sc1 lds
M 22,a[172:175],66
v_exp_f32 v98,v98
M 22,a[184:187],70
R 58,1,20480
v_mul_f32 v101,v37,v101
M 26,a[176:179],66
v_exp_f32 v99,v99
M 26,a[188:191],70
L 3
v_exp_f32 v100,v100
M 26,a[200:203],42
R 62,1,21504
v_fma_f32 v98,v98,v81,v81
M 26,a[212:215],46
v_exp_f32 v101,v101
M 22,a[196:199],42
v_rcp_f32 v98,v98
M 22,a[208:211],46
R 66,1,22528
v_fma_f32 v99,v99,v81,v81
M 18,a[192:195],42
v_rcp_f32 v99,v99
M 18,a[204:207],46
L 3
v_fma_f32 v100,v100,v81,v81
v_fma_f32 v101,v101,v81,v81
M 18,a[216:219],50
R 70,1,23552
v_mul_f32 v34,v34,v98
M 18,a[228:231],54
v_rcp_f32 v100,v100
M 22,a[220:223],50
v_rcp_f32 v101,v101
M 22,a[232:235],54
R 42,1,24576
v_mul_f32 v35,v35,v99
M 26,a[224:227],50
v_mul_f32 v36,v36,v100
v_mul_f32 v37,v37,v101
M 26,a[236:239],54
L 3
v_cvt_pk_bf16_f32 v108,v34,v35
v_cvt_pk_bf16_f32 v109,v36,v37
M 26,a[248:251],58
R 46,1,25600
v_fma_f32 v38,-v78,v90,v38
M 26,v[132:135],62
buffer_store_dwordx4 v[106:109],v0,s[36:39],s67 offen offset:0 sc0 sc1
v_fma_f32 v39,-v78,v91,v39
M 22,a[244:247],58
v_fma_f32 v40,-v78,v92,v40
v_fma_f32 v41,-v78,v93,v41
M 22,v[128:131],62
R 50,1,26624
v_mul_f32 v102,v38,v38
M 18,a[240:243],58
v_mul_f32 v103,v39,v39
v_mul_f32 v104,v40,v40
M 18,a[252:255],62
L 3
v_mul_f32 v105,v41,v41
v_fma_f32 v102,v80,v102,v79
M 18,v[136:139],66
R 54,1,27648
v_fma_f32 v103,v80,v103,v79
M 18,v[148:151],70
v_fma_f32 v104,v80,v104,v79
v_fma_f32 v105,v80,v105,v79
M 22,v[140:143],66
v_mul_f32 v102,v38,v102
v_mul_f32 v103,v39,v103
M 22,v[152:155],70
R 58,1,28672
v_mul_f32 v104,v40,v104
M 26,v[144:147],66
v_exp_f32 v102,v102
M 26,v[156:159],70
L 3
v_exp_f32 v103,v103
M 26,v[168:171],42
R 62,1,29696
v_mul_f32 v105,v41,v105
M 26,v[180:183],46
v_exp_f32 v104,v104
M 22,v[164:167],42
v_exp_f32 v105,v105
M 22,v[176:179],46
R 66,1,30720
v_fma_f32 v102,v102,v81,v81
M 18,v[160:163],42
v_rcp_f32 v102,v102
M 18,v[172:175],46
L 3
v_fma_f32 v103,v103,v81,v81
v_fma_f32 v104,v104,v81,v81
M 18,v[184:187],50
s_nop 0
R 70,1,31744
v_fma_f32 v105,v105,v81,v81
M 18,v[196:199],54
s_waitcnt vmcnt(1)
v_rcp_f32 v103,v103
M 22,v[188:191],50
s_barrier
buffer_store_dword v4,v3,s[36:39],s70 offen offset:0 sc0 sc1
M 22,v[200:203],54
s_mov_b32 s70,s69
ds_read_b32 v7,v6 offset:0
R 74,13,0
M 26,v[192:195],50
s_mov_b32 s69,s68
v_rcp_f32 v104,v104
M 26,v[204:207],54
L 4
v_rcp_f32 v105,v105
M 26,v[216:219],58
s_add_u32 s68,s68,s63
v_mul_f32 v38,v38,v102
v_mul_f32 v39,v39,v103
M 26,v[228:231],62
v_mul_f32 v40,v40,v104
v_mul_f32 v41,v41,v105
M 22,v[212:215],58
v_cvt_pk_bf16_f32 v110,v38,v39
v_cvt_pk_bf16_f32 v111,v40,v41
M 22,v[224:227],62
L 1
buffer_store_dwordx2 v[110:111],v10,s[36:39],s67 offen offset:0 sc0 sc1
M 18,v[208:211],58
v_cmp_eq_u32 vcc,s53,v7
M 18,v[220:223],62
s_nop 0
M 18,v[232:235],66
M 18,v[244:247],70
M 22,v[236:239],66
M 22,v[248:251],70
M 26,v[240:243],66
M 26,v[252:255],70
s_mov_b32 s80,1
s_cmp_eq_u32 vcc_lo,-1
s_cbranch_scc0 Lg3_xn0_%=
buffer_load_dword v114,v11,s[36:39],s73 offen offset:0 sc1
L 0
v_fma_f32 v18,-v74,v82,v18
v_fma_f32 v19,-v74,v83,v19
v_fma_f32 v20,-v74,v84,v20
v_fma_f32 v21,-v74,v85,v21
v_mul_f32 v94,v18,v18
v_mul_f32 v95,v19,v19
v_mul_f32 v96,v20,v20
v_mul_f32 v97,v21,v21
s_add_u32 s73,s73,s64
s_add_u32 m0,s57,0
s_add_u32 s56,s55,0
D 0,56,0
v_fma_f32 v94,v76,v94,v75
v_fma_f32 v95,v76,v95,v75
v_fma_f32 v96,v76,v96,v75
v_fma_f32 v97,v76,v97,v75
v_mul_f32 v94,v18,v94
v_mul_f32 v95,v19,v95
v_mul_f32 v96,v20,v96
v_mul_f32 v97,v21,v97
D 0,56,1024
v_exp_f32 v94,v94
v_exp_f32 v95,v95
v_exp_f32 v96,v96
v_exp_f32 v97,v97
v_fma_f32 v94,v94,v77,v77
v_fma_f32 v95,v95,v77,v77
v_fma_f32 v96,v96,v77,v77
v_fma_f32 v97,v97,v77,v77
D 0,56,2048
v_rcp_f32 v94,v94
v_rcp_f32 v95,v95
v_rcp_f32 v96,v96
v_rcp_f32 v97,v97
v_mul_f32 v18,v18,v94
v_mul_f32 v19,v19,v95
)"
R"(v_mul_f32 v20,v20,v96
v_mul_f32 v21,v21,v97
D 0,56,3072
v_cvt_pk_bf16_f32 v106,v18,v19
v_cvt_pk_bf16_f32 v107,v20,v21
v_fma_f32 v22,-v74,v86,v22
v_fma_f32 v23,-v74,v87,v23
v_fma_f32 v24,-v74,v88,v24
v_fma_f32 v25,-v74,v89,v25
v_mul_f32 v98,v22,v22
v_mul_f32 v99,v23,v23
s_add_u32 s58,s58,s61
s_add_u32 m0,s57,16384
s_add_u32 s56,s55,16384
D 0,56,0
v_mul_f32 v100,v24,v24
v_mul_f32 v101,v25,v25
v_fma_f32 v98,v76,v98,v75
v_fma_f32 v99,v76,v99,v75
v_fma_f32 v100,v76,v100,v75
v_fma_f32 v101,v76,v101,v75
v_mul_f32 v98,v22,v98
v_mul_f32 v99,v23,v99
D 0,56,1024
v_mul_f32 v100,v24,v100
v_mul_f32 v101,v25,v101
v_exp_f32 v98,v98
v_exp_f32 v99,v99
v_exp_f32 v100,v100
v_exp_f32 v101,v101
v_fma_f32 v98,v98,v77,v77
v_fma_f32 v99,v99,v77,v77
D 0,56,2048
v_fma_f32 v100,v100,v77,v77
v_fma_f32 v101,v101,v77,v77
v_rcp_f32 v98,v98
v_rcp_f32 v99,v99
v_rcp_f32 v100,v100
v_rcp_f32 v101,v101
v_mul_f32 v22,v22,v98
v_mul_f32 v23,v23,v99
D 0,56,3072
v_mul_f32 v24,v24,v100
v_mul_f32 v25,v25,v101
v_cvt_pk_bf16_f32 v108,v22,v23
v_cvt_pk_bf16_f32 v109,v24,v25
buffer_store_dwordx4 v[106:109],v0,s[36:39],s66 offen offset:0 sc0 sc1
v_fma_f32 v26,-v74,v90,v26
v_fma_f32 v27,-v74,v91,v27
v_fma_f32 v28,-v74,v92,v28
v_fma_f32 v29,-v74,v93,v29
s_add_u32 s55,s55,s60
s_sub_u32 s57,s84,s57
s_add_u32 s54,s54,s46
v_mul_f32 v102,v26,v26
v_mul_f32 v103,v27,v27
v_mul_f32 v104,v28,v28
v_mul_f32 v105,v29,v29
v_fma_f32 v102,v76,v102,v75
v_fma_f32 v103,v76,v103,v75
v_fma_f32 v104,v76,v104,v75
v_fma_f32 v105,v76,v105,v75
v_mul_f32 v102,v26,v102
v_mul_f32 v103,v27,v103
v_mul_f32 v104,v28,v104
v_mul_f32 v105,v29,v105
v_exp_f32 v102,v102
v_exp_f32 v103,v103
v_exp_f32 v104,v104
v_exp_f32 v105,v105
v_fma_f32 v102,v102,v77,v77
v_fma_f32 v103,v103,v77,v77
v_fma_f32 v104,v104,v77,v77
v_fma_f32 v105,v105,v77,v77
v_rcp_f32 v102,v102
v_rcp_f32 v103,v103
v_rcp_f32 v104,v104
v_rcp_f32 v105,v105
v_mul_f32 v26,v26,v102
v_mul_f32 v27,v27,v103
v_mul_f32 v28,v28,v104
v_mul_f32 v29,v29,v105
v_cvt_pk_bf16_f32 v110,v26,v27
v_cvt_pk_bf16_f32 v111,v28,v29
buffer_store_dwordx2 v[110:111],v10,s[36:39],s66 offen offset:0 sc0 sc1
s_mov_b32 s72,s69
s_mov_b32 s69,0x7fff0000
s_mov_b32 s66,0x7fff0000
s_mov_b32 s76,0x7fff0000
s_branch Lg3_gox_%=
Lg3_xn0_%=:
s_cmp_ge_u32 s54,256
s_cbranch_scc1 Lg3_fin0_%=
s_add_u32 m0,s59,4096
s_nop 0
buffer_load_dword v8,s[36:39],s58 offen offset:0 sc1 lds
buffer_store_dword v4,v3,s[36:39],s71 offen offset:0 sc0 sc1
s_add_u32 m0,s59,5120
s_nop 0
buffer_load_dword v8,s[36:39],s58 offen offset:0 sc1 lds
buffer_store_dword v4,v3,s[36:39],s71 offen offset:0 sc0 sc1
s_mov_b32 s83,0
L 0
v_fma_f32 v18,-v74,v82,v18
v_fma_f32 v19,-v74,v83,v19
v_fma_f32 v20,-v74,v84,v20
v_fma_f32 v21,-v74,v85,v21
v_mul_f32 v94,v18,v18
v_mul_f32 v95,v19,v19
v_mul_f32 v96,v20,v20
v_mul_f32 v97,v21,v21
v_fma_f32 v94,v76,v94,v75
v_fma_f32 v95,v76,v95,v75
v_fma_f32 v96,v76,v96,v75
v_fma_f32 v97,v76,v97,v75
v_mul_f32 v94,v18,v94
v_mul_f32 v95,v19,v95
v_mul_f32 v96,v20,v96
v_mul_f32 v97,v21,v97
v_exp_f32 v94,v94
v_exp_f32 v95,v95
v_exp_f32 v96,v96
v_exp_f32 v97,v97
v_fma_f32 v94,v94,v77,v77
v_fma_f32 v95,v95,v77,v77
v_fma_f32 v96,v96,v77,v77
v_fma_f32 v97,v97,v77,v77
v_rcp_f32 v94,v94
v_rcp_f32 v95,v95
v_rcp_f32 v96,v96
v_rcp_f32 v97,v97
v_mul_f32 v18,v18,v94
v_mul_f32 v19,v19,v95
v_mul_f32 v20,v20,v96
v_mul_f32 v21,v21,v97
v_cvt_pk_bf16_f32 v106,v18,v19
v_cvt_pk_bf16_f32 v107,v20,v21
v_fma_f32 v22,-v74,v86,v22
v_fma_f32 v23,-v74,v87,v23
v_fma_f32 v24,-v74,v88,v24
v_fma_f32 v25,-v74,v89,v25
v_mul_f32 v98,v22,v22
v_mul_f32 v99,v23,v23
v_mul_f32 v100,v24,v24
v_mul_f32 v101,v25,v25
v_fma_f32 v98,v76,v98,v75
v_fma_f32 v99,v76,v99,v75
v_fma_f32 v100,v76,v100,v75
v_fma_f32 v101,v76,v101,v75
v_mul_f32 v98,v22,v98
v_mul_f32 v99,v23,v99
v_mul_f32 v100,v24,v100
v_mul_f32 v101,v25,v101
v_exp_f32 v98,v98
v_exp_f32 v99,v99
v_exp_f32 v100,v100
v_exp_f32 v101,v101
v_fma_f32 v98,v98,v77,v77
v_fma_f32 v99,v99,v77,v77
v_fma_f32 v100,v100,v77,v77
v_fma_f32 v101,v101,v77,v77
v_rcp_f32 v98,v98
v_rcp_f32 v99,v99
v_rcp_f32 v100,v100
v_rcp_f32 v101,v101
v_mul_f32 v22,v22,v98
v_mul_f32 v23,v23,v99
v_mul_f32 v24,v24,v100
v_mul_f32 v25,v25,v101
v_cvt_pk_bf16_f32 v108,v22,v23
v_cvt_pk_bf16_f32 v109,v24,v25
buffer_store_dwordx4 v[106:109],v0,s[36:39],s66 offen offset:0 sc0 sc1
v_fma_f32 v26,-v74,v90,v26
v_fma_f32 v27,-v74,v91,v27
v_fma_f32 v28,-v74,v92,v28
v_fma_f32 v29,-v74,v93,v29
v_mul_f32 v102,v26,v26
v_mul_f32 v103,v27,v27
v_mul_f32 v104,v28,v28
v_mul_f32 v105,v29,v29
v_fma_f32 v102,v76,v102,v75
v_fma_f32 v103,v76,v103,v75
v_fma_f32 v104,v76,v104,v75
v_fma_f32 v105,v76,v105,v75
v_mul_f32 v102,v26,v102
v_mul_f32 v103,v27,v103
v_mul_f32 v104,v28,v104
v_mul_f32 v105,v29,v105
v_exp_f32 v102,v102
v_exp_f32 v103,v103
v_exp_f32 v104,v104
v_exp_f32 v105,v105
v_fma_f32 v102,v102,v77,v77
v_fma_f32 v103,v103,v77,v77
v_fma_f32 v104,v104,v77,v77
v_fma_f32 v105,v105,v77,v77
v_rcp_f32 v102,v102
v_rcp_f32 v103,v103
v_rcp_f32 v104,v104
v_rcp_f32 v105,v105
v_mul_f32 v26,v26,v102
v_mul_f32 v27,v27,v103
v_mul_f32 v28,v28,v104
v_mul_f32 v29,v29,v105
v_cvt_pk_bf16_f32 v110,v26,v27
v_cvt_pk_bf16_f32 v111,v28,v29
buffer_store_dwordx2 v[110:111],v10,s[36:39],s66 offen offset:0 sc0 sc1
s_mov_b32 s72,s69
s_mov_b32 s69,0x7fff0000
s_mov_b32 s66,0x7fff0000
s_mov_b32 s76,0x7fff0000
s_branch Lg3_poll_%=
Lg3_fin0_%=:
L 0
v_fma_f32 v18,-v74,v82,v18
v_fma_f32 v19,-v74,v83,v19
v_fma_f32 v20,-v74,v84,v20
v_fma_f32 v21,-v74,v85,v21
v_mul_f32 v94,v18,v18
v_mul_f32 v95,v19,v19
v_mul_f32 v96,v20,v20
v_mul_f32 v97,v21,v21
v_fma_f32 v94,v76,v94,v75
v_fma_f32 v95,v76,v95,v75
v_fma_f32 v96,v76,v96,v75
v_fma_f32 v97,v76,v97,v75
v_mul_f32 v94,v18,v94
v_mul_f32 v95,v19,v95
v_mul_f32 v96,v20,v96
v_mul_f32 v97,v21,v97
v_exp_f32 v94,v94
v_exp_f32 v95,v95
v_exp_f32 v96,v96
v_exp_f32 v97,v97
v_fma_f32 v94,v94,v77,v77
v_fma_f32 v95,v95,v77,v77
v_fma_f32 v96,v96,v77,v77
v_fma_f32 v97,v97,v77,v77
v_rcp_f32 v94,v94
v_rcp_f32 v95,v95
v_rcp_f32 v96,v96
v_rcp_f32 v97,v97
v_mul_f32 v18,v18,v94
v_mul_f32 v19,v19,v95
v_mul_f32 v20,v20,v96
v_mul_f32 v21,v21,v97
v_cvt_pk_bf16_f32 v106,v18,v19
v_cvt_pk_bf16_f32 v107,v20,v21
v_fma_f32 v22,-v74,v86,v22
v_fma_f32 v23,-v74,v87,v23
v_fma_f32 v24,-v74,v88,v24
v_fma_f32 v25,-v74,v89,v25
v_mul_f32 v98,v22,v22
v_mul_f32 v99,v23,v23
v_mul_f32 v100,v24,v24
v_mul_f32 v101,v25,v25
v_fma_f32 v98,v76,v98,v75
v_fma_f32 v99,v76,v99,v75
v_fma_f32 v100,v76,v100,v75
v_fma_f32 v101,v76,v101,v75
v_mul_f32 v98,v22,v98
v_mul_f32 v99,v23,v99
v_mul_f32 v100,v24,v100
v_mul_f32 v101,v25,v101
v_exp_f32 v98,v98
v_exp_f32 v99,v99
v_exp_f32 v100,v100
v_exp_f32 v101,v101
v_fma_f32 v98,v98,v77,v77
v_fma_f32 v99,v99,v77,v77
v_fma_f32 v100,v100,v77,v77
v_fma_f32 v101,v101,v77,v77
v_rcp_f32 v98,v98
v_rcp_f32 v99,v99
v_rcp_f32 v100,v100
v_rcp_f32 v101,v101
v_mul_f32 v22,v22,v98
v_mul_f32 v23,v23,v99
v_mul_f32 v24,v24,v100
v_mul_f32 v25,v25,v101
v_cvt_pk_bf16_f32 v108,v22,v23
v_cvt_pk_bf16_f32 v109,v24,v25
buffer_store_dwordx4 v[106:109],v0,s[36:39],s66 offen offset:0 sc0 sc1
v_fma_f32 v26,-v74,v90,v26
v_fma_f32 v27,-v74,v91,v27
v_fma_f32 v28,-v74,v92,v28
v_fma_f32 v29,-v74,v93,v29
v_mul_f32 v102,v26,v26
v_mul_f32 v103,v27,v27
v_mul_f32 v104,v28,v28
v_mul_f32 v105,v29,v29
v_fma_f32 v102,v76,v102,v75
v_fma_f32 v103,v76,v103,v75
v_fma_f32 v104,v76,v104,v75
v_fma_f32 v105,v76,v105,v75
v_mul_f32 v102,v26,v102
v_mul_f32 v103,v27,v103
v_mul_f32 v104,v28,v104
v_mul_f32 v105,v29,v105
v_exp_f32 v102,v102
v_exp_f32 v103,v103
v_exp_f32 v104,v104
v_exp_f32 v105,v105
v_fma_f32 v102,v102,v77,v77
v_fma_f32 v103,v103,v77,v77
v_fma_f32 v104,v104,v77,v77
v_fma_f32 v105,v105,v77,v77
v_rcp_f32 v102,v102
v_rcp_f32 v103,v103
v_rcp_f32 v104,v104
v_rcp_f32 v105,v105
v_mul_f32 v26,v26,v102
v_mul_f32 v27,v27,v103
v_mul_f32 v28,v28,v104
v_mul_f32 v29,v29,v105
v_cvt_pk_bf16_f32 v110,v26,v27
v_cvt_pk_bf16_f32 v111,v28,v29
buffer_store_dwordx2 v[110:111],v10,s[36:39],s66 offen offset:0 sc0 sc1
s_mov_b32 s72,s69
s_mov_b32 s69,0x7fff0000
s_mov_b32 s66,0x7fff0000
s_mov_b32 s76,0x7fff0000
s_branch Lg3_done_%=
Lg3_K1_%=:
Lg3_L1_%=:
v_mfma_f32_16x16x32_bf16 v[38:41],a[8:11],v[42:45],0
s_add_u32 m0,s59,3072
R 62,2,5120
v_add_f32_dpp v115,v114,v114 quad_perm:[1,0,3,2] row_mask:0xf bank_mask:0xf
M 38,a[20:23],46
buffer_load_dword v8,s[36:39],s58 offen offset:0 sc1 lds
v_mfma_f32_16x16x32_bf16 v[34:37],a[4:7],v[42:45],0
s_mov_b32 s67,s65
v_add_f32_dpp v115,v115,v115 quad_perm:[2,3,0,1] row_mask:0xf bank_mask:0xf
v_fma_f32 v18,-v74,v82,v18
M 34,a[16:19],46
s_add_u32 s65,s65,s62
R 66,2,6144
v_fma_f32 v19,-v74,v83,v19
v_mfma_f32_16x16x32_bf16 v[30:33],a[0:3],v[42:45],0
v_add_f32_dpp v118,v115,v115 row_half_mirror row_mask:0xf bank_mask:0xf
v_fma_f32 v20,-v74,v84,v20
v_fma_f32 v21,-v74,v85,v21
M 30,a[12:15],46
L 3
v_mul_f32 v94,v18,v18
v_mul_f32 v95,v19,v19
M 30,a[24:27],50
R 70,2,7168
v_mov_b32_dpp v115,v118 row_ror:8 row_mask:0xf bank_mask:0xf
M 30,a[36:39],54
v_fma_f32 v115,-v118,v118,v115
v_max_f32 v115,s87,v115
M 34,a[28:31],50
v_rsq_f32 v116,v115
M 34,a[40:43],54
R 42,2,8192
v_fma_f32 v115,v15,v115,v16
v_mul_f32 v118,v17,v118
M 38,a[32:35],50
v_mul_f32 v119,v116,v116
v_fma_f32 v115,v14,v119,v115
M 38,a[44:47],54
L 3
v_fma_f32 v118,v116,v115,v118
v_mul_f32 v96,v20,v20
M 38,a[56:59],58
R 46,2,9216
v_mul_f32 v97,v21,v21
M 38,a[68:71],62
ds_write_b32 v12,v118 offset:2048
M 34,a[52:55],58
v_fma_f32 v94,v76,v94,v75
v_fma_f32 v95,v76,v95,v75
v_fma_f32 v96,v76,v96,v75
M 34,a[64:67],62
R 50,2,10240
v_fma_f32 v97,v76,v97,v75
M 30,a[48:51],58
v_mul_f32 v94,v18,v94
v_mul_f32 v95,v19,v95
M 30,a[60:63],62
L 4
v_exp_f32 v94,v94
M 30,a[72:75],66
R 54,2,11264
v_mul_f32 v96,v20,v96
M 30,a[84:87],70
v_exp_f32 v95,v95
v_mul_f32 v97,v21,v97
M 34,a[76:79],66
v_exp_f32 v96,v96
M 34,a[88:91],70
R 58,2,12288
v_fma_f32 v94,v94,v77,v77
M 38,a[80:83],66
v_exp_f32 v97,v97
M 38,a[92:95],70
L 4
v_rcp_f32 v94,v94
M 38,a[104:107],42
R 62,2,13312
v_fma_f32 v95,v95,v77,v77
v_fma_f32 v96,v96,v77,v77
M 38,a[116:119],46
v_rcp_f32 v95,v95
M 34,a[100:103],42
v_rcp_f32 v96,v96
M 34,a[112:115],46
R 66,2,14336
v_fma_f32 v97,v97,v77,v77
M 30,a[96:99],42
v_rcp_f32 v97,v97
M 30,a[108:111],46
L 3
v_mul_f32 v18,v18,v94
v_mul_f32 v19,v19,v95
v_mul_f32 v20,v20,v96
M 30,a[120:123],50
R 70,2,15360
v_mul_f32 v21,v21,v97
M 30,a[132:135],54
s_waitcnt vmcnt(0)
v_cvt_pk_bf16_f32 v106,v18,v19
v_cvt_pk_bf16_f32 v107,v20,v21
M 34,a[124:127],50
s_barrier
ds_read_b32 v7,v6 offset:3072
v_fma_f32 v22,-v74,v86,v22
M 34,a[136:139],54
R 42,2,16384
v_fma_f32 v23,-v74,v87,v23
M 38,a[128:131],50
buffer_store_dword v4,v3,s[36:39],s70 offen offset:0 sc0 sc1
v_fma_f32 v24,-v74,v88,v24
M 38,a[140:143],54
L 4
v_fma_f32 v25,-v74,v89,v25
v_mul_f32 v98,v22,v22
M 38,a[152:155],58
s_mov_b32 s70,0x7fff0000
R 46,2,17408
v_mul_f32 v99,v23,v23
M 38,a[164:167],62
v_mul_f32 v100,v24,v24
v_mul_f32 v101,v25,v25
M 34,a[148:151],58
L 2
v_cmp_eq_u32 vcc,s53,v7
v_fma_f32 v98,v76,v98,v75
M 34,a[160:163],62
R 50,2,18432
v_fma_f32 v99,v76,v99,v75
v_fma_f32 v100,v76,v100,v75
M 30,a[144:147],58
v_fma_f32 v101,v76,v101,v75
v_mul_f32 v98,v22,v98
M 30,a[156:159],62
s_cmp_eq_u32 vcc_lo,-1
s_nop 0
v_mul_f32 v99,v23,v99
s_cbranch_scc0 Lg3_X1_%=
M 30,a[168:171],66
s_add_u32 m0,s57,0
s_add_u32 s56,s55,0
s_add_u32 s58,s58,s61
R 54,2,19456
M 30,a[180:183],70
buffer_load_dword v114,v11,s[36:39],s73 offen offset:0 sc1
M 34,a[172:175],66
D 0,56,0
v_mul_f32 v100,v24,v100
M 34,a[184:187],70
s_add_u32 s54,s54,s46
R 58,2,20480
v_mul_f32 v101,v25,v101
M 38,a[176:179],66
s_add_u32 s73,s73,s64
v_exp_f32 v98,v98
M 38,a[188:191],70
L 3
v_exp_f32 v99,v99
M 38,a[200:203],42
R 62,2,21504
v_fma_f32 v98,v98,v77,v77
M 38,a[212:215],46
D 0,56,1024
v_fma_f32 v99,v99,v77,v77
M 34,a[196:199],42
v_exp_f32 v100,v100
M 34,a[208:211],46
R 66,2,22528
v_fma_f32 v100,v100,v77,v77
M 30,a[192:195],42
D 0,56,2048
M 30,a[204:207],46
L 3
v_exp_f32 v101,v101
M 30,a[216:219],50
R 70,2,23552
)"
R"(v_rcp_f32 v98,v98
M 30,a[228:231],54
v_rcp_f32 v99,v99
M 34,a[220:223],50
D 0,56,3072
M 34,a[232:235],54
s_add_u32 m0,s59,1024
R 42,2,24576
v_fma_f32 v101,v101,v77,v77
M 38,a[224:227],50
buffer_load_dword v8,s[36:39],s58 offen offset:0 sc1 lds
M 38,a[236:239],54
L 3
v_rcp_f32 v100,v100
v_mul_f32 v22,v22,v98
M 38,a[248:251],58
s_add_u32 m0,s57,16384
R 46,2,25600
v_mul_f32 v23,v23,v99
M 38,v[132:135],62
s_add_u32 s56,s55,16384
v_rcp_f32 v101,v101
M 34,a[244:247],58
D 0,56,0
M 34,v[128:131],62
s_add_u32 s55,s55,s60
R 50,2,26624
v_mul_f32 v24,v24,v100
M 30,a[240:243],58
s_sub_u32 s57,s84,s57
v_mul_f32 v25,v25,v101
v_cvt_pk_bf16_f32 v108,v22,v23
v_cvt_pk_bf16_f32 v109,v24,v25
M 30,a[252:255],62
L 3
buffer_store_dwordx4 v[106:109],v0,s[36:39],s66 offen offset:0 sc0 sc1
v_fma_f32 v26,-v74,v90,v26
M 30,v[136:139],66
R 54,2,27648
v_fma_f32 v27,-v74,v91,v27
M 30,v[148:151],70
v_fma_f32 v28,-v74,v92,v28
v_fma_f32 v29,-v74,v93,v29
M 34,v[140:143],66
D 0,56,1024
M 34,v[152:155],70
R 58,2,28672
v_mul_f32 v102,v26,v26
v_mul_f32 v103,v27,v27
M 38,v[144:147],66
v_mul_f32 v104,v28,v28
v_mul_f32 v105,v29,v29
M 38,v[156:159],70
L 3
v_fma_f32 v102,v76,v102,v75
v_fma_f32 v103,v76,v103,v75
M 38,v[168:171],42
R 62,2,29696
v_fma_f32 v104,v76,v104,v75
M 38,v[180:183],46
D 0,56,2048
M 34,v[164:167],42
v_fma_f32 v105,v76,v105,v75
v_mul_f32 v102,v26,v102
v_mul_f32 v103,v27,v103
M 34,v[176:179],46
R 66,2,30720
v_mul_f32 v104,v28,v104
M 30,v[160:163],42
v_exp_f32 v102,v102
M 30,v[172:175],46
L 3
D 0,56,3072
M 30,v[184:187],50
s_nop 0
R 70,2,31744
v_mul_f32 v105,v29,v105
M 30,v[196:199],54
s_waitcnt vmcnt(5)
v_exp_f32 v103,v103
v_fma_f32 v102,v102,v77,v77
M 34,v[188:191],50
s_barrier
buffer_store_dword v4,v3,s[36:39],s70 offen offset:0 sc0 sc1
M 34,v[200:203],54
s_mov_b32 s70,s69
R 42,1,0
ds_read_b32 v7,v6 offset:1024
M 38,v[192:195],50
s_mov_b32 s69,s68
R 78,13,2048
v_fma_f32 v103,v103,v77,v77
M 38,v[204:207],54
L 5
v_exp_f32 v104,v104
M 38,v[216:219],58
s_add_u32 s68,s68,s63
R 46,1,1024
v_exp_f32 v105,v105
M 38,v[228:231],62
v_rcp_f32 v102,v102
M 34,v[212:215],58
v_rcp_f32 v103,v103
M 34,v[224:227],62
L 2
R 50,1,2048
v_fma_f32 v104,v104,v77,v77
M 30,v[208:211],58
v_rcp_f32 v104,v104
M 30,v[220:223],62
s_nop 0
v_fma_f32 v105,v105,v77,v77
v_rcp_f32 v105,v105
M 30,v[232:235],66
R 54,1,3072
v_mul_f32 v26,v26,v102
M 30,v[244:247],70
v_mul_f32 v27,v27,v103
v_mul_f32 v28,v28,v104
M 34,v[236:239],66
v_cmp_eq_u32 vcc,s53,v7
v_mul_f32 v29,v29,v105
M 34,v[248:251],70
R 58,1,4096
v_cvt_pk_bf16_f32 v110,v26,v27
v_cvt_pk_bf16_f32 v111,v28,v29
M 38,v[240:243],66
L 3
s_cmp_eq_u32 vcc_lo,-1
buffer_store_dwordx2 v[110:111],v10,s[36:39],s66 offen offset:0 sc0 sc1
M 38,v[252:255],70
buffer_store_dword v4,v3,s[36:39],s71 offen offset:0 sc0 sc1
s_cbranch_scc1 Lg3_F0_%=
s_branch Lg3_K0_%=
Lg3_X1_%=:
M 30,a[168:171],66
s_add_u32 m0,s59,1024
R 54,2,19456
v_mul_f32 v100,v24,v100
M 30,a[180:183],70
buffer_load_dword v8,s[36:39],s58 offen offset:0 sc1 lds
M 34,a[172:175],66
v_exp_f32 v98,v98
M 34,a[184:187],70
R 58,2,20480
v_mul_f32 v101,v25,v101
M 38,a[176:179],66
v_exp_f32 v99,v99
M 38,a[188:191],70
L 3
v_exp_f32 v100,v100
M 38,a[200:203],42
R 62,2,21504
v_fma_f32 v98,v98,v77,v77
M 38,a[212:215],46
v_exp_f32 v101,v101
M 34,a[196:199],42
v_rcp_f32 v98,v98
M 34,a[208:211],46
R 66,2,22528
v_fma_f32 v99,v99,v77,v77
M 30,a[192:195],42
v_rcp_f32 v99,v99
M 30,a[204:207],46
L 3
v_fma_f32 v100,v100,v77,v77
v_fma_f32 v101,v101,v77,v77
M 30,a[216:219],50
R 70,2,23552
v_mul_f32 v22,v22,v98
M 30,a[228:231],54
v_rcp_f32 v100,v100
M 34,a[220:223],50
v_rcp_f32 v101,v101
M 34,a[232:235],54
R 42,2,24576
v_mul_f32 v23,v23,v99
M 38,a[224:227],50
v_mul_f32 v24,v24,v100
v_mul_f32 v25,v25,v101
M 38,a[236:239],54
L 3
v_cvt_pk_bf16_f32 v108,v22,v23
v_cvt_pk_bf16_f32 v109,v24,v25
M 38,a[248:251],58
R 46,2,25600
v_fma_f32 v26,-v74,v90,v26
M 38,v[132:135],62
buffer_store_dwordx4 v[106:109],v0,s[36:39],s66 offen offset:0 sc0 sc1
v_fma_f32 v27,-v74,v91,v27
M 34,a[244:247],58
v_fma_f32 v28,-v74,v92,v28
v_fma_f32 v29,-v74,v93,v29
M 34,v[128:131],62
R 50,2,26624
v_mul_f32 v102,v26,v26
M 30,a[240:243],58
v_mul_f32 v103,v27,v27
v_mul_f32 v104,v28,v28
M 30,a[252:255],62
L 3
v_mul_f32 v105,v29,v29
v_fma_f32 v102,v76,v102,v75
M 30,v[136:139],66
R 54,2,27648
v_fma_f32 v103,v76,v103,v75
M 30,v[148:151],70
v_fma_f32 v104,v76,v104,v75
v_fma_f32 v105,v76,v105,v75
M 34,v[140:143],66
v_mul_f32 v102,v26,v102
v_mul_f32 v103,v27,v103
M 34,v[152:155],70
R 58,2,28672
v_mul_f32 v104,v28,v104
M 38,v[144:147],66
v_exp_f32 v102,v102
M 38,v[156:159],70
L 3
v_exp_f32 v103,v103
M 38,v[168:171],42
R 62,2,29696
v_mul_f32 v105,v29,v105
M 38,v[180:183],46
v_exp_f32 v104,v104
M 34,v[164:167],42
v_exp_f32 v105,v105
M 34,v[176:179],46
R 66,2,30720
v_fma_f32 v102,v102,v77,v77
M 30,v[160:163],42
v_rcp_f32 v102,v102
M 30,v[172:175],46
L 3
v_fma_f32 v103,v103,v77,v77
v_fma_f32 v104,v104,v77,v77
M 30,v[184:187],50
s_nop 0
R 70,2,31744
v_fma_f32 v105,v105,v77,v77
M 30,v[196:199],54
s_waitcnt vmcnt(1)
v_rcp_f32 v103,v103
M 34,v[188:191],50
s_barrier
buffer_store_dword v4,v3,s[36:39],s70 offen offset:0 sc0 sc1
M 34,v[200:203],54
s_mov_b32 s70,s69
ds_read_b32 v7,v6 offset:1024
R 78,13,2048
M 38,v[192:195],50
s_mov_b32 s69,s68
v_rcp_f32 v104,v104
M 38,v[204:207],54
L 4
v_rcp_f32 v105,v105
M 38,v[216:219],58
s_add_u32 s68,s68,s63
v_mul_f32 v26,v26,v102
v_mul_f32 v27,v27,v103
M 38,v[228:231],62
v_mul_f32 v28,v28,v104
v_mul_f32 v29,v29,v105
M 34,v[212:215],58
v_cvt_pk_bf16_f32 v110,v26,v27
v_cvt_pk_bf16_f32 v111,v28,v29
M 34,v[224:227],62
L 1
buffer_store_dwordx2 v[110:111],v10,s[36:39],s66 offen offset:0 sc0 sc1
M 30,v[208:211],58
v_cmp_eq_u32 vcc,s53,v7
M 30,v[220:223],62
s_nop 0
M 30,v[232:235],66
M 30,v[244:247],70
M 34,v[236:239],66
M 34,v[248:251],70
M 38,v[240:243],66
M 38,v[252:255],70
s_mov_b32 s80,0
s_cmp_eq_u32 vcc_lo,-1
s_cbranch_scc0 Lg3_xn1_%=
buffer_load_dword v114,v11,s[36:39],s73 offen offset:0 sc1
L 0
v_fma_f32 v30,-v78,v82,v30
v_fma_f32 v31,-v78,v83,v31
v_fma_f32 v32,-v78,v84,v32
v_fma_f32 v33,-v78,v85,v33
v_mul_f32 v94,v30,v30
v_mul_f32 v95,v31,v31
v_mul_f32 v96,v32,v32
v_mul_f32 v97,v33,v33
s_add_u32 s73,s73,s64
s_add_u32 m0,s57,0
s_add_u32 s56,s55,0
D 0,56,0
v_fma_f32 v94,v80,v94,v79
v_fma_f32 v95,v80,v95,v79
v_fma_f32 v96,v80,v96,v79
v_fma_f32 v97,v80,v97,v79
v_mul_f32 v94,v30,v94
v_mul_f32 v95,v31,v95
v_mul_f32 v96,v32,v96
v_mul_f32 v97,v33,v97
D 0,56,1024
v_exp_f32 v94,v94
v_exp_f32 v95,v95
v_exp_f32 v96,v96
v_exp_f32 v97,v97
v_fma_f32 v94,v94,v81,v81
v_fma_f32 v95,v95,v81,v81
v_fma_f32 v96,v96,v81,v81
v_fma_f32 v97,v97,v81,v81
D 0,56,2048
v_rcp_f32 v94,v94
v_rcp_f32 v95,v95
v_rcp_f32 v96,v96
v_rcp_f32 v97,v97
v_mul_f32 v30,v30,v94
v_mul_f32 v31,v31,v95
v_mul_f32 v32,v32,v96
v_mul_f32 v33,v33,v97
D 0,56,3072
v_cvt_pk_bf16_f32 v106,v30,v31
v_cvt_pk_bf16_f32 v107,v32,v33
v_fma_f32 v34,-v78,v86,v34
v_fma_f32 v35,-v78,v87,v35
v_fma_f32 v36,-v78,v88,v36
v_fma_f32 v37,-v78,v89,v37
v_mul_f32 v98,v34,v34
v_mul_f32 v99,v35,v35
s_add_u32 s58,s58,s61
s_add_u32 m0,s57,16384
s_add_u32 s56,s55,16384
D 0,56,0
v_mul_f32 v100,v36,v36
v_mul_f32 v101,v37,v37
v_fma_f32 v98,v80,v98,v79
v_fma_f32 v99,v80,v99,v79
v_fma_f32 v100,v80,v100,v79
v_fma_f32 v101,v80,v101,v79
v_mul_f32 v98,v34,v98
v_mul_f32 v99,v35,v99
D 0,56,1024
v_mul_f32 v100,v36,v100
v_mul_f32 v101,v37,v101
v_exp_f32 v98,v98
v_exp_f32 v99,v99
v_exp_f32 v100,v100
v_exp_f32 v101,v101
v_fma_f32 v98,v98,v81,v81
v_fma_f32 v99,v99,v81,v81
D 0,56,2048
v_fma_f32 v100,v100,v81,v81
v_fma_f32 v101,v101,v81,v81
v_rcp_f32 v98,v98
v_rcp_f32 v99,v99
v_rcp_f32 v100,v100
v_rcp_f32 v101,v101
v_mul_f32 v34,v34,v98
v_mul_f32 v35,v35,v99
D 0,56,3072
v_mul_f32 v36,v36,v100
v_mul_f32 v37,v37,v101
v_cvt_pk_bf16_f32 v108,v34,v35
v_cvt_pk_bf16_f32 v109,v36,v37
buffer_store_dwordx4 v[106:109],v0,s[36:39],s67 offen offset:0 sc0 sc1
v_fma_f32 v38,-v78,v90,v38
v_fma_f32 v39,-v78,v91,v39
v_fma_f32 v40,-v78,v92,v40
v_fma_f32 v41,-v78,v93,v41
s_add_u32 s55,s55,s60
s_sub_u32 s57,s84,s57
s_add_u32 s54,s54,s46
v_mul_f32 v102,v38,v38
v_mul_f32 v103,v39,v39
v_mul_f32 v104,v40,v40
v_mul_f32 v105,v41,v41
v_fma_f32 v102,v80,v102,v79
v_fma_f32 v103,v80,v103,v79
v_fma_f32 v104,v80,v104,v79
v_fma_f32 v105,v80,v105,v79
v_mul_f32 v102,v38,v102
v_mul_f32 v103,v39,v103
v_mul_f32 v104,v40,v104
v_mul_f32 v105,v41,v105
v_exp_f32 v102,v102
v_exp_f32 v103,v103
v_exp_f32 v104,v104
v_exp_f32 v105,v105
v_fma_f32 v102,v102,v81,v81
v_fma_f32 v103,v103,v81,v81
v_fma_f32 v104,v104,v81,v81
v_fma_f32 v105,v105,v81,v81
v_rcp_f32 v102,v102
v_rcp_f32 v103,v103
v_rcp_f32 v104,v104
v_rcp_f32 v105,v105
v_mul_f32 v38,v38,v102
v_mul_f32 v39,v39,v103
v_mul_f32 v40,v40,v104
v_mul_f32 v41,v41,v105
v_cvt_pk_bf16_f32 v110,v38,v39
v_cvt_pk_bf16_f32 v111,v40,v41
buffer_store_dwordx2 v[110:111],v10,s[36:39],s67 offen offset:0 sc0 sc1
s_mov_b32 s72,s69
s_mov_b32 s69,0x7fff0000
s_mov_b32 s67,0x7fff0000
s_mov_b32 s77,0x7fff0000
s_branch Lg3_gox_%=
Lg3_xn1_%=:
s_cmp_ge_u32 s54,256
s_cbranch_scc1 Lg3_fin1_%=
s_add_u32 m0,s59,4096
s_nop 0
buffer_load_dword v8,s[36:39],s58 offen offset:0 sc1 lds
buffer_store_dword v4,v3,s[36:39],s71 offen offset:0 sc0 sc1
s_add_u32 m0,s59,5120
s_nop 0
buffer_load_dword v8,s[36:39],s58 offen offset:0 sc1 lds
buffer_store_dword v4,v3,s[36:39],s71 offen offset:0 sc0 sc1
s_mov_b32 s83,0
L 0
v_fma_f32 v30,-v78,v82,v30
v_fma_f32 v31,-v78,v83,v31
v_fma_f32 v32,-v78,v84,v32
v_fma_f32 v33,-v78,v85,v33
v_mul_f32 v94,v30,v30
v_mul_f32 v95,v31,v31
v_mul_f32 v96,v32,v32
v_mul_f32 v97,v33,v33
v_fma_f32 v94,v80,v94,v79
v_fma_f32 v95,v80,v95,v79
v_fma_f32 v96,v80,v96,v79
v_fma_f32 v97,v80,v97,v79
v_mul_f32 v94,v30,v94
v_mul_f32 v95,v31,v95
v_mul_f32 v96,v32,v96
v_mul_f32 v97,v33,v97
v_exp_f32 v94,v94
v_exp_f32 v95,v95
v_exp_f32 v96,v96
v_exp_f32 v97,v97
v_fma_f32 v94,v94,v81,v81
v_fma_f32 v95,v95,v81,v81
v_fma_f32 v96,v96,v81,v81
v_fma_f32 v97,v97,v81,v81
v_rcp_f32 v94,v94
v_rcp_f32 v95,v95
v_rcp_f32 v96,v96
v_rcp_f32 v97,v97
v_mul_f32 v30,v30,v94
v_mul_f32 v31,v31,v95
v_mul_f32 v32,v32,v96
v_mul_f32 v33,v33,v97
v_cvt_pk_bf16_f32 v106,v30,v31
v_cvt_pk_bf16_f32 v107,v32,v33
v_fma_f32 v34,-v78,v86,v34
v_fma_f32 v35,-v78,v87,v35
v_fma_f32 v36,-v78,v88,v36
v_fma_f32 v37,-v78,v89,v37
v_mul_f32 v98,v34,v34
v_mul_f32 v99,v35,v35
v_mul_f32 v100,v36,v36
v_mul_f32 v101,v37,v37
v_fma_f32 v98,v80,v98,v79
v_fma_f32 v99,v80,v99,v79
v_fma_f32 v100,v80,v100,v79
v_fma_f32 v101,v80,v101,v79
v_mul_f32 v98,v34,v98
v_mul_f32 v99,v35,v99
v_mul_f32 v100,v36,v100
v_mul_f32 v101,v37,v101
v_exp_f32 v98,v98
v_exp_f32 v99,v99
v_exp_f32 v100,v100
v_exp_f32 v101,v101
v_fma_f32 v98,v98,v81,v81
v_fma_f32 v99,v99,v81,v81
v_fma_f32 v100,v100,v81,v81
v_fma_f32 v101,v101,v81,v81
v_rcp_f32 v98,v98
v_rcp_f32 v99,v99
v_rcp_f32 v100,v100
v_rcp_f32 v101,v101
v_mul_f32 v34,v34,v98
v_mul_f32 v35,v35,v99
v_mul_f32 v36,v36,v100
v_mul_f32 v37,v37,v101
v_cvt_pk_bf16_f32 v108,v34,v35
v_cvt_pk_bf16_f32 v109,v36,v37
buffer_store_dwordx4 v[106:109],v0,s[36:39],s67 offen offset:0 sc0 sc1
v_fma_f32 v38,-v78,v90,v38
v_fma_f32 v39,-v78,v91,v39
v_fma_f32 v40,-v78,v92,v40
v_fma_f32 v41,-v78,v93,v41
v_mul_f32 v102,v38,v38
v_mul_f32 v103,v39,v39
v_mul_f32 v104,v40,v40
v_mul_f32 v105,v41,v41
)"
R"(v_fma_f32 v102,v80,v102,v79
v_fma_f32 v103,v80,v103,v79
v_fma_f32 v104,v80,v104,v79
v_fma_f32 v105,v80,v105,v79
v_mul_f32 v102,v38,v102
v_mul_f32 v103,v39,v103
v_mul_f32 v104,v40,v104
v_mul_f32 v105,v41,v105
v_exp_f32 v102,v102
v_exp_f32 v103,v103
v_exp_f32 v104,v104
v_exp_f32 v105,v105
v_fma_f32 v102,v102,v81,v81
v_fma_f32 v103,v103,v81,v81
v_fma_f32 v104,v104,v81,v81
v_fma_f32 v105,v105,v81,v81
v_rcp_f32 v102,v102
v_rcp_f32 v103,v103
v_rcp_f32 v104,v104
v_rcp_f32 v105,v105
v_mul_f32 v38,v38,v102
v_mul_f32 v39,v39,v103
v_mul_f32 v40,v40,v104
v_mul_f32 v41,v41,v105
v_cvt_pk_bf16_f32 v110,v38,v39
v_cvt_pk_bf16_f32 v111,v40,v41
buffer_store_dwordx2 v[110:111],v10,s[36:39],s67 offen offset:0 sc0 sc1
s_mov_b32 s72,s69
s_mov_b32 s69,0x7fff0000
s_mov_b32 s67,0x7fff0000
s_mov_b32 s77,0x7fff0000
s_branch Lg3_poll_%=
Lg3_fin1_%=:
L 0
v_fma_f32 v30,-v78,v82,v30
v_fma_f32 v31,-v78,v83,v31
v_fma_f32 v32,-v78,v84,v32
v_fma_f32 v33,-v78,v85,v33
v_mul_f32 v94,v30,v30
v_mul_f32 v95,v31,v31
v_mul_f32 v96,v32,v32
v_mul_f32 v97,v33,v33
v_fma_f32 v94,v80,v94,v79
v_fma_f32 v95,v80,v95,v79
v_fma_f32 v96,v80,v96,v79
v_fma_f32 v97,v80,v97,v79
v_mul_f32 v94,v30,v94
v_mul_f32 v95,v31,v95
v_mul_f32 v96,v32,v96
v_mul_f32 v97,v33,v97
v_exp_f32 v94,v94
v_exp_f32 v95,v95
v_exp_f32 v96,v96
v_exp_f32 v97,v97
v_fma_f32 v94,v94,v81,v81
v_fma_f32 v95,v95,v81,v81
v_fma_f32 v96,v96,v81,v81
v_fma_f32 v97,v97,v81,v81
v_rcp_f32 v94,v94
v_rcp_f32 v95,v95
v_rcp_f32 v96,v96
v_rcp_f32 v97,v97
v_mul_f32 v30,v30,v94
v_mul_f32 v31,v31,v95
v_mul_f32 v32,v32,v96
v_mul_f32 v33,v33,v97
v_cvt_pk_bf16_f32 v106,v30,v31
v_cvt_pk_bf16_f32 v107,v32,v33
v_fma_f32 v34,-v78,v86,v34
v_fma_f32 v35,-v78,v87,v35
v_fma_f32 v36,-v78,v88,v36
v_fma_f32 v37,-v78,v89,v37
v_mul_f32 v98,v34,v34
v_mul_f32 v99,v35,v35
v_mul_f32 v100,v36,v36
v_mul_f32 v101,v37,v37
v_fma_f32 v98,v80,v98,v79
v_fma_f32 v99,v80,v99,v79
v_fma_f32 v100,v80,v100,v79
v_fma_f32 v101,v80,v101,v79
v_mul_f32 v98,v34,v98
v_mul_f32 v99,v35,v99
v_mul_f32 v100,v36,v100
v_mul_f32 v101,v37,v101
v_exp_f32 v98,v98
v_exp_f32 v99,v99
v_exp_f32 v100,v100
v_exp_f32 v101,v101
v_fma_f32 v98,v98,v81,v81
v_fma_f32 v99,v99,v81,v81
v_fma_f32 v100,v100,v81,v81
v_fma_f32 v101,v101,v81,v81
v_rcp_f32 v98,v98
v_rcp_f32 v99,v99
v_rcp_f32 v100,v100
v_rcp_f32 v101,v101
v_mul_f32 v34,v34,v98
v_mul_f32 v35,v35,v99
v_mul_f32 v36,v36,v100
v_mul_f32 v37,v37,v101
v_cvt_pk_bf16_f32 v108,v34,v35
v_cvt_pk_bf16_f32 v109,v36,v37
buffer_store_dwordx4 v[106:109],v0,s[36:39],s67 offen offset:0 sc0 sc1
v_fma_f32 v38,-v78,v90,v38
v_fma_f32 v39,-v78,v91,v39
v_fma_f32 v40,-v78,v92,v40
v_fma_f32 v41,-v78,v93,v41
v_mul_f32 v102,v38,v38
v_mul_f32 v103,v39,v39
v_mul_f32 v104,v40,v40
v_mul_f32 v105,v41,v41
v_fma_f32 v102,v80,v102,v79
v_fma_f32 v103,v80,v103,v79
v_fma_f32 v104,v80,v104,v79
v_fma_f32 v105,v80,v105,v79
v_mul_f32 v102,v38,v102
v_mul_f32 v103,v39,v103
v_mul_f32 v104,v40,v104
v_mul_f32 v105,v41,v105
v_exp_f32 v102,v102
v_exp_f32 v103,v103
v_exp_f32 v104,v104
v_exp_f32 v105,v105
v_fma_f32 v102,v102,v81,v81
v_fma_f32 v103,v103,v81,v81
v_fma_f32 v104,v104,v81,v81
v_fma_f32 v105,v105,v81,v81
v_rcp_f32 v102,v102
v_rcp_f32 v103,v103
v_rcp_f32 v104,v104
v_rcp_f32 v105,v105
v_mul_f32 v38,v38,v102
v_mul_f32 v39,v39,v103
v_mul_f32 v40,v40,v104
v_mul_f32 v41,v41,v105
v_cvt_pk_bf16_f32 v110,v38,v39
v_cvt_pk_bf16_f32 v111,v40,v41
buffer_store_dwordx2 v[110:111],v10,s[36:39],s67 offen offset:0 sc0 sc1
s_mov_b32 s72,s69
s_mov_b32 s69,0x7fff0000
s_mov_b32 s67,0x7fff0000
s_mov_b32 s77,0x7fff0000
s_branch Lg3_done_%=
Lg3_done_%=:
s_waitcnt vmcnt(2)
s_barrier
buffer_store_dword v4,v3,s[36:39],s70 offen offset:0 sc0 sc1
s_waitcnt vmcnt(1)
s_barrier
buffer_store_dword v4,v3,s[36:39],s72 offen offset:0 sc0 sc1
s_waitcnt vmcnt(0) lgkmcnt(0)
s_barrier
s_endpgm
Lcode_g3_e:
.purgem M
.purgem R
.purgem T
.purgem D
.purgem L
)"
  :: "s"(ws), "s"(__builtin_amdgcn_readfirstlane(sWave)), "s"(__builtin_amdgcn_readfirstlane(sG0)), "s"(__builtin_amdgcn_readfirstlane(sStep)), "s"(__builtin_amdgcn_readfirstlane(sOutOff)), "s"(__builtin_amdgcn_readfirstlane(sFoutOff)), "s"(__builtin_amdgcn_readfirstlane(sAuxOff)), "s"(__builtin_amdgcn_readfirstlane(sWw)), "s"(__builtin_amdgcn_readfirstlane(sBuf)), "s"(__builtin_amdgcn_readfirstlane(sExp)), "v"(vL16), "v"(vFlag), "v"(vSnapOff), "v"(vSnapRd), "v"(vWd0), "v"(vWd1), "v"(vWr0), "v"(vWr1), "v"(vWr2), "v"(vOutS), "v"(vAux0), "v"(vAux1), "v"(vAux2), "v"(vAux3), "v"(vAn), "v"(vAe), "s"(w3), "s"(out) : "v255", "v0", "v1", "v2", "v3", "v4", "v5", "v6", "v7", "v8", "v9", "v10", "v11", "v12", "v13", "v14", "v15", "v16", "v17", "v18", "v19", "v20", "v21", "v22", "v23", "v24", "v25", "v26", "v27", "v28", "v29", "v30", "v31", "v32", "v33", "v34", "v35", "v36", "v37", "v38", "v39", "v40", "v41", "v42", "v43", "v44", "v45", "v46", "v47", "v48", "v49", "v50", "v51", "v52", "v53", "v54", "v55", "v56", "v57", "v58", "v59", "v60", "v61", "v62", "v63", "v64", "v65", "v66", "v67", "v68", "v69", "v70", "v71", "v72", "v73", "v74", "v75", "v76", "v77", "v78", "v79", "v80", "v81", "v82", "v83", "v84", "v85", "v86", "v87", "v88", "v89", "v90", "v91", "v92", "v93", "v94", "v95", "v96", "v97", "v98", "v99", "v100", "v101", "v102", "v103", "v104", "v105", "v106", "v107", "v108", "v109", "v110", "v111", "v112", "v113", "v114", "v115", "v116", "v117", "v118", "v119", "v120", "v121", "v122", "v123", "v124", "v125", "v126", "v127", "v128", "v129", "v130", "v131", "v132", "v133", "v134", "v135", "v136", "v137", "v138", "v139", "v140", "v141", "v142", "v143", "v144", "v145", "v146", "v147", "v148", "v149", "v150", "v151", "v152", "v153", "v154", "v155", "v156", "v157", "v158", "v159", "v160", "v161", "v162", "v163", "v164", "v165", "v166", "v167", "v168", "v169", "v170", "v171", "v172", "v173", "v174", "v175", "v176", "v177", "v178", "v179", "v180", "v181", "v182", "v183", "v184", "v185", "v186", "v187", "v188", "v189", "v190", "v191", "v192", "v193", "v194", "v195", "v196", "v197", "v198", "v199", "v200", "v201", "v202", "v203", "v204", "v205", "v206", "v207", "v208", "v209", "v210", "v211", "v212", "v213", "v214", "v215", "v216", "v217", "v218", "v219", "v220", "v221", "v222", "v223", "v224", "v225", "v226", "v227", "v228", "v229", "v230", "v231", "a0", "a1", "a2", "a3", "a4", "a5", "a6", "a7", "a8", "a9", "a10", "a11", "a12", "a13", "a14", "a15", "a16", "a17", "a18", "a19", "a20", "a21", "a22", "a23", "a24", "a25", "a26", "a27", "a28", "a29", "a30", "a31", "a32", "a33", "a34", "a35", "a36", "a37", "a38", "a39", "a40", "a41", "a42", "a43", "a44", "a45", "a46", "a47", "a48", "a49", "a50", "a51", "a52", "a53", "a54", "a55", "a56", "a57", "a58", "a59", "a60", "a61", "a62", "a63", "a64", "a65", "a66", "a67", "a68", "a69", "a70", "a71", "a72", "a73", "a74", "a75", "a76", "a77", "a78", "a79", "a80", "a81", "a82", "a83", "a84", "a85", "a86", "a87", "a88", "a89", "a90", "a91", "a92", "a93", "a94", "a95", "a96", "a97", "a98", "a99", "a100", "a101", "a102", "a103", "a104", "a105", "a106", "a107", "a108", "a109", "a110", "a111", "a112", "a113", "a114", "a115", "a116", "a117", "a118", "a119", "a120", "a121", "a122", "a123", "a124", "a125", "a126", "a127", "a128", "a129", "a130", "a131", "a132", "a133", "a134", "a135", "a136", "a137", "a138", "a139", "a140", "a141", "a142", "a143", "a144", "a145", "a146", "a147", "a148", "a149", "a150", "a151", "a152", "a153", "a154", "a155", "a156", "a157", "a158", "a159", "a160", "a161", "a162", "a163", "a164", "a165", "a166", "a167", "a168", "a169", "a170", "a171", "a172", "a173", "a174", "a175", "a176", "a177", "a178", "a179", "a180", "a181", "a182", "a183", "a184", "a185", "a186", "a187", "a188", "a189", "a190", "a191", "a192", "a193", "a194", "a195", "a196", "a197", "a198", "a199", "a200", "a201", "a202", "a203", "a204", "a205", "a206", "a207", "a208", "a209", "a210", "a211", "a212", "a213", "a214", "a215", "a216", "a217", "a218", "a219", "a220", "a221", "a222", "a223", "a224", "a225", "a226", "a227", "a228", "a229", "a230", "a231", "a232", "a233", "a234", "a235", "a236", "a237", "a238", "a239", "a240", "a241", "a242", "a243", "a244", "a245", "a246", "a247", "a248", "a249", "a250", "a251", "a252", "a253", "a254", "a255", "s36", "s37", "s38", "s39", "s40", "s41", "s42", "s43", "s44", "s45", "s46", "s47", "s48", "s49", "s50", "s51", "s52", "s53", "s54", "s55", "s56", "s57", "s58", "s59", "s60", "s61", "s62", "s63", "s64", "s65", "s66", "s67", "s68", "s69", "s70", "s71", "s72", "s73", "s74", "s75", "s76", "s77", "s78", "s79", "s80", "s81", "s82", "s83", "s84", "s85", "s86", "s87", "s88", "s89", "s90", "s91", "s92", "s93", "s94", "s95", "s96", "s97", "vcc", "scc", "memory"); } else { asm volatile(
R"(.macro M d,w,x
v_mfma_f32_16x16x32_bf16 v[\d:\d+3], \w, v[\x:\x+3], v[\d:\d+3]
.endm
.macro R d,a,o
ds_read_b128 v[\d:\d+3], v\a offset:\o
.endm
.macro T d,a,o
ds_read_b64_tr_b16 \d, v\a offset:\o
.endm
.macro D a,s,o
buffer_load_dwordx4 v\a, s[36:39], s\s offen offset:\o sc1 lds
.endm
.macro L c
s_waitcnt lgkmcnt(\c)
.endm
Lcode_g3o_b:
s_mov_b64 s[36:37],%0
s_mov_b32 s38,111243392
s_mov_b32 s39,0x00020000
s_mov_b32 s44,%1
s_mov_b32 s45,%2
s_mov_b32 s46,%3
s_mov_b32 s47,%4
s_mov_b32 s48,%5
s_mov_b32 s49,%6
s_mov_b32 s50,%7
s_mov_b32 s51,%8
s_mov_b32 s53,%9
v_mov_b32 v0,%10
v_mov_b32 v3,%11
v_mov_b32 v5,%12
v_mov_b32 v6,%13
v_mov_b32 v18,%14
v_mov_b32 v19,%15
v_mov_b32 v20,%16
v_mov_b32 v21,%17
v_mov_b32 v22,%18
v_mov_b32 v10,%19
v_mov_b32 v11,%20
v_mov_b32 v12,%21
v_mov_b32 v13,%22
v_mov_b32 v14,%23
v_mov_b32 v15,%24
v_mov_b32 v16,%25
s_mov_b64 s[40:41],%26
s_mov_b32 s42,7340032
s_mov_b32 s43,0x00020000
s_lshl_b32 s57,s44,11
s_add_u32 s55,s50,0
s_add_u32 m0,s57,0
s_nop 0
buffer_load_dwordx4 v18,s[40:43],s55 offen offset:0  lds
s_add_u32 m0,s57,1024
s_nop 0
buffer_load_dwordx4 v19,s[40:43],s55 offen offset:0  lds
s_add_u32 m0,s57,8064
s_nop 0
buffer_load_dwordx4 v18,s[40:43],s55 offen offset:128  lds
s_add_u32 m0,s57,9088
s_nop 0
buffer_load_dwordx4 v19,s[40:43],s55 offen offset:128  lds
s_add_u32 s55,s50,458752
s_add_u32 m0,s57,16384
s_nop 0
buffer_load_dwordx4 v18,s[40:43],s55 offen offset:0  lds
s_add_u32 m0,s57,17408
s_nop 0
buffer_load_dwordx4 v19,s[40:43],s55 offen offset:0  lds
s_add_u32 m0,s57,24448
s_nop 0
buffer_load_dwordx4 v18,s[40:43],s55 offen offset:128  lds
s_add_u32 m0,s57,25472
s_nop 0
buffer_load_dwordx4 v19,s[40:43],s55 offen offset:128  lds
s_add_u32 s55,s50,917504
s_add_u32 m0,s57,32768
s_nop 0
buffer_load_dwordx4 v18,s[40:43],s55 offen offset:0  lds
s_add_u32 m0,s57,33792
s_nop 0
buffer_load_dwordx4 v19,s[40:43],s55 offen offset:0  lds
s_add_u32 m0,s57,40832
s_nop 0
buffer_load_dwordx4 v18,s[40:43],s55 offen offset:128  lds
s_add_u32 m0,s57,41856
s_nop 0
buffer_load_dwordx4 v19,s[40:43],s55 offen offset:128  lds
s_add_u32 s55,s50,1376256
s_add_u32 m0,s57,49152
s_nop 0
buffer_load_dwordx4 v18,s[40:43],s55 offen offset:0  lds
s_add_u32 m0,s57,50176
s_nop 0
buffer_load_dwordx4 v19,s[40:43],s55 offen offset:0  lds
s_add_u32 m0,s57,57216
s_nop 0
buffer_load_dwordx4 v18,s[40:43],s55 offen offset:128  lds
s_add_u32 m0,s57,58240
s_nop 0
buffer_load_dwordx4 v19,s[40:43],s55 offen offset:128  lds
s_add_u32 s55,s50,1835008
s_add_u32 m0,s57,65536
s_nop 0
buffer_load_dwordx4 v18,s[40:43],s55 offen offset:0  lds
s_add_u32 m0,s57,66560
s_nop 0
buffer_load_dwordx4 v19,s[40:43],s55 offen offset:0  lds
s_add_u32 m0,s57,73600
s_nop 0
buffer_load_dwordx4 v18,s[40:43],s55 offen offset:128  lds
s_add_u32 m0,s57,74624
s_nop 0
buffer_load_dwordx4 v19,s[40:43],s55 offen offset:128  lds
s_add_u32 s55,s50,2293760
s_add_u32 m0,s57,81920
s_nop 0
buffer_load_dwordx4 v18,s[40:43],s55 offen offset:0  lds
s_add_u32 m0,s57,82944
s_nop 0
buffer_load_dwordx4 v19,s[40:43],s55 offen offset:0  lds
s_add_u32 m0,s57,89984
s_nop 0
buffer_load_dwordx4 v18,s[40:43],s55 offen offset:128  lds
s_add_u32 m0,s57,91008
s_nop 0
buffer_load_dwordx4 v19,s[40:43],s55 offen offset:128  lds
s_waitcnt vmcnt(20) lgkmcnt(0)
s_barrier
s_add_u32 s55,s50,2752512
s_add_u32 m0,s57,98304
s_nop 0
buffer_load_dwordx4 v18,s[40:43],s55 offen offset:0  lds
s_add_u32 m0,s57,99328
s_nop 0
buffer_load_dwordx4 v19,s[40:43],s55 offen offset:0  lds
s_add_u32 m0,s57,106368
s_nop 0
buffer_load_dwordx4 v18,s[40:43],s55 offen offset:128  lds
s_add_u32 m0,s57,107392
s_nop 0
buffer_load_dwordx4 v19,s[40:43],s55 offen offset:128  lds
v_add_u32 v26,0,v20
v_add_u32 v27,0,v21
T a[0:1],26,0
T a[2:3],26,512
T a[4:5],27,0
T a[6:7],27,512
T a[8:9],26,4096
T a[10:11],26,4608
T a[12:13],27,4096
T a[14:15],27,4608
s_waitcnt vmcnt(20) lgkmcnt(0)
s_barrier
s_add_u32 s55,s50,3211264
s_add_u32 m0,s57,0
s_nop 0
buffer_load_dwordx4 v18,s[40:43],s55 offen offset:0  lds
s_add_u32 m0,s57,1024
s_nop 0
buffer_load_dwordx4 v19,s[40:43],s55 offen offset:0  lds
s_add_u32 m0,s57,8064
s_nop 0
buffer_load_dwordx4 v18,s[40:43],s55 offen offset:128  lds
s_add_u32 m0,s57,9088
s_nop 0
buffer_load_dwordx4 v19,s[40:43],s55 offen offset:128  lds
v_add_u32 v26,16384,v20
v_add_u32 v27,16384,v21
T a[16:17],26,0
T a[18:19],26,512
T a[20:21],27,0
T a[22:23],27,512
T a[24:25],26,4096
T a[26:27],26,4608
T a[28:29],27,4096
T a[30:31],27,4608
s_waitcnt vmcnt(20) lgkmcnt(0)
s_barrier
s_add_u32 s55,s50,3670016
s_add_u32 m0,s57,16384
s_nop 0
buffer_load_dwordx4 v18,s[40:43],s55 offen offset:0  lds
s_add_u32 m0,s57,17408
s_nop 0
buffer_load_dwordx4 v19,s[40:43],s55 offen offset:0  lds
s_add_u32 m0,s57,24448
s_nop 0
buffer_load_dwordx4 v18,s[40:43],s55 offen offset:128  lds
s_add_u32 m0,s57,25472
s_nop 0
buffer_load_dwordx4 v19,s[40:43],s55 offen offset:128  lds
v_add_u32 v26,32768,v20
v_add_u32 v27,32768,v21
T a[32:33],26,0
T a[34:35],26,512
T a[36:37],27,0
T a[38:39],27,512
T a[40:41],26,4096
T a[42:43],26,4608
T a[44:45],27,4096
T a[46:47],27,4608
s_waitcnt vmcnt(20) lgkmcnt(0)
s_barrier
s_add_u32 s55,s50,4128768
s_add_u32 m0,s57,32768
s_nop 0
buffer_load_dwordx4 v18,s[40:43],s55 offen offset:0  lds
s_add_u32 m0,s57,33792
s_nop 0
buffer_load_dwordx4 v19,s[40:43],s55 offen offset:0  lds
s_add_u32 m0,s57,40832
s_nop 0
buffer_load_dwordx4 v18,s[40:43],s55 offen offset:128  lds
s_add_u32 m0,s57,41856
s_nop 0
buffer_load_dwordx4 v19,s[40:43],s55 offen offset:128  lds
v_add_u32 v26,49152,v20
v_add_u32 v27,49152,v21
T a[48:49],26,0
T a[50:51],26,512
T a[52:53],27,0
T a[54:55],27,512
T a[56:57],26,4096
T a[58:59],26,4608
T a[60:61],27,4096
T a[62:63],27,4608
s_waitcnt vmcnt(20) lgkmcnt(0)
s_barrier
s_add_u32 s55,s50,4587520
s_add_u32 m0,s57,49152
s_nop 0
buffer_load_dwordx4 v18,s[40:43],s55 offen offset:0  lds
s_add_u32 m0,s57,50176
s_nop 0
buffer_load_dwordx4 v19,s[40:43],s55 offen offset:0  lds
s_add_u32 m0,s57,57216
s_nop 0
buffer_load_dwordx4 v18,s[40:43],s55 offen offset:128  lds
s_add_u32 m0,s57,58240
s_nop 0
buffer_load_dwordx4 v19,s[40:43],s55 offen offset:128  lds
v_add_u32 v26,65536,v20
v_add_u32 v27,65536,v21
T a[64:65],26,0
T a[66:67],26,512
T a[68:69],27,0
T a[70:71],27,512
T a[72:73],26,4096
T a[74:75],26,4608
T a[76:77],27,4096
T a[78:79],27,4608
s_waitcnt vmcnt(20) lgkmcnt(0)
s_barrier
s_add_u32 s55,s50,5046272
s_add_u32 m0,s57,65536
s_nop 0
buffer_load_dwordx4 v18,s[40:43],s55 offen offset:0  lds
s_add_u32 m0,s57,66560
s_nop 0
buffer_load_dwordx4 v19,s[40:43],s55 offen offset:0  lds
s_add_u32 m0,s57,73600
s_nop 0
buffer_load_dwordx4 v18,s[40:43],s55 offen offset:128  lds
s_add_u32 m0,s57,74624
s_nop 0
buffer_load_dwordx4 v19,s[40:43],s55 offen offset:128  lds
v_add_u32 v26,81920,v20
v_add_u32 v27,81920,v21
T a[80:81],26,0
T a[82:83],26,512
T a[84:85],27,0
T a[86:87],27,512
T a[88:89],26,4096
T a[90:91],26,4608
T a[92:93],27,4096
T a[94:95],27,4608
s_waitcnt vmcnt(20) lgkmcnt(0)
s_barrier
s_add_u32 s55,s50,5505024
s_add_u32 m0,s57,81920
s_nop 0
buffer_load_dwordx4 v18,s[40:43],s55 offen offset:0  lds
s_add_u32 m0,s57,82944
s_nop 0
buffer_load_dwordx4 v19,s[40:43],s55 offen offset:0  lds
s_add_u32 m0,s57,89984
s_nop 0
buffer_load_dwordx4 v18,s[40:43],s55 offen offset:128  lds
s_add_u32 m0,s57,91008
s_nop 0
buffer_load_dwordx4 v19,s[40:43],s55 offen offset:128  lds
v_add_u32 v26,98304,v20
v_add_u32 v27,98304,v21
T a[96:97],26,0
T a[98:99],26,512
T a[100:101],27,0
T a[102:103],27,512
T a[104:105],26,4096
T a[106:107],26,4608
T a[108:109],27,4096
T a[110:111],27,4608
s_waitcnt vmcnt(20) lgkmcnt(0)
s_barrier
s_add_u32 s55,s50,5963776
s_add_u32 m0,s57,98304
s_nop 0
buffer_load_dwordx4 v18,s[40:43],s55 offen offset:0  lds
s_add_u32 m0,s57,99328
s_nop 0
buffer_load_dwordx4 v19,s[40:43],s55 offen offset:0  lds
s_add_u32 m0,s57,106368
s_nop 0
buffer_load_dwordx4 v18,s[40:43],s55 offen offset:128  lds
s_add_u32 m0,s57,107392
s_nop 0
buffer_load_dwordx4 v19,s[40:43],s55 offen offset:128  lds
v_add_u32 v26,0,v20
v_add_u32 v27,0,v21
T a[112:113],26,0
T a[114:115],26,512
T a[116:117],27,0
T a[118:119],27,512
T a[120:121],26,4096
T a[122:123],26,4608
T a[124:125],27,4096
T a[126:127],27,4608
s_waitcnt vmcnt(20) lgkmcnt(0)
s_barrier
s_add_u32 s55,s50,6422528
s_add_u32 m0,s57,0
s_nop 0
buffer_load_dwordx4 v18,s[40:43],s55 offen offset:0  lds
s_add_u32 m0,s57,1024
s_nop 0
buffer_load_dwordx4 v19,s[40:43],s55 offen offset:0  lds
s_add_u32 m0,s57,8064
s_nop 0
buffer_load_dwordx4 v18,s[40:43],s55 offen offset:128  lds
s_add_u32 m0,s57,9088
s_nop 0
buffer_load_dwordx4 v19,s[40:43],s55 offen offset:128  lds
v_add_u32 v26,16384,v20
v_add_u32 v27,16384,v21
T a[128:129],26,0
T a[130:131],26,512
T a[132:133],27,0
T a[134:135],27,512
T a[136:137],26,4096
T a[138:139],26,4608
T a[140:141],27,4096
T a[142:143],27,4608
s_waitcnt vmcnt(20) lgkmcnt(0)
s_barrier
s_add_u32 s55,s50,6881280
s_add_u32 m0,s57,16384
s_nop 0
buffer_load_dwordx4 v18,s[40:43],s55 offen offset:0  lds
s_add_u32 m0,s57,17408
s_nop 0
buffer_load_dwordx4 v19,s[40:43],s55 offen offset:0  lds
s_add_u32 m0,s57,24448
s_nop 0
buffer_load_dwordx4 v18,s[40:43],s55 offen offset:128  lds
s_add_u32 m0,s57,25472
s_nop 0
buffer_load_dwordx4 v19,s[40:43],s55 offen offset:128  lds
v_add_u32 v26,32768,v20
v_add_u32 v27,32768,v21
T a[144:145],26,0
T a[146:147],26,512
T a[148:149],27,0
T a[150:151],27,512
T a[152:153],26,4096
T a[154:155],26,4608
T a[156:157],27,4096
T a[158:159],27,4608
s_waitcnt vmcnt(20) lgkmcnt(0)
s_barrier
v_add_u32 v26,49152,v20
v_add_u32 v27,49152,v21
T a[160:161],26,0
T a[162:163],26,512
T a[164:165],27,0
T a[166:167],27,512
T a[168:169],26,4096
T a[170:171],26,4608
T a[172:173],27,4096
T a[174:175],27,4608
s_waitcnt vmcnt(16) lgkmcnt(0)
s_barrier
v_add_u32 v26,65536,v20
v_add_u32 v27,65536,v21
T a[176:177],26,0
T a[178:179],26,512
T a[180:181],27,0
T a[182:183],27,512
T a[184:185],26,4096
T a[186:187],26,4608
T a[188:189],27,4096
T a[190:191],27,4608
s_waitcnt vmcnt(12) lgkmcnt(0)
s_barrier
v_add_u32 v26,81920,v20
v_add_u32 v27,81920,v21
T a[192:193],26,0
T a[194:195],26,512
T a[196:197],27,0
T a[198:199],27,512
T a[200:201],26,4096
T a[202:203],26,4608
T a[204:205],27,4096
T a[206:207],27,4608
s_waitcnt vmcnt(8) lgkmcnt(0)
s_barrier
v_add_u32 v26,98304,v20
v_add_u32 v27,98304,v21
T a[208:209],26,0
T a[210:211],26,512
T a[212:213],27,0
T a[214:215],27,512
T a[216:217],26,4096
T a[218:219],26,4608
T a[220:221],27,4096
T a[222:223],27,4608
s_waitcnt vmcnt(4) lgkmcnt(0)
s_barrier
v_add_u32 v26,0,v20
v_add_u32 v27,0,v21
T a[224:225],26,0
T a[226:227],26,512
T a[228:229],27,0
T a[230:231],27,512
T a[232:233],26,4096
T a[234:235],26,4608
T a[236:237],27,4096
T a[238:239],27,4608
s_waitcnt vmcnt(0) lgkmcnt(0)
s_barrier
v_add_u32 v26,16384,v20
v_add_u32 v27,16384,v21
T a[240:241],26,0
T a[242:243],26,512
T a[244:245],27,0
T a[246:247],27,512
T a[248:249],26,4096
T a[250:251],26,4608
T a[252:253],27,4096
T a[254:255],27,4608
L 0
s_barrier
v_mov_b32 v34,0x3f803f80
v_mov_b32 v35,0x3f803f80
v_mov_b32 v36,0x3f803f80
v_mov_b32 v37,0x3f803f80
s_nop 1
v_mfma_f32_16x16x32_bf16 v[106:109],a[0:3],v[34:37],0
v_mfma_f32_16x16x32_bf16 v[110:113],a[4:7],v[34:37],0
M 106,a[8:11],34
M 110,a[12:15],34
M 106,a[16:19],34
M 110,a[20:23],34
M 106,a[24:27],34
M 110,a[28:31],34
M 106,a[32:35],34
M 110,a[36:39],34
M 106,a[40:43],34
M 110,a[44:47],34
M 106,a[48:51],34
M 110,a[52:55],34
M 106,a[56:59],34
M 110,a[60:63],34
M 106,a[64:67],34
M 110,a[68:71],34
M 106,a[72:75],34
M 110,a[76:79],34
M 106,a[80:83],34
M 110,a[84:87],34
M 106,a[88:91],34
M 110,a[92:95],34
M 106,a[96:99],34
M 110,a[100:103],34
M 106,a[104:107],34
M 110,a[108:111],34
M 106,a[112:115],34
M 110,a[116:119],34
M 106,a[120:123],34
M 110,a[124:127],34
M 106,a[128:131],34
M 110,a[132:135],34
M 106,a[136:139],34
M 110,a[140:143],34
M 106,a[144:147],34
M 110,a[148:151],34
M 106,a[152:155],34
M 110,a[156:159],34
M 106,a[160:163],34
M 110,a[164:167],34
M 106,a[168:171],34
M 110,a[172:175],34
M 106,a[176:179],34
)"
R"(M 110,a[180:183],34
M 106,a[184:187],34
M 110,a[188:191],34
M 106,a[192:195],34
M 110,a[196:199],34
M 106,a[200:203],34
M 110,a[204:207],34
M 106,a[208:211],34
M 110,a[212:215],34
M 106,a[216:219],34
M 110,a[220:223],34
M 106,a[224:227],34
M 110,a[228:231],34
M 106,a[232:235],34
M 110,a[236:239],34
M 106,a[240:243],34
M 110,a[244:247],34
M 106,a[248:251],34
M 110,a[252:255],34
v_mov_b32 v4,s53
s_mov_b32 s82,0
s_mov_b32 s80,0
s_mov_b32 s54,s45
s_mul_i32 s57,s44,4096
s_mul_i32 s55,s45,32768
s_add_u32 s55,s55,s57
s_add_u32 s55,s55,31457280
s_mul_i32 s57,s44,4096
s_mul_i32 s60,s46,32768
s_mul_i32 s61,s46,1024
s_mul_i32 s62,s46,114688
s_mul_i32 s63,s46,2560
s_mul_i32 s64,s46,1024
s_lshl_b32 s59,s44,8
s_add_u32 s59,s59,131072
s_mul_i32 s58,s45,1024
s_add_u32 s58,s58,109613056
s_mul_i32 s65,s45,114688
s_add_u32 s65,s65,s47
s_mul_i32 s68,s45,2560
s_add_u32 s68,s68,s48
s_mul_i32 s73,s45,1024
s_add_u32 s73,s73,107479040
s_mul_i32 s74,s45,32768
s_add_u32 s74,s74,s49
s_mul_i32 s75,s45,1024
s_add_u32 s75,s75,107479040
s_mov_b32 s66,0x7fff0000
s_mov_b32 s67,0x7fff0000
s_mov_b32 s69,0x7fff0000
s_mov_b32 s76,0x7fff0000
s_mov_b32 s77,0x7fff0000
s_mov_b32 s70,0x7fff0000
s_mov_b32 s71,0x7fff0000
s_mov_b32 s72,0x7fff0000
v_mov_b32 v1,v0
v_add_u32 v2,16384,v0
s_mov_b32 s84,16384
s_mov_b32 s86,0x3a800000
s_mov_b32 s87,0x3727c5ac
s_mov_b32 s88,0xc0135761
s_mov_b32 s89,0x3d372713
s_lshr_b32 s85,s44,1
s_mul_i32 s85,s85,127
v_lshlrev_b32 v8,22,v0
v_and_b32 v8,0x80000000,v8
v_or_b32 v8,v8,v5
v_add_u32 v2,32768,v0
s_lshl_b32 s84,s57,1
s_add_u32 s84,s84,32768
v_and_b32 v114,240,v0
v_cmp_eq_u32 vcc,0,v114
s_nop 1
v_cndmask_b32 v17,0,1.0,vcc
s_getpc_b64 s[40:41]
Lg3o_pc_%=:
s_lshl_b32 s78,s44,13
v_lshlrev_b32 v114,3,v0
v_add_u32 v114,s78,v114
v_min_u32 v114,Lcode_g3o_e-Lg3o_pc_%=-4,v114
s_mov_b32 m0,163584
s_nop 0
global_load_lds_dword v114,s[40:41]
s_add_u32 m0,s59,4096
s_nop 0
buffer_load_dword v8,s[36:39],s58 offen offset:0 sc1 lds
buffer_store_dword v4,v3,s[36:39],s71 offen offset:0 sc0 sc1
s_add_u32 m0,s59,5120
s_nop 0
buffer_load_dword v8,s[36:39],s58 offen offset:0 sc1 lds
buffer_store_dword v4,v3,s[36:39],s71 offen offset:0 sc0 sc1
s_mov_b32 s83,0
Lg3o_poll_%=:
s_add_u32 s78,s83,2
s_and_b32 s78,s78,3
s_lshl_b32 s78,s78,10
s_add_u32 s78,s78,4096
s_add_u32 m0,s59,s78
s_nop 0
buffer_load_dword v8,s[36:39],s58 offen offset:0 sc1 lds
s_and_b32 s78,s83,3
s_lshl_b32 s78,s78,10
v_add_u32 v114,s78,v6
s_waitcnt vmcnt(4)
s_barrier
ds_read_b32 v7,v114 offset:4096
buffer_store_dword v4,v3,s[36:39],s70 offen offset:0 sc0 sc1
s_mov_b32 s70,s71
s_mov_b32 s71,s72
s_mov_b32 s72,0x7fff0000
s_add_u32 s83,s83,1
s_add_u32 s82,s82,1
L 0
v_cmp_eq_u32 vcc,s53,v7
s_nop 0
s_cmp_eq_u32 vcc_lo,-1
s_cbranch_scc1 Lg3o_go_%=
s_cmp_lt_u32 s82,20000
s_cbranch_scc1 Lg3o_poll_%=
Lg3o_go_%=:
buffer_load_dword v130,v11,s[36:39],s73 offen offset:0 sc1
s_add_u32 s73,s73,s64
s_add_u32 m0,s57,0
s_add_u32 s56,s55,0
D 0,56,0
D 0,56,1024
D 0,56,2048
D 0,56,3072
s_add_u32 s58,s58,s61
s_add_u32 m0,s59,2048
s_nop 0
buffer_load_dword v8,s[36:39],s58 offen offset:0 sc1 lds
s_add_u32 m0,s57,16384
s_add_u32 s56,s55,16384
D 0,56,0
D 0,56,1024
D 0,56,2048
D 0,56,3072
s_add_u32 s55,s55,s60
s_sub_u32 s57,s84,s57
s_add_u32 s54,s54,s46
s_waitcnt vmcnt(4)
s_barrier
buffer_store_dword v4,v3,s[36:39],s70 offen offset:0 sc0 sc1
s_min_u32 s71,s71,s72
buffer_store_dword v4,v3,s[36:39],s71 offen offset:0 sc0 sc1
s_mov_b32 s70,0x7fff0000
s_mov_b32 s71,0x7fff0000
s_mov_b32 s72,0x7fff0000
s_cmp_eq_u32 s80,1
s_cbranch_scc1 Lg3o_st1_%=
s_branch Lg3o_st0_%=
Lg3o_gox_%=:
s_add_u32 m0,s59,2048
s_nop 0
buffer_load_dword v8,s[36:39],s58 offen offset:0 sc1 lds
s_waitcnt vmcnt(0)
s_barrier
buffer_store_dword v4,v3,s[36:39],s70 offen offset:0 sc0 sc1
s_min_u32 s71,s71,s72
buffer_store_dword v4,v3,s[36:39],s71 offen offset:0 sc0 sc1
s_mov_b32 s70,0x7fff0000
s_mov_b32 s71,0x7fff0000
s_mov_b32 s72,0x7fff0000
s_cmp_eq_u32 s80,1
s_cbranch_scc1 Lg3o_st1_%=
Lg3o_st0_%=:
ds_read_b32 v7,v6 offset:2048
R 34,1,0
R 38,1,1024
R 42,1,2048
R 46,1,3072
R 50,1,4096
L 5
v_cmp_eq_u32 vcc,s53,v7
L 3
s_cmp_eq_u32 vcc_lo,-1
s_cbranch_scc1 Lg3o_F0_%=
s_branch Lg3o_L0_%=
Lg3o_st1_%=:
ds_read_b32 v7,v6 offset:2048
R 34,2,0
R 38,2,1024
R 42,2,2048
R 46,2,3072
R 50,2,4096
L 5
v_cmp_eq_u32 vcc,s53,v7
L 3
s_cmp_eq_u32 vcc_lo,-1
s_cbranch_scc1 Lg3o_F1_%=
s_branch Lg3o_L1_%=
Lg3o_F0_%=:
v_mfma_f32_16x16x32_bf16 v[22:25],a[4:7],v[34:37],0
s_add_u32 s58,s58,s61
s_add_u32 m0,s59,0
R 54,1,5120
M 22,a[12:15],38
buffer_load_dword v8,s[36:39],s58 offen offset:0 sc1 lds
v_mfma_f32_16x16x32_bf16 v[18:21],a[0:3],v[34:37],0
s_add_u32 m0,s57,0
R 58,1,6144
v_add_f32_dpp v131,v130,v130 quad_perm:[1,0,3,2] row_mask:0xf bank_mask:0xf
M 18,a[8:11],38
L 3
s_add_u32 s56,s55,0
buffer_load_dword v130,v11,s[36:39],s73 offen offset:0 sc1
M 18,a[16:19],42
s_mov_b32 s66,s65
R 62,1,7168
v_add_f32_dpp v131,v131,v131 quad_perm:[2,3,0,1] row_mask:0xf bank_mask:0xf
M 18,a[24:27],46
D 0,56,0
M 22,a[20:23],42
s_add_u32 s65,s65,s62
R 66,1,8192
v_add_f32_dpp v134,v131,v131 row_half_mirror row_mask:0xf bank_mask:0xf
M 22,a[28:31],46
L 3
D 0,56,1024
M 22,a[36:39],50
s_add_u32 s54,s54,s46
R 70,1,9216
v_mov_b32_dpp v131,v134 row_ror:8 row_mask:0xf bank_mask:0xf
M 22,a[44:47],54
s_add_u32 s73,s73,s64
v_fma_f32 v131,-v134,v134,v131
v_max_f32 v131,s87,v131
M 18,a[32:35],50
R 74,1,10240
v_mul_f32 v134,v17,v134
M 18,a[40:43],54
L 3
D 0,56,2048
M 18,a[48:51],58
R 78,1,11264
v_fma_f32 v26,-v102,v106,v26
M 18,a[56:59],62
D 0,56,3072
M 22,a[52:55],58
s_add_u32 m0,s57,16384
R 82,1,12288
v_fma_f32 v27,-v102,v107,v27
M 22,a[60:63],62
L 3
v_rsq_f32 v132,v131
M 22,a[68:71],66
s_add_u32 s56,s55,16384
R 86,1,13312
v_fma_f32 v131,v15,v131,v16
M 22,a[76:79],70
s_add_u32 s55,s55,s60
v_mul_f32 v135,v132,v132
v_fma_f32 v131,v14,v135,v131
M 18,a[64:67],66
s_sub_u32 s57,s84,s57
R 90,1,14336
v_fma_f32 v134,v132,v131,v134
M 18,a[72:75],70
L 3
ds_write_b32 v12,v134 offset:0
M 18,a[80:83],74
s_waitcnt vmcnt(8)
R 94,1,15360
v_fma_f32 v28,-v102,v108,v28
M 18,a[88:91],78
s_barrier
v_fma_f32 v29,-v102,v109,v29
v_mul_f32 v114,v26,v26
M 22,a[84:87],74
R 34,1,16384
v_mul_f32 v115,v27,v27
M 22,a[92:95],78
L 4
v_mul_f32 v116,v28,v28
v_mul_f32 v117,v29,v29
M 22,a[100:103],82
R 38,1,17408
v_fma_f32 v114,v104,v114,v103
M 22,a[108:111],86
D 0,56,0
M 18,a[96:99],82
R 42,1,18432
v_fma_f32 v115,v104,v115,v103
M 18,a[104:107],86
L 3
v_fma_f32 v116,v104,v116,v103
v_fma_f32 v117,v104,v117,v103
M 18,a[112:115],90
R 46,1,19456
v_mul_f32 v114,v26,v114
M 18,a[120:123],94
v_exp_f32 v114,v114
M 22,a[116:119],90
R 50,1,20480
v_mul_f32 v115,v27,v115
M 22,a[124:127],94
L 3
v_exp_f32 v115,v115
M 22,a[132:135],34
R 54,1,21504
v_mul_f32 v116,v28,v116
M 22,a[140:143],38
D 0,56,1024
M 18,a[128:131],34
R 58,1,22528
v_mul_f32 v117,v29,v117
M 18,a[136:139],38
L 3
v_exp_f32 v116,v116
M 18,a[144:147],42
R 62,1,23552
v_fma_f32 v114,v114,v105,v105
M 18,a[152:155],46
v_exp_f32 v117,v117
M 22,a[148:151],42
R 66,1,24576
v_fma_f32 v115,v115,v105,v105
M 22,a[156:159],46
L 3
D 0,56,2048
M 22,a[164:167],50
R 70,1,25600
v_fma_f32 v116,v116,v105,v105
M 22,a[172:175],54
v_rcp_f32 v114,v114
M 18,a[160:163],50
R 74,1,26624
v_fma_f32 v117,v117,v105,v105
M 18,a[168:171],54
L 3
v_rcp_f32 v115,v115
M 18,a[176:179],58
R 78,1,27648
v_mul_f32 v26,v26,v114
M 18,a[184:187],62
v_rcp_f32 v116,v116
M 22,a[180:183],58
R 82,1,28672
v_mul_f32 v27,v27,v115
M 22,a[188:191],62
L 3
D 0,56,3072
M 22,a[196:199],66
R 86,1,29696
v_mul_f32 v28,v28,v116
M 22,a[204:207],70
v_rcp_f32 v117,v117
M 18,a[192:195],66
R 90,1,30720
v_mul_f32 v29,v29,v117
M 18,a[200:203],70
L 3
s_nop 0
v_cvt_pk_bf16_f32 v122,v26,v27
M 18,a[208:211],74
s_waitcnt vmcnt(4)
R 94,1,31744
v_fma_f32 v30,-v102,v110,v30
M 18,a[216:219],78
s_barrier
ds_read_b32 v7,v6 offset:0
R 98,13,0
M 22,a[212:215],74
R 34,2,0
v_fma_f32 v31,-v102,v111,v31
M 22,a[220:223],78
L 5
buffer_store_dword v4,v3,s[36:39],s70 offen offset:0 sc0 sc1
M 22,a[228:231],82
s_mov_b32 s70,s69
R 38,2,1024
v_fma_f32 v32,-v102,v112,v32
M 22,a[236:239],86
s_mov_b32 s69,s68
v_fma_f32 v33,-v102,v113,v33
v_mul_f32 v118,v30,v30
M 18,a[224:227],82
s_add_u32 s68,s68,s63
R 42,2,2048
v_mul_f32 v119,v31,v31
M 18,a[232:235],86
L 5
v_mul_f32 v120,v32,v32
v_mul_f32 v121,v33,v33
v_fma_f32 v118,v104,v118,v103
v_fma_f32 v119,v104,v119,v103
v_fma_f32 v120,v104,v120,v103
v_fma_f32 v121,v104,v121,v103
v_mul_f32 v118,v30,v118
v_mul_f32 v119,v31,v119
v_mul_f32 v120,v32,v120
v_mul_f32 v121,v33,v121
v_exp_f32 v118,v118
v_exp_f32 v119,v119
v_exp_f32 v120,v120
v_exp_f32 v121,v121
M 18,a[240:243],90
L 4
R 46,2,3072
v_fma_f32 v118,v118,v105,v105
v_fma_f32 v119,v119,v105,v105
v_fma_f32 v120,v120,v105,v105
v_fma_f32 v121,v121,v105,v105
v_rcp_f32 v118,v118
v_rcp_f32 v119,v119
v_rcp_f32 v120,v120
v_rcp_f32 v121,v121
v_cmp_eq_u32 vcc,s53,v7
M 18,a[248:251],94
v_cvt_pk_bf16_f32 v123,v28,v29
v_mul_f32 v30,v30,v118
v_mul_f32 v31,v31,v119
v_mul_f32 v32,v32,v120
v_mul_f32 v33,v33,v121
v_cvt_pk_bf16_f32 v124,v30,v31
v_cvt_pk_bf16_f32 v125,v32,v33
M 22,a[244:247],90
L 2
s_cmp_eq_u32 vcc_lo,-1
R 50,2,4096
buffer_store_dwordx4 v[122:125],v0,s[36:39],s67 offen offset:0 sc0 sc1
M 22,a[252:255],94
s_cbranch_scc0 Lg3o_K1_%=
Lg3o_F1_%=:
v_mfma_f32_16x16x32_bf16 v[30:33],a[4:7],v[34:37],0
s_add_u32 s58,s58,s61
s_add_u32 m0,s59,1024
R 54,2,5120
M 30,a[12:15],38
buffer_load_dword v8,s[36:39],s58 offen offset:0 sc1 lds
v_mfma_f32_16x16x32_bf16 v[26:29],a[0:3],v[34:37],0
s_add_u32 m0,s57,0
R 58,2,6144
v_add_f32_dpp v131,v130,v130 quad_perm:[1,0,3,2] row_mask:0xf bank_mask:0xf
M 26,a[8:11],38
L 3
s_add_u32 s56,s55,0
buffer_load_dword v130,v11,s[36:39],s73 offen offset:0 sc1
M 26,a[16:19],42
s_mov_b32 s67,s65
R 62,2,7168
v_add_f32_dpp v131,v131,v131 quad_perm:[2,3,0,1] row_mask:0xf bank_mask:0xf
M 26,a[24:27],46
D 0,56,0
M 30,a[20:23],42
s_add_u32 s65,s65,s62
R 66,2,8192
v_add_f32_dpp v134,v131,v131 row_half_mirror row_mask:0xf bank_mask:0xf
M 30,a[28:31],46
L 3
D 0,56,1024
M 30,a[36:39],50
s_add_u32 s54,s54,s46
R 70,2,9216
v_mov_b32_dpp v131,v134 row_ror:8 row_mask:0xf bank_mask:0xf
M 30,a[44:47],54
s_add_u32 s73,s73,s64
v_fma_f32 v131,-v134,v134,v131
v_max_f32 v131,s87,v131
M 26,a[32:35],50
R 74,2,10240
v_mul_f32 v134,v17,v134
M 26,a[40:43],54
L 3
D 0,56,2048
M 26,a[48:51],58
R 78,2,11264
v_fma_f32 v18,-v98,v106,v18
M 26,a[56:59],62
D 0,56,3072
M 30,a[52:55],58
s_add_u32 m0,s57,16384
R 82,2,12288
v_fma_f32 v19,-v98,v107,v19
M 30,a[60:63],62
L 3
v_rsq_f32 v132,v131
M 30,a[68:71],66
s_add_u32 s56,s55,16384
R 86,2,13312
v_fma_f32 v131,v15,v131,v16
M 30,a[76:79],70
s_add_u32 s55,s55,s60
v_mul_f32 v135,v132,v132
v_fma_f32 v131,v14,v135,v131
M 26,a[64:67],66
s_sub_u32 s57,s84,s57
R 90,2,14336
v_fma_f32 v134,v132,v131,v134
M 26,a[72:75],70
L 3
ds_write_b32 v12,v134 offset:2048
M 26,a[80:83],74
s_waitcnt vmcnt(8)
R 94,2,15360
v_fma_f32 v20,-v98,v108,v20
M 26,a[88:91],78
s_barrier
v_fma_f32 v21,-v98,v109,v21
)"
R"(v_mul_f32 v114,v18,v18
M 30,a[84:87],74
R 34,2,16384
v_mul_f32 v115,v19,v19
M 30,a[92:95],78
L 4
v_mul_f32 v116,v20,v20
v_mul_f32 v117,v21,v21
M 30,a[100:103],82
R 38,2,17408
v_fma_f32 v114,v100,v114,v99
M 30,a[108:111],86
D 0,56,0
M 26,a[96:99],82
R 42,2,18432
v_fma_f32 v115,v100,v115,v99
M 26,a[104:107],86
L 3
v_fma_f32 v116,v100,v116,v99
v_fma_f32 v117,v100,v117,v99
M 26,a[112:115],90
R 46,2,19456
v_mul_f32 v114,v18,v114
M 26,a[120:123],94
v_exp_f32 v114,v114
M 30,a[116:119],90
R 50,2,20480
v_mul_f32 v115,v19,v115
M 30,a[124:127],94
L 3
v_exp_f32 v115,v115
M 30,a[132:135],34
R 54,2,21504
v_mul_f32 v116,v20,v116
M 30,a[140:143],38
D 0,56,1024
M 26,a[128:131],34
R 58,2,22528
v_mul_f32 v117,v21,v117
M 26,a[136:139],38
L 3
v_exp_f32 v116,v116
M 26,a[144:147],42
R 62,2,23552
v_fma_f32 v114,v114,v101,v101
M 26,a[152:155],46
v_exp_f32 v117,v117
M 30,a[148:151],42
R 66,2,24576
v_fma_f32 v115,v115,v101,v101
M 30,a[156:159],46
L 3
D 0,56,2048
M 30,a[164:167],50
R 70,2,25600
v_fma_f32 v116,v116,v101,v101
M 30,a[172:175],54
v_rcp_f32 v114,v114
M 26,a[160:163],50
R 74,2,26624
v_fma_f32 v117,v117,v101,v101
M 26,a[168:171],54
L 3
v_rcp_f32 v115,v115
M 26,a[176:179],58
R 78,2,27648
v_mul_f32 v18,v18,v114
M 26,a[184:187],62
v_rcp_f32 v116,v116
M 30,a[180:183],58
R 82,2,28672
v_mul_f32 v19,v19,v115
M 30,a[188:191],62
L 3
D 0,56,3072
M 30,a[196:199],66
R 86,2,29696
v_mul_f32 v20,v20,v116
M 30,a[204:207],70
v_rcp_f32 v117,v117
M 26,a[192:195],66
R 90,2,30720
v_mul_f32 v21,v21,v117
M 26,a[200:203],70
L 3
s_nop 0
v_cvt_pk_bf16_f32 v122,v18,v19
M 26,a[208:211],74
s_waitcnt vmcnt(4)
R 94,2,31744
v_fma_f32 v22,-v98,v110,v22
M 26,a[216:219],78
s_barrier
ds_read_b32 v7,v6 offset:1024
R 102,13,2048
M 30,a[212:215],74
R 34,1,0
v_fma_f32 v23,-v98,v111,v23
M 30,a[220:223],78
L 5
buffer_store_dword v4,v3,s[36:39],s70 offen offset:0 sc0 sc1
M 30,a[228:231],82
s_mov_b32 s70,s69
R 38,1,1024
v_fma_f32 v24,-v98,v112,v24
M 30,a[236:239],86
s_mov_b32 s69,s68
v_fma_f32 v25,-v98,v113,v25
v_mul_f32 v118,v22,v22
M 26,a[224:227],82
s_add_u32 s68,s68,s63
R 42,1,2048
v_mul_f32 v119,v23,v23
M 26,a[232:235],86
L 5
v_mul_f32 v120,v24,v24
v_mul_f32 v121,v25,v25
v_fma_f32 v118,v100,v118,v99
v_fma_f32 v119,v100,v119,v99
v_fma_f32 v120,v100,v120,v99
v_fma_f32 v121,v100,v121,v99
v_mul_f32 v118,v22,v118
v_mul_f32 v119,v23,v119
v_mul_f32 v120,v24,v120
v_mul_f32 v121,v25,v121
v_exp_f32 v118,v118
v_exp_f32 v119,v119
v_exp_f32 v120,v120
v_exp_f32 v121,v121
M 26,a[240:243],90
L 4
R 46,1,3072
v_fma_f32 v118,v118,v101,v101
v_fma_f32 v119,v119,v101,v101
v_fma_f32 v120,v120,v101,v101
v_fma_f32 v121,v121,v101,v101
v_rcp_f32 v118,v118
v_rcp_f32 v119,v119
v_rcp_f32 v120,v120
v_rcp_f32 v121,v121
v_cmp_eq_u32 vcc,s53,v7
M 26,a[248:251],94
v_cvt_pk_bf16_f32 v123,v20,v21
v_mul_f32 v22,v22,v118
v_mul_f32 v23,v23,v119
v_mul_f32 v24,v24,v120
v_mul_f32 v25,v25,v121
v_cvt_pk_bf16_f32 v124,v22,v23
v_cvt_pk_bf16_f32 v125,v24,v25
M 30,a[244:247],90
L 2
s_cmp_eq_u32 vcc_lo,-1
R 50,1,4096
buffer_store_dwordx4 v[122:125],v0,s[36:39],s66 offen offset:0 sc0 sc1
M 30,a[252:255],94
s_cbranch_scc1 Lg3o_F0_%=
Lg3o_K0_%=:
Lg3o_L0_%=:
v_mfma_f32_16x16x32_bf16 v[22:25],a[4:7],v[34:37],0
s_add_u32 m0,s59,3072
R 54,1,5120
v_add_f32_dpp v131,v130,v130 quad_perm:[1,0,3,2] row_mask:0xf bank_mask:0xf
M 22,a[12:15],38
buffer_load_dword v8,s[36:39],s58 offen offset:0 sc1 lds
v_mfma_f32_16x16x32_bf16 v[18:21],a[0:3],v[34:37],0
s_mov_b32 s66,s65
R 58,1,6144
v_add_f32_dpp v131,v131,v131 quad_perm:[2,3,0,1] row_mask:0xf bank_mask:0xf
M 18,a[8:11],38
L 3
v_fma_f32 v26,-v102,v106,v26
v_fma_f32 v27,-v102,v107,v27
M 18,a[16:19],42
s_add_u32 s65,s65,s62
R 62,1,7168
v_add_f32_dpp v134,v131,v131 row_half_mirror row_mask:0xf bank_mask:0xf
M 18,a[24:27],46
v_fma_f32 v28,-v102,v108,v28
v_fma_f32 v29,-v102,v109,v29
M 22,a[20:23],42
R 66,1,8192
v_mov_b32_dpp v131,v134 row_ror:8 row_mask:0xf bank_mask:0xf
M 22,a[28:31],46
L 3
v_fma_f32 v131,-v134,v134,v131
v_max_f32 v131,s87,v131
M 22,a[36:39],50
R 70,1,9216
v_mul_f32 v134,v17,v134
M 22,a[44:47],54
v_rsq_f32 v132,v131
M 18,a[32:35],50
R 74,1,10240
v_fma_f32 v131,v15,v131,v16
M 18,a[40:43],54
L 3
v_mul_f32 v135,v132,v132
v_fma_f32 v131,v14,v135,v131
M 18,a[48:51],58
R 78,1,11264
v_fma_f32 v134,v132,v131,v134
M 18,a[56:59],62
ds_write_b32 v12,v134 offset:0
M 22,a[52:55],58
R 82,1,12288
v_mul_f32 v114,v26,v26
M 22,a[60:63],62
L 4
v_mul_f32 v115,v27,v27
v_mul_f32 v116,v28,v28
M 22,a[68:71],66
R 86,1,13312
v_mul_f32 v117,v29,v29
M 22,a[76:79],70
v_fma_f32 v114,v104,v114,v103
v_fma_f32 v115,v104,v115,v103
M 18,a[64:67],66
R 90,1,14336
v_fma_f32 v116,v104,v116,v103
M 18,a[72:75],70
L 4
v_fma_f32 v117,v104,v117,v103
v_mul_f32 v114,v26,v114
M 18,a[80:83],74
s_waitcnt vmcnt(0)
R 94,1,15360
v_mul_f32 v115,v27,v115
M 18,a[88:91],78
s_barrier
ds_read_b32 v7,v6 offset:3072
v_mul_f32 v116,v28,v116
M 22,a[84:87],74
R 34,1,16384
v_mul_f32 v117,v29,v117
M 22,a[92:95],78
L 4
buffer_store_dword v4,v3,s[36:39],s70 offen offset:0 sc0 sc1
M 22,a[100:103],82
s_mov_b32 s70,0x7fff0000
R 38,1,17408
v_fma_f32 v30,-v102,v110,v30
M 22,a[108:111],86
v_exp_f32 v114,v114
M 18,a[96:99],82
R 42,1,18432
v_fma_f32 v114,v114,v105,v105
M 18,a[104:107],86
L 4
L 3
v_cmp_eq_u32 vcc,s53,v7
M 18,a[112:115],90
R 46,1,19456
v_fma_f32 v31,-v102,v111,v31
M 18,a[120:123],94
v_exp_f32 v115,v115
M 22,a[116:119],90
s_cmp_eq_u32 vcc_lo,-1
R 50,1,20480
v_fma_f32 v115,v115,v105,v105
s_cbranch_scc0 Lg3o_X0_%=
M 22,a[124:127],94
L 3
s_add_u32 m0,s57,0
s_add_u32 s56,s55,0
buffer_load_dword v130,v11,s[36:39],s73 offen offset:0 sc1
M 22,a[132:135],34
s_add_u32 s58,s58,s61
R 54,1,21504
v_fma_f32 v32,-v102,v112,v32
M 22,a[140:143],38
D 0,56,0
M 18,a[128:131],34
s_add_u32 s54,s54,s46
R 58,1,22528
v_fma_f32 v33,-v102,v113,v33
M 18,a[136:139],38
L 3
v_exp_f32 v116,v116
M 18,a[144:147],42
s_add_u32 s73,s73,s64
R 62,1,23552
v_fma_f32 v116,v116,v105,v105
M 18,a[152:155],46
D 0,56,1024
M 22,a[148:151],42
R 66,1,24576
v_mul_f32 v118,v30,v30
M 22,a[156:159],46
L 3
v_exp_f32 v117,v117
M 22,a[164:167],50
R 70,1,25600
v_fma_f32 v117,v117,v105,v105
M 22,a[172:175],54
D 0,56,2048
M 18,a[160:163],50
R 74,1,26624
v_mul_f32 v119,v31,v31
M 18,a[168:171],54
L 3
v_rcp_f32 v114,v114
M 18,a[176:179],58
R 78,1,27648
v_mul_f32 v26,v26,v114
M 18,a[184:187],62
D 0,56,3072
M 22,a[180:183],58
s_add_u32 m0,s59,0
R 82,1,28672
v_mul_f32 v120,v32,v32
M 22,a[188:191],62
L 3
buffer_load_dword v8,s[36:39],s58 offen offset:0 sc1 lds
M 22,a[196:199],66
s_add_u32 m0,s57,16384
R 86,1,29696
v_mul_f32 v121,v33,v33
M 22,a[204:207],70
s_add_u32 s56,s55,16384
v_rcp_f32 v115,v115
M 18,a[192:195],66
s_add_u32 s55,s55,s60
R 90,1,30720
v_mul_f32 v27,v27,v115
M 18,a[200:203],70
L 3
s_nop 0
D 0,56,0
M 18,a[208:211],74
s_waitcnt vmcnt(1)
R 94,1,31744
v_cvt_pk_bf16_f32 v122,v26,v27
M 18,a[216:219],78
s_barrier
ds_read_b32 v7,v6 offset:0
R 98,13,0
M 22,a[212:215],74
s_sub_u32 s57,s84,s57
R 34,2,0
v_fma_f32 v118,v104,v118,v103
M 22,a[220:223],78
L 5
buffer_store_dword v4,v3,s[36:39],s70 offen offset:0 sc0 sc1
M 22,a[228:231],82
s_mov_b32 s70,s69
R 38,2,1024
v_fma_f32 v119,v104,v119,v103
M 22,a[236:239],86
D 0,56,1024
M 18,a[224:227],82
s_mov_b32 s69,s68
R 42,2,2048
v_fma_f32 v120,v104,v120,v103
M 18,a[232:235],86
L 5
v_fma_f32 v121,v104,v121,v103
v_mul_f32 v118,v30,v118
v_mul_f32 v119,v31,v119
v_mul_f32 v120,v32,v120
v_mul_f32 v121,v33,v121
v_exp_f32 v118,v118
v_exp_f32 v119,v119
v_exp_f32 v120,v120
v_exp_f32 v121,v121
M 18,a[240:243],90
L 4
R 46,2,3072
v_rcp_f32 v116,v116
v_rcp_f32 v117,v117
v_fma_f32 v118,v118,v105,v105
v_fma_f32 v119,v119,v105,v105
v_fma_f32 v120,v120,v105,v105
v_fma_f32 v121,v121,v105,v105
v_rcp_f32 v118,v118
v_rcp_f32 v119,v119
v_rcp_f32 v120,v120
v_rcp_f32 v121,v121
v_cmp_eq_u32 vcc,s53,v7
M 18,a[248:251],94
s_add_u32 s68,s68,s63
v_mul_f32 v28,v28,v116
v_mul_f32 v29,v29,v117
v_cvt_pk_bf16_f32 v123,v28,v29
v_mul_f32 v30,v30,v118
v_mul_f32 v31,v31,v119
v_mul_f32 v32,v32,v120
v_mul_f32 v33,v33,v121
v_cvt_pk_bf16_f32 v124,v30,v31
v_cvt_pk_bf16_f32 v125,v32,v33
M 22,a[244:247],90
L 2
s_cmp_eq_u32 vcc_lo,-1
R 50,2,4096
buffer_store_dwordx4 v[122:125],v0,s[36:39],s67 offen offset:0 sc0 sc1
M 22,a[252:255],94
D 0,56,2048
D 0,56,3072
buffer_store_dword v4,v3,s[36:39],s71 offen offset:0 sc0 sc1
buffer_store_dword v4,v3,s[36:39],s71 offen offset:0 sc0 sc1
s_cbranch_scc1 Lg3o_F1_%=
s_branch Lg3o_K1_%=
Lg3o_X0_%=:
M 22,a[124:127],94
L 3
s_add_u32 m0,s59,0
v_fma_f32 v32,-v102,v112,v32
M 22,a[132:135],34
R 54,1,21504
v_fma_f32 v33,-v102,v113,v33
M 22,a[140:143],38
buffer_load_dword v8,s[36:39],s58 offen offset:0 sc1 lds
M 18,a[128:131],34
R 58,1,22528
v_mul_f32 v118,v30,v30
M 18,a[136:139],38
L 3
v_exp_f32 v116,v116
M 18,a[144:147],42
R 62,1,23552
v_fma_f32 v116,v116,v105,v105
M 18,a[152:155],46
v_exp_f32 v117,v117
M 22,a[148:151],42
R 66,1,24576
v_fma_f32 v117,v117,v105,v105
M 22,a[156:159],46
L 3
v_rcp_f32 v114,v114
v_mul_f32 v119,v31,v31
M 22,a[164:167],50
R 70,1,25600
v_mul_f32 v26,v26,v114
M 22,a[172:175],54
v_rcp_f32 v115,v115
M 18,a[160:163],50
R 74,1,26624
v_mul_f32 v27,v27,v115
M 18,a[168:171],54
L 3
v_rcp_f32 v116,v116
M 18,a[176:179],58
R 78,1,27648
v_mul_f32 v28,v28,v116
M 18,a[184:187],62
v_rcp_f32 v117,v117
M 22,a[180:183],58
R 82,1,28672
v_mul_f32 v29,v29,v117
M 22,a[188:191],62
L 3
v_cvt_pk_bf16_f32 v122,v26,v27
v_cvt_pk_bf16_f32 v123,v28,v29
M 22,a[196:199],66
R 86,1,29696
v_mul_f32 v120,v32,v32
M 22,a[204:207],70
v_mul_f32 v121,v33,v33
v_fma_f32 v118,v104,v118,v103
M 18,a[192:195],66
R 90,1,30720
v_fma_f32 v119,v104,v119,v103
M 18,a[200:203],70
L 3
s_nop 0
v_fma_f32 v120,v104,v120,v103
M 18,a[208:211],74
s_waitcnt vmcnt(0)
R 94,1,31744
v_fma_f32 v121,v104,v121,v103
M 18,a[216:219],78
s_barrier
buffer_store_dword v4,v3,s[36:39],s70 offen offset:0 sc0 sc1
M 22,a[212:215],74
s_mov_b32 s70,s69
ds_read_b32 v7,v6 offset:0
R 98,13,0
M 22,a[220:223],78
L 4
v_mul_f32 v118,v30,v118
v_mul_f32 v119,v31,v119
M 22,a[228:231],82
s_mov_b32 s69,s68
v_mul_f32 v120,v32,v120
v_mul_f32 v121,v33,v121
M 22,a[236:239],86
s_add_u32 s68,s68,s63
v_exp_f32 v118,v118
M 18,a[224:227],82
v_exp_f32 v119,v119
M 18,a[232:235],86
L 2
v_exp_f32 v120,v120
v_exp_f32 v121,v121
M 18,a[240:243],90
L 1
v_fma_f32 v118,v118,v105,v105
v_fma_f32 v119,v119,v105,v105
v_fma_f32 v120,v120,v105,v105
v_fma_f32 v121,v121,v105,v105
v_rcp_f32 v118,v118
)"
R"(v_rcp_f32 v119,v119
v_rcp_f32 v120,v120
v_rcp_f32 v121,v121
M 18,a[248:251],94
v_mul_f32 v30,v30,v118
v_mul_f32 v31,v31,v119
v_mul_f32 v32,v32,v120
v_mul_f32 v33,v33,v121
v_cvt_pk_bf16_f32 v124,v30,v31
v_cvt_pk_bf16_f32 v125,v32,v33
M 22,a[244:247],90
buffer_store_dwordx4 v[122:125],v0,s[36:39],s67 offen offset:0 sc0 sc1
v_cmp_eq_u32 vcc,s53,v7
M 22,a[252:255],94
s_mov_b32 s80,1
s_cmp_eq_u32 vcc_lo,-1
s_cbranch_scc0 Lg3o_xn0_%=
buffer_load_dword v130,v11,s[36:39],s73 offen offset:0 sc1
L 0
v_fma_f32 v18,-v98,v106,v18
v_fma_f32 v19,-v98,v107,v19
v_fma_f32 v20,-v98,v108,v20
v_fma_f32 v21,-v98,v109,v21
v_mul_f32 v114,v18,v18
v_mul_f32 v115,v19,v19
s_add_u32 s73,s73,s64
s_add_u32 m0,s57,0
s_add_u32 s56,s55,0
D 0,56,0
v_mul_f32 v116,v20,v20
v_mul_f32 v117,v21,v21
v_fma_f32 v114,v100,v114,v99
v_fma_f32 v115,v100,v115,v99
v_fma_f32 v116,v100,v116,v99
v_fma_f32 v117,v100,v117,v99
D 0,56,1024
v_mul_f32 v114,v18,v114
v_mul_f32 v115,v19,v115
v_mul_f32 v116,v20,v116
v_mul_f32 v117,v21,v117
v_exp_f32 v114,v114
v_exp_f32 v115,v115
D 0,56,2048
v_exp_f32 v116,v116
v_exp_f32 v117,v117
v_fma_f32 v114,v114,v101,v101
v_fma_f32 v115,v115,v101,v101
v_fma_f32 v116,v116,v101,v101
v_fma_f32 v117,v117,v101,v101
D 0,56,3072
v_rcp_f32 v114,v114
v_rcp_f32 v115,v115
v_rcp_f32 v116,v116
v_rcp_f32 v117,v117
v_mul_f32 v18,v18,v114
v_mul_f32 v19,v19,v115
s_add_u32 s58,s58,s61
s_add_u32 m0,s57,16384
s_add_u32 s56,s55,16384
D 0,56,0
v_mul_f32 v20,v20,v116
v_mul_f32 v21,v21,v117
v_cvt_pk_bf16_f32 v122,v18,v19
v_cvt_pk_bf16_f32 v123,v20,v21
v_fma_f32 v22,-v98,v110,v22
v_fma_f32 v23,-v98,v111,v23
D 0,56,1024
v_fma_f32 v24,-v98,v112,v24
v_fma_f32 v25,-v98,v113,v25
v_mul_f32 v118,v22,v22
v_mul_f32 v119,v23,v23
v_mul_f32 v120,v24,v24
v_mul_f32 v121,v25,v25
D 0,56,2048
v_fma_f32 v118,v100,v118,v99
v_fma_f32 v119,v100,v119,v99
v_fma_f32 v120,v100,v120,v99
v_fma_f32 v121,v100,v121,v99
v_mul_f32 v118,v22,v118
v_mul_f32 v119,v23,v119
D 0,56,3072
v_mul_f32 v120,v24,v120
v_mul_f32 v121,v25,v121
v_exp_f32 v118,v118
v_exp_f32 v119,v119
v_exp_f32 v120,v120
v_exp_f32 v121,v121
s_add_u32 s55,s55,s60
s_sub_u32 s57,s84,s57
s_add_u32 s54,s54,s46
v_fma_f32 v118,v118,v101,v101
v_fma_f32 v119,v119,v101,v101
v_fma_f32 v120,v120,v101,v101
v_fma_f32 v121,v121,v101,v101
v_rcp_f32 v118,v118
v_rcp_f32 v119,v119
v_rcp_f32 v120,v120
v_rcp_f32 v121,v121
v_mul_f32 v22,v22,v118
v_mul_f32 v23,v23,v119
v_mul_f32 v24,v24,v120
v_mul_f32 v25,v25,v121
v_cvt_pk_bf16_f32 v124,v22,v23
v_cvt_pk_bf16_f32 v125,v24,v25
buffer_store_dwordx4 v[122:125],v0,s[36:39],s66 offen offset:0 sc0 sc1
s_mov_b32 s72,s69
s_mov_b32 s69,0x7fff0000
s_mov_b32 s66,0x7fff0000
s_mov_b32 s76,0x7fff0000
s_branch Lg3o_gox_%=
Lg3o_xn0_%=:
s_cmp_ge_u32 s54,256
s_cbranch_scc1 Lg3o_fin0_%=
s_add_u32 m0,s59,4096
s_nop 0
buffer_load_dword v8,s[36:39],s58 offen offset:0 sc1 lds
buffer_store_dword v4,v3,s[36:39],s71 offen offset:0 sc0 sc1
s_add_u32 m0,s59,5120
s_nop 0
buffer_load_dword v8,s[36:39],s58 offen offset:0 sc1 lds
buffer_store_dword v4,v3,s[36:39],s71 offen offset:0 sc0 sc1
s_mov_b32 s83,0
L 0
v_fma_f32 v18,-v98,v106,v18
v_fma_f32 v19,-v98,v107,v19
v_fma_f32 v20,-v98,v108,v20
v_fma_f32 v21,-v98,v109,v21
v_mul_f32 v114,v18,v18
v_mul_f32 v115,v19,v19
v_mul_f32 v116,v20,v20
v_mul_f32 v117,v21,v21
v_fma_f32 v114,v100,v114,v99
v_fma_f32 v115,v100,v115,v99
v_fma_f32 v116,v100,v116,v99
v_fma_f32 v117,v100,v117,v99
v_mul_f32 v114,v18,v114
v_mul_f32 v115,v19,v115
v_mul_f32 v116,v20,v116
v_mul_f32 v117,v21,v117
v_exp_f32 v114,v114
v_exp_f32 v115,v115
v_exp_f32 v116,v116
v_exp_f32 v117,v117
v_fma_f32 v114,v114,v101,v101
v_fma_f32 v115,v115,v101,v101
v_fma_f32 v116,v116,v101,v101
v_fma_f32 v117,v117,v101,v101
v_rcp_f32 v114,v114
v_rcp_f32 v115,v115
v_rcp_f32 v116,v116
v_rcp_f32 v117,v117
v_mul_f32 v18,v18,v114
v_mul_f32 v19,v19,v115
v_mul_f32 v20,v20,v116
v_mul_f32 v21,v21,v117
v_cvt_pk_bf16_f32 v122,v18,v19
v_cvt_pk_bf16_f32 v123,v20,v21
v_fma_f32 v22,-v98,v110,v22
v_fma_f32 v23,-v98,v111,v23
v_fma_f32 v24,-v98,v112,v24
v_fma_f32 v25,-v98,v113,v25
v_mul_f32 v118,v22,v22
v_mul_f32 v119,v23,v23
v_mul_f32 v120,v24,v24
v_mul_f32 v121,v25,v25
v_fma_f32 v118,v100,v118,v99
v_fma_f32 v119,v100,v119,v99
v_fma_f32 v120,v100,v120,v99
v_fma_f32 v121,v100,v121,v99
v_mul_f32 v118,v22,v118
v_mul_f32 v119,v23,v119
v_mul_f32 v120,v24,v120
v_mul_f32 v121,v25,v121
v_exp_f32 v118,v118
v_exp_f32 v119,v119
v_exp_f32 v120,v120
v_exp_f32 v121,v121
v_fma_f32 v118,v118,v101,v101
v_fma_f32 v119,v119,v101,v101
v_fma_f32 v120,v120,v101,v101
v_fma_f32 v121,v121,v101,v101
v_rcp_f32 v118,v118
v_rcp_f32 v119,v119
v_rcp_f32 v120,v120
v_rcp_f32 v121,v121
v_mul_f32 v22,v22,v118
v_mul_f32 v23,v23,v119
v_mul_f32 v24,v24,v120
v_mul_f32 v25,v25,v121
v_cvt_pk_bf16_f32 v124,v22,v23
v_cvt_pk_bf16_f32 v125,v24,v25
buffer_store_dwordx4 v[122:125],v0,s[36:39],s66 offen offset:0 sc0 sc1
s_mov_b32 s72,s69
s_mov_b32 s69,0x7fff0000
s_mov_b32 s66,0x7fff0000
s_mov_b32 s76,0x7fff0000
s_branch Lg3o_poll_%=
Lg3o_fin0_%=:
L 0
v_fma_f32 v18,-v98,v106,v18
v_fma_f32 v19,-v98,v107,v19
v_fma_f32 v20,-v98,v108,v20
v_fma_f32 v21,-v98,v109,v21
v_mul_f32 v114,v18,v18
v_mul_f32 v115,v19,v19
v_mul_f32 v116,v20,v20
v_mul_f32 v117,v21,v21
v_fma_f32 v114,v100,v114,v99
v_fma_f32 v115,v100,v115,v99
v_fma_f32 v116,v100,v116,v99
v_fma_f32 v117,v100,v117,v99
v_mul_f32 v114,v18,v114
v_mul_f32 v115,v19,v115
v_mul_f32 v116,v20,v116
v_mul_f32 v117,v21,v117
v_exp_f32 v114,v114
v_exp_f32 v115,v115
v_exp_f32 v116,v116
v_exp_f32 v117,v117
v_fma_f32 v114,v114,v101,v101
v_fma_f32 v115,v115,v101,v101
v_fma_f32 v116,v116,v101,v101
v_fma_f32 v117,v117,v101,v101
v_rcp_f32 v114,v114
v_rcp_f32 v115,v115
v_rcp_f32 v116,v116
v_rcp_f32 v117,v117
v_mul_f32 v18,v18,v114
v_mul_f32 v19,v19,v115
v_mul_f32 v20,v20,v116
v_mul_f32 v21,v21,v117
v_cvt_pk_bf16_f32 v122,v18,v19
v_cvt_pk_bf16_f32 v123,v20,v21
v_fma_f32 v22,-v98,v110,v22
v_fma_f32 v23,-v98,v111,v23
v_fma_f32 v24,-v98,v112,v24
v_fma_f32 v25,-v98,v113,v25
v_mul_f32 v118,v22,v22
v_mul_f32 v119,v23,v23
v_mul_f32 v120,v24,v24
v_mul_f32 v121,v25,v25
v_fma_f32 v118,v100,v118,v99
v_fma_f32 v119,v100,v119,v99
v_fma_f32 v120,v100,v120,v99
v_fma_f32 v121,v100,v121,v99
v_mul_f32 v118,v22,v118
v_mul_f32 v119,v23,v119
v_mul_f32 v120,v24,v120
v_mul_f32 v121,v25,v121
v_exp_f32 v118,v118
v_exp_f32 v119,v119
v_exp_f32 v120,v120
v_exp_f32 v121,v121
v_fma_f32 v118,v118,v101,v101
v_fma_f32 v119,v119,v101,v101
v_fma_f32 v120,v120,v101,v101
v_fma_f32 v121,v121,v101,v101
v_rcp_f32 v118,v118
v_rcp_f32 v119,v119
v_rcp_f32 v120,v120
v_rcp_f32 v121,v121
v_mul_f32 v22,v22,v118
v_mul_f32 v23,v23,v119
v_mul_f32 v24,v24,v120
v_mul_f32 v25,v25,v121
v_cvt_pk_bf16_f32 v124,v22,v23
v_cvt_pk_bf16_f32 v125,v24,v25
buffer_store_dwordx4 v[122:125],v0,s[36:39],s66 offen offset:0 sc0 sc1
s_mov_b32 s72,s69
s_mov_b32 s69,0x7fff0000
s_mov_b32 s66,0x7fff0000
s_mov_b32 s76,0x7fff0000
s_branch Lg3o_done_%=
Lg3o_K1_%=:
Lg3o_L1_%=:
v_mfma_f32_16x16x32_bf16 v[30:33],a[4:7],v[34:37],0
s_add_u32 m0,s59,3072
R 54,2,5120
v_add_f32_dpp v131,v130,v130 quad_perm:[1,0,3,2] row_mask:0xf bank_mask:0xf
M 30,a[12:15],38
buffer_load_dword v8,s[36:39],s58 offen offset:0 sc1 lds
v_mfma_f32_16x16x32_bf16 v[26:29],a[0:3],v[34:37],0
s_mov_b32 s67,s65
R 58,2,6144
v_add_f32_dpp v131,v131,v131 quad_perm:[2,3,0,1] row_mask:0xf bank_mask:0xf
M 26,a[8:11],38
L 3
v_fma_f32 v18,-v98,v106,v18
v_fma_f32 v19,-v98,v107,v19
M 26,a[16:19],42
s_add_u32 s65,s65,s62
R 62,2,7168
v_add_f32_dpp v134,v131,v131 row_half_mirror row_mask:0xf bank_mask:0xf
M 26,a[24:27],46
v_fma_f32 v20,-v98,v108,v20
v_fma_f32 v21,-v98,v109,v21
M 30,a[20:23],42
R 66,2,8192
v_mov_b32_dpp v131,v134 row_ror:8 row_mask:0xf bank_mask:0xf
M 30,a[28:31],46
L 3
v_fma_f32 v131,-v134,v134,v131
v_max_f32 v131,s87,v131
M 30,a[36:39],50
R 70,2,9216
v_mul_f32 v134,v17,v134
M 30,a[44:47],54
v_rsq_f32 v132,v131
M 26,a[32:35],50
R 74,2,10240
v_fma_f32 v131,v15,v131,v16
M 26,a[40:43],54
L 3
v_mul_f32 v135,v132,v132
v_fma_f32 v131,v14,v135,v131
M 26,a[48:51],58
R 78,2,11264
v_fma_f32 v134,v132,v131,v134
M 26,a[56:59],62
ds_write_b32 v12,v134 offset:2048
M 30,a[52:55],58
R 82,2,12288
v_mul_f32 v114,v18,v18
M 30,a[60:63],62
L 4
v_mul_f32 v115,v19,v19
v_mul_f32 v116,v20,v20
M 30,a[68:71],66
R 86,2,13312
v_mul_f32 v117,v21,v21
M 30,a[76:79],70
v_fma_f32 v114,v100,v114,v99
v_fma_f32 v115,v100,v115,v99
M 26,a[64:67],66
R 90,2,14336
v_fma_f32 v116,v100,v116,v99
M 26,a[72:75],70
L 4
v_fma_f32 v117,v100,v117,v99
v_mul_f32 v114,v18,v114
M 26,a[80:83],74
s_waitcnt vmcnt(0)
R 94,2,15360
v_mul_f32 v115,v19,v115
M 26,a[88:91],78
s_barrier
ds_read_b32 v7,v6 offset:3072
v_mul_f32 v116,v20,v116
M 30,a[84:87],74
R 34,2,16384
v_mul_f32 v117,v21,v117
M 30,a[92:95],78
L 4
buffer_store_dword v4,v3,s[36:39],s70 offen offset:0 sc0 sc1
M 30,a[100:103],82
s_mov_b32 s70,0x7fff0000
R 38,2,17408
v_fma_f32 v22,-v98,v110,v22
M 30,a[108:111],86
v_exp_f32 v114,v114
M 26,a[96:99],82
R 42,2,18432
v_fma_f32 v114,v114,v101,v101
M 26,a[104:107],86
L 4
L 3
v_cmp_eq_u32 vcc,s53,v7
M 26,a[112:115],90
R 46,2,19456
v_fma_f32 v23,-v98,v111,v23
M 26,a[120:123],94
v_exp_f32 v115,v115
M 30,a[116:119],90
s_cmp_eq_u32 vcc_lo,-1
R 50,2,20480
v_fma_f32 v115,v115,v101,v101
s_cbranch_scc0 Lg3o_X1_%=
M 30,a[124:127],94
L 3
s_add_u32 m0,s57,0
s_add_u32 s56,s55,0
buffer_load_dword v130,v11,s[36:39],s73 offen offset:0 sc1
M 30,a[132:135],34
s_add_u32 s58,s58,s61
R 54,2,21504
v_fma_f32 v24,-v98,v112,v24
M 30,a[140:143],38
D 0,56,0
M 26,a[128:131],34
s_add_u32 s54,s54,s46
R 58,2,22528
v_fma_f32 v25,-v98,v113,v25
M 26,a[136:139],38
L 3
v_exp_f32 v116,v116
M 26,a[144:147],42
s_add_u32 s73,s73,s64
R 62,2,23552
v_fma_f32 v116,v116,v101,v101
M 26,a[152:155],46
D 0,56,1024
M 30,a[148:151],42
R 66,2,24576
v_mul_f32 v118,v22,v22
M 30,a[156:159],46
L 3
v_exp_f32 v117,v117
M 30,a[164:167],50
R 70,2,25600
v_fma_f32 v117,v117,v101,v101
M 30,a[172:175],54
D 0,56,2048
M 26,a[160:163],50
R 74,2,26624
v_mul_f32 v119,v23,v23
M 26,a[168:171],54
L 3
v_rcp_f32 v114,v114
M 26,a[176:179],58
R 78,2,27648
v_mul_f32 v18,v18,v114
M 26,a[184:187],62
D 0,56,3072
M 30,a[180:183],58
s_add_u32 m0,s59,1024
R 82,2,28672
v_mul_f32 v120,v24,v24
M 30,a[188:191],62
L 3
buffer_load_dword v8,s[36:39],s58 offen offset:0 sc1 lds
M 30,a[196:199],66
s_add_u32 m0,s57,16384
R 86,2,29696
v_mul_f32 v121,v25,v25
M 30,a[204:207],70
s_add_u32 s56,s55,16384
v_rcp_f32 v115,v115
M 26,a[192:195],66
s_add_u32 s55,s55,s60
R 90,2,30720
v_mul_f32 v19,v19,v115
M 26,a[200:203],70
L 3
s_nop 0
D 0,56,0
M 26,a[208:211],74
s_waitcnt vmcnt(1)
R 94,2,31744
v_cvt_pk_bf16_f32 v122,v18,v19
M 26,a[216:219],78
s_barrier
ds_read_b32 v7,v6 offset:1024
R 102,13,2048
M 30,a[212:215],74
s_sub_u32 s57,s84,s57
R 34,1,0
v_fma_f32 v118,v100,v118,v99
M 30,a[220:223],78
L 5
buffer_store_dword v4,v3,s[36:39],s70 offen offset:0 sc0 sc1
M 30,a[228:231],82
s_mov_b32 s70,s69
R 38,1,1024
v_fma_f32 v119,v100,v119,v99
M 30,a[236:239],86
D 0,56,1024
M 26,a[224:227],82
s_mov_b32 s69,s68
R 42,1,2048
v_fma_f32 v120,v100,v120,v99
M 26,a[232:235],86
L 5
v_fma_f32 v121,v100,v121,v99
v_mul_f32 v118,v22,v118
v_mul_f32 v119,v23,v119
v_mul_f32 v120,v24,v120
v_mul_f32 v121,v25,v121
v_exp_f32 v118,v118
v_exp_f32 v119,v119
v_exp_f32 v120,v120
v_exp_f32 v121,v121
M 26,a[240:243],90
L 4
R 46,1,3072
v_rcp_f32 v116,v116
v_rcp_f32 v117,v117
v_fma_f32 v118,v118,v101,v101
v_fma_f32 v119,v119,v101,v101
v_fma_f32 v120,v120,v101,v101
v_fma_f32 v121,v121,v101,v101
v_rcp_f32 v118,v118
v_rcp_f32 v119,v119
v_rcp_f32 v120,v120
v_rcp_f32 v121,v121
v_cmp_eq_u32 vcc,s53,v7
M 26,a[248:251],94
s_add_u32 s68,s68,s63
v_mul_f32 v20,v20,v116
v_mul_f32 v21,v21,v117
v_cvt_pk_bf16_f32 v123,v20,v21
)"
R"(v_mul_f32 v22,v22,v118
v_mul_f32 v23,v23,v119
v_mul_f32 v24,v24,v120
v_mul_f32 v25,v25,v121
v_cvt_pk_bf16_f32 v124,v22,v23
v_cvt_pk_bf16_f32 v125,v24,v25
M 30,a[244:247],90
L 2
s_cmp_eq_u32 vcc_lo,-1
R 50,1,4096
buffer_store_dwordx4 v[122:125],v0,s[36:39],s66 offen offset:0 sc0 sc1
M 30,a[252:255],94
D 0,56,2048
D 0,56,3072
buffer_store_dword v4,v3,s[36:39],s71 offen offset:0 sc0 sc1
buffer_store_dword v4,v3,s[36:39],s71 offen offset:0 sc0 sc1
s_cbranch_scc1 Lg3o_F0_%=
s_branch Lg3o_K0_%=
Lg3o_X1_%=:
M 30,a[124:127],94
L 3
s_add_u32 m0,s59,1024
v_fma_f32 v24,-v98,v112,v24
M 30,a[132:135],34
R 54,2,21504
v_fma_f32 v25,-v98,v113,v25
M 30,a[140:143],38
buffer_load_dword v8,s[36:39],s58 offen offset:0 sc1 lds
M 26,a[128:131],34
R 58,2,22528
v_mul_f32 v118,v22,v22
M 26,a[136:139],38
L 3
v_exp_f32 v116,v116
M 26,a[144:147],42
R 62,2,23552
v_fma_f32 v116,v116,v101,v101
M 26,a[152:155],46
v_exp_f32 v117,v117
M 30,a[148:151],42
R 66,2,24576
v_fma_f32 v117,v117,v101,v101
M 30,a[156:159],46
L 3
v_rcp_f32 v114,v114
v_mul_f32 v119,v23,v23
M 30,a[164:167],50
R 70,2,25600
v_mul_f32 v18,v18,v114
M 30,a[172:175],54
v_rcp_f32 v115,v115
M 26,a[160:163],50
R 74,2,26624
v_mul_f32 v19,v19,v115
M 26,a[168:171],54
L 3
v_rcp_f32 v116,v116
M 26,a[176:179],58
R 78,2,27648
v_mul_f32 v20,v20,v116
M 26,a[184:187],62
v_rcp_f32 v117,v117
M 30,a[180:183],58
R 82,2,28672
v_mul_f32 v21,v21,v117
M 30,a[188:191],62
L 3
v_cvt_pk_bf16_f32 v122,v18,v19
v_cvt_pk_bf16_f32 v123,v20,v21
M 30,a[196:199],66
R 86,2,29696
v_mul_f32 v120,v24,v24
M 30,a[204:207],70
v_mul_f32 v121,v25,v25
v_fma_f32 v118,v100,v118,v99
M 26,a[192:195],66
R 90,2,30720
v_fma_f32 v119,v100,v119,v99
M 26,a[200:203],70
L 3
s_nop 0
v_fma_f32 v120,v100,v120,v99
M 26,a[208:211],74
s_waitcnt vmcnt(0)
R 94,2,31744
v_fma_f32 v121,v100,v121,v99
M 26,a[216:219],78
s_barrier
buffer_store_dword v4,v3,s[36:39],s70 offen offset:0 sc0 sc1
M 30,a[212:215],74
s_mov_b32 s70,s69
ds_read_b32 v7,v6 offset:1024
R 102,13,2048
M 30,a[220:223],78
L 4
v_mul_f32 v118,v22,v118
v_mul_f32 v119,v23,v119
M 30,a[228:231],82
s_mov_b32 s69,s68
v_mul_f32 v120,v24,v120
v_mul_f32 v121,v25,v121
M 30,a[236:239],86
s_add_u32 s68,s68,s63
v_exp_f32 v118,v118
M 26,a[224:227],82
v_exp_f32 v119,v119
M 26,a[232:235],86
L 2
v_exp_f32 v120,v120
v_exp_f32 v121,v121
M 26,a[240:243],90
L 1
v_fma_f32 v118,v118,v101,v101
v_fma_f32 v119,v119,v101,v101
v_fma_f32 v120,v120,v101,v101
v_fma_f32 v121,v121,v101,v101
v_rcp_f32 v118,v118
v_rcp_f32 v119,v119
v_rcp_f32 v120,v120
v_rcp_f32 v121,v121
M 26,a[248:251],94
v_mul_f32 v22,v22,v118
v_mul_f32 v23,v23,v119
v_mul_f32 v24,v24,v120
v_mul_f32 v25,v25,v121
v_cvt_pk_bf16_f32 v124,v22,v23
v_cvt_pk_bf16_f32 v125,v24,v25
M 30,a[244:247],90
buffer_store_dwordx4 v[122:125],v0,s[36:39],s66 offen offset:0 sc0 sc1
v_cmp_eq_u32 vcc,s53,v7
M 30,a[252:255],94
s_mov_b32 s80,0
s_cmp_eq_u32 vcc_lo,-1
s_cbranch_scc0 Lg3o_xn1_%=
buffer_load_dword v130,v11,s[36:39],s73 offen offset:0 sc1
L 0
v_fma_f32 v26,-v102,v106,v26
v_fma_f32 v27,-v102,v107,v27
v_fma_f32 v28,-v102,v108,v28
v_fma_f32 v29,-v102,v109,v29
v_mul_f32 v114,v26,v26
v_mul_f32 v115,v27,v27
s_add_u32 s73,s73,s64
s_add_u32 m0,s57,0
s_add_u32 s56,s55,0
D 0,56,0
v_mul_f32 v116,v28,v28
v_mul_f32 v117,v29,v29
v_fma_f32 v114,v104,v114,v103
v_fma_f32 v115,v104,v115,v103
v_fma_f32 v116,v104,v116,v103
v_fma_f32 v117,v104,v117,v103
D 0,56,1024
v_mul_f32 v114,v26,v114
v_mul_f32 v115,v27,v115
v_mul_f32 v116,v28,v116
v_mul_f32 v117,v29,v117
v_exp_f32 v114,v114
v_exp_f32 v115,v115
D 0,56,2048
v_exp_f32 v116,v116
v_exp_f32 v117,v117
v_fma_f32 v114,v114,v105,v105
v_fma_f32 v115,v115,v105,v105
v_fma_f32 v116,v116,v105,v105
v_fma_f32 v117,v117,v105,v105
D 0,56,3072
v_rcp_f32 v114,v114
v_rcp_f32 v115,v115
v_rcp_f32 v116,v116
v_rcp_f32 v117,v117
v_mul_f32 v26,v26,v114
v_mul_f32 v27,v27,v115
s_add_u32 s58,s58,s61
s_add_u32 m0,s57,16384
s_add_u32 s56,s55,16384
D 0,56,0
v_mul_f32 v28,v28,v116
v_mul_f32 v29,v29,v117
v_cvt_pk_bf16_f32 v122,v26,v27
v_cvt_pk_bf16_f32 v123,v28,v29
v_fma_f32 v30,-v102,v110,v30
v_fma_f32 v31,-v102,v111,v31
D 0,56,1024
v_fma_f32 v32,-v102,v112,v32
v_fma_f32 v33,-v102,v113,v33
v_mul_f32 v118,v30,v30
v_mul_f32 v119,v31,v31
v_mul_f32 v120,v32,v32
v_mul_f32 v121,v33,v33
D 0,56,2048
v_fma_f32 v118,v104,v118,v103
v_fma_f32 v119,v104,v119,v103
v_fma_f32 v120,v104,v120,v103
v_fma_f32 v121,v104,v121,v103
v_mul_f32 v118,v30,v118
v_mul_f32 v119,v31,v119
D 0,56,3072
v_mul_f32 v120,v32,v120
v_mul_f32 v121,v33,v121
v_exp_f32 v118,v118
v_exp_f32 v119,v119
v_exp_f32 v120,v120
v_exp_f32 v121,v121
s_add_u32 s55,s55,s60
s_sub_u32 s57,s84,s57
s_add_u32 s54,s54,s46
v_fma_f32 v118,v118,v105,v105
v_fma_f32 v119,v119,v105,v105
v_fma_f32 v120,v120,v105,v105
v_fma_f32 v121,v121,v105,v105
v_rcp_f32 v118,v118
v_rcp_f32 v119,v119
v_rcp_f32 v120,v120
v_rcp_f32 v121,v121
v_mul_f32 v30,v30,v118
v_mul_f32 v31,v31,v119
v_mul_f32 v32,v32,v120
v_mul_f32 v33,v33,v121
v_cvt_pk_bf16_f32 v124,v30,v31
v_cvt_pk_bf16_f32 v125,v32,v33
buffer_store_dwordx4 v[122:125],v0,s[36:39],s67 offen offset:0 sc0 sc1
s_mov_b32 s72,s69
s_mov_b32 s69,0x7fff0000
s_mov_b32 s67,0x7fff0000
s_mov_b32 s77,0x7fff0000
s_branch Lg3o_gox_%=
Lg3o_xn1_%=:
s_cmp_ge_u32 s54,256
s_cbranch_scc1 Lg3o_fin1_%=
s_add_u32 m0,s59,4096
s_nop 0
buffer_load_dword v8,s[36:39],s58 offen offset:0 sc1 lds
buffer_store_dword v4,v3,s[36:39],s71 offen offset:0 sc0 sc1
s_add_u32 m0,s59,5120
s_nop 0
buffer_load_dword v8,s[36:39],s58 offen offset:0 sc1 lds
buffer_store_dword v4,v3,s[36:39],s71 offen offset:0 sc0 sc1
s_mov_b32 s83,0
L 0
v_fma_f32 v26,-v102,v106,v26
v_fma_f32 v27,-v102,v107,v27
v_fma_f32 v28,-v102,v108,v28
v_fma_f32 v29,-v102,v109,v29
v_mul_f32 v114,v26,v26
v_mul_f32 v115,v27,v27
v_mul_f32 v116,v28,v28
v_mul_f32 v117,v29,v29
v_fma_f32 v114,v104,v114,v103
v_fma_f32 v115,v104,v115,v103
v_fma_f32 v116,v104,v116,v103
v_fma_f32 v117,v104,v117,v103
v_mul_f32 v114,v26,v114
v_mul_f32 v115,v27,v115
v_mul_f32 v116,v28,v116
v_mul_f32 v117,v29,v117
v_exp_f32 v114,v114
v_exp_f32 v115,v115
v_exp_f32 v116,v116
v_exp_f32 v117,v117
v_fma_f32 v114,v114,v105,v105
v_fma_f32 v115,v115,v105,v105
v_fma_f32 v116,v116,v105,v105
v_fma_f32 v117,v117,v105,v105
v_rcp_f32 v114,v114
v_rcp_f32 v115,v115
v_rcp_f32 v116,v116
v_rcp_f32 v117,v117
v_mul_f32 v26,v26,v114
v_mul_f32 v27,v27,v115
v_mul_f32 v28,v28,v116
v_mul_f32 v29,v29,v117
v_cvt_pk_bf16_f32 v122,v26,v27
v_cvt_pk_bf16_f32 v123,v28,v29
v_fma_f32 v30,-v102,v110,v30
v_fma_f32 v31,-v102,v111,v31
v_fma_f32 v32,-v102,v112,v32
v_fma_f32 v33,-v102,v113,v33
v_mul_f32 v118,v30,v30
v_mul_f32 v119,v31,v31
v_mul_f32 v120,v32,v32
v_mul_f32 v121,v33,v33
v_fma_f32 v118,v104,v118,v103
v_fma_f32 v119,v104,v119,v103
v_fma_f32 v120,v104,v120,v103
v_fma_f32 v121,v104,v121,v103
v_mul_f32 v118,v30,v118
v_mul_f32 v119,v31,v119
v_mul_f32 v120,v32,v120
v_mul_f32 v121,v33,v121
v_exp_f32 v118,v118
v_exp_f32 v119,v119
v_exp_f32 v120,v120
v_exp_f32 v121,v121
v_fma_f32 v118,v118,v105,v105
v_fma_f32 v119,v119,v105,v105
v_fma_f32 v120,v120,v105,v105
v_fma_f32 v121,v121,v105,v105
v_rcp_f32 v118,v118
v_rcp_f32 v119,v119
v_rcp_f32 v120,v120
v_rcp_f32 v121,v121
v_mul_f32 v30,v30,v118
v_mul_f32 v31,v31,v119
v_mul_f32 v32,v32,v120
v_mul_f32 v33,v33,v121
v_cvt_pk_bf16_f32 v124,v30,v31
v_cvt_pk_bf16_f32 v125,v32,v33
buffer_store_dwordx4 v[122:125],v0,s[36:39],s67 offen offset:0 sc0 sc1
s_mov_b32 s72,s69
s_mov_b32 s69,0x7fff0000
s_mov_b32 s67,0x7fff0000
s_mov_b32 s77,0x7fff0000
s_branch Lg3o_poll_%=
Lg3o_fin1_%=:
L 0
v_fma_f32 v26,-v102,v106,v26
v_fma_f32 v27,-v102,v107,v27
v_fma_f32 v28,-v102,v108,v28
v_fma_f32 v29,-v102,v109,v29
v_mul_f32 v114,v26,v26
v_mul_f32 v115,v27,v27
v_mul_f32 v116,v28,v28
v_mul_f32 v117,v29,v29
v_fma_f32 v114,v104,v114,v103
v_fma_f32 v115,v104,v115,v103
v_fma_f32 v116,v104,v116,v103
v_fma_f32 v117,v104,v117,v103
v_mul_f32 v114,v26,v114
v_mul_f32 v115,v27,v115
v_mul_f32 v116,v28,v116
v_mul_f32 v117,v29,v117
v_exp_f32 v114,v114
v_exp_f32 v115,v115
v_exp_f32 v116,v116
v_exp_f32 v117,v117
v_fma_f32 v114,v114,v105,v105
v_fma_f32 v115,v115,v105,v105
v_fma_f32 v116,v116,v105,v105
v_fma_f32 v117,v117,v105,v105
v_rcp_f32 v114,v114
v_rcp_f32 v115,v115
v_rcp_f32 v116,v116
v_rcp_f32 v117,v117
v_mul_f32 v26,v26,v114
v_mul_f32 v27,v27,v115
v_mul_f32 v28,v28,v116
v_mul_f32 v29,v29,v117
v_cvt_pk_bf16_f32 v122,v26,v27
v_cvt_pk_bf16_f32 v123,v28,v29
v_fma_f32 v30,-v102,v110,v30
v_fma_f32 v31,-v102,v111,v31
v_fma_f32 v32,-v102,v112,v32
v_fma_f32 v33,-v102,v113,v33
v_mul_f32 v118,v30,v30
v_mul_f32 v119,v31,v31
v_mul_f32 v120,v32,v32
v_mul_f32 v121,v33,v33
v_fma_f32 v118,v104,v118,v103
v_fma_f32 v119,v104,v119,v103
v_fma_f32 v120,v104,v120,v103
v_fma_f32 v121,v104,v121,v103
v_mul_f32 v118,v30,v118
v_mul_f32 v119,v31,v119
v_mul_f32 v120,v32,v120
v_mul_f32 v121,v33,v121
v_exp_f32 v118,v118
v_exp_f32 v119,v119
v_exp_f32 v120,v120
v_exp_f32 v121,v121
v_fma_f32 v118,v118,v105,v105
v_fma_f32 v119,v119,v105,v105
v_fma_f32 v120,v120,v105,v105
v_fma_f32 v121,v121,v105,v105
v_rcp_f32 v118,v118
v_rcp_f32 v119,v119
v_rcp_f32 v120,v120
v_rcp_f32 v121,v121
v_mul_f32 v30,v30,v118
v_mul_f32 v31,v31,v119
v_mul_f32 v32,v32,v120
v_mul_f32 v33,v33,v121
v_cvt_pk_bf16_f32 v124,v30,v31
v_cvt_pk_bf16_f32 v125,v32,v33
buffer_store_dwordx4 v[122:125],v0,s[36:39],s67 offen offset:0 sc0 sc1
s_mov_b32 s72,s69
s_mov_b32 s69,0x7fff0000
s_mov_b32 s67,0x7fff0000
s_mov_b32 s77,0x7fff0000
s_branch Lg3o_done_%=
Lg3o_done_%=:
s_waitcnt vmcnt(1)
s_barrier
buffer_store_dword v4,v3,s[36:39],s70 offen offset:0 sc0 sc1
s_waitcnt vmcnt(1)
s_barrier
buffer_store_dword v4,v3,s[36:39],s72 offen offset:0 sc0 sc1
s_waitcnt vmcnt(0) lgkmcnt(0)
s_barrier
s_endpgm
Lcode_g3o_e:
.purgem M
.purgem R
.purgem T
.purgem D
.purgem L
)"
  :: "s"(ws), "s"(__builtin_amdgcn_readfirstlane(sWave)), "s"(__builtin_amdgcn_readfirstlane(sG0)), "s"(__builtin_amdgcn_readfirstlane(sStep)), "s"(__builtin_amdgcn_readfirstlane(sOutOff)), "s"(__builtin_amdgcn_readfirstlane(sFoutOff)), "s"(__builtin_amdgcn_readfirstlane(sAuxOff)), "s"(__builtin_amdgcn_readfirstlane(sWw)), "s"(__builtin_amdgcn_readfirstlane(sBuf)), "s"(__builtin_amdgcn_readfirstlane(sExp)), "v"(vL16), "v"(vFlag), "v"(vSnapOff), "v"(vSnapRd), "v"(vWd0), "v"(vWd1), "v"(vWr0), "v"(vWr1), "v"(vWr2), "v"(vOutS), "v"(vAux0), "v"(vAux1), "v"(vAux2), "v"(vAux3), "v"(vAn), "v"(vAe), "s"(w3), "s"(out) : "v255", "v0", "v1", "v2", "v3", "v4", "v5", "v6", "v7", "v8", "v9", "v10", "v11", "v12", "v13", "v14", "v15", "v16", "v17", "v18", "v19", "v20", "v21", "v22", "v23", "v24", "v25", "v26", "v27", "v28", "v29", "v30", "v31", "v32", "v33", "v34", "v35", "v36", "v37", "v38", "v39", "v40", "v41", "v42", "v43", "v44", "v45", "v46", "v47", "v48", "v49", "v50", "v51", "v52", "v53", "v54", "v55", "v56", "v57", "v58", "v59", "v60", "v61", "v62", "v63", "v64", "v65", "v66", "v67", "v68", "v69", "v70", "v71", "v72", "v73", "v74", "v75", "v76", "v77", "v78", "v79", "v80", "v81", "v82", "v83", "v84", "v85", "v86", "v87", "v88", "v89", "v90", "v91", "v92", "v93", "v94", "v95", "v96", "v97", "v98", "v99", "v100", "v101", "v102", "v103", "v104", "v105", "v106", "v107", "v108", "v109", "v110", "v111", "v112", "v113", "v114", "v115", "v116", "v117", "v118", "v119", "v120", "v121", "v122", "v123", "v124", "v125", "v126", "v127", "v128", "v129", "v130", "v131", "v132", "v133", "v134", "v135", "v136", "v137", "v138", "v139", "v140", "v141", "v142", "v143", "v144", "v145", "v146", "v147", "v148", "v149", "v150", "v151", "v152", "v153", "v154", "v155", "v156", "v157", "v158", "v159", "v160", "v161", "v162", "v163", "v164", "v165", "v166", "v167", "v168", "v169", "v170", "v171", "v172", "v173", "v174", "v175", "v176", "v177", "v178", "v179", "v180", "v181", "v182", "v183", "v184", "v185", "v186", "v187", "v188", "v189", "v190", "v191", "v192", "v193", "v194", "v195", "v196", "v197", "v198", "v199", "v200", "v201", "v202", "v203", "v204", "v205", "v206", "v207", "v208", "v209", "v210", "v211", "v212", "v213", "v214", "v215", "v216", "v217", "v218", "v219", "v220", "v221", "v222", "v223", "v224", "v225", "v226", "v227", "v228", "v229", "v230", "v231", "a0", "a1", "a2", "a3", "a4", "a5", "a6", "a7", "a8", "a9", "a10", "a11", "a12", "a13", "a14", "a15", "a16", "a17", "a18", "a19", "a20", "a21", "a22", "a23", "a24", "a25", "a26", "a27", "a28", "a29", "a30", "a31", "a32", "a33", "a34", "a35", "a36", "a37", "a38", "a39", "a40", "a41", "a42", "a43", "a44", "a45", "a46", "a47", "a48", "a49", "a50", "a51", "a52", "a53", "a54", "a55", "a56", "a57", "a58", "a59", "a60", "a61", "a62", "a63", "a64", "a65", "a66", "a67", "a68", "a69", "a70", "a71", "a72", "a73", "a74", "a75", "a76", "a77", "a78", "a79", "a80", "a81", "a82", "a83", "a84", "a85", "a86", "a87", "a88", "a89", "a90", "a91", "a92", "a93", "a94", "a95", "a96", "a97", "a98", "a99", "a100", "a101", "a102", "a103", "a104", "a105", "a106", "a107", "a108", "a109", "a110", "a111", "a112", "a113", "a114", "a115", "a116", "a117", "a118", "a119", "a120", "a121", "a122", "a123", "a124", "a125", "a126", "a127", "a128", "a129", "a130", "a131", "a132", "a133", "a134", "a135", "a136", "a137", "a138", "a139", "a140", "a141", "a142", "a143", "a144", "a145", "a146", "a147", "a148", "a149", "a150", "a151", "a152", "a153", "a154", "a155", "a156", "a157", "a158", "a159", "a160", "a161", "a162", "a163", "a164", "a165", "a166", "a167", "a168", "a169", "a170", "a171", "a172", "a173", "a174", "a175", "a176", "a177", "a178", "a179", "a180", "a181", "a182", "a183", "a184", "a185", "a186", "a187", "a188", "a189", "a190", "a191", "a192", "a193", "a194", "a195", "a196", "a197", "a198", "a199", "a200", "a201", "a202", "a203", "a204", "a205", "a206", "a207", "a208", "a209", "a210", "a211", "a212", "a213", "a214", "a215", "a216", "a217", "a218", "a219", "a220", "a221", "a222", "a223", "a224", "a225", "a226", "a227", "a228", "a229", "a230", "a231", "a232", "a233", "a234", "a235", "a236", "a237", "a238", "a239", "a240", "a241", "a242", "a243", "a244", "a245", "a246", "a247", "a248", "a249", "a250", "a251", "a252", "a253", "a254", "a255", "s36", "s37", "s38", "s39", "s40", "s41", "s42", "s43", "s44", "s45", "s46", "s47", "s48", "s49", "s50", "s51", "s52", "s53", "s54", "s55", "s56", "s57", "s58", "s59", "s60", "s61", "s62", "s63", "s64", "s65", "s66", "s67", "s68", "s69", "s70", "s71", "s72", "s73", "s74", "s75", "s76", "s77", "s78", "s79", "s80", "s81", "s82", "s83", "s84", "s85", "s86", "s87", "s88", "s89", "s90", "s91", "s92", "s93", "s94", "s95", "s96", "s97", "vcc", "scc", "memory"); }
    __builtin_unreachable();
  }
  if (kind < 3) {
    const unsigned sStep = 4;
    COMMON(kind == 1 ? 8 : 16, kind == 1 ? fj * 128 : fj < 8 ? 1024 + fj * 128 : (fj & 2) * 1024 + (fj - 8) * 128, 1024, 131072, 2048, 256, kind == 1 ? 0 : 32768);
    const int sr = l15 - ((wave & 2) ? -1 : 1), sr2 = l15 - ((wave & 2) ? -2 : 2), nx = (slice >> 1) & 1;
    auto src = [&](int r_) { return (unsigned)((r_ & 16) ? 1024 + lg * 64 + (r_ & 3) * 16 : (lg * 16 + r_) * 16); };
    const unsigned vOutS = src(sr2), vSp = (sr2 & 16) ? OOB : vOutS;
    (void)vWr2;
    const unsigned vAn = kind == 1 ? HU + vL16 : src(sr);
    const unsigned vAe = kind == 1 ? ((l15 >> 2) == (nx ? 0 : 3) ? (nx ? 0 : 2 * HU) + 1024 + lg * 64 + (l15 & 3) * 16 : OOB) : (sr & 16) ? OOB : vAn;
    const unsigned vAux0 = l15 * 16, vAux1 = 139264 + (l15 * 2 + (lg & 1)) * 16 + wave * 4 + (lg >> 1) * 512, vAux2 = 139264 + (l15 * 2 + (lg & 1)) * 16;
    const unsigned vAux3 = (wave == 0 && lg < 2) ? slice * 128 + l15 * 8 + lg * 4 : OOB;
    const unsigned sOutOff = kind == 1 ? OFF_HS - HU + (slice * 4 + wave) * 1280 : OFF_X1 + (slice * 4 + wave) * 1024, sFoutOff = (kind == 1 ? OFF_FH : OFF_FX1) + slice * 128;
    const unsigned sAuxOff = OFF_XP + (slice * 4 + wave) * 1024, sBuf = kind, sWw = 16 * wave * 2048 + slice * 256;
    const u16* w12 = kind == 1 ? w1 : w2;
    const unsigned sExp = __builtin_amdgcn_readfirstlane(ep);
    unsigned epOut;
    asm volatile(
R"(.macro M d,w,x
v_mfma_f32_16x16x32_bf16 v[\d:\d+3], \w, v[\x:\x+3], v[\d:\d+3]
.endm
.macro R d,a,o
ds_read_b128 v[\d:\d+3], v\a offset:\o
.endm
.macro T d,a,o
ds_read_b64_tr_b16 \d, v\a offset:\o
.endm
.macro D a,s,o
buffer_load_dwordx4 v\a, s[36:39], s\s offen offset:\o sc1 lds
.endm
.macro L c
s_waitcnt lgkmcnt(\c)
.endm
Lcode_g12_b:
s_cmp_eq_u32 %9,2
s_cbranch_scc1 Lg2_begin_%=
s_mov_b64 s[36:37],%1
s_mov_b32 s38,111243392
s_mov_b32 s39,0x00020000
s_mov_b32 s44,%2
s_mov_b32 s45,%3
s_mov_b32 s46,%4
s_mov_b32 s47,%5
s_mov_b32 s48,%6
s_mov_b32 s49,%7
s_mov_b32 s50,%8
s_mov_b32 s51,%9
s_mov_b32 s53,%10
v_mov_b32 v0,%11
v_mov_b32 v3,%12
v_mov_b32 v5,%13
v_mov_b32 v6,%14
v_mov_b32 v18,%15
v_mov_b32 v19,%16
v_mov_b32 v20,%17
v_mov_b32 v21,%18
v_mov_b32 v9,%19
v_mov_b32 v10,%20
v_mov_b32 v11,%21
v_mov_b32 v12,%22
v_mov_b32 v13,%23
v_mov_b32 v14,%24
v_mov_b32 v15,%25
v_mov_b32 v16,%26
s_mov_b64 s[40:41],%27
s_mov_b32 s42,2097152
s_mov_b32 s43,0x00020000
s_lshl_b32 s57,s44,11
s_waitcnt vmcnt(20) lgkmcnt(0)
s_barrier
s_add_u32 s55,s50,786432
s_add_u32 m0,s57,98304
s_nop 0
buffer_load_dwordx4 v18,s[40:43],s55 offen offset:0  lds
s_add_u32 m0,s57,99328
s_nop 0
buffer_load_dwordx4 v19,s[40:43],s55 offen offset:0  lds
s_add_u32 m0,s57,106368
s_nop 0
buffer_load_dwordx4 v18,s[40:43],s55 offen offset:128  lds
s_add_u32 m0,s57,107392
s_nop 0
buffer_load_dwordx4 v19,s[40:43],s55 offen offset:128  lds
v_add_u32 v26,0,v20
v_add_u32 v27,0,v21
T a[0:1],26,0
T a[2:3],26,512
T a[4:5],27,0
T a[6:7],27,512
T a[8:9],26,4096
T a[10:11],26,4608
T a[12:13],27,4096
T a[14:15],27,4608
s_waitcnt vmcnt(20) lgkmcnt(0)
s_barrier
s_add_u32 s55,s50,917504
s_add_u32 m0,s57,0
s_nop 0
buffer_load_dwordx4 v18,s[40:43],s55 offen offset:0  lds
s_add_u32 m0,s57,1024
s_nop 0
buffer_load_dwordx4 v19,s[40:43],s55 offen offset:0  lds
s_add_u32 m0,s57,8064
s_nop 0
buffer_load_dwordx4 v18,s[40:43],s55 offen offset:128  lds
s_add_u32 m0,s57,9088
s_nop 0
buffer_load_dwordx4 v19,s[40:43],s55 offen offset:128  lds
v_add_u32 v26,16384,v20
v_add_u32 v27,16384,v21
T a[16:17],26,0
T a[18:19],26,512
T a[20:21],27,0
T a[22:23],27,512
T a[24:25],26,4096
T a[26:27],26,4608
T a[28:29],27,4096
T a[30:31],27,4608
s_waitcnt vmcnt(20) lgkmcnt(0)
s_barrier
s_add_u32 s55,s50,1048576
s_add_u32 m0,s57,16384
s_nop 0
buffer_load_dwordx4 v18,s[40:43],s55 offen offset:0  lds
s_add_u32 m0,s57,17408
s_nop 0
buffer_load_dwordx4 v19,s[40:43],s55 offen offset:0  lds
s_add_u32 m0,s57,24448
s_nop 0
buffer_load_dwordx4 v18,s[40:43],s55 offen offset:128  lds
s_add_u32 m0,s57,25472
s_nop 0
buffer_load_dwordx4 v19,s[40:43],s55 offen offset:128  lds
v_add_u32 v26,32768,v20
v_add_u32 v27,32768,v21
T a[32:33],26,0
T a[34:35],26,512
T a[36:37],27,0
T a[38:39],27,512
T a[40:41],26,4096
T a[42:43],26,4608
T a[44:45],27,4096
T a[46:47],27,4608
s_waitcnt vmcnt(20) lgkmcnt(0)
s_barrier
s_add_u32 s55,s50,1179648
s_add_u32 m0,s57,32768
s_nop 0
buffer_load_dwordx4 v18,s[40:43],s55 offen offset:0  lds
s_add_u32 m0,s57,33792
s_nop 0
buffer_load_dwordx4 v19,s[40:43],s55 offen offset:0  lds
s_add_u32 m0,s57,40832
s_nop 0
buffer_load_dwordx4 v18,s[40:43],s55 offen offset:128  lds
s_add_u32 m0,s57,41856
s_nop 0
buffer_load_dwordx4 v19,s[40:43],s55 offen offset:128  lds
v_add_u32 v26,49152,v20
v_add_u32 v27,49152,v21
T a[48:49],26,0
T a[50:51],26,512
T a[52:53],27,0
T a[54:55],27,512
T a[56:57],26,4096
T a[58:59],26,4608
T a[60:61],27,4096
T a[62:63],27,4608
s_waitcnt vmcnt(20) lgkmcnt(0)
s_barrier
s_add_u32 s55,s50,1310720
s_add_u32 m0,s57,49152
s_nop 0
buffer_load_dwordx4 v18,s[40:43],s55 offen offset:0  lds
s_add_u32 m0,s57,50176
s_nop 0
buffer_load_dwordx4 v19,s[40:43],s55 offen offset:0  lds
s_add_u32 m0,s57,57216
s_nop 0
buffer_load_dwordx4 v18,s[40:43],s55 offen offset:128  lds
s_add_u32 m0,s57,58240
s_nop 0
buffer_load_dwordx4 v19,s[40:43],s55 offen offset:128  lds
v_add_u32 v26,65536,v20
v_add_u32 v27,65536,v21
T a[64:65],26,0
T a[66:67],26,512
T a[68:69],27,0
T a[70:71],27,512
T a[72:73],26,4096
T a[74:75],26,4608
T a[76:77],27,4096
T a[78:79],27,4608
s_waitcnt vmcnt(20) lgkmcnt(0)
s_barrier
s_add_u32 s55,s50,1441792
s_add_u32 m0,s57,65536
s_nop 0
buffer_load_dwordx4 v18,s[40:43],s55 offen offset:0  lds
s_add_u32 m0,s57,66560
s_nop 0
buffer_load_dwordx4 v19,s[40:43],s55 offen offset:0  lds
s_add_u32 m0,s57,73600
s_nop 0
buffer_load_dwordx4 v18,s[40:43],s55 offen offset:128  lds
s_add_u32 m0,s57,74624
s_nop 0
buffer_load_dwordx4 v19,s[40:43],s55 offen offset:128  lds
v_add_u32 v26,81920,v20
v_add_u32 v27,81920,v21
T a[80:81],26,0
T a[82:83],26,512
T a[84:85],27,0
T a[86:87],27,512
T a[88:89],26,4096
T a[90:91],26,4608
T a[92:93],27,4096
T a[94:95],27,4608
s_waitcnt vmcnt(20) lgkmcnt(0)
s_barrier
s_add_u32 s55,s50,1572864
s_add_u32 m0,s57,81920
s_nop 0
buffer_load_dwordx4 v18,s[40:43],s55 offen offset:0  lds
s_add_u32 m0,s57,82944
s_nop 0
buffer_load_dwordx4 v19,s[40:43],s55 offen offset:0  lds
s_add_u32 m0,s57,89984
s_nop 0
buffer_load_dwordx4 v18,s[40:43],s55 offen offset:128  lds
s_add_u32 m0,s57,91008
s_nop 0
buffer_load_dwordx4 v19,s[40:43],s55 offen offset:128  lds
v_add_u32 v26,98304,v20
v_add_u32 v27,98304,v21
T a[96:97],26,0
T a[98:99],26,512
T a[100:101],27,0
T a[102:103],27,512
T a[104:105],26,4096
T a[106:107],26,4608
T a[108:109],27,4096
T a[110:111],27,4608
s_waitcnt vmcnt(20) lgkmcnt(0)
s_barrier
s_add_u32 s55,s50,1703936
s_add_u32 m0,s57,98304
s_nop 0
buffer_load_dwordx4 v18,s[40:43],s55 offen offset:0  lds
s_add_u32 m0,s57,99328
s_nop 0
buffer_load_dwordx4 v19,s[40:43],s55 offen offset:0  lds
s_add_u32 m0,s57,106368
s_nop 0
buffer_load_dwordx4 v18,s[40:43],s55 offen offset:128  lds
s_add_u32 m0,s57,107392
s_nop 0
buffer_load_dwordx4 v19,s[40:43],s55 offen offset:128  lds
v_add_u32 v26,0,v20
v_add_u32 v27,0,v21
T a[112:113],26,0
T a[114:115],26,512
T a[116:117],27,0
T a[118:119],27,512
T a[120:121],26,4096
T a[122:123],26,4608
T a[124:125],27,4096
T a[126:127],27,4608
s_waitcnt vmcnt(20) lgkmcnt(0)
s_barrier
s_add_u32 s55,s50,1835008
s_add_u32 m0,s57,0
s_nop 0
buffer_load_dwordx4 v18,s[40:43],s55 offen offset:0  lds
s_add_u32 m0,s57,1024
s_nop 0
buffer_load_dwordx4 v19,s[40:43],s55 offen offset:0  lds
s_add_u32 m0,s57,8064
s_nop 0
buffer_load_dwordx4 v18,s[40:43],s55 offen offset:128  lds
s_add_u32 m0,s57,9088
s_nop 0
buffer_load_dwordx4 v19,s[40:43],s55 offen offset:128  lds
v_add_u32 v26,16384,v20
v_add_u32 v27,16384,v21
T a[128:129],26,0
T a[130:131],26,512
T a[132:133],27,0
T a[134:135],27,512
T a[136:137],26,4096
T a[138:139],26,4608
T a[140:141],27,4096
T a[142:143],27,4608
s_waitcnt vmcnt(20) lgkmcnt(0)
s_barrier
s_add_u32 s55,s50,1966080
s_add_u32 m0,s57,16384
s_nop 0
buffer_load_dwordx4 v18,s[40:43],s55 offen offset:0  lds
s_add_u32 m0,s57,17408
s_nop 0
buffer_load_dwordx4 v19,s[40:43],s55 offen offset:0  lds
s_add_u32 m0,s57,24448
s_nop 0
buffer_load_dwordx4 v18,s[40:43],s55 offen offset:128  lds
s_add_u32 m0,s57,25472
s_nop 0
buffer_load_dwordx4 v19,s[40:43],s55 offen offset:128  lds
v_add_u32 v26,32768,v20
v_add_u32 v27,32768,v21
T a[144:145],26,0
T a[146:147],26,512
T a[148:149],27,0
T a[150:151],27,512
T a[152:153],26,4096
T a[154:155],26,4608
T a[156:157],27,4096
T a[158:159],27,4608
s_waitcnt vmcnt(20) lgkmcnt(0)
s_barrier
v_add_u32 v26,49152,v20
v_add_u32 v27,49152,v21
T a[160:161],26,0
T a[162:163],26,512
T a[164:165],27,0
T a[166:167],27,512
T a[168:169],26,4096
T a[170:171],26,4608
T a[172:173],27,4096
T a[174:175],27,4608
s_waitcnt vmcnt(16) lgkmcnt(0)
s_barrier
v_add_u32 v26,65536,v20
v_add_u32 v27,65536,v21
T a[176:177],26,0
T a[178:179],26,512
T a[180:181],27,0
T a[182:183],27,512
T a[184:185],26,4096
T a[186:187],26,4608
T a[188:189],27,4096
T a[190:191],27,4608
s_waitcnt vmcnt(12) lgkmcnt(0)
s_barrier
v_add_u32 v26,81920,v20
v_add_u32 v27,81920,v21
T a[192:193],26,0
T a[194:195],26,512
T a[196:197],27,0
T a[198:199],27,512
T a[200:201],26,4096
T a[202:203],26,4608
T a[204:205],27,4096
T a[206:207],27,4608
s_waitcnt vmcnt(8) lgkmcnt(0)
s_barrier
v_add_u32 v26,98304,v20
v_add_u32 v27,98304,v21
T a[208:209],26,0
T a[210:211],26,512
T a[212:213],27,0
T a[214:215],27,512
T a[216:217],26,4096
T a[218:219],26,4608
T a[220:221],27,4096
T a[222:223],27,4608
s_waitcnt vmcnt(4) lgkmcnt(0)
s_barrier
v_add_u32 v26,0,v20
v_add_u32 v27,0,v21
T a[224:225],26,0
T a[226:227],26,512
T a[228:229],27,0
T a[230:231],27,512
T a[232:233],26,4096
T a[234:235],26,4608
T a[236:237],27,4096
T a[238:239],27,4608
s_waitcnt vmcnt(0) lgkmcnt(0)
s_barrier
v_add_u32 v26,16384,v20
v_add_u32 v27,16384,v21
T a[240:241],26,0
T a[242:243],26,512
T a[244:245],27,0
T a[246:247],27,512
T a[248:249],26,4096
T a[250:251],26,4608
T a[252:253],27,4096
T a[254:255],27,4608
L 0
s_barrier
v_mov_b32 v34,0x3f803f80
v_mov_b32 v35,0x3f803f80
v_mov_b32 v36,0x3f803f80
v_mov_b32 v37,0x3f803f80
s_nop 1
v_mfma_f32_16x16x32_bf16 v[106:109],a[0:3],v[34:37],0
v_mfma_f32_16x16x32_bf16 v[110:113],a[4:7],v[34:37],0
M 106,a[8:11],34
M 110,a[12:15],34
M 106,a[16:19],34
M 110,a[20:23],34
M 106,a[24:27],34
M 110,a[28:31],34
M 106,a[32:35],34
M 110,a[36:39],34
M 106,a[40:43],34
M 110,a[44:47],34
M 106,a[48:51],34
M 110,a[52:55],34
M 106,a[56:59],34
M 110,a[60:63],34
M 106,a[64:67],34
M 110,a[68:71],34
M 106,a[72:75],34
M 110,a[76:79],34
M 106,a[80:83],34
M 110,a[84:87],34
M 106,a[88:91],34
M 110,a[92:95],34
M 106,a[96:99],34
M 110,a[100:103],34
M 106,a[104:107],34
M 110,a[108:111],34
M 106,a[112:115],34
M 110,a[116:119],34
M 106,a[120:123],34
M 110,a[124:127],34
M 106,a[128:131],34
M 110,a[132:135],34
M 106,a[136:139],34
M 110,a[140:143],34
M 106,a[144:147],34
M 110,a[148:151],34
M 106,a[152:155],34
M 110,a[156:159],34
M 106,a[160:163],34
M 110,a[164:167],34
M 106,a[168:171],34
M 110,a[172:175],34
M 106,a[176:179],34
M 110,a[180:183],34
M 106,a[184:187],34
M 110,a[188:191],34
M 106,a[192:195],34
M 110,a[196:199],34
M 106,a[200:203],34
M 110,a[204:207],34
M 106,a[208:211],34
M 110,a[212:215],34
M 106,a[216:219],34
M 110,a[220:223],34
M 106,a[224:227],34
M 110,a[228:231],34
M 106,a[232:235],34
M 110,a[236:239],34
M 106,a[240:243],34
M 110,a[244:247],34
M 106,a[248:251],34
M 110,a[252:255],34
v_mov_b32 v4,s53
s_mov_b32 s82,0
s_mov_b32 s80,0
s_mov_b32 s54,s45
s_mul_i32 s57,s44,4096
s_mul_i32 s55,s45,32768
s_add_u32 s55,s55,s57
s_add_u32 s55,s55,8388608
s_mul_i32 s57,s44,4096
s_mul_i32 s60,s46,32768
s_mul_i32 s61,s46,1024
s_mul_i32 s62,s46,40960
s_mul_i32 s63,s46,1024
s_mul_i32 s64,s46,512
s_lshl_b32 s59,s44,8
s_add_u32 s59,s59,131072
s_mul_i32 s58,s45,1024
s_add_u32 s58,s58,109056000
s_mul_i32 s65,s45,40960
s_add_u32 s65,s65,s47
s_mul_i32 s68,s45,1024
s_add_u32 s68,s68,s48
s_mul_i32 s73,s45,512
s_add_u32 s73,s73,106954752
s_mul_i32 s74,s45,32768
s_add_u32 s74,s74,s49
s_mul_i32 s75,s45,1024
s_add_u32 s75,s75,107479040
s_mov_b32 s66,0x7fff0000
s_mov_b32 s67,0x7fff0000
s_mov_b32 s69,0x7fff0000
s_mov_b32 s76,0x7fff0000
s_mov_b32 s77,0x7fff0000
s_mov_b32 s70,0x7fff0000
s_mov_b32 s71,0x7fff0000
s_mov_b32 s72,0x7fff0000
v_mov_b32 v1,v0
v_add_u32 v2,16384,v0
s_mov_b32 s84,16384
s_mov_b32 s86,0x3a800000
s_mov_b32 s87,0x3727c5ac
s_mov_b32 s88,0xc0135761
s_mov_b32 s89,0x3d372713
s_lshr_b32 s85,s44,1
s_mul_i32 s85,s85,127
v_lshlrev_b32 v8,22,v0
v_and_b32 v8,0x80000000,v8
v_or_b32 v8,v8,v5
v_add_u32 v2,32768,v0
s_lshl_b32 s84,s57,1
s_add_u32 s84,s84,32768
s_getpc_b64 s[40:41]
Lg1_pc_%=:
s_lshl_b32 s78,s44,13
v_lshlrev_b32 v114,3,v0
v_add_u32 v114,s78,v114
v_min_u32 v114,Lcode_g2_b-Lg1_pc_%=-4,v114
)"
R"(s_mov_b32 m0,163584
s_nop 0
global_load_lds_dword v114,s[40:41]
s_add_u32 m0,s59,4096
s_nop 0
buffer_load_dword v8,s[36:39],s58 offen offset:0 sc1 lds
buffer_store_dword v4,v3,s[36:39],s71 offen offset:0 sc0 sc1
s_add_u32 m0,s59,5120
s_nop 0
buffer_load_dword v8,s[36:39],s58 offen offset:0 sc1 lds
buffer_store_dword v4,v3,s[36:39],s71 offen offset:0 sc0 sc1
s_mov_b32 s83,0
Lg1_poll_%=:
s_add_u32 s78,s83,2
s_and_b32 s78,s78,3
s_lshl_b32 s78,s78,10
s_add_u32 s78,s78,4096
s_add_u32 m0,s59,s78
s_nop 0
buffer_load_dword v8,s[36:39],s58 offen offset:0 sc1 lds
s_and_b32 s78,s83,3
s_lshl_b32 s78,s78,10
v_add_u32 v114,s78,v6
s_waitcnt vmcnt(4)
s_barrier
ds_read_b32 v7,v114 offset:4096
buffer_store_dword v4,v3,s[36:39],s70 offen offset:0 sc0 sc1
s_mov_b32 s70,s71
s_mov_b32 s71,s72
s_mov_b32 s72,0x7fff0000
s_add_u32 s83,s83,1
s_add_u32 s82,s82,1
L 0
v_cmp_eq_u32 vcc,s53,v7
s_nop 0
s_cmp_eq_u32 vcc_lo,-1
s_cbranch_scc1 Lg1_go_%=
s_cmp_lt_u32 s82,20000
s_cbranch_scc1 Lg1_poll_%=
Lg1_go_%=:
s_add_u32 m0,s57,0
s_add_u32 s56,s55,0
D 0,56,0
D 0,56,1024
D 0,56,2048
D 0,56,3072
s_add_u32 s58,s58,s61
s_add_u32 m0,s59,2048
s_nop 0
buffer_load_dword v8,s[36:39],s58 offen offset:0 sc1 lds
s_add_u32 m0,s57,16384
s_add_u32 s56,s55,16384
D 0,56,0
D 0,56,1024
D 0,56,2048
D 0,56,3072
s_add_u32 s55,s55,s60
s_sub_u32 s57,s84,s57
s_add_u32 s54,s54,s46
s_waitcnt vmcnt(4)
s_barrier
buffer_store_dword v4,v3,s[36:39],s70 offen offset:0 sc0 sc1
s_min_u32 s71,s71,s72
buffer_store_dword v4,v3,s[36:39],s71 offen offset:0 sc0 sc1
s_mov_b32 s70,0x7fff0000
s_mov_b32 s71,0x7fff0000
s_mov_b32 s72,0x7fff0000
s_cmp_eq_u32 s80,1
buffer_store_dword v4,v3,s[36:39],s71 offen offset:0 sc0 sc1
s_cbranch_scc1 Lg1_st1_%=
s_branch Lg1_st0_%=
Lg1_gox_%=:
s_add_u32 m0,s59,2048
s_nop 0
buffer_load_dword v8,s[36:39],s58 offen offset:0 sc1 lds
s_waitcnt vmcnt(0)
s_barrier
buffer_store_dword v4,v3,s[36:39],s70 offen offset:0 sc0 sc1
s_min_u32 s71,s71,s72
buffer_store_dword v4,v3,s[36:39],s71 offen offset:0 sc0 sc1
s_mov_b32 s70,0x7fff0000
s_mov_b32 s71,0x7fff0000
s_mov_b32 s72,0x7fff0000
s_cmp_eq_u32 s80,1
s_cbranch_scc1 Lg1_st1_%=
Lg1_st0_%=:
ds_read_b32 v7,v6 offset:2048
R 34,1,0
R 38,1,1024
R 42,1,2048
R 46,1,3072
R 50,1,4096
L 5
v_cmp_eq_u32 vcc,s53,v7
L 3
s_cmp_eq_u32 vcc_lo,-1
s_cbranch_scc1 Lg1_F0_%=
s_branch Lg1_L0_%=
Lg1_st1_%=:
ds_read_b32 v7,v6 offset:2048
R 34,2,0
R 38,2,1024
R 42,2,2048
R 46,2,3072
R 50,2,4096
L 5
v_cmp_eq_u32 vcc,s53,v7
L 3
s_cmp_eq_u32 vcc_lo,-1
s_cbranch_scc1 Lg1_F1_%=
s_branch Lg1_L1_%=
Lg1_F0_%=:
v_mfma_f32_16x16x32_bf16 v[22:25],a[4:7],v[34:37],0
s_add_u32 s58,s58,s61
s_add_u32 m0,s59,0
R 54,1,5120
M 22,a[12:15],38
buffer_load_dword v8,s[36:39],s58 offen offset:0 sc1 lds
v_mfma_f32_16x16x32_bf16 v[18:21],a[0:3],v[34:37],0
s_add_u32 m0,s57,0
R 58,1,6144
v_fma_f32 v26,-v102,v106,v26
M 18,a[8:11],38
L 3
s_add_u32 s56,s55,0
v_fma_f32 v27,-v102,v107,v27
M 18,a[16:19],42
s_mov_b32 s66,s65
R 62,1,7168
v_fma_f32 v28,-v102,v108,v28
M 18,a[24:27],46
D 0,56,0
M 22,a[20:23],42
s_add_u32 s65,s65,s62
R 66,1,8192
v_fma_f32 v29,-v102,v109,v29
v_mul_f32 v114,v26,v26
M 22,a[28:31],46
L 3
D 0,56,1024
M 22,a[36:39],50
s_add_u32 s54,s54,s46
R 70,1,9216
v_mul_f32 v115,v27,v27
M 22,a[44:47],54
v_mul_f32 v116,v28,v28
v_mul_f32 v117,v29,v29
M 18,a[32:35],50
R 74,1,10240
v_fma_f32 v114,v104,v114,v103
M 18,a[40:43],54
L 3
D 0,56,2048
M 18,a[48:51],58
R 78,1,11264
v_fma_f32 v115,v104,v115,v103
M 18,a[56:59],62
D 0,56,3072
v_fma_f32 v116,v104,v116,v103
M 22,a[52:55],58
s_add_u32 m0,s57,16384
R 82,1,12288
v_fma_f32 v117,v104,v117,v103
M 22,a[60:63],62
L 3
v_mul_f32 v114,v26,v114
v_mul_f32 v115,v27,v115
M 22,a[68:71],66
s_add_u32 s56,s55,16384
R 86,1,13312
v_mul_f32 v116,v28,v116
M 22,a[76:79],70
buffer_load_dwordx4 v[98:101],v11,s[36:39],s73 offen offset:0 sc1
M 18,a[64:67],66
s_add_u32 s73,s73,s64
R 90,1,14336
v_mul_f32 v117,v29,v117
M 18,a[72:75],70
L 3
v_exp_f32 v114,v114
v_fma_f32 v30,-v102,v110,v30
M 18,a[80:83],74
s_waitcnt vmcnt(9)
R 94,1,15360
v_fma_f32 v114,v114,v105,v105
M 18,a[88:91],78
s_barrier
v_exp_f32 v115,v115
M 22,a[84:87],74
s_add_u32 s55,s55,s60
R 34,1,16384
v_fma_f32 v115,v115,v105,v105
M 22,a[92:95],78
L 3
v_exp_f32 v116,v116
M 22,a[100:103],82
s_sub_u32 s57,s84,s57
R 38,1,17408
v_fma_f32 v116,v116,v105,v105
M 22,a[108:111],86
D 0,56,0
M 18,a[96:99],82
R 42,1,18432
v_exp_f32 v117,v117
M 18,a[104:107],86
L 3
v_rcp_f32 v114,v114
M 18,a[112:115],90
R 46,1,19456
v_fma_f32 v117,v117,v105,v105
M 18,a[120:123],94
v_rcp_f32 v115,v115
M 22,a[116:119],90
R 50,1,20480
v_mul_f32 v26,v26,v114
M 22,a[124:127],94
L 3
v_rcp_f32 v116,v116
M 22,a[132:135],34
R 54,1,21504
v_mul_f32 v27,v27,v115
M 22,a[140:143],38
D 0,56,1024
v_mul_f32 v28,v28,v116
M 18,a[128:131],34
R 58,1,22528
v_cvt_pk_bf16_f32 v122,v26,v27
M 18,a[136:139],38
L 3
v_rcp_f32 v117,v117
M 18,a[144:147],42
R 62,1,23552
v_mul_f32 v29,v29,v117
M 18,a[152:155],46
v_cvt_pk_bf16_f32 v123,v28,v29
v_fma_f32 v31,-v102,v111,v31
M 22,a[148:151],42
R 66,1,24576
v_fma_f32 v32,-v102,v112,v32
M 22,a[156:159],46
L 3
D 0,56,2048
v_fma_f32 v33,-v102,v113,v33
M 22,a[164:167],50
R 70,1,25600
v_mul_f32 v118,v30,v30
M 22,a[172:175],54
v_mul_f32 v119,v31,v31
v_mul_f32 v120,v32,v32
M 18,a[160:163],50
R 74,1,26624
v_mul_f32 v121,v33,v33
M 18,a[168:171],54
L 3
v_fma_f32 v118,v104,v118,v103
v_fma_f32 v119,v104,v119,v103
M 18,a[176:179],58
R 78,1,27648
v_fma_f32 v120,v104,v120,v103
M 18,a[184:187],62
v_fma_f32 v121,v104,v121,v103
v_mul_f32 v118,v30,v118
M 22,a[180:183],58
R 82,1,28672
v_exp_f32 v118,v118
M 22,a[188:191],62
L 3
D 0,56,3072
M 22,a[196:199],66
R 86,1,29696
v_mul_f32 v119,v31,v119
M 22,a[204:207],70
v_exp_f32 v119,v119
M 18,a[192:195],66
R 90,1,30720
v_mul_f32 v120,v32,v120
M 18,a[200:203],70
L 3
v_mul_f32 v121,v33,v121
v_fma_f32 v118,v118,v105,v105
M 18,a[208:211],74
s_waitcnt vmcnt(5)
R 94,1,31744
v_fma_f32 v119,v119,v105,v105
M 18,a[216:219],78
s_barrier
ds_read_b32 v7,v6 offset:0
v_exp_f32 v120,v120
M 22,a[212:215],74
R 34,2,0
v_fma_f32 v120,v120,v105,v105
M 22,a[220:223],78
L 4
buffer_store_dword v4,v3,s[36:39],s70 offen offset:0 sc0 sc1
v_exp_f32 v121,v121
M 22,a[228:231],82
s_mov_b32 s70,s69
R 38,2,1024
v_fma_f32 v121,v121,v105,v105
v_rcp_f32 v118,v118
v_rcp_f32 v119,v119
v_rcp_f32 v120,v120
v_rcp_f32 v121,v121
M 22,a[236:239],86
s_mov_b32 s69,s68
v_mul_f32 v30,v30,v118
v_mul_f32 v31,v31,v119
v_mul_f32 v32,v32,v120
v_mul_f32 v33,v33,v121
v_cvt_pk_bf16_f32 v124,v30,v31
v_cvt_pk_bf16_f32 v125,v32,v33
M 18,a[224:227],82
s_add_u32 s68,s68,s63
R 42,2,2048
buffer_store_dwordx4 v[122:125],v15,s[36:39],s67 offen offset:0 sc0 sc1
M 18,a[232:235],86
L 4
L 3
M 18,a[240:243],90
R 46,2,3072
v_cmp_eq_u32 vcc,s53,v7
M 18,a[248:251],94
buffer_store_dwordx4 v[122:125],v16,s[36:39],s67 offen offset:0 sc0 sc1
M 22,a[244:247],90
s_waitcnt vmcnt(7) lgkmcnt(2)
s_cmp_eq_u32 vcc_lo,-1
R 50,2,4096
M 22,a[252:255],94
s_cbranch_scc0 Lg1_K1_%=
Lg1_F1_%=:
v_mfma_f32_16x16x32_bf16 v[30:33],a[4:7],v[34:37],0
s_add_u32 s58,s58,s61
s_add_u32 m0,s59,1024
R 54,2,5120
M 30,a[12:15],38
buffer_load_dword v8,s[36:39],s58 offen offset:0 sc1 lds
v_mfma_f32_16x16x32_bf16 v[26:29],a[0:3],v[34:37],0
s_add_u32 m0,s57,0
R 58,2,6144
v_fma_f32 v18,-v98,v106,v18
M 26,a[8:11],38
L 3
s_add_u32 s56,s55,0
v_fma_f32 v19,-v98,v107,v19
M 26,a[16:19],42
s_mov_b32 s67,s65
R 62,2,7168
v_fma_f32 v20,-v98,v108,v20
M 26,a[24:27],46
D 0,56,0
M 30,a[20:23],42
s_add_u32 s65,s65,s62
R 66,2,8192
v_fma_f32 v21,-v98,v109,v21
v_mul_f32 v114,v18,v18
M 30,a[28:31],46
L 3
D 0,56,1024
M 30,a[36:39],50
s_add_u32 s54,s54,s46
R 70,2,9216
v_mul_f32 v115,v19,v19
M 30,a[44:47],54
v_mul_f32 v116,v20,v20
v_mul_f32 v117,v21,v21
M 26,a[32:35],50
R 74,2,10240
v_fma_f32 v114,v100,v114,v99
M 26,a[40:43],54
L 3
D 0,56,2048
M 26,a[48:51],58
R 78,2,11264
v_fma_f32 v115,v100,v115,v99
M 26,a[56:59],62
D 0,56,3072
v_fma_f32 v116,v100,v116,v99
M 30,a[52:55],58
s_add_u32 m0,s57,16384
R 82,2,12288
v_fma_f32 v117,v100,v117,v99
M 30,a[60:63],62
L 3
v_mul_f32 v114,v18,v114
v_mul_f32 v115,v19,v115
M 30,a[68:71],66
s_add_u32 s56,s55,16384
R 86,2,13312
v_mul_f32 v116,v20,v116
M 30,a[76:79],70
buffer_load_dwordx4 v[102:105],v11,s[36:39],s73 offen offset:0 sc1
M 26,a[64:67],66
s_add_u32 s73,s73,s64
R 90,2,14336
v_mul_f32 v117,v21,v117
M 26,a[72:75],70
L 3
v_exp_f32 v114,v114
v_fma_f32 v22,-v98,v110,v22
M 26,a[80:83],74
s_waitcnt vmcnt(9)
R 94,2,15360
v_fma_f32 v114,v114,v101,v101
M 26,a[88:91],78
s_barrier
v_exp_f32 v115,v115
M 30,a[84:87],74
s_add_u32 s55,s55,s60
R 34,2,16384
v_fma_f32 v115,v115,v101,v101
M 30,a[92:95],78
L 3
v_exp_f32 v116,v116
M 30,a[100:103],82
s_sub_u32 s57,s84,s57
R 38,2,17408
v_fma_f32 v116,v116,v101,v101
M 30,a[108:111],86
D 0,56,0
M 26,a[96:99],82
R 42,2,18432
v_exp_f32 v117,v117
M 26,a[104:107],86
L 3
v_rcp_f32 v114,v114
M 26,a[112:115],90
R 46,2,19456
v_fma_f32 v117,v117,v101,v101
M 26,a[120:123],94
v_rcp_f32 v115,v115
M 30,a[116:119],90
R 50,2,20480
v_mul_f32 v18,v18,v114
M 30,a[124:127],94
L 3
v_rcp_f32 v116,v116
M 30,a[132:135],34
R 54,2,21504
v_mul_f32 v19,v19,v115
M 30,a[140:143],38
D 0,56,1024
v_mul_f32 v20,v20,v116
M 26,a[128:131],34
R 58,2,22528
v_cvt_pk_bf16_f32 v122,v18,v19
M 26,a[136:139],38
L 3
v_rcp_f32 v117,v117
M 26,a[144:147],42
R 62,2,23552
v_mul_f32 v21,v21,v117
M 26,a[152:155],46
v_cvt_pk_bf16_f32 v123,v20,v21
v_fma_f32 v23,-v98,v111,v23
M 30,a[148:151],42
R 66,2,24576
v_fma_f32 v24,-v98,v112,v24
M 30,a[156:159],46
L 3
D 0,56,2048
v_fma_f32 v25,-v98,v113,v25
M 30,a[164:167],50
R 70,2,25600
v_mul_f32 v118,v22,v22
M 30,a[172:175],54
v_mul_f32 v119,v23,v23
v_mul_f32 v120,v24,v24
M 26,a[160:163],50
R 74,2,26624
v_mul_f32 v121,v25,v25
M 26,a[168:171],54
L 3
v_fma_f32 v118,v100,v118,v99
v_fma_f32 v119,v100,v119,v99
M 26,a[176:179],58
R 78,2,27648
v_fma_f32 v120,v100,v120,v99
M 26,a[184:187],62
v_fma_f32 v121,v100,v121,v99
v_mul_f32 v118,v22,v118
M 30,a[180:183],58
R 82,2,28672
v_exp_f32 v118,v118
M 30,a[188:191],62
L 3
D 0,56,3072
M 30,a[196:199],66
R 86,2,29696
v_mul_f32 v119,v23,v119
M 30,a[204:207],70
v_exp_f32 v119,v119
M 26,a[192:195],66
R 90,2,30720
v_mul_f32 v120,v24,v120
M 26,a[200:203],70
L 3
v_mul_f32 v121,v25,v121
v_fma_f32 v118,v118,v101,v101
M 26,a[208:211],74
s_waitcnt vmcnt(5)
)"
R"(R 94,2,31744
v_fma_f32 v119,v119,v101,v101
M 26,a[216:219],78
s_barrier
ds_read_b32 v7,v6 offset:1024
v_exp_f32 v120,v120
M 30,a[212:215],74
R 34,1,0
v_fma_f32 v120,v120,v101,v101
M 30,a[220:223],78
L 4
buffer_store_dword v4,v3,s[36:39],s70 offen offset:0 sc0 sc1
v_exp_f32 v121,v121
M 30,a[228:231],82
s_mov_b32 s70,s69
R 38,1,1024
v_fma_f32 v121,v121,v101,v101
v_rcp_f32 v118,v118
v_rcp_f32 v119,v119
v_rcp_f32 v120,v120
v_rcp_f32 v121,v121
M 30,a[236:239],86
s_mov_b32 s69,s68
v_mul_f32 v22,v22,v118
v_mul_f32 v23,v23,v119
v_mul_f32 v24,v24,v120
v_mul_f32 v25,v25,v121
v_cvt_pk_bf16_f32 v124,v22,v23
v_cvt_pk_bf16_f32 v125,v24,v25
M 26,a[224:227],82
s_add_u32 s68,s68,s63
R 42,1,2048
buffer_store_dwordx4 v[122:125],v15,s[36:39],s66 offen offset:0 sc0 sc1
M 26,a[232:235],86
L 4
L 3
M 26,a[240:243],90
R 46,1,3072
v_cmp_eq_u32 vcc,s53,v7
M 26,a[248:251],94
buffer_store_dwordx4 v[122:125],v16,s[36:39],s66 offen offset:0 sc0 sc1
M 30,a[244:247],90
s_waitcnt vmcnt(7) lgkmcnt(2)
s_cmp_eq_u32 vcc_lo,-1
R 50,1,4096
M 30,a[252:255],94
s_cbranch_scc1 Lg1_F0_%=
Lg1_K0_%=:
Lg1_L0_%=:
v_mfma_f32_16x16x32_bf16 v[22:25],a[4:7],v[34:37],0
s_add_u32 m0,s59,3072
s_mov_b32 s66,s65
R 54,1,5120
M 22,a[12:15],38
buffer_load_dword v8,s[36:39],s58 offen offset:0 sc1 lds
v_mfma_f32_16x16x32_bf16 v[18:21],a[0:3],v[34:37],0
s_add_u32 s65,s65,s62
R 58,1,6144
v_fma_f32 v26,-v102,v106,v26
M 18,a[8:11],38
L 3
v_fma_f32 v27,-v102,v107,v27
v_fma_f32 v28,-v102,v108,v28
M 18,a[16:19],42
R 62,1,7168
v_fma_f32 v29,-v102,v109,v29
M 18,a[24:27],46
v_mul_f32 v114,v26,v26
v_mul_f32 v115,v27,v27
M 22,a[20:23],42
R 66,1,8192
v_mul_f32 v116,v28,v28
M 22,a[28:31],46
L 3
v_mul_f32 v117,v29,v29
v_fma_f32 v114,v104,v114,v103
M 22,a[36:39],50
R 70,1,9216
v_fma_f32 v115,v104,v115,v103
M 22,a[44:47],54
v_fma_f32 v116,v104,v116,v103
v_fma_f32 v117,v104,v117,v103
M 18,a[32:35],50
R 74,1,10240
v_mul_f32 v114,v26,v114
M 18,a[40:43],54
L 3
v_exp_f32 v114,v114
M 18,a[48:51],58
R 78,1,11264
v_mul_f32 v115,v27,v115
M 18,a[56:59],62
v_exp_f32 v115,v115
M 22,a[52:55],58
R 82,1,12288
v_mul_f32 v116,v28,v116
M 22,a[60:63],62
L 3
v_exp_f32 v116,v116
M 22,a[68:71],66
R 86,1,13312
v_mul_f32 v117,v29,v117
M 22,a[76:79],70
buffer_load_dwordx4 v[98:101],v11,s[36:39],s73 offen offset:0 sc1
M 18,a[64:67],66
s_add_u32 s73,s73,s64
R 90,1,14336
v_fma_f32 v114,v114,v105,v105
M 18,a[72:75],70
L 3
v_exp_f32 v117,v117
M 18,a[80:83],74
s_waitcnt vmcnt(1)
R 94,1,15360
v_fma_f32 v115,v115,v105,v105
M 18,a[88:91],78
s_barrier
ds_read_b32 v7,v6 offset:3072
v_fma_f32 v116,v116,v105,v105
M 22,a[84:87],74
R 34,1,16384
v_fma_f32 v117,v117,v105,v105
M 22,a[92:95],78
L 4
buffer_store_dword v4,v3,s[36:39],s70 offen offset:0 sc0 sc1
M 22,a[100:103],82
s_mov_b32 s70,0x7fff0000
R 38,1,17408
v_fma_f32 v30,-v102,v110,v30
M 22,a[108:111],86
v_rcp_f32 v114,v114
M 18,a[96:99],82
R 42,1,18432
v_mul_f32 v26,v26,v114
M 18,a[104:107],86
L 4
L 3
v_cmp_eq_u32 vcc,s53,v7
M 18,a[112:115],90
R 46,1,19456
v_fma_f32 v31,-v102,v111,v31
M 18,a[120:123],94
v_rcp_f32 v115,v115
M 22,a[116:119],90
s_cmp_eq_u32 vcc_lo,-1
R 50,1,20480
v_mul_f32 v27,v27,v115
s_cbranch_scc0 Lg1_X0_%=
M 22,a[124:127],94
L 3
s_add_u32 m0,s57,0
s_add_u32 s56,s55,0
M 22,a[132:135],34
s_add_u32 s58,s58,s61
R 54,1,21504
v_cvt_pk_bf16_f32 v122,v26,v27
M 22,a[140:143],38
D 0,56,0
M 18,a[128:131],34
s_add_u32 s54,s54,s46
R 58,1,22528
v_fma_f32 v32,-v102,v112,v32
M 18,a[136:139],38
L 3
v_rcp_f32 v116,v116
M 18,a[144:147],42
R 62,1,23552
v_mul_f32 v28,v28,v116
M 18,a[152:155],46
D 0,56,1024
M 22,a[148:151],42
R 66,1,24576
v_fma_f32 v33,-v102,v113,v33
M 22,a[156:159],46
L 3
v_rcp_f32 v117,v117
M 22,a[164:167],50
R 70,1,25600
v_mul_f32 v29,v29,v117
M 22,a[172:175],54
D 0,56,2048
M 18,a[160:163],50
R 74,1,26624
v_cvt_pk_bf16_f32 v123,v28,v29
M 18,a[168:171],54
L 3
v_mul_f32 v118,v30,v30
v_mul_f32 v119,v31,v31
M 18,a[176:179],58
R 78,1,27648
v_mul_f32 v120,v32,v32
M 18,a[184:187],62
D 0,56,3072
M 22,a[180:183],58
s_add_u32 m0,s59,0
R 82,1,28672
v_mul_f32 v121,v33,v33
M 22,a[188:191],62
L 3
buffer_load_dword v8,s[36:39],s58 offen offset:0 sc1 lds
M 22,a[196:199],66
s_add_u32 m0,s57,16384
R 86,1,29696
v_fma_f32 v118,v104,v118,v103
M 22,a[204:207],70
s_add_u32 s56,s55,16384
v_fma_f32 v119,v104,v119,v103
v_fma_f32 v120,v104,v120,v103
M 18,a[192:195],66
s_add_u32 s55,s55,s60
R 90,1,30720
v_fma_f32 v121,v104,v121,v103
M 18,a[200:203],70
L 3
D 0,56,0
M 18,a[208:211],74
s_waitcnt vmcnt(1)
R 94,1,31744
v_mul_f32 v118,v30,v118
M 18,a[216:219],78
s_barrier
ds_read_b32 v7,v6 offset:0
v_mul_f32 v119,v31,v119
M 22,a[212:215],74
s_sub_u32 s57,s84,s57
R 34,2,0
v_mul_f32 v120,v32,v120
M 22,a[220:223],78
L 4
buffer_store_dword v4,v3,s[36:39],s70 offen offset:0 sc0 sc1
v_mul_f32 v121,v33,v121
v_exp_f32 v118,v118
v_exp_f32 v119,v119
v_exp_f32 v120,v120
v_exp_f32 v121,v121
M 22,a[228:231],82
s_mov_b32 s70,s69
R 38,2,1024
v_fma_f32 v118,v118,v105,v105
v_fma_f32 v119,v119,v105,v105
v_fma_f32 v120,v120,v105,v105
v_fma_f32 v121,v121,v105,v105
v_rcp_f32 v118,v118
v_rcp_f32 v119,v119
v_rcp_f32 v120,v120
v_rcp_f32 v121,v121
M 22,a[236:239],86
s_mov_b32 s69,s68
v_mul_f32 v30,v30,v118
v_mul_f32 v31,v31,v119
v_mul_f32 v32,v32,v120
v_mul_f32 v33,v33,v121
v_cvt_pk_bf16_f32 v124,v30,v31
v_cvt_pk_bf16_f32 v125,v32,v33
M 18,a[224:227],82
s_add_u32 s68,s68,s63
R 42,2,2048
buffer_store_dwordx4 v[122:125],v15,s[36:39],s67 offen offset:0 sc0 sc1
M 18,a[232:235],86
L 4
L 3
M 18,a[240:243],90
R 46,2,3072
v_cmp_eq_u32 vcc,s53,v7
M 18,a[248:251],94
D 0,56,1024
M 22,a[244:247],90
L 2
s_cmp_eq_u32 vcc_lo,-1
R 50,2,4096
buffer_store_dwordx4 v[122:125],v16,s[36:39],s67 offen offset:0 sc0 sc1
M 22,a[252:255],94
D 0,56,2048
D 0,56,3072
buffer_store_dword v4,v3,s[36:39],s71 offen offset:0 sc0 sc1
buffer_store_dword v4,v3,s[36:39],s71 offen offset:0 sc0 sc1
buffer_store_dword v4,v3,s[36:39],s71 offen offset:0 sc0 sc1
s_cbranch_scc1 Lg1_F1_%=
s_branch Lg1_K1_%=
Lg1_X0_%=:
M 22,a[124:127],94
L 3
s_add_u32 m0,s59,0
v_cvt_pk_bf16_f32 v122,v26,v27
M 22,a[132:135],34
R 54,1,21504
v_fma_f32 v32,-v102,v112,v32
M 22,a[140:143],38
buffer_load_dword v8,s[36:39],s58 offen offset:0 sc1 lds
M 18,a[128:131],34
R 58,1,22528
v_fma_f32 v33,-v102,v113,v33
M 18,a[136:139],38
L 3
v_rcp_f32 v116,v116
M 18,a[144:147],42
R 62,1,23552
v_mul_f32 v28,v28,v116
M 18,a[152:155],46
v_rcp_f32 v117,v117
M 22,a[148:151],42
R 66,1,24576
v_mul_f32 v29,v29,v117
M 22,a[156:159],46
L 3
v_cvt_pk_bf16_f32 v123,v28,v29
v_mul_f32 v118,v30,v30
M 22,a[164:167],50
R 70,1,25600
v_mul_f32 v119,v31,v31
M 22,a[172:175],54
v_mul_f32 v120,v32,v32
v_mul_f32 v121,v33,v33
M 18,a[160:163],50
R 74,1,26624
v_fma_f32 v118,v104,v118,v103
M 18,a[168:171],54
L 3
v_fma_f32 v119,v104,v119,v103
v_fma_f32 v120,v104,v120,v103
M 18,a[176:179],58
R 78,1,27648
v_fma_f32 v121,v104,v121,v103
M 18,a[184:187],62
v_mul_f32 v118,v30,v118
v_mul_f32 v119,v31,v119
M 22,a[180:183],58
R 82,1,28672
v_mul_f32 v120,v32,v120
M 22,a[188:191],62
L 3
v_exp_f32 v118,v118
M 22,a[196:199],66
R 86,1,29696
v_mul_f32 v121,v33,v121
M 22,a[204:207],70
v_exp_f32 v119,v119
M 18,a[192:195],66
R 90,1,30720
v_fma_f32 v118,v118,v105,v105
M 18,a[200:203],70
L 3
v_exp_f32 v120,v120
M 18,a[208:211],74
s_waitcnt vmcnt(0)
R 94,1,31744
v_fma_f32 v119,v119,v105,v105
M 18,a[216:219],78
s_barrier
buffer_store_dword v4,v3,s[36:39],s70 offen offset:0 sc0 sc1
M 22,a[212:215],74
s_mov_b32 s70,s69
ds_read_b32 v7,v6 offset:0
v_fma_f32 v120,v120,v105,v105
M 22,a[220:223],78
L 3
v_exp_f32 v121,v121
M 22,a[228:231],82
s_mov_b32 s69,s68
v_fma_f32 v121,v121,v105,v105
v_rcp_f32 v118,v118
v_rcp_f32 v119,v119
v_rcp_f32 v120,v120
v_rcp_f32 v121,v121
M 22,a[236:239],86
s_add_u32 s68,s68,s63
v_mul_f32 v30,v30,v118
v_mul_f32 v31,v31,v119
v_mul_f32 v32,v32,v120
v_mul_f32 v33,v33,v121
v_cvt_pk_bf16_f32 v124,v30,v31
v_cvt_pk_bf16_f32 v125,v32,v33
M 18,a[224:227],82
buffer_store_dwordx4 v[122:125],v15,s[36:39],s67 offen offset:0 sc0 sc1
M 18,a[232:235],86
L 1
M 18,a[240:243],90
L 0
v_cmp_eq_u32 vcc,s53,v7
M 18,a[248:251],94
buffer_store_dwordx4 v[122:125],v16,s[36:39],s67 offen offset:0 sc0 sc1
M 22,a[244:247],90
s_nop 0
M 22,a[252:255],94
s_mov_b32 s80,1
s_cmp_eq_u32 vcc_lo,-1
s_cbranch_scc0 Lg1_xn0_%=
s_add_u32 m0,s57,0
s_add_u32 s56,s55,0
D 0,56,0
v_fma_f32 v18,-v98,v106,v18
v_fma_f32 v19,-v98,v107,v19
v_fma_f32 v20,-v98,v108,v20
v_fma_f32 v21,-v98,v109,v21
v_mul_f32 v114,v18,v18
v_mul_f32 v115,v19,v19
v_mul_f32 v116,v20,v20
D 0,56,1024
v_mul_f32 v117,v21,v21
v_fma_f32 v114,v100,v114,v99
v_fma_f32 v115,v100,v115,v99
v_fma_f32 v116,v100,v116,v99
v_fma_f32 v117,v100,v117,v99
v_mul_f32 v114,v18,v114
v_mul_f32 v115,v19,v115
D 0,56,2048
v_mul_f32 v116,v20,v116
v_mul_f32 v117,v21,v117
v_exp_f32 v114,v114
v_exp_f32 v115,v115
v_exp_f32 v116,v116
v_exp_f32 v117,v117
v_fma_f32 v114,v114,v101,v101
D 0,56,3072
v_fma_f32 v115,v115,v101,v101
v_fma_f32 v116,v116,v101,v101
v_fma_f32 v117,v117,v101,v101
v_rcp_f32 v114,v114
v_rcp_f32 v115,v115
v_rcp_f32 v116,v116
v_rcp_f32 v117,v117
s_add_u32 s58,s58,s61
s_add_u32 m0,s57,16384
s_add_u32 s56,s55,16384
D 0,56,0
v_mul_f32 v18,v18,v114
v_mul_f32 v19,v19,v115
v_mul_f32 v20,v20,v116
v_mul_f32 v21,v21,v117
v_cvt_pk_bf16_f32 v122,v18,v19
v_cvt_pk_bf16_f32 v123,v20,v21
v_fma_f32 v22,-v98,v110,v22
D 0,56,1024
v_fma_f32 v23,-v98,v111,v23
v_fma_f32 v24,-v98,v112,v24
v_fma_f32 v25,-v98,v113,v25
v_mul_f32 v118,v22,v22
v_mul_f32 v119,v23,v23
v_mul_f32 v120,v24,v24
v_mul_f32 v121,v25,v25
D 0,56,2048
v_fma_f32 v118,v100,v118,v99
v_fma_f32 v119,v100,v119,v99
v_fma_f32 v120,v100,v120,v99
v_fma_f32 v121,v100,v121,v99
v_mul_f32 v118,v22,v118
v_mul_f32 v119,v23,v119
v_mul_f32 v120,v24,v120
D 0,56,3072
v_mul_f32 v121,v25,v121
v_exp_f32 v118,v118
v_exp_f32 v119,v119
v_exp_f32 v120,v120
v_exp_f32 v121,v121
v_fma_f32 v118,v118,v101,v101
v_fma_f32 v119,v119,v101,v101
s_add_u32 s55,s55,s60
s_sub_u32 s57,s84,s57
s_add_u32 s54,s54,s46
v_fma_f32 v120,v120,v101,v101
v_fma_f32 v121,v121,v101,v101
v_rcp_f32 v118,v118
v_rcp_f32 v119,v119
v_rcp_f32 v120,v120
v_rcp_f32 v121,v121
v_mul_f32 v22,v22,v118
v_mul_f32 v23,v23,v119
v_mul_f32 v24,v24,v120
v_mul_f32 v25,v25,v121
v_cvt_pk_bf16_f32 v124,v22,v23
v_cvt_pk_bf16_f32 v125,v24,v25
buffer_store_dwordx4 v[122:125],v15,s[36:39],s66 offen offset:0 sc0 sc1
buffer_store_dwordx4 v[122:125],v16,s[36:39],s66 offen offset:0 sc0 sc1
s_mov_b32 s72,s69
s_mov_b32 s69,0x7fff0000
s_mov_b32 s66,0x7fff0000
s_mov_b32 s76,0x7fff0000
s_branch Lg1_gox_%=
Lg1_xn0_%=:
s_cmp_ge_u32 s54,256
s_cbranch_scc1 Lg1_fin0_%=
s_add_u32 m0,s59,4096
s_nop 0
buffer_load_dword v8,s[36:39],s58 offen offset:0 sc1 lds
buffer_store_dword v4,v3,s[36:39],s71 offen offset:0 sc0 sc1
s_add_u32 m0,s59,5120
s_nop 0
buffer_load_dword v8,s[36:39],s58 offen offset:0 sc1 lds
buffer_store_dword v4,v3,s[36:39],s71 offen offset:0 sc0 sc1
s_mov_b32 s83,0
v_fma_f32 v18,-v98,v106,v18
v_fma_f32 v19,-v98,v107,v19
v_fma_f32 v20,-v98,v108,v20
v_fma_f32 v21,-v98,v109,v21
v_mul_f32 v114,v18,v18
v_mul_f32 v115,v19,v19
v_mul_f32 v116,v20,v20
v_mul_f32 v117,v21,v21
v_fma_f32 v114,v100,v114,v99
v_fma_f32 v115,v100,v115,v99
)"
R"(v_fma_f32 v116,v100,v116,v99
v_fma_f32 v117,v100,v117,v99
v_mul_f32 v114,v18,v114
v_mul_f32 v115,v19,v115
v_mul_f32 v116,v20,v116
v_mul_f32 v117,v21,v117
v_exp_f32 v114,v114
v_exp_f32 v115,v115
v_exp_f32 v116,v116
v_exp_f32 v117,v117
v_fma_f32 v114,v114,v101,v101
v_fma_f32 v115,v115,v101,v101
v_fma_f32 v116,v116,v101,v101
v_fma_f32 v117,v117,v101,v101
v_rcp_f32 v114,v114
v_rcp_f32 v115,v115
v_rcp_f32 v116,v116
v_rcp_f32 v117,v117
v_mul_f32 v18,v18,v114
v_mul_f32 v19,v19,v115
v_mul_f32 v20,v20,v116
v_mul_f32 v21,v21,v117
v_cvt_pk_bf16_f32 v122,v18,v19
v_cvt_pk_bf16_f32 v123,v20,v21
v_fma_f32 v22,-v98,v110,v22
v_fma_f32 v23,-v98,v111,v23
v_fma_f32 v24,-v98,v112,v24
v_fma_f32 v25,-v98,v113,v25
v_mul_f32 v118,v22,v22
v_mul_f32 v119,v23,v23
v_mul_f32 v120,v24,v24
v_mul_f32 v121,v25,v25
v_fma_f32 v118,v100,v118,v99
v_fma_f32 v119,v100,v119,v99
v_fma_f32 v120,v100,v120,v99
v_fma_f32 v121,v100,v121,v99
v_mul_f32 v118,v22,v118
v_mul_f32 v119,v23,v119
v_mul_f32 v120,v24,v120
v_mul_f32 v121,v25,v121
v_exp_f32 v118,v118
v_exp_f32 v119,v119
v_exp_f32 v120,v120
v_exp_f32 v121,v121
v_fma_f32 v118,v118,v101,v101
v_fma_f32 v119,v119,v101,v101
v_fma_f32 v120,v120,v101,v101
v_fma_f32 v121,v121,v101,v101
v_rcp_f32 v118,v118
v_rcp_f32 v119,v119
v_rcp_f32 v120,v120
v_rcp_f32 v121,v121
v_mul_f32 v22,v22,v118
v_mul_f32 v23,v23,v119
v_mul_f32 v24,v24,v120
v_mul_f32 v25,v25,v121
v_cvt_pk_bf16_f32 v124,v22,v23
v_cvt_pk_bf16_f32 v125,v24,v25
buffer_store_dwordx4 v[122:125],v15,s[36:39],s66 offen offset:0 sc0 sc1
buffer_store_dwordx4 v[122:125],v16,s[36:39],s66 offen offset:0 sc0 sc1
s_mov_b32 s72,s69
s_mov_b32 s69,0x7fff0000
s_mov_b32 s66,0x7fff0000
s_mov_b32 s76,0x7fff0000
s_branch Lg1_poll_%=
Lg1_fin0_%=:
v_fma_f32 v18,-v98,v106,v18
v_fma_f32 v19,-v98,v107,v19
v_fma_f32 v20,-v98,v108,v20
v_fma_f32 v21,-v98,v109,v21
v_mul_f32 v114,v18,v18
v_mul_f32 v115,v19,v19
v_mul_f32 v116,v20,v20
v_mul_f32 v117,v21,v21
v_fma_f32 v114,v100,v114,v99
v_fma_f32 v115,v100,v115,v99
v_fma_f32 v116,v100,v116,v99
v_fma_f32 v117,v100,v117,v99
v_mul_f32 v114,v18,v114
v_mul_f32 v115,v19,v115
v_mul_f32 v116,v20,v116
v_mul_f32 v117,v21,v117
v_exp_f32 v114,v114
v_exp_f32 v115,v115
v_exp_f32 v116,v116
v_exp_f32 v117,v117
v_fma_f32 v114,v114,v101,v101
v_fma_f32 v115,v115,v101,v101
v_fma_f32 v116,v116,v101,v101
v_fma_f32 v117,v117,v101,v101
v_rcp_f32 v114,v114
v_rcp_f32 v115,v115
v_rcp_f32 v116,v116
v_rcp_f32 v117,v117
v_mul_f32 v18,v18,v114
v_mul_f32 v19,v19,v115
v_mul_f32 v20,v20,v116
v_mul_f32 v21,v21,v117
v_cvt_pk_bf16_f32 v122,v18,v19
v_cvt_pk_bf16_f32 v123,v20,v21
v_fma_f32 v22,-v98,v110,v22
v_fma_f32 v23,-v98,v111,v23
v_fma_f32 v24,-v98,v112,v24
v_fma_f32 v25,-v98,v113,v25
v_mul_f32 v118,v22,v22
v_mul_f32 v119,v23,v23
v_mul_f32 v120,v24,v24
v_mul_f32 v121,v25,v25
v_fma_f32 v118,v100,v118,v99
v_fma_f32 v119,v100,v119,v99
v_fma_f32 v120,v100,v120,v99
v_fma_f32 v121,v100,v121,v99
v_mul_f32 v118,v22,v118
v_mul_f32 v119,v23,v119
v_mul_f32 v120,v24,v120
v_mul_f32 v121,v25,v121
v_exp_f32 v118,v118
v_exp_f32 v119,v119
v_exp_f32 v120,v120
v_exp_f32 v121,v121
v_fma_f32 v118,v118,v101,v101
v_fma_f32 v119,v119,v101,v101
v_fma_f32 v120,v120,v101,v101
v_fma_f32 v121,v121,v101,v101
v_rcp_f32 v118,v118
v_rcp_f32 v119,v119
v_rcp_f32 v120,v120
v_rcp_f32 v121,v121
v_mul_f32 v22,v22,v118
v_mul_f32 v23,v23,v119
v_mul_f32 v24,v24,v120
v_mul_f32 v25,v25,v121
v_cvt_pk_bf16_f32 v124,v22,v23
v_cvt_pk_bf16_f32 v125,v24,v25
buffer_store_dwordx4 v[122:125],v15,s[36:39],s66 offen offset:0 sc0 sc1
buffer_store_dwordx4 v[122:125],v16,s[36:39],s66 offen offset:0 sc0 sc1
s_mov_b32 s72,s69
s_mov_b32 s69,0x7fff0000
s_mov_b32 s66,0x7fff0000
s_mov_b32 s76,0x7fff0000
s_branch Lg1_done_%=
Lg1_K1_%=:
Lg1_L1_%=:
v_mfma_f32_16x16x32_bf16 v[30:33],a[4:7],v[34:37],0
s_add_u32 m0,s59,3072
s_mov_b32 s67,s65
R 54,2,5120
M 30,a[12:15],38
buffer_load_dword v8,s[36:39],s58 offen offset:0 sc1 lds
v_mfma_f32_16x16x32_bf16 v[26:29],a[0:3],v[34:37],0
s_add_u32 s65,s65,s62
R 58,2,6144
v_fma_f32 v18,-v98,v106,v18
M 26,a[8:11],38
L 3
v_fma_f32 v19,-v98,v107,v19
v_fma_f32 v20,-v98,v108,v20
M 26,a[16:19],42
R 62,2,7168
v_fma_f32 v21,-v98,v109,v21
M 26,a[24:27],46
v_mul_f32 v114,v18,v18
v_mul_f32 v115,v19,v19
M 30,a[20:23],42
R 66,2,8192
v_mul_f32 v116,v20,v20
M 30,a[28:31],46
L 3
v_mul_f32 v117,v21,v21
v_fma_f32 v114,v100,v114,v99
M 30,a[36:39],50
R 70,2,9216
v_fma_f32 v115,v100,v115,v99
M 30,a[44:47],54
v_fma_f32 v116,v100,v116,v99
v_fma_f32 v117,v100,v117,v99
M 26,a[32:35],50
R 74,2,10240
v_mul_f32 v114,v18,v114
M 26,a[40:43],54
L 3
v_exp_f32 v114,v114
M 26,a[48:51],58
R 78,2,11264
v_mul_f32 v115,v19,v115
M 26,a[56:59],62
v_exp_f32 v115,v115
M 30,a[52:55],58
R 82,2,12288
v_mul_f32 v116,v20,v116
M 30,a[60:63],62
L 3
v_exp_f32 v116,v116
M 30,a[68:71],66
R 86,2,13312
v_mul_f32 v117,v21,v117
M 30,a[76:79],70
buffer_load_dwordx4 v[102:105],v11,s[36:39],s73 offen offset:0 sc1
M 26,a[64:67],66
s_add_u32 s73,s73,s64
R 90,2,14336
v_fma_f32 v114,v114,v101,v101
M 26,a[72:75],70
L 3
v_exp_f32 v117,v117
M 26,a[80:83],74
s_waitcnt vmcnt(1)
R 94,2,15360
v_fma_f32 v115,v115,v101,v101
M 26,a[88:91],78
s_barrier
ds_read_b32 v7,v6 offset:3072
v_fma_f32 v116,v116,v101,v101
M 30,a[84:87],74
R 34,2,16384
v_fma_f32 v117,v117,v101,v101
M 30,a[92:95],78
L 4
buffer_store_dword v4,v3,s[36:39],s70 offen offset:0 sc0 sc1
M 30,a[100:103],82
s_mov_b32 s70,0x7fff0000
R 38,2,17408
v_fma_f32 v22,-v98,v110,v22
M 30,a[108:111],86
v_rcp_f32 v114,v114
M 26,a[96:99],82
R 42,2,18432
v_mul_f32 v18,v18,v114
M 26,a[104:107],86
L 4
L 3
v_cmp_eq_u32 vcc,s53,v7
M 26,a[112:115],90
R 46,2,19456
v_fma_f32 v23,-v98,v111,v23
M 26,a[120:123],94
v_rcp_f32 v115,v115
M 30,a[116:119],90
s_cmp_eq_u32 vcc_lo,-1
R 50,2,20480
v_mul_f32 v19,v19,v115
s_cbranch_scc0 Lg1_X1_%=
M 30,a[124:127],94
L 3
s_add_u32 m0,s57,0
s_add_u32 s56,s55,0
M 30,a[132:135],34
s_add_u32 s58,s58,s61
R 54,2,21504
v_cvt_pk_bf16_f32 v122,v18,v19
M 30,a[140:143],38
D 0,56,0
M 26,a[128:131],34
s_add_u32 s54,s54,s46
R 58,2,22528
v_fma_f32 v24,-v98,v112,v24
M 26,a[136:139],38
L 3
v_rcp_f32 v116,v116
M 26,a[144:147],42
R 62,2,23552
v_mul_f32 v20,v20,v116
M 26,a[152:155],46
D 0,56,1024
M 30,a[148:151],42
R 66,2,24576
v_fma_f32 v25,-v98,v113,v25
M 30,a[156:159],46
L 3
v_rcp_f32 v117,v117
M 30,a[164:167],50
R 70,2,25600
v_mul_f32 v21,v21,v117
M 30,a[172:175],54
D 0,56,2048
M 26,a[160:163],50
R 74,2,26624
v_cvt_pk_bf16_f32 v123,v20,v21
M 26,a[168:171],54
L 3
v_mul_f32 v118,v22,v22
v_mul_f32 v119,v23,v23
M 26,a[176:179],58
R 78,2,27648
v_mul_f32 v120,v24,v24
M 26,a[184:187],62
D 0,56,3072
M 30,a[180:183],58
s_add_u32 m0,s59,1024
R 82,2,28672
v_mul_f32 v121,v25,v25
M 30,a[188:191],62
L 3
buffer_load_dword v8,s[36:39],s58 offen offset:0 sc1 lds
M 30,a[196:199],66
s_add_u32 m0,s57,16384
R 86,2,29696
v_fma_f32 v118,v100,v118,v99
M 30,a[204:207],70
s_add_u32 s56,s55,16384
v_fma_f32 v119,v100,v119,v99
v_fma_f32 v120,v100,v120,v99
M 26,a[192:195],66
s_add_u32 s55,s55,s60
R 90,2,30720
v_fma_f32 v121,v100,v121,v99
M 26,a[200:203],70
L 3
D 0,56,0
M 26,a[208:211],74
s_waitcnt vmcnt(1)
R 94,2,31744
v_mul_f32 v118,v22,v118
M 26,a[216:219],78
s_barrier
ds_read_b32 v7,v6 offset:1024
v_mul_f32 v119,v23,v119
M 30,a[212:215],74
s_sub_u32 s57,s84,s57
R 34,1,0
v_mul_f32 v120,v24,v120
M 30,a[220:223],78
L 4
buffer_store_dword v4,v3,s[36:39],s70 offen offset:0 sc0 sc1
v_mul_f32 v121,v25,v121
v_exp_f32 v118,v118
v_exp_f32 v119,v119
v_exp_f32 v120,v120
v_exp_f32 v121,v121
M 30,a[228:231],82
s_mov_b32 s70,s69
R 38,1,1024
v_fma_f32 v118,v118,v101,v101
v_fma_f32 v119,v119,v101,v101
v_fma_f32 v120,v120,v101,v101
v_fma_f32 v121,v121,v101,v101
v_rcp_f32 v118,v118
v_rcp_f32 v119,v119
v_rcp_f32 v120,v120
v_rcp_f32 v121,v121
M 30,a[236:239],86
s_mov_b32 s69,s68
v_mul_f32 v22,v22,v118
v_mul_f32 v23,v23,v119
v_mul_f32 v24,v24,v120
v_mul_f32 v25,v25,v121
v_cvt_pk_bf16_f32 v124,v22,v23
v_cvt_pk_bf16_f32 v125,v24,v25
M 26,a[224:227],82
s_add_u32 s68,s68,s63
R 42,1,2048
buffer_store_dwordx4 v[122:125],v15,s[36:39],s66 offen offset:0 sc0 sc1
M 26,a[232:235],86
L 4
L 3
M 26,a[240:243],90
R 46,1,3072
v_cmp_eq_u32 vcc,s53,v7
M 26,a[248:251],94
D 0,56,1024
M 30,a[244:247],90
L 2
s_cmp_eq_u32 vcc_lo,-1
R 50,1,4096
buffer_store_dwordx4 v[122:125],v16,s[36:39],s66 offen offset:0 sc0 sc1
M 30,a[252:255],94
D 0,56,2048
D 0,56,3072
buffer_store_dword v4,v3,s[36:39],s71 offen offset:0 sc0 sc1
buffer_store_dword v4,v3,s[36:39],s71 offen offset:0 sc0 sc1
buffer_store_dword v4,v3,s[36:39],s71 offen offset:0 sc0 sc1
s_cbranch_scc1 Lg1_F0_%=
s_branch Lg1_K0_%=
Lg1_X1_%=:
M 30,a[124:127],94
L 3
s_add_u32 m0,s59,1024
v_cvt_pk_bf16_f32 v122,v18,v19
M 30,a[132:135],34
R 54,2,21504
v_fma_f32 v24,-v98,v112,v24
M 30,a[140:143],38
buffer_load_dword v8,s[36:39],s58 offen offset:0 sc1 lds
M 26,a[128:131],34
R 58,2,22528
v_fma_f32 v25,-v98,v113,v25
M 26,a[136:139],38
L 3
v_rcp_f32 v116,v116
M 26,a[144:147],42
R 62,2,23552
v_mul_f32 v20,v20,v116
M 26,a[152:155],46
v_rcp_f32 v117,v117
M 30,a[148:151],42
R 66,2,24576
v_mul_f32 v21,v21,v117
M 30,a[156:159],46
L 3
v_cvt_pk_bf16_f32 v123,v20,v21
v_mul_f32 v118,v22,v22
M 30,a[164:167],50
R 70,2,25600
v_mul_f32 v119,v23,v23
M 30,a[172:175],54
v_mul_f32 v120,v24,v24
v_mul_f32 v121,v25,v25
M 26,a[160:163],50
R 74,2,26624
v_fma_f32 v118,v100,v118,v99
M 26,a[168:171],54
L 3
v_fma_f32 v119,v100,v119,v99
v_fma_f32 v120,v100,v120,v99
M 26,a[176:179],58
R 78,2,27648
v_fma_f32 v121,v100,v121,v99
M 26,a[184:187],62
v_mul_f32 v118,v22,v118
v_mul_f32 v119,v23,v119
M 30,a[180:183],58
R 82,2,28672
v_mul_f32 v120,v24,v120
M 30,a[188:191],62
L 3
v_exp_f32 v118,v118
M 30,a[196:199],66
R 86,2,29696
v_mul_f32 v121,v25,v121
M 30,a[204:207],70
v_exp_f32 v119,v119
M 26,a[192:195],66
R 90,2,30720
v_fma_f32 v118,v118,v101,v101
M 26,a[200:203],70
L 3
v_exp_f32 v120,v120
M 26,a[208:211],74
s_waitcnt vmcnt(0)
R 94,2,31744
v_fma_f32 v119,v119,v101,v101
M 26,a[216:219],78
s_barrier
buffer_store_dword v4,v3,s[36:39],s70 offen offset:0 sc0 sc1
M 30,a[212:215],74
s_mov_b32 s70,s69
ds_read_b32 v7,v6 offset:1024
v_fma_f32 v120,v120,v101,v101
M 30,a[220:223],78
L 3
v_exp_f32 v121,v121
M 30,a[228:231],82
s_mov_b32 s69,s68
v_fma_f32 v121,v121,v101,v101
v_rcp_f32 v118,v118
v_rcp_f32 v119,v119
v_rcp_f32 v120,v120
v_rcp_f32 v121,v121
M 30,a[236:239],86
s_add_u32 s68,s68,s63
v_mul_f32 v22,v22,v118
v_mul_f32 v23,v23,v119
v_mul_f32 v24,v24,v120
v_mul_f32 v25,v25,v121
v_cvt_pk_bf16_f32 v124,v22,v23
v_cvt_pk_bf16_f32 v125,v24,v25
M 26,a[224:227],82
buffer_store_dwordx4 v[122:125],v15,s[36:39],s66 offen offset:0 sc0 sc1
M 26,a[232:235],86
L 1
M 26,a[240:243],90
L 0
v_cmp_eq_u32 vcc,s53,v7
M 26,a[248:251],94
buffer_store_dwordx4 v[122:125],v16,s[36:39],s66 offen offset:0 sc0 sc1
M 30,a[244:247],90
s_nop 0
M 30,a[252:255],94
s_mov_b32 s80,0
s_cmp_eq_u32 vcc_lo,-1
s_cbranch_scc0 Lg1_xn1_%=
s_add_u32 m0,s57,0
s_add_u32 s56,s55,0
D 0,56,0
v_fma_f32 v26,-v102,v106,v26
v_fma_f32 v27,-v102,v107,v27
v_fma_f32 v28,-v102,v108,v28
v_fma_f32 v29,-v102,v109,v29
v_mul_f32 v114,v26,v26
v_mul_f32 v115,v27,v27
v_mul_f32 v116,v28,v28
D 0,56,1024
v_mul_f32 v117,v29,v29
v_fma_f32 v114,v104,v114,v103
v_fma_f32 v115,v104,v115,v103
v_fma_f32 v116,v104,v116,v103
v_fma_f32 v117,v104,v117,v103
v_mul_f32 v114,v26,v114
v_mul_f32 v115,v27,v115
D 0,56,2048
)"
R"(v_mul_f32 v116,v28,v116
v_mul_f32 v117,v29,v117
v_exp_f32 v114,v114
v_exp_f32 v115,v115
v_exp_f32 v116,v116
v_exp_f32 v117,v117
v_fma_f32 v114,v114,v105,v105
D 0,56,3072
v_fma_f32 v115,v115,v105,v105
v_fma_f32 v116,v116,v105,v105
v_fma_f32 v117,v117,v105,v105
v_rcp_f32 v114,v114
v_rcp_f32 v115,v115
v_rcp_f32 v116,v116
v_rcp_f32 v117,v117
s_add_u32 s58,s58,s61
s_add_u32 m0,s57,16384
s_add_u32 s56,s55,16384
D 0,56,0
v_mul_f32 v26,v26,v114
v_mul_f32 v27,v27,v115
v_mul_f32 v28,v28,v116
v_mul_f32 v29,v29,v117
v_cvt_pk_bf16_f32 v122,v26,v27
v_cvt_pk_bf16_f32 v123,v28,v29
v_fma_f32 v30,-v102,v110,v30
D 0,56,1024
v_fma_f32 v31,-v102,v111,v31
v_fma_f32 v32,-v102,v112,v32
v_fma_f32 v33,-v102,v113,v33
v_mul_f32 v118,v30,v30
v_mul_f32 v119,v31,v31
v_mul_f32 v120,v32,v32
v_mul_f32 v121,v33,v33
D 0,56,2048
v_fma_f32 v118,v104,v118,v103
v_fma_f32 v119,v104,v119,v103
v_fma_f32 v120,v104,v120,v103
v_fma_f32 v121,v104,v121,v103
v_mul_f32 v118,v30,v118
v_mul_f32 v119,v31,v119
v_mul_f32 v120,v32,v120
D 0,56,3072
v_mul_f32 v121,v33,v121
v_exp_f32 v118,v118
v_exp_f32 v119,v119
v_exp_f32 v120,v120
v_exp_f32 v121,v121
v_fma_f32 v118,v118,v105,v105
v_fma_f32 v119,v119,v105,v105
s_add_u32 s55,s55,s60
s_sub_u32 s57,s84,s57
s_add_u32 s54,s54,s46
v_fma_f32 v120,v120,v105,v105
v_fma_f32 v121,v121,v105,v105
v_rcp_f32 v118,v118
v_rcp_f32 v119,v119
v_rcp_f32 v120,v120
v_rcp_f32 v121,v121
v_mul_f32 v30,v30,v118
v_mul_f32 v31,v31,v119
v_mul_f32 v32,v32,v120
v_mul_f32 v33,v33,v121
v_cvt_pk_bf16_f32 v124,v30,v31
v_cvt_pk_bf16_f32 v125,v32,v33
buffer_store_dwordx4 v[122:125],v15,s[36:39],s67 offen offset:0 sc0 sc1
buffer_store_dwordx4 v[122:125],v16,s[36:39],s67 offen offset:0 sc0 sc1
s_mov_b32 s72,s69
s_mov_b32 s69,0x7fff0000
s_mov_b32 s67,0x7fff0000
s_mov_b32 s77,0x7fff0000
s_branch Lg1_gox_%=
Lg1_xn1_%=:
s_cmp_ge_u32 s54,256
s_cbranch_scc1 Lg1_fin1_%=
s_add_u32 m0,s59,4096
s_nop 0
buffer_load_dword v8,s[36:39],s58 offen offset:0 sc1 lds
buffer_store_dword v4,v3,s[36:39],s71 offen offset:0 sc0 sc1
s_add_u32 m0,s59,5120
s_nop 0
buffer_load_dword v8,s[36:39],s58 offen offset:0 sc1 lds
buffer_store_dword v4,v3,s[36:39],s71 offen offset:0 sc0 sc1
s_mov_b32 s83,0
v_fma_f32 v26,-v102,v106,v26
v_fma_f32 v27,-v102,v107,v27
v_fma_f32 v28,-v102,v108,v28
v_fma_f32 v29,-v102,v109,v29
v_mul_f32 v114,v26,v26
v_mul_f32 v115,v27,v27
v_mul_f32 v116,v28,v28
v_mul_f32 v117,v29,v29
v_fma_f32 v114,v104,v114,v103
v_fma_f32 v115,v104,v115,v103
v_fma_f32 v116,v104,v116,v103
v_fma_f32 v117,v104,v117,v103
v_mul_f32 v114,v26,v114
v_mul_f32 v115,v27,v115
v_mul_f32 v116,v28,v116
v_mul_f32 v117,v29,v117
v_exp_f32 v114,v114
v_exp_f32 v115,v115
v_exp_f32 v116,v116
v_exp_f32 v117,v117
v_fma_f32 v114,v114,v105,v105
v_fma_f32 v115,v115,v105,v105
v_fma_f32 v116,v116,v105,v105
v_fma_f32 v117,v117,v105,v105
v_rcp_f32 v114,v114
v_rcp_f32 v115,v115
v_rcp_f32 v116,v116
v_rcp_f32 v117,v117
v_mul_f32 v26,v26,v114
v_mul_f32 v27,v27,v115
v_mul_f32 v28,v28,v116
v_mul_f32 v29,v29,v117
v_cvt_pk_bf16_f32 v122,v26,v27
v_cvt_pk_bf16_f32 v123,v28,v29
v_fma_f32 v30,-v102,v110,v30
v_fma_f32 v31,-v102,v111,v31
v_fma_f32 v32,-v102,v112,v32
v_fma_f32 v33,-v102,v113,v33
v_mul_f32 v118,v30,v30
v_mul_f32 v119,v31,v31
v_mul_f32 v120,v32,v32
v_mul_f32 v121,v33,v33
v_fma_f32 v118,v104,v118,v103
v_fma_f32 v119,v104,v119,v103
v_fma_f32 v120,v104,v120,v103
v_fma_f32 v121,v104,v121,v103
v_mul_f32 v118,v30,v118
v_mul_f32 v119,v31,v119
v_mul_f32 v120,v32,v120
v_mul_f32 v121,v33,v121
v_exp_f32 v118,v118
v_exp_f32 v119,v119
v_exp_f32 v120,v120
v_exp_f32 v121,v121
v_fma_f32 v118,v118,v105,v105
v_fma_f32 v119,v119,v105,v105
v_fma_f32 v120,v120,v105,v105
v_fma_f32 v121,v121,v105,v105
v_rcp_f32 v118,v118
v_rcp_f32 v119,v119
v_rcp_f32 v120,v120
v_rcp_f32 v121,v121
v_mul_f32 v30,v30,v118
v_mul_f32 v31,v31,v119
v_mul_f32 v32,v32,v120
v_mul_f32 v33,v33,v121
v_cvt_pk_bf16_f32 v124,v30,v31
v_cvt_pk_bf16_f32 v125,v32,v33
buffer_store_dwordx4 v[122:125],v15,s[36:39],s67 offen offset:0 sc0 sc1
buffer_store_dwordx4 v[122:125],v16,s[36:39],s67 offen offset:0 sc0 sc1
s_mov_b32 s72,s69
s_mov_b32 s69,0x7fff0000
s_mov_b32 s67,0x7fff0000
s_mov_b32 s77,0x7fff0000
s_branch Lg1_poll_%=
Lg1_fin1_%=:
v_fma_f32 v26,-v102,v106,v26
v_fma_f32 v27,-v102,v107,v27
v_fma_f32 v28,-v102,v108,v28
v_fma_f32 v29,-v102,v109,v29
v_mul_f32 v114,v26,v26
v_mul_f32 v115,v27,v27
v_mul_f32 v116,v28,v28
v_mul_f32 v117,v29,v29
v_fma_f32 v114,v104,v114,v103
v_fma_f32 v115,v104,v115,v103
v_fma_f32 v116,v104,v116,v103
v_fma_f32 v117,v104,v117,v103
v_mul_f32 v114,v26,v114
v_mul_f32 v115,v27,v115
v_mul_f32 v116,v28,v116
v_mul_f32 v117,v29,v117
v_exp_f32 v114,v114
v_exp_f32 v115,v115
v_exp_f32 v116,v116
v_exp_f32 v117,v117
v_fma_f32 v114,v114,v105,v105
v_fma_f32 v115,v115,v105,v105
v_fma_f32 v116,v116,v105,v105
v_fma_f32 v117,v117,v105,v105
v_rcp_f32 v114,v114
v_rcp_f32 v115,v115
v_rcp_f32 v116,v116
v_rcp_f32 v117,v117
v_mul_f32 v26,v26,v114
v_mul_f32 v27,v27,v115
v_mul_f32 v28,v28,v116
v_mul_f32 v29,v29,v117
v_cvt_pk_bf16_f32 v122,v26,v27
v_cvt_pk_bf16_f32 v123,v28,v29
v_fma_f32 v30,-v102,v110,v30
v_fma_f32 v31,-v102,v111,v31
v_fma_f32 v32,-v102,v112,v32
v_fma_f32 v33,-v102,v113,v33
v_mul_f32 v118,v30,v30
v_mul_f32 v119,v31,v31
v_mul_f32 v120,v32,v32
v_mul_f32 v121,v33,v33
v_fma_f32 v118,v104,v118,v103
v_fma_f32 v119,v104,v119,v103
v_fma_f32 v120,v104,v120,v103
v_fma_f32 v121,v104,v121,v103
v_mul_f32 v118,v30,v118
v_mul_f32 v119,v31,v119
v_mul_f32 v120,v32,v120
v_mul_f32 v121,v33,v121
v_exp_f32 v118,v118
v_exp_f32 v119,v119
v_exp_f32 v120,v120
v_exp_f32 v121,v121
v_fma_f32 v118,v118,v105,v105
v_fma_f32 v119,v119,v105,v105
v_fma_f32 v120,v120,v105,v105
v_fma_f32 v121,v121,v105,v105
v_rcp_f32 v118,v118
v_rcp_f32 v119,v119
v_rcp_f32 v120,v120
v_rcp_f32 v121,v121
v_mul_f32 v30,v30,v118
v_mul_f32 v31,v31,v119
v_mul_f32 v32,v32,v120
v_mul_f32 v33,v33,v121
v_cvt_pk_bf16_f32 v124,v30,v31
v_cvt_pk_bf16_f32 v125,v32,v33
buffer_store_dwordx4 v[122:125],v15,s[36:39],s67 offen offset:0 sc0 sc1
buffer_store_dwordx4 v[122:125],v16,s[36:39],s67 offen offset:0 sc0 sc1
s_mov_b32 s72,s69
s_mov_b32 s69,0x7fff0000
s_mov_b32 s67,0x7fff0000
s_mov_b32 s77,0x7fff0000
s_branch Lg1_done_%=
Lg1_done_%=:
s_waitcnt vmcnt(2)
s_barrier
buffer_store_dword v4,v3,s[36:39],s70 offen offset:0 sc0 sc1
s_waitcnt vmcnt(1)
s_barrier
buffer_store_dword v4,v3,s[36:39],s72 offen offset:0 sc0 sc1
s_waitcnt vmcnt(0) lgkmcnt(0)
s_barrier
s_branch Lg2_end_%=
Lg2_begin_%=:
Lcode_g2_b:
s_mov_b64 s[36:37],%1
s_mov_b32 s38,111243392
s_mov_b32 s39,0x00020000
s_mov_b32 s44,%2
s_mov_b32 s45,%3
s_mov_b32 s46,%4
s_mov_b32 s47,%5
s_mov_b32 s48,%6
s_mov_b32 s49,%7
s_mov_b32 s50,%8
s_mov_b32 s51,%9
s_mov_b32 s53,%10
v_mov_b32 v0,%11
v_mov_b32 v3,%12
v_mov_b32 v5,%13
v_mov_b32 v6,%14
v_mov_b32 v18,%15
v_mov_b32 v19,%16
v_mov_b32 v20,%17
v_mov_b32 v21,%18
v_mov_b32 v9,%19
v_mov_b32 v10,%20
v_mov_b32 v11,%21
v_mov_b32 v12,%22
v_mov_b32 v13,%23
v_mov_b32 v14,%24
v_mov_b32 v15,%25
v_mov_b32 v16,%26
s_mov_b64 s[40:41],%27
s_mov_b32 s42,2097152
s_mov_b32 s43,0x00020000
s_lshl_b32 s57,s44,11
s_cmp_ge_u32 s45,8
s_cbranch_scc1 Lg2_dly_%=
s_sleep 16
s_sleep 16
s_sleep 16
s_sleep 16
Lg2_dly_%=:
s_add_u32 s55,s50,0
s_add_u32 m0,s57,0
s_nop 0
buffer_load_dwordx4 v18,s[40:43],s55 offen offset:0  lds
s_add_u32 m0,s57,1024
s_nop 0
buffer_load_dwordx4 v19,s[40:43],s55 offen offset:0  lds
s_add_u32 m0,s57,8064
s_nop 0
buffer_load_dwordx4 v18,s[40:43],s55 offen offset:128  lds
s_add_u32 m0,s57,9088
s_nop 0
buffer_load_dwordx4 v19,s[40:43],s55 offen offset:128  lds
s_add_u32 s55,s50,131072
s_add_u32 m0,s57,16384
s_nop 0
buffer_load_dwordx4 v18,s[40:43],s55 offen offset:0  lds
s_add_u32 m0,s57,17408
s_nop 0
buffer_load_dwordx4 v19,s[40:43],s55 offen offset:0  lds
s_add_u32 m0,s57,24448
s_nop 0
buffer_load_dwordx4 v18,s[40:43],s55 offen offset:128  lds
s_add_u32 m0,s57,25472
s_nop 0
buffer_load_dwordx4 v19,s[40:43],s55 offen offset:128  lds
s_add_u32 s55,s50,262144
s_add_u32 m0,s57,32768
s_nop 0
buffer_load_dwordx4 v18,s[40:43],s55 offen offset:0  lds
s_add_u32 m0,s57,33792
s_nop 0
buffer_load_dwordx4 v19,s[40:43],s55 offen offset:0  lds
s_add_u32 m0,s57,40832
s_nop 0
buffer_load_dwordx4 v18,s[40:43],s55 offen offset:128  lds
s_add_u32 m0,s57,41856
s_nop 0
buffer_load_dwordx4 v19,s[40:43],s55 offen offset:128  lds
s_add_u32 s55,s50,393216
s_add_u32 m0,s57,49152
s_nop 0
buffer_load_dwordx4 v18,s[40:43],s55 offen offset:0  lds
s_add_u32 m0,s57,50176
s_nop 0
buffer_load_dwordx4 v19,s[40:43],s55 offen offset:0  lds
s_add_u32 m0,s57,57216
s_nop 0
buffer_load_dwordx4 v18,s[40:43],s55 offen offset:128  lds
s_add_u32 m0,s57,58240
s_nop 0
buffer_load_dwordx4 v19,s[40:43],s55 offen offset:128  lds
s_add_u32 s55,s50,524288
s_add_u32 m0,s57,65536
s_nop 0
buffer_load_dwordx4 v18,s[40:43],s55 offen offset:0  lds
s_add_u32 m0,s57,66560
s_nop 0
buffer_load_dwordx4 v19,s[40:43],s55 offen offset:0  lds
s_add_u32 m0,s57,73600
s_nop 0
buffer_load_dwordx4 v18,s[40:43],s55 offen offset:128  lds
s_add_u32 m0,s57,74624
s_nop 0
buffer_load_dwordx4 v19,s[40:43],s55 offen offset:128  lds
s_add_u32 s55,s50,655360
s_add_u32 m0,s57,81920
s_nop 0
buffer_load_dwordx4 v18,s[40:43],s55 offen offset:0  lds
s_add_u32 m0,s57,82944
s_nop 0
buffer_load_dwordx4 v19,s[40:43],s55 offen offset:0  lds
s_add_u32 m0,s57,89984
s_nop 0
buffer_load_dwordx4 v18,s[40:43],s55 offen offset:128  lds
s_add_u32 m0,s57,91008
s_nop 0
buffer_load_dwordx4 v19,s[40:43],s55 offen offset:128  lds
s_waitcnt vmcnt(20) lgkmcnt(0)
s_barrier
s_add_u32 s55,s50,786432
s_add_u32 m0,s57,98304
s_nop 0
buffer_load_dwordx4 v18,s[40:43],s55 offen offset:0  lds
s_add_u32 m0,s57,99328
s_nop 0
buffer_load_dwordx4 v19,s[40:43],s55 offen offset:0  lds
s_add_u32 m0,s57,106368
s_nop 0
buffer_load_dwordx4 v18,s[40:43],s55 offen offset:128  lds
s_add_u32 m0,s57,107392
s_nop 0
buffer_load_dwordx4 v19,s[40:43],s55 offen offset:128  lds
v_add_u32 v26,0,v20
v_add_u32 v27,0,v21
T a[0:1],26,0
T a[2:3],26,512
T a[4:5],27,0
T a[6:7],27,512
T a[8:9],26,4096
T a[10:11],26,4608
T a[12:13],27,4096
T a[14:15],27,4608
s_waitcnt vmcnt(20) lgkmcnt(0)
s_barrier
s_add_u32 s55,s50,917504
s_add_u32 m0,s57,0
s_nop 0
buffer_load_dwordx4 v18,s[40:43],s55 offen offset:0  lds
s_add_u32 m0,s57,1024
s_nop 0
buffer_load_dwordx4 v19,s[40:43],s55 offen offset:0  lds
s_add_u32 m0,s57,8064
s_nop 0
buffer_load_dwordx4 v18,s[40:43],s55 offen offset:128  lds
s_add_u32 m0,s57,9088
s_nop 0
buffer_load_dwordx4 v19,s[40:43],s55 offen offset:128  lds
v_add_u32 v26,16384,v20
v_add_u32 v27,16384,v21
T a[16:17],26,0
T a[18:19],26,512
T a[20:21],27,0
T a[22:23],27,512
T a[24:25],26,4096
T a[26:27],26,4608
T a[28:29],27,4096
T a[30:31],27,4608
s_waitcnt vmcnt(20) lgkmcnt(0)
s_barrier
s_add_u32 s55,s50,1048576
s_add_u32 m0,s57,16384
s_nop 0
buffer_load_dwordx4 v18,s[40:43],s55 offen offset:0  lds
s_add_u32 m0,s57,17408
s_nop 0
buffer_load_dwordx4 v19,s[40:43],s55 offen offset:0  lds
s_add_u32 m0,s57,24448
s_nop 0
buffer_load_dwordx4 v18,s[40:43],s55 offen offset:128  lds
s_add_u32 m0,s57,25472
s_nop 0
buffer_load_dwordx4 v19,s[40:43],s55 offen offset:128  lds
v_add_u32 v26,32768,v20
v_add_u32 v27,32768,v21
T a[32:33],26,0
T a[34:35],26,512
T a[36:37],27,0
T a[38:39],27,512
T a[40:41],26,4096
T a[42:43],26,4608
T a[44:45],27,4096
T a[46:47],27,4608
s_waitcnt vmcnt(20) lgkmcnt(0)
s_barrier
s_add_u32 s55,s50,1179648
s_add_u32 m0,s57,32768
s_nop 0
buffer_load_dwordx4 v18,s[40:43],s55 offen offset:0  lds
s_add_u32 m0,s57,33792
s_nop 0
buffer_load_dwordx4 v19,s[40:43],s55 offen offset:0  lds
s_add_u32 m0,s57,40832
s_nop 0
buffer_load_dwordx4 v18,s[40:43],s55 offen offset:128  lds
s_add_u32 m0,s57,41856
s_nop 0
buffer_load_dwordx4 v19,s[40:43],s55 offen offset:128  lds
v_add_u32 v26,49152,v20
v_add_u32 v27,49152,v21
T a[48:49],26,0
T a[50:51],26,512
T a[52:53],27,0
T a[54:55],27,512
T a[56:57],26,4096
T a[58:59],26,4608
T a[60:61],27,4096
T a[62:63],27,4608
s_waitcnt vmcnt(20) lgkmcnt(0)
s_barrier
s_add_u32 s55,s50,1310720
s_add_u32 m0,s57,49152
s_nop 0
buffer_load_dwordx4 v18,s[40:43],s55 offen offset:0  lds
s_add_u32 m0,s57,50176
s_nop 0
buffer_load_dwordx4 v19,s[40:43],s55 offen offset:0  lds
s_add_u32 m0,s57,57216
s_nop 0
buffer_load_dwordx4 v18,s[40:43],s55 offen offset:128  lds
s_add_u32 m0,s57,58240
s_nop 0
buffer_load_dwordx4 v19,s[40:43],s55 offen offset:128  lds
v_add_u32 v26,65536,v20
v_add_u32 v27,65536,v21
T a[64:65],26,0
T a[66:67],26,512
T a[68:69],27,0
T a[70:71],27,512
T a[72:73],26,4096
T a[74:75],26,4608
T a[76:77],27,4096
T a[78:79],27,4608
s_waitcnt vmcnt(20) lgkmcnt(0)
s_barrier
s_add_u32 s55,s50,1441792
s_add_u32 m0,s57,65536
s_nop 0
buffer_load_dwordx4 v18,s[40:43],s55 offen offset:0  lds
s_add_u32 m0,s57,66560
s_nop 0
buffer_load_dwordx4 v19,s[40:43],s55 offen offset:0  lds
s_add_u32 m0,s57,73600
s_nop 0
)"
R"(buffer_load_dwordx4 v18,s[40:43],s55 offen offset:128  lds
s_add_u32 m0,s57,74624
s_nop 0
buffer_load_dwordx4 v19,s[40:43],s55 offen offset:128  lds
v_add_u32 v26,81920,v20
v_add_u32 v27,81920,v21
T a[80:81],26,0
T a[82:83],26,512
T a[84:85],27,0
T a[86:87],27,512
T a[88:89],26,4096
T a[90:91],26,4608
T a[92:93],27,4096
T a[94:95],27,4608
s_waitcnt vmcnt(20) lgkmcnt(0)
s_barrier
s_add_u32 s55,s50,1572864
s_add_u32 m0,s57,81920
s_nop 0
buffer_load_dwordx4 v18,s[40:43],s55 offen offset:0  lds
s_add_u32 m0,s57,82944
s_nop 0
buffer_load_dwordx4 v19,s[40:43],s55 offen offset:0  lds
s_add_u32 m0,s57,89984
s_nop 0
buffer_load_dwordx4 v18,s[40:43],s55 offen offset:128  lds
s_add_u32 m0,s57,91008
s_nop 0
buffer_load_dwordx4 v19,s[40:43],s55 offen offset:128  lds
v_add_u32 v26,98304,v20
v_add_u32 v27,98304,v21
T a[96:97],26,0
T a[98:99],26,512
T a[100:101],27,0
T a[102:103],27,512
T a[104:105],26,4096
T a[106:107],26,4608
T a[108:109],27,4096
T a[110:111],27,4608
s_waitcnt vmcnt(20) lgkmcnt(0)
s_barrier
s_add_u32 s55,s50,1703936
s_add_u32 m0,s57,98304
s_nop 0
buffer_load_dwordx4 v18,s[40:43],s55 offen offset:0  lds
s_add_u32 m0,s57,99328
s_nop 0
buffer_load_dwordx4 v19,s[40:43],s55 offen offset:0  lds
s_add_u32 m0,s57,106368
s_nop 0
buffer_load_dwordx4 v18,s[40:43],s55 offen offset:128  lds
s_add_u32 m0,s57,107392
s_nop 0
buffer_load_dwordx4 v19,s[40:43],s55 offen offset:128  lds
v_add_u32 v26,0,v20
v_add_u32 v27,0,v21
T a[112:113],26,0
T a[114:115],26,512
T a[116:117],27,0
T a[118:119],27,512
T a[120:121],26,4096
T a[122:123],26,4608
T a[124:125],27,4096
T a[126:127],27,4608
s_waitcnt vmcnt(20) lgkmcnt(0)
s_barrier
s_add_u32 s55,s50,1835008
s_add_u32 m0,s57,0
s_nop 0
buffer_load_dwordx4 v18,s[40:43],s55 offen offset:0  lds
s_add_u32 m0,s57,1024
s_nop 0
buffer_load_dwordx4 v19,s[40:43],s55 offen offset:0  lds
s_add_u32 m0,s57,8064
s_nop 0
buffer_load_dwordx4 v18,s[40:43],s55 offen offset:128  lds
s_add_u32 m0,s57,9088
s_nop 0
buffer_load_dwordx4 v19,s[40:43],s55 offen offset:128  lds
v_add_u32 v26,16384,v20
v_add_u32 v27,16384,v21
T a[128:129],26,0
T a[130:131],26,512
T a[132:133],27,0
T a[134:135],27,512
T a[136:137],26,4096
T a[138:139],26,4608
T a[140:141],27,4096
T a[142:143],27,4608
s_waitcnt vmcnt(20) lgkmcnt(0)
s_barrier
s_add_u32 s55,s50,1966080
s_add_u32 m0,s57,16384
s_nop 0
buffer_load_dwordx4 v18,s[40:43],s55 offen offset:0  lds
s_add_u32 m0,s57,17408
s_nop 0
buffer_load_dwordx4 v19,s[40:43],s55 offen offset:0  lds
s_add_u32 m0,s57,24448
s_nop 0
buffer_load_dwordx4 v18,s[40:43],s55 offen offset:128  lds
s_add_u32 m0,s57,25472
s_nop 0
buffer_load_dwordx4 v19,s[40:43],s55 offen offset:128  lds
v_add_u32 v26,32768,v20
v_add_u32 v27,32768,v21
T a[144:145],26,0
T a[146:147],26,512
T a[148:149],27,0
T a[150:151],27,512
T a[152:153],26,4096
T a[154:155],26,4608
T a[156:157],27,4096
T a[158:159],27,4608
s_waitcnt vmcnt(20) lgkmcnt(0)
s_barrier
v_add_u32 v26,49152,v20
v_add_u32 v27,49152,v21
T a[160:161],26,0
T a[162:163],26,512
T a[164:165],27,0
T a[166:167],27,512
T a[168:169],26,4096
T a[170:171],26,4608
T a[172:173],27,4096
T a[174:175],27,4608
s_waitcnt vmcnt(16) lgkmcnt(0)
s_barrier
v_add_u32 v26,65536,v20
v_add_u32 v27,65536,v21
T a[176:177],26,0
T a[178:179],26,512
T a[180:181],27,0
T a[182:183],27,512
T a[184:185],26,4096
T a[186:187],26,4608
T a[188:189],27,4096
T a[190:191],27,4608
s_waitcnt vmcnt(12) lgkmcnt(0)
s_barrier
v_add_u32 v26,81920,v20
v_add_u32 v27,81920,v21
T a[192:193],26,0
T a[194:195],26,512
T a[196:197],27,0
T a[198:199],27,512
T a[200:201],26,4096
T a[202:203],26,4608
T a[204:205],27,4096
T a[206:207],27,4608
s_waitcnt vmcnt(8) lgkmcnt(0)
s_barrier
v_add_u32 v26,98304,v20
v_add_u32 v27,98304,v21
T a[208:209],26,0
T a[210:211],26,512
T a[212:213],27,0
T a[214:215],27,512
T a[216:217],26,4096
T a[218:219],26,4608
T a[220:221],27,4096
T a[222:223],27,4608
s_waitcnt vmcnt(4) lgkmcnt(0)
s_barrier
v_add_u32 v26,0,v20
v_add_u32 v27,0,v21
T a[224:225],26,0
T a[226:227],26,512
T a[228:229],27,0
T a[230:231],27,512
T a[232:233],26,4096
T a[234:235],26,4608
T a[236:237],27,4096
T a[238:239],27,4608
s_waitcnt vmcnt(0) lgkmcnt(0)
s_barrier
v_add_u32 v26,16384,v20
v_add_u32 v27,16384,v21
T a[240:241],26,0
T a[242:243],26,512
T a[244:245],27,0
T a[246:247],27,512
T a[248:249],26,4096
T a[250:251],26,4608
T a[252:253],27,4096
T a[254:255],27,4608
L 0
s_barrier
v_mov_b32 v4,s53
s_mov_b32 s82,0
s_mov_b32 s80,0
s_mov_b32 s54,s45
s_mul_i32 s57,s44,5120
s_mul_i32 s55,s45,40960
s_add_u32 s55,s55,s57
s_add_u32 s55,s55,17825792
s_mul_i32 s57,s44,4096
s_mul_i32 s60,s46,40960
s_mul_i32 s61,s46,1024
s_mul_i32 s62,s46,32768
s_mul_i32 s63,s46,1024
s_mul_i32 s64,s46,512
s_lshl_b32 s59,s44,8
s_add_u32 s59,s59,131072
s_mul_i32 s58,s45,1024
s_add_u32 s58,s58,109333504
s_mul_i32 s65,s45,32768
s_add_u32 s65,s65,s47
s_mul_i32 s68,s45,1024
s_add_u32 s68,s68,s48
s_mul_i32 s73,s45,512
s_add_u32 s73,s73,106954752
s_mul_i32 s74,s45,32768
s_add_u32 s74,s74,s49
s_mul_i32 s75,s45,1024
s_add_u32 s75,s75,107479040
s_mov_b32 s66,0x7fff0000
s_mov_b32 s67,0x7fff0000
s_mov_b32 s69,0x7fff0000
s_mov_b32 s76,0x7fff0000
s_mov_b32 s77,0x7fff0000
s_mov_b32 s70,0x7fff0000
s_mov_b32 s71,0x7fff0000
s_mov_b32 s72,0x7fff0000
v_mov_b32 v1,v0
v_add_u32 v2,16384,v0
s_mov_b32 s84,16384
s_mov_b32 s86,0x3a800000
s_mov_b32 s87,0x38a7c5ac
s_mov_b32 s88,0xc0135761
s_mov_b32 s89,0x3d372713
s_lshr_b32 s85,s44,1
s_mul_i32 s85,s85,127
v_lshlrev_b32 v8,22,v0
v_and_b32 v8,0x80000000,v8
v_or_b32 v8,v8,v5
v_add_u32 v2,32768,v0
s_lshl_b32 s84,s57,1
s_add_u32 s84,s84,32768
s_getpc_b64 s[40:41]
Lg2_pc_%=:
s_lshl_b32 s78,s44,13
v_lshlrev_b32 v106,3,v0
v_add_u32 v106,s78,v106
v_min_u32 v106,Lcode_g12_e-Lg2_pc_%=-4,v106
s_mov_b32 m0,163584
s_nop 0
global_load_lds_dword v106,s[40:41]
s_and_b32 s79,s54,127
s_cmp_eq_u32 s79,0
s_cselect_b32 s94,0x3300,0
s_cmp_eq_u32 s79,127
s_cselect_b32 s94,0xcc00,s94
s_add_u32 m0,s59,4096
s_nop 0
buffer_load_dword v8,s[36:39],s58 offen offset:0 sc1 lds
buffer_store_dword v4,v3,s[36:39],s71 offen offset:0 sc0 sc1
s_add_u32 m0,s59,5120
s_nop 0
buffer_load_dword v8,s[36:39],s58 offen offset:0 sc1 lds
buffer_store_dword v4,v3,s[36:39],s71 offen offset:0 sc0 sc1
s_mov_b32 s83,0
Lg2_poll_%=:
s_add_u32 s78,s83,2
s_and_b32 s78,s78,3
s_lshl_b32 s78,s78,10
s_add_u32 s78,s78,4096
s_add_u32 m0,s59,s78
s_nop 0
buffer_load_dword v8,s[36:39],s58 offen offset:0 sc1 lds
s_and_b32 s78,s83,3
s_lshl_b32 s78,s78,10
v_add_u32 v106,s78,v6
s_waitcnt vmcnt(4)
s_barrier
ds_read_b32 v7,v106 offset:4096
buffer_store_dword v4,v3,s[36:39],s70 offen offset:0 sc0 sc1
s_mov_b32 s70,s71
s_mov_b32 s71,s72
s_mov_b32 s72,0x7fff0000
s_add_u32 s83,s83,1
s_add_u32 s82,s82,1
L 0
v_cmp_eq_u32 vcc,s53,v7
s_nop 0
s_or_b64 vcc,vcc,s[94:95]
s_cmp_eq_u32 vcc_lo,-1
s_cbranch_scc1 Lg2_go_%=
s_cmp_lt_u32 s82,20000
s_cbranch_scc1 Lg2_poll_%=
Lg2_go_%=:
s_and_b32 s78,s54,127
s_cmp_eq_u32 s78,s85
s_cselect_b64 s[40:41],-1,0
v_cndmask_b32 v17,v15,v16,s[40:41]
s_add_u32 m0,s57,0
s_add_u32 s56,s55,0
D 17,56,0
s_add_u32 s56,s55,256
D 17,56,1024
s_add_u32 s56,s55,512
D 17,56,2048
s_add_u32 s56,s55,768
D 17,56,3072
s_add_u32 s58,s58,s61
s_add_u32 m0,s59,2048
s_nop 0
buffer_load_dword v8,s[36:39],s58 offen offset:0 sc1 lds
v_cndmask_b32 v17,v10,v9,s[40:41]
s_add_u32 m0,s57,16384
s_add_u32 s56,s55,20480
D 17,56,0
s_add_u32 s56,s55,20736
D 17,56,1024
s_add_u32 s56,s55,20992
D 17,56,2048
s_add_u32 s56,s55,21248
D 17,56,3072
s_add_u32 s55,s55,s60
s_sub_u32 s57,s84,s57
s_add_u32 s54,s54,s46
s_and_b32 s79,s54,127
s_cmp_eq_u32 s79,0
s_cselect_b32 s94,0x3300,0
s_cmp_eq_u32 s79,127
s_cselect_b32 s94,0xcc00,s94
s_waitcnt vmcnt(4)
s_barrier
buffer_store_dword v4,v3,s[36:39],s70 offen offset:0 sc0 sc1
s_min_u32 s71,s71,s72
buffer_store_dword v4,v3,s[36:39],s71 offen offset:0 sc0 sc1
s_mov_b32 s70,0x7fff0000
s_mov_b32 s71,0x7fff0000
s_mov_b32 s72,0x7fff0000
s_cmp_eq_u32 s80,1
buffer_store_dword v4,v3,s[36:39],s71 offen offset:0 sc0 sc1
s_cbranch_scc1 Lg2_st1_%=
s_branch Lg2_st0_%=
Lg2_gox_%=:
s_add_u32 m0,s59,2048
s_nop 0
buffer_load_dword v8,s[36:39],s58 offen offset:0 sc1 lds
s_and_b32 s79,s54,127
s_cmp_eq_u32 s79,0
s_cselect_b32 s94,0x3300,0
s_cmp_eq_u32 s79,127
s_cselect_b32 s94,0xcc00,s94
s_waitcnt vmcnt(0)
s_barrier
buffer_store_dword v4,v3,s[36:39],s70 offen offset:0 sc0 sc1
s_min_u32 s71,s71,s72
buffer_store_dword v4,v3,s[36:39],s71 offen offset:0 sc0 sc1
s_mov_b32 s70,0x7fff0000
s_mov_b32 s71,0x7fff0000
s_mov_b32 s72,0x7fff0000
s_cmp_eq_u32 s80,1
s_cbranch_scc1 Lg2_st1_%=
Lg2_st0_%=:
ds_read_b32 v7,v6 offset:2048
R 34,1,0
R 38,1,1024
R 42,1,2048
R 46,1,3072
R 50,1,4096
L 5
v_cmp_eq_u32 vcc,s53,v7
s_nop 0
s_or_b64 vcc,vcc,s[94:95]
L 3
s_cmp_eq_u32 vcc_lo,-1
s_cbranch_scc1 Lg2_F0_%=
s_branch Lg2_L0_%=
Lg2_st1_%=:
ds_read_b32 v7,v6 offset:2048
R 34,2,0
R 38,2,1024
R 42,2,2048
R 46,2,3072
R 50,2,4096
L 5
v_cmp_eq_u32 vcc,s53,v7
s_nop 0
s_or_b64 vcc,vcc,s[94:95]
L 3
s_cmp_eq_u32 vcc_lo,-1
s_cbranch_scc1 Lg2_F1_%=
s_branch Lg2_L1_%=
Lg2_F0_%=:
v_mfma_f32_16x16x32_bf16 v[22:25],a[4:7],v[34:37],0
s_add_u32 s58,s58,s61
s_add_u32 m0,s59,0
s_and_b32 s78,s54,127
R 54,1,5120
M 22,a[12:15],38
s_cmp_eq_u32 s78,s85
s_cselect_b64 s[40:41],-1,0
s_add_u32 s56,s55,0
buffer_load_dword v8,s[36:39],s58 offen offset:0 sc1 lds
v_mfma_f32_16x16x32_bf16 v[18:21],a[0:3],v[34:37],0
s_add_u32 m0,s57,0
R 58,1,6144
v_cndmask_b32 v17,v15,v16,s[40:41]
M 18,a[8:11],38
L 3
D 17,56,0
M 18,a[16:19],42
s_add_u32 s56,s55,256
R 62,1,7168
v_lshlrev_b32 v106,16,v126
M 18,a[24:27],46
s_add_u32 s54,s54,s46
v_and_b32 v107,0xffff0000,v126
v_lshlrev_b32 v108,16,v127
M 22,a[20:23],42
s_and_b32 s79,s54,127
R 66,1,8192
v_and_b32 v109,0xffff0000,v127
M 22,a[28:31],46
L 3
D 17,56,1024
M 22,a[36:39],50
s_add_u32 s56,s55,512
R 70,1,9216
v_lshlrev_b32 v110,16,v128
M 22,a[44:47],54
s_cmp_eq_u32 s79,0
v_and_b32 v111,0xffff0000,v128
v_lshlrev_b32 v112,16,v129
M 18,a[32:35],50
s_cselect_b32 s94,0x3300,0
R 74,1,10240
v_and_b32 v113,0xffff0000,v129
M 18,a[40:43],54
L 3
D 17,56,2048
M 18,a[48:51],58
s_add_u32 s56,s55,768
R 78,1,11264
v_fma_f32 v106,v106,v103,v102
M 18,a[56:59],62
D 17,56,3072
M 22,a[52:55],58
s_add_u32 m0,s57,16384
R 82,1,12288
v_fma_f32 v107,v107,v103,v102
M 22,a[60:63],62
L 3
v_fma_f32 v108,v108,v103,v102
v_fma_f32 v109,v109,v103,v102
M 22,a[68:71],66
s_add_u32 s56,s55,20480
R 86,1,13312
v_fma_f32 v110,v110,v103,v102
M 22,a[76:79],70
buffer_load_dwordx4 v[122:125],v0,s[36:39],s74 offen offset:0 sc1
M 18,a[64:67],66
s_cmp_eq_u32 s79,127
R 90,1,14336
v_fma_f32 v111,v111,v103,v102
M 18,a[72:75],70
L 3
buffer_load_dwordx2 v[98:99],v11,s[36:39],s73 offen offset:256 sc1
M 18,a[80:83],74
s_waitcnt vmcnt(10)
R 94,1,15360
v_fma_f32 v112,v112,v103,v102
M 18,a[88:91],78
s_barrier
v_fma_f32 v113,v113,v103,v102
v_add_f32 v26,v26,v106
M 22,a[84:87],74
s_cselect_b32 s94,0xcc00,s94
R 34,1,16384
v_add_f32 v27,v27,v107
M 22,a[92:95],78
L 3
v_add_f32 v28,v28,v108
v_add_f32 v29,v29,v109
M 22,a[100:103],82
s_mov_b32 s66,s65
R 38,1,17408
v_add_f32 v30,v30,v110
M 22,a[108:111],86
s_mov_b32 s76,s75
v_cndmask_b32 v17,v10,v9,s[40:41]
v_add_f32 v31,v31,v111
M 18,a[96:99],82
s_add_u32 s73,s73,s64
R 42,1,18432
v_add_f32 v32,v32,v112
M 18,a[104:107],86
L 3
D 17,56,0
M 18,a[112:115],90
s_add_u32 s56,s55,20736
R 46,1,19456
v_add_f32 v33,v33,v113
M 18,a[120:123],94
s_add_u32 s74,s74,s62
v_cvt_pk_bf16_f32 v114,v26,v27
)"
R"(v_cvt_pk_bf16_f32 v115,v28,v29
M 22,a[116:119],90
s_add_u32 s65,s65,s62
R 50,1,20480
v_fma_f32 v107,v26,v26,s87
M 22,a[124:127],94
L 3
v_add_f32 v106,v26,v27
v_fma_f32 v107,v27,v27,v107
M 22,a[132:135],34
s_add_u32 s75,s75,s63
R 54,1,21504
v_add_f32 v106,v106,v28
M 22,a[140:143],38
D 17,56,1024
M 18,a[128:131],34
s_add_u32 s56,s55,20992
R 58,1,22528
v_fma_f32 v107,v28,v28,v107
M 18,a[136:139],38
L 3
v_add_f32 v106,v106,v29
v_fma_f32 v107,v29,v29,v107
M 18,a[144:147],42
s_sub_u32 s57,s84,s57
R 62,1,23552
v_add_f32 v106,v106,v30
M 18,a[152:155],46
v_fma_f32 v107,v30,v30,v107
v_add_f32 v106,v106,v31
M 22,a[148:151],42
R 66,1,24576
v_fma_f32 v107,v31,v31,v107
M 22,a[156:159],46
L 3
v_add_f32 v106,v106,v32
v_fma_f32 v107,v32,v32,v107
M 22,a[164:167],50
R 70,1,25600
v_add_f32 v106,v106,v33
M 22,a[172:175],54
v_fma_f32 v107,v33,v33,v107
v_cvt_pk_bf16_f32 v116,v30,v31
M 18,a[160:163],50
R 74,1,26624
v_cvt_pk_bf16_f32 v117,v32,v33
M 18,a[168:171],54
L 3
v_permlane16_swap_b32 v106,v107
v_add_f32 v106,v106,v107
v_mov_b32 v107,v106
M 18,a[176:179],58
R 78,1,27648
M 18,a[184:187],62
v_permlane32_swap_b32 v106,v107
v_add_f32 v106,v106,v107
M 22,a[180:183],58
R 82,1,28672
ds_write_b32 v12,v106 offset:2048
M 22,a[188:191],62
L 4
D 17,56,2048
M 22,a[196:199],66
s_add_u32 s56,s55,21248
s_add_u32 s55,s55,s60
R 86,1,29696
M 22,a[204:207],70
D 17,56,3072
M 18,a[192:195],66
R 90,1,30720
M 18,a[200:203],70
L 4
L 2
buffer_store_dwordx4 v[114:117],v0,s[36:39],s67 offen offset:0 sc0 sc1
M 18,a[208:211],74
s_waitcnt vmcnt(7)
R 94,1,31744
M 18,a[216:219],78
s_barrier
ds_read_b32 v7,v6 offset:0
R 118,13,2048
M 22,a[212:215],74
R 34,2,0
M 22,a[220:223],78
L 5
buffer_store_dword v4,v3,s[36:39],s70 offen offset:0 sc0 sc1
M 22,a[228:231],82
s_mov_b32 s70,s69
s_mov_b32 s69,s68
R 38,2,1024
M 22,a[236:239],86
s_add_u32 s68,s68,s63
M 18,a[224:227],82
R 42,2,2048
M 18,a[232:235],86
L 5
L 4
v_cmp_eq_u32 vcc,s53,v7
M 18,a[240:243],90
L 3
R 46,2,3072
v_add_f32 v118,v118,v119
M 18,a[248:251],94
s_or_b64 vcc,vcc,s[94:95]
v_add_f32 v120,v120,v121
v_add_f32 v118,v118,v120
M 22,a[244:247],90
s_cmp_eq_u32 vcc_lo,-1
R 50,2,4096
v_mul_f32 v118,s86,v118
M 22,a[252:255],94
s_waitcnt vmcnt(6) lgkmcnt(3)
buffer_store_dword v118,v14,s[36:39],s77 offen offset:0 sc0 sc1
s_cbranch_scc0 Lg2_K1_%=
Lg2_F1_%=:
v_mfma_f32_16x16x32_bf16 v[30:33],a[4:7],v[34:37],0
s_add_u32 s58,s58,s61
s_add_u32 m0,s59,1024
s_and_b32 s78,s54,127
R 54,2,5120
M 30,a[12:15],38
s_cmp_eq_u32 s78,s85
s_cselect_b64 s[40:41],-1,0
s_add_u32 s56,s55,0
buffer_load_dword v8,s[36:39],s58 offen offset:0 sc1 lds
v_mfma_f32_16x16x32_bf16 v[26:29],a[0:3],v[34:37],0
s_add_u32 m0,s57,0
R 58,2,6144
v_cndmask_b32 v17,v15,v16,s[40:41]
M 26,a[8:11],38
L 3
D 17,56,0
M 26,a[16:19],42
s_add_u32 s56,s55,256
R 62,2,7168
v_lshlrev_b32 v106,16,v122
M 26,a[24:27],46
s_add_u32 s54,s54,s46
v_and_b32 v107,0xffff0000,v122
v_lshlrev_b32 v108,16,v123
M 30,a[20:23],42
s_and_b32 s79,s54,127
R 66,2,8192
v_and_b32 v109,0xffff0000,v123
M 30,a[28:31],46
L 3
D 17,56,1024
M 30,a[36:39],50
s_add_u32 s56,s55,512
R 70,2,9216
v_lshlrev_b32 v110,16,v124
M 30,a[44:47],54
s_cmp_eq_u32 s79,0
v_and_b32 v111,0xffff0000,v124
v_lshlrev_b32 v112,16,v125
M 26,a[32:35],50
s_cselect_b32 s94,0x3300,0
R 74,2,10240
v_and_b32 v113,0xffff0000,v125
M 26,a[40:43],54
L 3
D 17,56,2048
M 26,a[48:51],58
s_add_u32 s56,s55,768
R 78,2,11264
v_fma_f32 v106,v106,v99,v98
M 26,a[56:59],62
D 17,56,3072
M 30,a[52:55],58
s_add_u32 m0,s57,16384
R 82,2,12288
v_fma_f32 v107,v107,v99,v98
M 30,a[60:63],62
L 3
v_fma_f32 v108,v108,v99,v98
v_fma_f32 v109,v109,v99,v98
M 30,a[68:71],66
s_add_u32 s56,s55,20480
R 86,2,13312
v_fma_f32 v110,v110,v99,v98
M 30,a[76:79],70
buffer_load_dwordx4 v[126:129],v0,s[36:39],s74 offen offset:0 sc1
M 26,a[64:67],66
s_cmp_eq_u32 s79,127
R 90,2,14336
v_fma_f32 v111,v111,v99,v98
M 26,a[72:75],70
L 3
buffer_load_dwordx2 v[102:103],v11,s[36:39],s73 offen offset:256 sc1
M 26,a[80:83],74
s_waitcnt vmcnt(10)
R 94,2,15360
v_fma_f32 v112,v112,v99,v98
M 26,a[88:91],78
s_barrier
v_fma_f32 v113,v113,v99,v98
v_add_f32 v18,v18,v106
M 30,a[84:87],74
s_cselect_b32 s94,0xcc00,s94
R 34,2,16384
v_add_f32 v19,v19,v107
M 30,a[92:95],78
L 3
v_add_f32 v20,v20,v108
v_add_f32 v21,v21,v109
M 30,a[100:103],82
s_mov_b32 s67,s65
R 38,2,17408
v_add_f32 v22,v22,v110
M 30,a[108:111],86
s_mov_b32 s77,s75
v_cndmask_b32 v17,v10,v9,s[40:41]
v_add_f32 v23,v23,v111
M 26,a[96:99],82
s_add_u32 s73,s73,s64
R 42,2,18432
v_add_f32 v24,v24,v112
M 26,a[104:107],86
L 3
D 17,56,0
M 26,a[112:115],90
s_add_u32 s56,s55,20736
R 46,2,19456
v_add_f32 v25,v25,v113
M 26,a[120:123],94
s_add_u32 s74,s74,s62
v_cvt_pk_bf16_f32 v114,v18,v19
v_cvt_pk_bf16_f32 v115,v20,v21
M 30,a[116:119],90
s_add_u32 s65,s65,s62
R 50,2,20480
v_fma_f32 v107,v18,v18,s87
M 30,a[124:127],94
L 3
v_add_f32 v106,v18,v19
v_fma_f32 v107,v19,v19,v107
M 30,a[132:135],34
s_add_u32 s75,s75,s63
R 54,2,21504
v_add_f32 v106,v106,v20
M 30,a[140:143],38
D 17,56,1024
M 26,a[128:131],34
s_add_u32 s56,s55,20992
R 58,2,22528
v_fma_f32 v107,v20,v20,v107
M 26,a[136:139],38
L 3
v_add_f32 v106,v106,v21
v_fma_f32 v107,v21,v21,v107
M 26,a[144:147],42
s_sub_u32 s57,s84,s57
R 62,2,23552
v_add_f32 v106,v106,v22
M 26,a[152:155],46
v_fma_f32 v107,v22,v22,v107
v_add_f32 v106,v106,v23
M 30,a[148:151],42
R 66,2,24576
v_fma_f32 v107,v23,v23,v107
M 30,a[156:159],46
L 3
v_add_f32 v106,v106,v24
v_fma_f32 v107,v24,v24,v107
M 30,a[164:167],50
R 70,2,25600
v_add_f32 v106,v106,v25
M 30,a[172:175],54
v_fma_f32 v107,v25,v25,v107
v_cvt_pk_bf16_f32 v116,v22,v23
M 26,a[160:163],50
R 74,2,26624
v_cvt_pk_bf16_f32 v117,v24,v25
M 26,a[168:171],54
L 3
v_permlane16_swap_b32 v106,v107
v_add_f32 v106,v106,v107
v_mov_b32 v107,v106
M 26,a[176:179],58
R 78,2,27648
M 26,a[184:187],62
v_permlane32_swap_b32 v106,v107
v_add_f32 v106,v106,v107
M 30,a[180:183],58
R 82,2,28672
ds_write_b32 v12,v106 offset:0
M 30,a[188:191],62
L 4
D 17,56,2048
M 30,a[196:199],66
s_add_u32 s56,s55,21248
s_add_u32 s55,s55,s60
R 86,2,29696
M 30,a[204:207],70
D 17,56,3072
M 26,a[192:195],66
R 90,2,30720
M 26,a[200:203],70
L 4
L 2
buffer_store_dwordx4 v[114:117],v0,s[36:39],s66 offen offset:0 sc0 sc1
M 26,a[208:211],74
s_waitcnt vmcnt(7)
R 94,2,31744
M 26,a[216:219],78
s_barrier
ds_read_b32 v7,v6 offset:1024
R 118,13,0
M 30,a[212:215],74
R 34,1,0
M 30,a[220:223],78
L 5
buffer_store_dword v4,v3,s[36:39],s70 offen offset:0 sc0 sc1
M 30,a[228:231],82
s_mov_b32 s70,s69
s_mov_b32 s69,s68
R 38,1,1024
M 30,a[236:239],86
s_add_u32 s68,s68,s63
M 26,a[224:227],82
R 42,1,2048
M 26,a[232:235],86
L 5
L 4
v_cmp_eq_u32 vcc,s53,v7
M 26,a[240:243],90
L 3
R 46,1,3072
v_add_f32 v118,v118,v119
M 26,a[248:251],94
s_or_b64 vcc,vcc,s[94:95]
v_add_f32 v120,v120,v121
v_add_f32 v118,v118,v120
M 30,a[244:247],90
s_cmp_eq_u32 vcc_lo,-1
R 50,1,4096
v_mul_f32 v118,s86,v118
M 30,a[252:255],94
s_waitcnt vmcnt(6) lgkmcnt(3)
buffer_store_dword v118,v14,s[36:39],s76 offen offset:0 sc0 sc1
s_cbranch_scc1 Lg2_F0_%=
Lg2_K0_%=:
Lg2_L0_%=:
v_mfma_f32_16x16x32_bf16 v[22:25],a[4:7],v[34:37],0
s_add_u32 m0,s59,3072
R 54,1,5120
v_lshlrev_b32 v106,16,v126
M 22,a[12:15],38
buffer_load_dword v8,s[36:39],s58 offen offset:0 sc1 lds
v_mfma_f32_16x16x32_bf16 v[18:21],a[0:3],v[34:37],0
s_mov_b32 s66,s65
R 58,1,6144
v_and_b32 v107,0xffff0000,v126
M 18,a[8:11],38
L 3
v_lshlrev_b32 v108,16,v127
v_and_b32 v109,0xffff0000,v127
M 18,a[16:19],42
s_mov_b32 s76,s75
R 62,1,7168
v_lshlrev_b32 v110,16,v128
M 18,a[24:27],46
s_add_u32 s65,s65,s62
v_and_b32 v111,0xffff0000,v128
v_lshlrev_b32 v112,16,v129
M 22,a[20:23],42
s_add_u32 s75,s75,s63
R 66,1,8192
v_and_b32 v113,0xffff0000,v129
v_fma_f32 v106,v106,v103,v102
M 22,a[28:31],46
L 3
v_fma_f32 v107,v107,v103,v102
v_fma_f32 v108,v108,v103,v102
M 22,a[36:39],50
R 70,1,9216
v_fma_f32 v109,v109,v103,v102
M 22,a[44:47],54
v_fma_f32 v110,v110,v103,v102
v_fma_f32 v111,v111,v103,v102
M 18,a[32:35],50
R 74,1,10240
v_fma_f32 v112,v112,v103,v102
M 18,a[40:43],54
L 3
v_fma_f32 v113,v113,v103,v102
v_add_f32 v26,v26,v106
M 18,a[48:51],58
R 78,1,11264
v_add_f32 v27,v27,v107
M 18,a[56:59],62
v_add_f32 v28,v28,v108
v_add_f32 v29,v29,v109
v_add_f32 v30,v30,v110
M 22,a[52:55],58
R 82,1,12288
v_add_f32 v31,v31,v111
M 22,a[60:63],62
L 3
v_add_f32 v32,v32,v112
v_add_f32 v33,v33,v113
M 22,a[68:71],66
R 86,1,13312
v_cvt_pk_bf16_f32 v114,v26,v27
M 22,a[76:79],70
buffer_load_dwordx4 v[122:125],v0,s[36:39],s74 offen offset:0 sc1
M 18,a[64:67],66
s_add_u32 s74,s74,s62
R 90,1,14336
v_cvt_pk_bf16_f32 v115,v28,v29
M 18,a[72:75],70
L 3
buffer_load_dwordx2 v[98:99],v11,s[36:39],s73 offen offset:256 sc1
v_cvt_pk_bf16_f32 v116,v30,v31
M 18,a[80:83],74
s_waitcnt vmcnt(2)
s_add_u32 s73,s73,s64
R 94,1,15360
M 18,a[88:91],78
s_barrier
ds_read_b32 v7,v6 offset:3072
v_cvt_pk_bf16_f32 v117,v32,v33
M 22,a[84:87],74
R 34,1,16384
v_add_f32 v106,v26,v27
M 22,a[92:95],78
L 4
buffer_store_dword v4,v3,s[36:39],s70 offen offset:0 sc0 sc1
M 22,a[100:103],82
s_mov_b32 s70,0x7fff0000
R 38,1,17408
v_fma_f32 v107,v26,v26,s87
M 22,a[108:111],86
buffer_store_dwordx4 v[114:117],v0,s[36:39],s67 offen offset:0 sc0 sc1
v_fma_f32 v107,v27,v27,v107
M 18,a[96:99],82
R 42,1,18432
v_add_f32 v106,v106,v28
v_fma_f32 v107,v28,v28,v107
M 18,a[104:107],86
L 4
L 3
v_cmp_eq_u32 vcc,s53,v7
M 18,a[112:115],90
R 46,1,19456
v_add_f32 v106,v106,v29
M 18,a[120:123],94
s_or_b64 vcc,vcc,s[94:95]
v_fma_f32 v107,v29,v29,v107
v_add_f32 v106,v106,v30
M 22,a[116:119],90
R 50,1,20480
v_fma_f32 v107,v30,v30,v107
M 22,a[124:127],94
s_cmp_eq_u32 vcc_lo,-1
L 3
v_add_f32 v106,v106,v31
s_cbranch_scc0 Lg2_X0_%=
M 22,a[132:135],34
s_and_b32 s78,s54,127
s_cmp_eq_u32 s78,s85
s_cselect_b64 s[40:41],-1,0
R 54,1,21504
M 22,a[140:143],38
s_add_u32 m0,s57,0
s_add_u32 s56,s55,0
s_add_u32 s58,s58,s61
v_cndmask_b32 v17,v15,v16,s[40:41]
M 18,a[128:131],34
s_add_u32 s54,s54,s46
R 58,1,22528
v_fma_f32 v107,v31,v31,v107
M 18,a[136:139],38
L 3
D 17,56,0
M 18,a[144:147],42
s_add_u32 s56,s55,256
R 62,1,23552
v_add_f32 v106,v106,v32
M 18,a[152:155],46
s_and_b32 s79,s54,127
v_fma_f32 v107,v32,v32,v107
v_add_f32 v106,v106,v33
M 22,a[148:151],42
s_cmp_eq_u32 s79,0
R 66,1,24576
v_fma_f32 v107,v33,v33,v107
M 22,a[156:159],46
L 3
D 17,56,1024
M 22,a[164:167],50
s_cselect_b32 s94,0x3300,0
s_add_u32 s56,s55,512
R 70,1,25600
M 22,a[172:175],54
s_cmp_eq_u32 s79,127
v_permlane16_swap_b32 v106,v107
v_add_f32 v106,v106,v107
M 18,a[160:163],50
s_cselect_b32 s94,0xcc00,s94
)"
R"(R 74,1,26624
v_mov_b32 v107,v106
M 18,a[168:171],54
L 3
D 17,56,2048
M 18,a[176:179],58
s_add_u32 s56,s55,768
R 78,1,27648
M 18,a[184:187],62
v_permlane32_swap_b32 v106,v107
v_add_f32 v106,v106,v107
M 22,a[180:183],58
R 82,1,28672
ds_write_b32 v12,v106 offset:2048
M 22,a[188:191],62
L 4
D 17,56,3072
M 22,a[196:199],66
s_add_u32 m0,s59,0
s_add_u32 s56,s55,20480
R 86,1,29696
M 22,a[204:207],70
buffer_load_dword v8,s[36:39],s58 offen offset:0 sc1 lds
M 18,a[192:195],66
s_add_u32 m0,s57,16384
R 90,1,30720
v_cndmask_b32 v17,v10,v9,s[40:41]
M 18,a[200:203],70
L 4
L 2
s_sub_u32 s57,s84,s57
M 18,a[208:211],74
s_waitcnt vmcnt(0)
R 94,1,31744
M 18,a[216:219],78
s_barrier
D 17,56,0
M 22,a[212:215],74
s_add_u32 s56,s55,20736
R 34,2,0
ds_read_b32 v7,v6 offset:0
M 22,a[220:223],78
L 4
buffer_store_dword v4,v3,s[36:39],s70 offen offset:0 sc0 sc1
M 22,a[228:231],82
s_mov_b32 s70,s69
R 38,2,1024
R 118,13,2048
M 22,a[236:239],86
D 17,56,1024
M 18,a[224:227],82
s_add_u32 s56,s55,20992
s_mov_b32 s69,s68
R 42,2,2048
M 18,a[232:235],86
L 5
D 17,56,2048
M 18,a[240:243],90
L 3
s_add_u32 s56,s55,21248
R 46,2,3072
v_cmp_eq_u32 vcc,s53,v7
M 18,a[248:251],94
D 17,56,3072
M 22,a[244:247],90
s_add_u32 s55,s55,s60
s_add_u32 s68,s68,s63
L 2
s_or_b64 vcc,vcc,s[94:95]
s_cmp_eq_u32 vcc_lo,-1
R 50,2,4096
v_add_f32 v118,v118,v119
v_add_f32 v120,v120,v121
v_add_f32 v118,v118,v120
v_mul_f32 v118,s86,v118
M 22,a[252:255],94
s_nop 0
buffer_store_dword v118,v14,s[36:39],s77 offen offset:0 sc0 sc1
buffer_store_dword v4,v3,s[36:39],s71 offen offset:0 sc0 sc1
buffer_store_dword v4,v3,s[36:39],s71 offen offset:0 sc0 sc1
s_cbranch_scc1 Lg2_F1_%=
s_branch Lg2_K1_%=
Lg2_X0_%=:
M 22,a[132:135],34
s_add_u32 m0,s59,0
R 54,1,21504
v_fma_f32 v107,v31,v31,v107
M 22,a[140:143],38
buffer_load_dword v8,s[36:39],s58 offen offset:0 sc1 lds
M 18,a[128:131],34
R 58,1,22528
v_add_f32 v106,v106,v32
M 18,a[136:139],38
L 3
v_fma_f32 v107,v32,v32,v107
v_add_f32 v106,v106,v33
M 18,a[144:147],42
R 62,1,23552
v_fma_f32 v107,v33,v33,v107
M 18,a[152:155],46
M 22,a[148:151],42
R 66,1,24576
M 22,a[156:159],46
L 3
v_permlane16_swap_b32 v106,v107
v_add_f32 v106,v106,v107
M 22,a[164:167],50
R 70,1,25600
v_mov_b32 v107,v106
M 22,a[172:175],54
M 18,a[160:163],50
R 74,1,26624
M 18,a[168:171],54
L 3
v_permlane32_swap_b32 v106,v107
M 18,a[176:179],58
R 78,1,27648
v_add_f32 v106,v106,v107
M 18,a[184:187],62
ds_write_b32 v12,v106 offset:2048
M 22,a[180:183],58
R 82,1,28672
M 22,a[188:191],62
L 4
M 22,a[196:199],66
R 86,1,29696
M 22,a[204:207],70
M 18,a[192:195],66
R 90,1,30720
M 18,a[200:203],70
L 4
L 3
M 18,a[208:211],74
s_waitcnt vmcnt(0)
R 94,1,31744
M 18,a[216:219],78
s_barrier
R 118,13,2048
ds_read_b32 v7,v6 offset:0
M 22,a[212:215],74
buffer_store_dword v4,v3,s[36:39],s70 offen offset:0 sc0 sc1
M 22,a[220:223],78
L 4
s_mov_b32 s70,s69
s_mov_b32 s69,s68
M 22,a[228:231],82
s_add_u32 s68,s68,s63
M 22,a[236:239],86
M 18,a[224:227],82
M 18,a[232:235],86
L 2
L 1
L 0
M 18,a[240:243],90
v_add_f32 v118,v118,v119
v_add_f32 v120,v120,v121
v_add_f32 v118,v118,v120
M 18,a[248:251],94
v_cmp_eq_u32 vcc,s53,v7
v_mul_f32 v118,s86,v118
M 22,a[244:247],90
buffer_store_dword v118,v14,s[36:39],s77 offen offset:0 sc0 sc1
M 22,a[252:255],94
s_nop 0
s_or_b64 vcc,vcc,s[94:95]
s_mov_b32 s80,1
s_cmp_eq_u32 vcc_lo,-1
s_cbranch_scc0 Lg2_xn0_%=
s_and_b32 s78,s54,127
s_cmp_eq_u32 s78,s85
s_cselect_b64 s[40:41],-1,0
v_cndmask_b32 v17,v15,v16,s[40:41]
s_add_u32 m0,s57,0
s_add_u32 s56,s55,0
D 17,56,0
v_lshlrev_b32 v106,16,v122
v_and_b32 v107,0xffff0000,v122
v_lshlrev_b32 v108,16,v123
v_and_b32 v109,0xffff0000,v123
v_lshlrev_b32 v110,16,v124
s_add_u32 s56,s55,256
D 17,56,1024
v_and_b32 v111,0xffff0000,v124
v_lshlrev_b32 v112,16,v125
v_and_b32 v113,0xffff0000,v125
v_fma_f32 v106,v106,v99,v98
v_fma_f32 v107,v107,v99,v98
s_add_u32 s56,s55,512
D 17,56,2048
v_fma_f32 v108,v108,v99,v98
v_fma_f32 v109,v109,v99,v98
v_fma_f32 v110,v110,v99,v98
v_fma_f32 v111,v111,v99,v98
v_fma_f32 v112,v112,v99,v98
s_add_u32 s56,s55,768
D 17,56,3072
v_fma_f32 v113,v113,v99,v98
v_add_f32 v18,v18,v106
v_add_f32 v19,v19,v107
v_add_f32 v20,v20,v108
v_add_f32 v21,v21,v109
s_add_u32 s58,s58,s61
v_cndmask_b32 v17,v10,v9,s[40:41]
s_add_u32 m0,s57,16384
s_add_u32 s56,s55,20480
D 17,56,0
v_add_f32 v22,v22,v110
v_add_f32 v23,v23,v111
v_add_f32 v24,v24,v112
v_add_f32 v25,v25,v113
v_cvt_pk_bf16_f32 v114,v18,v19
s_add_u32 s56,s55,20736
D 17,56,1024
v_cvt_pk_bf16_f32 v115,v20,v21
v_cvt_pk_bf16_f32 v116,v22,v23
v_cvt_pk_bf16_f32 v117,v24,v25
s_add_u32 s56,s55,20992
D 17,56,2048
s_add_u32 s56,s55,21248
D 17,56,3072
buffer_store_dwordx4 v[114:117],v0,s[36:39],s66 offen offset:0 sc0 sc1
v_add_f32 v106,v18,v19
v_fma_f32 v107,v18,v18,s87
s_add_u32 s55,s55,s60
s_sub_u32 s57,s84,s57
s_add_u32 s54,s54,s46
v_fma_f32 v107,v19,v19,v107
v_add_f32 v106,v106,v20
v_fma_f32 v107,v20,v20,v107
v_add_f32 v106,v106,v21
v_fma_f32 v107,v21,v21,v107
v_add_f32 v106,v106,v22
v_fma_f32 v107,v22,v22,v107
v_add_f32 v106,v106,v23
v_fma_f32 v107,v23,v23,v107
v_add_f32 v106,v106,v24
v_fma_f32 v107,v24,v24,v107
v_add_f32 v106,v106,v25
v_fma_f32 v107,v25,v25,v107
s_nop 1
v_permlane16_swap_b32 v106,v107
v_add_f32 v106,v106,v107
v_mov_b32 v107,v106
s_nop 1
v_permlane32_swap_b32 v106,v107
v_add_f32 v106,v106,v107
ds_write_b32 v12,v106 offset:0
L 0
s_barrier
R 118,13,0
L 0
v_add_f32 v118,v118,v119
v_add_f32 v120,v120,v121
v_add_f32 v118,v118,v120
v_mul_f32 v118,s86,v118
buffer_store_dword v118,v14,s[36:39],s76 offen offset:0 sc0 sc1
s_mov_b32 s72,s69
s_mov_b32 s69,0x7fff0000
s_mov_b32 s66,0x7fff0000
s_mov_b32 s76,0x7fff0000
s_branch Lg2_gox_%=
Lg2_xn0_%=:
s_cmp_ge_u32 s54,256
s_cbranch_scc1 Lg2_fin0_%=
s_add_u32 m0,s59,4096
s_nop 0
buffer_load_dword v8,s[36:39],s58 offen offset:0 sc1 lds
buffer_store_dword v4,v3,s[36:39],s71 offen offset:0 sc0 sc1
s_add_u32 m0,s59,5120
s_nop 0
buffer_load_dword v8,s[36:39],s58 offen offset:0 sc1 lds
buffer_store_dword v4,v3,s[36:39],s71 offen offset:0 sc0 sc1
s_mov_b32 s83,0
v_lshlrev_b32 v106,16,v122
v_and_b32 v107,0xffff0000,v122
v_lshlrev_b32 v108,16,v123
v_and_b32 v109,0xffff0000,v123
v_lshlrev_b32 v110,16,v124
v_and_b32 v111,0xffff0000,v124
v_lshlrev_b32 v112,16,v125
v_and_b32 v113,0xffff0000,v125
v_fma_f32 v106,v106,v99,v98
v_fma_f32 v107,v107,v99,v98
v_fma_f32 v108,v108,v99,v98
v_fma_f32 v109,v109,v99,v98
v_fma_f32 v110,v110,v99,v98
v_fma_f32 v111,v111,v99,v98
v_fma_f32 v112,v112,v99,v98
v_fma_f32 v113,v113,v99,v98
v_add_f32 v18,v18,v106
v_add_f32 v19,v19,v107
v_add_f32 v20,v20,v108
v_add_f32 v21,v21,v109
v_add_f32 v22,v22,v110
v_add_f32 v23,v23,v111
v_add_f32 v24,v24,v112
v_add_f32 v25,v25,v113
v_cvt_pk_bf16_f32 v114,v18,v19
v_cvt_pk_bf16_f32 v115,v20,v21
v_cvt_pk_bf16_f32 v116,v22,v23
v_cvt_pk_bf16_f32 v117,v24,v25
buffer_store_dwordx4 v[114:117],v0,s[36:39],s66 offen offset:0 sc0 sc1
v_add_f32 v106,v18,v19
v_fma_f32 v107,v18,v18,s87
v_fma_f32 v107,v19,v19,v107
v_add_f32 v106,v106,v20
v_fma_f32 v107,v20,v20,v107
v_add_f32 v106,v106,v21
v_fma_f32 v107,v21,v21,v107
v_add_f32 v106,v106,v22
v_fma_f32 v107,v22,v22,v107
v_add_f32 v106,v106,v23
v_fma_f32 v107,v23,v23,v107
v_add_f32 v106,v106,v24
v_fma_f32 v107,v24,v24,v107
v_add_f32 v106,v106,v25
v_fma_f32 v107,v25,v25,v107
s_nop 1
v_permlane16_swap_b32 v106,v107
v_add_f32 v106,v106,v107
v_mov_b32 v107,v106
s_nop 1
v_permlane32_swap_b32 v106,v107
v_add_f32 v106,v106,v107
ds_write_b32 v12,v106 offset:0
L 0
s_barrier
R 118,13,0
L 0
v_add_f32 v118,v118,v119
v_add_f32 v120,v120,v121
v_add_f32 v118,v118,v120
v_mul_f32 v118,s86,v118
buffer_store_dword v118,v14,s[36:39],s76 offen offset:0 sc0 sc1
s_mov_b32 s72,s69
s_mov_b32 s69,0x7fff0000
s_mov_b32 s66,0x7fff0000
s_mov_b32 s76,0x7fff0000
s_branch Lg2_poll_%=
Lg2_fin0_%=:
v_lshlrev_b32 v106,16,v122
v_and_b32 v107,0xffff0000,v122
v_lshlrev_b32 v108,16,v123
v_and_b32 v109,0xffff0000,v123
v_lshlrev_b32 v110,16,v124
v_and_b32 v111,0xffff0000,v124
v_lshlrev_b32 v112,16,v125
v_and_b32 v113,0xffff0000,v125
v_fma_f32 v106,v106,v99,v98
v_fma_f32 v107,v107,v99,v98
v_fma_f32 v108,v108,v99,v98
v_fma_f32 v109,v109,v99,v98
v_fma_f32 v110,v110,v99,v98
v_fma_f32 v111,v111,v99,v98
v_fma_f32 v112,v112,v99,v98
v_fma_f32 v113,v113,v99,v98
v_add_f32 v18,v18,v106
v_add_f32 v19,v19,v107
v_add_f32 v20,v20,v108
v_add_f32 v21,v21,v109
v_add_f32 v22,v22,v110
v_add_f32 v23,v23,v111
v_add_f32 v24,v24,v112
v_add_f32 v25,v25,v113
v_cvt_pk_bf16_f32 v114,v18,v19
v_cvt_pk_bf16_f32 v115,v20,v21
v_cvt_pk_bf16_f32 v116,v22,v23
v_cvt_pk_bf16_f32 v117,v24,v25
buffer_store_dwordx4 v[114:117],v0,s[36:39],s66 offen offset:0 sc0 sc1
v_add_f32 v106,v18,v19
v_fma_f32 v107,v18,v18,s87
v_fma_f32 v107,v19,v19,v107
v_add_f32 v106,v106,v20
v_fma_f32 v107,v20,v20,v107
v_add_f32 v106,v106,v21
v_fma_f32 v107,v21,v21,v107
v_add_f32 v106,v106,v22
v_fma_f32 v107,v22,v22,v107
v_add_f32 v106,v106,v23
v_fma_f32 v107,v23,v23,v107
v_add_f32 v106,v106,v24
v_fma_f32 v107,v24,v24,v107
v_add_f32 v106,v106,v25
v_fma_f32 v107,v25,v25,v107
s_nop 1
v_permlane16_swap_b32 v106,v107
v_add_f32 v106,v106,v107
v_mov_b32 v107,v106
s_nop 1
v_permlane32_swap_b32 v106,v107
v_add_f32 v106,v106,v107
ds_write_b32 v12,v106 offset:0
L 0
s_barrier
R 118,13,0
L 0
v_add_f32 v118,v118,v119
v_add_f32 v120,v120,v121
v_add_f32 v118,v118,v120
v_mul_f32 v118,s86,v118
buffer_store_dword v118,v14,s[36:39],s76 offen offset:0 sc0 sc1
s_mov_b32 s72,s69
s_mov_b32 s69,0x7fff0000
s_mov_b32 s66,0x7fff0000
s_mov_b32 s76,0x7fff0000
s_branch Lg2_done_%=
Lg2_K1_%=:
Lg2_L1_%=:
v_mfma_f32_16x16x32_bf16 v[30:33],a[4:7],v[34:37],0
s_add_u32 m0,s59,3072
R 54,2,5120
v_lshlrev_b32 v106,16,v122
M 30,a[12:15],38
buffer_load_dword v8,s[36:39],s58 offen offset:0 sc1 lds
v_mfma_f32_16x16x32_bf16 v[26:29],a[0:3],v[34:37],0
s_mov_b32 s67,s65
R 58,2,6144
v_and_b32 v107,0xffff0000,v122
M 26,a[8:11],38
L 3
v_lshlrev_b32 v108,16,v123
v_and_b32 v109,0xffff0000,v123
M 26,a[16:19],42
s_mov_b32 s77,s75
R 62,2,7168
v_lshlrev_b32 v110,16,v124
M 26,a[24:27],46
s_add_u32 s65,s65,s62
v_and_b32 v111,0xffff0000,v124
v_lshlrev_b32 v112,16,v125
M 30,a[20:23],42
s_add_u32 s75,s75,s63
R 66,2,8192
v_and_b32 v113,0xffff0000,v125
v_fma_f32 v106,v106,v99,v98
M 30,a[28:31],46
L 3
v_fma_f32 v107,v107,v99,v98
v_fma_f32 v108,v108,v99,v98
M 30,a[36:39],50
R 70,2,9216
v_fma_f32 v109,v109,v99,v98
M 30,a[44:47],54
v_fma_f32 v110,v110,v99,v98
v_fma_f32 v111,v111,v99,v98
M 26,a[32:35],50
R 74,2,10240
v_fma_f32 v112,v112,v99,v98
M 26,a[40:43],54
L 3
v_fma_f32 v113,v113,v99,v98
v_add_f32 v18,v18,v106
M 26,a[48:51],58
R 78,2,11264
v_add_f32 v19,v19,v107
M 26,a[56:59],62
v_add_f32 v20,v20,v108
v_add_f32 v21,v21,v109
v_add_f32 v22,v22,v110
M 30,a[52:55],58
R 82,2,12288
v_add_f32 v23,v23,v111
M 30,a[60:63],62
L 3
v_add_f32 v24,v24,v112
v_add_f32 v25,v25,v113
M 30,a[68:71],66
R 86,2,13312
v_cvt_pk_bf16_f32 v114,v18,v19
M 30,a[76:79],70
buffer_load_dwordx4 v[126:129],v0,s[36:39],s74 offen offset:0 sc1
M 26,a[64:67],66
s_add_u32 s74,s74,s62
R 90,2,14336
v_cvt_pk_bf16_f32 v115,v20,v21
M 26,a[72:75],70
L 3
buffer_load_dwordx2 v[102:103],v11,s[36:39],s73 offen offset:256 sc1
v_cvt_pk_bf16_f32 v116,v22,v23
M 26,a[80:83],74
s_waitcnt vmcnt(2)
s_add_u32 s73,s73,s64
R 94,2,15360
M 26,a[88:91],78
s_barrier
ds_read_b32 v7,v6 offset:3072
v_cvt_pk_bf16_f32 v117,v24,v25
M 30,a[84:87],74
R 34,2,16384
v_add_f32 v106,v18,v19
M 30,a[92:95],78
L 4
buffer_store_dword v4,v3,s[36:39],s70 offen offset:0 sc0 sc1
M 30,a[100:103],82
s_mov_b32 s70,0x7fff0000
R 38,2,17408
v_fma_f32 v107,v18,v18,s87
M 30,a[108:111],86
buffer_store_dwordx4 v[114:117],v0,s[36:39],s66 offen offset:0 sc0 sc1
v_fma_f32 v107,v19,v19,v107
)"
R"(M 26,a[96:99],82
R 42,2,18432
v_add_f32 v106,v106,v20
v_fma_f32 v107,v20,v20,v107
M 26,a[104:107],86
L 4
L 3
v_cmp_eq_u32 vcc,s53,v7
M 26,a[112:115],90
R 46,2,19456
v_add_f32 v106,v106,v21
M 26,a[120:123],94
s_or_b64 vcc,vcc,s[94:95]
v_fma_f32 v107,v21,v21,v107
v_add_f32 v106,v106,v22
M 30,a[116:119],90
R 50,2,20480
v_fma_f32 v107,v22,v22,v107
M 30,a[124:127],94
s_cmp_eq_u32 vcc_lo,-1
L 3
v_add_f32 v106,v106,v23
s_cbranch_scc0 Lg2_X1_%=
M 30,a[132:135],34
s_and_b32 s78,s54,127
s_cmp_eq_u32 s78,s85
s_cselect_b64 s[40:41],-1,0
R 54,2,21504
M 30,a[140:143],38
s_add_u32 m0,s57,0
s_add_u32 s56,s55,0
s_add_u32 s58,s58,s61
v_cndmask_b32 v17,v15,v16,s[40:41]
M 26,a[128:131],34
s_add_u32 s54,s54,s46
R 58,2,22528
v_fma_f32 v107,v23,v23,v107
M 26,a[136:139],38
L 3
D 17,56,0
M 26,a[144:147],42
s_add_u32 s56,s55,256
R 62,2,23552
v_add_f32 v106,v106,v24
M 26,a[152:155],46
s_and_b32 s79,s54,127
v_fma_f32 v107,v24,v24,v107
v_add_f32 v106,v106,v25
M 30,a[148:151],42
s_cmp_eq_u32 s79,0
R 66,2,24576
v_fma_f32 v107,v25,v25,v107
M 30,a[156:159],46
L 3
D 17,56,1024
M 30,a[164:167],50
s_cselect_b32 s94,0x3300,0
s_add_u32 s56,s55,512
R 70,2,25600
M 30,a[172:175],54
s_cmp_eq_u32 s79,127
v_permlane16_swap_b32 v106,v107
v_add_f32 v106,v106,v107
M 26,a[160:163],50
s_cselect_b32 s94,0xcc00,s94
R 74,2,26624
v_mov_b32 v107,v106
M 26,a[168:171],54
L 3
D 17,56,2048
M 26,a[176:179],58
s_add_u32 s56,s55,768
R 78,2,27648
M 26,a[184:187],62
v_permlane32_swap_b32 v106,v107
v_add_f32 v106,v106,v107
M 30,a[180:183],58
R 82,2,28672
ds_write_b32 v12,v106 offset:0
M 30,a[188:191],62
L 4
D 17,56,3072
M 30,a[196:199],66
s_add_u32 m0,s59,1024
s_add_u32 s56,s55,20480
R 86,2,29696
M 30,a[204:207],70
buffer_load_dword v8,s[36:39],s58 offen offset:0 sc1 lds
M 26,a[192:195],66
s_add_u32 m0,s57,16384
R 90,2,30720
v_cndmask_b32 v17,v10,v9,s[40:41]
M 26,a[200:203],70
L 4
L 2
s_sub_u32 s57,s84,s57
M 26,a[208:211],74
s_waitcnt vmcnt(0)
R 94,2,31744
M 26,a[216:219],78
s_barrier
D 17,56,0
M 30,a[212:215],74
s_add_u32 s56,s55,20736
R 34,1,0
ds_read_b32 v7,v6 offset:1024
M 30,a[220:223],78
L 4
buffer_store_dword v4,v3,s[36:39],s70 offen offset:0 sc0 sc1
M 30,a[228:231],82
s_mov_b32 s70,s69
R 38,1,1024
R 118,13,0
M 30,a[236:239],86
D 17,56,1024
M 26,a[224:227],82
s_add_u32 s56,s55,20992
s_mov_b32 s69,s68
R 42,1,2048
M 26,a[232:235],86
L 5
D 17,56,2048
M 26,a[240:243],90
L 3
s_add_u32 s56,s55,21248
R 46,1,3072
v_cmp_eq_u32 vcc,s53,v7
M 26,a[248:251],94
D 17,56,3072
M 30,a[244:247],90
s_add_u32 s55,s55,s60
s_add_u32 s68,s68,s63
L 2
s_or_b64 vcc,vcc,s[94:95]
s_cmp_eq_u32 vcc_lo,-1
R 50,1,4096
v_add_f32 v118,v118,v119
v_add_f32 v120,v120,v121
v_add_f32 v118,v118,v120
v_mul_f32 v118,s86,v118
M 30,a[252:255],94
s_nop 0
buffer_store_dword v118,v14,s[36:39],s76 offen offset:0 sc0 sc1
buffer_store_dword v4,v3,s[36:39],s71 offen offset:0 sc0 sc1
buffer_store_dword v4,v3,s[36:39],s71 offen offset:0 sc0 sc1
s_cbranch_scc1 Lg2_F0_%=
s_branch Lg2_K0_%=
Lg2_X1_%=:
M 30,a[132:135],34
s_add_u32 m0,s59,1024
R 54,2,21504
v_fma_f32 v107,v23,v23,v107
M 30,a[140:143],38
buffer_load_dword v8,s[36:39],s58 offen offset:0 sc1 lds
M 26,a[128:131],34
R 58,2,22528
v_add_f32 v106,v106,v24
M 26,a[136:139],38
L 3
v_fma_f32 v107,v24,v24,v107
v_add_f32 v106,v106,v25
M 26,a[144:147],42
R 62,2,23552
v_fma_f32 v107,v25,v25,v107
M 26,a[152:155],46
M 30,a[148:151],42
R 66,2,24576
M 30,a[156:159],46
L 3
v_permlane16_swap_b32 v106,v107
v_add_f32 v106,v106,v107
M 30,a[164:167],50
R 70,2,25600
v_mov_b32 v107,v106
M 30,a[172:175],54
M 26,a[160:163],50
R 74,2,26624
M 26,a[168:171],54
L 3
v_permlane32_swap_b32 v106,v107
M 26,a[176:179],58
R 78,2,27648
v_add_f32 v106,v106,v107
M 26,a[184:187],62
ds_write_b32 v12,v106 offset:0
M 30,a[180:183],58
R 82,2,28672
M 30,a[188:191],62
L 4
M 30,a[196:199],66
R 86,2,29696
M 30,a[204:207],70
M 26,a[192:195],66
R 90,2,30720
M 26,a[200:203],70
L 4
L 3
M 26,a[208:211],74
s_waitcnt vmcnt(0)
R 94,2,31744
M 26,a[216:219],78
s_barrier
R 118,13,0
ds_read_b32 v7,v6 offset:1024
M 30,a[212:215],74
buffer_store_dword v4,v3,s[36:39],s70 offen offset:0 sc0 sc1
M 30,a[220:223],78
L 4
s_mov_b32 s70,s69
s_mov_b32 s69,s68
M 30,a[228:231],82
s_add_u32 s68,s68,s63
M 30,a[236:239],86
M 26,a[224:227],82
M 26,a[232:235],86
L 2
L 1
L 0
M 26,a[240:243],90
v_add_f32 v118,v118,v119
v_add_f32 v120,v120,v121
v_add_f32 v118,v118,v120
M 26,a[248:251],94
v_cmp_eq_u32 vcc,s53,v7
v_mul_f32 v118,s86,v118
M 30,a[244:247],90
buffer_store_dword v118,v14,s[36:39],s76 offen offset:0 sc0 sc1
M 30,a[252:255],94
s_nop 0
s_or_b64 vcc,vcc,s[94:95]
s_mov_b32 s80,0
s_cmp_eq_u32 vcc_lo,-1
s_cbranch_scc0 Lg2_xn1_%=
s_and_b32 s78,s54,127
s_cmp_eq_u32 s78,s85
s_cselect_b64 s[40:41],-1,0
v_cndmask_b32 v17,v15,v16,s[40:41]
s_add_u32 m0,s57,0
s_add_u32 s56,s55,0
D 17,56,0
v_lshlrev_b32 v106,16,v126
v_and_b32 v107,0xffff0000,v126
v_lshlrev_b32 v108,16,v127
v_and_b32 v109,0xffff0000,v127
v_lshlrev_b32 v110,16,v128
s_add_u32 s56,s55,256
D 17,56,1024
v_and_b32 v111,0xffff0000,v128
v_lshlrev_b32 v112,16,v129
v_and_b32 v113,0xffff0000,v129
v_fma_f32 v106,v106,v103,v102
v_fma_f32 v107,v107,v103,v102
s_add_u32 s56,s55,512
D 17,56,2048
v_fma_f32 v108,v108,v103,v102
v_fma_f32 v109,v109,v103,v102
v_fma_f32 v110,v110,v103,v102
v_fma_f32 v111,v111,v103,v102
v_fma_f32 v112,v112,v103,v102
s_add_u32 s56,s55,768
D 17,56,3072
v_fma_f32 v113,v113,v103,v102
v_add_f32 v26,v26,v106
v_add_f32 v27,v27,v107
v_add_f32 v28,v28,v108
v_add_f32 v29,v29,v109
s_add_u32 s58,s58,s61
v_cndmask_b32 v17,v10,v9,s[40:41]
s_add_u32 m0,s57,16384
s_add_u32 s56,s55,20480
D 17,56,0
v_add_f32 v30,v30,v110
v_add_f32 v31,v31,v111
v_add_f32 v32,v32,v112
v_add_f32 v33,v33,v113
v_cvt_pk_bf16_f32 v114,v26,v27
s_add_u32 s56,s55,20736
D 17,56,1024
v_cvt_pk_bf16_f32 v115,v28,v29
v_cvt_pk_bf16_f32 v116,v30,v31
v_cvt_pk_bf16_f32 v117,v32,v33
s_add_u32 s56,s55,20992
D 17,56,2048
s_add_u32 s56,s55,21248
D 17,56,3072
buffer_store_dwordx4 v[114:117],v0,s[36:39],s67 offen offset:0 sc0 sc1
v_add_f32 v106,v26,v27
v_fma_f32 v107,v26,v26,s87
s_add_u32 s55,s55,s60
s_sub_u32 s57,s84,s57
s_add_u32 s54,s54,s46
v_fma_f32 v107,v27,v27,v107
v_add_f32 v106,v106,v28
v_fma_f32 v107,v28,v28,v107
v_add_f32 v106,v106,v29
v_fma_f32 v107,v29,v29,v107
v_add_f32 v106,v106,v30
v_fma_f32 v107,v30,v30,v107
v_add_f32 v106,v106,v31
v_fma_f32 v107,v31,v31,v107
v_add_f32 v106,v106,v32
v_fma_f32 v107,v32,v32,v107
v_add_f32 v106,v106,v33
v_fma_f32 v107,v33,v33,v107
s_nop 1
v_permlane16_swap_b32 v106,v107
v_add_f32 v106,v106,v107
v_mov_b32 v107,v106
s_nop 1
v_permlane32_swap_b32 v106,v107
v_add_f32 v106,v106,v107
ds_write_b32 v12,v106 offset:2048
L 0
s_barrier
R 118,13,2048
L 0
v_add_f32 v118,v118,v119
v_add_f32 v120,v120,v121
v_add_f32 v118,v118,v120
v_mul_f32 v118,s86,v118
buffer_store_dword v118,v14,s[36:39],s77 offen offset:0 sc0 sc1
s_mov_b32 s72,s69
s_mov_b32 s69,0x7fff0000
s_mov_b32 s67,0x7fff0000
s_mov_b32 s77,0x7fff0000
s_branch Lg2_gox_%=
Lg2_xn1_%=:
s_cmp_ge_u32 s54,256
s_cbranch_scc1 Lg2_fin1_%=
s_add_u32 m0,s59,4096
s_nop 0
buffer_load_dword v8,s[36:39],s58 offen offset:0 sc1 lds
buffer_store_dword v4,v3,s[36:39],s71 offen offset:0 sc0 sc1
s_add_u32 m0,s59,5120
s_nop 0
buffer_load_dword v8,s[36:39],s58 offen offset:0 sc1 lds
buffer_store_dword v4,v3,s[36:39],s71 offen offset:0 sc0 sc1
s_mov_b32 s83,0
v_lshlrev_b32 v106,16,v126
v_and_b32 v107,0xffff0000,v126
v_lshlrev_b32 v108,16,v127
v_and_b32 v109,0xffff0000,v127
v_lshlrev_b32 v110,16,v128
v_and_b32 v111,0xffff0000,v128
v_lshlrev_b32 v112,16,v129
v_and_b32 v113,0xffff0000,v129
v_fma_f32 v106,v106,v103,v102
v_fma_f32 v107,v107,v103,v102
v_fma_f32 v108,v108,v103,v102
v_fma_f32 v109,v109,v103,v102
v_fma_f32 v110,v110,v103,v102
v_fma_f32 v111,v111,v103,v102
v_fma_f32 v112,v112,v103,v102
v_fma_f32 v113,v113,v103,v102
v_add_f32 v26,v26,v106
v_add_f32 v27,v27,v107
v_add_f32 v28,v28,v108
v_add_f32 v29,v29,v109
v_add_f32 v30,v30,v110
v_add_f32 v31,v31,v111
v_add_f32 v32,v32,v112
v_add_f32 v33,v33,v113
v_cvt_pk_bf16_f32 v114,v26,v27
v_cvt_pk_bf16_f32 v115,v28,v29
v_cvt_pk_bf16_f32 v116,v30,v31
v_cvt_pk_bf16_f32 v117,v32,v33
buffer_store_dwordx4 v[114:117],v0,s[36:39],s67 offen offset:0 sc0 sc1
v_add_f32 v106,v26,v27
v_fma_f32 v107,v26,v26,s87
v_fma_f32 v107,v27,v27,v107
v_add_f32 v106,v106,v28
v_fma_f32 v107,v28,v28,v107
v_add_f32 v106,v106,v29
v_fma_f32 v107,v29,v29,v107
v_add_f32 v106,v106,v30
v_fma_f32 v107,v30,v30,v107
v_add_f32 v106,v106,v31
v_fma_f32 v107,v31,v31,v107
v_add_f32 v106,v106,v32
v_fma_f32 v107,v32,v32,v107
v_add_f32 v106,v106,v33
v_fma_f32 v107,v33,v33,v107
s_nop 1
v_permlane16_swap_b32 v106,v107
v_add_f32 v106,v106,v107
v_mov_b32 v107,v106
s_nop 1
v_permlane32_swap_b32 v106,v107
v_add_f32 v106,v106,v107
ds_write_b32 v12,v106 offset:2048
L 0
s_barrier
R 118,13,2048
L 0
v_add_f32 v118,v118,v119
v_add_f32 v120,v120,v121
v_add_f32 v118,v118,v120
v_mul_f32 v118,s86,v118
buffer_store_dword v118,v14,s[36:39],s77 offen offset:0 sc0 sc1
s_mov_b32 s72,s69
s_mov_b32 s69,0x7fff0000
s_mov_b32 s67,0x7fff0000
s_mov_b32 s77,0x7fff0000
s_branch Lg2_poll_%=
Lg2_fin1_%=:
v_lshlrev_b32 v106,16,v126
v_and_b32 v107,0xffff0000,v126
v_lshlrev_b32 v108,16,v127
v_and_b32 v109,0xffff0000,v127
v_lshlrev_b32 v110,16,v128
v_and_b32 v111,0xffff0000,v128
v_lshlrev_b32 v112,16,v129
v_and_b32 v113,0xffff0000,v129
v_fma_f32 v106,v106,v103,v102
v_fma_f32 v107,v107,v103,v102
v_fma_f32 v108,v108,v103,v102
v_fma_f32 v109,v109,v103,v102
v_fma_f32 v110,v110,v103,v102
v_fma_f32 v111,v111,v103,v102
v_fma_f32 v112,v112,v103,v102
v_fma_f32 v113,v113,v103,v102
v_add_f32 v26,v26,v106
v_add_f32 v27,v27,v107
v_add_f32 v28,v28,v108
v_add_f32 v29,v29,v109
v_add_f32 v30,v30,v110
v_add_f32 v31,v31,v111
v_add_f32 v32,v32,v112
v_add_f32 v33,v33,v113
v_cvt_pk_bf16_f32 v114,v26,v27
v_cvt_pk_bf16_f32 v115,v28,v29
v_cvt_pk_bf16_f32 v116,v30,v31
v_cvt_pk_bf16_f32 v117,v32,v33
buffer_store_dwordx4 v[114:117],v0,s[36:39],s67 offen offset:0 sc0 sc1
v_add_f32 v106,v26,v27
v_fma_f32 v107,v26,v26,s87
v_fma_f32 v107,v27,v27,v107
v_add_f32 v106,v106,v28
v_fma_f32 v107,v28,v28,v107
v_add_f32 v106,v106,v29
v_fma_f32 v107,v29,v29,v107
v_add_f32 v106,v106,v30
v_fma_f32 v107,v30,v30,v107
v_add_f32 v106,v106,v31
v_fma_f32 v107,v31,v31,v107
v_add_f32 v106,v106,v32
v_fma_f32 v107,v32,v32,v107
v_add_f32 v106,v106,v33
v_fma_f32 v107,v33,v33,v107
s_nop 1
v_permlane16_swap_b32 v106,v107
v_add_f32 v106,v106,v107
v_mov_b32 v107,v106
s_nop 1
v_permlane32_swap_b32 v106,v107
v_add_f32 v106,v106,v107
ds_write_b32 v12,v106 offset:2048
L 0
s_barrier
R 118,13,2048
L 0
v_add_f32 v118,v118,v119
v_add_f32 v120,v120,v121
v_add_f32 v118,v118,v120
v_mul_f32 v118,s86,v118
buffer_store_dword v118,v14,s[36:39],s77 offen offset:0 sc0 sc1
s_mov_b32 s72,s69
s_mov_b32 s69,0x7fff0000
s_mov_b32 s67,0x7fff0000
s_mov_b32 s77,0x7fff0000
s_branch Lg2_done_%=
Lg2_done_%=:
s_waitcnt vmcnt(2)
s_barrier
buffer_store_dword v4,v3,s[36:39],s70 offen offset:0 sc0 sc1
s_waitcnt vmcnt(1)
s_barrier
buffer_store_dword v4,v3,s[36:39],s72 offen offset:0 sc0 sc1
s_waitcnt vmcnt(0) lgkmcnt(0)
s_barrier
Lg2_end_%=:
s_mov_b32 %0,s53
Lcode_g12_e:
.purgem M
.purgem R
.purgem T
.purgem D
.purgem L
)"
  : "=&s"(epOut) : "s"(ws), "s"(__builtin_amdgcn_readfirstlane(sWave)), "s"(__builtin_amdgcn_readfirstlane(sG0)), "s"(__builtin_amdgcn_readfirstlane(sStep)), "s"(__builtin_amdgcn_readfirstlane(sOutOff)), "s"(__builtin_amdgcn_readfirstlane(sFoutOff)), "s"(__builtin_amdgcn_readfirstlane(sAuxOff)), "s"(__builtin_amdgcn_readfirstlane(sWw)), "s"(__builtin_amdgcn_readfirstlane(sBuf)), "s"(__builtin_amdgcn_readfirstlane(sExp)), "v"(vL16), "v"(vFlag), "v"(vSnapOff), "v"(vSnapRd), "v"(vWd0), "v"(vWd1), "v"(vWr0), "v"(vWr1), "v"(vSp), "v"(vOutS), "v"(vAux0), "v"(vAux1), "v"(vAux2), "v"(vAux3), "v"(vAn), "v"(vAe), "s"(w12) : "v0", "v1", "v2", "v3", "v4", "v5", "v6", "v7", "v8", "v9", "v10", "v11", "v12", "v13", "v14", "v15", "v16", "v17", "v18", "v19", "v20", "v21", "v22", "v23", "v24", "v25", "v26", "v27", "v28", "v29", "v30", "v31", "v32", "v33", "v34", "v35", "v36", "v37", "v38", "v39", "v40", "v41", "v42", "v43", "v44", "v45", "v46", "v47", "v48", "v49", "v50", "v51", "v52", "v53", "v54", "v55", "v56", "v57", "v58", "v59", "v60", "v61", "v62", "v63", "v64", "v65", "v66", "v67", "v68", "v69", "v70", "v71", "v72", "v73", "v74", "v75", "v76", "v77", "v78", "v79", "v80", "v81", "v82", "v83", "v84", "v85", "v86", "v87", "v88", "v89", "v90", "v91", "v92", "v93", "v94", "v95", "v96", "v97", "v98", "v99", "v100", "v101", "v102", "v103", "v104", "v105", "v106", "v107", "v108", "v109", "v110", "v111", "v112", "v113", "v114", "v115", "v116", "v117", "v118", "v119", "v120", "v121", "v122", "v123", "v124", "v125", "v126", "v127", "v128", "v129", "v130", "v131", "v132", "v133", "v134", "v135", "v136", "v137", "v138", "v139", "v140", "v141", "v142", "v143", "v144", "v145", "v146", "v147", "v148", "v149", "v150", "v151", "v152", "v153", "v154", "v155", "v156", "v157", "v158", "v159", "v160", "v161", "v162", "v163", "v164", "v165", "v166", "v167", "v168", "v169", "v170", "v171", "v172", "v173", "v174", "v175", "v176", "v177", "v178", "v179", "v180", "v181", "v182", "v183", "v184", "v185", "v186", "v187", "v188", "v189", "v190", "v191", "v192", "v193", "v194", "v195", "v196", "v197", "v198", "v199", "v200", "v201", "v202", "v203", "v204", "v205", "v206", "v207", "v208", "v209", "v210", "v211", "v212", "v213", "v214", "v215", "v216", "v217", "v218", "v219", "v220", "v221", "v222", "v223", "v224", "v225", "v226", "v227", "v228", "v229", "v230", "v231", "a0", "a1", "a2", "a3", "a4", "a5", "a6", "a7", "a8", "a9", "a10", "a11", "a12", "a13", "a14", "a15", "a16", "a17", "a18", "a19", "a20", "a21", "a22", "a23", "a24", "a25", "a26", "a27", "a28", "a29", "a30", "a31", "a32", "a33", "a34", "a35", "a36", "a37", "a38", "a39", "a40", "a41", "a42", "a43", "a44", "a45", "a46", "a47", "a48", "a49", "a50", "a51", "a52", "a53", "a54", "a55", "a56", "a57", "a58", "a59", "a60", "a61", "a62", "a63", "a64", "a65", "a66", "a67", "a68", "a69", "a70", "a71", "a72", "a73", "a74", "a75", "a76", "a77", "a78", "a79", "a80", "a81", "a82", "a83", "a84", "a85", "a86", "a87", "a88", "a89", "a90", "a91", "a92", "a93", "a94", "a95", "a96", "a97", "a98", "a99", "a100", "a101", "a102", "a103", "a104", "a105", "a106", "a107", "a108", "a109", "a110", "a111", "a112", "a113", "a114", "a115", "a116", "a117", "a118", "a119", "a120", "a121", "a122", "a123", "a124", "a125", "a126", "a127", "a128", "a129", "a130", "a131", "a132", "a133", "a134", "a135", "a136", "a137", "a138", "a139", "a140", "a141", "a142", "a143", "a144", "a145", "a146", "a147", "a148", "a149", "a150", "a151", "a152", "a153", "a154", "a155", "a156", "a157", "a158", "a159", "a160", "a161", "a162", "a163", "a164", "a165", "a166", "a167", "a168", "a169", "a170", "a171", "a172", "a173", "a174", "a175", "a176", "a177", "a178", "a179", "a180", "a181", "a182", "a183", "a184", "a185", "a186", "a187", "a188", "a189", "a190", "a191", "a192", "a193", "a194", "a195", "a196", "a197", "a198", "a199", "a200", "a201", "a202", "a203", "a204", "a205", "a206", "a207", "a208", "a209", "a210", "a211", "a212", "a213", "a214", "a215", "a216", "a217", "a218", "a219", "a220", "a221", "a222", "a223", "a224", "a225", "a226", "a227", "a228", "a229", "a230", "a231", "a232", "a233", "a234", "a235", "a236", "a237", "a238", "a239", "a240", "a241", "a242", "a243", "a244", "a245", "a246", "a247", "a248", "a249", "a250", "a251", "a252", "a253", "a254", "a255", "s36", "s37", "s38", "s39", "s40", "s41", "s42", "s43", "s44", "s45", "s46", "s47", "s48", "s49", "s50", "s51", "s52", "s53", "s54", "s55", "s56", "s57", "s58", "s59", "s60", "s61", "s62", "s63", "s64", "s65", "s66", "s67", "s68", "s69", "s70", "s71", "s72", "s73", "s74", "s75", "s76", "s77", "s78", "s79", "s80", "s81", "s82", "s83", "s84", "s85", "s86", "s87", "s88", "s89", "s90", "s91", "s92", "s93", "s94", "s95", "s96", "s97", "vcc", "scc", "memory");
    ep = epOut;
    if (kind == 1 && 5 + RHELP + (rep >> 1) < NR4) { kind = 4; kh = rep & 1; rep = 5 + RHELP + (rep >> 1); }
  }
  if (kind == 4) {
    const unsigned sStep = rep < 5 ? 5 : rep == 5 ? 6 : NR4;
    COMMON(10, (kh * 9 + fj) * 128, 2560, 143360, 2048, 448, 0);
    const int cs = slice;
    const unsigned sOutOff = (kh ? OFF_P1 : OFF_P0) + (cs * 4 + wave) * 1024, sFoutOff = OFF_FP + (kh * 8 + cs) * 128;
    const unsigned sAuxOff = (kh ? OFF_XP : OFF_X1) + (cs * 4 + wave) * 1024, sWw = (kh * 1792 + 16 * wave) * 2048 + cs * 256, sBuf = kh * 57344;
    const int zu = 108 + (rep + 2) % 5;
    const unsigned vSp = lg * 2048 + l15 * 4, vSn = rep >= 5 ? OOB : !ALLZ ? (zu * 16 + wave * 4 + lg) * 2048 + (cs * 2 + kh) * 128 + l15 * 8 :
                         ((rep + 5 * (wave >> 1)) * 16 + (wave & 1) * 8 + (ln >> 3)) * 2048 + (cs * 2 + kh) * 128 + (ln & 7) * 16;
    (void)vWr2;
    const unsigned sExp = __builtin_amdgcn_readfirstlane(ep);
    asm volatile(
R"(.macro M d,w,x
v_mfma_f32_16x16x32_bf16 v[\d:\d+3], \w, v[\x:\x+3], v[\d:\d+3]
.endm
.macro R d,a,o
ds_read_b128 v[\d:\d+3], v\a offset:\o
.endm
.macro T d,a,o
ds_read_b64_tr_b16 \d, v\a offset:\o
.endm
.macro D a,s,o
buffer_load_dwordx4 v\a, s[36:39], s\s offen offset:\o sc1 lds
.endm
.macro L c
s_waitcnt lgkmcnt(\c)
.endm
Lcode_g4_b:
s_mov_b64 s[36:37],%0
s_mov_b32 s38,111243392
s_mov_b32 s39,0x00020000
s_mov_b32 s44,%1
s_mov_b32 s45,%2
s_mov_b32 s46,%3
s_mov_b32 s47,%4
s_mov_b32 s48,%5
s_mov_b32 s49,%6
s_mov_b32 s50,%7
s_mov_b32 s51,%8
s_mov_b32 s93,%9
s_mov_b32 s53,%10
v_mov_b32 v0,%11
v_mov_b32 v3,%12
v_mov_b32 v5,%13
v_mov_b32 v6,%14
v_mov_b32 v10,%15
v_mov_b32 v11,%16
v_mov_b32 v12,%17
v_mov_b32 v13,%18
v_mov_b32 v9,%19
v_mov_b32 v7,%20
s_mov_b64 s[40:41],%21
s_mov_b32 s42,7340032
s_mov_b32 s43,0x00020000
s_lshl_b32 s57,s44,11
s_cmp_eq_u32 s45,175
s_cbranch_scc1 Lg4_pre_%=
s_add_u32 s55,s50,0
s_add_u32 m0,s57,40960
s_nop 0
buffer_load_dwordx4 v10,s[40:43],s55 offen offset:0  lds
s_add_u32 m0,s57,41984
s_nop 0
buffer_load_dwordx4 v11,s[40:43],s55 offen offset:0  lds
s_add_u32 m0,s57,49024
s_nop 0
buffer_load_dwordx4 v10,s[40:43],s55 offen offset:128  lds
s_add_u32 m0,s57,50048
s_nop 0
buffer_load_dwordx4 v11,s[40:43],s55 offen offset:128  lds
s_add_u32 s55,s50,131072
s_add_u32 m0,s57,57344
s_nop 0
buffer_load_dwordx4 v10,s[40:43],s55 offen offset:0  lds
s_add_u32 m0,s57,58368
s_nop 0
buffer_load_dwordx4 v11,s[40:43],s55 offen offset:0  lds
s_add_u32 m0,s57,65408
s_nop 0
buffer_load_dwordx4 v10,s[40:43],s55 offen offset:128  lds
s_add_u32 m0,s57,66432
s_nop 0
buffer_load_dwordx4 v11,s[40:43],s55 offen offset:128  lds
s_add_u32 s55,s50,262144
s_add_u32 m0,s57,73728
s_nop 0
buffer_load_dwordx4 v10,s[40:43],s55 offen offset:0  lds
s_add_u32 m0,s57,74752
s_nop 0
buffer_load_dwordx4 v11,s[40:43],s55 offen offset:0  lds
s_add_u32 m0,s57,81792
s_nop 0
buffer_load_dwordx4 v10,s[40:43],s55 offen offset:128  lds
s_add_u32 m0,s57,82816
s_nop 0
buffer_load_dwordx4 v11,s[40:43],s55 offen offset:128  lds
s_add_u32 s55,s50,393216
s_add_u32 m0,s57,90112
s_nop 0
buffer_load_dwordx4 v10,s[40:43],s55 offen offset:0  lds
s_add_u32 m0,s57,91136
s_nop 0
buffer_load_dwordx4 v11,s[40:43],s55 offen offset:0  lds
s_add_u32 m0,s57,98176
s_nop 0
buffer_load_dwordx4 v10,s[40:43],s55 offen offset:128  lds
s_add_u32 m0,s57,99200
s_nop 0
buffer_load_dwordx4 v11,s[40:43],s55 offen offset:128  lds
s_add_u32 s55,s50,524288
s_add_u32 m0,s57,106496
s_nop 0
buffer_load_dwordx4 v10,s[40:43],s55 offen offset:0  lds
s_add_u32 m0,s57,107520
s_nop 0
buffer_load_dwordx4 v11,s[40:43],s55 offen offset:0  lds
s_add_u32 m0,s57,114560
s_nop 0
buffer_load_dwordx4 v10,s[40:43],s55 offen offset:128  lds
s_add_u32 m0,s57,115584
s_nop 0
buffer_load_dwordx4 v11,s[40:43],s55 offen offset:128  lds
s_add_u32 s55,s50,655360
s_add_u32 m0,s57,122880
s_nop 0
buffer_load_dwordx4 v10,s[40:43],s55 offen offset:0  lds
s_add_u32 m0,s57,123904
s_nop 0
buffer_load_dwordx4 v11,s[40:43],s55 offen offset:0  lds
s_add_u32 m0,s57,130944
s_nop 0
buffer_load_dwordx4 v10,s[40:43],s55 offen offset:128  lds
s_add_u32 m0,s57,131968
s_nop 0
buffer_load_dwordx4 v11,s[40:43],s55 offen offset:128  lds
s_waitcnt vmcnt(20) lgkmcnt(0)
s_barrier
s_add_u32 s55,s50,786432
s_add_u32 m0,s57,139264
s_nop 0
buffer_load_dwordx4 v10,s[40:43],s55 offen offset:0  lds
s_add_u32 m0,s57,140288
s_nop 0
buffer_load_dwordx4 v11,s[40:43],s55 offen offset:0  lds
s_add_u32 m0,s57,147328
s_nop 0
buffer_load_dwordx4 v10,s[40:43],s55 offen offset:128  lds
s_add_u32 m0,s57,148352
s_nop 0
buffer_load_dwordx4 v11,s[40:43],s55 offen offset:128  lds
v_add_u32 v18,40960,v12
v_add_u32 v19,40960,v13
T a[0:1],18,0
T a[2:3],18,512
T a[4:5],19,0
T a[6:7],19,512
T a[8:9],18,4096
T a[10:11],18,4608
T a[12:13],19,4096
T a[14:15],19,4608
s_waitcnt vmcnt(20) lgkmcnt(0)
s_barrier
s_add_u32 s55,s50,917504
s_add_u32 m0,s57,40960
s_nop 0
buffer_load_dwordx4 v10,s[40:43],s55 offen offset:0  lds
s_add_u32 m0,s57,41984
s_nop 0
buffer_load_dwordx4 v11,s[40:43],s55 offen offset:0  lds
s_add_u32 m0,s57,49024
s_nop 0
buffer_load_dwordx4 v10,s[40:43],s55 offen offset:128  lds
s_add_u32 m0,s57,50048
s_nop 0
buffer_load_dwordx4 v11,s[40:43],s55 offen offset:128  lds
v_add_u32 v18,57344,v12
v_add_u32 v19,57344,v13
T a[16:17],18,0
T a[18:19],18,512
T a[20:21],19,0
T a[22:23],19,512
T a[24:25],18,4096
T a[26:27],18,4608
T a[28:29],19,4096
T a[30:31],19,4608
s_waitcnt vmcnt(20) lgkmcnt(0)
s_barrier
s_add_u32 s55,s50,1048576
s_add_u32 m0,s57,57344
s_nop 0
buffer_load_dwordx4 v10,s[40:43],s55 offen offset:0  lds
s_add_u32 m0,s57,58368
s_nop 0
buffer_load_dwordx4 v11,s[40:43],s55 offen offset:0  lds
s_add_u32 m0,s57,65408
s_nop 0
buffer_load_dwordx4 v10,s[40:43],s55 offen offset:128  lds
s_add_u32 m0,s57,66432
s_nop 0
buffer_load_dwordx4 v11,s[40:43],s55 offen offset:128  lds
v_add_u32 v18,73728,v12
v_add_u32 v19,73728,v13
T a[32:33],18,0
T a[34:35],18,512
T a[36:37],19,0
T a[38:39],19,512
T a[40:41],18,4096
T a[42:43],18,4608
T a[44:45],19,4096
T a[46:47],19,4608
s_waitcnt vmcnt(20) lgkmcnt(0)
s_barrier
s_add_u32 s55,s50,1179648
s_add_u32 m0,s57,73728
s_nop 0
buffer_load_dwordx4 v10,s[40:43],s55 offen offset:0  lds
s_add_u32 m0,s57,74752
s_nop 0
buffer_load_dwordx4 v11,s[40:43],s55 offen offset:0  lds
s_add_u32 m0,s57,81792
s_nop 0
buffer_load_dwordx4 v10,s[40:43],s55 offen offset:128  lds
s_add_u32 m0,s57,82816
s_nop 0
buffer_load_dwordx4 v11,s[40:43],s55 offen offset:128  lds
v_add_u32 v18,90112,v12
v_add_u32 v19,90112,v13
T a[48:49],18,0
T a[50:51],18,512
T a[52:53],19,0
T a[54:55],19,512
T a[56:57],18,4096
T a[58:59],18,4608
T a[60:61],19,4096
T a[62:63],19,4608
s_waitcnt vmcnt(20) lgkmcnt(0)
s_barrier
s_add_u32 s55,s50,1310720
s_add_u32 m0,s57,90112
s_nop 0
buffer_load_dwordx4 v10,s[40:43],s55 offen offset:0  lds
s_add_u32 m0,s57,91136
s_nop 0
buffer_load_dwordx4 v11,s[40:43],s55 offen offset:0  lds
s_add_u32 m0,s57,98176
s_nop 0
buffer_load_dwordx4 v10,s[40:43],s55 offen offset:128  lds
s_add_u32 m0,s57,99200
s_nop 0
buffer_load_dwordx4 v11,s[40:43],s55 offen offset:128  lds
v_add_u32 v18,106496,v12
v_add_u32 v19,106496,v13
T a[64:65],18,0
T a[66:67],18,512
T a[68:69],19,0
T a[70:71],19,512
T a[72:73],18,4096
T a[74:75],18,4608
T a[76:77],19,4096
T a[78:79],19,4608
s_waitcnt vmcnt(20) lgkmcnt(0)
s_barrier
s_add_u32 s55,s50,1441792
s_add_u32 m0,s57,106496
s_nop 0
buffer_load_dwordx4 v10,s[40:43],s55 offen offset:0  lds
s_add_u32 m0,s57,107520
s_nop 0
buffer_load_dwordx4 v11,s[40:43],s55 offen offset:0  lds
s_add_u32 m0,s57,114560
s_nop 0
buffer_load_dwordx4 v10,s[40:43],s55 offen offset:128  lds
s_add_u32 m0,s57,115584
s_nop 0
buffer_load_dwordx4 v11,s[40:43],s55 offen offset:128  lds
v_add_u32 v18,122880,v12
v_add_u32 v19,122880,v13
T a[80:81],18,0
T a[82:83],18,512
T a[84:85],19,0
T a[86:87],19,512
T a[88:89],18,4096
T a[90:91],18,4608
T a[92:93],19,4096
T a[94:95],19,4608
s_waitcnt vmcnt(20) lgkmcnt(0)
s_barrier
s_add_u32 s55,s50,1572864
s_add_u32 m0,s57,122880
s_nop 0
buffer_load_dwordx4 v10,s[40:43],s55 offen offset:0  lds
s_add_u32 m0,s57,123904
s_nop 0
buffer_load_dwordx4 v11,s[40:43],s55 offen offset:0  lds
s_add_u32 m0,s57,130944
s_nop 0
buffer_load_dwordx4 v10,s[40:43],s55 offen offset:128  lds
s_add_u32 m0,s57,131968
s_nop 0
buffer_load_dwordx4 v11,s[40:43],s55 offen offset:128  lds
v_add_u32 v18,139264,v12
v_add_u32 v19,139264,v13
T a[96:97],18,0
T a[98:99],18,512
T a[100:101],19,0
T a[102:103],19,512
T a[104:105],18,4096
T a[106:107],18,4608
T a[108:109],19,4096
T a[110:111],19,4608
s_waitcnt vmcnt(20) lgkmcnt(0)
s_barrier
s_add_u32 s55,s50,1703936
s_add_u32 m0,s57,139264
s_nop 0
buffer_load_dwordx4 v10,s[40:43],s55 offen offset:0  lds
s_add_u32 m0,s57,140288
s_nop 0
buffer_load_dwordx4 v11,s[40:43],s55 offen offset:0  lds
s_add_u32 m0,s57,147328
s_nop 0
buffer_load_dwordx4 v10,s[40:43],s55 offen offset:128  lds
s_add_u32 m0,s57,148352
s_nop 0
buffer_load_dwordx4 v11,s[40:43],s55 offen offset:128  lds
v_add_u32 v18,40960,v12
v_add_u32 v19,40960,v13
T a[112:113],18,0
T a[114:115],18,512
T a[116:117],19,0
T a[118:119],19,512
T a[120:121],18,4096
T a[122:123],18,4608
T a[124:125],19,4096
T a[126:127],19,4608
s_waitcnt vmcnt(20) lgkmcnt(0)
s_barrier
s_add_u32 s55,s50,1835008
s_add_u32 m0,s57,40960
s_nop 0
buffer_load_dwordx4 v10,s[40:43],s55 offen offset:0  lds
s_add_u32 m0,s57,41984
s_nop 0
buffer_load_dwordx4 v11,s[40:43],s55 offen offset:0  lds
s_add_u32 m0,s57,49024
s_nop 0
buffer_load_dwordx4 v10,s[40:43],s55 offen offset:128  lds
s_add_u32 m0,s57,50048
s_nop 0
buffer_load_dwordx4 v11,s[40:43],s55 offen offset:128  lds
v_add_u32 v18,57344,v12
v_add_u32 v19,57344,v13
T a[128:129],18,0
T a[130:131],18,512
T a[132:133],19,0
T a[134:135],19,512
T a[136:137],18,4096
T a[138:139],18,4608
T a[140:141],19,4096
T a[142:143],19,4608
s_waitcnt vmcnt(20) lgkmcnt(0)
s_barrier
s_add_u32 s55,s50,1966080
s_add_u32 m0,s57,57344
s_nop 0
buffer_load_dwordx4 v10,s[40:43],s55 offen offset:0  lds
s_add_u32 m0,s57,58368
s_nop 0
buffer_load_dwordx4 v11,s[40:43],s55 offen offset:0  lds
s_add_u32 m0,s57,65408
s_nop 0
buffer_load_dwordx4 v10,s[40:43],s55 offen offset:128  lds
s_add_u32 m0,s57,66432
s_nop 0
buffer_load_dwordx4 v11,s[40:43],s55 offen offset:128  lds
v_add_u32 v18,73728,v12
v_add_u32 v19,73728,v13
T a[144:145],18,0
T a[146:147],18,512
T a[148:149],19,0
T a[150:151],19,512
T a[152:153],18,4096
T a[154:155],18,4608
T a[156:157],19,4096
T a[158:159],19,4608
s_waitcnt vmcnt(20) lgkmcnt(0)
s_barrier
s_add_u32 s55,s50,2097152
s_add_u32 m0,s57,73728
s_nop 0
buffer_load_dwordx4 v10,s[40:43],s55 offen offset:0  lds
s_add_u32 m0,s57,74752
s_nop 0
buffer_load_dwordx4 v11,s[40:43],s55 offen offset:0  lds
s_add_u32 m0,s57,81792
s_nop 0
buffer_load_dwordx4 v10,s[40:43],s55 offen offset:128  lds
s_add_u32 m0,s57,82816
s_nop 0
buffer_load_dwordx4 v11,s[40:43],s55 offen offset:128  lds
v_add_u32 v18,90112,v12
v_add_u32 v19,90112,v13
T a[160:161],18,0
T a[162:163],18,512
T a[164:165],19,0
T a[166:167],19,512
T a[168:169],18,4096
T a[170:171],18,4608
T a[172:173],19,4096
T a[174:175],19,4608
s_waitcnt vmcnt(20) lgkmcnt(0)
s_barrier
s_add_u32 s55,s50,2228224
s_add_u32 m0,s57,90112
s_nop 0
buffer_load_dwordx4 v10,s[40:43],s55 offen offset:0  lds
s_add_u32 m0,s57,91136
s_nop 0
buffer_load_dwordx4 v11,s[40:43],s55 offen offset:0  lds
s_add_u32 m0,s57,98176
s_nop 0
buffer_load_dwordx4 v10,s[40:43],s55 offen offset:128  lds
s_add_u32 m0,s57,99200
s_nop 0
buffer_load_dwordx4 v11,s[40:43],s55 offen offset:128  lds
v_add_u32 v18,106496,v12
v_add_u32 v19,106496,v13
T a[176:177],18,0
T a[178:179],18,512
T a[180:181],19,0
T a[182:183],19,512
T a[184:185],18,4096
T a[186:187],18,4608
T a[188:189],19,4096
T a[190:191],19,4608
s_waitcnt vmcnt(20) lgkmcnt(0)
s_barrier
s_add_u32 s55,s50,2359296
s_add_u32 m0,s57,106496
s_nop 0
buffer_load_dwordx4 v10,s[40:43],s55 offen offset:0  lds
s_add_u32 m0,s57,107520
s_nop 0
buffer_load_dwordx4 v11,s[40:43],s55 offen offset:0  lds
s_add_u32 m0,s57,114560
s_nop 0
buffer_load_dwordx4 v10,s[40:43],s55 offen offset:128  lds
s_add_u32 m0,s57,115584
s_nop 0
buffer_load_dwordx4 v11,s[40:43],s55 offen offset:128  lds
v_add_u32 v18,122880,v12
v_add_u32 v19,122880,v13
T a[192:193],18,0
T a[194:195],18,512
T a[196:197],19,0
T a[198:199],19,512
T a[200:201],18,4096
T a[202:203],18,4608
T a[204:205],19,4096
T a[206:207],19,4608
s_waitcnt vmcnt(20) lgkmcnt(0)
s_barrier
s_add_u32 s55,s50,2490368
s_add_u32 m0,s57,122880
s_nop 0
buffer_load_dwordx4 v10,s[40:43],s55 offen offset:0  lds
s_add_u32 m0,s57,123904
s_nop 0
buffer_load_dwordx4 v11,s[40:43],s55 offen offset:0  lds
s_add_u32 m0,s57,130944
s_nop 0
buffer_load_dwordx4 v10,s[40:43],s55 offen offset:128  lds
s_add_u32 m0,s57,131968
s_nop 0
buffer_load_dwordx4 v11,s[40:43],s55 offen offset:128  lds
v_add_u32 v18,139264,v12
v_add_u32 v19,139264,v13
T a[208:209],18,0
T a[210:211],18,512
T a[212:213],19,0
T a[214:215],19,512
T a[216:217],18,4096
T a[218:219],18,4608
T a[220:221],19,4096
T a[222:223],19,4608
s_waitcnt vmcnt(20) lgkmcnt(0)
s_barrier
s_add_u32 s55,s50,2621440
s_add_u32 m0,s57,139264
s_nop 0
buffer_load_dwordx4 v10,s[40:43],s55 offen offset:0  lds
s_add_u32 m0,s57,140288
s_nop 0
buffer_load_dwordx4 v11,s[40:43],s55 offen offset:0  lds
s_add_u32 m0,s57,147328
s_nop 0
buffer_load_dwordx4 v10,s[40:43],s55 offen offset:128  lds
s_add_u32 m0,s57,148352
s_nop 0
buffer_load_dwordx4 v11,s[40:43],s55 offen offset:128  lds
v_add_u32 v18,40960,v12
v_add_u32 v19,40960,v13
T a[224:225],18,0
T a[226:227],18,512
T a[228:229],19,0
T a[230:231],19,512
T a[232:233],18,4096
T a[234:235],18,4608
T a[236:237],19,4096
T a[238:239],19,4608
s_waitcnt vmcnt(20) lgkmcnt(0)
s_barrier
)"
R"(s_add_u32 s55,s50,2752512
s_add_u32 m0,s57,40960
s_nop 0
buffer_load_dwordx4 v10,s[40:43],s55 offen offset:0  lds
s_add_u32 m0,s57,41984
s_nop 0
buffer_load_dwordx4 v11,s[40:43],s55 offen offset:0  lds
s_add_u32 m0,s57,49024
s_nop 0
buffer_load_dwordx4 v10,s[40:43],s55 offen offset:128  lds
s_add_u32 m0,s57,50048
s_nop 0
buffer_load_dwordx4 v11,s[40:43],s55 offen offset:128  lds
v_add_u32 v18,57344,v12
v_add_u32 v19,57344,v13
T a[240:241],18,0
T a[242:243],18,512
T a[244:245],19,0
T a[246:247],19,512
T a[248:249],18,4096
T a[250:251],18,4608
T a[252:253],19,4096
T a[254:255],19,4608
Lg4_pre_%=:
s_waitcnt vmcnt(20) lgkmcnt(0)
s_barrier
s_add_u32 s55,s50,2883584
s_add_u32 m0,s57,57344
s_nop 0
buffer_load_dwordx4 v10,s[40:43],s55 offen offset:0  lds
s_add_u32 m0,s57,58368
s_nop 0
buffer_load_dwordx4 v11,s[40:43],s55 offen offset:0  lds
s_add_u32 m0,s57,65408
s_nop 0
buffer_load_dwordx4 v10,s[40:43],s55 offen offset:128  lds
s_add_u32 m0,s57,66432
s_nop 0
buffer_load_dwordx4 v11,s[40:43],s55 offen offset:128  lds
v_add_u32 v18,73728,v12
v_add_u32 v19,73728,v13
T v[64:65],18,0
T v[66:67],18,512
T v[68:69],19,0
T v[70:71],19,512
T v[72:73],18,4096
T v[74:75],18,4608
T v[76:77],19,4096
T v[78:79],19,4608
s_waitcnt vmcnt(20) lgkmcnt(0)
s_barrier
s_add_u32 s55,s50,3014656
s_add_u32 m0,s57,73728
s_nop 0
buffer_load_dwordx4 v10,s[40:43],s55 offen offset:0  lds
s_add_u32 m0,s57,74752
s_nop 0
buffer_load_dwordx4 v11,s[40:43],s55 offen offset:0  lds
s_add_u32 m0,s57,81792
s_nop 0
buffer_load_dwordx4 v10,s[40:43],s55 offen offset:128  lds
s_add_u32 m0,s57,82816
s_nop 0
buffer_load_dwordx4 v11,s[40:43],s55 offen offset:128  lds
v_add_u32 v18,90112,v12
v_add_u32 v19,90112,v13
T v[80:81],18,0
T v[82:83],18,512
T v[84:85],19,0
T v[86:87],19,512
T v[88:89],18,4096
T v[90:91],18,4608
T v[92:93],19,4096
T v[94:95],19,4608
s_waitcnt vmcnt(20) lgkmcnt(0)
s_barrier
s_add_u32 s55,s50,3145728
s_add_u32 m0,s57,90112
s_nop 0
buffer_load_dwordx4 v10,s[40:43],s55 offen offset:0  lds
s_add_u32 m0,s57,91136
s_nop 0
buffer_load_dwordx4 v11,s[40:43],s55 offen offset:0  lds
s_add_u32 m0,s57,98176
s_nop 0
buffer_load_dwordx4 v10,s[40:43],s55 offen offset:128  lds
s_add_u32 m0,s57,99200
s_nop 0
buffer_load_dwordx4 v11,s[40:43],s55 offen offset:128  lds
v_add_u32 v18,106496,v12
v_add_u32 v19,106496,v13
T v[96:97],18,0
T v[98:99],18,512
T v[100:101],19,0
T v[102:103],19,512
T v[104:105],18,4096
T v[106:107],18,4608
T v[108:109],19,4096
T v[110:111],19,4608
s_waitcnt vmcnt(20) lgkmcnt(0)
s_barrier
s_add_u32 s55,s50,3276800
s_add_u32 m0,s57,106496
s_nop 0
buffer_load_dwordx4 v10,s[40:43],s55 offen offset:0  lds
s_add_u32 m0,s57,107520
s_nop 0
buffer_load_dwordx4 v11,s[40:43],s55 offen offset:0  lds
s_add_u32 m0,s57,114560
s_nop 0
buffer_load_dwordx4 v10,s[40:43],s55 offen offset:128  lds
s_add_u32 m0,s57,115584
s_nop 0
buffer_load_dwordx4 v11,s[40:43],s55 offen offset:128  lds
v_add_u32 v18,122880,v12
v_add_u32 v19,122880,v13
T v[112:113],18,0
T v[114:115],18,512
T v[116:117],19,0
T v[118:119],19,512
T v[120:121],18,4096
T v[122:123],18,4608
T v[124:125],19,4096
T v[126:127],19,4608
s_waitcnt vmcnt(20) lgkmcnt(0)
s_barrier
s_add_u32 s55,s50,3407872
s_add_u32 m0,s57,122880
s_nop 0
buffer_load_dwordx4 v10,s[40:43],s55 offen offset:0  lds
s_add_u32 m0,s57,123904
s_nop 0
buffer_load_dwordx4 v11,s[40:43],s55 offen offset:0  lds
s_add_u32 m0,s57,130944
s_nop 0
buffer_load_dwordx4 v10,s[40:43],s55 offen offset:128  lds
s_add_u32 m0,s57,131968
s_nop 0
buffer_load_dwordx4 v11,s[40:43],s55 offen offset:128  lds
v_add_u32 v18,139264,v12
v_add_u32 v19,139264,v13
T v[128:129],18,0
T v[130:131],18,512
T v[132:133],19,0
T v[134:135],19,512
T v[136:137],18,4096
T v[138:139],18,4608
T v[140:141],19,4096
T v[142:143],19,4608
s_waitcnt vmcnt(20) lgkmcnt(0)
s_barrier
s_add_u32 s55,s50,3538944
s_add_u32 m0,s57,139264
s_nop 0
buffer_load_dwordx4 v10,s[40:43],s55 offen offset:0  lds
s_add_u32 m0,s57,140288
s_nop 0
buffer_load_dwordx4 v11,s[40:43],s55 offen offset:0  lds
s_add_u32 m0,s57,147328
s_nop 0
buffer_load_dwordx4 v10,s[40:43],s55 offen offset:128  lds
s_add_u32 m0,s57,148352
s_nop 0
buffer_load_dwordx4 v11,s[40:43],s55 offen offset:128  lds
v_add_u32 v18,40960,v12
v_add_u32 v19,40960,v13
T v[144:145],18,0
T v[146:147],18,512
T v[148:149],19,0
T v[150:151],19,512
T v[152:153],18,4096
T v[154:155],18,4608
T v[156:157],19,4096
T v[158:159],19,4608
s_waitcnt vmcnt(20) lgkmcnt(0)
s_barrier
v_add_u32 v18,57344,v12
v_add_u32 v19,57344,v13
T v[160:161],18,0
T v[162:163],18,512
T v[164:165],19,0
T v[166:167],19,512
T v[168:169],18,4096
T v[170:171],18,4608
T v[172:173],19,4096
T v[174:175],19,4608
s_waitcnt vmcnt(16) lgkmcnt(0)
s_barrier
v_add_u32 v18,73728,v12
v_add_u32 v19,73728,v13
T v[176:177],18,0
T v[178:179],18,512
T v[180:181],19,0
T v[182:183],19,512
T v[184:185],18,4096
T v[186:187],18,4608
T v[188:189],19,4096
T v[190:191],19,4608
s_waitcnt vmcnt(12) lgkmcnt(0)
s_barrier
v_add_u32 v18,90112,v12
v_add_u32 v19,90112,v13
T v[192:193],18,0
T v[194:195],18,512
T v[196:197],19,0
T v[198:199],19,512
T v[200:201],18,4096
T v[202:203],18,4608
T v[204:205],19,4096
T v[206:207],19,4608
s_waitcnt vmcnt(8) lgkmcnt(0)
s_barrier
v_add_u32 v18,106496,v12
v_add_u32 v19,106496,v13
T v[208:209],18,0
T v[210:211],18,512
T v[212:213],19,0
T v[214:215],19,512
T v[216:217],18,4096
T v[218:219],18,4608
T v[220:221],19,4096
T v[222:223],19,4608
s_waitcnt vmcnt(4) lgkmcnt(0)
s_barrier
v_add_u32 v18,122880,v12
v_add_u32 v19,122880,v13
T v[224:225],18,0
T v[226:227],18,512
T v[228:229],19,0
T v[230:231],19,512
T v[232:233],18,4096
T v[234:235],18,4608
T v[236:237],19,4096
T v[238:239],19,4608
s_waitcnt vmcnt(0) lgkmcnt(0)
s_barrier
v_add_u32 v18,139264,v12
v_add_u32 v19,139264,v13
T v[240:241],18,0
T v[242:243],18,512
T v[244:245],19,0
T v[246:247],19,512
T v[248:249],18,4096
T v[250:251],18,4608
T v[252:253],19,4096
T v[254:255],19,4608
L 0
s_barrier
v_mov_b32 v4,s53
s_mov_b32 s82,0
s_mov_b32 s80,0
s_mov_b32 s54,s45
s_mul_i32 s57,s44,7168
s_mul_i32 s55,s45,114688
s_add_u32 s55,s55,s57
s_add_u32 s55,s55,40894464
s_mul_i32 s57,s44,7168
s_mul_i32 s60,s46,114688
s_mul_i32 s61,s46,2560
s_mul_i32 s62,s46,32768
s_mul_i32 s63,s46,2048
s_mul_i32 s64,s46,512
s_lshl_b32 s59,s44,8
s_add_u32 s59,s59,143360
s_mul_i32 s58,s45,2560
s_add_u32 s58,s58,109891584
s_mul_i32 s65,s45,32768
s_add_u32 s65,s65,s47
s_mul_i32 s68,s45,2048
s_add_u32 s68,s68,s48
s_mul_i32 s73,s45,512
s_add_u32 s73,s73,106954752
s_mul_i32 s74,s45,32768
s_add_u32 s74,s74,s49
s_mul_i32 s75,s45,1024
s_add_u32 s75,s75,107479040
s_mov_b32 s66,0x7fff0000
s_mov_b32 s67,0x7fff0000
s_mov_b32 s69,0x7fff0000
s_mov_b32 s76,0x7fff0000
s_mov_b32 s77,0x7fff0000
s_mov_b32 s70,0x7fff0000
s_mov_b32 s71,0x7fff0000
s_mov_b32 s72,0x7fff0000
v_mov_b32 v1,v0
v_add_u32 v2,28672,v0
s_mov_b32 s84,28672
s_mov_b32 s86,0x3a800000
s_mov_b32 s87,0x3727c5ac
s_mov_b32 s88,0xc0135761
s_mov_b32 s89,0x3d372713
s_lshr_b32 s85,s44,1
s_mul_i32 s85,s85,127
s_add_u32 s55,s55,s51
s_mul_i32 s78,s45,114688
s_sub_u32 s60,s55,s78
s_add_u32 s50,s93,224
s_add_u32 s93,s93,170
s_cmp_lt_u32 s45,224
s_cselect_b32 s63,224,-1
s_cmp_lt_u32 s45,170
s_cselect_b32 s63,170,s63
s_cselect_b32 s93,s93,s50
s_cselect_b32 s88,6,8
s_cselect_b32 s89,224,-1
v_lshlrev_b32 v8,22,v0
v_and_b32 v8,0x80000000,v8
v_or_b32 v8,v8,v5
v_add_u32 v2,57344,v0
s_lshl_b32 s84,s57,1
s_add_u32 s84,s84,57344
s_mov_b64 s[86:87],%22
s_mov_b64 s[40:41],%22
s_mov_b32 s42,4194304
s_and_b32 s78,s47,65535
s_lshr_b32 s78,s78,4
s_sub_u32 s85,s47,s78
s_add_u32 s85,s85,4194304
s_add_u32 s45,s47,5832704
s_mul_i32 s73,s44,1280
s_add_u32 s73,s73,114688
v_lshrrev_b32 v62,4,v9
v_and_b32 v62,3,v62
v_mul_u32_u24 v62,288,v62
v_lshrrev_b32 v63,7,v9
v_and_b32 v5,12,v9
v_add3_u32 v5,v5,v62,v63
v_add_u32 v5,s73,v5
v_mov_b32 v10,0
v_mov_b32 v11,0
v_mov_b32 v12,0
v_mov_b32 v13,0
s_mov_b32 s79,1638400
buffer_store_dwordx4 v[10:13],v7,s[40:43],s79 offen offset:0 sc0 sc1
s_mov_b32 s79,1966080
buffer_store_dwordx4 v[10:13],v7,s[40:43],s79 offen offset:0 sc0 sc1
s_mov_b32 s79,2293760
buffer_store_dwordx4 v[10:13],v7,s[40:43],s79 offen offset:0 sc0 sc1
s_mov_b32 s79,2621440
buffer_store_dwordx4 v[10:13],v7,s[40:43],s79 offen offset:0 sc0 sc1
s_mov_b32 s79,2949120
buffer_store_dwordx4 v[10:13],v7,s[40:43],s79 offen offset:0 sc0 sc1
s_mov_b32 s79,3276800
buffer_store_dwordx4 v[10:13],v7,s[40:43],s79 offen offset:0 sc0 sc1
s_mov_b32 s79,3604480
buffer_store_dwordx4 v[10:13],v7,s[40:43],s79 offen offset:0 sc0 sc1
s_mov_b32 s79,3932160
buffer_store_dwordx4 v[10:13],v7,s[40:43],s79 offen offset:0 sc0 sc1
s_getpc_b64 s[40:41]
Lg4_pc_%=:
s_lshl_b32 s78,s44,13
v_lshlrev_b32 v62,3,v0
v_add_u32 v62,s78,v62
v_min_u32 v62,Lcode_g4_e-Lg4_pc_%=-4,v62
s_mov_b32 m0,163584
s_nop 0
global_load_lds_dword v62,s[40:41]
s_add_u32 m0,s59,4096
s_nop 0
buffer_load_dword v8,s[36:39],s58 offen offset:0 sc1 lds
buffer_store_dword v4,v3,s[36:39],s71 offen offset:0 sc0 sc1
s_add_u32 m0,s59,5120
s_nop 0
buffer_load_dword v8,s[36:39],s58 offen offset:0 sc1 lds
buffer_store_dword v4,v3,s[36:39],s71 offen offset:0 sc0 sc1
s_mov_b32 s83,0
Lg4_poll_%=:
s_add_u32 s78,s83,2
s_and_b32 s78,s78,3
s_lshl_b32 s78,s78,10
s_add_u32 s78,s78,4096
s_add_u32 m0,s59,s78
s_nop 0
buffer_load_dword v8,s[36:39],s58 offen offset:0 sc1 lds
s_and_b32 s78,s83,3
s_lshl_b32 s78,s78,10
v_add_u32 v62,s78,v6
s_waitcnt vmcnt(4)
s_barrier
ds_read_b32 v7,v62 offset:4096
buffer_store_dword v4,v3,s[36:39],s70 offen offset:0 sc0 sc1
s_mov_b32 s70,s71
s_mov_b32 s71,s72
s_mov_b32 s72,0x7fff0000
s_add_u32 s83,s83,1
s_add_u32 s82,s82,1
L 0
v_cmp_eq_u32 vcc,s53,v7
s_nop 0
s_cmp_eq_u32 vcc_lo,-1
s_cbranch_scc1 Lg4_go_%=
s_cmp_lt_u32 s82,20000
s_cbranch_scc1 Lg4_poll_%=
Lg4_go_%=:
s_add_u32 m0,s57,0
s_add_u32 s56,s55,0
D 0,56,0
D 0,56,1024
D 0,56,2048
D 0,56,3072
s_add_u32 m0,s57,4096
s_add_u32 s56,s56,4096
D 0,56,0
D 0,56,1024
D 0,56,2048
s_add_u32 s61,s54,s46
s_cmp_ge_u32 s61,s63
s_cselect_b32 s61,s93,s61
s_cselect_b32 s46,s88,s46
s_cselect_b32 s63,s89,s63
s_cselect_b32 s93,s50,s93
s_cselect_b32 s88,8,s88
s_cselect_b32 s89,-1,s89
s_mul_i32 s58,s61,2560
s_add_u32 s58,s58,109891584
s_add_u32 m0,s59,2048
s_nop 0
buffer_load_dword v8,s[36:39],s58 offen offset:0 sc1 lds
s_add_u32 m0,s57,28672
s_add_u32 s56,s55,28672
D 0,56,0
D 0,56,1024
D 0,56,2048
D 0,56,3072
s_add_u32 m0,s57,32768
s_add_u32 s56,s56,4096
D 0,56,0
D 0,56,1024
D 0,56,2048
s_mul_i32 s55,s61,114688
s_add_u32 s55,s55,s60
s_sub_u32 s57,s84,s57
s_mov_b32 s54,s61
s_waitcnt vmcnt(7)
s_barrier
buffer_store_dword v4,v3,s[36:39],s70 offen offset:0 sc0 sc1
s_min_u32 s71,s71,s72
buffer_store_dword v4,v3,s[36:39],s71 offen offset:0 sc0 sc1
s_mov_b32 s70,0x7fff0000
s_mov_b32 s71,0x7fff0000
s_mov_b32 s72,0x7fff0000
s_cmp_eq_u32 s80,1
s_cbranch_scc1 Lg4_st1_%=
s_branch Lg4_st0_%=
Lg4_gox_%=:
s_add_u32 m0,s59,2048
s_nop 0
buffer_load_dword v8,s[36:39],s58 offen offset:0 sc1 lds
s_waitcnt vmcnt(0)
s_barrier
buffer_store_dword v4,v3,s[36:39],s70 offen offset:0 sc0 sc1
s_min_u32 s71,s71,s72
buffer_store_dword v4,v3,s[36:39],s71 offen offset:0 sc0 sc1
s_mov_b32 s70,0x7fff0000
s_mov_b32 s71,0x7fff0000
s_mov_b32 s72,0x7fff0000
s_cmp_eq_u32 s80,1
s_cbranch_scc1 Lg4_st1_%=
Lg4_st0_%=:
ds_read_b32 v7,v6 offset:2048
R 26,1,0
R 30,1,1024
R 34,1,2048
R 38,1,3072
R 42,1,4096
L 5
v_cmp_eq_u32 vcc,s53,v7
L 3
s_cmp_eq_u32 vcc_lo,-1
s_cbranch_scc1 Lg4_F0_%=
s_branch Lg4_L0_%=
Lg4_st1_%=:
ds_read_b32 v7,v6 offset:2048
R 26,2,0
R 30,2,1024
R 34,2,2048
R 38,2,3072
R 42,2,4096
L 5
v_cmp_eq_u32 vcc,s53,v7
L 3
s_cmp_eq_u32 vcc_lo,-1
s_cbranch_scc1 Lg4_F1_%=
s_branch Lg4_L1_%=
Lg4_F0_%=:
v_mfma_f32_16x16x32_bf16 v[14:17],a[4:7],v[26:29],0
s_add_u32 m0,s57,0
R 46,1,5120
v_lshlrev_b32 v62,16,v58
M 14,a[12:15],30
s_add_u32 s56,s55,0
v_add_f32 v18,v18,v62
v_and_b32 v63,0xffff0000,v58
v_mfma_f32_16x16x32_bf16 v[10:13],a[0:3],v[26:29],0
s_add_u32 s61,s54,s46
R 50,1,6144
v_add_f32 v19,v19,v63
M 10,a[8:11],30
L 3
D 0,56,0
)"
R"(M 10,a[16:19],34
s_cmp_ge_u32 s61,s63
R 54,1,7168
v_lshlrev_b32 v62,16,v59
M 10,a[24:27],38
s_cselect_b32 s61,s93,s61
v_add_f32 v20,v20,v62
v_and_b32 v63,0xffff0000,v59
M 14,a[20:23],34
s_cselect_b32 s46,s88,s46
R 26,1,8192
v_add_f32 v21,v21,v63
M 14,a[28:31],38
L 3
v_lshlrev_b32 v62,16,v60
v_add_f32 v22,v22,v62
M 14,a[36:39],42
s_cselect_b32 s63,s89,s63
R 30,1,9216
v_and_b32 v63,0xffff0000,v60
M 14,a[44:47],46
D 0,56,1024
M 10,a[32:35],42
s_cselect_b32 s93,s50,s93
R 34,1,10240
v_add_f32 v23,v23,v63
M 10,a[40:43],46
L 3
v_lshlrev_b32 v62,16,v61
v_add_f32 v24,v24,v62
M 10,a[48:51],50
s_cselect_b32 s88,8,s88
R 38,1,11264
v_and_b32 v63,0xffff0000,v61
M 10,a[56:59],54
buffer_load_dwordx4 v[58:61],v0,s[36:39],s74 offen offset:0 sc1
M 14,a[52:55],50
s_cselect_b32 s89,-1,s89
R 42,1,12288
v_add_f32 v25,v25,v63
M 14,a[60:63],54
L 3
v_cvt_pk_bf16_f32 v18,v18,v19
v_cvt_pk_bf16_f32 v19,v20,v21
M 14,a[68:71],26
s_mul_i32 s58,s61,2560
R 46,1,13312
v_cvt_pk_bf16_f32 v20,v22,v23
M 14,a[76:79],30
D 0,56,2048
M 10,a[64:67],26
s_add_u32 s58,s58,109891584
R 50,1,14336
v_cvt_pk_bf16_f32 v21,v24,v25
M 10,a[72:75],30
L 3
buffer_store_dwordx4 v[18:21],v0,s[36:39],s67 offen offset:0 sc0 sc1
M 10,a[80:83],34
s_lshl_b32 s62,s54,15
s_mov_b32 s66,s65
R 54,1,15360
M 10,a[88:91],38
s_cmp_ge_u32 s66,s45
s_cselect_b32 s51,1,-1
s_add_u32 s74,s49,s62
M 14,a[84:87],34
s_add_u32 s65,s47,s62
s_mov_b32 s54,s61
R 26,1,16384
M 14,a[92:95],38
L 3
M 14,a[100:103],42
R 30,1,17408
M 14,a[108:111],46
D 0,56,3072
M 10,a[96:99],42
s_add_u32 m0,s57,4096
s_add_u32 s56,s56,4096
R 34,1,18432
M 10,a[104:107],46
L 3
M 10,a[112:115],50
R 38,1,19456
M 10,a[120:123],54
M 14,a[116:119],50
R 42,1,20480
M 14,a[124:127],54
L 3
D 0,56,0
M 14,a[132:135],26
R 46,1,21504
M 14,a[140:143],30
M 10,a[128:131],26
R 50,1,22528
M 10,a[136:139],30
L 3
M 10,a[144:147],34
R 54,1,23552
M 10,a[152:155],38
M 14,a[148:151],34
R 26,1,24576
M 14,a[156:159],38
L 3
D 0,56,1024
M 14,a[164:167],42
R 30,1,25600
M 14,a[172:175],46
M 10,a[160:163],42
R 34,1,26624
M 10,a[168:171],46
L 3
M 10,a[176:179],50
s_waitcnt vmcnt(9)
R 38,1,27648
M 10,a[184:187],54
s_barrier
M 14,a[180:183],50
R 42,1,28672
M 14,a[188:191],54
L 3
D 0,56,2048
M 14,a[196:199],26
s_add_u32 m0,s59,0
s_add_u32 s56,s55,28672
R 46,1,29696
M 14,a[204:207],30
s_mul_i32 s55,s61,114688
s_add_u32 s55,s55,s60
M 10,a[192:195],26
R 50,1,30720
M 10,a[200:203],30
L 3
M 10,a[208:211],34
R 54,1,31744
M 10,a[216:219],38
M 14,a[212:215],34
R 26,1,32768
M 14,a[220:223],38
L 3
buffer_load_dword v8,s[36:39],s58 offen offset:0 sc1 lds
M 14,a[228:231],42
s_add_u32 m0,s57,28672
R 30,1,33792
M 14,a[236:239],46
D 0,56,0
M 10,a[224:227],42
R 34,1,34816
M 10,a[232:235],46
L 3
M 10,a[240:243],50
R 38,1,35840
M 10,a[248:251],54
M 14,a[244:247],50
R 42,1,36864
M 14,a[252:255],54
L 3
D 0,56,1024
M 14,v[68:71],26
R 46,1,37888
M 14,v[76:79],30
M 10,v[64:67],26
R 50,1,38912
M 10,v[72:75],30
L 3
M 10,v[80:83],34
R 54,1,39936
M 10,v[88:91],38
D 0,56,2048
M 14,v[84:87],34
R 26,1,40960
M 14,v[92:95],38
L 3
M 14,v[100:103],42
R 30,1,41984
M 14,v[108:111],46
M 10,v[96:99],42
R 34,1,43008
M 10,v[104:107],46
L 3
D 0,56,3072
M 10,v[112:115],50
s_add_u32 m0,s57,32768
s_add_u32 s56,s56,4096
R 38,1,44032
M 10,v[120:123],54
s_sub_u32 s57,s84,s57
M 14,v[116:119],50
R 42,1,45056
M 14,v[124:127],54
L 3
M 14,v[132:135],26
R 46,1,46080
M 14,v[140:143],30
D 0,56,0
M 10,v[128:131],26
R 50,1,47104
M 10,v[136:139],30
L 3
M 10,v[144:147],34
R 54,1,48128
M 10,v[152:155],38
M 14,v[148:151],34
R 26,1,49152
M 14,v[156:159],38
L 3
D 0,56,1024
M 14,v[164:167],42
R 30,1,50176
M 14,v[172:175],46
M 10,v[160:163],42
R 34,1,51200
M 10,v[168:171],46
L 3
M 10,v[176:179],50
R 38,1,52224
M 10,v[184:187],54
D 0,56,2048
M 14,v[180:183],50
R 42,1,53248
M 14,v[188:191],54
L 3
M 14,v[196:199],26
R 46,1,54272
M 14,v[204:207],30
M 10,v[192:195],26
R 50,1,55296
M 10,v[200:203],30
L 3
M 10,v[208:211],34
s_waitcnt vmcnt(7)
R 54,1,56320
M 10,v[216:219],38
s_barrier
ds_read_b32 v7,v6 offset:0
M 14,v[212:215],34
R 26,2,0
M 14,v[220:223],38
L 4
buffer_store_dword v4,v3,s[36:39],s69 offen offset:0 sc0 sc1
M 14,v[228:231],42
s_mov_b32 s69,s68
s_lshr_b32 s68,s62,4
R 30,2,1024
M 14,v[236:239],46
s_add_u32 s68,s68,s48
M 10,v[224:227],42
R 34,2,2048
M 10,v[232:235],46
L 4
L 3
M 10,v[240:243],50
R 38,2,3072
v_cmp_eq_u32 vcc,s53,v7
M 10,v[248:251],54
M 14,v[244:247],50
L 2
s_cmp_eq_u32 vcc_lo,s51
R 42,2,4096
M 14,v[252:255],54
s_cbranch_scc0 Lg4_K1_%=
Lg4_F1_%=:
v_mfma_f32_16x16x32_bf16 v[22:25],a[4:7],v[26:29],0
s_add_u32 m0,s57,0
R 46,2,5120
v_lshlrev_b32 v62,16,v58
M 22,a[12:15],30
s_add_u32 s56,s55,0
v_add_f32 v10,v10,v62
v_and_b32 v63,0xffff0000,v58
v_mfma_f32_16x16x32_bf16 v[18:21],a[0:3],v[26:29],0
s_add_u32 s61,s54,s46
R 50,2,6144
v_add_f32 v11,v11,v63
M 18,a[8:11],30
L 3
D 0,56,0
M 18,a[16:19],34
s_cmp_ge_u32 s61,s63
R 54,2,7168
v_lshlrev_b32 v62,16,v59
M 18,a[24:27],38
s_cselect_b32 s61,s93,s61
v_add_f32 v12,v12,v62
v_and_b32 v63,0xffff0000,v59
M 22,a[20:23],34
s_cselect_b32 s46,s88,s46
R 26,2,8192
v_add_f32 v13,v13,v63
M 22,a[28:31],38
L 3
v_lshlrev_b32 v62,16,v60
v_add_f32 v14,v14,v62
M 22,a[36:39],42
s_cselect_b32 s63,s89,s63
R 30,2,9216
v_and_b32 v63,0xffff0000,v60
M 22,a[44:47],46
D 0,56,1024
M 18,a[32:35],42
s_cselect_b32 s93,s50,s93
R 34,2,10240
v_add_f32 v15,v15,v63
M 18,a[40:43],46
L 3
v_lshlrev_b32 v62,16,v61
v_add_f32 v16,v16,v62
M 18,a[48:51],50
s_cselect_b32 s88,8,s88
R 38,2,11264
v_and_b32 v63,0xffff0000,v61
M 18,a[56:59],54
buffer_load_dwordx4 v[58:61],v0,s[36:39],s74 offen offset:0 sc1
M 22,a[52:55],50
s_cselect_b32 s89,-1,s89
R 42,2,12288
v_add_f32 v17,v17,v63
M 22,a[60:63],54
L 3
v_cvt_pk_bf16_f32 v10,v10,v11
v_cvt_pk_bf16_f32 v11,v12,v13
M 22,a[68:71],26
s_mul_i32 s58,s61,2560
R 46,2,13312
v_cvt_pk_bf16_f32 v12,v14,v15
M 22,a[76:79],30
D 0,56,2048
M 18,a[64:67],26
s_add_u32 s58,s58,109891584
R 50,2,14336
v_cvt_pk_bf16_f32 v13,v16,v17
M 18,a[72:75],30
L 3
buffer_store_dwordx4 v[10:13],v0,s[36:39],s66 offen offset:0 sc0 sc1
M 18,a[80:83],34
s_lshl_b32 s62,s54,15
s_mov_b32 s67,s65
R 54,2,15360
M 18,a[88:91],38
s_cmp_ge_u32 s67,s45
s_cselect_b32 s51,1,-1
s_add_u32 s74,s49,s62
M 22,a[84:87],34
s_add_u32 s65,s47,s62
s_mov_b32 s54,s61
R 26,2,16384
M 22,a[92:95],38
L 3
M 22,a[100:103],42
R 30,2,17408
M 22,a[108:111],46
D 0,56,3072
M 18,a[96:99],42
s_add_u32 m0,s57,4096
s_add_u32 s56,s56,4096
R 34,2,18432
M 18,a[104:107],46
L 3
M 18,a[112:115],50
R 38,2,19456
M 18,a[120:123],54
M 22,a[116:119],50
R 42,2,20480
M 22,a[124:127],54
L 3
D 0,56,0
M 22,a[132:135],26
R 46,2,21504
M 22,a[140:143],30
M 18,a[128:131],26
R 50,2,22528
M 18,a[136:139],30
L 3
M 18,a[144:147],34
R 54,2,23552
M 18,a[152:155],38
M 22,a[148:151],34
R 26,2,24576
M 22,a[156:159],38
L 3
D 0,56,1024
M 22,a[164:167],42
R 30,2,25600
M 22,a[172:175],46
M 18,a[160:163],42
R 34,2,26624
M 18,a[168:171],46
L 3
M 18,a[176:179],50
s_waitcnt vmcnt(9)
R 38,2,27648
M 18,a[184:187],54
s_barrier
M 22,a[180:183],50
R 42,2,28672
M 22,a[188:191],54
L 3
D 0,56,2048
M 22,a[196:199],26
s_add_u32 m0,s59,1024
s_add_u32 s56,s55,28672
R 46,2,29696
M 22,a[204:207],30
s_mul_i32 s55,s61,114688
s_add_u32 s55,s55,s60
M 18,a[192:195],26
R 50,2,30720
M 18,a[200:203],30
L 3
M 18,a[208:211],34
R 54,2,31744
M 18,a[216:219],38
M 22,a[212:215],34
R 26,2,32768
M 22,a[220:223],38
L 3
buffer_load_dword v8,s[36:39],s58 offen offset:0 sc1 lds
M 22,a[228:231],42
s_add_u32 m0,s57,28672
R 30,2,33792
M 22,a[236:239],46
D 0,56,0
M 18,a[224:227],42
R 34,2,34816
M 18,a[232:235],46
L 3
M 18,a[240:243],50
R 38,2,35840
M 18,a[248:251],54
M 22,a[244:247],50
R 42,2,36864
M 22,a[252:255],54
L 3
D 0,56,1024
M 22,v[68:71],26
R 46,2,37888
M 22,v[76:79],30
M 18,v[64:67],26
R 50,2,38912
M 18,v[72:75],30
L 3
M 18,v[80:83],34
R 54,2,39936
M 18,v[88:91],38
D 0,56,2048
M 22,v[84:87],34
R 26,2,40960
M 22,v[92:95],38
L 3
M 22,v[100:103],42
R 30,2,41984
M 22,v[108:111],46
M 18,v[96:99],42
R 34,2,43008
M 18,v[104:107],46
L 3
D 0,56,3072
M 18,v[112:115],50
s_add_u32 m0,s57,32768
s_add_u32 s56,s56,4096
R 38,2,44032
M 18,v[120:123],54
s_sub_u32 s57,s84,s57
M 22,v[116:119],50
R 42,2,45056
M 22,v[124:127],54
L 3
M 22,v[132:135],26
R 46,2,46080
M 22,v[140:143],30
D 0,56,0
M 18,v[128:131],26
R 50,2,47104
M 18,v[136:139],30
L 3
M 18,v[144:147],34
R 54,2,48128
M 18,v[152:155],38
M 22,v[148:151],34
R 26,2,49152
M 22,v[156:159],38
L 3
D 0,56,1024
M 22,v[164:167],42
R 30,2,50176
M 22,v[172:175],46
M 18,v[160:163],42
R 34,2,51200
M 18,v[168:171],46
L 3
M 18,v[176:179],50
R 38,2,52224
M 18,v[184:187],54
D 0,56,2048
M 22,v[180:183],50
R 42,2,53248
M 22,v[188:191],54
L 3
M 22,v[196:199],26
R 46,2,54272
M 22,v[204:207],30
)"
R"(M 18,v[192:195],26
R 50,2,55296
M 18,v[200:203],30
L 3
M 18,v[208:211],34
s_waitcnt vmcnt(7)
R 54,2,56320
M 18,v[216:219],38
s_barrier
ds_read_b32 v7,v6 offset:1024
M 22,v[212:215],34
R 26,1,0
M 22,v[220:223],38
L 4
buffer_store_dword v4,v3,s[36:39],s69 offen offset:0 sc0 sc1
M 22,v[228:231],42
s_mov_b32 s69,s68
s_lshr_b32 s68,s62,4
R 30,1,1024
M 22,v[236:239],46
s_add_u32 s68,s68,s48
M 18,v[224:227],42
R 34,1,2048
M 18,v[232:235],46
L 4
L 3
M 18,v[240:243],50
R 38,1,3072
v_cmp_eq_u32 vcc,s53,v7
M 18,v[248:251],54
M 22,v[244:247],50
L 2
s_cmp_eq_u32 vcc_lo,s51
R 42,1,4096
M 22,v[252:255],54
s_cbranch_scc1 Lg4_F0_%=
Lg4_K0_%=:
s_cmp_eq_u32 s51,1
s_cbranch_scc0 Lg4_L0_%=
s_cmp_eq_u32 vcc_lo,-1
s_cbranch_scc1 Lg4_Fa0_%=
s_branch Lg4_La0_%=
Lg4_L0_%=:
v_mfma_f32_16x16x32_bf16 v[14:17],a[4:7],v[26:29],0
s_add_u32 m0,s59,3072
R 46,1,5120
v_lshlrev_b32 v62,16,v58
M 14,a[12:15],30
buffer_load_dword v8,s[36:39],s58 offen offset:0 sc1 lds
v_mfma_f32_16x16x32_bf16 v[10:13],a[0:3],v[26:29],0
s_mov_b32 s66,s65
R 50,1,6144
v_add_f32 v18,v18,v62
M 10,a[8:11],30
L 3
v_and_b32 v63,0xffff0000,v58
v_add_f32 v19,v19,v63
M 10,a[16:19],34
s_lshl_b32 s62,s54,15
R 54,1,7168
v_lshlrev_b32 v62,16,v59
M 10,a[24:27],38
s_cmp_ge_u32 s66,s45
v_add_f32 v20,v20,v62
v_and_b32 v63,0xffff0000,v59
M 14,a[20:23],34
s_cselect_b32 s51,1,-1
R 26,1,8192
v_add_f32 v21,v21,v63
M 14,a[28:31],38
L 3
v_lshlrev_b32 v62,16,v60
v_add_f32 v22,v22,v62
M 14,a[36:39],42
s_add_u32 s65,s47,s62
R 30,1,9216
v_and_b32 v63,0xffff0000,v60
M 14,a[44:47],46
v_add_f32 v23,v23,v63
v_lshlrev_b32 v62,16,v61
M 10,a[32:35],42
R 34,1,10240
v_add_f32 v24,v24,v62
M 10,a[40:43],46
L 3
v_and_b32 v63,0xffff0000,v61
v_add_f32 v25,v25,v63
M 10,a[48:51],50
R 38,1,11264
v_cvt_pk_bf16_f32 v18,v18,v19
M 10,a[56:59],54
buffer_load_dwordx4 v[58:61],v0,s[36:39],s74 offen offset:0 sc1
M 14,a[52:55],50
s_add_u32 s74,s49,s62
R 42,1,12288
v_cvt_pk_bf16_f32 v19,v20,v21
M 14,a[60:63],54
L 3
v_cvt_pk_bf16_f32 v20,v22,v23
v_cvt_pk_bf16_f32 v21,v24,v25
M 14,a[68:71],26
R 46,1,13312
M 14,a[76:79],30
buffer_store_dwordx4 v[18:21],v0,s[36:39],s67 offen offset:0 sc0 sc1
M 10,a[64:67],26
R 50,1,14336
M 10,a[72:75],30
L 3
M 10,a[80:83],34
R 54,1,15360
M 10,a[88:91],38
M 14,a[84:87],34
R 26,1,16384
M 14,a[92:95],38
L 3
M 14,a[100:103],42
R 30,1,17408
M 14,a[108:111],46
M 10,a[96:99],42
R 34,1,18432
M 10,a[104:107],46
L 3
M 10,a[112:115],50
R 38,1,19456
M 10,a[120:123],54
M 14,a[116:119],50
R 42,1,20480
M 14,a[124:127],54
L 3
M 14,a[132:135],26
R 46,1,21504
M 14,a[140:143],30
M 10,a[128:131],26
R 50,1,22528
M 10,a[136:139],30
L 3
M 10,a[144:147],34
R 54,1,23552
M 10,a[152:155],38
M 14,a[148:151],34
R 26,1,24576
M 14,a[156:159],38
L 3
M 14,a[164:167],42
R 30,1,25600
M 14,a[172:175],46
M 10,a[160:163],42
R 34,1,26624
M 10,a[168:171],46
L 3
M 10,a[176:179],50
s_waitcnt vmcnt(2)
R 38,1,27648
M 10,a[184:187],54
s_barrier
ds_read_b32 v7,v6 offset:3072
M 14,a[180:183],50
R 42,1,28672
M 14,a[188:191],54
L 4
M 14,a[196:199],26
R 46,1,29696
M 14,a[204:207],30
M 10,a[192:195],26
R 50,1,30720
M 10,a[200:203],30
L 4
L 3
v_cmp_eq_u32 vcc,s53,v7
M 10,a[208:211],34
R 54,1,31744
M 10,a[216:219],38
M 14,a[212:215],34
s_cmp_eq_u32 vcc_lo,-1
R 26,1,32768
s_cbranch_scc0 Lg4_X0_%=
M 14,a[220:223],38
L 3
s_add_u32 m0,s57,0
s_add_u32 s56,s55,0
M 14,a[228:231],42
s_add_u32 s61,s54,s46
s_cmp_ge_u32 s61,s63
s_cselect_b32 s61,s93,s61
R 30,1,33792
M 14,a[236:239],46
s_mul_i32 s58,s61,2560
D 0,56,0
M 10,a[224:227],42
s_cselect_b32 s46,s88,s46
s_cselect_b32 s63,s89,s63
R 34,1,34816
M 10,a[232:235],46
L 3
s_cselect_b32 s93,s50,s93
s_cselect_b32 s88,8,s88
M 10,a[240:243],50
s_cselect_b32 s89,-1,s89
s_add_u32 s58,s58,109891584
R 38,1,35840
M 10,a[248:251],54
D 0,56,1024
M 14,a[244:247],50
s_mov_b32 s54,s61
R 42,1,36864
M 14,a[252:255],54
L 3
M 14,v[68:71],26
R 46,1,37888
M 14,v[76:79],30
D 0,56,2048
M 10,v[64:67],26
R 50,1,38912
M 10,v[72:75],30
L 3
M 10,v[80:83],34
R 54,1,39936
M 10,v[88:91],38
D 0,56,3072
M 14,v[84:87],34
s_add_u32 m0,s57,4096
s_add_u32 s56,s56,4096
R 26,1,40960
M 14,v[92:95],38
L 3
M 14,v[100:103],42
R 30,1,41984
M 14,v[108:111],46
D 0,56,0
M 10,v[96:99],42
R 34,1,43008
M 10,v[104:107],46
L 3
M 10,v[112:115],50
R 38,1,44032
M 10,v[120:123],54
D 0,56,1024
M 14,v[116:119],50
R 42,1,45056
M 14,v[124:127],54
L 3
M 14,v[132:135],26
R 46,1,46080
M 14,v[140:143],30
D 0,56,2048
M 10,v[128:131],26
s_add_u32 m0,s59,0
s_add_u32 s56,s55,28672
R 50,1,47104
M 10,v[136:139],30
L 3
buffer_load_dword v8,s[36:39],s58 offen offset:0 sc1 lds
M 10,v[144:147],34
s_add_u32 m0,s57,28672
s_mul_i32 s55,s61,114688
R 54,1,48128
M 10,v[152:155],38
s_add_u32 s55,s55,s60
M 14,v[148:151],34
R 26,1,49152
M 14,v[156:159],38
L 3
D 0,56,0
M 14,v[164:167],42
R 30,1,50176
M 14,v[172:175],46
M 10,v[160:163],42
R 34,1,51200
M 10,v[168:171],46
L 3
D 0,56,1024
M 10,v[176:179],50
R 38,1,52224
M 10,v[184:187],54
D 0,56,2048
M 14,v[180:183],50
R 42,1,53248
M 14,v[188:191],54
L 3
M 14,v[196:199],26
R 46,1,54272
M 14,v[204:207],30
D 0,56,3072
M 10,v[192:195],26
s_add_u32 m0,s57,32768
s_add_u32 s56,s56,4096
R 50,1,55296
M 10,v[200:203],30
L 3
s_sub_u32 s57,s84,s57
M 10,v[208:211],34
s_waitcnt vmcnt(4)
R 54,1,56320
M 10,v[216:219],38
s_barrier
ds_read_b32 v7,v6 offset:0
M 14,v[212:215],34
R 26,2,0
M 14,v[220:223],38
L 4
buffer_store_dword v4,v3,s[36:39],s69 offen offset:0 sc0 sc1
M 14,v[228:231],42
s_mov_b32 s69,s68
s_lshr_b32 s68,s62,4
R 30,2,1024
M 14,v[236:239],46
D 0,56,0
M 10,v[224:227],42
s_add_u32 s68,s68,s48
R 34,2,2048
M 10,v[232:235],46
L 4
D 0,56,1024
M 10,v[240:243],50
L 3
R 38,2,3072
v_cmp_eq_u32 vcc,s53,v7
M 10,v[248:251],54
D 0,56,2048
M 14,v[244:247],50
L 2
s_cmp_eq_u32 vcc_lo,s51
R 42,2,4096
M 14,v[252:255],54
buffer_store_dword v4,v3,s[36:39],s71 offen offset:0 sc0 sc1
s_cbranch_scc1 Lg4_F1_%=
s_branch Lg4_K1_%=
Lg4_X0_%=:
M 14,a[220:223],38
L 3
s_add_u32 m0,s59,0
M 14,a[228:231],42
R 30,1,33792
M 14,a[236:239],46
buffer_load_dword v8,s[36:39],s58 offen offset:0 sc1 lds
M 10,a[224:227],42
R 34,1,34816
M 10,a[232:235],46
L 3
M 10,a[240:243],50
R 38,1,35840
M 10,a[248:251],54
M 14,a[244:247],50
R 42,1,36864
M 14,a[252:255],54
L 3
M 14,v[68:71],26
R 46,1,37888
M 14,v[76:79],30
M 10,v[64:67],26
R 50,1,38912
M 10,v[72:75],30
L 3
M 10,v[80:83],34
R 54,1,39936
M 10,v[88:91],38
M 14,v[84:87],34
R 26,1,40960
M 14,v[92:95],38
L 3
M 14,v[100:103],42
R 30,1,41984
M 14,v[108:111],46
M 10,v[96:99],42
R 34,1,43008
M 10,v[104:107],46
L 3
M 10,v[112:115],50
R 38,1,44032
M 10,v[120:123],54
M 14,v[116:119],50
R 42,1,45056
M 14,v[124:127],54
L 3
M 14,v[132:135],26
R 46,1,46080
M 14,v[140:143],30
M 10,v[128:131],26
R 50,1,47104
M 10,v[136:139],30
L 3
M 10,v[144:147],34
R 54,1,48128
M 10,v[152:155],38
M 14,v[148:151],34
R 26,1,49152
M 14,v[156:159],38
L 3
M 14,v[164:167],42
R 30,1,50176
M 14,v[172:175],46
M 10,v[160:163],42
R 34,1,51200
M 10,v[168:171],46
L 3
M 10,v[176:179],50
R 38,1,52224
M 10,v[184:187],54
M 14,v[180:183],50
R 42,1,53248
M 14,v[188:191],54
L 3
M 14,v[196:199],26
R 46,1,54272
M 14,v[204:207],30
M 10,v[192:195],26
R 50,1,55296
M 10,v[200:203],30
L 3
M 10,v[208:211],34
s_waitcnt vmcnt(0)
R 54,1,56320
M 10,v[216:219],38
s_barrier
buffer_store_dword v4,v3,s[36:39],s69 offen offset:0 sc0 sc1
M 14,v[212:215],34
s_mov_b32 s69,s68
s_lshr_b32 s68,s62,4
ds_read_b32 v7,v6 offset:0
M 14,v[220:223],38
L 3
s_add_u32 s68,s68,s48
M 14,v[228:231],42
M 14,v[236:239],46
M 10,v[224:227],42
M 10,v[232:235],46
L 1
M 10,v[240:243],50
L 0
v_cmp_eq_u32 vcc,s53,v7
M 10,v[248:251],54
M 14,v[244:247],50
M 14,v[252:255],54
s_cmp_eq_u32 s51,1
s_cbranch_scc1 Lg4_pa0_%=
s_mov_b32 s80,1
s_cmp_eq_u32 vcc_lo,-1
s_cbranch_scc0 Lg4_xn0_%=
s_add_u32 m0,s57,0
s_add_u32 s56,s55,0
D 0,56,0
v_lshlrev_b32 v62,16,v58
D 0,56,1024
v_add_f32 v10,v10,v62
D 0,56,2048
v_and_b32 v63,0xffff0000,v58
D 0,56,3072
v_add_f32 v11,v11,v63
s_add_u32 m0,s57,4096
s_add_u32 s56,s56,4096
D 0,56,0
v_lshlrev_b32 v62,16,v59
D 0,56,1024
v_add_f32 v12,v12,v62
D 0,56,2048
v_and_b32 v63,0xffff0000,v59
s_add_u32 s61,s54,s46
s_cmp_ge_u32 s61,s63
s_cselect_b32 s61,s93,s61
s_cselect_b32 s46,s88,s46
s_cselect_b32 s63,s89,s63
s_cselect_b32 s93,s50,s93
s_cselect_b32 s88,8,s88
s_cselect_b32 s89,-1,s89
s_mul_i32 s58,s61,2560
s_add_u32 s58,s58,109891584
s_add_u32 m0,s57,28672
s_add_u32 s56,s55,28672
D 0,56,0
v_add_f32 v13,v13,v63
D 0,56,1024
v_lshlrev_b32 v62,16,v60
D 0,56,2048
v_add_f32 v14,v14,v62
D 0,56,3072
v_and_b32 v63,0xffff0000,v60
s_add_u32 m0,s57,32768
s_add_u32 s56,s56,4096
D 0,56,0
v_add_f32 v15,v15,v63
D 0,56,1024
v_lshlrev_b32 v62,16,v61
D 0,56,2048
v_add_f32 v16,v16,v62
s_mul_i32 s55,s61,114688
s_add_u32 s55,s55,s60
s_sub_u32 s57,s84,s57
s_mov_b32 s54,s61
v_and_b32 v63,0xffff0000,v61
v_add_f32 v17,v17,v63
v_cvt_pk_bf16_f32 v10,v10,v11
v_cvt_pk_bf16_f32 v11,v12,v13
v_cvt_pk_bf16_f32 v12,v14,v15
v_cvt_pk_bf16_f32 v13,v16,v17
buffer_store_dwordx4 v[10:13],v0,s[36:39],s66 offen offset:0 sc0 sc1
s_mov_b32 s72,s69
s_mov_b32 s69,0x7fff0000
s_mov_b32 s66,0x7fff0000
s_mov_b32 s76,0x7fff0000
s_branch Lg4_gox_%=
)"
R"(Lg4_xn0_%=:
s_cmp_ge_u32 s54,256
s_cbranch_scc1 Lg4_fin0_%=
s_add_u32 m0,s59,4096
s_nop 0
buffer_load_dword v8,s[36:39],s58 offen offset:0 sc1 lds
buffer_store_dword v4,v3,s[36:39],s71 offen offset:0 sc0 sc1
s_add_u32 m0,s59,5120
s_nop 0
buffer_load_dword v8,s[36:39],s58 offen offset:0 sc1 lds
buffer_store_dword v4,v3,s[36:39],s71 offen offset:0 sc0 sc1
s_mov_b32 s83,0
v_lshlrev_b32 v62,16,v58
v_add_f32 v10,v10,v62
v_and_b32 v63,0xffff0000,v58
v_add_f32 v11,v11,v63
v_lshlrev_b32 v62,16,v59
v_add_f32 v12,v12,v62
v_and_b32 v63,0xffff0000,v59
v_add_f32 v13,v13,v63
v_lshlrev_b32 v62,16,v60
v_add_f32 v14,v14,v62
v_and_b32 v63,0xffff0000,v60
v_add_f32 v15,v15,v63
v_lshlrev_b32 v62,16,v61
v_add_f32 v16,v16,v62
v_and_b32 v63,0xffff0000,v61
v_add_f32 v17,v17,v63
v_cvt_pk_bf16_f32 v10,v10,v11
v_cvt_pk_bf16_f32 v11,v12,v13
v_cvt_pk_bf16_f32 v12,v14,v15
v_cvt_pk_bf16_f32 v13,v16,v17
buffer_store_dwordx4 v[10:13],v0,s[36:39],s66 offen offset:0 sc0 sc1
s_mov_b32 s72,s69
s_mov_b32 s69,0x7fff0000
s_mov_b32 s66,0x7fff0000
s_mov_b32 s76,0x7fff0000
s_branch Lg4_poll_%=
Lg4_fin0_%=:
v_lshlrev_b32 v62,16,v58
v_add_f32 v10,v10,v62
v_and_b32 v63,0xffff0000,v58
v_add_f32 v11,v11,v63
v_lshlrev_b32 v62,16,v59
v_add_f32 v12,v12,v62
v_and_b32 v63,0xffff0000,v59
v_add_f32 v13,v13,v63
v_lshlrev_b32 v62,16,v60
v_add_f32 v14,v14,v62
v_and_b32 v63,0xffff0000,v60
v_add_f32 v15,v15,v63
v_lshlrev_b32 v62,16,v61
v_add_f32 v16,v16,v62
v_and_b32 v63,0xffff0000,v61
v_add_f32 v17,v17,v63
v_cvt_pk_bf16_f32 v10,v10,v11
v_cvt_pk_bf16_f32 v11,v12,v13
v_cvt_pk_bf16_f32 v12,v14,v15
v_cvt_pk_bf16_f32 v13,v16,v17
buffer_store_dwordx4 v[10:13],v0,s[36:39],s66 offen offset:0 sc0 sc1
s_mov_b32 s72,s69
s_mov_b32 s69,0x7fff0000
s_mov_b32 s66,0x7fff0000
s_mov_b32 s76,0x7fff0000
s_branch Lg4_done_%=
Lg4_K1_%=:
s_cmp_eq_u32 s51,1
s_cbranch_scc0 Lg4_L1_%=
s_cmp_eq_u32 vcc_lo,-1
s_cbranch_scc1 Lg4_Fa1_%=
s_branch Lg4_La1_%=
Lg4_L1_%=:
v_mfma_f32_16x16x32_bf16 v[22:25],a[4:7],v[26:29],0
s_add_u32 m0,s59,3072
R 46,2,5120
v_lshlrev_b32 v62,16,v58
M 22,a[12:15],30
buffer_load_dword v8,s[36:39],s58 offen offset:0 sc1 lds
v_mfma_f32_16x16x32_bf16 v[18:21],a[0:3],v[26:29],0
s_mov_b32 s67,s65
R 50,2,6144
v_add_f32 v10,v10,v62
M 18,a[8:11],30
L 3
v_and_b32 v63,0xffff0000,v58
v_add_f32 v11,v11,v63
M 18,a[16:19],34
s_lshl_b32 s62,s54,15
R 54,2,7168
v_lshlrev_b32 v62,16,v59
M 18,a[24:27],38
s_cmp_ge_u32 s67,s45
v_add_f32 v12,v12,v62
v_and_b32 v63,0xffff0000,v59
M 22,a[20:23],34
s_cselect_b32 s51,1,-1
R 26,2,8192
v_add_f32 v13,v13,v63
M 22,a[28:31],38
L 3
v_lshlrev_b32 v62,16,v60
v_add_f32 v14,v14,v62
M 22,a[36:39],42
s_add_u32 s65,s47,s62
R 30,2,9216
v_and_b32 v63,0xffff0000,v60
M 22,a[44:47],46
v_add_f32 v15,v15,v63
v_lshlrev_b32 v62,16,v61
M 18,a[32:35],42
R 34,2,10240
v_add_f32 v16,v16,v62
M 18,a[40:43],46
L 3
v_and_b32 v63,0xffff0000,v61
v_add_f32 v17,v17,v63
M 18,a[48:51],50
R 38,2,11264
v_cvt_pk_bf16_f32 v10,v10,v11
M 18,a[56:59],54
buffer_load_dwordx4 v[58:61],v0,s[36:39],s74 offen offset:0 sc1
M 22,a[52:55],50
s_add_u32 s74,s49,s62
R 42,2,12288
v_cvt_pk_bf16_f32 v11,v12,v13
M 22,a[60:63],54
L 3
v_cvt_pk_bf16_f32 v12,v14,v15
v_cvt_pk_bf16_f32 v13,v16,v17
M 22,a[68:71],26
R 46,2,13312
M 22,a[76:79],30
buffer_store_dwordx4 v[10:13],v0,s[36:39],s66 offen offset:0 sc0 sc1
M 18,a[64:67],26
R 50,2,14336
M 18,a[72:75],30
L 3
M 18,a[80:83],34
R 54,2,15360
M 18,a[88:91],38
M 22,a[84:87],34
R 26,2,16384
M 22,a[92:95],38
L 3
M 22,a[100:103],42
R 30,2,17408
M 22,a[108:111],46
M 18,a[96:99],42
R 34,2,18432
M 18,a[104:107],46
L 3
M 18,a[112:115],50
R 38,2,19456
M 18,a[120:123],54
M 22,a[116:119],50
R 42,2,20480
M 22,a[124:127],54
L 3
M 22,a[132:135],26
R 46,2,21504
M 22,a[140:143],30
M 18,a[128:131],26
R 50,2,22528
M 18,a[136:139],30
L 3
M 18,a[144:147],34
R 54,2,23552
M 18,a[152:155],38
M 22,a[148:151],34
R 26,2,24576
M 22,a[156:159],38
L 3
M 22,a[164:167],42
R 30,2,25600
M 22,a[172:175],46
M 18,a[160:163],42
R 34,2,26624
M 18,a[168:171],46
L 3
M 18,a[176:179],50
s_waitcnt vmcnt(2)
R 38,2,27648
M 18,a[184:187],54
s_barrier
ds_read_b32 v7,v6 offset:3072
M 22,a[180:183],50
R 42,2,28672
M 22,a[188:191],54
L 4
M 22,a[196:199],26
R 46,2,29696
M 22,a[204:207],30
M 18,a[192:195],26
R 50,2,30720
M 18,a[200:203],30
L 4
L 3
v_cmp_eq_u32 vcc,s53,v7
M 18,a[208:211],34
R 54,2,31744
M 18,a[216:219],38
M 22,a[212:215],34
s_cmp_eq_u32 vcc_lo,-1
R 26,2,32768
s_cbranch_scc0 Lg4_X1_%=
M 22,a[220:223],38
L 3
s_add_u32 m0,s57,0
s_add_u32 s56,s55,0
M 22,a[228:231],42
s_add_u32 s61,s54,s46
s_cmp_ge_u32 s61,s63
s_cselect_b32 s61,s93,s61
R 30,2,33792
M 22,a[236:239],46
s_mul_i32 s58,s61,2560
D 0,56,0
M 18,a[224:227],42
s_cselect_b32 s46,s88,s46
s_cselect_b32 s63,s89,s63
R 34,2,34816
M 18,a[232:235],46
L 3
s_cselect_b32 s93,s50,s93
s_cselect_b32 s88,8,s88
M 18,a[240:243],50
s_cselect_b32 s89,-1,s89
s_add_u32 s58,s58,109891584
R 38,2,35840
M 18,a[248:251],54
D 0,56,1024
M 22,a[244:247],50
s_mov_b32 s54,s61
R 42,2,36864
M 22,a[252:255],54
L 3
M 22,v[68:71],26
R 46,2,37888
M 22,v[76:79],30
D 0,56,2048
M 18,v[64:67],26
R 50,2,38912
M 18,v[72:75],30
L 3
M 18,v[80:83],34
R 54,2,39936
M 18,v[88:91],38
D 0,56,3072
M 22,v[84:87],34
s_add_u32 m0,s57,4096
s_add_u32 s56,s56,4096
R 26,2,40960
M 22,v[92:95],38
L 3
M 22,v[100:103],42
R 30,2,41984
M 22,v[108:111],46
D 0,56,0
M 18,v[96:99],42
R 34,2,43008
M 18,v[104:107],46
L 3
M 18,v[112:115],50
R 38,2,44032
M 18,v[120:123],54
D 0,56,1024
M 22,v[116:119],50
R 42,2,45056
M 22,v[124:127],54
L 3
M 22,v[132:135],26
R 46,2,46080
M 22,v[140:143],30
D 0,56,2048
M 18,v[128:131],26
s_add_u32 m0,s59,1024
s_add_u32 s56,s55,28672
R 50,2,47104
M 18,v[136:139],30
L 3
buffer_load_dword v8,s[36:39],s58 offen offset:0 sc1 lds
M 18,v[144:147],34
s_add_u32 m0,s57,28672
s_mul_i32 s55,s61,114688
R 54,2,48128
M 18,v[152:155],38
s_add_u32 s55,s55,s60
M 22,v[148:151],34
R 26,2,49152
M 22,v[156:159],38
L 3
D 0,56,0
M 22,v[164:167],42
R 30,2,50176
M 22,v[172:175],46
M 18,v[160:163],42
R 34,2,51200
M 18,v[168:171],46
L 3
D 0,56,1024
M 18,v[176:179],50
R 38,2,52224
M 18,v[184:187],54
D 0,56,2048
M 22,v[180:183],50
R 42,2,53248
M 22,v[188:191],54
L 3
M 22,v[196:199],26
R 46,2,54272
M 22,v[204:207],30
D 0,56,3072
M 18,v[192:195],26
s_add_u32 m0,s57,32768
s_add_u32 s56,s56,4096
R 50,2,55296
M 18,v[200:203],30
L 3
s_sub_u32 s57,s84,s57
M 18,v[208:211],34
s_waitcnt vmcnt(4)
R 54,2,56320
M 18,v[216:219],38
s_barrier
ds_read_b32 v7,v6 offset:1024
M 22,v[212:215],34
R 26,1,0
M 22,v[220:223],38
L 4
buffer_store_dword v4,v3,s[36:39],s69 offen offset:0 sc0 sc1
M 22,v[228:231],42
s_mov_b32 s69,s68
s_lshr_b32 s68,s62,4
R 30,1,1024
M 22,v[236:239],46
D 0,56,0
M 18,v[224:227],42
s_add_u32 s68,s68,s48
R 34,1,2048
M 18,v[232:235],46
L 4
D 0,56,1024
M 18,v[240:243],50
L 3
R 38,1,3072
v_cmp_eq_u32 vcc,s53,v7
M 18,v[248:251],54
D 0,56,2048
M 22,v[244:247],50
L 2
s_cmp_eq_u32 vcc_lo,s51
R 42,1,4096
M 22,v[252:255],54
buffer_store_dword v4,v3,s[36:39],s71 offen offset:0 sc0 sc1
s_cbranch_scc1 Lg4_F0_%=
s_branch Lg4_K0_%=
Lg4_X1_%=:
M 22,a[220:223],38
L 3
s_add_u32 m0,s59,1024
M 22,a[228:231],42
R 30,2,33792
M 22,a[236:239],46
buffer_load_dword v8,s[36:39],s58 offen offset:0 sc1 lds
M 18,a[224:227],42
R 34,2,34816
M 18,a[232:235],46
L 3
M 18,a[240:243],50
R 38,2,35840
M 18,a[248:251],54
M 22,a[244:247],50
R 42,2,36864
M 22,a[252:255],54
L 3
M 22,v[68:71],26
R 46,2,37888
M 22,v[76:79],30
M 18,v[64:67],26
R 50,2,38912
M 18,v[72:75],30
L 3
M 18,v[80:83],34
R 54,2,39936
M 18,v[88:91],38
M 22,v[84:87],34
R 26,2,40960
M 22,v[92:95],38
L 3
M 22,v[100:103],42
R 30,2,41984
M 22,v[108:111],46
M 18,v[96:99],42
R 34,2,43008
M 18,v[104:107],46
L 3
M 18,v[112:115],50
R 38,2,44032
M 18,v[120:123],54
M 22,v[116:119],50
R 42,2,45056
M 22,v[124:127],54
L 3
M 22,v[132:135],26
R 46,2,46080
M 22,v[140:143],30
M 18,v[128:131],26
R 50,2,47104
M 18,v[136:139],30
L 3
M 18,v[144:147],34
R 54,2,48128
M 18,v[152:155],38
M 22,v[148:151],34
R 26,2,49152
M 22,v[156:159],38
L 3
M 22,v[164:167],42
R 30,2,50176
M 22,v[172:175],46
M 18,v[160:163],42
R 34,2,51200
M 18,v[168:171],46
L 3
M 18,v[176:179],50
R 38,2,52224
M 18,v[184:187],54
M 22,v[180:183],50
R 42,2,53248
M 22,v[188:191],54
L 3
M 22,v[196:199],26
R 46,2,54272
M 22,v[204:207],30
M 18,v[192:195],26
R 50,2,55296
M 18,v[200:203],30
L 3
M 18,v[208:211],34
s_waitcnt vmcnt(0)
R 54,2,56320
M 18,v[216:219],38
s_barrier
buffer_store_dword v4,v3,s[36:39],s69 offen offset:0 sc0 sc1
M 22,v[212:215],34
s_mov_b32 s69,s68
s_lshr_b32 s68,s62,4
ds_read_b32 v7,v6 offset:1024
M 22,v[220:223],38
L 3
s_add_u32 s68,s68,s48
M 22,v[228:231],42
M 22,v[236:239],46
M 18,v[224:227],42
M 18,v[232:235],46
L 1
M 18,v[240:243],50
L 0
v_cmp_eq_u32 vcc,s53,v7
M 18,v[248:251],54
M 22,v[244:247],50
M 22,v[252:255],54
s_cmp_eq_u32 s51,1
s_cbranch_scc1 Lg4_pa1_%=
s_mov_b32 s80,0
s_cmp_eq_u32 vcc_lo,-1
s_cbranch_scc0 Lg4_xn1_%=
s_add_u32 m0,s57,0
s_add_u32 s56,s55,0
D 0,56,0
v_lshlrev_b32 v62,16,v58
D 0,56,1024
v_add_f32 v18,v18,v62
D 0,56,2048
v_and_b32 v63,0xffff0000,v58
D 0,56,3072
v_add_f32 v19,v19,v63
s_add_u32 m0,s57,4096
s_add_u32 s56,s56,4096
D 0,56,0
v_lshlrev_b32 v62,16,v59
D 0,56,1024
v_add_f32 v20,v20,v62
D 0,56,2048
v_and_b32 v63,0xffff0000,v59
s_add_u32 s61,s54,s46
s_cmp_ge_u32 s61,s63
s_cselect_b32 s61,s93,s61
s_cselect_b32 s46,s88,s46
s_cselect_b32 s63,s89,s63
s_cselect_b32 s93,s50,s93
s_cselect_b32 s88,8,s88
s_cselect_b32 s89,-1,s89
s_mul_i32 s58,s61,2560
s_add_u32 s58,s58,109891584
s_add_u32 m0,s57,28672
s_add_u32 s56,s55,28672
D 0,56,0
v_add_f32 v21,v21,v63
D 0,56,1024
)"
R"(v_lshlrev_b32 v62,16,v60
D 0,56,2048
v_add_f32 v22,v22,v62
D 0,56,3072
v_and_b32 v63,0xffff0000,v60
s_add_u32 m0,s57,32768
s_add_u32 s56,s56,4096
D 0,56,0
v_add_f32 v23,v23,v63
D 0,56,1024
v_lshlrev_b32 v62,16,v61
D 0,56,2048
v_add_f32 v24,v24,v62
s_mul_i32 s55,s61,114688
s_add_u32 s55,s55,s60
s_sub_u32 s57,s84,s57
s_mov_b32 s54,s61
v_and_b32 v63,0xffff0000,v61
v_add_f32 v25,v25,v63
v_cvt_pk_bf16_f32 v18,v18,v19
v_cvt_pk_bf16_f32 v19,v20,v21
v_cvt_pk_bf16_f32 v20,v22,v23
v_cvt_pk_bf16_f32 v21,v24,v25
buffer_store_dwordx4 v[18:21],v0,s[36:39],s67 offen offset:0 sc0 sc1
s_mov_b32 s72,s69
s_mov_b32 s69,0x7fff0000
s_mov_b32 s67,0x7fff0000
s_mov_b32 s77,0x7fff0000
s_branch Lg4_gox_%=
Lg4_xn1_%=:
s_cmp_ge_u32 s54,256
s_cbranch_scc1 Lg4_fin1_%=
s_add_u32 m0,s59,4096
s_nop 0
buffer_load_dword v8,s[36:39],s58 offen offset:0 sc1 lds
buffer_store_dword v4,v3,s[36:39],s71 offen offset:0 sc0 sc1
s_add_u32 m0,s59,5120
s_nop 0
buffer_load_dword v8,s[36:39],s58 offen offset:0 sc1 lds
buffer_store_dword v4,v3,s[36:39],s71 offen offset:0 sc0 sc1
s_mov_b32 s83,0
v_lshlrev_b32 v62,16,v58
v_add_f32 v18,v18,v62
v_and_b32 v63,0xffff0000,v58
v_add_f32 v19,v19,v63
v_lshlrev_b32 v62,16,v59
v_add_f32 v20,v20,v62
v_and_b32 v63,0xffff0000,v59
v_add_f32 v21,v21,v63
v_lshlrev_b32 v62,16,v60
v_add_f32 v22,v22,v62
v_and_b32 v63,0xffff0000,v60
v_add_f32 v23,v23,v63
v_lshlrev_b32 v62,16,v61
v_add_f32 v24,v24,v62
v_and_b32 v63,0xffff0000,v61
v_add_f32 v25,v25,v63
v_cvt_pk_bf16_f32 v18,v18,v19
v_cvt_pk_bf16_f32 v19,v20,v21
v_cvt_pk_bf16_f32 v20,v22,v23
v_cvt_pk_bf16_f32 v21,v24,v25
buffer_store_dwordx4 v[18:21],v0,s[36:39],s67 offen offset:0 sc0 sc1
s_mov_b32 s72,s69
s_mov_b32 s69,0x7fff0000
s_mov_b32 s67,0x7fff0000
s_mov_b32 s77,0x7fff0000
s_branch Lg4_poll_%=
Lg4_fin1_%=:
v_lshlrev_b32 v62,16,v58
v_add_f32 v18,v18,v62
v_and_b32 v63,0xffff0000,v58
v_add_f32 v19,v19,v63
v_lshlrev_b32 v62,16,v59
v_add_f32 v20,v20,v62
v_and_b32 v63,0xffff0000,v59
v_add_f32 v21,v21,v63
v_lshlrev_b32 v62,16,v60
v_add_f32 v22,v22,v62
v_and_b32 v63,0xffff0000,v60
v_add_f32 v23,v23,v63
v_lshlrev_b32 v62,16,v61
v_add_f32 v24,v24,v62
v_and_b32 v63,0xffff0000,v61
v_add_f32 v25,v25,v63
v_cvt_pk_bf16_f32 v18,v18,v19
v_cvt_pk_bf16_f32 v19,v20,v21
v_cvt_pk_bf16_f32 v20,v22,v23
v_cvt_pk_bf16_f32 v21,v24,v25
buffer_store_dwordx4 v[18:21],v0,s[36:39],s67 offen offset:0 sc0 sc1
s_mov_b32 s72,s69
s_mov_b32 s69,0x7fff0000
s_mov_b32 s67,0x7fff0000
s_mov_b32 s77,0x7fff0000
s_branch Lg4_done_%=
Lg4_Fa0_%=:
s_nop 0
v_mfma_f32_16x16x32_bf16 v[14:17],a[4:7],v[26:29],0
s_add_u32 m0,s57,0
R 46,1,5120
v_lshlrev_b32 v62,16,v58
M 14,a[12:15],30
s_add_u32 s56,s55,0
v_add_f32 v18,v18,v62
v_and_b32 v63,0xffff0000,v58
v_mfma_f32_16x16x32_bf16 v[10:13],a[0:3],v[26:29],0
s_add_u32 s61,s54,s46
R 50,1,6144
v_add_f32 v19,v19,v63
M 10,a[8:11],30
L 3
D 0,56,0
M 10,a[16:19],34
s_cmp_ge_u32 s61,s63
R 54,1,7168
v_lshlrev_b32 v62,16,v59
M 10,a[24:27],38
s_cselect_b32 s61,s93,s61
v_add_f32 v20,v20,v62
v_and_b32 v63,0xffff0000,v59
M 14,a[20:23],34
s_cselect_b32 s46,s88,s46
R 26,1,8192
v_add_f32 v21,v21,v63
M 14,a[28:31],38
L 3
v_lshlrev_b32 v62,16,v60
v_add_f32 v22,v22,v62
M 14,a[36:39],42
s_cselect_b32 s63,s89,s63
R 30,1,9216
v_and_b32 v63,0xffff0000,v60
M 14,a[44:47],46
D 0,56,1024
M 10,a[32:35],42
s_cselect_b32 s93,s50,s93
R 34,1,10240
v_add_f32 v23,v23,v63
M 10,a[40:43],46
L 3
v_lshlrev_b32 v62,16,v61
v_add_f32 v24,v24,v62
M 10,a[48:51],50
s_cselect_b32 s88,8,s88
R 38,1,11264
v_and_b32 v63,0xffff0000,v61
M 10,a[56:59],54
buffer_load_dwordx4 v[58:61],v0,s[36:39],s74 offen offset:0 sc1
M 14,a[52:55],50
s_cselect_b32 s89,-1,s89
R 42,1,12288
v_add_f32 v25,v25,v63
M 14,a[60:63],54
L 3
v_cvt_pk_bf16_f32 v18,v18,v19
v_cvt_pk_bf16_f32 v19,v20,v21
M 14,a[68:71],26
s_mul_i32 s58,s61,2560
R 46,1,13312
v_cvt_pk_bf16_f32 v20,v22,v23
M 14,a[76:79],30
D 0,56,2048
M 10,a[64:67],26
s_add_u32 s58,s58,109891584
R 50,1,14336
v_cvt_pk_bf16_f32 v21,v24,v25
M 10,a[72:75],30
L 3
v_lshrrev_b32 v62,8,v0
v_lshl_add_u32 v62,v62,5,v0
M 10,a[80:83],34
s_sub_u32 s75,s67,s85
R 54,1,15360
v_add_u32 v62,s73,v62
M 10,a[88:91],38
s_mov_b64 s[40:41],s[86:87]
ds_write_b128 v62,v[18:21] offset:0
M 14,a[84:87],34
s_mov_b32 s42,4194304
R 26,1,16384
ds_read2_b32 v[22:23],v5 offset0:0 offset1:16
M 14,a[92:95],38
L 5
s_mov_b32 s43,0x00020000
ds_read2_b32 v[24:25],v5 offset0:32 offset1:48
M 14,a[100:103],42
s_add_u32 s64,s75,0
s_lshl_b32 s62,s54,15
R 30,1,17408
M 14,a[108:111],46
D 0,56,3072
M 10,a[96:99],42
s_add_u32 m0,s57,4096
s_add_u32 s56,s56,4096
R 34,1,18432
M 10,a[104:107],46
L 6
s_mov_b32 s66,s65
s_add_u32 s74,s49,s62
M 10,a[112:115],50
s_add_u32 s65,s47,s62
s_mov_b32 s54,s61
R 38,1,19456
M 10,a[120:123],54
L 3
M 14,a[116:119],50
R 42,1,20480
M 14,a[124:127],54
L 3
D 0,56,0
M 14,a[132:135],26
R 46,1,21504
M 14,a[140:143],30
M 10,a[128:131],26
R 50,1,22528
M 10,a[136:139],30
L 3
M 10,a[144:147],34
R 54,1,23552
M 10,a[152:155],38
M 14,a[148:151],34
R 26,1,24576
M 14,a[156:159],38
L 3
D 0,56,1024
M 14,a[164:167],42
R 30,1,25600
M 14,a[172:175],46
M 10,a[160:163],42
R 34,1,26624
M 10,a[168:171],46
L 3
M 10,a[176:179],50
s_waitcnt vmcnt(8)
R 38,1,27648
M 10,a[184:187],54
s_barrier
M 14,a[180:183],50
R 42,1,28672
M 14,a[188:191],54
L 3
D 0,56,2048
M 14,a[196:199],26
s_add_u32 m0,s59,0
s_add_u32 s56,s55,28672
R 46,1,29696
M 14,a[204:207],30
s_mul_i32 s55,s61,114688
s_add_u32 s55,s55,s60
M 10,a[192:195],26
R 50,1,30720
M 10,a[200:203],30
L 3
M 10,a[208:211],34
R 54,1,31744
M 10,a[216:219],38
M 14,a[212:215],34
R 26,1,32768
M 14,a[220:223],38
L 3
buffer_load_dword v8,s[36:39],s58 offen offset:0 sc1 lds
M 14,a[228:231],42
s_add_u32 m0,s57,28672
R 30,1,33792
M 14,a[236:239],46
D 0,56,0
M 10,a[224:227],42
R 34,1,34816
M 10,a[232:235],46
L 3
M 10,a[240:243],50
R 38,1,35840
M 10,a[248:251],54
M 14,a[244:247],50
R 42,1,36864
M 14,a[252:255],54
L 3
D 0,56,1024
M 14,v[68:71],26
R 46,1,37888
M 14,v[76:79],30
M 10,v[64:67],26
R 50,1,38912
M 10,v[72:75],30
L 3
M 10,v[80:83],34
R 54,1,39936
M 10,v[88:91],38
D 0,56,2048
M 14,v[84:87],34
R 26,1,40960
M 14,v[92:95],38
L 3
M 14,v[100:103],42
R 30,1,41984
M 14,v[108:111],46
M 10,v[96:99],42
R 34,1,43008
M 10,v[104:107],46
L 3
D 0,56,3072
M 10,v[112:115],50
s_add_u32 m0,s57,32768
s_add_u32 s56,s56,4096
R 38,1,44032
M 10,v[120:123],54
s_sub_u32 s57,s84,s57
M 14,v[116:119],50
R 42,1,45056
M 14,v[124:127],54
L 3
M 14,v[132:135],26
R 46,1,46080
M 14,v[140:143],30
D 0,56,0
M 10,v[128:131],26
R 50,1,47104
M 10,v[136:139],30
L 3
M 10,v[144:147],34
R 54,1,48128
M 10,v[152:155],38
M 14,v[148:151],34
R 26,1,49152
M 14,v[156:159],38
L 3
D 0,56,1024
M 14,v[164:167],42
R 30,1,50176
M 14,v[172:175],46
M 10,v[160:163],42
R 34,1,51200
M 10,v[168:171],46
L 3
M 10,v[176:179],50
R 38,1,52224
M 10,v[184:187],54
D 0,56,2048
M 14,v[180:183],50
R 42,1,53248
M 14,v[188:191],54
L 3
M 14,v[196:199],26
R 46,1,54272
M 14,v[204:207],30
M 10,v[192:195],26
R 50,1,55296
M 10,v[200:203],30
L 3
M 10,v[208:211],34
s_waitcnt vmcnt(7)
R 54,1,56320
M 10,v[216:219],38
s_barrier
ds_read_b32 v7,v6 offset:0
M 14,v[212:215],34
R 26,2,0
M 14,v[220:223],38
L 4
M 14,v[228:231],42
R 30,2,1024
M 14,v[236:239],46
buffer_atomic_pk_add_bf16 v22,v9,s[40:43],s64 offen sc1
M 10,v[224:227],42
s_add_u32 s64,s75,8192
R 34,2,2048
M 10,v[232:235],46
L 4
buffer_atomic_pk_add_bf16 v23,v9,s[40:43],s64 offen sc1
M 10,v[240:243],50
s_add_u32 s64,s75,16384
L 3
R 38,2,3072
v_cmp_eq_u32 vcc,s53,v7
M 10,v[248:251],54
buffer_atomic_pk_add_bf16 v24,v9,s[40:43],s64 offen sc1
M 14,v[244:247],50
L 2
s_add_u32 s64,s75,24576
s_cmp_eq_u32 vcc_lo,-1
R 42,2,4096
M 14,v[252:255],54
buffer_atomic_pk_add_bf16 v25,v9,s[40:43],s64 offen sc1
s_cbranch_scc0 Lg4_Ka1_%=
Lg4_Fa1_%=:
s_nop 0
v_mfma_f32_16x16x32_bf16 v[22:25],a[4:7],v[26:29],0
s_add_u32 m0,s57,0
R 46,2,5120
v_lshlrev_b32 v62,16,v58
M 22,a[12:15],30
s_add_u32 s56,s55,0
v_add_f32 v10,v10,v62
v_and_b32 v63,0xffff0000,v58
v_mfma_f32_16x16x32_bf16 v[18:21],a[0:3],v[26:29],0
s_add_u32 s61,s54,s46
R 50,2,6144
v_add_f32 v11,v11,v63
M 18,a[8:11],30
L 3
D 0,56,0
M 18,a[16:19],34
s_cmp_ge_u32 s61,s63
R 54,2,7168
v_lshlrev_b32 v62,16,v59
M 18,a[24:27],38
s_cselect_b32 s61,s93,s61
v_add_f32 v12,v12,v62
v_and_b32 v63,0xffff0000,v59
M 22,a[20:23],34
s_cselect_b32 s46,s88,s46
R 26,2,8192
v_add_f32 v13,v13,v63
M 22,a[28:31],38
L 3
v_lshlrev_b32 v62,16,v60
v_add_f32 v14,v14,v62
M 22,a[36:39],42
s_cselect_b32 s63,s89,s63
R 30,2,9216
v_and_b32 v63,0xffff0000,v60
M 22,a[44:47],46
D 0,56,1024
M 18,a[32:35],42
s_cselect_b32 s93,s50,s93
R 34,2,10240
v_add_f32 v15,v15,v63
M 18,a[40:43],46
L 3
v_lshlrev_b32 v62,16,v61
v_add_f32 v16,v16,v62
M 18,a[48:51],50
s_cselect_b32 s88,8,s88
R 38,2,11264
v_and_b32 v63,0xffff0000,v61
M 18,a[56:59],54
buffer_load_dwordx4 v[58:61],v0,s[36:39],s74 offen offset:0 sc1
M 22,a[52:55],50
s_cselect_b32 s89,-1,s89
R 42,2,12288
v_add_f32 v17,v17,v63
M 22,a[60:63],54
L 3
v_cvt_pk_bf16_f32 v10,v10,v11
v_cvt_pk_bf16_f32 v11,v12,v13
M 22,a[68:71],26
s_mul_i32 s58,s61,2560
R 46,2,13312
v_cvt_pk_bf16_f32 v12,v14,v15
M 22,a[76:79],30
D 0,56,2048
M 18,a[64:67],26
s_add_u32 s58,s58,109891584
R 50,2,14336
v_cvt_pk_bf16_f32 v13,v16,v17
M 18,a[72:75],30
L 3
v_lshrrev_b32 v62,8,v0
v_lshl_add_u32 v62,v62,5,v0
M 18,a[80:83],34
s_sub_u32 s75,s66,s85
R 54,2,15360
v_add_u32 v62,s73,v62
M 18,a[88:91],38
s_mov_b64 s[40:41],s[86:87]
ds_write_b128 v62,v[10:13] offset:0
M 22,a[84:87],34
s_mov_b32 s42,4194304
R 26,2,16384
ds_read2_b32 v[14:15],v5 offset0:0 offset1:16
M 22,a[92:95],38
L 5
s_mov_b32 s43,0x00020000
ds_read2_b32 v[16:17],v5 offset0:32 offset1:48
M 22,a[100:103],42
s_add_u32 s64,s75,0
s_lshl_b32 s62,s54,15
R 30,2,17408
M 22,a[108:111],46
D 0,56,3072
M 18,a[96:99],42
s_add_u32 m0,s57,4096
s_add_u32 s56,s56,4096
R 34,2,18432
M 18,a[104:107],46
L 6
s_mov_b32 s67,s65
s_add_u32 s74,s49,s62
M 18,a[112:115],50
s_add_u32 s65,s47,s62
s_mov_b32 s54,s61
R 38,2,19456
M 18,a[120:123],54
L 3
M 22,a[116:119],50
R 42,2,20480
M 22,a[124:127],54
L 3
D 0,56,0
M 22,a[132:135],26
R 46,2,21504
)"
R"(M 22,a[140:143],30
M 18,a[128:131],26
R 50,2,22528
M 18,a[136:139],30
L 3
M 18,a[144:147],34
R 54,2,23552
M 18,a[152:155],38
M 22,a[148:151],34
R 26,2,24576
M 22,a[156:159],38
L 3
D 0,56,1024
M 22,a[164:167],42
R 30,2,25600
M 22,a[172:175],46
M 18,a[160:163],42
R 34,2,26624
M 18,a[168:171],46
L 3
M 18,a[176:179],50
s_waitcnt vmcnt(8)
R 38,2,27648
M 18,a[184:187],54
s_barrier
M 22,a[180:183],50
R 42,2,28672
M 22,a[188:191],54
L 3
D 0,56,2048
M 22,a[196:199],26
s_add_u32 m0,s59,1024
s_add_u32 s56,s55,28672
R 46,2,29696
M 22,a[204:207],30
s_mul_i32 s55,s61,114688
s_add_u32 s55,s55,s60
M 18,a[192:195],26
R 50,2,30720
M 18,a[200:203],30
L 3
M 18,a[208:211],34
R 54,2,31744
M 18,a[216:219],38
M 22,a[212:215],34
R 26,2,32768
M 22,a[220:223],38
L 3
buffer_load_dword v8,s[36:39],s58 offen offset:0 sc1 lds
M 22,a[228:231],42
s_add_u32 m0,s57,28672
R 30,2,33792
M 22,a[236:239],46
D 0,56,0
M 18,a[224:227],42
R 34,2,34816
M 18,a[232:235],46
L 3
M 18,a[240:243],50
R 38,2,35840
M 18,a[248:251],54
M 22,a[244:247],50
R 42,2,36864
M 22,a[252:255],54
L 3
D 0,56,1024
M 22,v[68:71],26
R 46,2,37888
M 22,v[76:79],30
M 18,v[64:67],26
R 50,2,38912
M 18,v[72:75],30
L 3
M 18,v[80:83],34
R 54,2,39936
M 18,v[88:91],38
D 0,56,2048
M 22,v[84:87],34
R 26,2,40960
M 22,v[92:95],38
L 3
M 22,v[100:103],42
R 30,2,41984
M 22,v[108:111],46
M 18,v[96:99],42
R 34,2,43008
M 18,v[104:107],46
L 3
D 0,56,3072
M 18,v[112:115],50
s_add_u32 m0,s57,32768
s_add_u32 s56,s56,4096
R 38,2,44032
M 18,v[120:123],54
s_sub_u32 s57,s84,s57
M 22,v[116:119],50
R 42,2,45056
M 22,v[124:127],54
L 3
M 22,v[132:135],26
R 46,2,46080
M 22,v[140:143],30
D 0,56,0
M 18,v[128:131],26
R 50,2,47104
M 18,v[136:139],30
L 3
M 18,v[144:147],34
R 54,2,48128
M 18,v[152:155],38
M 22,v[148:151],34
R 26,2,49152
M 22,v[156:159],38
L 3
D 0,56,1024
M 22,v[164:167],42
R 30,2,50176
M 22,v[172:175],46
M 18,v[160:163],42
R 34,2,51200
M 18,v[168:171],46
L 3
M 18,v[176:179],50
R 38,2,52224
M 18,v[184:187],54
D 0,56,2048
M 22,v[180:183],50
R 42,2,53248
M 22,v[188:191],54
L 3
M 22,v[196:199],26
R 46,2,54272
M 22,v[204:207],30
M 18,v[192:195],26
R 50,2,55296
M 18,v[200:203],30
L 3
M 18,v[208:211],34
s_waitcnt vmcnt(7)
R 54,2,56320
M 18,v[216:219],38
s_barrier
ds_read_b32 v7,v6 offset:1024
M 22,v[212:215],34
R 26,1,0
M 22,v[220:223],38
L 4
M 22,v[228:231],42
R 30,1,1024
M 22,v[236:239],46
buffer_atomic_pk_add_bf16 v14,v9,s[40:43],s64 offen sc1
M 18,v[224:227],42
s_add_u32 s64,s75,8192
R 34,1,2048
M 18,v[232:235],46
L 4
buffer_atomic_pk_add_bf16 v15,v9,s[40:43],s64 offen sc1
M 18,v[240:243],50
s_add_u32 s64,s75,16384
L 3
R 38,1,3072
v_cmp_eq_u32 vcc,s53,v7
M 18,v[248:251],54
buffer_atomic_pk_add_bf16 v16,v9,s[40:43],s64 offen sc1
M 22,v[244:247],50
L 2
s_add_u32 s64,s75,24576
s_cmp_eq_u32 vcc_lo,-1
R 42,1,4096
M 22,v[252:255],54
buffer_atomic_pk_add_bf16 v17,v9,s[40:43],s64 offen sc1
s_cbranch_scc1 Lg4_Fa0_%=
Lg4_Ka0_%=:
Lg4_La0_%=:
s_nop 0
v_mfma_f32_16x16x32_bf16 v[14:17],a[4:7],v[26:29],0
s_add_u32 m0,s59,3072
R 46,1,5120
v_lshlrev_b32 v62,16,v58
M 14,a[12:15],30
buffer_load_dword v8,s[36:39],s58 offen offset:0 sc1 lds
v_mfma_f32_16x16x32_bf16 v[10:13],a[0:3],v[26:29],0
s_sub_u32 s75,s67,s85
R 50,1,6144
v_add_f32 v18,v18,v62
M 10,a[8:11],30
L 3
v_and_b32 v63,0xffff0000,v58
v_add_f32 v19,v19,v63
M 10,a[16:19],34
s_mov_b64 s[40:41],s[86:87]
R 54,1,7168
v_lshlrev_b32 v62,16,v59
M 10,a[24:27],38
s_mov_b32 s42,4194304
v_add_f32 v20,v20,v62
v_and_b32 v63,0xffff0000,v59
M 14,a[20:23],34
s_mov_b32 s43,0x00020000
R 26,1,8192
v_add_f32 v21,v21,v63
M 14,a[28:31],38
L 3
v_lshlrev_b32 v62,16,v60
v_add_f32 v22,v22,v62
M 14,a[36:39],42
s_add_u32 s64,s75,0
R 30,1,9216
v_and_b32 v63,0xffff0000,v60
M 14,a[44:47],46
s_lshl_b32 s62,s54,15
v_add_f32 v23,v23,v63
v_lshlrev_b32 v62,16,v61
M 10,a[32:35],42
s_mov_b32 s66,s65
R 34,1,10240
v_add_f32 v24,v24,v62
M 10,a[40:43],46
L 3
v_and_b32 v63,0xffff0000,v61
v_add_f32 v25,v25,v63
M 10,a[48:51],50
s_add_u32 s65,s47,s62
R 38,1,11264
v_cvt_pk_bf16_f32 v18,v18,v19
M 10,a[56:59],54
buffer_load_dwordx4 v[58:61],v0,s[36:39],s74 offen offset:0 sc1
M 14,a[52:55],50
s_add_u32 s74,s49,s62
R 42,1,12288
v_cvt_pk_bf16_f32 v19,v20,v21
M 14,a[60:63],54
L 3
v_cvt_pk_bf16_f32 v20,v22,v23
v_cvt_pk_bf16_f32 v21,v24,v25
M 14,a[68:71],26
R 46,1,13312
v_lshrrev_b32 v62,8,v0
M 14,a[76:79],30
v_lshl_add_u32 v62,v62,5,v0
v_add_u32 v62,s73,v62
M 10,a[64:67],26
R 50,1,14336
M 10,a[72:75],30
L 3
ds_write_b128 v62,v[18:21] offset:0
M 10,a[80:83],34
R 54,1,15360
ds_read2_b32 v[22:23],v5 offset0:0 offset1:16
M 10,a[88:91],38
ds_read2_b32 v[24:25],v5 offset0:32 offset1:48
M 14,a[84:87],34
R 26,1,16384
M 14,a[92:95],38
L 6
M 14,a[100:103],42
R 30,1,17408
M 14,a[108:111],46
M 10,a[96:99],42
R 34,1,18432
M 10,a[104:107],46
L 5
L 3
M 10,a[112:115],50
R 38,1,19456
M 10,a[120:123],54
buffer_atomic_pk_add_bf16 v22,v9,s[40:43],s64 offen sc1
M 14,a[116:119],50
s_add_u32 s64,s75,8192
R 42,1,20480
M 14,a[124:127],54
L 3
buffer_atomic_pk_add_bf16 v23,v9,s[40:43],s64 offen sc1
M 14,a[132:135],26
s_add_u32 s64,s75,16384
R 46,1,21504
M 14,a[140:143],30
buffer_atomic_pk_add_bf16 v24,v9,s[40:43],s64 offen sc1
M 10,a[128:131],26
s_add_u32 s64,s75,24576
R 50,1,22528
M 10,a[136:139],30
L 3
buffer_atomic_pk_add_bf16 v25,v9,s[40:43],s64 offen sc1
M 10,a[144:147],34
R 54,1,23552
M 10,a[152:155],38
M 14,a[148:151],34
R 26,1,24576
M 14,a[156:159],38
L 3
M 14,a[164:167],42
R 30,1,25600
M 14,a[172:175],46
M 10,a[160:163],42
R 34,1,26624
M 10,a[168:171],46
L 3
M 10,a[176:179],50
s_waitcnt vmcnt(5)
R 38,1,27648
M 10,a[184:187],54
s_barrier
ds_read_b32 v7,v6 offset:3072
M 14,a[180:183],50
R 42,1,28672
M 14,a[188:191],54
L 4
M 14,a[196:199],26
R 46,1,29696
M 14,a[204:207],30
M 10,a[192:195],26
R 50,1,30720
M 10,a[200:203],30
L 4
L 3
v_cmp_eq_u32 vcc,s53,v7
M 10,a[208:211],34
R 54,1,31744
M 10,a[216:219],38
M 14,a[212:215],34
s_cmp_eq_u32 vcc_lo,-1
R 26,1,32768
s_cbranch_scc0 Lg4_Xa0_%=
M 14,a[220:223],38
L 3
s_add_u32 m0,s57,0
s_add_u32 s56,s55,0
M 14,a[228:231],42
s_add_u32 s61,s54,s46
s_cmp_ge_u32 s61,s63
s_cselect_b32 s61,s93,s61
R 30,1,33792
M 14,a[236:239],46
s_mul_i32 s58,s61,2560
D 0,56,0
M 10,a[224:227],42
s_cselect_b32 s46,s88,s46
s_cselect_b32 s63,s89,s63
R 34,1,34816
M 10,a[232:235],46
L 3
s_cselect_b32 s93,s50,s93
s_cselect_b32 s88,8,s88
M 10,a[240:243],50
s_cselect_b32 s89,-1,s89
s_add_u32 s58,s58,109891584
R 38,1,35840
M 10,a[248:251],54
D 0,56,1024
M 14,a[244:247],50
s_mov_b32 s54,s61
R 42,1,36864
M 14,a[252:255],54
L 3
M 14,v[68:71],26
R 46,1,37888
M 14,v[76:79],30
D 0,56,2048
M 10,v[64:67],26
R 50,1,38912
M 10,v[72:75],30
L 3
M 10,v[80:83],34
R 54,1,39936
M 10,v[88:91],38
D 0,56,3072
M 14,v[84:87],34
s_add_u32 m0,s57,4096
s_add_u32 s56,s56,4096
R 26,1,40960
M 14,v[92:95],38
L 3
M 14,v[100:103],42
R 30,1,41984
M 14,v[108:111],46
D 0,56,0
M 10,v[96:99],42
R 34,1,43008
M 10,v[104:107],46
L 3
M 10,v[112:115],50
R 38,1,44032
M 10,v[120:123],54
D 0,56,1024
M 14,v[116:119],50
R 42,1,45056
M 14,v[124:127],54
L 3
M 14,v[132:135],26
R 46,1,46080
M 14,v[140:143],30
D 0,56,2048
M 10,v[128:131],26
s_add_u32 m0,s59,0
s_add_u32 s56,s55,28672
R 50,1,47104
M 10,v[136:139],30
L 3
buffer_load_dword v8,s[36:39],s58 offen offset:0 sc1 lds
M 10,v[144:147],34
s_add_u32 m0,s57,28672
s_mul_i32 s55,s61,114688
R 54,1,48128
M 10,v[152:155],38
s_add_u32 s55,s55,s60
M 14,v[148:151],34
R 26,1,49152
M 14,v[156:159],38
L 3
D 0,56,0
M 14,v[164:167],42
R 30,1,50176
M 14,v[172:175],46
M 10,v[160:163],42
R 34,1,51200
M 10,v[168:171],46
L 3
D 0,56,1024
M 10,v[176:179],50
R 38,1,52224
M 10,v[184:187],54
D 0,56,2048
M 14,v[180:183],50
R 42,1,53248
M 14,v[188:191],54
L 3
M 14,v[196:199],26
R 46,1,54272
M 14,v[204:207],30
D 0,56,3072
M 10,v[192:195],26
s_add_u32 m0,s57,32768
s_add_u32 s56,s56,4096
R 50,1,55296
M 10,v[200:203],30
L 3
s_sub_u32 s57,s84,s57
M 10,v[208:211],34
s_waitcnt vmcnt(4)
R 54,1,56320
M 10,v[216:219],38
s_barrier
ds_read_b32 v7,v6 offset:0
M 14,v[212:215],34
R 26,2,0
M 14,v[220:223],38
L 4
D 0,56,0
M 14,v[228:231],42
R 30,2,1024
M 14,v[236:239],46
D 0,56,1024
M 10,v[224:227],42
R 34,2,2048
M 10,v[232:235],46
L 4
D 0,56,2048
M 10,v[240:243],50
L 3
R 38,2,3072
v_cmp_eq_u32 vcc,s53,v7
M 10,v[248:251],54
M 14,v[244:247],50
L 2
s_cmp_eq_u32 vcc_lo,-1
R 42,2,4096
M 14,v[252:255],54
buffer_store_dword v4,v3,s[36:39],s71 offen offset:0 sc0 sc1
buffer_store_dword v4,v3,s[36:39],s71 offen offset:0 sc0 sc1
buffer_store_dword v4,v3,s[36:39],s71 offen offset:0 sc0 sc1
buffer_store_dword v4,v3,s[36:39],s71 offen offset:0 sc0 sc1
s_cbranch_scc1 Lg4_Fa1_%=
s_branch Lg4_Ka1_%=
Lg4_Xa0_%=:
M 14,a[220:223],38
L 3
s_add_u32 m0,s59,0
M 14,a[228:231],42
R 30,1,33792
M 14,a[236:239],46
buffer_load_dword v8,s[36:39],s58 offen offset:0 sc1 lds
M 10,a[224:227],42
R 34,1,34816
M 10,a[232:235],46
L 3
M 10,a[240:243],50
R 38,1,35840
M 10,a[248:251],54
M 14,a[244:247],50
R 42,1,36864
M 14,a[252:255],54
L 3
M 14,v[68:71],26
R 46,1,37888
M 14,v[76:79],30
M 10,v[64:67],26
R 50,1,38912
M 10,v[72:75],30
L 3
)"
R"(M 10,v[80:83],34
R 54,1,39936
M 10,v[88:91],38
M 14,v[84:87],34
R 26,1,40960
M 14,v[92:95],38
L 3
M 14,v[100:103],42
R 30,1,41984
M 14,v[108:111],46
M 10,v[96:99],42
R 34,1,43008
M 10,v[104:107],46
L 3
M 10,v[112:115],50
R 38,1,44032
M 10,v[120:123],54
M 14,v[116:119],50
R 42,1,45056
M 14,v[124:127],54
L 3
M 14,v[132:135],26
R 46,1,46080
M 14,v[140:143],30
M 10,v[128:131],26
R 50,1,47104
M 10,v[136:139],30
L 3
M 10,v[144:147],34
R 54,1,48128
M 10,v[152:155],38
M 14,v[148:151],34
R 26,1,49152
M 14,v[156:159],38
L 3
M 14,v[164:167],42
R 30,1,50176
M 14,v[172:175],46
M 10,v[160:163],42
R 34,1,51200
M 10,v[168:171],46
L 3
M 10,v[176:179],50
R 38,1,52224
M 10,v[184:187],54
M 14,v[180:183],50
R 42,1,53248
M 14,v[188:191],54
L 3
M 14,v[196:199],26
R 46,1,54272
M 14,v[204:207],30
M 10,v[192:195],26
R 50,1,55296
M 10,v[200:203],30
L 3
M 10,v[208:211],34
s_waitcnt vmcnt(0)
R 54,1,56320
M 10,v[216:219],38
s_barrier
ds_read_b32 v7,v6 offset:0
M 14,v[212:215],34
M 14,v[220:223],38
L 3
M 14,v[228:231],42
M 14,v[236:239],46
M 10,v[224:227],42
M 10,v[232:235],46
L 1
L 0
M 10,v[240:243],50
v_cmp_eq_u32 vcc,s53,v7
M 10,v[248:251],54
M 14,v[244:247],50
M 14,v[252:255],54
Lg4_pa0_%=:
s_mov_b32 s80,1
s_cmp_eq_u32 vcc_lo,-1
s_cbranch_scc0 Lg4_xna0_%=
s_add_u32 m0,s57,0
s_add_u32 s56,s55,0
D 0,56,0
s_sub_u32 s75,s66,s85
v_lshlrev_b32 v62,16,v58
D 0,56,1024
v_add_f32 v10,v10,v62
D 0,56,2048
v_and_b32 v63,0xffff0000,v58
D 0,56,3072
v_add_f32 v11,v11,v63
s_add_u32 m0,s57,4096
s_add_u32 s56,s56,4096
D 0,56,0
v_lshlrev_b32 v62,16,v59
D 0,56,1024
v_add_f32 v12,v12,v62
D 0,56,2048
v_and_b32 v63,0xffff0000,v59
s_add_u32 s61,s54,s46
s_cmp_ge_u32 s61,s63
s_cselect_b32 s61,s93,s61
s_cselect_b32 s46,s88,s46
s_cselect_b32 s63,s89,s63
s_cselect_b32 s93,s50,s93
s_cselect_b32 s88,8,s88
s_cselect_b32 s89,-1,s89
s_mul_i32 s58,s61,2560
s_add_u32 s58,s58,109891584
s_add_u32 m0,s57,28672
s_add_u32 s56,s55,28672
D 0,56,0
v_add_f32 v13,v13,v63
D 0,56,1024
v_lshlrev_b32 v62,16,v60
D 0,56,2048
v_add_f32 v14,v14,v62
D 0,56,3072
v_and_b32 v63,0xffff0000,v60
s_add_u32 m0,s57,32768
s_add_u32 s56,s56,4096
D 0,56,0
v_add_f32 v15,v15,v63
D 0,56,1024
v_lshlrev_b32 v62,16,v61
D 0,56,2048
v_add_f32 v16,v16,v62
s_mul_i32 s55,s61,114688
s_add_u32 s55,s55,s60
s_sub_u32 s57,s84,s57
s_mov_b32 s54,s61
v_and_b32 v63,0xffff0000,v61
v_add_f32 v17,v17,v63
v_cvt_pk_bf16_f32 v10,v10,v11
v_cvt_pk_bf16_f32 v11,v12,v13
v_cvt_pk_bf16_f32 v12,v14,v15
v_cvt_pk_bf16_f32 v13,v16,v17
v_lshrrev_b32 v62,8,v0
v_lshl_add_u32 v62,v62,5,v0
v_add_u32 v62,s73,v62
ds_write_b128 v62,v[10:13] offset:0
ds_read2_b32 v[14:15],v5 offset0:0 offset1:16
ds_read2_b32 v[16:17],v5 offset0:32 offset1:48
s_mov_b64 s[40:41],s[86:87]
s_mov_b32 s42,4194304
s_mov_b32 s43,0x00020000
L 0
s_add_u32 s64,s75,0
buffer_atomic_pk_add_bf16 v14,v9,s[40:43],s64 offen sc1
s_add_u32 s64,s75,8192
buffer_atomic_pk_add_bf16 v15,v9,s[40:43],s64 offen sc1
s_add_u32 s64,s75,16384
buffer_atomic_pk_add_bf16 v16,v9,s[40:43],s64 offen sc1
s_add_u32 s64,s75,24576
buffer_atomic_pk_add_bf16 v17,v9,s[40:43],s64 offen sc1
s_mov_b32 s72,0x7fff0000
s_mov_b32 s69,0x7fff0000
s_mov_b32 s66,0x7fff0000
s_mov_b32 s76,0x7fff0000
s_branch Lg4_gox_%=
Lg4_xna0_%=:
s_cmp_ge_u32 s54,256
s_cbranch_scc1 Lg4_fina0_%=
s_add_u32 m0,s59,4096
s_nop 0
buffer_load_dword v8,s[36:39],s58 offen offset:0 sc1 lds
buffer_store_dword v4,v3,s[36:39],s71 offen offset:0 sc0 sc1
s_add_u32 m0,s59,5120
s_nop 0
buffer_load_dword v8,s[36:39],s58 offen offset:0 sc1 lds
buffer_store_dword v4,v3,s[36:39],s71 offen offset:0 sc0 sc1
s_mov_b32 s83,0
s_sub_u32 s75,s66,s85
v_lshlrev_b32 v62,16,v58
v_add_f32 v10,v10,v62
v_and_b32 v63,0xffff0000,v58
v_add_f32 v11,v11,v63
v_lshlrev_b32 v62,16,v59
v_add_f32 v12,v12,v62
v_and_b32 v63,0xffff0000,v59
v_add_f32 v13,v13,v63
v_lshlrev_b32 v62,16,v60
v_add_f32 v14,v14,v62
v_and_b32 v63,0xffff0000,v60
v_add_f32 v15,v15,v63
v_lshlrev_b32 v62,16,v61
v_add_f32 v16,v16,v62
v_and_b32 v63,0xffff0000,v61
v_add_f32 v17,v17,v63
v_cvt_pk_bf16_f32 v10,v10,v11
v_cvt_pk_bf16_f32 v11,v12,v13
v_cvt_pk_bf16_f32 v12,v14,v15
v_cvt_pk_bf16_f32 v13,v16,v17
v_lshrrev_b32 v62,8,v0
v_lshl_add_u32 v62,v62,5,v0
v_add_u32 v62,s73,v62
ds_write_b128 v62,v[10:13] offset:0
ds_read2_b32 v[14:15],v5 offset0:0 offset1:16
ds_read2_b32 v[16:17],v5 offset0:32 offset1:48
s_mov_b64 s[40:41],s[86:87]
s_mov_b32 s42,4194304
s_mov_b32 s43,0x00020000
L 0
s_add_u32 s64,s75,0
buffer_atomic_pk_add_bf16 v14,v9,s[40:43],s64 offen sc1
s_add_u32 s64,s75,8192
buffer_atomic_pk_add_bf16 v15,v9,s[40:43],s64 offen sc1
s_add_u32 s64,s75,16384
buffer_atomic_pk_add_bf16 v16,v9,s[40:43],s64 offen sc1
s_add_u32 s64,s75,24576
buffer_atomic_pk_add_bf16 v17,v9,s[40:43],s64 offen sc1
s_mov_b32 s72,0x7fff0000
s_mov_b32 s69,0x7fff0000
s_mov_b32 s66,0x7fff0000
s_mov_b32 s76,0x7fff0000
s_branch Lg4_poll_%=
Lg4_fina0_%=:
s_sub_u32 s75,s66,s85
v_lshlrev_b32 v62,16,v58
v_add_f32 v10,v10,v62
v_and_b32 v63,0xffff0000,v58
v_add_f32 v11,v11,v63
v_lshlrev_b32 v62,16,v59
v_add_f32 v12,v12,v62
v_and_b32 v63,0xffff0000,v59
v_add_f32 v13,v13,v63
v_lshlrev_b32 v62,16,v60
v_add_f32 v14,v14,v62
v_and_b32 v63,0xffff0000,v60
v_add_f32 v15,v15,v63
v_lshlrev_b32 v62,16,v61
v_add_f32 v16,v16,v62
v_and_b32 v63,0xffff0000,v61
v_add_f32 v17,v17,v63
v_cvt_pk_bf16_f32 v10,v10,v11
v_cvt_pk_bf16_f32 v11,v12,v13
v_cvt_pk_bf16_f32 v12,v14,v15
v_cvt_pk_bf16_f32 v13,v16,v17
v_lshrrev_b32 v62,8,v0
v_lshl_add_u32 v62,v62,5,v0
v_add_u32 v62,s73,v62
ds_write_b128 v62,v[10:13] offset:0
ds_read2_b32 v[14:15],v5 offset0:0 offset1:16
ds_read2_b32 v[16:17],v5 offset0:32 offset1:48
s_mov_b64 s[40:41],s[86:87]
s_mov_b32 s42,4194304
s_mov_b32 s43,0x00020000
L 0
s_add_u32 s64,s75,0
buffer_atomic_pk_add_bf16 v14,v9,s[40:43],s64 offen sc1
s_add_u32 s64,s75,8192
buffer_atomic_pk_add_bf16 v15,v9,s[40:43],s64 offen sc1
s_add_u32 s64,s75,16384
buffer_atomic_pk_add_bf16 v16,v9,s[40:43],s64 offen sc1
s_add_u32 s64,s75,24576
buffer_atomic_pk_add_bf16 v17,v9,s[40:43],s64 offen sc1
s_mov_b32 s72,0x7fff0000
s_mov_b32 s69,0x7fff0000
s_mov_b32 s66,0x7fff0000
s_mov_b32 s76,0x7fff0000
s_branch Lg4_done_%=
Lg4_Ka1_%=:
Lg4_La1_%=:
s_nop 0
v_mfma_f32_16x16x32_bf16 v[22:25],a[4:7],v[26:29],0
s_add_u32 m0,s59,3072
R 46,2,5120
v_lshlrev_b32 v62,16,v58
M 22,a[12:15],30
buffer_load_dword v8,s[36:39],s58 offen offset:0 sc1 lds
v_mfma_f32_16x16x32_bf16 v[18:21],a[0:3],v[26:29],0
s_sub_u32 s75,s66,s85
R 50,2,6144
v_add_f32 v10,v10,v62
M 18,a[8:11],30
L 3
v_and_b32 v63,0xffff0000,v58
v_add_f32 v11,v11,v63
M 18,a[16:19],34
s_mov_b64 s[40:41],s[86:87]
R 54,2,7168
v_lshlrev_b32 v62,16,v59
M 18,a[24:27],38
s_mov_b32 s42,4194304
v_add_f32 v12,v12,v62
v_and_b32 v63,0xffff0000,v59
M 22,a[20:23],34
s_mov_b32 s43,0x00020000
R 26,2,8192
v_add_f32 v13,v13,v63
M 22,a[28:31],38
L 3
v_lshlrev_b32 v62,16,v60
v_add_f32 v14,v14,v62
M 22,a[36:39],42
s_add_u32 s64,s75,0
R 30,2,9216
v_and_b32 v63,0xffff0000,v60
M 22,a[44:47],46
s_lshl_b32 s62,s54,15
v_add_f32 v15,v15,v63
v_lshlrev_b32 v62,16,v61
M 18,a[32:35],42
s_mov_b32 s67,s65
R 34,2,10240
v_add_f32 v16,v16,v62
M 18,a[40:43],46
L 3
v_and_b32 v63,0xffff0000,v61
v_add_f32 v17,v17,v63
M 18,a[48:51],50
s_add_u32 s65,s47,s62
R 38,2,11264
v_cvt_pk_bf16_f32 v10,v10,v11
M 18,a[56:59],54
buffer_load_dwordx4 v[58:61],v0,s[36:39],s74 offen offset:0 sc1
M 22,a[52:55],50
s_add_u32 s74,s49,s62
R 42,2,12288
v_cvt_pk_bf16_f32 v11,v12,v13
M 22,a[60:63],54
L 3
v_cvt_pk_bf16_f32 v12,v14,v15
v_cvt_pk_bf16_f32 v13,v16,v17
M 22,a[68:71],26
R 46,2,13312
v_lshrrev_b32 v62,8,v0
M 22,a[76:79],30
v_lshl_add_u32 v62,v62,5,v0
v_add_u32 v62,s73,v62
M 18,a[64:67],26
R 50,2,14336
M 18,a[72:75],30
L 3
ds_write_b128 v62,v[10:13] offset:0
M 18,a[80:83],34
R 54,2,15360
ds_read2_b32 v[14:15],v5 offset0:0 offset1:16
M 18,a[88:91],38
ds_read2_b32 v[16:17],v5 offset0:32 offset1:48
M 22,a[84:87],34
R 26,2,16384
M 22,a[92:95],38
L 6
M 22,a[100:103],42
R 30,2,17408
M 22,a[108:111],46
M 18,a[96:99],42
R 34,2,18432
M 18,a[104:107],46
L 5
L 3
M 18,a[112:115],50
R 38,2,19456
M 18,a[120:123],54
buffer_atomic_pk_add_bf16 v14,v9,s[40:43],s64 offen sc1
M 22,a[116:119],50
s_add_u32 s64,s75,8192
R 42,2,20480
M 22,a[124:127],54
L 3
buffer_atomic_pk_add_bf16 v15,v9,s[40:43],s64 offen sc1
M 22,a[132:135],26
s_add_u32 s64,s75,16384
R 46,2,21504
M 22,a[140:143],30
buffer_atomic_pk_add_bf16 v16,v9,s[40:43],s64 offen sc1
M 18,a[128:131],26
s_add_u32 s64,s75,24576
R 50,2,22528
M 18,a[136:139],30
L 3
buffer_atomic_pk_add_bf16 v17,v9,s[40:43],s64 offen sc1
M 18,a[144:147],34
R 54,2,23552
M 18,a[152:155],38
M 22,a[148:151],34
R 26,2,24576
M 22,a[156:159],38
L 3
M 22,a[164:167],42
R 30,2,25600
M 22,a[172:175],46
M 18,a[160:163],42
R 34,2,26624
M 18,a[168:171],46
L 3
M 18,a[176:179],50
s_waitcnt vmcnt(5)
R 38,2,27648
M 18,a[184:187],54
s_barrier
ds_read_b32 v7,v6 offset:3072
M 22,a[180:183],50
R 42,2,28672
M 22,a[188:191],54
L 4
M 22,a[196:199],26
R 46,2,29696
M 22,a[204:207],30
M 18,a[192:195],26
R 50,2,30720
M 18,a[200:203],30
L 4
L 3
v_cmp_eq_u32 vcc,s53,v7
M 18,a[208:211],34
R 54,2,31744
M 18,a[216:219],38
M 22,a[212:215],34
s_cmp_eq_u32 vcc_lo,-1
R 26,2,32768
s_cbranch_scc0 Lg4_Xa1_%=
M 22,a[220:223],38
L 3
s_add_u32 m0,s57,0
s_add_u32 s56,s55,0
M 22,a[228:231],42
s_add_u32 s61,s54,s46
s_cmp_ge_u32 s61,s63
s_cselect_b32 s61,s93,s61
R 30,2,33792
M 22,a[236:239],46
s_mul_i32 s58,s61,2560
D 0,56,0
M 18,a[224:227],42
s_cselect_b32 s46,s88,s46
s_cselect_b32 s63,s89,s63
R 34,2,34816
M 18,a[232:235],46
L 3
s_cselect_b32 s93,s50,s93
s_cselect_b32 s88,8,s88
M 18,a[240:243],50
s_cselect_b32 s89,-1,s89
s_add_u32 s58,s58,109891584
R 38,2,35840
M 18,a[248:251],54
D 0,56,1024
M 22,a[244:247],50
s_mov_b32 s54,s61
R 42,2,36864
M 22,a[252:255],54
L 3
M 22,v[68:71],26
R 46,2,37888
M 22,v[76:79],30
D 0,56,2048
M 18,v[64:67],26
R 50,2,38912
M 18,v[72:75],30
L 3
M 18,v[80:83],34
R 54,2,39936
M 18,v[88:91],38
D 0,56,3072
M 22,v[84:87],34
s_add_u32 m0,s57,4096
s_add_u32 s56,s56,4096
R 26,2,40960
M 22,v[92:95],38
L 3
M 22,v[100:103],42
R 30,2,41984
M 22,v[108:111],46
D 0,56,0
M 18,v[96:99],42
R 34,2,43008
M 18,v[104:107],46
L 3
M 18,v[112:115],50
R 38,2,44032
M 18,v[120:123],54
D 0,56,1024
M 22,v[116:119],50
R 42,2,45056
M 22,v[124:127],54
L 3
M 22,v[132:135],26
R 46,2,46080
M 22,v[140:143],30
D 0,56,2048
M 18,v[128:131],26
s_add_u32 m0,s59,1024
s_add_u32 s56,s55,28672
R 50,2,47104
M 18,v[136:139],30
L 3
buffer_load_dword v8,s[36:39],s58 offen offset:0 sc1 lds
M 18,v[144:147],34
s_add_u32 m0,s57,28672
s_mul_i32 s55,s61,114688
R 54,2,48128
M 18,v[152:155],38
s_add_u32 s55,s55,s60
M 22,v[148:151],34
R 26,2,49152
M 22,v[156:159],38
L 3
D 0,56,0
)"
R"(M 22,v[164:167],42
R 30,2,50176
M 22,v[172:175],46
M 18,v[160:163],42
R 34,2,51200
M 18,v[168:171],46
L 3
D 0,56,1024
M 18,v[176:179],50
R 38,2,52224
M 18,v[184:187],54
D 0,56,2048
M 22,v[180:183],50
R 42,2,53248
M 22,v[188:191],54
L 3
M 22,v[196:199],26
R 46,2,54272
M 22,v[204:207],30
D 0,56,3072
M 18,v[192:195],26
s_add_u32 m0,s57,32768
s_add_u32 s56,s56,4096
R 50,2,55296
M 18,v[200:203],30
L 3
s_sub_u32 s57,s84,s57
M 18,v[208:211],34
s_waitcnt vmcnt(4)
R 54,2,56320
M 18,v[216:219],38
s_barrier
ds_read_b32 v7,v6 offset:1024
M 22,v[212:215],34
R 26,1,0
M 22,v[220:223],38
L 4
D 0,56,0
M 22,v[228:231],42
R 30,1,1024
M 22,v[236:239],46
D 0,56,1024
M 18,v[224:227],42
R 34,1,2048
M 18,v[232:235],46
L 4
D 0,56,2048
M 18,v[240:243],50
L 3
R 38,1,3072
v_cmp_eq_u32 vcc,s53,v7
M 18,v[248:251],54
M 22,v[244:247],50
L 2
s_cmp_eq_u32 vcc_lo,-1
R 42,1,4096
M 22,v[252:255],54
buffer_store_dword v4,v3,s[36:39],s71 offen offset:0 sc0 sc1
buffer_store_dword v4,v3,s[36:39],s71 offen offset:0 sc0 sc1
buffer_store_dword v4,v3,s[36:39],s71 offen offset:0 sc0 sc1
buffer_store_dword v4,v3,s[36:39],s71 offen offset:0 sc0 sc1
s_cbranch_scc1 Lg4_Fa0_%=
s_branch Lg4_Ka0_%=
Lg4_Xa1_%=:
M 22,a[220:223],38
L 3
s_add_u32 m0,s59,1024
M 22,a[228:231],42
R 30,2,33792
M 22,a[236:239],46
buffer_load_dword v8,s[36:39],s58 offen offset:0 sc1 lds
M 18,a[224:227],42
R 34,2,34816
M 18,a[232:235],46
L 3
M 18,a[240:243],50
R 38,2,35840
M 18,a[248:251],54
M 22,a[244:247],50
R 42,2,36864
M 22,a[252:255],54
L 3
M 22,v[68:71],26
R 46,2,37888
M 22,v[76:79],30
M 18,v[64:67],26
R 50,2,38912
M 18,v[72:75],30
L 3
M 18,v[80:83],34
R 54,2,39936
M 18,v[88:91],38
M 22,v[84:87],34
R 26,2,40960
M 22,v[92:95],38
L 3
M 22,v[100:103],42
R 30,2,41984
M 22,v[108:111],46
M 18,v[96:99],42
R 34,2,43008
M 18,v[104:107],46
L 3
M 18,v[112:115],50
R 38,2,44032
M 18,v[120:123],54
M 22,v[116:119],50
R 42,2,45056
M 22,v[124:127],54
L 3
M 22,v[132:135],26
R 46,2,46080
M 22,v[140:143],30
M 18,v[128:131],26
R 50,2,47104
M 18,v[136:139],30
L 3
M 18,v[144:147],34
R 54,2,48128
M 18,v[152:155],38
M 22,v[148:151],34
R 26,2,49152
M 22,v[156:159],38
L 3
M 22,v[164:167],42
R 30,2,50176
M 22,v[172:175],46
M 18,v[160:163],42
R 34,2,51200
M 18,v[168:171],46
L 3
M 18,v[176:179],50
R 38,2,52224
M 18,v[184:187],54
M 22,v[180:183],50
R 42,2,53248
M 22,v[188:191],54
L 3
M 22,v[196:199],26
R 46,2,54272
M 22,v[204:207],30
M 18,v[192:195],26
R 50,2,55296
M 18,v[200:203],30
L 3
M 18,v[208:211],34
s_waitcnt vmcnt(0)
R 54,2,56320
M 18,v[216:219],38
s_barrier
ds_read_b32 v7,v6 offset:1024
M 22,v[212:215],34
M 22,v[220:223],38
L 3
M 22,v[228:231],42
M 22,v[236:239],46
M 18,v[224:227],42
M 18,v[232:235],46
L 1
L 0
M 18,v[240:243],50
v_cmp_eq_u32 vcc,s53,v7
M 18,v[248:251],54
M 22,v[244:247],50
M 22,v[252:255],54
Lg4_pa1_%=:
s_mov_b32 s80,0
s_cmp_eq_u32 vcc_lo,-1
s_cbranch_scc0 Lg4_xna1_%=
s_add_u32 m0,s57,0
s_add_u32 s56,s55,0
D 0,56,0
s_sub_u32 s75,s67,s85
v_lshlrev_b32 v62,16,v58
D 0,56,1024
v_add_f32 v18,v18,v62
D 0,56,2048
v_and_b32 v63,0xffff0000,v58
D 0,56,3072
v_add_f32 v19,v19,v63
s_add_u32 m0,s57,4096
s_add_u32 s56,s56,4096
D 0,56,0
v_lshlrev_b32 v62,16,v59
D 0,56,1024
v_add_f32 v20,v20,v62
D 0,56,2048
v_and_b32 v63,0xffff0000,v59
s_add_u32 s61,s54,s46
s_cmp_ge_u32 s61,s63
s_cselect_b32 s61,s93,s61
s_cselect_b32 s46,s88,s46
s_cselect_b32 s63,s89,s63
s_cselect_b32 s93,s50,s93
s_cselect_b32 s88,8,s88
s_cselect_b32 s89,-1,s89
s_mul_i32 s58,s61,2560
s_add_u32 s58,s58,109891584
s_add_u32 m0,s57,28672
s_add_u32 s56,s55,28672
D 0,56,0
v_add_f32 v21,v21,v63
D 0,56,1024
v_lshlrev_b32 v62,16,v60
D 0,56,2048
v_add_f32 v22,v22,v62
D 0,56,3072
v_and_b32 v63,0xffff0000,v60
s_add_u32 m0,s57,32768
s_add_u32 s56,s56,4096
D 0,56,0
v_add_f32 v23,v23,v63
D 0,56,1024
v_lshlrev_b32 v62,16,v61
D 0,56,2048
v_add_f32 v24,v24,v62
s_mul_i32 s55,s61,114688
s_add_u32 s55,s55,s60
s_sub_u32 s57,s84,s57
s_mov_b32 s54,s61
v_and_b32 v63,0xffff0000,v61
v_add_f32 v25,v25,v63
v_cvt_pk_bf16_f32 v18,v18,v19
v_cvt_pk_bf16_f32 v19,v20,v21
v_cvt_pk_bf16_f32 v20,v22,v23
v_cvt_pk_bf16_f32 v21,v24,v25
v_lshrrev_b32 v62,8,v0
v_lshl_add_u32 v62,v62,5,v0
v_add_u32 v62,s73,v62
ds_write_b128 v62,v[18:21] offset:0
ds_read2_b32 v[22:23],v5 offset0:0 offset1:16
ds_read2_b32 v[24:25],v5 offset0:32 offset1:48
s_mov_b64 s[40:41],s[86:87]
s_mov_b32 s42,4194304
s_mov_b32 s43,0x00020000
L 0
s_add_u32 s64,s75,0
buffer_atomic_pk_add_bf16 v22,v9,s[40:43],s64 offen sc1
s_add_u32 s64,s75,8192
buffer_atomic_pk_add_bf16 v23,v9,s[40:43],s64 offen sc1
s_add_u32 s64,s75,16384
buffer_atomic_pk_add_bf16 v24,v9,s[40:43],s64 offen sc1
s_add_u32 s64,s75,24576
buffer_atomic_pk_add_bf16 v25,v9,s[40:43],s64 offen sc1
s_mov_b32 s72,0x7fff0000
s_mov_b32 s69,0x7fff0000
s_mov_b32 s67,0x7fff0000
s_mov_b32 s77,0x7fff0000
s_branch Lg4_gox_%=
Lg4_xna1_%=:
s_cmp_ge_u32 s54,256
s_cbranch_scc1 Lg4_fina1_%=
s_add_u32 m0,s59,4096
s_nop 0
buffer_load_dword v8,s[36:39],s58 offen offset:0 sc1 lds
buffer_store_dword v4,v3,s[36:39],s71 offen offset:0 sc0 sc1
s_add_u32 m0,s59,5120
s_nop 0
buffer_load_dword v8,s[36:39],s58 offen offset:0 sc1 lds
buffer_store_dword v4,v3,s[36:39],s71 offen offset:0 sc0 sc1
s_mov_b32 s83,0
s_sub_u32 s75,s67,s85
v_lshlrev_b32 v62,16,v58
v_add_f32 v18,v18,v62
v_and_b32 v63,0xffff0000,v58
v_add_f32 v19,v19,v63
v_lshlrev_b32 v62,16,v59
v_add_f32 v20,v20,v62
v_and_b32 v63,0xffff0000,v59
v_add_f32 v21,v21,v63
v_lshlrev_b32 v62,16,v60
v_add_f32 v22,v22,v62
v_and_b32 v63,0xffff0000,v60
v_add_f32 v23,v23,v63
v_lshlrev_b32 v62,16,v61
v_add_f32 v24,v24,v62
v_and_b32 v63,0xffff0000,v61
v_add_f32 v25,v25,v63
v_cvt_pk_bf16_f32 v18,v18,v19
v_cvt_pk_bf16_f32 v19,v20,v21
v_cvt_pk_bf16_f32 v20,v22,v23
v_cvt_pk_bf16_f32 v21,v24,v25
v_lshrrev_b32 v62,8,v0
v_lshl_add_u32 v62,v62,5,v0
v_add_u32 v62,s73,v62
ds_write_b128 v62,v[18:21] offset:0
ds_read2_b32 v[22:23],v5 offset0:0 offset1:16
ds_read2_b32 v[24:25],v5 offset0:32 offset1:48
s_mov_b64 s[40:41],s[86:87]
s_mov_b32 s42,4194304
s_mov_b32 s43,0x00020000
L 0
s_add_u32 s64,s75,0
buffer_atomic_pk_add_bf16 v22,v9,s[40:43],s64 offen sc1
s_add_u32 s64,s75,8192
buffer_atomic_pk_add_bf16 v23,v9,s[40:43],s64 offen sc1
s_add_u32 s64,s75,16384
buffer_atomic_pk_add_bf16 v24,v9,s[40:43],s64 offen sc1
s_add_u32 s64,s75,24576
buffer_atomic_pk_add_bf16 v25,v9,s[40:43],s64 offen sc1
s_mov_b32 s72,0x7fff0000
s_mov_b32 s69,0x7fff0000
s_mov_b32 s67,0x7fff0000
s_mov_b32 s77,0x7fff0000
s_branch Lg4_poll_%=
Lg4_fina1_%=:
s_sub_u32 s75,s67,s85
v_lshlrev_b32 v62,16,v58
v_add_f32 v18,v18,v62
v_and_b32 v63,0xffff0000,v58
v_add_f32 v19,v19,v63
v_lshlrev_b32 v62,16,v59
v_add_f32 v20,v20,v62
v_and_b32 v63,0xffff0000,v59
v_add_f32 v21,v21,v63
v_lshlrev_b32 v62,16,v60
v_add_f32 v22,v22,v62
v_and_b32 v63,0xffff0000,v60
v_add_f32 v23,v23,v63
v_lshlrev_b32 v62,16,v61
v_add_f32 v24,v24,v62
v_and_b32 v63,0xffff0000,v61
v_add_f32 v25,v25,v63
v_cvt_pk_bf16_f32 v18,v18,v19
v_cvt_pk_bf16_f32 v19,v20,v21
v_cvt_pk_bf16_f32 v20,v22,v23
v_cvt_pk_bf16_f32 v21,v24,v25
v_lshrrev_b32 v62,8,v0
v_lshl_add_u32 v62,v62,5,v0
v_add_u32 v62,s73,v62
ds_write_b128 v62,v[18:21] offset:0
ds_read2_b32 v[22:23],v5 offset0:0 offset1:16
ds_read2_b32 v[24:25],v5 offset0:32 offset1:48
s_mov_b64 s[40:41],s[86:87]
s_mov_b32 s42,4194304
s_mov_b32 s43,0x00020000
L 0
s_add_u32 s64,s75,0
buffer_atomic_pk_add_bf16 v22,v9,s[40:43],s64 offen sc1
s_add_u32 s64,s75,8192
buffer_atomic_pk_add_bf16 v23,v9,s[40:43],s64 offen sc1
s_add_u32 s64,s75,16384
buffer_atomic_pk_add_bf16 v24,v9,s[40:43],s64 offen sc1
s_add_u32 s64,s75,24576
buffer_atomic_pk_add_bf16 v25,v9,s[40:43],s64 offen sc1
s_mov_b32 s72,0x7fff0000
s_mov_b32 s69,0x7fff0000
s_mov_b32 s67,0x7fff0000
s_mov_b32 s77,0x7fff0000
s_branch Lg4_done_%=
Lg4_done_%=:
s_nop 0
s_barrier
buffer_store_dword v4,v3,s[36:39],s70 offen offset:0 sc0 sc1
s_waitcnt vmcnt(1)
s_barrier
buffer_store_dword v4,v3,s[36:39],s72 offen offset:0 sc0 sc1
s_waitcnt vmcnt(0) lgkmcnt(0)
s_barrier
s_endpgm
Lcode_g4_e:
.purgem M
.purgem R
.purgem T
.purgem D
.purgem L
)"
  :: "s"(ws), "s"(__builtin_amdgcn_readfirstlane(sWave)), "s"(__builtin_amdgcn_readfirstlane(sG0)), "s"(__builtin_amdgcn_readfirstlane(sStep)), "s"(__builtin_amdgcn_readfirstlane(sOutOff)), "s"(__builtin_amdgcn_readfirstlane(sFoutOff)), "s"(__builtin_amdgcn_readfirstlane(sAuxOff)), "s"(__builtin_amdgcn_readfirstlane(sWw)), "s"(__builtin_amdgcn_readfirstlane(sBuf)), "s"(__builtin_amdgcn_readfirstlane(sRk)), "s"(__builtin_amdgcn_readfirstlane(sExp)), "v"(vL16), "v"(vFlag), "v"(vSnapOff), "v"(vSnapRd), "v"(vWd0), "v"(vWd1), "v"(vWr0), "v"(vWr1), "v"(vSp), "v"(vSn), "s"(w4), "s"(out) : "v255", "v0", "v1", "v2", "v3", "v4", "v5", "v6", "v7", "v8", "v9", "v10", "v11", "v12", "v13", "v14", "v15", "v16", "v17", "v18", "v19", "v20", "v21", "v22", "v23", "v24", "v25", "v26", "v27", "v28", "v29", "v30", "v31", "v32", "v33", "v34", "v35", "v36", "v37", "v38", "v39", "v40", "v41", "v42", "v43", "v44", "v45", "v46", "v47", "v48", "v49", "v50", "v51", "v52", "v53", "v54", "v55", "v56", "v57", "v58", "v59", "v60", "v61", "v62", "v63", "v64", "v65", "v66", "v67", "v68", "v69", "v70", "v71", "v72", "v73", "v74", "v75", "v76", "v77", "v78", "v79", "v80", "v81", "v82", "v83", "v84", "v85", "v86", "v87", "v88", "v89", "v90", "v91", "v92", "v93", "v94", "v95", "v96", "v97", "v98", "v99", "v100", "v101", "v102", "v103", "v104", "v105", "v106", "v107", "v108", "v109", "v110", "v111", "v112", "v113", "v114", "v115", "v116", "v117", "v118", "v119", "v120", "v121", "v122", "v123", "v124", "v125", "v126", "v127", "v128", "v129", "v130", "v131", "v132", "v133", "v134", "v135", "v136", "v137", "v138", "v139", "v140", "v141", "v142", "v143", "v144", "v145", "v146", "v147", "v148", "v149", "v150", "v151", "v152", "v153", "v154", "v155", "v156", "v157", "v158", "v159", "v160", "v161", "v162", "v163", "v164", "v165", "v166", "v167", "v168", "v169", "v170", "v171", "v172", "v173", "v174", "v175", "v176", "v177", "v178", "v179", "v180", "v181", "v182", "v183", "v184", "v185", "v186", "v187", "v188", "v189", "v190", "v191", "v192", "v193", "v194", "v195", "v196", "v197", "v198", "v199", "v200", "v201", "v202", "v203", "v204", "v205", "v206", "v207", "v208", "v209", "v210", "v211", "v212", "v213", "v214", "v215", "v216", "v217", "v218", "v219", "v220", "v221", "v222", "v223", "v224", "v225", "v226", "v227", "v228", "v229", "v230", "v231", "a0", "a1", "a2", "a3", "a4", "a5", "a6", "a7", "a8", "a9", "a10", "a11", "a12", "a13", "a14", "a15", "a16", "a17", "a18", "a19", "a20", "a21", "a22", "a23", "a24", "a25", "a26", "a27", "a28", "a29", "a30", "a31", "a32", "a33", "a34", "a35", "a36", "a37", "a38", "a39", "a40", "a41", "a42", "a43", "a44", "a45", "a46", "a47", "a48", "a49", "a50", "a51", "a52", "a53", "a54", "a55", "a56", "a57", "a58", "a59", "a60", "a61", "a62", "a63", "a64", "a65", "a66", "a67", "a68", "a69", "a70", "a71", "a72", "a73", "a74", "a75", "a76", "a77", "a78", "a79", "a80", "a81", "a82", "a83", "a84", "a85", "a86", "a87", "a88", "a89", "a90", "a91", "a92", "a93", "a94", "a95", "a96", "a97", "a98", "a99", "a100", "a101", "a102", "a103", "a104", "a105", "a106", "a107", "a108", "a109", "a110", "a111", "a112", "a113", "a114", "a115", "a116", "a117", "a118", "a119", "a120", "a121", "a122", "a123", "a124", "a125", "a126", "a127", "a128", "a129", "a130", "a131", "a132", "a133", "a134", "a135", "a136", "a137", "a138", "a139", "a140", "a141", "a142", "a143", "a144", "a145", "a146", "a147", "a148", "a149", "a150", "a151", "a152", "a153", "a154", "a155", "a156", "a157", "a158", "a159", "a160", "a161", "a162", "a163", "a164", "a165", "a166", "a167", "a168", "a169", "a170", "a171", "a172", "a173", "a174", "a175", "a176", "a177", "a178", "a179", "a180", "a181", "a182", "a183", "a184", "a185", "a186", "a187", "a188", "a189", "a190", "a191", "a192", "a193", "a194", "a195", "a196", "a197", "a198", "a199", "a200", "a201", "a202", "a203", "a204", "a205", "a206", "a207", "a208", "a209", "a210", "a211", "a212", "a213", "a214", "a215", "a216", "a217", "a218", "a219", "a220", "a221", "a222", "a223", "a224", "a225", "a226", "a227", "a228", "a229", "a230", "a231", "a232", "a233", "a234", "a235", "a236", "a237", "a238", "a239", "a240", "a241", "a242", "a243", "a244", "a245", "a246", "a247", "a248", "a249", "a250", "a251", "a252", "a253", "a254", "a255", "s36", "s37", "s38", "s39", "s40", "s41", "s42", "s43", "s44", "s45", "s46", "s47", "s48", "s49", "s50", "s51", "s52", "s53", "s54", "s55", "s56", "s57", "s58", "s59", "s60", "s61", "s62", "s63", "s64", "s65", "s66", "s67", "s68", "s69", "s70", "s71", "s72", "s73", "s74", "s75", "s76", "s77", "s78", "s79", "s80", "s81", "s82", "s83", "s84", "s85", "s86", "s87", "s88", "s89", "s90", "s91", "s92", "s93", "s94", "s95", "s96", "s97", "vcc", "scc", "memory");
    __builtin_unreachable();
  } else {
    LANE_ID(lane2);
    constexpr int N1 = 33 - (NR4 - 6) * 16, T1 = LAST_TASKS > RLN * N1 ? RLN * N1 : LAST_TASKS;
    const int w = kind == 6 ? 0 : kind == 1 ? 1 + (3 - rep) * 8 + slice : N1 + rep * 8 + slice;
    if (kind != 2) for (int t = w; t < T1; t += N1) row_last(ws, out, t, lane2, wave, ep);
    for (int t = T1 + (w + 32) % (N1 + 32); t < LAST_TASKS; t += N1 + 32) row_last(ws, out, t, lane2, wave, ep);
    if (kind == 2 && rep == 0 && slice == 0 && wave == 0) set_flag(ws + OFF_EP, lane2, ep);
    CTS(14);
    asm volatile("s_endpgm\nLcode_end:" ::: "memory");
    __builtin_unreachable();
  }
}

extern "C" void kernel_launch(void* const* d_in, const int* in_sizes, int n_in, void* d_out, int out_size,
                              void* d_ws, size_t ws_size, hipStream_t stream) {
  const u16* q = (const u16*)d_in[0];
  const u16* k = (const u16*)d_in[1];
  const u16* v = (const u16*)d_in[2];
  const u16* shift_k = (const u16*)d_in[3];
  const u16* ssm_k = (const u16*)d_in[4];
  const u16* ssm_D = (const u16*)d_in[5];
  const u16* w1 = (const u16*)d_in[7];
  const u16* w2 = (const u16*)d_in[8];
  const u16* w3 = (const u16*)d_in[9];
  const u16* w4 = (const u16*)d_in[10];
  flow_kernel<<<256, 256, 0, stream>>>(q, k, v, shift_k, ssm_k, ssm_D, w1, w2, w3, w4, (char*)d_ws, (u16*)d_out);
}
```

```cpp
#include <hip/hip_runtime.h>
#include <hip/hip_bf16.h>
#include <stdint.h>
#include <type_traits>

typedef short s16x8 __attribute__((ext_vector_type(8)));
typedef short s16x4 __attribute__((ext_vector_type(4)));
typedef float f32x4 __attribute__((ext_vector_type(4)));
typedef unsigned short u16;
typedef unsigned short u16x8 __attribute__((ext_vector_type(8)));
typedef unsigned short u16x4 __attribute__((ext_vector_type(4)));
typedef float f32x2 __attribute__((ext_vector_type(2)));
typedef __bf16 bf16x2 __attribute__((ext_vector_type(2)));
typedef unsigned u32x4v __attribute__((ext_vector_type(4)));
typedef __attribute__((address_space(1))) unsigned gu32;

constexpr int S_ = 2048, D_ = 1024, F_ = 3584;

constexpr int WS_USED = 111243392, LAST_TASKS = 100, ALLZ = 1, U0 = 170, U1 = 224, NR4 = 8, RHELP = 1, PRE1 = 1;
constexpr int OFF_KVR = 4194304, OFF_XP = 8388608, OFF_HS = 17825792, OFF_X1 = 31457280, OFF_G = 40894464, OFF_P0 = 71303168, OFF_P1 = 89128960, OFF_Y = 98566144, OFF_S1 = 106954752, OFF_ST = 107479040, OFF_FL = 109051904, OFF_FXP = 109056000, OFF_FH = 109334528, OFF_FX1 = 109613056, OFF_FG = 109891584, OFF_FP = 110587904, OFF_FKV = 111144960, OFF_FY = 111177728, OFF_EP = 111243264, OFF_DBG = 115343360;

#define WS_DIAG 0

__device__ __forceinline__ float bf2f(u16 v) { return __uint_as_float(((unsigned)v) << 16); }
__device__ __forceinline__ u16 f2bf(float f) { return __builtin_bit_cast(u16, (__bf16)f); }
__device__ __forceinline__ float rbf(float f) { return bf2f(f2bf(f)); }
__device__ __forceinline__ void st16(void* p, u16x8 v) {
  asm volatile("global_store_dwordx4 %0, %1, off sc0 sc1\n\ts_nop 1" :: "v"(p), "v"(v) : "memory");
}
#define WAIT_VM(n) asm volatile("s_waitcnt vmcnt(%0)" ::"n"(n) : "memory")
#define WAIT_LGKM0() asm volatile("s_waitcnt lgkmcnt(0)" ::: "memory")

__device__ __forceinline__ void touch_lds(const char* p, unsigned a) {
  asm volatile("s_mov_b32 m0, %1\n\ts_nop 0\n\tglobal_load_lds_dword %0, off" :: "v"(p), "s"(a) : "memory");
}
#define CTS(i)
__device__ __forceinline__ f32x2 up2(unsigned p) { return (f32x2){__uint_as_float(p << 16), __uint_as_float(p & 0xffff0000u)}; }
__device__ __forceinline__ unsigned pk2(f32x2 v) { return __builtin_bit_cast(unsigned, __builtin_convertvector(v, bf16x2)); }
__device__ __forceinline__ f32x2 rbf2(f32x2 v) { return up2(pk2(v)); }
__device__ __forceinline__ void set_flag(char* p, int i, unsigned ep) {
  if (i < 32) __hip_atomic_store((gu32*)p + i, ep, __ATOMIC_RELAXED, __HIP_MEMORY_SCOPE_AGENT);
}
constexpr int CV_HP = (2048 + 32) * 2, CV_UR = (2048 + 256) * 2, CV_CH = 2 * CV_HP + CV_UR;
constexpr int NCV = 174;
constexpr int CV_STG = 8 * CV_CH;
constexpr int YP = D_ * 16;

struct KvRegs { u32x4v k0, k1, k2, k3, k4, k5, k6, v0, v1, v2, v3, s0, s1, s2, s3; };
#define KV_LOADS                                                                                                                       \
  "v_lshrrev_b32 %[a], 3, %[tid]\n\tv_and_b32 %[c], 7, %[tid]\n\ts_lshr_b32 s90, %[tile], 4\n\ts_and_b32 s91, %[tile], 15\n\t"          \
  "v_lshlrev_b32 %[a], 13, %[a]\n\tv_lshlrev_b32 %[c], 4, %[c]\n\ts_lshl_b32 s90, s90, 18\n\ts_lshl_b32 s91, s91, 7\n\t"                 \
  "v_add_u32 %[a], s90, %[a]\n\tv_add_u32 %[c], s91, %[c]\n\tv_subrev_u32 %[h], 0x1000, %[a]\n\tv_max_i32 %[h], 0x800, %[h]\n\t"         \
  "v_add3_u32 %[a], %[a], %[c], s92\n\tv_add_u32 %[h], %[h], %[c]\n\tv_lshlrev_b32 %[c], 2, %[c]\n\ts_waitcnt lgkmcnt(0)\n\t"            \
  "global_load_dwordx4 %3, %[a], s[76:77] offset:-4096 nt\n\tglobal_load_dwordx4 %4, %[a], s[76:77] offset:-2048 nt\n\t"                \
  "global_load_dwordx4 %5, %[a], s[76:77] nt\n\tglobal_load_dwordx4 %6, %[a], s[76:77] offset:2048 nt\n\t"                              \
  "global_load_dwordx4 %7, %[a], s[78:79] offset:-4096 nt\n\tglobal_load_dwordx4 %8, %[a], s[78:79] offset:-2048 nt\n\t"                \
  "global_load_dwordx4 %9, %[a], s[78:79] nt\n\tglobal_load_dwordx4 %10, %[a], s[78:79] offset:2048 nt\n\t"                             \
  "global_load_dwordx4 %0, %[h], s[76:77] offset:-2048 nt\n\tglobal_load_dwordx4 %1, %[h], s[76:77] nt\n\t"                             \
  "global_load_dwordx4 %2, %[h], s[76:77] offset:2048 nt\n\t"                                                                           \
  "global_load_dwordx4 %11, %[c], s[80:81]\n\tglobal_load_dwordx4 %12, %[c], s[80:81] offset:16\n\t"                                    \
  "global_load_dwordx4 %13, %[c], s[80:81] offset:32\n\tglobal_load_dwordx4 %14, %[c], s[80:81] offset:48\n\t"
#define KV_OUTS(R) "=&v"(R.k0), "=&v"(R.k1), "=&v"(R.k2), "=&v"(R.k3), "=&v"(R.k4), "=&v"(R.k5), "=&v"(R.k6), "=&v"(R.v0), "=&v"(R.v1), "=&v"(R.v2), \
                   "=&v"(R.v3), "=&v"(R.s0), "=&v"(R.s1), "=&v"(R.s2), "=&v"(R.s3), [a] "=&v"(ta), [h] "=&v"(th), [c] "=&v"(tc)
#define KV_CLOB "s74", "s75", "s76", "s77", "s78", "s79", "s80", "s81", "s82", "s83", "s90", "s91", "s92", "scc", "vcc", "memory"
template <int N>
__device__ __forceinline__ void kv_wait(KvRegs& R) {
  asm volatile("s_waitcnt vmcnt(%15)" : "+v"(R.k0), "+v"(R.k1), "+v"(R.k2), "+v"(R.k3), "+v"(R.k4), "+v"(R.k5), "+v"(R.k6), "+v"(R.v0), "+v"(R.v1), "+v"(R.v2),
               "+v"(R.v3), "+v"(R.s0), "+v"(R.s1), "+v"(R.s2), "+v"(R.s3) : "n"(N) : "memory");
}
__device__ __forceinline__ void kv_store(KvRegs& R, char* __restrict__ ws, char* T, int b, int tid) {
  const int t0 = (b >> 4) * 128, c0 = (b & 15) * 64, ch8 = tid & 7, rg = tid >> 3;
  if (t0 + 4 * rg < 3) R.k0 = R.k1 = R.k2 = (u32x4v){0u, 0u, 0u, 0u};
  typedef unsigned u32x2 __attribute__((ext_vector_type(2)));
  const u32x4v kr[7] = {R.k0, R.k1, R.k2, R.k3, R.k4, R.k5, R.k6}, vr[4] = {R.v0, R.v1, R.v2, R.v3}, sk[4] = {R.s0, R.s1, R.s2, R.s3};
#pragma unroll
  for (int j2 = 0; j2 < 4; ++j2) {
    const f32x2 sa = up2(sk[j2][0]), sb = up2(sk[j2][1]), sc = up2(sk[j2][2]), sd = up2(sk[j2][3]);
    const f32x2 s0 = {sa[0], sc[0]}, s1 = {sa[1], sc[1]}, s2 = {sb[0], sd[0]}, s3 = {sb[1], sd[1]};
    f32x2 kf[7], o[4];
#pragma unroll
    for (int r = 0; r < 7; ++r) kf[r] = up2(kr[r][j2]);
#pragma unroll
    for (int r = 0; r < 4; ++r)
      o[r] = __builtin_elementwise_fma(kf[r + 3], s0, __builtin_elementwise_fma(kf[r + 2], s1, __builtin_elementwise_fma(kf[r + 1], s2, kf[r] * s3))) * up2(vr[r][j2]);
#pragma unroll
    for (int e = 0; e < 2; ++e)
      *(u32x2*)(T + (8 * ch8 + 2 * j2 + e) * 264 + (124 - 4 * rg) * 2) = (u32x2){pk2((f32x2){o[3][e], o[2][e]}), pk2((f32x2){o[1][e], o[0][e]})};
  }
  __syncthreads();
  u16* kvr = (u16*)(ws + OFF_KVR);
#pragma unroll
  for (int i = 0; i < 4; ++i) {
    const int idx = tid + 256 * i, ch = idx >> 4, cb = idx & 15;
    const u16x4 lo = *(const u16x4*)(T + ch * 264 + cb * 16), hi = *(const u16x4*)(T + ch * 264 + cb * 16 + 8);
    st16(kvr + (size_t)(c0 + ch) * S_ + (1920 - t0) + cb * 8, __builtin_shufflevector(lo, hi, 0, 1, 2, 3, 4, 5, 6, 7));
  }
}

#define LASTSLEEP 4
__device__ __forceinline__ void wait_flags(const char* p, unsigned ep) {
  gu32* f = (gu32*)p;
  for (int it = 0; it < 20000; ++it) {
    if (__all(__hip_atomic_load(f, __ATOMIC_RELAXED, __HIP_MEMORY_SCOPE_AGENT) == ep)) break;
    __builtin_amdgcn_s_sleep(LASTSLEEP);
  }
  asm volatile("" ::: "memory");
}
__device__ __forceinline__ void row_last(char* __restrict__ ws, u16* __restrict__ out, int t, int lane, int wave, unsigned ep) {
  const __amdgpu_buffer_rsrc_t rs = __builtin_amdgcn_make_buffer_rsrc(ws, 0, WS_USED, 0x00020000);
  const int r8 = lane & 7, seg = lane >> 3, un = 128 + (t >> 1);
  const unsigned fo = un * 32768 + (wave * 8 + (seg >> 2)) * 1024 + ((seg & 3) * 16 + (t & 1) * 8 + r8) * 16;
  wait_flags(ws + OFF_FP + un * 2048 + ((lane & 2) * 4 + wave * 2 + (lane & 1)) * 128, ep);
  u32x4v p0[4], p1[4];
#pragma unroll
  for (int i = 0; i < 4; ++i) {
    p0[i] = __builtin_bit_cast(u32x4v, __builtin_amdgcn_raw_buffer_load_b128(rs, fo + i * 2048, OFF_P0, 16));
    p1[i] = __builtin_bit_cast(u32x4v, __builtin_amdgcn_raw_buffer_load_b128(rs, fo + i * 2048, OFF_P1, 16));
  }
#pragma unroll
  for (int i = 0; i < 4; ++i) {
    u32x4v o;
#pragma unroll
    for (int e = 0; e < 4; ++e) o[e] = pk2(up2(p0[i][e]) + up2(p1[i][e]));
    st16(out + (size_t)(t * 8 + r8) * D_ + wave * 256 + i * 64 + seg * 8, __builtin_bit_cast(u16x8, o));
  }
}

__device__ __forceinline__ int swzB(int r) { return ((((r >> 1) & 1) | (((r >> 3) & 1) << 1)) << 1); }
#define LANE_ID(x) int x; asm volatile("v_mbcnt_lo_u32_b32 %0, -1, 0\n\tv_mbcnt_hi_u32_b32 %0, -1, %0" : "=v"(x))
#define W12 6
#define RLN 3
#define QTB 1
constexpr int QT0 = 34 * 128;
#define CODE_MAX(x, y) "(((" x ">" y ")&" x ")|((" x "<=" y ")&" y "))"
constexpr int HU = 40960;
#define G3OR 5
constexpr int NRW = 22 - G3OR;
__global__ __launch_bounds__(256, 1) void flow_kernel(const u16* __restrict__ q, const u16* __restrict__ kin, const u16* __restrict__ vin,
                                                      const u16* __restrict__ shift_k, const u16* __restrict__ ssm_k, const u16* __restrict__ ssm_D,
                                                      const u16* __restrict__ w1, const u16* __restrict__ w2,
                                                      const u16* __restrict__ w3, const u16* __restrict__ w4, char* __restrict__ ws,
                                                      u16* __restrict__ out) {
  __shared__ __attribute__((aligned(16))) char lds[163840];
  KvRegs KR;
  unsigned ew, ta, th, tc;
  int b, tile;
  asm volatile("Lcode_b:\n\ts_load_dwordx4 s[76:79], %[ka], 0x8\n\ts_load_dwordx2 s[82:83], %[ka], 0x50\n\t"
               "s_getpc_b64 s[74:75]\n\ts_lshr_b32 s90, %[bi], 3\n\ts_mov_b32 m0, 0x27f00\n\tv_lshlrev_b32 %[a], 5, %[tid]\n\tv_add_lshl_u32 %[a], %[a], s90, 7\n\t"
               "v_min_u32 %[a], " CODE_MAX(CODE_MAX(CODE_MAX("(Lcode_g3_e-Lcode_b)", "(Lcode_g3o_e-Lcode_b)"), CODE_MAX("(Lcode_g4_e-Lcode_b)", "(Lcode_g12_e-Lcode_b)")), "(Lcode_end-Lcode_b)") "-64, %[a]\n\t"
               "global_load_lds_dword %[a], s[74:75]\n\ts_load_dwordx2 s[80:81], %[ka], 0x18\n\t"
               "s_mov_b32 %[b], %[bi]\n\ts_bfe_u32 s91, %[bi], 0x20001\n\ts_lshl2_add_u32 %[tile], s90, s91\n\ts_movk_i32 s92, 0x1000\n\t"
               "s_lshl_b32 s90, %[bi], 7\n\ts_add_u32 %[tile], %[tile], s90\n\ts_add_u32 %[tile], %[tile], 64\n\ts_and_b32 %[tile], %[tile], 255\n\t"
               KV_LOADS "v_mov_b32 %[a], %[eo]\n\tglobal_load_dword %[ew], %[a], s[82:83] sc1"
               : KV_OUTS(KR), [ew] "=&v"(ew), [b] "=&s"(b), [tile] "=&s"(tile)
               : [ka] "s"(__builtin_amdgcn_kernarg_segment_ptr()), [bi] "s"(blockIdx.x), [tid] "v"(threadIdx.x), [eo] "n"(OFF_EP) : KV_CLOB);
  const unsigned t_start = 0;
  const int tid = threadIdx.x, lane = tid & 63, wave = __builtin_amdgcn_readfirstlane(tid >> 6);
  const int xcd = b & 7, idx = b >> 3;
  const unsigned sLds = (unsigned)(size_t)lds;
  const int o8 = idx & 7;
  int kind, rep, slice, kh = 0;
  int j3 = 0, j4 = 0;
  const int xp = xcd >> 1, pr = xp & 1;
  if (!(xcd & 1)) {
    if (idx < 20) { kind = 4; rep = idx < 16 ? (xp >> 1) * 3 + (idx >> 3) : 2; kh = pr; slice = idx < 16 ? o8 : (xp >> 1) * 4 + o8; j4 = rep * 16 + kh * 8 + slice; }
    else if (idx >= 24) { kind = 1; rep = xp; slice = o8; }
    else { kind = 3; rep = 4; slice = xp * 4 + idx - 20; j3 = 72 + slice; }
  } else if (idx >= 24) { kind = 2; rep = xp; slice = o8; }
  else if (idx < 18) { kind = 3; rep = pr * 2 + (idx >= 9); slice = (xp >> 1) * 9 + idx - 9 * (idx >= 9); j3 = rep * 18 + slice; }
  else if (idx < 22 || b == 191) { kind = 6; rep = 0; slice = idx < 22 ? xp * 4 + idx - 18 : 16; }
  else if (idx == 22 || xp == 0) { kind = 5; rep = idx == 22 ? pr * 2 + (xp >> 1) : 4; slice = 18; j3 = 90 + rep; }
  else { kind = 3; rep = 4; slice = 15 + xp; j3 = 72 + slice; }
  CTS(0);
  const unsigned sScr = sLds + 163584;
  unsigned ep = 0;
  auto w_touch = [&](const u16* w) {
#pragma unroll
    for (int i = 0; i < 2; ++i) touch_lds((const char*)w + (size_t)(rep * 256 + i * 128 + (tid >> 1)) * 2048 + slice * 256 + (tid & 1) * 128, sScr);
  };
  {
  const bool isconv = kind >= 3 && kind != 6 && j3 < 94;
  const int nw = (kind == 4 || j3 >= 92) ? 2 : 1, c0 = kind == 4 ? 8 * j4 : j3 < 92 ? 640 + 4 * j3 : j3 < 94 ? 272 + 8 * j3 : 0, cw = c0 + nw * wave;
  const unsigned hd = isconv ? sLds + 2 * wave * CV_CH + 32 : 0x30000u;
  auto h_dma = [&](bool first) {
#pragma unroll
    for (int j = 0; j < 2; ++j) {
      const u16* hp = ssm_k + (size_t)cw * S_ + (isconv && j < nw ? j * S_ + lane * 8 : 0);
      if (first) asm volatile("s_mov_b32 m0, %1\n\ts_nop 0\n\tglobal_load_lds_dwordx4 %0, off nt" :: "v"(hp), "s"(hd + j * CV_CH) : "memory");
      else asm volatile("s_mov_b32 m0, %1\n\ts_nop 0\n\tglobal_load_lds_dwordx4 %0, off offset:1024 nt\n\t"
                        "global_load_lds_dwordx4 %0, off offset:2048 nt\n\tglobal_load_lds_dwordx4 %0, off offset:3072 nt" :: "v"(hp), "s"(hd + j * CV_CH) : "memory");
    }
  };
  auto w_dma = [&](int t0, int t1) {
    const char* wp = (const char*)w1 + (16 * wave + (lane >> 3)) * 2048 + slice * 256;
    const unsigned c0_ = ((lane & 7) ^ swzB(lane >> 3)) * 16, c1_ = 8 * 2048 + ((lane & 7) ^ swzB(8 + (lane >> 3))) * 16;
#pragma unroll
    for (int t = t0; t < t1; ++t)
#pragma unroll
      for (int i = 0; i < 2; ++i)
        asm volatile("s_mov_b32 m0, %1\n\ts_nop 0\n\tglobal_load_lds_dwordx4 %0, off\n\ts_add_u32 m0, m0, 8064\n\ts_nop 0\n\tglobal_load_lds_dwordx4 %0, off offset:128"
                     :: "v"(wp + t * 131072 + (i ? c1_ : c0_)), "s"(sLds + t * 16384 + wave * 2048 + i * 1024) : "scc", "memory");
  };
  if (PRE1 && kind == 1) w_dma(0, 2);
  else { h_dma(true); h_dma(false); }
  if (isconv) *(u32x4v*)(lds + (2 * wave + (lane >> 5)) * CV_CH + 2 * CV_HP + 4096 + (lane & 31) * 16) = (u32x4v){0u, 0u, 0u, 0u};
  if (isconv && lane < 8) *(u32x4v*)(lds + (2 * wave + (lane >> 2)) * CV_CH + ((lane >> 1) & 1) * CV_HP + (lane & 1) * 16) = (u32x4v){0u, 0u, 0u, 0u};
  kv_wait<9>(KR);
  CTS(12);
  kv_store(KR, ws, lds + CV_STG, tile, tid);
  CTS(1);
  if (PRE1 && kind == 1) w_dma(2, 6);
  const unsigned dw = ((const unsigned*)ssm_D)[cw >> 1];
  asm volatile("s_cmp_eq_u32 %1, %2\n\ts_cbranch_scc1 Lew%=\n\ts_waitcnt vmcnt(0)\nLew%=:\n\ts_waitcnt vmcnt(16)" : "+v"(ew) : "s"(__builtin_amdgcn_readfirstlane(kind)), "n"(PRE1 ? 1 : 99) : "scc", "memory");
  ep = __builtin_amdgcn_readfirstlane(ew) + 1u;
  if (ep == 0u) ep = 1u;
  __builtin_amdgcn_s_barrier();
  set_flag(ws + OFF_FKV + tile * 128, tid, ep);
  CTS(2);
  if (kind < 3 && (W12 & kind) != 0) w_touch(kind == 1 ? w1 : w2);
  if (kind == 2 && (W12 & 4) != 0) w_touch(w1);
  if (isconv) {
    LANE_ID(lane);
    const int tid = wave * 64 + lane;
    const unsigned sC0 = (cw & 1) ? dw & 0xffff0000u : dw << 16, sC1 = dw & 0xffff0000u;
    const int l15 = lane & 15, lg = lane >> 4, cp = (l15 + 1) & 1, iw = kind == 4 ? j4 : 80 + j3, own = 2 * wave * CV_CH;
    const int ch = tid & (4 * nw - 1), tb = nw == 2 ? tid >> 3 : tid >> 2, slot = nw == 2 ? ch : 2 * ch;
    const unsigned sWave = wave, sBuf = nw, sAuxOff = OFF_KVR + cw * 4096, sFoutOff = OFF_FY + iw * 128, sExp = __builtin_amdgcn_readfirstlane(ep), sDbg = 110u * 1048576u + (b * 4 + wave) * 8192;
    const unsigned vL16 = lane * 16, vPA = own + cp * CV_HP + 2 * (8 * lg + l15 + 1 - cp), vPB = own + 2 * CV_HP + 2 * (2032 - 16 * l15 + 8 * lg) - 512 * 7;
    const unsigned vPU = own + 2 * CV_HP + 2 * (2044 - 16 * l15 - 4 * lg) - 512 * 7, vPW = 32 * l15 + 8 * lg;
    const unsigned vOR = slot * CV_CH + tb * 16, vORA = CV_STG + slot * 1024 + tb * 16, vOG = tb * YP + (c0 + ch) * 16;
    const unsigned vFA = ((lane & 3) * 16 + (c0 >> 6)) * 128, vFB = ((4 + lane % 12) * 16 + (c0 >> 6)) * 128, vFlag = tid < 32 ? tid * 4 : 0x80000000u;
    const unsigned vTW = lane < 42 ? (iw * 168 + wave * 42 + lane) * 128 : 0x80000000u;
    const unsigned vTX = lane < 44 ? ((kind == 4 ? j4 : j3 - 12) * 176 + wave * 44 + lane) * 128 : 0x80000000u;
    (void)sDbg;
    asm volatile(
R"(.macro M d,w,x
v_mfma_f32_16x16x32_bf16 v[\d:\d+3], \w, v[\x:\x+3], v[\d:\d+3]
.endm
.macro R d,a,o
ds_read_b128 v[\d:\d+3], v\a offset:\o
.endm
.macro T d,a,o
ds_read_b64_tr_b16 \d, v\a offset:\o
.endm
.macro D a,s,o
buffer_load_dwordx4 v\a, s[36:39], s\s offen offset:\o sc1 lds
.endm
.macro L c
s_waitcnt lgkmcnt(\c)
.endm
Lcode_cv_b:
s_mov_b64 s[36:37],%0
s_mov_b32 s38,111243392
s_mov_b32 s39,0x00020000
s_mov_b32 s44,%1
s_mov_b32 s51,%2
s_mov_b32 s49,%3
s_mov_b32 s48,%4
s_mov_b32 s86,%5
s_mov_b32 s87,%6
s_mov_b32 s53,%7
v_mov_b32 v0,%8
v_mov_b32 v1,%9
v_mov_b32 v2,%10
v_mov_b32 v3,%11
v_mov_b32 v4,%12
v_mov_b32 v5,%13
v_mov_b32 v6,%14
v_mov_b32 v7,%15
v_mov_b32 v8,%16
v_mov_b32 v9,%17
v_mov_b32 v10,%18
v_mov_b32 v11,%19
v_mov_b32 v12,%20
s_mov_b32 s58,111144960
buffer_load_dword v13,v8,s[36:39],s58 offen offset:0 sc1
s_mul_i32 s57,s44,25856
v_add_u32 v18,s57,v0
v_add_u32 v18,32,v18
v_mov_b32 v19,s57
R 88,18,0
ds_read_b32 v92,v18 offset:16
R 152,18,12928
ds_read_b32 v156,v18 offset:12944
ds_read_b32 v24,v19 offset:32
ds_read_b32 v25,v19 offset:12960
s_mov_b64 s[40:41],%21
s_mov_b32 s42,7340032
s_mov_b32 s43,0x00020000
s_mov_b64 s[60:61],%22
s_mov_b32 s62,7340032
s_mov_b32 s63,0x00020000
v_mov_b32 v23,s53
s_mov_b32 s82,0
s_add_u32 s55,s49,4096
s_cmp_eq_u32 s51,2
s_cselect_b32 s55,s55,0x7fff0000
s_cselect_b32 s56,0,0x70000000
s_add_u32 s57,s57,8320
v_add_u32 v14,1024,v1
v_add_u32 v15,2048,v1
v_add_u32 v16,3072,v1
v_add_u32 v17,12928,v1
L 4
v_alignbyte_b32 v96,v89,v88,2
v_alignbyte_b32 v97,v90,v89,2
v_alignbyte_b32 v98,v91,v90,2
v_alignbyte_b32 v99,v92,v91,2
ds_write_b128 v18,v[96:99] offset:4160
L 3
v_alignbyte_b32 v160,v153,v152,2
v_alignbyte_b32 v161,v154,v153,2
v_alignbyte_b32 v162,v155,v154,2
v_alignbyte_b32 v163,v156,v155,2
ds_write_b128 v18,v[160:163] offset:17088
L 2
v_lshlrev_b32 v24,16,v24
v_lshlrev_b32 v25,16,v25
s_mov_b64 exec,1
ds_write_b32 v19,v24 offset:4188
ds_write_b32 v19,v25 offset:17116
s_mov_b64 exec,-1
s_lshl_b32 s78,s44,11
s_add_u32 s78,s78,103424
v_add_u32 v216,s78,v4
v_add_u32 v4,v19,v4
Lcv_bpa_%=:
s_waitcnt vmcnt(0)
v_cmp_eq_u32 vcc,s53,v13
s_add_u32 s82,s82,1
s_cmp_eq_u64 vcc,-1
s_cbranch_scc1 Lcv_bqa_%=
s_sleep 1
buffer_load_dword v13,v8,s[36:39],s58 offen offset:0 sc1
s_cmp_lt_u32 s82,20000
s_cbranch_scc1 Lcv_bpa_%=
s_waitcnt vmcnt(0)
Lcv_bqa_%=:
s_add_u32 m0,s57,0
s_nop 0
D 0,49,3072
s_add_u32 m0,s57,12928
s_nop 0
D 0,55,3072
ds_read2_b32 a[0:1],v1 offset0:0 offset1:1
ds_read2_b32 a[2:3],v1 offset0:2 offset1:3
ds_read2_b32 a[4:5],v1 offset0:128 offset1:129
ds_read2_b32 a[6:7],v1 offset0:130 offset1:131
ds_read2_b32 a[8:9],v1 offset0:16 offset1:17
ds_read2_b32 a[10:11],v1 offset0:18 offset1:19
ds_read2_b32 a[12:13],v1 offset0:144 offset1:145
ds_read2_b32 a[14:15],v1 offset0:146 offset1:147
ds_read2_b32 a[16:17],v1 offset0:32 offset1:33
ds_read2_b32 a[18:19],v1 offset0:34 offset1:35
ds_read2_b32 a[20:21],v1 offset0:160 offset1:161
ds_read2_b32 a[22:23],v1 offset0:162 offset1:163
ds_read2_b32 a[24:25],v1 offset0:48 offset1:49
ds_read2_b32 a[26:27],v1 offset0:50 offset1:51
ds_read2_b32 a[28:29],v1 offset0:176 offset1:177
ds_read2_b32 a[30:31],v1 offset0:178 offset1:179
ds_read2_b32 a[32:33],v1 offset0:64 offset1:65
ds_read2_b32 a[34:35],v1 offset0:66 offset1:67
ds_read2_b32 a[36:37],v1 offset0:192 offset1:193
ds_read2_b32 a[38:39],v1 offset0:194 offset1:195
ds_read2_b32 a[40:41],v1 offset0:80 offset1:81
ds_read2_b32 a[42:43],v1 offset0:82 offset1:83
ds_read2_b32 a[44:45],v1 offset0:208 offset1:209
ds_read2_b32 a[46:47],v1 offset0:210 offset1:211
ds_read2_b32 a[48:49],v1 offset0:96 offset1:97
ds_read2_b32 a[50:51],v1 offset0:98 offset1:99
ds_read2_b32 a[52:53],v1 offset0:224 offset1:225
ds_read2_b32 a[54:55],v1 offset0:226 offset1:227
ds_read2_b32 a[56:57],v1 offset0:112 offset1:113
ds_read2_b32 a[58:59],v1 offset0:114 offset1:115
ds_read2_b32 a[60:61],v1 offset0:240 offset1:241
ds_read2_b32 a[62:63],v1 offset0:242 offset1:243
s_cmp_eq_u32 s51,2
s_cbranch_scc1 Lcv_a2_%=
s_waitcnt vmcnt(0)
buffer_load_dword v13,v9,s[36:39],s58 offen offset:0 sc1
L 0
R 88,2,3584
R 92,2,3072
R 96,2,3648
R 100,2,3136
R 104,2,3712
R 108,2,3200
R 112,2,3776
R 116,2,3264
L 6
v_mfma_f32_16x16x32_bf16 v[40:43],a[0:3],v[88:91],0
R 120,2,3840
R 124,2,3328
v_mfma_f32_16x16x32_bf16 v[44:47],a[0:3],v[92:95],0
M 44,a[4:7],88
L 6
M 40,a[8:11],96
R 128,2,3904
R 132,2,3392
M 44,a[8:11],100
M 44,a[12:15],96
L 6
M 40,a[16:19],104
R 136,2,3968
R 140,2,3456
M 44,a[16:19],108
M 44,a[20:23],104
L 6
M 40,a[24:27],112
R 144,2,4032
R 148,2,3520
M 44,a[24:27],116
M 44,a[28:31],112
L 6
M 40,a[32:35],120
M 44,a[32:35],124
M 44,a[36:39],120
L 4
M 40,a[40:43],128
M 44,a[40:43],132
M 44,a[44:47],128
L 2
M 40,a[48:51],136
ds_read_b64 v[24:25],v3 offset:3584
ds_read_b64 v[26:27],v3 offset:3072
M 44,a[48:51],140
M 44,a[52:55],136
L 2
M 40,a[56:59],144
M 44,a[56:59],148
M 44,a[60:63],144
L 0
v_and_b32 v36,0xffff0000,v25
v_lshlrev_b32 v37,16,v25
v_and_b32 v38,0xffff0000,v24
v_lshlrev_b32 v39,16,v24
s_nop 0
v_fma_f32 v40,v36,s86,v40
v_fma_f32 v41,v37,s86,v41
v_fma_f32 v42,v38,s86,v42
v_fma_f32 v43,v39,s86,v43
v_cvt_pk_bf16_f32 v40,v40,v41
v_cvt_pk_bf16_f32 v41,v42,v43
ds_write_b64 v216,v[40:41] offset:0
v_and_b32 v36,0xffff0000,v27
v_lshlrev_b32 v37,16,v27
v_and_b32 v38,0xffff0000,v26
v_lshlrev_b32 v39,16,v26
v_fma_f32 v44,v36,s86,v44
v_fma_f32 v45,v37,s86,v45
v_fma_f32 v46,v38,s86,v46
v_fma_f32 v47,v39,s86,v47
v_cvt_pk_bf16_f32 v44,v44,v45
v_cvt_pk_bf16_f32 v45,v46,v47
ds_write_b64 v216,v[44:45] offset:512
s_branch Lcv_a3_%=
Lcv_a2_%=:
ds_read2_b32 a[64:65],v17 offset0:0 offset1:1
ds_read2_b32 a[66:67],v17 offset0:2 offset1:3
ds_read2_b32 a[68:69],v17 offset0:128 offset1:129
ds_read2_b32 a[70:71],v17 offset0:130 offset1:131
ds_read2_b32 a[72:73],v17 offset0:16 offset1:17
ds_read2_b32 a[74:75],v17 offset0:18 offset1:19
ds_read2_b32 a[76:77],v17 offset0:144 offset1:145
ds_read2_b32 a[78:79],v17 offset0:146 offset1:147
ds_read2_b32 a[80:81],v17 offset0:32 offset1:33
ds_read2_b32 a[82:83],v17 offset0:34 offset1:35
ds_read2_b32 a[84:85],v17 offset0:160 offset1:161
ds_read2_b32 a[86:87],v17 offset0:162 offset1:163
ds_read2_b32 a[88:89],v17 offset0:48 offset1:49
ds_read2_b32 a[90:91],v17 offset0:50 offset1:51
ds_read2_b32 a[92:93],v17 offset0:176 offset1:177
ds_read2_b32 a[94:95],v17 offset0:178 offset1:179
ds_read2_b32 a[96:97],v17 offset0:64 offset1:65
ds_read2_b32 a[98:99],v17 offset0:66 offset1:67
ds_read2_b32 a[100:101],v17 offset0:192 offset1:193
ds_read2_b32 a[102:103],v17 offset0:194 offset1:195
ds_read2_b32 a[104:105],v17 offset0:80 offset1:81
ds_read2_b32 a[106:107],v17 offset0:82 offset1:83
ds_read2_b32 a[108:109],v17 offset0:208 offset1:209
ds_read2_b32 a[110:111],v17 offset0:210 offset1:211
ds_read2_b32 a[112:113],v17 offset0:96 offset1:97
ds_read2_b32 a[114:115],v17 offset0:98 offset1:99
ds_read2_b32 a[116:117],v17 offset0:224 offset1:225
ds_read2_b32 a[118:119],v17 offset0:226 offset1:227
ds_read2_b32 a[120:121],v17 offset0:112 offset1:113
ds_read2_b32 a[122:123],v17 offset0:114 offset1:115
ds_read2_b32 a[124:125],v17 offset0:240 offset1:241
ds_read2_b32 a[126:127],v17 offset0:242 offset1:243
s_waitcnt vmcnt(0)
buffer_load_dword v13,v9,s[36:39],s58 offen offset:0 sc1
L 0
R 88,2,3584
R 92,2,3072
R 96,2,3648
R 100,2,3136
R 104,2,3712
R 108,2,3200
R 112,2,3776
R 116,2,3264
L 6
v_mfma_f32_16x16x32_bf16 v[40:43],a[0:3],v[88:91],0
R 120,2,3840
R 124,2,3328
v_mfma_f32_16x16x32_bf16 v[44:47],a[0:3],v[92:95],0
M 44,a[4:7],88
L 6
M 40,a[8:11],96
R 128,2,3904
R 132,2,3392
M 44,a[8:11],100
M 44,a[12:15],96
L 6
M 40,a[16:19],104
R 136,2,3968
R 140,2,3456
M 44,a[16:19],108
M 44,a[20:23],104
L 6
M 40,a[24:27],112
R 144,2,4032
R 148,2,3520
M 44,a[24:27],116
M 44,a[28:31],112
L 6
M 40,a[32:35],120
R 152,2,16512
R 156,2,16000
M 44,a[32:35],124
M 44,a[36:39],120
L 6
M 40,a[40:43],128
R 160,2,16576
R 164,2,16064
M 44,a[40:43],132
M 44,a[44:47],128
L 6
M 40,a[48:51],136
R 168,2,16640
R 172,2,16128
M 44,a[48:51],140
ds_read_b64 v[24:25],v3 offset:3584
ds_read_b64 v[26:27],v3 offset:3072
M 44,a[52:55],136
L 8
M 40,a[56:59],144
R 176,2,16704
R 180,2,16192
M 44,a[56:59],148
M 44,a[60:63],144
L 8
v_mfma_f32_16x16x32_bf16 v[48:51],a[64:67],v[152:155],0
R 184,2,16768
R 188,2,16256
v_mfma_f32_16x16x32_bf16 v[52:55],a[64:67],v[156:159],0
M 52,a[68:71],152
L 8
M 48,a[72:75],160
R 192,2,16832
R 196,2,16320
M 52,a[72:75],164
L 6
v_and_b32 v36,0xffff0000,v25
M 52,a[76:79],160
v_lshlrev_b32 v37,16,v25
v_and_b32 v38,0xffff0000,v24
v_lshlrev_b32 v39,16,v24
s_nop 0
M 48,a[80:83],168
R 200,2,16896
R 204,2,16384
M 52,a[80:83],172
v_fma_f32 v40,v36,s86,v40
v_fma_f32 v41,v37,s86,v41
M 52,a[84:87],168
v_fma_f32 v42,v38,s86,v42
v_fma_f32 v43,v39,s86,v43
v_cvt_pk_bf16_f32 v40,v40,v41
L 6
M 48,a[88:91],176
R 208,2,16960
R 212,2,16448
M 52,a[88:91],180
v_cvt_pk_bf16_f32 v41,v42,v43
ds_write_b64 v216,v[40:41] offset:0
M 52,a[92:95],176
v_and_b32 v36,0xffff0000,v27
v_lshlrev_b32 v37,16,v27
v_and_b32 v38,0xffff0000,v26
L 7
M 48,a[96:99],184
M 52,a[96:99],188
v_lshlrev_b32 v39,16,v26
v_fma_f32 v44,v36,s86,v44
M 52,a[100:103],184
v_fma_f32 v45,v37,s86,v45
v_fma_f32 v46,v38,s86,v46
v_fma_f32 v47,v39,s86,v47
L 5
M 48,a[104:107],192
M 52,a[104:107],196
v_cvt_pk_bf16_f32 v44,v44,v45
v_cvt_pk_bf16_f32 v45,v46,v47
M 52,a[108:111],192
ds_write_b64 v216,v[44:45] offset:512
L 4
M 48,a[112:115],200
ds_read_b64 v[24:25],v3 offset:16512
ds_read_b64 v[26:27],v3 offset:16000
M 52,a[112:115],204
M 52,a[116:119],200
L 4
M 48,a[120:123],208
M 52,a[120:123],212
M 52,a[124:127],208
L 0
v_and_b32 v36,0xffff0000,v25
v_lshlrev_b32 v37,16,v25
v_and_b32 v38,0xffff0000,v24
v_lshlrev_b32 v39,16,v24
s_nop 0
v_fma_f32 v48,v36,s87,v48
v_fma_f32 v49,v37,s87,v49
v_fma_f32 v50,v38,s87,v50
v_fma_f32 v51,v39,s87,v51
v_cvt_pk_bf16_f32 v48,v48,v49
v_cvt_pk_bf16_f32 v49,v50,v51
ds_write_b64 v216,v[48:49] offset:1024
v_and_b32 v36,0xffff0000,v27
v_lshlrev_b32 v37,16,v27
v_and_b32 v38,0xffff0000,v26
v_lshlrev_b32 v39,16,v26
v_fma_f32 v52,v36,s87,v52
v_fma_f32 v53,v37,s87,v53
v_fma_f32 v54,v38,s87,v54
v_fma_f32 v55,v39,s87,v55
v_cvt_pk_bf16_f32 v52,v52,v53
v_cvt_pk_bf16_f32 v53,v54,v55
ds_write_b64 v216,v[52:53] offset:1536
Lcv_a3_%=:
L 0
s_barrier
R 152,6,0
R 156,6,512
s_mov_b32 s78,98566144
L 1
buffer_store_dwordx4 v[152:155],v7,s[36:39],s78 offen offset:0 sc0 sc1
s_add_u32 s78,s56,99090432
L 0
buffer_store_dwordx4 v[156:159],v7,s[36:39],s78 offen offset:0 sc0 sc1
s_waitcnt vmcnt(2)
v_cmp_eq_u32 vcc,s53,v13
s_nop 0
s_cmp_eq_u64 vcc,-1
s_cbranch_scc0 Lcv_a_%=
s_add_u32 m0,s57,0
s_nop 0
D 0,49,2048
D 0,49,1024
D 0,49,0
s_add_u32 m0,s57,12928
s_nop 0
D 0,55,2048
D 0,55,1024
D 0,55,0
R 104,18,1024
ds_read_b32 v108,v18 offset:1040
R 120,18,2048
ds_read_b32 v124,v18 offset:2064
R 136,18,3072
ds_read_b32 v140,v18 offset:3088
L 4
v_alignbyte_b32 v112,v105,v104,2
v_alignbyte_b32 v113,v106,v105,2
v_alignbyte_b32 v114,v107,v106,2
v_alignbyte_b32 v115,v108,v107,2
ds_write_b128 v18,v[112:115] offset:5184
L 3
v_alignbyte_b32 v128,v121,v120,2
v_alignbyte_b32 v129,v122,v121,2
v_alignbyte_b32 v130,v123,v122,2
v_alignbyte_b32 v131,v124,v123,2
ds_write_b128 v18,v[128:131] offset:6208
L 2
v_alignbyte_b32 v144,v137,v136,2
v_alignbyte_b32 v145,v138,v137,2
v_alignbyte_b32 v146,v139,v138,2
v_alignbyte_b32 v147,v140,v139,2
ds_write_b128 v18,v[144:147] offset:7232
s_waitcnt vmcnt(6)
s_barrier
buffer_store_dword v23,v10,s[36:39],s48 offen offset:0 sc0 sc1
s_branch Lcv_b_%=
Lcv_a_%=:
R 104,18,1024
ds_read_b32 v108,v18 offset:1040
R 120,18,2048
ds_read_b32 v124,v18 offset:2064
R 136,18,3072
ds_read_b32 v140,v18 offset:3088
L 4
v_alignbyte_b32 v112,v105,v104,2
v_alignbyte_b32 v113,v106,v105,2
v_alignbyte_b32 v114,v107,v106,2
v_alignbyte_b32 v115,v108,v107,2
ds_write_b128 v18,v[112:115] offset:5184
L 3
v_alignbyte_b32 v128,v121,v120,2
v_alignbyte_b32 v129,v122,v121,2
v_alignbyte_b32 v130,v123,v122,2
v_alignbyte_b32 v131,v124,v123,2
ds_write_b128 v18,v[128:131] offset:6208
L 2
v_alignbyte_b32 v144,v137,v136,2
v_alignbyte_b32 v145,v138,v137,2
v_alignbyte_b32 v146,v139,v138,2
v_alignbyte_b32 v147,v140,v139,2
ds_write_b128 v18,v[144:147] offset:7232
s_waitcnt vmcnt(0)
s_barrier
buffer_store_dword v23,v10,s[36:39],s48 offen offset:0 sc0 sc1
buffer_load_dword v13,v9,s[36:39],s58 offen offset:0 sc1
Lcv_bpb_%=:
s_waitcnt vmcnt(0)
v_cmp_eq_u32 vcc,s53,v13
s_add_u32 s82,s82,1
s_cmp_eq_u64 vcc,-1
s_cbranch_scc1 Lcv_bqb_%=
s_sleep 1
buffer_load_dword v13,v9,s[36:39],s58 offen offset:0 sc1
s_cmp_lt_u32 s82,20000
s_cbranch_scc1 Lcv_bpb_%=
s_waitcnt vmcnt(0)
Lcv_bqb_%=:
s_add_u32 m0,s57,0
)"
R"(s_nop 0
D 0,49,2048
D 0,49,1024
D 0,49,0
s_add_u32 m0,s57,12928
s_nop 0
D 0,55,2048
D 0,55,1024
D 0,55,0
Lcv_b_%=:
s_waitcnt vmcnt(0)
ds_read2_b32 v[88:89],v1 offset0:0 offset1:1
ds_read2_b32 v[90:91],v1 offset0:2 offset1:3
R 120,2,3584
ds_read2_b32 v[92:93],v1 offset0:128 offset1:129
ds_read2_b32 v[94:95],v1 offset0:130 offset1:131
R 124,2,3072
ds_read2_b32 v[96:97],v14 offset0:0 offset1:1
ds_read2_b32 v[98:99],v14 offset0:2 offset1:3
R 128,2,2560
ds_read2_b32 v[100:101],v14 offset0:128 offset1:129
ds_read2_b32 v[102:103],v14 offset0:130 offset1:131
R 132,2,2048
ds_read2_b32 v[104:105],v15 offset0:0 offset1:1
ds_read2_b32 v[106:107],v15 offset0:2 offset1:3
R 136,2,1536
ds_read2_b32 v[108:109],v15 offset0:128 offset1:129
ds_read2_b32 v[110:111],v15 offset0:130 offset1:131
R 140,2,1024
ds_read2_b32 v[112:113],v16 offset0:0 offset1:1
ds_read2_b32 v[114:115],v16 offset0:2 offset1:3
R 144,2,512
ds_read2_b32 v[116:117],v16 offset0:128 offset1:129
ds_read2_b32 v[118:119],v16 offset0:130 offset1:131
R 148,2,0
L 15
v_mfma_f32_16x16x32_bf16 v[40:43],v[88:91],v[128:131],0
ds_read2_b32 v[152:153],v1 offset0:16 offset1:17
M 40,v[92:95],124
ds_read2_b32 v[154:155],v1 offset0:18 offset1:19
M 40,v[96:99],120
L 14
R 184,2,3648
v_mfma_f32_16x16x32_bf16 v[44:47],v[88:91],v[132:135],0
ds_read2_b32 v[156:157],v1 offset0:144 offset1:145
M 44,v[92:95],128
M 44,v[96:99],124
ds_read2_b32 v[158:159],v1 offset0:146 offset1:147
M 44,v[100:103],120
L 14
R 188,2,3136
v_mfma_f32_16x16x32_bf16 v[48:51],v[88:91],v[136:139],0
ds_read2_b32 v[160:161],v14 offset0:16 offset1:17
M 48,v[92:95],132
ds_read2_b32 v[162:163],v14 offset0:18 offset1:19
M 48,v[96:99],128
M 48,v[100:103],124
R 192,2,2624
M 48,v[104:107],120
L 15
ds_read2_b32 v[164:165],v14 offset0:144 offset1:145
v_mfma_f32_16x16x32_bf16 v[52:55],v[88:91],v[140:143],0
ds_read2_b32 v[166:167],v14 offset0:146 offset1:147
M 52,v[92:95],136
R 196,2,2112
M 52,v[96:99],132
M 52,v[100:103],128
ds_read2_b32 v[168:169],v15 offset0:16 offset1:17
M 52,v[104:107],124
ds_read2_b32 v[170:171],v15 offset0:18 offset1:19
M 52,v[108:111],120
L 15
R 200,2,1600
v_mfma_f32_16x16x32_bf16 v[56:59],v[88:91],v[144:147],0
ds_read2_b32 v[172:173],v15 offset0:144 offset1:145
M 56,v[92:95],140
M 56,v[96:99],136
ds_read2_b32 v[174:175],v15 offset0:146 offset1:147
M 56,v[100:103],132
R 204,2,1088
M 56,v[104:107],128
ds_read2_b32 v[176:177],v16 offset0:16 offset1:17
M 56,v[108:111],124
ds_read2_b32 v[178:179],v16 offset0:18 offset1:19
M 56,v[112:115],120
L 15
v_mfma_f32_16x16x32_bf16 v[60:63],v[88:91],v[148:151],0
R 208,2,576
M 60,v[92:95],144
ds_read2_b32 v[180:181],v16 offset0:144 offset1:145
M 60,v[96:99],140
ds_read2_b32 v[182:183],v16 offset0:146 offset1:147
M 60,v[100:103],136
R 212,2,64
M 60,v[104:107],132
M 60,v[108:111],128
M 60,v[112:115],124
M 60,v[116:119],120
L 15
M 40,v[152:155],192
ds_read2_b32 v[88:89],v1 offset0:32 offset1:33
M 40,v[156:159],188
ds_read2_b32 v[90:91],v1 offset0:34 offset1:35
M 40,v[160:163],184
L 14
R 120,2,3712
M 44,v[152:155],196
ds_read2_b32 v[92:93],v1 offset0:160 offset1:161
M 44,v[156:159],192
M 44,v[160:163],188
ds_read2_b32 v[94:95],v1 offset0:162 offset1:163
M 44,v[164:167],184
L 14
R 124,2,3200
M 48,v[152:155],200
ds_read2_b32 v[96:97],v14 offset0:32 offset1:33
M 48,v[156:159],196
ds_read2_b32 v[98:99],v14 offset0:34 offset1:35
M 48,v[160:163],192
M 48,v[164:167],188
R 128,2,2688
M 48,v[168:171],184
L 15
ds_read2_b32 v[100:101],v14 offset0:160 offset1:161
M 52,v[152:155],204
ds_read2_b32 v[102:103],v14 offset0:162 offset1:163
M 52,v[156:159],200
R 132,2,2176
M 52,v[160:163],196
M 52,v[164:167],192
ds_read2_b32 v[104:105],v15 offset0:32 offset1:33
M 52,v[168:171],188
ds_read2_b32 v[106:107],v15 offset0:34 offset1:35
M 52,v[172:175],184
L 15
R 136,2,1664
M 56,v[152:155],208
ds_read2_b32 v[108:109],v15 offset0:160 offset1:161
M 56,v[156:159],204
M 56,v[160:163],200
ds_read2_b32 v[110:111],v15 offset0:162 offset1:163
M 56,v[164:167],196
R 140,2,1152
M 56,v[168:171],192
ds_read2_b32 v[112:113],v16 offset0:32 offset1:33
M 56,v[172:175],188
ds_read2_b32 v[114:115],v16 offset0:34 offset1:35
M 56,v[176:179],184
L 15
M 60,v[152:155],212
R 144,2,640
M 60,v[156:159],208
ds_read2_b32 v[116:117],v16 offset0:160 offset1:161
M 60,v[160:163],204
ds_read2_b32 v[118:119],v16 offset0:162 offset1:163
R 218,18,13952
M 60,v[164:167],200
R 148,2,128
ds_read_b32 v222,v18 offset:13968
M 60,v[168:171],196
M 60,v[172:175],192
M 60,v[176:179],188
M 60,v[180:183],184
L 15
M 40,v[88:91],128
ds_read2_b32 v[152:153],v1 offset0:48 offset1:49
M 40,v[92:95],124
L 1
ds_read2_b32 v[154:155],v1 offset0:50 offset1:51
v_alignbyte_b32 v226,v219,v218,2
M 40,v[96:99],120
s_nop 0
R 184,2,3776
v_alignbyte_b32 v227,v220,v219,2
M 44,v[88:91],132
ds_read2_b32 v[156:157],v1 offset0:176 offset1:177
v_alignbyte_b32 v228,v221,v220,2
M 44,v[92:95],128
v_alignbyte_b32 v229,v222,v221,2
M 44,v[96:99],124
ds_read2_b32 v[158:159],v1 offset0:178 offset1:179
M 44,v[100:103],120
s_nop 0
R 188,2,3264
M 48,v[88:91],136
ds_read2_b32 v[160:161],v14 offset0:48 offset1:49
M 48,v[92:95],132
ds_read2_b32 v[162:163],v14 offset0:50 offset1:51
M 48,v[96:99],128
ds_write_b128 v18,v[226:229] offset:18112
M 48,v[100:103],124
R 192,2,2752
M 48,v[104:107],120
s_nop 0
ds_read2_b32 v[164:165],v14 offset0:176 offset1:177
M 52,v[88:91],140
s_mov_b32 m0,163584
s_mov_b32 s78,0
ds_read2_b32 v[166:167],v14 offset0:178 offset1:179
M 52,v[92:95],136
R 196,2,2240
M 52,v[96:99],132
buffer_load_dword v11,s[40:43],s78 offen offset:0  lds
M 52,v[100:103],128
ds_read2_b32 v[168:169],v15 offset0:48 offset1:49
M 52,v[104:107],124
ds_read2_b32 v[170:171],v15 offset0:50 offset1:51
M 52,v[108:111],120
s_nop 0
R 200,2,1728
M 56,v[88:91],144
ds_read2_b32 v[172:173],v15 offset0:176 offset1:177
M 56,v[92:95],140
M 56,v[96:99],136
ds_read2_b32 v[174:175],v15 offset0:178 offset1:179
M 56,v[100:103],132
R 204,2,1216
M 56,v[104:107],128
ds_read2_b32 v[176:177],v16 offset0:48 offset1:49
M 56,v[108:111],124
ds_read2_b32 v[178:179],v16 offset0:50 offset1:51
M 56,v[112:115],120
s_nop 0
M 60,v[88:91],148
R 208,2,704
M 60,v[92:95],144
ds_read2_b32 v[180:181],v16 offset0:176 offset1:177
M 60,v[96:99],140
ds_read2_b32 v[182:183],v16 offset0:178 offset1:179
M 60,v[100:103],136
R 212,2,192
M 60,v[104:107],132
M 60,v[108:111],128
M 60,v[112:115],124
M 60,v[116:119],120
L 15
M 40,v[152:155],192
ds_read2_b32 v[88:89],v1 offset0:64 offset1:65
M 40,v[156:159],188
ds_read2_b32 v[90:91],v1 offset0:66 offset1:67
R 218,18,14976
M 40,v[160:163],184
L 15
R 120,2,3840
ds_read_b32 v222,v18 offset:14992
M 44,v[152:155],196
ds_read2_b32 v[92:93],v1 offset0:192 offset1:193
M 44,v[156:159],192
M 44,v[160:163],188
ds_read2_b32 v[94:95],v1 offset0:194 offset1:195
M 44,v[164:167],184
L 15
R 124,2,3328
M 48,v[152:155],200
ds_read2_b32 v[96:97],v14 offset0:64 offset1:65
M 48,v[156:159],196
L 4
ds_read2_b32 v[98:99],v14 offset0:66 offset1:67
v_alignbyte_b32 v226,v219,v218,2
M 48,v[160:163],192
v_alignbyte_b32 v227,v220,v219,2
v_alignbyte_b32 v228,v221,v220,2
M 48,v[164:167],188
R 128,2,2816
v_alignbyte_b32 v229,v222,v221,2
M 48,v[168:171],184
s_nop 0
ds_read2_b32 v[100:101],v14 offset0:192 offset1:193
M 52,v[152:155],204
ds_read2_b32 v[102:103],v14 offset0:194 offset1:195
M 52,v[156:159],200
R 132,2,2304
M 52,v[160:163],196
ds_write_b128 v18,v[226:229] offset:19136
M 52,v[164:167],192
ds_read2_b32 v[104:105],v15 offset0:64 offset1:65
M 52,v[168:171],188
ds_read2_b32 v[106:107],v15 offset0:66 offset1:67
M 52,v[172:175],184
s_nop 0
R 136,2,1792
M 56,v[152:155],208
ds_read2_b32 v[108:109],v15 offset0:192 offset1:193
M 56,v[156:159],204
M 56,v[160:163],200
ds_read2_b32 v[110:111],v15 offset0:194 offset1:195
M 56,v[164:167],196
R 140,2,1280
M 56,v[168:171],192
ds_read2_b32 v[112:113],v16 offset0:64 offset1:65
M 56,v[172:175],188
ds_read2_b32 v[114:115],v16 offset0:66 offset1:67
M 56,v[176:179],184
s_nop 0
M 60,v[152:155],212
R 144,2,768
M 60,v[156:159],208
ds_read2_b32 v[116:117],v16 offset0:192 offset1:193
M 60,v[160:163],204
ds_read2_b32 v[118:119],v16 offset0:194 offset1:195
M 60,v[164:167],200
R 148,2,256
M 60,v[168:171],196
M 60,v[172:175],192
M 60,v[176:179],188
M 60,v[180:183],184
L 15
M 40,v[88:91],128
ds_read2_b32 v[152:153],v1 offset0:80 offset1:81
M 40,v[92:95],124
ds_read2_b32 v[154:155],v1 offset0:82 offset1:83
M 40,v[96:99],120
L 15
R 184,2,3904
M 44,v[88:91],132
ds_read2_b32 v[156:157],v1 offset0:208 offset1:209
M 44,v[92:95],128
M 44,v[96:99],124
ds_read2_b32 v[158:159],v1 offset0:210 offset1:211
M 44,v[100:103],120
L 14
R 188,2,3392
M 48,v[88:91],136
ds_read2_b32 v[160:161],v14 offset0:80 offset1:81
M 48,v[92:95],132
ds_read2_b32 v[162:163],v14 offset0:82 offset1:83
R 218,18,16000
M 48,v[96:99],128
ds_read_b32 v222,v18 offset:16016
M 48,v[100:103],124
R 192,2,2880
M 48,v[104:107],120
L 15
ds_read2_b32 v[164:165],v14 offset0:208 offset1:209
M 52,v[88:91],140
ds_read2_b32 v[166:167],v14 offset0:210 offset1:211
M 52,v[92:95],136
R 196,2,2368
M 52,v[96:99],132
M 52,v[100:103],128
L 4
ds_read2_b32 v[168:169],v15 offset0:80 offset1:81
v_alignbyte_b32 v226,v219,v218,2
M 52,v[104:107],124
ds_read2_b32 v[170:171],v15 offset0:82 offset1:83
v_alignbyte_b32 v227,v220,v219,2
M 52,v[108:111],120
s_nop 0
R 200,2,1856
v_alignbyte_b32 v228,v221,v220,2
M 56,v[88:91],144
ds_read2_b32 v[172:173],v15 offset0:208 offset1:209
v_alignbyte_b32 v229,v222,v221,2
M 56,v[92:95],140
ds_write_b128 v18,v[226:229] offset:20160
M 56,v[96:99],136
ds_read2_b32 v[174:175],v15 offset0:210 offset1:211
M 56,v[100:103],132
R 204,2,1344
M 56,v[104:107],128
ds_read2_b32 v[176:177],v16 offset0:80 offset1:81
M 56,v[108:111],124
ds_read2_b32 v[178:179],v16 offset0:82 offset1:83
M 56,v[112:115],120
s_nop 0
M 60,v[88:91],148
R 208,2,832
M 60,v[92:95],144
ds_read2_b32 v[180:181],v16 offset0:208 offset1:209
M 60,v[96:99],140
ds_read2_b32 v[182:183],v16 offset0:210 offset1:211
M 60,v[100:103],136
R 212,2,320
M 60,v[104:107],132
M 60,v[108:111],128
M 60,v[112:115],124
M 60,v[116:119],120
L 15
M 40,v[152:155],192
ds_read2_b32 v[88:89],v1 offset0:96 offset1:97
M 40,v[156:159],188
ds_read2_b32 v[90:91],v1 offset0:98 offset1:99
M 40,v[160:163],184
L 15
R 120,2,3968
M 44,v[152:155],196
s_mov_b32 m0,163584
s_mov_b32 s78,3741696
ds_read2_b32 v[92:93],v1 offset0:224 offset1:225
M 44,v[156:159],192
M 44,v[160:163],188
ds_read2_b32 v[94:95],v1 offset0:226 offset1:227
M 44,v[164:167],184
L 15
R 124,2,3456
M 48,v[152:155],200
ds_read2_b32 v[96:97],v14 offset0:96 offset1:97
M 48,v[156:159],196
ds_read2_b32 v[98:99],v14 offset0:98 offset1:99
M 48,v[160:163],192
buffer_load_dword v11,s[40:43],s78 offen offset:0  lds
M 48,v[164:167],188
R 128,2,2944
M 48,v[168:171],184
L 15
ds_read2_b32 v[100:101],v14 offset0:224 offset1:225
M 52,v[152:155],204
ds_read2_b32 v[102:103],v14 offset0:226 offset1:227
M 52,v[156:159],200
R 132,2,2432
M 52,v[160:163],196
M 52,v[164:167],192
ds_read2_b32 v[104:105],v15 offset0:96 offset1:97
M 52,v[168:171],188
ds_read2_b32 v[106:107],v15 offset0:98 offset1:99
M 52,v[172:175],184
L 15
R 136,2,1920
M 56,v[152:155],208
ds_read2_b32 v[108:109],v15 offset0:224 offset1:225
M 56,v[156:159],204
M 56,v[160:163],200
ds_read2_b32 v[110:111],v15 offset0:226 offset1:227
M 56,v[164:167],196
R 140,2,1408
M 56,v[168:171],192
ds_read2_b32 v[112:113],v16 offset0:96 offset1:97
M 56,v[172:175],188
ds_read2_b32 v[114:115],v16 offset0:98 offset1:99
M 56,v[176:179],184
L 15
M 60,v[152:155],212
R 144,2,896
M 60,v[156:159],208
ds_read2_b32 v[116:117],v16 offset0:224 offset1:225
M 60,v[160:163],204
ds_read2_b32 v[118:119],v16 offset0:226 offset1:227
M 60,v[164:167],200
R 148,2,384
M 60,v[168:171],196
M 60,v[172:175],192
M 60,v[176:179],188
M 60,v[180:183],184
L 15
M 40,v[88:91],128
ds_read2_b32 v[152:153],v1 offset0:112 offset1:113
M 40,v[92:95],124
ds_read2_b32 v[154:155],v1 offset0:114 offset1:115
M 40,v[96:99],120
L 14
R 184,2,4032
M 44,v[88:91],132
ds_read2_b32 v[156:157],v1 offset0:240 offset1:241
M 44,v[92:95],128
M 44,v[96:99],124
ds_read2_b32 v[158:159],v1 offset0:242 offset1:243
v_add_u32 v1,12928,v1
M 44,v[100:103],120
L 14
R 188,2,3520
M 48,v[88:91],136
ds_read2_b32 v[160:161],v14 offset0:112 offset1:113
M 48,v[92:95],132
ds_read2_b32 v[162:163],v14 offset0:114 offset1:115
M 48,v[96:99],128
M 48,v[100:103],124
R 192,2,3008
M 48,v[104:107],120
L 15
ds_read2_b32 v[164:165],v14 offset0:240 offset1:241
M 52,v[88:91],140
ds_read2_b32 v[166:167],v14 offset0:242 offset1:243
v_add_u32 v14,12928,v14
M 52,v[92:95],136
R 196,2,2496
M 52,v[96:99],132
M 52,v[100:103],128
ds_read2_b32 v[168:169],v15 offset0:112 offset1:113
M 52,v[104:107],124
ds_read2_b32 v[170:171],v15 offset0:114 offset1:115
M 52,v[108:111],120
L 15
R 200,2,1984
M 56,v[88:91],144
ds_read2_b32 v[172:173],v15 offset0:240 offset1:241
M 56,v[92:95],140
M 56,v[96:99],136
ds_read2_b32 v[174:175],v15 offset0:242 offset1:243
v_add_u32 v15,12928,v15
M 56,v[100:103],132
R 204,2,1472
M 56,v[104:107],128
ds_read2_b32 v[176:177],v16 offset0:112 offset1:113
M 56,v[108:111],124
ds_read2_b32 v[178:179],v16 offset0:114 offset1:115
M 56,v[112:115],120
L 15
M 60,v[88:91],148
R 208,2,960
M 60,v[92:95],144
)"
R"(ds_read2_b32 v[180:181],v16 offset0:240 offset1:241
M 60,v[96:99],140
ds_read2_b32 v[182:183],v16 offset0:242 offset1:243
v_add_u32 v16,12928,v16
M 60,v[100:103],136
R 212,2,448
M 60,v[104:107],132
M 60,v[108:111],128
M 60,v[112:115],124
M 60,v[116:119],120
L 15
M 40,v[152:155],192
ds_read2_b32 v[88:89],v1 offset0:0 offset1:1
M 40,v[156:159],188
ds_read2_b32 v[90:91],v1 offset0:2 offset1:3
M 40,v[160:163],184
L 14
R 120,2,16512
M 44,v[152:155],196
ds_read2_b32 v[92:93],v1 offset0:128 offset1:129
M 44,v[156:159],192
M 44,v[160:163],188
ds_read2_b32 v[94:95],v1 offset0:130 offset1:131
M 44,v[164:167],184
L 14
R 124,2,16000
M 48,v[152:155],200
ds_read2_b32 v[96:97],v14 offset0:0 offset1:1
M 48,v[156:159],196
ds_read2_b32 v[98:99],v14 offset0:2 offset1:3
M 48,v[160:163],192
M 48,v[164:167],188
R 128,2,15488
M 48,v[168:171],184
L 15
ds_read2_b32 v[100:101],v14 offset0:128 offset1:129
M 52,v[152:155],204
ds_read2_b32 v[102:103],v14 offset0:130 offset1:131
M 52,v[156:159],200
R 132,2,14976
M 52,v[160:163],196
M 52,v[164:167],192
ds_read2_b32 v[104:105],v15 offset0:0 offset1:1
M 52,v[168:171],188
ds_read2_b32 v[106:107],v15 offset0:2 offset1:3
M 52,v[172:175],184
L 15
R 136,2,14464
M 56,v[152:155],208
ds_read2_b32 v[108:109],v15 offset0:128 offset1:129
M 56,v[156:159],204
M 56,v[160:163],200
ds_read2_b32 v[110:111],v15 offset0:130 offset1:131
M 56,v[164:167],196
R 140,2,13952
M 56,v[168:171],192
ds_read2_b32 v[112:113],v16 offset0:0 offset1:1
M 56,v[172:175],188
ds_read2_b32 v[114:115],v16 offset0:2 offset1:3
M 56,v[176:179],184
L 15
M 60,v[152:155],212
R 144,2,13440
M 60,v[156:159],208
ds_read2_b32 v[116:117],v16 offset0:128 offset1:129
M 60,v[160:163],204
ds_read2_b32 v[118:119],v16 offset0:130 offset1:131
M 60,v[164:167],200
R 148,2,12928
M 60,v[168:171],196
M 60,v[172:175],192
M 60,v[176:179],188
M 60,v[180:183],184
s_cmp_eq_u32 s51,2
s_cbranch_scc1 Lcv_d_%=
ds_read_b64 v[24:25],v3 offset:2560
ds_read_b64 v[26:27],v3 offset:2048
ds_read_b64 v[28:29],v3 offset:1536
ds_read_b64 v[30:31],v3 offset:1024
ds_read_b64 v[32:33],v3 offset:512
ds_read_b64 v[34:35],v3 offset:0
L 0
v_and_b32 v36,0xffff0000,v25
v_lshlrev_b32 v37,16,v25
v_and_b32 v38,0xffff0000,v24
v_lshlrev_b32 v39,16,v24
v_fma_f32 v40,v36,s86,v40
v_fma_f32 v41,v37,s86,v41
v_fma_f32 v42,v38,s86,v42
v_fma_f32 v43,v39,s86,v43
v_cvt_pk_bf16_f32 v40,v40,v41
v_cvt_pk_bf16_f32 v41,v42,v43
ds_write_b64 v4,v[40:41] offset:0
v_and_b32 v36,0xffff0000,v27
v_lshlrev_b32 v37,16,v27
v_and_b32 v38,0xffff0000,v26
v_lshlrev_b32 v39,16,v26
v_fma_f32 v44,v36,s86,v44
v_fma_f32 v45,v37,s86,v45
v_fma_f32 v46,v38,s86,v46
v_fma_f32 v47,v39,s86,v47
v_cvt_pk_bf16_f32 v44,v44,v45
v_cvt_pk_bf16_f32 v45,v46,v47
ds_write_b64 v4,v[44:45] offset:512
v_and_b32 v36,0xffff0000,v29
v_lshlrev_b32 v37,16,v29
v_and_b32 v38,0xffff0000,v28
v_lshlrev_b32 v39,16,v28
v_fma_f32 v48,v36,s86,v48
v_fma_f32 v49,v37,s86,v49
v_fma_f32 v50,v38,s86,v50
v_fma_f32 v51,v39,s86,v51
v_cvt_pk_bf16_f32 v48,v48,v49
v_cvt_pk_bf16_f32 v49,v50,v51
ds_write_b64 v4,v[48:49] offset:1024
v_and_b32 v36,0xffff0000,v31
v_lshlrev_b32 v37,16,v31
v_and_b32 v38,0xffff0000,v30
v_lshlrev_b32 v39,16,v30
v_fma_f32 v52,v36,s86,v52
v_fma_f32 v53,v37,s86,v53
v_fma_f32 v54,v38,s86,v54
v_fma_f32 v55,v39,s86,v55
v_cvt_pk_bf16_f32 v52,v52,v53
v_cvt_pk_bf16_f32 v53,v54,v55
ds_write_b64 v4,v[52:53] offset:1536
v_and_b32 v36,0xffff0000,v33
v_lshlrev_b32 v37,16,v33
v_and_b32 v38,0xffff0000,v32
v_lshlrev_b32 v39,16,v32
v_fma_f32 v56,v36,s86,v56
v_fma_f32 v57,v37,s86,v57
v_fma_f32 v58,v38,s86,v58
v_fma_f32 v59,v39,s86,v59
v_cvt_pk_bf16_f32 v56,v56,v57
v_cvt_pk_bf16_f32 v57,v58,v59
ds_write_b64 v4,v[56:57] offset:2048
v_and_b32 v36,0xffff0000,v35
v_lshlrev_b32 v37,16,v35
v_and_b32 v38,0xffff0000,v34
v_lshlrev_b32 v39,16,v34
v_fma_f32 v60,v36,s86,v60
v_fma_f32 v61,v37,s86,v61
v_fma_f32 v62,v38,s86,v62
v_fma_f32 v63,v39,s86,v63
v_cvt_pk_bf16_f32 v60,v60,v61
v_cvt_pk_bf16_f32 v61,v62,v63
ds_write_b64 v4,v[60:61] offset:2560
s_branch Lcv_e_%=
Lcv_d_%=:
L 15
v_mfma_f32_16x16x32_bf16 v[64:67],v[88:91],v[128:131],0
ds_read2_b32 v[152:153],v1 offset0:16 offset1:17
ds_read_b64 v[24:25],v3 offset:2560
M 64,v[92:95],124
ds_read2_b32 v[154:155],v1 offset0:18 offset1:19
ds_read_b64 v[26:27],v3 offset:2048
M 64,v[96:99],120
L 15
R 184,2,16576
ds_read_b64 v[28:29],v3 offset:1536
v_mfma_f32_16x16x32_bf16 v[68:71],v[88:91],v[132:135],0
ds_read2_b32 v[156:157],v1 offset0:144 offset1:145
ds_read_b64 v[30:31],v3 offset:1024
M 68,v[92:95],128
ds_read_b64 v[32:33],v3 offset:512
ds_read_b64 v[34:35],v3 offset:0
M 68,v[96:99],124
ds_read2_b32 v[158:159],v1 offset0:146 offset1:147
M 68,v[100:103],120
L 15
R 188,2,16064
v_mfma_f32_16x16x32_bf16 v[72:75],v[88:91],v[136:139],0
ds_read2_b32 v[160:161],v14 offset0:16 offset1:17
M 72,v[92:95],132
s_mov_b32 m0,163584
s_mov_b32 s78,0
ds_read2_b32 v[162:163],v14 offset0:18 offset1:19
M 72,v[96:99],128
M 72,v[100:103],124
L 4
R 192,2,15552
v_and_b32 v36,0xffff0000,v25
M 72,v[104:107],120
s_nop 0
ds_read2_b32 v[164:165],v14 offset0:144 offset1:145
v_lshlrev_b32 v37,16,v25
v_mfma_f32_16x16x32_bf16 v[76:79],v[88:91],v[140:143],0
ds_read2_b32 v[166:167],v14 offset0:146 offset1:147
v_and_b32 v38,0xffff0000,v24
M 76,v[92:95],136
R 196,2,15040
v_lshlrev_b32 v39,16,v24
M 76,v[96:99],132
buffer_load_dword v12,s[60:63],s78 offen offset:0  lds
M 76,v[100:103],128
ds_read2_b32 v[168:169],v15 offset0:16 offset1:17
v_fma_f32 v40,v36,s86,v40
M 76,v[104:107],124
ds_read2_b32 v[170:171],v15 offset0:18 offset1:19
v_fma_f32 v41,v37,s86,v41
M 76,v[108:111],120
s_nop 0
R 200,2,14528
v_fma_f32 v42,v38,s86,v42
v_mfma_f32_16x16x32_bf16 v[80:83],v[88:91],v[144:147],0
ds_read2_b32 v[172:173],v15 offset0:144 offset1:145
v_fma_f32 v43,v39,s86,v43
M 80,v[92:95],140
v_cvt_pk_bf16_f32 v40,v40,v41
v_cvt_pk_bf16_f32 v41,v42,v43
M 80,v[96:99],136
ds_read2_b32 v[174:175],v15 offset0:146 offset1:147
v_and_b32 v36,0xffff0000,v27
M 80,v[100:103],132
R 204,2,14016
v_lshlrev_b32 v37,16,v27
M 80,v[104:107],128
ds_read2_b32 v[176:177],v16 offset0:16 offset1:17
v_and_b32 v38,0xffff0000,v26
M 80,v[108:111],124
ds_read2_b32 v[178:179],v16 offset0:18 offset1:19
v_lshlrev_b32 v39,16,v26
M 80,v[112:115],120
s_nop 0
ds_write_b64 v4,v[40:41] offset:0
v_mfma_f32_16x16x32_bf16 v[84:87],v[88:91],v[148:151],0
R 208,2,13504
v_fma_f32 v44,v36,s86,v44
M 84,v[92:95],144
ds_read2_b32 v[180:181],v16 offset0:144 offset1:145
v_fma_f32 v45,v37,s86,v45
M 84,v[96:99],140
ds_read2_b32 v[182:183],v16 offset0:146 offset1:147
v_fma_f32 v46,v38,s86,v46
M 84,v[100:103],136
R 212,2,12992
v_fma_f32 v47,v39,s86,v47
M 84,v[104:107],132
v_cvt_pk_bf16_f32 v44,v44,v45
v_cvt_pk_bf16_f32 v45,v46,v47
M 84,v[108:111],128
ds_write_b64 v4,v[44:45] offset:512
M 84,v[112:115],124
v_and_b32 v36,0xffff0000,v29
v_lshlrev_b32 v37,16,v29
M 84,v[116:119],120
L 15
v_and_b32 v38,0xffff0000,v28
v_lshlrev_b32 v39,16,v28
M 64,v[152:155],192
ds_read2_b32 v[88:89],v1 offset0:32 offset1:33
v_fma_f32 v48,v36,s86,v48
M 64,v[156:159],188
ds_read2_b32 v[90:91],v1 offset0:34 offset1:35
v_fma_f32 v49,v37,s86,v49
M 64,v[160:163],184
L 15
R 120,2,16640
v_fma_f32 v50,v38,s86,v50
M 68,v[152:155],196
ds_read2_b32 v[92:93],v1 offset0:160 offset1:161
v_fma_f32 v51,v39,s86,v51
M 68,v[156:159],192
v_cvt_pk_bf16_f32 v48,v48,v49
v_cvt_pk_bf16_f32 v49,v50,v51
M 68,v[160:163],188
ds_read2_b32 v[94:95],v1 offset0:162 offset1:163
v_and_b32 v36,0xffff0000,v31
M 68,v[164:167],184
L 15
R 124,2,16128
v_lshlrev_b32 v37,16,v31
M 72,v[152:155],200
ds_read2_b32 v[96:97],v14 offset0:32 offset1:33
v_and_b32 v38,0xffff0000,v30
M 72,v[156:159],196
ds_read2_b32 v[98:99],v14 offset0:34 offset1:35
v_lshlrev_b32 v39,16,v30
M 72,v[160:163],192
ds_write_b64 v4,v[48:49] offset:1024
M 72,v[164:167],188
R 128,2,15616
v_fma_f32 v52,v36,s86,v52
M 72,v[168:171],184
L 15
ds_read2_b32 v[100:101],v14 offset0:160 offset1:161
v_fma_f32 v53,v37,s86,v53
M 76,v[152:155],204
ds_read2_b32 v[102:103],v14 offset0:162 offset1:163
v_fma_f32 v54,v38,s86,v54
M 76,v[156:159],200
R 132,2,15104
v_fma_f32 v55,v39,s86,v55
M 76,v[160:163],196
v_cvt_pk_bf16_f32 v52,v52,v53
v_cvt_pk_bf16_f32 v53,v54,v55
M 76,v[164:167],192
ds_read2_b32 v[104:105],v15 offset0:32 offset1:33
v_and_b32 v36,0xffff0000,v33
M 76,v[168:171],188
ds_read2_b32 v[106:107],v15 offset0:34 offset1:35
v_lshlrev_b32 v37,16,v33
M 76,v[172:175],184
L 15
R 136,2,14592
v_and_b32 v38,0xffff0000,v32
M 80,v[152:155],208
ds_read2_b32 v[108:109],v15 offset0:160 offset1:161
v_lshlrev_b32 v39,16,v32
M 80,v[156:159],204
ds_write_b64 v4,v[52:53] offset:1536
M 80,v[160:163],200
ds_read2_b32 v[110:111],v15 offset0:162 offset1:163
v_fma_f32 v56,v36,s86,v56
M 80,v[164:167],196
R 140,2,14080
v_fma_f32 v57,v37,s86,v57
M 80,v[168:171],192
ds_read2_b32 v[112:113],v16 offset0:32 offset1:33
v_fma_f32 v58,v38,s86,v58
M 80,v[172:175],188
ds_read2_b32 v[114:115],v16 offset0:34 offset1:35
v_fma_f32 v59,v39,s86,v59
M 80,v[176:179],184
L 15
v_cvt_pk_bf16_f32 v56,v56,v57
v_cvt_pk_bf16_f32 v57,v58,v59
M 84,v[152:155],212
R 144,2,13568
v_and_b32 v36,0xffff0000,v35
M 84,v[156:159],208
ds_read2_b32 v[116:117],v16 offset0:160 offset1:161
v_lshlrev_b32 v37,16,v35
M 84,v[160:163],204
ds_read2_b32 v[118:119],v16 offset0:162 offset1:163
v_and_b32 v38,0xffff0000,v34
M 84,v[164:167],200
R 148,2,13056
v_lshlrev_b32 v39,16,v34
M 84,v[168:171],196
ds_write_b64 v4,v[56:57] offset:2048
M 84,v[172:175],192
v_fma_f32 v60,v36,s86,v60
v_fma_f32 v61,v37,s86,v61
M 84,v[176:179],188
v_fma_f32 v62,v38,s86,v62
v_fma_f32 v63,v39,s86,v63
M 84,v[180:183],184
L 15
v_cvt_pk_bf16_f32 v60,v60,v61
v_cvt_pk_bf16_f32 v61,v62,v63
M 64,v[88:91],128
ds_read2_b32 v[152:153],v1 offset0:48 offset1:49
M 64,v[92:95],124
ds_read2_b32 v[154:155],v1 offset0:50 offset1:51
M 64,v[96:99],120
L 15
R 184,2,16704
M 68,v[88:91],132
ds_read2_b32 v[156:157],v1 offset0:176 offset1:177
M 68,v[92:95],128
ds_write_b64 v4,v[60:61] offset:2560
M 68,v[96:99],124
ds_read2_b32 v[158:159],v1 offset0:178 offset1:179
M 68,v[100:103],120
L 15
R 188,2,16192
M 72,v[88:91],136
ds_read2_b32 v[160:161],v14 offset0:48 offset1:49
M 72,v[92:95],132
ds_read2_b32 v[162:163],v14 offset0:50 offset1:51
M 72,v[96:99],128
M 72,v[100:103],124
R 192,2,15680
M 72,v[104:107],120
L 15
ds_read2_b32 v[164:165],v14 offset0:176 offset1:177
M 76,v[88:91],140
ds_read2_b32 v[166:167],v14 offset0:178 offset1:179
M 76,v[92:95],136
R 196,2,15168
M 76,v[96:99],132
M 76,v[100:103],128
ds_read2_b32 v[168:169],v15 offset0:48 offset1:49
M 76,v[104:107],124
ds_read2_b32 v[170:171],v15 offset0:50 offset1:51
M 76,v[108:111],120
L 15
R 200,2,14656
M 80,v[88:91],144
ds_read2_b32 v[172:173],v15 offset0:176 offset1:177
M 80,v[92:95],140
M 80,v[96:99],136
ds_read2_b32 v[174:175],v15 offset0:178 offset1:179
M 80,v[100:103],132
R 204,2,14144
M 80,v[104:107],128
s_mov_b32 m0,163584
s_mov_b32 s78,1847296
ds_read2_b32 v[176:177],v16 offset0:48 offset1:49
M 80,v[108:111],124
ds_read2_b32 v[178:179],v16 offset0:50 offset1:51
M 80,v[112:115],120
L 15
buffer_load_dword v12,s[60:63],s78 offen offset:0  lds
M 84,v[88:91],148
R 208,2,13632
M 84,v[92:95],144
ds_read2_b32 v[180:181],v16 offset0:176 offset1:177
M 84,v[96:99],140
ds_read2_b32 v[182:183],v16 offset0:178 offset1:179
M 84,v[100:103],136
R 212,2,13120
M 84,v[104:107],132
M 84,v[108:111],128
M 84,v[112:115],124
M 84,v[116:119],120
L 15
M 64,v[152:155],192
ds_read2_b32 v[88:89],v1 offset0:64 offset1:65
M 64,v[156:159],188
ds_read2_b32 v[90:91],v1 offset0:66 offset1:67
M 64,v[160:163],184
L 14
R 120,2,16768
M 68,v[152:155],196
ds_read2_b32 v[92:93],v1 offset0:192 offset1:193
M 68,v[156:159],192
M 68,v[160:163],188
ds_read2_b32 v[94:95],v1 offset0:194 offset1:195
M 68,v[164:167],184
L 14
R 124,2,16256
M 72,v[152:155],200
ds_read2_b32 v[96:97],v14 offset0:64 offset1:65
M 72,v[156:159],196
ds_read2_b32 v[98:99],v14 offset0:66 offset1:67
M 72,v[160:163],192
M 72,v[164:167],188
R 128,2,15744
M 72,v[168:171],184
L 15
ds_read2_b32 v[100:101],v14 offset0:192 offset1:193
M 76,v[152:155],204
ds_read2_b32 v[102:103],v14 offset0:194 offset1:195
M 76,v[156:159],200
R 132,2,15232
M 76,v[160:163],196
M 76,v[164:167],192
ds_read2_b32 v[104:105],v15 offset0:64 offset1:65
M 76,v[168:171],188
ds_read2_b32 v[106:107],v15 offset0:66 offset1:67
M 76,v[172:175],184
L 15
R 136,2,14720
M 80,v[152:155],208
ds_read2_b32 v[108:109],v15 offset0:192 offset1:193
M 80,v[156:159],204
M 80,v[160:163],200
ds_read2_b32 v[110:111],v15 offset0:194 offset1:195
M 80,v[164:167],196
R 140,2,14208
M 80,v[168:171],192
ds_read2_b32 v[112:113],v16 offset0:64 offset1:65
M 80,v[172:175],188
ds_read2_b32 v[114:115],v16 offset0:66 offset1:67
M 80,v[176:179],184
L 15
M 84,v[152:155],212
R 144,2,13696
M 84,v[156:159],208
ds_read2_b32 v[116:117],v16 offset0:192 offset1:193
M 84,v[160:163],204
ds_read2_b32 v[118:119],v16 offset0:194 offset1:195
M 84,v[164:167],200
R 148,2,13184
M 84,v[168:171],196
M 84,v[172:175],192
M 84,v[176:179],188
M 84,v[180:183],184
L 15
M 64,v[88:91],128
ds_read2_b32 v[152:153],v1 offset0:80 offset1:81
M 64,v[92:95],124
ds_read2_b32 v[154:155],v1 offset0:82 offset1:83
M 64,v[96:99],120
L 14
R 184,2,16832
M 68,v[88:91],132
ds_read2_b32 v[156:157],v1 offset0:208 offset1:209
M 68,v[92:95],128
M 68,v[96:99],124
ds_read2_b32 v[158:159],v1 offset0:210 offset1:211
M 68,v[100:103],120
L 14
R 188,2,16320
M 72,v[88:91],136
ds_read2_b32 v[160:161],v14 offset0:80 offset1:81
M 72,v[92:95],132
)"
R"(ds_read2_b32 v[162:163],v14 offset0:82 offset1:83
M 72,v[96:99],128
M 72,v[100:103],124
R 192,2,15808
M 72,v[104:107],120
L 15
ds_read2_b32 v[164:165],v14 offset0:208 offset1:209
M 76,v[88:91],140
ds_read2_b32 v[166:167],v14 offset0:210 offset1:211
M 76,v[92:95],136
R 196,2,15296
M 76,v[96:99],132
M 76,v[100:103],128
ds_read2_b32 v[168:169],v15 offset0:80 offset1:81
M 76,v[104:107],124
ds_read2_b32 v[170:171],v15 offset0:82 offset1:83
M 76,v[108:111],120
L 15
R 200,2,14784
M 80,v[88:91],144
ds_read2_b32 v[172:173],v15 offset0:208 offset1:209
M 80,v[92:95],140
M 80,v[96:99],136
ds_read2_b32 v[174:175],v15 offset0:210 offset1:211
M 80,v[100:103],132
R 204,2,14272
M 80,v[104:107],128
ds_read2_b32 v[176:177],v16 offset0:80 offset1:81
M 80,v[108:111],124
ds_read2_b32 v[178:179],v16 offset0:82 offset1:83
M 80,v[112:115],120
L 15
M 84,v[88:91],148
R 208,2,13760
M 84,v[92:95],144
ds_read2_b32 v[180:181],v16 offset0:208 offset1:209
M 84,v[96:99],140
ds_read2_b32 v[182:183],v16 offset0:210 offset1:211
M 84,v[100:103],136
R 212,2,13248
M 84,v[104:107],132
M 84,v[108:111],128
M 84,v[112:115],124
M 84,v[116:119],120
L 15
M 64,v[152:155],192
ds_read2_b32 v[88:89],v1 offset0:96 offset1:97
M 64,v[156:159],188
ds_read2_b32 v[90:91],v1 offset0:98 offset1:99
M 64,v[160:163],184
L 14
R 120,2,16896
M 68,v[152:155],196
ds_read2_b32 v[92:93],v1 offset0:224 offset1:225
M 68,v[156:159],192
M 68,v[160:163],188
ds_read2_b32 v[94:95],v1 offset0:226 offset1:227
M 68,v[164:167],184
L 14
R 124,2,16384
M 72,v[152:155],200
ds_read2_b32 v[96:97],v14 offset0:96 offset1:97
M 72,v[156:159],196
ds_read2_b32 v[98:99],v14 offset0:98 offset1:99
M 72,v[160:163],192
M 72,v[164:167],188
R 128,2,15872
M 72,v[168:171],184
L 15
ds_read2_b32 v[100:101],v14 offset0:224 offset1:225
M 76,v[152:155],204
ds_read2_b32 v[102:103],v14 offset0:226 offset1:227
M 76,v[156:159],200
R 132,2,15360
M 76,v[160:163],196
M 76,v[164:167],192
ds_read2_b32 v[104:105],v15 offset0:96 offset1:97
M 76,v[168:171],188
ds_read2_b32 v[106:107],v15 offset0:98 offset1:99
M 76,v[172:175],184
L 15
R 136,2,14848
M 80,v[152:155],208
ds_read2_b32 v[108:109],v15 offset0:224 offset1:225
M 80,v[156:159],204
M 80,v[160:163],200
ds_read2_b32 v[110:111],v15 offset0:226 offset1:227
M 80,v[164:167],196
R 140,2,14336
M 80,v[168:171],192
ds_read2_b32 v[112:113],v16 offset0:96 offset1:97
M 80,v[172:175],188
ds_read2_b32 v[114:115],v16 offset0:98 offset1:99
M 80,v[176:179],184
L 15
M 84,v[152:155],212
R 144,2,13824
M 84,v[156:159],208
ds_read2_b32 v[116:117],v16 offset0:224 offset1:225
M 84,v[160:163],204
ds_read2_b32 v[118:119],v16 offset0:226 offset1:227
M 84,v[164:167],200
R 148,2,13312
M 84,v[168:171],196
M 84,v[172:175],192
M 84,v[176:179],188
M 84,v[180:183],184
L 15
M 64,v[88:91],128
ds_read2_b32 v[152:153],v1 offset0:112 offset1:113
M 64,v[92:95],124
ds_read2_b32 v[154:155],v1 offset0:114 offset1:115
M 64,v[96:99],120
L 14
R 184,2,16960
M 68,v[88:91],132
ds_read2_b32 v[156:157],v1 offset0:240 offset1:241
M 68,v[92:95],128
M 68,v[96:99],124
ds_read2_b32 v[158:159],v1 offset0:242 offset1:243
M 68,v[100:103],120
L 14
R 188,2,16448
M 72,v[88:91],136
ds_read2_b32 v[160:161],v14 offset0:112 offset1:113
M 72,v[92:95],132
ds_read2_b32 v[162:163],v14 offset0:114 offset1:115
M 72,v[96:99],128
M 72,v[100:103],124
R 192,2,15936
M 72,v[104:107],120
L 15
ds_read2_b32 v[164:165],v14 offset0:240 offset1:241
M 76,v[88:91],140
ds_read2_b32 v[166:167],v14 offset0:242 offset1:243
M 76,v[92:95],136
R 196,2,15424
M 76,v[96:99],132
M 76,v[100:103],128
ds_read2_b32 v[168:169],v15 offset0:112 offset1:113
M 76,v[104:107],124
ds_read2_b32 v[170:171],v15 offset0:114 offset1:115
M 76,v[108:111],120
L 15
R 200,2,14912
M 80,v[88:91],144
ds_read2_b32 v[172:173],v15 offset0:240 offset1:241
M 80,v[92:95],140
M 80,v[96:99],136
ds_read2_b32 v[174:175],v15 offset0:242 offset1:243
M 80,v[100:103],132
R 204,2,14400
M 80,v[104:107],128
ds_read2_b32 v[176:177],v16 offset0:112 offset1:113
M 80,v[108:111],124
ds_read2_b32 v[178:179],v16 offset0:114 offset1:115
M 80,v[112:115],120
L 15
M 84,v[88:91],148
R 208,2,13888
M 84,v[92:95],144
ds_read2_b32 v[180:181],v16 offset0:240 offset1:241
M 84,v[96:99],140
ds_read2_b32 v[182:183],v16 offset0:242 offset1:243
M 84,v[100:103],136
R 212,2,13376
M 84,v[104:107],132
M 84,v[108:111],128
M 84,v[112:115],124
M 84,v[116:119],120
L 15
M 64,v[152:155],192
M 64,v[156:159],188
M 64,v[160:163],184
L 12
M 68,v[152:155],196
M 68,v[156:159],192
M 68,v[160:163],188
M 68,v[164:167],184
L 9
M 72,v[152:155],200
M 72,v[156:159],196
M 72,v[160:163],192
ds_read_b64 v[24:25],v3 offset:15488
ds_read_b64 v[26:27],v3 offset:14976
M 72,v[164:167],188
ds_read_b64 v[28:29],v3 offset:14464
ds_read_b64 v[30:31],v3 offset:13952
M 72,v[168:171],184
L 10
ds_read_b64 v[32:33],v3 offset:13440
ds_read_b64 v[34:35],v3 offset:12928
M 76,v[152:155],204
M 76,v[156:159],200
M 76,v[160:163],196
M 76,v[164:167],192
M 76,v[168:171],188
M 76,v[172:175],184
L 9
M 80,v[152:155],208
M 80,v[156:159],204
M 80,v[160:163],200
M 80,v[164:167],196
M 80,v[168:171],192
M 80,v[172:175],188
M 80,v[176:179],184
L 6
M 84,v[152:155],212
M 84,v[156:159],208
M 84,v[160:163],204
M 84,v[164:167],200
M 84,v[168:171],196
M 84,v[172:175],192
M 84,v[176:179],188
M 84,v[180:183],184
L 0
v_and_b32 v36,0xffff0000,v25
v_lshlrev_b32 v37,16,v25
v_and_b32 v38,0xffff0000,v24
v_lshlrev_b32 v39,16,v24
v_fma_f32 v64,v36,s87,v64
v_fma_f32 v65,v37,s87,v65
v_fma_f32 v66,v38,s87,v66
v_fma_f32 v67,v39,s87,v67
v_cvt_pk_bf16_f32 v64,v64,v65
v_cvt_pk_bf16_f32 v65,v66,v67
ds_write_b64 v4,v[64:65] offset:12928
v_and_b32 v36,0xffff0000,v27
v_lshlrev_b32 v37,16,v27
v_and_b32 v38,0xffff0000,v26
v_lshlrev_b32 v39,16,v26
v_fma_f32 v68,v36,s87,v68
v_fma_f32 v69,v37,s87,v69
v_fma_f32 v70,v38,s87,v70
v_fma_f32 v71,v39,s87,v71
v_cvt_pk_bf16_f32 v68,v68,v69
v_cvt_pk_bf16_f32 v69,v70,v71
ds_write_b64 v4,v[68:69] offset:13440
v_and_b32 v36,0xffff0000,v29
v_lshlrev_b32 v37,16,v29
v_and_b32 v38,0xffff0000,v28
v_lshlrev_b32 v39,16,v28
v_fma_f32 v72,v36,s87,v72
v_fma_f32 v73,v37,s87,v73
v_fma_f32 v74,v38,s87,v74
v_fma_f32 v75,v39,s87,v75
v_cvt_pk_bf16_f32 v72,v72,v73
v_cvt_pk_bf16_f32 v73,v74,v75
ds_write_b64 v4,v[72:73] offset:13952
v_and_b32 v36,0xffff0000,v31
v_lshlrev_b32 v37,16,v31
v_and_b32 v38,0xffff0000,v30
v_lshlrev_b32 v39,16,v30
v_fma_f32 v76,v36,s87,v76
v_fma_f32 v77,v37,s87,v77
v_fma_f32 v78,v38,s87,v78
v_fma_f32 v79,v39,s87,v79
v_cvt_pk_bf16_f32 v76,v76,v77
v_cvt_pk_bf16_f32 v77,v78,v79
ds_write_b64 v4,v[76:77] offset:14464
v_and_b32 v36,0xffff0000,v33
v_lshlrev_b32 v37,16,v33
v_and_b32 v38,0xffff0000,v32
v_lshlrev_b32 v39,16,v32
v_fma_f32 v80,v36,s87,v80
v_fma_f32 v81,v37,s87,v81
v_fma_f32 v82,v38,s87,v82
v_fma_f32 v83,v39,s87,v83
v_cvt_pk_bf16_f32 v80,v80,v81
v_cvt_pk_bf16_f32 v81,v82,v83
ds_write_b64 v4,v[80:81] offset:14976
v_and_b32 v36,0xffff0000,v35
v_lshlrev_b32 v37,16,v35
v_and_b32 v38,0xffff0000,v34
v_lshlrev_b32 v39,16,v34
v_fma_f32 v84,v36,s87,v84
v_fma_f32 v85,v37,s87,v85
v_fma_f32 v86,v38,s87,v86
v_fma_f32 v87,v39,s87,v87
v_cvt_pk_bf16_f32 v84,v84,v85
v_cvt_pk_bf16_f32 v85,v86,v87
ds_write_b64 v4,v[84:85] offset:15488
Lcv_e_%=:
L 0
s_barrier
R 152,5,0
R 156,5,512
R 160,5,1024
R 164,5,1536
R 168,5,2048
R 172,5,2560
s_mov_b32 s78,99614720
L 5
buffer_store_dwordx4 v[152:155],v7,s[36:39],s78 offen offset:0 sc0 sc1
s_add_u32 s78,s56,100139008
L 4
buffer_store_dwordx4 v[156:159],v7,s[36:39],s78 offen offset:0 sc0 sc1
s_mov_b32 s78,100663296
L 3
buffer_store_dwordx4 v[160:163],v7,s[36:39],s78 offen offset:0 sc0 sc1
s_add_u32 s78,s56,101187584
L 2
buffer_store_dwordx4 v[164:167],v7,s[36:39],s78 offen offset:0 sc0 sc1
s_mov_b32 s78,101711872
L 1
buffer_store_dwordx4 v[168:171],v7,s[36:39],s78 offen offset:0 sc0 sc1
s_add_u32 s78,s56,102236160
L 0
buffer_store_dwordx4 v[172:175],v7,s[36:39],s78 offen offset:0 sc0 sc1
s_waitcnt vmcnt(0)
s_barrier
s_add_u32 s48,s48,32768
buffer_store_dword v23,v10,s[36:39],s48 offen offset:0 sc0 sc1
s_cmp_eq_u32 s51,2
s_cbranch_scc0 Lcv_f_%=
s_mov_b32 m0,163584
s_mov_b32 s78,3694592
buffer_load_dword v12,s[60:63],s78 offen offset:0  lds
s_mov_b32 m0,163584
s_mov_b32 s78,5541888
buffer_load_dword v12,s[60:63],s78 offen offset:0  lds
Lcv_f_%=:
Lcode_cv_e:
.purgem M
.purgem R
.purgem T
.purgem D
.purgem L
)"
  :: "s"(ws), "s"(__builtin_amdgcn_readfirstlane(sWave)), "s"(__builtin_amdgcn_readfirstlane(sBuf)), "s"(__builtin_amdgcn_readfirstlane(sAuxOff)), "s"(__builtin_amdgcn_readfirstlane(sFoutOff)), "s"(__builtin_amdgcn_readfirstlane(sC0)), "s"(__builtin_amdgcn_readfirstlane(sC1)), "s"(__builtin_amdgcn_readfirstlane(sExp)), "v"(vL16), "v"(vPA), "v"(vPB), "v"(vPU), "v"(vPW), "v"(vOR), "v"(vORA), "v"(vOG), "v"(vFA), "v"(vFB), "v"(vFlag), "v"(vTW), "v"(vTX), "s"(w3), "s"(w4) : "v0", "v1", "v2", "v3", "v4", "v5", "v6", "v7", "v8", "v9", "v10", "v11", "v12", "v13", "v14", "v15", "v16", "v17", "v18", "v19", "v20", "v21", "v22", "v23", "v24", "v25", "v26", "v27", "v28", "v29", "v30", "v31", "v32", "v33", "v34", "v35", "v36", "v37", "v38", "v39", "v40", "v41", "v42", "v43", "v44", "v45", "v46", "v47", "v48", "v49", "v50", "v51", "v52", "v53", "v54", "v55", "v56", "v57", "v58", "v59", "v60", "v61", "v62", "v63", "v64", "v65", "v66", "v67", "v68", "v69", "v70", "v71", "v72", "v73", "v74", "v75", "v76", "v77", "v78", "v79", "v80", "v81", "v82", "v83", "v84", "v85", "v86", "v87", "v88", "v89", "v90", "v91", "v92", "v93", "v94", "v95", "v96", "v97", "v98", "v99", "v100", "v101", "v102", "v103", "v104", "v105", "v106", "v107", "v108", "v109", "v110", "v111", "v112", "v113", "v114", "v115", "v116", "v117", "v118", "v119", "v120", "v121", "v122", "v123", "v124", "v125", "v126", "v127", "v128", "v129", "v130", "v131", "v132", "v133", "v134", "v135", "v136", "v137", "v138", "v139", "v140", "v141", "v142", "v143", "v144", "v145", "v146", "v147", "v148", "v149", "v150", "v151", "v152", "v153", "v154", "v155", "v156", "v157", "v158", "v159", "v160", "v161", "v162", "v163", "v164", "v165", "v166", "v167", "v168", "v169", "v170", "v171", "v172", "v173", "v174", "v175", "v176", "v177", "v178", "v179", "v180", "v181", "v182", "v183", "v184", "v185", "v186", "v187", "v188", "v189", "v190", "v191", "v192", "v193", "v194", "v195", "v196", "v197", "v198", "v199", "v200", "v201", "v202", "v203", "v204", "v205", "v206", "v207", "v208", "v209", "v210", "v211", "v212", "v213", "v214", "v215", "v216", "v217", "v218", "v219", "v220", "v221", "v222", "v223", "v224", "v225", "v226", "v227", "v228", "v229", "v230", "v231", "a0", "a1", "a2", "a3", "a4", "a5", "a6", "a7", "a8", "a9", "a10", "a11", "a12", "a13", "a14", "a15", "a16", "a17", "a18", "a19", "a20", "a21", "a22", "a23", "a24", "a25", "a26", "a27", "a28", "a29", "a30", "a31", "a32", "a33", "a34", "a35", "a36", "a37", "a38", "a39", "a40", "a41", "a42", "a43", "a44", "a45", "a46", "a47", "a48", "a49", "a50", "a51", "a52", "a53", "a54", "a55", "a56", "a57", "a58", "a59", "a60", "a61", "a62", "a63", "a64", "a65", "a66", "a67", "a68", "a69", "a70", "a71", "a72", "a73", "a74", "a75", "a76", "a77", "a78", "a79", "a80", "a81", "a82", "a83", "a84", "a85", "a86", "a87", "a88", "a89", "a90", "a91", "a92", "a93", "a94", "a95", "a96", "a97", "a98", "a99", "a100", "a101", "a102", "a103", "a104", "a105", "a106", "a107", "a108", "a109", "a110", "a111", "a112", "a113", "a114", "a115", "a116", "a117", "a118", "a119", "a120", "a121", "a122", "a123", "a124", "a125", "a126", "a127", "a128", "a129", "a130", "a131", "a132", "a133", "a134", "a135", "a136", "a137", "a138", "a139", "a140", "a141", "a142", "a143", "a144", "a145", "a146", "a147", "a148", "a149", "a150", "a151", "a152", "a153", "a154", "a155", "a156", "a157", "a158", "a159", "a160", "a161", "a162", "a163", "a164", "a165", "a166", "a167", "a168", "a169", "a170", "a171", "a172", "a173", "a174", "a175", "a176", "a177", "a178", "a179", "a180", "a181", "a182", "a183", "a184", "a185", "a186", "a187", "a188", "a189", "a190", "a191", "a192", "a193", "a194", "a195", "a196", "a197", "a198", "a199", "a200", "a201", "a202", "a203", "a204", "a205", "a206", "a207", "a208", "a209", "a210", "a211", "a212", "a213", "a214", "a215", "a216", "a217", "a218", "a219", "a220", "a221", "a222", "a223", "a224", "a225", "a226", "a227", "a228", "a229", "a230", "a231", "a232", "a233", "a234", "a235", "a236", "a237", "a238", "a239", "a240", "a241", "a242", "a243", "a244", "a245", "a246", "a247", "a248", "a249", "a250", "a251", "a252", "a253", "a254", "a255", "s36", "s37", "s38", "s39", "s40", "s41", "s42", "s43", "s44", "s45", "s46", "s47", "s48", "s49", "s50", "s51", "s52", "s53", "s54", "s55", "s56", "s57", "s58", "s59", "s60", "s61", "s62", "s63", "s64", "s65", "s66", "s67", "s68", "s69", "s70", "s71", "s72", "s73", "s74", "s75", "s76", "s77", "s78", "s79", "s80", "s81", "s82", "s83", "s84", "s85", "s86", "s87", "s88", "s89", "s90", "s91", "s92", "s93", "s94", "s95", "s96", "s97", "vcc", "scc", "memory");
    LANE_ID(lq_);
    const int g = QT0 + (NCV - 1 - iw) * 164 + min(wave * 64 + lq_, 163);
    if (QTB) touch_lds((const char*)q + (size_t)(g < 32768 ? g : 0) * 128, sScr);
  }
  WAIT_LGKM0();
  __builtin_amdgcn_s_barrier();
  CTS(10);
  }
  const unsigned sDbg0 = 110u * 1048576u + (b * 4 + wave) * 8192;
  constexpr unsigned OOB = 0x80000000u;
  if (kind == 6) {
    LANE_ID(lr);
    const int r8 = lr & 7, seg = lr >> 3, j = lr & 15, g = lr >> 4, rr = 8 * g + (j >> 2);
    const unsigned sWave = wave, sExp = __builtin_amdgcn_readfirstlane(ep), sG0 = slice < 16 ? slice + 1 : 0, sStep = NRW, sDbg = sDbg0;
    const unsigned sWw = ((slice >> 3) * 1792 + 16 * wave) * 2048 + (slice & 7) * 256;
    const unsigned vWd0 = seg * 2048 + (r8 ^ swzB(seg)) * 16, vWd1 = (seg + 8) * 2048 + (r8 ^ swzB(8 + seg)) * 16;
    const unsigned vWr0 = (wave >> 1) * 8192 + rr * 128 + (((wave & 1) * 4 + (lr & 3)) ^ swzB(rr)) * 16, vWr1 = vWr0 + 8;
    const unsigned vRF = (wave * 8 + (seg >> 2)) * 1024 + ((seg & 3) * 16 + r8) * 16;
    const unsigned vRwr = seg == 0 ? r8 * 64 + wave * 4 : 512 + lr * 4, vRrd = r8 * 64;
    const unsigned vRP = g == (j >> 2) ? r8 * 64 + 16 + wave * 8 + (seg & 1) * 4 : 768 + lr * 4;
    const unsigned vRS = (wave == 0 && seg < 2) ? r8 * 16 + seg * 256 : OOB, vFlag = lr < 32 ? lr * 4 : OOB;
    const unsigned vL4 = ((lr & 2) * 4 + wave * 2 + (lr & 1)) * 128, vLY = min((wave < 3 ? wave * 32 : 112) + lr, wave < 2 ? wave * 32 + 31 : wave == 2 ? 111 : NCV - 1) * 128;
    const unsigned vQ = r8 * 2048 + wave * 512 + seg * 16;
    const unsigned vY0 = wave * 4096 + lr * 16;
    const unsigned vTr = 4096 + wave * 4096 + ((2 * g + ((j & 3) >> 1)) * 8 + (j >> 2)) * 16 + (j & 1) * 8;
    const unsigned one = g == (j >> 2) ? 0x3f80u << (16 * (j & 1)) : 0u, vSa = (j & 2) ? 0u : one, vSb = (j & 2) ? one : 0u;
    (void)sDbg;
    asm volatile(
R"(.macro M d,w,x
v_mfma_f32_16x16x32_bf16 v[\d:\d+3], \w, v[\x:\x+3], v[\d:\d+3]
.endm
.macro R d,a,o
ds_read_b128 v[\d:\d+3], v\a offset:\o
.endm
.macro T d,a,o
ds_read_b64_tr_b16 \d, v\a offset:\o
.endm
.macro D a,s,o
buffer_load_dwordx4 v\a, s[36:39], s\s offen offset:\o sc1 lds
.endm
.macro L c
s_waitcnt lgkmcnt(\c)
.endm
Lcode_r_b:
s_mov_b64 s[36:37],%0
s_mov_b32 s38,111243392
s_mov_b32 s39,0x00020000
s_mov_b32 s44,%1
s_mov_b32 s45,%2
s_mov_b32 s46,%3
s_mov_b32 s50,%4
s_mov_b32 s53,%5
v_mov_b32 v0,%6
v_mov_b32 v1,%7
v_mov_b32 v2,%8
v_mov_b32 v3,%9
v_mov_b32 v4,%10
v_mov_b32 v5,%11
v_mov_b32 v6,%12
v_mov_b32 v7,%13
v_mov_b32 v8,%14
v_mov_b32 v9,%15
v_mov_b32 v10,%16
v_mov_b32 v11,%17
v_mov_b32 v12,%18
v_mov_b32 v13,%19
v_mov_b32 v14,%20
v_mov_b32 v15,%21
v_mov_b32 v16,%22
s_mov_b32 s47,256
s_mov_b32 s49,0
s_mov_b32 s93,s45
s_mov_b64 s[84:85],%23
s_mov_b32 s86,4194304
s_mov_b32 s87,0x00020000
s_lshl_b32 s55,s45,14
buffer_load_dwordx4 v[164:167],v8,s[84:87],s55 offen offset:0 nt
buffer_load_dwordx4 v[168:171],v8,s[84:87],s55 offen offset:128 nt
buffer_load_dwordx4 v[172:175],v8,s[84:87],s55 offen offset:256 nt
buffer_load_dwordx4 v[176:179],v8,s[84:87],s55 offen offset:384 nt
v_mov_b32 v17,s53
v_mbcnt_lo_u32_b32 v19,-1,0
v_mbcnt_hi_u32_b32 v19,-1,v19
v_lshlrev_b32 v19,4,v19
s_mov_b32 s82,0
s_mov_b32 s63,0x7fff0000
s_mov_b32 s64,0
s_mov_b32 s67,0
s_lshl_b32 s65,s44,12
s_add_u32 s65,s65,4096
s_lshl_b32 s66,s44,7
s_add_u32 s66,s66,109056000
s_mov_b32 s70,0xaaaaaaaa
s_mov_b32 s71,0xaaaaaaaa
s_mov_b32 s72,0xcccccccc
s_mov_b32 s73,0xcccccccc
s_mov_b32 s74,0xff00ff00
s_mov_b32 s75,0xff00ff00
s_mov_b32 s68,0x3a800000
s_mov_b32 s69,0x3727c5ac
s_mov_b32 s76,0xc0135761
s_mov_b32 s77,0x3d372713
v_mov_b32 v32,0x3f803f80
v_mov_b32 v33,0x3f803f80
v_mov_b32 v34,0x3f803f80
v_mov_b32 v35,0x3f803f80
v_mov_b32 v24,0
v_mov_b32 v25,0
v_mov_b32 v26,v11
v_mov_b32 v27,v12
v_mov_b32 v28,0
v_mov_b32 v29,0
s_getpc_b64 s[94:95]
Lr_pc_%=:
s_add_u32 s94,s94,Lr_tab_%=-Lr_pc_%=
s_addc_u32 s95,s95,0
s_mov_b32 s81,0x7fff0000
s_lshl_b32 s54,s44,11
s_add_u32 s80,s45,85
s_cmp_eq_u32 s45,0
s_cselect_b32 s80,-1,s80
s_waitcnt vmcnt(0)
Lr_a0_%=:
s_cmp_lg_u32 s67,0
s_cbranch_scc1 Lr_b0_%=
s_waitcnt vmcnt(14)
v_mfma_f32_16x16x32_bf16 v[36:39],v[32:35],v[164:167],0
s_add_u32 s79,s45,s46
s_cmp_lt_u32 s45,64
s_cselect_b32 s78,s49,0
v_mfma_f32_16x16x32_bf16 v[40:43],v[164:167],v[164:167],0
s_cmp_ge_u32 s79,64
s_cselect_b32 s78,s78,0
s_add_u32 s62,s79,s78
M 36,v[32:35],168
s_lshr_b32 s78,s45,1
s_and_b32 s79,s45,1
s_lshl_b32 s79,s79,7
M 40,v[168:171],168
s_lshl_b32 s57,s78,15
s_lshl_b32 s55,s62,14
s_cmp_lt_u32 s62,s47
M 36,v[32:35],172
s_cselect_b32 s55,s55,0x7fff0000
s_add_u32 s57,s57,s79
s_lshl_b32 s59,s78,9
M 40,v[172:175],172
buffer_load_dwordx4 v[196:199],v8,s[84:87],s55 offen offset:0 nt
M 36,v[32:35],176
buffer_load_dwordx4 v[200:203],v8,s[84:87],s55 offen offset:128 nt
M 40,v[176:179],176
buffer_load_dwordx4 v[204:207],v8,s[84:87],s55 offen offset:256 nt
v_mfma_f32_16x16x32_bf16 v[116:119],v[26:29],v[164:167],0
buffer_load_dwordx4 v[208:211],v8,s[84:87],s55 offen offset:384 nt
v_mfma_f32_16x16x32_bf16 v[120:123],v[24:27],v[164:167],0
s_add_u32 s57,s57,8388608
s_add_u32 s59,s59,s79
s_lshl_b32 s60,s45,9
v_mfma_f32_16x16x32_bf16 v[124:127],v[26:29],v[168:171],0
s_add_u32 s58,s57,4096
s_add_u32 s59,s59,106954752
s_add_u32 s60,s60,s66
v_mfma_f32_16x16x32_bf16 v[128:131],v[24:27],v[168:171],0
v_add_f32_dpp v44,v36,v36 row_ror:8 row_mask:0xf bank_mask:0xf
v_cndmask_b32 v45,v40,v41,s[70:71]
v_mfma_f32_16x16x32_bf16 v[132:135],v[26:29],v[172:175],0
ds_write_b32 v2,v44 offset:0
v_mfma_f32_16x16x32_bf16 v[136:139],v[24:27],v[172:175],0
v_cndmask_b32 v46,v42,v43,s[70:71]
v_cndmask_b32 v45,v45,v46,s[72:73]
v_mfma_f32_16x16x32_bf16 v[140:143],v[26:29],v[176:179],0
ds_write_b32 v0,v45 offset:0
v_mfma_f32_16x16x32_bf16 v[144:147],v[24:27],v[176:179],0
L 0
s_barrier
R 52,3,0
R 56,3,16
R 60,3,32
s_waitcnt vmcnt(8)
buffer_store_dword v17,v5,s[36:39],s63 offen offset:0 sc0 sc1
s_mov_b32 s63,0x7fff0000
s_cmp_lt_u32 s45,64
s_cselect_b32 s64,-192,0
s_add_u32 s64,s64,256
s_cmp_lt_u32 s45,64
s_cselect_b32 s61,0,32768
s_add_u32 s61,s61,111177728
s_mul_i32 s56,s45,16384
s_add_u32 s56,s56,98566144
Lr_bpy0_%=:
buffer_load_dword v18,v7,s[36:39],s61 offen offset:0 sc1
s_add_u32 s82,s82,1
s_waitcnt vmcnt(0)
v_cmp_eq_u32 vcc,s53,v18
s_nop 0
s_cmp_eq_u64 vcc,-1
s_cbranch_scc1 Lr_bqy0_%=
s_sleep 2
s_cmp_lt_u32 s82,20000
s_cbranch_scc1 Lr_bpy0_%=
Lr_bqy0_%=:
s_add_u32 m0,s65,0
s_nop 0
D 9,56,0
D 9,56,1024
D 9,56,2048
D 9,56,3072
s_waitcnt vmcnt(0)
T v[68:69],10,0
T v[70:71],10,64
T v[72:73],10,1024
T v[74:75],10,1088
T v[76:77],10,2048
T v[78:79],10,2112
T v[80:81],10,3072
T v[82:83],10,3136
s_mov_b32 s45,s62
s_min_u32 s78,s64,s47
s_mul_i32 s56,s45,16384
s_add_u32 s56,s56,98566144
s_cmp_lt_u32 s45,s78
s_cselect_b32 s67,1,0
s_cselect_b32 s56,s56,0x7fff0000
s_add_u32 m0,s65,16384
s_nop 0
D 9,56,0
D 9,56,1024
D 9,56,2048
D 9,56,3072
L 4
v_lshlrev_b32 v84,16,v68
v_and_b32 v85,0xffff0000,v68
v_lshlrev_b32 v86,16,v69
v_and_b32 v87,0xffff0000,v69
v_lshlrev_b32 v88,16,v70
v_and_b32 v89,0xffff0000,v70
v_lshlrev_b32 v90,16,v71
v_and_b32 v91,0xffff0000,v71
v_lshlrev_b32 v92,16,v72
v_and_b32 v93,0xffff0000,v72
v_lshlrev_b32 v94,16,v73
v_and_b32 v95,0xffff0000,v73
v_lshlrev_b32 v96,16,v74
v_and_b32 v97,0xffff0000,v74
v_lshlrev_b32 v98,16,v75
v_and_b32 v99,0xffff0000,v75
L 0
v_lshlrev_b32 v100,16,v76
v_and_b32 v101,0xffff0000,v76
v_lshlrev_b32 v102,16,v77
v_and_b32 v103,0xffff0000,v77
v_lshlrev_b32 v104,16,v78
v_and_b32 v105,0xffff0000,v78
v_lshlrev_b32 v106,16,v79
v_and_b32 v107,0xffff0000,v79
v_lshlrev_b32 v108,16,v80
v_and_b32 v109,0xffff0000,v80
v_lshlrev_b32 v110,16,v81
v_and_b32 v111,0xffff0000,v81
v_lshlrev_b32 v112,16,v82
v_and_b32 v113,0xffff0000,v82
v_lshlrev_b32 v114,16,v83
v_and_b32 v115,0xffff0000,v83
s_branch Lr_c0_%=
Lr_b0_%=:
s_waitcnt vmcnt(10)
T v[68:69],10,0
T v[70:71],10,64
T v[72:73],10,1024
T v[74:75],10,1088
T v[76:77],10,2048
T v[78:79],10,2112
T v[80:81],10,3072
T v[82:83],10,3136
v_mfma_f32_16x16x32_bf16 v[36:39],v[32:35],v[164:167],0
s_add_u32 s79,s45,s46
s_cmp_lt_u32 s45,64
s_cselect_b32 s78,s49,0
v_mfma_f32_16x16x32_bf16 v[40:43],v[164:167],v[164:167],0
s_cmp_ge_u32 s79,64
s_cselect_b32 s78,s78,0
s_add_u32 s62,s79,s78
M 36,v[32:35],168
s_lshr_b32 s78,s45,1
s_and_b32 s79,s45,1
s_lshl_b32 s60,s45,9
M 40,v[168:171],168
s_lshl_b32 s57,s78,15
s_lshl_b32 s59,s78,9
s_mov_b32 s45,s62
M 36,v[32:35],172
s_lshl_b32 s79,s79,7
s_min_u32 s78,s64,s47
s_mul_i32 s56,s45,16384
M 40,v[172:175],172
s_lshl_b32 s55,s62,14
s_cmp_lt_u32 s62,s47
s_cselect_b32 s55,s55,0x7fff0000
M 36,v[32:35],176
buffer_load_dwordx4 v[196:199],v8,s[84:87],s55 offen offset:0 nt
M 40,v[176:179],176
buffer_load_dwordx4 v[200:203],v8,s[84:87],s55 offen offset:128 nt
v_mfma_f32_16x16x32_bf16 v[116:119],v[26:29],v[164:167],0
buffer_load_dwordx4 v[204:207],v8,s[84:87],s55 offen offset:256 nt
v_mfma_f32_16x16x32_bf16 v[120:123],v[24:27],v[164:167],0
buffer_load_dwordx4 v[208:211],v8,s[84:87],s55 offen offset:384 nt
v_mfma_f32_16x16x32_bf16 v[124:127],v[26:29],v[168:171],0
s_add_u32 s57,s57,s79
s_add_u32 s56,s56,98566144
s_cmp_lt_u32 s45,s78
v_mfma_f32_16x16x32_bf16 v[128:131],v[24:27],v[168:171],0
s_cselect_b32 s56,s56,0x7fff0000
v_add_f32_dpp v44,v36,v36 row_ror:8 row_mask:0xf bank_mask:0xf
v_cndmask_b32 v45,v40,v41,s[70:71]
v_mfma_f32_16x16x32_bf16 v[132:135],v[26:29],v[172:175],0
s_cselect_b32 s67,1,0
ds_write_b32 v2,v44 offset:0
v_mfma_f32_16x16x32_bf16 v[136:139],v[24:27],v[172:175],0
s_add_u32 s57,s57,8388608
v_cndmask_b32 v46,v42,v43,s[70:71]
v_cndmask_b32 v45,v45,v46,s[72:73]
v_mfma_f32_16x16x32_bf16 v[140:143],v[26:29],v[176:179],0
s_add_u32 s59,s59,s79
ds_write_b32 v0,v45 offset:0
v_mfma_f32_16x16x32_bf16 v[144:147],v[24:27],v[176:179],0
L 2
s_add_u32 m0,s65,16384
s_add_u32 s58,s57,4096
v_mfma_f32_16x16x32_bf16 v[84:87],v[26:29],v[68:71],0
D 9,56,0
v_mfma_f32_16x16x32_bf16 v[88:91],v[24:27],v[68:71],0
D 9,56,1024
v_mfma_f32_16x16x32_bf16 v[92:95],v[26:29],v[72:75],0
L 0
D 9,56,2048
v_mfma_f32_16x16x32_bf16 v[96:99],v[24:27],v[72:75],0
s_barrier
R 52,3,0
R 56,3,16
v_mfma_f32_16x16x32_bf16 v[100:103],v[26:29],v[76:79],0
s_add_u32 s59,s59,106954752
s_add_u32 s60,s60,s66
R 60,3,32
v_mfma_f32_16x16x32_bf16 v[104:107],v[24:27],v[76:79],0
D 9,56,3072
v_mfma_f32_16x16x32_bf16 v[108:111],v[26:29],v[80:83],0
v_mfma_f32_16x16x32_bf16 v[112:115],v[24:27],v[80:83],0
Lr_c0_%=:
L 0
v_pk_add_f32 v[52:53],v[52:53],v[54:55]
v_pk_add_f32 v[56:57],v[56:57],v[58:59]
v_pk_add_f32 v[60:61],v[60:61],v[62:63]
v_add_f32 v52,v52,v53
v_pk_add_f32 v[56:57],v[56:57],v[60:61]
v_mul_f32 v52,s68,v52
v_add_f32 v53,v56,v57
v_mul_f32 v53,s68,v53
v_fma_f32 v53,-v52,v52,v53
v_max_f32 v53,0,v53
s_waitcnt vmcnt(12)
buffer_store_dword v17,v5,s[36:39],s63 offen offset:0 sc0 sc1
s_mov_b32 s63,0x7fff0000
v_add_f32 v53,s69,v53
v_rsq_f32 v48,v53
s_nop 0
v_mul_f32_e64 v50,-v52,v48
v_mov_b32 v49,v48
v_mov_b32 v51,v50
v_pk_fma_f32 v[52:53],v[116:117],v[48:49],v[50:51]
v_pk_fma_f32 v[54:55],v[118:119],v[48:49],v[50:51]
v_pk_fma_f32 v[56:57],v[120:121],v[48:49],v[50:51]
v_pk_fma_f32 v[58:59],v[122:123],v[48:49],v[50:51]
v_pk_fma_f32 v[116:117],v[52:53],v[84:85],v[116:117]
v_pk_fma_f32 v[118:119],v[54:55],v[86:87],v[118:119]
v_pk_fma_f32 v[120:121],v[56:57],v[88:89],v[120:121]
v_pk_fma_f32 v[122:123],v[58:59],v[90:91],v[122:123]
v_pk_fma_f32 v[52:53],v[124:125],v[48:49],v[50:51]
v_pk_fma_f32 v[54:55],v[126:127],v[48:49],v[50:51]
v_pk_fma_f32 v[56:57],v[128:129],v[48:49],v[50:51]
v_pk_fma_f32 v[58:59],v[130:131],v[48:49],v[50:51]
v_pk_fma_f32 v[124:125],v[52:53],v[92:93],v[124:125]
v_pk_fma_f32 v[126:127],v[54:55],v[94:95],v[126:127]
v_pk_fma_f32 v[128:129],v[56:57],v[96:97],v[128:129]
v_pk_fma_f32 v[130:131],v[58:59],v[98:99],v[130:131]
v_pk_fma_f32 v[52:53],v[132:133],v[48:49],v[50:51]
v_pk_fma_f32 v[54:55],v[134:135],v[48:49],v[50:51]
v_pk_fma_f32 v[56:57],v[136:137],v[48:49],v[50:51]
v_pk_fma_f32 v[58:59],v[138:139],v[48:49],v[50:51]
v_pk_fma_f32 v[132:133],v[52:53],v[100:101],v[132:133]
v_pk_fma_f32 v[134:135],v[54:55],v[102:103],v[134:135]
v_pk_fma_f32 v[136:137],v[56:57],v[104:105],v[136:137]
v_pk_fma_f32 v[138:139],v[58:59],v[106:107],v[138:139]
v_pk_fma_f32 v[52:53],v[140:141],v[48:49],v[50:51]
v_pk_fma_f32 v[54:55],v[142:143],v[48:49],v[50:51]
v_pk_fma_f32 v[56:57],v[144:145],v[48:49],v[50:51]
v_pk_fma_f32 v[58:59],v[146:147],v[48:49],v[50:51]
v_pk_fma_f32 v[140:141],v[52:53],v[108:109],v[140:141]
v_pk_fma_f32 v[142:143],v[54:55],v[110:111],v[142:143]
v_pk_fma_f32 v[144:145],v[56:57],v[112:113],v[144:145]
v_pk_fma_f32 v[146:147],v[58:59],v[114:115],v[146:147]
v_cvt_pk_bf16_f32 v148,v116,v117
v_cvt_pk_bf16_f32 v149,v118,v119
v_cvt_pk_bf16_f32 v150,v120,v121
v_cvt_pk_bf16_f32 v151,v122,v123
v_cvt_pk_bf16_f32 v152,v124,v125
v_cvt_pk_bf16_f32 v153,v126,v127
v_mfma_f32_16x16x32_bf16 v[36:39],v[32:35],v[148:151],0
v_cvt_pk_bf16_f32 v154,v128,v129
v_cvt_pk_bf16_f32 v155,v130,v131
v_mfma_f32_16x16x32_bf16 v[40:43],v[148:151],v[148:151],0
v_cvt_pk_bf16_f32 v156,v132,v133
v_cvt_pk_bf16_f32 v157,v134,v135
M 36,v[32:35],152
v_cvt_pk_bf16_f32 v158,v136,v137
v_cvt_pk_bf16_f32 v159,v138,v139
M 40,v[152:155],152
v_cvt_pk_bf16_f32 v160,v140,v141
v_cvt_pk_bf16_f32 v161,v142,v143
M 36,v[32:35],156
v_cvt_pk_bf16_f32 v162,v144,v145
v_cvt_pk_bf16_f32 v163,v146,v147
M 40,v[156:159],156
buffer_store_dwordx4 v[148:151],v1,s[36:39],s57 offen offset:0 sc0 sc1
M 36,v[32:35],160
buffer_store_dwordx4 v[152:155],v1,s[36:39],s57 offen offset:2048 sc0 sc1
M 40,v[160:163],160
buffer_store_dwordx4 v[156:159],v1,s[36:39],s58 offen offset:0 sc0 sc1
s_nop 4
v_add_f32_dpp v44,v36,v36 row_ror:8 row_mask:0xf bank_mask:0xf
s_nop 0
v_cndmask_b32 v45,v40,v41,s[70:71]
v_cndmask_b32 v46,v42,v43,s[70:71]
ds_write_b32 v2,v44 offset:1024
v_cndmask_b32 v45,v45,v46,s[72:73]
ds_write_b32 v0,v45 offset:1024
buffer_store_dwordx4 v[160:163],v1,s[36:39],s58 offen offset:2048 sc0 sc1
s_waitcnt vmcnt(13) lgkmcnt(0)
s_barrier
R 52,3,1024
R 56,3,1040
R 60,3,1056
L 0
v_pk_add_f32 v[52:53],v[52:53],v[54:55]
v_pk_add_f32 v[56:57],v[56:57],v[58:59]
v_pk_add_f32 v[60:61],v[60:61],v[62:63]
v_add_f32 v52,v52,v53
v_pk_add_f32 v[56:57],v[56:57],v[60:61]
v_mul_f32 v52,s68,v52
v_add_f32 v53,v56,v57
v_mul_f32 v53,s68,v53
v_fma_f32 v53,-v52,v52,v53
v_max_f32 v53,0,v53
v_add_f32 v54,s69,v53
v_fma_f32 v55,s69,v54,v53
v_rsq_f32 v65,v54
v_rsq_f32 v54,v55
v_mul_f32_e64 v64,-v52,v65
v_mul_f32 v63,v55,v54
v_mul_f32 v61,s76,v54
v_mul_f32 v55,v54,v54
v_mul_f32 v62,s77,v55
v_mul_f32 v62,v62,v61
v_cndmask_b32 v60,v52,v64,s[74:75]
v_cndmask_b32 v61,v61,v65,s[74:75]
buffer_store_dwordx4 v[60:63],v4,s[36:39],s59 offen offset:0 sc0 sc1
s_mov_b32 s63,s60
s_cmp_lt_u32 s45,s80
s_cbranch_scc1 Lr_bg0_%=
s_swappc_b64 s[88:89],s[94:95]
s_add_u32 s94,s94,256
s_addc_u32 s95,s95,0
s_branch Lr_bh0_%=
Lr_bg0_%=:
buffer_store_dword v17,v5,s[36:39],s81 offen offset:0 sc0 sc1
buffer_store_dword v17,v5,s[36:39],s81 offen offset:0 sc0 sc1
buffer_store_dword v17,v5,s[36:39],s81 offen offset:0 sc0 sc1
buffer_store_dword v17,v5,s[36:39],s81 offen offset:0 sc0 sc1
Lr_bh0_%=:
s_cmp_lt_u32 s45,s47
s_cbranch_scc0 Lr_mid_%=
Lr_a1_%=:
s_cmp_lg_u32 s67,0
s_cbranch_scc1 Lr_b1_%=
s_waitcnt vmcnt(14)
v_mfma_f32_16x16x32_bf16 v[36:39],v[32:35],v[196:199],0
s_add_u32 s79,s45,s46
s_cmp_lt_u32 s45,64
s_cselect_b32 s78,s49,0
v_mfma_f32_16x16x32_bf16 v[40:43],v[196:199],v[196:199],0
s_cmp_ge_u32 s79,64
s_cselect_b32 s78,s78,0
s_add_u32 s62,s79,s78
M 36,v[32:35],200
s_lshr_b32 s78,s45,1
s_and_b32 s79,s45,1
s_lshl_b32 s79,s79,7
M 40,v[200:203],200
s_lshl_b32 s57,s78,15
s_lshl_b32 s55,s62,14
s_cmp_lt_u32 s62,s47
M 36,v[32:35],204
s_cselect_b32 s55,s55,0x7fff0000
s_add_u32 s57,s57,s79
s_lshl_b32 s59,s78,9
M 40,v[204:207],204
buffer_load_dwordx4 v[164:167],v8,s[84:87],s55 offen offset:0 nt
M 36,v[32:35],208
buffer_load_dwordx4 v[168:171],v8,s[84:87],s55 offen offset:128 nt
M 40,v[208:211],208
buffer_load_dwordx4 v[172:175],v8,s[84:87],s55 offen offset:256 nt
v_mfma_f32_16x16x32_bf16 v[116:119],v[26:29],v[196:199],0
buffer_load_dwordx4 v[176:179],v8,s[84:87],s55 offen offset:384 nt
v_mfma_f32_16x16x32_bf16 v[120:123],v[24:27],v[196:199],0
s_add_u32 s57,s57,8388608
s_add_u32 s59,s59,s79
s_lshl_b32 s60,s45,9
v_mfma_f32_16x16x32_bf16 v[124:127],v[26:29],v[200:203],0
s_add_u32 s58,s57,4096
s_add_u32 s59,s59,106954752
s_add_u32 s60,s60,s66
v_mfma_f32_16x16x32_bf16 v[128:131],v[24:27],v[200:203],0
v_add_f32_dpp v44,v36,v36 row_ror:8 row_mask:0xf bank_mask:0xf
v_cndmask_b32 v45,v40,v41,s[70:71]
v_mfma_f32_16x16x32_bf16 v[132:135],v[26:29],v[204:207],0
ds_write_b32 v2,v44 offset:0
v_mfma_f32_16x16x32_bf16 v[136:139],v[24:27],v[204:207],0
v_cndmask_b32 v46,v42,v43,s[70:71]
v_cndmask_b32 v45,v45,v46,s[72:73]
)"
R"(v_mfma_f32_16x16x32_bf16 v[140:143],v[26:29],v[208:211],0
ds_write_b32 v0,v45 offset:0
v_mfma_f32_16x16x32_bf16 v[144:147],v[24:27],v[208:211],0
L 0
s_barrier
R 52,3,0
R 56,3,16
R 60,3,32
s_waitcnt vmcnt(8)
buffer_store_dword v17,v5,s[36:39],s63 offen offset:0 sc0 sc1
s_mov_b32 s63,0x7fff0000
s_cmp_lt_u32 s45,64
s_cselect_b32 s64,-192,0
s_add_u32 s64,s64,256
s_cmp_lt_u32 s45,64
s_cselect_b32 s61,0,32768
s_add_u32 s61,s61,111177728
s_mul_i32 s56,s45,16384
s_add_u32 s56,s56,98566144
Lr_bpy1_%=:
buffer_load_dword v18,v7,s[36:39],s61 offen offset:0 sc1
s_add_u32 s82,s82,1
s_waitcnt vmcnt(0)
v_cmp_eq_u32 vcc,s53,v18
s_nop 0
s_cmp_eq_u64 vcc,-1
s_cbranch_scc1 Lr_bqy1_%=
s_sleep 2
s_cmp_lt_u32 s82,20000
s_cbranch_scc1 Lr_bpy1_%=
Lr_bqy1_%=:
s_add_u32 m0,s65,16384
s_nop 0
D 9,56,0
D 9,56,1024
D 9,56,2048
D 9,56,3072
s_waitcnt vmcnt(0)
T v[68:69],10,16384
T v[70:71],10,16448
T v[72:73],10,17408
T v[74:75],10,17472
T v[76:77],10,18432
T v[78:79],10,18496
T v[80:81],10,19456
T v[82:83],10,19520
s_mov_b32 s45,s62
s_min_u32 s78,s64,s47
s_mul_i32 s56,s45,16384
s_add_u32 s56,s56,98566144
s_cmp_lt_u32 s45,s78
s_cselect_b32 s67,1,0
s_cselect_b32 s56,s56,0x7fff0000
s_add_u32 m0,s65,0
s_nop 0
D 9,56,0
D 9,56,1024
D 9,56,2048
D 9,56,3072
L 4
v_lshlrev_b32 v84,16,v68
v_and_b32 v85,0xffff0000,v68
v_lshlrev_b32 v86,16,v69
v_and_b32 v87,0xffff0000,v69
v_lshlrev_b32 v88,16,v70
v_and_b32 v89,0xffff0000,v70
v_lshlrev_b32 v90,16,v71
v_and_b32 v91,0xffff0000,v71
v_lshlrev_b32 v92,16,v72
v_and_b32 v93,0xffff0000,v72
v_lshlrev_b32 v94,16,v73
v_and_b32 v95,0xffff0000,v73
v_lshlrev_b32 v96,16,v74
v_and_b32 v97,0xffff0000,v74
v_lshlrev_b32 v98,16,v75
v_and_b32 v99,0xffff0000,v75
L 0
v_lshlrev_b32 v100,16,v76
v_and_b32 v101,0xffff0000,v76
v_lshlrev_b32 v102,16,v77
v_and_b32 v103,0xffff0000,v77
v_lshlrev_b32 v104,16,v78
v_and_b32 v105,0xffff0000,v78
v_lshlrev_b32 v106,16,v79
v_and_b32 v107,0xffff0000,v79
v_lshlrev_b32 v108,16,v80
v_and_b32 v109,0xffff0000,v80
v_lshlrev_b32 v110,16,v81
v_and_b32 v111,0xffff0000,v81
v_lshlrev_b32 v112,16,v82
v_and_b32 v113,0xffff0000,v82
v_lshlrev_b32 v114,16,v83
v_and_b32 v115,0xffff0000,v83
s_branch Lr_c1_%=
Lr_b1_%=:
s_waitcnt vmcnt(10)
T v[68:69],10,16384
T v[70:71],10,16448
T v[72:73],10,17408
T v[74:75],10,17472
T v[76:77],10,18432
T v[78:79],10,18496
T v[80:81],10,19456
T v[82:83],10,19520
v_mfma_f32_16x16x32_bf16 v[36:39],v[32:35],v[196:199],0
s_add_u32 s79,s45,s46
s_cmp_lt_u32 s45,64
s_cselect_b32 s78,s49,0
v_mfma_f32_16x16x32_bf16 v[40:43],v[196:199],v[196:199],0
s_cmp_ge_u32 s79,64
s_cselect_b32 s78,s78,0
s_add_u32 s62,s79,s78
M 36,v[32:35],200
s_lshr_b32 s78,s45,1
s_and_b32 s79,s45,1
s_lshl_b32 s60,s45,9
M 40,v[200:203],200
s_lshl_b32 s57,s78,15
s_lshl_b32 s59,s78,9
s_mov_b32 s45,s62
M 36,v[32:35],204
s_lshl_b32 s79,s79,7
s_min_u32 s78,s64,s47
s_mul_i32 s56,s45,16384
M 40,v[204:207],204
s_lshl_b32 s55,s62,14
s_cmp_lt_u32 s62,s47
s_cselect_b32 s55,s55,0x7fff0000
M 36,v[32:35],208
buffer_load_dwordx4 v[164:167],v8,s[84:87],s55 offen offset:0 nt
M 40,v[208:211],208
buffer_load_dwordx4 v[168:171],v8,s[84:87],s55 offen offset:128 nt
v_mfma_f32_16x16x32_bf16 v[116:119],v[26:29],v[196:199],0
buffer_load_dwordx4 v[172:175],v8,s[84:87],s55 offen offset:256 nt
v_mfma_f32_16x16x32_bf16 v[120:123],v[24:27],v[196:199],0
buffer_load_dwordx4 v[176:179],v8,s[84:87],s55 offen offset:384 nt
v_mfma_f32_16x16x32_bf16 v[124:127],v[26:29],v[200:203],0
s_add_u32 s57,s57,s79
s_add_u32 s56,s56,98566144
s_cmp_lt_u32 s45,s78
v_mfma_f32_16x16x32_bf16 v[128:131],v[24:27],v[200:203],0
s_cselect_b32 s56,s56,0x7fff0000
v_add_f32_dpp v44,v36,v36 row_ror:8 row_mask:0xf bank_mask:0xf
v_cndmask_b32 v45,v40,v41,s[70:71]
v_mfma_f32_16x16x32_bf16 v[132:135],v[26:29],v[204:207],0
s_cselect_b32 s67,1,0
ds_write_b32 v2,v44 offset:0
v_mfma_f32_16x16x32_bf16 v[136:139],v[24:27],v[204:207],0
s_add_u32 s57,s57,8388608
v_cndmask_b32 v46,v42,v43,s[70:71]
v_cndmask_b32 v45,v45,v46,s[72:73]
v_mfma_f32_16x16x32_bf16 v[140:143],v[26:29],v[208:211],0
s_add_u32 s59,s59,s79
ds_write_b32 v0,v45 offset:0
v_mfma_f32_16x16x32_bf16 v[144:147],v[24:27],v[208:211],0
L 2
s_add_u32 m0,s65,0
s_add_u32 s58,s57,4096
v_mfma_f32_16x16x32_bf16 v[84:87],v[26:29],v[68:71],0
D 9,56,0
v_mfma_f32_16x16x32_bf16 v[88:91],v[24:27],v[68:71],0
D 9,56,1024
v_mfma_f32_16x16x32_bf16 v[92:95],v[26:29],v[72:75],0
L 0
D 9,56,2048
v_mfma_f32_16x16x32_bf16 v[96:99],v[24:27],v[72:75],0
s_barrier
R 52,3,0
R 56,3,16
v_mfma_f32_16x16x32_bf16 v[100:103],v[26:29],v[76:79],0
s_add_u32 s59,s59,106954752
s_add_u32 s60,s60,s66
R 60,3,32
v_mfma_f32_16x16x32_bf16 v[104:107],v[24:27],v[76:79],0
D 9,56,3072
v_mfma_f32_16x16x32_bf16 v[108:111],v[26:29],v[80:83],0
v_mfma_f32_16x16x32_bf16 v[112:115],v[24:27],v[80:83],0
Lr_c1_%=:
L 0
v_pk_add_f32 v[52:53],v[52:53],v[54:55]
v_pk_add_f32 v[56:57],v[56:57],v[58:59]
v_pk_add_f32 v[60:61],v[60:61],v[62:63]
v_add_f32 v52,v52,v53
v_pk_add_f32 v[56:57],v[56:57],v[60:61]
v_mul_f32 v52,s68,v52
v_add_f32 v53,v56,v57
v_mul_f32 v53,s68,v53
v_fma_f32 v53,-v52,v52,v53
v_max_f32 v53,0,v53
s_waitcnt vmcnt(12)
buffer_store_dword v17,v5,s[36:39],s63 offen offset:0 sc0 sc1
s_mov_b32 s63,0x7fff0000
v_add_f32 v53,s69,v53
v_rsq_f32 v48,v53
s_nop 0
v_mul_f32_e64 v50,-v52,v48
v_mov_b32 v49,v48
v_mov_b32 v51,v50
v_pk_fma_f32 v[52:53],v[116:117],v[48:49],v[50:51]
v_pk_fma_f32 v[54:55],v[118:119],v[48:49],v[50:51]
v_pk_fma_f32 v[56:57],v[120:121],v[48:49],v[50:51]
v_pk_fma_f32 v[58:59],v[122:123],v[48:49],v[50:51]
v_pk_fma_f32 v[116:117],v[52:53],v[84:85],v[116:117]
v_pk_fma_f32 v[118:119],v[54:55],v[86:87],v[118:119]
v_pk_fma_f32 v[120:121],v[56:57],v[88:89],v[120:121]
v_pk_fma_f32 v[122:123],v[58:59],v[90:91],v[122:123]
v_pk_fma_f32 v[52:53],v[124:125],v[48:49],v[50:51]
v_pk_fma_f32 v[54:55],v[126:127],v[48:49],v[50:51]
v_pk_fma_f32 v[56:57],v[128:129],v[48:49],v[50:51]
v_pk_fma_f32 v[58:59],v[130:131],v[48:49],v[50:51]
v_pk_fma_f32 v[124:125],v[52:53],v[92:93],v[124:125]
v_pk_fma_f32 v[126:127],v[54:55],v[94:95],v[126:127]
v_pk_fma_f32 v[128:129],v[56:57],v[96:97],v[128:129]
v_pk_fma_f32 v[130:131],v[58:59],v[98:99],v[130:131]
v_pk_fma_f32 v[52:53],v[132:133],v[48:49],v[50:51]
v_pk_fma_f32 v[54:55],v[134:135],v[48:49],v[50:51]
v_pk_fma_f32 v[56:57],v[136:137],v[48:49],v[50:51]
v_pk_fma_f32 v[58:59],v[138:139],v[48:49],v[50:51]
v_pk_fma_f32 v[132:133],v[52:53],v[100:101],v[132:133]
v_pk_fma_f32 v[134:135],v[54:55],v[102:103],v[134:135]
v_pk_fma_f32 v[136:137],v[56:57],v[104:105],v[136:137]
v_pk_fma_f32 v[138:139],v[58:59],v[106:107],v[138:139]
v_pk_fma_f32 v[52:53],v[140:141],v[48:49],v[50:51]
v_pk_fma_f32 v[54:55],v[142:143],v[48:49],v[50:51]
v_pk_fma_f32 v[56:57],v[144:145],v[48:49],v[50:51]
v_pk_fma_f32 v[58:59],v[146:147],v[48:49],v[50:51]
v_pk_fma_f32 v[140:141],v[52:53],v[108:109],v[140:141]
v_pk_fma_f32 v[142:143],v[54:55],v[110:111],v[142:143]
v_pk_fma_f32 v[144:145],v[56:57],v[112:113],v[144:145]
v_pk_fma_f32 v[146:147],v[58:59],v[114:115],v[146:147]
v_cvt_pk_bf16_f32 v148,v116,v117
v_cvt_pk_bf16_f32 v149,v118,v119
v_cvt_pk_bf16_f32 v150,v120,v121
v_cvt_pk_bf16_f32 v151,v122,v123
v_cvt_pk_bf16_f32 v152,v124,v125
v_cvt_pk_bf16_f32 v153,v126,v127
v_mfma_f32_16x16x32_bf16 v[36:39],v[32:35],v[148:151],0
v_cvt_pk_bf16_f32 v154,v128,v129
v_cvt_pk_bf16_f32 v155,v130,v131
v_mfma_f32_16x16x32_bf16 v[40:43],v[148:151],v[148:151],0
v_cvt_pk_bf16_f32 v156,v132,v133
v_cvt_pk_bf16_f32 v157,v134,v135
M 36,v[32:35],152
v_cvt_pk_bf16_f32 v158,v136,v137
v_cvt_pk_bf16_f32 v159,v138,v139
M 40,v[152:155],152
v_cvt_pk_bf16_f32 v160,v140,v141
v_cvt_pk_bf16_f32 v161,v142,v143
M 36,v[32:35],156
v_cvt_pk_bf16_f32 v162,v144,v145
v_cvt_pk_bf16_f32 v163,v146,v147
M 40,v[156:159],156
buffer_store_dwordx4 v[148:151],v1,s[36:39],s57 offen offset:0 sc0 sc1
M 36,v[32:35],160
buffer_store_dwordx4 v[152:155],v1,s[36:39],s57 offen offset:2048 sc0 sc1
M 40,v[160:163],160
buffer_store_dwordx4 v[156:159],v1,s[36:39],s58 offen offset:0 sc0 sc1
s_nop 4
v_add_f32_dpp v44,v36,v36 row_ror:8 row_mask:0xf bank_mask:0xf
s_nop 0
v_cndmask_b32 v45,v40,v41,s[70:71]
v_cndmask_b32 v46,v42,v43,s[70:71]
ds_write_b32 v2,v44 offset:1024
v_cndmask_b32 v45,v45,v46,s[72:73]
ds_write_b32 v0,v45 offset:1024
buffer_store_dwordx4 v[160:163],v1,s[36:39],s58 offen offset:2048 sc0 sc1
s_waitcnt vmcnt(13) lgkmcnt(0)
s_barrier
R 52,3,1024
R 56,3,1040
R 60,3,1056
L 0
v_pk_add_f32 v[52:53],v[52:53],v[54:55]
v_pk_add_f32 v[56:57],v[56:57],v[58:59]
v_pk_add_f32 v[60:61],v[60:61],v[62:63]
v_add_f32 v52,v52,v53
v_pk_add_f32 v[56:57],v[56:57],v[60:61]
v_mul_f32 v52,s68,v52
v_add_f32 v53,v56,v57
v_mul_f32 v53,s68,v53
v_fma_f32 v53,-v52,v52,v53
v_max_f32 v53,0,v53
v_add_f32 v54,s69,v53
v_fma_f32 v55,s69,v54,v53
v_rsq_f32 v65,v54
v_rsq_f32 v54,v55
v_mul_f32_e64 v64,-v52,v65
v_mul_f32 v63,v55,v54
v_mul_f32 v61,s76,v54
v_mul_f32 v55,v54,v54
v_mul_f32 v62,s77,v55
v_mul_f32 v62,v62,v61
v_cndmask_b32 v60,v52,v64,s[74:75]
v_cndmask_b32 v61,v61,v65,s[74:75]
buffer_store_dwordx4 v[60:63],v4,s[36:39],s59 offen offset:0 sc0 sc1
s_mov_b32 s63,s60
s_cmp_lt_u32 s45,s80
s_cbranch_scc1 Lr_bg1_%=
s_swappc_b64 s[88:89],s[94:95]
s_add_u32 s94,s94,256
s_addc_u32 s95,s95,0
s_branch Lr_bh1_%=
Lr_bg1_%=:
buffer_store_dword v17,v5,s[36:39],s81 offen offset:0 sc0 sc1
buffer_store_dword v17,v5,s[36:39],s81 offen offset:0 sc0 sc1
buffer_store_dword v17,v5,s[36:39],s81 offen offset:0 sc0 sc1
buffer_store_dword v17,v5,s[36:39],s81 offen offset:0 sc0 sc1
Lr_bh1_%=:
s_cmp_lt_u32 s45,s47
s_cbranch_scc1 Lr_a0_%=
Lr_mid_%=:
s_waitcnt vmcnt(4)
buffer_store_dword v17,v5,s[36:39],s63 offen offset:0 sc0 sc1
s_mov_b32 s63,0x7fff0000
s_mov_b32 s45,s93
s_cmp_eq_u32 s93,0
s_cbranch_scc1 Lr_h0_%=
s_mov_b64 s[40:41],%24
s_mov_b32 s42,7340032
s_mov_b32 s43,0x00020000
s_lshl_b32 s57,s44,11
s_add_u32 s55,s50,1441792
s_add_u32 m0,s57,106496
s_nop 0
buffer_load_dwordx4 v13,s[40:43],s55 offen offset:0  lds
s_add_u32 m0,s57,107520
s_nop 0
buffer_load_dwordx4 v14,s[40:43],s55 offen offset:0  lds
s_add_u32 m0,s57,114560
s_nop 0
buffer_load_dwordx4 v13,s[40:43],s55 offen offset:128  lds
s_add_u32 m0,s57,115584
s_nop 0
buffer_load_dwordx4 v14,s[40:43],s55 offen offset:128  lds
s_add_u32 s55,s50,1572864
s_add_u32 m0,s57,122880
s_nop 0
buffer_load_dwordx4 v13,s[40:43],s55 offen offset:0  lds
s_add_u32 m0,s57,123904
s_nop 0
buffer_load_dwordx4 v14,s[40:43],s55 offen offset:0  lds
s_add_u32 m0,s57,130944
s_nop 0
buffer_load_dwordx4 v13,s[40:43],s55 offen offset:128  lds
s_add_u32 m0,s57,131968
s_nop 0
buffer_load_dwordx4 v14,s[40:43],s55 offen offset:128  lds
s_add_u32 s55,s50,1703936
s_add_u32 m0,s57,139264
s_nop 0
buffer_load_dwordx4 v13,s[40:43],s55 offen offset:0  lds
s_add_u32 m0,s57,140288
s_nop 0
buffer_load_dwordx4 v14,s[40:43],s55 offen offset:0  lds
s_add_u32 m0,s57,147328
s_nop 0
buffer_load_dwordx4 v13,s[40:43],s55 offen offset:128  lds
s_add_u32 m0,s57,148352
s_nop 0
buffer_load_dwordx4 v14,s[40:43],s55 offen offset:128  lds
s_add_u32 s55,s50,1835008
s_add_u32 m0,s57,40960
s_nop 0
buffer_load_dwordx4 v13,s[40:43],s55 offen offset:0  lds
s_add_u32 m0,s57,41984
s_nop 0
buffer_load_dwordx4 v14,s[40:43],s55 offen offset:0  lds
s_add_u32 m0,s57,49024
s_nop 0
buffer_load_dwordx4 v13,s[40:43],s55 offen offset:128  lds
s_add_u32 m0,s57,50048
s_nop 0
buffer_load_dwordx4 v14,s[40:43],s55 offen offset:128  lds
s_add_u32 s55,s50,1966080
s_add_u32 m0,s57,57344
s_nop 0
buffer_load_dwordx4 v13,s[40:43],s55 offen offset:0  lds
s_add_u32 m0,s57,58368
s_nop 0
buffer_load_dwordx4 v14,s[40:43],s55 offen offset:0  lds
s_add_u32 m0,s57,65408
s_nop 0
buffer_load_dwordx4 v13,s[40:43],s55 offen offset:128  lds
s_add_u32 m0,s57,66432
s_nop 0
buffer_load_dwordx4 v14,s[40:43],s55 offen offset:128  lds
Lr_h0_%=:
s_lshr_b32 s78,s45,1
s_and_b32 s79,s45,1
s_lshl_b32 s79,s79,7
s_lshl_b32 s55,s78,15
s_add_u32 s55,s55,s79
s_mul_i32 s56,s45,16384
s_add_u32 s56,s56,98566144
s_lshl_b32 s61,s78,11
s_add_u32 s61,s61,110587904
s_add_u32 s78,s45,s46
s_lshr_b32 s78,s78,1
s_lshl_b32 s62,s78,11
s_add_u32 s62,s62,110587904
s_add_u32 m0,s65,0
s_nop 0
D 9,56,0
D 9,56,1024
D 9,56,2048
D 9,56,3072
Lr_bps_%=:
buffer_load_dword v18,v6,s[36:39],s61 offen offset:0 sc1
s_add_u32 s82,s82,1
s_waitcnt vmcnt(0)
v_cmp_eq_u32 vcc,s53,v18
s_nop 0
s_cmp_eq_u64 vcc,-1
s_cbranch_scc1 Lr_bqs_%=
s_sleep 2
s_cmp_lt_u32 s82,20000
s_cbranch_scc1 Lr_bps_%=
Lr_bqs_%=:
s_add_u32 s78,s55,71303168
buffer_load_dwordx4 v[164:167],v1,s[36:39],s78 offen offset:0 sc1
buffer_load_dwordx4 v[168:171],v1,s[36:39],s78 offen offset:2048 sc1
s_add_u32 s78,s55,71307264
buffer_load_dwordx4 v[172:175],v1,s[36:39],s78 offen offset:0 sc1
buffer_load_dwordx4 v[176:179],v1,s[36:39],s78 offen offset:2048 sc1
s_add_u32 s78,s55,89128960
buffer_load_dwordx4 v[180:183],v1,s[36:39],s78 offen offset:0 sc1
buffer_load_dwordx4 v[184:187],v1,s[36:39],s78 offen offset:2048 sc1
s_add_u32 s78,s55,89133056
buffer_load_dwordx4 v[188:191],v1,s[36:39],s78 offen offset:0 sc1
buffer_load_dwordx4 v[192:195],v1,s[36:39],s78 offen offset:2048 sc1
buffer_load_dword v18,v6,s[36:39],s62 offen offset:0 sc1
s_cmp_eq_u32 s93,0
s_cbranch_scc1 Lr_h2_%=
s_mov_b64 s[40:41],%24
s_mov_b32 s42,7340032
s_mov_b32 s43,0x00020000
s_lshl_b32 s57,s44,11
L 0
s_barrier
s_add_u32 s55,s50,2097152
s_add_u32 m0,s57,73728
s_nop 0
buffer_load_dwordx4 v13,s[40:43],s55 offen offset:0  lds
s_add_u32 m0,s57,74752
s_nop 0
buffer_load_dwordx4 v14,s[40:43],s55 offen offset:0  lds
s_add_u32 m0,s57,81792
s_nop 0
buffer_load_dwordx4 v13,s[40:43],s55 offen offset:128  lds
s_add_u32 m0,s57,82816
s_nop 0
buffer_load_dwordx4 v14,s[40:43],s55 offen offset:128  lds
v_add_u32 v44,90112,v15
v_add_u32 v45,90112,v16
T a[160:161],44,0
T a[162:163],44,512
T a[164:165],45,0
T a[166:167],45,512
T a[168:169],44,4096
T a[170:171],44,4608
T a[172:173],45,4096
T a[174:175],45,4608
L 0
s_barrier
s_add_u32 s55,s50,2228224
s_add_u32 m0,s57,90112
s_nop 0
buffer_load_dwordx4 v13,s[40:43],s55 offen offset:0  lds
s_add_u32 m0,s57,91136
s_nop 0
buffer_load_dwordx4 v14,s[40:43],s55 offen offset:0  lds
s_add_u32 m0,s57,98176
s_nop 0
buffer_load_dwordx4 v13,s[40:43],s55 offen offset:128  lds
s_add_u32 m0,s57,99200
s_nop 0
buffer_load_dwordx4 v14,s[40:43],s55 offen offset:128  lds
v_add_u32 v44,106496,v15
v_add_u32 v45,106496,v16
T a[176:177],44,0
T a[178:179],44,512
T a[180:181],45,0
T a[182:183],45,512
T a[184:185],44,4096
T a[186:187],44,4608
T a[188:189],45,4096
T a[190:191],45,4608
L 0
s_barrier
s_add_u32 s55,s50,2359296
s_add_u32 m0,s57,106496
s_nop 0
buffer_load_dwordx4 v13,s[40:43],s55 offen offset:0  lds
s_add_u32 m0,s57,107520
s_nop 0
buffer_load_dwordx4 v14,s[40:43],s55 offen offset:0  lds
s_add_u32 m0,s57,114560
s_nop 0
buffer_load_dwordx4 v13,s[40:43],s55 offen offset:128  lds
s_add_u32 m0,s57,115584
)"
R"(s_nop 0
buffer_load_dwordx4 v14,s[40:43],s55 offen offset:128  lds
v_add_u32 v44,122880,v15
v_add_u32 v45,122880,v16
T a[192:193],44,0
T a[194:195],44,512
T a[196:197],45,0
T a[198:199],45,512
T a[200:201],44,4096
T a[202:203],44,4608
T a[204:205],45,4096
T a[206:207],45,4608
L 0
s_barrier
s_add_u32 s55,s50,2490368
s_add_u32 m0,s57,122880
s_nop 0
buffer_load_dwordx4 v13,s[40:43],s55 offen offset:0  lds
s_add_u32 m0,s57,123904
s_nop 0
buffer_load_dwordx4 v14,s[40:43],s55 offen offset:0  lds
s_add_u32 m0,s57,130944
s_nop 0
buffer_load_dwordx4 v13,s[40:43],s55 offen offset:128  lds
s_add_u32 m0,s57,131968
s_nop 0
buffer_load_dwordx4 v14,s[40:43],s55 offen offset:128  lds
v_add_u32 v44,139264,v15
v_add_u32 v45,139264,v16
T a[208:209],44,0
T a[210:211],44,512
T a[212:213],45,0
T a[214:215],45,512
T a[216:217],44,4096
T a[218:219],44,4608
T a[220:221],45,4096
T a[222:223],45,4608
L 0
s_barrier
s_add_u32 s55,s50,2621440
s_add_u32 m0,s57,139264
s_nop 0
buffer_load_dwordx4 v13,s[40:43],s55 offen offset:0  lds
s_add_u32 m0,s57,140288
s_nop 0
buffer_load_dwordx4 v14,s[40:43],s55 offen offset:0  lds
s_add_u32 m0,s57,147328
s_nop 0
buffer_load_dwordx4 v13,s[40:43],s55 offen offset:128  lds
s_add_u32 m0,s57,148352
s_nop 0
buffer_load_dwordx4 v14,s[40:43],s55 offen offset:128  lds
v_add_u32 v44,40960,v15
v_add_u32 v45,40960,v16
T a[224:225],44,0
T a[226:227],44,512
T a[228:229],45,0
T a[230:231],45,512
T a[232:233],44,4096
T a[234:235],44,4608
T a[236:237],45,4096
T a[238:239],45,4608
L 0
s_barrier
s_add_u32 s55,s50,2752512
s_add_u32 m0,s57,40960
s_nop 0
buffer_load_dwordx4 v13,s[40:43],s55 offen offset:0  lds
s_add_u32 m0,s57,41984
s_nop 0
buffer_load_dwordx4 v14,s[40:43],s55 offen offset:0  lds
s_add_u32 m0,s57,49024
s_nop 0
buffer_load_dwordx4 v13,s[40:43],s55 offen offset:128  lds
s_add_u32 m0,s57,50048
s_nop 0
buffer_load_dwordx4 v14,s[40:43],s55 offen offset:128  lds
v_add_u32 v44,57344,v15
v_add_u32 v45,57344,v16
T a[240:241],44,0
T a[242:243],44,512
T a[244:245],45,0
T a[246:247],45,512
T a[248:249],44,4096
T a[250:251],44,4608
T a[252:253],45,4096
T a[254:255],45,4608
L 0
s_barrier
s_waitcnt vmcnt(24)
Lr_h2_%=:
s_lshr_b32 s78,s45,1
s_and_b32 s79,s45,1
s_lshl_b32 s79,s79,7
s_lshl_b32 s57,s78,15
s_add_u32 s57,s57,s79
s_add_u32 s57,s57,12582912
s_add_u32 s58,s57,4096
s_lshl_b32 s59,s78,9
s_add_u32 s59,s59,s79
s_add_u32 s59,s59,107020288
s_lshl_b32 s60,s45,9
s_add_u32 s60,s60,s66
s_add_u32 s60,s60,131072
s_add_u32 s45,s45,s46
s_lshr_b32 s78,s45,1
s_and_b32 s79,s45,1
s_lshl_b32 s79,s79,7
s_lshl_b32 s55,s78,15
s_add_u32 s55,s55,s79
s_mul_i32 s56,s45,16384
s_add_u32 s56,s56,98566144
s_lshl_b32 s61,s78,11
s_add_u32 s61,s61,110587904
s_add_u32 s78,s45,s46
s_lshr_b32 s78,s78,1
s_lshl_b32 s62,s78,11
s_add_u32 s62,s62,110587904
s_waitcnt vmcnt(0)
Lr_it0_%=:
T v[68:69],10,0
T v[70:71],10,64
T v[72:73],10,1024
T v[74:75],10,1088
T v[76:77],10,2048
T v[78:79],10,2112
T v[80:81],10,3072
T v[82:83],10,3136
s_nop 0
v_cmp_eq_u32 vcc,s53,v18
s_nop 0
s_cmp_eq_u64 vcc,-1
s_cselect_b32 s78,s45,256
s_cmp_lt_u32 s78,256
s_cbranch_scc1 Lr_ta0_%=
buffer_load_dword v18,v6,s[36:39],s61 offen offset:0 sc1
v_mfma_f32_16x16x32_bf16 v[148:151],v[164:167],v[180:183],0
s_add_u32 m0,s65,16384
M 148,v[168:171],184
D 9,56,0
M 148,v[172:175],188
D 9,56,1024
M 148,v[176:179],192
L 4
D 9,56,2048
v_mfma_f32_16x16x32_bf16 v[40:43],v[164:167],v[164:167],0
D 9,56,3072
M 40,v[168:171],168
L 0
v_lshlrev_b32 v84,16,v68
v_and_b32 v85,0xffff0000,v68
M 40,v[172:175],172
v_lshlrev_b32 v86,16,v69
v_and_b32 v87,0xffff0000,v69
M 40,v[176:179],176
v_lshlrev_b32 v88,16,v70
v_and_b32 v89,0xffff0000,v70
M 40,v[180:183],180
v_lshlrev_b32 v90,16,v71
v_and_b32 v91,0xffff0000,v71
M 40,v[184:187],184
v_lshlrev_b32 v92,16,v72
v_and_b32 v93,0xffff0000,v72
M 40,v[188:191],188
v_lshlrev_b32 v94,16,v73
v_and_b32 v95,0xffff0000,v73
M 40,v[192:195],192
v_lshlrev_b32 v96,16,v74
v_and_b32 v97,0xffff0000,v74
v_mfma_f32_16x16x32_bf16 v[36:39],v[32:35],v[164:167],0
v_lshlrev_b32 v98,16,v75
v_and_b32 v99,0xffff0000,v75
M 36,v[32:35],168
v_lshlrev_b32 v100,16,v76
v_and_b32 v101,0xffff0000,v76
M 36,v[32:35],172
v_lshlrev_b32 v102,16,v77
v_and_b32 v103,0xffff0000,v77
M 36,v[32:35],176
v_lshlrev_b32 v104,16,v78
v_and_b32 v105,0xffff0000,v78
M 36,v[32:35],180
v_fma_f32 v40,2.0,v148,v40
v_fma_f32 v41,2.0,v149,v41
M 36,v[32:35],184
v_fma_f32 v42,2.0,v150,v42
v_fma_f32 v43,2.0,v151,v43
M 36,v[32:35],188
v_cndmask_b32 v45,v40,v41,s[70:71]
v_cndmask_b32 v46,v42,v43,s[70:71]
M 36,v[32:35],192
v_cndmask_b32 v45,v45,v46,s[72:73]
v_lshlrev_b32 v106,16,v79
v_mfma_f32_16x16x32_bf16 v[116:119],v[26:29],v[164:167],0
v_and_b32 v107,0xffff0000,v79
v_lshlrev_b32 v108,16,v80
M 116,v[26:29],180
v_and_b32 v109,0xffff0000,v80
v_lshlrev_b32 v110,16,v81
v_mfma_f32_16x16x32_bf16 v[120:123],v[24:27],v[164:167],0
v_and_b32 v111,0xffff0000,v81
v_lshlrev_b32 v112,16,v82
M 120,v[24:27],180
v_and_b32 v113,0xffff0000,v82
v_lshlrev_b32 v114,16,v83
v_mfma_f32_16x16x32_bf16 v[124:127],v[26:29],v[168:171],0
v_add_f32_dpp v44,v36,v36 row_ror:8 row_mask:0xf bank_mask:0xf
v_and_b32 v115,0xffff0000,v83
M 124,v[26:29],184
ds_write_b32 v2,v44 offset:0
v_mfma_f32_16x16x32_bf16 v[128:131],v[24:27],v[168:171],0
ds_write_b32 v0,v45 offset:0
M 128,v[24:27],184
v_mfma_f32_16x16x32_bf16 v[132:135],v[26:29],v[172:175],0
M 132,v[26:29],188
L 0
v_mfma_f32_16x16x32_bf16 v[136:139],v[24:27],v[172:175],0
s_barrier
R 52,3,0
R 56,3,16
M 136,v[24:27],188
R 60,3,32
v_mfma_f32_16x16x32_bf16 v[140:143],v[26:29],v[176:179],0
M 140,v[26:29],192
v_mfma_f32_16x16x32_bf16 v[144:147],v[24:27],v[176:179],0
M 144,v[24:27],192
L 0
v_pk_add_f32 v[52:53],v[52:53],v[54:55]
v_pk_add_f32 v[56:57],v[56:57],v[58:59]
v_pk_add_f32 v[60:61],v[60:61],v[62:63]
v_add_f32 v52,v52,v53
v_pk_add_f32 v[56:57],v[56:57],v[60:61]
v_mul_f32 v52,s68,v52
v_add_f32 v53,v56,v57
v_mul_f32 v53,s68,v53
v_fma_f32 v53,-v52,v52,v53
v_max_f32 v53,0,v53
s_waitcnt vmcnt(5)
buffer_store_dword v17,v5,s[36:39],s63 offen offset:0 sc0 sc1
s_mov_b32 s63,0x7fff0000
s_waitcnt vmcnt(5)
v_cmp_eq_u32 vcc,s53,v18
s_nop 0
s_cmp_eq_u64 vcc,-1
s_cselect_b32 s78,s45,256
s_cmp_lt_u32 s78,256
s_cbranch_scc1 Lr_tb0_%=
buffer_load_dword v18,v6,s[36:39],s61 offen offset:0 sc1
v_add_f32 v53,s69,v53
v_rsq_f32 v48,v53
s_nop 0
v_mul_f32_e64 v50,-v52,v48
v_mov_b32 v49,v48
v_mov_b32 v51,v50
v_pk_fma_f32 v[52:53],v[116:117],v[48:49],v[50:51]
v_pk_fma_f32 v[54:55],v[118:119],v[48:49],v[50:51]
v_pk_fma_f32 v[56:57],v[120:121],v[48:49],v[50:51]
v_pk_fma_f32 v[58:59],v[122:123],v[48:49],v[50:51]
v_pk_fma_f32 v[116:117],v[52:53],v[84:85],v[116:117]
v_pk_fma_f32 v[118:119],v[54:55],v[86:87],v[118:119]
v_pk_fma_f32 v[120:121],v[56:57],v[88:89],v[120:121]
v_pk_fma_f32 v[122:123],v[58:59],v[90:91],v[122:123]
v_pk_fma_f32 v[52:53],v[124:125],v[48:49],v[50:51]
v_pk_fma_f32 v[54:55],v[126:127],v[48:49],v[50:51]
v_pk_fma_f32 v[56:57],v[128:129],v[48:49],v[50:51]
v_pk_fma_f32 v[58:59],v[130:131],v[48:49],v[50:51]
v_pk_fma_f32 v[124:125],v[52:53],v[92:93],v[124:125]
v_pk_fma_f32 v[126:127],v[54:55],v[94:95],v[126:127]
v_pk_fma_f32 v[128:129],v[56:57],v[96:97],v[128:129]
v_pk_fma_f32 v[130:131],v[58:59],v[98:99],v[130:131]
v_pk_fma_f32 v[52:53],v[132:133],v[48:49],v[50:51]
v_pk_fma_f32 v[54:55],v[134:135],v[48:49],v[50:51]
v_pk_fma_f32 v[56:57],v[136:137],v[48:49],v[50:51]
v_pk_fma_f32 v[58:59],v[138:139],v[48:49],v[50:51]
v_pk_fma_f32 v[132:133],v[52:53],v[100:101],v[132:133]
v_pk_fma_f32 v[134:135],v[54:55],v[102:103],v[134:135]
v_pk_fma_f32 v[136:137],v[56:57],v[104:105],v[136:137]
v_pk_fma_f32 v[138:139],v[58:59],v[106:107],v[138:139]
v_pk_fma_f32 v[52:53],v[140:141],v[48:49],v[50:51]
v_pk_fma_f32 v[54:55],v[142:143],v[48:49],v[50:51]
v_pk_fma_f32 v[56:57],v[144:145],v[48:49],v[50:51]
v_pk_fma_f32 v[58:59],v[146:147],v[48:49],v[50:51]
v_pk_fma_f32 v[140:141],v[52:53],v[108:109],v[140:141]
v_pk_fma_f32 v[142:143],v[54:55],v[110:111],v[142:143]
v_pk_fma_f32 v[144:145],v[56:57],v[112:113],v[144:145]
v_pk_fma_f32 v[146:147],v[58:59],v[114:115],v[146:147]
v_cvt_pk_bf16_f32 v148,v116,v117
v_cvt_pk_bf16_f32 v149,v118,v119
v_cvt_pk_bf16_f32 v150,v120,v121
v_cvt_pk_bf16_f32 v151,v122,v123
v_cvt_pk_bf16_f32 v152,v124,v125
v_cvt_pk_bf16_f32 v153,v126,v127
v_mfma_f32_16x16x32_bf16 v[36:39],v[32:35],v[148:151],0
v_cvt_pk_bf16_f32 v154,v128,v129
v_cvt_pk_bf16_f32 v155,v130,v131
v_mfma_f32_16x16x32_bf16 v[40:43],v[148:151],v[148:151],0
v_cvt_pk_bf16_f32 v156,v132,v133
v_cvt_pk_bf16_f32 v157,v134,v135
M 36,v[32:35],152
v_cvt_pk_bf16_f32 v158,v136,v137
v_cvt_pk_bf16_f32 v159,v138,v139
M 40,v[152:155],152
v_cvt_pk_bf16_f32 v160,v140,v141
v_cvt_pk_bf16_f32 v161,v142,v143
M 36,v[32:35],156
v_cvt_pk_bf16_f32 v162,v144,v145
v_cvt_pk_bf16_f32 v163,v146,v147
M 40,v[156:159],156
buffer_store_dwordx4 v[148:151],v1,s[36:39],s57 offen offset:0 sc0 sc1
M 36,v[32:35],160
buffer_store_dwordx4 v[152:155],v1,s[36:39],s57 offen offset:2048 sc0 sc1
M 40,v[160:163],160
buffer_store_dwordx4 v[156:159],v1,s[36:39],s58 offen offset:0 sc0 sc1
s_nop 4
v_add_f32_dpp v44,v36,v36 row_ror:8 row_mask:0xf bank_mask:0xf
s_nop 0
v_cndmask_b32 v45,v40,v41,s[70:71]
v_cndmask_b32 v46,v42,v43,s[70:71]
ds_write_b32 v2,v44 offset:1024
v_cndmask_b32 v45,v45,v46,s[72:73]
ds_write_b32 v0,v45 offset:1024
buffer_store_dwordx4 v[160:163],v1,s[36:39],s58 offen offset:2048 sc0 sc1
L 0
s_barrier
R 52,3,1024
R 56,3,1040
R 60,3,1056
L 0
v_pk_add_f32 v[52:53],v[52:53],v[54:55]
v_pk_add_f32 v[56:57],v[56:57],v[58:59]
v_pk_add_f32 v[60:61],v[60:61],v[62:63]
v_add_f32 v52,v52,v53
v_pk_add_f32 v[56:57],v[56:57],v[60:61]
v_mul_f32 v52,s68,v52
v_add_f32 v53,v56,v57
v_mul_f32 v53,s68,v53
v_fma_f32 v53,-v52,v52,v53
v_max_f32 v53,0,v53
v_add_f32 v54,s69,v53
v_fma_f32 v55,s69,v54,v53
v_rsq_f32 v65,v54
v_rsq_f32 v54,v55
v_mul_f32_e64 v64,-v52,v65
v_mul_f32 v63,v55,v54
v_mul_f32 v61,s76,v54
v_mul_f32 v55,v54,v54
v_mul_f32 v62,s77,v55
v_mul_f32 v62,v62,v61
v_cndmask_b32 v60,v52,v64,s[74:75]
v_cndmask_b32 v61,v61,v65,s[74:75]
buffer_store_dwordx4 v[60:63],v4,s[36:39],s59 offen offset:0 sc0 sc1
s_mov_b32 s63,s60
s_waitcnt vmcnt(5)
v_cmp_eq_u32 vcc,s53,v18
s_nop 0
s_cmp_eq_u64 vcc,-1
s_cselect_b32 s78,s45,256
s_cmp_lt_u32 s78,256
s_cbranch_scc1 Lr_tc0_%=
s_waitcnt vmcnt(0)
buffer_store_dword v17,v5,s[36:39],s63 offen offset:0 sc0 sc1
s_mov_b32 s63,0x7fff0000
s_cmp_ge_u32 s45,256
s_cbranch_scc1 Lr_done_%=
Lr_bp0_%=:
buffer_load_dword v18,v6,s[36:39],s61 offen offset:0 sc1
s_add_u32 s82,s82,1
s_waitcnt vmcnt(0)
v_cmp_eq_u32 vcc,s53,v18
s_nop 0
s_cmp_eq_u64 vcc,-1
s_cbranch_scc1 Lr_bq0_%=
s_sleep 2
s_cmp_lt_u32 s82,20000
s_cbranch_scc1 Lr_bp0_%=
Lr_bq0_%=:
s_add_u32 s78,s55,71303168
buffer_load_dwordx4 v[196:199],v1,s[36:39],s78 offen offset:0 sc1
buffer_load_dwordx4 v[200:203],v1,s[36:39],s78 offen offset:2048 sc1
s_add_u32 s78,s55,71307264
buffer_load_dwordx4 v[204:207],v1,s[36:39],s78 offen offset:0 sc1
buffer_load_dwordx4 v[208:211],v1,s[36:39],s78 offen offset:2048 sc1
s_add_u32 s78,s55,89128960
buffer_load_dwordx4 v[212:215],v1,s[36:39],s78 offen offset:0 sc1
buffer_load_dwordx4 v[216:219],v1,s[36:39],s78 offen offset:2048 sc1
s_add_u32 s78,s55,89133056
buffer_load_dwordx4 v[220:223],v1,s[36:39],s78 offen offset:0 sc1
buffer_load_dwordx4 v[224:227],v1,s[36:39],s78 offen offset:2048 sc1
buffer_load_dword v18,v6,s[36:39],s62 offen offset:0 sc1
s_lshr_b32 s78,s45,1
s_and_b32 s79,s45,1
s_lshl_b32 s79,s79,7
s_lshl_b32 s57,s78,15
s_add_u32 s57,s57,s79
s_add_u32 s57,s57,12582912
s_add_u32 s58,s57,4096
s_lshl_b32 s59,s78,9
s_add_u32 s59,s59,s79
s_add_u32 s59,s59,107020288
s_lshl_b32 s60,s45,9
s_add_u32 s60,s60,s66
s_add_u32 s60,s60,131072
s_add_u32 s45,s45,s46
s_lshr_b32 s78,s45,1
s_and_b32 s79,s45,1
s_lshl_b32 s79,s79,7
s_lshl_b32 s55,s78,15
s_add_u32 s55,s55,s79
s_mul_i32 s56,s45,16384
s_add_u32 s56,s56,98566144
s_lshl_b32 s61,s78,11
s_add_u32 s61,s61,110587904
s_add_u32 s78,s45,s46
s_lshr_b32 s78,s78,1
s_lshl_b32 s62,s78,11
s_add_u32 s62,s62,110587904
s_waitcnt vmcnt(0)
s_branch Lr_it1_%=
Lr_ta0_%=:
v_mfma_f32_16x16x32_bf16 v[148:151],v[164:167],v[180:183],0
s_add_u32 s78,s55,71303168
s_add_u32 m0,s65,16384
M 148,v[168:171],184
buffer_load_dwordx4 v[196:199],v1,s[36:39],s78 offen offset:0 sc1
M 148,v[172:175],188
buffer_load_dwordx4 v[200:203],v1,s[36:39],s78 offen offset:2048 sc1
M 148,v[176:179],192
L 4
D 9,56,0
v_mfma_f32_16x16x32_bf16 v[40:43],v[164:167],v[164:167],0
D 9,56,1024
M 40,v[168:171],168
L 0
D 9,56,2048
M 40,v[172:175],172
D 9,56,3072
M 40,v[176:179],176
s_add_u32 s78,s55,71307264
v_lshlrev_b32 v84,16,v68
v_and_b32 v85,0xffff0000,v68
M 40,v[180:183],180
buffer_load_dwordx4 v[204:207],v1,s[36:39],s78 offen offset:0 sc1
M 40,v[184:187],184
buffer_load_dwordx4 v[208:211],v1,s[36:39],s78 offen offset:2048 sc1
M 40,v[188:191],188
s_add_u32 s78,s55,89128960
v_lshlrev_b32 v86,16,v69
v_and_b32 v87,0xffff0000,v69
M 40,v[192:195],192
buffer_load_dwordx4 v[212:215],v1,s[36:39],s78 offen offset:0 sc1
v_mfma_f32_16x16x32_bf16 v[36:39],v[32:35],v[164:167],0
buffer_load_dwordx4 v[216:219],v1,s[36:39],s78 offen offset:2048 sc1
M 36,v[32:35],168
s_add_u32 s78,s55,89133056
v_lshlrev_b32 v88,16,v70
v_and_b32 v89,0xffff0000,v70
M 36,v[32:35],172
buffer_load_dwordx4 v[220:223],v1,s[36:39],s78 offen offset:0 sc1
M 36,v[32:35],176
buffer_load_dwordx4 v[224:227],v1,s[36:39],s78 offen offset:2048 sc1
M 36,v[32:35],180
v_fma_f32 v40,2.0,v148,v40
v_fma_f32 v41,2.0,v149,v41
M 36,v[32:35],184
v_fma_f32 v42,2.0,v150,v42
v_fma_f32 v43,2.0,v151,v43
M 36,v[32:35],188
v_cndmask_b32 v45,v40,v41,s[70:71]
v_cndmask_b32 v46,v42,v43,s[70:71]
M 36,v[32:35],192
v_cndmask_b32 v45,v45,v46,s[72:73]
v_lshlrev_b32 v90,16,v71
v_mfma_f32_16x16x32_bf16 v[116:119],v[26:29],v[164:167],0
v_and_b32 v91,0xffff0000,v71
v_lshlrev_b32 v92,16,v72
M 116,v[26:29],180
v_and_b32 v93,0xffff0000,v72
v_lshlrev_b32 v94,16,v73
v_mfma_f32_16x16x32_bf16 v[120:123],v[24:27],v[164:167],0
v_and_b32 v95,0xffff0000,v73
v_lshlrev_b32 v96,16,v74
M 120,v[24:27],180
v_and_b32 v97,0xffff0000,v74
v_lshlrev_b32 v98,16,v75
v_mfma_f32_16x16x32_bf16 v[124:127],v[26:29],v[168:171],0
v_add_f32_dpp v44,v36,v36 row_ror:8 row_mask:0xf bank_mask:0xf
v_and_b32 v99,0xffff0000,v75
M 124,v[26:29],184
ds_write_b32 v2,v44 offset:0
v_mfma_f32_16x16x32_bf16 v[128:131],v[24:27],v[168:171],0
ds_write_b32 v0,v45 offset:0
M 128,v[24:27],184
v_lshlrev_b32 v100,16,v76
v_and_b32 v101,0xffff0000,v76
v_mfma_f32_16x16x32_bf16 v[132:135],v[26:29],v[172:175],0
v_lshlrev_b32 v102,16,v77
v_and_b32 v103,0xffff0000,v77
M 132,v[26:29],188
L 0
v_lshlrev_b32 v104,16,v78
)"
R"(v_and_b32 v105,0xffff0000,v78
v_mfma_f32_16x16x32_bf16 v[136:139],v[24:27],v[172:175],0
s_barrier
R 52,3,0
R 56,3,16
M 136,v[24:27],188
R 60,3,32
v_lshlrev_b32 v106,16,v79
v_mfma_f32_16x16x32_bf16 v[140:143],v[26:29],v[176:179],0
v_and_b32 v107,0xffff0000,v79
v_lshlrev_b32 v108,16,v80
M 140,v[26:29],192
v_and_b32 v109,0xffff0000,v80
v_lshlrev_b32 v110,16,v81
v_mfma_f32_16x16x32_bf16 v[144:147],v[24:27],v[176:179],0
v_and_b32 v111,0xffff0000,v81
v_lshlrev_b32 v112,16,v82
M 144,v[24:27],192
v_and_b32 v113,0xffff0000,v82
v_lshlrev_b32 v114,16,v83
v_and_b32 v115,0xffff0000,v83
L 0
v_pk_add_f32 v[52:53],v[52:53],v[54:55]
v_pk_add_f32 v[56:57],v[56:57],v[58:59]
v_pk_add_f32 v[60:61],v[60:61],v[62:63]
v_add_f32 v52,v52,v53
v_pk_add_f32 v[56:57],v[56:57],v[60:61]
v_mul_f32 v52,s68,v52
v_add_f32 v53,v56,v57
v_mul_f32 v53,s68,v53
v_fma_f32 v53,-v52,v52,v53
v_max_f32 v53,0,v53
buffer_load_dword v18,v6,s[36:39],s62 offen offset:0 sc1
v_add_f32 v53,s69,v53
v_rsq_f32 v48,v53
s_nop 0
v_mul_f32_e64 v50,-v52,v48
v_mov_b32 v49,v48
v_mov_b32 v51,v50
v_pk_fma_f32 v[52:53],v[116:117],v[48:49],v[50:51]
v_pk_fma_f32 v[54:55],v[118:119],v[48:49],v[50:51]
v_pk_fma_f32 v[56:57],v[120:121],v[48:49],v[50:51]
v_pk_fma_f32 v[58:59],v[122:123],v[48:49],v[50:51]
v_pk_fma_f32 v[116:117],v[52:53],v[84:85],v[116:117]
v_pk_fma_f32 v[118:119],v[54:55],v[86:87],v[118:119]
v_pk_fma_f32 v[120:121],v[56:57],v[88:89],v[120:121]
v_pk_fma_f32 v[122:123],v[58:59],v[90:91],v[122:123]
v_pk_fma_f32 v[52:53],v[124:125],v[48:49],v[50:51]
v_pk_fma_f32 v[54:55],v[126:127],v[48:49],v[50:51]
v_pk_fma_f32 v[56:57],v[128:129],v[48:49],v[50:51]
v_pk_fma_f32 v[58:59],v[130:131],v[48:49],v[50:51]
v_pk_fma_f32 v[124:125],v[52:53],v[92:93],v[124:125]
v_pk_fma_f32 v[126:127],v[54:55],v[94:95],v[126:127]
v_pk_fma_f32 v[128:129],v[56:57],v[96:97],v[128:129]
v_pk_fma_f32 v[130:131],v[58:59],v[98:99],v[130:131]
v_pk_fma_f32 v[52:53],v[132:133],v[48:49],v[50:51]
v_pk_fma_f32 v[54:55],v[134:135],v[48:49],v[50:51]
v_pk_fma_f32 v[56:57],v[136:137],v[48:49],v[50:51]
v_pk_fma_f32 v[58:59],v[138:139],v[48:49],v[50:51]
v_pk_fma_f32 v[132:133],v[52:53],v[100:101],v[132:133]
v_pk_fma_f32 v[134:135],v[54:55],v[102:103],v[134:135]
v_pk_fma_f32 v[136:137],v[56:57],v[104:105],v[136:137]
v_pk_fma_f32 v[138:139],v[58:59],v[106:107],v[138:139]
v_pk_fma_f32 v[52:53],v[140:141],v[48:49],v[50:51]
v_pk_fma_f32 v[54:55],v[142:143],v[48:49],v[50:51]
v_pk_fma_f32 v[56:57],v[144:145],v[48:49],v[50:51]
v_pk_fma_f32 v[58:59],v[146:147],v[48:49],v[50:51]
v_pk_fma_f32 v[140:141],v[52:53],v[108:109],v[140:141]
v_pk_fma_f32 v[142:143],v[54:55],v[110:111],v[142:143]
v_pk_fma_f32 v[144:145],v[56:57],v[112:113],v[144:145]
v_pk_fma_f32 v[146:147],v[58:59],v[114:115],v[146:147]
v_cvt_pk_bf16_f32 v148,v116,v117
v_cvt_pk_bf16_f32 v149,v118,v119
v_cvt_pk_bf16_f32 v150,v120,v121
v_cvt_pk_bf16_f32 v151,v122,v123
v_cvt_pk_bf16_f32 v152,v124,v125
v_cvt_pk_bf16_f32 v153,v126,v127
s_waitcnt vmcnt(13)
buffer_store_dword v17,v5,s[36:39],s63 offen offset:0 sc0 sc1
s_mov_b32 s63,0x7fff0000
v_mfma_f32_16x16x32_bf16 v[36:39],v[32:35],v[148:151],0
v_cvt_pk_bf16_f32 v154,v128,v129
v_cvt_pk_bf16_f32 v155,v130,v131
v_mfma_f32_16x16x32_bf16 v[40:43],v[148:151],v[148:151],0
v_cvt_pk_bf16_f32 v156,v132,v133
v_cvt_pk_bf16_f32 v157,v134,v135
M 36,v[32:35],152
v_cvt_pk_bf16_f32 v158,v136,v137
v_cvt_pk_bf16_f32 v159,v138,v139
M 40,v[152:155],152
v_cvt_pk_bf16_f32 v160,v140,v141
v_cvt_pk_bf16_f32 v161,v142,v143
M 36,v[32:35],156
v_cvt_pk_bf16_f32 v162,v144,v145
v_cvt_pk_bf16_f32 v163,v146,v147
M 40,v[156:159],156
buffer_store_dwordx4 v[148:151],v1,s[36:39],s57 offen offset:0 sc0 sc1
M 36,v[32:35],160
buffer_store_dwordx4 v[152:155],v1,s[36:39],s57 offen offset:2048 sc0 sc1
M 40,v[160:163],160
buffer_store_dwordx4 v[156:159],v1,s[36:39],s58 offen offset:0 sc0 sc1
s_nop 4
v_add_f32_dpp v44,v36,v36 row_ror:8 row_mask:0xf bank_mask:0xf
s_nop 0
v_cndmask_b32 v45,v40,v41,s[70:71]
v_cndmask_b32 v46,v42,v43,s[70:71]
ds_write_b32 v2,v44 offset:1024
v_cndmask_b32 v45,v45,v46,s[72:73]
ds_write_b32 v0,v45 offset:1024
buffer_store_dwordx4 v[160:163],v1,s[36:39],s58 offen offset:2048 sc0 sc1
L 0
s_barrier
R 52,3,1024
R 56,3,1040
R 60,3,1056
L 0
v_pk_add_f32 v[52:53],v[52:53],v[54:55]
v_pk_add_f32 v[56:57],v[56:57],v[58:59]
v_pk_add_f32 v[60:61],v[60:61],v[62:63]
v_add_f32 v52,v52,v53
v_pk_add_f32 v[56:57],v[56:57],v[60:61]
v_mul_f32 v52,s68,v52
v_add_f32 v53,v56,v57
v_mul_f32 v53,s68,v53
v_fma_f32 v53,-v52,v52,v53
v_max_f32 v53,0,v53
v_add_f32 v54,s69,v53
v_fma_f32 v55,s69,v54,v53
v_rsq_f32 v65,v54
v_rsq_f32 v54,v55
v_mul_f32_e64 v64,-v52,v65
v_mul_f32 v63,v55,v54
v_mul_f32 v61,s76,v54
v_mul_f32 v55,v54,v54
v_mul_f32 v62,s77,v55
v_mul_f32 v62,v62,v61
v_cndmask_b32 v60,v52,v64,s[74:75]
v_cndmask_b32 v61,v61,v65,s[74:75]
buffer_store_dwordx4 v[60:63],v4,s[36:39],s59 offen offset:0 sc0 sc1
s_mov_b32 s63,s60
s_lshr_b32 s78,s45,1
s_and_b32 s79,s45,1
s_lshl_b32 s79,s79,7
s_lshl_b32 s57,s78,15
s_add_u32 s57,s57,s79
s_add_u32 s57,s57,12582912
s_add_u32 s58,s57,4096
s_lshl_b32 s59,s78,9
s_add_u32 s59,s59,s79
s_add_u32 s59,s59,107020288
s_lshl_b32 s60,s45,9
s_add_u32 s60,s60,s66
s_add_u32 s60,s60,131072
s_add_u32 s45,s45,s46
s_lshr_b32 s78,s45,1
s_and_b32 s79,s45,1
s_lshl_b32 s79,s79,7
s_lshl_b32 s55,s78,15
s_add_u32 s55,s55,s79
s_mul_i32 s56,s45,16384
s_add_u32 s56,s56,98566144
s_lshl_b32 s61,s78,11
s_add_u32 s61,s61,110587904
s_add_u32 s78,s45,s46
s_lshr_b32 s78,s78,1
s_lshl_b32 s62,s78,11
s_add_u32 s62,s62,110587904
s_waitcnt vmcnt(6)
s_branch Lr_it1_%=
Lr_tb0_%=:
v_add_f32 v53,s69,v53
v_rsq_f32 v48,v53
s_nop 0
v_mul_f32_e64 v50,-v52,v48
v_mov_b32 v49,v48
v_mov_b32 v51,v50
v_pk_fma_f32 v[52:53],v[116:117],v[48:49],v[50:51]
v_pk_fma_f32 v[54:55],v[118:119],v[48:49],v[50:51]
v_pk_fma_f32 v[56:57],v[120:121],v[48:49],v[50:51]
v_pk_fma_f32 v[58:59],v[122:123],v[48:49],v[50:51]
s_add_u32 s78,s55,71303168
buffer_load_dwordx4 v[196:199],v1,s[36:39],s78 offen offset:0 sc1
buffer_load_dwordx4 v[200:203],v1,s[36:39],s78 offen offset:2048 sc1
s_add_u32 s78,s55,71307264
buffer_load_dwordx4 v[204:207],v1,s[36:39],s78 offen offset:0 sc1
buffer_load_dwordx4 v[208:211],v1,s[36:39],s78 offen offset:2048 sc1
v_pk_fma_f32 v[116:117],v[52:53],v[84:85],v[116:117]
v_pk_fma_f32 v[118:119],v[54:55],v[86:87],v[118:119]
v_pk_fma_f32 v[120:121],v[56:57],v[88:89],v[120:121]
v_pk_fma_f32 v[122:123],v[58:59],v[90:91],v[122:123]
v_pk_fma_f32 v[52:53],v[124:125],v[48:49],v[50:51]
v_pk_fma_f32 v[54:55],v[126:127],v[48:49],v[50:51]
v_pk_fma_f32 v[56:57],v[128:129],v[48:49],v[50:51]
v_pk_fma_f32 v[58:59],v[130:131],v[48:49],v[50:51]
s_add_u32 s78,s55,89128960
buffer_load_dwordx4 v[212:215],v1,s[36:39],s78 offen offset:0 sc1
buffer_load_dwordx4 v[216:219],v1,s[36:39],s78 offen offset:2048 sc1
s_add_u32 s78,s55,89133056
buffer_load_dwordx4 v[220:223],v1,s[36:39],s78 offen offset:0 sc1
buffer_load_dwordx4 v[224:227],v1,s[36:39],s78 offen offset:2048 sc1
v_pk_fma_f32 v[124:125],v[52:53],v[92:93],v[124:125]
v_pk_fma_f32 v[126:127],v[54:55],v[94:95],v[126:127]
v_pk_fma_f32 v[128:129],v[56:57],v[96:97],v[128:129]
v_pk_fma_f32 v[130:131],v[58:59],v[98:99],v[130:131]
v_pk_fma_f32 v[52:53],v[132:133],v[48:49],v[50:51]
v_pk_fma_f32 v[54:55],v[134:135],v[48:49],v[50:51]
v_pk_fma_f32 v[56:57],v[136:137],v[48:49],v[50:51]
v_pk_fma_f32 v[58:59],v[138:139],v[48:49],v[50:51]
v_pk_fma_f32 v[132:133],v[52:53],v[100:101],v[132:133]
v_pk_fma_f32 v[134:135],v[54:55],v[102:103],v[134:135]
v_pk_fma_f32 v[136:137],v[56:57],v[104:105],v[136:137]
v_pk_fma_f32 v[138:139],v[58:59],v[106:107],v[138:139]
v_pk_fma_f32 v[52:53],v[140:141],v[48:49],v[50:51]
v_pk_fma_f32 v[54:55],v[142:143],v[48:49],v[50:51]
v_pk_fma_f32 v[56:57],v[144:145],v[48:49],v[50:51]
v_pk_fma_f32 v[58:59],v[146:147],v[48:49],v[50:51]
v_pk_fma_f32 v[140:141],v[52:53],v[108:109],v[140:141]
v_pk_fma_f32 v[142:143],v[54:55],v[110:111],v[142:143]
v_pk_fma_f32 v[144:145],v[56:57],v[112:113],v[144:145]
v_pk_fma_f32 v[146:147],v[58:59],v[114:115],v[146:147]
buffer_load_dword v18,v6,s[36:39],s62 offen offset:0 sc1
v_cvt_pk_bf16_f32 v148,v116,v117
v_cvt_pk_bf16_f32 v149,v118,v119
v_cvt_pk_bf16_f32 v150,v120,v121
v_cvt_pk_bf16_f32 v151,v122,v123
v_cvt_pk_bf16_f32 v152,v124,v125
v_cvt_pk_bf16_f32 v153,v126,v127
v_mfma_f32_16x16x32_bf16 v[36:39],v[32:35],v[148:151],0
v_cvt_pk_bf16_f32 v154,v128,v129
v_cvt_pk_bf16_f32 v155,v130,v131
v_mfma_f32_16x16x32_bf16 v[40:43],v[148:151],v[148:151],0
v_cvt_pk_bf16_f32 v156,v132,v133
v_cvt_pk_bf16_f32 v157,v134,v135
M 36,v[32:35],152
v_cvt_pk_bf16_f32 v158,v136,v137
v_cvt_pk_bf16_f32 v159,v138,v139
M 40,v[152:155],152
v_cvt_pk_bf16_f32 v160,v140,v141
v_cvt_pk_bf16_f32 v161,v142,v143
M 36,v[32:35],156
v_cvt_pk_bf16_f32 v162,v144,v145
v_cvt_pk_bf16_f32 v163,v146,v147
M 40,v[156:159],156
buffer_store_dwordx4 v[148:151],v1,s[36:39],s57 offen offset:0 sc0 sc1
M 36,v[32:35],160
buffer_store_dwordx4 v[152:155],v1,s[36:39],s57 offen offset:2048 sc0 sc1
M 40,v[160:163],160
buffer_store_dwordx4 v[156:159],v1,s[36:39],s58 offen offset:0 sc0 sc1
s_nop 4
v_add_f32_dpp v44,v36,v36 row_ror:8 row_mask:0xf bank_mask:0xf
s_nop 0
v_cndmask_b32 v45,v40,v41,s[70:71]
v_cndmask_b32 v46,v42,v43,s[70:71]
ds_write_b32 v2,v44 offset:1024
v_cndmask_b32 v45,v45,v46,s[72:73]
ds_write_b32 v0,v45 offset:1024
buffer_store_dwordx4 v[160:163],v1,s[36:39],s58 offen offset:2048 sc0 sc1
L 0
s_barrier
R 52,3,1024
R 56,3,1040
R 60,3,1056
L 0
v_pk_add_f32 v[52:53],v[52:53],v[54:55]
v_pk_add_f32 v[56:57],v[56:57],v[58:59]
v_pk_add_f32 v[60:61],v[60:61],v[62:63]
v_add_f32 v52,v52,v53
v_pk_add_f32 v[56:57],v[56:57],v[60:61]
v_mul_f32 v52,s68,v52
v_add_f32 v53,v56,v57
v_mul_f32 v53,s68,v53
v_fma_f32 v53,-v52,v52,v53
v_max_f32 v53,0,v53
v_add_f32 v54,s69,v53
v_fma_f32 v55,s69,v54,v53
v_rsq_f32 v65,v54
v_rsq_f32 v54,v55
v_mul_f32_e64 v64,-v52,v65
v_mul_f32 v63,v55,v54
v_mul_f32 v61,s76,v54
v_mul_f32 v55,v54,v54
v_mul_f32 v62,s77,v55
v_mul_f32 v62,v62,v61
v_cndmask_b32 v60,v52,v64,s[74:75]
v_cndmask_b32 v61,v61,v65,s[74:75]
buffer_store_dwordx4 v[60:63],v4,s[36:39],s59 offen offset:0 sc0 sc1
s_mov_b32 s63,s60
s_lshr_b32 s78,s45,1
s_and_b32 s79,s45,1
s_lshl_b32 s79,s79,7
s_lshl_b32 s57,s78,15
s_add_u32 s57,s57,s79
s_add_u32 s57,s57,12582912
s_add_u32 s58,s57,4096
s_lshl_b32 s59,s78,9
s_add_u32 s59,s59,s79
s_add_u32 s59,s59,107020288
s_lshl_b32 s60,s45,9
s_add_u32 s60,s60,s66
s_add_u32 s60,s60,131072
s_add_u32 s45,s45,s46
s_lshr_b32 s78,s45,1
s_and_b32 s79,s45,1
s_lshl_b32 s79,s79,7
s_lshl_b32 s55,s78,15
s_add_u32 s55,s55,s79
s_mul_i32 s56,s45,16384
s_add_u32 s56,s56,98566144
s_lshl_b32 s61,s78,11
s_add_u32 s61,s61,110587904
s_add_u32 s78,s45,s46
s_lshr_b32 s78,s78,1
s_lshl_b32 s62,s78,11
s_add_u32 s62,s62,110587904
s_waitcnt vmcnt(5)
s_branch Lr_it1_%=
Lr_tc0_%=:
s_add_u32 s78,s55,71303168
buffer_load_dwordx4 v[196:199],v1,s[36:39],s78 offen offset:0 sc1
buffer_load_dwordx4 v[200:203],v1,s[36:39],s78 offen offset:2048 sc1
s_add_u32 s78,s55,71307264
buffer_load_dwordx4 v[204:207],v1,s[36:39],s78 offen offset:0 sc1
buffer_load_dwordx4 v[208:211],v1,s[36:39],s78 offen offset:2048 sc1
s_add_u32 s78,s55,89128960
buffer_load_dwordx4 v[212:215],v1,s[36:39],s78 offen offset:0 sc1
buffer_load_dwordx4 v[216:219],v1,s[36:39],s78 offen offset:2048 sc1
s_add_u32 s78,s55,89133056
buffer_load_dwordx4 v[220:223],v1,s[36:39],s78 offen offset:0 sc1
buffer_load_dwordx4 v[224:227],v1,s[36:39],s78 offen offset:2048 sc1
buffer_load_dword v18,v6,s[36:39],s62 offen offset:0 sc1
s_waitcnt vmcnt(9)
buffer_store_dword v17,v5,s[36:39],s63 offen offset:0 sc0 sc1
s_mov_b32 s63,0x7fff0000
s_lshr_b32 s78,s45,1
s_and_b32 s79,s45,1
s_lshl_b32 s79,s79,7
s_lshl_b32 s57,s78,15
s_add_u32 s57,s57,s79
s_add_u32 s57,s57,12582912
s_add_u32 s58,s57,4096
s_lshl_b32 s59,s78,9
s_add_u32 s59,s59,s79
s_add_u32 s59,s59,107020288
s_lshl_b32 s60,s45,9
s_add_u32 s60,s60,s66
s_add_u32 s60,s60,131072
s_add_u32 s45,s45,s46
s_lshr_b32 s78,s45,1
s_and_b32 s79,s45,1
s_lshl_b32 s79,s79,7
s_lshl_b32 s55,s78,15
s_add_u32 s55,s55,s79
s_mul_i32 s56,s45,16384
s_add_u32 s56,s56,98566144
s_lshl_b32 s61,s78,11
s_add_u32 s61,s61,110587904
s_add_u32 s78,s45,s46
s_lshr_b32 s78,s78,1
s_lshl_b32 s62,s78,11
s_add_u32 s62,s62,110587904
s_waitcnt vmcnt(1)
s_branch Lr_it1_%=
Lr_it1_%=:
T v[68:69],10,16384
T v[70:71],10,16448
T v[72:73],10,17408
T v[74:75],10,17472
T v[76:77],10,18432
T v[78:79],10,18496
T v[80:81],10,19456
T v[82:83],10,19520
s_nop 0
v_cmp_eq_u32 vcc,s53,v18
s_nop 0
s_cmp_eq_u64 vcc,-1
s_cselect_b32 s78,s45,256
s_cmp_lt_u32 s78,256
s_cbranch_scc1 Lr_ta1_%=
buffer_load_dword v18,v6,s[36:39],s61 offen offset:0 sc1
v_mfma_f32_16x16x32_bf16 v[148:151],v[196:199],v[212:215],0
s_add_u32 m0,s65,0
M 148,v[200:203],216
D 9,56,0
M 148,v[204:207],220
D 9,56,1024
M 148,v[208:211],224
L 4
D 9,56,2048
v_mfma_f32_16x16x32_bf16 v[40:43],v[196:199],v[196:199],0
D 9,56,3072
M 40,v[200:203],200
L 0
v_lshlrev_b32 v84,16,v68
v_and_b32 v85,0xffff0000,v68
M 40,v[204:207],204
v_lshlrev_b32 v86,16,v69
v_and_b32 v87,0xffff0000,v69
M 40,v[208:211],208
v_lshlrev_b32 v88,16,v70
v_and_b32 v89,0xffff0000,v70
M 40,v[212:215],212
v_lshlrev_b32 v90,16,v71
v_and_b32 v91,0xffff0000,v71
M 40,v[216:219],216
v_lshlrev_b32 v92,16,v72
v_and_b32 v93,0xffff0000,v72
M 40,v[220:223],220
v_lshlrev_b32 v94,16,v73
v_and_b32 v95,0xffff0000,v73
M 40,v[224:227],224
v_lshlrev_b32 v96,16,v74
v_and_b32 v97,0xffff0000,v74
v_mfma_f32_16x16x32_bf16 v[36:39],v[32:35],v[196:199],0
v_lshlrev_b32 v98,16,v75
v_and_b32 v99,0xffff0000,v75
M 36,v[32:35],200
v_lshlrev_b32 v100,16,v76
v_and_b32 v101,0xffff0000,v76
M 36,v[32:35],204
v_lshlrev_b32 v102,16,v77
v_and_b32 v103,0xffff0000,v77
M 36,v[32:35],208
v_lshlrev_b32 v104,16,v78
v_and_b32 v105,0xffff0000,v78
M 36,v[32:35],212
v_fma_f32 v40,2.0,v148,v40
v_fma_f32 v41,2.0,v149,v41
M 36,v[32:35],216
v_fma_f32 v42,2.0,v150,v42
v_fma_f32 v43,2.0,v151,v43
M 36,v[32:35],220
v_cndmask_b32 v45,v40,v41,s[70:71]
v_cndmask_b32 v46,v42,v43,s[70:71]
M 36,v[32:35],224
v_cndmask_b32 v45,v45,v46,s[72:73]
v_lshlrev_b32 v106,16,v79
v_mfma_f32_16x16x32_bf16 v[116:119],v[26:29],v[196:199],0
v_and_b32 v107,0xffff0000,v79
v_lshlrev_b32 v108,16,v80
M 116,v[26:29],212
v_and_b32 v109,0xffff0000,v80
v_lshlrev_b32 v110,16,v81
v_mfma_f32_16x16x32_bf16 v[120:123],v[24:27],v[196:199],0
v_and_b32 v111,0xffff0000,v81
v_lshlrev_b32 v112,16,v82
M 120,v[24:27],212
v_and_b32 v113,0xffff0000,v82
v_lshlrev_b32 v114,16,v83
v_mfma_f32_16x16x32_bf16 v[124:127],v[26:29],v[200:203],0
v_add_f32_dpp v44,v36,v36 row_ror:8 row_mask:0xf bank_mask:0xf
v_and_b32 v115,0xffff0000,v83
M 124,v[26:29],216
ds_write_b32 v2,v44 offset:0
v_mfma_f32_16x16x32_bf16 v[128:131],v[24:27],v[200:203],0
ds_write_b32 v0,v45 offset:0
M 128,v[24:27],216
v_mfma_f32_16x16x32_bf16 v[132:135],v[26:29],v[204:207],0
M 132,v[26:29],220
L 0
v_mfma_f32_16x16x32_bf16 v[136:139],v[24:27],v[204:207],0
s_barrier
R 52,3,0
R 56,3,16
M 136,v[24:27],220
R 60,3,32
v_mfma_f32_16x16x32_bf16 v[140:143],v[26:29],v[208:211],0
M 140,v[26:29],224
v_mfma_f32_16x16x32_bf16 v[144:147],v[24:27],v[208:211],0
M 144,v[24:27],224
L 0
v_pk_add_f32 v[52:53],v[52:53],v[54:55]
v_pk_add_f32 v[56:57],v[56:57],v[58:59]
v_pk_add_f32 v[60:61],v[60:61],v[62:63]
v_add_f32 v52,v52,v53
v_pk_add_f32 v[56:57],v[56:57],v[60:61]
v_mul_f32 v52,s68,v52
v_add_f32 v53,v56,v57
v_mul_f32 v53,s68,v53
v_fma_f32 v53,-v52,v52,v53
v_max_f32 v53,0,v53
s_waitcnt vmcnt(5)
buffer_store_dword v17,v5,s[36:39],s63 offen offset:0 sc0 sc1
s_mov_b32 s63,0x7fff0000
s_waitcnt vmcnt(5)
v_cmp_eq_u32 vcc,s53,v18
s_nop 0
s_cmp_eq_u64 vcc,-1
s_cselect_b32 s78,s45,256
s_cmp_lt_u32 s78,256
s_cbranch_scc1 Lr_tb1_%=
buffer_load_dword v18,v6,s[36:39],s61 offen offset:0 sc1
v_add_f32 v53,s69,v53
v_rsq_f32 v48,v53
s_nop 0
v_mul_f32_e64 v50,-v52,v48
v_mov_b32 v49,v48
v_mov_b32 v51,v50
v_pk_fma_f32 v[52:53],v[116:117],v[48:49],v[50:51]
v_pk_fma_f32 v[54:55],v[118:119],v[48:49],v[50:51]
v_pk_fma_f32 v[56:57],v[120:121],v[48:49],v[50:51]
)"
R"(v_pk_fma_f32 v[58:59],v[122:123],v[48:49],v[50:51]
v_pk_fma_f32 v[116:117],v[52:53],v[84:85],v[116:117]
v_pk_fma_f32 v[118:119],v[54:55],v[86:87],v[118:119]
v_pk_fma_f32 v[120:121],v[56:57],v[88:89],v[120:121]
v_pk_fma_f32 v[122:123],v[58:59],v[90:91],v[122:123]
v_pk_fma_f32 v[52:53],v[124:125],v[48:49],v[50:51]
v_pk_fma_f32 v[54:55],v[126:127],v[48:49],v[50:51]
v_pk_fma_f32 v[56:57],v[128:129],v[48:49],v[50:51]
v_pk_fma_f32 v[58:59],v[130:131],v[48:49],v[50:51]
v_pk_fma_f32 v[124:125],v[52:53],v[92:93],v[124:125]
v_pk_fma_f32 v[126:127],v[54:55],v[94:95],v[126:127]
v_pk_fma_f32 v[128:129],v[56:57],v[96:97],v[128:129]
v_pk_fma_f32 v[130:131],v[58:59],v[98:99],v[130:131]
v_pk_fma_f32 v[52:53],v[132:133],v[48:49],v[50:51]
v_pk_fma_f32 v[54:55],v[134:135],v[48:49],v[50:51]
v_pk_fma_f32 v[56:57],v[136:137],v[48:49],v[50:51]
v_pk_fma_f32 v[58:59],v[138:139],v[48:49],v[50:51]
v_pk_fma_f32 v[132:133],v[52:53],v[100:101],v[132:133]
v_pk_fma_f32 v[134:135],v[54:55],v[102:103],v[134:135]
v_pk_fma_f32 v[136:137],v[56:57],v[104:105],v[136:137]
v_pk_fma_f32 v[138:139],v[58:59],v[106:107],v[138:139]
v_pk_fma_f32 v[52:53],v[140:141],v[48:49],v[50:51]
v_pk_fma_f32 v[54:55],v[142:143],v[48:49],v[50:51]
v_pk_fma_f32 v[56:57],v[144:145],v[48:49],v[50:51]
v_pk_fma_f32 v[58:59],v[146:147],v[48:49],v[50:51]
v_pk_fma_f32 v[140:141],v[52:53],v[108:109],v[140:141]
v_pk_fma_f32 v[142:143],v[54:55],v[110:111],v[142:143]
v_pk_fma_f32 v[144:145],v[56:57],v[112:113],v[144:145]
v_pk_fma_f32 v[146:147],v[58:59],v[114:115],v[146:147]
v_cvt_pk_bf16_f32 v148,v116,v117
v_cvt_pk_bf16_f32 v149,v118,v119
v_cvt_pk_bf16_f32 v150,v120,v121
v_cvt_pk_bf16_f32 v151,v122,v123
v_cvt_pk_bf16_f32 v152,v124,v125
v_cvt_pk_bf16_f32 v153,v126,v127
v_mfma_f32_16x16x32_bf16 v[36:39],v[32:35],v[148:151],0
v_cvt_pk_bf16_f32 v154,v128,v129
v_cvt_pk_bf16_f32 v155,v130,v131
v_mfma_f32_16x16x32_bf16 v[40:43],v[148:151],v[148:151],0
v_cvt_pk_bf16_f32 v156,v132,v133
v_cvt_pk_bf16_f32 v157,v134,v135
M 36,v[32:35],152
v_cvt_pk_bf16_f32 v158,v136,v137
v_cvt_pk_bf16_f32 v159,v138,v139
M 40,v[152:155],152
v_cvt_pk_bf16_f32 v160,v140,v141
v_cvt_pk_bf16_f32 v161,v142,v143
M 36,v[32:35],156
v_cvt_pk_bf16_f32 v162,v144,v145
v_cvt_pk_bf16_f32 v163,v146,v147
M 40,v[156:159],156
buffer_store_dwordx4 v[148:151],v1,s[36:39],s57 offen offset:0 sc0 sc1
M 36,v[32:35],160
buffer_store_dwordx4 v[152:155],v1,s[36:39],s57 offen offset:2048 sc0 sc1
M 40,v[160:163],160
buffer_store_dwordx4 v[156:159],v1,s[36:39],s58 offen offset:0 sc0 sc1
s_nop 4
v_add_f32_dpp v44,v36,v36 row_ror:8 row_mask:0xf bank_mask:0xf
s_nop 0
v_cndmask_b32 v45,v40,v41,s[70:71]
v_cndmask_b32 v46,v42,v43,s[70:71]
ds_write_b32 v2,v44 offset:1024
v_cndmask_b32 v45,v45,v46,s[72:73]
ds_write_b32 v0,v45 offset:1024
buffer_store_dwordx4 v[160:163],v1,s[36:39],s58 offen offset:2048 sc0 sc1
L 0
s_barrier
R 52,3,1024
R 56,3,1040
R 60,3,1056
L 0
v_pk_add_f32 v[52:53],v[52:53],v[54:55]
v_pk_add_f32 v[56:57],v[56:57],v[58:59]
v_pk_add_f32 v[60:61],v[60:61],v[62:63]
v_add_f32 v52,v52,v53
v_pk_add_f32 v[56:57],v[56:57],v[60:61]
v_mul_f32 v52,s68,v52
v_add_f32 v53,v56,v57
v_mul_f32 v53,s68,v53
v_fma_f32 v53,-v52,v52,v53
v_max_f32 v53,0,v53
v_add_f32 v54,s69,v53
v_fma_f32 v55,s69,v54,v53
v_rsq_f32 v65,v54
v_rsq_f32 v54,v55
v_mul_f32_e64 v64,-v52,v65
v_mul_f32 v63,v55,v54
v_mul_f32 v61,s76,v54
v_mul_f32 v55,v54,v54
v_mul_f32 v62,s77,v55
v_mul_f32 v62,v62,v61
v_cndmask_b32 v60,v52,v64,s[74:75]
v_cndmask_b32 v61,v61,v65,s[74:75]
buffer_store_dwordx4 v[60:63],v4,s[36:39],s59 offen offset:0 sc0 sc1
s_mov_b32 s63,s60
s_waitcnt vmcnt(5)
v_cmp_eq_u32 vcc,s53,v18
s_nop 0
s_cmp_eq_u64 vcc,-1
s_cselect_b32 s78,s45,256
s_cmp_lt_u32 s78,256
s_cbranch_scc1 Lr_tc1_%=
s_waitcnt vmcnt(0)
buffer_store_dword v17,v5,s[36:39],s63 offen offset:0 sc0 sc1
s_mov_b32 s63,0x7fff0000
s_cmp_ge_u32 s45,256
s_cbranch_scc1 Lr_done_%=
Lr_bp1_%=:
buffer_load_dword v18,v6,s[36:39],s61 offen offset:0 sc1
s_add_u32 s82,s82,1
s_waitcnt vmcnt(0)
v_cmp_eq_u32 vcc,s53,v18
s_nop 0
s_cmp_eq_u64 vcc,-1
s_cbranch_scc1 Lr_bq1_%=
s_sleep 2
s_cmp_lt_u32 s82,20000
s_cbranch_scc1 Lr_bp1_%=
Lr_bq1_%=:
s_add_u32 s78,s55,71303168
buffer_load_dwordx4 v[164:167],v1,s[36:39],s78 offen offset:0 sc1
buffer_load_dwordx4 v[168:171],v1,s[36:39],s78 offen offset:2048 sc1
s_add_u32 s78,s55,71307264
buffer_load_dwordx4 v[172:175],v1,s[36:39],s78 offen offset:0 sc1
buffer_load_dwordx4 v[176:179],v1,s[36:39],s78 offen offset:2048 sc1
s_add_u32 s78,s55,89128960
buffer_load_dwordx4 v[180:183],v1,s[36:39],s78 offen offset:0 sc1
buffer_load_dwordx4 v[184:187],v1,s[36:39],s78 offen offset:2048 sc1
s_add_u32 s78,s55,89133056
buffer_load_dwordx4 v[188:191],v1,s[36:39],s78 offen offset:0 sc1
buffer_load_dwordx4 v[192:195],v1,s[36:39],s78 offen offset:2048 sc1
buffer_load_dword v18,v6,s[36:39],s62 offen offset:0 sc1
s_lshr_b32 s78,s45,1
s_and_b32 s79,s45,1
s_lshl_b32 s79,s79,7
s_lshl_b32 s57,s78,15
s_add_u32 s57,s57,s79
s_add_u32 s57,s57,12582912
s_add_u32 s58,s57,4096
s_lshl_b32 s59,s78,9
s_add_u32 s59,s59,s79
s_add_u32 s59,s59,107020288
s_lshl_b32 s60,s45,9
s_add_u32 s60,s60,s66
s_add_u32 s60,s60,131072
s_add_u32 s45,s45,s46
s_lshr_b32 s78,s45,1
s_and_b32 s79,s45,1
s_lshl_b32 s79,s79,7
s_lshl_b32 s55,s78,15
s_add_u32 s55,s55,s79
s_mul_i32 s56,s45,16384
s_add_u32 s56,s56,98566144
s_lshl_b32 s61,s78,11
s_add_u32 s61,s61,110587904
s_add_u32 s78,s45,s46
s_lshr_b32 s78,s78,1
s_lshl_b32 s62,s78,11
s_add_u32 s62,s62,110587904
s_waitcnt vmcnt(0)
s_branch Lr_it0_%=
Lr_ta1_%=:
v_mfma_f32_16x16x32_bf16 v[148:151],v[196:199],v[212:215],0
s_add_u32 s78,s55,71303168
s_add_u32 m0,s65,0
M 148,v[200:203],216
buffer_load_dwordx4 v[164:167],v1,s[36:39],s78 offen offset:0 sc1
M 148,v[204:207],220
buffer_load_dwordx4 v[168:171],v1,s[36:39],s78 offen offset:2048 sc1
M 148,v[208:211],224
L 4
D 9,56,0
v_mfma_f32_16x16x32_bf16 v[40:43],v[196:199],v[196:199],0
D 9,56,1024
M 40,v[200:203],200
L 0
D 9,56,2048
M 40,v[204:207],204
D 9,56,3072
M 40,v[208:211],208
s_add_u32 s78,s55,71307264
v_lshlrev_b32 v84,16,v68
v_and_b32 v85,0xffff0000,v68
M 40,v[212:215],212
buffer_load_dwordx4 v[172:175],v1,s[36:39],s78 offen offset:0 sc1
M 40,v[216:219],216
buffer_load_dwordx4 v[176:179],v1,s[36:39],s78 offen offset:2048 sc1
M 40,v[220:223],220
s_add_u32 s78,s55,89128960
v_lshlrev_b32 v86,16,v69
v_and_b32 v87,0xffff0000,v69
M 40,v[224:227],224
buffer_load_dwordx4 v[180:183],v1,s[36:39],s78 offen offset:0 sc1
v_mfma_f32_16x16x32_bf16 v[36:39],v[32:35],v[196:199],0
buffer_load_dwordx4 v[184:187],v1,s[36:39],s78 offen offset:2048 sc1
M 36,v[32:35],200
s_add_u32 s78,s55,89133056
v_lshlrev_b32 v88,16,v70
v_and_b32 v89,0xffff0000,v70
M 36,v[32:35],204
buffer_load_dwordx4 v[188:191],v1,s[36:39],s78 offen offset:0 sc1
M 36,v[32:35],208
buffer_load_dwordx4 v[192:195],v1,s[36:39],s78 offen offset:2048 sc1
M 36,v[32:35],212
v_fma_f32 v40,2.0,v148,v40
v_fma_f32 v41,2.0,v149,v41
M 36,v[32:35],216
v_fma_f32 v42,2.0,v150,v42
v_fma_f32 v43,2.0,v151,v43
M 36,v[32:35],220
v_cndmask_b32 v45,v40,v41,s[70:71]
v_cndmask_b32 v46,v42,v43,s[70:71]
M 36,v[32:35],224
v_cndmask_b32 v45,v45,v46,s[72:73]
v_lshlrev_b32 v90,16,v71
v_mfma_f32_16x16x32_bf16 v[116:119],v[26:29],v[196:199],0
v_and_b32 v91,0xffff0000,v71
v_lshlrev_b32 v92,16,v72
M 116,v[26:29],212
v_and_b32 v93,0xffff0000,v72
v_lshlrev_b32 v94,16,v73
v_mfma_f32_16x16x32_bf16 v[120:123],v[24:27],v[196:199],0
v_and_b32 v95,0xffff0000,v73
v_lshlrev_b32 v96,16,v74
M 120,v[24:27],212
v_and_b32 v97,0xffff0000,v74
v_lshlrev_b32 v98,16,v75
v_mfma_f32_16x16x32_bf16 v[124:127],v[26:29],v[200:203],0
v_add_f32_dpp v44,v36,v36 row_ror:8 row_mask:0xf bank_mask:0xf
v_and_b32 v99,0xffff0000,v75
M 124,v[26:29],216
ds_write_b32 v2,v44 offset:0
v_mfma_f32_16x16x32_bf16 v[128:131],v[24:27],v[200:203],0
ds_write_b32 v0,v45 offset:0
M 128,v[24:27],216
v_lshlrev_b32 v100,16,v76
v_and_b32 v101,0xffff0000,v76
v_mfma_f32_16x16x32_bf16 v[132:135],v[26:29],v[204:207],0
v_lshlrev_b32 v102,16,v77
v_and_b32 v103,0xffff0000,v77
M 132,v[26:29],220
L 0
v_lshlrev_b32 v104,16,v78
v_and_b32 v105,0xffff0000,v78
v_mfma_f32_16x16x32_bf16 v[136:139],v[24:27],v[204:207],0
s_barrier
R 52,3,0
R 56,3,16
M 136,v[24:27],220
R 60,3,32
v_lshlrev_b32 v106,16,v79
v_mfma_f32_16x16x32_bf16 v[140:143],v[26:29],v[208:211],0
v_and_b32 v107,0xffff0000,v79
v_lshlrev_b32 v108,16,v80
M 140,v[26:29],224
v_and_b32 v109,0xffff0000,v80
v_lshlrev_b32 v110,16,v81
v_mfma_f32_16x16x32_bf16 v[144:147],v[24:27],v[208:211],0
v_and_b32 v111,0xffff0000,v81
v_lshlrev_b32 v112,16,v82
M 144,v[24:27],224
v_and_b32 v113,0xffff0000,v82
v_lshlrev_b32 v114,16,v83
v_and_b32 v115,0xffff0000,v83
L 0
v_pk_add_f32 v[52:53],v[52:53],v[54:55]
v_pk_add_f32 v[56:57],v[56:57],v[58:59]
v_pk_add_f32 v[60:61],v[60:61],v[62:63]
v_add_f32 v52,v52,v53
v_pk_add_f32 v[56:57],v[56:57],v[60:61]
v_mul_f32 v52,s68,v52
v_add_f32 v53,v56,v57
v_mul_f32 v53,s68,v53
v_fma_f32 v53,-v52,v52,v53
v_max_f32 v53,0,v53
buffer_load_dword v18,v6,s[36:39],s62 offen offset:0 sc1
v_add_f32 v53,s69,v53
v_rsq_f32 v48,v53
s_nop 0
v_mul_f32_e64 v50,-v52,v48
v_mov_b32 v49,v48
v_mov_b32 v51,v50
v_pk_fma_f32 v[52:53],v[116:117],v[48:49],v[50:51]
v_pk_fma_f32 v[54:55],v[118:119],v[48:49],v[50:51]
v_pk_fma_f32 v[56:57],v[120:121],v[48:49],v[50:51]
v_pk_fma_f32 v[58:59],v[122:123],v[48:49],v[50:51]
v_pk_fma_f32 v[116:117],v[52:53],v[84:85],v[116:117]
v_pk_fma_f32 v[118:119],v[54:55],v[86:87],v[118:119]
v_pk_fma_f32 v[120:121],v[56:57],v[88:89],v[120:121]
v_pk_fma_f32 v[122:123],v[58:59],v[90:91],v[122:123]
v_pk_fma_f32 v[52:53],v[124:125],v[48:49],v[50:51]
v_pk_fma_f32 v[54:55],v[126:127],v[48:49],v[50:51]
v_pk_fma_f32 v[56:57],v[128:129],v[48:49],v[50:51]
v_pk_fma_f32 v[58:59],v[130:131],v[48:49],v[50:51]
v_pk_fma_f32 v[124:125],v[52:53],v[92:93],v[124:125]
v_pk_fma_f32 v[126:127],v[54:55],v[94:95],v[126:127]
v_pk_fma_f32 v[128:129],v[56:57],v[96:97],v[128:129]
v_pk_fma_f32 v[130:131],v[58:59],v[98:99],v[130:131]
v_pk_fma_f32 v[52:53],v[132:133],v[48:49],v[50:51]
v_pk_fma_f32 v[54:55],v[134:135],v[48:49],v[50:51]
v_pk_fma_f32 v[56:57],v[136:137],v[48:49],v[50:51]
v_pk_fma_f32 v[58:59],v[138:139],v[48:49],v[50:51]
v_pk_fma_f32 v[132:133],v[52:53],v[100:101],v[132:133]
v_pk_fma_f32 v[134:135],v[54:55],v[102:103],v[134:135]
v_pk_fma_f32 v[136:137],v[56:57],v[104:105],v[136:137]
v_pk_fma_f32 v[138:139],v[58:59],v[106:107],v[138:139]
v_pk_fma_f32 v[52:53],v[140:141],v[48:49],v[50:51]
v_pk_fma_f32 v[54:55],v[142:143],v[48:49],v[50:51]
v_pk_fma_f32 v[56:57],v[144:145],v[48:49],v[50:51]
v_pk_fma_f32 v[58:59],v[146:147],v[48:49],v[50:51]
v_pk_fma_f32 v[140:141],v[52:53],v[108:109],v[140:141]
v_pk_fma_f32 v[142:143],v[54:55],v[110:111],v[142:143]
v_pk_fma_f32 v[144:145],v[56:57],v[112:113],v[144:145]
v_pk_fma_f32 v[146:147],v[58:59],v[114:115],v[146:147]
v_cvt_pk_bf16_f32 v148,v116,v117
v_cvt_pk_bf16_f32 v149,v118,v119
v_cvt_pk_bf16_f32 v150,v120,v121
v_cvt_pk_bf16_f32 v151,v122,v123
v_cvt_pk_bf16_f32 v152,v124,v125
v_cvt_pk_bf16_f32 v153,v126,v127
s_waitcnt vmcnt(13)
buffer_store_dword v17,v5,s[36:39],s63 offen offset:0 sc0 sc1
s_mov_b32 s63,0x7fff0000
v_mfma_f32_16x16x32_bf16 v[36:39],v[32:35],v[148:151],0
v_cvt_pk_bf16_f32 v154,v128,v129
v_cvt_pk_bf16_f32 v155,v130,v131
v_mfma_f32_16x16x32_bf16 v[40:43],v[148:151],v[148:151],0
v_cvt_pk_bf16_f32 v156,v132,v133
v_cvt_pk_bf16_f32 v157,v134,v135
M 36,v[32:35],152
v_cvt_pk_bf16_f32 v158,v136,v137
v_cvt_pk_bf16_f32 v159,v138,v139
M 40,v[152:155],152
v_cvt_pk_bf16_f32 v160,v140,v141
v_cvt_pk_bf16_f32 v161,v142,v143
M 36,v[32:35],156
v_cvt_pk_bf16_f32 v162,v144,v145
v_cvt_pk_bf16_f32 v163,v146,v147
M 40,v[156:159],156
buffer_store_dwordx4 v[148:151],v1,s[36:39],s57 offen offset:0 sc0 sc1
M 36,v[32:35],160
buffer_store_dwordx4 v[152:155],v1,s[36:39],s57 offen offset:2048 sc0 sc1
M 40,v[160:163],160
buffer_store_dwordx4 v[156:159],v1,s[36:39],s58 offen offset:0 sc0 sc1
s_nop 4
v_add_f32_dpp v44,v36,v36 row_ror:8 row_mask:0xf bank_mask:0xf
s_nop 0
v_cndmask_b32 v45,v40,v41,s[70:71]
v_cndmask_b32 v46,v42,v43,s[70:71]
ds_write_b32 v2,v44 offset:1024
v_cndmask_b32 v45,v45,v46,s[72:73]
ds_write_b32 v0,v45 offset:1024
buffer_store_dwordx4 v[160:163],v1,s[36:39],s58 offen offset:2048 sc0 sc1
L 0
s_barrier
R 52,3,1024
R 56,3,1040
R 60,3,1056
L 0
v_pk_add_f32 v[52:53],v[52:53],v[54:55]
v_pk_add_f32 v[56:57],v[56:57],v[58:59]
v_pk_add_f32 v[60:61],v[60:61],v[62:63]
v_add_f32 v52,v52,v53
v_pk_add_f32 v[56:57],v[56:57],v[60:61]
v_mul_f32 v52,s68,v52
v_add_f32 v53,v56,v57
v_mul_f32 v53,s68,v53
v_fma_f32 v53,-v52,v52,v53
v_max_f32 v53,0,v53
v_add_f32 v54,s69,v53
v_fma_f32 v55,s69,v54,v53
v_rsq_f32 v65,v54
v_rsq_f32 v54,v55
v_mul_f32_e64 v64,-v52,v65
v_mul_f32 v63,v55,v54
v_mul_f32 v61,s76,v54
v_mul_f32 v55,v54,v54
v_mul_f32 v62,s77,v55
v_mul_f32 v62,v62,v61
v_cndmask_b32 v60,v52,v64,s[74:75]
v_cndmask_b32 v61,v61,v65,s[74:75]
buffer_store_dwordx4 v[60:63],v4,s[36:39],s59 offen offset:0 sc0 sc1
s_mov_b32 s63,s60
s_lshr_b32 s78,s45,1
s_and_b32 s79,s45,1
s_lshl_b32 s79,s79,7
s_lshl_b32 s57,s78,15
s_add_u32 s57,s57,s79
s_add_u32 s57,s57,12582912
s_add_u32 s58,s57,4096
s_lshl_b32 s59,s78,9
s_add_u32 s59,s59,s79
s_add_u32 s59,s59,107020288
s_lshl_b32 s60,s45,9
s_add_u32 s60,s60,s66
s_add_u32 s60,s60,131072
s_add_u32 s45,s45,s46
s_lshr_b32 s78,s45,1
s_and_b32 s79,s45,1
s_lshl_b32 s79,s79,7
s_lshl_b32 s55,s78,15
s_add_u32 s55,s55,s79
s_mul_i32 s56,s45,16384
s_add_u32 s56,s56,98566144
s_lshl_b32 s61,s78,11
s_add_u32 s61,s61,110587904
s_add_u32 s78,s45,s46
s_lshr_b32 s78,s78,1
s_lshl_b32 s62,s78,11
s_add_u32 s62,s62,110587904
s_waitcnt vmcnt(6)
s_branch Lr_it0_%=
Lr_tb1_%=:
v_add_f32 v53,s69,v53
v_rsq_f32 v48,v53
s_nop 0
v_mul_f32_e64 v50,-v52,v48
v_mov_b32 v49,v48
v_mov_b32 v51,v50
v_pk_fma_f32 v[52:53],v[116:117],v[48:49],v[50:51]
v_pk_fma_f32 v[54:55],v[118:119],v[48:49],v[50:51]
v_pk_fma_f32 v[56:57],v[120:121],v[48:49],v[50:51]
v_pk_fma_f32 v[58:59],v[122:123],v[48:49],v[50:51]
s_add_u32 s78,s55,71303168
buffer_load_dwordx4 v[164:167],v1,s[36:39],s78 offen offset:0 sc1
buffer_load_dwordx4 v[168:171],v1,s[36:39],s78 offen offset:2048 sc1
s_add_u32 s78,s55,71307264
buffer_load_dwordx4 v[172:175],v1,s[36:39],s78 offen offset:0 sc1
buffer_load_dwordx4 v[176:179],v1,s[36:39],s78 offen offset:2048 sc1
v_pk_fma_f32 v[116:117],v[52:53],v[84:85],v[116:117]
v_pk_fma_f32 v[118:119],v[54:55],v[86:87],v[118:119]
v_pk_fma_f32 v[120:121],v[56:57],v[88:89],v[120:121]
v_pk_fma_f32 v[122:123],v[58:59],v[90:91],v[122:123]
v_pk_fma_f32 v[52:53],v[124:125],v[48:49],v[50:51]
v_pk_fma_f32 v[54:55],v[126:127],v[48:49],v[50:51]
v_pk_fma_f32 v[56:57],v[128:129],v[48:49],v[50:51]
v_pk_fma_f32 v[58:59],v[130:131],v[48:49],v[50:51]
s_add_u32 s78,s55,89128960
buffer_load_dwordx4 v[180:183],v1,s[36:39],s78 offen offset:0 sc1
buffer_load_dwordx4 v[184:187],v1,s[36:39],s78 offen offset:2048 sc1
s_add_u32 s78,s55,89133056
buffer_load_dwordx4 v[188:191],v1,s[36:39],s78 offen offset:0 sc1
buffer_load_dwordx4 v[192:195],v1,s[36:39],s78 offen offset:2048 sc1
v_pk_fma_f32 v[124:125],v[52:53],v[92:93],v[124:125]
v_pk_fma_f32 v[126:127],v[54:55],v[94:95],v[126:127]
v_pk_fma_f32 v[128:129],v[56:57],v[96:97],v[128:129]
v_pk_fma_f32 v[130:131],v[58:59],v[98:99],v[130:131]
v_pk_fma_f32 v[52:53],v[132:133],v[48:49],v[50:51]
v_pk_fma_f32 v[54:55],v[134:135],v[48:49],v[50:51]
v_pk_fma_f32 v[56:57],v[136:137],v[48:49],v[50:51]
v_pk_fma_f32 v[58:59],v[138:139],v[48:49],v[50:51]
v_pk_fma_f32 v[132:133],v[52:53],v[100:101],v[132:133]
v_pk_fma_f32 v[134:135],v[54:55],v[102:103],v[134:135]
v_pk_fma_f32 v[136:137],v[56:57],v[104:105],v[136:137]
v_pk_fma_f32 v[138:139],v[58:59],v[106:107],v[138:139]
v_pk_fma_f32 v[52:53],v[140:141],v[48:49],v[50:51]
v_pk_fma_f32 v[54:55],v[142:143],v[48:49],v[50:51]
v_pk_fma_f32 v[56:57],v[144:145],v[48:49],v[50:51]
v_pk_fma_f32 v[58:59],v[146:147],v[48:49],v[50:51]
v_pk_fma_f32 v[140:141],v[52:53],v[108:109],v[140:141]
v_pk_fma_f32 v[142:143],v[54:55],v[110:111],v[142:143]
v_pk_fma_f32 v[144:145],v[56:57],v[112:113],v[144:145]
v_pk_fma_f32 v[146:147],v[58:59],v[114:115],v[146:147]
buffer_load_dword v18,v6,s[36:39],s62 offen offset:0 sc1
v_cvt_pk_bf16_f32 v148,v116,v117
v_cvt_pk_bf16_f32 v149,v118,v119
v_cvt_pk_bf16_f32 v150,v120,v121
v_cvt_pk_bf16_f32 v151,v122,v123
v_cvt_pk_bf16_f32 v152,v124,v125
v_cvt_pk_bf16_f32 v153,v126,v127
v_mfma_f32_16x16x32_bf16 v[36:39],v[32:35],v[148:151],0
v_cvt_pk_bf16_f32 v154,v128,v129
v_cvt_pk_bf16_f32 v155,v130,v131
v_mfma_f32_16x16x32_bf16 v[40:43],v[148:151],v[148:151],0
v_cvt_pk_bf16_f32 v156,v132,v133
v_cvt_pk_bf16_f32 v157,v134,v135
M 36,v[32:35],152
v_cvt_pk_bf16_f32 v158,v136,v137
v_cvt_pk_bf16_f32 v159,v138,v139
M 40,v[152:155],152
v_cvt_pk_bf16_f32 v160,v140,v141
v_cvt_pk_bf16_f32 v161,v142,v143
M 36,v[32:35],156
v_cvt_pk_bf16_f32 v162,v144,v145
v_cvt_pk_bf16_f32 v163,v146,v147
M 40,v[156:159],156
buffer_store_dwordx4 v[148:151],v1,s[36:39],s57 offen offset:0 sc0 sc1
M 36,v[32:35],160
buffer_store_dwordx4 v[152:155],v1,s[36:39],s57 offen offset:2048 sc0 sc1
M 40,v[160:163],160
buffer_store_dwordx4 v[156:159],v1,s[36:39],s58 offen offset:0 sc0 sc1
s_nop 4
v_add_f32_dpp v44,v36,v36 row_ror:8 row_mask:0xf bank_mask:0xf
s_nop 0
v_cndmask_b32 v45,v40,v41,s[70:71]
v_cndmask_b32 v46,v42,v43,s[70:71]
ds_write_b32 v2,v44 offset:1024
v_cndmask_b32 v45,v45,v46,s[72:73]
ds_write_b32 v0,v45 offset:1024
buffer_store_dwordx4 v[160:163],v1,s[36:39],s58 offen offset:2048 sc0 sc1
)"
R"(L 0
s_barrier
R 52,3,1024
R 56,3,1040
R 60,3,1056
L 0
v_pk_add_f32 v[52:53],v[52:53],v[54:55]
v_pk_add_f32 v[56:57],v[56:57],v[58:59]
v_pk_add_f32 v[60:61],v[60:61],v[62:63]
v_add_f32 v52,v52,v53
v_pk_add_f32 v[56:57],v[56:57],v[60:61]
v_mul_f32 v52,s68,v52
v_add_f32 v53,v56,v57
v_mul_f32 v53,s68,v53
v_fma_f32 v53,-v52,v52,v53
v_max_f32 v53,0,v53
v_add_f32 v54,s69,v53
v_fma_f32 v55,s69,v54,v53
v_rsq_f32 v65,v54
v_rsq_f32 v54,v55
v_mul_f32_e64 v64,-v52,v65
v_mul_f32 v63,v55,v54
v_mul_f32 v61,s76,v54
v_mul_f32 v55,v54,v54
v_mul_f32 v62,s77,v55
v_mul_f32 v62,v62,v61
v_cndmask_b32 v60,v52,v64,s[74:75]
v_cndmask_b32 v61,v61,v65,s[74:75]
buffer_store_dwordx4 v[60:63],v4,s[36:39],s59 offen offset:0 sc0 sc1
s_mov_b32 s63,s60
s_lshr_b32 s78,s45,1
s_and_b32 s79,s45,1
s_lshl_b32 s79,s79,7
s_lshl_b32 s57,s78,15
s_add_u32 s57,s57,s79
s_add_u32 s57,s57,12582912
s_add_u32 s58,s57,4096
s_lshl_b32 s59,s78,9
s_add_u32 s59,s59,s79
s_add_u32 s59,s59,107020288
s_lshl_b32 s60,s45,9
s_add_u32 s60,s60,s66
s_add_u32 s60,s60,131072
s_add_u32 s45,s45,s46
s_lshr_b32 s78,s45,1
s_and_b32 s79,s45,1
s_lshl_b32 s79,s79,7
s_lshl_b32 s55,s78,15
s_add_u32 s55,s55,s79
s_mul_i32 s56,s45,16384
s_add_u32 s56,s56,98566144
s_lshl_b32 s61,s78,11
s_add_u32 s61,s61,110587904
s_add_u32 s78,s45,s46
s_lshr_b32 s78,s78,1
s_lshl_b32 s62,s78,11
s_add_u32 s62,s62,110587904
s_waitcnt vmcnt(5)
s_branch Lr_it0_%=
Lr_tc1_%=:
s_add_u32 s78,s55,71303168
buffer_load_dwordx4 v[164:167],v1,s[36:39],s78 offen offset:0 sc1
buffer_load_dwordx4 v[168:171],v1,s[36:39],s78 offen offset:2048 sc1
s_add_u32 s78,s55,71307264
buffer_load_dwordx4 v[172:175],v1,s[36:39],s78 offen offset:0 sc1
buffer_load_dwordx4 v[176:179],v1,s[36:39],s78 offen offset:2048 sc1
s_add_u32 s78,s55,89128960
buffer_load_dwordx4 v[180:183],v1,s[36:39],s78 offen offset:0 sc1
buffer_load_dwordx4 v[184:187],v1,s[36:39],s78 offen offset:2048 sc1
s_add_u32 s78,s55,89133056
buffer_load_dwordx4 v[188:191],v1,s[36:39],s78 offen offset:0 sc1
buffer_load_dwordx4 v[192:195],v1,s[36:39],s78 offen offset:2048 sc1
buffer_load_dword v18,v6,s[36:39],s62 offen offset:0 sc1
s_waitcnt vmcnt(9)
buffer_store_dword v17,v5,s[36:39],s63 offen offset:0 sc0 sc1
s_mov_b32 s63,0x7fff0000
s_lshr_b32 s78,s45,1
s_and_b32 s79,s45,1
s_lshl_b32 s79,s79,7
s_lshl_b32 s57,s78,15
s_add_u32 s57,s57,s79
s_add_u32 s57,s57,12582912
s_add_u32 s58,s57,4096
s_lshl_b32 s59,s78,9
s_add_u32 s59,s59,s79
s_add_u32 s59,s59,107020288
s_lshl_b32 s60,s45,9
s_add_u32 s60,s60,s66
s_add_u32 s60,s60,131072
s_add_u32 s45,s45,s46
s_lshr_b32 s78,s45,1
s_and_b32 s79,s45,1
s_lshl_b32 s79,s79,7
s_lshl_b32 s55,s78,15
s_add_u32 s55,s55,s79
s_mul_i32 s56,s45,16384
s_add_u32 s56,s56,98566144
s_lshl_b32 s61,s78,11
s_add_u32 s61,s61,110587904
s_add_u32 s78,s45,s46
s_lshr_b32 s78,s78,1
s_lshl_b32 s62,s78,11
s_add_u32 s62,s62,110587904
s_waitcnt vmcnt(1)
s_branch Lr_it0_%=
Lr_done_%=:
s_waitcnt vmcnt(0) lgkmcnt(0)
s_barrier
s_branch Lr_end_%=
.p2align 8
Lr_tab_%=:
s_mov_b64 s[40:41],%24
s_mov_b32 s42,7340032
s_mov_b32 s43,0x00020000
s_add_u32 s83,s50,0
s_add_u32 m0,s54,40960
s_nop 0
buffer_load_dwordx4 v13,s[40:43],s83 offen offset:0  lds
s_add_u32 m0,s54,41984
s_nop 0
buffer_load_dwordx4 v14,s[40:43],s83 offen offset:0  lds
s_add_u32 m0,s54,49024
s_nop 0
buffer_load_dwordx4 v13,s[40:43],s83 offen offset:128  lds
s_add_u32 m0,s54,50048
s_nop 0
buffer_load_dwordx4 v14,s[40:43],s83 offen offset:128  lds
s_setpc_b64 s[88:89]
.p2align 8
s_mov_b64 s[40:41],%24
s_mov_b32 s42,7340032
s_mov_b32 s43,0x00020000
v_add_u32 v230,40960,v15
v_add_u32 v231,40960,v16
T a[0:1],230,0
T a[2:3],230,512
T a[4:5],231,0
T a[6:7],231,512
T a[8:9],230,4096
T a[10:11],230,4608
T a[12:13],231,4096
T a[14:15],231,4608
s_add_u32 s83,s50,131072
s_add_u32 m0,s54,57344
s_nop 0
buffer_load_dwordx4 v13,s[40:43],s83 offen offset:0  lds
s_add_u32 m0,s54,58368
s_nop 0
buffer_load_dwordx4 v14,s[40:43],s83 offen offset:0  lds
s_add_u32 m0,s54,65408
s_nop 0
buffer_load_dwordx4 v13,s[40:43],s83 offen offset:128  lds
s_add_u32 m0,s54,66432
s_nop 0
buffer_load_dwordx4 v14,s[40:43],s83 offen offset:128  lds
s_setpc_b64 s[88:89]
.p2align 8
s_mov_b64 s[40:41],%24
s_mov_b32 s42,7340032
s_mov_b32 s43,0x00020000
v_add_u32 v230,57344,v15
v_add_u32 v231,57344,v16
T a[16:17],230,0
T a[18:19],230,512
T a[20:21],231,0
T a[22:23],231,512
T a[24:25],230,4096
T a[26:27],230,4608
T a[28:29],231,4096
T a[30:31],231,4608
s_add_u32 s83,s50,262144
s_add_u32 m0,s54,73728
s_nop 0
buffer_load_dwordx4 v13,s[40:43],s83 offen offset:0  lds
s_add_u32 m0,s54,74752
s_nop 0
buffer_load_dwordx4 v14,s[40:43],s83 offen offset:0  lds
s_add_u32 m0,s54,81792
s_nop 0
buffer_load_dwordx4 v13,s[40:43],s83 offen offset:128  lds
s_add_u32 m0,s54,82816
s_nop 0
buffer_load_dwordx4 v14,s[40:43],s83 offen offset:128  lds
s_setpc_b64 s[88:89]
.p2align 8
s_mov_b64 s[40:41],%24
s_mov_b32 s42,7340032
s_mov_b32 s43,0x00020000
v_add_u32 v230,73728,v15
v_add_u32 v231,73728,v16
T a[32:33],230,0
T a[34:35],230,512
T a[36:37],231,0
T a[38:39],231,512
T a[40:41],230,4096
T a[42:43],230,4608
T a[44:45],231,4096
T a[46:47],231,4608
s_add_u32 s83,s50,393216
s_add_u32 m0,s54,90112
s_nop 0
buffer_load_dwordx4 v13,s[40:43],s83 offen offset:0  lds
s_add_u32 m0,s54,91136
s_nop 0
buffer_load_dwordx4 v14,s[40:43],s83 offen offset:0  lds
s_add_u32 m0,s54,98176
s_nop 0
buffer_load_dwordx4 v13,s[40:43],s83 offen offset:128  lds
s_add_u32 m0,s54,99200
s_nop 0
buffer_load_dwordx4 v14,s[40:43],s83 offen offset:128  lds
s_setpc_b64 s[88:89]
.p2align 8
s_mov_b64 s[40:41],%24
s_mov_b32 s42,7340032
s_mov_b32 s43,0x00020000
v_add_u32 v230,90112,v15
v_add_u32 v231,90112,v16
T a[48:49],230,0
T a[50:51],230,512
T a[52:53],231,0
T a[54:55],231,512
T a[56:57],230,4096
T a[58:59],230,4608
T a[60:61],231,4096
T a[62:63],231,4608
s_add_u32 s83,s50,524288
s_add_u32 m0,s54,106496
s_nop 0
buffer_load_dwordx4 v13,s[40:43],s83 offen offset:0  lds
s_add_u32 m0,s54,107520
s_nop 0
buffer_load_dwordx4 v14,s[40:43],s83 offen offset:0  lds
s_add_u32 m0,s54,114560
s_nop 0
buffer_load_dwordx4 v13,s[40:43],s83 offen offset:128  lds
s_add_u32 m0,s54,115584
s_nop 0
buffer_load_dwordx4 v14,s[40:43],s83 offen offset:128  lds
s_setpc_b64 s[88:89]
.p2align 8
s_mov_b64 s[40:41],%24
s_mov_b32 s42,7340032
s_mov_b32 s43,0x00020000
v_add_u32 v230,106496,v15
v_add_u32 v231,106496,v16
T a[64:65],230,0
T a[66:67],230,512
T a[68:69],231,0
T a[70:71],231,512
T a[72:73],230,4096
T a[74:75],230,4608
T a[76:77],231,4096
T a[78:79],231,4608
s_add_u32 s83,s50,655360
s_add_u32 m0,s54,122880
s_nop 0
buffer_load_dwordx4 v13,s[40:43],s83 offen offset:0  lds
s_add_u32 m0,s54,123904
s_nop 0
buffer_load_dwordx4 v14,s[40:43],s83 offen offset:0  lds
s_add_u32 m0,s54,130944
s_nop 0
buffer_load_dwordx4 v13,s[40:43],s83 offen offset:128  lds
s_add_u32 m0,s54,131968
s_nop 0
buffer_load_dwordx4 v14,s[40:43],s83 offen offset:128  lds
s_setpc_b64 s[88:89]
.p2align 8
s_mov_b64 s[40:41],%24
s_mov_b32 s42,7340032
s_mov_b32 s43,0x00020000
v_add_u32 v230,122880,v15
v_add_u32 v231,122880,v16
T a[80:81],230,0
T a[82:83],230,512
T a[84:85],231,0
T a[86:87],231,512
T a[88:89],230,4096
T a[90:91],230,4608
T a[92:93],231,4096
T a[94:95],231,4608
s_add_u32 s83,s50,786432
s_add_u32 m0,s54,139264
s_nop 0
buffer_load_dwordx4 v13,s[40:43],s83 offen offset:0  lds
s_add_u32 m0,s54,140288
s_nop 0
buffer_load_dwordx4 v14,s[40:43],s83 offen offset:0  lds
s_add_u32 m0,s54,147328
s_nop 0
buffer_load_dwordx4 v13,s[40:43],s83 offen offset:128  lds
s_add_u32 m0,s54,148352
s_nop 0
buffer_load_dwordx4 v14,s[40:43],s83 offen offset:128  lds
s_setpc_b64 s[88:89]
.p2align 8
s_mov_b64 s[40:41],%24
s_mov_b32 s42,7340032
s_mov_b32 s43,0x00020000
v_add_u32 v230,139264,v15
v_add_u32 v231,139264,v16
T a[96:97],230,0
T a[98:99],230,512
T a[100:101],231,0
T a[102:103],231,512
T a[104:105],230,4096
T a[106:107],230,4608
T a[108:109],231,4096
T a[110:111],231,4608
s_add_u32 s83,s50,917504
s_add_u32 m0,s54,40960
s_nop 0
buffer_load_dwordx4 v13,s[40:43],s83 offen offset:0  lds
s_add_u32 m0,s54,41984
s_nop 0
buffer_load_dwordx4 v14,s[40:43],s83 offen offset:0  lds
s_add_u32 m0,s54,49024
s_nop 0
buffer_load_dwordx4 v13,s[40:43],s83 offen offset:128  lds
s_add_u32 m0,s54,50048
s_nop 0
buffer_load_dwordx4 v14,s[40:43],s83 offen offset:128  lds
s_setpc_b64 s[88:89]
.p2align 8
s_mov_b64 s[40:41],%24
s_mov_b32 s42,7340032
s_mov_b32 s43,0x00020000
v_add_u32 v230,40960,v15
v_add_u32 v231,40960,v16
T a[112:113],230,0
T a[114:115],230,512
T a[116:117],231,0
T a[118:119],231,512
T a[120:121],230,4096
T a[122:123],230,4608
T a[124:125],231,4096
T a[126:127],231,4608
s_add_u32 s83,s50,1048576
s_add_u32 m0,s54,57344
s_nop 0
buffer_load_dwordx4 v13,s[40:43],s83 offen offset:0  lds
s_add_u32 m0,s54,58368
s_nop 0
buffer_load_dwordx4 v14,s[40:43],s83 offen offset:0  lds
s_add_u32 m0,s54,65408
s_nop 0
buffer_load_dwordx4 v13,s[40:43],s83 offen offset:128  lds
s_add_u32 m0,s54,66432
s_nop 0
buffer_load_dwordx4 v14,s[40:43],s83 offen offset:128  lds
s_setpc_b64 s[88:89]
.p2align 8
s_mov_b64 s[40:41],%24
s_mov_b32 s42,7340032
s_mov_b32 s43,0x00020000
v_add_u32 v230,57344,v15
v_add_u32 v231,57344,v16
T a[128:129],230,0
T a[130:131],230,512
T a[132:133],231,0
T a[134:135],231,512
T a[136:137],230,4096
T a[138:139],230,4608
T a[140:141],231,4096
T a[142:143],231,4608
s_add_u32 s83,s50,1179648
s_add_u32 m0,s54,73728
s_nop 0
buffer_load_dwordx4 v13,s[40:43],s83 offen offset:0  lds
s_add_u32 m0,s54,74752
s_nop 0
buffer_load_dwordx4 v14,s[40:43],s83 offen offset:0  lds
s_add_u32 m0,s54,81792
s_nop 0
buffer_load_dwordx4 v13,s[40:43],s83 offen offset:128  lds
s_add_u32 m0,s54,82816
s_nop 0
buffer_load_dwordx4 v14,s[40:43],s83 offen offset:128  lds
s_setpc_b64 s[88:89]
.p2align 8
s_mov_b64 s[40:41],%24
s_mov_b32 s42,7340032
s_mov_b32 s43,0x00020000
v_add_u32 v230,73728,v15
v_add_u32 v231,73728,v16
T a[144:145],230,0
T a[146:147],230,512
T a[148:149],231,0
T a[150:151],231,512
T a[152:153],230,4096
T a[154:155],230,4608
T a[156:157],231,4096
T a[158:159],231,4608
s_add_u32 s83,s50,1310720
s_add_u32 m0,s54,90112
s_nop 0
buffer_load_dwordx4 v13,s[40:43],s83 offen offset:0  lds
s_add_u32 m0,s54,91136
s_nop 0
buffer_load_dwordx4 v14,s[40:43],s83 offen offset:0  lds
s_add_u32 m0,s54,98176
s_nop 0
buffer_load_dwordx4 v13,s[40:43],s83 offen offset:128  lds
s_add_u32 m0,s54,99200
s_nop 0
buffer_load_dwordx4 v14,s[40:43],s83 offen offset:128  lds
s_setpc_b64 s[88:89]
.p2align 8
Lr_end_%=:
Lcode_r_e:
.purgem M
.purgem R
.purgem T
.purgem D
.purgem L
)"
  :: "s"(ws), "s"(__builtin_amdgcn_readfirstlane(sWave)), "s"(__builtin_amdgcn_readfirstlane(sG0)), "s"(__builtin_amdgcn_readfirstlane(sStep)), "s"(__builtin_amdgcn_readfirstlane(sWw)), "s"(__builtin_amdgcn_readfirstlane(sExp)), "v"(vRP), "v"(vRF), "v"(vRwr), "v"(vRrd), "v"(vRS), "v"(vFlag), "v"(vL4), "v"(vLY), "v"(vQ), "v"(vY0), "v"(vTr), "v"(vSa), "v"(vSb), "v"(vWd0), "v"(vWd1), "v"(vWr0), "v"(vWr1), "s"(q), "s"(w4) : "v0", "v1", "v2", "v3", "v4", "v5", "v6", "v7", "v8", "v9", "v10", "v11", "v12", "v13", "v14", "v15", "v16", "v17", "v18", "v19", "v20", "v21", "v22", "v23", "v24", "v25", "v26", "v27", "v28", "v29", "v30", "v31", "v32", "v33", "v34", "v35", "v36", "v37", "v38", "v39", "v40", "v41", "v42", "v43", "v44", "v45", "v46", "v47", "v48", "v49", "v50", "v51", "v52", "v53", "v54", "v55", "v56", "v57", "v58", "v59", "v60", "v61", "v62", "v63", "v64", "v65", "v66", "v67", "v68", "v69", "v70", "v71", "v72", "v73", "v74", "v75", "v76", "v77", "v78", "v79", "v80", "v81", "v82", "v83", "v84", "v85", "v86", "v87", "v88", "v89", "v90", "v91", "v92", "v93", "v94", "v95", "v96", "v97", "v98", "v99", "v100", "v101", "v102", "v103", "v104", "v105", "v106", "v107", "v108", "v109", "v110", "v111", "v112", "v113", "v114", "v115", "v116", "v117", "v118", "v119", "v120", "v121", "v122", "v123", "v124", "v125", "v126", "v127", "v128", "v129", "v130", "v131", "v132", "v133", "v134", "v135", "v136", "v137", "v138", "v139", "v140", "v141", "v142", "v143", "v144", "v145", "v146", "v147", "v148", "v149", "v150", "v151", "v152", "v153", "v154", "v155", "v156", "v157", "v158", "v159", "v160", "v161", "v162", "v163", "v164", "v165", "v166", "v167", "v168", "v169", "v170", "v171", "v172", "v173", "v174", "v175", "v176", "v177", "v178", "v179", "v180", "v181", "v182", "v183", "v184", "v185", "v186", "v187", "v188", "v189", "v190", "v191", "v192", "v193", "v194", "v195", "v196", "v197", "v198", "v199", "v200", "v201", "v202", "v203", "v204", "v205", "v206", "v207", "v208", "v209", "v210", "v211", "v212", "v213", "v214", "v215", "v216", "v217", "v218", "v219", "v220", "v221", "v222", "v223", "v224", "v225", "v226", "v227", "v228", "v229", "v230", "v231", "a0", "a1", "a2", "a3", "a4", "a5", "a6", "a7", "a8", "a9", "a10", "a11", "a12", "a13", "a14", "a15", "a16", "a17", "a18", "a19", "a20", "a21", "a22", "a23", "a24", "a25", "a26", "a27", "a28", "a29", "a30", "a31", "a32", "a33", "a34", "a35", "a36", "a37", "a38", "a39", "a40", "a41", "a42", "a43", "a44", "a45", "a46", "a47", "a48", "a49", "a50", "a51", "a52", "a53", "a54", "a55", "a56", "a57", "a58", "a59", "a60", "a61", "a62", "a63", "a64", "a65", "a66", "a67", "a68", "a69", "a70", "a71", "a72", "a73", "a74", "a75", "a76", "a77", "a78", "a79", "a80", "a81", "a82", "a83", "a84", "a85", "a86", "a87", "a88", "a89", "a90", "a91", "a92", "a93", "a94", "a95", "a96", "a97", "a98", "a99", "a100", "a101", "a102", "a103", "a104", "a105", "a106", "a107", "a108", "a109", "a110", "a111", "a112", "a113", "a114", "a115", "a116", "a117", "a118", "a119", "a120", "a121", "a122", "a123", "a124", "a125", "a126", "a127", "a128", "a129", "a130", "a131", "a132", "a133", "a134", "a135", "a136", "a137", "a138", "a139", "a140", "a141", "a142", "a143", "a144", "a145", "a146", "a147", "a148", "a149", "a150", "a151", "a152", "a153", "a154", "a155", "a156", "a157", "a158", "a159", "a160", "a161", "a162", "a163", "a164", "a165", "a166", "a167", "a168", "a169", "a170", "a171", "a172", "a173", "a174", "a175", "a176", "a177", "a178", "a179", "a180", "a181", "a182", "a183", "a184", "a185", "a186", "a187", "a188", "a189", "a190", "a191", "a192", "a193", "a194", "a195", "a196", "a197", "a198", "a199", "a200", "a201", "a202", "a203", "a204", "a205", "a206", "a207", "a208", "a209", "a210", "a211", "a212", "a213", "a214", "a215", "a216", "a217", "a218", "a219", "a220", "a221", "a222", "a223", "a224", "a225", "a226", "a227", "a228", "a229", "a230", "a231", "a232", "a233", "a234", "a235", "a236", "a237", "a238", "a239", "a240", "a241", "a242", "a243", "a244", "a245", "a246", "a247", "a248", "a249", "a250", "a251", "a252", "a253", "a254", "a255", "s36", "s37", "s38", "s39", "s40", "s41", "s42", "s43", "s44", "s45", "s46", "s47", "s48", "s49", "s50", "s51", "s52", "s53", "s54", "s55", "s56", "s57", "s58", "s59", "s60", "s61", "s62", "s63", "s64", "s65", "s66", "s67", "s68", "s69", "s70", "s71", "s72", "s73", "s74", "s75", "s76", "s77", "s78", "s79", "s80", "s81", "s82", "s83", "s84", "s85", "s86", "s87", "s88", "s89", "s90", "s91", "s92", "s93", "s94", "s95", "s96", "s97", "vcc", "scc", "memory");
    if (RHELP && slice < 16) { kind = 4; rep = 5; kh = slice >> 3; slice &= 7; }
  }
  CTS(9);
#define COMMON(NF, FOFF, FSTR, SNAP, LDW, NL, TOFF)                                                                                            \
  LANE_ID(ln);                                                                                                                       \
  const int l15 = ln & 15, lg = ln >> 4, lq = l15 >> 2, lp = ln & 3, rr = 8 * lg + lq, fj = ln & 31;                                 \
  const unsigned vL16 = ln * 16, vFlag = (ln < 32 && wave == 0) ? ln * 4 : OOB;                                                      \
  const unsigned vSnapOff = (wave == 0 && fj < (NF)) ? (FOFF) + (ln >> 5) * sStep * (FSTR) : OOB;                                    \
  const unsigned vSnapRd = (SNAP) + (fj < (NF) ? ln : ln & 32) * 4;                                                                  \
  const unsigned vWd0 = (ln >> 3) * (LDW) + ((ln & 7) ^ swzB(ln >> 3)) * 16, vWd1 = ((ln >> 3) + 8) * (LDW) + ((ln & 7) ^ swzB(8 + (ln >> 3))) * 16; \
  const unsigned vWr0 = (wave >> 1) * 8192 + rr * 128 + (((wave & 1) * 4 + lp) ^ swzB(rr)) * 16, vWr1 = vWr0 + 8;                    \
  const unsigned vWr2 = 16384 + rr * 128 + ((2 * wave + (lp >> 1)) ^ swzB(rr)) * 16 + (lp & 1) * 8;                                  \
  const unsigned sWave = wave, sRk = kind == 4 ? rep : t_start, sG0 = rep < 5 ? rep : rep == 5 ? U0 + 5 : U1 + rep, sDbg = sDbg0 + (rep < 5 ? 0 : 2048)
  if (kind == 3 || kind == 5) {
    const unsigned sStep = kind == 3 ? 5 : G3OR;
    COMMON(8, fj * 128, 1024, 131072, 7168, 256, 0);
    const unsigned vOutS = (lg & 1) * 8 + (((wave & 1) * 2 + (lg >> 1)) * 16 + l15) * 16 + (4 + (wave >> 1) - wave) * 1024;
    const unsigned vAux0 = (ln & 7) * 128 + (4 * wave + lg) * 8 + ((ln >> 3) & 1) * 4;
    const float k1 = -2.0f * 0.7978845608028654f * 1.4426950408889634f;
    const unsigned vAux1 = 139264 + (l15 < 4 ? (4 * wave + lg) * 16 + l15 * 4 : 512 + ln * 4), vAux2 = 139264 + l15 * 16;
    const unsigned vAux3 = l15 == 2 ? __float_as_uint(k1 * 0.044715f) : 0u, vAn = l15 == 3 ? 0x3f800000u : 0u, vAe = l15 == 1 ? __float_as_uint(k1) : 0u;
    const unsigned sOutOff = OFF_G + (slice * 6 + wave) * 1024, sFoutOff = OFF_FG + slice * 128, sAuxOff = 0, sWw = 16 * wave * 7168 + slice * 384, sBuf = 0;
    const unsigned sExp = __builtin_amdgcn_readfirstlane(ep);
    if (kind == 3) { asm volatile(
R"(.macro M d,w,x
v_mfma_f32_16x16x32_bf16 v[\d:\d+3], \w, v[\x:\x+3], v[\d:\d+3]
.endm
.macro R d,a,o
ds_read_b128 v[\d:\d+3], v\a offset:\o
.endm
.macro T d,a,o
ds_read_b64_tr_b16 \d, v\a offset:\o
.endm
.macro D a,s,o
buffer_load_dwordx4 v\a, s[36:39], s\s offen offset:\o sc1 lds
.endm
.macro L c
s_waitcnt lgkmcnt(\c)
.endm
Lcode_g3_b:
s_mov_b64 s[36:37],%0
s_mov_b32 s38,111243392
s_mov_b32 s39,0x00020000
s_mov_b32 s44,%1
s_mov_b32 s45,%2
s_mov_b32 s46,%3
s_mov_b32 s47,%4
s_mov_b32 s48,%5
s_mov_b32 s49,%6
s_mov_b32 s50,%7
s_mov_b32 s51,%8
s_mov_b32 s53,%9
v_mov_b32 v0,%10
v_mov_b32 v3,%11
v_mov_b32 v5,%12
v_mov_b32 v6,%13
v_mov_b32 v18,%14
v_mov_b32 v19,%15
v_mov_b32 v20,%16
v_mov_b32 v21,%17
v_mov_b32 v22,%18
v_mov_b32 v10,%19
v_mov_b32 v11,%20
v_mov_b32 v12,%21
v_mov_b32 v13,%22
v_mov_b32 v14,%23
v_mov_b32 v15,%24
v_mov_b32 v16,%25
s_mov_b64 s[40:41],%26
s_mov_b32 s42,7340032
s_mov_b32 s43,0x00020000
s_lshl_b32 s57,s44,11
s_cmp_ge_u32 s45,8
s_cbranch_scc1 Lg3_dly_%=
s_sleep 16
Lg3_dly_%=:
s_add_u32 s55,s50,0
s_add_u32 m0,s57,0
s_nop 0
buffer_load_dwordx4 v18,s[40:43],s55 offen offset:0  lds
s_add_u32 m0,s57,1024
s_nop 0
buffer_load_dwordx4 v19,s[40:43],s55 offen offset:0  lds
s_add_u32 m0,s57,8064
s_nop 0
buffer_load_dwordx4 v18,s[40:43],s55 offen offset:128  lds
s_add_u32 m0,s57,9088
s_nop 0
buffer_load_dwordx4 v19,s[40:43],s55 offen offset:128  lds
s_add_u32 m0,s57,16128
s_nop 0
buffer_load_dwordx4 v18,s[40:43],s55 offen offset:256  lds
s_add_u32 m0,s57,17152
s_nop 0
buffer_load_dwordx4 v19,s[40:43],s55 offen offset:256  lds
s_add_u32 s55,s50,458752
s_add_u32 m0,s57,24576
s_nop 0
buffer_load_dwordx4 v18,s[40:43],s55 offen offset:0  lds
s_add_u32 m0,s57,25600
s_nop 0
buffer_load_dwordx4 v19,s[40:43],s55 offen offset:0  lds
s_add_u32 m0,s57,32640
s_nop 0
buffer_load_dwordx4 v18,s[40:43],s55 offen offset:128  lds
s_add_u32 m0,s57,33664
s_nop 0
buffer_load_dwordx4 v19,s[40:43],s55 offen offset:128  lds
s_add_u32 m0,s57,40704
s_nop 0
buffer_load_dwordx4 v18,s[40:43],s55 offen offset:256  lds
s_add_u32 m0,s57,41728
s_nop 0
buffer_load_dwordx4 v19,s[40:43],s55 offen offset:256  lds
s_add_u32 s55,s50,917504
s_add_u32 m0,s57,49152
s_nop 0
buffer_load_dwordx4 v18,s[40:43],s55 offen offset:0  lds
s_add_u32 m0,s57,50176
s_nop 0
buffer_load_dwordx4 v19,s[40:43],s55 offen offset:0  lds
s_add_u32 m0,s57,57216
s_nop 0
buffer_load_dwordx4 v18,s[40:43],s55 offen offset:128  lds
s_add_u32 m0,s57,58240
s_nop 0
buffer_load_dwordx4 v19,s[40:43],s55 offen offset:128  lds
s_add_u32 m0,s57,65280
s_nop 0
buffer_load_dwordx4 v18,s[40:43],s55 offen offset:256  lds
s_add_u32 m0,s57,66304
s_nop 0
buffer_load_dwordx4 v19,s[40:43],s55 offen offset:256  lds
s_add_u32 s55,s50,1376256
s_add_u32 m0,s57,73728
s_nop 0
buffer_load_dwordx4 v18,s[40:43],s55 offen offset:0  lds
s_add_u32 m0,s57,74752
s_nop 0
buffer_load_dwordx4 v19,s[40:43],s55 offen offset:0  lds
s_add_u32 m0,s57,81792
s_nop 0
buffer_load_dwordx4 v18,s[40:43],s55 offen offset:128  lds
s_add_u32 m0,s57,82816
s_nop 0
buffer_load_dwordx4 v19,s[40:43],s55 offen offset:128  lds
s_add_u32 m0,s57,89856
s_nop 0
buffer_load_dwordx4 v18,s[40:43],s55 offen offset:256  lds
s_add_u32 m0,s57,90880
s_nop 0
buffer_load_dwordx4 v19,s[40:43],s55 offen offset:256  lds
s_add_u32 s55,s50,1835008
s_add_u32 m0,s57,98304
s_nop 0
buffer_load_dwordx4 v18,s[40:43],s55 offen offset:0  lds
s_add_u32 m0,s57,99328
s_nop 0
buffer_load_dwordx4 v19,s[40:43],s55 offen offset:0  lds
s_add_u32 m0,s57,106368
s_nop 0
buffer_load_dwordx4 v18,s[40:43],s55 offen offset:128  lds
s_add_u32 m0,s57,107392
s_nop 0
buffer_load_dwordx4 v19,s[40:43],s55 offen offset:128  lds
s_add_u32 m0,s57,114432
s_nop 0
buffer_load_dwordx4 v18,s[40:43],s55 offen offset:256  lds
s_add_u32 m0,s57,115456
s_nop 0
buffer_load_dwordx4 v19,s[40:43],s55 offen offset:256  lds
s_waitcnt vmcnt(24) lgkmcnt(0)
s_barrier
s_add_u32 s55,s50,2293760
s_add_u32 m0,s57,122880
s_nop 0
buffer_load_dwordx4 v18,s[40:43],s55 offen offset:0  lds
s_add_u32 m0,s57,123904
s_nop 0
buffer_load_dwordx4 v19,s[40:43],s55 offen offset:0  lds
s_add_u32 m0,s57,130944
s_nop 0
buffer_load_dwordx4 v18,s[40:43],s55 offen offset:128  lds
s_add_u32 m0,s57,131968
s_nop 0
buffer_load_dwordx4 v19,s[40:43],s55 offen offset:128  lds
s_add_u32 m0,s57,139008
s_nop 0
buffer_load_dwordx4 v18,s[40:43],s55 offen offset:256  lds
s_add_u32 m0,s57,140032
s_nop 0
buffer_load_dwordx4 v19,s[40:43],s55 offen offset:256  lds
v_add_u32 v26,0,v20
v_add_u32 v27,0,v21
v_add_u32 v28,0,v22
T a[0:1],26,0
T a[2:3],26,512
T a[4:5],27,0
T a[6:7],27,512
T a[8:9],28,0
T a[10:11],28,512
T a[12:13],26,4096
T a[14:15],26,4608
T a[16:17],27,4096
T a[18:19],27,4608
T a[20:21],28,4096
T a[22:23],28,4608
s_waitcnt vmcnt(24) lgkmcnt(0)
s_barrier
s_add_u32 s55,s50,2752512
s_add_u32 m0,s57,0
s_nop 0
buffer_load_dwordx4 v18,s[40:43],s55 offen offset:0  lds
s_add_u32 m0,s57,1024
s_nop 0
buffer_load_dwordx4 v19,s[40:43],s55 offen offset:0  lds
s_add_u32 m0,s57,8064
s_nop 0
buffer_load_dwordx4 v18,s[40:43],s55 offen offset:128  lds
s_add_u32 m0,s57,9088
s_nop 0
buffer_load_dwordx4 v19,s[40:43],s55 offen offset:128  lds
s_add_u32 m0,s57,16128
s_nop 0
buffer_load_dwordx4 v18,s[40:43],s55 offen offset:256  lds
s_add_u32 m0,s57,17152
s_nop 0
buffer_load_dwordx4 v19,s[40:43],s55 offen offset:256  lds
v_add_u32 v26,24576,v20
v_add_u32 v27,24576,v21
v_add_u32 v28,24576,v22
T a[24:25],26,0
T a[26:27],26,512
T a[28:29],27,0
T a[30:31],27,512
T a[32:33],28,0
T a[34:35],28,512
T a[36:37],26,4096
T a[38:39],26,4608
T a[40:41],27,4096
T a[42:43],27,4608
T a[44:45],28,4096
T a[46:47],28,4608
s_waitcnt vmcnt(24) lgkmcnt(0)
s_barrier
s_add_u32 s55,s50,3211264
s_add_u32 m0,s57,24576
s_nop 0
buffer_load_dwordx4 v18,s[40:43],s55 offen offset:0  lds
s_add_u32 m0,s57,25600
s_nop 0
buffer_load_dwordx4 v19,s[40:43],s55 offen offset:0  lds
s_add_u32 m0,s57,32640
s_nop 0
buffer_load_dwordx4 v18,s[40:43],s55 offen offset:128  lds
s_add_u32 m0,s57,33664
s_nop 0
buffer_load_dwordx4 v19,s[40:43],s55 offen offset:128  lds
s_add_u32 m0,s57,40704
s_nop 0
buffer_load_dwordx4 v18,s[40:43],s55 offen offset:256  lds
s_add_u32 m0,s57,41728
s_nop 0
buffer_load_dwordx4 v19,s[40:43],s55 offen offset:256  lds
v_add_u32 v26,49152,v20
v_add_u32 v27,49152,v21
v_add_u32 v28,49152,v22
T a[48:49],26,0
T a[50:51],26,512
T a[52:53],27,0
T a[54:55],27,512
T a[56:57],28,0
T a[58:59],28,512
T a[60:61],26,4096
T a[62:63],26,4608
T a[64:65],27,4096
T a[66:67],27,4608
T a[68:69],28,4096
T a[70:71],28,4608
s_waitcnt vmcnt(24) lgkmcnt(0)
s_barrier
s_add_u32 s55,s50,3670016
s_add_u32 m0,s57,49152
s_nop 0
buffer_load_dwordx4 v18,s[40:43],s55 offen offset:0  lds
s_add_u32 m0,s57,50176
s_nop 0
buffer_load_dwordx4 v19,s[40:43],s55 offen offset:0  lds
s_add_u32 m0,s57,57216
s_nop 0
buffer_load_dwordx4 v18,s[40:43],s55 offen offset:128  lds
s_add_u32 m0,s57,58240
s_nop 0
buffer_load_dwordx4 v19,s[40:43],s55 offen offset:128  lds
s_add_u32 m0,s57,65280
s_nop 0
buffer_load_dwordx4 v18,s[40:43],s55 offen offset:256  lds
s_add_u32 m0,s57,66304
s_nop 0
buffer_load_dwordx4 v19,s[40:43],s55 offen offset:256  lds
v_add_u32 v26,73728,v20
v_add_u32 v27,73728,v21
v_add_u32 v28,73728,v22
T a[72:73],26,0
T a[74:75],26,512
T a[76:77],27,0
T a[78:79],27,512
T a[80:81],28,0
T a[82:83],28,512
T a[84:85],26,4096
T a[86:87],26,4608
T a[88:89],27,4096
T a[90:91],27,4608
T a[92:93],28,4096
T a[94:95],28,4608
s_waitcnt vmcnt(24) lgkmcnt(0)
s_barrier
s_add_u32 s55,s50,4128768
s_add_u32 m0,s57,73728
s_nop 0
buffer_load_dwordx4 v18,s[40:43],s55 offen offset:0  lds
s_add_u32 m0,s57,74752
s_nop 0
buffer_load_dwordx4 v19,s[40:43],s55 offen offset:0  lds
s_add_u32 m0,s57,81792
s_nop 0
buffer_load_dwordx4 v18,s[40:43],s55 offen offset:128  lds
s_add_u32 m0,s57,82816
s_nop 0
buffer_load_dwordx4 v19,s[40:43],s55 offen offset:128  lds
s_add_u32 m0,s57,89856
s_nop 0
buffer_load_dwordx4 v18,s[40:43],s55 offen offset:256  lds
s_add_u32 m0,s57,90880
s_nop 0
buffer_load_dwordx4 v19,s[40:43],s55 offen offset:256  lds
v_add_u32 v26,98304,v20
v_add_u32 v27,98304,v21
v_add_u32 v28,98304,v22
T a[96:97],26,0
T a[98:99],26,512
T a[100:101],27,0
T a[102:103],27,512
T a[104:105],28,0
T a[106:107],28,512
T a[108:109],26,4096
T a[110:111],26,4608
T a[112:113],27,4096
T a[114:115],27,4608
T a[116:117],28,4096
T a[118:119],28,4608
s_waitcnt vmcnt(24) lgkmcnt(0)
s_barrier
s_add_u32 s55,s50,4587520
s_add_u32 m0,s57,98304
s_nop 0
buffer_load_dwordx4 v18,s[40:43],s55 offen offset:0  lds
s_add_u32 m0,s57,99328
s_nop 0
buffer_load_dwordx4 v19,s[40:43],s55 offen offset:0  lds
s_add_u32 m0,s57,106368
s_nop 0
buffer_load_dwordx4 v18,s[40:43],s55 offen offset:128  lds
s_add_u32 m0,s57,107392
s_nop 0
buffer_load_dwordx4 v19,s[40:43],s55 offen offset:128  lds
s_add_u32 m0,s57,114432
s_nop 0
buffer_load_dwordx4 v18,s[40:43],s55 offen offset:256  lds
s_add_u32 m0,s57,115456
s_nop 0
buffer_load_dwordx4 v19,s[40:43],s55 offen offset:256  lds
v_add_u32 v26,122880,v20
v_add_u32 v27,122880,v21
v_add_u32 v28,122880,v22
T a[120:121],26,0
T a[122:123],26,512
T a[124:125],27,0
T a[126:127],27,512
T a[128:129],28,0
T a[130:131],28,512
T a[132:133],26,4096
T a[134:135],26,4608
T a[136:137],27,4096
T a[138:139],27,4608
T a[140:141],28,4096
T a[142:143],28,4608
s_waitcnt vmcnt(24) lgkmcnt(0)
s_barrier
s_add_u32 s55,s50,5046272
s_add_u32 m0,s57,122880
s_nop 0
buffer_load_dwordx4 v18,s[40:43],s55 offen offset:0  lds
s_add_u32 m0,s57,123904
s_nop 0
buffer_load_dwordx4 v19,s[40:43],s55 offen offset:0  lds
s_add_u32 m0,s57,130944
s_nop 0
buffer_load_dwordx4 v18,s[40:43],s55 offen offset:128  lds
s_add_u32 m0,s57,131968
s_nop 0
buffer_load_dwordx4 v19,s[40:43],s55 offen offset:128  lds
s_add_u32 m0,s57,139008
s_nop 0
buffer_load_dwordx4 v18,s[40:43],s55 offen offset:256  lds
s_add_u32 m0,s57,140032
s_nop 0
buffer_load_dwordx4 v19,s[40:43],s55 offen offset:256  lds
v_add_u32 v26,0,v20
v_add_u32 v27,0,v21
v_add_u32 v28,0,v22
T a[144:145],26,0
T a[146:147],26,512
T a[148:149],27,0
T a[150:151],27,512
T a[152:153],28,0
T a[154:155],28,512
T a[156:157],26,4096
T a[158:159],26,4608
T a[160:161],27,4096
T a[162:163],27,4608
T a[164:165],28,4096
T a[166:167],28,4608
s_waitcnt vmcnt(24) lgkmcnt(0)
s_barrier
s_add_u32 s55,s50,5505024
s_add_u32 m0,s57,0
s_nop 0
buffer_load_dwordx4 v18,s[40:43],s55 offen offset:0  lds
s_add_u32 m0,s57,1024
s_nop 0
buffer_load_dwordx4 v19,s[40:43],s55 offen offset:0  lds
s_add_u32 m0,s57,8064
s_nop 0
buffer_load_dwordx4 v18,s[40:43],s55 offen offset:128  lds
s_add_u32 m0,s57,9088
s_nop 0
buffer_load_dwordx4 v19,s[40:43],s55 offen offset:128  lds
s_add_u32 m0,s57,16128
s_nop 0
buffer_load_dwordx4 v18,s[40:43],s55 offen offset:256  lds
s_add_u32 m0,s57,17152
s_nop 0
buffer_load_dwordx4 v19,s[40:43],s55 offen offset:256  lds
v_add_u32 v26,24576,v20
v_add_u32 v27,24576,v21
v_add_u32 v28,24576,v22
T a[168:169],26,0
T a[170:171],26,512
T a[172:173],27,0
T a[174:175],27,512
T a[176:177],28,0
T a[178:179],28,512
T a[180:181],26,4096
T a[182:183],26,4608
T a[184:185],27,4096
T a[186:187],27,4608
T a[188:189],28,4096
T a[190:191],28,4608
s_waitcnt vmcnt(24) lgkmcnt(0)
s_barrier
s_add_u32 s55,s50,5963776
s_add_u32 m0,s57,24576
s_nop 0
buffer_load_dwordx4 v18,s[40:43],s55 offen offset:0  lds
s_add_u32 m0,s57,25600
s_nop 0
buffer_load_dwordx4 v19,s[40:43],s55 offen offset:0  lds
s_add_u32 m0,s57,32640
s_nop 0
buffer_load_dwordx4 v18,s[40:43],s55 offen offset:128  lds
s_add_u32 m0,s57,33664
s_nop 0
buffer_load_dwordx4 v19,s[40:43],s55 offen offset:128  lds
s_add_u32 m0,s57,40704
s_nop 0
buffer_load_dwordx4 v18,s[40:43],s55 offen offset:256  lds
s_add_u32 m0,s57,41728
s_nop 0
buffer_load_dwordx4 v19,s[40:43],s55 offen offset:256  lds
v_add_u32 v26,49152,v20
v_add_u32 v27,49152,v21
v_add_u32 v28,49152,v22
T a[192:193],26,0
T a[194:195],26,512
T a[196:197],27,0
T a[198:199],27,512
T a[200:201],28,0
T a[202:203],28,512
T a[204:205],26,4096
T a[206:207],26,4608
T a[208:209],27,4096
T a[210:211],27,4608
T a[212:213],28,4096
T a[214:215],28,4608
s_waitcnt vmcnt(24) lgkmcnt(0)
s_barrier
s_add_u32 s55,s50,6422528
s_add_u32 m0,s57,49152
s_nop 0
buffer_load_dwordx4 v18,s[40:43],s55 offen offset:0  lds
s_add_u32 m0,s57,50176
s_nop 0
buffer_load_dwordx4 v19,s[40:43],s55 offen offset:0  lds
s_add_u32 m0,s57,57216
s_nop 0
buffer_load_dwordx4 v18,s[40:43],s55 offen offset:128  lds
s_add_u32 m0,s57,58240
s_nop 0
buffer_load_dwordx4 v19,s[40:43],s55 offen offset:128  lds
s_add_u32 m0,s57,65280
s_nop 0
buffer_load_dwordx4 v18,s[40:43],s55 offen offset:256  lds
s_add_u32 m0,s57,66304
s_nop 0
buffer_load_dwordx4 v19,s[40:43],s55 offen offset:256  lds
v_add_u32 v26,73728,v20
v_add_u32 v27,73728,v21
v_add_u32 v28,73728,v22
T a[216:217],26,0
T a[218:219],26,512
T a[220:221],27,0
T a[222:223],27,512
T a[224:225],28,0
)"
R"(T a[226:227],28,512
T a[228:229],26,4096
T a[230:231],26,4608
T a[232:233],27,4096
T a[234:235],27,4608
T a[236:237],28,4096
T a[238:239],28,4608
s_waitcnt vmcnt(24) lgkmcnt(0)
s_barrier
s_add_u32 s55,s50,6881280
s_add_u32 m0,s57,73728
s_nop 0
buffer_load_dwordx4 v18,s[40:43],s55 offen offset:0  lds
s_add_u32 m0,s57,74752
s_nop 0
buffer_load_dwordx4 v19,s[40:43],s55 offen offset:0  lds
s_add_u32 m0,s57,81792
s_nop 0
buffer_load_dwordx4 v18,s[40:43],s55 offen offset:128  lds
s_add_u32 m0,s57,82816
s_nop 0
buffer_load_dwordx4 v19,s[40:43],s55 offen offset:128  lds
s_add_u32 m0,s57,89856
s_nop 0
buffer_load_dwordx4 v18,s[40:43],s55 offen offset:256  lds
s_add_u32 m0,s57,90880
s_nop 0
buffer_load_dwordx4 v19,s[40:43],s55 offen offset:256  lds
v_add_u32 v26,98304,v20
v_add_u32 v27,98304,v21
v_add_u32 v28,98304,v22
T a[240:241],26,0
T a[242:243],26,512
T a[244:245],27,0
T a[246:247],27,512
T a[248:249],28,0
T a[250:251],28,512
T a[252:253],26,4096
T a[254:255],26,4608
T v[128:129],27,4096
T v[130:131],27,4608
T v[132:133],28,4096
T v[134:135],28,4608
s_waitcnt vmcnt(24) lgkmcnt(0)
s_barrier
v_add_u32 v26,122880,v20
v_add_u32 v27,122880,v21
v_add_u32 v28,122880,v22
T v[136:137],26,0
T v[138:139],26,512
T v[140:141],27,0
T v[142:143],27,512
T v[144:145],28,0
T v[146:147],28,512
T v[148:149],26,4096
T v[150:151],26,4608
T v[152:153],27,4096
T v[154:155],27,4608
T v[156:157],28,4096
T v[158:159],28,4608
s_waitcnt vmcnt(18) lgkmcnt(0)
s_barrier
v_add_u32 v26,0,v20
v_add_u32 v27,0,v21
v_add_u32 v28,0,v22
T v[160:161],26,0
T v[162:163],26,512
T v[164:165],27,0
T v[166:167],27,512
T v[168:169],28,0
T v[170:171],28,512
T v[172:173],26,4096
T v[174:175],26,4608
T v[176:177],27,4096
T v[178:179],27,4608
T v[180:181],28,4096
T v[182:183],28,4608
s_waitcnt vmcnt(12) lgkmcnt(0)
s_barrier
v_add_u32 v26,24576,v20
v_add_u32 v27,24576,v21
v_add_u32 v28,24576,v22
T v[184:185],26,0
T v[186:187],26,512
T v[188:189],27,0
T v[190:191],27,512
T v[192:193],28,0
T v[194:195],28,512
T v[196:197],26,4096
T v[198:199],26,4608
T v[200:201],27,4096
T v[202:203],27,4608
T v[204:205],28,4096
T v[206:207],28,4608
s_waitcnt vmcnt(6) lgkmcnt(0)
s_barrier
v_add_u32 v26,49152,v20
v_add_u32 v27,49152,v21
v_add_u32 v28,49152,v22
T v[208:209],26,0
T v[210:211],26,512
T v[212:213],27,0
T v[214:215],27,512
T v[216:217],28,0
T v[218:219],28,512
T v[220:221],26,4096
T v[222:223],26,4608
T v[224:225],27,4096
T v[226:227],27,4608
T v[228:229],28,4096
T v[230:231],28,4608
s_waitcnt vmcnt(0) lgkmcnt(0)
s_barrier
v_add_u32 v26,73728,v20
v_add_u32 v27,73728,v21
v_add_u32 v28,73728,v22
T v[232:233],26,0
T v[234:235],26,512
T v[236:237],27,0
T v[238:239],27,512
T v[240:241],28,0
T v[242:243],28,512
T v[244:245],26,4096
T v[246:247],26,4608
T v[248:249],27,4096
T v[250:251],27,4608
T v[252:253],28,4096
T v[254:255],28,4608
L 0
s_barrier
v_mov_b32 v42,0x3f803f80
v_mov_b32 v43,0x3f803f80
v_mov_b32 v44,0x3f803f80
v_mov_b32 v45,0x3f803f80
s_nop 1
v_mfma_f32_16x16x32_bf16 v[82:85],a[0:3],v[42:45],0
v_mfma_f32_16x16x32_bf16 v[86:89],a[4:7],v[42:45],0
v_mfma_f32_16x16x32_bf16 v[90:93],a[8:11],v[42:45],0
M 82,a[12:15],42
M 86,a[16:19],42
M 90,a[20:23],42
M 82,a[24:27],42
M 86,a[28:31],42
M 90,a[32:35],42
M 82,a[36:39],42
M 86,a[40:43],42
M 90,a[44:47],42
M 82,a[48:51],42
M 86,a[52:55],42
M 90,a[56:59],42
M 82,a[60:63],42
M 86,a[64:67],42
M 90,a[68:71],42
M 82,a[72:75],42
M 86,a[76:79],42
M 90,a[80:83],42
M 82,a[84:87],42
M 86,a[88:91],42
M 90,a[92:95],42
M 82,a[96:99],42
M 86,a[100:103],42
M 90,a[104:107],42
M 82,a[108:111],42
M 86,a[112:115],42
M 90,a[116:119],42
M 82,a[120:123],42
M 86,a[124:127],42
M 90,a[128:131],42
M 82,a[132:135],42
M 86,a[136:139],42
M 90,a[140:143],42
M 82,a[144:147],42
M 86,a[148:151],42
M 90,a[152:155],42
M 82,a[156:159],42
M 86,a[160:163],42
M 90,a[164:167],42
M 82,a[168:171],42
M 86,a[172:175],42
M 90,a[176:179],42
M 82,a[180:183],42
M 86,a[184:187],42
M 90,a[188:191],42
M 82,a[192:195],42
M 86,a[196:199],42
M 90,a[200:203],42
M 82,a[204:207],42
M 86,a[208:211],42
M 90,a[212:215],42
M 82,a[216:219],42
M 86,a[220:223],42
M 90,a[224:227],42
M 82,a[228:231],42
M 86,a[232:235],42
M 90,a[236:239],42
M 82,a[240:243],42
M 86,a[244:247],42
M 90,a[248:251],42
M 82,a[252:255],42
M 86,v[128:131],42
M 90,v[132:135],42
M 82,v[136:139],42
M 86,v[140:143],42
M 90,v[144:147],42
M 82,v[148:151],42
M 86,v[152:155],42
M 90,v[156:159],42
M 82,v[160:163],42
M 86,v[164:167],42
M 90,v[168:171],42
M 82,v[172:175],42
M 86,v[176:179],42
M 90,v[180:183],42
M 82,v[184:187],42
M 86,v[188:191],42
M 90,v[192:195],42
M 82,v[196:199],42
M 86,v[200:203],42
M 90,v[204:207],42
M 82,v[208:211],42
M 86,v[212:215],42
M 90,v[216:219],42
M 82,v[220:223],42
M 86,v[224:227],42
M 90,v[228:231],42
M 82,v[232:235],42
M 86,v[236:239],42
M 90,v[240:243],42
M 82,v[244:247],42
M 86,v[248:251],42
M 90,v[252:255],42
v_mov_b32 v4,s53
s_mov_b32 s82,0
s_mov_b32 s80,0
s_mov_b32 s54,s45
s_mul_i32 s57,s44,4096
s_mul_i32 s55,s45,32768
s_add_u32 s55,s55,s57
s_add_u32 s55,s55,31457280
s_mul_i32 s57,s44,4096
s_mul_i32 s60,s46,32768
s_mul_i32 s61,s46,1024
s_mul_i32 s62,s46,114688
s_mul_i32 s63,s46,2560
s_mul_i32 s64,s46,1024
s_lshl_b32 s59,s44,8
s_add_u32 s59,s59,131072
s_mul_i32 s58,s45,1024
s_add_u32 s58,s58,109613056
s_mul_i32 s65,s45,114688
s_add_u32 s65,s65,s47
s_mul_i32 s68,s45,2560
s_add_u32 s68,s68,s48
s_mul_i32 s73,s45,1024
s_add_u32 s73,s73,107479040
s_mul_i32 s74,s45,32768
s_add_u32 s74,s74,s49
s_mul_i32 s75,s45,1024
s_add_u32 s75,s75,107479040
s_mov_b32 s66,0x7fff0000
s_mov_b32 s67,0x7fff0000
s_mov_b32 s69,0x7fff0000
s_mov_b32 s76,0x7fff0000
s_mov_b32 s77,0x7fff0000
s_mov_b32 s70,0x7fff0000
s_mov_b32 s71,0x7fff0000
s_mov_b32 s72,0x7fff0000
v_mov_b32 v1,v0
v_add_u32 v2,16384,v0
s_mov_b32 s84,16384
s_mov_b32 s86,0x3a800000
s_mov_b32 s87,0x3727c5ac
s_mov_b32 s88,0xc0135761
s_mov_b32 s89,0x3d372713
s_lshr_b32 s85,s44,1
s_mul_i32 s85,s85,127
v_lshlrev_b32 v8,22,v0
v_and_b32 v8,0x80000000,v8
v_or_b32 v8,v8,v5
v_add_u32 v2,32768,v0
s_lshl_b32 s84,s57,1
s_add_u32 s84,s84,32768
v_and_b32 v94,240,v0
v_cmp_eq_u32 vcc,0,v94
s_nop 1
v_cndmask_b32 v17,0,1.0,vcc
s_getpc_b64 s[40:41]
Lg3_pc_%=:
s_lshl_b32 s78,s44,13
v_lshlrev_b32 v94,3,v0
v_add_u32 v94,s78,v94
v_min_u32 v94,Lcode_g3_e-Lg3_pc_%=-4,v94
s_mov_b32 m0,163584
s_nop 0
global_load_lds_dword v94,s[40:41]
s_add_u32 m0,s59,4096
s_nop 0
buffer_load_dword v8,s[36:39],s58 offen offset:0 sc1 lds
buffer_store_dword v4,v3,s[36:39],s71 offen offset:0 sc0 sc1
s_add_u32 m0,s59,5120
s_nop 0
buffer_load_dword v8,s[36:39],s58 offen offset:0 sc1 lds
buffer_store_dword v4,v3,s[36:39],s71 offen offset:0 sc0 sc1
s_mov_b32 s83,0
Lg3_poll_%=:
s_add_u32 s78,s83,2
s_and_b32 s78,s78,3
s_lshl_b32 s78,s78,10
s_add_u32 s78,s78,4096
s_add_u32 m0,s59,s78
s_nop 0
buffer_load_dword v8,s[36:39],s58 offen offset:0 sc1 lds
s_and_b32 s78,s83,3
s_lshl_b32 s78,s78,10
v_add_u32 v94,s78,v6
s_waitcnt vmcnt(4)
s_barrier
ds_read_b32 v7,v94 offset:4096
buffer_store_dword v4,v3,s[36:39],s70 offen offset:0 sc0 sc1
s_mov_b32 s70,s71
s_mov_b32 s71,s72
s_mov_b32 s72,0x7fff0000
s_add_u32 s83,s83,1
s_add_u32 s82,s82,1
L 0
v_cmp_eq_u32 vcc,s53,v7
s_nop 0
s_cmp_eq_u32 vcc_lo,-1
s_cbranch_scc1 Lg3_go_%=
s_cmp_lt_u32 s82,20000
s_cbranch_scc1 Lg3_poll_%=
Lg3_go_%=:
buffer_load_dword v114,v11,s[36:39],s73 offen offset:0 sc1
s_add_u32 s73,s73,s64
s_add_u32 m0,s57,0
s_add_u32 s56,s55,0
D 0,56,0
D 0,56,1024
D 0,56,2048
D 0,56,3072
s_add_u32 s58,s58,s61
s_add_u32 m0,s59,2048
s_nop 0
buffer_load_dword v8,s[36:39],s58 offen offset:0 sc1 lds
s_add_u32 m0,s57,16384
s_add_u32 s56,s55,16384
D 0,56,0
D 0,56,1024
D 0,56,2048
D 0,56,3072
s_add_u32 s55,s55,s60
s_sub_u32 s57,s84,s57
s_add_u32 s54,s54,s46
s_waitcnt vmcnt(4)
s_barrier
buffer_store_dword v4,v3,s[36:39],s70 offen offset:0 sc0 sc1
s_min_u32 s71,s71,s72
buffer_store_dword v4,v3,s[36:39],s71 offen offset:0 sc0 sc1
s_mov_b32 s70,0x7fff0000
s_mov_b32 s71,0x7fff0000
s_mov_b32 s72,0x7fff0000
s_cmp_eq_u32 s80,1
buffer_store_dword v4,v3,s[36:39],s71 offen offset:0 sc0 sc1
s_cbranch_scc1 Lg3_st1_%=
s_branch Lg3_st0_%=
Lg3_gox_%=:
s_add_u32 m0,s59,2048
s_nop 0
buffer_load_dword v8,s[36:39],s58 offen offset:0 sc1 lds
s_waitcnt vmcnt(0)
s_barrier
buffer_store_dword v4,v3,s[36:39],s70 offen offset:0 sc0 sc1
s_min_u32 s71,s71,s72
buffer_store_dword v4,v3,s[36:39],s71 offen offset:0 sc0 sc1
s_mov_b32 s70,0x7fff0000
s_mov_b32 s71,0x7fff0000
s_mov_b32 s72,0x7fff0000
s_cmp_eq_u32 s80,1
s_cbranch_scc1 Lg3_st1_%=
Lg3_st0_%=:
ds_read_b32 v7,v6 offset:2048
R 42,1,0
R 46,1,1024
R 50,1,2048
R 54,1,3072
R 58,1,4096
L 5
v_cmp_eq_u32 vcc,s53,v7
L 3
s_cmp_eq_u32 vcc_lo,-1
s_cbranch_scc1 Lg3_F0_%=
s_branch Lg3_L0_%=
Lg3_st1_%=:
ds_read_b32 v7,v6 offset:2048
R 42,2,0
R 46,2,1024
R 50,2,2048
R 54,2,3072
R 58,2,4096
L 5
v_cmp_eq_u32 vcc,s53,v7
L 3
s_cmp_eq_u32 vcc_lo,-1
s_cbranch_scc1 Lg3_F1_%=
s_branch Lg3_L1_%=
Lg3_F0_%=:
v_mfma_f32_16x16x32_bf16 v[26:29],a[8:11],v[42:45],0
s_add_u32 m0,s57,0
R 62,1,5120
v_add_f32_dpp v115,v114,v114 quad_perm:[1,0,3,2] row_mask:0xf bank_mask:0xf
M 26,a[20:23],46
buffer_load_dword v114,v11,s[36:39],s73 offen offset:0 sc1
v_mfma_f32_16x16x32_bf16 v[22:25],a[4:7],v[42:45],0
s_add_u32 s56,s55,0
v_add_f32_dpp v115,v115,v115 quad_perm:[2,3,0,1] row_mask:0xf bank_mask:0xf
v_fma_f32 v30,-v78,v82,v30
M 22,a[16:19],46
s_add_u32 s58,s58,s61
R 66,1,6144
v_fma_f32 v31,-v78,v83,v31
v_mfma_f32_16x16x32_bf16 v[18:21],a[0:3],v[42:45],0
D 0,56,0
M 18,a[12:15],46
L 3
v_add_f32_dpp v118,v115,v115 row_half_mirror row_mask:0xf bank_mask:0xf
v_fma_f32 v32,-v78,v84,v32
M 18,a[24:27],50
s_mov_b32 s66,s65
R 70,1,7168
v_fma_f32 v33,-v78,v85,v33
M 18,a[36:39],54
s_add_u32 s65,s65,s62
v_mov_b32_dpp v115,v118 row_ror:8 row_mask:0xf bank_mask:0xf
v_fma_f32 v115,-v118,v118,v115
M 22,a[28:31],50
s_add_u32 s54,s54,s46
v_max_f32 v115,s87,v115
v_mul_f32 v118,v17,v118
M 22,a[40:43],54
s_add_u32 s73,s73,s64
R 42,1,8192
v_mul_f32 v94,v30,v30
M 26,a[32:35],50
v_rsq_f32 v116,v115
M 26,a[44:47],54
L 3
D 0,56,1024
M 26,a[56:59],58
R 46,1,9216
v_fma_f32 v115,v15,v115,v16
M 26,a[68:71],62
v_mul_f32 v119,v116,v116
v_fma_f32 v115,v14,v119,v115
M 22,a[52:55],58
v_fma_f32 v118,v116,v115,v118
v_mul_f32 v95,v31,v31
M 22,a[64:67],62
R 50,1,10240
v_mul_f32 v96,v32,v32
M 18,a[48:51],58
ds_write_b32 v12,v118 offset:0
M 18,a[60:63],62
L 4
v_mul_f32 v97,v33,v33
v_fma_f32 v94,v80,v94,v79
M 18,a[72:75],66
R 54,1,11264
v_fma_f32 v95,v80,v95,v79
M 18,a[84:87],70
v_fma_f32 v96,v80,v96,v79
v_fma_f32 v97,v80,v97,v79
M 22,a[76:79],66
v_mul_f32 v94,v30,v94
v_mul_f32 v95,v31,v95
M 22,a[88:91],70
R 58,1,12288
v_mul_f32 v96,v32,v96
M 26,a[80:83],66
D 0,56,2048
M 26,a[92:95],70
L 4
v_exp_f32 v94,v94
M 26,a[104:107],42
R 62,1,13312
v_mul_f32 v97,v33,v97
M 26,a[116:119],46
v_exp_f32 v95,v95
M 22,a[100:103],42
v_exp_f32 v96,v96
M 22,a[112:115],46
R 66,1,14336
v_fma_f32 v94,v94,v81,v81
M 18,a[96:99],42
v_exp_f32 v97,v97
M 18,a[108:111],46
L 3
v_rcp_f32 v94,v94
M 18,a[120:123],50
R 70,1,15360
v_fma_f32 v95,v95,v81,v81
)"
R"(M 18,a[132:135],54
s_waitcnt vmcnt(7)
D 0,56,3072
M 22,a[124:127],50
s_barrier
v_rcp_f32 v95,v95
M 22,a[136:139],54
s_add_u32 m0,s59,0
R 42,1,16384
v_fma_f32 v96,v96,v81,v81
M 26,a[128:131],50
s_add_u32 s56,s55,16384
v_rcp_f32 v96,v96
M 26,a[140:143],54
L 3
v_fma_f32 v97,v97,v81,v81
v_mul_f32 v30,v30,v94
M 26,a[152:155],58
s_add_u32 s55,s55,s60
R 46,1,17408
v_mul_f32 v31,v31,v95
M 26,a[164:167],62
v_rcp_f32 v97,v97
M 22,a[148:151],58
v_mul_f32 v32,v32,v96
v_mul_f32 v33,v33,v97
M 22,a[160:163],62
R 50,1,18432
v_cvt_pk_bf16_f32 v106,v30,v31
M 18,a[144:147],58
buffer_load_dword v8,s[36:39],s58 offen offset:0 sc1 lds
M 18,a[156:159],62
L 3
v_cvt_pk_bf16_f32 v107,v32,v33
v_fma_f32 v34,-v78,v86,v34
M 18,a[168:171],66
s_add_u32 m0,s57,16384
R 54,1,19456
v_fma_f32 v35,-v78,v87,v35
M 18,a[180:183],70
s_sub_u32 s57,s84,s57
v_fma_f32 v36,-v78,v88,v36
v_fma_f32 v37,-v78,v89,v37
M 22,a[172:175],66
v_mul_f32 v98,v34,v34
v_mul_f32 v99,v35,v35
M 22,a[184:187],70
R 58,1,20480
v_mul_f32 v100,v36,v36
M 26,a[176:179],66
D 0,56,0
M 26,a[188:191],70
L 3
v_mul_f32 v101,v37,v37
v_fma_f32 v98,v80,v98,v79
M 26,a[200:203],42
R 62,1,21504
v_fma_f32 v99,v80,v99,v79
M 26,a[212:215],46
v_fma_f32 v100,v80,v100,v79
v_fma_f32 v101,v80,v101,v79
M 22,a[196:199],42
v_mul_f32 v98,v34,v98
v_mul_f32 v99,v35,v99
M 22,a[208:211],46
R 66,1,22528
v_mul_f32 v100,v36,v100
M 18,a[192:195],42
v_exp_f32 v98,v98
M 18,a[204:207],46
L 3
v_exp_f32 v99,v99
M 18,a[216:219],50
R 70,1,23552
v_mul_f32 v101,v37,v101
M 18,a[228:231],54
D 0,56,1024
M 22,a[220:223],50
v_exp_f32 v100,v100
M 22,a[232:235],54
R 42,1,24576
v_fma_f32 v98,v98,v81,v81
M 26,a[224:227],50
v_exp_f32 v101,v101
M 26,a[236:239],54
L 3
v_rcp_f32 v98,v98
M 26,a[248:251],58
R 46,1,25600
v_fma_f32 v99,v99,v81,v81
M 26,v[132:135],62
v_rcp_f32 v99,v99
M 22,a[244:247],58
v_fma_f32 v100,v100,v81,v81
v_fma_f32 v101,v101,v81,v81
M 22,v[128:131],62
R 50,1,26624
v_mul_f32 v34,v34,v98
M 18,a[240:243],58
v_rcp_f32 v100,v100
M 18,a[252:255],62
L 3
v_rcp_f32 v101,v101
M 18,v[136:139],66
R 54,1,27648
v_mul_f32 v35,v35,v99
M 18,v[148:151],70
D 0,56,2048
M 22,v[140:143],66
v_mul_f32 v36,v36,v100
v_mul_f32 v37,v37,v101
M 22,v[152:155],70
R 58,1,28672
v_cvt_pk_bf16_f32 v108,v34,v35
M 26,v[144:147],66
v_cvt_pk_bf16_f32 v109,v36,v37
v_fma_f32 v38,-v78,v90,v38
M 26,v[156:159],70
L 3
v_fma_f32 v39,-v78,v91,v39
v_fma_f32 v40,-v78,v92,v40
M 26,v[168:171],42
R 62,1,29696
v_fma_f32 v41,-v78,v93,v41
M 26,v[180:183],46
v_mul_f32 v102,v38,v38
v_mul_f32 v103,v39,v39
M 22,v[164:167],42
v_mul_f32 v104,v40,v40
v_mul_f32 v105,v41,v41
M 22,v[176:179],46
R 66,1,30720
v_fma_f32 v102,v80,v102,v79
M 18,v[160:163],42
v_fma_f32 v103,v80,v103,v79
v_fma_f32 v104,v80,v104,v79
M 18,v[172:175],46
L 3
D 0,56,3072
M 18,v[184:187],50
s_nop 0
R 70,1,31744
v_fma_f32 v105,v80,v105,v79
M 18,v[196:199],54
s_waitcnt vmcnt(4)
v_mul_f32 v102,v38,v102
v_mul_f32 v103,v39,v103
M 22,v[188:191],50
s_barrier
buffer_store_dword v4,v3,s[36:39],s70 offen offset:0 sc0 sc1
M 22,v[200:203],54
s_mov_b32 s70,s69
R 42,2,0
ds_read_b32 v7,v6 offset:0
M 26,v[192:195],50
s_mov_b32 s69,s68
R 74,13,0
v_mul_f32 v104,v40,v104
M 26,v[204:207],54
L 5
v_exp_f32 v102,v102
M 26,v[216:219],58
s_add_u32 s68,s68,s63
R 46,2,1024
v_mul_f32 v105,v41,v105
M 26,v[228:231],62
v_exp_f32 v103,v103
M 22,v[212:215],58
buffer_store_dwordx4 v[106:109],v0,s[36:39],s67 offen offset:0 sc0 sc1
v_fma_f32 v102,v102,v81,v81
M 22,v[224:227],62
L 2
R 50,2,2048
v_fma_f32 v103,v103,v81,v81
M 18,v[208:211],58
v_exp_f32 v104,v104
M 18,v[220:223],62
s_nop 0
v_exp_f32 v105,v105
M 18,v[232:235],66
R 54,2,3072
v_fma_f32 v104,v104,v81,v81
M 18,v[244:247],70
v_fma_f32 v105,v105,v81,v81
v_cmp_eq_u32 vcc,s53,v7
M 22,v[236:239],66
v_rcp_f32 v102,v102
v_rcp_f32 v103,v103
v_rcp_f32 v104,v104
v_rcp_f32 v105,v105
M 22,v[248:251],70
s_cmp_eq_u32 vcc_lo,-1
R 58,2,4096
v_mul_f32 v38,v38,v102
v_mul_f32 v39,v39,v103
v_mul_f32 v40,v40,v104
v_mul_f32 v41,v41,v105
v_cvt_pk_bf16_f32 v110,v38,v39
v_cvt_pk_bf16_f32 v111,v40,v41
M 26,v[240:243],66
L 3
buffer_store_dwordx2 v[110:111],v10,s[36:39],s67 offen offset:0 sc0 sc1
M 26,v[252:255],70
s_cbranch_scc0 Lg3_K1_%=
Lg3_F1_%=:
v_mfma_f32_16x16x32_bf16 v[38:41],a[8:11],v[42:45],0
s_add_u32 m0,s57,0
R 62,2,5120
v_add_f32_dpp v115,v114,v114 quad_perm:[1,0,3,2] row_mask:0xf bank_mask:0xf
M 38,a[20:23],46
buffer_load_dword v114,v11,s[36:39],s73 offen offset:0 sc1
v_mfma_f32_16x16x32_bf16 v[34:37],a[4:7],v[42:45],0
s_add_u32 s56,s55,0
v_add_f32_dpp v115,v115,v115 quad_perm:[2,3,0,1] row_mask:0xf bank_mask:0xf
v_fma_f32 v18,-v74,v82,v18
M 34,a[16:19],46
s_add_u32 s58,s58,s61
R 66,2,6144
v_fma_f32 v19,-v74,v83,v19
v_mfma_f32_16x16x32_bf16 v[30:33],a[0:3],v[42:45],0
D 0,56,0
M 30,a[12:15],46
L 3
v_add_f32_dpp v118,v115,v115 row_half_mirror row_mask:0xf bank_mask:0xf
v_fma_f32 v20,-v74,v84,v20
M 30,a[24:27],50
s_mov_b32 s67,s65
R 70,2,7168
v_fma_f32 v21,-v74,v85,v21
M 30,a[36:39],54
s_add_u32 s65,s65,s62
v_mov_b32_dpp v115,v118 row_ror:8 row_mask:0xf bank_mask:0xf
v_fma_f32 v115,-v118,v118,v115
M 34,a[28:31],50
s_add_u32 s54,s54,s46
v_max_f32 v115,s87,v115
v_mul_f32 v118,v17,v118
M 34,a[40:43],54
s_add_u32 s73,s73,s64
R 42,2,8192
v_mul_f32 v94,v18,v18
M 38,a[32:35],50
v_rsq_f32 v116,v115
M 38,a[44:47],54
L 3
D 0,56,1024
M 38,a[56:59],58
R 46,2,9216
v_fma_f32 v115,v15,v115,v16
M 38,a[68:71],62
v_mul_f32 v119,v116,v116
v_fma_f32 v115,v14,v119,v115
M 34,a[52:55],58
v_fma_f32 v118,v116,v115,v118
v_mul_f32 v95,v19,v19
M 34,a[64:67],62
R 50,2,10240
v_mul_f32 v96,v20,v20
M 30,a[48:51],58
ds_write_b32 v12,v118 offset:2048
M 30,a[60:63],62
L 4
v_mul_f32 v97,v21,v21
v_fma_f32 v94,v76,v94,v75
M 30,a[72:75],66
R 54,2,11264
v_fma_f32 v95,v76,v95,v75
M 30,a[84:87],70
v_fma_f32 v96,v76,v96,v75
v_fma_f32 v97,v76,v97,v75
M 34,a[76:79],66
v_mul_f32 v94,v18,v94
v_mul_f32 v95,v19,v95
M 34,a[88:91],70
R 58,2,12288
v_mul_f32 v96,v20,v96
M 38,a[80:83],66
D 0,56,2048
M 38,a[92:95],70
L 4
v_exp_f32 v94,v94
M 38,a[104:107],42
R 62,2,13312
v_mul_f32 v97,v21,v97
M 38,a[116:119],46
v_exp_f32 v95,v95
M 34,a[100:103],42
v_exp_f32 v96,v96
M 34,a[112:115],46
R 66,2,14336
v_fma_f32 v94,v94,v77,v77
M 30,a[96:99],42
v_exp_f32 v97,v97
M 30,a[108:111],46
L 3
v_rcp_f32 v94,v94
M 30,a[120:123],50
R 70,2,15360
v_fma_f32 v95,v95,v77,v77
M 30,a[132:135],54
s_waitcnt vmcnt(7)
D 0,56,3072
M 34,a[124:127],50
s_barrier
v_rcp_f32 v95,v95
M 34,a[136:139],54
s_add_u32 m0,s59,1024
R 42,2,16384
v_fma_f32 v96,v96,v77,v77
M 38,a[128:131],50
s_add_u32 s56,s55,16384
v_rcp_f32 v96,v96
M 38,a[140:143],54
L 3
v_fma_f32 v97,v97,v77,v77
v_mul_f32 v18,v18,v94
M 38,a[152:155],58
s_add_u32 s55,s55,s60
R 46,2,17408
v_mul_f32 v19,v19,v95
M 38,a[164:167],62
v_rcp_f32 v97,v97
M 34,a[148:151],58
v_mul_f32 v20,v20,v96
v_mul_f32 v21,v21,v97
M 34,a[160:163],62
R 50,2,18432
v_cvt_pk_bf16_f32 v106,v18,v19
M 30,a[144:147],58
buffer_load_dword v8,s[36:39],s58 offen offset:0 sc1 lds
M 30,a[156:159],62
L 3
v_cvt_pk_bf16_f32 v107,v20,v21
v_fma_f32 v22,-v74,v86,v22
M 30,a[168:171],66
s_add_u32 m0,s57,16384
R 54,2,19456
v_fma_f32 v23,-v74,v87,v23
M 30,a[180:183],70
s_sub_u32 s57,s84,s57
v_fma_f32 v24,-v74,v88,v24
v_fma_f32 v25,-v74,v89,v25
M 34,a[172:175],66
v_mul_f32 v98,v22,v22
v_mul_f32 v99,v23,v23
M 34,a[184:187],70
R 58,2,20480
v_mul_f32 v100,v24,v24
M 38,a[176:179],66
D 0,56,0
M 38,a[188:191],70
L 3
v_mul_f32 v101,v25,v25
v_fma_f32 v98,v76,v98,v75
M 38,a[200:203],42
R 62,2,21504
v_fma_f32 v99,v76,v99,v75
M 38,a[212:215],46
v_fma_f32 v100,v76,v100,v75
v_fma_f32 v101,v76,v101,v75
M 34,a[196:199],42
v_mul_f32 v98,v22,v98
v_mul_f32 v99,v23,v99
M 34,a[208:211],46
R 66,2,22528
v_mul_f32 v100,v24,v100
M 30,a[192:195],42
v_exp_f32 v98,v98
M 30,a[204:207],46
L 3
v_exp_f32 v99,v99
M 30,a[216:219],50
R 70,2,23552
v_mul_f32 v101,v25,v101
M 30,a[228:231],54
D 0,56,1024
M 34,a[220:223],50
v_exp_f32 v100,v100
M 34,a[232:235],54
R 42,2,24576
v_fma_f32 v98,v98,v77,v77
M 38,a[224:227],50
v_exp_f32 v101,v101
M 38,a[236:239],54
L 3
v_rcp_f32 v98,v98
M 38,a[248:251],58
R 46,2,25600
v_fma_f32 v99,v99,v77,v77
M 38,v[132:135],62
v_rcp_f32 v99,v99
M 34,a[244:247],58
v_fma_f32 v100,v100,v77,v77
v_fma_f32 v101,v101,v77,v77
M 34,v[128:131],62
R 50,2,26624
v_mul_f32 v22,v22,v98
M 30,a[240:243],58
v_rcp_f32 v100,v100
M 30,a[252:255],62
L 3
v_rcp_f32 v101,v101
M 30,v[136:139],66
R 54,2,27648
v_mul_f32 v23,v23,v99
M 30,v[148:151],70
D 0,56,2048
M 34,v[140:143],66
v_mul_f32 v24,v24,v100
v_mul_f32 v25,v25,v101
M 34,v[152:155],70
R 58,2,28672
v_cvt_pk_bf16_f32 v108,v22,v23
M 38,v[144:147],66
v_cvt_pk_bf16_f32 v109,v24,v25
v_fma_f32 v26,-v74,v90,v26
M 38,v[156:159],70
L 3
v_fma_f32 v27,-v74,v91,v27
v_fma_f32 v28,-v74,v92,v28
M 38,v[168:171],42
R 62,2,29696
v_fma_f32 v29,-v74,v93,v29
M 38,v[180:183],46
v_mul_f32 v102,v26,v26
v_mul_f32 v103,v27,v27
M 34,v[164:167],42
v_mul_f32 v104,v28,v28
v_mul_f32 v105,v29,v29
M 34,v[176:179],46
R 66,2,30720
v_fma_f32 v102,v76,v102,v75
M 30,v[160:163],42
v_fma_f32 v103,v76,v103,v75
v_fma_f32 v104,v76,v104,v75
M 30,v[172:175],46
L 3
D 0,56,3072
M 30,v[184:187],50
s_nop 0
R 70,2,31744
v_fma_f32 v105,v76,v105,v75
M 30,v[196:199],54
s_waitcnt vmcnt(4)
v_mul_f32 v102,v26,v102
v_mul_f32 v103,v27,v103
M 34,v[188:191],50
s_barrier
buffer_store_dword v4,v3,s[36:39],s70 offen offset:0 sc0 sc1
M 34,v[200:203],54
s_mov_b32 s70,s69
R 42,1,0
ds_read_b32 v7,v6 offset:1024
M 38,v[192:195],50
s_mov_b32 s69,s68
R 78,13,2048
v_mul_f32 v104,v28,v104
M 38,v[204:207],54
L 5
v_exp_f32 v102,v102
M 38,v[216:219],58
s_add_u32 s68,s68,s63
R 46,1,1024
v_mul_f32 v105,v29,v105
M 38,v[228:231],62
v_exp_f32 v103,v103
M 34,v[212:215],58
buffer_store_dwordx4 v[106:109],v0,s[36:39],s66 offen offset:0 sc0 sc1
v_fma_f32 v102,v102,v77,v77
M 34,v[224:227],62
L 2
R 50,1,2048
v_fma_f32 v103,v103,v77,v77
M 30,v[208:211],58
v_exp_f32 v104,v104
M 30,v[220:223],62
s_nop 0
v_exp_f32 v105,v105
M 30,v[232:235],66
R 54,1,3072
v_fma_f32 v104,v104,v77,v77
M 30,v[244:247],70
v_fma_f32 v105,v105,v77,v77
v_cmp_eq_u32 vcc,s53,v7
M 34,v[236:239],66
v_rcp_f32 v102,v102
v_rcp_f32 v103,v103
v_rcp_f32 v104,v104
v_rcp_f32 v105,v105
M 34,v[248:251],70
s_cmp_eq_u32 vcc_lo,-1
R 58,1,4096
v_mul_f32 v26,v26,v102
v_mul_f32 v27,v27,v103
v_mul_f32 v28,v28,v104
v_mul_f32 v29,v29,v105
v_cvt_pk_bf16_f32 v110,v26,v27
v_cvt_pk_bf16_f32 v111,v28,v29
M 38,v[240:243],66
L 3
)"
R"(buffer_store_dwordx2 v[110:111],v10,s[36:39],s66 offen offset:0 sc0 sc1
M 38,v[252:255],70
s_cbranch_scc1 Lg3_F0_%=
Lg3_K0_%=:
Lg3_L0_%=:
v_mfma_f32_16x16x32_bf16 v[26:29],a[8:11],v[42:45],0
s_add_u32 m0,s59,3072
R 62,1,5120
v_add_f32_dpp v115,v114,v114 quad_perm:[1,0,3,2] row_mask:0xf bank_mask:0xf
M 26,a[20:23],46
buffer_load_dword v8,s[36:39],s58 offen offset:0 sc1 lds
v_mfma_f32_16x16x32_bf16 v[22:25],a[4:7],v[42:45],0
s_mov_b32 s66,s65
v_add_f32_dpp v115,v115,v115 quad_perm:[2,3,0,1] row_mask:0xf bank_mask:0xf
v_fma_f32 v30,-v78,v82,v30
M 22,a[16:19],46
s_add_u32 s65,s65,s62
R 66,1,6144
v_fma_f32 v31,-v78,v83,v31
v_mfma_f32_16x16x32_bf16 v[18:21],a[0:3],v[42:45],0
v_add_f32_dpp v118,v115,v115 row_half_mirror row_mask:0xf bank_mask:0xf
v_fma_f32 v32,-v78,v84,v32
v_fma_f32 v33,-v78,v85,v33
M 18,a[12:15],46
L 3
v_mul_f32 v94,v30,v30
v_mul_f32 v95,v31,v31
M 18,a[24:27],50
R 70,1,7168
v_mov_b32_dpp v115,v118 row_ror:8 row_mask:0xf bank_mask:0xf
M 18,a[36:39],54
v_fma_f32 v115,-v118,v118,v115
v_max_f32 v115,s87,v115
M 22,a[28:31],50
v_rsq_f32 v116,v115
M 22,a[40:43],54
R 42,1,8192
v_fma_f32 v115,v15,v115,v16
v_mul_f32 v118,v17,v118
M 26,a[32:35],50
v_mul_f32 v119,v116,v116
v_fma_f32 v115,v14,v119,v115
M 26,a[44:47],54
L 3
v_fma_f32 v118,v116,v115,v118
v_mul_f32 v96,v32,v32
M 26,a[56:59],58
R 46,1,9216
v_mul_f32 v97,v33,v33
M 26,a[68:71],62
ds_write_b32 v12,v118 offset:0
M 22,a[52:55],58
v_fma_f32 v94,v80,v94,v79
v_fma_f32 v95,v80,v95,v79
v_fma_f32 v96,v80,v96,v79
M 22,a[64:67],62
R 50,1,10240
v_fma_f32 v97,v80,v97,v79
M 18,a[48:51],58
v_mul_f32 v94,v30,v94
v_mul_f32 v95,v31,v95
M 18,a[60:63],62
L 4
v_exp_f32 v94,v94
M 18,a[72:75],66
R 54,1,11264
v_mul_f32 v96,v32,v96
M 18,a[84:87],70
v_exp_f32 v95,v95
v_mul_f32 v97,v33,v97
M 22,a[76:79],66
v_exp_f32 v96,v96
M 22,a[88:91],70
R 58,1,12288
v_fma_f32 v94,v94,v81,v81
M 26,a[80:83],66
v_exp_f32 v97,v97
M 26,a[92:95],70
L 4
v_rcp_f32 v94,v94
M 26,a[104:107],42
R 62,1,13312
v_fma_f32 v95,v95,v81,v81
v_fma_f32 v96,v96,v81,v81
M 26,a[116:119],46
v_rcp_f32 v95,v95
M 22,a[100:103],42
v_rcp_f32 v96,v96
M 22,a[112:115],46
R 66,1,14336
v_fma_f32 v97,v97,v81,v81
M 18,a[96:99],42
v_rcp_f32 v97,v97
M 18,a[108:111],46
L 3
v_mul_f32 v30,v30,v94
v_mul_f32 v31,v31,v95
v_mul_f32 v32,v32,v96
M 18,a[120:123],50
R 70,1,15360
v_mul_f32 v33,v33,v97
M 18,a[132:135],54
s_waitcnt vmcnt(0)
v_cvt_pk_bf16_f32 v106,v30,v31
v_cvt_pk_bf16_f32 v107,v32,v33
M 22,a[124:127],50
s_barrier
ds_read_b32 v7,v6 offset:3072
v_fma_f32 v34,-v78,v86,v34
M 22,a[136:139],54
R 42,1,16384
v_fma_f32 v35,-v78,v87,v35
M 26,a[128:131],50
buffer_store_dword v4,v3,s[36:39],s70 offen offset:0 sc0 sc1
v_fma_f32 v36,-v78,v88,v36
M 26,a[140:143],54
L 4
v_fma_f32 v37,-v78,v89,v37
v_mul_f32 v98,v34,v34
M 26,a[152:155],58
s_mov_b32 s70,0x7fff0000
R 46,1,17408
v_mul_f32 v99,v35,v35
M 26,a[164:167],62
v_mul_f32 v100,v36,v36
v_mul_f32 v101,v37,v37
M 22,a[148:151],58
L 2
v_cmp_eq_u32 vcc,s53,v7
v_fma_f32 v98,v80,v98,v79
M 22,a[160:163],62
R 50,1,18432
v_fma_f32 v99,v80,v99,v79
v_fma_f32 v100,v80,v100,v79
M 18,a[144:147],58
v_fma_f32 v101,v80,v101,v79
v_mul_f32 v98,v34,v98
M 18,a[156:159],62
s_cmp_eq_u32 vcc_lo,-1
s_nop 0
v_mul_f32 v99,v35,v99
s_cbranch_scc0 Lg3_X0_%=
M 18,a[168:171],66
s_add_u32 m0,s57,0
s_add_u32 s56,s55,0
s_add_u32 s58,s58,s61
R 54,1,19456
M 18,a[180:183],70
buffer_load_dword v114,v11,s[36:39],s73 offen offset:0 sc1
M 22,a[172:175],66
D 0,56,0
v_mul_f32 v100,v36,v100
M 22,a[184:187],70
s_add_u32 s54,s54,s46
R 58,1,20480
v_mul_f32 v101,v37,v101
M 26,a[176:179],66
s_add_u32 s73,s73,s64
v_exp_f32 v98,v98
M 26,a[188:191],70
L 3
v_exp_f32 v99,v99
M 26,a[200:203],42
R 62,1,21504
v_fma_f32 v98,v98,v81,v81
M 26,a[212:215],46
D 0,56,1024
v_fma_f32 v99,v99,v81,v81
M 22,a[196:199],42
v_exp_f32 v100,v100
M 22,a[208:211],46
R 66,1,22528
v_fma_f32 v100,v100,v81,v81
M 18,a[192:195],42
D 0,56,2048
M 18,a[204:207],46
L 3
v_exp_f32 v101,v101
M 18,a[216:219],50
R 70,1,23552
v_rcp_f32 v98,v98
M 18,a[228:231],54
v_rcp_f32 v99,v99
M 22,a[220:223],50
D 0,56,3072
M 22,a[232:235],54
s_add_u32 m0,s59,0
R 42,1,24576
v_fma_f32 v101,v101,v81,v81
M 26,a[224:227],50
buffer_load_dword v8,s[36:39],s58 offen offset:0 sc1 lds
M 26,a[236:239],54
L 3
v_rcp_f32 v100,v100
v_mul_f32 v34,v34,v98
M 26,a[248:251],58
s_add_u32 m0,s57,16384
R 46,1,25600
v_mul_f32 v35,v35,v99
M 26,v[132:135],62
s_add_u32 s56,s55,16384
v_rcp_f32 v101,v101
M 22,a[244:247],58
D 0,56,0
M 22,v[128:131],62
s_add_u32 s55,s55,s60
R 50,1,26624
v_mul_f32 v36,v36,v100
M 18,a[240:243],58
s_sub_u32 s57,s84,s57
v_mul_f32 v37,v37,v101
v_cvt_pk_bf16_f32 v108,v34,v35
v_cvt_pk_bf16_f32 v109,v36,v37
M 18,a[252:255],62
L 3
buffer_store_dwordx4 v[106:109],v0,s[36:39],s67 offen offset:0 sc0 sc1
v_fma_f32 v38,-v78,v90,v38
M 18,v[136:139],66
R 54,1,27648
v_fma_f32 v39,-v78,v91,v39
M 18,v[148:151],70
v_fma_f32 v40,-v78,v92,v40
v_fma_f32 v41,-v78,v93,v41
M 22,v[140:143],66
D 0,56,1024
M 22,v[152:155],70
R 58,1,28672
v_mul_f32 v102,v38,v38
v_mul_f32 v103,v39,v39
M 26,v[144:147],66
v_mul_f32 v104,v40,v40
v_mul_f32 v105,v41,v41
M 26,v[156:159],70
L 3
v_fma_f32 v102,v80,v102,v79
v_fma_f32 v103,v80,v103,v79
M 26,v[168:171],42
R 62,1,29696
v_fma_f32 v104,v80,v104,v79
M 26,v[180:183],46
D 0,56,2048
M 22,v[164:167],42
v_fma_f32 v105,v80,v105,v79
v_mul_f32 v102,v38,v102
v_mul_f32 v103,v39,v103
M 22,v[176:179],46
R 66,1,30720
v_mul_f32 v104,v40,v104
M 18,v[160:163],42
v_exp_f32 v102,v102
M 18,v[172:175],46
L 3
D 0,56,3072
M 18,v[184:187],50
s_nop 0
R 70,1,31744
v_mul_f32 v105,v41,v105
M 18,v[196:199],54
s_waitcnt vmcnt(5)
v_exp_f32 v103,v103
v_fma_f32 v102,v102,v81,v81
M 22,v[188:191],50
s_barrier
buffer_store_dword v4,v3,s[36:39],s70 offen offset:0 sc0 sc1
M 22,v[200:203],54
s_mov_b32 s70,s69
R 42,2,0
ds_read_b32 v7,v6 offset:0
M 26,v[192:195],50
s_mov_b32 s69,s68
R 74,13,0
v_fma_f32 v103,v103,v81,v81
M 26,v[204:207],54
L 5
v_exp_f32 v104,v104
M 26,v[216:219],58
s_add_u32 s68,s68,s63
R 46,2,1024
v_exp_f32 v105,v105
M 26,v[228:231],62
v_rcp_f32 v102,v102
M 22,v[212:215],58
v_rcp_f32 v103,v103
M 22,v[224:227],62
L 2
R 50,2,2048
v_fma_f32 v104,v104,v81,v81
M 18,v[208:211],58
v_rcp_f32 v104,v104
M 18,v[220:223],62
s_nop 0
v_fma_f32 v105,v105,v81,v81
v_rcp_f32 v105,v105
M 18,v[232:235],66
R 54,2,3072
v_mul_f32 v38,v38,v102
M 18,v[244:247],70
v_mul_f32 v39,v39,v103
v_mul_f32 v40,v40,v104
M 22,v[236:239],66
v_cmp_eq_u32 vcc,s53,v7
v_mul_f32 v41,v41,v105
M 22,v[248:251],70
R 58,2,4096
v_cvt_pk_bf16_f32 v110,v38,v39
v_cvt_pk_bf16_f32 v111,v40,v41
M 26,v[240:243],66
L 3
s_cmp_eq_u32 vcc_lo,-1
buffer_store_dwordx2 v[110:111],v10,s[36:39],s67 offen offset:0 sc0 sc1
M 26,v[252:255],70
buffer_store_dword v4,v3,s[36:39],s71 offen offset:0 sc0 sc1
s_cbranch_scc1 Lg3_F1_%=
s_branch Lg3_K1_%=
Lg3_X0_%=:
M 18,a[168:171],66
s_add_u32 m0,s59,0
R 54,1,19456
v_mul_f32 v100,v36,v100
M 18,a[180:183],70
buffer_load_dword v8,s[36:39],s58 offen offset:0 sc1 lds
M 22,a[172:175],66
v_exp_f32 v98,v98
M 22,a[184:187],70
R 58,1,20480
v_mul_f32 v101,v37,v101
M 26,a[176:179],66
v_exp_f32 v99,v99
M 26,a[188:191],70
L 3
v_exp_f32 v100,v100
M 26,a[200:203],42
R 62,1,21504
v_fma_f32 v98,v98,v81,v81
M 26,a[212:215],46
v_exp_f32 v101,v101
M 22,a[196:199],42
v_rcp_f32 v98,v98
M 22,a[208:211],46
R 66,1,22528
v_fma_f32 v99,v99,v81,v81
M 18,a[192:195],42
v_rcp_f32 v99,v99
M 18,a[204:207],46
L 3
v_fma_f32 v100,v100,v81,v81
v_fma_f32 v101,v101,v81,v81
M 18,a[216:219],50
R 70,1,23552
v_mul_f32 v34,v34,v98
M 18,a[228:231],54
v_rcp_f32 v100,v100
M 22,a[220:223],50
v_rcp_f32 v101,v101
M 22,a[232:235],54
R 42,1,24576
v_mul_f32 v35,v35,v99
M 26,a[224:227],50
v_mul_f32 v36,v36,v100
v_mul_f32 v37,v37,v101
M 26,a[236:239],54
L 3
v_cvt_pk_bf16_f32 v108,v34,v35
v_cvt_pk_bf16_f32 v109,v36,v37
M 26,a[248:251],58
R 46,1,25600
v_fma_f32 v38,-v78,v90,v38
M 26,v[132:135],62
buffer_store_dwordx4 v[106:109],v0,s[36:39],s67 offen offset:0 sc0 sc1
v_fma_f32 v39,-v78,v91,v39
M 22,a[244:247],58
v_fma_f32 v40,-v78,v92,v40
v_fma_f32 v41,-v78,v93,v41
M 22,v[128:131],62
R 50,1,26624
v_mul_f32 v102,v38,v38
M 18,a[240:243],58
v_mul_f32 v103,v39,v39
v_mul_f32 v104,v40,v40
M 18,a[252:255],62
L 3
v_mul_f32 v105,v41,v41
v_fma_f32 v102,v80,v102,v79
M 18,v[136:139],66
R 54,1,27648
v_fma_f32 v103,v80,v103,v79
M 18,v[148:151],70
v_fma_f32 v104,v80,v104,v79
v_fma_f32 v105,v80,v105,v79
M 22,v[140:143],66
v_mul_f32 v102,v38,v102
v_mul_f32 v103,v39,v103
M 22,v[152:155],70
R 58,1,28672
v_mul_f32 v104,v40,v104
M 26,v[144:147],66
v_exp_f32 v102,v102
M 26,v[156:159],70
L 3
v_exp_f32 v103,v103
M 26,v[168:171],42
R 62,1,29696
v_mul_f32 v105,v41,v105
M 26,v[180:183],46
v_exp_f32 v104,v104
M 22,v[164:167],42
v_exp_f32 v105,v105
M 22,v[176:179],46
R 66,1,30720
v_fma_f32 v102,v102,v81,v81
M 18,v[160:163],42
v_rcp_f32 v102,v102
M 18,v[172:175],46
L 3
v_fma_f32 v103,v103,v81,v81
v_fma_f32 v104,v104,v81,v81
M 18,v[184:187],50
s_nop 0
R 70,1,31744
v_fma_f32 v105,v105,v81,v81
M 18,v[196:199],54
s_waitcnt vmcnt(1)
v_rcp_f32 v103,v103
M 22,v[188:191],50
s_barrier
buffer_store_dword v4,v3,s[36:39],s70 offen offset:0 sc0 sc1
M 22,v[200:203],54
s_mov_b32 s70,s69
ds_read_b32 v7,v6 offset:0
R 74,13,0
M 26,v[192:195],50
s_mov_b32 s69,s68
v_rcp_f32 v104,v104
M 26,v[204:207],54
L 4
v_rcp_f32 v105,v105
M 26,v[216:219],58
s_add_u32 s68,s68,s63
v_mul_f32 v38,v38,v102
v_mul_f32 v39,v39,v103
M 26,v[228:231],62
v_mul_f32 v40,v40,v104
v_mul_f32 v41,v41,v105
M 22,v[212:215],58
v_cvt_pk_bf16_f32 v110,v38,v39
v_cvt_pk_bf16_f32 v111,v40,v41
M 22,v[224:227],62
L 1
buffer_store_dwordx2 v[110:111],v10,s[36:39],s67 offen offset:0 sc0 sc1
M 18,v[208:211],58
v_cmp_eq_u32 vcc,s53,v7
M 18,v[220:223],62
s_nop 0
M 18,v[232:235],66
M 18,v[244:247],70
M 22,v[236:239],66
M 22,v[248:251],70
M 26,v[240:243],66
M 26,v[252:255],70
s_mov_b32 s80,1
s_cmp_eq_u32 vcc_lo,-1
s_cbranch_scc0 Lg3_xn0_%=
buffer_load_dword v114,v11,s[36:39],s73 offen offset:0 sc1
L 0
v_fma_f32 v18,-v74,v82,v18
v_fma_f32 v19,-v74,v83,v19
v_fma_f32 v20,-v74,v84,v20
v_fma_f32 v21,-v74,v85,v21
v_mul_f32 v94,v18,v18
v_mul_f32 v95,v19,v19
v_mul_f32 v96,v20,v20
v_mul_f32 v97,v21,v21
s_add_u32 s73,s73,s64
s_add_u32 m0,s57,0
s_add_u32 s56,s55,0
D 0,56,0
v_fma_f32 v94,v76,v94,v75
v_fma_f32 v95,v76,v95,v75
v_fma_f32 v96,v76,v96,v75
v_fma_f32 v97,v76,v97,v75
v_mul_f32 v94,v18,v94
v_mul_f32 v95,v19,v95
v_mul_f32 v96,v20,v96
v_mul_f32 v97,v21,v97
D 0,56,1024
v_exp_f32 v94,v94
v_exp_f32 v95,v95
v_exp_f32 v96,v96
v_exp_f32 v97,v97
v_fma_f32 v94,v94,v77,v77
v_fma_f32 v95,v95,v77,v77
v_fma_f32 v96,v96,v77,v77
v_fma_f32 v97,v97,v77,v77
D 0,56,2048
v_rcp_f32 v94,v94
v_rcp_f32 v95,v95
v_rcp_f32 v96,v96
v_rcp_f32 v97,v97
v_mul_f32 v18,v18,v94
v_mul_f32 v19,v19,v95
)"
R"(v_mul_f32 v20,v20,v96
v_mul_f32 v21,v21,v97
D 0,56,3072
v_cvt_pk_bf16_f32 v106,v18,v19
v_cvt_pk_bf16_f32 v107,v20,v21
v_fma_f32 v22,-v74,v86,v22
v_fma_f32 v23,-v74,v87,v23
v_fma_f32 v24,-v74,v88,v24
v_fma_f32 v25,-v74,v89,v25
v_mul_f32 v98,v22,v22
v_mul_f32 v99,v23,v23
s_add_u32 s58,s58,s61
s_add_u32 m0,s57,16384
s_add_u32 s56,s55,16384
D 0,56,0
v_mul_f32 v100,v24,v24
v_mul_f32 v101,v25,v25
v_fma_f32 v98,v76,v98,v75
v_fma_f32 v99,v76,v99,v75
v_fma_f32 v100,v76,v100,v75
v_fma_f32 v101,v76,v101,v75
v_mul_f32 v98,v22,v98
v_mul_f32 v99,v23,v99
D 0,56,1024
v_mul_f32 v100,v24,v100
v_mul_f32 v101,v25,v101
v_exp_f32 v98,v98
v_exp_f32 v99,v99
v_exp_f32 v100,v100
v_exp_f32 v101,v101
v_fma_f32 v98,v98,v77,v77
v_fma_f32 v99,v99,v77,v77
D 0,56,2048
v_fma_f32 v100,v100,v77,v77
v_fma_f32 v101,v101,v77,v77
v_rcp_f32 v98,v98
v_rcp_f32 v99,v99
v_rcp_f32 v100,v100
v_rcp_f32 v101,v101
v_mul_f32 v22,v22,v98
v_mul_f32 v23,v23,v99
D 0,56,3072
v_mul_f32 v24,v24,v100
v_mul_f32 v25,v25,v101
v_cvt_pk_bf16_f32 v108,v22,v23
v_cvt_pk_bf16_f32 v109,v24,v25
buffer_store_dwordx4 v[106:109],v0,s[36:39],s66 offen offset:0 sc0 sc1
v_fma_f32 v26,-v74,v90,v26
v_fma_f32 v27,-v74,v91,v27
v_fma_f32 v28,-v74,v92,v28
v_fma_f32 v29,-v74,v93,v29
s_add_u32 s55,s55,s60
s_sub_u32 s57,s84,s57
s_add_u32 s54,s54,s46
v_mul_f32 v102,v26,v26
v_mul_f32 v103,v27,v27
v_mul_f32 v104,v28,v28
v_mul_f32 v105,v29,v29
v_fma_f32 v102,v76,v102,v75
v_fma_f32 v103,v76,v103,v75
v_fma_f32 v104,v76,v104,v75
v_fma_f32 v105,v76,v105,v75
v_mul_f32 v102,v26,v102
v_mul_f32 v103,v27,v103
v_mul_f32 v104,v28,v104
v_mul_f32 v105,v29,v105
v_exp_f32 v102,v102
v_exp_f32 v103,v103
v_exp_f32 v104,v104
v_exp_f32 v105,v105
v_fma_f32 v102,v102,v77,v77
v_fma_f32 v103,v103,v77,v77
v_fma_f32 v104,v104,v77,v77
v_fma_f32 v105,v105,v77,v77
v_rcp_f32 v102,v102
v_rcp_f32 v103,v103
v_rcp_f32 v104,v104
v_rcp_f32 v105,v105
v_mul_f32 v26,v26,v102
v_mul_f32 v27,v27,v103
v_mul_f32 v28,v28,v104
v_mul_f32 v29,v29,v105
v_cvt_pk_bf16_f32 v110,v26,v27
v_cvt_pk_bf16_f32 v111,v28,v29
buffer_store_dwordx2 v[110:111],v10,s[36:39],s66 offen offset:0 sc0 sc1
s_mov_b32 s72,s69
s_mov_b32 s69,0x7fff0000
s_mov_b32 s66,0x7fff0000
s_mov_b32 s76,0x7fff0000
s_branch Lg3_gox_%=
Lg3_xn0_%=:
s_cmp_ge_u32 s54,256
s_cbranch_scc1 Lg3_fin0_%=
s_add_u32 m0,s59,4096
s_nop 0
buffer_load_dword v8,s[36:39],s58 offen offset:0 sc1 lds
buffer_store_dword v4,v3,s[36:39],s71 offen offset:0 sc0 sc1
s_add_u32 m0,s59,5120
s_nop 0
buffer_load_dword v8,s[36:39],s58 offen offset:0 sc1 lds
buffer_store_dword v4,v3,s[36:39],s71 offen offset:0 sc0 sc1
s_mov_b32 s83,0
L 0
v_fma_f32 v18,-v74,v82,v18
v_fma_f32 v19,-v74,v83,v19
v_fma_f32 v20,-v74,v84,v20
v_fma_f32 v21,-v74,v85,v21
v_mul_f32 v94,v18,v18
v_mul_f32 v95,v19,v19
v_mul_f32 v96,v20,v20
v_mul_f32 v97,v21,v21
v_fma_f32 v94,v76,v94,v75
v_fma_f32 v95,v76,v95,v75
v_fma_f32 v96,v76,v96,v75
v_fma_f32 v97,v76,v97,v75
v_mul_f32 v94,v18,v94
v_mul_f32 v95,v19,v95
v_mul_f32 v96,v20,v96
v_mul_f32 v97,v21,v97
v_exp_f32 v94,v94
v_exp_f32 v95,v95
v_exp_f32 v96,v96
v_exp_f32 v97,v97
v_fma_f32 v94,v94,v77,v77
v_fma_f32 v95,v95,v77,v77
v_fma_f32 v96,v96,v77,v77
v_fma_f32 v97,v97,v77,v77
v_rcp_f32 v94,v94
v_rcp_f32 v95,v95
v_rcp_f32 v96,v96
v_rcp_f32 v97,v97
v_mul_f32 v18,v18,v94
v_mul_f32 v19,v19,v95
v_mul_f32 v20,v20,v96
v_mul_f32 v21,v21,v97
v_cvt_pk_bf16_f32 v106,v18,v19
v_cvt_pk_bf16_f32 v107,v20,v21
v_fma_f32 v22,-v74,v86,v22
v_fma_f32 v23,-v74,v87,v23
v_fma_f32 v24,-v74,v88,v24
v_fma_f32 v25,-v74,v89,v25
v_mul_f32 v98,v22,v22
v_mul_f32 v99,v23,v23
v_mul_f32 v100,v24,v24
v_mul_f32 v101,v25,v25
v_fma_f32 v98,v76,v98,v75
v_fma_f32 v99,v76,v99,v75
v_fma_f32 v100,v76,v100,v75
v_fma_f32 v101,v76,v101,v75
v_mul_f32 v98,v22,v98
v_mul_f32 v99,v23,v99
v_mul_f32 v100,v24,v100
v_mul_f32 v101,v25,v101
v_exp_f32 v98,v98
v_exp_f32 v99,v99
v_exp_f32 v100,v100
v_exp_f32 v101,v101
v_fma_f32 v98,v98,v77,v77
v_fma_f32 v99,v99,v77,v77
v_fma_f32 v100,v100,v77,v77
v_fma_f32 v101,v101,v77,v77
v_rcp_f32 v98,v98
v_rcp_f32 v99,v99
v_rcp_f32 v100,v100
v_rcp_f32 v101,v101
v_mul_f32 v22,v22,v98
v_mul_f32 v23,v23,v99
v_mul_f32 v24,v24,v100
v_mul_f32 v25,v25,v101
v_cvt_pk_bf16_f32 v108,v22,v23
v_cvt_pk_bf16_f32 v109,v24,v25
buffer_store_dwordx4 v[106:109],v0,s[36:39],s66 offen offset:0 sc0 sc1
v_fma_f32 v26,-v74,v90,v26
v_fma_f32 v27,-v74,v91,v27
v_fma_f32 v28,-v74,v92,v28
v_fma_f32 v29,-v74,v93,v29
v_mul_f32 v102,v26,v26
v_mul_f32 v103,v27,v27
v_mul_f32 v104,v28,v28
v_mul_f32 v105,v29,v29
v_fma_f32 v102,v76,v102,v75
v_fma_f32 v103,v76,v103,v75
v_fma_f32 v104,v76,v104,v75
v_fma_f32 v105,v76,v105,v75
v_mul_f32 v102,v26,v102
v_mul_f32 v103,v27,v103
v_mul_f32 v104,v28,v104
v_mul_f32 v105,v29,v105
v_exp_f32 v102,v102
v_exp_f32 v103,v103
v_exp_f32 v104,v104
v_exp_f32 v105,v105
v_fma_f32 v102,v102,v77,v77
v_fma_f32 v103,v103,v77,v77
v_fma_f32 v104,v104,v77,v77
v_fma_f32 v105,v105,v77,v77
v_rcp_f32 v102,v102
v_rcp_f32 v103,v103
v_rcp_f32 v104,v104
v_rcp_f32 v105,v105
v_mul_f32 v26,v26,v102
v_mul_f32 v27,v27,v103
v_mul_f32 v28,v28,v104
v_mul_f32 v29,v29,v105
v_cvt_pk_bf16_f32 v110,v26,v27
v_cvt_pk_bf16_f32 v111,v28,v29
buffer_store_dwordx2 v[110:111],v10,s[36:39],s66 offen offset:0 sc0 sc1
s_mov_b32 s72,s69
s_mov_b32 s69,0x7fff0000
s_mov_b32 s66,0x7fff0000
s_mov_b32 s76,0x7fff0000
s_branch Lg3_poll_%=
Lg3_fin0_%=:
L 0
v_fma_f32 v18,-v74,v82,v18
v_fma_f32 v19,-v74,v83,v19
v_fma_f32 v20,-v74,v84,v20
v_fma_f32 v21,-v74,v85,v21
v_mul_f32 v94,v18,v18
v_mul_f32 v95,v19,v19
v_mul_f32 v96,v20,v20
v_mul_f32 v97,v21,v21
v_fma_f32 v94,v76,v94,v75
v_fma_f32 v95,v76,v95,v75
v_fma_f32 v96,v76,v96,v75
v_fma_f32 v97,v76,v97,v75
v_mul_f32 v94,v18,v94
v_mul_f32 v95,v19,v95
v_mul_f32 v96,v20,v96
v_mul_f32 v97,v21,v97
v_exp_f32 v94,v94
v_exp_f32 v95,v95
v_exp_f32 v96,v96
v_exp_f32 v97,v97
v_fma_f32 v94,v94,v77,v77
v_fma_f32 v95,v95,v77,v77
v_fma_f32 v96,v96,v77,v77
v_fma_f32 v97,v97,v77,v77
v_rcp_f32 v94,v94
v_rcp_f32 v95,v95
v_rcp_f32 v96,v96
v_rcp_f32 v97,v97
v_mul_f32 v18,v18,v94
v_mul_f32 v19,v19,v95
v_mul_f32 v20,v20,v96
v_mul_f32 v21,v21,v97
v_cvt_pk_bf16_f32 v106,v18,v19
v_cvt_pk_bf16_f32 v107,v20,v21
v_fma_f32 v22,-v74,v86,v22
v_fma_f32 v23,-v74,v87,v23
v_fma_f32 v24,-v74,v88,v24
v_fma_f32 v25,-v74,v89,v25
v_mul_f32 v98,v22,v22
v_mul_f32 v99,v23,v23
v_mul_f32 v100,v24,v24
v_mul_f32 v101,v25,v25
v_fma_f32 v98,v76,v98,v75
v_fma_f32 v99,v76,v99,v75
v_fma_f32 v100,v76,v100,v75
v_fma_f32 v101,v76,v101,v75
v_mul_f32 v98,v22,v98
v_mul_f32 v99,v23,v99
v_mul_f32 v100,v24,v100
v_mul_f32 v101,v25,v101
v_exp_f32 v98,v98
v_exp_f32 v99,v99
v_exp_f32 v100,v100
v_exp_f32 v101,v101
v_fma_f32 v98,v98,v77,v77
v_fma_f32 v99,v99,v77,v77
v_fma_f32 v100,v100,v77,v77
v_fma_f32 v101,v101,v77,v77
v_rcp_f32 v98,v98
v_rcp_f32 v99,v99
v_rcp_f32 v100,v100
v_rcp_f32 v101,v101
v_mul_f32 v22,v22,v98
v_mul_f32 v23,v23,v99
v_mul_f32 v24,v24,v100
v_mul_f32 v25,v25,v101
v_cvt_pk_bf16_f32 v108,v22,v23
v_cvt_pk_bf16_f32 v109,v24,v25
buffer_store_dwordx4 v[106:109],v0,s[36:39],s66 offen offset:0 sc0 sc1
v_fma_f32 v26,-v74,v90,v26
v_fma_f32 v27,-v74,v91,v27
v_fma_f32 v28,-v74,v92,v28
v_fma_f32 v29,-v74,v93,v29
v_mul_f32 v102,v26,v26
v_mul_f32 v103,v27,v27
v_mul_f32 v104,v28,v28
v_mul_f32 v105,v29,v29
v_fma_f32 v102,v76,v102,v75
v_fma_f32 v103,v76,v103,v75
v_fma_f32 v104,v76,v104,v75
v_fma_f32 v105,v76,v105,v75
v_mul_f32 v102,v26,v102
v_mul_f32 v103,v27,v103
v_mul_f32 v104,v28,v104
v_mul_f32 v105,v29,v105
v_exp_f32 v102,v102
v_exp_f32 v103,v103
v_exp_f32 v104,v104
v_exp_f32 v105,v105
v_fma_f32 v102,v102,v77,v77
v_fma_f32 v103,v103,v77,v77
v_fma_f32 v104,v104,v77,v77
v_fma_f32 v105,v105,v77,v77
v_rcp_f32 v102,v102
v_rcp_f32 v103,v103
v_rcp_f32 v104,v104
v_rcp_f32 v105,v105
v_mul_f32 v26,v26,v102
v_mul_f32 v27,v27,v103
v_mul_f32 v28,v28,v104
v_mul_f32 v29,v29,v105
v_cvt_pk_bf16_f32 v110,v26,v27
v_cvt_pk_bf16_f32 v111,v28,v29
buffer_store_dwordx2 v[110:111],v10,s[36:39],s66 offen offset:0 sc0 sc1
s_mov_b32 s72,s69
s_mov_b32 s69,0x7fff0000
s_mov_b32 s66,0x7fff0000
s_mov_b32 s76,0x7fff0000
s_branch Lg3_done_%=
Lg3_K1_%=:
Lg3_L1_%=:
v_mfma_f32_16x16x32_bf16 v[38:41],a[8:11],v[42:45],0
s_add_u32 m0,s59,3072
R 62,2,5120
v_add_f32_dpp v115,v114,v114 quad_perm:[1,0,3,2] row_mask:0xf bank_mask:0xf
M 38,a[20:23],46
buffer_load_dword v8,s[36:39],s58 offen offset:0 sc1 lds
v_mfma_f32_16x16x32_bf16 v[34:37],a[4:7],v[42:45],0
s_mov_b32 s67,s65
v_add_f32_dpp v115,v115,v115 quad_perm:[2,3,0,1] row_mask:0xf bank_mask:0xf
v_fma_f32 v18,-v74,v82,v18
M 34,a[16:19],46
s_add_u32 s65,s65,s62
R 66,2,6144
v_fma_f32 v19,-v74,v83,v19
v_mfma_f32_16x16x32_bf16 v[30:33],a[0:3],v[42:45],0
v_add_f32_dpp v118,v115,v115 row_half_mirror row_mask:0xf bank_mask:0xf
v_fma_f32 v20,-v74,v84,v20
v_fma_f32 v21,-v74,v85,v21
M 30,a[12:15],46
L 3
v_mul_f32 v94,v18,v18
v_mul_f32 v95,v19,v19
M 30,a[24:27],50
R 70,2,7168
v_mov_b32_dpp v115,v118 row_ror:8 row_mask:0xf bank_mask:0xf
M 30,a[36:39],54
v_fma_f32 v115,-v118,v118,v115
v_max_f32 v115,s87,v115
M 34,a[28:31],50
v_rsq_f32 v116,v115
M 34,a[40:43],54
R 42,2,8192
v_fma_f32 v115,v15,v115,v16
v_mul_f32 v118,v17,v118
M 38,a[32:35],50
v_mul_f32 v119,v116,v116
v_fma_f32 v115,v14,v119,v115
M 38,a[44:47],54
L 3
v_fma_f32 v118,v116,v115,v118
v_mul_f32 v96,v20,v20
M 38,a[56:59],58
R 46,2,9216
v_mul_f32 v97,v21,v21
M 38,a[68:71],62
ds_write_b32 v12,v118 offset:2048
M 34,a[52:55],58
v_fma_f32 v94,v76,v94,v75
v_fma_f32 v95,v76,v95,v75
v_fma_f32 v96,v76,v96,v75
M 34,a[64:67],62
R 50,2,10240
v_fma_f32 v97,v76,v97,v75
M 30,a[48:51],58
v_mul_f32 v94,v18,v94
v_mul_f32 v95,v19,v95
M 30,a[60:63],62
L 4
v_exp_f32 v94,v94
M 30,a[72:75],66
R 54,2,11264
v_mul_f32 v96,v20,v96
M 30,a[84:87],70
v_exp_f32 v95,v95
v_mul_f32 v97,v21,v97
M 34,a[76:79],66
v_exp_f32 v96,v96
M 34,a[88:91],70
R 58,2,12288
v_fma_f32 v94,v94,v77,v77
M 38,a[80:83],66
v_exp_f32 v97,v97
M 38,a[92:95],70
L 4
v_rcp_f32 v94,v94
M 38,a[104:107],42
R 62,2,13312
v_fma_f32 v95,v95,v77,v77
v_fma_f32 v96,v96,v77,v77
M 38,a[116:119],46
v_rcp_f32 v95,v95
M 34,a[100:103],42
v_rcp_f32 v96,v96
M 34,a[112:115],46
R 66,2,14336
v_fma_f32 v97,v97,v77,v77
M 30,a[96:99],42
v_rcp_f32 v97,v97
M 30,a[108:111],46
L 3
v_mul_f32 v18,v18,v94
v_mul_f32 v19,v19,v95
v_mul_f32 v20,v20,v96
M 30,a[120:123],50
R 70,2,15360
v_mul_f32 v21,v21,v97
M 30,a[132:135],54
s_waitcnt vmcnt(0)
v_cvt_pk_bf16_f32 v106,v18,v19
v_cvt_pk_bf16_f32 v107,v20,v21
M 34,a[124:127],50
s_barrier
ds_read_b32 v7,v6 offset:3072
v_fma_f32 v22,-v74,v86,v22
M 34,a[136:139],54
R 42,2,16384
v_fma_f32 v23,-v74,v87,v23
M 38,a[128:131],50
buffer_store_dword v4,v3,s[36:39],s70 offen offset:0 sc0 sc1
v_fma_f32 v24,-v74,v88,v24
M 38,a[140:143],54
L 4
v_fma_f32 v25,-v74,v89,v25
v_mul_f32 v98,v22,v22
M 38,a[152:155],58
s_mov_b32 s70,0x7fff0000
R 46,2,17408
v_mul_f32 v99,v23,v23
M 38,a[164:167],62
v_mul_f32 v100,v24,v24
v_mul_f32 v101,v25,v25
M 34,a[148:151],58
L 2
v_cmp_eq_u32 vcc,s53,v7
v_fma_f32 v98,v76,v98,v75
M 34,a[160:163],62
R 50,2,18432
v_fma_f32 v99,v76,v99,v75
v_fma_f32 v100,v76,v100,v75
M 30,a[144:147],58
v_fma_f32 v101,v76,v101,v75
v_mul_f32 v98,v22,v98
M 30,a[156:159],62
s_cmp_eq_u32 vcc_lo,-1
s_nop 0
v_mul_f32 v99,v23,v99
s_cbranch_scc0 Lg3_X1_%=
M 30,a[168:171],66
s_add_u32 m0,s57,0
s_add_u32 s56,s55,0
s_add_u32 s58,s58,s61
R 54,2,19456
M 30,a[180:183],70
buffer_load_dword v114,v11,s[36:39],s73 offen offset:0 sc1
M 34,a[172:175],66
D 0,56,0
v_mul_f32 v100,v24,v100
M 34,a[184:187],70
s_add_u32 s54,s54,s46
R 58,2,20480
v_mul_f32 v101,v25,v101
M 38,a[176:179],66
s_add_u32 s73,s73,s64
v_exp_f32 v98,v98
M 38,a[188:191],70
L 3
v_exp_f32 v99,v99
M 38,a[200:203],42
R 62,2,21504
v_fma_f32 v98,v98,v77,v77
M 38,a[212:215],46
D 0,56,1024
v_fma_f32 v99,v99,v77,v77
M 34,a[196:199],42
v_exp_f32 v100,v100
M 34,a[208:211],46
R 66,2,22528
v_fma_f32 v100,v100,v77,v77
M 30,a[192:195],42
D 0,56,2048
M 30,a[204:207],46
L 3
v_exp_f32 v101,v101
M 30,a[216:219],50
R 70,2,23552
)"
R"(v_rcp_f32 v98,v98
M 30,a[228:231],54
v_rcp_f32 v99,v99
M 34,a[220:223],50
D 0,56,3072
M 34,a[232:235],54
s_add_u32 m0,s59,1024
R 42,2,24576
v_fma_f32 v101,v101,v77,v77
M 38,a[224:227],50
buffer_load_dword v8,s[36:39],s58 offen offset:0 sc1 lds
M 38,a[236:239],54
L 3
v_rcp_f32 v100,v100
v_mul_f32 v22,v22,v98
M 38,a[248:251],58
s_add_u32 m0,s57,16384
R 46,2,25600
v_mul_f32 v23,v23,v99
M 38,v[132:135],62
s_add_u32 s56,s55,16384
v_rcp_f32 v101,v101
M 34,a[244:247],58
D 0,56,0
M 34,v[128:131],62
s_add_u32 s55,s55,s60
R 50,2,26624
v_mul_f32 v24,v24,v100
M 30,a[240:243],58
s_sub_u32 s57,s84,s57
v_mul_f32 v25,v25,v101
v_cvt_pk_bf16_f32 v108,v22,v23
v_cvt_pk_bf16_f32 v109,v24,v25
M 30,a[252:255],62
L 3
buffer_store_dwordx4 v[106:109],v0,s[36:39],s66 offen offset:0 sc0 sc1
v_fma_f32 v26,-v74,v90,v26
M 30,v[136:139],66
R 54,2,27648
v_fma_f32 v27,-v74,v91,v27
M 30,v[148:151],70
v_fma_f32 v28,-v74,v92,v28
v_fma_f32 v29,-v74,v93,v29
M 34,v[140:143],66
D 0,56,1024
M 34,v[152:155],70
R 58,2,28672
v_mul_f32 v102,v26,v26
v_mul_f32 v103,v27,v27
M 38,v[144:147],66
v_mul_f32 v104,v28,v28
v_mul_f32 v105,v29,v29
M 38,v[156:159],70
L 3
v_fma_f32 v102,v76,v102,v75
v_fma_f32 v103,v76,v103,v75
M 38,v[168:171],42
R 62,2,29696
v_fma_f32 v104,v76,v104,v75
M 38,v[180:183],46
D 0,56,2048
M 34,v[164:167],42
v_fma_f32 v105,v76,v105,v75
v_mul_f32 v102,v26,v102
v_mul_f32 v103,v27,v103
M 34,v[176:179],46
R 66,2,30720
v_mul_f32 v104,v28,v104
M 30,v[160:163],42
v_exp_f32 v102,v102
M 30,v[172:175],46
L 3
D 0,56,3072
M 30,v[184:187],50
s_nop 0
R 70,2,31744
v_mul_f32 v105,v29,v105
M 30,v[196:199],54
s_waitcnt vmcnt(5)
v_exp_f32 v103,v103
v_fma_f32 v102,v102,v77,v77
M 34,v[188:191],50
s_barrier
buffer_store_dword v4,v3,s[36:39],s70 offen offset:0 sc0 sc1
M 34,v[200:203],54
s_mov_b32 s70,s69
R 42,1,0
ds_read_b32 v7,v6 offset:1024
M 38,v[192:195],50
s_mov_b32 s69,s68
R 78,13,2048
v_fma_f32 v103,v103,v77,v77
M 38,v[204:207],54
L 5
v_exp_f32 v104,v104
M 38,v[216:219],58
s_add_u32 s68,s68,s63
R 46,1,1024
v_exp_f32 v105,v105
M 38,v[228:231],62
v_rcp_f32 v102,v102
M 34,v[212:215],58
v_rcp_f32 v103,v103
M 34,v[224:227],62
L 2
R 50,1,2048
v_fma_f32 v104,v104,v77,v77
M 30,v[208:211],58
v_rcp_f32 v104,v104
M 30,v[220:223],62
s_nop 0
v_fma_f32 v105,v105,v77,v77
v_rcp_f32 v105,v105
M 30,v[232:235],66
R 54,1,3072
v_mul_f32 v26,v26,v102
M 30,v[244:247],70
v_mul_f32 v27,v27,v103
v_mul_f32 v28,v28,v104
M 34,v[236:239],66
v_cmp_eq_u32 vcc,s53,v7
v_mul_f32 v29,v29,v105
M 34,v[248:251],70
R 58,1,4096
v_cvt_pk_bf16_f32 v110,v26,v27
v_cvt_pk_bf16_f32 v111,v28,v29
M 38,v[240:243],66
L 3
s_cmp_eq_u32 vcc_lo,-1
buffer_store_dwordx2 v[110:111],v10,s[36:39],s66 offen offset:0 sc0 sc1
M 38,v[252:255],70
buffer_store_dword v4,v3,s[36:39],s71 offen offset:0 sc0 sc1
s_cbranch_scc1 Lg3_F0_%=
s_branch Lg3_K0_%=
Lg3_X1_%=:
M 30,a[168:171],66
s_add_u32 m0,s59,1024
R 54,2,19456
v_mul_f32 v100,v24,v100
M 30,a[180:183],70
buffer_load_dword v8,s[36:39],s58 offen offset:0 sc1 lds
M 34,a[172:175],66
v_exp_f32 v98,v98
M 34,a[184:187],70
R 58,2,20480
v_mul_f32 v101,v25,v101
M 38,a[176:179],66
v_exp_f32 v99,v99
M 38,a[188:191],70
L 3
v_exp_f32 v100,v100
M 38,a[200:203],42
R 62,2,21504
v_fma_f32 v98,v98,v77,v77
M 38,a[212:215],46
v_exp_f32 v101,v101
M 34,a[196:199],42
v_rcp_f32 v98,v98
M 34,a[208:211],46
R 66,2,22528
v_fma_f32 v99,v99,v77,v77
M 30,a[192:195],42
v_rcp_f32 v99,v99
M 30,a[204:207],46
L 3
v_fma_f32 v100,v100,v77,v77
v_fma_f32 v101,v101,v77,v77
M 30,a[216:219],50
R 70,2,23552
v_mul_f32 v22,v22,v98
M 30,a[228:231],54
v_rcp_f32 v100,v100
M 34,a[220:223],50
v_rcp_f32 v101,v101
M 34,a[232:235],54
R 42,2,24576
v_mul_f32 v23,v23,v99
M 38,a[224:227],50
v_mul_f32 v24,v24,v100
v_mul_f32 v25,v25,v101
M 38,a[236:239],54
L 3
v_cvt_pk_bf16_f32 v108,v22,v23
v_cvt_pk_bf16_f32 v109,v24,v25
M 38,a[248:251],58
R 46,2,25600
v_fma_f32 v26,-v74,v90,v26
M 38,v[132:135],62
buffer_store_dwordx4 v[106:109],v0,s[36:39],s66 offen offset:0 sc0 sc1
v_fma_f32 v27,-v74,v91,v27
M 34,a[244:247],58
v_fma_f32 v28,-v74,v92,v28
v_fma_f32 v29,-v74,v93,v29
M 34,v[128:131],62
R 50,2,26624
v_mul_f32 v102,v26,v26
M 30,a[240:243],58
v_mul_f32 v103,v27,v27
v_mul_f32 v104,v28,v28
M 30,a[252:255],62
L 3
v_mul_f32 v105,v29,v29
v_fma_f32 v102,v76,v102,v75
M 30,v[136:139],66
R 54,2,27648
v_fma_f32 v103,v76,v103,v75
M 30,v[148:151],70
v_fma_f32 v104,v76,v104,v75
v_fma_f32 v105,v76,v105,v75
M 34,v[140:143],66
v_mul_f32 v102,v26,v102
v_mul_f32 v103,v27,v103
M 34,v[152:155],70
R 58,2,28672
v_mul_f32 v104,v28,v104
M 38,v[144:147],66
v_exp_f32 v102,v102
M 38,v[156:159],70
L 3
v_exp_f32 v103,v103
M 38,v[168:171],42
R 62,2,29696
v_mul_f32 v105,v29,v105
M 38,v[180:183],46
v_exp_f32 v104,v104
M 34,v[164:167],42
v_exp_f32 v105,v105
M 34,v[176:179],46
R 66,2,30720
v_fma_f32 v102,v102,v77,v77
M 30,v[160:163],42
v_rcp_f32 v102,v102
M 30,v[172:175],46
L 3
v_fma_f32 v103,v103,v77,v77
v_fma_f32 v104,v104,v77,v77
M 30,v[184:187],50
s_nop 0
R 70,2,31744
v_fma_f32 v105,v105,v77,v77
M 30,v[196:199],54
s_waitcnt vmcnt(1)
v_rcp_f32 v103,v103
M 34,v[188:191],50
s_barrier
buffer_store_dword v4,v3,s[36:39],s70 offen offset:0 sc0 sc1
M 34,v[200:203],54
s_mov_b32 s70,s69
ds_read_b32 v7,v6 offset:1024
R 78,13,2048
M 38,v[192:195],50
s_mov_b32 s69,s68
v_rcp_f32 v104,v104
M 38,v[204:207],54
L 4
v_rcp_f32 v105,v105
M 38,v[216:219],58
s_add_u32 s68,s68,s63
v_mul_f32 v26,v26,v102
v_mul_f32 v27,v27,v103
M 38,v[228:231],62
v_mul_f32 v28,v28,v104
v_mul_f32 v29,v29,v105
M 34,v[212:215],58
v_cvt_pk_bf16_f32 v110,v26,v27
v_cvt_pk_bf16_f32 v111,v28,v29
M 34,v[224:227],62
L 1
buffer_store_dwordx2 v[110:111],v10,s[36:39],s66 offen offset:0 sc0 sc1
M 30,v[208:211],58
v_cmp_eq_u32 vcc,s53,v7
M 30,v[220:223],62
s_nop 0
M 30,v[232:235],66
M 30,v[244:247],70
M 34,v[236:239],66
M 34,v[248:251],70
M 38,v[240:243],66
M 38,v[252:255],70
s_mov_b32 s80,0
s_cmp_eq_u32 vcc_lo,-1
s_cbranch_scc0 Lg3_xn1_%=
buffer_load_dword v114,v11,s[36:39],s73 offen offset:0 sc1
L 0
v_fma_f32 v30,-v78,v82,v30
v_fma_f32 v31,-v78,v83,v31
v_fma_f32 v32,-v78,v84,v32
v_fma_f32 v33,-v78,v85,v33
v_mul_f32 v94,v30,v30
v_mul_f32 v95,v31,v31
v_mul_f32 v96,v32,v32
v_mul_f32 v97,v33,v33
s_add_u32 s73,s73,s64
s_add_u32 m0,s57,0
s_add_u32 s56,s55,0
D 0,56,0
v_fma_f32 v94,v80,v94,v79
v_fma_f32 v95,v80,v95,v79
v_fma_f32 v96,v80,v96,v79
v_fma_f32 v97,v80,v97,v79
v_mul_f32 v94,v30,v94
v_mul_f32 v95,v31,v95
v_mul_f32 v96,v32,v96
v_mul_f32 v97,v33,v97
D 0,56,1024
v_exp_f32 v94,v94
v_exp_f32 v95,v95
v_exp_f32 v96,v96
v_exp_f32 v97,v97
v_fma_f32 v94,v94,v81,v81
v_fma_f32 v95,v95,v81,v81
v_fma_f32 v96,v96,v81,v81
v_fma_f32 v97,v97,v81,v81
D 0,56,2048
v_rcp_f32 v94,v94
v_rcp_f32 v95,v95
v_rcp_f32 v96,v96
v_rcp_f32 v97,v97
v_mul_f32 v30,v30,v94
v_mul_f32 v31,v31,v95
v_mul_f32 v32,v32,v96
v_mul_f32 v33,v33,v97
D 0,56,3072
v_cvt_pk_bf16_f32 v106,v30,v31
v_cvt_pk_bf16_f32 v107,v32,v33
v_fma_f32 v34,-v78,v86,v34
v_fma_f32 v35,-v78,v87,v35
v_fma_f32 v36,-v78,v88,v36
v_fma_f32 v37,-v78,v89,v37
v_mul_f32 v98,v34,v34
v_mul_f32 v99,v35,v35
s_add_u32 s58,s58,s61
s_add_u32 m0,s57,16384
s_add_u32 s56,s55,16384
D 0,56,0
v_mul_f32 v100,v36,v36
v_mul_f32 v101,v37,v37
v_fma_f32 v98,v80,v98,v79
v_fma_f32 v99,v80,v99,v79
v_fma_f32 v100,v80,v100,v79
v_fma_f32 v101,v80,v101,v79
v_mul_f32 v98,v34,v98
v_mul_f32 v99,v35,v99
D 0,56,1024
v_mul_f32 v100,v36,v100
v_mul_f32 v101,v37,v101
v_exp_f32 v98,v98
v_exp_f32 v99,v99
v_exp_f32 v100,v100
v_exp_f32 v101,v101
v_fma_f32 v98,v98,v81,v81
v_fma_f32 v99,v99,v81,v81
D 0,56,2048
v_fma_f32 v100,v100,v81,v81
v_fma_f32 v101,v101,v81,v81
v_rcp_f32 v98,v98
v_rcp_f32 v99,v99
v_rcp_f32 v100,v100
v_rcp_f32 v101,v101
v_mul_f32 v34,v34,v98
v_mul_f32 v35,v35,v99
D 0,56,3072
v_mul_f32 v36,v36,v100
v_mul_f32 v37,v37,v101
v_cvt_pk_bf16_f32 v108,v34,v35
v_cvt_pk_bf16_f32 v109,v36,v37
buffer_store_dwordx4 v[106:109],v0,s[36:39],s67 offen offset:0 sc0 sc1
v_fma_f32 v38,-v78,v90,v38
v_fma_f32 v39,-v78,v91,v39
v_fma_f32 v40,-v78,v92,v40
v_fma_f32 v41,-v78,v93,v41
s_add_u32 s55,s55,s60
s_sub_u32 s57,s84,s57
s_add_u32 s54,s54,s46
v_mul_f32 v102,v38,v38
v_mul_f32 v103,v39,v39
v_mul_f32 v104,v40,v40
v_mul_f32 v105,v41,v41
v_fma_f32 v102,v80,v102,v79
v_fma_f32 v103,v80,v103,v79
v_fma_f32 v104,v80,v104,v79
v_fma_f32 v105,v80,v105,v79
v_mul_f32 v102,v38,v102
v_mul_f32 v103,v39,v103
v_mul_f32 v104,v40,v104
v_mul_f32 v105,v41,v105
v_exp_f32 v102,v102
v_exp_f32 v103,v103
v_exp_f32 v104,v104
v_exp_f32 v105,v105
v_fma_f32 v102,v102,v81,v81
v_fma_f32 v103,v103,v81,v81
v_fma_f32 v104,v104,v81,v81
v_fma_f32 v105,v105,v81,v81
v_rcp_f32 v102,v102
v_rcp_f32 v103,v103
v_rcp_f32 v104,v104
v_rcp_f32 v105,v105
v_mul_f32 v38,v38,v102
v_mul_f32 v39,v39,v103
v_mul_f32 v40,v40,v104
v_mul_f32 v41,v41,v105
v_cvt_pk_bf16_f32 v110,v38,v39
v_cvt_pk_bf16_f32 v111,v40,v41
buffer_store_dwordx2 v[110:111],v10,s[36:39],s67 offen offset:0 sc0 sc1
s_mov_b32 s72,s69
s_mov_b32 s69,0x7fff0000
s_mov_b32 s67,0x7fff0000
s_mov_b32 s77,0x7fff0000
s_branch Lg3_gox_%=
Lg3_xn1_%=:
s_cmp_ge_u32 s54,256
s_cbranch_scc1 Lg3_fin1_%=
s_add_u32 m0,s59,4096
s_nop 0
buffer_load_dword v8,s[36:39],s58 offen offset:0 sc1 lds
buffer_store_dword v4,v3,s[36:39],s71 offen offset:0 sc0 sc1
s_add_u32 m0,s59,5120
s_nop 0
buffer_load_dword v8,s[36:39],s58 offen offset:0 sc1 lds
buffer_store_dword v4,v3,s[36:39],s71 offen offset:0 sc0 sc1
s_mov_b32 s83,0
L 0
v_fma_f32 v30,-v78,v82,v30
v_fma_f32 v31,-v78,v83,v31
v_fma_f32 v32,-v78,v84,v32
v_fma_f32 v33,-v78,v85,v33
v_mul_f32 v94,v30,v30
v_mul_f32 v95,v31,v31
v_mul_f32 v96,v32,v32
v_mul_f32 v97,v33,v33
v_fma_f32 v94,v80,v94,v79
v_fma_f32 v95,v80,v95,v79
v_fma_f32 v96,v80,v96,v79
v_fma_f32 v97,v80,v97,v79
v_mul_f32 v94,v30,v94
v_mul_f32 v95,v31,v95
v_mul_f32 v96,v32,v96
v_mul_f32 v97,v33,v97
v_exp_f32 v94,v94
v_exp_f32 v95,v95
v_exp_f32 v96,v96
v_exp_f32 v97,v97
v_fma_f32 v94,v94,v81,v81
v_fma_f32 v95,v95,v81,v81
v_fma_f32 v96,v96,v81,v81
v_fma_f32 v97,v97,v81,v81
v_rcp_f32 v94,v94
v_rcp_f32 v95,v95
v_rcp_f32 v96,v96
v_rcp_f32 v97,v97
v_mul_f32 v30,v30,v94
v_mul_f32 v31,v31,v95
v_mul_f32 v32,v32,v96
v_mul_f32 v33,v33,v97
v_cvt_pk_bf16_f32 v106,v30,v31
v_cvt_pk_bf16_f32 v107,v32,v33
v_fma_f32 v34,-v78,v86,v34
v_fma_f32 v35,-v78,v87,v35
v_fma_f32 v36,-v78,v88,v36
v_fma_f32 v37,-v78,v89,v37
v_mul_f32 v98,v34,v34
v_mul_f32 v99,v35,v35
v_mul_f32 v100,v36,v36
v_mul_f32 v101,v37,v37
v_fma_f32 v98,v80,v98,v79
v_fma_f32 v99,v80,v99,v79
v_fma_f32 v100,v80,v100,v79
v_fma_f32 v101,v80,v101,v79
v_mul_f32 v98,v34,v98
v_mul_f32 v99,v35,v99
v_mul_f32 v100,v36,v100
v_mul_f32 v101,v37,v101
v_exp_f32 v98,v98
v_exp_f32 v99,v99
v_exp_f32 v100,v100
v_exp_f32 v101,v101
v_fma_f32 v98,v98,v81,v81
v_fma_f32 v99,v99,v81,v81
v_fma_f32 v100,v100,v81,v81
v_fma_f32 v101,v101,v81,v81
v_rcp_f32 v98,v98
v_rcp_f32 v99,v99
v_rcp_f32 v100,v100
v_rcp_f32 v101,v101
v_mul_f32 v34,v34,v98
v_mul_f32 v35,v35,v99
v_mul_f32 v36,v36,v100
v_mul_f32 v37,v37,v101
v_cvt_pk_bf16_f32 v108,v34,v35
v_cvt_pk_bf16_f32 v109,v36,v37
buffer_store_dwordx4 v[106:109],v0,s[36:39],s67 offen offset:0 sc0 sc1
v_fma_f32 v38,-v78,v90,v38
v_fma_f32 v39,-v78,v91,v39
v_fma_f32 v40,-v78,v92,v40
v_fma_f32 v41,-v78,v93,v41
v_mul_f32 v102,v38,v38
v_mul_f32 v103,v39,v39
v_mul_f32 v104,v40,v40
v_mul_f32 v105,v41,v41
)"
R"(v_fma_f32 v102,v80,v102,v79
v_fma_f32 v103,v80,v103,v79
v_fma_f32 v104,v80,v104,v79
v_fma_f32 v105,v80,v105,v79
v_mul_f32 v102,v38,v102
v_mul_f32 v103,v39,v103
v_mul_f32 v104,v40,v104
v_mul_f32 v105,v41,v105
v_exp_f32 v102,v102
v_exp_f32 v103,v103
v_exp_f32 v104,v104
v_exp_f32 v105,v105
v_fma_f32 v102,v102,v81,v81
v_fma_f32 v103,v103,v81,v81
v_fma_f32 v104,v104,v81,v81
v_fma_f32 v105,v105,v81,v81
v_rcp_f32 v102,v102
v_rcp_f32 v103,v103
v_rcp_f32 v104,v104
v_rcp_f32 v105,v105
v_mul_f32 v38,v38,v102
v_mul_f32 v39,v39,v103
v_mul_f32 v40,v40,v104
v_mul_f32 v41,v41,v105
v_cvt_pk_bf16_f32 v110,v38,v39
v_cvt_pk_bf16_f32 v111,v40,v41
buffer_store_dwordx2 v[110:111],v10,s[36:39],s67 offen offset:0 sc0 sc1
s_mov_b32 s72,s69
s_mov_b32 s69,0x7fff0000
s_mov_b32 s67,0x7fff0000
s_mov_b32 s77,0x7fff0000
s_branch Lg3_poll_%=
Lg3_fin1_%=:
L 0
v_fma_f32 v30,-v78,v82,v30
v_fma_f32 v31,-v78,v83,v31
v_fma_f32 v32,-v78,v84,v32
v_fma_f32 v33,-v78,v85,v33
v_mul_f32 v94,v30,v30
v_mul_f32 v95,v31,v31
v_mul_f32 v96,v32,v32
v_mul_f32 v97,v33,v33
v_fma_f32 v94,v80,v94,v79
v_fma_f32 v95,v80,v95,v79
v_fma_f32 v96,v80,v96,v79
v_fma_f32 v97,v80,v97,v79
v_mul_f32 v94,v30,v94
v_mul_f32 v95,v31,v95
v_mul_f32 v96,v32,v96
v_mul_f32 v97,v33,v97
v_exp_f32 v94,v94
v_exp_f32 v95,v95
v_exp_f32 v96,v96
v_exp_f32 v97,v97
v_fma_f32 v94,v94,v81,v81
v_fma_f32 v95,v95,v81,v81
v_fma_f32 v96,v96,v81,v81
v_fma_f32 v97,v97,v81,v81
v_rcp_f32 v94,v94
v_rcp_f32 v95,v95
v_rcp_f32 v96,v96
v_rcp_f32 v97,v97
v_mul_f32 v30,v30,v94
v_mul_f32 v31,v31,v95
v_mul_f32 v32,v32,v96
v_mul_f32 v33,v33,v97
v_cvt_pk_bf16_f32 v106,v30,v31
v_cvt_pk_bf16_f32 v107,v32,v33
v_fma_f32 v34,-v78,v86,v34
v_fma_f32 v35,-v78,v87,v35
v_fma_f32 v36,-v78,v88,v36
v_fma_f32 v37,-v78,v89,v37
v_mul_f32 v98,v34,v34
v_mul_f32 v99,v35,v35
v_mul_f32 v100,v36,v36
v_mul_f32 v101,v37,v37
v_fma_f32 v98,v80,v98,v79
v_fma_f32 v99,v80,v99,v79
v_fma_f32 v100,v80,v100,v79
v_fma_f32 v101,v80,v101,v79
v_mul_f32 v98,v34,v98
v_mul_f32 v99,v35,v99
v_mul_f32 v100,v36,v100
v_mul_f32 v101,v37,v101
v_exp_f32 v98,v98
v_exp_f32 v99,v99
v_exp_f32 v100,v100
v_exp_f32 v101,v101
v_fma_f32 v98,v98,v81,v81
v_fma_f32 v99,v99,v81,v81
v_fma_f32 v100,v100,v81,v81
v_fma_f32 v101,v101,v81,v81
v_rcp_f32 v98,v98
v_rcp_f32 v99,v99
v_rcp_f32 v100,v100
v_rcp_f32 v101,v101
v_mul_f32 v34,v34,v98
v_mul_f32 v35,v35,v99
v_mul_f32 v36,v36,v100
v_mul_f32 v37,v37,v101
v_cvt_pk_bf16_f32 v108,v34,v35
v_cvt_pk_bf16_f32 v109,v36,v37
buffer_store_dwordx4 v[106:109],v0,s[36:39],s67 offen offset:0 sc0 sc1
v_fma_f32 v38,-v78,v90,v38
v_fma_f32 v39,-v78,v91,v39
v_fma_f32 v40,-v78,v92,v40
v_fma_f32 v41,-v78,v93,v41
v_mul_f32 v102,v38,v38
v_mul_f32 v103,v39,v39
v_mul_f32 v104,v40,v40
v_mul_f32 v105,v41,v41
v_fma_f32 v102,v80,v102,v79
v_fma_f32 v103,v80,v103,v79
v_fma_f32 v104,v80,v104,v79
v_fma_f32 v105,v80,v105,v79
v_mul_f32 v102,v38,v102
v_mul_f32 v103,v39,v103
v_mul_f32 v104,v40,v104
v_mul_f32 v105,v41,v105
v_exp_f32 v102,v102
v_exp_f32 v103,v103
v_exp_f32 v104,v104
v_exp_f32 v105,v105
v_fma_f32 v102,v102,v81,v81
v_fma_f32 v103,v103,v81,v81
v_fma_f32 v104,v104,v81,v81
v_fma_f32 v105,v105,v81,v81
v_rcp_f32 v102,v102
v_rcp_f32 v103,v103
v_rcp_f32 v104,v104
v_rcp_f32 v105,v105
v_mul_f32 v38,v38,v102
v_mul_f32 v39,v39,v103
v_mul_f32 v40,v40,v104
v_mul_f32 v41,v41,v105
v_cvt_pk_bf16_f32 v110,v38,v39
v_cvt_pk_bf16_f32 v111,v40,v41
buffer_store_dwordx2 v[110:111],v10,s[36:39],s67 offen offset:0 sc0 sc1
s_mov_b32 s72,s69
s_mov_b32 s69,0x7fff0000
s_mov_b32 s67,0x7fff0000
s_mov_b32 s77,0x7fff0000
s_branch Lg3_done_%=
Lg3_done_%=:
s_waitcnt vmcnt(2)
s_barrier
buffer_store_dword v4,v3,s[36:39],s70 offen offset:0 sc0 sc1
s_waitcnt vmcnt(1)
s_barrier
buffer_store_dword v4,v3,s[36:39],s72 offen offset:0 sc0 sc1
s_waitcnt vmcnt(0) lgkmcnt(0)
s_barrier
s_endpgm
Lcode_g3_e:
.purgem M
.purgem R
.purgem T
.purgem D
.purgem L
)"
  :: "s"(ws), "s"(__builtin_amdgcn_readfirstlane(sWave)), "s"(__builtin_amdgcn_readfirstlane(sG0)), "s"(__builtin_amdgcn_readfirstlane(sStep)), "s"(__builtin_amdgcn_readfirstlane(sOutOff)), "s"(__builtin_amdgcn_readfirstlane(sFoutOff)), "s"(__builtin_amdgcn_readfirstlane(sAuxOff)), "s"(__builtin_amdgcn_readfirstlane(sWw)), "s"(__builtin_amdgcn_readfirstlane(sBuf)), "s"(__builtin_amdgcn_readfirstlane(sExp)), "v"(vL16), "v"(vFlag), "v"(vSnapOff), "v"(vSnapRd), "v"(vWd0), "v"(vWd1), "v"(vWr0), "v"(vWr1), "v"(vWr2), "v"(vOutS), "v"(vAux0), "v"(vAux1), "v"(vAux2), "v"(vAux3), "v"(vAn), "v"(vAe), "s"(w3), "s"(out) : "v255", "v0", "v1", "v2", "v3", "v4", "v5", "v6", "v7", "v8", "v9", "v10", "v11", "v12", "v13", "v14", "v15", "v16", "v17", "v18", "v19", "v20", "v21", "v22", "v23", "v24", "v25", "v26", "v27", "v28", "v29", "v30", "v31", "v32", "v33", "v34", "v35", "v36", "v37", "v38", "v39", "v40", "v41", "v42", "v43", "v44", "v45", "v46", "v47", "v48", "v49", "v50", "v51", "v52", "v53", "v54", "v55", "v56", "v57", "v58", "v59", "v60", "v61", "v62", "v63", "v64", "v65", "v66", "v67", "v68", "v69", "v70", "v71", "v72", "v73", "v74", "v75", "v76", "v77", "v78", "v79", "v80", "v81", "v82", "v83", "v84", "v85", "v86", "v87", "v88", "v89", "v90", "v91", "v92", "v93", "v94", "v95", "v96", "v97", "v98", "v99", "v100", "v101", "v102", "v103", "v104", "v105", "v106", "v107", "v108", "v109", "v110", "v111", "v112", "v113", "v114", "v115", "v116", "v117", "v118", "v119", "v120", "v121", "v122", "v123", "v124", "v125", "v126", "v127", "v128", "v129", "v130", "v131", "v132", "v133", "v134", "v135", "v136", "v137", "v138", "v139", "v140", "v141", "v142", "v143", "v144", "v145", "v146", "v147", "v148", "v149", "v150", "v151", "v152", "v153", "v154", "v155", "v156", "v157", "v158", "v159", "v160", "v161", "v162", "v163", "v164", "v165", "v166", "v167", "v168", "v169", "v170", "v171", "v172", "v173", "v174", "v175", "v176", "v177", "v178", "v179", "v180", "v181", "v182", "v183", "v184", "v185", "v186", "v187", "v188", "v189", "v190", "v191", "v192", "v193", "v194", "v195", "v196", "v197", "v198", "v199", "v200", "v201", "v202", "v203", "v204", "v205", "v206", "v207", "v208", "v209", "v210", "v211", "v212", "v213", "v214", "v215", "v216", "v217", "v218", "v219", "v220", "v221", "v222", "v223", "v224", "v225", "v226", "v227", "v228", "v229", "v230", "v231", "a0", "a1", "a2", "a3", "a4", "a5", "a6", "a7", "a8", "a9", "a10", "a11", "a12", "a13", "a14", "a15", "a16", "a17", "a18", "a19", "a20", "a21", "a22", "a23", "a24", "a25", "a26", "a27", "a28", "a29", "a30", "a31", "a32", "a33", "a34", "a35", "a36", "a37", "a38", "a39", "a40", "a41", "a42", "a43", "a44", "a45", "a46", "a47", "a48", "a49", "a50", "a51", "a52", "a53", "a54", "a55", "a56", "a57", "a58", "a59", "a60", "a61", "a62", "a63", "a64", "a65", "a66", "a67", "a68", "a69", "a70", "a71", "a72", "a73", "a74", "a75", "a76", "a77", "a78", "a79", "a80", "a81", "a82", "a83", "a84", "a85", "a86", "a87", "a88", "a89", "a90", "a91", "a92", "a93", "a94", "a95", "a96", "a97", "a98", "a99", "a100", "a101", "a102", "a103", "a104", "a105", "a106", "a107", "a108", "a109", "a110", "a111", "a112", "a113", "a114", "a115", "a116", "a117", "a118", "a119", "a120", "a121", "a122", "a123", "a124", "a125", "a126", "a127", "a128", "a129", "a130", "a131", "a132", "a133", "a134", "a135", "a136", "a137", "a138", "a139", "a140", "a141", "a142", "a143", "a144", "a145", "a146", "a147", "a148", "a149", "a150", "a151", "a152", "a153", "a154", "a155", "a156", "a157", "a158", "a159", "a160", "a161", "a162", "a163", "a164", "a165", "a166", "a167", "a168", "a169", "a170", "a171", "a172", "a173", "a174", "a175", "a176", "a177", "a178", "a179", "a180", "a181", "a182", "a183", "a184", "a185", "a186", "a187", "a188", "a189", "a190", "a191", "a192", "a193", "a194", "a195", "a196", "a197", "a198", "a199", "a200", "a201", "a202", "a203", "a204", "a205", "a206", "a207", "a208", "a209", "a210", "a211", "a212", "a213", "a214", "a215", "a216", "a217", "a218", "a219", "a220", "a221", "a222", "a223", "a224", "a225", "a226", "a227", "a228", "a229", "a230", "a231", "a232", "a233", "a234", "a235", "a236", "a237", "a238", "a239", "a240", "a241", "a242", "a243", "a244", "a245", "a246", "a247", "a248", "a249", "a250", "a251", "a252", "a253", "a254", "a255", "s36", "s37", "s38", "s39", "s40", "s41", "s42", "s43", "s44", "s45", "s46", "s47", "s48", "s49", "s50", "s51", "s52", "s53", "s54", "s55", "s56", "s57", "s58", "s59", "s60", "s61", "s62", "s63", "s64", "s65", "s66", "s67", "s68", "s69", "s70", "s71", "s72", "s73", "s74", "s75", "s76", "s77", "s78", "s79", "s80", "s81", "s82", "s83", "s84", "s85", "s86", "s87", "s88", "s89", "s90", "s91", "s92", "s93", "s94", "s95", "s96", "s97", "vcc", "scc", "memory"); } else { asm volatile(
R"(.macro M d,w,x
v_mfma_f32_16x16x32_bf16 v[\d:\d+3], \w, v[\x:\x+3], v[\d:\d+3]
.endm
.macro R d,a,o
ds_read_b128 v[\d:\d+3], v\a offset:\o
.endm
.macro T d,a,o
ds_read_b64_tr_b16 \d, v\a offset:\o
.endm
.macro D a,s,o
buffer_load_dwordx4 v\a, s[36:39], s\s offen offset:\o sc1 lds
.endm
.macro L c
s_waitcnt lgkmcnt(\c)
.endm
Lcode_g3o_b:
s_mov_b64 s[36:37],%0
s_mov_b32 s38,111243392
s_mov_b32 s39,0x00020000
s_mov_b32 s44,%1
s_mov_b32 s45,%2
s_mov_b32 s46,%3
s_mov_b32 s47,%4
s_mov_b32 s48,%5
s_mov_b32 s49,%6
s_mov_b32 s50,%7
s_mov_b32 s51,%8
s_mov_b32 s53,%9
v_mov_b32 v0,%10
v_mov_b32 v3,%11
v_mov_b32 v5,%12
v_mov_b32 v6,%13
v_mov_b32 v18,%14
v_mov_b32 v19,%15
v_mov_b32 v20,%16
v_mov_b32 v21,%17
v_mov_b32 v22,%18
v_mov_b32 v10,%19
v_mov_b32 v11,%20
v_mov_b32 v12,%21
v_mov_b32 v13,%22
v_mov_b32 v14,%23
v_mov_b32 v15,%24
v_mov_b32 v16,%25
s_mov_b64 s[40:41],%26
s_mov_b32 s42,7340032
s_mov_b32 s43,0x00020000
s_lshl_b32 s57,s44,11
s_add_u32 s55,s50,0
s_add_u32 m0,s57,0
s_nop 0
buffer_load_dwordx4 v18,s[40:43],s55 offen offset:0  lds
s_add_u32 m0,s57,1024
s_nop 0
buffer_load_dwordx4 v19,s[40:43],s55 offen offset:0  lds
s_add_u32 m0,s57,8064
s_nop 0
buffer_load_dwordx4 v18,s[40:43],s55 offen offset:128  lds
s_add_u32 m0,s57,9088
s_nop 0
buffer_load_dwordx4 v19,s[40:43],s55 offen offset:128  lds
s_add_u32 s55,s50,458752
s_add_u32 m0,s57,16384
s_nop 0
buffer_load_dwordx4 v18,s[40:43],s55 offen offset:0  lds
s_add_u32 m0,s57,17408
s_nop 0
buffer_load_dwordx4 v19,s[40:43],s55 offen offset:0  lds
s_add_u32 m0,s57,24448
s_nop 0
buffer_load_dwordx4 v18,s[40:43],s55 offen offset:128  lds
s_add_u32 m0,s57,25472
s_nop 0
buffer_load_dwordx4 v19,s[40:43],s55 offen offset:128  lds
s_add_u32 s55,s50,917504
s_add_u32 m0,s57,32768
s_nop 0
buffer_load_dwordx4 v18,s[40:43],s55 offen offset:0  lds
s_add_u32 m0,s57,33792
s_nop 0
buffer_load_dwordx4 v19,s[40:43],s55 offen offset:0  lds
s_add_u32 m0,s57,40832
s_nop 0
buffer_load_dwordx4 v18,s[40:43],s55 offen offset:128  lds
s_add_u32 m0,s57,41856
s_nop 0
buffer_load_dwordx4 v19,s[40:43],s55 offen offset:128  lds
s_add_u32 s55,s50,1376256
s_add_u32 m0,s57,49152
s_nop 0
buffer_load_dwordx4 v18,s[40:43],s55 offen offset:0  lds
s_add_u32 m0,s57,50176
s_nop 0
buffer_load_dwordx4 v19,s[40:43],s55 offen offset:0  lds
s_add_u32 m0,s57,57216
s_nop 0
buffer_load_dwordx4 v18,s[40:43],s55 offen offset:128  lds
s_add_u32 m0,s57,58240
s_nop 0
buffer_load_dwordx4 v19,s[40:43],s55 offen offset:128  lds
s_add_u32 s55,s50,1835008
s_add_u32 m0,s57,65536
s_nop 0
buffer_load_dwordx4 v18,s[40:43],s55 offen offset:0  lds
s_add_u32 m0,s57,66560
s_nop 0
buffer_load_dwordx4 v19,s[40:43],s55 offen offset:0  lds
s_add_u32 m0,s57,73600
s_nop 0
buffer_load_dwordx4 v18,s[40:43],s55 offen offset:128  lds
s_add_u32 m0,s57,74624
s_nop 0
buffer_load_dwordx4 v19,s[40:43],s55 offen offset:128  lds
s_add_u32 s55,s50,2293760
s_add_u32 m0,s57,81920
s_nop 0
buffer_load_dwordx4 v18,s[40:43],s55 offen offset:0  lds
s_add_u32 m0,s57,82944
s_nop 0
buffer_load_dwordx4 v19,s[40:43],s55 offen offset:0  lds
s_add_u32 m0,s57,89984
s_nop 0
buffer_load_dwordx4 v18,s[40:43],s55 offen offset:128  lds
s_add_u32 m0,s57,91008
s_nop 0
buffer_load_dwordx4 v19,s[40:43],s55 offen offset:128  lds
s_waitcnt vmcnt(20) lgkmcnt(0)
s_barrier
s_add_u32 s55,s50,2752512
s_add_u32 m0,s57,98304
s_nop 0
buffer_load_dwordx4 v18,s[40:43],s55 offen offset:0  lds
s_add_u32 m0,s57,99328
s_nop 0
buffer_load_dwordx4 v19,s[40:43],s55 offen offset:0  lds
s_add_u32 m0,s57,106368
s_nop 0
buffer_load_dwordx4 v18,s[40:43],s55 offen offset:128  lds
s_add_u32 m0,s57,107392
s_nop 0
buffer_load_dwordx4 v19,s[40:43],s55 offen offset:128  lds
v_add_u32 v26,0,v20
v_add_u32 v27,0,v21
T a[0:1],26,0
T a[2:3],26,512
T a[4:5],27,0
T a[6:7],27,512
T a[8:9],26,4096
T a[10:11],26,4608
T a[12:13],27,4096
T a[14:15],27,4608
s_waitcnt vmcnt(20) lgkmcnt(0)
s_barrier
s_add_u32 s55,s50,3211264
s_add_u32 m0,s57,0
s_nop 0
buffer_load_dwordx4 v18,s[40:43],s55 offen offset:0  lds
s_add_u32 m0,s57,1024
s_nop 0
buffer_load_dwordx4 v19,s[40:43],s55 offen offset:0  lds
s_add_u32 m0,s57,8064
s_nop 0
buffer_load_dwordx4 v18,s[40:43],s55 offen offset:128  lds
s_add_u32 m0,s57,9088
s_nop 0
buffer_load_dwordx4 v19,s[40:43],s55 offen offset:128  lds
v_add_u32 v26,16384,v20
v_add_u32 v27,16384,v21
T a[16:17],26,0
T a[18:19],26,512
T a[20:21],27,0
T a[22:23],27,512
T a[24:25],26,4096
T a[26:27],26,4608
T a[28:29],27,4096
T a[30:31],27,4608
s_waitcnt vmcnt(20) lgkmcnt(0)
s_barrier
s_add_u32 s55,s50,3670016
s_add_u32 m0,s57,16384
s_nop 0
buffer_load_dwordx4 v18,s[40:43],s55 offen offset:0  lds
s_add_u32 m0,s57,17408
s_nop 0
buffer_load_dwordx4 v19,s[40:43],s55 offen offset:0  lds
s_add_u32 m0,s57,24448
s_nop 0
buffer_load_dwordx4 v18,s[40:43],s55 offen offset:128  lds
s_add_u32 m0,s57,25472
s_nop 0
buffer_load_dwordx4 v19,s[40:43],s55 offen offset:128  lds
v_add_u32 v26,32768,v20
v_add_u32 v27,32768,v21
T a[32:33],26,0
T a[34:35],26,512
T a[36:37],27,0
T a[38:39],27,512
T a[40:41],26,4096
T a[42:43],26,4608
T a[44:45],27,4096
T a[46:47],27,4608
s_waitcnt vmcnt(20) lgkmcnt(0)
s_barrier
s_add_u32 s55,s50,4128768
s_add_u32 m0,s57,32768
s_nop 0
buffer_load_dwordx4 v18,s[40:43],s55 offen offset:0  lds
s_add_u32 m0,s57,33792
s_nop 0
buffer_load_dwordx4 v19,s[40:43],s55 offen offset:0  lds
s_add_u32 m0,s57,40832
s_nop 0
buffer_load_dwordx4 v18,s[40:43],s55 offen offset:128  lds
s_add_u32 m0,s57,41856
s_nop 0
buffer_load_dwordx4 v19,s[40:43],s55 offen offset:128  lds
v_add_u32 v26,49152,v20
v_add_u32 v27,49152,v21
T a[48:49],26,0
T a[50:51],26,512
T a[52:53],27,0
T a[54:55],27,512
T a[56:57],26,4096
T a[58:59],26,4608
T a[60:61],27,4096
T a[62:63],27,4608
s_waitcnt vmcnt(20) lgkmcnt(0)
s_barrier
s_add_u32 s55,s50,4587520
s_add_u32 m0,s57,49152
s_nop 0
buffer_load_dwordx4 v18,s[40:43],s55 offen offset:0  lds
s_add_u32 m0,s57,50176
s_nop 0
buffer_load_dwordx4 v19,s[40:43],s55 offen offset:0  lds
s_add_u32 m0,s57,57216
s_nop 0
buffer_load_dwordx4 v18,s[40:43],s55 offen offset:128  lds
s_add_u32 m0,s57,58240
s_nop 0
buffer_load_dwordx4 v19,s[40:43],s55 offen offset:128  lds
v_add_u32 v26,65536,v20
v_add_u32 v27,65536,v21
T a[64:65],26,0
T a[66:67],26,512
T a[68:69],27,0
T a[70:71],27,512
T a[72:73],26,4096
T a[74:75],26,4608
T a[76:77],27,4096
T a[78:79],27,4608
s_waitcnt vmcnt(20) lgkmcnt(0)
s_barrier
s_add_u32 s55,s50,5046272
s_add_u32 m0,s57,65536
s_nop 0
buffer_load_dwordx4 v18,s[40:43],s55 offen offset:0  lds
s_add_u32 m0,s57,66560
s_nop 0
buffer_load_dwordx4 v19,s[40:43],s55 offen offset:0  lds
s_add_u32 m0,s57,73600
s_nop 0
buffer_load_dwordx4 v18,s[40:43],s55 offen offset:128  lds
s_add_u32 m0,s57,74624
s_nop 0
buffer_load_dwordx4 v19,s[40:43],s55 offen offset:128  lds
v_add_u32 v26,81920,v20
v_add_u32 v27,81920,v21
T a[80:81],26,0
T a[82:83],26,512
T a[84:85],27,0
T a[86:87],27,512
T a[88:89],26,4096
T a[90:91],26,4608
T a[92:93],27,4096
T a[94:95],27,4608
s_waitcnt vmcnt(20) lgkmcnt(0)
s_barrier
s_add_u32 s55,s50,5505024
s_add_u32 m0,s57,81920
s_nop 0
buffer_load_dwordx4 v18,s[40:43],s55 offen offset:0  lds
s_add_u32 m0,s57,82944
s_nop 0
buffer_load_dwordx4 v19,s[40:43],s55 offen offset:0  lds
s_add_u32 m0,s57,89984
s_nop 0
buffer_load_dwordx4 v18,s[40:43],s55 offen offset:128  lds
s_add_u32 m0,s57,91008
s_nop 0
buffer_load_dwordx4 v19,s[40:43],s55 offen offset:128  lds
v_add_u32 v26,98304,v20
v_add_u32 v27,98304,v21
T a[96:97],26,0
T a[98:99],26,512
T a[100:101],27,0
T a[102:103],27,512
T a[104:105],26,4096
T a[106:107],26,4608
T a[108:109],27,4096
T a[110:111],27,4608
s_waitcnt vmcnt(20) lgkmcnt(0)
s_barrier
s_add_u32 s55,s50,5963776
s_add_u32 m0,s57,98304
s_nop 0
buffer_load_dwordx4 v18,s[40:43],s55 offen offset:0  lds
s_add_u32 m0,s57,99328
s_nop 0
buffer_load_dwordx4 v19,s[40:43],s55 offen offset:0  lds
s_add_u32 m0,s57,106368
s_nop 0
buffer_load_dwordx4 v18,s[40:43],s55 offen offset:128  lds
s_add_u32 m0,s57,107392
s_nop 0
buffer_load_dwordx4 v19,s[40:43],s55 offen offset:128  lds
v_add_u32 v26,0,v20
v_add_u32 v27,0,v21
T a[112:113],26,0
T a[114:115],26,512
T a[116:117],27,0
T a[118:119],27,512
T a[120:121],26,4096
T a[122:123],26,4608
T a[124:125],27,4096
T a[126:127],27,4608
s_waitcnt vmcnt(20) lgkmcnt(0)
s_barrier
s_add_u32 s55,s50,6422528
s_add_u32 m0,s57,0
s_nop 0
buffer_load_dwordx4 v18,s[40:43],s55 offen offset:0  lds
s_add_u32 m0,s57,1024
s_nop 0
buffer_load_dwordx4 v19,s[40:43],s55 offen offset:0  lds
s_add_u32 m0,s57,8064
s_nop 0
buffer_load_dwordx4 v18,s[40:43],s55 offen offset:128  lds
s_add_u32 m0,s57,9088
s_nop 0
buffer_load_dwordx4 v19,s[40:43],s55 offen offset:128  lds
v_add_u32 v26,16384,v20
v_add_u32 v27,16384,v21
T a[128:129],26,0
T a[130:131],26,512
T a[132:133],27,0
T a[134:135],27,512
T a[136:137],26,4096
T a[138:139],26,4608
T a[140:141],27,4096
T a[142:143],27,4608
s_waitcnt vmcnt(20) lgkmcnt(0)
s_barrier
s_add_u32 s55,s50,6881280
s_add_u32 m0,s57,16384
s_nop 0
buffer_load_dwordx4 v18,s[40:43],s55 offen offset:0  lds
s_add_u32 m0,s57,17408
s_nop 0
buffer_load_dwordx4 v19,s[40:43],s55 offen offset:0  lds
s_add_u32 m0,s57,24448
s_nop 0
buffer_load_dwordx4 v18,s[40:43],s55 offen offset:128  lds
s_add_u32 m0,s57,25472
s_nop 0
buffer_load_dwordx4 v19,s[40:43],s55 offen offset:128  lds
v_add_u32 v26,32768,v20
v_add_u32 v27,32768,v21
T a[144:145],26,0
T a[146:147],26,512
T a[148:149],27,0
T a[150:151],27,512
T a[152:153],26,4096
T a[154:155],26,4608
T a[156:157],27,4096
T a[158:159],27,4608
s_waitcnt vmcnt(20) lgkmcnt(0)
s_barrier
v_add_u32 v26,49152,v20
v_add_u32 v27,49152,v21
T a[160:161],26,0
T a[162:163],26,512
T a[164:165],27,0
T a[166:167],27,512
T a[168:169],26,4096
T a[170:171],26,4608
T a[172:173],27,4096
T a[174:175],27,4608
s_waitcnt vmcnt(16) lgkmcnt(0)
s_barrier
v_add_u32 v26,65536,v20
v_add_u32 v27,65536,v21
T a[176:177],26,0
T a[178:179],26,512
T a[180:181],27,0
T a[182:183],27,512
T a[184:185],26,4096
T a[186:187],26,4608
T a[188:189],27,4096
T a[190:191],27,4608
s_waitcnt vmcnt(12) lgkmcnt(0)
s_barrier
v_add_u32 v26,81920,v20
v_add_u32 v27,81920,v21
T a[192:193],26,0
T a[194:195],26,512
T a[196:197],27,0
T a[198:199],27,512
T a[200:201],26,4096
T a[202:203],26,4608
T a[204:205],27,4096
T a[206:207],27,4608
s_waitcnt vmcnt(8) lgkmcnt(0)
s_barrier
v_add_u32 v26,98304,v20
v_add_u32 v27,98304,v21
T a[208:209],26,0
T a[210:211],26,512
T a[212:213],27,0
T a[214:215],27,512
T a[216:217],26,4096
T a[218:219],26,4608
T a[220:221],27,4096
T a[222:223],27,4608
s_waitcnt vmcnt(4) lgkmcnt(0)
s_barrier
v_add_u32 v26,0,v20
v_add_u32 v27,0,v21
T a[224:225],26,0
T a[226:227],26,512
T a[228:229],27,0
T a[230:231],27,512
T a[232:233],26,4096
T a[234:235],26,4608
T a[236:237],27,4096
T a[238:239],27,4608
s_waitcnt vmcnt(0) lgkmcnt(0)
s_barrier
v_add_u32 v26,16384,v20
v_add_u32 v27,16384,v21
T a[240:241],26,0
T a[242:243],26,512
T a[244:245],27,0
T a[246:247],27,512
T a[248:249],26,4096
T a[250:251],26,4608
T a[252:253],27,4096
T a[254:255],27,4608
L 0
s_barrier
v_mov_b32 v34,0x3f803f80
v_mov_b32 v35,0x3f803f80
v_mov_b32 v36,0x3f803f80
v_mov_b32 v37,0x3f803f80
s_nop 1
v_mfma_f32_16x16x32_bf16 v[106:109],a[0:3],v[34:37],0
v_mfma_f32_16x16x32_bf16 v[110:113],a[4:7],v[34:37],0
M 106,a[8:11],34
M 110,a[12:15],34
M 106,a[16:19],34
M 110,a[20:23],34
M 106,a[24:27],34
M 110,a[28:31],34
M 106,a[32:35],34
M 110,a[36:39],34
M 106,a[40:43],34
M 110,a[44:47],34
M 106,a[48:51],34
M 110,a[52:55],34
M 106,a[56:59],34
M 110,a[60:63],34
M 106,a[64:67],34
M 110,a[68:71],34
M 106,a[72:75],34
M 110,a[76:79],34
M 106,a[80:83],34
M 110,a[84:87],34
M 106,a[88:91],34
M 110,a[92:95],34
M 106,a[96:99],34
M 110,a[100:103],34
M 106,a[104:107],34
M 110,a[108:111],34
M 106,a[112:115],34
M 110,a[116:119],34
M 106,a[120:123],34
M 110,a[124:127],34
M 106,a[128:131],34
M 110,a[132:135],34
M 106,a[136:139],34
M 110,a[140:143],34
M 106,a[144:147],34
M 110,a[148:151],34
M 106,a[152:155],34
M 110,a[156:159],34
M 106,a[160:163],34
M 110,a[164:167],34
M 106,a[168:171],34
M 110,a[172:175],34
M 106,a[176:179],34
)"
R"(M 110,a[180:183],34
M 106,a[184:187],34
M 110,a[188:191],34
M 106,a[192:195],34
M 110,a[196:199],34
M 106,a[200:203],34
M 110,a[204:207],34
M 106,a[208:211],34
M 110,a[212:215],34
M 106,a[216:219],34
M 110,a[220:223],34
M 106,a[224:227],34
M 110,a[228:231],34
M 106,a[232:235],34
M 110,a[236:239],34
M 106,a[240:243],34
M 110,a[244:247],34
M 106,a[248:251],34
M 110,a[252:255],34
v_mov_b32 v4,s53
s_mov_b32 s82,0
s_mov_b32 s80,0
s_mov_b32 s54,s45
s_mul_i32 s57,s44,4096
s_mul_i32 s55,s45,32768
s_add_u32 s55,s55,s57
s_add_u32 s55,s55,31457280
s_mul_i32 s57,s44,4096
s_mul_i32 s60,s46,32768
s_mul_i32 s61,s46,1024
s_mul_i32 s62,s46,114688
s_mul_i32 s63,s46,2560
s_mul_i32 s64,s46,1024
s_lshl_b32 s59,s44,8
s_add_u32 s59,s59,131072
s_mul_i32 s58,s45,1024
s_add_u32 s58,s58,109613056
s_mul_i32 s65,s45,114688
s_add_u32 s65,s65,s47
s_mul_i32 s68,s45,2560
s_add_u32 s68,s68,s48
s_mul_i32 s73,s45,1024
s_add_u32 s73,s73,107479040
s_mul_i32 s74,s45,32768
s_add_u32 s74,s74,s49
s_mul_i32 s75,s45,1024
s_add_u32 s75,s75,107479040
s_mov_b32 s66,0x7fff0000
s_mov_b32 s67,0x7fff0000
s_mov_b32 s69,0x7fff0000
s_mov_b32 s76,0x7fff0000
s_mov_b32 s77,0x7fff0000
s_mov_b32 s70,0x7fff0000
s_mov_b32 s71,0x7fff0000
s_mov_b32 s72,0x7fff0000
v_mov_b32 v1,v0
v_add_u32 v2,16384,v0
s_mov_b32 s84,16384
s_mov_b32 s86,0x3a800000
s_mov_b32 s87,0x3727c5ac
s_mov_b32 s88,0xc0135761
s_mov_b32 s89,0x3d372713
s_lshr_b32 s85,s44,1
s_mul_i32 s85,s85,127
v_lshlrev_b32 v8,22,v0
v_and_b32 v8,0x80000000,v8
v_or_b32 v8,v8,v5
v_add_u32 v2,32768,v0
s_lshl_b32 s84,s57,1
s_add_u32 s84,s84,32768
v_and_b32 v114,240,v0
v_cmp_eq_u32 vcc,0,v114
s_nop 1
v_cndmask_b32 v17,0,1.0,vcc
s_getpc_b64 s[40:41]
Lg3o_pc_%=:
s_lshl_b32 s78,s44,13
v_lshlrev_b32 v114,3,v0
v_add_u32 v114,s78,v114
v_min_u32 v114,Lcode_g3o_e-Lg3o_pc_%=-4,v114
s_mov_b32 m0,163584
s_nop 0
global_load_lds_dword v114,s[40:41]
s_add_u32 m0,s59,4096
s_nop 0
buffer_load_dword v8,s[36:39],s58 offen offset:0 sc1 lds
buffer_store_dword v4,v3,s[36:39],s71 offen offset:0 sc0 sc1
s_add_u32 m0,s59,5120
s_nop 0
buffer_load_dword v8,s[36:39],s58 offen offset:0 sc1 lds
buffer_store_dword v4,v3,s[36:39],s71 offen offset:0 sc0 sc1
s_mov_b32 s83,0
Lg3o_poll_%=:
s_add_u32 s78,s83,2
s_and_b32 s78,s78,3
s_lshl_b32 s78,s78,10
s_add_u32 s78,s78,4096
s_add_u32 m0,s59,s78
s_nop 0
buffer_load_dword v8,s[36:39],s58 offen offset:0 sc1 lds
s_and_b32 s78,s83,3
s_lshl_b32 s78,s78,10
v_add_u32 v114,s78,v6
s_waitcnt vmcnt(4)
s_barrier
ds_read_b32 v7,v114 offset:4096
buffer_store_dword v4,v3,s[36:39],s70 offen offset:0 sc0 sc1
s_mov_b32 s70,s71
s_mov_b32 s71,s72
s_mov_b32 s72,0x7fff0000
s_add_u32 s83,s83,1
s_add_u32 s82,s82,1
L 0
v_cmp_eq_u32 vcc,s53,v7
s_nop 0
s_cmp_eq_u32 vcc_lo,-1
s_cbranch_scc1 Lg3o_go_%=
s_cmp_lt_u32 s82,20000
s_cbranch_scc1 Lg3o_poll_%=
Lg3o_go_%=:
buffer_load_dword v130,v11,s[36:39],s73 offen offset:0 sc1
s_add_u32 s73,s73,s64
s_add_u32 m0,s57,0
s_add_u32 s56,s55,0
D 0,56,0
D 0,56,1024
D 0,56,2048
D 0,56,3072
s_add_u32 s58,s58,s61
s_add_u32 m0,s59,2048
s_nop 0
buffer_load_dword v8,s[36:39],s58 offen offset:0 sc1 lds
s_add_u32 m0,s57,16384
s_add_u32 s56,s55,16384
D 0,56,0
D 0,56,1024
D 0,56,2048
D 0,56,3072
s_add_u32 s55,s55,s60
s_sub_u32 s57,s84,s57
s_add_u32 s54,s54,s46
s_waitcnt vmcnt(4)
s_barrier
buffer_store_dword v4,v3,s[36:39],s70 offen offset:0 sc0 sc1
s_min_u32 s71,s71,s72
buffer_store_dword v4,v3,s[36:39],s71 offen offset:0 sc0 sc1
s_mov_b32 s70,0x7fff0000
s_mov_b32 s71,0x7fff0000
s_mov_b32 s72,0x7fff0000
s_cmp_eq_u32 s80,1
s_cbranch_scc1 Lg3o_st1_%=
s_branch Lg3o_st0_%=
Lg3o_gox_%=:
s_add_u32 m0,s59,2048
s_nop 0
buffer_load_dword v8,s[36:39],s58 offen offset:0 sc1 lds
s_waitcnt vmcnt(0)
s_barrier
buffer_store_dword v4,v3,s[36:39],s70 offen offset:0 sc0 sc1
s_min_u32 s71,s71,s72
buffer_store_dword v4,v3,s[36:39],s71 offen offset:0 sc0 sc1
s_mov_b32 s70,0x7fff0000
s_mov_b32 s71,0x7fff0000
s_mov_b32 s72,0x7fff0000
s_cmp_eq_u32 s80,1
s_cbranch_scc1 Lg3o_st1_%=
Lg3o_st0_%=:
ds_read_b32 v7,v6 offset:2048
R 34,1,0
R 38,1,1024
R 42,1,2048
R 46,1,3072
R 50,1,4096
L 5
v_cmp_eq_u32 vcc,s53,v7
L 3
s_cmp_eq_u32 vcc_lo,-1
s_cbranch_scc1 Lg3o_F0_%=
s_branch Lg3o_L0_%=
Lg3o_st1_%=:
ds_read_b32 v7,v6 offset:2048
R 34,2,0
R 38,2,1024
R 42,2,2048
R 46,2,3072
R 50,2,4096
L 5
v_cmp_eq_u32 vcc,s53,v7
L 3
s_cmp_eq_u32 vcc_lo,-1
s_cbranch_scc1 Lg3o_F1_%=
s_branch Lg3o_L1_%=
Lg3o_F0_%=:
v_mfma_f32_16x16x32_bf16 v[22:25],a[4:7],v[34:37],0
s_add_u32 s58,s58,s61
s_add_u32 m0,s59,0
R 54,1,5120
M 22,a[12:15],38
buffer_load_dword v8,s[36:39],s58 offen offset:0 sc1 lds
v_mfma_f32_16x16x32_bf16 v[18:21],a[0:3],v[34:37],0
s_add_u32 m0,s57,0
R 58,1,6144
v_add_f32_dpp v131,v130,v130 quad_perm:[1,0,3,2] row_mask:0xf bank_mask:0xf
M 18,a[8:11],38
L 3
s_add_u32 s56,s55,0
buffer_load_dword v130,v11,s[36:39],s73 offen offset:0 sc1
M 18,a[16:19],42
s_mov_b32 s66,s65
R 62,1,7168
v_add_f32_dpp v131,v131,v131 quad_perm:[2,3,0,1] row_mask:0xf bank_mask:0xf
M 18,a[24:27],46
D 0,56,0
M 22,a[20:23],42
s_add_u32 s65,s65,s62
R 66,1,8192
v_add_f32_dpp v134,v131,v131 row_half_mirror row_mask:0xf bank_mask:0xf
M 22,a[28:31],46
L 3
D 0,56,1024
M 22,a[36:39],50
s_add_u32 s54,s54,s46
R 70,1,9216
v_mov_b32_dpp v131,v134 row_ror:8 row_mask:0xf bank_mask:0xf
M 22,a[44:47],54
s_add_u32 s73,s73,s64
v_fma_f32 v131,-v134,v134,v131
v_max_f32 v131,s87,v131
M 18,a[32:35],50
R 74,1,10240
v_mul_f32 v134,v17,v134
M 18,a[40:43],54
L 3
D 0,56,2048
M 18,a[48:51],58
R 78,1,11264
v_fma_f32 v26,-v102,v106,v26
M 18,a[56:59],62
D 0,56,3072
M 22,a[52:55],58
s_add_u32 m0,s57,16384
R 82,1,12288
v_fma_f32 v27,-v102,v107,v27
M 22,a[60:63],62
L 3
v_rsq_f32 v132,v131
M 22,a[68:71],66
s_add_u32 s56,s55,16384
R 86,1,13312
v_fma_f32 v131,v15,v131,v16
M 22,a[76:79],70
s_add_u32 s55,s55,s60
v_mul_f32 v135,v132,v132
v_fma_f32 v131,v14,v135,v131
M 18,a[64:67],66
s_sub_u32 s57,s84,s57
R 90,1,14336
v_fma_f32 v134,v132,v131,v134
M 18,a[72:75],70
L 3
ds_write_b32 v12,v134 offset:0
M 18,a[80:83],74
s_waitcnt vmcnt(8)
R 94,1,15360
v_fma_f32 v28,-v102,v108,v28
M 18,a[88:91],78
s_barrier
v_fma_f32 v29,-v102,v109,v29
v_mul_f32 v114,v26,v26
M 22,a[84:87],74
R 34,1,16384
v_mul_f32 v115,v27,v27
M 22,a[92:95],78
L 4
v_mul_f32 v116,v28,v28
v_mul_f32 v117,v29,v29
M 22,a[100:103],82
R 38,1,17408
v_fma_f32 v114,v104,v114,v103
M 22,a[108:111],86
D 0,56,0
M 18,a[96:99],82
R 42,1,18432
v_fma_f32 v115,v104,v115,v103
M 18,a[104:107],86
L 3
v_fma_f32 v116,v104,v116,v103
v_fma_f32 v117,v104,v117,v103
M 18,a[112:115],90
R 46,1,19456
v_mul_f32 v114,v26,v114
M 18,a[120:123],94
v_exp_f32 v114,v114
M 22,a[116:119],90
R 50,1,20480
v_mul_f32 v115,v27,v115
M 22,a[124:127],94
L 3
v_exp_f32 v115,v115
M 22,a[132:135],34
R 54,1,21504
v_mul_f32 v116,v28,v116
M 22,a[140:143],38
D 0,56,1024
M 18,a[128:131],34
R 58,1,22528
v_mul_f32 v117,v29,v117
M 18,a[136:139],38
L 3
v_exp_f32 v116,v116
M 18,a[144:147],42
R 62,1,23552
v_fma_f32 v114,v114,v105,v105
M 18,a[152:155],46
v_exp_f32 v117,v117
M 22,a[148:151],42
R 66,1,24576
v_fma_f32 v115,v115,v105,v105
M 22,a[156:159],46
L 3
D 0,56,2048
M 22,a[164:167],50
R 70,1,25600
v_fma_f32 v116,v116,v105,v105
M 22,a[172:175],54
v_rcp_f32 v114,v114
M 18,a[160:163],50
R 74,1,26624
v_fma_f32 v117,v117,v105,v105
M 18,a[168:171],54
L 3
v_rcp_f32 v115,v115
M 18,a[176:179],58
R 78,1,27648
v_mul_f32 v26,v26,v114
M 18,a[184:187],62
v_rcp_f32 v116,v116
M 22,a[180:183],58
R 82,1,28672
v_mul_f32 v27,v27,v115
M 22,a[188:191],62
L 3
D 0,56,3072
M 22,a[196:199],66
R 86,1,29696
v_mul_f32 v28,v28,v116
M 22,a[204:207],70
v_rcp_f32 v117,v117
M 18,a[192:195],66
R 90,1,30720
v_mul_f32 v29,v29,v117
M 18,a[200:203],70
L 3
s_nop 0
v_cvt_pk_bf16_f32 v122,v26,v27
M 18,a[208:211],74
s_waitcnt vmcnt(4)
R 94,1,31744
v_fma_f32 v30,-v102,v110,v30
M 18,a[216:219],78
s_barrier
ds_read_b32 v7,v6 offset:0
R 98,13,0
M 22,a[212:215],74
R 34,2,0
v_fma_f32 v31,-v102,v111,v31
M 22,a[220:223],78
L 5
buffer_store_dword v4,v3,s[36:39],s70 offen offset:0 sc0 sc1
M 22,a[228:231],82
s_mov_b32 s70,s69
R 38,2,1024
v_fma_f32 v32,-v102,v112,v32
M 22,a[236:239],86
s_mov_b32 s69,s68
v_fma_f32 v33,-v102,v113,v33
v_mul_f32 v118,v30,v30
M 18,a[224:227],82
s_add_u32 s68,s68,s63
R 42,2,2048
v_mul_f32 v119,v31,v31
M 18,a[232:235],86
L 5
v_mul_f32 v120,v32,v32
v_mul_f32 v121,v33,v33
v_fma_f32 v118,v104,v118,v103
v_fma_f32 v119,v104,v119,v103
v_fma_f32 v120,v104,v120,v103
v_fma_f32 v121,v104,v121,v103
v_mul_f32 v118,v30,v118
v_mul_f32 v119,v31,v119
v_mul_f32 v120,v32,v120
v_mul_f32 v121,v33,v121
v_exp_f32 v118,v118
v_exp_f32 v119,v119
v_exp_f32 v120,v120
v_exp_f32 v121,v121
M 18,a[240:243],90
L 4
R 46,2,3072
v_fma_f32 v118,v118,v105,v105
v_fma_f32 v119,v119,v105,v105
v_fma_f32 v120,v120,v105,v105
v_fma_f32 v121,v121,v105,v105
v_rcp_f32 v118,v118
v_rcp_f32 v119,v119
v_rcp_f32 v120,v120
v_rcp_f32 v121,v121
v_cmp_eq_u32 vcc,s53,v7
M 18,a[248:251],94
v_cvt_pk_bf16_f32 v123,v28,v29
v_mul_f32 v30,v30,v118
v_mul_f32 v31,v31,v119
v_mul_f32 v32,v32,v120
v_mul_f32 v33,v33,v121
v_cvt_pk_bf16_f32 v124,v30,v31
v_cvt_pk_bf16_f32 v125,v32,v33
M 22,a[244:247],90
L 2
s_cmp_eq_u32 vcc_lo,-1
R 50,2,4096
buffer_store_dwordx4 v[122:125],v0,s[36:39],s67 offen offset:0 sc0 sc1
M 22,a[252:255],94
s_cbranch_scc0 Lg3o_K1_%=
Lg3o_F1_%=:
v_mfma_f32_16x16x32_bf16 v[30:33],a[4:7],v[34:37],0
s_add_u32 s58,s58,s61
s_add_u32 m0,s59,1024
R 54,2,5120
M 30,a[12:15],38
buffer_load_dword v8,s[36:39],s58 offen offset:0 sc1 lds
v_mfma_f32_16x16x32_bf16 v[26:29],a[0:3],v[34:37],0
s_add_u32 m0,s57,0
R 58,2,6144
v_add_f32_dpp v131,v130,v130 quad_perm:[1,0,3,2] row_mask:0xf bank_mask:0xf
M 26,a[8:11],38
L 3
s_add_u32 s56,s55,0
buffer_load_dword v130,v11,s[36:39],s73 offen offset:0 sc1
M 26,a[16:19],42
s_mov_b32 s67,s65
R 62,2,7168
v_add_f32_dpp v131,v131,v131 quad_perm:[2,3,0,1] row_mask:0xf bank_mask:0xf
M 26,a[24:27],46
D 0,56,0
M 30,a[20:23],42
s_add_u32 s65,s65,s62
R 66,2,8192
v_add_f32_dpp v134,v131,v131 row_half_mirror row_mask:0xf bank_mask:0xf
M 30,a[28:31],46
L 3
D 0,56,1024
M 30,a[36:39],50
s_add_u32 s54,s54,s46
R 70,2,9216
v_mov_b32_dpp v131,v134 row_ror:8 row_mask:0xf bank_mask:0xf
M 30,a[44:47],54
s_add_u32 s73,s73,s64
v_fma_f32 v131,-v134,v134,v131
v_max_f32 v131,s87,v131
M 26,a[32:35],50
R 74,2,10240
v_mul_f32 v134,v17,v134
M 26,a[40:43],54
L 3
D 0,56,2048
M 26,a[48:51],58
R 78,2,11264
v_fma_f32 v18,-v98,v106,v18
M 26,a[56:59],62
D 0,56,3072
M 30,a[52:55],58
s_add_u32 m0,s57,16384
R 82,2,12288
v_fma_f32 v19,-v98,v107,v19
M 30,a[60:63],62
L 3
v_rsq_f32 v132,v131
M 30,a[68:71],66
s_add_u32 s56,s55,16384
R 86,2,13312
v_fma_f32 v131,v15,v131,v16
M 30,a[76:79],70
s_add_u32 s55,s55,s60
v_mul_f32 v135,v132,v132
v_fma_f32 v131,v14,v135,v131
M 26,a[64:67],66
s_sub_u32 s57,s84,s57
R 90,2,14336
v_fma_f32 v134,v132,v131,v134
M 26,a[72:75],70
L 3
ds_write_b32 v12,v134 offset:2048
M 26,a[80:83],74
s_waitcnt vmcnt(8)
R 94,2,15360
v_fma_f32 v20,-v98,v108,v20
M 26,a[88:91],78
s_barrier
v_fma_f32 v21,-v98,v109,v21
)"
R"(v_mul_f32 v114,v18,v18
M 30,a[84:87],74
R 34,2,16384
v_mul_f32 v115,v19,v19
M 30,a[92:95],78
L 4
v_mul_f32 v116,v20,v20
v_mul_f32 v117,v21,v21
M 30,a[100:103],82
R 38,2,17408
v_fma_f32 v114,v100,v114,v99
M 30,a[108:111],86
D 0,56,0
M 26,a[96:99],82
R 42,2,18432
v_fma_f32 v115,v100,v115,v99
M 26,a[104:107],86
L 3
v_fma_f32 v116,v100,v116,v99
v_fma_f32 v117,v100,v117,v99
M 26,a[112:115],90
R 46,2,19456
v_mul_f32 v114,v18,v114
M 26,a[120:123],94
v_exp_f32 v114,v114
M 30,a[116:119],90
R 50,2,20480
v_mul_f32 v115,v19,v115
M 30,a[124:127],94
L 3
v_exp_f32 v115,v115
M 30,a[132:135],34
R 54,2,21504
v_mul_f32 v116,v20,v116
M 30,a[140:143],38
D 0,56,1024
M 26,a[128:131],34
R 58,2,22528
v_mul_f32 v117,v21,v117
M 26,a[136:139],38
L 3
v_exp_f32 v116,v116
M 26,a[144:147],42
R 62,2,23552
v_fma_f32 v114,v114,v101,v101
M 26,a[152:155],46
v_exp_f32 v117,v117
M 30,a[148:151],42
R 66,2,24576
v_fma_f32 v115,v115,v101,v101
M 30,a[156:159],46
L 3
D 0,56,2048
M 30,a[164:167],50
R 70,2,25600
v_fma_f32 v116,v116,v101,v101
M 30,a[172:175],54
v_rcp_f32 v114,v114
M 26,a[160:163],50
R 74,2,26624
v_fma_f32 v117,v117,v101,v101
M 26,a[168:171],54
L 3
v_rcp_f32 v115,v115
M 26,a[176:179],58
R 78,2,27648
v_mul_f32 v18,v18,v114
M 26,a[184:187],62
v_rcp_f32 v116,v116
M 30,a[180:183],58
R 82,2,28672
v_mul_f32 v19,v19,v115
M 30,a[188:191],62
L 3
D 0,56,3072
M 30,a[196:199],66
R 86,2,29696
v_mul_f32 v20,v20,v116
M 30,a[204:207],70
v_rcp_f32 v117,v117
M 26,a[192:195],66
R 90,2,30720
v_mul_f32 v21,v21,v117
M 26,a[200:203],70
L 3
s_nop 0
v_cvt_pk_bf16_f32 v122,v18,v19
M 26,a[208:211],74
s_waitcnt vmcnt(4)
R 94,2,31744
v_fma_f32 v22,-v98,v110,v22
M 26,a[216:219],78
s_barrier
ds_read_b32 v7,v6 offset:1024
R 102,13,2048
M 30,a[212:215],74
R 34,1,0
v_fma_f32 v23,-v98,v111,v23
M 30,a[220:223],78
L 5
buffer_store_dword v4,v3,s[36:39],s70 offen offset:0 sc0 sc1
M 30,a[228:231],82
s_mov_b32 s70,s69
R 38,1,1024
v_fma_f32 v24,-v98,v112,v24
M 30,a[236:239],86
s_mov_b32 s69,s68
v_fma_f32 v25,-v98,v113,v25
v_mul_f32 v118,v22,v22
M 26,a[224:227],82
s_add_u32 s68,s68,s63
R 42,1,2048
v_mul_f32 v119,v23,v23
M 26,a[232:235],86
L 5
v_mul_f32 v120,v24,v24
v_mul_f32 v121,v25,v25
v_fma_f32 v118,v100,v118,v99
v_fma_f32 v119,v100,v119,v99
v_fma_f32 v120,v100,v120,v99
v_fma_f32 v121,v100,v121,v99
v_mul_f32 v118,v22,v118
v_mul_f32 v119,v23,v119
v_mul_f32 v120,v24,v120
v_mul_f32 v121,v25,v121
v_exp_f32 v118,v118
v_exp_f32 v119,v119
v_exp_f32 v120,v120
v_exp_f32 v121,v121
M 26,a[240:243],90
L 4
R 46,1,3072
v_fma_f32 v118,v118,v101,v101
v_fma_f32 v119,v119,v101,v101
v_fma_f32 v120,v120,v101,v101
v_fma_f32 v121,v121,v101,v101
v_rcp_f32 v118,v118
v_rcp_f32 v119,v119
v_rcp_f32 v120,v120
v_rcp_f32 v121,v121
v_cmp_eq_u32 vcc,s53,v7
M 26,a[248:251],94
v_cvt_pk_bf16_f32 v123,v20,v21
v_mul_f32 v22,v22,v118
v_mul_f32 v23,v23,v119
v_mul_f32 v24,v24,v120
v_mul_f32 v25,v25,v121
v_cvt_pk_bf16_f32 v124,v22,v23
v_cvt_pk_bf16_f32 v125,v24,v25
M 30,a[244:247],90
L 2
s_cmp_eq_u32 vcc_lo,-1
R 50,1,4096
buffer_store_dwordx4 v[122:125],v0,s[36:39],s66 offen offset:0 sc0 sc1
M 30,a[252:255],94
s_cbranch_scc1 Lg3o_F0_%=
Lg3o_K0_%=:
Lg3o_L0_%=:
v_mfma_f32_16x16x32_bf16 v[22:25],a[4:7],v[34:37],0
s_add_u32 m0,s59,3072
R 54,1,5120
v_add_f32_dpp v131,v130,v130 quad_perm:[1,0,3,2] row_mask:0xf bank_mask:0xf
M 22,a[12:15],38
buffer_load_dword v8,s[36:39],s58 offen offset:0 sc1 lds
v_mfma_f32_16x16x32_bf16 v[18:21],a[0:3],v[34:37],0
s_mov_b32 s66,s65
R 58,1,6144
v_add_f32_dpp v131,v131,v131 quad_perm:[2,3,0,1] row_mask:0xf bank_mask:0xf
M 18,a[8:11],38
L 3
v_fma_f32 v26,-v102,v106,v26
v_fma_f32 v27,-v102,v107,v27
M 18,a[16:19],42
s_add_u32 s65,s65,s62
R 62,1,7168
v_add_f32_dpp v134,v131,v131 row_half_mirror row_mask:0xf bank_mask:0xf
M 18,a[24:27],46
v_fma_f32 v28,-v102,v108,v28
v_fma_f32 v29,-v102,v109,v29
M 22,a[20:23],42
R 66,1,8192
v_mov_b32_dpp v131,v134 row_ror:8 row_mask:0xf bank_mask:0xf
M 22,a[28:31],46
L 3
v_fma_f32 v131,-v134,v134,v131
v_max_f32 v131,s87,v131
M 22,a[36:39],50
R 70,1,9216
v_mul_f32 v134,v17,v134
M 22,a[44:47],54
v_rsq_f32 v132,v131
M 18,a[32:35],50
R 74,1,10240
v_fma_f32 v131,v15,v131,v16
M 18,a[40:43],54
L 3
v_mul_f32 v135,v132,v132
v_fma_f32 v131,v14,v135,v131
M 18,a[48:51],58
R 78,1,11264
v_fma_f32 v134,v132,v131,v134
M 18,a[56:59],62
ds_write_b32 v12,v134 offset:0
M 22,a[52:55],58
R 82,1,12288
v_mul_f32 v114,v26,v26
M 22,a[60:63],62
L 4
v_mul_f32 v115,v27,v27
v_mul_f32 v116,v28,v28
M 22,a[68:71],66
R 86,1,13312
v_mul_f32 v117,v29,v29
M 22,a[76:79],70
v_fma_f32 v114,v104,v114,v103
v_fma_f32 v115,v104,v115,v103
M 18,a[64:67],66
R 90,1,14336
v_fma_f32 v116,v104,v116,v103
M 18,a[72:75],70
L 4
v_fma_f32 v117,v104,v117,v103
v_mul_f32 v114,v26,v114
M 18,a[80:83],74
s_waitcnt vmcnt(0)
R 94,1,15360
v_mul_f32 v115,v27,v115
M 18,a[88:91],78
s_barrier
ds_read_b32 v7,v6 offset:3072
v_mul_f32 v116,v28,v116
M 22,a[84:87],74
R 34,1,16384
v_mul_f32 v117,v29,v117
M 22,a[92:95],78
L 4
buffer_store_dword v4,v3,s[36:39],s70 offen offset:0 sc0 sc1
M 22,a[100:103],82
s_mov_b32 s70,0x7fff0000
R 38,1,17408
v_fma_f32 v30,-v102,v110,v30
M 22,a[108:111],86
v_exp_f32 v114,v114
M 18,a[96:99],82
R 42,1,18432
v_fma_f32 v114,v114,v105,v105
M 18,a[104:107],86
L 4
L 3
v_cmp_eq_u32 vcc,s53,v7
M 18,a[112:115],90
R 46,1,19456
v_fma_f32 v31,-v102,v111,v31
M 18,a[120:123],94
v_exp_f32 v115,v115
M 22,a[116:119],90
s_cmp_eq_u32 vcc_lo,-1
R 50,1,20480
v_fma_f32 v115,v115,v105,v105
s_cbranch_scc0 Lg3o_X0_%=
M 22,a[124:127],94
L 3
s_add_u32 m0,s57,0
s_add_u32 s56,s55,0
buffer_load_dword v130,v11,s[36:39],s73 offen offset:0 sc1
M 22,a[132:135],34
s_add_u32 s58,s58,s61
R 54,1,21504
v_fma_f32 v32,-v102,v112,v32
M 22,a[140:143],38
D 0,56,0
M 18,a[128:131],34
s_add_u32 s54,s54,s46
R 58,1,22528
v_fma_f32 v33,-v102,v113,v33
M 18,a[136:139],38
L 3
v_exp_f32 v116,v116
M 18,a[144:147],42
s_add_u32 s73,s73,s64
R 62,1,23552
v_fma_f32 v116,v116,v105,v105
M 18,a[152:155],46
D 0,56,1024
M 22,a[148:151],42
R 66,1,24576
v_mul_f32 v118,v30,v30
M 22,a[156:159],46
L 3
v_exp_f32 v117,v117
M 22,a[164:167],50
R 70,1,25600
v_fma_f32 v117,v117,v105,v105
M 22,a[172:175],54
D 0,56,2048
M 18,a[160:163],50
R 74,1,26624
v_mul_f32 v119,v31,v31
M 18,a[168:171],54
L 3
v_rcp_f32 v114,v114
M 18,a[176:179],58
R 78,1,27648
v_mul_f32 v26,v26,v114
M 18,a[184:187],62
D 0,56,3072
M 22,a[180:183],58
s_add_u32 m0,s59,0
R 82,1,28672
v_mul_f32 v120,v32,v32
M 22,a[188:191],62
L 3
buffer_load_dword v8,s[36:39],s58 offen offset:0 sc1 lds
M 22,a[196:199],66
s_add_u32 m0,s57,16384
R 86,1,29696
v_mul_f32 v121,v33,v33
M 22,a[204:207],70
s_add_u32 s56,s55,16384
v_rcp_f32 v115,v115
M 18,a[192:195],66
s_add_u32 s55,s55,s60
R 90,1,30720
v_mul_f32 v27,v27,v115
M 18,a[200:203],70
L 3
s_nop 0
D 0,56,0
M 18,a[208:211],74
s_waitcnt vmcnt(1)
R 94,1,31744
v_cvt_pk_bf16_f32 v122,v26,v27
M 18,a[216:219],78
s_barrier
ds_read_b32 v7,v6 offset:0
R 98,13,0
M 22,a[212:215],74
s_sub_u32 s57,s84,s57
R 34,2,0
v_fma_f32 v118,v104,v118,v103
M 22,a[220:223],78
L 5
buffer_store_dword v4,v3,s[36:39],s70 offen offset:0 sc0 sc1
M 22,a[228:231],82
s_mov_b32 s70,s69
R 38,2,1024
v_fma_f32 v119,v104,v119,v103
M 22,a[236:239],86
D 0,56,1024
M 18,a[224:227],82
s_mov_b32 s69,s68
R 42,2,2048
v_fma_f32 v120,v104,v120,v103
M 18,a[232:235],86
L 5
v_fma_f32 v121,v104,v121,v103
v_mul_f32 v118,v30,v118
v_mul_f32 v119,v31,v119
v_mul_f32 v120,v32,v120
v_mul_f32 v121,v33,v121
v_exp_f32 v118,v118
v_exp_f32 v119,v119
v_exp_f32 v120,v120
v_exp_f32 v121,v121
M 18,a[240:243],90
L 4
R 46,2,3072
v_rcp_f32 v116,v116
v_rcp_f32 v117,v117
v_fma_f32 v118,v118,v105,v105
v_fma_f32 v119,v119,v105,v105
v_fma_f32 v120,v120,v105,v105
v_fma_f32 v121,v121,v105,v105
v_rcp_f32 v118,v118
v_rcp_f32 v119,v119
v_rcp_f32 v120,v120
v_rcp_f32 v121,v121
v_cmp_eq_u32 vcc,s53,v7
M 18,a[248:251],94
s_add_u32 s68,s68,s63
v_mul_f32 v28,v28,v116
v_mul_f32 v29,v29,v117
v_cvt_pk_bf16_f32 v123,v28,v29
v_mul_f32 v30,v30,v118
v_mul_f32 v31,v31,v119
v_mul_f32 v32,v32,v120
v_mul_f32 v33,v33,v121
v_cvt_pk_bf16_f32 v124,v30,v31
v_cvt_pk_bf16_f32 v125,v32,v33
M 22,a[244:247],90
L 2
s_cmp_eq_u32 vcc_lo,-1
R 50,2,4096
buffer_store_dwordx4 v[122:125],v0,s[36:39],s67 offen offset:0 sc0 sc1
M 22,a[252:255],94
D 0,56,2048
D 0,56,3072
buffer_store_dword v4,v3,s[36:39],s71 offen offset:0 sc0 sc1
buffer_store_dword v4,v3,s[36:39],s71 offen offset:0 sc0 sc1
s_cbranch_scc1 Lg3o_F1_%=
s_branch Lg3o_K1_%=
Lg3o_X0_%=:
M 22,a[124:127],94
L 3
s_add_u32 m0,s59,0
v_fma_f32 v32,-v102,v112,v32
M 22,a[132:135],34
R 54,1,21504
v_fma_f32 v33,-v102,v113,v33
M 22,a[140:143],38
buffer_load_dword v8,s[36:39],s58 offen offset:0 sc1 lds
M 18,a[128:131],34
R 58,1,22528
v_mul_f32 v118,v30,v30
M 18,a[136:139],38
L 3
v_exp_f32 v116,v116
M 18,a[144:147],42
R 62,1,23552
v_fma_f32 v116,v116,v105,v105
M 18,a[152:155],46
v_exp_f32 v117,v117
M 22,a[148:151],42
R 66,1,24576
v_fma_f32 v117,v117,v105,v105
M 22,a[156:159],46
L 3
v_rcp_f32 v114,v114
v_mul_f32 v119,v31,v31
M 22,a[164:167],50
R 70,1,25600
v_mul_f32 v26,v26,v114
M 22,a[172:175],54
v_rcp_f32 v115,v115
M 18,a[160:163],50
R 74,1,26624
v_mul_f32 v27,v27,v115
M 18,a[168:171],54
L 3
v_rcp_f32 v116,v116
M 18,a[176:179],58
R 78,1,27648
v_mul_f32 v28,v28,v116
M 18,a[184:187],62
v_rcp_f32 v117,v117
M 22,a[180:183],58
R 82,1,28672
v_mul_f32 v29,v29,v117
M 22,a[188:191],62
L 3
v_cvt_pk_bf16_f32 v122,v26,v27
v_cvt_pk_bf16_f32 v123,v28,v29
M 22,a[196:199],66
R 86,1,29696
v_mul_f32 v120,v32,v32
M 22,a[204:207],70
v_mul_f32 v121,v33,v33
v_fma_f32 v118,v104,v118,v103
M 18,a[192:195],66
R 90,1,30720
v_fma_f32 v119,v104,v119,v103
M 18,a[200:203],70
L 3
s_nop 0
v_fma_f32 v120,v104,v120,v103
M 18,a[208:211],74
s_waitcnt vmcnt(0)
R 94,1,31744
v_fma_f32 v121,v104,v121,v103
M 18,a[216:219],78
s_barrier
buffer_store_dword v4,v3,s[36:39],s70 offen offset:0 sc0 sc1
M 22,a[212:215],74
s_mov_b32 s70,s69
ds_read_b32 v7,v6 offset:0
R 98,13,0
M 22,a[220:223],78
L 4
v_mul_f32 v118,v30,v118
v_mul_f32 v119,v31,v119
M 22,a[228:231],82
s_mov_b32 s69,s68
v_mul_f32 v120,v32,v120
v_mul_f32 v121,v33,v121
M 22,a[236:239],86
s_add_u32 s68,s68,s63
v_exp_f32 v118,v118
M 18,a[224:227],82
v_exp_f32 v119,v119
M 18,a[232:235],86
L 2
v_exp_f32 v120,v120
v_exp_f32 v121,v121
M 18,a[240:243],90
L 1
v_fma_f32 v118,v118,v105,v105
v_fma_f32 v119,v119,v105,v105
v_fma_f32 v120,v120,v105,v105
v_fma_f32 v121,v121,v105,v105
v_rcp_f32 v118,v118
)"
R"(v_rcp_f32 v119,v119
v_rcp_f32 v120,v120
v_rcp_f32 v121,v121
M 18,a[248:251],94
v_mul_f32 v30,v30,v118
v_mul_f32 v31,v31,v119
v_mul_f32 v32,v32,v120
v_mul_f32 v33,v33,v121
v_cvt_pk_bf16_f32 v124,v30,v31
v_cvt_pk_bf16_f32 v125,v32,v33
M 22,a[244:247],90
buffer_store_dwordx4 v[122:125],v0,s[36:39],s67 offen offset:0 sc0 sc1
v_cmp_eq_u32 vcc,s53,v7
M 22,a[252:255],94
s_mov_b32 s80,1
s_cmp_eq_u32 vcc_lo,-1
s_cbranch_scc0 Lg3o_xn0_%=
buffer_load_dword v130,v11,s[36:39],s73 offen offset:0 sc1
L 0
v_fma_f32 v18,-v98,v106,v18
v_fma_f32 v19,-v98,v107,v19
v_fma_f32 v20,-v98,v108,v20
v_fma_f32 v21,-v98,v109,v21
v_mul_f32 v114,v18,v18
v_mul_f32 v115,v19,v19
s_add_u32 s73,s73,s64
s_add_u32 m0,s57,0
s_add_u32 s56,s55,0
D 0,56,0
v_mul_f32 v116,v20,v20
v_mul_f32 v117,v21,v21
v_fma_f32 v114,v100,v114,v99
v_fma_f32 v115,v100,v115,v99
v_fma_f32 v116,v100,v116,v99
v_fma_f32 v117,v100,v117,v99
D 0,56,1024
v_mul_f32 v114,v18,v114
v_mul_f32 v115,v19,v115
v_mul_f32 v116,v20,v116
v_mul_f32 v117,v21,v117
v_exp_f32 v114,v114
v_exp_f32 v115,v115
D 0,56,2048
v_exp_f32 v116,v116
v_exp_f32 v117,v117
v_fma_f32 v114,v114,v101,v101
v_fma_f32 v115,v115,v101,v101
v_fma_f32 v116,v116,v101,v101
v_fma_f32 v117,v117,v101,v101
D 0,56,3072
v_rcp_f32 v114,v114
v_rcp_f32 v115,v115
v_rcp_f32 v116,v116
v_rcp_f32 v117,v117
v_mul_f32 v18,v18,v114
v_mul_f32 v19,v19,v115
s_add_u32 s58,s58,s61
s_add_u32 m0,s57,16384
s_add_u32 s56,s55,16384
D 0,56,0
v_mul_f32 v20,v20,v116
v_mul_f32 v21,v21,v117
v_cvt_pk_bf16_f32 v122,v18,v19
v_cvt_pk_bf16_f32 v123,v20,v21
v_fma_f32 v22,-v98,v110,v22
v_fma_f32 v23,-v98,v111,v23
D 0,56,1024
v_fma_f32 v24,-v98,v112,v24
v_fma_f32 v25,-v98,v113,v25
v_mul_f32 v118,v22,v22
v_mul_f32 v119,v23,v23
v_mul_f32 v120,v24,v24
v_mul_f32 v121,v25,v25
D 0,56,2048
v_fma_f32 v118,v100,v118,v99
v_fma_f32 v119,v100,v119,v99
v_fma_f32 v120,v100,v120,v99
v_fma_f32 v121,v100,v121,v99
v_mul_f32 v118,v22,v118
v_mul_f32 v119,v23,v119
D 0,56,3072
v_mul_f32 v120,v24,v120
v_mul_f32 v121,v25,v121
v_exp_f32 v118,v118
v_exp_f32 v119,v119
v_exp_f32 v120,v120
v_exp_f32 v121,v121
s_add_u32 s55,s55,s60
s_sub_u32 s57,s84,s57
s_add_u32 s54,s54,s46
v_fma_f32 v118,v118,v101,v101
v_fma_f32 v119,v119,v101,v101
v_fma_f32 v120,v120,v101,v101
v_fma_f32 v121,v121,v101,v101
v_rcp_f32 v118,v118
v_rcp_f32 v119,v119
v_rcp_f32 v120,v120
v_rcp_f32 v121,v121
v_mul_f32 v22,v22,v118
v_mul_f32 v23,v23,v119
v_mul_f32 v24,v24,v120
v_mul_f32 v25,v25,v121
v_cvt_pk_bf16_f32 v124,v22,v23
v_cvt_pk_bf16_f32 v125,v24,v25
buffer_store_dwordx4 v[122:125],v0,s[36:39],s66 offen offset:0 sc0 sc1
s_mov_b32 s72,s69
s_mov_b32 s69,0x7fff0000
s_mov_b32 s66,0x7fff0000
s_mov_b32 s76,0x7fff0000
s_branch Lg3o_gox_%=
Lg3o_xn0_%=:
s_cmp_ge_u32 s54,256
s_cbranch_scc1 Lg3o_fin0_%=
s_add_u32 m0,s59,4096
s_nop 0
buffer_load_dword v8,s[36:39],s58 offen offset:0 sc1 lds
buffer_store_dword v4,v3,s[36:39],s71 offen offset:0 sc0 sc1
s_add_u32 m0,s59,5120
s_nop 0
buffer_load_dword v8,s[36:39],s58 offen offset:0 sc1 lds
buffer_store_dword v4,v3,s[36:39],s71 offen offset:0 sc0 sc1
s_mov_b32 s83,0
L 0
v_fma_f32 v18,-v98,v106,v18
v_fma_f32 v19,-v98,v107,v19
v_fma_f32 v20,-v98,v108,v20
v_fma_f32 v21,-v98,v109,v21
v_mul_f32 v114,v18,v18
v_mul_f32 v115,v19,v19
v_mul_f32 v116,v20,v20
v_mul_f32 v117,v21,v21
v_fma_f32 v114,v100,v114,v99
v_fma_f32 v115,v100,v115,v99
v_fma_f32 v116,v100,v116,v99
v_fma_f32 v117,v100,v117,v99
v_mul_f32 v114,v18,v114
v_mul_f32 v115,v19,v115
v_mul_f32 v116,v20,v116
v_mul_f32 v117,v21,v117
v_exp_f32 v114,v114
v_exp_f32 v115,v115
v_exp_f32 v116,v116
v_exp_f32 v117,v117
v_fma_f32 v114,v114,v101,v101
v_fma_f32 v115,v115,v101,v101
v_fma_f32 v116,v116,v101,v101
v_fma_f32 v117,v117,v101,v101
v_rcp_f32 v114,v114
v_rcp_f32 v115,v115
v_rcp_f32 v116,v116
v_rcp_f32 v117,v117
v_mul_f32 v18,v18,v114
v_mul_f32 v19,v19,v115
v_mul_f32 v20,v20,v116
v_mul_f32 v21,v21,v117
v_cvt_pk_bf16_f32 v122,v18,v19
v_cvt_pk_bf16_f32 v123,v20,v21
v_fma_f32 v22,-v98,v110,v22
v_fma_f32 v23,-v98,v111,v23
v_fma_f32 v24,-v98,v112,v24
v_fma_f32 v25,-v98,v113,v25
v_mul_f32 v118,v22,v22
v_mul_f32 v119,v23,v23
v_mul_f32 v120,v24,v24
v_mul_f32 v121,v25,v25
v_fma_f32 v118,v100,v118,v99
v_fma_f32 v119,v100,v119,v99
v_fma_f32 v120,v100,v120,v99
v_fma_f32 v121,v100,v121,v99
v_mul_f32 v118,v22,v118
v_mul_f32 v119,v23,v119
v_mul_f32 v120,v24,v120
v_mul_f32 v121,v25,v121
v_exp_f32 v118,v118
v_exp_f32 v119,v119
v_exp_f32 v120,v120
v_exp_f32 v121,v121
v_fma_f32 v118,v118,v101,v101
v_fma_f32 v119,v119,v101,v101
v_fma_f32 v120,v120,v101,v101
v_fma_f32 v121,v121,v101,v101
v_rcp_f32 v118,v118
v_rcp_f32 v119,v119
v_rcp_f32 v120,v120
v_rcp_f32 v121,v121
v_mul_f32 v22,v22,v118
v_mul_f32 v23,v23,v119
v_mul_f32 v24,v24,v120
v_mul_f32 v25,v25,v121
v_cvt_pk_bf16_f32 v124,v22,v23
v_cvt_pk_bf16_f32 v125,v24,v25
buffer_store_dwordx4 v[122:125],v0,s[36:39],s66 offen offset:0 sc0 sc1
s_mov_b32 s72,s69
s_mov_b32 s69,0x7fff0000
s_mov_b32 s66,0x7fff0000
s_mov_b32 s76,0x7fff0000
s_branch Lg3o_poll_%=
Lg3o_fin0_%=:
L 0
v_fma_f32 v18,-v98,v106,v18
v_fma_f32 v19,-v98,v107,v19
v_fma_f32 v20,-v98,v108,v20
v_fma_f32 v21,-v98,v109,v21
v_mul_f32 v114,v18,v18
v_mul_f32 v115,v19,v19
v_mul_f32 v116,v20,v20
v_mul_f32 v117,v21,v21
v_fma_f32 v114,v100,v114,v99
v_fma_f32 v115,v100,v115,v99
v_fma_f32 v116,v100,v116,v99
v_fma_f32 v117,v100,v117,v99
v_mul_f32 v114,v18,v114
v_mul_f32 v115,v19,v115
v_mul_f32 v116,v20,v116
v_mul_f32 v117,v21,v117
v_exp_f32 v114,v114
v_exp_f32 v115,v115
v_exp_f32 v116,v116
v_exp_f32 v117,v117
v_fma_f32 v114,v114,v101,v101
v_fma_f32 v115,v115,v101,v101
v_fma_f32 v116,v116,v101,v101
v_fma_f32 v117,v117,v101,v101
v_rcp_f32 v114,v114
v_rcp_f32 v115,v115
v_rcp_f32 v116,v116
v_rcp_f32 v117,v117
v_mul_f32 v18,v18,v114
v_mul_f32 v19,v19,v115
v_mul_f32 v20,v20,v116
v_mul_f32 v21,v21,v117
v_cvt_pk_bf16_f32 v122,v18,v19
v_cvt_pk_bf16_f32 v123,v20,v21
v_fma_f32 v22,-v98,v110,v22
v_fma_f32 v23,-v98,v111,v23
v_fma_f32 v24,-v98,v112,v24
v_fma_f32 v25,-v98,v113,v25
v_mul_f32 v118,v22,v22
v_mul_f32 v119,v23,v23
v_mul_f32 v120,v24,v24
v_mul_f32 v121,v25,v25
v_fma_f32 v118,v100,v118,v99
v_fma_f32 v119,v100,v119,v99
v_fma_f32 v120,v100,v120,v99
v_fma_f32 v121,v100,v121,v99
v_mul_f32 v118,v22,v118
v_mul_f32 v119,v23,v119
v_mul_f32 v120,v24,v120
v_mul_f32 v121,v25,v121
v_exp_f32 v118,v118
v_exp_f32 v119,v119
v_exp_f32 v120,v120
v_exp_f32 v121,v121
v_fma_f32 v118,v118,v101,v101
v_fma_f32 v119,v119,v101,v101
v_fma_f32 v120,v120,v101,v101
v_fma_f32 v121,v121,v101,v101
v_rcp_f32 v118,v118
v_rcp_f32 v119,v119
v_rcp_f32 v120,v120
v_rcp_f32 v121,v121
v_mul_f32 v22,v22,v118
v_mul_f32 v23,v23,v119
v_mul_f32 v24,v24,v120
v_mul_f32 v25,v25,v121
v_cvt_pk_bf16_f32 v124,v22,v23
v_cvt_pk_bf16_f32 v125,v24,v25
buffer_store_dwordx4 v[122:125],v0,s[36:39],s66 offen offset:0 sc0 sc1
s_mov_b32 s72,s69
s_mov_b32 s69,0x7fff0000
s_mov_b32 s66,0x7fff0000
s_mov_b32 s76,0x7fff0000
s_branch Lg3o_done_%=
Lg3o_K1_%=:
Lg3o_L1_%=:
v_mfma_f32_16x16x32_bf16 v[30:33],a[4:7],v[34:37],0
s_add_u32 m0,s59,3072
R 54,2,5120
v_add_f32_dpp v131,v130,v130 quad_perm:[1,0,3,2] row_mask:0xf bank_mask:0xf
M 30,a[12:15],38
buffer_load_dword v8,s[36:39],s58 offen offset:0 sc1 lds
v_mfma_f32_16x16x32_bf16 v[26:29],a[0:3],v[34:37],0
s_mov_b32 s67,s65
R 58,2,6144
v_add_f32_dpp v131,v131,v131 quad_perm:[2,3,0,1] row_mask:0xf bank_mask:0xf
M 26,a[8:11],38
L 3
v_fma_f32 v18,-v98,v106,v18
v_fma_f32 v19,-v98,v107,v19
M 26,a[16:19],42
s_add_u32 s65,s65,s62
R 62,2,7168
v_add_f32_dpp v134,v131,v131 row_half_mirror row_mask:0xf bank_mask:0xf
M 26,a[24:27],46
v_fma_f32 v20,-v98,v108,v20
v_fma_f32 v21,-v98,v109,v21
M 30,a[20:23],42
R 66,2,8192
v_mov_b32_dpp v131,v134 row_ror:8 row_mask:0xf bank_mask:0xf
M 30,a[28:31],46
L 3
v_fma_f32 v131,-v134,v134,v131
v_max_f32 v131,s87,v131
M 30,a[36:39],50
R 70,2,9216
v_mul_f32 v134,v17,v134
M 30,a[44:47],54
v_rsq_f32 v132,v131
M 26,a[32:35],50
R 74,2,10240
v_fma_f32 v131,v15,v131,v16
M 26,a[40:43],54
L 3
v_mul_f32 v135,v132,v132
v_fma_f32 v131,v14,v135,v131
M 26,a[48:51],58
R 78,2,11264
v_fma_f32 v134,v132,v131,v134
M 26,a[56:59],62
ds_write_b32 v12,v134 offset:2048
M 30,a[52:55],58
R 82,2,12288
v_mul_f32 v114,v18,v18
M 30,a[60:63],62
L 4
v_mul_f32 v115,v19,v19
v_mul_f32 v116,v20,v20
M 30,a[68:71],66
R 86,2,13312
v_mul_f32 v117,v21,v21
M 30,a[76:79],70
v_fma_f32 v114,v100,v114,v99
v_fma_f32 v115,v100,v115,v99
M 26,a[64:67],66
R 90,2,14336
v_fma_f32 v116,v100,v116,v99
M 26,a[72:75],70
L 4
v_fma_f32 v117,v100,v117,v99
v_mul_f32 v114,v18,v114
M 26,a[80:83],74
s_waitcnt vmcnt(0)
R 94,2,15360
v_mul_f32 v115,v19,v115
M 26,a[88:91],78
s_barrier
ds_read_b32 v7,v6 offset:3072
v_mul_f32 v116,v20,v116
M 30,a[84:87],74
R 34,2,16384
v_mul_f32 v117,v21,v117
M 30,a[92:95],78
L 4
buffer_store_dword v4,v3,s[36:39],s70 offen offset:0 sc0 sc1
M 30,a[100:103],82
s_mov_b32 s70,0x7fff0000
R 38,2,17408
v_fma_f32 v22,-v98,v110,v22
M 30,a[108:111],86
v_exp_f32 v114,v114
M 26,a[96:99],82
R 42,2,18432
v_fma_f32 v114,v114,v101,v101
M 26,a[104:107],86
L 4
L 3
v_cmp_eq_u32 vcc,s53,v7
M 26,a[112:115],90
R 46,2,19456
v_fma_f32 v23,-v98,v111,v23
M 26,a[120:123],94
v_exp_f32 v115,v115
M 30,a[116:119],90
s_cmp_eq_u32 vcc_lo,-1
R 50,2,20480
v_fma_f32 v115,v115,v101,v101
s_cbranch_scc0 Lg3o_X1_%=
M 30,a[124:127],94
L 3
s_add_u32 m0,s57,0
s_add_u32 s56,s55,0
buffer_load_dword v130,v11,s[36:39],s73 offen offset:0 sc1
M 30,a[132:135],34
s_add_u32 s58,s58,s61
R 54,2,21504
v_fma_f32 v24,-v98,v112,v24
M 30,a[140:143],38
D 0,56,0
M 26,a[128:131],34
s_add_u32 s54,s54,s46
R 58,2,22528
v_fma_f32 v25,-v98,v113,v25
M 26,a[136:139],38
L 3
v_exp_f32 v116,v116
M 26,a[144:147],42
s_add_u32 s73,s73,s64
R 62,2,23552
v_fma_f32 v116,v116,v101,v101
M 26,a[152:155],46
D 0,56,1024
M 30,a[148:151],42
R 66,2,24576
v_mul_f32 v118,v22,v22
M 30,a[156:159],46
L 3
v_exp_f32 v117,v117
M 30,a[164:167],50
R 70,2,25600
v_fma_f32 v117,v117,v101,v101
M 30,a[172:175],54
D 0,56,2048
M 26,a[160:163],50
R 74,2,26624
v_mul_f32 v119,v23,v23
M 26,a[168:171],54
L 3
v_rcp_f32 v114,v114
M 26,a[176:179],58
R 78,2,27648
v_mul_f32 v18,v18,v114
M 26,a[184:187],62
D 0,56,3072
M 30,a[180:183],58
s_add_u32 m0,s59,1024
R 82,2,28672
v_mul_f32 v120,v24,v24
M 30,a[188:191],62
L 3
buffer_load_dword v8,s[36:39],s58 offen offset:0 sc1 lds
M 30,a[196:199],66
s_add_u32 m0,s57,16384
R 86,2,29696
v_mul_f32 v121,v25,v25
M 30,a[204:207],70
s_add_u32 s56,s55,16384
v_rcp_f32 v115,v115
M 26,a[192:195],66
s_add_u32 s55,s55,s60
R 90,2,30720
v_mul_f32 v19,v19,v115
M 26,a[200:203],70
L 3
s_nop 0
D 0,56,0
M 26,a[208:211],74
s_waitcnt vmcnt(1)
R 94,2,31744
v_cvt_pk_bf16_f32 v122,v18,v19
M 26,a[216:219],78
s_barrier
ds_read_b32 v7,v6 offset:1024
R 102,13,2048
M 30,a[212:215],74
s_sub_u32 s57,s84,s57
R 34,1,0
v_fma_f32 v118,v100,v118,v99
M 30,a[220:223],78
L 5
buffer_store_dword v4,v3,s[36:39],s70 offen offset:0 sc0 sc1
M 30,a[228:231],82
s_mov_b32 s70,s69
R 38,1,1024
v_fma_f32 v119,v100,v119,v99
M 30,a[236:239],86
D 0,56,1024
M 26,a[224:227],82
s_mov_b32 s69,s68
R 42,1,2048
v_fma_f32 v120,v100,v120,v99
M 26,a[232:235],86
L 5
v_fma_f32 v121,v100,v121,v99
v_mul_f32 v118,v22,v118
v_mul_f32 v119,v23,v119
v_mul_f32 v120,v24,v120
v_mul_f32 v121,v25,v121
v_exp_f32 v118,v118
v_exp_f32 v119,v119
v_exp_f32 v120,v120
v_exp_f32 v121,v121
M 26,a[240:243],90
L 4
R 46,1,3072
v_rcp_f32 v116,v116
v_rcp_f32 v117,v117
v_fma_f32 v118,v118,v101,v101
v_fma_f32 v119,v119,v101,v101
v_fma_f32 v120,v120,v101,v101
v_fma_f32 v121,v121,v101,v101
v_rcp_f32 v118,v118
v_rcp_f32 v119,v119
v_rcp_f32 v120,v120
v_rcp_f32 v121,v121
v_cmp_eq_u32 vcc,s53,v7
M 26,a[248:251],94
s_add_u32 s68,s68,s63
v_mul_f32 v20,v20,v116
v_mul_f32 v21,v21,v117
v_cvt_pk_bf16_f32 v123,v20,v21
)"
R"(v_mul_f32 v22,v22,v118
v_mul_f32 v23,v23,v119
v_mul_f32 v24,v24,v120
v_mul_f32 v25,v25,v121
v_cvt_pk_bf16_f32 v124,v22,v23
v_cvt_pk_bf16_f32 v125,v24,v25
M 30,a[244:247],90
L 2
s_cmp_eq_u32 vcc_lo,-1
R 50,1,4096
buffer_store_dwordx4 v[122:125],v0,s[36:39],s66 offen offset:0 sc0 sc1
M 30,a[252:255],94
D 0,56,2048
D 0,56,3072
buffer_store_dword v4,v3,s[36:39],s71 offen offset:0 sc0 sc1
buffer_store_dword v4,v3,s[36:39],s71 offen offset:0 sc0 sc1
s_cbranch_scc1 Lg3o_F0_%=
s_branch Lg3o_K0_%=
Lg3o_X1_%=:
M 30,a[124:127],94
L 3
s_add_u32 m0,s59,1024
v_fma_f32 v24,-v98,v112,v24
M 30,a[132:135],34
R 54,2,21504
v_fma_f32 v25,-v98,v113,v25
M 30,a[140:143],38
buffer_load_dword v8,s[36:39],s58 offen offset:0 sc1 lds
M 26,a[128:131],34
R 58,2,22528
v_mul_f32 v118,v22,v22
M 26,a[136:139],38
L 3
v_exp_f32 v116,v116
M 26,a[144:147],42
R 62,2,23552
v_fma_f32 v116,v116,v101,v101
M 26,a[152:155],46
v_exp_f32 v117,v117
M 30,a[148:151],42
R 66,2,24576
v_fma_f32 v117,v117,v101,v101
M 30,a[156:159],46
L 3
v_rcp_f32 v114,v114
v_mul_f32 v119,v23,v23
M 30,a[164:167],50
R 70,2,25600
v_mul_f32 v18,v18,v114
M 30,a[172:175],54
v_rcp_f32 v115,v115
M 26,a[160:163],50
R 74,2,26624
v_mul_f32 v19,v19,v115
M 26,a[168:171],54
L 3
v_rcp_f32 v116,v116
M 26,a[176:179],58
R 78,2,27648
v_mul_f32 v20,v20,v116
M 26,a[184:187],62
v_rcp_f32 v117,v117
M 30,a[180:183],58
R 82,2,28672
v_mul_f32 v21,v21,v117
M 30,a[188:191],62
L 3
v_cvt_pk_bf16_f32 v122,v18,v19
v_cvt_pk_bf16_f32 v123,v20,v21
M 30,a[196:199],66
R 86,2,29696
v_mul_f32 v120,v24,v24
M 30,a[204:207],70
v_mul_f32 v121,v25,v25
v_fma_f32 v118,v100,v118,v99
M 26,a[192:195],66
R 90,2,30720
v_fma_f32 v119,v100,v119,v99
M 26,a[200:203],70
L 3
s_nop 0
v_fma_f32 v120,v100,v120,v99
M 26,a[208:211],74
s_waitcnt vmcnt(0)
R 94,2,31744
v_fma_f32 v121,v100,v121,v99
M 26,a[216:219],78
s_barrier
buffer_store_dword v4,v3,s[36:39],s70 offen offset:0 sc0 sc1
M 30,a[212:215],74
s_mov_b32 s70,s69
ds_read_b32 v7,v6 offset:1024
R 102,13,2048
M 30,a[220:223],78
L 4
v_mul_f32 v118,v22,v118
v_mul_f32 v119,v23,v119
M 30,a[228:231],82
s_mov_b32 s69,s68
v_mul_f32 v120,v24,v120
v_mul_f32 v121,v25,v121
M 30,a[236:239],86
s_add_u32 s68,s68,s63
v_exp_f32 v118,v118
M 26,a[224:227],82
v_exp_f32 v119,v119
M 26,a[232:235],86
L 2
v_exp_f32 v120,v120
v_exp_f32 v121,v121
M 26,a[240:243],90
L 1
v_fma_f32 v118,v118,v101,v101
v_fma_f32 v119,v119,v101,v101
v_fma_f32 v120,v120,v101,v101
v_fma_f32 v121,v121,v101,v101
v_rcp_f32 v118,v118
v_rcp_f32 v119,v119
v_rcp_f32 v120,v120
v_rcp_f32 v121,v121
M 26,a[248:251],94
v_mul_f32 v22,v22,v118
v_mul_f32 v23,v23,v119
v_mul_f32 v24,v24,v120
v_mul_f32 v25,v25,v121
v_cvt_pk_bf16_f32 v124,v22,v23
v_cvt_pk_bf16_f32 v125,v24,v25
M 30,a[244:247],90
buffer_store_dwordx4 v[122:125],v0,s[36:39],s66 offen offset:0 sc0 sc1
v_cmp_eq_u32 vcc,s53,v7
M 30,a[252:255],94
s_mov_b32 s80,0
s_cmp_eq_u32 vcc_lo,-1
s_cbranch_scc0 Lg3o_xn1_%=
buffer_load_dword v130,v11,s[36:39],s73 offen offset:0 sc1
L 0
v_fma_f32 v26,-v102,v106,v26
v_fma_f32 v27,-v102,v107,v27
v_fma_f32 v28,-v102,v108,v28
v_fma_f32 v29,-v102,v109,v29
v_mul_f32 v114,v26,v26
v_mul_f32 v115,v27,v27
s_add_u32 s73,s73,s64
s_add_u32 m0,s57,0
s_add_u32 s56,s55,0
D 0,56,0
v_mul_f32 v116,v28,v28
v_mul_f32 v117,v29,v29
v_fma_f32 v114,v104,v114,v103
v_fma_f32 v115,v104,v115,v103
v_fma_f32 v116,v104,v116,v103
v_fma_f32 v117,v104,v117,v103
D 0,56,1024
v_mul_f32 v114,v26,v114
v_mul_f32 v115,v27,v115
v_mul_f32 v116,v28,v116
v_mul_f32 v117,v29,v117
v_exp_f32 v114,v114
v_exp_f32 v115,v115
D 0,56,2048
v_exp_f32 v116,v116
v_exp_f32 v117,v117
v_fma_f32 v114,v114,v105,v105
v_fma_f32 v115,v115,v105,v105
v_fma_f32 v116,v116,v105,v105
v_fma_f32 v117,v117,v105,v105
D 0,56,3072
v_rcp_f32 v114,v114
v_rcp_f32 v115,v115
v_rcp_f32 v116,v116
v_rcp_f32 v117,v117
v_mul_f32 v26,v26,v114
v_mul_f32 v27,v27,v115
s_add_u32 s58,s58,s61
s_add_u32 m0,s57,16384
s_add_u32 s56,s55,16384
D 0,56,0
v_mul_f32 v28,v28,v116
v_mul_f32 v29,v29,v117
v_cvt_pk_bf16_f32 v122,v26,v27
v_cvt_pk_bf16_f32 v123,v28,v29
v_fma_f32 v30,-v102,v110,v30
v_fma_f32 v31,-v102,v111,v31
D 0,56,1024
v_fma_f32 v32,-v102,v112,v32
v_fma_f32 v33,-v102,v113,v33
v_mul_f32 v118,v30,v30
v_mul_f32 v119,v31,v31
v_mul_f32 v120,v32,v32
v_mul_f32 v121,v33,v33
D 0,56,2048
v_fma_f32 v118,v104,v118,v103
v_fma_f32 v119,v104,v119,v103
v_fma_f32 v120,v104,v120,v103
v_fma_f32 v121,v104,v121,v103
v_mul_f32 v118,v30,v118
v_mul_f32 v119,v31,v119
D 0,56,3072
v_mul_f32 v120,v32,v120
v_mul_f32 v121,v33,v121
v_exp_f32 v118,v118
v_exp_f32 v119,v119
v_exp_f32 v120,v120
v_exp_f32 v121,v121
s_add_u32 s55,s55,s60
s_sub_u32 s57,s84,s57
s_add_u32 s54,s54,s46
v_fma_f32 v118,v118,v105,v105
v_fma_f32 v119,v119,v105,v105
v_fma_f32 v120,v120,v105,v105
v_fma_f32 v121,v121,v105,v105
v_rcp_f32 v118,v118
v_rcp_f32 v119,v119
v_rcp_f32 v120,v120
v_rcp_f32 v121,v121
v_mul_f32 v30,v30,v118
v_mul_f32 v31,v31,v119
v_mul_f32 v32,v32,v120
v_mul_f32 v33,v33,v121
v_cvt_pk_bf16_f32 v124,v30,v31
v_cvt_pk_bf16_f32 v125,v32,v33
buffer_store_dwordx4 v[122:125],v0,s[36:39],s67 offen offset:0 sc0 sc1
s_mov_b32 s72,s69
s_mov_b32 s69,0x7fff0000
s_mov_b32 s67,0x7fff0000
s_mov_b32 s77,0x7fff0000
s_branch Lg3o_gox_%=
Lg3o_xn1_%=:
s_cmp_ge_u32 s54,256
s_cbranch_scc1 Lg3o_fin1_%=
s_add_u32 m0,s59,4096
s_nop 0
buffer_load_dword v8,s[36:39],s58 offen offset:0 sc1 lds
buffer_store_dword v4,v3,s[36:39],s71 offen offset:0 sc0 sc1
s_add_u32 m0,s59,5120
s_nop 0
buffer_load_dword v8,s[36:39],s58 offen offset:0 sc1 lds
buffer_store_dword v4,v3,s[36:39],s71 offen offset:0 sc0 sc1
s_mov_b32 s83,0
L 0
v_fma_f32 v26,-v102,v106,v26
v_fma_f32 v27,-v102,v107,v27
v_fma_f32 v28,-v102,v108,v28
v_fma_f32 v29,-v102,v109,v29
v_mul_f32 v114,v26,v26
v_mul_f32 v115,v27,v27
v_mul_f32 v116,v28,v28
v_mul_f32 v117,v29,v29
v_fma_f32 v114,v104,v114,v103
v_fma_f32 v115,v104,v115,v103
v_fma_f32 v116,v104,v116,v103
v_fma_f32 v117,v104,v117,v103
v_mul_f32 v114,v26,v114
v_mul_f32 v115,v27,v115
v_mul_f32 v116,v28,v116
v_mul_f32 v117,v29,v117
v_exp_f32 v114,v114
v_exp_f32 v115,v115
v_exp_f32 v116,v116
v_exp_f32 v117,v117
v_fma_f32 v114,v114,v105,v105
v_fma_f32 v115,v115,v105,v105
v_fma_f32 v116,v116,v105,v105
v_fma_f32 v117,v117,v105,v105
v_rcp_f32 v114,v114
v_rcp_f32 v115,v115
v_rcp_f32 v116,v116
v_rcp_f32 v117,v117
v_mul_f32 v26,v26,v114
v_mul_f32 v27,v27,v115
v_mul_f32 v28,v28,v116
v_mul_f32 v29,v29,v117
v_cvt_pk_bf16_f32 v122,v26,v27
v_cvt_pk_bf16_f32 v123,v28,v29
v_fma_f32 v30,-v102,v110,v30
v_fma_f32 v31,-v102,v111,v31
v_fma_f32 v32,-v102,v112,v32
v_fma_f32 v33,-v102,v113,v33
v_mul_f32 v118,v30,v30
v_mul_f32 v119,v31,v31
v_mul_f32 v120,v32,v32
v_mul_f32 v121,v33,v33
v_fma_f32 v118,v104,v118,v103
v_fma_f32 v119,v104,v119,v103
v_fma_f32 v120,v104,v120,v103
v_fma_f32 v121,v104,v121,v103
v_mul_f32 v118,v30,v118
v_mul_f32 v119,v31,v119
v_mul_f32 v120,v32,v120
v_mul_f32 v121,v33,v121
v_exp_f32 v118,v118
v_exp_f32 v119,v119
v_exp_f32 v120,v120
v_exp_f32 v121,v121
v_fma_f32 v118,v118,v105,v105
v_fma_f32 v119,v119,v105,v105
v_fma_f32 v120,v120,v105,v105
v_fma_f32 v121,v121,v105,v105
v_rcp_f32 v118,v118
v_rcp_f32 v119,v119
v_rcp_f32 v120,v120
v_rcp_f32 v121,v121
v_mul_f32 v30,v30,v118
v_mul_f32 v31,v31,v119
v_mul_f32 v32,v32,v120
v_mul_f32 v33,v33,v121
v_cvt_pk_bf16_f32 v124,v30,v31
v_cvt_pk_bf16_f32 v125,v32,v33
buffer_store_dwordx4 v[122:125],v0,s[36:39],s67 offen offset:0 sc0 sc1
s_mov_b32 s72,s69
s_mov_b32 s69,0x7fff0000
s_mov_b32 s67,0x7fff0000
s_mov_b32 s77,0x7fff0000
s_branch Lg3o_poll_%=
Lg3o_fin1_%=:
L 0
v_fma_f32 v26,-v102,v106,v26
v_fma_f32 v27,-v102,v107,v27
v_fma_f32 v28,-v102,v108,v28
v_fma_f32 v29,-v102,v109,v29
v_mul_f32 v114,v26,v26
v_mul_f32 v115,v27,v27
v_mul_f32 v116,v28,v28
v_mul_f32 v117,v29,v29
v_fma_f32 v114,v104,v114,v103
v_fma_f32 v115,v104,v115,v103
v_fma_f32 v116,v104,v116,v103
v_fma_f32 v117,v104,v117,v103
v_mul_f32 v114,v26,v114
v_mul_f32 v115,v27,v115
v_mul_f32 v116,v28,v116
v_mul_f32 v117,v29,v117
v_exp_f32 v114,v114
v_exp_f32 v115,v115
v_exp_f32 v116,v116
v_exp_f32 v117,v117
v_fma_f32 v114,v114,v105,v105
v_fma_f32 v115,v115,v105,v105
v_fma_f32 v116,v116,v105,v105
v_fma_f32 v117,v117,v105,v105
v_rcp_f32 v114,v114
v_rcp_f32 v115,v115
v_rcp_f32 v116,v116
v_rcp_f32 v117,v117
v_mul_f32 v26,v26,v114
v_mul_f32 v27,v27,v115
v_mul_f32 v28,v28,v116
v_mul_f32 v29,v29,v117
v_cvt_pk_bf16_f32 v122,v26,v27
v_cvt_pk_bf16_f32 v123,v28,v29
v_fma_f32 v30,-v102,v110,v30
v_fma_f32 v31,-v102,v111,v31
v_fma_f32 v32,-v102,v112,v32
v_fma_f32 v33,-v102,v113,v33
v_mul_f32 v118,v30,v30
v_mul_f32 v119,v31,v31
v_mul_f32 v120,v32,v32
v_mul_f32 v121,v33,v33
v_fma_f32 v118,v104,v118,v103
v_fma_f32 v119,v104,v119,v103
v_fma_f32 v120,v104,v120,v103
v_fma_f32 v121,v104,v121,v103
v_mul_f32 v118,v30,v118
v_mul_f32 v119,v31,v119
v_mul_f32 v120,v32,v120
v_mul_f32 v121,v33,v121
v_exp_f32 v118,v118
v_exp_f32 v119,v119
v_exp_f32 v120,v120
v_exp_f32 v121,v121
v_fma_f32 v118,v118,v105,v105
v_fma_f32 v119,v119,v105,v105
v_fma_f32 v120,v120,v105,v105
v_fma_f32 v121,v121,v105,v105
v_rcp_f32 v118,v118
v_rcp_f32 v119,v119
v_rcp_f32 v120,v120
v_rcp_f32 v121,v121
v_mul_f32 v30,v30,v118
v_mul_f32 v31,v31,v119
v_mul_f32 v32,v32,v120
v_mul_f32 v33,v33,v121
v_cvt_pk_bf16_f32 v124,v30,v31
v_cvt_pk_bf16_f32 v125,v32,v33
buffer_store_dwordx4 v[122:125],v0,s[36:39],s67 offen offset:0 sc0 sc1
s_mov_b32 s72,s69
s_mov_b32 s69,0x7fff0000
s_mov_b32 s67,0x7fff0000
s_mov_b32 s77,0x7fff0000
s_branch Lg3o_done_%=
Lg3o_done_%=:
s_waitcnt vmcnt(1)
s_barrier
buffer_store_dword v4,v3,s[36:39],s70 offen offset:0 sc0 sc1
s_waitcnt vmcnt(1)
s_barrier
buffer_store_dword v4,v3,s[36:39],s72 offen offset:0 sc0 sc1
s_waitcnt vmcnt(0) lgkmcnt(0)
s_barrier
s_endpgm
Lcode_g3o_e:
.purgem M
.purgem R
.purgem T
.purgem D
.purgem L
)"
  :: "s"(ws), "s"(__builtin_amdgcn_readfirstlane(sWave)), "s"(__builtin_amdgcn_readfirstlane(sG0)), "s"(__builtin_amdgcn_readfirstlane(sStep)), "s"(__builtin_amdgcn_readfirstlane(sOutOff)), "s"(__builtin_amdgcn_readfirstlane(sFoutOff)), "s"(__builtin_amdgcn_readfirstlane(sAuxOff)), "s"(__builtin_amdgcn_readfirstlane(sWw)), "s"(__builtin_amdgcn_readfirstlane(sBuf)), "s"(__builtin_amdgcn_readfirstlane(sExp)), "v"(vL16), "v"(vFlag), "v"(vSnapOff), "v"(vSnapRd), "v"(vWd0), "v"(vWd1), "v"(vWr0), "v"(vWr1), "v"(vWr2), "v"(vOutS), "v"(vAux0), "v"(vAux1), "v"(vAux2), "v"(vAux3), "v"(vAn), "v"(vAe), "s"(w3), "s"(out) : "v255", "v0", "v1", "v2", "v3", "v4", "v5", "v6", "v7", "v8", "v9", "v10", "v11", "v12", "v13", "v14", "v15", "v16", "v17", "v18", "v19", "v20", "v21", "v22", "v23", "v24", "v25", "v26", "v27", "v28", "v29", "v30", "v31", "v32", "v33", "v34", "v35", "v36", "v37", "v38", "v39", "v40", "v41", "v42", "v43", "v44", "v45", "v46", "v47", "v48", "v49", "v50", "v51", "v52", "v53", "v54", "v55", "v56", "v57", "v58", "v59", "v60", "v61", "v62", "v63", "v64", "v65", "v66", "v67", "v68", "v69", "v70", "v71", "v72", "v73", "v74", "v75", "v76", "v77", "v78", "v79", "v80", "v81", "v82", "v83", "v84", "v85", "v86", "v87", "v88", "v89", "v90", "v91", "v92", "v93", "v94", "v95", "v96", "v97", "v98", "v99", "v100", "v101", "v102", "v103", "v104", "v105", "v106", "v107", "v108", "v109", "v110", "v111", "v112", "v113", "v114", "v115", "v116", "v117", "v118", "v119", "v120", "v121", "v122", "v123", "v124", "v125", "v126", "v127", "v128", "v129", "v130", "v131", "v132", "v133", "v134", "v135", "v136", "v137", "v138", "v139", "v140", "v141", "v142", "v143", "v144", "v145", "v146", "v147", "v148", "v149", "v150", "v151", "v152", "v153", "v154", "v155", "v156", "v157", "v158", "v159", "v160", "v161", "v162", "v163", "v164", "v165", "v166", "v167", "v168", "v169", "v170", "v171", "v172", "v173", "v174", "v175", "v176", "v177", "v178", "v179", "v180", "v181", "v182", "v183", "v184", "v185", "v186", "v187", "v188", "v189", "v190", "v191", "v192", "v193", "v194", "v195", "v196", "v197", "v198", "v199", "v200", "v201", "v202", "v203", "v204", "v205", "v206", "v207", "v208", "v209", "v210", "v211", "v212", "v213", "v214", "v215", "v216", "v217", "v218", "v219", "v220", "v221", "v222", "v223", "v224", "v225", "v226", "v227", "v228", "v229", "v230", "v231", "a0", "a1", "a2", "a3", "a4", "a5", "a6", "a7", "a8", "a9", "a10", "a11", "a12", "a13", "a14", "a15", "a16", "a17", "a18", "a19", "a20", "a21", "a22", "a23", "a24", "a25", "a26", "a27", "a28", "a29", "a30", "a31", "a32", "a33", "a34", "a35", "a36", "a37", "a38", "a39", "a40", "a41", "a42", "a43", "a44", "a45", "a46", "a47", "a48", "a49", "a50", "a51", "a52", "a53", "a54", "a55", "a56", "a57", "a58", "a59", "a60", "a61", "a62", "a63", "a64", "a65", "a66", "a67", "a68", "a69", "a70", "a71", "a72", "a73", "a74", "a75", "a76", "a77", "a78", "a79", "a80", "a81", "a82", "a83", "a84", "a85", "a86", "a87", "a88", "a89", "a90", "a91", "a92", "a93", "a94", "a95", "a96", "a97", "a98", "a99", "a100", "a101", "a102", "a103", "a104", "a105", "a106", "a107", "a108", "a109", "a110", "a111", "a112", "a113", "a114", "a115", "a116", "a117", "a118", "a119", "a120", "a121", "a122", "a123", "a124", "a125", "a126", "a127", "a128", "a129", "a130", "a131", "a132", "a133", "a134", "a135", "a136", "a137", "a138", "a139", "a140", "a141", "a142", "a143", "a144", "a145", "a146", "a147", "a148", "a149", "a150", "a151", "a152", "a153", "a154", "a155", "a156", "a157", "a158", "a159", "a160", "a161", "a162", "a163", "a164", "a165", "a166", "a167", "a168", "a169", "a170", "a171", "a172", "a173", "a174", "a175", "a176", "a177", "a178", "a179", "a180", "a181", "a182", "a183", "a184", "a185", "a186", "a187", "a188", "a189", "a190", "a191", "a192", "a193", "a194", "a195", "a196", "a197", "a198", "a199", "a200", "a201", "a202", "a203", "a204", "a205", "a206", "a207", "a208", "a209", "a210", "a211", "a212", "a213", "a214", "a215", "a216", "a217", "a218", "a219", "a220", "a221", "a222", "a223", "a224", "a225", "a226", "a227", "a228", "a229", "a230", "a231", "a232", "a233", "a234", "a235", "a236", "a237", "a238", "a239", "a240", "a241", "a242", "a243", "a244", "a245", "a246", "a247", "a248", "a249", "a250", "a251", "a252", "a253", "a254", "a255", "s36", "s37", "s38", "s39", "s40", "s41", "s42", "s43", "s44", "s45", "s46", "s47", "s48", "s49", "s50", "s51", "s52", "s53", "s54", "s55", "s56", "s57", "s58", "s59", "s60", "s61", "s62", "s63", "s64", "s65", "s66", "s67", "s68", "s69", "s70", "s71", "s72", "s73", "s74", "s75", "s76", "s77", "s78", "s79", "s80", "s81", "s82", "s83", "s84", "s85", "s86", "s87", "s88", "s89", "s90", "s91", "s92", "s93", "s94", "s95", "s96", "s97", "vcc", "scc", "memory"); }
    __builtin_unreachable();
  }
  if (kind < 3) {
    const unsigned sStep = 4;
    COMMON(kind == 1 ? 8 : 16, kind == 1 ? fj * 128 : fj < 8 ? 1024 + fj * 128 : (fj & 2) * 1024 + (fj - 8) * 128, 1024, 131072, 2048, 256, kind == 1 ? 0 : 32768);
    const int sr = l15 - ((wave & 2) ? -1 : 1), sr2 = l15 - ((wave & 2) ? -2 : 2), nx = (slice >> 1) & 1;
    auto src = [&](int r_) { return (unsigned)((r_ & 16) ? 1024 + lg * 64 + (r_ & 3) * 16 : (lg * 16 + r_) * 16); };
    const unsigned vOutS = src(sr2), vSp = (sr2 & 16) ? OOB : vOutS;
    (void)vWr2;
    const unsigned vAn = kind == 1 ? HU + vL16 : src(sr);
    const unsigned vAe = kind == 1 ? ((l15 >> 2) == (nx ? 0 : 3) ? (nx ? 0 : 2 * HU) + 1024 + lg * 64 + (l15 & 3) * 16 : OOB) : (sr & 16) ? OOB : vAn;
    const unsigned vAux0 = l15 * 16, vAux1 = 139264 + (l15 * 2 + (lg & 1)) * 16 + wave * 4 + (lg >> 1) * 512, vAux2 = 139264 + (l15 * 2 + (lg & 1)) * 16;
    const unsigned vAux3 = (wave == 0 && lg < 2) ? slice * 128 + l15 * 8 + lg * 4 : OOB;
    const unsigned sOutOff = kind == 1 ? OFF_HS - HU + (slice * 4 + wave) * 1280 : OFF_X1 + (slice * 4 + wave) * 1024, sFoutOff = (kind == 1 ? OFF_FH : OFF_FX1) + slice * 128;
    const unsigned sAuxOff = OFF_XP + (slice * 4 + wave) * 1024, sBuf = kind, sWw = 16 * wave * 2048 + slice * 256;
    const u16* w12 = kind == 1 ? w1 : w2;
    const unsigned sExp = __builtin_amdgcn_readfirstlane(ep);
    unsigned epOut;
    asm volatile(
R"(.macro M d,w,x
v_mfma_f32_16x16x32_bf16 v[\d:\d+3], \w, v[\x:\x+3], v[\d:\d+3]
.endm
.macro R d,a,o
ds_read_b128 v[\d:\d+3], v\a offset:\o
.endm
.macro T d,a,o
ds_read_b64_tr_b16 \d, v\a offset:\o
.endm
.macro D a,s,o
buffer_load_dwordx4 v\a, s[36:39], s\s offen offset:\o sc1 lds
.endm
.macro L c
s_waitcnt lgkmcnt(\c)
.endm
Lcode_g12_b:
s_cmp_eq_u32 %9,2
s_cbranch_scc1 Lg2_begin_%=
s_mov_b64 s[36:37],%1
s_mov_b32 s38,111243392
s_mov_b32 s39,0x00020000
s_mov_b32 s44,%2
s_mov_b32 s45,%3
s_mov_b32 s46,%4
s_mov_b32 s47,%5
s_mov_b32 s48,%6
s_mov_b32 s49,%7
s_mov_b32 s50,%8
s_mov_b32 s51,%9
s_mov_b32 s53,%10
v_mov_b32 v0,%11
v_mov_b32 v3,%12
v_mov_b32 v5,%13
v_mov_b32 v6,%14
v_mov_b32 v18,%15
v_mov_b32 v19,%16
v_mov_b32 v20,%17
v_mov_b32 v21,%18
v_mov_b32 v9,%19
v_mov_b32 v10,%20
v_mov_b32 v11,%21
v_mov_b32 v12,%22
v_mov_b32 v13,%23
v_mov_b32 v14,%24
v_mov_b32 v15,%25
v_mov_b32 v16,%26
s_mov_b64 s[40:41],%27
s_mov_b32 s42,2097152
s_mov_b32 s43,0x00020000
s_lshl_b32 s57,s44,11
s_waitcnt vmcnt(20) lgkmcnt(0)
s_barrier
s_add_u32 s55,s50,786432
s_add_u32 m0,s57,98304
s_nop 0
buffer_load_dwordx4 v18,s[40:43],s55 offen offset:0  lds
s_add_u32 m0,s57,99328
s_nop 0
buffer_load_dwordx4 v19,s[40:43],s55 offen offset:0  lds
s_add_u32 m0,s57,106368
s_nop 0
buffer_load_dwordx4 v18,s[40:43],s55 offen offset:128  lds
s_add_u32 m0,s57,107392
s_nop 0
buffer_load_dwordx4 v19,s[40:43],s55 offen offset:128  lds
v_add_u32 v26,0,v20
v_add_u32 v27,0,v21
T a[0:1],26,0
T a[2:3],26,512
T a[4:5],27,0
T a[6:7],27,512
T a[8:9],26,4096
T a[10:11],26,4608
T a[12:13],27,4096
T a[14:15],27,4608
s_waitcnt vmcnt(20) lgkmcnt(0)
s_barrier
s_add_u32 s55,s50,917504
s_add_u32 m0,s57,0
s_nop 0
buffer_load_dwordx4 v18,s[40:43],s55 offen offset:0  lds
s_add_u32 m0,s57,1024
s_nop 0
buffer_load_dwordx4 v19,s[40:43],s55 offen offset:0  lds
s_add_u32 m0,s57,8064
s_nop 0
buffer_load_dwordx4 v18,s[40:43],s55 offen offset:128  lds
s_add_u32 m0,s57,9088
s_nop 0
buffer_load_dwordx4 v19,s[40:43],s55 offen offset:128  lds
v_add_u32 v26,16384,v20
v_add_u32 v27,16384,v21
T a[16:17],26,0
T a[18:19],26,512
T a[20:21],27,0
T a[22:23],27,512
T a[24:25],26,4096
T a[26:27],26,4608
T a[28:29],27,4096
T a[30:31],27,4608
s_waitcnt vmcnt(20) lgkmcnt(0)
s_barrier
s_add_u32 s55,s50,1048576
s_add_u32 m0,s57,16384
s_nop 0
buffer_load_dwordx4 v18,s[40:43],s55 offen offset:0  lds
s_add_u32 m0,s57,17408
s_nop 0
buffer_load_dwordx4 v19,s[40:43],s55 offen offset:0  lds
s_add_u32 m0,s57,24448
s_nop 0
buffer_load_dwordx4 v18,s[40:43],s55 offen offset:128  lds
s_add_u32 m0,s57,25472
s_nop 0
buffer_load_dwordx4 v19,s[40:43],s55 offen offset:128  lds
v_add_u32 v26,32768,v20
v_add_u32 v27,32768,v21
T a[32:33],26,0
T a[34:35],26,512
T a[36:37],27,0
T a[38:39],27,512
T a[40:41],26,4096
T a[42:43],26,4608
T a[44:45],27,4096
T a[46:47],27,4608
s_waitcnt vmcnt(20) lgkmcnt(0)
s_barrier
s_add_u32 s55,s50,1179648
s_add_u32 m0,s57,32768
s_nop 0
buffer_load_dwordx4 v18,s[40:43],s55 offen offset:0  lds
s_add_u32 m0,s57,33792
s_nop 0
buffer_load_dwordx4 v19,s[40:43],s55 offen offset:0  lds
s_add_u32 m0,s57,40832
s_nop 0
buffer_load_dwordx4 v18,s[40:43],s55 offen offset:128  lds
s_add_u32 m0,s57,41856
s_nop 0
buffer_load_dwordx4 v19,s[40:43],s55 offen offset:128  lds
v_add_u32 v26,49152,v20
v_add_u32 v27,49152,v21
T a[48:49],26,0
T a[50:51],26,512
T a[52:53],27,0
T a[54:55],27,512
T a[56:57],26,4096
T a[58:59],26,4608
T a[60:61],27,4096
T a[62:63],27,4608
s_waitcnt vmcnt(20) lgkmcnt(0)
s_barrier
s_add_u32 s55,s50,1310720
s_add_u32 m0,s57,49152
s_nop 0
buffer_load_dwordx4 v18,s[40:43],s55 offen offset:0  lds
s_add_u32 m0,s57,50176
s_nop 0
buffer_load_dwordx4 v19,s[40:43],s55 offen offset:0  lds
s_add_u32 m0,s57,57216
s_nop 0
buffer_load_dwordx4 v18,s[40:43],s55 offen offset:128  lds
s_add_u32 m0,s57,58240
s_nop 0
buffer_load_dwordx4 v19,s[40:43],s55 offen offset:128  lds
v_add_u32 v26,65536,v20
v_add_u32 v27,65536,v21
T a[64:65],26,0
T a[66:67],26,512
T a[68:69],27,0
T a[70:71],27,512
T a[72:73],26,4096
T a[74:75],26,4608
T a[76:77],27,4096
T a[78:79],27,4608
s_waitcnt vmcnt(20) lgkmcnt(0)
s_barrier
s_add_u32 s55,s50,1441792
s_add_u32 m0,s57,65536
s_nop 0
buffer_load_dwordx4 v18,s[40:43],s55 offen offset:0  lds
s_add_u32 m0,s57,66560
s_nop 0
buffer_load_dwordx4 v19,s[40:43],s55 offen offset:0  lds
s_add_u32 m0,s57,73600
s_nop 0
buffer_load_dwordx4 v18,s[40:43],s55 offen offset:128  lds
s_add_u32 m0,s57,74624
s_nop 0
buffer_load_dwordx4 v19,s[40:43],s55 offen offset:128  lds
v_add_u32 v26,81920,v20
v_add_u32 v27,81920,v21
T a[80:81],26,0
T a[82:83],26,512
T a[84:85],27,0
T a[86:87],27,512
T a[88:89],26,4096
T a[90:91],26,4608
T a[92:93],27,4096
T a[94:95],27,4608
s_waitcnt vmcnt(20) lgkmcnt(0)
s_barrier
s_add_u32 s55,s50,1572864
s_add_u32 m0,s57,81920
s_nop 0
buffer_load_dwordx4 v18,s[40:43],s55 offen offset:0  lds
s_add_u32 m0,s57,82944
s_nop 0
buffer_load_dwordx4 v19,s[40:43],s55 offen offset:0  lds
s_add_u32 m0,s57,89984
s_nop 0
buffer_load_dwordx4 v18,s[40:43],s55 offen offset:128  lds
s_add_u32 m0,s57,91008
s_nop 0
buffer_load_dwordx4 v19,s[40:43],s55 offen offset:128  lds
v_add_u32 v26,98304,v20
v_add_u32 v27,98304,v21
T a[96:97],26,0
T a[98:99],26,512
T a[100:101],27,0
T a[102:103],27,512
T a[104:105],26,4096
T a[106:107],26,4608
T a[108:109],27,4096
T a[110:111],27,4608
s_waitcnt vmcnt(20) lgkmcnt(0)
s_barrier
s_add_u32 s55,s50,1703936
s_add_u32 m0,s57,98304
s_nop 0
buffer_load_dwordx4 v18,s[40:43],s55 offen offset:0  lds
s_add_u32 m0,s57,99328
s_nop 0
buffer_load_dwordx4 v19,s[40:43],s55 offen offset:0  lds
s_add_u32 m0,s57,106368
s_nop 0
buffer_load_dwordx4 v18,s[40:43],s55 offen offset:128  lds
s_add_u32 m0,s57,107392
s_nop 0
buffer_load_dwordx4 v19,s[40:43],s55 offen offset:128  lds
v_add_u32 v26,0,v20
v_add_u32 v27,0,v21
T a[112:113],26,0
T a[114:115],26,512
T a[116:117],27,0
T a[118:119],27,512
T a[120:121],26,4096
T a[122:123],26,4608
T a[124:125],27,4096
T a[126:127],27,4608
s_waitcnt vmcnt(20) lgkmcnt(0)
s_barrier
s_add_u32 s55,s50,1835008
s_add_u32 m0,s57,0
s_nop 0
buffer_load_dwordx4 v18,s[40:43],s55 offen offset:0  lds
s_add_u32 m0,s57,1024
s_nop 0
buffer_load_dwordx4 v19,s[40:43],s55 offen offset:0  lds
s_add_u32 m0,s57,8064
s_nop 0
buffer_load_dwordx4 v18,s[40:43],s55 offen offset:128  lds
s_add_u32 m0,s57,9088
s_nop 0
buffer_load_dwordx4 v19,s[40:43],s55 offen offset:128  lds
v_add_u32 v26,16384,v20
v_add_u32 v27,16384,v21
T a[128:129],26,0
T a[130:131],26,512
T a[132:133],27,0
T a[134:135],27,512
T a[136:137],26,4096
T a[138:139],26,4608
T a[140:141],27,4096
T a[142:143],27,4608
s_waitcnt vmcnt(20) lgkmcnt(0)
s_barrier
s_add_u32 s55,s50,1966080
s_add_u32 m0,s57,16384
s_nop 0
buffer_load_dwordx4 v18,s[40:43],s55 offen offset:0  lds
s_add_u32 m0,s57,17408
s_nop 0
buffer_load_dwordx4 v19,s[40:43],s55 offen offset:0  lds
s_add_u32 m0,s57,24448
s_nop 0
buffer_load_dwordx4 v18,s[40:43],s55 offen offset:128  lds
s_add_u32 m0,s57,25472
s_nop 0
buffer_load_dwordx4 v19,s[40:43],s55 offen offset:128  lds
v_add_u32 v26,32768,v20
v_add_u32 v27,32768,v21
T a[144:145],26,0
T a[146:147],26,512
T a[148:149],27,0
T a[150:151],27,512
T a[152:153],26,4096
T a[154:155],26,4608
T a[156:157],27,4096
T a[158:159],27,4608
s_waitcnt vmcnt(20) lgkmcnt(0)
s_barrier
v_add_u32 v26,49152,v20
v_add_u32 v27,49152,v21
T a[160:161],26,0
T a[162:163],26,512
T a[164:165],27,0
T a[166:167],27,512
T a[168:169],26,4096
T a[170:171],26,4608
T a[172:173],27,4096
T a[174:175],27,4608
s_waitcnt vmcnt(16) lgkmcnt(0)
s_barrier
v_add_u32 v26,65536,v20
v_add_u32 v27,65536,v21
T a[176:177],26,0
T a[178:179],26,512
T a[180:181],27,0
T a[182:183],27,512
T a[184:185],26,4096
T a[186:187],26,4608
T a[188:189],27,4096
T a[190:191],27,4608
s_waitcnt vmcnt(12) lgkmcnt(0)
s_barrier
v_add_u32 v26,81920,v20
v_add_u32 v27,81920,v21
T a[192:193],26,0
T a[194:195],26,512
T a[196:197],27,0
T a[198:199],27,512
T a[200:201],26,4096
T a[202:203],26,4608
T a[204:205],27,4096
T a[206:207],27,4608
s_waitcnt vmcnt(8) lgkmcnt(0)
s_barrier
v_add_u32 v26,98304,v20
v_add_u32 v27,98304,v21
T a[208:209],26,0
T a[210:211],26,512
T a[212:213],27,0
T a[214:215],27,512
T a[216:217],26,4096
T a[218:219],26,4608
T a[220:221],27,4096
T a[222:223],27,4608
s_waitcnt vmcnt(4) lgkmcnt(0)
s_barrier
v_add_u32 v26,0,v20
v_add_u32 v27,0,v21
T a[224:225],26,0
T a[226:227],26,512
T a[228:229],27,0
T a[230:231],27,512
T a[232:233],26,4096
T a[234:235],26,4608
T a[236:237],27,4096
T a[238:239],27,4608
s_waitcnt vmcnt(0) lgkmcnt(0)
s_barrier
v_add_u32 v26,16384,v20
v_add_u32 v27,16384,v21
T a[240:241],26,0
T a[242:243],26,512
T a[244:245],27,0
T a[246:247],27,512
T a[248:249],26,4096
T a[250:251],26,4608
T a[252:253],27,4096
T a[254:255],27,4608
L 0
s_barrier
v_mov_b32 v34,0x3f803f80
v_mov_b32 v35,0x3f803f80
v_mov_b32 v36,0x3f803f80
v_mov_b32 v37,0x3f803f80
s_nop 1
v_mfma_f32_16x16x32_bf16 v[106:109],a[0:3],v[34:37],0
v_mfma_f32_16x16x32_bf16 v[110:113],a[4:7],v[34:37],0
M 106,a[8:11],34
M 110,a[12:15],34
M 106,a[16:19],34
M 110,a[20:23],34
M 106,a[24:27],34
M 110,a[28:31],34
M 106,a[32:35],34
M 110,a[36:39],34
M 106,a[40:43],34
M 110,a[44:47],34
M 106,a[48:51],34
M 110,a[52:55],34
M 106,a[56:59],34
M 110,a[60:63],34
M 106,a[64:67],34
M 110,a[68:71],34
M 106,a[72:75],34
M 110,a[76:79],34
M 106,a[80:83],34
M 110,a[84:87],34
M 106,a[88:91],34
M 110,a[92:95],34
M 106,a[96:99],34
M 110,a[100:103],34
M 106,a[104:107],34
M 110,a[108:111],34
M 106,a[112:115],34
M 110,a[116:119],34
M 106,a[120:123],34
M 110,a[124:127],34
M 106,a[128:131],34
M 110,a[132:135],34
M 106,a[136:139],34
M 110,a[140:143],34
M 106,a[144:147],34
M 110,a[148:151],34
M 106,a[152:155],34
M 110,a[156:159],34
M 106,a[160:163],34
M 110,a[164:167],34
M 106,a[168:171],34
M 110,a[172:175],34
M 106,a[176:179],34
M 110,a[180:183],34
M 106,a[184:187],34
M 110,a[188:191],34
M 106,a[192:195],34
M 110,a[196:199],34
M 106,a[200:203],34
M 110,a[204:207],34
M 106,a[208:211],34
M 110,a[212:215],34
M 106,a[216:219],34
M 110,a[220:223],34
M 106,a[224:227],34
M 110,a[228:231],34
M 106,a[232:235],34
M 110,a[236:239],34
M 106,a[240:243],34
M 110,a[244:247],34
M 106,a[248:251],34
M 110,a[252:255],34
v_mov_b32 v4,s53
s_mov_b32 s82,0
s_mov_b32 s80,0
s_mov_b32 s54,s45
s_mul_i32 s57,s44,4096
s_mul_i32 s55,s45,32768
s_add_u32 s55,s55,s57
s_add_u32 s55,s55,8388608
s_mul_i32 s57,s44,4096
s_mul_i32 s60,s46,32768
s_mul_i32 s61,s46,1024
s_mul_i32 s62,s46,40960
s_mul_i32 s63,s46,1024
s_mul_i32 s64,s46,512
s_lshl_b32 s59,s44,8
s_add_u32 s59,s59,131072
s_mul_i32 s58,s45,1024
s_add_u32 s58,s58,109056000
s_mul_i32 s65,s45,40960
s_add_u32 s65,s65,s47
s_mul_i32 s68,s45,1024
s_add_u32 s68,s68,s48
s_mul_i32 s73,s45,512
s_add_u32 s73,s73,106954752
s_mul_i32 s74,s45,32768
s_add_u32 s74,s74,s49
s_mul_i32 s75,s45,1024
s_add_u32 s75,s75,107479040
s_mov_b32 s66,0x7fff0000
s_mov_b32 s67,0x7fff0000
s_mov_b32 s69,0x7fff0000
s_mov_b32 s76,0x7fff0000
s_mov_b32 s77,0x7fff0000
s_mov_b32 s70,0x7fff0000
s_mov_b32 s71,0x7fff0000
s_mov_b32 s72,0x7fff0000
v_mov_b32 v1,v0
v_add_u32 v2,16384,v0
s_mov_b32 s84,16384
s_mov_b32 s86,0x3a800000
s_mov_b32 s87,0x3727c5ac
s_mov_b32 s88,0xc0135761
s_mov_b32 s89,0x3d372713
s_lshr_b32 s85,s44,1
s_mul_i32 s85,s85,127
v_lshlrev_b32 v8,22,v0
v_and_b32 v8,0x80000000,v8
v_or_b32 v8,v8,v5
v_add_u32 v2,32768,v0
s_lshl_b32 s84,s57,1
s_add_u32 s84,s84,32768
s_getpc_b64 s[40:41]
Lg1_pc_%=:
s_lshl_b32 s78,s44,13
v_lshlrev_b32 v114,3,v0
v_add_u32 v114,s78,v114
v_min_u32 v114,Lcode_g2_b-Lg1_pc_%=-4,v114
)"
R"(s_mov_b32 m0,163584
s_nop 0
global_load_lds_dword v114,s[40:41]
s_add_u32 m0,s59,4096
s_nop 0
buffer_load_dword v8,s[36:39],s58 offen offset:0 sc1 lds
buffer_store_dword v4,v3,s[36:39],s71 offen offset:0 sc0 sc1
s_add_u32 m0,s59,5120
s_nop 0
buffer_load_dword v8,s[36:39],s58 offen offset:0 sc1 lds
buffer_store_dword v4,v3,s[36:39],s71 offen offset:0 sc0 sc1
s_mov_b32 s83,0
Lg1_poll_%=:
s_add_u32 s78,s83,2
s_and_b32 s78,s78,3
s_lshl_b32 s78,s78,10
s_add_u32 s78,s78,4096
s_add_u32 m0,s59,s78
s_nop 0
buffer_load_dword v8,s[36:39],s58 offen offset:0 sc1 lds
s_and_b32 s78,s83,3
s_lshl_b32 s78,s78,10
v_add_u32 v114,s78,v6
s_waitcnt vmcnt(4)
s_barrier
ds_read_b32 v7,v114 offset:4096
buffer_store_dword v4,v3,s[36:39],s70 offen offset:0 sc0 sc1
s_mov_b32 s70,s71
s_mov_b32 s71,s72
s_mov_b32 s72,0x7fff0000
s_add_u32 s83,s83,1
s_add_u32 s82,s82,1
L 0
v_cmp_eq_u32 vcc,s53,v7
s_nop 0
s_cmp_eq_u32 vcc_lo,-1
s_cbranch_scc1 Lg1_go_%=
s_cmp_lt_u32 s82,20000
s_cbranch_scc1 Lg1_poll_%=
Lg1_go_%=:
s_add_u32 m0,s57,0
s_add_u32 s56,s55,0
D 0,56,0
D 0,56,1024
D 0,56,2048
D 0,56,3072
s_add_u32 s58,s58,s61
s_add_u32 m0,s59,2048
s_nop 0
buffer_load_dword v8,s[36:39],s58 offen offset:0 sc1 lds
s_add_u32 m0,s57,16384
s_add_u32 s56,s55,16384
D 0,56,0
D 0,56,1024
D 0,56,2048
D 0,56,3072
s_add_u32 s55,s55,s60
s_sub_u32 s57,s84,s57
s_add_u32 s54,s54,s46
s_waitcnt vmcnt(4)
s_barrier
buffer_store_dword v4,v3,s[36:39],s70 offen offset:0 sc0 sc1
s_min_u32 s71,s71,s72
buffer_store_dword v4,v3,s[36:39],s71 offen offset:0 sc0 sc1
s_mov_b32 s70,0x7fff0000
s_mov_b32 s71,0x7fff0000
s_mov_b32 s72,0x7fff0000
s_cmp_eq_u32 s80,1
buffer_store_dword v4,v3,s[36:39],s71 offen offset:0 sc0 sc1
s_cbranch_scc1 Lg1_st1_%=
s_branch Lg1_st0_%=
Lg1_gox_%=:
s_add_u32 m0,s59,2048
s_nop 0
buffer_load_dword v8,s[36:39],s58 offen offset:0 sc1 lds
s_waitcnt vmcnt(0)
s_barrier
buffer_store_dword v4,v3,s[36:39],s70 offen offset:0 sc0 sc1
s_min_u32 s71,s71,s72
buffer_store_dword v4,v3,s[36:39],s71 offen offset:0 sc0 sc1
s_mov_b32 s70,0x7fff0000
s_mov_b32 s71,0x7fff0000
s_mov_b32 s72,0x7fff0000
s_cmp_eq_u32 s80,1
s_cbranch_scc1 Lg1_st1_%=
Lg1_st0_%=:
ds_read_b32 v7,v6 offset:2048
R 34,1,0
R 38,1,1024
R 42,1,2048
R 46,1,3072
R 50,1,4096
L 5
v_cmp_eq_u32 vcc,s53,v7
L 3
s_cmp_eq_u32 vcc_lo,-1
s_cbranch_scc1 Lg1_F0_%=
s_branch Lg1_L0_%=
Lg1_st1_%=:
ds_read_b32 v7,v6 offset:2048
R 34,2,0
R 38,2,1024
R 42,2,2048
R 46,2,3072
R 50,2,4096
L 5
v_cmp_eq_u32 vcc,s53,v7
L 3
s_cmp_eq_u32 vcc_lo,-1
s_cbranch_scc1 Lg1_F1_%=
s_branch Lg1_L1_%=
Lg1_F0_%=:
v_mfma_f32_16x16x32_bf16 v[22:25],a[4:7],v[34:37],0
s_add_u32 s58,s58,s61
s_add_u32 m0,s59,0
R 54,1,5120
M 22,a[12:15],38
buffer_load_dword v8,s[36:39],s58 offen offset:0 sc1 lds
v_mfma_f32_16x16x32_bf16 v[18:21],a[0:3],v[34:37],0
s_add_u32 m0,s57,0
R 58,1,6144
v_fma_f32 v26,-v102,v106,v26
M 18,a[8:11],38
L 3
s_add_u32 s56,s55,0
v_fma_f32 v27,-v102,v107,v27
M 18,a[16:19],42
s_mov_b32 s66,s65
R 62,1,7168
v_fma_f32 v28,-v102,v108,v28
M 18,a[24:27],46
D 0,56,0
M 22,a[20:23],42
s_add_u32 s65,s65,s62
R 66,1,8192
v_fma_f32 v29,-v102,v109,v29
v_mul_f32 v114,v26,v26
M 22,a[28:31],46
L 3
D 0,56,1024
M 22,a[36:39],50
s_add_u32 s54,s54,s46
R 70,1,9216
v_mul_f32 v115,v27,v27
M 22,a[44:47],54
v_mul_f32 v116,v28,v28
v_mul_f32 v117,v29,v29
M 18,a[32:35],50
R 74,1,10240
v_fma_f32 v114,v104,v114,v103
M 18,a[40:43],54
L 3
D 0,56,2048
M 18,a[48:51],58
R 78,1,11264
v_fma_f32 v115,v104,v115,v103
M 18,a[56:59],62
D 0,56,3072
v_fma_f32 v116,v104,v116,v103
M 22,a[52:55],58
s_add_u32 m0,s57,16384
R 82,1,12288
v_fma_f32 v117,v104,v117,v103
M 22,a[60:63],62
L 3
v_mul_f32 v114,v26,v114
v_mul_f32 v115,v27,v115
M 22,a[68:71],66
s_add_u32 s56,s55,16384
R 86,1,13312
v_mul_f32 v116,v28,v116
M 22,a[76:79],70
buffer_load_dwordx4 v[98:101],v11,s[36:39],s73 offen offset:0 sc1
M 18,a[64:67],66
s_add_u32 s73,s73,s64
R 90,1,14336
v_mul_f32 v117,v29,v117
M 18,a[72:75],70
L 3
v_exp_f32 v114,v114
v_fma_f32 v30,-v102,v110,v30
M 18,a[80:83],74
s_waitcnt vmcnt(9)
R 94,1,15360
v_fma_f32 v114,v114,v105,v105
M 18,a[88:91],78
s_barrier
v_exp_f32 v115,v115
M 22,a[84:87],74
s_add_u32 s55,s55,s60
R 34,1,16384
v_fma_f32 v115,v115,v105,v105
M 22,a[92:95],78
L 3
v_exp_f32 v116,v116
M 22,a[100:103],82
s_sub_u32 s57,s84,s57
R 38,1,17408
v_fma_f32 v116,v116,v105,v105
M 22,a[108:111],86
D 0,56,0
M 18,a[96:99],82
R 42,1,18432
v_exp_f32 v117,v117
M 18,a[104:107],86
L 3
v_rcp_f32 v114,v114
M 18,a[112:115],90
R 46,1,19456
v_fma_f32 v117,v117,v105,v105
M 18,a[120:123],94
v_rcp_f32 v115,v115
M 22,a[116:119],90
R 50,1,20480
v_mul_f32 v26,v26,v114
M 22,a[124:127],94
L 3
v_rcp_f32 v116,v116
M 22,a[132:135],34
R 54,1,21504
v_mul_f32 v27,v27,v115
M 22,a[140:143],38
D 0,56,1024
v_mul_f32 v28,v28,v116
M 18,a[128:131],34
R 58,1,22528
v_cvt_pk_bf16_f32 v122,v26,v27
M 18,a[136:139],38
L 3
v_rcp_f32 v117,v117
M 18,a[144:147],42
R 62,1,23552
v_mul_f32 v29,v29,v117
M 18,a[152:155],46
v_cvt_pk_bf16_f32 v123,v28,v29
v_fma_f32 v31,-v102,v111,v31
M 22,a[148:151],42
R 66,1,24576
v_fma_f32 v32,-v102,v112,v32
M 22,a[156:159],46
L 3
D 0,56,2048
v_fma_f32 v33,-v102,v113,v33
M 22,a[164:167],50
R 70,1,25600
v_mul_f32 v118,v30,v30
M 22,a[172:175],54
v_mul_f32 v119,v31,v31
v_mul_f32 v120,v32,v32
M 18,a[160:163],50
R 74,1,26624
v_mul_f32 v121,v33,v33
M 18,a[168:171],54
L 3
v_fma_f32 v118,v104,v118,v103
v_fma_f32 v119,v104,v119,v103
M 18,a[176:179],58
R 78,1,27648
v_fma_f32 v120,v104,v120,v103
M 18,a[184:187],62
v_fma_f32 v121,v104,v121,v103
v_mul_f32 v118,v30,v118
M 22,a[180:183],58
R 82,1,28672
v_exp_f32 v118,v118
M 22,a[188:191],62
L 3
D 0,56,3072
M 22,a[196:199],66
R 86,1,29696
v_mul_f32 v119,v31,v119
M 22,a[204:207],70
v_exp_f32 v119,v119
M 18,a[192:195],66
R 90,1,30720
v_mul_f32 v120,v32,v120
M 18,a[200:203],70
L 3
v_mul_f32 v121,v33,v121
v_fma_f32 v118,v118,v105,v105
M 18,a[208:211],74
s_waitcnt vmcnt(5)
R 94,1,31744
v_fma_f32 v119,v119,v105,v105
M 18,a[216:219],78
s_barrier
ds_read_b32 v7,v6 offset:0
v_exp_f32 v120,v120
M 22,a[212:215],74
R 34,2,0
v_fma_f32 v120,v120,v105,v105
M 22,a[220:223],78
L 4
buffer_store_dword v4,v3,s[36:39],s70 offen offset:0 sc0 sc1
v_exp_f32 v121,v121
M 22,a[228:231],82
s_mov_b32 s70,s69
R 38,2,1024
v_fma_f32 v121,v121,v105,v105
v_rcp_f32 v118,v118
v_rcp_f32 v119,v119
v_rcp_f32 v120,v120
v_rcp_f32 v121,v121
M 22,a[236:239],86
s_mov_b32 s69,s68
v_mul_f32 v30,v30,v118
v_mul_f32 v31,v31,v119
v_mul_f32 v32,v32,v120
v_mul_f32 v33,v33,v121
v_cvt_pk_bf16_f32 v124,v30,v31
v_cvt_pk_bf16_f32 v125,v32,v33
M 18,a[224:227],82
s_add_u32 s68,s68,s63
R 42,2,2048
buffer_store_dwordx4 v[122:125],v15,s[36:39],s67 offen offset:0 sc0 sc1
M 18,a[232:235],86
L 4
L 3
M 18,a[240:243],90
R 46,2,3072
v_cmp_eq_u32 vcc,s53,v7
M 18,a[248:251],94
buffer_store_dwordx4 v[122:125],v16,s[36:39],s67 offen offset:0 sc0 sc1
M 22,a[244:247],90
s_waitcnt vmcnt(7) lgkmcnt(2)
s_cmp_eq_u32 vcc_lo,-1
R 50,2,4096
M 22,a[252:255],94
s_cbranch_scc0 Lg1_K1_%=
Lg1_F1_%=:
v_mfma_f32_16x16x32_bf16 v[30:33],a[4:7],v[34:37],0
s_add_u32 s58,s58,s61
s_add_u32 m0,s59,1024
R 54,2,5120
M 30,a[12:15],38
buffer_load_dword v8,s[36:39],s58 offen offset:0 sc1 lds
v_mfma_f32_16x16x32_bf16 v[26:29],a[0:3],v[34:37],0
s_add_u32 m0,s57,0
R 58,2,6144
v_fma_f32 v18,-v98,v106,v18
M 26,a[8:11],38
L 3
s_add_u32 s56,s55,0
v_fma_f32 v19,-v98,v107,v19
M 26,a[16:19],42
s_mov_b32 s67,s65
R 62,2,7168
v_fma_f32 v20,-v98,v108,v20
M 26,a[24:27],46
D 0,56,0
M 30,a[20:23],42
s_add_u32 s65,s65,s62
R 66,2,8192
v_fma_f32 v21,-v98,v109,v21
v_mul_f32 v114,v18,v18
M 30,a[28:31],46
L 3
D 0,56,1024
M 30,a[36:39],50
s_add_u32 s54,s54,s46
R 70,2,9216
v_mul_f32 v115,v19,v19
M 30,a[44:47],54
v_mul_f32 v116,v20,v20
v_mul_f32 v117,v21,v21
M 26,a[32:35],50
R 74,2,10240
v_fma_f32 v114,v100,v114,v99
M 26,a[40:43],54
L 3
D 0,56,2048
M 26,a[48:51],58
R 78,2,11264
v_fma_f32 v115,v100,v115,v99
M 26,a[56:59],62
D 0,56,3072
v_fma_f32 v116,v100,v116,v99
M 30,a[52:55],58
s_add_u32 m0,s57,16384
R 82,2,12288
v_fma_f32 v117,v100,v117,v99
M 30,a[60:63],62
L 3
v_mul_f32 v114,v18,v114
v_mul_f32 v115,v19,v115
M 30,a[68:71],66
s_add_u32 s56,s55,16384
R 86,2,13312
v_mul_f32 v116,v20,v116
M 30,a[76:79],70
buffer_load_dwordx4 v[102:105],v11,s[36:39],s73 offen offset:0 sc1
M 26,a[64:67],66
s_add_u32 s73,s73,s64
R 90,2,14336
v_mul_f32 v117,v21,v117
M 26,a[72:75],70
L 3
v_exp_f32 v114,v114
v_fma_f32 v22,-v98,v110,v22
M 26,a[80:83],74
s_waitcnt vmcnt(9)
R 94,2,15360
v_fma_f32 v114,v114,v101,v101
M 26,a[88:91],78
s_barrier
v_exp_f32 v115,v115
M 30,a[84:87],74
s_add_u32 s55,s55,s60
R 34,2,16384
v_fma_f32 v115,v115,v101,v101
M 30,a[92:95],78
L 3
v_exp_f32 v116,v116
M 30,a[100:103],82
s_sub_u32 s57,s84,s57
R 38,2,17408
v_fma_f32 v116,v116,v101,v101
M 30,a[108:111],86
D 0,56,0
M 26,a[96:99],82
R 42,2,18432
v_exp_f32 v117,v117
M 26,a[104:107],86
L 3
v_rcp_f32 v114,v114
M 26,a[112:115],90
R 46,2,19456
v_fma_f32 v117,v117,v101,v101
M 26,a[120:123],94
v_rcp_f32 v115,v115
M 30,a[116:119],90
R 50,2,20480
v_mul_f32 v18,v18,v114
M 30,a[124:127],94
L 3
v_rcp_f32 v116,v116
M 30,a[132:135],34
R 54,2,21504
v_mul_f32 v19,v19,v115
M 30,a[140:143],38
D 0,56,1024
v_mul_f32 v20,v20,v116
M 26,a[128:131],34
R 58,2,22528
v_cvt_pk_bf16_f32 v122,v18,v19
M 26,a[136:139],38
L 3
v_rcp_f32 v117,v117
M 26,a[144:147],42
R 62,2,23552
v_mul_f32 v21,v21,v117
M 26,a[152:155],46
v_cvt_pk_bf16_f32 v123,v20,v21
v_fma_f32 v23,-v98,v111,v23
M 30,a[148:151],42
R 66,2,24576
v_fma_f32 v24,-v98,v112,v24
M 30,a[156:159],46
L 3
D 0,56,2048
v_fma_f32 v25,-v98,v113,v25
M 30,a[164:167],50
R 70,2,25600
v_mul_f32 v118,v22,v22
M 30,a[172:175],54
v_mul_f32 v119,v23,v23
v_mul_f32 v120,v24,v24
M 26,a[160:163],50
R 74,2,26624
v_mul_f32 v121,v25,v25
M 26,a[168:171],54
L 3
v_fma_f32 v118,v100,v118,v99
v_fma_f32 v119,v100,v119,v99
M 26,a[176:179],58
R 78,2,27648
v_fma_f32 v120,v100,v120,v99
M 26,a[184:187],62
v_fma_f32 v121,v100,v121,v99
v_mul_f32 v118,v22,v118
M 30,a[180:183],58
R 82,2,28672
v_exp_f32 v118,v118
M 30,a[188:191],62
L 3
D 0,56,3072
M 30,a[196:199],66
R 86,2,29696
v_mul_f32 v119,v23,v119
M 30,a[204:207],70
v_exp_f32 v119,v119
M 26,a[192:195],66
R 90,2,30720
v_mul_f32 v120,v24,v120
M 26,a[200:203],70
L 3
v_mul_f32 v121,v25,v121
v_fma_f32 v118,v118,v101,v101
M 26,a[208:211],74
s_waitcnt vmcnt(5)
)"
R"(R 94,2,31744
v_fma_f32 v119,v119,v101,v101
M 26,a[216:219],78
s_barrier
ds_read_b32 v7,v6 offset:1024
v_exp_f32 v120,v120
M 30,a[212:215],74
R 34,1,0
v_fma_f32 v120,v120,v101,v101
M 30,a[220:223],78
L 4
buffer_store_dword v4,v3,s[36:39],s70 offen offset:0 sc0 sc1
v_exp_f32 v121,v121
M 30,a[228:231],82
s_mov_b32 s70,s69
R 38,1,1024
v_fma_f32 v121,v121,v101,v101
v_rcp_f32 v118,v118
v_rcp_f32 v119,v119
v_rcp_f32 v120,v120
v_rcp_f32 v121,v121
M 30,a[236:239],86
s_mov_b32 s69,s68
v_mul_f32 v22,v22,v118
v_mul_f32 v23,v23,v119
v_mul_f32 v24,v24,v120
v_mul_f32 v25,v25,v121
v_cvt_pk_bf16_f32 v124,v22,v23
v_cvt_pk_bf16_f32 v125,v24,v25
M 26,a[224:227],82
s_add_u32 s68,s68,s63
R 42,1,2048
buffer_store_dwordx4 v[122:125],v15,s[36:39],s66 offen offset:0 sc0 sc1
M 26,a[232:235],86
L 4
L 3
M 26,a[240:243],90
R 46,1,3072
v_cmp_eq_u32 vcc,s53,v7
M 26,a[248:251],94
buffer_store_dwordx4 v[122:125],v16,s[36:39],s66 offen offset:0 sc0 sc1
M 30,a[244:247],90
s_waitcnt vmcnt(7) lgkmcnt(2)
s_cmp_eq_u32 vcc_lo,-1
R 50,1,4096
M 30,a[252:255],94
s_cbranch_scc1 Lg1_F0_%=
Lg1_K0_%=:
Lg1_L0_%=:
v_mfma_f32_16x16x32_bf16 v[22:25],a[4:7],v[34:37],0
s_add_u32 m0,s59,3072
s_mov_b32 s66,s65
R 54,1,5120
M 22,a[12:15],38
buffer_load_dword v8,s[36:39],s58 offen offset:0 sc1 lds
v_mfma_f32_16x16x32_bf16 v[18:21],a[0:3],v[34:37],0
s_add_u32 s65,s65,s62
R 58,1,6144
v_fma_f32 v26,-v102,v106,v26
M 18,a[8:11],38
L 3
v_fma_f32 v27,-v102,v107,v27
v_fma_f32 v28,-v102,v108,v28
M 18,a[16:19],42
R 62,1,7168
v_fma_f32 v29,-v102,v109,v29
M 18,a[24:27],46
v_mul_f32 v114,v26,v26
v_mul_f32 v115,v27,v27
M 22,a[20:23],42
R 66,1,8192
v_mul_f32 v116,v28,v28
M 22,a[28:31],46
L 3
v_mul_f32 v117,v29,v29
v_fma_f32 v114,v104,v114,v103
M 22,a[36:39],50
R 70,1,9216
v_fma_f32 v115,v104,v115,v103
M 22,a[44:47],54
v_fma_f32 v116,v104,v116,v103
v_fma_f32 v117,v104,v117,v103
M 18,a[32:35],50
R 74,1,10240
v_mul_f32 v114,v26,v114
M 18,a[40:43],54
L 3
v_exp_f32 v114,v114
M 18,a[48:51],58
R 78,1,11264
v_mul_f32 v115,v27,v115
M 18,a[56:59],62
v_exp_f32 v115,v115
M 22,a[52:55],58
R 82,1,12288
v_mul_f32 v116,v28,v116
M 22,a[60:63],62
L 3
v_exp_f32 v116,v116
M 22,a[68:71],66
R 86,1,13312
v_mul_f32 v117,v29,v117
M 22,a[76:79],70
buffer_load_dwordx4 v[98:101],v11,s[36:39],s73 offen offset:0 sc1
M 18,a[64:67],66
s_add_u32 s73,s73,s64
R 90,1,14336
v_fma_f32 v114,v114,v105,v105
M 18,a[72:75],70
L 3
v_exp_f32 v117,v117
M 18,a[80:83],74
s_waitcnt vmcnt(1)
R 94,1,15360
v_fma_f32 v115,v115,v105,v105
M 18,a[88:91],78
s_barrier
ds_read_b32 v7,v6 offset:3072
v_fma_f32 v116,v116,v105,v105
M 22,a[84:87],74
R 34,1,16384
v_fma_f32 v117,v117,v105,v105
M 22,a[92:95],78
L 4
buffer_store_dword v4,v3,s[36:39],s70 offen offset:0 sc0 sc1
M 22,a[100:103],82
s_mov_b32 s70,0x7fff0000
R 38,1,17408
v_fma_f32 v30,-v102,v110,v30
M 22,a[108:111],86
v_rcp_f32 v114,v114
M 18,a[96:99],82
R 42,1,18432
v_mul_f32 v26,v26,v114
M 18,a[104:107],86
L 4
L 3
v_cmp_eq_u32 vcc,s53,v7
M 18,a[112:115],90
R 46,1,19456
v_fma_f32 v31,-v102,v111,v31
M 18,a[120:123],94
v_rcp_f32 v115,v115
M 22,a[116:119],90
s_cmp_eq_u32 vcc_lo,-1
R 50,1,20480
v_mul_f32 v27,v27,v115
s_cbranch_scc0 Lg1_X0_%=
M 22,a[124:127],94
L 3
s_add_u32 m0,s57,0
s_add_u32 s56,s55,0
M 22,a[132:135],34
s_add_u32 s58,s58,s61
R 54,1,21504
v_cvt_pk_bf16_f32 v122,v26,v27
M 22,a[140:143],38
D 0,56,0
M 18,a[128:131],34
s_add_u32 s54,s54,s46
R 58,1,22528
v_fma_f32 v32,-v102,v112,v32
M 18,a[136:139],38
L 3
v_rcp_f32 v116,v116
M 18,a[144:147],42
R 62,1,23552
v_mul_f32 v28,v28,v116
M 18,a[152:155],46
D 0,56,1024
M 22,a[148:151],42
R 66,1,24576
v_fma_f32 v33,-v102,v113,v33
M 22,a[156:159],46
L 3
v_rcp_f32 v117,v117
M 22,a[164:167],50
R 70,1,25600
v_mul_f32 v29,v29,v117
M 22,a[172:175],54
D 0,56,2048
M 18,a[160:163],50
R 74,1,26624
v_cvt_pk_bf16_f32 v123,v28,v29
M 18,a[168:171],54
L 3
v_mul_f32 v118,v30,v30
v_mul_f32 v119,v31,v31
M 18,a[176:179],58
R 78,1,27648
v_mul_f32 v120,v32,v32
M 18,a[184:187],62
D 0,56,3072
M 22,a[180:183],58
s_add_u32 m0,s59,0
R 82,1,28672
v_mul_f32 v121,v33,v33
M 22,a[188:191],62
L 3
buffer_load_dword v8,s[36:39],s58 offen offset:0 sc1 lds
M 22,a[196:199],66
s_add_u32 m0,s57,16384
R 86,1,29696
v_fma_f32 v118,v104,v118,v103
M 22,a[204:207],70
s_add_u32 s56,s55,16384
v_fma_f32 v119,v104,v119,v103
v_fma_f32 v120,v104,v120,v103
M 18,a[192:195],66
s_add_u32 s55,s55,s60
R 90,1,30720
v_fma_f32 v121,v104,v121,v103
M 18,a[200:203],70
L 3
D 0,56,0
M 18,a[208:211],74
s_waitcnt vmcnt(1)
R 94,1,31744
v_mul_f32 v118,v30,v118
M 18,a[216:219],78
s_barrier
ds_read_b32 v7,v6 offset:0
v_mul_f32 v119,v31,v119
M 22,a[212:215],74
s_sub_u32 s57,s84,s57
R 34,2,0
v_mul_f32 v120,v32,v120
M 22,a[220:223],78
L 4
buffer_store_dword v4,v3,s[36:39],s70 offen offset:0 sc0 sc1
v_mul_f32 v121,v33,v121
v_exp_f32 v118,v118
v_exp_f32 v119,v119
v_exp_f32 v120,v120
v_exp_f32 v121,v121
M 22,a[228:231],82
s_mov_b32 s70,s69
R 38,2,1024
v_fma_f32 v118,v118,v105,v105
v_fma_f32 v119,v119,v105,v105
v_fma_f32 v120,v120,v105,v105
v_fma_f32 v121,v121,v105,v105
v_rcp_f32 v118,v118
v_rcp_f32 v119,v119
v_rcp_f32 v120,v120
v_rcp_f32 v121,v121
M 22,a[236:239],86
s_mov_b32 s69,s68
v_mul_f32 v30,v30,v118
v_mul_f32 v31,v31,v119
v_mul_f32 v32,v32,v120
v_mul_f32 v33,v33,v121
v_cvt_pk_bf16_f32 v124,v30,v31
v_cvt_pk_bf16_f32 v125,v32,v33
M 18,a[224:227],82
s_add_u32 s68,s68,s63
R 42,2,2048
buffer_store_dwordx4 v[122:125],v15,s[36:39],s67 offen offset:0 sc0 sc1
M 18,a[232:235],86
L 4
L 3
M 18,a[240:243],90
R 46,2,3072
v_cmp_eq_u32 vcc,s53,v7
M 18,a[248:251],94
D 0,56,1024
M 22,a[244:247],90
L 2
s_cmp_eq_u32 vcc_lo,-1
R 50,2,4096
buffer_store_dwordx4 v[122:125],v16,s[36:39],s67 offen offset:0 sc0 sc1
M 22,a[252:255],94
D 0,56,2048
D 0,56,3072
buffer_store_dword v4,v3,s[36:39],s71 offen offset:0 sc0 sc1
buffer_store_dword v4,v3,s[36:39],s71 offen offset:0 sc0 sc1
buffer_store_dword v4,v3,s[36:39],s71 offen offset:0 sc0 sc1
s_cbranch_scc1 Lg1_F1_%=
s_branch Lg1_K1_%=
Lg1_X0_%=:
M 22,a[124:127],94
L 3
s_add_u32 m0,s59,0
v_cvt_pk_bf16_f32 v122,v26,v27
M 22,a[132:135],34
R 54,1,21504
v_fma_f32 v32,-v102,v112,v32
M 22,a[140:143],38
buffer_load_dword v8,s[36:39],s58 offen offset:0 sc1 lds
M 18,a[128:131],34
R 58,1,22528
v_fma_f32 v33,-v102,v113,v33
M 18,a[136:139],38
L 3
v_rcp_f32 v116,v116
M 18,a[144:147],42
R 62,1,23552
v_mul_f32 v28,v28,v116
M 18,a[152:155],46
v_rcp_f32 v117,v117
M 22,a[148:151],42
R 66,1,24576
v_mul_f32 v29,v29,v117
M 22,a[156:159],46
L 3
v_cvt_pk_bf16_f32 v123,v28,v29
v_mul_f32 v118,v30,v30
M 22,a[164:167],50
R 70,1,25600
v_mul_f32 v119,v31,v31
M 22,a[172:175],54
v_mul_f32 v120,v32,v32
v_mul_f32 v121,v33,v33
M 18,a[160:163],50
R 74,1,26624
v_fma_f32 v118,v104,v118,v103
M 18,a[168:171],54
L 3
v_fma_f32 v119,v104,v119,v103
v_fma_f32 v120,v104,v120,v103
M 18,a[176:179],58
R 78,1,27648
v_fma_f32 v121,v104,v121,v103
M 18,a[184:187],62
v_mul_f32 v118,v30,v118
v_mul_f32 v119,v31,v119
M 22,a[180:183],58
R 82,1,28672
v_mul_f32 v120,v32,v120
M 22,a[188:191],62
L 3
v_exp_f32 v118,v118
M 22,a[196:199],66
R 86,1,29696
v_mul_f32 v121,v33,v121
M 22,a[204:207],70
v_exp_f32 v119,v119
M 18,a[192:195],66
R 90,1,30720
v_fma_f32 v118,v118,v105,v105
M 18,a[200:203],70
L 3
v_exp_f32 v120,v120
M 18,a[208:211],74
s_waitcnt vmcnt(0)
R 94,1,31744
v_fma_f32 v119,v119,v105,v105
M 18,a[216:219],78
s_barrier
buffer_store_dword v4,v3,s[36:39],s70 offen offset:0 sc0 sc1
M 22,a[212:215],74
s_mov_b32 s70,s69
ds_read_b32 v7,v6 offset:0
v_fma_f32 v120,v120,v105,v105
M 22,a[220:223],78
L 3
v_exp_f32 v121,v121
M 22,a[228:231],82
s_mov_b32 s69,s68
v_fma_f32 v121,v121,v105,v105
v_rcp_f32 v118,v118
v_rcp_f32 v119,v119
v_rcp_f32 v120,v120
v_rcp_f32 v121,v121
M 22,a[236:239],86
s_add_u32 s68,s68,s63
v_mul_f32 v30,v30,v118
v_mul_f32 v31,v31,v119
v_mul_f32 v32,v32,v120
v_mul_f32 v33,v33,v121
v_cvt_pk_bf16_f32 v124,v30,v31
v_cvt_pk_bf16_f32 v125,v32,v33
M 18,a[224:227],82
buffer_store_dwordx4 v[122:125],v15,s[36:39],s67 offen offset:0 sc0 sc1
M 18,a[232:235],86
L 1
M 18,a[240:243],90
L 0
v_cmp_eq_u32 vcc,s53,v7
M 18,a[248:251],94
buffer_store_dwordx4 v[122:125],v16,s[36:39],s67 offen offset:0 sc0 sc1
M 22,a[244:247],90
s_nop 0
M 22,a[252:255],94
s_mov_b32 s80,1
s_cmp_eq_u32 vcc_lo,-1
s_cbranch_scc0 Lg1_xn0_%=
s_add_u32 m0,s57,0
s_add_u32 s56,s55,0
D 0,56,0
v_fma_f32 v18,-v98,v106,v18
v_fma_f32 v19,-v98,v107,v19
v_fma_f32 v20,-v98,v108,v20
v_fma_f32 v21,-v98,v109,v21
v_mul_f32 v114,v18,v18
v_mul_f32 v115,v19,v19
v_mul_f32 v116,v20,v20
D 0,56,1024
v_mul_f32 v117,v21,v21
v_fma_f32 v114,v100,v114,v99
v_fma_f32 v115,v100,v115,v99
v_fma_f32 v116,v100,v116,v99
v_fma_f32 v117,v100,v117,v99
v_mul_f32 v114,v18,v114
v_mul_f32 v115,v19,v115
D 0,56,2048
v_mul_f32 v116,v20,v116
v_mul_f32 v117,v21,v117
v_exp_f32 v114,v114
v_exp_f32 v115,v115
v_exp_f32 v116,v116
v_exp_f32 v117,v117
v_fma_f32 v114,v114,v101,v101
D 0,56,3072
v_fma_f32 v115,v115,v101,v101
v_fma_f32 v116,v116,v101,v101
v_fma_f32 v117,v117,v101,v101
v_rcp_f32 v114,v114
v_rcp_f32 v115,v115
v_rcp_f32 v116,v116
v_rcp_f32 v117,v117
s_add_u32 s58,s58,s61
s_add_u32 m0,s57,16384
s_add_u32 s56,s55,16384
D 0,56,0
v_mul_f32 v18,v18,v114
v_mul_f32 v19,v19,v115
v_mul_f32 v20,v20,v116
v_mul_f32 v21,v21,v117
v_cvt_pk_bf16_f32 v122,v18,v19
v_cvt_pk_bf16_f32 v123,v20,v21
v_fma_f32 v22,-v98,v110,v22
D 0,56,1024
v_fma_f32 v23,-v98,v111,v23
v_fma_f32 v24,-v98,v112,v24
v_fma_f32 v25,-v98,v113,v25
v_mul_f32 v118,v22,v22
v_mul_f32 v119,v23,v23
v_mul_f32 v120,v24,v24
v_mul_f32 v121,v25,v25
D 0,56,2048
v_fma_f32 v118,v100,v118,v99
v_fma_f32 v119,v100,v119,v99
v_fma_f32 v120,v100,v120,v99
v_fma_f32 v121,v100,v121,v99
v_mul_f32 v118,v22,v118
v_mul_f32 v119,v23,v119
v_mul_f32 v120,v24,v120
D 0,56,3072
v_mul_f32 v121,v25,v121
v_exp_f32 v118,v118
v_exp_f32 v119,v119
v_exp_f32 v120,v120
v_exp_f32 v121,v121
v_fma_f32 v118,v118,v101,v101
v_fma_f32 v119,v119,v101,v101
s_add_u32 s55,s55,s60
s_sub_u32 s57,s84,s57
s_add_u32 s54,s54,s46
v_fma_f32 v120,v120,v101,v101
v_fma_f32 v121,v121,v101,v101
v_rcp_f32 v118,v118
v_rcp_f32 v119,v119
v_rcp_f32 v120,v120
v_rcp_f32 v121,v121
v_mul_f32 v22,v22,v118
v_mul_f32 v23,v23,v119
v_mul_f32 v24,v24,v120
v_mul_f32 v25,v25,v121
v_cvt_pk_bf16_f32 v124,v22,v23
v_cvt_pk_bf16_f32 v125,v24,v25
buffer_store_dwordx4 v[122:125],v15,s[36:39],s66 offen offset:0 sc0 sc1
buffer_store_dwordx4 v[122:125],v16,s[36:39],s66 offen offset:0 sc0 sc1
s_mov_b32 s72,s69
s_mov_b32 s69,0x7fff0000
s_mov_b32 s66,0x7fff0000
s_mov_b32 s76,0x7fff0000
s_branch Lg1_gox_%=
Lg1_xn0_%=:
s_cmp_ge_u32 s54,256
s_cbranch_scc1 Lg1_fin0_%=
s_add_u32 m0,s59,4096
s_nop 0
buffer_load_dword v8,s[36:39],s58 offen offset:0 sc1 lds
buffer_store_dword v4,v3,s[36:39],s71 offen offset:0 sc0 sc1
s_add_u32 m0,s59,5120
s_nop 0
buffer_load_dword v8,s[36:39],s58 offen offset:0 sc1 lds
buffer_store_dword v4,v3,s[36:39],s71 offen offset:0 sc0 sc1
s_mov_b32 s83,0
v_fma_f32 v18,-v98,v106,v18
v_fma_f32 v19,-v98,v107,v19
v_fma_f32 v20,-v98,v108,v20
v_fma_f32 v21,-v98,v109,v21
v_mul_f32 v114,v18,v18
v_mul_f32 v115,v19,v19
v_mul_f32 v116,v20,v20
v_mul_f32 v117,v21,v21
v_fma_f32 v114,v100,v114,v99
v_fma_f32 v115,v100,v115,v99
)"
R"(v_fma_f32 v116,v100,v116,v99
v_fma_f32 v117,v100,v117,v99
v_mul_f32 v114,v18,v114
v_mul_f32 v115,v19,v115
v_mul_f32 v116,v20,v116
v_mul_f32 v117,v21,v117
v_exp_f32 v114,v114
v_exp_f32 v115,v115
v_exp_f32 v116,v116
v_exp_f32 v117,v117
v_fma_f32 v114,v114,v101,v101
v_fma_f32 v115,v115,v101,v101
v_fma_f32 v116,v116,v101,v101
v_fma_f32 v117,v117,v101,v101
v_rcp_f32 v114,v114
v_rcp_f32 v115,v115
v_rcp_f32 v116,v116
v_rcp_f32 v117,v117
v_mul_f32 v18,v18,v114
v_mul_f32 v19,v19,v115
v_mul_f32 v20,v20,v116
v_mul_f32 v21,v21,v117
v_cvt_pk_bf16_f32 v122,v18,v19
v_cvt_pk_bf16_f32 v123,v20,v21
v_fma_f32 v22,-v98,v110,v22
v_fma_f32 v23,-v98,v111,v23
v_fma_f32 v24,-v98,v112,v24
v_fma_f32 v25,-v98,v113,v25
v_mul_f32 v118,v22,v22
v_mul_f32 v119,v23,v23
v_mul_f32 v120,v24,v24
v_mul_f32 v121,v25,v25
v_fma_f32 v118,v100,v118,v99
v_fma_f32 v119,v100,v119,v99
v_fma_f32 v120,v100,v120,v99
v_fma_f32 v121,v100,v121,v99
v_mul_f32 v118,v22,v118
v_mul_f32 v119,v23,v119
v_mul_f32 v120,v24,v120
v_mul_f32 v121,v25,v121
v_exp_f32 v118,v118
v_exp_f32 v119,v119
v_exp_f32 v120,v120
v_exp_f32 v121,v121
v_fma_f32 v118,v118,v101,v101
v_fma_f32 v119,v119,v101,v101
v_fma_f32 v120,v120,v101,v101
v_fma_f32 v121,v121,v101,v101
v_rcp_f32 v118,v118
v_rcp_f32 v119,v119
v_rcp_f32 v120,v120
v_rcp_f32 v121,v121
v_mul_f32 v22,v22,v118
v_mul_f32 v23,v23,v119
v_mul_f32 v24,v24,v120
v_mul_f32 v25,v25,v121
v_cvt_pk_bf16_f32 v124,v22,v23
v_cvt_pk_bf16_f32 v125,v24,v25
buffer_store_dwordx4 v[122:125],v15,s[36:39],s66 offen offset:0 sc0 sc1
buffer_store_dwordx4 v[122:125],v16,s[36:39],s66 offen offset:0 sc0 sc1
s_mov_b32 s72,s69
s_mov_b32 s69,0x7fff0000
s_mov_b32 s66,0x7fff0000
s_mov_b32 s76,0x7fff0000
s_branch Lg1_poll_%=
Lg1_fin0_%=:
v_fma_f32 v18,-v98,v106,v18
v_fma_f32 v19,-v98,v107,v19
v_fma_f32 v20,-v98,v108,v20
v_fma_f32 v21,-v98,v109,v21
v_mul_f32 v114,v18,v18
v_mul_f32 v115,v19,v19
v_mul_f32 v116,v20,v20
v_mul_f32 v117,v21,v21
v_fma_f32 v114,v100,v114,v99
v_fma_f32 v115,v100,v115,v99
v_fma_f32 v116,v100,v116,v99
v_fma_f32 v117,v100,v117,v99
v_mul_f32 v114,v18,v114
v_mul_f32 v115,v19,v115
v_mul_f32 v116,v20,v116
v_mul_f32 v117,v21,v117
v_exp_f32 v114,v114
v_exp_f32 v115,v115
v_exp_f32 v116,v116
v_exp_f32 v117,v117
v_fma_f32 v114,v114,v101,v101
v_fma_f32 v115,v115,v101,v101
v_fma_f32 v116,v116,v101,v101
v_fma_f32 v117,v117,v101,v101
v_rcp_f32 v114,v114
v_rcp_f32 v115,v115
v_rcp_f32 v116,v116
v_rcp_f32 v117,v117
v_mul_f32 v18,v18,v114
v_mul_f32 v19,v19,v115
v_mul_f32 v20,v20,v116
v_mul_f32 v21,v21,v117
v_cvt_pk_bf16_f32 v122,v18,v19
v_cvt_pk_bf16_f32 v123,v20,v21
v_fma_f32 v22,-v98,v110,v22
v_fma_f32 v23,-v98,v111,v23
v_fma_f32 v24,-v98,v112,v24
v_fma_f32 v25,-v98,v113,v25
v_mul_f32 v118,v22,v22
v_mul_f32 v119,v23,v23
v_mul_f32 v120,v24,v24
v_mul_f32 v121,v25,v25
v_fma_f32 v118,v100,v118,v99
v_fma_f32 v119,v100,v119,v99
v_fma_f32 v120,v100,v120,v99
v_fma_f32 v121,v100,v121,v99
v_mul_f32 v118,v22,v118
v_mul_f32 v119,v23,v119
v_mul_f32 v120,v24,v120
v_mul_f32 v121,v25,v121
v_exp_f32 v118,v118
v_exp_f32 v119,v119
v_exp_f32 v120,v120
v_exp_f32 v121,v121
v_fma_f32 v118,v118,v101,v101
v_fma_f32 v119,v119,v101,v101
v_fma_f32 v120,v120,v101,v101
v_fma_f32 v121,v121,v101,v101
v_rcp_f32 v118,v118
v_rcp_f32 v119,v119
v_rcp_f32 v120,v120
v_rcp_f32 v121,v121
v_mul_f32 v22,v22,v118
v_mul_f32 v23,v23,v119
v_mul_f32 v24,v24,v120
v_mul_f32 v25,v25,v121
v_cvt_pk_bf16_f32 v124,v22,v23
v_cvt_pk_bf16_f32 v125,v24,v25
buffer_store_dwordx4 v[122:125],v15,s[36:39],s66 offen offset:0 sc0 sc1
buffer_store_dwordx4 v[122:125],v16,s[36:39],s66 offen offset:0 sc0 sc1
s_mov_b32 s72,s69
s_mov_b32 s69,0x7fff0000
s_mov_b32 s66,0x7fff0000
s_mov_b32 s76,0x7fff0000
s_branch Lg1_done_%=
Lg1_K1_%=:
Lg1_L1_%=:
v_mfma_f32_16x16x32_bf16 v[30:33],a[4:7],v[34:37],0
s_add_u32 m0,s59,3072
s_mov_b32 s67,s65
R 54,2,5120
M 30,a[12:15],38
buffer_load_dword v8,s[36:39],s58 offen offset:0 sc1 lds
v_mfma_f32_16x16x32_bf16 v[26:29],a[0:3],v[34:37],0
s_add_u32 s65,s65,s62
R 58,2,6144
v_fma_f32 v18,-v98,v106,v18
M 26,a[8:11],38
L 3
v_fma_f32 v19,-v98,v107,v19
v_fma_f32 v20,-v98,v108,v20
M 26,a[16:19],42
R 62,2,7168
v_fma_f32 v21,-v98,v109,v21
M 26,a[24:27],46
v_mul_f32 v114,v18,v18
v_mul_f32 v115,v19,v19
M 30,a[20:23],42
R 66,2,8192
v_mul_f32 v116,v20,v20
M 30,a[28:31],46
L 3
v_mul_f32 v117,v21,v21
v_fma_f32 v114,v100,v114,v99
M 30,a[36:39],50
R 70,2,9216
v_fma_f32 v115,v100,v115,v99
M 30,a[44:47],54
v_fma_f32 v116,v100,v116,v99
v_fma_f32 v117,v100,v117,v99
M 26,a[32:35],50
R 74,2,10240
v_mul_f32 v114,v18,v114
M 26,a[40:43],54
L 3
v_exp_f32 v114,v114
M 26,a[48:51],58
R 78,2,11264
v_mul_f32 v115,v19,v115
M 26,a[56:59],62
v_exp_f32 v115,v115
M 30,a[52:55],58
R 82,2,12288
v_mul_f32 v116,v20,v116
M 30,a[60:63],62
L 3
v_exp_f32 v116,v116
M 30,a[68:71],66
R 86,2,13312
v_mul_f32 v117,v21,v117
M 30,a[76:79],70
buffer_load_dwordx4 v[102:105],v11,s[36:39],s73 offen offset:0 sc1
M 26,a[64:67],66
s_add_u32 s73,s73,s64
R 90,2,14336
v_fma_f32 v114,v114,v101,v101
M 26,a[72:75],70
L 3
v_exp_f32 v117,v117
M 26,a[80:83],74
s_waitcnt vmcnt(1)
R 94,2,15360
v_fma_f32 v115,v115,v101,v101
M 26,a[88:91],78
s_barrier
ds_read_b32 v7,v6 offset:3072
v_fma_f32 v116,v116,v101,v101
M 30,a[84:87],74
R 34,2,16384
v_fma_f32 v117,v117,v101,v101
M 30,a[92:95],78
L 4
buffer_store_dword v4,v3,s[36:39],s70 offen offset:0 sc0 sc1
M 30,a[100:103],82
s_mov_b32 s70,0x7fff0000
R 38,2,17408
v_fma_f32 v22,-v98,v110,v22
M 30,a[108:111],86
v_rcp_f32 v114,v114
M 26,a[96:99],82
R 42,2,18432
v_mul_f32 v18,v18,v114
M 26,a[104:107],86
L 4
L 3
v_cmp_eq_u32 vcc,s53,v7
M 26,a[112:115],90
R 46,2,19456
v_fma_f32 v23,-v98,v111,v23
M 26,a[120:123],94
v_rcp_f32 v115,v115
M 30,a[116:119],90
s_cmp_eq_u32 vcc_lo,-1
R 50,2,20480
v_mul_f32 v19,v19,v115
s_cbranch_scc0 Lg1_X1_%=
M 30,a[124:127],94
L 3
s_add_u32 m0,s57,0
s_add_u32 s56,s55,0
M 30,a[132:135],34
s_add_u32 s58,s58,s61
R 54,2,21504
v_cvt_pk_bf16_f32 v122,v18,v19
M 30,a[140:143],38
D 0,56,0
M 26,a[128:131],34
s_add_u32 s54,s54,s46
R 58,2,22528
v_fma_f32 v24,-v98,v112,v24
M 26,a[136:139],38
L 3
v_rcp_f32 v116,v116
M 26,a[144:147],42
R 62,2,23552
v_mul_f32 v20,v20,v116
M 26,a[152:155],46
D 0,56,1024
M 30,a[148:151],42
R 66,2,24576
v_fma_f32 v25,-v98,v113,v25
M 30,a[156:159],46
L 3
v_rcp_f32 v117,v117
M 30,a[164:167],50
R 70,2,25600
v_mul_f32 v21,v21,v117
M 30,a[172:175],54
D 0,56,2048
M 26,a[160:163],50
R 74,2,26624
v_cvt_pk_bf16_f32 v123,v20,v21
M 26,a[168:171],54
L 3
v_mul_f32 v118,v22,v22
v_mul_f32 v119,v23,v23
M 26,a[176:179],58
R 78,2,27648
v_mul_f32 v120,v24,v24
M 26,a[184:187],62
D 0,56,3072
M 30,a[180:183],58
s_add_u32 m0,s59,1024
R 82,2,28672
v_mul_f32 v121,v25,v25
M 30,a[188:191],62
L 3
buffer_load_dword v8,s[36:39],s58 offen offset:0 sc1 lds
M 30,a[196:199],66
s_add_u32 m0,s57,16384
R 86,2,29696
v_fma_f32 v118,v100,v118,v99
M 30,a[204:207],70
s_add_u32 s56,s55,16384
v_fma_f32 v119,v100,v119,v99
v_fma_f32 v120,v100,v120,v99
M 26,a[192:195],66
s_add_u32 s55,s55,s60
R 90,2,30720
v_fma_f32 v121,v100,v121,v99
M 26,a[200:203],70
L 3
D 0,56,0
M 26,a[208:211],74
s_waitcnt vmcnt(1)
R 94,2,31744
v_mul_f32 v118,v22,v118
M 26,a[216:219],78
s_barrier
ds_read_b32 v7,v6 offset:1024
v_mul_f32 v119,v23,v119
M 30,a[212:215],74
s_sub_u32 s57,s84,s57
R 34,1,0
v_mul_f32 v120,v24,v120
M 30,a[220:223],78
L 4
buffer_store_dword v4,v3,s[36:39],s70 offen offset:0 sc0 sc1
v_mul_f32 v121,v25,v121
v_exp_f32 v118,v118
v_exp_f32 v119,v119
v_exp_f32 v120,v120
v_exp_f32 v121,v121
M 30,a[228:231],82
s_mov_b32 s70,s69
R 38,1,1024
v_fma_f32 v118,v118,v101,v101
v_fma_f32 v119,v119,v101,v101
v_fma_f32 v120,v120,v101,v101
v_fma_f32 v121,v121,v101,v101
v_rcp_f32 v118,v118
v_rcp_f32 v119,v119
v_rcp_f32 v120,v120
v_rcp_f32 v121,v121
M 30,a[236:239],86
s_mov_b32 s69,s68
v_mul_f32 v22,v22,v118
v_mul_f32 v23,v23,v119
v_mul_f32 v24,v24,v120
v_mul_f32 v25,v25,v121
v_cvt_pk_bf16_f32 v124,v22,v23
v_cvt_pk_bf16_f32 v125,v24,v25
M 26,a[224:227],82
s_add_u32 s68,s68,s63
R 42,1,2048
buffer_store_dwordx4 v[122:125],v15,s[36:39],s66 offen offset:0 sc0 sc1
M 26,a[232:235],86
L 4
L 3
M 26,a[240:243],90
R 46,1,3072
v_cmp_eq_u32 vcc,s53,v7
M 26,a[248:251],94
D 0,56,1024
M 30,a[244:247],90
L 2
s_cmp_eq_u32 vcc_lo,-1
R 50,1,4096
buffer_store_dwordx4 v[122:125],v16,s[36:39],s66 offen offset:0 sc0 sc1
M 30,a[252:255],94
D 0,56,2048
D 0,56,3072
buffer_store_dword v4,v3,s[36:39],s71 offen offset:0 sc0 sc1
buffer_store_dword v4,v3,s[36:39],s71 offen offset:0 sc0 sc1
buffer_store_dword v4,v3,s[36:39],s71 offen offset:0 sc0 sc1
s_cbranch_scc1 Lg1_F0_%=
s_branch Lg1_K0_%=
Lg1_X1_%=:
M 30,a[124:127],94
L 3
s_add_u32 m0,s59,1024
v_cvt_pk_bf16_f32 v122,v18,v19
M 30,a[132:135],34
R 54,2,21504
v_fma_f32 v24,-v98,v112,v24
M 30,a[140:143],38
buffer_load_dword v8,s[36:39],s58 offen offset:0 sc1 lds
M 26,a[128:131],34
R 58,2,22528
v_fma_f32 v25,-v98,v113,v25
M 26,a[136:139],38
L 3
v_rcp_f32 v116,v116
M 26,a[144:147],42
R 62,2,23552
v_mul_f32 v20,v20,v116
M 26,a[152:155],46
v_rcp_f32 v117,v117
M 30,a[148:151],42
R 66,2,24576
v_mul_f32 v21,v21,v117
M 30,a[156:159],46
L 3
v_cvt_pk_bf16_f32 v123,v20,v21
v_mul_f32 v118,v22,v22
M 30,a[164:167],50
R 70,2,25600
v_mul_f32 v119,v23,v23
M 30,a[172:175],54
v_mul_f32 v120,v24,v24
v_mul_f32 v121,v25,v25
M 26,a[160:163],50
R 74,2,26624
v_fma_f32 v118,v100,v118,v99
M 26,a[168:171],54
L 3
v_fma_f32 v119,v100,v119,v99
v_fma_f32 v120,v100,v120,v99
M 26,a[176:179],58
R 78,2,27648
v_fma_f32 v121,v100,v121,v99
M 26,a[184:187],62
v_mul_f32 v118,v22,v118
v_mul_f32 v119,v23,v119
M 30,a[180:183],58
R 82,2,28672
v_mul_f32 v120,v24,v120
M 30,a[188:191],62
L 3
v_exp_f32 v118,v118
M 30,a[196:199],66
R 86,2,29696
v_mul_f32 v121,v25,v121
M 30,a[204:207],70
v_exp_f32 v119,v119
M 26,a[192:195],66
R 90,2,30720
v_fma_f32 v118,v118,v101,v101
M 26,a[200:203],70
L 3
v_exp_f32 v120,v120
M 26,a[208:211],74
s_waitcnt vmcnt(0)
R 94,2,31744
v_fma_f32 v119,v119,v101,v101
M 26,a[216:219],78
s_barrier
buffer_store_dword v4,v3,s[36:39],s70 offen offset:0 sc0 sc1
M 30,a[212:215],74
s_mov_b32 s70,s69
ds_read_b32 v7,v6 offset:1024
v_fma_f32 v120,v120,v101,v101
M 30,a[220:223],78
L 3
v_exp_f32 v121,v121
M 30,a[228:231],82
s_mov_b32 s69,s68
v_fma_f32 v121,v121,v101,v101
v_rcp_f32 v118,v118
v_rcp_f32 v119,v119
v_rcp_f32 v120,v120
v_rcp_f32 v121,v121
M 30,a[236:239],86
s_add_u32 s68,s68,s63
v_mul_f32 v22,v22,v118
v_mul_f32 v23,v23,v119
v_mul_f32 v24,v24,v120
v_mul_f32 v25,v25,v121
v_cvt_pk_bf16_f32 v124,v22,v23
v_cvt_pk_bf16_f32 v125,v24,v25
M 26,a[224:227],82
buffer_store_dwordx4 v[122:125],v15,s[36:39],s66 offen offset:0 sc0 sc1
M 26,a[232:235],86
L 1
M 26,a[240:243],90
L 0
v_cmp_eq_u32 vcc,s53,v7
M 26,a[248:251],94
buffer_store_dwordx4 v[122:125],v16,s[36:39],s66 offen offset:0 sc0 sc1
M 30,a[244:247],90
s_nop 0
M 30,a[252:255],94
s_mov_b32 s80,0
s_cmp_eq_u32 vcc_lo,-1
s_cbranch_scc0 Lg1_xn1_%=
s_add_u32 m0,s57,0
s_add_u32 s56,s55,0
D 0,56,0
v_fma_f32 v26,-v102,v106,v26
v_fma_f32 v27,-v102,v107,v27
v_fma_f32 v28,-v102,v108,v28
v_fma_f32 v29,-v102,v109,v29
v_mul_f32 v114,v26,v26
v_mul_f32 v115,v27,v27
v_mul_f32 v116,v28,v28
D 0,56,1024
v_mul_f32 v117,v29,v29
v_fma_f32 v114,v104,v114,v103
v_fma_f32 v115,v104,v115,v103
v_fma_f32 v116,v104,v116,v103
v_fma_f32 v117,v104,v117,v103
v_mul_f32 v114,v26,v114
v_mul_f32 v115,v27,v115
D 0,56,2048
)"
R"(v_mul_f32 v116,v28,v116
v_mul_f32 v117,v29,v117
v_exp_f32 v114,v114
v_exp_f32 v115,v115
v_exp_f32 v116,v116
v_exp_f32 v117,v117
v_fma_f32 v114,v114,v105,v105
D 0,56,3072
v_fma_f32 v115,v115,v105,v105
v_fma_f32 v116,v116,v105,v105
v_fma_f32 v117,v117,v105,v105
v_rcp_f32 v114,v114
v_rcp_f32 v115,v115
v_rcp_f32 v116,v116
v_rcp_f32 v117,v117
s_add_u32 s58,s58,s61
s_add_u32 m0,s57,16384
s_add_u32 s56,s55,16384
D 0,56,0
v_mul_f32 v26,v26,v114
v_mul_f32 v27,v27,v115
v_mul_f32 v28,v28,v116
v_mul_f32 v29,v29,v117
v_cvt_pk_bf16_f32 v122,v26,v27
v_cvt_pk_bf16_f32 v123,v28,v29
v_fma_f32 v30,-v102,v110,v30
D 0,56,1024
v_fma_f32 v31,-v102,v111,v31
v_fma_f32 v32,-v102,v112,v32
v_fma_f32 v33,-v102,v113,v33
v_mul_f32 v118,v30,v30
v_mul_f32 v119,v31,v31
v_mul_f32 v120,v32,v32
v_mul_f32 v121,v33,v33
D 0,56,2048
v_fma_f32 v118,v104,v118,v103
v_fma_f32 v119,v104,v119,v103
v_fma_f32 v120,v104,v120,v103
v_fma_f32 v121,v104,v121,v103
v_mul_f32 v118,v30,v118
v_mul_f32 v119,v31,v119
v_mul_f32 v120,v32,v120
D 0,56,3072
v_mul_f32 v121,v33,v121
v_exp_f32 v118,v118
v_exp_f32 v119,v119
v_exp_f32 v120,v120
v_exp_f32 v121,v121
v_fma_f32 v118,v118,v105,v105
v_fma_f32 v119,v119,v105,v105
s_add_u32 s55,s55,s60
s_sub_u32 s57,s84,s57
s_add_u32 s54,s54,s46
v_fma_f32 v120,v120,v105,v105
v_fma_f32 v121,v121,v105,v105
v_rcp_f32 v118,v118
v_rcp_f32 v119,v119
v_rcp_f32 v120,v120
v_rcp_f32 v121,v121
v_mul_f32 v30,v30,v118
v_mul_f32 v31,v31,v119
v_mul_f32 v32,v32,v120
v_mul_f32 v33,v33,v121
v_cvt_pk_bf16_f32 v124,v30,v31
v_cvt_pk_bf16_f32 v125,v32,v33
buffer_store_dwordx4 v[122:125],v15,s[36:39],s67 offen offset:0 sc0 sc1
buffer_store_dwordx4 v[122:125],v16,s[36:39],s67 offen offset:0 sc0 sc1
s_mov_b32 s72,s69
s_mov_b32 s69,0x7fff0000
s_mov_b32 s67,0x7fff0000
s_mov_b32 s77,0x7fff0000
s_branch Lg1_gox_%=
Lg1_xn1_%=:
s_cmp_ge_u32 s54,256
s_cbranch_scc1 Lg1_fin1_%=
s_add_u32 m0,s59,4096
s_nop 0
buffer_load_dword v8,s[36:39],s58 offen offset:0 sc1 lds
buffer_store_dword v4,v3,s[36:39],s71 offen offset:0 sc0 sc1
s_add_u32 m0,s59,5120
s_nop 0
buffer_load_dword v8,s[36:39],s58 offen offset:0 sc1 lds
buffer_store_dword v4,v3,s[36:39],s71 offen offset:0 sc0 sc1
s_mov_b32 s83,0
v_fma_f32 v26,-v102,v106,v26
v_fma_f32 v27,-v102,v107,v27
v_fma_f32 v28,-v102,v108,v28
v_fma_f32 v29,-v102,v109,v29
v_mul_f32 v114,v26,v26
v_mul_f32 v115,v27,v27
v_mul_f32 v116,v28,v28
v_mul_f32 v117,v29,v29
v_fma_f32 v114,v104,v114,v103
v_fma_f32 v115,v104,v115,v103
v_fma_f32 v116,v104,v116,v103
v_fma_f32 v117,v104,v117,v103
v_mul_f32 v114,v26,v114
v_mul_f32 v115,v27,v115
v_mul_f32 v116,v28,v116
v_mul_f32 v117,v29,v117
v_exp_f32 v114,v114
v_exp_f32 v115,v115
v_exp_f32 v116,v116
v_exp_f32 v117,v117
v_fma_f32 v114,v114,v105,v105
v_fma_f32 v115,v115,v105,v105
v_fma_f32 v116,v116,v105,v105
v_fma_f32 v117,v117,v105,v105
v_rcp_f32 v114,v114
v_rcp_f32 v115,v115
v_rcp_f32 v116,v116
v_rcp_f32 v117,v117
v_mul_f32 v26,v26,v114
v_mul_f32 v27,v27,v115
v_mul_f32 v28,v28,v116
v_mul_f32 v29,v29,v117
v_cvt_pk_bf16_f32 v122,v26,v27
v_cvt_pk_bf16_f32 v123,v28,v29
v_fma_f32 v30,-v102,v110,v30
v_fma_f32 v31,-v102,v111,v31
v_fma_f32 v32,-v102,v112,v32
v_fma_f32 v33,-v102,v113,v33
v_mul_f32 v118,v30,v30
v_mul_f32 v119,v31,v31
v_mul_f32 v120,v32,v32
v_mul_f32 v121,v33,v33
v_fma_f32 v118,v104,v118,v103
v_fma_f32 v119,v104,v119,v103
v_fma_f32 v120,v104,v120,v103
v_fma_f32 v121,v104,v121,v103
v_mul_f32 v118,v30,v118
v_mul_f32 v119,v31,v119
v_mul_f32 v120,v32,v120
v_mul_f32 v121,v33,v121
v_exp_f32 v118,v118
v_exp_f32 v119,v119
v_exp_f32 v120,v120
v_exp_f32 v121,v121
v_fma_f32 v118,v118,v105,v105
v_fma_f32 v119,v119,v105,v105
v_fma_f32 v120,v120,v105,v105
v_fma_f32 v121,v121,v105,v105
v_rcp_f32 v118,v118
v_rcp_f32 v119,v119
v_rcp_f32 v120,v120
v_rcp_f32 v121,v121
v_mul_f32 v30,v30,v118
v_mul_f32 v31,v31,v119
v_mul_f32 v32,v32,v120
v_mul_f32 v33,v33,v121
v_cvt_pk_bf16_f32 v124,v30,v31
v_cvt_pk_bf16_f32 v125,v32,v33
buffer_store_dwordx4 v[122:125],v15,s[36:39],s67 offen offset:0 sc0 sc1
buffer_store_dwordx4 v[122:125],v16,s[36:39],s67 offen offset:0 sc0 sc1
s_mov_b32 s72,s69
s_mov_b32 s69,0x7fff0000
s_mov_b32 s67,0x7fff0000
s_mov_b32 s77,0x7fff0000
s_branch Lg1_poll_%=
Lg1_fin1_%=:
v_fma_f32 v26,-v102,v106,v26
v_fma_f32 v27,-v102,v107,v27
v_fma_f32 v28,-v102,v108,v28
v_fma_f32 v29,-v102,v109,v29
v_mul_f32 v114,v26,v26
v_mul_f32 v115,v27,v27
v_mul_f32 v116,v28,v28
v_mul_f32 v117,v29,v29
v_fma_f32 v114,v104,v114,v103
v_fma_f32 v115,v104,v115,v103
v_fma_f32 v116,v104,v116,v103
v_fma_f32 v117,v104,v117,v103
v_mul_f32 v114,v26,v114
v_mul_f32 v115,v27,v115
v_mul_f32 v116,v28,v116
v_mul_f32 v117,v29,v117
v_exp_f32 v114,v114
v_exp_f32 v115,v115
v_exp_f32 v116,v116
v_exp_f32 v117,v117
v_fma_f32 v114,v114,v105,v105
v_fma_f32 v115,v115,v105,v105
v_fma_f32 v116,v116,v105,v105
v_fma_f32 v117,v117,v105,v105
v_rcp_f32 v114,v114
v_rcp_f32 v115,v115
v_rcp_f32 v116,v116
v_rcp_f32 v117,v117
v_mul_f32 v26,v26,v114
v_mul_f32 v27,v27,v115
v_mul_f32 v28,v28,v116
v_mul_f32 v29,v29,v117
v_cvt_pk_bf16_f32 v122,v26,v27
v_cvt_pk_bf16_f32 v123,v28,v29
v_fma_f32 v30,-v102,v110,v30
v_fma_f32 v31,-v102,v111,v31
v_fma_f32 v32,-v102,v112,v32
v_fma_f32 v33,-v102,v113,v33
v_mul_f32 v118,v30,v30
v_mul_f32 v119,v31,v31
v_mul_f32 v120,v32,v32
v_mul_f32 v121,v33,v33
v_fma_f32 v118,v104,v118,v103
v_fma_f32 v119,v104,v119,v103
v_fma_f32 v120,v104,v120,v103
v_fma_f32 v121,v104,v121,v103
v_mul_f32 v118,v30,v118
v_mul_f32 v119,v31,v119
v_mul_f32 v120,v32,v120
v_mul_f32 v121,v33,v121
v_exp_f32 v118,v118
v_exp_f32 v119,v119
v_exp_f32 v120,v120
v_exp_f32 v121,v121
v_fma_f32 v118,v118,v105,v105
v_fma_f32 v119,v119,v105,v105
v_fma_f32 v120,v120,v105,v105
v_fma_f32 v121,v121,v105,v105
v_rcp_f32 v118,v118
v_rcp_f32 v119,v119
v_rcp_f32 v120,v120
v_rcp_f32 v121,v121
v_mul_f32 v30,v30,v118
v_mul_f32 v31,v31,v119
v_mul_f32 v32,v32,v120
v_mul_f32 v33,v33,v121
v_cvt_pk_bf16_f32 v124,v30,v31
v_cvt_pk_bf16_f32 v125,v32,v33
buffer_store_dwordx4 v[122:125],v15,s[36:39],s67 offen offset:0 sc0 sc1
buffer_store_dwordx4 v[122:125],v16,s[36:39],s67 offen offset:0 sc0 sc1
s_mov_b32 s72,s69
s_mov_b32 s69,0x7fff0000
s_mov_b32 s67,0x7fff0000
s_mov_b32 s77,0x7fff0000
s_branch Lg1_done_%=
Lg1_done_%=:
s_waitcnt vmcnt(2)
s_barrier
buffer_store_dword v4,v3,s[36:39],s70 offen offset:0 sc0 sc1
s_waitcnt vmcnt(1)
s_barrier
buffer_store_dword v4,v3,s[36:39],s72 offen offset:0 sc0 sc1
s_waitcnt vmcnt(0) lgkmcnt(0)
s_barrier
s_branch Lg2_end_%=
Lg2_begin_%=:
Lcode_g2_b:
s_mov_b64 s[36:37],%1
s_mov_b32 s38,111243392
s_mov_b32 s39,0x00020000
s_mov_b32 s44,%2
s_mov_b32 s45,%3
s_mov_b32 s46,%4
s_mov_b32 s47,%5
s_mov_b32 s48,%6
s_mov_b32 s49,%7
s_mov_b32 s50,%8
s_mov_b32 s51,%9
s_mov_b32 s53,%10
v_mov_b32 v0,%11
v_mov_b32 v3,%12
v_mov_b32 v5,%13
v_mov_b32 v6,%14
v_mov_b32 v18,%15
v_mov_b32 v19,%16
v_mov_b32 v20,%17
v_mov_b32 v21,%18
v_mov_b32 v9,%19
v_mov_b32 v10,%20
v_mov_b32 v11,%21
v_mov_b32 v12,%22
v_mov_b32 v13,%23
v_mov_b32 v14,%24
v_mov_b32 v15,%25
v_mov_b32 v16,%26
s_mov_b64 s[40:41],%27
s_mov_b32 s42,2097152
s_mov_b32 s43,0x00020000
s_lshl_b32 s57,s44,11
s_cmp_ge_u32 s45,8
s_cbranch_scc1 Lg2_dly_%=
s_sleep 16
s_sleep 16
s_sleep 16
s_sleep 16
Lg2_dly_%=:
s_add_u32 s55,s50,0
s_add_u32 m0,s57,0
s_nop 0
buffer_load_dwordx4 v18,s[40:43],s55 offen offset:0  lds
s_add_u32 m0,s57,1024
s_nop 0
buffer_load_dwordx4 v19,s[40:43],s55 offen offset:0  lds
s_add_u32 m0,s57,8064
s_nop 0
buffer_load_dwordx4 v18,s[40:43],s55 offen offset:128  lds
s_add_u32 m0,s57,9088
s_nop 0
buffer_load_dwordx4 v19,s[40:43],s55 offen offset:128  lds
s_add_u32 s55,s50,131072
s_add_u32 m0,s57,16384
s_nop 0
buffer_load_dwordx4 v18,s[40:43],s55 offen offset:0  lds
s_add_u32 m0,s57,17408
s_nop 0
buffer_load_dwordx4 v19,s[40:43],s55 offen offset:0  lds
s_add_u32 m0,s57,24448
s_nop 0
buffer_load_dwordx4 v18,s[40:43],s55 offen offset:128  lds
s_add_u32 m0,s57,25472
s_nop 0
buffer_load_dwordx4 v19,s[40:43],s55 offen offset:128  lds
s_add_u32 s55,s50,262144
s_add_u32 m0,s57,32768
s_nop 0
buffer_load_dwordx4 v18,s[40:43],s55 offen offset:0  lds
s_add_u32 m0,s57,33792
s_nop 0
buffer_load_dwordx4 v19,s[40:43],s55 offen offset:0  lds
s_add_u32 m0,s57,40832
s_nop 0
buffer_load_dwordx4 v18,s[40:43],s55 offen offset:128  lds
s_add_u32 m0,s57,41856
s_nop 0
buffer_load_dwordx4 v19,s[40:43],s55 offen offset:128  lds
s_add_u32 s55,s50,393216
s_add_u32 m0,s57,49152
s_nop 0
buffer_load_dwordx4 v18,s[40:43],s55 offen offset:0  lds
s_add_u32 m0,s57,50176
s_nop 0
buffer_load_dwordx4 v19,s[40:43],s55 offen offset:0  lds
s_add_u32 m0,s57,57216
s_nop 0
buffer_load_dwordx4 v18,s[40:43],s55 offen offset:128  lds
s_add_u32 m0,s57,58240
s_nop 0
buffer_load_dwordx4 v19,s[40:43],s55 offen offset:128  lds
s_add_u32 s55,s50,524288
s_add_u32 m0,s57,65536
s_nop 0
buffer_load_dwordx4 v18,s[40:43],s55 offen offset:0  lds
s_add_u32 m0,s57,66560
s_nop 0
buffer_load_dwordx4 v19,s[40:43],s55 offen offset:0  lds
s_add_u32 m0,s57,73600
s_nop 0
buffer_load_dwordx4 v18,s[40:43],s55 offen offset:128  lds
s_add_u32 m0,s57,74624
s_nop 0
buffer_load_dwordx4 v19,s[40:43],s55 offen offset:128  lds
s_add_u32 s55,s50,655360
s_add_u32 m0,s57,81920
s_nop 0
buffer_load_dwordx4 v18,s[40:43],s55 offen offset:0  lds
s_add_u32 m0,s57,82944
s_nop 0
buffer_load_dwordx4 v19,s[40:43],s55 offen offset:0  lds
s_add_u32 m0,s57,89984
s_nop 0
buffer_load_dwordx4 v18,s[40:43],s55 offen offset:128  lds
s_add_u32 m0,s57,91008
s_nop 0
buffer_load_dwordx4 v19,s[40:43],s55 offen offset:128  lds
s_waitcnt vmcnt(20) lgkmcnt(0)
s_barrier
s_add_u32 s55,s50,786432
s_add_u32 m0,s57,98304
s_nop 0
buffer_load_dwordx4 v18,s[40:43],s55 offen offset:0  lds
s_add_u32 m0,s57,99328
s_nop 0
buffer_load_dwordx4 v19,s[40:43],s55 offen offset:0  lds
s_add_u32 m0,s57,106368
s_nop 0
buffer_load_dwordx4 v18,s[40:43],s55 offen offset:128  lds
s_add_u32 m0,s57,107392
s_nop 0
buffer_load_dwordx4 v19,s[40:43],s55 offen offset:128  lds
v_add_u32 v26,0,v20
v_add_u32 v27,0,v21
T a[0:1],26,0
T a[2:3],26,512
T a[4:5],27,0
T a[6:7],27,512
T a[8:9],26,4096
T a[10:11],26,4608
T a[12:13],27,4096
T a[14:15],27,4608
s_waitcnt vmcnt(20) lgkmcnt(0)
s_barrier
s_add_u32 s55,s50,917504
s_add_u32 m0,s57,0
s_nop 0
buffer_load_dwordx4 v18,s[40:43],s55 offen offset:0  lds
s_add_u32 m0,s57,1024
s_nop 0
buffer_load_dwordx4 v19,s[40:43],s55 offen offset:0  lds
s_add_u32 m0,s57,8064
s_nop 0
buffer_load_dwordx4 v18,s[40:43],s55 offen offset:128  lds
s_add_u32 m0,s57,9088
s_nop 0
buffer_load_dwordx4 v19,s[40:43],s55 offen offset:128  lds
v_add_u32 v26,16384,v20
v_add_u32 v27,16384,v21
T a[16:17],26,0
T a[18:19],26,512
T a[20:21],27,0
T a[22:23],27,512
T a[24:25],26,4096
T a[26:27],26,4608
T a[28:29],27,4096
T a[30:31],27,4608
s_waitcnt vmcnt(20) lgkmcnt(0)
s_barrier
s_add_u32 s55,s50,1048576
s_add_u32 m0,s57,16384
s_nop 0
buffer_load_dwordx4 v18,s[40:43],s55 offen offset:0  lds
s_add_u32 m0,s57,17408
s_nop 0
buffer_load_dwordx4 v19,s[40:43],s55 offen offset:0  lds
s_add_u32 m0,s57,24448
s_nop 0
buffer_load_dwordx4 v18,s[40:43],s55 offen offset:128  lds
s_add_u32 m0,s57,25472
s_nop 0
buffer_load_dwordx4 v19,s[40:43],s55 offen offset:128  lds
v_add_u32 v26,32768,v20
v_add_u32 v27,32768,v21
T a[32:33],26,0
T a[34:35],26,512
T a[36:37],27,0
T a[38:39],27,512
T a[40:41],26,4096
T a[42:43],26,4608
T a[44:45],27,4096
T a[46:47],27,4608
s_waitcnt vmcnt(20) lgkmcnt(0)
s_barrier
s_add_u32 s55,s50,1179648
s_add_u32 m0,s57,32768
s_nop 0
buffer_load_dwordx4 v18,s[40:43],s55 offen offset:0  lds
s_add_u32 m0,s57,33792
s_nop 0
buffer_load_dwordx4 v19,s[40:43],s55 offen offset:0  lds
s_add_u32 m0,s57,40832
s_nop 0
buffer_load_dwordx4 v18,s[40:43],s55 offen offset:128  lds
s_add_u32 m0,s57,41856
s_nop 0
buffer_load_dwordx4 v19,s[40:43],s55 offen offset:128  lds
v_add_u32 v26,49152,v20
v_add_u32 v27,49152,v21
T a[48:49],26,0
T a[50:51],26,512
T a[52:53],27,0
T a[54:55],27,512
T a[56:57],26,4096
T a[58:59],26,4608
T a[60:61],27,4096
T a[62:63],27,4608
s_waitcnt vmcnt(20) lgkmcnt(0)
s_barrier
s_add_u32 s55,s50,1310720
s_add_u32 m0,s57,49152
s_nop 0
buffer_load_dwordx4 v18,s[40:43],s55 offen offset:0  lds
s_add_u32 m0,s57,50176
s_nop 0
buffer_load_dwordx4 v19,s[40:43],s55 offen offset:0  lds
s_add_u32 m0,s57,57216
s_nop 0
buffer_load_dwordx4 v18,s[40:43],s55 offen offset:128  lds
s_add_u32 m0,s57,58240
s_nop 0
buffer_load_dwordx4 v19,s[40:43],s55 offen offset:128  lds
v_add_u32 v26,65536,v20
v_add_u32 v27,65536,v21
T a[64:65],26,0
T a[66:67],26,512
T a[68:69],27,0
T a[70:71],27,512
T a[72:73],26,4096
T a[74:75],26,4608
T a[76:77],27,4096
T a[78:79],27,4608
s_waitcnt vmcnt(20) lgkmcnt(0)
s_barrier
s_add_u32 s55,s50,1441792
s_add_u32 m0,s57,65536
s_nop 0
buffer_load_dwordx4 v18,s[40:43],s55 offen offset:0  lds
s_add_u32 m0,s57,66560
s_nop 0
buffer_load_dwordx4 v19,s[40:43],s55 offen offset:0  lds
s_add_u32 m0,s57,73600
s_nop 0
)"
R"(buffer_load_dwordx4 v18,s[40:43],s55 offen offset:128  lds
s_add_u32 m0,s57,74624
s_nop 0
buffer_load_dwordx4 v19,s[40:43],s55 offen offset:128  lds
v_add_u32 v26,81920,v20
v_add_u32 v27,81920,v21
T a[80:81],26,0
T a[82:83],26,512
T a[84:85],27,0
T a[86:87],27,512
T a[88:89],26,4096
T a[90:91],26,4608
T a[92:93],27,4096
T a[94:95],27,4608
s_waitcnt vmcnt(20) lgkmcnt(0)
s_barrier
s_add_u32 s55,s50,1572864
s_add_u32 m0,s57,81920
s_nop 0
buffer_load_dwordx4 v18,s[40:43],s55 offen offset:0  lds
s_add_u32 m0,s57,82944
s_nop 0
buffer_load_dwordx4 v19,s[40:43],s55 offen offset:0  lds
s_add_u32 m0,s57,89984
s_nop 0
buffer_load_dwordx4 v18,s[40:43],s55 offen offset:128  lds
s_add_u32 m0,s57,91008
s_nop 0
buffer_load_dwordx4 v19,s[40:43],s55 offen offset:128  lds
v_add_u32 v26,98304,v20
v_add_u32 v27,98304,v21
T a[96:97],26,0
T a[98:99],26,512
T a[100:101],27,0
T a[102:103],27,512
T a[104:105],26,4096
T a[106:107],26,4608
T a[108:109],27,4096
T a[110:111],27,4608
s_waitcnt vmcnt(20) lgkmcnt(0)
s_barrier
s_add_u32 s55,s50,1703936
s_add_u32 m0,s57,98304
s_nop 0
buffer_load_dwordx4 v18,s[40:43],s55 offen offset:0  lds
s_add_u32 m0,s57,99328
s_nop 0
buffer_load_dwordx4 v19,s[40:43],s55 offen offset:0  lds
s_add_u32 m0,s57,106368
s_nop 0
buffer_load_dwordx4 v18,s[40:43],s55 offen offset:128  lds
s_add_u32 m0,s57,107392
s_nop 0
buffer_load_dwordx4 v19,s[40:43],s55 offen offset:128  lds
v_add_u32 v26,0,v20
v_add_u32 v27,0,v21
T a[112:113],26,0
T a[114:115],26,512
T a[116:117],27,0
T a[118:119],27,512
T a[120:121],26,4096
T a[122:123],26,4608
T a[124:125],27,4096
T a[126:127],27,4608
s_waitcnt vmcnt(20) lgkmcnt(0)
s_barrier
s_add_u32 s55,s50,1835008
s_add_u32 m0,s57,0
s_nop 0
buffer_load_dwordx4 v18,s[40:43],s55 offen offset:0  lds
s_add_u32 m0,s57,1024
s_nop 0
buffer_load_dwordx4 v19,s[40:43],s55 offen offset:0  lds
s_add_u32 m0,s57,8064
s_nop 0
buffer_load_dwordx4 v18,s[40:43],s55 offen offset:128  lds
s_add_u32 m0,s57,9088
s_nop 0
buffer_load_dwordx4 v19,s[40:43],s55 offen offset:128  lds
v_add_u32 v26,16384,v20
v_add_u32 v27,16384,v21
T a[128:129],26,0
T a[130:131],26,512
T a[132:133],27,0
T a[134:135],27,512
T a[136:137],26,4096
T a[138:139],26,4608
T a[140:141],27,4096
T a[142:143],27,4608
s_waitcnt vmcnt(20) lgkmcnt(0)
s_barrier
s_add_u32 s55,s50,1966080
s_add_u32 m0,s57,16384
s_nop 0
buffer_load_dwordx4 v18,s[40:43],s55 offen offset:0  lds
s_add_u32 m0,s57,17408
s_nop 0
buffer_load_dwordx4 v19,s[40:43],s55 offen offset:0  lds
s_add_u32 m0,s57,24448
s_nop 0
buffer_load_dwordx4 v18,s[40:43],s55 offen offset:128  lds
s_add_u32 m0,s57,25472
s_nop 0
buffer_load_dwordx4 v19,s[40:43],s55 offen offset:128  lds
v_add_u32 v26,32768,v20
v_add_u32 v27,32768,v21
T a[144:145],26,0
T a[146:147],26,512
T a[148:149],27,0
T a[150:151],27,512
T a[152:153],26,4096
T a[154:155],26,4608
T a[156:157],27,4096
T a[158:159],27,4608
s_waitcnt vmcnt(20) lgkmcnt(0)
s_barrier
v_add_u32 v26,49152,v20
v_add_u32 v27,49152,v21
T a[160:161],26,0
T a[162:163],26,512
T a[164:165],27,0
T a[166:167],27,512
T a[168:169],26,4096
T a[170:171],26,4608
T a[172:173],27,4096
T a[174:175],27,4608
s_waitcnt vmcnt(16) lgkmcnt(0)
s_barrier
v_add_u32 v26,65536,v20
v_add_u32 v27,65536,v21
T a[176:177],26,0
T a[178:179],26,512
T a[180:181],27,0
T a[182:183],27,512
T a[184:185],26,4096
T a[186:187],26,4608
T a[188:189],27,4096
T a[190:191],27,4608
s_waitcnt vmcnt(12) lgkmcnt(0)
s_barrier
v_add_u32 v26,81920,v20
v_add_u32 v27,81920,v21
T a[192:193],26,0
T a[194:195],26,512
T a[196:197],27,0
T a[198:199],27,512
T a[200:201],26,4096
T a[202:203],26,4608
T a[204:205],27,4096
T a[206:207],27,4608
s_waitcnt vmcnt(8) lgkmcnt(0)
s_barrier
v_add_u32 v26,98304,v20
v_add_u32 v27,98304,v21
T a[208:209],26,0
T a[210:211],26,512
T a[212:213],27,0
T a[214:215],27,512
T a[216:217],26,4096
T a[218:219],26,4608
T a[220:221],27,4096
T a[222:223],27,4608
s_waitcnt vmcnt(4) lgkmcnt(0)
s_barrier
v_add_u32 v26,0,v20
v_add_u32 v27,0,v21
T a[224:225],26,0
T a[226:227],26,512
T a[228:229],27,0
T a[230:231],27,512
T a[232:233],26,4096
T a[234:235],26,4608
T a[236:237],27,4096
T a[238:239],27,4608
s_waitcnt vmcnt(0) lgkmcnt(0)
s_barrier
v_add_u32 v26,16384,v20
v_add_u32 v27,16384,v21
T a[240:241],26,0
T a[242:243],26,512
T a[244:245],27,0
T a[246:247],27,512
T a[248:249],26,4096
T a[250:251],26,4608
T a[252:253],27,4096
T a[254:255],27,4608
L 0
s_barrier
v_mov_b32 v4,s53
s_mov_b32 s82,0
s_mov_b32 s80,0
s_mov_b32 s54,s45
s_mul_i32 s57,s44,5120
s_mul_i32 s55,s45,40960
s_add_u32 s55,s55,s57
s_add_u32 s55,s55,17825792
s_mul_i32 s57,s44,4096
s_mul_i32 s60,s46,40960
s_mul_i32 s61,s46,1024
s_mul_i32 s62,s46,32768
s_mul_i32 s63,s46,1024
s_mul_i32 s64,s46,512
s_lshl_b32 s59,s44,8
s_add_u32 s59,s59,131072
s_mul_i32 s58,s45,1024
s_add_u32 s58,s58,109333504
s_mul_i32 s65,s45,32768
s_add_u32 s65,s65,s47
s_mul_i32 s68,s45,1024
s_add_u32 s68,s68,s48
s_mul_i32 s73,s45,512
s_add_u32 s73,s73,106954752
s_mul_i32 s74,s45,32768
s_add_u32 s74,s74,s49
s_mul_i32 s75,s45,1024
s_add_u32 s75,s75,107479040
s_mov_b32 s66,0x7fff0000
s_mov_b32 s67,0x7fff0000
s_mov_b32 s69,0x7fff0000
s_mov_b32 s76,0x7fff0000
s_mov_b32 s77,0x7fff0000
s_mov_b32 s70,0x7fff0000
s_mov_b32 s71,0x7fff0000
s_mov_b32 s72,0x7fff0000
v_mov_b32 v1,v0
v_add_u32 v2,16384,v0
s_mov_b32 s84,16384
s_mov_b32 s86,0x3a800000
s_mov_b32 s87,0x38a7c5ac
s_mov_b32 s88,0xc0135761
s_mov_b32 s89,0x3d372713
s_lshr_b32 s85,s44,1
s_mul_i32 s85,s85,127
v_lshlrev_b32 v8,22,v0
v_and_b32 v8,0x80000000,v8
v_or_b32 v8,v8,v5
v_add_u32 v2,32768,v0
s_lshl_b32 s84,s57,1
s_add_u32 s84,s84,32768
s_getpc_b64 s[40:41]
Lg2_pc_%=:
s_lshl_b32 s78,s44,13
v_lshlrev_b32 v106,3,v0
v_add_u32 v106,s78,v106
v_min_u32 v106,Lcode_g12_e-Lg2_pc_%=-4,v106
s_mov_b32 m0,163584
s_nop 0
global_load_lds_dword v106,s[40:41]
s_and_b32 s79,s54,127
s_cmp_eq_u32 s79,0
s_cselect_b32 s94,0x3300,0
s_cmp_eq_u32 s79,127
s_cselect_b32 s94,0xcc00,s94
s_add_u32 m0,s59,4096
s_nop 0
buffer_load_dword v8,s[36:39],s58 offen offset:0 sc1 lds
buffer_store_dword v4,v3,s[36:39],s71 offen offset:0 sc0 sc1
s_add_u32 m0,s59,5120
s_nop 0
buffer_load_dword v8,s[36:39],s58 offen offset:0 sc1 lds
buffer_store_dword v4,v3,s[36:39],s71 offen offset:0 sc0 sc1
s_mov_b32 s83,0
Lg2_poll_%=:
s_add_u32 s78,s83,2
s_and_b32 s78,s78,3
s_lshl_b32 s78,s78,10
s_add_u32 s78,s78,4096
s_add_u32 m0,s59,s78
s_nop 0
buffer_load_dword v8,s[36:39],s58 offen offset:0 sc1 lds
s_and_b32 s78,s83,3
s_lshl_b32 s78,s78,10
v_add_u32 v106,s78,v6
s_waitcnt vmcnt(4)
s_barrier
ds_read_b32 v7,v106 offset:4096
buffer_store_dword v4,v3,s[36:39],s70 offen offset:0 sc0 sc1
s_mov_b32 s70,s71
s_mov_b32 s71,s72
s_mov_b32 s72,0x7fff0000
s_add_u32 s83,s83,1
s_add_u32 s82,s82,1
L 0
v_cmp_eq_u32 vcc,s53,v7
s_nop 0
s_or_b64 vcc,vcc,s[94:95]
s_cmp_eq_u32 vcc_lo,-1
s_cbranch_scc1 Lg2_go_%=
s_cmp_lt_u32 s82,20000
s_cbranch_scc1 Lg2_poll_%=
Lg2_go_%=:
s_and_b32 s78,s54,127
s_cmp_eq_u32 s78,s85
s_cselect_b64 s[40:41],-1,0
v_cndmask_b32 v17,v15,v16,s[40:41]
s_add_u32 m0,s57,0
s_add_u32 s56,s55,0
D 17,56,0
s_add_u32 s56,s55,256
D 17,56,1024
s_add_u32 s56,s55,512
D 17,56,2048
s_add_u32 s56,s55,768
D 17,56,3072
s_add_u32 s58,s58,s61
s_add_u32 m0,s59,2048
s_nop 0
buffer_load_dword v8,s[36:39],s58 offen offset:0 sc1 lds
v_cndmask_b32 v17,v10,v9,s[40:41]
s_add_u32 m0,s57,16384
s_add_u32 s56,s55,20480
D 17,56,0
s_add_u32 s56,s55,20736
D 17,56,1024
s_add_u32 s56,s55,20992
D 17,56,2048
s_add_u32 s56,s55,21248
D 17,56,3072
s_add_u32 s55,s55,s60
s_sub_u32 s57,s84,s57
s_add_u32 s54,s54,s46
s_and_b32 s79,s54,127
s_cmp_eq_u32 s79,0
s_cselect_b32 s94,0x3300,0
s_cmp_eq_u32 s79,127
s_cselect_b32 s94,0xcc00,s94
s_waitcnt vmcnt(4)
s_barrier
buffer_store_dword v4,v3,s[36:39],s70 offen offset:0 sc0 sc1
s_min_u32 s71,s71,s72
buffer_store_dword v4,v3,s[36:39],s71 offen offset:0 sc0 sc1
s_mov_b32 s70,0x7fff0000
s_mov_b32 s71,0x7fff0000
s_mov_b32 s72,0x7fff0000
s_cmp_eq_u32 s80,1
buffer_store_dword v4,v3,s[36:39],s71 offen offset:0 sc0 sc1
s_cbranch_scc1 Lg2_st1_%=
s_branch Lg2_st0_%=
Lg2_gox_%=:
s_add_u32 m0,s59,2048
s_nop 0
buffer_load_dword v8,s[36:39],s58 offen offset:0 sc1 lds
s_and_b32 s79,s54,127
s_cmp_eq_u32 s79,0
s_cselect_b32 s94,0x3300,0
s_cmp_eq_u32 s79,127
s_cselect_b32 s94,0xcc00,s94
s_waitcnt vmcnt(0)
s_barrier
buffer_store_dword v4,v3,s[36:39],s70 offen offset:0 sc0 sc1
s_min_u32 s71,s71,s72
buffer_store_dword v4,v3,s[36:39],s71 offen offset:0 sc0 sc1
s_mov_b32 s70,0x7fff0000
s_mov_b32 s71,0x7fff0000
s_mov_b32 s72,0x7fff0000
s_cmp_eq_u32 s80,1
s_cbranch_scc1 Lg2_st1_%=
Lg2_st0_%=:
ds_read_b32 v7,v6 offset:2048
R 34,1,0
R 38,1,1024
R 42,1,2048
R 46,1,3072
R 50,1,4096
L 5
v_cmp_eq_u32 vcc,s53,v7
s_nop 0
s_or_b64 vcc,vcc,s[94:95]
L 3
s_cmp_eq_u32 vcc_lo,-1
s_cbranch_scc1 Lg2_F0_%=
s_branch Lg2_L0_%=
Lg2_st1_%=:
ds_read_b32 v7,v6 offset:2048
R 34,2,0
R 38,2,1024
R 42,2,2048
R 46,2,3072
R 50,2,4096
L 5
v_cmp_eq_u32 vcc,s53,v7
s_nop 0
s_or_b64 vcc,vcc,s[94:95]
L 3
s_cmp_eq_u32 vcc_lo,-1
s_cbranch_scc1 Lg2_F1_%=
s_branch Lg2_L1_%=
Lg2_F0_%=:
v_mfma_f32_16x16x32_bf16 v[22:25],a[4:7],v[34:37],0
s_add_u32 s58,s58,s61
s_add_u32 m0,s59,0
s_and_b32 s78,s54,127
R 54,1,5120
M 22,a[12:15],38
s_cmp_eq_u32 s78,s85
s_cselect_b64 s[40:41],-1,0
s_add_u32 s56,s55,0
buffer_load_dword v8,s[36:39],s58 offen offset:0 sc1 lds
v_mfma_f32_16x16x32_bf16 v[18:21],a[0:3],v[34:37],0
s_add_u32 m0,s57,0
R 58,1,6144
v_cndmask_b32 v17,v15,v16,s[40:41]
M 18,a[8:11],38
L 3
D 17,56,0
M 18,a[16:19],42
s_add_u32 s56,s55,256
R 62,1,7168
v_lshlrev_b32 v106,16,v126
M 18,a[24:27],46
s_add_u32 s54,s54,s46
v_and_b32 v107,0xffff0000,v126
v_lshlrev_b32 v108,16,v127
M 22,a[20:23],42
s_and_b32 s79,s54,127
R 66,1,8192
v_and_b32 v109,0xffff0000,v127
M 22,a[28:31],46
L 3
D 17,56,1024
M 22,a[36:39],50
s_add_u32 s56,s55,512
R 70,1,9216
v_lshlrev_b32 v110,16,v128
M 22,a[44:47],54
s_cmp_eq_u32 s79,0
v_and_b32 v111,0xffff0000,v128
v_lshlrev_b32 v112,16,v129
M 18,a[32:35],50
s_cselect_b32 s94,0x3300,0
R 74,1,10240
v_and_b32 v113,0xffff0000,v129
M 18,a[40:43],54
L 3
D 17,56,2048
M 18,a[48:51],58
s_add_u32 s56,s55,768
R 78,1,11264
v_fma_f32 v106,v106,v103,v102
M 18,a[56:59],62
D 17,56,3072
M 22,a[52:55],58
s_add_u32 m0,s57,16384
R 82,1,12288
v_fma_f32 v107,v107,v103,v102
M 22,a[60:63],62
L 3
v_fma_f32 v108,v108,v103,v102
v_fma_f32 v109,v109,v103,v102
M 22,a[68:71],66
s_add_u32 s56,s55,20480
R 86,1,13312
v_fma_f32 v110,v110,v103,v102
M 22,a[76:79],70
buffer_load_dwordx4 v[122:125],v0,s[36:39],s74 offen offset:0 sc1
M 18,a[64:67],66
s_cmp_eq_u32 s79,127
R 90,1,14336
v_fma_f32 v111,v111,v103,v102
M 18,a[72:75],70
L 3
buffer_load_dwordx2 v[98:99],v11,s[36:39],s73 offen offset:256 sc1
M 18,a[80:83],74
s_waitcnt vmcnt(10)
R 94,1,15360
v_fma_f32 v112,v112,v103,v102
M 18,a[88:91],78
s_barrier
v_fma_f32 v113,v113,v103,v102
v_add_f32 v26,v26,v106
M 22,a[84:87],74
s_cselect_b32 s94,0xcc00,s94
R 34,1,16384
v_add_f32 v27,v27,v107
M 22,a[92:95],78
L 3
v_add_f32 v28,v28,v108
v_add_f32 v29,v29,v109
M 22,a[100:103],82
s_mov_b32 s66,s65
R 38,1,17408
v_add_f32 v30,v30,v110
M 22,a[108:111],86
s_mov_b32 s76,s75
v_cndmask_b32 v17,v10,v9,s[40:41]
v_add_f32 v31,v31,v111
M 18,a[96:99],82
s_add_u32 s73,s73,s64
R 42,1,18432
v_add_f32 v32,v32,v112
M 18,a[104:107],86
L 3
D 17,56,0
M 18,a[112:115],90
s_add_u32 s56,s55,20736
R 46,1,19456
v_add_f32 v33,v33,v113
M 18,a[120:123],94
s_add_u32 s74,s74,s62
v_cvt_pk_bf16_f32 v114,v26,v27
)"
R"(v_cvt_pk_bf16_f32 v115,v28,v29
M 22,a[116:119],90
s_add_u32 s65,s65,s62
R 50,1,20480
v_fma_f32 v107,v26,v26,s87
M 22,a[124:127],94
L 3
v_add_f32 v106,v26,v27
v_fma_f32 v107,v27,v27,v107
M 22,a[132:135],34
s_add_u32 s75,s75,s63
R 54,1,21504
v_add_f32 v106,v106,v28
M 22,a[140:143],38
D 17,56,1024
M 18,a[128:131],34
s_add_u32 s56,s55,20992
R 58,1,22528
v_fma_f32 v107,v28,v28,v107
M 18,a[136:139],38
L 3
v_add_f32 v106,v106,v29
v_fma_f32 v107,v29,v29,v107
M 18,a[144:147],42
s_sub_u32 s57,s84,s57
R 62,1,23552
v_add_f32 v106,v106,v30
M 18,a[152:155],46
v_fma_f32 v107,v30,v30,v107
v_add_f32 v106,v106,v31
M 22,a[148:151],42
R 66,1,24576
v_fma_f32 v107,v31,v31,v107
M 22,a[156:159],46
L 3
v_add_f32 v106,v106,v32
v_fma_f32 v107,v32,v32,v107
M 22,a[164:167],50
R 70,1,25600
v_add_f32 v106,v106,v33
M 22,a[172:175],54
v_fma_f32 v107,v33,v33,v107
v_cvt_pk_bf16_f32 v116,v30,v31
M 18,a[160:163],50
R 74,1,26624
v_cvt_pk_bf16_f32 v117,v32,v33
M 18,a[168:171],54
L 3
v_permlane16_swap_b32 v106,v107
v_add_f32 v106,v106,v107
v_mov_b32 v107,v106
M 18,a[176:179],58
R 78,1,27648
M 18,a[184:187],62
v_permlane32_swap_b32 v106,v107
v_add_f32 v106,v106,v107
M 22,a[180:183],58
R 82,1,28672
ds_write_b32 v12,v106 offset:2048
M 22,a[188:191],62
L 4
D 17,56,2048
M 22,a[196:199],66
s_add_u32 s56,s55,21248
s_add_u32 s55,s55,s60
R 86,1,29696
M 22,a[204:207],70
D 17,56,3072
M 18,a[192:195],66
R 90,1,30720
M 18,a[200:203],70
L 4
L 2
buffer_store_dwordx4 v[114:117],v0,s[36:39],s67 offen offset:0 sc0 sc1
M 18,a[208:211],74
s_waitcnt vmcnt(7)
R 94,1,31744
M 18,a[216:219],78
s_barrier
ds_read_b32 v7,v6 offset:0
R 118,13,2048
M 22,a[212:215],74
R 34,2,0
M 22,a[220:223],78
L 5
buffer_store_dword v4,v3,s[36:39],s70 offen offset:0 sc0 sc1
M 22,a[228:231],82
s_mov_b32 s70,s69
s_mov_b32 s69,s68
R 38,2,1024
M 22,a[236:239],86
s_add_u32 s68,s68,s63
M 18,a[224:227],82
R 42,2,2048
M 18,a[232:235],86
L 5
L 4
v_cmp_eq_u32 vcc,s53,v7
M 18,a[240:243],90
L 3
R 46,2,3072
v_add_f32 v118,v118,v119
M 18,a[248:251],94
s_or_b64 vcc,vcc,s[94:95]
v_add_f32 v120,v120,v121
v_add_f32 v118,v118,v120
M 22,a[244:247],90
s_cmp_eq_u32 vcc_lo,-1
R 50,2,4096
v_mul_f32 v118,s86,v118
M 22,a[252:255],94
s_waitcnt vmcnt(6) lgkmcnt(3)
buffer_store_dword v118,v14,s[36:39],s77 offen offset:0 sc0 sc1
s_cbranch_scc0 Lg2_K1_%=
Lg2_F1_%=:
v_mfma_f32_16x16x32_bf16 v[30:33],a[4:7],v[34:37],0
s_add_u32 s58,s58,s61
s_add_u32 m0,s59,1024
s_and_b32 s78,s54,127
R 54,2,5120
M 30,a[12:15],38
s_cmp_eq_u32 s78,s85
s_cselect_b64 s[40:41],-1,0
s_add_u32 s56,s55,0
buffer_load_dword v8,s[36:39],s58 offen offset:0 sc1 lds
v_mfma_f32_16x16x32_bf16 v[26:29],a[0:3],v[34:37],0
s_add_u32 m0,s57,0
R 58,2,6144
v_cndmask_b32 v17,v15,v16,s[40:41]
M 26,a[8:11],38
L 3
D 17,56,0
M 26,a[16:19],42
s_add_u32 s56,s55,256
R 62,2,7168
v_lshlrev_b32 v106,16,v122
M 26,a[24:27],46
s_add_u32 s54,s54,s46
v_and_b32 v107,0xffff0000,v122
v_lshlrev_b32 v108,16,v123
M 30,a[20:23],42
s_and_b32 s79,s54,127
R 66,2,8192
v_and_b32 v109,0xffff0000,v123
M 30,a[28:31],46
L 3
D 17,56,1024
M 30,a[36:39],50
s_add_u32 s56,s55,512
R 70,2,9216
v_lshlrev_b32 v110,16,v124
M 30,a[44:47],54
s_cmp_eq_u32 s79,0
v_and_b32 v111,0xffff0000,v124
v_lshlrev_b32 v112,16,v125
M 26,a[32:35],50
s_cselect_b32 s94,0x3300,0
R 74,2,10240
v_and_b32 v113,0xffff0000,v125
M 26,a[40:43],54
L 3
D 17,56,2048
M 26,a[48:51],58
s_add_u32 s56,s55,768
R 78,2,11264
v_fma_f32 v106,v106,v99,v98
M 26,a[56:59],62
D 17,56,3072
M 30,a[52:55],58
s_add_u32 m0,s57,16384
R 82,2,12288
v_fma_f32 v107,v107,v99,v98
M 30,a[60:63],62
L 3
v_fma_f32 v108,v108,v99,v98
v_fma_f32 v109,v109,v99,v98
M 30,a[68:71],66
s_add_u32 s56,s55,20480
R 86,2,13312
v_fma_f32 v110,v110,v99,v98
M 30,a[76:79],70
buffer_load_dwordx4 v[126:129],v0,s[36:39],s74 offen offset:0 sc1
M 26,a[64:67],66
s_cmp_eq_u32 s79,127
R 90,2,14336
v_fma_f32 v111,v111,v99,v98
M 26,a[72:75],70
L 3
buffer_load_dwordx2 v[102:103],v11,s[36:39],s73 offen offset:256 sc1
M 26,a[80:83],74
s_waitcnt vmcnt(10)
R 94,2,15360
v_fma_f32 v112,v112,v99,v98
M 26,a[88:91],78
s_barrier
v_fma_f32 v113,v113,v99,v98
v_add_f32 v18,v18,v106
M 30,a[84:87],74
s_cselect_b32 s94,0xcc00,s94
R 34,2,16384
v_add_f32 v19,v19,v107
M 30,a[92:95],78
L 3
v_add_f32 v20,v20,v108
v_add_f32 v21,v21,v109
M 30,a[100:103],82
s_mov_b32 s67,s65
R 38,2,17408
v_add_f32 v22,v22,v110
M 30,a[108:111],86
s_mov_b32 s77,s75
v_cndmask_b32 v17,v10,v9,s[40:41]
v_add_f32 v23,v23,v111
M 26,a[96:99],82
s_add_u32 s73,s73,s64
R 42,2,18432
v_add_f32 v24,v24,v112
M 26,a[104:107],86
L 3
D 17,56,0
M 26,a[112:115],90
s_add_u32 s56,s55,20736
R 46,2,19456
v_add_f32 v25,v25,v113
M 26,a[120:123],94
s_add_u32 s74,s74,s62
v_cvt_pk_bf16_f32 v114,v18,v19
v_cvt_pk_bf16_f32 v115,v20,v21
M 30,a[116:119],90
s_add_u32 s65,s65,s62
R 50,2,20480
v_fma_f32 v107,v18,v18,s87
M 30,a[124:127],94
L 3
v_add_f32 v106,v18,v19
v_fma_f32 v107,v19,v19,v107
M 30,a[132:135],34
s_add_u32 s75,s75,s63
R 54,2,21504
v_add_f32 v106,v106,v20
M 30,a[140:143],38
D 17,56,1024
M 26,a[128:131],34
s_add_u32 s56,s55,20992
R 58,2,22528
v_fma_f32 v107,v20,v20,v107
M 26,a[136:139],38
L 3
v_add_f32 v106,v106,v21
v_fma_f32 v107,v21,v21,v107
M 26,a[144:147],42
s_sub_u32 s57,s84,s57
R 62,2,23552
v_add_f32 v106,v106,v22
M 26,a[152:155],46
v_fma_f32 v107,v22,v22,v107
v_add_f32 v106,v106,v23
M 30,a[148:151],42
R 66,2,24576
v_fma_f32 v107,v23,v23,v107
M 30,a[156:159],46
L 3
v_add_f32 v106,v106,v24
v_fma_f32 v107,v24,v24,v107
M 30,a[164:167],50
R 70,2,25600
v_add_f32 v106,v106,v25
M 30,a[172:175],54
v_fma_f32 v107,v25,v25,v107
v_cvt_pk_bf16_f32 v116,v22,v23
M 26,a[160:163],50
R 74,2,26624
v_cvt_pk_bf16_f32 v117,v24,v25
M 26,a[168:171],54
L 3
v_permlane16_swap_b32 v106,v107
v_add_f32 v106,v106,v107
v_mov_b32 v107,v106
M 26,a[176:179],58
R 78,2,27648
M 26,a[184:187],62
v_permlane32_swap_b32 v106,v107
v_add_f32 v106,v106,v107
M 30,a[180:183],58
R 82,2,28672
ds_write_b32 v12,v106 offset:0
M 30,a[188:191],62
L 4
D 17,56,2048
M 30,a[196:199],66
s_add_u32 s56,s55,21248
s_add_u32 s55,s55,s60
R 86,2,29696
M 30,a[204:207],70
D 17,56,3072
M 26,a[192:195],66
R 90,2,30720
M 26,a[200:203],70
L 4
L 2
buffer_store_dwordx4 v[114:117],v0,s[36:39],s66 offen offset:0 sc0 sc1
M 26,a[208:211],74
s_waitcnt vmcnt(7)
R 94,2,31744
M 26,a[216:219],78
s_barrier
ds_read_b32 v7,v6 offset:1024
R 118,13,0
M 30,a[212:215],74
R 34,1,0
M 30,a[220:223],78
L 5
buffer_store_dword v4,v3,s[36:39],s70 offen offset:0 sc0 sc1
M 30,a[228:231],82
s_mov_b32 s70,s69
s_mov_b32 s69,s68
R 38,1,1024
M 30,a[236:239],86
s_add_u32 s68,s68,s63
M 26,a[224:227],82
R 42,1,2048
M 26,a[232:235],86
L 5
L 4
v_cmp_eq_u32 vcc,s53,v7
M 26,a[240:243],90
L 3
R 46,1,3072
v_add_f32 v118,v118,v119
M 26,a[248:251],94
s_or_b64 vcc,vcc,s[94:95]
v_add_f32 v120,v120,v121
v_add_f32 v118,v118,v120
M 30,a[244:247],90
s_cmp_eq_u32 vcc_lo,-1
R 50,1,4096
v_mul_f32 v118,s86,v118
M 30,a[252:255],94
s_waitcnt vmcnt(6) lgkmcnt(3)
buffer_store_dword v118,v14,s[36:39],s76 offen offset:0 sc0 sc1
s_cbranch_scc1 Lg2_F0_%=
Lg2_K0_%=:
Lg2_L0_%=:
v_mfma_f32_16x16x32_bf16 v[22:25],a[4:7],v[34:37],0
s_add_u32 m0,s59,3072
R 54,1,5120
v_lshlrev_b32 v106,16,v126
M 22,a[12:15],38
buffer_load_dword v8,s[36:39],s58 offen offset:0 sc1 lds
v_mfma_f32_16x16x32_bf16 v[18:21],a[0:3],v[34:37],0
s_mov_b32 s66,s65
R 58,1,6144
v_and_b32 v107,0xffff0000,v126
M 18,a[8:11],38
L 3
v_lshlrev_b32 v108,16,v127
v_and_b32 v109,0xffff0000,v127
M 18,a[16:19],42
s_mov_b32 s76,s75
R 62,1,7168
v_lshlrev_b32 v110,16,v128
M 18,a[24:27],46
s_add_u32 s65,s65,s62
v_and_b32 v111,0xffff0000,v128
v_lshlrev_b32 v112,16,v129
M 22,a[20:23],42
s_add_u32 s75,s75,s63
R 66,1,8192
v_and_b32 v113,0xffff0000,v129
v_fma_f32 v106,v106,v103,v102
M 22,a[28:31],46
L 3
v_fma_f32 v107,v107,v103,v102
v_fma_f32 v108,v108,v103,v102
M 22,a[36:39],50
R 70,1,9216
v_fma_f32 v109,v109,v103,v102
M 22,a[44:47],54
v_fma_f32 v110,v110,v103,v102
v_fma_f32 v111,v111,v103,v102
M 18,a[32:35],50
R 74,1,10240
v_fma_f32 v112,v112,v103,v102
M 18,a[40:43],54
L 3
v_fma_f32 v113,v113,v103,v102
v_add_f32 v26,v26,v106
M 18,a[48:51],58
R 78,1,11264
v_add_f32 v27,v27,v107
M 18,a[56:59],62
v_add_f32 v28,v28,v108
v_add_f32 v29,v29,v109
v_add_f32 v30,v30,v110
M 22,a[52:55],58
R 82,1,12288
v_add_f32 v31,v31,v111
M 22,a[60:63],62
L 3
v_add_f32 v32,v32,v112
v_add_f32 v33,v33,v113
M 22,a[68:71],66
R 86,1,13312
v_cvt_pk_bf16_f32 v114,v26,v27
M 22,a[76:79],70
buffer_load_dwordx4 v[122:125],v0,s[36:39],s74 offen offset:0 sc1
M 18,a[64:67],66
s_add_u32 s74,s74,s62
R 90,1,14336
v_cvt_pk_bf16_f32 v115,v28,v29
M 18,a[72:75],70
L 3
buffer_load_dwordx2 v[98:99],v11,s[36:39],s73 offen offset:256 sc1
v_cvt_pk_bf16_f32 v116,v30,v31
M 18,a[80:83],74
s_waitcnt vmcnt(2)
s_add_u32 s73,s73,s64
R 94,1,15360
M 18,a[88:91],78
s_barrier
ds_read_b32 v7,v6 offset:3072
v_cvt_pk_bf16_f32 v117,v32,v33
M 22,a[84:87],74
R 34,1,16384
v_add_f32 v106,v26,v27
M 22,a[92:95],78
L 4
buffer_store_dword v4,v3,s[36:39],s70 offen offset:0 sc0 sc1
M 22,a[100:103],82
s_mov_b32 s70,0x7fff0000
R 38,1,17408
v_fma_f32 v107,v26,v26,s87
M 22,a[108:111],86
buffer_store_dwordx4 v[114:117],v0,s[36:39],s67 offen offset:0 sc0 sc1
v_fma_f32 v107,v27,v27,v107
M 18,a[96:99],82
R 42,1,18432
v_add_f32 v106,v106,v28
v_fma_f32 v107,v28,v28,v107
M 18,a[104:107],86
L 4
L 3
v_cmp_eq_u32 vcc,s53,v7
M 18,a[112:115],90
R 46,1,19456
v_add_f32 v106,v106,v29
M 18,a[120:123],94
s_or_b64 vcc,vcc,s[94:95]
v_fma_f32 v107,v29,v29,v107
v_add_f32 v106,v106,v30
M 22,a[116:119],90
R 50,1,20480
v_fma_f32 v107,v30,v30,v107
M 22,a[124:127],94
s_cmp_eq_u32 vcc_lo,-1
L 3
v_add_f32 v106,v106,v31
s_cbranch_scc0 Lg2_X0_%=
M 22,a[132:135],34
s_and_b32 s78,s54,127
s_cmp_eq_u32 s78,s85
s_cselect_b64 s[40:41],-1,0
R 54,1,21504
M 22,a[140:143],38
s_add_u32 m0,s57,0
s_add_u32 s56,s55,0
s_add_u32 s58,s58,s61
v_cndmask_b32 v17,v15,v16,s[40:41]
M 18,a[128:131],34
s_add_u32 s54,s54,s46
R 58,1,22528
v_fma_f32 v107,v31,v31,v107
M 18,a[136:139],38
L 3
D 17,56,0
M 18,a[144:147],42
s_add_u32 s56,s55,256
R 62,1,23552
v_add_f32 v106,v106,v32
M 18,a[152:155],46
s_and_b32 s79,s54,127
v_fma_f32 v107,v32,v32,v107
v_add_f32 v106,v106,v33
M 22,a[148:151],42
s_cmp_eq_u32 s79,0
R 66,1,24576
v_fma_f32 v107,v33,v33,v107
M 22,a[156:159],46
L 3
D 17,56,1024
M 22,a[164:167],50
s_cselect_b32 s94,0x3300,0
s_add_u32 s56,s55,512
R 70,1,25600
M 22,a[172:175],54
s_cmp_eq_u32 s79,127
v_permlane16_swap_b32 v106,v107
v_add_f32 v106,v106,v107
M 18,a[160:163],50
s_cselect_b32 s94,0xcc00,s94
)"
R"(R 74,1,26624
v_mov_b32 v107,v106
M 18,a[168:171],54
L 3
D 17,56,2048
M 18,a[176:179],58
s_add_u32 s56,s55,768
R 78,1,27648
M 18,a[184:187],62
v_permlane32_swap_b32 v106,v107
v_add_f32 v106,v106,v107
M 22,a[180:183],58
R 82,1,28672
ds_write_b32 v12,v106 offset:2048
M 22,a[188:191],62
L 4
D 17,56,3072
M 22,a[196:199],66
s_add_u32 m0,s59,0
s_add_u32 s56,s55,20480
R 86,1,29696
M 22,a[204:207],70
buffer_load_dword v8,s[36:39],s58 offen offset:0 sc1 lds
M 18,a[192:195],66
s_add_u32 m0,s57,16384
R 90,1,30720
v_cndmask_b32 v17,v10,v9,s[40:41]
M 18,a[200:203],70
L 4
L 2
s_sub_u32 s57,s84,s57
M 18,a[208:211],74
s_waitcnt vmcnt(0)
R 94,1,31744
M 18,a[216:219],78
s_barrier
D 17,56,0
M 22,a[212:215],74
s_add_u32 s56,s55,20736
R 34,2,0
ds_read_b32 v7,v6 offset:0
M 22,a[220:223],78
L 4
buffer_store_dword v4,v3,s[36:39],s70 offen offset:0 sc0 sc1
M 22,a[228:231],82
s_mov_b32 s70,s69
R 38,2,1024
R 118,13,2048
M 22,a[236:239],86
D 17,56,1024
M 18,a[224:227],82
s_add_u32 s56,s55,20992
s_mov_b32 s69,s68
R 42,2,2048
M 18,a[232:235],86
L 5
D 17,56,2048
M 18,a[240:243],90
L 3
s_add_u32 s56,s55,21248
R 46,2,3072
v_cmp_eq_u32 vcc,s53,v7
M 18,a[248:251],94
D 17,56,3072
M 22,a[244:247],90
s_add_u32 s55,s55,s60
s_add_u32 s68,s68,s63
L 2
s_or_b64 vcc,vcc,s[94:95]
s_cmp_eq_u32 vcc_lo,-1
R 50,2,4096
v_add_f32 v118,v118,v119
v_add_f32 v120,v120,v121
v_add_f32 v118,v118,v120
v_mul_f32 v118,s86,v118
M 22,a[252:255],94
s_nop 0
buffer_store_dword v118,v14,s[36:39],s77 offen offset:0 sc0 sc1
buffer_store_dword v4,v3,s[36:39],s71 offen offset:0 sc0 sc1
buffer_store_dword v4,v3,s[36:39],s71 offen offset:0 sc0 sc1
s_cbranch_scc1 Lg2_F1_%=
s_branch Lg2_K1_%=
Lg2_X0_%=:
M 22,a[132:135],34
s_add_u32 m0,s59,0
R 54,1,21504
v_fma_f32 v107,v31,v31,v107
M 22,a[140:143],38
buffer_load_dword v8,s[36:39],s58 offen offset:0 sc1 lds
M 18,a[128:131],34
R 58,1,22528
v_add_f32 v106,v106,v32
M 18,a[136:139],38
L 3
v_fma_f32 v107,v32,v32,v107
v_add_f32 v106,v106,v33
M 18,a[144:147],42
R 62,1,23552
v_fma_f32 v107,v33,v33,v107
M 18,a[152:155],46
M 22,a[148:151],42
R 66,1,24576
M 22,a[156:159],46
L 3
v_permlane16_swap_b32 v106,v107
v_add_f32 v106,v106,v107
M 22,a[164:167],50
R 70,1,25600
v_mov_b32 v107,v106
M 22,a[172:175],54
M 18,a[160:163],50
R 74,1,26624
M 18,a[168:171],54
L 3
v_permlane32_swap_b32 v106,v107
M 18,a[176:179],58
R 78,1,27648
v_add_f32 v106,v106,v107
M 18,a[184:187],62
ds_write_b32 v12,v106 offset:2048
M 22,a[180:183],58
R 82,1,28672
M 22,a[188:191],62
L 4
M 22,a[196:199],66
R 86,1,29696
M 22,a[204:207],70
M 18,a[192:195],66
R 90,1,30720
M 18,a[200:203],70
L 4
L 3
M 18,a[208:211],74
s_waitcnt vmcnt(0)
R 94,1,31744
M 18,a[216:219],78
s_barrier
R 118,13,2048
ds_read_b32 v7,v6 offset:0
M 22,a[212:215],74
buffer_store_dword v4,v3,s[36:39],s70 offen offset:0 sc0 sc1
M 22,a[220:223],78
L 4
s_mov_b32 s70,s69
s_mov_b32 s69,s68
M 22,a[228:231],82
s_add_u32 s68,s68,s63
M 22,a[236:239],86
M 18,a[224:227],82
M 18,a[232:235],86
L 2
L 1
L 0
M 18,a[240:243],90
v_add_f32 v118,v118,v119
v_add_f32 v120,v120,v121
v_add_f32 v118,v118,v120
M 18,a[248:251],94
v_cmp_eq_u32 vcc,s53,v7
v_mul_f32 v118,s86,v118
M 22,a[244:247],90
buffer_store_dword v118,v14,s[36:39],s77 offen offset:0 sc0 sc1
M 22,a[252:255],94
s_nop 0
s_or_b64 vcc,vcc,s[94:95]
s_mov_b32 s80,1
s_cmp_eq_u32 vcc_lo,-1
s_cbranch_scc0 Lg2_xn0_%=
s_and_b32 s78,s54,127
s_cmp_eq_u32 s78,s85
s_cselect_b64 s[40:41],-1,0
v_cndmask_b32 v17,v15,v16,s[40:41]
s_add_u32 m0,s57,0
s_add_u32 s56,s55,0
D 17,56,0
v_lshlrev_b32 v106,16,v122
v_and_b32 v107,0xffff0000,v122
v_lshlrev_b32 v108,16,v123
v_and_b32 v109,0xffff0000,v123
v_lshlrev_b32 v110,16,v124
s_add_u32 s56,s55,256
D 17,56,1024
v_and_b32 v111,0xffff0000,v124
v_lshlrev_b32 v112,16,v125
v_and_b32 v113,0xffff0000,v125
v_fma_f32 v106,v106,v99,v98
v_fma_f32 v107,v107,v99,v98
s_add_u32 s56,s55,512
D 17,56,2048
v_fma_f32 v108,v108,v99,v98
v_fma_f32 v109,v109,v99,v98
v_fma_f32 v110,v110,v99,v98
v_fma_f32 v111,v111,v99,v98
v_fma_f32 v112,v112,v99,v98
s_add_u32 s56,s55,768
D 17,56,3072
v_fma_f32 v113,v113,v99,v98
v_add_f32 v18,v18,v106
v_add_f32 v19,v19,v107
v_add_f32 v20,v20,v108
v_add_f32 v21,v21,v109
s_add_u32 s58,s58,s61
v_cndmask_b32 v17,v10,v9,s[40:41]
s_add_u32 m0,s57,16384
s_add_u32 s56,s55,20480
D 17,56,0
v_add_f32 v22,v22,v110
v_add_f32 v23,v23,v111
v_add_f32 v24,v24,v112
v_add_f32 v25,v25,v113
v_cvt_pk_bf16_f32 v114,v18,v19
s_add_u32 s56,s55,20736
D 17,56,1024
v_cvt_pk_bf16_f32 v115,v20,v21
v_cvt_pk_bf16_f32 v116,v22,v23
v_cvt_pk_bf16_f32 v117,v24,v25
s_add_u32 s56,s55,20992
D 17,56,2048
s_add_u32 s56,s55,21248
D 17,56,3072
buffer_store_dwordx4 v[114:117],v0,s[36:39],s66 offen offset:0 sc0 sc1
v_add_f32 v106,v18,v19
v_fma_f32 v107,v18,v18,s87
s_add_u32 s55,s55,s60
s_sub_u32 s57,s84,s57
s_add_u32 s54,s54,s46
v_fma_f32 v107,v19,v19,v107
v_add_f32 v106,v106,v20
v_fma_f32 v107,v20,v20,v107
v_add_f32 v106,v106,v21
v_fma_f32 v107,v21,v21,v107
v_add_f32 v106,v106,v22
v_fma_f32 v107,v22,v22,v107
v_add_f32 v106,v106,v23
v_fma_f32 v107,v23,v23,v107
v_add_f32 v106,v106,v24
v_fma_f32 v107,v24,v24,v107
v_add_f32 v106,v106,v25
v_fma_f32 v107,v25,v25,v107
s_nop 1
v_permlane16_swap_b32 v106,v107
v_add_f32 v106,v106,v107
v_mov_b32 v107,v106
s_nop 1
v_permlane32_swap_b32 v106,v107
v_add_f32 v106,v106,v107
ds_write_b32 v12,v106 offset:0
L 0
s_barrier
R 118,13,0
L 0
v_add_f32 v118,v118,v119
v_add_f32 v120,v120,v121
v_add_f32 v118,v118,v120
v_mul_f32 v118,s86,v118
buffer_store_dword v118,v14,s[36:39],s76 offen offset:0 sc0 sc1
s_mov_b32 s72,s69
s_mov_b32 s69,0x7fff0000
s_mov_b32 s66,0x7fff0000
s_mov_b32 s76,0x7fff0000
s_branch Lg2_gox_%=
Lg2_xn0_%=:
s_cmp_ge_u32 s54,256
s_cbranch_scc1 Lg2_fin0_%=
s_add_u32 m0,s59,4096
s_nop 0
buffer_load_dword v8,s[36:39],s58 offen offset:0 sc1 lds
buffer_store_dword v4,v3,s[36:39],s71 offen offset:0 sc0 sc1
s_add_u32 m0,s59,5120
s_nop 0
buffer_load_dword v8,s[36:39],s58 offen offset:0 sc1 lds
buffer_store_dword v4,v3,s[36:39],s71 offen offset:0 sc0 sc1
s_mov_b32 s83,0
v_lshlrev_b32 v106,16,v122
v_and_b32 v107,0xffff0000,v122
v_lshlrev_b32 v108,16,v123
v_and_b32 v109,0xffff0000,v123
v_lshlrev_b32 v110,16,v124
v_and_b32 v111,0xffff0000,v124
v_lshlrev_b32 v112,16,v125
v_and_b32 v113,0xffff0000,v125
v_fma_f32 v106,v106,v99,v98
v_fma_f32 v107,v107,v99,v98
v_fma_f32 v108,v108,v99,v98
v_fma_f32 v109,v109,v99,v98
v_fma_f32 v110,v110,v99,v98
v_fma_f32 v111,v111,v99,v98
v_fma_f32 v112,v112,v99,v98
v_fma_f32 v113,v113,v99,v98
v_add_f32 v18,v18,v106
v_add_f32 v19,v19,v107
v_add_f32 v20,v20,v108
v_add_f32 v21,v21,v109
v_add_f32 v22,v22,v110
v_add_f32 v23,v23,v111
v_add_f32 v24,v24,v112
v_add_f32 v25,v25,v113
v_cvt_pk_bf16_f32 v114,v18,v19
v_cvt_pk_bf16_f32 v115,v20,v21
v_cvt_pk_bf16_f32 v116,v22,v23
v_cvt_pk_bf16_f32 v117,v24,v25
buffer_store_dwordx4 v[114:117],v0,s[36:39],s66 offen offset:0 sc0 sc1
v_add_f32 v106,v18,v19
v_fma_f32 v107,v18,v18,s87
v_fma_f32 v107,v19,v19,v107
v_add_f32 v106,v106,v20
v_fma_f32 v107,v20,v20,v107
v_add_f32 v106,v106,v21
v_fma_f32 v107,v21,v21,v107
v_add_f32 v106,v106,v22
v_fma_f32 v107,v22,v22,v107
v_add_f32 v106,v106,v23
v_fma_f32 v107,v23,v23,v107
v_add_f32 v106,v106,v24
v_fma_f32 v107,v24,v24,v107
v_add_f32 v106,v106,v25
v_fma_f32 v107,v25,v25,v107
s_nop 1
v_permlane16_swap_b32 v106,v107
v_add_f32 v106,v106,v107
v_mov_b32 v107,v106
s_nop 1
v_permlane32_swap_b32 v106,v107
v_add_f32 v106,v106,v107
ds_write_b32 v12,v106 offset:0
L 0
s_barrier
R 118,13,0
L 0
v_add_f32 v118,v118,v119
v_add_f32 v120,v120,v121
v_add_f32 v118,v118,v120
v_mul_f32 v118,s86,v118
buffer_store_dword v118,v14,s[36:39],s76 offen offset:0 sc0 sc1
s_mov_b32 s72,s69
s_mov_b32 s69,0x7fff0000
s_mov_b32 s66,0x7fff0000
s_mov_b32 s76,0x7fff0000
s_branch Lg2_poll_%=
Lg2_fin0_%=:
v_lshlrev_b32 v106,16,v122
v_and_b32 v107,0xffff0000,v122
v_lshlrev_b32 v108,16,v123
v_and_b32 v109,0xffff0000,v123
v_lshlrev_b32 v110,16,v124
v_and_b32 v111,0xffff0000,v124
v_lshlrev_b32 v112,16,v125
v_and_b32 v113,0xffff0000,v125
v_fma_f32 v106,v106,v99,v98
v_fma_f32 v107,v107,v99,v98
v_fma_f32 v108,v108,v99,v98
v_fma_f32 v109,v109,v99,v98
v_fma_f32 v110,v110,v99,v98
v_fma_f32 v111,v111,v99,v98
v_fma_f32 v112,v112,v99,v98
v_fma_f32 v113,v113,v99,v98
v_add_f32 v18,v18,v106
v_add_f32 v19,v19,v107
v_add_f32 v20,v20,v108
v_add_f32 v21,v21,v109
v_add_f32 v22,v22,v110
v_add_f32 v23,v23,v111
v_add_f32 v24,v24,v112
v_add_f32 v25,v25,v113
v_cvt_pk_bf16_f32 v114,v18,v19
v_cvt_pk_bf16_f32 v115,v20,v21
v_cvt_pk_bf16_f32 v116,v22,v23
v_cvt_pk_bf16_f32 v117,v24,v25
buffer_store_dwordx4 v[114:117],v0,s[36:39],s66 offen offset:0 sc0 sc1
v_add_f32 v106,v18,v19
v_fma_f32 v107,v18,v18,s87
v_fma_f32 v107,v19,v19,v107
v_add_f32 v106,v106,v20
v_fma_f32 v107,v20,v20,v107
v_add_f32 v106,v106,v21
v_fma_f32 v107,v21,v21,v107
v_add_f32 v106,v106,v22
v_fma_f32 v107,v22,v22,v107
v_add_f32 v106,v106,v23
v_fma_f32 v107,v23,v23,v107
v_add_f32 v106,v106,v24
v_fma_f32 v107,v24,v24,v107
v_add_f32 v106,v106,v25
v_fma_f32 v107,v25,v25,v107
s_nop 1
v_permlane16_swap_b32 v106,v107
v_add_f32 v106,v106,v107
v_mov_b32 v107,v106
s_nop 1
v_permlane32_swap_b32 v106,v107
v_add_f32 v106,v106,v107
ds_write_b32 v12,v106 offset:0
L 0
s_barrier
R 118,13,0
L 0
v_add_f32 v118,v118,v119
v_add_f32 v120,v120,v121
v_add_f32 v118,v118,v120
v_mul_f32 v118,s86,v118
buffer_store_dword v118,v14,s[36:39],s76 offen offset:0 sc0 sc1
s_mov_b32 s72,s69
s_mov_b32 s69,0x7fff0000
s_mov_b32 s66,0x7fff0000
s_mov_b32 s76,0x7fff0000
s_branch Lg2_done_%=
Lg2_K1_%=:
Lg2_L1_%=:
v_mfma_f32_16x16x32_bf16 v[30:33],a[4:7],v[34:37],0
s_add_u32 m0,s59,3072
R 54,2,5120
v_lshlrev_b32 v106,16,v122
M 30,a[12:15],38
buffer_load_dword v8,s[36:39],s58 offen offset:0 sc1 lds
v_mfma_f32_16x16x32_bf16 v[26:29],a[0:3],v[34:37],0
s_mov_b32 s67,s65
R 58,2,6144
v_and_b32 v107,0xffff0000,v122
M 26,a[8:11],38
L 3
v_lshlrev_b32 v108,16,v123
v_and_b32 v109,0xffff0000,v123
M 26,a[16:19],42
s_mov_b32 s77,s75
R 62,2,7168
v_lshlrev_b32 v110,16,v124
M 26,a[24:27],46
s_add_u32 s65,s65,s62
v_and_b32 v111,0xffff0000,v124
v_lshlrev_b32 v112,16,v125
M 30,a[20:23],42
s_add_u32 s75,s75,s63
R 66,2,8192
v_and_b32 v113,0xffff0000,v125
v_fma_f32 v106,v106,v99,v98
M 30,a[28:31],46
L 3
v_fma_f32 v107,v107,v99,v98
v_fma_f32 v108,v108,v99,v98
M 30,a[36:39],50
R 70,2,9216
v_fma_f32 v109,v109,v99,v98
M 30,a[44:47],54
v_fma_f32 v110,v110,v99,v98
v_fma_f32 v111,v111,v99,v98
M 26,a[32:35],50
R 74,2,10240
v_fma_f32 v112,v112,v99,v98
M 26,a[40:43],54
L 3
v_fma_f32 v113,v113,v99,v98
v_add_f32 v18,v18,v106
M 26,a[48:51],58
R 78,2,11264
v_add_f32 v19,v19,v107
M 26,a[56:59],62
v_add_f32 v20,v20,v108
v_add_f32 v21,v21,v109
v_add_f32 v22,v22,v110
M 30,a[52:55],58
R 82,2,12288
v_add_f32 v23,v23,v111
M 30,a[60:63],62
L 3
v_add_f32 v24,v24,v112
v_add_f32 v25,v25,v113
M 30,a[68:71],66
R 86,2,13312
v_cvt_pk_bf16_f32 v114,v18,v19
M 30,a[76:79],70
buffer_load_dwordx4 v[126:129],v0,s[36:39],s74 offen offset:0 sc1
M 26,a[64:67],66
s_add_u32 s74,s74,s62
R 90,2,14336
v_cvt_pk_bf16_f32 v115,v20,v21
M 26,a[72:75],70
L 3
buffer_load_dwordx2 v[102:103],v11,s[36:39],s73 offen offset:256 sc1
v_cvt_pk_bf16_f32 v116,v22,v23
M 26,a[80:83],74
s_waitcnt vmcnt(2)
s_add_u32 s73,s73,s64
R 94,2,15360
M 26,a[88:91],78
s_barrier
ds_read_b32 v7,v6 offset:3072
v_cvt_pk_bf16_f32 v117,v24,v25
M 30,a[84:87],74
R 34,2,16384
v_add_f32 v106,v18,v19
M 30,a[92:95],78
L 4
buffer_store_dword v4,v3,s[36:39],s70 offen offset:0 sc0 sc1
M 30,a[100:103],82
s_mov_b32 s70,0x7fff0000
R 38,2,17408
v_fma_f32 v107,v18,v18,s87
M 30,a[108:111],86
buffer_store_dwordx4 v[114:117],v0,s[36:39],s66 offen offset:0 sc0 sc1
v_fma_f32 v107,v19,v19,v107
)"
R"(M 26,a[96:99],82
R 42,2,18432
v_add_f32 v106,v106,v20
v_fma_f32 v107,v20,v20,v107
M 26,a[104:107],86
L 4
L 3
v_cmp_eq_u32 vcc,s53,v7
M 26,a[112:115],90
R 46,2,19456
v_add_f32 v106,v106,v21
M 26,a[120:123],94
s_or_b64 vcc,vcc,s[94:95]
v_fma_f32 v107,v21,v21,v107
v_add_f32 v106,v106,v22
M 30,a[116:119],90
R 50,2,20480
v_fma_f32 v107,v22,v22,v107
M 30,a[124:127],94
s_cmp_eq_u32 vcc_lo,-1
L 3
v_add_f32 v106,v106,v23
s_cbranch_scc0 Lg2_X1_%=
M 30,a[132:135],34
s_and_b32 s78,s54,127
s_cmp_eq_u32 s78,s85
s_cselect_b64 s[40:41],-1,0
R 54,2,21504
M 30,a[140:143],38
s_add_u32 m0,s57,0
s_add_u32 s56,s55,0
s_add_u32 s58,s58,s61
v_cndmask_b32 v17,v15,v16,s[40:41]
M 26,a[128:131],34
s_add_u32 s54,s54,s46
R 58,2,22528
v_fma_f32 v107,v23,v23,v107
M 26,a[136:139],38
L 3
D 17,56,0
M 26,a[144:147],42
s_add_u32 s56,s55,256
R 62,2,23552
v_add_f32 v106,v106,v24
M 26,a[152:155],46
s_and_b32 s79,s54,127
v_fma_f32 v107,v24,v24,v107
v_add_f32 v106,v106,v25
M 30,a[148:151],42
s_cmp_eq_u32 s79,0
R 66,2,24576
v_fma_f32 v107,v25,v25,v107
M 30,a[156:159],46
L 3
D 17,56,1024
M 30,a[164:167],50
s_cselect_b32 s94,0x3300,0
s_add_u32 s56,s55,512
R 70,2,25600
M 30,a[172:175],54
s_cmp_eq_u32 s79,127
v_permlane16_swap_b32 v106,v107
v_add_f32 v106,v106,v107
M 26,a[160:163],50
s_cselect_b32 s94,0xcc00,s94
R 74,2,26624
v_mov_b32 v107,v106
M 26,a[168:171],54
L 3
D 17,56,2048
M 26,a[176:179],58
s_add_u32 s56,s55,768
R 78,2,27648
M 26,a[184:187],62
v_permlane32_swap_b32 v106,v107
v_add_f32 v106,v106,v107
M 30,a[180:183],58
R 82,2,28672
ds_write_b32 v12,v106 offset:0
M 30,a[188:191],62
L 4
D 17,56,3072
M 30,a[196:199],66
s_add_u32 m0,s59,1024
s_add_u32 s56,s55,20480
R 86,2,29696
M 30,a[204:207],70
buffer_load_dword v8,s[36:39],s58 offen offset:0 sc1 lds
M 26,a[192:195],66
s_add_u32 m0,s57,16384
R 90,2,30720
v_cndmask_b32 v17,v10,v9,s[40:41]
M 26,a[200:203],70
L 4
L 2
s_sub_u32 s57,s84,s57
M 26,a[208:211],74
s_waitcnt vmcnt(0)
R 94,2,31744
M 26,a[216:219],78
s_barrier
D 17,56,0
M 30,a[212:215],74
s_add_u32 s56,s55,20736
R 34,1,0
ds_read_b32 v7,v6 offset:1024
M 30,a[220:223],78
L 4
buffer_store_dword v4,v3,s[36:39],s70 offen offset:0 sc0 sc1
M 30,a[228:231],82
s_mov_b32 s70,s69
R 38,1,1024
R 118,13,0
M 30,a[236:239],86
D 17,56,1024
M 26,a[224:227],82
s_add_u32 s56,s55,20992
s_mov_b32 s69,s68
R 42,1,2048
M 26,a[232:235],86
L 5
D 17,56,2048
M 26,a[240:243],90
L 3
s_add_u32 s56,s55,21248
R 46,1,3072
v_cmp_eq_u32 vcc,s53,v7
M 26,a[248:251],94
D 17,56,3072
M 30,a[244:247],90
s_add_u32 s55,s55,s60
s_add_u32 s68,s68,s63
L 2
s_or_b64 vcc,vcc,s[94:95]
s_cmp_eq_u32 vcc_lo,-1
R 50,1,4096
v_add_f32 v118,v118,v119
v_add_f32 v120,v120,v121
v_add_f32 v118,v118,v120
v_mul_f32 v118,s86,v118
M 30,a[252:255],94
s_nop 0
buffer_store_dword v118,v14,s[36:39],s76 offen offset:0 sc0 sc1
buffer_store_dword v4,v3,s[36:39],s71 offen offset:0 sc0 sc1
buffer_store_dword v4,v3,s[36:39],s71 offen offset:0 sc0 sc1
s_cbranch_scc1 Lg2_F0_%=
s_branch Lg2_K0_%=
Lg2_X1_%=:
M 30,a[132:135],34
s_add_u32 m0,s59,1024
R 54,2,21504
v_fma_f32 v107,v23,v23,v107
M 30,a[140:143],38
buffer_load_dword v8,s[36:39],s58 offen offset:0 sc1 lds
M 26,a[128:131],34
R 58,2,22528
v_add_f32 v106,v106,v24
M 26,a[136:139],38
L 3
v_fma_f32 v107,v24,v24,v107
v_add_f32 v106,v106,v25
M 26,a[144:147],42
R 62,2,23552
v_fma_f32 v107,v25,v25,v107
M 26,a[152:155],46
M 30,a[148:151],42
R 66,2,24576
M 30,a[156:159],46
L 3
v_permlane16_swap_b32 v106,v107
v_add_f32 v106,v106,v107
M 30,a[164:167],50
R 70,2,25600
v_mov_b32 v107,v106
M 30,a[172:175],54
M 26,a[160:163],50
R 74,2,26624
M 26,a[168:171],54
L 3
v_permlane32_swap_b32 v106,v107
M 26,a[176:179],58
R 78,2,27648
v_add_f32 v106,v106,v107
M 26,a[184:187],62
ds_write_b32 v12,v106 offset:0
M 30,a[180:183],58
R 82,2,28672
M 30,a[188:191],62
L 4
M 30,a[196:199],66
R 86,2,29696
M 30,a[204:207],70
M 26,a[192:195],66
R 90,2,30720
M 26,a[200:203],70
L 4
L 3
M 26,a[208:211],74
s_waitcnt vmcnt(0)
R 94,2,31744
M 26,a[216:219],78
s_barrier
R 118,13,0
ds_read_b32 v7,v6 offset:1024
M 30,a[212:215],74
buffer_store_dword v4,v3,s[36:39],s70 offen offset:0 sc0 sc1
M 30,a[220:223],78
L 4
s_mov_b32 s70,s69
s_mov_b32 s69,s68
M 30,a[228:231],82
s_add_u32 s68,s68,s63
M 30,a[236:239],86
M 26,a[224:227],82
M 26,a[232:235],86
L 2
L 1
L 0
M 26,a[240:243],90
v_add_f32 v118,v118,v119
v_add_f32 v120,v120,v121
v_add_f32 v118,v118,v120
M 26,a[248:251],94
v_cmp_eq_u32 vcc,s53,v7
v_mul_f32 v118,s86,v118
M 30,a[244:247],90
buffer_store_dword v118,v14,s[36:39],s76 offen offset:0 sc0 sc1
M 30,a[252:255],94
s_nop 0
s_or_b64 vcc,vcc,s[94:95]
s_mov_b32 s80,0
s_cmp_eq_u32 vcc_lo,-1
s_cbranch_scc0 Lg2_xn1_%=
s_and_b32 s78,s54,127
s_cmp_eq_u32 s78,s85
s_cselect_b64 s[40:41],-1,0
v_cndmask_b32 v17,v15,v16,s[40:41]
s_add_u32 m0,s57,0
s_add_u32 s56,s55,0
D 17,56,0
v_lshlrev_b32 v106,16,v126
v_and_b32 v107,0xffff0000,v126
v_lshlrev_b32 v108,16,v127
v_and_b32 v109,0xffff0000,v127
v_lshlrev_b32 v110,16,v128
s_add_u32 s56,s55,256
D 17,56,1024
v_and_b32 v111,0xffff0000,v128
v_lshlrev_b32 v112,16,v129
v_and_b32 v113,0xffff0000,v129
v_fma_f32 v106,v106,v103,v102
v_fma_f32 v107,v107,v103,v102
s_add_u32 s56,s55,512
D 17,56,2048
v_fma_f32 v108,v108,v103,v102
v_fma_f32 v109,v109,v103,v102
v_fma_f32 v110,v110,v103,v102
v_fma_f32 v111,v111,v103,v102
v_fma_f32 v112,v112,v103,v102
s_add_u32 s56,s55,768
D 17,56,3072
v_fma_f32 v113,v113,v103,v102
v_add_f32 v26,v26,v106
v_add_f32 v27,v27,v107
v_add_f32 v28,v28,v108
v_add_f32 v29,v29,v109
s_add_u32 s58,s58,s61
v_cndmask_b32 v17,v10,v9,s[40:41]
s_add_u32 m0,s57,16384
s_add_u32 s56,s55,20480
D 17,56,0
v_add_f32 v30,v30,v110
v_add_f32 v31,v31,v111
v_add_f32 v32,v32,v112
v_add_f32 v33,v33,v113
v_cvt_pk_bf16_f32 v114,v26,v27
s_add_u32 s56,s55,20736
D 17,56,1024
v_cvt_pk_bf16_f32 v115,v28,v29
v_cvt_pk_bf16_f32 v116,v30,v31
v_cvt_pk_bf16_f32 v117,v32,v33
s_add_u32 s56,s55,20992
D 17,56,2048
s_add_u32 s56,s55,21248
D 17,56,3072
buffer_store_dwordx4 v[114:117],v0,s[36:39],s67 offen offset:0 sc0 sc1
v_add_f32 v106,v26,v27
v_fma_f32 v107,v26,v26,s87
s_add_u32 s55,s55,s60
s_sub_u32 s57,s84,s57
s_add_u32 s54,s54,s46
v_fma_f32 v107,v27,v27,v107
v_add_f32 v106,v106,v28
v_fma_f32 v107,v28,v28,v107
v_add_f32 v106,v106,v29
v_fma_f32 v107,v29,v29,v107
v_add_f32 v106,v106,v30
v_fma_f32 v107,v30,v30,v107
v_add_f32 v106,v106,v31
v_fma_f32 v107,v31,v31,v107
v_add_f32 v106,v106,v32
v_fma_f32 v107,v32,v32,v107
v_add_f32 v106,v106,v33
v_fma_f32 v107,v33,v33,v107
s_nop 1
v_permlane16_swap_b32 v106,v107
v_add_f32 v106,v106,v107
v_mov_b32 v107,v106
s_nop 1
v_permlane32_swap_b32 v106,v107
v_add_f32 v106,v106,v107
ds_write_b32 v12,v106 offset:2048
L 0
s_barrier
R 118,13,2048
L 0
v_add_f32 v118,v118,v119
v_add_f32 v120,v120,v121
v_add_f32 v118,v118,v120
v_mul_f32 v118,s86,v118
buffer_store_dword v118,v14,s[36:39],s77 offen offset:0 sc0 sc1
s_mov_b32 s72,s69
s_mov_b32 s69,0x7fff0000
s_mov_b32 s67,0x7fff0000
s_mov_b32 s77,0x7fff0000
s_branch Lg2_gox_%=
Lg2_xn1_%=:
s_cmp_ge_u32 s54,256
s_cbranch_scc1 Lg2_fin1_%=
s_add_u32 m0,s59,4096
s_nop 0
buffer_load_dword v8,s[36:39],s58 offen offset:0 sc1 lds
buffer_store_dword v4,v3,s[36:39],s71 offen offset:0 sc0 sc1
s_add_u32 m0,s59,5120
s_nop 0
buffer_load_dword v8,s[36:39],s58 offen offset:0 sc1 lds
buffer_store_dword v4,v3,s[36:39],s71 offen offset:0 sc0 sc1
s_mov_b32 s83,0
v_lshlrev_b32 v106,16,v126
v_and_b32 v107,0xffff0000,v126
v_lshlrev_b32 v108,16,v127
v_and_b32 v109,0xffff0000,v127
v_lshlrev_b32 v110,16,v128
v_and_b32 v111,0xffff0000,v128
v_lshlrev_b32 v112,16,v129
v_and_b32 v113,0xffff0000,v129
v_fma_f32 v106,v106,v103,v102
v_fma_f32 v107,v107,v103,v102
v_fma_f32 v108,v108,v103,v102
v_fma_f32 v109,v109,v103,v102
v_fma_f32 v110,v110,v103,v102
v_fma_f32 v111,v111,v103,v102
v_fma_f32 v112,v112,v103,v102
v_fma_f32 v113,v113,v103,v102
v_add_f32 v26,v26,v106
v_add_f32 v27,v27,v107
v_add_f32 v28,v28,v108
v_add_f32 v29,v29,v109
v_add_f32 v30,v30,v110
v_add_f32 v31,v31,v111
v_add_f32 v32,v32,v112
v_add_f32 v33,v33,v113
v_cvt_pk_bf16_f32 v114,v26,v27
v_cvt_pk_bf16_f32 v115,v28,v29
v_cvt_pk_bf16_f32 v116,v30,v31
v_cvt_pk_bf16_f32 v117,v32,v33
buffer_store_dwordx4 v[114:117],v0,s[36:39],s67 offen offset:0 sc0 sc1
v_add_f32 v106,v26,v27
v_fma_f32 v107,v26,v26,s87
v_fma_f32 v107,v27,v27,v107
v_add_f32 v106,v106,v28
v_fma_f32 v107,v28,v28,v107
v_add_f32 v106,v106,v29
v_fma_f32 v107,v29,v29,v107
v_add_f32 v106,v106,v30
v_fma_f32 v107,v30,v30,v107
v_add_f32 v106,v106,v31
v_fma_f32 v107,v31,v31,v107
v_add_f32 v106,v106,v32
v_fma_f32 v107,v32,v32,v107
v_add_f32 v106,v106,v33
v_fma_f32 v107,v33,v33,v107
s_nop 1
v_permlane16_swap_b32 v106,v107
v_add_f32 v106,v106,v107
v_mov_b32 v107,v106
s_nop 1
v_permlane32_swap_b32 v106,v107
v_add_f32 v106,v106,v107
ds_write_b32 v12,v106 offset:2048
L 0
s_barrier
R 118,13,2048
L 0
v_add_f32 v118,v118,v119
v_add_f32 v120,v120,v121
v_add_f32 v118,v118,v120
v_mul_f32 v118,s86,v118
buffer_store_dword v118,v14,s[36:39],s77 offen offset:0 sc0 sc1
s_mov_b32 s72,s69
s_mov_b32 s69,0x7fff0000
s_mov_b32 s67,0x7fff0000
s_mov_b32 s77,0x7fff0000
s_branch Lg2_poll_%=
Lg2_fin1_%=:
v_lshlrev_b32 v106,16,v126
v_and_b32 v107,0xffff0000,v126
v_lshlrev_b32 v108,16,v127
v_and_b32 v109,0xffff0000,v127
v_lshlrev_b32 v110,16,v128
v_and_b32 v111,0xffff0000,v128
v_lshlrev_b32 v112,16,v129
v_and_b32 v113,0xffff0000,v129
v_fma_f32 v106,v106,v103,v102
v_fma_f32 v107,v107,v103,v102
v_fma_f32 v108,v108,v103,v102
v_fma_f32 v109,v109,v103,v102
v_fma_f32 v110,v110,v103,v102
v_fma_f32 v111,v111,v103,v102
v_fma_f32 v112,v112,v103,v102
v_fma_f32 v113,v113,v103,v102
v_add_f32 v26,v26,v106
v_add_f32 v27,v27,v107
v_add_f32 v28,v28,v108
v_add_f32 v29,v29,v109
v_add_f32 v30,v30,v110
v_add_f32 v31,v31,v111
v_add_f32 v32,v32,v112
v_add_f32 v33,v33,v113
v_cvt_pk_bf16_f32 v114,v26,v27
v_cvt_pk_bf16_f32 v115,v28,v29
v_cvt_pk_bf16_f32 v116,v30,v31
v_cvt_pk_bf16_f32 v117,v32,v33
buffer_store_dwordx4 v[114:117],v0,s[36:39],s67 offen offset:0 sc0 sc1
v_add_f32 v106,v26,v27
v_fma_f32 v107,v26,v26,s87
v_fma_f32 v107,v27,v27,v107
v_add_f32 v106,v106,v28
v_fma_f32 v107,v28,v28,v107
v_add_f32 v106,v106,v29
v_fma_f32 v107,v29,v29,v107
v_add_f32 v106,v106,v30
v_fma_f32 v107,v30,v30,v107
v_add_f32 v106,v106,v31
v_fma_f32 v107,v31,v31,v107
v_add_f32 v106,v106,v32
v_fma_f32 v107,v32,v32,v107
v_add_f32 v106,v106,v33
v_fma_f32 v107,v33,v33,v107
s_nop 1
v_permlane16_swap_b32 v106,v107
v_add_f32 v106,v106,v107
v_mov_b32 v107,v106
s_nop 1
v_permlane32_swap_b32 v106,v107
v_add_f32 v106,v106,v107
ds_write_b32 v12,v106 offset:2048
L 0
s_barrier
R 118,13,2048
L 0
v_add_f32 v118,v118,v119
v_add_f32 v120,v120,v121
v_add_f32 v118,v118,v120
v_mul_f32 v118,s86,v118
buffer_store_dword v118,v14,s[36:39],s77 offen offset:0 sc0 sc1
s_mov_b32 s72,s69
s_mov_b32 s69,0x7fff0000
s_mov_b32 s67,0x7fff0000
s_mov_b32 s77,0x7fff0000
s_branch Lg2_done_%=
Lg2_done_%=:
s_waitcnt vmcnt(2)
s_barrier
buffer_store_dword v4,v3,s[36:39],s70 offen offset:0 sc0 sc1
s_waitcnt vmcnt(1)
s_barrier
buffer_store_dword v4,v3,s[36:39],s72 offen offset:0 sc0 sc1
s_waitcnt vmcnt(0) lgkmcnt(0)
s_barrier
Lg2_end_%=:
s_mov_b32 %0,s53
Lcode_g12_e:
.purgem M
.purgem R
.purgem T
.purgem D
.purgem L
)"
  : "=&s"(epOut) : "s"(ws), "s"(__builtin_amdgcn_readfirstlane(sWave)), "s"(__builtin_amdgcn_readfirstlane(sG0)), "s"(__builtin_amdgcn_readfirstlane(sStep)), "s"(__builtin_amdgcn_readfirstlane(sOutOff)), "s"(__builtin_amdgcn_readfirstlane(sFoutOff)), "s"(__builtin_amdgcn_readfirstlane(sAuxOff)), "s"(__builtin_amdgcn_readfirstlane(sWw)), "s"(__builtin_amdgcn_readfirstlane(sBuf)), "s"(__builtin_amdgcn_readfirstlane(sExp)), "v"(vL16), "v"(vFlag), "v"(vSnapOff), "v"(vSnapRd), "v"(vWd0), "v"(vWd1), "v"(vWr0), "v"(vWr1), "v"(vSp), "v"(vOutS), "v"(vAux0), "v"(vAux1), "v"(vAux2), "v"(vAux3), "v"(vAn), "v"(vAe), "s"(w12) : "v0", "v1", "v2", "v3", "v4", "v5", "v6", "v7", "v8", "v9", "v10", "v11", "v12", "v13", "v14", "v15", "v16", "v17", "v18", "v19", "v20", "v21", "v22", "v23", "v24", "v25", "v26", "v27", "v28", "v29", "v30", "v31", "v32", "v33", "v34", "v35", "v36", "v37", "v38", "v39", "v40", "v41", "v42", "v43", "v44", "v45", "v46", "v47", "v48", "v49", "v50", "v51", "v52", "v53", "v54", "v55", "v56", "v57", "v58", "v59", "v60", "v61", "v62", "v63", "v64", "v65", "v66", "v67", "v68", "v69", "v70", "v71", "v72", "v73", "v74", "v75", "v76", "v77", "v78", "v79", "v80", "v81", "v82", "v83", "v84", "v85", "v86", "v87", "v88", "v89", "v90", "v91", "v92", "v93", "v94", "v95", "v96", "v97", "v98", "v99", "v100", "v101", "v102", "v103", "v104", "v105", "v106", "v107", "v108", "v109", "v110", "v111", "v112", "v113", "v114", "v115", "v116", "v117", "v118", "v119", "v120", "v121", "v122", "v123", "v124", "v125", "v126", "v127", "v128", "v129", "v130", "v131", "v132", "v133", "v134", "v135", "v136", "v137", "v138", "v139", "v140", "v141", "v142", "v143", "v144", "v145", "v146", "v147", "v148", "v149", "v150", "v151", "v152", "v153", "v154", "v155", "v156", "v157", "v158", "v159", "v160", "v161", "v162", "v163", "v164", "v165", "v166", "v167", "v168", "v169", "v170", "v171", "v172", "v173", "v174", "v175", "v176", "v177", "v178", "v179", "v180", "v181", "v182", "v183", "v184", "v185", "v186", "v187", "v188", "v189", "v190", "v191", "v192", "v193", "v194", "v195", "v196", "v197", "v198", "v199", "v200", "v201", "v202", "v203", "v204", "v205", "v206", "v207", "v208", "v209", "v210", "v211", "v212", "v213", "v214", "v215", "v216", "v217", "v218", "v219", "v220", "v221", "v222", "v223", "v224", "v225", "v226", "v227", "v228", "v229", "v230", "v231", "a0", "a1", "a2", "a3", "a4", "a5", "a6", "a7", "a8", "a9", "a10", "a11", "a12", "a13", "a14", "a15", "a16", "a17", "a18", "a19", "a20", "a21", "a22", "a23", "a24", "a25", "a26", "a27", "a28", "a29", "a30", "a31", "a32", "a33", "a34", "a35", "a36", "a37", "a38", "a39", "a40", "a41", "a42", "a43", "a44", "a45", "a46", "a47", "a48", "a49", "a50", "a51", "a52", "a53", "a54", "a55", "a56", "a57", "a58", "a59", "a60", "a61", "a62", "a63", "a64", "a65", "a66", "a67", "a68", "a69", "a70", "a71", "a72", "a73", "a74", "a75", "a76", "a77", "a78", "a79", "a80", "a81", "a82", "a83", "a84", "a85", "a86", "a87", "a88", "a89", "a90", "a91", "a92", "a93", "a94", "a95", "a96", "a97", "a98", "a99", "a100", "a101", "a102", "a103", "a104", "a105", "a106", "a107", "a108", "a109", "a110", "a111", "a112", "a113", "a114", "a115", "a116", "a117", "a118", "a119", "a120", "a121", "a122", "a123", "a124", "a125", "a126", "a127", "a128", "a129", "a130", "a131", "a132", "a133", "a134", "a135", "a136", "a137", "a138", "a139", "a140", "a141", "a142", "a143", "a144", "a145", "a146", "a147", "a148", "a149", "a150", "a151", "a152", "a153", "a154", "a155", "a156", "a157", "a158", "a159", "a160", "a161", "a162", "a163", "a164", "a165", "a166", "a167", "a168", "a169", "a170", "a171", "a172", "a173", "a174", "a175", "a176", "a177", "a178", "a179", "a180", "a181", "a182", "a183", "a184", "a185", "a186", "a187", "a188", "a189", "a190", "a191", "a192", "a193", "a194", "a195", "a196", "a197", "a198", "a199", "a200", "a201", "a202", "a203", "a204", "a205", "a206", "a207", "a208", "a209", "a210", "a211", "a212", "a213", "a214", "a215", "a216", "a217", "a218", "a219", "a220", "a221", "a222", "a223", "a224", "a225", "a226", "a227", "a228", "a229", "a230", "a231", "a232", "a233", "a234", "a235", "a236", "a237", "a238", "a239", "a240", "a241", "a242", "a243", "a244", "a245", "a246", "a247", "a248", "a249", "a250", "a251", "a252", "a253", "a254", "a255", "s36", "s37", "s38", "s39", "s40", "s41", "s42", "s43", "s44", "s45", "s46", "s47", "s48", "s49", "s50", "s51", "s52", "s53", "s54", "s55", "s56", "s57", "s58", "s59", "s60", "s61", "s62", "s63", "s64", "s65", "s66", "s67", "s68", "s69", "s70", "s71", "s72", "s73", "s74", "s75", "s76", "s77", "s78", "s79", "s80", "s81", "s82", "s83", "s84", "s85", "s86", "s87", "s88", "s89", "s90", "s91", "s92", "s93", "s94", "s95", "s96", "s97", "vcc", "scc", "memory");
    ep = epOut;
    if (kind == 1 && 5 + RHELP + (rep >> 1) < NR4) { kind = 4; kh = rep & 1; rep = 5 + RHELP + (rep >> 1); }
  }
  if (kind == 4) {
    const unsigned sStep = rep < 5 ? 5 : rep == 5 ? 6 : NR4;
    COMMON(10, (kh * 9 + fj) * 128, 2560, 143360, 2048, 448, 0);
    const int cs = slice;
    const unsigned sOutOff = (kh ? OFF_P1 : OFF_P0) + (cs * 4 + wave) * 1024, sFoutOff = OFF_FP + (kh * 8 + cs) * 128;
    const unsigned sAuxOff = (kh ? OFF_XP : OFF_X1) + (cs * 4 + wave) * 1024, sWw = (kh * 1792 + 16 * wave) * 2048 + cs * 256, sBuf = kh * 57344;
    const int zu = 108 + (rep + 2) % 5;
    const unsigned vSp = lg * 2048 + l15 * 4, vSn = rep >= 5 ? OOB : !ALLZ ? (zu * 16 + wave * 4 + lg) * 2048 + (cs * 2 + kh) * 128 + l15 * 8 :
                         ((rep + 5 * (wave >> 1)) * 16 + (wave & 1) * 8 + (ln >> 3)) * 2048 + (cs * 2 + kh) * 128 + (ln & 7) * 16;
    (void)vWr2;
    const unsigned sExp = __builtin_amdgcn_readfirstlane(ep);
    asm volatile(
R"(.macro M d,w,x
v_mfma_f32_16x16x32_bf16 v[\d:\d+3], \w, v[\x:\x+3], v[\d:\d+3]
.endm
.macro R d,a,o
ds_read_b128 v[\d:\d+3], v\a offset:\o
.endm
.macro T d,a,o
ds_read_b64_tr_b16 \d, v\a offset:\o
.endm
.macro D a,s,o
buffer_load_dwordx4 v\a, s[36:39], s\s offen offset:\o sc1 lds
.endm
.macro L c
s_waitcnt lgkmcnt(\c)
.endm
Lcode_g4_b:
s_mov_b64 s[36:37],%0
s_mov_b32 s38,111243392
s_mov_b32 s39,0x00020000
s_mov_b32 s44,%1
s_mov_b32 s45,%2
s_mov_b32 s46,%3
s_mov_b32 s47,%4
s_mov_b32 s48,%5
s_mov_b32 s49,%6
s_mov_b32 s50,%7
s_mov_b32 s51,%8
s_mov_b32 s93,%9
s_mov_b32 s53,%10
v_mov_b32 v0,%11
v_mov_b32 v3,%12
v_mov_b32 v5,%13
v_mov_b32 v6,%14
v_mov_b32 v10,%15
v_mov_b32 v11,%16
v_mov_b32 v12,%17
v_mov_b32 v13,%18
v_mov_b32 v9,%19
v_mov_b32 v7,%20
s_mov_b64 s[40:41],%21
s_mov_b32 s42,7340032
s_mov_b32 s43,0x00020000
s_lshl_b32 s57,s44,11
s_cmp_eq_u32 s45,175
s_cbranch_scc1 Lg4_pre_%=
s_add_u32 s55,s50,0
s_add_u32 m0,s57,40960
s_nop 0
buffer_load_dwordx4 v10,s[40:43],s55 offen offset:0  lds
s_add_u32 m0,s57,41984
s_nop 0
buffer_load_dwordx4 v11,s[40:43],s55 offen offset:0  lds
s_add_u32 m0,s57,49024
s_nop 0
buffer_load_dwordx4 v10,s[40:43],s55 offen offset:128  lds
s_add_u32 m0,s57,50048
s_nop 0
buffer_load_dwordx4 v11,s[40:43],s55 offen offset:128  lds
s_add_u32 s55,s50,131072
s_add_u32 m0,s57,57344
s_nop 0
buffer_load_dwordx4 v10,s[40:43],s55 offen offset:0  lds
s_add_u32 m0,s57,58368
s_nop 0
buffer_load_dwordx4 v11,s[40:43],s55 offen offset:0  lds
s_add_u32 m0,s57,65408
s_nop 0
buffer_load_dwordx4 v10,s[40:43],s55 offen offset:128  lds
s_add_u32 m0,s57,66432
s_nop 0
buffer_load_dwordx4 v11,s[40:43],s55 offen offset:128  lds
s_add_u32 s55,s50,262144
s_add_u32 m0,s57,73728
s_nop 0
buffer_load_dwordx4 v10,s[40:43],s55 offen offset:0  lds
s_add_u32 m0,s57,74752
s_nop 0
buffer_load_dwordx4 v11,s[40:43],s55 offen offset:0  lds
s_add_u32 m0,s57,81792
s_nop 0
buffer_load_dwordx4 v10,s[40:43],s55 offen offset:128  lds
s_add_u32 m0,s57,82816
s_nop 0
buffer_load_dwordx4 v11,s[40:43],s55 offen offset:128  lds
s_add_u32 s55,s50,393216
s_add_u32 m0,s57,90112
s_nop 0
buffer_load_dwordx4 v10,s[40:43],s55 offen offset:0  lds
s_add_u32 m0,s57,91136
s_nop 0
buffer_load_dwordx4 v11,s[40:43],s55 offen offset:0  lds
s_add_u32 m0,s57,98176
s_nop 0
buffer_load_dwordx4 v10,s[40:43],s55 offen offset:128  lds
s_add_u32 m0,s57,99200
s_nop 0
buffer_load_dwordx4 v11,s[40:43],s55 offen offset:128  lds
s_add_u32 s55,s50,524288
s_add_u32 m0,s57,106496
s_nop 0
buffer_load_dwordx4 v10,s[40:43],s55 offen offset:0  lds
s_add_u32 m0,s57,107520
s_nop 0
buffer_load_dwordx4 v11,s[40:43],s55 offen offset:0  lds
s_add_u32 m0,s57,114560
s_nop 0
buffer_load_dwordx4 v10,s[40:43],s55 offen offset:128  lds
s_add_u32 m0,s57,115584
s_nop 0
buffer_load_dwordx4 v11,s[40:43],s55 offen offset:128  lds
s_add_u32 s55,s50,655360
s_add_u32 m0,s57,122880
s_nop 0
buffer_load_dwordx4 v10,s[40:43],s55 offen offset:0  lds
s_add_u32 m0,s57,123904
s_nop 0
buffer_load_dwordx4 v11,s[40:43],s55 offen offset:0  lds
s_add_u32 m0,s57,130944
s_nop 0
buffer_load_dwordx4 v10,s[40:43],s55 offen offset:128  lds
s_add_u32 m0,s57,131968
s_nop 0
buffer_load_dwordx4 v11,s[40:43],s55 offen offset:128  lds
s_waitcnt vmcnt(20) lgkmcnt(0)
s_barrier
s_add_u32 s55,s50,786432
s_add_u32 m0,s57,139264
s_nop 0
buffer_load_dwordx4 v10,s[40:43],s55 offen offset:0  lds
s_add_u32 m0,s57,140288
s_nop 0
buffer_load_dwordx4 v11,s[40:43],s55 offen offset:0  lds
s_add_u32 m0,s57,147328
s_nop 0
buffer_load_dwordx4 v10,s[40:43],s55 offen offset:128  lds
s_add_u32 m0,s57,148352
s_nop 0
buffer_load_dwordx4 v11,s[40:43],s55 offen offset:128  lds
v_add_u32 v18,40960,v12
v_add_u32 v19,40960,v13
T a[0:1],18,0
T a[2:3],18,512
T a[4:5],19,0
T a[6:7],19,512
T a[8:9],18,4096
T a[10:11],18,4608
T a[12:13],19,4096
T a[14:15],19,4608
s_waitcnt vmcnt(20) lgkmcnt(0)
s_barrier
s_add_u32 s55,s50,917504
s_add_u32 m0,s57,40960
s_nop 0
buffer_load_dwordx4 v10,s[40:43],s55 offen offset:0  lds
s_add_u32 m0,s57,41984
s_nop 0
buffer_load_dwordx4 v11,s[40:43],s55 offen offset:0  lds
s_add_u32 m0,s57,49024
s_nop 0
buffer_load_dwordx4 v10,s[40:43],s55 offen offset:128  lds
s_add_u32 m0,s57,50048
s_nop 0
buffer_load_dwordx4 v11,s[40:43],s55 offen offset:128  lds
v_add_u32 v18,57344,v12
v_add_u32 v19,57344,v13
T a[16:17],18,0
T a[18:19],18,512
T a[20:21],19,0
T a[22:23],19,512
T a[24:25],18,4096
T a[26:27],18,4608
T a[28:29],19,4096
T a[30:31],19,4608
s_waitcnt vmcnt(20) lgkmcnt(0)
s_barrier
s_add_u32 s55,s50,1048576
s_add_u32 m0,s57,57344
s_nop 0
buffer_load_dwordx4 v10,s[40:43],s55 offen offset:0  lds
s_add_u32 m0,s57,58368
s_nop 0
buffer_load_dwordx4 v11,s[40:43],s55 offen offset:0  lds
s_add_u32 m0,s57,65408
s_nop 0
buffer_load_dwordx4 v10,s[40:43],s55 offen offset:128  lds
s_add_u32 m0,s57,66432
s_nop 0
buffer_load_dwordx4 v11,s[40:43],s55 offen offset:128  lds
v_add_u32 v18,73728,v12
v_add_u32 v19,73728,v13
T a[32:33],18,0
T a[34:35],18,512
T a[36:37],19,0
T a[38:39],19,512
T a[40:41],18,4096
T a[42:43],18,4608
T a[44:45],19,4096
T a[46:47],19,4608
s_waitcnt vmcnt(20) lgkmcnt(0)
s_barrier
s_add_u32 s55,s50,1179648
s_add_u32 m0,s57,73728
s_nop 0
buffer_load_dwordx4 v10,s[40:43],s55 offen offset:0  lds
s_add_u32 m0,s57,74752
s_nop 0
buffer_load_dwordx4 v11,s[40:43],s55 offen offset:0  lds
s_add_u32 m0,s57,81792
s_nop 0
buffer_load_dwordx4 v10,s[40:43],s55 offen offset:128  lds
s_add_u32 m0,s57,82816
s_nop 0
buffer_load_dwordx4 v11,s[40:43],s55 offen offset:128  lds
v_add_u32 v18,90112,v12
v_add_u32 v19,90112,v13
T a[48:49],18,0
T a[50:51],18,512
T a[52:53],19,0
T a[54:55],19,512
T a[56:57],18,4096
T a[58:59],18,4608
T a[60:61],19,4096
T a[62:63],19,4608
s_waitcnt vmcnt(20) lgkmcnt(0)
s_barrier
s_add_u32 s55,s50,1310720
s_add_u32 m0,s57,90112
s_nop 0
buffer_load_dwordx4 v10,s[40:43],s55 offen offset:0  lds
s_add_u32 m0,s57,91136
s_nop 0
buffer_load_dwordx4 v11,s[40:43],s55 offen offset:0  lds
s_add_u32 m0,s57,98176
s_nop 0
buffer_load_dwordx4 v10,s[40:43],s55 offen offset:128  lds
s_add_u32 m0,s57,99200
s_nop 0
buffer_load_dwordx4 v11,s[40:43],s55 offen offset:128  lds
v_add_u32 v18,106496,v12
v_add_u32 v19,106496,v13
T a[64:65],18,0
T a[66:67],18,512
T a[68:69],19,0
T a[70:71],19,512
T a[72:73],18,4096
T a[74:75],18,4608
T a[76:77],19,4096
T a[78:79],19,4608
s_waitcnt vmcnt(20) lgkmcnt(0)
s_barrier
s_add_u32 s55,s50,1441792
s_add_u32 m0,s57,106496
s_nop 0
buffer_load_dwordx4 v10,s[40:43],s55 offen offset:0  lds
s_add_u32 m0,s57,107520
s_nop 0
buffer_load_dwordx4 v11,s[40:43],s55 offen offset:0  lds
s_add_u32 m0,s57,114560
s_nop 0
buffer_load_dwordx4 v10,s[40:43],s55 offen offset:128  lds
s_add_u32 m0,s57,115584
s_nop 0
buffer_load_dwordx4 v11,s[40:43],s55 offen offset:128  lds
v_add_u32 v18,122880,v12
v_add_u32 v19,122880,v13
T a[80:81],18,0
T a[82:83],18,512
T a[84:85],19,0
T a[86:87],19,512
T a[88:89],18,4096
T a[90:91],18,4608
T a[92:93],19,4096
T a[94:95],19,4608
s_waitcnt vmcnt(20) lgkmcnt(0)
s_barrier
s_add_u32 s55,s50,1572864
s_add_u32 m0,s57,122880
s_nop 0
buffer_load_dwordx4 v10,s[40:43],s55 offen offset:0  lds
s_add_u32 m0,s57,123904
s_nop 0
buffer_load_dwordx4 v11,s[40:43],s55 offen offset:0  lds
s_add_u32 m0,s57,130944
s_nop 0
buffer_load_dwordx4 v10,s[40:43],s55 offen offset:128  lds
s_add_u32 m0,s57,131968
s_nop 0
buffer_load_dwordx4 v11,s[40:43],s55 offen offset:128  lds
v_add_u32 v18,139264,v12
v_add_u32 v19,139264,v13
T a[96:97],18,0
T a[98:99],18,512
T a[100:101],19,0
T a[102:103],19,512
T a[104:105],18,4096
T a[106:107],18,4608
T a[108:109],19,4096
T a[110:111],19,4608
s_waitcnt vmcnt(20) lgkmcnt(0)
s_barrier
s_add_u32 s55,s50,1703936
s_add_u32 m0,s57,139264
s_nop 0
buffer_load_dwordx4 v10,s[40:43],s55 offen offset:0  lds
s_add_u32 m0,s57,140288
s_nop 0
buffer_load_dwordx4 v11,s[40:43],s55 offen offset:0  lds
s_add_u32 m0,s57,147328
s_nop 0
buffer_load_dwordx4 v10,s[40:43],s55 offen offset:128  lds
s_add_u32 m0,s57,148352
s_nop 0
buffer_load_dwordx4 v11,s[40:43],s55 offen offset:128  lds
v_add_u32 v18,40960,v12
v_add_u32 v19,40960,v13
T a[112:113],18,0
T a[114:115],18,512
T a[116:117],19,0
T a[118:119],19,512
T a[120:121],18,4096
T a[122:123],18,4608
T a[124:125],19,4096
T a[126:127],19,4608
s_waitcnt vmcnt(20) lgkmcnt(0)
s_barrier
s_add_u32 s55,s50,1835008
s_add_u32 m0,s57,40960
s_nop 0
buffer_load_dwordx4 v10,s[40:43],s55 offen offset:0  lds
s_add_u32 m0,s57,41984
s_nop 0
buffer_load_dwordx4 v11,s[40:43],s55 offen offset:0  lds
s_add_u32 m0,s57,49024
s_nop 0
buffer_load_dwordx4 v10,s[40:43],s55 offen offset:128  lds
s_add_u32 m0,s57,50048
s_nop 0
buffer_load_dwordx4 v11,s[40:43],s55 offen offset:128  lds
v_add_u32 v18,57344,v12
v_add_u32 v19,57344,v13
T a[128:129],18,0
T a[130:131],18,512
T a[132:133],19,0
T a[134:135],19,512
T a[136:137],18,4096
T a[138:139],18,4608
T a[140:141],19,4096
T a[142:143],19,4608
s_waitcnt vmcnt(20) lgkmcnt(0)
s_barrier
s_add_u32 s55,s50,1966080
s_add_u32 m0,s57,57344
s_nop 0
buffer_load_dwordx4 v10,s[40:43],s55 offen offset:0  lds
s_add_u32 m0,s57,58368
s_nop 0
buffer_load_dwordx4 v11,s[40:43],s55 offen offset:0  lds
s_add_u32 m0,s57,65408
s_nop 0
buffer_load_dwordx4 v10,s[40:43],s55 offen offset:128  lds
s_add_u32 m0,s57,66432
s_nop 0
buffer_load_dwordx4 v11,s[40:43],s55 offen offset:128  lds
v_add_u32 v18,73728,v12
v_add_u32 v19,73728,v13
T a[144:145],18,0
T a[146:147],18,512
T a[148:149],19,0
T a[150:151],19,512
T a[152:153],18,4096
T a[154:155],18,4608
T a[156:157],19,4096
T a[158:159],19,4608
s_waitcnt vmcnt(20) lgkmcnt(0)
s_barrier
s_add_u32 s55,s50,2097152
s_add_u32 m0,s57,73728
s_nop 0
buffer_load_dwordx4 v10,s[40:43],s55 offen offset:0  lds
s_add_u32 m0,s57,74752
s_nop 0
buffer_load_dwordx4 v11,s[40:43],s55 offen offset:0  lds
s_add_u32 m0,s57,81792
s_nop 0
buffer_load_dwordx4 v10,s[40:43],s55 offen offset:128  lds
s_add_u32 m0,s57,82816
s_nop 0
buffer_load_dwordx4 v11,s[40:43],s55 offen offset:128  lds
v_add_u32 v18,90112,v12
v_add_u32 v19,90112,v13
T a[160:161],18,0
T a[162:163],18,512
T a[164:165],19,0
T a[166:167],19,512
T a[168:169],18,4096
T a[170:171],18,4608
T a[172:173],19,4096
T a[174:175],19,4608
s_waitcnt vmcnt(20) lgkmcnt(0)
s_barrier
s_add_u32 s55,s50,2228224
s_add_u32 m0,s57,90112
s_nop 0
buffer_load_dwordx4 v10,s[40:43],s55 offen offset:0  lds
s_add_u32 m0,s57,91136
s_nop 0
buffer_load_dwordx4 v11,s[40:43],s55 offen offset:0  lds
s_add_u32 m0,s57,98176
s_nop 0
buffer_load_dwordx4 v10,s[40:43],s55 offen offset:128  lds
s_add_u32 m0,s57,99200
s_nop 0
buffer_load_dwordx4 v11,s[40:43],s55 offen offset:128  lds
v_add_u32 v18,106496,v12
v_add_u32 v19,106496,v13
T a[176:177],18,0
T a[178:179],18,512
T a[180:181],19,0
T a[182:183],19,512
T a[184:185],18,4096
T a[186:187],18,4608
T a[188:189],19,4096
T a[190:191],19,4608
s_waitcnt vmcnt(20) lgkmcnt(0)
s_barrier
s_add_u32 s55,s50,2359296
s_add_u32 m0,s57,106496
s_nop 0
buffer_load_dwordx4 v10,s[40:43],s55 offen offset:0  lds
s_add_u32 m0,s57,107520
s_nop 0
buffer_load_dwordx4 v11,s[40:43],s55 offen offset:0  lds
s_add_u32 m0,s57,114560
s_nop 0
buffer_load_dwordx4 v10,s[40:43],s55 offen offset:128  lds
s_add_u32 m0,s57,115584
s_nop 0
buffer_load_dwordx4 v11,s[40:43],s55 offen offset:128  lds
v_add_u32 v18,122880,v12
v_add_u32 v19,122880,v13
T a[192:193],18,0
T a[194:195],18,512
T a[196:197],19,0
T a[198:199],19,512
T a[200:201],18,4096
T a[202:203],18,4608
T a[204:205],19,4096
T a[206:207],19,4608
s_waitcnt vmcnt(20) lgkmcnt(0)
s_barrier
s_add_u32 s55,s50,2490368
s_add_u32 m0,s57,122880
s_nop 0
buffer_load_dwordx4 v10,s[40:43],s55 offen offset:0  lds
s_add_u32 m0,s57,123904
s_nop 0
buffer_load_dwordx4 v11,s[40:43],s55 offen offset:0  lds
s_add_u32 m0,s57,130944
s_nop 0
buffer_load_dwordx4 v10,s[40:43],s55 offen offset:128  lds
s_add_u32 m0,s57,131968
s_nop 0
buffer_load_dwordx4 v11,s[40:43],s55 offen offset:128  lds
v_add_u32 v18,139264,v12
v_add_u32 v19,139264,v13
T a[208:209],18,0
T a[210:211],18,512
T a[212:213],19,0
T a[214:215],19,512
T a[216:217],18,4096
T a[218:219],18,4608
T a[220:221],19,4096
T a[222:223],19,4608
s_waitcnt vmcnt(20) lgkmcnt(0)
s_barrier
s_add_u32 s55,s50,2621440
s_add_u32 m0,s57,139264
s_nop 0
buffer_load_dwordx4 v10,s[40:43],s55 offen offset:0  lds
s_add_u32 m0,s57,140288
s_nop 0
buffer_load_dwordx4 v11,s[40:43],s55 offen offset:0  lds
s_add_u32 m0,s57,147328
s_nop 0
buffer_load_dwordx4 v10,s[40:43],s55 offen offset:128  lds
s_add_u32 m0,s57,148352
s_nop 0
buffer_load_dwordx4 v11,s[40:43],s55 offen offset:128  lds
v_add_u32 v18,40960,v12
v_add_u32 v19,40960,v13
T a[224:225],18,0
T a[226:227],18,512
T a[228:229],19,0
T a[230:231],19,512
T a[232:233],18,4096
T a[234:235],18,4608
T a[236:237],19,4096
T a[238:239],19,4608
s_waitcnt vmcnt(20) lgkmcnt(0)
s_barrier
)"
R"(s_add_u32 s55,s50,2752512
s_add_u32 m0,s57,40960
s_nop 0
buffer_load_dwordx4 v10,s[40:43],s55 offen offset:0  lds
s_add_u32 m0,s57,41984
s_nop 0
buffer_load_dwordx4 v11,s[40:43],s55 offen offset:0  lds
s_add_u32 m0,s57,49024
s_nop 0
buffer_load_dwordx4 v10,s[40:43],s55 offen offset:128  lds
s_add_u32 m0,s57,50048
s_nop 0
buffer_load_dwordx4 v11,s[40:43],s55 offen offset:128  lds
v_add_u32 v18,57344,v12
v_add_u32 v19,57344,v13
T a[240:241],18,0
T a[242:243],18,512
T a[244:245],19,0
T a[246:247],19,512
T a[248:249],18,4096
T a[250:251],18,4608
T a[252:253],19,4096
T a[254:255],19,4608
Lg4_pre_%=:
s_waitcnt vmcnt(20) lgkmcnt(0)
s_barrier
s_add_u32 s55,s50,2883584
s_add_u32 m0,s57,57344
s_nop 0
buffer_load_dwordx4 v10,s[40:43],s55 offen offset:0  lds
s_add_u32 m0,s57,58368
s_nop 0
buffer_load_dwordx4 v11,s[40:43],s55 offen offset:0  lds
s_add_u32 m0,s57,65408
s_nop 0
buffer_load_dwordx4 v10,s[40:43],s55 offen offset:128  lds
s_add_u32 m0,s57,66432
s_nop 0
buffer_load_dwordx4 v11,s[40:43],s55 offen offset:128  lds
v_add_u32 v18,73728,v12
v_add_u32 v19,73728,v13
T v[64:65],18,0
T v[66:67],18,512
T v[68:69],19,0
T v[70:71],19,512
T v[72:73],18,4096
T v[74:75],18,4608
T v[76:77],19,4096
T v[78:79],19,4608
s_waitcnt vmcnt(20) lgkmcnt(0)
s_barrier
s_add_u32 s55,s50,3014656
s_add_u32 m0,s57,73728
s_nop 0
buffer_load_dwordx4 v10,s[40:43],s55 offen offset:0  lds
s_add_u32 m0,s57,74752
s_nop 0
buffer_load_dwordx4 v11,s[40:43],s55 offen offset:0  lds
s_add_u32 m0,s57,81792
s_nop 0
buffer_load_dwordx4 v10,s[40:43],s55 offen offset:128  lds
s_add_u32 m0,s57,82816
s_nop 0
buffer_load_dwordx4 v11,s[40:43],s55 offen offset:128  lds
v_add_u32 v18,90112,v12
v_add_u32 v19,90112,v13
T v[80:81],18,0
T v[82:83],18,512
T v[84:85],19,0
T v[86:87],19,512
T v[88:89],18,4096
T v[90:91],18,4608
T v[92:93],19,4096
T v[94:95],19,4608
s_waitcnt vmcnt(20) lgkmcnt(0)
s_barrier
s_add_u32 s55,s50,3145728
s_add_u32 m0,s57,90112
s_nop 0
buffer_load_dwordx4 v10,s[40:43],s55 offen offset:0  lds
s_add_u32 m0,s57,91136
s_nop 0
buffer_load_dwordx4 v11,s[40:43],s55 offen offset:0  lds
s_add_u32 m0,s57,98176
s_nop 0
buffer_load_dwordx4 v10,s[40:43],s55 offen offset:128  lds
s_add_u32 m0,s57,99200
s_nop 0
buffer_load_dwordx4 v11,s[40:43],s55 offen offset:128  lds
v_add_u32 v18,106496,v12
v_add_u32 v19,106496,v13
T v[96:97],18,0
T v[98:99],18,512
T v[100:101],19,0
T v[102:103],19,512
T v[104:105],18,4096
T v[106:107],18,4608
T v[108:109],19,4096
T v[110:111],19,4608
s_waitcnt vmcnt(20) lgkmcnt(0)
s_barrier
s_add_u32 s55,s50,3276800
s_add_u32 m0,s57,106496
s_nop 0
buffer_load_dwordx4 v10,s[40:43],s55 offen offset:0  lds
s_add_u32 m0,s57,107520
s_nop 0
buffer_load_dwordx4 v11,s[40:43],s55 offen offset:0  lds
s_add_u32 m0,s57,114560
s_nop 0
buffer_load_dwordx4 v10,s[40:43],s55 offen offset:128  lds
s_add_u32 m0,s57,115584
s_nop 0
buffer_load_dwordx4 v11,s[40:43],s55 offen offset:128  lds
v_add_u32 v18,122880,v12
v_add_u32 v19,122880,v13
T v[112:113],18,0
T v[114:115],18,512
T v[116:117],19,0
T v[118:119],19,512
T v[120:121],18,4096
T v[122:123],18,4608
T v[124:125],19,4096
T v[126:127],19,4608
s_waitcnt vmcnt(20) lgkmcnt(0)
s_barrier
s_add_u32 s55,s50,3407872
s_add_u32 m0,s57,122880
s_nop 0
buffer_load_dwordx4 v10,s[40:43],s55 offen offset:0  lds
s_add_u32 m0,s57,123904
s_nop 0
buffer_load_dwordx4 v11,s[40:43],s55 offen offset:0  lds
s_add_u32 m0,s57,130944
s_nop 0
buffer_load_dwordx4 v10,s[40:43],s55 offen offset:128  lds
s_add_u32 m0,s57,131968
s_nop 0
buffer_load_dwordx4 v11,s[40:43],s55 offen offset:128  lds
v_add_u32 v18,139264,v12
v_add_u32 v19,139264,v13
T v[128:129],18,0
T v[130:131],18,512
T v[132:133],19,0
T v[134:135],19,512
T v[136:137],18,4096
T v[138:139],18,4608
T v[140:141],19,4096
T v[142:143],19,4608
s_waitcnt vmcnt(20) lgkmcnt(0)
s_barrier
s_add_u32 s55,s50,3538944
s_add_u32 m0,s57,139264
s_nop 0
buffer_load_dwordx4 v10,s[40:43],s55 offen offset:0  lds
s_add_u32 m0,s57,140288
s_nop 0
buffer_load_dwordx4 v11,s[40:43],s55 offen offset:0  lds
s_add_u32 m0,s57,147328
s_nop 0
buffer_load_dwordx4 v10,s[40:43],s55 offen offset:128  lds
s_add_u32 m0,s57,148352
s_nop 0
buffer_load_dwordx4 v11,s[40:43],s55 offen offset:128  lds
v_add_u32 v18,40960,v12
v_add_u32 v19,40960,v13
T v[144:145],18,0
T v[146:147],18,512
T v[148:149],19,0
T v[150:151],19,512
T v[152:153],18,4096
T v[154:155],18,4608
T v[156:157],19,4096
T v[158:159],19,4608
s_waitcnt vmcnt(20) lgkmcnt(0)
s_barrier
v_add_u32 v18,57344,v12
v_add_u32 v19,57344,v13
T v[160:161],18,0
T v[162:163],18,512
T v[164:165],19,0
T v[166:167],19,512
T v[168:169],18,4096
T v[170:171],18,4608
T v[172:173],19,4096
T v[174:175],19,4608
s_waitcnt vmcnt(16) lgkmcnt(0)
s_barrier
v_add_u32 v18,73728,v12
v_add_u32 v19,73728,v13
T v[176:177],18,0
T v[178:179],18,512
T v[180:181],19,0
T v[182:183],19,512
T v[184:185],18,4096
T v[186:187],18,4608
T v[188:189],19,4096
T v[190:191],19,4608
s_waitcnt vmcnt(12) lgkmcnt(0)
s_barrier
v_add_u32 v18,90112,v12
v_add_u32 v19,90112,v13
T v[192:193],18,0
T v[194:195],18,512
T v[196:197],19,0
T v[198:199],19,512
T v[200:201],18,4096
T v[202:203],18,4608
T v[204:205],19,4096
T v[206:207],19,4608
s_waitcnt vmcnt(8) lgkmcnt(0)
s_barrier
v_add_u32 v18,106496,v12
v_add_u32 v19,106496,v13
T v[208:209],18,0
T v[210:211],18,512
T v[212:213],19,0
T v[214:215],19,512
T v[216:217],18,4096
T v[218:219],18,4608
T v[220:221],19,4096
T v[222:223],19,4608
s_waitcnt vmcnt(4) lgkmcnt(0)
s_barrier
v_add_u32 v18,122880,v12
v_add_u32 v19,122880,v13
T v[224:225],18,0
T v[226:227],18,512
T v[228:229],19,0
T v[230:231],19,512
T v[232:233],18,4096
T v[234:235],18,4608
T v[236:237],19,4096
T v[238:239],19,4608
s_waitcnt vmcnt(0) lgkmcnt(0)
s_barrier
v_add_u32 v18,139264,v12
v_add_u32 v19,139264,v13
T v[240:241],18,0
T v[242:243],18,512
T v[244:245],19,0
T v[246:247],19,512
T v[248:249],18,4096
T v[250:251],18,4608
T v[252:253],19,4096
T v[254:255],19,4608
L 0
s_barrier
v_mov_b32 v4,s53
s_mov_b32 s82,0
s_mov_b32 s80,0
s_mov_b32 s54,s45
s_mul_i32 s57,s44,7168
s_mul_i32 s55,s45,114688
s_add_u32 s55,s55,s57
s_add_u32 s55,s55,40894464
s_mul_i32 s57,s44,7168
s_mul_i32 s60,s46,114688
s_mul_i32 s61,s46,2560
s_mul_i32 s62,s46,32768
s_mul_i32 s63,s46,2048
s_mul_i32 s64,s46,512
s_lshl_b32 s59,s44,8
s_add_u32 s59,s59,143360
s_mul_i32 s58,s45,2560
s_add_u32 s58,s58,109891584
s_mul_i32 s65,s45,32768
s_add_u32 s65,s65,s47
s_mul_i32 s68,s45,2048
s_add_u32 s68,s68,s48
s_mul_i32 s73,s45,512
s_add_u32 s73,s73,106954752
s_mul_i32 s74,s45,32768
s_add_u32 s74,s74,s49
s_mul_i32 s75,s45,1024
s_add_u32 s75,s75,107479040
s_mov_b32 s66,0x7fff0000
s_mov_b32 s67,0x7fff0000
s_mov_b32 s69,0x7fff0000
s_mov_b32 s76,0x7fff0000
s_mov_b32 s77,0x7fff0000
s_mov_b32 s70,0x7fff0000
s_mov_b32 s71,0x7fff0000
s_mov_b32 s72,0x7fff0000
v_mov_b32 v1,v0
v_add_u32 v2,28672,v0
s_mov_b32 s84,28672
s_mov_b32 s86,0x3a800000
s_mov_b32 s87,0x3727c5ac
s_mov_b32 s88,0xc0135761
s_mov_b32 s89,0x3d372713
s_lshr_b32 s85,s44,1
s_mul_i32 s85,s85,127
s_add_u32 s55,s55,s51
s_mul_i32 s78,s45,114688
s_sub_u32 s60,s55,s78
s_add_u32 s50,s93,224
s_add_u32 s93,s93,170
s_cmp_lt_u32 s45,224
s_cselect_b32 s63,224,-1
s_cmp_lt_u32 s45,170
s_cselect_b32 s63,170,s63
s_cselect_b32 s93,s93,s50
s_cselect_b32 s88,6,8
s_cselect_b32 s89,224,-1
v_lshlrev_b32 v8,22,v0
v_and_b32 v8,0x80000000,v8
v_or_b32 v8,v8,v5
v_add_u32 v2,57344,v0
s_lshl_b32 s84,s57,1
s_add_u32 s84,s84,57344
s_mov_b64 s[86:87],%22
s_mov_b64 s[40:41],%22
s_mov_b32 s42,4194304
s_and_b32 s78,s47,65535
s_lshr_b32 s78,s78,4
s_sub_u32 s85,s47,s78
s_add_u32 s85,s85,4194304
s_add_u32 s45,s47,5832704
s_mul_i32 s73,s44,1280
s_add_u32 s73,s73,114688
v_lshrrev_b32 v62,4,v9
v_and_b32 v62,3,v62
v_mul_u32_u24 v62,288,v62
v_lshrrev_b32 v63,7,v9
v_and_b32 v5,12,v9
v_add3_u32 v5,v5,v62,v63
v_add_u32 v5,s73,v5
v_mov_b32 v10,0
v_mov_b32 v11,0
v_mov_b32 v12,0
v_mov_b32 v13,0
s_mov_b32 s79,1638400
buffer_store_dwordx4 v[10:13],v7,s[40:43],s79 offen offset:0 sc0 sc1
s_mov_b32 s79,1966080
buffer_store_dwordx4 v[10:13],v7,s[40:43],s79 offen offset:0 sc0 sc1
s_mov_b32 s79,2293760
buffer_store_dwordx4 v[10:13],v7,s[40:43],s79 offen offset:0 sc0 sc1
s_mov_b32 s79,2621440
buffer_store_dwordx4 v[10:13],v7,s[40:43],s79 offen offset:0 sc0 sc1
s_mov_b32 s79,2949120
buffer_store_dwordx4 v[10:13],v7,s[40:43],s79 offen offset:0 sc0 sc1
s_mov_b32 s79,3276800
buffer_store_dwordx4 v[10:13],v7,s[40:43],s79 offen offset:0 sc0 sc1
s_mov_b32 s79,3604480
buffer_store_dwordx4 v[10:13],v7,s[40:43],s79 offen offset:0 sc0 sc1
s_mov_b32 s79,3932160
buffer_store_dwordx4 v[10:13],v7,s[40:43],s79 offen offset:0 sc0 sc1
s_getpc_b64 s[40:41]
Lg4_pc_%=:
s_lshl_b32 s78,s44,13
v_lshlrev_b32 v62,3,v0
v_add_u32 v62,s78,v62
v_min_u32 v62,Lcode_g4_e-Lg4_pc_%=-4,v62
s_mov_b32 m0,163584
s_nop 0
global_load_lds_dword v62,s[40:41]
s_add_u32 m0,s59,4096
s_nop 0
buffer_load_dword v8,s[36:39],s58 offen offset:0 sc1 lds
buffer_store_dword v4,v3,s[36:39],s71 offen offset:0 sc0 sc1
s_add_u32 m0,s59,5120
s_nop 0
buffer_load_dword v8,s[36:39],s58 offen offset:0 sc1 lds
buffer_store_dword v4,v3,s[36:39],s71 offen offset:0 sc0 sc1
s_mov_b32 s83,0
Lg4_poll_%=:
s_add_u32 s78,s83,2
s_and_b32 s78,s78,3
s_lshl_b32 s78,s78,10
s_add_u32 s78,s78,4096
s_add_u32 m0,s59,s78
s_nop 0
buffer_load_dword v8,s[36:39],s58 offen offset:0 sc1 lds
s_and_b32 s78,s83,3
s_lshl_b32 s78,s78,10
v_add_u32 v62,s78,v6
s_waitcnt vmcnt(4)
s_barrier
ds_read_b32 v7,v62 offset:4096
buffer_store_dword v4,v3,s[36:39],s70 offen offset:0 sc0 sc1
s_mov_b32 s70,s71
s_mov_b32 s71,s72
s_mov_b32 s72,0x7fff0000
s_add_u32 s83,s83,1
s_add_u32 s82,s82,1
L 0
v_cmp_eq_u32 vcc,s53,v7
s_nop 0
s_cmp_eq_u32 vcc_lo,-1
s_cbranch_scc1 Lg4_go_%=
s_cmp_lt_u32 s82,20000
s_cbranch_scc1 Lg4_poll_%=
Lg4_go_%=:
s_add_u32 m0,s57,0
s_add_u32 s56,s55,0
D 0,56,0
D 0,56,1024
D 0,56,2048
D 0,56,3072
s_add_u32 m0,s57,4096
s_add_u32 s56,s56,4096
D 0,56,0
D 0,56,1024
D 0,56,2048
s_add_u32 s61,s54,s46
s_cmp_ge_u32 s61,s63
s_cselect_b32 s61,s93,s61
s_cselect_b32 s46,s88,s46
s_cselect_b32 s63,s89,s63
s_cselect_b32 s93,s50,s93
s_cselect_b32 s88,8,s88
s_cselect_b32 s89,-1,s89
s_mul_i32 s58,s61,2560
s_add_u32 s58,s58,109891584
s_add_u32 m0,s59,2048
s_nop 0
buffer_load_dword v8,s[36:39],s58 offen offset:0 sc1 lds
s_add_u32 m0,s57,28672
s_add_u32 s56,s55,28672
D 0,56,0
D 0,56,1024
D 0,56,2048
D 0,56,3072
s_add_u32 m0,s57,32768
s_add_u32 s56,s56,4096
D 0,56,0
D 0,56,1024
D 0,56,2048
s_mul_i32 s55,s61,114688
s_add_u32 s55,s55,s60
s_sub_u32 s57,s84,s57
s_mov_b32 s54,s61
s_waitcnt vmcnt(7)
s_barrier
buffer_store_dword v4,v3,s[36:39],s70 offen offset:0 sc0 sc1
s_min_u32 s71,s71,s72
buffer_store_dword v4,v3,s[36:39],s71 offen offset:0 sc0 sc1
s_mov_b32 s70,0x7fff0000
s_mov_b32 s71,0x7fff0000
s_mov_b32 s72,0x7fff0000
s_cmp_eq_u32 s80,1
s_cbranch_scc1 Lg4_st1_%=
s_branch Lg4_st0_%=
Lg4_gox_%=:
s_add_u32 m0,s59,2048
s_nop 0
buffer_load_dword v8,s[36:39],s58 offen offset:0 sc1 lds
s_waitcnt vmcnt(0)
s_barrier
buffer_store_dword v4,v3,s[36:39],s70 offen offset:0 sc0 sc1
s_min_u32 s71,s71,s72
buffer_store_dword v4,v3,s[36:39],s71 offen offset:0 sc0 sc1
s_mov_b32 s70,0x7fff0000
s_mov_b32 s71,0x7fff0000
s_mov_b32 s72,0x7fff0000
s_cmp_eq_u32 s80,1
s_cbranch_scc1 Lg4_st1_%=
Lg4_st0_%=:
ds_read_b32 v7,v6 offset:2048
R 26,1,0
R 30,1,1024
R 34,1,2048
R 38,1,3072
R 42,1,4096
L 5
v_cmp_eq_u32 vcc,s53,v7
L 3
s_cmp_eq_u32 vcc_lo,-1
s_cbranch_scc1 Lg4_F0_%=
s_branch Lg4_L0_%=
Lg4_st1_%=:
ds_read_b32 v7,v6 offset:2048
R 26,2,0
R 30,2,1024
R 34,2,2048
R 38,2,3072
R 42,2,4096
L 5
v_cmp_eq_u32 vcc,s53,v7
L 3
s_cmp_eq_u32 vcc_lo,-1
s_cbranch_scc1 Lg4_F1_%=
s_branch Lg4_L1_%=
Lg4_F0_%=:
v_mfma_f32_16x16x32_bf16 v[14:17],a[4:7],v[26:29],0
s_add_u32 m0,s57,0
R 46,1,5120
v_lshlrev_b32 v62,16,v58
M 14,a[12:15],30
s_add_u32 s56,s55,0
v_add_f32 v18,v18,v62
v_and_b32 v63,0xffff0000,v58
v_mfma_f32_16x16x32_bf16 v[10:13],a[0:3],v[26:29],0
s_add_u32 s61,s54,s46
R 50,1,6144
v_add_f32 v19,v19,v63
M 10,a[8:11],30
L 3
D 0,56,0
)"
R"(M 10,a[16:19],34
s_cmp_ge_u32 s61,s63
R 54,1,7168
v_lshlrev_b32 v62,16,v59
M 10,a[24:27],38
s_cselect_b32 s61,s93,s61
v_add_f32 v20,v20,v62
v_and_b32 v63,0xffff0000,v59
M 14,a[20:23],34
s_cselect_b32 s46,s88,s46
R 26,1,8192
v_add_f32 v21,v21,v63
M 14,a[28:31],38
L 3
v_lshlrev_b32 v62,16,v60
v_add_f32 v22,v22,v62
M 14,a[36:39],42
s_cselect_b32 s63,s89,s63
R 30,1,9216
v_and_b32 v63,0xffff0000,v60
M 14,a[44:47],46
D 0,56,1024
M 10,a[32:35],42
s_cselect_b32 s93,s50,s93
R 34,1,10240
v_add_f32 v23,v23,v63
M 10,a[40:43],46
L 3
v_lshlrev_b32 v62,16,v61
v_add_f32 v24,v24,v62
M 10,a[48:51],50
s_cselect_b32 s88,8,s88
R 38,1,11264
v_and_b32 v63,0xffff0000,v61
M 10,a[56:59],54
buffer_load_dwordx4 v[58:61],v0,s[36:39],s74 offen offset:0 sc1
M 14,a[52:55],50
s_cselect_b32 s89,-1,s89
R 42,1,12288
v_add_f32 v25,v25,v63
M 14,a[60:63],54
L 3
v_cvt_pk_bf16_f32 v18,v18,v19
v_cvt_pk_bf16_f32 v19,v20,v21
M 14,a[68:71],26
s_mul_i32 s58,s61,2560
R 46,1,13312
v_cvt_pk_bf16_f32 v20,v22,v23
M 14,a[76:79],30
D 0,56,2048
M 10,a[64:67],26
s_add_u32 s58,s58,109891584
R 50,1,14336
v_cvt_pk_bf16_f32 v21,v24,v25
M 10,a[72:75],30
L 3
buffer_store_dwordx4 v[18:21],v0,s[36:39],s67 offen offset:0 sc0 sc1
M 10,a[80:83],34
s_lshl_b32 s62,s54,15
s_mov_b32 s66,s65
R 54,1,15360
M 10,a[88:91],38
s_cmp_ge_u32 s66,s45
s_cselect_b32 s51,1,-1
s_add_u32 s74,s49,s62
M 14,a[84:87],34
s_add_u32 s65,s47,s62
s_mov_b32 s54,s61
R 26,1,16384
M 14,a[92:95],38
L 3
M 14,a[100:103],42
R 30,1,17408
M 14,a[108:111],46
D 0,56,3072
M 10,a[96:99],42
s_add_u32 m0,s57,4096
s_add_u32 s56,s56,4096
R 34,1,18432
M 10,a[104:107],46
L 3
M 10,a[112:115],50
R 38,1,19456
M 10,a[120:123],54
M 14,a[116:119],50
R 42,1,20480
M 14,a[124:127],54
L 3
D 0,56,0
M 14,a[132:135],26
R 46,1,21504
M 14,a[140:143],30
M 10,a[128:131],26
R 50,1,22528
M 10,a[136:139],30
L 3
M 10,a[144:147],34
R 54,1,23552
M 10,a[152:155],38
M 14,a[148:151],34
R 26,1,24576
M 14,a[156:159],38
L 3
D 0,56,1024
M 14,a[164:167],42
R 30,1,25600
M 14,a[172:175],46
M 10,a[160:163],42
R 34,1,26624
M 10,a[168:171],46
L 3
M 10,a[176:179],50
s_waitcnt vmcnt(9)
R 38,1,27648
M 10,a[184:187],54
s_barrier
M 14,a[180:183],50
R 42,1,28672
M 14,a[188:191],54
L 3
D 0,56,2048
M 14,a[196:199],26
s_add_u32 m0,s59,0
s_add_u32 s56,s55,28672
R 46,1,29696
M 14,a[204:207],30
s_mul_i32 s55,s61,114688
s_add_u32 s55,s55,s60
M 10,a[192:195],26
R 50,1,30720
M 10,a[200:203],30
L 3
M 10,a[208:211],34
R 54,1,31744
M 10,a[216:219],38
M 14,a[212:215],34
R 26,1,32768
M 14,a[220:223],38
L 3
buffer_load_dword v8,s[36:39],s58 offen offset:0 sc1 lds
M 14,a[228:231],42
s_add_u32 m0,s57,28672
R 30,1,33792
M 14,a[236:239],46
D 0,56,0
M 10,a[224:227],42
R 34,1,34816
M 10,a[232:235],46
L 3
M 10,a[240:243],50
R 38,1,35840
M 10,a[248:251],54
M 14,a[244:247],50
R 42,1,36864
M 14,a[252:255],54
L 3
D 0,56,1024
M 14,v[68:71],26
R 46,1,37888
M 14,v[76:79],30
M 10,v[64:67],26
R 50,1,38912
M 10,v[72:75],30
L 3
M 10,v[80:83],34
R 54,1,39936
M 10,v[88:91],38
D 0,56,2048
M 14,v[84:87],34
R 26,1,40960
M 14,v[92:95],38
L 3
M 14,v[100:103],42
R 30,1,41984
M 14,v[108:111],46
M 10,v[96:99],42
R 34,1,43008
M 10,v[104:107],46
L 3
D 0,56,3072
M 10,v[112:115],50
s_add_u32 m0,s57,32768
s_add_u32 s56,s56,4096
R 38,1,44032
M 10,v[120:123],54
s_sub_u32 s57,s84,s57
M 14,v[116:119],50
R 42,1,45056
M 14,v[124:127],54
L 3
M 14,v[132:135],26
R 46,1,46080
M 14,v[140:143],30
D 0,56,0
M 10,v[128:131],26
R 50,1,47104
M 10,v[136:139],30
L 3
M 10,v[144:147],34
R 54,1,48128
M 10,v[152:155],38
M 14,v[148:151],34
R 26,1,49152
M 14,v[156:159],38
L 3
D 0,56,1024
M 14,v[164:167],42
R 30,1,50176
M 14,v[172:175],46
M 10,v[160:163],42
R 34,1,51200
M 10,v[168:171],46
L 3
M 10,v[176:179],50
R 38,1,52224
M 10,v[184:187],54
D 0,56,2048
M 14,v[180:183],50
R 42,1,53248
M 14,v[188:191],54
L 3
M 14,v[196:199],26
R 46,1,54272
M 14,v[204:207],30
M 10,v[192:195],26
R 50,1,55296
M 10,v[200:203],30
L 3
M 10,v[208:211],34
s_waitcnt vmcnt(7)
R 54,1,56320
M 10,v[216:219],38
s_barrier
ds_read_b32 v7,v6 offset:0
M 14,v[212:215],34
R 26,2,0
M 14,v[220:223],38
L 4
buffer_store_dword v4,v3,s[36:39],s69 offen offset:0 sc0 sc1
M 14,v[228:231],42
s_mov_b32 s69,s68
s_lshr_b32 s68,s62,4
R 30,2,1024
M 14,v[236:239],46
s_add_u32 s68,s68,s48
M 10,v[224:227],42
R 34,2,2048
M 10,v[232:235],46
L 4
L 3
M 10,v[240:243],50
R 38,2,3072
v_cmp_eq_u32 vcc,s53,v7
M 10,v[248:251],54
M 14,v[244:247],50
L 2
s_cmp_eq_u32 vcc_lo,s51
R 42,2,4096
M 14,v[252:255],54
s_cbranch_scc0 Lg4_K1_%=
Lg4_F1_%=:
v_mfma_f32_16x16x32_bf16 v[22:25],a[4:7],v[26:29],0
s_add_u32 m0,s57,0
R 46,2,5120
v_lshlrev_b32 v62,16,v58
M 22,a[12:15],30
s_add_u32 s56,s55,0
v_add_f32 v10,v10,v62
v_and_b32 v63,0xffff0000,v58
v_mfma_f32_16x16x32_bf16 v[18:21],a[0:3],v[26:29],0
s_add_u32 s61,s54,s46
R 50,2,6144
v_add_f32 v11,v11,v63
M 18,a[8:11],30
L 3
D 0,56,0
M 18,a[16:19],34
s_cmp_ge_u32 s61,s63
R 54,2,7168
v_lshlrev_b32 v62,16,v59
M 18,a[24:27],38
s_cselect_b32 s61,s93,s61
v_add_f32 v12,v12,v62
v_and_b32 v63,0xffff0000,v59
M 22,a[20:23],34
s_cselect_b32 s46,s88,s46
R 26,2,8192
v_add_f32 v13,v13,v63
M 22,a[28:31],38
L 3
v_lshlrev_b32 v62,16,v60
v_add_f32 v14,v14,v62
M 22,a[36:39],42
s_cselect_b32 s63,s89,s63
R 30,2,9216
v_and_b32 v63,0xffff0000,v60
M 22,a[44:47],46
D 0,56,1024
M 18,a[32:35],42
s_cselect_b32 s93,s50,s93
R 34,2,10240
v_add_f32 v15,v15,v63
M 18,a[40:43],46
L 3
v_lshlrev_b32 v62,16,v61
v_add_f32 v16,v16,v62
M 18,a[48:51],50
s_cselect_b32 s88,8,s88
R 38,2,11264
v_and_b32 v63,0xffff0000,v61
M 18,a[56:59],54
buffer_load_dwordx4 v[58:61],v0,s[36:39],s74 offen offset:0 sc1
M 22,a[52:55],50
s_cselect_b32 s89,-1,s89
R 42,2,12288
v_add_f32 v17,v17,v63
M 22,a[60:63],54
L 3
v_cvt_pk_bf16_f32 v10,v10,v11
v_cvt_pk_bf16_f32 v11,v12,v13
M 22,a[68:71],26
s_mul_i32 s58,s61,2560
R 46,2,13312
v_cvt_pk_bf16_f32 v12,v14,v15
M 22,a[76:79],30
D 0,56,2048
M 18,a[64:67],26
s_add_u32 s58,s58,109891584
R 50,2,14336
v_cvt_pk_bf16_f32 v13,v16,v17
M 18,a[72:75],30
L 3
buffer_store_dwordx4 v[10:13],v0,s[36:39],s66 offen offset:0 sc0 sc1
M 18,a[80:83],34
s_lshl_b32 s62,s54,15
s_mov_b32 s67,s65
R 54,2,15360
M 18,a[88:91],38
s_cmp_ge_u32 s67,s45
s_cselect_b32 s51,1,-1
s_add_u32 s74,s49,s62
M 22,a[84:87],34
s_add_u32 s65,s47,s62
s_mov_b32 s54,s61
R 26,2,16384
M 22,a[92:95],38
L 3
M 22,a[100:103],42
R 30,2,17408
M 22,a[108:111],46
D 0,56,3072
M 18,a[96:99],42
s_add_u32 m0,s57,4096
s_add_u32 s56,s56,4096
R 34,2,18432
M 18,a[104:107],46
L 3
M 18,a[112:115],50
R 38,2,19456
M 18,a[120:123],54
M 22,a[116:119],50
R 42,2,20480
M 22,a[124:127],54
L 3
D 0,56,0
M 22,a[132:135],26
R 46,2,21504
M 22,a[140:143],30
M 18,a[128:131],26
R 50,2,22528
M 18,a[136:139],30
L 3
M 18,a[144:147],34
R 54,2,23552
M 18,a[152:155],38
M 22,a[148:151],34
R 26,2,24576
M 22,a[156:159],38
L 3
D 0,56,1024
M 22,a[164:167],42
R 30,2,25600
M 22,a[172:175],46
M 18,a[160:163],42
R 34,2,26624
M 18,a[168:171],46
L 3
M 18,a[176:179],50
s_waitcnt vmcnt(9)
R 38,2,27648
M 18,a[184:187],54
s_barrier
M 22,a[180:183],50
R 42,2,28672
M 22,a[188:191],54
L 3
D 0,56,2048
M 22,a[196:199],26
s_add_u32 m0,s59,1024
s_add_u32 s56,s55,28672
R 46,2,29696
M 22,a[204:207],30
s_mul_i32 s55,s61,114688
s_add_u32 s55,s55,s60
M 18,a[192:195],26
R 50,2,30720
M 18,a[200:203],30
L 3
M 18,a[208:211],34
R 54,2,31744
M 18,a[216:219],38
M 22,a[212:215],34
R 26,2,32768
M 22,a[220:223],38
L 3
buffer_load_dword v8,s[36:39],s58 offen offset:0 sc1 lds
M 22,a[228:231],42
s_add_u32 m0,s57,28672
R 30,2,33792
M 22,a[236:239],46
D 0,56,0
M 18,a[224:227],42
R 34,2,34816
M 18,a[232:235],46
L 3
M 18,a[240:243],50
R 38,2,35840
M 18,a[248:251],54
M 22,a[244:247],50
R 42,2,36864
M 22,a[252:255],54
L 3
D 0,56,1024
M 22,v[68:71],26
R 46,2,37888
M 22,v[76:79],30
M 18,v[64:67],26
R 50,2,38912
M 18,v[72:75],30
L 3
M 18,v[80:83],34
R 54,2,39936
M 18,v[88:91],38
D 0,56,2048
M 22,v[84:87],34
R 26,2,40960
M 22,v[92:95],38
L 3
M 22,v[100:103],42
R 30,2,41984
M 22,v[108:111],46
M 18,v[96:99],42
R 34,2,43008
M 18,v[104:107],46
L 3
D 0,56,3072
M 18,v[112:115],50
s_add_u32 m0,s57,32768
s_add_u32 s56,s56,4096
R 38,2,44032
M 18,v[120:123],54
s_sub_u32 s57,s84,s57
M 22,v[116:119],50
R 42,2,45056
M 22,v[124:127],54
L 3
M 22,v[132:135],26
R 46,2,46080
M 22,v[140:143],30
D 0,56,0
M 18,v[128:131],26
R 50,2,47104
M 18,v[136:139],30
L 3
M 18,v[144:147],34
R 54,2,48128
M 18,v[152:155],38
M 22,v[148:151],34
R 26,2,49152
M 22,v[156:159],38
L 3
D 0,56,1024
M 22,v[164:167],42
R 30,2,50176
M 22,v[172:175],46
M 18,v[160:163],42
R 34,2,51200
M 18,v[168:171],46
L 3
M 18,v[176:179],50
R 38,2,52224
M 18,v[184:187],54
D 0,56,2048
M 22,v[180:183],50
R 42,2,53248
M 22,v[188:191],54
L 3
M 22,v[196:199],26
R 46,2,54272
M 22,v[204:207],30
)"
R"(M 18,v[192:195],26
R 50,2,55296
M 18,v[200:203],30
L 3
M 18,v[208:211],34
s_waitcnt vmcnt(7)
R 54,2,56320
M 18,v[216:219],38
s_barrier
ds_read_b32 v7,v6 offset:1024
M 22,v[212:215],34
R 26,1,0
M 22,v[220:223],38
L 4
buffer_store_dword v4,v3,s[36:39],s69 offen offset:0 sc0 sc1
M 22,v[228:231],42
s_mov_b32 s69,s68
s_lshr_b32 s68,s62,4
R 30,1,1024
M 22,v[236:239],46
s_add_u32 s68,s68,s48
M 18,v[224:227],42
R 34,1,2048
M 18,v[232:235],46
L 4
L 3
M 18,v[240:243],50
R 38,1,3072
v_cmp_eq_u32 vcc,s53,v7
M 18,v[248:251],54
M 22,v[244:247],50
L 2
s_cmp_eq_u32 vcc_lo,s51
R 42,1,4096
M 22,v[252:255],54
s_cbranch_scc1 Lg4_F0_%=
Lg4_K0_%=:
s_cmp_eq_u32 s51,1
s_cbranch_scc0 Lg4_L0_%=
s_cmp_eq_u32 vcc_lo,-1
s_cbranch_scc1 Lg4_Fa0_%=
s_branch Lg4_La0_%=
Lg4_L0_%=:
v_mfma_f32_16x16x32_bf16 v[14:17],a[4:7],v[26:29],0
s_add_u32 m0,s59,3072
R 46,1,5120
v_lshlrev_b32 v62,16,v58
M 14,a[12:15],30
buffer_load_dword v8,s[36:39],s58 offen offset:0 sc1 lds
v_mfma_f32_16x16x32_bf16 v[10:13],a[0:3],v[26:29],0
s_mov_b32 s66,s65
R 50,1,6144
v_add_f32 v18,v18,v62
M 10,a[8:11],30
L 3
v_and_b32 v63,0xffff0000,v58
v_add_f32 v19,v19,v63
M 10,a[16:19],34
s_lshl_b32 s62,s54,15
R 54,1,7168
v_lshlrev_b32 v62,16,v59
M 10,a[24:27],38
s_cmp_ge_u32 s66,s45
v_add_f32 v20,v20,v62
v_and_b32 v63,0xffff0000,v59
M 14,a[20:23],34
s_cselect_b32 s51,1,-1
R 26,1,8192
v_add_f32 v21,v21,v63
M 14,a[28:31],38
L 3
v_lshlrev_b32 v62,16,v60
v_add_f32 v22,v22,v62
M 14,a[36:39],42
s_add_u32 s65,s47,s62
R 30,1,9216
v_and_b32 v63,0xffff0000,v60
M 14,a[44:47],46
v_add_f32 v23,v23,v63
v_lshlrev_b32 v62,16,v61
M 10,a[32:35],42
R 34,1,10240
v_add_f32 v24,v24,v62
M 10,a[40:43],46
L 3
v_and_b32 v63,0xffff0000,v61
v_add_f32 v25,v25,v63
M 10,a[48:51],50
R 38,1,11264
v_cvt_pk_bf16_f32 v18,v18,v19
M 10,a[56:59],54
buffer_load_dwordx4 v[58:61],v0,s[36:39],s74 offen offset:0 sc1
M 14,a[52:55],50
s_add_u32 s74,s49,s62
R 42,1,12288
v_cvt_pk_bf16_f32 v19,v20,v21
M 14,a[60:63],54
L 3
v_cvt_pk_bf16_f32 v20,v22,v23
v_cvt_pk_bf16_f32 v21,v24,v25
M 14,a[68:71],26
R 46,1,13312
M 14,a[76:79],30
buffer_store_dwordx4 v[18:21],v0,s[36:39],s67 offen offset:0 sc0 sc1
M 10,a[64:67],26
R 50,1,14336
M 10,a[72:75],30
L 3
M 10,a[80:83],34
R 54,1,15360
M 10,a[88:91],38
M 14,a[84:87],34
R 26,1,16384
M 14,a[92:95],38
L 3
M 14,a[100:103],42
R 30,1,17408
M 14,a[108:111],46
M 10,a[96:99],42
R 34,1,18432
M 10,a[104:107],46
L 3
M 10,a[112:115],50
R 38,1,19456
M 10,a[120:123],54
M 14,a[116:119],50
R 42,1,20480
M 14,a[124:127],54
L 3
M 14,a[132:135],26
R 46,1,21504
M 14,a[140:143],30
M 10,a[128:131],26
R 50,1,22528
M 10,a[136:139],30
L 3
M 10,a[144:147],34
R 54,1,23552
M 10,a[152:155],38
M 14,a[148:151],34
R 26,1,24576
M 14,a[156:159],38
L 3
M 14,a[164:167],42
R 30,1,25600
M 14,a[172:175],46
M 10,a[160:163],42
R 34,1,26624
M 10,a[168:171],46
L 3
M 10,a[176:179],50
s_waitcnt vmcnt(2)
R 38,1,27648
M 10,a[184:187],54
s_barrier
ds_read_b32 v7,v6 offset:3072
M 14,a[180:183],50
R 42,1,28672
M 14,a[188:191],54
L 4
M 14,a[196:199],26
R 46,1,29696
M 14,a[204:207],30
M 10,a[192:195],26
R 50,1,30720
M 10,a[200:203],30
L 4
L 3
v_cmp_eq_u32 vcc,s53,v7
M 10,a[208:211],34
R 54,1,31744
M 10,a[216:219],38
M 14,a[212:215],34
s_cmp_eq_u32 vcc_lo,-1
R 26,1,32768
s_cbranch_scc0 Lg4_X0_%=
M 14,a[220:223],38
L 3
s_add_u32 m0,s57,0
s_add_u32 s56,s55,0
M 14,a[228:231],42
s_add_u32 s61,s54,s46
s_cmp_ge_u32 s61,s63
s_cselect_b32 s61,s93,s61
R 30,1,33792
M 14,a[236:239],46
s_mul_i32 s58,s61,2560
D 0,56,0
M 10,a[224:227],42
s_cselect_b32 s46,s88,s46
s_cselect_b32 s63,s89,s63
R 34,1,34816
M 10,a[232:235],46
L 3
s_cselect_b32 s93,s50,s93
s_cselect_b32 s88,8,s88
M 10,a[240:243],50
s_cselect_b32 s89,-1,s89
s_add_u32 s58,s58,109891584
R 38,1,35840
M 10,a[248:251],54
D 0,56,1024
M 14,a[244:247],50
s_mov_b32 s54,s61
R 42,1,36864
M 14,a[252:255],54
L 3
M 14,v[68:71],26
R 46,1,37888
M 14,v[76:79],30
D 0,56,2048
M 10,v[64:67],26
R 50,1,38912
M 10,v[72:75],30
L 3
M 10,v[80:83],34
R 54,1,39936
M 10,v[88:91],38
D 0,56,3072
M 14,v[84:87],34
s_add_u32 m0,s57,4096
s_add_u32 s56,s56,4096
R 26,1,40960
M 14,v[92:95],38
L 3
M 14,v[100:103],42
R 30,1,41984
M 14,v[108:111],46
D 0,56,0
M 10,v[96:99],42
R 34,1,43008
M 10,v[104:107],46
L 3
M 10,v[112:115],50
R 38,1,44032
M 10,v[120:123],54
D 0,56,1024
M 14,v[116:119],50
R 42,1,45056
M 14,v[124:127],54
L 3
M 14,v[132:135],26
R 46,1,46080
M 14,v[140:143],30
D 0,56,2048
M 10,v[128:131],26
s_add_u32 m0,s59,0
s_add_u32 s56,s55,28672
R 50,1,47104
M 10,v[136:139],30
L 3
buffer_load_dword v8,s[36:39],s58 offen offset:0 sc1 lds
M 10,v[144:147],34
s_add_u32 m0,s57,28672
s_mul_i32 s55,s61,114688
R 54,1,48128
M 10,v[152:155],38
s_add_u32 s55,s55,s60
M 14,v[148:151],34
R 26,1,49152
M 14,v[156:159],38
L 3
D 0,56,0
M 14,v[164:167],42
R 30,1,50176
M 14,v[172:175],46
M 10,v[160:163],42
R 34,1,51200
M 10,v[168:171],46
L 3
D 0,56,1024
M 10,v[176:179],50
R 38,1,52224
M 10,v[184:187],54
D 0,56,2048
M 14,v[180:183],50
R 42,1,53248
M 14,v[188:191],54
L 3
M 14,v[196:199],26
R 46,1,54272
M 14,v[204:207],30
D 0,56,3072
M 10,v[192:195],26
s_add_u32 m0,s57,32768
s_add_u32 s56,s56,4096
R 50,1,55296
M 10,v[200:203],30
L 3
s_sub_u32 s57,s84,s57
M 10,v[208:211],34
s_waitcnt vmcnt(4)
R 54,1,56320
M 10,v[216:219],38
s_barrier
ds_read_b32 v7,v6 offset:0
M 14,v[212:215],34
R 26,2,0
M 14,v[220:223],38
L 4
buffer_store_dword v4,v3,s[36:39],s69 offen offset:0 sc0 sc1
M 14,v[228:231],42
s_mov_b32 s69,s68
s_lshr_b32 s68,s62,4
R 30,2,1024
M 14,v[236:239],46
D 0,56,0
M 10,v[224:227],42
s_add_u32 s68,s68,s48
R 34,2,2048
M 10,v[232:235],46
L 4
D 0,56,1024
M 10,v[240:243],50
L 3
R 38,2,3072
v_cmp_eq_u32 vcc,s53,v7
M 10,v[248:251],54
D 0,56,2048
M 14,v[244:247],50
L 2
s_cmp_eq_u32 vcc_lo,s51
R 42,2,4096
M 14,v[252:255],54
buffer_store_dword v4,v3,s[36:39],s71 offen offset:0 sc0 sc1
s_cbranch_scc1 Lg4_F1_%=
s_branch Lg4_K1_%=
Lg4_X0_%=:
M 14,a[220:223],38
L 3
s_add_u32 m0,s59,0
M 14,a[228:231],42
R 30,1,33792
M 14,a[236:239],46
buffer_load_dword v8,s[36:39],s58 offen offset:0 sc1 lds
M 10,a[224:227],42
R 34,1,34816
M 10,a[232:235],46
L 3
M 10,a[240:243],50
R 38,1,35840
M 10,a[248:251],54
M 14,a[244:247],50
R 42,1,36864
M 14,a[252:255],54
L 3
M 14,v[68:71],26
R 46,1,37888
M 14,v[76:79],30
M 10,v[64:67],26
R 50,1,38912
M 10,v[72:75],30
L 3
M 10,v[80:83],34
R 54,1,39936
M 10,v[88:91],38
M 14,v[84:87],34
R 26,1,40960
M 14,v[92:95],38
L 3
M 14,v[100:103],42
R 30,1,41984
M 14,v[108:111],46
M 10,v[96:99],42
R 34,1,43008
M 10,v[104:107],46
L 3
M 10,v[112:115],50
R 38,1,44032
M 10,v[120:123],54
M 14,v[116:119],50
R 42,1,45056
M 14,v[124:127],54
L 3
M 14,v[132:135],26
R 46,1,46080
M 14,v[140:143],30
M 10,v[128:131],26
R 50,1,47104
M 10,v[136:139],30
L 3
M 10,v[144:147],34
R 54,1,48128
M 10,v[152:155],38
M 14,v[148:151],34
R 26,1,49152
M 14,v[156:159],38
L 3
M 14,v[164:167],42
R 30,1,50176
M 14,v[172:175],46
M 10,v[160:163],42
R 34,1,51200
M 10,v[168:171],46
L 3
M 10,v[176:179],50
R 38,1,52224
M 10,v[184:187],54
M 14,v[180:183],50
R 42,1,53248
M 14,v[188:191],54
L 3
M 14,v[196:199],26
R 46,1,54272
M 14,v[204:207],30
M 10,v[192:195],26
R 50,1,55296
M 10,v[200:203],30
L 3
M 10,v[208:211],34
s_waitcnt vmcnt(0)
R 54,1,56320
M 10,v[216:219],38
s_barrier
buffer_store_dword v4,v3,s[36:39],s69 offen offset:0 sc0 sc1
M 14,v[212:215],34
s_mov_b32 s69,s68
s_lshr_b32 s68,s62,4
ds_read_b32 v7,v6 offset:0
M 14,v[220:223],38
L 3
s_add_u32 s68,s68,s48
M 14,v[228:231],42
M 14,v[236:239],46
M 10,v[224:227],42
M 10,v[232:235],46
L 1
M 10,v[240:243],50
L 0
v_cmp_eq_u32 vcc,s53,v7
M 10,v[248:251],54
M 14,v[244:247],50
M 14,v[252:255],54
s_cmp_eq_u32 s51,1
s_cbranch_scc1 Lg4_pa0_%=
s_mov_b32 s80,1
s_cmp_eq_u32 vcc_lo,-1
s_cbranch_scc0 Lg4_xn0_%=
s_add_u32 m0,s57,0
s_add_u32 s56,s55,0
D 0,56,0
v_lshlrev_b32 v62,16,v58
D 0,56,1024
v_add_f32 v10,v10,v62
D 0,56,2048
v_and_b32 v63,0xffff0000,v58
D 0,56,3072
v_add_f32 v11,v11,v63
s_add_u32 m0,s57,4096
s_add_u32 s56,s56,4096
D 0,56,0
v_lshlrev_b32 v62,16,v59
D 0,56,1024
v_add_f32 v12,v12,v62
D 0,56,2048
v_and_b32 v63,0xffff0000,v59
s_add_u32 s61,s54,s46
s_cmp_ge_u32 s61,s63
s_cselect_b32 s61,s93,s61
s_cselect_b32 s46,s88,s46
s_cselect_b32 s63,s89,s63
s_cselect_b32 s93,s50,s93
s_cselect_b32 s88,8,s88
s_cselect_b32 s89,-1,s89
s_mul_i32 s58,s61,2560
s_add_u32 s58,s58,109891584
s_add_u32 m0,s57,28672
s_add_u32 s56,s55,28672
D 0,56,0
v_add_f32 v13,v13,v63
D 0,56,1024
v_lshlrev_b32 v62,16,v60
D 0,56,2048
v_add_f32 v14,v14,v62
D 0,56,3072
v_and_b32 v63,0xffff0000,v60
s_add_u32 m0,s57,32768
s_add_u32 s56,s56,4096
D 0,56,0
v_add_f32 v15,v15,v63
D 0,56,1024
v_lshlrev_b32 v62,16,v61
D 0,56,2048
v_add_f32 v16,v16,v62
s_mul_i32 s55,s61,114688
s_add_u32 s55,s55,s60
s_sub_u32 s57,s84,s57
s_mov_b32 s54,s61
v_and_b32 v63,0xffff0000,v61
v_add_f32 v17,v17,v63
v_cvt_pk_bf16_f32 v10,v10,v11
v_cvt_pk_bf16_f32 v11,v12,v13
v_cvt_pk_bf16_f32 v12,v14,v15
v_cvt_pk_bf16_f32 v13,v16,v17
buffer_store_dwordx4 v[10:13],v0,s[36:39],s66 offen offset:0 sc0 sc1
s_mov_b32 s72,s69
s_mov_b32 s69,0x7fff0000
s_mov_b32 s66,0x7fff0000
s_mov_b32 s76,0x7fff0000
s_branch Lg4_gox_%=
)"
R"(Lg4_xn0_%=:
s_cmp_ge_u32 s54,256
s_cbranch_scc1 Lg4_fin0_%=
s_add_u32 m0,s59,4096
s_nop 0
buffer_load_dword v8,s[36:39],s58 offen offset:0 sc1 lds
buffer_store_dword v4,v3,s[36:39],s71 offen offset:0 sc0 sc1
s_add_u32 m0,s59,5120
s_nop 0
buffer_load_dword v8,s[36:39],s58 offen offset:0 sc1 lds
buffer_store_dword v4,v3,s[36:39],s71 offen offset:0 sc0 sc1
s_mov_b32 s83,0
v_lshlrev_b32 v62,16,v58
v_add_f32 v10,v10,v62
v_and_b32 v63,0xffff0000,v58
v_add_f32 v11,v11,v63
v_lshlrev_b32 v62,16,v59
v_add_f32 v12,v12,v62
v_and_b32 v63,0xffff0000,v59
v_add_f32 v13,v13,v63
v_lshlrev_b32 v62,16,v60
v_add_f32 v14,v14,v62
v_and_b32 v63,0xffff0000,v60
v_add_f32 v15,v15,v63
v_lshlrev_b32 v62,16,v61
v_add_f32 v16,v16,v62
v_and_b32 v63,0xffff0000,v61
v_add_f32 v17,v17,v63
v_cvt_pk_bf16_f32 v10,v10,v11
v_cvt_pk_bf16_f32 v11,v12,v13
v_cvt_pk_bf16_f32 v12,v14,v15
v_cvt_pk_bf16_f32 v13,v16,v17
buffer_store_dwordx4 v[10:13],v0,s[36:39],s66 offen offset:0 sc0 sc1
s_mov_b32 s72,s69
s_mov_b32 s69,0x7fff0000
s_mov_b32 s66,0x7fff0000
s_mov_b32 s76,0x7fff0000
s_branch Lg4_poll_%=
Lg4_fin0_%=:
v_lshlrev_b32 v62,16,v58
v_add_f32 v10,v10,v62
v_and_b32 v63,0xffff0000,v58
v_add_f32 v11,v11,v63
v_lshlrev_b32 v62,16,v59
v_add_f32 v12,v12,v62
v_and_b32 v63,0xffff0000,v59
v_add_f32 v13,v13,v63
v_lshlrev_b32 v62,16,v60
v_add_f32 v14,v14,v62
v_and_b32 v63,0xffff0000,v60
v_add_f32 v15,v15,v63
v_lshlrev_b32 v62,16,v61
v_add_f32 v16,v16,v62
v_and_b32 v63,0xffff0000,v61
v_add_f32 v17,v17,v63
v_cvt_pk_bf16_f32 v10,v10,v11
v_cvt_pk_bf16_f32 v11,v12,v13
v_cvt_pk_bf16_f32 v12,v14,v15
v_cvt_pk_bf16_f32 v13,v16,v17
buffer_store_dwordx4 v[10:13],v0,s[36:39],s66 offen offset:0 sc0 sc1
s_mov_b32 s72,s69
s_mov_b32 s69,0x7fff0000
s_mov_b32 s66,0x7fff0000
s_mov_b32 s76,0x7fff0000
s_branch Lg4_done_%=
Lg4_K1_%=:
s_cmp_eq_u32 s51,1
s_cbranch_scc0 Lg4_L1_%=
s_cmp_eq_u32 vcc_lo,-1
s_cbranch_scc1 Lg4_Fa1_%=
s_branch Lg4_La1_%=
Lg4_L1_%=:
v_mfma_f32_16x16x32_bf16 v[22:25],a[4:7],v[26:29],0
s_add_u32 m0,s59,3072
R 46,2,5120
v_lshlrev_b32 v62,16,v58
M 22,a[12:15],30
buffer_load_dword v8,s[36:39],s58 offen offset:0 sc1 lds
v_mfma_f32_16x16x32_bf16 v[18:21],a[0:3],v[26:29],0
s_mov_b32 s67,s65
R 50,2,6144
v_add_f32 v10,v10,v62
M 18,a[8:11],30
L 3
v_and_b32 v63,0xffff0000,v58
v_add_f32 v11,v11,v63
M 18,a[16:19],34
s_lshl_b32 s62,s54,15
R 54,2,7168
v_lshlrev_b32 v62,16,v59
M 18,a[24:27],38
s_cmp_ge_u32 s67,s45
v_add_f32 v12,v12,v62
v_and_b32 v63,0xffff0000,v59
M 22,a[20:23],34
s_cselect_b32 s51,1,-1
R 26,2,8192
v_add_f32 v13,v13,v63
M 22,a[28:31],38
L 3
v_lshlrev_b32 v62,16,v60
v_add_f32 v14,v14,v62
M 22,a[36:39],42
s_add_u32 s65,s47,s62
R 30,2,9216
v_and_b32 v63,0xffff0000,v60
M 22,a[44:47],46
v_add_f32 v15,v15,v63
v_lshlrev_b32 v62,16,v61
M 18,a[32:35],42
R 34,2,10240
v_add_f32 v16,v16,v62
M 18,a[40:43],46
L 3
v_and_b32 v63,0xffff0000,v61
v_add_f32 v17,v17,v63
M 18,a[48:51],50
R 38,2,11264
v_cvt_pk_bf16_f32 v10,v10,v11
M 18,a[56:59],54
buffer_load_dwordx4 v[58:61],v0,s[36:39],s74 offen offset:0 sc1
M 22,a[52:55],50
s_add_u32 s74,s49,s62
R 42,2,12288
v_cvt_pk_bf16_f32 v11,v12,v13
M 22,a[60:63],54
L 3
v_cvt_pk_bf16_f32 v12,v14,v15
v_cvt_pk_bf16_f32 v13,v16,v17
M 22,a[68:71],26
R 46,2,13312
M 22,a[76:79],30
buffer_store_dwordx4 v[10:13],v0,s[36:39],s66 offen offset:0 sc0 sc1
M 18,a[64:67],26
R 50,2,14336
M 18,a[72:75],30
L 3
M 18,a[80:83],34
R 54,2,15360
M 18,a[88:91],38
M 22,a[84:87],34
R 26,2,16384
M 22,a[92:95],38
L 3
M 22,a[100:103],42
R 30,2,17408
M 22,a[108:111],46
M 18,a[96:99],42
R 34,2,18432
M 18,a[104:107],46
L 3
M 18,a[112:115],50
R 38,2,19456
M 18,a[120:123],54
M 22,a[116:119],50
R 42,2,20480
M 22,a[124:127],54
L 3
M 22,a[132:135],26
R 46,2,21504
M 22,a[140:143],30
M 18,a[128:131],26
R 50,2,22528
M 18,a[136:139],30
L 3
M 18,a[144:147],34
R 54,2,23552
M 18,a[152:155],38
M 22,a[148:151],34
R 26,2,24576
M 22,a[156:159],38
L 3
M 22,a[164:167],42
R 30,2,25600
M 22,a[172:175],46
M 18,a[160:163],42
R 34,2,26624
M 18,a[168:171],46
L 3
M 18,a[176:179],50
s_waitcnt vmcnt(2)
R 38,2,27648
M 18,a[184:187],54
s_barrier
ds_read_b32 v7,v6 offset:3072
M 22,a[180:183],50
R 42,2,28672
M 22,a[188:191],54
L 4
M 22,a[196:199],26
R 46,2,29696
M 22,a[204:207],30
M 18,a[192:195],26
R 50,2,30720
M 18,a[200:203],30
L 4
L 3
v_cmp_eq_u32 vcc,s53,v7
M 18,a[208:211],34
R 54,2,31744
M 18,a[216:219],38
M 22,a[212:215],34
s_cmp_eq_u32 vcc_lo,-1
R 26,2,32768
s_cbranch_scc0 Lg4_X1_%=
M 22,a[220:223],38
L 3
s_add_u32 m0,s57,0
s_add_u32 s56,s55,0
M 22,a[228:231],42
s_add_u32 s61,s54,s46
s_cmp_ge_u32 s61,s63
s_cselect_b32 s61,s93,s61
R 30,2,33792
M 22,a[236:239],46
s_mul_i32 s58,s61,2560
D 0,56,0
M 18,a[224:227],42
s_cselect_b32 s46,s88,s46
s_cselect_b32 s63,s89,s63
R 34,2,34816
M 18,a[232:235],46
L 3
s_cselect_b32 s93,s50,s93
s_cselect_b32 s88,8,s88
M 18,a[240:243],50
s_cselect_b32 s89,-1,s89
s_add_u32 s58,s58,109891584
R 38,2,35840
M 18,a[248:251],54
D 0,56,1024
M 22,a[244:247],50
s_mov_b32 s54,s61
R 42,2,36864
M 22,a[252:255],54
L 3
M 22,v[68:71],26
R 46,2,37888
M 22,v[76:79],30
D 0,56,2048
M 18,v[64:67],26
R 50,2,38912
M 18,v[72:75],30
L 3
M 18,v[80:83],34
R 54,2,39936
M 18,v[88:91],38
D 0,56,3072
M 22,v[84:87],34
s_add_u32 m0,s57,4096
s_add_u32 s56,s56,4096
R 26,2,40960
M 22,v[92:95],38
L 3
M 22,v[100:103],42
R 30,2,41984
M 22,v[108:111],46
D 0,56,0
M 18,v[96:99],42
R 34,2,43008
M 18,v[104:107],46
L 3
M 18,v[112:115],50
R 38,2,44032
M 18,v[120:123],54
D 0,56,1024
M 22,v[116:119],50
R 42,2,45056
M 22,v[124:127],54
L 3
M 22,v[132:135],26
R 46,2,46080
M 22,v[140:143],30
D 0,56,2048
M 18,v[128:131],26
s_add_u32 m0,s59,1024
s_add_u32 s56,s55,28672
R 50,2,47104
M 18,v[136:139],30
L 3
buffer_load_dword v8,s[36:39],s58 offen offset:0 sc1 lds
M 18,v[144:147],34
s_add_u32 m0,s57,28672
s_mul_i32 s55,s61,114688
R 54,2,48128
M 18,v[152:155],38
s_add_u32 s55,s55,s60
M 22,v[148:151],34
R 26,2,49152
M 22,v[156:159],38
L 3
D 0,56,0
M 22,v[164:167],42
R 30,2,50176
M 22,v[172:175],46
M 18,v[160:163],42
R 34,2,51200
M 18,v[168:171],46
L 3
D 0,56,1024
M 18,v[176:179],50
R 38,2,52224
M 18,v[184:187],54
D 0,56,2048
M 22,v[180:183],50
R 42,2,53248
M 22,v[188:191],54
L 3
M 22,v[196:199],26
R 46,2,54272
M 22,v[204:207],30
D 0,56,3072
M 18,v[192:195],26
s_add_u32 m0,s57,32768
s_add_u32 s56,s56,4096
R 50,2,55296
M 18,v[200:203],30
L 3
s_sub_u32 s57,s84,s57
M 18,v[208:211],34
s_waitcnt vmcnt(4)
R 54,2,56320
M 18,v[216:219],38
s_barrier
ds_read_b32 v7,v6 offset:1024
M 22,v[212:215],34
R 26,1,0
M 22,v[220:223],38
L 4
buffer_store_dword v4,v3,s[36:39],s69 offen offset:0 sc0 sc1
M 22,v[228:231],42
s_mov_b32 s69,s68
s_lshr_b32 s68,s62,4
R 30,1,1024
M 22,v[236:239],46
D 0,56,0
M 18,v[224:227],42
s_add_u32 s68,s68,s48
R 34,1,2048
M 18,v[232:235],46
L 4
D 0,56,1024
M 18,v[240:243],50
L 3
R 38,1,3072
v_cmp_eq_u32 vcc,s53,v7
M 18,v[248:251],54
D 0,56,2048
M 22,v[244:247],50
L 2
s_cmp_eq_u32 vcc_lo,s51
R 42,1,4096
M 22,v[252:255],54
buffer_store_dword v4,v3,s[36:39],s71 offen offset:0 sc0 sc1
s_cbranch_scc1 Lg4_F0_%=
s_branch Lg4_K0_%=
Lg4_X1_%=:
M 22,a[220:223],38
L 3
s_add_u32 m0,s59,1024
M 22,a[228:231],42
R 30,2,33792
M 22,a[236:239],46
buffer_load_dword v8,s[36:39],s58 offen offset:0 sc1 lds
M 18,a[224:227],42
R 34,2,34816
M 18,a[232:235],46
L 3
M 18,a[240:243],50
R 38,2,35840
M 18,a[248:251],54
M 22,a[244:247],50
R 42,2,36864
M 22,a[252:255],54
L 3
M 22,v[68:71],26
R 46,2,37888
M 22,v[76:79],30
M 18,v[64:67],26
R 50,2,38912
M 18,v[72:75],30
L 3
M 18,v[80:83],34
R 54,2,39936
M 18,v[88:91],38
M 22,v[84:87],34
R 26,2,40960
M 22,v[92:95],38
L 3
M 22,v[100:103],42
R 30,2,41984
M 22,v[108:111],46
M 18,v[96:99],42
R 34,2,43008
M 18,v[104:107],46
L 3
M 18,v[112:115],50
R 38,2,44032
M 18,v[120:123],54
M 22,v[116:119],50
R 42,2,45056
M 22,v[124:127],54
L 3
M 22,v[132:135],26
R 46,2,46080
M 22,v[140:143],30
M 18,v[128:131],26
R 50,2,47104
M 18,v[136:139],30
L 3
M 18,v[144:147],34
R 54,2,48128
M 18,v[152:155],38
M 22,v[148:151],34
R 26,2,49152
M 22,v[156:159],38
L 3
M 22,v[164:167],42
R 30,2,50176
M 22,v[172:175],46
M 18,v[160:163],42
R 34,2,51200
M 18,v[168:171],46
L 3
M 18,v[176:179],50
R 38,2,52224
M 18,v[184:187],54
M 22,v[180:183],50
R 42,2,53248
M 22,v[188:191],54
L 3
M 22,v[196:199],26
R 46,2,54272
M 22,v[204:207],30
M 18,v[192:195],26
R 50,2,55296
M 18,v[200:203],30
L 3
M 18,v[208:211],34
s_waitcnt vmcnt(0)
R 54,2,56320
M 18,v[216:219],38
s_barrier
buffer_store_dword v4,v3,s[36:39],s69 offen offset:0 sc0 sc1
M 22,v[212:215],34
s_mov_b32 s69,s68
s_lshr_b32 s68,s62,4
ds_read_b32 v7,v6 offset:1024
M 22,v[220:223],38
L 3
s_add_u32 s68,s68,s48
M 22,v[228:231],42
M 22,v[236:239],46
M 18,v[224:227],42
M 18,v[232:235],46
L 1
M 18,v[240:243],50
L 0
v_cmp_eq_u32 vcc,s53,v7
M 18,v[248:251],54
M 22,v[244:247],50
M 22,v[252:255],54
s_cmp_eq_u32 s51,1
s_cbranch_scc1 Lg4_pa1_%=
s_mov_b32 s80,0
s_cmp_eq_u32 vcc_lo,-1
s_cbranch_scc0 Lg4_xn1_%=
s_add_u32 m0,s57,0
s_add_u32 s56,s55,0
D 0,56,0
v_lshlrev_b32 v62,16,v58
D 0,56,1024
v_add_f32 v18,v18,v62
D 0,56,2048
v_and_b32 v63,0xffff0000,v58
D 0,56,3072
v_add_f32 v19,v19,v63
s_add_u32 m0,s57,4096
s_add_u32 s56,s56,4096
D 0,56,0
v_lshlrev_b32 v62,16,v59
D 0,56,1024
v_add_f32 v20,v20,v62
D 0,56,2048
v_and_b32 v63,0xffff0000,v59
s_add_u32 s61,s54,s46
s_cmp_ge_u32 s61,s63
s_cselect_b32 s61,s93,s61
s_cselect_b32 s46,s88,s46
s_cselect_b32 s63,s89,s63
s_cselect_b32 s93,s50,s93
s_cselect_b32 s88,8,s88
s_cselect_b32 s89,-1,s89
s_mul_i32 s58,s61,2560
s_add_u32 s58,s58,109891584
s_add_u32 m0,s57,28672
s_add_u32 s56,s55,28672
D 0,56,0
v_add_f32 v21,v21,v63
D 0,56,1024
)"
R"(v_lshlrev_b32 v62,16,v60
D 0,56,2048
v_add_f32 v22,v22,v62
D 0,56,3072
v_and_b32 v63,0xffff0000,v60
s_add_u32 m0,s57,32768
s_add_u32 s56,s56,4096
D 0,56,0
v_add_f32 v23,v23,v63
D 0,56,1024
v_lshlrev_b32 v62,16,v61
D 0,56,2048
v_add_f32 v24,v24,v62
s_mul_i32 s55,s61,114688
s_add_u32 s55,s55,s60
s_sub_u32 s57,s84,s57
s_mov_b32 s54,s61
v_and_b32 v63,0xffff0000,v61
v_add_f32 v25,v25,v63
v_cvt_pk_bf16_f32 v18,v18,v19
v_cvt_pk_bf16_f32 v19,v20,v21
v_cvt_pk_bf16_f32 v20,v22,v23
v_cvt_pk_bf16_f32 v21,v24,v25
buffer_store_dwordx4 v[18:21],v0,s[36:39],s67 offen offset:0 sc0 sc1
s_mov_b32 s72,s69
s_mov_b32 s69,0x7fff0000
s_mov_b32 s67,0x7fff0000
s_mov_b32 s77,0x7fff0000
s_branch Lg4_gox_%=
Lg4_xn1_%=:
s_cmp_ge_u32 s54,256
s_cbranch_scc1 Lg4_fin1_%=
s_add_u32 m0,s59,4096
s_nop 0
buffer_load_dword v8,s[36:39],s58 offen offset:0 sc1 lds
buffer_store_dword v4,v3,s[36:39],s71 offen offset:0 sc0 sc1
s_add_u32 m0,s59,5120
s_nop 0
buffer_load_dword v8,s[36:39],s58 offen offset:0 sc1 lds
buffer_store_dword v4,v3,s[36:39],s71 offen offset:0 sc0 sc1
s_mov_b32 s83,0
v_lshlrev_b32 v62,16,v58
v_add_f32 v18,v18,v62
v_and_b32 v63,0xffff0000,v58
v_add_f32 v19,v19,v63
v_lshlrev_b32 v62,16,v59
v_add_f32 v20,v20,v62
v_and_b32 v63,0xffff0000,v59
v_add_f32 v21,v21,v63
v_lshlrev_b32 v62,16,v60
v_add_f32 v22,v22,v62
v_and_b32 v63,0xffff0000,v60
v_add_f32 v23,v23,v63
v_lshlrev_b32 v62,16,v61
v_add_f32 v24,v24,v62
v_and_b32 v63,0xffff0000,v61
v_add_f32 v25,v25,v63
v_cvt_pk_bf16_f32 v18,v18,v19
v_cvt_pk_bf16_f32 v19,v20,v21
v_cvt_pk_bf16_f32 v20,v22,v23
v_cvt_pk_bf16_f32 v21,v24,v25
buffer_store_dwordx4 v[18:21],v0,s[36:39],s67 offen offset:0 sc0 sc1
s_mov_b32 s72,s69
s_mov_b32 s69,0x7fff0000
s_mov_b32 s67,0x7fff0000
s_mov_b32 s77,0x7fff0000
s_branch Lg4_poll_%=
Lg4_fin1_%=:
v_lshlrev_b32 v62,16,v58
v_add_f32 v18,v18,v62
v_and_b32 v63,0xffff0000,v58
v_add_f32 v19,v19,v63
v_lshlrev_b32 v62,16,v59
v_add_f32 v20,v20,v62
v_and_b32 v63,0xffff0000,v59
v_add_f32 v21,v21,v63
v_lshlrev_b32 v62,16,v60
v_add_f32 v22,v22,v62
v_and_b32 v63,0xffff0000,v60
v_add_f32 v23,v23,v63
v_lshlrev_b32 v62,16,v61
v_add_f32 v24,v24,v62
v_and_b32 v63,0xffff0000,v61
v_add_f32 v25,v25,v63
v_cvt_pk_bf16_f32 v18,v18,v19
v_cvt_pk_bf16_f32 v19,v20,v21
v_cvt_pk_bf16_f32 v20,v22,v23
v_cvt_pk_bf16_f32 v21,v24,v25
buffer_store_dwordx4 v[18:21],v0,s[36:39],s67 offen offset:0 sc0 sc1
s_mov_b32 s72,s69
s_mov_b32 s69,0x7fff0000
s_mov_b32 s67,0x7fff0000
s_mov_b32 s77,0x7fff0000
s_branch Lg4_done_%=
Lg4_Fa0_%=:
s_nop 0
v_mfma_f32_16x16x32_bf16 v[14:17],a[4:7],v[26:29],0
s_add_u32 m0,s57,0
R 46,1,5120
v_lshlrev_b32 v62,16,v58
M 14,a[12:15],30
s_add_u32 s56,s55,0
v_add_f32 v18,v18,v62
v_and_b32 v63,0xffff0000,v58
v_mfma_f32_16x16x32_bf16 v[10:13],a[0:3],v[26:29],0
s_add_u32 s61,s54,s46
R 50,1,6144
v_add_f32 v19,v19,v63
M 10,a[8:11],30
L 3
D 0,56,0
M 10,a[16:19],34
s_cmp_ge_u32 s61,s63
R 54,1,7168
v_lshlrev_b32 v62,16,v59
M 10,a[24:27],38
s_cselect_b32 s61,s93,s61
v_add_f32 v20,v20,v62
v_and_b32 v63,0xffff0000,v59
M 14,a[20:23],34
s_cselect_b32 s46,s88,s46
R 26,1,8192
v_add_f32 v21,v21,v63
M 14,a[28:31],38
L 3
v_lshlrev_b32 v62,16,v60
v_add_f32 v22,v22,v62
M 14,a[36:39],42
s_cselect_b32 s63,s89,s63
R 30,1,9216
v_and_b32 v63,0xffff0000,v60
M 14,a[44:47],46
D 0,56,1024
M 10,a[32:35],42
s_cselect_b32 s93,s50,s93
R 34,1,10240
v_add_f32 v23,v23,v63
M 10,a[40:43],46
L 3
v_lshlrev_b32 v62,16,v61
v_add_f32 v24,v24,v62
M 10,a[48:51],50
s_cselect_b32 s88,8,s88
R 38,1,11264
v_and_b32 v63,0xffff0000,v61
M 10,a[56:59],54
buffer_load_dwordx4 v[58:61],v0,s[36:39],s74 offen offset:0 sc1
M 14,a[52:55],50
s_cselect_b32 s89,-1,s89
R 42,1,12288
v_add_f32 v25,v25,v63
M 14,a[60:63],54
L 3
v_cvt_pk_bf16_f32 v18,v18,v19
v_cvt_pk_bf16_f32 v19,v20,v21
M 14,a[68:71],26
s_mul_i32 s58,s61,2560
R 46,1,13312
v_cvt_pk_bf16_f32 v20,v22,v23
M 14,a[76:79],30
D 0,56,2048
M 10,a[64:67],26
s_add_u32 s58,s58,109891584
R 50,1,14336
v_cvt_pk_bf16_f32 v21,v24,v25
M 10,a[72:75],30
L 3
v_lshrrev_b32 v62,8,v0
v_lshl_add_u32 v62,v62,5,v0
M 10,a[80:83],34
s_sub_u32 s75,s67,s85
R 54,1,15360
v_add_u32 v62,s73,v62
M 10,a[88:91],38
s_mov_b64 s[40:41],s[86:87]
ds_write_b128 v62,v[18:21] offset:0
M 14,a[84:87],34
s_mov_b32 s42,4194304
R 26,1,16384
ds_read2_b32 v[22:23],v5 offset0:0 offset1:16
M 14,a[92:95],38
L 5
s_mov_b32 s43,0x00020000
ds_read2_b32 v[24:25],v5 offset0:32 offset1:48
M 14,a[100:103],42
s_add_u32 s64,s75,0
s_lshl_b32 s62,s54,15
R 30,1,17408
M 14,a[108:111],46
D 0,56,3072
M 10,a[96:99],42
s_add_u32 m0,s57,4096
s_add_u32 s56,s56,4096
R 34,1,18432
M 10,a[104:107],46
L 6
s_mov_b32 s66,s65
s_add_u32 s74,s49,s62
M 10,a[112:115],50
s_add_u32 s65,s47,s62
s_mov_b32 s54,s61
R 38,1,19456
M 10,a[120:123],54
L 3
M 14,a[116:119],50
R 42,1,20480
M 14,a[124:127],54
L 3
D 0,56,0
M 14,a[132:135],26
R 46,1,21504
M 14,a[140:143],30
M 10,a[128:131],26
R 50,1,22528
M 10,a[136:139],30
L 3
M 10,a[144:147],34
R 54,1,23552
M 10,a[152:155],38
M 14,a[148:151],34
R 26,1,24576
M 14,a[156:159],38
L 3
D 0,56,1024
M 14,a[164:167],42
R 30,1,25600
M 14,a[172:175],46
M 10,a[160:163],42
R 34,1,26624
M 10,a[168:171],46
L 3
M 10,a[176:179],50
s_waitcnt vmcnt(8)
R 38,1,27648
M 10,a[184:187],54
s_barrier
M 14,a[180:183],50
R 42,1,28672
M 14,a[188:191],54
L 3
D 0,56,2048
M 14,a[196:199],26
s_add_u32 m0,s59,0
s_add_u32 s56,s55,28672
R 46,1,29696
M 14,a[204:207],30
s_mul_i32 s55,s61,114688
s_add_u32 s55,s55,s60
M 10,a[192:195],26
R 50,1,30720
M 10,a[200:203],30
L 3
M 10,a[208:211],34
R 54,1,31744
M 10,a[216:219],38
M 14,a[212:215],34
R 26,1,32768
M 14,a[220:223],38
L 3
buffer_load_dword v8,s[36:39],s58 offen offset:0 sc1 lds
M 14,a[228:231],42
s_add_u32 m0,s57,28672
R 30,1,33792
M 14,a[236:239],46
D 0,56,0
M 10,a[224:227],42
R 34,1,34816
M 10,a[232:235],46
L 3
M 10,a[240:243],50
R 38,1,35840
M 10,a[248:251],54
M 14,a[244:247],50
R 42,1,36864
M 14,a[252:255],54
L 3
D 0,56,1024
M 14,v[68:71],26
R 46,1,37888
M 14,v[76:79],30
M 10,v[64:67],26
R 50,1,38912
M 10,v[72:75],30
L 3
M 10,v[80:83],34
R 54,1,39936
M 10,v[88:91],38
D 0,56,2048
M 14,v[84:87],34
R 26,1,40960
M 14,v[92:95],38
L 3
M 14,v[100:103],42
R 30,1,41984
M 14,v[108:111],46
M 10,v[96:99],42
R 34,1,43008
M 10,v[104:107],46
L 3
D 0,56,3072
M 10,v[112:115],50
s_add_u32 m0,s57,32768
s_add_u32 s56,s56,4096
R 38,1,44032
M 10,v[120:123],54
s_sub_u32 s57,s84,s57
M 14,v[116:119],50
R 42,1,45056
M 14,v[124:127],54
L 3
M 14,v[132:135],26
R 46,1,46080
M 14,v[140:143],30
D 0,56,0
M 10,v[128:131],26
R 50,1,47104
M 10,v[136:139],30
L 3
M 10,v[144:147],34
R 54,1,48128
M 10,v[152:155],38
M 14,v[148:151],34
R 26,1,49152
M 14,v[156:159],38
L 3
D 0,56,1024
M 14,v[164:167],42
R 30,1,50176
M 14,v[172:175],46
M 10,v[160:163],42
R 34,1,51200
M 10,v[168:171],46
L 3
M 10,v[176:179],50
R 38,1,52224
M 10,v[184:187],54
D 0,56,2048
M 14,v[180:183],50
R 42,1,53248
M 14,v[188:191],54
L 3
M 14,v[196:199],26
R 46,1,54272
M 14,v[204:207],30
M 10,v[192:195],26
R 50,1,55296
M 10,v[200:203],30
L 3
M 10,v[208:211],34
s_waitcnt vmcnt(7)
R 54,1,56320
M 10,v[216:219],38
s_barrier
ds_read_b32 v7,v6 offset:0
M 14,v[212:215],34
R 26,2,0
M 14,v[220:223],38
L 4
M 14,v[228:231],42
R 30,2,1024
M 14,v[236:239],46
buffer_atomic_pk_add_bf16 v22,v9,s[40:43],s64 offen sc1
M 10,v[224:227],42
s_add_u32 s64,s75,8192
R 34,2,2048
M 10,v[232:235],46
L 4
buffer_atomic_pk_add_bf16 v23,v9,s[40:43],s64 offen sc1
M 10,v[240:243],50
s_add_u32 s64,s75,16384
L 3
R 38,2,3072
v_cmp_eq_u32 vcc,s53,v7
M 10,v[248:251],54
buffer_atomic_pk_add_bf16 v24,v9,s[40:43],s64 offen sc1
M 14,v[244:247],50
L 2
s_add_u32 s64,s75,24576
s_cmp_eq_u32 vcc_lo,-1
R 42,2,4096
M 14,v[252:255],54
buffer_atomic_pk_add_bf16 v25,v9,s[40:43],s64 offen sc1
s_cbranch_scc0 Lg4_Ka1_%=
Lg4_Fa1_%=:
s_nop 0
v_mfma_f32_16x16x32_bf16 v[22:25],a[4:7],v[26:29],0
s_add_u32 m0,s57,0
R 46,2,5120
v_lshlrev_b32 v62,16,v58
M 22,a[12:15],30
s_add_u32 s56,s55,0
v_add_f32 v10,v10,v62
v_and_b32 v63,0xffff0000,v58
v_mfma_f32_16x16x32_bf16 v[18:21],a[0:3],v[26:29],0
s_add_u32 s61,s54,s46
R 50,2,6144
v_add_f32 v11,v11,v63
M 18,a[8:11],30
L 3
D 0,56,0
M 18,a[16:19],34
s_cmp_ge_u32 s61,s63
R 54,2,7168
v_lshlrev_b32 v62,16,v59
M 18,a[24:27],38
s_cselect_b32 s61,s93,s61
v_add_f32 v12,v12,v62
v_and_b32 v63,0xffff0000,v59
M 22,a[20:23],34
s_cselect_b32 s46,s88,s46
R 26,2,8192
v_add_f32 v13,v13,v63
M 22,a[28:31],38
L 3
v_lshlrev_b32 v62,16,v60
v_add_f32 v14,v14,v62
M 22,a[36:39],42
s_cselect_b32 s63,s89,s63
R 30,2,9216
v_and_b32 v63,0xffff0000,v60
M 22,a[44:47],46
D 0,56,1024
M 18,a[32:35],42
s_cselect_b32 s93,s50,s93
R 34,2,10240
v_add_f32 v15,v15,v63
M 18,a[40:43],46
L 3
v_lshlrev_b32 v62,16,v61
v_add_f32 v16,v16,v62
M 18,a[48:51],50
s_cselect_b32 s88,8,s88
R 38,2,11264
v_and_b32 v63,0xffff0000,v61
M 18,a[56:59],54
buffer_load_dwordx4 v[58:61],v0,s[36:39],s74 offen offset:0 sc1
M 22,a[52:55],50
s_cselect_b32 s89,-1,s89
R 42,2,12288
v_add_f32 v17,v17,v63
M 22,a[60:63],54
L 3
v_cvt_pk_bf16_f32 v10,v10,v11
v_cvt_pk_bf16_f32 v11,v12,v13
M 22,a[68:71],26
s_mul_i32 s58,s61,2560
R 46,2,13312
v_cvt_pk_bf16_f32 v12,v14,v15
M 22,a[76:79],30
D 0,56,2048
M 18,a[64:67],26
s_add_u32 s58,s58,109891584
R 50,2,14336
v_cvt_pk_bf16_f32 v13,v16,v17
M 18,a[72:75],30
L 3
v_lshrrev_b32 v62,8,v0
v_lshl_add_u32 v62,v62,5,v0
M 18,a[80:83],34
s_sub_u32 s75,s66,s85
R 54,2,15360
v_add_u32 v62,s73,v62
M 18,a[88:91],38
s_mov_b64 s[40:41],s[86:87]
ds_write_b128 v62,v[10:13] offset:0
M 22,a[84:87],34
s_mov_b32 s42,4194304
R 26,2,16384
ds_read2_b32 v[14:15],v5 offset0:0 offset1:16
M 22,a[92:95],38
L 5
s_mov_b32 s43,0x00020000
ds_read2_b32 v[16:17],v5 offset0:32 offset1:48
M 22,a[100:103],42
s_add_u32 s64,s75,0
s_lshl_b32 s62,s54,15
R 30,2,17408
M 22,a[108:111],46
D 0,56,3072
M 18,a[96:99],42
s_add_u32 m0,s57,4096
s_add_u32 s56,s56,4096
R 34,2,18432
M 18,a[104:107],46
L 6
s_mov_b32 s67,s65
s_add_u32 s74,s49,s62
M 18,a[112:115],50
s_add_u32 s65,s47,s62
s_mov_b32 s54,s61
R 38,2,19456
M 18,a[120:123],54
L 3
M 22,a[116:119],50
R 42,2,20480
M 22,a[124:127],54
L 3
D 0,56,0
M 22,a[132:135],26
R 46,2,21504
)"
R"(M 22,a[140:143],30
M 18,a[128:131],26
R 50,2,22528
M 18,a[136:139],30
L 3
M 18,a[144:147],34
R 54,2,23552
M 18,a[152:155],38
M 22,a[148:151],34
R 26,2,24576
M 22,a[156:159],38
L 3
D 0,56,1024
M 22,a[164:167],42
R 30,2,25600
M 22,a[172:175],46
M 18,a[160:163],42
R 34,2,26624
M 18,a[168:171],46
L 3
M 18,a[176:179],50
s_waitcnt vmcnt(8)
R 38,2,27648
M 18,a[184:187],54
s_barrier
M 22,a[180:183],50
R 42,2,28672
M 22,a[188:191],54
L 3
D 0,56,2048
M 22,a[196:199],26
s_add_u32 m0,s59,1024
s_add_u32 s56,s55,28672
R 46,2,29696
M 22,a[204:207],30
s_mul_i32 s55,s61,114688
s_add_u32 s55,s55,s60
M 18,a[192:195],26
R 50,2,30720
M 18,a[200:203],30
L 3
M 18,a[208:211],34
R 54,2,31744
M 18,a[216:219],38
M 22,a[212:215],34
R 26,2,32768
M 22,a[220:223],38
L 3
buffer_load_dword v8,s[36:39],s58 offen offset:0 sc1 lds
M 22,a[228:231],42
s_add_u32 m0,s57,28672
R 30,2,33792
M 22,a[236:239],46
D 0,56,0
M 18,a[224:227],42
R 34,2,34816
M 18,a[232:235],46
L 3
M 18,a[240:243],50
R 38,2,35840
M 18,a[248:251],54
M 22,a[244:247],50
R 42,2,36864
M 22,a[252:255],54
L 3
D 0,56,1024
M 22,v[68:71],26
R 46,2,37888
M 22,v[76:79],30
M 18,v[64:67],26
R 50,2,38912
M 18,v[72:75],30
L 3
M 18,v[80:83],34
R 54,2,39936
M 18,v[88:91],38
D 0,56,2048
M 22,v[84:87],34
R 26,2,40960
M 22,v[92:95],38
L 3
M 22,v[100:103],42
R 30,2,41984
M 22,v[108:111],46
M 18,v[96:99],42
R 34,2,43008
M 18,v[104:107],46
L 3
D 0,56,3072
M 18,v[112:115],50
s_add_u32 m0,s57,32768
s_add_u32 s56,s56,4096
R 38,2,44032
M 18,v[120:123],54
s_sub_u32 s57,s84,s57
M 22,v[116:119],50
R 42,2,45056
M 22,v[124:127],54
L 3
M 22,v[132:135],26
R 46,2,46080
M 22,v[140:143],30
D 0,56,0
M 18,v[128:131],26
R 50,2,47104
M 18,v[136:139],30
L 3
M 18,v[144:147],34
R 54,2,48128
M 18,v[152:155],38
M 22,v[148:151],34
R 26,2,49152
M 22,v[156:159],38
L 3
D 0,56,1024
M 22,v[164:167],42
R 30,2,50176
M 22,v[172:175],46
M 18,v[160:163],42
R 34,2,51200
M 18,v[168:171],46
L 3
M 18,v[176:179],50
R 38,2,52224
M 18,v[184:187],54
D 0,56,2048
M 22,v[180:183],50
R 42,2,53248
M 22,v[188:191],54
L 3
M 22,v[196:199],26
R 46,2,54272
M 22,v[204:207],30
M 18,v[192:195],26
R 50,2,55296
M 18,v[200:203],30
L 3
M 18,v[208:211],34
s_waitcnt vmcnt(7)
R 54,2,56320
M 18,v[216:219],38
s_barrier
ds_read_b32 v7,v6 offset:1024
M 22,v[212:215],34
R 26,1,0
M 22,v[220:223],38
L 4
M 22,v[228:231],42
R 30,1,1024
M 22,v[236:239],46
buffer_atomic_pk_add_bf16 v14,v9,s[40:43],s64 offen sc1
M 18,v[224:227],42
s_add_u32 s64,s75,8192
R 34,1,2048
M 18,v[232:235],46
L 4
buffer_atomic_pk_add_bf16 v15,v9,s[40:43],s64 offen sc1
M 18,v[240:243],50
s_add_u32 s64,s75,16384
L 3
R 38,1,3072
v_cmp_eq_u32 vcc,s53,v7
M 18,v[248:251],54
buffer_atomic_pk_add_bf16 v16,v9,s[40:43],s64 offen sc1
M 22,v[244:247],50
L 2
s_add_u32 s64,s75,24576
s_cmp_eq_u32 vcc_lo,-1
R 42,1,4096
M 22,v[252:255],54
buffer_atomic_pk_add_bf16 v17,v9,s[40:43],s64 offen sc1
s_cbranch_scc1 Lg4_Fa0_%=
Lg4_Ka0_%=:
Lg4_La0_%=:
s_nop 0
v_mfma_f32_16x16x32_bf16 v[14:17],a[4:7],v[26:29],0
s_add_u32 m0,s59,3072
R 46,1,5120
v_lshlrev_b32 v62,16,v58
M 14,a[12:15],30
buffer_load_dword v8,s[36:39],s58 offen offset:0 sc1 lds
v_mfma_f32_16x16x32_bf16 v[10:13],a[0:3],v[26:29],0
s_sub_u32 s75,s67,s85
R 50,1,6144
v_add_f32 v18,v18,v62
M 10,a[8:11],30
L 3
v_and_b32 v63,0xffff0000,v58
v_add_f32 v19,v19,v63
M 10,a[16:19],34
s_mov_b64 s[40:41],s[86:87]
R 54,1,7168
v_lshlrev_b32 v62,16,v59
M 10,a[24:27],38
s_mov_b32 s42,4194304
v_add_f32 v20,v20,v62
v_and_b32 v63,0xffff0000,v59
M 14,a[20:23],34
s_mov_b32 s43,0x00020000
R 26,1,8192
v_add_f32 v21,v21,v63
M 14,a[28:31],38
L 3
v_lshlrev_b32 v62,16,v60
v_add_f32 v22,v22,v62
M 14,a[36:39],42
s_add_u32 s64,s75,0
R 30,1,9216
v_and_b32 v63,0xffff0000,v60
M 14,a[44:47],46
s_lshl_b32 s62,s54,15
v_add_f32 v23,v23,v63
v_lshlrev_b32 v62,16,v61
M 10,a[32:35],42
s_mov_b32 s66,s65
R 34,1,10240
v_add_f32 v24,v24,v62
M 10,a[40:43],46
L 3
v_and_b32 v63,0xffff0000,v61
v_add_f32 v25,v25,v63
M 10,a[48:51],50
s_add_u32 s65,s47,s62
R 38,1,11264
v_cvt_pk_bf16_f32 v18,v18,v19
M 10,a[56:59],54
buffer_load_dwordx4 v[58:61],v0,s[36:39],s74 offen offset:0 sc1
M 14,a[52:55],50
s_add_u32 s74,s49,s62
R 42,1,12288
v_cvt_pk_bf16_f32 v19,v20,v21
M 14,a[60:63],54
L 3
v_cvt_pk_bf16_f32 v20,v22,v23
v_cvt_pk_bf16_f32 v21,v24,v25
M 14,a[68:71],26
R 46,1,13312
v_lshrrev_b32 v62,8,v0
M 14,a[76:79],30
v_lshl_add_u32 v62,v62,5,v0
v_add_u32 v62,s73,v62
M 10,a[64:67],26
R 50,1,14336
M 10,a[72:75],30
L 3
ds_write_b128 v62,v[18:21] offset:0
M 10,a[80:83],34
R 54,1,15360
ds_read2_b32 v[22:23],v5 offset0:0 offset1:16
M 10,a[88:91],38
ds_read2_b32 v[24:25],v5 offset0:32 offset1:48
M 14,a[84:87],34
R 26,1,16384
M 14,a[92:95],38
L 6
M 14,a[100:103],42
R 30,1,17408
M 14,a[108:111],46
M 10,a[96:99],42
R 34,1,18432
M 10,a[104:107],46
L 5
L 3
M 10,a[112:115],50
R 38,1,19456
M 10,a[120:123],54
buffer_atomic_pk_add_bf16 v22,v9,s[40:43],s64 offen sc1
M 14,a[116:119],50
s_add_u32 s64,s75,8192
R 42,1,20480
M 14,a[124:127],54
L 3
buffer_atomic_pk_add_bf16 v23,v9,s[40:43],s64 offen sc1
M 14,a[132:135],26
s_add_u32 s64,s75,16384
R 46,1,21504
M 14,a[140:143],30
buffer_atomic_pk_add_bf16 v24,v9,s[40:43],s64 offen sc1
M 10,a[128:131],26
s_add_u32 s64,s75,24576
R 50,1,22528
M 10,a[136:139],30
L 3
buffer_atomic_pk_add_bf16 v25,v9,s[40:43],s64 offen sc1
M 10,a[144:147],34
R 54,1,23552
M 10,a[152:155],38
M 14,a[148:151],34
R 26,1,24576
M 14,a[156:159],38
L 3
M 14,a[164:167],42
R 30,1,25600
M 14,a[172:175],46
M 10,a[160:163],42
R 34,1,26624
M 10,a[168:171],46
L 3
M 10,a[176:179],50
s_waitcnt vmcnt(5)
R 38,1,27648
M 10,a[184:187],54
s_barrier
ds_read_b32 v7,v6 offset:3072
M 14,a[180:183],50
R 42,1,28672
M 14,a[188:191],54
L 4
M 14,a[196:199],26
R 46,1,29696
M 14,a[204:207],30
M 10,a[192:195],26
R 50,1,30720
M 10,a[200:203],30
L 4
L 3
v_cmp_eq_u32 vcc,s53,v7
M 10,a[208:211],34
R 54,1,31744
M 10,a[216:219],38
M 14,a[212:215],34
s_cmp_eq_u32 vcc_lo,-1
R 26,1,32768
s_cbranch_scc0 Lg4_Xa0_%=
M 14,a[220:223],38
L 3
s_add_u32 m0,s57,0
s_add_u32 s56,s55,0
M 14,a[228:231],42
s_add_u32 s61,s54,s46
s_cmp_ge_u32 s61,s63
s_cselect_b32 s61,s93,s61
R 30,1,33792
M 14,a[236:239],46
s_mul_i32 s58,s61,2560
D 0,56,0
M 10,a[224:227],42
s_cselect_b32 s46,s88,s46
s_cselect_b32 s63,s89,s63
R 34,1,34816
M 10,a[232:235],46
L 3
s_cselect_b32 s93,s50,s93
s_cselect_b32 s88,8,s88
M 10,a[240:243],50
s_cselect_b32 s89,-1,s89
s_add_u32 s58,s58,109891584
R 38,1,35840
M 10,a[248:251],54
D 0,56,1024
M 14,a[244:247],50
s_mov_b32 s54,s61
R 42,1,36864
M 14,a[252:255],54
L 3
M 14,v[68:71],26
R 46,1,37888
M 14,v[76:79],30
D 0,56,2048
M 10,v[64:67],26
R 50,1,38912
M 10,v[72:75],30
L 3
M 10,v[80:83],34
R 54,1,39936
M 10,v[88:91],38
D 0,56,3072
M 14,v[84:87],34
s_add_u32 m0,s57,4096
s_add_u32 s56,s56,4096
R 26,1,40960
M 14,v[92:95],38
L 3
M 14,v[100:103],42
R 30,1,41984
M 14,v[108:111],46
D 0,56,0
M 10,v[96:99],42
R 34,1,43008
M 10,v[104:107],46
L 3
M 10,v[112:115],50
R 38,1,44032
M 10,v[120:123],54
D 0,56,1024
M 14,v[116:119],50
R 42,1,45056
M 14,v[124:127],54
L 3
M 14,v[132:135],26
R 46,1,46080
M 14,v[140:143],30
D 0,56,2048
M 10,v[128:131],26
s_add_u32 m0,s59,0
s_add_u32 s56,s55,28672
R 50,1,47104
M 10,v[136:139],30
L 3
buffer_load_dword v8,s[36:39],s58 offen offset:0 sc1 lds
M 10,v[144:147],34
s_add_u32 m0,s57,28672
s_mul_i32 s55,s61,114688
R 54,1,48128
M 10,v[152:155],38
s_add_u32 s55,s55,s60
M 14,v[148:151],34
R 26,1,49152
M 14,v[156:159],38
L 3
D 0,56,0
M 14,v[164:167],42
R 30,1,50176
M 14,v[172:175],46
M 10,v[160:163],42
R 34,1,51200
M 10,v[168:171],46
L 3
D 0,56,1024
M 10,v[176:179],50
R 38,1,52224
M 10,v[184:187],54
D 0,56,2048
M 14,v[180:183],50
R 42,1,53248
M 14,v[188:191],54
L 3
M 14,v[196:199],26
R 46,1,54272
M 14,v[204:207],30
D 0,56,3072
M 10,v[192:195],26
s_add_u32 m0,s57,32768
s_add_u32 s56,s56,4096
R 50,1,55296
M 10,v[200:203],30
L 3
s_sub_u32 s57,s84,s57
M 10,v[208:211],34
s_waitcnt vmcnt(4)
R 54,1,56320
M 10,v[216:219],38
s_barrier
ds_read_b32 v7,v6 offset:0
M 14,v[212:215],34
R 26,2,0
M 14,v[220:223],38
L 4
D 0,56,0
M 14,v[228:231],42
R 30,2,1024
M 14,v[236:239],46
D 0,56,1024
M 10,v[224:227],42
R 34,2,2048
M 10,v[232:235],46
L 4
D 0,56,2048
M 10,v[240:243],50
L 3
R 38,2,3072
v_cmp_eq_u32 vcc,s53,v7
M 10,v[248:251],54
M 14,v[244:247],50
L 2
s_cmp_eq_u32 vcc_lo,-1
R 42,2,4096
M 14,v[252:255],54
buffer_store_dword v4,v3,s[36:39],s71 offen offset:0 sc0 sc1
buffer_store_dword v4,v3,s[36:39],s71 offen offset:0 sc0 sc1
buffer_store_dword v4,v3,s[36:39],s71 offen offset:0 sc0 sc1
buffer_store_dword v4,v3,s[36:39],s71 offen offset:0 sc0 sc1
s_cbranch_scc1 Lg4_Fa1_%=
s_branch Lg4_Ka1_%=
Lg4_Xa0_%=:
M 14,a[220:223],38
L 3
s_add_u32 m0,s59,0
M 14,a[228:231],42
R 30,1,33792
M 14,a[236:239],46
buffer_load_dword v8,s[36:39],s58 offen offset:0 sc1 lds
M 10,a[224:227],42
R 34,1,34816
M 10,a[232:235],46
L 3
M 10,a[240:243],50
R 38,1,35840
M 10,a[248:251],54
M 14,a[244:247],50
R 42,1,36864
M 14,a[252:255],54
L 3
M 14,v[68:71],26
R 46,1,37888
M 14,v[76:79],30
M 10,v[64:67],26
R 50,1,38912
M 10,v[72:75],30
L 3
)"
R"(M 10,v[80:83],34
R 54,1,39936
M 10,v[88:91],38
M 14,v[84:87],34
R 26,1,40960
M 14,v[92:95],38
L 3
M 14,v[100:103],42
R 30,1,41984
M 14,v[108:111],46
M 10,v[96:99],42
R 34,1,43008
M 10,v[104:107],46
L 3
M 10,v[112:115],50
R 38,1,44032
M 10,v[120:123],54
M 14,v[116:119],50
R 42,1,45056
M 14,v[124:127],54
L 3
M 14,v[132:135],26
R 46,1,46080
M 14,v[140:143],30
M 10,v[128:131],26
R 50,1,47104
M 10,v[136:139],30
L 3
M 10,v[144:147],34
R 54,1,48128
M 10,v[152:155],38
M 14,v[148:151],34
R 26,1,49152
M 14,v[156:159],38
L 3
M 14,v[164:167],42
R 30,1,50176
M 14,v[172:175],46
M 10,v[160:163],42
R 34,1,51200
M 10,v[168:171],46
L 3
M 10,v[176:179],50
R 38,1,52224
M 10,v[184:187],54
M 14,v[180:183],50
R 42,1,53248
M 14,v[188:191],54
L 3
M 14,v[196:199],26
R 46,1,54272
M 14,v[204:207],30
M 10,v[192:195],26
R 50,1,55296
M 10,v[200:203],30
L 3
M 10,v[208:211],34
s_waitcnt vmcnt(0)
R 54,1,56320
M 10,v[216:219],38
s_barrier
ds_read_b32 v7,v6 offset:0
M 14,v[212:215],34
M 14,v[220:223],38
L 3
M 14,v[228:231],42
M 14,v[236:239],46
M 10,v[224:227],42
M 10,v[232:235],46
L 1
L 0
M 10,v[240:243],50
v_cmp_eq_u32 vcc,s53,v7
M 10,v[248:251],54
M 14,v[244:247],50
M 14,v[252:255],54
Lg4_pa0_%=:
s_mov_b32 s80,1
s_cmp_eq_u32 vcc_lo,-1
s_cbranch_scc0 Lg4_xna0_%=
s_add_u32 m0,s57,0
s_add_u32 s56,s55,0
D 0,56,0
s_sub_u32 s75,s66,s85
v_lshlrev_b32 v62,16,v58
D 0,56,1024
v_add_f32 v10,v10,v62
D 0,56,2048
v_and_b32 v63,0xffff0000,v58
D 0,56,3072
v_add_f32 v11,v11,v63
s_add_u32 m0,s57,4096
s_add_u32 s56,s56,4096
D 0,56,0
v_lshlrev_b32 v62,16,v59
D 0,56,1024
v_add_f32 v12,v12,v62
D 0,56,2048
v_and_b32 v63,0xffff0000,v59
s_add_u32 s61,s54,s46
s_cmp_ge_u32 s61,s63
s_cselect_b32 s61,s93,s61
s_cselect_b32 s46,s88,s46
s_cselect_b32 s63,s89,s63
s_cselect_b32 s93,s50,s93
s_cselect_b32 s88,8,s88
s_cselect_b32 s89,-1,s89
s_mul_i32 s58,s61,2560
s_add_u32 s58,s58,109891584
s_add_u32 m0,s57,28672
s_add_u32 s56,s55,28672
D 0,56,0
v_add_f32 v13,v13,v63
D 0,56,1024
v_lshlrev_b32 v62,16,v60
D 0,56,2048
v_add_f32 v14,v14,v62
D 0,56,3072
v_and_b32 v63,0xffff0000,v60
s_add_u32 m0,s57,32768
s_add_u32 s56,s56,4096
D 0,56,0
v_add_f32 v15,v15,v63
D 0,56,1024
v_lshlrev_b32 v62,16,v61
D 0,56,2048
v_add_f32 v16,v16,v62
s_mul_i32 s55,s61,114688
s_add_u32 s55,s55,s60
s_sub_u32 s57,s84,s57
s_mov_b32 s54,s61
v_and_b32 v63,0xffff0000,v61
v_add_f32 v17,v17,v63
v_cvt_pk_bf16_f32 v10,v10,v11
v_cvt_pk_bf16_f32 v11,v12,v13
v_cvt_pk_bf16_f32 v12,v14,v15
v_cvt_pk_bf16_f32 v13,v16,v17
v_lshrrev_b32 v62,8,v0
v_lshl_add_u32 v62,v62,5,v0
v_add_u32 v62,s73,v62
ds_write_b128 v62,v[10:13] offset:0
ds_read2_b32 v[14:15],v5 offset0:0 offset1:16
ds_read2_b32 v[16:17],v5 offset0:32 offset1:48
s_mov_b64 s[40:41],s[86:87]
s_mov_b32 s42,4194304
s_mov_b32 s43,0x00020000
L 0
s_add_u32 s64,s75,0
buffer_atomic_pk_add_bf16 v14,v9,s[40:43],s64 offen sc1
s_add_u32 s64,s75,8192
buffer_atomic_pk_add_bf16 v15,v9,s[40:43],s64 offen sc1
s_add_u32 s64,s75,16384
buffer_atomic_pk_add_bf16 v16,v9,s[40:43],s64 offen sc1
s_add_u32 s64,s75,24576
buffer_atomic_pk_add_bf16 v17,v9,s[40:43],s64 offen sc1
s_mov_b32 s72,0x7fff0000
s_mov_b32 s69,0x7fff0000
s_mov_b32 s66,0x7fff0000
s_mov_b32 s76,0x7fff0000
s_branch Lg4_gox_%=
Lg4_xna0_%=:
s_cmp_ge_u32 s54,256
s_cbranch_scc1 Lg4_fina0_%=
s_add_u32 m0,s59,4096
s_nop 0
buffer_load_dword v8,s[36:39],s58 offen offset:0 sc1 lds
buffer_store_dword v4,v3,s[36:39],s71 offen offset:0 sc0 sc1
s_add_u32 m0,s59,5120
s_nop 0
buffer_load_dword v8,s[36:39],s58 offen offset:0 sc1 lds
buffer_store_dword v4,v3,s[36:39],s71 offen offset:0 sc0 sc1
s_mov_b32 s83,0
s_sub_u32 s75,s66,s85
v_lshlrev_b32 v62,16,v58
v_add_f32 v10,v10,v62
v_and_b32 v63,0xffff0000,v58
v_add_f32 v11,v11,v63
v_lshlrev_b32 v62,16,v59
v_add_f32 v12,v12,v62
v_and_b32 v63,0xffff0000,v59
v_add_f32 v13,v13,v63
v_lshlrev_b32 v62,16,v60
v_add_f32 v14,v14,v62
v_and_b32 v63,0xffff0000,v60
v_add_f32 v15,v15,v63
v_lshlrev_b32 v62,16,v61
v_add_f32 v16,v16,v62
v_and_b32 v63,0xffff0000,v61
v_add_f32 v17,v17,v63
v_cvt_pk_bf16_f32 v10,v10,v11
v_cvt_pk_bf16_f32 v11,v12,v13
v_cvt_pk_bf16_f32 v12,v14,v15
v_cvt_pk_bf16_f32 v13,v16,v17
v_lshrrev_b32 v62,8,v0
v_lshl_add_u32 v62,v62,5,v0
v_add_u32 v62,s73,v62
ds_write_b128 v62,v[10:13] offset:0
ds_read2_b32 v[14:15],v5 offset0:0 offset1:16
ds_read2_b32 v[16:17],v5 offset0:32 offset1:48
s_mov_b64 s[40:41],s[86:87]
s_mov_b32 s42,4194304
s_mov_b32 s43,0x00020000
L 0
s_add_u32 s64,s75,0
buffer_atomic_pk_add_bf16 v14,v9,s[40:43],s64 offen sc1
s_add_u32 s64,s75,8192
buffer_atomic_pk_add_bf16 v15,v9,s[40:43],s64 offen sc1
s_add_u32 s64,s75,16384
buffer_atomic_pk_add_bf16 v16,v9,s[40:43],s64 offen sc1
s_add_u32 s64,s75,24576
buffer_atomic_pk_add_bf16 v17,v9,s[40:43],s64 offen sc1
s_mov_b32 s72,0x7fff0000
s_mov_b32 s69,0x7fff0000
s_mov_b32 s66,0x7fff0000
s_mov_b32 s76,0x7fff0000
s_branch Lg4_poll_%=
Lg4_fina0_%=:
s_sub_u32 s75,s66,s85
v_lshlrev_b32 v62,16,v58
v_add_f32 v10,v10,v62
v_and_b32 v63,0xffff0000,v58
v_add_f32 v11,v11,v63
v_lshlrev_b32 v62,16,v59
v_add_f32 v12,v12,v62
v_and_b32 v63,0xffff0000,v59
v_add_f32 v13,v13,v63
v_lshlrev_b32 v62,16,v60
v_add_f32 v14,v14,v62
v_and_b32 v63,0xffff0000,v60
v_add_f32 v15,v15,v63
v_lshlrev_b32 v62,16,v61
v_add_f32 v16,v16,v62
v_and_b32 v63,0xffff0000,v61
v_add_f32 v17,v17,v63
v_cvt_pk_bf16_f32 v10,v10,v11
v_cvt_pk_bf16_f32 v11,v12,v13
v_cvt_pk_bf16_f32 v12,v14,v15
v_cvt_pk_bf16_f32 v13,v16,v17
v_lshrrev_b32 v62,8,v0
v_lshl_add_u32 v62,v62,5,v0
v_add_u32 v62,s73,v62
ds_write_b128 v62,v[10:13] offset:0
ds_read2_b32 v[14:15],v5 offset0:0 offset1:16
ds_read2_b32 v[16:17],v5 offset0:32 offset1:48
s_mov_b64 s[40:41],s[86:87]
s_mov_b32 s42,4194304
s_mov_b32 s43,0x00020000
L 0
s_add_u32 s64,s75,0
buffer_atomic_pk_add_bf16 v14,v9,s[40:43],s64 offen sc1
s_add_u32 s64,s75,8192
buffer_atomic_pk_add_bf16 v15,v9,s[40:43],s64 offen sc1
s_add_u32 s64,s75,16384
buffer_atomic_pk_add_bf16 v16,v9,s[40:43],s64 offen sc1
s_add_u32 s64,s75,24576
buffer_atomic_pk_add_bf16 v17,v9,s[40:43],s64 offen sc1
s_mov_b32 s72,0x7fff0000
s_mov_b32 s69,0x7fff0000
s_mov_b32 s66,0x7fff0000
s_mov_b32 s76,0x7fff0000
s_branch Lg4_done_%=
Lg4_Ka1_%=:
Lg4_La1_%=:
s_nop 0
v_mfma_f32_16x16x32_bf16 v[22:25],a[4:7],v[26:29],0
s_add_u32 m0,s59,3072
R 46,2,5120
v_lshlrev_b32 v62,16,v58
M 22,a[12:15],30
buffer_load_dword v8,s[36:39],s58 offen offset:0 sc1 lds
v_mfma_f32_16x16x32_bf16 v[18:21],a[0:3],v[26:29],0
s_sub_u32 s75,s66,s85
R 50,2,6144
v_add_f32 v10,v10,v62
M 18,a[8:11],30
L 3
v_and_b32 v63,0xffff0000,v58
v_add_f32 v11,v11,v63
M 18,a[16:19],34
s_mov_b64 s[40:41],s[86:87]
R 54,2,7168
v_lshlrev_b32 v62,16,v59
M 18,a[24:27],38
s_mov_b32 s42,4194304
v_add_f32 v12,v12,v62
v_and_b32 v63,0xffff0000,v59
M 22,a[20:23],34
s_mov_b32 s43,0x00020000
R 26,2,8192
v_add_f32 v13,v13,v63
M 22,a[28:31],38
L 3
v_lshlrev_b32 v62,16,v60
v_add_f32 v14,v14,v62
M 22,a[36:39],42
s_add_u32 s64,s75,0
R 30,2,9216
v_and_b32 v63,0xffff0000,v60
M 22,a[44:47],46
s_lshl_b32 s62,s54,15
v_add_f32 v15,v15,v63
v_lshlrev_b32 v62,16,v61
M 18,a[32:35],42
s_mov_b32 s67,s65
R 34,2,10240
v_add_f32 v16,v16,v62
M 18,a[40:43],46
L 3
v_and_b32 v63,0xffff0000,v61
v_add_f32 v17,v17,v63
M 18,a[48:51],50
s_add_u32 s65,s47,s62
R 38,2,11264
v_cvt_pk_bf16_f32 v10,v10,v11
M 18,a[56:59],54
buffer_load_dwordx4 v[58:61],v0,s[36:39],s74 offen offset:0 sc1
M 22,a[52:55],50
s_add_u32 s74,s49,s62
R 42,2,12288
v_cvt_pk_bf16_f32 v11,v12,v13
M 22,a[60:63],54
L 3
v_cvt_pk_bf16_f32 v12,v14,v15
v_cvt_pk_bf16_f32 v13,v16,v17
M 22,a[68:71],26
R 46,2,13312
v_lshrrev_b32 v62,8,v0
M 22,a[76:79],30
v_lshl_add_u32 v62,v62,5,v0
v_add_u32 v62,s73,v62
M 18,a[64:67],26
R 50,2,14336
M 18,a[72:75],30
L 3
ds_write_b128 v62,v[10:13] offset:0
M 18,a[80:83],34
R 54,2,15360
ds_read2_b32 v[14:15],v5 offset0:0 offset1:16
M 18,a[88:91],38
ds_read2_b32 v[16:17],v5 offset0:32 offset1:48
M 22,a[84:87],34
R 26,2,16384
M 22,a[92:95],38
L 6
M 22,a[100:103],42
R 30,2,17408
M 22,a[108:111],46
M 18,a[96:99],42
R 34,2,18432
M 18,a[104:107],46
L 5
L 3
M 18,a[112:115],50
R 38,2,19456
M 18,a[120:123],54
buffer_atomic_pk_add_bf16 v14,v9,s[40:43],s64 offen sc1
M 22,a[116:119],50
s_add_u32 s64,s75,8192
R 42,2,20480
M 22,a[124:127],54
L 3
buffer_atomic_pk_add_bf16 v15,v9,s[40:43],s64 offen sc1
M 22,a[132:135],26
s_add_u32 s64,s75,16384
R 46,2,21504
M 22,a[140:143],30
buffer_atomic_pk_add_bf16 v16,v9,s[40:43],s64 offen sc1
M 18,a[128:131],26
s_add_u32 s64,s75,24576
R 50,2,22528
M 18,a[136:139],30
L 3
buffer_atomic_pk_add_bf16 v17,v9,s[40:43],s64 offen sc1
M 18,a[144:147],34
R 54,2,23552
M 18,a[152:155],38
M 22,a[148:151],34
R 26,2,24576
M 22,a[156:159],38
L 3
M 22,a[164:167],42
R 30,2,25600
M 22,a[172:175],46
M 18,a[160:163],42
R 34,2,26624
M 18,a[168:171],46
L 3
M 18,a[176:179],50
s_waitcnt vmcnt(5)
R 38,2,27648
M 18,a[184:187],54
s_barrier
ds_read_b32 v7,v6 offset:3072
M 22,a[180:183],50
R 42,2,28672
M 22,a[188:191],54
L 4
M 22,a[196:199],26
R 46,2,29696
M 22,a[204:207],30
M 18,a[192:195],26
R 50,2,30720
M 18,a[200:203],30
L 4
L 3
v_cmp_eq_u32 vcc,s53,v7
M 18,a[208:211],34
R 54,2,31744
M 18,a[216:219],38
M 22,a[212:215],34
s_cmp_eq_u32 vcc_lo,-1
R 26,2,32768
s_cbranch_scc0 Lg4_Xa1_%=
M 22,a[220:223],38
L 3
s_add_u32 m0,s57,0
s_add_u32 s56,s55,0
M 22,a[228:231],42
s_add_u32 s61,s54,s46
s_cmp_ge_u32 s61,s63
s_cselect_b32 s61,s93,s61
R 30,2,33792
M 22,a[236:239],46
s_mul_i32 s58,s61,2560
D 0,56,0
M 18,a[224:227],42
s_cselect_b32 s46,s88,s46
s_cselect_b32 s63,s89,s63
R 34,2,34816
M 18,a[232:235],46
L 3
s_cselect_b32 s93,s50,s93
s_cselect_b32 s88,8,s88
M 18,a[240:243],50
s_cselect_b32 s89,-1,s89
s_add_u32 s58,s58,109891584
R 38,2,35840
M 18,a[248:251],54
D 0,56,1024
M 22,a[244:247],50
s_mov_b32 s54,s61
R 42,2,36864
M 22,a[252:255],54
L 3
M 22,v[68:71],26
R 46,2,37888
M 22,v[76:79],30
D 0,56,2048
M 18,v[64:67],26
R 50,2,38912
M 18,v[72:75],30
L 3
M 18,v[80:83],34
R 54,2,39936
M 18,v[88:91],38
D 0,56,3072
M 22,v[84:87],34
s_add_u32 m0,s57,4096
s_add_u32 s56,s56,4096
R 26,2,40960
M 22,v[92:95],38
L 3
M 22,v[100:103],42
R 30,2,41984
M 22,v[108:111],46
D 0,56,0
M 18,v[96:99],42
R 34,2,43008
M 18,v[104:107],46
L 3
M 18,v[112:115],50
R 38,2,44032
M 18,v[120:123],54
D 0,56,1024
M 22,v[116:119],50
R 42,2,45056
M 22,v[124:127],54
L 3
M 22,v[132:135],26
R 46,2,46080
M 22,v[140:143],30
D 0,56,2048
M 18,v[128:131],26
s_add_u32 m0,s59,1024
s_add_u32 s56,s55,28672
R 50,2,47104
M 18,v[136:139],30
L 3
buffer_load_dword v8,s[36:39],s58 offen offset:0 sc1 lds
M 18,v[144:147],34
s_add_u32 m0,s57,28672
s_mul_i32 s55,s61,114688
R 54,2,48128
M 18,v[152:155],38
s_add_u32 s55,s55,s60
M 22,v[148:151],34
R 26,2,49152
M 22,v[156:159],38
L 3
D 0,56,0
)"
R"(M 22,v[164:167],42
R 30,2,50176
M 22,v[172:175],46
M 18,v[160:163],42
R 34,2,51200
M 18,v[168:171],46
L 3
D 0,56,1024
M 18,v[176:179],50
R 38,2,52224
M 18,v[184:187],54
D 0,56,2048
M 22,v[180:183],50
R 42,2,53248
M 22,v[188:191],54
L 3
M 22,v[196:199],26
R 46,2,54272
M 22,v[204:207],30
D 0,56,3072
M 18,v[192:195],26
s_add_u32 m0,s57,32768
s_add_u32 s56,s56,4096
R 50,2,55296
M 18,v[200:203],30
L 3
s_sub_u32 s57,s84,s57
M 18,v[208:211],34
s_waitcnt vmcnt(4)
R 54,2,56320
M 18,v[216:219],38
s_barrier
ds_read_b32 v7,v6 offset:1024
M 22,v[212:215],34
R 26,1,0
M 22,v[220:223],38
L 4
D 0,56,0
M 22,v[228:231],42
R 30,1,1024
M 22,v[236:239],46
D 0,56,1024
M 18,v[224:227],42
R 34,1,2048
M 18,v[232:235],46
L 4
D 0,56,2048
M 18,v[240:243],50
L 3
R 38,1,3072
v_cmp_eq_u32 vcc,s53,v7
M 18,v[248:251],54
M 22,v[244:247],50
L 2
s_cmp_eq_u32 vcc_lo,-1
R 42,1,4096
M 22,v[252:255],54
buffer_store_dword v4,v3,s[36:39],s71 offen offset:0 sc0 sc1
buffer_store_dword v4,v3,s[36:39],s71 offen offset:0 sc0 sc1
buffer_store_dword v4,v3,s[36:39],s71 offen offset:0 sc0 sc1
buffer_store_dword v4,v3,s[36:39],s71 offen offset:0 sc0 sc1
s_cbranch_scc1 Lg4_Fa0_%=
s_branch Lg4_Ka0_%=
Lg4_Xa1_%=:
M 22,a[220:223],38
L 3
s_add_u32 m0,s59,1024
M 22,a[228:231],42
R 30,2,33792
M 22,a[236:239],46
buffer_load_dword v8,s[36:39],s58 offen offset:0 sc1 lds
M 18,a[224:227],42
R 34,2,34816
M 18,a[232:235],46
L 3
M 18,a[240:243],50
R 38,2,35840
M 18,a[248:251],54
M 22,a[244:247],50
R 42,2,36864
M 22,a[252:255],54
L 3
M 22,v[68:71],26
R 46,2,37888
M 22,v[76:79],30
M 18,v[64:67],26
R 50,2,38912
M 18,v[72:75],30
L 3
M 18,v[80:83],34
R 54,2,39936
M 18,v[88:91],38
M 22,v[84:87],34
R 26,2,40960
M 22,v[92:95],38
L 3
M 22,v[100:103],42
R 30,2,41984
M 22,v[108:111],46
M 18,v[96:99],42
R 34,2,43008
M 18,v[104:107],46
L 3
M 18,v[112:115],50
R 38,2,44032
M 18,v[120:123],54
M 22,v[116:119],50
R 42,2,45056
M 22,v[124:127],54
L 3
M 22,v[132:135],26
R 46,2,46080
M 22,v[140:143],30
M 18,v[128:131],26
R 50,2,47104
M 18,v[136:139],30
L 3
M 18,v[144:147],34
R 54,2,48128
M 18,v[152:155],38
M 22,v[148:151],34
R 26,2,49152
M 22,v[156:159],38
L 3
M 22,v[164:167],42
R 30,2,50176
M 22,v[172:175],46
M 18,v[160:163],42
R 34,2,51200
M 18,v[168:171],46
L 3
M 18,v[176:179],50
R 38,2,52224
M 18,v[184:187],54
M 22,v[180:183],50
R 42,2,53248
M 22,v[188:191],54
L 3
M 22,v[196:199],26
R 46,2,54272
M 22,v[204:207],30
M 18,v[192:195],26
R 50,2,55296
M 18,v[200:203],30
L 3
M 18,v[208:211],34
s_waitcnt vmcnt(0)
R 54,2,56320
M 18,v[216:219],38
s_barrier
ds_read_b32 v7,v6 offset:1024
M 22,v[212:215],34
M 22,v[220:223],38
L 3
M 22,v[228:231],42
M 22,v[236:239],46
M 18,v[224:227],42
M 18,v[232:235],46
L 1
L 0
M 18,v[240:243],50
v_cmp_eq_u32 vcc,s53,v7
M 18,v[248:251],54
M 22,v[244:247],50
M 22,v[252:255],54
Lg4_pa1_%=:
s_mov_b32 s80,0
s_cmp_eq_u32 vcc_lo,-1
s_cbranch_scc0 Lg4_xna1_%=
s_add_u32 m0,s57,0
s_add_u32 s56,s55,0
D 0,56,0
s_sub_u32 s75,s67,s85
v_lshlrev_b32 v62,16,v58
D 0,56,1024
v_add_f32 v18,v18,v62
D 0,56,2048
v_and_b32 v63,0xffff0000,v58
D 0,56,3072
v_add_f32 v19,v19,v63
s_add_u32 m0,s57,4096
s_add_u32 s56,s56,4096
D 0,56,0
v_lshlrev_b32 v62,16,v59
D 0,56,1024
v_add_f32 v20,v20,v62
D 0,56,2048
v_and_b32 v63,0xffff0000,v59
s_add_u32 s61,s54,s46
s_cmp_ge_u32 s61,s63
s_cselect_b32 s61,s93,s61
s_cselect_b32 s46,s88,s46
s_cselect_b32 s63,s89,s63
s_cselect_b32 s93,s50,s93
s_cselect_b32 s88,8,s88
s_cselect_b32 s89,-1,s89
s_mul_i32 s58,s61,2560
s_add_u32 s58,s58,109891584
s_add_u32 m0,s57,28672
s_add_u32 s56,s55,28672
D 0,56,0
v_add_f32 v21,v21,v63
D 0,56,1024
v_lshlrev_b32 v62,16,v60
D 0,56,2048
v_add_f32 v22,v22,v62
D 0,56,3072
v_and_b32 v63,0xffff0000,v60
s_add_u32 m0,s57,32768
s_add_u32 s56,s56,4096
D 0,56,0
v_add_f32 v23,v23,v63
D 0,56,1024
v_lshlrev_b32 v62,16,v61
D 0,56,2048
v_add_f32 v24,v24,v62
s_mul_i32 s55,s61,114688
s_add_u32 s55,s55,s60
s_sub_u32 s57,s84,s57
s_mov_b32 s54,s61
v_and_b32 v63,0xffff0000,v61
v_add_f32 v25,v25,v63
v_cvt_pk_bf16_f32 v18,v18,v19
v_cvt_pk_bf16_f32 v19,v20,v21
v_cvt_pk_bf16_f32 v20,v22,v23
v_cvt_pk_bf16_f32 v21,v24,v25
v_lshrrev_b32 v62,8,v0
v_lshl_add_u32 v62,v62,5,v0
v_add_u32 v62,s73,v62
ds_write_b128 v62,v[18:21] offset:0
ds_read2_b32 v[22:23],v5 offset0:0 offset1:16
ds_read2_b32 v[24:25],v5 offset0:32 offset1:48
s_mov_b64 s[40:41],s[86:87]
s_mov_b32 s42,4194304
s_mov_b32 s43,0x00020000
L 0
s_add_u32 s64,s75,0
buffer_atomic_pk_add_bf16 v22,v9,s[40:43],s64 offen sc1
s_add_u32 s64,s75,8192
buffer_atomic_pk_add_bf16 v23,v9,s[40:43],s64 offen sc1
s_add_u32 s64,s75,16384
buffer_atomic_pk_add_bf16 v24,v9,s[40:43],s64 offen sc1
s_add_u32 s64,s75,24576
buffer_atomic_pk_add_bf16 v25,v9,s[40:43],s64 offen sc1
s_mov_b32 s72,0x7fff0000
s_mov_b32 s69,0x7fff0000
s_mov_b32 s67,0x7fff0000
s_mov_b32 s77,0x7fff0000
s_branch Lg4_gox_%=
Lg4_xna1_%=:
s_cmp_ge_u32 s54,256
s_cbranch_scc1 Lg4_fina1_%=
s_add_u32 m0,s59,4096
s_nop 0
buffer_load_dword v8,s[36:39],s58 offen offset:0 sc1 lds
buffer_store_dword v4,v3,s[36:39],s71 offen offset:0 sc0 sc1
s_add_u32 m0,s59,5120
s_nop 0
buffer_load_dword v8,s[36:39],s58 offen offset:0 sc1 lds
buffer_store_dword v4,v3,s[36:39],s71 offen offset:0 sc0 sc1
s_mov_b32 s83,0
s_sub_u32 s75,s67,s85
v_lshlrev_b32 v62,16,v58
v_add_f32 v18,v18,v62
v_and_b32 v63,0xffff0000,v58
v_add_f32 v19,v19,v63
v_lshlrev_b32 v62,16,v59
v_add_f32 v20,v20,v62
v_and_b32 v63,0xffff0000,v59
v_add_f32 v21,v21,v63
v_lshlrev_b32 v62,16,v60
v_add_f32 v22,v22,v62
v_and_b32 v63,0xffff0000,v60
v_add_f32 v23,v23,v63
v_lshlrev_b32 v62,16,v61
v_add_f32 v24,v24,v62
v_and_b32 v63,0xffff0000,v61
v_add_f32 v25,v25,v63
v_cvt_pk_bf16_f32 v18,v18,v19
v_cvt_pk_bf16_f32 v19,v20,v21
v_cvt_pk_bf16_f32 v20,v22,v23
v_cvt_pk_bf16_f32 v21,v24,v25
v_lshrrev_b32 v62,8,v0
v_lshl_add_u32 v62,v62,5,v0
v_add_u32 v62,s73,v62
ds_write_b128 v62,v[18:21] offset:0
ds_read2_b32 v[22:23],v5 offset0:0 offset1:16
ds_read2_b32 v[24:25],v5 offset0:32 offset1:48
s_mov_b64 s[40:41],s[86:87]
s_mov_b32 s42,4194304
s_mov_b32 s43,0x00020000
L 0
s_add_u32 s64,s75,0
buffer_atomic_pk_add_bf16 v22,v9,s[40:43],s64 offen sc1
s_add_u32 s64,s75,8192
buffer_atomic_pk_add_bf16 v23,v9,s[40:43],s64 offen sc1
s_add_u32 s64,s75,16384
buffer_atomic_pk_add_bf16 v24,v9,s[40:43],s64 offen sc1
s_add_u32 s64,s75,24576
buffer_atomic_pk_add_bf16 v25,v9,s[40:43],s64 offen sc1
s_mov_b32 s72,0x7fff0000
s_mov_b32 s69,0x7fff0000
s_mov_b32 s67,0x7fff0000
s_mov_b32 s77,0x7fff0000
s_branch Lg4_poll_%=
Lg4_fina1_%=:
s_sub_u32 s75,s67,s85
v_lshlrev_b32 v62,16,v58
v_add_f32 v18,v18,v62
v_and_b32 v63,0xffff0000,v58
v_add_f32 v19,v19,v63
v_lshlrev_b32 v62,16,v59
v_add_f32 v20,v20,v62
v_and_b32 v63,0xffff0000,v59
v_add_f32 v21,v21,v63
v_lshlrev_b32 v62,16,v60
v_add_f32 v22,v22,v62
v_and_b32 v63,0xffff0000,v60
v_add_f32 v23,v23,v63
v_lshlrev_b32 v62,16,v61
v_add_f32 v24,v24,v62
v_and_b32 v63,0xffff0000,v61
v_add_f32 v25,v25,v63
v_cvt_pk_bf16_f32 v18,v18,v19
v_cvt_pk_bf16_f32 v19,v20,v21
v_cvt_pk_bf16_f32 v20,v22,v23
v_cvt_pk_bf16_f32 v21,v24,v25
v_lshrrev_b32 v62,8,v0
v_lshl_add_u32 v62,v62,5,v0
v_add_u32 v62,s73,v62
ds_write_b128 v62,v[18:21] offset:0
ds_read2_b32 v[22:23],v5 offset0:0 offset1:16
ds_read2_b32 v[24:25],v5 offset0:32 offset1:48
s_mov_b64 s[40:41],s[86:87]
s_mov_b32 s42,4194304
s_mov_b32 s43,0x00020000
L 0
s_add_u32 s64,s75,0
buffer_atomic_pk_add_bf16 v22,v9,s[40:43],s64 offen sc1
s_add_u32 s64,s75,8192
buffer_atomic_pk_add_bf16 v23,v9,s[40:43],s64 offen sc1
s_add_u32 s64,s75,16384
buffer_atomic_pk_add_bf16 v24,v9,s[40:43],s64 offen sc1
s_add_u32 s64,s75,24576
buffer_atomic_pk_add_bf16 v25,v9,s[40:43],s64 offen sc1
s_mov_b32 s72,0x7fff0000
s_mov_b32 s69,0x7fff0000
s_mov_b32 s67,0x7fff0000
s_mov_b32 s77,0x7fff0000
s_branch Lg4_done_%=
Lg4_done_%=:
s_nop 0
s_barrier
buffer_store_dword v4,v3,s[36:39],s70 offen offset:0 sc0 sc1
s_waitcnt vmcnt(1)
s_barrier
buffer_store_dword v4,v3,s[36:39],s72 offen offset:0 sc0 sc1
s_waitcnt vmcnt(0) lgkmcnt(0)
s_barrier
s_endpgm
Lcode_g4_e:
.purgem M
.purgem R
.purgem T
.purgem D
.purgem L
)"
  :: "s"(ws), "s"(__builtin_amdgcn_readfirstlane(sWave)), "s"(__builtin_amdgcn_readfirstlane(sG0)), "s"(__builtin_amdgcn_readfirstlane(sStep)), "s"(__builtin_amdgcn_readfirstlane(sOutOff)), "s"(__builtin_amdgcn_readfirstlane(sFoutOff)), "s"(__builtin_amdgcn_readfirstlane(sAuxOff)), "s"(__builtin_amdgcn_readfirstlane(sWw)), "s"(__builtin_amdgcn_readfirstlane(sBuf)), "s"(__builtin_amdgcn_readfirstlane(sRk)), "s"(__builtin_amdgcn_readfirstlane(sExp)), "v"(vL16), "v"(vFlag), "v"(vSnapOff), "v"(vSnapRd), "v"(vWd0), "v"(vWd1), "v"(vWr0), "v"(vWr1), "v"(vSp), "v"(vSn), "s"(w4), "s"(out) : "v255", "v0", "v1", "v2", "v3", "v4", "v5", "v6", "v7", "v8", "v9", "v10", "v11", "v12", "v13", "v14", "v15", "v16", "v17", "v18", "v19", "v20", "v21", "v22", "v23", "v24", "v25", "v26", "v27", "v28", "v29", "v30", "v31", "v32", "v33", "v34", "v35", "v36", "v37", "v38", "v39", "v40", "v41", "v42", "v43", "v44", "v45", "v46", "v47", "v48", "v49", "v50", "v51", "v52", "v53", "v54", "v55", "v56", "v57", "v58", "v59", "v60", "v61", "v62", "v63", "v64", "v65", "v66", "v67", "v68", "v69", "v70", "v71", "v72", "v73", "v74", "v75", "v76", "v77", "v78", "v79", "v80", "v81", "v82", "v83", "v84", "v85", "v86", "v87", "v88", "v89", "v90", "v91", "v92", "v93", "v94", "v95", "v96", "v97", "v98", "v99", "v100", "v101", "v102", "v103", "v104", "v105", "v106", "v107", "v108", "v109", "v110", "v111", "v112", "v113", "v114", "v115", "v116", "v117", "v118", "v119", "v120", "v121", "v122", "v123", "v124", "v125", "v126", "v127", "v128", "v129", "v130", "v131", "v132", "v133", "v134", "v135", "v136", "v137", "v138", "v139", "v140", "v141", "v142", "v143", "v144", "v145", "v146", "v147", "v148", "v149", "v150", "v151", "v152", "v153", "v154", "v155", "v156", "v157", "v158", "v159", "v160", "v161", "v162", "v163", "v164", "v165", "v166", "v167", "v168", "v169", "v170", "v171", "v172", "v173", "v174", "v175", "v176", "v177", "v178", "v179", "v180", "v181", "v182", "v183", "v184", "v185", "v186", "v187", "v188", "v189", "v190", "v191", "v192", "v193", "v194", "v195", "v196", "v197", "v198", "v199", "v200", "v201", "v202", "v203", "v204", "v205", "v206", "v207", "v208", "v209", "v210", "v211", "v212", "v213", "v214", "v215", "v216", "v217", "v218", "v219", "v220", "v221", "v222", "v223", "v224", "v225", "v226", "v227", "v228", "v229", "v230", "v231", "a0", "a1", "a2", "a3", "a4", "a5", "a6", "a7", "a8", "a9", "a10", "a11", "a12", "a13", "a14", "a15", "a16", "a17", "a18", "a19", "a20", "a21", "a22", "a23", "a24", "a25", "a26", "a27", "a28", "a29", "a30", "a31", "a32", "a33", "a34", "a35", "a36", "a37", "a38", "a39", "a40", "a41", "a42", "a43", "a44", "a45", "a46", "a47", "a48", "a49", "a50", "a51", "a52", "a53", "a54", "a55", "a56", "a57", "a58", "a59", "a60", "a61", "a62", "a63", "a64", "a65", "a66", "a67", "a68", "a69", "a70", "a71", "a72", "a73", "a74", "a75", "a76", "a77", "a78", "a79", "a80", "a81", "a82", "a83", "a84", "a85", "a86", "a87", "a88", "a89", "a90", "a91", "a92", "a93", "a94", "a95", "a96", "a97", "a98", "a99", "a100", "a101", "a102", "a103", "a104", "a105", "a106", "a107", "a108", "a109", "a110", "a111", "a112", "a113", "a114", "a115", "a116", "a117", "a118", "a119", "a120", "a121", "a122", "a123", "a124", "a125", "a126", "a127", "a128", "a129", "a130", "a131", "a132", "a133", "a134", "a135", "a136", "a137", "a138", "a139", "a140", "a141", "a142", "a143", "a144", "a145", "a146", "a147", "a148", "a149", "a150", "a151", "a152", "a153", "a154", "a155", "a156", "a157", "a158", "a159", "a160", "a161", "a162", "a163", "a164", "a165", "a166", "a167", "a168", "a169", "a170", "a171", "a172", "a173", "a174", "a175", "a176", "a177", "a178", "a179", "a180", "a181", "a182", "a183", "a184", "a185", "a186", "a187", "a188", "a189", "a190", "a191", "a192", "a193", "a194", "a195", "a196", "a197", "a198", "a199", "a200", "a201", "a202", "a203", "a204", "a205", "a206", "a207", "a208", "a209", "a210", "a211", "a212", "a213", "a214", "a215", "a216", "a217", "a218", "a219", "a220", "a221", "a222", "a223", "a224", "a225", "a226", "a227", "a228", "a229", "a230", "a231", "a232", "a233", "a234", "a235", "a236", "a237", "a238", "a239", "a240", "a241", "a242", "a243", "a244", "a245", "a246", "a247", "a248", "a249", "a250", "a251", "a252", "a253", "a254", "a255", "s36", "s37", "s38", "s39", "s40", "s41", "s42", "s43", "s44", "s45", "s46", "s47", "s48", "s49", "s50", "s51", "s52", "s53", "s54", "s55", "s56", "s57", "s58", "s59", "s60", "s61", "s62", "s63", "s64", "s65", "s66", "s67", "s68", "s69", "s70", "s71", "s72", "s73", "s74", "s75", "s76", "s77", "s78", "s79", "s80", "s81", "s82", "s83", "s84", "s85", "s86", "s87", "s88", "s89", "s90", "s91", "s92", "s93", "s94", "s95", "s96", "s97", "vcc", "scc", "memory");
    __builtin_unreachable();
  } else {
    LANE_ID(lane2);
    constexpr int N1 = 33 - (NR4 - 6) * 16, T1 = LAST_TASKS > RLN * N1 ? RLN * N1 : LAST_TASKS;
    const int w = kind == 6 ? 0 : kind == 1 ? 1 + (3 - rep) * 8 + slice : N1 + rep * 8 + slice;
    if (kind != 2) for (int t = w; t < T1; t += N1) row_last(ws, out, t, lane2, wave, ep);
    for (int t = T1 + (w + 32) % (N1 + 32); t < LAST_TASKS; t += N1 + 32) row_last(ws, out, t, lane2, wave, ep);
    if (kind == 2 && rep == 0 && slice == 0 && wave == 0) set_flag(ws + OFF_EP, lane2, ep);
    CTS(14);
    asm volatile("s_endpgm\nLcode_end:" ::: "memory");
    __builtin_unreachable();
  }
}

extern "C" void kernel_launch(void* const* d_in, const int* in_sizes, int n_in, void* d_out, int out_size,
                              void* d_ws, size_t ws_size, hipStream_t stream) {
  const u16* q = (const u16*)d_in[0];
  const u16* k = (const u16*)d_in[1];
  const u16* v = (const u16*)d_in[2];
  const u16* shift_k = (const u16*)d_in[3];
  const u16* ssm_k = (const u16*)d_in[4];
  const u16* ssm_D = (const u16*)d_in[5];
  const u16* w1 = (const u16*)d_in[7];
  const u16* w2 = (const u16*)d_in[8];
  const u16* w3 = (const u16*)d_in[9];
  const u16* w4 = (const u16*)d_in[10];
  flow_kernel<<<256, 256, 0, stream>>>(q, k, v, shift_k, ssm_k, ssm_D, w1, w2, w3, w4, (char*)d_ws, (u16*)d_out);
}
```

```cpp
#include <hip/hip_runtime.h>
#include <hip/hip_bf16.h>
#include <stdint.h>
#include <type_traits>

typedef short s16x8 __attribute__((ext_vector_type(8)));
typedef short s16x4 __attribute__((ext_vector_type(4)));
typedef float f32x4 __attribute__((ext_vector_type(4)));
typedef unsigned short u16;
typedef unsigned short u16x8 __attribute__((ext_vector_type(8)));
typedef unsigned short u16x4 __attribute__((ext_vector_type(4)));
typedef float f32x2 __attribute__((ext_vector_type(2)));
typedef __bf16 bf16x2 __attribute__((ext_vector_type(2)));
typedef unsigned u32x4v __attribute__((ext_vector_type(4)));
typedef __attribute__((address_space(1))) unsigned gu32;

constexpr int S_ = 2048, D_ = 1024, F_ = 3584;

constexpr unsigned GELU_CB = 0xbeeff558u;
constexpr int WS_USED = 111243392, LAST_TASKS = 100, ALLZ = 1, U0 = 170, U1 = 224, NR4 = 8, RHELP = 1, PRE1 = 1;
constexpr int OFF_KVR = 4194304, OFF_XP = 8388608, OFF_HS = 17825792, OFF_X1 = 31457280, OFF_G = 40894464, OFF_P0 = 71303168, OFF_P1 = 89128960, OFF_Y = 98566144, OFF_S1 = 106954752, OFF_ST = 107479040, OFF_FL = 109051904, OFF_FXP = 109056000, OFF_FH = 109334528, OFF_FX1 = 109613056, OFF_FG = 109891584, OFF_FP = 110587904, OFF_FKV = 111144960, OFF_FY = 111177728, OFF_EP = 111243264, OFF_DBG = 115343360;

#define WS_DIAG 0

__device__ __forceinline__ float bf2f(u16 v) { return __uint_as_float(((unsigned)v) << 16); }
__device__ __forceinline__ u16 f2bf(float f) { return __builtin_bit_cast(u16, (__bf16)f); }
__device__ __forceinline__ float rbf(float f) { return bf2f(f2bf(f)); }
__device__ __forceinline__ void st16(void* p, u16x8 v) {
  asm volatile("global_store_dwordx4 %0, %1, off sc0 sc1\n\ts_nop 1" :: "v"(p), "v"(v) : "memory");
}
#define WAIT_VM(n) asm volatile("s_waitcnt vmcnt(%0)" ::"n"(n) : "memory")
#define WAIT_LGKM0() asm volatile("s_waitcnt lgkmcnt(0)" ::: "memory")

__device__ __forceinline__ void touch_lds(const char* p, unsigned a) {
  asm volatile("s_mov_b32 m0, %1\n\ts_nop 0\n\tglobal_load_lds_dword %0, off" :: "v"(p), "s"(a) : "memory");
}
#define CTS(i)
__device__ __forceinline__ f32x2 up2(unsigned p) { return (f32x2){__uint_as_float(p << 16), __uint_as_float(p & 0xffff0000u)}; }
__device__ __forceinline__ unsigned pk2(f32x2 v) { return __builtin_bit_cast(unsigned, __builtin_convertvector(v, bf16x2)); }
__device__ __forceinline__ f32x2 rbf2(f32x2 v) { return up2(pk2(v)); }
__device__ __forceinline__ void set_flag(char* p, int i, unsigned ep) {
  if (i < 32) __hip_atomic_store((gu32*)p + i, ep, __ATOMIC_RELAXED, __HIP_MEMORY_SCOPE_AGENT);
}
constexpr int CV_HP = (2048 + 32) * 2, CV_UR = (2048 + 256) * 2, CV_CH = 2 * CV_HP + CV_UR;
constexpr int NCV = 174;
constexpr int CV_STG = 8 * CV_CH;
constexpr int YP = D_ * 16;

struct KvRegs { u32x4v k0, k1, k2, k3, k4, k5, k6, v0, v1, v2, v3, s0, s1, s2, s3; };
#define KV_LOADS                                                                                                                       \
  "v_lshrrev_b32 %[a], 3, %[tid]\n\tv_and_b32 %[c], 7, %[tid]\n\ts_lshr_b32 s90, %[tile], 4\n\ts_and_b32 s91, %[tile], 15\n\t"          \
  "v_lshlrev_b32 %[a], 13, %[a]\n\tv_lshlrev_b32 %[c], 4, %[c]\n\ts_lshl_b32 s90, s90, 18\n\ts_lshl_b32 s91, s91, 7\n\t"                 \
  "v_add_u32 %[a], s90, %[a]\n\tv_add_u32 %[c], s91, %[c]\n\tv_subrev_u32 %[h], 0x1000, %[a]\n\tv_max_i32 %[h], 0x800, %[h]\n\t"         \
  "v_add3_u32 %[a], %[a], %[c], s92\n\tv_add_u32 %[h], %[h], %[c]\n\tv_lshlrev_b32 %[c], 2, %[c]\n\ts_waitcnt lgkmcnt(0)\n\t"            \
  "global_load_dwordx4 %3, %[a], s[76:77] offset:-4096 nt\n\tglobal_load_dwordx4 %4, %[a], s[76:77] offset:-2048 nt\n\t"                \
  "global_load_dwordx4 %5, %[a], s[76:77] nt\n\tglobal_load_dwordx4 %6, %[a], s[76:77] offset:2048 nt\n\t"                              \
  "global_load_dwordx4 %7, %[a], s[78:79] offset:-4096 nt\n\tglobal_load_dwordx4 %8, %[a], s[78:79] offset:-2048 nt\n\t"                \
  "global_load_dwordx4 %9, %[a], s[78:79] nt\n\tglobal_load_dwordx4 %10, %[a], s[78:79] offset:2048 nt\n\t"                             \
  "global_load_dwordx4 %0, %[h], s[76:77] offset:-2048 nt\n\tglobal_load_dwordx4 %1, %[h], s[76:77] nt\n\t"                             \
  "global_load_dwordx4 %2, %[h], s[76:77] offset:2048 nt\n\t"                                                                           \
  "global_load_dwordx4 %11, %[c], s[80:81]\n\tglobal_load_dwordx4 %12, %[c], s[80:81] offset:16\n\t"                                    \
  "global_load_dwordx4 %13, %[c], s[80:81] offset:32\n\tglobal_load_dwordx4 %14, %[c], s[80:81] offset:48\n\t"
#define KV_OUTS(R) "=&v"(R.k0), "=&v"(R.k1), "=&v"(R.k2), "=&v"(R.k3), "=&v"(R.k4), "=&v"(R.k5), "=&v"(R.k6), "=&v"(R.v0), "=&v"(R.v1), "=&v"(R.v2), \
                   "=&v"(R.v3), "=&v"(R.s0), "=&v"(R.s1), "=&v"(R.s2), "=&v"(R.s3), [a] "=&v"(ta), [h] "=&v"(th), [c] "=&v"(tc)
#define KV_CLOB "s74", "s75", "s76", "s77", "s78", "s79", "s80", "s81", "s82", "s83", "s90", "s91", "s92", "scc", "vcc", "memory"
template <int N>
__device__ __forceinline__ void kv_wait(KvRegs& R) {
  asm volatile("s_waitcnt vmcnt(%15)" : "+v"(R.k0), "+v"(R.k1), "+v"(R.k2), "+v"(R.k3), "+v"(R.k4), "+v"(R.k5), "+v"(R.k6), "+v"(R.v0), "+v"(R.v1), "+v"(R.v2),
               "+v"(R.v3), "+v"(R.s0), "+v"(R.s1), "+v"(R.s2), "+v"(R.s3) : "n"(N) : "memory");
}
__device__ __forceinline__ void kv_store(KvRegs& R, char* __restrict__ ws, char* T, int b, int tid) {
  const int t0 = (b >> 4) * 128, c0 = (b & 15) * 64, ch8 = tid & 7, rg = tid >> 3;
  if (t0 + 4 * rg < 3) R.k0 = R.k1 = R.k2 = (u32x4v){0u, 0u, 0u, 0u};
  typedef unsigned u32x2 __attribute__((ext_vector_type(2)));
  const u32x4v kr[7] = {R.k0, R.k1, R.k2, R.k3, R.k4, R.k5, R.k6}, vr[4] = {R.v0, R.v1, R.v2, R.v3}, sk[4] = {R.s0, R.s1, R.s2, R.s3};
#pragma unroll
  for (int j2 = 0; j2 < 4; ++j2) {
    const f32x2 sa = up2(sk[j2][0]), sb = up2(sk[j2][1]), sc = up2(sk[j2][2]), sd = up2(sk[j2][3]);
    const f32x2 s0 = {sa[0], sc[0]}, s1 = {sa[1], sc[1]}, s2 = {sb[0], sd[0]}, s3 = {sb[1], sd[1]};
    f32x2 kf[7], o[4];
#pragma unroll
    for (int r = 0; r < 7; ++r) kf[r] = up2(kr[r][j2]);
#pragma unroll
    for (int r = 0; r < 4; ++r)
      o[r] = __builtin_elementwise_fma(kf[r + 3], s0, __builtin_elementwise_fma(kf[r + 2], s1, __builtin_elementwise_fma(kf[r + 1], s2, kf[r] * s3))) * up2(vr[r][j2]);
#pragma unroll
    for (int e = 0; e < 2; ++e)
      *(u32x2*)(T + (8 * ch8 + 2 * j2 + e) * 264 + (124 - 4 * rg) * 2) = (u32x2){pk2((f32x2){o[3][e], o[2][e]}), pk2((f32x2){o[1][e], o[0][e]})};
  }
  __syncthreads();
  u16* kvr = (u16*)(ws + OFF_KVR);
#pragma unroll
  for (int i = 0; i < 4; ++i) {
    const int idx = tid + 256 * i, ch = idx >> 4, cb = idx & 15;
    const u16x4 lo = *(const u16x4*)(T + ch * 264 + cb * 16), hi = *(const u16x4*)(T + ch * 264 + cb * 16 + 8);
    st16(kvr + (size_t)(c0 + ch) * S_ + (1920 - t0) + cb * 8, __builtin_shufflevector(lo, hi, 0, 1, 2, 3, 4, 5, 6, 7));
  }
}

#define LASTSLEEP 4
__device__ __forceinline__ void wait_flags(const char* p, unsigned ep) {
  gu32* f = (gu32*)p;
  for (int it = 0; it < 20000; ++it) {
    if (__all(__hip_atomic_load(f, __ATOMIC_RELAXED, __HIP_MEMORY_SCOPE_AGENT) == ep)) break;
    __builtin_amdgcn_s_sleep(LASTSLEEP);
  }
  asm volatile("" ::: "memory");
}
__device__ __forceinline__ void row_last(char* __restrict__ ws, u16* __restrict__ out, int t, int lane, int wave, unsigned ep) {
  const __amdgpu_buffer_rsrc_t rs = __builtin_amdgcn_make_buffer_rsrc(ws, 0, WS_USED, 0x00020000);
  const int r8 = lane & 7, seg = lane >> 3, un = 128 + (t >> 1);
  const unsigned fo = un * 32768 + (wave * 8 + (seg >> 2)) * 1024 + ((seg & 3) * 16 + (t & 1) * 8 + r8) * 16;
  wait_flags(ws + OFF_FP + un * 2048 + ((lane & 2) * 4 + wave * 2 + (lane & 1)) * 128, ep);
  u32x4v p0[4], p1[4];
#pragma unroll
  for (int i = 0; i < 4; ++i) {
    p0[i] = __builtin_bit_cast(u32x4v, __builtin_amdgcn_raw_buffer_load_b128(rs, fo + i * 2048, OFF_P0, 16));
    p1[i] = __builtin_bit_cast(u32x4v, __builtin_amdgcn_raw_buffer_load_b128(rs, fo + i * 2048, OFF_P1, 16));
  }
#pragma unroll
  for (int i = 0; i < 4; ++i) {
    u32x4v o;
#pragma unroll
    for (int e = 0; e < 4; ++e) o[e] = pk2(up2(p0[i][e]) + up2(p1[i][e]));
    st16(out + (size_t)(t * 8 + r8) * D_ + wave * 256 + i * 64 + seg * 8, __builtin_bit_cast(u16x8, o));
  }
}

__device__ __forceinline__ int swzB(int r) { return ((((r >> 1) & 1) | (((r >> 3) & 1) << 1)) << 1); }
#define LANE_ID(x) int x; asm volatile("v_mbcnt_lo_u32_b32 %0, -1, 0\n\tv_mbcnt_hi_u32_b32 %0, -1, %0" : "=v"(x))
#define W12 6
#define RLN 3
#define QTB 1
constexpr int QT0 = 34 * 128;
#define CODE_MAX(x, y) "(((" x ">" y ")&" x ")|((" x "<=" y ")&" y "))"
constexpr int HU = 40960;
#define G3OR 5
constexpr int NRW = 22 - G3OR;
__global__ __launch_bounds__(256, 1) void flow_kernel(const u16* __restrict__ q, const u16* __restrict__ kin, const u16* __restrict__ vin,
                                                      const u16* __restrict__ shift_k, const u16* __restrict__ ssm_k, const u16* __restrict__ ssm_D,
                                                      const u16* __restrict__ w1, const u16* __restrict__ w2,
                                                      const u16* __restrict__ w3, const u16* __restrict__ w4, char* __restrict__ ws,
                                                      u16* __restrict__ out) {
  __shared__ __attribute__((aligned(16))) char lds[163840];
  KvRegs KR;
  unsigned ew, ta, th, tc;
  int b, tile;
  asm volatile("Lcode_b:\n\ts_load_dwordx4 s[76:79], %[ka], 0x8\n\ts_load_dwordx2 s[82:83], %[ka], 0x50\n\t"
               "s_getpc_b64 s[74:75]\n\ts_lshr_b32 s90, %[bi], 3\n\ts_mov_b32 m0, 0x27f00\n\tv_lshlrev_b32 %[a], 5, %[tid]\n\tv_add_lshl_u32 %[a], %[a], s90, 7\n\t"
               "v_min_u32 %[a], " CODE_MAX(CODE_MAX(CODE_MAX("(Lcode_g3_e-Lcode_b)", "(Lcode_g3o_e-Lcode_b)"), CODE_MAX("(Lcode_g4_e-Lcode_b)", "(Lcode_g12_e-Lcode_b)")), "(Lcode_end-Lcode_b)") "-64, %[a]\n\t"
               "global_load_lds_dword %[a], s[74:75]\n\ts_load_dwordx2 s[80:81], %[ka], 0x18\n\t"
               "s_mov_b32 %[b], %[bi]\n\ts_bfe_u32 s91, %[bi], 0x20001\n\ts_lshl2_add_u32 %[tile], s90, s91\n\ts_movk_i32 s92, 0x1000\n\t"
               "s_lshl_b32 s90, %[bi], 7\n\ts_add_u32 %[tile], %[tile], s90\n\ts_add_u32 %[tile], %[tile], 64\n\ts_and_b32 %[tile], %[tile], 255\n\t"
               KV_LOADS "v_mov_b32 %[a], %[eo]\n\tglobal_load_dword %[ew], %[a], s[82:83] sc1"
               : KV_OUTS(KR), [ew] "=&v"(ew), [b] "=&s"(b), [tile] "=&s"(tile)
               : [ka] "s"(__builtin_amdgcn_kernarg_segment_ptr()), [bi] "s"(blockIdx.x), [tid] "v"(threadIdx.x), [eo] "n"(OFF_EP) : KV_CLOB);
  const unsigned t_start = 0;
  const int tid = threadIdx.x, lane = tid & 63, wave = __builtin_amdgcn_readfirstlane(tid >> 6);
  const int xcd = b & 7, idx = b >> 3;
  const unsigned sLds = (unsigned)(size_t)lds;
  const int o8 = idx & 7;
  int kind, rep, slice, kh = 0;
  int j3 = 0, j4 = 0;
  const int xp = xcd >> 1, pr = xp & 1;
  if (!(xcd & 1)) {
    if (idx < 20) { kind = 4; rep = idx < 16 ? (xp >> 1) * 3 + (idx >> 3) : 2; kh = pr; slice = idx < 16 ? o8 : (xp >> 1) * 4 + o8; j4 = rep * 16 + kh * 8 + slice; }
    else if (idx >= 24) { kind = 1; rep = xp; slice = o8; }
    else { kind = 3; rep = 4; slice = xp * 4 + idx - 20; j3 = 72 + slice; }
  } else if (idx >= 24) { kind = 2; rep = xp; slice = o8; }
  else if (idx < 18) { kind = 3; rep = pr * 2 + (idx >= 9); slice = (xp >> 1) * 9 + idx - 9 * (idx >= 9); j3 = rep * 18 + slice; }
  else if (idx < 22 || b == 191) { kind = 6; rep = 0; slice = idx < 22 ? xp * 4 + idx - 18 : 16; }
  else if (idx == 22 || xp == 0) { kind = 5; rep = idx == 22 ? pr * 2 + (xp >> 1) : 4; slice = 18; j3 = 90 + rep; }
  else { kind = 3; rep = 4; slice = 15 + xp; j3 = 72 + slice; }
  CTS(0);
  const unsigned sScr = sLds + 163584;
  unsigned ep = 0;
  auto w_touch = [&](const u16* w) {
#pragma unroll
    for (int i = 0; i < 2; ++i) touch_lds((const char*)w + (size_t)(rep * 256 + i * 128 + (tid >> 1)) * 2048 + slice * 256 + (tid & 1) * 128, sScr);
  };
  {
  const bool isconv = kind >= 3 && kind != 6 && j3 < 94;
  const int nw = (kind == 4 || j3 >= 92) ? 2 : 1, c0 = kind == 4 ? 8 * j4 : j3 < 92 ? 640 + 4 * j3 : j3 < 94 ? 272 + 8 * j3 : 0, cw = c0 + nw * wave;
  const unsigned hd = isconv ? sLds + 2 * wave * CV_CH + 32 : 0x30000u;
  auto h_dma = [&](bool first) {
#pragma unroll
    for (int j = 0; j < 2; ++j) {
      const u16* hp = ssm_k + (size_t)cw * S_ + (isconv && j < nw ? j * S_ + lane * 8 : 0);
      if (first) asm volatile("s_mov_b32 m0, %1\n\ts_nop 0\n\tglobal_load_lds_dwordx4 %0, off nt" :: "v"(hp), "s"(hd + j * CV_CH) : "memory");
      else asm volatile("s_mov_b32 m0, %1\n\ts_nop 0\n\tglobal_load_lds_dwordx4 %0, off offset:1024 nt\n\t"
                        "global_load_lds_dwordx4 %0, off offset:2048 nt\n\tglobal_load_lds_dwordx4 %0, off offset:3072 nt" :: "v"(hp), "s"(hd + j * CV_CH) : "memory");
    }
  };
  auto w_dma = [&](int t0, int t1) {
    const char* wp = (const char*)w1 + (16 * wave + (lane >> 3)) * 2048 + slice * 256;
    const unsigned c0_ = ((lane & 7) ^ swzB(lane >> 3)) * 16, c1_ = 8 * 2048 + ((lane & 7) ^ swzB(8 + (lane >> 3))) * 16;
#pragma unroll
    for (int t = t0; t < t1; ++t)
#pragma unroll
      for (int i = 0; i < 2; ++i)
        asm volatile("s_mov_b32 m0, %1\n\ts_nop 0\n\tglobal_load_lds_dwordx4 %0, off\n\ts_add_u32 m0, m0, 8064\n\ts_nop 0\n\tglobal_load_lds_dwordx4 %0, off offset:128"
                     :: "v"(wp + t * 131072 + (i ? c1_ : c0_)), "s"(sLds + t * 16384 + wave * 2048 + i * 1024) : "scc", "memory");
  };
  if (PRE1 && kind == 1) w_dma(0, 2);
  else { h_dma(true); h_dma(false); }
  if (isconv) *(u32x4v*)(lds + (2 * wave + (lane >> 5)) * CV_CH + 2 * CV_HP + 4096 + (lane & 31) * 16) = (u32x4v){0u, 0u, 0u, 0u};
  if (isconv && lane < 8) *(u32x4v*)(lds + (2 * wave + (lane >> 2)) * CV_CH + ((lane >> 1) & 1) * CV_HP + (lane & 1) * 16) = (u32x4v){0u, 0u, 0u, 0u};
  kv_wait<9>(KR);
  CTS(12);
  kv_store(KR, ws, lds + CV_STG, tile, tid);
  CTS(1);
  if (PRE1 && kind == 1) w_dma(2, 6);
  const unsigned dw = ((const unsigned*)ssm_D)[cw >> 1];
  asm volatile("s_cmp_eq_u32 %1, %2\n\ts_cbranch_scc1 Lew%=\n\ts_waitcnt vmcnt(0)\nLew%=:\n\ts_waitcnt vmcnt(16)" : "+v"(ew) : "s"(__builtin_amdgcn_readfirstlane(kind)), "n"(PRE1 ? 1 : 99) : "scc", "memory");
  ep = __builtin_amdgcn_readfirstlane(ew) + 1u;
  if (ep == 0u) ep = 1u;
  __builtin_amdgcn_s_barrier();
  set_flag(ws + OFF_FKV + tile * 128, tid, ep);
  CTS(2);
  if (kind < 3 && (W12 & kind) != 0) w_touch(kind == 1 ? w1 : w2);
  if (kind == 2 && (W12 & 4) != 0) w_touch(w1);
  if (isconv) {
    LANE_ID(lane);
    const int tid = wave * 64 + lane;
    const unsigned sC0 = (cw & 1) ? dw & 0xffff0000u : dw << 16, sC1 = dw & 0xffff0000u;
    const int l15 = lane & 15, lg = lane >> 4, cp = (l15 + 1) & 1, iw = kind == 4 ? j4 : 80 + j3, own = 2 * wave * CV_CH;
    const int ch = tid & (4 * nw - 1), tb = nw == 2 ? tid >> 3 : tid >> 2, slot = nw == 2 ? ch : 2 * ch;
    const unsigned sWave = wave, sBuf = nw, sAuxOff = OFF_KVR + cw * 4096, sFoutOff = OFF_FY + iw * 128, sExp = __builtin_amdgcn_readfirstlane(ep), sDbg = 110u * 1048576u + (b * 4 + wave) * 8192;
    const unsigned vL16 = lane * 16, vPA = own + cp * CV_HP + 2 * (8 * lg + l15 + 1 - cp), vPB = own + 2 * CV_HP + 2 * (2032 - 16 * l15 + 8 * lg) - 512 * 7;
    const unsigned vPU = own + 2 * CV_HP + 2 * (2044 - 16 * l15 - 4 * lg) - 512 * 7, vPW = 32 * l15 + 8 * lg;
    const unsigned vOR = slot * CV_CH + tb * 16, vORA = CV_STG + slot * 1024 + tb * 16, vOG = tb * YP + (c0 + ch) * 16;
    const unsigned vFA = ((lane & 3) * 16 + (c0 >> 6)) * 128, vFB = ((4 + lane % 12) * 16 + (c0 >> 6)) * 128, vFlag = tid < 32 ? tid * 4 : 0x80000000u;
    const unsigned vTW = lane < 42 ? (iw * 168 + wave * 42 + lane) * 128 : 0x80000000u;
    const unsigned vTX = lane < 44 ? ((kind == 4 ? j4 : j3 - 12) * 176 + wave * 44 + lane) * 128 : 0x80000000u;
    (void)sDbg;
    asm volatile(
R"(.macro M d,w,x
v_mfma_f32_16x16x32_bf16 v[\d:\d+3], \w, v[\x:\x+3], v[\d:\d+3]
.endm
.macro R d,a,o
ds_read_b128 v[\d:\d+3], v\a offset:\o
.endm
.macro T d,a,o
ds_read_b64_tr_b16 \d, v\a offset:\o
.endm
.macro D a,s,o
buffer_load_dwordx4 v\a, s[36:39], s\s offen offset:\o sc1 lds
.endm
.macro L c
s_waitcnt lgkmcnt(\c)
.endm
Lcode_cv_b:
s_mov_b64 s[36:37],%0
s_mov_b32 s38,111243392
s_mov_b32 s39,0x00020000
s_mov_b32 s44,%1
s_mov_b32 s51,%2
s_mov_b32 s49,%3
s_mov_b32 s48,%4
s_mov_b32 s86,%5
s_mov_b32 s87,%6
s_mov_b32 s53,%7
v_mov_b32 v0,%8
v_mov_b32 v1,%9
v_mov_b32 v2,%10
v_mov_b32 v3,%11
v_mov_b32 v4,%12
v_mov_b32 v5,%13
v_mov_b32 v6,%14
v_mov_b32 v7,%15
v_mov_b32 v8,%16
v_mov_b32 v9,%17
v_mov_b32 v10,%18
v_mov_b32 v11,%19
v_mov_b32 v12,%20
s_mov_b32 s58,111144960
buffer_load_dword v13,v8,s[36:39],s58 offen offset:0 sc1
s_mul_i32 s57,s44,25856
v_add_u32 v18,s57,v0
v_add_u32 v18,32,v18
v_mov_b32 v19,s57
R 88,18,0
ds_read_b32 v92,v18 offset:16
R 152,18,12928
ds_read_b32 v156,v18 offset:12944
ds_read_b32 v24,v19 offset:32
ds_read_b32 v25,v19 offset:12960
s_mov_b64 s[40:41],%21
s_mov_b32 s42,7340032
s_mov_b32 s43,0x00020000
s_mov_b64 s[60:61],%22
s_mov_b32 s62,7340032
s_mov_b32 s63,0x00020000
v_mov_b32 v23,s53
s_mov_b32 s82,0
s_add_u32 s55,s49,4096
s_cmp_eq_u32 s51,2
s_cselect_b32 s55,s55,0x7fff0000
s_cselect_b32 s56,0,0x70000000
s_add_u32 s57,s57,8320
v_add_u32 v14,1024,v1
v_add_u32 v15,2048,v1
v_add_u32 v16,3072,v1
v_add_u32 v17,12928,v1
L 4
v_alignbyte_b32 v96,v89,v88,2
v_alignbyte_b32 v97,v90,v89,2
v_alignbyte_b32 v98,v91,v90,2
v_alignbyte_b32 v99,v92,v91,2
ds_write_b128 v18,v[96:99] offset:4160
L 3
v_alignbyte_b32 v160,v153,v152,2
v_alignbyte_b32 v161,v154,v153,2
v_alignbyte_b32 v162,v155,v154,2
v_alignbyte_b32 v163,v156,v155,2
ds_write_b128 v18,v[160:163] offset:17088
L 2
v_lshlrev_b32 v24,16,v24
v_lshlrev_b32 v25,16,v25
s_mov_b64 exec,1
ds_write_b32 v19,v24 offset:4188
ds_write_b32 v19,v25 offset:17116
s_mov_b64 exec,-1
s_lshl_b32 s78,s44,11
s_add_u32 s78,s78,103424
v_add_u32 v216,s78,v4
v_add_u32 v4,v19,v4
Lcv_bpa_%=:
s_waitcnt vmcnt(0)
v_cmp_eq_u32 vcc,s53,v13
s_add_u32 s82,s82,1
s_cmp_eq_u64 vcc,-1
s_cbranch_scc1 Lcv_bqa_%=
s_sleep 1
buffer_load_dword v13,v8,s[36:39],s58 offen offset:0 sc1
s_cmp_lt_u32 s82,20000
s_cbranch_scc1 Lcv_bpa_%=
s_waitcnt vmcnt(0)
Lcv_bqa_%=:
s_add_u32 m0,s57,0
s_nop 0
D 0,49,3072
s_add_u32 m0,s57,12928
s_nop 0
D 0,55,3072
ds_read2_b32 a[0:1],v1 offset0:0 offset1:1
ds_read2_b32 a[2:3],v1 offset0:2 offset1:3
ds_read2_b32 a[4:5],v1 offset0:128 offset1:129
ds_read2_b32 a[6:7],v1 offset0:130 offset1:131
ds_read2_b32 a[8:9],v1 offset0:16 offset1:17
ds_read2_b32 a[10:11],v1 offset0:18 offset1:19
ds_read2_b32 a[12:13],v1 offset0:144 offset1:145
ds_read2_b32 a[14:15],v1 offset0:146 offset1:147
ds_read2_b32 a[16:17],v1 offset0:32 offset1:33
ds_read2_b32 a[18:19],v1 offset0:34 offset1:35
ds_read2_b32 a[20:21],v1 offset0:160 offset1:161
ds_read2_b32 a[22:23],v1 offset0:162 offset1:163
ds_read2_b32 a[24:25],v1 offset0:48 offset1:49
ds_read2_b32 a[26:27],v1 offset0:50 offset1:51
ds_read2_b32 a[28:29],v1 offset0:176 offset1:177
ds_read2_b32 a[30:31],v1 offset0:178 offset1:179
ds_read2_b32 a[32:33],v1 offset0:64 offset1:65
ds_read2_b32 a[34:35],v1 offset0:66 offset1:67
ds_read2_b32 a[36:37],v1 offset0:192 offset1:193
ds_read2_b32 a[38:39],v1 offset0:194 offset1:195
ds_read2_b32 a[40:41],v1 offset0:80 offset1:81
ds_read2_b32 a[42:43],v1 offset0:82 offset1:83
ds_read2_b32 a[44:45],v1 offset0:208 offset1:209
ds_read2_b32 a[46:47],v1 offset0:210 offset1:211
ds_read2_b32 a[48:49],v1 offset0:96 offset1:97
ds_read2_b32 a[50:51],v1 offset0:98 offset1:99
ds_read2_b32 a[52:53],v1 offset0:224 offset1:225
ds_read2_b32 a[54:55],v1 offset0:226 offset1:227
ds_read2_b32 a[56:57],v1 offset0:112 offset1:113
ds_read2_b32 a[58:59],v1 offset0:114 offset1:115
ds_read2_b32 a[60:61],v1 offset0:240 offset1:241
ds_read2_b32 a[62:63],v1 offset0:242 offset1:243
s_cmp_eq_u32 s51,2
s_cbranch_scc1 Lcv_a2_%=
s_waitcnt vmcnt(0)
buffer_load_dword v13,v9,s[36:39],s58 offen offset:0 sc1
L 0
R 88,2,3584
R 92,2,3072
R 96,2,3648
R 100,2,3136
R 104,2,3712
R 108,2,3200
R 112,2,3776
R 116,2,3264
L 6
v_mfma_f32_16x16x32_bf16 v[40:43],a[0:3],v[88:91],0
R 120,2,3840
R 124,2,3328
v_mfma_f32_16x16x32_bf16 v[44:47],a[0:3],v[92:95],0
M 44,a[4:7],88
L 6
M 40,a[8:11],96
R 128,2,3904
R 132,2,3392
M 44,a[8:11],100
M 44,a[12:15],96
L 6
M 40,a[16:19],104
R 136,2,3968
R 140,2,3456
M 44,a[16:19],108
M 44,a[20:23],104
L 6
M 40,a[24:27],112
R 144,2,4032
R 148,2,3520
M 44,a[24:27],116
M 44,a[28:31],112
L 6
M 40,a[32:35],120
M 44,a[32:35],124
M 44,a[36:39],120
L 4
M 40,a[40:43],128
M 44,a[40:43],132
M 44,a[44:47],128
L 2
M 40,a[48:51],136
ds_read_b64 v[24:25],v3 offset:3584
ds_read_b64 v[26:27],v3 offset:3072
M 44,a[48:51],140
M 44,a[52:55],136
L 2
M 40,a[56:59],144
M 44,a[56:59],148
M 44,a[60:63],144
L 0
v_and_b32 v36,0xffff0000,v25
v_lshlrev_b32 v37,16,v25
v_and_b32 v38,0xffff0000,v24
v_lshlrev_b32 v39,16,v24
s_nop 0
v_fma_f32 v40,v36,s86,v40
v_fma_f32 v41,v37,s86,v41
v_fma_f32 v42,v38,s86,v42
v_fma_f32 v43,v39,s86,v43
v_cvt_pk_bf16_f32 v40,v40,v41
v_cvt_pk_bf16_f32 v41,v42,v43
ds_write_b64 v216,v[40:41] offset:0
v_and_b32 v36,0xffff0000,v27
v_lshlrev_b32 v37,16,v27
v_and_b32 v38,0xffff0000,v26
v_lshlrev_b32 v39,16,v26
v_fma_f32 v44,v36,s86,v44
v_fma_f32 v45,v37,s86,v45
v_fma_f32 v46,v38,s86,v46
v_fma_f32 v47,v39,s86,v47
v_cvt_pk_bf16_f32 v44,v44,v45
v_cvt_pk_bf16_f32 v45,v46,v47
ds_write_b64 v216,v[44:45] offset:512
s_branch Lcv_a3_%=
Lcv_a2_%=:
ds_read2_b32 a[64:65],v17 offset0:0 offset1:1
ds_read2_b32 a[66:67],v17 offset0:2 offset1:3
ds_read2_b32 a[68:69],v17 offset0:128 offset1:129
ds_read2_b32 a[70:71],v17 offset0:130 offset1:131
ds_read2_b32 a[72:73],v17 offset0:16 offset1:17
ds_read2_b32 a[74:75],v17 offset0:18 offset1:19
ds_read2_b32 a[76:77],v17 offset0:144 offset1:145
ds_read2_b32 a[78:79],v17 offset0:146 offset1:147
ds_read2_b32 a[80:81],v17 offset0:32 offset1:33
ds_read2_b32 a[82:83],v17 offset0:34 offset1:35
ds_read2_b32 a[84:85],v17 offset0:160 offset1:161
ds_read2_b32 a[86:87],v17 offset0:162 offset1:163
ds_read2_b32 a[88:89],v17 offset0:48 offset1:49
ds_read2_b32 a[90:91],v17 offset0:50 offset1:51
ds_read2_b32 a[92:93],v17 offset0:176 offset1:177
ds_read2_b32 a[94:95],v17 offset0:178 offset1:179
ds_read2_b32 a[96:97],v17 offset0:64 offset1:65
ds_read2_b32 a[98:99],v17 offset0:66 offset1:67
ds_read2_b32 a[100:101],v17 offset0:192 offset1:193
ds_read2_b32 a[102:103],v17 offset0:194 offset1:195
ds_read2_b32 a[104:105],v17 offset0:80 offset1:81
ds_read2_b32 a[106:107],v17 offset0:82 offset1:83
ds_read2_b32 a[108:109],v17 offset0:208 offset1:209
ds_read2_b32 a[110:111],v17 offset0:210 offset1:211
ds_read2_b32 a[112:113],v17 offset0:96 offset1:97
ds_read2_b32 a[114:115],v17 offset0:98 offset1:99
ds_read2_b32 a[116:117],v17 offset0:224 offset1:225
ds_read2_b32 a[118:119],v17 offset0:226 offset1:227
ds_read2_b32 a[120:121],v17 offset0:112 offset1:113
ds_read2_b32 a[122:123],v17 offset0:114 offset1:115
ds_read2_b32 a[124:125],v17 offset0:240 offset1:241
ds_read2_b32 a[126:127],v17 offset0:242 offset1:243
s_waitcnt vmcnt(0)
buffer_load_dword v13,v9,s[36:39],s58 offen offset:0 sc1
L 0
R 88,2,3584
R 92,2,3072
R 96,2,3648
R 100,2,3136
R 104,2,3712
R 108,2,3200
R 112,2,3776
R 116,2,3264
L 6
v_mfma_f32_16x16x32_bf16 v[40:43],a[0:3],v[88:91],0
R 120,2,3840
R 124,2,3328
v_mfma_f32_16x16x32_bf16 v[44:47],a[0:3],v[92:95],0
M 44,a[4:7],88
L 6
M 40,a[8:11],96
R 128,2,3904
R 132,2,3392
M 44,a[8:11],100
M 44,a[12:15],96
L 6
M 40,a[16:19],104
R 136,2,3968
R 140,2,3456
M 44,a[16:19],108
M 44,a[20:23],104
L 6
M 40,a[24:27],112
R 144,2,4032
R 148,2,3520
M 44,a[24:27],116
M 44,a[28:31],112
L 6
M 40,a[32:35],120
R 152,2,16512
R 156,2,16000
M 44,a[32:35],124
M 44,a[36:39],120
L 6
M 40,a[40:43],128
R 160,2,16576
R 164,2,16064
M 44,a[40:43],132
M 44,a[44:47],128
L 6
M 40,a[48:51],136
R 168,2,16640
R 172,2,16128
M 44,a[48:51],140
ds_read_b64 v[24:25],v3 offset:3584
ds_read_b64 v[26:27],v3 offset:3072
M 44,a[52:55],136
L 8
M 40,a[56:59],144
R 176,2,16704
R 180,2,16192
M 44,a[56:59],148
M 44,a[60:63],144
L 8
v_mfma_f32_16x16x32_bf16 v[48:51],a[64:67],v[152:155],0
R 184,2,16768
R 188,2,16256
v_mfma_f32_16x16x32_bf16 v[52:55],a[64:67],v[156:159],0
M 52,a[68:71],152
L 8
M 48,a[72:75],160
R 192,2,16832
R 196,2,16320
M 52,a[72:75],164
L 6
v_and_b32 v36,0xffff0000,v25
M 52,a[76:79],160
v_lshlrev_b32 v37,16,v25
v_and_b32 v38,0xffff0000,v24
v_lshlrev_b32 v39,16,v24
s_nop 0
M 48,a[80:83],168
R 200,2,16896
R 204,2,16384
M 52,a[80:83],172
v_fma_f32 v40,v36,s86,v40
v_fma_f32 v41,v37,s86,v41
M 52,a[84:87],168
v_fma_f32 v42,v38,s86,v42
v_fma_f32 v43,v39,s86,v43
v_cvt_pk_bf16_f32 v40,v40,v41
L 6
M 48,a[88:91],176
R 208,2,16960
R 212,2,16448
M 52,a[88:91],180
v_cvt_pk_bf16_f32 v41,v42,v43
ds_write_b64 v216,v[40:41] offset:0
M 52,a[92:95],176
v_and_b32 v36,0xffff0000,v27
v_lshlrev_b32 v37,16,v27
v_and_b32 v38,0xffff0000,v26
L 7
M 48,a[96:99],184
M 52,a[96:99],188
v_lshlrev_b32 v39,16,v26
v_fma_f32 v44,v36,s86,v44
M 52,a[100:103],184
v_fma_f32 v45,v37,s86,v45
v_fma_f32 v46,v38,s86,v46
v_fma_f32 v47,v39,s86,v47
L 5
M 48,a[104:107],192
M 52,a[104:107],196
v_cvt_pk_bf16_f32 v44,v44,v45
v_cvt_pk_bf16_f32 v45,v46,v47
M 52,a[108:111],192
ds_write_b64 v216,v[44:45] offset:512
L 4
M 48,a[112:115],200
ds_read_b64 v[24:25],v3 offset:16512
ds_read_b64 v[26:27],v3 offset:16000
M 52,a[112:115],204
M 52,a[116:119],200
L 4
M 48,a[120:123],208
M 52,a[120:123],212
M 52,a[124:127],208
L 0
v_and_b32 v36,0xffff0000,v25
v_lshlrev_b32 v37,16,v25
v_and_b32 v38,0xffff0000,v24
v_lshlrev_b32 v39,16,v24
s_nop 0
v_fma_f32 v48,v36,s87,v48
v_fma_f32 v49,v37,s87,v49
v_fma_f32 v50,v38,s87,v50
v_fma_f32 v51,v39,s87,v51
v_cvt_pk_bf16_f32 v48,v48,v49
v_cvt_pk_bf16_f32 v49,v50,v51
ds_write_b64 v216,v[48:49] offset:1024
v_and_b32 v36,0xffff0000,v27
v_lshlrev_b32 v37,16,v27
v_and_b32 v38,0xffff0000,v26
v_lshlrev_b32 v39,16,v26
v_fma_f32 v52,v36,s87,v52
v_fma_f32 v53,v37,s87,v53
v_fma_f32 v54,v38,s87,v54
v_fma_f32 v55,v39,s87,v55
v_cvt_pk_bf16_f32 v52,v52,v53
v_cvt_pk_bf16_f32 v53,v54,v55
ds_write_b64 v216,v[52:53] offset:1536
Lcv_a3_%=:
L 0
s_barrier
R 152,6,0
R 156,6,512
s_mov_b32 s78,98566144
L 1
buffer_store_dwordx4 v[152:155],v7,s[36:39],s78 offen offset:0 sc0 sc1
s_add_u32 s78,s56,99090432
L 0
buffer_store_dwordx4 v[156:159],v7,s[36:39],s78 offen offset:0 sc0 sc1
s_waitcnt vmcnt(2)
v_cmp_eq_u32 vcc,s53,v13
s_nop 0
s_cmp_eq_u64 vcc,-1
s_cbranch_scc0 Lcv_a_%=
s_add_u32 m0,s57,0
s_nop 0
D 0,49,2048
D 0,49,1024
D 0,49,0
s_add_u32 m0,s57,12928
s_nop 0
D 0,55,2048
D 0,55,1024
D 0,55,0
R 104,18,1024
ds_read_b32 v108,v18 offset:1040
R 120,18,2048
ds_read_b32 v124,v18 offset:2064
R 136,18,3072
ds_read_b32 v140,v18 offset:3088
L 4
v_alignbyte_b32 v112,v105,v104,2
v_alignbyte_b32 v113,v106,v105,2
v_alignbyte_b32 v114,v107,v106,2
v_alignbyte_b32 v115,v108,v107,2
ds_write_b128 v18,v[112:115] offset:5184
L 3
v_alignbyte_b32 v128,v121,v120,2
v_alignbyte_b32 v129,v122,v121,2
v_alignbyte_b32 v130,v123,v122,2
v_alignbyte_b32 v131,v124,v123,2
ds_write_b128 v18,v[128:131] offset:6208
L 2
v_alignbyte_b32 v144,v137,v136,2
v_alignbyte_b32 v145,v138,v137,2
v_alignbyte_b32 v146,v139,v138,2
v_alignbyte_b32 v147,v140,v139,2
ds_write_b128 v18,v[144:147] offset:7232
s_waitcnt vmcnt(6)
s_barrier
buffer_store_dword v23,v10,s[36:39],s48 offen offset:0 sc0 sc1
s_branch Lcv_b_%=
Lcv_a_%=:
R 104,18,1024
ds_read_b32 v108,v18 offset:1040
R 120,18,2048
ds_read_b32 v124,v18 offset:2064
R 136,18,3072
ds_read_b32 v140,v18 offset:3088
L 4
v_alignbyte_b32 v112,v105,v104,2
v_alignbyte_b32 v113,v106,v105,2
v_alignbyte_b32 v114,v107,v106,2
v_alignbyte_b32 v115,v108,v107,2
ds_write_b128 v18,v[112:115] offset:5184
L 3
v_alignbyte_b32 v128,v121,v120,2
v_alignbyte_b32 v129,v122,v121,2
v_alignbyte_b32 v130,v123,v122,2
v_alignbyte_b32 v131,v124,v123,2
ds_write_b128 v18,v[128:131] offset:6208
L 2
v_alignbyte_b32 v144,v137,v136,2
v_alignbyte_b32 v145,v138,v137,2
v_alignbyte_b32 v146,v139,v138,2
v_alignbyte_b32 v147,v140,v139,2
ds_write_b128 v18,v[144:147] offset:7232
s_waitcnt vmcnt(0)
s_barrier
buffer_store_dword v23,v10,s[36:39],s48 offen offset:0 sc0 sc1
buffer_load_dword v13,v9,s[36:39],s58 offen offset:0 sc1
Lcv_bpb_%=:
s_waitcnt vmcnt(0)
v_cmp_eq_u32 vcc,s53,v13
s_add_u32 s82,s82,1
s_cmp_eq_u64 vcc,-1
s_cbranch_scc1 Lcv_bqb_%=
s_sleep 1
buffer_load_dword v13,v9,s[36:39],s58 offen offset:0 sc1
s_cmp_lt_u32 s82,20000
s_cbranch_scc1 Lcv_bpb_%=
s_waitcnt vmcnt(0)
Lcv_bqb_%=:
s_add_u32 m0,s57,0
)"
R"(s_nop 0
D 0,49,2048
D 0,49,1024
D 0,49,0
s_add_u32 m0,s57,12928
s_nop 0
D 0,55,2048
D 0,55,1024
D 0,55,0
Lcv_b_%=:
s_waitcnt vmcnt(0)
ds_read2_b32 v[88:89],v1 offset0:0 offset1:1
ds_read2_b32 v[90:91],v1 offset0:2 offset1:3
R 120,2,3584
ds_read2_b32 v[92:93],v1 offset0:128 offset1:129
ds_read2_b32 v[94:95],v1 offset0:130 offset1:131
R 124,2,3072
ds_read2_b32 v[96:97],v14 offset0:0 offset1:1
ds_read2_b32 v[98:99],v14 offset0:2 offset1:3
R 128,2,2560
ds_read2_b32 v[100:101],v14 offset0:128 offset1:129
ds_read2_b32 v[102:103],v14 offset0:130 offset1:131
R 132,2,2048
ds_read2_b32 v[104:105],v15 offset0:0 offset1:1
ds_read2_b32 v[106:107],v15 offset0:2 offset1:3
R 136,2,1536
ds_read2_b32 v[108:109],v15 offset0:128 offset1:129
ds_read2_b32 v[110:111],v15 offset0:130 offset1:131
R 140,2,1024
ds_read2_b32 v[112:113],v16 offset0:0 offset1:1
ds_read2_b32 v[114:115],v16 offset0:2 offset1:3
R 144,2,512
ds_read2_b32 v[116:117],v16 offset0:128 offset1:129
ds_read2_b32 v[118:119],v16 offset0:130 offset1:131
R 148,2,0
L 15
v_mfma_f32_16x16x32_bf16 v[40:43],v[88:91],v[128:131],0
ds_read2_b32 v[152:153],v1 offset0:16 offset1:17
M 40,v[92:95],124
ds_read2_b32 v[154:155],v1 offset0:18 offset1:19
M 40,v[96:99],120
L 14
R 184,2,3648
v_mfma_f32_16x16x32_bf16 v[44:47],v[88:91],v[132:135],0
ds_read2_b32 v[156:157],v1 offset0:144 offset1:145
M 44,v[92:95],128
M 44,v[96:99],124
ds_read2_b32 v[158:159],v1 offset0:146 offset1:147
M 44,v[100:103],120
L 14
R 188,2,3136
v_mfma_f32_16x16x32_bf16 v[48:51],v[88:91],v[136:139],0
ds_read2_b32 v[160:161],v14 offset0:16 offset1:17
M 48,v[92:95],132
ds_read2_b32 v[162:163],v14 offset0:18 offset1:19
M 48,v[96:99],128
M 48,v[100:103],124
R 192,2,2624
M 48,v[104:107],120
L 15
ds_read2_b32 v[164:165],v14 offset0:144 offset1:145
v_mfma_f32_16x16x32_bf16 v[52:55],v[88:91],v[140:143],0
ds_read2_b32 v[166:167],v14 offset0:146 offset1:147
M 52,v[92:95],136
R 196,2,2112
M 52,v[96:99],132
M 52,v[100:103],128
ds_read2_b32 v[168:169],v15 offset0:16 offset1:17
M 52,v[104:107],124
ds_read2_b32 v[170:171],v15 offset0:18 offset1:19
M 52,v[108:111],120
L 15
R 200,2,1600
v_mfma_f32_16x16x32_bf16 v[56:59],v[88:91],v[144:147],0
ds_read2_b32 v[172:173],v15 offset0:144 offset1:145
M 56,v[92:95],140
M 56,v[96:99],136
ds_read2_b32 v[174:175],v15 offset0:146 offset1:147
M 56,v[100:103],132
R 204,2,1088
M 56,v[104:107],128
ds_read2_b32 v[176:177],v16 offset0:16 offset1:17
M 56,v[108:111],124
ds_read2_b32 v[178:179],v16 offset0:18 offset1:19
M 56,v[112:115],120
L 15
v_mfma_f32_16x16x32_bf16 v[60:63],v[88:91],v[148:151],0
R 208,2,576
M 60,v[92:95],144
ds_read2_b32 v[180:181],v16 offset0:144 offset1:145
M 60,v[96:99],140
ds_read2_b32 v[182:183],v16 offset0:146 offset1:147
M 60,v[100:103],136
R 212,2,64
M 60,v[104:107],132
M 60,v[108:111],128
M 60,v[112:115],124
M 60,v[116:119],120
L 15
M 40,v[152:155],192
ds_read2_b32 v[88:89],v1 offset0:32 offset1:33
M 40,v[156:159],188
ds_read2_b32 v[90:91],v1 offset0:34 offset1:35
M 40,v[160:163],184
L 14
R 120,2,3712
M 44,v[152:155],196
ds_read2_b32 v[92:93],v1 offset0:160 offset1:161
M 44,v[156:159],192
M 44,v[160:163],188
ds_read2_b32 v[94:95],v1 offset0:162 offset1:163
M 44,v[164:167],184
L 14
R 124,2,3200
M 48,v[152:155],200
ds_read2_b32 v[96:97],v14 offset0:32 offset1:33
M 48,v[156:159],196
ds_read2_b32 v[98:99],v14 offset0:34 offset1:35
M 48,v[160:163],192
M 48,v[164:167],188
R 128,2,2688
M 48,v[168:171],184
L 15
ds_read2_b32 v[100:101],v14 offset0:160 offset1:161
M 52,v[152:155],204
ds_read2_b32 v[102:103],v14 offset0:162 offset1:163
M 52,v[156:159],200
R 132,2,2176
M 52,v[160:163],196
M 52,v[164:167],192
ds_read2_b32 v[104:105],v15 offset0:32 offset1:33
M 52,v[168:171],188
ds_read2_b32 v[106:107],v15 offset0:34 offset1:35
M 52,v[172:175],184
L 15
R 136,2,1664
M 56,v[152:155],208
ds_read2_b32 v[108:109],v15 offset0:160 offset1:161
M 56,v[156:159],204
M 56,v[160:163],200
ds_read2_b32 v[110:111],v15 offset0:162 offset1:163
M 56,v[164:167],196
R 140,2,1152
M 56,v[168:171],192
ds_read2_b32 v[112:113],v16 offset0:32 offset1:33
M 56,v[172:175],188
ds_read2_b32 v[114:115],v16 offset0:34 offset1:35
M 56,v[176:179],184
L 15
M 60,v[152:155],212
R 144,2,640
M 60,v[156:159],208
ds_read2_b32 v[116:117],v16 offset0:160 offset1:161
M 60,v[160:163],204
ds_read2_b32 v[118:119],v16 offset0:162 offset1:163
R 218,18,13952
M 60,v[164:167],200
R 148,2,128
ds_read_b32 v222,v18 offset:13968
M 60,v[168:171],196
M 60,v[172:175],192
M 60,v[176:179],188
M 60,v[180:183],184
L 15
M 40,v[88:91],128
ds_read2_b32 v[152:153],v1 offset0:48 offset1:49
M 40,v[92:95],124
L 1
ds_read2_b32 v[154:155],v1 offset0:50 offset1:51
v_alignbyte_b32 v226,v219,v218,2
M 40,v[96:99],120
s_nop 0
R 184,2,3776
v_alignbyte_b32 v227,v220,v219,2
M 44,v[88:91],132
ds_read2_b32 v[156:157],v1 offset0:176 offset1:177
v_alignbyte_b32 v228,v221,v220,2
M 44,v[92:95],128
v_alignbyte_b32 v229,v222,v221,2
M 44,v[96:99],124
ds_read2_b32 v[158:159],v1 offset0:178 offset1:179
M 44,v[100:103],120
s_nop 0
R 188,2,3264
M 48,v[88:91],136
ds_read2_b32 v[160:161],v14 offset0:48 offset1:49
M 48,v[92:95],132
ds_read2_b32 v[162:163],v14 offset0:50 offset1:51
M 48,v[96:99],128
ds_write_b128 v18,v[226:229] offset:18112
M 48,v[100:103],124
R 192,2,2752
M 48,v[104:107],120
s_nop 0
ds_read2_b32 v[164:165],v14 offset0:176 offset1:177
M 52,v[88:91],140
s_mov_b32 m0,163584
s_mov_b32 s78,0
ds_read2_b32 v[166:167],v14 offset0:178 offset1:179
M 52,v[92:95],136
R 196,2,2240
M 52,v[96:99],132
buffer_load_dword v11,s[40:43],s78 offen offset:0  lds
M 52,v[100:103],128
ds_read2_b32 v[168:169],v15 offset0:48 offset1:49
M 52,v[104:107],124
ds_read2_b32 v[170:171],v15 offset0:50 offset1:51
M 52,v[108:111],120
s_nop 0
R 200,2,1728
M 56,v[88:91],144
ds_read2_b32 v[172:173],v15 offset0:176 offset1:177
M 56,v[92:95],140
M 56,v[96:99],136
ds_read2_b32 v[174:175],v15 offset0:178 offset1:179
M 56,v[100:103],132
R 204,2,1216
M 56,v[104:107],128
ds_read2_b32 v[176:177],v16 offset0:48 offset1:49
M 56,v[108:111],124
ds_read2_b32 v[178:179],v16 offset0:50 offset1:51
M 56,v[112:115],120
s_nop 0
M 60,v[88:91],148
R 208,2,704
M 60,v[92:95],144
ds_read2_b32 v[180:181],v16 offset0:176 offset1:177
M 60,v[96:99],140
ds_read2_b32 v[182:183],v16 offset0:178 offset1:179
M 60,v[100:103],136
R 212,2,192
M 60,v[104:107],132
M 60,v[108:111],128
M 60,v[112:115],124
M 60,v[116:119],120
L 15
M 40,v[152:155],192
ds_read2_b32 v[88:89],v1 offset0:64 offset1:65
M 40,v[156:159],188
ds_read2_b32 v[90:91],v1 offset0:66 offset1:67
R 218,18,14976
M 40,v[160:163],184
L 15
R 120,2,3840
ds_read_b32 v222,v18 offset:14992
M 44,v[152:155],196
ds_read2_b32 v[92:93],v1 offset0:192 offset1:193
M 44,v[156:159],192
M 44,v[160:163],188
ds_read2_b32 v[94:95],v1 offset0:194 offset1:195
M 44,v[164:167],184
L 15
R 124,2,3328
M 48,v[152:155],200
ds_read2_b32 v[96:97],v14 offset0:64 offset1:65
M 48,v[156:159],196
L 4
ds_read2_b32 v[98:99],v14 offset0:66 offset1:67
v_alignbyte_b32 v226,v219,v218,2
M 48,v[160:163],192
v_alignbyte_b32 v227,v220,v219,2
v_alignbyte_b32 v228,v221,v220,2
M 48,v[164:167],188
R 128,2,2816
v_alignbyte_b32 v229,v222,v221,2
M 48,v[168:171],184
s_nop 0
ds_read2_b32 v[100:101],v14 offset0:192 offset1:193
M 52,v[152:155],204
ds_read2_b32 v[102:103],v14 offset0:194 offset1:195
M 52,v[156:159],200
R 132,2,2304
M 52,v[160:163],196
ds_write_b128 v18,v[226:229] offset:19136
M 52,v[164:167],192
ds_read2_b32 v[104:105],v15 offset0:64 offset1:65
M 52,v[168:171],188
ds_read2_b32 v[106:107],v15 offset0:66 offset1:67
M 52,v[172:175],184
s_nop 0
R 136,2,1792
M 56,v[152:155],208
ds_read2_b32 v[108:109],v15 offset0:192 offset1:193
M 56,v[156:159],204
M 56,v[160:163],200
ds_read2_b32 v[110:111],v15 offset0:194 offset1:195
M 56,v[164:167],196
R 140,2,1280
M 56,v[168:171],192
ds_read2_b32 v[112:113],v16 offset0:64 offset1:65
M 56,v[172:175],188
ds_read2_b32 v[114:115],v16 offset0:66 offset1:67
M 56,v[176:179],184
s_nop 0
M 60,v[152:155],212
R 144,2,768
M 60,v[156:159],208
ds_read2_b32 v[116:117],v16 offset0:192 offset1:193
M 60,v[160:163],204
ds_read2_b32 v[118:119],v16 offset0:194 offset1:195
M 60,v[164:167],200
R 148,2,256
M 60,v[168:171],196
M 60,v[172:175],192
M 60,v[176:179],188
M 60,v[180:183],184
L 15
M 40,v[88:91],128
ds_read2_b32 v[152:153],v1 offset0:80 offset1:81
M 40,v[92:95],124
ds_read2_b32 v[154:155],v1 offset0:82 offset1:83
M 40,v[96:99],120
L 15
R 184,2,3904
M 44,v[88:91],132
ds_read2_b32 v[156:157],v1 offset0:208 offset1:209
M 44,v[92:95],128
M 44,v[96:99],124
ds_read2_b32 v[158:159],v1 offset0:210 offset1:211
M 44,v[100:103],120
L 14
R 188,2,3392
M 48,v[88:91],136
ds_read2_b32 v[160:161],v14 offset0:80 offset1:81
M 48,v[92:95],132
ds_read2_b32 v[162:163],v14 offset0:82 offset1:83
R 218,18,16000
M 48,v[96:99],128
ds_read_b32 v222,v18 offset:16016
M 48,v[100:103],124
R 192,2,2880
M 48,v[104:107],120
L 15
ds_read2_b32 v[164:165],v14 offset0:208 offset1:209
M 52,v[88:91],140
ds_read2_b32 v[166:167],v14 offset0:210 offset1:211
M 52,v[92:95],136
R 196,2,2368
M 52,v[96:99],132
M 52,v[100:103],128
L 4
ds_read2_b32 v[168:169],v15 offset0:80 offset1:81
v_alignbyte_b32 v226,v219,v218,2
M 52,v[104:107],124
ds_read2_b32 v[170:171],v15 offset0:82 offset1:83
v_alignbyte_b32 v227,v220,v219,2
M 52,v[108:111],120
s_nop 0
R 200,2,1856
v_alignbyte_b32 v228,v221,v220,2
M 56,v[88:91],144
ds_read2_b32 v[172:173],v15 offset0:208 offset1:209
v_alignbyte_b32 v229,v222,v221,2
M 56,v[92:95],140
ds_write_b128 v18,v[226:229] offset:20160
M 56,v[96:99],136
ds_read2_b32 v[174:175],v15 offset0:210 offset1:211
M 56,v[100:103],132
R 204,2,1344
M 56,v[104:107],128
ds_read2_b32 v[176:177],v16 offset0:80 offset1:81
M 56,v[108:111],124
ds_read2_b32 v[178:179],v16 offset0:82 offset1:83
M 56,v[112:115],120
s_nop 0
M 60,v[88:91],148
R 208,2,832
M 60,v[92:95],144
ds_read2_b32 v[180:181],v16 offset0:208 offset1:209
M 60,v[96:99],140
ds_read2_b32 v[182:183],v16 offset0:210 offset1:211
M 60,v[100:103],136
R 212,2,320
M 60,v[104:107],132
M 60,v[108:111],128
M 60,v[112:115],124
M 60,v[116:119],120
L 15
M 40,v[152:155],192
ds_read2_b32 v[88:89],v1 offset0:96 offset1:97
M 40,v[156:159],188
ds_read2_b32 v[90:91],v1 offset0:98 offset1:99
M 40,v[160:163],184
L 15
R 120,2,3968
M 44,v[152:155],196
s_mov_b32 m0,163584
s_mov_b32 s78,3741696
ds_read2_b32 v[92:93],v1 offset0:224 offset1:225
M 44,v[156:159],192
M 44,v[160:163],188
ds_read2_b32 v[94:95],v1 offset0:226 offset1:227
M 44,v[164:167],184
L 15
R 124,2,3456
M 48,v[152:155],200
ds_read2_b32 v[96:97],v14 offset0:96 offset1:97
M 48,v[156:159],196
ds_read2_b32 v[98:99],v14 offset0:98 offset1:99
M 48,v[160:163],192
buffer_load_dword v11,s[40:43],s78 offen offset:0  lds
M 48,v[164:167],188
R 128,2,2944
M 48,v[168:171],184
L 15
ds_read2_b32 v[100:101],v14 offset0:224 offset1:225
M 52,v[152:155],204
ds_read2_b32 v[102:103],v14 offset0:226 offset1:227
M 52,v[156:159],200
R 132,2,2432
M 52,v[160:163],196
M 52,v[164:167],192
ds_read2_b32 v[104:105],v15 offset0:96 offset1:97
M 52,v[168:171],188
ds_read2_b32 v[106:107],v15 offset0:98 offset1:99
M 52,v[172:175],184
L 15
R 136,2,1920
M 56,v[152:155],208
ds_read2_b32 v[108:109],v15 offset0:224 offset1:225
M 56,v[156:159],204
M 56,v[160:163],200
ds_read2_b32 v[110:111],v15 offset0:226 offset1:227
M 56,v[164:167],196
R 140,2,1408
M 56,v[168:171],192
ds_read2_b32 v[112:113],v16 offset0:96 offset1:97
M 56,v[172:175],188
ds_read2_b32 v[114:115],v16 offset0:98 offset1:99
M 56,v[176:179],184
L 15
M 60,v[152:155],212
R 144,2,896
M 60,v[156:159],208
ds_read2_b32 v[116:117],v16 offset0:224 offset1:225
M 60,v[160:163],204
ds_read2_b32 v[118:119],v16 offset0:226 offset1:227
M 60,v[164:167],200
R 148,2,384
M 60,v[168:171],196
M 60,v[172:175],192
M 60,v[176:179],188
M 60,v[180:183],184
L 15
M 40,v[88:91],128
ds_read2_b32 v[152:153],v1 offset0:112 offset1:113
M 40,v[92:95],124
ds_read2_b32 v[154:155],v1 offset0:114 offset1:115
M 40,v[96:99],120
L 14
R 184,2,4032
M 44,v[88:91],132
ds_read2_b32 v[156:157],v1 offset0:240 offset1:241
M 44,v[92:95],128
M 44,v[96:99],124
ds_read2_b32 v[158:159],v1 offset0:242 offset1:243
v_add_u32 v1,12928,v1
M 44,v[100:103],120
L 14
R 188,2,3520
M 48,v[88:91],136
ds_read2_b32 v[160:161],v14 offset0:112 offset1:113
M 48,v[92:95],132
ds_read2_b32 v[162:163],v14 offset0:114 offset1:115
M 48,v[96:99],128
M 48,v[100:103],124
R 192,2,3008
M 48,v[104:107],120
L 15
ds_read2_b32 v[164:165],v14 offset0:240 offset1:241
M 52,v[88:91],140
ds_read2_b32 v[166:167],v14 offset0:242 offset1:243
v_add_u32 v14,12928,v14
M 52,v[92:95],136
R 196,2,2496
M 52,v[96:99],132
M 52,v[100:103],128
ds_read2_b32 v[168:169],v15 offset0:112 offset1:113
M 52,v[104:107],124
ds_read2_b32 v[170:171],v15 offset0:114 offset1:115
M 52,v[108:111],120
L 15
R 200,2,1984
M 56,v[88:91],144
ds_read2_b32 v[172:173],v15 offset0:240 offset1:241
M 56,v[92:95],140
M 56,v[96:99],136
ds_read2_b32 v[174:175],v15 offset0:242 offset1:243
v_add_u32 v15,12928,v15
M 56,v[100:103],132
R 204,2,1472
M 56,v[104:107],128
ds_read2_b32 v[176:177],v16 offset0:112 offset1:113
M 56,v[108:111],124
ds_read2_b32 v[178:179],v16 offset0:114 offset1:115
M 56,v[112:115],120
L 15
M 60,v[88:91],148
R 208,2,960
M 60,v[92:95],144
)"
R"(ds_read2_b32 v[180:181],v16 offset0:240 offset1:241
M 60,v[96:99],140
ds_read2_b32 v[182:183],v16 offset0:242 offset1:243
v_add_u32 v16,12928,v16
M 60,v[100:103],136
R 212,2,448
M 60,v[104:107],132
M 60,v[108:111],128
M 60,v[112:115],124
M 60,v[116:119],120
L 15
M 40,v[152:155],192
ds_read2_b32 v[88:89],v1 offset0:0 offset1:1
M 40,v[156:159],188
ds_read2_b32 v[90:91],v1 offset0:2 offset1:3
M 40,v[160:163],184
L 14
R 120,2,16512
M 44,v[152:155],196
ds_read2_b32 v[92:93],v1 offset0:128 offset1:129
M 44,v[156:159],192
M 44,v[160:163],188
ds_read2_b32 v[94:95],v1 offset0:130 offset1:131
M 44,v[164:167],184
L 14
R 124,2,16000
M 48,v[152:155],200
ds_read2_b32 v[96:97],v14 offset0:0 offset1:1
M 48,v[156:159],196
ds_read2_b32 v[98:99],v14 offset0:2 offset1:3
M 48,v[160:163],192
M 48,v[164:167],188
R 128,2,15488
M 48,v[168:171],184
L 15
ds_read2_b32 v[100:101],v14 offset0:128 offset1:129
M 52,v[152:155],204
ds_read2_b32 v[102:103],v14 offset0:130 offset1:131
M 52,v[156:159],200
R 132,2,14976
M 52,v[160:163],196
M 52,v[164:167],192
ds_read2_b32 v[104:105],v15 offset0:0 offset1:1
M 52,v[168:171],188
ds_read2_b32 v[106:107],v15 offset0:2 offset1:3
M 52,v[172:175],184
L 15
R 136,2,14464
M 56,v[152:155],208
ds_read2_b32 v[108:109],v15 offset0:128 offset1:129
M 56,v[156:159],204
M 56,v[160:163],200
ds_read2_b32 v[110:111],v15 offset0:130 offset1:131
M 56,v[164:167],196
R 140,2,13952
M 56,v[168:171],192
ds_read2_b32 v[112:113],v16 offset0:0 offset1:1
M 56,v[172:175],188
ds_read2_b32 v[114:115],v16 offset0:2 offset1:3
M 56,v[176:179],184
L 15
M 60,v[152:155],212
R 144,2,13440
M 60,v[156:159],208
ds_read2_b32 v[116:117],v16 offset0:128 offset1:129
M 60,v[160:163],204
ds_read2_b32 v[118:119],v16 offset0:130 offset1:131
M 60,v[164:167],200
R 148,2,12928
M 60,v[168:171],196
M 60,v[172:175],192
M 60,v[176:179],188
M 60,v[180:183],184
s_cmp_eq_u32 s51,2
s_cbranch_scc1 Lcv_d_%=
ds_read_b64 v[24:25],v3 offset:2560
ds_read_b64 v[26:27],v3 offset:2048
ds_read_b64 v[28:29],v3 offset:1536
ds_read_b64 v[30:31],v3 offset:1024
ds_read_b64 v[32:33],v3 offset:512
ds_read_b64 v[34:35],v3 offset:0
L 0
v_and_b32 v36,0xffff0000,v25
v_lshlrev_b32 v37,16,v25
v_and_b32 v38,0xffff0000,v24
v_lshlrev_b32 v39,16,v24
v_fma_f32 v40,v36,s86,v40
v_fma_f32 v41,v37,s86,v41
v_fma_f32 v42,v38,s86,v42
v_fma_f32 v43,v39,s86,v43
v_cvt_pk_bf16_f32 v40,v40,v41
v_cvt_pk_bf16_f32 v41,v42,v43
ds_write_b64 v4,v[40:41] offset:0
v_and_b32 v36,0xffff0000,v27
v_lshlrev_b32 v37,16,v27
v_and_b32 v38,0xffff0000,v26
v_lshlrev_b32 v39,16,v26
v_fma_f32 v44,v36,s86,v44
v_fma_f32 v45,v37,s86,v45
v_fma_f32 v46,v38,s86,v46
v_fma_f32 v47,v39,s86,v47
v_cvt_pk_bf16_f32 v44,v44,v45
v_cvt_pk_bf16_f32 v45,v46,v47
ds_write_b64 v4,v[44:45] offset:512
v_and_b32 v36,0xffff0000,v29
v_lshlrev_b32 v37,16,v29
v_and_b32 v38,0xffff0000,v28
v_lshlrev_b32 v39,16,v28
v_fma_f32 v48,v36,s86,v48
v_fma_f32 v49,v37,s86,v49
v_fma_f32 v50,v38,s86,v50
v_fma_f32 v51,v39,s86,v51
v_cvt_pk_bf16_f32 v48,v48,v49
v_cvt_pk_bf16_f32 v49,v50,v51
ds_write_b64 v4,v[48:49] offset:1024
v_and_b32 v36,0xffff0000,v31
v_lshlrev_b32 v37,16,v31
v_and_b32 v38,0xffff0000,v30
v_lshlrev_b32 v39,16,v30
v_fma_f32 v52,v36,s86,v52
v_fma_f32 v53,v37,s86,v53
v_fma_f32 v54,v38,s86,v54
v_fma_f32 v55,v39,s86,v55
v_cvt_pk_bf16_f32 v52,v52,v53
v_cvt_pk_bf16_f32 v53,v54,v55
ds_write_b64 v4,v[52:53] offset:1536
v_and_b32 v36,0xffff0000,v33
v_lshlrev_b32 v37,16,v33
v_and_b32 v38,0xffff0000,v32
v_lshlrev_b32 v39,16,v32
v_fma_f32 v56,v36,s86,v56
v_fma_f32 v57,v37,s86,v57
v_fma_f32 v58,v38,s86,v58
v_fma_f32 v59,v39,s86,v59
v_cvt_pk_bf16_f32 v56,v56,v57
v_cvt_pk_bf16_f32 v57,v58,v59
ds_write_b64 v4,v[56:57] offset:2048
v_and_b32 v36,0xffff0000,v35
v_lshlrev_b32 v37,16,v35
v_and_b32 v38,0xffff0000,v34
v_lshlrev_b32 v39,16,v34
v_fma_f32 v60,v36,s86,v60
v_fma_f32 v61,v37,s86,v61
v_fma_f32 v62,v38,s86,v62
v_fma_f32 v63,v39,s86,v63
v_cvt_pk_bf16_f32 v60,v60,v61
v_cvt_pk_bf16_f32 v61,v62,v63
ds_write_b64 v4,v[60:61] offset:2560
s_branch Lcv_e_%=
Lcv_d_%=:
L 15
v_mfma_f32_16x16x32_bf16 v[64:67],v[88:91],v[128:131],0
ds_read2_b32 v[152:153],v1 offset0:16 offset1:17
ds_read_b64 v[24:25],v3 offset:2560
M 64,v[92:95],124
ds_read2_b32 v[154:155],v1 offset0:18 offset1:19
ds_read_b64 v[26:27],v3 offset:2048
M 64,v[96:99],120
L 15
R 184,2,16576
ds_read_b64 v[28:29],v3 offset:1536
v_mfma_f32_16x16x32_bf16 v[68:71],v[88:91],v[132:135],0
ds_read2_b32 v[156:157],v1 offset0:144 offset1:145
ds_read_b64 v[30:31],v3 offset:1024
M 68,v[92:95],128
ds_read_b64 v[32:33],v3 offset:512
ds_read_b64 v[34:35],v3 offset:0
M 68,v[96:99],124
ds_read2_b32 v[158:159],v1 offset0:146 offset1:147
M 68,v[100:103],120
L 15
R 188,2,16064
v_mfma_f32_16x16x32_bf16 v[72:75],v[88:91],v[136:139],0
ds_read2_b32 v[160:161],v14 offset0:16 offset1:17
M 72,v[92:95],132
s_mov_b32 m0,163584
s_mov_b32 s78,0
ds_read2_b32 v[162:163],v14 offset0:18 offset1:19
M 72,v[96:99],128
M 72,v[100:103],124
L 4
R 192,2,15552
v_and_b32 v36,0xffff0000,v25
M 72,v[104:107],120
s_nop 0
ds_read2_b32 v[164:165],v14 offset0:144 offset1:145
v_lshlrev_b32 v37,16,v25
v_mfma_f32_16x16x32_bf16 v[76:79],v[88:91],v[140:143],0
ds_read2_b32 v[166:167],v14 offset0:146 offset1:147
v_and_b32 v38,0xffff0000,v24
M 76,v[92:95],136
R 196,2,15040
v_lshlrev_b32 v39,16,v24
M 76,v[96:99],132
buffer_load_dword v12,s[60:63],s78 offen offset:0  lds
M 76,v[100:103],128
ds_read2_b32 v[168:169],v15 offset0:16 offset1:17
v_fma_f32 v40,v36,s86,v40
M 76,v[104:107],124
ds_read2_b32 v[170:171],v15 offset0:18 offset1:19
v_fma_f32 v41,v37,s86,v41
M 76,v[108:111],120
s_nop 0
R 200,2,14528
v_fma_f32 v42,v38,s86,v42
v_mfma_f32_16x16x32_bf16 v[80:83],v[88:91],v[144:147],0
ds_read2_b32 v[172:173],v15 offset0:144 offset1:145
v_fma_f32 v43,v39,s86,v43
M 80,v[92:95],140
v_cvt_pk_bf16_f32 v40,v40,v41
v_cvt_pk_bf16_f32 v41,v42,v43
M 80,v[96:99],136
ds_read2_b32 v[174:175],v15 offset0:146 offset1:147
v_and_b32 v36,0xffff0000,v27
M 80,v[100:103],132
R 204,2,14016
v_lshlrev_b32 v37,16,v27
M 80,v[104:107],128
ds_read2_b32 v[176:177],v16 offset0:16 offset1:17
v_and_b32 v38,0xffff0000,v26
M 80,v[108:111],124
ds_read2_b32 v[178:179],v16 offset0:18 offset1:19
v_lshlrev_b32 v39,16,v26
M 80,v[112:115],120
s_nop 0
ds_write_b64 v4,v[40:41] offset:0
v_mfma_f32_16x16x32_bf16 v[84:87],v[88:91],v[148:151],0
R 208,2,13504
v_fma_f32 v44,v36,s86,v44
M 84,v[92:95],144
ds_read2_b32 v[180:181],v16 offset0:144 offset1:145
v_fma_f32 v45,v37,s86,v45
M 84,v[96:99],140
ds_read2_b32 v[182:183],v16 offset0:146 offset1:147
v_fma_f32 v46,v38,s86,v46
M 84,v[100:103],136
R 212,2,12992
v_fma_f32 v47,v39,s86,v47
M 84,v[104:107],132
v_cvt_pk_bf16_f32 v44,v44,v45
v_cvt_pk_bf16_f32 v45,v46,v47
M 84,v[108:111],128
ds_write_b64 v4,v[44:45] offset:512
M 84,v[112:115],124
v_and_b32 v36,0xffff0000,v29
v_lshlrev_b32 v37,16,v29
M 84,v[116:119],120
L 15
v_and_b32 v38,0xffff0000,v28
v_lshlrev_b32 v39,16,v28
M 64,v[152:155],192
ds_read2_b32 v[88:89],v1 offset0:32 offset1:33
v_fma_f32 v48,v36,s86,v48
M 64,v[156:159],188
ds_read2_b32 v[90:91],v1 offset0:34 offset1:35
v_fma_f32 v49,v37,s86,v49
M 64,v[160:163],184
L 15
R 120,2,16640
v_fma_f32 v50,v38,s86,v50
M 68,v[152:155],196
ds_read2_b32 v[92:93],v1 offset0:160 offset1:161
v_fma_f32 v51,v39,s86,v51
M 68,v[156:159],192
v_cvt_pk_bf16_f32 v48,v48,v49
v_cvt_pk_bf16_f32 v49,v50,v51
M 68,v[160:163],188
ds_read2_b32 v[94:95],v1 offset0:162 offset1:163
v_and_b32 v36,0xffff0000,v31
M 68,v[164:167],184
L 15
R 124,2,16128
v_lshlrev_b32 v37,16,v31
M 72,v[152:155],200
ds_read2_b32 v[96:97],v14 offset0:32 offset1:33
v_and_b32 v38,0xffff0000,v30
M 72,v[156:159],196
ds_read2_b32 v[98:99],v14 offset0:34 offset1:35
v_lshlrev_b32 v39,16,v30
M 72,v[160:163],192
ds_write_b64 v4,v[48:49] offset:1024
M 72,v[164:167],188
R 128,2,15616
v_fma_f32 v52,v36,s86,v52
M 72,v[168:171],184
L 15
ds_read2_b32 v[100:101],v14 offset0:160 offset1:161
v_fma_f32 v53,v37,s86,v53
M 76,v[152:155],204
ds_read2_b32 v[102:103],v14 offset0:162 offset1:163
v_fma_f32 v54,v38,s86,v54
M 76,v[156:159],200
R 132,2,15104
v_fma_f32 v55,v39,s86,v55
M 76,v[160:163],196
v_cvt_pk_bf16_f32 v52,v52,v53
v_cvt_pk_bf16_f32 v53,v54,v55
M 76,v[164:167],192
ds_read2_b32 v[104:105],v15 offset0:32 offset1:33
v_and_b32 v36,0xffff0000,v33
M 76,v[168:171],188
ds_read2_b32 v[106:107],v15 offset0:34 offset1:35
v_lshlrev_b32 v37,16,v33
M 76,v[172:175],184
L 15
R 136,2,14592
v_and_b32 v38,0xffff0000,v32
M 80,v[152:155],208
ds_read2_b32 v[108:109],v15 offset0:160 offset1:161
v_lshlrev_b32 v39,16,v32
M 80,v[156:159],204
ds_write_b64 v4,v[52:53] offset:1536
M 80,v[160:163],200
ds_read2_b32 v[110:111],v15 offset0:162 offset1:163
v_fma_f32 v56,v36,s86,v56
M 80,v[164:167],196
R 140,2,14080
v_fma_f32 v57,v37,s86,v57
M 80,v[168:171],192
ds_read2_b32 v[112:113],v16 offset0:32 offset1:33
v_fma_f32 v58,v38,s86,v58
M 80,v[172:175],188
ds_read2_b32 v[114:115],v16 offset0:34 offset1:35
v_fma_f32 v59,v39,s86,v59
M 80,v[176:179],184
L 15
v_cvt_pk_bf16_f32 v56,v56,v57
v_cvt_pk_bf16_f32 v57,v58,v59
M 84,v[152:155],212
R 144,2,13568
v_and_b32 v36,0xffff0000,v35
M 84,v[156:159],208
ds_read2_b32 v[116:117],v16 offset0:160 offset1:161
v_lshlrev_b32 v37,16,v35
M 84,v[160:163],204
ds_read2_b32 v[118:119],v16 offset0:162 offset1:163
v_and_b32 v38,0xffff0000,v34
M 84,v[164:167],200
R 148,2,13056
v_lshlrev_b32 v39,16,v34
M 84,v[168:171],196
ds_write_b64 v4,v[56:57] offset:2048
M 84,v[172:175],192
v_fma_f32 v60,v36,s86,v60
v_fma_f32 v61,v37,s86,v61
M 84,v[176:179],188
v_fma_f32 v62,v38,s86,v62
v_fma_f32 v63,v39,s86,v63
M 84,v[180:183],184
L 15
v_cvt_pk_bf16_f32 v60,v60,v61
v_cvt_pk_bf16_f32 v61,v62,v63
M 64,v[88:91],128
ds_read2_b32 v[152:153],v1 offset0:48 offset1:49
M 64,v[92:95],124
ds_read2_b32 v[154:155],v1 offset0:50 offset1:51
M 64,v[96:99],120
L 15
R 184,2,16704
M 68,v[88:91],132
ds_read2_b32 v[156:157],v1 offset0:176 offset1:177
M 68,v[92:95],128
ds_write_b64 v4,v[60:61] offset:2560
M 68,v[96:99],124
ds_read2_b32 v[158:159],v1 offset0:178 offset1:179
M 68,v[100:103],120
L 15
R 188,2,16192
M 72,v[88:91],136
ds_read2_b32 v[160:161],v14 offset0:48 offset1:49
M 72,v[92:95],132
ds_read2_b32 v[162:163],v14 offset0:50 offset1:51
M 72,v[96:99],128
M 72,v[100:103],124
R 192,2,15680
M 72,v[104:107],120
L 15
ds_read2_b32 v[164:165],v14 offset0:176 offset1:177
M 76,v[88:91],140
ds_read2_b32 v[166:167],v14 offset0:178 offset1:179
M 76,v[92:95],136
R 196,2,15168
M 76,v[96:99],132
M 76,v[100:103],128
ds_read2_b32 v[168:169],v15 offset0:48 offset1:49
M 76,v[104:107],124
ds_read2_b32 v[170:171],v15 offset0:50 offset1:51
M 76,v[108:111],120
L 15
R 200,2,14656
M 80,v[88:91],144
ds_read2_b32 v[172:173],v15 offset0:176 offset1:177
M 80,v[92:95],140
M 80,v[96:99],136
ds_read2_b32 v[174:175],v15 offset0:178 offset1:179
M 80,v[100:103],132
R 204,2,14144
M 80,v[104:107],128
s_mov_b32 m0,163584
s_mov_b32 s78,1847296
ds_read2_b32 v[176:177],v16 offset0:48 offset1:49
M 80,v[108:111],124
ds_read2_b32 v[178:179],v16 offset0:50 offset1:51
M 80,v[112:115],120
L 15
buffer_load_dword v12,s[60:63],s78 offen offset:0  lds
M 84,v[88:91],148
R 208,2,13632
M 84,v[92:95],144
ds_read2_b32 v[180:181],v16 offset0:176 offset1:177
M 84,v[96:99],140
ds_read2_b32 v[182:183],v16 offset0:178 offset1:179
M 84,v[100:103],136
R 212,2,13120
M 84,v[104:107],132
M 84,v[108:111],128
M 84,v[112:115],124
M 84,v[116:119],120
L 15
M 64,v[152:155],192
ds_read2_b32 v[88:89],v1 offset0:64 offset1:65
M 64,v[156:159],188
ds_read2_b32 v[90:91],v1 offset0:66 offset1:67
M 64,v[160:163],184
L 14
R 120,2,16768
M 68,v[152:155],196
ds_read2_b32 v[92:93],v1 offset0:192 offset1:193
M 68,v[156:159],192
M 68,v[160:163],188
ds_read2_b32 v[94:95],v1 offset0:194 offset1:195
M 68,v[164:167],184
L 14
R 124,2,16256
M 72,v[152:155],200
ds_read2_b32 v[96:97],v14 offset0:64 offset1:65
M 72,v[156:159],196
ds_read2_b32 v[98:99],v14 offset0:66 offset1:67
M 72,v[160:163],192
M 72,v[164:167],188
R 128,2,15744
M 72,v[168:171],184
L 15
ds_read2_b32 v[100:101],v14 offset0:192 offset1:193
M 76,v[152:155],204
ds_read2_b32 v[102:103],v14 offset0:194 offset1:195
M 76,v[156:159],200
R 132,2,15232
M 76,v[160:163],196
M 76,v[164:167],192
ds_read2_b32 v[104:105],v15 offset0:64 offset1:65
M 76,v[168:171],188
ds_read2_b32 v[106:107],v15 offset0:66 offset1:67
M 76,v[172:175],184
L 15
R 136,2,14720
M 80,v[152:155],208
ds_read2_b32 v[108:109],v15 offset0:192 offset1:193
M 80,v[156:159],204
M 80,v[160:163],200
ds_read2_b32 v[110:111],v15 offset0:194 offset1:195
M 80,v[164:167],196
R 140,2,14208
M 80,v[168:171],192
ds_read2_b32 v[112:113],v16 offset0:64 offset1:65
M 80,v[172:175],188
ds_read2_b32 v[114:115],v16 offset0:66 offset1:67
M 80,v[176:179],184
L 15
M 84,v[152:155],212
R 144,2,13696
M 84,v[156:159],208
ds_read2_b32 v[116:117],v16 offset0:192 offset1:193
M 84,v[160:163],204
ds_read2_b32 v[118:119],v16 offset0:194 offset1:195
M 84,v[164:167],200
R 148,2,13184
M 84,v[168:171],196
M 84,v[172:175],192
M 84,v[176:179],188
M 84,v[180:183],184
L 15
M 64,v[88:91],128
ds_read2_b32 v[152:153],v1 offset0:80 offset1:81
M 64,v[92:95],124
ds_read2_b32 v[154:155],v1 offset0:82 offset1:83
M 64,v[96:99],120
L 14
R 184,2,16832
M 68,v[88:91],132
ds_read2_b32 v[156:157],v1 offset0:208 offset1:209
M 68,v[92:95],128
M 68,v[96:99],124
ds_read2_b32 v[158:159],v1 offset0:210 offset1:211
M 68,v[100:103],120
L 14
R 188,2,16320
M 72,v[88:91],136
ds_read2_b32 v[160:161],v14 offset0:80 offset1:81
M 72,v[92:95],132
)"
R"(ds_read2_b32 v[162:163],v14 offset0:82 offset1:83
M 72,v[96:99],128
M 72,v[100:103],124
R 192,2,15808
M 72,v[104:107],120
L 15
ds_read2_b32 v[164:165],v14 offset0:208 offset1:209
M 76,v[88:91],140
ds_read2_b32 v[166:167],v14 offset0:210 offset1:211
M 76,v[92:95],136
R 196,2,15296
M 76,v[96:99],132
M 76,v[100:103],128
ds_read2_b32 v[168:169],v15 offset0:80 offset1:81
M 76,v[104:107],124
ds_read2_b32 v[170:171],v15 offset0:82 offset1:83
M 76,v[108:111],120
L 15
R 200,2,14784
M 80,v[88:91],144
ds_read2_b32 v[172:173],v15 offset0:208 offset1:209
M 80,v[92:95],140
M 80,v[96:99],136
ds_read2_b32 v[174:175],v15 offset0:210 offset1:211
M 80,v[100:103],132
R 204,2,14272
M 80,v[104:107],128
ds_read2_b32 v[176:177],v16 offset0:80 offset1:81
M 80,v[108:111],124
ds_read2_b32 v[178:179],v16 offset0:82 offset1:83
M 80,v[112:115],120
L 15
M 84,v[88:91],148
R 208,2,13760
M 84,v[92:95],144
ds_read2_b32 v[180:181],v16 offset0:208 offset1:209
M 84,v[96:99],140
ds_read2_b32 v[182:183],v16 offset0:210 offset1:211
M 84,v[100:103],136
R 212,2,13248
M 84,v[104:107],132
M 84,v[108:111],128
M 84,v[112:115],124
M 84,v[116:119],120
L 15
M 64,v[152:155],192
ds_read2_b32 v[88:89],v1 offset0:96 offset1:97
M 64,v[156:159],188
ds_read2_b32 v[90:91],v1 offset0:98 offset1:99
M 64,v[160:163],184
L 14
R 120,2,16896
M 68,v[152:155],196
ds_read2_b32 v[92:93],v1 offset0:224 offset1:225
M 68,v[156:159],192
M 68,v[160:163],188
ds_read2_b32 v[94:95],v1 offset0:226 offset1:227
M 68,v[164:167],184
L 14
R 124,2,16384
M 72,v[152:155],200
ds_read2_b32 v[96:97],v14 offset0:96 offset1:97
M 72,v[156:159],196
ds_read2_b32 v[98:99],v14 offset0:98 offset1:99
M 72,v[160:163],192
M 72,v[164:167],188
R 128,2,15872
M 72,v[168:171],184
L 15
ds_read2_b32 v[100:101],v14 offset0:224 offset1:225
M 76,v[152:155],204
ds_read2_b32 v[102:103],v14 offset0:226 offset1:227
M 76,v[156:159],200
R 132,2,15360
M 76,v[160:163],196
M 76,v[164:167],192
ds_read2_b32 v[104:105],v15 offset0:96 offset1:97
M 76,v[168:171],188
ds_read2_b32 v[106:107],v15 offset0:98 offset1:99
M 76,v[172:175],184
L 15
R 136,2,14848
M 80,v[152:155],208
ds_read2_b32 v[108:109],v15 offset0:224 offset1:225
M 80,v[156:159],204
M 80,v[160:163],200
ds_read2_b32 v[110:111],v15 offset0:226 offset1:227
M 80,v[164:167],196
R 140,2,14336
M 80,v[168:171],192
ds_read2_b32 v[112:113],v16 offset0:96 offset1:97
M 80,v[172:175],188
ds_read2_b32 v[114:115],v16 offset0:98 offset1:99
M 80,v[176:179],184
L 15
M 84,v[152:155],212
R 144,2,13824
M 84,v[156:159],208
ds_read2_b32 v[116:117],v16 offset0:224 offset1:225
M 84,v[160:163],204
ds_read2_b32 v[118:119],v16 offset0:226 offset1:227
M 84,v[164:167],200
R 148,2,13312
M 84,v[168:171],196
M 84,v[172:175],192
M 84,v[176:179],188
M 84,v[180:183],184
L 15
M 64,v[88:91],128
ds_read2_b32 v[152:153],v1 offset0:112 offset1:113
M 64,v[92:95],124
ds_read2_b32 v[154:155],v1 offset0:114 offset1:115
M 64,v[96:99],120
L 14
R 184,2,16960
M 68,v[88:91],132
ds_read2_b32 v[156:157],v1 offset0:240 offset1:241
M 68,v[92:95],128
M 68,v[96:99],124
ds_read2_b32 v[158:159],v1 offset0:242 offset1:243
M 68,v[100:103],120
L 14
R 188,2,16448
M 72,v[88:91],136
ds_read2_b32 v[160:161],v14 offset0:112 offset1:113
M 72,v[92:95],132
ds_read2_b32 v[162:163],v14 offset0:114 offset1:115
M 72,v[96:99],128
M 72,v[100:103],124
R 192,2,15936
M 72,v[104:107],120
L 15
ds_read2_b32 v[164:165],v14 offset0:240 offset1:241
M 76,v[88:91],140
ds_read2_b32 v[166:167],v14 offset0:242 offset1:243
M 76,v[92:95],136
R 196,2,15424
M 76,v[96:99],132
M 76,v[100:103],128
ds_read2_b32 v[168:169],v15 offset0:112 offset1:113
M 76,v[104:107],124
ds_read2_b32 v[170:171],v15 offset0:114 offset1:115
M 76,v[108:111],120
L 15
R 200,2,14912
M 80,v[88:91],144
ds_read2_b32 v[172:173],v15 offset0:240 offset1:241
M 80,v[92:95],140
M 80,v[96:99],136
ds_read2_b32 v[174:175],v15 offset0:242 offset1:243
M 80,v[100:103],132
R 204,2,14400
M 80,v[104:107],128
ds_read2_b32 v[176:177],v16 offset0:112 offset1:113
M 80,v[108:111],124
ds_read2_b32 v[178:179],v16 offset0:114 offset1:115
M 80,v[112:115],120
L 15
M 84,v[88:91],148
R 208,2,13888
M 84,v[92:95],144
ds_read2_b32 v[180:181],v16 offset0:240 offset1:241
M 84,v[96:99],140
ds_read2_b32 v[182:183],v16 offset0:242 offset1:243
M 84,v[100:103],136
R 212,2,13376
M 84,v[104:107],132
M 84,v[108:111],128
M 84,v[112:115],124
M 84,v[116:119],120
L 15
M 64,v[152:155],192
M 64,v[156:159],188
M 64,v[160:163],184
L 12
M 68,v[152:155],196
M 68,v[156:159],192
M 68,v[160:163],188
M 68,v[164:167],184
L 9
M 72,v[152:155],200
M 72,v[156:159],196
M 72,v[160:163],192
ds_read_b64 v[24:25],v3 offset:15488
ds_read_b64 v[26:27],v3 offset:14976
M 72,v[164:167],188
ds_read_b64 v[28:29],v3 offset:14464
ds_read_b64 v[30:31],v3 offset:13952
M 72,v[168:171],184
L 10
ds_read_b64 v[32:33],v3 offset:13440
ds_read_b64 v[34:35],v3 offset:12928
M 76,v[152:155],204
M 76,v[156:159],200
M 76,v[160:163],196
M 76,v[164:167],192
M 76,v[168:171],188
M 76,v[172:175],184
L 9
M 80,v[152:155],208
M 80,v[156:159],204
M 80,v[160:163],200
M 80,v[164:167],196
M 80,v[168:171],192
M 80,v[172:175],188
M 80,v[176:179],184
L 6
M 84,v[152:155],212
M 84,v[156:159],208
M 84,v[160:163],204
M 84,v[164:167],200
M 84,v[168:171],196
M 84,v[172:175],192
M 84,v[176:179],188
M 84,v[180:183],184
L 0
v_and_b32 v36,0xffff0000,v25
v_lshlrev_b32 v37,16,v25
v_and_b32 v38,0xffff0000,v24
v_lshlrev_b32 v39,16,v24
v_fma_f32 v64,v36,s87,v64
v_fma_f32 v65,v37,s87,v65
v_fma_f32 v66,v38,s87,v66
v_fma_f32 v67,v39,s87,v67
v_cvt_pk_bf16_f32 v64,v64,v65
v_cvt_pk_bf16_f32 v65,v66,v67
ds_write_b64 v4,v[64:65] offset:12928
v_and_b32 v36,0xffff0000,v27
v_lshlrev_b32 v37,16,v27
v_and_b32 v38,0xffff0000,v26
v_lshlrev_b32 v39,16,v26
v_fma_f32 v68,v36,s87,v68
v_fma_f32 v69,v37,s87,v69
v_fma_f32 v70,v38,s87,v70
v_fma_f32 v71,v39,s87,v71
v_cvt_pk_bf16_f32 v68,v68,v69
v_cvt_pk_bf16_f32 v69,v70,v71
ds_write_b64 v4,v[68:69] offset:13440
v_and_b32 v36,0xffff0000,v29
v_lshlrev_b32 v37,16,v29
v_and_b32 v38,0xffff0000,v28
v_lshlrev_b32 v39,16,v28
v_fma_f32 v72,v36,s87,v72
v_fma_f32 v73,v37,s87,v73
v_fma_f32 v74,v38,s87,v74
v_fma_f32 v75,v39,s87,v75
v_cvt_pk_bf16_f32 v72,v72,v73
v_cvt_pk_bf16_f32 v73,v74,v75
ds_write_b64 v4,v[72:73] offset:13952
v_and_b32 v36,0xffff0000,v31
v_lshlrev_b32 v37,16,v31
v_and_b32 v38,0xffff0000,v30
v_lshlrev_b32 v39,16,v30
v_fma_f32 v76,v36,s87,v76
v_fma_f32 v77,v37,s87,v77
v_fma_f32 v78,v38,s87,v78
v_fma_f32 v79,v39,s87,v79
v_cvt_pk_bf16_f32 v76,v76,v77
v_cvt_pk_bf16_f32 v77,v78,v79
ds_write_b64 v4,v[76:77] offset:14464
v_and_b32 v36,0xffff0000,v33
v_lshlrev_b32 v37,16,v33
v_and_b32 v38,0xffff0000,v32
v_lshlrev_b32 v39,16,v32
v_fma_f32 v80,v36,s87,v80
v_fma_f32 v81,v37,s87,v81
v_fma_f32 v82,v38,s87,v82
v_fma_f32 v83,v39,s87,v83
v_cvt_pk_bf16_f32 v80,v80,v81
v_cvt_pk_bf16_f32 v81,v82,v83
ds_write_b64 v4,v[80:81] offset:14976
v_and_b32 v36,0xffff0000,v35
v_lshlrev_b32 v37,16,v35
v_and_b32 v38,0xffff0000,v34
v_lshlrev_b32 v39,16,v34
v_fma_f32 v84,v36,s87,v84
v_fma_f32 v85,v37,s87,v85
v_fma_f32 v86,v38,s87,v86
v_fma_f32 v87,v39,s87,v87
v_cvt_pk_bf16_f32 v84,v84,v85
v_cvt_pk_bf16_f32 v85,v86,v87
ds_write_b64 v4,v[84:85] offset:15488
Lcv_e_%=:
L 0
s_barrier
R 152,5,0
R 156,5,512
R 160,5,1024
R 164,5,1536
R 168,5,2048
R 172,5,2560
s_mov_b32 s78,99614720
L 5
buffer_store_dwordx4 v[152:155],v7,s[36:39],s78 offen offset:0 sc0 sc1
s_add_u32 s78,s56,100139008
L 4
buffer_store_dwordx4 v[156:159],v7,s[36:39],s78 offen offset:0 sc0 sc1
s_mov_b32 s78,100663296
L 3
buffer_store_dwordx4 v[160:163],v7,s[36:39],s78 offen offset:0 sc0 sc1
s_add_u32 s78,s56,101187584
L 2
buffer_store_dwordx4 v[164:167],v7,s[36:39],s78 offen offset:0 sc0 sc1
s_mov_b32 s78,101711872
L 1
buffer_store_dwordx4 v[168:171],v7,s[36:39],s78 offen offset:0 sc0 sc1
s_add_u32 s78,s56,102236160
L 0
buffer_store_dwordx4 v[172:175],v7,s[36:39],s78 offen offset:0 sc0 sc1
s_waitcnt vmcnt(0)
s_barrier
s_add_u32 s48,s48,32768
buffer_store_dword v23,v10,s[36:39],s48 offen offset:0 sc0 sc1
s_cmp_eq_u32 s51,2
s_cbranch_scc0 Lcv_f_%=
s_mov_b32 m0,163584
s_mov_b32 s78,3694592
buffer_load_dword v12,s[60:63],s78 offen offset:0  lds
s_mov_b32 m0,163584
s_mov_b32 s78,5541888
buffer_load_dword v12,s[60:63],s78 offen offset:0  lds
Lcv_f_%=:
Lcode_cv_e:
.purgem M
.purgem R
.purgem T
.purgem D
.purgem L
)"
  :: "s"(ws), "s"(__builtin_amdgcn_readfirstlane(sWave)), "s"(__builtin_amdgcn_readfirstlane(sBuf)), "s"(__builtin_amdgcn_readfirstlane(sAuxOff)), "s"(__builtin_amdgcn_readfirstlane(sFoutOff)), "s"(__builtin_amdgcn_readfirstlane(sC0)), "s"(__builtin_amdgcn_readfirstlane(sC1)), "s"(__builtin_amdgcn_readfirstlane(sExp)), "v"(vL16), "v"(vPA), "v"(vPB), "v"(vPU), "v"(vPW), "v"(vOR), "v"(vORA), "v"(vOG), "v"(vFA), "v"(vFB), "v"(vFlag), "v"(vTW), "v"(vTX), "s"(w3), "s"(w4) : "v0", "v1", "v2", "v3", "v4", "v5", "v6", "v7", "v8", "v9", "v10", "v11", "v12", "v13", "v14", "v15", "v16", "v17", "v18", "v19", "v20", "v21", "v22", "v23", "v24", "v25", "v26", "v27", "v28", "v29", "v30", "v31", "v32", "v33", "v34", "v35", "v36", "v37", "v38", "v39", "v40", "v41", "v42", "v43", "v44", "v45", "v46", "v47", "v48", "v49", "v50", "v51", "v52", "v53", "v54", "v55", "v56", "v57", "v58", "v59", "v60", "v61", "v62", "v63", "v64", "v65", "v66", "v67", "v68", "v69", "v70", "v71", "v72", "v73", "v74", "v75", "v76", "v77", "v78", "v79", "v80", "v81", "v82", "v83", "v84", "v85", "v86", "v87", "v88", "v89", "v90", "v91", "v92", "v93", "v94", "v95", "v96", "v97", "v98", "v99", "v100", "v101", "v102", "v103", "v104", "v105", "v106", "v107", "v108", "v109", "v110", "v111", "v112", "v113", "v114", "v115", "v116", "v117", "v118", "v119", "v120", "v121", "v122", "v123", "v124", "v125", "v126", "v127", "v128", "v129", "v130", "v131", "v132", "v133", "v134", "v135", "v136", "v137", "v138", "v139", "v140", "v141", "v142", "v143", "v144", "v145", "v146", "v147", "v148", "v149", "v150", "v151", "v152", "v153", "v154", "v155", "v156", "v157", "v158", "v159", "v160", "v161", "v162", "v163", "v164", "v165", "v166", "v167", "v168", "v169", "v170", "v171", "v172", "v173", "v174", "v175", "v176", "v177", "v178", "v179", "v180", "v181", "v182", "v183", "v184", "v185", "v186", "v187", "v188", "v189", "v190", "v191", "v192", "v193", "v194", "v195", "v196", "v197", "v198", "v199", "v200", "v201", "v202", "v203", "v204", "v205", "v206", "v207", "v208", "v209", "v210", "v211", "v212", "v213", "v214", "v215", "v216", "v217", "v218", "v219", "v220", "v221", "v222", "v223", "v224", "v225", "v226", "v227", "v228", "v229", "v230", "v231", "a0", "a1", "a2", "a3", "a4", "a5", "a6", "a7", "a8", "a9", "a10", "a11", "a12", "a13", "a14", "a15", "a16", "a17", "a18", "a19", "a20", "a21", "a22", "a23", "a24", "a25", "a26", "a27", "a28", "a29", "a30", "a31", "a32", "a33", "a34", "a35", "a36", "a37", "a38", "a39", "a40", "a41", "a42", "a43", "a44", "a45", "a46", "a47", "a48", "a49", "a50", "a51", "a52", "a53", "a54", "a55", "a56", "a57", "a58", "a59", "a60", "a61", "a62", "a63", "a64", "a65", "a66", "a67", "a68", "a69", "a70", "a71", "a72", "a73", "a74", "a75", "a76", "a77", "a78", "a79", "a80", "a81", "a82", "a83", "a84", "a85", "a86", "a87", "a88", "a89", "a90", "a91", "a92", "a93", "a94", "a95", "a96", "a97", "a98", "a99", "a100", "a101", "a102", "a103", "a104", "a105", "a106", "a107", "a108", "a109", "a110", "a111", "a112", "a113", "a114", "a115", "a116", "a117", "a118", "a119", "a120", "a121", "a122", "a123", "a124", "a125", "a126", "a127", "a128", "a129", "a130", "a131", "a132", "a133", "a134", "a135", "a136", "a137", "a138", "a139", "a140", "a141", "a142", "a143", "a144", "a145", "a146", "a147", "a148", "a149", "a150", "a151", "a152", "a153", "a154", "a155", "a156", "a157", "a158", "a159", "a160", "a161", "a162", "a163", "a164", "a165", "a166", "a167", "a168", "a169", "a170", "a171", "a172", "a173", "a174", "a175", "a176", "a177", "a178", "a179", "a180", "a181", "a182", "a183", "a184", "a185", "a186", "a187", "a188", "a189", "a190", "a191", "a192", "a193", "a194", "a195", "a196", "a197", "a198", "a199", "a200", "a201", "a202", "a203", "a204", "a205", "a206", "a207", "a208", "a209", "a210", "a211", "a212", "a213", "a214", "a215", "a216", "a217", "a218", "a219", "a220", "a221", "a222", "a223", "a224", "a225", "a226", "a227", "a228", "a229", "a230", "a231", "a232", "a233", "a234", "a235", "a236", "a237", "a238", "a239", "a240", "a241", "a242", "a243", "a244", "a245", "a246", "a247", "a248", "a249", "a250", "a251", "a252", "a253", "a254", "a255", "s36", "s37", "s38", "s39", "s40", "s41", "s42", "s43", "s44", "s45", "s46", "s47", "s48", "s49", "s50", "s51", "s52", "s53", "s54", "s55", "s56", "s57", "s58", "s59", "s60", "s61", "s62", "s63", "s64", "s65", "s66", "s67", "s68", "s69", "s70", "s71", "s72", "s73", "s74", "s75", "s76", "s77", "s78", "s79", "s80", "s81", "s82", "s83", "s84", "s85", "s86", "s87", "s88", "s89", "s90", "s91", "s92", "s93", "s94", "s95", "s96", "s97", "vcc", "scc", "memory");
    LANE_ID(lq_);
    const int g = QT0 + (NCV - 1 - iw) * 164 + min(wave * 64 + lq_, 163);
    if (QTB) touch_lds((const char*)q + (size_t)(g < 32768 ? g : 0) * 128, sScr);
  }
  WAIT_LGKM0();
  __builtin_amdgcn_s_barrier();
  CTS(10);
  }
  const unsigned sDbg0 = 110u * 1048576u + (b * 4 + wave) * 8192;
  constexpr unsigned OOB = 0x80000000u;
  if (kind == 6) {
    LANE_ID(lr);
    const int r8 = lr & 7, seg = lr >> 3, j = lr & 15, g = lr >> 4, rr = 8 * g + (j >> 2);
    const unsigned sWave = wave, sExp = __builtin_amdgcn_readfirstlane(ep), sG0 = slice < 16 ? slice + 1 : 0, sStep = NRW, sDbg = sDbg0;
    const unsigned sWw = ((slice >> 3) * 1792 + 16 * wave) * 2048 + (slice & 7) * 256;
    const unsigned vWd0 = seg * 2048 + (r8 ^ swzB(seg)) * 16, vWd1 = (seg + 8) * 2048 + (r8 ^ swzB(8 + seg)) * 16;
    const unsigned vWr0 = (wave >> 1) * 8192 + rr * 128 + (((wave & 1) * 4 + (lr & 3)) ^ swzB(rr)) * 16, vWr1 = vWr0 + 8;
    const unsigned vRF = (wave * 8 + (seg >> 2)) * 1024 + ((seg & 3) * 16 + r8) * 16;
    const unsigned vRwr = seg == 0 ? r8 * 64 + wave * 4 : 512 + lr * 4, vRrd = r8 * 64;
    const unsigned vRP = g == (j >> 2) ? r8 * 64 + 16 + wave * 8 + (seg & 1) * 4 : 768 + lr * 4;
    const unsigned vRS = (wave == 0 && seg == 0) ? r8 * 16 : OOB, vFlag = lr < 32 ? lr * 4 : OOB;
    const unsigned vL4 = ((lr & 2) * 4 + wave * 2 + (lr & 1)) * 128, vLY = min((wave < 3 ? wave * 32 : 112) + lr, wave < 2 ? wave * 32 + 31 : wave == 2 ? 111 : NCV - 1) * 128;
    const unsigned vQ = r8 * 2048 + wave * 512 + seg * 16;
    const unsigned vY0 = wave * 4096 + lr * 16;
    const unsigned vTr = 4096 + wave * 4096 + ((2 * g + ((j & 3) >> 1)) * 8 + (j >> 2)) * 16 + (j & 1) * 8;
    const unsigned one = g == (j >> 2) ? 0x3f80u << (16 * (j & 1)) : 0u, vSa = (j & 2) ? 0u : one, vSb = (j & 2) ? one : 0u;
    (void)sDbg;
    asm volatile(
R"(.macro M d,w,x
v_mfma_f32_16x16x32_bf16 v[\d:\d+3], \w, v[\x:\x+3], v[\d:\d+3]
.endm
.macro R d,a,o
ds_read_b128 v[\d:\d+3], v\a offset:\o
.endm
.macro T d,a,o
ds_read_b64_tr_b16 \d, v\a offset:\o
.endm
.macro D a,s,o
buffer_load_dwordx4 v\a, s[36:39], s\s offen offset:\o sc1 lds
.endm
.macro L c
s_waitcnt lgkmcnt(\c)
.endm
Lcode_r_b:
s_mov_b64 s[36:37],%0
s_mov_b32 s38,111243392
s_mov_b32 s39,0x00020000
s_mov_b32 s44,%1
s_mov_b32 s45,%2
s_mov_b32 s46,%3
s_mov_b32 s50,%4
s_mov_b32 s53,%5
v_mov_b32 v0,%6
v_mov_b32 v1,%7
v_mov_b32 v2,%8
v_mov_b32 v3,%9
v_mov_b32 v4,%10
v_mov_b32 v5,%11
v_mov_b32 v6,%12
v_mov_b32 v7,%13
v_mov_b32 v8,%14
v_mov_b32 v9,%15
v_mov_b32 v10,%16
v_mov_b32 v11,%17
v_mov_b32 v12,%18
v_mov_b32 v13,%19
v_mov_b32 v14,%20
v_mov_b32 v15,%21
v_mov_b32 v16,%22
s_mov_b32 s47,256
s_mov_b32 s49,0
s_mov_b32 s93,s45
s_mov_b64 s[84:85],%23
s_mov_b32 s86,4194304
s_mov_b32 s87,0x00020000
s_lshl_b32 s55,s45,14
buffer_load_dwordx4 v[164:167],v8,s[84:87],s55 offen offset:0 nt
buffer_load_dwordx4 v[168:171],v8,s[84:87],s55 offen offset:128 nt
buffer_load_dwordx4 v[172:175],v8,s[84:87],s55 offen offset:256 nt
buffer_load_dwordx4 v[176:179],v8,s[84:87],s55 offen offset:384 nt
v_mov_b32 v17,s53
v_mbcnt_lo_u32_b32 v19,-1,0
v_mbcnt_hi_u32_b32 v19,-1,v19
v_lshlrev_b32 v19,4,v19
s_mov_b32 s82,0
s_mov_b32 s63,0x7fff0000
s_mov_b32 s64,0
s_mov_b32 s67,0
s_lshl_b32 s65,s44,12
s_add_u32 s65,s65,4096
s_lshl_b32 s66,s44,7
s_add_u32 s66,s66,109056000
s_mov_b32 s70,0xaaaaaaaa
s_mov_b32 s71,0xaaaaaaaa
s_mov_b32 s72,0xcccccccc
s_mov_b32 s73,0xcccccccc
s_mov_b32 s74,0xff00ff00
s_mov_b32 s75,0xff00ff00
s_mov_b32 s68,0x3a800000
s_mov_b32 s69,0x3727c5ac
s_mov_b32 s76,0xbeeff558
v_mov_b32 v32,0x3f803f80
v_mov_b32 v33,0x3f803f80
v_mov_b32 v34,0x3f803f80
v_mov_b32 v35,0x3f803f80
v_mov_b32 v24,0
v_mov_b32 v25,0
v_mov_b32 v26,v11
v_mov_b32 v27,v12
v_mov_b32 v28,0
v_mov_b32 v29,0
s_getpc_b64 s[94:95]
Lr_pc_%=:
s_add_u32 s94,s94,Lr_tab_%=-Lr_pc_%=
s_addc_u32 s95,s95,0
s_mov_b32 s81,0x7fff0000
s_lshl_b32 s54,s44,11
s_add_u32 s80,s45,85
s_cmp_eq_u32 s45,0
s_cselect_b32 s80,-1,s80
s_waitcnt vmcnt(0)
Lr_a0_%=:
s_cmp_lg_u32 s67,0
s_cbranch_scc1 Lr_b0_%=
s_waitcnt vmcnt(14)
v_mfma_f32_16x16x32_bf16 v[36:39],v[32:35],v[164:167],0
s_add_u32 s79,s45,s46
s_cmp_lt_u32 s45,64
s_cselect_b32 s78,s49,0
v_mfma_f32_16x16x32_bf16 v[40:43],v[164:167],v[164:167],0
s_cmp_ge_u32 s79,64
s_cselect_b32 s78,s78,0
s_add_u32 s62,s79,s78
M 36,v[32:35],168
s_lshr_b32 s78,s45,1
s_and_b32 s79,s45,1
s_lshl_b32 s79,s79,7
M 40,v[168:171],168
s_lshl_b32 s57,s78,15
s_lshl_b32 s55,s62,14
s_cmp_lt_u32 s62,s47
M 36,v[32:35],172
s_cselect_b32 s55,s55,0x7fff0000
s_add_u32 s57,s57,s79
s_lshl_b32 s59,s78,9
M 40,v[172:175],172
buffer_load_dwordx4 v[196:199],v8,s[84:87],s55 offen offset:0 nt
M 36,v[32:35],176
buffer_load_dwordx4 v[200:203],v8,s[84:87],s55 offen offset:128 nt
M 40,v[176:179],176
buffer_load_dwordx4 v[204:207],v8,s[84:87],s55 offen offset:256 nt
v_mfma_f32_16x16x32_bf16 v[116:119],v[26:29],v[164:167],0
buffer_load_dwordx4 v[208:211],v8,s[84:87],s55 offen offset:384 nt
v_mfma_f32_16x16x32_bf16 v[120:123],v[24:27],v[164:167],0
s_add_u32 s57,s57,8388608
s_add_u32 s59,s59,s79
s_lshl_b32 s60,s45,9
v_mfma_f32_16x16x32_bf16 v[124:127],v[26:29],v[168:171],0
s_add_u32 s58,s57,4096
s_add_u32 s59,s59,106954752
s_add_u32 s60,s60,s66
v_mfma_f32_16x16x32_bf16 v[128:131],v[24:27],v[168:171],0
v_add_f32_dpp v44,v36,v36 row_ror:8 row_mask:0xf bank_mask:0xf
v_cndmask_b32 v45,v40,v41,s[70:71]
v_mfma_f32_16x16x32_bf16 v[132:135],v[26:29],v[172:175],0
ds_write_b32 v2,v44 offset:0
v_mfma_f32_16x16x32_bf16 v[136:139],v[24:27],v[172:175],0
v_cndmask_b32 v46,v42,v43,s[70:71]
v_cndmask_b32 v45,v45,v46,s[72:73]
v_mfma_f32_16x16x32_bf16 v[140:143],v[26:29],v[176:179],0
ds_write_b32 v0,v45 offset:0
v_mfma_f32_16x16x32_bf16 v[144:147],v[24:27],v[176:179],0
L 0
s_barrier
R 52,3,0
R 56,3,16
R 60,3,32
s_waitcnt vmcnt(8)
buffer_store_dword v17,v5,s[36:39],s63 offen offset:0 sc0 sc1
s_mov_b32 s63,0x7fff0000
s_cmp_lt_u32 s45,64
s_cselect_b32 s64,-192,0
s_add_u32 s64,s64,256
s_cmp_lt_u32 s45,64
s_cselect_b32 s61,0,32768
s_add_u32 s61,s61,111177728
s_mul_i32 s56,s45,16384
s_add_u32 s56,s56,98566144
Lr_bpy0_%=:
buffer_load_dword v18,v7,s[36:39],s61 offen offset:0 sc1
s_add_u32 s82,s82,1
s_waitcnt vmcnt(0)
v_cmp_eq_u32 vcc,s53,v18
s_nop 0
s_cmp_eq_u64 vcc,-1
s_cbranch_scc1 Lr_bqy0_%=
s_sleep 2
s_cmp_lt_u32 s82,20000
s_cbranch_scc1 Lr_bpy0_%=
Lr_bqy0_%=:
s_add_u32 m0,s65,0
s_nop 0
D 9,56,0
D 9,56,1024
D 9,56,2048
D 9,56,3072
s_waitcnt vmcnt(0)
T v[68:69],10,0
T v[70:71],10,64
T v[72:73],10,1024
T v[74:75],10,1088
T v[76:77],10,2048
T v[78:79],10,2112
T v[80:81],10,3072
T v[82:83],10,3136
s_mov_b32 s45,s62
s_min_u32 s78,s64,s47
s_mul_i32 s56,s45,16384
s_add_u32 s56,s56,98566144
s_cmp_lt_u32 s45,s78
s_cselect_b32 s67,1,0
s_cselect_b32 s56,s56,0x7fff0000
s_add_u32 m0,s65,16384
s_nop 0
D 9,56,0
D 9,56,1024
D 9,56,2048
D 9,56,3072
L 4
v_lshlrev_b32 v84,16,v68
v_and_b32 v85,0xffff0000,v68
v_lshlrev_b32 v86,16,v69
v_and_b32 v87,0xffff0000,v69
v_lshlrev_b32 v88,16,v70
v_and_b32 v89,0xffff0000,v70
v_lshlrev_b32 v90,16,v71
v_and_b32 v91,0xffff0000,v71
v_lshlrev_b32 v92,16,v72
v_and_b32 v93,0xffff0000,v72
v_lshlrev_b32 v94,16,v73
v_and_b32 v95,0xffff0000,v73
v_lshlrev_b32 v96,16,v74
v_and_b32 v97,0xffff0000,v74
v_lshlrev_b32 v98,16,v75
v_and_b32 v99,0xffff0000,v75
L 0
v_lshlrev_b32 v100,16,v76
v_and_b32 v101,0xffff0000,v76
v_lshlrev_b32 v102,16,v77
v_and_b32 v103,0xffff0000,v77
v_lshlrev_b32 v104,16,v78
v_and_b32 v105,0xffff0000,v78
v_lshlrev_b32 v106,16,v79
v_and_b32 v107,0xffff0000,v79
v_lshlrev_b32 v108,16,v80
v_and_b32 v109,0xffff0000,v80
v_lshlrev_b32 v110,16,v81
v_and_b32 v111,0xffff0000,v81
v_lshlrev_b32 v112,16,v82
v_and_b32 v113,0xffff0000,v82
v_lshlrev_b32 v114,16,v83
v_and_b32 v115,0xffff0000,v83
s_branch Lr_c0_%=
Lr_b0_%=:
s_waitcnt vmcnt(10)
T v[68:69],10,0
T v[70:71],10,64
T v[72:73],10,1024
T v[74:75],10,1088
T v[76:77],10,2048
T v[78:79],10,2112
T v[80:81],10,3072
T v[82:83],10,3136
v_mfma_f32_16x16x32_bf16 v[36:39],v[32:35],v[164:167],0
s_add_u32 s79,s45,s46
s_cmp_lt_u32 s45,64
s_cselect_b32 s78,s49,0
v_mfma_f32_16x16x32_bf16 v[40:43],v[164:167],v[164:167],0
s_cmp_ge_u32 s79,64
s_cselect_b32 s78,s78,0
s_add_u32 s62,s79,s78
M 36,v[32:35],168
s_lshr_b32 s78,s45,1
s_and_b32 s79,s45,1
s_lshl_b32 s60,s45,9
M 40,v[168:171],168
s_lshl_b32 s57,s78,15
s_lshl_b32 s59,s78,9
s_mov_b32 s45,s62
M 36,v[32:35],172
s_lshl_b32 s79,s79,7
s_min_u32 s78,s64,s47
s_mul_i32 s56,s45,16384
M 40,v[172:175],172
s_lshl_b32 s55,s62,14
s_cmp_lt_u32 s62,s47
s_cselect_b32 s55,s55,0x7fff0000
M 36,v[32:35],176
buffer_load_dwordx4 v[196:199],v8,s[84:87],s55 offen offset:0 nt
M 40,v[176:179],176
buffer_load_dwordx4 v[200:203],v8,s[84:87],s55 offen offset:128 nt
v_mfma_f32_16x16x32_bf16 v[116:119],v[26:29],v[164:167],0
buffer_load_dwordx4 v[204:207],v8,s[84:87],s55 offen offset:256 nt
v_mfma_f32_16x16x32_bf16 v[120:123],v[24:27],v[164:167],0
buffer_load_dwordx4 v[208:211],v8,s[84:87],s55 offen offset:384 nt
v_mfma_f32_16x16x32_bf16 v[124:127],v[26:29],v[168:171],0
s_add_u32 s57,s57,s79
s_add_u32 s56,s56,98566144
s_cmp_lt_u32 s45,s78
v_mfma_f32_16x16x32_bf16 v[128:131],v[24:27],v[168:171],0
s_cselect_b32 s56,s56,0x7fff0000
v_add_f32_dpp v44,v36,v36 row_ror:8 row_mask:0xf bank_mask:0xf
v_cndmask_b32 v45,v40,v41,s[70:71]
v_mfma_f32_16x16x32_bf16 v[132:135],v[26:29],v[172:175],0
s_cselect_b32 s67,1,0
ds_write_b32 v2,v44 offset:0
v_mfma_f32_16x16x32_bf16 v[136:139],v[24:27],v[172:175],0
s_add_u32 s57,s57,8388608
v_cndmask_b32 v46,v42,v43,s[70:71]
v_cndmask_b32 v45,v45,v46,s[72:73]
v_mfma_f32_16x16x32_bf16 v[140:143],v[26:29],v[176:179],0
s_add_u32 s59,s59,s79
ds_write_b32 v0,v45 offset:0
v_mfma_f32_16x16x32_bf16 v[144:147],v[24:27],v[176:179],0
L 2
s_add_u32 m0,s65,16384
s_add_u32 s58,s57,4096
v_mfma_f32_16x16x32_bf16 v[84:87],v[26:29],v[68:71],0
D 9,56,0
v_mfma_f32_16x16x32_bf16 v[88:91],v[24:27],v[68:71],0
D 9,56,1024
v_mfma_f32_16x16x32_bf16 v[92:95],v[26:29],v[72:75],0
L 0
D 9,56,2048
v_mfma_f32_16x16x32_bf16 v[96:99],v[24:27],v[72:75],0
s_barrier
R 52,3,0
R 56,3,16
v_mfma_f32_16x16x32_bf16 v[100:103],v[26:29],v[76:79],0
s_add_u32 s59,s59,106954752
s_add_u32 s60,s60,s66
R 60,3,32
v_mfma_f32_16x16x32_bf16 v[104:107],v[24:27],v[76:79],0
D 9,56,3072
v_mfma_f32_16x16x32_bf16 v[108:111],v[26:29],v[80:83],0
v_mfma_f32_16x16x32_bf16 v[112:115],v[24:27],v[80:83],0
Lr_c0_%=:
L 0
v_pk_add_f32 v[52:53],v[52:53],v[54:55]
v_pk_add_f32 v[56:57],v[56:57],v[58:59]
v_pk_add_f32 v[60:61],v[60:61],v[62:63]
v_add_f32 v52,v52,v53
v_pk_add_f32 v[56:57],v[56:57],v[60:61]
v_mul_f32 v52,s68,v52
v_add_f32 v53,v56,v57
v_mul_f32 v53,s68,v53
v_fma_f32 v53,-v52,v52,v53
v_max_f32 v53,0,v53
s_waitcnt vmcnt(12)
buffer_store_dword v17,v5,s[36:39],s63 offen offset:0 sc0 sc1
s_mov_b32 s63,0x7fff0000
v_add_f32 v53,s69,v53
v_rsq_f32 v48,v53
s_nop 0
v_mul_f32_e64 v50,-v52,v48
v_mov_b32 v49,v48
v_mov_b32 v51,v50
v_pk_fma_f32 v[52:53],v[116:117],v[48:49],v[50:51]
v_pk_fma_f32 v[54:55],v[118:119],v[48:49],v[50:51]
v_pk_fma_f32 v[56:57],v[120:121],v[48:49],v[50:51]
v_pk_fma_f32 v[58:59],v[122:123],v[48:49],v[50:51]
v_pk_fma_f32 v[116:117],v[52:53],v[84:85],v[116:117]
v_pk_fma_f32 v[118:119],v[54:55],v[86:87],v[118:119]
v_pk_fma_f32 v[120:121],v[56:57],v[88:89],v[120:121]
v_pk_fma_f32 v[122:123],v[58:59],v[90:91],v[122:123]
v_pk_fma_f32 v[52:53],v[124:125],v[48:49],v[50:51]
v_pk_fma_f32 v[54:55],v[126:127],v[48:49],v[50:51]
v_pk_fma_f32 v[56:57],v[128:129],v[48:49],v[50:51]
v_pk_fma_f32 v[58:59],v[130:131],v[48:49],v[50:51]
v_pk_fma_f32 v[124:125],v[52:53],v[92:93],v[124:125]
v_pk_fma_f32 v[126:127],v[54:55],v[94:95],v[126:127]
v_pk_fma_f32 v[128:129],v[56:57],v[96:97],v[128:129]
v_pk_fma_f32 v[130:131],v[58:59],v[98:99],v[130:131]
v_pk_fma_f32 v[52:53],v[132:133],v[48:49],v[50:51]
v_pk_fma_f32 v[54:55],v[134:135],v[48:49],v[50:51]
v_pk_fma_f32 v[56:57],v[136:137],v[48:49],v[50:51]
v_pk_fma_f32 v[58:59],v[138:139],v[48:49],v[50:51]
v_pk_fma_f32 v[132:133],v[52:53],v[100:101],v[132:133]
v_pk_fma_f32 v[134:135],v[54:55],v[102:103],v[134:135]
v_pk_fma_f32 v[136:137],v[56:57],v[104:105],v[136:137]
v_pk_fma_f32 v[138:139],v[58:59],v[106:107],v[138:139]
v_pk_fma_f32 v[52:53],v[140:141],v[48:49],v[50:51]
v_pk_fma_f32 v[54:55],v[142:143],v[48:49],v[50:51]
v_pk_fma_f32 v[56:57],v[144:145],v[48:49],v[50:51]
v_pk_fma_f32 v[58:59],v[146:147],v[48:49],v[50:51]
v_pk_fma_f32 v[140:141],v[52:53],v[108:109],v[140:141]
v_pk_fma_f32 v[142:143],v[54:55],v[110:111],v[142:143]
v_pk_fma_f32 v[144:145],v[56:57],v[112:113],v[144:145]
v_pk_fma_f32 v[146:147],v[58:59],v[114:115],v[146:147]
v_cvt_pk_bf16_f32 v148,v116,v117
v_cvt_pk_bf16_f32 v149,v118,v119
v_cvt_pk_bf16_f32 v150,v120,v121
v_cvt_pk_bf16_f32 v151,v122,v123
v_cvt_pk_bf16_f32 v152,v124,v125
v_cvt_pk_bf16_f32 v153,v126,v127
v_mfma_f32_16x16x32_bf16 v[36:39],v[32:35],v[148:151],0
v_cvt_pk_bf16_f32 v154,v128,v129
v_cvt_pk_bf16_f32 v155,v130,v131
v_mfma_f32_16x16x32_bf16 v[40:43],v[148:151],v[148:151],0
v_cvt_pk_bf16_f32 v156,v132,v133
v_cvt_pk_bf16_f32 v157,v134,v135
M 36,v[32:35],152
v_cvt_pk_bf16_f32 v158,v136,v137
v_cvt_pk_bf16_f32 v159,v138,v139
M 40,v[152:155],152
v_cvt_pk_bf16_f32 v160,v140,v141
v_cvt_pk_bf16_f32 v161,v142,v143
M 36,v[32:35],156
v_cvt_pk_bf16_f32 v162,v144,v145
v_cvt_pk_bf16_f32 v163,v146,v147
M 40,v[156:159],156
buffer_store_dwordx4 v[148:151],v1,s[36:39],s57 offen offset:0 sc0 sc1
M 36,v[32:35],160
buffer_store_dwordx4 v[152:155],v1,s[36:39],s57 offen offset:2048 sc0 sc1
M 40,v[160:163],160
buffer_store_dwordx4 v[156:159],v1,s[36:39],s58 offen offset:0 sc0 sc1
s_nop 4
v_add_f32_dpp v44,v36,v36 row_ror:8 row_mask:0xf bank_mask:0xf
s_nop 0
v_cndmask_b32 v45,v40,v41,s[70:71]
v_cndmask_b32 v46,v42,v43,s[70:71]
ds_write_b32 v2,v44 offset:1024
v_cndmask_b32 v45,v45,v46,s[72:73]
ds_write_b32 v0,v45 offset:1024
buffer_store_dwordx4 v[160:163],v1,s[36:39],s58 offen offset:2048 sc0 sc1
s_waitcnt vmcnt(13) lgkmcnt(0)
s_barrier
R 52,3,1024
R 56,3,1040
R 60,3,1056
L 0
v_pk_add_f32 v[52:53],v[52:53],v[54:55]
v_pk_add_f32 v[56:57],v[56:57],v[58:59]
v_pk_add_f32 v[60:61],v[60:61],v[62:63]
v_add_f32 v52,v52,v53
v_pk_add_f32 v[56:57],v[56:57],v[60:61]
v_mul_f32 v52,s68,v52
v_add_f32 v53,v56,v57
v_mul_f32 v53,s68,v53
v_fma_f32 v53,-v52,v52,v53
v_max_f32 v53,0,v53
v_add_f32 v54,s69,v53
v_fma_f32 v53,s69,v54,v53
v_rsq_f32 v55,v54
v_rsq_f32 v53,v53
v_mul_f32_e64 v54,-v52,v55
v_mul_f32 v53,s76,v53
buffer_store_dwordx4 v[52:55],v4,s[36:39],s59 offen offset:0 sc0 sc1
s_mov_b32 s63,s60
s_cmp_lt_u32 s45,s80
s_cbranch_scc1 Lr_bg0_%=
s_swappc_b64 s[88:89],s[94:95]
s_add_u32 s94,s94,256
s_addc_u32 s95,s95,0
s_branch Lr_bh0_%=
Lr_bg0_%=:
buffer_store_dword v17,v5,s[36:39],s81 offen offset:0 sc0 sc1
buffer_store_dword v17,v5,s[36:39],s81 offen offset:0 sc0 sc1
buffer_store_dword v17,v5,s[36:39],s81 offen offset:0 sc0 sc1
buffer_store_dword v17,v5,s[36:39],s81 offen offset:0 sc0 sc1
Lr_bh0_%=:
s_cmp_lt_u32 s45,s47
s_cbranch_scc0 Lr_mid_%=
Lr_a1_%=:
s_cmp_lg_u32 s67,0
s_cbranch_scc1 Lr_b1_%=
s_waitcnt vmcnt(14)
v_mfma_f32_16x16x32_bf16 v[36:39],v[32:35],v[196:199],0
s_add_u32 s79,s45,s46
s_cmp_lt_u32 s45,64
s_cselect_b32 s78,s49,0
v_mfma_f32_16x16x32_bf16 v[40:43],v[196:199],v[196:199],0
s_cmp_ge_u32 s79,64
s_cselect_b32 s78,s78,0
s_add_u32 s62,s79,s78
M 36,v[32:35],200
s_lshr_b32 s78,s45,1
s_and_b32 s79,s45,1
s_lshl_b32 s79,s79,7
M 40,v[200:203],200
s_lshl_b32 s57,s78,15
s_lshl_b32 s55,s62,14
s_cmp_lt_u32 s62,s47
M 36,v[32:35],204
s_cselect_b32 s55,s55,0x7fff0000
s_add_u32 s57,s57,s79
s_lshl_b32 s59,s78,9
M 40,v[204:207],204
buffer_load_dwordx4 v[164:167],v8,s[84:87],s55 offen offset:0 nt
M 36,v[32:35],208
buffer_load_dwordx4 v[168:171],v8,s[84:87],s55 offen offset:128 nt
M 40,v[208:211],208
buffer_load_dwordx4 v[172:175],v8,s[84:87],s55 offen offset:256 nt
v_mfma_f32_16x16x32_bf16 v[116:119],v[26:29],v[196:199],0
buffer_load_dwordx4 v[176:179],v8,s[84:87],s55 offen offset:384 nt
v_mfma_f32_16x16x32_bf16 v[120:123],v[24:27],v[196:199],0
s_add_u32 s57,s57,8388608
s_add_u32 s59,s59,s79
s_lshl_b32 s60,s45,9
v_mfma_f32_16x16x32_bf16 v[124:127],v[26:29],v[200:203],0
s_add_u32 s58,s57,4096
s_add_u32 s59,s59,106954752
s_add_u32 s60,s60,s66
v_mfma_f32_16x16x32_bf16 v[128:131],v[24:27],v[200:203],0
v_add_f32_dpp v44,v36,v36 row_ror:8 row_mask:0xf bank_mask:0xf
v_cndmask_b32 v45,v40,v41,s[70:71]
v_mfma_f32_16x16x32_bf16 v[132:135],v[26:29],v[204:207],0
ds_write_b32 v2,v44 offset:0
v_mfma_f32_16x16x32_bf16 v[136:139],v[24:27],v[204:207],0
v_cndmask_b32 v46,v42,v43,s[70:71]
v_cndmask_b32 v45,v45,v46,s[72:73]
v_mfma_f32_16x16x32_bf16 v[140:143],v[26:29],v[208:211],0
ds_write_b32 v0,v45 offset:0
v_mfma_f32_16x16x32_bf16 v[144:147],v[24:27],v[208:211],0
L 0
s_barrier
R 52,3,0
R 56,3,16
)"
R"(R 60,3,32
s_waitcnt vmcnt(8)
buffer_store_dword v17,v5,s[36:39],s63 offen offset:0 sc0 sc1
s_mov_b32 s63,0x7fff0000
s_cmp_lt_u32 s45,64
s_cselect_b32 s64,-192,0
s_add_u32 s64,s64,256
s_cmp_lt_u32 s45,64
s_cselect_b32 s61,0,32768
s_add_u32 s61,s61,111177728
s_mul_i32 s56,s45,16384
s_add_u32 s56,s56,98566144
Lr_bpy1_%=:
buffer_load_dword v18,v7,s[36:39],s61 offen offset:0 sc1
s_add_u32 s82,s82,1
s_waitcnt vmcnt(0)
v_cmp_eq_u32 vcc,s53,v18
s_nop 0
s_cmp_eq_u64 vcc,-1
s_cbranch_scc1 Lr_bqy1_%=
s_sleep 2
s_cmp_lt_u32 s82,20000
s_cbranch_scc1 Lr_bpy1_%=
Lr_bqy1_%=:
s_add_u32 m0,s65,16384
s_nop 0
D 9,56,0
D 9,56,1024
D 9,56,2048
D 9,56,3072
s_waitcnt vmcnt(0)
T v[68:69],10,16384
T v[70:71],10,16448
T v[72:73],10,17408
T v[74:75],10,17472
T v[76:77],10,18432
T v[78:79],10,18496
T v[80:81],10,19456
T v[82:83],10,19520
s_mov_b32 s45,s62
s_min_u32 s78,s64,s47
s_mul_i32 s56,s45,16384
s_add_u32 s56,s56,98566144
s_cmp_lt_u32 s45,s78
s_cselect_b32 s67,1,0
s_cselect_b32 s56,s56,0x7fff0000
s_add_u32 m0,s65,0
s_nop 0
D 9,56,0
D 9,56,1024
D 9,56,2048
D 9,56,3072
L 4
v_lshlrev_b32 v84,16,v68
v_and_b32 v85,0xffff0000,v68
v_lshlrev_b32 v86,16,v69
v_and_b32 v87,0xffff0000,v69
v_lshlrev_b32 v88,16,v70
v_and_b32 v89,0xffff0000,v70
v_lshlrev_b32 v90,16,v71
v_and_b32 v91,0xffff0000,v71
v_lshlrev_b32 v92,16,v72
v_and_b32 v93,0xffff0000,v72
v_lshlrev_b32 v94,16,v73
v_and_b32 v95,0xffff0000,v73
v_lshlrev_b32 v96,16,v74
v_and_b32 v97,0xffff0000,v74
v_lshlrev_b32 v98,16,v75
v_and_b32 v99,0xffff0000,v75
L 0
v_lshlrev_b32 v100,16,v76
v_and_b32 v101,0xffff0000,v76
v_lshlrev_b32 v102,16,v77
v_and_b32 v103,0xffff0000,v77
v_lshlrev_b32 v104,16,v78
v_and_b32 v105,0xffff0000,v78
v_lshlrev_b32 v106,16,v79
v_and_b32 v107,0xffff0000,v79
v_lshlrev_b32 v108,16,v80
v_and_b32 v109,0xffff0000,v80
v_lshlrev_b32 v110,16,v81
v_and_b32 v111,0xffff0000,v81
v_lshlrev_b32 v112,16,v82
v_and_b32 v113,0xffff0000,v82
v_lshlrev_b32 v114,16,v83
v_and_b32 v115,0xffff0000,v83
s_branch Lr_c1_%=
Lr_b1_%=:
s_waitcnt vmcnt(10)
T v[68:69],10,16384
T v[70:71],10,16448
T v[72:73],10,17408
T v[74:75],10,17472
T v[76:77],10,18432
T v[78:79],10,18496
T v[80:81],10,19456
T v[82:83],10,19520
v_mfma_f32_16x16x32_bf16 v[36:39],v[32:35],v[196:199],0
s_add_u32 s79,s45,s46
s_cmp_lt_u32 s45,64
s_cselect_b32 s78,s49,0
v_mfma_f32_16x16x32_bf16 v[40:43],v[196:199],v[196:199],0
s_cmp_ge_u32 s79,64
s_cselect_b32 s78,s78,0
s_add_u32 s62,s79,s78
M 36,v[32:35],200
s_lshr_b32 s78,s45,1
s_and_b32 s79,s45,1
s_lshl_b32 s60,s45,9
M 40,v[200:203],200
s_lshl_b32 s57,s78,15
s_lshl_b32 s59,s78,9
s_mov_b32 s45,s62
M 36,v[32:35],204
s_lshl_b32 s79,s79,7
s_min_u32 s78,s64,s47
s_mul_i32 s56,s45,16384
M 40,v[204:207],204
s_lshl_b32 s55,s62,14
s_cmp_lt_u32 s62,s47
s_cselect_b32 s55,s55,0x7fff0000
M 36,v[32:35],208
buffer_load_dwordx4 v[164:167],v8,s[84:87],s55 offen offset:0 nt
M 40,v[208:211],208
buffer_load_dwordx4 v[168:171],v8,s[84:87],s55 offen offset:128 nt
v_mfma_f32_16x16x32_bf16 v[116:119],v[26:29],v[196:199],0
buffer_load_dwordx4 v[172:175],v8,s[84:87],s55 offen offset:256 nt
v_mfma_f32_16x16x32_bf16 v[120:123],v[24:27],v[196:199],0
buffer_load_dwordx4 v[176:179],v8,s[84:87],s55 offen offset:384 nt
v_mfma_f32_16x16x32_bf16 v[124:127],v[26:29],v[200:203],0
s_add_u32 s57,s57,s79
s_add_u32 s56,s56,98566144
s_cmp_lt_u32 s45,s78
v_mfma_f32_16x16x32_bf16 v[128:131],v[24:27],v[200:203],0
s_cselect_b32 s56,s56,0x7fff0000
v_add_f32_dpp v44,v36,v36 row_ror:8 row_mask:0xf bank_mask:0xf
v_cndmask_b32 v45,v40,v41,s[70:71]
v_mfma_f32_16x16x32_bf16 v[132:135],v[26:29],v[204:207],0
s_cselect_b32 s67,1,0
ds_write_b32 v2,v44 offset:0
v_mfma_f32_16x16x32_bf16 v[136:139],v[24:27],v[204:207],0
s_add_u32 s57,s57,8388608
v_cndmask_b32 v46,v42,v43,s[70:71]
v_cndmask_b32 v45,v45,v46,s[72:73]
v_mfma_f32_16x16x32_bf16 v[140:143],v[26:29],v[208:211],0
s_add_u32 s59,s59,s79
ds_write_b32 v0,v45 offset:0
v_mfma_f32_16x16x32_bf16 v[144:147],v[24:27],v[208:211],0
L 2
s_add_u32 m0,s65,0
s_add_u32 s58,s57,4096
v_mfma_f32_16x16x32_bf16 v[84:87],v[26:29],v[68:71],0
D 9,56,0
v_mfma_f32_16x16x32_bf16 v[88:91],v[24:27],v[68:71],0
D 9,56,1024
v_mfma_f32_16x16x32_bf16 v[92:95],v[26:29],v[72:75],0
L 0
D 9,56,2048
v_mfma_f32_16x16x32_bf16 v[96:99],v[24:27],v[72:75],0
s_barrier
R 52,3,0
R 56,3,16
v_mfma_f32_16x16x32_bf16 v[100:103],v[26:29],v[76:79],0
s_add_u32 s59,s59,106954752
s_add_u32 s60,s60,s66
R 60,3,32
v_mfma_f32_16x16x32_bf16 v[104:107],v[24:27],v[76:79],0
D 9,56,3072
v_mfma_f32_16x16x32_bf16 v[108:111],v[26:29],v[80:83],0
v_mfma_f32_16x16x32_bf16 v[112:115],v[24:27],v[80:83],0
Lr_c1_%=:
L 0
v_pk_add_f32 v[52:53],v[52:53],v[54:55]
v_pk_add_f32 v[56:57],v[56:57],v[58:59]
v_pk_add_f32 v[60:61],v[60:61],v[62:63]
v_add_f32 v52,v52,v53
v_pk_add_f32 v[56:57],v[56:57],v[60:61]
v_mul_f32 v52,s68,v52
v_add_f32 v53,v56,v57
v_mul_f32 v53,s68,v53
v_fma_f32 v53,-v52,v52,v53
v_max_f32 v53,0,v53
s_waitcnt vmcnt(12)
buffer_store_dword v17,v5,s[36:39],s63 offen offset:0 sc0 sc1
s_mov_b32 s63,0x7fff0000
v_add_f32 v53,s69,v53
v_rsq_f32 v48,v53
s_nop 0
v_mul_f32_e64 v50,-v52,v48
v_mov_b32 v49,v48
v_mov_b32 v51,v50
v_pk_fma_f32 v[52:53],v[116:117],v[48:49],v[50:51]
v_pk_fma_f32 v[54:55],v[118:119],v[48:49],v[50:51]
v_pk_fma_f32 v[56:57],v[120:121],v[48:49],v[50:51]
v_pk_fma_f32 v[58:59],v[122:123],v[48:49],v[50:51]
v_pk_fma_f32 v[116:117],v[52:53],v[84:85],v[116:117]
v_pk_fma_f32 v[118:119],v[54:55],v[86:87],v[118:119]
v_pk_fma_f32 v[120:121],v[56:57],v[88:89],v[120:121]
v_pk_fma_f32 v[122:123],v[58:59],v[90:91],v[122:123]
v_pk_fma_f32 v[52:53],v[124:125],v[48:49],v[50:51]
v_pk_fma_f32 v[54:55],v[126:127],v[48:49],v[50:51]
v_pk_fma_f32 v[56:57],v[128:129],v[48:49],v[50:51]
v_pk_fma_f32 v[58:59],v[130:131],v[48:49],v[50:51]
v_pk_fma_f32 v[124:125],v[52:53],v[92:93],v[124:125]
v_pk_fma_f32 v[126:127],v[54:55],v[94:95],v[126:127]
v_pk_fma_f32 v[128:129],v[56:57],v[96:97],v[128:129]
v_pk_fma_f32 v[130:131],v[58:59],v[98:99],v[130:131]
v_pk_fma_f32 v[52:53],v[132:133],v[48:49],v[50:51]
v_pk_fma_f32 v[54:55],v[134:135],v[48:49],v[50:51]
v_pk_fma_f32 v[56:57],v[136:137],v[48:49],v[50:51]
v_pk_fma_f32 v[58:59],v[138:139],v[48:49],v[50:51]
v_pk_fma_f32 v[132:133],v[52:53],v[100:101],v[132:133]
v_pk_fma_f32 v[134:135],v[54:55],v[102:103],v[134:135]
v_pk_fma_f32 v[136:137],v[56:57],v[104:105],v[136:137]
v_pk_fma_f32 v[138:139],v[58:59],v[106:107],v[138:139]
v_pk_fma_f32 v[52:53],v[140:141],v[48:49],v[50:51]
v_pk_fma_f32 v[54:55],v[142:143],v[48:49],v[50:51]
v_pk_fma_f32 v[56:57],v[144:145],v[48:49],v[50:51]
v_pk_fma_f32 v[58:59],v[146:147],v[48:49],v[50:51]
v_pk_fma_f32 v[140:141],v[52:53],v[108:109],v[140:141]
v_pk_fma_f32 v[142:143],v[54:55],v[110:111],v[142:143]
v_pk_fma_f32 v[144:145],v[56:57],v[112:113],v[144:145]
v_pk_fma_f32 v[146:147],v[58:59],v[114:115],v[146:147]
v_cvt_pk_bf16_f32 v148,v116,v117
v_cvt_pk_bf16_f32 v149,v118,v119
v_cvt_pk_bf16_f32 v150,v120,v121
v_cvt_pk_bf16_f32 v151,v122,v123
v_cvt_pk_bf16_f32 v152,v124,v125
v_cvt_pk_bf16_f32 v153,v126,v127
v_mfma_f32_16x16x32_bf16 v[36:39],v[32:35],v[148:151],0
v_cvt_pk_bf16_f32 v154,v128,v129
v_cvt_pk_bf16_f32 v155,v130,v131
v_mfma_f32_16x16x32_bf16 v[40:43],v[148:151],v[148:151],0
v_cvt_pk_bf16_f32 v156,v132,v133
v_cvt_pk_bf16_f32 v157,v134,v135
M 36,v[32:35],152
v_cvt_pk_bf16_f32 v158,v136,v137
v_cvt_pk_bf16_f32 v159,v138,v139
M 40,v[152:155],152
v_cvt_pk_bf16_f32 v160,v140,v141
v_cvt_pk_bf16_f32 v161,v142,v143
M 36,v[32:35],156
v_cvt_pk_bf16_f32 v162,v144,v145
v_cvt_pk_bf16_f32 v163,v146,v147
M 40,v[156:159],156
buffer_store_dwordx4 v[148:151],v1,s[36:39],s57 offen offset:0 sc0 sc1
M 36,v[32:35],160
buffer_store_dwordx4 v[152:155],v1,s[36:39],s57 offen offset:2048 sc0 sc1
M 40,v[160:163],160
buffer_store_dwordx4 v[156:159],v1,s[36:39],s58 offen offset:0 sc0 sc1
s_nop 4
v_add_f32_dpp v44,v36,v36 row_ror:8 row_mask:0xf bank_mask:0xf
s_nop 0
v_cndmask_b32 v45,v40,v41,s[70:71]
v_cndmask_b32 v46,v42,v43,s[70:71]
ds_write_b32 v2,v44 offset:1024
v_cndmask_b32 v45,v45,v46,s[72:73]
ds_write_b32 v0,v45 offset:1024
buffer_store_dwordx4 v[160:163],v1,s[36:39],s58 offen offset:2048 sc0 sc1
s_waitcnt vmcnt(13) lgkmcnt(0)
s_barrier
R 52,3,1024
R 56,3,1040
R 60,3,1056
L 0
v_pk_add_f32 v[52:53],v[52:53],v[54:55]
v_pk_add_f32 v[56:57],v[56:57],v[58:59]
v_pk_add_f32 v[60:61],v[60:61],v[62:63]
v_add_f32 v52,v52,v53
v_pk_add_f32 v[56:57],v[56:57],v[60:61]
v_mul_f32 v52,s68,v52
v_add_f32 v53,v56,v57
v_mul_f32 v53,s68,v53
v_fma_f32 v53,-v52,v52,v53
v_max_f32 v53,0,v53
v_add_f32 v54,s69,v53
v_fma_f32 v53,s69,v54,v53
v_rsq_f32 v55,v54
v_rsq_f32 v53,v53
v_mul_f32_e64 v54,-v52,v55
v_mul_f32 v53,s76,v53
buffer_store_dwordx4 v[52:55],v4,s[36:39],s59 offen offset:0 sc0 sc1
s_mov_b32 s63,s60
s_cmp_lt_u32 s45,s80
s_cbranch_scc1 Lr_bg1_%=
s_swappc_b64 s[88:89],s[94:95]
s_add_u32 s94,s94,256
s_addc_u32 s95,s95,0
s_branch Lr_bh1_%=
Lr_bg1_%=:
buffer_store_dword v17,v5,s[36:39],s81 offen offset:0 sc0 sc1
buffer_store_dword v17,v5,s[36:39],s81 offen offset:0 sc0 sc1
buffer_store_dword v17,v5,s[36:39],s81 offen offset:0 sc0 sc1
buffer_store_dword v17,v5,s[36:39],s81 offen offset:0 sc0 sc1
Lr_bh1_%=:
s_cmp_lt_u32 s45,s47
s_cbranch_scc1 Lr_a0_%=
Lr_mid_%=:
s_waitcnt vmcnt(4)
buffer_store_dword v17,v5,s[36:39],s63 offen offset:0 sc0 sc1
s_mov_b32 s63,0x7fff0000
s_mov_b32 s45,s93
s_cmp_eq_u32 s93,0
s_cbranch_scc1 Lr_h0_%=
s_mov_b64 s[40:41],%24
s_mov_b32 s42,7340032
s_mov_b32 s43,0x00020000
s_lshl_b32 s57,s44,11
s_add_u32 s55,s50,1441792
s_add_u32 m0,s57,106496
s_nop 0
buffer_load_dwordx4 v13,s[40:43],s55 offen offset:0  lds
s_add_u32 m0,s57,107520
s_nop 0
buffer_load_dwordx4 v14,s[40:43],s55 offen offset:0  lds
s_add_u32 m0,s57,114560
s_nop 0
buffer_load_dwordx4 v13,s[40:43],s55 offen offset:128  lds
s_add_u32 m0,s57,115584
s_nop 0
buffer_load_dwordx4 v14,s[40:43],s55 offen offset:128  lds
s_add_u32 s55,s50,1572864
s_add_u32 m0,s57,122880
s_nop 0
buffer_load_dwordx4 v13,s[40:43],s55 offen offset:0  lds
s_add_u32 m0,s57,123904
s_nop 0
buffer_load_dwordx4 v14,s[40:43],s55 offen offset:0  lds
s_add_u32 m0,s57,130944
s_nop 0
buffer_load_dwordx4 v13,s[40:43],s55 offen offset:128  lds
s_add_u32 m0,s57,131968
s_nop 0
buffer_load_dwordx4 v14,s[40:43],s55 offen offset:128  lds
s_add_u32 s55,s50,1703936
s_add_u32 m0,s57,139264
s_nop 0
buffer_load_dwordx4 v13,s[40:43],s55 offen offset:0  lds
s_add_u32 m0,s57,140288
s_nop 0
buffer_load_dwordx4 v14,s[40:43],s55 offen offset:0  lds
s_add_u32 m0,s57,147328
s_nop 0
buffer_load_dwordx4 v13,s[40:43],s55 offen offset:128  lds
s_add_u32 m0,s57,148352
s_nop 0
buffer_load_dwordx4 v14,s[40:43],s55 offen offset:128  lds
s_add_u32 s55,s50,1835008
s_add_u32 m0,s57,40960
s_nop 0
buffer_load_dwordx4 v13,s[40:43],s55 offen offset:0  lds
s_add_u32 m0,s57,41984
s_nop 0
buffer_load_dwordx4 v14,s[40:43],s55 offen offset:0  lds
s_add_u32 m0,s57,49024
s_nop 0
buffer_load_dwordx4 v13,s[40:43],s55 offen offset:128  lds
s_add_u32 m0,s57,50048
s_nop 0
buffer_load_dwordx4 v14,s[40:43],s55 offen offset:128  lds
s_add_u32 s55,s50,1966080
s_add_u32 m0,s57,57344
s_nop 0
buffer_load_dwordx4 v13,s[40:43],s55 offen offset:0  lds
s_add_u32 m0,s57,58368
s_nop 0
buffer_load_dwordx4 v14,s[40:43],s55 offen offset:0  lds
s_add_u32 m0,s57,65408
s_nop 0
buffer_load_dwordx4 v13,s[40:43],s55 offen offset:128  lds
s_add_u32 m0,s57,66432
s_nop 0
buffer_load_dwordx4 v14,s[40:43],s55 offen offset:128  lds
Lr_h0_%=:
s_lshr_b32 s78,s45,1
s_and_b32 s79,s45,1
s_lshl_b32 s79,s79,7
s_lshl_b32 s55,s78,15
s_add_u32 s55,s55,s79
s_mul_i32 s56,s45,16384
s_add_u32 s56,s56,98566144
s_lshl_b32 s61,s78,11
s_add_u32 s61,s61,110587904
s_add_u32 s78,s45,s46
s_lshr_b32 s78,s78,1
s_lshl_b32 s62,s78,11
s_add_u32 s62,s62,110587904
s_add_u32 m0,s65,0
s_nop 0
D 9,56,0
D 9,56,1024
D 9,56,2048
D 9,56,3072
Lr_bps_%=:
buffer_load_dword v18,v6,s[36:39],s61 offen offset:0 sc1
s_add_u32 s82,s82,1
s_waitcnt vmcnt(0)
v_cmp_eq_u32 vcc,s53,v18
s_nop 0
s_cmp_eq_u64 vcc,-1
s_cbranch_scc1 Lr_bqs_%=
s_sleep 2
s_cmp_lt_u32 s82,20000
s_cbranch_scc1 Lr_bps_%=
Lr_bqs_%=:
s_add_u32 s78,s55,71303168
buffer_load_dwordx4 v[164:167],v1,s[36:39],s78 offen offset:0 sc1
buffer_load_dwordx4 v[168:171],v1,s[36:39],s78 offen offset:2048 sc1
s_add_u32 s78,s55,71307264
buffer_load_dwordx4 v[172:175],v1,s[36:39],s78 offen offset:0 sc1
buffer_load_dwordx4 v[176:179],v1,s[36:39],s78 offen offset:2048 sc1
s_add_u32 s78,s55,89128960
buffer_load_dwordx4 v[180:183],v1,s[36:39],s78 offen offset:0 sc1
buffer_load_dwordx4 v[184:187],v1,s[36:39],s78 offen offset:2048 sc1
s_add_u32 s78,s55,89133056
buffer_load_dwordx4 v[188:191],v1,s[36:39],s78 offen offset:0 sc1
buffer_load_dwordx4 v[192:195],v1,s[36:39],s78 offen offset:2048 sc1
buffer_load_dword v18,v6,s[36:39],s62 offen offset:0 sc1
s_cmp_eq_u32 s93,0
s_cbranch_scc1 Lr_h2_%=
s_mov_b64 s[40:41],%24
s_mov_b32 s42,7340032
s_mov_b32 s43,0x00020000
s_lshl_b32 s57,s44,11
L 0
s_barrier
s_add_u32 s55,s50,2097152
s_add_u32 m0,s57,73728
s_nop 0
buffer_load_dwordx4 v13,s[40:43],s55 offen offset:0  lds
s_add_u32 m0,s57,74752
s_nop 0
buffer_load_dwordx4 v14,s[40:43],s55 offen offset:0  lds
s_add_u32 m0,s57,81792
s_nop 0
buffer_load_dwordx4 v13,s[40:43],s55 offen offset:128  lds
s_add_u32 m0,s57,82816
s_nop 0
buffer_load_dwordx4 v14,s[40:43],s55 offen offset:128  lds
v_add_u32 v44,90112,v15
v_add_u32 v45,90112,v16
T a[160:161],44,0
T a[162:163],44,512
T a[164:165],45,0
T a[166:167],45,512
T a[168:169],44,4096
T a[170:171],44,4608
T a[172:173],45,4096
T a[174:175],45,4608
L 0
s_barrier
s_add_u32 s55,s50,2228224
s_add_u32 m0,s57,90112
s_nop 0
buffer_load_dwordx4 v13,s[40:43],s55 offen offset:0  lds
s_add_u32 m0,s57,91136
s_nop 0
buffer_load_dwordx4 v14,s[40:43],s55 offen offset:0  lds
s_add_u32 m0,s57,98176
s_nop 0
buffer_load_dwordx4 v13,s[40:43],s55 offen offset:128  lds
s_add_u32 m0,s57,99200
s_nop 0
buffer_load_dwordx4 v14,s[40:43],s55 offen offset:128  lds
v_add_u32 v44,106496,v15
v_add_u32 v45,106496,v16
T a[176:177],44,0
T a[178:179],44,512
T a[180:181],45,0
T a[182:183],45,512
T a[184:185],44,4096
T a[186:187],44,4608
T a[188:189],45,4096
T a[190:191],45,4608
L 0
s_barrier
s_add_u32 s55,s50,2359296
s_add_u32 m0,s57,106496
s_nop 0
buffer_load_dwordx4 v13,s[40:43],s55 offen offset:0  lds
s_add_u32 m0,s57,107520
s_nop 0
buffer_load_dwordx4 v14,s[40:43],s55 offen offset:0  lds
s_add_u32 m0,s57,114560
s_nop 0
buffer_load_dwordx4 v13,s[40:43],s55 offen offset:128  lds
s_add_u32 m0,s57,115584
s_nop 0
buffer_load_dwordx4 v14,s[40:43],s55 offen offset:128  lds
v_add_u32 v44,122880,v15
v_add_u32 v45,122880,v16
T a[192:193],44,0
T a[194:195],44,512
T a[196:197],45,0
T a[198:199],45,512
T a[200:201],44,4096
T a[202:203],44,4608
T a[204:205],45,4096
T a[206:207],45,4608
L 0
)"
R"(s_barrier
s_add_u32 s55,s50,2490368
s_add_u32 m0,s57,122880
s_nop 0
buffer_load_dwordx4 v13,s[40:43],s55 offen offset:0  lds
s_add_u32 m0,s57,123904
s_nop 0
buffer_load_dwordx4 v14,s[40:43],s55 offen offset:0  lds
s_add_u32 m0,s57,130944
s_nop 0
buffer_load_dwordx4 v13,s[40:43],s55 offen offset:128  lds
s_add_u32 m0,s57,131968
s_nop 0
buffer_load_dwordx4 v14,s[40:43],s55 offen offset:128  lds
v_add_u32 v44,139264,v15
v_add_u32 v45,139264,v16
T a[208:209],44,0
T a[210:211],44,512
T a[212:213],45,0
T a[214:215],45,512
T a[216:217],44,4096
T a[218:219],44,4608
T a[220:221],45,4096
T a[222:223],45,4608
L 0
s_barrier
s_add_u32 s55,s50,2621440
s_add_u32 m0,s57,139264
s_nop 0
buffer_load_dwordx4 v13,s[40:43],s55 offen offset:0  lds
s_add_u32 m0,s57,140288
s_nop 0
buffer_load_dwordx4 v14,s[40:43],s55 offen offset:0  lds
s_add_u32 m0,s57,147328
s_nop 0
buffer_load_dwordx4 v13,s[40:43],s55 offen offset:128  lds
s_add_u32 m0,s57,148352
s_nop 0
buffer_load_dwordx4 v14,s[40:43],s55 offen offset:128  lds
v_add_u32 v44,40960,v15
v_add_u32 v45,40960,v16
T a[224:225],44,0
T a[226:227],44,512
T a[228:229],45,0
T a[230:231],45,512
T a[232:233],44,4096
T a[234:235],44,4608
T a[236:237],45,4096
T a[238:239],45,4608
L 0
s_barrier
s_add_u32 s55,s50,2752512
s_add_u32 m0,s57,40960
s_nop 0
buffer_load_dwordx4 v13,s[40:43],s55 offen offset:0  lds
s_add_u32 m0,s57,41984
s_nop 0
buffer_load_dwordx4 v14,s[40:43],s55 offen offset:0  lds
s_add_u32 m0,s57,49024
s_nop 0
buffer_load_dwordx4 v13,s[40:43],s55 offen offset:128  lds
s_add_u32 m0,s57,50048
s_nop 0
buffer_load_dwordx4 v14,s[40:43],s55 offen offset:128  lds
v_add_u32 v44,57344,v15
v_add_u32 v45,57344,v16
T a[240:241],44,0
T a[242:243],44,512
T a[244:245],45,0
T a[246:247],45,512
T a[248:249],44,4096
T a[250:251],44,4608
T a[252:253],45,4096
T a[254:255],45,4608
L 0
s_barrier
s_waitcnt vmcnt(24)
Lr_h2_%=:
s_lshr_b32 s78,s45,1
s_and_b32 s79,s45,1
s_lshl_b32 s79,s79,7
s_lshl_b32 s57,s78,15
s_add_u32 s57,s57,s79
s_add_u32 s57,s57,12582912
s_add_u32 s58,s57,4096
s_lshl_b32 s59,s78,9
s_add_u32 s59,s59,s79
s_add_u32 s59,s59,107020288
s_lshl_b32 s60,s45,9
s_add_u32 s60,s60,s66
s_add_u32 s60,s60,131072
s_add_u32 s45,s45,s46
s_lshr_b32 s78,s45,1
s_and_b32 s79,s45,1
s_lshl_b32 s79,s79,7
s_lshl_b32 s55,s78,15
s_add_u32 s55,s55,s79
s_mul_i32 s56,s45,16384
s_add_u32 s56,s56,98566144
s_lshl_b32 s61,s78,11
s_add_u32 s61,s61,110587904
s_add_u32 s78,s45,s46
s_lshr_b32 s78,s78,1
s_lshl_b32 s62,s78,11
s_add_u32 s62,s62,110587904
s_waitcnt vmcnt(0)
Lr_it0_%=:
T v[68:69],10,0
T v[70:71],10,64
T v[72:73],10,1024
T v[74:75],10,1088
T v[76:77],10,2048
T v[78:79],10,2112
T v[80:81],10,3072
T v[82:83],10,3136
s_nop 0
v_cmp_eq_u32 vcc,s53,v18
s_nop 0
s_cmp_eq_u64 vcc,-1
s_cselect_b32 s78,s45,256
s_cmp_lt_u32 s78,256
s_cbranch_scc1 Lr_ta0_%=
buffer_load_dword v18,v6,s[36:39],s61 offen offset:0 sc1
v_mfma_f32_16x16x32_bf16 v[148:151],v[164:167],v[180:183],0
s_add_u32 m0,s65,16384
M 148,v[168:171],184
D 9,56,0
M 148,v[172:175],188
D 9,56,1024
M 148,v[176:179],192
L 4
D 9,56,2048
v_mfma_f32_16x16x32_bf16 v[40:43],v[164:167],v[164:167],0
D 9,56,3072
M 40,v[168:171],168
L 0
v_lshlrev_b32 v84,16,v68
v_and_b32 v85,0xffff0000,v68
M 40,v[172:175],172
v_lshlrev_b32 v86,16,v69
v_and_b32 v87,0xffff0000,v69
M 40,v[176:179],176
v_lshlrev_b32 v88,16,v70
v_and_b32 v89,0xffff0000,v70
M 40,v[180:183],180
v_lshlrev_b32 v90,16,v71
v_and_b32 v91,0xffff0000,v71
M 40,v[184:187],184
v_lshlrev_b32 v92,16,v72
v_and_b32 v93,0xffff0000,v72
M 40,v[188:191],188
v_lshlrev_b32 v94,16,v73
v_and_b32 v95,0xffff0000,v73
M 40,v[192:195],192
v_lshlrev_b32 v96,16,v74
v_and_b32 v97,0xffff0000,v74
v_mfma_f32_16x16x32_bf16 v[36:39],v[32:35],v[164:167],0
v_lshlrev_b32 v98,16,v75
v_and_b32 v99,0xffff0000,v75
M 36,v[32:35],168
v_lshlrev_b32 v100,16,v76
v_and_b32 v101,0xffff0000,v76
M 36,v[32:35],172
v_lshlrev_b32 v102,16,v77
v_and_b32 v103,0xffff0000,v77
M 36,v[32:35],176
v_lshlrev_b32 v104,16,v78
v_and_b32 v105,0xffff0000,v78
M 36,v[32:35],180
v_fma_f32 v40,2.0,v148,v40
v_fma_f32 v41,2.0,v149,v41
M 36,v[32:35],184
v_fma_f32 v42,2.0,v150,v42
v_fma_f32 v43,2.0,v151,v43
M 36,v[32:35],188
v_cndmask_b32 v45,v40,v41,s[70:71]
v_cndmask_b32 v46,v42,v43,s[70:71]
M 36,v[32:35],192
v_cndmask_b32 v45,v45,v46,s[72:73]
v_lshlrev_b32 v106,16,v79
v_mfma_f32_16x16x32_bf16 v[116:119],v[26:29],v[164:167],0
v_and_b32 v107,0xffff0000,v79
v_lshlrev_b32 v108,16,v80
M 116,v[26:29],180
v_and_b32 v109,0xffff0000,v80
v_lshlrev_b32 v110,16,v81
v_mfma_f32_16x16x32_bf16 v[120:123],v[24:27],v[164:167],0
v_and_b32 v111,0xffff0000,v81
v_lshlrev_b32 v112,16,v82
M 120,v[24:27],180
v_and_b32 v113,0xffff0000,v82
v_lshlrev_b32 v114,16,v83
v_mfma_f32_16x16x32_bf16 v[124:127],v[26:29],v[168:171],0
v_add_f32_dpp v44,v36,v36 row_ror:8 row_mask:0xf bank_mask:0xf
v_and_b32 v115,0xffff0000,v83
M 124,v[26:29],184
ds_write_b32 v2,v44 offset:0
v_mfma_f32_16x16x32_bf16 v[128:131],v[24:27],v[168:171],0
ds_write_b32 v0,v45 offset:0
M 128,v[24:27],184
v_mfma_f32_16x16x32_bf16 v[132:135],v[26:29],v[172:175],0
M 132,v[26:29],188
L 0
v_mfma_f32_16x16x32_bf16 v[136:139],v[24:27],v[172:175],0
s_barrier
R 52,3,0
R 56,3,16
M 136,v[24:27],188
R 60,3,32
v_mfma_f32_16x16x32_bf16 v[140:143],v[26:29],v[176:179],0
M 140,v[26:29],192
v_mfma_f32_16x16x32_bf16 v[144:147],v[24:27],v[176:179],0
M 144,v[24:27],192
L 0
v_pk_add_f32 v[52:53],v[52:53],v[54:55]
v_pk_add_f32 v[56:57],v[56:57],v[58:59]
v_pk_add_f32 v[60:61],v[60:61],v[62:63]
v_add_f32 v52,v52,v53
v_pk_add_f32 v[56:57],v[56:57],v[60:61]
v_mul_f32 v52,s68,v52
v_add_f32 v53,v56,v57
v_mul_f32 v53,s68,v53
v_fma_f32 v53,-v52,v52,v53
v_max_f32 v53,0,v53
s_waitcnt vmcnt(5)
buffer_store_dword v17,v5,s[36:39],s63 offen offset:0 sc0 sc1
s_mov_b32 s63,0x7fff0000
s_waitcnt vmcnt(5)
v_cmp_eq_u32 vcc,s53,v18
s_nop 0
s_cmp_eq_u64 vcc,-1
s_cselect_b32 s78,s45,256
s_cmp_lt_u32 s78,256
s_cbranch_scc1 Lr_tb0_%=
buffer_load_dword v18,v6,s[36:39],s61 offen offset:0 sc1
v_add_f32 v53,s69,v53
v_rsq_f32 v48,v53
s_nop 0
v_mul_f32_e64 v50,-v52,v48
v_mov_b32 v49,v48
v_mov_b32 v51,v50
v_pk_fma_f32 v[52:53],v[116:117],v[48:49],v[50:51]
v_pk_fma_f32 v[54:55],v[118:119],v[48:49],v[50:51]
v_pk_fma_f32 v[56:57],v[120:121],v[48:49],v[50:51]
v_pk_fma_f32 v[58:59],v[122:123],v[48:49],v[50:51]
v_pk_fma_f32 v[116:117],v[52:53],v[84:85],v[116:117]
v_pk_fma_f32 v[118:119],v[54:55],v[86:87],v[118:119]
v_pk_fma_f32 v[120:121],v[56:57],v[88:89],v[120:121]
v_pk_fma_f32 v[122:123],v[58:59],v[90:91],v[122:123]
v_pk_fma_f32 v[52:53],v[124:125],v[48:49],v[50:51]
v_pk_fma_f32 v[54:55],v[126:127],v[48:49],v[50:51]
v_pk_fma_f32 v[56:57],v[128:129],v[48:49],v[50:51]
v_pk_fma_f32 v[58:59],v[130:131],v[48:49],v[50:51]
v_pk_fma_f32 v[124:125],v[52:53],v[92:93],v[124:125]
v_pk_fma_f32 v[126:127],v[54:55],v[94:95],v[126:127]
v_pk_fma_f32 v[128:129],v[56:57],v[96:97],v[128:129]
v_pk_fma_f32 v[130:131],v[58:59],v[98:99],v[130:131]
v_pk_fma_f32 v[52:53],v[132:133],v[48:49],v[50:51]
v_pk_fma_f32 v[54:55],v[134:135],v[48:49],v[50:51]
v_pk_fma_f32 v[56:57],v[136:137],v[48:49],v[50:51]
v_pk_fma_f32 v[58:59],v[138:139],v[48:49],v[50:51]
v_pk_fma_f32 v[132:133],v[52:53],v[100:101],v[132:133]
v_pk_fma_f32 v[134:135],v[54:55],v[102:103],v[134:135]
v_pk_fma_f32 v[136:137],v[56:57],v[104:105],v[136:137]
v_pk_fma_f32 v[138:139],v[58:59],v[106:107],v[138:139]
v_pk_fma_f32 v[52:53],v[140:141],v[48:49],v[50:51]
v_pk_fma_f32 v[54:55],v[142:143],v[48:49],v[50:51]
v_pk_fma_f32 v[56:57],v[144:145],v[48:49],v[50:51]
v_pk_fma_f32 v[58:59],v[146:147],v[48:49],v[50:51]
v_pk_fma_f32 v[140:141],v[52:53],v[108:109],v[140:141]
v_pk_fma_f32 v[142:143],v[54:55],v[110:111],v[142:143]
v_pk_fma_f32 v[144:145],v[56:57],v[112:113],v[144:145]
v_pk_fma_f32 v[146:147],v[58:59],v[114:115],v[146:147]
v_cvt_pk_bf16_f32 v148,v116,v117
v_cvt_pk_bf16_f32 v149,v118,v119
v_cvt_pk_bf16_f32 v150,v120,v121
v_cvt_pk_bf16_f32 v151,v122,v123
v_cvt_pk_bf16_f32 v152,v124,v125
v_cvt_pk_bf16_f32 v153,v126,v127
v_mfma_f32_16x16x32_bf16 v[36:39],v[32:35],v[148:151],0
v_cvt_pk_bf16_f32 v154,v128,v129
v_cvt_pk_bf16_f32 v155,v130,v131
v_mfma_f32_16x16x32_bf16 v[40:43],v[148:151],v[148:151],0
v_cvt_pk_bf16_f32 v156,v132,v133
v_cvt_pk_bf16_f32 v157,v134,v135
M 36,v[32:35],152
v_cvt_pk_bf16_f32 v158,v136,v137
v_cvt_pk_bf16_f32 v159,v138,v139
M 40,v[152:155],152
v_cvt_pk_bf16_f32 v160,v140,v141
v_cvt_pk_bf16_f32 v161,v142,v143
M 36,v[32:35],156
v_cvt_pk_bf16_f32 v162,v144,v145
v_cvt_pk_bf16_f32 v163,v146,v147
M 40,v[156:159],156
buffer_store_dwordx4 v[148:151],v1,s[36:39],s57 offen offset:0 sc0 sc1
M 36,v[32:35],160
buffer_store_dwordx4 v[152:155],v1,s[36:39],s57 offen offset:2048 sc0 sc1
M 40,v[160:163],160
buffer_store_dwordx4 v[156:159],v1,s[36:39],s58 offen offset:0 sc0 sc1
s_nop 4
v_add_f32_dpp v44,v36,v36 row_ror:8 row_mask:0xf bank_mask:0xf
s_nop 0
v_cndmask_b32 v45,v40,v41,s[70:71]
v_cndmask_b32 v46,v42,v43,s[70:71]
ds_write_b32 v2,v44 offset:1024
v_cndmask_b32 v45,v45,v46,s[72:73]
ds_write_b32 v0,v45 offset:1024
buffer_store_dwordx4 v[160:163],v1,s[36:39],s58 offen offset:2048 sc0 sc1
L 0
s_barrier
R 52,3,1024
R 56,3,1040
R 60,3,1056
L 0
v_pk_add_f32 v[52:53],v[52:53],v[54:55]
v_pk_add_f32 v[56:57],v[56:57],v[58:59]
v_pk_add_f32 v[60:61],v[60:61],v[62:63]
v_add_f32 v52,v52,v53
v_pk_add_f32 v[56:57],v[56:57],v[60:61]
v_mul_f32 v52,s68,v52
v_add_f32 v53,v56,v57
v_mul_f32 v53,s68,v53
v_fma_f32 v53,-v52,v52,v53
v_max_f32 v53,0,v53
v_add_f32 v54,s69,v53
v_fma_f32 v53,s69,v54,v53
v_rsq_f32 v55,v54
v_rsq_f32 v53,v53
v_mul_f32_e64 v54,-v52,v55
v_mul_f32 v53,s76,v53
buffer_store_dwordx4 v[52:55],v4,s[36:39],s59 offen offset:0 sc0 sc1
s_mov_b32 s63,s60
s_waitcnt vmcnt(5)
v_cmp_eq_u32 vcc,s53,v18
s_nop 0
s_cmp_eq_u64 vcc,-1
s_cselect_b32 s78,s45,256
s_cmp_lt_u32 s78,256
s_cbranch_scc1 Lr_tc0_%=
s_waitcnt vmcnt(0)
buffer_store_dword v17,v5,s[36:39],s63 offen offset:0 sc0 sc1
s_mov_b32 s63,0x7fff0000
s_cmp_ge_u32 s45,256
s_cbranch_scc1 Lr_done_%=
Lr_bp0_%=:
buffer_load_dword v18,v6,s[36:39],s61 offen offset:0 sc1
s_add_u32 s82,s82,1
s_waitcnt vmcnt(0)
v_cmp_eq_u32 vcc,s53,v18
s_nop 0
s_cmp_eq_u64 vcc,-1
s_cbranch_scc1 Lr_bq0_%=
s_sleep 2
s_cmp_lt_u32 s82,20000
s_cbranch_scc1 Lr_bp0_%=
Lr_bq0_%=:
s_add_u32 s78,s55,71303168
buffer_load_dwordx4 v[196:199],v1,s[36:39],s78 offen offset:0 sc1
buffer_load_dwordx4 v[200:203],v1,s[36:39],s78 offen offset:2048 sc1
s_add_u32 s78,s55,71307264
buffer_load_dwordx4 v[204:207],v1,s[36:39],s78 offen offset:0 sc1
buffer_load_dwordx4 v[208:211],v1,s[36:39],s78 offen offset:2048 sc1
s_add_u32 s78,s55,89128960
buffer_load_dwordx4 v[212:215],v1,s[36:39],s78 offen offset:0 sc1
buffer_load_dwordx4 v[216:219],v1,s[36:39],s78 offen offset:2048 sc1
s_add_u32 s78,s55,89133056
buffer_load_dwordx4 v[220:223],v1,s[36:39],s78 offen offset:0 sc1
buffer_load_dwordx4 v[224:227],v1,s[36:39],s78 offen offset:2048 sc1
buffer_load_dword v18,v6,s[36:39],s62 offen offset:0 sc1
s_lshr_b32 s78,s45,1
s_and_b32 s79,s45,1
s_lshl_b32 s79,s79,7
s_lshl_b32 s57,s78,15
s_add_u32 s57,s57,s79
s_add_u32 s57,s57,12582912
s_add_u32 s58,s57,4096
s_lshl_b32 s59,s78,9
s_add_u32 s59,s59,s79
s_add_u32 s59,s59,107020288
s_lshl_b32 s60,s45,9
s_add_u32 s60,s60,s66
s_add_u32 s60,s60,131072
s_add_u32 s45,s45,s46
s_lshr_b32 s78,s45,1
s_and_b32 s79,s45,1
s_lshl_b32 s79,s79,7
s_lshl_b32 s55,s78,15
s_add_u32 s55,s55,s79
s_mul_i32 s56,s45,16384
s_add_u32 s56,s56,98566144
s_lshl_b32 s61,s78,11
s_add_u32 s61,s61,110587904
s_add_u32 s78,s45,s46
s_lshr_b32 s78,s78,1
s_lshl_b32 s62,s78,11
s_add_u32 s62,s62,110587904
s_waitcnt vmcnt(0)
s_branch Lr_it1_%=
Lr_ta0_%=:
v_mfma_f32_16x16x32_bf16 v[148:151],v[164:167],v[180:183],0
s_add_u32 s78,s55,71303168
s_add_u32 m0,s65,16384
M 148,v[168:171],184
buffer_load_dwordx4 v[196:199],v1,s[36:39],s78 offen offset:0 sc1
M 148,v[172:175],188
buffer_load_dwordx4 v[200:203],v1,s[36:39],s78 offen offset:2048 sc1
M 148,v[176:179],192
L 4
D 9,56,0
v_mfma_f32_16x16x32_bf16 v[40:43],v[164:167],v[164:167],0
D 9,56,1024
M 40,v[168:171],168
L 0
D 9,56,2048
M 40,v[172:175],172
D 9,56,3072
M 40,v[176:179],176
s_add_u32 s78,s55,71307264
v_lshlrev_b32 v84,16,v68
v_and_b32 v85,0xffff0000,v68
M 40,v[180:183],180
buffer_load_dwordx4 v[204:207],v1,s[36:39],s78 offen offset:0 sc1
M 40,v[184:187],184
buffer_load_dwordx4 v[208:211],v1,s[36:39],s78 offen offset:2048 sc1
M 40,v[188:191],188
s_add_u32 s78,s55,89128960
v_lshlrev_b32 v86,16,v69
v_and_b32 v87,0xffff0000,v69
M 40,v[192:195],192
buffer_load_dwordx4 v[212:215],v1,s[36:39],s78 offen offset:0 sc1
v_mfma_f32_16x16x32_bf16 v[36:39],v[32:35],v[164:167],0
buffer_load_dwordx4 v[216:219],v1,s[36:39],s78 offen offset:2048 sc1
M 36,v[32:35],168
s_add_u32 s78,s55,89133056
v_lshlrev_b32 v88,16,v70
v_and_b32 v89,0xffff0000,v70
M 36,v[32:35],172
buffer_load_dwordx4 v[220:223],v1,s[36:39],s78 offen offset:0 sc1
M 36,v[32:35],176
buffer_load_dwordx4 v[224:227],v1,s[36:39],s78 offen offset:2048 sc1
M 36,v[32:35],180
v_fma_f32 v40,2.0,v148,v40
v_fma_f32 v41,2.0,v149,v41
M 36,v[32:35],184
v_fma_f32 v42,2.0,v150,v42
v_fma_f32 v43,2.0,v151,v43
M 36,v[32:35],188
v_cndmask_b32 v45,v40,v41,s[70:71]
v_cndmask_b32 v46,v42,v43,s[70:71]
M 36,v[32:35],192
v_cndmask_b32 v45,v45,v46,s[72:73]
v_lshlrev_b32 v90,16,v71
v_mfma_f32_16x16x32_bf16 v[116:119],v[26:29],v[164:167],0
v_and_b32 v91,0xffff0000,v71
v_lshlrev_b32 v92,16,v72
M 116,v[26:29],180
v_and_b32 v93,0xffff0000,v72
v_lshlrev_b32 v94,16,v73
v_mfma_f32_16x16x32_bf16 v[120:123],v[24:27],v[164:167],0
v_and_b32 v95,0xffff0000,v73
v_lshlrev_b32 v96,16,v74
M 120,v[24:27],180
v_and_b32 v97,0xffff0000,v74
v_lshlrev_b32 v98,16,v75
v_mfma_f32_16x16x32_bf16 v[124:127],v[26:29],v[168:171],0
v_add_f32_dpp v44,v36,v36 row_ror:8 row_mask:0xf bank_mask:0xf
v_and_b32 v99,0xffff0000,v75
M 124,v[26:29],184
ds_write_b32 v2,v44 offset:0
v_mfma_f32_16x16x32_bf16 v[128:131],v[24:27],v[168:171],0
ds_write_b32 v0,v45 offset:0
M 128,v[24:27],184
v_lshlrev_b32 v100,16,v76
v_and_b32 v101,0xffff0000,v76
v_mfma_f32_16x16x32_bf16 v[132:135],v[26:29],v[172:175],0
v_lshlrev_b32 v102,16,v77
v_and_b32 v103,0xffff0000,v77
M 132,v[26:29],188
L 0
v_lshlrev_b32 v104,16,v78
v_and_b32 v105,0xffff0000,v78
v_mfma_f32_16x16x32_bf16 v[136:139],v[24:27],v[172:175],0
s_barrier
R 52,3,0
R 56,3,16
M 136,v[24:27],188
R 60,3,32
v_lshlrev_b32 v106,16,v79
v_mfma_f32_16x16x32_bf16 v[140:143],v[26:29],v[176:179],0
v_and_b32 v107,0xffff0000,v79
v_lshlrev_b32 v108,16,v80
M 140,v[26:29],192
v_and_b32 v109,0xffff0000,v80
v_lshlrev_b32 v110,16,v81
v_mfma_f32_16x16x32_bf16 v[144:147],v[24:27],v[176:179],0
v_and_b32 v111,0xffff0000,v81
v_lshlrev_b32 v112,16,v82
M 144,v[24:27],192
v_and_b32 v113,0xffff0000,v82
)"
R"(v_lshlrev_b32 v114,16,v83
v_and_b32 v115,0xffff0000,v83
L 0
v_pk_add_f32 v[52:53],v[52:53],v[54:55]
v_pk_add_f32 v[56:57],v[56:57],v[58:59]
v_pk_add_f32 v[60:61],v[60:61],v[62:63]
v_add_f32 v52,v52,v53
v_pk_add_f32 v[56:57],v[56:57],v[60:61]
v_mul_f32 v52,s68,v52
v_add_f32 v53,v56,v57
v_mul_f32 v53,s68,v53
v_fma_f32 v53,-v52,v52,v53
v_max_f32 v53,0,v53
buffer_load_dword v18,v6,s[36:39],s62 offen offset:0 sc1
v_add_f32 v53,s69,v53
v_rsq_f32 v48,v53
s_nop 0
v_mul_f32_e64 v50,-v52,v48
v_mov_b32 v49,v48
v_mov_b32 v51,v50
v_pk_fma_f32 v[52:53],v[116:117],v[48:49],v[50:51]
v_pk_fma_f32 v[54:55],v[118:119],v[48:49],v[50:51]
v_pk_fma_f32 v[56:57],v[120:121],v[48:49],v[50:51]
v_pk_fma_f32 v[58:59],v[122:123],v[48:49],v[50:51]
v_pk_fma_f32 v[116:117],v[52:53],v[84:85],v[116:117]
v_pk_fma_f32 v[118:119],v[54:55],v[86:87],v[118:119]
v_pk_fma_f32 v[120:121],v[56:57],v[88:89],v[120:121]
v_pk_fma_f32 v[122:123],v[58:59],v[90:91],v[122:123]
v_pk_fma_f32 v[52:53],v[124:125],v[48:49],v[50:51]
v_pk_fma_f32 v[54:55],v[126:127],v[48:49],v[50:51]
v_pk_fma_f32 v[56:57],v[128:129],v[48:49],v[50:51]
v_pk_fma_f32 v[58:59],v[130:131],v[48:49],v[50:51]
v_pk_fma_f32 v[124:125],v[52:53],v[92:93],v[124:125]
v_pk_fma_f32 v[126:127],v[54:55],v[94:95],v[126:127]
v_pk_fma_f32 v[128:129],v[56:57],v[96:97],v[128:129]
v_pk_fma_f32 v[130:131],v[58:59],v[98:99],v[130:131]
v_pk_fma_f32 v[52:53],v[132:133],v[48:49],v[50:51]
v_pk_fma_f32 v[54:55],v[134:135],v[48:49],v[50:51]
v_pk_fma_f32 v[56:57],v[136:137],v[48:49],v[50:51]
v_pk_fma_f32 v[58:59],v[138:139],v[48:49],v[50:51]
v_pk_fma_f32 v[132:133],v[52:53],v[100:101],v[132:133]
v_pk_fma_f32 v[134:135],v[54:55],v[102:103],v[134:135]
v_pk_fma_f32 v[136:137],v[56:57],v[104:105],v[136:137]
v_pk_fma_f32 v[138:139],v[58:59],v[106:107],v[138:139]
v_pk_fma_f32 v[52:53],v[140:141],v[48:49],v[50:51]
v_pk_fma_f32 v[54:55],v[142:143],v[48:49],v[50:51]
v_pk_fma_f32 v[56:57],v[144:145],v[48:49],v[50:51]
v_pk_fma_f32 v[58:59],v[146:147],v[48:49],v[50:51]
v_pk_fma_f32 v[140:141],v[52:53],v[108:109],v[140:141]
v_pk_fma_f32 v[142:143],v[54:55],v[110:111],v[142:143]
v_pk_fma_f32 v[144:145],v[56:57],v[112:113],v[144:145]
v_pk_fma_f32 v[146:147],v[58:59],v[114:115],v[146:147]
v_cvt_pk_bf16_f32 v148,v116,v117
v_cvt_pk_bf16_f32 v149,v118,v119
v_cvt_pk_bf16_f32 v150,v120,v121
v_cvt_pk_bf16_f32 v151,v122,v123
v_cvt_pk_bf16_f32 v152,v124,v125
v_cvt_pk_bf16_f32 v153,v126,v127
s_waitcnt vmcnt(13)
buffer_store_dword v17,v5,s[36:39],s63 offen offset:0 sc0 sc1
s_mov_b32 s63,0x7fff0000
v_mfma_f32_16x16x32_bf16 v[36:39],v[32:35],v[148:151],0
v_cvt_pk_bf16_f32 v154,v128,v129
v_cvt_pk_bf16_f32 v155,v130,v131
v_mfma_f32_16x16x32_bf16 v[40:43],v[148:151],v[148:151],0
v_cvt_pk_bf16_f32 v156,v132,v133
v_cvt_pk_bf16_f32 v157,v134,v135
M 36,v[32:35],152
v_cvt_pk_bf16_f32 v158,v136,v137
v_cvt_pk_bf16_f32 v159,v138,v139
M 40,v[152:155],152
v_cvt_pk_bf16_f32 v160,v140,v141
v_cvt_pk_bf16_f32 v161,v142,v143
M 36,v[32:35],156
v_cvt_pk_bf16_f32 v162,v144,v145
v_cvt_pk_bf16_f32 v163,v146,v147
M 40,v[156:159],156
buffer_store_dwordx4 v[148:151],v1,s[36:39],s57 offen offset:0 sc0 sc1
M 36,v[32:35],160
buffer_store_dwordx4 v[152:155],v1,s[36:39],s57 offen offset:2048 sc0 sc1
M 40,v[160:163],160
buffer_store_dwordx4 v[156:159],v1,s[36:39],s58 offen offset:0 sc0 sc1
s_nop 4
v_add_f32_dpp v44,v36,v36 row_ror:8 row_mask:0xf bank_mask:0xf
s_nop 0
v_cndmask_b32 v45,v40,v41,s[70:71]
v_cndmask_b32 v46,v42,v43,s[70:71]
ds_write_b32 v2,v44 offset:1024
v_cndmask_b32 v45,v45,v46,s[72:73]
ds_write_b32 v0,v45 offset:1024
buffer_store_dwordx4 v[160:163],v1,s[36:39],s58 offen offset:2048 sc0 sc1
L 0
s_barrier
R 52,3,1024
R 56,3,1040
R 60,3,1056
L 0
v_pk_add_f32 v[52:53],v[52:53],v[54:55]
v_pk_add_f32 v[56:57],v[56:57],v[58:59]
v_pk_add_f32 v[60:61],v[60:61],v[62:63]
v_add_f32 v52,v52,v53
v_pk_add_f32 v[56:57],v[56:57],v[60:61]
v_mul_f32 v52,s68,v52
v_add_f32 v53,v56,v57
v_mul_f32 v53,s68,v53
v_fma_f32 v53,-v52,v52,v53
v_max_f32 v53,0,v53
v_add_f32 v54,s69,v53
v_fma_f32 v53,s69,v54,v53
v_rsq_f32 v55,v54
v_rsq_f32 v53,v53
v_mul_f32_e64 v54,-v52,v55
v_mul_f32 v53,s76,v53
buffer_store_dwordx4 v[52:55],v4,s[36:39],s59 offen offset:0 sc0 sc1
s_mov_b32 s63,s60
s_lshr_b32 s78,s45,1
s_and_b32 s79,s45,1
s_lshl_b32 s79,s79,7
s_lshl_b32 s57,s78,15
s_add_u32 s57,s57,s79
s_add_u32 s57,s57,12582912
s_add_u32 s58,s57,4096
s_lshl_b32 s59,s78,9
s_add_u32 s59,s59,s79
s_add_u32 s59,s59,107020288
s_lshl_b32 s60,s45,9
s_add_u32 s60,s60,s66
s_add_u32 s60,s60,131072
s_add_u32 s45,s45,s46
s_lshr_b32 s78,s45,1
s_and_b32 s79,s45,1
s_lshl_b32 s79,s79,7
s_lshl_b32 s55,s78,15
s_add_u32 s55,s55,s79
s_mul_i32 s56,s45,16384
s_add_u32 s56,s56,98566144
s_lshl_b32 s61,s78,11
s_add_u32 s61,s61,110587904
s_add_u32 s78,s45,s46
s_lshr_b32 s78,s78,1
s_lshl_b32 s62,s78,11
s_add_u32 s62,s62,110587904
s_waitcnt vmcnt(6)
s_branch Lr_it1_%=
Lr_tb0_%=:
v_add_f32 v53,s69,v53
v_rsq_f32 v48,v53
s_nop 0
v_mul_f32_e64 v50,-v52,v48
v_mov_b32 v49,v48
v_mov_b32 v51,v50
v_pk_fma_f32 v[52:53],v[116:117],v[48:49],v[50:51]
v_pk_fma_f32 v[54:55],v[118:119],v[48:49],v[50:51]
v_pk_fma_f32 v[56:57],v[120:121],v[48:49],v[50:51]
v_pk_fma_f32 v[58:59],v[122:123],v[48:49],v[50:51]
s_add_u32 s78,s55,71303168
buffer_load_dwordx4 v[196:199],v1,s[36:39],s78 offen offset:0 sc1
buffer_load_dwordx4 v[200:203],v1,s[36:39],s78 offen offset:2048 sc1
s_add_u32 s78,s55,71307264
buffer_load_dwordx4 v[204:207],v1,s[36:39],s78 offen offset:0 sc1
buffer_load_dwordx4 v[208:211],v1,s[36:39],s78 offen offset:2048 sc1
v_pk_fma_f32 v[116:117],v[52:53],v[84:85],v[116:117]
v_pk_fma_f32 v[118:119],v[54:55],v[86:87],v[118:119]
v_pk_fma_f32 v[120:121],v[56:57],v[88:89],v[120:121]
v_pk_fma_f32 v[122:123],v[58:59],v[90:91],v[122:123]
v_pk_fma_f32 v[52:53],v[124:125],v[48:49],v[50:51]
v_pk_fma_f32 v[54:55],v[126:127],v[48:49],v[50:51]
v_pk_fma_f32 v[56:57],v[128:129],v[48:49],v[50:51]
v_pk_fma_f32 v[58:59],v[130:131],v[48:49],v[50:51]
s_add_u32 s78,s55,89128960
buffer_load_dwordx4 v[212:215],v1,s[36:39],s78 offen offset:0 sc1
buffer_load_dwordx4 v[216:219],v1,s[36:39],s78 offen offset:2048 sc1
s_add_u32 s78,s55,89133056
buffer_load_dwordx4 v[220:223],v1,s[36:39],s78 offen offset:0 sc1
buffer_load_dwordx4 v[224:227],v1,s[36:39],s78 offen offset:2048 sc1
v_pk_fma_f32 v[124:125],v[52:53],v[92:93],v[124:125]
v_pk_fma_f32 v[126:127],v[54:55],v[94:95],v[126:127]
v_pk_fma_f32 v[128:129],v[56:57],v[96:97],v[128:129]
v_pk_fma_f32 v[130:131],v[58:59],v[98:99],v[130:131]
v_pk_fma_f32 v[52:53],v[132:133],v[48:49],v[50:51]
v_pk_fma_f32 v[54:55],v[134:135],v[48:49],v[50:51]
v_pk_fma_f32 v[56:57],v[136:137],v[48:49],v[50:51]
v_pk_fma_f32 v[58:59],v[138:139],v[48:49],v[50:51]
v_pk_fma_f32 v[132:133],v[52:53],v[100:101],v[132:133]
v_pk_fma_f32 v[134:135],v[54:55],v[102:103],v[134:135]
v_pk_fma_f32 v[136:137],v[56:57],v[104:105],v[136:137]
v_pk_fma_f32 v[138:139],v[58:59],v[106:107],v[138:139]
v_pk_fma_f32 v[52:53],v[140:141],v[48:49],v[50:51]
v_pk_fma_f32 v[54:55],v[142:143],v[48:49],v[50:51]
v_pk_fma_f32 v[56:57],v[144:145],v[48:49],v[50:51]
v_pk_fma_f32 v[58:59],v[146:147],v[48:49],v[50:51]
v_pk_fma_f32 v[140:141],v[52:53],v[108:109],v[140:141]
v_pk_fma_f32 v[142:143],v[54:55],v[110:111],v[142:143]
v_pk_fma_f32 v[144:145],v[56:57],v[112:113],v[144:145]
v_pk_fma_f32 v[146:147],v[58:59],v[114:115],v[146:147]
buffer_load_dword v18,v6,s[36:39],s62 offen offset:0 sc1
v_cvt_pk_bf16_f32 v148,v116,v117
v_cvt_pk_bf16_f32 v149,v118,v119
v_cvt_pk_bf16_f32 v150,v120,v121
v_cvt_pk_bf16_f32 v151,v122,v123
v_cvt_pk_bf16_f32 v152,v124,v125
v_cvt_pk_bf16_f32 v153,v126,v127
v_mfma_f32_16x16x32_bf16 v[36:39],v[32:35],v[148:151],0
v_cvt_pk_bf16_f32 v154,v128,v129
v_cvt_pk_bf16_f32 v155,v130,v131
v_mfma_f32_16x16x32_bf16 v[40:43],v[148:151],v[148:151],0
v_cvt_pk_bf16_f32 v156,v132,v133
v_cvt_pk_bf16_f32 v157,v134,v135
M 36,v[32:35],152
v_cvt_pk_bf16_f32 v158,v136,v137
v_cvt_pk_bf16_f32 v159,v138,v139
M 40,v[152:155],152
v_cvt_pk_bf16_f32 v160,v140,v141
v_cvt_pk_bf16_f32 v161,v142,v143
M 36,v[32:35],156
v_cvt_pk_bf16_f32 v162,v144,v145
v_cvt_pk_bf16_f32 v163,v146,v147
M 40,v[156:159],156
buffer_store_dwordx4 v[148:151],v1,s[36:39],s57 offen offset:0 sc0 sc1
M 36,v[32:35],160
buffer_store_dwordx4 v[152:155],v1,s[36:39],s57 offen offset:2048 sc0 sc1
M 40,v[160:163],160
buffer_store_dwordx4 v[156:159],v1,s[36:39],s58 offen offset:0 sc0 sc1
s_nop 4
v_add_f32_dpp v44,v36,v36 row_ror:8 row_mask:0xf bank_mask:0xf
s_nop 0
v_cndmask_b32 v45,v40,v41,s[70:71]
v_cndmask_b32 v46,v42,v43,s[70:71]
ds_write_b32 v2,v44 offset:1024
v_cndmask_b32 v45,v45,v46,s[72:73]
ds_write_b32 v0,v45 offset:1024
buffer_store_dwordx4 v[160:163],v1,s[36:39],s58 offen offset:2048 sc0 sc1
L 0
s_barrier
R 52,3,1024
R 56,3,1040
R 60,3,1056
L 0
v_pk_add_f32 v[52:53],v[52:53],v[54:55]
v_pk_add_f32 v[56:57],v[56:57],v[58:59]
v_pk_add_f32 v[60:61],v[60:61],v[62:63]
v_add_f32 v52,v52,v53
v_pk_add_f32 v[56:57],v[56:57],v[60:61]
v_mul_f32 v52,s68,v52
v_add_f32 v53,v56,v57
v_mul_f32 v53,s68,v53
v_fma_f32 v53,-v52,v52,v53
v_max_f32 v53,0,v53
v_add_f32 v54,s69,v53
v_fma_f32 v53,s69,v54,v53
v_rsq_f32 v55,v54
v_rsq_f32 v53,v53
v_mul_f32_e64 v54,-v52,v55
v_mul_f32 v53,s76,v53
buffer_store_dwordx4 v[52:55],v4,s[36:39],s59 offen offset:0 sc0 sc1
s_mov_b32 s63,s60
s_lshr_b32 s78,s45,1
s_and_b32 s79,s45,1
s_lshl_b32 s79,s79,7
s_lshl_b32 s57,s78,15
s_add_u32 s57,s57,s79
s_add_u32 s57,s57,12582912
s_add_u32 s58,s57,4096
s_lshl_b32 s59,s78,9
s_add_u32 s59,s59,s79
s_add_u32 s59,s59,107020288
s_lshl_b32 s60,s45,9
s_add_u32 s60,s60,s66
s_add_u32 s60,s60,131072
s_add_u32 s45,s45,s46
s_lshr_b32 s78,s45,1
s_and_b32 s79,s45,1
s_lshl_b32 s79,s79,7
s_lshl_b32 s55,s78,15
s_add_u32 s55,s55,s79
s_mul_i32 s56,s45,16384
s_add_u32 s56,s56,98566144
s_lshl_b32 s61,s78,11
s_add_u32 s61,s61,110587904
s_add_u32 s78,s45,s46
s_lshr_b32 s78,s78,1
s_lshl_b32 s62,s78,11
s_add_u32 s62,s62,110587904
s_waitcnt vmcnt(5)
s_branch Lr_it1_%=
Lr_tc0_%=:
s_add_u32 s78,s55,71303168
buffer_load_dwordx4 v[196:199],v1,s[36:39],s78 offen offset:0 sc1
buffer_load_dwordx4 v[200:203],v1,s[36:39],s78 offen offset:2048 sc1
s_add_u32 s78,s55,71307264
buffer_load_dwordx4 v[204:207],v1,s[36:39],s78 offen offset:0 sc1
buffer_load_dwordx4 v[208:211],v1,s[36:39],s78 offen offset:2048 sc1
s_add_u32 s78,s55,89128960
buffer_load_dwordx4 v[212:215],v1,s[36:39],s78 offen offset:0 sc1
buffer_load_dwordx4 v[216:219],v1,s[36:39],s78 offen offset:2048 sc1
s_add_u32 s78,s55,89133056
buffer_load_dwordx4 v[220:223],v1,s[36:39],s78 offen offset:0 sc1
buffer_load_dwordx4 v[224:227],v1,s[36:39],s78 offen offset:2048 sc1
buffer_load_dword v18,v6,s[36:39],s62 offen offset:0 sc1
s_waitcnt vmcnt(9)
buffer_store_dword v17,v5,s[36:39],s63 offen offset:0 sc0 sc1
s_mov_b32 s63,0x7fff0000
s_lshr_b32 s78,s45,1
s_and_b32 s79,s45,1
s_lshl_b32 s79,s79,7
s_lshl_b32 s57,s78,15
s_add_u32 s57,s57,s79
s_add_u32 s57,s57,12582912
s_add_u32 s58,s57,4096
s_lshl_b32 s59,s78,9
s_add_u32 s59,s59,s79
s_add_u32 s59,s59,107020288
s_lshl_b32 s60,s45,9
s_add_u32 s60,s60,s66
s_add_u32 s60,s60,131072
s_add_u32 s45,s45,s46
s_lshr_b32 s78,s45,1
s_and_b32 s79,s45,1
s_lshl_b32 s79,s79,7
s_lshl_b32 s55,s78,15
s_add_u32 s55,s55,s79
s_mul_i32 s56,s45,16384
s_add_u32 s56,s56,98566144
s_lshl_b32 s61,s78,11
s_add_u32 s61,s61,110587904
s_add_u32 s78,s45,s46
s_lshr_b32 s78,s78,1
s_lshl_b32 s62,s78,11
s_add_u32 s62,s62,110587904
s_waitcnt vmcnt(1)
s_branch Lr_it1_%=
Lr_it1_%=:
T v[68:69],10,16384
T v[70:71],10,16448
T v[72:73],10,17408
T v[74:75],10,17472
T v[76:77],10,18432
T v[78:79],10,18496
T v[80:81],10,19456
T v[82:83],10,19520
s_nop 0
v_cmp_eq_u32 vcc,s53,v18
s_nop 0
s_cmp_eq_u64 vcc,-1
s_cselect_b32 s78,s45,256
s_cmp_lt_u32 s78,256
s_cbranch_scc1 Lr_ta1_%=
buffer_load_dword v18,v6,s[36:39],s61 offen offset:0 sc1
v_mfma_f32_16x16x32_bf16 v[148:151],v[196:199],v[212:215],0
s_add_u32 m0,s65,0
M 148,v[200:203],216
D 9,56,0
M 148,v[204:207],220
D 9,56,1024
M 148,v[208:211],224
L 4
D 9,56,2048
v_mfma_f32_16x16x32_bf16 v[40:43],v[196:199],v[196:199],0
D 9,56,3072
M 40,v[200:203],200
L 0
v_lshlrev_b32 v84,16,v68
v_and_b32 v85,0xffff0000,v68
M 40,v[204:207],204
v_lshlrev_b32 v86,16,v69
v_and_b32 v87,0xffff0000,v69
M 40,v[208:211],208
v_lshlrev_b32 v88,16,v70
v_and_b32 v89,0xffff0000,v70
M 40,v[212:215],212
v_lshlrev_b32 v90,16,v71
v_and_b32 v91,0xffff0000,v71
M 40,v[216:219],216
v_lshlrev_b32 v92,16,v72
v_and_b32 v93,0xffff0000,v72
M 40,v[220:223],220
v_lshlrev_b32 v94,16,v73
v_and_b32 v95,0xffff0000,v73
M 40,v[224:227],224
v_lshlrev_b32 v96,16,v74
v_and_b32 v97,0xffff0000,v74
v_mfma_f32_16x16x32_bf16 v[36:39],v[32:35],v[196:199],0
v_lshlrev_b32 v98,16,v75
v_and_b32 v99,0xffff0000,v75
M 36,v[32:35],200
v_lshlrev_b32 v100,16,v76
v_and_b32 v101,0xffff0000,v76
M 36,v[32:35],204
v_lshlrev_b32 v102,16,v77
v_and_b32 v103,0xffff0000,v77
M 36,v[32:35],208
v_lshlrev_b32 v104,16,v78
v_and_b32 v105,0xffff0000,v78
M 36,v[32:35],212
v_fma_f32 v40,2.0,v148,v40
v_fma_f32 v41,2.0,v149,v41
M 36,v[32:35],216
v_fma_f32 v42,2.0,v150,v42
v_fma_f32 v43,2.0,v151,v43
M 36,v[32:35],220
v_cndmask_b32 v45,v40,v41,s[70:71]
v_cndmask_b32 v46,v42,v43,s[70:71]
M 36,v[32:35],224
v_cndmask_b32 v45,v45,v46,s[72:73]
v_lshlrev_b32 v106,16,v79
v_mfma_f32_16x16x32_bf16 v[116:119],v[26:29],v[196:199],0
v_and_b32 v107,0xffff0000,v79
v_lshlrev_b32 v108,16,v80
M 116,v[26:29],212
v_and_b32 v109,0xffff0000,v80
v_lshlrev_b32 v110,16,v81
v_mfma_f32_16x16x32_bf16 v[120:123],v[24:27],v[196:199],0
v_and_b32 v111,0xffff0000,v81
v_lshlrev_b32 v112,16,v82
M 120,v[24:27],212
v_and_b32 v113,0xffff0000,v82
v_lshlrev_b32 v114,16,v83
v_mfma_f32_16x16x32_bf16 v[124:127],v[26:29],v[200:203],0
v_add_f32_dpp v44,v36,v36 row_ror:8 row_mask:0xf bank_mask:0xf
v_and_b32 v115,0xffff0000,v83
M 124,v[26:29],216
ds_write_b32 v2,v44 offset:0
v_mfma_f32_16x16x32_bf16 v[128:131],v[24:27],v[200:203],0
ds_write_b32 v0,v45 offset:0
M 128,v[24:27],216
v_mfma_f32_16x16x32_bf16 v[132:135],v[26:29],v[204:207],0
M 132,v[26:29],220
L 0
v_mfma_f32_16x16x32_bf16 v[136:139],v[24:27],v[204:207],0
s_barrier
R 52,3,0
R 56,3,16
M 136,v[24:27],220
R 60,3,32
v_mfma_f32_16x16x32_bf16 v[140:143],v[26:29],v[208:211],0
M 140,v[26:29],224
v_mfma_f32_16x16x32_bf16 v[144:147],v[24:27],v[208:211],0
M 144,v[24:27],224
L 0
v_pk_add_f32 v[52:53],v[52:53],v[54:55]
v_pk_add_f32 v[56:57],v[56:57],v[58:59]
v_pk_add_f32 v[60:61],v[60:61],v[62:63]
v_add_f32 v52,v52,v53
v_pk_add_f32 v[56:57],v[56:57],v[60:61]
v_mul_f32 v52,s68,v52
v_add_f32 v53,v56,v57
v_mul_f32 v53,s68,v53
v_fma_f32 v53,-v52,v52,v53
v_max_f32 v53,0,v53
s_waitcnt vmcnt(5)
buffer_store_dword v17,v5,s[36:39],s63 offen offset:0 sc0 sc1
s_mov_b32 s63,0x7fff0000
s_waitcnt vmcnt(5)
v_cmp_eq_u32 vcc,s53,v18
s_nop 0
s_cmp_eq_u64 vcc,-1
s_cselect_b32 s78,s45,256
s_cmp_lt_u32 s78,256
s_cbranch_scc1 Lr_tb1_%=
buffer_load_dword v18,v6,s[36:39],s61 offen offset:0 sc1
v_add_f32 v53,s69,v53
v_rsq_f32 v48,v53
s_nop 0
v_mul_f32_e64 v50,-v52,v48
v_mov_b32 v49,v48
v_mov_b32 v51,v50
v_pk_fma_f32 v[52:53],v[116:117],v[48:49],v[50:51]
v_pk_fma_f32 v[54:55],v[118:119],v[48:49],v[50:51]
v_pk_fma_f32 v[56:57],v[120:121],v[48:49],v[50:51]
v_pk_fma_f32 v[58:59],v[122:123],v[48:49],v[50:51]
v_pk_fma_f32 v[116:117],v[52:53],v[84:85],v[116:117]
v_pk_fma_f32 v[118:119],v[54:55],v[86:87],v[118:119]
v_pk_fma_f32 v[120:121],v[56:57],v[88:89],v[120:121]
v_pk_fma_f32 v[122:123],v[58:59],v[90:91],v[122:123]
v_pk_fma_f32 v[52:53],v[124:125],v[48:49],v[50:51]
v_pk_fma_f32 v[54:55],v[126:127],v[48:49],v[50:51]
v_pk_fma_f32 v[56:57],v[128:129],v[48:49],v[50:51]
v_pk_fma_f32 v[58:59],v[130:131],v[48:49],v[50:51]
v_pk_fma_f32 v[124:125],v[52:53],v[92:93],v[124:125]
v_pk_fma_f32 v[126:127],v[54:55],v[94:95],v[126:127]
v_pk_fma_f32 v[128:129],v[56:57],v[96:97],v[128:129]
v_pk_fma_f32 v[130:131],v[58:59],v[98:99],v[130:131]
v_pk_fma_f32 v[52:53],v[132:133],v[48:49],v[50:51]
v_pk_fma_f32 v[54:55],v[134:135],v[48:49],v[50:51]
v_pk_fma_f32 v[56:57],v[136:137],v[48:49],v[50:51]
v_pk_fma_f32 v[58:59],v[138:139],v[48:49],v[50:51]
v_pk_fma_f32 v[132:133],v[52:53],v[100:101],v[132:133]
v_pk_fma_f32 v[134:135],v[54:55],v[102:103],v[134:135]
v_pk_fma_f32 v[136:137],v[56:57],v[104:105],v[136:137]
v_pk_fma_f32 v[138:139],v[58:59],v[106:107],v[138:139]
v_pk_fma_f32 v[52:53],v[140:141],v[48:49],v[50:51]
v_pk_fma_f32 v[54:55],v[142:143],v[48:49],v[50:51]
v_pk_fma_f32 v[56:57],v[144:145],v[48:49],v[50:51]
v_pk_fma_f32 v[58:59],v[146:147],v[48:49],v[50:51]
v_pk_fma_f32 v[140:141],v[52:53],v[108:109],v[140:141]
v_pk_fma_f32 v[142:143],v[54:55],v[110:111],v[142:143]
v_pk_fma_f32 v[144:145],v[56:57],v[112:113],v[144:145]
v_pk_fma_f32 v[146:147],v[58:59],v[114:115],v[146:147]
v_cvt_pk_bf16_f32 v148,v116,v117
v_cvt_pk_bf16_f32 v149,v118,v119
)"
R"(v_cvt_pk_bf16_f32 v150,v120,v121
v_cvt_pk_bf16_f32 v151,v122,v123
v_cvt_pk_bf16_f32 v152,v124,v125
v_cvt_pk_bf16_f32 v153,v126,v127
v_mfma_f32_16x16x32_bf16 v[36:39],v[32:35],v[148:151],0
v_cvt_pk_bf16_f32 v154,v128,v129
v_cvt_pk_bf16_f32 v155,v130,v131
v_mfma_f32_16x16x32_bf16 v[40:43],v[148:151],v[148:151],0
v_cvt_pk_bf16_f32 v156,v132,v133
v_cvt_pk_bf16_f32 v157,v134,v135
M 36,v[32:35],152
v_cvt_pk_bf16_f32 v158,v136,v137
v_cvt_pk_bf16_f32 v159,v138,v139
M 40,v[152:155],152
v_cvt_pk_bf16_f32 v160,v140,v141
v_cvt_pk_bf16_f32 v161,v142,v143
M 36,v[32:35],156
v_cvt_pk_bf16_f32 v162,v144,v145
v_cvt_pk_bf16_f32 v163,v146,v147
M 40,v[156:159],156
buffer_store_dwordx4 v[148:151],v1,s[36:39],s57 offen offset:0 sc0 sc1
M 36,v[32:35],160
buffer_store_dwordx4 v[152:155],v1,s[36:39],s57 offen offset:2048 sc0 sc1
M 40,v[160:163],160
buffer_store_dwordx4 v[156:159],v1,s[36:39],s58 offen offset:0 sc0 sc1
s_nop 4
v_add_f32_dpp v44,v36,v36 row_ror:8 row_mask:0xf bank_mask:0xf
s_nop 0
v_cndmask_b32 v45,v40,v41,s[70:71]
v_cndmask_b32 v46,v42,v43,s[70:71]
ds_write_b32 v2,v44 offset:1024
v_cndmask_b32 v45,v45,v46,s[72:73]
ds_write_b32 v0,v45 offset:1024
buffer_store_dwordx4 v[160:163],v1,s[36:39],s58 offen offset:2048 sc0 sc1
L 0
s_barrier
R 52,3,1024
R 56,3,1040
R 60,3,1056
L 0
v_pk_add_f32 v[52:53],v[52:53],v[54:55]
v_pk_add_f32 v[56:57],v[56:57],v[58:59]
v_pk_add_f32 v[60:61],v[60:61],v[62:63]
v_add_f32 v52,v52,v53
v_pk_add_f32 v[56:57],v[56:57],v[60:61]
v_mul_f32 v52,s68,v52
v_add_f32 v53,v56,v57
v_mul_f32 v53,s68,v53
v_fma_f32 v53,-v52,v52,v53
v_max_f32 v53,0,v53
v_add_f32 v54,s69,v53
v_fma_f32 v53,s69,v54,v53
v_rsq_f32 v55,v54
v_rsq_f32 v53,v53
v_mul_f32_e64 v54,-v52,v55
v_mul_f32 v53,s76,v53
buffer_store_dwordx4 v[52:55],v4,s[36:39],s59 offen offset:0 sc0 sc1
s_mov_b32 s63,s60
s_waitcnt vmcnt(5)
v_cmp_eq_u32 vcc,s53,v18
s_nop 0
s_cmp_eq_u64 vcc,-1
s_cselect_b32 s78,s45,256
s_cmp_lt_u32 s78,256
s_cbranch_scc1 Lr_tc1_%=
s_waitcnt vmcnt(0)
buffer_store_dword v17,v5,s[36:39],s63 offen offset:0 sc0 sc1
s_mov_b32 s63,0x7fff0000
s_cmp_ge_u32 s45,256
s_cbranch_scc1 Lr_done_%=
Lr_bp1_%=:
buffer_load_dword v18,v6,s[36:39],s61 offen offset:0 sc1
s_add_u32 s82,s82,1
s_waitcnt vmcnt(0)
v_cmp_eq_u32 vcc,s53,v18
s_nop 0
s_cmp_eq_u64 vcc,-1
s_cbranch_scc1 Lr_bq1_%=
s_sleep 2
s_cmp_lt_u32 s82,20000
s_cbranch_scc1 Lr_bp1_%=
Lr_bq1_%=:
s_add_u32 s78,s55,71303168
buffer_load_dwordx4 v[164:167],v1,s[36:39],s78 offen offset:0 sc1
buffer_load_dwordx4 v[168:171],v1,s[36:39],s78 offen offset:2048 sc1
s_add_u32 s78,s55,71307264
buffer_load_dwordx4 v[172:175],v1,s[36:39],s78 offen offset:0 sc1
buffer_load_dwordx4 v[176:179],v1,s[36:39],s78 offen offset:2048 sc1
s_add_u32 s78,s55,89128960
buffer_load_dwordx4 v[180:183],v1,s[36:39],s78 offen offset:0 sc1
buffer_load_dwordx4 v[184:187],v1,s[36:39],s78 offen offset:2048 sc1
s_add_u32 s78,s55,89133056
buffer_load_dwordx4 v[188:191],v1,s[36:39],s78 offen offset:0 sc1
buffer_load_dwordx4 v[192:195],v1,s[36:39],s78 offen offset:2048 sc1
buffer_load_dword v18,v6,s[36:39],s62 offen offset:0 sc1
s_lshr_b32 s78,s45,1
s_and_b32 s79,s45,1
s_lshl_b32 s79,s79,7
s_lshl_b32 s57,s78,15
s_add_u32 s57,s57,s79
s_add_u32 s57,s57,12582912
s_add_u32 s58,s57,4096
s_lshl_b32 s59,s78,9
s_add_u32 s59,s59,s79
s_add_u32 s59,s59,107020288
s_lshl_b32 s60,s45,9
s_add_u32 s60,s60,s66
s_add_u32 s60,s60,131072
s_add_u32 s45,s45,s46
s_lshr_b32 s78,s45,1
s_and_b32 s79,s45,1
s_lshl_b32 s79,s79,7
s_lshl_b32 s55,s78,15
s_add_u32 s55,s55,s79
s_mul_i32 s56,s45,16384
s_add_u32 s56,s56,98566144
s_lshl_b32 s61,s78,11
s_add_u32 s61,s61,110587904
s_add_u32 s78,s45,s46
s_lshr_b32 s78,s78,1
s_lshl_b32 s62,s78,11
s_add_u32 s62,s62,110587904
s_waitcnt vmcnt(0)
s_branch Lr_it0_%=
Lr_ta1_%=:
v_mfma_f32_16x16x32_bf16 v[148:151],v[196:199],v[212:215],0
s_add_u32 s78,s55,71303168
s_add_u32 m0,s65,0
M 148,v[200:203],216
buffer_load_dwordx4 v[164:167],v1,s[36:39],s78 offen offset:0 sc1
M 148,v[204:207],220
buffer_load_dwordx4 v[168:171],v1,s[36:39],s78 offen offset:2048 sc1
M 148,v[208:211],224
L 4
D 9,56,0
v_mfma_f32_16x16x32_bf16 v[40:43],v[196:199],v[196:199],0
D 9,56,1024
M 40,v[200:203],200
L 0
D 9,56,2048
M 40,v[204:207],204
D 9,56,3072
M 40,v[208:211],208
s_add_u32 s78,s55,71307264
v_lshlrev_b32 v84,16,v68
v_and_b32 v85,0xffff0000,v68
M 40,v[212:215],212
buffer_load_dwordx4 v[172:175],v1,s[36:39],s78 offen offset:0 sc1
M 40,v[216:219],216
buffer_load_dwordx4 v[176:179],v1,s[36:39],s78 offen offset:2048 sc1
M 40,v[220:223],220
s_add_u32 s78,s55,89128960
v_lshlrev_b32 v86,16,v69
v_and_b32 v87,0xffff0000,v69
M 40,v[224:227],224
buffer_load_dwordx4 v[180:183],v1,s[36:39],s78 offen offset:0 sc1
v_mfma_f32_16x16x32_bf16 v[36:39],v[32:35],v[196:199],0
buffer_load_dwordx4 v[184:187],v1,s[36:39],s78 offen offset:2048 sc1
M 36,v[32:35],200
s_add_u32 s78,s55,89133056
v_lshlrev_b32 v88,16,v70
v_and_b32 v89,0xffff0000,v70
M 36,v[32:35],204
buffer_load_dwordx4 v[188:191],v1,s[36:39],s78 offen offset:0 sc1
M 36,v[32:35],208
buffer_load_dwordx4 v[192:195],v1,s[36:39],s78 offen offset:2048 sc1
M 36,v[32:35],212
v_fma_f32 v40,2.0,v148,v40
v_fma_f32 v41,2.0,v149,v41
M 36,v[32:35],216
v_fma_f32 v42,2.0,v150,v42
v_fma_f32 v43,2.0,v151,v43
M 36,v[32:35],220
v_cndmask_b32 v45,v40,v41,s[70:71]
v_cndmask_b32 v46,v42,v43,s[70:71]
M 36,v[32:35],224
v_cndmask_b32 v45,v45,v46,s[72:73]
v_lshlrev_b32 v90,16,v71
v_mfma_f32_16x16x32_bf16 v[116:119],v[26:29],v[196:199],0
v_and_b32 v91,0xffff0000,v71
v_lshlrev_b32 v92,16,v72
M 116,v[26:29],212
v_and_b32 v93,0xffff0000,v72
v_lshlrev_b32 v94,16,v73
v_mfma_f32_16x16x32_bf16 v[120:123],v[24:27],v[196:199],0
v_and_b32 v95,0xffff0000,v73
v_lshlrev_b32 v96,16,v74
M 120,v[24:27],212
v_and_b32 v97,0xffff0000,v74
v_lshlrev_b32 v98,16,v75
v_mfma_f32_16x16x32_bf16 v[124:127],v[26:29],v[200:203],0
v_add_f32_dpp v44,v36,v36 row_ror:8 row_mask:0xf bank_mask:0xf
v_and_b32 v99,0xffff0000,v75
M 124,v[26:29],216
ds_write_b32 v2,v44 offset:0
v_mfma_f32_16x16x32_bf16 v[128:131],v[24:27],v[200:203],0
ds_write_b32 v0,v45 offset:0
M 128,v[24:27],216
v_lshlrev_b32 v100,16,v76
v_and_b32 v101,0xffff0000,v76
v_mfma_f32_16x16x32_bf16 v[132:135],v[26:29],v[204:207],0
v_lshlrev_b32 v102,16,v77
v_and_b32 v103,0xffff0000,v77
M 132,v[26:29],220
L 0
v_lshlrev_b32 v104,16,v78
v_and_b32 v105,0xffff0000,v78
v_mfma_f32_16x16x32_bf16 v[136:139],v[24:27],v[204:207],0
s_barrier
R 52,3,0
R 56,3,16
M 136,v[24:27],220
R 60,3,32
v_lshlrev_b32 v106,16,v79
v_mfma_f32_16x16x32_bf16 v[140:143],v[26:29],v[208:211],0
v_and_b32 v107,0xffff0000,v79
v_lshlrev_b32 v108,16,v80
M 140,v[26:29],224
v_and_b32 v109,0xffff0000,v80
v_lshlrev_b32 v110,16,v81
v_mfma_f32_16x16x32_bf16 v[144:147],v[24:27],v[208:211],0
v_and_b32 v111,0xffff0000,v81
v_lshlrev_b32 v112,16,v82
M 144,v[24:27],224
v_and_b32 v113,0xffff0000,v82
v_lshlrev_b32 v114,16,v83
v_and_b32 v115,0xffff0000,v83
L 0
v_pk_add_f32 v[52:53],v[52:53],v[54:55]
v_pk_add_f32 v[56:57],v[56:57],v[58:59]
v_pk_add_f32 v[60:61],v[60:61],v[62:63]
v_add_f32 v52,v52,v53
v_pk_add_f32 v[56:57],v[56:57],v[60:61]
v_mul_f32 v52,s68,v52
v_add_f32 v53,v56,v57
v_mul_f32 v53,s68,v53
v_fma_f32 v53,-v52,v52,v53
v_max_f32 v53,0,v53
buffer_load_dword v18,v6,s[36:39],s62 offen offset:0 sc1
v_add_f32 v53,s69,v53
v_rsq_f32 v48,v53
s_nop 0
v_mul_f32_e64 v50,-v52,v48
v_mov_b32 v49,v48
v_mov_b32 v51,v50
v_pk_fma_f32 v[52:53],v[116:117],v[48:49],v[50:51]
v_pk_fma_f32 v[54:55],v[118:119],v[48:49],v[50:51]
v_pk_fma_f32 v[56:57],v[120:121],v[48:49],v[50:51]
v_pk_fma_f32 v[58:59],v[122:123],v[48:49],v[50:51]
v_pk_fma_f32 v[116:117],v[52:53],v[84:85],v[116:117]
v_pk_fma_f32 v[118:119],v[54:55],v[86:87],v[118:119]
v_pk_fma_f32 v[120:121],v[56:57],v[88:89],v[120:121]
v_pk_fma_f32 v[122:123],v[58:59],v[90:91],v[122:123]
v_pk_fma_f32 v[52:53],v[124:125],v[48:49],v[50:51]
v_pk_fma_f32 v[54:55],v[126:127],v[48:49],v[50:51]
v_pk_fma_f32 v[56:57],v[128:129],v[48:49],v[50:51]
v_pk_fma_f32 v[58:59],v[130:131],v[48:49],v[50:51]
v_pk_fma_f32 v[124:125],v[52:53],v[92:93],v[124:125]
v_pk_fma_f32 v[126:127],v[54:55],v[94:95],v[126:127]
v_pk_fma_f32 v[128:129],v[56:57],v[96:97],v[128:129]
v_pk_fma_f32 v[130:131],v[58:59],v[98:99],v[130:131]
v_pk_fma_f32 v[52:53],v[132:133],v[48:49],v[50:51]
v_pk_fma_f32 v[54:55],v[134:135],v[48:49],v[50:51]
v_pk_fma_f32 v[56:57],v[136:137],v[48:49],v[50:51]
v_pk_fma_f32 v[58:59],v[138:139],v[48:49],v[50:51]
v_pk_fma_f32 v[132:133],v[52:53],v[100:101],v[132:133]
v_pk_fma_f32 v[134:135],v[54:55],v[102:103],v[134:135]
v_pk_fma_f32 v[136:137],v[56:57],v[104:105],v[136:137]
v_pk_fma_f32 v[138:139],v[58:59],v[106:107],v[138:139]
v_pk_fma_f32 v[52:53],v[140:141],v[48:49],v[50:51]
v_pk_fma_f32 v[54:55],v[142:143],v[48:49],v[50:51]
v_pk_fma_f32 v[56:57],v[144:145],v[48:49],v[50:51]
v_pk_fma_f32 v[58:59],v[146:147],v[48:49],v[50:51]
v_pk_fma_f32 v[140:141],v[52:53],v[108:109],v[140:141]
v_pk_fma_f32 v[142:143],v[54:55],v[110:111],v[142:143]
v_pk_fma_f32 v[144:145],v[56:57],v[112:113],v[144:145]
v_pk_fma_f32 v[146:147],v[58:59],v[114:115],v[146:147]
v_cvt_pk_bf16_f32 v148,v116,v117
v_cvt_pk_bf16_f32 v149,v118,v119
v_cvt_pk_bf16_f32 v150,v120,v121
v_cvt_pk_bf16_f32 v151,v122,v123
v_cvt_pk_bf16_f32 v152,v124,v125
v_cvt_pk_bf16_f32 v153,v126,v127
s_waitcnt vmcnt(13)
buffer_store_dword v17,v5,s[36:39],s63 offen offset:0 sc0 sc1
s_mov_b32 s63,0x7fff0000
v_mfma_f32_16x16x32_bf16 v[36:39],v[32:35],v[148:151],0
v_cvt_pk_bf16_f32 v154,v128,v129
v_cvt_pk_bf16_f32 v155,v130,v131
v_mfma_f32_16x16x32_bf16 v[40:43],v[148:151],v[148:151],0
v_cvt_pk_bf16_f32 v156,v132,v133
v_cvt_pk_bf16_f32 v157,v134,v135
M 36,v[32:35],152
v_cvt_pk_bf16_f32 v158,v136,v137
v_cvt_pk_bf16_f32 v159,v138,v139
M 40,v[152:155],152
v_cvt_pk_bf16_f32 v160,v140,v141
v_cvt_pk_bf16_f32 v161,v142,v143
M 36,v[32:35],156
v_cvt_pk_bf16_f32 v162,v144,v145
v_cvt_pk_bf16_f32 v163,v146,v147
M 40,v[156:159],156
buffer_store_dwordx4 v[148:151],v1,s[36:39],s57 offen offset:0 sc0 sc1
M 36,v[32:35],160
buffer_store_dwordx4 v[152:155],v1,s[36:39],s57 offen offset:2048 sc0 sc1
M 40,v[160:163],160
buffer_store_dwordx4 v[156:159],v1,s[36:39],s58 offen offset:0 sc0 sc1
s_nop 4
v_add_f32_dpp v44,v36,v36 row_ror:8 row_mask:0xf bank_mask:0xf
s_nop 0
v_cndmask_b32 v45,v40,v41,s[70:71]
v_cndmask_b32 v46,v42,v43,s[70:71]
ds_write_b32 v2,v44 offset:1024
v_cndmask_b32 v45,v45,v46,s[72:73]
ds_write_b32 v0,v45 offset:1024
buffer_store_dwordx4 v[160:163],v1,s[36:39],s58 offen offset:2048 sc0 sc1
L 0
s_barrier
R 52,3,1024
R 56,3,1040
R 60,3,1056
L 0
v_pk_add_f32 v[52:53],v[52:53],v[54:55]
v_pk_add_f32 v[56:57],v[56:57],v[58:59]
v_pk_add_f32 v[60:61],v[60:61],v[62:63]
v_add_f32 v52,v52,v53
v_pk_add_f32 v[56:57],v[56:57],v[60:61]
v_mul_f32 v52,s68,v52
v_add_f32 v53,v56,v57
v_mul_f32 v53,s68,v53
v_fma_f32 v53,-v52,v52,v53
v_max_f32 v53,0,v53
v_add_f32 v54,s69,v53
v_fma_f32 v53,s69,v54,v53
v_rsq_f32 v55,v54
v_rsq_f32 v53,v53
v_mul_f32_e64 v54,-v52,v55
v_mul_f32 v53,s76,v53
buffer_store_dwordx4 v[52:55],v4,s[36:39],s59 offen offset:0 sc0 sc1
s_mov_b32 s63,s60
s_lshr_b32 s78,s45,1
s_and_b32 s79,s45,1
s_lshl_b32 s79,s79,7
s_lshl_b32 s57,s78,15
s_add_u32 s57,s57,s79
s_add_u32 s57,s57,12582912
s_add_u32 s58,s57,4096
s_lshl_b32 s59,s78,9
s_add_u32 s59,s59,s79
s_add_u32 s59,s59,107020288
s_lshl_b32 s60,s45,9
s_add_u32 s60,s60,s66
s_add_u32 s60,s60,131072
s_add_u32 s45,s45,s46
s_lshr_b32 s78,s45,1
s_and_b32 s79,s45,1
s_lshl_b32 s79,s79,7
s_lshl_b32 s55,s78,15
s_add_u32 s55,s55,s79
s_mul_i32 s56,s45,16384
s_add_u32 s56,s56,98566144
s_lshl_b32 s61,s78,11
s_add_u32 s61,s61,110587904
s_add_u32 s78,s45,s46
s_lshr_b32 s78,s78,1
s_lshl_b32 s62,s78,11
s_add_u32 s62,s62,110587904
s_waitcnt vmcnt(6)
s_branch Lr_it0_%=
Lr_tb1_%=:
v_add_f32 v53,s69,v53
v_rsq_f32 v48,v53
s_nop 0
v_mul_f32_e64 v50,-v52,v48
v_mov_b32 v49,v48
v_mov_b32 v51,v50
v_pk_fma_f32 v[52:53],v[116:117],v[48:49],v[50:51]
v_pk_fma_f32 v[54:55],v[118:119],v[48:49],v[50:51]
v_pk_fma_f32 v[56:57],v[120:121],v[48:49],v[50:51]
v_pk_fma_f32 v[58:59],v[122:123],v[48:49],v[50:51]
s_add_u32 s78,s55,71303168
buffer_load_dwordx4 v[164:167],v1,s[36:39],s78 offen offset:0 sc1
buffer_load_dwordx4 v[168:171],v1,s[36:39],s78 offen offset:2048 sc1
s_add_u32 s78,s55,71307264
buffer_load_dwordx4 v[172:175],v1,s[36:39],s78 offen offset:0 sc1
buffer_load_dwordx4 v[176:179],v1,s[36:39],s78 offen offset:2048 sc1
v_pk_fma_f32 v[116:117],v[52:53],v[84:85],v[116:117]
v_pk_fma_f32 v[118:119],v[54:55],v[86:87],v[118:119]
v_pk_fma_f32 v[120:121],v[56:57],v[88:89],v[120:121]
v_pk_fma_f32 v[122:123],v[58:59],v[90:91],v[122:123]
v_pk_fma_f32 v[52:53],v[124:125],v[48:49],v[50:51]
v_pk_fma_f32 v[54:55],v[126:127],v[48:49],v[50:51]
v_pk_fma_f32 v[56:57],v[128:129],v[48:49],v[50:51]
v_pk_fma_f32 v[58:59],v[130:131],v[48:49],v[50:51]
s_add_u32 s78,s55,89128960
buffer_load_dwordx4 v[180:183],v1,s[36:39],s78 offen offset:0 sc1
buffer_load_dwordx4 v[184:187],v1,s[36:39],s78 offen offset:2048 sc1
s_add_u32 s78,s55,89133056
buffer_load_dwordx4 v[188:191],v1,s[36:39],s78 offen offset:0 sc1
buffer_load_dwordx4 v[192:195],v1,s[36:39],s78 offen offset:2048 sc1
v_pk_fma_f32 v[124:125],v[52:53],v[92:93],v[124:125]
v_pk_fma_f32 v[126:127],v[54:55],v[94:95],v[126:127]
v_pk_fma_f32 v[128:129],v[56:57],v[96:97],v[128:129]
v_pk_fma_f32 v[130:131],v[58:59],v[98:99],v[130:131]
v_pk_fma_f32 v[52:53],v[132:133],v[48:49],v[50:51]
v_pk_fma_f32 v[54:55],v[134:135],v[48:49],v[50:51]
v_pk_fma_f32 v[56:57],v[136:137],v[48:49],v[50:51]
v_pk_fma_f32 v[58:59],v[138:139],v[48:49],v[50:51]
v_pk_fma_f32 v[132:133],v[52:53],v[100:101],v[132:133]
v_pk_fma_f32 v[134:135],v[54:55],v[102:103],v[134:135]
v_pk_fma_f32 v[136:137],v[56:57],v[104:105],v[136:137]
v_pk_fma_f32 v[138:139],v[58:59],v[106:107],v[138:139]
v_pk_fma_f32 v[52:53],v[140:141],v[48:49],v[50:51]
v_pk_fma_f32 v[54:55],v[142:143],v[48:49],v[50:51]
v_pk_fma_f32 v[56:57],v[144:145],v[48:49],v[50:51]
v_pk_fma_f32 v[58:59],v[146:147],v[48:49],v[50:51]
v_pk_fma_f32 v[140:141],v[52:53],v[108:109],v[140:141]
v_pk_fma_f32 v[142:143],v[54:55],v[110:111],v[142:143]
v_pk_fma_f32 v[144:145],v[56:57],v[112:113],v[144:145]
v_pk_fma_f32 v[146:147],v[58:59],v[114:115],v[146:147]
buffer_load_dword v18,v6,s[36:39],s62 offen offset:0 sc1
v_cvt_pk_bf16_f32 v148,v116,v117
v_cvt_pk_bf16_f32 v149,v118,v119
v_cvt_pk_bf16_f32 v150,v120,v121
v_cvt_pk_bf16_f32 v151,v122,v123
v_cvt_pk_bf16_f32 v152,v124,v125
v_cvt_pk_bf16_f32 v153,v126,v127
v_mfma_f32_16x16x32_bf16 v[36:39],v[32:35],v[148:151],0
v_cvt_pk_bf16_f32 v154,v128,v129
v_cvt_pk_bf16_f32 v155,v130,v131
v_mfma_f32_16x16x32_bf16 v[40:43],v[148:151],v[148:151],0
v_cvt_pk_bf16_f32 v156,v132,v133
v_cvt_pk_bf16_f32 v157,v134,v135
M 36,v[32:35],152
v_cvt_pk_bf16_f32 v158,v136,v137
v_cvt_pk_bf16_f32 v159,v138,v139
M 40,v[152:155],152
v_cvt_pk_bf16_f32 v160,v140,v141
v_cvt_pk_bf16_f32 v161,v142,v143
M 36,v[32:35],156
v_cvt_pk_bf16_f32 v162,v144,v145
v_cvt_pk_bf16_f32 v163,v146,v147
M 40,v[156:159],156
buffer_store_dwordx4 v[148:151],v1,s[36:39],s57 offen offset:0 sc0 sc1
M 36,v[32:35],160
buffer_store_dwordx4 v[152:155],v1,s[36:39],s57 offen offset:2048 sc0 sc1
M 40,v[160:163],160
buffer_store_dwordx4 v[156:159],v1,s[36:39],s58 offen offset:0 sc0 sc1
s_nop 4
v_add_f32_dpp v44,v36,v36 row_ror:8 row_mask:0xf bank_mask:0xf
s_nop 0
v_cndmask_b32 v45,v40,v41,s[70:71]
v_cndmask_b32 v46,v42,v43,s[70:71]
ds_write_b32 v2,v44 offset:1024
v_cndmask_b32 v45,v45,v46,s[72:73]
ds_write_b32 v0,v45 offset:1024
buffer_store_dwordx4 v[160:163],v1,s[36:39],s58 offen offset:2048 sc0 sc1
L 0
s_barrier
R 52,3,1024
R 56,3,1040
R 60,3,1056
L 0
v_pk_add_f32 v[52:53],v[52:53],v[54:55]
v_pk_add_f32 v[56:57],v[56:57],v[58:59]
v_pk_add_f32 v[60:61],v[60:61],v[62:63]
v_add_f32 v52,v52,v53
v_pk_add_f32 v[56:57],v[56:57],v[60:61]
v_mul_f32 v52,s68,v52
v_add_f32 v53,v56,v57
v_mul_f32 v53,s68,v53
v_fma_f32 v53,-v52,v52,v53
v_max_f32 v53,0,v53
v_add_f32 v54,s69,v53
v_fma_f32 v53,s69,v54,v53
v_rsq_f32 v55,v54
v_rsq_f32 v53,v53
v_mul_f32_e64 v54,-v52,v55
v_mul_f32 v53,s76,v53
buffer_store_dwordx4 v[52:55],v4,s[36:39],s59 offen offset:0 sc0 sc1
s_mov_b32 s63,s60
s_lshr_b32 s78,s45,1
s_and_b32 s79,s45,1
s_lshl_b32 s79,s79,7
s_lshl_b32 s57,s78,15
s_add_u32 s57,s57,s79
s_add_u32 s57,s57,12582912
s_add_u32 s58,s57,4096
s_lshl_b32 s59,s78,9
s_add_u32 s59,s59,s79
s_add_u32 s59,s59,107020288
s_lshl_b32 s60,s45,9
s_add_u32 s60,s60,s66
s_add_u32 s60,s60,131072
s_add_u32 s45,s45,s46
s_lshr_b32 s78,s45,1
s_and_b32 s79,s45,1
s_lshl_b32 s79,s79,7
s_lshl_b32 s55,s78,15
s_add_u32 s55,s55,s79
)"
R"(s_mul_i32 s56,s45,16384
s_add_u32 s56,s56,98566144
s_lshl_b32 s61,s78,11
s_add_u32 s61,s61,110587904
s_add_u32 s78,s45,s46
s_lshr_b32 s78,s78,1
s_lshl_b32 s62,s78,11
s_add_u32 s62,s62,110587904
s_waitcnt vmcnt(5)
s_branch Lr_it0_%=
Lr_tc1_%=:
s_add_u32 s78,s55,71303168
buffer_load_dwordx4 v[164:167],v1,s[36:39],s78 offen offset:0 sc1
buffer_load_dwordx4 v[168:171],v1,s[36:39],s78 offen offset:2048 sc1
s_add_u32 s78,s55,71307264
buffer_load_dwordx4 v[172:175],v1,s[36:39],s78 offen offset:0 sc1
buffer_load_dwordx4 v[176:179],v1,s[36:39],s78 offen offset:2048 sc1
s_add_u32 s78,s55,89128960
buffer_load_dwordx4 v[180:183],v1,s[36:39],s78 offen offset:0 sc1
buffer_load_dwordx4 v[184:187],v1,s[36:39],s78 offen offset:2048 sc1
s_add_u32 s78,s55,89133056
buffer_load_dwordx4 v[188:191],v1,s[36:39],s78 offen offset:0 sc1
buffer_load_dwordx4 v[192:195],v1,s[36:39],s78 offen offset:2048 sc1
buffer_load_dword v18,v6,s[36:39],s62 offen offset:0 sc1
s_waitcnt vmcnt(9)
buffer_store_dword v17,v5,s[36:39],s63 offen offset:0 sc0 sc1
s_mov_b32 s63,0x7fff0000
s_lshr_b32 s78,s45,1
s_and_b32 s79,s45,1
s_lshl_b32 s79,s79,7
s_lshl_b32 s57,s78,15
s_add_u32 s57,s57,s79
s_add_u32 s57,s57,12582912
s_add_u32 s58,s57,4096
s_lshl_b32 s59,s78,9
s_add_u32 s59,s59,s79
s_add_u32 s59,s59,107020288
s_lshl_b32 s60,s45,9
s_add_u32 s60,s60,s66
s_add_u32 s60,s60,131072
s_add_u32 s45,s45,s46
s_lshr_b32 s78,s45,1
s_and_b32 s79,s45,1
s_lshl_b32 s79,s79,7
s_lshl_b32 s55,s78,15
s_add_u32 s55,s55,s79
s_mul_i32 s56,s45,16384
s_add_u32 s56,s56,98566144
s_lshl_b32 s61,s78,11
s_add_u32 s61,s61,110587904
s_add_u32 s78,s45,s46
s_lshr_b32 s78,s78,1
s_lshl_b32 s62,s78,11
s_add_u32 s62,s62,110587904
s_waitcnt vmcnt(1)
s_branch Lr_it0_%=
Lr_done_%=:
s_waitcnt vmcnt(0) lgkmcnt(0)
s_barrier
s_branch Lr_end_%=
.p2align 8
Lr_tab_%=:
s_mov_b64 s[40:41],%24
s_mov_b32 s42,7340032
s_mov_b32 s43,0x00020000
s_add_u32 s83,s50,0
s_add_u32 m0,s54,40960
s_nop 0
buffer_load_dwordx4 v13,s[40:43],s83 offen offset:0  lds
s_add_u32 m0,s54,41984
s_nop 0
buffer_load_dwordx4 v14,s[40:43],s83 offen offset:0  lds
s_add_u32 m0,s54,49024
s_nop 0
buffer_load_dwordx4 v13,s[40:43],s83 offen offset:128  lds
s_add_u32 m0,s54,50048
s_nop 0
buffer_load_dwordx4 v14,s[40:43],s83 offen offset:128  lds
s_setpc_b64 s[88:89]
.p2align 8
s_mov_b64 s[40:41],%24
s_mov_b32 s42,7340032
s_mov_b32 s43,0x00020000
v_add_u32 v230,40960,v15
v_add_u32 v231,40960,v16
T a[0:1],230,0
T a[2:3],230,512
T a[4:5],231,0
T a[6:7],231,512
T a[8:9],230,4096
T a[10:11],230,4608
T a[12:13],231,4096
T a[14:15],231,4608
s_add_u32 s83,s50,131072
s_add_u32 m0,s54,57344
s_nop 0
buffer_load_dwordx4 v13,s[40:43],s83 offen offset:0  lds
s_add_u32 m0,s54,58368
s_nop 0
buffer_load_dwordx4 v14,s[40:43],s83 offen offset:0  lds
s_add_u32 m0,s54,65408
s_nop 0
buffer_load_dwordx4 v13,s[40:43],s83 offen offset:128  lds
s_add_u32 m0,s54,66432
s_nop 0
buffer_load_dwordx4 v14,s[40:43],s83 offen offset:128  lds
s_setpc_b64 s[88:89]
.p2align 8
s_mov_b64 s[40:41],%24
s_mov_b32 s42,7340032
s_mov_b32 s43,0x00020000
v_add_u32 v230,57344,v15
v_add_u32 v231,57344,v16
T a[16:17],230,0
T a[18:19],230,512
T a[20:21],231,0
T a[22:23],231,512
T a[24:25],230,4096
T a[26:27],230,4608
T a[28:29],231,4096
T a[30:31],231,4608
s_add_u32 s83,s50,262144
s_add_u32 m0,s54,73728
s_nop 0
buffer_load_dwordx4 v13,s[40:43],s83 offen offset:0  lds
s_add_u32 m0,s54,74752
s_nop 0
buffer_load_dwordx4 v14,s[40:43],s83 offen offset:0  lds
s_add_u32 m0,s54,81792
s_nop 0
buffer_load_dwordx4 v13,s[40:43],s83 offen offset:128  lds
s_add_u32 m0,s54,82816
s_nop 0
buffer_load_dwordx4 v14,s[40:43],s83 offen offset:128  lds
s_setpc_b64 s[88:89]
.p2align 8
s_mov_b64 s[40:41],%24
s_mov_b32 s42,7340032
s_mov_b32 s43,0x00020000
v_add_u32 v230,73728,v15
v_add_u32 v231,73728,v16
T a[32:33],230,0
T a[34:35],230,512
T a[36:37],231,0
T a[38:39],231,512
T a[40:41],230,4096
T a[42:43],230,4608
T a[44:45],231,4096
T a[46:47],231,4608
s_add_u32 s83,s50,393216
s_add_u32 m0,s54,90112
s_nop 0
buffer_load_dwordx4 v13,s[40:43],s83 offen offset:0  lds
s_add_u32 m0,s54,91136
s_nop 0
buffer_load_dwordx4 v14,s[40:43],s83 offen offset:0  lds
s_add_u32 m0,s54,98176
s_nop 0
buffer_load_dwordx4 v13,s[40:43],s83 offen offset:128  lds
s_add_u32 m0,s54,99200
s_nop 0
buffer_load_dwordx4 v14,s[40:43],s83 offen offset:128  lds
s_setpc_b64 s[88:89]
.p2align 8
s_mov_b64 s[40:41],%24
s_mov_b32 s42,7340032
s_mov_b32 s43,0x00020000
v_add_u32 v230,90112,v15
v_add_u32 v231,90112,v16
T a[48:49],230,0
T a[50:51],230,512
T a[52:53],231,0
T a[54:55],231,512
T a[56:57],230,4096
T a[58:59],230,4608
T a[60:61],231,4096
T a[62:63],231,4608
s_add_u32 s83,s50,524288
s_add_u32 m0,s54,106496
s_nop 0
buffer_load_dwordx4 v13,s[40:43],s83 offen offset:0  lds
s_add_u32 m0,s54,107520
s_nop 0
buffer_load_dwordx4 v14,s[40:43],s83 offen offset:0  lds
s_add_u32 m0,s54,114560
s_nop 0
buffer_load_dwordx4 v13,s[40:43],s83 offen offset:128  lds
s_add_u32 m0,s54,115584
s_nop 0
buffer_load_dwordx4 v14,s[40:43],s83 offen offset:128  lds
s_setpc_b64 s[88:89]
.p2align 8
s_mov_b64 s[40:41],%24
s_mov_b32 s42,7340032
s_mov_b32 s43,0x00020000
v_add_u32 v230,106496,v15
v_add_u32 v231,106496,v16
T a[64:65],230,0
T a[66:67],230,512
T a[68:69],231,0
T a[70:71],231,512
T a[72:73],230,4096
T a[74:75],230,4608
T a[76:77],231,4096
T a[78:79],231,4608
s_add_u32 s83,s50,655360
s_add_u32 m0,s54,122880
s_nop 0
buffer_load_dwordx4 v13,s[40:43],s83 offen offset:0  lds
s_add_u32 m0,s54,123904
s_nop 0
buffer_load_dwordx4 v14,s[40:43],s83 offen offset:0  lds
s_add_u32 m0,s54,130944
s_nop 0
buffer_load_dwordx4 v13,s[40:43],s83 offen offset:128  lds
s_add_u32 m0,s54,131968
s_nop 0
buffer_load_dwordx4 v14,s[40:43],s83 offen offset:128  lds
s_setpc_b64 s[88:89]
.p2align 8
s_mov_b64 s[40:41],%24
s_mov_b32 s42,7340032
s_mov_b32 s43,0x00020000
v_add_u32 v230,122880,v15
v_add_u32 v231,122880,v16
T a[80:81],230,0
T a[82:83],230,512
T a[84:85],231,0
T a[86:87],231,512
T a[88:89],230,4096
T a[90:91],230,4608
T a[92:93],231,4096
T a[94:95],231,4608
s_add_u32 s83,s50,786432
s_add_u32 m0,s54,139264
s_nop 0
buffer_load_dwordx4 v13,s[40:43],s83 offen offset:0  lds
s_add_u32 m0,s54,140288
s_nop 0
buffer_load_dwordx4 v14,s[40:43],s83 offen offset:0  lds
s_add_u32 m0,s54,147328
s_nop 0
buffer_load_dwordx4 v13,s[40:43],s83 offen offset:128  lds
s_add_u32 m0,s54,148352
s_nop 0
buffer_load_dwordx4 v14,s[40:43],s83 offen offset:128  lds
s_setpc_b64 s[88:89]
.p2align 8
s_mov_b64 s[40:41],%24
s_mov_b32 s42,7340032
s_mov_b32 s43,0x00020000
v_add_u32 v230,139264,v15
v_add_u32 v231,139264,v16
T a[96:97],230,0
T a[98:99],230,512
T a[100:101],231,0
T a[102:103],231,512
T a[104:105],230,4096
T a[106:107],230,4608
T a[108:109],231,4096
T a[110:111],231,4608
s_add_u32 s83,s50,917504
s_add_u32 m0,s54,40960
s_nop 0
buffer_load_dwordx4 v13,s[40:43],s83 offen offset:0  lds
s_add_u32 m0,s54,41984
s_nop 0
buffer_load_dwordx4 v14,s[40:43],s83 offen offset:0  lds
s_add_u32 m0,s54,49024
s_nop 0
buffer_load_dwordx4 v13,s[40:43],s83 offen offset:128  lds
s_add_u32 m0,s54,50048
s_nop 0
buffer_load_dwordx4 v14,s[40:43],s83 offen offset:128  lds
s_setpc_b64 s[88:89]
.p2align 8
s_mov_b64 s[40:41],%24
s_mov_b32 s42,7340032
s_mov_b32 s43,0x00020000
v_add_u32 v230,40960,v15
v_add_u32 v231,40960,v16
T a[112:113],230,0
T a[114:115],230,512
T a[116:117],231,0
T a[118:119],231,512
T a[120:121],230,4096
T a[122:123],230,4608
T a[124:125],231,4096
T a[126:127],231,4608
s_add_u32 s83,s50,1048576
s_add_u32 m0,s54,57344
s_nop 0
buffer_load_dwordx4 v13,s[40:43],s83 offen offset:0  lds
s_add_u32 m0,s54,58368
s_nop 0
buffer_load_dwordx4 v14,s[40:43],s83 offen offset:0  lds
s_add_u32 m0,s54,65408
s_nop 0
buffer_load_dwordx4 v13,s[40:43],s83 offen offset:128  lds
s_add_u32 m0,s54,66432
s_nop 0
buffer_load_dwordx4 v14,s[40:43],s83 offen offset:128  lds
s_setpc_b64 s[88:89]
.p2align 8
s_mov_b64 s[40:41],%24
s_mov_b32 s42,7340032
s_mov_b32 s43,0x00020000
v_add_u32 v230,57344,v15
v_add_u32 v231,57344,v16
T a[128:129],230,0
T a[130:131],230,512
T a[132:133],231,0
T a[134:135],231,512
T a[136:137],230,4096
T a[138:139],230,4608
T a[140:141],231,4096
T a[142:143],231,4608
s_add_u32 s83,s50,1179648
s_add_u32 m0,s54,73728
s_nop 0
buffer_load_dwordx4 v13,s[40:43],s83 offen offset:0  lds
s_add_u32 m0,s54,74752
s_nop 0
buffer_load_dwordx4 v14,s[40:43],s83 offen offset:0  lds
s_add_u32 m0,s54,81792
s_nop 0
buffer_load_dwordx4 v13,s[40:43],s83 offen offset:128  lds
s_add_u32 m0,s54,82816
s_nop 0
buffer_load_dwordx4 v14,s[40:43],s83 offen offset:128  lds
s_setpc_b64 s[88:89]
.p2align 8
s_mov_b64 s[40:41],%24
s_mov_b32 s42,7340032
s_mov_b32 s43,0x00020000
v_add_u32 v230,73728,v15
v_add_u32 v231,73728,v16
T a[144:145],230,0
T a[146:147],230,512
T a[148:149],231,0
T a[150:151],231,512
T a[152:153],230,4096
T a[154:155],230,4608
T a[156:157],231,4096
T a[158:159],231,4608
s_add_u32 s83,s50,1310720
s_add_u32 m0,s54,90112
s_nop 0
buffer_load_dwordx4 v13,s[40:43],s83 offen offset:0  lds
s_add_u32 m0,s54,91136
s_nop 0
buffer_load_dwordx4 v14,s[40:43],s83 offen offset:0  lds
s_add_u32 m0,s54,98176
s_nop 0
buffer_load_dwordx4 v13,s[40:43],s83 offen offset:128  lds
s_add_u32 m0,s54,99200
s_nop 0
buffer_load_dwordx4 v14,s[40:43],s83 offen offset:128  lds
s_setpc_b64 s[88:89]
.p2align 8
Lr_end_%=:
Lcode_r_e:
.purgem M
.purgem R
.purgem T
.purgem D
.purgem L
)"
  :: "s"(ws), "s"(__builtin_amdgcn_readfirstlane(sWave)), "s"(__builtin_amdgcn_readfirstlane(sG0)), "s"(__builtin_amdgcn_readfirstlane(sStep)), "s"(__builtin_amdgcn_readfirstlane(sWw)), "s"(__builtin_amdgcn_readfirstlane(sExp)), "v"(vRP), "v"(vRF), "v"(vRwr), "v"(vRrd), "v"(vRS), "v"(vFlag), "v"(vL4), "v"(vLY), "v"(vQ), "v"(vY0), "v"(vTr), "v"(vSa), "v"(vSb), "v"(vWd0), "v"(vWd1), "v"(vWr0), "v"(vWr1), "s"(q), "s"(w4) : "v0", "v1", "v2", "v3", "v4", "v5", "v6", "v7", "v8", "v9", "v10", "v11", "v12", "v13", "v14", "v15", "v16", "v17", "v18", "v19", "v20", "v21", "v22", "v23", "v24", "v25", "v26", "v27", "v28", "v29", "v30", "v31", "v32", "v33", "v34", "v35", "v36", "v37", "v38", "v39", "v40", "v41", "v42", "v43", "v44", "v45", "v46", "v47", "v48", "v49", "v50", "v51", "v52", "v53", "v54", "v55", "v56", "v57", "v58", "v59", "v60", "v61", "v62", "v63", "v64", "v65", "v66", "v67", "v68", "v69", "v70", "v71", "v72", "v73", "v74", "v75", "v76", "v77", "v78", "v79", "v80", "v81", "v82", "v83", "v84", "v85", "v86", "v87", "v88", "v89", "v90", "v91", "v92", "v93", "v94", "v95", "v96", "v97", "v98", "v99", "v100", "v101", "v102", "v103", "v104", "v105", "v106", "v107", "v108", "v109", "v110", "v111", "v112", "v113", "v114", "v115", "v116", "v117", "v118", "v119", "v120", "v121", "v122", "v123", "v124", "v125", "v126", "v127", "v128", "v129", "v130", "v131", "v132", "v133", "v134", "v135", "v136", "v137", "v138", "v139", "v140", "v141", "v142", "v143", "v144", "v145", "v146", "v147", "v148", "v149", "v150", "v151", "v152", "v153", "v154", "v155", "v156", "v157", "v158", "v159", "v160", "v161", "v162", "v163", "v164", "v165", "v166", "v167", "v168", "v169", "v170", "v171", "v172", "v173", "v174", "v175", "v176", "v177", "v178", "v179", "v180", "v181", "v182", "v183", "v184", "v185", "v186", "v187", "v188", "v189", "v190", "v191", "v192", "v193", "v194", "v195", "v196", "v197", "v198", "v199", "v200", "v201", "v202", "v203", "v204", "v205", "v206", "v207", "v208", "v209", "v210", "v211", "v212", "v213", "v214", "v215", "v216", "v217", "v218", "v219", "v220", "v221", "v222", "v223", "v224", "v225", "v226", "v227", "v228", "v229", "v230", "v231", "a0", "a1", "a2", "a3", "a4", "a5", "a6", "a7", "a8", "a9", "a10", "a11", "a12", "a13", "a14", "a15", "a16", "a17", "a18", "a19", "a20", "a21", "a22", "a23", "a24", "a25", "a26", "a27", "a28", "a29", "a30", "a31", "a32", "a33", "a34", "a35", "a36", "a37", "a38", "a39", "a40", "a41", "a42", "a43", "a44", "a45", "a46", "a47", "a48", "a49", "a50", "a51", "a52", "a53", "a54", "a55", "a56", "a57", "a58", "a59", "a60", "a61", "a62", "a63", "a64", "a65", "a66", "a67", "a68", "a69", "a70", "a71", "a72", "a73", "a74", "a75", "a76", "a77", "a78", "a79", "a80", "a81", "a82", "a83", "a84", "a85", "a86", "a87", "a88", "a89", "a90", "a91", "a92", "a93", "a94", "a95", "a96", "a97", "a98", "a99", "a100", "a101", "a102", "a103", "a104", "a105", "a106", "a107", "a108", "a109", "a110", "a111", "a112", "a113", "a114", "a115", "a116", "a117", "a118", "a119", "a120", "a121", "a122", "a123", "a124", "a125", "a126", "a127", "a128", "a129", "a130", "a131", "a132", "a133", "a134", "a135", "a136", "a137", "a138", "a139", "a140", "a141", "a142", "a143", "a144", "a145", "a146", "a147", "a148", "a149", "a150", "a151", "a152", "a153", "a154", "a155", "a156", "a157", "a158", "a159", "a160", "a161", "a162", "a163", "a164", "a165", "a166", "a167", "a168", "a169", "a170", "a171", "a172", "a173", "a174", "a175", "a176", "a177", "a178", "a179", "a180", "a181", "a182", "a183", "a184", "a185", "a186", "a187", "a188", "a189", "a190", "a191", "a192", "a193", "a194", "a195", "a196", "a197", "a198", "a199", "a200", "a201", "a202", "a203", "a204", "a205", "a206", "a207", "a208", "a209", "a210", "a211", "a212", "a213", "a214", "a215", "a216", "a217", "a218", "a219", "a220", "a221", "a222", "a223", "a224", "a225", "a226", "a227", "a228", "a229", "a230", "a231", "a232", "a233", "a234", "a235", "a236", "a237", "a238", "a239", "a240", "a241", "a242", "a243", "a244", "a245", "a246", "a247", "a248", "a249", "a250", "a251", "a252", "a253", "a254", "a255", "s36", "s37", "s38", "s39", "s40", "s41", "s42", "s43", "s44", "s45", "s46", "s47", "s48", "s49", "s50", "s51", "s52", "s53", "s54", "s55", "s56", "s57", "s58", "s59", "s60", "s61", "s62", "s63", "s64", "s65", "s66", "s67", "s68", "s69", "s70", "s71", "s72", "s73", "s74", "s75", "s76", "s77", "s78", "s79", "s80", "s81", "s82", "s83", "s84", "s85", "s86", "s87", "s88", "s89", "s90", "s91", "s92", "s93", "s94", "s95", "s96", "s97", "vcc", "scc", "memory");
    if (RHELP && slice < 16) { kind = 4; rep = 5; kh = slice >> 3; slice &= 7; }
  }
  CTS(9);
#define COMMON(NF, FOFF, FSTR, SNAP, LDW, NL, TOFF)                                                                                            \
  LANE_ID(ln);                                                                                                                       \
  const int l15 = ln & 15, lg = ln >> 4, lq = l15 >> 2, lp = ln & 3, rr = 8 * lg + lq, fj = ln & 31;                                 \
  const unsigned vL16 = ln * 16, vFlag = (ln < 32 && wave == 0) ? ln * 4 : OOB;                                                      \
  const unsigned vSnapOff = (wave == 0 && fj < (NF)) ? (FOFF) + (ln >> 5) * sStep * (FSTR) : OOB;                                    \
  const unsigned vSnapRd = (SNAP) + (fj < (NF) ? ln : ln & 32) * 4;                                                                  \
  const unsigned vWd0 = (ln >> 3) * (LDW) + ((ln & 7) ^ swzB(ln >> 3)) * 16, vWd1 = ((ln >> 3) + 8) * (LDW) + ((ln & 7) ^ swzB(8 + (ln >> 3))) * 16; \
  const unsigned vWr0 = (wave >> 1) * 8192 + rr * 128 + (((wave & 1) * 4 + lp) ^ swzB(rr)) * 16, vWr1 = vWr0 + 8;                    \
  const unsigned vWr2 = 16384 + rr * 128 + ((2 * wave + (lp >> 1)) ^ swzB(rr)) * 16 + (lp & 1) * 8;                                  \
  const unsigned sWave = wave, sRk = kind == 4 ? rep : t_start, sG0 = rep < 5 ? rep : rep == 5 ? U0 + 5 : U1 + rep, sDbg = sDbg0 + (rep < 5 ? 0 : 2048)
  if (kind == 3 || kind == 5) {
    const unsigned sStep = kind == 3 ? 5 : G3OR;
    COMMON(8, fj * 128, 1024, 131072, 7168, 256, 0);
    const unsigned vOutS = (lg & 1) * 8 + (((wave & 1) * 2 + (lg >> 1)) * 16 + l15) * 16 + (4 + (wave >> 1) - wave) * 1024;
    const unsigned vAux0 = (ln & 7) * 128 + (4 * wave + lg) * 8 + ((ln >> 3) & 1) * 4;
    const unsigned vAux1 = 139264 + (l15 < 2 ? (4 * wave + lg) * 8 + l15 * 4 : 512 + ln * 4), vAux2 = 139264 + l15 * 8;
    const unsigned vAux3 = 0, vAn = l15 == 0 ? 0x3f800000u : 0u, vAe = l15 == 1 ? GELU_CB : 0u;
    const unsigned sOutOff = OFF_G + (slice * 6 + wave) * 1024, sFoutOff = OFF_FG + slice * 128, sAuxOff = 0, sWw = 16 * wave * 7168 + slice * 384, sBuf = 0;
    const unsigned sExp = __builtin_amdgcn_readfirstlane(ep);
    if (kind == 3) { asm volatile(
R"(.macro M d,w,x
v_mfma_f32_16x16x32_bf16 v[\d:\d+3], \w, v[\x:\x+3], v[\d:\d+3]
.endm
.macro R d,a,o
ds_read_b128 v[\d:\d+3], v\a offset:\o
.endm
.macro T d,a,o
ds_read_b64_tr_b16 \d, v\a offset:\o
.endm
.macro D a,s,o
buffer_load_dwordx4 v\a, s[36:39], s\s offen offset:\o sc1 lds
.endm
.macro L c
s_waitcnt lgkmcnt(\c)
.endm
Lcode_g3_b:
s_mov_b64 s[36:37],%0
s_mov_b32 s38,111243392
s_mov_b32 s39,0x00020000
s_mov_b32 s44,%1
s_mov_b32 s45,%2
s_mov_b32 s46,%3
s_mov_b32 s47,%4
s_mov_b32 s48,%5
s_mov_b32 s49,%6
s_mov_b32 s50,%7
s_mov_b32 s51,%8
s_mov_b32 s53,%9
v_mov_b32 v0,%10
v_mov_b32 v3,%11
v_mov_b32 v5,%12
v_mov_b32 v6,%13
v_mov_b32 v18,%14
v_mov_b32 v19,%15
v_mov_b32 v20,%16
v_mov_b32 v21,%17
v_mov_b32 v22,%18
v_mov_b32 v10,%19
v_mov_b32 v11,%20
v_mov_b32 v12,%21
v_mov_b32 v13,%22
v_mov_b32 v14,%23
v_mov_b32 v15,%24
v_mov_b32 v16,%25
s_mov_b64 s[40:41],%26
s_mov_b32 s42,7340032
s_mov_b32 s43,0x00020000
s_lshl_b32 s57,s44,11
s_cmp_ge_u32 s45,8
s_cbranch_scc1 Lg3_dly_%=
s_sleep 16
Lg3_dly_%=:
s_add_u32 s55,s50,0
s_add_u32 m0,s57,0
s_nop 0
buffer_load_dwordx4 v18,s[40:43],s55 offen offset:0  lds
s_add_u32 m0,s57,1024
s_nop 0
buffer_load_dwordx4 v19,s[40:43],s55 offen offset:0  lds
s_add_u32 m0,s57,8064
s_nop 0
buffer_load_dwordx4 v18,s[40:43],s55 offen offset:128  lds
s_add_u32 m0,s57,9088
s_nop 0
buffer_load_dwordx4 v19,s[40:43],s55 offen offset:128  lds
s_add_u32 m0,s57,16128
s_nop 0
buffer_load_dwordx4 v18,s[40:43],s55 offen offset:256  lds
s_add_u32 m0,s57,17152
s_nop 0
buffer_load_dwordx4 v19,s[40:43],s55 offen offset:256  lds
s_add_u32 s55,s50,458752
s_add_u32 m0,s57,24576
s_nop 0
buffer_load_dwordx4 v18,s[40:43],s55 offen offset:0  lds
s_add_u32 m0,s57,25600
s_nop 0
buffer_load_dwordx4 v19,s[40:43],s55 offen offset:0  lds
s_add_u32 m0,s57,32640
s_nop 0
buffer_load_dwordx4 v18,s[40:43],s55 offen offset:128  lds
s_add_u32 m0,s57,33664
s_nop 0
buffer_load_dwordx4 v19,s[40:43],s55 offen offset:128  lds
s_add_u32 m0,s57,40704
s_nop 0
buffer_load_dwordx4 v18,s[40:43],s55 offen offset:256  lds
s_add_u32 m0,s57,41728
s_nop 0
buffer_load_dwordx4 v19,s[40:43],s55 offen offset:256  lds
s_add_u32 s55,s50,917504
s_add_u32 m0,s57,49152
s_nop 0
buffer_load_dwordx4 v18,s[40:43],s55 offen offset:0  lds
s_add_u32 m0,s57,50176
s_nop 0
buffer_load_dwordx4 v19,s[40:43],s55 offen offset:0  lds
s_add_u32 m0,s57,57216
s_nop 0
buffer_load_dwordx4 v18,s[40:43],s55 offen offset:128  lds
s_add_u32 m0,s57,58240
s_nop 0
buffer_load_dwordx4 v19,s[40:43],s55 offen offset:128  lds
s_add_u32 m0,s57,65280
s_nop 0
buffer_load_dwordx4 v18,s[40:43],s55 offen offset:256  lds
s_add_u32 m0,s57,66304
s_nop 0
buffer_load_dwordx4 v19,s[40:43],s55 offen offset:256  lds
s_add_u32 s55,s50,1376256
s_add_u32 m0,s57,73728
s_nop 0
buffer_load_dwordx4 v18,s[40:43],s55 offen offset:0  lds
s_add_u32 m0,s57,74752
s_nop 0
buffer_load_dwordx4 v19,s[40:43],s55 offen offset:0  lds
s_add_u32 m0,s57,81792
s_nop 0
buffer_load_dwordx4 v18,s[40:43],s55 offen offset:128  lds
s_add_u32 m0,s57,82816
s_nop 0
buffer_load_dwordx4 v19,s[40:43],s55 offen offset:128  lds
s_add_u32 m0,s57,89856
s_nop 0
buffer_load_dwordx4 v18,s[40:43],s55 offen offset:256  lds
s_add_u32 m0,s57,90880
s_nop 0
buffer_load_dwordx4 v19,s[40:43],s55 offen offset:256  lds
s_add_u32 s55,s50,1835008
s_add_u32 m0,s57,98304
s_nop 0
buffer_load_dwordx4 v18,s[40:43],s55 offen offset:0  lds
s_add_u32 m0,s57,99328
s_nop 0
buffer_load_dwordx4 v19,s[40:43],s55 offen offset:0  lds
s_add_u32 m0,s57,106368
s_nop 0
buffer_load_dwordx4 v18,s[40:43],s55 offen offset:128  lds
s_add_u32 m0,s57,107392
s_nop 0
buffer_load_dwordx4 v19,s[40:43],s55 offen offset:128  lds
s_add_u32 m0,s57,114432
s_nop 0
buffer_load_dwordx4 v18,s[40:43],s55 offen offset:256  lds
s_add_u32 m0,s57,115456
s_nop 0
buffer_load_dwordx4 v19,s[40:43],s55 offen offset:256  lds
s_waitcnt vmcnt(24) lgkmcnt(0)
s_barrier
s_add_u32 s55,s50,2293760
s_add_u32 m0,s57,122880
s_nop 0
buffer_load_dwordx4 v18,s[40:43],s55 offen offset:0  lds
s_add_u32 m0,s57,123904
s_nop 0
buffer_load_dwordx4 v19,s[40:43],s55 offen offset:0  lds
s_add_u32 m0,s57,130944
s_nop 0
buffer_load_dwordx4 v18,s[40:43],s55 offen offset:128  lds
s_add_u32 m0,s57,131968
s_nop 0
buffer_load_dwordx4 v19,s[40:43],s55 offen offset:128  lds
s_add_u32 m0,s57,139008
s_nop 0
buffer_load_dwordx4 v18,s[40:43],s55 offen offset:256  lds
s_add_u32 m0,s57,140032
s_nop 0
buffer_load_dwordx4 v19,s[40:43],s55 offen offset:256  lds
v_add_u32 v26,0,v20
v_add_u32 v27,0,v21
v_add_u32 v28,0,v22
T a[0:1],26,0
T a[2:3],26,512
T a[4:5],27,0
T a[6:7],27,512
T a[8:9],28,0
T a[10:11],28,512
T a[12:13],26,4096
T a[14:15],26,4608
T a[16:17],27,4096
T a[18:19],27,4608
T a[20:21],28,4096
T a[22:23],28,4608
s_waitcnt vmcnt(24) lgkmcnt(0)
s_barrier
s_add_u32 s55,s50,2752512
s_add_u32 m0,s57,0
s_nop 0
buffer_load_dwordx4 v18,s[40:43],s55 offen offset:0  lds
s_add_u32 m0,s57,1024
s_nop 0
buffer_load_dwordx4 v19,s[40:43],s55 offen offset:0  lds
s_add_u32 m0,s57,8064
s_nop 0
buffer_load_dwordx4 v18,s[40:43],s55 offen offset:128  lds
s_add_u32 m0,s57,9088
s_nop 0
buffer_load_dwordx4 v19,s[40:43],s55 offen offset:128  lds
s_add_u32 m0,s57,16128
s_nop 0
buffer_load_dwordx4 v18,s[40:43],s55 offen offset:256  lds
s_add_u32 m0,s57,17152
s_nop 0
buffer_load_dwordx4 v19,s[40:43],s55 offen offset:256  lds
v_add_u32 v26,24576,v20
v_add_u32 v27,24576,v21
v_add_u32 v28,24576,v22
T a[24:25],26,0
T a[26:27],26,512
T a[28:29],27,0
T a[30:31],27,512
T a[32:33],28,0
T a[34:35],28,512
T a[36:37],26,4096
T a[38:39],26,4608
T a[40:41],27,4096
T a[42:43],27,4608
T a[44:45],28,4096
T a[46:47],28,4608
s_waitcnt vmcnt(24) lgkmcnt(0)
s_barrier
s_add_u32 s55,s50,3211264
s_add_u32 m0,s57,24576
s_nop 0
buffer_load_dwordx4 v18,s[40:43],s55 offen offset:0  lds
s_add_u32 m0,s57,25600
s_nop 0
buffer_load_dwordx4 v19,s[40:43],s55 offen offset:0  lds
s_add_u32 m0,s57,32640
s_nop 0
buffer_load_dwordx4 v18,s[40:43],s55 offen offset:128  lds
s_add_u32 m0,s57,33664
s_nop 0
buffer_load_dwordx4 v19,s[40:43],s55 offen offset:128  lds
s_add_u32 m0,s57,40704
s_nop 0
buffer_load_dwordx4 v18,s[40:43],s55 offen offset:256  lds
s_add_u32 m0,s57,41728
s_nop 0
buffer_load_dwordx4 v19,s[40:43],s55 offen offset:256  lds
v_add_u32 v26,49152,v20
v_add_u32 v27,49152,v21
v_add_u32 v28,49152,v22
T a[48:49],26,0
T a[50:51],26,512
T a[52:53],27,0
T a[54:55],27,512
T a[56:57],28,0
T a[58:59],28,512
T a[60:61],26,4096
T a[62:63],26,4608
T a[64:65],27,4096
T a[66:67],27,4608
T a[68:69],28,4096
T a[70:71],28,4608
s_waitcnt vmcnt(24) lgkmcnt(0)
s_barrier
s_add_u32 s55,s50,3670016
s_add_u32 m0,s57,49152
s_nop 0
buffer_load_dwordx4 v18,s[40:43],s55 offen offset:0  lds
s_add_u32 m0,s57,50176
s_nop 0
buffer_load_dwordx4 v19,s[40:43],s55 offen offset:0  lds
s_add_u32 m0,s57,57216
s_nop 0
buffer_load_dwordx4 v18,s[40:43],s55 offen offset:128  lds
s_add_u32 m0,s57,58240
s_nop 0
buffer_load_dwordx4 v19,s[40:43],s55 offen offset:128  lds
s_add_u32 m0,s57,65280
s_nop 0
buffer_load_dwordx4 v18,s[40:43],s55 offen offset:256  lds
s_add_u32 m0,s57,66304
s_nop 0
buffer_load_dwordx4 v19,s[40:43],s55 offen offset:256  lds
v_add_u32 v26,73728,v20
v_add_u32 v27,73728,v21
v_add_u32 v28,73728,v22
T a[72:73],26,0
T a[74:75],26,512
T a[76:77],27,0
T a[78:79],27,512
T a[80:81],28,0
T a[82:83],28,512
T a[84:85],26,4096
T a[86:87],26,4608
T a[88:89],27,4096
T a[90:91],27,4608
T a[92:93],28,4096
T a[94:95],28,4608
s_waitcnt vmcnt(24) lgkmcnt(0)
s_barrier
s_add_u32 s55,s50,4128768
s_add_u32 m0,s57,73728
s_nop 0
buffer_load_dwordx4 v18,s[40:43],s55 offen offset:0  lds
s_add_u32 m0,s57,74752
s_nop 0
buffer_load_dwordx4 v19,s[40:43],s55 offen offset:0  lds
s_add_u32 m0,s57,81792
s_nop 0
buffer_load_dwordx4 v18,s[40:43],s55 offen offset:128  lds
s_add_u32 m0,s57,82816
s_nop 0
buffer_load_dwordx4 v19,s[40:43],s55 offen offset:128  lds
s_add_u32 m0,s57,89856
s_nop 0
buffer_load_dwordx4 v18,s[40:43],s55 offen offset:256  lds
s_add_u32 m0,s57,90880
s_nop 0
buffer_load_dwordx4 v19,s[40:43],s55 offen offset:256  lds
v_add_u32 v26,98304,v20
v_add_u32 v27,98304,v21
v_add_u32 v28,98304,v22
T a[96:97],26,0
T a[98:99],26,512
T a[100:101],27,0
T a[102:103],27,512
T a[104:105],28,0
T a[106:107],28,512
T a[108:109],26,4096
T a[110:111],26,4608
T a[112:113],27,4096
T a[114:115],27,4608
T a[116:117],28,4096
T a[118:119],28,4608
s_waitcnt vmcnt(24) lgkmcnt(0)
s_barrier
s_add_u32 s55,s50,4587520
s_add_u32 m0,s57,98304
s_nop 0
buffer_load_dwordx4 v18,s[40:43],s55 offen offset:0  lds
s_add_u32 m0,s57,99328
s_nop 0
buffer_load_dwordx4 v19,s[40:43],s55 offen offset:0  lds
s_add_u32 m0,s57,106368
s_nop 0
buffer_load_dwordx4 v18,s[40:43],s55 offen offset:128  lds
s_add_u32 m0,s57,107392
s_nop 0
buffer_load_dwordx4 v19,s[40:43],s55 offen offset:128  lds
s_add_u32 m0,s57,114432
s_nop 0
buffer_load_dwordx4 v18,s[40:43],s55 offen offset:256  lds
s_add_u32 m0,s57,115456
s_nop 0
buffer_load_dwordx4 v19,s[40:43],s55 offen offset:256  lds
v_add_u32 v26,122880,v20
v_add_u32 v27,122880,v21
v_add_u32 v28,122880,v22
T a[120:121],26,0
T a[122:123],26,512
T a[124:125],27,0
T a[126:127],27,512
T a[128:129],28,0
T a[130:131],28,512
T a[132:133],26,4096
T a[134:135],26,4608
T a[136:137],27,4096
T a[138:139],27,4608
T a[140:141],28,4096
T a[142:143],28,4608
s_waitcnt vmcnt(24) lgkmcnt(0)
s_barrier
s_add_u32 s55,s50,5046272
s_add_u32 m0,s57,122880
s_nop 0
buffer_load_dwordx4 v18,s[40:43],s55 offen offset:0  lds
s_add_u32 m0,s57,123904
s_nop 0
buffer_load_dwordx4 v19,s[40:43],s55 offen offset:0  lds
s_add_u32 m0,s57,130944
s_nop 0
buffer_load_dwordx4 v18,s[40:43],s55 offen offset:128  lds
s_add_u32 m0,s57,131968
s_nop 0
buffer_load_dwordx4 v19,s[40:43],s55 offen offset:128  lds
s_add_u32 m0,s57,139008
s_nop 0
buffer_load_dwordx4 v18,s[40:43],s55 offen offset:256  lds
s_add_u32 m0,s57,140032
s_nop 0
buffer_load_dwordx4 v19,s[40:43],s55 offen offset:256  lds
v_add_u32 v26,0,v20
v_add_u32 v27,0,v21
v_add_u32 v28,0,v22
T a[144:145],26,0
T a[146:147],26,512
T a[148:149],27,0
T a[150:151],27,512
T a[152:153],28,0
T a[154:155],28,512
T a[156:157],26,4096
T a[158:159],26,4608
T a[160:161],27,4096
T a[162:163],27,4608
T a[164:165],28,4096
T a[166:167],28,4608
s_waitcnt vmcnt(24) lgkmcnt(0)
s_barrier
s_add_u32 s55,s50,5505024
s_add_u32 m0,s57,0
s_nop 0
buffer_load_dwordx4 v18,s[40:43],s55 offen offset:0  lds
s_add_u32 m0,s57,1024
s_nop 0
buffer_load_dwordx4 v19,s[40:43],s55 offen offset:0  lds
s_add_u32 m0,s57,8064
s_nop 0
buffer_load_dwordx4 v18,s[40:43],s55 offen offset:128  lds
s_add_u32 m0,s57,9088
s_nop 0
buffer_load_dwordx4 v19,s[40:43],s55 offen offset:128  lds
s_add_u32 m0,s57,16128
s_nop 0
buffer_load_dwordx4 v18,s[40:43],s55 offen offset:256  lds
s_add_u32 m0,s57,17152
s_nop 0
buffer_load_dwordx4 v19,s[40:43],s55 offen offset:256  lds
v_add_u32 v26,24576,v20
v_add_u32 v27,24576,v21
v_add_u32 v28,24576,v22
T a[168:169],26,0
T a[170:171],26,512
T a[172:173],27,0
T a[174:175],27,512
T a[176:177],28,0
T a[178:179],28,512
T a[180:181],26,4096
T a[182:183],26,4608
T a[184:185],27,4096
T a[186:187],27,4608
T a[188:189],28,4096
T a[190:191],28,4608
s_waitcnt vmcnt(24) lgkmcnt(0)
s_barrier
s_add_u32 s55,s50,5963776
s_add_u32 m0,s57,24576
s_nop 0
buffer_load_dwordx4 v18,s[40:43],s55 offen offset:0  lds
s_add_u32 m0,s57,25600
s_nop 0
buffer_load_dwordx4 v19,s[40:43],s55 offen offset:0  lds
s_add_u32 m0,s57,32640
s_nop 0
buffer_load_dwordx4 v18,s[40:43],s55 offen offset:128  lds
s_add_u32 m0,s57,33664
s_nop 0
buffer_load_dwordx4 v19,s[40:43],s55 offen offset:128  lds
s_add_u32 m0,s57,40704
s_nop 0
buffer_load_dwordx4 v18,s[40:43],s55 offen offset:256  lds
s_add_u32 m0,s57,41728
s_nop 0
buffer_load_dwordx4 v19,s[40:43],s55 offen offset:256  lds
v_add_u32 v26,49152,v20
v_add_u32 v27,49152,v21
v_add_u32 v28,49152,v22
T a[192:193],26,0
T a[194:195],26,512
T a[196:197],27,0
T a[198:199],27,512
T a[200:201],28,0
T a[202:203],28,512
T a[204:205],26,4096
T a[206:207],26,4608
T a[208:209],27,4096
T a[210:211],27,4608
T a[212:213],28,4096
T a[214:215],28,4608
s_waitcnt vmcnt(24) lgkmcnt(0)
s_barrier
s_add_u32 s55,s50,6422528
s_add_u32 m0,s57,49152
s_nop 0
buffer_load_dwordx4 v18,s[40:43],s55 offen offset:0  lds
s_add_u32 m0,s57,50176
s_nop 0
buffer_load_dwordx4 v19,s[40:43],s55 offen offset:0  lds
s_add_u32 m0,s57,57216
s_nop 0
buffer_load_dwordx4 v18,s[40:43],s55 offen offset:128  lds
s_add_u32 m0,s57,58240
s_nop 0
buffer_load_dwordx4 v19,s[40:43],s55 offen offset:128  lds
s_add_u32 m0,s57,65280
s_nop 0
buffer_load_dwordx4 v18,s[40:43],s55 offen offset:256  lds
s_add_u32 m0,s57,66304
s_nop 0
buffer_load_dwordx4 v19,s[40:43],s55 offen offset:256  lds
v_add_u32 v26,73728,v20
v_add_u32 v27,73728,v21
v_add_u32 v28,73728,v22
T a[216:217],26,0
T a[218:219],26,512
T a[220:221],27,0
T a[222:223],27,512
T a[224:225],28,0
)"
R"(T a[226:227],28,512
T a[228:229],26,4096
T a[230:231],26,4608
T a[232:233],27,4096
T a[234:235],27,4608
T a[236:237],28,4096
T a[238:239],28,4608
s_waitcnt vmcnt(24) lgkmcnt(0)
s_barrier
s_add_u32 s55,s50,6881280
s_add_u32 m0,s57,73728
s_nop 0
buffer_load_dwordx4 v18,s[40:43],s55 offen offset:0  lds
s_add_u32 m0,s57,74752
s_nop 0
buffer_load_dwordx4 v19,s[40:43],s55 offen offset:0  lds
s_add_u32 m0,s57,81792
s_nop 0
buffer_load_dwordx4 v18,s[40:43],s55 offen offset:128  lds
s_add_u32 m0,s57,82816
s_nop 0
buffer_load_dwordx4 v19,s[40:43],s55 offen offset:128  lds
s_add_u32 m0,s57,89856
s_nop 0
buffer_load_dwordx4 v18,s[40:43],s55 offen offset:256  lds
s_add_u32 m0,s57,90880
s_nop 0
buffer_load_dwordx4 v19,s[40:43],s55 offen offset:256  lds
v_add_u32 v26,98304,v20
v_add_u32 v27,98304,v21
v_add_u32 v28,98304,v22
T a[240:241],26,0
T a[242:243],26,512
T a[244:245],27,0
T a[246:247],27,512
T a[248:249],28,0
T a[250:251],28,512
T a[252:253],26,4096
T a[254:255],26,4608
T v[128:129],27,4096
T v[130:131],27,4608
T v[132:133],28,4096
T v[134:135],28,4608
s_waitcnt vmcnt(24) lgkmcnt(0)
s_barrier
v_add_u32 v26,122880,v20
v_add_u32 v27,122880,v21
v_add_u32 v28,122880,v22
T v[136:137],26,0
T v[138:139],26,512
T v[140:141],27,0
T v[142:143],27,512
T v[144:145],28,0
T v[146:147],28,512
T v[148:149],26,4096
T v[150:151],26,4608
T v[152:153],27,4096
T v[154:155],27,4608
T v[156:157],28,4096
T v[158:159],28,4608
s_waitcnt vmcnt(18) lgkmcnt(0)
s_barrier
v_add_u32 v26,0,v20
v_add_u32 v27,0,v21
v_add_u32 v28,0,v22
T v[160:161],26,0
T v[162:163],26,512
T v[164:165],27,0
T v[166:167],27,512
T v[168:169],28,0
T v[170:171],28,512
T v[172:173],26,4096
T v[174:175],26,4608
T v[176:177],27,4096
T v[178:179],27,4608
T v[180:181],28,4096
T v[182:183],28,4608
s_waitcnt vmcnt(12) lgkmcnt(0)
s_barrier
v_add_u32 v26,24576,v20
v_add_u32 v27,24576,v21
v_add_u32 v28,24576,v22
T v[184:185],26,0
T v[186:187],26,512
T v[188:189],27,0
T v[190:191],27,512
T v[192:193],28,0
T v[194:195],28,512
T v[196:197],26,4096
T v[198:199],26,4608
T v[200:201],27,4096
T v[202:203],27,4608
T v[204:205],28,4096
T v[206:207],28,4608
s_waitcnt vmcnt(6) lgkmcnt(0)
s_barrier
v_add_u32 v26,49152,v20
v_add_u32 v27,49152,v21
v_add_u32 v28,49152,v22
T v[208:209],26,0
T v[210:211],26,512
T v[212:213],27,0
T v[214:215],27,512
T v[216:217],28,0
T v[218:219],28,512
T v[220:221],26,4096
T v[222:223],26,4608
T v[224:225],27,4096
T v[226:227],27,4608
T v[228:229],28,4096
T v[230:231],28,4608
s_waitcnt vmcnt(0) lgkmcnt(0)
s_barrier
v_add_u32 v26,73728,v20
v_add_u32 v27,73728,v21
v_add_u32 v28,73728,v22
T v[232:233],26,0
T v[234:235],26,512
T v[236:237],27,0
T v[238:239],27,512
T v[240:241],28,0
T v[242:243],28,512
T v[244:245],26,4096
T v[246:247],26,4608
T v[248:249],27,4096
T v[250:251],27,4608
T v[252:253],28,4096
T v[254:255],28,4608
L 0
s_barrier
v_mov_b32 v42,0x3f803f80
v_mov_b32 v43,0x3f803f80
v_mov_b32 v44,0x3f803f80
v_mov_b32 v45,0x3f803f80
s_nop 1
v_mfma_f32_16x16x32_bf16 v[82:85],a[0:3],v[42:45],0
v_mfma_f32_16x16x32_bf16 v[86:89],a[4:7],v[42:45],0
v_mfma_f32_16x16x32_bf16 v[90:93],a[8:11],v[42:45],0
M 82,a[12:15],42
M 86,a[16:19],42
M 90,a[20:23],42
M 82,a[24:27],42
M 86,a[28:31],42
M 90,a[32:35],42
M 82,a[36:39],42
M 86,a[40:43],42
M 90,a[44:47],42
M 82,a[48:51],42
M 86,a[52:55],42
M 90,a[56:59],42
M 82,a[60:63],42
M 86,a[64:67],42
M 90,a[68:71],42
M 82,a[72:75],42
M 86,a[76:79],42
M 90,a[80:83],42
M 82,a[84:87],42
M 86,a[88:91],42
M 90,a[92:95],42
M 82,a[96:99],42
M 86,a[100:103],42
M 90,a[104:107],42
M 82,a[108:111],42
M 86,a[112:115],42
M 90,a[116:119],42
M 82,a[120:123],42
M 86,a[124:127],42
M 90,a[128:131],42
M 82,a[132:135],42
M 86,a[136:139],42
M 90,a[140:143],42
M 82,a[144:147],42
M 86,a[148:151],42
M 90,a[152:155],42
M 82,a[156:159],42
M 86,a[160:163],42
M 90,a[164:167],42
M 82,a[168:171],42
M 86,a[172:175],42
M 90,a[176:179],42
M 82,a[180:183],42
M 86,a[184:187],42
M 90,a[188:191],42
M 82,a[192:195],42
M 86,a[196:199],42
M 90,a[200:203],42
M 82,a[204:207],42
M 86,a[208:211],42
M 90,a[212:215],42
M 82,a[216:219],42
M 86,a[220:223],42
M 90,a[224:227],42
M 82,a[228:231],42
M 86,a[232:235],42
M 90,a[236:239],42
M 82,a[240:243],42
M 86,a[244:247],42
M 90,a[248:251],42
M 82,a[252:255],42
M 86,v[128:131],42
M 90,v[132:135],42
M 82,v[136:139],42
M 86,v[140:143],42
M 90,v[144:147],42
M 82,v[148:151],42
M 86,v[152:155],42
M 90,v[156:159],42
M 82,v[160:163],42
M 86,v[164:167],42
M 90,v[168:171],42
M 82,v[172:175],42
M 86,v[176:179],42
M 90,v[180:183],42
M 82,v[184:187],42
M 86,v[188:191],42
M 90,v[192:195],42
M 82,v[196:199],42
M 86,v[200:203],42
M 90,v[204:207],42
M 82,v[208:211],42
M 86,v[212:215],42
M 90,v[216:219],42
M 82,v[220:223],42
M 86,v[224:227],42
M 90,v[228:231],42
M 82,v[232:235],42
M 86,v[236:239],42
M 90,v[240:243],42
M 82,v[244:247],42
M 86,v[248:251],42
M 90,v[252:255],42
v_mov_b32 v4,s53
s_mov_b32 s82,0
s_mov_b32 s80,0
s_mov_b32 s54,s45
s_mul_i32 s57,s44,4096
s_mul_i32 s55,s45,32768
s_add_u32 s55,s55,s57
s_add_u32 s55,s55,31457280
s_mul_i32 s57,s44,4096
s_mul_i32 s60,s46,32768
s_mul_i32 s61,s46,1024
s_mul_i32 s62,s46,114688
s_mul_i32 s63,s46,2560
s_mul_i32 s64,s46,1024
s_lshl_b32 s59,s44,8
s_add_u32 s59,s59,131072
s_mul_i32 s58,s45,1024
s_add_u32 s58,s58,109613056
s_mul_i32 s65,s45,114688
s_add_u32 s65,s65,s47
s_mul_i32 s68,s45,2560
s_add_u32 s68,s68,s48
s_mul_i32 s73,s45,1024
s_add_u32 s73,s73,107479040
s_mul_i32 s74,s45,32768
s_add_u32 s74,s74,s49
s_mul_i32 s75,s45,1024
s_add_u32 s75,s75,107479040
s_mov_b32 s66,0x7fff0000
s_mov_b32 s67,0x7fff0000
s_mov_b32 s69,0x7fff0000
s_mov_b32 s76,0x7fff0000
s_mov_b32 s77,0x7fff0000
s_mov_b32 s70,0x7fff0000
s_mov_b32 s71,0x7fff0000
s_mov_b32 s72,0x7fff0000
v_mov_b32 v1,v0
v_add_u32 v2,16384,v0
s_mov_b32 s84,16384
s_mov_b32 s86,0x3a800000
s_mov_b32 s87,0x3727c5ac
s_mov_b32 s88,0x409d30fc
s_mov_b32 s89,0xbeeff558
s_lshr_b32 s85,s44,1
s_mul_i32 s85,s85,127
v_lshlrev_b32 v8,22,v0
v_and_b32 v8,0x80000000,v8
v_or_b32 v8,v8,v5
v_add_u32 v2,32768,v0
s_lshl_b32 s84,s57,1
s_add_u32 s84,s84,32768
s_getpc_b64 s[40:41]
Lg3_pc_%=:
s_lshl_b32 s78,s44,13
v_lshlrev_b32 v94,3,v0
v_add_u32 v94,s78,v94
v_min_u32 v94,Lcode_g3_e-Lg3_pc_%=-4,v94
s_mov_b32 m0,163584
s_nop 0
global_load_lds_dword v94,s[40:41]
v_add_u32 v94,32768,v94
v_min_u32 v94,Lcode_g3_e-Lg3_pc_%=-4,v94
global_load_lds_dword v94,s[40:41]
s_add_u32 m0,s59,4096
s_nop 0
buffer_load_dword v8,s[36:39],s58 offen offset:0 sc1 lds
buffer_store_dword v4,v3,s[36:39],s71 offen offset:0 sc0 sc1
s_add_u32 m0,s59,5120
s_nop 0
buffer_load_dword v8,s[36:39],s58 offen offset:0 sc1 lds
buffer_store_dword v4,v3,s[36:39],s71 offen offset:0 sc0 sc1
s_mov_b32 s83,0
Lg3_poll_%=:
s_add_u32 s78,s83,2
s_and_b32 s78,s78,3
s_lshl_b32 s78,s78,10
s_add_u32 s78,s78,4096
s_add_u32 m0,s59,s78
s_nop 0
buffer_load_dword v8,s[36:39],s58 offen offset:0 sc1 lds
s_and_b32 s78,s83,3
s_lshl_b32 s78,s78,10
v_add_u32 v94,s78,v6
s_waitcnt vmcnt(4)
s_barrier
ds_read_b32 v7,v94 offset:4096
buffer_store_dword v4,v3,s[36:39],s70 offen offset:0 sc0 sc1
s_mov_b32 s70,s71
s_mov_b32 s71,s72
s_mov_b32 s72,0x7fff0000
s_add_u32 s83,s83,1
s_add_u32 s82,s82,1
L 0
v_cmp_eq_u32 vcc,s53,v7
s_nop 0
s_cmp_eq_u32 vcc_lo,-1
s_cbranch_scc1 Lg3_go_%=
s_cmp_lt_u32 s82,20000
s_cbranch_scc1 Lg3_poll_%=
Lg3_go_%=:
buffer_load_dword v114,v11,s[36:39],s73 offen offset:0 sc1
s_add_u32 s73,s73,s64
s_add_u32 m0,s57,0
s_add_u32 s56,s55,0
D 0,56,0
D 0,56,1024
D 0,56,2048
D 0,56,3072
s_add_u32 s58,s58,s61
s_add_u32 m0,s59,2048
s_nop 0
buffer_load_dword v8,s[36:39],s58 offen offset:0 sc1 lds
s_add_u32 m0,s57,16384
s_add_u32 s56,s55,16384
D 0,56,0
D 0,56,1024
D 0,56,2048
D 0,56,3072
s_add_u32 s55,s55,s60
s_sub_u32 s57,s84,s57
s_add_u32 s54,s54,s46
s_waitcnt vmcnt(4)
s_barrier
buffer_store_dword v4,v3,s[36:39],s70 offen offset:0 sc0 sc1
s_min_u32 s71,s71,s72
buffer_store_dword v4,v3,s[36:39],s71 offen offset:0 sc0 sc1
s_mov_b32 s70,0x7fff0000
s_mov_b32 s71,0x7fff0000
s_mov_b32 s72,0x7fff0000
s_cmp_eq_u32 s80,1
buffer_store_dword v4,v3,s[36:39],s71 offen offset:0 sc0 sc1
s_cbranch_scc1 Lg3_st1_%=
s_branch Lg3_st0_%=
Lg3_gox_%=:
s_add_u32 m0,s59,2048
s_nop 0
buffer_load_dword v8,s[36:39],s58 offen offset:0 sc1 lds
s_waitcnt vmcnt(0)
s_barrier
buffer_store_dword v4,v3,s[36:39],s70 offen offset:0 sc0 sc1
s_min_u32 s71,s71,s72
buffer_store_dword v4,v3,s[36:39],s71 offen offset:0 sc0 sc1
s_mov_b32 s70,0x7fff0000
s_mov_b32 s71,0x7fff0000
s_mov_b32 s72,0x7fff0000
s_cmp_eq_u32 s80,1
s_cbranch_scc1 Lg3_st1_%=
Lg3_st0_%=:
ds_read_b32 v7,v6 offset:2048
R 42,1,0
R 46,1,1024
R 50,1,2048
R 54,1,3072
R 58,1,4096
L 5
v_cmp_eq_u32 vcc,s53,v7
L 3
s_cmp_eq_u32 vcc_lo,-1
s_cbranch_scc1 Lg3_F0_%=
s_branch Lg3_L0_%=
Lg3_st1_%=:
ds_read_b32 v7,v6 offset:2048
R 42,2,0
R 46,2,1024
R 50,2,2048
R 54,2,3072
R 58,2,4096
L 5
v_cmp_eq_u32 vcc,s53,v7
L 3
s_cmp_eq_u32 vcc_lo,-1
s_cbranch_scc1 Lg3_F1_%=
s_branch Lg3_L1_%=
Lg3_F1_%=:
s_cmp_eq_u32 s44,1
s_cbranch_scc1 Lg3_F1w1_%=
s_cmp_eq_u32 s44,2
s_cbranch_scc1 Lg3_F1w2_%=
s_cmp_eq_u32 s44,3
s_cbranch_scc1 Lg3_F1w3_%=
s_branch Lg3_F1w0_%=
Lg3_F0_%=:
s_cmp_eq_u32 s44,1
s_cbranch_scc1 Lg3_F0w1_%=
s_cmp_eq_u32 s44,2
s_cbranch_scc1 Lg3_F0w2_%=
s_cmp_eq_u32 s44,3
s_cbranch_scc1 Lg3_F0w3_%=
Lg3_F0w0_%=:
v_mfma_f32_16x16x32_bf16 v[26:29],a[8:11],v[42:45],0
s_add_u32 m0,s57,0
R 62,1,5120
v_add_f32_dpp v115,v114,v114 quad_perm:[1,0,3,2] row_mask:0xf bank_mask:0xf
M 26,a[20:23],46
buffer_load_dword v114,v11,s[36:39],s73 offen offset:0 sc1
v_mfma_f32_16x16x32_bf16 v[22:25],a[4:7],v[42:45],0
s_add_u32 s56,s55,0
v_add_f32_dpp v115,v115,v115 quad_perm:[2,3,0,1] row_mask:0xf bank_mask:0xf
v_fma_f32 v30,-v78,v82,v30
M 22,a[16:19],46
s_add_u32 s58,s58,s61
R 66,1,6144
v_fma_f32 v31,-v78,v83,v31
v_mfma_f32_16x16x32_bf16 v[18:21],a[0:3],v[42:45],0
D 0,56,0
M 18,a[12:15],46
L 3
v_add_f32_dpp v118,v115,v115 row_half_mirror row_mask:0xf bank_mask:0xf
v_fma_f32 v32,-v78,v84,v32
M 18,a[24:27],50
s_mov_b32 s66,s65
R 70,1,7168
v_fma_f32 v33,-v78,v85,v33
M 18,a[36:39],54
s_add_u32 s65,s65,s62
v_mov_b32_dpp v115,v118 row_ror:8 row_mask:0xf bank_mask:0xf
v_fma_f32 v115,-v118,v118,v115
M 22,a[28:31],50
s_add_u32 s54,s54,s46
v_max_f32 v115,s87,v115
v_mul_f32 v118,v15,v118
M 22,a[40:43],54
s_add_u32 s73,s73,s64
R 42,1,8192
v_mul_f32 v30,v79,v30
M 26,a[32:35],50
v_rsq_f32 v116,v115
M 26,a[44:47],54
L 3
D 0,56,1024
M 26,a[56:59],58
R 46,1,9216
v_fma_f32 v118,v116,v16,v118
M 26,a[68:71],62
ds_write_b32 v12,v118 offset:0
M 22,a[52:55],58
v_mul_f32 v31,v79,v31
v_mul_f32 v32,v79,v32
M 22,a[64:67],62
R 50,1,10240
v_mul_f32 v33,v79,v33
M 18,a[48:51],58
v_fma_f32 v94,v30,v30,s88
v_fma_f32 v95,v31,v31,s88
M 18,a[60:63],62
L 4
v_fma_f32 v96,v32,v32,s88
v_fma_f32 v97,v33,v33,s88
M 18,a[72:75],66
R 54,1,11264
v_mul_f32 v94,v30,v94
M 18,a[84:87],70
v_exp_f32 v94,v94
M 22,a[76:79],66
v_mul_f32 v95,v31,v95
v_mul_f32 v96,v32,v96
M 22,a[88:91],70
R 58,1,12288
v_mul_f32 v97,v33,v97
M 26,a[80:83],66
D 0,56,2048
M 26,a[92:95],70
L 4
v_exp_f32 v95,v95
M 26,a[104:107],42
R 62,1,13312
v_fma_f32 v94,v94,s89,s89
M 26,a[116:119],46
v_exp_f32 v96,v96
)"
R"(M 22,a[100:103],42
v_exp_f32 v97,v97
M 22,a[112:115],46
R 66,1,14336
v_fma_f32 v95,v95,s89,s89
M 18,a[96:99],42
v_rcp_f32 v94,v94
M 18,a[108:111],46
L 3
v_rcp_f32 v95,v95
M 18,a[120:123],50
R 70,1,15360
v_fma_f32 v96,v96,s89,s89
M 18,a[132:135],54
s_waitcnt vmcnt(7)
D 0,56,3072
M 22,a[124:127],50
s_barrier
v_rcp_f32 v96,v96
M 22,a[136:139],54
s_add_u32 m0,s59,0
R 42,1,16384
v_fma_f32 v97,v97,s89,s89
M 26,a[128:131],50
s_add_u32 s56,s55,16384
v_rcp_f32 v97,v97
M 26,a[140:143],54
L 3
v_mul_f32 v30,v30,v94
v_mul_f32 v31,v31,v95
M 26,a[152:155],58
s_add_u32 s55,s55,s60
R 46,1,17408
v_mul_f32 v32,v32,v96
M 26,a[164:167],62
v_mul_f32 v33,v33,v97
v_cvt_pk_bf16_f32 v106,v30,v31
M 22,a[148:151],58
v_cvt_pk_bf16_f32 v107,v32,v33
v_fma_f32 v34,-v78,v86,v34
M 22,a[160:163],62
R 50,1,18432
v_fma_f32 v35,-v78,v87,v35
v_fma_f32 v36,-v78,v88,v36
M 18,a[144:147],58
buffer_load_dword v8,s[36:39],s58 offen offset:0 sc1 lds
M 18,a[156:159],62
L 3
v_fma_f32 v37,-v78,v89,v37
v_mul_f32 v34,v79,v34
M 18,a[168:171],66
s_add_u32 m0,s57,16384
R 54,1,19456
v_mul_f32 v35,v79,v35
M 18,a[180:183],70
s_sub_u32 s57,s84,s57
v_mul_f32 v36,v79,v36
v_mul_f32 v37,v79,v37
M 22,a[172:175],66
v_fma_f32 v98,v34,v34,s88
v_fma_f32 v99,v35,v35,s88
M 22,a[184:187],70
R 58,1,20480
v_fma_f32 v100,v36,v36,s88
M 26,a[176:179],66
D 0,56,0
M 26,a[188:191],70
L 3
v_fma_f32 v101,v37,v37,s88
v_mul_f32 v98,v34,v98
M 26,a[200:203],42
R 62,1,21504
v_mul_f32 v99,v35,v99
M 26,a[212:215],46
v_exp_f32 v98,v98
M 22,a[196:199],42
v_exp_f32 v99,v99
M 22,a[208:211],46
R 66,1,22528
v_mul_f32 v100,v36,v100
M 18,a[192:195],42
v_exp_f32 v100,v100
M 18,a[204:207],46
L 3
v_mul_f32 v101,v37,v101
v_fma_f32 v98,v98,s89,s89
M 18,a[216:219],50
R 70,1,23552
v_fma_f32 v99,v99,s89,s89
M 18,a[228:231],54
D 0,56,1024
M 22,a[220:223],50
v_exp_f32 v101,v101
M 22,a[232:235],54
R 42,1,24576
v_fma_f32 v100,v100,s89,s89
M 26,a[224:227],50
v_rcp_f32 v98,v98
M 26,a[236:239],54
L 3
v_rcp_f32 v99,v99
M 26,a[248:251],58
R 46,1,25600
v_fma_f32 v101,v101,s89,s89
M 26,v[132:135],62
v_rcp_f32 v100,v100
M 22,a[244:247],58
v_rcp_f32 v101,v101
M 22,v[128:131],62
R 50,1,26624
v_mul_f32 v34,v34,v98
M 18,a[240:243],58
v_mul_f32 v35,v35,v99
v_mul_f32 v36,v36,v100
M 18,a[252:255],62
L 3
v_mul_f32 v37,v37,v101
v_cvt_pk_bf16_f32 v108,v34,v35
M 18,v[136:139],66
R 54,1,27648
v_cvt_pk_bf16_f32 v109,v36,v37
M 18,v[148:151],70
D 0,56,2048
M 22,v[140:143],66
v_fma_f32 v38,-v78,v90,v38
v_fma_f32 v39,-v78,v91,v39
M 22,v[152:155],70
R 58,1,28672
v_fma_f32 v40,-v78,v92,v40
M 26,v[144:147],66
v_fma_f32 v41,-v78,v93,v41
v_mul_f32 v38,v79,v38
M 26,v[156:159],70
L 3
v_mul_f32 v39,v79,v39
v_mul_f32 v40,v79,v40
M 26,v[168:171],42
R 62,1,29696
v_mul_f32 v41,v79,v41
M 26,v[180:183],46
v_fma_f32 v102,v38,v38,s88
v_fma_f32 v103,v39,v39,s88
M 22,v[164:167],42
v_fma_f32 v104,v40,v40,s88
v_fma_f32 v105,v41,v41,s88
M 22,v[176:179],46
R 66,1,30720
v_mul_f32 v102,v38,v102
M 18,v[160:163],42
v_exp_f32 v102,v102
M 18,v[172:175],46
L 3
D 0,56,3072
M 18,v[184:187],50
s_nop 0
R 70,1,31744
v_mul_f32 v103,v39,v103
M 18,v[196:199],54
s_waitcnt vmcnt(4)
v_exp_f32 v103,v103
v_mul_f32 v104,v40,v104
M 22,v[188:191],50
s_barrier
buffer_store_dword v4,v3,s[36:39],s70 offen offset:0 sc0 sc1
M 22,v[200:203],54
s_mov_b32 s70,s69
R 42,2,0
ds_read_b32 v7,v6 offset:0
M 26,v[192:195],50
s_mov_b32 s69,s68
ds_read_b64 v[74:75],v13 offset:0
v_mul_f32 v105,v41,v105
M 26,v[204:207],54
L 5
v_exp_f32 v104,v104
M 26,v[216:219],58
s_add_u32 s68,s68,s63
R 46,2,1024
v_fma_f32 v102,v102,s89,s89
M 26,v[228:231],62
v_exp_f32 v105,v105
M 22,v[212:215],58
buffer_store_dwordx4 v[106:109],v0,s[36:39],s67 offen offset:0 sc0 sc1
v_fma_f32 v103,v103,s89,s89
M 22,v[224:227],62
L 2
R 50,2,2048
v_fma_f32 v104,v104,s89,s89
M 18,v[208:211],58
v_rcp_f32 v102,v102
M 18,v[220:223],62
s_nop 0
v_fma_f32 v105,v105,s89,s89
v_mul_f32 v38,v38,v102
M 18,v[232:235],66
R 54,2,3072
v_cmp_eq_u32 vcc,s53,v7
M 18,v[244:247],70
v_rcp_f32 v103,v103
M 22,v[236:239],66
s_cmp_eq_u32 vcc_lo,-1
v_rcp_f32 v104,v104
v_rcp_f32 v105,v105
M 22,v[248:251],70
R 58,2,4096
v_mul_f32 v39,v39,v103
v_mul_f32 v40,v40,v104
v_mul_f32 v41,v41,v105
v_cvt_pk_bf16_f32 v110,v38,v39
v_cvt_pk_bf16_f32 v111,v40,v41
M 26,v[240:243],66
L 3
buffer_store_dwordx2 v[110:111],v10,s[36:39],s67 offen offset:0 sc0 sc1
M 26,v[252:255],70
s_cbranch_scc0 Lg3_K1_%=
Lg3_F1w0_%=:
v_mfma_f32_16x16x32_bf16 v[38:41],a[8:11],v[42:45],0
s_add_u32 m0,s57,0
R 62,2,5120
v_add_f32_dpp v115,v114,v114 quad_perm:[1,0,3,2] row_mask:0xf bank_mask:0xf
M 38,a[20:23],46
buffer_load_dword v114,v11,s[36:39],s73 offen offset:0 sc1
v_mfma_f32_16x16x32_bf16 v[34:37],a[4:7],v[42:45],0
s_add_u32 s56,s55,0
v_add_f32_dpp v115,v115,v115 quad_perm:[2,3,0,1] row_mask:0xf bank_mask:0xf
v_fma_f32 v18,-v74,v82,v18
M 34,a[16:19],46
s_add_u32 s58,s58,s61
R 66,2,6144
v_fma_f32 v19,-v74,v83,v19
v_mfma_f32_16x16x32_bf16 v[30:33],a[0:3],v[42:45],0
D 0,56,0
M 30,a[12:15],46
L 3
v_add_f32_dpp v118,v115,v115 row_half_mirror row_mask:0xf bank_mask:0xf
v_fma_f32 v20,-v74,v84,v20
M 30,a[24:27],50
s_mov_b32 s67,s65
R 70,2,7168
v_fma_f32 v21,-v74,v85,v21
M 30,a[36:39],54
s_add_u32 s65,s65,s62
v_mov_b32_dpp v115,v118 row_ror:8 row_mask:0xf bank_mask:0xf
v_fma_f32 v115,-v118,v118,v115
M 34,a[28:31],50
s_add_u32 s54,s54,s46
v_max_f32 v115,s87,v115
v_mul_f32 v118,v15,v118
M 34,a[40:43],54
s_add_u32 s73,s73,s64
R 42,2,8192
v_mul_f32 v18,v75,v18
M 38,a[32:35],50
v_rsq_f32 v116,v115
M 38,a[44:47],54
L 3
D 0,56,1024
M 38,a[56:59],58
R 46,2,9216
v_fma_f32 v118,v116,v16,v118
M 38,a[68:71],62
ds_write_b32 v12,v118 offset:2048
M 34,a[52:55],58
v_mul_f32 v19,v75,v19
v_mul_f32 v20,v75,v20
M 34,a[64:67],62
R 50,2,10240
v_mul_f32 v21,v75,v21
M 30,a[48:51],58
v_fma_f32 v94,v18,v18,s88
v_fma_f32 v95,v19,v19,s88
M 30,a[60:63],62
L 4
v_fma_f32 v96,v20,v20,s88
v_fma_f32 v97,v21,v21,s88
M 30,a[72:75],66
R 54,2,11264
v_mul_f32 v94,v18,v94
M 30,a[84:87],70
v_exp_f32 v94,v94
M 34,a[76:79],66
v_mul_f32 v95,v19,v95
v_mul_f32 v96,v20,v96
M 34,a[88:91],70
R 58,2,12288
v_mul_f32 v97,v21,v97
M 38,a[80:83],66
D 0,56,2048
M 38,a[92:95],70
L 4
v_exp_f32 v95,v95
M 38,a[104:107],42
R 62,2,13312
v_fma_f32 v94,v94,s89,s89
M 38,a[116:119],46
v_exp_f32 v96,v96
M 34,a[100:103],42
v_exp_f32 v97,v97
M 34,a[112:115],46
R 66,2,14336
v_fma_f32 v95,v95,s89,s89
M 30,a[96:99],42
v_rcp_f32 v94,v94
M 30,a[108:111],46
L 3
v_rcp_f32 v95,v95
M 30,a[120:123],50
R 70,2,15360
v_fma_f32 v96,v96,s89,s89
M 30,a[132:135],54
s_waitcnt vmcnt(7)
D 0,56,3072
M 34,a[124:127],50
s_barrier
v_rcp_f32 v96,v96
M 34,a[136:139],54
s_add_u32 m0,s59,1024
R 42,2,16384
v_fma_f32 v97,v97,s89,s89
M 38,a[128:131],50
s_add_u32 s56,s55,16384
v_rcp_f32 v97,v97
M 38,a[140:143],54
L 3
v_mul_f32 v18,v18,v94
v_mul_f32 v19,v19,v95
M 38,a[152:155],58
s_add_u32 s55,s55,s60
R 46,2,17408
v_mul_f32 v20,v20,v96
M 38,a[164:167],62
v_mul_f32 v21,v21,v97
v_cvt_pk_bf16_f32 v106,v18,v19
M 34,a[148:151],58
v_cvt_pk_bf16_f32 v107,v20,v21
v_fma_f32 v22,-v74,v86,v22
M 34,a[160:163],62
R 50,2,18432
v_fma_f32 v23,-v74,v87,v23
v_fma_f32 v24,-v74,v88,v24
M 30,a[144:147],58
buffer_load_dword v8,s[36:39],s58 offen offset:0 sc1 lds
M 30,a[156:159],62
L 3
v_fma_f32 v25,-v74,v89,v25
v_mul_f32 v22,v75,v22
M 30,a[168:171],66
s_add_u32 m0,s57,16384
R 54,2,19456
v_mul_f32 v23,v75,v23
M 30,a[180:183],70
s_sub_u32 s57,s84,s57
v_mul_f32 v24,v75,v24
v_mul_f32 v25,v75,v25
M 34,a[172:175],66
v_fma_f32 v98,v22,v22,s88
v_fma_f32 v99,v23,v23,s88
M 34,a[184:187],70
R 58,2,20480
v_fma_f32 v100,v24,v24,s88
M 38,a[176:179],66
D 0,56,0
M 38,a[188:191],70
L 3
v_fma_f32 v101,v25,v25,s88
v_mul_f32 v98,v22,v98
M 38,a[200:203],42
R 62,2,21504
v_mul_f32 v99,v23,v99
M 38,a[212:215],46
v_exp_f32 v98,v98
M 34,a[196:199],42
v_exp_f32 v99,v99
M 34,a[208:211],46
R 66,2,22528
v_mul_f32 v100,v24,v100
M 30,a[192:195],42
v_exp_f32 v100,v100
M 30,a[204:207],46
L 3
v_mul_f32 v101,v25,v101
v_fma_f32 v98,v98,s89,s89
M 30,a[216:219],50
R 70,2,23552
v_fma_f32 v99,v99,s89,s89
M 30,a[228:231],54
D 0,56,1024
M 34,a[220:223],50
v_exp_f32 v101,v101
M 34,a[232:235],54
R 42,2,24576
v_fma_f32 v100,v100,s89,s89
M 38,a[224:227],50
v_rcp_f32 v98,v98
M 38,a[236:239],54
L 3
v_rcp_f32 v99,v99
M 38,a[248:251],58
R 46,2,25600
v_fma_f32 v101,v101,s89,s89
M 38,v[132:135],62
v_rcp_f32 v100,v100
M 34,a[244:247],58
v_rcp_f32 v101,v101
M 34,v[128:131],62
R 50,2,26624
v_mul_f32 v22,v22,v98
M 30,a[240:243],58
v_mul_f32 v23,v23,v99
v_mul_f32 v24,v24,v100
M 30,a[252:255],62
L 3
v_mul_f32 v25,v25,v101
v_cvt_pk_bf16_f32 v108,v22,v23
M 30,v[136:139],66
R 54,2,27648
v_cvt_pk_bf16_f32 v109,v24,v25
M 30,v[148:151],70
D 0,56,2048
M 34,v[140:143],66
v_fma_f32 v26,-v74,v90,v26
v_fma_f32 v27,-v74,v91,v27
M 34,v[152:155],70
R 58,2,28672
v_fma_f32 v28,-v74,v92,v28
M 38,v[144:147],66
v_fma_f32 v29,-v74,v93,v29
v_mul_f32 v26,v75,v26
M 38,v[156:159],70
L 3
v_mul_f32 v27,v75,v27
v_mul_f32 v28,v75,v28
M 38,v[168:171],42
R 62,2,29696
v_mul_f32 v29,v75,v29
M 38,v[180:183],46
v_fma_f32 v102,v26,v26,s88
v_fma_f32 v103,v27,v27,s88
M 34,v[164:167],42
v_fma_f32 v104,v28,v28,s88
v_fma_f32 v105,v29,v29,s88
M 34,v[176:179],46
R 66,2,30720
v_mul_f32 v102,v26,v102
M 30,v[160:163],42
v_exp_f32 v102,v102
M 30,v[172:175],46
L 3
D 0,56,3072
M 30,v[184:187],50
s_nop 0
R 70,2,31744
v_mul_f32 v103,v27,v103
M 30,v[196:199],54
s_waitcnt vmcnt(4)
v_exp_f32 v103,v103
v_mul_f32 v104,v28,v104
M 34,v[188:191],50
s_barrier
buffer_store_dword v4,v3,s[36:39],s70 offen offset:0 sc0 sc1
M 34,v[200:203],54
s_mov_b32 s70,s69
R 42,1,0
ds_read_b32 v7,v6 offset:1024
M 38,v[192:195],50
s_mov_b32 s69,s68
ds_read_b64 v[78:79],v13 offset:2048
v_mul_f32 v105,v29,v105
M 38,v[204:207],54
L 5
v_exp_f32 v104,v104
M 38,v[216:219],58
s_add_u32 s68,s68,s63
R 46,1,1024
v_fma_f32 v102,v102,s89,s89
M 38,v[228:231],62
v_exp_f32 v105,v105
M 34,v[212:215],58
buffer_store_dwordx4 v[106:109],v0,s[36:39],s66 offen offset:0 sc0 sc1
v_fma_f32 v103,v103,s89,s89
M 34,v[224:227],62
L 2
R 50,1,2048
v_fma_f32 v104,v104,s89,s89
M 30,v[208:211],58
v_rcp_f32 v102,v102
M 30,v[220:223],62
s_nop 0
v_fma_f32 v105,v105,s89,s89
v_mul_f32 v26,v26,v102
M 30,v[232:235],66
R 54,1,3072
v_cmp_eq_u32 vcc,s53,v7
M 30,v[244:247],70
v_rcp_f32 v103,v103
M 34,v[236:239],66
s_cmp_eq_u32 vcc_lo,-1
v_rcp_f32 v104,v104
v_rcp_f32 v105,v105
M 34,v[248:251],70
)"
R"(R 58,1,4096
v_mul_f32 v27,v27,v103
v_mul_f32 v28,v28,v104
v_mul_f32 v29,v29,v105
v_cvt_pk_bf16_f32 v110,v26,v27
v_cvt_pk_bf16_f32 v111,v28,v29
M 38,v[240:243],66
L 3
buffer_store_dwordx2 v[110:111],v10,s[36:39],s66 offen offset:0 sc0 sc1
M 38,v[252:255],70
s_cbranch_scc1 Lg3_F0w0_%=
s_branch Lg3_K0_%=
Lg3_F0w1_%=:
v_mfma_f32_16x16x32_bf16 v[26:29],a[8:11],v[42:45],0
s_add_u32 m0,s57,0
R 62,1,5120
v_add_f32_dpp v115,v114,v114 quad_perm:[1,0,3,2] row_mask:0xf bank_mask:0xf
M 26,a[20:23],46
buffer_load_dword v114,v11,s[36:39],s73 offen offset:0 sc1
v_mfma_f32_16x16x32_bf16 v[22:25],a[4:7],v[42:45],0
s_add_u32 s56,s55,0
v_add_f32_dpp v115,v115,v115 quad_perm:[2,3,0,1] row_mask:0xf bank_mask:0xf
v_fma_f32 v30,-v78,v82,v30
M 22,a[16:19],46
s_add_u32 s58,s58,s61
R 66,1,6144
v_fma_f32 v31,-v78,v83,v31
v_mfma_f32_16x16x32_bf16 v[18:21],a[0:3],v[42:45],0
D 0,56,0
M 18,a[12:15],46
L 3
v_add_f32_dpp v118,v115,v115 row_half_mirror row_mask:0xf bank_mask:0xf
v_fma_f32 v32,-v78,v84,v32
M 18,a[24:27],50
s_mov_b32 s66,s65
R 70,1,7168
v_fma_f32 v33,-v78,v85,v33
M 18,a[36:39],54
s_add_u32 s65,s65,s62
v_mov_b32_dpp v115,v118 row_ror:8 row_mask:0xf bank_mask:0xf
v_fma_f32 v115,-v118,v118,v115
M 22,a[28:31],50
s_add_u32 s54,s54,s46
v_max_f32 v115,s87,v115
v_mul_f32 v118,v15,v118
M 22,a[40:43],54
s_add_u32 s73,s73,s64
R 42,1,8192
v_mul_f32 v30,v79,v30
M 26,a[32:35],50
v_rsq_f32 v116,v115
M 26,a[44:47],54
L 3
D 0,56,1024
M 26,a[56:59],58
R 46,1,9216
v_fma_f32 v118,v116,v16,v118
M 26,a[68:71],62
ds_write_b32 v12,v118 offset:0
M 22,a[52:55],58
v_mul_f32 v31,v79,v31
v_mul_f32 v32,v79,v32
M 22,a[64:67],62
R 50,1,10240
v_mul_f32 v33,v79,v33
M 18,a[48:51],58
v_fma_f32 v94,v30,v30,s88
v_fma_f32 v95,v31,v31,s88
M 18,a[60:63],62
L 4
v_fma_f32 v96,v32,v32,s88
v_fma_f32 v97,v33,v33,s88
M 18,a[72:75],66
R 54,1,11264
v_mul_f32 v94,v30,v94
M 18,a[84:87],70
v_exp_f32 v94,v94
M 22,a[76:79],66
v_mul_f32 v95,v31,v95
v_mul_f32 v96,v32,v96
M 22,a[88:91],70
R 58,1,12288
v_mul_f32 v97,v33,v97
M 26,a[80:83],66
D 0,56,2048
M 26,a[92:95],70
L 4
v_exp_f32 v95,v95
M 26,a[104:107],42
R 62,1,13312
v_fma_f32 v94,v94,s89,s89
M 26,a[116:119],46
v_exp_f32 v96,v96
M 22,a[100:103],42
v_exp_f32 v97,v97
M 22,a[112:115],46
R 66,1,14336
v_fma_f32 v95,v95,s89,s89
M 18,a[96:99],42
v_rcp_f32 v94,v94
M 18,a[108:111],46
s_waitcnt vmcnt(7) lgkmcnt(3)
v_rcp_f32 v95,v95
M 18,a[120:123],50
s_barrier
R 70,1,15360
v_fma_f32 v96,v96,s89,s89
M 18,a[132:135],54
D 0,56,3072
M 22,a[124:127],50
s_add_u32 m0,s59,0
v_rcp_f32 v96,v96
M 22,a[136:139],54
s_add_u32 s56,s55,16384
R 42,1,16384
v_fma_f32 v97,v97,s89,s89
M 26,a[128:131],50
s_add_u32 s55,s55,s60
v_rcp_f32 v97,v97
M 26,a[140:143],54
L 3
v_mul_f32 v30,v30,v94
v_mul_f32 v31,v31,v95
M 26,a[152:155],58
R 46,1,17408
v_mul_f32 v32,v32,v96
M 26,a[164:167],62
v_mul_f32 v33,v33,v97
v_cvt_pk_bf16_f32 v106,v30,v31
M 22,a[148:151],58
v_cvt_pk_bf16_f32 v107,v32,v33
v_fma_f32 v34,-v78,v86,v34
M 22,a[160:163],62
R 50,1,18432
v_fma_f32 v35,-v78,v87,v35
v_fma_f32 v36,-v78,v88,v36
M 18,a[144:147],58
buffer_load_dword v8,s[36:39],s58 offen offset:0 sc1 lds
M 18,a[156:159],62
L 3
v_fma_f32 v37,-v78,v89,v37
v_mul_f32 v34,v79,v34
M 18,a[168:171],66
s_add_u32 m0,s57,16384
R 54,1,19456
v_mul_f32 v35,v79,v35
M 18,a[180:183],70
s_sub_u32 s57,s84,s57
v_mul_f32 v36,v79,v36
v_mul_f32 v37,v79,v37
M 22,a[172:175],66
v_fma_f32 v98,v34,v34,s88
v_fma_f32 v99,v35,v35,s88
M 22,a[184:187],70
R 58,1,20480
v_fma_f32 v100,v36,v36,s88
M 26,a[176:179],66
D 0,56,0
M 26,a[188:191],70
L 3
v_fma_f32 v101,v37,v37,s88
v_mul_f32 v98,v34,v98
M 26,a[200:203],42
R 62,1,21504
v_mul_f32 v99,v35,v99
M 26,a[212:215],46
v_exp_f32 v98,v98
M 22,a[196:199],42
v_exp_f32 v99,v99
M 22,a[208:211],46
R 66,1,22528
v_mul_f32 v100,v36,v100
M 18,a[192:195],42
v_exp_f32 v100,v100
M 18,a[204:207],46
L 3
v_mul_f32 v101,v37,v101
v_fma_f32 v98,v98,s89,s89
M 18,a[216:219],50
R 70,1,23552
v_fma_f32 v99,v99,s89,s89
M 18,a[228:231],54
D 0,56,1024
M 22,a[220:223],50
v_exp_f32 v101,v101
M 22,a[232:235],54
R 42,1,24576
v_fma_f32 v100,v100,s89,s89
M 26,a[224:227],50
v_rcp_f32 v98,v98
M 26,a[236:239],54
L 3
v_rcp_f32 v99,v99
M 26,a[248:251],58
R 46,1,25600
v_fma_f32 v101,v101,s89,s89
M 26,v[132:135],62
v_rcp_f32 v100,v100
M 22,a[244:247],58
v_rcp_f32 v101,v101
M 22,v[128:131],62
R 50,1,26624
v_mul_f32 v34,v34,v98
M 18,a[240:243],58
v_mul_f32 v35,v35,v99
v_mul_f32 v36,v36,v100
M 18,a[252:255],62
L 3
v_mul_f32 v37,v37,v101
v_cvt_pk_bf16_f32 v108,v34,v35
M 18,v[136:139],66
R 54,1,27648
v_cvt_pk_bf16_f32 v109,v36,v37
M 18,v[148:151],70
D 0,56,2048
M 22,v[140:143],66
v_fma_f32 v38,-v78,v90,v38
v_fma_f32 v39,-v78,v91,v39
M 22,v[152:155],70
R 58,1,28672
v_fma_f32 v40,-v78,v92,v40
M 26,v[144:147],66
v_fma_f32 v41,-v78,v93,v41
v_mul_f32 v38,v79,v38
M 26,v[156:159],70
L 3
v_mul_f32 v39,v79,v39
v_mul_f32 v40,v79,v40
M 26,v[168:171],42
R 62,1,29696
v_mul_f32 v41,v79,v41
M 26,v[180:183],46
v_fma_f32 v102,v38,v38,s88
v_fma_f32 v103,v39,v39,s88
M 22,v[164:167],42
v_fma_f32 v104,v40,v40,s88
v_fma_f32 v105,v41,v41,s88
M 22,v[176:179],46
R 66,1,30720
v_mul_f32 v102,v38,v102
M 18,v[160:163],42
s_nop 0
v_exp_f32 v102,v102
M 18,v[172:175],46
s_waitcnt vmcnt(3) lgkmcnt(3)
D 0,56,3072
M 18,v[184:187],50
s_barrier
R 70,1,31744
ds_read_b32 v7,v6 offset:0
M 18,v[196:199],54
buffer_store_dword v4,v3,s[36:39],s70 offen offset:0 sc0 sc1
v_mul_f32 v103,v39,v103
M 22,v[188:191],50
s_mov_b32 s70,s69
ds_read_b64 v[74:75],v13 offset:0
v_mul_f32 v104,v40,v104
M 22,v[200:203],54
s_mov_b32 s69,s68
R 42,2,0
v_mul_f32 v105,v41,v105
M 26,v[192:195],50
s_add_u32 s68,s68,s63
v_exp_f32 v103,v103
M 26,v[204:207],54
L 5
v_exp_f32 v104,v104
M 26,v[216:219],58
L 2
R 46,2,1024
v_fma_f32 v102,v102,s89,s89
M 26,v[228:231],62
v_exp_f32 v105,v105
M 22,v[212:215],58
buffer_store_dwordx4 v[106:109],v0,s[36:39],s67 offen offset:0 sc0 sc1
v_fma_f32 v103,v103,s89,s89
M 22,v[224:227],62
R 50,2,2048
v_fma_f32 v104,v104,s89,s89
M 18,v[208:211],58
v_rcp_f32 v102,v102
M 18,v[220:223],62
s_nop 0
v_fma_f32 v105,v105,s89,s89
v_mul_f32 v38,v38,v102
M 18,v[232:235],66
R 54,2,3072
v_cmp_eq_u32 vcc,s53,v7
M 18,v[244:247],70
v_rcp_f32 v103,v103
M 22,v[236:239],66
s_cmp_eq_u32 vcc_lo,-1
v_rcp_f32 v104,v104
v_rcp_f32 v105,v105
M 22,v[248:251],70
R 58,2,4096
v_mul_f32 v39,v39,v103
v_mul_f32 v40,v40,v104
v_mul_f32 v41,v41,v105
v_cvt_pk_bf16_f32 v110,v38,v39
v_cvt_pk_bf16_f32 v111,v40,v41
M 26,v[240:243],66
L 3
buffer_store_dwordx2 v[110:111],v10,s[36:39],s67 offen offset:0 sc0 sc1
M 26,v[252:255],70
s_cbranch_scc0 Lg3_K1_%=
Lg3_F1w1_%=:
v_mfma_f32_16x16x32_bf16 v[38:41],a[8:11],v[42:45],0
s_add_u32 m0,s57,0
R 62,2,5120
v_add_f32_dpp v115,v114,v114 quad_perm:[1,0,3,2] row_mask:0xf bank_mask:0xf
M 38,a[20:23],46
buffer_load_dword v114,v11,s[36:39],s73 offen offset:0 sc1
v_mfma_f32_16x16x32_bf16 v[34:37],a[4:7],v[42:45],0
s_add_u32 s56,s55,0
v_add_f32_dpp v115,v115,v115 quad_perm:[2,3,0,1] row_mask:0xf bank_mask:0xf
v_fma_f32 v18,-v74,v82,v18
M 34,a[16:19],46
s_add_u32 s58,s58,s61
R 66,2,6144
v_fma_f32 v19,-v74,v83,v19
v_mfma_f32_16x16x32_bf16 v[30:33],a[0:3],v[42:45],0
D 0,56,0
M 30,a[12:15],46
L 3
v_add_f32_dpp v118,v115,v115 row_half_mirror row_mask:0xf bank_mask:0xf
v_fma_f32 v20,-v74,v84,v20
M 30,a[24:27],50
s_mov_b32 s67,s65
R 70,2,7168
v_fma_f32 v21,-v74,v85,v21
M 30,a[36:39],54
s_add_u32 s65,s65,s62
v_mov_b32_dpp v115,v118 row_ror:8 row_mask:0xf bank_mask:0xf
v_fma_f32 v115,-v118,v118,v115
M 34,a[28:31],50
s_add_u32 s54,s54,s46
v_max_f32 v115,s87,v115
v_mul_f32 v118,v15,v118
M 34,a[40:43],54
s_add_u32 s73,s73,s64
R 42,2,8192
v_mul_f32 v18,v75,v18
M 38,a[32:35],50
v_rsq_f32 v116,v115
M 38,a[44:47],54
L 3
D 0,56,1024
M 38,a[56:59],58
R 46,2,9216
v_fma_f32 v118,v116,v16,v118
M 38,a[68:71],62
ds_write_b32 v12,v118 offset:2048
M 34,a[52:55],58
v_mul_f32 v19,v75,v19
v_mul_f32 v20,v75,v20
M 34,a[64:67],62
R 50,2,10240
v_mul_f32 v21,v75,v21
M 30,a[48:51],58
v_fma_f32 v94,v18,v18,s88
v_fma_f32 v95,v19,v19,s88
M 30,a[60:63],62
L 4
v_fma_f32 v96,v20,v20,s88
v_fma_f32 v97,v21,v21,s88
M 30,a[72:75],66
R 54,2,11264
v_mul_f32 v94,v18,v94
M 30,a[84:87],70
v_exp_f32 v94,v94
M 34,a[76:79],66
v_mul_f32 v95,v19,v95
v_mul_f32 v96,v20,v96
M 34,a[88:91],70
R 58,2,12288
v_mul_f32 v97,v21,v97
M 38,a[80:83],66
D 0,56,2048
M 38,a[92:95],70
L 4
v_exp_f32 v95,v95
M 38,a[104:107],42
R 62,2,13312
v_fma_f32 v94,v94,s89,s89
M 38,a[116:119],46
v_exp_f32 v96,v96
M 34,a[100:103],42
v_exp_f32 v97,v97
M 34,a[112:115],46
R 66,2,14336
v_fma_f32 v95,v95,s89,s89
M 30,a[96:99],42
v_rcp_f32 v94,v94
M 30,a[108:111],46
s_waitcnt vmcnt(7) lgkmcnt(3)
v_rcp_f32 v95,v95
M 30,a[120:123],50
s_barrier
R 70,2,15360
v_fma_f32 v96,v96,s89,s89
M 30,a[132:135],54
D 0,56,3072
M 34,a[124:127],50
s_add_u32 m0,s59,1024
v_rcp_f32 v96,v96
M 34,a[136:139],54
s_add_u32 s56,s55,16384
R 42,2,16384
v_fma_f32 v97,v97,s89,s89
M 38,a[128:131],50
s_add_u32 s55,s55,s60
v_rcp_f32 v97,v97
M 38,a[140:143],54
L 3
v_mul_f32 v18,v18,v94
v_mul_f32 v19,v19,v95
M 38,a[152:155],58
R 46,2,17408
v_mul_f32 v20,v20,v96
M 38,a[164:167],62
v_mul_f32 v21,v21,v97
v_cvt_pk_bf16_f32 v106,v18,v19
M 34,a[148:151],58
v_cvt_pk_bf16_f32 v107,v20,v21
v_fma_f32 v22,-v74,v86,v22
M 34,a[160:163],62
R 50,2,18432
v_fma_f32 v23,-v74,v87,v23
v_fma_f32 v24,-v74,v88,v24
M 30,a[144:147],58
buffer_load_dword v8,s[36:39],s58 offen offset:0 sc1 lds
M 30,a[156:159],62
L 3
v_fma_f32 v25,-v74,v89,v25
v_mul_f32 v22,v75,v22
M 30,a[168:171],66
s_add_u32 m0,s57,16384
R 54,2,19456
v_mul_f32 v23,v75,v23
M 30,a[180:183],70
s_sub_u32 s57,s84,s57
v_mul_f32 v24,v75,v24
v_mul_f32 v25,v75,v25
M 34,a[172:175],66
v_fma_f32 v98,v22,v22,s88
v_fma_f32 v99,v23,v23,s88
M 34,a[184:187],70
R 58,2,20480
v_fma_f32 v100,v24,v24,s88
M 38,a[176:179],66
D 0,56,0
M 38,a[188:191],70
L 3
v_fma_f32 v101,v25,v25,s88
v_mul_f32 v98,v22,v98
M 38,a[200:203],42
R 62,2,21504
v_mul_f32 v99,v23,v99
M 38,a[212:215],46
v_exp_f32 v98,v98
M 34,a[196:199],42
v_exp_f32 v99,v99
M 34,a[208:211],46
R 66,2,22528
v_mul_f32 v100,v24,v100
M 30,a[192:195],42
v_exp_f32 v100,v100
M 30,a[204:207],46
L 3
v_mul_f32 v101,v25,v101
v_fma_f32 v98,v98,s89,s89
M 30,a[216:219],50
R 70,2,23552
v_fma_f32 v99,v99,s89,s89
M 30,a[228:231],54
D 0,56,1024
M 34,a[220:223],50
v_exp_f32 v101,v101
M 34,a[232:235],54
R 42,2,24576
v_fma_f32 v100,v100,s89,s89
M 38,a[224:227],50
v_rcp_f32 v98,v98
M 38,a[236:239],54
L 3
v_rcp_f32 v99,v99
M 38,a[248:251],58
R 46,2,25600
v_fma_f32 v101,v101,s89,s89
M 38,v[132:135],62
v_rcp_f32 v100,v100
M 34,a[244:247],58
v_rcp_f32 v101,v101
M 34,v[128:131],62
R 50,2,26624
v_mul_f32 v22,v22,v98
M 30,a[240:243],58
v_mul_f32 v23,v23,v99
v_mul_f32 v24,v24,v100
)"
R"(M 30,a[252:255],62
L 3
v_mul_f32 v25,v25,v101
v_cvt_pk_bf16_f32 v108,v22,v23
M 30,v[136:139],66
R 54,2,27648
v_cvt_pk_bf16_f32 v109,v24,v25
M 30,v[148:151],70
D 0,56,2048
M 34,v[140:143],66
v_fma_f32 v26,-v74,v90,v26
v_fma_f32 v27,-v74,v91,v27
M 34,v[152:155],70
R 58,2,28672
v_fma_f32 v28,-v74,v92,v28
M 38,v[144:147],66
v_fma_f32 v29,-v74,v93,v29
v_mul_f32 v26,v75,v26
M 38,v[156:159],70
L 3
v_mul_f32 v27,v75,v27
v_mul_f32 v28,v75,v28
M 38,v[168:171],42
R 62,2,29696
v_mul_f32 v29,v75,v29
M 38,v[180:183],46
v_fma_f32 v102,v26,v26,s88
v_fma_f32 v103,v27,v27,s88
M 34,v[164:167],42
v_fma_f32 v104,v28,v28,s88
v_fma_f32 v105,v29,v29,s88
M 34,v[176:179],46
R 66,2,30720
v_mul_f32 v102,v26,v102
M 30,v[160:163],42
s_nop 0
v_exp_f32 v102,v102
M 30,v[172:175],46
s_waitcnt vmcnt(3) lgkmcnt(3)
D 0,56,3072
M 30,v[184:187],50
s_barrier
R 70,2,31744
ds_read_b32 v7,v6 offset:1024
M 30,v[196:199],54
buffer_store_dword v4,v3,s[36:39],s70 offen offset:0 sc0 sc1
v_mul_f32 v103,v27,v103
M 34,v[188:191],50
s_mov_b32 s70,s69
ds_read_b64 v[78:79],v13 offset:2048
v_mul_f32 v104,v28,v104
M 34,v[200:203],54
s_mov_b32 s69,s68
R 42,1,0
v_mul_f32 v105,v29,v105
M 38,v[192:195],50
s_add_u32 s68,s68,s63
v_exp_f32 v103,v103
M 38,v[204:207],54
L 5
v_exp_f32 v104,v104
M 38,v[216:219],58
L 2
R 46,1,1024
v_fma_f32 v102,v102,s89,s89
M 38,v[228:231],62
v_exp_f32 v105,v105
M 34,v[212:215],58
buffer_store_dwordx4 v[106:109],v0,s[36:39],s66 offen offset:0 sc0 sc1
v_fma_f32 v103,v103,s89,s89
M 34,v[224:227],62
R 50,1,2048
v_fma_f32 v104,v104,s89,s89
M 30,v[208:211],58
v_rcp_f32 v102,v102
M 30,v[220:223],62
s_nop 0
v_fma_f32 v105,v105,s89,s89
v_mul_f32 v26,v26,v102
M 30,v[232:235],66
R 54,1,3072
v_cmp_eq_u32 vcc,s53,v7
M 30,v[244:247],70
v_rcp_f32 v103,v103
M 34,v[236:239],66
s_cmp_eq_u32 vcc_lo,-1
v_rcp_f32 v104,v104
v_rcp_f32 v105,v105
M 34,v[248:251],70
R 58,1,4096
v_mul_f32 v27,v27,v103
v_mul_f32 v28,v28,v104
v_mul_f32 v29,v29,v105
v_cvt_pk_bf16_f32 v110,v26,v27
v_cvt_pk_bf16_f32 v111,v28,v29
M 38,v[240:243],66
L 3
buffer_store_dwordx2 v[110:111],v10,s[36:39],s66 offen offset:0 sc0 sc1
M 38,v[252:255],70
s_cbranch_scc1 Lg3_F0w1_%=
s_branch Lg3_K0_%=
Lg3_F0w2_%=:
v_mfma_f32_16x16x32_bf16 v[26:29],a[8:11],v[42:45],0
s_add_u32 m0,s57,0
R 62,1,5120
v_add_f32_dpp v115,v114,v114 quad_perm:[1,0,3,2] row_mask:0xf bank_mask:0xf
M 26,a[20:23],46
buffer_load_dword v114,v11,s[36:39],s73 offen offset:0 sc1
v_mfma_f32_16x16x32_bf16 v[22:25],a[4:7],v[42:45],0
s_add_u32 s56,s55,0
v_add_f32_dpp v115,v115,v115 quad_perm:[2,3,0,1] row_mask:0xf bank_mask:0xf
v_fma_f32 v30,-v78,v82,v30
M 22,a[16:19],46
s_add_u32 s58,s58,s61
R 66,1,6144
v_fma_f32 v31,-v78,v83,v31
v_mfma_f32_16x16x32_bf16 v[18:21],a[0:3],v[42:45],0
D 0,56,0
M 18,a[12:15],46
L 3
v_add_f32_dpp v118,v115,v115 row_half_mirror row_mask:0xf bank_mask:0xf
v_fma_f32 v32,-v78,v84,v32
M 18,a[24:27],50
s_mov_b32 s66,s65
R 70,1,7168
v_fma_f32 v33,-v78,v85,v33
M 18,a[36:39],54
s_add_u32 s65,s65,s62
v_mov_b32_dpp v115,v118 row_ror:8 row_mask:0xf bank_mask:0xf
v_fma_f32 v115,-v118,v118,v115
M 22,a[28:31],50
s_add_u32 s54,s54,s46
v_max_f32 v115,s87,v115
v_mul_f32 v118,v15,v118
M 22,a[40:43],54
s_add_u32 s73,s73,s64
R 42,1,8192
v_mul_f32 v30,v79,v30
M 26,a[32:35],50
v_rsq_f32 v116,v115
M 26,a[44:47],54
L 3
D 0,56,1024
M 26,a[56:59],58
R 46,1,9216
v_fma_f32 v118,v116,v16,v118
M 26,a[68:71],62
ds_write_b32 v12,v118 offset:0
M 22,a[52:55],58
v_mul_f32 v31,v79,v31
v_mul_f32 v32,v79,v32
M 22,a[64:67],62
R 50,1,10240
v_mul_f32 v33,v79,v33
M 18,a[48:51],58
v_fma_f32 v94,v30,v30,s88
v_fma_f32 v95,v31,v31,s88
M 18,a[60:63],62
L 4
v_fma_f32 v96,v32,v32,s88
v_fma_f32 v97,v33,v33,s88
M 18,a[72:75],66
R 54,1,11264
v_mul_f32 v94,v30,v94
M 18,a[84:87],70
v_exp_f32 v94,v94
M 22,a[76:79],66
v_mul_f32 v95,v31,v95
v_mul_f32 v96,v32,v96
M 22,a[88:91],70
R 58,1,12288
v_mul_f32 v97,v33,v97
M 26,a[80:83],66
D 0,56,2048
M 26,a[92:95],70
L 4
v_exp_f32 v95,v95
M 26,a[104:107],42
R 62,1,13312
v_fma_f32 v94,v94,s89,s89
M 26,a[116:119],46
v_exp_f32 v96,v96
M 22,a[100:103],42
v_exp_f32 v97,v97
M 22,a[112:115],46
s_waitcnt vmcnt(6)
R 66,1,14336
v_fma_f32 v95,v95,s89,s89
M 18,a[96:99],42
s_barrier
v_rcp_f32 v94,v94
M 18,a[108:111],46
L 3
v_rcp_f32 v95,v95
M 18,a[120:123],50
R 70,1,15360
v_fma_f32 v96,v96,s89,s89
M 18,a[132:135],54
D 0,56,3072
M 22,a[124:127],50
s_add_u32 m0,s59,0
v_rcp_f32 v96,v96
M 22,a[136:139],54
s_add_u32 s56,s55,16384
R 42,1,16384
v_fma_f32 v97,v97,s89,s89
M 26,a[128:131],50
s_add_u32 s55,s55,s60
v_rcp_f32 v97,v97
M 26,a[140:143],54
L 3
v_mul_f32 v30,v30,v94
v_mul_f32 v31,v31,v95
M 26,a[152:155],58
R 46,1,17408
v_mul_f32 v32,v32,v96
M 26,a[164:167],62
v_mul_f32 v33,v33,v97
v_cvt_pk_bf16_f32 v106,v30,v31
M 22,a[148:151],58
v_cvt_pk_bf16_f32 v107,v32,v33
v_fma_f32 v34,-v78,v86,v34
M 22,a[160:163],62
R 50,1,18432
v_fma_f32 v35,-v78,v87,v35
v_fma_f32 v36,-v78,v88,v36
M 18,a[144:147],58
buffer_load_dword v8,s[36:39],s58 offen offset:0 sc1 lds
M 18,a[156:159],62
L 3
v_fma_f32 v37,-v78,v89,v37
v_mul_f32 v34,v79,v34
M 18,a[168:171],66
s_add_u32 m0,s57,16384
R 54,1,19456
v_mul_f32 v35,v79,v35
M 18,a[180:183],70
s_sub_u32 s57,s84,s57
v_mul_f32 v36,v79,v36
v_mul_f32 v37,v79,v37
M 22,a[172:175],66
v_fma_f32 v98,v34,v34,s88
v_fma_f32 v99,v35,v35,s88
M 22,a[184:187],70
R 58,1,20480
v_fma_f32 v100,v36,v36,s88
M 26,a[176:179],66
D 0,56,0
M 26,a[188:191],70
L 3
v_fma_f32 v101,v37,v37,s88
v_mul_f32 v98,v34,v98
M 26,a[200:203],42
R 62,1,21504
v_mul_f32 v99,v35,v99
M 26,a[212:215],46
v_exp_f32 v98,v98
M 22,a[196:199],42
v_exp_f32 v99,v99
M 22,a[208:211],46
R 66,1,22528
v_mul_f32 v100,v36,v100
M 18,a[192:195],42
v_exp_f32 v100,v100
M 18,a[204:207],46
L 3
v_mul_f32 v101,v37,v101
v_fma_f32 v98,v98,s89,s89
M 18,a[216:219],50
R 70,1,23552
v_fma_f32 v99,v99,s89,s89
M 18,a[228:231],54
D 0,56,1024
M 22,a[220:223],50
v_exp_f32 v101,v101
M 22,a[232:235],54
R 42,1,24576
v_fma_f32 v100,v100,s89,s89
M 26,a[224:227],50
v_rcp_f32 v98,v98
M 26,a[236:239],54
L 3
v_rcp_f32 v99,v99
M 26,a[248:251],58
R 46,1,25600
v_fma_f32 v101,v101,s89,s89
M 26,v[132:135],62
v_rcp_f32 v100,v100
M 22,a[244:247],58
v_rcp_f32 v101,v101
M 22,v[128:131],62
R 50,1,26624
v_mul_f32 v34,v34,v98
M 18,a[240:243],58
v_mul_f32 v35,v35,v99
v_mul_f32 v36,v36,v100
M 18,a[252:255],62
L 3
v_mul_f32 v37,v37,v101
v_cvt_pk_bf16_f32 v108,v34,v35
M 18,v[136:139],66
R 54,1,27648
v_cvt_pk_bf16_f32 v109,v36,v37
M 18,v[148:151],70
D 0,56,2048
M 22,v[140:143],66
v_fma_f32 v38,-v78,v90,v38
v_fma_f32 v39,-v78,v91,v39
M 22,v[152:155],70
R 58,1,28672
v_fma_f32 v40,-v78,v92,v40
M 26,v[144:147],66
v_fma_f32 v41,-v78,v93,v41
v_mul_f32 v38,v79,v38
M 26,v[156:159],70
L 3
v_mul_f32 v39,v79,v39
v_mul_f32 v40,v79,v40
M 26,v[168:171],42
R 62,1,29696
v_mul_f32 v41,v79,v41
M 26,v[180:183],46
v_fma_f32 v102,v38,v38,s88
v_fma_f32 v103,v39,v39,s88
M 22,v[164:167],42
s_nop 0
v_fma_f32 v104,v40,v40,s88
v_fma_f32 v105,v41,v41,s88
M 22,v[176:179],46
s_waitcnt vmcnt(3)
R 66,1,30720
v_mul_f32 v102,v38,v102
M 18,v[160:163],42
s_barrier
buffer_store_dword v4,v3,s[36:39],s70 offen offset:0 sc0 sc1
M 18,v[172:175],46
L 3
D 0,56,3072
M 18,v[184:187],50
s_mov_b32 s70,s69
R 70,1,31744
ds_read_b32 v7,v6 offset:0
M 18,v[196:199],54
s_mov_b32 s69,s68
ds_read_b64 v[74:75],v13 offset:0
v_exp_f32 v102,v102
M 22,v[188:191],50
s_add_u32 s68,s68,s63
v_mul_f32 v103,v39,v103
v_mul_f32 v104,v40,v104
M 22,v[200:203],54
R 42,2,0
v_mul_f32 v105,v41,v105
M 26,v[192:195],50
v_exp_f32 v103,v103
M 26,v[204:207],54
L 5
v_exp_f32 v104,v104
M 26,v[216:219],58
L 2
R 46,2,1024
v_fma_f32 v102,v102,s89,s89
M 26,v[228:231],62
v_exp_f32 v105,v105
M 22,v[212:215],58
buffer_store_dwordx4 v[106:109],v0,s[36:39],s67 offen offset:0 sc0 sc1
v_fma_f32 v103,v103,s89,s89
M 22,v[224:227],62
R 50,2,2048
v_fma_f32 v104,v104,s89,s89
M 18,v[208:211],58
v_rcp_f32 v102,v102
M 18,v[220:223],62
s_nop 0
v_fma_f32 v105,v105,s89,s89
v_mul_f32 v38,v38,v102
M 18,v[232:235],66
R 54,2,3072
v_cmp_eq_u32 vcc,s53,v7
M 18,v[244:247],70
v_rcp_f32 v103,v103
M 22,v[236:239],66
s_cmp_eq_u32 vcc_lo,-1
v_rcp_f32 v104,v104
v_rcp_f32 v105,v105
M 22,v[248:251],70
R 58,2,4096
v_mul_f32 v39,v39,v103
v_mul_f32 v40,v40,v104
v_mul_f32 v41,v41,v105
v_cvt_pk_bf16_f32 v110,v38,v39
v_cvt_pk_bf16_f32 v111,v40,v41
M 26,v[240:243],66
L 3
buffer_store_dwordx2 v[110:111],v10,s[36:39],s67 offen offset:0 sc0 sc1
M 26,v[252:255],70
s_cbranch_scc0 Lg3_K1_%=
Lg3_F1w2_%=:
v_mfma_f32_16x16x32_bf16 v[38:41],a[8:11],v[42:45],0
s_add_u32 m0,s57,0
R 62,2,5120
v_add_f32_dpp v115,v114,v114 quad_perm:[1,0,3,2] row_mask:0xf bank_mask:0xf
M 38,a[20:23],46
buffer_load_dword v114,v11,s[36:39],s73 offen offset:0 sc1
v_mfma_f32_16x16x32_bf16 v[34:37],a[4:7],v[42:45],0
s_add_u32 s56,s55,0
v_add_f32_dpp v115,v115,v115 quad_perm:[2,3,0,1] row_mask:0xf bank_mask:0xf
v_fma_f32 v18,-v74,v82,v18
M 34,a[16:19],46
s_add_u32 s58,s58,s61
R 66,2,6144
v_fma_f32 v19,-v74,v83,v19
v_mfma_f32_16x16x32_bf16 v[30:33],a[0:3],v[42:45],0
D 0,56,0
M 30,a[12:15],46
L 3
v_add_f32_dpp v118,v115,v115 row_half_mirror row_mask:0xf bank_mask:0xf
v_fma_f32 v20,-v74,v84,v20
M 30,a[24:27],50
s_mov_b32 s67,s65
R 70,2,7168
v_fma_f32 v21,-v74,v85,v21
M 30,a[36:39],54
s_add_u32 s65,s65,s62
v_mov_b32_dpp v115,v118 row_ror:8 row_mask:0xf bank_mask:0xf
v_fma_f32 v115,-v118,v118,v115
M 34,a[28:31],50
s_add_u32 s54,s54,s46
v_max_f32 v115,s87,v115
v_mul_f32 v118,v15,v118
M 34,a[40:43],54
s_add_u32 s73,s73,s64
R 42,2,8192
v_mul_f32 v18,v75,v18
M 38,a[32:35],50
v_rsq_f32 v116,v115
M 38,a[44:47],54
L 3
D 0,56,1024
M 38,a[56:59],58
R 46,2,9216
v_fma_f32 v118,v116,v16,v118
M 38,a[68:71],62
ds_write_b32 v12,v118 offset:2048
M 34,a[52:55],58
v_mul_f32 v19,v75,v19
v_mul_f32 v20,v75,v20
M 34,a[64:67],62
R 50,2,10240
v_mul_f32 v21,v75,v21
M 30,a[48:51],58
v_fma_f32 v94,v18,v18,s88
v_fma_f32 v95,v19,v19,s88
M 30,a[60:63],62
L 4
v_fma_f32 v96,v20,v20,s88
v_fma_f32 v97,v21,v21,s88
M 30,a[72:75],66
R 54,2,11264
v_mul_f32 v94,v18,v94
M 30,a[84:87],70
v_exp_f32 v94,v94
M 34,a[76:79],66
v_mul_f32 v95,v19,v95
v_mul_f32 v96,v20,v96
M 34,a[88:91],70
R 58,2,12288
v_mul_f32 v97,v21,v97
M 38,a[80:83],66
D 0,56,2048
M 38,a[92:95],70
L 4
v_exp_f32 v95,v95
M 38,a[104:107],42
R 62,2,13312
v_fma_f32 v94,v94,s89,s89
M 38,a[116:119],46
v_exp_f32 v96,v96
M 34,a[100:103],42
v_exp_f32 v97,v97
M 34,a[112:115],46
s_waitcnt vmcnt(6)
R 66,2,14336
v_fma_f32 v95,v95,s89,s89
M 30,a[96:99],42
s_barrier
v_rcp_f32 v94,v94
M 30,a[108:111],46
L 3
v_rcp_f32 v95,v95
M 30,a[120:123],50
R 70,2,15360
v_fma_f32 v96,v96,s89,s89
M 30,a[132:135],54
D 0,56,3072
M 34,a[124:127],50
s_add_u32 m0,s59,1024
v_rcp_f32 v96,v96
M 34,a[136:139],54
s_add_u32 s56,s55,16384
)"
R"(R 42,2,16384
v_fma_f32 v97,v97,s89,s89
M 38,a[128:131],50
s_add_u32 s55,s55,s60
v_rcp_f32 v97,v97
M 38,a[140:143],54
L 3
v_mul_f32 v18,v18,v94
v_mul_f32 v19,v19,v95
M 38,a[152:155],58
R 46,2,17408
v_mul_f32 v20,v20,v96
M 38,a[164:167],62
v_mul_f32 v21,v21,v97
v_cvt_pk_bf16_f32 v106,v18,v19
M 34,a[148:151],58
v_cvt_pk_bf16_f32 v107,v20,v21
v_fma_f32 v22,-v74,v86,v22
M 34,a[160:163],62
R 50,2,18432
v_fma_f32 v23,-v74,v87,v23
v_fma_f32 v24,-v74,v88,v24
M 30,a[144:147],58
buffer_load_dword v8,s[36:39],s58 offen offset:0 sc1 lds
M 30,a[156:159],62
L 3
v_fma_f32 v25,-v74,v89,v25
v_mul_f32 v22,v75,v22
M 30,a[168:171],66
s_add_u32 m0,s57,16384
R 54,2,19456
v_mul_f32 v23,v75,v23
M 30,a[180:183],70
s_sub_u32 s57,s84,s57
v_mul_f32 v24,v75,v24
v_mul_f32 v25,v75,v25
M 34,a[172:175],66
v_fma_f32 v98,v22,v22,s88
v_fma_f32 v99,v23,v23,s88
M 34,a[184:187],70
R 58,2,20480
v_fma_f32 v100,v24,v24,s88
M 38,a[176:179],66
D 0,56,0
M 38,a[188:191],70
L 3
v_fma_f32 v101,v25,v25,s88
v_mul_f32 v98,v22,v98
M 38,a[200:203],42
R 62,2,21504
v_mul_f32 v99,v23,v99
M 38,a[212:215],46
v_exp_f32 v98,v98
M 34,a[196:199],42
v_exp_f32 v99,v99
M 34,a[208:211],46
R 66,2,22528
v_mul_f32 v100,v24,v100
M 30,a[192:195],42
v_exp_f32 v100,v100
M 30,a[204:207],46
L 3
v_mul_f32 v101,v25,v101
v_fma_f32 v98,v98,s89,s89
M 30,a[216:219],50
R 70,2,23552
v_fma_f32 v99,v99,s89,s89
M 30,a[228:231],54
D 0,56,1024
M 34,a[220:223],50
v_exp_f32 v101,v101
M 34,a[232:235],54
R 42,2,24576
v_fma_f32 v100,v100,s89,s89
M 38,a[224:227],50
v_rcp_f32 v98,v98
M 38,a[236:239],54
L 3
v_rcp_f32 v99,v99
M 38,a[248:251],58
R 46,2,25600
v_fma_f32 v101,v101,s89,s89
M 38,v[132:135],62
v_rcp_f32 v100,v100
M 34,a[244:247],58
v_rcp_f32 v101,v101
M 34,v[128:131],62
R 50,2,26624
v_mul_f32 v22,v22,v98
M 30,a[240:243],58
v_mul_f32 v23,v23,v99
v_mul_f32 v24,v24,v100
M 30,a[252:255],62
L 3
v_mul_f32 v25,v25,v101
v_cvt_pk_bf16_f32 v108,v22,v23
M 30,v[136:139],66
R 54,2,27648
v_cvt_pk_bf16_f32 v109,v24,v25
M 30,v[148:151],70
D 0,56,2048
M 34,v[140:143],66
v_fma_f32 v26,-v74,v90,v26
v_fma_f32 v27,-v74,v91,v27
M 34,v[152:155],70
R 58,2,28672
v_fma_f32 v28,-v74,v92,v28
M 38,v[144:147],66
v_fma_f32 v29,-v74,v93,v29
v_mul_f32 v26,v75,v26
M 38,v[156:159],70
L 3
v_mul_f32 v27,v75,v27
v_mul_f32 v28,v75,v28
M 38,v[168:171],42
R 62,2,29696
v_mul_f32 v29,v75,v29
M 38,v[180:183],46
v_fma_f32 v102,v26,v26,s88
v_fma_f32 v103,v27,v27,s88
M 34,v[164:167],42
s_nop 0
v_fma_f32 v104,v28,v28,s88
v_fma_f32 v105,v29,v29,s88
M 34,v[176:179],46
s_waitcnt vmcnt(3)
R 66,2,30720
v_mul_f32 v102,v26,v102
M 30,v[160:163],42
s_barrier
buffer_store_dword v4,v3,s[36:39],s70 offen offset:0 sc0 sc1
M 30,v[172:175],46
L 3
D 0,56,3072
M 30,v[184:187],50
s_mov_b32 s70,s69
R 70,2,31744
ds_read_b32 v7,v6 offset:1024
M 30,v[196:199],54
s_mov_b32 s69,s68
ds_read_b64 v[78:79],v13 offset:2048
v_exp_f32 v102,v102
M 34,v[188:191],50
s_add_u32 s68,s68,s63
v_mul_f32 v103,v27,v103
v_mul_f32 v104,v28,v104
M 34,v[200:203],54
R 42,1,0
v_mul_f32 v105,v29,v105
M 38,v[192:195],50
v_exp_f32 v103,v103
M 38,v[204:207],54
L 5
v_exp_f32 v104,v104
M 38,v[216:219],58
L 2
R 46,1,1024
v_fma_f32 v102,v102,s89,s89
M 38,v[228:231],62
v_exp_f32 v105,v105
M 34,v[212:215],58
buffer_store_dwordx4 v[106:109],v0,s[36:39],s66 offen offset:0 sc0 sc1
v_fma_f32 v103,v103,s89,s89
M 34,v[224:227],62
R 50,1,2048
v_fma_f32 v104,v104,s89,s89
M 30,v[208:211],58
v_rcp_f32 v102,v102
M 30,v[220:223],62
s_nop 0
v_fma_f32 v105,v105,s89,s89
v_mul_f32 v26,v26,v102
M 30,v[232:235],66
R 54,1,3072
v_cmp_eq_u32 vcc,s53,v7
M 30,v[244:247],70
v_rcp_f32 v103,v103
M 34,v[236:239],66
s_cmp_eq_u32 vcc_lo,-1
v_rcp_f32 v104,v104
v_rcp_f32 v105,v105
M 34,v[248:251],70
R 58,1,4096
v_mul_f32 v27,v27,v103
v_mul_f32 v28,v28,v104
v_mul_f32 v29,v29,v105
v_cvt_pk_bf16_f32 v110,v26,v27
v_cvt_pk_bf16_f32 v111,v28,v29
M 38,v[240:243],66
L 3
buffer_store_dwordx2 v[110:111],v10,s[36:39],s66 offen offset:0 sc0 sc1
M 38,v[252:255],70
s_cbranch_scc1 Lg3_F0w2_%=
s_branch Lg3_K0_%=
Lg3_F0w3_%=:
v_mfma_f32_16x16x32_bf16 v[26:29],a[8:11],v[42:45],0
s_add_u32 m0,s57,0
R 62,1,5120
v_add_f32_dpp v115,v114,v114 quad_perm:[1,0,3,2] row_mask:0xf bank_mask:0xf
M 26,a[20:23],46
buffer_load_dword v114,v11,s[36:39],s73 offen offset:0 sc1
v_mfma_f32_16x16x32_bf16 v[22:25],a[4:7],v[42:45],0
s_add_u32 s56,s55,0
v_add_f32_dpp v115,v115,v115 quad_perm:[2,3,0,1] row_mask:0xf bank_mask:0xf
v_fma_f32 v30,-v78,v82,v30
M 22,a[16:19],46
s_add_u32 s58,s58,s61
R 66,1,6144
v_fma_f32 v31,-v78,v83,v31
v_mfma_f32_16x16x32_bf16 v[18:21],a[0:3],v[42:45],0
D 0,56,0
M 18,a[12:15],46
L 3
v_add_f32_dpp v118,v115,v115 row_half_mirror row_mask:0xf bank_mask:0xf
v_fma_f32 v32,-v78,v84,v32
M 18,a[24:27],50
s_mov_b32 s66,s65
R 70,1,7168
v_fma_f32 v33,-v78,v85,v33
M 18,a[36:39],54
s_add_u32 s65,s65,s62
v_mov_b32_dpp v115,v118 row_ror:8 row_mask:0xf bank_mask:0xf
v_fma_f32 v115,-v118,v118,v115
M 22,a[28:31],50
s_add_u32 s54,s54,s46
v_max_f32 v115,s87,v115
v_mul_f32 v118,v15,v118
M 22,a[40:43],54
s_add_u32 s73,s73,s64
R 42,1,8192
v_mul_f32 v30,v79,v30
M 26,a[32:35],50
v_rsq_f32 v116,v115
M 26,a[44:47],54
L 3
D 0,56,1024
M 26,a[56:59],58
R 46,1,9216
v_fma_f32 v118,v116,v16,v118
M 26,a[68:71],62
ds_write_b32 v12,v118 offset:0
M 22,a[52:55],58
v_mul_f32 v31,v79,v31
v_mul_f32 v32,v79,v32
M 22,a[64:67],62
R 50,1,10240
v_mul_f32 v33,v79,v33
M 18,a[48:51],58
v_fma_f32 v94,v30,v30,s88
v_fma_f32 v95,v31,v31,s88
M 18,a[60:63],62
L 4
v_fma_f32 v96,v32,v32,s88
v_fma_f32 v97,v33,v33,s88
M 18,a[72:75],66
R 54,1,11264
v_mul_f32 v94,v30,v94
M 18,a[84:87],70
v_exp_f32 v94,v94
M 22,a[76:79],66
v_mul_f32 v95,v31,v95
v_mul_f32 v96,v32,v96
M 22,a[88:91],70
R 58,1,12288
v_mul_f32 v97,v33,v97
M 26,a[80:83],66
D 0,56,2048
M 26,a[92:95],70
L 4
v_exp_f32 v95,v95
M 26,a[104:107],42
R 62,1,13312
v_fma_f32 v94,v94,s89,s89
M 26,a[116:119],46
s_waitcnt vmcnt(6)
v_exp_f32 v96,v96
M 22,a[100:103],42
s_barrier
v_exp_f32 v97,v97
M 22,a[112:115],46
R 66,1,14336
v_fma_f32 v95,v95,s89,s89
M 18,a[96:99],42
v_rcp_f32 v94,v94
M 18,a[108:111],46
L 3
v_rcp_f32 v95,v95
M 18,a[120:123],50
R 70,1,15360
v_fma_f32 v96,v96,s89,s89
M 18,a[132:135],54
D 0,56,3072
M 22,a[124:127],50
s_add_u32 m0,s59,0
v_rcp_f32 v96,v96
M 22,a[136:139],54
s_add_u32 s56,s55,16384
R 42,1,16384
v_fma_f32 v97,v97,s89,s89
M 26,a[128:131],50
s_add_u32 s55,s55,s60
v_rcp_f32 v97,v97
M 26,a[140:143],54
L 3
v_mul_f32 v30,v30,v94
v_mul_f32 v31,v31,v95
M 26,a[152:155],58
R 46,1,17408
v_mul_f32 v32,v32,v96
M 26,a[164:167],62
v_mul_f32 v33,v33,v97
v_cvt_pk_bf16_f32 v106,v30,v31
M 22,a[148:151],58
v_cvt_pk_bf16_f32 v107,v32,v33
v_fma_f32 v34,-v78,v86,v34
M 22,a[160:163],62
R 50,1,18432
v_fma_f32 v35,-v78,v87,v35
v_fma_f32 v36,-v78,v88,v36
M 18,a[144:147],58
buffer_load_dword v8,s[36:39],s58 offen offset:0 sc1 lds
M 18,a[156:159],62
L 3
v_fma_f32 v37,-v78,v89,v37
v_mul_f32 v34,v79,v34
M 18,a[168:171],66
s_add_u32 m0,s57,16384
R 54,1,19456
v_mul_f32 v35,v79,v35
M 18,a[180:183],70
s_sub_u32 s57,s84,s57
v_mul_f32 v36,v79,v36
v_mul_f32 v37,v79,v37
M 22,a[172:175],66
v_fma_f32 v98,v34,v34,s88
v_fma_f32 v99,v35,v35,s88
M 22,a[184:187],70
R 58,1,20480
v_fma_f32 v100,v36,v36,s88
M 26,a[176:179],66
D 0,56,0
M 26,a[188:191],70
L 3
v_fma_f32 v101,v37,v37,s88
v_mul_f32 v98,v34,v98
M 26,a[200:203],42
R 62,1,21504
v_mul_f32 v99,v35,v99
M 26,a[212:215],46
v_exp_f32 v98,v98
M 22,a[196:199],42
v_exp_f32 v99,v99
M 22,a[208:211],46
R 66,1,22528
v_mul_f32 v100,v36,v100
M 18,a[192:195],42
v_exp_f32 v100,v100
M 18,a[204:207],46
L 3
v_mul_f32 v101,v37,v101
v_fma_f32 v98,v98,s89,s89
M 18,a[216:219],50
R 70,1,23552
v_fma_f32 v99,v99,s89,s89
M 18,a[228:231],54
D 0,56,1024
M 22,a[220:223],50
v_exp_f32 v101,v101
M 22,a[232:235],54
R 42,1,24576
v_fma_f32 v100,v100,s89,s89
M 26,a[224:227],50
v_rcp_f32 v98,v98
M 26,a[236:239],54
L 3
v_rcp_f32 v99,v99
M 26,a[248:251],58
R 46,1,25600
v_fma_f32 v101,v101,s89,s89
M 26,v[132:135],62
v_rcp_f32 v100,v100
M 22,a[244:247],58
v_rcp_f32 v101,v101
M 22,v[128:131],62
R 50,1,26624
v_mul_f32 v34,v34,v98
M 18,a[240:243],58
v_mul_f32 v35,v35,v99
v_mul_f32 v36,v36,v100
M 18,a[252:255],62
L 3
v_mul_f32 v37,v37,v101
v_cvt_pk_bf16_f32 v108,v34,v35
M 18,v[136:139],66
R 54,1,27648
v_cvt_pk_bf16_f32 v109,v36,v37
M 18,v[148:151],70
D 0,56,2048
M 22,v[140:143],66
v_fma_f32 v38,-v78,v90,v38
v_fma_f32 v39,-v78,v91,v39
M 22,v[152:155],70
R 58,1,28672
v_fma_f32 v40,-v78,v92,v40
M 26,v[144:147],66
v_fma_f32 v41,-v78,v93,v41
v_mul_f32 v38,v79,v38
M 26,v[156:159],70
L 3
v_mul_f32 v39,v79,v39
v_mul_f32 v40,v79,v40
M 26,v[168:171],42
s_nop 0
R 62,1,29696
v_mul_f32 v41,v79,v41
M 26,v[180:183],46
s_waitcnt vmcnt(3)
v_fma_f32 v102,v38,v38,s88
v_fma_f32 v103,v39,v39,s88
M 22,v[164:167],42
s_barrier
buffer_store_dword v4,v3,s[36:39],s70 offen offset:0 sc0 sc1
M 22,v[176:179],46
s_mov_b32 s70,s69
R 66,1,30720
ds_read_b32 v7,v6 offset:0
M 18,v[160:163],42
s_mov_b32 s69,s68
ds_read_b64 v[74:75],v13 offset:0
v_fma_f32 v104,v40,v40,s88
M 18,v[172:175],46
L 5
D 0,56,3072
M 18,v[184:187],50
s_add_u32 s68,s68,s63
R 70,1,31744
v_fma_f32 v105,v41,v41,s88
M 18,v[196:199],54
v_mul_f32 v102,v38,v102
v_exp_f32 v102,v102
M 22,v[188:191],50
v_mul_f32 v103,v39,v103
v_mul_f32 v104,v40,v104
M 22,v[200:203],54
L 2
R 42,2,0
v_mul_f32 v105,v41,v105
M 26,v[192:195],50
v_exp_f32 v103,v103
M 26,v[204:207],54
s_nop 0
v_exp_f32 v104,v104
M 26,v[216:219],58
R 46,2,1024
v_fma_f32 v102,v102,s89,s89
M 26,v[228:231],62
v_exp_f32 v105,v105
M 22,v[212:215],58
buffer_store_dwordx4 v[106:109],v0,s[36:39],s67 offen offset:0 sc0 sc1
v_fma_f32 v103,v103,s89,s89
M 22,v[224:227],62
R 50,2,2048
v_fma_f32 v104,v104,s89,s89
M 18,v[208:211],58
v_rcp_f32 v102,v102
M 18,v[220:223],62
L 3
v_fma_f32 v105,v105,s89,s89
v_mul_f32 v38,v38,v102
M 18,v[232:235],66
R 54,2,3072
v_cmp_eq_u32 vcc,s53,v7
M 18,v[244:247],70
v_rcp_f32 v103,v103
M 22,v[236:239],66
s_cmp_eq_u32 vcc_lo,-1
v_rcp_f32 v104,v104
v_rcp_f32 v105,v105
M 22,v[248:251],70
R 58,2,4096
v_mul_f32 v39,v39,v103
v_mul_f32 v40,v40,v104
v_mul_f32 v41,v41,v105
v_cvt_pk_bf16_f32 v110,v38,v39
v_cvt_pk_bf16_f32 v111,v40,v41
M 26,v[240:243],66
L 3
buffer_store_dwordx2 v[110:111],v10,s[36:39],s67 offen offset:0 sc0 sc1
M 26,v[252:255],70
s_cbranch_scc0 Lg3_K1_%=
Lg3_F1w3_%=:
v_mfma_f32_16x16x32_bf16 v[38:41],a[8:11],v[42:45],0
s_add_u32 m0,s57,0
R 62,2,5120
v_add_f32_dpp v115,v114,v114 quad_perm:[1,0,3,2] row_mask:0xf bank_mask:0xf
M 38,a[20:23],46
buffer_load_dword v114,v11,s[36:39],s73 offen offset:0 sc1
v_mfma_f32_16x16x32_bf16 v[34:37],a[4:7],v[42:45],0
s_add_u32 s56,s55,0
v_add_f32_dpp v115,v115,v115 quad_perm:[2,3,0,1] row_mask:0xf bank_mask:0xf
)"
R"(v_fma_f32 v18,-v74,v82,v18
M 34,a[16:19],46
s_add_u32 s58,s58,s61
R 66,2,6144
v_fma_f32 v19,-v74,v83,v19
v_mfma_f32_16x16x32_bf16 v[30:33],a[0:3],v[42:45],0
D 0,56,0
M 30,a[12:15],46
L 3
v_add_f32_dpp v118,v115,v115 row_half_mirror row_mask:0xf bank_mask:0xf
v_fma_f32 v20,-v74,v84,v20
M 30,a[24:27],50
s_mov_b32 s67,s65
R 70,2,7168
v_fma_f32 v21,-v74,v85,v21
M 30,a[36:39],54
s_add_u32 s65,s65,s62
v_mov_b32_dpp v115,v118 row_ror:8 row_mask:0xf bank_mask:0xf
v_fma_f32 v115,-v118,v118,v115
M 34,a[28:31],50
s_add_u32 s54,s54,s46
v_max_f32 v115,s87,v115
v_mul_f32 v118,v15,v118
M 34,a[40:43],54
s_add_u32 s73,s73,s64
R 42,2,8192
v_mul_f32 v18,v75,v18
M 38,a[32:35],50
v_rsq_f32 v116,v115
M 38,a[44:47],54
L 3
D 0,56,1024
M 38,a[56:59],58
R 46,2,9216
v_fma_f32 v118,v116,v16,v118
M 38,a[68:71],62
ds_write_b32 v12,v118 offset:2048
M 34,a[52:55],58
v_mul_f32 v19,v75,v19
v_mul_f32 v20,v75,v20
M 34,a[64:67],62
R 50,2,10240
v_mul_f32 v21,v75,v21
M 30,a[48:51],58
v_fma_f32 v94,v18,v18,s88
v_fma_f32 v95,v19,v19,s88
M 30,a[60:63],62
L 4
v_fma_f32 v96,v20,v20,s88
v_fma_f32 v97,v21,v21,s88
M 30,a[72:75],66
R 54,2,11264
v_mul_f32 v94,v18,v94
M 30,a[84:87],70
v_exp_f32 v94,v94
M 34,a[76:79],66
v_mul_f32 v95,v19,v95
v_mul_f32 v96,v20,v96
M 34,a[88:91],70
R 58,2,12288
v_mul_f32 v97,v21,v97
M 38,a[80:83],66
D 0,56,2048
M 38,a[92:95],70
L 4
v_exp_f32 v95,v95
M 38,a[104:107],42
R 62,2,13312
v_fma_f32 v94,v94,s89,s89
M 38,a[116:119],46
s_waitcnt vmcnt(6)
v_exp_f32 v96,v96
M 34,a[100:103],42
s_barrier
v_exp_f32 v97,v97
M 34,a[112:115],46
R 66,2,14336
v_fma_f32 v95,v95,s89,s89
M 30,a[96:99],42
v_rcp_f32 v94,v94
M 30,a[108:111],46
L 3
v_rcp_f32 v95,v95
M 30,a[120:123],50
R 70,2,15360
v_fma_f32 v96,v96,s89,s89
M 30,a[132:135],54
D 0,56,3072
M 34,a[124:127],50
s_add_u32 m0,s59,1024
v_rcp_f32 v96,v96
M 34,a[136:139],54
s_add_u32 s56,s55,16384
R 42,2,16384
v_fma_f32 v97,v97,s89,s89
M 38,a[128:131],50
s_add_u32 s55,s55,s60
v_rcp_f32 v97,v97
M 38,a[140:143],54
L 3
v_mul_f32 v18,v18,v94
v_mul_f32 v19,v19,v95
M 38,a[152:155],58
R 46,2,17408
v_mul_f32 v20,v20,v96
M 38,a[164:167],62
v_mul_f32 v21,v21,v97
v_cvt_pk_bf16_f32 v106,v18,v19
M 34,a[148:151],58
v_cvt_pk_bf16_f32 v107,v20,v21
v_fma_f32 v22,-v74,v86,v22
M 34,a[160:163],62
R 50,2,18432
v_fma_f32 v23,-v74,v87,v23
v_fma_f32 v24,-v74,v88,v24
M 30,a[144:147],58
buffer_load_dword v8,s[36:39],s58 offen offset:0 sc1 lds
M 30,a[156:159],62
L 3
v_fma_f32 v25,-v74,v89,v25
v_mul_f32 v22,v75,v22
M 30,a[168:171],66
s_add_u32 m0,s57,16384
R 54,2,19456
v_mul_f32 v23,v75,v23
M 30,a[180:183],70
s_sub_u32 s57,s84,s57
v_mul_f32 v24,v75,v24
v_mul_f32 v25,v75,v25
M 34,a[172:175],66
v_fma_f32 v98,v22,v22,s88
v_fma_f32 v99,v23,v23,s88
M 34,a[184:187],70
R 58,2,20480
v_fma_f32 v100,v24,v24,s88
M 38,a[176:179],66
D 0,56,0
M 38,a[188:191],70
L 3
v_fma_f32 v101,v25,v25,s88
v_mul_f32 v98,v22,v98
M 38,a[200:203],42
R 62,2,21504
v_mul_f32 v99,v23,v99
M 38,a[212:215],46
v_exp_f32 v98,v98
M 34,a[196:199],42
v_exp_f32 v99,v99
M 34,a[208:211],46
R 66,2,22528
v_mul_f32 v100,v24,v100
M 30,a[192:195],42
v_exp_f32 v100,v100
M 30,a[204:207],46
L 3
v_mul_f32 v101,v25,v101
v_fma_f32 v98,v98,s89,s89
M 30,a[216:219],50
R 70,2,23552
v_fma_f32 v99,v99,s89,s89
M 30,a[228:231],54
D 0,56,1024
M 34,a[220:223],50
v_exp_f32 v101,v101
M 34,a[232:235],54
R 42,2,24576
v_fma_f32 v100,v100,s89,s89
M 38,a[224:227],50
v_rcp_f32 v98,v98
M 38,a[236:239],54
L 3
v_rcp_f32 v99,v99
M 38,a[248:251],58
R 46,2,25600
v_fma_f32 v101,v101,s89,s89
M 38,v[132:135],62
v_rcp_f32 v100,v100
M 34,a[244:247],58
v_rcp_f32 v101,v101
M 34,v[128:131],62
R 50,2,26624
v_mul_f32 v22,v22,v98
M 30,a[240:243],58
v_mul_f32 v23,v23,v99
v_mul_f32 v24,v24,v100
M 30,a[252:255],62
L 3
v_mul_f32 v25,v25,v101
v_cvt_pk_bf16_f32 v108,v22,v23
M 30,v[136:139],66
R 54,2,27648
v_cvt_pk_bf16_f32 v109,v24,v25
M 30,v[148:151],70
D 0,56,2048
M 34,v[140:143],66
v_fma_f32 v26,-v74,v90,v26
v_fma_f32 v27,-v74,v91,v27
M 34,v[152:155],70
R 58,2,28672
v_fma_f32 v28,-v74,v92,v28
M 38,v[144:147],66
v_fma_f32 v29,-v74,v93,v29
v_mul_f32 v26,v75,v26
M 38,v[156:159],70
L 3
v_mul_f32 v27,v75,v27
v_mul_f32 v28,v75,v28
M 38,v[168:171],42
s_nop 0
R 62,2,29696
v_mul_f32 v29,v75,v29
M 38,v[180:183],46
s_waitcnt vmcnt(3)
v_fma_f32 v102,v26,v26,s88
v_fma_f32 v103,v27,v27,s88
M 34,v[164:167],42
s_barrier
buffer_store_dword v4,v3,s[36:39],s70 offen offset:0 sc0 sc1
M 34,v[176:179],46
s_mov_b32 s70,s69
R 66,2,30720
ds_read_b32 v7,v6 offset:1024
M 30,v[160:163],42
s_mov_b32 s69,s68
ds_read_b64 v[78:79],v13 offset:2048
v_fma_f32 v104,v28,v28,s88
M 30,v[172:175],46
L 5
D 0,56,3072
M 30,v[184:187],50
s_add_u32 s68,s68,s63
R 70,2,31744
v_fma_f32 v105,v29,v29,s88
M 30,v[196:199],54
v_mul_f32 v102,v26,v102
v_exp_f32 v102,v102
M 34,v[188:191],50
v_mul_f32 v103,v27,v103
v_mul_f32 v104,v28,v104
M 34,v[200:203],54
L 2
R 42,1,0
v_mul_f32 v105,v29,v105
M 38,v[192:195],50
v_exp_f32 v103,v103
M 38,v[204:207],54
s_nop 0
v_exp_f32 v104,v104
M 38,v[216:219],58
R 46,1,1024
v_fma_f32 v102,v102,s89,s89
M 38,v[228:231],62
v_exp_f32 v105,v105
M 34,v[212:215],58
buffer_store_dwordx4 v[106:109],v0,s[36:39],s66 offen offset:0 sc0 sc1
v_fma_f32 v103,v103,s89,s89
M 34,v[224:227],62
R 50,1,2048
v_fma_f32 v104,v104,s89,s89
M 30,v[208:211],58
v_rcp_f32 v102,v102
M 30,v[220:223],62
L 3
v_fma_f32 v105,v105,s89,s89
v_mul_f32 v26,v26,v102
M 30,v[232:235],66
R 54,1,3072
v_cmp_eq_u32 vcc,s53,v7
M 30,v[244:247],70
v_rcp_f32 v103,v103
M 34,v[236:239],66
s_cmp_eq_u32 vcc_lo,-1
v_rcp_f32 v104,v104
v_rcp_f32 v105,v105
M 34,v[248:251],70
R 58,1,4096
v_mul_f32 v27,v27,v103
v_mul_f32 v28,v28,v104
v_mul_f32 v29,v29,v105
v_cvt_pk_bf16_f32 v110,v26,v27
v_cvt_pk_bf16_f32 v111,v28,v29
M 38,v[240:243],66
L 3
buffer_store_dwordx2 v[110:111],v10,s[36:39],s66 offen offset:0 sc0 sc1
M 38,v[252:255],70
s_cbranch_scc1 Lg3_F0w3_%=
Lg3_K0_%=:
Lg3_L0_%=:
v_mfma_f32_16x16x32_bf16 v[26:29],a[8:11],v[42:45],0
s_add_u32 m0,s59,3072
R 62,1,5120
v_add_f32_dpp v115,v114,v114 quad_perm:[1,0,3,2] row_mask:0xf bank_mask:0xf
M 26,a[20:23],46
buffer_load_dword v8,s[36:39],s58 offen offset:0 sc1 lds
v_mfma_f32_16x16x32_bf16 v[22:25],a[4:7],v[42:45],0
s_mov_b32 s66,s65
v_add_f32_dpp v115,v115,v115 quad_perm:[2,3,0,1] row_mask:0xf bank_mask:0xf
v_fma_f32 v30,-v78,v82,v30
M 22,a[16:19],46
s_add_u32 s65,s65,s62
R 66,1,6144
v_fma_f32 v31,-v78,v83,v31
v_mfma_f32_16x16x32_bf16 v[18:21],a[0:3],v[42:45],0
v_add_f32_dpp v118,v115,v115 row_half_mirror row_mask:0xf bank_mask:0xf
v_fma_f32 v32,-v78,v84,v32
v_fma_f32 v33,-v78,v85,v33
M 18,a[12:15],46
L 3
v_mul_f32 v30,v79,v30
v_mul_f32 v31,v79,v31
M 18,a[24:27],50
R 70,1,7168
v_mov_b32_dpp v115,v118 row_ror:8 row_mask:0xf bank_mask:0xf
M 18,a[36:39],54
v_fma_f32 v115,-v118,v118,v115
v_max_f32 v115,s87,v115
M 22,a[28:31],50
v_rsq_f32 v116,v115
M 22,a[40:43],54
R 42,1,8192
v_mul_f32 v118,v15,v118
v_fma_f32 v118,v116,v16,v118
M 26,a[32:35],50
ds_write_b32 v12,v118 offset:0
M 26,a[44:47],54
L 4
v_mul_f32 v32,v79,v32
v_mul_f32 v33,v79,v33
M 26,a[56:59],58
R 46,1,9216
v_fma_f32 v94,v30,v30,s88
M 26,a[68:71],62
v_fma_f32 v95,v31,v31,s88
v_fma_f32 v96,v32,v32,s88
M 22,a[52:55],58
v_fma_f32 v97,v33,v33,s88
v_mul_f32 v94,v30,v94
v_mul_f32 v95,v31,v95
M 22,a[64:67],62
R 50,1,10240
v_mul_f32 v96,v32,v96
M 18,a[48:51],58
v_exp_f32 v94,v94
M 18,a[60:63],62
L 4
v_exp_f32 v95,v95
M 18,a[72:75],66
R 54,1,11264
v_mul_f32 v97,v33,v97
M 18,a[84:87],70
v_exp_f32 v96,v96
v_fma_f32 v94,v94,s89,s89
M 22,a[76:79],66
v_exp_f32 v97,v97
M 22,a[88:91],70
R 58,1,12288
v_fma_f32 v95,v95,s89,s89
M 26,a[80:83],66
v_rcp_f32 v94,v94
M 26,a[92:95],70
L 3
v_rcp_f32 v95,v95
M 26,a[104:107],42
R 62,1,13312
v_fma_f32 v96,v96,s89,s89
v_fma_f32 v97,v97,s89,s89
M 26,a[116:119],46
v_rcp_f32 v96,v96
M 22,a[100:103],42
v_rcp_f32 v97,v97
M 22,a[112:115],46
R 66,1,14336
v_mul_f32 v30,v30,v94
M 18,a[96:99],42
v_mul_f32 v31,v31,v95
v_mul_f32 v32,v32,v96
M 18,a[108:111],46
L 3
v_mul_f32 v33,v33,v97
v_cvt_pk_bf16_f32 v106,v30,v31
v_cvt_pk_bf16_f32 v107,v32,v33
M 18,a[120:123],50
R 70,1,15360
v_fma_f32 v34,-v78,v86,v34
M 18,a[132:135],54
s_waitcnt vmcnt(0)
v_fma_f32 v35,-v78,v87,v35
v_fma_f32 v36,-v78,v88,v36
M 22,a[124:127],50
s_barrier
ds_read_b32 v7,v6 offset:3072
v_fma_f32 v37,-v78,v89,v37
M 22,a[136:139],54
R 42,1,16384
v_mul_f32 v34,v79,v34
M 26,a[128:131],50
buffer_store_dword v4,v3,s[36:39],s70 offen offset:0 sc0 sc1
v_mul_f32 v35,v79,v35
M 26,a[140:143],54
L 4
v_mul_f32 v36,v79,v36
v_mul_f32 v37,v79,v37
M 26,a[152:155],58
s_mov_b32 s70,0x7fff0000
R 46,1,17408
v_fma_f32 v98,v34,v34,s88
M 26,a[164:167],62
v_fma_f32 v99,v35,v35,s88
v_fma_f32 v100,v36,v36,s88
M 22,a[148:151],58
L 2
v_cmp_eq_u32 vcc,s53,v7
v_fma_f32 v101,v37,v37,s88
M 22,a[160:163],62
R 50,1,18432
v_mul_f32 v98,v34,v98
v_mul_f32 v99,v35,v99
M 18,a[144:147],58
v_exp_f32 v98,v98
M 18,a[156:159],62
s_cmp_eq_u32 vcc_lo,-1
s_nop 0
v_mul_f32 v100,v36,v100
s_cbranch_scc0 Lg3_X0_%=
M 18,a[168:171],66
s_add_u32 m0,s57,0
s_add_u32 s56,s55,0
s_add_u32 s58,s58,s61
R 54,1,19456
M 18,a[180:183],70
buffer_load_dword v114,v11,s[36:39],s73 offen offset:0 sc1
M 22,a[172:175],66
D 0,56,0
v_mul_f32 v101,v37,v101
M 22,a[184:187],70
s_add_u32 s54,s54,s46
R 58,1,20480
v_fma_f32 v98,v98,s89,s89
M 26,a[176:179],66
s_add_u32 s73,s73,s64
v_exp_f32 v99,v99
M 26,a[188:191],70
L 3
v_exp_f32 v100,v100
M 26,a[200:203],42
R 62,1,21504
v_fma_f32 v99,v99,s89,s89
M 26,a[212:215],46
D 0,56,1024
v_fma_f32 v100,v100,s89,s89
M 22,a[196:199],42
v_exp_f32 v101,v101
M 22,a[208:211],46
R 66,1,22528
v_fma_f32 v101,v101,s89,s89
M 18,a[192:195],42
D 0,56,2048
M 18,a[204:207],46
L 3
v_rcp_f32 v98,v98
M 18,a[216:219],50
R 70,1,23552
v_rcp_f32 v99,v99
M 18,a[228:231],54
v_rcp_f32 v100,v100
M 22,a[220:223],50
D 0,56,3072
M 22,a[232:235],54
s_add_u32 m0,s59,0
R 42,1,24576
v_mul_f32 v34,v34,v98
M 26,a[224:227],50
buffer_load_dword v8,s[36:39],s58 offen offset:0 sc1 lds
M 26,a[236:239],54
L 3
v_rcp_f32 v101,v101
v_mul_f32 v35,v35,v99
M 26,a[248:251],58
s_add_u32 m0,s57,16384
R 46,1,25600
v_mul_f32 v36,v36,v100
M 26,v[132:135],62
s_add_u32 s56,s55,16384
v_mul_f32 v37,v37,v101
v_cvt_pk_bf16_f32 v108,v34,v35
M 22,a[244:247],58
D 0,56,0
M 22,v[128:131],62
s_add_u32 s55,s55,s60
R 50,1,26624
v_cvt_pk_bf16_f32 v109,v36,v37
M 18,a[240:243],58
buffer_store_dwordx4 v[106:109],v0,s[36:39],s67 offen offset:0 sc0 sc1
v_fma_f32 v38,-v78,v90,v38
M 18,a[252:255],62
L 3
v_fma_f32 v39,-v78,v91,v39
v_fma_f32 v40,-v78,v92,v40
M 18,v[136:139],66
s_sub_u32 s57,s84,s57
)"
R"(R 54,1,27648
v_fma_f32 v41,-v78,v93,v41
M 18,v[148:151],70
D 0,56,1024
M 22,v[140:143],66
v_mul_f32 v38,v79,v38
v_mul_f32 v39,v79,v39
M 22,v[152:155],70
R 58,1,28672
v_mul_f32 v40,v79,v40
v_mul_f32 v41,v79,v41
M 26,v[144:147],66
v_fma_f32 v102,v38,v38,s88
v_fma_f32 v103,v39,v39,s88
M 26,v[156:159],70
L 3
v_fma_f32 v104,v40,v40,s88
v_fma_f32 v105,v41,v41,s88
M 26,v[168:171],42
R 62,1,29696
v_mul_f32 v102,v38,v102
M 26,v[180:183],46
D 0,56,2048
M 22,v[164:167],42
v_exp_f32 v102,v102
v_mul_f32 v103,v39,v103
M 22,v[176:179],46
R 66,1,30720
v_mul_f32 v104,v40,v104
M 18,v[160:163],42
v_exp_f32 v103,v103
M 18,v[172:175],46
L 3
D 0,56,3072
M 18,v[184:187],50
s_nop 0
R 70,1,31744
v_mul_f32 v105,v41,v105
M 18,v[196:199],54
s_waitcnt vmcnt(5)
v_exp_f32 v104,v104
v_fma_f32 v102,v102,s89,s89
M 22,v[188:191],50
s_barrier
buffer_store_dword v4,v3,s[36:39],s70 offen offset:0 sc0 sc1
M 22,v[200:203],54
s_mov_b32 s70,s69
R 42,2,0
ds_read_b32 v7,v6 offset:0
M 26,v[192:195],50
s_mov_b32 s69,s68
ds_read_b64 v[74:75],v13 offset:0
v_fma_f32 v103,v103,s89,s89
M 26,v[204:207],54
L 5
v_exp_f32 v105,v105
M 26,v[216:219],58
s_add_u32 s68,s68,s63
R 46,2,1024
v_rcp_f32 v102,v102
M 26,v[228:231],62
v_rcp_f32 v103,v103
M 22,v[212:215],58
v_fma_f32 v104,v104,s89,s89
v_fma_f32 v105,v105,s89,s89
M 22,v[224:227],62
L 2
R 50,2,2048
v_mul_f32 v38,v38,v102
M 18,v[208:211],58
v_rcp_f32 v104,v104
M 18,v[220:223],62
s_nop 0
v_rcp_f32 v105,v105
v_mul_f32 v39,v39,v103
M 18,v[232:235],66
R 54,2,3072
v_mul_f32 v40,v40,v104
M 18,v[244:247],70
v_mul_f32 v41,v41,v105
v_cmp_eq_u32 vcc,s53,v7
M 22,v[236:239],66
v_cvt_pk_bf16_f32 v110,v38,v39
v_cvt_pk_bf16_f32 v111,v40,v41
M 22,v[248:251],70
s_cmp_eq_u32 vcc_lo,-1
R 58,2,4096
M 26,v[240:243],66
L 3
buffer_store_dwordx2 v[110:111],v10,s[36:39],s67 offen offset:0 sc0 sc1
M 26,v[252:255],70
buffer_store_dword v4,v3,s[36:39],s71 offen offset:0 sc0 sc1
s_cbranch_scc1 Lg3_F1_%=
s_branch Lg3_K1_%=
Lg3_X0_%=:
M 18,a[168:171],66
s_add_u32 m0,s59,0
R 54,1,19456
v_mul_f32 v101,v37,v101
M 18,a[180:183],70
buffer_load_dword v8,s[36:39],s58 offen offset:0 sc1 lds
M 22,a[172:175],66
v_exp_f32 v99,v99
M 22,a[184:187],70
R 58,1,20480
v_fma_f32 v98,v98,s89,s89
M 26,a[176:179],66
v_exp_f32 v100,v100
M 26,a[188:191],70
L 3
v_exp_f32 v101,v101
M 26,a[200:203],42
R 62,1,21504
v_fma_f32 v99,v99,s89,s89
M 26,a[212:215],46
v_rcp_f32 v98,v98
M 22,a[196:199],42
v_rcp_f32 v99,v99
M 22,a[208:211],46
R 66,1,22528
v_fma_f32 v100,v100,s89,s89
M 18,a[192:195],42
v_rcp_f32 v100,v100
M 18,a[204:207],46
L 3
v_fma_f32 v101,v101,s89,s89
v_mul_f32 v34,v34,v98
M 18,a[216:219],50
R 70,1,23552
v_mul_f32 v35,v35,v99
M 18,a[228:231],54
v_rcp_f32 v101,v101
M 22,a[220:223],50
v_mul_f32 v36,v36,v100
v_mul_f32 v37,v37,v101
M 22,a[232:235],54
R 42,1,24576
v_cvt_pk_bf16_f32 v108,v34,v35
M 26,a[224:227],50
v_cvt_pk_bf16_f32 v109,v36,v37
v_fma_f32 v38,-v78,v90,v38
M 26,a[236:239],54
L 3
buffer_store_dwordx4 v[106:109],v0,s[36:39],s67 offen offset:0 sc0 sc1
v_fma_f32 v39,-v78,v91,v39
M 26,a[248:251],58
R 46,1,25600
v_fma_f32 v40,-v78,v92,v40
M 26,v[132:135],62
v_fma_f32 v41,-v78,v93,v41
v_mul_f32 v38,v79,v38
M 22,a[244:247],58
v_mul_f32 v39,v79,v39
v_mul_f32 v40,v79,v40
M 22,v[128:131],62
R 50,1,26624
v_mul_f32 v41,v79,v41
M 18,a[240:243],58
v_fma_f32 v102,v38,v38,s88
v_fma_f32 v103,v39,v39,s88
M 18,a[252:255],62
L 3
v_fma_f32 v104,v40,v40,s88
v_fma_f32 v105,v41,v41,s88
M 18,v[136:139],66
R 54,1,27648
v_mul_f32 v102,v38,v102
M 18,v[148:151],70
v_exp_f32 v102,v102
M 22,v[140:143],66
v_mul_f32 v103,v39,v103
v_mul_f32 v104,v40,v104
M 22,v[152:155],70
R 58,1,28672
v_mul_f32 v105,v41,v105
M 26,v[144:147],66
v_exp_f32 v103,v103
M 26,v[156:159],70
L 3
v_exp_f32 v104,v104
M 26,v[168:171],42
R 62,1,29696
v_fma_f32 v102,v102,s89,s89
M 26,v[180:183],46
v_exp_f32 v105,v105
M 22,v[164:167],42
v_rcp_f32 v102,v102
M 22,v[176:179],46
R 66,1,30720
v_fma_f32 v103,v103,s89,s89
M 18,v[160:163],42
v_rcp_f32 v103,v103
M 18,v[172:175],46
L 3
v_fma_f32 v104,v104,s89,s89
v_fma_f32 v105,v105,s89,s89
M 18,v[184:187],50
s_nop 0
R 70,1,31744
v_mul_f32 v38,v38,v102
M 18,v[196:199],54
s_waitcnt vmcnt(1)
v_rcp_f32 v104,v104
M 22,v[188:191],50
s_barrier
buffer_store_dword v4,v3,s[36:39],s70 offen offset:0 sc0 sc1
M 22,v[200:203],54
s_mov_b32 s70,s69
ds_read_b32 v7,v6 offset:0
ds_read_b64 v[74:75],v13 offset:0
M 26,v[192:195],50
s_mov_b32 s69,s68
v_rcp_f32 v105,v105
M 26,v[204:207],54
L 4
v_mul_f32 v39,v39,v103
v_mul_f32 v40,v40,v104
M 26,v[216:219],58
s_add_u32 s68,s68,s63
v_mul_f32 v41,v41,v105
v_cvt_pk_bf16_f32 v110,v38,v39
M 26,v[228:231],62
v_cvt_pk_bf16_f32 v111,v40,v41
M 22,v[212:215],58
buffer_store_dwordx2 v[110:111],v10,s[36:39],s67 offen offset:0 sc0 sc1
M 22,v[224:227],62
L 1
v_cmp_eq_u32 vcc,s53,v7
M 18,v[208:211],58
M 18,v[220:223],62
s_nop 0
M 18,v[232:235],66
M 18,v[244:247],70
M 22,v[236:239],66
M 22,v[248:251],70
M 26,v[240:243],66
M 26,v[252:255],70
s_mov_b32 s80,1
s_cmp_eq_u32 vcc_lo,-1
s_cbranch_scc0 Lg3_xn0_%=
buffer_load_dword v114,v11,s[36:39],s73 offen offset:0 sc1
L 0
v_fma_f32 v18,-v74,v82,v18
v_fma_f32 v19,-v74,v83,v19
v_fma_f32 v20,-v74,v84,v20
v_fma_f32 v21,-v74,v85,v21
v_mul_f32 v18,v75,v18
v_mul_f32 v19,v75,v19
v_mul_f32 v20,v75,v20
v_mul_f32 v21,v75,v21
s_add_u32 s73,s73,s64
s_add_u32 m0,s57,0
s_add_u32 s56,s55,0
D 0,56,0
v_fma_f32 v94,v18,v18,s88
v_fma_f32 v95,v19,v19,s88
v_fma_f32 v96,v20,v20,s88
v_fma_f32 v97,v21,v21,s88
v_mul_f32 v94,v18,v94
v_mul_f32 v95,v19,v95
v_mul_f32 v96,v20,v96
v_mul_f32 v97,v21,v97
D 0,56,1024
v_exp_f32 v94,v94
v_exp_f32 v95,v95
v_exp_f32 v96,v96
v_exp_f32 v97,v97
v_fma_f32 v94,v94,s89,s89
v_fma_f32 v95,v95,s89,s89
v_fma_f32 v96,v96,s89,s89
v_fma_f32 v97,v97,s89,s89
D 0,56,2048
v_rcp_f32 v94,v94
v_rcp_f32 v95,v95
v_rcp_f32 v96,v96
v_rcp_f32 v97,v97
v_mul_f32 v18,v18,v94
v_mul_f32 v19,v19,v95
v_mul_f32 v20,v20,v96
v_mul_f32 v21,v21,v97
D 0,56,3072
v_cvt_pk_bf16_f32 v106,v18,v19
v_cvt_pk_bf16_f32 v107,v20,v21
v_fma_f32 v22,-v74,v86,v22
v_fma_f32 v23,-v74,v87,v23
v_fma_f32 v24,-v74,v88,v24
v_fma_f32 v25,-v74,v89,v25
v_mul_f32 v22,v75,v22
v_mul_f32 v23,v75,v23
s_add_u32 s58,s58,s61
s_add_u32 m0,s57,16384
s_add_u32 s56,s55,16384
D 0,56,0
v_mul_f32 v24,v75,v24
v_mul_f32 v25,v75,v25
v_fma_f32 v98,v22,v22,s88
v_fma_f32 v99,v23,v23,s88
v_fma_f32 v100,v24,v24,s88
v_fma_f32 v101,v25,v25,s88
v_mul_f32 v98,v22,v98
v_mul_f32 v99,v23,v99
D 0,56,1024
v_mul_f32 v100,v24,v100
v_mul_f32 v101,v25,v101
v_exp_f32 v98,v98
v_exp_f32 v99,v99
v_exp_f32 v100,v100
v_exp_f32 v101,v101
v_fma_f32 v98,v98,s89,s89
v_fma_f32 v99,v99,s89,s89
D 0,56,2048
v_fma_f32 v100,v100,s89,s89
v_fma_f32 v101,v101,s89,s89
v_rcp_f32 v98,v98
v_rcp_f32 v99,v99
v_rcp_f32 v100,v100
v_rcp_f32 v101,v101
v_mul_f32 v22,v22,v98
v_mul_f32 v23,v23,v99
D 0,56,3072
v_mul_f32 v24,v24,v100
v_mul_f32 v25,v25,v101
v_cvt_pk_bf16_f32 v108,v22,v23
v_cvt_pk_bf16_f32 v109,v24,v25
buffer_store_dwordx4 v[106:109],v0,s[36:39],s66 offen offset:0 sc0 sc1
v_fma_f32 v26,-v74,v90,v26
v_fma_f32 v27,-v74,v91,v27
v_fma_f32 v28,-v74,v92,v28
v_fma_f32 v29,-v74,v93,v29
s_add_u32 s55,s55,s60
s_sub_u32 s57,s84,s57
s_add_u32 s54,s54,s46
v_mul_f32 v26,v75,v26
v_mul_f32 v27,v75,v27
v_mul_f32 v28,v75,v28
v_mul_f32 v29,v75,v29
v_fma_f32 v102,v26,v26,s88
v_fma_f32 v103,v27,v27,s88
v_fma_f32 v104,v28,v28,s88
v_fma_f32 v105,v29,v29,s88
v_mul_f32 v102,v26,v102
v_mul_f32 v103,v27,v103
v_mul_f32 v104,v28,v104
v_mul_f32 v105,v29,v105
v_exp_f32 v102,v102
v_exp_f32 v103,v103
v_exp_f32 v104,v104
v_exp_f32 v105,v105
v_fma_f32 v102,v102,s89,s89
v_fma_f32 v103,v103,s89,s89
v_fma_f32 v104,v104,s89,s89
v_fma_f32 v105,v105,s89,s89
v_rcp_f32 v102,v102
v_rcp_f32 v103,v103
v_rcp_f32 v104,v104
v_rcp_f32 v105,v105
v_mul_f32 v26,v26,v102
v_mul_f32 v27,v27,v103
v_mul_f32 v28,v28,v104
v_mul_f32 v29,v29,v105
v_cvt_pk_bf16_f32 v110,v26,v27
v_cvt_pk_bf16_f32 v111,v28,v29
buffer_store_dwordx2 v[110:111],v10,s[36:39],s66 offen offset:0 sc0 sc1
s_mov_b32 s72,s69
s_mov_b32 s69,0x7fff0000
s_mov_b32 s66,0x7fff0000
s_mov_b32 s76,0x7fff0000
s_branch Lg3_gox_%=
Lg3_xn0_%=:
s_cmp_ge_u32 s54,256
s_cbranch_scc1 Lg3_fin0_%=
s_add_u32 m0,s59,4096
s_nop 0
buffer_load_dword v8,s[36:39],s58 offen offset:0 sc1 lds
buffer_store_dword v4,v3,s[36:39],s71 offen offset:0 sc0 sc1
s_add_u32 m0,s59,5120
s_nop 0
buffer_load_dword v8,s[36:39],s58 offen offset:0 sc1 lds
buffer_store_dword v4,v3,s[36:39],s71 offen offset:0 sc0 sc1
s_mov_b32 s83,0
L 0
v_fma_f32 v18,-v74,v82,v18
v_fma_f32 v19,-v74,v83,v19
v_fma_f32 v20,-v74,v84,v20
v_fma_f32 v21,-v74,v85,v21
v_mul_f32 v18,v75,v18
v_mul_f32 v19,v75,v19
v_mul_f32 v20,v75,v20
v_mul_f32 v21,v75,v21
v_fma_f32 v94,v18,v18,s88
v_fma_f32 v95,v19,v19,s88
v_fma_f32 v96,v20,v20,s88
v_fma_f32 v97,v21,v21,s88
v_mul_f32 v94,v18,v94
v_mul_f32 v95,v19,v95
v_mul_f32 v96,v20,v96
v_mul_f32 v97,v21,v97
v_exp_f32 v94,v94
v_exp_f32 v95,v95
v_exp_f32 v96,v96
v_exp_f32 v97,v97
v_fma_f32 v94,v94,s89,s89
v_fma_f32 v95,v95,s89,s89
v_fma_f32 v96,v96,s89,s89
v_fma_f32 v97,v97,s89,s89
v_rcp_f32 v94,v94
v_rcp_f32 v95,v95
v_rcp_f32 v96,v96
v_rcp_f32 v97,v97
v_mul_f32 v18,v18,v94
v_mul_f32 v19,v19,v95
v_mul_f32 v20,v20,v96
v_mul_f32 v21,v21,v97
v_cvt_pk_bf16_f32 v106,v18,v19
v_cvt_pk_bf16_f32 v107,v20,v21
v_fma_f32 v22,-v74,v86,v22
v_fma_f32 v23,-v74,v87,v23
v_fma_f32 v24,-v74,v88,v24
v_fma_f32 v25,-v74,v89,v25
v_mul_f32 v22,v75,v22
v_mul_f32 v23,v75,v23
v_mul_f32 v24,v75,v24
v_mul_f32 v25,v75,v25
v_fma_f32 v98,v22,v22,s88
v_fma_f32 v99,v23,v23,s88
v_fma_f32 v100,v24,v24,s88
v_fma_f32 v101,v25,v25,s88
v_mul_f32 v98,v22,v98
v_mul_f32 v99,v23,v99
v_mul_f32 v100,v24,v100
v_mul_f32 v101,v25,v101
v_exp_f32 v98,v98
v_exp_f32 v99,v99
v_exp_f32 v100,v100
v_exp_f32 v101,v101
v_fma_f32 v98,v98,s89,s89
v_fma_f32 v99,v99,s89,s89
v_fma_f32 v100,v100,s89,s89
v_fma_f32 v101,v101,s89,s89
v_rcp_f32 v98,v98
v_rcp_f32 v99,v99
v_rcp_f32 v100,v100
v_rcp_f32 v101,v101
v_mul_f32 v22,v22,v98
v_mul_f32 v23,v23,v99
v_mul_f32 v24,v24,v100
v_mul_f32 v25,v25,v101
v_cvt_pk_bf16_f32 v108,v22,v23
v_cvt_pk_bf16_f32 v109,v24,v25
buffer_store_dwordx4 v[106:109],v0,s[36:39],s66 offen offset:0 sc0 sc1
v_fma_f32 v26,-v74,v90,v26
v_fma_f32 v27,-v74,v91,v27
v_fma_f32 v28,-v74,v92,v28
v_fma_f32 v29,-v74,v93,v29
v_mul_f32 v26,v75,v26
v_mul_f32 v27,v75,v27
v_mul_f32 v28,v75,v28
v_mul_f32 v29,v75,v29
v_fma_f32 v102,v26,v26,s88
v_fma_f32 v103,v27,v27,s88
v_fma_f32 v104,v28,v28,s88
v_fma_f32 v105,v29,v29,s88
v_mul_f32 v102,v26,v102
v_mul_f32 v103,v27,v103
v_mul_f32 v104,v28,v104
v_mul_f32 v105,v29,v105
v_exp_f32 v102,v102
v_exp_f32 v103,v103
v_exp_f32 v104,v104
v_exp_f32 v105,v105
v_fma_f32 v102,v102,s89,s89
v_fma_f32 v103,v103,s89,s89
v_fma_f32 v104,v104,s89,s89
v_fma_f32 v105,v105,s89,s89
v_rcp_f32 v102,v102
v_rcp_f32 v103,v103
v_rcp_f32 v104,v104
v_rcp_f32 v105,v105
v_mul_f32 v26,v26,v102
v_mul_f32 v27,v27,v103
v_mul_f32 v28,v28,v104
v_mul_f32 v29,v29,v105
v_cvt_pk_bf16_f32 v110,v26,v27
v_cvt_pk_bf16_f32 v111,v28,v29
buffer_store_dwordx2 v[110:111],v10,s[36:39],s66 offen offset:0 sc0 sc1
s_mov_b32 s72,s69
s_mov_b32 s69,0x7fff0000
s_mov_b32 s66,0x7fff0000
s_mov_b32 s76,0x7fff0000
s_branch Lg3_poll_%=
Lg3_fin0_%=:
L 0
v_fma_f32 v18,-v74,v82,v18
v_fma_f32 v19,-v74,v83,v19
v_fma_f32 v20,-v74,v84,v20
v_fma_f32 v21,-v74,v85,v21
v_mul_f32 v18,v75,v18
v_mul_f32 v19,v75,v19
v_mul_f32 v20,v75,v20
v_mul_f32 v21,v75,v21
)"
R"(v_fma_f32 v94,v18,v18,s88
v_fma_f32 v95,v19,v19,s88
v_fma_f32 v96,v20,v20,s88
v_fma_f32 v97,v21,v21,s88
v_mul_f32 v94,v18,v94
v_mul_f32 v95,v19,v95
v_mul_f32 v96,v20,v96
v_mul_f32 v97,v21,v97
v_exp_f32 v94,v94
v_exp_f32 v95,v95
v_exp_f32 v96,v96
v_exp_f32 v97,v97
v_fma_f32 v94,v94,s89,s89
v_fma_f32 v95,v95,s89,s89
v_fma_f32 v96,v96,s89,s89
v_fma_f32 v97,v97,s89,s89
v_rcp_f32 v94,v94
v_rcp_f32 v95,v95
v_rcp_f32 v96,v96
v_rcp_f32 v97,v97
v_mul_f32 v18,v18,v94
v_mul_f32 v19,v19,v95
v_mul_f32 v20,v20,v96
v_mul_f32 v21,v21,v97
v_cvt_pk_bf16_f32 v106,v18,v19
v_cvt_pk_bf16_f32 v107,v20,v21
v_fma_f32 v22,-v74,v86,v22
v_fma_f32 v23,-v74,v87,v23
v_fma_f32 v24,-v74,v88,v24
v_fma_f32 v25,-v74,v89,v25
v_mul_f32 v22,v75,v22
v_mul_f32 v23,v75,v23
v_mul_f32 v24,v75,v24
v_mul_f32 v25,v75,v25
v_fma_f32 v98,v22,v22,s88
v_fma_f32 v99,v23,v23,s88
v_fma_f32 v100,v24,v24,s88
v_fma_f32 v101,v25,v25,s88
v_mul_f32 v98,v22,v98
v_mul_f32 v99,v23,v99
v_mul_f32 v100,v24,v100
v_mul_f32 v101,v25,v101
v_exp_f32 v98,v98
v_exp_f32 v99,v99
v_exp_f32 v100,v100
v_exp_f32 v101,v101
v_fma_f32 v98,v98,s89,s89
v_fma_f32 v99,v99,s89,s89
v_fma_f32 v100,v100,s89,s89
v_fma_f32 v101,v101,s89,s89
v_rcp_f32 v98,v98
v_rcp_f32 v99,v99
v_rcp_f32 v100,v100
v_rcp_f32 v101,v101
v_mul_f32 v22,v22,v98
v_mul_f32 v23,v23,v99
v_mul_f32 v24,v24,v100
v_mul_f32 v25,v25,v101
v_cvt_pk_bf16_f32 v108,v22,v23
v_cvt_pk_bf16_f32 v109,v24,v25
buffer_store_dwordx4 v[106:109],v0,s[36:39],s66 offen offset:0 sc0 sc1
v_fma_f32 v26,-v74,v90,v26
v_fma_f32 v27,-v74,v91,v27
v_fma_f32 v28,-v74,v92,v28
v_fma_f32 v29,-v74,v93,v29
v_mul_f32 v26,v75,v26
v_mul_f32 v27,v75,v27
v_mul_f32 v28,v75,v28
v_mul_f32 v29,v75,v29
v_fma_f32 v102,v26,v26,s88
v_fma_f32 v103,v27,v27,s88
v_fma_f32 v104,v28,v28,s88
v_fma_f32 v105,v29,v29,s88
v_mul_f32 v102,v26,v102
v_mul_f32 v103,v27,v103
v_mul_f32 v104,v28,v104
v_mul_f32 v105,v29,v105
v_exp_f32 v102,v102
v_exp_f32 v103,v103
v_exp_f32 v104,v104
v_exp_f32 v105,v105
v_fma_f32 v102,v102,s89,s89
v_fma_f32 v103,v103,s89,s89
v_fma_f32 v104,v104,s89,s89
v_fma_f32 v105,v105,s89,s89
v_rcp_f32 v102,v102
v_rcp_f32 v103,v103
v_rcp_f32 v104,v104
v_rcp_f32 v105,v105
v_mul_f32 v26,v26,v102
v_mul_f32 v27,v27,v103
v_mul_f32 v28,v28,v104
v_mul_f32 v29,v29,v105
v_cvt_pk_bf16_f32 v110,v26,v27
v_cvt_pk_bf16_f32 v111,v28,v29
buffer_store_dwordx2 v[110:111],v10,s[36:39],s66 offen offset:0 sc0 sc1
s_mov_b32 s72,s69
s_mov_b32 s69,0x7fff0000
s_mov_b32 s66,0x7fff0000
s_mov_b32 s76,0x7fff0000
s_branch Lg3_done_%=
Lg3_K1_%=:
Lg3_L1_%=:
v_mfma_f32_16x16x32_bf16 v[38:41],a[8:11],v[42:45],0
s_add_u32 m0,s59,3072
R 62,2,5120
v_add_f32_dpp v115,v114,v114 quad_perm:[1,0,3,2] row_mask:0xf bank_mask:0xf
M 38,a[20:23],46
buffer_load_dword v8,s[36:39],s58 offen offset:0 sc1 lds
v_mfma_f32_16x16x32_bf16 v[34:37],a[4:7],v[42:45],0
s_mov_b32 s67,s65
v_add_f32_dpp v115,v115,v115 quad_perm:[2,3,0,1] row_mask:0xf bank_mask:0xf
v_fma_f32 v18,-v74,v82,v18
M 34,a[16:19],46
s_add_u32 s65,s65,s62
R 66,2,6144
v_fma_f32 v19,-v74,v83,v19
v_mfma_f32_16x16x32_bf16 v[30:33],a[0:3],v[42:45],0
v_add_f32_dpp v118,v115,v115 row_half_mirror row_mask:0xf bank_mask:0xf
v_fma_f32 v20,-v74,v84,v20
v_fma_f32 v21,-v74,v85,v21
M 30,a[12:15],46
L 3
v_mul_f32 v18,v75,v18
v_mul_f32 v19,v75,v19
M 30,a[24:27],50
R 70,2,7168
v_mov_b32_dpp v115,v118 row_ror:8 row_mask:0xf bank_mask:0xf
M 30,a[36:39],54
v_fma_f32 v115,-v118,v118,v115
v_max_f32 v115,s87,v115
M 34,a[28:31],50
v_rsq_f32 v116,v115
M 34,a[40:43],54
R 42,2,8192
v_mul_f32 v118,v15,v118
v_fma_f32 v118,v116,v16,v118
M 38,a[32:35],50
ds_write_b32 v12,v118 offset:2048
M 38,a[44:47],54
L 4
v_mul_f32 v20,v75,v20
v_mul_f32 v21,v75,v21
M 38,a[56:59],58
R 46,2,9216
v_fma_f32 v94,v18,v18,s88
M 38,a[68:71],62
v_fma_f32 v95,v19,v19,s88
v_fma_f32 v96,v20,v20,s88
M 34,a[52:55],58
v_fma_f32 v97,v21,v21,s88
v_mul_f32 v94,v18,v94
v_mul_f32 v95,v19,v95
M 34,a[64:67],62
R 50,2,10240
v_mul_f32 v96,v20,v96
M 30,a[48:51],58
v_exp_f32 v94,v94
M 30,a[60:63],62
L 4
v_exp_f32 v95,v95
M 30,a[72:75],66
R 54,2,11264
v_mul_f32 v97,v21,v97
M 30,a[84:87],70
v_exp_f32 v96,v96
v_fma_f32 v94,v94,s89,s89
M 34,a[76:79],66
v_exp_f32 v97,v97
M 34,a[88:91],70
R 58,2,12288
v_fma_f32 v95,v95,s89,s89
M 38,a[80:83],66
v_rcp_f32 v94,v94
M 38,a[92:95],70
L 3
v_rcp_f32 v95,v95
M 38,a[104:107],42
R 62,2,13312
v_fma_f32 v96,v96,s89,s89
v_fma_f32 v97,v97,s89,s89
M 38,a[116:119],46
v_rcp_f32 v96,v96
M 34,a[100:103],42
v_rcp_f32 v97,v97
M 34,a[112:115],46
R 66,2,14336
v_mul_f32 v18,v18,v94
M 30,a[96:99],42
v_mul_f32 v19,v19,v95
v_mul_f32 v20,v20,v96
M 30,a[108:111],46
L 3
v_mul_f32 v21,v21,v97
v_cvt_pk_bf16_f32 v106,v18,v19
v_cvt_pk_bf16_f32 v107,v20,v21
M 30,a[120:123],50
R 70,2,15360
v_fma_f32 v22,-v74,v86,v22
M 30,a[132:135],54
s_waitcnt vmcnt(0)
v_fma_f32 v23,-v74,v87,v23
v_fma_f32 v24,-v74,v88,v24
M 34,a[124:127],50
s_barrier
ds_read_b32 v7,v6 offset:3072
v_fma_f32 v25,-v74,v89,v25
M 34,a[136:139],54
R 42,2,16384
v_mul_f32 v22,v75,v22
M 38,a[128:131],50
buffer_store_dword v4,v3,s[36:39],s70 offen offset:0 sc0 sc1
v_mul_f32 v23,v75,v23
M 38,a[140:143],54
L 4
v_mul_f32 v24,v75,v24
v_mul_f32 v25,v75,v25
M 38,a[152:155],58
s_mov_b32 s70,0x7fff0000
R 46,2,17408
v_fma_f32 v98,v22,v22,s88
M 38,a[164:167],62
v_fma_f32 v99,v23,v23,s88
v_fma_f32 v100,v24,v24,s88
M 34,a[148:151],58
L 2
v_cmp_eq_u32 vcc,s53,v7
v_fma_f32 v101,v25,v25,s88
M 34,a[160:163],62
R 50,2,18432
v_mul_f32 v98,v22,v98
v_mul_f32 v99,v23,v99
M 30,a[144:147],58
v_exp_f32 v98,v98
M 30,a[156:159],62
s_cmp_eq_u32 vcc_lo,-1
s_nop 0
v_mul_f32 v100,v24,v100
s_cbranch_scc0 Lg3_X1_%=
M 30,a[168:171],66
s_add_u32 m0,s57,0
s_add_u32 s56,s55,0
s_add_u32 s58,s58,s61
R 54,2,19456
M 30,a[180:183],70
buffer_load_dword v114,v11,s[36:39],s73 offen offset:0 sc1
M 34,a[172:175],66
D 0,56,0
v_mul_f32 v101,v25,v101
M 34,a[184:187],70
s_add_u32 s54,s54,s46
R 58,2,20480
v_fma_f32 v98,v98,s89,s89
M 38,a[176:179],66
s_add_u32 s73,s73,s64
v_exp_f32 v99,v99
M 38,a[188:191],70
L 3
v_exp_f32 v100,v100
M 38,a[200:203],42
R 62,2,21504
v_fma_f32 v99,v99,s89,s89
M 38,a[212:215],46
D 0,56,1024
v_fma_f32 v100,v100,s89,s89
M 34,a[196:199],42
v_exp_f32 v101,v101
M 34,a[208:211],46
R 66,2,22528
v_fma_f32 v101,v101,s89,s89
M 30,a[192:195],42
D 0,56,2048
M 30,a[204:207],46
L 3
v_rcp_f32 v98,v98
M 30,a[216:219],50
R 70,2,23552
v_rcp_f32 v99,v99
M 30,a[228:231],54
v_rcp_f32 v100,v100
M 34,a[220:223],50
D 0,56,3072
M 34,a[232:235],54
s_add_u32 m0,s59,1024
R 42,2,24576
v_mul_f32 v22,v22,v98
M 38,a[224:227],50
buffer_load_dword v8,s[36:39],s58 offen offset:0 sc1 lds
M 38,a[236:239],54
L 3
v_rcp_f32 v101,v101
v_mul_f32 v23,v23,v99
M 38,a[248:251],58
s_add_u32 m0,s57,16384
R 46,2,25600
v_mul_f32 v24,v24,v100
M 38,v[132:135],62
s_add_u32 s56,s55,16384
v_mul_f32 v25,v25,v101
v_cvt_pk_bf16_f32 v108,v22,v23
M 34,a[244:247],58
D 0,56,0
M 34,v[128:131],62
s_add_u32 s55,s55,s60
R 50,2,26624
v_cvt_pk_bf16_f32 v109,v24,v25
M 30,a[240:243],58
buffer_store_dwordx4 v[106:109],v0,s[36:39],s66 offen offset:0 sc0 sc1
v_fma_f32 v26,-v74,v90,v26
M 30,a[252:255],62
L 3
v_fma_f32 v27,-v74,v91,v27
v_fma_f32 v28,-v74,v92,v28
M 30,v[136:139],66
s_sub_u32 s57,s84,s57
R 54,2,27648
v_fma_f32 v29,-v74,v93,v29
M 30,v[148:151],70
D 0,56,1024
M 34,v[140:143],66
v_mul_f32 v26,v75,v26
v_mul_f32 v27,v75,v27
M 34,v[152:155],70
R 58,2,28672
v_mul_f32 v28,v75,v28
v_mul_f32 v29,v75,v29
M 38,v[144:147],66
v_fma_f32 v102,v26,v26,s88
v_fma_f32 v103,v27,v27,s88
M 38,v[156:159],70
L 3
v_fma_f32 v104,v28,v28,s88
v_fma_f32 v105,v29,v29,s88
M 38,v[168:171],42
R 62,2,29696
v_mul_f32 v102,v26,v102
M 38,v[180:183],46
D 0,56,2048
M 34,v[164:167],42
v_exp_f32 v102,v102
v_mul_f32 v103,v27,v103
M 34,v[176:179],46
R 66,2,30720
v_mul_f32 v104,v28,v104
M 30,v[160:163],42
v_exp_f32 v103,v103
M 30,v[172:175],46
L 3
D 0,56,3072
M 30,v[184:187],50
s_nop 0
R 70,2,31744
v_mul_f32 v105,v29,v105
M 30,v[196:199],54
s_waitcnt vmcnt(5)
v_exp_f32 v104,v104
v_fma_f32 v102,v102,s89,s89
M 34,v[188:191],50
s_barrier
buffer_store_dword v4,v3,s[36:39],s70 offen offset:0 sc0 sc1
M 34,v[200:203],54
s_mov_b32 s70,s69
R 42,1,0
ds_read_b32 v7,v6 offset:1024
M 38,v[192:195],50
s_mov_b32 s69,s68
ds_read_b64 v[78:79],v13 offset:2048
v_fma_f32 v103,v103,s89,s89
M 38,v[204:207],54
L 5
v_exp_f32 v105,v105
M 38,v[216:219],58
s_add_u32 s68,s68,s63
R 46,1,1024
v_rcp_f32 v102,v102
M 38,v[228:231],62
v_rcp_f32 v103,v103
M 34,v[212:215],58
v_fma_f32 v104,v104,s89,s89
v_fma_f32 v105,v105,s89,s89
M 34,v[224:227],62
L 2
R 50,1,2048
v_mul_f32 v26,v26,v102
M 30,v[208:211],58
v_rcp_f32 v104,v104
M 30,v[220:223],62
s_nop 0
v_rcp_f32 v105,v105
v_mul_f32 v27,v27,v103
M 30,v[232:235],66
R 54,1,3072
v_mul_f32 v28,v28,v104
M 30,v[244:247],70
v_mul_f32 v29,v29,v105
v_cmp_eq_u32 vcc,s53,v7
M 34,v[236:239],66
v_cvt_pk_bf16_f32 v110,v26,v27
v_cvt_pk_bf16_f32 v111,v28,v29
M 34,v[248:251],70
s_cmp_eq_u32 vcc_lo,-1
R 58,1,4096
M 38,v[240:243],66
L 3
buffer_store_dwordx2 v[110:111],v10,s[36:39],s66 offen offset:0 sc0 sc1
M 38,v[252:255],70
buffer_store_dword v4,v3,s[36:39],s71 offen offset:0 sc0 sc1
s_cbranch_scc1 Lg3_F0_%=
s_branch Lg3_K0_%=
Lg3_X1_%=:
M 30,a[168:171],66
s_add_u32 m0,s59,1024
R 54,2,19456
v_mul_f32 v101,v25,v101
M 30,a[180:183],70
buffer_load_dword v8,s[36:39],s58 offen offset:0 sc1 lds
M 34,a[172:175],66
v_exp_f32 v99,v99
M 34,a[184:187],70
R 58,2,20480
v_fma_f32 v98,v98,s89,s89
M 38,a[176:179],66
v_exp_f32 v100,v100
M 38,a[188:191],70
L 3
v_exp_f32 v101,v101
M 38,a[200:203],42
R 62,2,21504
v_fma_f32 v99,v99,s89,s89
M 38,a[212:215],46
v_rcp_f32 v98,v98
M 34,a[196:199],42
v_rcp_f32 v99,v99
M 34,a[208:211],46
R 66,2,22528
v_fma_f32 v100,v100,s89,s89
M 30,a[192:195],42
v_rcp_f32 v100,v100
M 30,a[204:207],46
L 3
v_fma_f32 v101,v101,s89,s89
v_mul_f32 v22,v22,v98
M 30,a[216:219],50
R 70,2,23552
v_mul_f32 v23,v23,v99
M 30,a[228:231],54
v_rcp_f32 v101,v101
M 34,a[220:223],50
v_mul_f32 v24,v24,v100
v_mul_f32 v25,v25,v101
M 34,a[232:235],54
R 42,2,24576
v_cvt_pk_bf16_f32 v108,v22,v23
M 38,a[224:227],50
v_cvt_pk_bf16_f32 v109,v24,v25
v_fma_f32 v26,-v74,v90,v26
M 38,a[236:239],54
L 3
buffer_store_dwordx4 v[106:109],v0,s[36:39],s66 offen offset:0 sc0 sc1
v_fma_f32 v27,-v74,v91,v27
M 38,a[248:251],58
R 46,2,25600
v_fma_f32 v28,-v74,v92,v28
M 38,v[132:135],62
v_fma_f32 v29,-v74,v93,v29
v_mul_f32 v26,v75,v26
M 34,a[244:247],58
v_mul_f32 v27,v75,v27
v_mul_f32 v28,v75,v28
M 34,v[128:131],62
R 50,2,26624
v_mul_f32 v29,v75,v29
M 30,a[240:243],58
v_fma_f32 v102,v26,v26,s88
v_fma_f32 v103,v27,v27,s88
M 30,a[252:255],62
L 3
v_fma_f32 v104,v28,v28,s88
v_fma_f32 v105,v29,v29,s88
M 30,v[136:139],66
R 54,2,27648
v_mul_f32 v102,v26,v102
M 30,v[148:151],70
v_exp_f32 v102,v102
M 34,v[140:143],66
v_mul_f32 v103,v27,v103
v_mul_f32 v104,v28,v104
M 34,v[152:155],70
R 58,2,28672
v_mul_f32 v105,v29,v105
M 38,v[144:147],66
v_exp_f32 v103,v103
M 38,v[156:159],70
L 3
v_exp_f32 v104,v104
M 38,v[168:171],42
R 62,2,29696
v_fma_f32 v102,v102,s89,s89
M 38,v[180:183],46
v_exp_f32 v105,v105
)"
R"(M 34,v[164:167],42
v_rcp_f32 v102,v102
M 34,v[176:179],46
R 66,2,30720
v_fma_f32 v103,v103,s89,s89
M 30,v[160:163],42
v_rcp_f32 v103,v103
M 30,v[172:175],46
L 3
v_fma_f32 v104,v104,s89,s89
v_fma_f32 v105,v105,s89,s89
M 30,v[184:187],50
s_nop 0
R 70,2,31744
v_mul_f32 v26,v26,v102
M 30,v[196:199],54
s_waitcnt vmcnt(1)
v_rcp_f32 v104,v104
M 34,v[188:191],50
s_barrier
buffer_store_dword v4,v3,s[36:39],s70 offen offset:0 sc0 sc1
M 34,v[200:203],54
s_mov_b32 s70,s69
ds_read_b32 v7,v6 offset:1024
ds_read_b64 v[78:79],v13 offset:2048
M 38,v[192:195],50
s_mov_b32 s69,s68
v_rcp_f32 v105,v105
M 38,v[204:207],54
L 4
v_mul_f32 v27,v27,v103
v_mul_f32 v28,v28,v104
M 38,v[216:219],58
s_add_u32 s68,s68,s63
v_mul_f32 v29,v29,v105
v_cvt_pk_bf16_f32 v110,v26,v27
M 38,v[228:231],62
v_cvt_pk_bf16_f32 v111,v28,v29
M 34,v[212:215],58
buffer_store_dwordx2 v[110:111],v10,s[36:39],s66 offen offset:0 sc0 sc1
M 34,v[224:227],62
L 1
v_cmp_eq_u32 vcc,s53,v7
M 30,v[208:211],58
M 30,v[220:223],62
s_nop 0
M 30,v[232:235],66
M 30,v[244:247],70
M 34,v[236:239],66
M 34,v[248:251],70
M 38,v[240:243],66
M 38,v[252:255],70
s_mov_b32 s80,0
s_cmp_eq_u32 vcc_lo,-1
s_cbranch_scc0 Lg3_xn1_%=
buffer_load_dword v114,v11,s[36:39],s73 offen offset:0 sc1
L 0
v_fma_f32 v30,-v78,v82,v30
v_fma_f32 v31,-v78,v83,v31
v_fma_f32 v32,-v78,v84,v32
v_fma_f32 v33,-v78,v85,v33
v_mul_f32 v30,v79,v30
v_mul_f32 v31,v79,v31
v_mul_f32 v32,v79,v32
v_mul_f32 v33,v79,v33
s_add_u32 s73,s73,s64
s_add_u32 m0,s57,0
s_add_u32 s56,s55,0
D 0,56,0
v_fma_f32 v94,v30,v30,s88
v_fma_f32 v95,v31,v31,s88
v_fma_f32 v96,v32,v32,s88
v_fma_f32 v97,v33,v33,s88
v_mul_f32 v94,v30,v94
v_mul_f32 v95,v31,v95
v_mul_f32 v96,v32,v96
v_mul_f32 v97,v33,v97
D 0,56,1024
v_exp_f32 v94,v94
v_exp_f32 v95,v95
v_exp_f32 v96,v96
v_exp_f32 v97,v97
v_fma_f32 v94,v94,s89,s89
v_fma_f32 v95,v95,s89,s89
v_fma_f32 v96,v96,s89,s89
v_fma_f32 v97,v97,s89,s89
D 0,56,2048
v_rcp_f32 v94,v94
v_rcp_f32 v95,v95
v_rcp_f32 v96,v96
v_rcp_f32 v97,v97
v_mul_f32 v30,v30,v94
v_mul_f32 v31,v31,v95
v_mul_f32 v32,v32,v96
v_mul_f32 v33,v33,v97
D 0,56,3072
v_cvt_pk_bf16_f32 v106,v30,v31
v_cvt_pk_bf16_f32 v107,v32,v33
v_fma_f32 v34,-v78,v86,v34
v_fma_f32 v35,-v78,v87,v35
v_fma_f32 v36,-v78,v88,v36
v_fma_f32 v37,-v78,v89,v37
v_mul_f32 v34,v79,v34
v_mul_f32 v35,v79,v35
s_add_u32 s58,s58,s61
s_add_u32 m0,s57,16384
s_add_u32 s56,s55,16384
D 0,56,0
v_mul_f32 v36,v79,v36
v_mul_f32 v37,v79,v37
v_fma_f32 v98,v34,v34,s88
v_fma_f32 v99,v35,v35,s88
v_fma_f32 v100,v36,v36,s88
v_fma_f32 v101,v37,v37,s88
v_mul_f32 v98,v34,v98
v_mul_f32 v99,v35,v99
D 0,56,1024
v_mul_f32 v100,v36,v100
v_mul_f32 v101,v37,v101
v_exp_f32 v98,v98
v_exp_f32 v99,v99
v_exp_f32 v100,v100
v_exp_f32 v101,v101
v_fma_f32 v98,v98,s89,s89
v_fma_f32 v99,v99,s89,s89
D 0,56,2048
v_fma_f32 v100,v100,s89,s89
v_fma_f32 v101,v101,s89,s89
v_rcp_f32 v98,v98
v_rcp_f32 v99,v99
v_rcp_f32 v100,v100
v_rcp_f32 v101,v101
v_mul_f32 v34,v34,v98
v_mul_f32 v35,v35,v99
D 0,56,3072
v_mul_f32 v36,v36,v100
v_mul_f32 v37,v37,v101
v_cvt_pk_bf16_f32 v108,v34,v35
v_cvt_pk_bf16_f32 v109,v36,v37
buffer_store_dwordx4 v[106:109],v0,s[36:39],s67 offen offset:0 sc0 sc1
v_fma_f32 v38,-v78,v90,v38
v_fma_f32 v39,-v78,v91,v39
v_fma_f32 v40,-v78,v92,v40
v_fma_f32 v41,-v78,v93,v41
s_add_u32 s55,s55,s60
s_sub_u32 s57,s84,s57
s_add_u32 s54,s54,s46
v_mul_f32 v38,v79,v38
v_mul_f32 v39,v79,v39
v_mul_f32 v40,v79,v40
v_mul_f32 v41,v79,v41
v_fma_f32 v102,v38,v38,s88
v_fma_f32 v103,v39,v39,s88
v_fma_f32 v104,v40,v40,s88
v_fma_f32 v105,v41,v41,s88
v_mul_f32 v102,v38,v102
v_mul_f32 v103,v39,v103
v_mul_f32 v104,v40,v104
v_mul_f32 v105,v41,v105
v_exp_f32 v102,v102
v_exp_f32 v103,v103
v_exp_f32 v104,v104
v_exp_f32 v105,v105
v_fma_f32 v102,v102,s89,s89
v_fma_f32 v103,v103,s89,s89
v_fma_f32 v104,v104,s89,s89
v_fma_f32 v105,v105,s89,s89
v_rcp_f32 v102,v102
v_rcp_f32 v103,v103
v_rcp_f32 v104,v104
v_rcp_f32 v105,v105
v_mul_f32 v38,v38,v102
v_mul_f32 v39,v39,v103
v_mul_f32 v40,v40,v104
v_mul_f32 v41,v41,v105
v_cvt_pk_bf16_f32 v110,v38,v39
v_cvt_pk_bf16_f32 v111,v40,v41
buffer_store_dwordx2 v[110:111],v10,s[36:39],s67 offen offset:0 sc0 sc1
s_mov_b32 s72,s69
s_mov_b32 s69,0x7fff0000
s_mov_b32 s67,0x7fff0000
s_mov_b32 s77,0x7fff0000
s_branch Lg3_gox_%=
Lg3_xn1_%=:
s_cmp_ge_u32 s54,256
s_cbranch_scc1 Lg3_fin1_%=
s_add_u32 m0,s59,4096
s_nop 0
buffer_load_dword v8,s[36:39],s58 offen offset:0 sc1 lds
buffer_store_dword v4,v3,s[36:39],s71 offen offset:0 sc0 sc1
s_add_u32 m0,s59,5120
s_nop 0
buffer_load_dword v8,s[36:39],s58 offen offset:0 sc1 lds
buffer_store_dword v4,v3,s[36:39],s71 offen offset:0 sc0 sc1
s_mov_b32 s83,0
L 0
v_fma_f32 v30,-v78,v82,v30
v_fma_f32 v31,-v78,v83,v31
v_fma_f32 v32,-v78,v84,v32
v_fma_f32 v33,-v78,v85,v33
v_mul_f32 v30,v79,v30
v_mul_f32 v31,v79,v31
v_mul_f32 v32,v79,v32
v_mul_f32 v33,v79,v33
v_fma_f32 v94,v30,v30,s88
v_fma_f32 v95,v31,v31,s88
v_fma_f32 v96,v32,v32,s88
v_fma_f32 v97,v33,v33,s88
v_mul_f32 v94,v30,v94
v_mul_f32 v95,v31,v95
v_mul_f32 v96,v32,v96
v_mul_f32 v97,v33,v97
v_exp_f32 v94,v94
v_exp_f32 v95,v95
v_exp_f32 v96,v96
v_exp_f32 v97,v97
v_fma_f32 v94,v94,s89,s89
v_fma_f32 v95,v95,s89,s89
v_fma_f32 v96,v96,s89,s89
v_fma_f32 v97,v97,s89,s89
v_rcp_f32 v94,v94
v_rcp_f32 v95,v95
v_rcp_f32 v96,v96
v_rcp_f32 v97,v97
v_mul_f32 v30,v30,v94
v_mul_f32 v31,v31,v95
v_mul_f32 v32,v32,v96
v_mul_f32 v33,v33,v97
v_cvt_pk_bf16_f32 v106,v30,v31
v_cvt_pk_bf16_f32 v107,v32,v33
v_fma_f32 v34,-v78,v86,v34
v_fma_f32 v35,-v78,v87,v35
v_fma_f32 v36,-v78,v88,v36
v_fma_f32 v37,-v78,v89,v37
v_mul_f32 v34,v79,v34
v_mul_f32 v35,v79,v35
v_mul_f32 v36,v79,v36
v_mul_f32 v37,v79,v37
v_fma_f32 v98,v34,v34,s88
v_fma_f32 v99,v35,v35,s88
v_fma_f32 v100,v36,v36,s88
v_fma_f32 v101,v37,v37,s88
v_mul_f32 v98,v34,v98
v_mul_f32 v99,v35,v99
v_mul_f32 v100,v36,v100
v_mul_f32 v101,v37,v101
v_exp_f32 v98,v98
v_exp_f32 v99,v99
v_exp_f32 v100,v100
v_exp_f32 v101,v101
v_fma_f32 v98,v98,s89,s89
v_fma_f32 v99,v99,s89,s89
v_fma_f32 v100,v100,s89,s89
v_fma_f32 v101,v101,s89,s89
v_rcp_f32 v98,v98
v_rcp_f32 v99,v99
v_rcp_f32 v100,v100
v_rcp_f32 v101,v101
v_mul_f32 v34,v34,v98
v_mul_f32 v35,v35,v99
v_mul_f32 v36,v36,v100
v_mul_f32 v37,v37,v101
v_cvt_pk_bf16_f32 v108,v34,v35
v_cvt_pk_bf16_f32 v109,v36,v37
buffer_store_dwordx4 v[106:109],v0,s[36:39],s67 offen offset:0 sc0 sc1
v_fma_f32 v38,-v78,v90,v38
v_fma_f32 v39,-v78,v91,v39
v_fma_f32 v40,-v78,v92,v40
v_fma_f32 v41,-v78,v93,v41
v_mul_f32 v38,v79,v38
v_mul_f32 v39,v79,v39
v_mul_f32 v40,v79,v40
v_mul_f32 v41,v79,v41
v_fma_f32 v102,v38,v38,s88
v_fma_f32 v103,v39,v39,s88
v_fma_f32 v104,v40,v40,s88
v_fma_f32 v105,v41,v41,s88
v_mul_f32 v102,v38,v102
v_mul_f32 v103,v39,v103
v_mul_f32 v104,v40,v104
v_mul_f32 v105,v41,v105
v_exp_f32 v102,v102
v_exp_f32 v103,v103
v_exp_f32 v104,v104
v_exp_f32 v105,v105
v_fma_f32 v102,v102,s89,s89
v_fma_f32 v103,v103,s89,s89
v_fma_f32 v104,v104,s89,s89
v_fma_f32 v105,v105,s89,s89
v_rcp_f32 v102,v102
v_rcp_f32 v103,v103
v_rcp_f32 v104,v104
v_rcp_f32 v105,v105
v_mul_f32 v38,v38,v102
v_mul_f32 v39,v39,v103
v_mul_f32 v40,v40,v104
v_mul_f32 v41,v41,v105
v_cvt_pk_bf16_f32 v110,v38,v39
v_cvt_pk_bf16_f32 v111,v40,v41
buffer_store_dwordx2 v[110:111],v10,s[36:39],s67 offen offset:0 sc0 sc1
s_mov_b32 s72,s69
s_mov_b32 s69,0x7fff0000
s_mov_b32 s67,0x7fff0000
s_mov_b32 s77,0x7fff0000
s_branch Lg3_poll_%=
Lg3_fin1_%=:
L 0
v_fma_f32 v30,-v78,v82,v30
v_fma_f32 v31,-v78,v83,v31
v_fma_f32 v32,-v78,v84,v32
v_fma_f32 v33,-v78,v85,v33
v_mul_f32 v30,v79,v30
v_mul_f32 v31,v79,v31
v_mul_f32 v32,v79,v32
v_mul_f32 v33,v79,v33
v_fma_f32 v94,v30,v30,s88
v_fma_f32 v95,v31,v31,s88
v_fma_f32 v96,v32,v32,s88
v_fma_f32 v97,v33,v33,s88
v_mul_f32 v94,v30,v94
v_mul_f32 v95,v31,v95
v_mul_f32 v96,v32,v96
v_mul_f32 v97,v33,v97
v_exp_f32 v94,v94
v_exp_f32 v95,v95
v_exp_f32 v96,v96
v_exp_f32 v97,v97
v_fma_f32 v94,v94,s89,s89
v_fma_f32 v95,v95,s89,s89
v_fma_f32 v96,v96,s89,s89
v_fma_f32 v97,v97,s89,s89
v_rcp_f32 v94,v94
v_rcp_f32 v95,v95
v_rcp_f32 v96,v96
v_rcp_f32 v97,v97
v_mul_f32 v30,v30,v94
v_mul_f32 v31,v31,v95
v_mul_f32 v32,v32,v96
v_mul_f32 v33,v33,v97
v_cvt_pk_bf16_f32 v106,v30,v31
v_cvt_pk_bf16_f32 v107,v32,v33
v_fma_f32 v34,-v78,v86,v34
v_fma_f32 v35,-v78,v87,v35
v_fma_f32 v36,-v78,v88,v36
v_fma_f32 v37,-v78,v89,v37
v_mul_f32 v34,v79,v34
v_mul_f32 v35,v79,v35
v_mul_f32 v36,v79,v36
v_mul_f32 v37,v79,v37
v_fma_f32 v98,v34,v34,s88
v_fma_f32 v99,v35,v35,s88
v_fma_f32 v100,v36,v36,s88
v_fma_f32 v101,v37,v37,s88
v_mul_f32 v98,v34,v98
v_mul_f32 v99,v35,v99
v_mul_f32 v100,v36,v100
v_mul_f32 v101,v37,v101
v_exp_f32 v98,v98
v_exp_f32 v99,v99
v_exp_f32 v100,v100
v_exp_f32 v101,v101
v_fma_f32 v98,v98,s89,s89
v_fma_f32 v99,v99,s89,s89
v_fma_f32 v100,v100,s89,s89
v_fma_f32 v101,v101,s89,s89
v_rcp_f32 v98,v98
v_rcp_f32 v99,v99
v_rcp_f32 v100,v100
v_rcp_f32 v101,v101
v_mul_f32 v34,v34,v98
v_mul_f32 v35,v35,v99
v_mul_f32 v36,v36,v100
v_mul_f32 v37,v37,v101
v_cvt_pk_bf16_f32 v108,v34,v35
v_cvt_pk_bf16_f32 v109,v36,v37
buffer_store_dwordx4 v[106:109],v0,s[36:39],s67 offen offset:0 sc0 sc1
v_fma_f32 v38,-v78,v90,v38
v_fma_f32 v39,-v78,v91,v39
v_fma_f32 v40,-v78,v92,v40
v_fma_f32 v41,-v78,v93,v41
v_mul_f32 v38,v79,v38
v_mul_f32 v39,v79,v39
v_mul_f32 v40,v79,v40
v_mul_f32 v41,v79,v41
v_fma_f32 v102,v38,v38,s88
v_fma_f32 v103,v39,v39,s88
v_fma_f32 v104,v40,v40,s88
v_fma_f32 v105,v41,v41,s88
v_mul_f32 v102,v38,v102
v_mul_f32 v103,v39,v103
v_mul_f32 v104,v40,v104
v_mul_f32 v105,v41,v105
v_exp_f32 v102,v102
v_exp_f32 v103,v103
v_exp_f32 v104,v104
v_exp_f32 v105,v105
v_fma_f32 v102,v102,s89,s89
v_fma_f32 v103,v103,s89,s89
v_fma_f32 v104,v104,s89,s89
v_fma_f32 v105,v105,s89,s89
v_rcp_f32 v102,v102
v_rcp_f32 v103,v103
v_rcp_f32 v104,v104
v_rcp_f32 v105,v105
v_mul_f32 v38,v38,v102
v_mul_f32 v39,v39,v103
v_mul_f32 v40,v40,v104
v_mul_f32 v41,v41,v105
v_cvt_pk_bf16_f32 v110,v38,v39
v_cvt_pk_bf16_f32 v111,v40,v41
buffer_store_dwordx2 v[110:111],v10,s[36:39],s67 offen offset:0 sc0 sc1
s_mov_b32 s72,s69
s_mov_b32 s69,0x7fff0000
s_mov_b32 s67,0x7fff0000
s_mov_b32 s77,0x7fff0000
s_branch Lg3_done_%=
Lg3_done_%=:
s_waitcnt vmcnt(2)
s_barrier
buffer_store_dword v4,v3,s[36:39],s70 offen offset:0 sc0 sc1
s_waitcnt vmcnt(1)
s_barrier
buffer_store_dword v4,v3,s[36:39],s72 offen offset:0 sc0 sc1
s_waitcnt vmcnt(0) lgkmcnt(0)
s_barrier
s_endpgm
Lcode_g3_e:
.purgem M
.purgem R
.purgem T
.purgem D
.purgem L
)"
  :: "s"(ws), "s"(__builtin_amdgcn_readfirstlane(sWave)), "s"(__builtin_amdgcn_readfirstlane(sG0)), "s"(__builtin_amdgcn_readfirstlane(sStep)), "s"(__builtin_amdgcn_readfirstlane(sOutOff)), "s"(__builtin_amdgcn_readfirstlane(sFoutOff)), "s"(__builtin_amdgcn_readfirstlane(sAuxOff)), "s"(__builtin_amdgcn_readfirstlane(sWw)), "s"(__builtin_amdgcn_readfirstlane(sBuf)), "s"(__builtin_amdgcn_readfirstlane(sExp)), "v"(vL16), "v"(vFlag), "v"(vSnapOff), "v"(vSnapRd), "v"(vWd0), "v"(vWd1), "v"(vWr0), "v"(vWr1), "v"(vWr2), "v"(vOutS), "v"(vAux0), "v"(vAux1), "v"(vAux2), "v"(vAux3), "v"(vAn), "v"(vAe), "s"(w3), "s"(out) : "v255", "v0", "v1", "v2", "v3", "v4", "v5", "v6", "v7", "v8", "v9", "v10", "v11", "v12", "v13", "v14", "v15", "v16", "v17", "v18", "v19", "v20", "v21", "v22", "v23", "v24", "v25", "v26", "v27", "v28", "v29", "v30", "v31", "v32", "v33", "v34", "v35", "v36", "v37", "v38", "v39", "v40", "v41", "v42", "v43", "v44", "v45", "v46", "v47", "v48", "v49", "v50", "v51", "v52", "v53", "v54", "v55", "v56", "v57", "v58", "v59", "v60", "v61", "v62", "v63", "v64", "v65", "v66", "v67", "v68", "v69", "v70", "v71", "v72", "v73", "v74", "v75", "v76", "v77", "v78", "v79", "v80", "v81", "v82", "v83", "v84", "v85", "v86", "v87", "v88", "v89", "v90", "v91", "v92", "v93", "v94", "v95", "v96", "v97", "v98", "v99", "v100", "v101", "v102", "v103", "v104", "v105", "v106", "v107", "v108", "v109", "v110", "v111", "v112", "v113", "v114", "v115", "v116", "v117", "v118", "v119", "v120", "v121", "v122", "v123", "v124", "v125", "v126", "v127", "v128", "v129", "v130", "v131", "v132", "v133", "v134", "v135", "v136", "v137", "v138", "v139", "v140", "v141", "v142", "v143", "v144", "v145", "v146", "v147", "v148", "v149", "v150", "v151", "v152", "v153", "v154", "v155", "v156", "v157", "v158", "v159", "v160", "v161", "v162", "v163", "v164", "v165", "v166", "v167", "v168", "v169", "v170", "v171", "v172", "v173", "v174", "v175", "v176", "v177", "v178", "v179", "v180", "v181", "v182", "v183", "v184", "v185", "v186", "v187", "v188", "v189", "v190", "v191", "v192", "v193", "v194", "v195", "v196", "v197", "v198", "v199", "v200", "v201", "v202", "v203", "v204", "v205", "v206", "v207", "v208", "v209", "v210", "v211", "v212", "v213", "v214", "v215", "v216", "v217", "v218", "v219", "v220", "v221", "v222", "v223", "v224", "v225", "v226", "v227", "v228", "v229", "v230", "v231", "a0", "a1", "a2", "a3", "a4", "a5", "a6", "a7", "a8", "a9", "a10", "a11", "a12", "a13", "a14", "a15", "a16", "a17", "a18", "a19", "a20", "a21", "a22", "a23", "a24", "a25", "a26", "a27", "a28", "a29", "a30", "a31", "a32", "a33", "a34", "a35", "a36", "a37", "a38", "a39", "a40", "a41", "a42", "a43", "a44", "a45", "a46", "a47", "a48", "a49", "a50", "a51", "a52", "a53", "a54", "a55", "a56", "a57", "a58", "a59", "a60", "a61", "a62", "a63", "a64", "a65", "a66", "a67", "a68", "a69", "a70", "a71", "a72", "a73", "a74", "a75", "a76", "a77", "a78", "a79", "a80", "a81", "a82", "a83", "a84", "a85", "a86", "a87", "a88", "a89", "a90", "a91", "a92", "a93", "a94", "a95", "a96", "a97", "a98", "a99", "a100", "a101", "a102", "a103", "a104", "a105", "a106", "a107", "a108", "a109", "a110", "a111", "a112", "a113", "a114", "a115", "a116", "a117", "a118", "a119", "a120", "a121", "a122", "a123", "a124", "a125", "a126", "a127", "a128", "a129", "a130", "a131", "a132", "a133", "a134", "a135", "a136", "a137", "a138", "a139", "a140", "a141", "a142", "a143", "a144", "a145", "a146", "a147", "a148", "a149", "a150", "a151", "a152", "a153", "a154", "a155", "a156", "a157", "a158", "a159", "a160", "a161", "a162", "a163", "a164", "a165", "a166", "a167", "a168", "a169", "a170", "a171", "a172", "a173", "a174", "a175", "a176", "a177", "a178", "a179", "a180", "a181", "a182", "a183", "a184", "a185", "a186", "a187", "a188", "a189", "a190", "a191", "a192", "a193", "a194", "a195", "a196", "a197", "a198", "a199", "a200", "a201", "a202", "a203", "a204", "a205", "a206", "a207", "a208", "a209", "a210", "a211", "a212", "a213", "a214", "a215", "a216", "a217", "a218", "a219", "a220", "a221", "a222", "a223", "a224", "a225", "a226", "a227", "a228", "a229", "a230", "a231", "a232", "a233", "a234", "a235", "a236", "a237", "a238", "a239", "a240", "a241", "a242", "a243", "a244", "a245", "a246", "a247", "a248", "a249", "a250", "a251", "a252", "a253", "a254", "a255", "s36", "s37", "s38", "s39", "s40", "s41", "s42", "s43", "s44", "s45", "s46", "s47", "s48", "s49", "s50", "s51", "s52", "s53", "s54", "s55", "s56", "s57", "s58", "s59", "s60", "s61", "s62", "s63", "s64", "s65", "s66", "s67", "s68", "s69", "s70", "s71", "s72", "s73", "s74", "s75", "s76", "s77", "s78", "s79", "s80", "s81", "s82", "s83", "s84", "s85", "s86", "s87", "s88", "s89", "s90", "s91", "s92", "s93", "s94", "s95", "s96", "s97", "vcc", "scc", "memory"); } else { asm volatile(
R"(.macro M d,w,x
v_mfma_f32_16x16x32_bf16 v[\d:\d+3], \w, v[\x:\x+3], v[\d:\d+3]
.endm
.macro R d,a,o
ds_read_b128 v[\d:\d+3], v\a offset:\o
.endm
.macro T d,a,o
ds_read_b64_tr_b16 \d, v\a offset:\o
.endm
.macro D a,s,o
buffer_load_dwordx4 v\a, s[36:39], s\s offen offset:\o sc1 lds
.endm
.macro L c
s_waitcnt lgkmcnt(\c)
.endm
Lcode_g3o_b:
s_mov_b64 s[36:37],%0
s_mov_b32 s38,111243392
s_mov_b32 s39,0x00020000
s_mov_b32 s44,%1
s_mov_b32 s45,%2
s_mov_b32 s46,%3
s_mov_b32 s47,%4
s_mov_b32 s48,%5
s_mov_b32 s49,%6
s_mov_b32 s50,%7
s_mov_b32 s51,%8
s_mov_b32 s53,%9
v_mov_b32 v0,%10
v_mov_b32 v3,%11
v_mov_b32 v5,%12
v_mov_b32 v6,%13
v_mov_b32 v18,%14
v_mov_b32 v19,%15
v_mov_b32 v20,%16
v_mov_b32 v21,%17
v_mov_b32 v22,%18
v_mov_b32 v10,%19
v_mov_b32 v11,%20
v_mov_b32 v12,%21
v_mov_b32 v13,%22
v_mov_b32 v14,%23
v_mov_b32 v15,%24
v_mov_b32 v16,%25
s_mov_b64 s[40:41],%26
s_mov_b32 s42,7340032
s_mov_b32 s43,0x00020000
s_lshl_b32 s57,s44,11
s_add_u32 s55,s50,0
s_add_u32 m0,s57,0
s_nop 0
buffer_load_dwordx4 v18,s[40:43],s55 offen offset:0  lds
s_add_u32 m0,s57,1024
s_nop 0
buffer_load_dwordx4 v19,s[40:43],s55 offen offset:0  lds
s_add_u32 m0,s57,8064
s_nop 0
buffer_load_dwordx4 v18,s[40:43],s55 offen offset:128  lds
s_add_u32 m0,s57,9088
s_nop 0
buffer_load_dwordx4 v19,s[40:43],s55 offen offset:128  lds
s_add_u32 s55,s50,458752
s_add_u32 m0,s57,16384
s_nop 0
buffer_load_dwordx4 v18,s[40:43],s55 offen offset:0  lds
s_add_u32 m0,s57,17408
s_nop 0
buffer_load_dwordx4 v19,s[40:43],s55 offen offset:0  lds
s_add_u32 m0,s57,24448
s_nop 0
buffer_load_dwordx4 v18,s[40:43],s55 offen offset:128  lds
s_add_u32 m0,s57,25472
s_nop 0
buffer_load_dwordx4 v19,s[40:43],s55 offen offset:128  lds
s_add_u32 s55,s50,917504
s_add_u32 m0,s57,32768
s_nop 0
buffer_load_dwordx4 v18,s[40:43],s55 offen offset:0  lds
s_add_u32 m0,s57,33792
s_nop 0
buffer_load_dwordx4 v19,s[40:43],s55 offen offset:0  lds
s_add_u32 m0,s57,40832
s_nop 0
buffer_load_dwordx4 v18,s[40:43],s55 offen offset:128  lds
s_add_u32 m0,s57,41856
s_nop 0
buffer_load_dwordx4 v19,s[40:43],s55 offen offset:128  lds
s_add_u32 s55,s50,1376256
s_add_u32 m0,s57,49152
s_nop 0
buffer_load_dwordx4 v18,s[40:43],s55 offen offset:0  lds
s_add_u32 m0,s57,50176
s_nop 0
buffer_load_dwordx4 v19,s[40:43],s55 offen offset:0  lds
s_add_u32 m0,s57,57216
s_nop 0
buffer_load_dwordx4 v18,s[40:43],s55 offen offset:128  lds
s_add_u32 m0,s57,58240
s_nop 0
buffer_load_dwordx4 v19,s[40:43],s55 offen offset:128  lds
s_add_u32 s55,s50,1835008
s_add_u32 m0,s57,65536
s_nop 0
buffer_load_dwordx4 v18,s[40:43],s55 offen offset:0  lds
s_add_u32 m0,s57,66560
s_nop 0
buffer_load_dwordx4 v19,s[40:43],s55 offen offset:0  lds
s_add_u32 m0,s57,73600
s_nop 0
buffer_load_dwordx4 v18,s[40:43],s55 offen offset:128  lds
s_add_u32 m0,s57,74624
s_nop 0
buffer_load_dwordx4 v19,s[40:43],s55 offen offset:128  lds
s_add_u32 s55,s50,2293760
s_add_u32 m0,s57,81920
s_nop 0
buffer_load_dwordx4 v18,s[40:43],s55 offen offset:0  lds
s_add_u32 m0,s57,82944
s_nop 0
buffer_load_dwordx4 v19,s[40:43],s55 offen offset:0  lds
s_add_u32 m0,s57,89984
s_nop 0
buffer_load_dwordx4 v18,s[40:43],s55 offen offset:128  lds
s_add_u32 m0,s57,91008
s_nop 0
buffer_load_dwordx4 v19,s[40:43],s55 offen offset:128  lds
s_waitcnt vmcnt(20) lgkmcnt(0)
s_barrier
s_add_u32 s55,s50,2752512
s_add_u32 m0,s57,98304
s_nop 0
buffer_load_dwordx4 v18,s[40:43],s55 offen offset:0  lds
s_add_u32 m0,s57,99328
s_nop 0
buffer_load_dwordx4 v19,s[40:43],s55 offen offset:0  lds
s_add_u32 m0,s57,106368
s_nop 0
buffer_load_dwordx4 v18,s[40:43],s55 offen offset:128  lds
s_add_u32 m0,s57,107392
s_nop 0
buffer_load_dwordx4 v19,s[40:43],s55 offen offset:128  lds
v_add_u32 v26,0,v20
v_add_u32 v27,0,v21
T a[0:1],26,0
T a[2:3],26,512
T a[4:5],27,0
T a[6:7],27,512
T a[8:9],26,4096
T a[10:11],26,4608
T a[12:13],27,4096
T a[14:15],27,4608
s_waitcnt vmcnt(20) lgkmcnt(0)
s_barrier
s_add_u32 s55,s50,3211264
s_add_u32 m0,s57,0
s_nop 0
buffer_load_dwordx4 v18,s[40:43],s55 offen offset:0  lds
s_add_u32 m0,s57,1024
s_nop 0
buffer_load_dwordx4 v19,s[40:43],s55 offen offset:0  lds
s_add_u32 m0,s57,8064
s_nop 0
buffer_load_dwordx4 v18,s[40:43],s55 offen offset:128  lds
s_add_u32 m0,s57,9088
s_nop 0
buffer_load_dwordx4 v19,s[40:43],s55 offen offset:128  lds
v_add_u32 v26,16384,v20
v_add_u32 v27,16384,v21
T a[16:17],26,0
T a[18:19],26,512
T a[20:21],27,0
T a[22:23],27,512
T a[24:25],26,4096
T a[26:27],26,4608
T a[28:29],27,4096
T a[30:31],27,4608
s_waitcnt vmcnt(20) lgkmcnt(0)
s_barrier
s_add_u32 s55,s50,3670016
s_add_u32 m0,s57,16384
s_nop 0
buffer_load_dwordx4 v18,s[40:43],s55 offen offset:0  lds
s_add_u32 m0,s57,17408
s_nop 0
buffer_load_dwordx4 v19,s[40:43],s55 offen offset:0  lds
s_add_u32 m0,s57,24448
s_nop 0
buffer_load_dwordx4 v18,s[40:43],s55 offen offset:128  lds
s_add_u32 m0,s57,25472
s_nop 0
buffer_load_dwordx4 v19,s[40:43],s55 offen offset:128  lds
v_add_u32 v26,32768,v20
v_add_u32 v27,32768,v21
T a[32:33],26,0
T a[34:35],26,512
T a[36:37],27,0
T a[38:39],27,512
T a[40:41],26,4096
T a[42:43],26,4608
T a[44:45],27,4096
T a[46:47],27,4608
s_waitcnt vmcnt(20) lgkmcnt(0)
s_barrier
s_add_u32 s55,s50,4128768
s_add_u32 m0,s57,32768
s_nop 0
buffer_load_dwordx4 v18,s[40:43],s55 offen offset:0  lds
s_add_u32 m0,s57,33792
s_nop 0
buffer_load_dwordx4 v19,s[40:43],s55 offen offset:0  lds
s_add_u32 m0,s57,40832
s_nop 0
buffer_load_dwordx4 v18,s[40:43],s55 offen offset:128  lds
s_add_u32 m0,s57,41856
s_nop 0
buffer_load_dwordx4 v19,s[40:43],s55 offen offset:128  lds
v_add_u32 v26,49152,v20
v_add_u32 v27,49152,v21
T a[48:49],26,0
T a[50:51],26,512
T a[52:53],27,0
T a[54:55],27,512
T a[56:57],26,4096
T a[58:59],26,4608
T a[60:61],27,4096
T a[62:63],27,4608
s_waitcnt vmcnt(20) lgkmcnt(0)
s_barrier
s_add_u32 s55,s50,4587520
s_add_u32 m0,s57,49152
s_nop 0
buffer_load_dwordx4 v18,s[40:43],s55 offen offset:0  lds
s_add_u32 m0,s57,50176
s_nop 0
buffer_load_dwordx4 v19,s[40:43],s55 offen offset:0  lds
s_add_u32 m0,s57,57216
s_nop 0
buffer_load_dwordx4 v18,s[40:43],s55 offen offset:128  lds
s_add_u32 m0,s57,58240
s_nop 0
buffer_load_dwordx4 v19,s[40:43],s55 offen offset:128  lds
v_add_u32 v26,65536,v20
v_add_u32 v27,65536,v21
T a[64:65],26,0
T a[66:67],26,512
T a[68:69],27,0
T a[70:71],27,512
T a[72:73],26,4096
T a[74:75],26,4608
T a[76:77],27,4096
T a[78:79],27,4608
s_waitcnt vmcnt(20) lgkmcnt(0)
s_barrier
s_add_u32 s55,s50,5046272
s_add_u32 m0,s57,65536
s_nop 0
buffer_load_dwordx4 v18,s[40:43],s55 offen offset:0  lds
s_add_u32 m0,s57,66560
s_nop 0
buffer_load_dwordx4 v19,s[40:43],s55 offen offset:0  lds
s_add_u32 m0,s57,73600
s_nop 0
buffer_load_dwordx4 v18,s[40:43],s55 offen offset:128  lds
s_add_u32 m0,s57,74624
s_nop 0
buffer_load_dwordx4 v19,s[40:43],s55 offen offset:128  lds
v_add_u32 v26,81920,v20
v_add_u32 v27,81920,v21
T a[80:81],26,0
T a[82:83],26,512
T a[84:85],27,0
T a[86:87],27,512
T a[88:89],26,4096
T a[90:91],26,4608
T a[92:93],27,4096
T a[94:95],27,4608
s_waitcnt vmcnt(20) lgkmcnt(0)
s_barrier
s_add_u32 s55,s50,5505024
s_add_u32 m0,s57,81920
s_nop 0
buffer_load_dwordx4 v18,s[40:43],s55 offen offset:0  lds
s_add_u32 m0,s57,82944
s_nop 0
buffer_load_dwordx4 v19,s[40:43],s55 offen offset:0  lds
s_add_u32 m0,s57,89984
s_nop 0
buffer_load_dwordx4 v18,s[40:43],s55 offen offset:128  lds
s_add_u32 m0,s57,91008
s_nop 0
buffer_load_dwordx4 v19,s[40:43],s55 offen offset:128  lds
v_add_u32 v26,98304,v20
v_add_u32 v27,98304,v21
T a[96:97],26,0
T a[98:99],26,512
T a[100:101],27,0
T a[102:103],27,512
T a[104:105],26,4096
T a[106:107],26,4608
T a[108:109],27,4096
T a[110:111],27,4608
s_waitcnt vmcnt(20) lgkmcnt(0)
s_barrier
s_add_u32 s55,s50,5963776
s_add_u32 m0,s57,98304
s_nop 0
buffer_load_dwordx4 v18,s[40:43],s55 offen offset:0  lds
s_add_u32 m0,s57,99328
s_nop 0
buffer_load_dwordx4 v19,s[40:43],s55 offen offset:0  lds
s_add_u32 m0,s57,106368
s_nop 0
buffer_load_dwordx4 v18,s[40:43],s55 offen offset:128  lds
s_add_u32 m0,s57,107392
s_nop 0
buffer_load_dwordx4 v19,s[40:43],s55 offen offset:128  lds
v_add_u32 v26,0,v20
v_add_u32 v27,0,v21
T a[112:113],26,0
T a[114:115],26,512
T a[116:117],27,0
T a[118:119],27,512
T a[120:121],26,4096
T a[122:123],26,4608
T a[124:125],27,4096
T a[126:127],27,4608
s_waitcnt vmcnt(20) lgkmcnt(0)
s_barrier
s_add_u32 s55,s50,6422528
s_add_u32 m0,s57,0
s_nop 0
buffer_load_dwordx4 v18,s[40:43],s55 offen offset:0  lds
s_add_u32 m0,s57,1024
s_nop 0
buffer_load_dwordx4 v19,s[40:43],s55 offen offset:0  lds
s_add_u32 m0,s57,8064
s_nop 0
buffer_load_dwordx4 v18,s[40:43],s55 offen offset:128  lds
s_add_u32 m0,s57,9088
s_nop 0
buffer_load_dwordx4 v19,s[40:43],s55 offen offset:128  lds
v_add_u32 v26,16384,v20
v_add_u32 v27,16384,v21
T a[128:129],26,0
T a[130:131],26,512
T a[132:133],27,0
T a[134:135],27,512
T a[136:137],26,4096
T a[138:139],26,4608
T a[140:141],27,4096
T a[142:143],27,4608
s_waitcnt vmcnt(20) lgkmcnt(0)
s_barrier
s_add_u32 s55,s50,6881280
s_add_u32 m0,s57,16384
s_nop 0
buffer_load_dwordx4 v18,s[40:43],s55 offen offset:0  lds
s_add_u32 m0,s57,17408
s_nop 0
buffer_load_dwordx4 v19,s[40:43],s55 offen offset:0  lds
s_add_u32 m0,s57,24448
s_nop 0
buffer_load_dwordx4 v18,s[40:43],s55 offen offset:128  lds
s_add_u32 m0,s57,25472
s_nop 0
buffer_load_dwordx4 v19,s[40:43],s55 offen offset:128  lds
v_add_u32 v26,32768,v20
v_add_u32 v27,32768,v21
T a[144:145],26,0
T a[146:147],26,512
T a[148:149],27,0
T a[150:151],27,512
T a[152:153],26,4096
T a[154:155],26,4608
T a[156:157],27,4096
T a[158:159],27,4608
s_waitcnt vmcnt(20) lgkmcnt(0)
s_barrier
v_add_u32 v26,49152,v20
v_add_u32 v27,49152,v21
T a[160:161],26,0
T a[162:163],26,512
T a[164:165],27,0
T a[166:167],27,512
T a[168:169],26,4096
T a[170:171],26,4608
T a[172:173],27,4096
T a[174:175],27,4608
s_waitcnt vmcnt(16) lgkmcnt(0)
s_barrier
v_add_u32 v26,65536,v20
v_add_u32 v27,65536,v21
T a[176:177],26,0
T a[178:179],26,512
T a[180:181],27,0
T a[182:183],27,512
T a[184:185],26,4096
T a[186:187],26,4608
T a[188:189],27,4096
T a[190:191],27,4608
s_waitcnt vmcnt(12) lgkmcnt(0)
s_barrier
v_add_u32 v26,81920,v20
v_add_u32 v27,81920,v21
T a[192:193],26,0
T a[194:195],26,512
T a[196:197],27,0
T a[198:199],27,512
T a[200:201],26,4096
T a[202:203],26,4608
T a[204:205],27,4096
T a[206:207],27,4608
s_waitcnt vmcnt(8) lgkmcnt(0)
s_barrier
v_add_u32 v26,98304,v20
v_add_u32 v27,98304,v21
T a[208:209],26,0
T a[210:211],26,512
T a[212:213],27,0
T a[214:215],27,512
T a[216:217],26,4096
T a[218:219],26,4608
T a[220:221],27,4096
T a[222:223],27,4608
s_waitcnt vmcnt(4) lgkmcnt(0)
s_barrier
v_add_u32 v26,0,v20
v_add_u32 v27,0,v21
T a[224:225],26,0
T a[226:227],26,512
T a[228:229],27,0
T a[230:231],27,512
T a[232:233],26,4096
T a[234:235],26,4608
T a[236:237],27,4096
T a[238:239],27,4608
s_waitcnt vmcnt(0) lgkmcnt(0)
s_barrier
v_add_u32 v26,16384,v20
v_add_u32 v27,16384,v21
T a[240:241],26,0
T a[242:243],26,512
T a[244:245],27,0
T a[246:247],27,512
T a[248:249],26,4096
T a[250:251],26,4608
T a[252:253],27,4096
T a[254:255],27,4608
L 0
s_barrier
v_mov_b32 v34,0x3f803f80
v_mov_b32 v35,0x3f803f80
v_mov_b32 v36,0x3f803f80
v_mov_b32 v37,0x3f803f80
s_nop 1
v_mfma_f32_16x16x32_bf16 v[106:109],a[0:3],v[34:37],0
v_mfma_f32_16x16x32_bf16 v[110:113],a[4:7],v[34:37],0
M 106,a[8:11],34
M 110,a[12:15],34
M 106,a[16:19],34
M 110,a[20:23],34
M 106,a[24:27],34
M 110,a[28:31],34
M 106,a[32:35],34
M 110,a[36:39],34
M 106,a[40:43],34
M 110,a[44:47],34
M 106,a[48:51],34
M 110,a[52:55],34
M 106,a[56:59],34
M 110,a[60:63],34
M 106,a[64:67],34
M 110,a[68:71],34
M 106,a[72:75],34
M 110,a[76:79],34
M 106,a[80:83],34
M 110,a[84:87],34
M 106,a[88:91],34
M 110,a[92:95],34
M 106,a[96:99],34
M 110,a[100:103],34
M 106,a[104:107],34
M 110,a[108:111],34
M 106,a[112:115],34
M 110,a[116:119],34
M 106,a[120:123],34
M 110,a[124:127],34
M 106,a[128:131],34
M 110,a[132:135],34
M 106,a[136:139],34
M 110,a[140:143],34
M 106,a[144:147],34
M 110,a[148:151],34
M 106,a[152:155],34
M 110,a[156:159],34
M 106,a[160:163],34
M 110,a[164:167],34
M 106,a[168:171],34
M 110,a[172:175],34
M 106,a[176:179],34
)"
R"(M 110,a[180:183],34
M 106,a[184:187],34
M 110,a[188:191],34
M 106,a[192:195],34
M 110,a[196:199],34
M 106,a[200:203],34
M 110,a[204:207],34
M 106,a[208:211],34
M 110,a[212:215],34
M 106,a[216:219],34
M 110,a[220:223],34
M 106,a[224:227],34
M 110,a[228:231],34
M 106,a[232:235],34
M 110,a[236:239],34
M 106,a[240:243],34
M 110,a[244:247],34
M 106,a[248:251],34
M 110,a[252:255],34
v_mov_b32 v4,s53
s_mov_b32 s82,0
s_mov_b32 s80,0
s_mov_b32 s54,s45
s_mul_i32 s57,s44,4096
s_mul_i32 s55,s45,32768
s_add_u32 s55,s55,s57
s_add_u32 s55,s55,31457280
s_mul_i32 s57,s44,4096
s_mul_i32 s60,s46,32768
s_mul_i32 s61,s46,1024
s_mul_i32 s62,s46,114688
s_mul_i32 s63,s46,2560
s_mul_i32 s64,s46,1024
s_lshl_b32 s59,s44,8
s_add_u32 s59,s59,131072
s_mul_i32 s58,s45,1024
s_add_u32 s58,s58,109613056
s_mul_i32 s65,s45,114688
s_add_u32 s65,s65,s47
s_mul_i32 s68,s45,2560
s_add_u32 s68,s68,s48
s_mul_i32 s73,s45,1024
s_add_u32 s73,s73,107479040
s_mul_i32 s74,s45,32768
s_add_u32 s74,s74,s49
s_mul_i32 s75,s45,1024
s_add_u32 s75,s75,107479040
s_mov_b32 s66,0x7fff0000
s_mov_b32 s67,0x7fff0000
s_mov_b32 s69,0x7fff0000
s_mov_b32 s76,0x7fff0000
s_mov_b32 s77,0x7fff0000
s_mov_b32 s70,0x7fff0000
s_mov_b32 s71,0x7fff0000
s_mov_b32 s72,0x7fff0000
v_mov_b32 v1,v0
v_add_u32 v2,16384,v0
s_mov_b32 s84,16384
s_mov_b32 s86,0x3a800000
s_mov_b32 s87,0x3727c5ac
s_mov_b32 s88,0x409d30fc
s_mov_b32 s89,0xbeeff558
s_lshr_b32 s85,s44,1
s_mul_i32 s85,s85,127
v_lshlrev_b32 v8,22,v0
v_and_b32 v8,0x80000000,v8
v_or_b32 v8,v8,v5
v_add_u32 v2,32768,v0
s_lshl_b32 s84,s57,1
s_add_u32 s84,s84,32768
s_getpc_b64 s[40:41]
Lg3o_pc_%=:
s_lshl_b32 s78,s44,13
v_lshlrev_b32 v114,3,v0
v_add_u32 v114,s78,v114
v_min_u32 v114,Lcode_g3o_e-Lg3o_pc_%=-4,v114
s_mov_b32 m0,163584
s_nop 0
global_load_lds_dword v114,s[40:41]
s_add_u32 m0,s59,4096
s_nop 0
buffer_load_dword v8,s[36:39],s58 offen offset:0 sc1 lds
buffer_store_dword v4,v3,s[36:39],s71 offen offset:0 sc0 sc1
s_add_u32 m0,s59,5120
s_nop 0
buffer_load_dword v8,s[36:39],s58 offen offset:0 sc1 lds
buffer_store_dword v4,v3,s[36:39],s71 offen offset:0 sc0 sc1
s_mov_b32 s83,0
Lg3o_poll_%=:
s_add_u32 s78,s83,2
s_and_b32 s78,s78,3
s_lshl_b32 s78,s78,10
s_add_u32 s78,s78,4096
s_add_u32 m0,s59,s78
s_nop 0
buffer_load_dword v8,s[36:39],s58 offen offset:0 sc1 lds
s_and_b32 s78,s83,3
s_lshl_b32 s78,s78,10
v_add_u32 v114,s78,v6
s_waitcnt vmcnt(4)
s_barrier
ds_read_b32 v7,v114 offset:4096
buffer_store_dword v4,v3,s[36:39],s70 offen offset:0 sc0 sc1
s_mov_b32 s70,s71
s_mov_b32 s71,s72
s_mov_b32 s72,0x7fff0000
s_add_u32 s83,s83,1
s_add_u32 s82,s82,1
L 0
v_cmp_eq_u32 vcc,s53,v7
s_nop 0
s_cmp_eq_u32 vcc_lo,-1
s_cbranch_scc1 Lg3o_go_%=
s_cmp_lt_u32 s82,20000
s_cbranch_scc1 Lg3o_poll_%=
Lg3o_go_%=:
buffer_load_dword v130,v11,s[36:39],s73 offen offset:0 sc1
s_add_u32 s73,s73,s64
s_add_u32 m0,s57,0
s_add_u32 s56,s55,0
D 0,56,0
D 0,56,1024
D 0,56,2048
D 0,56,3072
s_add_u32 s58,s58,s61
s_add_u32 m0,s59,2048
s_nop 0
buffer_load_dword v8,s[36:39],s58 offen offset:0 sc1 lds
s_add_u32 m0,s57,16384
s_add_u32 s56,s55,16384
D 0,56,0
D 0,56,1024
D 0,56,2048
D 0,56,3072
s_add_u32 s55,s55,s60
s_sub_u32 s57,s84,s57
s_add_u32 s54,s54,s46
s_waitcnt vmcnt(4)
s_barrier
buffer_store_dword v4,v3,s[36:39],s70 offen offset:0 sc0 sc1
s_min_u32 s71,s71,s72
buffer_store_dword v4,v3,s[36:39],s71 offen offset:0 sc0 sc1
s_mov_b32 s70,0x7fff0000
s_mov_b32 s71,0x7fff0000
s_mov_b32 s72,0x7fff0000
s_cmp_eq_u32 s80,1
s_cbranch_scc1 Lg3o_st1_%=
s_branch Lg3o_st0_%=
Lg3o_gox_%=:
s_add_u32 m0,s59,2048
s_nop 0
buffer_load_dword v8,s[36:39],s58 offen offset:0 sc1 lds
s_waitcnt vmcnt(0)
s_barrier
buffer_store_dword v4,v3,s[36:39],s70 offen offset:0 sc0 sc1
s_min_u32 s71,s71,s72
buffer_store_dword v4,v3,s[36:39],s71 offen offset:0 sc0 sc1
s_mov_b32 s70,0x7fff0000
s_mov_b32 s71,0x7fff0000
s_mov_b32 s72,0x7fff0000
s_cmp_eq_u32 s80,1
s_cbranch_scc1 Lg3o_st1_%=
Lg3o_st0_%=:
ds_read_b32 v7,v6 offset:2048
R 34,1,0
R 38,1,1024
R 42,1,2048
R 46,1,3072
R 50,1,4096
L 5
v_cmp_eq_u32 vcc,s53,v7
L 3
s_cmp_eq_u32 vcc_lo,-1
s_cbranch_scc1 Lg3o_F0_%=
s_branch Lg3o_L0_%=
Lg3o_st1_%=:
ds_read_b32 v7,v6 offset:2048
R 34,2,0
R 38,2,1024
R 42,2,2048
R 46,2,3072
R 50,2,4096
L 5
v_cmp_eq_u32 vcc,s53,v7
L 3
s_cmp_eq_u32 vcc_lo,-1
s_cbranch_scc1 Lg3o_F1_%=
s_branch Lg3o_L1_%=
Lg3o_F0_%=:
v_mfma_f32_16x16x32_bf16 v[22:25],a[4:7],v[34:37],0
s_add_u32 s58,s58,s61
s_add_u32 m0,s59,0
R 54,1,5120
M 22,a[12:15],38
buffer_load_dword v8,s[36:39],s58 offen offset:0 sc1 lds
v_mfma_f32_16x16x32_bf16 v[18:21],a[0:3],v[34:37],0
s_add_u32 m0,s57,0
R 58,1,6144
v_add_f32_dpp v131,v130,v130 quad_perm:[1,0,3,2] row_mask:0xf bank_mask:0xf
M 18,a[8:11],38
L 3
s_add_u32 s56,s55,0
buffer_load_dword v130,v11,s[36:39],s73 offen offset:0 sc1
M 18,a[16:19],42
s_mov_b32 s66,s65
R 62,1,7168
v_add_f32_dpp v131,v131,v131 quad_perm:[2,3,0,1] row_mask:0xf bank_mask:0xf
M 18,a[24:27],46
D 0,56,0
M 22,a[20:23],42
s_add_u32 s65,s65,s62
R 66,1,8192
v_add_f32_dpp v134,v131,v131 row_half_mirror row_mask:0xf bank_mask:0xf
M 22,a[28:31],46
L 3
D 0,56,1024
M 22,a[36:39],50
s_add_u32 s54,s54,s46
R 70,1,9216
v_mov_b32_dpp v131,v134 row_ror:8 row_mask:0xf bank_mask:0xf
M 22,a[44:47],54
s_add_u32 s73,s73,s64
v_fma_f32 v131,-v134,v134,v131
v_max_f32 v131,s87,v131
M 18,a[32:35],50
R 74,1,10240
v_mul_f32 v134,v15,v134
M 18,a[40:43],54
L 3
D 0,56,2048
M 18,a[48:51],58
R 78,1,11264
v_fma_f32 v26,-v102,v106,v26
M 18,a[56:59],62
D 0,56,3072
M 22,a[52:55],58
s_add_u32 m0,s57,16384
R 82,1,12288
v_fma_f32 v27,-v102,v107,v27
M 22,a[60:63],62
L 3
v_rsq_f32 v132,v131
M 22,a[68:71],66
s_add_u32 s56,s55,16384
R 86,1,13312
v_fma_f32 v134,v132,v16,v134
M 22,a[76:79],70
s_add_u32 s55,s55,s60
ds_write_b32 v12,v134 offset:0
M 18,a[64:67],66
s_sub_u32 s57,s84,s57
R 90,1,14336
v_fma_f32 v28,-v102,v108,v28
M 18,a[72:75],70
L 4
v_fma_f32 v29,-v102,v109,v29
v_mul_f32 v26,v103,v26
M 18,a[80:83],74
s_waitcnt vmcnt(8)
R 94,1,15360
v_mul_f32 v27,v103,v27
M 18,a[88:91],78
s_barrier
v_mul_f32 v28,v103,v28
v_mul_f32 v29,v103,v29
M 22,a[84:87],74
R 34,1,16384
v_fma_f32 v114,v26,v26,s88
M 22,a[92:95],78
L 4
v_fma_f32 v115,v27,v27,s88
v_fma_f32 v116,v28,v28,s88
M 22,a[100:103],82
R 38,1,17408
v_fma_f32 v117,v29,v29,s88
M 22,a[108:111],86
D 0,56,0
M 18,a[96:99],82
R 42,1,18432
v_mul_f32 v114,v26,v114
M 18,a[104:107],86
L 3
v_exp_f32 v114,v114
M 18,a[112:115],90
R 46,1,19456
v_mul_f32 v115,v27,v115
M 18,a[120:123],94
v_exp_f32 v115,v115
M 22,a[116:119],90
R 50,1,20480
v_mul_f32 v116,v28,v116
M 22,a[124:127],94
L 3
v_exp_f32 v116,v116
M 22,a[132:135],34
R 54,1,21504
v_mul_f32 v117,v29,v117
M 22,a[140:143],38
D 0,56,1024
M 18,a[128:131],34
R 58,1,22528
v_fma_f32 v114,v114,s89,s89
M 18,a[136:139],38
L 3
v_exp_f32 v117,v117
M 18,a[144:147],42
R 62,1,23552
v_fma_f32 v115,v115,s89,s89
M 18,a[152:155],46
v_rcp_f32 v114,v114
M 22,a[148:151],42
R 66,1,24576
v_fma_f32 v116,v116,s89,s89
M 22,a[156:159],46
L 3
D 0,56,2048
M 22,a[164:167],50
R 70,1,25600
v_fma_f32 v117,v117,s89,s89
M 22,a[172:175],54
v_rcp_f32 v115,v115
M 18,a[160:163],50
R 74,1,26624
v_mul_f32 v26,v26,v114
M 18,a[168:171],54
L 3
v_rcp_f32 v116,v116
M 18,a[176:179],58
R 78,1,27648
v_mul_f32 v27,v27,v115
M 18,a[184:187],62
v_rcp_f32 v117,v117
M 22,a[180:183],58
R 82,1,28672
v_mul_f32 v28,v28,v116
M 22,a[188:191],62
L 3
D 0,56,3072
M 22,a[196:199],66
R 86,1,29696
v_mul_f32 v29,v29,v117
M 22,a[204:207],70
v_cvt_pk_bf16_f32 v122,v26,v27
v_cvt_pk_bf16_f32 v123,v28,v29
M 18,a[192:195],66
R 90,1,30720
v_fma_f32 v30,-v102,v110,v30
M 18,a[200:203],70
L 3
s_nop 0
v_fma_f32 v31,-v102,v111,v31
M 18,a[208:211],74
s_waitcnt vmcnt(4)
R 94,1,31744
v_fma_f32 v32,-v102,v112,v32
M 18,a[216:219],78
s_barrier
ds_read_b32 v7,v6 offset:0
ds_read_b64 v[98:99],v13 offset:0
M 22,a[212:215],74
R 34,2,0
v_fma_f32 v33,-v102,v113,v33
M 22,a[220:223],78
L 5
buffer_store_dword v4,v3,s[36:39],s70 offen offset:0 sc0 sc1
M 22,a[228:231],82
s_mov_b32 s70,s69
R 38,2,1024
v_mul_f32 v30,v103,v30
M 22,a[236:239],86
s_mov_b32 s69,s68
v_mul_f32 v31,v103,v31
v_mul_f32 v32,v103,v32
M 18,a[224:227],82
s_add_u32 s68,s68,s63
R 42,2,2048
v_mul_f32 v33,v103,v33
M 18,a[232:235],86
L 5
v_fma_f32 v118,v30,v30,s88
v_fma_f32 v119,v31,v31,s88
v_fma_f32 v120,v32,v32,s88
v_fma_f32 v121,v33,v33,s88
v_mul_f32 v118,v30,v118
v_mul_f32 v119,v31,v119
v_mul_f32 v120,v32,v120
v_mul_f32 v121,v33,v121
v_exp_f32 v118,v118
v_exp_f32 v119,v119
v_exp_f32 v120,v120
v_exp_f32 v121,v121
M 18,a[240:243],90
L 4
R 46,2,3072
v_fma_f32 v118,v118,s89,s89
v_fma_f32 v119,v119,s89,s89
v_fma_f32 v120,v120,s89,s89
v_fma_f32 v121,v121,s89,s89
v_rcp_f32 v118,v118
v_rcp_f32 v119,v119
v_rcp_f32 v120,v120
v_rcp_f32 v121,v121
v_cmp_eq_u32 vcc,s53,v7
M 18,a[248:251],94
v_mul_f32 v30,v30,v118
v_mul_f32 v31,v31,v119
v_mul_f32 v32,v32,v120
v_mul_f32 v33,v33,v121
v_cvt_pk_bf16_f32 v124,v30,v31
v_cvt_pk_bf16_f32 v125,v32,v33
M 22,a[244:247],90
L 2
s_cmp_eq_u32 vcc_lo,-1
R 50,2,4096
buffer_store_dwordx4 v[122:125],v0,s[36:39],s67 offen offset:0 sc0 sc1
M 22,a[252:255],94
s_cbranch_scc0 Lg3o_K1_%=
Lg3o_F1_%=:
v_mfma_f32_16x16x32_bf16 v[30:33],a[4:7],v[34:37],0
s_add_u32 s58,s58,s61
s_add_u32 m0,s59,1024
R 54,2,5120
M 30,a[12:15],38
buffer_load_dword v8,s[36:39],s58 offen offset:0 sc1 lds
v_mfma_f32_16x16x32_bf16 v[26:29],a[0:3],v[34:37],0
s_add_u32 m0,s57,0
R 58,2,6144
v_add_f32_dpp v131,v130,v130 quad_perm:[1,0,3,2] row_mask:0xf bank_mask:0xf
M 26,a[8:11],38
L 3
s_add_u32 s56,s55,0
buffer_load_dword v130,v11,s[36:39],s73 offen offset:0 sc1
M 26,a[16:19],42
s_mov_b32 s67,s65
R 62,2,7168
v_add_f32_dpp v131,v131,v131 quad_perm:[2,3,0,1] row_mask:0xf bank_mask:0xf
M 26,a[24:27],46
D 0,56,0
M 30,a[20:23],42
s_add_u32 s65,s65,s62
R 66,2,8192
v_add_f32_dpp v134,v131,v131 row_half_mirror row_mask:0xf bank_mask:0xf
M 30,a[28:31],46
L 3
D 0,56,1024
M 30,a[36:39],50
s_add_u32 s54,s54,s46
R 70,2,9216
v_mov_b32_dpp v131,v134 row_ror:8 row_mask:0xf bank_mask:0xf
M 30,a[44:47],54
s_add_u32 s73,s73,s64
v_fma_f32 v131,-v134,v134,v131
v_max_f32 v131,s87,v131
M 26,a[32:35],50
R 74,2,10240
v_mul_f32 v134,v15,v134
M 26,a[40:43],54
L 3
D 0,56,2048
M 26,a[48:51],58
R 78,2,11264
v_fma_f32 v18,-v98,v106,v18
M 26,a[56:59],62
D 0,56,3072
M 30,a[52:55],58
s_add_u32 m0,s57,16384
R 82,2,12288
v_fma_f32 v19,-v98,v107,v19
M 30,a[60:63],62
L 3
v_rsq_f32 v132,v131
M 30,a[68:71],66
s_add_u32 s56,s55,16384
R 86,2,13312
v_fma_f32 v134,v132,v16,v134
M 30,a[76:79],70
s_add_u32 s55,s55,s60
ds_write_b32 v12,v134 offset:2048
M 26,a[64:67],66
s_sub_u32 s57,s84,s57
R 90,2,14336
v_fma_f32 v20,-v98,v108,v20
M 26,a[72:75],70
L 4
v_fma_f32 v21,-v98,v109,v21
v_mul_f32 v18,v99,v18
M 26,a[80:83],74
s_waitcnt vmcnt(8)
R 94,2,15360
v_mul_f32 v19,v99,v19
M 26,a[88:91],78
s_barrier
v_mul_f32 v20,v99,v20
v_mul_f32 v21,v99,v21
M 30,a[84:87],74
R 34,2,16384
v_fma_f32 v114,v18,v18,s88
M 30,a[92:95],78
L 4
v_fma_f32 v115,v19,v19,s88
)"
R"(v_fma_f32 v116,v20,v20,s88
M 30,a[100:103],82
R 38,2,17408
v_fma_f32 v117,v21,v21,s88
M 30,a[108:111],86
D 0,56,0
M 26,a[96:99],82
R 42,2,18432
v_mul_f32 v114,v18,v114
M 26,a[104:107],86
L 3
v_exp_f32 v114,v114
M 26,a[112:115],90
R 46,2,19456
v_mul_f32 v115,v19,v115
M 26,a[120:123],94
v_exp_f32 v115,v115
M 30,a[116:119],90
R 50,2,20480
v_mul_f32 v116,v20,v116
M 30,a[124:127],94
L 3
v_exp_f32 v116,v116
M 30,a[132:135],34
R 54,2,21504
v_mul_f32 v117,v21,v117
M 30,a[140:143],38
D 0,56,1024
M 26,a[128:131],34
R 58,2,22528
v_fma_f32 v114,v114,s89,s89
M 26,a[136:139],38
L 3
v_exp_f32 v117,v117
M 26,a[144:147],42
R 62,2,23552
v_fma_f32 v115,v115,s89,s89
M 26,a[152:155],46
v_rcp_f32 v114,v114
M 30,a[148:151],42
R 66,2,24576
v_fma_f32 v116,v116,s89,s89
M 30,a[156:159],46
L 3
D 0,56,2048
M 30,a[164:167],50
R 70,2,25600
v_fma_f32 v117,v117,s89,s89
M 30,a[172:175],54
v_rcp_f32 v115,v115
M 26,a[160:163],50
R 74,2,26624
v_mul_f32 v18,v18,v114
M 26,a[168:171],54
L 3
v_rcp_f32 v116,v116
M 26,a[176:179],58
R 78,2,27648
v_mul_f32 v19,v19,v115
M 26,a[184:187],62
v_rcp_f32 v117,v117
M 30,a[180:183],58
R 82,2,28672
v_mul_f32 v20,v20,v116
M 30,a[188:191],62
L 3
D 0,56,3072
M 30,a[196:199],66
R 86,2,29696
v_mul_f32 v21,v21,v117
M 30,a[204:207],70
v_cvt_pk_bf16_f32 v122,v18,v19
v_cvt_pk_bf16_f32 v123,v20,v21
M 26,a[192:195],66
R 90,2,30720
v_fma_f32 v22,-v98,v110,v22
M 26,a[200:203],70
L 3
s_nop 0
v_fma_f32 v23,-v98,v111,v23
M 26,a[208:211],74
s_waitcnt vmcnt(4)
R 94,2,31744
v_fma_f32 v24,-v98,v112,v24
M 26,a[216:219],78
s_barrier
ds_read_b32 v7,v6 offset:1024
ds_read_b64 v[102:103],v13 offset:2048
M 30,a[212:215],74
R 34,1,0
v_fma_f32 v25,-v98,v113,v25
M 30,a[220:223],78
L 5
buffer_store_dword v4,v3,s[36:39],s70 offen offset:0 sc0 sc1
M 30,a[228:231],82
s_mov_b32 s70,s69
R 38,1,1024
v_mul_f32 v22,v99,v22
M 30,a[236:239],86
s_mov_b32 s69,s68
v_mul_f32 v23,v99,v23
v_mul_f32 v24,v99,v24
M 26,a[224:227],82
s_add_u32 s68,s68,s63
R 42,1,2048
v_mul_f32 v25,v99,v25
M 26,a[232:235],86
L 5
v_fma_f32 v118,v22,v22,s88
v_fma_f32 v119,v23,v23,s88
v_fma_f32 v120,v24,v24,s88
v_fma_f32 v121,v25,v25,s88
v_mul_f32 v118,v22,v118
v_mul_f32 v119,v23,v119
v_mul_f32 v120,v24,v120
v_mul_f32 v121,v25,v121
v_exp_f32 v118,v118
v_exp_f32 v119,v119
v_exp_f32 v120,v120
v_exp_f32 v121,v121
M 26,a[240:243],90
L 4
R 46,1,3072
v_fma_f32 v118,v118,s89,s89
v_fma_f32 v119,v119,s89,s89
v_fma_f32 v120,v120,s89,s89
v_fma_f32 v121,v121,s89,s89
v_rcp_f32 v118,v118
v_rcp_f32 v119,v119
v_rcp_f32 v120,v120
v_rcp_f32 v121,v121
v_cmp_eq_u32 vcc,s53,v7
M 26,a[248:251],94
v_mul_f32 v22,v22,v118
v_mul_f32 v23,v23,v119
v_mul_f32 v24,v24,v120
v_mul_f32 v25,v25,v121
v_cvt_pk_bf16_f32 v124,v22,v23
v_cvt_pk_bf16_f32 v125,v24,v25
M 30,a[244:247],90
L 2
s_cmp_eq_u32 vcc_lo,-1
R 50,1,4096
buffer_store_dwordx4 v[122:125],v0,s[36:39],s66 offen offset:0 sc0 sc1
M 30,a[252:255],94
s_cbranch_scc1 Lg3o_F0_%=
Lg3o_K0_%=:
Lg3o_L0_%=:
v_mfma_f32_16x16x32_bf16 v[22:25],a[4:7],v[34:37],0
s_add_u32 m0,s59,3072
R 54,1,5120
v_add_f32_dpp v131,v130,v130 quad_perm:[1,0,3,2] row_mask:0xf bank_mask:0xf
M 22,a[12:15],38
buffer_load_dword v8,s[36:39],s58 offen offset:0 sc1 lds
v_mfma_f32_16x16x32_bf16 v[18:21],a[0:3],v[34:37],0
s_mov_b32 s66,s65
R 58,1,6144
v_add_f32_dpp v131,v131,v131 quad_perm:[2,3,0,1] row_mask:0xf bank_mask:0xf
M 18,a[8:11],38
L 3
v_fma_f32 v26,-v102,v106,v26
v_fma_f32 v27,-v102,v107,v27
M 18,a[16:19],42
s_add_u32 s65,s65,s62
R 62,1,7168
v_add_f32_dpp v134,v131,v131 row_half_mirror row_mask:0xf bank_mask:0xf
M 18,a[24:27],46
v_fma_f32 v28,-v102,v108,v28
v_fma_f32 v29,-v102,v109,v29
M 22,a[20:23],42
R 66,1,8192
v_mov_b32_dpp v131,v134 row_ror:8 row_mask:0xf bank_mask:0xf
M 22,a[28:31],46
L 3
v_fma_f32 v131,-v134,v134,v131
v_max_f32 v131,s87,v131
M 22,a[36:39],50
R 70,1,9216
v_mul_f32 v134,v15,v134
M 22,a[44:47],54
v_rsq_f32 v132,v131
M 18,a[32:35],50
R 74,1,10240
v_fma_f32 v134,v132,v16,v134
M 18,a[40:43],54
L 3
ds_write_b32 v12,v134 offset:0
M 18,a[48:51],58
R 78,1,11264
v_mul_f32 v26,v103,v26
M 18,a[56:59],62
v_mul_f32 v27,v103,v27
v_mul_f32 v28,v103,v28
M 22,a[52:55],58
R 82,1,12288
v_mul_f32 v29,v103,v29
M 22,a[60:63],62
L 4
v_fma_f32 v114,v26,v26,s88
v_fma_f32 v115,v27,v27,s88
M 22,a[68:71],66
R 86,1,13312
v_fma_f32 v116,v28,v28,s88
M 22,a[76:79],70
v_fma_f32 v117,v29,v29,s88
v_mul_f32 v114,v26,v114
M 18,a[64:67],66
R 90,1,14336
v_mul_f32 v115,v27,v115
M 18,a[72:75],70
L 3
v_exp_f32 v114,v114
M 18,a[80:83],74
s_waitcnt vmcnt(0)
R 94,1,15360
v_mul_f32 v116,v28,v116
M 18,a[88:91],78
s_barrier
ds_read_b32 v7,v6 offset:3072
v_mul_f32 v117,v29,v117
M 22,a[84:87],74
R 34,1,16384
v_fma_f32 v114,v114,s89,s89
M 22,a[92:95],78
L 4
buffer_store_dword v4,v3,s[36:39],s70 offen offset:0 sc0 sc1
M 22,a[100:103],82
s_mov_b32 s70,0x7fff0000
R 38,1,17408
v_fma_f32 v30,-v102,v110,v30
M 22,a[108:111],86
v_exp_f32 v115,v115
M 18,a[96:99],82
R 42,1,18432
v_fma_f32 v115,v115,s89,s89
M 18,a[104:107],86
L 4
L 3
v_cmp_eq_u32 vcc,s53,v7
M 18,a[112:115],90
R 46,1,19456
v_fma_f32 v31,-v102,v111,v31
M 18,a[120:123],94
v_exp_f32 v116,v116
M 22,a[116:119],90
s_cmp_eq_u32 vcc_lo,-1
R 50,1,20480
v_fma_f32 v116,v116,s89,s89
s_cbranch_scc0 Lg3o_X0_%=
M 22,a[124:127],94
L 3
s_add_u32 m0,s57,0
s_add_u32 s56,s55,0
buffer_load_dword v130,v11,s[36:39],s73 offen offset:0 sc1
M 22,a[132:135],34
s_add_u32 s58,s58,s61
R 54,1,21504
v_fma_f32 v32,-v102,v112,v32
M 22,a[140:143],38
D 0,56,0
M 18,a[128:131],34
s_add_u32 s54,s54,s46
R 58,1,22528
v_fma_f32 v33,-v102,v113,v33
M 18,a[136:139],38
L 3
v_exp_f32 v117,v117
M 18,a[144:147],42
s_add_u32 s73,s73,s64
R 62,1,23552
v_fma_f32 v117,v117,s89,s89
M 18,a[152:155],46
D 0,56,1024
M 22,a[148:151],42
R 66,1,24576
v_mul_f32 v30,v103,v30
M 22,a[156:159],46
L 3
v_rcp_f32 v114,v114
M 22,a[164:167],50
R 70,1,25600
v_mul_f32 v26,v26,v114
M 22,a[172:175],54
D 0,56,2048
M 18,a[160:163],50
R 74,1,26624
v_mul_f32 v31,v103,v31
M 18,a[168:171],54
L 3
v_rcp_f32 v115,v115
M 18,a[176:179],58
R 78,1,27648
v_mul_f32 v27,v27,v115
M 18,a[184:187],62
D 0,56,3072
M 22,a[180:183],58
s_add_u32 m0,s59,0
R 82,1,28672
v_cvt_pk_bf16_f32 v122,v26,v27
M 22,a[188:191],62
L 3
buffer_load_dword v8,s[36:39],s58 offen offset:0 sc1 lds
M 22,a[196:199],66
s_add_u32 m0,s57,16384
R 86,1,29696
v_mul_f32 v32,v103,v32
M 22,a[204:207],70
s_add_u32 s56,s55,16384
v_rcp_f32 v116,v116
M 18,a[192:195],66
s_add_u32 s55,s55,s60
R 90,1,30720
v_mul_f32 v28,v28,v116
M 18,a[200:203],70
L 3
s_nop 0
D 0,56,0
M 18,a[208:211],74
s_waitcnt vmcnt(1)
R 94,1,31744
v_mul_f32 v33,v103,v33
M 18,a[216:219],78
s_barrier
ds_read_b32 v7,v6 offset:0
ds_read_b64 v[98:99],v13 offset:0
M 22,a[212:215],74
s_sub_u32 s57,s84,s57
R 34,2,0
v_fma_f32 v118,v30,v30,s88
M 22,a[220:223],78
L 5
buffer_store_dword v4,v3,s[36:39],s70 offen offset:0 sc0 sc1
M 22,a[228:231],82
s_mov_b32 s70,s69
R 38,2,1024
v_fma_f32 v119,v31,v31,s88
M 22,a[236:239],86
D 0,56,1024
M 18,a[224:227],82
s_mov_b32 s69,s68
R 42,2,2048
v_fma_f32 v120,v32,v32,s88
M 18,a[232:235],86
L 5
v_fma_f32 v121,v33,v33,s88
v_mul_f32 v118,v30,v118
v_mul_f32 v119,v31,v119
v_mul_f32 v120,v32,v120
v_mul_f32 v121,v33,v121
v_exp_f32 v118,v118
v_exp_f32 v119,v119
v_exp_f32 v120,v120
v_exp_f32 v121,v121
M 18,a[240:243],90
L 4
R 46,2,3072
v_rcp_f32 v117,v117
v_fma_f32 v118,v118,s89,s89
v_fma_f32 v119,v119,s89,s89
v_fma_f32 v120,v120,s89,s89
v_fma_f32 v121,v121,s89,s89
v_rcp_f32 v118,v118
v_rcp_f32 v119,v119
v_rcp_f32 v120,v120
v_rcp_f32 v121,v121
v_cmp_eq_u32 vcc,s53,v7
M 18,a[248:251],94
s_add_u32 s68,s68,s63
v_mul_f32 v29,v29,v117
v_cvt_pk_bf16_f32 v123,v28,v29
v_mul_f32 v30,v30,v118
v_mul_f32 v31,v31,v119
v_mul_f32 v32,v32,v120
v_mul_f32 v33,v33,v121
v_cvt_pk_bf16_f32 v124,v30,v31
v_cvt_pk_bf16_f32 v125,v32,v33
M 22,a[244:247],90
L 2
s_cmp_eq_u32 vcc_lo,-1
R 50,2,4096
buffer_store_dwordx4 v[122:125],v0,s[36:39],s67 offen offset:0 sc0 sc1
M 22,a[252:255],94
D 0,56,2048
D 0,56,3072
buffer_store_dword v4,v3,s[36:39],s71 offen offset:0 sc0 sc1
buffer_store_dword v4,v3,s[36:39],s71 offen offset:0 sc0 sc1
s_cbranch_scc1 Lg3o_F1_%=
s_branch Lg3o_K1_%=
Lg3o_X0_%=:
M 22,a[124:127],94
L 3
s_add_u32 m0,s59,0
v_fma_f32 v32,-v102,v112,v32
M 22,a[132:135],34
R 54,1,21504
v_fma_f32 v33,-v102,v113,v33
M 22,a[140:143],38
buffer_load_dword v8,s[36:39],s58 offen offset:0 sc1 lds
M 18,a[128:131],34
R 58,1,22528
v_mul_f32 v30,v103,v30
M 18,a[136:139],38
L 3
v_exp_f32 v117,v117
M 18,a[144:147],42
R 62,1,23552
v_fma_f32 v117,v117,s89,s89
M 18,a[152:155],46
v_rcp_f32 v114,v114
M 22,a[148:151],42
R 66,1,24576
v_mul_f32 v26,v26,v114
M 22,a[156:159],46
L 3
v_rcp_f32 v115,v115
M 22,a[164:167],50
R 70,1,25600
v_mul_f32 v27,v27,v115
M 22,a[172:175],54
v_rcp_f32 v116,v116
M 18,a[160:163],50
R 74,1,26624
v_mul_f32 v28,v28,v116
M 18,a[168:171],54
L 3
v_rcp_f32 v117,v117
M 18,a[176:179],58
R 78,1,27648
v_mul_f32 v29,v29,v117
M 18,a[184:187],62
v_cvt_pk_bf16_f32 v122,v26,v27
v_cvt_pk_bf16_f32 v123,v28,v29
M 22,a[180:183],58
R 82,1,28672
v_mul_f32 v31,v103,v31
M 22,a[188:191],62
L 3
v_mul_f32 v32,v103,v32
v_mul_f32 v33,v103,v33
M 22,a[196:199],66
R 86,1,29696
v_fma_f32 v118,v30,v30,s88
M 22,a[204:207],70
v_fma_f32 v119,v31,v31,s88
v_fma_f32 v120,v32,v32,s88
M 18,a[192:195],66
R 90,1,30720
v_fma_f32 v121,v33,v33,s88
M 18,a[200:203],70
L 3
s_nop 0
v_mul_f32 v118,v30,v118
M 18,a[208:211],74
s_waitcnt vmcnt(0)
R 94,1,31744
v_mul_f32 v119,v31,v119
M 18,a[216:219],78
s_barrier
buffer_store_dword v4,v3,s[36:39],s70 offen offset:0 sc0 sc1
M 22,a[212:215],74
s_mov_b32 s70,s69
ds_read_b32 v7,v6 offset:0
ds_read_b64 v[98:99],v13 offset:0
M 22,a[220:223],78
L 4
v_mul_f32 v120,v32,v120
v_mul_f32 v121,v33,v121
M 22,a[228:231],82
s_mov_b32 s69,s68
v_exp_f32 v118,v118
M 22,a[236:239],86
s_add_u32 s68,s68,s63
v_exp_f32 v119,v119
M 18,a[224:227],82
v_exp_f32 v120,v120
M 18,a[232:235],86
L 2
v_exp_f32 v121,v121
M 18,a[240:243],90
L 1
v_fma_f32 v118,v118,s89,s89
v_fma_f32 v119,v119,s89,s89
v_fma_f32 v120,v120,s89,s89
v_fma_f32 v121,v121,s89,s89
v_rcp_f32 v118,v118
v_rcp_f32 v119,v119
v_rcp_f32 v120,v120
v_rcp_f32 v121,v121
M 18,a[248:251],94
v_mul_f32 v30,v30,v118
v_mul_f32 v31,v31,v119
v_mul_f32 v32,v32,v120
v_mul_f32 v33,v33,v121
v_cvt_pk_bf16_f32 v124,v30,v31
v_cvt_pk_bf16_f32 v125,v32,v33
M 22,a[244:247],90
buffer_store_dwordx4 v[122:125],v0,s[36:39],s67 offen offset:0 sc0 sc1
v_cmp_eq_u32 vcc,s53,v7
M 22,a[252:255],94
s_mov_b32 s80,1
)"
R"(s_cmp_eq_u32 vcc_lo,-1
s_cbranch_scc0 Lg3o_xn0_%=
buffer_load_dword v130,v11,s[36:39],s73 offen offset:0 sc1
L 0
v_fma_f32 v18,-v98,v106,v18
v_fma_f32 v19,-v98,v107,v19
v_fma_f32 v20,-v98,v108,v20
v_fma_f32 v21,-v98,v109,v21
v_mul_f32 v18,v99,v18
v_mul_f32 v19,v99,v19
s_add_u32 s73,s73,s64
s_add_u32 m0,s57,0
s_add_u32 s56,s55,0
D 0,56,0
v_mul_f32 v20,v99,v20
v_mul_f32 v21,v99,v21
v_fma_f32 v114,v18,v18,s88
v_fma_f32 v115,v19,v19,s88
v_fma_f32 v116,v20,v20,s88
v_fma_f32 v117,v21,v21,s88
D 0,56,1024
v_mul_f32 v114,v18,v114
v_mul_f32 v115,v19,v115
v_mul_f32 v116,v20,v116
v_mul_f32 v117,v21,v117
v_exp_f32 v114,v114
v_exp_f32 v115,v115
D 0,56,2048
v_exp_f32 v116,v116
v_exp_f32 v117,v117
v_fma_f32 v114,v114,s89,s89
v_fma_f32 v115,v115,s89,s89
v_fma_f32 v116,v116,s89,s89
v_fma_f32 v117,v117,s89,s89
D 0,56,3072
v_rcp_f32 v114,v114
v_rcp_f32 v115,v115
v_rcp_f32 v116,v116
v_rcp_f32 v117,v117
v_mul_f32 v18,v18,v114
v_mul_f32 v19,v19,v115
s_add_u32 s58,s58,s61
s_add_u32 m0,s57,16384
s_add_u32 s56,s55,16384
D 0,56,0
v_mul_f32 v20,v20,v116
v_mul_f32 v21,v21,v117
v_cvt_pk_bf16_f32 v122,v18,v19
v_cvt_pk_bf16_f32 v123,v20,v21
v_fma_f32 v22,-v98,v110,v22
v_fma_f32 v23,-v98,v111,v23
D 0,56,1024
v_fma_f32 v24,-v98,v112,v24
v_fma_f32 v25,-v98,v113,v25
v_mul_f32 v22,v99,v22
v_mul_f32 v23,v99,v23
v_mul_f32 v24,v99,v24
v_mul_f32 v25,v99,v25
D 0,56,2048
v_fma_f32 v118,v22,v22,s88
v_fma_f32 v119,v23,v23,s88
v_fma_f32 v120,v24,v24,s88
v_fma_f32 v121,v25,v25,s88
v_mul_f32 v118,v22,v118
v_mul_f32 v119,v23,v119
D 0,56,3072
v_mul_f32 v120,v24,v120
v_mul_f32 v121,v25,v121
v_exp_f32 v118,v118
v_exp_f32 v119,v119
v_exp_f32 v120,v120
v_exp_f32 v121,v121
s_add_u32 s55,s55,s60
s_sub_u32 s57,s84,s57
s_add_u32 s54,s54,s46
v_fma_f32 v118,v118,s89,s89
v_fma_f32 v119,v119,s89,s89
v_fma_f32 v120,v120,s89,s89
v_fma_f32 v121,v121,s89,s89
v_rcp_f32 v118,v118
v_rcp_f32 v119,v119
v_rcp_f32 v120,v120
v_rcp_f32 v121,v121
v_mul_f32 v22,v22,v118
v_mul_f32 v23,v23,v119
v_mul_f32 v24,v24,v120
v_mul_f32 v25,v25,v121
v_cvt_pk_bf16_f32 v124,v22,v23
v_cvt_pk_bf16_f32 v125,v24,v25
buffer_store_dwordx4 v[122:125],v0,s[36:39],s66 offen offset:0 sc0 sc1
s_mov_b32 s72,s69
s_mov_b32 s69,0x7fff0000
s_mov_b32 s66,0x7fff0000
s_mov_b32 s76,0x7fff0000
s_branch Lg3o_gox_%=
Lg3o_xn0_%=:
s_cmp_ge_u32 s54,256
s_cbranch_scc1 Lg3o_fin0_%=
s_add_u32 m0,s59,4096
s_nop 0
buffer_load_dword v8,s[36:39],s58 offen offset:0 sc1 lds
buffer_store_dword v4,v3,s[36:39],s71 offen offset:0 sc0 sc1
s_add_u32 m0,s59,5120
s_nop 0
buffer_load_dword v8,s[36:39],s58 offen offset:0 sc1 lds
buffer_store_dword v4,v3,s[36:39],s71 offen offset:0 sc0 sc1
s_mov_b32 s83,0
L 0
v_fma_f32 v18,-v98,v106,v18
v_fma_f32 v19,-v98,v107,v19
v_fma_f32 v20,-v98,v108,v20
v_fma_f32 v21,-v98,v109,v21
v_mul_f32 v18,v99,v18
v_mul_f32 v19,v99,v19
v_mul_f32 v20,v99,v20
v_mul_f32 v21,v99,v21
v_fma_f32 v114,v18,v18,s88
v_fma_f32 v115,v19,v19,s88
v_fma_f32 v116,v20,v20,s88
v_fma_f32 v117,v21,v21,s88
v_mul_f32 v114,v18,v114
v_mul_f32 v115,v19,v115
v_mul_f32 v116,v20,v116
v_mul_f32 v117,v21,v117
v_exp_f32 v114,v114
v_exp_f32 v115,v115
v_exp_f32 v116,v116
v_exp_f32 v117,v117
v_fma_f32 v114,v114,s89,s89
v_fma_f32 v115,v115,s89,s89
v_fma_f32 v116,v116,s89,s89
v_fma_f32 v117,v117,s89,s89
v_rcp_f32 v114,v114
v_rcp_f32 v115,v115
v_rcp_f32 v116,v116
v_rcp_f32 v117,v117
v_mul_f32 v18,v18,v114
v_mul_f32 v19,v19,v115
v_mul_f32 v20,v20,v116
v_mul_f32 v21,v21,v117
v_cvt_pk_bf16_f32 v122,v18,v19
v_cvt_pk_bf16_f32 v123,v20,v21
v_fma_f32 v22,-v98,v110,v22
v_fma_f32 v23,-v98,v111,v23
v_fma_f32 v24,-v98,v112,v24
v_fma_f32 v25,-v98,v113,v25
v_mul_f32 v22,v99,v22
v_mul_f32 v23,v99,v23
v_mul_f32 v24,v99,v24
v_mul_f32 v25,v99,v25
v_fma_f32 v118,v22,v22,s88
v_fma_f32 v119,v23,v23,s88
v_fma_f32 v120,v24,v24,s88
v_fma_f32 v121,v25,v25,s88
v_mul_f32 v118,v22,v118
v_mul_f32 v119,v23,v119
v_mul_f32 v120,v24,v120
v_mul_f32 v121,v25,v121
v_exp_f32 v118,v118
v_exp_f32 v119,v119
v_exp_f32 v120,v120
v_exp_f32 v121,v121
v_fma_f32 v118,v118,s89,s89
v_fma_f32 v119,v119,s89,s89
v_fma_f32 v120,v120,s89,s89
v_fma_f32 v121,v121,s89,s89
v_rcp_f32 v118,v118
v_rcp_f32 v119,v119
v_rcp_f32 v120,v120
v_rcp_f32 v121,v121
v_mul_f32 v22,v22,v118
v_mul_f32 v23,v23,v119
v_mul_f32 v24,v24,v120
v_mul_f32 v25,v25,v121
v_cvt_pk_bf16_f32 v124,v22,v23
v_cvt_pk_bf16_f32 v125,v24,v25
buffer_store_dwordx4 v[122:125],v0,s[36:39],s66 offen offset:0 sc0 sc1
s_mov_b32 s72,s69
s_mov_b32 s69,0x7fff0000
s_mov_b32 s66,0x7fff0000
s_mov_b32 s76,0x7fff0000
s_branch Lg3o_poll_%=
Lg3o_fin0_%=:
L 0
v_fma_f32 v18,-v98,v106,v18
v_fma_f32 v19,-v98,v107,v19
v_fma_f32 v20,-v98,v108,v20
v_fma_f32 v21,-v98,v109,v21
v_mul_f32 v18,v99,v18
v_mul_f32 v19,v99,v19
v_mul_f32 v20,v99,v20
v_mul_f32 v21,v99,v21
v_fma_f32 v114,v18,v18,s88
v_fma_f32 v115,v19,v19,s88
v_fma_f32 v116,v20,v20,s88
v_fma_f32 v117,v21,v21,s88
v_mul_f32 v114,v18,v114
v_mul_f32 v115,v19,v115
v_mul_f32 v116,v20,v116
v_mul_f32 v117,v21,v117
v_exp_f32 v114,v114
v_exp_f32 v115,v115
v_exp_f32 v116,v116
v_exp_f32 v117,v117
v_fma_f32 v114,v114,s89,s89
v_fma_f32 v115,v115,s89,s89
v_fma_f32 v116,v116,s89,s89
v_fma_f32 v117,v117,s89,s89
v_rcp_f32 v114,v114
v_rcp_f32 v115,v115
v_rcp_f32 v116,v116
v_rcp_f32 v117,v117
v_mul_f32 v18,v18,v114
v_mul_f32 v19,v19,v115
v_mul_f32 v20,v20,v116
v_mul_f32 v21,v21,v117
v_cvt_pk_bf16_f32 v122,v18,v19
v_cvt_pk_bf16_f32 v123,v20,v21
v_fma_f32 v22,-v98,v110,v22
v_fma_f32 v23,-v98,v111,v23
v_fma_f32 v24,-v98,v112,v24
v_fma_f32 v25,-v98,v113,v25
v_mul_f32 v22,v99,v22
v_mul_f32 v23,v99,v23
v_mul_f32 v24,v99,v24
v_mul_f32 v25,v99,v25
v_fma_f32 v118,v22,v22,s88
v_fma_f32 v119,v23,v23,s88
v_fma_f32 v120,v24,v24,s88
v_fma_f32 v121,v25,v25,s88
v_mul_f32 v118,v22,v118
v_mul_f32 v119,v23,v119
v_mul_f32 v120,v24,v120
v_mul_f32 v121,v25,v121
v_exp_f32 v118,v118
v_exp_f32 v119,v119
v_exp_f32 v120,v120
v_exp_f32 v121,v121
v_fma_f32 v118,v118,s89,s89
v_fma_f32 v119,v119,s89,s89
v_fma_f32 v120,v120,s89,s89
v_fma_f32 v121,v121,s89,s89
v_rcp_f32 v118,v118
v_rcp_f32 v119,v119
v_rcp_f32 v120,v120
v_rcp_f32 v121,v121
v_mul_f32 v22,v22,v118
v_mul_f32 v23,v23,v119
v_mul_f32 v24,v24,v120
v_mul_f32 v25,v25,v121
v_cvt_pk_bf16_f32 v124,v22,v23
v_cvt_pk_bf16_f32 v125,v24,v25
buffer_store_dwordx4 v[122:125],v0,s[36:39],s66 offen offset:0 sc0 sc1
s_mov_b32 s72,s69
s_mov_b32 s69,0x7fff0000
s_mov_b32 s66,0x7fff0000
s_mov_b32 s76,0x7fff0000
s_branch Lg3o_done_%=
Lg3o_K1_%=:
Lg3o_L1_%=:
v_mfma_f32_16x16x32_bf16 v[30:33],a[4:7],v[34:37],0
s_add_u32 m0,s59,3072
R 54,2,5120
v_add_f32_dpp v131,v130,v130 quad_perm:[1,0,3,2] row_mask:0xf bank_mask:0xf
M 30,a[12:15],38
buffer_load_dword v8,s[36:39],s58 offen offset:0 sc1 lds
v_mfma_f32_16x16x32_bf16 v[26:29],a[0:3],v[34:37],0
s_mov_b32 s67,s65
R 58,2,6144
v_add_f32_dpp v131,v131,v131 quad_perm:[2,3,0,1] row_mask:0xf bank_mask:0xf
M 26,a[8:11],38
L 3
v_fma_f32 v18,-v98,v106,v18
v_fma_f32 v19,-v98,v107,v19
M 26,a[16:19],42
s_add_u32 s65,s65,s62
R 62,2,7168
v_add_f32_dpp v134,v131,v131 row_half_mirror row_mask:0xf bank_mask:0xf
M 26,a[24:27],46
v_fma_f32 v20,-v98,v108,v20
v_fma_f32 v21,-v98,v109,v21
M 30,a[20:23],42
R 66,2,8192
v_mov_b32_dpp v131,v134 row_ror:8 row_mask:0xf bank_mask:0xf
M 30,a[28:31],46
L 3
v_fma_f32 v131,-v134,v134,v131
v_max_f32 v131,s87,v131
M 30,a[36:39],50
R 70,2,9216
v_mul_f32 v134,v15,v134
M 30,a[44:47],54
v_rsq_f32 v132,v131
M 26,a[32:35],50
R 74,2,10240
v_fma_f32 v134,v132,v16,v134
M 26,a[40:43],54
L 3
ds_write_b32 v12,v134 offset:2048
M 26,a[48:51],58
R 78,2,11264
v_mul_f32 v18,v99,v18
M 26,a[56:59],62
v_mul_f32 v19,v99,v19
v_mul_f32 v20,v99,v20
M 30,a[52:55],58
R 82,2,12288
v_mul_f32 v21,v99,v21
M 30,a[60:63],62
L 4
v_fma_f32 v114,v18,v18,s88
v_fma_f32 v115,v19,v19,s88
M 30,a[68:71],66
R 86,2,13312
v_fma_f32 v116,v20,v20,s88
M 30,a[76:79],70
v_fma_f32 v117,v21,v21,s88
v_mul_f32 v114,v18,v114
M 26,a[64:67],66
R 90,2,14336
v_mul_f32 v115,v19,v115
M 26,a[72:75],70
L 3
v_exp_f32 v114,v114
M 26,a[80:83],74
s_waitcnt vmcnt(0)
R 94,2,15360
v_mul_f32 v116,v20,v116
M 26,a[88:91],78
s_barrier
ds_read_b32 v7,v6 offset:3072
v_mul_f32 v117,v21,v117
M 30,a[84:87],74
R 34,2,16384
v_fma_f32 v114,v114,s89,s89
M 30,a[92:95],78
L 4
buffer_store_dword v4,v3,s[36:39],s70 offen offset:0 sc0 sc1
M 30,a[100:103],82
s_mov_b32 s70,0x7fff0000
R 38,2,17408
v_fma_f32 v22,-v98,v110,v22
M 30,a[108:111],86
v_exp_f32 v115,v115
M 26,a[96:99],82
R 42,2,18432
v_fma_f32 v115,v115,s89,s89
M 26,a[104:107],86
L 4
L 3
v_cmp_eq_u32 vcc,s53,v7
M 26,a[112:115],90
R 46,2,19456
v_fma_f32 v23,-v98,v111,v23
M 26,a[120:123],94
v_exp_f32 v116,v116
M 30,a[116:119],90
s_cmp_eq_u32 vcc_lo,-1
R 50,2,20480
v_fma_f32 v116,v116,s89,s89
s_cbranch_scc0 Lg3o_X1_%=
M 30,a[124:127],94
L 3
s_add_u32 m0,s57,0
s_add_u32 s56,s55,0
buffer_load_dword v130,v11,s[36:39],s73 offen offset:0 sc1
M 30,a[132:135],34
s_add_u32 s58,s58,s61
R 54,2,21504
v_fma_f32 v24,-v98,v112,v24
M 30,a[140:143],38
D 0,56,0
M 26,a[128:131],34
s_add_u32 s54,s54,s46
R 58,2,22528
v_fma_f32 v25,-v98,v113,v25
M 26,a[136:139],38
L 3
v_exp_f32 v117,v117
M 26,a[144:147],42
s_add_u32 s73,s73,s64
R 62,2,23552
v_fma_f32 v117,v117,s89,s89
M 26,a[152:155],46
D 0,56,1024
M 30,a[148:151],42
R 66,2,24576
v_mul_f32 v22,v99,v22
M 30,a[156:159],46
L 3
v_rcp_f32 v114,v114
M 30,a[164:167],50
R 70,2,25600
v_mul_f32 v18,v18,v114
M 30,a[172:175],54
D 0,56,2048
M 26,a[160:163],50
R 74,2,26624
v_mul_f32 v23,v99,v23
M 26,a[168:171],54
L 3
v_rcp_f32 v115,v115
M 26,a[176:179],58
R 78,2,27648
v_mul_f32 v19,v19,v115
M 26,a[184:187],62
D 0,56,3072
M 30,a[180:183],58
s_add_u32 m0,s59,1024
R 82,2,28672
v_cvt_pk_bf16_f32 v122,v18,v19
M 30,a[188:191],62
L 3
buffer_load_dword v8,s[36:39],s58 offen offset:0 sc1 lds
M 30,a[196:199],66
s_add_u32 m0,s57,16384
R 86,2,29696
v_mul_f32 v24,v99,v24
M 30,a[204:207],70
s_add_u32 s56,s55,16384
v_rcp_f32 v116,v116
M 26,a[192:195],66
s_add_u32 s55,s55,s60
R 90,2,30720
v_mul_f32 v20,v20,v116
M 26,a[200:203],70
L 3
s_nop 0
D 0,56,0
M 26,a[208:211],74
s_waitcnt vmcnt(1)
R 94,2,31744
v_mul_f32 v25,v99,v25
M 26,a[216:219],78
s_barrier
ds_read_b32 v7,v6 offset:1024
ds_read_b64 v[102:103],v13 offset:2048
M 30,a[212:215],74
s_sub_u32 s57,s84,s57
R 34,1,0
v_fma_f32 v118,v22,v22,s88
M 30,a[220:223],78
L 5
buffer_store_dword v4,v3,s[36:39],s70 offen offset:0 sc0 sc1
M 30,a[228:231],82
s_mov_b32 s70,s69
R 38,1,1024
v_fma_f32 v119,v23,v23,s88
M 30,a[236:239],86
D 0,56,1024
M 26,a[224:227],82
s_mov_b32 s69,s68
R 42,1,2048
v_fma_f32 v120,v24,v24,s88
M 26,a[232:235],86
L 5
v_fma_f32 v121,v25,v25,s88
v_mul_f32 v118,v22,v118
v_mul_f32 v119,v23,v119
v_mul_f32 v120,v24,v120
v_mul_f32 v121,v25,v121
v_exp_f32 v118,v118
v_exp_f32 v119,v119
v_exp_f32 v120,v120
v_exp_f32 v121,v121
M 26,a[240:243],90
L 4
R 46,1,3072
v_rcp_f32 v117,v117
v_fma_f32 v118,v118,s89,s89
v_fma_f32 v119,v119,s89,s89
v_fma_f32 v120,v120,s89,s89
v_fma_f32 v121,v121,s89,s89
v_rcp_f32 v118,v118
v_rcp_f32 v119,v119
v_rcp_f32 v120,v120
v_rcp_f32 v121,v121
v_cmp_eq_u32 vcc,s53,v7
M 26,a[248:251],94
s_add_u32 s68,s68,s63
v_mul_f32 v21,v21,v117
v_cvt_pk_bf16_f32 v123,v20,v21
v_mul_f32 v22,v22,v118
v_mul_f32 v23,v23,v119
v_mul_f32 v24,v24,v120
v_mul_f32 v25,v25,v121
v_cvt_pk_bf16_f32 v124,v22,v23
v_cvt_pk_bf16_f32 v125,v24,v25
M 30,a[244:247],90
L 2
s_cmp_eq_u32 vcc_lo,-1
R 50,1,4096
buffer_store_dwordx4 v[122:125],v0,s[36:39],s66 offen offset:0 sc0 sc1
M 30,a[252:255],94
D 0,56,2048
D 0,56,3072
buffer_store_dword v4,v3,s[36:39],s71 offen offset:0 sc0 sc1
buffer_store_dword v4,v3,s[36:39],s71 offen offset:0 sc0 sc1
s_cbranch_scc1 Lg3o_F0_%=
s_branch Lg3o_K0_%=
)"
R"(Lg3o_X1_%=:
M 30,a[124:127],94
L 3
s_add_u32 m0,s59,1024
v_fma_f32 v24,-v98,v112,v24
M 30,a[132:135],34
R 54,2,21504
v_fma_f32 v25,-v98,v113,v25
M 30,a[140:143],38
buffer_load_dword v8,s[36:39],s58 offen offset:0 sc1 lds
M 26,a[128:131],34
R 58,2,22528
v_mul_f32 v22,v99,v22
M 26,a[136:139],38
L 3
v_exp_f32 v117,v117
M 26,a[144:147],42
R 62,2,23552
v_fma_f32 v117,v117,s89,s89
M 26,a[152:155],46
v_rcp_f32 v114,v114
M 30,a[148:151],42
R 66,2,24576
v_mul_f32 v18,v18,v114
M 30,a[156:159],46
L 3
v_rcp_f32 v115,v115
M 30,a[164:167],50
R 70,2,25600
v_mul_f32 v19,v19,v115
M 30,a[172:175],54
v_rcp_f32 v116,v116
M 26,a[160:163],50
R 74,2,26624
v_mul_f32 v20,v20,v116
M 26,a[168:171],54
L 3
v_rcp_f32 v117,v117
M 26,a[176:179],58
R 78,2,27648
v_mul_f32 v21,v21,v117
M 26,a[184:187],62
v_cvt_pk_bf16_f32 v122,v18,v19
v_cvt_pk_bf16_f32 v123,v20,v21
M 30,a[180:183],58
R 82,2,28672
v_mul_f32 v23,v99,v23
M 30,a[188:191],62
L 3
v_mul_f32 v24,v99,v24
v_mul_f32 v25,v99,v25
M 30,a[196:199],66
R 86,2,29696
v_fma_f32 v118,v22,v22,s88
M 30,a[204:207],70
v_fma_f32 v119,v23,v23,s88
v_fma_f32 v120,v24,v24,s88
M 26,a[192:195],66
R 90,2,30720
v_fma_f32 v121,v25,v25,s88
M 26,a[200:203],70
L 3
s_nop 0
v_mul_f32 v118,v22,v118
M 26,a[208:211],74
s_waitcnt vmcnt(0)
R 94,2,31744
v_mul_f32 v119,v23,v119
M 26,a[216:219],78
s_barrier
buffer_store_dword v4,v3,s[36:39],s70 offen offset:0 sc0 sc1
M 30,a[212:215],74
s_mov_b32 s70,s69
ds_read_b32 v7,v6 offset:1024
ds_read_b64 v[102:103],v13 offset:2048
M 30,a[220:223],78
L 4
v_mul_f32 v120,v24,v120
v_mul_f32 v121,v25,v121
M 30,a[228:231],82
s_mov_b32 s69,s68
v_exp_f32 v118,v118
M 30,a[236:239],86
s_add_u32 s68,s68,s63
v_exp_f32 v119,v119
M 26,a[224:227],82
v_exp_f32 v120,v120
M 26,a[232:235],86
L 2
v_exp_f32 v121,v121
M 26,a[240:243],90
L 1
v_fma_f32 v118,v118,s89,s89
v_fma_f32 v119,v119,s89,s89
v_fma_f32 v120,v120,s89,s89
v_fma_f32 v121,v121,s89,s89
v_rcp_f32 v118,v118
v_rcp_f32 v119,v119
v_rcp_f32 v120,v120
v_rcp_f32 v121,v121
M 26,a[248:251],94
v_mul_f32 v22,v22,v118
v_mul_f32 v23,v23,v119
v_mul_f32 v24,v24,v120
v_mul_f32 v25,v25,v121
v_cvt_pk_bf16_f32 v124,v22,v23
v_cvt_pk_bf16_f32 v125,v24,v25
M 30,a[244:247],90
buffer_store_dwordx4 v[122:125],v0,s[36:39],s66 offen offset:0 sc0 sc1
v_cmp_eq_u32 vcc,s53,v7
M 30,a[252:255],94
s_mov_b32 s80,0
s_cmp_eq_u32 vcc_lo,-1
s_cbranch_scc0 Lg3o_xn1_%=
buffer_load_dword v130,v11,s[36:39],s73 offen offset:0 sc1
L 0
v_fma_f32 v26,-v102,v106,v26
v_fma_f32 v27,-v102,v107,v27
v_fma_f32 v28,-v102,v108,v28
v_fma_f32 v29,-v102,v109,v29
v_mul_f32 v26,v103,v26
v_mul_f32 v27,v103,v27
s_add_u32 s73,s73,s64
s_add_u32 m0,s57,0
s_add_u32 s56,s55,0
D 0,56,0
v_mul_f32 v28,v103,v28
v_mul_f32 v29,v103,v29
v_fma_f32 v114,v26,v26,s88
v_fma_f32 v115,v27,v27,s88
v_fma_f32 v116,v28,v28,s88
v_fma_f32 v117,v29,v29,s88
D 0,56,1024
v_mul_f32 v114,v26,v114
v_mul_f32 v115,v27,v115
v_mul_f32 v116,v28,v116
v_mul_f32 v117,v29,v117
v_exp_f32 v114,v114
v_exp_f32 v115,v115
D 0,56,2048
v_exp_f32 v116,v116
v_exp_f32 v117,v117
v_fma_f32 v114,v114,s89,s89
v_fma_f32 v115,v115,s89,s89
v_fma_f32 v116,v116,s89,s89
v_fma_f32 v117,v117,s89,s89
D 0,56,3072
v_rcp_f32 v114,v114
v_rcp_f32 v115,v115
v_rcp_f32 v116,v116
v_rcp_f32 v117,v117
v_mul_f32 v26,v26,v114
v_mul_f32 v27,v27,v115
s_add_u32 s58,s58,s61
s_add_u32 m0,s57,16384
s_add_u32 s56,s55,16384
D 0,56,0
v_mul_f32 v28,v28,v116
v_mul_f32 v29,v29,v117
v_cvt_pk_bf16_f32 v122,v26,v27
v_cvt_pk_bf16_f32 v123,v28,v29
v_fma_f32 v30,-v102,v110,v30
v_fma_f32 v31,-v102,v111,v31
D 0,56,1024
v_fma_f32 v32,-v102,v112,v32
v_fma_f32 v33,-v102,v113,v33
v_mul_f32 v30,v103,v30
v_mul_f32 v31,v103,v31
v_mul_f32 v32,v103,v32
v_mul_f32 v33,v103,v33
D 0,56,2048
v_fma_f32 v118,v30,v30,s88
v_fma_f32 v119,v31,v31,s88
v_fma_f32 v120,v32,v32,s88
v_fma_f32 v121,v33,v33,s88
v_mul_f32 v118,v30,v118
v_mul_f32 v119,v31,v119
D 0,56,3072
v_mul_f32 v120,v32,v120
v_mul_f32 v121,v33,v121
v_exp_f32 v118,v118
v_exp_f32 v119,v119
v_exp_f32 v120,v120
v_exp_f32 v121,v121
s_add_u32 s55,s55,s60
s_sub_u32 s57,s84,s57
s_add_u32 s54,s54,s46
v_fma_f32 v118,v118,s89,s89
v_fma_f32 v119,v119,s89,s89
v_fma_f32 v120,v120,s89,s89
v_fma_f32 v121,v121,s89,s89
v_rcp_f32 v118,v118
v_rcp_f32 v119,v119
v_rcp_f32 v120,v120
v_rcp_f32 v121,v121
v_mul_f32 v30,v30,v118
v_mul_f32 v31,v31,v119
v_mul_f32 v32,v32,v120
v_mul_f32 v33,v33,v121
v_cvt_pk_bf16_f32 v124,v30,v31
v_cvt_pk_bf16_f32 v125,v32,v33
buffer_store_dwordx4 v[122:125],v0,s[36:39],s67 offen offset:0 sc0 sc1
s_mov_b32 s72,s69
s_mov_b32 s69,0x7fff0000
s_mov_b32 s67,0x7fff0000
s_mov_b32 s77,0x7fff0000
s_branch Lg3o_gox_%=
Lg3o_xn1_%=:
s_cmp_ge_u32 s54,256
s_cbranch_scc1 Lg3o_fin1_%=
s_add_u32 m0,s59,4096
s_nop 0
buffer_load_dword v8,s[36:39],s58 offen offset:0 sc1 lds
buffer_store_dword v4,v3,s[36:39],s71 offen offset:0 sc0 sc1
s_add_u32 m0,s59,5120
s_nop 0
buffer_load_dword v8,s[36:39],s58 offen offset:0 sc1 lds
buffer_store_dword v4,v3,s[36:39],s71 offen offset:0 sc0 sc1
s_mov_b32 s83,0
L 0
v_fma_f32 v26,-v102,v106,v26
v_fma_f32 v27,-v102,v107,v27
v_fma_f32 v28,-v102,v108,v28
v_fma_f32 v29,-v102,v109,v29
v_mul_f32 v26,v103,v26
v_mul_f32 v27,v103,v27
v_mul_f32 v28,v103,v28
v_mul_f32 v29,v103,v29
v_fma_f32 v114,v26,v26,s88
v_fma_f32 v115,v27,v27,s88
v_fma_f32 v116,v28,v28,s88
v_fma_f32 v117,v29,v29,s88
v_mul_f32 v114,v26,v114
v_mul_f32 v115,v27,v115
v_mul_f32 v116,v28,v116
v_mul_f32 v117,v29,v117
v_exp_f32 v114,v114
v_exp_f32 v115,v115
v_exp_f32 v116,v116
v_exp_f32 v117,v117
v_fma_f32 v114,v114,s89,s89
v_fma_f32 v115,v115,s89,s89
v_fma_f32 v116,v116,s89,s89
v_fma_f32 v117,v117,s89,s89
v_rcp_f32 v114,v114
v_rcp_f32 v115,v115
v_rcp_f32 v116,v116
v_rcp_f32 v117,v117
v_mul_f32 v26,v26,v114
v_mul_f32 v27,v27,v115
v_mul_f32 v28,v28,v116
v_mul_f32 v29,v29,v117
v_cvt_pk_bf16_f32 v122,v26,v27
v_cvt_pk_bf16_f32 v123,v28,v29
v_fma_f32 v30,-v102,v110,v30
v_fma_f32 v31,-v102,v111,v31
v_fma_f32 v32,-v102,v112,v32
v_fma_f32 v33,-v102,v113,v33
v_mul_f32 v30,v103,v30
v_mul_f32 v31,v103,v31
v_mul_f32 v32,v103,v32
v_mul_f32 v33,v103,v33
v_fma_f32 v118,v30,v30,s88
v_fma_f32 v119,v31,v31,s88
v_fma_f32 v120,v32,v32,s88
v_fma_f32 v121,v33,v33,s88
v_mul_f32 v118,v30,v118
v_mul_f32 v119,v31,v119
v_mul_f32 v120,v32,v120
v_mul_f32 v121,v33,v121
v_exp_f32 v118,v118
v_exp_f32 v119,v119
v_exp_f32 v120,v120
v_exp_f32 v121,v121
v_fma_f32 v118,v118,s89,s89
v_fma_f32 v119,v119,s89,s89
v_fma_f32 v120,v120,s89,s89
v_fma_f32 v121,v121,s89,s89
v_rcp_f32 v118,v118
v_rcp_f32 v119,v119
v_rcp_f32 v120,v120
v_rcp_f32 v121,v121
v_mul_f32 v30,v30,v118
v_mul_f32 v31,v31,v119
v_mul_f32 v32,v32,v120
v_mul_f32 v33,v33,v121
v_cvt_pk_bf16_f32 v124,v30,v31
v_cvt_pk_bf16_f32 v125,v32,v33
buffer_store_dwordx4 v[122:125],v0,s[36:39],s67 offen offset:0 sc0 sc1
s_mov_b32 s72,s69
s_mov_b32 s69,0x7fff0000
s_mov_b32 s67,0x7fff0000
s_mov_b32 s77,0x7fff0000
s_branch Lg3o_poll_%=
Lg3o_fin1_%=:
L 0
v_fma_f32 v26,-v102,v106,v26
v_fma_f32 v27,-v102,v107,v27
v_fma_f32 v28,-v102,v108,v28
v_fma_f32 v29,-v102,v109,v29
v_mul_f32 v26,v103,v26
v_mul_f32 v27,v103,v27
v_mul_f32 v28,v103,v28
v_mul_f32 v29,v103,v29
v_fma_f32 v114,v26,v26,s88
v_fma_f32 v115,v27,v27,s88
v_fma_f32 v116,v28,v28,s88
v_fma_f32 v117,v29,v29,s88
v_mul_f32 v114,v26,v114
v_mul_f32 v115,v27,v115
v_mul_f32 v116,v28,v116
v_mul_f32 v117,v29,v117
v_exp_f32 v114,v114
v_exp_f32 v115,v115
v_exp_f32 v116,v116
v_exp_f32 v117,v117
v_fma_f32 v114,v114,s89,s89
v_fma_f32 v115,v115,s89,s89
v_fma_f32 v116,v116,s89,s89
v_fma_f32 v117,v117,s89,s89
v_rcp_f32 v114,v114
v_rcp_f32 v115,v115
v_rcp_f32 v116,v116
v_rcp_f32 v117,v117
v_mul_f32 v26,v26,v114
v_mul_f32 v27,v27,v115
v_mul_f32 v28,v28,v116
v_mul_f32 v29,v29,v117
v_cvt_pk_bf16_f32 v122,v26,v27
v_cvt_pk_bf16_f32 v123,v28,v29
v_fma_f32 v30,-v102,v110,v30
v_fma_f32 v31,-v102,v111,v31
v_fma_f32 v32,-v102,v112,v32
v_fma_f32 v33,-v102,v113,v33
v_mul_f32 v30,v103,v30
v_mul_f32 v31,v103,v31
v_mul_f32 v32,v103,v32
v_mul_f32 v33,v103,v33
v_fma_f32 v118,v30,v30,s88
v_fma_f32 v119,v31,v31,s88
v_fma_f32 v120,v32,v32,s88
v_fma_f32 v121,v33,v33,s88
v_mul_f32 v118,v30,v118
v_mul_f32 v119,v31,v119
v_mul_f32 v120,v32,v120
v_mul_f32 v121,v33,v121
v_exp_f32 v118,v118
v_exp_f32 v119,v119
v_exp_f32 v120,v120
v_exp_f32 v121,v121
v_fma_f32 v118,v118,s89,s89
v_fma_f32 v119,v119,s89,s89
v_fma_f32 v120,v120,s89,s89
v_fma_f32 v121,v121,s89,s89
v_rcp_f32 v118,v118
v_rcp_f32 v119,v119
v_rcp_f32 v120,v120
v_rcp_f32 v121,v121
v_mul_f32 v30,v30,v118
v_mul_f32 v31,v31,v119
v_mul_f32 v32,v32,v120
v_mul_f32 v33,v33,v121
v_cvt_pk_bf16_f32 v124,v30,v31
v_cvt_pk_bf16_f32 v125,v32,v33
buffer_store_dwordx4 v[122:125],v0,s[36:39],s67 offen offset:0 sc0 sc1
s_mov_b32 s72,s69
s_mov_b32 s69,0x7fff0000
s_mov_b32 s67,0x7fff0000
s_mov_b32 s77,0x7fff0000
s_branch Lg3o_done_%=
Lg3o_done_%=:
s_waitcnt vmcnt(1)
s_barrier
buffer_store_dword v4,v3,s[36:39],s70 offen offset:0 sc0 sc1
s_waitcnt vmcnt(1)
s_barrier
buffer_store_dword v4,v3,s[36:39],s72 offen offset:0 sc0 sc1
s_waitcnt vmcnt(0) lgkmcnt(0)
s_barrier
s_endpgm
Lcode_g3o_e:
.purgem M
.purgem R
.purgem T
.purgem D
.purgem L
)"
  :: "s"(ws), "s"(__builtin_amdgcn_readfirstlane(sWave)), "s"(__builtin_amdgcn_readfirstlane(sG0)), "s"(__builtin_amdgcn_readfirstlane(sStep)), "s"(__builtin_amdgcn_readfirstlane(sOutOff)), "s"(__builtin_amdgcn_readfirstlane(sFoutOff)), "s"(__builtin_amdgcn_readfirstlane(sAuxOff)), "s"(__builtin_amdgcn_readfirstlane(sWw)), "s"(__builtin_amdgcn_readfirstlane(sBuf)), "s"(__builtin_amdgcn_readfirstlane(sExp)), "v"(vL16), "v"(vFlag), "v"(vSnapOff), "v"(vSnapRd), "v"(vWd0), "v"(vWd1), "v"(vWr0), "v"(vWr1), "v"(vWr2), "v"(vOutS), "v"(vAux0), "v"(vAux1), "v"(vAux2), "v"(vAux3), "v"(vAn), "v"(vAe), "s"(w3), "s"(out) : "v255", "v0", "v1", "v2", "v3", "v4", "v5", "v6", "v7", "v8", "v9", "v10", "v11", "v12", "v13", "v14", "v15", "v16", "v17", "v18", "v19", "v20", "v21", "v22", "v23", "v24", "v25", "v26", "v27", "v28", "v29", "v30", "v31", "v32", "v33", "v34", "v35", "v36", "v37", "v38", "v39", "v40", "v41", "v42", "v43", "v44", "v45", "v46", "v47", "v48", "v49", "v50", "v51", "v52", "v53", "v54", "v55", "v56", "v57", "v58", "v59", "v60", "v61", "v62", "v63", "v64", "v65", "v66", "v67", "v68", "v69", "v70", "v71", "v72", "v73", "v74", "v75", "v76", "v77", "v78", "v79", "v80", "v81", "v82", "v83", "v84", "v85", "v86", "v87", "v88", "v89", "v90", "v91", "v92", "v93", "v94", "v95", "v96", "v97", "v98", "v99", "v100", "v101", "v102", "v103", "v104", "v105", "v106", "v107", "v108", "v109", "v110", "v111", "v112", "v113", "v114", "v115", "v116", "v117", "v118", "v119", "v120", "v121", "v122", "v123", "v124", "v125", "v126", "v127", "v128", "v129", "v130", "v131", "v132", "v133", "v134", "v135", "v136", "v137", "v138", "v139", "v140", "v141", "v142", "v143", "v144", "v145", "v146", "v147", "v148", "v149", "v150", "v151", "v152", "v153", "v154", "v155", "v156", "v157", "v158", "v159", "v160", "v161", "v162", "v163", "v164", "v165", "v166", "v167", "v168", "v169", "v170", "v171", "v172", "v173", "v174", "v175", "v176", "v177", "v178", "v179", "v180", "v181", "v182", "v183", "v184", "v185", "v186", "v187", "v188", "v189", "v190", "v191", "v192", "v193", "v194", "v195", "v196", "v197", "v198", "v199", "v200", "v201", "v202", "v203", "v204", "v205", "v206", "v207", "v208", "v209", "v210", "v211", "v212", "v213", "v214", "v215", "v216", "v217", "v218", "v219", "v220", "v221", "v222", "v223", "v224", "v225", "v226", "v227", "v228", "v229", "v230", "v231", "a0", "a1", "a2", "a3", "a4", "a5", "a6", "a7", "a8", "a9", "a10", "a11", "a12", "a13", "a14", "a15", "a16", "a17", "a18", "a19", "a20", "a21", "a22", "a23", "a24", "a25", "a26", "a27", "a28", "a29", "a30", "a31", "a32", "a33", "a34", "a35", "a36", "a37", "a38", "a39", "a40", "a41", "a42", "a43", "a44", "a45", "a46", "a47", "a48", "a49", "a50", "a51", "a52", "a53", "a54", "a55", "a56", "a57", "a58", "a59", "a60", "a61", "a62", "a63", "a64", "a65", "a66", "a67", "a68", "a69", "a70", "a71", "a72", "a73", "a74", "a75", "a76", "a77", "a78", "a79", "a80", "a81", "a82", "a83", "a84", "a85", "a86", "a87", "a88", "a89", "a90", "a91", "a92", "a93", "a94", "a95", "a96", "a97", "a98", "a99", "a100", "a101", "a102", "a103", "a104", "a105", "a106", "a107", "a108", "a109", "a110", "a111", "a112", "a113", "a114", "a115", "a116", "a117", "a118", "a119", "a120", "a121", "a122", "a123", "a124", "a125", "a126", "a127", "a128", "a129", "a130", "a131", "a132", "a133", "a134", "a135", "a136", "a137", "a138", "a139", "a140", "a141", "a142", "a143", "a144", "a145", "a146", "a147", "a148", "a149", "a150", "a151", "a152", "a153", "a154", "a155", "a156", "a157", "a158", "a159", "a160", "a161", "a162", "a163", "a164", "a165", "a166", "a167", "a168", "a169", "a170", "a171", "a172", "a173", "a174", "a175", "a176", "a177", "a178", "a179", "a180", "a181", "a182", "a183", "a184", "a185", "a186", "a187", "a188", "a189", "a190", "a191", "a192", "a193", "a194", "a195", "a196", "a197", "a198", "a199", "a200", "a201", "a202", "a203", "a204", "a205", "a206", "a207", "a208", "a209", "a210", "a211", "a212", "a213", "a214", "a215", "a216", "a217", "a218", "a219", "a220", "a221", "a222", "a223", "a224", "a225", "a226", "a227", "a228", "a229", "a230", "a231", "a232", "a233", "a234", "a235", "a236", "a237", "a238", "a239", "a240", "a241", "a242", "a243", "a244", "a245", "a246", "a247", "a248", "a249", "a250", "a251", "a252", "a253", "a254", "a255", "s36", "s37", "s38", "s39", "s40", "s41", "s42", "s43", "s44", "s45", "s46", "s47", "s48", "s49", "s50", "s51", "s52", "s53", "s54", "s55", "s56", "s57", "s58", "s59", "s60", "s61", "s62", "s63", "s64", "s65", "s66", "s67", "s68", "s69", "s70", "s71", "s72", "s73", "s74", "s75", "s76", "s77", "s78", "s79", "s80", "s81", "s82", "s83", "s84", "s85", "s86", "s87", "s88", "s89", "s90", "s91", "s92", "s93", "s94", "s95", "s96", "s97", "vcc", "scc", "memory"); }
    __builtin_unreachable();
  }
  if (kind < 3) {
    const unsigned sStep = 4;
    COMMON(kind == 1 ? 8 : 16, kind == 1 ? fj * 128 : fj < 8 ? 1024 + fj * 128 : (fj & 2) * 1024 + (fj - 8) * 128, 1024, 131072, 2048, 256, kind == 1 ? 0 : 32768);
    const int sr = l15 - ((wave & 2) ? -1 : 1), sr2 = l15 - ((wave & 2) ? -2 : 2), nx = (slice >> 1) & 1;
    auto src = [&](int r_) { return (unsigned)((r_ & 16) ? 1024 + lg * 64 + (r_ & 3) * 16 : (lg * 16 + r_) * 16); };
    const unsigned vOutS = src(sr2), vSp = (sr2 & 16) ? OOB : vOutS;
    (void)vWr2;
    const unsigned vAn = kind == 1 ? HU + vL16 : src(sr);
    const unsigned vAe = kind == 1 ? ((l15 >> 2) == (nx ? 0 : 3) ? (nx ? 0 : 2 * HU) + 1024 + lg * 64 + (l15 & 3) * 16 : OOB) : (sr & 16) ? OOB : vAn;
    const unsigned vAux0 = l15 * 16, vAux1 = 139264 + (l15 * 2 + (lg & 1)) * 16 + wave * 4 + (lg >> 1) * 512, vAux2 = 139264 + (l15 * 2 + (lg & 1)) * 16;
    const unsigned vAux3 = (wave == 0 && lg < 2) ? slice * 128 + l15 * 8 + lg * 4 : OOB;
    const unsigned sOutOff = kind == 1 ? OFF_HS - HU + (slice * 4 + wave) * 1280 : OFF_X1 + (slice * 4 + wave) * 1024, sFoutOff = (kind == 1 ? OFF_FH : OFF_FX1) + slice * 128;
    const unsigned sAuxOff = OFF_XP + (slice * 4 + wave) * 1024, sBuf = kind, sWw = 16 * wave * 2048 + slice * 256;
    const u16* w12 = kind == 1 ? w1 : w2;
    const unsigned sExp = __builtin_amdgcn_readfirstlane(ep);
    unsigned epOut;
    asm volatile(
R"(.macro M d,w,x
v_mfma_f32_16x16x32_bf16 v[\d:\d+3], \w, v[\x:\x+3], v[\d:\d+3]
.endm
.macro R d,a,o
ds_read_b128 v[\d:\d+3], v\a offset:\o
.endm
.macro T d,a,o
ds_read_b64_tr_b16 \d, v\a offset:\o
.endm
.macro D a,s,o
buffer_load_dwordx4 v\a, s[36:39], s\s offen offset:\o sc1 lds
.endm
.macro L c
s_waitcnt lgkmcnt(\c)
.endm
Lcode_g12_b:
s_cmp_eq_u32 %9,2
s_cbranch_scc1 Lg2_begin_%=
s_mov_b64 s[36:37],%1
s_mov_b32 s38,111243392
s_mov_b32 s39,0x00020000
s_mov_b32 s44,%2
s_mov_b32 s45,%3
s_mov_b32 s46,%4
s_mov_b32 s47,%5
s_mov_b32 s48,%6
s_mov_b32 s49,%7
s_mov_b32 s50,%8
s_mov_b32 s51,%9
s_mov_b32 s53,%10
v_mov_b32 v0,%11
v_mov_b32 v3,%12
v_mov_b32 v5,%13
v_mov_b32 v6,%14
v_mov_b32 v18,%15
v_mov_b32 v19,%16
v_mov_b32 v20,%17
v_mov_b32 v21,%18
v_mov_b32 v9,%19
v_mov_b32 v10,%20
v_mov_b32 v11,%21
v_mov_b32 v12,%22
v_mov_b32 v13,%23
v_mov_b32 v14,%24
v_mov_b32 v15,%25
v_mov_b32 v16,%26
s_mov_b64 s[40:41],%27
s_mov_b32 s42,2097152
s_mov_b32 s43,0x00020000
s_lshl_b32 s57,s44,11
s_waitcnt vmcnt(20) lgkmcnt(0)
s_barrier
s_add_u32 s55,s50,786432
s_add_u32 m0,s57,98304
s_nop 0
buffer_load_dwordx4 v18,s[40:43],s55 offen offset:0  lds
s_add_u32 m0,s57,99328
s_nop 0
buffer_load_dwordx4 v19,s[40:43],s55 offen offset:0  lds
s_add_u32 m0,s57,106368
s_nop 0
buffer_load_dwordx4 v18,s[40:43],s55 offen offset:128  lds
s_add_u32 m0,s57,107392
s_nop 0
buffer_load_dwordx4 v19,s[40:43],s55 offen offset:128  lds
v_add_u32 v26,0,v20
v_add_u32 v27,0,v21
T a[0:1],26,0
T a[2:3],26,512
T a[4:5],27,0
T a[6:7],27,512
T a[8:9],26,4096
T a[10:11],26,4608
T a[12:13],27,4096
T a[14:15],27,4608
s_waitcnt vmcnt(20) lgkmcnt(0)
s_barrier
s_add_u32 s55,s50,917504
s_add_u32 m0,s57,0
s_nop 0
buffer_load_dwordx4 v18,s[40:43],s55 offen offset:0  lds
s_add_u32 m0,s57,1024
s_nop 0
buffer_load_dwordx4 v19,s[40:43],s55 offen offset:0  lds
s_add_u32 m0,s57,8064
s_nop 0
buffer_load_dwordx4 v18,s[40:43],s55 offen offset:128  lds
s_add_u32 m0,s57,9088
s_nop 0
buffer_load_dwordx4 v19,s[40:43],s55 offen offset:128  lds
v_add_u32 v26,16384,v20
v_add_u32 v27,16384,v21
T a[16:17],26,0
T a[18:19],26,512
T a[20:21],27,0
T a[22:23],27,512
T a[24:25],26,4096
T a[26:27],26,4608
T a[28:29],27,4096
T a[30:31],27,4608
s_waitcnt vmcnt(20) lgkmcnt(0)
s_barrier
s_add_u32 s55,s50,1048576
s_add_u32 m0,s57,16384
s_nop 0
buffer_load_dwordx4 v18,s[40:43],s55 offen offset:0  lds
s_add_u32 m0,s57,17408
s_nop 0
buffer_load_dwordx4 v19,s[40:43],s55 offen offset:0  lds
s_add_u32 m0,s57,24448
s_nop 0
buffer_load_dwordx4 v18,s[40:43],s55 offen offset:128  lds
s_add_u32 m0,s57,25472
s_nop 0
buffer_load_dwordx4 v19,s[40:43],s55 offen offset:128  lds
v_add_u32 v26,32768,v20
v_add_u32 v27,32768,v21
T a[32:33],26,0
T a[34:35],26,512
T a[36:37],27,0
T a[38:39],27,512
T a[40:41],26,4096
T a[42:43],26,4608
T a[44:45],27,4096
T a[46:47],27,4608
s_waitcnt vmcnt(20) lgkmcnt(0)
s_barrier
s_add_u32 s55,s50,1179648
s_add_u32 m0,s57,32768
s_nop 0
buffer_load_dwordx4 v18,s[40:43],s55 offen offset:0  lds
s_add_u32 m0,s57,33792
s_nop 0
buffer_load_dwordx4 v19,s[40:43],s55 offen offset:0  lds
s_add_u32 m0,s57,40832
s_nop 0
buffer_load_dwordx4 v18,s[40:43],s55 offen offset:128  lds
s_add_u32 m0,s57,41856
s_nop 0
buffer_load_dwordx4 v19,s[40:43],s55 offen offset:128  lds
v_add_u32 v26,49152,v20
v_add_u32 v27,49152,v21
T a[48:49],26,0
T a[50:51],26,512
T a[52:53],27,0
T a[54:55],27,512
T a[56:57],26,4096
T a[58:59],26,4608
T a[60:61],27,4096
T a[62:63],27,4608
s_waitcnt vmcnt(20) lgkmcnt(0)
s_barrier
s_add_u32 s55,s50,1310720
s_add_u32 m0,s57,49152
s_nop 0
buffer_load_dwordx4 v18,s[40:43],s55 offen offset:0  lds
s_add_u32 m0,s57,50176
s_nop 0
buffer_load_dwordx4 v19,s[40:43],s55 offen offset:0  lds
s_add_u32 m0,s57,57216
s_nop 0
buffer_load_dwordx4 v18,s[40:43],s55 offen offset:128  lds
s_add_u32 m0,s57,58240
s_nop 0
buffer_load_dwordx4 v19,s[40:43],s55 offen offset:128  lds
v_add_u32 v26,65536,v20
v_add_u32 v27,65536,v21
T a[64:65],26,0
T a[66:67],26,512
T a[68:69],27,0
T a[70:71],27,512
T a[72:73],26,4096
T a[74:75],26,4608
T a[76:77],27,4096
T a[78:79],27,4608
s_waitcnt vmcnt(20) lgkmcnt(0)
s_barrier
s_add_u32 s55,s50,1441792
s_add_u32 m0,s57,65536
s_nop 0
buffer_load_dwordx4 v18,s[40:43],s55 offen offset:0  lds
s_add_u32 m0,s57,66560
s_nop 0
buffer_load_dwordx4 v19,s[40:43],s55 offen offset:0  lds
s_add_u32 m0,s57,73600
s_nop 0
buffer_load_dwordx4 v18,s[40:43],s55 offen offset:128  lds
s_add_u32 m0,s57,74624
s_nop 0
buffer_load_dwordx4 v19,s[40:43],s55 offen offset:128  lds
v_add_u32 v26,81920,v20
v_add_u32 v27,81920,v21
T a[80:81],26,0
T a[82:83],26,512
T a[84:85],27,0
T a[86:87],27,512
T a[88:89],26,4096
T a[90:91],26,4608
T a[92:93],27,4096
T a[94:95],27,4608
s_waitcnt vmcnt(20) lgkmcnt(0)
s_barrier
s_add_u32 s55,s50,1572864
s_add_u32 m0,s57,81920
s_nop 0
buffer_load_dwordx4 v18,s[40:43],s55 offen offset:0  lds
s_add_u32 m0,s57,82944
s_nop 0
buffer_load_dwordx4 v19,s[40:43],s55 offen offset:0  lds
s_add_u32 m0,s57,89984
s_nop 0
buffer_load_dwordx4 v18,s[40:43],s55 offen offset:128  lds
s_add_u32 m0,s57,91008
s_nop 0
buffer_load_dwordx4 v19,s[40:43],s55 offen offset:128  lds
v_add_u32 v26,98304,v20
v_add_u32 v27,98304,v21
T a[96:97],26,0
T a[98:99],26,512
T a[100:101],27,0
T a[102:103],27,512
T a[104:105],26,4096
T a[106:107],26,4608
T a[108:109],27,4096
T a[110:111],27,4608
s_waitcnt vmcnt(20) lgkmcnt(0)
s_barrier
s_add_u32 s55,s50,1703936
s_add_u32 m0,s57,98304
s_nop 0
buffer_load_dwordx4 v18,s[40:43],s55 offen offset:0  lds
s_add_u32 m0,s57,99328
s_nop 0
buffer_load_dwordx4 v19,s[40:43],s55 offen offset:0  lds
s_add_u32 m0,s57,106368
s_nop 0
buffer_load_dwordx4 v18,s[40:43],s55 offen offset:128  lds
s_add_u32 m0,s57,107392
s_nop 0
buffer_load_dwordx4 v19,s[40:43],s55 offen offset:128  lds
v_add_u32 v26,0,v20
v_add_u32 v27,0,v21
T a[112:113],26,0
T a[114:115],26,512
T a[116:117],27,0
T a[118:119],27,512
T a[120:121],26,4096
T a[122:123],26,4608
T a[124:125],27,4096
T a[126:127],27,4608
s_waitcnt vmcnt(20) lgkmcnt(0)
s_barrier
s_add_u32 s55,s50,1835008
s_add_u32 m0,s57,0
s_nop 0
buffer_load_dwordx4 v18,s[40:43],s55 offen offset:0  lds
s_add_u32 m0,s57,1024
s_nop 0
buffer_load_dwordx4 v19,s[40:43],s55 offen offset:0  lds
s_add_u32 m0,s57,8064
s_nop 0
buffer_load_dwordx4 v18,s[40:43],s55 offen offset:128  lds
s_add_u32 m0,s57,9088
s_nop 0
buffer_load_dwordx4 v19,s[40:43],s55 offen offset:128  lds
v_add_u32 v26,16384,v20
v_add_u32 v27,16384,v21
T a[128:129],26,0
T a[130:131],26,512
T a[132:133],27,0
T a[134:135],27,512
T a[136:137],26,4096
T a[138:139],26,4608
T a[140:141],27,4096
T a[142:143],27,4608
s_waitcnt vmcnt(20) lgkmcnt(0)
s_barrier
s_add_u32 s55,s50,1966080
s_add_u32 m0,s57,16384
s_nop 0
buffer_load_dwordx4 v18,s[40:43],s55 offen offset:0  lds
s_add_u32 m0,s57,17408
s_nop 0
buffer_load_dwordx4 v19,s[40:43],s55 offen offset:0  lds
s_add_u32 m0,s57,24448
s_nop 0
buffer_load_dwordx4 v18,s[40:43],s55 offen offset:128  lds
s_add_u32 m0,s57,25472
s_nop 0
buffer_load_dwordx4 v19,s[40:43],s55 offen offset:128  lds
v_add_u32 v26,32768,v20
v_add_u32 v27,32768,v21
T a[144:145],26,0
T a[146:147],26,512
T a[148:149],27,0
T a[150:151],27,512
T a[152:153],26,4096
T a[154:155],26,4608
T a[156:157],27,4096
T a[158:159],27,4608
s_waitcnt vmcnt(20) lgkmcnt(0)
s_barrier
v_add_u32 v26,49152,v20
v_add_u32 v27,49152,v21
T a[160:161],26,0
T a[162:163],26,512
T a[164:165],27,0
T a[166:167],27,512
T a[168:169],26,4096
T a[170:171],26,4608
T a[172:173],27,4096
T a[174:175],27,4608
s_waitcnt vmcnt(16) lgkmcnt(0)
s_barrier
v_add_u32 v26,65536,v20
v_add_u32 v27,65536,v21
T a[176:177],26,0
T a[178:179],26,512
T a[180:181],27,0
T a[182:183],27,512
T a[184:185],26,4096
T a[186:187],26,4608
T a[188:189],27,4096
T a[190:191],27,4608
s_waitcnt vmcnt(12) lgkmcnt(0)
s_barrier
v_add_u32 v26,81920,v20
v_add_u32 v27,81920,v21
T a[192:193],26,0
T a[194:195],26,512
T a[196:197],27,0
T a[198:199],27,512
T a[200:201],26,4096
T a[202:203],26,4608
T a[204:205],27,4096
T a[206:207],27,4608
s_waitcnt vmcnt(8) lgkmcnt(0)
s_barrier
v_add_u32 v26,98304,v20
v_add_u32 v27,98304,v21
T a[208:209],26,0
T a[210:211],26,512
T a[212:213],27,0
T a[214:215],27,512
T a[216:217],26,4096
T a[218:219],26,4608
T a[220:221],27,4096
T a[222:223],27,4608
s_waitcnt vmcnt(4) lgkmcnt(0)
s_barrier
v_add_u32 v26,0,v20
v_add_u32 v27,0,v21
T a[224:225],26,0
T a[226:227],26,512
T a[228:229],27,0
T a[230:231],27,512
T a[232:233],26,4096
T a[234:235],26,4608
T a[236:237],27,4096
T a[238:239],27,4608
s_waitcnt vmcnt(0) lgkmcnt(0)
s_barrier
v_add_u32 v26,16384,v20
v_add_u32 v27,16384,v21
T a[240:241],26,0
T a[242:243],26,512
T a[244:245],27,0
T a[246:247],27,512
T a[248:249],26,4096
T a[250:251],26,4608
T a[252:253],27,4096
T a[254:255],27,4608
L 0
s_barrier
v_mov_b32 v34,0x3f803f80
v_mov_b32 v35,0x3f803f80
v_mov_b32 v36,0x3f803f80
v_mov_b32 v37,0x3f803f80
s_nop 1
v_mfma_f32_16x16x32_bf16 v[106:109],a[0:3],v[34:37],0
v_mfma_f32_16x16x32_bf16 v[110:113],a[4:7],v[34:37],0
M 106,a[8:11],34
M 110,a[12:15],34
M 106,a[16:19],34
M 110,a[20:23],34
M 106,a[24:27],34
M 110,a[28:31],34
M 106,a[32:35],34
M 110,a[36:39],34
M 106,a[40:43],34
M 110,a[44:47],34
M 106,a[48:51],34
M 110,a[52:55],34
M 106,a[56:59],34
M 110,a[60:63],34
M 106,a[64:67],34
M 110,a[68:71],34
M 106,a[72:75],34
M 110,a[76:79],34
M 106,a[80:83],34
M 110,a[84:87],34
M 106,a[88:91],34
M 110,a[92:95],34
M 106,a[96:99],34
M 110,a[100:103],34
M 106,a[104:107],34
M 110,a[108:111],34
M 106,a[112:115],34
M 110,a[116:119],34
M 106,a[120:123],34
M 110,a[124:127],34
M 106,a[128:131],34
M 110,a[132:135],34
M 106,a[136:139],34
M 110,a[140:143],34
M 106,a[144:147],34
M 110,a[148:151],34
M 106,a[152:155],34
M 110,a[156:159],34
M 106,a[160:163],34
M 110,a[164:167],34
M 106,a[168:171],34
M 110,a[172:175],34
M 106,a[176:179],34
M 110,a[180:183],34
M 106,a[184:187],34
M 110,a[188:191],34
M 106,a[192:195],34
M 110,a[196:199],34
M 106,a[200:203],34
M 110,a[204:207],34
M 106,a[208:211],34
M 110,a[212:215],34
M 106,a[216:219],34
M 110,a[220:223],34
M 106,a[224:227],34
M 110,a[228:231],34
M 106,a[232:235],34
M 110,a[236:239],34
M 106,a[240:243],34
M 110,a[244:247],34
M 106,a[248:251],34
M 110,a[252:255],34
v_mov_b32 v4,s53
s_mov_b32 s82,0
s_mov_b32 s80,0
s_mov_b32 s54,s45
s_mul_i32 s57,s44,4096
s_mul_i32 s55,s45,32768
s_add_u32 s55,s55,s57
s_add_u32 s55,s55,8388608
s_mul_i32 s57,s44,4096
s_mul_i32 s60,s46,32768
s_mul_i32 s61,s46,1024
s_mul_i32 s62,s46,40960
s_mul_i32 s63,s46,1024
s_mul_i32 s64,s46,512
s_lshl_b32 s59,s44,8
s_add_u32 s59,s59,131072
s_mul_i32 s58,s45,1024
s_add_u32 s58,s58,109056000
s_mul_i32 s65,s45,40960
s_add_u32 s65,s65,s47
s_mul_i32 s68,s45,1024
s_add_u32 s68,s68,s48
s_mul_i32 s73,s45,512
s_add_u32 s73,s73,106954752
s_mul_i32 s74,s45,32768
s_add_u32 s74,s74,s49
s_mul_i32 s75,s45,1024
s_add_u32 s75,s75,107479040
s_mov_b32 s66,0x7fff0000
s_mov_b32 s67,0x7fff0000
s_mov_b32 s69,0x7fff0000
s_mov_b32 s76,0x7fff0000
s_mov_b32 s77,0x7fff0000
s_mov_b32 s70,0x7fff0000
s_mov_b32 s71,0x7fff0000
s_mov_b32 s72,0x7fff0000
v_mov_b32 v1,v0
v_add_u32 v2,16384,v0
s_mov_b32 s84,16384
s_mov_b32 s86,0x3a800000
s_mov_b32 s87,0x3727c5ac
s_mov_b32 s88,0x409d30fc
s_mov_b32 s89,0xbeeff558
s_lshr_b32 s85,s44,1
s_mul_i32 s85,s85,127
v_lshlrev_b32 v8,22,v0
v_and_b32 v8,0x80000000,v8
v_or_b32 v8,v8,v5
v_add_u32 v2,32768,v0
s_lshl_b32 s84,s57,1
s_add_u32 s84,s84,32768
s_getpc_b64 s[40:41]
Lg1_pc_%=:
s_lshl_b32 s78,s44,13
v_lshlrev_b32 v114,3,v0
v_add_u32 v114,s78,v114
v_min_u32 v114,Lcode_g2_b-Lg1_pc_%=-4,v114
)"
R"(s_mov_b32 m0,163584
s_nop 0
global_load_lds_dword v114,s[40:41]
s_add_u32 m0,s59,4096
s_nop 0
buffer_load_dword v8,s[36:39],s58 offen offset:0 sc1 lds
buffer_store_dword v4,v3,s[36:39],s71 offen offset:0 sc0 sc1
s_add_u32 m0,s59,5120
s_nop 0
buffer_load_dword v8,s[36:39],s58 offen offset:0 sc1 lds
buffer_store_dword v4,v3,s[36:39],s71 offen offset:0 sc0 sc1
s_mov_b32 s83,0
Lg1_poll_%=:
s_add_u32 s78,s83,2
s_and_b32 s78,s78,3
s_lshl_b32 s78,s78,10
s_add_u32 s78,s78,4096
s_add_u32 m0,s59,s78
s_nop 0
buffer_load_dword v8,s[36:39],s58 offen offset:0 sc1 lds
s_and_b32 s78,s83,3
s_lshl_b32 s78,s78,10
v_add_u32 v114,s78,v6
s_waitcnt vmcnt(4)
s_barrier
ds_read_b32 v7,v114 offset:4096
buffer_store_dword v4,v3,s[36:39],s70 offen offset:0 sc0 sc1
s_mov_b32 s70,s71
s_mov_b32 s71,s72
s_mov_b32 s72,0x7fff0000
s_add_u32 s83,s83,1
s_add_u32 s82,s82,1
L 0
v_cmp_eq_u32 vcc,s53,v7
s_nop 0
s_cmp_eq_u32 vcc_lo,-1
s_cbranch_scc1 Lg1_go_%=
s_cmp_lt_u32 s82,20000
s_cbranch_scc1 Lg1_poll_%=
Lg1_go_%=:
s_add_u32 m0,s57,0
s_add_u32 s56,s55,0
D 0,56,0
D 0,56,1024
D 0,56,2048
D 0,56,3072
s_add_u32 s58,s58,s61
s_add_u32 m0,s59,2048
s_nop 0
buffer_load_dword v8,s[36:39],s58 offen offset:0 sc1 lds
s_add_u32 m0,s57,16384
s_add_u32 s56,s55,16384
D 0,56,0
D 0,56,1024
D 0,56,2048
D 0,56,3072
s_add_u32 s55,s55,s60
s_sub_u32 s57,s84,s57
s_add_u32 s54,s54,s46
s_waitcnt vmcnt(4)
s_barrier
buffer_store_dword v4,v3,s[36:39],s70 offen offset:0 sc0 sc1
s_min_u32 s71,s71,s72
buffer_store_dword v4,v3,s[36:39],s71 offen offset:0 sc0 sc1
s_mov_b32 s70,0x7fff0000
s_mov_b32 s71,0x7fff0000
s_mov_b32 s72,0x7fff0000
s_cmp_eq_u32 s80,1
buffer_store_dword v4,v3,s[36:39],s71 offen offset:0 sc0 sc1
s_cbranch_scc1 Lg1_st1_%=
s_branch Lg1_st0_%=
Lg1_gox_%=:
s_add_u32 m0,s59,2048
s_nop 0
buffer_load_dword v8,s[36:39],s58 offen offset:0 sc1 lds
s_waitcnt vmcnt(0)
s_barrier
buffer_store_dword v4,v3,s[36:39],s70 offen offset:0 sc0 sc1
s_min_u32 s71,s71,s72
buffer_store_dword v4,v3,s[36:39],s71 offen offset:0 sc0 sc1
s_mov_b32 s70,0x7fff0000
s_mov_b32 s71,0x7fff0000
s_mov_b32 s72,0x7fff0000
s_cmp_eq_u32 s80,1
s_cbranch_scc1 Lg1_st1_%=
Lg1_st0_%=:
ds_read_b32 v7,v6 offset:2048
R 34,1,0
R 38,1,1024
R 42,1,2048
R 46,1,3072
R 50,1,4096
L 5
v_cmp_eq_u32 vcc,s53,v7
L 3
s_cmp_eq_u32 vcc_lo,-1
s_cbranch_scc1 Lg1_F0_%=
s_branch Lg1_L0_%=
Lg1_st1_%=:
ds_read_b32 v7,v6 offset:2048
R 34,2,0
R 38,2,1024
R 42,2,2048
R 46,2,3072
R 50,2,4096
L 5
v_cmp_eq_u32 vcc,s53,v7
L 3
s_cmp_eq_u32 vcc_lo,-1
s_cbranch_scc1 Lg1_F1_%=
s_branch Lg1_L1_%=
Lg1_F0_%=:
v_mfma_f32_16x16x32_bf16 v[22:25],a[4:7],v[34:37],0
s_add_u32 s58,s58,s61
s_add_u32 m0,s59,0
R 54,1,5120
M 22,a[12:15],38
buffer_load_dword v8,s[36:39],s58 offen offset:0 sc1 lds
v_mfma_f32_16x16x32_bf16 v[18:21],a[0:3],v[34:37],0
s_add_u32 m0,s57,0
R 58,1,6144
v_fma_f32 v26,-v102,v106,v26
M 18,a[8:11],38
L 3
s_add_u32 s56,s55,0
v_fma_f32 v27,-v102,v107,v27
M 18,a[16:19],42
s_mov_b32 s66,s65
R 62,1,7168
v_fma_f32 v28,-v102,v108,v28
M 18,a[24:27],46
D 0,56,0
M 22,a[20:23],42
s_add_u32 s65,s65,s62
R 66,1,8192
v_fma_f32 v29,-v102,v109,v29
v_mul_f32 v26,v103,v26
M 22,a[28:31],46
L 3
D 0,56,1024
M 22,a[36:39],50
s_add_u32 s54,s54,s46
R 70,1,9216
v_mul_f32 v27,v103,v27
M 22,a[44:47],54
v_mul_f32 v28,v103,v28
v_mul_f32 v29,v103,v29
M 18,a[32:35],50
R 74,1,10240
v_fma_f32 v114,v26,v26,s88
M 18,a[40:43],54
L 3
D 0,56,2048
M 18,a[48:51],58
R 78,1,11264
v_fma_f32 v115,v27,v27,s88
M 18,a[56:59],62
D 0,56,3072
v_fma_f32 v116,v28,v28,s88
M 22,a[52:55],58
s_add_u32 m0,s57,16384
R 82,1,12288
v_fma_f32 v117,v29,v29,s88
M 22,a[60:63],62
L 3
v_mul_f32 v114,v26,v114
v_mul_f32 v115,v27,v115
M 22,a[68:71],66
s_add_u32 s56,s55,16384
R 86,1,13312
v_mul_f32 v116,v28,v116
M 22,a[76:79],70
buffer_load_dwordx2 v[98:99],v11,s[36:39],s73 offen offset:0 sc1
M 18,a[64:67],66
s_add_u32 s73,s73,s64
R 90,1,14336
v_mul_f32 v117,v29,v117
M 18,a[72:75],70
L 3
v_exp_f32 v114,v114
v_fma_f32 v30,-v102,v110,v30
M 18,a[80:83],74
s_waitcnt vmcnt(9)
R 94,1,15360
v_fma_f32 v114,v114,s89,s89
M 18,a[88:91],78
s_barrier
v_exp_f32 v115,v115
M 22,a[84:87],74
s_add_u32 s55,s55,s60
R 34,1,16384
v_fma_f32 v115,v115,s89,s89
M 22,a[92:95],78
L 3
v_exp_f32 v116,v116
M 22,a[100:103],82
s_sub_u32 s57,s84,s57
R 38,1,17408
v_fma_f32 v116,v116,s89,s89
M 22,a[108:111],86
D 0,56,0
M 18,a[96:99],82
R 42,1,18432
v_exp_f32 v117,v117
M 18,a[104:107],86
L 3
v_rcp_f32 v114,v114
M 18,a[112:115],90
R 46,1,19456
v_fma_f32 v117,v117,s89,s89
M 18,a[120:123],94
v_rcp_f32 v115,v115
M 22,a[116:119],90
R 50,1,20480
v_mul_f32 v26,v26,v114
M 22,a[124:127],94
L 3
v_rcp_f32 v116,v116
M 22,a[132:135],34
R 54,1,21504
v_mul_f32 v27,v27,v115
M 22,a[140:143],38
D 0,56,1024
v_mul_f32 v28,v28,v116
M 18,a[128:131],34
R 58,1,22528
v_cvt_pk_bf16_f32 v122,v26,v27
M 18,a[136:139],38
L 3
v_rcp_f32 v117,v117
M 18,a[144:147],42
R 62,1,23552
v_mul_f32 v29,v29,v117
M 18,a[152:155],46
v_cvt_pk_bf16_f32 v123,v28,v29
v_fma_f32 v31,-v102,v111,v31
M 22,a[148:151],42
R 66,1,24576
v_fma_f32 v32,-v102,v112,v32
M 22,a[156:159],46
L 3
D 0,56,2048
v_fma_f32 v33,-v102,v113,v33
M 22,a[164:167],50
R 70,1,25600
v_mul_f32 v30,v103,v30
M 22,a[172:175],54
v_mul_f32 v31,v103,v31
v_mul_f32 v32,v103,v32
M 18,a[160:163],50
R 74,1,26624
v_mul_f32 v33,v103,v33
M 18,a[168:171],54
L 3
v_fma_f32 v118,v30,v30,s88
v_fma_f32 v119,v31,v31,s88
M 18,a[176:179],58
R 78,1,27648
v_fma_f32 v120,v32,v32,s88
M 18,a[184:187],62
v_fma_f32 v121,v33,v33,s88
v_mul_f32 v118,v30,v118
M 22,a[180:183],58
R 82,1,28672
v_exp_f32 v118,v118
M 22,a[188:191],62
L 3
D 0,56,3072
M 22,a[196:199],66
R 86,1,29696
v_mul_f32 v119,v31,v119
M 22,a[204:207],70
v_exp_f32 v119,v119
M 18,a[192:195],66
R 90,1,30720
v_mul_f32 v120,v32,v120
M 18,a[200:203],70
L 3
v_mul_f32 v121,v33,v121
v_fma_f32 v118,v118,s89,s89
M 18,a[208:211],74
s_waitcnt vmcnt(5)
R 94,1,31744
v_fma_f32 v119,v119,s89,s89
M 18,a[216:219],78
s_barrier
ds_read_b32 v7,v6 offset:0
v_exp_f32 v120,v120
M 22,a[212:215],74
R 34,2,0
v_fma_f32 v120,v120,s89,s89
M 22,a[220:223],78
L 4
buffer_store_dword v4,v3,s[36:39],s70 offen offset:0 sc0 sc1
v_exp_f32 v121,v121
M 22,a[228:231],82
s_mov_b32 s70,s69
R 38,2,1024
v_fma_f32 v121,v121,s89,s89
v_rcp_f32 v118,v118
v_rcp_f32 v119,v119
v_rcp_f32 v120,v120
v_rcp_f32 v121,v121
M 22,a[236:239],86
s_mov_b32 s69,s68
v_mul_f32 v30,v30,v118
v_mul_f32 v31,v31,v119
v_mul_f32 v32,v32,v120
v_mul_f32 v33,v33,v121
v_cvt_pk_bf16_f32 v124,v30,v31
v_cvt_pk_bf16_f32 v125,v32,v33
M 18,a[224:227],82
s_add_u32 s68,s68,s63
R 42,2,2048
buffer_store_dwordx4 v[122:125],v15,s[36:39],s67 offen offset:0 sc0 sc1
M 18,a[232:235],86
L 4
L 3
M 18,a[240:243],90
R 46,2,3072
v_cmp_eq_u32 vcc,s53,v7
M 18,a[248:251],94
buffer_store_dwordx4 v[122:125],v16,s[36:39],s67 offen offset:0 sc0 sc1
M 22,a[244:247],90
s_waitcnt vmcnt(7) lgkmcnt(2)
s_cmp_eq_u32 vcc_lo,-1
R 50,2,4096
M 22,a[252:255],94
s_cbranch_scc0 Lg1_K1_%=
Lg1_F1_%=:
v_mfma_f32_16x16x32_bf16 v[30:33],a[4:7],v[34:37],0
s_add_u32 s58,s58,s61
s_add_u32 m0,s59,1024
R 54,2,5120
M 30,a[12:15],38
buffer_load_dword v8,s[36:39],s58 offen offset:0 sc1 lds
v_mfma_f32_16x16x32_bf16 v[26:29],a[0:3],v[34:37],0
s_add_u32 m0,s57,0
R 58,2,6144
v_fma_f32 v18,-v98,v106,v18
M 26,a[8:11],38
L 3
s_add_u32 s56,s55,0
v_fma_f32 v19,-v98,v107,v19
M 26,a[16:19],42
s_mov_b32 s67,s65
R 62,2,7168
v_fma_f32 v20,-v98,v108,v20
M 26,a[24:27],46
D 0,56,0
M 30,a[20:23],42
s_add_u32 s65,s65,s62
R 66,2,8192
v_fma_f32 v21,-v98,v109,v21
v_mul_f32 v18,v99,v18
M 30,a[28:31],46
L 3
D 0,56,1024
M 30,a[36:39],50
s_add_u32 s54,s54,s46
R 70,2,9216
v_mul_f32 v19,v99,v19
M 30,a[44:47],54
v_mul_f32 v20,v99,v20
v_mul_f32 v21,v99,v21
M 26,a[32:35],50
R 74,2,10240
v_fma_f32 v114,v18,v18,s88
M 26,a[40:43],54
L 3
D 0,56,2048
M 26,a[48:51],58
R 78,2,11264
v_fma_f32 v115,v19,v19,s88
M 26,a[56:59],62
D 0,56,3072
v_fma_f32 v116,v20,v20,s88
M 30,a[52:55],58
s_add_u32 m0,s57,16384
R 82,2,12288
v_fma_f32 v117,v21,v21,s88
M 30,a[60:63],62
L 3
v_mul_f32 v114,v18,v114
v_mul_f32 v115,v19,v115
M 30,a[68:71],66
s_add_u32 s56,s55,16384
R 86,2,13312
v_mul_f32 v116,v20,v116
M 30,a[76:79],70
buffer_load_dwordx2 v[102:103],v11,s[36:39],s73 offen offset:0 sc1
M 26,a[64:67],66
s_add_u32 s73,s73,s64
R 90,2,14336
v_mul_f32 v117,v21,v117
M 26,a[72:75],70
L 3
v_exp_f32 v114,v114
v_fma_f32 v22,-v98,v110,v22
M 26,a[80:83],74
s_waitcnt vmcnt(9)
R 94,2,15360
v_fma_f32 v114,v114,s89,s89
M 26,a[88:91],78
s_barrier
v_exp_f32 v115,v115
M 30,a[84:87],74
s_add_u32 s55,s55,s60
R 34,2,16384
v_fma_f32 v115,v115,s89,s89
M 30,a[92:95],78
L 3
v_exp_f32 v116,v116
M 30,a[100:103],82
s_sub_u32 s57,s84,s57
R 38,2,17408
v_fma_f32 v116,v116,s89,s89
M 30,a[108:111],86
D 0,56,0
M 26,a[96:99],82
R 42,2,18432
v_exp_f32 v117,v117
M 26,a[104:107],86
L 3
v_rcp_f32 v114,v114
M 26,a[112:115],90
R 46,2,19456
v_fma_f32 v117,v117,s89,s89
M 26,a[120:123],94
v_rcp_f32 v115,v115
M 30,a[116:119],90
R 50,2,20480
v_mul_f32 v18,v18,v114
M 30,a[124:127],94
L 3
v_rcp_f32 v116,v116
M 30,a[132:135],34
R 54,2,21504
v_mul_f32 v19,v19,v115
M 30,a[140:143],38
D 0,56,1024
v_mul_f32 v20,v20,v116
M 26,a[128:131],34
R 58,2,22528
v_cvt_pk_bf16_f32 v122,v18,v19
M 26,a[136:139],38
L 3
v_rcp_f32 v117,v117
M 26,a[144:147],42
R 62,2,23552
v_mul_f32 v21,v21,v117
M 26,a[152:155],46
v_cvt_pk_bf16_f32 v123,v20,v21
v_fma_f32 v23,-v98,v111,v23
M 30,a[148:151],42
R 66,2,24576
v_fma_f32 v24,-v98,v112,v24
M 30,a[156:159],46
L 3
D 0,56,2048
v_fma_f32 v25,-v98,v113,v25
M 30,a[164:167],50
R 70,2,25600
v_mul_f32 v22,v99,v22
M 30,a[172:175],54
v_mul_f32 v23,v99,v23
v_mul_f32 v24,v99,v24
M 26,a[160:163],50
R 74,2,26624
v_mul_f32 v25,v99,v25
M 26,a[168:171],54
L 3
v_fma_f32 v118,v22,v22,s88
v_fma_f32 v119,v23,v23,s88
M 26,a[176:179],58
R 78,2,27648
v_fma_f32 v120,v24,v24,s88
M 26,a[184:187],62
v_fma_f32 v121,v25,v25,s88
v_mul_f32 v118,v22,v118
M 30,a[180:183],58
R 82,2,28672
v_exp_f32 v118,v118
M 30,a[188:191],62
L 3
D 0,56,3072
M 30,a[196:199],66
R 86,2,29696
v_mul_f32 v119,v23,v119
M 30,a[204:207],70
v_exp_f32 v119,v119
M 26,a[192:195],66
R 90,2,30720
v_mul_f32 v120,v24,v120
M 26,a[200:203],70
L 3
v_mul_f32 v121,v25,v121
v_fma_f32 v118,v118,s89,s89
M 26,a[208:211],74
s_waitcnt vmcnt(5)
)"
R"(R 94,2,31744
v_fma_f32 v119,v119,s89,s89
M 26,a[216:219],78
s_barrier
ds_read_b32 v7,v6 offset:1024
v_exp_f32 v120,v120
M 30,a[212:215],74
R 34,1,0
v_fma_f32 v120,v120,s89,s89
M 30,a[220:223],78
L 4
buffer_store_dword v4,v3,s[36:39],s70 offen offset:0 sc0 sc1
v_exp_f32 v121,v121
M 30,a[228:231],82
s_mov_b32 s70,s69
R 38,1,1024
v_fma_f32 v121,v121,s89,s89
v_rcp_f32 v118,v118
v_rcp_f32 v119,v119
v_rcp_f32 v120,v120
v_rcp_f32 v121,v121
M 30,a[236:239],86
s_mov_b32 s69,s68
v_mul_f32 v22,v22,v118
v_mul_f32 v23,v23,v119
v_mul_f32 v24,v24,v120
v_mul_f32 v25,v25,v121
v_cvt_pk_bf16_f32 v124,v22,v23
v_cvt_pk_bf16_f32 v125,v24,v25
M 26,a[224:227],82
s_add_u32 s68,s68,s63
R 42,1,2048
buffer_store_dwordx4 v[122:125],v15,s[36:39],s66 offen offset:0 sc0 sc1
M 26,a[232:235],86
L 4
L 3
M 26,a[240:243],90
R 46,1,3072
v_cmp_eq_u32 vcc,s53,v7
M 26,a[248:251],94
buffer_store_dwordx4 v[122:125],v16,s[36:39],s66 offen offset:0 sc0 sc1
M 30,a[244:247],90
s_waitcnt vmcnt(7) lgkmcnt(2)
s_cmp_eq_u32 vcc_lo,-1
R 50,1,4096
M 30,a[252:255],94
s_cbranch_scc1 Lg1_F0_%=
Lg1_K0_%=:
Lg1_L0_%=:
v_mfma_f32_16x16x32_bf16 v[22:25],a[4:7],v[34:37],0
s_add_u32 m0,s59,3072
s_mov_b32 s66,s65
R 54,1,5120
M 22,a[12:15],38
buffer_load_dword v8,s[36:39],s58 offen offset:0 sc1 lds
v_mfma_f32_16x16x32_bf16 v[18:21],a[0:3],v[34:37],0
s_add_u32 s65,s65,s62
R 58,1,6144
v_fma_f32 v26,-v102,v106,v26
M 18,a[8:11],38
L 3
v_fma_f32 v27,-v102,v107,v27
v_fma_f32 v28,-v102,v108,v28
M 18,a[16:19],42
R 62,1,7168
v_fma_f32 v29,-v102,v109,v29
M 18,a[24:27],46
v_mul_f32 v26,v103,v26
v_mul_f32 v27,v103,v27
M 22,a[20:23],42
R 66,1,8192
v_mul_f32 v28,v103,v28
M 22,a[28:31],46
L 3
v_mul_f32 v29,v103,v29
v_fma_f32 v114,v26,v26,s88
M 22,a[36:39],50
R 70,1,9216
v_fma_f32 v115,v27,v27,s88
M 22,a[44:47],54
v_fma_f32 v116,v28,v28,s88
v_fma_f32 v117,v29,v29,s88
M 18,a[32:35],50
R 74,1,10240
v_mul_f32 v114,v26,v114
M 18,a[40:43],54
L 3
v_exp_f32 v114,v114
M 18,a[48:51],58
R 78,1,11264
v_mul_f32 v115,v27,v115
M 18,a[56:59],62
v_exp_f32 v115,v115
M 22,a[52:55],58
R 82,1,12288
v_mul_f32 v116,v28,v116
M 22,a[60:63],62
L 3
v_exp_f32 v116,v116
M 22,a[68:71],66
R 86,1,13312
v_mul_f32 v117,v29,v117
M 22,a[76:79],70
buffer_load_dwordx2 v[98:99],v11,s[36:39],s73 offen offset:0 sc1
M 18,a[64:67],66
s_add_u32 s73,s73,s64
R 90,1,14336
v_fma_f32 v114,v114,s89,s89
M 18,a[72:75],70
L 3
v_exp_f32 v117,v117
M 18,a[80:83],74
s_waitcnt vmcnt(1)
R 94,1,15360
v_fma_f32 v115,v115,s89,s89
M 18,a[88:91],78
s_barrier
ds_read_b32 v7,v6 offset:3072
v_fma_f32 v116,v116,s89,s89
M 22,a[84:87],74
R 34,1,16384
v_fma_f32 v117,v117,s89,s89
M 22,a[92:95],78
L 4
buffer_store_dword v4,v3,s[36:39],s70 offen offset:0 sc0 sc1
M 22,a[100:103],82
s_mov_b32 s70,0x7fff0000
R 38,1,17408
v_fma_f32 v30,-v102,v110,v30
M 22,a[108:111],86
v_rcp_f32 v114,v114
M 18,a[96:99],82
R 42,1,18432
v_mul_f32 v26,v26,v114
M 18,a[104:107],86
L 4
L 3
v_cmp_eq_u32 vcc,s53,v7
M 18,a[112:115],90
R 46,1,19456
v_fma_f32 v31,-v102,v111,v31
M 18,a[120:123],94
v_rcp_f32 v115,v115
M 22,a[116:119],90
s_cmp_eq_u32 vcc_lo,-1
R 50,1,20480
v_mul_f32 v27,v27,v115
s_cbranch_scc0 Lg1_X0_%=
M 22,a[124:127],94
L 3
s_add_u32 m0,s57,0
s_add_u32 s56,s55,0
M 22,a[132:135],34
s_add_u32 s58,s58,s61
R 54,1,21504
v_cvt_pk_bf16_f32 v122,v26,v27
M 22,a[140:143],38
D 0,56,0
M 18,a[128:131],34
s_add_u32 s54,s54,s46
R 58,1,22528
v_fma_f32 v32,-v102,v112,v32
M 18,a[136:139],38
L 3
v_rcp_f32 v116,v116
M 18,a[144:147],42
R 62,1,23552
v_mul_f32 v28,v28,v116
M 18,a[152:155],46
D 0,56,1024
M 22,a[148:151],42
R 66,1,24576
v_fma_f32 v33,-v102,v113,v33
M 22,a[156:159],46
L 3
v_rcp_f32 v117,v117
M 22,a[164:167],50
R 70,1,25600
v_mul_f32 v29,v29,v117
M 22,a[172:175],54
D 0,56,2048
M 18,a[160:163],50
R 74,1,26624
v_cvt_pk_bf16_f32 v123,v28,v29
M 18,a[168:171],54
L 3
v_mul_f32 v30,v103,v30
v_mul_f32 v31,v103,v31
M 18,a[176:179],58
R 78,1,27648
v_mul_f32 v32,v103,v32
M 18,a[184:187],62
D 0,56,3072
M 22,a[180:183],58
s_add_u32 m0,s59,0
R 82,1,28672
v_mul_f32 v33,v103,v33
M 22,a[188:191],62
L 3
buffer_load_dword v8,s[36:39],s58 offen offset:0 sc1 lds
M 22,a[196:199],66
s_add_u32 m0,s57,16384
R 86,1,29696
v_fma_f32 v118,v30,v30,s88
M 22,a[204:207],70
s_add_u32 s56,s55,16384
v_fma_f32 v119,v31,v31,s88
v_fma_f32 v120,v32,v32,s88
M 18,a[192:195],66
s_add_u32 s55,s55,s60
R 90,1,30720
v_fma_f32 v121,v33,v33,s88
M 18,a[200:203],70
L 3
D 0,56,0
M 18,a[208:211],74
s_waitcnt vmcnt(1)
R 94,1,31744
v_mul_f32 v118,v30,v118
M 18,a[216:219],78
s_barrier
ds_read_b32 v7,v6 offset:0
v_mul_f32 v119,v31,v119
M 22,a[212:215],74
s_sub_u32 s57,s84,s57
R 34,2,0
v_mul_f32 v120,v32,v120
M 22,a[220:223],78
L 4
buffer_store_dword v4,v3,s[36:39],s70 offen offset:0 sc0 sc1
v_mul_f32 v121,v33,v121
v_exp_f32 v118,v118
v_exp_f32 v119,v119
v_exp_f32 v120,v120
v_exp_f32 v121,v121
M 22,a[228:231],82
s_mov_b32 s70,s69
R 38,2,1024
v_fma_f32 v118,v118,s89,s89
v_fma_f32 v119,v119,s89,s89
v_fma_f32 v120,v120,s89,s89
v_fma_f32 v121,v121,s89,s89
v_rcp_f32 v118,v118
v_rcp_f32 v119,v119
v_rcp_f32 v120,v120
v_rcp_f32 v121,v121
M 22,a[236:239],86
s_mov_b32 s69,s68
v_mul_f32 v30,v30,v118
v_mul_f32 v31,v31,v119
v_mul_f32 v32,v32,v120
v_mul_f32 v33,v33,v121
v_cvt_pk_bf16_f32 v124,v30,v31
v_cvt_pk_bf16_f32 v125,v32,v33
M 18,a[224:227],82
s_add_u32 s68,s68,s63
R 42,2,2048
buffer_store_dwordx4 v[122:125],v15,s[36:39],s67 offen offset:0 sc0 sc1
M 18,a[232:235],86
L 4
L 3
M 18,a[240:243],90
R 46,2,3072
v_cmp_eq_u32 vcc,s53,v7
M 18,a[248:251],94
D 0,56,1024
M 22,a[244:247],90
L 2
s_cmp_eq_u32 vcc_lo,-1
R 50,2,4096
buffer_store_dwordx4 v[122:125],v16,s[36:39],s67 offen offset:0 sc0 sc1
M 22,a[252:255],94
D 0,56,2048
D 0,56,3072
buffer_store_dword v4,v3,s[36:39],s71 offen offset:0 sc0 sc1
buffer_store_dword v4,v3,s[36:39],s71 offen offset:0 sc0 sc1
buffer_store_dword v4,v3,s[36:39],s71 offen offset:0 sc0 sc1
s_cbranch_scc1 Lg1_F1_%=
s_branch Lg1_K1_%=
Lg1_X0_%=:
M 22,a[124:127],94
L 3
s_add_u32 m0,s59,0
v_cvt_pk_bf16_f32 v122,v26,v27
M 22,a[132:135],34
R 54,1,21504
v_fma_f32 v32,-v102,v112,v32
M 22,a[140:143],38
buffer_load_dword v8,s[36:39],s58 offen offset:0 sc1 lds
M 18,a[128:131],34
R 58,1,22528
v_fma_f32 v33,-v102,v113,v33
M 18,a[136:139],38
L 3
v_rcp_f32 v116,v116
M 18,a[144:147],42
R 62,1,23552
v_mul_f32 v28,v28,v116
M 18,a[152:155],46
v_rcp_f32 v117,v117
M 22,a[148:151],42
R 66,1,24576
v_mul_f32 v29,v29,v117
M 22,a[156:159],46
L 3
v_cvt_pk_bf16_f32 v123,v28,v29
v_mul_f32 v30,v103,v30
M 22,a[164:167],50
R 70,1,25600
v_mul_f32 v31,v103,v31
M 22,a[172:175],54
v_mul_f32 v32,v103,v32
v_mul_f32 v33,v103,v33
M 18,a[160:163],50
R 74,1,26624
v_fma_f32 v118,v30,v30,s88
M 18,a[168:171],54
L 3
v_fma_f32 v119,v31,v31,s88
v_fma_f32 v120,v32,v32,s88
M 18,a[176:179],58
R 78,1,27648
v_fma_f32 v121,v33,v33,s88
M 18,a[184:187],62
v_mul_f32 v118,v30,v118
v_mul_f32 v119,v31,v119
M 22,a[180:183],58
R 82,1,28672
v_mul_f32 v120,v32,v120
M 22,a[188:191],62
L 3
v_exp_f32 v118,v118
M 22,a[196:199],66
R 86,1,29696
v_mul_f32 v121,v33,v121
M 22,a[204:207],70
v_exp_f32 v119,v119
M 18,a[192:195],66
R 90,1,30720
v_fma_f32 v118,v118,s89,s89
M 18,a[200:203],70
L 3
v_exp_f32 v120,v120
M 18,a[208:211],74
s_waitcnt vmcnt(0)
R 94,1,31744
v_fma_f32 v119,v119,s89,s89
M 18,a[216:219],78
s_barrier
buffer_store_dword v4,v3,s[36:39],s70 offen offset:0 sc0 sc1
M 22,a[212:215],74
s_mov_b32 s70,s69
ds_read_b32 v7,v6 offset:0
v_fma_f32 v120,v120,s89,s89
M 22,a[220:223],78
L 3
v_exp_f32 v121,v121
M 22,a[228:231],82
s_mov_b32 s69,s68
v_fma_f32 v121,v121,s89,s89
v_rcp_f32 v118,v118
v_rcp_f32 v119,v119
v_rcp_f32 v120,v120
v_rcp_f32 v121,v121
M 22,a[236:239],86
s_add_u32 s68,s68,s63
v_mul_f32 v30,v30,v118
v_mul_f32 v31,v31,v119
v_mul_f32 v32,v32,v120
v_mul_f32 v33,v33,v121
v_cvt_pk_bf16_f32 v124,v30,v31
v_cvt_pk_bf16_f32 v125,v32,v33
M 18,a[224:227],82
buffer_store_dwordx4 v[122:125],v15,s[36:39],s67 offen offset:0 sc0 sc1
M 18,a[232:235],86
L 1
M 18,a[240:243],90
L 0
v_cmp_eq_u32 vcc,s53,v7
M 18,a[248:251],94
buffer_store_dwordx4 v[122:125],v16,s[36:39],s67 offen offset:0 sc0 sc1
M 22,a[244:247],90
s_nop 0
M 22,a[252:255],94
s_mov_b32 s80,1
s_cmp_eq_u32 vcc_lo,-1
s_cbranch_scc0 Lg1_xn0_%=
s_add_u32 m0,s57,0
s_add_u32 s56,s55,0
D 0,56,0
v_fma_f32 v18,-v98,v106,v18
v_fma_f32 v19,-v98,v107,v19
v_fma_f32 v20,-v98,v108,v20
v_fma_f32 v21,-v98,v109,v21
v_mul_f32 v18,v99,v18
v_mul_f32 v19,v99,v19
v_mul_f32 v20,v99,v20
D 0,56,1024
v_mul_f32 v21,v99,v21
v_fma_f32 v114,v18,v18,s88
v_fma_f32 v115,v19,v19,s88
v_fma_f32 v116,v20,v20,s88
v_fma_f32 v117,v21,v21,s88
v_mul_f32 v114,v18,v114
v_mul_f32 v115,v19,v115
D 0,56,2048
v_mul_f32 v116,v20,v116
v_mul_f32 v117,v21,v117
v_exp_f32 v114,v114
v_exp_f32 v115,v115
v_exp_f32 v116,v116
v_exp_f32 v117,v117
v_fma_f32 v114,v114,s89,s89
D 0,56,3072
v_fma_f32 v115,v115,s89,s89
v_fma_f32 v116,v116,s89,s89
v_fma_f32 v117,v117,s89,s89
v_rcp_f32 v114,v114
v_rcp_f32 v115,v115
v_rcp_f32 v116,v116
v_rcp_f32 v117,v117
s_add_u32 s58,s58,s61
s_add_u32 m0,s57,16384
s_add_u32 s56,s55,16384
D 0,56,0
v_mul_f32 v18,v18,v114
v_mul_f32 v19,v19,v115
v_mul_f32 v20,v20,v116
v_mul_f32 v21,v21,v117
v_cvt_pk_bf16_f32 v122,v18,v19
v_cvt_pk_bf16_f32 v123,v20,v21
v_fma_f32 v22,-v98,v110,v22
D 0,56,1024
v_fma_f32 v23,-v98,v111,v23
v_fma_f32 v24,-v98,v112,v24
v_fma_f32 v25,-v98,v113,v25
v_mul_f32 v22,v99,v22
v_mul_f32 v23,v99,v23
v_mul_f32 v24,v99,v24
v_mul_f32 v25,v99,v25
D 0,56,2048
v_fma_f32 v118,v22,v22,s88
v_fma_f32 v119,v23,v23,s88
v_fma_f32 v120,v24,v24,s88
v_fma_f32 v121,v25,v25,s88
v_mul_f32 v118,v22,v118
v_mul_f32 v119,v23,v119
v_mul_f32 v120,v24,v120
D 0,56,3072
v_mul_f32 v121,v25,v121
v_exp_f32 v118,v118
v_exp_f32 v119,v119
v_exp_f32 v120,v120
v_exp_f32 v121,v121
v_fma_f32 v118,v118,s89,s89
v_fma_f32 v119,v119,s89,s89
s_add_u32 s55,s55,s60
s_sub_u32 s57,s84,s57
s_add_u32 s54,s54,s46
v_fma_f32 v120,v120,s89,s89
v_fma_f32 v121,v121,s89,s89
v_rcp_f32 v118,v118
v_rcp_f32 v119,v119
v_rcp_f32 v120,v120
v_rcp_f32 v121,v121
v_mul_f32 v22,v22,v118
v_mul_f32 v23,v23,v119
v_mul_f32 v24,v24,v120
v_mul_f32 v25,v25,v121
v_cvt_pk_bf16_f32 v124,v22,v23
v_cvt_pk_bf16_f32 v125,v24,v25
buffer_store_dwordx4 v[122:125],v15,s[36:39],s66 offen offset:0 sc0 sc1
buffer_store_dwordx4 v[122:125],v16,s[36:39],s66 offen offset:0 sc0 sc1
s_mov_b32 s72,s69
s_mov_b32 s69,0x7fff0000
s_mov_b32 s66,0x7fff0000
s_mov_b32 s76,0x7fff0000
s_branch Lg1_gox_%=
Lg1_xn0_%=:
s_cmp_ge_u32 s54,256
s_cbranch_scc1 Lg1_fin0_%=
s_add_u32 m0,s59,4096
s_nop 0
buffer_load_dword v8,s[36:39],s58 offen offset:0 sc1 lds
buffer_store_dword v4,v3,s[36:39],s71 offen offset:0 sc0 sc1
s_add_u32 m0,s59,5120
s_nop 0
buffer_load_dword v8,s[36:39],s58 offen offset:0 sc1 lds
buffer_store_dword v4,v3,s[36:39],s71 offen offset:0 sc0 sc1
s_mov_b32 s83,0
v_fma_f32 v18,-v98,v106,v18
v_fma_f32 v19,-v98,v107,v19
v_fma_f32 v20,-v98,v108,v20
v_fma_f32 v21,-v98,v109,v21
v_mul_f32 v18,v99,v18
v_mul_f32 v19,v99,v19
v_mul_f32 v20,v99,v20
v_mul_f32 v21,v99,v21
v_fma_f32 v114,v18,v18,s88
v_fma_f32 v115,v19,v19,s88
)"
R"(v_fma_f32 v116,v20,v20,s88
v_fma_f32 v117,v21,v21,s88
v_mul_f32 v114,v18,v114
v_mul_f32 v115,v19,v115
v_mul_f32 v116,v20,v116
v_mul_f32 v117,v21,v117
v_exp_f32 v114,v114
v_exp_f32 v115,v115
v_exp_f32 v116,v116
v_exp_f32 v117,v117
v_fma_f32 v114,v114,s89,s89
v_fma_f32 v115,v115,s89,s89
v_fma_f32 v116,v116,s89,s89
v_fma_f32 v117,v117,s89,s89
v_rcp_f32 v114,v114
v_rcp_f32 v115,v115
v_rcp_f32 v116,v116
v_rcp_f32 v117,v117
v_mul_f32 v18,v18,v114
v_mul_f32 v19,v19,v115
v_mul_f32 v20,v20,v116
v_mul_f32 v21,v21,v117
v_cvt_pk_bf16_f32 v122,v18,v19
v_cvt_pk_bf16_f32 v123,v20,v21
v_fma_f32 v22,-v98,v110,v22
v_fma_f32 v23,-v98,v111,v23
v_fma_f32 v24,-v98,v112,v24
v_fma_f32 v25,-v98,v113,v25
v_mul_f32 v22,v99,v22
v_mul_f32 v23,v99,v23
v_mul_f32 v24,v99,v24
v_mul_f32 v25,v99,v25
v_fma_f32 v118,v22,v22,s88
v_fma_f32 v119,v23,v23,s88
v_fma_f32 v120,v24,v24,s88
v_fma_f32 v121,v25,v25,s88
v_mul_f32 v118,v22,v118
v_mul_f32 v119,v23,v119
v_mul_f32 v120,v24,v120
v_mul_f32 v121,v25,v121
v_exp_f32 v118,v118
v_exp_f32 v119,v119
v_exp_f32 v120,v120
v_exp_f32 v121,v121
v_fma_f32 v118,v118,s89,s89
v_fma_f32 v119,v119,s89,s89
v_fma_f32 v120,v120,s89,s89
v_fma_f32 v121,v121,s89,s89
v_rcp_f32 v118,v118
v_rcp_f32 v119,v119
v_rcp_f32 v120,v120
v_rcp_f32 v121,v121
v_mul_f32 v22,v22,v118
v_mul_f32 v23,v23,v119
v_mul_f32 v24,v24,v120
v_mul_f32 v25,v25,v121
v_cvt_pk_bf16_f32 v124,v22,v23
v_cvt_pk_bf16_f32 v125,v24,v25
buffer_store_dwordx4 v[122:125],v15,s[36:39],s66 offen offset:0 sc0 sc1
buffer_store_dwordx4 v[122:125],v16,s[36:39],s66 offen offset:0 sc0 sc1
s_mov_b32 s72,s69
s_mov_b32 s69,0x7fff0000
s_mov_b32 s66,0x7fff0000
s_mov_b32 s76,0x7fff0000
s_branch Lg1_poll_%=
Lg1_fin0_%=:
v_fma_f32 v18,-v98,v106,v18
v_fma_f32 v19,-v98,v107,v19
v_fma_f32 v20,-v98,v108,v20
v_fma_f32 v21,-v98,v109,v21
v_mul_f32 v18,v99,v18
v_mul_f32 v19,v99,v19
v_mul_f32 v20,v99,v20
v_mul_f32 v21,v99,v21
v_fma_f32 v114,v18,v18,s88
v_fma_f32 v115,v19,v19,s88
v_fma_f32 v116,v20,v20,s88
v_fma_f32 v117,v21,v21,s88
v_mul_f32 v114,v18,v114
v_mul_f32 v115,v19,v115
v_mul_f32 v116,v20,v116
v_mul_f32 v117,v21,v117
v_exp_f32 v114,v114
v_exp_f32 v115,v115
v_exp_f32 v116,v116
v_exp_f32 v117,v117
v_fma_f32 v114,v114,s89,s89
v_fma_f32 v115,v115,s89,s89
v_fma_f32 v116,v116,s89,s89
v_fma_f32 v117,v117,s89,s89
v_rcp_f32 v114,v114
v_rcp_f32 v115,v115
v_rcp_f32 v116,v116
v_rcp_f32 v117,v117
v_mul_f32 v18,v18,v114
v_mul_f32 v19,v19,v115
v_mul_f32 v20,v20,v116
v_mul_f32 v21,v21,v117
v_cvt_pk_bf16_f32 v122,v18,v19
v_cvt_pk_bf16_f32 v123,v20,v21
v_fma_f32 v22,-v98,v110,v22
v_fma_f32 v23,-v98,v111,v23
v_fma_f32 v24,-v98,v112,v24
v_fma_f32 v25,-v98,v113,v25
v_mul_f32 v22,v99,v22
v_mul_f32 v23,v99,v23
v_mul_f32 v24,v99,v24
v_mul_f32 v25,v99,v25
v_fma_f32 v118,v22,v22,s88
v_fma_f32 v119,v23,v23,s88
v_fma_f32 v120,v24,v24,s88
v_fma_f32 v121,v25,v25,s88
v_mul_f32 v118,v22,v118
v_mul_f32 v119,v23,v119
v_mul_f32 v120,v24,v120
v_mul_f32 v121,v25,v121
v_exp_f32 v118,v118
v_exp_f32 v119,v119
v_exp_f32 v120,v120
v_exp_f32 v121,v121
v_fma_f32 v118,v118,s89,s89
v_fma_f32 v119,v119,s89,s89
v_fma_f32 v120,v120,s89,s89
v_fma_f32 v121,v121,s89,s89
v_rcp_f32 v118,v118
v_rcp_f32 v119,v119
v_rcp_f32 v120,v120
v_rcp_f32 v121,v121
v_mul_f32 v22,v22,v118
v_mul_f32 v23,v23,v119
v_mul_f32 v24,v24,v120
v_mul_f32 v25,v25,v121
v_cvt_pk_bf16_f32 v124,v22,v23
v_cvt_pk_bf16_f32 v125,v24,v25
buffer_store_dwordx4 v[122:125],v15,s[36:39],s66 offen offset:0 sc0 sc1
buffer_store_dwordx4 v[122:125],v16,s[36:39],s66 offen offset:0 sc0 sc1
s_mov_b32 s72,s69
s_mov_b32 s69,0x7fff0000
s_mov_b32 s66,0x7fff0000
s_mov_b32 s76,0x7fff0000
s_branch Lg1_done_%=
Lg1_K1_%=:
Lg1_L1_%=:
v_mfma_f32_16x16x32_bf16 v[30:33],a[4:7],v[34:37],0
s_add_u32 m0,s59,3072
s_mov_b32 s67,s65
R 54,2,5120
M 30,a[12:15],38
buffer_load_dword v8,s[36:39],s58 offen offset:0 sc1 lds
v_mfma_f32_16x16x32_bf16 v[26:29],a[0:3],v[34:37],0
s_add_u32 s65,s65,s62
R 58,2,6144
v_fma_f32 v18,-v98,v106,v18
M 26,a[8:11],38
L 3
v_fma_f32 v19,-v98,v107,v19
v_fma_f32 v20,-v98,v108,v20
M 26,a[16:19],42
R 62,2,7168
v_fma_f32 v21,-v98,v109,v21
M 26,a[24:27],46
v_mul_f32 v18,v99,v18
v_mul_f32 v19,v99,v19
M 30,a[20:23],42
R 66,2,8192
v_mul_f32 v20,v99,v20
M 30,a[28:31],46
L 3
v_mul_f32 v21,v99,v21
v_fma_f32 v114,v18,v18,s88
M 30,a[36:39],50
R 70,2,9216
v_fma_f32 v115,v19,v19,s88
M 30,a[44:47],54
v_fma_f32 v116,v20,v20,s88
v_fma_f32 v117,v21,v21,s88
M 26,a[32:35],50
R 74,2,10240
v_mul_f32 v114,v18,v114
M 26,a[40:43],54
L 3
v_exp_f32 v114,v114
M 26,a[48:51],58
R 78,2,11264
v_mul_f32 v115,v19,v115
M 26,a[56:59],62
v_exp_f32 v115,v115
M 30,a[52:55],58
R 82,2,12288
v_mul_f32 v116,v20,v116
M 30,a[60:63],62
L 3
v_exp_f32 v116,v116
M 30,a[68:71],66
R 86,2,13312
v_mul_f32 v117,v21,v117
M 30,a[76:79],70
buffer_load_dwordx2 v[102:103],v11,s[36:39],s73 offen offset:0 sc1
M 26,a[64:67],66
s_add_u32 s73,s73,s64
R 90,2,14336
v_fma_f32 v114,v114,s89,s89
M 26,a[72:75],70
L 3
v_exp_f32 v117,v117
M 26,a[80:83],74
s_waitcnt vmcnt(1)
R 94,2,15360
v_fma_f32 v115,v115,s89,s89
M 26,a[88:91],78
s_barrier
ds_read_b32 v7,v6 offset:3072
v_fma_f32 v116,v116,s89,s89
M 30,a[84:87],74
R 34,2,16384
v_fma_f32 v117,v117,s89,s89
M 30,a[92:95],78
L 4
buffer_store_dword v4,v3,s[36:39],s70 offen offset:0 sc0 sc1
M 30,a[100:103],82
s_mov_b32 s70,0x7fff0000
R 38,2,17408
v_fma_f32 v22,-v98,v110,v22
M 30,a[108:111],86
v_rcp_f32 v114,v114
M 26,a[96:99],82
R 42,2,18432
v_mul_f32 v18,v18,v114
M 26,a[104:107],86
L 4
L 3
v_cmp_eq_u32 vcc,s53,v7
M 26,a[112:115],90
R 46,2,19456
v_fma_f32 v23,-v98,v111,v23
M 26,a[120:123],94
v_rcp_f32 v115,v115
M 30,a[116:119],90
s_cmp_eq_u32 vcc_lo,-1
R 50,2,20480
v_mul_f32 v19,v19,v115
s_cbranch_scc0 Lg1_X1_%=
M 30,a[124:127],94
L 3
s_add_u32 m0,s57,0
s_add_u32 s56,s55,0
M 30,a[132:135],34
s_add_u32 s58,s58,s61
R 54,2,21504
v_cvt_pk_bf16_f32 v122,v18,v19
M 30,a[140:143],38
D 0,56,0
M 26,a[128:131],34
s_add_u32 s54,s54,s46
R 58,2,22528
v_fma_f32 v24,-v98,v112,v24
M 26,a[136:139],38
L 3
v_rcp_f32 v116,v116
M 26,a[144:147],42
R 62,2,23552
v_mul_f32 v20,v20,v116
M 26,a[152:155],46
D 0,56,1024
M 30,a[148:151],42
R 66,2,24576
v_fma_f32 v25,-v98,v113,v25
M 30,a[156:159],46
L 3
v_rcp_f32 v117,v117
M 30,a[164:167],50
R 70,2,25600
v_mul_f32 v21,v21,v117
M 30,a[172:175],54
D 0,56,2048
M 26,a[160:163],50
R 74,2,26624
v_cvt_pk_bf16_f32 v123,v20,v21
M 26,a[168:171],54
L 3
v_mul_f32 v22,v99,v22
v_mul_f32 v23,v99,v23
M 26,a[176:179],58
R 78,2,27648
v_mul_f32 v24,v99,v24
M 26,a[184:187],62
D 0,56,3072
M 30,a[180:183],58
s_add_u32 m0,s59,1024
R 82,2,28672
v_mul_f32 v25,v99,v25
M 30,a[188:191],62
L 3
buffer_load_dword v8,s[36:39],s58 offen offset:0 sc1 lds
M 30,a[196:199],66
s_add_u32 m0,s57,16384
R 86,2,29696
v_fma_f32 v118,v22,v22,s88
M 30,a[204:207],70
s_add_u32 s56,s55,16384
v_fma_f32 v119,v23,v23,s88
v_fma_f32 v120,v24,v24,s88
M 26,a[192:195],66
s_add_u32 s55,s55,s60
R 90,2,30720
v_fma_f32 v121,v25,v25,s88
M 26,a[200:203],70
L 3
D 0,56,0
M 26,a[208:211],74
s_waitcnt vmcnt(1)
R 94,2,31744
v_mul_f32 v118,v22,v118
M 26,a[216:219],78
s_barrier
ds_read_b32 v7,v6 offset:1024
v_mul_f32 v119,v23,v119
M 30,a[212:215],74
s_sub_u32 s57,s84,s57
R 34,1,0
v_mul_f32 v120,v24,v120
M 30,a[220:223],78
L 4
buffer_store_dword v4,v3,s[36:39],s70 offen offset:0 sc0 sc1
v_mul_f32 v121,v25,v121
v_exp_f32 v118,v118
v_exp_f32 v119,v119
v_exp_f32 v120,v120
v_exp_f32 v121,v121
M 30,a[228:231],82
s_mov_b32 s70,s69
R 38,1,1024
v_fma_f32 v118,v118,s89,s89
v_fma_f32 v119,v119,s89,s89
v_fma_f32 v120,v120,s89,s89
v_fma_f32 v121,v121,s89,s89
v_rcp_f32 v118,v118
v_rcp_f32 v119,v119
v_rcp_f32 v120,v120
v_rcp_f32 v121,v121
M 30,a[236:239],86
s_mov_b32 s69,s68
v_mul_f32 v22,v22,v118
v_mul_f32 v23,v23,v119
v_mul_f32 v24,v24,v120
v_mul_f32 v25,v25,v121
v_cvt_pk_bf16_f32 v124,v22,v23
v_cvt_pk_bf16_f32 v125,v24,v25
M 26,a[224:227],82
s_add_u32 s68,s68,s63
R 42,1,2048
buffer_store_dwordx4 v[122:125],v15,s[36:39],s66 offen offset:0 sc0 sc1
M 26,a[232:235],86
L 4
L 3
M 26,a[240:243],90
R 46,1,3072
v_cmp_eq_u32 vcc,s53,v7
M 26,a[248:251],94
D 0,56,1024
M 30,a[244:247],90
L 2
s_cmp_eq_u32 vcc_lo,-1
R 50,1,4096
buffer_store_dwordx4 v[122:125],v16,s[36:39],s66 offen offset:0 sc0 sc1
M 30,a[252:255],94
D 0,56,2048
D 0,56,3072
buffer_store_dword v4,v3,s[36:39],s71 offen offset:0 sc0 sc1
buffer_store_dword v4,v3,s[36:39],s71 offen offset:0 sc0 sc1
buffer_store_dword v4,v3,s[36:39],s71 offen offset:0 sc0 sc1
s_cbranch_scc1 Lg1_F0_%=
s_branch Lg1_K0_%=
Lg1_X1_%=:
M 30,a[124:127],94
L 3
s_add_u32 m0,s59,1024
v_cvt_pk_bf16_f32 v122,v18,v19
M 30,a[132:135],34
R 54,2,21504
v_fma_f32 v24,-v98,v112,v24
M 30,a[140:143],38
buffer_load_dword v8,s[36:39],s58 offen offset:0 sc1 lds
M 26,a[128:131],34
R 58,2,22528
v_fma_f32 v25,-v98,v113,v25
M 26,a[136:139],38
L 3
v_rcp_f32 v116,v116
M 26,a[144:147],42
R 62,2,23552
v_mul_f32 v20,v20,v116
M 26,a[152:155],46
v_rcp_f32 v117,v117
M 30,a[148:151],42
R 66,2,24576
v_mul_f32 v21,v21,v117
M 30,a[156:159],46
L 3
v_cvt_pk_bf16_f32 v123,v20,v21
v_mul_f32 v22,v99,v22
M 30,a[164:167],50
R 70,2,25600
v_mul_f32 v23,v99,v23
M 30,a[172:175],54
v_mul_f32 v24,v99,v24
v_mul_f32 v25,v99,v25
M 26,a[160:163],50
R 74,2,26624
v_fma_f32 v118,v22,v22,s88
M 26,a[168:171],54
L 3
v_fma_f32 v119,v23,v23,s88
v_fma_f32 v120,v24,v24,s88
M 26,a[176:179],58
R 78,2,27648
v_fma_f32 v121,v25,v25,s88
M 26,a[184:187],62
v_mul_f32 v118,v22,v118
v_mul_f32 v119,v23,v119
M 30,a[180:183],58
R 82,2,28672
v_mul_f32 v120,v24,v120
M 30,a[188:191],62
L 3
v_exp_f32 v118,v118
M 30,a[196:199],66
R 86,2,29696
v_mul_f32 v121,v25,v121
M 30,a[204:207],70
v_exp_f32 v119,v119
M 26,a[192:195],66
R 90,2,30720
v_fma_f32 v118,v118,s89,s89
M 26,a[200:203],70
L 3
v_exp_f32 v120,v120
M 26,a[208:211],74
s_waitcnt vmcnt(0)
R 94,2,31744
v_fma_f32 v119,v119,s89,s89
M 26,a[216:219],78
s_barrier
buffer_store_dword v4,v3,s[36:39],s70 offen offset:0 sc0 sc1
M 30,a[212:215],74
s_mov_b32 s70,s69
ds_read_b32 v7,v6 offset:1024
v_fma_f32 v120,v120,s89,s89
M 30,a[220:223],78
L 3
v_exp_f32 v121,v121
M 30,a[228:231],82
s_mov_b32 s69,s68
v_fma_f32 v121,v121,s89,s89
v_rcp_f32 v118,v118
v_rcp_f32 v119,v119
v_rcp_f32 v120,v120
v_rcp_f32 v121,v121
M 30,a[236:239],86
s_add_u32 s68,s68,s63
v_mul_f32 v22,v22,v118
v_mul_f32 v23,v23,v119
v_mul_f32 v24,v24,v120
v_mul_f32 v25,v25,v121
v_cvt_pk_bf16_f32 v124,v22,v23
v_cvt_pk_bf16_f32 v125,v24,v25
M 26,a[224:227],82
buffer_store_dwordx4 v[122:125],v15,s[36:39],s66 offen offset:0 sc0 sc1
M 26,a[232:235],86
L 1
M 26,a[240:243],90
L 0
v_cmp_eq_u32 vcc,s53,v7
M 26,a[248:251],94
buffer_store_dwordx4 v[122:125],v16,s[36:39],s66 offen offset:0 sc0 sc1
M 30,a[244:247],90
s_nop 0
M 30,a[252:255],94
s_mov_b32 s80,0
s_cmp_eq_u32 vcc_lo,-1
s_cbranch_scc0 Lg1_xn1_%=
s_add_u32 m0,s57,0
s_add_u32 s56,s55,0
D 0,56,0
v_fma_f32 v26,-v102,v106,v26
v_fma_f32 v27,-v102,v107,v27
v_fma_f32 v28,-v102,v108,v28
v_fma_f32 v29,-v102,v109,v29
v_mul_f32 v26,v103,v26
v_mul_f32 v27,v103,v27
v_mul_f32 v28,v103,v28
D 0,56,1024
v_mul_f32 v29,v103,v29
v_fma_f32 v114,v26,v26,s88
v_fma_f32 v115,v27,v27,s88
v_fma_f32 v116,v28,v28,s88
v_fma_f32 v117,v29,v29,s88
v_mul_f32 v114,v26,v114
v_mul_f32 v115,v27,v115
D 0,56,2048
)"
R"(v_mul_f32 v116,v28,v116
v_mul_f32 v117,v29,v117
v_exp_f32 v114,v114
v_exp_f32 v115,v115
v_exp_f32 v116,v116
v_exp_f32 v117,v117
v_fma_f32 v114,v114,s89,s89
D 0,56,3072
v_fma_f32 v115,v115,s89,s89
v_fma_f32 v116,v116,s89,s89
v_fma_f32 v117,v117,s89,s89
v_rcp_f32 v114,v114
v_rcp_f32 v115,v115
v_rcp_f32 v116,v116
v_rcp_f32 v117,v117
s_add_u32 s58,s58,s61
s_add_u32 m0,s57,16384
s_add_u32 s56,s55,16384
D 0,56,0
v_mul_f32 v26,v26,v114
v_mul_f32 v27,v27,v115
v_mul_f32 v28,v28,v116
v_mul_f32 v29,v29,v117
v_cvt_pk_bf16_f32 v122,v26,v27
v_cvt_pk_bf16_f32 v123,v28,v29
v_fma_f32 v30,-v102,v110,v30
D 0,56,1024
v_fma_f32 v31,-v102,v111,v31
v_fma_f32 v32,-v102,v112,v32
v_fma_f32 v33,-v102,v113,v33
v_mul_f32 v30,v103,v30
v_mul_f32 v31,v103,v31
v_mul_f32 v32,v103,v32
v_mul_f32 v33,v103,v33
D 0,56,2048
v_fma_f32 v118,v30,v30,s88
v_fma_f32 v119,v31,v31,s88
v_fma_f32 v120,v32,v32,s88
v_fma_f32 v121,v33,v33,s88
v_mul_f32 v118,v30,v118
v_mul_f32 v119,v31,v119
v_mul_f32 v120,v32,v120
D 0,56,3072
v_mul_f32 v121,v33,v121
v_exp_f32 v118,v118
v_exp_f32 v119,v119
v_exp_f32 v120,v120
v_exp_f32 v121,v121
v_fma_f32 v118,v118,s89,s89
v_fma_f32 v119,v119,s89,s89
s_add_u32 s55,s55,s60
s_sub_u32 s57,s84,s57
s_add_u32 s54,s54,s46
v_fma_f32 v120,v120,s89,s89
v_fma_f32 v121,v121,s89,s89
v_rcp_f32 v118,v118
v_rcp_f32 v119,v119
v_rcp_f32 v120,v120
v_rcp_f32 v121,v121
v_mul_f32 v30,v30,v118
v_mul_f32 v31,v31,v119
v_mul_f32 v32,v32,v120
v_mul_f32 v33,v33,v121
v_cvt_pk_bf16_f32 v124,v30,v31
v_cvt_pk_bf16_f32 v125,v32,v33
buffer_store_dwordx4 v[122:125],v15,s[36:39],s67 offen offset:0 sc0 sc1
buffer_store_dwordx4 v[122:125],v16,s[36:39],s67 offen offset:0 sc0 sc1
s_mov_b32 s72,s69
s_mov_b32 s69,0x7fff0000
s_mov_b32 s67,0x7fff0000
s_mov_b32 s77,0x7fff0000
s_branch Lg1_gox_%=
Lg1_xn1_%=:
s_cmp_ge_u32 s54,256
s_cbranch_scc1 Lg1_fin1_%=
s_add_u32 m0,s59,4096
s_nop 0
buffer_load_dword v8,s[36:39],s58 offen offset:0 sc1 lds
buffer_store_dword v4,v3,s[36:39],s71 offen offset:0 sc0 sc1
s_add_u32 m0,s59,5120
s_nop 0
buffer_load_dword v8,s[36:39],s58 offen offset:0 sc1 lds
buffer_store_dword v4,v3,s[36:39],s71 offen offset:0 sc0 sc1
s_mov_b32 s83,0
v_fma_f32 v26,-v102,v106,v26
v_fma_f32 v27,-v102,v107,v27
v_fma_f32 v28,-v102,v108,v28
v_fma_f32 v29,-v102,v109,v29
v_mul_f32 v26,v103,v26
v_mul_f32 v27,v103,v27
v_mul_f32 v28,v103,v28
v_mul_f32 v29,v103,v29
v_fma_f32 v114,v26,v26,s88
v_fma_f32 v115,v27,v27,s88
v_fma_f32 v116,v28,v28,s88
v_fma_f32 v117,v29,v29,s88
v_mul_f32 v114,v26,v114
v_mul_f32 v115,v27,v115
v_mul_f32 v116,v28,v116
v_mul_f32 v117,v29,v117
v_exp_f32 v114,v114
v_exp_f32 v115,v115
v_exp_f32 v116,v116
v_exp_f32 v117,v117
v_fma_f32 v114,v114,s89,s89
v_fma_f32 v115,v115,s89,s89
v_fma_f32 v116,v116,s89,s89
v_fma_f32 v117,v117,s89,s89
v_rcp_f32 v114,v114
v_rcp_f32 v115,v115
v_rcp_f32 v116,v116
v_rcp_f32 v117,v117
v_mul_f32 v26,v26,v114
v_mul_f32 v27,v27,v115
v_mul_f32 v28,v28,v116
v_mul_f32 v29,v29,v117
v_cvt_pk_bf16_f32 v122,v26,v27
v_cvt_pk_bf16_f32 v123,v28,v29
v_fma_f32 v30,-v102,v110,v30
v_fma_f32 v31,-v102,v111,v31
v_fma_f32 v32,-v102,v112,v32
v_fma_f32 v33,-v102,v113,v33
v_mul_f32 v30,v103,v30
v_mul_f32 v31,v103,v31
v_mul_f32 v32,v103,v32
v_mul_f32 v33,v103,v33
v_fma_f32 v118,v30,v30,s88
v_fma_f32 v119,v31,v31,s88
v_fma_f32 v120,v32,v32,s88
v_fma_f32 v121,v33,v33,s88
v_mul_f32 v118,v30,v118
v_mul_f32 v119,v31,v119
v_mul_f32 v120,v32,v120
v_mul_f32 v121,v33,v121
v_exp_f32 v118,v118
v_exp_f32 v119,v119
v_exp_f32 v120,v120
v_exp_f32 v121,v121
v_fma_f32 v118,v118,s89,s89
v_fma_f32 v119,v119,s89,s89
v_fma_f32 v120,v120,s89,s89
v_fma_f32 v121,v121,s89,s89
v_rcp_f32 v118,v118
v_rcp_f32 v119,v119
v_rcp_f32 v120,v120
v_rcp_f32 v121,v121
v_mul_f32 v30,v30,v118
v_mul_f32 v31,v31,v119
v_mul_f32 v32,v32,v120
v_mul_f32 v33,v33,v121
v_cvt_pk_bf16_f32 v124,v30,v31
v_cvt_pk_bf16_f32 v125,v32,v33
buffer_store_dwordx4 v[122:125],v15,s[36:39],s67 offen offset:0 sc0 sc1
buffer_store_dwordx4 v[122:125],v16,s[36:39],s67 offen offset:0 sc0 sc1
s_mov_b32 s72,s69
s_mov_b32 s69,0x7fff0000
s_mov_b32 s67,0x7fff0000
s_mov_b32 s77,0x7fff0000
s_branch Lg1_poll_%=
Lg1_fin1_%=:
v_fma_f32 v26,-v102,v106,v26
v_fma_f32 v27,-v102,v107,v27
v_fma_f32 v28,-v102,v108,v28
v_fma_f32 v29,-v102,v109,v29
v_mul_f32 v26,v103,v26
v_mul_f32 v27,v103,v27
v_mul_f32 v28,v103,v28
v_mul_f32 v29,v103,v29
v_fma_f32 v114,v26,v26,s88
v_fma_f32 v115,v27,v27,s88
v_fma_f32 v116,v28,v28,s88
v_fma_f32 v117,v29,v29,s88
v_mul_f32 v114,v26,v114
v_mul_f32 v115,v27,v115
v_mul_f32 v116,v28,v116
v_mul_f32 v117,v29,v117
v_exp_f32 v114,v114
v_exp_f32 v115,v115
v_exp_f32 v116,v116
v_exp_f32 v117,v117
v_fma_f32 v114,v114,s89,s89
v_fma_f32 v115,v115,s89,s89
v_fma_f32 v116,v116,s89,s89
v_fma_f32 v117,v117,s89,s89
v_rcp_f32 v114,v114
v_rcp_f32 v115,v115
v_rcp_f32 v116,v116
v_rcp_f32 v117,v117
v_mul_f32 v26,v26,v114
v_mul_f32 v27,v27,v115
v_mul_f32 v28,v28,v116
v_mul_f32 v29,v29,v117
v_cvt_pk_bf16_f32 v122,v26,v27
v_cvt_pk_bf16_f32 v123,v28,v29
v_fma_f32 v30,-v102,v110,v30
v_fma_f32 v31,-v102,v111,v31
v_fma_f32 v32,-v102,v112,v32
v_fma_f32 v33,-v102,v113,v33
v_mul_f32 v30,v103,v30
v_mul_f32 v31,v103,v31
v_mul_f32 v32,v103,v32
v_mul_f32 v33,v103,v33
v_fma_f32 v118,v30,v30,s88
v_fma_f32 v119,v31,v31,s88
v_fma_f32 v120,v32,v32,s88
v_fma_f32 v121,v33,v33,s88
v_mul_f32 v118,v30,v118
v_mul_f32 v119,v31,v119
v_mul_f32 v120,v32,v120
v_mul_f32 v121,v33,v121
v_exp_f32 v118,v118
v_exp_f32 v119,v119
v_exp_f32 v120,v120
v_exp_f32 v121,v121
v_fma_f32 v118,v118,s89,s89
v_fma_f32 v119,v119,s89,s89
v_fma_f32 v120,v120,s89,s89
v_fma_f32 v121,v121,s89,s89
v_rcp_f32 v118,v118
v_rcp_f32 v119,v119
v_rcp_f32 v120,v120
v_rcp_f32 v121,v121
v_mul_f32 v30,v30,v118
v_mul_f32 v31,v31,v119
v_mul_f32 v32,v32,v120
v_mul_f32 v33,v33,v121
v_cvt_pk_bf16_f32 v124,v30,v31
v_cvt_pk_bf16_f32 v125,v32,v33
buffer_store_dwordx4 v[122:125],v15,s[36:39],s67 offen offset:0 sc0 sc1
buffer_store_dwordx4 v[122:125],v16,s[36:39],s67 offen offset:0 sc0 sc1
s_mov_b32 s72,s69
s_mov_b32 s69,0x7fff0000
s_mov_b32 s67,0x7fff0000
s_mov_b32 s77,0x7fff0000
s_branch Lg1_done_%=
Lg1_done_%=:
s_waitcnt vmcnt(2)
s_barrier
buffer_store_dword v4,v3,s[36:39],s70 offen offset:0 sc0 sc1
s_waitcnt vmcnt(1)
s_barrier
buffer_store_dword v4,v3,s[36:39],s72 offen offset:0 sc0 sc1
s_waitcnt vmcnt(0) lgkmcnt(0)
s_barrier
s_branch Lg2_end_%=
Lg2_begin_%=:
Lcode_g2_b:
s_mov_b64 s[36:37],%1
s_mov_b32 s38,111243392
s_mov_b32 s39,0x00020000
s_mov_b32 s44,%2
s_mov_b32 s45,%3
s_mov_b32 s46,%4
s_mov_b32 s47,%5
s_mov_b32 s48,%6
s_mov_b32 s49,%7
s_mov_b32 s50,%8
s_mov_b32 s51,%9
s_mov_b32 s53,%10
v_mov_b32 v0,%11
v_mov_b32 v3,%12
v_mov_b32 v5,%13
v_mov_b32 v6,%14
v_mov_b32 v18,%15
v_mov_b32 v19,%16
v_mov_b32 v20,%17
v_mov_b32 v21,%18
v_mov_b32 v9,%19
v_mov_b32 v10,%20
v_mov_b32 v11,%21
v_mov_b32 v12,%22
v_mov_b32 v13,%23
v_mov_b32 v14,%24
v_mov_b32 v15,%25
v_mov_b32 v16,%26
s_mov_b64 s[40:41],%27
s_mov_b32 s42,2097152
s_mov_b32 s43,0x00020000
s_lshl_b32 s57,s44,11
s_cmp_ge_u32 s45,8
s_cbranch_scc1 Lg2_dly_%=
s_sleep 16
s_sleep 16
s_sleep 16
s_sleep 16
Lg2_dly_%=:
s_add_u32 s55,s50,0
s_add_u32 m0,s57,0
s_nop 0
buffer_load_dwordx4 v18,s[40:43],s55 offen offset:0  lds
s_add_u32 m0,s57,1024
s_nop 0
buffer_load_dwordx4 v19,s[40:43],s55 offen offset:0  lds
s_add_u32 m0,s57,8064
s_nop 0
buffer_load_dwordx4 v18,s[40:43],s55 offen offset:128  lds
s_add_u32 m0,s57,9088
s_nop 0
buffer_load_dwordx4 v19,s[40:43],s55 offen offset:128  lds
s_add_u32 s55,s50,131072
s_add_u32 m0,s57,16384
s_nop 0
buffer_load_dwordx4 v18,s[40:43],s55 offen offset:0  lds
s_add_u32 m0,s57,17408
s_nop 0
buffer_load_dwordx4 v19,s[40:43],s55 offen offset:0  lds
s_add_u32 m0,s57,24448
s_nop 0
buffer_load_dwordx4 v18,s[40:43],s55 offen offset:128  lds
s_add_u32 m0,s57,25472
s_nop 0
buffer_load_dwordx4 v19,s[40:43],s55 offen offset:128  lds
s_add_u32 s55,s50,262144
s_add_u32 m0,s57,32768
s_nop 0
buffer_load_dwordx4 v18,s[40:43],s55 offen offset:0  lds
s_add_u32 m0,s57,33792
s_nop 0
buffer_load_dwordx4 v19,s[40:43],s55 offen offset:0  lds
s_add_u32 m0,s57,40832
s_nop 0
buffer_load_dwordx4 v18,s[40:43],s55 offen offset:128  lds
s_add_u32 m0,s57,41856
s_nop 0
buffer_load_dwordx4 v19,s[40:43],s55 offen offset:128  lds
s_add_u32 s55,s50,393216
s_add_u32 m0,s57,49152
s_nop 0
buffer_load_dwordx4 v18,s[40:43],s55 offen offset:0  lds
s_add_u32 m0,s57,50176
s_nop 0
buffer_load_dwordx4 v19,s[40:43],s55 offen offset:0  lds
s_add_u32 m0,s57,57216
s_nop 0
buffer_load_dwordx4 v18,s[40:43],s55 offen offset:128  lds
s_add_u32 m0,s57,58240
s_nop 0
buffer_load_dwordx4 v19,s[40:43],s55 offen offset:128  lds
s_add_u32 s55,s50,524288
s_add_u32 m0,s57,65536
s_nop 0
buffer_load_dwordx4 v18,s[40:43],s55 offen offset:0  lds
s_add_u32 m0,s57,66560
s_nop 0
buffer_load_dwordx4 v19,s[40:43],s55 offen offset:0  lds
s_add_u32 m0,s57,73600
s_nop 0
buffer_load_dwordx4 v18,s[40:43],s55 offen offset:128  lds
s_add_u32 m0,s57,74624
s_nop 0
buffer_load_dwordx4 v19,s[40:43],s55 offen offset:128  lds
s_add_u32 s55,s50,655360
s_add_u32 m0,s57,81920
s_nop 0
buffer_load_dwordx4 v18,s[40:43],s55 offen offset:0  lds
s_add_u32 m0,s57,82944
s_nop 0
buffer_load_dwordx4 v19,s[40:43],s55 offen offset:0  lds
s_add_u32 m0,s57,89984
s_nop 0
buffer_load_dwordx4 v18,s[40:43],s55 offen offset:128  lds
s_add_u32 m0,s57,91008
s_nop 0
buffer_load_dwordx4 v19,s[40:43],s55 offen offset:128  lds
s_waitcnt vmcnt(20) lgkmcnt(0)
s_barrier
s_add_u32 s55,s50,786432
s_add_u32 m0,s57,98304
s_nop 0
buffer_load_dwordx4 v18,s[40:43],s55 offen offset:0  lds
s_add_u32 m0,s57,99328
s_nop 0
buffer_load_dwordx4 v19,s[40:43],s55 offen offset:0  lds
s_add_u32 m0,s57,106368
s_nop 0
buffer_load_dwordx4 v18,s[40:43],s55 offen offset:128  lds
s_add_u32 m0,s57,107392
s_nop 0
buffer_load_dwordx4 v19,s[40:43],s55 offen offset:128  lds
v_add_u32 v26,0,v20
v_add_u32 v27,0,v21
T a[0:1],26,0
T a[2:3],26,512
T a[4:5],27,0
T a[6:7],27,512
T a[8:9],26,4096
T a[10:11],26,4608
T a[12:13],27,4096
T a[14:15],27,4608
s_waitcnt vmcnt(20) lgkmcnt(0)
s_barrier
s_add_u32 s55,s50,917504
s_add_u32 m0,s57,0
s_nop 0
buffer_load_dwordx4 v18,s[40:43],s55 offen offset:0  lds
s_add_u32 m0,s57,1024
s_nop 0
buffer_load_dwordx4 v19,s[40:43],s55 offen offset:0  lds
s_add_u32 m0,s57,8064
s_nop 0
buffer_load_dwordx4 v18,s[40:43],s55 offen offset:128  lds
s_add_u32 m0,s57,9088
s_nop 0
buffer_load_dwordx4 v19,s[40:43],s55 offen offset:128  lds
v_add_u32 v26,16384,v20
v_add_u32 v27,16384,v21
T a[16:17],26,0
T a[18:19],26,512
T a[20:21],27,0
T a[22:23],27,512
T a[24:25],26,4096
T a[26:27],26,4608
T a[28:29],27,4096
T a[30:31],27,4608
s_waitcnt vmcnt(20) lgkmcnt(0)
s_barrier
s_add_u32 s55,s50,1048576
s_add_u32 m0,s57,16384
s_nop 0
buffer_load_dwordx4 v18,s[40:43],s55 offen offset:0  lds
s_add_u32 m0,s57,17408
s_nop 0
buffer_load_dwordx4 v19,s[40:43],s55 offen offset:0  lds
s_add_u32 m0,s57,24448
s_nop 0
buffer_load_dwordx4 v18,s[40:43],s55 offen offset:128  lds
s_add_u32 m0,s57,25472
s_nop 0
buffer_load_dwordx4 v19,s[40:43],s55 offen offset:128  lds
v_add_u32 v26,32768,v20
v_add_u32 v27,32768,v21
T a[32:33],26,0
T a[34:35],26,512
T a[36:37],27,0
T a[38:39],27,512
T a[40:41],26,4096
T a[42:43],26,4608
T a[44:45],27,4096
T a[46:47],27,4608
s_waitcnt vmcnt(20) lgkmcnt(0)
s_barrier
s_add_u32 s55,s50,1179648
s_add_u32 m0,s57,32768
s_nop 0
buffer_load_dwordx4 v18,s[40:43],s55 offen offset:0  lds
s_add_u32 m0,s57,33792
s_nop 0
buffer_load_dwordx4 v19,s[40:43],s55 offen offset:0  lds
s_add_u32 m0,s57,40832
s_nop 0
buffer_load_dwordx4 v18,s[40:43],s55 offen offset:128  lds
s_add_u32 m0,s57,41856
s_nop 0
buffer_load_dwordx4 v19,s[40:43],s55 offen offset:128  lds
v_add_u32 v26,49152,v20
v_add_u32 v27,49152,v21
T a[48:49],26,0
T a[50:51],26,512
T a[52:53],27,0
T a[54:55],27,512
T a[56:57],26,4096
T a[58:59],26,4608
T a[60:61],27,4096
T a[62:63],27,4608
s_waitcnt vmcnt(20) lgkmcnt(0)
s_barrier
s_add_u32 s55,s50,1310720
s_add_u32 m0,s57,49152
s_nop 0
buffer_load_dwordx4 v18,s[40:43],s55 offen offset:0  lds
s_add_u32 m0,s57,50176
s_nop 0
buffer_load_dwordx4 v19,s[40:43],s55 offen offset:0  lds
s_add_u32 m0,s57,57216
s_nop 0
buffer_load_dwordx4 v18,s[40:43],s55 offen offset:128  lds
s_add_u32 m0,s57,58240
s_nop 0
buffer_load_dwordx4 v19,s[40:43],s55 offen offset:128  lds
v_add_u32 v26,65536,v20
v_add_u32 v27,65536,v21
T a[64:65],26,0
T a[66:67],26,512
T a[68:69],27,0
T a[70:71],27,512
T a[72:73],26,4096
T a[74:75],26,4608
T a[76:77],27,4096
T a[78:79],27,4608
s_waitcnt vmcnt(20) lgkmcnt(0)
s_barrier
s_add_u32 s55,s50,1441792
s_add_u32 m0,s57,65536
s_nop 0
buffer_load_dwordx4 v18,s[40:43],s55 offen offset:0  lds
s_add_u32 m0,s57,66560
s_nop 0
buffer_load_dwordx4 v19,s[40:43],s55 offen offset:0  lds
s_add_u32 m0,s57,73600
s_nop 0
)"
R"(buffer_load_dwordx4 v18,s[40:43],s55 offen offset:128  lds
s_add_u32 m0,s57,74624
s_nop 0
buffer_load_dwordx4 v19,s[40:43],s55 offen offset:128  lds
v_add_u32 v26,81920,v20
v_add_u32 v27,81920,v21
T a[80:81],26,0
T a[82:83],26,512
T a[84:85],27,0
T a[86:87],27,512
T a[88:89],26,4096
T a[90:91],26,4608
T a[92:93],27,4096
T a[94:95],27,4608
s_waitcnt vmcnt(20) lgkmcnt(0)
s_barrier
s_add_u32 s55,s50,1572864
s_add_u32 m0,s57,81920
s_nop 0
buffer_load_dwordx4 v18,s[40:43],s55 offen offset:0  lds
s_add_u32 m0,s57,82944
s_nop 0
buffer_load_dwordx4 v19,s[40:43],s55 offen offset:0  lds
s_add_u32 m0,s57,89984
s_nop 0
buffer_load_dwordx4 v18,s[40:43],s55 offen offset:128  lds
s_add_u32 m0,s57,91008
s_nop 0
buffer_load_dwordx4 v19,s[40:43],s55 offen offset:128  lds
v_add_u32 v26,98304,v20
v_add_u32 v27,98304,v21
T a[96:97],26,0
T a[98:99],26,512
T a[100:101],27,0
T a[102:103],27,512
T a[104:105],26,4096
T a[106:107],26,4608
T a[108:109],27,4096
T a[110:111],27,4608
s_waitcnt vmcnt(20) lgkmcnt(0)
s_barrier
s_add_u32 s55,s50,1703936
s_add_u32 m0,s57,98304
s_nop 0
buffer_load_dwordx4 v18,s[40:43],s55 offen offset:0  lds
s_add_u32 m0,s57,99328
s_nop 0
buffer_load_dwordx4 v19,s[40:43],s55 offen offset:0  lds
s_add_u32 m0,s57,106368
s_nop 0
buffer_load_dwordx4 v18,s[40:43],s55 offen offset:128  lds
s_add_u32 m0,s57,107392
s_nop 0
buffer_load_dwordx4 v19,s[40:43],s55 offen offset:128  lds
v_add_u32 v26,0,v20
v_add_u32 v27,0,v21
T a[112:113],26,0
T a[114:115],26,512
T a[116:117],27,0
T a[118:119],27,512
T a[120:121],26,4096
T a[122:123],26,4608
T a[124:125],27,4096
T a[126:127],27,4608
s_waitcnt vmcnt(20) lgkmcnt(0)
s_barrier
s_add_u32 s55,s50,1835008
s_add_u32 m0,s57,0
s_nop 0
buffer_load_dwordx4 v18,s[40:43],s55 offen offset:0  lds
s_add_u32 m0,s57,1024
s_nop 0
buffer_load_dwordx4 v19,s[40:43],s55 offen offset:0  lds
s_add_u32 m0,s57,8064
s_nop 0
buffer_load_dwordx4 v18,s[40:43],s55 offen offset:128  lds
s_add_u32 m0,s57,9088
s_nop 0
buffer_load_dwordx4 v19,s[40:43],s55 offen offset:128  lds
v_add_u32 v26,16384,v20
v_add_u32 v27,16384,v21
T a[128:129],26,0
T a[130:131],26,512
T a[132:133],27,0
T a[134:135],27,512
T a[136:137],26,4096
T a[138:139],26,4608
T a[140:141],27,4096
T a[142:143],27,4608
s_waitcnt vmcnt(20) lgkmcnt(0)
s_barrier
s_add_u32 s55,s50,1966080
s_add_u32 m0,s57,16384
s_nop 0
buffer_load_dwordx4 v18,s[40:43],s55 offen offset:0  lds
s_add_u32 m0,s57,17408
s_nop 0
buffer_load_dwordx4 v19,s[40:43],s55 offen offset:0  lds
s_add_u32 m0,s57,24448
s_nop 0
buffer_load_dwordx4 v18,s[40:43],s55 offen offset:128  lds
s_add_u32 m0,s57,25472
s_nop 0
buffer_load_dwordx4 v19,s[40:43],s55 offen offset:128  lds
v_add_u32 v26,32768,v20
v_add_u32 v27,32768,v21
T a[144:145],26,0
T a[146:147],26,512
T a[148:149],27,0
T a[150:151],27,512
T a[152:153],26,4096
T a[154:155],26,4608
T a[156:157],27,4096
T a[158:159],27,4608
s_waitcnt vmcnt(20) lgkmcnt(0)
s_barrier
v_add_u32 v26,49152,v20
v_add_u32 v27,49152,v21
T a[160:161],26,0
T a[162:163],26,512
T a[164:165],27,0
T a[166:167],27,512
T a[168:169],26,4096
T a[170:171],26,4608
T a[172:173],27,4096
T a[174:175],27,4608
s_waitcnt vmcnt(16) lgkmcnt(0)
s_barrier
v_add_u32 v26,65536,v20
v_add_u32 v27,65536,v21
T a[176:177],26,0
T a[178:179],26,512
T a[180:181],27,0
T a[182:183],27,512
T a[184:185],26,4096
T a[186:187],26,4608
T a[188:189],27,4096
T a[190:191],27,4608
s_waitcnt vmcnt(12) lgkmcnt(0)
s_barrier
v_add_u32 v26,81920,v20
v_add_u32 v27,81920,v21
T a[192:193],26,0
T a[194:195],26,512
T a[196:197],27,0
T a[198:199],27,512
T a[200:201],26,4096
T a[202:203],26,4608
T a[204:205],27,4096
T a[206:207],27,4608
s_waitcnt vmcnt(8) lgkmcnt(0)
s_barrier
v_add_u32 v26,98304,v20
v_add_u32 v27,98304,v21
T a[208:209],26,0
T a[210:211],26,512
T a[212:213],27,0
T a[214:215],27,512
T a[216:217],26,4096
T a[218:219],26,4608
T a[220:221],27,4096
T a[222:223],27,4608
s_waitcnt vmcnt(4) lgkmcnt(0)
s_barrier
v_add_u32 v26,0,v20
v_add_u32 v27,0,v21
T a[224:225],26,0
T a[226:227],26,512
T a[228:229],27,0
T a[230:231],27,512
T a[232:233],26,4096
T a[234:235],26,4608
T a[236:237],27,4096
T a[238:239],27,4608
s_waitcnt vmcnt(0) lgkmcnt(0)
s_barrier
v_add_u32 v26,16384,v20
v_add_u32 v27,16384,v21
T a[240:241],26,0
T a[242:243],26,512
T a[244:245],27,0
T a[246:247],27,512
T a[248:249],26,4096
T a[250:251],26,4608
T a[252:253],27,4096
T a[254:255],27,4608
L 0
s_barrier
v_mov_b32 v4,s53
s_mov_b32 s82,0
s_mov_b32 s80,0
s_mov_b32 s54,s45
s_mul_i32 s57,s44,5120
s_mul_i32 s55,s45,40960
s_add_u32 s55,s55,s57
s_add_u32 s55,s55,17825792
s_mul_i32 s57,s44,4096
s_mul_i32 s60,s46,40960
s_mul_i32 s61,s46,1024
s_mul_i32 s62,s46,32768
s_mul_i32 s63,s46,1024
s_mul_i32 s64,s46,512
s_lshl_b32 s59,s44,8
s_add_u32 s59,s59,131072
s_mul_i32 s58,s45,1024
s_add_u32 s58,s58,109333504
s_mul_i32 s65,s45,32768
s_add_u32 s65,s65,s47
s_mul_i32 s68,s45,1024
s_add_u32 s68,s68,s48
s_mul_i32 s73,s45,512
s_add_u32 s73,s73,106954752
s_mul_i32 s74,s45,32768
s_add_u32 s74,s74,s49
s_mul_i32 s75,s45,1024
s_add_u32 s75,s75,107479040
s_mov_b32 s66,0x7fff0000
s_mov_b32 s67,0x7fff0000
s_mov_b32 s69,0x7fff0000
s_mov_b32 s76,0x7fff0000
s_mov_b32 s77,0x7fff0000
s_mov_b32 s70,0x7fff0000
s_mov_b32 s71,0x7fff0000
s_mov_b32 s72,0x7fff0000
v_mov_b32 v1,v0
v_add_u32 v2,16384,v0
s_mov_b32 s84,16384
s_mov_b32 s86,0x3a800000
s_mov_b32 s87,0x38a7c5ac
s_mov_b32 s88,0x409d30fc
s_mov_b32 s89,0xbeeff558
s_lshr_b32 s85,s44,1
s_mul_i32 s85,s85,127
v_lshlrev_b32 v8,22,v0
v_and_b32 v8,0x80000000,v8
v_or_b32 v8,v8,v5
v_add_u32 v2,32768,v0
s_lshl_b32 s84,s57,1
s_add_u32 s84,s84,32768
s_getpc_b64 s[40:41]
Lg2_pc_%=:
s_lshl_b32 s78,s44,13
v_lshlrev_b32 v106,3,v0
v_add_u32 v106,s78,v106
v_min_u32 v106,Lcode_g12_e-Lg2_pc_%=-4,v106
s_mov_b32 m0,163584
s_nop 0
global_load_lds_dword v106,s[40:41]
s_and_b32 s79,s54,127
s_cmp_eq_u32 s79,0
s_cselect_b32 s94,0x3300,0
s_cmp_eq_u32 s79,127
s_cselect_b32 s94,0xcc00,s94
s_add_u32 m0,s59,4096
s_nop 0
buffer_load_dword v8,s[36:39],s58 offen offset:0 sc1 lds
buffer_store_dword v4,v3,s[36:39],s71 offen offset:0 sc0 sc1
s_add_u32 m0,s59,5120
s_nop 0
buffer_load_dword v8,s[36:39],s58 offen offset:0 sc1 lds
buffer_store_dword v4,v3,s[36:39],s71 offen offset:0 sc0 sc1
s_mov_b32 s83,0
Lg2_poll_%=:
s_add_u32 s78,s83,2
s_and_b32 s78,s78,3
s_lshl_b32 s78,s78,10
s_add_u32 s78,s78,4096
s_add_u32 m0,s59,s78
s_nop 0
buffer_load_dword v8,s[36:39],s58 offen offset:0 sc1 lds
s_and_b32 s78,s83,3
s_lshl_b32 s78,s78,10
v_add_u32 v106,s78,v6
s_waitcnt vmcnt(4)
s_barrier
ds_read_b32 v7,v106 offset:4096
buffer_store_dword v4,v3,s[36:39],s70 offen offset:0 sc0 sc1
s_mov_b32 s70,s71
s_mov_b32 s71,s72
s_mov_b32 s72,0x7fff0000
s_add_u32 s83,s83,1
s_add_u32 s82,s82,1
L 0
v_cmp_eq_u32 vcc,s53,v7
s_nop 0
s_or_b64 vcc,vcc,s[94:95]
s_cmp_eq_u32 vcc_lo,-1
s_cbranch_scc1 Lg2_go_%=
s_cmp_lt_u32 s82,20000
s_cbranch_scc1 Lg2_poll_%=
Lg2_go_%=:
s_and_b32 s78,s54,127
s_cmp_eq_u32 s78,s85
s_cselect_b64 s[40:41],-1,0
v_cndmask_b32 v17,v15,v16,s[40:41]
s_add_u32 m0,s57,0
s_add_u32 s56,s55,0
D 17,56,0
s_add_u32 s56,s55,256
D 17,56,1024
s_add_u32 s56,s55,512
D 17,56,2048
s_add_u32 s56,s55,768
D 17,56,3072
s_add_u32 s58,s58,s61
s_add_u32 m0,s59,2048
s_nop 0
buffer_load_dword v8,s[36:39],s58 offen offset:0 sc1 lds
v_cndmask_b32 v17,v10,v9,s[40:41]
s_add_u32 m0,s57,16384
s_add_u32 s56,s55,20480
D 17,56,0
s_add_u32 s56,s55,20736
D 17,56,1024
s_add_u32 s56,s55,20992
D 17,56,2048
s_add_u32 s56,s55,21248
D 17,56,3072
s_add_u32 s55,s55,s60
s_sub_u32 s57,s84,s57
s_add_u32 s54,s54,s46
s_and_b32 s79,s54,127
s_cmp_eq_u32 s79,0
s_cselect_b32 s94,0x3300,0
s_cmp_eq_u32 s79,127
s_cselect_b32 s94,0xcc00,s94
s_waitcnt vmcnt(4)
s_barrier
buffer_store_dword v4,v3,s[36:39],s70 offen offset:0 sc0 sc1
s_min_u32 s71,s71,s72
buffer_store_dword v4,v3,s[36:39],s71 offen offset:0 sc0 sc1
s_mov_b32 s70,0x7fff0000
s_mov_b32 s71,0x7fff0000
s_mov_b32 s72,0x7fff0000
s_cmp_eq_u32 s80,1
buffer_store_dword v4,v3,s[36:39],s71 offen offset:0 sc0 sc1
s_cbranch_scc1 Lg2_st1_%=
s_branch Lg2_st0_%=
Lg2_gox_%=:
s_add_u32 m0,s59,2048
s_nop 0
buffer_load_dword v8,s[36:39],s58 offen offset:0 sc1 lds
s_and_b32 s79,s54,127
s_cmp_eq_u32 s79,0
s_cselect_b32 s94,0x3300,0
s_cmp_eq_u32 s79,127
s_cselect_b32 s94,0xcc00,s94
s_waitcnt vmcnt(0)
s_barrier
buffer_store_dword v4,v3,s[36:39],s70 offen offset:0 sc0 sc1
s_min_u32 s71,s71,s72
buffer_store_dword v4,v3,s[36:39],s71 offen offset:0 sc0 sc1
s_mov_b32 s70,0x7fff0000
s_mov_b32 s71,0x7fff0000
s_mov_b32 s72,0x7fff0000
s_cmp_eq_u32 s80,1
s_cbranch_scc1 Lg2_st1_%=
Lg2_st0_%=:
ds_read_b32 v7,v6 offset:2048
R 34,1,0
R 38,1,1024
R 42,1,2048
R 46,1,3072
R 50,1,4096
L 5
v_cmp_eq_u32 vcc,s53,v7
s_nop 0
s_or_b64 vcc,vcc,s[94:95]
L 3
s_cmp_eq_u32 vcc_lo,-1
s_cbranch_scc1 Lg2_F0_%=
s_branch Lg2_L0_%=
Lg2_st1_%=:
ds_read_b32 v7,v6 offset:2048
R 34,2,0
R 38,2,1024
R 42,2,2048
R 46,2,3072
R 50,2,4096
L 5
v_cmp_eq_u32 vcc,s53,v7
s_nop 0
s_or_b64 vcc,vcc,s[94:95]
L 3
s_cmp_eq_u32 vcc_lo,-1
s_cbranch_scc1 Lg2_F1_%=
s_branch Lg2_L1_%=
Lg2_F0_%=:
v_mfma_f32_16x16x32_bf16 v[22:25],a[4:7],v[34:37],0
s_add_u32 s58,s58,s61
s_add_u32 m0,s59,0
s_and_b32 s78,s54,127
R 54,1,5120
M 22,a[12:15],38
s_cmp_eq_u32 s78,s85
s_cselect_b64 s[40:41],-1,0
s_add_u32 s56,s55,0
buffer_load_dword v8,s[36:39],s58 offen offset:0 sc1 lds
v_mfma_f32_16x16x32_bf16 v[18:21],a[0:3],v[34:37],0
s_add_u32 m0,s57,0
R 58,1,6144
v_cndmask_b32 v17,v15,v16,s[40:41]
M 18,a[8:11],38
L 3
D 17,56,0
M 18,a[16:19],42
s_add_u32 s56,s55,256
R 62,1,7168
v_lshlrev_b32 v106,16,v126
M 18,a[24:27],46
s_add_u32 s54,s54,s46
v_and_b32 v107,0xffff0000,v126
v_lshlrev_b32 v108,16,v127
M 22,a[20:23],42
s_and_b32 s79,s54,127
R 66,1,8192
v_and_b32 v109,0xffff0000,v127
M 22,a[28:31],46
L 3
D 17,56,1024
M 22,a[36:39],50
s_add_u32 s56,s55,512
R 70,1,9216
v_lshlrev_b32 v110,16,v128
M 22,a[44:47],54
s_cmp_eq_u32 s79,0
v_and_b32 v111,0xffff0000,v128
v_lshlrev_b32 v112,16,v129
M 18,a[32:35],50
s_cselect_b32 s94,0x3300,0
R 74,1,10240
v_and_b32 v113,0xffff0000,v129
M 18,a[40:43],54
L 3
D 17,56,2048
M 18,a[48:51],58
s_add_u32 s56,s55,768
R 78,1,11264
v_fma_f32 v106,v106,v103,v102
M 18,a[56:59],62
D 17,56,3072
M 22,a[52:55],58
s_add_u32 m0,s57,16384
R 82,1,12288
v_fma_f32 v107,v107,v103,v102
M 22,a[60:63],62
L 3
v_fma_f32 v108,v108,v103,v102
v_fma_f32 v109,v109,v103,v102
M 22,a[68:71],66
s_add_u32 s56,s55,20480
R 86,1,13312
v_fma_f32 v110,v110,v103,v102
M 22,a[76:79],70
buffer_load_dwordx4 v[122:125],v0,s[36:39],s74 offen offset:0 sc1
M 18,a[64:67],66
s_cmp_eq_u32 s79,127
R 90,1,14336
v_fma_f32 v111,v111,v103,v102
M 18,a[72:75],70
L 3
buffer_load_dwordx2 v[98:99],v11,s[36:39],s73 offen offset:8 sc1
M 18,a[80:83],74
s_waitcnt vmcnt(10)
R 94,1,15360
v_fma_f32 v112,v112,v103,v102
M 18,a[88:91],78
s_barrier
v_fma_f32 v113,v113,v103,v102
v_add_f32 v26,v26,v106
M 22,a[84:87],74
s_cselect_b32 s94,0xcc00,s94
R 34,1,16384
v_add_f32 v27,v27,v107
M 22,a[92:95],78
L 3
v_add_f32 v28,v28,v108
v_add_f32 v29,v29,v109
M 22,a[100:103],82
s_mov_b32 s66,s65
R 38,1,17408
v_add_f32 v30,v30,v110
M 22,a[108:111],86
s_mov_b32 s76,s75
v_cndmask_b32 v17,v10,v9,s[40:41]
v_add_f32 v31,v31,v111
M 18,a[96:99],82
s_add_u32 s73,s73,s64
R 42,1,18432
v_add_f32 v32,v32,v112
M 18,a[104:107],86
L 3
D 17,56,0
M 18,a[112:115],90
s_add_u32 s56,s55,20736
R 46,1,19456
v_add_f32 v33,v33,v113
M 18,a[120:123],94
s_add_u32 s74,s74,s62
v_cvt_pk_bf16_f32 v114,v26,v27
)"
R"(v_cvt_pk_bf16_f32 v115,v28,v29
M 22,a[116:119],90
s_add_u32 s65,s65,s62
R 50,1,20480
v_fma_f32 v107,v26,v26,s87
M 22,a[124:127],94
L 3
v_add_f32 v106,v26,v27
v_fma_f32 v107,v27,v27,v107
M 22,a[132:135],34
s_add_u32 s75,s75,s63
R 54,1,21504
v_add_f32 v106,v106,v28
M 22,a[140:143],38
D 17,56,1024
M 18,a[128:131],34
s_add_u32 s56,s55,20992
R 58,1,22528
v_fma_f32 v107,v28,v28,v107
M 18,a[136:139],38
L 3
v_add_f32 v106,v106,v29
v_fma_f32 v107,v29,v29,v107
M 18,a[144:147],42
s_sub_u32 s57,s84,s57
R 62,1,23552
v_add_f32 v106,v106,v30
M 18,a[152:155],46
v_fma_f32 v107,v30,v30,v107
v_add_f32 v106,v106,v31
M 22,a[148:151],42
R 66,1,24576
v_fma_f32 v107,v31,v31,v107
M 22,a[156:159],46
L 3
v_add_f32 v106,v106,v32
v_fma_f32 v107,v32,v32,v107
M 22,a[164:167],50
R 70,1,25600
v_add_f32 v106,v106,v33
M 22,a[172:175],54
v_fma_f32 v107,v33,v33,v107
v_cvt_pk_bf16_f32 v116,v30,v31
M 18,a[160:163],50
R 74,1,26624
v_cvt_pk_bf16_f32 v117,v32,v33
M 18,a[168:171],54
L 3
v_permlane16_swap_b32 v106,v107
v_add_f32 v106,v106,v107
v_mov_b32 v107,v106
M 18,a[176:179],58
R 78,1,27648
M 18,a[184:187],62
v_permlane32_swap_b32 v106,v107
v_add_f32 v106,v106,v107
M 22,a[180:183],58
R 82,1,28672
ds_write_b32 v12,v106 offset:2048
M 22,a[188:191],62
L 4
D 17,56,2048
M 22,a[196:199],66
s_add_u32 s56,s55,21248
s_add_u32 s55,s55,s60
R 86,1,29696
M 22,a[204:207],70
D 17,56,3072
M 18,a[192:195],66
R 90,1,30720
M 18,a[200:203],70
L 4
L 2
buffer_store_dwordx4 v[114:117],v0,s[36:39],s67 offen offset:0 sc0 sc1
M 18,a[208:211],74
s_waitcnt vmcnt(7)
R 94,1,31744
M 18,a[216:219],78
s_barrier
ds_read_b32 v7,v6 offset:0
R 118,13,2048
M 22,a[212:215],74
R 34,2,0
M 22,a[220:223],78
L 5
buffer_store_dword v4,v3,s[36:39],s70 offen offset:0 sc0 sc1
M 22,a[228:231],82
s_mov_b32 s70,s69
s_mov_b32 s69,s68
R 38,2,1024
M 22,a[236:239],86
s_add_u32 s68,s68,s63
M 18,a[224:227],82
R 42,2,2048
M 18,a[232:235],86
L 5
L 4
v_cmp_eq_u32 vcc,s53,v7
M 18,a[240:243],90
L 3
R 46,2,3072
v_add_f32 v118,v118,v119
M 18,a[248:251],94
s_or_b64 vcc,vcc,s[94:95]
v_add_f32 v120,v120,v121
v_add_f32 v118,v118,v120
M 22,a[244:247],90
s_cmp_eq_u32 vcc_lo,-1
R 50,2,4096
v_mul_f32 v118,s86,v118
M 22,a[252:255],94
s_waitcnt vmcnt(6) lgkmcnt(3)
buffer_store_dword v118,v14,s[36:39],s77 offen offset:0 sc0 sc1
s_cbranch_scc0 Lg2_K1_%=
Lg2_F1_%=:
v_mfma_f32_16x16x32_bf16 v[30:33],a[4:7],v[34:37],0
s_add_u32 s58,s58,s61
s_add_u32 m0,s59,1024
s_and_b32 s78,s54,127
R 54,2,5120
M 30,a[12:15],38
s_cmp_eq_u32 s78,s85
s_cselect_b64 s[40:41],-1,0
s_add_u32 s56,s55,0
buffer_load_dword v8,s[36:39],s58 offen offset:0 sc1 lds
v_mfma_f32_16x16x32_bf16 v[26:29],a[0:3],v[34:37],0
s_add_u32 m0,s57,0
R 58,2,6144
v_cndmask_b32 v17,v15,v16,s[40:41]
M 26,a[8:11],38
L 3
D 17,56,0
M 26,a[16:19],42
s_add_u32 s56,s55,256
R 62,2,7168
v_lshlrev_b32 v106,16,v122
M 26,a[24:27],46
s_add_u32 s54,s54,s46
v_and_b32 v107,0xffff0000,v122
v_lshlrev_b32 v108,16,v123
M 30,a[20:23],42
s_and_b32 s79,s54,127
R 66,2,8192
v_and_b32 v109,0xffff0000,v123
M 30,a[28:31],46
L 3
D 17,56,1024
M 30,a[36:39],50
s_add_u32 s56,s55,512
R 70,2,9216
v_lshlrev_b32 v110,16,v124
M 30,a[44:47],54
s_cmp_eq_u32 s79,0
v_and_b32 v111,0xffff0000,v124
v_lshlrev_b32 v112,16,v125
M 26,a[32:35],50
s_cselect_b32 s94,0x3300,0
R 74,2,10240
v_and_b32 v113,0xffff0000,v125
M 26,a[40:43],54
L 3
D 17,56,2048
M 26,a[48:51],58
s_add_u32 s56,s55,768
R 78,2,11264
v_fma_f32 v106,v106,v99,v98
M 26,a[56:59],62
D 17,56,3072
M 30,a[52:55],58
s_add_u32 m0,s57,16384
R 82,2,12288
v_fma_f32 v107,v107,v99,v98
M 30,a[60:63],62
L 3
v_fma_f32 v108,v108,v99,v98
v_fma_f32 v109,v109,v99,v98
M 30,a[68:71],66
s_add_u32 s56,s55,20480
R 86,2,13312
v_fma_f32 v110,v110,v99,v98
M 30,a[76:79],70
buffer_load_dwordx4 v[126:129],v0,s[36:39],s74 offen offset:0 sc1
M 26,a[64:67],66
s_cmp_eq_u32 s79,127
R 90,2,14336
v_fma_f32 v111,v111,v99,v98
M 26,a[72:75],70
L 3
buffer_load_dwordx2 v[102:103],v11,s[36:39],s73 offen offset:8 sc1
M 26,a[80:83],74
s_waitcnt vmcnt(10)
R 94,2,15360
v_fma_f32 v112,v112,v99,v98
M 26,a[88:91],78
s_barrier
v_fma_f32 v113,v113,v99,v98
v_add_f32 v18,v18,v106
M 30,a[84:87],74
s_cselect_b32 s94,0xcc00,s94
R 34,2,16384
v_add_f32 v19,v19,v107
M 30,a[92:95],78
L 3
v_add_f32 v20,v20,v108
v_add_f32 v21,v21,v109
M 30,a[100:103],82
s_mov_b32 s67,s65
R 38,2,17408
v_add_f32 v22,v22,v110
M 30,a[108:111],86
s_mov_b32 s77,s75
v_cndmask_b32 v17,v10,v9,s[40:41]
v_add_f32 v23,v23,v111
M 26,a[96:99],82
s_add_u32 s73,s73,s64
R 42,2,18432
v_add_f32 v24,v24,v112
M 26,a[104:107],86
L 3
D 17,56,0
M 26,a[112:115],90
s_add_u32 s56,s55,20736
R 46,2,19456
v_add_f32 v25,v25,v113
M 26,a[120:123],94
s_add_u32 s74,s74,s62
v_cvt_pk_bf16_f32 v114,v18,v19
v_cvt_pk_bf16_f32 v115,v20,v21
M 30,a[116:119],90
s_add_u32 s65,s65,s62
R 50,2,20480
v_fma_f32 v107,v18,v18,s87
M 30,a[124:127],94
L 3
v_add_f32 v106,v18,v19
v_fma_f32 v107,v19,v19,v107
M 30,a[132:135],34
s_add_u32 s75,s75,s63
R 54,2,21504
v_add_f32 v106,v106,v20
M 30,a[140:143],38
D 17,56,1024
M 26,a[128:131],34
s_add_u32 s56,s55,20992
R 58,2,22528
v_fma_f32 v107,v20,v20,v107
M 26,a[136:139],38
L 3
v_add_f32 v106,v106,v21
v_fma_f32 v107,v21,v21,v107
M 26,a[144:147],42
s_sub_u32 s57,s84,s57
R 62,2,23552
v_add_f32 v106,v106,v22
M 26,a[152:155],46
v_fma_f32 v107,v22,v22,v107
v_add_f32 v106,v106,v23
M 30,a[148:151],42
R 66,2,24576
v_fma_f32 v107,v23,v23,v107
M 30,a[156:159],46
L 3
v_add_f32 v106,v106,v24
v_fma_f32 v107,v24,v24,v107
M 30,a[164:167],50
R 70,2,25600
v_add_f32 v106,v106,v25
M 30,a[172:175],54
v_fma_f32 v107,v25,v25,v107
v_cvt_pk_bf16_f32 v116,v22,v23
M 26,a[160:163],50
R 74,2,26624
v_cvt_pk_bf16_f32 v117,v24,v25
M 26,a[168:171],54
L 3
v_permlane16_swap_b32 v106,v107
v_add_f32 v106,v106,v107
v_mov_b32 v107,v106
M 26,a[176:179],58
R 78,2,27648
M 26,a[184:187],62
v_permlane32_swap_b32 v106,v107
v_add_f32 v106,v106,v107
M 30,a[180:183],58
R 82,2,28672
ds_write_b32 v12,v106 offset:0
M 30,a[188:191],62
L 4
D 17,56,2048
M 30,a[196:199],66
s_add_u32 s56,s55,21248
s_add_u32 s55,s55,s60
R 86,2,29696
M 30,a[204:207],70
D 17,56,3072
M 26,a[192:195],66
R 90,2,30720
M 26,a[200:203],70
L 4
L 2
buffer_store_dwordx4 v[114:117],v0,s[36:39],s66 offen offset:0 sc0 sc1
M 26,a[208:211],74
s_waitcnt vmcnt(7)
R 94,2,31744
M 26,a[216:219],78
s_barrier
ds_read_b32 v7,v6 offset:1024
R 118,13,0
M 30,a[212:215],74
R 34,1,0
M 30,a[220:223],78
L 5
buffer_store_dword v4,v3,s[36:39],s70 offen offset:0 sc0 sc1
M 30,a[228:231],82
s_mov_b32 s70,s69
s_mov_b32 s69,s68
R 38,1,1024
M 30,a[236:239],86
s_add_u32 s68,s68,s63
M 26,a[224:227],82
R 42,1,2048
M 26,a[232:235],86
L 5
L 4
v_cmp_eq_u32 vcc,s53,v7
M 26,a[240:243],90
L 3
R 46,1,3072
v_add_f32 v118,v118,v119
M 26,a[248:251],94
s_or_b64 vcc,vcc,s[94:95]
v_add_f32 v120,v120,v121
v_add_f32 v118,v118,v120
M 30,a[244:247],90
s_cmp_eq_u32 vcc_lo,-1
R 50,1,4096
v_mul_f32 v118,s86,v118
M 30,a[252:255],94
s_waitcnt vmcnt(6) lgkmcnt(3)
buffer_store_dword v118,v14,s[36:39],s76 offen offset:0 sc0 sc1
s_cbranch_scc1 Lg2_F0_%=
Lg2_K0_%=:
Lg2_L0_%=:
v_mfma_f32_16x16x32_bf16 v[22:25],a[4:7],v[34:37],0
s_add_u32 m0,s59,3072
R 54,1,5120
v_lshlrev_b32 v106,16,v126
M 22,a[12:15],38
buffer_load_dword v8,s[36:39],s58 offen offset:0 sc1 lds
v_mfma_f32_16x16x32_bf16 v[18:21],a[0:3],v[34:37],0
s_mov_b32 s66,s65
R 58,1,6144
v_and_b32 v107,0xffff0000,v126
M 18,a[8:11],38
L 3
v_lshlrev_b32 v108,16,v127
v_and_b32 v109,0xffff0000,v127
M 18,a[16:19],42
s_mov_b32 s76,s75
R 62,1,7168
v_lshlrev_b32 v110,16,v128
M 18,a[24:27],46
s_add_u32 s65,s65,s62
v_and_b32 v111,0xffff0000,v128
v_lshlrev_b32 v112,16,v129
M 22,a[20:23],42
s_add_u32 s75,s75,s63
R 66,1,8192
v_and_b32 v113,0xffff0000,v129
v_fma_f32 v106,v106,v103,v102
M 22,a[28:31],46
L 3
v_fma_f32 v107,v107,v103,v102
v_fma_f32 v108,v108,v103,v102
M 22,a[36:39],50
R 70,1,9216
v_fma_f32 v109,v109,v103,v102
M 22,a[44:47],54
v_fma_f32 v110,v110,v103,v102
v_fma_f32 v111,v111,v103,v102
M 18,a[32:35],50
R 74,1,10240
v_fma_f32 v112,v112,v103,v102
M 18,a[40:43],54
L 3
v_fma_f32 v113,v113,v103,v102
v_add_f32 v26,v26,v106
M 18,a[48:51],58
R 78,1,11264
v_add_f32 v27,v27,v107
M 18,a[56:59],62
v_add_f32 v28,v28,v108
v_add_f32 v29,v29,v109
v_add_f32 v30,v30,v110
M 22,a[52:55],58
R 82,1,12288
v_add_f32 v31,v31,v111
M 22,a[60:63],62
L 3
v_add_f32 v32,v32,v112
v_add_f32 v33,v33,v113
M 22,a[68:71],66
R 86,1,13312
v_cvt_pk_bf16_f32 v114,v26,v27
M 22,a[76:79],70
buffer_load_dwordx4 v[122:125],v0,s[36:39],s74 offen offset:0 sc1
M 18,a[64:67],66
s_add_u32 s74,s74,s62
R 90,1,14336
v_cvt_pk_bf16_f32 v115,v28,v29
M 18,a[72:75],70
L 3
buffer_load_dwordx2 v[98:99],v11,s[36:39],s73 offen offset:8 sc1
v_cvt_pk_bf16_f32 v116,v30,v31
M 18,a[80:83],74
s_waitcnt vmcnt(2)
s_add_u32 s73,s73,s64
R 94,1,15360
M 18,a[88:91],78
s_barrier
ds_read_b32 v7,v6 offset:3072
v_cvt_pk_bf16_f32 v117,v32,v33
M 22,a[84:87],74
R 34,1,16384
v_add_f32 v106,v26,v27
M 22,a[92:95],78
L 4
buffer_store_dword v4,v3,s[36:39],s70 offen offset:0 sc0 sc1
M 22,a[100:103],82
s_mov_b32 s70,0x7fff0000
R 38,1,17408
v_fma_f32 v107,v26,v26,s87
M 22,a[108:111],86
buffer_store_dwordx4 v[114:117],v0,s[36:39],s67 offen offset:0 sc0 sc1
v_fma_f32 v107,v27,v27,v107
M 18,a[96:99],82
R 42,1,18432
v_add_f32 v106,v106,v28
v_fma_f32 v107,v28,v28,v107
M 18,a[104:107],86
L 4
L 3
v_cmp_eq_u32 vcc,s53,v7
M 18,a[112:115],90
R 46,1,19456
v_add_f32 v106,v106,v29
M 18,a[120:123],94
s_or_b64 vcc,vcc,s[94:95]
v_fma_f32 v107,v29,v29,v107
v_add_f32 v106,v106,v30
M 22,a[116:119],90
R 50,1,20480
v_fma_f32 v107,v30,v30,v107
M 22,a[124:127],94
s_cmp_eq_u32 vcc_lo,-1
L 3
v_add_f32 v106,v106,v31
s_cbranch_scc0 Lg2_X0_%=
M 22,a[132:135],34
s_and_b32 s78,s54,127
s_cmp_eq_u32 s78,s85
s_cselect_b64 s[40:41],-1,0
R 54,1,21504
M 22,a[140:143],38
s_add_u32 m0,s57,0
s_add_u32 s56,s55,0
s_add_u32 s58,s58,s61
v_cndmask_b32 v17,v15,v16,s[40:41]
M 18,a[128:131],34
s_add_u32 s54,s54,s46
R 58,1,22528
v_fma_f32 v107,v31,v31,v107
M 18,a[136:139],38
L 3
D 17,56,0
M 18,a[144:147],42
s_add_u32 s56,s55,256
R 62,1,23552
v_add_f32 v106,v106,v32
M 18,a[152:155],46
s_and_b32 s79,s54,127
v_fma_f32 v107,v32,v32,v107
v_add_f32 v106,v106,v33
M 22,a[148:151],42
s_cmp_eq_u32 s79,0
R 66,1,24576
v_fma_f32 v107,v33,v33,v107
M 22,a[156:159],46
L 3
D 17,56,1024
M 22,a[164:167],50
s_cselect_b32 s94,0x3300,0
s_add_u32 s56,s55,512
R 70,1,25600
M 22,a[172:175],54
s_cmp_eq_u32 s79,127
v_permlane16_swap_b32 v106,v107
v_add_f32 v106,v106,v107
M 18,a[160:163],50
s_cselect_b32 s94,0xcc00,s94
)"
R"(R 74,1,26624
v_mov_b32 v107,v106
M 18,a[168:171],54
L 3
D 17,56,2048
M 18,a[176:179],58
s_add_u32 s56,s55,768
R 78,1,27648
M 18,a[184:187],62
v_permlane32_swap_b32 v106,v107
v_add_f32 v106,v106,v107
M 22,a[180:183],58
R 82,1,28672
ds_write_b32 v12,v106 offset:2048
M 22,a[188:191],62
L 4
D 17,56,3072
M 22,a[196:199],66
s_add_u32 m0,s59,0
s_add_u32 s56,s55,20480
R 86,1,29696
M 22,a[204:207],70
buffer_load_dword v8,s[36:39],s58 offen offset:0 sc1 lds
M 18,a[192:195],66
s_add_u32 m0,s57,16384
R 90,1,30720
v_cndmask_b32 v17,v10,v9,s[40:41]
M 18,a[200:203],70
L 4
L 2
s_sub_u32 s57,s84,s57
M 18,a[208:211],74
s_waitcnt vmcnt(0)
R 94,1,31744
M 18,a[216:219],78
s_barrier
D 17,56,0
M 22,a[212:215],74
s_add_u32 s56,s55,20736
R 34,2,0
ds_read_b32 v7,v6 offset:0
M 22,a[220:223],78
L 4
buffer_store_dword v4,v3,s[36:39],s70 offen offset:0 sc0 sc1
M 22,a[228:231],82
s_mov_b32 s70,s69
R 38,2,1024
R 118,13,2048
M 22,a[236:239],86
D 17,56,1024
M 18,a[224:227],82
s_add_u32 s56,s55,20992
s_mov_b32 s69,s68
R 42,2,2048
M 18,a[232:235],86
L 5
D 17,56,2048
M 18,a[240:243],90
L 3
s_add_u32 s56,s55,21248
R 46,2,3072
v_cmp_eq_u32 vcc,s53,v7
M 18,a[248:251],94
D 17,56,3072
M 22,a[244:247],90
s_add_u32 s55,s55,s60
s_add_u32 s68,s68,s63
L 2
s_or_b64 vcc,vcc,s[94:95]
s_cmp_eq_u32 vcc_lo,-1
R 50,2,4096
v_add_f32 v118,v118,v119
v_add_f32 v120,v120,v121
v_add_f32 v118,v118,v120
v_mul_f32 v118,s86,v118
M 22,a[252:255],94
s_nop 0
buffer_store_dword v118,v14,s[36:39],s77 offen offset:0 sc0 sc1
buffer_store_dword v4,v3,s[36:39],s71 offen offset:0 sc0 sc1
buffer_store_dword v4,v3,s[36:39],s71 offen offset:0 sc0 sc1
s_cbranch_scc1 Lg2_F1_%=
s_branch Lg2_K1_%=
Lg2_X0_%=:
M 22,a[132:135],34
s_add_u32 m0,s59,0
R 54,1,21504
v_fma_f32 v107,v31,v31,v107
M 22,a[140:143],38
buffer_load_dword v8,s[36:39],s58 offen offset:0 sc1 lds
M 18,a[128:131],34
R 58,1,22528
v_add_f32 v106,v106,v32
M 18,a[136:139],38
L 3
v_fma_f32 v107,v32,v32,v107
v_add_f32 v106,v106,v33
M 18,a[144:147],42
R 62,1,23552
v_fma_f32 v107,v33,v33,v107
M 18,a[152:155],46
M 22,a[148:151],42
R 66,1,24576
M 22,a[156:159],46
L 3
v_permlane16_swap_b32 v106,v107
v_add_f32 v106,v106,v107
M 22,a[164:167],50
R 70,1,25600
v_mov_b32 v107,v106
M 22,a[172:175],54
M 18,a[160:163],50
R 74,1,26624
M 18,a[168:171],54
L 3
v_permlane32_swap_b32 v106,v107
M 18,a[176:179],58
R 78,1,27648
v_add_f32 v106,v106,v107
M 18,a[184:187],62
ds_write_b32 v12,v106 offset:2048
M 22,a[180:183],58
R 82,1,28672
M 22,a[188:191],62
L 4
M 22,a[196:199],66
R 86,1,29696
M 22,a[204:207],70
M 18,a[192:195],66
R 90,1,30720
M 18,a[200:203],70
L 4
L 3
M 18,a[208:211],74
s_waitcnt vmcnt(0)
R 94,1,31744
M 18,a[216:219],78
s_barrier
R 118,13,2048
ds_read_b32 v7,v6 offset:0
M 22,a[212:215],74
buffer_store_dword v4,v3,s[36:39],s70 offen offset:0 sc0 sc1
M 22,a[220:223],78
L 4
s_mov_b32 s70,s69
s_mov_b32 s69,s68
M 22,a[228:231],82
s_add_u32 s68,s68,s63
M 22,a[236:239],86
M 18,a[224:227],82
M 18,a[232:235],86
L 2
L 1
L 0
M 18,a[240:243],90
v_add_f32 v118,v118,v119
v_add_f32 v120,v120,v121
v_add_f32 v118,v118,v120
M 18,a[248:251],94
v_cmp_eq_u32 vcc,s53,v7
v_mul_f32 v118,s86,v118
M 22,a[244:247],90
buffer_store_dword v118,v14,s[36:39],s77 offen offset:0 sc0 sc1
M 22,a[252:255],94
s_nop 0
s_or_b64 vcc,vcc,s[94:95]
s_mov_b32 s80,1
s_cmp_eq_u32 vcc_lo,-1
s_cbranch_scc0 Lg2_xn0_%=
s_and_b32 s78,s54,127
s_cmp_eq_u32 s78,s85
s_cselect_b64 s[40:41],-1,0
v_cndmask_b32 v17,v15,v16,s[40:41]
s_add_u32 m0,s57,0
s_add_u32 s56,s55,0
D 17,56,0
v_lshlrev_b32 v106,16,v122
v_and_b32 v107,0xffff0000,v122
v_lshlrev_b32 v108,16,v123
v_and_b32 v109,0xffff0000,v123
v_lshlrev_b32 v110,16,v124
s_add_u32 s56,s55,256
D 17,56,1024
v_and_b32 v111,0xffff0000,v124
v_lshlrev_b32 v112,16,v125
v_and_b32 v113,0xffff0000,v125
v_fma_f32 v106,v106,v99,v98
v_fma_f32 v107,v107,v99,v98
s_add_u32 s56,s55,512
D 17,56,2048
v_fma_f32 v108,v108,v99,v98
v_fma_f32 v109,v109,v99,v98
v_fma_f32 v110,v110,v99,v98
v_fma_f32 v111,v111,v99,v98
v_fma_f32 v112,v112,v99,v98
s_add_u32 s56,s55,768
D 17,56,3072
v_fma_f32 v113,v113,v99,v98
v_add_f32 v18,v18,v106
v_add_f32 v19,v19,v107
v_add_f32 v20,v20,v108
v_add_f32 v21,v21,v109
s_add_u32 s58,s58,s61
v_cndmask_b32 v17,v10,v9,s[40:41]
s_add_u32 m0,s57,16384
s_add_u32 s56,s55,20480
D 17,56,0
v_add_f32 v22,v22,v110
v_add_f32 v23,v23,v111
v_add_f32 v24,v24,v112
v_add_f32 v25,v25,v113
v_cvt_pk_bf16_f32 v114,v18,v19
s_add_u32 s56,s55,20736
D 17,56,1024
v_cvt_pk_bf16_f32 v115,v20,v21
v_cvt_pk_bf16_f32 v116,v22,v23
v_cvt_pk_bf16_f32 v117,v24,v25
s_add_u32 s56,s55,20992
D 17,56,2048
s_add_u32 s56,s55,21248
D 17,56,3072
buffer_store_dwordx4 v[114:117],v0,s[36:39],s66 offen offset:0 sc0 sc1
v_add_f32 v106,v18,v19
v_fma_f32 v107,v18,v18,s87
s_add_u32 s55,s55,s60
s_sub_u32 s57,s84,s57
s_add_u32 s54,s54,s46
v_fma_f32 v107,v19,v19,v107
v_add_f32 v106,v106,v20
v_fma_f32 v107,v20,v20,v107
v_add_f32 v106,v106,v21
v_fma_f32 v107,v21,v21,v107
v_add_f32 v106,v106,v22
v_fma_f32 v107,v22,v22,v107
v_add_f32 v106,v106,v23
v_fma_f32 v107,v23,v23,v107
v_add_f32 v106,v106,v24
v_fma_f32 v107,v24,v24,v107
v_add_f32 v106,v106,v25
v_fma_f32 v107,v25,v25,v107
s_nop 1
v_permlane16_swap_b32 v106,v107
v_add_f32 v106,v106,v107
v_mov_b32 v107,v106
s_nop 1
v_permlane32_swap_b32 v106,v107
v_add_f32 v106,v106,v107
ds_write_b32 v12,v106 offset:0
L 0
s_barrier
R 118,13,0
L 0
v_add_f32 v118,v118,v119
v_add_f32 v120,v120,v121
v_add_f32 v118,v118,v120
v_mul_f32 v118,s86,v118
buffer_store_dword v118,v14,s[36:39],s76 offen offset:0 sc0 sc1
s_mov_b32 s72,s69
s_mov_b32 s69,0x7fff0000
s_mov_b32 s66,0x7fff0000
s_mov_b32 s76,0x7fff0000
s_branch Lg2_gox_%=
Lg2_xn0_%=:
s_cmp_ge_u32 s54,256
s_cbranch_scc1 Lg2_fin0_%=
s_add_u32 m0,s59,4096
s_nop 0
buffer_load_dword v8,s[36:39],s58 offen offset:0 sc1 lds
buffer_store_dword v4,v3,s[36:39],s71 offen offset:0 sc0 sc1
s_add_u32 m0,s59,5120
s_nop 0
buffer_load_dword v8,s[36:39],s58 offen offset:0 sc1 lds
buffer_store_dword v4,v3,s[36:39],s71 offen offset:0 sc0 sc1
s_mov_b32 s83,0
v_lshlrev_b32 v106,16,v122
v_and_b32 v107,0xffff0000,v122
v_lshlrev_b32 v108,16,v123
v_and_b32 v109,0xffff0000,v123
v_lshlrev_b32 v110,16,v124
v_and_b32 v111,0xffff0000,v124
v_lshlrev_b32 v112,16,v125
v_and_b32 v113,0xffff0000,v125
v_fma_f32 v106,v106,v99,v98
v_fma_f32 v107,v107,v99,v98
v_fma_f32 v108,v108,v99,v98
v_fma_f32 v109,v109,v99,v98
v_fma_f32 v110,v110,v99,v98
v_fma_f32 v111,v111,v99,v98
v_fma_f32 v112,v112,v99,v98
v_fma_f32 v113,v113,v99,v98
v_add_f32 v18,v18,v106
v_add_f32 v19,v19,v107
v_add_f32 v20,v20,v108
v_add_f32 v21,v21,v109
v_add_f32 v22,v22,v110
v_add_f32 v23,v23,v111
v_add_f32 v24,v24,v112
v_add_f32 v25,v25,v113
v_cvt_pk_bf16_f32 v114,v18,v19
v_cvt_pk_bf16_f32 v115,v20,v21
v_cvt_pk_bf16_f32 v116,v22,v23
v_cvt_pk_bf16_f32 v117,v24,v25
buffer_store_dwordx4 v[114:117],v0,s[36:39],s66 offen offset:0 sc0 sc1
v_add_f32 v106,v18,v19
v_fma_f32 v107,v18,v18,s87
v_fma_f32 v107,v19,v19,v107
v_add_f32 v106,v106,v20
v_fma_f32 v107,v20,v20,v107
v_add_f32 v106,v106,v21
v_fma_f32 v107,v21,v21,v107
v_add_f32 v106,v106,v22
v_fma_f32 v107,v22,v22,v107
v_add_f32 v106,v106,v23
v_fma_f32 v107,v23,v23,v107
v_add_f32 v106,v106,v24
v_fma_f32 v107,v24,v24,v107
v_add_f32 v106,v106,v25
v_fma_f32 v107,v25,v25,v107
s_nop 1
v_permlane16_swap_b32 v106,v107
v_add_f32 v106,v106,v107
v_mov_b32 v107,v106
s_nop 1
v_permlane32_swap_b32 v106,v107
v_add_f32 v106,v106,v107
ds_write_b32 v12,v106 offset:0
L 0
s_barrier
R 118,13,0
L 0
v_add_f32 v118,v118,v119
v_add_f32 v120,v120,v121
v_add_f32 v118,v118,v120
v_mul_f32 v118,s86,v118
buffer_store_dword v118,v14,s[36:39],s76 offen offset:0 sc0 sc1
s_mov_b32 s72,s69
s_mov_b32 s69,0x7fff0000
s_mov_b32 s66,0x7fff0000
s_mov_b32 s76,0x7fff0000
s_branch Lg2_poll_%=
Lg2_fin0_%=:
v_lshlrev_b32 v106,16,v122
v_and_b32 v107,0xffff0000,v122
v_lshlrev_b32 v108,16,v123
v_and_b32 v109,0xffff0000,v123
v_lshlrev_b32 v110,16,v124
v_and_b32 v111,0xffff0000,v124
v_lshlrev_b32 v112,16,v125
v_and_b32 v113,0xffff0000,v125
v_fma_f32 v106,v106,v99,v98
v_fma_f32 v107,v107,v99,v98
v_fma_f32 v108,v108,v99,v98
v_fma_f32 v109,v109,v99,v98
v_fma_f32 v110,v110,v99,v98
v_fma_f32 v111,v111,v99,v98
v_fma_f32 v112,v112,v99,v98
v_fma_f32 v113,v113,v99,v98
v_add_f32 v18,v18,v106
v_add_f32 v19,v19,v107
v_add_f32 v20,v20,v108
v_add_f32 v21,v21,v109
v_add_f32 v22,v22,v110
v_add_f32 v23,v23,v111
v_add_f32 v24,v24,v112
v_add_f32 v25,v25,v113
v_cvt_pk_bf16_f32 v114,v18,v19
v_cvt_pk_bf16_f32 v115,v20,v21
v_cvt_pk_bf16_f32 v116,v22,v23
v_cvt_pk_bf16_f32 v117,v24,v25
buffer_store_dwordx4 v[114:117],v0,s[36:39],s66 offen offset:0 sc0 sc1
v_add_f32 v106,v18,v19
v_fma_f32 v107,v18,v18,s87
v_fma_f32 v107,v19,v19,v107
v_add_f32 v106,v106,v20
v_fma_f32 v107,v20,v20,v107
v_add_f32 v106,v106,v21
v_fma_f32 v107,v21,v21,v107
v_add_f32 v106,v106,v22
v_fma_f32 v107,v22,v22,v107
v_add_f32 v106,v106,v23
v_fma_f32 v107,v23,v23,v107
v_add_f32 v106,v106,v24
v_fma_f32 v107,v24,v24,v107
v_add_f32 v106,v106,v25
v_fma_f32 v107,v25,v25,v107
s_nop 1
v_permlane16_swap_b32 v106,v107
v_add_f32 v106,v106,v107
v_mov_b32 v107,v106
s_nop 1
v_permlane32_swap_b32 v106,v107
v_add_f32 v106,v106,v107
ds_write_b32 v12,v106 offset:0
L 0
s_barrier
R 118,13,0
L 0
v_add_f32 v118,v118,v119
v_add_f32 v120,v120,v121
v_add_f32 v118,v118,v120
v_mul_f32 v118,s86,v118
buffer_store_dword v118,v14,s[36:39],s76 offen offset:0 sc0 sc1
s_mov_b32 s72,s69
s_mov_b32 s69,0x7fff0000
s_mov_b32 s66,0x7fff0000
s_mov_b32 s76,0x7fff0000
s_branch Lg2_done_%=
Lg2_K1_%=:
Lg2_L1_%=:
v_mfma_f32_16x16x32_bf16 v[30:33],a[4:7],v[34:37],0
s_add_u32 m0,s59,3072
R 54,2,5120
v_lshlrev_b32 v106,16,v122
M 30,a[12:15],38
buffer_load_dword v8,s[36:39],s58 offen offset:0 sc1 lds
v_mfma_f32_16x16x32_bf16 v[26:29],a[0:3],v[34:37],0
s_mov_b32 s67,s65
R 58,2,6144
v_and_b32 v107,0xffff0000,v122
M 26,a[8:11],38
L 3
v_lshlrev_b32 v108,16,v123
v_and_b32 v109,0xffff0000,v123
M 26,a[16:19],42
s_mov_b32 s77,s75
R 62,2,7168
v_lshlrev_b32 v110,16,v124
M 26,a[24:27],46
s_add_u32 s65,s65,s62
v_and_b32 v111,0xffff0000,v124
v_lshlrev_b32 v112,16,v125
M 30,a[20:23],42
s_add_u32 s75,s75,s63
R 66,2,8192
v_and_b32 v113,0xffff0000,v125
v_fma_f32 v106,v106,v99,v98
M 30,a[28:31],46
L 3
v_fma_f32 v107,v107,v99,v98
v_fma_f32 v108,v108,v99,v98
M 30,a[36:39],50
R 70,2,9216
v_fma_f32 v109,v109,v99,v98
M 30,a[44:47],54
v_fma_f32 v110,v110,v99,v98
v_fma_f32 v111,v111,v99,v98
M 26,a[32:35],50
R 74,2,10240
v_fma_f32 v112,v112,v99,v98
M 26,a[40:43],54
L 3
v_fma_f32 v113,v113,v99,v98
v_add_f32 v18,v18,v106
M 26,a[48:51],58
R 78,2,11264
v_add_f32 v19,v19,v107
M 26,a[56:59],62
v_add_f32 v20,v20,v108
v_add_f32 v21,v21,v109
v_add_f32 v22,v22,v110
M 30,a[52:55],58
R 82,2,12288
v_add_f32 v23,v23,v111
M 30,a[60:63],62
L 3
v_add_f32 v24,v24,v112
v_add_f32 v25,v25,v113
M 30,a[68:71],66
R 86,2,13312
v_cvt_pk_bf16_f32 v114,v18,v19
M 30,a[76:79],70
buffer_load_dwordx4 v[126:129],v0,s[36:39],s74 offen offset:0 sc1
M 26,a[64:67],66
s_add_u32 s74,s74,s62
R 90,2,14336
v_cvt_pk_bf16_f32 v115,v20,v21
M 26,a[72:75],70
L 3
buffer_load_dwordx2 v[102:103],v11,s[36:39],s73 offen offset:8 sc1
v_cvt_pk_bf16_f32 v116,v22,v23
M 26,a[80:83],74
s_waitcnt vmcnt(2)
s_add_u32 s73,s73,s64
R 94,2,15360
M 26,a[88:91],78
s_barrier
ds_read_b32 v7,v6 offset:3072
v_cvt_pk_bf16_f32 v117,v24,v25
M 30,a[84:87],74
R 34,2,16384
v_add_f32 v106,v18,v19
M 30,a[92:95],78
L 4
buffer_store_dword v4,v3,s[36:39],s70 offen offset:0 sc0 sc1
M 30,a[100:103],82
s_mov_b32 s70,0x7fff0000
R 38,2,17408
v_fma_f32 v107,v18,v18,s87
M 30,a[108:111],86
buffer_store_dwordx4 v[114:117],v0,s[36:39],s66 offen offset:0 sc0 sc1
v_fma_f32 v107,v19,v19,v107
)"
R"(M 26,a[96:99],82
R 42,2,18432
v_add_f32 v106,v106,v20
v_fma_f32 v107,v20,v20,v107
M 26,a[104:107],86
L 4
L 3
v_cmp_eq_u32 vcc,s53,v7
M 26,a[112:115],90
R 46,2,19456
v_add_f32 v106,v106,v21
M 26,a[120:123],94
s_or_b64 vcc,vcc,s[94:95]
v_fma_f32 v107,v21,v21,v107
v_add_f32 v106,v106,v22
M 30,a[116:119],90
R 50,2,20480
v_fma_f32 v107,v22,v22,v107
M 30,a[124:127],94
s_cmp_eq_u32 vcc_lo,-1
L 3
v_add_f32 v106,v106,v23
s_cbranch_scc0 Lg2_X1_%=
M 30,a[132:135],34
s_and_b32 s78,s54,127
s_cmp_eq_u32 s78,s85
s_cselect_b64 s[40:41],-1,0
R 54,2,21504
M 30,a[140:143],38
s_add_u32 m0,s57,0
s_add_u32 s56,s55,0
s_add_u32 s58,s58,s61
v_cndmask_b32 v17,v15,v16,s[40:41]
M 26,a[128:131],34
s_add_u32 s54,s54,s46
R 58,2,22528
v_fma_f32 v107,v23,v23,v107
M 26,a[136:139],38
L 3
D 17,56,0
M 26,a[144:147],42
s_add_u32 s56,s55,256
R 62,2,23552
v_add_f32 v106,v106,v24
M 26,a[152:155],46
s_and_b32 s79,s54,127
v_fma_f32 v107,v24,v24,v107
v_add_f32 v106,v106,v25
M 30,a[148:151],42
s_cmp_eq_u32 s79,0
R 66,2,24576
v_fma_f32 v107,v25,v25,v107
M 30,a[156:159],46
L 3
D 17,56,1024
M 30,a[164:167],50
s_cselect_b32 s94,0x3300,0
s_add_u32 s56,s55,512
R 70,2,25600
M 30,a[172:175],54
s_cmp_eq_u32 s79,127
v_permlane16_swap_b32 v106,v107
v_add_f32 v106,v106,v107
M 26,a[160:163],50
s_cselect_b32 s94,0xcc00,s94
R 74,2,26624
v_mov_b32 v107,v106
M 26,a[168:171],54
L 3
D 17,56,2048
M 26,a[176:179],58
s_add_u32 s56,s55,768
R 78,2,27648
M 26,a[184:187],62
v_permlane32_swap_b32 v106,v107
v_add_f32 v106,v106,v107
M 30,a[180:183],58
R 82,2,28672
ds_write_b32 v12,v106 offset:0
M 30,a[188:191],62
L 4
D 17,56,3072
M 30,a[196:199],66
s_add_u32 m0,s59,1024
s_add_u32 s56,s55,20480
R 86,2,29696
M 30,a[204:207],70
buffer_load_dword v8,s[36:39],s58 offen offset:0 sc1 lds
M 26,a[192:195],66
s_add_u32 m0,s57,16384
R 90,2,30720
v_cndmask_b32 v17,v10,v9,s[40:41]
M 26,a[200:203],70
L 4
L 2
s_sub_u32 s57,s84,s57
M 26,a[208:211],74
s_waitcnt vmcnt(0)
R 94,2,31744
M 26,a[216:219],78
s_barrier
D 17,56,0
M 30,a[212:215],74
s_add_u32 s56,s55,20736
R 34,1,0
ds_read_b32 v7,v6 offset:1024
M 30,a[220:223],78
L 4
buffer_store_dword v4,v3,s[36:39],s70 offen offset:0 sc0 sc1
M 30,a[228:231],82
s_mov_b32 s70,s69
R 38,1,1024
R 118,13,0
M 30,a[236:239],86
D 17,56,1024
M 26,a[224:227],82
s_add_u32 s56,s55,20992
s_mov_b32 s69,s68
R 42,1,2048
M 26,a[232:235],86
L 5
D 17,56,2048
M 26,a[240:243],90
L 3
s_add_u32 s56,s55,21248
R 46,1,3072
v_cmp_eq_u32 vcc,s53,v7
M 26,a[248:251],94
D 17,56,3072
M 30,a[244:247],90
s_add_u32 s55,s55,s60
s_add_u32 s68,s68,s63
L 2
s_or_b64 vcc,vcc,s[94:95]
s_cmp_eq_u32 vcc_lo,-1
R 50,1,4096
v_add_f32 v118,v118,v119
v_add_f32 v120,v120,v121
v_add_f32 v118,v118,v120
v_mul_f32 v118,s86,v118
M 30,a[252:255],94
s_nop 0
buffer_store_dword v118,v14,s[36:39],s76 offen offset:0 sc0 sc1
buffer_store_dword v4,v3,s[36:39],s71 offen offset:0 sc0 sc1
buffer_store_dword v4,v3,s[36:39],s71 offen offset:0 sc0 sc1
s_cbranch_scc1 Lg2_F0_%=
s_branch Lg2_K0_%=
Lg2_X1_%=:
M 30,a[132:135],34
s_add_u32 m0,s59,1024
R 54,2,21504
v_fma_f32 v107,v23,v23,v107
M 30,a[140:143],38
buffer_load_dword v8,s[36:39],s58 offen offset:0 sc1 lds
M 26,a[128:131],34
R 58,2,22528
v_add_f32 v106,v106,v24
M 26,a[136:139],38
L 3
v_fma_f32 v107,v24,v24,v107
v_add_f32 v106,v106,v25
M 26,a[144:147],42
R 62,2,23552
v_fma_f32 v107,v25,v25,v107
M 26,a[152:155],46
M 30,a[148:151],42
R 66,2,24576
M 30,a[156:159],46
L 3
v_permlane16_swap_b32 v106,v107
v_add_f32 v106,v106,v107
M 30,a[164:167],50
R 70,2,25600
v_mov_b32 v107,v106
M 30,a[172:175],54
M 26,a[160:163],50
R 74,2,26624
M 26,a[168:171],54
L 3
v_permlane32_swap_b32 v106,v107
M 26,a[176:179],58
R 78,2,27648
v_add_f32 v106,v106,v107
M 26,a[184:187],62
ds_write_b32 v12,v106 offset:0
M 30,a[180:183],58
R 82,2,28672
M 30,a[188:191],62
L 4
M 30,a[196:199],66
R 86,2,29696
M 30,a[204:207],70
M 26,a[192:195],66
R 90,2,30720
M 26,a[200:203],70
L 4
L 3
M 26,a[208:211],74
s_waitcnt vmcnt(0)
R 94,2,31744
M 26,a[216:219],78
s_barrier
R 118,13,0
ds_read_b32 v7,v6 offset:1024
M 30,a[212:215],74
buffer_store_dword v4,v3,s[36:39],s70 offen offset:0 sc0 sc1
M 30,a[220:223],78
L 4
s_mov_b32 s70,s69
s_mov_b32 s69,s68
M 30,a[228:231],82
s_add_u32 s68,s68,s63
M 30,a[236:239],86
M 26,a[224:227],82
M 26,a[232:235],86
L 2
L 1
L 0
M 26,a[240:243],90
v_add_f32 v118,v118,v119
v_add_f32 v120,v120,v121
v_add_f32 v118,v118,v120
M 26,a[248:251],94
v_cmp_eq_u32 vcc,s53,v7
v_mul_f32 v118,s86,v118
M 30,a[244:247],90
buffer_store_dword v118,v14,s[36:39],s76 offen offset:0 sc0 sc1
M 30,a[252:255],94
s_nop 0
s_or_b64 vcc,vcc,s[94:95]
s_mov_b32 s80,0
s_cmp_eq_u32 vcc_lo,-1
s_cbranch_scc0 Lg2_xn1_%=
s_and_b32 s78,s54,127
s_cmp_eq_u32 s78,s85
s_cselect_b64 s[40:41],-1,0
v_cndmask_b32 v17,v15,v16,s[40:41]
s_add_u32 m0,s57,0
s_add_u32 s56,s55,0
D 17,56,0
v_lshlrev_b32 v106,16,v126
v_and_b32 v107,0xffff0000,v126
v_lshlrev_b32 v108,16,v127
v_and_b32 v109,0xffff0000,v127
v_lshlrev_b32 v110,16,v128
s_add_u32 s56,s55,256
D 17,56,1024
v_and_b32 v111,0xffff0000,v128
v_lshlrev_b32 v112,16,v129
v_and_b32 v113,0xffff0000,v129
v_fma_f32 v106,v106,v103,v102
v_fma_f32 v107,v107,v103,v102
s_add_u32 s56,s55,512
D 17,56,2048
v_fma_f32 v108,v108,v103,v102
v_fma_f32 v109,v109,v103,v102
v_fma_f32 v110,v110,v103,v102
v_fma_f32 v111,v111,v103,v102
v_fma_f32 v112,v112,v103,v102
s_add_u32 s56,s55,768
D 17,56,3072
v_fma_f32 v113,v113,v103,v102
v_add_f32 v26,v26,v106
v_add_f32 v27,v27,v107
v_add_f32 v28,v28,v108
v_add_f32 v29,v29,v109
s_add_u32 s58,s58,s61
v_cndmask_b32 v17,v10,v9,s[40:41]
s_add_u32 m0,s57,16384
s_add_u32 s56,s55,20480
D 17,56,0
v_add_f32 v30,v30,v110
v_add_f32 v31,v31,v111
v_add_f32 v32,v32,v112
v_add_f32 v33,v33,v113
v_cvt_pk_bf16_f32 v114,v26,v27
s_add_u32 s56,s55,20736
D 17,56,1024
v_cvt_pk_bf16_f32 v115,v28,v29
v_cvt_pk_bf16_f32 v116,v30,v31
v_cvt_pk_bf16_f32 v117,v32,v33
s_add_u32 s56,s55,20992
D 17,56,2048
s_add_u32 s56,s55,21248
D 17,56,3072
buffer_store_dwordx4 v[114:117],v0,s[36:39],s67 offen offset:0 sc0 sc1
v_add_f32 v106,v26,v27
v_fma_f32 v107,v26,v26,s87
s_add_u32 s55,s55,s60
s_sub_u32 s57,s84,s57
s_add_u32 s54,s54,s46
v_fma_f32 v107,v27,v27,v107
v_add_f32 v106,v106,v28
v_fma_f32 v107,v28,v28,v107
v_add_f32 v106,v106,v29
v_fma_f32 v107,v29,v29,v107
v_add_f32 v106,v106,v30
v_fma_f32 v107,v30,v30,v107
v_add_f32 v106,v106,v31
v_fma_f32 v107,v31,v31,v107
v_add_f32 v106,v106,v32
v_fma_f32 v107,v32,v32,v107
v_add_f32 v106,v106,v33
v_fma_f32 v107,v33,v33,v107
s_nop 1
v_permlane16_swap_b32 v106,v107
v_add_f32 v106,v106,v107
v_mov_b32 v107,v106
s_nop 1
v_permlane32_swap_b32 v106,v107
v_add_f32 v106,v106,v107
ds_write_b32 v12,v106 offset:2048
L 0
s_barrier
R 118,13,2048
L 0
v_add_f32 v118,v118,v119
v_add_f32 v120,v120,v121
v_add_f32 v118,v118,v120
v_mul_f32 v118,s86,v118
buffer_store_dword v118,v14,s[36:39],s77 offen offset:0 sc0 sc1
s_mov_b32 s72,s69
s_mov_b32 s69,0x7fff0000
s_mov_b32 s67,0x7fff0000
s_mov_b32 s77,0x7fff0000
s_branch Lg2_gox_%=
Lg2_xn1_%=:
s_cmp_ge_u32 s54,256
s_cbranch_scc1 Lg2_fin1_%=
s_add_u32 m0,s59,4096
s_nop 0
buffer_load_dword v8,s[36:39],s58 offen offset:0 sc1 lds
buffer_store_dword v4,v3,s[36:39],s71 offen offset:0 sc0 sc1
s_add_u32 m0,s59,5120
s_nop 0
buffer_load_dword v8,s[36:39],s58 offen offset:0 sc1 lds
buffer_store_dword v4,v3,s[36:39],s71 offen offset:0 sc0 sc1
s_mov_b32 s83,0
v_lshlrev_b32 v106,16,v126
v_and_b32 v107,0xffff0000,v126
v_lshlrev_b32 v108,16,v127
v_and_b32 v109,0xffff0000,v127
v_lshlrev_b32 v110,16,v128
v_and_b32 v111,0xffff0000,v128
v_lshlrev_b32 v112,16,v129
v_and_b32 v113,0xffff0000,v129
v_fma_f32 v106,v106,v103,v102
v_fma_f32 v107,v107,v103,v102
v_fma_f32 v108,v108,v103,v102
v_fma_f32 v109,v109,v103,v102
v_fma_f32 v110,v110,v103,v102
v_fma_f32 v111,v111,v103,v102
v_fma_f32 v112,v112,v103,v102
v_fma_f32 v113,v113,v103,v102
v_add_f32 v26,v26,v106
v_add_f32 v27,v27,v107
v_add_f32 v28,v28,v108
v_add_f32 v29,v29,v109
v_add_f32 v30,v30,v110
v_add_f32 v31,v31,v111
v_add_f32 v32,v32,v112
v_add_f32 v33,v33,v113
v_cvt_pk_bf16_f32 v114,v26,v27
v_cvt_pk_bf16_f32 v115,v28,v29
v_cvt_pk_bf16_f32 v116,v30,v31
v_cvt_pk_bf16_f32 v117,v32,v33
buffer_store_dwordx4 v[114:117],v0,s[36:39],s67 offen offset:0 sc0 sc1
v_add_f32 v106,v26,v27
v_fma_f32 v107,v26,v26,s87
v_fma_f32 v107,v27,v27,v107
v_add_f32 v106,v106,v28
v_fma_f32 v107,v28,v28,v107
v_add_f32 v106,v106,v29
v_fma_f32 v107,v29,v29,v107
v_add_f32 v106,v106,v30
v_fma_f32 v107,v30,v30,v107
v_add_f32 v106,v106,v31
v_fma_f32 v107,v31,v31,v107
v_add_f32 v106,v106,v32
v_fma_f32 v107,v32,v32,v107
v_add_f32 v106,v106,v33
v_fma_f32 v107,v33,v33,v107
s_nop 1
v_permlane16_swap_b32 v106,v107
v_add_f32 v106,v106,v107
v_mov_b32 v107,v106
s_nop 1
v_permlane32_swap_b32 v106,v107
v_add_f32 v106,v106,v107
ds_write_b32 v12,v106 offset:2048
L 0
s_barrier
R 118,13,2048
L 0
v_add_f32 v118,v118,v119
v_add_f32 v120,v120,v121
v_add_f32 v118,v118,v120
v_mul_f32 v118,s86,v118
buffer_store_dword v118,v14,s[36:39],s77 offen offset:0 sc0 sc1
s_mov_b32 s72,s69
s_mov_b32 s69,0x7fff0000
s_mov_b32 s67,0x7fff0000
s_mov_b32 s77,0x7fff0000
s_branch Lg2_poll_%=
Lg2_fin1_%=:
v_lshlrev_b32 v106,16,v126
v_and_b32 v107,0xffff0000,v126
v_lshlrev_b32 v108,16,v127
v_and_b32 v109,0xffff0000,v127
v_lshlrev_b32 v110,16,v128
v_and_b32 v111,0xffff0000,v128
v_lshlrev_b32 v112,16,v129
v_and_b32 v113,0xffff0000,v129
v_fma_f32 v106,v106,v103,v102
v_fma_f32 v107,v107,v103,v102
v_fma_f32 v108,v108,v103,v102
v_fma_f32 v109,v109,v103,v102
v_fma_f32 v110,v110,v103,v102
v_fma_f32 v111,v111,v103,v102
v_fma_f32 v112,v112,v103,v102
v_fma_f32 v113,v113,v103,v102
v_add_f32 v26,v26,v106
v_add_f32 v27,v27,v107
v_add_f32 v28,v28,v108
v_add_f32 v29,v29,v109
v_add_f32 v30,v30,v110
v_add_f32 v31,v31,v111
v_add_f32 v32,v32,v112
v_add_f32 v33,v33,v113
v_cvt_pk_bf16_f32 v114,v26,v27
v_cvt_pk_bf16_f32 v115,v28,v29
v_cvt_pk_bf16_f32 v116,v30,v31
v_cvt_pk_bf16_f32 v117,v32,v33
buffer_store_dwordx4 v[114:117],v0,s[36:39],s67 offen offset:0 sc0 sc1
v_add_f32 v106,v26,v27
v_fma_f32 v107,v26,v26,s87
v_fma_f32 v107,v27,v27,v107
v_add_f32 v106,v106,v28
v_fma_f32 v107,v28,v28,v107
v_add_f32 v106,v106,v29
v_fma_f32 v107,v29,v29,v107
v_add_f32 v106,v106,v30
v_fma_f32 v107,v30,v30,v107
v_add_f32 v106,v106,v31
v_fma_f32 v107,v31,v31,v107
v_add_f32 v106,v106,v32
v_fma_f32 v107,v32,v32,v107
v_add_f32 v106,v106,v33
v_fma_f32 v107,v33,v33,v107
s_nop 1
v_permlane16_swap_b32 v106,v107
v_add_f32 v106,v106,v107
v_mov_b32 v107,v106
s_nop 1
v_permlane32_swap_b32 v106,v107
v_add_f32 v106,v106,v107
ds_write_b32 v12,v106 offset:2048
L 0
s_barrier
R 118,13,2048
L 0
v_add_f32 v118,v118,v119
v_add_f32 v120,v120,v121
v_add_f32 v118,v118,v120
v_mul_f32 v118,s86,v118
buffer_store_dword v118,v14,s[36:39],s77 offen offset:0 sc0 sc1
s_mov_b32 s72,s69
s_mov_b32 s69,0x7fff0000
s_mov_b32 s67,0x7fff0000
s_mov_b32 s77,0x7fff0000
s_branch Lg2_done_%=
Lg2_done_%=:
s_waitcnt vmcnt(2)
s_barrier
buffer_store_dword v4,v3,s[36:39],s70 offen offset:0 sc0 sc1
s_waitcnt vmcnt(1)
s_barrier
buffer_store_dword v4,v3,s[36:39],s72 offen offset:0 sc0 sc1
s_waitcnt vmcnt(0) lgkmcnt(0)
s_barrier
Lg2_end_%=:
s_mov_b32 %0,s53
Lcode_g12_e:
.purgem M
.purgem R
.purgem T
.purgem D
.purgem L
)"
  : "=&s"(epOut) : "s"(ws), "s"(__builtin_amdgcn_readfirstlane(sWave)), "s"(__builtin_amdgcn_readfirstlane(sG0)), "s"(__builtin_amdgcn_readfirstlane(sStep)), "s"(__builtin_amdgcn_readfirstlane(sOutOff)), "s"(__builtin_amdgcn_readfirstlane(sFoutOff)), "s"(__builtin_amdgcn_readfirstlane(sAuxOff)), "s"(__builtin_amdgcn_readfirstlane(sWw)), "s"(__builtin_amdgcn_readfirstlane(sBuf)), "s"(__builtin_amdgcn_readfirstlane(sExp)), "v"(vL16), "v"(vFlag), "v"(vSnapOff), "v"(vSnapRd), "v"(vWd0), "v"(vWd1), "v"(vWr0), "v"(vWr1), "v"(vSp), "v"(vOutS), "v"(vAux0), "v"(vAux1), "v"(vAux2), "v"(vAux3), "v"(vAn), "v"(vAe), "s"(w12) : "v0", "v1", "v2", "v3", "v4", "v5", "v6", "v7", "v8", "v9", "v10", "v11", "v12", "v13", "v14", "v15", "v16", "v17", "v18", "v19", "v20", "v21", "v22", "v23", "v24", "v25", "v26", "v27", "v28", "v29", "v30", "v31", "v32", "v33", "v34", "v35", "v36", "v37", "v38", "v39", "v40", "v41", "v42", "v43", "v44", "v45", "v46", "v47", "v48", "v49", "v50", "v51", "v52", "v53", "v54", "v55", "v56", "v57", "v58", "v59", "v60", "v61", "v62", "v63", "v64", "v65", "v66", "v67", "v68", "v69", "v70", "v71", "v72", "v73", "v74", "v75", "v76", "v77", "v78", "v79", "v80", "v81", "v82", "v83", "v84", "v85", "v86", "v87", "v88", "v89", "v90", "v91", "v92", "v93", "v94", "v95", "v96", "v97", "v98", "v99", "v100", "v101", "v102", "v103", "v104", "v105", "v106", "v107", "v108", "v109", "v110", "v111", "v112", "v113", "v114", "v115", "v116", "v117", "v118", "v119", "v120", "v121", "v122", "v123", "v124", "v125", "v126", "v127", "v128", "v129", "v130", "v131", "v132", "v133", "v134", "v135", "v136", "v137", "v138", "v139", "v140", "v141", "v142", "v143", "v144", "v145", "v146", "v147", "v148", "v149", "v150", "v151", "v152", "v153", "v154", "v155", "v156", "v157", "v158", "v159", "v160", "v161", "v162", "v163", "v164", "v165", "v166", "v167", "v168", "v169", "v170", "v171", "v172", "v173", "v174", "v175", "v176", "v177", "v178", "v179", "v180", "v181", "v182", "v183", "v184", "v185", "v186", "v187", "v188", "v189", "v190", "v191", "v192", "v193", "v194", "v195", "v196", "v197", "v198", "v199", "v200", "v201", "v202", "v203", "v204", "v205", "v206", "v207", "v208", "v209", "v210", "v211", "v212", "v213", "v214", "v215", "v216", "v217", "v218", "v219", "v220", "v221", "v222", "v223", "v224", "v225", "v226", "v227", "v228", "v229", "v230", "v231", "a0", "a1", "a2", "a3", "a4", "a5", "a6", "a7", "a8", "a9", "a10", "a11", "a12", "a13", "a14", "a15", "a16", "a17", "a18", "a19", "a20", "a21", "a22", "a23", "a24", "a25", "a26", "a27", "a28", "a29", "a30", "a31", "a32", "a33", "a34", "a35", "a36", "a37", "a38", "a39", "a40", "a41", "a42", "a43", "a44", "a45", "a46", "a47", "a48", "a49", "a50", "a51", "a52", "a53", "a54", "a55", "a56", "a57", "a58", "a59", "a60", "a61", "a62", "a63", "a64", "a65", "a66", "a67", "a68", "a69", "a70", "a71", "a72", "a73", "a74", "a75", "a76", "a77", "a78", "a79", "a80", "a81", "a82", "a83", "a84", "a85", "a86", "a87", "a88", "a89", "a90", "a91", "a92", "a93", "a94", "a95", "a96", "a97", "a98", "a99", "a100", "a101", "a102", "a103", "a104", "a105", "a106", "a107", "a108", "a109", "a110", "a111", "a112", "a113", "a114", "a115", "a116", "a117", "a118", "a119", "a120", "a121", "a122", "a123", "a124", "a125", "a126", "a127", "a128", "a129", "a130", "a131", "a132", "a133", "a134", "a135", "a136", "a137", "a138", "a139", "a140", "a141", "a142", "a143", "a144", "a145", "a146", "a147", "a148", "a149", "a150", "a151", "a152", "a153", "a154", "a155", "a156", "a157", "a158", "a159", "a160", "a161", "a162", "a163", "a164", "a165", "a166", "a167", "a168", "a169", "a170", "a171", "a172", "a173", "a174", "a175", "a176", "a177", "a178", "a179", "a180", "a181", "a182", "a183", "a184", "a185", "a186", "a187", "a188", "a189", "a190", "a191", "a192", "a193", "a194", "a195", "a196", "a197", "a198", "a199", "a200", "a201", "a202", "a203", "a204", "a205", "a206", "a207", "a208", "a209", "a210", "a211", "a212", "a213", "a214", "a215", "a216", "a217", "a218", "a219", "a220", "a221", "a222", "a223", "a224", "a225", "a226", "a227", "a228", "a229", "a230", "a231", "a232", "a233", "a234", "a235", "a236", "a237", "a238", "a239", "a240", "a241", "a242", "a243", "a244", "a245", "a246", "a247", "a248", "a249", "a250", "a251", "a252", "a253", "a254", "a255", "s36", "s37", "s38", "s39", "s40", "s41", "s42", "s43", "s44", "s45", "s46", "s47", "s48", "s49", "s50", "s51", "s52", "s53", "s54", "s55", "s56", "s57", "s58", "s59", "s60", "s61", "s62", "s63", "s64", "s65", "s66", "s67", "s68", "s69", "s70", "s71", "s72", "s73", "s74", "s75", "s76", "s77", "s78", "s79", "s80", "s81", "s82", "s83", "s84", "s85", "s86", "s87", "s88", "s89", "s90", "s91", "s92", "s93", "s94", "s95", "s96", "s97", "vcc", "scc", "memory");
    ep = epOut;
    if (kind == 1 && 5 + RHELP + (rep >> 1) < NR4) { kind = 4; kh = rep & 1; rep = 5 + RHELP + (rep >> 1); }
  }
  if (kind == 4) {
    const unsigned sStep = rep < 5 ? 5 : rep == 5 ? 6 : NR4;
    COMMON(10, (kh * 9 + fj) * 128, 2560, 143360, 2048, 448, 0);
    const int cs = slice;
    const unsigned sOutOff = (kh ? OFF_P1 : OFF_P0) + (cs * 4 + wave) * 1024, sFoutOff = OFF_FP + (kh * 8 + cs) * 128;
    const unsigned sAuxOff = (kh ? OFF_XP : OFF_X1) + (cs * 4 + wave) * 1024, sWw = (kh * 1792 + 16 * wave) * 2048 + cs * 256, sBuf = kh * 57344;
    const int zu = 108 + (rep + 2) % 5;
    const unsigned vSp = lg * 2048 + l15 * 4, vSn = rep >= 5 ? OOB : !ALLZ ? (zu * 16 + wave * 4 + lg) * 2048 + (cs * 2 + kh) * 128 + l15 * 8 :
                         ((rep + 5 * (wave >> 1)) * 16 + (wave & 1) * 8 + (ln >> 3)) * 2048 + (cs * 2 + kh) * 128 + (ln & 7) * 16;
    (void)vWr2;
    const unsigned sExp = __builtin_amdgcn_readfirstlane(ep);
    asm volatile(
R"(.macro M d,w,x
v_mfma_f32_16x16x32_bf16 v[\d:\d+3], \w, v[\x:\x+3], v[\d:\d+3]
.endm
.macro R d,a,o
ds_read_b128 v[\d:\d+3], v\a offset:\o
.endm
.macro T d,a,o
ds_read_b64_tr_b16 \d, v\a offset:\o
.endm
.macro D a,s,o
buffer_load_dwordx4 v\a, s[36:39], s\s offen offset:\o sc1 lds
.endm
.macro L c
s_waitcnt lgkmcnt(\c)
.endm
Lcode_g4_b:
s_mov_b64 s[36:37],%0
s_mov_b32 s38,111243392
s_mov_b32 s39,0x00020000
s_mov_b32 s44,%1
s_mov_b32 s45,%2
s_mov_b32 s46,%3
s_mov_b32 s47,%4
s_mov_b32 s48,%5
s_mov_b32 s49,%6
s_mov_b32 s50,%7
s_mov_b32 s51,%8
s_mov_b32 s93,%9
s_mov_b32 s53,%10
v_mov_b32 v0,%11
v_mov_b32 v3,%12
v_mov_b32 v5,%13
v_mov_b32 v6,%14
v_mov_b32 v10,%15
v_mov_b32 v11,%16
v_mov_b32 v12,%17
v_mov_b32 v13,%18
v_mov_b32 v9,%19
v_mov_b32 v7,%20
s_mov_b64 s[40:41],%21
s_mov_b32 s42,7340032
s_mov_b32 s43,0x00020000
s_lshl_b32 s57,s44,11
s_cmp_eq_u32 s45,175
s_cbranch_scc1 Lg4_pre_%=
s_add_u32 s55,s50,0
s_add_u32 m0,s57,40960
s_nop 0
buffer_load_dwordx4 v10,s[40:43],s55 offen offset:0  lds
s_add_u32 m0,s57,41984
s_nop 0
buffer_load_dwordx4 v11,s[40:43],s55 offen offset:0  lds
s_add_u32 m0,s57,49024
s_nop 0
buffer_load_dwordx4 v10,s[40:43],s55 offen offset:128  lds
s_add_u32 m0,s57,50048
s_nop 0
buffer_load_dwordx4 v11,s[40:43],s55 offen offset:128  lds
s_add_u32 s55,s50,131072
s_add_u32 m0,s57,57344
s_nop 0
buffer_load_dwordx4 v10,s[40:43],s55 offen offset:0  lds
s_add_u32 m0,s57,58368
s_nop 0
buffer_load_dwordx4 v11,s[40:43],s55 offen offset:0  lds
s_add_u32 m0,s57,65408
s_nop 0
buffer_load_dwordx4 v10,s[40:43],s55 offen offset:128  lds
s_add_u32 m0,s57,66432
s_nop 0
buffer_load_dwordx4 v11,s[40:43],s55 offen offset:128  lds
s_add_u32 s55,s50,262144
s_add_u32 m0,s57,73728
s_nop 0
buffer_load_dwordx4 v10,s[40:43],s55 offen offset:0  lds
s_add_u32 m0,s57,74752
s_nop 0
buffer_load_dwordx4 v11,s[40:43],s55 offen offset:0  lds
s_add_u32 m0,s57,81792
s_nop 0
buffer_load_dwordx4 v10,s[40:43],s55 offen offset:128  lds
s_add_u32 m0,s57,82816
s_nop 0
buffer_load_dwordx4 v11,s[40:43],s55 offen offset:128  lds
s_add_u32 s55,s50,393216
s_add_u32 m0,s57,90112
s_nop 0
buffer_load_dwordx4 v10,s[40:43],s55 offen offset:0  lds
s_add_u32 m0,s57,91136
s_nop 0
buffer_load_dwordx4 v11,s[40:43],s55 offen offset:0  lds
s_add_u32 m0,s57,98176
s_nop 0
buffer_load_dwordx4 v10,s[40:43],s55 offen offset:128  lds
s_add_u32 m0,s57,99200
s_nop 0
buffer_load_dwordx4 v11,s[40:43],s55 offen offset:128  lds
s_add_u32 s55,s50,524288
s_add_u32 m0,s57,106496
s_nop 0
buffer_load_dwordx4 v10,s[40:43],s55 offen offset:0  lds
s_add_u32 m0,s57,107520
s_nop 0
buffer_load_dwordx4 v11,s[40:43],s55 offen offset:0  lds
s_add_u32 m0,s57,114560
s_nop 0
buffer_load_dwordx4 v10,s[40:43],s55 offen offset:128  lds
s_add_u32 m0,s57,115584
s_nop 0
buffer_load_dwordx4 v11,s[40:43],s55 offen offset:128  lds
s_add_u32 s55,s50,655360
s_add_u32 m0,s57,122880
s_nop 0
buffer_load_dwordx4 v10,s[40:43],s55 offen offset:0  lds
s_add_u32 m0,s57,123904
s_nop 0
buffer_load_dwordx4 v11,s[40:43],s55 offen offset:0  lds
s_add_u32 m0,s57,130944
s_nop 0
buffer_load_dwordx4 v10,s[40:43],s55 offen offset:128  lds
s_add_u32 m0,s57,131968
s_nop 0
buffer_load_dwordx4 v11,s[40:43],s55 offen offset:128  lds
s_waitcnt vmcnt(20) lgkmcnt(0)
s_barrier
s_add_u32 s55,s50,786432
s_add_u32 m0,s57,139264
s_nop 0
buffer_load_dwordx4 v10,s[40:43],s55 offen offset:0  lds
s_add_u32 m0,s57,140288
s_nop 0
buffer_load_dwordx4 v11,s[40:43],s55 offen offset:0  lds
s_add_u32 m0,s57,147328
s_nop 0
buffer_load_dwordx4 v10,s[40:43],s55 offen offset:128  lds
s_add_u32 m0,s57,148352
s_nop 0
buffer_load_dwordx4 v11,s[40:43],s55 offen offset:128  lds
v_add_u32 v18,40960,v12
v_add_u32 v19,40960,v13
T a[0:1],18,0
T a[2:3],18,512
T a[4:5],19,0
T a[6:7],19,512
T a[8:9],18,4096
T a[10:11],18,4608
T a[12:13],19,4096
T a[14:15],19,4608
s_waitcnt vmcnt(20) lgkmcnt(0)
s_barrier
s_add_u32 s55,s50,917504
s_add_u32 m0,s57,40960
s_nop 0
buffer_load_dwordx4 v10,s[40:43],s55 offen offset:0  lds
s_add_u32 m0,s57,41984
s_nop 0
buffer_load_dwordx4 v11,s[40:43],s55 offen offset:0  lds
s_add_u32 m0,s57,49024
s_nop 0
buffer_load_dwordx4 v10,s[40:43],s55 offen offset:128  lds
s_add_u32 m0,s57,50048
s_nop 0
buffer_load_dwordx4 v11,s[40:43],s55 offen offset:128  lds
v_add_u32 v18,57344,v12
v_add_u32 v19,57344,v13
T a[16:17],18,0
T a[18:19],18,512
T a[20:21],19,0
T a[22:23],19,512
T a[24:25],18,4096
T a[26:27],18,4608
T a[28:29],19,4096
T a[30:31],19,4608
s_waitcnt vmcnt(20) lgkmcnt(0)
s_barrier
s_add_u32 s55,s50,1048576
s_add_u32 m0,s57,57344
s_nop 0
buffer_load_dwordx4 v10,s[40:43],s55 offen offset:0  lds
s_add_u32 m0,s57,58368
s_nop 0
buffer_load_dwordx4 v11,s[40:43],s55 offen offset:0  lds
s_add_u32 m0,s57,65408
s_nop 0
buffer_load_dwordx4 v10,s[40:43],s55 offen offset:128  lds
s_add_u32 m0,s57,66432
s_nop 0
buffer_load_dwordx4 v11,s[40:43],s55 offen offset:128  lds
v_add_u32 v18,73728,v12
v_add_u32 v19,73728,v13
T a[32:33],18,0
T a[34:35],18,512
T a[36:37],19,0
T a[38:39],19,512
T a[40:41],18,4096
T a[42:43],18,4608
T a[44:45],19,4096
T a[46:47],19,4608
s_waitcnt vmcnt(20) lgkmcnt(0)
s_barrier
s_add_u32 s55,s50,1179648
s_add_u32 m0,s57,73728
s_nop 0
buffer_load_dwordx4 v10,s[40:43],s55 offen offset:0  lds
s_add_u32 m0,s57,74752
s_nop 0
buffer_load_dwordx4 v11,s[40:43],s55 offen offset:0  lds
s_add_u32 m0,s57,81792
s_nop 0
buffer_load_dwordx4 v10,s[40:43],s55 offen offset:128  lds
s_add_u32 m0,s57,82816
s_nop 0
buffer_load_dwordx4 v11,s[40:43],s55 offen offset:128  lds
v_add_u32 v18,90112,v12
v_add_u32 v19,90112,v13
T a[48:49],18,0
T a[50:51],18,512
T a[52:53],19,0
T a[54:55],19,512
T a[56:57],18,4096
T a[58:59],18,4608
T a[60:61],19,4096
T a[62:63],19,4608
s_waitcnt vmcnt(20) lgkmcnt(0)
s_barrier
s_add_u32 s55,s50,1310720
s_add_u32 m0,s57,90112
s_nop 0
buffer_load_dwordx4 v10,s[40:43],s55 offen offset:0  lds
s_add_u32 m0,s57,91136
s_nop 0
buffer_load_dwordx4 v11,s[40:43],s55 offen offset:0  lds
s_add_u32 m0,s57,98176
s_nop 0
buffer_load_dwordx4 v10,s[40:43],s55 offen offset:128  lds
s_add_u32 m0,s57,99200
s_nop 0
buffer_load_dwordx4 v11,s[40:43],s55 offen offset:128  lds
v_add_u32 v18,106496,v12
v_add_u32 v19,106496,v13
T a[64:65],18,0
T a[66:67],18,512
T a[68:69],19,0
T a[70:71],19,512
T a[72:73],18,4096
T a[74:75],18,4608
T a[76:77],19,4096
T a[78:79],19,4608
s_waitcnt vmcnt(20) lgkmcnt(0)
s_barrier
s_add_u32 s55,s50,1441792
s_add_u32 m0,s57,106496
s_nop 0
buffer_load_dwordx4 v10,s[40:43],s55 offen offset:0  lds
s_add_u32 m0,s57,107520
s_nop 0
buffer_load_dwordx4 v11,s[40:43],s55 offen offset:0  lds
s_add_u32 m0,s57,114560
s_nop 0
buffer_load_dwordx4 v10,s[40:43],s55 offen offset:128  lds
s_add_u32 m0,s57,115584
s_nop 0
buffer_load_dwordx4 v11,s[40:43],s55 offen offset:128  lds
v_add_u32 v18,122880,v12
v_add_u32 v19,122880,v13
T a[80:81],18,0
T a[82:83],18,512
T a[84:85],19,0
T a[86:87],19,512
T a[88:89],18,4096
T a[90:91],18,4608
T a[92:93],19,4096
T a[94:95],19,4608
s_waitcnt vmcnt(20) lgkmcnt(0)
s_barrier
s_add_u32 s55,s50,1572864
s_add_u32 m0,s57,122880
s_nop 0
buffer_load_dwordx4 v10,s[40:43],s55 offen offset:0  lds
s_add_u32 m0,s57,123904
s_nop 0
buffer_load_dwordx4 v11,s[40:43],s55 offen offset:0  lds
s_add_u32 m0,s57,130944
s_nop 0
buffer_load_dwordx4 v10,s[40:43],s55 offen offset:128  lds
s_add_u32 m0,s57,131968
s_nop 0
buffer_load_dwordx4 v11,s[40:43],s55 offen offset:128  lds
v_add_u32 v18,139264,v12
v_add_u32 v19,139264,v13
T a[96:97],18,0
T a[98:99],18,512
T a[100:101],19,0
T a[102:103],19,512
T a[104:105],18,4096
T a[106:107],18,4608
T a[108:109],19,4096
T a[110:111],19,4608
s_waitcnt vmcnt(20) lgkmcnt(0)
s_barrier
s_add_u32 s55,s50,1703936
s_add_u32 m0,s57,139264
s_nop 0
buffer_load_dwordx4 v10,s[40:43],s55 offen offset:0  lds
s_add_u32 m0,s57,140288
s_nop 0
buffer_load_dwordx4 v11,s[40:43],s55 offen offset:0  lds
s_add_u32 m0,s57,147328
s_nop 0
buffer_load_dwordx4 v10,s[40:43],s55 offen offset:128  lds
s_add_u32 m0,s57,148352
s_nop 0
buffer_load_dwordx4 v11,s[40:43],s55 offen offset:128  lds
v_add_u32 v18,40960,v12
v_add_u32 v19,40960,v13
T a[112:113],18,0
T a[114:115],18,512
T a[116:117],19,0
T a[118:119],19,512
T a[120:121],18,4096
T a[122:123],18,4608
T a[124:125],19,4096
T a[126:127],19,4608
s_waitcnt vmcnt(20) lgkmcnt(0)
s_barrier
s_add_u32 s55,s50,1835008
s_add_u32 m0,s57,40960
s_nop 0
buffer_load_dwordx4 v10,s[40:43],s55 offen offset:0  lds
s_add_u32 m0,s57,41984
s_nop 0
buffer_load_dwordx4 v11,s[40:43],s55 offen offset:0  lds
s_add_u32 m0,s57,49024
s_nop 0
buffer_load_dwordx4 v10,s[40:43],s55 offen offset:128  lds
s_add_u32 m0,s57,50048
s_nop 0
buffer_load_dwordx4 v11,s[40:43],s55 offen offset:128  lds
v_add_u32 v18,57344,v12
v_add_u32 v19,57344,v13
T a[128:129],18,0
T a[130:131],18,512
T a[132:133],19,0
T a[134:135],19,512
T a[136:137],18,4096
T a[138:139],18,4608
T a[140:141],19,4096
T a[142:143],19,4608
s_waitcnt vmcnt(20) lgkmcnt(0)
s_barrier
s_add_u32 s55,s50,1966080
s_add_u32 m0,s57,57344
s_nop 0
buffer_load_dwordx4 v10,s[40:43],s55 offen offset:0  lds
s_add_u32 m0,s57,58368
s_nop 0
buffer_load_dwordx4 v11,s[40:43],s55 offen offset:0  lds
s_add_u32 m0,s57,65408
s_nop 0
buffer_load_dwordx4 v10,s[40:43],s55 offen offset:128  lds
s_add_u32 m0,s57,66432
s_nop 0
buffer_load_dwordx4 v11,s[40:43],s55 offen offset:128  lds
v_add_u32 v18,73728,v12
v_add_u32 v19,73728,v13
T a[144:145],18,0
T a[146:147],18,512
T a[148:149],19,0
T a[150:151],19,512
T a[152:153],18,4096
T a[154:155],18,4608
T a[156:157],19,4096
T a[158:159],19,4608
s_waitcnt vmcnt(20) lgkmcnt(0)
s_barrier
s_add_u32 s55,s50,2097152
s_add_u32 m0,s57,73728
s_nop 0
buffer_load_dwordx4 v10,s[40:43],s55 offen offset:0  lds
s_add_u32 m0,s57,74752
s_nop 0
buffer_load_dwordx4 v11,s[40:43],s55 offen offset:0  lds
s_add_u32 m0,s57,81792
s_nop 0
buffer_load_dwordx4 v10,s[40:43],s55 offen offset:128  lds
s_add_u32 m0,s57,82816
s_nop 0
buffer_load_dwordx4 v11,s[40:43],s55 offen offset:128  lds
v_add_u32 v18,90112,v12
v_add_u32 v19,90112,v13
T a[160:161],18,0
T a[162:163],18,512
T a[164:165],19,0
T a[166:167],19,512
T a[168:169],18,4096
T a[170:171],18,4608
T a[172:173],19,4096
T a[174:175],19,4608
s_waitcnt vmcnt(20) lgkmcnt(0)
s_barrier
s_add_u32 s55,s50,2228224
s_add_u32 m0,s57,90112
s_nop 0
buffer_load_dwordx4 v10,s[40:43],s55 offen offset:0  lds
s_add_u32 m0,s57,91136
s_nop 0
buffer_load_dwordx4 v11,s[40:43],s55 offen offset:0  lds
s_add_u32 m0,s57,98176
s_nop 0
buffer_load_dwordx4 v10,s[40:43],s55 offen offset:128  lds
s_add_u32 m0,s57,99200
s_nop 0
buffer_load_dwordx4 v11,s[40:43],s55 offen offset:128  lds
v_add_u32 v18,106496,v12
v_add_u32 v19,106496,v13
T a[176:177],18,0
T a[178:179],18,512
T a[180:181],19,0
T a[182:183],19,512
T a[184:185],18,4096
T a[186:187],18,4608
T a[188:189],19,4096
T a[190:191],19,4608
s_waitcnt vmcnt(20) lgkmcnt(0)
s_barrier
s_add_u32 s55,s50,2359296
s_add_u32 m0,s57,106496
s_nop 0
buffer_load_dwordx4 v10,s[40:43],s55 offen offset:0  lds
s_add_u32 m0,s57,107520
s_nop 0
buffer_load_dwordx4 v11,s[40:43],s55 offen offset:0  lds
s_add_u32 m0,s57,114560
s_nop 0
buffer_load_dwordx4 v10,s[40:43],s55 offen offset:128  lds
s_add_u32 m0,s57,115584
s_nop 0
buffer_load_dwordx4 v11,s[40:43],s55 offen offset:128  lds
v_add_u32 v18,122880,v12
v_add_u32 v19,122880,v13
T a[192:193],18,0
T a[194:195],18,512
T a[196:197],19,0
T a[198:199],19,512
T a[200:201],18,4096
T a[202:203],18,4608
T a[204:205],19,4096
T a[206:207],19,4608
s_waitcnt vmcnt(20) lgkmcnt(0)
s_barrier
s_add_u32 s55,s50,2490368
s_add_u32 m0,s57,122880
s_nop 0
buffer_load_dwordx4 v10,s[40:43],s55 offen offset:0  lds
s_add_u32 m0,s57,123904
s_nop 0
buffer_load_dwordx4 v11,s[40:43],s55 offen offset:0  lds
s_add_u32 m0,s57,130944
s_nop 0
buffer_load_dwordx4 v10,s[40:43],s55 offen offset:128  lds
s_add_u32 m0,s57,131968
s_nop 0
buffer_load_dwordx4 v11,s[40:43],s55 offen offset:128  lds
v_add_u32 v18,139264,v12
v_add_u32 v19,139264,v13
T a[208:209],18,0
T a[210:211],18,512
T a[212:213],19,0
T a[214:215],19,512
T a[216:217],18,4096
T a[218:219],18,4608
T a[220:221],19,4096
T a[222:223],19,4608
s_waitcnt vmcnt(20) lgkmcnt(0)
s_barrier
s_add_u32 s55,s50,2621440
s_add_u32 m0,s57,139264
s_nop 0
buffer_load_dwordx4 v10,s[40:43],s55 offen offset:0  lds
s_add_u32 m0,s57,140288
s_nop 0
buffer_load_dwordx4 v11,s[40:43],s55 offen offset:0  lds
s_add_u32 m0,s57,147328
s_nop 0
buffer_load_dwordx4 v10,s[40:43],s55 offen offset:128  lds
s_add_u32 m0,s57,148352
s_nop 0
buffer_load_dwordx4 v11,s[40:43],s55 offen offset:128  lds
v_add_u32 v18,40960,v12
v_add_u32 v19,40960,v13
T a[224:225],18,0
T a[226:227],18,512
T a[228:229],19,0
T a[230:231],19,512
T a[232:233],18,4096
T a[234:235],18,4608
T a[236:237],19,4096
T a[238:239],19,4608
s_waitcnt vmcnt(20) lgkmcnt(0)
s_barrier
)"
R"(s_add_u32 s55,s50,2752512
s_add_u32 m0,s57,40960
s_nop 0
buffer_load_dwordx4 v10,s[40:43],s55 offen offset:0  lds
s_add_u32 m0,s57,41984
s_nop 0
buffer_load_dwordx4 v11,s[40:43],s55 offen offset:0  lds
s_add_u32 m0,s57,49024
s_nop 0
buffer_load_dwordx4 v10,s[40:43],s55 offen offset:128  lds
s_add_u32 m0,s57,50048
s_nop 0
buffer_load_dwordx4 v11,s[40:43],s55 offen offset:128  lds
v_add_u32 v18,57344,v12
v_add_u32 v19,57344,v13
T a[240:241],18,0
T a[242:243],18,512
T a[244:245],19,0
T a[246:247],19,512
T a[248:249],18,4096
T a[250:251],18,4608
T a[252:253],19,4096
T a[254:255],19,4608
Lg4_pre_%=:
s_waitcnt vmcnt(20) lgkmcnt(0)
s_barrier
s_add_u32 s55,s50,2883584
s_add_u32 m0,s57,57344
s_nop 0
buffer_load_dwordx4 v10,s[40:43],s55 offen offset:0  lds
s_add_u32 m0,s57,58368
s_nop 0
buffer_load_dwordx4 v11,s[40:43],s55 offen offset:0  lds
s_add_u32 m0,s57,65408
s_nop 0
buffer_load_dwordx4 v10,s[40:43],s55 offen offset:128  lds
s_add_u32 m0,s57,66432
s_nop 0
buffer_load_dwordx4 v11,s[40:43],s55 offen offset:128  lds
v_add_u32 v18,73728,v12
v_add_u32 v19,73728,v13
T v[64:65],18,0
T v[66:67],18,512
T v[68:69],19,0
T v[70:71],19,512
T v[72:73],18,4096
T v[74:75],18,4608
T v[76:77],19,4096
T v[78:79],19,4608
s_waitcnt vmcnt(20) lgkmcnt(0)
s_barrier
s_add_u32 s55,s50,3014656
s_add_u32 m0,s57,73728
s_nop 0
buffer_load_dwordx4 v10,s[40:43],s55 offen offset:0  lds
s_add_u32 m0,s57,74752
s_nop 0
buffer_load_dwordx4 v11,s[40:43],s55 offen offset:0  lds
s_add_u32 m0,s57,81792
s_nop 0
buffer_load_dwordx4 v10,s[40:43],s55 offen offset:128  lds
s_add_u32 m0,s57,82816
s_nop 0
buffer_load_dwordx4 v11,s[40:43],s55 offen offset:128  lds
v_add_u32 v18,90112,v12
v_add_u32 v19,90112,v13
T v[80:81],18,0
T v[82:83],18,512
T v[84:85],19,0
T v[86:87],19,512
T v[88:89],18,4096
T v[90:91],18,4608
T v[92:93],19,4096
T v[94:95],19,4608
s_waitcnt vmcnt(20) lgkmcnt(0)
s_barrier
s_add_u32 s55,s50,3145728
s_add_u32 m0,s57,90112
s_nop 0
buffer_load_dwordx4 v10,s[40:43],s55 offen offset:0  lds
s_add_u32 m0,s57,91136
s_nop 0
buffer_load_dwordx4 v11,s[40:43],s55 offen offset:0  lds
s_add_u32 m0,s57,98176
s_nop 0
buffer_load_dwordx4 v10,s[40:43],s55 offen offset:128  lds
s_add_u32 m0,s57,99200
s_nop 0
buffer_load_dwordx4 v11,s[40:43],s55 offen offset:128  lds
v_add_u32 v18,106496,v12
v_add_u32 v19,106496,v13
T v[96:97],18,0
T v[98:99],18,512
T v[100:101],19,0
T v[102:103],19,512
T v[104:105],18,4096
T v[106:107],18,4608
T v[108:109],19,4096
T v[110:111],19,4608
s_waitcnt vmcnt(20) lgkmcnt(0)
s_barrier
s_add_u32 s55,s50,3276800
s_add_u32 m0,s57,106496
s_nop 0
buffer_load_dwordx4 v10,s[40:43],s55 offen offset:0  lds
s_add_u32 m0,s57,107520
s_nop 0
buffer_load_dwordx4 v11,s[40:43],s55 offen offset:0  lds
s_add_u32 m0,s57,114560
s_nop 0
buffer_load_dwordx4 v10,s[40:43],s55 offen offset:128  lds
s_add_u32 m0,s57,115584
s_nop 0
buffer_load_dwordx4 v11,s[40:43],s55 offen offset:128  lds
v_add_u32 v18,122880,v12
v_add_u32 v19,122880,v13
T v[112:113],18,0
T v[114:115],18,512
T v[116:117],19,0
T v[118:119],19,512
T v[120:121],18,4096
T v[122:123],18,4608
T v[124:125],19,4096
T v[126:127],19,4608
s_waitcnt vmcnt(20) lgkmcnt(0)
s_barrier
s_add_u32 s55,s50,3407872
s_add_u32 m0,s57,122880
s_nop 0
buffer_load_dwordx4 v10,s[40:43],s55 offen offset:0  lds
s_add_u32 m0,s57,123904
s_nop 0
buffer_load_dwordx4 v11,s[40:43],s55 offen offset:0  lds
s_add_u32 m0,s57,130944
s_nop 0
buffer_load_dwordx4 v10,s[40:43],s55 offen offset:128  lds
s_add_u32 m0,s57,131968
s_nop 0
buffer_load_dwordx4 v11,s[40:43],s55 offen offset:128  lds
v_add_u32 v18,139264,v12
v_add_u32 v19,139264,v13
T v[128:129],18,0
T v[130:131],18,512
T v[132:133],19,0
T v[134:135],19,512
T v[136:137],18,4096
T v[138:139],18,4608
T v[140:141],19,4096
T v[142:143],19,4608
s_waitcnt vmcnt(20) lgkmcnt(0)
s_barrier
s_add_u32 s55,s50,3538944
s_add_u32 m0,s57,139264
s_nop 0
buffer_load_dwordx4 v10,s[40:43],s55 offen offset:0  lds
s_add_u32 m0,s57,140288
s_nop 0
buffer_load_dwordx4 v11,s[40:43],s55 offen offset:0  lds
s_add_u32 m0,s57,147328
s_nop 0
buffer_load_dwordx4 v10,s[40:43],s55 offen offset:128  lds
s_add_u32 m0,s57,148352
s_nop 0
buffer_load_dwordx4 v11,s[40:43],s55 offen offset:128  lds
v_add_u32 v18,40960,v12
v_add_u32 v19,40960,v13
T v[144:145],18,0
T v[146:147],18,512
T v[148:149],19,0
T v[150:151],19,512
T v[152:153],18,4096
T v[154:155],18,4608
T v[156:157],19,4096
T v[158:159],19,4608
s_waitcnt vmcnt(20) lgkmcnt(0)
s_barrier
v_add_u32 v18,57344,v12
v_add_u32 v19,57344,v13
T v[160:161],18,0
T v[162:163],18,512
T v[164:165],19,0
T v[166:167],19,512
T v[168:169],18,4096
T v[170:171],18,4608
T v[172:173],19,4096
T v[174:175],19,4608
s_waitcnt vmcnt(16) lgkmcnt(0)
s_barrier
v_add_u32 v18,73728,v12
v_add_u32 v19,73728,v13
T v[176:177],18,0
T v[178:179],18,512
T v[180:181],19,0
T v[182:183],19,512
T v[184:185],18,4096
T v[186:187],18,4608
T v[188:189],19,4096
T v[190:191],19,4608
s_waitcnt vmcnt(12) lgkmcnt(0)
s_barrier
v_add_u32 v18,90112,v12
v_add_u32 v19,90112,v13
T v[192:193],18,0
T v[194:195],18,512
T v[196:197],19,0
T v[198:199],19,512
T v[200:201],18,4096
T v[202:203],18,4608
T v[204:205],19,4096
T v[206:207],19,4608
s_waitcnt vmcnt(8) lgkmcnt(0)
s_barrier
v_add_u32 v18,106496,v12
v_add_u32 v19,106496,v13
T v[208:209],18,0
T v[210:211],18,512
T v[212:213],19,0
T v[214:215],19,512
T v[216:217],18,4096
T v[218:219],18,4608
T v[220:221],19,4096
T v[222:223],19,4608
s_waitcnt vmcnt(4) lgkmcnt(0)
s_barrier
v_add_u32 v18,122880,v12
v_add_u32 v19,122880,v13
T v[224:225],18,0
T v[226:227],18,512
T v[228:229],19,0
T v[230:231],19,512
T v[232:233],18,4096
T v[234:235],18,4608
T v[236:237],19,4096
T v[238:239],19,4608
s_waitcnt vmcnt(0) lgkmcnt(0)
s_barrier
v_add_u32 v18,139264,v12
v_add_u32 v19,139264,v13
T v[240:241],18,0
T v[242:243],18,512
T v[244:245],19,0
T v[246:247],19,512
T v[248:249],18,4096
T v[250:251],18,4608
T v[252:253],19,4096
T v[254:255],19,4608
L 0
s_barrier
v_mov_b32 v4,s53
s_mov_b32 s82,0
s_mov_b32 s80,0
s_mov_b32 s54,s45
s_mul_i32 s57,s44,7168
s_mul_i32 s55,s45,114688
s_add_u32 s55,s55,s57
s_add_u32 s55,s55,40894464
s_mul_i32 s57,s44,7168
s_mul_i32 s60,s46,114688
s_mul_i32 s61,s46,2560
s_mul_i32 s62,s46,32768
s_mul_i32 s63,s46,2048
s_mul_i32 s64,s46,512
s_lshl_b32 s59,s44,8
s_add_u32 s59,s59,143360
s_mul_i32 s58,s45,2560
s_add_u32 s58,s58,109891584
s_mul_i32 s65,s45,32768
s_add_u32 s65,s65,s47
s_mul_i32 s68,s45,2048
s_add_u32 s68,s68,s48
s_mul_i32 s73,s45,512
s_add_u32 s73,s73,106954752
s_mul_i32 s74,s45,32768
s_add_u32 s74,s74,s49
s_mul_i32 s75,s45,1024
s_add_u32 s75,s75,107479040
s_mov_b32 s66,0x7fff0000
s_mov_b32 s67,0x7fff0000
s_mov_b32 s69,0x7fff0000
s_mov_b32 s76,0x7fff0000
s_mov_b32 s77,0x7fff0000
s_mov_b32 s70,0x7fff0000
s_mov_b32 s71,0x7fff0000
s_mov_b32 s72,0x7fff0000
v_mov_b32 v1,v0
v_add_u32 v2,28672,v0
s_mov_b32 s84,28672
s_mov_b32 s86,0x3a800000
s_mov_b32 s87,0x3727c5ac
s_mov_b32 s88,0x409d30fc
s_mov_b32 s89,0xbeeff558
s_lshr_b32 s85,s44,1
s_mul_i32 s85,s85,127
s_add_u32 s55,s55,s51
s_mul_i32 s78,s45,114688
s_sub_u32 s60,s55,s78
s_add_u32 s50,s93,224
s_add_u32 s93,s93,170
s_cmp_lt_u32 s45,224
s_cselect_b32 s63,224,-1
s_cmp_lt_u32 s45,170
s_cselect_b32 s63,170,s63
s_cselect_b32 s93,s93,s50
s_cselect_b32 s88,6,8
s_cselect_b32 s89,224,-1
v_lshlrev_b32 v8,22,v0
v_and_b32 v8,0x80000000,v8
v_or_b32 v8,v8,v5
v_add_u32 v2,57344,v0
s_lshl_b32 s84,s57,1
s_add_u32 s84,s84,57344
s_mov_b64 s[86:87],%22
s_mov_b64 s[40:41],%22
s_mov_b32 s42,4194304
s_and_b32 s78,s47,65535
s_lshr_b32 s78,s78,4
s_sub_u32 s85,s47,s78
s_add_u32 s85,s85,4194304
s_add_u32 s45,s47,5832704
s_mul_i32 s73,s44,1280
s_add_u32 s73,s73,114688
v_lshrrev_b32 v62,4,v9
v_and_b32 v62,3,v62
v_mul_u32_u24 v62,288,v62
v_lshrrev_b32 v63,7,v9
v_and_b32 v5,12,v9
v_add3_u32 v5,v5,v62,v63
v_add_u32 v5,s73,v5
v_mov_b32 v10,0
v_mov_b32 v11,0
v_mov_b32 v12,0
v_mov_b32 v13,0
s_mov_b32 s79,1638400
buffer_store_dwordx4 v[10:13],v7,s[40:43],s79 offen offset:0 sc0 sc1
s_mov_b32 s79,1966080
buffer_store_dwordx4 v[10:13],v7,s[40:43],s79 offen offset:0 sc0 sc1
s_mov_b32 s79,2293760
buffer_store_dwordx4 v[10:13],v7,s[40:43],s79 offen offset:0 sc0 sc1
s_mov_b32 s79,2621440
buffer_store_dwordx4 v[10:13],v7,s[40:43],s79 offen offset:0 sc0 sc1
s_mov_b32 s79,2949120
buffer_store_dwordx4 v[10:13],v7,s[40:43],s79 offen offset:0 sc0 sc1
s_mov_b32 s79,3276800
buffer_store_dwordx4 v[10:13],v7,s[40:43],s79 offen offset:0 sc0 sc1
s_mov_b32 s79,3604480
buffer_store_dwordx4 v[10:13],v7,s[40:43],s79 offen offset:0 sc0 sc1
s_mov_b32 s79,3932160
buffer_store_dwordx4 v[10:13],v7,s[40:43],s79 offen offset:0 sc0 sc1
s_getpc_b64 s[40:41]
Lg4_pc_%=:
s_lshl_b32 s78,s44,13
v_lshlrev_b32 v62,3,v0
v_add_u32 v62,s78,v62
v_min_u32 v62,Lcode_g4_e-Lg4_pc_%=-4,v62
s_mov_b32 m0,163584
s_nop 0
global_load_lds_dword v62,s[40:41]
s_add_u32 m0,s59,4096
s_nop 0
buffer_load_dword v8,s[36:39],s58 offen offset:0 sc1 lds
buffer_store_dword v4,v3,s[36:39],s71 offen offset:0 sc0 sc1
s_add_u32 m0,s59,5120
s_nop 0
buffer_load_dword v8,s[36:39],s58 offen offset:0 sc1 lds
buffer_store_dword v4,v3,s[36:39],s71 offen offset:0 sc0 sc1
s_mov_b32 s83,0
Lg4_poll_%=:
s_add_u32 s78,s83,2
s_and_b32 s78,s78,3
s_lshl_b32 s78,s78,10
s_add_u32 s78,s78,4096
s_add_u32 m0,s59,s78
s_nop 0
buffer_load_dword v8,s[36:39],s58 offen offset:0 sc1 lds
s_and_b32 s78,s83,3
s_lshl_b32 s78,s78,10
v_add_u32 v62,s78,v6
s_waitcnt vmcnt(4)
s_barrier
ds_read_b32 v7,v62 offset:4096
buffer_store_dword v4,v3,s[36:39],s70 offen offset:0 sc0 sc1
s_mov_b32 s70,s71
s_mov_b32 s71,s72
s_mov_b32 s72,0x7fff0000
s_add_u32 s83,s83,1
s_add_u32 s82,s82,1
L 0
v_cmp_eq_u32 vcc,s53,v7
s_nop 0
s_cmp_eq_u32 vcc_lo,-1
s_cbranch_scc1 Lg4_go_%=
s_cmp_lt_u32 s82,20000
s_cbranch_scc1 Lg4_poll_%=
Lg4_go_%=:
s_add_u32 m0,s57,0
s_add_u32 s56,s55,0
D 0,56,0
D 0,56,1024
D 0,56,2048
D 0,56,3072
s_add_u32 m0,s57,4096
s_add_u32 s56,s56,4096
D 0,56,0
D 0,56,1024
D 0,56,2048
s_add_u32 s61,s54,s46
s_cmp_ge_u32 s61,s63
s_cselect_b32 s61,s93,s61
s_cselect_b32 s46,s88,s46
s_cselect_b32 s63,s89,s63
s_cselect_b32 s93,s50,s93
s_cselect_b32 s88,8,s88
s_cselect_b32 s89,-1,s89
s_mul_i32 s58,s61,2560
s_add_u32 s58,s58,109891584
s_add_u32 m0,s59,2048
s_nop 0
buffer_load_dword v8,s[36:39],s58 offen offset:0 sc1 lds
s_add_u32 m0,s57,28672
s_add_u32 s56,s55,28672
D 0,56,0
D 0,56,1024
D 0,56,2048
D 0,56,3072
s_add_u32 m0,s57,32768
s_add_u32 s56,s56,4096
D 0,56,0
D 0,56,1024
D 0,56,2048
s_mul_i32 s55,s61,114688
s_add_u32 s55,s55,s60
s_sub_u32 s57,s84,s57
s_mov_b32 s54,s61
s_waitcnt vmcnt(7)
s_barrier
buffer_store_dword v4,v3,s[36:39],s70 offen offset:0 sc0 sc1
s_min_u32 s71,s71,s72
buffer_store_dword v4,v3,s[36:39],s71 offen offset:0 sc0 sc1
s_mov_b32 s70,0x7fff0000
s_mov_b32 s71,0x7fff0000
s_mov_b32 s72,0x7fff0000
s_cmp_eq_u32 s80,1
s_cbranch_scc1 Lg4_st1_%=
s_branch Lg4_st0_%=
Lg4_gox_%=:
s_add_u32 m0,s59,2048
s_nop 0
buffer_load_dword v8,s[36:39],s58 offen offset:0 sc1 lds
s_waitcnt vmcnt(0)
s_barrier
buffer_store_dword v4,v3,s[36:39],s70 offen offset:0 sc0 sc1
s_min_u32 s71,s71,s72
buffer_store_dword v4,v3,s[36:39],s71 offen offset:0 sc0 sc1
s_mov_b32 s70,0x7fff0000
s_mov_b32 s71,0x7fff0000
s_mov_b32 s72,0x7fff0000
s_cmp_eq_u32 s80,1
s_cbranch_scc1 Lg4_st1_%=
Lg4_st0_%=:
ds_read_b32 v7,v6 offset:2048
R 26,1,0
R 30,1,1024
R 34,1,2048
R 38,1,3072
R 42,1,4096
L 5
v_cmp_eq_u32 vcc,s53,v7
L 3
s_cmp_eq_u32 vcc_lo,-1
s_cbranch_scc1 Lg4_F0_%=
s_branch Lg4_L0_%=
Lg4_st1_%=:
ds_read_b32 v7,v6 offset:2048
R 26,2,0
R 30,2,1024
R 34,2,2048
R 38,2,3072
R 42,2,4096
L 5
v_cmp_eq_u32 vcc,s53,v7
L 3
s_cmp_eq_u32 vcc_lo,-1
s_cbranch_scc1 Lg4_F1_%=
s_branch Lg4_L1_%=
Lg4_F0_%=:
v_mfma_f32_16x16x32_bf16 v[14:17],a[4:7],v[26:29],0
s_add_u32 m0,s57,0
R 46,1,5120
v_lshlrev_b32 v62,16,v58
M 14,a[12:15],30
s_add_u32 s56,s55,0
v_add_f32 v18,v18,v62
v_and_b32 v63,0xffff0000,v58
v_mfma_f32_16x16x32_bf16 v[10:13],a[0:3],v[26:29],0
s_add_u32 s61,s54,s46
R 50,1,6144
v_add_f32 v19,v19,v63
M 10,a[8:11],30
L 3
D 0,56,0
)"
R"(M 10,a[16:19],34
s_cmp_ge_u32 s61,s63
R 54,1,7168
v_lshlrev_b32 v62,16,v59
M 10,a[24:27],38
s_cselect_b32 s61,s93,s61
v_add_f32 v20,v20,v62
v_and_b32 v63,0xffff0000,v59
M 14,a[20:23],34
s_cselect_b32 s46,s88,s46
R 26,1,8192
v_add_f32 v21,v21,v63
M 14,a[28:31],38
L 3
v_lshlrev_b32 v62,16,v60
v_add_f32 v22,v22,v62
M 14,a[36:39],42
s_cselect_b32 s63,s89,s63
R 30,1,9216
v_and_b32 v63,0xffff0000,v60
M 14,a[44:47],46
D 0,56,1024
M 10,a[32:35],42
s_cselect_b32 s93,s50,s93
R 34,1,10240
v_add_f32 v23,v23,v63
M 10,a[40:43],46
L 3
v_lshlrev_b32 v62,16,v61
v_add_f32 v24,v24,v62
M 10,a[48:51],50
s_cselect_b32 s88,8,s88
R 38,1,11264
v_and_b32 v63,0xffff0000,v61
M 10,a[56:59],54
buffer_load_dwordx4 v[58:61],v0,s[36:39],s74 offen offset:0 sc1
M 14,a[52:55],50
s_cselect_b32 s89,-1,s89
R 42,1,12288
v_add_f32 v25,v25,v63
M 14,a[60:63],54
L 3
v_cvt_pk_bf16_f32 v18,v18,v19
v_cvt_pk_bf16_f32 v19,v20,v21
M 14,a[68:71],26
s_mul_i32 s58,s61,2560
R 46,1,13312
v_cvt_pk_bf16_f32 v20,v22,v23
M 14,a[76:79],30
D 0,56,2048
M 10,a[64:67],26
s_add_u32 s58,s58,109891584
R 50,1,14336
v_cvt_pk_bf16_f32 v21,v24,v25
M 10,a[72:75],30
L 3
buffer_store_dwordx4 v[18:21],v0,s[36:39],s67 offen offset:0 sc0 sc1
M 10,a[80:83],34
s_lshl_b32 s62,s54,15
s_mov_b32 s66,s65
R 54,1,15360
M 10,a[88:91],38
s_cmp_ge_u32 s66,s45
s_cselect_b32 s51,1,-1
s_add_u32 s74,s49,s62
M 14,a[84:87],34
s_add_u32 s65,s47,s62
s_mov_b32 s54,s61
R 26,1,16384
M 14,a[92:95],38
L 3
M 14,a[100:103],42
R 30,1,17408
M 14,a[108:111],46
D 0,56,3072
M 10,a[96:99],42
s_add_u32 m0,s57,4096
s_add_u32 s56,s56,4096
R 34,1,18432
M 10,a[104:107],46
L 3
M 10,a[112:115],50
R 38,1,19456
M 10,a[120:123],54
M 14,a[116:119],50
R 42,1,20480
M 14,a[124:127],54
L 3
D 0,56,0
M 14,a[132:135],26
R 46,1,21504
M 14,a[140:143],30
M 10,a[128:131],26
R 50,1,22528
M 10,a[136:139],30
L 3
M 10,a[144:147],34
R 54,1,23552
M 10,a[152:155],38
M 14,a[148:151],34
R 26,1,24576
M 14,a[156:159],38
L 3
D 0,56,1024
M 14,a[164:167],42
R 30,1,25600
M 14,a[172:175],46
M 10,a[160:163],42
R 34,1,26624
M 10,a[168:171],46
L 3
M 10,a[176:179],50
s_waitcnt vmcnt(9)
R 38,1,27648
M 10,a[184:187],54
s_barrier
M 14,a[180:183],50
R 42,1,28672
M 14,a[188:191],54
L 3
D 0,56,2048
M 14,a[196:199],26
s_add_u32 m0,s59,0
s_add_u32 s56,s55,28672
R 46,1,29696
M 14,a[204:207],30
s_mul_i32 s55,s61,114688
s_add_u32 s55,s55,s60
M 10,a[192:195],26
R 50,1,30720
M 10,a[200:203],30
L 3
M 10,a[208:211],34
R 54,1,31744
M 10,a[216:219],38
M 14,a[212:215],34
R 26,1,32768
M 14,a[220:223],38
L 3
buffer_load_dword v8,s[36:39],s58 offen offset:0 sc1 lds
M 14,a[228:231],42
s_add_u32 m0,s57,28672
R 30,1,33792
M 14,a[236:239],46
D 0,56,0
M 10,a[224:227],42
R 34,1,34816
M 10,a[232:235],46
L 3
M 10,a[240:243],50
R 38,1,35840
M 10,a[248:251],54
M 14,a[244:247],50
R 42,1,36864
M 14,a[252:255],54
L 3
D 0,56,1024
M 14,v[68:71],26
R 46,1,37888
M 14,v[76:79],30
M 10,v[64:67],26
R 50,1,38912
M 10,v[72:75],30
L 3
M 10,v[80:83],34
R 54,1,39936
M 10,v[88:91],38
D 0,56,2048
M 14,v[84:87],34
R 26,1,40960
M 14,v[92:95],38
L 3
M 14,v[100:103],42
R 30,1,41984
M 14,v[108:111],46
M 10,v[96:99],42
R 34,1,43008
M 10,v[104:107],46
L 3
D 0,56,3072
M 10,v[112:115],50
s_add_u32 m0,s57,32768
s_add_u32 s56,s56,4096
R 38,1,44032
M 10,v[120:123],54
s_sub_u32 s57,s84,s57
M 14,v[116:119],50
R 42,1,45056
M 14,v[124:127],54
L 3
M 14,v[132:135],26
R 46,1,46080
M 14,v[140:143],30
D 0,56,0
M 10,v[128:131],26
R 50,1,47104
M 10,v[136:139],30
L 3
M 10,v[144:147],34
R 54,1,48128
M 10,v[152:155],38
M 14,v[148:151],34
R 26,1,49152
M 14,v[156:159],38
L 3
D 0,56,1024
M 14,v[164:167],42
R 30,1,50176
M 14,v[172:175],46
M 10,v[160:163],42
R 34,1,51200
M 10,v[168:171],46
L 3
M 10,v[176:179],50
R 38,1,52224
M 10,v[184:187],54
D 0,56,2048
M 14,v[180:183],50
R 42,1,53248
M 14,v[188:191],54
L 3
M 14,v[196:199],26
R 46,1,54272
M 14,v[204:207],30
M 10,v[192:195],26
R 50,1,55296
M 10,v[200:203],30
L 3
M 10,v[208:211],34
s_waitcnt vmcnt(7)
R 54,1,56320
M 10,v[216:219],38
s_barrier
ds_read_b32 v7,v6 offset:0
M 14,v[212:215],34
R 26,2,0
M 14,v[220:223],38
L 4
buffer_store_dword v4,v3,s[36:39],s69 offen offset:0 sc0 sc1
M 14,v[228:231],42
s_mov_b32 s69,s68
s_lshr_b32 s68,s62,4
R 30,2,1024
M 14,v[236:239],46
s_add_u32 s68,s68,s48
M 10,v[224:227],42
R 34,2,2048
M 10,v[232:235],46
L 4
L 3
M 10,v[240:243],50
R 38,2,3072
v_cmp_eq_u32 vcc,s53,v7
M 10,v[248:251],54
M 14,v[244:247],50
L 2
s_cmp_eq_u32 vcc_lo,s51
R 42,2,4096
M 14,v[252:255],54
s_cbranch_scc0 Lg4_K1_%=
Lg4_F1_%=:
v_mfma_f32_16x16x32_bf16 v[22:25],a[4:7],v[26:29],0
s_add_u32 m0,s57,0
R 46,2,5120
v_lshlrev_b32 v62,16,v58
M 22,a[12:15],30
s_add_u32 s56,s55,0
v_add_f32 v10,v10,v62
v_and_b32 v63,0xffff0000,v58
v_mfma_f32_16x16x32_bf16 v[18:21],a[0:3],v[26:29],0
s_add_u32 s61,s54,s46
R 50,2,6144
v_add_f32 v11,v11,v63
M 18,a[8:11],30
L 3
D 0,56,0
M 18,a[16:19],34
s_cmp_ge_u32 s61,s63
R 54,2,7168
v_lshlrev_b32 v62,16,v59
M 18,a[24:27],38
s_cselect_b32 s61,s93,s61
v_add_f32 v12,v12,v62
v_and_b32 v63,0xffff0000,v59
M 22,a[20:23],34
s_cselect_b32 s46,s88,s46
R 26,2,8192
v_add_f32 v13,v13,v63
M 22,a[28:31],38
L 3
v_lshlrev_b32 v62,16,v60
v_add_f32 v14,v14,v62
M 22,a[36:39],42
s_cselect_b32 s63,s89,s63
R 30,2,9216
v_and_b32 v63,0xffff0000,v60
M 22,a[44:47],46
D 0,56,1024
M 18,a[32:35],42
s_cselect_b32 s93,s50,s93
R 34,2,10240
v_add_f32 v15,v15,v63
M 18,a[40:43],46
L 3
v_lshlrev_b32 v62,16,v61
v_add_f32 v16,v16,v62
M 18,a[48:51],50
s_cselect_b32 s88,8,s88
R 38,2,11264
v_and_b32 v63,0xffff0000,v61
M 18,a[56:59],54
buffer_load_dwordx4 v[58:61],v0,s[36:39],s74 offen offset:0 sc1
M 22,a[52:55],50
s_cselect_b32 s89,-1,s89
R 42,2,12288
v_add_f32 v17,v17,v63
M 22,a[60:63],54
L 3
v_cvt_pk_bf16_f32 v10,v10,v11
v_cvt_pk_bf16_f32 v11,v12,v13
M 22,a[68:71],26
s_mul_i32 s58,s61,2560
R 46,2,13312
v_cvt_pk_bf16_f32 v12,v14,v15
M 22,a[76:79],30
D 0,56,2048
M 18,a[64:67],26
s_add_u32 s58,s58,109891584
R 50,2,14336
v_cvt_pk_bf16_f32 v13,v16,v17
M 18,a[72:75],30
L 3
buffer_store_dwordx4 v[10:13],v0,s[36:39],s66 offen offset:0 sc0 sc1
M 18,a[80:83],34
s_lshl_b32 s62,s54,15
s_mov_b32 s67,s65
R 54,2,15360
M 18,a[88:91],38
s_cmp_ge_u32 s67,s45
s_cselect_b32 s51,1,-1
s_add_u32 s74,s49,s62
M 22,a[84:87],34
s_add_u32 s65,s47,s62
s_mov_b32 s54,s61
R 26,2,16384
M 22,a[92:95],38
L 3
M 22,a[100:103],42
R 30,2,17408
M 22,a[108:111],46
D 0,56,3072
M 18,a[96:99],42
s_add_u32 m0,s57,4096
s_add_u32 s56,s56,4096
R 34,2,18432
M 18,a[104:107],46
L 3
M 18,a[112:115],50
R 38,2,19456
M 18,a[120:123],54
M 22,a[116:119],50
R 42,2,20480
M 22,a[124:127],54
L 3
D 0,56,0
M 22,a[132:135],26
R 46,2,21504
M 22,a[140:143],30
M 18,a[128:131],26
R 50,2,22528
M 18,a[136:139],30
L 3
M 18,a[144:147],34
R 54,2,23552
M 18,a[152:155],38
M 22,a[148:151],34
R 26,2,24576
M 22,a[156:159],38
L 3
D 0,56,1024
M 22,a[164:167],42
R 30,2,25600
M 22,a[172:175],46
M 18,a[160:163],42
R 34,2,26624
M 18,a[168:171],46
L 3
M 18,a[176:179],50
s_waitcnt vmcnt(9)
R 38,2,27648
M 18,a[184:187],54
s_barrier
M 22,a[180:183],50
R 42,2,28672
M 22,a[188:191],54
L 3
D 0,56,2048
M 22,a[196:199],26
s_add_u32 m0,s59,1024
s_add_u32 s56,s55,28672
R 46,2,29696
M 22,a[204:207],30
s_mul_i32 s55,s61,114688
s_add_u32 s55,s55,s60
M 18,a[192:195],26
R 50,2,30720
M 18,a[200:203],30
L 3
M 18,a[208:211],34
R 54,2,31744
M 18,a[216:219],38
M 22,a[212:215],34
R 26,2,32768
M 22,a[220:223],38
L 3
buffer_load_dword v8,s[36:39],s58 offen offset:0 sc1 lds
M 22,a[228:231],42
s_add_u32 m0,s57,28672
R 30,2,33792
M 22,a[236:239],46
D 0,56,0
M 18,a[224:227],42
R 34,2,34816
M 18,a[232:235],46
L 3
M 18,a[240:243],50
R 38,2,35840
M 18,a[248:251],54
M 22,a[244:247],50
R 42,2,36864
M 22,a[252:255],54
L 3
D 0,56,1024
M 22,v[68:71],26
R 46,2,37888
M 22,v[76:79],30
M 18,v[64:67],26
R 50,2,38912
M 18,v[72:75],30
L 3
M 18,v[80:83],34
R 54,2,39936
M 18,v[88:91],38
D 0,56,2048
M 22,v[84:87],34
R 26,2,40960
M 22,v[92:95],38
L 3
M 22,v[100:103],42
R 30,2,41984
M 22,v[108:111],46
M 18,v[96:99],42
R 34,2,43008
M 18,v[104:107],46
L 3
D 0,56,3072
M 18,v[112:115],50
s_add_u32 m0,s57,32768
s_add_u32 s56,s56,4096
R 38,2,44032
M 18,v[120:123],54
s_sub_u32 s57,s84,s57
M 22,v[116:119],50
R 42,2,45056
M 22,v[124:127],54
L 3
M 22,v[132:135],26
R 46,2,46080
M 22,v[140:143],30
D 0,56,0
M 18,v[128:131],26
R 50,2,47104
M 18,v[136:139],30
L 3
M 18,v[144:147],34
R 54,2,48128
M 18,v[152:155],38
M 22,v[148:151],34
R 26,2,49152
M 22,v[156:159],38
L 3
D 0,56,1024
M 22,v[164:167],42
R 30,2,50176
M 22,v[172:175],46
M 18,v[160:163],42
R 34,2,51200
M 18,v[168:171],46
L 3
M 18,v[176:179],50
R 38,2,52224
M 18,v[184:187],54
D 0,56,2048
M 22,v[180:183],50
R 42,2,53248
M 22,v[188:191],54
L 3
M 22,v[196:199],26
R 46,2,54272
M 22,v[204:207],30
)"
R"(M 18,v[192:195],26
R 50,2,55296
M 18,v[200:203],30
L 3
M 18,v[208:211],34
s_waitcnt vmcnt(7)
R 54,2,56320
M 18,v[216:219],38
s_barrier
ds_read_b32 v7,v6 offset:1024
M 22,v[212:215],34
R 26,1,0
M 22,v[220:223],38
L 4
buffer_store_dword v4,v3,s[36:39],s69 offen offset:0 sc0 sc1
M 22,v[228:231],42
s_mov_b32 s69,s68
s_lshr_b32 s68,s62,4
R 30,1,1024
M 22,v[236:239],46
s_add_u32 s68,s68,s48
M 18,v[224:227],42
R 34,1,2048
M 18,v[232:235],46
L 4
L 3
M 18,v[240:243],50
R 38,1,3072
v_cmp_eq_u32 vcc,s53,v7
M 18,v[248:251],54
M 22,v[244:247],50
L 2
s_cmp_eq_u32 vcc_lo,s51
R 42,1,4096
M 22,v[252:255],54
s_cbranch_scc1 Lg4_F0_%=
Lg4_K0_%=:
s_cmp_eq_u32 s51,1
s_cbranch_scc0 Lg4_L0_%=
s_cmp_eq_u32 vcc_lo,-1
s_cbranch_scc1 Lg4_Fa0_%=
s_branch Lg4_La0_%=
Lg4_L0_%=:
v_mfma_f32_16x16x32_bf16 v[14:17],a[4:7],v[26:29],0
s_add_u32 m0,s59,3072
R 46,1,5120
v_lshlrev_b32 v62,16,v58
M 14,a[12:15],30
buffer_load_dword v8,s[36:39],s58 offen offset:0 sc1 lds
v_mfma_f32_16x16x32_bf16 v[10:13],a[0:3],v[26:29],0
s_mov_b32 s66,s65
R 50,1,6144
v_add_f32 v18,v18,v62
M 10,a[8:11],30
L 3
v_and_b32 v63,0xffff0000,v58
v_add_f32 v19,v19,v63
M 10,a[16:19],34
s_lshl_b32 s62,s54,15
R 54,1,7168
v_lshlrev_b32 v62,16,v59
M 10,a[24:27],38
s_cmp_ge_u32 s66,s45
v_add_f32 v20,v20,v62
v_and_b32 v63,0xffff0000,v59
M 14,a[20:23],34
s_cselect_b32 s51,1,-1
R 26,1,8192
v_add_f32 v21,v21,v63
M 14,a[28:31],38
L 3
v_lshlrev_b32 v62,16,v60
v_add_f32 v22,v22,v62
M 14,a[36:39],42
s_add_u32 s65,s47,s62
R 30,1,9216
v_and_b32 v63,0xffff0000,v60
M 14,a[44:47],46
v_add_f32 v23,v23,v63
v_lshlrev_b32 v62,16,v61
M 10,a[32:35],42
R 34,1,10240
v_add_f32 v24,v24,v62
M 10,a[40:43],46
L 3
v_and_b32 v63,0xffff0000,v61
v_add_f32 v25,v25,v63
M 10,a[48:51],50
R 38,1,11264
v_cvt_pk_bf16_f32 v18,v18,v19
M 10,a[56:59],54
buffer_load_dwordx4 v[58:61],v0,s[36:39],s74 offen offset:0 sc1
M 14,a[52:55],50
s_add_u32 s74,s49,s62
R 42,1,12288
v_cvt_pk_bf16_f32 v19,v20,v21
M 14,a[60:63],54
L 3
v_cvt_pk_bf16_f32 v20,v22,v23
v_cvt_pk_bf16_f32 v21,v24,v25
M 14,a[68:71],26
R 46,1,13312
M 14,a[76:79],30
buffer_store_dwordx4 v[18:21],v0,s[36:39],s67 offen offset:0 sc0 sc1
M 10,a[64:67],26
R 50,1,14336
M 10,a[72:75],30
L 3
M 10,a[80:83],34
R 54,1,15360
M 10,a[88:91],38
M 14,a[84:87],34
R 26,1,16384
M 14,a[92:95],38
L 3
M 14,a[100:103],42
R 30,1,17408
M 14,a[108:111],46
M 10,a[96:99],42
R 34,1,18432
M 10,a[104:107],46
L 3
M 10,a[112:115],50
R 38,1,19456
M 10,a[120:123],54
M 14,a[116:119],50
R 42,1,20480
M 14,a[124:127],54
L 3
M 14,a[132:135],26
R 46,1,21504
M 14,a[140:143],30
M 10,a[128:131],26
R 50,1,22528
M 10,a[136:139],30
L 3
M 10,a[144:147],34
R 54,1,23552
M 10,a[152:155],38
M 14,a[148:151],34
R 26,1,24576
M 14,a[156:159],38
L 3
M 14,a[164:167],42
R 30,1,25600
M 14,a[172:175],46
M 10,a[160:163],42
R 34,1,26624
M 10,a[168:171],46
L 3
M 10,a[176:179],50
s_waitcnt vmcnt(2)
R 38,1,27648
M 10,a[184:187],54
s_barrier
ds_read_b32 v7,v6 offset:3072
M 14,a[180:183],50
R 42,1,28672
M 14,a[188:191],54
L 4
M 14,a[196:199],26
R 46,1,29696
M 14,a[204:207],30
M 10,a[192:195],26
R 50,1,30720
M 10,a[200:203],30
L 4
L 3
v_cmp_eq_u32 vcc,s53,v7
M 10,a[208:211],34
R 54,1,31744
M 10,a[216:219],38
M 14,a[212:215],34
s_cmp_eq_u32 vcc_lo,-1
R 26,1,32768
s_cbranch_scc0 Lg4_X0_%=
M 14,a[220:223],38
L 3
s_add_u32 m0,s57,0
s_add_u32 s56,s55,0
M 14,a[228:231],42
s_add_u32 s61,s54,s46
s_cmp_ge_u32 s61,s63
s_cselect_b32 s61,s93,s61
R 30,1,33792
M 14,a[236:239],46
s_mul_i32 s58,s61,2560
D 0,56,0
M 10,a[224:227],42
s_cselect_b32 s46,s88,s46
s_cselect_b32 s63,s89,s63
R 34,1,34816
M 10,a[232:235],46
L 3
s_cselect_b32 s93,s50,s93
s_cselect_b32 s88,8,s88
M 10,a[240:243],50
s_cselect_b32 s89,-1,s89
s_add_u32 s58,s58,109891584
R 38,1,35840
M 10,a[248:251],54
D 0,56,1024
M 14,a[244:247],50
s_mov_b32 s54,s61
R 42,1,36864
M 14,a[252:255],54
L 3
M 14,v[68:71],26
R 46,1,37888
M 14,v[76:79],30
D 0,56,2048
M 10,v[64:67],26
R 50,1,38912
M 10,v[72:75],30
L 3
M 10,v[80:83],34
R 54,1,39936
M 10,v[88:91],38
D 0,56,3072
M 14,v[84:87],34
s_add_u32 m0,s57,4096
s_add_u32 s56,s56,4096
R 26,1,40960
M 14,v[92:95],38
L 3
M 14,v[100:103],42
R 30,1,41984
M 14,v[108:111],46
D 0,56,0
M 10,v[96:99],42
R 34,1,43008
M 10,v[104:107],46
L 3
M 10,v[112:115],50
R 38,1,44032
M 10,v[120:123],54
D 0,56,1024
M 14,v[116:119],50
R 42,1,45056
M 14,v[124:127],54
L 3
M 14,v[132:135],26
R 46,1,46080
M 14,v[140:143],30
D 0,56,2048
M 10,v[128:131],26
s_add_u32 m0,s59,0
s_add_u32 s56,s55,28672
R 50,1,47104
M 10,v[136:139],30
L 3
buffer_load_dword v8,s[36:39],s58 offen offset:0 sc1 lds
M 10,v[144:147],34
s_add_u32 m0,s57,28672
s_mul_i32 s55,s61,114688
R 54,1,48128
M 10,v[152:155],38
s_add_u32 s55,s55,s60
M 14,v[148:151],34
R 26,1,49152
M 14,v[156:159],38
L 3
D 0,56,0
M 14,v[164:167],42
R 30,1,50176
M 14,v[172:175],46
M 10,v[160:163],42
R 34,1,51200
M 10,v[168:171],46
L 3
D 0,56,1024
M 10,v[176:179],50
R 38,1,52224
M 10,v[184:187],54
D 0,56,2048
M 14,v[180:183],50
R 42,1,53248
M 14,v[188:191],54
L 3
M 14,v[196:199],26
R 46,1,54272
M 14,v[204:207],30
D 0,56,3072
M 10,v[192:195],26
s_add_u32 m0,s57,32768
s_add_u32 s56,s56,4096
R 50,1,55296
M 10,v[200:203],30
L 3
s_sub_u32 s57,s84,s57
M 10,v[208:211],34
s_waitcnt vmcnt(4)
R 54,1,56320
M 10,v[216:219],38
s_barrier
ds_read_b32 v7,v6 offset:0
M 14,v[212:215],34
R 26,2,0
M 14,v[220:223],38
L 4
buffer_store_dword v4,v3,s[36:39],s69 offen offset:0 sc0 sc1
M 14,v[228:231],42
s_mov_b32 s69,s68
s_lshr_b32 s68,s62,4
R 30,2,1024
M 14,v[236:239],46
D 0,56,0
M 10,v[224:227],42
s_add_u32 s68,s68,s48
R 34,2,2048
M 10,v[232:235],46
L 4
D 0,56,1024
M 10,v[240:243],50
L 3
R 38,2,3072
v_cmp_eq_u32 vcc,s53,v7
M 10,v[248:251],54
D 0,56,2048
M 14,v[244:247],50
L 2
s_cmp_eq_u32 vcc_lo,s51
R 42,2,4096
M 14,v[252:255],54
buffer_store_dword v4,v3,s[36:39],s71 offen offset:0 sc0 sc1
s_cbranch_scc1 Lg4_F1_%=
s_branch Lg4_K1_%=
Lg4_X0_%=:
M 14,a[220:223],38
L 3
s_add_u32 m0,s59,0
M 14,a[228:231],42
R 30,1,33792
M 14,a[236:239],46
buffer_load_dword v8,s[36:39],s58 offen offset:0 sc1 lds
M 10,a[224:227],42
R 34,1,34816
M 10,a[232:235],46
L 3
M 10,a[240:243],50
R 38,1,35840
M 10,a[248:251],54
M 14,a[244:247],50
R 42,1,36864
M 14,a[252:255],54
L 3
M 14,v[68:71],26
R 46,1,37888
M 14,v[76:79],30
M 10,v[64:67],26
R 50,1,38912
M 10,v[72:75],30
L 3
M 10,v[80:83],34
R 54,1,39936
M 10,v[88:91],38
M 14,v[84:87],34
R 26,1,40960
M 14,v[92:95],38
L 3
M 14,v[100:103],42
R 30,1,41984
M 14,v[108:111],46
M 10,v[96:99],42
R 34,1,43008
M 10,v[104:107],46
L 3
M 10,v[112:115],50
R 38,1,44032
M 10,v[120:123],54
M 14,v[116:119],50
R 42,1,45056
M 14,v[124:127],54
L 3
M 14,v[132:135],26
R 46,1,46080
M 14,v[140:143],30
M 10,v[128:131],26
R 50,1,47104
M 10,v[136:139],30
L 3
M 10,v[144:147],34
R 54,1,48128
M 10,v[152:155],38
M 14,v[148:151],34
R 26,1,49152
M 14,v[156:159],38
L 3
M 14,v[164:167],42
R 30,1,50176
M 14,v[172:175],46
M 10,v[160:163],42
R 34,1,51200
M 10,v[168:171],46
L 3
M 10,v[176:179],50
R 38,1,52224
M 10,v[184:187],54
M 14,v[180:183],50
R 42,1,53248
M 14,v[188:191],54
L 3
M 14,v[196:199],26
R 46,1,54272
M 14,v[204:207],30
M 10,v[192:195],26
R 50,1,55296
M 10,v[200:203],30
L 3
M 10,v[208:211],34
s_waitcnt vmcnt(0)
R 54,1,56320
M 10,v[216:219],38
s_barrier
buffer_store_dword v4,v3,s[36:39],s69 offen offset:0 sc0 sc1
M 14,v[212:215],34
s_mov_b32 s69,s68
s_lshr_b32 s68,s62,4
ds_read_b32 v7,v6 offset:0
M 14,v[220:223],38
L 3
s_add_u32 s68,s68,s48
M 14,v[228:231],42
M 14,v[236:239],46
M 10,v[224:227],42
M 10,v[232:235],46
L 1
M 10,v[240:243],50
L 0
v_cmp_eq_u32 vcc,s53,v7
M 10,v[248:251],54
M 14,v[244:247],50
M 14,v[252:255],54
s_cmp_eq_u32 s51,1
s_cbranch_scc1 Lg4_pa0_%=
s_mov_b32 s80,1
s_cmp_eq_u32 vcc_lo,-1
s_cbranch_scc0 Lg4_xn0_%=
s_add_u32 m0,s57,0
s_add_u32 s56,s55,0
D 0,56,0
v_lshlrev_b32 v62,16,v58
D 0,56,1024
v_add_f32 v10,v10,v62
D 0,56,2048
v_and_b32 v63,0xffff0000,v58
D 0,56,3072
v_add_f32 v11,v11,v63
s_add_u32 m0,s57,4096
s_add_u32 s56,s56,4096
D 0,56,0
v_lshlrev_b32 v62,16,v59
D 0,56,1024
v_add_f32 v12,v12,v62
D 0,56,2048
v_and_b32 v63,0xffff0000,v59
s_add_u32 s61,s54,s46
s_cmp_ge_u32 s61,s63
s_cselect_b32 s61,s93,s61
s_cselect_b32 s46,s88,s46
s_cselect_b32 s63,s89,s63
s_cselect_b32 s93,s50,s93
s_cselect_b32 s88,8,s88
s_cselect_b32 s89,-1,s89
s_mul_i32 s58,s61,2560
s_add_u32 s58,s58,109891584
s_add_u32 m0,s57,28672
s_add_u32 s56,s55,28672
D 0,56,0
v_add_f32 v13,v13,v63
D 0,56,1024
v_lshlrev_b32 v62,16,v60
D 0,56,2048
v_add_f32 v14,v14,v62
D 0,56,3072
v_and_b32 v63,0xffff0000,v60
s_add_u32 m0,s57,32768
s_add_u32 s56,s56,4096
D 0,56,0
v_add_f32 v15,v15,v63
D 0,56,1024
v_lshlrev_b32 v62,16,v61
D 0,56,2048
v_add_f32 v16,v16,v62
s_mul_i32 s55,s61,114688
s_add_u32 s55,s55,s60
s_sub_u32 s57,s84,s57
s_mov_b32 s54,s61
v_and_b32 v63,0xffff0000,v61
v_add_f32 v17,v17,v63
v_cvt_pk_bf16_f32 v10,v10,v11
v_cvt_pk_bf16_f32 v11,v12,v13
v_cvt_pk_bf16_f32 v12,v14,v15
v_cvt_pk_bf16_f32 v13,v16,v17
buffer_store_dwordx4 v[10:13],v0,s[36:39],s66 offen offset:0 sc0 sc1
s_mov_b32 s72,s69
s_mov_b32 s69,0x7fff0000
s_mov_b32 s66,0x7fff0000
s_mov_b32 s76,0x7fff0000
s_branch Lg4_gox_%=
)"
R"(Lg4_xn0_%=:
s_cmp_ge_u32 s54,256
s_cbranch_scc1 Lg4_fin0_%=
s_add_u32 m0,s59,4096
s_nop 0
buffer_load_dword v8,s[36:39],s58 offen offset:0 sc1 lds
buffer_store_dword v4,v3,s[36:39],s71 offen offset:0 sc0 sc1
s_add_u32 m0,s59,5120
s_nop 0
buffer_load_dword v8,s[36:39],s58 offen offset:0 sc1 lds
buffer_store_dword v4,v3,s[36:39],s71 offen offset:0 sc0 sc1
s_mov_b32 s83,0
v_lshlrev_b32 v62,16,v58
v_add_f32 v10,v10,v62
v_and_b32 v63,0xffff0000,v58
v_add_f32 v11,v11,v63
v_lshlrev_b32 v62,16,v59
v_add_f32 v12,v12,v62
v_and_b32 v63,0xffff0000,v59
v_add_f32 v13,v13,v63
v_lshlrev_b32 v62,16,v60
v_add_f32 v14,v14,v62
v_and_b32 v63,0xffff0000,v60
v_add_f32 v15,v15,v63
v_lshlrev_b32 v62,16,v61
v_add_f32 v16,v16,v62
v_and_b32 v63,0xffff0000,v61
v_add_f32 v17,v17,v63
v_cvt_pk_bf16_f32 v10,v10,v11
v_cvt_pk_bf16_f32 v11,v12,v13
v_cvt_pk_bf16_f32 v12,v14,v15
v_cvt_pk_bf16_f32 v13,v16,v17
buffer_store_dwordx4 v[10:13],v0,s[36:39],s66 offen offset:0 sc0 sc1
s_mov_b32 s72,s69
s_mov_b32 s69,0x7fff0000
s_mov_b32 s66,0x7fff0000
s_mov_b32 s76,0x7fff0000
s_branch Lg4_poll_%=
Lg4_fin0_%=:
v_lshlrev_b32 v62,16,v58
v_add_f32 v10,v10,v62
v_and_b32 v63,0xffff0000,v58
v_add_f32 v11,v11,v63
v_lshlrev_b32 v62,16,v59
v_add_f32 v12,v12,v62
v_and_b32 v63,0xffff0000,v59
v_add_f32 v13,v13,v63
v_lshlrev_b32 v62,16,v60
v_add_f32 v14,v14,v62
v_and_b32 v63,0xffff0000,v60
v_add_f32 v15,v15,v63
v_lshlrev_b32 v62,16,v61
v_add_f32 v16,v16,v62
v_and_b32 v63,0xffff0000,v61
v_add_f32 v17,v17,v63
v_cvt_pk_bf16_f32 v10,v10,v11
v_cvt_pk_bf16_f32 v11,v12,v13
v_cvt_pk_bf16_f32 v12,v14,v15
v_cvt_pk_bf16_f32 v13,v16,v17
buffer_store_dwordx4 v[10:13],v0,s[36:39],s66 offen offset:0 sc0 sc1
s_mov_b32 s72,s69
s_mov_b32 s69,0x7fff0000
s_mov_b32 s66,0x7fff0000
s_mov_b32 s76,0x7fff0000
s_branch Lg4_done_%=
Lg4_K1_%=:
s_cmp_eq_u32 s51,1
s_cbranch_scc0 Lg4_L1_%=
s_cmp_eq_u32 vcc_lo,-1
s_cbranch_scc1 Lg4_Fa1_%=
s_branch Lg4_La1_%=
Lg4_L1_%=:
v_mfma_f32_16x16x32_bf16 v[22:25],a[4:7],v[26:29],0
s_add_u32 m0,s59,3072
R 46,2,5120
v_lshlrev_b32 v62,16,v58
M 22,a[12:15],30
buffer_load_dword v8,s[36:39],s58 offen offset:0 sc1 lds
v_mfma_f32_16x16x32_bf16 v[18:21],a[0:3],v[26:29],0
s_mov_b32 s67,s65
R 50,2,6144
v_add_f32 v10,v10,v62
M 18,a[8:11],30
L 3
v_and_b32 v63,0xffff0000,v58
v_add_f32 v11,v11,v63
M 18,a[16:19],34
s_lshl_b32 s62,s54,15
R 54,2,7168
v_lshlrev_b32 v62,16,v59
M 18,a[24:27],38
s_cmp_ge_u32 s67,s45
v_add_f32 v12,v12,v62
v_and_b32 v63,0xffff0000,v59
M 22,a[20:23],34
s_cselect_b32 s51,1,-1
R 26,2,8192
v_add_f32 v13,v13,v63
M 22,a[28:31],38
L 3
v_lshlrev_b32 v62,16,v60
v_add_f32 v14,v14,v62
M 22,a[36:39],42
s_add_u32 s65,s47,s62
R 30,2,9216
v_and_b32 v63,0xffff0000,v60
M 22,a[44:47],46
v_add_f32 v15,v15,v63
v_lshlrev_b32 v62,16,v61
M 18,a[32:35],42
R 34,2,10240
v_add_f32 v16,v16,v62
M 18,a[40:43],46
L 3
v_and_b32 v63,0xffff0000,v61
v_add_f32 v17,v17,v63
M 18,a[48:51],50
R 38,2,11264
v_cvt_pk_bf16_f32 v10,v10,v11
M 18,a[56:59],54
buffer_load_dwordx4 v[58:61],v0,s[36:39],s74 offen offset:0 sc1
M 22,a[52:55],50
s_add_u32 s74,s49,s62
R 42,2,12288
v_cvt_pk_bf16_f32 v11,v12,v13
M 22,a[60:63],54
L 3
v_cvt_pk_bf16_f32 v12,v14,v15
v_cvt_pk_bf16_f32 v13,v16,v17
M 22,a[68:71],26
R 46,2,13312
M 22,a[76:79],30
buffer_store_dwordx4 v[10:13],v0,s[36:39],s66 offen offset:0 sc0 sc1
M 18,a[64:67],26
R 50,2,14336
M 18,a[72:75],30
L 3
M 18,a[80:83],34
R 54,2,15360
M 18,a[88:91],38
M 22,a[84:87],34
R 26,2,16384
M 22,a[92:95],38
L 3
M 22,a[100:103],42
R 30,2,17408
M 22,a[108:111],46
M 18,a[96:99],42
R 34,2,18432
M 18,a[104:107],46
L 3
M 18,a[112:115],50
R 38,2,19456
M 18,a[120:123],54
M 22,a[116:119],50
R 42,2,20480
M 22,a[124:127],54
L 3
M 22,a[132:135],26
R 46,2,21504
M 22,a[140:143],30
M 18,a[128:131],26
R 50,2,22528
M 18,a[136:139],30
L 3
M 18,a[144:147],34
R 54,2,23552
M 18,a[152:155],38
M 22,a[148:151],34
R 26,2,24576
M 22,a[156:159],38
L 3
M 22,a[164:167],42
R 30,2,25600
M 22,a[172:175],46
M 18,a[160:163],42
R 34,2,26624
M 18,a[168:171],46
L 3
M 18,a[176:179],50
s_waitcnt vmcnt(2)
R 38,2,27648
M 18,a[184:187],54
s_barrier
ds_read_b32 v7,v6 offset:3072
M 22,a[180:183],50
R 42,2,28672
M 22,a[188:191],54
L 4
M 22,a[196:199],26
R 46,2,29696
M 22,a[204:207],30
M 18,a[192:195],26
R 50,2,30720
M 18,a[200:203],30
L 4
L 3
v_cmp_eq_u32 vcc,s53,v7
M 18,a[208:211],34
R 54,2,31744
M 18,a[216:219],38
M 22,a[212:215],34
s_cmp_eq_u32 vcc_lo,-1
R 26,2,32768
s_cbranch_scc0 Lg4_X1_%=
M 22,a[220:223],38
L 3
s_add_u32 m0,s57,0
s_add_u32 s56,s55,0
M 22,a[228:231],42
s_add_u32 s61,s54,s46
s_cmp_ge_u32 s61,s63
s_cselect_b32 s61,s93,s61
R 30,2,33792
M 22,a[236:239],46
s_mul_i32 s58,s61,2560
D 0,56,0
M 18,a[224:227],42
s_cselect_b32 s46,s88,s46
s_cselect_b32 s63,s89,s63
R 34,2,34816
M 18,a[232:235],46
L 3
s_cselect_b32 s93,s50,s93
s_cselect_b32 s88,8,s88
M 18,a[240:243],50
s_cselect_b32 s89,-1,s89
s_add_u32 s58,s58,109891584
R 38,2,35840
M 18,a[248:251],54
D 0,56,1024
M 22,a[244:247],50
s_mov_b32 s54,s61
R 42,2,36864
M 22,a[252:255],54
L 3
M 22,v[68:71],26
R 46,2,37888
M 22,v[76:79],30
D 0,56,2048
M 18,v[64:67],26
R 50,2,38912
M 18,v[72:75],30
L 3
M 18,v[80:83],34
R 54,2,39936
M 18,v[88:91],38
D 0,56,3072
M 22,v[84:87],34
s_add_u32 m0,s57,4096
s_add_u32 s56,s56,4096
R 26,2,40960
M 22,v[92:95],38
L 3
M 22,v[100:103],42
R 30,2,41984
M 22,v[108:111],46
D 0,56,0
M 18,v[96:99],42
R 34,2,43008
M 18,v[104:107],46
L 3
M 18,v[112:115],50
R 38,2,44032
M 18,v[120:123],54
D 0,56,1024
M 22,v[116:119],50
R 42,2,45056
M 22,v[124:127],54
L 3
M 22,v[132:135],26
R 46,2,46080
M 22,v[140:143],30
D 0,56,2048
M 18,v[128:131],26
s_add_u32 m0,s59,1024
s_add_u32 s56,s55,28672
R 50,2,47104
M 18,v[136:139],30
L 3
buffer_load_dword v8,s[36:39],s58 offen offset:0 sc1 lds
M 18,v[144:147],34
s_add_u32 m0,s57,28672
s_mul_i32 s55,s61,114688
R 54,2,48128
M 18,v[152:155],38
s_add_u32 s55,s55,s60
M 22,v[148:151],34
R 26,2,49152
M 22,v[156:159],38
L 3
D 0,56,0
M 22,v[164:167],42
R 30,2,50176
M 22,v[172:175],46
M 18,v[160:163],42
R 34,2,51200
M 18,v[168:171],46
L 3
D 0,56,1024
M 18,v[176:179],50
R 38,2,52224
M 18,v[184:187],54
D 0,56,2048
M 22,v[180:183],50
R 42,2,53248
M 22,v[188:191],54
L 3
M 22,v[196:199],26
R 46,2,54272
M 22,v[204:207],30
D 0,56,3072
M 18,v[192:195],26
s_add_u32 m0,s57,32768
s_add_u32 s56,s56,4096
R 50,2,55296
M 18,v[200:203],30
L 3
s_sub_u32 s57,s84,s57
M 18,v[208:211],34
s_waitcnt vmcnt(4)
R 54,2,56320
M 18,v[216:219],38
s_barrier
ds_read_b32 v7,v6 offset:1024
M 22,v[212:215],34
R 26,1,0
M 22,v[220:223],38
L 4
buffer_store_dword v4,v3,s[36:39],s69 offen offset:0 sc0 sc1
M 22,v[228:231],42
s_mov_b32 s69,s68
s_lshr_b32 s68,s62,4
R 30,1,1024
M 22,v[236:239],46
D 0,56,0
M 18,v[224:227],42
s_add_u32 s68,s68,s48
R 34,1,2048
M 18,v[232:235],46
L 4
D 0,56,1024
M 18,v[240:243],50
L 3
R 38,1,3072
v_cmp_eq_u32 vcc,s53,v7
M 18,v[248:251],54
D 0,56,2048
M 22,v[244:247],50
L 2
s_cmp_eq_u32 vcc_lo,s51
R 42,1,4096
M 22,v[252:255],54
buffer_store_dword v4,v3,s[36:39],s71 offen offset:0 sc0 sc1
s_cbranch_scc1 Lg4_F0_%=
s_branch Lg4_K0_%=
Lg4_X1_%=:
M 22,a[220:223],38
L 3
s_add_u32 m0,s59,1024
M 22,a[228:231],42
R 30,2,33792
M 22,a[236:239],46
buffer_load_dword v8,s[36:39],s58 offen offset:0 sc1 lds
M 18,a[224:227],42
R 34,2,34816
M 18,a[232:235],46
L 3
M 18,a[240:243],50
R 38,2,35840
M 18,a[248:251],54
M 22,a[244:247],50
R 42,2,36864
M 22,a[252:255],54
L 3
M 22,v[68:71],26
R 46,2,37888
M 22,v[76:79],30
M 18,v[64:67],26
R 50,2,38912
M 18,v[72:75],30
L 3
M 18,v[80:83],34
R 54,2,39936
M 18,v[88:91],38
M 22,v[84:87],34
R 26,2,40960
M 22,v[92:95],38
L 3
M 22,v[100:103],42
R 30,2,41984
M 22,v[108:111],46
M 18,v[96:99],42
R 34,2,43008
M 18,v[104:107],46
L 3
M 18,v[112:115],50
R 38,2,44032
M 18,v[120:123],54
M 22,v[116:119],50
R 42,2,45056
M 22,v[124:127],54
L 3
M 22,v[132:135],26
R 46,2,46080
M 22,v[140:143],30
M 18,v[128:131],26
R 50,2,47104
M 18,v[136:139],30
L 3
M 18,v[144:147],34
R 54,2,48128
M 18,v[152:155],38
M 22,v[148:151],34
R 26,2,49152
M 22,v[156:159],38
L 3
M 22,v[164:167],42
R 30,2,50176
M 22,v[172:175],46
M 18,v[160:163],42
R 34,2,51200
M 18,v[168:171],46
L 3
M 18,v[176:179],50
R 38,2,52224
M 18,v[184:187],54
M 22,v[180:183],50
R 42,2,53248
M 22,v[188:191],54
L 3
M 22,v[196:199],26
R 46,2,54272
M 22,v[204:207],30
M 18,v[192:195],26
R 50,2,55296
M 18,v[200:203],30
L 3
M 18,v[208:211],34
s_waitcnt vmcnt(0)
R 54,2,56320
M 18,v[216:219],38
s_barrier
buffer_store_dword v4,v3,s[36:39],s69 offen offset:0 sc0 sc1
M 22,v[212:215],34
s_mov_b32 s69,s68
s_lshr_b32 s68,s62,4
ds_read_b32 v7,v6 offset:1024
M 22,v[220:223],38
L 3
s_add_u32 s68,s68,s48
M 22,v[228:231],42
M 22,v[236:239],46
M 18,v[224:227],42
M 18,v[232:235],46
L 1
M 18,v[240:243],50
L 0
v_cmp_eq_u32 vcc,s53,v7
M 18,v[248:251],54
M 22,v[244:247],50
M 22,v[252:255],54
s_cmp_eq_u32 s51,1
s_cbranch_scc1 Lg4_pa1_%=
s_mov_b32 s80,0
s_cmp_eq_u32 vcc_lo,-1
s_cbranch_scc0 Lg4_xn1_%=
s_add_u32 m0,s57,0
s_add_u32 s56,s55,0
D 0,56,0
v_lshlrev_b32 v62,16,v58
D 0,56,1024
v_add_f32 v18,v18,v62
D 0,56,2048
v_and_b32 v63,0xffff0000,v58
D 0,56,3072
v_add_f32 v19,v19,v63
s_add_u32 m0,s57,4096
s_add_u32 s56,s56,4096
D 0,56,0
v_lshlrev_b32 v62,16,v59
D 0,56,1024
v_add_f32 v20,v20,v62
D 0,56,2048
v_and_b32 v63,0xffff0000,v59
s_add_u32 s61,s54,s46
s_cmp_ge_u32 s61,s63
s_cselect_b32 s61,s93,s61
s_cselect_b32 s46,s88,s46
s_cselect_b32 s63,s89,s63
s_cselect_b32 s93,s50,s93
s_cselect_b32 s88,8,s88
s_cselect_b32 s89,-1,s89
s_mul_i32 s58,s61,2560
s_add_u32 s58,s58,109891584
s_add_u32 m0,s57,28672
s_add_u32 s56,s55,28672
D 0,56,0
v_add_f32 v21,v21,v63
D 0,56,1024
)"
R"(v_lshlrev_b32 v62,16,v60
D 0,56,2048
v_add_f32 v22,v22,v62
D 0,56,3072
v_and_b32 v63,0xffff0000,v60
s_add_u32 m0,s57,32768
s_add_u32 s56,s56,4096
D 0,56,0
v_add_f32 v23,v23,v63
D 0,56,1024
v_lshlrev_b32 v62,16,v61
D 0,56,2048
v_add_f32 v24,v24,v62
s_mul_i32 s55,s61,114688
s_add_u32 s55,s55,s60
s_sub_u32 s57,s84,s57
s_mov_b32 s54,s61
v_and_b32 v63,0xffff0000,v61
v_add_f32 v25,v25,v63
v_cvt_pk_bf16_f32 v18,v18,v19
v_cvt_pk_bf16_f32 v19,v20,v21
v_cvt_pk_bf16_f32 v20,v22,v23
v_cvt_pk_bf16_f32 v21,v24,v25
buffer_store_dwordx4 v[18:21],v0,s[36:39],s67 offen offset:0 sc0 sc1
s_mov_b32 s72,s69
s_mov_b32 s69,0x7fff0000
s_mov_b32 s67,0x7fff0000
s_mov_b32 s77,0x7fff0000
s_branch Lg4_gox_%=
Lg4_xn1_%=:
s_cmp_ge_u32 s54,256
s_cbranch_scc1 Lg4_fin1_%=
s_add_u32 m0,s59,4096
s_nop 0
buffer_load_dword v8,s[36:39],s58 offen offset:0 sc1 lds
buffer_store_dword v4,v3,s[36:39],s71 offen offset:0 sc0 sc1
s_add_u32 m0,s59,5120
s_nop 0
buffer_load_dword v8,s[36:39],s58 offen offset:0 sc1 lds
buffer_store_dword v4,v3,s[36:39],s71 offen offset:0 sc0 sc1
s_mov_b32 s83,0
v_lshlrev_b32 v62,16,v58
v_add_f32 v18,v18,v62
v_and_b32 v63,0xffff0000,v58
v_add_f32 v19,v19,v63
v_lshlrev_b32 v62,16,v59
v_add_f32 v20,v20,v62
v_and_b32 v63,0xffff0000,v59
v_add_f32 v21,v21,v63
v_lshlrev_b32 v62,16,v60
v_add_f32 v22,v22,v62
v_and_b32 v63,0xffff0000,v60
v_add_f32 v23,v23,v63
v_lshlrev_b32 v62,16,v61
v_add_f32 v24,v24,v62
v_and_b32 v63,0xffff0000,v61
v_add_f32 v25,v25,v63
v_cvt_pk_bf16_f32 v18,v18,v19
v_cvt_pk_bf16_f32 v19,v20,v21
v_cvt_pk_bf16_f32 v20,v22,v23
v_cvt_pk_bf16_f32 v21,v24,v25
buffer_store_dwordx4 v[18:21],v0,s[36:39],s67 offen offset:0 sc0 sc1
s_mov_b32 s72,s69
s_mov_b32 s69,0x7fff0000
s_mov_b32 s67,0x7fff0000
s_mov_b32 s77,0x7fff0000
s_branch Lg4_poll_%=
Lg4_fin1_%=:
v_lshlrev_b32 v62,16,v58
v_add_f32 v18,v18,v62
v_and_b32 v63,0xffff0000,v58
v_add_f32 v19,v19,v63
v_lshlrev_b32 v62,16,v59
v_add_f32 v20,v20,v62
v_and_b32 v63,0xffff0000,v59
v_add_f32 v21,v21,v63
v_lshlrev_b32 v62,16,v60
v_add_f32 v22,v22,v62
v_and_b32 v63,0xffff0000,v60
v_add_f32 v23,v23,v63
v_lshlrev_b32 v62,16,v61
v_add_f32 v24,v24,v62
v_and_b32 v63,0xffff0000,v61
v_add_f32 v25,v25,v63
v_cvt_pk_bf16_f32 v18,v18,v19
v_cvt_pk_bf16_f32 v19,v20,v21
v_cvt_pk_bf16_f32 v20,v22,v23
v_cvt_pk_bf16_f32 v21,v24,v25
buffer_store_dwordx4 v[18:21],v0,s[36:39],s67 offen offset:0 sc0 sc1
s_mov_b32 s72,s69
s_mov_b32 s69,0x7fff0000
s_mov_b32 s67,0x7fff0000
s_mov_b32 s77,0x7fff0000
s_branch Lg4_done_%=
Lg4_Fa0_%=:
s_nop 0
v_mfma_f32_16x16x32_bf16 v[14:17],a[4:7],v[26:29],0
s_add_u32 m0,s57,0
R 46,1,5120
v_lshlrev_b32 v62,16,v58
M 14,a[12:15],30
s_add_u32 s56,s55,0
v_add_f32 v18,v18,v62
v_and_b32 v63,0xffff0000,v58
v_mfma_f32_16x16x32_bf16 v[10:13],a[0:3],v[26:29],0
s_add_u32 s61,s54,s46
R 50,1,6144
v_add_f32 v19,v19,v63
M 10,a[8:11],30
L 3
D 0,56,0
M 10,a[16:19],34
s_cmp_ge_u32 s61,s63
R 54,1,7168
v_lshlrev_b32 v62,16,v59
M 10,a[24:27],38
s_cselect_b32 s61,s93,s61
v_add_f32 v20,v20,v62
v_and_b32 v63,0xffff0000,v59
M 14,a[20:23],34
s_cselect_b32 s46,s88,s46
R 26,1,8192
v_add_f32 v21,v21,v63
M 14,a[28:31],38
L 3
v_lshlrev_b32 v62,16,v60
v_add_f32 v22,v22,v62
M 14,a[36:39],42
s_cselect_b32 s63,s89,s63
R 30,1,9216
v_and_b32 v63,0xffff0000,v60
M 14,a[44:47],46
D 0,56,1024
M 10,a[32:35],42
s_cselect_b32 s93,s50,s93
R 34,1,10240
v_add_f32 v23,v23,v63
M 10,a[40:43],46
L 3
v_lshlrev_b32 v62,16,v61
v_add_f32 v24,v24,v62
M 10,a[48:51],50
s_cselect_b32 s88,8,s88
R 38,1,11264
v_and_b32 v63,0xffff0000,v61
M 10,a[56:59],54
buffer_load_dwordx4 v[58:61],v0,s[36:39],s74 offen offset:0 sc1
M 14,a[52:55],50
s_cselect_b32 s89,-1,s89
R 42,1,12288
v_add_f32 v25,v25,v63
M 14,a[60:63],54
L 3
v_cvt_pk_bf16_f32 v18,v18,v19
v_cvt_pk_bf16_f32 v19,v20,v21
M 14,a[68:71],26
s_mul_i32 s58,s61,2560
R 46,1,13312
v_cvt_pk_bf16_f32 v20,v22,v23
M 14,a[76:79],30
D 0,56,2048
M 10,a[64:67],26
s_add_u32 s58,s58,109891584
R 50,1,14336
v_cvt_pk_bf16_f32 v21,v24,v25
M 10,a[72:75],30
L 3
v_lshrrev_b32 v62,8,v0
v_lshl_add_u32 v62,v62,5,v0
M 10,a[80:83],34
s_sub_u32 s75,s67,s85
R 54,1,15360
v_add_u32 v62,s73,v62
M 10,a[88:91],38
s_mov_b64 s[40:41],s[86:87]
ds_write_b128 v62,v[18:21] offset:0
M 14,a[84:87],34
s_mov_b32 s42,4194304
R 26,1,16384
ds_read2_b32 v[22:23],v5 offset0:0 offset1:16
M 14,a[92:95],38
L 5
s_mov_b32 s43,0x00020000
ds_read2_b32 v[24:25],v5 offset0:32 offset1:48
M 14,a[100:103],42
s_add_u32 s64,s75,0
s_lshl_b32 s62,s54,15
R 30,1,17408
M 14,a[108:111],46
D 0,56,3072
M 10,a[96:99],42
s_add_u32 m0,s57,4096
s_add_u32 s56,s56,4096
R 34,1,18432
M 10,a[104:107],46
L 6
s_mov_b32 s66,s65
s_add_u32 s74,s49,s62
M 10,a[112:115],50
s_add_u32 s65,s47,s62
s_mov_b32 s54,s61
R 38,1,19456
M 10,a[120:123],54
L 3
M 14,a[116:119],50
R 42,1,20480
M 14,a[124:127],54
L 3
D 0,56,0
M 14,a[132:135],26
R 46,1,21504
M 14,a[140:143],30
M 10,a[128:131],26
R 50,1,22528
M 10,a[136:139],30
L 3
M 10,a[144:147],34
R 54,1,23552
M 10,a[152:155],38
M 14,a[148:151],34
R 26,1,24576
M 14,a[156:159],38
L 3
D 0,56,1024
M 14,a[164:167],42
R 30,1,25600
M 14,a[172:175],46
M 10,a[160:163],42
R 34,1,26624
M 10,a[168:171],46
L 3
M 10,a[176:179],50
s_waitcnt vmcnt(8)
R 38,1,27648
M 10,a[184:187],54
s_barrier
M 14,a[180:183],50
R 42,1,28672
M 14,a[188:191],54
L 3
D 0,56,2048
M 14,a[196:199],26
s_add_u32 m0,s59,0
s_add_u32 s56,s55,28672
R 46,1,29696
M 14,a[204:207],30
s_mul_i32 s55,s61,114688
s_add_u32 s55,s55,s60
M 10,a[192:195],26
R 50,1,30720
M 10,a[200:203],30
L 3
M 10,a[208:211],34
R 54,1,31744
M 10,a[216:219],38
M 14,a[212:215],34
R 26,1,32768
M 14,a[220:223],38
L 3
buffer_load_dword v8,s[36:39],s58 offen offset:0 sc1 lds
M 14,a[228:231],42
s_add_u32 m0,s57,28672
R 30,1,33792
M 14,a[236:239],46
D 0,56,0
M 10,a[224:227],42
R 34,1,34816
M 10,a[232:235],46
L 3
M 10,a[240:243],50
R 38,1,35840
M 10,a[248:251],54
M 14,a[244:247],50
R 42,1,36864
M 14,a[252:255],54
L 3
D 0,56,1024
M 14,v[68:71],26
R 46,1,37888
M 14,v[76:79],30
M 10,v[64:67],26
R 50,1,38912
M 10,v[72:75],30
L 3
M 10,v[80:83],34
R 54,1,39936
M 10,v[88:91],38
D 0,56,2048
M 14,v[84:87],34
R 26,1,40960
M 14,v[92:95],38
L 3
M 14,v[100:103],42
R 30,1,41984
M 14,v[108:111],46
M 10,v[96:99],42
R 34,1,43008
M 10,v[104:107],46
L 3
D 0,56,3072
M 10,v[112:115],50
s_add_u32 m0,s57,32768
s_add_u32 s56,s56,4096
R 38,1,44032
M 10,v[120:123],54
s_sub_u32 s57,s84,s57
M 14,v[116:119],50
R 42,1,45056
M 14,v[124:127],54
L 3
M 14,v[132:135],26
R 46,1,46080
M 14,v[140:143],30
D 0,56,0
M 10,v[128:131],26
R 50,1,47104
M 10,v[136:139],30
L 3
M 10,v[144:147],34
R 54,1,48128
M 10,v[152:155],38
M 14,v[148:151],34
R 26,1,49152
M 14,v[156:159],38
L 3
D 0,56,1024
M 14,v[164:167],42
R 30,1,50176
M 14,v[172:175],46
M 10,v[160:163],42
R 34,1,51200
M 10,v[168:171],46
L 3
M 10,v[176:179],50
R 38,1,52224
M 10,v[184:187],54
D 0,56,2048
M 14,v[180:183],50
R 42,1,53248
M 14,v[188:191],54
L 3
M 14,v[196:199],26
R 46,1,54272
M 14,v[204:207],30
M 10,v[192:195],26
R 50,1,55296
M 10,v[200:203],30
L 3
M 10,v[208:211],34
s_waitcnt vmcnt(7)
R 54,1,56320
M 10,v[216:219],38
s_barrier
ds_read_b32 v7,v6 offset:0
M 14,v[212:215],34
R 26,2,0
M 14,v[220:223],38
L 4
M 14,v[228:231],42
R 30,2,1024
M 14,v[236:239],46
buffer_atomic_pk_add_bf16 v22,v9,s[40:43],s64 offen sc1
M 10,v[224:227],42
s_add_u32 s64,s75,8192
R 34,2,2048
M 10,v[232:235],46
L 4
buffer_atomic_pk_add_bf16 v23,v9,s[40:43],s64 offen sc1
M 10,v[240:243],50
s_add_u32 s64,s75,16384
L 3
R 38,2,3072
v_cmp_eq_u32 vcc,s53,v7
M 10,v[248:251],54
buffer_atomic_pk_add_bf16 v24,v9,s[40:43],s64 offen sc1
M 14,v[244:247],50
L 2
s_add_u32 s64,s75,24576
s_cmp_eq_u32 vcc_lo,-1
R 42,2,4096
M 14,v[252:255],54
buffer_atomic_pk_add_bf16 v25,v9,s[40:43],s64 offen sc1
s_cbranch_scc0 Lg4_Ka1_%=
Lg4_Fa1_%=:
s_nop 0
v_mfma_f32_16x16x32_bf16 v[22:25],a[4:7],v[26:29],0
s_add_u32 m0,s57,0
R 46,2,5120
v_lshlrev_b32 v62,16,v58
M 22,a[12:15],30
s_add_u32 s56,s55,0
v_add_f32 v10,v10,v62
v_and_b32 v63,0xffff0000,v58
v_mfma_f32_16x16x32_bf16 v[18:21],a[0:3],v[26:29],0
s_add_u32 s61,s54,s46
R 50,2,6144
v_add_f32 v11,v11,v63
M 18,a[8:11],30
L 3
D 0,56,0
M 18,a[16:19],34
s_cmp_ge_u32 s61,s63
R 54,2,7168
v_lshlrev_b32 v62,16,v59
M 18,a[24:27],38
s_cselect_b32 s61,s93,s61
v_add_f32 v12,v12,v62
v_and_b32 v63,0xffff0000,v59
M 22,a[20:23],34
s_cselect_b32 s46,s88,s46
R 26,2,8192
v_add_f32 v13,v13,v63
M 22,a[28:31],38
L 3
v_lshlrev_b32 v62,16,v60
v_add_f32 v14,v14,v62
M 22,a[36:39],42
s_cselect_b32 s63,s89,s63
R 30,2,9216
v_and_b32 v63,0xffff0000,v60
M 22,a[44:47],46
D 0,56,1024
M 18,a[32:35],42
s_cselect_b32 s93,s50,s93
R 34,2,10240
v_add_f32 v15,v15,v63
M 18,a[40:43],46
L 3
v_lshlrev_b32 v62,16,v61
v_add_f32 v16,v16,v62
M 18,a[48:51],50
s_cselect_b32 s88,8,s88
R 38,2,11264
v_and_b32 v63,0xffff0000,v61
M 18,a[56:59],54
buffer_load_dwordx4 v[58:61],v0,s[36:39],s74 offen offset:0 sc1
M 22,a[52:55],50
s_cselect_b32 s89,-1,s89
R 42,2,12288
v_add_f32 v17,v17,v63
M 22,a[60:63],54
L 3
v_cvt_pk_bf16_f32 v10,v10,v11
v_cvt_pk_bf16_f32 v11,v12,v13
M 22,a[68:71],26
s_mul_i32 s58,s61,2560
R 46,2,13312
v_cvt_pk_bf16_f32 v12,v14,v15
M 22,a[76:79],30
D 0,56,2048
M 18,a[64:67],26
s_add_u32 s58,s58,109891584
R 50,2,14336
v_cvt_pk_bf16_f32 v13,v16,v17
M 18,a[72:75],30
L 3
v_lshrrev_b32 v62,8,v0
v_lshl_add_u32 v62,v62,5,v0
M 18,a[80:83],34
s_sub_u32 s75,s66,s85
R 54,2,15360
v_add_u32 v62,s73,v62
M 18,a[88:91],38
s_mov_b64 s[40:41],s[86:87]
ds_write_b128 v62,v[10:13] offset:0
M 22,a[84:87],34
s_mov_b32 s42,4194304
R 26,2,16384
ds_read2_b32 v[14:15],v5 offset0:0 offset1:16
M 22,a[92:95],38
L 5
s_mov_b32 s43,0x00020000
ds_read2_b32 v[16:17],v5 offset0:32 offset1:48
M 22,a[100:103],42
s_add_u32 s64,s75,0
s_lshl_b32 s62,s54,15
R 30,2,17408
M 22,a[108:111],46
D 0,56,3072
M 18,a[96:99],42
s_add_u32 m0,s57,4096
s_add_u32 s56,s56,4096
R 34,2,18432
M 18,a[104:107],46
L 6
s_mov_b32 s67,s65
s_add_u32 s74,s49,s62
M 18,a[112:115],50
s_add_u32 s65,s47,s62
s_mov_b32 s54,s61
R 38,2,19456
M 18,a[120:123],54
L 3
M 22,a[116:119],50
R 42,2,20480
M 22,a[124:127],54
L 3
D 0,56,0
M 22,a[132:135],26
R 46,2,21504
)"
R"(M 22,a[140:143],30
M 18,a[128:131],26
R 50,2,22528
M 18,a[136:139],30
L 3
M 18,a[144:147],34
R 54,2,23552
M 18,a[152:155],38
M 22,a[148:151],34
R 26,2,24576
M 22,a[156:159],38
L 3
D 0,56,1024
M 22,a[164:167],42
R 30,2,25600
M 22,a[172:175],46
M 18,a[160:163],42
R 34,2,26624
M 18,a[168:171],46
L 3
M 18,a[176:179],50
s_waitcnt vmcnt(8)
R 38,2,27648
M 18,a[184:187],54
s_barrier
M 22,a[180:183],50
R 42,2,28672
M 22,a[188:191],54
L 3
D 0,56,2048
M 22,a[196:199],26
s_add_u32 m0,s59,1024
s_add_u32 s56,s55,28672
R 46,2,29696
M 22,a[204:207],30
s_mul_i32 s55,s61,114688
s_add_u32 s55,s55,s60
M 18,a[192:195],26
R 50,2,30720
M 18,a[200:203],30
L 3
M 18,a[208:211],34
R 54,2,31744
M 18,a[216:219],38
M 22,a[212:215],34
R 26,2,32768
M 22,a[220:223],38
L 3
buffer_load_dword v8,s[36:39],s58 offen offset:0 sc1 lds
M 22,a[228:231],42
s_add_u32 m0,s57,28672
R 30,2,33792
M 22,a[236:239],46
D 0,56,0
M 18,a[224:227],42
R 34,2,34816
M 18,a[232:235],46
L 3
M 18,a[240:243],50
R 38,2,35840
M 18,a[248:251],54
M 22,a[244:247],50
R 42,2,36864
M 22,a[252:255],54
L 3
D 0,56,1024
M 22,v[68:71],26
R 46,2,37888
M 22,v[76:79],30
M 18,v[64:67],26
R 50,2,38912
M 18,v[72:75],30
L 3
M 18,v[80:83],34
R 54,2,39936
M 18,v[88:91],38
D 0,56,2048
M 22,v[84:87],34
R 26,2,40960
M 22,v[92:95],38
L 3
M 22,v[100:103],42
R 30,2,41984
M 22,v[108:111],46
M 18,v[96:99],42
R 34,2,43008
M 18,v[104:107],46
L 3
D 0,56,3072
M 18,v[112:115],50
s_add_u32 m0,s57,32768
s_add_u32 s56,s56,4096
R 38,2,44032
M 18,v[120:123],54
s_sub_u32 s57,s84,s57
M 22,v[116:119],50
R 42,2,45056
M 22,v[124:127],54
L 3
M 22,v[132:135],26
R 46,2,46080
M 22,v[140:143],30
D 0,56,0
M 18,v[128:131],26
R 50,2,47104
M 18,v[136:139],30
L 3
M 18,v[144:147],34
R 54,2,48128
M 18,v[152:155],38
M 22,v[148:151],34
R 26,2,49152
M 22,v[156:159],38
L 3
D 0,56,1024
M 22,v[164:167],42
R 30,2,50176
M 22,v[172:175],46
M 18,v[160:163],42
R 34,2,51200
M 18,v[168:171],46
L 3
M 18,v[176:179],50
R 38,2,52224
M 18,v[184:187],54
D 0,56,2048
M 22,v[180:183],50
R 42,2,53248
M 22,v[188:191],54
L 3
M 22,v[196:199],26
R 46,2,54272
M 22,v[204:207],30
M 18,v[192:195],26
R 50,2,55296
M 18,v[200:203],30
L 3
M 18,v[208:211],34
s_waitcnt vmcnt(7)
R 54,2,56320
M 18,v[216:219],38
s_barrier
ds_read_b32 v7,v6 offset:1024
M 22,v[212:215],34
R 26,1,0
M 22,v[220:223],38
L 4
M 22,v[228:231],42
R 30,1,1024
M 22,v[236:239],46
buffer_atomic_pk_add_bf16 v14,v9,s[40:43],s64 offen sc1
M 18,v[224:227],42
s_add_u32 s64,s75,8192
R 34,1,2048
M 18,v[232:235],46
L 4
buffer_atomic_pk_add_bf16 v15,v9,s[40:43],s64 offen sc1
M 18,v[240:243],50
s_add_u32 s64,s75,16384
L 3
R 38,1,3072
v_cmp_eq_u32 vcc,s53,v7
M 18,v[248:251],54
buffer_atomic_pk_add_bf16 v16,v9,s[40:43],s64 offen sc1
M 22,v[244:247],50
L 2
s_add_u32 s64,s75,24576
s_cmp_eq_u32 vcc_lo,-1
R 42,1,4096
M 22,v[252:255],54
buffer_atomic_pk_add_bf16 v17,v9,s[40:43],s64 offen sc1
s_cbranch_scc1 Lg4_Fa0_%=
Lg4_Ka0_%=:
Lg4_La0_%=:
s_nop 0
v_mfma_f32_16x16x32_bf16 v[14:17],a[4:7],v[26:29],0
s_add_u32 m0,s59,3072
R 46,1,5120
v_lshlrev_b32 v62,16,v58
M 14,a[12:15],30
buffer_load_dword v8,s[36:39],s58 offen offset:0 sc1 lds
v_mfma_f32_16x16x32_bf16 v[10:13],a[0:3],v[26:29],0
s_sub_u32 s75,s67,s85
R 50,1,6144
v_add_f32 v18,v18,v62
M 10,a[8:11],30
L 3
v_and_b32 v63,0xffff0000,v58
v_add_f32 v19,v19,v63
M 10,a[16:19],34
s_mov_b64 s[40:41],s[86:87]
R 54,1,7168
v_lshlrev_b32 v62,16,v59
M 10,a[24:27],38
s_mov_b32 s42,4194304
v_add_f32 v20,v20,v62
v_and_b32 v63,0xffff0000,v59
M 14,a[20:23],34
s_mov_b32 s43,0x00020000
R 26,1,8192
v_add_f32 v21,v21,v63
M 14,a[28:31],38
L 3
v_lshlrev_b32 v62,16,v60
v_add_f32 v22,v22,v62
M 14,a[36:39],42
s_add_u32 s64,s75,0
R 30,1,9216
v_and_b32 v63,0xffff0000,v60
M 14,a[44:47],46
s_lshl_b32 s62,s54,15
v_add_f32 v23,v23,v63
v_lshlrev_b32 v62,16,v61
M 10,a[32:35],42
s_mov_b32 s66,s65
R 34,1,10240
v_add_f32 v24,v24,v62
M 10,a[40:43],46
L 3
v_and_b32 v63,0xffff0000,v61
v_add_f32 v25,v25,v63
M 10,a[48:51],50
s_add_u32 s65,s47,s62
R 38,1,11264
v_cvt_pk_bf16_f32 v18,v18,v19
M 10,a[56:59],54
buffer_load_dwordx4 v[58:61],v0,s[36:39],s74 offen offset:0 sc1
M 14,a[52:55],50
s_add_u32 s74,s49,s62
R 42,1,12288
v_cvt_pk_bf16_f32 v19,v20,v21
M 14,a[60:63],54
L 3
v_cvt_pk_bf16_f32 v20,v22,v23
v_cvt_pk_bf16_f32 v21,v24,v25
M 14,a[68:71],26
R 46,1,13312
v_lshrrev_b32 v62,8,v0
M 14,a[76:79],30
v_lshl_add_u32 v62,v62,5,v0
v_add_u32 v62,s73,v62
M 10,a[64:67],26
R 50,1,14336
M 10,a[72:75],30
L 3
ds_write_b128 v62,v[18:21] offset:0
M 10,a[80:83],34
R 54,1,15360
ds_read2_b32 v[22:23],v5 offset0:0 offset1:16
M 10,a[88:91],38
ds_read2_b32 v[24:25],v5 offset0:32 offset1:48
M 14,a[84:87],34
R 26,1,16384
M 14,a[92:95],38
L 6
M 14,a[100:103],42
R 30,1,17408
M 14,a[108:111],46
M 10,a[96:99],42
R 34,1,18432
M 10,a[104:107],46
L 5
L 3
M 10,a[112:115],50
R 38,1,19456
M 10,a[120:123],54
buffer_atomic_pk_add_bf16 v22,v9,s[40:43],s64 offen sc1
M 14,a[116:119],50
s_add_u32 s64,s75,8192
R 42,1,20480
M 14,a[124:127],54
L 3
buffer_atomic_pk_add_bf16 v23,v9,s[40:43],s64 offen sc1
M 14,a[132:135],26
s_add_u32 s64,s75,16384
R 46,1,21504
M 14,a[140:143],30
buffer_atomic_pk_add_bf16 v24,v9,s[40:43],s64 offen sc1
M 10,a[128:131],26
s_add_u32 s64,s75,24576
R 50,1,22528
M 10,a[136:139],30
L 3
buffer_atomic_pk_add_bf16 v25,v9,s[40:43],s64 offen sc1
M 10,a[144:147],34
R 54,1,23552
M 10,a[152:155],38
M 14,a[148:151],34
R 26,1,24576
M 14,a[156:159],38
L 3
M 14,a[164:167],42
R 30,1,25600
M 14,a[172:175],46
M 10,a[160:163],42
R 34,1,26624
M 10,a[168:171],46
L 3
M 10,a[176:179],50
s_waitcnt vmcnt(5)
R 38,1,27648
M 10,a[184:187],54
s_barrier
ds_read_b32 v7,v6 offset:3072
M 14,a[180:183],50
R 42,1,28672
M 14,a[188:191],54
L 4
M 14,a[196:199],26
R 46,1,29696
M 14,a[204:207],30
M 10,a[192:195],26
R 50,1,30720
M 10,a[200:203],30
L 4
L 3
v_cmp_eq_u32 vcc,s53,v7
M 10,a[208:211],34
R 54,1,31744
M 10,a[216:219],38
M 14,a[212:215],34
s_cmp_eq_u32 vcc_lo,-1
R 26,1,32768
s_cbranch_scc0 Lg4_Xa0_%=
M 14,a[220:223],38
L 3
s_add_u32 m0,s57,0
s_add_u32 s56,s55,0
M 14,a[228:231],42
s_add_u32 s61,s54,s46
s_cmp_ge_u32 s61,s63
s_cselect_b32 s61,s93,s61
R 30,1,33792
M 14,a[236:239],46
s_mul_i32 s58,s61,2560
D 0,56,0
M 10,a[224:227],42
s_cselect_b32 s46,s88,s46
s_cselect_b32 s63,s89,s63
R 34,1,34816
M 10,a[232:235],46
L 3
s_cselect_b32 s93,s50,s93
s_cselect_b32 s88,8,s88
M 10,a[240:243],50
s_cselect_b32 s89,-1,s89
s_add_u32 s58,s58,109891584
R 38,1,35840
M 10,a[248:251],54
D 0,56,1024
M 14,a[244:247],50
s_mov_b32 s54,s61
R 42,1,36864
M 14,a[252:255],54
L 3
M 14,v[68:71],26
R 46,1,37888
M 14,v[76:79],30
D 0,56,2048
M 10,v[64:67],26
R 50,1,38912
M 10,v[72:75],30
L 3
M 10,v[80:83],34
R 54,1,39936
M 10,v[88:91],38
D 0,56,3072
M 14,v[84:87],34
s_add_u32 m0,s57,4096
s_add_u32 s56,s56,4096
R 26,1,40960
M 14,v[92:95],38
L 3
M 14,v[100:103],42
R 30,1,41984
M 14,v[108:111],46
D 0,56,0
M 10,v[96:99],42
R 34,1,43008
M 10,v[104:107],46
L 3
M 10,v[112:115],50
R 38,1,44032
M 10,v[120:123],54
D 0,56,1024
M 14,v[116:119],50
R 42,1,45056
M 14,v[124:127],54
L 3
M 14,v[132:135],26
R 46,1,46080
M 14,v[140:143],30
D 0,56,2048
M 10,v[128:131],26
s_add_u32 m0,s59,0
s_add_u32 s56,s55,28672
R 50,1,47104
M 10,v[136:139],30
L 3
buffer_load_dword v8,s[36:39],s58 offen offset:0 sc1 lds
M 10,v[144:147],34
s_add_u32 m0,s57,28672
s_mul_i32 s55,s61,114688
R 54,1,48128
M 10,v[152:155],38
s_add_u32 s55,s55,s60
M 14,v[148:151],34
R 26,1,49152
M 14,v[156:159],38
L 3
D 0,56,0
M 14,v[164:167],42
R 30,1,50176
M 14,v[172:175],46
M 10,v[160:163],42
R 34,1,51200
M 10,v[168:171],46
L 3
D 0,56,1024
M 10,v[176:179],50
R 38,1,52224
M 10,v[184:187],54
D 0,56,2048
M 14,v[180:183],50
R 42,1,53248
M 14,v[188:191],54
L 3
M 14,v[196:199],26
R 46,1,54272
M 14,v[204:207],30
D 0,56,3072
M 10,v[192:195],26
s_add_u32 m0,s57,32768
s_add_u32 s56,s56,4096
R 50,1,55296
M 10,v[200:203],30
L 3
s_sub_u32 s57,s84,s57
M 10,v[208:211],34
s_waitcnt vmcnt(4)
R 54,1,56320
M 10,v[216:219],38
s_barrier
ds_read_b32 v7,v6 offset:0
M 14,v[212:215],34
R 26,2,0
M 14,v[220:223],38
L 4
D 0,56,0
M 14,v[228:231],42
R 30,2,1024
M 14,v[236:239],46
D 0,56,1024
M 10,v[224:227],42
R 34,2,2048
M 10,v[232:235],46
L 4
D 0,56,2048
M 10,v[240:243],50
L 3
R 38,2,3072
v_cmp_eq_u32 vcc,s53,v7
M 10,v[248:251],54
M 14,v[244:247],50
L 2
s_cmp_eq_u32 vcc_lo,-1
R 42,2,4096
M 14,v[252:255],54
buffer_store_dword v4,v3,s[36:39],s71 offen offset:0 sc0 sc1
buffer_store_dword v4,v3,s[36:39],s71 offen offset:0 sc0 sc1
buffer_store_dword v4,v3,s[36:39],s71 offen offset:0 sc0 sc1
buffer_store_dword v4,v3,s[36:39],s71 offen offset:0 sc0 sc1
s_cbranch_scc1 Lg4_Fa1_%=
s_branch Lg4_Ka1_%=
Lg4_Xa0_%=:
M 14,a[220:223],38
L 3
s_add_u32 m0,s59,0
M 14,a[228:231],42
R 30,1,33792
M 14,a[236:239],46
buffer_load_dword v8,s[36:39],s58 offen offset:0 sc1 lds
M 10,a[224:227],42
R 34,1,34816
M 10,a[232:235],46
L 3
M 10,a[240:243],50
R 38,1,35840
M 10,a[248:251],54
M 14,a[244:247],50
R 42,1,36864
M 14,a[252:255],54
L 3
M 14,v[68:71],26
R 46,1,37888
M 14,v[76:79],30
M 10,v[64:67],26
R 50,1,38912
M 10,v[72:75],30
L 3
)"
R"(M 10,v[80:83],34
R 54,1,39936
M 10,v[88:91],38
M 14,v[84:87],34
R 26,1,40960
M 14,v[92:95],38
L 3
M 14,v[100:103],42
R 30,1,41984
M 14,v[108:111],46
M 10,v[96:99],42
R 34,1,43008
M 10,v[104:107],46
L 3
M 10,v[112:115],50
R 38,1,44032
M 10,v[120:123],54
M 14,v[116:119],50
R 42,1,45056
M 14,v[124:127],54
L 3
M 14,v[132:135],26
R 46,1,46080
M 14,v[140:143],30
M 10,v[128:131],26
R 50,1,47104
M 10,v[136:139],30
L 3
M 10,v[144:147],34
R 54,1,48128
M 10,v[152:155],38
M 14,v[148:151],34
R 26,1,49152
M 14,v[156:159],38
L 3
M 14,v[164:167],42
R 30,1,50176
M 14,v[172:175],46
M 10,v[160:163],42
R 34,1,51200
M 10,v[168:171],46
L 3
M 10,v[176:179],50
R 38,1,52224
M 10,v[184:187],54
M 14,v[180:183],50
R 42,1,53248
M 14,v[188:191],54
L 3
M 14,v[196:199],26
R 46,1,54272
M 14,v[204:207],30
M 10,v[192:195],26
R 50,1,55296
M 10,v[200:203],30
L 3
M 10,v[208:211],34
s_waitcnt vmcnt(0)
R 54,1,56320
M 10,v[216:219],38
s_barrier
ds_read_b32 v7,v6 offset:0
M 14,v[212:215],34
M 14,v[220:223],38
L 3
M 14,v[228:231],42
M 14,v[236:239],46
M 10,v[224:227],42
M 10,v[232:235],46
L 1
L 0
M 10,v[240:243],50
v_cmp_eq_u32 vcc,s53,v7
M 10,v[248:251],54
M 14,v[244:247],50
M 14,v[252:255],54
Lg4_pa0_%=:
s_mov_b32 s80,1
s_cmp_eq_u32 vcc_lo,-1
s_cbranch_scc0 Lg4_xna0_%=
s_add_u32 m0,s57,0
s_add_u32 s56,s55,0
D 0,56,0
s_sub_u32 s75,s66,s85
v_lshlrev_b32 v62,16,v58
D 0,56,1024
v_add_f32 v10,v10,v62
D 0,56,2048
v_and_b32 v63,0xffff0000,v58
D 0,56,3072
v_add_f32 v11,v11,v63
s_add_u32 m0,s57,4096
s_add_u32 s56,s56,4096
D 0,56,0
v_lshlrev_b32 v62,16,v59
D 0,56,1024
v_add_f32 v12,v12,v62
D 0,56,2048
v_and_b32 v63,0xffff0000,v59
s_add_u32 s61,s54,s46
s_cmp_ge_u32 s61,s63
s_cselect_b32 s61,s93,s61
s_cselect_b32 s46,s88,s46
s_cselect_b32 s63,s89,s63
s_cselect_b32 s93,s50,s93
s_cselect_b32 s88,8,s88
s_cselect_b32 s89,-1,s89
s_mul_i32 s58,s61,2560
s_add_u32 s58,s58,109891584
s_add_u32 m0,s59,2048
s_nop 0
buffer_load_dword v8,s[36:39],s58 offen offset:0 sc1 lds
v_add_f32 v13,v13,v63
s_add_u32 m0,s57,28672
s_add_u32 s56,s55,28672
D 0,56,0
v_lshlrev_b32 v62,16,v60
D 0,56,1024
v_add_f32 v14,v14,v62
D 0,56,2048
v_and_b32 v63,0xffff0000,v60
D 0,56,3072
v_add_f32 v15,v15,v63
s_add_u32 m0,s57,32768
s_add_u32 s56,s56,4096
D 0,56,0
v_lshlrev_b32 v62,16,v61
D 0,56,1024
v_add_f32 v16,v16,v62
D 0,56,2048
v_and_b32 v63,0xffff0000,v61
s_mul_i32 s55,s61,114688
s_add_u32 s55,s55,s60
s_sub_u32 s57,s84,s57
s_mov_b32 s54,s61
v_add_f32 v17,v17,v63
v_cvt_pk_bf16_f32 v10,v10,v11
v_cvt_pk_bf16_f32 v11,v12,v13
v_cvt_pk_bf16_f32 v12,v14,v15
v_cvt_pk_bf16_f32 v13,v16,v17
v_lshrrev_b32 v62,8,v0
v_lshl_add_u32 v62,v62,5,v0
v_add_u32 v62,s73,v62
ds_write_b128 v62,v[10:13] offset:0
ds_read2_b32 v[14:15],v5 offset0:0 offset1:16
ds_read2_b32 v[16:17],v5 offset0:32 offset1:48
s_mov_b64 s[40:41],s[86:87]
s_mov_b32 s42,4194304
s_mov_b32 s43,0x00020000
L 0
s_add_u32 s64,s75,0
buffer_atomic_pk_add_bf16 v14,v9,s[40:43],s64 offen sc1
s_add_u32 s64,s75,8192
buffer_atomic_pk_add_bf16 v15,v9,s[40:43],s64 offen sc1
s_add_u32 s64,s75,16384
buffer_atomic_pk_add_bf16 v16,v9,s[40:43],s64 offen sc1
s_add_u32 s64,s75,24576
buffer_atomic_pk_add_bf16 v17,v9,s[40:43],s64 offen sc1
s_mov_b32 s72,0x7fff0000
s_mov_b32 s69,0x7fff0000
s_mov_b32 s66,0x7fff0000
s_mov_b32 s76,0x7fff0000
s_waitcnt vmcnt(11)
s_barrier
s_branch Lg4_st1_%=
Lg4_xna0_%=:
s_cmp_ge_u32 s54,256
s_cbranch_scc1 Lg4_fina0_%=
s_add_u32 m0,s59,4096
s_nop 0
buffer_load_dword v8,s[36:39],s58 offen offset:0 sc1 lds
buffer_store_dword v4,v3,s[36:39],s71 offen offset:0 sc0 sc1
s_add_u32 m0,s59,5120
s_nop 0
buffer_load_dword v8,s[36:39],s58 offen offset:0 sc1 lds
buffer_store_dword v4,v3,s[36:39],s71 offen offset:0 sc0 sc1
s_mov_b32 s83,0
s_sub_u32 s75,s66,s85
v_lshlrev_b32 v62,16,v58
v_add_f32 v10,v10,v62
v_and_b32 v63,0xffff0000,v58
v_add_f32 v11,v11,v63
v_lshlrev_b32 v62,16,v59
v_add_f32 v12,v12,v62
v_and_b32 v63,0xffff0000,v59
v_add_f32 v13,v13,v63
v_lshlrev_b32 v62,16,v60
v_add_f32 v14,v14,v62
v_and_b32 v63,0xffff0000,v60
v_add_f32 v15,v15,v63
v_lshlrev_b32 v62,16,v61
v_add_f32 v16,v16,v62
v_and_b32 v63,0xffff0000,v61
v_add_f32 v17,v17,v63
v_cvt_pk_bf16_f32 v10,v10,v11
v_cvt_pk_bf16_f32 v11,v12,v13
v_cvt_pk_bf16_f32 v12,v14,v15
v_cvt_pk_bf16_f32 v13,v16,v17
v_lshrrev_b32 v62,8,v0
v_lshl_add_u32 v62,v62,5,v0
v_add_u32 v62,s73,v62
ds_write_b128 v62,v[10:13] offset:0
ds_read2_b32 v[14:15],v5 offset0:0 offset1:16
ds_read2_b32 v[16:17],v5 offset0:32 offset1:48
s_mov_b64 s[40:41],s[86:87]
s_mov_b32 s42,4194304
s_mov_b32 s43,0x00020000
L 0
s_add_u32 s64,s75,0
buffer_atomic_pk_add_bf16 v14,v9,s[40:43],s64 offen sc1
s_add_u32 s64,s75,8192
buffer_atomic_pk_add_bf16 v15,v9,s[40:43],s64 offen sc1
s_add_u32 s64,s75,16384
buffer_atomic_pk_add_bf16 v16,v9,s[40:43],s64 offen sc1
s_add_u32 s64,s75,24576
buffer_atomic_pk_add_bf16 v17,v9,s[40:43],s64 offen sc1
s_mov_b32 s72,0x7fff0000
s_mov_b32 s69,0x7fff0000
s_mov_b32 s66,0x7fff0000
s_mov_b32 s76,0x7fff0000
s_branch Lg4_poll_%=
Lg4_fina0_%=:
s_sub_u32 s75,s66,s85
v_lshlrev_b32 v62,16,v58
v_add_f32 v10,v10,v62
v_and_b32 v63,0xffff0000,v58
v_add_f32 v11,v11,v63
v_lshlrev_b32 v62,16,v59
v_add_f32 v12,v12,v62
v_and_b32 v63,0xffff0000,v59
v_add_f32 v13,v13,v63
v_lshlrev_b32 v62,16,v60
v_add_f32 v14,v14,v62
v_and_b32 v63,0xffff0000,v60
v_add_f32 v15,v15,v63
v_lshlrev_b32 v62,16,v61
v_add_f32 v16,v16,v62
v_and_b32 v63,0xffff0000,v61
v_add_f32 v17,v17,v63
v_cvt_pk_bf16_f32 v10,v10,v11
v_cvt_pk_bf16_f32 v11,v12,v13
v_cvt_pk_bf16_f32 v12,v14,v15
v_cvt_pk_bf16_f32 v13,v16,v17
v_lshrrev_b32 v62,8,v0
v_lshl_add_u32 v62,v62,5,v0
v_add_u32 v62,s73,v62
ds_write_b128 v62,v[10:13] offset:0
ds_read2_b32 v[14:15],v5 offset0:0 offset1:16
ds_read2_b32 v[16:17],v5 offset0:32 offset1:48
s_mov_b64 s[40:41],s[86:87]
s_mov_b32 s42,4194304
s_mov_b32 s43,0x00020000
L 0
s_add_u32 s64,s75,0
buffer_atomic_pk_add_bf16 v14,v9,s[40:43],s64 offen sc1
s_add_u32 s64,s75,8192
buffer_atomic_pk_add_bf16 v15,v9,s[40:43],s64 offen sc1
s_add_u32 s64,s75,16384
buffer_atomic_pk_add_bf16 v16,v9,s[40:43],s64 offen sc1
s_add_u32 s64,s75,24576
buffer_atomic_pk_add_bf16 v17,v9,s[40:43],s64 offen sc1
s_mov_b32 s72,0x7fff0000
s_mov_b32 s69,0x7fff0000
s_mov_b32 s66,0x7fff0000
s_mov_b32 s76,0x7fff0000
s_branch Lg4_done_%=
Lg4_Ka1_%=:
Lg4_La1_%=:
s_nop 0
v_mfma_f32_16x16x32_bf16 v[22:25],a[4:7],v[26:29],0
s_add_u32 m0,s59,3072
R 46,2,5120
v_lshlrev_b32 v62,16,v58
M 22,a[12:15],30
buffer_load_dword v8,s[36:39],s58 offen offset:0 sc1 lds
v_mfma_f32_16x16x32_bf16 v[18:21],a[0:3],v[26:29],0
s_sub_u32 s75,s66,s85
R 50,2,6144
v_add_f32 v10,v10,v62
M 18,a[8:11],30
L 3
v_and_b32 v63,0xffff0000,v58
v_add_f32 v11,v11,v63
M 18,a[16:19],34
s_mov_b64 s[40:41],s[86:87]
R 54,2,7168
v_lshlrev_b32 v62,16,v59
M 18,a[24:27],38
s_mov_b32 s42,4194304
v_add_f32 v12,v12,v62
v_and_b32 v63,0xffff0000,v59
M 22,a[20:23],34
s_mov_b32 s43,0x00020000
R 26,2,8192
v_add_f32 v13,v13,v63
M 22,a[28:31],38
L 3
v_lshlrev_b32 v62,16,v60
v_add_f32 v14,v14,v62
M 22,a[36:39],42
s_add_u32 s64,s75,0
R 30,2,9216
v_and_b32 v63,0xffff0000,v60
M 22,a[44:47],46
s_lshl_b32 s62,s54,15
v_add_f32 v15,v15,v63
v_lshlrev_b32 v62,16,v61
M 18,a[32:35],42
s_mov_b32 s67,s65
R 34,2,10240
v_add_f32 v16,v16,v62
M 18,a[40:43],46
L 3
v_and_b32 v63,0xffff0000,v61
v_add_f32 v17,v17,v63
M 18,a[48:51],50
s_add_u32 s65,s47,s62
R 38,2,11264
v_cvt_pk_bf16_f32 v10,v10,v11
M 18,a[56:59],54
buffer_load_dwordx4 v[58:61],v0,s[36:39],s74 offen offset:0 sc1
M 22,a[52:55],50
s_add_u32 s74,s49,s62
R 42,2,12288
v_cvt_pk_bf16_f32 v11,v12,v13
M 22,a[60:63],54
L 3
v_cvt_pk_bf16_f32 v12,v14,v15
v_cvt_pk_bf16_f32 v13,v16,v17
M 22,a[68:71],26
R 46,2,13312
v_lshrrev_b32 v62,8,v0
M 22,a[76:79],30
v_lshl_add_u32 v62,v62,5,v0
v_add_u32 v62,s73,v62
M 18,a[64:67],26
R 50,2,14336
M 18,a[72:75],30
L 3
ds_write_b128 v62,v[10:13] offset:0
M 18,a[80:83],34
R 54,2,15360
ds_read2_b32 v[14:15],v5 offset0:0 offset1:16
M 18,a[88:91],38
ds_read2_b32 v[16:17],v5 offset0:32 offset1:48
M 22,a[84:87],34
R 26,2,16384
M 22,a[92:95],38
L 6
M 22,a[100:103],42
R 30,2,17408
M 22,a[108:111],46
M 18,a[96:99],42
R 34,2,18432
M 18,a[104:107],46
L 5
L 3
M 18,a[112:115],50
R 38,2,19456
M 18,a[120:123],54
buffer_atomic_pk_add_bf16 v14,v9,s[40:43],s64 offen sc1
M 22,a[116:119],50
s_add_u32 s64,s75,8192
R 42,2,20480
M 22,a[124:127],54
L 3
buffer_atomic_pk_add_bf16 v15,v9,s[40:43],s64 offen sc1
M 22,a[132:135],26
s_add_u32 s64,s75,16384
R 46,2,21504
M 22,a[140:143],30
buffer_atomic_pk_add_bf16 v16,v9,s[40:43],s64 offen sc1
M 18,a[128:131],26
s_add_u32 s64,s75,24576
R 50,2,22528
M 18,a[136:139],30
L 3
buffer_atomic_pk_add_bf16 v17,v9,s[40:43],s64 offen sc1
M 18,a[144:147],34
R 54,2,23552
M 18,a[152:155],38
M 22,a[148:151],34
R 26,2,24576
M 22,a[156:159],38
L 3
M 22,a[164:167],42
R 30,2,25600
M 22,a[172:175],46
M 18,a[160:163],42
R 34,2,26624
M 18,a[168:171],46
L 3
M 18,a[176:179],50
s_waitcnt vmcnt(5)
R 38,2,27648
M 18,a[184:187],54
s_barrier
ds_read_b32 v7,v6 offset:3072
M 22,a[180:183],50
R 42,2,28672
M 22,a[188:191],54
L 4
M 22,a[196:199],26
R 46,2,29696
M 22,a[204:207],30
M 18,a[192:195],26
R 50,2,30720
M 18,a[200:203],30
L 4
L 3
v_cmp_eq_u32 vcc,s53,v7
M 18,a[208:211],34
R 54,2,31744
M 18,a[216:219],38
M 22,a[212:215],34
s_cmp_eq_u32 vcc_lo,-1
R 26,2,32768
s_cbranch_scc0 Lg4_Xa1_%=
M 22,a[220:223],38
L 3
s_add_u32 m0,s57,0
s_add_u32 s56,s55,0
M 22,a[228:231],42
s_add_u32 s61,s54,s46
s_cmp_ge_u32 s61,s63
s_cselect_b32 s61,s93,s61
R 30,2,33792
M 22,a[236:239],46
s_mul_i32 s58,s61,2560
D 0,56,0
M 18,a[224:227],42
s_cselect_b32 s46,s88,s46
s_cselect_b32 s63,s89,s63
R 34,2,34816
M 18,a[232:235],46
L 3
s_cselect_b32 s93,s50,s93
s_cselect_b32 s88,8,s88
M 18,a[240:243],50
s_cselect_b32 s89,-1,s89
s_add_u32 s58,s58,109891584
R 38,2,35840
M 18,a[248:251],54
D 0,56,1024
M 22,a[244:247],50
s_mov_b32 s54,s61
R 42,2,36864
M 22,a[252:255],54
L 3
M 22,v[68:71],26
R 46,2,37888
M 22,v[76:79],30
D 0,56,2048
M 18,v[64:67],26
R 50,2,38912
M 18,v[72:75],30
L 3
M 18,v[80:83],34
R 54,2,39936
M 18,v[88:91],38
D 0,56,3072
M 22,v[84:87],34
s_add_u32 m0,s57,4096
s_add_u32 s56,s56,4096
R 26,2,40960
M 22,v[92:95],38
L 3
M 22,v[100:103],42
R 30,2,41984
M 22,v[108:111],46
D 0,56,0
M 18,v[96:99],42
R 34,2,43008
M 18,v[104:107],46
L 3
M 18,v[112:115],50
R 38,2,44032
M 18,v[120:123],54
D 0,56,1024
M 22,v[116:119],50
R 42,2,45056
M 22,v[124:127],54
L 3
M 22,v[132:135],26
R 46,2,46080
M 22,v[140:143],30
D 0,56,2048
M 18,v[128:131],26
s_add_u32 m0,s59,1024
s_add_u32 s56,s55,28672
R 50,2,47104
M 18,v[136:139],30
L 3
buffer_load_dword v8,s[36:39],s58 offen offset:0 sc1 lds
M 18,v[144:147],34
s_add_u32 m0,s57,28672
s_mul_i32 s55,s61,114688
R 54,2,48128
M 18,v[152:155],38
s_add_u32 s55,s55,s60
)"
R"(M 22,v[148:151],34
R 26,2,49152
M 22,v[156:159],38
L 3
D 0,56,0
M 22,v[164:167],42
R 30,2,50176
M 22,v[172:175],46
M 18,v[160:163],42
R 34,2,51200
M 18,v[168:171],46
L 3
D 0,56,1024
M 18,v[176:179],50
R 38,2,52224
M 18,v[184:187],54
D 0,56,2048
M 22,v[180:183],50
R 42,2,53248
M 22,v[188:191],54
L 3
M 22,v[196:199],26
R 46,2,54272
M 22,v[204:207],30
D 0,56,3072
M 18,v[192:195],26
s_add_u32 m0,s57,32768
s_add_u32 s56,s56,4096
R 50,2,55296
M 18,v[200:203],30
L 3
s_sub_u32 s57,s84,s57
M 18,v[208:211],34
s_waitcnt vmcnt(4)
R 54,2,56320
M 18,v[216:219],38
s_barrier
ds_read_b32 v7,v6 offset:1024
M 22,v[212:215],34
R 26,1,0
M 22,v[220:223],38
L 4
D 0,56,0
M 22,v[228:231],42
R 30,1,1024
M 22,v[236:239],46
D 0,56,1024
M 18,v[224:227],42
R 34,1,2048
M 18,v[232:235],46
L 4
D 0,56,2048
M 18,v[240:243],50
L 3
R 38,1,3072
v_cmp_eq_u32 vcc,s53,v7
M 18,v[248:251],54
M 22,v[244:247],50
L 2
s_cmp_eq_u32 vcc_lo,-1
R 42,1,4096
M 22,v[252:255],54
buffer_store_dword v4,v3,s[36:39],s71 offen offset:0 sc0 sc1
buffer_store_dword v4,v3,s[36:39],s71 offen offset:0 sc0 sc1
buffer_store_dword v4,v3,s[36:39],s71 offen offset:0 sc0 sc1
buffer_store_dword v4,v3,s[36:39],s71 offen offset:0 sc0 sc1
s_cbranch_scc1 Lg4_Fa0_%=
s_branch Lg4_Ka0_%=
Lg4_Xa1_%=:
M 22,a[220:223],38
L 3
s_add_u32 m0,s59,1024
M 22,a[228:231],42
R 30,2,33792
M 22,a[236:239],46
buffer_load_dword v8,s[36:39],s58 offen offset:0 sc1 lds
M 18,a[224:227],42
R 34,2,34816
M 18,a[232:235],46
L 3
M 18,a[240:243],50
R 38,2,35840
M 18,a[248:251],54
M 22,a[244:247],50
R 42,2,36864
M 22,a[252:255],54
L 3
M 22,v[68:71],26
R 46,2,37888
M 22,v[76:79],30
M 18,v[64:67],26
R 50,2,38912
M 18,v[72:75],30
L 3
M 18,v[80:83],34
R 54,2,39936
M 18,v[88:91],38
M 22,v[84:87],34
R 26,2,40960
M 22,v[92:95],38
L 3
M 22,v[100:103],42
R 30,2,41984
M 22,v[108:111],46
M 18,v[96:99],42
R 34,2,43008
M 18,v[104:107],46
L 3
M 18,v[112:115],50
R 38,2,44032
M 18,v[120:123],54
M 22,v[116:119],50
R 42,2,45056
M 22,v[124:127],54
L 3
M 22,v[132:135],26
R 46,2,46080
M 22,v[140:143],30
M 18,v[128:131],26
R 50,2,47104
M 18,v[136:139],30
L 3
M 18,v[144:147],34
R 54,2,48128
M 18,v[152:155],38
M 22,v[148:151],34
R 26,2,49152
M 22,v[156:159],38
L 3
M 22,v[164:167],42
R 30,2,50176
M 22,v[172:175],46
M 18,v[160:163],42
R 34,2,51200
M 18,v[168:171],46
L 3
M 18,v[176:179],50
R 38,2,52224
M 18,v[184:187],54
M 22,v[180:183],50
R 42,2,53248
M 22,v[188:191],54
L 3
M 22,v[196:199],26
R 46,2,54272
M 22,v[204:207],30
M 18,v[192:195],26
R 50,2,55296
M 18,v[200:203],30
L 3
M 18,v[208:211],34
s_waitcnt vmcnt(0)
R 54,2,56320
M 18,v[216:219],38
s_barrier
ds_read_b32 v7,v6 offset:1024
M 22,v[212:215],34
M 22,v[220:223],38
L 3
M 22,v[228:231],42
M 22,v[236:239],46
M 18,v[224:227],42
M 18,v[232:235],46
L 1
L 0
M 18,v[240:243],50
v_cmp_eq_u32 vcc,s53,v7
M 18,v[248:251],54
M 22,v[244:247],50
M 22,v[252:255],54
Lg4_pa1_%=:
s_mov_b32 s80,0
s_cmp_eq_u32 vcc_lo,-1
s_cbranch_scc0 Lg4_xna1_%=
s_add_u32 m0,s57,0
s_add_u32 s56,s55,0
D 0,56,0
s_sub_u32 s75,s67,s85
v_lshlrev_b32 v62,16,v58
D 0,56,1024
v_add_f32 v18,v18,v62
D 0,56,2048
v_and_b32 v63,0xffff0000,v58
D 0,56,3072
v_add_f32 v19,v19,v63
s_add_u32 m0,s57,4096
s_add_u32 s56,s56,4096
D 0,56,0
v_lshlrev_b32 v62,16,v59
D 0,56,1024
v_add_f32 v20,v20,v62
D 0,56,2048
v_and_b32 v63,0xffff0000,v59
s_add_u32 s61,s54,s46
s_cmp_ge_u32 s61,s63
s_cselect_b32 s61,s93,s61
s_cselect_b32 s46,s88,s46
s_cselect_b32 s63,s89,s63
s_cselect_b32 s93,s50,s93
s_cselect_b32 s88,8,s88
s_cselect_b32 s89,-1,s89
s_mul_i32 s58,s61,2560
s_add_u32 s58,s58,109891584
s_add_u32 m0,s59,2048
s_nop 0
buffer_load_dword v8,s[36:39],s58 offen offset:0 sc1 lds
v_add_f32 v21,v21,v63
s_add_u32 m0,s57,28672
s_add_u32 s56,s55,28672
D 0,56,0
v_lshlrev_b32 v62,16,v60
D 0,56,1024
v_add_f32 v22,v22,v62
D 0,56,2048
v_and_b32 v63,0xffff0000,v60
D 0,56,3072
v_add_f32 v23,v23,v63
s_add_u32 m0,s57,32768
s_add_u32 s56,s56,4096
D 0,56,0
v_lshlrev_b32 v62,16,v61
D 0,56,1024
v_add_f32 v24,v24,v62
D 0,56,2048
v_and_b32 v63,0xffff0000,v61
s_mul_i32 s55,s61,114688
s_add_u32 s55,s55,s60
s_sub_u32 s57,s84,s57
s_mov_b32 s54,s61
v_add_f32 v25,v25,v63
v_cvt_pk_bf16_f32 v18,v18,v19
v_cvt_pk_bf16_f32 v19,v20,v21
v_cvt_pk_bf16_f32 v20,v22,v23
v_cvt_pk_bf16_f32 v21,v24,v25
v_lshrrev_b32 v62,8,v0
v_lshl_add_u32 v62,v62,5,v0
v_add_u32 v62,s73,v62
ds_write_b128 v62,v[18:21] offset:0
ds_read2_b32 v[22:23],v5 offset0:0 offset1:16
ds_read2_b32 v[24:25],v5 offset0:32 offset1:48
s_mov_b64 s[40:41],s[86:87]
s_mov_b32 s42,4194304
s_mov_b32 s43,0x00020000
L 0
s_add_u32 s64,s75,0
buffer_atomic_pk_add_bf16 v22,v9,s[40:43],s64 offen sc1
s_add_u32 s64,s75,8192
buffer_atomic_pk_add_bf16 v23,v9,s[40:43],s64 offen sc1
s_add_u32 s64,s75,16384
buffer_atomic_pk_add_bf16 v24,v9,s[40:43],s64 offen sc1
s_add_u32 s64,s75,24576
buffer_atomic_pk_add_bf16 v25,v9,s[40:43],s64 offen sc1
s_mov_b32 s72,0x7fff0000
s_mov_b32 s69,0x7fff0000
s_mov_b32 s67,0x7fff0000
s_mov_b32 s77,0x7fff0000
s_waitcnt vmcnt(11)
s_barrier
s_branch Lg4_st0_%=
Lg4_xna1_%=:
s_cmp_ge_u32 s54,256
s_cbranch_scc1 Lg4_fina1_%=
s_add_u32 m0,s59,4096
s_nop 0
buffer_load_dword v8,s[36:39],s58 offen offset:0 sc1 lds
buffer_store_dword v4,v3,s[36:39],s71 offen offset:0 sc0 sc1
s_add_u32 m0,s59,5120
s_nop 0
buffer_load_dword v8,s[36:39],s58 offen offset:0 sc1 lds
buffer_store_dword v4,v3,s[36:39],s71 offen offset:0 sc0 sc1
s_mov_b32 s83,0
s_sub_u32 s75,s67,s85
v_lshlrev_b32 v62,16,v58
v_add_f32 v18,v18,v62
v_and_b32 v63,0xffff0000,v58
v_add_f32 v19,v19,v63
v_lshlrev_b32 v62,16,v59
v_add_f32 v20,v20,v62
v_and_b32 v63,0xffff0000,v59
v_add_f32 v21,v21,v63
v_lshlrev_b32 v62,16,v60
v_add_f32 v22,v22,v62
v_and_b32 v63,0xffff0000,v60
v_add_f32 v23,v23,v63
v_lshlrev_b32 v62,16,v61
v_add_f32 v24,v24,v62
v_and_b32 v63,0xffff0000,v61
v_add_f32 v25,v25,v63
v_cvt_pk_bf16_f32 v18,v18,v19
v_cvt_pk_bf16_f32 v19,v20,v21
v_cvt_pk_bf16_f32 v20,v22,v23
v_cvt_pk_bf16_f32 v21,v24,v25
v_lshrrev_b32 v62,8,v0
v_lshl_add_u32 v62,v62,5,v0
v_add_u32 v62,s73,v62
ds_write_b128 v62,v[18:21] offset:0
ds_read2_b32 v[22:23],v5 offset0:0 offset1:16
ds_read2_b32 v[24:25],v5 offset0:32 offset1:48
s_mov_b64 s[40:41],s[86:87]
s_mov_b32 s42,4194304
s_mov_b32 s43,0x00020000
L 0
s_add_u32 s64,s75,0
buffer_atomic_pk_add_bf16 v22,v9,s[40:43],s64 offen sc1
s_add_u32 s64,s75,8192
buffer_atomic_pk_add_bf16 v23,v9,s[40:43],s64 offen sc1
s_add_u32 s64,s75,16384
buffer_atomic_pk_add_bf16 v24,v9,s[40:43],s64 offen sc1
s_add_u32 s64,s75,24576
buffer_atomic_pk_add_bf16 v25,v9,s[40:43],s64 offen sc1
s_mov_b32 s72,0x7fff0000
s_mov_b32 s69,0x7fff0000
s_mov_b32 s67,0x7fff0000
s_mov_b32 s77,0x7fff0000
s_branch Lg4_poll_%=
Lg4_fina1_%=:
s_sub_u32 s75,s67,s85
v_lshlrev_b32 v62,16,v58
v_add_f32 v18,v18,v62
v_and_b32 v63,0xffff0000,v58
v_add_f32 v19,v19,v63
v_lshlrev_b32 v62,16,v59
v_add_f32 v20,v20,v62
v_and_b32 v63,0xffff0000,v59
v_add_f32 v21,v21,v63
v_lshlrev_b32 v62,16,v60
v_add_f32 v22,v22,v62
v_and_b32 v63,0xffff0000,v60
v_add_f32 v23,v23,v63
v_lshlrev_b32 v62,16,v61
v_add_f32 v24,v24,v62
v_and_b32 v63,0xffff0000,v61
v_add_f32 v25,v25,v63
v_cvt_pk_bf16_f32 v18,v18,v19
v_cvt_pk_bf16_f32 v19,v20,v21
v_cvt_pk_bf16_f32 v20,v22,v23
v_cvt_pk_bf16_f32 v21,v24,v25
v_lshrrev_b32 v62,8,v0
v_lshl_add_u32 v62,v62,5,v0
v_add_u32 v62,s73,v62
ds_write_b128 v62,v[18:21] offset:0
ds_read2_b32 v[22:23],v5 offset0:0 offset1:16
ds_read2_b32 v[24:25],v5 offset0:32 offset1:48
s_mov_b64 s[40:41],s[86:87]
s_mov_b32 s42,4194304
s_mov_b32 s43,0x00020000
L 0
s_add_u32 s64,s75,0
buffer_atomic_pk_add_bf16 v22,v9,s[40:43],s64 offen sc1
s_add_u32 s64,s75,8192
buffer_atomic_pk_add_bf16 v23,v9,s[40:43],s64 offen sc1
s_add_u32 s64,s75,16384
buffer_atomic_pk_add_bf16 v24,v9,s[40:43],s64 offen sc1
s_add_u32 s64,s75,24576
buffer_atomic_pk_add_bf16 v25,v9,s[40:43],s64 offen sc1
s_mov_b32 s72,0x7fff0000
s_mov_b32 s69,0x7fff0000
s_mov_b32 s67,0x7fff0000
s_mov_b32 s77,0x7fff0000
s_branch Lg4_done_%=
Lg4_done_%=:
s_nop 0
s_barrier
buffer_store_dword v4,v3,s[36:39],s70 offen offset:0 sc0 sc1
s_waitcnt vmcnt(1)
s_barrier
buffer_store_dword v4,v3,s[36:39],s72 offen offset:0 sc0 sc1
s_waitcnt vmcnt(0) lgkmcnt(0)
s_barrier
s_endpgm
Lcode_g4_e:
.purgem M
.purgem R
.purgem T
.purgem D
.purgem L
)"
  :: "s"(ws), "s"(__builtin_amdgcn_readfirstlane(sWave)), "s"(__builtin_amdgcn_readfirstlane(sG0)), "s"(__builtin_amdgcn_readfirstlane(sStep)), "s"(__builtin_amdgcn_readfirstlane(sOutOff)), "s"(__builtin_amdgcn_readfirstlane(sFoutOff)), "s"(__builtin_amdgcn_readfirstlane(sAuxOff)), "s"(__builtin_amdgcn_readfirstlane(sWw)), "s"(__builtin_amdgcn_readfirstlane(sBuf)), "s"(__builtin_amdgcn_readfirstlane(sRk)), "s"(__builtin_amdgcn_readfirstlane(sExp)), "v"(vL16), "v"(vFlag), "v"(vSnapOff), "v"(vSnapRd), "v"(vWd0), "v"(vWd1), "v"(vWr0), "v"(vWr1), "v"(vSp), "v"(vSn), "s"(w4), "s"(out) : "v255", "v0", "v1", "v2", "v3", "v4", "v5", "v6", "v7", "v8", "v9", "v10", "v11", "v12", "v13", "v14", "v15", "v16", "v17", "v18", "v19", "v20", "v21", "v22", "v23", "v24", "v25", "v26", "v27", "v28", "v29", "v30", "v31", "v32", "v33", "v34", "v35", "v36", "v37", "v38", "v39", "v40", "v41", "v42", "v43", "v44", "v45", "v46", "v47", "v48", "v49", "v50", "v51", "v52", "v53", "v54", "v55", "v56", "v57", "v58", "v59", "v60", "v61", "v62", "v63", "v64", "v65", "v66", "v67", "v68", "v69", "v70", "v71", "v72", "v73", "v74", "v75", "v76", "v77", "v78", "v79", "v80", "v81", "v82", "v83", "v84", "v85", "v86", "v87", "v88", "v89", "v90", "v91", "v92", "v93", "v94", "v95", "v96", "v97", "v98", "v99", "v100", "v101", "v102", "v103", "v104", "v105", "v106", "v107", "v108", "v109", "v110", "v111", "v112", "v113", "v114", "v115", "v116", "v117", "v118", "v119", "v120", "v121", "v122", "v123", "v124", "v125", "v126", "v127", "v128", "v129", "v130", "v131", "v132", "v133", "v134", "v135", "v136", "v137", "v138", "v139", "v140", "v141", "v142", "v143", "v144", "v145", "v146", "v147", "v148", "v149", "v150", "v151", "v152", "v153", "v154", "v155", "v156", "v157", "v158", "v159", "v160", "v161", "v162", "v163", "v164", "v165", "v166", "v167", "v168", "v169", "v170", "v171", "v172", "v173", "v174", "v175", "v176", "v177", "v178", "v179", "v180", "v181", "v182", "v183", "v184", "v185", "v186", "v187", "v188", "v189", "v190", "v191", "v192", "v193", "v194", "v195", "v196", "v197", "v198", "v199", "v200", "v201", "v202", "v203", "v204", "v205", "v206", "v207", "v208", "v209", "v210", "v211", "v212", "v213", "v214", "v215", "v216", "v217", "v218", "v219", "v220", "v221", "v222", "v223", "v224", "v225", "v226", "v227", "v228", "v229", "v230", "v231", "a0", "a1", "a2", "a3", "a4", "a5", "a6", "a7", "a8", "a9", "a10", "a11", "a12", "a13", "a14", "a15", "a16", "a17", "a18", "a19", "a20", "a21", "a22", "a23", "a24", "a25", "a26", "a27", "a28", "a29", "a30", "a31", "a32", "a33", "a34", "a35", "a36", "a37", "a38", "a39", "a40", "a41", "a42", "a43", "a44", "a45", "a46", "a47", "a48", "a49", "a50", "a51", "a52", "a53", "a54", "a55", "a56", "a57", "a58", "a59", "a60", "a61", "a62", "a63", "a64", "a65", "a66", "a67", "a68", "a69", "a70", "a71", "a72", "a73", "a74", "a75", "a76", "a77", "a78", "a79", "a80", "a81", "a82", "a83", "a84", "a85", "a86", "a87", "a88", "a89", "a90", "a91", "a92", "a93", "a94", "a95", "a96", "a97", "a98", "a99", "a100", "a101", "a102", "a103", "a104", "a105", "a106", "a107", "a108", "a109", "a110", "a111", "a112", "a113", "a114", "a115", "a116", "a117", "a118", "a119", "a120", "a121", "a122", "a123", "a124", "a125", "a126", "a127", "a128", "a129", "a130", "a131", "a132", "a133", "a134", "a135", "a136", "a137", "a138", "a139", "a140", "a141", "a142", "a143", "a144", "a145", "a146", "a147", "a148", "a149", "a150", "a151", "a152", "a153", "a154", "a155", "a156", "a157", "a158", "a159", "a160", "a161", "a162", "a163", "a164", "a165", "a166", "a167", "a168", "a169", "a170", "a171", "a172", "a173", "a174", "a175", "a176", "a177", "a178", "a179", "a180", "a181", "a182", "a183", "a184", "a185", "a186", "a187", "a188", "a189", "a190", "a191", "a192", "a193", "a194", "a195", "a196", "a197", "a198", "a199", "a200", "a201", "a202", "a203", "a204", "a205", "a206", "a207", "a208", "a209", "a210", "a211", "a212", "a213", "a214", "a215", "a216", "a217", "a218", "a219", "a220", "a221", "a222", "a223", "a224", "a225", "a226", "a227", "a228", "a229", "a230", "a231", "a232", "a233", "a234", "a235", "a236", "a237", "a238", "a239", "a240", "a241", "a242", "a243", "a244", "a245", "a246", "a247", "a248", "a249", "a250", "a251", "a252", "a253", "a254", "a255", "s36", "s37", "s38", "s39", "s40", "s41", "s42", "s43", "s44", "s45", "s46", "s47", "s48", "s49", "s50", "s51", "s52", "s53", "s54", "s55", "s56", "s57", "s58", "s59", "s60", "s61", "s62", "s63", "s64", "s65", "s66", "s67", "s68", "s69", "s70", "s71", "s72", "s73", "s74", "s75", "s76", "s77", "s78", "s79", "s80", "s81", "s82", "s83", "s84", "s85", "s86", "s87", "s88", "s89", "s90", "s91", "s92", "s93", "s94", "s95", "s96", "s97", "vcc", "scc", "memory");
    __builtin_unreachable();
  } else {
    LANE_ID(lane2);
    constexpr int N1 = 33 - (NR4 - 6) * 16, T1 = LAST_TASKS > RLN * N1 ? RLN * N1 : LAST_TASKS;
    const int w = kind == 6 ? 0 : kind == 1 ? 1 + (3 - rep) * 8 + slice : N1 + rep * 8 + slice;
    if (kind != 2) for (int t = w; t < T1; t += N1) row_last(ws, out, t, lane2, wave, ep);
    for (int t = T1 + (w + 32) % (N1 + 32); t < LAST_TASKS; t += N1 + 32) row_last(ws, out, t, lane2, wave, ep);
    if (kind == 2 && rep == 0 && slice == 0 && wave == 0) set_flag(ws + OFF_EP, lane2, ep);
    CTS(14);
    asm volatile("s_endpgm\nLcode_end:" ::: "memory");
    __builtin_unreachable();
  }
}

extern "C" void kernel_launch(void* const* d_in, const int* in_sizes, int n_in, void* d_out, int out_size,
                              void* d_ws, size_t ws_size, hipStream_t stream) {
  const u16* q = (const u16*)d_in[0];
  const u16* k = (const u16*)d_in[1];
  const u16* v = (const u16*)d_in[2];
  const u16* shift_k = (const u16*)d_in[3];
  const u16* ssm_k = (const u16*)d_in[4];
  const u16* ssm_D = (const u16*)d_in[5];
  const u16* w1 = (const u16*)d_in[7];
  const u16* w2 = (const u16*)d_in[8];
  const u16* w3 = (const u16*)d_in[9];
  const u16* w4 = (const u16*)d_in[10];
  flow_kernel<<<256, 256, 0, stream>>>(q, k, v, shift_k, ssm_k, ssm_D, w1, w2, w3, w4, (char*)d_ws, (u16*)d_out);
}
```

```cpp
#include <hip/hip_runtime.h>
#include <hip/hip_bf16.h>
#include <stdint.h>
#include <type_traits>

typedef short s16x8 __attribute__((ext_vector_type(8)));
typedef short s16x4 __attribute__((ext_vector_type(4)));
typedef float f32x4 __attribute__((ext_vector_type(4)));
typedef unsigned short u16;
typedef unsigned short u16x8 __attribute__((ext_vector_type(8)));
typedef unsigned short u16x4 __attribute__((ext_vector_type(4)));
typedef float f32x2 __attribute__((ext_vector_type(2)));
typedef __bf16 bf16x2 __attribute__((ext_vector_type(2)));
typedef unsigned u32x4v __attribute__((ext_vector_type(4)));
typedef __attribute__((address_space(1))) unsigned gu32;

constexpr int S_ = 2048, D_ = 1024, F_ = 3584;

constexpr unsigned GELU_CB = 0xbeeff558u;
constexpr int WS_USED = 111243392, LAST_TASKS = 100, ALLZ = 1, U0 = 170, U1 = 224, NR4 = 8, RHELP = 1, PRE1 = 1;
constexpr int OFF_KVR = 4194304, OFF_XP = 8388608, OFF_HS = 17825792, OFF_X1 = 31457280, OFF_G = 40894464, OFF_P0 = 71303168, OFF_P1 = 89128960, OFF_Y = 98566144, OFF_S1 = 106954752, OFF_ST = 107479040, OFF_FL = 109051904, OFF_FXP = 109056000, OFF_FH = 109334528, OFF_FX1 = 109613056, OFF_FG = 109891584, OFF_FP = 110587904, OFF_FKV = 111144960, OFF_FY = 111177728, OFF_EP = 111243264, OFF_DBG = 115343360;

#define WS_DIAG 0

__device__ __forceinline__ float bf2f(u16 v) { return __uint_as_float(((unsigned)v) << 16); }
__device__ __forceinline__ u16 f2bf(float f) { return __builtin_bit_cast(u16, (__bf16)f); }
__device__ __forceinline__ float rbf(float f) { return bf2f(f2bf(f)); }
__device__ __forceinline__ void st16(void* p, u16x8 v) {
  asm volatile("global_store_dwordx4 %0, %1, off sc0 sc1\n\ts_nop 1" :: "v"(p), "v"(v) : "memory");
}
#define WAIT_VM(n) asm volatile("s_waitcnt vmcnt(%0)" ::"n"(n) : "memory")
#define WAIT_LGKM0() asm volatile("s_waitcnt lgkmcnt(0)" ::: "memory")

__device__ __forceinline__ void touch_lds(const char* p, unsigned a) {
  asm volatile("s_mov_b32 m0, %1\n\ts_nop 0\n\tglobal_load_lds_dword %0, off" :: "v"(p), "s"(a) : "memory");
}
__device__ __forceinline__ void touch_sc1(const char* p, unsigned a) {
  asm volatile("s_mov_b32 m0, %1\n\ts_nop 0\n\tglobal_load_lds_dword %0, off sc1" :: "v"(p), "s"(a) : "memory");
}
#define CTS(i)
__device__ __forceinline__ f32x2 up2(unsigned p) { return (f32x2){__uint_as_float(p << 16), __uint_as_float(p & 0xffff0000u)}; }
__device__ __forceinline__ unsigned pk2(f32x2 v) { return __builtin_bit_cast(unsigned, __builtin_convertvector(v, bf16x2)); }
__device__ __forceinline__ f32x2 rbf2(f32x2 v) { return up2(pk2(v)); }
__device__ __forceinline__ void set_flag(char* p, int i, unsigned ep) {
  if (i < 32) __hip_atomic_store((gu32*)p + i, ep, __ATOMIC_RELAXED, __HIP_MEMORY_SCOPE_AGENT);
}
constexpr int CV_HP = (2048 + 32) * 2, CV_UR = (2048 + 256) * 2, CV_CH = 2 * CV_HP + CV_UR;
constexpr int NCV = 174;
constexpr int CV_STG = 8 * CV_CH;
constexpr int YP = D_ * 16;

struct KvRegs { u32x4v k0, k1, k2, k3, k4, k5, k6, v0, v1, v2, v3, s0, s1, s2, s3; };
#define KV_LOADS                                                                                                                       \
  "v_lshrrev_b32 %[a], 3, %[tid]\n\tv_and_b32 %[c], 7, %[tid]\n\ts_lshr_b32 s90, %[tile], 4\n\ts_and_b32 s91, %[tile], 15\n\t"          \
  "v_lshlrev_b32 %[a], 13, %[a]\n\tv_lshlrev_b32 %[c], 4, %[c]\n\ts_lshl_b32 s90, s90, 18\n\ts_lshl_b32 s91, s91, 7\n\t"                 \
  "v_add_u32 %[a], s90, %[a]\n\tv_add_u32 %[c], s91, %[c]\n\tv_subrev_u32 %[h], 0x1000, %[a]\n\tv_max_i32 %[h], 0x800, %[h]\n\t"         \
  "v_add3_u32 %[a], %[a], %[c], s92\n\tv_add_u32 %[h], %[h], %[c]\n\tv_lshlrev_b32 %[c], 2, %[c]\n\ts_waitcnt lgkmcnt(0)\n\t"            \
  "global_load_dwordx4 %3, %[a], s[76:77] offset:-4096 nt\n\tglobal_load_dwordx4 %4, %[a], s[76:77] offset:-2048 nt\n\t"                \
  "global_load_dwordx4 %5, %[a], s[76:77] nt\n\tglobal_load_dwordx4 %6, %[a], s[76:77] offset:2048 nt\n\t"                              \
  "global_load_dwordx4 %7, %[a], s[78:79] offset:-4096 nt\n\tglobal_load_dwordx4 %8, %[a], s[78:79] offset:-2048 nt\n\t"                \
  "global_load_dwordx4 %9, %[a], s[78:79] nt\n\tglobal_load_dwordx4 %10, %[a], s[78:79] offset:2048 nt\n\t"                             \
  "global_load_dwordx4 %0, %[h], s[76:77] offset:-2048 nt\n\tglobal_load_dwordx4 %1, %[h], s[76:77] nt\n\t"                             \
  "global_load_dwordx4 %2, %[h], s[76:77] offset:2048 nt\n\t"                                                                           \
  "global_load_dwordx4 %11, %[c], s[80:81]\n\tglobal_load_dwordx4 %12, %[c], s[80:81] offset:16\n\t"                                    \
  "global_load_dwordx4 %13, %[c], s[80:81] offset:32\n\tglobal_load_dwordx4 %14, %[c], s[80:81] offset:48\n\t"
#define KV_OUTS(R) "=&v"(R.k0), "=&v"(R.k1), "=&v"(R.k2), "=&v"(R.k3), "=&v"(R.k4), "=&v"(R.k5), "=&v"(R.k6), "=&v"(R.v0), "=&v"(R.v1), "=&v"(R.v2), \
                   "=&v"(R.v3), "=&v"(R.s0), "=&v"(R.s1), "=&v"(R.s2), "=&v"(R.s3), [a] "=&v"(ta), [h] "=&v"(th), [c] "=&v"(tc)
#define KV_CLOB "s74", "s75", "s76", "s77", "s78", "s79", "s80", "s81", "s82", "s83", "s90", "s91", "s92", "scc", "vcc", "memory"
template <int N>
__device__ __forceinline__ void kv_wait(KvRegs& R) {
  asm volatile("s_waitcnt vmcnt(%15)" : "+v"(R.k0), "+v"(R.k1), "+v"(R.k2), "+v"(R.k3), "+v"(R.k4), "+v"(R.k5), "+v"(R.k6), "+v"(R.v0), "+v"(R.v1), "+v"(R.v2),
               "+v"(R.v3), "+v"(R.s0), "+v"(R.s1), "+v"(R.s2), "+v"(R.s3) : "n"(N) : "memory");
}
__device__ __forceinline__ void kv_store(KvRegs& R, char* __restrict__ ws, char* T, int b, int tid) {
  const int t0 = (b >> 4) * 128, c0 = (b & 15) * 64, ch8 = tid & 7, rg = tid >> 3;
  if (t0 + 4 * rg < 3) R.k0 = R.k1 = R.k2 = (u32x4v){0u, 0u, 0u, 0u};
  typedef unsigned u32x2 __attribute__((ext_vector_type(2)));
  const u32x4v kr[7] = {R.k0, R.k1, R.k2, R.k3, R.k4, R.k5, R.k6}, vr[4] = {R.v0, R.v1, R.v2, R.v3}, sk[4] = {R.s0, R.s1, R.s2, R.s3};
#pragma unroll
  for (int j2 = 0; j2 < 4; ++j2) {
    const f32x2 sa = up2(sk[j2][0]), sb = up2(sk[j2][1]), sc = up2(sk[j2][2]), sd = up2(sk[j2][3]);
    const f32x2 s0 = {sa[0], sc[0]}, s1 = {sa[1], sc[1]}, s2 = {sb[0], sd[0]}, s3 = {sb[1], sd[1]};
    f32x2 kf[7], o[4];
#pragma unroll
    for (int r = 0; r < 7; ++r) kf[r] = up2(kr[r][j2]);
#pragma unroll
    for (int r = 0; r < 4; ++r)
      o[r] = __builtin_elementwise_fma(kf[r + 3], s0, __builtin_elementwise_fma(kf[r + 2], s1, __builtin_elementwise_fma(kf[r + 1], s2, kf[r] * s3))) * up2(vr[r][j2]);
#pragma unroll
    for (int e = 0; e < 2; ++e)
      *(u32x2*)(T + (8 * ch8 + 2 * j2 + e) * 264 + (124 - 4 * rg) * 2) = (u32x2){pk2((f32x2){o[3][e], o[2][e]}), pk2((f32x2){o[1][e], o[0][e]})};
  }
  __syncthreads();
  u16* kvr = (u16*)(ws + OFF_KVR);
#pragma unroll
  for (int i = 0; i < 4; ++i) {
    const int idx = tid + 256 * i, ch = idx >> 4, cb = idx & 15;
    const u16x4 lo = *(const u16x4*)(T + ch * 264 + cb * 16), hi = *(const u16x4*)(T + ch * 264 + cb * 16 + 8);
    st16(kvr + (size_t)(c0 + ch) * S_ + (1920 - t0) + cb * 8, __builtin_shufflevector(lo, hi, 0, 1, 2, 3, 4, 5, 6, 7));
  }
}

#define LASTSLEEP 4
__device__ __forceinline__ void wait_flags(const char* p, unsigned ep) {
  gu32* f = (gu32*)p;
  for (int it = 0; it < 20000; ++it) {
    if (__all(__hip_atomic_load(f, __ATOMIC_RELAXED, __HIP_MEMORY_SCOPE_AGENT) == ep)) break;
    __builtin_amdgcn_s_sleep(LASTSLEEP);
  }
  asm volatile("" ::: "memory");
}
__device__ __forceinline__ void row_last(char* __restrict__ ws, u16* __restrict__ out, int t, int lane, int wave, unsigned ep) {
  const __amdgpu_buffer_rsrc_t rs = __builtin_amdgcn_make_buffer_rsrc(ws, 0, WS_USED, 0x00020000);
  const int r8 = lane & 7, seg = lane >> 3, un = 128 + (t >> 1);
  const unsigned fo = un * 32768 + (wave * 8 + (seg >> 2)) * 1024 + ((seg & 3) * 16 + (t & 1) * 8 + r8) * 16;
  wait_flags(ws + OFF_FP + un * 2048 + ((lane & 2) * 4 + wave * 2 + (lane & 1)) * 128, ep);
  u32x4v p0[4], p1[4];
#pragma unroll
  for (int i = 0; i < 4; ++i) {
    p0[i] = __builtin_bit_cast(u32x4v, __builtin_amdgcn_raw_buffer_load_b128(rs, fo + i * 2048, OFF_P0, 16));
    p1[i] = __builtin_bit_cast(u32x4v, __builtin_amdgcn_raw_buffer_load_b128(rs, fo + i * 2048, OFF_P1, 16));
  }
#pragma unroll
  for (int i = 0; i < 4; ++i) {
    u32x4v o;
#pragma unroll
    for (int e = 0; e < 4; ++e) o[e] = pk2(up2(p0[i][e]) + up2(p1[i][e]));
    st16(out + (size_t)(t * 8 + r8) * D_ + wave * 256 + i * 64 + seg * 8, __builtin_bit_cast(u16x8, o));
  }
}

__device__ __forceinline__ int swzB(int r) { return ((((r >> 1) & 1) | (((r >> 3) & 1) << 1)) << 1); }
#define LANE_ID(x) int x; asm volatile("v_mbcnt_lo_u32_b32 %0, -1, 0\n\tv_mbcnt_hi_u32_b32 %0, -1, %0" : "=v"(x))
#define W12 6
#define RLN 3
#define FT 3
#define QTB 1
constexpr int QT0 = 34 * 128;
#define CODE_MAX(x, y) "(((" x ">" y ")&" x ")|((" x "<=" y ")&" y "))"
constexpr int HU = 40960;
#define G3OR 5
constexpr int NRW = 22 - G3OR;
__global__ __launch_bounds__(256, 1) void flow_kernel(const u16* __restrict__ q, const u16* __restrict__ kin, const u16* __restrict__ vin,
                                                      const u16* __restrict__ shift_k, const u16* __restrict__ ssm_k, const u16* __restrict__ ssm_D,
                                                      const u16* __restrict__ w1, const u16* __restrict__ w2,
                                                      const u16* __restrict__ w3, const u16* __restrict__ w4, char* __restrict__ ws,
                                                      u16* __restrict__ out) {
  __shared__ __attribute__((aligned(16))) char lds[163840];
  KvRegs KR;
  unsigned ew, ta, th, tc;
  int b, tile;
  asm volatile("Lcode_b:\n\ts_load_dwordx4 s[76:79], %[ka], 0x8\n\ts_load_dwordx2 s[82:83], %[ka], 0x50\n\t"
               "s_getpc_b64 s[74:75]\n\ts_lshr_b32 s90, %[bi], 3\n\ts_mov_b32 m0, 0x27f00\n\tv_lshlrev_b32 %[a], 5, %[tid]\n\tv_add_lshl_u32 %[a], %[a], s90, 7\n\t"
               "v_min_u32 %[a], " CODE_MAX(CODE_MAX(CODE_MAX("(Lcode_g3_e-Lcode_b)", "(Lcode_g3o_e-Lcode_b)"), CODE_MAX("(Lcode_g4_e-Lcode_b)", "(Lcode_g12_e-Lcode_b)")), "(Lcode_end-Lcode_b)") "-64, %[a]\n\t"
               "global_load_lds_dword %[a], s[74:75]\n\ts_load_dwordx2 s[80:81], %[ka], 0x18\n\t"
               "s_mov_b32 %[b], %[bi]\n\ts_bfe_u32 s91, %[bi], 0x20001\n\ts_lshl2_add_u32 %[tile], s90, s91\n\ts_movk_i32 s92, 0x1000\n\t"
               "s_lshl_b32 s90, %[bi], 7\n\ts_add_u32 %[tile], %[tile], s90\n\ts_add_u32 %[tile], %[tile], 64\n\ts_and_b32 %[tile], %[tile], 255\n\t"
               KV_LOADS "v_mov_b32 %[a], %[eo]\n\tglobal_load_dword %[ew], %[a], s[82:83] sc1"
               : KV_OUTS(KR), [ew] "=&v"(ew), [b] "=&s"(b), [tile] "=&s"(tile)
               : [ka] "s"(__builtin_amdgcn_kernarg_segment_ptr()), [bi] "s"(blockIdx.x), [tid] "v"(threadIdx.x), [eo] "n"(OFF_EP) : KV_CLOB);
  const unsigned t_start = 0;
  const int tid = threadIdx.x, lane = tid & 63, wave = __builtin_amdgcn_readfirstlane(tid >> 6);
  const int xcd = b & 7, idx = b >> 3;
  const unsigned sLds = (unsigned)(size_t)lds;
  const int o8 = idx & 7;
  int kind, rep, slice, kh = 0;
  int j3 = 0, j4 = 0;
  const int xp = xcd >> 1, pr = xp & 1;
  if (!(xcd & 1)) {
    if (idx < 20) { kind = 4; rep = idx < 16 ? (xp >> 1) * 3 + (idx >> 3) : 2; kh = pr; slice = idx < 16 ? o8 : (xp >> 1) * 4 + o8; j4 = rep * 16 + kh * 8 + slice; }
    else if (idx >= 24) { kind = 1; rep = xp; slice = o8; }
    else { kind = 3; rep = 4; slice = xp * 4 + idx - 20; j3 = 72 + slice; }
  } else if (idx >= 24) { kind = 2; rep = xp; slice = o8; }
  else if (idx < 18) { kind = 3; rep = pr * 2 + (idx >= 9); slice = (xp >> 1) * 9 + idx - 9 * (idx >= 9); j3 = rep * 18 + slice; }
  else if (idx < 22 || b == 191) { kind = 6; rep = 0; slice = idx < 22 ? xp * 4 + idx - 18 : 16; }
  else if (idx == 22 || xp == 0) { kind = 5; rep = idx == 22 ? pr * 2 + (xp >> 1) : 4; slice = 18; j3 = 90 + rep; }
  else { kind = 3; rep = 4; slice = 15 + xp; j3 = 72 + slice; }
  CTS(0);
  const unsigned sScr = sLds + 163584;
  unsigned ep = 0;
  auto w_touch = [&](const u16* w) {
#pragma unroll
    for (int i = 0; i < 2; ++i) touch_lds((const char*)w + (size_t)(rep * 256 + i * 128 + (tid >> 1)) * 2048 + slice * 256 + (tid & 1) * 128, sScr);
  };
  {
  const bool isconv = kind >= 3 && kind != 6 && j3 < 94;
  const int nw = (kind == 4 || j3 >= 92) ? 2 : 1, c0 = kind == 4 ? 8 * j4 : j3 < 92 ? 640 + 4 * j3 : j3 < 94 ? 272 + 8 * j3 : 0, cw = c0 + nw * wave;
  const unsigned hd = isconv ? sLds + 2 * wave * CV_CH + 32 : 0x30000u;
  auto h_dma = [&](bool first) {
#pragma unroll
    for (int j = 0; j < 2; ++j) {
      const u16* hp = ssm_k + (size_t)cw * S_ + (isconv && j < nw ? j * S_ + lane * 8 : 0);
      if (first) asm volatile("s_mov_b32 m0, %1\n\ts_nop 0\n\tglobal_load_lds_dwordx4 %0, off nt" :: "v"(hp), "s"(hd + j * CV_CH) : "memory");
      else asm volatile("s_mov_b32 m0, %1\n\ts_nop 0\n\tglobal_load_lds_dwordx4 %0, off offset:1024 nt\n\t"
                        "global_load_lds_dwordx4 %0, off offset:2048 nt\n\tglobal_load_lds_dwordx4 %0, off offset:3072 nt" :: "v"(hp), "s"(hd + j * CV_CH) : "memory");
    }
  };
  auto w_dma = [&](int t0, int t1) {
    const char* wp = (const char*)w1 + (16 * wave + (lane >> 3)) * 2048 + slice * 256;
    const unsigned c0_ = ((lane & 7) ^ swzB(lane >> 3)) * 16, c1_ = 8 * 2048 + ((lane & 7) ^ swzB(8 + (lane >> 3))) * 16;
#pragma unroll
    for (int t = t0; t < t1; ++t)
#pragma unroll
      for (int i = 0; i < 2; ++i)
        asm volatile("s_mov_b32 m0, %1\n\ts_nop 0\n\tglobal_load_lds_dwordx4 %0, off\n\ts_add_u32 m0, m0, 8064\n\ts_nop 0\n\tglobal_load_lds_dwordx4 %0, off offset:128"
                     :: "v"(wp + t * 131072 + (i ? c1_ : c0_)), "s"(sLds + t * 16384 + wave * 2048 + i * 1024) : "scc", "memory");
  };
  if (PRE1 && kind == 1) w_dma(0, 2);
  else { h_dma(true); h_dma(false); }
  if (isconv) *(u32x4v*)(lds + (2 * wave + (lane >> 5)) * CV_CH + 2 * CV_HP + 4096 + (lane & 31) * 16) = (u32x4v){0u, 0u, 0u, 0u};
  if (isconv && lane < 8) *(u32x4v*)(lds + (2 * wave + (lane >> 2)) * CV_CH + ((lane >> 1) & 1) * CV_HP + (lane & 1) * 16) = (u32x4v){0u, 0u, 0u, 0u};
  if (FT & 2) touch_sc1(ws + OFF_FKV + (min(wave, 2) * 256 + b) * 128, sScr);
  kv_wait<(FT & 2) ? 10 : 9>(KR);
  CTS(12);
  kv_store(KR, ws, lds + CV_STG, tile, tid);
  CTS(1);
  if (PRE1 && kind == 1) w_dma(2, 6);
  const unsigned dw = ((const unsigned*)ssm_D)[cw >> 1];
  asm volatile("s_cmp_eq_u32 %1, %2\n\ts_cbranch_scc1 Lew%=\n\ts_waitcnt vmcnt(0)\nLew%=:\n\ts_waitcnt vmcnt(16)" : "+v"(ew) : "s"(__builtin_amdgcn_readfirstlane(kind)), "n"(PRE1 ? 1 : 99) : "scc", "memory");
  ep = __builtin_amdgcn_readfirstlane(ew) + 1u;
  if (ep == 0u) ep = 1u;
  __builtin_amdgcn_s_barrier();
  set_flag(ws + OFF_FKV + tile * 128, tid, ep);
  CTS(2);
  if (kind < 3 && (W12 & kind) != 0) w_touch(kind == 1 ? w1 : w2);
  if (kind == 2 && (W12 & 4) != 0) w_touch(w1);
  if (kind == 2 && FT) {
#pragma unroll
    for (int i = 0; i < 2; ++i) touch_sc1(ws + OFF_FXP + (size_t)min((i * 32 + rep * 8 + slice) * 256 + tid, (OFF_FKV - OFF_FXP) / 128 - 1) * 128, sScr);
  }
  if (isconv) {
    LANE_ID(lane);
    const int tid = wave * 64 + lane;
    const unsigned sC0 = (cw & 1) ? dw & 0xffff0000u : dw << 16, sC1 = dw & 0xffff0000u;
    const int l15 = lane & 15, lg = lane >> 4, cp = (l15 + 1) & 1, iw = kind == 4 ? j4 : 80 + j3, own = 2 * wave * CV_CH;
    const int ch = tid & (4 * nw - 1), tb = nw == 2 ? tid >> 3 : tid >> 2, slot = nw == 2 ? ch : 2 * ch;
    const unsigned sWave = wave, sBuf = nw, sAuxOff = OFF_KVR + cw * 4096, sFoutOff = OFF_FY + iw * 128, sExp = __builtin_amdgcn_readfirstlane(ep), sDbg = 110u * 1048576u + (b * 4 + wave) * 8192;
    const unsigned vL16 = lane * 16, vPA = own + cp * CV_HP + 2 * (8 * lg + l15 + 1 - cp), vPB = own + 2 * CV_HP + 2 * (2032 - 16 * l15 + 8 * lg) - 512 * 7;
    const unsigned vPU = own + 2 * CV_HP + 2 * (2044 - 16 * l15 - 4 * lg) - 512 * 7, vPW = 32 * l15 + 8 * lg;
    const unsigned vOR = slot * CV_CH + tb * 16, vORA = CV_STG + slot * 1024 + tb * 16, vOG = tb * YP + (c0 + ch) * 16;
    const unsigned vFA = ((lane & 3) * 16 + (c0 >> 6)) * 128, vFB = ((4 + lane % 12) * 16 + (c0 >> 6)) * 128, vFlag = tid < 32 ? tid * 4 : 0x80000000u;
    const unsigned vTW = lane < 42 ? (iw * 168 + wave * 42 + lane) * 128 : 0x80000000u;
    const unsigned vTX = lane < 44 ? ((kind == 4 ? j4 : j3 - 12) * 176 + wave * 44 + lane) * 128 : 0x80000000u;
    (void)sDbg;
    asm volatile(
R"(.macro M d,w,x
v_mfma_f32_16x16x32_bf16 v[\d:\d+3], \w, v[\x:\x+3], v[\d:\d+3]
.endm
.macro R d,a,o
ds_read_b128 v[\d:\d+3], v\a offset:\o
.endm
.macro T d,a,o
ds_read_b64_tr_b16 \d, v\a offset:\o
.endm
.macro D a,s,o
buffer_load_dwordx4 v\a, s[36:39], s\s offen offset:\o sc1 lds
.endm
.macro L c
s_waitcnt lgkmcnt(\c)
.endm
Lcode_cv_b:
s_mov_b64 s[36:37],%0
s_mov_b32 s38,111243392
s_mov_b32 s39,0x00020000
s_mov_b32 s44,%1
s_mov_b32 s51,%2
s_mov_b32 s49,%3
s_mov_b32 s48,%4
s_mov_b32 s86,%5
s_mov_b32 s87,%6
s_mov_b32 s53,%7
v_mov_b32 v0,%8
v_mov_b32 v1,%9
v_mov_b32 v2,%10
v_mov_b32 v3,%11
v_mov_b32 v4,%12
v_mov_b32 v5,%13
v_mov_b32 v6,%14
v_mov_b32 v7,%15
v_mov_b32 v8,%16
v_mov_b32 v9,%17
v_mov_b32 v10,%18
v_mov_b32 v11,%19
v_mov_b32 v12,%20
s_mov_b32 s58,111144960
buffer_load_dword v13,v8,s[36:39],s58 offen offset:0 sc1
s_mul_i32 s57,s44,25856
v_add_u32 v18,s57,v0
v_add_u32 v18,32,v18
v_mov_b32 v19,s57
R 88,18,0
ds_read_b32 v92,v18 offset:16
R 152,18,12928
ds_read_b32 v156,v18 offset:12944
ds_read_b32 v24,v19 offset:32
ds_read_b32 v25,v19 offset:12960
s_mov_b64 s[40:41],%21
s_mov_b32 s42,7340032
s_mov_b32 s43,0x00020000
s_mov_b64 s[60:61],%22
s_mov_b32 s62,7340032
s_mov_b32 s63,0x00020000
v_mov_b32 v23,s53
s_mov_b32 s82,0
s_add_u32 s55,s49,4096
s_cmp_eq_u32 s51,2
s_cselect_b32 s55,s55,0x7fff0000
s_cselect_b32 s56,0,0x70000000
s_add_u32 s57,s57,8320
v_add_u32 v14,1024,v1
v_add_u32 v15,2048,v1
v_add_u32 v16,3072,v1
v_add_u32 v17,12928,v1
L 4
v_alignbyte_b32 v96,v89,v88,2
v_alignbyte_b32 v97,v90,v89,2
v_alignbyte_b32 v98,v91,v90,2
v_alignbyte_b32 v99,v92,v91,2
ds_write_b128 v18,v[96:99] offset:4160
L 3
v_alignbyte_b32 v160,v153,v152,2
v_alignbyte_b32 v161,v154,v153,2
v_alignbyte_b32 v162,v155,v154,2
v_alignbyte_b32 v163,v156,v155,2
ds_write_b128 v18,v[160:163] offset:17088
L 2
v_lshlrev_b32 v24,16,v24
v_lshlrev_b32 v25,16,v25
s_mov_b64 exec,1
ds_write_b32 v19,v24 offset:4188
ds_write_b32 v19,v25 offset:17116
s_mov_b64 exec,-1
s_lshl_b32 s78,s44,11
s_add_u32 s78,s78,103424
v_add_u32 v216,s78,v4
v_add_u32 v4,v19,v4
Lcv_bpa_%=:
s_waitcnt vmcnt(0)
v_cmp_eq_u32 vcc,s53,v13
s_add_u32 s82,s82,1
s_cmp_eq_u64 vcc,-1
s_cbranch_scc1 Lcv_bqa_%=
s_sleep 1
buffer_load_dword v13,v8,s[36:39],s58 offen offset:0 sc1
s_cmp_lt_u32 s82,20000
s_cbranch_scc1 Lcv_bpa_%=
s_waitcnt vmcnt(0)
Lcv_bqa_%=:
s_add_u32 m0,s57,0
s_nop 0
D 0,49,3072
s_add_u32 m0,s57,12928
s_nop 0
D 0,55,3072
ds_read2_b32 a[0:1],v1 offset0:0 offset1:1
ds_read2_b32 a[2:3],v1 offset0:2 offset1:3
ds_read2_b32 a[4:5],v1 offset0:128 offset1:129
ds_read2_b32 a[6:7],v1 offset0:130 offset1:131
ds_read2_b32 a[8:9],v1 offset0:16 offset1:17
ds_read2_b32 a[10:11],v1 offset0:18 offset1:19
ds_read2_b32 a[12:13],v1 offset0:144 offset1:145
ds_read2_b32 a[14:15],v1 offset0:146 offset1:147
ds_read2_b32 a[16:17],v1 offset0:32 offset1:33
ds_read2_b32 a[18:19],v1 offset0:34 offset1:35
ds_read2_b32 a[20:21],v1 offset0:160 offset1:161
ds_read2_b32 a[22:23],v1 offset0:162 offset1:163
ds_read2_b32 a[24:25],v1 offset0:48 offset1:49
ds_read2_b32 a[26:27],v1 offset0:50 offset1:51
ds_read2_b32 a[28:29],v1 offset0:176 offset1:177
ds_read2_b32 a[30:31],v1 offset0:178 offset1:179
ds_read2_b32 a[32:33],v1 offset0:64 offset1:65
ds_read2_b32 a[34:35],v1 offset0:66 offset1:67
ds_read2_b32 a[36:37],v1 offset0:192 offset1:193
ds_read2_b32 a[38:39],v1 offset0:194 offset1:195
ds_read2_b32 a[40:41],v1 offset0:80 offset1:81
ds_read2_b32 a[42:43],v1 offset0:82 offset1:83
ds_read2_b32 a[44:45],v1 offset0:208 offset1:209
ds_read2_b32 a[46:47],v1 offset0:210 offset1:211
ds_read2_b32 a[48:49],v1 offset0:96 offset1:97
ds_read2_b32 a[50:51],v1 offset0:98 offset1:99
ds_read2_b32 a[52:53],v1 offset0:224 offset1:225
ds_read2_b32 a[54:55],v1 offset0:226 offset1:227
ds_read2_b32 a[56:57],v1 offset0:112 offset1:113
ds_read2_b32 a[58:59],v1 offset0:114 offset1:115
ds_read2_b32 a[60:61],v1 offset0:240 offset1:241
ds_read2_b32 a[62:63],v1 offset0:242 offset1:243
s_cmp_eq_u32 s51,2
s_cbranch_scc1 Lcv_a2_%=
s_waitcnt vmcnt(0)
buffer_load_dword v13,v9,s[36:39],s58 offen offset:0 sc1
L 0
R 88,2,3584
R 92,2,3072
R 96,2,3648
R 100,2,3136
R 104,2,3712
R 108,2,3200
R 112,2,3776
R 116,2,3264
L 6
v_mfma_f32_16x16x32_bf16 v[40:43],a[0:3],v[88:91],0
R 120,2,3840
R 124,2,3328
v_mfma_f32_16x16x32_bf16 v[44:47],a[0:3],v[92:95],0
M 44,a[4:7],88
L 6
M 40,a[8:11],96
R 128,2,3904
R 132,2,3392
M 44,a[8:11],100
M 44,a[12:15],96
L 6
M 40,a[16:19],104
R 136,2,3968
R 140,2,3456
M 44,a[16:19],108
M 44,a[20:23],104
L 6
M 40,a[24:27],112
R 144,2,4032
R 148,2,3520
M 44,a[24:27],116
M 44,a[28:31],112
L 6
M 40,a[32:35],120
M 44,a[32:35],124
M 44,a[36:39],120
L 4
M 40,a[40:43],128
M 44,a[40:43],132
M 44,a[44:47],128
L 2
M 40,a[48:51],136
ds_read_b64 v[24:25],v3 offset:3584
ds_read_b64 v[26:27],v3 offset:3072
M 44,a[48:51],140
M 44,a[52:55],136
L 2
M 40,a[56:59],144
M 44,a[56:59],148
M 44,a[60:63],144
L 0
v_and_b32 v36,0xffff0000,v25
v_lshlrev_b32 v37,16,v25
v_and_b32 v38,0xffff0000,v24
v_lshlrev_b32 v39,16,v24
s_nop 0
v_fma_f32 v40,v36,s86,v40
v_fma_f32 v41,v37,s86,v41
v_fma_f32 v42,v38,s86,v42
v_fma_f32 v43,v39,s86,v43
v_cvt_pk_bf16_f32 v40,v40,v41
v_cvt_pk_bf16_f32 v41,v42,v43
ds_write_b64 v216,v[40:41] offset:0
v_and_b32 v36,0xffff0000,v27
v_lshlrev_b32 v37,16,v27
v_and_b32 v38,0xffff0000,v26
v_lshlrev_b32 v39,16,v26
v_fma_f32 v44,v36,s86,v44
v_fma_f32 v45,v37,s86,v45
v_fma_f32 v46,v38,s86,v46
v_fma_f32 v47,v39,s86,v47
v_cvt_pk_bf16_f32 v44,v44,v45
v_cvt_pk_bf16_f32 v45,v46,v47
ds_write_b64 v216,v[44:45] offset:512
s_branch Lcv_a3_%=
Lcv_a2_%=:
ds_read2_b32 a[64:65],v17 offset0:0 offset1:1
ds_read2_b32 a[66:67],v17 offset0:2 offset1:3
ds_read2_b32 a[68:69],v17 offset0:128 offset1:129
ds_read2_b32 a[70:71],v17 offset0:130 offset1:131
ds_read2_b32 a[72:73],v17 offset0:16 offset1:17
ds_read2_b32 a[74:75],v17 offset0:18 offset1:19
ds_read2_b32 a[76:77],v17 offset0:144 offset1:145
ds_read2_b32 a[78:79],v17 offset0:146 offset1:147
ds_read2_b32 a[80:81],v17 offset0:32 offset1:33
ds_read2_b32 a[82:83],v17 offset0:34 offset1:35
ds_read2_b32 a[84:85],v17 offset0:160 offset1:161
ds_read2_b32 a[86:87],v17 offset0:162 offset1:163
ds_read2_b32 a[88:89],v17 offset0:48 offset1:49
ds_read2_b32 a[90:91],v17 offset0:50 offset1:51
ds_read2_b32 a[92:93],v17 offset0:176 offset1:177
ds_read2_b32 a[94:95],v17 offset0:178 offset1:179
ds_read2_b32 a[96:97],v17 offset0:64 offset1:65
ds_read2_b32 a[98:99],v17 offset0:66 offset1:67
ds_read2_b32 a[100:101],v17 offset0:192 offset1:193
ds_read2_b32 a[102:103],v17 offset0:194 offset1:195
ds_read2_b32 a[104:105],v17 offset0:80 offset1:81
ds_read2_b32 a[106:107],v17 offset0:82 offset1:83
ds_read2_b32 a[108:109],v17 offset0:208 offset1:209
ds_read2_b32 a[110:111],v17 offset0:210 offset1:211
ds_read2_b32 a[112:113],v17 offset0:96 offset1:97
ds_read2_b32 a[114:115],v17 offset0:98 offset1:99
ds_read2_b32 a[116:117],v17 offset0:224 offset1:225
ds_read2_b32 a[118:119],v17 offset0:226 offset1:227
ds_read2_b32 a[120:121],v17 offset0:112 offset1:113
ds_read2_b32 a[122:123],v17 offset0:114 offset1:115
ds_read2_b32 a[124:125],v17 offset0:240 offset1:241
ds_read2_b32 a[126:127],v17 offset0:242 offset1:243
s_waitcnt vmcnt(0)
buffer_load_dword v13,v9,s[36:39],s58 offen offset:0 sc1
L 0
R 88,2,3584
R 92,2,3072
R 96,2,3648
R 100,2,3136
R 104,2,3712
R 108,2,3200
R 112,2,3776
R 116,2,3264
L 6
v_mfma_f32_16x16x32_bf16 v[40:43],a[0:3],v[88:91],0
R 120,2,3840
R 124,2,3328
v_mfma_f32_16x16x32_bf16 v[44:47],a[0:3],v[92:95],0
M 44,a[4:7],88
L 6
M 40,a[8:11],96
R 128,2,3904
R 132,2,3392
M 44,a[8:11],100
M 44,a[12:15],96
L 6
M 40,a[16:19],104
R 136,2,3968
R 140,2,3456
M 44,a[16:19],108
M 44,a[20:23],104
L 6
M 40,a[24:27],112
R 144,2,4032
R 148,2,3520
M 44,a[24:27],116
M 44,a[28:31],112
L 6
M 40,a[32:35],120
R 152,2,16512
R 156,2,16000
M 44,a[32:35],124
M 44,a[36:39],120
L 6
M 40,a[40:43],128
R 160,2,16576
R 164,2,16064
M 44,a[40:43],132
M 44,a[44:47],128
L 6
M 40,a[48:51],136
R 168,2,16640
R 172,2,16128
M 44,a[48:51],140
ds_read_b64 v[24:25],v3 offset:3584
ds_read_b64 v[26:27],v3 offset:3072
M 44,a[52:55],136
L 8
M 40,a[56:59],144
R 176,2,16704
R 180,2,16192
M 44,a[56:59],148
M 44,a[60:63],144
L 8
v_mfma_f32_16x16x32_bf16 v[48:51],a[64:67],v[152:155],0
R 184,2,16768
R 188,2,16256
v_mfma_f32_16x16x32_bf16 v[52:55],a[64:67],v[156:159],0
M 52,a[68:71],152
L 8
M 48,a[72:75],160
R 192,2,16832
R 196,2,16320
M 52,a[72:75],164
L 6
v_and_b32 v36,0xffff0000,v25
M 52,a[76:79],160
v_lshlrev_b32 v37,16,v25
v_and_b32 v38,0xffff0000,v24
v_lshlrev_b32 v39,16,v24
s_nop 0
M 48,a[80:83],168
R 200,2,16896
R 204,2,16384
M 52,a[80:83],172
v_fma_f32 v40,v36,s86,v40
v_fma_f32 v41,v37,s86,v41
M 52,a[84:87],168
v_fma_f32 v42,v38,s86,v42
v_fma_f32 v43,v39,s86,v43
v_cvt_pk_bf16_f32 v40,v40,v41
L 6
M 48,a[88:91],176
R 208,2,16960
R 212,2,16448
M 52,a[88:91],180
v_cvt_pk_bf16_f32 v41,v42,v43
ds_write_b64 v216,v[40:41] offset:0
M 52,a[92:95],176
v_and_b32 v36,0xffff0000,v27
v_lshlrev_b32 v37,16,v27
v_and_b32 v38,0xffff0000,v26
L 7
M 48,a[96:99],184
M 52,a[96:99],188
v_lshlrev_b32 v39,16,v26
v_fma_f32 v44,v36,s86,v44
M 52,a[100:103],184
v_fma_f32 v45,v37,s86,v45
v_fma_f32 v46,v38,s86,v46
v_fma_f32 v47,v39,s86,v47
L 5
M 48,a[104:107],192
M 52,a[104:107],196
v_cvt_pk_bf16_f32 v44,v44,v45
v_cvt_pk_bf16_f32 v45,v46,v47
M 52,a[108:111],192
ds_write_b64 v216,v[44:45] offset:512
L 4
M 48,a[112:115],200
ds_read_b64 v[24:25],v3 offset:16512
ds_read_b64 v[26:27],v3 offset:16000
M 52,a[112:115],204
M 52,a[116:119],200
L 4
M 48,a[120:123],208
M 52,a[120:123],212
M 52,a[124:127],208
L 0
v_and_b32 v36,0xffff0000,v25
v_lshlrev_b32 v37,16,v25
v_and_b32 v38,0xffff0000,v24
v_lshlrev_b32 v39,16,v24
s_nop 0
v_fma_f32 v48,v36,s87,v48
v_fma_f32 v49,v37,s87,v49
v_fma_f32 v50,v38,s87,v50
v_fma_f32 v51,v39,s87,v51
v_cvt_pk_bf16_f32 v48,v48,v49
v_cvt_pk_bf16_f32 v49,v50,v51
ds_write_b64 v216,v[48:49] offset:1024
v_and_b32 v36,0xffff0000,v27
v_lshlrev_b32 v37,16,v27
v_and_b32 v38,0xffff0000,v26
v_lshlrev_b32 v39,16,v26
v_fma_f32 v52,v36,s87,v52
v_fma_f32 v53,v37,s87,v53
v_fma_f32 v54,v38,s87,v54
v_fma_f32 v55,v39,s87,v55
v_cvt_pk_bf16_f32 v52,v52,v53
v_cvt_pk_bf16_f32 v53,v54,v55
ds_write_b64 v216,v[52:53] offset:1536
Lcv_a3_%=:
L 0
s_barrier
R 152,6,0
R 156,6,512
s_mov_b32 s78,98566144
L 1
buffer_store_dwordx4 v[152:155],v7,s[36:39],s78 offen offset:0 sc0 sc1
s_add_u32 s78,s56,99090432
L 0
buffer_store_dwordx4 v[156:159],v7,s[36:39],s78 offen offset:0 sc0 sc1
s_waitcnt vmcnt(2)
v_cmp_eq_u32 vcc,s53,v13
s_nop 0
s_cmp_eq_u64 vcc,-1
s_cbranch_scc0 Lcv_a_%=
s_add_u32 m0,s57,0
s_nop 0
D 0,49,2048
D 0,49,1024
D 0,49,0
s_add_u32 m0,s57,12928
s_nop 0
D 0,55,2048
D 0,55,1024
D 0,55,0
R 104,18,1024
ds_read_b32 v108,v18 offset:1040
R 120,18,2048
ds_read_b32 v124,v18 offset:2064
R 136,18,3072
ds_read_b32 v140,v18 offset:3088
L 4
v_alignbyte_b32 v112,v105,v104,2
v_alignbyte_b32 v113,v106,v105,2
v_alignbyte_b32 v114,v107,v106,2
v_alignbyte_b32 v115,v108,v107,2
ds_write_b128 v18,v[112:115] offset:5184
L 3
v_alignbyte_b32 v128,v121,v120,2
v_alignbyte_b32 v129,v122,v121,2
v_alignbyte_b32 v130,v123,v122,2
v_alignbyte_b32 v131,v124,v123,2
ds_write_b128 v18,v[128:131] offset:6208
L 2
v_alignbyte_b32 v144,v137,v136,2
v_alignbyte_b32 v145,v138,v137,2
v_alignbyte_b32 v146,v139,v138,2
v_alignbyte_b32 v147,v140,v139,2
ds_write_b128 v18,v[144:147] offset:7232
s_waitcnt vmcnt(6)
s_barrier
buffer_store_dword v23,v10,s[36:39],s48 offen offset:0 sc0 sc1
s_branch Lcv_b_%=
Lcv_a_%=:
R 104,18,1024
ds_read_b32 v108,v18 offset:1040
R 120,18,2048
ds_read_b32 v124,v18 offset:2064
R 136,18,3072
ds_read_b32 v140,v18 offset:3088
L 4
v_alignbyte_b32 v112,v105,v104,2
v_alignbyte_b32 v113,v106,v105,2
v_alignbyte_b32 v114,v107,v106,2
v_alignbyte_b32 v115,v108,v107,2
ds_write_b128 v18,v[112:115] offset:5184
L 3
v_alignbyte_b32 v128,v121,v120,2
v_alignbyte_b32 v129,v122,v121,2
v_alignbyte_b32 v130,v123,v122,2
v_alignbyte_b32 v131,v124,v123,2
ds_write_b128 v18,v[128:131] offset:6208
L 2
v_alignbyte_b32 v144,v137,v136,2
v_alignbyte_b32 v145,v138,v137,2
v_alignbyte_b32 v146,v139,v138,2
v_alignbyte_b32 v147,v140,v139,2
ds_write_b128 v18,v[144:147] offset:7232
s_waitcnt vmcnt(0)
s_barrier
buffer_store_dword v23,v10,s[36:39],s48 offen offset:0 sc0 sc1
buffer_load_dword v13,v9,s[36:39],s58 offen offset:0 sc1
Lcv_bpb_%=:
s_waitcnt vmcnt(0)
v_cmp_eq_u32 vcc,s53,v13
s_add_u32 s82,s82,1
s_cmp_eq_u64 vcc,-1
s_cbranch_scc1 Lcv_bqb_%=
s_sleep 1
buffer_load_dword v13,v9,s[36:39],s58 offen offset:0 sc1
s_cmp_lt_u32 s82,20000
s_cbranch_scc1 Lcv_bpb_%=
s_waitcnt vmcnt(0)
Lcv_bqb_%=:
s_add_u32 m0,s57,0
)"
R"(s_nop 0
D 0,49,2048
D 0,49,1024
D 0,49,0
s_add_u32 m0,s57,12928
s_nop 0
D 0,55,2048
D 0,55,1024
D 0,55,0
Lcv_b_%=:
s_waitcnt vmcnt(0)
ds_read2_b32 v[88:89],v1 offset0:0 offset1:1
ds_read2_b32 v[90:91],v1 offset0:2 offset1:3
R 120,2,3584
ds_read2_b32 v[92:93],v1 offset0:128 offset1:129
ds_read2_b32 v[94:95],v1 offset0:130 offset1:131
R 124,2,3072
ds_read2_b32 v[96:97],v14 offset0:0 offset1:1
ds_read2_b32 v[98:99],v14 offset0:2 offset1:3
R 128,2,2560
ds_read2_b32 v[100:101],v14 offset0:128 offset1:129
ds_read2_b32 v[102:103],v14 offset0:130 offset1:131
R 132,2,2048
ds_read2_b32 v[104:105],v15 offset0:0 offset1:1
ds_read2_b32 v[106:107],v15 offset0:2 offset1:3
R 136,2,1536
ds_read2_b32 v[108:109],v15 offset0:128 offset1:129
ds_read2_b32 v[110:111],v15 offset0:130 offset1:131
R 140,2,1024
ds_read2_b32 v[112:113],v16 offset0:0 offset1:1
ds_read2_b32 v[114:115],v16 offset0:2 offset1:3
R 144,2,512
ds_read2_b32 v[116:117],v16 offset0:128 offset1:129
ds_read2_b32 v[118:119],v16 offset0:130 offset1:131
R 148,2,0
L 15
v_mfma_f32_16x16x32_bf16 v[40:43],v[88:91],v[128:131],0
ds_read2_b32 v[152:153],v1 offset0:16 offset1:17
M 40,v[92:95],124
ds_read2_b32 v[154:155],v1 offset0:18 offset1:19
M 40,v[96:99],120
L 14
R 184,2,3648
v_mfma_f32_16x16x32_bf16 v[44:47],v[88:91],v[132:135],0
ds_read2_b32 v[156:157],v1 offset0:144 offset1:145
M 44,v[92:95],128
M 44,v[96:99],124
ds_read2_b32 v[158:159],v1 offset0:146 offset1:147
M 44,v[100:103],120
L 14
R 188,2,3136
v_mfma_f32_16x16x32_bf16 v[48:51],v[88:91],v[136:139],0
ds_read2_b32 v[160:161],v14 offset0:16 offset1:17
M 48,v[92:95],132
ds_read2_b32 v[162:163],v14 offset0:18 offset1:19
M 48,v[96:99],128
M 48,v[100:103],124
R 192,2,2624
M 48,v[104:107],120
L 15
ds_read2_b32 v[164:165],v14 offset0:144 offset1:145
v_mfma_f32_16x16x32_bf16 v[52:55],v[88:91],v[140:143],0
ds_read2_b32 v[166:167],v14 offset0:146 offset1:147
M 52,v[92:95],136
R 196,2,2112
M 52,v[96:99],132
M 52,v[100:103],128
ds_read2_b32 v[168:169],v15 offset0:16 offset1:17
M 52,v[104:107],124
ds_read2_b32 v[170:171],v15 offset0:18 offset1:19
M 52,v[108:111],120
L 15
R 200,2,1600
v_mfma_f32_16x16x32_bf16 v[56:59],v[88:91],v[144:147],0
ds_read2_b32 v[172:173],v15 offset0:144 offset1:145
M 56,v[92:95],140
M 56,v[96:99],136
ds_read2_b32 v[174:175],v15 offset0:146 offset1:147
M 56,v[100:103],132
R 204,2,1088
M 56,v[104:107],128
ds_read2_b32 v[176:177],v16 offset0:16 offset1:17
M 56,v[108:111],124
ds_read2_b32 v[178:179],v16 offset0:18 offset1:19
M 56,v[112:115],120
L 15
v_mfma_f32_16x16x32_bf16 v[60:63],v[88:91],v[148:151],0
R 208,2,576
M 60,v[92:95],144
ds_read2_b32 v[180:181],v16 offset0:144 offset1:145
M 60,v[96:99],140
ds_read2_b32 v[182:183],v16 offset0:146 offset1:147
M 60,v[100:103],136
R 212,2,64
M 60,v[104:107],132
M 60,v[108:111],128
M 60,v[112:115],124
M 60,v[116:119],120
L 15
M 40,v[152:155],192
ds_read2_b32 v[88:89],v1 offset0:32 offset1:33
M 40,v[156:159],188
ds_read2_b32 v[90:91],v1 offset0:34 offset1:35
M 40,v[160:163],184
L 14
R 120,2,3712
M 44,v[152:155],196
ds_read2_b32 v[92:93],v1 offset0:160 offset1:161
M 44,v[156:159],192
M 44,v[160:163],188
ds_read2_b32 v[94:95],v1 offset0:162 offset1:163
M 44,v[164:167],184
L 14
R 124,2,3200
M 48,v[152:155],200
ds_read2_b32 v[96:97],v14 offset0:32 offset1:33
M 48,v[156:159],196
ds_read2_b32 v[98:99],v14 offset0:34 offset1:35
M 48,v[160:163],192
M 48,v[164:167],188
R 128,2,2688
M 48,v[168:171],184
L 15
ds_read2_b32 v[100:101],v14 offset0:160 offset1:161
M 52,v[152:155],204
ds_read2_b32 v[102:103],v14 offset0:162 offset1:163
M 52,v[156:159],200
R 132,2,2176
M 52,v[160:163],196
M 52,v[164:167],192
ds_read2_b32 v[104:105],v15 offset0:32 offset1:33
M 52,v[168:171],188
ds_read2_b32 v[106:107],v15 offset0:34 offset1:35
M 52,v[172:175],184
L 15
R 136,2,1664
M 56,v[152:155],208
ds_read2_b32 v[108:109],v15 offset0:160 offset1:161
M 56,v[156:159],204
M 56,v[160:163],200
ds_read2_b32 v[110:111],v15 offset0:162 offset1:163
M 56,v[164:167],196
R 140,2,1152
M 56,v[168:171],192
ds_read2_b32 v[112:113],v16 offset0:32 offset1:33
M 56,v[172:175],188
ds_read2_b32 v[114:115],v16 offset0:34 offset1:35
M 56,v[176:179],184
L 15
M 60,v[152:155],212
R 144,2,640
M 60,v[156:159],208
ds_read2_b32 v[116:117],v16 offset0:160 offset1:161
M 60,v[160:163],204
ds_read2_b32 v[118:119],v16 offset0:162 offset1:163
R 218,18,13952
M 60,v[164:167],200
R 148,2,128
ds_read_b32 v222,v18 offset:13968
M 60,v[168:171],196
M 60,v[172:175],192
M 60,v[176:179],188
M 60,v[180:183],184
L 15
M 40,v[88:91],128
ds_read2_b32 v[152:153],v1 offset0:48 offset1:49
M 40,v[92:95],124
L 1
ds_read2_b32 v[154:155],v1 offset0:50 offset1:51
v_alignbyte_b32 v226,v219,v218,2
M 40,v[96:99],120
s_nop 0
R 184,2,3776
v_alignbyte_b32 v227,v220,v219,2
M 44,v[88:91],132
ds_read2_b32 v[156:157],v1 offset0:176 offset1:177
v_alignbyte_b32 v228,v221,v220,2
M 44,v[92:95],128
v_alignbyte_b32 v229,v222,v221,2
M 44,v[96:99],124
ds_read2_b32 v[158:159],v1 offset0:178 offset1:179
M 44,v[100:103],120
s_nop 0
R 188,2,3264
M 48,v[88:91],136
ds_read2_b32 v[160:161],v14 offset0:48 offset1:49
M 48,v[92:95],132
ds_read2_b32 v[162:163],v14 offset0:50 offset1:51
M 48,v[96:99],128
ds_write_b128 v18,v[226:229] offset:18112
M 48,v[100:103],124
R 192,2,2752
M 48,v[104:107],120
s_nop 0
ds_read2_b32 v[164:165],v14 offset0:176 offset1:177
M 52,v[88:91],140
s_mov_b32 m0,163584
s_mov_b32 s78,0
ds_read2_b32 v[166:167],v14 offset0:178 offset1:179
M 52,v[92:95],136
R 196,2,2240
M 52,v[96:99],132
buffer_load_dword v11,s[40:43],s78 offen offset:0  lds
M 52,v[100:103],128
ds_read2_b32 v[168:169],v15 offset0:48 offset1:49
M 52,v[104:107],124
ds_read2_b32 v[170:171],v15 offset0:50 offset1:51
M 52,v[108:111],120
s_nop 0
R 200,2,1728
M 56,v[88:91],144
ds_read2_b32 v[172:173],v15 offset0:176 offset1:177
M 56,v[92:95],140
M 56,v[96:99],136
ds_read2_b32 v[174:175],v15 offset0:178 offset1:179
M 56,v[100:103],132
R 204,2,1216
M 56,v[104:107],128
ds_read2_b32 v[176:177],v16 offset0:48 offset1:49
M 56,v[108:111],124
ds_read2_b32 v[178:179],v16 offset0:50 offset1:51
M 56,v[112:115],120
s_nop 0
M 60,v[88:91],148
R 208,2,704
M 60,v[92:95],144
ds_read2_b32 v[180:181],v16 offset0:176 offset1:177
M 60,v[96:99],140
ds_read2_b32 v[182:183],v16 offset0:178 offset1:179
M 60,v[100:103],136
R 212,2,192
M 60,v[104:107],132
M 60,v[108:111],128
M 60,v[112:115],124
M 60,v[116:119],120
L 15
M 40,v[152:155],192
ds_read2_b32 v[88:89],v1 offset0:64 offset1:65
M 40,v[156:159],188
ds_read2_b32 v[90:91],v1 offset0:66 offset1:67
R 218,18,14976
M 40,v[160:163],184
L 15
R 120,2,3840
ds_read_b32 v222,v18 offset:14992
M 44,v[152:155],196
ds_read2_b32 v[92:93],v1 offset0:192 offset1:193
M 44,v[156:159],192
M 44,v[160:163],188
ds_read2_b32 v[94:95],v1 offset0:194 offset1:195
M 44,v[164:167],184
L 15
R 124,2,3328
M 48,v[152:155],200
ds_read2_b32 v[96:97],v14 offset0:64 offset1:65
M 48,v[156:159],196
L 4
ds_read2_b32 v[98:99],v14 offset0:66 offset1:67
v_alignbyte_b32 v226,v219,v218,2
M 48,v[160:163],192
v_alignbyte_b32 v227,v220,v219,2
v_alignbyte_b32 v228,v221,v220,2
M 48,v[164:167],188
R 128,2,2816
v_alignbyte_b32 v229,v222,v221,2
M 48,v[168:171],184
s_nop 0
ds_read2_b32 v[100:101],v14 offset0:192 offset1:193
M 52,v[152:155],204
ds_read2_b32 v[102:103],v14 offset0:194 offset1:195
M 52,v[156:159],200
R 132,2,2304
M 52,v[160:163],196
ds_write_b128 v18,v[226:229] offset:19136
M 52,v[164:167],192
ds_read2_b32 v[104:105],v15 offset0:64 offset1:65
M 52,v[168:171],188
ds_read2_b32 v[106:107],v15 offset0:66 offset1:67
M 52,v[172:175],184
s_nop 0
R 136,2,1792
M 56,v[152:155],208
ds_read2_b32 v[108:109],v15 offset0:192 offset1:193
M 56,v[156:159],204
M 56,v[160:163],200
ds_read2_b32 v[110:111],v15 offset0:194 offset1:195
M 56,v[164:167],196
R 140,2,1280
M 56,v[168:171],192
ds_read2_b32 v[112:113],v16 offset0:64 offset1:65
M 56,v[172:175],188
ds_read2_b32 v[114:115],v16 offset0:66 offset1:67
M 56,v[176:179],184
s_nop 0
M 60,v[152:155],212
R 144,2,768
M 60,v[156:159],208
ds_read2_b32 v[116:117],v16 offset0:192 offset1:193
M 60,v[160:163],204
ds_read2_b32 v[118:119],v16 offset0:194 offset1:195
M 60,v[164:167],200
R 148,2,256
M 60,v[168:171],196
M 60,v[172:175],192
M 60,v[176:179],188
M 60,v[180:183],184
L 15
M 40,v[88:91],128
ds_read2_b32 v[152:153],v1 offset0:80 offset1:81
M 40,v[92:95],124
ds_read2_b32 v[154:155],v1 offset0:82 offset1:83
M 40,v[96:99],120
L 15
R 184,2,3904
M 44,v[88:91],132
ds_read2_b32 v[156:157],v1 offset0:208 offset1:209
M 44,v[92:95],128
M 44,v[96:99],124
ds_read2_b32 v[158:159],v1 offset0:210 offset1:211
M 44,v[100:103],120
L 14
R 188,2,3392
M 48,v[88:91],136
ds_read2_b32 v[160:161],v14 offset0:80 offset1:81
M 48,v[92:95],132
ds_read2_b32 v[162:163],v14 offset0:82 offset1:83
R 218,18,16000
M 48,v[96:99],128
ds_read_b32 v222,v18 offset:16016
M 48,v[100:103],124
R 192,2,2880
M 48,v[104:107],120
L 15
ds_read2_b32 v[164:165],v14 offset0:208 offset1:209
M 52,v[88:91],140
ds_read2_b32 v[166:167],v14 offset0:210 offset1:211
M 52,v[92:95],136
R 196,2,2368
M 52,v[96:99],132
M 52,v[100:103],128
L 4
ds_read2_b32 v[168:169],v15 offset0:80 offset1:81
v_alignbyte_b32 v226,v219,v218,2
M 52,v[104:107],124
ds_read2_b32 v[170:171],v15 offset0:82 offset1:83
v_alignbyte_b32 v227,v220,v219,2
M 52,v[108:111],120
s_nop 0
R 200,2,1856
v_alignbyte_b32 v228,v221,v220,2
M 56,v[88:91],144
ds_read2_b32 v[172:173],v15 offset0:208 offset1:209
v_alignbyte_b32 v229,v222,v221,2
M 56,v[92:95],140
ds_write_b128 v18,v[226:229] offset:20160
M 56,v[96:99],136
ds_read2_b32 v[174:175],v15 offset0:210 offset1:211
M 56,v[100:103],132
R 204,2,1344
M 56,v[104:107],128
ds_read2_b32 v[176:177],v16 offset0:80 offset1:81
M 56,v[108:111],124
ds_read2_b32 v[178:179],v16 offset0:82 offset1:83
M 56,v[112:115],120
s_nop 0
M 60,v[88:91],148
R 208,2,832
M 60,v[92:95],144
ds_read2_b32 v[180:181],v16 offset0:208 offset1:209
M 60,v[96:99],140
ds_read2_b32 v[182:183],v16 offset0:210 offset1:211
M 60,v[100:103],136
R 212,2,320
M 60,v[104:107],132
M 60,v[108:111],128
M 60,v[112:115],124
M 60,v[116:119],120
L 15
M 40,v[152:155],192
ds_read2_b32 v[88:89],v1 offset0:96 offset1:97
M 40,v[156:159],188
ds_read2_b32 v[90:91],v1 offset0:98 offset1:99
M 40,v[160:163],184
L 15
R 120,2,3968
M 44,v[152:155],196
s_mov_b32 m0,163584
s_mov_b32 s78,3741696
ds_read2_b32 v[92:93],v1 offset0:224 offset1:225
M 44,v[156:159],192
M 44,v[160:163],188
ds_read2_b32 v[94:95],v1 offset0:226 offset1:227
M 44,v[164:167],184
L 15
R 124,2,3456
M 48,v[152:155],200
ds_read2_b32 v[96:97],v14 offset0:96 offset1:97
M 48,v[156:159],196
ds_read2_b32 v[98:99],v14 offset0:98 offset1:99
M 48,v[160:163],192
buffer_load_dword v11,s[40:43],s78 offen offset:0  lds
M 48,v[164:167],188
R 128,2,2944
M 48,v[168:171],184
L 15
ds_read2_b32 v[100:101],v14 offset0:224 offset1:225
M 52,v[152:155],204
ds_read2_b32 v[102:103],v14 offset0:226 offset1:227
M 52,v[156:159],200
R 132,2,2432
M 52,v[160:163],196
M 52,v[164:167],192
ds_read2_b32 v[104:105],v15 offset0:96 offset1:97
M 52,v[168:171],188
ds_read2_b32 v[106:107],v15 offset0:98 offset1:99
M 52,v[172:175],184
L 15
R 136,2,1920
M 56,v[152:155],208
ds_read2_b32 v[108:109],v15 offset0:224 offset1:225
M 56,v[156:159],204
M 56,v[160:163],200
ds_read2_b32 v[110:111],v15 offset0:226 offset1:227
M 56,v[164:167],196
R 140,2,1408
M 56,v[168:171],192
ds_read2_b32 v[112:113],v16 offset0:96 offset1:97
M 56,v[172:175],188
ds_read2_b32 v[114:115],v16 offset0:98 offset1:99
M 56,v[176:179],184
L 15
M 60,v[152:155],212
R 144,2,896
M 60,v[156:159],208
ds_read2_b32 v[116:117],v16 offset0:224 offset1:225
M 60,v[160:163],204
ds_read2_b32 v[118:119],v16 offset0:226 offset1:227
M 60,v[164:167],200
R 148,2,384
M 60,v[168:171],196
M 60,v[172:175],192
M 60,v[176:179],188
M 60,v[180:183],184
L 15
M 40,v[88:91],128
ds_read2_b32 v[152:153],v1 offset0:112 offset1:113
M 40,v[92:95],124
ds_read2_b32 v[154:155],v1 offset0:114 offset1:115
M 40,v[96:99],120
L 14
R 184,2,4032
M 44,v[88:91],132
ds_read2_b32 v[156:157],v1 offset0:240 offset1:241
M 44,v[92:95],128
M 44,v[96:99],124
ds_read2_b32 v[158:159],v1 offset0:242 offset1:243
v_add_u32 v1,12928,v1
M 44,v[100:103],120
L 14
R 188,2,3520
M 48,v[88:91],136
ds_read2_b32 v[160:161],v14 offset0:112 offset1:113
M 48,v[92:95],132
ds_read2_b32 v[162:163],v14 offset0:114 offset1:115
M 48,v[96:99],128
M 48,v[100:103],124
R 192,2,3008
M 48,v[104:107],120
L 15
ds_read2_b32 v[164:165],v14 offset0:240 offset1:241
M 52,v[88:91],140
ds_read2_b32 v[166:167],v14 offset0:242 offset1:243
v_add_u32 v14,12928,v14
M 52,v[92:95],136
R 196,2,2496
M 52,v[96:99],132
M 52,v[100:103],128
ds_read2_b32 v[168:169],v15 offset0:112 offset1:113
M 52,v[104:107],124
ds_read2_b32 v[170:171],v15 offset0:114 offset1:115
M 52,v[108:111],120
L 15
R 200,2,1984
M 56,v[88:91],144
ds_read2_b32 v[172:173],v15 offset0:240 offset1:241
M 56,v[92:95],140
M 56,v[96:99],136
ds_read2_b32 v[174:175],v15 offset0:242 offset1:243
v_add_u32 v15,12928,v15
M 56,v[100:103],132
R 204,2,1472
M 56,v[104:107],128
ds_read2_b32 v[176:177],v16 offset0:112 offset1:113
M 56,v[108:111],124
ds_read2_b32 v[178:179],v16 offset0:114 offset1:115
M 56,v[112:115],120
L 15
M 60,v[88:91],148
R 208,2,960
M 60,v[92:95],144
)"
R"(ds_read2_b32 v[180:181],v16 offset0:240 offset1:241
M 60,v[96:99],140
ds_read2_b32 v[182:183],v16 offset0:242 offset1:243
v_add_u32 v16,12928,v16
M 60,v[100:103],136
R 212,2,448
M 60,v[104:107],132
M 60,v[108:111],128
M 60,v[112:115],124
M 60,v[116:119],120
L 15
M 40,v[152:155],192
ds_read2_b32 v[88:89],v1 offset0:0 offset1:1
M 40,v[156:159],188
ds_read2_b32 v[90:91],v1 offset0:2 offset1:3
M 40,v[160:163],184
L 14
R 120,2,16512
M 44,v[152:155],196
ds_read2_b32 v[92:93],v1 offset0:128 offset1:129
M 44,v[156:159],192
M 44,v[160:163],188
ds_read2_b32 v[94:95],v1 offset0:130 offset1:131
M 44,v[164:167],184
L 14
R 124,2,16000
M 48,v[152:155],200
ds_read2_b32 v[96:97],v14 offset0:0 offset1:1
M 48,v[156:159],196
ds_read2_b32 v[98:99],v14 offset0:2 offset1:3
M 48,v[160:163],192
M 48,v[164:167],188
R 128,2,15488
M 48,v[168:171],184
L 15
ds_read2_b32 v[100:101],v14 offset0:128 offset1:129
M 52,v[152:155],204
ds_read2_b32 v[102:103],v14 offset0:130 offset1:131
M 52,v[156:159],200
R 132,2,14976
M 52,v[160:163],196
M 52,v[164:167],192
ds_read2_b32 v[104:105],v15 offset0:0 offset1:1
M 52,v[168:171],188
ds_read2_b32 v[106:107],v15 offset0:2 offset1:3
M 52,v[172:175],184
L 15
R 136,2,14464
M 56,v[152:155],208
ds_read2_b32 v[108:109],v15 offset0:128 offset1:129
M 56,v[156:159],204
M 56,v[160:163],200
ds_read2_b32 v[110:111],v15 offset0:130 offset1:131
M 56,v[164:167],196
R 140,2,13952
M 56,v[168:171],192
ds_read2_b32 v[112:113],v16 offset0:0 offset1:1
M 56,v[172:175],188
ds_read2_b32 v[114:115],v16 offset0:2 offset1:3
M 56,v[176:179],184
L 15
M 60,v[152:155],212
R 144,2,13440
M 60,v[156:159],208
ds_read2_b32 v[116:117],v16 offset0:128 offset1:129
M 60,v[160:163],204
ds_read2_b32 v[118:119],v16 offset0:130 offset1:131
M 60,v[164:167],200
R 148,2,12928
M 60,v[168:171],196
M 60,v[172:175],192
M 60,v[176:179],188
M 60,v[180:183],184
s_cmp_eq_u32 s51,2
s_cbranch_scc1 Lcv_d_%=
ds_read_b64 v[24:25],v3 offset:2560
ds_read_b64 v[26:27],v3 offset:2048
ds_read_b64 v[28:29],v3 offset:1536
ds_read_b64 v[30:31],v3 offset:1024
ds_read_b64 v[32:33],v3 offset:512
ds_read_b64 v[34:35],v3 offset:0
L 0
v_and_b32 v36,0xffff0000,v25
v_lshlrev_b32 v37,16,v25
v_and_b32 v38,0xffff0000,v24
v_lshlrev_b32 v39,16,v24
v_fma_f32 v40,v36,s86,v40
v_fma_f32 v41,v37,s86,v41
v_fma_f32 v42,v38,s86,v42
v_fma_f32 v43,v39,s86,v43
v_cvt_pk_bf16_f32 v40,v40,v41
v_cvt_pk_bf16_f32 v41,v42,v43
ds_write_b64 v4,v[40:41] offset:0
v_and_b32 v36,0xffff0000,v27
v_lshlrev_b32 v37,16,v27
v_and_b32 v38,0xffff0000,v26
v_lshlrev_b32 v39,16,v26
v_fma_f32 v44,v36,s86,v44
v_fma_f32 v45,v37,s86,v45
v_fma_f32 v46,v38,s86,v46
v_fma_f32 v47,v39,s86,v47
v_cvt_pk_bf16_f32 v44,v44,v45
v_cvt_pk_bf16_f32 v45,v46,v47
ds_write_b64 v4,v[44:45] offset:512
v_and_b32 v36,0xffff0000,v29
v_lshlrev_b32 v37,16,v29
v_and_b32 v38,0xffff0000,v28
v_lshlrev_b32 v39,16,v28
v_fma_f32 v48,v36,s86,v48
v_fma_f32 v49,v37,s86,v49
v_fma_f32 v50,v38,s86,v50
v_fma_f32 v51,v39,s86,v51
v_cvt_pk_bf16_f32 v48,v48,v49
v_cvt_pk_bf16_f32 v49,v50,v51
ds_write_b64 v4,v[48:49] offset:1024
v_and_b32 v36,0xffff0000,v31
v_lshlrev_b32 v37,16,v31
v_and_b32 v38,0xffff0000,v30
v_lshlrev_b32 v39,16,v30
v_fma_f32 v52,v36,s86,v52
v_fma_f32 v53,v37,s86,v53
v_fma_f32 v54,v38,s86,v54
v_fma_f32 v55,v39,s86,v55
v_cvt_pk_bf16_f32 v52,v52,v53
v_cvt_pk_bf16_f32 v53,v54,v55
ds_write_b64 v4,v[52:53] offset:1536
v_and_b32 v36,0xffff0000,v33
v_lshlrev_b32 v37,16,v33
v_and_b32 v38,0xffff0000,v32
v_lshlrev_b32 v39,16,v32
v_fma_f32 v56,v36,s86,v56
v_fma_f32 v57,v37,s86,v57
v_fma_f32 v58,v38,s86,v58
v_fma_f32 v59,v39,s86,v59
v_cvt_pk_bf16_f32 v56,v56,v57
v_cvt_pk_bf16_f32 v57,v58,v59
ds_write_b64 v4,v[56:57] offset:2048
v_and_b32 v36,0xffff0000,v35
v_lshlrev_b32 v37,16,v35
v_and_b32 v38,0xffff0000,v34
v_lshlrev_b32 v39,16,v34
v_fma_f32 v60,v36,s86,v60
v_fma_f32 v61,v37,s86,v61
v_fma_f32 v62,v38,s86,v62
v_fma_f32 v63,v39,s86,v63
v_cvt_pk_bf16_f32 v60,v60,v61
v_cvt_pk_bf16_f32 v61,v62,v63
ds_write_b64 v4,v[60:61] offset:2560
s_branch Lcv_e_%=
Lcv_d_%=:
L 15
v_mfma_f32_16x16x32_bf16 v[64:67],v[88:91],v[128:131],0
ds_read2_b32 v[152:153],v1 offset0:16 offset1:17
ds_read_b64 v[24:25],v3 offset:2560
M 64,v[92:95],124
ds_read2_b32 v[154:155],v1 offset0:18 offset1:19
ds_read_b64 v[26:27],v3 offset:2048
M 64,v[96:99],120
L 15
R 184,2,16576
ds_read_b64 v[28:29],v3 offset:1536
v_mfma_f32_16x16x32_bf16 v[68:71],v[88:91],v[132:135],0
ds_read2_b32 v[156:157],v1 offset0:144 offset1:145
ds_read_b64 v[30:31],v3 offset:1024
M 68,v[92:95],128
ds_read_b64 v[32:33],v3 offset:512
ds_read_b64 v[34:35],v3 offset:0
M 68,v[96:99],124
ds_read2_b32 v[158:159],v1 offset0:146 offset1:147
M 68,v[100:103],120
L 15
R 188,2,16064
v_mfma_f32_16x16x32_bf16 v[72:75],v[88:91],v[136:139],0
ds_read2_b32 v[160:161],v14 offset0:16 offset1:17
M 72,v[92:95],132
s_mov_b32 m0,163584
s_mov_b32 s78,0
ds_read2_b32 v[162:163],v14 offset0:18 offset1:19
M 72,v[96:99],128
M 72,v[100:103],124
L 4
R 192,2,15552
v_and_b32 v36,0xffff0000,v25
M 72,v[104:107],120
s_nop 0
ds_read2_b32 v[164:165],v14 offset0:144 offset1:145
v_lshlrev_b32 v37,16,v25
v_mfma_f32_16x16x32_bf16 v[76:79],v[88:91],v[140:143],0
ds_read2_b32 v[166:167],v14 offset0:146 offset1:147
v_and_b32 v38,0xffff0000,v24
M 76,v[92:95],136
R 196,2,15040
v_lshlrev_b32 v39,16,v24
M 76,v[96:99],132
buffer_load_dword v12,s[60:63],s78 offen offset:0  lds
M 76,v[100:103],128
ds_read2_b32 v[168:169],v15 offset0:16 offset1:17
v_fma_f32 v40,v36,s86,v40
M 76,v[104:107],124
ds_read2_b32 v[170:171],v15 offset0:18 offset1:19
v_fma_f32 v41,v37,s86,v41
M 76,v[108:111],120
s_nop 0
R 200,2,14528
v_fma_f32 v42,v38,s86,v42
v_mfma_f32_16x16x32_bf16 v[80:83],v[88:91],v[144:147],0
ds_read2_b32 v[172:173],v15 offset0:144 offset1:145
v_fma_f32 v43,v39,s86,v43
M 80,v[92:95],140
v_cvt_pk_bf16_f32 v40,v40,v41
v_cvt_pk_bf16_f32 v41,v42,v43
M 80,v[96:99],136
ds_read2_b32 v[174:175],v15 offset0:146 offset1:147
v_and_b32 v36,0xffff0000,v27
M 80,v[100:103],132
R 204,2,14016
v_lshlrev_b32 v37,16,v27
M 80,v[104:107],128
ds_read2_b32 v[176:177],v16 offset0:16 offset1:17
v_and_b32 v38,0xffff0000,v26
M 80,v[108:111],124
ds_read2_b32 v[178:179],v16 offset0:18 offset1:19
v_lshlrev_b32 v39,16,v26
M 80,v[112:115],120
s_nop 0
ds_write_b64 v4,v[40:41] offset:0
v_mfma_f32_16x16x32_bf16 v[84:87],v[88:91],v[148:151],0
R 208,2,13504
v_fma_f32 v44,v36,s86,v44
M 84,v[92:95],144
ds_read2_b32 v[180:181],v16 offset0:144 offset1:145
v_fma_f32 v45,v37,s86,v45
M 84,v[96:99],140
ds_read2_b32 v[182:183],v16 offset0:146 offset1:147
v_fma_f32 v46,v38,s86,v46
M 84,v[100:103],136
R 212,2,12992
v_fma_f32 v47,v39,s86,v47
M 84,v[104:107],132
v_cvt_pk_bf16_f32 v44,v44,v45
v_cvt_pk_bf16_f32 v45,v46,v47
M 84,v[108:111],128
ds_write_b64 v4,v[44:45] offset:512
M 84,v[112:115],124
v_and_b32 v36,0xffff0000,v29
v_lshlrev_b32 v37,16,v29
M 84,v[116:119],120
L 15
v_and_b32 v38,0xffff0000,v28
v_lshlrev_b32 v39,16,v28
M 64,v[152:155],192
ds_read2_b32 v[88:89],v1 offset0:32 offset1:33
v_fma_f32 v48,v36,s86,v48
M 64,v[156:159],188
ds_read2_b32 v[90:91],v1 offset0:34 offset1:35
v_fma_f32 v49,v37,s86,v49
M 64,v[160:163],184
L 15
R 120,2,16640
v_fma_f32 v50,v38,s86,v50
M 68,v[152:155],196
ds_read2_b32 v[92:93],v1 offset0:160 offset1:161
v_fma_f32 v51,v39,s86,v51
M 68,v[156:159],192
v_cvt_pk_bf16_f32 v48,v48,v49
v_cvt_pk_bf16_f32 v49,v50,v51
M 68,v[160:163],188
ds_read2_b32 v[94:95],v1 offset0:162 offset1:163
v_and_b32 v36,0xffff0000,v31
M 68,v[164:167],184
L 15
R 124,2,16128
v_lshlrev_b32 v37,16,v31
M 72,v[152:155],200
ds_read2_b32 v[96:97],v14 offset0:32 offset1:33
v_and_b32 v38,0xffff0000,v30
M 72,v[156:159],196
ds_read2_b32 v[98:99],v14 offset0:34 offset1:35
v_lshlrev_b32 v39,16,v30
M 72,v[160:163],192
ds_write_b64 v4,v[48:49] offset:1024
M 72,v[164:167],188
R 128,2,15616
v_fma_f32 v52,v36,s86,v52
M 72,v[168:171],184
L 15
ds_read2_b32 v[100:101],v14 offset0:160 offset1:161
v_fma_f32 v53,v37,s86,v53
M 76,v[152:155],204
ds_read2_b32 v[102:103],v14 offset0:162 offset1:163
v_fma_f32 v54,v38,s86,v54
M 76,v[156:159],200
R 132,2,15104
v_fma_f32 v55,v39,s86,v55
M 76,v[160:163],196
v_cvt_pk_bf16_f32 v52,v52,v53
v_cvt_pk_bf16_f32 v53,v54,v55
M 76,v[164:167],192
ds_read2_b32 v[104:105],v15 offset0:32 offset1:33
v_and_b32 v36,0xffff0000,v33
M 76,v[168:171],188
ds_read2_b32 v[106:107],v15 offset0:34 offset1:35
v_lshlrev_b32 v37,16,v33
M 76,v[172:175],184
L 15
R 136,2,14592
v_and_b32 v38,0xffff0000,v32
M 80,v[152:155],208
ds_read2_b32 v[108:109],v15 offset0:160 offset1:161
v_lshlrev_b32 v39,16,v32
M 80,v[156:159],204
ds_write_b64 v4,v[52:53] offset:1536
M 80,v[160:163],200
ds_read2_b32 v[110:111],v15 offset0:162 offset1:163
v_fma_f32 v56,v36,s86,v56
M 80,v[164:167],196
R 140,2,14080
v_fma_f32 v57,v37,s86,v57
M 80,v[168:171],192
ds_read2_b32 v[112:113],v16 offset0:32 offset1:33
v_fma_f32 v58,v38,s86,v58
M 80,v[172:175],188
ds_read2_b32 v[114:115],v16 offset0:34 offset1:35
v_fma_f32 v59,v39,s86,v59
M 80,v[176:179],184
L 15
v_cvt_pk_bf16_f32 v56,v56,v57
v_cvt_pk_bf16_f32 v57,v58,v59
M 84,v[152:155],212
R 144,2,13568
v_and_b32 v36,0xffff0000,v35
M 84,v[156:159],208
ds_read2_b32 v[116:117],v16 offset0:160 offset1:161
v_lshlrev_b32 v37,16,v35
M 84,v[160:163],204
ds_read2_b32 v[118:119],v16 offset0:162 offset1:163
v_and_b32 v38,0xffff0000,v34
M 84,v[164:167],200
R 148,2,13056
v_lshlrev_b32 v39,16,v34
M 84,v[168:171],196
ds_write_b64 v4,v[56:57] offset:2048
M 84,v[172:175],192
v_fma_f32 v60,v36,s86,v60
v_fma_f32 v61,v37,s86,v61
M 84,v[176:179],188
v_fma_f32 v62,v38,s86,v62
v_fma_f32 v63,v39,s86,v63
M 84,v[180:183],184
L 15
v_cvt_pk_bf16_f32 v60,v60,v61
v_cvt_pk_bf16_f32 v61,v62,v63
M 64,v[88:91],128
ds_read2_b32 v[152:153],v1 offset0:48 offset1:49
M 64,v[92:95],124
ds_read2_b32 v[154:155],v1 offset0:50 offset1:51
M 64,v[96:99],120
L 15
R 184,2,16704
M 68,v[88:91],132
ds_read2_b32 v[156:157],v1 offset0:176 offset1:177
M 68,v[92:95],128
ds_write_b64 v4,v[60:61] offset:2560
M 68,v[96:99],124
ds_read2_b32 v[158:159],v1 offset0:178 offset1:179
M 68,v[100:103],120
L 15
R 188,2,16192
M 72,v[88:91],136
ds_read2_b32 v[160:161],v14 offset0:48 offset1:49
M 72,v[92:95],132
ds_read2_b32 v[162:163],v14 offset0:50 offset1:51
M 72,v[96:99],128
M 72,v[100:103],124
R 192,2,15680
M 72,v[104:107],120
L 15
ds_read2_b32 v[164:165],v14 offset0:176 offset1:177
M 76,v[88:91],140
ds_read2_b32 v[166:167],v14 offset0:178 offset1:179
M 76,v[92:95],136
R 196,2,15168
M 76,v[96:99],132
M 76,v[100:103],128
ds_read2_b32 v[168:169],v15 offset0:48 offset1:49
M 76,v[104:107],124
ds_read2_b32 v[170:171],v15 offset0:50 offset1:51
M 76,v[108:111],120
L 15
R 200,2,14656
M 80,v[88:91],144
ds_read2_b32 v[172:173],v15 offset0:176 offset1:177
M 80,v[92:95],140
M 80,v[96:99],136
ds_read2_b32 v[174:175],v15 offset0:178 offset1:179
M 80,v[100:103],132
R 204,2,14144
M 80,v[104:107],128
s_mov_b32 m0,163584
s_mov_b32 s78,1847296
ds_read2_b32 v[176:177],v16 offset0:48 offset1:49
M 80,v[108:111],124
ds_read2_b32 v[178:179],v16 offset0:50 offset1:51
M 80,v[112:115],120
L 15
buffer_load_dword v12,s[60:63],s78 offen offset:0  lds
M 84,v[88:91],148
R 208,2,13632
M 84,v[92:95],144
ds_read2_b32 v[180:181],v16 offset0:176 offset1:177
M 84,v[96:99],140
ds_read2_b32 v[182:183],v16 offset0:178 offset1:179
M 84,v[100:103],136
R 212,2,13120
M 84,v[104:107],132
M 84,v[108:111],128
M 84,v[112:115],124
M 84,v[116:119],120
L 15
M 64,v[152:155],192
ds_read2_b32 v[88:89],v1 offset0:64 offset1:65
M 64,v[156:159],188
ds_read2_b32 v[90:91],v1 offset0:66 offset1:67
M 64,v[160:163],184
L 14
R 120,2,16768
M 68,v[152:155],196
ds_read2_b32 v[92:93],v1 offset0:192 offset1:193
M 68,v[156:159],192
M 68,v[160:163],188
ds_read2_b32 v[94:95],v1 offset0:194 offset1:195
M 68,v[164:167],184
L 14
R 124,2,16256
M 72,v[152:155],200
ds_read2_b32 v[96:97],v14 offset0:64 offset1:65
M 72,v[156:159],196
ds_read2_b32 v[98:99],v14 offset0:66 offset1:67
M 72,v[160:163],192
M 72,v[164:167],188
R 128,2,15744
M 72,v[168:171],184
L 15
ds_read2_b32 v[100:101],v14 offset0:192 offset1:193
M 76,v[152:155],204
ds_read2_b32 v[102:103],v14 offset0:194 offset1:195
M 76,v[156:159],200
R 132,2,15232
M 76,v[160:163],196
M 76,v[164:167],192
ds_read2_b32 v[104:105],v15 offset0:64 offset1:65
M 76,v[168:171],188
ds_read2_b32 v[106:107],v15 offset0:66 offset1:67
M 76,v[172:175],184
L 15
R 136,2,14720
M 80,v[152:155],208
ds_read2_b32 v[108:109],v15 offset0:192 offset1:193
M 80,v[156:159],204
M 80,v[160:163],200
ds_read2_b32 v[110:111],v15 offset0:194 offset1:195
M 80,v[164:167],196
R 140,2,14208
M 80,v[168:171],192
ds_read2_b32 v[112:113],v16 offset0:64 offset1:65
M 80,v[172:175],188
ds_read2_b32 v[114:115],v16 offset0:66 offset1:67
M 80,v[176:179],184
L 15
M 84,v[152:155],212
R 144,2,13696
M 84,v[156:159],208
ds_read2_b32 v[116:117],v16 offset0:192 offset1:193
M 84,v[160:163],204
ds_read2_b32 v[118:119],v16 offset0:194 offset1:195
M 84,v[164:167],200
R 148,2,13184
M 84,v[168:171],196
M 84,v[172:175],192
M 84,v[176:179],188
M 84,v[180:183],184
L 15
M 64,v[88:91],128
ds_read2_b32 v[152:153],v1 offset0:80 offset1:81
M 64,v[92:95],124
ds_read2_b32 v[154:155],v1 offset0:82 offset1:83
M 64,v[96:99],120
L 14
R 184,2,16832
M 68,v[88:91],132
ds_read2_b32 v[156:157],v1 offset0:208 offset1:209
M 68,v[92:95],128
M 68,v[96:99],124
ds_read2_b32 v[158:159],v1 offset0:210 offset1:211
M 68,v[100:103],120
L 14
R 188,2,16320
M 72,v[88:91],136
ds_read2_b32 v[160:161],v14 offset0:80 offset1:81
M 72,v[92:95],132
)"
R"(ds_read2_b32 v[162:163],v14 offset0:82 offset1:83
M 72,v[96:99],128
M 72,v[100:103],124
R 192,2,15808
M 72,v[104:107],120
L 15
ds_read2_b32 v[164:165],v14 offset0:208 offset1:209
M 76,v[88:91],140
ds_read2_b32 v[166:167],v14 offset0:210 offset1:211
M 76,v[92:95],136
R 196,2,15296
M 76,v[96:99],132
M 76,v[100:103],128
ds_read2_b32 v[168:169],v15 offset0:80 offset1:81
M 76,v[104:107],124
ds_read2_b32 v[170:171],v15 offset0:82 offset1:83
M 76,v[108:111],120
L 15
R 200,2,14784
M 80,v[88:91],144
ds_read2_b32 v[172:173],v15 offset0:208 offset1:209
M 80,v[92:95],140
M 80,v[96:99],136
ds_read2_b32 v[174:175],v15 offset0:210 offset1:211
M 80,v[100:103],132
R 204,2,14272
M 80,v[104:107],128
ds_read2_b32 v[176:177],v16 offset0:80 offset1:81
M 80,v[108:111],124
ds_read2_b32 v[178:179],v16 offset0:82 offset1:83
M 80,v[112:115],120
L 15
M 84,v[88:91],148
R 208,2,13760
M 84,v[92:95],144
ds_read2_b32 v[180:181],v16 offset0:208 offset1:209
M 84,v[96:99],140
ds_read2_b32 v[182:183],v16 offset0:210 offset1:211
M 84,v[100:103],136
R 212,2,13248
M 84,v[104:107],132
M 84,v[108:111],128
M 84,v[112:115],124
M 84,v[116:119],120
L 15
M 64,v[152:155],192
ds_read2_b32 v[88:89],v1 offset0:96 offset1:97
M 64,v[156:159],188
ds_read2_b32 v[90:91],v1 offset0:98 offset1:99
M 64,v[160:163],184
L 14
R 120,2,16896
M 68,v[152:155],196
ds_read2_b32 v[92:93],v1 offset0:224 offset1:225
M 68,v[156:159],192
M 68,v[160:163],188
ds_read2_b32 v[94:95],v1 offset0:226 offset1:227
M 68,v[164:167],184
L 14
R 124,2,16384
M 72,v[152:155],200
ds_read2_b32 v[96:97],v14 offset0:96 offset1:97
M 72,v[156:159],196
ds_read2_b32 v[98:99],v14 offset0:98 offset1:99
M 72,v[160:163],192
M 72,v[164:167],188
R 128,2,15872
M 72,v[168:171],184
L 15
ds_read2_b32 v[100:101],v14 offset0:224 offset1:225
M 76,v[152:155],204
ds_read2_b32 v[102:103],v14 offset0:226 offset1:227
M 76,v[156:159],200
R 132,2,15360
M 76,v[160:163],196
M 76,v[164:167],192
ds_read2_b32 v[104:105],v15 offset0:96 offset1:97
M 76,v[168:171],188
ds_read2_b32 v[106:107],v15 offset0:98 offset1:99
M 76,v[172:175],184
L 15
R 136,2,14848
M 80,v[152:155],208
ds_read2_b32 v[108:109],v15 offset0:224 offset1:225
M 80,v[156:159],204
M 80,v[160:163],200
ds_read2_b32 v[110:111],v15 offset0:226 offset1:227
M 80,v[164:167],196
R 140,2,14336
M 80,v[168:171],192
ds_read2_b32 v[112:113],v16 offset0:96 offset1:97
M 80,v[172:175],188
ds_read2_b32 v[114:115],v16 offset0:98 offset1:99
M 80,v[176:179],184
L 15
M 84,v[152:155],212
R 144,2,13824
M 84,v[156:159],208
ds_read2_b32 v[116:117],v16 offset0:224 offset1:225
M 84,v[160:163],204
ds_read2_b32 v[118:119],v16 offset0:226 offset1:227
M 84,v[164:167],200
R 148,2,13312
M 84,v[168:171],196
M 84,v[172:175],192
M 84,v[176:179],188
M 84,v[180:183],184
L 15
M 64,v[88:91],128
ds_read2_b32 v[152:153],v1 offset0:112 offset1:113
M 64,v[92:95],124
ds_read2_b32 v[154:155],v1 offset0:114 offset1:115
M 64,v[96:99],120
L 14
R 184,2,16960
M 68,v[88:91],132
ds_read2_b32 v[156:157],v1 offset0:240 offset1:241
M 68,v[92:95],128
M 68,v[96:99],124
ds_read2_b32 v[158:159],v1 offset0:242 offset1:243
M 68,v[100:103],120
L 14
R 188,2,16448
M 72,v[88:91],136
ds_read2_b32 v[160:161],v14 offset0:112 offset1:113
M 72,v[92:95],132
ds_read2_b32 v[162:163],v14 offset0:114 offset1:115
M 72,v[96:99],128
M 72,v[100:103],124
R 192,2,15936
M 72,v[104:107],120
L 15
ds_read2_b32 v[164:165],v14 offset0:240 offset1:241
M 76,v[88:91],140
ds_read2_b32 v[166:167],v14 offset0:242 offset1:243
M 76,v[92:95],136
R 196,2,15424
M 76,v[96:99],132
M 76,v[100:103],128
ds_read2_b32 v[168:169],v15 offset0:112 offset1:113
M 76,v[104:107],124
ds_read2_b32 v[170:171],v15 offset0:114 offset1:115
M 76,v[108:111],120
L 15
R 200,2,14912
M 80,v[88:91],144
ds_read2_b32 v[172:173],v15 offset0:240 offset1:241
M 80,v[92:95],140
M 80,v[96:99],136
ds_read2_b32 v[174:175],v15 offset0:242 offset1:243
M 80,v[100:103],132
R 204,2,14400
M 80,v[104:107],128
ds_read2_b32 v[176:177],v16 offset0:112 offset1:113
M 80,v[108:111],124
ds_read2_b32 v[178:179],v16 offset0:114 offset1:115
M 80,v[112:115],120
L 15
M 84,v[88:91],148
R 208,2,13888
M 84,v[92:95],144
ds_read2_b32 v[180:181],v16 offset0:240 offset1:241
M 84,v[96:99],140
ds_read2_b32 v[182:183],v16 offset0:242 offset1:243
M 84,v[100:103],136
R 212,2,13376
M 84,v[104:107],132
M 84,v[108:111],128
M 84,v[112:115],124
M 84,v[116:119],120
L 15
M 64,v[152:155],192
M 64,v[156:159],188
M 64,v[160:163],184
L 12
M 68,v[152:155],196
M 68,v[156:159],192
M 68,v[160:163],188
M 68,v[164:167],184
L 9
M 72,v[152:155],200
M 72,v[156:159],196
M 72,v[160:163],192
ds_read_b64 v[24:25],v3 offset:15488
ds_read_b64 v[26:27],v3 offset:14976
M 72,v[164:167],188
ds_read_b64 v[28:29],v3 offset:14464
ds_read_b64 v[30:31],v3 offset:13952
M 72,v[168:171],184
L 10
ds_read_b64 v[32:33],v3 offset:13440
ds_read_b64 v[34:35],v3 offset:12928
M 76,v[152:155],204
M 76,v[156:159],200
M 76,v[160:163],196
M 76,v[164:167],192
M 76,v[168:171],188
M 76,v[172:175],184
L 9
M 80,v[152:155],208
M 80,v[156:159],204
M 80,v[160:163],200
M 80,v[164:167],196
M 80,v[168:171],192
M 80,v[172:175],188
M 80,v[176:179],184
L 6
M 84,v[152:155],212
M 84,v[156:159],208
M 84,v[160:163],204
M 84,v[164:167],200
M 84,v[168:171],196
M 84,v[172:175],192
M 84,v[176:179],188
M 84,v[180:183],184
L 0
v_and_b32 v36,0xffff0000,v25
v_lshlrev_b32 v37,16,v25
v_and_b32 v38,0xffff0000,v24
v_lshlrev_b32 v39,16,v24
v_fma_f32 v64,v36,s87,v64
v_fma_f32 v65,v37,s87,v65
v_fma_f32 v66,v38,s87,v66
v_fma_f32 v67,v39,s87,v67
v_cvt_pk_bf16_f32 v64,v64,v65
v_cvt_pk_bf16_f32 v65,v66,v67
ds_write_b64 v4,v[64:65] offset:12928
v_and_b32 v36,0xffff0000,v27
v_lshlrev_b32 v37,16,v27
v_and_b32 v38,0xffff0000,v26
v_lshlrev_b32 v39,16,v26
v_fma_f32 v68,v36,s87,v68
v_fma_f32 v69,v37,s87,v69
v_fma_f32 v70,v38,s87,v70
v_fma_f32 v71,v39,s87,v71
v_cvt_pk_bf16_f32 v68,v68,v69
v_cvt_pk_bf16_f32 v69,v70,v71
ds_write_b64 v4,v[68:69] offset:13440
v_and_b32 v36,0xffff0000,v29
v_lshlrev_b32 v37,16,v29
v_and_b32 v38,0xffff0000,v28
v_lshlrev_b32 v39,16,v28
v_fma_f32 v72,v36,s87,v72
v_fma_f32 v73,v37,s87,v73
v_fma_f32 v74,v38,s87,v74
v_fma_f32 v75,v39,s87,v75
v_cvt_pk_bf16_f32 v72,v72,v73
v_cvt_pk_bf16_f32 v73,v74,v75
ds_write_b64 v4,v[72:73] offset:13952
v_and_b32 v36,0xffff0000,v31
v_lshlrev_b32 v37,16,v31
v_and_b32 v38,0xffff0000,v30
v_lshlrev_b32 v39,16,v30
v_fma_f32 v76,v36,s87,v76
v_fma_f32 v77,v37,s87,v77
v_fma_f32 v78,v38,s87,v78
v_fma_f32 v79,v39,s87,v79
v_cvt_pk_bf16_f32 v76,v76,v77
v_cvt_pk_bf16_f32 v77,v78,v79
ds_write_b64 v4,v[76:77] offset:14464
v_and_b32 v36,0xffff0000,v33
v_lshlrev_b32 v37,16,v33
v_and_b32 v38,0xffff0000,v32
v_lshlrev_b32 v39,16,v32
v_fma_f32 v80,v36,s87,v80
v_fma_f32 v81,v37,s87,v81
v_fma_f32 v82,v38,s87,v82
v_fma_f32 v83,v39,s87,v83
v_cvt_pk_bf16_f32 v80,v80,v81
v_cvt_pk_bf16_f32 v81,v82,v83
ds_write_b64 v4,v[80:81] offset:14976
v_and_b32 v36,0xffff0000,v35
v_lshlrev_b32 v37,16,v35
v_and_b32 v38,0xffff0000,v34
v_lshlrev_b32 v39,16,v34
v_fma_f32 v84,v36,s87,v84
v_fma_f32 v85,v37,s87,v85
v_fma_f32 v86,v38,s87,v86
v_fma_f32 v87,v39,s87,v87
v_cvt_pk_bf16_f32 v84,v84,v85
v_cvt_pk_bf16_f32 v85,v86,v87
ds_write_b64 v4,v[84:85] offset:15488
Lcv_e_%=:
L 0
s_barrier
R 152,5,0
R 156,5,512
R 160,5,1024
R 164,5,1536
R 168,5,2048
R 172,5,2560
s_mov_b32 s78,99614720
L 5
buffer_store_dwordx4 v[152:155],v7,s[36:39],s78 offen offset:0 sc0 sc1
s_add_u32 s78,s56,100139008
L 4
buffer_store_dwordx4 v[156:159],v7,s[36:39],s78 offen offset:0 sc0 sc1
s_mov_b32 s78,100663296
L 3
buffer_store_dwordx4 v[160:163],v7,s[36:39],s78 offen offset:0 sc0 sc1
s_add_u32 s78,s56,101187584
L 2
buffer_store_dwordx4 v[164:167],v7,s[36:39],s78 offen offset:0 sc0 sc1
s_mov_b32 s78,101711872
L 1
buffer_store_dwordx4 v[168:171],v7,s[36:39],s78 offen offset:0 sc0 sc1
s_add_u32 s78,s56,102236160
L 0
buffer_store_dwordx4 v[172:175],v7,s[36:39],s78 offen offset:0 sc0 sc1
s_waitcnt vmcnt(0)
s_barrier
s_add_u32 s48,s48,32768
buffer_store_dword v23,v10,s[36:39],s48 offen offset:0 sc0 sc1
s_cmp_eq_u32 s51,2
s_cbranch_scc0 Lcv_f_%=
s_mov_b32 m0,163584
s_mov_b32 s78,3694592
buffer_load_dword v12,s[60:63],s78 offen offset:0  lds
s_mov_b32 m0,163584
s_mov_b32 s78,5541888
buffer_load_dword v12,s[60:63],s78 offen offset:0  lds
Lcv_f_%=:
Lcode_cv_e:
.purgem M
.purgem R
.purgem T
.purgem D
.purgem L
)"
  :: "s"(ws), "s"(__builtin_amdgcn_readfirstlane(sWave)), "s"(__builtin_amdgcn_readfirstlane(sBuf)), "s"(__builtin_amdgcn_readfirstlane(sAuxOff)), "s"(__builtin_amdgcn_readfirstlane(sFoutOff)), "s"(__builtin_amdgcn_readfirstlane(sC0)), "s"(__builtin_amdgcn_readfirstlane(sC1)), "s"(__builtin_amdgcn_readfirstlane(sExp)), "v"(vL16), "v"(vPA), "v"(vPB), "v"(vPU), "v"(vPW), "v"(vOR), "v"(vORA), "v"(vOG), "v"(vFA), "v"(vFB), "v"(vFlag), "v"(vTW), "v"(vTX), "s"(w3), "s"(w4) : "v0", "v1", "v2", "v3", "v4", "v5", "v6", "v7", "v8", "v9", "v10", "v11", "v12", "v13", "v14", "v15", "v16", "v17", "v18", "v19", "v20", "v21", "v22", "v23", "v24", "v25", "v26", "v27", "v28", "v29", "v30", "v31", "v32", "v33", "v34", "v35", "v36", "v37", "v38", "v39", "v40", "v41", "v42", "v43", "v44", "v45", "v46", "v47", "v48", "v49", "v50", "v51", "v52", "v53", "v54", "v55", "v56", "v57", "v58", "v59", "v60", "v61", "v62", "v63", "v64", "v65", "v66", "v67", "v68", "v69", "v70", "v71", "v72", "v73", "v74", "v75", "v76", "v77", "v78", "v79", "v80", "v81", "v82", "v83", "v84", "v85", "v86", "v87", "v88", "v89", "v90", "v91", "v92", "v93", "v94", "v95", "v96", "v97", "v98", "v99", "v100", "v101", "v102", "v103", "v104", "v105", "v106", "v107", "v108", "v109", "v110", "v111", "v112", "v113", "v114", "v115", "v116", "v117", "v118", "v119", "v120", "v121", "v122", "v123", "v124", "v125", "v126", "v127", "v128", "v129", "v130", "v131", "v132", "v133", "v134", "v135", "v136", "v137", "v138", "v139", "v140", "v141", "v142", "v143", "v144", "v145", "v146", "v147", "v148", "v149", "v150", "v151", "v152", "v153", "v154", "v155", "v156", "v157", "v158", "v159", "v160", "v161", "v162", "v163", "v164", "v165", "v166", "v167", "v168", "v169", "v170", "v171", "v172", "v173", "v174", "v175", "v176", "v177", "v178", "v179", "v180", "v181", "v182", "v183", "v184", "v185", "v186", "v187", "v188", "v189", "v190", "v191", "v192", "v193", "v194", "v195", "v196", "v197", "v198", "v199", "v200", "v201", "v202", "v203", "v204", "v205", "v206", "v207", "v208", "v209", "v210", "v211", "v212", "v213", "v214", "v215", "v216", "v217", "v218", "v219", "v220", "v221", "v222", "v223", "v224", "v225", "v226", "v227", "v228", "v229", "v230", "v231", "a0", "a1", "a2", "a3", "a4", "a5", "a6", "a7", "a8", "a9", "a10", "a11", "a12", "a13", "a14", "a15", "a16", "a17", "a18", "a19", "a20", "a21", "a22", "a23", "a24", "a25", "a26", "a27", "a28", "a29", "a30", "a31", "a32", "a33", "a34", "a35", "a36", "a37", "a38", "a39", "a40", "a41", "a42", "a43", "a44", "a45", "a46", "a47", "a48", "a49", "a50", "a51", "a52", "a53", "a54", "a55", "a56", "a57", "a58", "a59", "a60", "a61", "a62", "a63", "a64", "a65", "a66", "a67", "a68", "a69", "a70", "a71", "a72", "a73", "a74", "a75", "a76", "a77", "a78", "a79", "a80", "a81", "a82", "a83", "a84", "a85", "a86", "a87", "a88", "a89", "a90", "a91", "a92", "a93", "a94", "a95", "a96", "a97", "a98", "a99", "a100", "a101", "a102", "a103", "a104", "a105", "a106", "a107", "a108", "a109", "a110", "a111", "a112", "a113", "a114", "a115", "a116", "a117", "a118", "a119", "a120", "a121", "a122", "a123", "a124", "a125", "a126", "a127", "a128", "a129", "a130", "a131", "a132", "a133", "a134", "a135", "a136", "a137", "a138", "a139", "a140", "a141", "a142", "a143", "a144", "a145", "a146", "a147", "a148", "a149", "a150", "a151", "a152", "a153", "a154", "a155", "a156", "a157", "a158", "a159", "a160", "a161", "a162", "a163", "a164", "a165", "a166", "a167", "a168", "a169", "a170", "a171", "a172", "a173", "a174", "a175", "a176", "a177", "a178", "a179", "a180", "a181", "a182", "a183", "a184", "a185", "a186", "a187", "a188", "a189", "a190", "a191", "a192", "a193", "a194", "a195", "a196", "a197", "a198", "a199", "a200", "a201", "a202", "a203", "a204", "a205", "a206", "a207", "a208", "a209", "a210", "a211", "a212", "a213", "a214", "a215", "a216", "a217", "a218", "a219", "a220", "a221", "a222", "a223", "a224", "a225", "a226", "a227", "a228", "a229", "a230", "a231", "a232", "a233", "a234", "a235", "a236", "a237", "a238", "a239", "a240", "a241", "a242", "a243", "a244", "a245", "a246", "a247", "a248", "a249", "a250", "a251", "a252", "a253", "a254", "a255", "s36", "s37", "s38", "s39", "s40", "s41", "s42", "s43", "s44", "s45", "s46", "s47", "s48", "s49", "s50", "s51", "s52", "s53", "s54", "s55", "s56", "s57", "s58", "s59", "s60", "s61", "s62", "s63", "s64", "s65", "s66", "s67", "s68", "s69", "s70", "s71", "s72", "s73", "s74", "s75", "s76", "s77", "s78", "s79", "s80", "s81", "s82", "s83", "s84", "s85", "s86", "s87", "s88", "s89", "s90", "s91", "s92", "s93", "s94", "s95", "s96", "s97", "vcc", "scc", "memory");
    LANE_ID(lq_);
    const int g = QT0 + (NCV - 1 - iw) * 164 + min(wave * 64 + lq_, 163);
    if (QTB) touch_lds((const char*)q + (size_t)(g < 32768 ? g : 0) * 128, sScr);
  }
  WAIT_LGKM0();
  __builtin_amdgcn_s_barrier();
  CTS(10);
  }
  const unsigned sDbg0 = 110u * 1048576u + (b * 4 + wave) * 8192;
  constexpr unsigned OOB = 0x80000000u;
  if (kind == 6) {
    LANE_ID(lr);
    const int r8 = lr & 7, seg = lr >> 3, j = lr & 15, g = lr >> 4, rr = 8 * g + (j >> 2);
    const unsigned sWave = wave, sExp = __builtin_amdgcn_readfirstlane(ep), sG0 = slice < 16 ? slice + 1 : 0, sStep = NRW, sDbg = sDbg0;
    const unsigned sWw = ((slice >> 3) * 1792 + 16 * wave) * 2048 + (slice & 7) * 256;
    const unsigned vWd0 = seg * 2048 + (r8 ^ swzB(seg)) * 16, vWd1 = (seg + 8) * 2048 + (r8 ^ swzB(8 + seg)) * 16;
    const unsigned vWr0 = (wave >> 1) * 8192 + rr * 128 + (((wave & 1) * 4 + (lr & 3)) ^ swzB(rr)) * 16, vWr1 = vWr0 + 8;
    const unsigned vRF = (wave * 8 + (seg >> 2)) * 1024 + ((seg & 3) * 16 + r8) * 16;
    const unsigned vRwr = seg == 0 ? r8 * 64 + wave * 4 : 512 + lr * 4, vRrd = r8 * 64;
    const unsigned vRP = g == (j >> 2) ? r8 * 64 + 16 + wave * 8 + (seg & 1) * 4 : 768 + lr * 4;
    const unsigned vRS = (wave == 0 && seg == 0) ? r8 * 16 : OOB, vFlag = lr < 32 ? lr * 4 : OOB;
    const unsigned vL4 = ((lr & 2) * 4 + wave * 2 + (lr & 1)) * 128, vLY = min((wave < 3 ? wave * 32 : 112) + lr, wave < 2 ? wave * 32 + 31 : wave == 2 ? 111 : NCV - 1) * 128;
    const unsigned vQ = r8 * 2048 + wave * 512 + seg * 16;
    const unsigned vY0 = wave * 4096 + lr * 16;
    const unsigned vTr = 4096 + wave * 4096 + ((2 * g + ((j & 3) >> 1)) * 8 + (j >> 2)) * 16 + (j & 1) * 8;
    const unsigned one = g == (j >> 2) ? 0x3f80u << (16 * (j & 1)) : 0u, vSa = (j & 2) ? 0u : one, vSb = (j & 2) ? one : 0u;
    (void)sDbg;
    asm volatile(
R"(.macro M d,w,x
v_mfma_f32_16x16x32_bf16 v[\d:\d+3], \w, v[\x:\x+3], v[\d:\d+3]
.endm
.macro R d,a,o
ds_read_b128 v[\d:\d+3], v\a offset:\o
.endm
.macro T d,a,o
ds_read_b64_tr_b16 \d, v\a offset:\o
.endm
.macro D a,s,o
buffer_load_dwordx4 v\a, s[36:39], s\s offen offset:\o sc1 lds
.endm
.macro L c
s_waitcnt lgkmcnt(\c)
.endm
Lcode_r_b:
s_mov_b64 s[36:37],%0
s_mov_b32 s38,111243392
s_mov_b32 s39,0x00020000
s_mov_b32 s44,%1
s_mov_b32 s45,%2
s_mov_b32 s46,%3
s_mov_b32 s50,%4
s_mov_b32 s53,%5
v_mov_b32 v0,%6
v_mov_b32 v1,%7
v_mov_b32 v2,%8
v_mov_b32 v3,%9
v_mov_b32 v4,%10
v_mov_b32 v5,%11
v_mov_b32 v6,%12
v_mov_b32 v7,%13
v_mov_b32 v8,%14
v_mov_b32 v9,%15
v_mov_b32 v10,%16
v_mov_b32 v11,%17
v_mov_b32 v12,%18
v_mov_b32 v13,%19
v_mov_b32 v14,%20
v_mov_b32 v15,%21
v_mov_b32 v16,%22
s_mov_b32 s47,256
s_mov_b32 s49,0
s_mov_b32 s93,s45
s_mov_b64 s[84:85],%23
s_mov_b32 s86,4194304
s_mov_b32 s87,0x00020000
s_lshl_b32 s55,s45,14
buffer_load_dwordx4 v[164:167],v8,s[84:87],s55 offen offset:0 nt
buffer_load_dwordx4 v[168:171],v8,s[84:87],s55 offen offset:128 nt
buffer_load_dwordx4 v[172:175],v8,s[84:87],s55 offen offset:256 nt
buffer_load_dwordx4 v[176:179],v8,s[84:87],s55 offen offset:384 nt
v_mov_b32 v17,s53
v_mbcnt_lo_u32_b32 v19,-1,0
v_mbcnt_hi_u32_b32 v19,-1,v19
v_lshlrev_b32 v19,4,v19
s_mov_b32 s82,0
s_mov_b32 s63,0x7fff0000
s_mov_b32 s64,0
s_mov_b32 s67,0
s_lshl_b32 s65,s44,12
s_add_u32 s65,s65,4096
s_lshl_b32 s66,s44,7
s_add_u32 s66,s66,109056000
s_mov_b32 s70,0xaaaaaaaa
s_mov_b32 s71,0xaaaaaaaa
s_mov_b32 s72,0xcccccccc
s_mov_b32 s73,0xcccccccc
s_mov_b32 s74,0xff00ff00
s_mov_b32 s75,0xff00ff00
s_mov_b32 s68,0x3a800000
s_mov_b32 s69,0x3727c5ac
s_mov_b32 s76,0xbeeff558
v_mov_b32 v32,0x3f803f80
v_mov_b32 v33,0x3f803f80
v_mov_b32 v34,0x3f803f80
v_mov_b32 v35,0x3f803f80
v_mov_b32 v24,0
v_mov_b32 v25,0
v_mov_b32 v26,v11
v_mov_b32 v27,v12
v_mov_b32 v28,0
v_mov_b32 v29,0
s_getpc_b64 s[94:95]
Lr_pc_%=:
s_add_u32 s94,s94,Lr_tab_%=-Lr_pc_%=
s_addc_u32 s95,s95,0
s_mov_b32 s81,0x7fff0000
s_lshl_b32 s54,s44,11
s_add_u32 s80,s45,85
s_cmp_eq_u32 s45,0
s_cselect_b32 s80,-1,s80
s_waitcnt vmcnt(0)
Lr_a0_%=:
s_cmp_lg_u32 s67,0
s_cbranch_scc1 Lr_b0_%=
s_waitcnt vmcnt(14)
v_mfma_f32_16x16x32_bf16 v[36:39],v[32:35],v[164:167],0
s_add_u32 s79,s45,s46
s_cmp_lt_u32 s45,64
s_cselect_b32 s78,s49,0
v_mfma_f32_16x16x32_bf16 v[40:43],v[164:167],v[164:167],0
s_cmp_ge_u32 s79,64
s_cselect_b32 s78,s78,0
s_add_u32 s62,s79,s78
M 36,v[32:35],168
s_lshr_b32 s78,s45,1
s_and_b32 s79,s45,1
s_lshl_b32 s79,s79,7
M 40,v[168:171],168
s_lshl_b32 s57,s78,15
s_lshl_b32 s55,s62,14
s_cmp_lt_u32 s62,s47
M 36,v[32:35],172
s_cselect_b32 s55,s55,0x7fff0000
s_add_u32 s57,s57,s79
s_lshl_b32 s59,s78,9
M 40,v[172:175],172
buffer_load_dwordx4 v[196:199],v8,s[84:87],s55 offen offset:0 nt
M 36,v[32:35],176
buffer_load_dwordx4 v[200:203],v8,s[84:87],s55 offen offset:128 nt
M 40,v[176:179],176
buffer_load_dwordx4 v[204:207],v8,s[84:87],s55 offen offset:256 nt
v_mfma_f32_16x16x32_bf16 v[116:119],v[26:29],v[164:167],0
buffer_load_dwordx4 v[208:211],v8,s[84:87],s55 offen offset:384 nt
v_mfma_f32_16x16x32_bf16 v[120:123],v[24:27],v[164:167],0
s_add_u32 s57,s57,8388608
s_add_u32 s59,s59,s79
s_lshl_b32 s60,s45,9
v_mfma_f32_16x16x32_bf16 v[124:127],v[26:29],v[168:171],0
s_add_u32 s58,s57,4096
s_add_u32 s59,s59,106954752
s_add_u32 s60,s60,s66
v_mfma_f32_16x16x32_bf16 v[128:131],v[24:27],v[168:171],0
v_add_f32_dpp v44,v36,v36 row_ror:8 row_mask:0xf bank_mask:0xf
v_cndmask_b32 v45,v40,v41,s[70:71]
v_mfma_f32_16x16x32_bf16 v[132:135],v[26:29],v[172:175],0
ds_write_b32 v2,v44 offset:0
v_mfma_f32_16x16x32_bf16 v[136:139],v[24:27],v[172:175],0
v_cndmask_b32 v46,v42,v43,s[70:71]
v_cndmask_b32 v45,v45,v46,s[72:73]
v_mfma_f32_16x16x32_bf16 v[140:143],v[26:29],v[176:179],0
ds_write_b32 v0,v45 offset:0
v_mfma_f32_16x16x32_bf16 v[144:147],v[24:27],v[176:179],0
L 0
s_barrier
R 52,3,0
R 56,3,16
R 60,3,32
s_waitcnt vmcnt(8)
buffer_store_dword v17,v5,s[36:39],s63 offen offset:0 sc0 sc1
s_mov_b32 s63,0x7fff0000
s_cmp_lt_u32 s45,64
s_cselect_b32 s64,-192,0
s_add_u32 s64,s64,256
s_cmp_lt_u32 s45,64
s_cselect_b32 s61,0,32768
s_add_u32 s61,s61,111177728
s_mul_i32 s56,s45,16384
s_add_u32 s56,s56,98566144
Lr_bpy0_%=:
buffer_load_dword v18,v7,s[36:39],s61 offen offset:0 sc1
s_add_u32 s82,s82,1
s_waitcnt vmcnt(0)
v_cmp_eq_u32 vcc,s53,v18
s_nop 0
s_cmp_eq_u64 vcc,-1
s_cbranch_scc1 Lr_bqy0_%=
s_sleep 2
s_cmp_lt_u32 s82,20000
s_cbranch_scc1 Lr_bpy0_%=
Lr_bqy0_%=:
s_add_u32 m0,s65,0
s_nop 0
D 9,56,0
D 9,56,1024
D 9,56,2048
D 9,56,3072
s_waitcnt vmcnt(0)
T v[68:69],10,0
T v[70:71],10,64
T v[72:73],10,1024
T v[74:75],10,1088
T v[76:77],10,2048
T v[78:79],10,2112
T v[80:81],10,3072
T v[82:83],10,3136
s_mov_b32 s45,s62
s_min_u32 s78,s64,s47
s_mul_i32 s56,s45,16384
s_add_u32 s56,s56,98566144
s_cmp_lt_u32 s45,s78
s_cselect_b32 s67,1,0
s_cselect_b32 s56,s56,0x7fff0000
s_add_u32 m0,s65,16384
s_nop 0
D 9,56,0
D 9,56,1024
D 9,56,2048
D 9,56,3072
L 4
v_lshlrev_b32 v84,16,v68
v_and_b32 v85,0xffff0000,v68
v_lshlrev_b32 v86,16,v69
v_and_b32 v87,0xffff0000,v69
v_lshlrev_b32 v88,16,v70
v_and_b32 v89,0xffff0000,v70
v_lshlrev_b32 v90,16,v71
v_and_b32 v91,0xffff0000,v71
v_lshlrev_b32 v92,16,v72
v_and_b32 v93,0xffff0000,v72
v_lshlrev_b32 v94,16,v73
v_and_b32 v95,0xffff0000,v73
v_lshlrev_b32 v96,16,v74
v_and_b32 v97,0xffff0000,v74
v_lshlrev_b32 v98,16,v75
v_and_b32 v99,0xffff0000,v75
L 0
v_lshlrev_b32 v100,16,v76
v_and_b32 v101,0xffff0000,v76
v_lshlrev_b32 v102,16,v77
v_and_b32 v103,0xffff0000,v77
v_lshlrev_b32 v104,16,v78
v_and_b32 v105,0xffff0000,v78
v_lshlrev_b32 v106,16,v79
v_and_b32 v107,0xffff0000,v79
v_lshlrev_b32 v108,16,v80
v_and_b32 v109,0xffff0000,v80
v_lshlrev_b32 v110,16,v81
v_and_b32 v111,0xffff0000,v81
v_lshlrev_b32 v112,16,v82
v_and_b32 v113,0xffff0000,v82
v_lshlrev_b32 v114,16,v83
v_and_b32 v115,0xffff0000,v83
s_branch Lr_c0_%=
Lr_b0_%=:
s_waitcnt vmcnt(10)
T v[68:69],10,0
T v[70:71],10,64
T v[72:73],10,1024
T v[74:75],10,1088
T v[76:77],10,2048
T v[78:79],10,2112
T v[80:81],10,3072
T v[82:83],10,3136
v_mfma_f32_16x16x32_bf16 v[36:39],v[32:35],v[164:167],0
s_add_u32 s79,s45,s46
s_cmp_lt_u32 s45,64
s_cselect_b32 s78,s49,0
v_mfma_f32_16x16x32_bf16 v[40:43],v[164:167],v[164:167],0
s_cmp_ge_u32 s79,64
s_cselect_b32 s78,s78,0
s_add_u32 s62,s79,s78
M 36,v[32:35],168
s_lshr_b32 s78,s45,1
s_and_b32 s79,s45,1
s_lshl_b32 s60,s45,9
M 40,v[168:171],168
s_lshl_b32 s57,s78,15
s_lshl_b32 s59,s78,9
s_mov_b32 s45,s62
M 36,v[32:35],172
s_lshl_b32 s79,s79,7
s_min_u32 s78,s64,s47
s_mul_i32 s56,s45,16384
M 40,v[172:175],172
s_lshl_b32 s55,s62,14
s_cmp_lt_u32 s62,s47
s_cselect_b32 s55,s55,0x7fff0000
M 36,v[32:35],176
buffer_load_dwordx4 v[196:199],v8,s[84:87],s55 offen offset:0 nt
M 40,v[176:179],176
buffer_load_dwordx4 v[200:203],v8,s[84:87],s55 offen offset:128 nt
v_mfma_f32_16x16x32_bf16 v[116:119],v[26:29],v[164:167],0
buffer_load_dwordx4 v[204:207],v8,s[84:87],s55 offen offset:256 nt
v_mfma_f32_16x16x32_bf16 v[120:123],v[24:27],v[164:167],0
buffer_load_dwordx4 v[208:211],v8,s[84:87],s55 offen offset:384 nt
v_mfma_f32_16x16x32_bf16 v[124:127],v[26:29],v[168:171],0
s_add_u32 s57,s57,s79
s_add_u32 s56,s56,98566144
s_cmp_lt_u32 s45,s78
v_mfma_f32_16x16x32_bf16 v[128:131],v[24:27],v[168:171],0
s_cselect_b32 s56,s56,0x7fff0000
v_add_f32_dpp v44,v36,v36 row_ror:8 row_mask:0xf bank_mask:0xf
v_cndmask_b32 v45,v40,v41,s[70:71]
v_mfma_f32_16x16x32_bf16 v[132:135],v[26:29],v[172:175],0
s_cselect_b32 s67,1,0
ds_write_b32 v2,v44 offset:0
v_mfma_f32_16x16x32_bf16 v[136:139],v[24:27],v[172:175],0
s_add_u32 s57,s57,8388608
v_cndmask_b32 v46,v42,v43,s[70:71]
v_cndmask_b32 v45,v45,v46,s[72:73]
v_mfma_f32_16x16x32_bf16 v[140:143],v[26:29],v[176:179],0
s_add_u32 s59,s59,s79
ds_write_b32 v0,v45 offset:0
v_mfma_f32_16x16x32_bf16 v[144:147],v[24:27],v[176:179],0
L 2
s_add_u32 m0,s65,16384
s_add_u32 s58,s57,4096
v_mfma_f32_16x16x32_bf16 v[84:87],v[26:29],v[68:71],0
D 9,56,0
v_mfma_f32_16x16x32_bf16 v[88:91],v[24:27],v[68:71],0
D 9,56,1024
v_mfma_f32_16x16x32_bf16 v[92:95],v[26:29],v[72:75],0
L 0
D 9,56,2048
v_mfma_f32_16x16x32_bf16 v[96:99],v[24:27],v[72:75],0
s_barrier
R 52,3,0
R 56,3,16
v_mfma_f32_16x16x32_bf16 v[100:103],v[26:29],v[76:79],0
s_add_u32 s59,s59,106954752
s_add_u32 s60,s60,s66
R 60,3,32
v_mfma_f32_16x16x32_bf16 v[104:107],v[24:27],v[76:79],0
D 9,56,3072
v_mfma_f32_16x16x32_bf16 v[108:111],v[26:29],v[80:83],0
v_mfma_f32_16x16x32_bf16 v[112:115],v[24:27],v[80:83],0
Lr_c0_%=:
L 0
v_pk_add_f32 v[52:53],v[52:53],v[54:55]
v_pk_add_f32 v[56:57],v[56:57],v[58:59]
v_pk_add_f32 v[60:61],v[60:61],v[62:63]
v_add_f32 v52,v52,v53
v_pk_add_f32 v[56:57],v[56:57],v[60:61]
v_mul_f32 v52,s68,v52
v_add_f32 v53,v56,v57
v_mul_f32 v53,s68,v53
v_fma_f32 v53,-v52,v52,v53
v_max_f32 v53,0,v53
s_waitcnt vmcnt(12)
buffer_store_dword v17,v5,s[36:39],s63 offen offset:0 sc0 sc1
s_mov_b32 s63,0x7fff0000
v_add_f32 v53,s69,v53
v_rsq_f32 v48,v53
s_nop 0
v_mul_f32_e64 v50,-v52,v48
v_mov_b32 v49,v48
v_mov_b32 v51,v50
v_pk_fma_f32 v[52:53],v[116:117],v[48:49],v[50:51]
v_pk_fma_f32 v[54:55],v[118:119],v[48:49],v[50:51]
v_pk_fma_f32 v[56:57],v[120:121],v[48:49],v[50:51]
v_pk_fma_f32 v[58:59],v[122:123],v[48:49],v[50:51]
v_pk_fma_f32 v[116:117],v[52:53],v[84:85],v[116:117]
v_pk_fma_f32 v[118:119],v[54:55],v[86:87],v[118:119]
v_pk_fma_f32 v[120:121],v[56:57],v[88:89],v[120:121]
v_pk_fma_f32 v[122:123],v[58:59],v[90:91],v[122:123]
v_pk_fma_f32 v[52:53],v[124:125],v[48:49],v[50:51]
v_pk_fma_f32 v[54:55],v[126:127],v[48:49],v[50:51]
v_pk_fma_f32 v[56:57],v[128:129],v[48:49],v[50:51]
v_pk_fma_f32 v[58:59],v[130:131],v[48:49],v[50:51]
v_pk_fma_f32 v[124:125],v[52:53],v[92:93],v[124:125]
v_pk_fma_f32 v[126:127],v[54:55],v[94:95],v[126:127]
v_pk_fma_f32 v[128:129],v[56:57],v[96:97],v[128:129]
v_pk_fma_f32 v[130:131],v[58:59],v[98:99],v[130:131]
v_pk_fma_f32 v[52:53],v[132:133],v[48:49],v[50:51]
v_pk_fma_f32 v[54:55],v[134:135],v[48:49],v[50:51]
v_pk_fma_f32 v[56:57],v[136:137],v[48:49],v[50:51]
v_pk_fma_f32 v[58:59],v[138:139],v[48:49],v[50:51]
v_pk_fma_f32 v[132:133],v[52:53],v[100:101],v[132:133]
v_pk_fma_f32 v[134:135],v[54:55],v[102:103],v[134:135]
v_pk_fma_f32 v[136:137],v[56:57],v[104:105],v[136:137]
v_pk_fma_f32 v[138:139],v[58:59],v[106:107],v[138:139]
v_pk_fma_f32 v[52:53],v[140:141],v[48:49],v[50:51]
v_pk_fma_f32 v[54:55],v[142:143],v[48:49],v[50:51]
v_pk_fma_f32 v[56:57],v[144:145],v[48:49],v[50:51]
v_pk_fma_f32 v[58:59],v[146:147],v[48:49],v[50:51]
v_pk_fma_f32 v[140:141],v[52:53],v[108:109],v[140:141]
v_pk_fma_f32 v[142:143],v[54:55],v[110:111],v[142:143]
v_pk_fma_f32 v[144:145],v[56:57],v[112:113],v[144:145]
v_pk_fma_f32 v[146:147],v[58:59],v[114:115],v[146:147]
v_cvt_pk_bf16_f32 v148,v116,v117
v_cvt_pk_bf16_f32 v149,v118,v119
v_cvt_pk_bf16_f32 v150,v120,v121
v_cvt_pk_bf16_f32 v151,v122,v123
v_cvt_pk_bf16_f32 v152,v124,v125
v_cvt_pk_bf16_f32 v153,v126,v127
v_mfma_f32_16x16x32_bf16 v[36:39],v[32:35],v[148:151],0
v_cvt_pk_bf16_f32 v154,v128,v129
v_cvt_pk_bf16_f32 v155,v130,v131
v_mfma_f32_16x16x32_bf16 v[40:43],v[148:151],v[148:151],0
v_cvt_pk_bf16_f32 v156,v132,v133
v_cvt_pk_bf16_f32 v157,v134,v135
M 36,v[32:35],152
v_cvt_pk_bf16_f32 v158,v136,v137
v_cvt_pk_bf16_f32 v159,v138,v139
M 40,v[152:155],152
v_cvt_pk_bf16_f32 v160,v140,v141
v_cvt_pk_bf16_f32 v161,v142,v143
M 36,v[32:35],156
v_cvt_pk_bf16_f32 v162,v144,v145
v_cvt_pk_bf16_f32 v163,v146,v147
M 40,v[156:159],156
buffer_store_dwordx4 v[148:151],v1,s[36:39],s57 offen offset:0 sc0 sc1
M 36,v[32:35],160
buffer_store_dwordx4 v[152:155],v1,s[36:39],s57 offen offset:2048 sc0 sc1
M 40,v[160:163],160
buffer_store_dwordx4 v[156:159],v1,s[36:39],s58 offen offset:0 sc0 sc1
s_nop 4
v_add_f32_dpp v44,v36,v36 row_ror:8 row_mask:0xf bank_mask:0xf
s_nop 0
v_cndmask_b32 v45,v40,v41,s[70:71]
v_cndmask_b32 v46,v42,v43,s[70:71]
ds_write_b32 v2,v44 offset:1024
v_cndmask_b32 v45,v45,v46,s[72:73]
ds_write_b32 v0,v45 offset:1024
buffer_store_dwordx4 v[160:163],v1,s[36:39],s58 offen offset:2048 sc0 sc1
s_waitcnt vmcnt(13) lgkmcnt(0)
s_barrier
R 52,3,1024
R 56,3,1040
R 60,3,1056
L 0
v_pk_add_f32 v[52:53],v[52:53],v[54:55]
v_pk_add_f32 v[56:57],v[56:57],v[58:59]
v_pk_add_f32 v[60:61],v[60:61],v[62:63]
v_add_f32 v52,v52,v53
v_pk_add_f32 v[56:57],v[56:57],v[60:61]
v_mul_f32 v52,s68,v52
v_add_f32 v53,v56,v57
v_mul_f32 v53,s68,v53
v_fma_f32 v53,-v52,v52,v53
v_max_f32 v53,0,v53
v_add_f32 v54,s69,v53
v_fma_f32 v53,s69,v54,v53
v_rsq_f32 v55,v54
v_rsq_f32 v53,v53
v_mul_f32_e64 v54,-v52,v55
v_mul_f32 v53,s76,v53
buffer_store_dwordx4 v[52:55],v4,s[36:39],s59 offen offset:0 sc0 sc1
s_mov_b32 s63,s60
s_cmp_lt_u32 s45,s80
s_cbranch_scc1 Lr_bg0_%=
s_swappc_b64 s[88:89],s[94:95]
s_add_u32 s94,s94,256
s_addc_u32 s95,s95,0
s_branch Lr_bh0_%=
Lr_bg0_%=:
buffer_store_dword v17,v5,s[36:39],s81 offen offset:0 sc0 sc1
buffer_store_dword v17,v5,s[36:39],s81 offen offset:0 sc0 sc1
buffer_store_dword v17,v5,s[36:39],s81 offen offset:0 sc0 sc1
buffer_store_dword v17,v5,s[36:39],s81 offen offset:0 sc0 sc1
Lr_bh0_%=:
s_cmp_lt_u32 s45,s47
s_cbranch_scc0 Lr_mid_%=
Lr_a1_%=:
s_cmp_lg_u32 s67,0
s_cbranch_scc1 Lr_b1_%=
s_waitcnt vmcnt(14)
v_mfma_f32_16x16x32_bf16 v[36:39],v[32:35],v[196:199],0
s_add_u32 s79,s45,s46
s_cmp_lt_u32 s45,64
s_cselect_b32 s78,s49,0
v_mfma_f32_16x16x32_bf16 v[40:43],v[196:199],v[196:199],0
s_cmp_ge_u32 s79,64
s_cselect_b32 s78,s78,0
s_add_u32 s62,s79,s78
M 36,v[32:35],200
s_lshr_b32 s78,s45,1
s_and_b32 s79,s45,1
s_lshl_b32 s79,s79,7
M 40,v[200:203],200
s_lshl_b32 s57,s78,15
s_lshl_b32 s55,s62,14
s_cmp_lt_u32 s62,s47
M 36,v[32:35],204
s_cselect_b32 s55,s55,0x7fff0000
s_add_u32 s57,s57,s79
s_lshl_b32 s59,s78,9
M 40,v[204:207],204
buffer_load_dwordx4 v[164:167],v8,s[84:87],s55 offen offset:0 nt
M 36,v[32:35],208
buffer_load_dwordx4 v[168:171],v8,s[84:87],s55 offen offset:128 nt
M 40,v[208:211],208
buffer_load_dwordx4 v[172:175],v8,s[84:87],s55 offen offset:256 nt
v_mfma_f32_16x16x32_bf16 v[116:119],v[26:29],v[196:199],0
buffer_load_dwordx4 v[176:179],v8,s[84:87],s55 offen offset:384 nt
v_mfma_f32_16x16x32_bf16 v[120:123],v[24:27],v[196:199],0
s_add_u32 s57,s57,8388608
s_add_u32 s59,s59,s79
s_lshl_b32 s60,s45,9
v_mfma_f32_16x16x32_bf16 v[124:127],v[26:29],v[200:203],0
s_add_u32 s58,s57,4096
s_add_u32 s59,s59,106954752
s_add_u32 s60,s60,s66
v_mfma_f32_16x16x32_bf16 v[128:131],v[24:27],v[200:203],0
v_add_f32_dpp v44,v36,v36 row_ror:8 row_mask:0xf bank_mask:0xf
v_cndmask_b32 v45,v40,v41,s[70:71]
v_mfma_f32_16x16x32_bf16 v[132:135],v[26:29],v[204:207],0
ds_write_b32 v2,v44 offset:0
v_mfma_f32_16x16x32_bf16 v[136:139],v[24:27],v[204:207],0
v_cndmask_b32 v46,v42,v43,s[70:71]
v_cndmask_b32 v45,v45,v46,s[72:73]
v_mfma_f32_16x16x32_bf16 v[140:143],v[26:29],v[208:211],0
ds_write_b32 v0,v45 offset:0
v_mfma_f32_16x16x32_bf16 v[144:147],v[24:27],v[208:211],0
L 0
s_barrier
R 52,3,0
R 56,3,16
)"
R"(R 60,3,32
s_waitcnt vmcnt(8)
buffer_store_dword v17,v5,s[36:39],s63 offen offset:0 sc0 sc1
s_mov_b32 s63,0x7fff0000
s_cmp_lt_u32 s45,64
s_cselect_b32 s64,-192,0
s_add_u32 s64,s64,256
s_cmp_lt_u32 s45,64
s_cselect_b32 s61,0,32768
s_add_u32 s61,s61,111177728
s_mul_i32 s56,s45,16384
s_add_u32 s56,s56,98566144
Lr_bpy1_%=:
buffer_load_dword v18,v7,s[36:39],s61 offen offset:0 sc1
s_add_u32 s82,s82,1
s_waitcnt vmcnt(0)
v_cmp_eq_u32 vcc,s53,v18
s_nop 0
s_cmp_eq_u64 vcc,-1
s_cbranch_scc1 Lr_bqy1_%=
s_sleep 2
s_cmp_lt_u32 s82,20000
s_cbranch_scc1 Lr_bpy1_%=
Lr_bqy1_%=:
s_add_u32 m0,s65,16384
s_nop 0
D 9,56,0
D 9,56,1024
D 9,56,2048
D 9,56,3072
s_waitcnt vmcnt(0)
T v[68:69],10,16384
T v[70:71],10,16448
T v[72:73],10,17408
T v[74:75],10,17472
T v[76:77],10,18432
T v[78:79],10,18496
T v[80:81],10,19456
T v[82:83],10,19520
s_mov_b32 s45,s62
s_min_u32 s78,s64,s47
s_mul_i32 s56,s45,16384
s_add_u32 s56,s56,98566144
s_cmp_lt_u32 s45,s78
s_cselect_b32 s67,1,0
s_cselect_b32 s56,s56,0x7fff0000
s_add_u32 m0,s65,0
s_nop 0
D 9,56,0
D 9,56,1024
D 9,56,2048
D 9,56,3072
L 4
v_lshlrev_b32 v84,16,v68
v_and_b32 v85,0xffff0000,v68
v_lshlrev_b32 v86,16,v69
v_and_b32 v87,0xffff0000,v69
v_lshlrev_b32 v88,16,v70
v_and_b32 v89,0xffff0000,v70
v_lshlrev_b32 v90,16,v71
v_and_b32 v91,0xffff0000,v71
v_lshlrev_b32 v92,16,v72
v_and_b32 v93,0xffff0000,v72
v_lshlrev_b32 v94,16,v73
v_and_b32 v95,0xffff0000,v73
v_lshlrev_b32 v96,16,v74
v_and_b32 v97,0xffff0000,v74
v_lshlrev_b32 v98,16,v75
v_and_b32 v99,0xffff0000,v75
L 0
v_lshlrev_b32 v100,16,v76
v_and_b32 v101,0xffff0000,v76
v_lshlrev_b32 v102,16,v77
v_and_b32 v103,0xffff0000,v77
v_lshlrev_b32 v104,16,v78
v_and_b32 v105,0xffff0000,v78
v_lshlrev_b32 v106,16,v79
v_and_b32 v107,0xffff0000,v79
v_lshlrev_b32 v108,16,v80
v_and_b32 v109,0xffff0000,v80
v_lshlrev_b32 v110,16,v81
v_and_b32 v111,0xffff0000,v81
v_lshlrev_b32 v112,16,v82
v_and_b32 v113,0xffff0000,v82
v_lshlrev_b32 v114,16,v83
v_and_b32 v115,0xffff0000,v83
s_branch Lr_c1_%=
Lr_b1_%=:
s_waitcnt vmcnt(10)
T v[68:69],10,16384
T v[70:71],10,16448
T v[72:73],10,17408
T v[74:75],10,17472
T v[76:77],10,18432
T v[78:79],10,18496
T v[80:81],10,19456
T v[82:83],10,19520
v_mfma_f32_16x16x32_bf16 v[36:39],v[32:35],v[196:199],0
s_add_u32 s79,s45,s46
s_cmp_lt_u32 s45,64
s_cselect_b32 s78,s49,0
v_mfma_f32_16x16x32_bf16 v[40:43],v[196:199],v[196:199],0
s_cmp_ge_u32 s79,64
s_cselect_b32 s78,s78,0
s_add_u32 s62,s79,s78
M 36,v[32:35],200
s_lshr_b32 s78,s45,1
s_and_b32 s79,s45,1
s_lshl_b32 s60,s45,9
M 40,v[200:203],200
s_lshl_b32 s57,s78,15
s_lshl_b32 s59,s78,9
s_mov_b32 s45,s62
M 36,v[32:35],204
s_lshl_b32 s79,s79,7
s_min_u32 s78,s64,s47
s_mul_i32 s56,s45,16384
M 40,v[204:207],204
s_lshl_b32 s55,s62,14
s_cmp_lt_u32 s62,s47
s_cselect_b32 s55,s55,0x7fff0000
M 36,v[32:35],208
buffer_load_dwordx4 v[164:167],v8,s[84:87],s55 offen offset:0 nt
M 40,v[208:211],208
buffer_load_dwordx4 v[168:171],v8,s[84:87],s55 offen offset:128 nt
v_mfma_f32_16x16x32_bf16 v[116:119],v[26:29],v[196:199],0
buffer_load_dwordx4 v[172:175],v8,s[84:87],s55 offen offset:256 nt
v_mfma_f32_16x16x32_bf16 v[120:123],v[24:27],v[196:199],0
buffer_load_dwordx4 v[176:179],v8,s[84:87],s55 offen offset:384 nt
v_mfma_f32_16x16x32_bf16 v[124:127],v[26:29],v[200:203],0
s_add_u32 s57,s57,s79
s_add_u32 s56,s56,98566144
s_cmp_lt_u32 s45,s78
v_mfma_f32_16x16x32_bf16 v[128:131],v[24:27],v[200:203],0
s_cselect_b32 s56,s56,0x7fff0000
v_add_f32_dpp v44,v36,v36 row_ror:8 row_mask:0xf bank_mask:0xf
v_cndmask_b32 v45,v40,v41,s[70:71]
v_mfma_f32_16x16x32_bf16 v[132:135],v[26:29],v[204:207],0
s_cselect_b32 s67,1,0
ds_write_b32 v2,v44 offset:0
v_mfma_f32_16x16x32_bf16 v[136:139],v[24:27],v[204:207],0
s_add_u32 s57,s57,8388608
v_cndmask_b32 v46,v42,v43,s[70:71]
v_cndmask_b32 v45,v45,v46,s[72:73]
v_mfma_f32_16x16x32_bf16 v[140:143],v[26:29],v[208:211],0
s_add_u32 s59,s59,s79
ds_write_b32 v0,v45 offset:0
v_mfma_f32_16x16x32_bf16 v[144:147],v[24:27],v[208:211],0
L 2
s_add_u32 m0,s65,0
s_add_u32 s58,s57,4096
v_mfma_f32_16x16x32_bf16 v[84:87],v[26:29],v[68:71],0
D 9,56,0
v_mfma_f32_16x16x32_bf16 v[88:91],v[24:27],v[68:71],0
D 9,56,1024
v_mfma_f32_16x16x32_bf16 v[92:95],v[26:29],v[72:75],0
L 0
D 9,56,2048
v_mfma_f32_16x16x32_bf16 v[96:99],v[24:27],v[72:75],0
s_barrier
R 52,3,0
R 56,3,16
v_mfma_f32_16x16x32_bf16 v[100:103],v[26:29],v[76:79],0
s_add_u32 s59,s59,106954752
s_add_u32 s60,s60,s66
R 60,3,32
v_mfma_f32_16x16x32_bf16 v[104:107],v[24:27],v[76:79],0
D 9,56,3072
v_mfma_f32_16x16x32_bf16 v[108:111],v[26:29],v[80:83],0
v_mfma_f32_16x16x32_bf16 v[112:115],v[24:27],v[80:83],0
Lr_c1_%=:
L 0
v_pk_add_f32 v[52:53],v[52:53],v[54:55]
v_pk_add_f32 v[56:57],v[56:57],v[58:59]
v_pk_add_f32 v[60:61],v[60:61],v[62:63]
v_add_f32 v52,v52,v53
v_pk_add_f32 v[56:57],v[56:57],v[60:61]
v_mul_f32 v52,s68,v52
v_add_f32 v53,v56,v57
v_mul_f32 v53,s68,v53
v_fma_f32 v53,-v52,v52,v53
v_max_f32 v53,0,v53
s_waitcnt vmcnt(12)
buffer_store_dword v17,v5,s[36:39],s63 offen offset:0 sc0 sc1
s_mov_b32 s63,0x7fff0000
v_add_f32 v53,s69,v53
v_rsq_f32 v48,v53
s_nop 0
v_mul_f32_e64 v50,-v52,v48
v_mov_b32 v49,v48
v_mov_b32 v51,v50
v_pk_fma_f32 v[52:53],v[116:117],v[48:49],v[50:51]
v_pk_fma_f32 v[54:55],v[118:119],v[48:49],v[50:51]
v_pk_fma_f32 v[56:57],v[120:121],v[48:49],v[50:51]
v_pk_fma_f32 v[58:59],v[122:123],v[48:49],v[50:51]
v_pk_fma_f32 v[116:117],v[52:53],v[84:85],v[116:117]
v_pk_fma_f32 v[118:119],v[54:55],v[86:87],v[118:119]
v_pk_fma_f32 v[120:121],v[56:57],v[88:89],v[120:121]
v_pk_fma_f32 v[122:123],v[58:59],v[90:91],v[122:123]
v_pk_fma_f32 v[52:53],v[124:125],v[48:49],v[50:51]
v_pk_fma_f32 v[54:55],v[126:127],v[48:49],v[50:51]
v_pk_fma_f32 v[56:57],v[128:129],v[48:49],v[50:51]
v_pk_fma_f32 v[58:59],v[130:131],v[48:49],v[50:51]
v_pk_fma_f32 v[124:125],v[52:53],v[92:93],v[124:125]
v_pk_fma_f32 v[126:127],v[54:55],v[94:95],v[126:127]
v_pk_fma_f32 v[128:129],v[56:57],v[96:97],v[128:129]
v_pk_fma_f32 v[130:131],v[58:59],v[98:99],v[130:131]
v_pk_fma_f32 v[52:53],v[132:133],v[48:49],v[50:51]
v_pk_fma_f32 v[54:55],v[134:135],v[48:49],v[50:51]
v_pk_fma_f32 v[56:57],v[136:137],v[48:49],v[50:51]
v_pk_fma_f32 v[58:59],v[138:139],v[48:49],v[50:51]
v_pk_fma_f32 v[132:133],v[52:53],v[100:101],v[132:133]
v_pk_fma_f32 v[134:135],v[54:55],v[102:103],v[134:135]
v_pk_fma_f32 v[136:137],v[56:57],v[104:105],v[136:137]
v_pk_fma_f32 v[138:139],v[58:59],v[106:107],v[138:139]
v_pk_fma_f32 v[52:53],v[140:141],v[48:49],v[50:51]
v_pk_fma_f32 v[54:55],v[142:143],v[48:49],v[50:51]
v_pk_fma_f32 v[56:57],v[144:145],v[48:49],v[50:51]
v_pk_fma_f32 v[58:59],v[146:147],v[48:49],v[50:51]
v_pk_fma_f32 v[140:141],v[52:53],v[108:109],v[140:141]
v_pk_fma_f32 v[142:143],v[54:55],v[110:111],v[142:143]
v_pk_fma_f32 v[144:145],v[56:57],v[112:113],v[144:145]
v_pk_fma_f32 v[146:147],v[58:59],v[114:115],v[146:147]
v_cvt_pk_bf16_f32 v148,v116,v117
v_cvt_pk_bf16_f32 v149,v118,v119
v_cvt_pk_bf16_f32 v150,v120,v121
v_cvt_pk_bf16_f32 v151,v122,v123
v_cvt_pk_bf16_f32 v152,v124,v125
v_cvt_pk_bf16_f32 v153,v126,v127
v_mfma_f32_16x16x32_bf16 v[36:39],v[32:35],v[148:151],0
v_cvt_pk_bf16_f32 v154,v128,v129
v_cvt_pk_bf16_f32 v155,v130,v131
v_mfma_f32_16x16x32_bf16 v[40:43],v[148:151],v[148:151],0
v_cvt_pk_bf16_f32 v156,v132,v133
v_cvt_pk_bf16_f32 v157,v134,v135
M 36,v[32:35],152
v_cvt_pk_bf16_f32 v158,v136,v137
v_cvt_pk_bf16_f32 v159,v138,v139
M 40,v[152:155],152
v_cvt_pk_bf16_f32 v160,v140,v141
v_cvt_pk_bf16_f32 v161,v142,v143
M 36,v[32:35],156
v_cvt_pk_bf16_f32 v162,v144,v145
v_cvt_pk_bf16_f32 v163,v146,v147
M 40,v[156:159],156
buffer_store_dwordx4 v[148:151],v1,s[36:39],s57 offen offset:0 sc0 sc1
M 36,v[32:35],160
buffer_store_dwordx4 v[152:155],v1,s[36:39],s57 offen offset:2048 sc0 sc1
M 40,v[160:163],160
buffer_store_dwordx4 v[156:159],v1,s[36:39],s58 offen offset:0 sc0 sc1
s_nop 4
v_add_f32_dpp v44,v36,v36 row_ror:8 row_mask:0xf bank_mask:0xf
s_nop 0
v_cndmask_b32 v45,v40,v41,s[70:71]
v_cndmask_b32 v46,v42,v43,s[70:71]
ds_write_b32 v2,v44 offset:1024
v_cndmask_b32 v45,v45,v46,s[72:73]
ds_write_b32 v0,v45 offset:1024
buffer_store_dwordx4 v[160:163],v1,s[36:39],s58 offen offset:2048 sc0 sc1
s_waitcnt vmcnt(13) lgkmcnt(0)
s_barrier
R 52,3,1024
R 56,3,1040
R 60,3,1056
L 0
v_pk_add_f32 v[52:53],v[52:53],v[54:55]
v_pk_add_f32 v[56:57],v[56:57],v[58:59]
v_pk_add_f32 v[60:61],v[60:61],v[62:63]
v_add_f32 v52,v52,v53
v_pk_add_f32 v[56:57],v[56:57],v[60:61]
v_mul_f32 v52,s68,v52
v_add_f32 v53,v56,v57
v_mul_f32 v53,s68,v53
v_fma_f32 v53,-v52,v52,v53
v_max_f32 v53,0,v53
v_add_f32 v54,s69,v53
v_fma_f32 v53,s69,v54,v53
v_rsq_f32 v55,v54
v_rsq_f32 v53,v53
v_mul_f32_e64 v54,-v52,v55
v_mul_f32 v53,s76,v53
buffer_store_dwordx4 v[52:55],v4,s[36:39],s59 offen offset:0 sc0 sc1
s_mov_b32 s63,s60
s_cmp_lt_u32 s45,s80
s_cbranch_scc1 Lr_bg1_%=
s_swappc_b64 s[88:89],s[94:95]
s_add_u32 s94,s94,256
s_addc_u32 s95,s95,0
s_branch Lr_bh1_%=
Lr_bg1_%=:
buffer_store_dword v17,v5,s[36:39],s81 offen offset:0 sc0 sc1
buffer_store_dword v17,v5,s[36:39],s81 offen offset:0 sc0 sc1
buffer_store_dword v17,v5,s[36:39],s81 offen offset:0 sc0 sc1
buffer_store_dword v17,v5,s[36:39],s81 offen offset:0 sc0 sc1
Lr_bh1_%=:
s_cmp_lt_u32 s45,s47
s_cbranch_scc1 Lr_a0_%=
Lr_mid_%=:
s_waitcnt vmcnt(4)
buffer_store_dword v17,v5,s[36:39],s63 offen offset:0 sc0 sc1
s_mov_b32 s63,0x7fff0000
s_mov_b32 s45,s93
s_cmp_eq_u32 s93,0
s_cbranch_scc1 Lr_h0_%=
s_mov_b64 s[40:41],%24
s_mov_b32 s42,7340032
s_mov_b32 s43,0x00020000
s_lshl_b32 s57,s44,11
s_add_u32 s55,s50,1441792
s_add_u32 m0,s57,106496
s_nop 0
buffer_load_dwordx4 v13,s[40:43],s55 offen offset:0  lds
s_add_u32 m0,s57,107520
s_nop 0
buffer_load_dwordx4 v14,s[40:43],s55 offen offset:0  lds
s_add_u32 m0,s57,114560
s_nop 0
buffer_load_dwordx4 v13,s[40:43],s55 offen offset:128  lds
s_add_u32 m0,s57,115584
s_nop 0
buffer_load_dwordx4 v14,s[40:43],s55 offen offset:128  lds
s_add_u32 s55,s50,1572864
s_add_u32 m0,s57,122880
s_nop 0
buffer_load_dwordx4 v13,s[40:43],s55 offen offset:0  lds
s_add_u32 m0,s57,123904
s_nop 0
buffer_load_dwordx4 v14,s[40:43],s55 offen offset:0  lds
s_add_u32 m0,s57,130944
s_nop 0
buffer_load_dwordx4 v13,s[40:43],s55 offen offset:128  lds
s_add_u32 m0,s57,131968
s_nop 0
buffer_load_dwordx4 v14,s[40:43],s55 offen offset:128  lds
s_add_u32 s55,s50,1703936
s_add_u32 m0,s57,139264
s_nop 0
buffer_load_dwordx4 v13,s[40:43],s55 offen offset:0  lds
s_add_u32 m0,s57,140288
s_nop 0
buffer_load_dwordx4 v14,s[40:43],s55 offen offset:0  lds
s_add_u32 m0,s57,147328
s_nop 0
buffer_load_dwordx4 v13,s[40:43],s55 offen offset:128  lds
s_add_u32 m0,s57,148352
s_nop 0
buffer_load_dwordx4 v14,s[40:43],s55 offen offset:128  lds
s_add_u32 s55,s50,1835008
s_add_u32 m0,s57,40960
s_nop 0
buffer_load_dwordx4 v13,s[40:43],s55 offen offset:0  lds
s_add_u32 m0,s57,41984
s_nop 0
buffer_load_dwordx4 v14,s[40:43],s55 offen offset:0  lds
s_add_u32 m0,s57,49024
s_nop 0
buffer_load_dwordx4 v13,s[40:43],s55 offen offset:128  lds
s_add_u32 m0,s57,50048
s_nop 0
buffer_load_dwordx4 v14,s[40:43],s55 offen offset:128  lds
s_add_u32 s55,s50,1966080
s_add_u32 m0,s57,57344
s_nop 0
buffer_load_dwordx4 v13,s[40:43],s55 offen offset:0  lds
s_add_u32 m0,s57,58368
s_nop 0
buffer_load_dwordx4 v14,s[40:43],s55 offen offset:0  lds
s_add_u32 m0,s57,65408
s_nop 0
buffer_load_dwordx4 v13,s[40:43],s55 offen offset:128  lds
s_add_u32 m0,s57,66432
s_nop 0
buffer_load_dwordx4 v14,s[40:43],s55 offen offset:128  lds
Lr_h0_%=:
s_lshr_b32 s78,s45,1
s_and_b32 s79,s45,1
s_lshl_b32 s79,s79,7
s_lshl_b32 s55,s78,15
s_add_u32 s55,s55,s79
s_mul_i32 s56,s45,16384
s_add_u32 s56,s56,98566144
s_lshl_b32 s61,s78,11
s_add_u32 s61,s61,110587904
s_add_u32 s78,s45,s46
s_lshr_b32 s78,s78,1
s_lshl_b32 s62,s78,11
s_add_u32 s62,s62,110587904
s_add_u32 m0,s65,0
s_nop 0
D 9,56,0
D 9,56,1024
D 9,56,2048
D 9,56,3072
Lr_bps_%=:
buffer_load_dword v18,v6,s[36:39],s61 offen offset:0 sc1
s_add_u32 s82,s82,1
s_waitcnt vmcnt(0)
v_cmp_eq_u32 vcc,s53,v18
s_nop 0
s_cmp_eq_u64 vcc,-1
s_cbranch_scc1 Lr_bqs_%=
s_sleep 2
s_cmp_lt_u32 s82,20000
s_cbranch_scc1 Lr_bps_%=
Lr_bqs_%=:
s_add_u32 s78,s55,71303168
buffer_load_dwordx4 v[164:167],v1,s[36:39],s78 offen offset:0 sc1
buffer_load_dwordx4 v[168:171],v1,s[36:39],s78 offen offset:2048 sc1
s_add_u32 s78,s55,71307264
buffer_load_dwordx4 v[172:175],v1,s[36:39],s78 offen offset:0 sc1
buffer_load_dwordx4 v[176:179],v1,s[36:39],s78 offen offset:2048 sc1
s_add_u32 s78,s55,89128960
buffer_load_dwordx4 v[180:183],v1,s[36:39],s78 offen offset:0 sc1
buffer_load_dwordx4 v[184:187],v1,s[36:39],s78 offen offset:2048 sc1
s_add_u32 s78,s55,89133056
buffer_load_dwordx4 v[188:191],v1,s[36:39],s78 offen offset:0 sc1
buffer_load_dwordx4 v[192:195],v1,s[36:39],s78 offen offset:2048 sc1
buffer_load_dword v18,v6,s[36:39],s62 offen offset:0 sc1
s_cmp_eq_u32 s93,0
s_cbranch_scc1 Lr_h2_%=
s_mov_b64 s[40:41],%24
s_mov_b32 s42,7340032
s_mov_b32 s43,0x00020000
s_lshl_b32 s57,s44,11
L 0
s_barrier
s_add_u32 s55,s50,2097152
s_add_u32 m0,s57,73728
s_nop 0
buffer_load_dwordx4 v13,s[40:43],s55 offen offset:0  lds
s_add_u32 m0,s57,74752
s_nop 0
buffer_load_dwordx4 v14,s[40:43],s55 offen offset:0  lds
s_add_u32 m0,s57,81792
s_nop 0
buffer_load_dwordx4 v13,s[40:43],s55 offen offset:128  lds
s_add_u32 m0,s57,82816
s_nop 0
buffer_load_dwordx4 v14,s[40:43],s55 offen offset:128  lds
v_add_u32 v44,90112,v15
v_add_u32 v45,90112,v16
T a[160:161],44,0
T a[162:163],44,512
T a[164:165],45,0
T a[166:167],45,512
T a[168:169],44,4096
T a[170:171],44,4608
T a[172:173],45,4096
T a[174:175],45,4608
L 0
s_barrier
s_add_u32 s55,s50,2228224
s_add_u32 m0,s57,90112
s_nop 0
buffer_load_dwordx4 v13,s[40:43],s55 offen offset:0  lds
s_add_u32 m0,s57,91136
s_nop 0
buffer_load_dwordx4 v14,s[40:43],s55 offen offset:0  lds
s_add_u32 m0,s57,98176
s_nop 0
buffer_load_dwordx4 v13,s[40:43],s55 offen offset:128  lds
s_add_u32 m0,s57,99200
s_nop 0
buffer_load_dwordx4 v14,s[40:43],s55 offen offset:128  lds
v_add_u32 v44,106496,v15
v_add_u32 v45,106496,v16
T a[176:177],44,0
T a[178:179],44,512
T a[180:181],45,0
T a[182:183],45,512
T a[184:185],44,4096
T a[186:187],44,4608
T a[188:189],45,4096
T a[190:191],45,4608
L 0
s_barrier
s_add_u32 s55,s50,2359296
s_add_u32 m0,s57,106496
s_nop 0
buffer_load_dwordx4 v13,s[40:43],s55 offen offset:0  lds
s_add_u32 m0,s57,107520
s_nop 0
buffer_load_dwordx4 v14,s[40:43],s55 offen offset:0  lds
s_add_u32 m0,s57,114560
s_nop 0
buffer_load_dwordx4 v13,s[40:43],s55 offen offset:128  lds
s_add_u32 m0,s57,115584
s_nop 0
buffer_load_dwordx4 v14,s[40:43],s55 offen offset:128  lds
v_add_u32 v44,122880,v15
v_add_u32 v45,122880,v16
T a[192:193],44,0
T a[194:195],44,512
T a[196:197],45,0
T a[198:199],45,512
T a[200:201],44,4096
T a[202:203],44,4608
T a[204:205],45,4096
T a[206:207],45,4608
L 0
)"
R"(s_barrier
s_add_u32 s55,s50,2490368
s_add_u32 m0,s57,122880
s_nop 0
buffer_load_dwordx4 v13,s[40:43],s55 offen offset:0  lds
s_add_u32 m0,s57,123904
s_nop 0
buffer_load_dwordx4 v14,s[40:43],s55 offen offset:0  lds
s_add_u32 m0,s57,130944
s_nop 0
buffer_load_dwordx4 v13,s[40:43],s55 offen offset:128  lds
s_add_u32 m0,s57,131968
s_nop 0
buffer_load_dwordx4 v14,s[40:43],s55 offen offset:128  lds
v_add_u32 v44,139264,v15
v_add_u32 v45,139264,v16
T a[208:209],44,0
T a[210:211],44,512
T a[212:213],45,0
T a[214:215],45,512
T a[216:217],44,4096
T a[218:219],44,4608
T a[220:221],45,4096
T a[222:223],45,4608
L 0
s_barrier
s_add_u32 s55,s50,2621440
s_add_u32 m0,s57,139264
s_nop 0
buffer_load_dwordx4 v13,s[40:43],s55 offen offset:0  lds
s_add_u32 m0,s57,140288
s_nop 0
buffer_load_dwordx4 v14,s[40:43],s55 offen offset:0  lds
s_add_u32 m0,s57,147328
s_nop 0
buffer_load_dwordx4 v13,s[40:43],s55 offen offset:128  lds
s_add_u32 m0,s57,148352
s_nop 0
buffer_load_dwordx4 v14,s[40:43],s55 offen offset:128  lds
v_add_u32 v44,40960,v15
v_add_u32 v45,40960,v16
T a[224:225],44,0
T a[226:227],44,512
T a[228:229],45,0
T a[230:231],45,512
T a[232:233],44,4096
T a[234:235],44,4608
T a[236:237],45,4096
T a[238:239],45,4608
L 0
s_barrier
s_add_u32 s55,s50,2752512
s_add_u32 m0,s57,40960
s_nop 0
buffer_load_dwordx4 v13,s[40:43],s55 offen offset:0  lds
s_add_u32 m0,s57,41984
s_nop 0
buffer_load_dwordx4 v14,s[40:43],s55 offen offset:0  lds
s_add_u32 m0,s57,49024
s_nop 0
buffer_load_dwordx4 v13,s[40:43],s55 offen offset:128  lds
s_add_u32 m0,s57,50048
s_nop 0
buffer_load_dwordx4 v14,s[40:43],s55 offen offset:128  lds
v_add_u32 v44,57344,v15
v_add_u32 v45,57344,v16
T a[240:241],44,0
T a[242:243],44,512
T a[244:245],45,0
T a[246:247],45,512
T a[248:249],44,4096
T a[250:251],44,4608
T a[252:253],45,4096
T a[254:255],45,4608
L 0
s_barrier
s_waitcnt vmcnt(24)
Lr_h2_%=:
s_lshr_b32 s78,s45,1
s_and_b32 s79,s45,1
s_lshl_b32 s79,s79,7
s_lshl_b32 s57,s78,15
s_add_u32 s57,s57,s79
s_add_u32 s57,s57,12582912
s_add_u32 s58,s57,4096
s_lshl_b32 s59,s78,9
s_add_u32 s59,s59,s79
s_add_u32 s59,s59,107020288
s_lshl_b32 s60,s45,9
s_add_u32 s60,s60,s66
s_add_u32 s60,s60,131072
s_add_u32 s45,s45,s46
s_lshr_b32 s78,s45,1
s_and_b32 s79,s45,1
s_lshl_b32 s79,s79,7
s_lshl_b32 s55,s78,15
s_add_u32 s55,s55,s79
s_mul_i32 s56,s45,16384
s_add_u32 s56,s56,98566144
s_lshl_b32 s61,s78,11
s_add_u32 s61,s61,110587904
s_add_u32 s78,s45,s46
s_lshr_b32 s78,s78,1
s_lshl_b32 s62,s78,11
s_add_u32 s62,s62,110587904
s_waitcnt vmcnt(0)
Lr_it0_%=:
T v[68:69],10,0
T v[70:71],10,64
T v[72:73],10,1024
T v[74:75],10,1088
T v[76:77],10,2048
T v[78:79],10,2112
T v[80:81],10,3072
T v[82:83],10,3136
s_nop 0
v_cmp_eq_u32 vcc,s53,v18
s_nop 0
s_cmp_eq_u64 vcc,-1
s_cselect_b32 s78,s45,256
s_cmp_lt_u32 s78,256
s_cbranch_scc1 Lr_ta0_%=
buffer_load_dword v18,v6,s[36:39],s61 offen offset:0 sc1
v_mfma_f32_16x16x32_bf16 v[148:151],v[164:167],v[180:183],0
s_add_u32 m0,s65,16384
M 148,v[168:171],184
D 9,56,0
M 148,v[172:175],188
D 9,56,1024
M 148,v[176:179],192
L 4
D 9,56,2048
v_mfma_f32_16x16x32_bf16 v[40:43],v[164:167],v[164:167],0
D 9,56,3072
M 40,v[168:171],168
L 0
v_lshlrev_b32 v84,16,v68
v_and_b32 v85,0xffff0000,v68
M 40,v[172:175],172
v_lshlrev_b32 v86,16,v69
v_and_b32 v87,0xffff0000,v69
M 40,v[176:179],176
v_lshlrev_b32 v88,16,v70
v_and_b32 v89,0xffff0000,v70
M 40,v[180:183],180
v_lshlrev_b32 v90,16,v71
v_and_b32 v91,0xffff0000,v71
M 40,v[184:187],184
v_lshlrev_b32 v92,16,v72
v_and_b32 v93,0xffff0000,v72
M 40,v[188:191],188
v_lshlrev_b32 v94,16,v73
v_and_b32 v95,0xffff0000,v73
M 40,v[192:195],192
v_lshlrev_b32 v96,16,v74
v_and_b32 v97,0xffff0000,v74
v_mfma_f32_16x16x32_bf16 v[36:39],v[32:35],v[164:167],0
v_lshlrev_b32 v98,16,v75
v_and_b32 v99,0xffff0000,v75
M 36,v[32:35],168
v_lshlrev_b32 v100,16,v76
v_and_b32 v101,0xffff0000,v76
M 36,v[32:35],172
v_lshlrev_b32 v102,16,v77
v_and_b32 v103,0xffff0000,v77
M 36,v[32:35],176
v_lshlrev_b32 v104,16,v78
v_and_b32 v105,0xffff0000,v78
M 36,v[32:35],180
v_fma_f32 v40,2.0,v148,v40
v_fma_f32 v41,2.0,v149,v41
M 36,v[32:35],184
v_fma_f32 v42,2.0,v150,v42
v_fma_f32 v43,2.0,v151,v43
M 36,v[32:35],188
v_cndmask_b32 v45,v40,v41,s[70:71]
v_cndmask_b32 v46,v42,v43,s[70:71]
M 36,v[32:35],192
v_cndmask_b32 v45,v45,v46,s[72:73]
v_lshlrev_b32 v106,16,v79
v_mfma_f32_16x16x32_bf16 v[116:119],v[26:29],v[164:167],0
v_and_b32 v107,0xffff0000,v79
v_lshlrev_b32 v108,16,v80
M 116,v[26:29],180
v_and_b32 v109,0xffff0000,v80
v_lshlrev_b32 v110,16,v81
v_mfma_f32_16x16x32_bf16 v[120:123],v[24:27],v[164:167],0
v_and_b32 v111,0xffff0000,v81
v_lshlrev_b32 v112,16,v82
M 120,v[24:27],180
v_and_b32 v113,0xffff0000,v82
v_lshlrev_b32 v114,16,v83
v_mfma_f32_16x16x32_bf16 v[124:127],v[26:29],v[168:171],0
v_add_f32_dpp v44,v36,v36 row_ror:8 row_mask:0xf bank_mask:0xf
v_and_b32 v115,0xffff0000,v83
M 124,v[26:29],184
ds_write_b32 v2,v44 offset:0
v_mfma_f32_16x16x32_bf16 v[128:131],v[24:27],v[168:171],0
ds_write_b32 v0,v45 offset:0
M 128,v[24:27],184
v_mfma_f32_16x16x32_bf16 v[132:135],v[26:29],v[172:175],0
M 132,v[26:29],188
L 0
v_mfma_f32_16x16x32_bf16 v[136:139],v[24:27],v[172:175],0
s_barrier
R 52,3,0
R 56,3,16
M 136,v[24:27],188
R 60,3,32
v_mfma_f32_16x16x32_bf16 v[140:143],v[26:29],v[176:179],0
M 140,v[26:29],192
v_mfma_f32_16x16x32_bf16 v[144:147],v[24:27],v[176:179],0
M 144,v[24:27],192
L 0
v_pk_add_f32 v[52:53],v[52:53],v[54:55]
v_pk_add_f32 v[56:57],v[56:57],v[58:59]
v_pk_add_f32 v[60:61],v[60:61],v[62:63]
v_add_f32 v52,v52,v53
v_pk_add_f32 v[56:57],v[56:57],v[60:61]
v_mul_f32 v52,s68,v52
v_add_f32 v53,v56,v57
v_mul_f32 v53,s68,v53
v_fma_f32 v53,-v52,v52,v53
v_max_f32 v53,0,v53
s_waitcnt vmcnt(5)
buffer_store_dword v17,v5,s[36:39],s63 offen offset:0 sc0 sc1
s_mov_b32 s63,0x7fff0000
s_waitcnt vmcnt(5)
v_cmp_eq_u32 vcc,s53,v18
s_nop 0
s_cmp_eq_u64 vcc,-1
s_cselect_b32 s78,s45,256
s_cmp_lt_u32 s78,256
s_cbranch_scc1 Lr_tb0_%=
buffer_load_dword v18,v6,s[36:39],s61 offen offset:0 sc1
v_add_f32 v53,s69,v53
v_rsq_f32 v48,v53
s_nop 0
v_mul_f32_e64 v50,-v52,v48
v_mov_b32 v49,v48
v_mov_b32 v51,v50
v_pk_fma_f32 v[52:53],v[116:117],v[48:49],v[50:51]
v_pk_fma_f32 v[54:55],v[118:119],v[48:49],v[50:51]
v_pk_fma_f32 v[56:57],v[120:121],v[48:49],v[50:51]
v_pk_fma_f32 v[58:59],v[122:123],v[48:49],v[50:51]
v_pk_fma_f32 v[116:117],v[52:53],v[84:85],v[116:117]
v_pk_fma_f32 v[118:119],v[54:55],v[86:87],v[118:119]
v_pk_fma_f32 v[120:121],v[56:57],v[88:89],v[120:121]
v_pk_fma_f32 v[122:123],v[58:59],v[90:91],v[122:123]
v_pk_fma_f32 v[52:53],v[124:125],v[48:49],v[50:51]
v_pk_fma_f32 v[54:55],v[126:127],v[48:49],v[50:51]
v_pk_fma_f32 v[56:57],v[128:129],v[48:49],v[50:51]
v_pk_fma_f32 v[58:59],v[130:131],v[48:49],v[50:51]
v_pk_fma_f32 v[124:125],v[52:53],v[92:93],v[124:125]
v_pk_fma_f32 v[126:127],v[54:55],v[94:95],v[126:127]
v_pk_fma_f32 v[128:129],v[56:57],v[96:97],v[128:129]
v_pk_fma_f32 v[130:131],v[58:59],v[98:99],v[130:131]
v_pk_fma_f32 v[52:53],v[132:133],v[48:49],v[50:51]
v_pk_fma_f32 v[54:55],v[134:135],v[48:49],v[50:51]
v_pk_fma_f32 v[56:57],v[136:137],v[48:49],v[50:51]
v_pk_fma_f32 v[58:59],v[138:139],v[48:49],v[50:51]
v_pk_fma_f32 v[132:133],v[52:53],v[100:101],v[132:133]
v_pk_fma_f32 v[134:135],v[54:55],v[102:103],v[134:135]
v_pk_fma_f32 v[136:137],v[56:57],v[104:105],v[136:137]
v_pk_fma_f32 v[138:139],v[58:59],v[106:107],v[138:139]
v_pk_fma_f32 v[52:53],v[140:141],v[48:49],v[50:51]
v_pk_fma_f32 v[54:55],v[142:143],v[48:49],v[50:51]
v_pk_fma_f32 v[56:57],v[144:145],v[48:49],v[50:51]
v_pk_fma_f32 v[58:59],v[146:147],v[48:49],v[50:51]
v_pk_fma_f32 v[140:141],v[52:53],v[108:109],v[140:141]
v_pk_fma_f32 v[142:143],v[54:55],v[110:111],v[142:143]
v_pk_fma_f32 v[144:145],v[56:57],v[112:113],v[144:145]
v_pk_fma_f32 v[146:147],v[58:59],v[114:115],v[146:147]
v_cvt_pk_bf16_f32 v148,v116,v117
v_cvt_pk_bf16_f32 v149,v118,v119
v_cvt_pk_bf16_f32 v150,v120,v121
v_cvt_pk_bf16_f32 v151,v122,v123
v_cvt_pk_bf16_f32 v152,v124,v125
v_cvt_pk_bf16_f32 v153,v126,v127
v_mfma_f32_16x16x32_bf16 v[36:39],v[32:35],v[148:151],0
v_cvt_pk_bf16_f32 v154,v128,v129
v_cvt_pk_bf16_f32 v155,v130,v131
v_mfma_f32_16x16x32_bf16 v[40:43],v[148:151],v[148:151],0
v_cvt_pk_bf16_f32 v156,v132,v133
v_cvt_pk_bf16_f32 v157,v134,v135
M 36,v[32:35],152
v_cvt_pk_bf16_f32 v158,v136,v137
v_cvt_pk_bf16_f32 v159,v138,v139
M 40,v[152:155],152
v_cvt_pk_bf16_f32 v160,v140,v141
v_cvt_pk_bf16_f32 v161,v142,v143
M 36,v[32:35],156
v_cvt_pk_bf16_f32 v162,v144,v145
v_cvt_pk_bf16_f32 v163,v146,v147
M 40,v[156:159],156
buffer_store_dwordx4 v[148:151],v1,s[36:39],s57 offen offset:0 sc0 sc1
M 36,v[32:35],160
buffer_store_dwordx4 v[152:155],v1,s[36:39],s57 offen offset:2048 sc0 sc1
M 40,v[160:163],160
buffer_store_dwordx4 v[156:159],v1,s[36:39],s58 offen offset:0 sc0 sc1
s_nop 4
v_add_f32_dpp v44,v36,v36 row_ror:8 row_mask:0xf bank_mask:0xf
s_nop 0
v_cndmask_b32 v45,v40,v41,s[70:71]
v_cndmask_b32 v46,v42,v43,s[70:71]
ds_write_b32 v2,v44 offset:1024
v_cndmask_b32 v45,v45,v46,s[72:73]
ds_write_b32 v0,v45 offset:1024
buffer_store_dwordx4 v[160:163],v1,s[36:39],s58 offen offset:2048 sc0 sc1
L 0
s_barrier
R 52,3,1024
R 56,3,1040
R 60,3,1056
L 0
v_pk_add_f32 v[52:53],v[52:53],v[54:55]
v_pk_add_f32 v[56:57],v[56:57],v[58:59]
v_pk_add_f32 v[60:61],v[60:61],v[62:63]
v_add_f32 v52,v52,v53
v_pk_add_f32 v[56:57],v[56:57],v[60:61]
v_mul_f32 v52,s68,v52
v_add_f32 v53,v56,v57
v_mul_f32 v53,s68,v53
v_fma_f32 v53,-v52,v52,v53
v_max_f32 v53,0,v53
v_add_f32 v54,s69,v53
v_fma_f32 v53,s69,v54,v53
v_rsq_f32 v55,v54
v_rsq_f32 v53,v53
v_mul_f32_e64 v54,-v52,v55
v_mul_f32 v53,s76,v53
buffer_store_dwordx4 v[52:55],v4,s[36:39],s59 offen offset:0 sc0 sc1
s_mov_b32 s63,s60
s_waitcnt vmcnt(5)
v_cmp_eq_u32 vcc,s53,v18
s_nop 0
s_cmp_eq_u64 vcc,-1
s_cselect_b32 s78,s45,256
s_cmp_lt_u32 s78,256
s_cbranch_scc1 Lr_tc0_%=
s_waitcnt vmcnt(0)
buffer_store_dword v17,v5,s[36:39],s63 offen offset:0 sc0 sc1
s_mov_b32 s63,0x7fff0000
s_cmp_ge_u32 s45,256
s_cbranch_scc1 Lr_done_%=
Lr_bp0_%=:
buffer_load_dword v18,v6,s[36:39],s61 offen offset:0 sc1
s_add_u32 s82,s82,1
s_waitcnt vmcnt(0)
v_cmp_eq_u32 vcc,s53,v18
s_nop 0
s_cmp_eq_u64 vcc,-1
s_cbranch_scc1 Lr_bq0_%=
s_sleep 2
s_cmp_lt_u32 s82,20000
s_cbranch_scc1 Lr_bp0_%=
Lr_bq0_%=:
s_add_u32 s78,s55,71303168
buffer_load_dwordx4 v[196:199],v1,s[36:39],s78 offen offset:0 sc1
buffer_load_dwordx4 v[200:203],v1,s[36:39],s78 offen offset:2048 sc1
s_add_u32 s78,s55,71307264
buffer_load_dwordx4 v[204:207],v1,s[36:39],s78 offen offset:0 sc1
buffer_load_dwordx4 v[208:211],v1,s[36:39],s78 offen offset:2048 sc1
s_add_u32 s78,s55,89128960
buffer_load_dwordx4 v[212:215],v1,s[36:39],s78 offen offset:0 sc1
buffer_load_dwordx4 v[216:219],v1,s[36:39],s78 offen offset:2048 sc1
s_add_u32 s78,s55,89133056
buffer_load_dwordx4 v[220:223],v1,s[36:39],s78 offen offset:0 sc1
buffer_load_dwordx4 v[224:227],v1,s[36:39],s78 offen offset:2048 sc1
buffer_load_dword v18,v6,s[36:39],s62 offen offset:0 sc1
s_lshr_b32 s78,s45,1
s_and_b32 s79,s45,1
s_lshl_b32 s79,s79,7
s_lshl_b32 s57,s78,15
s_add_u32 s57,s57,s79
s_add_u32 s57,s57,12582912
s_add_u32 s58,s57,4096
s_lshl_b32 s59,s78,9
s_add_u32 s59,s59,s79
s_add_u32 s59,s59,107020288
s_lshl_b32 s60,s45,9
s_add_u32 s60,s60,s66
s_add_u32 s60,s60,131072
s_add_u32 s45,s45,s46
s_lshr_b32 s78,s45,1
s_and_b32 s79,s45,1
s_lshl_b32 s79,s79,7
s_lshl_b32 s55,s78,15
s_add_u32 s55,s55,s79
s_mul_i32 s56,s45,16384
s_add_u32 s56,s56,98566144
s_lshl_b32 s61,s78,11
s_add_u32 s61,s61,110587904
s_add_u32 s78,s45,s46
s_lshr_b32 s78,s78,1
s_lshl_b32 s62,s78,11
s_add_u32 s62,s62,110587904
s_waitcnt vmcnt(0)
s_branch Lr_it1_%=
Lr_ta0_%=:
v_mfma_f32_16x16x32_bf16 v[148:151],v[164:167],v[180:183],0
s_add_u32 s78,s55,71303168
s_add_u32 m0,s65,16384
M 148,v[168:171],184
buffer_load_dwordx4 v[196:199],v1,s[36:39],s78 offen offset:0 sc1
M 148,v[172:175],188
buffer_load_dwordx4 v[200:203],v1,s[36:39],s78 offen offset:2048 sc1
M 148,v[176:179],192
L 4
D 9,56,0
v_mfma_f32_16x16x32_bf16 v[40:43],v[164:167],v[164:167],0
D 9,56,1024
M 40,v[168:171],168
L 0
D 9,56,2048
M 40,v[172:175],172
D 9,56,3072
M 40,v[176:179],176
s_add_u32 s78,s55,71307264
v_lshlrev_b32 v84,16,v68
v_and_b32 v85,0xffff0000,v68
M 40,v[180:183],180
buffer_load_dwordx4 v[204:207],v1,s[36:39],s78 offen offset:0 sc1
M 40,v[184:187],184
buffer_load_dwordx4 v[208:211],v1,s[36:39],s78 offen offset:2048 sc1
M 40,v[188:191],188
s_add_u32 s78,s55,89128960
v_lshlrev_b32 v86,16,v69
v_and_b32 v87,0xffff0000,v69
M 40,v[192:195],192
buffer_load_dwordx4 v[212:215],v1,s[36:39],s78 offen offset:0 sc1
v_mfma_f32_16x16x32_bf16 v[36:39],v[32:35],v[164:167],0
buffer_load_dwordx4 v[216:219],v1,s[36:39],s78 offen offset:2048 sc1
M 36,v[32:35],168
s_add_u32 s78,s55,89133056
v_lshlrev_b32 v88,16,v70
v_and_b32 v89,0xffff0000,v70
M 36,v[32:35],172
buffer_load_dwordx4 v[220:223],v1,s[36:39],s78 offen offset:0 sc1
M 36,v[32:35],176
buffer_load_dwordx4 v[224:227],v1,s[36:39],s78 offen offset:2048 sc1
M 36,v[32:35],180
v_fma_f32 v40,2.0,v148,v40
v_fma_f32 v41,2.0,v149,v41
M 36,v[32:35],184
v_fma_f32 v42,2.0,v150,v42
v_fma_f32 v43,2.0,v151,v43
M 36,v[32:35],188
v_cndmask_b32 v45,v40,v41,s[70:71]
v_cndmask_b32 v46,v42,v43,s[70:71]
M 36,v[32:35],192
v_cndmask_b32 v45,v45,v46,s[72:73]
v_lshlrev_b32 v90,16,v71
v_mfma_f32_16x16x32_bf16 v[116:119],v[26:29],v[164:167],0
v_and_b32 v91,0xffff0000,v71
v_lshlrev_b32 v92,16,v72
M 116,v[26:29],180
v_and_b32 v93,0xffff0000,v72
v_lshlrev_b32 v94,16,v73
v_mfma_f32_16x16x32_bf16 v[120:123],v[24:27],v[164:167],0
v_and_b32 v95,0xffff0000,v73
v_lshlrev_b32 v96,16,v74
M 120,v[24:27],180
v_and_b32 v97,0xffff0000,v74
v_lshlrev_b32 v98,16,v75
v_mfma_f32_16x16x32_bf16 v[124:127],v[26:29],v[168:171],0
v_add_f32_dpp v44,v36,v36 row_ror:8 row_mask:0xf bank_mask:0xf
v_and_b32 v99,0xffff0000,v75
M 124,v[26:29],184
ds_write_b32 v2,v44 offset:0
v_mfma_f32_16x16x32_bf16 v[128:131],v[24:27],v[168:171],0
ds_write_b32 v0,v45 offset:0
M 128,v[24:27],184
v_lshlrev_b32 v100,16,v76
v_and_b32 v101,0xffff0000,v76
v_mfma_f32_16x16x32_bf16 v[132:135],v[26:29],v[172:175],0
v_lshlrev_b32 v102,16,v77
v_and_b32 v103,0xffff0000,v77
M 132,v[26:29],188
L 0
v_lshlrev_b32 v104,16,v78
v_and_b32 v105,0xffff0000,v78
v_mfma_f32_16x16x32_bf16 v[136:139],v[24:27],v[172:175],0
s_barrier
R 52,3,0
R 56,3,16
M 136,v[24:27],188
R 60,3,32
v_lshlrev_b32 v106,16,v79
v_mfma_f32_16x16x32_bf16 v[140:143],v[26:29],v[176:179],0
v_and_b32 v107,0xffff0000,v79
v_lshlrev_b32 v108,16,v80
M 140,v[26:29],192
v_and_b32 v109,0xffff0000,v80
v_lshlrev_b32 v110,16,v81
v_mfma_f32_16x16x32_bf16 v[144:147],v[24:27],v[176:179],0
v_and_b32 v111,0xffff0000,v81
v_lshlrev_b32 v112,16,v82
M 144,v[24:27],192
v_and_b32 v113,0xffff0000,v82
)"
R"(v_lshlrev_b32 v114,16,v83
v_and_b32 v115,0xffff0000,v83
L 0
v_pk_add_f32 v[52:53],v[52:53],v[54:55]
v_pk_add_f32 v[56:57],v[56:57],v[58:59]
v_pk_add_f32 v[60:61],v[60:61],v[62:63]
v_add_f32 v52,v52,v53
v_pk_add_f32 v[56:57],v[56:57],v[60:61]
v_mul_f32 v52,s68,v52
v_add_f32 v53,v56,v57
v_mul_f32 v53,s68,v53
v_fma_f32 v53,-v52,v52,v53
v_max_f32 v53,0,v53
buffer_load_dword v18,v6,s[36:39],s62 offen offset:0 sc1
v_add_f32 v53,s69,v53
v_rsq_f32 v48,v53
s_nop 0
v_mul_f32_e64 v50,-v52,v48
v_mov_b32 v49,v48
v_mov_b32 v51,v50
v_pk_fma_f32 v[52:53],v[116:117],v[48:49],v[50:51]
v_pk_fma_f32 v[54:55],v[118:119],v[48:49],v[50:51]
v_pk_fma_f32 v[56:57],v[120:121],v[48:49],v[50:51]
v_pk_fma_f32 v[58:59],v[122:123],v[48:49],v[50:51]
v_pk_fma_f32 v[116:117],v[52:53],v[84:85],v[116:117]
v_pk_fma_f32 v[118:119],v[54:55],v[86:87],v[118:119]
v_pk_fma_f32 v[120:121],v[56:57],v[88:89],v[120:121]
v_pk_fma_f32 v[122:123],v[58:59],v[90:91],v[122:123]
v_pk_fma_f32 v[52:53],v[124:125],v[48:49],v[50:51]
v_pk_fma_f32 v[54:55],v[126:127],v[48:49],v[50:51]
v_pk_fma_f32 v[56:57],v[128:129],v[48:49],v[50:51]
v_pk_fma_f32 v[58:59],v[130:131],v[48:49],v[50:51]
v_pk_fma_f32 v[124:125],v[52:53],v[92:93],v[124:125]
v_pk_fma_f32 v[126:127],v[54:55],v[94:95],v[126:127]
v_pk_fma_f32 v[128:129],v[56:57],v[96:97],v[128:129]
v_pk_fma_f32 v[130:131],v[58:59],v[98:99],v[130:131]
v_pk_fma_f32 v[52:53],v[132:133],v[48:49],v[50:51]
v_pk_fma_f32 v[54:55],v[134:135],v[48:49],v[50:51]
v_pk_fma_f32 v[56:57],v[136:137],v[48:49],v[50:51]
v_pk_fma_f32 v[58:59],v[138:139],v[48:49],v[50:51]
v_pk_fma_f32 v[132:133],v[52:53],v[100:101],v[132:133]
v_pk_fma_f32 v[134:135],v[54:55],v[102:103],v[134:135]
v_pk_fma_f32 v[136:137],v[56:57],v[104:105],v[136:137]
v_pk_fma_f32 v[138:139],v[58:59],v[106:107],v[138:139]
v_pk_fma_f32 v[52:53],v[140:141],v[48:49],v[50:51]
v_pk_fma_f32 v[54:55],v[142:143],v[48:49],v[50:51]
v_pk_fma_f32 v[56:57],v[144:145],v[48:49],v[50:51]
v_pk_fma_f32 v[58:59],v[146:147],v[48:49],v[50:51]
v_pk_fma_f32 v[140:141],v[52:53],v[108:109],v[140:141]
v_pk_fma_f32 v[142:143],v[54:55],v[110:111],v[142:143]
v_pk_fma_f32 v[144:145],v[56:57],v[112:113],v[144:145]
v_pk_fma_f32 v[146:147],v[58:59],v[114:115],v[146:147]
v_cvt_pk_bf16_f32 v148,v116,v117
v_cvt_pk_bf16_f32 v149,v118,v119
v_cvt_pk_bf16_f32 v150,v120,v121
v_cvt_pk_bf16_f32 v151,v122,v123
v_cvt_pk_bf16_f32 v152,v124,v125
v_cvt_pk_bf16_f32 v153,v126,v127
s_waitcnt vmcnt(13)
buffer_store_dword v17,v5,s[36:39],s63 offen offset:0 sc0 sc1
s_mov_b32 s63,0x7fff0000
v_mfma_f32_16x16x32_bf16 v[36:39],v[32:35],v[148:151],0
v_cvt_pk_bf16_f32 v154,v128,v129
v_cvt_pk_bf16_f32 v155,v130,v131
v_mfma_f32_16x16x32_bf16 v[40:43],v[148:151],v[148:151],0
v_cvt_pk_bf16_f32 v156,v132,v133
v_cvt_pk_bf16_f32 v157,v134,v135
M 36,v[32:35],152
v_cvt_pk_bf16_f32 v158,v136,v137
v_cvt_pk_bf16_f32 v159,v138,v139
M 40,v[152:155],152
v_cvt_pk_bf16_f32 v160,v140,v141
v_cvt_pk_bf16_f32 v161,v142,v143
M 36,v[32:35],156
v_cvt_pk_bf16_f32 v162,v144,v145
v_cvt_pk_bf16_f32 v163,v146,v147
M 40,v[156:159],156
buffer_store_dwordx4 v[148:151],v1,s[36:39],s57 offen offset:0 sc0 sc1
M 36,v[32:35],160
buffer_store_dwordx4 v[152:155],v1,s[36:39],s57 offen offset:2048 sc0 sc1
M 40,v[160:163],160
buffer_store_dwordx4 v[156:159],v1,s[36:39],s58 offen offset:0 sc0 sc1
s_nop 4
v_add_f32_dpp v44,v36,v36 row_ror:8 row_mask:0xf bank_mask:0xf
s_nop 0
v_cndmask_b32 v45,v40,v41,s[70:71]
v_cndmask_b32 v46,v42,v43,s[70:71]
ds_write_b32 v2,v44 offset:1024
v_cndmask_b32 v45,v45,v46,s[72:73]
ds_write_b32 v0,v45 offset:1024
buffer_store_dwordx4 v[160:163],v1,s[36:39],s58 offen offset:2048 sc0 sc1
L 0
s_barrier
R 52,3,1024
R 56,3,1040
R 60,3,1056
L 0
v_pk_add_f32 v[52:53],v[52:53],v[54:55]
v_pk_add_f32 v[56:57],v[56:57],v[58:59]
v_pk_add_f32 v[60:61],v[60:61],v[62:63]
v_add_f32 v52,v52,v53
v_pk_add_f32 v[56:57],v[56:57],v[60:61]
v_mul_f32 v52,s68,v52
v_add_f32 v53,v56,v57
v_mul_f32 v53,s68,v53
v_fma_f32 v53,-v52,v52,v53
v_max_f32 v53,0,v53
v_add_f32 v54,s69,v53
v_fma_f32 v53,s69,v54,v53
v_rsq_f32 v55,v54
v_rsq_f32 v53,v53
v_mul_f32_e64 v54,-v52,v55
v_mul_f32 v53,s76,v53
buffer_store_dwordx4 v[52:55],v4,s[36:39],s59 offen offset:0 sc0 sc1
s_mov_b32 s63,s60
s_lshr_b32 s78,s45,1
s_and_b32 s79,s45,1
s_lshl_b32 s79,s79,7
s_lshl_b32 s57,s78,15
s_add_u32 s57,s57,s79
s_add_u32 s57,s57,12582912
s_add_u32 s58,s57,4096
s_lshl_b32 s59,s78,9
s_add_u32 s59,s59,s79
s_add_u32 s59,s59,107020288
s_lshl_b32 s60,s45,9
s_add_u32 s60,s60,s66
s_add_u32 s60,s60,131072
s_add_u32 s45,s45,s46
s_lshr_b32 s78,s45,1
s_and_b32 s79,s45,1
s_lshl_b32 s79,s79,7
s_lshl_b32 s55,s78,15
s_add_u32 s55,s55,s79
s_mul_i32 s56,s45,16384
s_add_u32 s56,s56,98566144
s_lshl_b32 s61,s78,11
s_add_u32 s61,s61,110587904
s_add_u32 s78,s45,s46
s_lshr_b32 s78,s78,1
s_lshl_b32 s62,s78,11
s_add_u32 s62,s62,110587904
s_waitcnt vmcnt(6)
s_branch Lr_it1_%=
Lr_tb0_%=:
v_add_f32 v53,s69,v53
v_rsq_f32 v48,v53
s_nop 0
v_mul_f32_e64 v50,-v52,v48
v_mov_b32 v49,v48
v_mov_b32 v51,v50
v_pk_fma_f32 v[52:53],v[116:117],v[48:49],v[50:51]
v_pk_fma_f32 v[54:55],v[118:119],v[48:49],v[50:51]
v_pk_fma_f32 v[56:57],v[120:121],v[48:49],v[50:51]
v_pk_fma_f32 v[58:59],v[122:123],v[48:49],v[50:51]
s_add_u32 s78,s55,71303168
buffer_load_dwordx4 v[196:199],v1,s[36:39],s78 offen offset:0 sc1
buffer_load_dwordx4 v[200:203],v1,s[36:39],s78 offen offset:2048 sc1
s_add_u32 s78,s55,71307264
buffer_load_dwordx4 v[204:207],v1,s[36:39],s78 offen offset:0 sc1
buffer_load_dwordx4 v[208:211],v1,s[36:39],s78 offen offset:2048 sc1
v_pk_fma_f32 v[116:117],v[52:53],v[84:85],v[116:117]
v_pk_fma_f32 v[118:119],v[54:55],v[86:87],v[118:119]
v_pk_fma_f32 v[120:121],v[56:57],v[88:89],v[120:121]
v_pk_fma_f32 v[122:123],v[58:59],v[90:91],v[122:123]
v_pk_fma_f32 v[52:53],v[124:125],v[48:49],v[50:51]
v_pk_fma_f32 v[54:55],v[126:127],v[48:49],v[50:51]
v_pk_fma_f32 v[56:57],v[128:129],v[48:49],v[50:51]
v_pk_fma_f32 v[58:59],v[130:131],v[48:49],v[50:51]
s_add_u32 s78,s55,89128960
buffer_load_dwordx4 v[212:215],v1,s[36:39],s78 offen offset:0 sc1
buffer_load_dwordx4 v[216:219],v1,s[36:39],s78 offen offset:2048 sc1
s_add_u32 s78,s55,89133056
buffer_load_dwordx4 v[220:223],v1,s[36:39],s78 offen offset:0 sc1
buffer_load_dwordx4 v[224:227],v1,s[36:39],s78 offen offset:2048 sc1
v_pk_fma_f32 v[124:125],v[52:53],v[92:93],v[124:125]
v_pk_fma_f32 v[126:127],v[54:55],v[94:95],v[126:127]
v_pk_fma_f32 v[128:129],v[56:57],v[96:97],v[128:129]
v_pk_fma_f32 v[130:131],v[58:59],v[98:99],v[130:131]
v_pk_fma_f32 v[52:53],v[132:133],v[48:49],v[50:51]
v_pk_fma_f32 v[54:55],v[134:135],v[48:49],v[50:51]
v_pk_fma_f32 v[56:57],v[136:137],v[48:49],v[50:51]
v_pk_fma_f32 v[58:59],v[138:139],v[48:49],v[50:51]
v_pk_fma_f32 v[132:133],v[52:53],v[100:101],v[132:133]
v_pk_fma_f32 v[134:135],v[54:55],v[102:103],v[134:135]
v_pk_fma_f32 v[136:137],v[56:57],v[104:105],v[136:137]
v_pk_fma_f32 v[138:139],v[58:59],v[106:107],v[138:139]
v_pk_fma_f32 v[52:53],v[140:141],v[48:49],v[50:51]
v_pk_fma_f32 v[54:55],v[142:143],v[48:49],v[50:51]
v_pk_fma_f32 v[56:57],v[144:145],v[48:49],v[50:51]
v_pk_fma_f32 v[58:59],v[146:147],v[48:49],v[50:51]
v_pk_fma_f32 v[140:141],v[52:53],v[108:109],v[140:141]
v_pk_fma_f32 v[142:143],v[54:55],v[110:111],v[142:143]
v_pk_fma_f32 v[144:145],v[56:57],v[112:113],v[144:145]
v_pk_fma_f32 v[146:147],v[58:59],v[114:115],v[146:147]
buffer_load_dword v18,v6,s[36:39],s62 offen offset:0 sc1
v_cvt_pk_bf16_f32 v148,v116,v117
v_cvt_pk_bf16_f32 v149,v118,v119
v_cvt_pk_bf16_f32 v150,v120,v121
v_cvt_pk_bf16_f32 v151,v122,v123
v_cvt_pk_bf16_f32 v152,v124,v125
v_cvt_pk_bf16_f32 v153,v126,v127
v_mfma_f32_16x16x32_bf16 v[36:39],v[32:35],v[148:151],0
v_cvt_pk_bf16_f32 v154,v128,v129
v_cvt_pk_bf16_f32 v155,v130,v131
v_mfma_f32_16x16x32_bf16 v[40:43],v[148:151],v[148:151],0
v_cvt_pk_bf16_f32 v156,v132,v133
v_cvt_pk_bf16_f32 v157,v134,v135
M 36,v[32:35],152
v_cvt_pk_bf16_f32 v158,v136,v137
v_cvt_pk_bf16_f32 v159,v138,v139
M 40,v[152:155],152
v_cvt_pk_bf16_f32 v160,v140,v141
v_cvt_pk_bf16_f32 v161,v142,v143
M 36,v[32:35],156
v_cvt_pk_bf16_f32 v162,v144,v145
v_cvt_pk_bf16_f32 v163,v146,v147
M 40,v[156:159],156
buffer_store_dwordx4 v[148:151],v1,s[36:39],s57 offen offset:0 sc0 sc1
M 36,v[32:35],160
buffer_store_dwordx4 v[152:155],v1,s[36:39],s57 offen offset:2048 sc0 sc1
M 40,v[160:163],160
buffer_store_dwordx4 v[156:159],v1,s[36:39],s58 offen offset:0 sc0 sc1
s_nop 4
v_add_f32_dpp v44,v36,v36 row_ror:8 row_mask:0xf bank_mask:0xf
s_nop 0
v_cndmask_b32 v45,v40,v41,s[70:71]
v_cndmask_b32 v46,v42,v43,s[70:71]
ds_write_b32 v2,v44 offset:1024
v_cndmask_b32 v45,v45,v46,s[72:73]
ds_write_b32 v0,v45 offset:1024
buffer_store_dwordx4 v[160:163],v1,s[36:39],s58 offen offset:2048 sc0 sc1
L 0
s_barrier
R 52,3,1024
R 56,3,1040
R 60,3,1056
L 0
v_pk_add_f32 v[52:53],v[52:53],v[54:55]
v_pk_add_f32 v[56:57],v[56:57],v[58:59]
v_pk_add_f32 v[60:61],v[60:61],v[62:63]
v_add_f32 v52,v52,v53
v_pk_add_f32 v[56:57],v[56:57],v[60:61]
v_mul_f32 v52,s68,v52
v_add_f32 v53,v56,v57
v_mul_f32 v53,s68,v53
v_fma_f32 v53,-v52,v52,v53
v_max_f32 v53,0,v53
v_add_f32 v54,s69,v53
v_fma_f32 v53,s69,v54,v53
v_rsq_f32 v55,v54
v_rsq_f32 v53,v53
v_mul_f32_e64 v54,-v52,v55
v_mul_f32 v53,s76,v53
buffer_store_dwordx4 v[52:55],v4,s[36:39],s59 offen offset:0 sc0 sc1
s_mov_b32 s63,s60
s_lshr_b32 s78,s45,1
s_and_b32 s79,s45,1
s_lshl_b32 s79,s79,7
s_lshl_b32 s57,s78,15
s_add_u32 s57,s57,s79
s_add_u32 s57,s57,12582912
s_add_u32 s58,s57,4096
s_lshl_b32 s59,s78,9
s_add_u32 s59,s59,s79
s_add_u32 s59,s59,107020288
s_lshl_b32 s60,s45,9
s_add_u32 s60,s60,s66
s_add_u32 s60,s60,131072
s_add_u32 s45,s45,s46
s_lshr_b32 s78,s45,1
s_and_b32 s79,s45,1
s_lshl_b32 s79,s79,7
s_lshl_b32 s55,s78,15
s_add_u32 s55,s55,s79
s_mul_i32 s56,s45,16384
s_add_u32 s56,s56,98566144
s_lshl_b32 s61,s78,11
s_add_u32 s61,s61,110587904
s_add_u32 s78,s45,s46
s_lshr_b32 s78,s78,1
s_lshl_b32 s62,s78,11
s_add_u32 s62,s62,110587904
s_waitcnt vmcnt(5)
s_branch Lr_it1_%=
Lr_tc0_%=:
s_add_u32 s78,s55,71303168
buffer_load_dwordx4 v[196:199],v1,s[36:39],s78 offen offset:0 sc1
buffer_load_dwordx4 v[200:203],v1,s[36:39],s78 offen offset:2048 sc1
s_add_u32 s78,s55,71307264
buffer_load_dwordx4 v[204:207],v1,s[36:39],s78 offen offset:0 sc1
buffer_load_dwordx4 v[208:211],v1,s[36:39],s78 offen offset:2048 sc1
s_add_u32 s78,s55,89128960
buffer_load_dwordx4 v[212:215],v1,s[36:39],s78 offen offset:0 sc1
buffer_load_dwordx4 v[216:219],v1,s[36:39],s78 offen offset:2048 sc1
s_add_u32 s78,s55,89133056
buffer_load_dwordx4 v[220:223],v1,s[36:39],s78 offen offset:0 sc1
buffer_load_dwordx4 v[224:227],v1,s[36:39],s78 offen offset:2048 sc1
buffer_load_dword v18,v6,s[36:39],s62 offen offset:0 sc1
s_waitcnt vmcnt(9)
buffer_store_dword v17,v5,s[36:39],s63 offen offset:0 sc0 sc1
s_mov_b32 s63,0x7fff0000
s_lshr_b32 s78,s45,1
s_and_b32 s79,s45,1
s_lshl_b32 s79,s79,7
s_lshl_b32 s57,s78,15
s_add_u32 s57,s57,s79
s_add_u32 s57,s57,12582912
s_add_u32 s58,s57,4096
s_lshl_b32 s59,s78,9
s_add_u32 s59,s59,s79
s_add_u32 s59,s59,107020288
s_lshl_b32 s60,s45,9
s_add_u32 s60,s60,s66
s_add_u32 s60,s60,131072
s_add_u32 s45,s45,s46
s_lshr_b32 s78,s45,1
s_and_b32 s79,s45,1
s_lshl_b32 s79,s79,7
s_lshl_b32 s55,s78,15
s_add_u32 s55,s55,s79
s_mul_i32 s56,s45,16384
s_add_u32 s56,s56,98566144
s_lshl_b32 s61,s78,11
s_add_u32 s61,s61,110587904
s_add_u32 s78,s45,s46
s_lshr_b32 s78,s78,1
s_lshl_b32 s62,s78,11
s_add_u32 s62,s62,110587904
s_waitcnt vmcnt(1)
s_branch Lr_it1_%=
Lr_it1_%=:
T v[68:69],10,16384
T v[70:71],10,16448
T v[72:73],10,17408
T v[74:75],10,17472
T v[76:77],10,18432
T v[78:79],10,18496
T v[80:81],10,19456
T v[82:83],10,19520
s_nop 0
v_cmp_eq_u32 vcc,s53,v18
s_nop 0
s_cmp_eq_u64 vcc,-1
s_cselect_b32 s78,s45,256
s_cmp_lt_u32 s78,256
s_cbranch_scc1 Lr_ta1_%=
buffer_load_dword v18,v6,s[36:39],s61 offen offset:0 sc1
v_mfma_f32_16x16x32_bf16 v[148:151],v[196:199],v[212:215],0
s_add_u32 m0,s65,0
M 148,v[200:203],216
D 9,56,0
M 148,v[204:207],220
D 9,56,1024
M 148,v[208:211],224
L 4
D 9,56,2048
v_mfma_f32_16x16x32_bf16 v[40:43],v[196:199],v[196:199],0
D 9,56,3072
M 40,v[200:203],200
L 0
v_lshlrev_b32 v84,16,v68
v_and_b32 v85,0xffff0000,v68
M 40,v[204:207],204
v_lshlrev_b32 v86,16,v69
v_and_b32 v87,0xffff0000,v69
M 40,v[208:211],208
v_lshlrev_b32 v88,16,v70
v_and_b32 v89,0xffff0000,v70
M 40,v[212:215],212
v_lshlrev_b32 v90,16,v71
v_and_b32 v91,0xffff0000,v71
M 40,v[216:219],216
v_lshlrev_b32 v92,16,v72
v_and_b32 v93,0xffff0000,v72
M 40,v[220:223],220
v_lshlrev_b32 v94,16,v73
v_and_b32 v95,0xffff0000,v73
M 40,v[224:227],224
v_lshlrev_b32 v96,16,v74
v_and_b32 v97,0xffff0000,v74
v_mfma_f32_16x16x32_bf16 v[36:39],v[32:35],v[196:199],0
v_lshlrev_b32 v98,16,v75
v_and_b32 v99,0xffff0000,v75
M 36,v[32:35],200
v_lshlrev_b32 v100,16,v76
v_and_b32 v101,0xffff0000,v76
M 36,v[32:35],204
v_lshlrev_b32 v102,16,v77
v_and_b32 v103,0xffff0000,v77
M 36,v[32:35],208
v_lshlrev_b32 v104,16,v78
v_and_b32 v105,0xffff0000,v78
M 36,v[32:35],212
v_fma_f32 v40,2.0,v148,v40
v_fma_f32 v41,2.0,v149,v41
M 36,v[32:35],216
v_fma_f32 v42,2.0,v150,v42
v_fma_f32 v43,2.0,v151,v43
M 36,v[32:35],220
v_cndmask_b32 v45,v40,v41,s[70:71]
v_cndmask_b32 v46,v42,v43,s[70:71]
M 36,v[32:35],224
v_cndmask_b32 v45,v45,v46,s[72:73]
v_lshlrev_b32 v106,16,v79
v_mfma_f32_16x16x32_bf16 v[116:119],v[26:29],v[196:199],0
v_and_b32 v107,0xffff0000,v79
v_lshlrev_b32 v108,16,v80
M 116,v[26:29],212
v_and_b32 v109,0xffff0000,v80
v_lshlrev_b32 v110,16,v81
v_mfma_f32_16x16x32_bf16 v[120:123],v[24:27],v[196:199],0
v_and_b32 v111,0xffff0000,v81
v_lshlrev_b32 v112,16,v82
M 120,v[24:27],212
v_and_b32 v113,0xffff0000,v82
v_lshlrev_b32 v114,16,v83
v_mfma_f32_16x16x32_bf16 v[124:127],v[26:29],v[200:203],0
v_add_f32_dpp v44,v36,v36 row_ror:8 row_mask:0xf bank_mask:0xf
v_and_b32 v115,0xffff0000,v83
M 124,v[26:29],216
ds_write_b32 v2,v44 offset:0
v_mfma_f32_16x16x32_bf16 v[128:131],v[24:27],v[200:203],0
ds_write_b32 v0,v45 offset:0
M 128,v[24:27],216
v_mfma_f32_16x16x32_bf16 v[132:135],v[26:29],v[204:207],0
M 132,v[26:29],220
L 0
v_mfma_f32_16x16x32_bf16 v[136:139],v[24:27],v[204:207],0
s_barrier
R 52,3,0
R 56,3,16
M 136,v[24:27],220
R 60,3,32
v_mfma_f32_16x16x32_bf16 v[140:143],v[26:29],v[208:211],0
M 140,v[26:29],224
v_mfma_f32_16x16x32_bf16 v[144:147],v[24:27],v[208:211],0
M 144,v[24:27],224
L 0
v_pk_add_f32 v[52:53],v[52:53],v[54:55]
v_pk_add_f32 v[56:57],v[56:57],v[58:59]
v_pk_add_f32 v[60:61],v[60:61],v[62:63]
v_add_f32 v52,v52,v53
v_pk_add_f32 v[56:57],v[56:57],v[60:61]
v_mul_f32 v52,s68,v52
v_add_f32 v53,v56,v57
v_mul_f32 v53,s68,v53
v_fma_f32 v53,-v52,v52,v53
v_max_f32 v53,0,v53
s_waitcnt vmcnt(5)
buffer_store_dword v17,v5,s[36:39],s63 offen offset:0 sc0 sc1
s_mov_b32 s63,0x7fff0000
s_waitcnt vmcnt(5)
v_cmp_eq_u32 vcc,s53,v18
s_nop 0
s_cmp_eq_u64 vcc,-1
s_cselect_b32 s78,s45,256
s_cmp_lt_u32 s78,256
s_cbranch_scc1 Lr_tb1_%=
buffer_load_dword v18,v6,s[36:39],s61 offen offset:0 sc1
v_add_f32 v53,s69,v53
v_rsq_f32 v48,v53
s_nop 0
v_mul_f32_e64 v50,-v52,v48
v_mov_b32 v49,v48
v_mov_b32 v51,v50
v_pk_fma_f32 v[52:53],v[116:117],v[48:49],v[50:51]
v_pk_fma_f32 v[54:55],v[118:119],v[48:49],v[50:51]
v_pk_fma_f32 v[56:57],v[120:121],v[48:49],v[50:51]
v_pk_fma_f32 v[58:59],v[122:123],v[48:49],v[50:51]
v_pk_fma_f32 v[116:117],v[52:53],v[84:85],v[116:117]
v_pk_fma_f32 v[118:119],v[54:55],v[86:87],v[118:119]
v_pk_fma_f32 v[120:121],v[56:57],v[88:89],v[120:121]
v_pk_fma_f32 v[122:123],v[58:59],v[90:91],v[122:123]
v_pk_fma_f32 v[52:53],v[124:125],v[48:49],v[50:51]
v_pk_fma_f32 v[54:55],v[126:127],v[48:49],v[50:51]
v_pk_fma_f32 v[56:57],v[128:129],v[48:49],v[50:51]
v_pk_fma_f32 v[58:59],v[130:131],v[48:49],v[50:51]
v_pk_fma_f32 v[124:125],v[52:53],v[92:93],v[124:125]
v_pk_fma_f32 v[126:127],v[54:55],v[94:95],v[126:127]
v_pk_fma_f32 v[128:129],v[56:57],v[96:97],v[128:129]
v_pk_fma_f32 v[130:131],v[58:59],v[98:99],v[130:131]
v_pk_fma_f32 v[52:53],v[132:133],v[48:49],v[50:51]
v_pk_fma_f32 v[54:55],v[134:135],v[48:49],v[50:51]
v_pk_fma_f32 v[56:57],v[136:137],v[48:49],v[50:51]
v_pk_fma_f32 v[58:59],v[138:139],v[48:49],v[50:51]
v_pk_fma_f32 v[132:133],v[52:53],v[100:101],v[132:133]
v_pk_fma_f32 v[134:135],v[54:55],v[102:103],v[134:135]
v_pk_fma_f32 v[136:137],v[56:57],v[104:105],v[136:137]
v_pk_fma_f32 v[138:139],v[58:59],v[106:107],v[138:139]
v_pk_fma_f32 v[52:53],v[140:141],v[48:49],v[50:51]
v_pk_fma_f32 v[54:55],v[142:143],v[48:49],v[50:51]
v_pk_fma_f32 v[56:57],v[144:145],v[48:49],v[50:51]
v_pk_fma_f32 v[58:59],v[146:147],v[48:49],v[50:51]
v_pk_fma_f32 v[140:141],v[52:53],v[108:109],v[140:141]
v_pk_fma_f32 v[142:143],v[54:55],v[110:111],v[142:143]
v_pk_fma_f32 v[144:145],v[56:57],v[112:113],v[144:145]
v_pk_fma_f32 v[146:147],v[58:59],v[114:115],v[146:147]
v_cvt_pk_bf16_f32 v148,v116,v117
v_cvt_pk_bf16_f32 v149,v118,v119
)"
R"(v_cvt_pk_bf16_f32 v150,v120,v121
v_cvt_pk_bf16_f32 v151,v122,v123
v_cvt_pk_bf16_f32 v152,v124,v125
v_cvt_pk_bf16_f32 v153,v126,v127
v_mfma_f32_16x16x32_bf16 v[36:39],v[32:35],v[148:151],0
v_cvt_pk_bf16_f32 v154,v128,v129
v_cvt_pk_bf16_f32 v155,v130,v131
v_mfma_f32_16x16x32_bf16 v[40:43],v[148:151],v[148:151],0
v_cvt_pk_bf16_f32 v156,v132,v133
v_cvt_pk_bf16_f32 v157,v134,v135
M 36,v[32:35],152
v_cvt_pk_bf16_f32 v158,v136,v137
v_cvt_pk_bf16_f32 v159,v138,v139
M 40,v[152:155],152
v_cvt_pk_bf16_f32 v160,v140,v141
v_cvt_pk_bf16_f32 v161,v142,v143
M 36,v[32:35],156
v_cvt_pk_bf16_f32 v162,v144,v145
v_cvt_pk_bf16_f32 v163,v146,v147
M 40,v[156:159],156
buffer_store_dwordx4 v[148:151],v1,s[36:39],s57 offen offset:0 sc0 sc1
M 36,v[32:35],160
buffer_store_dwordx4 v[152:155],v1,s[36:39],s57 offen offset:2048 sc0 sc1
M 40,v[160:163],160
buffer_store_dwordx4 v[156:159],v1,s[36:39],s58 offen offset:0 sc0 sc1
s_nop 4
v_add_f32_dpp v44,v36,v36 row_ror:8 row_mask:0xf bank_mask:0xf
s_nop 0
v_cndmask_b32 v45,v40,v41,s[70:71]
v_cndmask_b32 v46,v42,v43,s[70:71]
ds_write_b32 v2,v44 offset:1024
v_cndmask_b32 v45,v45,v46,s[72:73]
ds_write_b32 v0,v45 offset:1024
buffer_store_dwordx4 v[160:163],v1,s[36:39],s58 offen offset:2048 sc0 sc1
L 0
s_barrier
R 52,3,1024
R 56,3,1040
R 60,3,1056
L 0
v_pk_add_f32 v[52:53],v[52:53],v[54:55]
v_pk_add_f32 v[56:57],v[56:57],v[58:59]
v_pk_add_f32 v[60:61],v[60:61],v[62:63]
v_add_f32 v52,v52,v53
v_pk_add_f32 v[56:57],v[56:57],v[60:61]
v_mul_f32 v52,s68,v52
v_add_f32 v53,v56,v57
v_mul_f32 v53,s68,v53
v_fma_f32 v53,-v52,v52,v53
v_max_f32 v53,0,v53
v_add_f32 v54,s69,v53
v_fma_f32 v53,s69,v54,v53
v_rsq_f32 v55,v54
v_rsq_f32 v53,v53
v_mul_f32_e64 v54,-v52,v55
v_mul_f32 v53,s76,v53
buffer_store_dwordx4 v[52:55],v4,s[36:39],s59 offen offset:0 sc0 sc1
s_mov_b32 s63,s60
s_waitcnt vmcnt(5)
v_cmp_eq_u32 vcc,s53,v18
s_nop 0
s_cmp_eq_u64 vcc,-1
s_cselect_b32 s78,s45,256
s_cmp_lt_u32 s78,256
s_cbranch_scc1 Lr_tc1_%=
s_waitcnt vmcnt(0)
buffer_store_dword v17,v5,s[36:39],s63 offen offset:0 sc0 sc1
s_mov_b32 s63,0x7fff0000
s_cmp_ge_u32 s45,256
s_cbranch_scc1 Lr_done_%=
Lr_bp1_%=:
buffer_load_dword v18,v6,s[36:39],s61 offen offset:0 sc1
s_add_u32 s82,s82,1
s_waitcnt vmcnt(0)
v_cmp_eq_u32 vcc,s53,v18
s_nop 0
s_cmp_eq_u64 vcc,-1
s_cbranch_scc1 Lr_bq1_%=
s_sleep 2
s_cmp_lt_u32 s82,20000
s_cbranch_scc1 Lr_bp1_%=
Lr_bq1_%=:
s_add_u32 s78,s55,71303168
buffer_load_dwordx4 v[164:167],v1,s[36:39],s78 offen offset:0 sc1
buffer_load_dwordx4 v[168:171],v1,s[36:39],s78 offen offset:2048 sc1
s_add_u32 s78,s55,71307264
buffer_load_dwordx4 v[172:175],v1,s[36:39],s78 offen offset:0 sc1
buffer_load_dwordx4 v[176:179],v1,s[36:39],s78 offen offset:2048 sc1
s_add_u32 s78,s55,89128960
buffer_load_dwordx4 v[180:183],v1,s[36:39],s78 offen offset:0 sc1
buffer_load_dwordx4 v[184:187],v1,s[36:39],s78 offen offset:2048 sc1
s_add_u32 s78,s55,89133056
buffer_load_dwordx4 v[188:191],v1,s[36:39],s78 offen offset:0 sc1
buffer_load_dwordx4 v[192:195],v1,s[36:39],s78 offen offset:2048 sc1
buffer_load_dword v18,v6,s[36:39],s62 offen offset:0 sc1
s_lshr_b32 s78,s45,1
s_and_b32 s79,s45,1
s_lshl_b32 s79,s79,7
s_lshl_b32 s57,s78,15
s_add_u32 s57,s57,s79
s_add_u32 s57,s57,12582912
s_add_u32 s58,s57,4096
s_lshl_b32 s59,s78,9
s_add_u32 s59,s59,s79
s_add_u32 s59,s59,107020288
s_lshl_b32 s60,s45,9
s_add_u32 s60,s60,s66
s_add_u32 s60,s60,131072
s_add_u32 s45,s45,s46
s_lshr_b32 s78,s45,1
s_and_b32 s79,s45,1
s_lshl_b32 s79,s79,7
s_lshl_b32 s55,s78,15
s_add_u32 s55,s55,s79
s_mul_i32 s56,s45,16384
s_add_u32 s56,s56,98566144
s_lshl_b32 s61,s78,11
s_add_u32 s61,s61,110587904
s_add_u32 s78,s45,s46
s_lshr_b32 s78,s78,1
s_lshl_b32 s62,s78,11
s_add_u32 s62,s62,110587904
s_waitcnt vmcnt(0)
s_branch Lr_it0_%=
Lr_ta1_%=:
v_mfma_f32_16x16x32_bf16 v[148:151],v[196:199],v[212:215],0
s_add_u32 s78,s55,71303168
s_add_u32 m0,s65,0
M 148,v[200:203],216
buffer_load_dwordx4 v[164:167],v1,s[36:39],s78 offen offset:0 sc1
M 148,v[204:207],220
buffer_load_dwordx4 v[168:171],v1,s[36:39],s78 offen offset:2048 sc1
M 148,v[208:211],224
L 4
D 9,56,0
v_mfma_f32_16x16x32_bf16 v[40:43],v[196:199],v[196:199],0
D 9,56,1024
M 40,v[200:203],200
L 0
D 9,56,2048
M 40,v[204:207],204
D 9,56,3072
M 40,v[208:211],208
s_add_u32 s78,s55,71307264
v_lshlrev_b32 v84,16,v68
v_and_b32 v85,0xffff0000,v68
M 40,v[212:215],212
buffer_load_dwordx4 v[172:175],v1,s[36:39],s78 offen offset:0 sc1
M 40,v[216:219],216
buffer_load_dwordx4 v[176:179],v1,s[36:39],s78 offen offset:2048 sc1
M 40,v[220:223],220
s_add_u32 s78,s55,89128960
v_lshlrev_b32 v86,16,v69
v_and_b32 v87,0xffff0000,v69
M 40,v[224:227],224
buffer_load_dwordx4 v[180:183],v1,s[36:39],s78 offen offset:0 sc1
v_mfma_f32_16x16x32_bf16 v[36:39],v[32:35],v[196:199],0
buffer_load_dwordx4 v[184:187],v1,s[36:39],s78 offen offset:2048 sc1
M 36,v[32:35],200
s_add_u32 s78,s55,89133056
v_lshlrev_b32 v88,16,v70
v_and_b32 v89,0xffff0000,v70
M 36,v[32:35],204
buffer_load_dwordx4 v[188:191],v1,s[36:39],s78 offen offset:0 sc1
M 36,v[32:35],208
buffer_load_dwordx4 v[192:195],v1,s[36:39],s78 offen offset:2048 sc1
M 36,v[32:35],212
v_fma_f32 v40,2.0,v148,v40
v_fma_f32 v41,2.0,v149,v41
M 36,v[32:35],216
v_fma_f32 v42,2.0,v150,v42
v_fma_f32 v43,2.0,v151,v43
M 36,v[32:35],220
v_cndmask_b32 v45,v40,v41,s[70:71]
v_cndmask_b32 v46,v42,v43,s[70:71]
M 36,v[32:35],224
v_cndmask_b32 v45,v45,v46,s[72:73]
v_lshlrev_b32 v90,16,v71
v_mfma_f32_16x16x32_bf16 v[116:119],v[26:29],v[196:199],0
v_and_b32 v91,0xffff0000,v71
v_lshlrev_b32 v92,16,v72
M 116,v[26:29],212
v_and_b32 v93,0xffff0000,v72
v_lshlrev_b32 v94,16,v73
v_mfma_f32_16x16x32_bf16 v[120:123],v[24:27],v[196:199],0
v_and_b32 v95,0xffff0000,v73
v_lshlrev_b32 v96,16,v74
M 120,v[24:27],212
v_and_b32 v97,0xffff0000,v74
v_lshlrev_b32 v98,16,v75
v_mfma_f32_16x16x32_bf16 v[124:127],v[26:29],v[200:203],0
v_add_f32_dpp v44,v36,v36 row_ror:8 row_mask:0xf bank_mask:0xf
v_and_b32 v99,0xffff0000,v75
M 124,v[26:29],216
ds_write_b32 v2,v44 offset:0
v_mfma_f32_16x16x32_bf16 v[128:131],v[24:27],v[200:203],0
ds_write_b32 v0,v45 offset:0
M 128,v[24:27],216
v_lshlrev_b32 v100,16,v76
v_and_b32 v101,0xffff0000,v76
v_mfma_f32_16x16x32_bf16 v[132:135],v[26:29],v[204:207],0
v_lshlrev_b32 v102,16,v77
v_and_b32 v103,0xffff0000,v77
M 132,v[26:29],220
L 0
v_lshlrev_b32 v104,16,v78
v_and_b32 v105,0xffff0000,v78
v_mfma_f32_16x16x32_bf16 v[136:139],v[24:27],v[204:207],0
s_barrier
R 52,3,0
R 56,3,16
M 136,v[24:27],220
R 60,3,32
v_lshlrev_b32 v106,16,v79
v_mfma_f32_16x16x32_bf16 v[140:143],v[26:29],v[208:211],0
v_and_b32 v107,0xffff0000,v79
v_lshlrev_b32 v108,16,v80
M 140,v[26:29],224
v_and_b32 v109,0xffff0000,v80
v_lshlrev_b32 v110,16,v81
v_mfma_f32_16x16x32_bf16 v[144:147],v[24:27],v[208:211],0
v_and_b32 v111,0xffff0000,v81
v_lshlrev_b32 v112,16,v82
M 144,v[24:27],224
v_and_b32 v113,0xffff0000,v82
v_lshlrev_b32 v114,16,v83
v_and_b32 v115,0xffff0000,v83
L 0
v_pk_add_f32 v[52:53],v[52:53],v[54:55]
v_pk_add_f32 v[56:57],v[56:57],v[58:59]
v_pk_add_f32 v[60:61],v[60:61],v[62:63]
v_add_f32 v52,v52,v53
v_pk_add_f32 v[56:57],v[56:57],v[60:61]
v_mul_f32 v52,s68,v52
v_add_f32 v53,v56,v57
v_mul_f32 v53,s68,v53
v_fma_f32 v53,-v52,v52,v53
v_max_f32 v53,0,v53
buffer_load_dword v18,v6,s[36:39],s62 offen offset:0 sc1
v_add_f32 v53,s69,v53
v_rsq_f32 v48,v53
s_nop 0
v_mul_f32_e64 v50,-v52,v48
v_mov_b32 v49,v48
v_mov_b32 v51,v50
v_pk_fma_f32 v[52:53],v[116:117],v[48:49],v[50:51]
v_pk_fma_f32 v[54:55],v[118:119],v[48:49],v[50:51]
v_pk_fma_f32 v[56:57],v[120:121],v[48:49],v[50:51]
v_pk_fma_f32 v[58:59],v[122:123],v[48:49],v[50:51]
v_pk_fma_f32 v[116:117],v[52:53],v[84:85],v[116:117]
v_pk_fma_f32 v[118:119],v[54:55],v[86:87],v[118:119]
v_pk_fma_f32 v[120:121],v[56:57],v[88:89],v[120:121]
v_pk_fma_f32 v[122:123],v[58:59],v[90:91],v[122:123]
v_pk_fma_f32 v[52:53],v[124:125],v[48:49],v[50:51]
v_pk_fma_f32 v[54:55],v[126:127],v[48:49],v[50:51]
v_pk_fma_f32 v[56:57],v[128:129],v[48:49],v[50:51]
v_pk_fma_f32 v[58:59],v[130:131],v[48:49],v[50:51]
v_pk_fma_f32 v[124:125],v[52:53],v[92:93],v[124:125]
v_pk_fma_f32 v[126:127],v[54:55],v[94:95],v[126:127]
v_pk_fma_f32 v[128:129],v[56:57],v[96:97],v[128:129]
v_pk_fma_f32 v[130:131],v[58:59],v[98:99],v[130:131]
v_pk_fma_f32 v[52:53],v[132:133],v[48:49],v[50:51]
v_pk_fma_f32 v[54:55],v[134:135],v[48:49],v[50:51]
v_pk_fma_f32 v[56:57],v[136:137],v[48:49],v[50:51]
v_pk_fma_f32 v[58:59],v[138:139],v[48:49],v[50:51]
v_pk_fma_f32 v[132:133],v[52:53],v[100:101],v[132:133]
v_pk_fma_f32 v[134:135],v[54:55],v[102:103],v[134:135]
v_pk_fma_f32 v[136:137],v[56:57],v[104:105],v[136:137]
v_pk_fma_f32 v[138:139],v[58:59],v[106:107],v[138:139]
v_pk_fma_f32 v[52:53],v[140:141],v[48:49],v[50:51]
v_pk_fma_f32 v[54:55],v[142:143],v[48:49],v[50:51]
v_pk_fma_f32 v[56:57],v[144:145],v[48:49],v[50:51]
v_pk_fma_f32 v[58:59],v[146:147],v[48:49],v[50:51]
v_pk_fma_f32 v[140:141],v[52:53],v[108:109],v[140:141]
v_pk_fma_f32 v[142:143],v[54:55],v[110:111],v[142:143]
v_pk_fma_f32 v[144:145],v[56:57],v[112:113],v[144:145]
v_pk_fma_f32 v[146:147],v[58:59],v[114:115],v[146:147]
v_cvt_pk_bf16_f32 v148,v116,v117
v_cvt_pk_bf16_f32 v149,v118,v119
v_cvt_pk_bf16_f32 v150,v120,v121
v_cvt_pk_bf16_f32 v151,v122,v123
v_cvt_pk_bf16_f32 v152,v124,v125
v_cvt_pk_bf16_f32 v153,v126,v127
s_waitcnt vmcnt(13)
buffer_store_dword v17,v5,s[36:39],s63 offen offset:0 sc0 sc1
s_mov_b32 s63,0x7fff0000
v_mfma_f32_16x16x32_bf16 v[36:39],v[32:35],v[148:151],0
v_cvt_pk_bf16_f32 v154,v128,v129
v_cvt_pk_bf16_f32 v155,v130,v131
v_mfma_f32_16x16x32_bf16 v[40:43],v[148:151],v[148:151],0
v_cvt_pk_bf16_f32 v156,v132,v133
v_cvt_pk_bf16_f32 v157,v134,v135
M 36,v[32:35],152
v_cvt_pk_bf16_f32 v158,v136,v137
v_cvt_pk_bf16_f32 v159,v138,v139
M 40,v[152:155],152
v_cvt_pk_bf16_f32 v160,v140,v141
v_cvt_pk_bf16_f32 v161,v142,v143
M 36,v[32:35],156
v_cvt_pk_bf16_f32 v162,v144,v145
v_cvt_pk_bf16_f32 v163,v146,v147
M 40,v[156:159],156
buffer_store_dwordx4 v[148:151],v1,s[36:39],s57 offen offset:0 sc0 sc1
M 36,v[32:35],160
buffer_store_dwordx4 v[152:155],v1,s[36:39],s57 offen offset:2048 sc0 sc1
M 40,v[160:163],160
buffer_store_dwordx4 v[156:159],v1,s[36:39],s58 offen offset:0 sc0 sc1
s_nop 4
v_add_f32_dpp v44,v36,v36 row_ror:8 row_mask:0xf bank_mask:0xf
s_nop 0
v_cndmask_b32 v45,v40,v41,s[70:71]
v_cndmask_b32 v46,v42,v43,s[70:71]
ds_write_b32 v2,v44 offset:1024
v_cndmask_b32 v45,v45,v46,s[72:73]
ds_write_b32 v0,v45 offset:1024
buffer_store_dwordx4 v[160:163],v1,s[36:39],s58 offen offset:2048 sc0 sc1
L 0
s_barrier
R 52,3,1024
R 56,3,1040
R 60,3,1056
L 0
v_pk_add_f32 v[52:53],v[52:53],v[54:55]
v_pk_add_f32 v[56:57],v[56:57],v[58:59]
v_pk_add_f32 v[60:61],v[60:61],v[62:63]
v_add_f32 v52,v52,v53
v_pk_add_f32 v[56:57],v[56:57],v[60:61]
v_mul_f32 v52,s68,v52
v_add_f32 v53,v56,v57
v_mul_f32 v53,s68,v53
v_fma_f32 v53,-v52,v52,v53
v_max_f32 v53,0,v53
v_add_f32 v54,s69,v53
v_fma_f32 v53,s69,v54,v53
v_rsq_f32 v55,v54
v_rsq_f32 v53,v53
v_mul_f32_e64 v54,-v52,v55
v_mul_f32 v53,s76,v53
buffer_store_dwordx4 v[52:55],v4,s[36:39],s59 offen offset:0 sc0 sc1
s_mov_b32 s63,s60
s_lshr_b32 s78,s45,1
s_and_b32 s79,s45,1
s_lshl_b32 s79,s79,7
s_lshl_b32 s57,s78,15
s_add_u32 s57,s57,s79
s_add_u32 s57,s57,12582912
s_add_u32 s58,s57,4096
s_lshl_b32 s59,s78,9
s_add_u32 s59,s59,s79
s_add_u32 s59,s59,107020288
s_lshl_b32 s60,s45,9
s_add_u32 s60,s60,s66
s_add_u32 s60,s60,131072
s_add_u32 s45,s45,s46
s_lshr_b32 s78,s45,1
s_and_b32 s79,s45,1
s_lshl_b32 s79,s79,7
s_lshl_b32 s55,s78,15
s_add_u32 s55,s55,s79
s_mul_i32 s56,s45,16384
s_add_u32 s56,s56,98566144
s_lshl_b32 s61,s78,11
s_add_u32 s61,s61,110587904
s_add_u32 s78,s45,s46
s_lshr_b32 s78,s78,1
s_lshl_b32 s62,s78,11
s_add_u32 s62,s62,110587904
s_waitcnt vmcnt(6)
s_branch Lr_it0_%=
Lr_tb1_%=:
v_add_f32 v53,s69,v53
v_rsq_f32 v48,v53
s_nop 0
v_mul_f32_e64 v50,-v52,v48
v_mov_b32 v49,v48
v_mov_b32 v51,v50
v_pk_fma_f32 v[52:53],v[116:117],v[48:49],v[50:51]
v_pk_fma_f32 v[54:55],v[118:119],v[48:49],v[50:51]
v_pk_fma_f32 v[56:57],v[120:121],v[48:49],v[50:51]
v_pk_fma_f32 v[58:59],v[122:123],v[48:49],v[50:51]
s_add_u32 s78,s55,71303168
buffer_load_dwordx4 v[164:167],v1,s[36:39],s78 offen offset:0 sc1
buffer_load_dwordx4 v[168:171],v1,s[36:39],s78 offen offset:2048 sc1
s_add_u32 s78,s55,71307264
buffer_load_dwordx4 v[172:175],v1,s[36:39],s78 offen offset:0 sc1
buffer_load_dwordx4 v[176:179],v1,s[36:39],s78 offen offset:2048 sc1
v_pk_fma_f32 v[116:117],v[52:53],v[84:85],v[116:117]
v_pk_fma_f32 v[118:119],v[54:55],v[86:87],v[118:119]
v_pk_fma_f32 v[120:121],v[56:57],v[88:89],v[120:121]
v_pk_fma_f32 v[122:123],v[58:59],v[90:91],v[122:123]
v_pk_fma_f32 v[52:53],v[124:125],v[48:49],v[50:51]
v_pk_fma_f32 v[54:55],v[126:127],v[48:49],v[50:51]
v_pk_fma_f32 v[56:57],v[128:129],v[48:49],v[50:51]
v_pk_fma_f32 v[58:59],v[130:131],v[48:49],v[50:51]
s_add_u32 s78,s55,89128960
buffer_load_dwordx4 v[180:183],v1,s[36:39],s78 offen offset:0 sc1
buffer_load_dwordx4 v[184:187],v1,s[36:39],s78 offen offset:2048 sc1
s_add_u32 s78,s55,89133056
buffer_load_dwordx4 v[188:191],v1,s[36:39],s78 offen offset:0 sc1
buffer_load_dwordx4 v[192:195],v1,s[36:39],s78 offen offset:2048 sc1
v_pk_fma_f32 v[124:125],v[52:53],v[92:93],v[124:125]
v_pk_fma_f32 v[126:127],v[54:55],v[94:95],v[126:127]
v_pk_fma_f32 v[128:129],v[56:57],v[96:97],v[128:129]
v_pk_fma_f32 v[130:131],v[58:59],v[98:99],v[130:131]
v_pk_fma_f32 v[52:53],v[132:133],v[48:49],v[50:51]
v_pk_fma_f32 v[54:55],v[134:135],v[48:49],v[50:51]
v_pk_fma_f32 v[56:57],v[136:137],v[48:49],v[50:51]
v_pk_fma_f32 v[58:59],v[138:139],v[48:49],v[50:51]
v_pk_fma_f32 v[132:133],v[52:53],v[100:101],v[132:133]
v_pk_fma_f32 v[134:135],v[54:55],v[102:103],v[134:135]
v_pk_fma_f32 v[136:137],v[56:57],v[104:105],v[136:137]
v_pk_fma_f32 v[138:139],v[58:59],v[106:107],v[138:139]
v_pk_fma_f32 v[52:53],v[140:141],v[48:49],v[50:51]
v_pk_fma_f32 v[54:55],v[142:143],v[48:49],v[50:51]
v_pk_fma_f32 v[56:57],v[144:145],v[48:49],v[50:51]
v_pk_fma_f32 v[58:59],v[146:147],v[48:49],v[50:51]
v_pk_fma_f32 v[140:141],v[52:53],v[108:109],v[140:141]
v_pk_fma_f32 v[142:143],v[54:55],v[110:111],v[142:143]
v_pk_fma_f32 v[144:145],v[56:57],v[112:113],v[144:145]
v_pk_fma_f32 v[146:147],v[58:59],v[114:115],v[146:147]
buffer_load_dword v18,v6,s[36:39],s62 offen offset:0 sc1
v_cvt_pk_bf16_f32 v148,v116,v117
v_cvt_pk_bf16_f32 v149,v118,v119
v_cvt_pk_bf16_f32 v150,v120,v121
v_cvt_pk_bf16_f32 v151,v122,v123
v_cvt_pk_bf16_f32 v152,v124,v125
v_cvt_pk_bf16_f32 v153,v126,v127
v_mfma_f32_16x16x32_bf16 v[36:39],v[32:35],v[148:151],0
v_cvt_pk_bf16_f32 v154,v128,v129
v_cvt_pk_bf16_f32 v155,v130,v131
v_mfma_f32_16x16x32_bf16 v[40:43],v[148:151],v[148:151],0
v_cvt_pk_bf16_f32 v156,v132,v133
v_cvt_pk_bf16_f32 v157,v134,v135
M 36,v[32:35],152
v_cvt_pk_bf16_f32 v158,v136,v137
v_cvt_pk_bf16_f32 v159,v138,v139
M 40,v[152:155],152
v_cvt_pk_bf16_f32 v160,v140,v141
v_cvt_pk_bf16_f32 v161,v142,v143
M 36,v[32:35],156
v_cvt_pk_bf16_f32 v162,v144,v145
v_cvt_pk_bf16_f32 v163,v146,v147
M 40,v[156:159],156
buffer_store_dwordx4 v[148:151],v1,s[36:39],s57 offen offset:0 sc0 sc1
M 36,v[32:35],160
buffer_store_dwordx4 v[152:155],v1,s[36:39],s57 offen offset:2048 sc0 sc1
M 40,v[160:163],160
buffer_store_dwordx4 v[156:159],v1,s[36:39],s58 offen offset:0 sc0 sc1
s_nop 4
v_add_f32_dpp v44,v36,v36 row_ror:8 row_mask:0xf bank_mask:0xf
s_nop 0
v_cndmask_b32 v45,v40,v41,s[70:71]
v_cndmask_b32 v46,v42,v43,s[70:71]
ds_write_b32 v2,v44 offset:1024
v_cndmask_b32 v45,v45,v46,s[72:73]
ds_write_b32 v0,v45 offset:1024
buffer_store_dwordx4 v[160:163],v1,s[36:39],s58 offen offset:2048 sc0 sc1
L 0
s_barrier
R 52,3,1024
R 56,3,1040
R 60,3,1056
L 0
v_pk_add_f32 v[52:53],v[52:53],v[54:55]
v_pk_add_f32 v[56:57],v[56:57],v[58:59]
v_pk_add_f32 v[60:61],v[60:61],v[62:63]
v_add_f32 v52,v52,v53
v_pk_add_f32 v[56:57],v[56:57],v[60:61]
v_mul_f32 v52,s68,v52
v_add_f32 v53,v56,v57
v_mul_f32 v53,s68,v53
v_fma_f32 v53,-v52,v52,v53
v_max_f32 v53,0,v53
v_add_f32 v54,s69,v53
v_fma_f32 v53,s69,v54,v53
v_rsq_f32 v55,v54
v_rsq_f32 v53,v53
v_mul_f32_e64 v54,-v52,v55
v_mul_f32 v53,s76,v53
buffer_store_dwordx4 v[52:55],v4,s[36:39],s59 offen offset:0 sc0 sc1
s_mov_b32 s63,s60
s_lshr_b32 s78,s45,1
s_and_b32 s79,s45,1
s_lshl_b32 s79,s79,7
s_lshl_b32 s57,s78,15
s_add_u32 s57,s57,s79
s_add_u32 s57,s57,12582912
s_add_u32 s58,s57,4096
s_lshl_b32 s59,s78,9
s_add_u32 s59,s59,s79
s_add_u32 s59,s59,107020288
s_lshl_b32 s60,s45,9
s_add_u32 s60,s60,s66
s_add_u32 s60,s60,131072
s_add_u32 s45,s45,s46
s_lshr_b32 s78,s45,1
s_and_b32 s79,s45,1
s_lshl_b32 s79,s79,7
s_lshl_b32 s55,s78,15
s_add_u32 s55,s55,s79
)"
R"(s_mul_i32 s56,s45,16384
s_add_u32 s56,s56,98566144
s_lshl_b32 s61,s78,11
s_add_u32 s61,s61,110587904
s_add_u32 s78,s45,s46
s_lshr_b32 s78,s78,1
s_lshl_b32 s62,s78,11
s_add_u32 s62,s62,110587904
s_waitcnt vmcnt(5)
s_branch Lr_it0_%=
Lr_tc1_%=:
s_add_u32 s78,s55,71303168
buffer_load_dwordx4 v[164:167],v1,s[36:39],s78 offen offset:0 sc1
buffer_load_dwordx4 v[168:171],v1,s[36:39],s78 offen offset:2048 sc1
s_add_u32 s78,s55,71307264
buffer_load_dwordx4 v[172:175],v1,s[36:39],s78 offen offset:0 sc1
buffer_load_dwordx4 v[176:179],v1,s[36:39],s78 offen offset:2048 sc1
s_add_u32 s78,s55,89128960
buffer_load_dwordx4 v[180:183],v1,s[36:39],s78 offen offset:0 sc1
buffer_load_dwordx4 v[184:187],v1,s[36:39],s78 offen offset:2048 sc1
s_add_u32 s78,s55,89133056
buffer_load_dwordx4 v[188:191],v1,s[36:39],s78 offen offset:0 sc1
buffer_load_dwordx4 v[192:195],v1,s[36:39],s78 offen offset:2048 sc1
buffer_load_dword v18,v6,s[36:39],s62 offen offset:0 sc1
s_waitcnt vmcnt(9)
buffer_store_dword v17,v5,s[36:39],s63 offen offset:0 sc0 sc1
s_mov_b32 s63,0x7fff0000
s_lshr_b32 s78,s45,1
s_and_b32 s79,s45,1
s_lshl_b32 s79,s79,7
s_lshl_b32 s57,s78,15
s_add_u32 s57,s57,s79
s_add_u32 s57,s57,12582912
s_add_u32 s58,s57,4096
s_lshl_b32 s59,s78,9
s_add_u32 s59,s59,s79
s_add_u32 s59,s59,107020288
s_lshl_b32 s60,s45,9
s_add_u32 s60,s60,s66
s_add_u32 s60,s60,131072
s_add_u32 s45,s45,s46
s_lshr_b32 s78,s45,1
s_and_b32 s79,s45,1
s_lshl_b32 s79,s79,7
s_lshl_b32 s55,s78,15
s_add_u32 s55,s55,s79
s_mul_i32 s56,s45,16384
s_add_u32 s56,s56,98566144
s_lshl_b32 s61,s78,11
s_add_u32 s61,s61,110587904
s_add_u32 s78,s45,s46
s_lshr_b32 s78,s78,1
s_lshl_b32 s62,s78,11
s_add_u32 s62,s62,110587904
s_waitcnt vmcnt(1)
s_branch Lr_it0_%=
Lr_done_%=:
s_waitcnt vmcnt(0) lgkmcnt(0)
s_barrier
s_branch Lr_end_%=
.p2align 8
Lr_tab_%=:
s_mov_b64 s[40:41],%24
s_mov_b32 s42,7340032
s_mov_b32 s43,0x00020000
s_add_u32 s83,s50,0
s_add_u32 m0,s54,40960
s_nop 0
buffer_load_dwordx4 v13,s[40:43],s83 offen offset:0  lds
s_add_u32 m0,s54,41984
s_nop 0
buffer_load_dwordx4 v14,s[40:43],s83 offen offset:0  lds
s_add_u32 m0,s54,49024
s_nop 0
buffer_load_dwordx4 v13,s[40:43],s83 offen offset:128  lds
s_add_u32 m0,s54,50048
s_nop 0
buffer_load_dwordx4 v14,s[40:43],s83 offen offset:128  lds
s_setpc_b64 s[88:89]
.p2align 8
s_mov_b64 s[40:41],%24
s_mov_b32 s42,7340032
s_mov_b32 s43,0x00020000
v_add_u32 v230,40960,v15
v_add_u32 v231,40960,v16
T a[0:1],230,0
T a[2:3],230,512
T a[4:5],231,0
T a[6:7],231,512
T a[8:9],230,4096
T a[10:11],230,4608
T a[12:13],231,4096
T a[14:15],231,4608
s_add_u32 s83,s50,131072
s_add_u32 m0,s54,57344
s_nop 0
buffer_load_dwordx4 v13,s[40:43],s83 offen offset:0  lds
s_add_u32 m0,s54,58368
s_nop 0
buffer_load_dwordx4 v14,s[40:43],s83 offen offset:0  lds
s_add_u32 m0,s54,65408
s_nop 0
buffer_load_dwordx4 v13,s[40:43],s83 offen offset:128  lds
s_add_u32 m0,s54,66432
s_nop 0
buffer_load_dwordx4 v14,s[40:43],s83 offen offset:128  lds
s_setpc_b64 s[88:89]
.p2align 8
s_mov_b64 s[40:41],%24
s_mov_b32 s42,7340032
s_mov_b32 s43,0x00020000
v_add_u32 v230,57344,v15
v_add_u32 v231,57344,v16
T a[16:17],230,0
T a[18:19],230,512
T a[20:21],231,0
T a[22:23],231,512
T a[24:25],230,4096
T a[26:27],230,4608
T a[28:29],231,4096
T a[30:31],231,4608
s_add_u32 s83,s50,262144
s_add_u32 m0,s54,73728
s_nop 0
buffer_load_dwordx4 v13,s[40:43],s83 offen offset:0  lds
s_add_u32 m0,s54,74752
s_nop 0
buffer_load_dwordx4 v14,s[40:43],s83 offen offset:0  lds
s_add_u32 m0,s54,81792
s_nop 0
buffer_load_dwordx4 v13,s[40:43],s83 offen offset:128  lds
s_add_u32 m0,s54,82816
s_nop 0
buffer_load_dwordx4 v14,s[40:43],s83 offen offset:128  lds
s_setpc_b64 s[88:89]
.p2align 8
s_mov_b64 s[40:41],%24
s_mov_b32 s42,7340032
s_mov_b32 s43,0x00020000
v_add_u32 v230,73728,v15
v_add_u32 v231,73728,v16
T a[32:33],230,0
T a[34:35],230,512
T a[36:37],231,0
T a[38:39],231,512
T a[40:41],230,4096
T a[42:43],230,4608
T a[44:45],231,4096
T a[46:47],231,4608
s_add_u32 s83,s50,393216
s_add_u32 m0,s54,90112
s_nop 0
buffer_load_dwordx4 v13,s[40:43],s83 offen offset:0  lds
s_add_u32 m0,s54,91136
s_nop 0
buffer_load_dwordx4 v14,s[40:43],s83 offen offset:0  lds
s_add_u32 m0,s54,98176
s_nop 0
buffer_load_dwordx4 v13,s[40:43],s83 offen offset:128  lds
s_add_u32 m0,s54,99200
s_nop 0
buffer_load_dwordx4 v14,s[40:43],s83 offen offset:128  lds
s_setpc_b64 s[88:89]
.p2align 8
s_mov_b64 s[40:41],%24
s_mov_b32 s42,7340032
s_mov_b32 s43,0x00020000
v_add_u32 v230,90112,v15
v_add_u32 v231,90112,v16
T a[48:49],230,0
T a[50:51],230,512
T a[52:53],231,0
T a[54:55],231,512
T a[56:57],230,4096
T a[58:59],230,4608
T a[60:61],231,4096
T a[62:63],231,4608
s_add_u32 s83,s50,524288
s_add_u32 m0,s54,106496
s_nop 0
buffer_load_dwordx4 v13,s[40:43],s83 offen offset:0  lds
s_add_u32 m0,s54,107520
s_nop 0
buffer_load_dwordx4 v14,s[40:43],s83 offen offset:0  lds
s_add_u32 m0,s54,114560
s_nop 0
buffer_load_dwordx4 v13,s[40:43],s83 offen offset:128  lds
s_add_u32 m0,s54,115584
s_nop 0
buffer_load_dwordx4 v14,s[40:43],s83 offen offset:128  lds
s_setpc_b64 s[88:89]
.p2align 8
s_mov_b64 s[40:41],%24
s_mov_b32 s42,7340032
s_mov_b32 s43,0x00020000
v_add_u32 v230,106496,v15
v_add_u32 v231,106496,v16
T a[64:65],230,0
T a[66:67],230,512
T a[68:69],231,0
T a[70:71],231,512
T a[72:73],230,4096
T a[74:75],230,4608
T a[76:77],231,4096
T a[78:79],231,4608
s_add_u32 s83,s50,655360
s_add_u32 m0,s54,122880
s_nop 0
buffer_load_dwordx4 v13,s[40:43],s83 offen offset:0  lds
s_add_u32 m0,s54,123904
s_nop 0
buffer_load_dwordx4 v14,s[40:43],s83 offen offset:0  lds
s_add_u32 m0,s54,130944
s_nop 0
buffer_load_dwordx4 v13,s[40:43],s83 offen offset:128  lds
s_add_u32 m0,s54,131968
s_nop 0
buffer_load_dwordx4 v14,s[40:43],s83 offen offset:128  lds
s_setpc_b64 s[88:89]
.p2align 8
s_mov_b64 s[40:41],%24
s_mov_b32 s42,7340032
s_mov_b32 s43,0x00020000
v_add_u32 v230,122880,v15
v_add_u32 v231,122880,v16
T a[80:81],230,0
T a[82:83],230,512
T a[84:85],231,0
T a[86:87],231,512
T a[88:89],230,4096
T a[90:91],230,4608
T a[92:93],231,4096
T a[94:95],231,4608
s_add_u32 s83,s50,786432
s_add_u32 m0,s54,139264
s_nop 0
buffer_load_dwordx4 v13,s[40:43],s83 offen offset:0  lds
s_add_u32 m0,s54,140288
s_nop 0
buffer_load_dwordx4 v14,s[40:43],s83 offen offset:0  lds
s_add_u32 m0,s54,147328
s_nop 0
buffer_load_dwordx4 v13,s[40:43],s83 offen offset:128  lds
s_add_u32 m0,s54,148352
s_nop 0
buffer_load_dwordx4 v14,s[40:43],s83 offen offset:128  lds
s_setpc_b64 s[88:89]
.p2align 8
s_mov_b64 s[40:41],%24
s_mov_b32 s42,7340032
s_mov_b32 s43,0x00020000
v_add_u32 v230,139264,v15
v_add_u32 v231,139264,v16
T a[96:97],230,0
T a[98:99],230,512
T a[100:101],231,0
T a[102:103],231,512
T a[104:105],230,4096
T a[106:107],230,4608
T a[108:109],231,4096
T a[110:111],231,4608
s_add_u32 s83,s50,917504
s_add_u32 m0,s54,40960
s_nop 0
buffer_load_dwordx4 v13,s[40:43],s83 offen offset:0  lds
s_add_u32 m0,s54,41984
s_nop 0
buffer_load_dwordx4 v14,s[40:43],s83 offen offset:0  lds
s_add_u32 m0,s54,49024
s_nop 0
buffer_load_dwordx4 v13,s[40:43],s83 offen offset:128  lds
s_add_u32 m0,s54,50048
s_nop 0
buffer_load_dwordx4 v14,s[40:43],s83 offen offset:128  lds
s_setpc_b64 s[88:89]
.p2align 8
s_mov_b64 s[40:41],%24
s_mov_b32 s42,7340032
s_mov_b32 s43,0x00020000
v_add_u32 v230,40960,v15
v_add_u32 v231,40960,v16
T a[112:113],230,0
T a[114:115],230,512
T a[116:117],231,0
T a[118:119],231,512
T a[120:121],230,4096
T a[122:123],230,4608
T a[124:125],231,4096
T a[126:127],231,4608
s_add_u32 s83,s50,1048576
s_add_u32 m0,s54,57344
s_nop 0
buffer_load_dwordx4 v13,s[40:43],s83 offen offset:0  lds
s_add_u32 m0,s54,58368
s_nop 0
buffer_load_dwordx4 v14,s[40:43],s83 offen offset:0  lds
s_add_u32 m0,s54,65408
s_nop 0
buffer_load_dwordx4 v13,s[40:43],s83 offen offset:128  lds
s_add_u32 m0,s54,66432
s_nop 0
buffer_load_dwordx4 v14,s[40:43],s83 offen offset:128  lds
s_setpc_b64 s[88:89]
.p2align 8
s_mov_b64 s[40:41],%24
s_mov_b32 s42,7340032
s_mov_b32 s43,0x00020000
v_add_u32 v230,57344,v15
v_add_u32 v231,57344,v16
T a[128:129],230,0
T a[130:131],230,512
T a[132:133],231,0
T a[134:135],231,512
T a[136:137],230,4096
T a[138:139],230,4608
T a[140:141],231,4096
T a[142:143],231,4608
s_add_u32 s83,s50,1179648
s_add_u32 m0,s54,73728
s_nop 0
buffer_load_dwordx4 v13,s[40:43],s83 offen offset:0  lds
s_add_u32 m0,s54,74752
s_nop 0
buffer_load_dwordx4 v14,s[40:43],s83 offen offset:0  lds
s_add_u32 m0,s54,81792
s_nop 0
buffer_load_dwordx4 v13,s[40:43],s83 offen offset:128  lds
s_add_u32 m0,s54,82816
s_nop 0
buffer_load_dwordx4 v14,s[40:43],s83 offen offset:128  lds
s_setpc_b64 s[88:89]
.p2align 8
s_mov_b64 s[40:41],%24
s_mov_b32 s42,7340032
s_mov_b32 s43,0x00020000
v_add_u32 v230,73728,v15
v_add_u32 v231,73728,v16
T a[144:145],230,0
T a[146:147],230,512
T a[148:149],231,0
T a[150:151],231,512
T a[152:153],230,4096
T a[154:155],230,4608
T a[156:157],231,4096
T a[158:159],231,4608
s_add_u32 s83,s50,1310720
s_add_u32 m0,s54,90112
s_nop 0
buffer_load_dwordx4 v13,s[40:43],s83 offen offset:0  lds
s_add_u32 m0,s54,91136
s_nop 0
buffer_load_dwordx4 v14,s[40:43],s83 offen offset:0  lds
s_add_u32 m0,s54,98176
s_nop 0
buffer_load_dwordx4 v13,s[40:43],s83 offen offset:128  lds
s_add_u32 m0,s54,99200
s_nop 0
buffer_load_dwordx4 v14,s[40:43],s83 offen offset:128  lds
s_setpc_b64 s[88:89]
.p2align 8
Lr_end_%=:
Lcode_r_e:
.purgem M
.purgem R
.purgem T
.purgem D
.purgem L
)"
  :: "s"(ws), "s"(__builtin_amdgcn_readfirstlane(sWave)), "s"(__builtin_amdgcn_readfirstlane(sG0)), "s"(__builtin_amdgcn_readfirstlane(sStep)), "s"(__builtin_amdgcn_readfirstlane(sWw)), "s"(__builtin_amdgcn_readfirstlane(sExp)), "v"(vRP), "v"(vRF), "v"(vRwr), "v"(vRrd), "v"(vRS), "v"(vFlag), "v"(vL4), "v"(vLY), "v"(vQ), "v"(vY0), "v"(vTr), "v"(vSa), "v"(vSb), "v"(vWd0), "v"(vWd1), "v"(vWr0), "v"(vWr1), "s"(q), "s"(w4) : "v0", "v1", "v2", "v3", "v4", "v5", "v6", "v7", "v8", "v9", "v10", "v11", "v12", "v13", "v14", "v15", "v16", "v17", "v18", "v19", "v20", "v21", "v22", "v23", "v24", "v25", "v26", "v27", "v28", "v29", "v30", "v31", "v32", "v33", "v34", "v35", "v36", "v37", "v38", "v39", "v40", "v41", "v42", "v43", "v44", "v45", "v46", "v47", "v48", "v49", "v50", "v51", "v52", "v53", "v54", "v55", "v56", "v57", "v58", "v59", "v60", "v61", "v62", "v63", "v64", "v65", "v66", "v67", "v68", "v69", "v70", "v71", "v72", "v73", "v74", "v75", "v76", "v77", "v78", "v79", "v80", "v81", "v82", "v83", "v84", "v85", "v86", "v87", "v88", "v89", "v90", "v91", "v92", "v93", "v94", "v95", "v96", "v97", "v98", "v99", "v100", "v101", "v102", "v103", "v104", "v105", "v106", "v107", "v108", "v109", "v110", "v111", "v112", "v113", "v114", "v115", "v116", "v117", "v118", "v119", "v120", "v121", "v122", "v123", "v124", "v125", "v126", "v127", "v128", "v129", "v130", "v131", "v132", "v133", "v134", "v135", "v136", "v137", "v138", "v139", "v140", "v141", "v142", "v143", "v144", "v145", "v146", "v147", "v148", "v149", "v150", "v151", "v152", "v153", "v154", "v155", "v156", "v157", "v158", "v159", "v160", "v161", "v162", "v163", "v164", "v165", "v166", "v167", "v168", "v169", "v170", "v171", "v172", "v173", "v174", "v175", "v176", "v177", "v178", "v179", "v180", "v181", "v182", "v183", "v184", "v185", "v186", "v187", "v188", "v189", "v190", "v191", "v192", "v193", "v194", "v195", "v196", "v197", "v198", "v199", "v200", "v201", "v202", "v203", "v204", "v205", "v206", "v207", "v208", "v209", "v210", "v211", "v212", "v213", "v214", "v215", "v216", "v217", "v218", "v219", "v220", "v221", "v222", "v223", "v224", "v225", "v226", "v227", "v228", "v229", "v230", "v231", "a0", "a1", "a2", "a3", "a4", "a5", "a6", "a7", "a8", "a9", "a10", "a11", "a12", "a13", "a14", "a15", "a16", "a17", "a18", "a19", "a20", "a21", "a22", "a23", "a24", "a25", "a26", "a27", "a28", "a29", "a30", "a31", "a32", "a33", "a34", "a35", "a36", "a37", "a38", "a39", "a40", "a41", "a42", "a43", "a44", "a45", "a46", "a47", "a48", "a49", "a50", "a51", "a52", "a53", "a54", "a55", "a56", "a57", "a58", "a59", "a60", "a61", "a62", "a63", "a64", "a65", "a66", "a67", "a68", "a69", "a70", "a71", "a72", "a73", "a74", "a75", "a76", "a77", "a78", "a79", "a80", "a81", "a82", "a83", "a84", "a85", "a86", "a87", "a88", "a89", "a90", "a91", "a92", "a93", "a94", "a95", "a96", "a97", "a98", "a99", "a100", "a101", "a102", "a103", "a104", "a105", "a106", "a107", "a108", "a109", "a110", "a111", "a112", "a113", "a114", "a115", "a116", "a117", "a118", "a119", "a120", "a121", "a122", "a123", "a124", "a125", "a126", "a127", "a128", "a129", "a130", "a131", "a132", "a133", "a134", "a135", "a136", "a137", "a138", "a139", "a140", "a141", "a142", "a143", "a144", "a145", "a146", "a147", "a148", "a149", "a150", "a151", "a152", "a153", "a154", "a155", "a156", "a157", "a158", "a159", "a160", "a161", "a162", "a163", "a164", "a165", "a166", "a167", "a168", "a169", "a170", "a171", "a172", "a173", "a174", "a175", "a176", "a177", "a178", "a179", "a180", "a181", "a182", "a183", "a184", "a185", "a186", "a187", "a188", "a189", "a190", "a191", "a192", "a193", "a194", "a195", "a196", "a197", "a198", "a199", "a200", "a201", "a202", "a203", "a204", "a205", "a206", "a207", "a208", "a209", "a210", "a211", "a212", "a213", "a214", "a215", "a216", "a217", "a218", "a219", "a220", "a221", "a222", "a223", "a224", "a225", "a226", "a227", "a228", "a229", "a230", "a231", "a232", "a233", "a234", "a235", "a236", "a237", "a238", "a239", "a240", "a241", "a242", "a243", "a244", "a245", "a246", "a247", "a248", "a249", "a250", "a251", "a252", "a253", "a254", "a255", "s36", "s37", "s38", "s39", "s40", "s41", "s42", "s43", "s44", "s45", "s46", "s47", "s48", "s49", "s50", "s51", "s52", "s53", "s54", "s55", "s56", "s57", "s58", "s59", "s60", "s61", "s62", "s63", "s64", "s65", "s66", "s67", "s68", "s69", "s70", "s71", "s72", "s73", "s74", "s75", "s76", "s77", "s78", "s79", "s80", "s81", "s82", "s83", "s84", "s85", "s86", "s87", "s88", "s89", "s90", "s91", "s92", "s93", "s94", "s95", "s96", "s97", "vcc", "scc", "memory");
    if (RHELP && slice < 16) { kind = 4; rep = 5; kh = slice >> 3; slice &= 7; }
  }
  CTS(9);
#define COMMON(NF, FOFF, FSTR, SNAP, LDW, NL, TOFF)                                                                                            \
  LANE_ID(ln);                                                                                                                       \
  const int l15 = ln & 15, lg = ln >> 4, lq = l15 >> 2, lp = ln & 3, rr = 8 * lg + lq, fj = ln & 31;                                 \
  const unsigned vL16 = ln * 16, vFlag = (ln < 32 && wave == 0) ? ln * 4 : OOB;                                                      \
  const unsigned vSnapOff = (wave == 0 && fj < (NF)) ? (FOFF) + (ln >> 5) * sStep * (FSTR) : OOB;                                    \
  const unsigned vSnapRd = (SNAP) + (fj < (NF) ? ln : ln & 32) * 4;                                                                  \
  const unsigned vWd0 = (ln >> 3) * (LDW) + ((ln & 7) ^ swzB(ln >> 3)) * 16, vWd1 = ((ln >> 3) + 8) * (LDW) + ((ln & 7) ^ swzB(8 + (ln >> 3))) * 16; \
  const unsigned vWr0 = (wave >> 1) * 8192 + rr * 128 + (((wave & 1) * 4 + lp) ^ swzB(rr)) * 16, vWr1 = vWr0 + 8;                    \
  const unsigned vWr2 = 16384 + rr * 128 + ((2 * wave + (lp >> 1)) ^ swzB(rr)) * 16 + (lp & 1) * 8;                                  \
  const unsigned sWave = wave, sRk = kind == 4 ? rep : t_start, sG0 = rep < 5 ? rep : rep == 5 ? U0 + 5 : U1 + rep, sDbg = sDbg0 + (rep < 5 ? 0 : 2048)
  if (kind == 3 || kind == 5) {
    const unsigned sStep = kind == 3 ? 5 : G3OR;
    COMMON(8, fj * 128, 1024, 131072, 7168, 256, 0);
    const unsigned vOutS = (lg & 1) * 8 + (((wave & 1) * 2 + (lg >> 1)) * 16 + l15) * 16 + (4 + (wave >> 1) - wave) * 1024;
    const unsigned vAux0 = (ln & 7) * 128 + (4 * wave + lg) * 8 + ((ln >> 3) & 1) * 4;
    const unsigned vAux1 = 139264 + (l15 < 2 ? (4 * wave + lg) * 8 + l15 * 4 : 512 + ln * 4), vAux2 = 139264 + l15 * 8;
    const unsigned vAux3 = 0, vAn = l15 == 0 ? 0x3f800000u : 0u, vAe = l15 == 1 ? GELU_CB : 0u;
    const unsigned sOutOff = OFF_G + (slice * 6 + wave) * 1024, sFoutOff = OFF_FG + slice * 128, sAuxOff = 0, sWw = 16 * wave * 7168 + slice * 384, sBuf = 0;
    const unsigned sExp = __builtin_amdgcn_readfirstlane(ep);
    if (kind == 3) { asm volatile(
R"(.macro M d,w,x
v_mfma_f32_16x16x32_bf16 v[\d:\d+3], \w, v[\x:\x+3], v[\d:\d+3]
.endm
.macro R d,a,o
ds_read_b128 v[\d:\d+3], v\a offset:\o
.endm
.macro T d,a,o
ds_read_b64_tr_b16 \d, v\a offset:\o
.endm
.macro D a,s,o
buffer_load_dwordx4 v\a, s[36:39], s\s offen offset:\o sc1 lds
.endm
.macro L c
s_waitcnt lgkmcnt(\c)
.endm
Lcode_g3_b:
s_mov_b64 s[36:37],%0
s_mov_b32 s38,111243392
s_mov_b32 s39,0x00020000
s_mov_b32 s44,%1
s_mov_b32 s45,%2
s_mov_b32 s46,%3
s_mov_b32 s47,%4
s_mov_b32 s48,%5
s_mov_b32 s49,%6
s_mov_b32 s50,%7
s_mov_b32 s51,%8
s_mov_b32 s53,%9
v_mov_b32 v0,%10
v_mov_b32 v3,%11
v_mov_b32 v5,%12
v_mov_b32 v6,%13
v_mov_b32 v18,%14
v_mov_b32 v19,%15
v_mov_b32 v20,%16
v_mov_b32 v21,%17
v_mov_b32 v22,%18
v_mov_b32 v10,%19
v_mov_b32 v11,%20
v_mov_b32 v12,%21
v_mov_b32 v13,%22
v_mov_b32 v14,%23
v_mov_b32 v15,%24
v_mov_b32 v16,%25
s_mov_b64 s[40:41],%26
s_mov_b32 s42,7340032
s_mov_b32 s43,0x00020000
s_lshl_b32 s57,s44,11
s_cmp_ge_u32 s45,8
s_cbranch_scc1 Lg3_dly_%=
s_sleep 16
Lg3_dly_%=:
s_add_u32 s55,s50,0
s_add_u32 m0,s57,0
s_nop 0
buffer_load_dwordx4 v18,s[40:43],s55 offen offset:0  lds
s_add_u32 m0,s57,1024
s_nop 0
buffer_load_dwordx4 v19,s[40:43],s55 offen offset:0  lds
s_add_u32 m0,s57,8064
s_nop 0
buffer_load_dwordx4 v18,s[40:43],s55 offen offset:128  lds
s_add_u32 m0,s57,9088
s_nop 0
buffer_load_dwordx4 v19,s[40:43],s55 offen offset:128  lds
s_add_u32 m0,s57,16128
s_nop 0
buffer_load_dwordx4 v18,s[40:43],s55 offen offset:256  lds
s_add_u32 m0,s57,17152
s_nop 0
buffer_load_dwordx4 v19,s[40:43],s55 offen offset:256  lds
s_add_u32 s55,s50,458752
s_add_u32 m0,s57,24576
s_nop 0
buffer_load_dwordx4 v18,s[40:43],s55 offen offset:0  lds
s_add_u32 m0,s57,25600
s_nop 0
buffer_load_dwordx4 v19,s[40:43],s55 offen offset:0  lds
s_add_u32 m0,s57,32640
s_nop 0
buffer_load_dwordx4 v18,s[40:43],s55 offen offset:128  lds
s_add_u32 m0,s57,33664
s_nop 0
buffer_load_dwordx4 v19,s[40:43],s55 offen offset:128  lds
s_add_u32 m0,s57,40704
s_nop 0
buffer_load_dwordx4 v18,s[40:43],s55 offen offset:256  lds
s_add_u32 m0,s57,41728
s_nop 0
buffer_load_dwordx4 v19,s[40:43],s55 offen offset:256  lds
s_add_u32 s55,s50,917504
s_add_u32 m0,s57,49152
s_nop 0
buffer_load_dwordx4 v18,s[40:43],s55 offen offset:0  lds
s_add_u32 m0,s57,50176
s_nop 0
buffer_load_dwordx4 v19,s[40:43],s55 offen offset:0  lds
s_add_u32 m0,s57,57216
s_nop 0
buffer_load_dwordx4 v18,s[40:43],s55 offen offset:128  lds
s_add_u32 m0,s57,58240
s_nop 0
buffer_load_dwordx4 v19,s[40:43],s55 offen offset:128  lds
s_add_u32 m0,s57,65280
s_nop 0
buffer_load_dwordx4 v18,s[40:43],s55 offen offset:256  lds
s_add_u32 m0,s57,66304
s_nop 0
buffer_load_dwordx4 v19,s[40:43],s55 offen offset:256  lds
s_add_u32 s55,s50,1376256
s_add_u32 m0,s57,73728
s_nop 0
buffer_load_dwordx4 v18,s[40:43],s55 offen offset:0  lds
s_add_u32 m0,s57,74752
s_nop 0
buffer_load_dwordx4 v19,s[40:43],s55 offen offset:0  lds
s_add_u32 m0,s57,81792
s_nop 0
buffer_load_dwordx4 v18,s[40:43],s55 offen offset:128  lds
s_add_u32 m0,s57,82816
s_nop 0
buffer_load_dwordx4 v19,s[40:43],s55 offen offset:128  lds
s_add_u32 m0,s57,89856
s_nop 0
buffer_load_dwordx4 v18,s[40:43],s55 offen offset:256  lds
s_add_u32 m0,s57,90880
s_nop 0
buffer_load_dwordx4 v19,s[40:43],s55 offen offset:256  lds
s_add_u32 s55,s50,1835008
s_add_u32 m0,s57,98304
s_nop 0
buffer_load_dwordx4 v18,s[40:43],s55 offen offset:0  lds
s_add_u32 m0,s57,99328
s_nop 0
buffer_load_dwordx4 v19,s[40:43],s55 offen offset:0  lds
s_add_u32 m0,s57,106368
s_nop 0
buffer_load_dwordx4 v18,s[40:43],s55 offen offset:128  lds
s_add_u32 m0,s57,107392
s_nop 0
buffer_load_dwordx4 v19,s[40:43],s55 offen offset:128  lds
s_add_u32 m0,s57,114432
s_nop 0
buffer_load_dwordx4 v18,s[40:43],s55 offen offset:256  lds
s_add_u32 m0,s57,115456
s_nop 0
buffer_load_dwordx4 v19,s[40:43],s55 offen offset:256  lds
s_waitcnt vmcnt(24) lgkmcnt(0)
s_barrier
s_add_u32 s55,s50,2293760
s_add_u32 m0,s57,122880
s_nop 0
buffer_load_dwordx4 v18,s[40:43],s55 offen offset:0  lds
s_add_u32 m0,s57,123904
s_nop 0
buffer_load_dwordx4 v19,s[40:43],s55 offen offset:0  lds
s_add_u32 m0,s57,130944
s_nop 0
buffer_load_dwordx4 v18,s[40:43],s55 offen offset:128  lds
s_add_u32 m0,s57,131968
s_nop 0
buffer_load_dwordx4 v19,s[40:43],s55 offen offset:128  lds
s_add_u32 m0,s57,139008
s_nop 0
buffer_load_dwordx4 v18,s[40:43],s55 offen offset:256  lds
s_add_u32 m0,s57,140032
s_nop 0
buffer_load_dwordx4 v19,s[40:43],s55 offen offset:256  lds
v_add_u32 v26,0,v20
v_add_u32 v27,0,v21
v_add_u32 v28,0,v22
T a[0:1],26,0
T a[2:3],26,512
T a[4:5],27,0
T a[6:7],27,512
T a[8:9],28,0
T a[10:11],28,512
T a[12:13],26,4096
T a[14:15],26,4608
T a[16:17],27,4096
T a[18:19],27,4608
T a[20:21],28,4096
T a[22:23],28,4608
s_waitcnt vmcnt(24) lgkmcnt(0)
s_barrier
s_add_u32 s55,s50,2752512
s_add_u32 m0,s57,0
s_nop 0
buffer_load_dwordx4 v18,s[40:43],s55 offen offset:0  lds
s_add_u32 m0,s57,1024
s_nop 0
buffer_load_dwordx4 v19,s[40:43],s55 offen offset:0  lds
s_add_u32 m0,s57,8064
s_nop 0
buffer_load_dwordx4 v18,s[40:43],s55 offen offset:128  lds
s_add_u32 m0,s57,9088
s_nop 0
buffer_load_dwordx4 v19,s[40:43],s55 offen offset:128  lds
s_add_u32 m0,s57,16128
s_nop 0
buffer_load_dwordx4 v18,s[40:43],s55 offen offset:256  lds
s_add_u32 m0,s57,17152
s_nop 0
buffer_load_dwordx4 v19,s[40:43],s55 offen offset:256  lds
v_add_u32 v26,24576,v20
v_add_u32 v27,24576,v21
v_add_u32 v28,24576,v22
T a[24:25],26,0
T a[26:27],26,512
T a[28:29],27,0
T a[30:31],27,512
T a[32:33],28,0
T a[34:35],28,512
T a[36:37],26,4096
T a[38:39],26,4608
T a[40:41],27,4096
T a[42:43],27,4608
T a[44:45],28,4096
T a[46:47],28,4608
s_waitcnt vmcnt(24) lgkmcnt(0)
s_barrier
s_add_u32 s55,s50,3211264
s_add_u32 m0,s57,24576
s_nop 0
buffer_load_dwordx4 v18,s[40:43],s55 offen offset:0  lds
s_add_u32 m0,s57,25600
s_nop 0
buffer_load_dwordx4 v19,s[40:43],s55 offen offset:0  lds
s_add_u32 m0,s57,32640
s_nop 0
buffer_load_dwordx4 v18,s[40:43],s55 offen offset:128  lds
s_add_u32 m0,s57,33664
s_nop 0
buffer_load_dwordx4 v19,s[40:43],s55 offen offset:128  lds
s_add_u32 m0,s57,40704
s_nop 0
buffer_load_dwordx4 v18,s[40:43],s55 offen offset:256  lds
s_add_u32 m0,s57,41728
s_nop 0
buffer_load_dwordx4 v19,s[40:43],s55 offen offset:256  lds
v_add_u32 v26,49152,v20
v_add_u32 v27,49152,v21
v_add_u32 v28,49152,v22
T a[48:49],26,0
T a[50:51],26,512
T a[52:53],27,0
T a[54:55],27,512
T a[56:57],28,0
T a[58:59],28,512
T a[60:61],26,4096
T a[62:63],26,4608
T a[64:65],27,4096
T a[66:67],27,4608
T a[68:69],28,4096
T a[70:71],28,4608
s_waitcnt vmcnt(24) lgkmcnt(0)
s_barrier
s_add_u32 s55,s50,3670016
s_add_u32 m0,s57,49152
s_nop 0
buffer_load_dwordx4 v18,s[40:43],s55 offen offset:0  lds
s_add_u32 m0,s57,50176
s_nop 0
buffer_load_dwordx4 v19,s[40:43],s55 offen offset:0  lds
s_add_u32 m0,s57,57216
s_nop 0
buffer_load_dwordx4 v18,s[40:43],s55 offen offset:128  lds
s_add_u32 m0,s57,58240
s_nop 0
buffer_load_dwordx4 v19,s[40:43],s55 offen offset:128  lds
s_add_u32 m0,s57,65280
s_nop 0
buffer_load_dwordx4 v18,s[40:43],s55 offen offset:256  lds
s_add_u32 m0,s57,66304
s_nop 0
buffer_load_dwordx4 v19,s[40:43],s55 offen offset:256  lds
v_add_u32 v26,73728,v20
v_add_u32 v27,73728,v21
v_add_u32 v28,73728,v22
T a[72:73],26,0
T a[74:75],26,512
T a[76:77],27,0
T a[78:79],27,512
T a[80:81],28,0
T a[82:83],28,512
T a[84:85],26,4096
T a[86:87],26,4608
T a[88:89],27,4096
T a[90:91],27,4608
T a[92:93],28,4096
T a[94:95],28,4608
s_waitcnt vmcnt(24) lgkmcnt(0)
s_barrier
s_add_u32 s55,s50,4128768
s_add_u32 m0,s57,73728
s_nop 0
buffer_load_dwordx4 v18,s[40:43],s55 offen offset:0  lds
s_add_u32 m0,s57,74752
s_nop 0
buffer_load_dwordx4 v19,s[40:43],s55 offen offset:0  lds
s_add_u32 m0,s57,81792
s_nop 0
buffer_load_dwordx4 v18,s[40:43],s55 offen offset:128  lds
s_add_u32 m0,s57,82816
s_nop 0
buffer_load_dwordx4 v19,s[40:43],s55 offen offset:128  lds
s_add_u32 m0,s57,89856
s_nop 0
buffer_load_dwordx4 v18,s[40:43],s55 offen offset:256  lds
s_add_u32 m0,s57,90880
s_nop 0
buffer_load_dwordx4 v19,s[40:43],s55 offen offset:256  lds
v_add_u32 v26,98304,v20
v_add_u32 v27,98304,v21
v_add_u32 v28,98304,v22
T a[96:97],26,0
T a[98:99],26,512
T a[100:101],27,0
T a[102:103],27,512
T a[104:105],28,0
T a[106:107],28,512
T a[108:109],26,4096
T a[110:111],26,4608
T a[112:113],27,4096
T a[114:115],27,4608
T a[116:117],28,4096
T a[118:119],28,4608
s_waitcnt vmcnt(24) lgkmcnt(0)
s_barrier
s_add_u32 s55,s50,4587520
s_add_u32 m0,s57,98304
s_nop 0
buffer_load_dwordx4 v18,s[40:43],s55 offen offset:0  lds
s_add_u32 m0,s57,99328
s_nop 0
buffer_load_dwordx4 v19,s[40:43],s55 offen offset:0  lds
s_add_u32 m0,s57,106368
s_nop 0
buffer_load_dwordx4 v18,s[40:43],s55 offen offset:128  lds
s_add_u32 m0,s57,107392
s_nop 0
buffer_load_dwordx4 v19,s[40:43],s55 offen offset:128  lds
s_add_u32 m0,s57,114432
s_nop 0
buffer_load_dwordx4 v18,s[40:43],s55 offen offset:256  lds
s_add_u32 m0,s57,115456
s_nop 0
buffer_load_dwordx4 v19,s[40:43],s55 offen offset:256  lds
v_add_u32 v26,122880,v20
v_add_u32 v27,122880,v21
v_add_u32 v28,122880,v22
T a[120:121],26,0
T a[122:123],26,512
T a[124:125],27,0
T a[126:127],27,512
T a[128:129],28,0
T a[130:131],28,512
T a[132:133],26,4096
T a[134:135],26,4608
T a[136:137],27,4096
T a[138:139],27,4608
T a[140:141],28,4096
T a[142:143],28,4608
s_waitcnt vmcnt(24) lgkmcnt(0)
s_barrier
s_add_u32 s55,s50,5046272
s_add_u32 m0,s57,122880
s_nop 0
buffer_load_dwordx4 v18,s[40:43],s55 offen offset:0  lds
s_add_u32 m0,s57,123904
s_nop 0
buffer_load_dwordx4 v19,s[40:43],s55 offen offset:0  lds
s_add_u32 m0,s57,130944
s_nop 0
buffer_load_dwordx4 v18,s[40:43],s55 offen offset:128  lds
s_add_u32 m0,s57,131968
s_nop 0
buffer_load_dwordx4 v19,s[40:43],s55 offen offset:128  lds
s_add_u32 m0,s57,139008
s_nop 0
buffer_load_dwordx4 v18,s[40:43],s55 offen offset:256  lds
s_add_u32 m0,s57,140032
s_nop 0
buffer_load_dwordx4 v19,s[40:43],s55 offen offset:256  lds
v_add_u32 v26,0,v20
v_add_u32 v27,0,v21
v_add_u32 v28,0,v22
T a[144:145],26,0
T a[146:147],26,512
T a[148:149],27,0
T a[150:151],27,512
T a[152:153],28,0
T a[154:155],28,512
T a[156:157],26,4096
T a[158:159],26,4608
T a[160:161],27,4096
T a[162:163],27,4608
T a[164:165],28,4096
T a[166:167],28,4608
s_waitcnt vmcnt(24) lgkmcnt(0)
s_barrier
s_add_u32 s55,s50,5505024
s_add_u32 m0,s57,0
s_nop 0
buffer_load_dwordx4 v18,s[40:43],s55 offen offset:0  lds
s_add_u32 m0,s57,1024
s_nop 0
buffer_load_dwordx4 v19,s[40:43],s55 offen offset:0  lds
s_add_u32 m0,s57,8064
s_nop 0
buffer_load_dwordx4 v18,s[40:43],s55 offen offset:128  lds
s_add_u32 m0,s57,9088
s_nop 0
buffer_load_dwordx4 v19,s[40:43],s55 offen offset:128  lds
s_add_u32 m0,s57,16128
s_nop 0
buffer_load_dwordx4 v18,s[40:43],s55 offen offset:256  lds
s_add_u32 m0,s57,17152
s_nop 0
buffer_load_dwordx4 v19,s[40:43],s55 offen offset:256  lds
v_add_u32 v26,24576,v20
v_add_u32 v27,24576,v21
v_add_u32 v28,24576,v22
T a[168:169],26,0
T a[170:171],26,512
T a[172:173],27,0
T a[174:175],27,512
T a[176:177],28,0
T a[178:179],28,512
T a[180:181],26,4096
T a[182:183],26,4608
T a[184:185],27,4096
T a[186:187],27,4608
T a[188:189],28,4096
T a[190:191],28,4608
s_waitcnt vmcnt(24) lgkmcnt(0)
s_barrier
s_add_u32 s55,s50,5963776
s_add_u32 m0,s57,24576
s_nop 0
buffer_load_dwordx4 v18,s[40:43],s55 offen offset:0  lds
s_add_u32 m0,s57,25600
s_nop 0
buffer_load_dwordx4 v19,s[40:43],s55 offen offset:0  lds
s_add_u32 m0,s57,32640
s_nop 0
buffer_load_dwordx4 v18,s[40:43],s55 offen offset:128  lds
s_add_u32 m0,s57,33664
s_nop 0
buffer_load_dwordx4 v19,s[40:43],s55 offen offset:128  lds
s_add_u32 m0,s57,40704
s_nop 0
buffer_load_dwordx4 v18,s[40:43],s55 offen offset:256  lds
s_add_u32 m0,s57,41728
s_nop 0
buffer_load_dwordx4 v19,s[40:43],s55 offen offset:256  lds
v_add_u32 v26,49152,v20
v_add_u32 v27,49152,v21
v_add_u32 v28,49152,v22
T a[192:193],26,0
T a[194:195],26,512
T a[196:197],27,0
T a[198:199],27,512
T a[200:201],28,0
T a[202:203],28,512
T a[204:205],26,4096
T a[206:207],26,4608
T a[208:209],27,4096
T a[210:211],27,4608
T a[212:213],28,4096
T a[214:215],28,4608
s_waitcnt vmcnt(24) lgkmcnt(0)
s_barrier
s_add_u32 s55,s50,6422528
s_add_u32 m0,s57,49152
s_nop 0
buffer_load_dwordx4 v18,s[40:43],s55 offen offset:0  lds
s_add_u32 m0,s57,50176
s_nop 0
buffer_load_dwordx4 v19,s[40:43],s55 offen offset:0  lds
s_add_u32 m0,s57,57216
s_nop 0
buffer_load_dwordx4 v18,s[40:43],s55 offen offset:128  lds
s_add_u32 m0,s57,58240
s_nop 0
buffer_load_dwordx4 v19,s[40:43],s55 offen offset:128  lds
s_add_u32 m0,s57,65280
s_nop 0
buffer_load_dwordx4 v18,s[40:43],s55 offen offset:256  lds
s_add_u32 m0,s57,66304
s_nop 0
buffer_load_dwordx4 v19,s[40:43],s55 offen offset:256  lds
v_add_u32 v26,73728,v20
v_add_u32 v27,73728,v21
v_add_u32 v28,73728,v22
T a[216:217],26,0
T a[218:219],26,512
T a[220:221],27,0
T a[222:223],27,512
T a[224:225],28,0
)"
R"(T a[226:227],28,512
T a[228:229],26,4096
T a[230:231],26,4608
T a[232:233],27,4096
T a[234:235],27,4608
T a[236:237],28,4096
T a[238:239],28,4608
s_waitcnt vmcnt(24) lgkmcnt(0)
s_barrier
s_add_u32 s55,s50,6881280
s_add_u32 m0,s57,73728
s_nop 0
buffer_load_dwordx4 v18,s[40:43],s55 offen offset:0  lds
s_add_u32 m0,s57,74752
s_nop 0
buffer_load_dwordx4 v19,s[40:43],s55 offen offset:0  lds
s_add_u32 m0,s57,81792
s_nop 0
buffer_load_dwordx4 v18,s[40:43],s55 offen offset:128  lds
s_add_u32 m0,s57,82816
s_nop 0
buffer_load_dwordx4 v19,s[40:43],s55 offen offset:128  lds
s_add_u32 m0,s57,89856
s_nop 0
buffer_load_dwordx4 v18,s[40:43],s55 offen offset:256  lds
s_add_u32 m0,s57,90880
s_nop 0
buffer_load_dwordx4 v19,s[40:43],s55 offen offset:256  lds
v_add_u32 v26,98304,v20
v_add_u32 v27,98304,v21
v_add_u32 v28,98304,v22
T a[240:241],26,0
T a[242:243],26,512
T a[244:245],27,0
T a[246:247],27,512
T a[248:249],28,0
T a[250:251],28,512
T a[252:253],26,4096
T a[254:255],26,4608
T v[128:129],27,4096
T v[130:131],27,4608
T v[132:133],28,4096
T v[134:135],28,4608
s_waitcnt vmcnt(24) lgkmcnt(0)
s_barrier
v_add_u32 v26,122880,v20
v_add_u32 v27,122880,v21
v_add_u32 v28,122880,v22
T v[136:137],26,0
T v[138:139],26,512
T v[140:141],27,0
T v[142:143],27,512
T v[144:145],28,0
T v[146:147],28,512
T v[148:149],26,4096
T v[150:151],26,4608
T v[152:153],27,4096
T v[154:155],27,4608
T v[156:157],28,4096
T v[158:159],28,4608
s_waitcnt vmcnt(18) lgkmcnt(0)
s_barrier
v_add_u32 v26,0,v20
v_add_u32 v27,0,v21
v_add_u32 v28,0,v22
T v[160:161],26,0
T v[162:163],26,512
T v[164:165],27,0
T v[166:167],27,512
T v[168:169],28,0
T v[170:171],28,512
T v[172:173],26,4096
T v[174:175],26,4608
T v[176:177],27,4096
T v[178:179],27,4608
T v[180:181],28,4096
T v[182:183],28,4608
s_waitcnt vmcnt(12) lgkmcnt(0)
s_barrier
v_add_u32 v26,24576,v20
v_add_u32 v27,24576,v21
v_add_u32 v28,24576,v22
T v[184:185],26,0
T v[186:187],26,512
T v[188:189],27,0
T v[190:191],27,512
T v[192:193],28,0
T v[194:195],28,512
T v[196:197],26,4096
T v[198:199],26,4608
T v[200:201],27,4096
T v[202:203],27,4608
T v[204:205],28,4096
T v[206:207],28,4608
s_waitcnt vmcnt(6) lgkmcnt(0)
s_barrier
v_add_u32 v26,49152,v20
v_add_u32 v27,49152,v21
v_add_u32 v28,49152,v22
T v[208:209],26,0
T v[210:211],26,512
T v[212:213],27,0
T v[214:215],27,512
T v[216:217],28,0
T v[218:219],28,512
T v[220:221],26,4096
T v[222:223],26,4608
T v[224:225],27,4096
T v[226:227],27,4608
T v[228:229],28,4096
T v[230:231],28,4608
s_waitcnt vmcnt(0) lgkmcnt(0)
s_barrier
v_add_u32 v26,73728,v20
v_add_u32 v27,73728,v21
v_add_u32 v28,73728,v22
T v[232:233],26,0
T v[234:235],26,512
T v[236:237],27,0
T v[238:239],27,512
T v[240:241],28,0
T v[242:243],28,512
T v[244:245],26,4096
T v[246:247],26,4608
T v[248:249],27,4096
T v[250:251],27,4608
T v[252:253],28,4096
T v[254:255],28,4608
L 0
s_barrier
v_mov_b32 v42,0x3f803f80
v_mov_b32 v43,0x3f803f80
v_mov_b32 v44,0x3f803f80
v_mov_b32 v45,0x3f803f80
s_nop 1
v_mfma_f32_16x16x32_bf16 v[82:85],a[0:3],v[42:45],0
v_mfma_f32_16x16x32_bf16 v[86:89],a[4:7],v[42:45],0
v_mfma_f32_16x16x32_bf16 v[90:93],a[8:11],v[42:45],0
M 82,a[12:15],42
M 86,a[16:19],42
M 90,a[20:23],42
M 82,a[24:27],42
M 86,a[28:31],42
M 90,a[32:35],42
M 82,a[36:39],42
M 86,a[40:43],42
M 90,a[44:47],42
M 82,a[48:51],42
M 86,a[52:55],42
M 90,a[56:59],42
M 82,a[60:63],42
M 86,a[64:67],42
M 90,a[68:71],42
M 82,a[72:75],42
M 86,a[76:79],42
M 90,a[80:83],42
M 82,a[84:87],42
M 86,a[88:91],42
M 90,a[92:95],42
M 82,a[96:99],42
M 86,a[100:103],42
M 90,a[104:107],42
M 82,a[108:111],42
M 86,a[112:115],42
M 90,a[116:119],42
M 82,a[120:123],42
M 86,a[124:127],42
M 90,a[128:131],42
M 82,a[132:135],42
M 86,a[136:139],42
M 90,a[140:143],42
M 82,a[144:147],42
M 86,a[148:151],42
M 90,a[152:155],42
M 82,a[156:159],42
M 86,a[160:163],42
M 90,a[164:167],42
M 82,a[168:171],42
M 86,a[172:175],42
M 90,a[176:179],42
M 82,a[180:183],42
M 86,a[184:187],42
M 90,a[188:191],42
M 82,a[192:195],42
M 86,a[196:199],42
M 90,a[200:203],42
M 82,a[204:207],42
M 86,a[208:211],42
M 90,a[212:215],42
M 82,a[216:219],42
M 86,a[220:223],42
M 90,a[224:227],42
M 82,a[228:231],42
M 86,a[232:235],42
M 90,a[236:239],42
M 82,a[240:243],42
M 86,a[244:247],42
M 90,a[248:251],42
M 82,a[252:255],42
M 86,v[128:131],42
M 90,v[132:135],42
M 82,v[136:139],42
M 86,v[140:143],42
M 90,v[144:147],42
M 82,v[148:151],42
M 86,v[152:155],42
M 90,v[156:159],42
M 82,v[160:163],42
M 86,v[164:167],42
M 90,v[168:171],42
M 82,v[172:175],42
M 86,v[176:179],42
M 90,v[180:183],42
M 82,v[184:187],42
M 86,v[188:191],42
M 90,v[192:195],42
M 82,v[196:199],42
M 86,v[200:203],42
M 90,v[204:207],42
M 82,v[208:211],42
M 86,v[212:215],42
M 90,v[216:219],42
M 82,v[220:223],42
M 86,v[224:227],42
M 90,v[228:231],42
M 82,v[232:235],42
M 86,v[236:239],42
M 90,v[240:243],42
M 82,v[244:247],42
M 86,v[248:251],42
M 90,v[252:255],42
v_mov_b32 v4,s53
s_mov_b32 s82,0
s_mov_b32 s80,0
s_mov_b32 s54,s45
s_mul_i32 s57,s44,4096
s_mul_i32 s55,s45,32768
s_add_u32 s55,s55,s57
s_add_u32 s55,s55,31457280
s_mul_i32 s57,s44,4096
s_mul_i32 s60,s46,32768
s_mul_i32 s61,s46,1024
s_mul_i32 s62,s46,114688
s_mul_i32 s63,s46,2560
s_mul_i32 s64,s46,1024
s_lshl_b32 s59,s44,8
s_add_u32 s59,s59,131072
s_mul_i32 s58,s45,1024
s_add_u32 s58,s58,109613056
s_mul_i32 s65,s45,114688
s_add_u32 s65,s65,s47
s_mul_i32 s68,s45,2560
s_add_u32 s68,s68,s48
s_mul_i32 s73,s45,1024
s_add_u32 s73,s73,107479040
s_mul_i32 s74,s45,32768
s_add_u32 s74,s74,s49
s_mul_i32 s75,s45,1024
s_add_u32 s75,s75,107479040
s_mov_b32 s66,0x7fff0000
s_mov_b32 s67,0x7fff0000
s_mov_b32 s69,0x7fff0000
s_mov_b32 s76,0x7fff0000
s_mov_b32 s77,0x7fff0000
s_mov_b32 s70,0x7fff0000
s_mov_b32 s71,0x7fff0000
s_mov_b32 s72,0x7fff0000
v_mov_b32 v1,v0
v_add_u32 v2,16384,v0
s_mov_b32 s84,16384
s_mov_b32 s86,0x3a800000
s_mov_b32 s87,0x3727c5ac
s_mov_b32 s88,0x409d30fc
s_mov_b32 s89,0xbeeff558
s_lshr_b32 s85,s44,1
s_mul_i32 s85,s85,127
v_lshlrev_b32 v8,22,v0
v_and_b32 v8,0x80000000,v8
v_or_b32 v8,v8,v5
v_add_u32 v2,32768,v0
s_lshl_b32 s84,s57,1
s_add_u32 s84,s84,32768
s_getpc_b64 s[40:41]
Lg3_pc_%=:
s_lshl_b32 s78,s44,13
v_lshlrev_b32 v94,3,v0
v_add_u32 v94,s78,v94
v_min_u32 v94,Lcode_g3_e-Lg3_pc_%=-4,v94
s_mov_b32 m0,163584
s_nop 0
global_load_lds_dword v94,s[40:41]
v_add_u32 v94,32768,v94
v_min_u32 v94,Lcode_g3_e-Lg3_pc_%=-4,v94
global_load_lds_dword v94,s[40:41]
s_add_u32 m0,s59,4096
s_nop 0
buffer_load_dword v8,s[36:39],s58 offen offset:0 sc1 lds
buffer_store_dword v4,v3,s[36:39],s71 offen offset:0 sc0 sc1
s_add_u32 m0,s59,5120
s_nop 0
buffer_load_dword v8,s[36:39],s58 offen offset:0 sc1 lds
buffer_store_dword v4,v3,s[36:39],s71 offen offset:0 sc0 sc1
s_mov_b32 s83,0
Lg3_poll_%=:
s_add_u32 s78,s83,2
s_and_b32 s78,s78,3
s_lshl_b32 s78,s78,10
s_add_u32 s78,s78,4096
s_add_u32 m0,s59,s78
s_nop 0
buffer_load_dword v8,s[36:39],s58 offen offset:0 sc1 lds
s_and_b32 s78,s83,3
s_lshl_b32 s78,s78,10
v_add_u32 v94,s78,v6
s_waitcnt vmcnt(4)
s_barrier
ds_read_b32 v7,v94 offset:4096
buffer_store_dword v4,v3,s[36:39],s70 offen offset:0 sc0 sc1
s_mov_b32 s70,s71
s_mov_b32 s71,s72
s_mov_b32 s72,0x7fff0000
s_add_u32 s83,s83,1
s_add_u32 s82,s82,1
L 0
v_cmp_eq_u32 vcc,s53,v7
s_nop 0
s_cmp_eq_u32 vcc_lo,-1
s_cbranch_scc1 Lg3_go_%=
s_cmp_lt_u32 s82,20000
s_cbranch_scc1 Lg3_poll_%=
Lg3_go_%=:
buffer_load_dword v114,v11,s[36:39],s73 offen offset:0 sc1
s_add_u32 s73,s73,s64
s_add_u32 m0,s57,0
s_add_u32 s56,s55,0
D 0,56,0
D 0,56,1024
D 0,56,2048
D 0,56,3072
s_add_u32 s58,s58,s61
s_add_u32 m0,s59,2048
s_nop 0
buffer_load_dword v8,s[36:39],s58 offen offset:0 sc1 lds
s_add_u32 m0,s57,16384
s_add_u32 s56,s55,16384
D 0,56,0
D 0,56,1024
D 0,56,2048
D 0,56,3072
s_add_u32 s55,s55,s60
s_sub_u32 s57,s84,s57
s_add_u32 s54,s54,s46
s_waitcnt vmcnt(4)
s_barrier
buffer_store_dword v4,v3,s[36:39],s70 offen offset:0 sc0 sc1
s_min_u32 s71,s71,s72
buffer_store_dword v4,v3,s[36:39],s71 offen offset:0 sc0 sc1
s_mov_b32 s70,0x7fff0000
s_mov_b32 s71,0x7fff0000
s_mov_b32 s72,0x7fff0000
s_cmp_eq_u32 s80,1
buffer_store_dword v4,v3,s[36:39],s71 offen offset:0 sc0 sc1
s_cbranch_scc1 Lg3_st1_%=
s_branch Lg3_st0_%=
Lg3_gox_%=:
s_add_u32 m0,s59,2048
s_nop 0
buffer_load_dword v8,s[36:39],s58 offen offset:0 sc1 lds
s_waitcnt vmcnt(0)
s_barrier
buffer_store_dword v4,v3,s[36:39],s70 offen offset:0 sc0 sc1
s_min_u32 s71,s71,s72
buffer_store_dword v4,v3,s[36:39],s71 offen offset:0 sc0 sc1
s_mov_b32 s70,0x7fff0000
s_mov_b32 s71,0x7fff0000
s_mov_b32 s72,0x7fff0000
s_cmp_eq_u32 s80,1
s_cbranch_scc1 Lg3_st1_%=
Lg3_st0_%=:
ds_read_b32 v7,v6 offset:2048
R 42,1,0
R 46,1,1024
R 50,1,2048
R 54,1,3072
R 58,1,4096
L 5
v_cmp_eq_u32 vcc,s53,v7
L 3
s_cmp_eq_u32 vcc_lo,-1
s_cbranch_scc1 Lg3_F0_%=
s_branch Lg3_L0_%=
Lg3_st1_%=:
ds_read_b32 v7,v6 offset:2048
R 42,2,0
R 46,2,1024
R 50,2,2048
R 54,2,3072
R 58,2,4096
L 5
v_cmp_eq_u32 vcc,s53,v7
L 3
s_cmp_eq_u32 vcc_lo,-1
s_cbranch_scc1 Lg3_F1_%=
s_branch Lg3_L1_%=
Lg3_F1_%=:
s_cmp_eq_u32 s44,1
s_cbranch_scc1 Lg3_F1w1_%=
s_cmp_eq_u32 s44,2
s_cbranch_scc1 Lg3_F1w2_%=
s_cmp_eq_u32 s44,3
s_cbranch_scc1 Lg3_F1w3_%=
s_branch Lg3_F1w0_%=
Lg3_F0_%=:
s_cmp_eq_u32 s44,1
s_cbranch_scc1 Lg3_F0w1_%=
s_cmp_eq_u32 s44,2
s_cbranch_scc1 Lg3_F0w2_%=
s_cmp_eq_u32 s44,3
s_cbranch_scc1 Lg3_F0w3_%=
Lg3_F0w0_%=:
v_mfma_f32_16x16x32_bf16 v[26:29],a[8:11],v[42:45],0
s_add_u32 m0,s57,0
R 62,1,5120
v_add_f32_dpp v115,v114,v114 quad_perm:[1,0,3,2] row_mask:0xf bank_mask:0xf
M 26,a[20:23],46
buffer_load_dword v114,v11,s[36:39],s73 offen offset:0 sc1
v_mfma_f32_16x16x32_bf16 v[22:25],a[4:7],v[42:45],0
s_add_u32 s56,s55,0
v_add_f32_dpp v115,v115,v115 quad_perm:[2,3,0,1] row_mask:0xf bank_mask:0xf
v_fma_f32 v30,-v78,v82,v30
M 22,a[16:19],46
s_add_u32 s58,s58,s61
R 66,1,6144
v_fma_f32 v31,-v78,v83,v31
v_mfma_f32_16x16x32_bf16 v[18:21],a[0:3],v[42:45],0
D 0,56,0
M 18,a[12:15],46
L 3
v_add_f32_dpp v118,v115,v115 row_half_mirror row_mask:0xf bank_mask:0xf
v_fma_f32 v32,-v78,v84,v32
M 18,a[24:27],50
s_mov_b32 s66,s65
R 70,1,7168
v_fma_f32 v33,-v78,v85,v33
M 18,a[36:39],54
s_add_u32 s65,s65,s62
v_mov_b32_dpp v115,v118 row_ror:8 row_mask:0xf bank_mask:0xf
v_fma_f32 v115,-v118,v118,v115
M 22,a[28:31],50
s_add_u32 s54,s54,s46
v_max_f32 v115,s87,v115
v_mul_f32 v118,v15,v118
M 22,a[40:43],54
s_add_u32 s73,s73,s64
R 42,1,8192
v_mul_f32 v30,v79,v30
M 26,a[32:35],50
v_rsq_f32 v116,v115
M 26,a[44:47],54
L 3
D 0,56,1024
M 26,a[56:59],58
R 46,1,9216
v_fma_f32 v118,v116,v16,v118
M 26,a[68:71],62
ds_write_b32 v12,v118 offset:0
M 22,a[52:55],58
v_mul_f32 v31,v79,v31
v_mul_f32 v32,v79,v32
M 22,a[64:67],62
R 50,1,10240
v_mul_f32 v33,v79,v33
M 18,a[48:51],58
v_fma_f32 v94,v30,v30,s88
v_fma_f32 v95,v31,v31,s88
M 18,a[60:63],62
L 4
v_fma_f32 v96,v32,v32,s88
v_fma_f32 v97,v33,v33,s88
M 18,a[72:75],66
R 54,1,11264
v_mul_f32 v94,v30,v94
M 18,a[84:87],70
v_exp_f32 v94,v94
M 22,a[76:79],66
v_mul_f32 v95,v31,v95
v_mul_f32 v96,v32,v96
M 22,a[88:91],70
R 58,1,12288
v_mul_f32 v97,v33,v97
M 26,a[80:83],66
D 0,56,2048
M 26,a[92:95],70
L 4
v_exp_f32 v95,v95
M 26,a[104:107],42
R 62,1,13312
v_fma_f32 v94,v94,s89,s89
M 26,a[116:119],46
v_exp_f32 v96,v96
)"
R"(M 22,a[100:103],42
v_exp_f32 v97,v97
M 22,a[112:115],46
R 66,1,14336
v_fma_f32 v95,v95,s89,s89
M 18,a[96:99],42
v_rcp_f32 v94,v94
M 18,a[108:111],46
L 3
v_rcp_f32 v95,v95
M 18,a[120:123],50
R 70,1,15360
v_fma_f32 v96,v96,s89,s89
M 18,a[132:135],54
s_waitcnt vmcnt(7)
D 0,56,3072
M 22,a[124:127],50
s_barrier
v_rcp_f32 v96,v96
M 22,a[136:139],54
s_add_u32 m0,s59,0
R 42,1,16384
v_fma_f32 v97,v97,s89,s89
M 26,a[128:131],50
s_add_u32 s56,s55,16384
v_rcp_f32 v97,v97
M 26,a[140:143],54
L 3
v_mul_f32 v30,v30,v94
v_mul_f32 v31,v31,v95
M 26,a[152:155],58
s_add_u32 s55,s55,s60
R 46,1,17408
v_mul_f32 v32,v32,v96
M 26,a[164:167],62
v_mul_f32 v33,v33,v97
v_cvt_pk_bf16_f32 v106,v30,v31
M 22,a[148:151],58
v_cvt_pk_bf16_f32 v107,v32,v33
v_fma_f32 v34,-v78,v86,v34
M 22,a[160:163],62
R 50,1,18432
v_fma_f32 v35,-v78,v87,v35
v_fma_f32 v36,-v78,v88,v36
M 18,a[144:147],58
buffer_load_dword v8,s[36:39],s58 offen offset:0 sc1 lds
M 18,a[156:159],62
L 3
v_fma_f32 v37,-v78,v89,v37
v_mul_f32 v34,v79,v34
M 18,a[168:171],66
s_add_u32 m0,s57,16384
R 54,1,19456
v_mul_f32 v35,v79,v35
M 18,a[180:183],70
s_sub_u32 s57,s84,s57
v_mul_f32 v36,v79,v36
v_mul_f32 v37,v79,v37
M 22,a[172:175],66
v_fma_f32 v98,v34,v34,s88
v_fma_f32 v99,v35,v35,s88
M 22,a[184:187],70
R 58,1,20480
v_fma_f32 v100,v36,v36,s88
M 26,a[176:179],66
D 0,56,0
M 26,a[188:191],70
L 3
v_fma_f32 v101,v37,v37,s88
v_mul_f32 v98,v34,v98
M 26,a[200:203],42
R 62,1,21504
v_mul_f32 v99,v35,v99
M 26,a[212:215],46
v_exp_f32 v98,v98
M 22,a[196:199],42
v_exp_f32 v99,v99
M 22,a[208:211],46
R 66,1,22528
v_mul_f32 v100,v36,v100
M 18,a[192:195],42
v_exp_f32 v100,v100
M 18,a[204:207],46
L 3
v_mul_f32 v101,v37,v101
v_fma_f32 v98,v98,s89,s89
M 18,a[216:219],50
R 70,1,23552
v_fma_f32 v99,v99,s89,s89
M 18,a[228:231],54
D 0,56,1024
M 22,a[220:223],50
v_exp_f32 v101,v101
M 22,a[232:235],54
R 42,1,24576
v_fma_f32 v100,v100,s89,s89
M 26,a[224:227],50
v_rcp_f32 v98,v98
M 26,a[236:239],54
L 3
v_rcp_f32 v99,v99
M 26,a[248:251],58
R 46,1,25600
v_fma_f32 v101,v101,s89,s89
M 26,v[132:135],62
v_rcp_f32 v100,v100
M 22,a[244:247],58
v_rcp_f32 v101,v101
M 22,v[128:131],62
R 50,1,26624
v_mul_f32 v34,v34,v98
M 18,a[240:243],58
v_mul_f32 v35,v35,v99
v_mul_f32 v36,v36,v100
M 18,a[252:255],62
L 3
v_mul_f32 v37,v37,v101
v_cvt_pk_bf16_f32 v108,v34,v35
M 18,v[136:139],66
R 54,1,27648
v_cvt_pk_bf16_f32 v109,v36,v37
M 18,v[148:151],70
D 0,56,2048
M 22,v[140:143],66
v_fma_f32 v38,-v78,v90,v38
v_fma_f32 v39,-v78,v91,v39
M 22,v[152:155],70
R 58,1,28672
v_fma_f32 v40,-v78,v92,v40
M 26,v[144:147],66
v_fma_f32 v41,-v78,v93,v41
v_mul_f32 v38,v79,v38
M 26,v[156:159],70
L 3
v_mul_f32 v39,v79,v39
v_mul_f32 v40,v79,v40
M 26,v[168:171],42
R 62,1,29696
v_mul_f32 v41,v79,v41
M 26,v[180:183],46
v_fma_f32 v102,v38,v38,s88
v_fma_f32 v103,v39,v39,s88
M 22,v[164:167],42
v_fma_f32 v104,v40,v40,s88
v_fma_f32 v105,v41,v41,s88
M 22,v[176:179],46
R 66,1,30720
v_mul_f32 v102,v38,v102
M 18,v[160:163],42
v_exp_f32 v102,v102
M 18,v[172:175],46
L 3
D 0,56,3072
M 18,v[184:187],50
s_nop 0
R 70,1,31744
v_mul_f32 v103,v39,v103
M 18,v[196:199],54
s_waitcnt vmcnt(4)
v_exp_f32 v103,v103
v_mul_f32 v104,v40,v104
M 22,v[188:191],50
s_barrier
buffer_store_dword v4,v3,s[36:39],s70 offen offset:0 sc0 sc1
M 22,v[200:203],54
s_mov_b32 s70,s69
R 42,2,0
ds_read_b32 v7,v6 offset:0
M 26,v[192:195],50
s_mov_b32 s69,s68
ds_read_b64 v[74:75],v13 offset:0
v_mul_f32 v105,v41,v105
M 26,v[204:207],54
L 5
v_exp_f32 v104,v104
M 26,v[216:219],58
s_add_u32 s68,s68,s63
R 46,2,1024
v_fma_f32 v102,v102,s89,s89
M 26,v[228:231],62
v_exp_f32 v105,v105
M 22,v[212:215],58
buffer_store_dwordx4 v[106:109],v0,s[36:39],s67 offen offset:0 sc0 sc1
v_fma_f32 v103,v103,s89,s89
M 22,v[224:227],62
L 2
R 50,2,2048
v_fma_f32 v104,v104,s89,s89
M 18,v[208:211],58
v_rcp_f32 v102,v102
M 18,v[220:223],62
s_nop 0
v_fma_f32 v105,v105,s89,s89
v_mul_f32 v38,v38,v102
M 18,v[232:235],66
R 54,2,3072
v_cmp_eq_u32 vcc,s53,v7
M 18,v[244:247],70
v_rcp_f32 v103,v103
M 22,v[236:239],66
s_cmp_eq_u32 vcc_lo,-1
v_rcp_f32 v104,v104
v_rcp_f32 v105,v105
M 22,v[248:251],70
R 58,2,4096
v_mul_f32 v39,v39,v103
v_mul_f32 v40,v40,v104
v_mul_f32 v41,v41,v105
v_cvt_pk_bf16_f32 v110,v38,v39
v_cvt_pk_bf16_f32 v111,v40,v41
M 26,v[240:243],66
L 3
buffer_store_dwordx2 v[110:111],v10,s[36:39],s67 offen offset:0 sc0 sc1
M 26,v[252:255],70
s_cbranch_scc0 Lg3_K1_%=
Lg3_F1w0_%=:
v_mfma_f32_16x16x32_bf16 v[38:41],a[8:11],v[42:45],0
s_add_u32 m0,s57,0
R 62,2,5120
v_add_f32_dpp v115,v114,v114 quad_perm:[1,0,3,2] row_mask:0xf bank_mask:0xf
M 38,a[20:23],46
buffer_load_dword v114,v11,s[36:39],s73 offen offset:0 sc1
v_mfma_f32_16x16x32_bf16 v[34:37],a[4:7],v[42:45],0
s_add_u32 s56,s55,0
v_add_f32_dpp v115,v115,v115 quad_perm:[2,3,0,1] row_mask:0xf bank_mask:0xf
v_fma_f32 v18,-v74,v82,v18
M 34,a[16:19],46
s_add_u32 s58,s58,s61
R 66,2,6144
v_fma_f32 v19,-v74,v83,v19
v_mfma_f32_16x16x32_bf16 v[30:33],a[0:3],v[42:45],0
D 0,56,0
M 30,a[12:15],46
L 3
v_add_f32_dpp v118,v115,v115 row_half_mirror row_mask:0xf bank_mask:0xf
v_fma_f32 v20,-v74,v84,v20
M 30,a[24:27],50
s_mov_b32 s67,s65
R 70,2,7168
v_fma_f32 v21,-v74,v85,v21
M 30,a[36:39],54
s_add_u32 s65,s65,s62
v_mov_b32_dpp v115,v118 row_ror:8 row_mask:0xf bank_mask:0xf
v_fma_f32 v115,-v118,v118,v115
M 34,a[28:31],50
s_add_u32 s54,s54,s46
v_max_f32 v115,s87,v115
v_mul_f32 v118,v15,v118
M 34,a[40:43],54
s_add_u32 s73,s73,s64
R 42,2,8192
v_mul_f32 v18,v75,v18
M 38,a[32:35],50
v_rsq_f32 v116,v115
M 38,a[44:47],54
L 3
D 0,56,1024
M 38,a[56:59],58
R 46,2,9216
v_fma_f32 v118,v116,v16,v118
M 38,a[68:71],62
ds_write_b32 v12,v118 offset:2048
M 34,a[52:55],58
v_mul_f32 v19,v75,v19
v_mul_f32 v20,v75,v20
M 34,a[64:67],62
R 50,2,10240
v_mul_f32 v21,v75,v21
M 30,a[48:51],58
v_fma_f32 v94,v18,v18,s88
v_fma_f32 v95,v19,v19,s88
M 30,a[60:63],62
L 4
v_fma_f32 v96,v20,v20,s88
v_fma_f32 v97,v21,v21,s88
M 30,a[72:75],66
R 54,2,11264
v_mul_f32 v94,v18,v94
M 30,a[84:87],70
v_exp_f32 v94,v94
M 34,a[76:79],66
v_mul_f32 v95,v19,v95
v_mul_f32 v96,v20,v96
M 34,a[88:91],70
R 58,2,12288
v_mul_f32 v97,v21,v97
M 38,a[80:83],66
D 0,56,2048
M 38,a[92:95],70
L 4
v_exp_f32 v95,v95
M 38,a[104:107],42
R 62,2,13312
v_fma_f32 v94,v94,s89,s89
M 38,a[116:119],46
v_exp_f32 v96,v96
M 34,a[100:103],42
v_exp_f32 v97,v97
M 34,a[112:115],46
R 66,2,14336
v_fma_f32 v95,v95,s89,s89
M 30,a[96:99],42
v_rcp_f32 v94,v94
M 30,a[108:111],46
L 3
v_rcp_f32 v95,v95
M 30,a[120:123],50
R 70,2,15360
v_fma_f32 v96,v96,s89,s89
M 30,a[132:135],54
s_waitcnt vmcnt(7)
D 0,56,3072
M 34,a[124:127],50
s_barrier
v_rcp_f32 v96,v96
M 34,a[136:139],54
s_add_u32 m0,s59,1024
R 42,2,16384
v_fma_f32 v97,v97,s89,s89
M 38,a[128:131],50
s_add_u32 s56,s55,16384
v_rcp_f32 v97,v97
M 38,a[140:143],54
L 3
v_mul_f32 v18,v18,v94
v_mul_f32 v19,v19,v95
M 38,a[152:155],58
s_add_u32 s55,s55,s60
R 46,2,17408
v_mul_f32 v20,v20,v96
M 38,a[164:167],62
v_mul_f32 v21,v21,v97
v_cvt_pk_bf16_f32 v106,v18,v19
M 34,a[148:151],58
v_cvt_pk_bf16_f32 v107,v20,v21
v_fma_f32 v22,-v74,v86,v22
M 34,a[160:163],62
R 50,2,18432
v_fma_f32 v23,-v74,v87,v23
v_fma_f32 v24,-v74,v88,v24
M 30,a[144:147],58
buffer_load_dword v8,s[36:39],s58 offen offset:0 sc1 lds
M 30,a[156:159],62
L 3
v_fma_f32 v25,-v74,v89,v25
v_mul_f32 v22,v75,v22
M 30,a[168:171],66
s_add_u32 m0,s57,16384
R 54,2,19456
v_mul_f32 v23,v75,v23
M 30,a[180:183],70
s_sub_u32 s57,s84,s57
v_mul_f32 v24,v75,v24
v_mul_f32 v25,v75,v25
M 34,a[172:175],66
v_fma_f32 v98,v22,v22,s88
v_fma_f32 v99,v23,v23,s88
M 34,a[184:187],70
R 58,2,20480
v_fma_f32 v100,v24,v24,s88
M 38,a[176:179],66
D 0,56,0
M 38,a[188:191],70
L 3
v_fma_f32 v101,v25,v25,s88
v_mul_f32 v98,v22,v98
M 38,a[200:203],42
R 62,2,21504
v_mul_f32 v99,v23,v99
M 38,a[212:215],46
v_exp_f32 v98,v98
M 34,a[196:199],42
v_exp_f32 v99,v99
M 34,a[208:211],46
R 66,2,22528
v_mul_f32 v100,v24,v100
M 30,a[192:195],42
v_exp_f32 v100,v100
M 30,a[204:207],46
L 3
v_mul_f32 v101,v25,v101
v_fma_f32 v98,v98,s89,s89
M 30,a[216:219],50
R 70,2,23552
v_fma_f32 v99,v99,s89,s89
M 30,a[228:231],54
D 0,56,1024
M 34,a[220:223],50
v_exp_f32 v101,v101
M 34,a[232:235],54
R 42,2,24576
v_fma_f32 v100,v100,s89,s89
M 38,a[224:227],50
v_rcp_f32 v98,v98
M 38,a[236:239],54
L 3
v_rcp_f32 v99,v99
M 38,a[248:251],58
R 46,2,25600
v_fma_f32 v101,v101,s89,s89
M 38,v[132:135],62
v_rcp_f32 v100,v100
M 34,a[244:247],58
v_rcp_f32 v101,v101
M 34,v[128:131],62
R 50,2,26624
v_mul_f32 v22,v22,v98
M 30,a[240:243],58
v_mul_f32 v23,v23,v99
v_mul_f32 v24,v24,v100
M 30,a[252:255],62
L 3
v_mul_f32 v25,v25,v101
v_cvt_pk_bf16_f32 v108,v22,v23
M 30,v[136:139],66
R 54,2,27648
v_cvt_pk_bf16_f32 v109,v24,v25
M 30,v[148:151],70
D 0,56,2048
M 34,v[140:143],66
v_fma_f32 v26,-v74,v90,v26
v_fma_f32 v27,-v74,v91,v27
M 34,v[152:155],70
R 58,2,28672
v_fma_f32 v28,-v74,v92,v28
M 38,v[144:147],66
v_fma_f32 v29,-v74,v93,v29
v_mul_f32 v26,v75,v26
M 38,v[156:159],70
L 3
v_mul_f32 v27,v75,v27
v_mul_f32 v28,v75,v28
M 38,v[168:171],42
R 62,2,29696
v_mul_f32 v29,v75,v29
M 38,v[180:183],46
v_fma_f32 v102,v26,v26,s88
v_fma_f32 v103,v27,v27,s88
M 34,v[164:167],42
v_fma_f32 v104,v28,v28,s88
v_fma_f32 v105,v29,v29,s88
M 34,v[176:179],46
R 66,2,30720
v_mul_f32 v102,v26,v102
M 30,v[160:163],42
v_exp_f32 v102,v102
M 30,v[172:175],46
L 3
D 0,56,3072
M 30,v[184:187],50
s_nop 0
R 70,2,31744
v_mul_f32 v103,v27,v103
M 30,v[196:199],54
s_waitcnt vmcnt(4)
v_exp_f32 v103,v103
v_mul_f32 v104,v28,v104
M 34,v[188:191],50
s_barrier
buffer_store_dword v4,v3,s[36:39],s70 offen offset:0 sc0 sc1
M 34,v[200:203],54
s_mov_b32 s70,s69
R 42,1,0
ds_read_b32 v7,v6 offset:1024
M 38,v[192:195],50
s_mov_b32 s69,s68
ds_read_b64 v[78:79],v13 offset:2048
v_mul_f32 v105,v29,v105
M 38,v[204:207],54
L 5
v_exp_f32 v104,v104
M 38,v[216:219],58
s_add_u32 s68,s68,s63
R 46,1,1024
v_fma_f32 v102,v102,s89,s89
M 38,v[228:231],62
v_exp_f32 v105,v105
M 34,v[212:215],58
buffer_store_dwordx4 v[106:109],v0,s[36:39],s66 offen offset:0 sc0 sc1
v_fma_f32 v103,v103,s89,s89
M 34,v[224:227],62
L 2
R 50,1,2048
v_fma_f32 v104,v104,s89,s89
M 30,v[208:211],58
v_rcp_f32 v102,v102
M 30,v[220:223],62
s_nop 0
v_fma_f32 v105,v105,s89,s89
v_mul_f32 v26,v26,v102
M 30,v[232:235],66
R 54,1,3072
v_cmp_eq_u32 vcc,s53,v7
M 30,v[244:247],70
v_rcp_f32 v103,v103
M 34,v[236:239],66
s_cmp_eq_u32 vcc_lo,-1
v_rcp_f32 v104,v104
v_rcp_f32 v105,v105
M 34,v[248:251],70
)"
R"(R 58,1,4096
v_mul_f32 v27,v27,v103
v_mul_f32 v28,v28,v104
v_mul_f32 v29,v29,v105
v_cvt_pk_bf16_f32 v110,v26,v27
v_cvt_pk_bf16_f32 v111,v28,v29
M 38,v[240:243],66
L 3
buffer_store_dwordx2 v[110:111],v10,s[36:39],s66 offen offset:0 sc0 sc1
M 38,v[252:255],70
s_cbranch_scc1 Lg3_F0w0_%=
s_branch Lg3_K0_%=
Lg3_F0w1_%=:
v_mfma_f32_16x16x32_bf16 v[26:29],a[8:11],v[42:45],0
s_add_u32 m0,s57,0
R 62,1,5120
v_add_f32_dpp v115,v114,v114 quad_perm:[1,0,3,2] row_mask:0xf bank_mask:0xf
M 26,a[20:23],46
buffer_load_dword v114,v11,s[36:39],s73 offen offset:0 sc1
v_mfma_f32_16x16x32_bf16 v[22:25],a[4:7],v[42:45],0
s_add_u32 s56,s55,0
v_add_f32_dpp v115,v115,v115 quad_perm:[2,3,0,1] row_mask:0xf bank_mask:0xf
v_fma_f32 v30,-v78,v82,v30
M 22,a[16:19],46
s_add_u32 s58,s58,s61
R 66,1,6144
v_fma_f32 v31,-v78,v83,v31
v_mfma_f32_16x16x32_bf16 v[18:21],a[0:3],v[42:45],0
D 0,56,0
M 18,a[12:15],46
L 3
v_add_f32_dpp v118,v115,v115 row_half_mirror row_mask:0xf bank_mask:0xf
v_fma_f32 v32,-v78,v84,v32
M 18,a[24:27],50
s_mov_b32 s66,s65
R 70,1,7168
v_fma_f32 v33,-v78,v85,v33
M 18,a[36:39],54
s_add_u32 s65,s65,s62
v_mov_b32_dpp v115,v118 row_ror:8 row_mask:0xf bank_mask:0xf
v_fma_f32 v115,-v118,v118,v115
M 22,a[28:31],50
s_add_u32 s54,s54,s46
v_max_f32 v115,s87,v115
v_mul_f32 v118,v15,v118
M 22,a[40:43],54
s_add_u32 s73,s73,s64
R 42,1,8192
v_mul_f32 v30,v79,v30
M 26,a[32:35],50
v_rsq_f32 v116,v115
M 26,a[44:47],54
L 3
D 0,56,1024
M 26,a[56:59],58
R 46,1,9216
v_fma_f32 v118,v116,v16,v118
M 26,a[68:71],62
ds_write_b32 v12,v118 offset:0
M 22,a[52:55],58
v_mul_f32 v31,v79,v31
v_mul_f32 v32,v79,v32
M 22,a[64:67],62
R 50,1,10240
v_mul_f32 v33,v79,v33
M 18,a[48:51],58
v_fma_f32 v94,v30,v30,s88
v_fma_f32 v95,v31,v31,s88
M 18,a[60:63],62
L 4
v_fma_f32 v96,v32,v32,s88
v_fma_f32 v97,v33,v33,s88
M 18,a[72:75],66
R 54,1,11264
v_mul_f32 v94,v30,v94
M 18,a[84:87],70
v_exp_f32 v94,v94
M 22,a[76:79],66
v_mul_f32 v95,v31,v95
v_mul_f32 v96,v32,v96
M 22,a[88:91],70
R 58,1,12288
v_mul_f32 v97,v33,v97
M 26,a[80:83],66
D 0,56,2048
M 26,a[92:95],70
L 4
v_exp_f32 v95,v95
M 26,a[104:107],42
R 62,1,13312
v_fma_f32 v94,v94,s89,s89
M 26,a[116:119],46
v_exp_f32 v96,v96
M 22,a[100:103],42
v_exp_f32 v97,v97
M 22,a[112:115],46
R 66,1,14336
v_fma_f32 v95,v95,s89,s89
M 18,a[96:99],42
v_rcp_f32 v94,v94
M 18,a[108:111],46
s_waitcnt vmcnt(7) lgkmcnt(3)
v_rcp_f32 v95,v95
M 18,a[120:123],50
s_barrier
R 70,1,15360
v_fma_f32 v96,v96,s89,s89
M 18,a[132:135],54
D 0,56,3072
M 22,a[124:127],50
s_add_u32 m0,s59,0
v_rcp_f32 v96,v96
M 22,a[136:139],54
s_add_u32 s56,s55,16384
R 42,1,16384
v_fma_f32 v97,v97,s89,s89
M 26,a[128:131],50
s_add_u32 s55,s55,s60
v_rcp_f32 v97,v97
M 26,a[140:143],54
L 3
v_mul_f32 v30,v30,v94
v_mul_f32 v31,v31,v95
M 26,a[152:155],58
R 46,1,17408
v_mul_f32 v32,v32,v96
M 26,a[164:167],62
v_mul_f32 v33,v33,v97
v_cvt_pk_bf16_f32 v106,v30,v31
M 22,a[148:151],58
v_cvt_pk_bf16_f32 v107,v32,v33
v_fma_f32 v34,-v78,v86,v34
M 22,a[160:163],62
R 50,1,18432
v_fma_f32 v35,-v78,v87,v35
v_fma_f32 v36,-v78,v88,v36
M 18,a[144:147],58
buffer_load_dword v8,s[36:39],s58 offen offset:0 sc1 lds
M 18,a[156:159],62
L 3
v_fma_f32 v37,-v78,v89,v37
v_mul_f32 v34,v79,v34
M 18,a[168:171],66
s_add_u32 m0,s57,16384
R 54,1,19456
v_mul_f32 v35,v79,v35
M 18,a[180:183],70
s_sub_u32 s57,s84,s57
v_mul_f32 v36,v79,v36
v_mul_f32 v37,v79,v37
M 22,a[172:175],66
v_fma_f32 v98,v34,v34,s88
v_fma_f32 v99,v35,v35,s88
M 22,a[184:187],70
R 58,1,20480
v_fma_f32 v100,v36,v36,s88
M 26,a[176:179],66
D 0,56,0
M 26,a[188:191],70
L 3
v_fma_f32 v101,v37,v37,s88
v_mul_f32 v98,v34,v98
M 26,a[200:203],42
R 62,1,21504
v_mul_f32 v99,v35,v99
M 26,a[212:215],46
v_exp_f32 v98,v98
M 22,a[196:199],42
v_exp_f32 v99,v99
M 22,a[208:211],46
R 66,1,22528
v_mul_f32 v100,v36,v100
M 18,a[192:195],42
v_exp_f32 v100,v100
M 18,a[204:207],46
L 3
v_mul_f32 v101,v37,v101
v_fma_f32 v98,v98,s89,s89
M 18,a[216:219],50
R 70,1,23552
v_fma_f32 v99,v99,s89,s89
M 18,a[228:231],54
D 0,56,1024
M 22,a[220:223],50
v_exp_f32 v101,v101
M 22,a[232:235],54
R 42,1,24576
v_fma_f32 v100,v100,s89,s89
M 26,a[224:227],50
v_rcp_f32 v98,v98
M 26,a[236:239],54
L 3
v_rcp_f32 v99,v99
M 26,a[248:251],58
R 46,1,25600
v_fma_f32 v101,v101,s89,s89
M 26,v[132:135],62
v_rcp_f32 v100,v100
M 22,a[244:247],58
v_rcp_f32 v101,v101
M 22,v[128:131],62
R 50,1,26624
v_mul_f32 v34,v34,v98
M 18,a[240:243],58
v_mul_f32 v35,v35,v99
v_mul_f32 v36,v36,v100
M 18,a[252:255],62
L 3
v_mul_f32 v37,v37,v101
v_cvt_pk_bf16_f32 v108,v34,v35
M 18,v[136:139],66
R 54,1,27648
v_cvt_pk_bf16_f32 v109,v36,v37
M 18,v[148:151],70
D 0,56,2048
M 22,v[140:143],66
v_fma_f32 v38,-v78,v90,v38
v_fma_f32 v39,-v78,v91,v39
M 22,v[152:155],70
R 58,1,28672
v_fma_f32 v40,-v78,v92,v40
M 26,v[144:147],66
v_fma_f32 v41,-v78,v93,v41
v_mul_f32 v38,v79,v38
M 26,v[156:159],70
L 3
v_mul_f32 v39,v79,v39
v_mul_f32 v40,v79,v40
M 26,v[168:171],42
R 62,1,29696
v_mul_f32 v41,v79,v41
M 26,v[180:183],46
v_fma_f32 v102,v38,v38,s88
v_fma_f32 v103,v39,v39,s88
M 22,v[164:167],42
v_fma_f32 v104,v40,v40,s88
v_fma_f32 v105,v41,v41,s88
M 22,v[176:179],46
R 66,1,30720
v_mul_f32 v102,v38,v102
M 18,v[160:163],42
s_nop 0
v_exp_f32 v102,v102
M 18,v[172:175],46
s_waitcnt vmcnt(3) lgkmcnt(3)
D 0,56,3072
M 18,v[184:187],50
s_barrier
R 70,1,31744
ds_read_b32 v7,v6 offset:0
M 18,v[196:199],54
buffer_store_dword v4,v3,s[36:39],s70 offen offset:0 sc0 sc1
v_mul_f32 v103,v39,v103
M 22,v[188:191],50
s_mov_b32 s70,s69
ds_read_b64 v[74:75],v13 offset:0
v_mul_f32 v104,v40,v104
M 22,v[200:203],54
s_mov_b32 s69,s68
R 42,2,0
v_mul_f32 v105,v41,v105
M 26,v[192:195],50
s_add_u32 s68,s68,s63
v_exp_f32 v103,v103
M 26,v[204:207],54
L 5
v_exp_f32 v104,v104
M 26,v[216:219],58
L 2
R 46,2,1024
v_fma_f32 v102,v102,s89,s89
M 26,v[228:231],62
v_exp_f32 v105,v105
M 22,v[212:215],58
buffer_store_dwordx4 v[106:109],v0,s[36:39],s67 offen offset:0 sc0 sc1
v_fma_f32 v103,v103,s89,s89
M 22,v[224:227],62
R 50,2,2048
v_fma_f32 v104,v104,s89,s89
M 18,v[208:211],58
v_rcp_f32 v102,v102
M 18,v[220:223],62
s_nop 0
v_fma_f32 v105,v105,s89,s89
v_mul_f32 v38,v38,v102
M 18,v[232:235],66
R 54,2,3072
v_cmp_eq_u32 vcc,s53,v7
M 18,v[244:247],70
v_rcp_f32 v103,v103
M 22,v[236:239],66
s_cmp_eq_u32 vcc_lo,-1
v_rcp_f32 v104,v104
v_rcp_f32 v105,v105
M 22,v[248:251],70
R 58,2,4096
v_mul_f32 v39,v39,v103
v_mul_f32 v40,v40,v104
v_mul_f32 v41,v41,v105
v_cvt_pk_bf16_f32 v110,v38,v39
v_cvt_pk_bf16_f32 v111,v40,v41
M 26,v[240:243],66
L 3
buffer_store_dwordx2 v[110:111],v10,s[36:39],s67 offen offset:0 sc0 sc1
M 26,v[252:255],70
s_cbranch_scc0 Lg3_K1_%=
Lg3_F1w1_%=:
v_mfma_f32_16x16x32_bf16 v[38:41],a[8:11],v[42:45],0
s_add_u32 m0,s57,0
R 62,2,5120
v_add_f32_dpp v115,v114,v114 quad_perm:[1,0,3,2] row_mask:0xf bank_mask:0xf
M 38,a[20:23],46
buffer_load_dword v114,v11,s[36:39],s73 offen offset:0 sc1
v_mfma_f32_16x16x32_bf16 v[34:37],a[4:7],v[42:45],0
s_add_u32 s56,s55,0
v_add_f32_dpp v115,v115,v115 quad_perm:[2,3,0,1] row_mask:0xf bank_mask:0xf
v_fma_f32 v18,-v74,v82,v18
M 34,a[16:19],46
s_add_u32 s58,s58,s61
R 66,2,6144
v_fma_f32 v19,-v74,v83,v19
v_mfma_f32_16x16x32_bf16 v[30:33],a[0:3],v[42:45],0
D 0,56,0
M 30,a[12:15],46
L 3
v_add_f32_dpp v118,v115,v115 row_half_mirror row_mask:0xf bank_mask:0xf
v_fma_f32 v20,-v74,v84,v20
M 30,a[24:27],50
s_mov_b32 s67,s65
R 70,2,7168
v_fma_f32 v21,-v74,v85,v21
M 30,a[36:39],54
s_add_u32 s65,s65,s62
v_mov_b32_dpp v115,v118 row_ror:8 row_mask:0xf bank_mask:0xf
v_fma_f32 v115,-v118,v118,v115
M 34,a[28:31],50
s_add_u32 s54,s54,s46
v_max_f32 v115,s87,v115
v_mul_f32 v118,v15,v118
M 34,a[40:43],54
s_add_u32 s73,s73,s64
R 42,2,8192
v_mul_f32 v18,v75,v18
M 38,a[32:35],50
v_rsq_f32 v116,v115
M 38,a[44:47],54
L 3
D 0,56,1024
M 38,a[56:59],58
R 46,2,9216
v_fma_f32 v118,v116,v16,v118
M 38,a[68:71],62
ds_write_b32 v12,v118 offset:2048
M 34,a[52:55],58
v_mul_f32 v19,v75,v19
v_mul_f32 v20,v75,v20
M 34,a[64:67],62
R 50,2,10240
v_mul_f32 v21,v75,v21
M 30,a[48:51],58
v_fma_f32 v94,v18,v18,s88
v_fma_f32 v95,v19,v19,s88
M 30,a[60:63],62
L 4
v_fma_f32 v96,v20,v20,s88
v_fma_f32 v97,v21,v21,s88
M 30,a[72:75],66
R 54,2,11264
v_mul_f32 v94,v18,v94
M 30,a[84:87],70
v_exp_f32 v94,v94
M 34,a[76:79],66
v_mul_f32 v95,v19,v95
v_mul_f32 v96,v20,v96
M 34,a[88:91],70
R 58,2,12288
v_mul_f32 v97,v21,v97
M 38,a[80:83],66
D 0,56,2048
M 38,a[92:95],70
L 4
v_exp_f32 v95,v95
M 38,a[104:107],42
R 62,2,13312
v_fma_f32 v94,v94,s89,s89
M 38,a[116:119],46
v_exp_f32 v96,v96
M 34,a[100:103],42
v_exp_f32 v97,v97
M 34,a[112:115],46
R 66,2,14336
v_fma_f32 v95,v95,s89,s89
M 30,a[96:99],42
v_rcp_f32 v94,v94
M 30,a[108:111],46
s_waitcnt vmcnt(7) lgkmcnt(3)
v_rcp_f32 v95,v95
M 30,a[120:123],50
s_barrier
R 70,2,15360
v_fma_f32 v96,v96,s89,s89
M 30,a[132:135],54
D 0,56,3072
M 34,a[124:127],50
s_add_u32 m0,s59,1024
v_rcp_f32 v96,v96
M 34,a[136:139],54
s_add_u32 s56,s55,16384
R 42,2,16384
v_fma_f32 v97,v97,s89,s89
M 38,a[128:131],50
s_add_u32 s55,s55,s60
v_rcp_f32 v97,v97
M 38,a[140:143],54
L 3
v_mul_f32 v18,v18,v94
v_mul_f32 v19,v19,v95
M 38,a[152:155],58
R 46,2,17408
v_mul_f32 v20,v20,v96
M 38,a[164:167],62
v_mul_f32 v21,v21,v97
v_cvt_pk_bf16_f32 v106,v18,v19
M 34,a[148:151],58
v_cvt_pk_bf16_f32 v107,v20,v21
v_fma_f32 v22,-v74,v86,v22
M 34,a[160:163],62
R 50,2,18432
v_fma_f32 v23,-v74,v87,v23
v_fma_f32 v24,-v74,v88,v24
M 30,a[144:147],58
buffer_load_dword v8,s[36:39],s58 offen offset:0 sc1 lds
M 30,a[156:159],62
L 3
v_fma_f32 v25,-v74,v89,v25
v_mul_f32 v22,v75,v22
M 30,a[168:171],66
s_add_u32 m0,s57,16384
R 54,2,19456
v_mul_f32 v23,v75,v23
M 30,a[180:183],70
s_sub_u32 s57,s84,s57
v_mul_f32 v24,v75,v24
v_mul_f32 v25,v75,v25
M 34,a[172:175],66
v_fma_f32 v98,v22,v22,s88
v_fma_f32 v99,v23,v23,s88
M 34,a[184:187],70
R 58,2,20480
v_fma_f32 v100,v24,v24,s88
M 38,a[176:179],66
D 0,56,0
M 38,a[188:191],70
L 3
v_fma_f32 v101,v25,v25,s88
v_mul_f32 v98,v22,v98
M 38,a[200:203],42
R 62,2,21504
v_mul_f32 v99,v23,v99
M 38,a[212:215],46
v_exp_f32 v98,v98
M 34,a[196:199],42
v_exp_f32 v99,v99
M 34,a[208:211],46
R 66,2,22528
v_mul_f32 v100,v24,v100
M 30,a[192:195],42
v_exp_f32 v100,v100
M 30,a[204:207],46
L 3
v_mul_f32 v101,v25,v101
v_fma_f32 v98,v98,s89,s89
M 30,a[216:219],50
R 70,2,23552
v_fma_f32 v99,v99,s89,s89
M 30,a[228:231],54
D 0,56,1024
M 34,a[220:223],50
v_exp_f32 v101,v101
M 34,a[232:235],54
R 42,2,24576
v_fma_f32 v100,v100,s89,s89
M 38,a[224:227],50
v_rcp_f32 v98,v98
M 38,a[236:239],54
L 3
v_rcp_f32 v99,v99
M 38,a[248:251],58
R 46,2,25600
v_fma_f32 v101,v101,s89,s89
M 38,v[132:135],62
v_rcp_f32 v100,v100
M 34,a[244:247],58
v_rcp_f32 v101,v101
M 34,v[128:131],62
R 50,2,26624
v_mul_f32 v22,v22,v98
M 30,a[240:243],58
v_mul_f32 v23,v23,v99
v_mul_f32 v24,v24,v100
)"
R"(M 30,a[252:255],62
L 3
v_mul_f32 v25,v25,v101
v_cvt_pk_bf16_f32 v108,v22,v23
M 30,v[136:139],66
R 54,2,27648
v_cvt_pk_bf16_f32 v109,v24,v25
M 30,v[148:151],70
D 0,56,2048
M 34,v[140:143],66
v_fma_f32 v26,-v74,v90,v26
v_fma_f32 v27,-v74,v91,v27
M 34,v[152:155],70
R 58,2,28672
v_fma_f32 v28,-v74,v92,v28
M 38,v[144:147],66
v_fma_f32 v29,-v74,v93,v29
v_mul_f32 v26,v75,v26
M 38,v[156:159],70
L 3
v_mul_f32 v27,v75,v27
v_mul_f32 v28,v75,v28
M 38,v[168:171],42
R 62,2,29696
v_mul_f32 v29,v75,v29
M 38,v[180:183],46
v_fma_f32 v102,v26,v26,s88
v_fma_f32 v103,v27,v27,s88
M 34,v[164:167],42
v_fma_f32 v104,v28,v28,s88
v_fma_f32 v105,v29,v29,s88
M 34,v[176:179],46
R 66,2,30720
v_mul_f32 v102,v26,v102
M 30,v[160:163],42
s_nop 0
v_exp_f32 v102,v102
M 30,v[172:175],46
s_waitcnt vmcnt(3) lgkmcnt(3)
D 0,56,3072
M 30,v[184:187],50
s_barrier
R 70,2,31744
ds_read_b32 v7,v6 offset:1024
M 30,v[196:199],54
buffer_store_dword v4,v3,s[36:39],s70 offen offset:0 sc0 sc1
v_mul_f32 v103,v27,v103
M 34,v[188:191],50
s_mov_b32 s70,s69
ds_read_b64 v[78:79],v13 offset:2048
v_mul_f32 v104,v28,v104
M 34,v[200:203],54
s_mov_b32 s69,s68
R 42,1,0
v_mul_f32 v105,v29,v105
M 38,v[192:195],50
s_add_u32 s68,s68,s63
v_exp_f32 v103,v103
M 38,v[204:207],54
L 5
v_exp_f32 v104,v104
M 38,v[216:219],58
L 2
R 46,1,1024
v_fma_f32 v102,v102,s89,s89
M 38,v[228:231],62
v_exp_f32 v105,v105
M 34,v[212:215],58
buffer_store_dwordx4 v[106:109],v0,s[36:39],s66 offen offset:0 sc0 sc1
v_fma_f32 v103,v103,s89,s89
M 34,v[224:227],62
R 50,1,2048
v_fma_f32 v104,v104,s89,s89
M 30,v[208:211],58
v_rcp_f32 v102,v102
M 30,v[220:223],62
s_nop 0
v_fma_f32 v105,v105,s89,s89
v_mul_f32 v26,v26,v102
M 30,v[232:235],66
R 54,1,3072
v_cmp_eq_u32 vcc,s53,v7
M 30,v[244:247],70
v_rcp_f32 v103,v103
M 34,v[236:239],66
s_cmp_eq_u32 vcc_lo,-1
v_rcp_f32 v104,v104
v_rcp_f32 v105,v105
M 34,v[248:251],70
R 58,1,4096
v_mul_f32 v27,v27,v103
v_mul_f32 v28,v28,v104
v_mul_f32 v29,v29,v105
v_cvt_pk_bf16_f32 v110,v26,v27
v_cvt_pk_bf16_f32 v111,v28,v29
M 38,v[240:243],66
L 3
buffer_store_dwordx2 v[110:111],v10,s[36:39],s66 offen offset:0 sc0 sc1
M 38,v[252:255],70
s_cbranch_scc1 Lg3_F0w1_%=
s_branch Lg3_K0_%=
Lg3_F0w2_%=:
v_mfma_f32_16x16x32_bf16 v[26:29],a[8:11],v[42:45],0
s_add_u32 m0,s57,0
R 62,1,5120
v_add_f32_dpp v115,v114,v114 quad_perm:[1,0,3,2] row_mask:0xf bank_mask:0xf
M 26,a[20:23],46
buffer_load_dword v114,v11,s[36:39],s73 offen offset:0 sc1
v_mfma_f32_16x16x32_bf16 v[22:25],a[4:7],v[42:45],0
s_add_u32 s56,s55,0
v_add_f32_dpp v115,v115,v115 quad_perm:[2,3,0,1] row_mask:0xf bank_mask:0xf
v_fma_f32 v30,-v78,v82,v30
M 22,a[16:19],46
s_add_u32 s58,s58,s61
R 66,1,6144
v_fma_f32 v31,-v78,v83,v31
v_mfma_f32_16x16x32_bf16 v[18:21],a[0:3],v[42:45],0
D 0,56,0
M 18,a[12:15],46
L 3
v_add_f32_dpp v118,v115,v115 row_half_mirror row_mask:0xf bank_mask:0xf
v_fma_f32 v32,-v78,v84,v32
M 18,a[24:27],50
s_mov_b32 s66,s65
R 70,1,7168
v_fma_f32 v33,-v78,v85,v33
M 18,a[36:39],54
s_add_u32 s65,s65,s62
v_mov_b32_dpp v115,v118 row_ror:8 row_mask:0xf bank_mask:0xf
v_fma_f32 v115,-v118,v118,v115
M 22,a[28:31],50
s_add_u32 s54,s54,s46
v_max_f32 v115,s87,v115
v_mul_f32 v118,v15,v118
M 22,a[40:43],54
s_add_u32 s73,s73,s64
R 42,1,8192
v_mul_f32 v30,v79,v30
M 26,a[32:35],50
v_rsq_f32 v116,v115
M 26,a[44:47],54
L 3
D 0,56,1024
M 26,a[56:59],58
R 46,1,9216
v_fma_f32 v118,v116,v16,v118
M 26,a[68:71],62
ds_write_b32 v12,v118 offset:0
M 22,a[52:55],58
v_mul_f32 v31,v79,v31
v_mul_f32 v32,v79,v32
M 22,a[64:67],62
R 50,1,10240
v_mul_f32 v33,v79,v33
M 18,a[48:51],58
v_fma_f32 v94,v30,v30,s88
v_fma_f32 v95,v31,v31,s88
M 18,a[60:63],62
L 4
v_fma_f32 v96,v32,v32,s88
v_fma_f32 v97,v33,v33,s88
M 18,a[72:75],66
R 54,1,11264
v_mul_f32 v94,v30,v94
M 18,a[84:87],70
v_exp_f32 v94,v94
M 22,a[76:79],66
v_mul_f32 v95,v31,v95
v_mul_f32 v96,v32,v96
M 22,a[88:91],70
R 58,1,12288
v_mul_f32 v97,v33,v97
M 26,a[80:83],66
D 0,56,2048
M 26,a[92:95],70
L 4
v_exp_f32 v95,v95
M 26,a[104:107],42
R 62,1,13312
v_fma_f32 v94,v94,s89,s89
M 26,a[116:119],46
v_exp_f32 v96,v96
M 22,a[100:103],42
v_exp_f32 v97,v97
M 22,a[112:115],46
s_waitcnt vmcnt(6)
R 66,1,14336
v_fma_f32 v95,v95,s89,s89
M 18,a[96:99],42
s_barrier
v_rcp_f32 v94,v94
M 18,a[108:111],46
L 3
v_rcp_f32 v95,v95
M 18,a[120:123],50
R 70,1,15360
v_fma_f32 v96,v96,s89,s89
M 18,a[132:135],54
D 0,56,3072
M 22,a[124:127],50
s_add_u32 m0,s59,0
v_rcp_f32 v96,v96
M 22,a[136:139],54
s_add_u32 s56,s55,16384
R 42,1,16384
v_fma_f32 v97,v97,s89,s89
M 26,a[128:131],50
s_add_u32 s55,s55,s60
v_rcp_f32 v97,v97
M 26,a[140:143],54
L 3
v_mul_f32 v30,v30,v94
v_mul_f32 v31,v31,v95
M 26,a[152:155],58
R 46,1,17408
v_mul_f32 v32,v32,v96
M 26,a[164:167],62
v_mul_f32 v33,v33,v97
v_cvt_pk_bf16_f32 v106,v30,v31
M 22,a[148:151],58
v_cvt_pk_bf16_f32 v107,v32,v33
v_fma_f32 v34,-v78,v86,v34
M 22,a[160:163],62
R 50,1,18432
v_fma_f32 v35,-v78,v87,v35
v_fma_f32 v36,-v78,v88,v36
M 18,a[144:147],58
buffer_load_dword v8,s[36:39],s58 offen offset:0 sc1 lds
M 18,a[156:159],62
L 3
v_fma_f32 v37,-v78,v89,v37
v_mul_f32 v34,v79,v34
M 18,a[168:171],66
s_add_u32 m0,s57,16384
R 54,1,19456
v_mul_f32 v35,v79,v35
M 18,a[180:183],70
s_sub_u32 s57,s84,s57
v_mul_f32 v36,v79,v36
v_mul_f32 v37,v79,v37
M 22,a[172:175],66
v_fma_f32 v98,v34,v34,s88
v_fma_f32 v99,v35,v35,s88
M 22,a[184:187],70
R 58,1,20480
v_fma_f32 v100,v36,v36,s88
M 26,a[176:179],66
D 0,56,0
M 26,a[188:191],70
L 3
v_fma_f32 v101,v37,v37,s88
v_mul_f32 v98,v34,v98
M 26,a[200:203],42
R 62,1,21504
v_mul_f32 v99,v35,v99
M 26,a[212:215],46
v_exp_f32 v98,v98
M 22,a[196:199],42
v_exp_f32 v99,v99
M 22,a[208:211],46
R 66,1,22528
v_mul_f32 v100,v36,v100
M 18,a[192:195],42
v_exp_f32 v100,v100
M 18,a[204:207],46
L 3
v_mul_f32 v101,v37,v101
v_fma_f32 v98,v98,s89,s89
M 18,a[216:219],50
R 70,1,23552
v_fma_f32 v99,v99,s89,s89
M 18,a[228:231],54
D 0,56,1024
M 22,a[220:223],50
v_exp_f32 v101,v101
M 22,a[232:235],54
R 42,1,24576
v_fma_f32 v100,v100,s89,s89
M 26,a[224:227],50
v_rcp_f32 v98,v98
M 26,a[236:239],54
L 3
v_rcp_f32 v99,v99
M 26,a[248:251],58
R 46,1,25600
v_fma_f32 v101,v101,s89,s89
M 26,v[132:135],62
v_rcp_f32 v100,v100
M 22,a[244:247],58
v_rcp_f32 v101,v101
M 22,v[128:131],62
R 50,1,26624
v_mul_f32 v34,v34,v98
M 18,a[240:243],58
v_mul_f32 v35,v35,v99
v_mul_f32 v36,v36,v100
M 18,a[252:255],62
L 3
v_mul_f32 v37,v37,v101
v_cvt_pk_bf16_f32 v108,v34,v35
M 18,v[136:139],66
R 54,1,27648
v_cvt_pk_bf16_f32 v109,v36,v37
M 18,v[148:151],70
D 0,56,2048
M 22,v[140:143],66
v_fma_f32 v38,-v78,v90,v38
v_fma_f32 v39,-v78,v91,v39
M 22,v[152:155],70
R 58,1,28672
v_fma_f32 v40,-v78,v92,v40
M 26,v[144:147],66
v_fma_f32 v41,-v78,v93,v41
v_mul_f32 v38,v79,v38
M 26,v[156:159],70
L 3
v_mul_f32 v39,v79,v39
v_mul_f32 v40,v79,v40
M 26,v[168:171],42
R 62,1,29696
v_mul_f32 v41,v79,v41
M 26,v[180:183],46
v_fma_f32 v102,v38,v38,s88
v_fma_f32 v103,v39,v39,s88
M 22,v[164:167],42
s_nop 0
v_fma_f32 v104,v40,v40,s88
v_fma_f32 v105,v41,v41,s88
M 22,v[176:179],46
s_waitcnt vmcnt(3)
R 66,1,30720
v_mul_f32 v102,v38,v102
M 18,v[160:163],42
s_barrier
buffer_store_dword v4,v3,s[36:39],s70 offen offset:0 sc0 sc1
M 18,v[172:175],46
L 3
D 0,56,3072
M 18,v[184:187],50
s_mov_b32 s70,s69
R 70,1,31744
ds_read_b32 v7,v6 offset:0
M 18,v[196:199],54
s_mov_b32 s69,s68
ds_read_b64 v[74:75],v13 offset:0
v_exp_f32 v102,v102
M 22,v[188:191],50
s_add_u32 s68,s68,s63
v_mul_f32 v103,v39,v103
v_mul_f32 v104,v40,v104
M 22,v[200:203],54
R 42,2,0
v_mul_f32 v105,v41,v105
M 26,v[192:195],50
v_exp_f32 v103,v103
M 26,v[204:207],54
L 5
v_exp_f32 v104,v104
M 26,v[216:219],58
L 2
R 46,2,1024
v_fma_f32 v102,v102,s89,s89
M 26,v[228:231],62
v_exp_f32 v105,v105
M 22,v[212:215],58
buffer_store_dwordx4 v[106:109],v0,s[36:39],s67 offen offset:0 sc0 sc1
v_fma_f32 v103,v103,s89,s89
M 22,v[224:227],62
R 50,2,2048
v_fma_f32 v104,v104,s89,s89
M 18,v[208:211],58
v_rcp_f32 v102,v102
M 18,v[220:223],62
s_nop 0
v_fma_f32 v105,v105,s89,s89
v_mul_f32 v38,v38,v102
M 18,v[232:235],66
R 54,2,3072
v_cmp_eq_u32 vcc,s53,v7
M 18,v[244:247],70
v_rcp_f32 v103,v103
M 22,v[236:239],66
s_cmp_eq_u32 vcc_lo,-1
v_rcp_f32 v104,v104
v_rcp_f32 v105,v105
M 22,v[248:251],70
R 58,2,4096
v_mul_f32 v39,v39,v103
v_mul_f32 v40,v40,v104
v_mul_f32 v41,v41,v105
v_cvt_pk_bf16_f32 v110,v38,v39
v_cvt_pk_bf16_f32 v111,v40,v41
M 26,v[240:243],66
L 3
buffer_store_dwordx2 v[110:111],v10,s[36:39],s67 offen offset:0 sc0 sc1
M 26,v[252:255],70
s_cbranch_scc0 Lg3_K1_%=
Lg3_F1w2_%=:
v_mfma_f32_16x16x32_bf16 v[38:41],a[8:11],v[42:45],0
s_add_u32 m0,s57,0
R 62,2,5120
v_add_f32_dpp v115,v114,v114 quad_perm:[1,0,3,2] row_mask:0xf bank_mask:0xf
M 38,a[20:23],46
buffer_load_dword v114,v11,s[36:39],s73 offen offset:0 sc1
v_mfma_f32_16x16x32_bf16 v[34:37],a[4:7],v[42:45],0
s_add_u32 s56,s55,0
v_add_f32_dpp v115,v115,v115 quad_perm:[2,3,0,1] row_mask:0xf bank_mask:0xf
v_fma_f32 v18,-v74,v82,v18
M 34,a[16:19],46
s_add_u32 s58,s58,s61
R 66,2,6144
v_fma_f32 v19,-v74,v83,v19
v_mfma_f32_16x16x32_bf16 v[30:33],a[0:3],v[42:45],0
D 0,56,0
M 30,a[12:15],46
L 3
v_add_f32_dpp v118,v115,v115 row_half_mirror row_mask:0xf bank_mask:0xf
v_fma_f32 v20,-v74,v84,v20
M 30,a[24:27],50
s_mov_b32 s67,s65
R 70,2,7168
v_fma_f32 v21,-v74,v85,v21
M 30,a[36:39],54
s_add_u32 s65,s65,s62
v_mov_b32_dpp v115,v118 row_ror:8 row_mask:0xf bank_mask:0xf
v_fma_f32 v115,-v118,v118,v115
M 34,a[28:31],50
s_add_u32 s54,s54,s46
v_max_f32 v115,s87,v115
v_mul_f32 v118,v15,v118
M 34,a[40:43],54
s_add_u32 s73,s73,s64
R 42,2,8192
v_mul_f32 v18,v75,v18
M 38,a[32:35],50
v_rsq_f32 v116,v115
M 38,a[44:47],54
L 3
D 0,56,1024
M 38,a[56:59],58
R 46,2,9216
v_fma_f32 v118,v116,v16,v118
M 38,a[68:71],62
ds_write_b32 v12,v118 offset:2048
M 34,a[52:55],58
v_mul_f32 v19,v75,v19
v_mul_f32 v20,v75,v20
M 34,a[64:67],62
R 50,2,10240
v_mul_f32 v21,v75,v21
M 30,a[48:51],58
v_fma_f32 v94,v18,v18,s88
v_fma_f32 v95,v19,v19,s88
M 30,a[60:63],62
L 4
v_fma_f32 v96,v20,v20,s88
v_fma_f32 v97,v21,v21,s88
M 30,a[72:75],66
R 54,2,11264
v_mul_f32 v94,v18,v94
M 30,a[84:87],70
v_exp_f32 v94,v94
M 34,a[76:79],66
v_mul_f32 v95,v19,v95
v_mul_f32 v96,v20,v96
M 34,a[88:91],70
R 58,2,12288
v_mul_f32 v97,v21,v97
M 38,a[80:83],66
D 0,56,2048
M 38,a[92:95],70
L 4
v_exp_f32 v95,v95
M 38,a[104:107],42
R 62,2,13312
v_fma_f32 v94,v94,s89,s89
M 38,a[116:119],46
v_exp_f32 v96,v96
M 34,a[100:103],42
v_exp_f32 v97,v97
M 34,a[112:115],46
s_waitcnt vmcnt(6)
R 66,2,14336
v_fma_f32 v95,v95,s89,s89
M 30,a[96:99],42
s_barrier
v_rcp_f32 v94,v94
M 30,a[108:111],46
L 3
v_rcp_f32 v95,v95
M 30,a[120:123],50
R 70,2,15360
v_fma_f32 v96,v96,s89,s89
M 30,a[132:135],54
D 0,56,3072
M 34,a[124:127],50
s_add_u32 m0,s59,1024
v_rcp_f32 v96,v96
M 34,a[136:139],54
s_add_u32 s56,s55,16384
)"
R"(R 42,2,16384
v_fma_f32 v97,v97,s89,s89
M 38,a[128:131],50
s_add_u32 s55,s55,s60
v_rcp_f32 v97,v97
M 38,a[140:143],54
L 3
v_mul_f32 v18,v18,v94
v_mul_f32 v19,v19,v95
M 38,a[152:155],58
R 46,2,17408
v_mul_f32 v20,v20,v96
M 38,a[164:167],62
v_mul_f32 v21,v21,v97
v_cvt_pk_bf16_f32 v106,v18,v19
M 34,a[148:151],58
v_cvt_pk_bf16_f32 v107,v20,v21
v_fma_f32 v22,-v74,v86,v22
M 34,a[160:163],62
R 50,2,18432
v_fma_f32 v23,-v74,v87,v23
v_fma_f32 v24,-v74,v88,v24
M 30,a[144:147],58
buffer_load_dword v8,s[36:39],s58 offen offset:0 sc1 lds
M 30,a[156:159],62
L 3
v_fma_f32 v25,-v74,v89,v25
v_mul_f32 v22,v75,v22
M 30,a[168:171],66
s_add_u32 m0,s57,16384
R 54,2,19456
v_mul_f32 v23,v75,v23
M 30,a[180:183],70
s_sub_u32 s57,s84,s57
v_mul_f32 v24,v75,v24
v_mul_f32 v25,v75,v25
M 34,a[172:175],66
v_fma_f32 v98,v22,v22,s88
v_fma_f32 v99,v23,v23,s88
M 34,a[184:187],70
R 58,2,20480
v_fma_f32 v100,v24,v24,s88
M 38,a[176:179],66
D 0,56,0
M 38,a[188:191],70
L 3
v_fma_f32 v101,v25,v25,s88
v_mul_f32 v98,v22,v98
M 38,a[200:203],42
R 62,2,21504
v_mul_f32 v99,v23,v99
M 38,a[212:215],46
v_exp_f32 v98,v98
M 34,a[196:199],42
v_exp_f32 v99,v99
M 34,a[208:211],46
R 66,2,22528
v_mul_f32 v100,v24,v100
M 30,a[192:195],42
v_exp_f32 v100,v100
M 30,a[204:207],46
L 3
v_mul_f32 v101,v25,v101
v_fma_f32 v98,v98,s89,s89
M 30,a[216:219],50
R 70,2,23552
v_fma_f32 v99,v99,s89,s89
M 30,a[228:231],54
D 0,56,1024
M 34,a[220:223],50
v_exp_f32 v101,v101
M 34,a[232:235],54
R 42,2,24576
v_fma_f32 v100,v100,s89,s89
M 38,a[224:227],50
v_rcp_f32 v98,v98
M 38,a[236:239],54
L 3
v_rcp_f32 v99,v99
M 38,a[248:251],58
R 46,2,25600
v_fma_f32 v101,v101,s89,s89
M 38,v[132:135],62
v_rcp_f32 v100,v100
M 34,a[244:247],58
v_rcp_f32 v101,v101
M 34,v[128:131],62
R 50,2,26624
v_mul_f32 v22,v22,v98
M 30,a[240:243],58
v_mul_f32 v23,v23,v99
v_mul_f32 v24,v24,v100
M 30,a[252:255],62
L 3
v_mul_f32 v25,v25,v101
v_cvt_pk_bf16_f32 v108,v22,v23
M 30,v[136:139],66
R 54,2,27648
v_cvt_pk_bf16_f32 v109,v24,v25
M 30,v[148:151],70
D 0,56,2048
M 34,v[140:143],66
v_fma_f32 v26,-v74,v90,v26
v_fma_f32 v27,-v74,v91,v27
M 34,v[152:155],70
R 58,2,28672
v_fma_f32 v28,-v74,v92,v28
M 38,v[144:147],66
v_fma_f32 v29,-v74,v93,v29
v_mul_f32 v26,v75,v26
M 38,v[156:159],70
L 3
v_mul_f32 v27,v75,v27
v_mul_f32 v28,v75,v28
M 38,v[168:171],42
R 62,2,29696
v_mul_f32 v29,v75,v29
M 38,v[180:183],46
v_fma_f32 v102,v26,v26,s88
v_fma_f32 v103,v27,v27,s88
M 34,v[164:167],42
s_nop 0
v_fma_f32 v104,v28,v28,s88
v_fma_f32 v105,v29,v29,s88
M 34,v[176:179],46
s_waitcnt vmcnt(3)
R 66,2,30720
v_mul_f32 v102,v26,v102
M 30,v[160:163],42
s_barrier
buffer_store_dword v4,v3,s[36:39],s70 offen offset:0 sc0 sc1
M 30,v[172:175],46
L 3
D 0,56,3072
M 30,v[184:187],50
s_mov_b32 s70,s69
R 70,2,31744
ds_read_b32 v7,v6 offset:1024
M 30,v[196:199],54
s_mov_b32 s69,s68
ds_read_b64 v[78:79],v13 offset:2048
v_exp_f32 v102,v102
M 34,v[188:191],50
s_add_u32 s68,s68,s63
v_mul_f32 v103,v27,v103
v_mul_f32 v104,v28,v104
M 34,v[200:203],54
R 42,1,0
v_mul_f32 v105,v29,v105
M 38,v[192:195],50
v_exp_f32 v103,v103
M 38,v[204:207],54
L 5
v_exp_f32 v104,v104
M 38,v[216:219],58
L 2
R 46,1,1024
v_fma_f32 v102,v102,s89,s89
M 38,v[228:231],62
v_exp_f32 v105,v105
M 34,v[212:215],58
buffer_store_dwordx4 v[106:109],v0,s[36:39],s66 offen offset:0 sc0 sc1
v_fma_f32 v103,v103,s89,s89
M 34,v[224:227],62
R 50,1,2048
v_fma_f32 v104,v104,s89,s89
M 30,v[208:211],58
v_rcp_f32 v102,v102
M 30,v[220:223],62
s_nop 0
v_fma_f32 v105,v105,s89,s89
v_mul_f32 v26,v26,v102
M 30,v[232:235],66
R 54,1,3072
v_cmp_eq_u32 vcc,s53,v7
M 30,v[244:247],70
v_rcp_f32 v103,v103
M 34,v[236:239],66
s_cmp_eq_u32 vcc_lo,-1
v_rcp_f32 v104,v104
v_rcp_f32 v105,v105
M 34,v[248:251],70
R 58,1,4096
v_mul_f32 v27,v27,v103
v_mul_f32 v28,v28,v104
v_mul_f32 v29,v29,v105
v_cvt_pk_bf16_f32 v110,v26,v27
v_cvt_pk_bf16_f32 v111,v28,v29
M 38,v[240:243],66
L 3
buffer_store_dwordx2 v[110:111],v10,s[36:39],s66 offen offset:0 sc0 sc1
M 38,v[252:255],70
s_cbranch_scc1 Lg3_F0w2_%=
s_branch Lg3_K0_%=
Lg3_F0w3_%=:
v_mfma_f32_16x16x32_bf16 v[26:29],a[8:11],v[42:45],0
s_add_u32 m0,s57,0
R 62,1,5120
v_add_f32_dpp v115,v114,v114 quad_perm:[1,0,3,2] row_mask:0xf bank_mask:0xf
M 26,a[20:23],46
buffer_load_dword v114,v11,s[36:39],s73 offen offset:0 sc1
v_mfma_f32_16x16x32_bf16 v[22:25],a[4:7],v[42:45],0
s_add_u32 s56,s55,0
v_add_f32_dpp v115,v115,v115 quad_perm:[2,3,0,1] row_mask:0xf bank_mask:0xf
v_fma_f32 v30,-v78,v82,v30
M 22,a[16:19],46
s_add_u32 s58,s58,s61
R 66,1,6144
v_fma_f32 v31,-v78,v83,v31
v_mfma_f32_16x16x32_bf16 v[18:21],a[0:3],v[42:45],0
D 0,56,0
M 18,a[12:15],46
L 3
v_add_f32_dpp v118,v115,v115 row_half_mirror row_mask:0xf bank_mask:0xf
v_fma_f32 v32,-v78,v84,v32
M 18,a[24:27],50
s_mov_b32 s66,s65
R 70,1,7168
v_fma_f32 v33,-v78,v85,v33
M 18,a[36:39],54
s_add_u32 s65,s65,s62
v_mov_b32_dpp v115,v118 row_ror:8 row_mask:0xf bank_mask:0xf
v_fma_f32 v115,-v118,v118,v115
M 22,a[28:31],50
s_add_u32 s54,s54,s46
v_max_f32 v115,s87,v115
v_mul_f32 v118,v15,v118
M 22,a[40:43],54
s_add_u32 s73,s73,s64
R 42,1,8192
v_mul_f32 v30,v79,v30
M 26,a[32:35],50
v_rsq_f32 v116,v115
M 26,a[44:47],54
L 3
D 0,56,1024
M 26,a[56:59],58
R 46,1,9216
v_fma_f32 v118,v116,v16,v118
M 26,a[68:71],62
ds_write_b32 v12,v118 offset:0
M 22,a[52:55],58
v_mul_f32 v31,v79,v31
v_mul_f32 v32,v79,v32
M 22,a[64:67],62
R 50,1,10240
v_mul_f32 v33,v79,v33
M 18,a[48:51],58
v_fma_f32 v94,v30,v30,s88
v_fma_f32 v95,v31,v31,s88
M 18,a[60:63],62
L 4
v_fma_f32 v96,v32,v32,s88
v_fma_f32 v97,v33,v33,s88
M 18,a[72:75],66
R 54,1,11264
v_mul_f32 v94,v30,v94
M 18,a[84:87],70
v_exp_f32 v94,v94
M 22,a[76:79],66
v_mul_f32 v95,v31,v95
v_mul_f32 v96,v32,v96
M 22,a[88:91],70
R 58,1,12288
v_mul_f32 v97,v33,v97
M 26,a[80:83],66
D 0,56,2048
M 26,a[92:95],70
L 4
v_exp_f32 v95,v95
M 26,a[104:107],42
R 62,1,13312
v_fma_f32 v94,v94,s89,s89
M 26,a[116:119],46
s_waitcnt vmcnt(6)
v_exp_f32 v96,v96
M 22,a[100:103],42
s_barrier
v_exp_f32 v97,v97
M 22,a[112:115],46
R 66,1,14336
v_fma_f32 v95,v95,s89,s89
M 18,a[96:99],42
v_rcp_f32 v94,v94
M 18,a[108:111],46
L 3
v_rcp_f32 v95,v95
M 18,a[120:123],50
R 70,1,15360
v_fma_f32 v96,v96,s89,s89
M 18,a[132:135],54
D 0,56,3072
M 22,a[124:127],50
s_add_u32 m0,s59,0
v_rcp_f32 v96,v96
M 22,a[136:139],54
s_add_u32 s56,s55,16384
R 42,1,16384
v_fma_f32 v97,v97,s89,s89
M 26,a[128:131],50
s_add_u32 s55,s55,s60
v_rcp_f32 v97,v97
M 26,a[140:143],54
L 3
v_mul_f32 v30,v30,v94
v_mul_f32 v31,v31,v95
M 26,a[152:155],58
R 46,1,17408
v_mul_f32 v32,v32,v96
M 26,a[164:167],62
v_mul_f32 v33,v33,v97
v_cvt_pk_bf16_f32 v106,v30,v31
M 22,a[148:151],58
v_cvt_pk_bf16_f32 v107,v32,v33
v_fma_f32 v34,-v78,v86,v34
M 22,a[160:163],62
R 50,1,18432
v_fma_f32 v35,-v78,v87,v35
v_fma_f32 v36,-v78,v88,v36
M 18,a[144:147],58
buffer_load_dword v8,s[36:39],s58 offen offset:0 sc1 lds
M 18,a[156:159],62
L 3
v_fma_f32 v37,-v78,v89,v37
v_mul_f32 v34,v79,v34
M 18,a[168:171],66
s_add_u32 m0,s57,16384
R 54,1,19456
v_mul_f32 v35,v79,v35
M 18,a[180:183],70
s_sub_u32 s57,s84,s57
v_mul_f32 v36,v79,v36
v_mul_f32 v37,v79,v37
M 22,a[172:175],66
v_fma_f32 v98,v34,v34,s88
v_fma_f32 v99,v35,v35,s88
M 22,a[184:187],70
R 58,1,20480
v_fma_f32 v100,v36,v36,s88
M 26,a[176:179],66
D 0,56,0
M 26,a[188:191],70
L 3
v_fma_f32 v101,v37,v37,s88
v_mul_f32 v98,v34,v98
M 26,a[200:203],42
R 62,1,21504
v_mul_f32 v99,v35,v99
M 26,a[212:215],46
v_exp_f32 v98,v98
M 22,a[196:199],42
v_exp_f32 v99,v99
M 22,a[208:211],46
R 66,1,22528
v_mul_f32 v100,v36,v100
M 18,a[192:195],42
v_exp_f32 v100,v100
M 18,a[204:207],46
L 3
v_mul_f32 v101,v37,v101
v_fma_f32 v98,v98,s89,s89
M 18,a[216:219],50
R 70,1,23552
v_fma_f32 v99,v99,s89,s89
M 18,a[228:231],54
D 0,56,1024
M 22,a[220:223],50
v_exp_f32 v101,v101
M 22,a[232:235],54
R 42,1,24576
v_fma_f32 v100,v100,s89,s89
M 26,a[224:227],50
v_rcp_f32 v98,v98
M 26,a[236:239],54
L 3
v_rcp_f32 v99,v99
M 26,a[248:251],58
R 46,1,25600
v_fma_f32 v101,v101,s89,s89
M 26,v[132:135],62
v_rcp_f32 v100,v100
M 22,a[244:247],58
v_rcp_f32 v101,v101
M 22,v[128:131],62
R 50,1,26624
v_mul_f32 v34,v34,v98
M 18,a[240:243],58
v_mul_f32 v35,v35,v99
v_mul_f32 v36,v36,v100
M 18,a[252:255],62
L 3
v_mul_f32 v37,v37,v101
v_cvt_pk_bf16_f32 v108,v34,v35
M 18,v[136:139],66
R 54,1,27648
v_cvt_pk_bf16_f32 v109,v36,v37
M 18,v[148:151],70
D 0,56,2048
M 22,v[140:143],66
v_fma_f32 v38,-v78,v90,v38
v_fma_f32 v39,-v78,v91,v39
M 22,v[152:155],70
R 58,1,28672
v_fma_f32 v40,-v78,v92,v40
M 26,v[144:147],66
v_fma_f32 v41,-v78,v93,v41
v_mul_f32 v38,v79,v38
M 26,v[156:159],70
L 3
v_mul_f32 v39,v79,v39
v_mul_f32 v40,v79,v40
M 26,v[168:171],42
s_nop 0
R 62,1,29696
v_mul_f32 v41,v79,v41
M 26,v[180:183],46
s_waitcnt vmcnt(3)
v_fma_f32 v102,v38,v38,s88
v_fma_f32 v103,v39,v39,s88
M 22,v[164:167],42
s_barrier
buffer_store_dword v4,v3,s[36:39],s70 offen offset:0 sc0 sc1
M 22,v[176:179],46
s_mov_b32 s70,s69
R 66,1,30720
ds_read_b32 v7,v6 offset:0
M 18,v[160:163],42
s_mov_b32 s69,s68
ds_read_b64 v[74:75],v13 offset:0
v_fma_f32 v104,v40,v40,s88
M 18,v[172:175],46
L 5
D 0,56,3072
M 18,v[184:187],50
s_add_u32 s68,s68,s63
R 70,1,31744
v_fma_f32 v105,v41,v41,s88
M 18,v[196:199],54
v_mul_f32 v102,v38,v102
v_exp_f32 v102,v102
M 22,v[188:191],50
v_mul_f32 v103,v39,v103
v_mul_f32 v104,v40,v104
M 22,v[200:203],54
L 2
R 42,2,0
v_mul_f32 v105,v41,v105
M 26,v[192:195],50
v_exp_f32 v103,v103
M 26,v[204:207],54
s_nop 0
v_exp_f32 v104,v104
M 26,v[216:219],58
R 46,2,1024
v_fma_f32 v102,v102,s89,s89
M 26,v[228:231],62
v_exp_f32 v105,v105
M 22,v[212:215],58
buffer_store_dwordx4 v[106:109],v0,s[36:39],s67 offen offset:0 sc0 sc1
v_fma_f32 v103,v103,s89,s89
M 22,v[224:227],62
R 50,2,2048
v_fma_f32 v104,v104,s89,s89
M 18,v[208:211],58
v_rcp_f32 v102,v102
M 18,v[220:223],62
L 3
v_fma_f32 v105,v105,s89,s89
v_mul_f32 v38,v38,v102
M 18,v[232:235],66
R 54,2,3072
v_cmp_eq_u32 vcc,s53,v7
M 18,v[244:247],70
v_rcp_f32 v103,v103
M 22,v[236:239],66
s_cmp_eq_u32 vcc_lo,-1
v_rcp_f32 v104,v104
v_rcp_f32 v105,v105
M 22,v[248:251],70
R 58,2,4096
v_mul_f32 v39,v39,v103
v_mul_f32 v40,v40,v104
v_mul_f32 v41,v41,v105
v_cvt_pk_bf16_f32 v110,v38,v39
v_cvt_pk_bf16_f32 v111,v40,v41
M 26,v[240:243],66
L 3
buffer_store_dwordx2 v[110:111],v10,s[36:39],s67 offen offset:0 sc0 sc1
M 26,v[252:255],70
s_cbranch_scc0 Lg3_K1_%=
Lg3_F1w3_%=:
v_mfma_f32_16x16x32_bf16 v[38:41],a[8:11],v[42:45],0
s_add_u32 m0,s57,0
R 62,2,5120
v_add_f32_dpp v115,v114,v114 quad_perm:[1,0,3,2] row_mask:0xf bank_mask:0xf
M 38,a[20:23],46
buffer_load_dword v114,v11,s[36:39],s73 offen offset:0 sc1
v_mfma_f32_16x16x32_bf16 v[34:37],a[4:7],v[42:45],0
s_add_u32 s56,s55,0
v_add_f32_dpp v115,v115,v115 quad_perm:[2,3,0,1] row_mask:0xf bank_mask:0xf
)"
R"(v_fma_f32 v18,-v74,v82,v18
M 34,a[16:19],46
s_add_u32 s58,s58,s61
R 66,2,6144
v_fma_f32 v19,-v74,v83,v19
v_mfma_f32_16x16x32_bf16 v[30:33],a[0:3],v[42:45],0
D 0,56,0
M 30,a[12:15],46
L 3
v_add_f32_dpp v118,v115,v115 row_half_mirror row_mask:0xf bank_mask:0xf
v_fma_f32 v20,-v74,v84,v20
M 30,a[24:27],50
s_mov_b32 s67,s65
R 70,2,7168
v_fma_f32 v21,-v74,v85,v21
M 30,a[36:39],54
s_add_u32 s65,s65,s62
v_mov_b32_dpp v115,v118 row_ror:8 row_mask:0xf bank_mask:0xf
v_fma_f32 v115,-v118,v118,v115
M 34,a[28:31],50
s_add_u32 s54,s54,s46
v_max_f32 v115,s87,v115
v_mul_f32 v118,v15,v118
M 34,a[40:43],54
s_add_u32 s73,s73,s64
R 42,2,8192
v_mul_f32 v18,v75,v18
M 38,a[32:35],50
v_rsq_f32 v116,v115
M 38,a[44:47],54
L 3
D 0,56,1024
M 38,a[56:59],58
R 46,2,9216
v_fma_f32 v118,v116,v16,v118
M 38,a[68:71],62
ds_write_b32 v12,v118 offset:2048
M 34,a[52:55],58
v_mul_f32 v19,v75,v19
v_mul_f32 v20,v75,v20
M 34,a[64:67],62
R 50,2,10240
v_mul_f32 v21,v75,v21
M 30,a[48:51],58
v_fma_f32 v94,v18,v18,s88
v_fma_f32 v95,v19,v19,s88
M 30,a[60:63],62
L 4
v_fma_f32 v96,v20,v20,s88
v_fma_f32 v97,v21,v21,s88
M 30,a[72:75],66
R 54,2,11264
v_mul_f32 v94,v18,v94
M 30,a[84:87],70
v_exp_f32 v94,v94
M 34,a[76:79],66
v_mul_f32 v95,v19,v95
v_mul_f32 v96,v20,v96
M 34,a[88:91],70
R 58,2,12288
v_mul_f32 v97,v21,v97
M 38,a[80:83],66
D 0,56,2048
M 38,a[92:95],70
L 4
v_exp_f32 v95,v95
M 38,a[104:107],42
R 62,2,13312
v_fma_f32 v94,v94,s89,s89
M 38,a[116:119],46
s_waitcnt vmcnt(6)
v_exp_f32 v96,v96
M 34,a[100:103],42
s_barrier
v_exp_f32 v97,v97
M 34,a[112:115],46
R 66,2,14336
v_fma_f32 v95,v95,s89,s89
M 30,a[96:99],42
v_rcp_f32 v94,v94
M 30,a[108:111],46
L 3
v_rcp_f32 v95,v95
M 30,a[120:123],50
R 70,2,15360
v_fma_f32 v96,v96,s89,s89
M 30,a[132:135],54
D 0,56,3072
M 34,a[124:127],50
s_add_u32 m0,s59,1024
v_rcp_f32 v96,v96
M 34,a[136:139],54
s_add_u32 s56,s55,16384
R 42,2,16384
v_fma_f32 v97,v97,s89,s89
M 38,a[128:131],50
s_add_u32 s55,s55,s60
v_rcp_f32 v97,v97
M 38,a[140:143],54
L 3
v_mul_f32 v18,v18,v94
v_mul_f32 v19,v19,v95
M 38,a[152:155],58
R 46,2,17408
v_mul_f32 v20,v20,v96
M 38,a[164:167],62
v_mul_f32 v21,v21,v97
v_cvt_pk_bf16_f32 v106,v18,v19
M 34,a[148:151],58
v_cvt_pk_bf16_f32 v107,v20,v21
v_fma_f32 v22,-v74,v86,v22
M 34,a[160:163],62
R 50,2,18432
v_fma_f32 v23,-v74,v87,v23
v_fma_f32 v24,-v74,v88,v24
M 30,a[144:147],58
buffer_load_dword v8,s[36:39],s58 offen offset:0 sc1 lds
M 30,a[156:159],62
L 3
v_fma_f32 v25,-v74,v89,v25
v_mul_f32 v22,v75,v22
M 30,a[168:171],66
s_add_u32 m0,s57,16384
R 54,2,19456
v_mul_f32 v23,v75,v23
M 30,a[180:183],70
s_sub_u32 s57,s84,s57
v_mul_f32 v24,v75,v24
v_mul_f32 v25,v75,v25
M 34,a[172:175],66
v_fma_f32 v98,v22,v22,s88
v_fma_f32 v99,v23,v23,s88
M 34,a[184:187],70
R 58,2,20480
v_fma_f32 v100,v24,v24,s88
M 38,a[176:179],66
D 0,56,0
M 38,a[188:191],70
L 3
v_fma_f32 v101,v25,v25,s88
v_mul_f32 v98,v22,v98
M 38,a[200:203],42
R 62,2,21504
v_mul_f32 v99,v23,v99
M 38,a[212:215],46
v_exp_f32 v98,v98
M 34,a[196:199],42
v_exp_f32 v99,v99
M 34,a[208:211],46
R 66,2,22528
v_mul_f32 v100,v24,v100
M 30,a[192:195],42
v_exp_f32 v100,v100
M 30,a[204:207],46
L 3
v_mul_f32 v101,v25,v101
v_fma_f32 v98,v98,s89,s89
M 30,a[216:219],50
R 70,2,23552
v_fma_f32 v99,v99,s89,s89
M 30,a[228:231],54
D 0,56,1024
M 34,a[220:223],50
v_exp_f32 v101,v101
M 34,a[232:235],54
R 42,2,24576
v_fma_f32 v100,v100,s89,s89
M 38,a[224:227],50
v_rcp_f32 v98,v98
M 38,a[236:239],54
L 3
v_rcp_f32 v99,v99
M 38,a[248:251],58
R 46,2,25600
v_fma_f32 v101,v101,s89,s89
M 38,v[132:135],62
v_rcp_f32 v100,v100
M 34,a[244:247],58
v_rcp_f32 v101,v101
M 34,v[128:131],62
R 50,2,26624
v_mul_f32 v22,v22,v98
M 30,a[240:243],58
v_mul_f32 v23,v23,v99
v_mul_f32 v24,v24,v100
M 30,a[252:255],62
L 3
v_mul_f32 v25,v25,v101
v_cvt_pk_bf16_f32 v108,v22,v23
M 30,v[136:139],66
R 54,2,27648
v_cvt_pk_bf16_f32 v109,v24,v25
M 30,v[148:151],70
D 0,56,2048
M 34,v[140:143],66
v_fma_f32 v26,-v74,v90,v26
v_fma_f32 v27,-v74,v91,v27
M 34,v[152:155],70
R 58,2,28672
v_fma_f32 v28,-v74,v92,v28
M 38,v[144:147],66
v_fma_f32 v29,-v74,v93,v29
v_mul_f32 v26,v75,v26
M 38,v[156:159],70
L 3
v_mul_f32 v27,v75,v27
v_mul_f32 v28,v75,v28
M 38,v[168:171],42
s_nop 0
R 62,2,29696
v_mul_f32 v29,v75,v29
M 38,v[180:183],46
s_waitcnt vmcnt(3)
v_fma_f32 v102,v26,v26,s88
v_fma_f32 v103,v27,v27,s88
M 34,v[164:167],42
s_barrier
buffer_store_dword v4,v3,s[36:39],s70 offen offset:0 sc0 sc1
M 34,v[176:179],46
s_mov_b32 s70,s69
R 66,2,30720
ds_read_b32 v7,v6 offset:1024
M 30,v[160:163],42
s_mov_b32 s69,s68
ds_read_b64 v[78:79],v13 offset:2048
v_fma_f32 v104,v28,v28,s88
M 30,v[172:175],46
L 5
D 0,56,3072
M 30,v[184:187],50
s_add_u32 s68,s68,s63
R 70,2,31744
v_fma_f32 v105,v29,v29,s88
M 30,v[196:199],54
v_mul_f32 v102,v26,v102
v_exp_f32 v102,v102
M 34,v[188:191],50
v_mul_f32 v103,v27,v103
v_mul_f32 v104,v28,v104
M 34,v[200:203],54
L 2
R 42,1,0
v_mul_f32 v105,v29,v105
M 38,v[192:195],50
v_exp_f32 v103,v103
M 38,v[204:207],54
s_nop 0
v_exp_f32 v104,v104
M 38,v[216:219],58
R 46,1,1024
v_fma_f32 v102,v102,s89,s89
M 38,v[228:231],62
v_exp_f32 v105,v105
M 34,v[212:215],58
buffer_store_dwordx4 v[106:109],v0,s[36:39],s66 offen offset:0 sc0 sc1
v_fma_f32 v103,v103,s89,s89
M 34,v[224:227],62
R 50,1,2048
v_fma_f32 v104,v104,s89,s89
M 30,v[208:211],58
v_rcp_f32 v102,v102
M 30,v[220:223],62
L 3
v_fma_f32 v105,v105,s89,s89
v_mul_f32 v26,v26,v102
M 30,v[232:235],66
R 54,1,3072
v_cmp_eq_u32 vcc,s53,v7
M 30,v[244:247],70
v_rcp_f32 v103,v103
M 34,v[236:239],66
s_cmp_eq_u32 vcc_lo,-1
v_rcp_f32 v104,v104
v_rcp_f32 v105,v105
M 34,v[248:251],70
R 58,1,4096
v_mul_f32 v27,v27,v103
v_mul_f32 v28,v28,v104
v_mul_f32 v29,v29,v105
v_cvt_pk_bf16_f32 v110,v26,v27
v_cvt_pk_bf16_f32 v111,v28,v29
M 38,v[240:243],66
L 3
buffer_store_dwordx2 v[110:111],v10,s[36:39],s66 offen offset:0 sc0 sc1
M 38,v[252:255],70
s_cbranch_scc1 Lg3_F0w3_%=
Lg3_K0_%=:
Lg3_L0_%=:
v_mfma_f32_16x16x32_bf16 v[26:29],a[8:11],v[42:45],0
s_add_u32 m0,s59,3072
R 62,1,5120
v_add_f32_dpp v115,v114,v114 quad_perm:[1,0,3,2] row_mask:0xf bank_mask:0xf
M 26,a[20:23],46
buffer_load_dword v8,s[36:39],s58 offen offset:0 sc1 lds
v_mfma_f32_16x16x32_bf16 v[22:25],a[4:7],v[42:45],0
s_mov_b32 s66,s65
v_add_f32_dpp v115,v115,v115 quad_perm:[2,3,0,1] row_mask:0xf bank_mask:0xf
v_fma_f32 v30,-v78,v82,v30
M 22,a[16:19],46
s_add_u32 s65,s65,s62
R 66,1,6144
v_fma_f32 v31,-v78,v83,v31
v_mfma_f32_16x16x32_bf16 v[18:21],a[0:3],v[42:45],0
v_add_f32_dpp v118,v115,v115 row_half_mirror row_mask:0xf bank_mask:0xf
v_fma_f32 v32,-v78,v84,v32
v_fma_f32 v33,-v78,v85,v33
M 18,a[12:15],46
L 3
v_mul_f32 v30,v79,v30
v_mul_f32 v31,v79,v31
M 18,a[24:27],50
R 70,1,7168
v_mov_b32_dpp v115,v118 row_ror:8 row_mask:0xf bank_mask:0xf
M 18,a[36:39],54
v_fma_f32 v115,-v118,v118,v115
v_max_f32 v115,s87,v115
M 22,a[28:31],50
v_rsq_f32 v116,v115
M 22,a[40:43],54
R 42,1,8192
v_mul_f32 v118,v15,v118
v_fma_f32 v118,v116,v16,v118
M 26,a[32:35],50
ds_write_b32 v12,v118 offset:0
M 26,a[44:47],54
L 4
v_mul_f32 v32,v79,v32
v_mul_f32 v33,v79,v33
M 26,a[56:59],58
R 46,1,9216
v_fma_f32 v94,v30,v30,s88
M 26,a[68:71],62
v_fma_f32 v95,v31,v31,s88
v_fma_f32 v96,v32,v32,s88
M 22,a[52:55],58
v_fma_f32 v97,v33,v33,s88
v_mul_f32 v94,v30,v94
v_mul_f32 v95,v31,v95
M 22,a[64:67],62
R 50,1,10240
v_mul_f32 v96,v32,v96
M 18,a[48:51],58
v_exp_f32 v94,v94
M 18,a[60:63],62
L 4
v_exp_f32 v95,v95
M 18,a[72:75],66
R 54,1,11264
v_mul_f32 v97,v33,v97
M 18,a[84:87],70
v_exp_f32 v96,v96
v_fma_f32 v94,v94,s89,s89
M 22,a[76:79],66
v_exp_f32 v97,v97
M 22,a[88:91],70
R 58,1,12288
v_fma_f32 v95,v95,s89,s89
M 26,a[80:83],66
v_rcp_f32 v94,v94
M 26,a[92:95],70
L 3
v_rcp_f32 v95,v95
M 26,a[104:107],42
R 62,1,13312
v_fma_f32 v96,v96,s89,s89
v_fma_f32 v97,v97,s89,s89
M 26,a[116:119],46
v_rcp_f32 v96,v96
M 22,a[100:103],42
v_rcp_f32 v97,v97
M 22,a[112:115],46
R 66,1,14336
v_mul_f32 v30,v30,v94
M 18,a[96:99],42
v_mul_f32 v31,v31,v95
v_mul_f32 v32,v32,v96
M 18,a[108:111],46
L 3
v_mul_f32 v33,v33,v97
v_cvt_pk_bf16_f32 v106,v30,v31
v_cvt_pk_bf16_f32 v107,v32,v33
M 18,a[120:123],50
R 70,1,15360
v_fma_f32 v34,-v78,v86,v34
M 18,a[132:135],54
s_waitcnt vmcnt(0)
v_fma_f32 v35,-v78,v87,v35
v_fma_f32 v36,-v78,v88,v36
M 22,a[124:127],50
s_barrier
ds_read_b32 v7,v6 offset:3072
v_fma_f32 v37,-v78,v89,v37
M 22,a[136:139],54
R 42,1,16384
v_mul_f32 v34,v79,v34
M 26,a[128:131],50
buffer_store_dword v4,v3,s[36:39],s70 offen offset:0 sc0 sc1
v_mul_f32 v35,v79,v35
M 26,a[140:143],54
L 4
v_mul_f32 v36,v79,v36
v_mul_f32 v37,v79,v37
M 26,a[152:155],58
s_mov_b32 s70,0x7fff0000
R 46,1,17408
v_fma_f32 v98,v34,v34,s88
M 26,a[164:167],62
v_fma_f32 v99,v35,v35,s88
v_fma_f32 v100,v36,v36,s88
M 22,a[148:151],58
L 2
v_cmp_eq_u32 vcc,s53,v7
v_fma_f32 v101,v37,v37,s88
M 22,a[160:163],62
R 50,1,18432
v_mul_f32 v98,v34,v98
v_mul_f32 v99,v35,v99
M 18,a[144:147],58
v_exp_f32 v98,v98
M 18,a[156:159],62
s_cmp_eq_u32 vcc_lo,-1
s_nop 0
v_mul_f32 v100,v36,v100
s_cbranch_scc0 Lg3_X0_%=
M 18,a[168:171],66
s_add_u32 m0,s57,0
s_add_u32 s56,s55,0
s_add_u32 s58,s58,s61
R 54,1,19456
M 18,a[180:183],70
buffer_load_dword v114,v11,s[36:39],s73 offen offset:0 sc1
M 22,a[172:175],66
D 0,56,0
v_mul_f32 v101,v37,v101
M 22,a[184:187],70
s_add_u32 s54,s54,s46
R 58,1,20480
v_fma_f32 v98,v98,s89,s89
M 26,a[176:179],66
s_add_u32 s73,s73,s64
v_exp_f32 v99,v99
M 26,a[188:191],70
L 3
v_exp_f32 v100,v100
M 26,a[200:203],42
R 62,1,21504
v_fma_f32 v99,v99,s89,s89
M 26,a[212:215],46
D 0,56,1024
v_fma_f32 v100,v100,s89,s89
M 22,a[196:199],42
v_exp_f32 v101,v101
M 22,a[208:211],46
R 66,1,22528
v_fma_f32 v101,v101,s89,s89
M 18,a[192:195],42
D 0,56,2048
M 18,a[204:207],46
L 3
v_rcp_f32 v98,v98
M 18,a[216:219],50
R 70,1,23552
v_rcp_f32 v99,v99
M 18,a[228:231],54
v_rcp_f32 v100,v100
M 22,a[220:223],50
D 0,56,3072
M 22,a[232:235],54
s_add_u32 m0,s59,0
R 42,1,24576
v_mul_f32 v34,v34,v98
M 26,a[224:227],50
buffer_load_dword v8,s[36:39],s58 offen offset:0 sc1 lds
M 26,a[236:239],54
L 3
v_rcp_f32 v101,v101
v_mul_f32 v35,v35,v99
M 26,a[248:251],58
s_add_u32 m0,s57,16384
R 46,1,25600
v_mul_f32 v36,v36,v100
M 26,v[132:135],62
s_add_u32 s56,s55,16384
v_mul_f32 v37,v37,v101
v_cvt_pk_bf16_f32 v108,v34,v35
M 22,a[244:247],58
D 0,56,0
M 22,v[128:131],62
s_add_u32 s55,s55,s60
R 50,1,26624
v_cvt_pk_bf16_f32 v109,v36,v37
M 18,a[240:243],58
buffer_store_dwordx4 v[106:109],v0,s[36:39],s67 offen offset:0 sc0 sc1
v_fma_f32 v38,-v78,v90,v38
M 18,a[252:255],62
L 3
v_fma_f32 v39,-v78,v91,v39
v_fma_f32 v40,-v78,v92,v40
M 18,v[136:139],66
s_sub_u32 s57,s84,s57
)"
R"(R 54,1,27648
v_fma_f32 v41,-v78,v93,v41
M 18,v[148:151],70
D 0,56,1024
M 22,v[140:143],66
v_mul_f32 v38,v79,v38
v_mul_f32 v39,v79,v39
M 22,v[152:155],70
R 58,1,28672
v_mul_f32 v40,v79,v40
v_mul_f32 v41,v79,v41
M 26,v[144:147],66
v_fma_f32 v102,v38,v38,s88
v_fma_f32 v103,v39,v39,s88
M 26,v[156:159],70
L 3
v_fma_f32 v104,v40,v40,s88
v_fma_f32 v105,v41,v41,s88
M 26,v[168:171],42
R 62,1,29696
v_mul_f32 v102,v38,v102
M 26,v[180:183],46
D 0,56,2048
M 22,v[164:167],42
v_exp_f32 v102,v102
v_mul_f32 v103,v39,v103
M 22,v[176:179],46
R 66,1,30720
v_mul_f32 v104,v40,v104
M 18,v[160:163],42
v_exp_f32 v103,v103
M 18,v[172:175],46
L 3
D 0,56,3072
M 18,v[184:187],50
s_nop 0
R 70,1,31744
v_mul_f32 v105,v41,v105
M 18,v[196:199],54
s_waitcnt vmcnt(5)
v_exp_f32 v104,v104
v_fma_f32 v102,v102,s89,s89
M 22,v[188:191],50
s_barrier
buffer_store_dword v4,v3,s[36:39],s70 offen offset:0 sc0 sc1
M 22,v[200:203],54
s_mov_b32 s70,s69
R 42,2,0
ds_read_b32 v7,v6 offset:0
M 26,v[192:195],50
s_mov_b32 s69,s68
ds_read_b64 v[74:75],v13 offset:0
v_fma_f32 v103,v103,s89,s89
M 26,v[204:207],54
L 5
v_exp_f32 v105,v105
M 26,v[216:219],58
s_add_u32 s68,s68,s63
R 46,2,1024
v_rcp_f32 v102,v102
M 26,v[228:231],62
v_rcp_f32 v103,v103
M 22,v[212:215],58
v_fma_f32 v104,v104,s89,s89
v_fma_f32 v105,v105,s89,s89
M 22,v[224:227],62
L 2
R 50,2,2048
v_mul_f32 v38,v38,v102
M 18,v[208:211],58
v_rcp_f32 v104,v104
M 18,v[220:223],62
s_nop 0
v_rcp_f32 v105,v105
v_mul_f32 v39,v39,v103
M 18,v[232:235],66
R 54,2,3072
v_mul_f32 v40,v40,v104
M 18,v[244:247],70
v_mul_f32 v41,v41,v105
v_cmp_eq_u32 vcc,s53,v7
M 22,v[236:239],66
v_cvt_pk_bf16_f32 v110,v38,v39
v_cvt_pk_bf16_f32 v111,v40,v41
M 22,v[248:251],70
s_cmp_eq_u32 vcc_lo,-1
R 58,2,4096
M 26,v[240:243],66
L 3
buffer_store_dwordx2 v[110:111],v10,s[36:39],s67 offen offset:0 sc0 sc1
M 26,v[252:255],70
buffer_store_dword v4,v3,s[36:39],s71 offen offset:0 sc0 sc1
s_cbranch_scc1 Lg3_F1_%=
s_branch Lg3_K1_%=
Lg3_X0_%=:
M 18,a[168:171],66
s_add_u32 m0,s59,0
R 54,1,19456
v_mul_f32 v101,v37,v101
M 18,a[180:183],70
buffer_load_dword v8,s[36:39],s58 offen offset:0 sc1 lds
M 22,a[172:175],66
v_exp_f32 v99,v99
M 22,a[184:187],70
R 58,1,20480
v_fma_f32 v98,v98,s89,s89
M 26,a[176:179],66
v_exp_f32 v100,v100
M 26,a[188:191],70
L 3
v_exp_f32 v101,v101
M 26,a[200:203],42
R 62,1,21504
v_fma_f32 v99,v99,s89,s89
M 26,a[212:215],46
v_rcp_f32 v98,v98
M 22,a[196:199],42
v_rcp_f32 v99,v99
M 22,a[208:211],46
R 66,1,22528
v_fma_f32 v100,v100,s89,s89
M 18,a[192:195],42
v_rcp_f32 v100,v100
M 18,a[204:207],46
L 3
v_fma_f32 v101,v101,s89,s89
v_mul_f32 v34,v34,v98
M 18,a[216:219],50
R 70,1,23552
v_mul_f32 v35,v35,v99
M 18,a[228:231],54
v_rcp_f32 v101,v101
M 22,a[220:223],50
v_mul_f32 v36,v36,v100
v_mul_f32 v37,v37,v101
M 22,a[232:235],54
R 42,1,24576
v_cvt_pk_bf16_f32 v108,v34,v35
M 26,a[224:227],50
v_cvt_pk_bf16_f32 v109,v36,v37
v_fma_f32 v38,-v78,v90,v38
M 26,a[236:239],54
L 3
buffer_store_dwordx4 v[106:109],v0,s[36:39],s67 offen offset:0 sc0 sc1
v_fma_f32 v39,-v78,v91,v39
M 26,a[248:251],58
R 46,1,25600
v_fma_f32 v40,-v78,v92,v40
M 26,v[132:135],62
v_fma_f32 v41,-v78,v93,v41
v_mul_f32 v38,v79,v38
M 22,a[244:247],58
v_mul_f32 v39,v79,v39
v_mul_f32 v40,v79,v40
M 22,v[128:131],62
R 50,1,26624
v_mul_f32 v41,v79,v41
M 18,a[240:243],58
v_fma_f32 v102,v38,v38,s88
v_fma_f32 v103,v39,v39,s88
M 18,a[252:255],62
L 3
v_fma_f32 v104,v40,v40,s88
v_fma_f32 v105,v41,v41,s88
M 18,v[136:139],66
R 54,1,27648
v_mul_f32 v102,v38,v102
M 18,v[148:151],70
v_exp_f32 v102,v102
M 22,v[140:143],66
v_mul_f32 v103,v39,v103
v_mul_f32 v104,v40,v104
M 22,v[152:155],70
R 58,1,28672
v_mul_f32 v105,v41,v105
M 26,v[144:147],66
v_exp_f32 v103,v103
M 26,v[156:159],70
L 3
v_exp_f32 v104,v104
M 26,v[168:171],42
R 62,1,29696
v_fma_f32 v102,v102,s89,s89
M 26,v[180:183],46
v_exp_f32 v105,v105
M 22,v[164:167],42
v_rcp_f32 v102,v102
M 22,v[176:179],46
R 66,1,30720
v_fma_f32 v103,v103,s89,s89
M 18,v[160:163],42
v_rcp_f32 v103,v103
M 18,v[172:175],46
L 3
v_fma_f32 v104,v104,s89,s89
v_fma_f32 v105,v105,s89,s89
M 18,v[184:187],50
s_nop 0
R 70,1,31744
v_mul_f32 v38,v38,v102
M 18,v[196:199],54
s_waitcnt vmcnt(1)
v_rcp_f32 v104,v104
M 22,v[188:191],50
s_barrier
buffer_store_dword v4,v3,s[36:39],s70 offen offset:0 sc0 sc1
M 22,v[200:203],54
s_mov_b32 s70,s69
ds_read_b32 v7,v6 offset:0
ds_read_b64 v[74:75],v13 offset:0
M 26,v[192:195],50
s_mov_b32 s69,s68
v_rcp_f32 v105,v105
M 26,v[204:207],54
L 4
v_mul_f32 v39,v39,v103
v_mul_f32 v40,v40,v104
M 26,v[216:219],58
s_add_u32 s68,s68,s63
v_mul_f32 v41,v41,v105
v_cvt_pk_bf16_f32 v110,v38,v39
M 26,v[228:231],62
v_cvt_pk_bf16_f32 v111,v40,v41
M 22,v[212:215],58
buffer_store_dwordx2 v[110:111],v10,s[36:39],s67 offen offset:0 sc0 sc1
M 22,v[224:227],62
L 1
v_cmp_eq_u32 vcc,s53,v7
M 18,v[208:211],58
M 18,v[220:223],62
s_nop 0
M 18,v[232:235],66
M 18,v[244:247],70
M 22,v[236:239],66
M 22,v[248:251],70
M 26,v[240:243],66
M 26,v[252:255],70
s_mov_b32 s80,1
s_cmp_eq_u32 vcc_lo,-1
s_cbranch_scc0 Lg3_xn0_%=
buffer_load_dword v114,v11,s[36:39],s73 offen offset:0 sc1
L 0
v_fma_f32 v18,-v74,v82,v18
v_fma_f32 v19,-v74,v83,v19
v_fma_f32 v20,-v74,v84,v20
v_fma_f32 v21,-v74,v85,v21
v_mul_f32 v18,v75,v18
v_mul_f32 v19,v75,v19
v_mul_f32 v20,v75,v20
v_mul_f32 v21,v75,v21
s_add_u32 s73,s73,s64
s_add_u32 m0,s57,0
s_add_u32 s56,s55,0
D 0,56,0
v_fma_f32 v94,v18,v18,s88
v_fma_f32 v95,v19,v19,s88
v_fma_f32 v96,v20,v20,s88
v_fma_f32 v97,v21,v21,s88
v_mul_f32 v94,v18,v94
v_mul_f32 v95,v19,v95
v_mul_f32 v96,v20,v96
v_mul_f32 v97,v21,v97
D 0,56,1024
v_exp_f32 v94,v94
v_exp_f32 v95,v95
v_exp_f32 v96,v96
v_exp_f32 v97,v97
v_fma_f32 v94,v94,s89,s89
v_fma_f32 v95,v95,s89,s89
v_fma_f32 v96,v96,s89,s89
v_fma_f32 v97,v97,s89,s89
D 0,56,2048
v_rcp_f32 v94,v94
v_rcp_f32 v95,v95
v_rcp_f32 v96,v96
v_rcp_f32 v97,v97
v_mul_f32 v18,v18,v94
v_mul_f32 v19,v19,v95
v_mul_f32 v20,v20,v96
v_mul_f32 v21,v21,v97
D 0,56,3072
v_cvt_pk_bf16_f32 v106,v18,v19
v_cvt_pk_bf16_f32 v107,v20,v21
v_fma_f32 v22,-v74,v86,v22
v_fma_f32 v23,-v74,v87,v23
v_fma_f32 v24,-v74,v88,v24
v_fma_f32 v25,-v74,v89,v25
v_mul_f32 v22,v75,v22
v_mul_f32 v23,v75,v23
s_add_u32 s58,s58,s61
s_add_u32 m0,s57,16384
s_add_u32 s56,s55,16384
D 0,56,0
v_mul_f32 v24,v75,v24
v_mul_f32 v25,v75,v25
v_fma_f32 v98,v22,v22,s88
v_fma_f32 v99,v23,v23,s88
v_fma_f32 v100,v24,v24,s88
v_fma_f32 v101,v25,v25,s88
v_mul_f32 v98,v22,v98
v_mul_f32 v99,v23,v99
D 0,56,1024
v_mul_f32 v100,v24,v100
v_mul_f32 v101,v25,v101
v_exp_f32 v98,v98
v_exp_f32 v99,v99
v_exp_f32 v100,v100
v_exp_f32 v101,v101
v_fma_f32 v98,v98,s89,s89
v_fma_f32 v99,v99,s89,s89
D 0,56,2048
v_fma_f32 v100,v100,s89,s89
v_fma_f32 v101,v101,s89,s89
v_rcp_f32 v98,v98
v_rcp_f32 v99,v99
v_rcp_f32 v100,v100
v_rcp_f32 v101,v101
v_mul_f32 v22,v22,v98
v_mul_f32 v23,v23,v99
D 0,56,3072
v_mul_f32 v24,v24,v100
v_mul_f32 v25,v25,v101
v_cvt_pk_bf16_f32 v108,v22,v23
v_cvt_pk_bf16_f32 v109,v24,v25
buffer_store_dwordx4 v[106:109],v0,s[36:39],s66 offen offset:0 sc0 sc1
v_fma_f32 v26,-v74,v90,v26
v_fma_f32 v27,-v74,v91,v27
v_fma_f32 v28,-v74,v92,v28
v_fma_f32 v29,-v74,v93,v29
s_add_u32 s55,s55,s60
s_sub_u32 s57,s84,s57
s_add_u32 s54,s54,s46
v_mul_f32 v26,v75,v26
v_mul_f32 v27,v75,v27
v_mul_f32 v28,v75,v28
v_mul_f32 v29,v75,v29
v_fma_f32 v102,v26,v26,s88
v_fma_f32 v103,v27,v27,s88
v_fma_f32 v104,v28,v28,s88
v_fma_f32 v105,v29,v29,s88
v_mul_f32 v102,v26,v102
v_mul_f32 v103,v27,v103
v_mul_f32 v104,v28,v104
v_mul_f32 v105,v29,v105
v_exp_f32 v102,v102
v_exp_f32 v103,v103
v_exp_f32 v104,v104
v_exp_f32 v105,v105
v_fma_f32 v102,v102,s89,s89
v_fma_f32 v103,v103,s89,s89
v_fma_f32 v104,v104,s89,s89
v_fma_f32 v105,v105,s89,s89
v_rcp_f32 v102,v102
v_rcp_f32 v103,v103
v_rcp_f32 v104,v104
v_rcp_f32 v105,v105
v_mul_f32 v26,v26,v102
v_mul_f32 v27,v27,v103
v_mul_f32 v28,v28,v104
v_mul_f32 v29,v29,v105
v_cvt_pk_bf16_f32 v110,v26,v27
v_cvt_pk_bf16_f32 v111,v28,v29
buffer_store_dwordx2 v[110:111],v10,s[36:39],s66 offen offset:0 sc0 sc1
s_mov_b32 s72,s69
s_mov_b32 s69,0x7fff0000
s_mov_b32 s66,0x7fff0000
s_mov_b32 s76,0x7fff0000
s_branch Lg3_gox_%=
Lg3_xn0_%=:
s_cmp_ge_u32 s54,256
s_cbranch_scc1 Lg3_fin0_%=
s_add_u32 m0,s59,4096
s_nop 0
buffer_load_dword v8,s[36:39],s58 offen offset:0 sc1 lds
buffer_store_dword v4,v3,s[36:39],s71 offen offset:0 sc0 sc1
s_add_u32 m0,s59,5120
s_nop 0
buffer_load_dword v8,s[36:39],s58 offen offset:0 sc1 lds
buffer_store_dword v4,v3,s[36:39],s71 offen offset:0 sc0 sc1
s_mov_b32 s83,0
L 0
v_fma_f32 v18,-v74,v82,v18
v_fma_f32 v19,-v74,v83,v19
v_fma_f32 v20,-v74,v84,v20
v_fma_f32 v21,-v74,v85,v21
v_mul_f32 v18,v75,v18
v_mul_f32 v19,v75,v19
v_mul_f32 v20,v75,v20
v_mul_f32 v21,v75,v21
v_fma_f32 v94,v18,v18,s88
v_fma_f32 v95,v19,v19,s88
v_fma_f32 v96,v20,v20,s88
v_fma_f32 v97,v21,v21,s88
v_mul_f32 v94,v18,v94
v_mul_f32 v95,v19,v95
v_mul_f32 v96,v20,v96
v_mul_f32 v97,v21,v97
v_exp_f32 v94,v94
v_exp_f32 v95,v95
v_exp_f32 v96,v96
v_exp_f32 v97,v97
v_fma_f32 v94,v94,s89,s89
v_fma_f32 v95,v95,s89,s89
v_fma_f32 v96,v96,s89,s89
v_fma_f32 v97,v97,s89,s89
v_rcp_f32 v94,v94
v_rcp_f32 v95,v95
v_rcp_f32 v96,v96
v_rcp_f32 v97,v97
v_mul_f32 v18,v18,v94
v_mul_f32 v19,v19,v95
v_mul_f32 v20,v20,v96
v_mul_f32 v21,v21,v97
v_cvt_pk_bf16_f32 v106,v18,v19
v_cvt_pk_bf16_f32 v107,v20,v21
v_fma_f32 v22,-v74,v86,v22
v_fma_f32 v23,-v74,v87,v23
v_fma_f32 v24,-v74,v88,v24
v_fma_f32 v25,-v74,v89,v25
v_mul_f32 v22,v75,v22
v_mul_f32 v23,v75,v23
v_mul_f32 v24,v75,v24
v_mul_f32 v25,v75,v25
v_fma_f32 v98,v22,v22,s88
v_fma_f32 v99,v23,v23,s88
v_fma_f32 v100,v24,v24,s88
v_fma_f32 v101,v25,v25,s88
v_mul_f32 v98,v22,v98
v_mul_f32 v99,v23,v99
v_mul_f32 v100,v24,v100
v_mul_f32 v101,v25,v101
v_exp_f32 v98,v98
v_exp_f32 v99,v99
v_exp_f32 v100,v100
v_exp_f32 v101,v101
v_fma_f32 v98,v98,s89,s89
v_fma_f32 v99,v99,s89,s89
v_fma_f32 v100,v100,s89,s89
v_fma_f32 v101,v101,s89,s89
v_rcp_f32 v98,v98
v_rcp_f32 v99,v99
v_rcp_f32 v100,v100
v_rcp_f32 v101,v101
v_mul_f32 v22,v22,v98
v_mul_f32 v23,v23,v99
v_mul_f32 v24,v24,v100
v_mul_f32 v25,v25,v101
v_cvt_pk_bf16_f32 v108,v22,v23
v_cvt_pk_bf16_f32 v109,v24,v25
buffer_store_dwordx4 v[106:109],v0,s[36:39],s66 offen offset:0 sc0 sc1
v_fma_f32 v26,-v74,v90,v26
v_fma_f32 v27,-v74,v91,v27
v_fma_f32 v28,-v74,v92,v28
v_fma_f32 v29,-v74,v93,v29
v_mul_f32 v26,v75,v26
v_mul_f32 v27,v75,v27
v_mul_f32 v28,v75,v28
v_mul_f32 v29,v75,v29
v_fma_f32 v102,v26,v26,s88
v_fma_f32 v103,v27,v27,s88
v_fma_f32 v104,v28,v28,s88
v_fma_f32 v105,v29,v29,s88
v_mul_f32 v102,v26,v102
v_mul_f32 v103,v27,v103
v_mul_f32 v104,v28,v104
v_mul_f32 v105,v29,v105
v_exp_f32 v102,v102
v_exp_f32 v103,v103
v_exp_f32 v104,v104
v_exp_f32 v105,v105
v_fma_f32 v102,v102,s89,s89
v_fma_f32 v103,v103,s89,s89
v_fma_f32 v104,v104,s89,s89
v_fma_f32 v105,v105,s89,s89
v_rcp_f32 v102,v102
v_rcp_f32 v103,v103
v_rcp_f32 v104,v104
v_rcp_f32 v105,v105
v_mul_f32 v26,v26,v102
v_mul_f32 v27,v27,v103
v_mul_f32 v28,v28,v104
v_mul_f32 v29,v29,v105
v_cvt_pk_bf16_f32 v110,v26,v27
v_cvt_pk_bf16_f32 v111,v28,v29
buffer_store_dwordx2 v[110:111],v10,s[36:39],s66 offen offset:0 sc0 sc1
s_mov_b32 s72,s69
s_mov_b32 s69,0x7fff0000
s_mov_b32 s66,0x7fff0000
s_mov_b32 s76,0x7fff0000
s_branch Lg3_poll_%=
Lg3_fin0_%=:
L 0
v_fma_f32 v18,-v74,v82,v18
v_fma_f32 v19,-v74,v83,v19
v_fma_f32 v20,-v74,v84,v20
v_fma_f32 v21,-v74,v85,v21
v_mul_f32 v18,v75,v18
v_mul_f32 v19,v75,v19
v_mul_f32 v20,v75,v20
v_mul_f32 v21,v75,v21
)"
R"(v_fma_f32 v94,v18,v18,s88
v_fma_f32 v95,v19,v19,s88
v_fma_f32 v96,v20,v20,s88
v_fma_f32 v97,v21,v21,s88
v_mul_f32 v94,v18,v94
v_mul_f32 v95,v19,v95
v_mul_f32 v96,v20,v96
v_mul_f32 v97,v21,v97
v_exp_f32 v94,v94
v_exp_f32 v95,v95
v_exp_f32 v96,v96
v_exp_f32 v97,v97
v_fma_f32 v94,v94,s89,s89
v_fma_f32 v95,v95,s89,s89
v_fma_f32 v96,v96,s89,s89
v_fma_f32 v97,v97,s89,s89
v_rcp_f32 v94,v94
v_rcp_f32 v95,v95
v_rcp_f32 v96,v96
v_rcp_f32 v97,v97
v_mul_f32 v18,v18,v94
v_mul_f32 v19,v19,v95
v_mul_f32 v20,v20,v96
v_mul_f32 v21,v21,v97
v_cvt_pk_bf16_f32 v106,v18,v19
v_cvt_pk_bf16_f32 v107,v20,v21
v_fma_f32 v22,-v74,v86,v22
v_fma_f32 v23,-v74,v87,v23
v_fma_f32 v24,-v74,v88,v24
v_fma_f32 v25,-v74,v89,v25
v_mul_f32 v22,v75,v22
v_mul_f32 v23,v75,v23
v_mul_f32 v24,v75,v24
v_mul_f32 v25,v75,v25
v_fma_f32 v98,v22,v22,s88
v_fma_f32 v99,v23,v23,s88
v_fma_f32 v100,v24,v24,s88
v_fma_f32 v101,v25,v25,s88
v_mul_f32 v98,v22,v98
v_mul_f32 v99,v23,v99
v_mul_f32 v100,v24,v100
v_mul_f32 v101,v25,v101
v_exp_f32 v98,v98
v_exp_f32 v99,v99
v_exp_f32 v100,v100
v_exp_f32 v101,v101
v_fma_f32 v98,v98,s89,s89
v_fma_f32 v99,v99,s89,s89
v_fma_f32 v100,v100,s89,s89
v_fma_f32 v101,v101,s89,s89
v_rcp_f32 v98,v98
v_rcp_f32 v99,v99
v_rcp_f32 v100,v100
v_rcp_f32 v101,v101
v_mul_f32 v22,v22,v98
v_mul_f32 v23,v23,v99
v_mul_f32 v24,v24,v100
v_mul_f32 v25,v25,v101
v_cvt_pk_bf16_f32 v108,v22,v23
v_cvt_pk_bf16_f32 v109,v24,v25
buffer_store_dwordx4 v[106:109],v0,s[36:39],s66 offen offset:0 sc0 sc1
v_fma_f32 v26,-v74,v90,v26
v_fma_f32 v27,-v74,v91,v27
v_fma_f32 v28,-v74,v92,v28
v_fma_f32 v29,-v74,v93,v29
v_mul_f32 v26,v75,v26
v_mul_f32 v27,v75,v27
v_mul_f32 v28,v75,v28
v_mul_f32 v29,v75,v29
v_fma_f32 v102,v26,v26,s88
v_fma_f32 v103,v27,v27,s88
v_fma_f32 v104,v28,v28,s88
v_fma_f32 v105,v29,v29,s88
v_mul_f32 v102,v26,v102
v_mul_f32 v103,v27,v103
v_mul_f32 v104,v28,v104
v_mul_f32 v105,v29,v105
v_exp_f32 v102,v102
v_exp_f32 v103,v103
v_exp_f32 v104,v104
v_exp_f32 v105,v105
v_fma_f32 v102,v102,s89,s89
v_fma_f32 v103,v103,s89,s89
v_fma_f32 v104,v104,s89,s89
v_fma_f32 v105,v105,s89,s89
v_rcp_f32 v102,v102
v_rcp_f32 v103,v103
v_rcp_f32 v104,v104
v_rcp_f32 v105,v105
v_mul_f32 v26,v26,v102
v_mul_f32 v27,v27,v103
v_mul_f32 v28,v28,v104
v_mul_f32 v29,v29,v105
v_cvt_pk_bf16_f32 v110,v26,v27
v_cvt_pk_bf16_f32 v111,v28,v29
buffer_store_dwordx2 v[110:111],v10,s[36:39],s66 offen offset:0 sc0 sc1
s_mov_b32 s72,s69
s_mov_b32 s69,0x7fff0000
s_mov_b32 s66,0x7fff0000
s_mov_b32 s76,0x7fff0000
s_branch Lg3_done_%=
Lg3_K1_%=:
Lg3_L1_%=:
v_mfma_f32_16x16x32_bf16 v[38:41],a[8:11],v[42:45],0
s_add_u32 m0,s59,3072
R 62,2,5120
v_add_f32_dpp v115,v114,v114 quad_perm:[1,0,3,2] row_mask:0xf bank_mask:0xf
M 38,a[20:23],46
buffer_load_dword v8,s[36:39],s58 offen offset:0 sc1 lds
v_mfma_f32_16x16x32_bf16 v[34:37],a[4:7],v[42:45],0
s_mov_b32 s67,s65
v_add_f32_dpp v115,v115,v115 quad_perm:[2,3,0,1] row_mask:0xf bank_mask:0xf
v_fma_f32 v18,-v74,v82,v18
M 34,a[16:19],46
s_add_u32 s65,s65,s62
R 66,2,6144
v_fma_f32 v19,-v74,v83,v19
v_mfma_f32_16x16x32_bf16 v[30:33],a[0:3],v[42:45],0
v_add_f32_dpp v118,v115,v115 row_half_mirror row_mask:0xf bank_mask:0xf
v_fma_f32 v20,-v74,v84,v20
v_fma_f32 v21,-v74,v85,v21
M 30,a[12:15],46
L 3
v_mul_f32 v18,v75,v18
v_mul_f32 v19,v75,v19
M 30,a[24:27],50
R 70,2,7168
v_mov_b32_dpp v115,v118 row_ror:8 row_mask:0xf bank_mask:0xf
M 30,a[36:39],54
v_fma_f32 v115,-v118,v118,v115
v_max_f32 v115,s87,v115
M 34,a[28:31],50
v_rsq_f32 v116,v115
M 34,a[40:43],54
R 42,2,8192
v_mul_f32 v118,v15,v118
v_fma_f32 v118,v116,v16,v118
M 38,a[32:35],50
ds_write_b32 v12,v118 offset:2048
M 38,a[44:47],54
L 4
v_mul_f32 v20,v75,v20
v_mul_f32 v21,v75,v21
M 38,a[56:59],58
R 46,2,9216
v_fma_f32 v94,v18,v18,s88
M 38,a[68:71],62
v_fma_f32 v95,v19,v19,s88
v_fma_f32 v96,v20,v20,s88
M 34,a[52:55],58
v_fma_f32 v97,v21,v21,s88
v_mul_f32 v94,v18,v94
v_mul_f32 v95,v19,v95
M 34,a[64:67],62
R 50,2,10240
v_mul_f32 v96,v20,v96
M 30,a[48:51],58
v_exp_f32 v94,v94
M 30,a[60:63],62
L 4
v_exp_f32 v95,v95
M 30,a[72:75],66
R 54,2,11264
v_mul_f32 v97,v21,v97
M 30,a[84:87],70
v_exp_f32 v96,v96
v_fma_f32 v94,v94,s89,s89
M 34,a[76:79],66
v_exp_f32 v97,v97
M 34,a[88:91],70
R 58,2,12288
v_fma_f32 v95,v95,s89,s89
M 38,a[80:83],66
v_rcp_f32 v94,v94
M 38,a[92:95],70
L 3
v_rcp_f32 v95,v95
M 38,a[104:107],42
R 62,2,13312
v_fma_f32 v96,v96,s89,s89
v_fma_f32 v97,v97,s89,s89
M 38,a[116:119],46
v_rcp_f32 v96,v96
M 34,a[100:103],42
v_rcp_f32 v97,v97
M 34,a[112:115],46
R 66,2,14336
v_mul_f32 v18,v18,v94
M 30,a[96:99],42
v_mul_f32 v19,v19,v95
v_mul_f32 v20,v20,v96
M 30,a[108:111],46
L 3
v_mul_f32 v21,v21,v97
v_cvt_pk_bf16_f32 v106,v18,v19
v_cvt_pk_bf16_f32 v107,v20,v21
M 30,a[120:123],50
R 70,2,15360
v_fma_f32 v22,-v74,v86,v22
M 30,a[132:135],54
s_waitcnt vmcnt(0)
v_fma_f32 v23,-v74,v87,v23
v_fma_f32 v24,-v74,v88,v24
M 34,a[124:127],50
s_barrier
ds_read_b32 v7,v6 offset:3072
v_fma_f32 v25,-v74,v89,v25
M 34,a[136:139],54
R 42,2,16384
v_mul_f32 v22,v75,v22
M 38,a[128:131],50
buffer_store_dword v4,v3,s[36:39],s70 offen offset:0 sc0 sc1
v_mul_f32 v23,v75,v23
M 38,a[140:143],54
L 4
v_mul_f32 v24,v75,v24
v_mul_f32 v25,v75,v25
M 38,a[152:155],58
s_mov_b32 s70,0x7fff0000
R 46,2,17408
v_fma_f32 v98,v22,v22,s88
M 38,a[164:167],62
v_fma_f32 v99,v23,v23,s88
v_fma_f32 v100,v24,v24,s88
M 34,a[148:151],58
L 2
v_cmp_eq_u32 vcc,s53,v7
v_fma_f32 v101,v25,v25,s88
M 34,a[160:163],62
R 50,2,18432
v_mul_f32 v98,v22,v98
v_mul_f32 v99,v23,v99
M 30,a[144:147],58
v_exp_f32 v98,v98
M 30,a[156:159],62
s_cmp_eq_u32 vcc_lo,-1
s_nop 0
v_mul_f32 v100,v24,v100
s_cbranch_scc0 Lg3_X1_%=
M 30,a[168:171],66
s_add_u32 m0,s57,0
s_add_u32 s56,s55,0
s_add_u32 s58,s58,s61
R 54,2,19456
M 30,a[180:183],70
buffer_load_dword v114,v11,s[36:39],s73 offen offset:0 sc1
M 34,a[172:175],66
D 0,56,0
v_mul_f32 v101,v25,v101
M 34,a[184:187],70
s_add_u32 s54,s54,s46
R 58,2,20480
v_fma_f32 v98,v98,s89,s89
M 38,a[176:179],66
s_add_u32 s73,s73,s64
v_exp_f32 v99,v99
M 38,a[188:191],70
L 3
v_exp_f32 v100,v100
M 38,a[200:203],42
R 62,2,21504
v_fma_f32 v99,v99,s89,s89
M 38,a[212:215],46
D 0,56,1024
v_fma_f32 v100,v100,s89,s89
M 34,a[196:199],42
v_exp_f32 v101,v101
M 34,a[208:211],46
R 66,2,22528
v_fma_f32 v101,v101,s89,s89
M 30,a[192:195],42
D 0,56,2048
M 30,a[204:207],46
L 3
v_rcp_f32 v98,v98
M 30,a[216:219],50
R 70,2,23552
v_rcp_f32 v99,v99
M 30,a[228:231],54
v_rcp_f32 v100,v100
M 34,a[220:223],50
D 0,56,3072
M 34,a[232:235],54
s_add_u32 m0,s59,1024
R 42,2,24576
v_mul_f32 v22,v22,v98
M 38,a[224:227],50
buffer_load_dword v8,s[36:39],s58 offen offset:0 sc1 lds
M 38,a[236:239],54
L 3
v_rcp_f32 v101,v101
v_mul_f32 v23,v23,v99
M 38,a[248:251],58
s_add_u32 m0,s57,16384
R 46,2,25600
v_mul_f32 v24,v24,v100
M 38,v[132:135],62
s_add_u32 s56,s55,16384
v_mul_f32 v25,v25,v101
v_cvt_pk_bf16_f32 v108,v22,v23
M 34,a[244:247],58
D 0,56,0
M 34,v[128:131],62
s_add_u32 s55,s55,s60
R 50,2,26624
v_cvt_pk_bf16_f32 v109,v24,v25
M 30,a[240:243],58
buffer_store_dwordx4 v[106:109],v0,s[36:39],s66 offen offset:0 sc0 sc1
v_fma_f32 v26,-v74,v90,v26
M 30,a[252:255],62
L 3
v_fma_f32 v27,-v74,v91,v27
v_fma_f32 v28,-v74,v92,v28
M 30,v[136:139],66
s_sub_u32 s57,s84,s57
R 54,2,27648
v_fma_f32 v29,-v74,v93,v29
M 30,v[148:151],70
D 0,56,1024
M 34,v[140:143],66
v_mul_f32 v26,v75,v26
v_mul_f32 v27,v75,v27
M 34,v[152:155],70
R 58,2,28672
v_mul_f32 v28,v75,v28
v_mul_f32 v29,v75,v29
M 38,v[144:147],66
v_fma_f32 v102,v26,v26,s88
v_fma_f32 v103,v27,v27,s88
M 38,v[156:159],70
L 3
v_fma_f32 v104,v28,v28,s88
v_fma_f32 v105,v29,v29,s88
M 38,v[168:171],42
R 62,2,29696
v_mul_f32 v102,v26,v102
M 38,v[180:183],46
D 0,56,2048
M 34,v[164:167],42
v_exp_f32 v102,v102
v_mul_f32 v103,v27,v103
M 34,v[176:179],46
R 66,2,30720
v_mul_f32 v104,v28,v104
M 30,v[160:163],42
v_exp_f32 v103,v103
M 30,v[172:175],46
L 3
D 0,56,3072
M 30,v[184:187],50
s_nop 0
R 70,2,31744
v_mul_f32 v105,v29,v105
M 30,v[196:199],54
s_waitcnt vmcnt(5)
v_exp_f32 v104,v104
v_fma_f32 v102,v102,s89,s89
M 34,v[188:191],50
s_barrier
buffer_store_dword v4,v3,s[36:39],s70 offen offset:0 sc0 sc1
M 34,v[200:203],54
s_mov_b32 s70,s69
R 42,1,0
ds_read_b32 v7,v6 offset:1024
M 38,v[192:195],50
s_mov_b32 s69,s68
ds_read_b64 v[78:79],v13 offset:2048
v_fma_f32 v103,v103,s89,s89
M 38,v[204:207],54
L 5
v_exp_f32 v105,v105
M 38,v[216:219],58
s_add_u32 s68,s68,s63
R 46,1,1024
v_rcp_f32 v102,v102
M 38,v[228:231],62
v_rcp_f32 v103,v103
M 34,v[212:215],58
v_fma_f32 v104,v104,s89,s89
v_fma_f32 v105,v105,s89,s89
M 34,v[224:227],62
L 2
R 50,1,2048
v_mul_f32 v26,v26,v102
M 30,v[208:211],58
v_rcp_f32 v104,v104
M 30,v[220:223],62
s_nop 0
v_rcp_f32 v105,v105
v_mul_f32 v27,v27,v103
M 30,v[232:235],66
R 54,1,3072
v_mul_f32 v28,v28,v104
M 30,v[244:247],70
v_mul_f32 v29,v29,v105
v_cmp_eq_u32 vcc,s53,v7
M 34,v[236:239],66
v_cvt_pk_bf16_f32 v110,v26,v27
v_cvt_pk_bf16_f32 v111,v28,v29
M 34,v[248:251],70
s_cmp_eq_u32 vcc_lo,-1
R 58,1,4096
M 38,v[240:243],66
L 3
buffer_store_dwordx2 v[110:111],v10,s[36:39],s66 offen offset:0 sc0 sc1
M 38,v[252:255],70
buffer_store_dword v4,v3,s[36:39],s71 offen offset:0 sc0 sc1
s_cbranch_scc1 Lg3_F0_%=
s_branch Lg3_K0_%=
Lg3_X1_%=:
M 30,a[168:171],66
s_add_u32 m0,s59,1024
R 54,2,19456
v_mul_f32 v101,v25,v101
M 30,a[180:183],70
buffer_load_dword v8,s[36:39],s58 offen offset:0 sc1 lds
M 34,a[172:175],66
v_exp_f32 v99,v99
M 34,a[184:187],70
R 58,2,20480
v_fma_f32 v98,v98,s89,s89
M 38,a[176:179],66
v_exp_f32 v100,v100
M 38,a[188:191],70
L 3
v_exp_f32 v101,v101
M 38,a[200:203],42
R 62,2,21504
v_fma_f32 v99,v99,s89,s89
M 38,a[212:215],46
v_rcp_f32 v98,v98
M 34,a[196:199],42
v_rcp_f32 v99,v99
M 34,a[208:211],46
R 66,2,22528
v_fma_f32 v100,v100,s89,s89
M 30,a[192:195],42
v_rcp_f32 v100,v100
M 30,a[204:207],46
L 3
v_fma_f32 v101,v101,s89,s89
v_mul_f32 v22,v22,v98
M 30,a[216:219],50
R 70,2,23552
v_mul_f32 v23,v23,v99
M 30,a[228:231],54
v_rcp_f32 v101,v101
M 34,a[220:223],50
v_mul_f32 v24,v24,v100
v_mul_f32 v25,v25,v101
M 34,a[232:235],54
R 42,2,24576
v_cvt_pk_bf16_f32 v108,v22,v23
M 38,a[224:227],50
v_cvt_pk_bf16_f32 v109,v24,v25
v_fma_f32 v26,-v74,v90,v26
M 38,a[236:239],54
L 3
buffer_store_dwordx4 v[106:109],v0,s[36:39],s66 offen offset:0 sc0 sc1
v_fma_f32 v27,-v74,v91,v27
M 38,a[248:251],58
R 46,2,25600
v_fma_f32 v28,-v74,v92,v28
M 38,v[132:135],62
v_fma_f32 v29,-v74,v93,v29
v_mul_f32 v26,v75,v26
M 34,a[244:247],58
v_mul_f32 v27,v75,v27
v_mul_f32 v28,v75,v28
M 34,v[128:131],62
R 50,2,26624
v_mul_f32 v29,v75,v29
M 30,a[240:243],58
v_fma_f32 v102,v26,v26,s88
v_fma_f32 v103,v27,v27,s88
M 30,a[252:255],62
L 3
v_fma_f32 v104,v28,v28,s88
v_fma_f32 v105,v29,v29,s88
M 30,v[136:139],66
R 54,2,27648
v_mul_f32 v102,v26,v102
M 30,v[148:151],70
v_exp_f32 v102,v102
M 34,v[140:143],66
v_mul_f32 v103,v27,v103
v_mul_f32 v104,v28,v104
M 34,v[152:155],70
R 58,2,28672
v_mul_f32 v105,v29,v105
M 38,v[144:147],66
v_exp_f32 v103,v103
M 38,v[156:159],70
L 3
v_exp_f32 v104,v104
M 38,v[168:171],42
R 62,2,29696
v_fma_f32 v102,v102,s89,s89
M 38,v[180:183],46
v_exp_f32 v105,v105
)"
R"(M 34,v[164:167],42
v_rcp_f32 v102,v102
M 34,v[176:179],46
R 66,2,30720
v_fma_f32 v103,v103,s89,s89
M 30,v[160:163],42
v_rcp_f32 v103,v103
M 30,v[172:175],46
L 3
v_fma_f32 v104,v104,s89,s89
v_fma_f32 v105,v105,s89,s89
M 30,v[184:187],50
s_nop 0
R 70,2,31744
v_mul_f32 v26,v26,v102
M 30,v[196:199],54
s_waitcnt vmcnt(1)
v_rcp_f32 v104,v104
M 34,v[188:191],50
s_barrier
buffer_store_dword v4,v3,s[36:39],s70 offen offset:0 sc0 sc1
M 34,v[200:203],54
s_mov_b32 s70,s69
ds_read_b32 v7,v6 offset:1024
ds_read_b64 v[78:79],v13 offset:2048
M 38,v[192:195],50
s_mov_b32 s69,s68
v_rcp_f32 v105,v105
M 38,v[204:207],54
L 4
v_mul_f32 v27,v27,v103
v_mul_f32 v28,v28,v104
M 38,v[216:219],58
s_add_u32 s68,s68,s63
v_mul_f32 v29,v29,v105
v_cvt_pk_bf16_f32 v110,v26,v27
M 38,v[228:231],62
v_cvt_pk_bf16_f32 v111,v28,v29
M 34,v[212:215],58
buffer_store_dwordx2 v[110:111],v10,s[36:39],s66 offen offset:0 sc0 sc1
M 34,v[224:227],62
L 1
v_cmp_eq_u32 vcc,s53,v7
M 30,v[208:211],58
M 30,v[220:223],62
s_nop 0
M 30,v[232:235],66
M 30,v[244:247],70
M 34,v[236:239],66
M 34,v[248:251],70
M 38,v[240:243],66
M 38,v[252:255],70
s_mov_b32 s80,0
s_cmp_eq_u32 vcc_lo,-1
s_cbranch_scc0 Lg3_xn1_%=
buffer_load_dword v114,v11,s[36:39],s73 offen offset:0 sc1
L 0
v_fma_f32 v30,-v78,v82,v30
v_fma_f32 v31,-v78,v83,v31
v_fma_f32 v32,-v78,v84,v32
v_fma_f32 v33,-v78,v85,v33
v_mul_f32 v30,v79,v30
v_mul_f32 v31,v79,v31
v_mul_f32 v32,v79,v32
v_mul_f32 v33,v79,v33
s_add_u32 s73,s73,s64
s_add_u32 m0,s57,0
s_add_u32 s56,s55,0
D 0,56,0
v_fma_f32 v94,v30,v30,s88
v_fma_f32 v95,v31,v31,s88
v_fma_f32 v96,v32,v32,s88
v_fma_f32 v97,v33,v33,s88
v_mul_f32 v94,v30,v94
v_mul_f32 v95,v31,v95
v_mul_f32 v96,v32,v96
v_mul_f32 v97,v33,v97
D 0,56,1024
v_exp_f32 v94,v94
v_exp_f32 v95,v95
v_exp_f32 v96,v96
v_exp_f32 v97,v97
v_fma_f32 v94,v94,s89,s89
v_fma_f32 v95,v95,s89,s89
v_fma_f32 v96,v96,s89,s89
v_fma_f32 v97,v97,s89,s89
D 0,56,2048
v_rcp_f32 v94,v94
v_rcp_f32 v95,v95
v_rcp_f32 v96,v96
v_rcp_f32 v97,v97
v_mul_f32 v30,v30,v94
v_mul_f32 v31,v31,v95
v_mul_f32 v32,v32,v96
v_mul_f32 v33,v33,v97
D 0,56,3072
v_cvt_pk_bf16_f32 v106,v30,v31
v_cvt_pk_bf16_f32 v107,v32,v33
v_fma_f32 v34,-v78,v86,v34
v_fma_f32 v35,-v78,v87,v35
v_fma_f32 v36,-v78,v88,v36
v_fma_f32 v37,-v78,v89,v37
v_mul_f32 v34,v79,v34
v_mul_f32 v35,v79,v35
s_add_u32 s58,s58,s61
s_add_u32 m0,s57,16384
s_add_u32 s56,s55,16384
D 0,56,0
v_mul_f32 v36,v79,v36
v_mul_f32 v37,v79,v37
v_fma_f32 v98,v34,v34,s88
v_fma_f32 v99,v35,v35,s88
v_fma_f32 v100,v36,v36,s88
v_fma_f32 v101,v37,v37,s88
v_mul_f32 v98,v34,v98
v_mul_f32 v99,v35,v99
D 0,56,1024
v_mul_f32 v100,v36,v100
v_mul_f32 v101,v37,v101
v_exp_f32 v98,v98
v_exp_f32 v99,v99
v_exp_f32 v100,v100
v_exp_f32 v101,v101
v_fma_f32 v98,v98,s89,s89
v_fma_f32 v99,v99,s89,s89
D 0,56,2048
v_fma_f32 v100,v100,s89,s89
v_fma_f32 v101,v101,s89,s89
v_rcp_f32 v98,v98
v_rcp_f32 v99,v99
v_rcp_f32 v100,v100
v_rcp_f32 v101,v101
v_mul_f32 v34,v34,v98
v_mul_f32 v35,v35,v99
D 0,56,3072
v_mul_f32 v36,v36,v100
v_mul_f32 v37,v37,v101
v_cvt_pk_bf16_f32 v108,v34,v35
v_cvt_pk_bf16_f32 v109,v36,v37
buffer_store_dwordx4 v[106:109],v0,s[36:39],s67 offen offset:0 sc0 sc1
v_fma_f32 v38,-v78,v90,v38
v_fma_f32 v39,-v78,v91,v39
v_fma_f32 v40,-v78,v92,v40
v_fma_f32 v41,-v78,v93,v41
s_add_u32 s55,s55,s60
s_sub_u32 s57,s84,s57
s_add_u32 s54,s54,s46
v_mul_f32 v38,v79,v38
v_mul_f32 v39,v79,v39
v_mul_f32 v40,v79,v40
v_mul_f32 v41,v79,v41
v_fma_f32 v102,v38,v38,s88
v_fma_f32 v103,v39,v39,s88
v_fma_f32 v104,v40,v40,s88
v_fma_f32 v105,v41,v41,s88
v_mul_f32 v102,v38,v102
v_mul_f32 v103,v39,v103
v_mul_f32 v104,v40,v104
v_mul_f32 v105,v41,v105
v_exp_f32 v102,v102
v_exp_f32 v103,v103
v_exp_f32 v104,v104
v_exp_f32 v105,v105
v_fma_f32 v102,v102,s89,s89
v_fma_f32 v103,v103,s89,s89
v_fma_f32 v104,v104,s89,s89
v_fma_f32 v105,v105,s89,s89
v_rcp_f32 v102,v102
v_rcp_f32 v103,v103
v_rcp_f32 v104,v104
v_rcp_f32 v105,v105
v_mul_f32 v38,v38,v102
v_mul_f32 v39,v39,v103
v_mul_f32 v40,v40,v104
v_mul_f32 v41,v41,v105
v_cvt_pk_bf16_f32 v110,v38,v39
v_cvt_pk_bf16_f32 v111,v40,v41
buffer_store_dwordx2 v[110:111],v10,s[36:39],s67 offen offset:0 sc0 sc1
s_mov_b32 s72,s69
s_mov_b32 s69,0x7fff0000
s_mov_b32 s67,0x7fff0000
s_mov_b32 s77,0x7fff0000
s_branch Lg3_gox_%=
Lg3_xn1_%=:
s_cmp_ge_u32 s54,256
s_cbranch_scc1 Lg3_fin1_%=
s_add_u32 m0,s59,4096
s_nop 0
buffer_load_dword v8,s[36:39],s58 offen offset:0 sc1 lds
buffer_store_dword v4,v3,s[36:39],s71 offen offset:0 sc0 sc1
s_add_u32 m0,s59,5120
s_nop 0
buffer_load_dword v8,s[36:39],s58 offen offset:0 sc1 lds
buffer_store_dword v4,v3,s[36:39],s71 offen offset:0 sc0 sc1
s_mov_b32 s83,0
L 0
v_fma_f32 v30,-v78,v82,v30
v_fma_f32 v31,-v78,v83,v31
v_fma_f32 v32,-v78,v84,v32
v_fma_f32 v33,-v78,v85,v33
v_mul_f32 v30,v79,v30
v_mul_f32 v31,v79,v31
v_mul_f32 v32,v79,v32
v_mul_f32 v33,v79,v33
v_fma_f32 v94,v30,v30,s88
v_fma_f32 v95,v31,v31,s88
v_fma_f32 v96,v32,v32,s88
v_fma_f32 v97,v33,v33,s88
v_mul_f32 v94,v30,v94
v_mul_f32 v95,v31,v95
v_mul_f32 v96,v32,v96
v_mul_f32 v97,v33,v97
v_exp_f32 v94,v94
v_exp_f32 v95,v95
v_exp_f32 v96,v96
v_exp_f32 v97,v97
v_fma_f32 v94,v94,s89,s89
v_fma_f32 v95,v95,s89,s89
v_fma_f32 v96,v96,s89,s89
v_fma_f32 v97,v97,s89,s89
v_rcp_f32 v94,v94
v_rcp_f32 v95,v95
v_rcp_f32 v96,v96
v_rcp_f32 v97,v97
v_mul_f32 v30,v30,v94
v_mul_f32 v31,v31,v95
v_mul_f32 v32,v32,v96
v_mul_f32 v33,v33,v97
v_cvt_pk_bf16_f32 v106,v30,v31
v_cvt_pk_bf16_f32 v107,v32,v33
v_fma_f32 v34,-v78,v86,v34
v_fma_f32 v35,-v78,v87,v35
v_fma_f32 v36,-v78,v88,v36
v_fma_f32 v37,-v78,v89,v37
v_mul_f32 v34,v79,v34
v_mul_f32 v35,v79,v35
v_mul_f32 v36,v79,v36
v_mul_f32 v37,v79,v37
v_fma_f32 v98,v34,v34,s88
v_fma_f32 v99,v35,v35,s88
v_fma_f32 v100,v36,v36,s88
v_fma_f32 v101,v37,v37,s88
v_mul_f32 v98,v34,v98
v_mul_f32 v99,v35,v99
v_mul_f32 v100,v36,v100
v_mul_f32 v101,v37,v101
v_exp_f32 v98,v98
v_exp_f32 v99,v99
v_exp_f32 v100,v100
v_exp_f32 v101,v101
v_fma_f32 v98,v98,s89,s89
v_fma_f32 v99,v99,s89,s89
v_fma_f32 v100,v100,s89,s89
v_fma_f32 v101,v101,s89,s89
v_rcp_f32 v98,v98
v_rcp_f32 v99,v99
v_rcp_f32 v100,v100
v_rcp_f32 v101,v101
v_mul_f32 v34,v34,v98
v_mul_f32 v35,v35,v99
v_mul_f32 v36,v36,v100
v_mul_f32 v37,v37,v101
v_cvt_pk_bf16_f32 v108,v34,v35
v_cvt_pk_bf16_f32 v109,v36,v37
buffer_store_dwordx4 v[106:109],v0,s[36:39],s67 offen offset:0 sc0 sc1
v_fma_f32 v38,-v78,v90,v38
v_fma_f32 v39,-v78,v91,v39
v_fma_f32 v40,-v78,v92,v40
v_fma_f32 v41,-v78,v93,v41
v_mul_f32 v38,v79,v38
v_mul_f32 v39,v79,v39
v_mul_f32 v40,v79,v40
v_mul_f32 v41,v79,v41
v_fma_f32 v102,v38,v38,s88
v_fma_f32 v103,v39,v39,s88
v_fma_f32 v104,v40,v40,s88
v_fma_f32 v105,v41,v41,s88
v_mul_f32 v102,v38,v102
v_mul_f32 v103,v39,v103
v_mul_f32 v104,v40,v104
v_mul_f32 v105,v41,v105
v_exp_f32 v102,v102
v_exp_f32 v103,v103
v_exp_f32 v104,v104
v_exp_f32 v105,v105
v_fma_f32 v102,v102,s89,s89
v_fma_f32 v103,v103,s89,s89
v_fma_f32 v104,v104,s89,s89
v_fma_f32 v105,v105,s89,s89
v_rcp_f32 v102,v102
v_rcp_f32 v103,v103
v_rcp_f32 v104,v104
v_rcp_f32 v105,v105
v_mul_f32 v38,v38,v102
v_mul_f32 v39,v39,v103
v_mul_f32 v40,v40,v104
v_mul_f32 v41,v41,v105
v_cvt_pk_bf16_f32 v110,v38,v39
v_cvt_pk_bf16_f32 v111,v40,v41
buffer_store_dwordx2 v[110:111],v10,s[36:39],s67 offen offset:0 sc0 sc1
s_mov_b32 s72,s69
s_mov_b32 s69,0x7fff0000
s_mov_b32 s67,0x7fff0000
s_mov_b32 s77,0x7fff0000
s_branch Lg3_poll_%=
Lg3_fin1_%=:
L 0
v_fma_f32 v30,-v78,v82,v30
v_fma_f32 v31,-v78,v83,v31
v_fma_f32 v32,-v78,v84,v32
v_fma_f32 v33,-v78,v85,v33
v_mul_f32 v30,v79,v30
v_mul_f32 v31,v79,v31
v_mul_f32 v32,v79,v32
v_mul_f32 v33,v79,v33
v_fma_f32 v94,v30,v30,s88
v_fma_f32 v95,v31,v31,s88
v_fma_f32 v96,v32,v32,s88
v_fma_f32 v97,v33,v33,s88
v_mul_f32 v94,v30,v94
v_mul_f32 v95,v31,v95
v_mul_f32 v96,v32,v96
v_mul_f32 v97,v33,v97
v_exp_f32 v94,v94
v_exp_f32 v95,v95
v_exp_f32 v96,v96
v_exp_f32 v97,v97
v_fma_f32 v94,v94,s89,s89
v_fma_f32 v95,v95,s89,s89
v_fma_f32 v96,v96,s89,s89
v_fma_f32 v97,v97,s89,s89
v_rcp_f32 v94,v94
v_rcp_f32 v95,v95
v_rcp_f32 v96,v96
v_rcp_f32 v97,v97
v_mul_f32 v30,v30,v94
v_mul_f32 v31,v31,v95
v_mul_f32 v32,v32,v96
v_mul_f32 v33,v33,v97
v_cvt_pk_bf16_f32 v106,v30,v31
v_cvt_pk_bf16_f32 v107,v32,v33
v_fma_f32 v34,-v78,v86,v34
v_fma_f32 v35,-v78,v87,v35
v_fma_f32 v36,-v78,v88,v36
v_fma_f32 v37,-v78,v89,v37
v_mul_f32 v34,v79,v34
v_mul_f32 v35,v79,v35
v_mul_f32 v36,v79,v36
v_mul_f32 v37,v79,v37
v_fma_f32 v98,v34,v34,s88
v_fma_f32 v99,v35,v35,s88
v_fma_f32 v100,v36,v36,s88
v_fma_f32 v101,v37,v37,s88
v_mul_f32 v98,v34,v98
v_mul_f32 v99,v35,v99
v_mul_f32 v100,v36,v100
v_mul_f32 v101,v37,v101
v_exp_f32 v98,v98
v_exp_f32 v99,v99
v_exp_f32 v100,v100
v_exp_f32 v101,v101
v_fma_f32 v98,v98,s89,s89
v_fma_f32 v99,v99,s89,s89
v_fma_f32 v100,v100,s89,s89
v_fma_f32 v101,v101,s89,s89
v_rcp_f32 v98,v98
v_rcp_f32 v99,v99
v_rcp_f32 v100,v100
v_rcp_f32 v101,v101
v_mul_f32 v34,v34,v98
v_mul_f32 v35,v35,v99
v_mul_f32 v36,v36,v100
v_mul_f32 v37,v37,v101
v_cvt_pk_bf16_f32 v108,v34,v35
v_cvt_pk_bf16_f32 v109,v36,v37
buffer_store_dwordx4 v[106:109],v0,s[36:39],s67 offen offset:0 sc0 sc1
v_fma_f32 v38,-v78,v90,v38
v_fma_f32 v39,-v78,v91,v39
v_fma_f32 v40,-v78,v92,v40
v_fma_f32 v41,-v78,v93,v41
v_mul_f32 v38,v79,v38
v_mul_f32 v39,v79,v39
v_mul_f32 v40,v79,v40
v_mul_f32 v41,v79,v41
v_fma_f32 v102,v38,v38,s88
v_fma_f32 v103,v39,v39,s88
v_fma_f32 v104,v40,v40,s88
v_fma_f32 v105,v41,v41,s88
v_mul_f32 v102,v38,v102
v_mul_f32 v103,v39,v103
v_mul_f32 v104,v40,v104
v_mul_f32 v105,v41,v105
v_exp_f32 v102,v102
v_exp_f32 v103,v103
v_exp_f32 v104,v104
v_exp_f32 v105,v105
v_fma_f32 v102,v102,s89,s89
v_fma_f32 v103,v103,s89,s89
v_fma_f32 v104,v104,s89,s89
v_fma_f32 v105,v105,s89,s89
v_rcp_f32 v102,v102
v_rcp_f32 v103,v103
v_rcp_f32 v104,v104
v_rcp_f32 v105,v105
v_mul_f32 v38,v38,v102
v_mul_f32 v39,v39,v103
v_mul_f32 v40,v40,v104
v_mul_f32 v41,v41,v105
v_cvt_pk_bf16_f32 v110,v38,v39
v_cvt_pk_bf16_f32 v111,v40,v41
buffer_store_dwordx2 v[110:111],v10,s[36:39],s67 offen offset:0 sc0 sc1
s_mov_b32 s72,s69
s_mov_b32 s69,0x7fff0000
s_mov_b32 s67,0x7fff0000
s_mov_b32 s77,0x7fff0000
s_branch Lg3_done_%=
Lg3_done_%=:
s_waitcnt vmcnt(2)
s_barrier
buffer_store_dword v4,v3,s[36:39],s70 offen offset:0 sc0 sc1
s_waitcnt vmcnt(1)
s_barrier
buffer_store_dword v4,v3,s[36:39],s72 offen offset:0 sc0 sc1
s_waitcnt vmcnt(0) lgkmcnt(0)
s_barrier
s_endpgm
Lcode_g3_e:
.purgem M
.purgem R
.purgem T
.purgem D
.purgem L
)"
  :: "s"(ws), "s"(__builtin_amdgcn_readfirstlane(sWave)), "s"(__builtin_amdgcn_readfirstlane(sG0)), "s"(__builtin_amdgcn_readfirstlane(sStep)), "s"(__builtin_amdgcn_readfirstlane(sOutOff)), "s"(__builtin_amdgcn_readfirstlane(sFoutOff)), "s"(__builtin_amdgcn_readfirstlane(sAuxOff)), "s"(__builtin_amdgcn_readfirstlane(sWw)), "s"(__builtin_amdgcn_readfirstlane(sBuf)), "s"(__builtin_amdgcn_readfirstlane(sExp)), "v"(vL16), "v"(vFlag), "v"(vSnapOff), "v"(vSnapRd), "v"(vWd0), "v"(vWd1), "v"(vWr0), "v"(vWr1), "v"(vWr2), "v"(vOutS), "v"(vAux0), "v"(vAux1), "v"(vAux2), "v"(vAux3), "v"(vAn), "v"(vAe), "s"(w3), "s"(out) : "v255", "v0", "v1", "v2", "v3", "v4", "v5", "v6", "v7", "v8", "v9", "v10", "v11", "v12", "v13", "v14", "v15", "v16", "v17", "v18", "v19", "v20", "v21", "v22", "v23", "v24", "v25", "v26", "v27", "v28", "v29", "v30", "v31", "v32", "v33", "v34", "v35", "v36", "v37", "v38", "v39", "v40", "v41", "v42", "v43", "v44", "v45", "v46", "v47", "v48", "v49", "v50", "v51", "v52", "v53", "v54", "v55", "v56", "v57", "v58", "v59", "v60", "v61", "v62", "v63", "v64", "v65", "v66", "v67", "v68", "v69", "v70", "v71", "v72", "v73", "v74", "v75", "v76", "v77", "v78", "v79", "v80", "v81", "v82", "v83", "v84", "v85", "v86", "v87", "v88", "v89", "v90", "v91", "v92", "v93", "v94", "v95", "v96", "v97", "v98", "v99", "v100", "v101", "v102", "v103", "v104", "v105", "v106", "v107", "v108", "v109", "v110", "v111", "v112", "v113", "v114", "v115", "v116", "v117", "v118", "v119", "v120", "v121", "v122", "v123", "v124", "v125", "v126", "v127", "v128", "v129", "v130", "v131", "v132", "v133", "v134", "v135", "v136", "v137", "v138", "v139", "v140", "v141", "v142", "v143", "v144", "v145", "v146", "v147", "v148", "v149", "v150", "v151", "v152", "v153", "v154", "v155", "v156", "v157", "v158", "v159", "v160", "v161", "v162", "v163", "v164", "v165", "v166", "v167", "v168", "v169", "v170", "v171", "v172", "v173", "v174", "v175", "v176", "v177", "v178", "v179", "v180", "v181", "v182", "v183", "v184", "v185", "v186", "v187", "v188", "v189", "v190", "v191", "v192", "v193", "v194", "v195", "v196", "v197", "v198", "v199", "v200", "v201", "v202", "v203", "v204", "v205", "v206", "v207", "v208", "v209", "v210", "v211", "v212", "v213", "v214", "v215", "v216", "v217", "v218", "v219", "v220", "v221", "v222", "v223", "v224", "v225", "v226", "v227", "v228", "v229", "v230", "v231", "a0", "a1", "a2", "a3", "a4", "a5", "a6", "a7", "a8", "a9", "a10", "a11", "a12", "a13", "a14", "a15", "a16", "a17", "a18", "a19", "a20", "a21", "a22", "a23", "a24", "a25", "a26", "a27", "a28", "a29", "a30", "a31", "a32", "a33", "a34", "a35", "a36", "a37", "a38", "a39", "a40", "a41", "a42", "a43", "a44", "a45", "a46", "a47", "a48", "a49", "a50", "a51", "a52", "a53", "a54", "a55", "a56", "a57", "a58", "a59", "a60", "a61", "a62", "a63", "a64", "a65", "a66", "a67", "a68", "a69", "a70", "a71", "a72", "a73", "a74", "a75", "a76", "a77", "a78", "a79", "a80", "a81", "a82", "a83", "a84", "a85", "a86", "a87", "a88", "a89", "a90", "a91", "a92", "a93", "a94", "a95", "a96", "a97", "a98", "a99", "a100", "a101", "a102", "a103", "a104", "a105", "a106", "a107", "a108", "a109", "a110", "a111", "a112", "a113", "a114", "a115", "a116", "a117", "a118", "a119", "a120", "a121", "a122", "a123", "a124", "a125", "a126", "a127", "a128", "a129", "a130", "a131", "a132", "a133", "a134", "a135", "a136", "a137", "a138", "a139", "a140", "a141", "a142", "a143", "a144", "a145", "a146", "a147", "a148", "a149", "a150", "a151", "a152", "a153", "a154", "a155", "a156", "a157", "a158", "a159", "a160", "a161", "a162", "a163", "a164", "a165", "a166", "a167", "a168", "a169", "a170", "a171", "a172", "a173", "a174", "a175", "a176", "a177", "a178", "a179", "a180", "a181", "a182", "a183", "a184", "a185", "a186", "a187", "a188", "a189", "a190", "a191", "a192", "a193", "a194", "a195", "a196", "a197", "a198", "a199", "a200", "a201", "a202", "a203", "a204", "a205", "a206", "a207", "a208", "a209", "a210", "a211", "a212", "a213", "a214", "a215", "a216", "a217", "a218", "a219", "a220", "a221", "a222", "a223", "a224", "a225", "a226", "a227", "a228", "a229", "a230", "a231", "a232", "a233", "a234", "a235", "a236", "a237", "a238", "a239", "a240", "a241", "a242", "a243", "a244", "a245", "a246", "a247", "a248", "a249", "a250", "a251", "a252", "a253", "a254", "a255", "s36", "s37", "s38", "s39", "s40", "s41", "s42", "s43", "s44", "s45", "s46", "s47", "s48", "s49", "s50", "s51", "s52", "s53", "s54", "s55", "s56", "s57", "s58", "s59", "s60", "s61", "s62", "s63", "s64", "s65", "s66", "s67", "s68", "s69", "s70", "s71", "s72", "s73", "s74", "s75", "s76", "s77", "s78", "s79", "s80", "s81", "s82", "s83", "s84", "s85", "s86", "s87", "s88", "s89", "s90", "s91", "s92", "s93", "s94", "s95", "s96", "s97", "vcc", "scc", "memory"); } else { asm volatile(
R"(.macro M d,w,x
v_mfma_f32_16x16x32_bf16 v[\d:\d+3], \w, v[\x:\x+3], v[\d:\d+3]
.endm
.macro R d,a,o
ds_read_b128 v[\d:\d+3], v\a offset:\o
.endm
.macro T d,a,o
ds_read_b64_tr_b16 \d, v\a offset:\o
.endm
.macro D a,s,o
buffer_load_dwordx4 v\a, s[36:39], s\s offen offset:\o sc1 lds
.endm
.macro L c
s_waitcnt lgkmcnt(\c)
.endm
Lcode_g3o_b:
s_mov_b64 s[36:37],%0
s_mov_b32 s38,111243392
s_mov_b32 s39,0x00020000
s_mov_b32 s44,%1
s_mov_b32 s45,%2
s_mov_b32 s46,%3
s_mov_b32 s47,%4
s_mov_b32 s48,%5
s_mov_b32 s49,%6
s_mov_b32 s50,%7
s_mov_b32 s51,%8
s_mov_b32 s53,%9
v_mov_b32 v0,%10
v_mov_b32 v3,%11
v_mov_b32 v5,%12
v_mov_b32 v6,%13
v_mov_b32 v18,%14
v_mov_b32 v19,%15
v_mov_b32 v20,%16
v_mov_b32 v21,%17
v_mov_b32 v22,%18
v_mov_b32 v10,%19
v_mov_b32 v11,%20
v_mov_b32 v12,%21
v_mov_b32 v13,%22
v_mov_b32 v14,%23
v_mov_b32 v15,%24
v_mov_b32 v16,%25
s_mov_b64 s[40:41],%26
s_mov_b32 s42,7340032
s_mov_b32 s43,0x00020000
s_lshl_b32 s57,s44,11
s_add_u32 s55,s50,0
s_add_u32 m0,s57,0
s_nop 0
buffer_load_dwordx4 v18,s[40:43],s55 offen offset:0  lds
s_add_u32 m0,s57,1024
s_nop 0
buffer_load_dwordx4 v19,s[40:43],s55 offen offset:0  lds
s_add_u32 m0,s57,8064
s_nop 0
buffer_load_dwordx4 v18,s[40:43],s55 offen offset:128  lds
s_add_u32 m0,s57,9088
s_nop 0
buffer_load_dwordx4 v19,s[40:43],s55 offen offset:128  lds
s_add_u32 s55,s50,458752
s_add_u32 m0,s57,16384
s_nop 0
buffer_load_dwordx4 v18,s[40:43],s55 offen offset:0  lds
s_add_u32 m0,s57,17408
s_nop 0
buffer_load_dwordx4 v19,s[40:43],s55 offen offset:0  lds
s_add_u32 m0,s57,24448
s_nop 0
buffer_load_dwordx4 v18,s[40:43],s55 offen offset:128  lds
s_add_u32 m0,s57,25472
s_nop 0
buffer_load_dwordx4 v19,s[40:43],s55 offen offset:128  lds
s_add_u32 s55,s50,917504
s_add_u32 m0,s57,32768
s_nop 0
buffer_load_dwordx4 v18,s[40:43],s55 offen offset:0  lds
s_add_u32 m0,s57,33792
s_nop 0
buffer_load_dwordx4 v19,s[40:43],s55 offen offset:0  lds
s_add_u32 m0,s57,40832
s_nop 0
buffer_load_dwordx4 v18,s[40:43],s55 offen offset:128  lds
s_add_u32 m0,s57,41856
s_nop 0
buffer_load_dwordx4 v19,s[40:43],s55 offen offset:128  lds
s_add_u32 s55,s50,1376256
s_add_u32 m0,s57,49152
s_nop 0
buffer_load_dwordx4 v18,s[40:43],s55 offen offset:0  lds
s_add_u32 m0,s57,50176
s_nop 0
buffer_load_dwordx4 v19,s[40:43],s55 offen offset:0  lds
s_add_u32 m0,s57,57216
s_nop 0
buffer_load_dwordx4 v18,s[40:43],s55 offen offset:128  lds
s_add_u32 m0,s57,58240
s_nop 0
buffer_load_dwordx4 v19,s[40:43],s55 offen offset:128  lds
s_add_u32 s55,s50,1835008
s_add_u32 m0,s57,65536
s_nop 0
buffer_load_dwordx4 v18,s[40:43],s55 offen offset:0  lds
s_add_u32 m0,s57,66560
s_nop 0
buffer_load_dwordx4 v19,s[40:43],s55 offen offset:0  lds
s_add_u32 m0,s57,73600
s_nop 0
buffer_load_dwordx4 v18,s[40:43],s55 offen offset:128  lds
s_add_u32 m0,s57,74624
s_nop 0
buffer_load_dwordx4 v19,s[40:43],s55 offen offset:128  lds
s_add_u32 s55,s50,2293760
s_add_u32 m0,s57,81920
s_nop 0
buffer_load_dwordx4 v18,s[40:43],s55 offen offset:0  lds
s_add_u32 m0,s57,82944
s_nop 0
buffer_load_dwordx4 v19,s[40:43],s55 offen offset:0  lds
s_add_u32 m0,s57,89984
s_nop 0
buffer_load_dwordx4 v18,s[40:43],s55 offen offset:128  lds
s_add_u32 m0,s57,91008
s_nop 0
buffer_load_dwordx4 v19,s[40:43],s55 offen offset:128  lds
s_waitcnt vmcnt(20) lgkmcnt(0)
s_barrier
s_add_u32 s55,s50,2752512
s_add_u32 m0,s57,98304
s_nop 0
buffer_load_dwordx4 v18,s[40:43],s55 offen offset:0  lds
s_add_u32 m0,s57,99328
s_nop 0
buffer_load_dwordx4 v19,s[40:43],s55 offen offset:0  lds
s_add_u32 m0,s57,106368
s_nop 0
buffer_load_dwordx4 v18,s[40:43],s55 offen offset:128  lds
s_add_u32 m0,s57,107392
s_nop 0
buffer_load_dwordx4 v19,s[40:43],s55 offen offset:128  lds
v_add_u32 v26,0,v20
v_add_u32 v27,0,v21
T a[0:1],26,0
T a[2:3],26,512
T a[4:5],27,0
T a[6:7],27,512
T a[8:9],26,4096
T a[10:11],26,4608
T a[12:13],27,4096
T a[14:15],27,4608
s_waitcnt vmcnt(20) lgkmcnt(0)
s_barrier
s_add_u32 s55,s50,3211264
s_add_u32 m0,s57,0
s_nop 0
buffer_load_dwordx4 v18,s[40:43],s55 offen offset:0  lds
s_add_u32 m0,s57,1024
s_nop 0
buffer_load_dwordx4 v19,s[40:43],s55 offen offset:0  lds
s_add_u32 m0,s57,8064
s_nop 0
buffer_load_dwordx4 v18,s[40:43],s55 offen offset:128  lds
s_add_u32 m0,s57,9088
s_nop 0
buffer_load_dwordx4 v19,s[40:43],s55 offen offset:128  lds
v_add_u32 v26,16384,v20
v_add_u32 v27,16384,v21
T a[16:17],26,0
T a[18:19],26,512
T a[20:21],27,0
T a[22:23],27,512
T a[24:25],26,4096
T a[26:27],26,4608
T a[28:29],27,4096
T a[30:31],27,4608
s_waitcnt vmcnt(20) lgkmcnt(0)
s_barrier
s_add_u32 s55,s50,3670016
s_add_u32 m0,s57,16384
s_nop 0
buffer_load_dwordx4 v18,s[40:43],s55 offen offset:0  lds
s_add_u32 m0,s57,17408
s_nop 0
buffer_load_dwordx4 v19,s[40:43],s55 offen offset:0  lds
s_add_u32 m0,s57,24448
s_nop 0
buffer_load_dwordx4 v18,s[40:43],s55 offen offset:128  lds
s_add_u32 m0,s57,25472
s_nop 0
buffer_load_dwordx4 v19,s[40:43],s55 offen offset:128  lds
v_add_u32 v26,32768,v20
v_add_u32 v27,32768,v21
T a[32:33],26,0
T a[34:35],26,512
T a[36:37],27,0
T a[38:39],27,512
T a[40:41],26,4096
T a[42:43],26,4608
T a[44:45],27,4096
T a[46:47],27,4608
s_waitcnt vmcnt(20) lgkmcnt(0)
s_barrier
s_add_u32 s55,s50,4128768
s_add_u32 m0,s57,32768
s_nop 0
buffer_load_dwordx4 v18,s[40:43],s55 offen offset:0  lds
s_add_u32 m0,s57,33792
s_nop 0
buffer_load_dwordx4 v19,s[40:43],s55 offen offset:0  lds
s_add_u32 m0,s57,40832
s_nop 0
buffer_load_dwordx4 v18,s[40:43],s55 offen offset:128  lds
s_add_u32 m0,s57,41856
s_nop 0
buffer_load_dwordx4 v19,s[40:43],s55 offen offset:128  lds
v_add_u32 v26,49152,v20
v_add_u32 v27,49152,v21
T a[48:49],26,0
T a[50:51],26,512
T a[52:53],27,0
T a[54:55],27,512
T a[56:57],26,4096
T a[58:59],26,4608
T a[60:61],27,4096
T a[62:63],27,4608
s_waitcnt vmcnt(20) lgkmcnt(0)
s_barrier
s_add_u32 s55,s50,4587520
s_add_u32 m0,s57,49152
s_nop 0
buffer_load_dwordx4 v18,s[40:43],s55 offen offset:0  lds
s_add_u32 m0,s57,50176
s_nop 0
buffer_load_dwordx4 v19,s[40:43],s55 offen offset:0  lds
s_add_u32 m0,s57,57216
s_nop 0
buffer_load_dwordx4 v18,s[40:43],s55 offen offset:128  lds
s_add_u32 m0,s57,58240
s_nop 0
buffer_load_dwordx4 v19,s[40:43],s55 offen offset:128  lds
v_add_u32 v26,65536,v20
v_add_u32 v27,65536,v21
T a[64:65],26,0
T a[66:67],26,512
T a[68:69],27,0
T a[70:71],27,512
T a[72:73],26,4096
T a[74:75],26,4608
T a[76:77],27,4096
T a[78:79],27,4608
s_waitcnt vmcnt(20) lgkmcnt(0)
s_barrier
s_add_u32 s55,s50,5046272
s_add_u32 m0,s57,65536
s_nop 0
buffer_load_dwordx4 v18,s[40:43],s55 offen offset:0  lds
s_add_u32 m0,s57,66560
s_nop 0
buffer_load_dwordx4 v19,s[40:43],s55 offen offset:0  lds
s_add_u32 m0,s57,73600
s_nop 0
buffer_load_dwordx4 v18,s[40:43],s55 offen offset:128  lds
s_add_u32 m0,s57,74624
s_nop 0
buffer_load_dwordx4 v19,s[40:43],s55 offen offset:128  lds
v_add_u32 v26,81920,v20
v_add_u32 v27,81920,v21
T a[80:81],26,0
T a[82:83],26,512
T a[84:85],27,0
T a[86:87],27,512
T a[88:89],26,4096
T a[90:91],26,4608
T a[92:93],27,4096
T a[94:95],27,4608
s_waitcnt vmcnt(20) lgkmcnt(0)
s_barrier
s_add_u32 s55,s50,5505024
s_add_u32 m0,s57,81920
s_nop 0
buffer_load_dwordx4 v18,s[40:43],s55 offen offset:0  lds
s_add_u32 m0,s57,82944
s_nop 0
buffer_load_dwordx4 v19,s[40:43],s55 offen offset:0  lds
s_add_u32 m0,s57,89984
s_nop 0
buffer_load_dwordx4 v18,s[40:43],s55 offen offset:128  lds
s_add_u32 m0,s57,91008
s_nop 0
buffer_load_dwordx4 v19,s[40:43],s55 offen offset:128  lds
v_add_u32 v26,98304,v20
v_add_u32 v27,98304,v21
T a[96:97],26,0
T a[98:99],26,512
T a[100:101],27,0
T a[102:103],27,512
T a[104:105],26,4096
T a[106:107],26,4608
T a[108:109],27,4096
T a[110:111],27,4608
s_waitcnt vmcnt(20) lgkmcnt(0)
s_barrier
s_add_u32 s55,s50,5963776
s_add_u32 m0,s57,98304
s_nop 0
buffer_load_dwordx4 v18,s[40:43],s55 offen offset:0  lds
s_add_u32 m0,s57,99328
s_nop 0
buffer_load_dwordx4 v19,s[40:43],s55 offen offset:0  lds
s_add_u32 m0,s57,106368
s_nop 0
buffer_load_dwordx4 v18,s[40:43],s55 offen offset:128  lds
s_add_u32 m0,s57,107392
s_nop 0
buffer_load_dwordx4 v19,s[40:43],s55 offen offset:128  lds
v_add_u32 v26,0,v20
v_add_u32 v27,0,v21
T a[112:113],26,0
T a[114:115],26,512
T a[116:117],27,0
T a[118:119],27,512
T a[120:121],26,4096
T a[122:123],26,4608
T a[124:125],27,4096
T a[126:127],27,4608
s_waitcnt vmcnt(20) lgkmcnt(0)
s_barrier
s_add_u32 s55,s50,6422528
s_add_u32 m0,s57,0
s_nop 0
buffer_load_dwordx4 v18,s[40:43],s55 offen offset:0  lds
s_add_u32 m0,s57,1024
s_nop 0
buffer_load_dwordx4 v19,s[40:43],s55 offen offset:0  lds
s_add_u32 m0,s57,8064
s_nop 0
buffer_load_dwordx4 v18,s[40:43],s55 offen offset:128  lds
s_add_u32 m0,s57,9088
s_nop 0
buffer_load_dwordx4 v19,s[40:43],s55 offen offset:128  lds
v_add_u32 v26,16384,v20
v_add_u32 v27,16384,v21
T a[128:129],26,0
T a[130:131],26,512
T a[132:133],27,0
T a[134:135],27,512
T a[136:137],26,4096
T a[138:139],26,4608
T a[140:141],27,4096
T a[142:143],27,4608
s_waitcnt vmcnt(20) lgkmcnt(0)
s_barrier
s_add_u32 s55,s50,6881280
s_add_u32 m0,s57,16384
s_nop 0
buffer_load_dwordx4 v18,s[40:43],s55 offen offset:0  lds
s_add_u32 m0,s57,17408
s_nop 0
buffer_load_dwordx4 v19,s[40:43],s55 offen offset:0  lds
s_add_u32 m0,s57,24448
s_nop 0
buffer_load_dwordx4 v18,s[40:43],s55 offen offset:128  lds
s_add_u32 m0,s57,25472
s_nop 0
buffer_load_dwordx4 v19,s[40:43],s55 offen offset:128  lds
v_add_u32 v26,32768,v20
v_add_u32 v27,32768,v21
T a[144:145],26,0
T a[146:147],26,512
T a[148:149],27,0
T a[150:151],27,512
T a[152:153],26,4096
T a[154:155],26,4608
T a[156:157],27,4096
T a[158:159],27,4608
s_waitcnt vmcnt(20) lgkmcnt(0)
s_barrier
v_add_u32 v26,49152,v20
v_add_u32 v27,49152,v21
T a[160:161],26,0
T a[162:163],26,512
T a[164:165],27,0
T a[166:167],27,512
T a[168:169],26,4096
T a[170:171],26,4608
T a[172:173],27,4096
T a[174:175],27,4608
s_waitcnt vmcnt(16) lgkmcnt(0)
s_barrier
v_add_u32 v26,65536,v20
v_add_u32 v27,65536,v21
T a[176:177],26,0
T a[178:179],26,512
T a[180:181],27,0
T a[182:183],27,512
T a[184:185],26,4096
T a[186:187],26,4608
T a[188:189],27,4096
T a[190:191],27,4608
s_waitcnt vmcnt(12) lgkmcnt(0)
s_barrier
v_add_u32 v26,81920,v20
v_add_u32 v27,81920,v21
T a[192:193],26,0
T a[194:195],26,512
T a[196:197],27,0
T a[198:199],27,512
T a[200:201],26,4096
T a[202:203],26,4608
T a[204:205],27,4096
T a[206:207],27,4608
s_waitcnt vmcnt(8) lgkmcnt(0)
s_barrier
v_add_u32 v26,98304,v20
v_add_u32 v27,98304,v21
T a[208:209],26,0
T a[210:211],26,512
T a[212:213],27,0
T a[214:215],27,512
T a[216:217],26,4096
T a[218:219],26,4608
T a[220:221],27,4096
T a[222:223],27,4608
s_waitcnt vmcnt(4) lgkmcnt(0)
s_barrier
v_add_u32 v26,0,v20
v_add_u32 v27,0,v21
T a[224:225],26,0
T a[226:227],26,512
T a[228:229],27,0
T a[230:231],27,512
T a[232:233],26,4096
T a[234:235],26,4608
T a[236:237],27,4096
T a[238:239],27,4608
s_waitcnt vmcnt(0) lgkmcnt(0)
s_barrier
v_add_u32 v26,16384,v20
v_add_u32 v27,16384,v21
T a[240:241],26,0
T a[242:243],26,512
T a[244:245],27,0
T a[246:247],27,512
T a[248:249],26,4096
T a[250:251],26,4608
T a[252:253],27,4096
T a[254:255],27,4608
L 0
s_barrier
v_mov_b32 v34,0x3f803f80
v_mov_b32 v35,0x3f803f80
v_mov_b32 v36,0x3f803f80
v_mov_b32 v37,0x3f803f80
s_nop 1
v_mfma_f32_16x16x32_bf16 v[106:109],a[0:3],v[34:37],0
v_mfma_f32_16x16x32_bf16 v[110:113],a[4:7],v[34:37],0
M 106,a[8:11],34
M 110,a[12:15],34
M 106,a[16:19],34
M 110,a[20:23],34
M 106,a[24:27],34
M 110,a[28:31],34
M 106,a[32:35],34
M 110,a[36:39],34
M 106,a[40:43],34
M 110,a[44:47],34
M 106,a[48:51],34
M 110,a[52:55],34
M 106,a[56:59],34
M 110,a[60:63],34
M 106,a[64:67],34
M 110,a[68:71],34
M 106,a[72:75],34
M 110,a[76:79],34
M 106,a[80:83],34
M 110,a[84:87],34
M 106,a[88:91],34
M 110,a[92:95],34
M 106,a[96:99],34
M 110,a[100:103],34
M 106,a[104:107],34
M 110,a[108:111],34
M 106,a[112:115],34
M 110,a[116:119],34
M 106,a[120:123],34
M 110,a[124:127],34
M 106,a[128:131],34
M 110,a[132:135],34
M 106,a[136:139],34
M 110,a[140:143],34
M 106,a[144:147],34
M 110,a[148:151],34
M 106,a[152:155],34
M 110,a[156:159],34
M 106,a[160:163],34
M 110,a[164:167],34
M 106,a[168:171],34
M 110,a[172:175],34
M 106,a[176:179],34
)"
R"(M 110,a[180:183],34
M 106,a[184:187],34
M 110,a[188:191],34
M 106,a[192:195],34
M 110,a[196:199],34
M 106,a[200:203],34
M 110,a[204:207],34
M 106,a[208:211],34
M 110,a[212:215],34
M 106,a[216:219],34
M 110,a[220:223],34
M 106,a[224:227],34
M 110,a[228:231],34
M 106,a[232:235],34
M 110,a[236:239],34
M 106,a[240:243],34
M 110,a[244:247],34
M 106,a[248:251],34
M 110,a[252:255],34
v_mov_b32 v4,s53
s_mov_b32 s82,0
s_mov_b32 s80,0
s_mov_b32 s54,s45
s_mul_i32 s57,s44,4096
s_mul_i32 s55,s45,32768
s_add_u32 s55,s55,s57
s_add_u32 s55,s55,31457280
s_mul_i32 s57,s44,4096
s_mul_i32 s60,s46,32768
s_mul_i32 s61,s46,1024
s_mul_i32 s62,s46,114688
s_mul_i32 s63,s46,2560
s_mul_i32 s64,s46,1024
s_lshl_b32 s59,s44,8
s_add_u32 s59,s59,131072
s_mul_i32 s58,s45,1024
s_add_u32 s58,s58,109613056
s_mul_i32 s65,s45,114688
s_add_u32 s65,s65,s47
s_mul_i32 s68,s45,2560
s_add_u32 s68,s68,s48
s_mul_i32 s73,s45,1024
s_add_u32 s73,s73,107479040
s_mul_i32 s74,s45,32768
s_add_u32 s74,s74,s49
s_mul_i32 s75,s45,1024
s_add_u32 s75,s75,107479040
s_mov_b32 s66,0x7fff0000
s_mov_b32 s67,0x7fff0000
s_mov_b32 s69,0x7fff0000
s_mov_b32 s76,0x7fff0000
s_mov_b32 s77,0x7fff0000
s_mov_b32 s70,0x7fff0000
s_mov_b32 s71,0x7fff0000
s_mov_b32 s72,0x7fff0000
v_mov_b32 v1,v0
v_add_u32 v2,16384,v0
s_mov_b32 s84,16384
s_mov_b32 s86,0x3a800000
s_mov_b32 s87,0x3727c5ac
s_mov_b32 s88,0x409d30fc
s_mov_b32 s89,0xbeeff558
s_lshr_b32 s85,s44,1
s_mul_i32 s85,s85,127
v_lshlrev_b32 v8,22,v0
v_and_b32 v8,0x80000000,v8
v_or_b32 v8,v8,v5
v_add_u32 v2,32768,v0
s_lshl_b32 s84,s57,1
s_add_u32 s84,s84,32768
s_getpc_b64 s[40:41]
Lg3o_pc_%=:
s_lshl_b32 s78,s44,13
v_lshlrev_b32 v114,3,v0
v_add_u32 v114,s78,v114
v_min_u32 v114,Lcode_g3o_e-Lg3o_pc_%=-4,v114
s_mov_b32 m0,163584
s_nop 0
global_load_lds_dword v114,s[40:41]
s_add_u32 m0,s59,4096
s_nop 0
buffer_load_dword v8,s[36:39],s58 offen offset:0 sc1 lds
buffer_store_dword v4,v3,s[36:39],s71 offen offset:0 sc0 sc1
s_add_u32 m0,s59,5120
s_nop 0
buffer_load_dword v8,s[36:39],s58 offen offset:0 sc1 lds
buffer_store_dword v4,v3,s[36:39],s71 offen offset:0 sc0 sc1
s_mov_b32 s83,0
Lg3o_poll_%=:
s_add_u32 s78,s83,2
s_and_b32 s78,s78,3
s_lshl_b32 s78,s78,10
s_add_u32 s78,s78,4096
s_add_u32 m0,s59,s78
s_nop 0
buffer_load_dword v8,s[36:39],s58 offen offset:0 sc1 lds
s_and_b32 s78,s83,3
s_lshl_b32 s78,s78,10
v_add_u32 v114,s78,v6
s_waitcnt vmcnt(4)
s_barrier
ds_read_b32 v7,v114 offset:4096
buffer_store_dword v4,v3,s[36:39],s70 offen offset:0 sc0 sc1
s_mov_b32 s70,s71
s_mov_b32 s71,s72
s_mov_b32 s72,0x7fff0000
s_add_u32 s83,s83,1
s_add_u32 s82,s82,1
L 0
v_cmp_eq_u32 vcc,s53,v7
s_nop 0
s_cmp_eq_u32 vcc_lo,-1
s_cbranch_scc1 Lg3o_go_%=
s_cmp_lt_u32 s82,20000
s_cbranch_scc1 Lg3o_poll_%=
Lg3o_go_%=:
buffer_load_dword v130,v11,s[36:39],s73 offen offset:0 sc1
s_add_u32 s73,s73,s64
s_add_u32 m0,s57,0
s_add_u32 s56,s55,0
D 0,56,0
D 0,56,1024
D 0,56,2048
D 0,56,3072
s_add_u32 s58,s58,s61
s_add_u32 m0,s59,2048
s_nop 0
buffer_load_dword v8,s[36:39],s58 offen offset:0 sc1 lds
s_add_u32 m0,s57,16384
s_add_u32 s56,s55,16384
D 0,56,0
D 0,56,1024
D 0,56,2048
D 0,56,3072
s_add_u32 s55,s55,s60
s_sub_u32 s57,s84,s57
s_add_u32 s54,s54,s46
s_waitcnt vmcnt(4)
s_barrier
buffer_store_dword v4,v3,s[36:39],s70 offen offset:0 sc0 sc1
s_min_u32 s71,s71,s72
buffer_store_dword v4,v3,s[36:39],s71 offen offset:0 sc0 sc1
s_mov_b32 s70,0x7fff0000
s_mov_b32 s71,0x7fff0000
s_mov_b32 s72,0x7fff0000
s_cmp_eq_u32 s80,1
s_cbranch_scc1 Lg3o_st1_%=
s_branch Lg3o_st0_%=
Lg3o_gox_%=:
s_add_u32 m0,s59,2048
s_nop 0
buffer_load_dword v8,s[36:39],s58 offen offset:0 sc1 lds
s_waitcnt vmcnt(0)
s_barrier
buffer_store_dword v4,v3,s[36:39],s70 offen offset:0 sc0 sc1
s_min_u32 s71,s71,s72
buffer_store_dword v4,v3,s[36:39],s71 offen offset:0 sc0 sc1
s_mov_b32 s70,0x7fff0000
s_mov_b32 s71,0x7fff0000
s_mov_b32 s72,0x7fff0000
s_cmp_eq_u32 s80,1
s_cbranch_scc1 Lg3o_st1_%=
Lg3o_st0_%=:
ds_read_b32 v7,v6 offset:2048
R 34,1,0
R 38,1,1024
R 42,1,2048
R 46,1,3072
R 50,1,4096
L 5
v_cmp_eq_u32 vcc,s53,v7
L 3
s_cmp_eq_u32 vcc_lo,-1
s_cbranch_scc1 Lg3o_F0_%=
s_branch Lg3o_L0_%=
Lg3o_st1_%=:
ds_read_b32 v7,v6 offset:2048
R 34,2,0
R 38,2,1024
R 42,2,2048
R 46,2,3072
R 50,2,4096
L 5
v_cmp_eq_u32 vcc,s53,v7
L 3
s_cmp_eq_u32 vcc_lo,-1
s_cbranch_scc1 Lg3o_F1_%=
s_branch Lg3o_L1_%=
Lg3o_F0_%=:
v_mfma_f32_16x16x32_bf16 v[22:25],a[4:7],v[34:37],0
s_add_u32 s58,s58,s61
s_add_u32 m0,s59,0
R 54,1,5120
M 22,a[12:15],38
buffer_load_dword v8,s[36:39],s58 offen offset:0 sc1 lds
v_mfma_f32_16x16x32_bf16 v[18:21],a[0:3],v[34:37],0
s_add_u32 m0,s57,0
R 58,1,6144
v_add_f32_dpp v131,v130,v130 quad_perm:[1,0,3,2] row_mask:0xf bank_mask:0xf
M 18,a[8:11],38
L 3
s_add_u32 s56,s55,0
buffer_load_dword v130,v11,s[36:39],s73 offen offset:0 sc1
M 18,a[16:19],42
s_mov_b32 s66,s65
R 62,1,7168
v_add_f32_dpp v131,v131,v131 quad_perm:[2,3,0,1] row_mask:0xf bank_mask:0xf
M 18,a[24:27],46
D 0,56,0
M 22,a[20:23],42
s_add_u32 s65,s65,s62
R 66,1,8192
v_add_f32_dpp v134,v131,v131 row_half_mirror row_mask:0xf bank_mask:0xf
M 22,a[28:31],46
L 3
D 0,56,1024
M 22,a[36:39],50
s_add_u32 s54,s54,s46
R 70,1,9216
v_mov_b32_dpp v131,v134 row_ror:8 row_mask:0xf bank_mask:0xf
M 22,a[44:47],54
s_add_u32 s73,s73,s64
v_fma_f32 v131,-v134,v134,v131
v_max_f32 v131,s87,v131
M 18,a[32:35],50
R 74,1,10240
v_mul_f32 v134,v15,v134
M 18,a[40:43],54
L 3
D 0,56,2048
M 18,a[48:51],58
R 78,1,11264
v_fma_f32 v26,-v102,v106,v26
M 18,a[56:59],62
D 0,56,3072
M 22,a[52:55],58
s_add_u32 m0,s57,16384
R 82,1,12288
v_fma_f32 v27,-v102,v107,v27
M 22,a[60:63],62
L 3
v_rsq_f32 v132,v131
M 22,a[68:71],66
s_add_u32 s56,s55,16384
R 86,1,13312
v_fma_f32 v134,v132,v16,v134
M 22,a[76:79],70
s_add_u32 s55,s55,s60
ds_write_b32 v12,v134 offset:0
M 18,a[64:67],66
s_sub_u32 s57,s84,s57
R 90,1,14336
v_fma_f32 v28,-v102,v108,v28
M 18,a[72:75],70
L 4
v_fma_f32 v29,-v102,v109,v29
v_mul_f32 v26,v103,v26
M 18,a[80:83],74
s_waitcnt vmcnt(8)
R 94,1,15360
v_mul_f32 v27,v103,v27
M 18,a[88:91],78
s_barrier
v_mul_f32 v28,v103,v28
v_mul_f32 v29,v103,v29
M 22,a[84:87],74
R 34,1,16384
v_fma_f32 v114,v26,v26,s88
M 22,a[92:95],78
L 4
v_fma_f32 v115,v27,v27,s88
v_fma_f32 v116,v28,v28,s88
M 22,a[100:103],82
R 38,1,17408
v_fma_f32 v117,v29,v29,s88
M 22,a[108:111],86
D 0,56,0
M 18,a[96:99],82
R 42,1,18432
v_mul_f32 v114,v26,v114
M 18,a[104:107],86
L 3
v_exp_f32 v114,v114
M 18,a[112:115],90
R 46,1,19456
v_mul_f32 v115,v27,v115
M 18,a[120:123],94
v_exp_f32 v115,v115
M 22,a[116:119],90
R 50,1,20480
v_mul_f32 v116,v28,v116
M 22,a[124:127],94
L 3
v_exp_f32 v116,v116
M 22,a[132:135],34
R 54,1,21504
v_mul_f32 v117,v29,v117
M 22,a[140:143],38
D 0,56,1024
M 18,a[128:131],34
R 58,1,22528
v_fma_f32 v114,v114,s89,s89
M 18,a[136:139],38
L 3
v_exp_f32 v117,v117
M 18,a[144:147],42
R 62,1,23552
v_fma_f32 v115,v115,s89,s89
M 18,a[152:155],46
v_rcp_f32 v114,v114
M 22,a[148:151],42
R 66,1,24576
v_fma_f32 v116,v116,s89,s89
M 22,a[156:159],46
L 3
D 0,56,2048
M 22,a[164:167],50
R 70,1,25600
v_fma_f32 v117,v117,s89,s89
M 22,a[172:175],54
v_rcp_f32 v115,v115
M 18,a[160:163],50
R 74,1,26624
v_mul_f32 v26,v26,v114
M 18,a[168:171],54
L 3
v_rcp_f32 v116,v116
M 18,a[176:179],58
R 78,1,27648
v_mul_f32 v27,v27,v115
M 18,a[184:187],62
v_rcp_f32 v117,v117
M 22,a[180:183],58
R 82,1,28672
v_mul_f32 v28,v28,v116
M 22,a[188:191],62
L 3
D 0,56,3072
M 22,a[196:199],66
R 86,1,29696
v_mul_f32 v29,v29,v117
M 22,a[204:207],70
v_cvt_pk_bf16_f32 v122,v26,v27
v_cvt_pk_bf16_f32 v123,v28,v29
M 18,a[192:195],66
R 90,1,30720
v_fma_f32 v30,-v102,v110,v30
M 18,a[200:203],70
L 3
s_nop 0
v_fma_f32 v31,-v102,v111,v31
M 18,a[208:211],74
s_waitcnt vmcnt(4)
R 94,1,31744
v_fma_f32 v32,-v102,v112,v32
M 18,a[216:219],78
s_barrier
ds_read_b32 v7,v6 offset:0
ds_read_b64 v[98:99],v13 offset:0
M 22,a[212:215],74
R 34,2,0
v_fma_f32 v33,-v102,v113,v33
M 22,a[220:223],78
L 5
buffer_store_dword v4,v3,s[36:39],s70 offen offset:0 sc0 sc1
M 22,a[228:231],82
s_mov_b32 s70,s69
R 38,2,1024
v_mul_f32 v30,v103,v30
M 22,a[236:239],86
s_mov_b32 s69,s68
v_mul_f32 v31,v103,v31
v_mul_f32 v32,v103,v32
M 18,a[224:227],82
s_add_u32 s68,s68,s63
R 42,2,2048
v_mul_f32 v33,v103,v33
M 18,a[232:235],86
L 5
v_fma_f32 v118,v30,v30,s88
v_fma_f32 v119,v31,v31,s88
v_fma_f32 v120,v32,v32,s88
v_fma_f32 v121,v33,v33,s88
v_mul_f32 v118,v30,v118
v_mul_f32 v119,v31,v119
v_mul_f32 v120,v32,v120
v_mul_f32 v121,v33,v121
v_exp_f32 v118,v118
v_exp_f32 v119,v119
v_exp_f32 v120,v120
v_exp_f32 v121,v121
M 18,a[240:243],90
L 4
R 46,2,3072
v_fma_f32 v118,v118,s89,s89
v_fma_f32 v119,v119,s89,s89
v_fma_f32 v120,v120,s89,s89
v_fma_f32 v121,v121,s89,s89
v_rcp_f32 v118,v118
v_rcp_f32 v119,v119
v_rcp_f32 v120,v120
v_rcp_f32 v121,v121
v_cmp_eq_u32 vcc,s53,v7
M 18,a[248:251],94
v_mul_f32 v30,v30,v118
v_mul_f32 v31,v31,v119
v_mul_f32 v32,v32,v120
v_mul_f32 v33,v33,v121
v_cvt_pk_bf16_f32 v124,v30,v31
v_cvt_pk_bf16_f32 v125,v32,v33
M 22,a[244:247],90
L 2
s_cmp_eq_u32 vcc_lo,-1
R 50,2,4096
buffer_store_dwordx4 v[122:125],v0,s[36:39],s67 offen offset:0 sc0 sc1
M 22,a[252:255],94
s_cbranch_scc0 Lg3o_K1_%=
Lg3o_F1_%=:
v_mfma_f32_16x16x32_bf16 v[30:33],a[4:7],v[34:37],0
s_add_u32 s58,s58,s61
s_add_u32 m0,s59,1024
R 54,2,5120
M 30,a[12:15],38
buffer_load_dword v8,s[36:39],s58 offen offset:0 sc1 lds
v_mfma_f32_16x16x32_bf16 v[26:29],a[0:3],v[34:37],0
s_add_u32 m0,s57,0
R 58,2,6144
v_add_f32_dpp v131,v130,v130 quad_perm:[1,0,3,2] row_mask:0xf bank_mask:0xf
M 26,a[8:11],38
L 3
s_add_u32 s56,s55,0
buffer_load_dword v130,v11,s[36:39],s73 offen offset:0 sc1
M 26,a[16:19],42
s_mov_b32 s67,s65
R 62,2,7168
v_add_f32_dpp v131,v131,v131 quad_perm:[2,3,0,1] row_mask:0xf bank_mask:0xf
M 26,a[24:27],46
D 0,56,0
M 30,a[20:23],42
s_add_u32 s65,s65,s62
R 66,2,8192
v_add_f32_dpp v134,v131,v131 row_half_mirror row_mask:0xf bank_mask:0xf
M 30,a[28:31],46
L 3
D 0,56,1024
M 30,a[36:39],50
s_add_u32 s54,s54,s46
R 70,2,9216
v_mov_b32_dpp v131,v134 row_ror:8 row_mask:0xf bank_mask:0xf
M 30,a[44:47],54
s_add_u32 s73,s73,s64
v_fma_f32 v131,-v134,v134,v131
v_max_f32 v131,s87,v131
M 26,a[32:35],50
R 74,2,10240
v_mul_f32 v134,v15,v134
M 26,a[40:43],54
L 3
D 0,56,2048
M 26,a[48:51],58
R 78,2,11264
v_fma_f32 v18,-v98,v106,v18
M 26,a[56:59],62
D 0,56,3072
M 30,a[52:55],58
s_add_u32 m0,s57,16384
R 82,2,12288
v_fma_f32 v19,-v98,v107,v19
M 30,a[60:63],62
L 3
v_rsq_f32 v132,v131
M 30,a[68:71],66
s_add_u32 s56,s55,16384
R 86,2,13312
v_fma_f32 v134,v132,v16,v134
M 30,a[76:79],70
s_add_u32 s55,s55,s60
ds_write_b32 v12,v134 offset:2048
M 26,a[64:67],66
s_sub_u32 s57,s84,s57
R 90,2,14336
v_fma_f32 v20,-v98,v108,v20
M 26,a[72:75],70
L 4
v_fma_f32 v21,-v98,v109,v21
v_mul_f32 v18,v99,v18
M 26,a[80:83],74
s_waitcnt vmcnt(8)
R 94,2,15360
v_mul_f32 v19,v99,v19
M 26,a[88:91],78
s_barrier
v_mul_f32 v20,v99,v20
v_mul_f32 v21,v99,v21
M 30,a[84:87],74
R 34,2,16384
v_fma_f32 v114,v18,v18,s88
M 30,a[92:95],78
L 4
v_fma_f32 v115,v19,v19,s88
)"
R"(v_fma_f32 v116,v20,v20,s88
M 30,a[100:103],82
R 38,2,17408
v_fma_f32 v117,v21,v21,s88
M 30,a[108:111],86
D 0,56,0
M 26,a[96:99],82
R 42,2,18432
v_mul_f32 v114,v18,v114
M 26,a[104:107],86
L 3
v_exp_f32 v114,v114
M 26,a[112:115],90
R 46,2,19456
v_mul_f32 v115,v19,v115
M 26,a[120:123],94
v_exp_f32 v115,v115
M 30,a[116:119],90
R 50,2,20480
v_mul_f32 v116,v20,v116
M 30,a[124:127],94
L 3
v_exp_f32 v116,v116
M 30,a[132:135],34
R 54,2,21504
v_mul_f32 v117,v21,v117
M 30,a[140:143],38
D 0,56,1024
M 26,a[128:131],34
R 58,2,22528
v_fma_f32 v114,v114,s89,s89
M 26,a[136:139],38
L 3
v_exp_f32 v117,v117
M 26,a[144:147],42
R 62,2,23552
v_fma_f32 v115,v115,s89,s89
M 26,a[152:155],46
v_rcp_f32 v114,v114
M 30,a[148:151],42
R 66,2,24576
v_fma_f32 v116,v116,s89,s89
M 30,a[156:159],46
L 3
D 0,56,2048
M 30,a[164:167],50
R 70,2,25600
v_fma_f32 v117,v117,s89,s89
M 30,a[172:175],54
v_rcp_f32 v115,v115
M 26,a[160:163],50
R 74,2,26624
v_mul_f32 v18,v18,v114
M 26,a[168:171],54
L 3
v_rcp_f32 v116,v116
M 26,a[176:179],58
R 78,2,27648
v_mul_f32 v19,v19,v115
M 26,a[184:187],62
v_rcp_f32 v117,v117
M 30,a[180:183],58
R 82,2,28672
v_mul_f32 v20,v20,v116
M 30,a[188:191],62
L 3
D 0,56,3072
M 30,a[196:199],66
R 86,2,29696
v_mul_f32 v21,v21,v117
M 30,a[204:207],70
v_cvt_pk_bf16_f32 v122,v18,v19
v_cvt_pk_bf16_f32 v123,v20,v21
M 26,a[192:195],66
R 90,2,30720
v_fma_f32 v22,-v98,v110,v22
M 26,a[200:203],70
L 3
s_nop 0
v_fma_f32 v23,-v98,v111,v23
M 26,a[208:211],74
s_waitcnt vmcnt(4)
R 94,2,31744
v_fma_f32 v24,-v98,v112,v24
M 26,a[216:219],78
s_barrier
ds_read_b32 v7,v6 offset:1024
ds_read_b64 v[102:103],v13 offset:2048
M 30,a[212:215],74
R 34,1,0
v_fma_f32 v25,-v98,v113,v25
M 30,a[220:223],78
L 5
buffer_store_dword v4,v3,s[36:39],s70 offen offset:0 sc0 sc1
M 30,a[228:231],82
s_mov_b32 s70,s69
R 38,1,1024
v_mul_f32 v22,v99,v22
M 30,a[236:239],86
s_mov_b32 s69,s68
v_mul_f32 v23,v99,v23
v_mul_f32 v24,v99,v24
M 26,a[224:227],82
s_add_u32 s68,s68,s63
R 42,1,2048
v_mul_f32 v25,v99,v25
M 26,a[232:235],86
L 5
v_fma_f32 v118,v22,v22,s88
v_fma_f32 v119,v23,v23,s88
v_fma_f32 v120,v24,v24,s88
v_fma_f32 v121,v25,v25,s88
v_mul_f32 v118,v22,v118
v_mul_f32 v119,v23,v119
v_mul_f32 v120,v24,v120
v_mul_f32 v121,v25,v121
v_exp_f32 v118,v118
v_exp_f32 v119,v119
v_exp_f32 v120,v120
v_exp_f32 v121,v121
M 26,a[240:243],90
L 4
R 46,1,3072
v_fma_f32 v118,v118,s89,s89
v_fma_f32 v119,v119,s89,s89
v_fma_f32 v120,v120,s89,s89
v_fma_f32 v121,v121,s89,s89
v_rcp_f32 v118,v118
v_rcp_f32 v119,v119
v_rcp_f32 v120,v120
v_rcp_f32 v121,v121
v_cmp_eq_u32 vcc,s53,v7
M 26,a[248:251],94
v_mul_f32 v22,v22,v118
v_mul_f32 v23,v23,v119
v_mul_f32 v24,v24,v120
v_mul_f32 v25,v25,v121
v_cvt_pk_bf16_f32 v124,v22,v23
v_cvt_pk_bf16_f32 v125,v24,v25
M 30,a[244:247],90
L 2
s_cmp_eq_u32 vcc_lo,-1
R 50,1,4096
buffer_store_dwordx4 v[122:125],v0,s[36:39],s66 offen offset:0 sc0 sc1
M 30,a[252:255],94
s_cbranch_scc1 Lg3o_F0_%=
Lg3o_K0_%=:
Lg3o_L0_%=:
v_mfma_f32_16x16x32_bf16 v[22:25],a[4:7],v[34:37],0
s_add_u32 m0,s59,3072
R 54,1,5120
v_add_f32_dpp v131,v130,v130 quad_perm:[1,0,3,2] row_mask:0xf bank_mask:0xf
M 22,a[12:15],38
buffer_load_dword v8,s[36:39],s58 offen offset:0 sc1 lds
v_mfma_f32_16x16x32_bf16 v[18:21],a[0:3],v[34:37],0
s_mov_b32 s66,s65
R 58,1,6144
v_add_f32_dpp v131,v131,v131 quad_perm:[2,3,0,1] row_mask:0xf bank_mask:0xf
M 18,a[8:11],38
L 3
v_fma_f32 v26,-v102,v106,v26
v_fma_f32 v27,-v102,v107,v27
M 18,a[16:19],42
s_add_u32 s65,s65,s62
R 62,1,7168
v_add_f32_dpp v134,v131,v131 row_half_mirror row_mask:0xf bank_mask:0xf
M 18,a[24:27],46
v_fma_f32 v28,-v102,v108,v28
v_fma_f32 v29,-v102,v109,v29
M 22,a[20:23],42
R 66,1,8192
v_mov_b32_dpp v131,v134 row_ror:8 row_mask:0xf bank_mask:0xf
M 22,a[28:31],46
L 3
v_fma_f32 v131,-v134,v134,v131
v_max_f32 v131,s87,v131
M 22,a[36:39],50
R 70,1,9216
v_mul_f32 v134,v15,v134
M 22,a[44:47],54
v_rsq_f32 v132,v131
M 18,a[32:35],50
R 74,1,10240
v_fma_f32 v134,v132,v16,v134
M 18,a[40:43],54
L 3
ds_write_b32 v12,v134 offset:0
M 18,a[48:51],58
R 78,1,11264
v_mul_f32 v26,v103,v26
M 18,a[56:59],62
v_mul_f32 v27,v103,v27
v_mul_f32 v28,v103,v28
M 22,a[52:55],58
R 82,1,12288
v_mul_f32 v29,v103,v29
M 22,a[60:63],62
L 4
v_fma_f32 v114,v26,v26,s88
v_fma_f32 v115,v27,v27,s88
M 22,a[68:71],66
R 86,1,13312
v_fma_f32 v116,v28,v28,s88
M 22,a[76:79],70
v_fma_f32 v117,v29,v29,s88
v_mul_f32 v114,v26,v114
M 18,a[64:67],66
R 90,1,14336
v_mul_f32 v115,v27,v115
M 18,a[72:75],70
L 3
v_exp_f32 v114,v114
M 18,a[80:83],74
s_waitcnt vmcnt(0)
R 94,1,15360
v_mul_f32 v116,v28,v116
M 18,a[88:91],78
s_barrier
ds_read_b32 v7,v6 offset:3072
v_mul_f32 v117,v29,v117
M 22,a[84:87],74
R 34,1,16384
v_fma_f32 v114,v114,s89,s89
M 22,a[92:95],78
L 4
buffer_store_dword v4,v3,s[36:39],s70 offen offset:0 sc0 sc1
M 22,a[100:103],82
s_mov_b32 s70,0x7fff0000
R 38,1,17408
v_fma_f32 v30,-v102,v110,v30
M 22,a[108:111],86
v_exp_f32 v115,v115
M 18,a[96:99],82
R 42,1,18432
v_fma_f32 v115,v115,s89,s89
M 18,a[104:107],86
L 4
L 3
v_cmp_eq_u32 vcc,s53,v7
M 18,a[112:115],90
R 46,1,19456
v_fma_f32 v31,-v102,v111,v31
M 18,a[120:123],94
v_exp_f32 v116,v116
M 22,a[116:119],90
s_cmp_eq_u32 vcc_lo,-1
R 50,1,20480
v_fma_f32 v116,v116,s89,s89
s_cbranch_scc0 Lg3o_X0_%=
M 22,a[124:127],94
L 3
s_add_u32 m0,s57,0
s_add_u32 s56,s55,0
buffer_load_dword v130,v11,s[36:39],s73 offen offset:0 sc1
M 22,a[132:135],34
s_add_u32 s58,s58,s61
R 54,1,21504
v_fma_f32 v32,-v102,v112,v32
M 22,a[140:143],38
D 0,56,0
M 18,a[128:131],34
s_add_u32 s54,s54,s46
R 58,1,22528
v_fma_f32 v33,-v102,v113,v33
M 18,a[136:139],38
L 3
v_exp_f32 v117,v117
M 18,a[144:147],42
s_add_u32 s73,s73,s64
R 62,1,23552
v_fma_f32 v117,v117,s89,s89
M 18,a[152:155],46
D 0,56,1024
M 22,a[148:151],42
R 66,1,24576
v_mul_f32 v30,v103,v30
M 22,a[156:159],46
L 3
v_rcp_f32 v114,v114
M 22,a[164:167],50
R 70,1,25600
v_mul_f32 v26,v26,v114
M 22,a[172:175],54
D 0,56,2048
M 18,a[160:163],50
R 74,1,26624
v_mul_f32 v31,v103,v31
M 18,a[168:171],54
L 3
v_rcp_f32 v115,v115
M 18,a[176:179],58
R 78,1,27648
v_mul_f32 v27,v27,v115
M 18,a[184:187],62
D 0,56,3072
M 22,a[180:183],58
s_add_u32 m0,s59,0
R 82,1,28672
v_cvt_pk_bf16_f32 v122,v26,v27
M 22,a[188:191],62
L 3
buffer_load_dword v8,s[36:39],s58 offen offset:0 sc1 lds
M 22,a[196:199],66
s_add_u32 m0,s57,16384
R 86,1,29696
v_mul_f32 v32,v103,v32
M 22,a[204:207],70
s_add_u32 s56,s55,16384
v_rcp_f32 v116,v116
M 18,a[192:195],66
s_add_u32 s55,s55,s60
R 90,1,30720
v_mul_f32 v28,v28,v116
M 18,a[200:203],70
L 3
s_nop 0
D 0,56,0
M 18,a[208:211],74
s_waitcnt vmcnt(1)
R 94,1,31744
v_mul_f32 v33,v103,v33
M 18,a[216:219],78
s_barrier
ds_read_b32 v7,v6 offset:0
ds_read_b64 v[98:99],v13 offset:0
M 22,a[212:215],74
s_sub_u32 s57,s84,s57
R 34,2,0
v_fma_f32 v118,v30,v30,s88
M 22,a[220:223],78
L 5
buffer_store_dword v4,v3,s[36:39],s70 offen offset:0 sc0 sc1
M 22,a[228:231],82
s_mov_b32 s70,s69
R 38,2,1024
v_fma_f32 v119,v31,v31,s88
M 22,a[236:239],86
D 0,56,1024
M 18,a[224:227],82
s_mov_b32 s69,s68
R 42,2,2048
v_fma_f32 v120,v32,v32,s88
M 18,a[232:235],86
L 5
v_fma_f32 v121,v33,v33,s88
v_mul_f32 v118,v30,v118
v_mul_f32 v119,v31,v119
v_mul_f32 v120,v32,v120
v_mul_f32 v121,v33,v121
v_exp_f32 v118,v118
v_exp_f32 v119,v119
v_exp_f32 v120,v120
v_exp_f32 v121,v121
M 18,a[240:243],90
L 4
R 46,2,3072
v_rcp_f32 v117,v117
v_fma_f32 v118,v118,s89,s89
v_fma_f32 v119,v119,s89,s89
v_fma_f32 v120,v120,s89,s89
v_fma_f32 v121,v121,s89,s89
v_rcp_f32 v118,v118
v_rcp_f32 v119,v119
v_rcp_f32 v120,v120
v_rcp_f32 v121,v121
v_cmp_eq_u32 vcc,s53,v7
M 18,a[248:251],94
s_add_u32 s68,s68,s63
v_mul_f32 v29,v29,v117
v_cvt_pk_bf16_f32 v123,v28,v29
v_mul_f32 v30,v30,v118
v_mul_f32 v31,v31,v119
v_mul_f32 v32,v32,v120
v_mul_f32 v33,v33,v121
v_cvt_pk_bf16_f32 v124,v30,v31
v_cvt_pk_bf16_f32 v125,v32,v33
M 22,a[244:247],90
L 2
s_cmp_eq_u32 vcc_lo,-1
R 50,2,4096
buffer_store_dwordx4 v[122:125],v0,s[36:39],s67 offen offset:0 sc0 sc1
M 22,a[252:255],94
D 0,56,2048
D 0,56,3072
buffer_store_dword v4,v3,s[36:39],s71 offen offset:0 sc0 sc1
buffer_store_dword v4,v3,s[36:39],s71 offen offset:0 sc0 sc1
s_cbranch_scc1 Lg3o_F1_%=
s_branch Lg3o_K1_%=
Lg3o_X0_%=:
M 22,a[124:127],94
L 3
s_add_u32 m0,s59,0
v_fma_f32 v32,-v102,v112,v32
M 22,a[132:135],34
R 54,1,21504
v_fma_f32 v33,-v102,v113,v33
M 22,a[140:143],38
buffer_load_dword v8,s[36:39],s58 offen offset:0 sc1 lds
M 18,a[128:131],34
R 58,1,22528
v_mul_f32 v30,v103,v30
M 18,a[136:139],38
L 3
v_exp_f32 v117,v117
M 18,a[144:147],42
R 62,1,23552
v_fma_f32 v117,v117,s89,s89
M 18,a[152:155],46
v_rcp_f32 v114,v114
M 22,a[148:151],42
R 66,1,24576
v_mul_f32 v26,v26,v114
M 22,a[156:159],46
L 3
v_rcp_f32 v115,v115
M 22,a[164:167],50
R 70,1,25600
v_mul_f32 v27,v27,v115
M 22,a[172:175],54
v_rcp_f32 v116,v116
M 18,a[160:163],50
R 74,1,26624
v_mul_f32 v28,v28,v116
M 18,a[168:171],54
L 3
v_rcp_f32 v117,v117
M 18,a[176:179],58
R 78,1,27648
v_mul_f32 v29,v29,v117
M 18,a[184:187],62
v_cvt_pk_bf16_f32 v122,v26,v27
v_cvt_pk_bf16_f32 v123,v28,v29
M 22,a[180:183],58
R 82,1,28672
v_mul_f32 v31,v103,v31
M 22,a[188:191],62
L 3
v_mul_f32 v32,v103,v32
v_mul_f32 v33,v103,v33
M 22,a[196:199],66
R 86,1,29696
v_fma_f32 v118,v30,v30,s88
M 22,a[204:207],70
v_fma_f32 v119,v31,v31,s88
v_fma_f32 v120,v32,v32,s88
M 18,a[192:195],66
R 90,1,30720
v_fma_f32 v121,v33,v33,s88
M 18,a[200:203],70
L 3
s_nop 0
v_mul_f32 v118,v30,v118
M 18,a[208:211],74
s_waitcnt vmcnt(0)
R 94,1,31744
v_mul_f32 v119,v31,v119
M 18,a[216:219],78
s_barrier
buffer_store_dword v4,v3,s[36:39],s70 offen offset:0 sc0 sc1
M 22,a[212:215],74
s_mov_b32 s70,s69
ds_read_b32 v7,v6 offset:0
ds_read_b64 v[98:99],v13 offset:0
M 22,a[220:223],78
L 4
v_mul_f32 v120,v32,v120
v_mul_f32 v121,v33,v121
M 22,a[228:231],82
s_mov_b32 s69,s68
v_exp_f32 v118,v118
M 22,a[236:239],86
s_add_u32 s68,s68,s63
v_exp_f32 v119,v119
M 18,a[224:227],82
v_exp_f32 v120,v120
M 18,a[232:235],86
L 2
v_exp_f32 v121,v121
M 18,a[240:243],90
L 1
v_fma_f32 v118,v118,s89,s89
v_fma_f32 v119,v119,s89,s89
v_fma_f32 v120,v120,s89,s89
v_fma_f32 v121,v121,s89,s89
v_rcp_f32 v118,v118
v_rcp_f32 v119,v119
v_rcp_f32 v120,v120
v_rcp_f32 v121,v121
M 18,a[248:251],94
v_mul_f32 v30,v30,v118
v_mul_f32 v31,v31,v119
v_mul_f32 v32,v32,v120
v_mul_f32 v33,v33,v121
v_cvt_pk_bf16_f32 v124,v30,v31
v_cvt_pk_bf16_f32 v125,v32,v33
M 22,a[244:247],90
buffer_store_dwordx4 v[122:125],v0,s[36:39],s67 offen offset:0 sc0 sc1
v_cmp_eq_u32 vcc,s53,v7
M 22,a[252:255],94
s_mov_b32 s80,1
)"
R"(s_cmp_eq_u32 vcc_lo,-1
s_cbranch_scc0 Lg3o_xn0_%=
buffer_load_dword v130,v11,s[36:39],s73 offen offset:0 sc1
L 0
v_fma_f32 v18,-v98,v106,v18
v_fma_f32 v19,-v98,v107,v19
v_fma_f32 v20,-v98,v108,v20
v_fma_f32 v21,-v98,v109,v21
v_mul_f32 v18,v99,v18
v_mul_f32 v19,v99,v19
s_add_u32 s73,s73,s64
s_add_u32 m0,s57,0
s_add_u32 s56,s55,0
D 0,56,0
v_mul_f32 v20,v99,v20
v_mul_f32 v21,v99,v21
v_fma_f32 v114,v18,v18,s88
v_fma_f32 v115,v19,v19,s88
v_fma_f32 v116,v20,v20,s88
v_fma_f32 v117,v21,v21,s88
D 0,56,1024
v_mul_f32 v114,v18,v114
v_mul_f32 v115,v19,v115
v_mul_f32 v116,v20,v116
v_mul_f32 v117,v21,v117
v_exp_f32 v114,v114
v_exp_f32 v115,v115
D 0,56,2048
v_exp_f32 v116,v116
v_exp_f32 v117,v117
v_fma_f32 v114,v114,s89,s89
v_fma_f32 v115,v115,s89,s89
v_fma_f32 v116,v116,s89,s89
v_fma_f32 v117,v117,s89,s89
D 0,56,3072
v_rcp_f32 v114,v114
v_rcp_f32 v115,v115
v_rcp_f32 v116,v116
v_rcp_f32 v117,v117
v_mul_f32 v18,v18,v114
v_mul_f32 v19,v19,v115
s_add_u32 s58,s58,s61
s_add_u32 m0,s57,16384
s_add_u32 s56,s55,16384
D 0,56,0
v_mul_f32 v20,v20,v116
v_mul_f32 v21,v21,v117
v_cvt_pk_bf16_f32 v122,v18,v19
v_cvt_pk_bf16_f32 v123,v20,v21
v_fma_f32 v22,-v98,v110,v22
v_fma_f32 v23,-v98,v111,v23
D 0,56,1024
v_fma_f32 v24,-v98,v112,v24
v_fma_f32 v25,-v98,v113,v25
v_mul_f32 v22,v99,v22
v_mul_f32 v23,v99,v23
v_mul_f32 v24,v99,v24
v_mul_f32 v25,v99,v25
D 0,56,2048
v_fma_f32 v118,v22,v22,s88
v_fma_f32 v119,v23,v23,s88
v_fma_f32 v120,v24,v24,s88
v_fma_f32 v121,v25,v25,s88
v_mul_f32 v118,v22,v118
v_mul_f32 v119,v23,v119
D 0,56,3072
v_mul_f32 v120,v24,v120
v_mul_f32 v121,v25,v121
v_exp_f32 v118,v118
v_exp_f32 v119,v119
v_exp_f32 v120,v120
v_exp_f32 v121,v121
s_add_u32 s55,s55,s60
s_sub_u32 s57,s84,s57
s_add_u32 s54,s54,s46
v_fma_f32 v118,v118,s89,s89
v_fma_f32 v119,v119,s89,s89
v_fma_f32 v120,v120,s89,s89
v_fma_f32 v121,v121,s89,s89
v_rcp_f32 v118,v118
v_rcp_f32 v119,v119
v_rcp_f32 v120,v120
v_rcp_f32 v121,v121
v_mul_f32 v22,v22,v118
v_mul_f32 v23,v23,v119
v_mul_f32 v24,v24,v120
v_mul_f32 v25,v25,v121
v_cvt_pk_bf16_f32 v124,v22,v23
v_cvt_pk_bf16_f32 v125,v24,v25
buffer_store_dwordx4 v[122:125],v0,s[36:39],s66 offen offset:0 sc0 sc1
s_mov_b32 s72,s69
s_mov_b32 s69,0x7fff0000
s_mov_b32 s66,0x7fff0000
s_mov_b32 s76,0x7fff0000
s_branch Lg3o_gox_%=
Lg3o_xn0_%=:
s_cmp_ge_u32 s54,256
s_cbranch_scc1 Lg3o_fin0_%=
s_add_u32 m0,s59,4096
s_nop 0
buffer_load_dword v8,s[36:39],s58 offen offset:0 sc1 lds
buffer_store_dword v4,v3,s[36:39],s71 offen offset:0 sc0 sc1
s_add_u32 m0,s59,5120
s_nop 0
buffer_load_dword v8,s[36:39],s58 offen offset:0 sc1 lds
buffer_store_dword v4,v3,s[36:39],s71 offen offset:0 sc0 sc1
s_mov_b32 s83,0
L 0
v_fma_f32 v18,-v98,v106,v18
v_fma_f32 v19,-v98,v107,v19
v_fma_f32 v20,-v98,v108,v20
v_fma_f32 v21,-v98,v109,v21
v_mul_f32 v18,v99,v18
v_mul_f32 v19,v99,v19
v_mul_f32 v20,v99,v20
v_mul_f32 v21,v99,v21
v_fma_f32 v114,v18,v18,s88
v_fma_f32 v115,v19,v19,s88
v_fma_f32 v116,v20,v20,s88
v_fma_f32 v117,v21,v21,s88
v_mul_f32 v114,v18,v114
v_mul_f32 v115,v19,v115
v_mul_f32 v116,v20,v116
v_mul_f32 v117,v21,v117
v_exp_f32 v114,v114
v_exp_f32 v115,v115
v_exp_f32 v116,v116
v_exp_f32 v117,v117
v_fma_f32 v114,v114,s89,s89
v_fma_f32 v115,v115,s89,s89
v_fma_f32 v116,v116,s89,s89
v_fma_f32 v117,v117,s89,s89
v_rcp_f32 v114,v114
v_rcp_f32 v115,v115
v_rcp_f32 v116,v116
v_rcp_f32 v117,v117
v_mul_f32 v18,v18,v114
v_mul_f32 v19,v19,v115
v_mul_f32 v20,v20,v116
v_mul_f32 v21,v21,v117
v_cvt_pk_bf16_f32 v122,v18,v19
v_cvt_pk_bf16_f32 v123,v20,v21
v_fma_f32 v22,-v98,v110,v22
v_fma_f32 v23,-v98,v111,v23
v_fma_f32 v24,-v98,v112,v24
v_fma_f32 v25,-v98,v113,v25
v_mul_f32 v22,v99,v22
v_mul_f32 v23,v99,v23
v_mul_f32 v24,v99,v24
v_mul_f32 v25,v99,v25
v_fma_f32 v118,v22,v22,s88
v_fma_f32 v119,v23,v23,s88
v_fma_f32 v120,v24,v24,s88
v_fma_f32 v121,v25,v25,s88
v_mul_f32 v118,v22,v118
v_mul_f32 v119,v23,v119
v_mul_f32 v120,v24,v120
v_mul_f32 v121,v25,v121
v_exp_f32 v118,v118
v_exp_f32 v119,v119
v_exp_f32 v120,v120
v_exp_f32 v121,v121
v_fma_f32 v118,v118,s89,s89
v_fma_f32 v119,v119,s89,s89
v_fma_f32 v120,v120,s89,s89
v_fma_f32 v121,v121,s89,s89
v_rcp_f32 v118,v118
v_rcp_f32 v119,v119
v_rcp_f32 v120,v120
v_rcp_f32 v121,v121
v_mul_f32 v22,v22,v118
v_mul_f32 v23,v23,v119
v_mul_f32 v24,v24,v120
v_mul_f32 v25,v25,v121
v_cvt_pk_bf16_f32 v124,v22,v23
v_cvt_pk_bf16_f32 v125,v24,v25
buffer_store_dwordx4 v[122:125],v0,s[36:39],s66 offen offset:0 sc0 sc1
s_mov_b32 s72,s69
s_mov_b32 s69,0x7fff0000
s_mov_b32 s66,0x7fff0000
s_mov_b32 s76,0x7fff0000
s_branch Lg3o_poll_%=
Lg3o_fin0_%=:
L 0
v_fma_f32 v18,-v98,v106,v18
v_fma_f32 v19,-v98,v107,v19
v_fma_f32 v20,-v98,v108,v20
v_fma_f32 v21,-v98,v109,v21
v_mul_f32 v18,v99,v18
v_mul_f32 v19,v99,v19
v_mul_f32 v20,v99,v20
v_mul_f32 v21,v99,v21
v_fma_f32 v114,v18,v18,s88
v_fma_f32 v115,v19,v19,s88
v_fma_f32 v116,v20,v20,s88
v_fma_f32 v117,v21,v21,s88
v_mul_f32 v114,v18,v114
v_mul_f32 v115,v19,v115
v_mul_f32 v116,v20,v116
v_mul_f32 v117,v21,v117
v_exp_f32 v114,v114
v_exp_f32 v115,v115
v_exp_f32 v116,v116
v_exp_f32 v117,v117
v_fma_f32 v114,v114,s89,s89
v_fma_f32 v115,v115,s89,s89
v_fma_f32 v116,v116,s89,s89
v_fma_f32 v117,v117,s89,s89
v_rcp_f32 v114,v114
v_rcp_f32 v115,v115
v_rcp_f32 v116,v116
v_rcp_f32 v117,v117
v_mul_f32 v18,v18,v114
v_mul_f32 v19,v19,v115
v_mul_f32 v20,v20,v116
v_mul_f32 v21,v21,v117
v_cvt_pk_bf16_f32 v122,v18,v19
v_cvt_pk_bf16_f32 v123,v20,v21
v_fma_f32 v22,-v98,v110,v22
v_fma_f32 v23,-v98,v111,v23
v_fma_f32 v24,-v98,v112,v24
v_fma_f32 v25,-v98,v113,v25
v_mul_f32 v22,v99,v22
v_mul_f32 v23,v99,v23
v_mul_f32 v24,v99,v24
v_mul_f32 v25,v99,v25
v_fma_f32 v118,v22,v22,s88
v_fma_f32 v119,v23,v23,s88
v_fma_f32 v120,v24,v24,s88
v_fma_f32 v121,v25,v25,s88
v_mul_f32 v118,v22,v118
v_mul_f32 v119,v23,v119
v_mul_f32 v120,v24,v120
v_mul_f32 v121,v25,v121
v_exp_f32 v118,v118
v_exp_f32 v119,v119
v_exp_f32 v120,v120
v_exp_f32 v121,v121
v_fma_f32 v118,v118,s89,s89
v_fma_f32 v119,v119,s89,s89
v_fma_f32 v120,v120,s89,s89
v_fma_f32 v121,v121,s89,s89
v_rcp_f32 v118,v118
v_rcp_f32 v119,v119
v_rcp_f32 v120,v120
v_rcp_f32 v121,v121
v_mul_f32 v22,v22,v118
v_mul_f32 v23,v23,v119
v_mul_f32 v24,v24,v120
v_mul_f32 v25,v25,v121
v_cvt_pk_bf16_f32 v124,v22,v23
v_cvt_pk_bf16_f32 v125,v24,v25
buffer_store_dwordx4 v[122:125],v0,s[36:39],s66 offen offset:0 sc0 sc1
s_mov_b32 s72,s69
s_mov_b32 s69,0x7fff0000
s_mov_b32 s66,0x7fff0000
s_mov_b32 s76,0x7fff0000
s_branch Lg3o_done_%=
Lg3o_K1_%=:
Lg3o_L1_%=:
v_mfma_f32_16x16x32_bf16 v[30:33],a[4:7],v[34:37],0
s_add_u32 m0,s59,3072
R 54,2,5120
v_add_f32_dpp v131,v130,v130 quad_perm:[1,0,3,2] row_mask:0xf bank_mask:0xf
M 30,a[12:15],38
buffer_load_dword v8,s[36:39],s58 offen offset:0 sc1 lds
v_mfma_f32_16x16x32_bf16 v[26:29],a[0:3],v[34:37],0
s_mov_b32 s67,s65
R 58,2,6144
v_add_f32_dpp v131,v131,v131 quad_perm:[2,3,0,1] row_mask:0xf bank_mask:0xf
M 26,a[8:11],38
L 3
v_fma_f32 v18,-v98,v106,v18
v_fma_f32 v19,-v98,v107,v19
M 26,a[16:19],42
s_add_u32 s65,s65,s62
R 62,2,7168
v_add_f32_dpp v134,v131,v131 row_half_mirror row_mask:0xf bank_mask:0xf
M 26,a[24:27],46
v_fma_f32 v20,-v98,v108,v20
v_fma_f32 v21,-v98,v109,v21
M 30,a[20:23],42
R 66,2,8192
v_mov_b32_dpp v131,v134 row_ror:8 row_mask:0xf bank_mask:0xf
M 30,a[28:31],46
L 3
v_fma_f32 v131,-v134,v134,v131
v_max_f32 v131,s87,v131
M 30,a[36:39],50
R 70,2,9216
v_mul_f32 v134,v15,v134
M 30,a[44:47],54
v_rsq_f32 v132,v131
M 26,a[32:35],50
R 74,2,10240
v_fma_f32 v134,v132,v16,v134
M 26,a[40:43],54
L 3
ds_write_b32 v12,v134 offset:2048
M 26,a[48:51],58
R 78,2,11264
v_mul_f32 v18,v99,v18
M 26,a[56:59],62
v_mul_f32 v19,v99,v19
v_mul_f32 v20,v99,v20
M 30,a[52:55],58
R 82,2,12288
v_mul_f32 v21,v99,v21
M 30,a[60:63],62
L 4
v_fma_f32 v114,v18,v18,s88
v_fma_f32 v115,v19,v19,s88
M 30,a[68:71],66
R 86,2,13312
v_fma_f32 v116,v20,v20,s88
M 30,a[76:79],70
v_fma_f32 v117,v21,v21,s88
v_mul_f32 v114,v18,v114
M 26,a[64:67],66
R 90,2,14336
v_mul_f32 v115,v19,v115
M 26,a[72:75],70
L 3
v_exp_f32 v114,v114
M 26,a[80:83],74
s_waitcnt vmcnt(0)
R 94,2,15360
v_mul_f32 v116,v20,v116
M 26,a[88:91],78
s_barrier
ds_read_b32 v7,v6 offset:3072
v_mul_f32 v117,v21,v117
M 30,a[84:87],74
R 34,2,16384
v_fma_f32 v114,v114,s89,s89
M 30,a[92:95],78
L 4
buffer_store_dword v4,v3,s[36:39],s70 offen offset:0 sc0 sc1
M 30,a[100:103],82
s_mov_b32 s70,0x7fff0000
R 38,2,17408
v_fma_f32 v22,-v98,v110,v22
M 30,a[108:111],86
v_exp_f32 v115,v115
M 26,a[96:99],82
R 42,2,18432
v_fma_f32 v115,v115,s89,s89
M 26,a[104:107],86
L 4
L 3
v_cmp_eq_u32 vcc,s53,v7
M 26,a[112:115],90
R 46,2,19456
v_fma_f32 v23,-v98,v111,v23
M 26,a[120:123],94
v_exp_f32 v116,v116
M 30,a[116:119],90
s_cmp_eq_u32 vcc_lo,-1
R 50,2,20480
v_fma_f32 v116,v116,s89,s89
s_cbranch_scc0 Lg3o_X1_%=
M 30,a[124:127],94
L 3
s_add_u32 m0,s57,0
s_add_u32 s56,s55,0
buffer_load_dword v130,v11,s[36:39],s73 offen offset:0 sc1
M 30,a[132:135],34
s_add_u32 s58,s58,s61
R 54,2,21504
v_fma_f32 v24,-v98,v112,v24
M 30,a[140:143],38
D 0,56,0
M 26,a[128:131],34
s_add_u32 s54,s54,s46
R 58,2,22528
v_fma_f32 v25,-v98,v113,v25
M 26,a[136:139],38
L 3
v_exp_f32 v117,v117
M 26,a[144:147],42
s_add_u32 s73,s73,s64
R 62,2,23552
v_fma_f32 v117,v117,s89,s89
M 26,a[152:155],46
D 0,56,1024
M 30,a[148:151],42
R 66,2,24576
v_mul_f32 v22,v99,v22
M 30,a[156:159],46
L 3
v_rcp_f32 v114,v114
M 30,a[164:167],50
R 70,2,25600
v_mul_f32 v18,v18,v114
M 30,a[172:175],54
D 0,56,2048
M 26,a[160:163],50
R 74,2,26624
v_mul_f32 v23,v99,v23
M 26,a[168:171],54
L 3
v_rcp_f32 v115,v115
M 26,a[176:179],58
R 78,2,27648
v_mul_f32 v19,v19,v115
M 26,a[184:187],62
D 0,56,3072
M 30,a[180:183],58
s_add_u32 m0,s59,1024
R 82,2,28672
v_cvt_pk_bf16_f32 v122,v18,v19
M 30,a[188:191],62
L 3
buffer_load_dword v8,s[36:39],s58 offen offset:0 sc1 lds
M 30,a[196:199],66
s_add_u32 m0,s57,16384
R 86,2,29696
v_mul_f32 v24,v99,v24
M 30,a[204:207],70
s_add_u32 s56,s55,16384
v_rcp_f32 v116,v116
M 26,a[192:195],66
s_add_u32 s55,s55,s60
R 90,2,30720
v_mul_f32 v20,v20,v116
M 26,a[200:203],70
L 3
s_nop 0
D 0,56,0
M 26,a[208:211],74
s_waitcnt vmcnt(1)
R 94,2,31744
v_mul_f32 v25,v99,v25
M 26,a[216:219],78
s_barrier
ds_read_b32 v7,v6 offset:1024
ds_read_b64 v[102:103],v13 offset:2048
M 30,a[212:215],74
s_sub_u32 s57,s84,s57
R 34,1,0
v_fma_f32 v118,v22,v22,s88
M 30,a[220:223],78
L 5
buffer_store_dword v4,v3,s[36:39],s70 offen offset:0 sc0 sc1
M 30,a[228:231],82
s_mov_b32 s70,s69
R 38,1,1024
v_fma_f32 v119,v23,v23,s88
M 30,a[236:239],86
D 0,56,1024
M 26,a[224:227],82
s_mov_b32 s69,s68
R 42,1,2048
v_fma_f32 v120,v24,v24,s88
M 26,a[232:235],86
L 5
v_fma_f32 v121,v25,v25,s88
v_mul_f32 v118,v22,v118
v_mul_f32 v119,v23,v119
v_mul_f32 v120,v24,v120
v_mul_f32 v121,v25,v121
v_exp_f32 v118,v118
v_exp_f32 v119,v119
v_exp_f32 v120,v120
v_exp_f32 v121,v121
M 26,a[240:243],90
L 4
R 46,1,3072
v_rcp_f32 v117,v117
v_fma_f32 v118,v118,s89,s89
v_fma_f32 v119,v119,s89,s89
v_fma_f32 v120,v120,s89,s89
v_fma_f32 v121,v121,s89,s89
v_rcp_f32 v118,v118
v_rcp_f32 v119,v119
v_rcp_f32 v120,v120
v_rcp_f32 v121,v121
v_cmp_eq_u32 vcc,s53,v7
M 26,a[248:251],94
s_add_u32 s68,s68,s63
v_mul_f32 v21,v21,v117
v_cvt_pk_bf16_f32 v123,v20,v21
v_mul_f32 v22,v22,v118
v_mul_f32 v23,v23,v119
v_mul_f32 v24,v24,v120
v_mul_f32 v25,v25,v121
v_cvt_pk_bf16_f32 v124,v22,v23
v_cvt_pk_bf16_f32 v125,v24,v25
M 30,a[244:247],90
L 2
s_cmp_eq_u32 vcc_lo,-1
R 50,1,4096
buffer_store_dwordx4 v[122:125],v0,s[36:39],s66 offen offset:0 sc0 sc1
M 30,a[252:255],94
D 0,56,2048
D 0,56,3072
buffer_store_dword v4,v3,s[36:39],s71 offen offset:0 sc0 sc1
buffer_store_dword v4,v3,s[36:39],s71 offen offset:0 sc0 sc1
s_cbranch_scc1 Lg3o_F0_%=
s_branch Lg3o_K0_%=
)"
R"(Lg3o_X1_%=:
M 30,a[124:127],94
L 3
s_add_u32 m0,s59,1024
v_fma_f32 v24,-v98,v112,v24
M 30,a[132:135],34
R 54,2,21504
v_fma_f32 v25,-v98,v113,v25
M 30,a[140:143],38
buffer_load_dword v8,s[36:39],s58 offen offset:0 sc1 lds
M 26,a[128:131],34
R 58,2,22528
v_mul_f32 v22,v99,v22
M 26,a[136:139],38
L 3
v_exp_f32 v117,v117
M 26,a[144:147],42
R 62,2,23552
v_fma_f32 v117,v117,s89,s89
M 26,a[152:155],46
v_rcp_f32 v114,v114
M 30,a[148:151],42
R 66,2,24576
v_mul_f32 v18,v18,v114
M 30,a[156:159],46
L 3
v_rcp_f32 v115,v115
M 30,a[164:167],50
R 70,2,25600
v_mul_f32 v19,v19,v115
M 30,a[172:175],54
v_rcp_f32 v116,v116
M 26,a[160:163],50
R 74,2,26624
v_mul_f32 v20,v20,v116
M 26,a[168:171],54
L 3
v_rcp_f32 v117,v117
M 26,a[176:179],58
R 78,2,27648
v_mul_f32 v21,v21,v117
M 26,a[184:187],62
v_cvt_pk_bf16_f32 v122,v18,v19
v_cvt_pk_bf16_f32 v123,v20,v21
M 30,a[180:183],58
R 82,2,28672
v_mul_f32 v23,v99,v23
M 30,a[188:191],62
L 3
v_mul_f32 v24,v99,v24
v_mul_f32 v25,v99,v25
M 30,a[196:199],66
R 86,2,29696
v_fma_f32 v118,v22,v22,s88
M 30,a[204:207],70
v_fma_f32 v119,v23,v23,s88
v_fma_f32 v120,v24,v24,s88
M 26,a[192:195],66
R 90,2,30720
v_fma_f32 v121,v25,v25,s88
M 26,a[200:203],70
L 3
s_nop 0
v_mul_f32 v118,v22,v118
M 26,a[208:211],74
s_waitcnt vmcnt(0)
R 94,2,31744
v_mul_f32 v119,v23,v119
M 26,a[216:219],78
s_barrier
buffer_store_dword v4,v3,s[36:39],s70 offen offset:0 sc0 sc1
M 30,a[212:215],74
s_mov_b32 s70,s69
ds_read_b32 v7,v6 offset:1024
ds_read_b64 v[102:103],v13 offset:2048
M 30,a[220:223],78
L 4
v_mul_f32 v120,v24,v120
v_mul_f32 v121,v25,v121
M 30,a[228:231],82
s_mov_b32 s69,s68
v_exp_f32 v118,v118
M 30,a[236:239],86
s_add_u32 s68,s68,s63
v_exp_f32 v119,v119
M 26,a[224:227],82
v_exp_f32 v120,v120
M 26,a[232:235],86
L 2
v_exp_f32 v121,v121
M 26,a[240:243],90
L 1
v_fma_f32 v118,v118,s89,s89
v_fma_f32 v119,v119,s89,s89
v_fma_f32 v120,v120,s89,s89
v_fma_f32 v121,v121,s89,s89
v_rcp_f32 v118,v118
v_rcp_f32 v119,v119
v_rcp_f32 v120,v120
v_rcp_f32 v121,v121
M 26,a[248:251],94
v_mul_f32 v22,v22,v118
v_mul_f32 v23,v23,v119
v_mul_f32 v24,v24,v120
v_mul_f32 v25,v25,v121
v_cvt_pk_bf16_f32 v124,v22,v23
v_cvt_pk_bf16_f32 v125,v24,v25
M 30,a[244:247],90
buffer_store_dwordx4 v[122:125],v0,s[36:39],s66 offen offset:0 sc0 sc1
v_cmp_eq_u32 vcc,s53,v7
M 30,a[252:255],94
s_mov_b32 s80,0
s_cmp_eq_u32 vcc_lo,-1
s_cbranch_scc0 Lg3o_xn1_%=
buffer_load_dword v130,v11,s[36:39],s73 offen offset:0 sc1
L 0
v_fma_f32 v26,-v102,v106,v26
v_fma_f32 v27,-v102,v107,v27
v_fma_f32 v28,-v102,v108,v28
v_fma_f32 v29,-v102,v109,v29
v_mul_f32 v26,v103,v26
v_mul_f32 v27,v103,v27
s_add_u32 s73,s73,s64
s_add_u32 m0,s57,0
s_add_u32 s56,s55,0
D 0,56,0
v_mul_f32 v28,v103,v28
v_mul_f32 v29,v103,v29
v_fma_f32 v114,v26,v26,s88
v_fma_f32 v115,v27,v27,s88
v_fma_f32 v116,v28,v28,s88
v_fma_f32 v117,v29,v29,s88
D 0,56,1024
v_mul_f32 v114,v26,v114
v_mul_f32 v115,v27,v115
v_mul_f32 v116,v28,v116
v_mul_f32 v117,v29,v117
v_exp_f32 v114,v114
v_exp_f32 v115,v115
D 0,56,2048
v_exp_f32 v116,v116
v_exp_f32 v117,v117
v_fma_f32 v114,v114,s89,s89
v_fma_f32 v115,v115,s89,s89
v_fma_f32 v116,v116,s89,s89
v_fma_f32 v117,v117,s89,s89
D 0,56,3072
v_rcp_f32 v114,v114
v_rcp_f32 v115,v115
v_rcp_f32 v116,v116
v_rcp_f32 v117,v117
v_mul_f32 v26,v26,v114
v_mul_f32 v27,v27,v115
s_add_u32 s58,s58,s61
s_add_u32 m0,s57,16384
s_add_u32 s56,s55,16384
D 0,56,0
v_mul_f32 v28,v28,v116
v_mul_f32 v29,v29,v117
v_cvt_pk_bf16_f32 v122,v26,v27
v_cvt_pk_bf16_f32 v123,v28,v29
v_fma_f32 v30,-v102,v110,v30
v_fma_f32 v31,-v102,v111,v31
D 0,56,1024
v_fma_f32 v32,-v102,v112,v32
v_fma_f32 v33,-v102,v113,v33
v_mul_f32 v30,v103,v30
v_mul_f32 v31,v103,v31
v_mul_f32 v32,v103,v32
v_mul_f32 v33,v103,v33
D 0,56,2048
v_fma_f32 v118,v30,v30,s88
v_fma_f32 v119,v31,v31,s88
v_fma_f32 v120,v32,v32,s88
v_fma_f32 v121,v33,v33,s88
v_mul_f32 v118,v30,v118
v_mul_f32 v119,v31,v119
D 0,56,3072
v_mul_f32 v120,v32,v120
v_mul_f32 v121,v33,v121
v_exp_f32 v118,v118
v_exp_f32 v119,v119
v_exp_f32 v120,v120
v_exp_f32 v121,v121
s_add_u32 s55,s55,s60
s_sub_u32 s57,s84,s57
s_add_u32 s54,s54,s46
v_fma_f32 v118,v118,s89,s89
v_fma_f32 v119,v119,s89,s89
v_fma_f32 v120,v120,s89,s89
v_fma_f32 v121,v121,s89,s89
v_rcp_f32 v118,v118
v_rcp_f32 v119,v119
v_rcp_f32 v120,v120
v_rcp_f32 v121,v121
v_mul_f32 v30,v30,v118
v_mul_f32 v31,v31,v119
v_mul_f32 v32,v32,v120
v_mul_f32 v33,v33,v121
v_cvt_pk_bf16_f32 v124,v30,v31
v_cvt_pk_bf16_f32 v125,v32,v33
buffer_store_dwordx4 v[122:125],v0,s[36:39],s67 offen offset:0 sc0 sc1
s_mov_b32 s72,s69
s_mov_b32 s69,0x7fff0000
s_mov_b32 s67,0x7fff0000
s_mov_b32 s77,0x7fff0000
s_branch Lg3o_gox_%=
Lg3o_xn1_%=:
s_cmp_ge_u32 s54,256
s_cbranch_scc1 Lg3o_fin1_%=
s_add_u32 m0,s59,4096
s_nop 0
buffer_load_dword v8,s[36:39],s58 offen offset:0 sc1 lds
buffer_store_dword v4,v3,s[36:39],s71 offen offset:0 sc0 sc1
s_add_u32 m0,s59,5120
s_nop 0
buffer_load_dword v8,s[36:39],s58 offen offset:0 sc1 lds
buffer_store_dword v4,v3,s[36:39],s71 offen offset:0 sc0 sc1
s_mov_b32 s83,0
L 0
v_fma_f32 v26,-v102,v106,v26
v_fma_f32 v27,-v102,v107,v27
v_fma_f32 v28,-v102,v108,v28
v_fma_f32 v29,-v102,v109,v29
v_mul_f32 v26,v103,v26
v_mul_f32 v27,v103,v27
v_mul_f32 v28,v103,v28
v_mul_f32 v29,v103,v29
v_fma_f32 v114,v26,v26,s88
v_fma_f32 v115,v27,v27,s88
v_fma_f32 v116,v28,v28,s88
v_fma_f32 v117,v29,v29,s88
v_mul_f32 v114,v26,v114
v_mul_f32 v115,v27,v115
v_mul_f32 v116,v28,v116
v_mul_f32 v117,v29,v117
v_exp_f32 v114,v114
v_exp_f32 v115,v115
v_exp_f32 v116,v116
v_exp_f32 v117,v117
v_fma_f32 v114,v114,s89,s89
v_fma_f32 v115,v115,s89,s89
v_fma_f32 v116,v116,s89,s89
v_fma_f32 v117,v117,s89,s89
v_rcp_f32 v114,v114
v_rcp_f32 v115,v115
v_rcp_f32 v116,v116
v_rcp_f32 v117,v117
v_mul_f32 v26,v26,v114
v_mul_f32 v27,v27,v115
v_mul_f32 v28,v28,v116
v_mul_f32 v29,v29,v117
v_cvt_pk_bf16_f32 v122,v26,v27
v_cvt_pk_bf16_f32 v123,v28,v29
v_fma_f32 v30,-v102,v110,v30
v_fma_f32 v31,-v102,v111,v31
v_fma_f32 v32,-v102,v112,v32
v_fma_f32 v33,-v102,v113,v33
v_mul_f32 v30,v103,v30
v_mul_f32 v31,v103,v31
v_mul_f32 v32,v103,v32
v_mul_f32 v33,v103,v33
v_fma_f32 v118,v30,v30,s88
v_fma_f32 v119,v31,v31,s88
v_fma_f32 v120,v32,v32,s88
v_fma_f32 v121,v33,v33,s88
v_mul_f32 v118,v30,v118
v_mul_f32 v119,v31,v119
v_mul_f32 v120,v32,v120
v_mul_f32 v121,v33,v121
v_exp_f32 v118,v118
v_exp_f32 v119,v119
v_exp_f32 v120,v120
v_exp_f32 v121,v121
v_fma_f32 v118,v118,s89,s89
v_fma_f32 v119,v119,s89,s89
v_fma_f32 v120,v120,s89,s89
v_fma_f32 v121,v121,s89,s89
v_rcp_f32 v118,v118
v_rcp_f32 v119,v119
v_rcp_f32 v120,v120
v_rcp_f32 v121,v121
v_mul_f32 v30,v30,v118
v_mul_f32 v31,v31,v119
v_mul_f32 v32,v32,v120
v_mul_f32 v33,v33,v121
v_cvt_pk_bf16_f32 v124,v30,v31
v_cvt_pk_bf16_f32 v125,v32,v33
buffer_store_dwordx4 v[122:125],v0,s[36:39],s67 offen offset:0 sc0 sc1
s_mov_b32 s72,s69
s_mov_b32 s69,0x7fff0000
s_mov_b32 s67,0x7fff0000
s_mov_b32 s77,0x7fff0000
s_branch Lg3o_poll_%=
Lg3o_fin1_%=:
L 0
v_fma_f32 v26,-v102,v106,v26
v_fma_f32 v27,-v102,v107,v27
v_fma_f32 v28,-v102,v108,v28
v_fma_f32 v29,-v102,v109,v29
v_mul_f32 v26,v103,v26
v_mul_f32 v27,v103,v27
v_mul_f32 v28,v103,v28
v_mul_f32 v29,v103,v29
v_fma_f32 v114,v26,v26,s88
v_fma_f32 v115,v27,v27,s88
v_fma_f32 v116,v28,v28,s88
v_fma_f32 v117,v29,v29,s88
v_mul_f32 v114,v26,v114
v_mul_f32 v115,v27,v115
v_mul_f32 v116,v28,v116
v_mul_f32 v117,v29,v117
v_exp_f32 v114,v114
v_exp_f32 v115,v115
v_exp_f32 v116,v116
v_exp_f32 v117,v117
v_fma_f32 v114,v114,s89,s89
v_fma_f32 v115,v115,s89,s89
v_fma_f32 v116,v116,s89,s89
v_fma_f32 v117,v117,s89,s89
v_rcp_f32 v114,v114
v_rcp_f32 v115,v115
v_rcp_f32 v116,v116
v_rcp_f32 v117,v117
v_mul_f32 v26,v26,v114
v_mul_f32 v27,v27,v115
v_mul_f32 v28,v28,v116
v_mul_f32 v29,v29,v117
v_cvt_pk_bf16_f32 v122,v26,v27
v_cvt_pk_bf16_f32 v123,v28,v29
v_fma_f32 v30,-v102,v110,v30
v_fma_f32 v31,-v102,v111,v31
v_fma_f32 v32,-v102,v112,v32
v_fma_f32 v33,-v102,v113,v33
v_mul_f32 v30,v103,v30
v_mul_f32 v31,v103,v31
v_mul_f32 v32,v103,v32
v_mul_f32 v33,v103,v33
v_fma_f32 v118,v30,v30,s88
v_fma_f32 v119,v31,v31,s88
v_fma_f32 v120,v32,v32,s88
v_fma_f32 v121,v33,v33,s88
v_mul_f32 v118,v30,v118
v_mul_f32 v119,v31,v119
v_mul_f32 v120,v32,v120
v_mul_f32 v121,v33,v121
v_exp_f32 v118,v118
v_exp_f32 v119,v119
v_exp_f32 v120,v120
v_exp_f32 v121,v121
v_fma_f32 v118,v118,s89,s89
v_fma_f32 v119,v119,s89,s89
v_fma_f32 v120,v120,s89,s89
v_fma_f32 v121,v121,s89,s89
v_rcp_f32 v118,v118
v_rcp_f32 v119,v119
v_rcp_f32 v120,v120
v_rcp_f32 v121,v121
v_mul_f32 v30,v30,v118
v_mul_f32 v31,v31,v119
v_mul_f32 v32,v32,v120
v_mul_f32 v33,v33,v121
v_cvt_pk_bf16_f32 v124,v30,v31
v_cvt_pk_bf16_f32 v125,v32,v33
buffer_store_dwordx4 v[122:125],v0,s[36:39],s67 offen offset:0 sc0 sc1
s_mov_b32 s72,s69
s_mov_b32 s69,0x7fff0000
s_mov_b32 s67,0x7fff0000
s_mov_b32 s77,0x7fff0000
s_branch Lg3o_done_%=
Lg3o_done_%=:
s_waitcnt vmcnt(1)
s_barrier
buffer_store_dword v4,v3,s[36:39],s70 offen offset:0 sc0 sc1
s_waitcnt vmcnt(1)
s_barrier
buffer_store_dword v4,v3,s[36:39],s72 offen offset:0 sc0 sc1
s_waitcnt vmcnt(0) lgkmcnt(0)
s_barrier
s_endpgm
Lcode_g3o_e:
.purgem M
.purgem R
.purgem T
.purgem D
.purgem L
)"
  :: "s"(ws), "s"(__builtin_amdgcn_readfirstlane(sWave)), "s"(__builtin_amdgcn_readfirstlane(sG0)), "s"(__builtin_amdgcn_readfirstlane(sStep)), "s"(__builtin_amdgcn_readfirstlane(sOutOff)), "s"(__builtin_amdgcn_readfirstlane(sFoutOff)), "s"(__builtin_amdgcn_readfirstlane(sAuxOff)), "s"(__builtin_amdgcn_readfirstlane(sWw)), "s"(__builtin_amdgcn_readfirstlane(sBuf)), "s"(__builtin_amdgcn_readfirstlane(sExp)), "v"(vL16), "v"(vFlag), "v"(vSnapOff), "v"(vSnapRd), "v"(vWd0), "v"(vWd1), "v"(vWr0), "v"(vWr1), "v"(vWr2), "v"(vOutS), "v"(vAux0), "v"(vAux1), "v"(vAux2), "v"(vAux3), "v"(vAn), "v"(vAe), "s"(w3), "s"(out) : "v255", "v0", "v1", "v2", "v3", "v4", "v5", "v6", "v7", "v8", "v9", "v10", "v11", "v12", "v13", "v14", "v15", "v16", "v17", "v18", "v19", "v20", "v21", "v22", "v23", "v24", "v25", "v26", "v27", "v28", "v29", "v30", "v31", "v32", "v33", "v34", "v35", "v36", "v37", "v38", "v39", "v40", "v41", "v42", "v43", "v44", "v45", "v46", "v47", "v48", "v49", "v50", "v51", "v52", "v53", "v54", "v55", "v56", "v57", "v58", "v59", "v60", "v61", "v62", "v63", "v64", "v65", "v66", "v67", "v68", "v69", "v70", "v71", "v72", "v73", "v74", "v75", "v76", "v77", "v78", "v79", "v80", "v81", "v82", "v83", "v84", "v85", "v86", "v87", "v88", "v89", "v90", "v91", "v92", "v93", "v94", "v95", "v96", "v97", "v98", "v99", "v100", "v101", "v102", "v103", "v104", "v105", "v106", "v107", "v108", "v109", "v110", "v111", "v112", "v113", "v114", "v115", "v116", "v117", "v118", "v119", "v120", "v121", "v122", "v123", "v124", "v125", "v126", "v127", "v128", "v129", "v130", "v131", "v132", "v133", "v134", "v135", "v136", "v137", "v138", "v139", "v140", "v141", "v142", "v143", "v144", "v145", "v146", "v147", "v148", "v149", "v150", "v151", "v152", "v153", "v154", "v155", "v156", "v157", "v158", "v159", "v160", "v161", "v162", "v163", "v164", "v165", "v166", "v167", "v168", "v169", "v170", "v171", "v172", "v173", "v174", "v175", "v176", "v177", "v178", "v179", "v180", "v181", "v182", "v183", "v184", "v185", "v186", "v187", "v188", "v189", "v190", "v191", "v192", "v193", "v194", "v195", "v196", "v197", "v198", "v199", "v200", "v201", "v202", "v203", "v204", "v205", "v206", "v207", "v208", "v209", "v210", "v211", "v212", "v213", "v214", "v215", "v216", "v217", "v218", "v219", "v220", "v221", "v222", "v223", "v224", "v225", "v226", "v227", "v228", "v229", "v230", "v231", "a0", "a1", "a2", "a3", "a4", "a5", "a6", "a7", "a8", "a9", "a10", "a11", "a12", "a13", "a14", "a15", "a16", "a17", "a18", "a19", "a20", "a21", "a22", "a23", "a24", "a25", "a26", "a27", "a28", "a29", "a30", "a31", "a32", "a33", "a34", "a35", "a36", "a37", "a38", "a39", "a40", "a41", "a42", "a43", "a44", "a45", "a46", "a47", "a48", "a49", "a50", "a51", "a52", "a53", "a54", "a55", "a56", "a57", "a58", "a59", "a60", "a61", "a62", "a63", "a64", "a65", "a66", "a67", "a68", "a69", "a70", "a71", "a72", "a73", "a74", "a75", "a76", "a77", "a78", "a79", "a80", "a81", "a82", "a83", "a84", "a85", "a86", "a87", "a88", "a89", "a90", "a91", "a92", "a93", "a94", "a95", "a96", "a97", "a98", "a99", "a100", "a101", "a102", "a103", "a104", "a105", "a106", "a107", "a108", "a109", "a110", "a111", "a112", "a113", "a114", "a115", "a116", "a117", "a118", "a119", "a120", "a121", "a122", "a123", "a124", "a125", "a126", "a127", "a128", "a129", "a130", "a131", "a132", "a133", "a134", "a135", "a136", "a137", "a138", "a139", "a140", "a141", "a142", "a143", "a144", "a145", "a146", "a147", "a148", "a149", "a150", "a151", "a152", "a153", "a154", "a155", "a156", "a157", "a158", "a159", "a160", "a161", "a162", "a163", "a164", "a165", "a166", "a167", "a168", "a169", "a170", "a171", "a172", "a173", "a174", "a175", "a176", "a177", "a178", "a179", "a180", "a181", "a182", "a183", "a184", "a185", "a186", "a187", "a188", "a189", "a190", "a191", "a192", "a193", "a194", "a195", "a196", "a197", "a198", "a199", "a200", "a201", "a202", "a203", "a204", "a205", "a206", "a207", "a208", "a209", "a210", "a211", "a212", "a213", "a214", "a215", "a216", "a217", "a218", "a219", "a220", "a221", "a222", "a223", "a224", "a225", "a226", "a227", "a228", "a229", "a230", "a231", "a232", "a233", "a234", "a235", "a236", "a237", "a238", "a239", "a240", "a241", "a242", "a243", "a244", "a245", "a246", "a247", "a248", "a249", "a250", "a251", "a252", "a253", "a254", "a255", "s36", "s37", "s38", "s39", "s40", "s41", "s42", "s43", "s44", "s45", "s46", "s47", "s48", "s49", "s50", "s51", "s52", "s53", "s54", "s55", "s56", "s57", "s58", "s59", "s60", "s61", "s62", "s63", "s64", "s65", "s66", "s67", "s68", "s69", "s70", "s71", "s72", "s73", "s74", "s75", "s76", "s77", "s78", "s79", "s80", "s81", "s82", "s83", "s84", "s85", "s86", "s87", "s88", "s89", "s90", "s91", "s92", "s93", "s94", "s95", "s96", "s97", "vcc", "scc", "memory"); }
    __builtin_unreachable();
  }
  if (kind < 3) {
    const unsigned sStep = 4;
    COMMON(kind == 1 ? 8 : 16, kind == 1 ? fj * 128 : fj < 8 ? 1024 + fj * 128 : (fj & 2) * 1024 + (fj - 8) * 128, 1024, 131072, 2048, 256, kind == 1 ? 0 : 32768);
    const int sr = l15 - ((wave & 2) ? -1 : 1), sr2 = l15 - ((wave & 2) ? -2 : 2), nx = (slice >> 1) & 1;
    auto src = [&](int r_) { return (unsigned)((r_ & 16) ? 1024 + lg * 64 + (r_ & 3) * 16 : (lg * 16 + r_) * 16); };
    const unsigned vOutS = src(sr2), vSp = (sr2 & 16) ? OOB : vOutS;
    (void)vWr2;
    const unsigned vAn = kind == 1 ? HU + vL16 : src(sr);
    const unsigned vAe = kind == 1 ? ((l15 >> 2) == (nx ? 0 : 3) ? (nx ? 0 : 2 * HU) + 1024 + lg * 64 + (l15 & 3) * 16 : OOB) : (sr & 16) ? OOB : vAn;
    const unsigned vAux0 = l15 * 16, vAux1 = 139264 + (l15 * 2 + (lg & 1)) * 16 + wave * 4 + (lg >> 1) * 512, vAux2 = 139264 + (l15 * 2 + (lg & 1)) * 16;
    const unsigned vAux3 = (wave == 0 && lg < 2) ? slice * 128 + l15 * 8 + lg * 4 : OOB;
    const unsigned sOutOff = kind == 1 ? OFF_HS - HU + (slice * 4 + wave) * 1280 : OFF_X1 + (slice * 4 + wave) * 1024, sFoutOff = (kind == 1 ? OFF_FH : OFF_FX1) + slice * 128;
    const unsigned sAuxOff = OFF_XP + (slice * 4 + wave) * 1024, sBuf = kind, sWw = 16 * wave * 2048 + slice * 256;
    const u16* w12 = kind == 1 ? w1 : w2;
    const unsigned sExp = __builtin_amdgcn_readfirstlane(ep);
    unsigned epOut;
    asm volatile(
R"(.macro M d,w,x
v_mfma_f32_16x16x32_bf16 v[\d:\d+3], \w, v[\x:\x+3], v[\d:\d+3]
.endm
.macro R d,a,o
ds_read_b128 v[\d:\d+3], v\a offset:\o
.endm
.macro T d,a,o
ds_read_b64_tr_b16 \d, v\a offset:\o
.endm
.macro D a,s,o
buffer_load_dwordx4 v\a, s[36:39], s\s offen offset:\o sc1 lds
.endm
.macro L c
s_waitcnt lgkmcnt(\c)
.endm
Lcode_g12_b:
s_cmp_eq_u32 %9,2
s_cbranch_scc1 Lg2_begin_%=
s_mov_b64 s[36:37],%1
s_mov_b32 s38,111243392
s_mov_b32 s39,0x00020000
s_mov_b32 s44,%2
s_mov_b32 s45,%3
s_mov_b32 s46,%4
s_mov_b32 s47,%5
s_mov_b32 s48,%6
s_mov_b32 s49,%7
s_mov_b32 s50,%8
s_mov_b32 s51,%9
s_mov_b32 s53,%10
v_mov_b32 v0,%11
v_mov_b32 v3,%12
v_mov_b32 v5,%13
v_mov_b32 v6,%14
v_mov_b32 v18,%15
v_mov_b32 v19,%16
v_mov_b32 v20,%17
v_mov_b32 v21,%18
v_mov_b32 v9,%19
v_mov_b32 v10,%20
v_mov_b32 v11,%21
v_mov_b32 v12,%22
v_mov_b32 v13,%23
v_mov_b32 v14,%24
v_mov_b32 v15,%25
v_mov_b32 v16,%26
s_mov_b64 s[40:41],%27
s_mov_b32 s42,2097152
s_mov_b32 s43,0x00020000
s_lshl_b32 s57,s44,11
s_waitcnt vmcnt(20) lgkmcnt(0)
s_barrier
s_add_u32 s55,s50,786432
s_add_u32 m0,s57,98304
s_nop 0
buffer_load_dwordx4 v18,s[40:43],s55 offen offset:0  lds
s_add_u32 m0,s57,99328
s_nop 0
buffer_load_dwordx4 v19,s[40:43],s55 offen offset:0  lds
s_add_u32 m0,s57,106368
s_nop 0
buffer_load_dwordx4 v18,s[40:43],s55 offen offset:128  lds
s_add_u32 m0,s57,107392
s_nop 0
buffer_load_dwordx4 v19,s[40:43],s55 offen offset:128  lds
v_add_u32 v26,0,v20
v_add_u32 v27,0,v21
T a[0:1],26,0
T a[2:3],26,512
T a[4:5],27,0
T a[6:7],27,512
T a[8:9],26,4096
T a[10:11],26,4608
T a[12:13],27,4096
T a[14:15],27,4608
s_waitcnt vmcnt(20) lgkmcnt(0)
s_barrier
s_add_u32 s55,s50,917504
s_add_u32 m0,s57,0
s_nop 0
buffer_load_dwordx4 v18,s[40:43],s55 offen offset:0  lds
s_add_u32 m0,s57,1024
s_nop 0
buffer_load_dwordx4 v19,s[40:43],s55 offen offset:0  lds
s_add_u32 m0,s57,8064
s_nop 0
buffer_load_dwordx4 v18,s[40:43],s55 offen offset:128  lds
s_add_u32 m0,s57,9088
s_nop 0
buffer_load_dwordx4 v19,s[40:43],s55 offen offset:128  lds
v_add_u32 v26,16384,v20
v_add_u32 v27,16384,v21
T a[16:17],26,0
T a[18:19],26,512
T a[20:21],27,0
T a[22:23],27,512
T a[24:25],26,4096
T a[26:27],26,4608
T a[28:29],27,4096
T a[30:31],27,4608
s_waitcnt vmcnt(20) lgkmcnt(0)
s_barrier
s_add_u32 s55,s50,1048576
s_add_u32 m0,s57,16384
s_nop 0
buffer_load_dwordx4 v18,s[40:43],s55 offen offset:0  lds
s_add_u32 m0,s57,17408
s_nop 0
buffer_load_dwordx4 v19,s[40:43],s55 offen offset:0  lds
s_add_u32 m0,s57,24448
s_nop 0
buffer_load_dwordx4 v18,s[40:43],s55 offen offset:128  lds
s_add_u32 m0,s57,25472
s_nop 0
buffer_load_dwordx4 v19,s[40:43],s55 offen offset:128  lds
v_add_u32 v26,32768,v20
v_add_u32 v27,32768,v21
T a[32:33],26,0
T a[34:35],26,512
T a[36:37],27,0
T a[38:39],27,512
T a[40:41],26,4096
T a[42:43],26,4608
T a[44:45],27,4096
T a[46:47],27,4608
s_waitcnt vmcnt(20) lgkmcnt(0)
s_barrier
s_add_u32 s55,s50,1179648
s_add_u32 m0,s57,32768
s_nop 0
buffer_load_dwordx4 v18,s[40:43],s55 offen offset:0  lds
s_add_u32 m0,s57,33792
s_nop 0
buffer_load_dwordx4 v19,s[40:43],s55 offen offset:0  lds
s_add_u32 m0,s57,40832
s_nop 0
buffer_load_dwordx4 v18,s[40:43],s55 offen offset:128  lds
s_add_u32 m0,s57,41856
s_nop 0
buffer_load_dwordx4 v19,s[40:43],s55 offen offset:128  lds
v_add_u32 v26,49152,v20
v_add_u32 v27,49152,v21
T a[48:49],26,0
T a[50:51],26,512
T a[52:53],27,0
T a[54:55],27,512
T a[56:57],26,4096
T a[58:59],26,4608
T a[60:61],27,4096
T a[62:63],27,4608
s_waitcnt vmcnt(20) lgkmcnt(0)
s_barrier
s_add_u32 s55,s50,1310720
s_add_u32 m0,s57,49152
s_nop 0
buffer_load_dwordx4 v18,s[40:43],s55 offen offset:0  lds
s_add_u32 m0,s57,50176
s_nop 0
buffer_load_dwordx4 v19,s[40:43],s55 offen offset:0  lds
s_add_u32 m0,s57,57216
s_nop 0
buffer_load_dwordx4 v18,s[40:43],s55 offen offset:128  lds
s_add_u32 m0,s57,58240
s_nop 0
buffer_load_dwordx4 v19,s[40:43],s55 offen offset:128  lds
v_add_u32 v26,65536,v20
v_add_u32 v27,65536,v21
T a[64:65],26,0
T a[66:67],26,512
T a[68:69],27,0
T a[70:71],27,512
T a[72:73],26,4096
T a[74:75],26,4608
T a[76:77],27,4096
T a[78:79],27,4608
s_waitcnt vmcnt(20) lgkmcnt(0)
s_barrier
s_add_u32 s55,s50,1441792
s_add_u32 m0,s57,65536
s_nop 0
buffer_load_dwordx4 v18,s[40:43],s55 offen offset:0  lds
s_add_u32 m0,s57,66560
s_nop 0
buffer_load_dwordx4 v19,s[40:43],s55 offen offset:0  lds
s_add_u32 m0,s57,73600
s_nop 0
buffer_load_dwordx4 v18,s[40:43],s55 offen offset:128  lds
s_add_u32 m0,s57,74624
s_nop 0
buffer_load_dwordx4 v19,s[40:43],s55 offen offset:128  lds
v_add_u32 v26,81920,v20
v_add_u32 v27,81920,v21
T a[80:81],26,0
T a[82:83],26,512
T a[84:85],27,0
T a[86:87],27,512
T a[88:89],26,4096
T a[90:91],26,4608
T a[92:93],27,4096
T a[94:95],27,4608
s_waitcnt vmcnt(20) lgkmcnt(0)
s_barrier
s_add_u32 s55,s50,1572864
s_add_u32 m0,s57,81920
s_nop 0
buffer_load_dwordx4 v18,s[40:43],s55 offen offset:0  lds
s_add_u32 m0,s57,82944
s_nop 0
buffer_load_dwordx4 v19,s[40:43],s55 offen offset:0  lds
s_add_u32 m0,s57,89984
s_nop 0
buffer_load_dwordx4 v18,s[40:43],s55 offen offset:128  lds
s_add_u32 m0,s57,91008
s_nop 0
buffer_load_dwordx4 v19,s[40:43],s55 offen offset:128  lds
v_add_u32 v26,98304,v20
v_add_u32 v27,98304,v21
T a[96:97],26,0
T a[98:99],26,512
T a[100:101],27,0
T a[102:103],27,512
T a[104:105],26,4096
T a[106:107],26,4608
T a[108:109],27,4096
T a[110:111],27,4608
s_waitcnt vmcnt(20) lgkmcnt(0)
s_barrier
s_add_u32 s55,s50,1703936
s_add_u32 m0,s57,98304
s_nop 0
buffer_load_dwordx4 v18,s[40:43],s55 offen offset:0  lds
s_add_u32 m0,s57,99328
s_nop 0
buffer_load_dwordx4 v19,s[40:43],s55 offen offset:0  lds
s_add_u32 m0,s57,106368
s_nop 0
buffer_load_dwordx4 v18,s[40:43],s55 offen offset:128  lds
s_add_u32 m0,s57,107392
s_nop 0
buffer_load_dwordx4 v19,s[40:43],s55 offen offset:128  lds
v_add_u32 v26,0,v20
v_add_u32 v27,0,v21
T a[112:113],26,0
T a[114:115],26,512
T a[116:117],27,0
T a[118:119],27,512
T a[120:121],26,4096
T a[122:123],26,4608
T a[124:125],27,4096
T a[126:127],27,4608
s_waitcnt vmcnt(20) lgkmcnt(0)
s_barrier
s_add_u32 s55,s50,1835008
s_add_u32 m0,s57,0
s_nop 0
buffer_load_dwordx4 v18,s[40:43],s55 offen offset:0  lds
s_add_u32 m0,s57,1024
s_nop 0
buffer_load_dwordx4 v19,s[40:43],s55 offen offset:0  lds
s_add_u32 m0,s57,8064
s_nop 0
buffer_load_dwordx4 v18,s[40:43],s55 offen offset:128  lds
s_add_u32 m0,s57,9088
s_nop 0
buffer_load_dwordx4 v19,s[40:43],s55 offen offset:128  lds
v_add_u32 v26,16384,v20
v_add_u32 v27,16384,v21
T a[128:129],26,0
T a[130:131],26,512
T a[132:133],27,0
T a[134:135],27,512
T a[136:137],26,4096
T a[138:139],26,4608
T a[140:141],27,4096
T a[142:143],27,4608
s_waitcnt vmcnt(20) lgkmcnt(0)
s_barrier
s_add_u32 s55,s50,1966080
s_add_u32 m0,s57,16384
s_nop 0
buffer_load_dwordx4 v18,s[40:43],s55 offen offset:0  lds
s_add_u32 m0,s57,17408
s_nop 0
buffer_load_dwordx4 v19,s[40:43],s55 offen offset:0  lds
s_add_u32 m0,s57,24448
s_nop 0
buffer_load_dwordx4 v18,s[40:43],s55 offen offset:128  lds
s_add_u32 m0,s57,25472
s_nop 0
buffer_load_dwordx4 v19,s[40:43],s55 offen offset:128  lds
v_add_u32 v26,32768,v20
v_add_u32 v27,32768,v21
T a[144:145],26,0
T a[146:147],26,512
T a[148:149],27,0
T a[150:151],27,512
T a[152:153],26,4096
T a[154:155],26,4608
T a[156:157],27,4096
T a[158:159],27,4608
s_waitcnt vmcnt(20) lgkmcnt(0)
s_barrier
v_add_u32 v26,49152,v20
v_add_u32 v27,49152,v21
T a[160:161],26,0
T a[162:163],26,512
T a[164:165],27,0
T a[166:167],27,512
T a[168:169],26,4096
T a[170:171],26,4608
T a[172:173],27,4096
T a[174:175],27,4608
s_waitcnt vmcnt(16) lgkmcnt(0)
s_barrier
v_add_u32 v26,65536,v20
v_add_u32 v27,65536,v21
T a[176:177],26,0
T a[178:179],26,512
T a[180:181],27,0
T a[182:183],27,512
T a[184:185],26,4096
T a[186:187],26,4608
T a[188:189],27,4096
T a[190:191],27,4608
s_waitcnt vmcnt(12) lgkmcnt(0)
s_barrier
v_add_u32 v26,81920,v20
v_add_u32 v27,81920,v21
T a[192:193],26,0
T a[194:195],26,512
T a[196:197],27,0
T a[198:199],27,512
T a[200:201],26,4096
T a[202:203],26,4608
T a[204:205],27,4096
T a[206:207],27,4608
s_waitcnt vmcnt(8) lgkmcnt(0)
s_barrier
v_add_u32 v26,98304,v20
v_add_u32 v27,98304,v21
T a[208:209],26,0
T a[210:211],26,512
T a[212:213],27,0
T a[214:215],27,512
T a[216:217],26,4096
T a[218:219],26,4608
T a[220:221],27,4096
T a[222:223],27,4608
s_waitcnt vmcnt(4) lgkmcnt(0)
s_barrier
v_add_u32 v26,0,v20
v_add_u32 v27,0,v21
T a[224:225],26,0
T a[226:227],26,512
T a[228:229],27,0
T a[230:231],27,512
T a[232:233],26,4096
T a[234:235],26,4608
T a[236:237],27,4096
T a[238:239],27,4608
s_waitcnt vmcnt(0) lgkmcnt(0)
s_barrier
v_add_u32 v26,16384,v20
v_add_u32 v27,16384,v21
T a[240:241],26,0
T a[242:243],26,512
T a[244:245],27,0
T a[246:247],27,512
T a[248:249],26,4096
T a[250:251],26,4608
T a[252:253],27,4096
T a[254:255],27,4608
L 0
s_barrier
v_mov_b32 v34,0x3f803f80
v_mov_b32 v35,0x3f803f80
v_mov_b32 v36,0x3f803f80
v_mov_b32 v37,0x3f803f80
s_nop 1
v_mfma_f32_16x16x32_bf16 v[106:109],a[0:3],v[34:37],0
v_mfma_f32_16x16x32_bf16 v[110:113],a[4:7],v[34:37],0
M 106,a[8:11],34
M 110,a[12:15],34
M 106,a[16:19],34
M 110,a[20:23],34
M 106,a[24:27],34
M 110,a[28:31],34
M 106,a[32:35],34
M 110,a[36:39],34
M 106,a[40:43],34
M 110,a[44:47],34
M 106,a[48:51],34
M 110,a[52:55],34
M 106,a[56:59],34
M 110,a[60:63],34
M 106,a[64:67],34
M 110,a[68:71],34
M 106,a[72:75],34
M 110,a[76:79],34
M 106,a[80:83],34
M 110,a[84:87],34
M 106,a[88:91],34
M 110,a[92:95],34
M 106,a[96:99],34
M 110,a[100:103],34
M 106,a[104:107],34
M 110,a[108:111],34
M 106,a[112:115],34
M 110,a[116:119],34
M 106,a[120:123],34
M 110,a[124:127],34
M 106,a[128:131],34
M 110,a[132:135],34
M 106,a[136:139],34
M 110,a[140:143],34
M 106,a[144:147],34
M 110,a[148:151],34
M 106,a[152:155],34
M 110,a[156:159],34
M 106,a[160:163],34
M 110,a[164:167],34
M 106,a[168:171],34
M 110,a[172:175],34
M 106,a[176:179],34
M 110,a[180:183],34
M 106,a[184:187],34
M 110,a[188:191],34
M 106,a[192:195],34
M 110,a[196:199],34
M 106,a[200:203],34
M 110,a[204:207],34
M 106,a[208:211],34
M 110,a[212:215],34
M 106,a[216:219],34
M 110,a[220:223],34
M 106,a[224:227],34
M 110,a[228:231],34
M 106,a[232:235],34
M 110,a[236:239],34
M 106,a[240:243],34
M 110,a[244:247],34
M 106,a[248:251],34
M 110,a[252:255],34
v_mov_b32 v4,s53
s_mov_b32 s82,0
s_mov_b32 s80,0
s_mov_b32 s54,s45
s_mul_i32 s57,s44,4096
s_mul_i32 s55,s45,32768
s_add_u32 s55,s55,s57
s_add_u32 s55,s55,8388608
s_mul_i32 s57,s44,4096
s_mul_i32 s60,s46,32768
s_mul_i32 s61,s46,1024
s_mul_i32 s62,s46,40960
s_mul_i32 s63,s46,1024
s_mul_i32 s64,s46,512
s_lshl_b32 s59,s44,8
s_add_u32 s59,s59,131072
s_mul_i32 s58,s45,1024
s_add_u32 s58,s58,109056000
s_mul_i32 s65,s45,40960
s_add_u32 s65,s65,s47
s_mul_i32 s68,s45,1024
s_add_u32 s68,s68,s48
s_mul_i32 s73,s45,512
s_add_u32 s73,s73,106954752
s_mul_i32 s74,s45,32768
s_add_u32 s74,s74,s49
s_mul_i32 s75,s45,1024
s_add_u32 s75,s75,107479040
s_mov_b32 s66,0x7fff0000
s_mov_b32 s67,0x7fff0000
s_mov_b32 s69,0x7fff0000
s_mov_b32 s76,0x7fff0000
s_mov_b32 s77,0x7fff0000
s_mov_b32 s70,0x7fff0000
s_mov_b32 s71,0x7fff0000
s_mov_b32 s72,0x7fff0000
v_mov_b32 v1,v0
v_add_u32 v2,16384,v0
s_mov_b32 s84,16384
s_mov_b32 s86,0x3a800000
s_mov_b32 s87,0x3727c5ac
s_mov_b32 s88,0x409d30fc
s_mov_b32 s89,0xbeeff558
s_lshr_b32 s85,s44,1
s_mul_i32 s85,s85,127
v_lshlrev_b32 v8,22,v0
v_and_b32 v8,0x80000000,v8
v_or_b32 v8,v8,v5
v_add_u32 v2,32768,v0
s_lshl_b32 s84,s57,1
s_add_u32 s84,s84,32768
s_getpc_b64 s[40:41]
Lg1_pc_%=:
s_lshl_b32 s78,s44,13
v_lshlrev_b32 v114,3,v0
v_add_u32 v114,s78,v114
v_min_u32 v114,Lcode_g2_b-Lg1_pc_%=-4,v114
)"
R"(s_mov_b32 m0,163584
s_nop 0
global_load_lds_dword v114,s[40:41]
s_add_u32 m0,s59,4096
s_nop 0
buffer_load_dword v8,s[36:39],s58 offen offset:0 sc1 lds
buffer_store_dword v4,v3,s[36:39],s71 offen offset:0 sc0 sc1
s_add_u32 m0,s59,5120
s_nop 0
buffer_load_dword v8,s[36:39],s58 offen offset:0 sc1 lds
buffer_store_dword v4,v3,s[36:39],s71 offen offset:0 sc0 sc1
s_mov_b32 s83,0
Lg1_poll_%=:
s_add_u32 s78,s83,2
s_and_b32 s78,s78,3
s_lshl_b32 s78,s78,10
s_add_u32 s78,s78,4096
s_add_u32 m0,s59,s78
s_nop 0
buffer_load_dword v8,s[36:39],s58 offen offset:0 sc1 lds
s_and_b32 s78,s83,3
s_lshl_b32 s78,s78,10
v_add_u32 v114,s78,v6
s_waitcnt vmcnt(4)
s_barrier
ds_read_b32 v7,v114 offset:4096
buffer_store_dword v4,v3,s[36:39],s70 offen offset:0 sc0 sc1
s_mov_b32 s70,s71
s_mov_b32 s71,s72
s_mov_b32 s72,0x7fff0000
s_add_u32 s83,s83,1
s_add_u32 s82,s82,1
L 0
v_cmp_eq_u32 vcc,s53,v7
s_nop 0
s_cmp_eq_u32 vcc_lo,-1
s_cbranch_scc1 Lg1_go_%=
s_cmp_lt_u32 s82,20000
s_cbranch_scc1 Lg1_poll_%=
Lg1_go_%=:
s_add_u32 m0,s57,0
s_add_u32 s56,s55,0
D 0,56,0
D 0,56,1024
D 0,56,2048
D 0,56,3072
s_add_u32 s58,s58,s61
s_add_u32 m0,s59,2048
s_nop 0
buffer_load_dword v8,s[36:39],s58 offen offset:0 sc1 lds
s_add_u32 m0,s57,16384
s_add_u32 s56,s55,16384
D 0,56,0
D 0,56,1024
D 0,56,2048
D 0,56,3072
s_add_u32 s55,s55,s60
s_sub_u32 s57,s84,s57
s_add_u32 s54,s54,s46
s_waitcnt vmcnt(4)
s_barrier
buffer_store_dword v4,v3,s[36:39],s70 offen offset:0 sc0 sc1
s_min_u32 s71,s71,s72
buffer_store_dword v4,v3,s[36:39],s71 offen offset:0 sc0 sc1
s_mov_b32 s70,0x7fff0000
s_mov_b32 s71,0x7fff0000
s_mov_b32 s72,0x7fff0000
s_cmp_eq_u32 s80,1
buffer_store_dword v4,v3,s[36:39],s71 offen offset:0 sc0 sc1
s_cbranch_scc1 Lg1_st1_%=
s_branch Lg1_st0_%=
Lg1_gox_%=:
s_add_u32 m0,s59,2048
s_nop 0
buffer_load_dword v8,s[36:39],s58 offen offset:0 sc1 lds
s_waitcnt vmcnt(0)
s_barrier
buffer_store_dword v4,v3,s[36:39],s70 offen offset:0 sc0 sc1
s_min_u32 s71,s71,s72
buffer_store_dword v4,v3,s[36:39],s71 offen offset:0 sc0 sc1
s_mov_b32 s70,0x7fff0000
s_mov_b32 s71,0x7fff0000
s_mov_b32 s72,0x7fff0000
s_cmp_eq_u32 s80,1
s_cbranch_scc1 Lg1_st1_%=
Lg1_st0_%=:
ds_read_b32 v7,v6 offset:2048
R 34,1,0
R 38,1,1024
R 42,1,2048
R 46,1,3072
R 50,1,4096
L 5
v_cmp_eq_u32 vcc,s53,v7
L 3
s_cmp_eq_u32 vcc_lo,-1
s_cbranch_scc1 Lg1_F0_%=
s_branch Lg1_L0_%=
Lg1_st1_%=:
ds_read_b32 v7,v6 offset:2048
R 34,2,0
R 38,2,1024
R 42,2,2048
R 46,2,3072
R 50,2,4096
L 5
v_cmp_eq_u32 vcc,s53,v7
L 3
s_cmp_eq_u32 vcc_lo,-1
s_cbranch_scc1 Lg1_F1_%=
s_branch Lg1_L1_%=
Lg1_F0_%=:
v_mfma_f32_16x16x32_bf16 v[22:25],a[4:7],v[34:37],0
s_add_u32 s58,s58,s61
s_add_u32 m0,s59,0
R 54,1,5120
M 22,a[12:15],38
buffer_load_dword v8,s[36:39],s58 offen offset:0 sc1 lds
v_mfma_f32_16x16x32_bf16 v[18:21],a[0:3],v[34:37],0
s_add_u32 m0,s57,0
R 58,1,6144
v_fma_f32 v26,-v102,v106,v26
M 18,a[8:11],38
L 3
s_add_u32 s56,s55,0
v_fma_f32 v27,-v102,v107,v27
M 18,a[16:19],42
s_mov_b32 s66,s65
R 62,1,7168
v_fma_f32 v28,-v102,v108,v28
M 18,a[24:27],46
D 0,56,0
M 22,a[20:23],42
s_add_u32 s65,s65,s62
R 66,1,8192
v_fma_f32 v29,-v102,v109,v29
v_mul_f32 v26,v103,v26
M 22,a[28:31],46
L 3
D 0,56,1024
M 22,a[36:39],50
s_add_u32 s54,s54,s46
R 70,1,9216
v_mul_f32 v27,v103,v27
M 22,a[44:47],54
v_mul_f32 v28,v103,v28
v_mul_f32 v29,v103,v29
M 18,a[32:35],50
R 74,1,10240
v_fma_f32 v114,v26,v26,s88
M 18,a[40:43],54
L 3
D 0,56,2048
M 18,a[48:51],58
R 78,1,11264
v_fma_f32 v115,v27,v27,s88
M 18,a[56:59],62
D 0,56,3072
v_fma_f32 v116,v28,v28,s88
M 22,a[52:55],58
s_add_u32 m0,s57,16384
R 82,1,12288
v_fma_f32 v117,v29,v29,s88
M 22,a[60:63],62
L 3
v_mul_f32 v114,v26,v114
v_mul_f32 v115,v27,v115
M 22,a[68:71],66
s_add_u32 s56,s55,16384
R 86,1,13312
v_mul_f32 v116,v28,v116
M 22,a[76:79],70
buffer_load_dwordx2 v[98:99],v11,s[36:39],s73 offen offset:0 sc1
M 18,a[64:67],66
s_add_u32 s73,s73,s64
R 90,1,14336
v_mul_f32 v117,v29,v117
M 18,a[72:75],70
L 3
v_exp_f32 v114,v114
v_fma_f32 v30,-v102,v110,v30
M 18,a[80:83],74
s_waitcnt vmcnt(9)
R 94,1,15360
v_fma_f32 v114,v114,s89,s89
M 18,a[88:91],78
s_barrier
v_exp_f32 v115,v115
M 22,a[84:87],74
s_add_u32 s55,s55,s60
R 34,1,16384
v_fma_f32 v115,v115,s89,s89
M 22,a[92:95],78
L 3
v_exp_f32 v116,v116
M 22,a[100:103],82
s_sub_u32 s57,s84,s57
R 38,1,17408
v_fma_f32 v116,v116,s89,s89
M 22,a[108:111],86
D 0,56,0
M 18,a[96:99],82
R 42,1,18432
v_exp_f32 v117,v117
M 18,a[104:107],86
L 3
v_rcp_f32 v114,v114
M 18,a[112:115],90
R 46,1,19456
v_fma_f32 v117,v117,s89,s89
M 18,a[120:123],94
v_rcp_f32 v115,v115
M 22,a[116:119],90
R 50,1,20480
v_mul_f32 v26,v26,v114
M 22,a[124:127],94
L 3
v_rcp_f32 v116,v116
M 22,a[132:135],34
R 54,1,21504
v_mul_f32 v27,v27,v115
M 22,a[140:143],38
D 0,56,1024
v_mul_f32 v28,v28,v116
M 18,a[128:131],34
R 58,1,22528
v_cvt_pk_bf16_f32 v122,v26,v27
M 18,a[136:139],38
L 3
v_rcp_f32 v117,v117
M 18,a[144:147],42
R 62,1,23552
v_mul_f32 v29,v29,v117
M 18,a[152:155],46
v_cvt_pk_bf16_f32 v123,v28,v29
v_fma_f32 v31,-v102,v111,v31
M 22,a[148:151],42
R 66,1,24576
v_fma_f32 v32,-v102,v112,v32
M 22,a[156:159],46
L 3
D 0,56,2048
v_fma_f32 v33,-v102,v113,v33
M 22,a[164:167],50
R 70,1,25600
v_mul_f32 v30,v103,v30
M 22,a[172:175],54
v_mul_f32 v31,v103,v31
v_mul_f32 v32,v103,v32
M 18,a[160:163],50
R 74,1,26624
v_mul_f32 v33,v103,v33
M 18,a[168:171],54
L 3
v_fma_f32 v118,v30,v30,s88
v_fma_f32 v119,v31,v31,s88
M 18,a[176:179],58
R 78,1,27648
v_fma_f32 v120,v32,v32,s88
M 18,a[184:187],62
v_fma_f32 v121,v33,v33,s88
v_mul_f32 v118,v30,v118
M 22,a[180:183],58
R 82,1,28672
v_exp_f32 v118,v118
M 22,a[188:191],62
L 3
D 0,56,3072
M 22,a[196:199],66
R 86,1,29696
v_mul_f32 v119,v31,v119
M 22,a[204:207],70
v_exp_f32 v119,v119
M 18,a[192:195],66
R 90,1,30720
v_mul_f32 v120,v32,v120
M 18,a[200:203],70
L 3
v_mul_f32 v121,v33,v121
v_fma_f32 v118,v118,s89,s89
M 18,a[208:211],74
s_waitcnt vmcnt(5)
R 94,1,31744
v_fma_f32 v119,v119,s89,s89
M 18,a[216:219],78
s_barrier
ds_read_b32 v7,v6 offset:0
v_exp_f32 v120,v120
M 22,a[212:215],74
R 34,2,0
v_fma_f32 v120,v120,s89,s89
M 22,a[220:223],78
L 4
buffer_store_dword v4,v3,s[36:39],s70 offen offset:0 sc0 sc1
v_exp_f32 v121,v121
M 22,a[228:231],82
s_mov_b32 s70,s69
R 38,2,1024
v_fma_f32 v121,v121,s89,s89
v_rcp_f32 v118,v118
v_rcp_f32 v119,v119
v_rcp_f32 v120,v120
v_rcp_f32 v121,v121
M 22,a[236:239],86
s_mov_b32 s69,s68
v_mul_f32 v30,v30,v118
v_mul_f32 v31,v31,v119
v_mul_f32 v32,v32,v120
v_mul_f32 v33,v33,v121
v_cvt_pk_bf16_f32 v124,v30,v31
v_cvt_pk_bf16_f32 v125,v32,v33
M 18,a[224:227],82
s_add_u32 s68,s68,s63
R 42,2,2048
buffer_store_dwordx4 v[122:125],v15,s[36:39],s67 offen offset:0 sc0 sc1
M 18,a[232:235],86
L 4
L 3
M 18,a[240:243],90
R 46,2,3072
v_cmp_eq_u32 vcc,s53,v7
M 18,a[248:251],94
buffer_store_dwordx4 v[122:125],v16,s[36:39],s67 offen offset:0 sc0 sc1
M 22,a[244:247],90
s_waitcnt vmcnt(7) lgkmcnt(2)
s_cmp_eq_u32 vcc_lo,-1
R 50,2,4096
M 22,a[252:255],94
s_cbranch_scc0 Lg1_K1_%=
Lg1_F1_%=:
v_mfma_f32_16x16x32_bf16 v[30:33],a[4:7],v[34:37],0
s_add_u32 s58,s58,s61
s_add_u32 m0,s59,1024
R 54,2,5120
M 30,a[12:15],38
buffer_load_dword v8,s[36:39],s58 offen offset:0 sc1 lds
v_mfma_f32_16x16x32_bf16 v[26:29],a[0:3],v[34:37],0
s_add_u32 m0,s57,0
R 58,2,6144
v_fma_f32 v18,-v98,v106,v18
M 26,a[8:11],38
L 3
s_add_u32 s56,s55,0
v_fma_f32 v19,-v98,v107,v19
M 26,a[16:19],42
s_mov_b32 s67,s65
R 62,2,7168
v_fma_f32 v20,-v98,v108,v20
M 26,a[24:27],46
D 0,56,0
M 30,a[20:23],42
s_add_u32 s65,s65,s62
R 66,2,8192
v_fma_f32 v21,-v98,v109,v21
v_mul_f32 v18,v99,v18
M 30,a[28:31],46
L 3
D 0,56,1024
M 30,a[36:39],50
s_add_u32 s54,s54,s46
R 70,2,9216
v_mul_f32 v19,v99,v19
M 30,a[44:47],54
v_mul_f32 v20,v99,v20
v_mul_f32 v21,v99,v21
M 26,a[32:35],50
R 74,2,10240
v_fma_f32 v114,v18,v18,s88
M 26,a[40:43],54
L 3
D 0,56,2048
M 26,a[48:51],58
R 78,2,11264
v_fma_f32 v115,v19,v19,s88
M 26,a[56:59],62
D 0,56,3072
v_fma_f32 v116,v20,v20,s88
M 30,a[52:55],58
s_add_u32 m0,s57,16384
R 82,2,12288
v_fma_f32 v117,v21,v21,s88
M 30,a[60:63],62
L 3
v_mul_f32 v114,v18,v114
v_mul_f32 v115,v19,v115
M 30,a[68:71],66
s_add_u32 s56,s55,16384
R 86,2,13312
v_mul_f32 v116,v20,v116
M 30,a[76:79],70
buffer_load_dwordx2 v[102:103],v11,s[36:39],s73 offen offset:0 sc1
M 26,a[64:67],66
s_add_u32 s73,s73,s64
R 90,2,14336
v_mul_f32 v117,v21,v117
M 26,a[72:75],70
L 3
v_exp_f32 v114,v114
v_fma_f32 v22,-v98,v110,v22
M 26,a[80:83],74
s_waitcnt vmcnt(9)
R 94,2,15360
v_fma_f32 v114,v114,s89,s89
M 26,a[88:91],78
s_barrier
v_exp_f32 v115,v115
M 30,a[84:87],74
s_add_u32 s55,s55,s60
R 34,2,16384
v_fma_f32 v115,v115,s89,s89
M 30,a[92:95],78
L 3
v_exp_f32 v116,v116
M 30,a[100:103],82
s_sub_u32 s57,s84,s57
R 38,2,17408
v_fma_f32 v116,v116,s89,s89
M 30,a[108:111],86
D 0,56,0
M 26,a[96:99],82
R 42,2,18432
v_exp_f32 v117,v117
M 26,a[104:107],86
L 3
v_rcp_f32 v114,v114
M 26,a[112:115],90
R 46,2,19456
v_fma_f32 v117,v117,s89,s89
M 26,a[120:123],94
v_rcp_f32 v115,v115
M 30,a[116:119],90
R 50,2,20480
v_mul_f32 v18,v18,v114
M 30,a[124:127],94
L 3
v_rcp_f32 v116,v116
M 30,a[132:135],34
R 54,2,21504
v_mul_f32 v19,v19,v115
M 30,a[140:143],38
D 0,56,1024
v_mul_f32 v20,v20,v116
M 26,a[128:131],34
R 58,2,22528
v_cvt_pk_bf16_f32 v122,v18,v19
M 26,a[136:139],38
L 3
v_rcp_f32 v117,v117
M 26,a[144:147],42
R 62,2,23552
v_mul_f32 v21,v21,v117
M 26,a[152:155],46
v_cvt_pk_bf16_f32 v123,v20,v21
v_fma_f32 v23,-v98,v111,v23
M 30,a[148:151],42
R 66,2,24576
v_fma_f32 v24,-v98,v112,v24
M 30,a[156:159],46
L 3
D 0,56,2048
v_fma_f32 v25,-v98,v113,v25
M 30,a[164:167],50
R 70,2,25600
v_mul_f32 v22,v99,v22
M 30,a[172:175],54
v_mul_f32 v23,v99,v23
v_mul_f32 v24,v99,v24
M 26,a[160:163],50
R 74,2,26624
v_mul_f32 v25,v99,v25
M 26,a[168:171],54
L 3
v_fma_f32 v118,v22,v22,s88
v_fma_f32 v119,v23,v23,s88
M 26,a[176:179],58
R 78,2,27648
v_fma_f32 v120,v24,v24,s88
M 26,a[184:187],62
v_fma_f32 v121,v25,v25,s88
v_mul_f32 v118,v22,v118
M 30,a[180:183],58
R 82,2,28672
v_exp_f32 v118,v118
M 30,a[188:191],62
L 3
D 0,56,3072
M 30,a[196:199],66
R 86,2,29696
v_mul_f32 v119,v23,v119
M 30,a[204:207],70
v_exp_f32 v119,v119
M 26,a[192:195],66
R 90,2,30720
v_mul_f32 v120,v24,v120
M 26,a[200:203],70
L 3
v_mul_f32 v121,v25,v121
v_fma_f32 v118,v118,s89,s89
M 26,a[208:211],74
s_waitcnt vmcnt(5)
)"
R"(R 94,2,31744
v_fma_f32 v119,v119,s89,s89
M 26,a[216:219],78
s_barrier
ds_read_b32 v7,v6 offset:1024
v_exp_f32 v120,v120
M 30,a[212:215],74
R 34,1,0
v_fma_f32 v120,v120,s89,s89
M 30,a[220:223],78
L 4
buffer_store_dword v4,v3,s[36:39],s70 offen offset:0 sc0 sc1
v_exp_f32 v121,v121
M 30,a[228:231],82
s_mov_b32 s70,s69
R 38,1,1024
v_fma_f32 v121,v121,s89,s89
v_rcp_f32 v118,v118
v_rcp_f32 v119,v119
v_rcp_f32 v120,v120
v_rcp_f32 v121,v121
M 30,a[236:239],86
s_mov_b32 s69,s68
v_mul_f32 v22,v22,v118
v_mul_f32 v23,v23,v119
v_mul_f32 v24,v24,v120
v_mul_f32 v25,v25,v121
v_cvt_pk_bf16_f32 v124,v22,v23
v_cvt_pk_bf16_f32 v125,v24,v25
M 26,a[224:227],82
s_add_u32 s68,s68,s63
R 42,1,2048
buffer_store_dwordx4 v[122:125],v15,s[36:39],s66 offen offset:0 sc0 sc1
M 26,a[232:235],86
L 4
L 3
M 26,a[240:243],90
R 46,1,3072
v_cmp_eq_u32 vcc,s53,v7
M 26,a[248:251],94
buffer_store_dwordx4 v[122:125],v16,s[36:39],s66 offen offset:0 sc0 sc1
M 30,a[244:247],90
s_waitcnt vmcnt(7) lgkmcnt(2)
s_cmp_eq_u32 vcc_lo,-1
R 50,1,4096
M 30,a[252:255],94
s_cbranch_scc1 Lg1_F0_%=
Lg1_K0_%=:
Lg1_L0_%=:
v_mfma_f32_16x16x32_bf16 v[22:25],a[4:7],v[34:37],0
s_add_u32 m0,s59,3072
s_mov_b32 s66,s65
R 54,1,5120
M 22,a[12:15],38
buffer_load_dword v8,s[36:39],s58 offen offset:0 sc1 lds
v_mfma_f32_16x16x32_bf16 v[18:21],a[0:3],v[34:37],0
s_add_u32 s65,s65,s62
R 58,1,6144
v_fma_f32 v26,-v102,v106,v26
M 18,a[8:11],38
L 3
v_fma_f32 v27,-v102,v107,v27
v_fma_f32 v28,-v102,v108,v28
M 18,a[16:19],42
R 62,1,7168
v_fma_f32 v29,-v102,v109,v29
M 18,a[24:27],46
v_mul_f32 v26,v103,v26
v_mul_f32 v27,v103,v27
M 22,a[20:23],42
R 66,1,8192
v_mul_f32 v28,v103,v28
M 22,a[28:31],46
L 3
v_mul_f32 v29,v103,v29
v_fma_f32 v114,v26,v26,s88
M 22,a[36:39],50
R 70,1,9216
v_fma_f32 v115,v27,v27,s88
M 22,a[44:47],54
v_fma_f32 v116,v28,v28,s88
v_fma_f32 v117,v29,v29,s88
M 18,a[32:35],50
R 74,1,10240
v_mul_f32 v114,v26,v114
M 18,a[40:43],54
L 3
v_exp_f32 v114,v114
M 18,a[48:51],58
R 78,1,11264
v_mul_f32 v115,v27,v115
M 18,a[56:59],62
v_exp_f32 v115,v115
M 22,a[52:55],58
R 82,1,12288
v_mul_f32 v116,v28,v116
M 22,a[60:63],62
L 3
v_exp_f32 v116,v116
M 22,a[68:71],66
R 86,1,13312
v_mul_f32 v117,v29,v117
M 22,a[76:79],70
buffer_load_dwordx2 v[98:99],v11,s[36:39],s73 offen offset:0 sc1
M 18,a[64:67],66
s_add_u32 s73,s73,s64
R 90,1,14336
v_fma_f32 v114,v114,s89,s89
M 18,a[72:75],70
L 3
v_exp_f32 v117,v117
M 18,a[80:83],74
s_waitcnt vmcnt(1)
R 94,1,15360
v_fma_f32 v115,v115,s89,s89
M 18,a[88:91],78
s_barrier
ds_read_b32 v7,v6 offset:3072
v_fma_f32 v116,v116,s89,s89
M 22,a[84:87],74
R 34,1,16384
v_fma_f32 v117,v117,s89,s89
M 22,a[92:95],78
L 4
buffer_store_dword v4,v3,s[36:39],s70 offen offset:0 sc0 sc1
M 22,a[100:103],82
s_mov_b32 s70,0x7fff0000
R 38,1,17408
v_fma_f32 v30,-v102,v110,v30
M 22,a[108:111],86
v_rcp_f32 v114,v114
M 18,a[96:99],82
R 42,1,18432
v_mul_f32 v26,v26,v114
M 18,a[104:107],86
L 4
L 3
v_cmp_eq_u32 vcc,s53,v7
M 18,a[112:115],90
R 46,1,19456
v_fma_f32 v31,-v102,v111,v31
M 18,a[120:123],94
v_rcp_f32 v115,v115
M 22,a[116:119],90
s_cmp_eq_u32 vcc_lo,-1
R 50,1,20480
v_mul_f32 v27,v27,v115
s_cbranch_scc0 Lg1_X0_%=
M 22,a[124:127],94
L 3
s_add_u32 m0,s57,0
s_add_u32 s56,s55,0
M 22,a[132:135],34
s_add_u32 s58,s58,s61
R 54,1,21504
v_cvt_pk_bf16_f32 v122,v26,v27
M 22,a[140:143],38
D 0,56,0
M 18,a[128:131],34
s_add_u32 s54,s54,s46
R 58,1,22528
v_fma_f32 v32,-v102,v112,v32
M 18,a[136:139],38
L 3
v_rcp_f32 v116,v116
M 18,a[144:147],42
R 62,1,23552
v_mul_f32 v28,v28,v116
M 18,a[152:155],46
D 0,56,1024
M 22,a[148:151],42
R 66,1,24576
v_fma_f32 v33,-v102,v113,v33
M 22,a[156:159],46
L 3
v_rcp_f32 v117,v117
M 22,a[164:167],50
R 70,1,25600
v_mul_f32 v29,v29,v117
M 22,a[172:175],54
D 0,56,2048
M 18,a[160:163],50
R 74,1,26624
v_cvt_pk_bf16_f32 v123,v28,v29
M 18,a[168:171],54
L 3
v_mul_f32 v30,v103,v30
v_mul_f32 v31,v103,v31
M 18,a[176:179],58
R 78,1,27648
v_mul_f32 v32,v103,v32
M 18,a[184:187],62
D 0,56,3072
M 22,a[180:183],58
s_add_u32 m0,s59,0
R 82,1,28672
v_mul_f32 v33,v103,v33
M 22,a[188:191],62
L 3
buffer_load_dword v8,s[36:39],s58 offen offset:0 sc1 lds
M 22,a[196:199],66
s_add_u32 m0,s57,16384
R 86,1,29696
v_fma_f32 v118,v30,v30,s88
M 22,a[204:207],70
s_add_u32 s56,s55,16384
v_fma_f32 v119,v31,v31,s88
v_fma_f32 v120,v32,v32,s88
M 18,a[192:195],66
s_add_u32 s55,s55,s60
R 90,1,30720
v_fma_f32 v121,v33,v33,s88
M 18,a[200:203],70
L 3
D 0,56,0
M 18,a[208:211],74
s_waitcnt vmcnt(1)
R 94,1,31744
v_mul_f32 v118,v30,v118
M 18,a[216:219],78
s_barrier
ds_read_b32 v7,v6 offset:0
v_mul_f32 v119,v31,v119
M 22,a[212:215],74
s_sub_u32 s57,s84,s57
R 34,2,0
v_mul_f32 v120,v32,v120
M 22,a[220:223],78
L 4
buffer_store_dword v4,v3,s[36:39],s70 offen offset:0 sc0 sc1
v_mul_f32 v121,v33,v121
v_exp_f32 v118,v118
v_exp_f32 v119,v119
v_exp_f32 v120,v120
v_exp_f32 v121,v121
M 22,a[228:231],82
s_mov_b32 s70,s69
R 38,2,1024
v_fma_f32 v118,v118,s89,s89
v_fma_f32 v119,v119,s89,s89
v_fma_f32 v120,v120,s89,s89
v_fma_f32 v121,v121,s89,s89
v_rcp_f32 v118,v118
v_rcp_f32 v119,v119
v_rcp_f32 v120,v120
v_rcp_f32 v121,v121
M 22,a[236:239],86
s_mov_b32 s69,s68
v_mul_f32 v30,v30,v118
v_mul_f32 v31,v31,v119
v_mul_f32 v32,v32,v120
v_mul_f32 v33,v33,v121
v_cvt_pk_bf16_f32 v124,v30,v31
v_cvt_pk_bf16_f32 v125,v32,v33
M 18,a[224:227],82
s_add_u32 s68,s68,s63
R 42,2,2048
buffer_store_dwordx4 v[122:125],v15,s[36:39],s67 offen offset:0 sc0 sc1
M 18,a[232:235],86
L 4
L 3
M 18,a[240:243],90
R 46,2,3072
v_cmp_eq_u32 vcc,s53,v7
M 18,a[248:251],94
D 0,56,1024
M 22,a[244:247],90
L 2
s_cmp_eq_u32 vcc_lo,-1
R 50,2,4096
buffer_store_dwordx4 v[122:125],v16,s[36:39],s67 offen offset:0 sc0 sc1
M 22,a[252:255],94
D 0,56,2048
D 0,56,3072
buffer_store_dword v4,v3,s[36:39],s71 offen offset:0 sc0 sc1
buffer_store_dword v4,v3,s[36:39],s71 offen offset:0 sc0 sc1
buffer_store_dword v4,v3,s[36:39],s71 offen offset:0 sc0 sc1
s_cbranch_scc1 Lg1_F1_%=
s_branch Lg1_K1_%=
Lg1_X0_%=:
M 22,a[124:127],94
L 3
s_add_u32 m0,s59,0
v_cvt_pk_bf16_f32 v122,v26,v27
M 22,a[132:135],34
R 54,1,21504
v_fma_f32 v32,-v102,v112,v32
M 22,a[140:143],38
buffer_load_dword v8,s[36:39],s58 offen offset:0 sc1 lds
M 18,a[128:131],34
R 58,1,22528
v_fma_f32 v33,-v102,v113,v33
M 18,a[136:139],38
L 3
v_rcp_f32 v116,v116
M 18,a[144:147],42
R 62,1,23552
v_mul_f32 v28,v28,v116
M 18,a[152:155],46
v_rcp_f32 v117,v117
M 22,a[148:151],42
R 66,1,24576
v_mul_f32 v29,v29,v117
M 22,a[156:159],46
L 3
v_cvt_pk_bf16_f32 v123,v28,v29
v_mul_f32 v30,v103,v30
M 22,a[164:167],50
R 70,1,25600
v_mul_f32 v31,v103,v31
M 22,a[172:175],54
v_mul_f32 v32,v103,v32
v_mul_f32 v33,v103,v33
M 18,a[160:163],50
R 74,1,26624
v_fma_f32 v118,v30,v30,s88
M 18,a[168:171],54
L 3
v_fma_f32 v119,v31,v31,s88
v_fma_f32 v120,v32,v32,s88
M 18,a[176:179],58
R 78,1,27648
v_fma_f32 v121,v33,v33,s88
M 18,a[184:187],62
v_mul_f32 v118,v30,v118
v_mul_f32 v119,v31,v119
M 22,a[180:183],58
R 82,1,28672
v_mul_f32 v120,v32,v120
M 22,a[188:191],62
L 3
v_exp_f32 v118,v118
M 22,a[196:199],66
R 86,1,29696
v_mul_f32 v121,v33,v121
M 22,a[204:207],70
v_exp_f32 v119,v119
M 18,a[192:195],66
R 90,1,30720
v_fma_f32 v118,v118,s89,s89
M 18,a[200:203],70
L 3
v_exp_f32 v120,v120
M 18,a[208:211],74
s_waitcnt vmcnt(0)
R 94,1,31744
v_fma_f32 v119,v119,s89,s89
M 18,a[216:219],78
s_barrier
buffer_store_dword v4,v3,s[36:39],s70 offen offset:0 sc0 sc1
M 22,a[212:215],74
s_mov_b32 s70,s69
ds_read_b32 v7,v6 offset:0
v_fma_f32 v120,v120,s89,s89
M 22,a[220:223],78
L 3
v_exp_f32 v121,v121
M 22,a[228:231],82
s_mov_b32 s69,s68
v_fma_f32 v121,v121,s89,s89
v_rcp_f32 v118,v118
v_rcp_f32 v119,v119
v_rcp_f32 v120,v120
v_rcp_f32 v121,v121
M 22,a[236:239],86
s_add_u32 s68,s68,s63
v_mul_f32 v30,v30,v118
v_mul_f32 v31,v31,v119
v_mul_f32 v32,v32,v120
v_mul_f32 v33,v33,v121
v_cvt_pk_bf16_f32 v124,v30,v31
v_cvt_pk_bf16_f32 v125,v32,v33
M 18,a[224:227],82
buffer_store_dwordx4 v[122:125],v15,s[36:39],s67 offen offset:0 sc0 sc1
M 18,a[232:235],86
L 1
M 18,a[240:243],90
L 0
v_cmp_eq_u32 vcc,s53,v7
M 18,a[248:251],94
buffer_store_dwordx4 v[122:125],v16,s[36:39],s67 offen offset:0 sc0 sc1
M 22,a[244:247],90
s_nop 0
M 22,a[252:255],94
s_mov_b32 s80,1
s_cmp_eq_u32 vcc_lo,-1
s_cbranch_scc0 Lg1_xn0_%=
s_add_u32 m0,s57,0
s_add_u32 s56,s55,0
D 0,56,0
v_fma_f32 v18,-v98,v106,v18
v_fma_f32 v19,-v98,v107,v19
v_fma_f32 v20,-v98,v108,v20
v_fma_f32 v21,-v98,v109,v21
v_mul_f32 v18,v99,v18
v_mul_f32 v19,v99,v19
v_mul_f32 v20,v99,v20
D 0,56,1024
v_mul_f32 v21,v99,v21
v_fma_f32 v114,v18,v18,s88
v_fma_f32 v115,v19,v19,s88
v_fma_f32 v116,v20,v20,s88
v_fma_f32 v117,v21,v21,s88
v_mul_f32 v114,v18,v114
v_mul_f32 v115,v19,v115
D 0,56,2048
v_mul_f32 v116,v20,v116
v_mul_f32 v117,v21,v117
v_exp_f32 v114,v114
v_exp_f32 v115,v115
v_exp_f32 v116,v116
v_exp_f32 v117,v117
v_fma_f32 v114,v114,s89,s89
D 0,56,3072
v_fma_f32 v115,v115,s89,s89
v_fma_f32 v116,v116,s89,s89
v_fma_f32 v117,v117,s89,s89
v_rcp_f32 v114,v114
v_rcp_f32 v115,v115
v_rcp_f32 v116,v116
v_rcp_f32 v117,v117
s_add_u32 s58,s58,s61
s_add_u32 m0,s57,16384
s_add_u32 s56,s55,16384
D 0,56,0
v_mul_f32 v18,v18,v114
v_mul_f32 v19,v19,v115
v_mul_f32 v20,v20,v116
v_mul_f32 v21,v21,v117
v_cvt_pk_bf16_f32 v122,v18,v19
v_cvt_pk_bf16_f32 v123,v20,v21
v_fma_f32 v22,-v98,v110,v22
D 0,56,1024
v_fma_f32 v23,-v98,v111,v23
v_fma_f32 v24,-v98,v112,v24
v_fma_f32 v25,-v98,v113,v25
v_mul_f32 v22,v99,v22
v_mul_f32 v23,v99,v23
v_mul_f32 v24,v99,v24
v_mul_f32 v25,v99,v25
D 0,56,2048
v_fma_f32 v118,v22,v22,s88
v_fma_f32 v119,v23,v23,s88
v_fma_f32 v120,v24,v24,s88
v_fma_f32 v121,v25,v25,s88
v_mul_f32 v118,v22,v118
v_mul_f32 v119,v23,v119
v_mul_f32 v120,v24,v120
D 0,56,3072
v_mul_f32 v121,v25,v121
v_exp_f32 v118,v118
v_exp_f32 v119,v119
v_exp_f32 v120,v120
v_exp_f32 v121,v121
v_fma_f32 v118,v118,s89,s89
v_fma_f32 v119,v119,s89,s89
s_add_u32 s55,s55,s60
s_sub_u32 s57,s84,s57
s_add_u32 s54,s54,s46
v_fma_f32 v120,v120,s89,s89
v_fma_f32 v121,v121,s89,s89
v_rcp_f32 v118,v118
v_rcp_f32 v119,v119
v_rcp_f32 v120,v120
v_rcp_f32 v121,v121
v_mul_f32 v22,v22,v118
v_mul_f32 v23,v23,v119
v_mul_f32 v24,v24,v120
v_mul_f32 v25,v25,v121
v_cvt_pk_bf16_f32 v124,v22,v23
v_cvt_pk_bf16_f32 v125,v24,v25
buffer_store_dwordx4 v[122:125],v15,s[36:39],s66 offen offset:0 sc0 sc1
buffer_store_dwordx4 v[122:125],v16,s[36:39],s66 offen offset:0 sc0 sc1
s_mov_b32 s72,s69
s_mov_b32 s69,0x7fff0000
s_mov_b32 s66,0x7fff0000
s_mov_b32 s76,0x7fff0000
s_branch Lg1_gox_%=
Lg1_xn0_%=:
s_cmp_ge_u32 s54,256
s_cbranch_scc1 Lg1_fin0_%=
s_add_u32 m0,s59,4096
s_nop 0
buffer_load_dword v8,s[36:39],s58 offen offset:0 sc1 lds
buffer_store_dword v4,v3,s[36:39],s71 offen offset:0 sc0 sc1
s_add_u32 m0,s59,5120
s_nop 0
buffer_load_dword v8,s[36:39],s58 offen offset:0 sc1 lds
buffer_store_dword v4,v3,s[36:39],s71 offen offset:0 sc0 sc1
s_mov_b32 s83,0
v_fma_f32 v18,-v98,v106,v18
v_fma_f32 v19,-v98,v107,v19
v_fma_f32 v20,-v98,v108,v20
v_fma_f32 v21,-v98,v109,v21
v_mul_f32 v18,v99,v18
v_mul_f32 v19,v99,v19
v_mul_f32 v20,v99,v20
v_mul_f32 v21,v99,v21
v_fma_f32 v114,v18,v18,s88
v_fma_f32 v115,v19,v19,s88
)"
R"(v_fma_f32 v116,v20,v20,s88
v_fma_f32 v117,v21,v21,s88
v_mul_f32 v114,v18,v114
v_mul_f32 v115,v19,v115
v_mul_f32 v116,v20,v116
v_mul_f32 v117,v21,v117
v_exp_f32 v114,v114
v_exp_f32 v115,v115
v_exp_f32 v116,v116
v_exp_f32 v117,v117
v_fma_f32 v114,v114,s89,s89
v_fma_f32 v115,v115,s89,s89
v_fma_f32 v116,v116,s89,s89
v_fma_f32 v117,v117,s89,s89
v_rcp_f32 v114,v114
v_rcp_f32 v115,v115
v_rcp_f32 v116,v116
v_rcp_f32 v117,v117
v_mul_f32 v18,v18,v114
v_mul_f32 v19,v19,v115
v_mul_f32 v20,v20,v116
v_mul_f32 v21,v21,v117
v_cvt_pk_bf16_f32 v122,v18,v19
v_cvt_pk_bf16_f32 v123,v20,v21
v_fma_f32 v22,-v98,v110,v22
v_fma_f32 v23,-v98,v111,v23
v_fma_f32 v24,-v98,v112,v24
v_fma_f32 v25,-v98,v113,v25
v_mul_f32 v22,v99,v22
v_mul_f32 v23,v99,v23
v_mul_f32 v24,v99,v24
v_mul_f32 v25,v99,v25
v_fma_f32 v118,v22,v22,s88
v_fma_f32 v119,v23,v23,s88
v_fma_f32 v120,v24,v24,s88
v_fma_f32 v121,v25,v25,s88
v_mul_f32 v118,v22,v118
v_mul_f32 v119,v23,v119
v_mul_f32 v120,v24,v120
v_mul_f32 v121,v25,v121
v_exp_f32 v118,v118
v_exp_f32 v119,v119
v_exp_f32 v120,v120
v_exp_f32 v121,v121
v_fma_f32 v118,v118,s89,s89
v_fma_f32 v119,v119,s89,s89
v_fma_f32 v120,v120,s89,s89
v_fma_f32 v121,v121,s89,s89
v_rcp_f32 v118,v118
v_rcp_f32 v119,v119
v_rcp_f32 v120,v120
v_rcp_f32 v121,v121
v_mul_f32 v22,v22,v118
v_mul_f32 v23,v23,v119
v_mul_f32 v24,v24,v120
v_mul_f32 v25,v25,v121
v_cvt_pk_bf16_f32 v124,v22,v23
v_cvt_pk_bf16_f32 v125,v24,v25
buffer_store_dwordx4 v[122:125],v15,s[36:39],s66 offen offset:0 sc0 sc1
buffer_store_dwordx4 v[122:125],v16,s[36:39],s66 offen offset:0 sc0 sc1
s_mov_b32 s72,s69
s_mov_b32 s69,0x7fff0000
s_mov_b32 s66,0x7fff0000
s_mov_b32 s76,0x7fff0000
s_branch Lg1_poll_%=
Lg1_fin0_%=:
v_fma_f32 v18,-v98,v106,v18
v_fma_f32 v19,-v98,v107,v19
v_fma_f32 v20,-v98,v108,v20
v_fma_f32 v21,-v98,v109,v21
v_mul_f32 v18,v99,v18
v_mul_f32 v19,v99,v19
v_mul_f32 v20,v99,v20
v_mul_f32 v21,v99,v21
v_fma_f32 v114,v18,v18,s88
v_fma_f32 v115,v19,v19,s88
v_fma_f32 v116,v20,v20,s88
v_fma_f32 v117,v21,v21,s88
v_mul_f32 v114,v18,v114
v_mul_f32 v115,v19,v115
v_mul_f32 v116,v20,v116
v_mul_f32 v117,v21,v117
v_exp_f32 v114,v114
v_exp_f32 v115,v115
v_exp_f32 v116,v116
v_exp_f32 v117,v117
v_fma_f32 v114,v114,s89,s89
v_fma_f32 v115,v115,s89,s89
v_fma_f32 v116,v116,s89,s89
v_fma_f32 v117,v117,s89,s89
v_rcp_f32 v114,v114
v_rcp_f32 v115,v115
v_rcp_f32 v116,v116
v_rcp_f32 v117,v117
v_mul_f32 v18,v18,v114
v_mul_f32 v19,v19,v115
v_mul_f32 v20,v20,v116
v_mul_f32 v21,v21,v117
v_cvt_pk_bf16_f32 v122,v18,v19
v_cvt_pk_bf16_f32 v123,v20,v21
v_fma_f32 v22,-v98,v110,v22
v_fma_f32 v23,-v98,v111,v23
v_fma_f32 v24,-v98,v112,v24
v_fma_f32 v25,-v98,v113,v25
v_mul_f32 v22,v99,v22
v_mul_f32 v23,v99,v23
v_mul_f32 v24,v99,v24
v_mul_f32 v25,v99,v25
v_fma_f32 v118,v22,v22,s88
v_fma_f32 v119,v23,v23,s88
v_fma_f32 v120,v24,v24,s88
v_fma_f32 v121,v25,v25,s88
v_mul_f32 v118,v22,v118
v_mul_f32 v119,v23,v119
v_mul_f32 v120,v24,v120
v_mul_f32 v121,v25,v121
v_exp_f32 v118,v118
v_exp_f32 v119,v119
v_exp_f32 v120,v120
v_exp_f32 v121,v121
v_fma_f32 v118,v118,s89,s89
v_fma_f32 v119,v119,s89,s89
v_fma_f32 v120,v120,s89,s89
v_fma_f32 v121,v121,s89,s89
v_rcp_f32 v118,v118
v_rcp_f32 v119,v119
v_rcp_f32 v120,v120
v_rcp_f32 v121,v121
v_mul_f32 v22,v22,v118
v_mul_f32 v23,v23,v119
v_mul_f32 v24,v24,v120
v_mul_f32 v25,v25,v121
v_cvt_pk_bf16_f32 v124,v22,v23
v_cvt_pk_bf16_f32 v125,v24,v25
buffer_store_dwordx4 v[122:125],v15,s[36:39],s66 offen offset:0 sc0 sc1
buffer_store_dwordx4 v[122:125],v16,s[36:39],s66 offen offset:0 sc0 sc1
s_mov_b32 s72,s69
s_mov_b32 s69,0x7fff0000
s_mov_b32 s66,0x7fff0000
s_mov_b32 s76,0x7fff0000
s_branch Lg1_done_%=
Lg1_K1_%=:
Lg1_L1_%=:
v_mfma_f32_16x16x32_bf16 v[30:33],a[4:7],v[34:37],0
s_add_u32 m0,s59,3072
s_mov_b32 s67,s65
R 54,2,5120
M 30,a[12:15],38
buffer_load_dword v8,s[36:39],s58 offen offset:0 sc1 lds
v_mfma_f32_16x16x32_bf16 v[26:29],a[0:3],v[34:37],0
s_add_u32 s65,s65,s62
R 58,2,6144
v_fma_f32 v18,-v98,v106,v18
M 26,a[8:11],38
L 3
v_fma_f32 v19,-v98,v107,v19
v_fma_f32 v20,-v98,v108,v20
M 26,a[16:19],42
R 62,2,7168
v_fma_f32 v21,-v98,v109,v21
M 26,a[24:27],46
v_mul_f32 v18,v99,v18
v_mul_f32 v19,v99,v19
M 30,a[20:23],42
R 66,2,8192
v_mul_f32 v20,v99,v20
M 30,a[28:31],46
L 3
v_mul_f32 v21,v99,v21
v_fma_f32 v114,v18,v18,s88
M 30,a[36:39],50
R 70,2,9216
v_fma_f32 v115,v19,v19,s88
M 30,a[44:47],54
v_fma_f32 v116,v20,v20,s88
v_fma_f32 v117,v21,v21,s88
M 26,a[32:35],50
R 74,2,10240
v_mul_f32 v114,v18,v114
M 26,a[40:43],54
L 3
v_exp_f32 v114,v114
M 26,a[48:51],58
R 78,2,11264
v_mul_f32 v115,v19,v115
M 26,a[56:59],62
v_exp_f32 v115,v115
M 30,a[52:55],58
R 82,2,12288
v_mul_f32 v116,v20,v116
M 30,a[60:63],62
L 3
v_exp_f32 v116,v116
M 30,a[68:71],66
R 86,2,13312
v_mul_f32 v117,v21,v117
M 30,a[76:79],70
buffer_load_dwordx2 v[102:103],v11,s[36:39],s73 offen offset:0 sc1
M 26,a[64:67],66
s_add_u32 s73,s73,s64
R 90,2,14336
v_fma_f32 v114,v114,s89,s89
M 26,a[72:75],70
L 3
v_exp_f32 v117,v117
M 26,a[80:83],74
s_waitcnt vmcnt(1)
R 94,2,15360
v_fma_f32 v115,v115,s89,s89
M 26,a[88:91],78
s_barrier
ds_read_b32 v7,v6 offset:3072
v_fma_f32 v116,v116,s89,s89
M 30,a[84:87],74
R 34,2,16384
v_fma_f32 v117,v117,s89,s89
M 30,a[92:95],78
L 4
buffer_store_dword v4,v3,s[36:39],s70 offen offset:0 sc0 sc1
M 30,a[100:103],82
s_mov_b32 s70,0x7fff0000
R 38,2,17408
v_fma_f32 v22,-v98,v110,v22
M 30,a[108:111],86
v_rcp_f32 v114,v114
M 26,a[96:99],82
R 42,2,18432
v_mul_f32 v18,v18,v114
M 26,a[104:107],86
L 4
L 3
v_cmp_eq_u32 vcc,s53,v7
M 26,a[112:115],90
R 46,2,19456
v_fma_f32 v23,-v98,v111,v23
M 26,a[120:123],94
v_rcp_f32 v115,v115
M 30,a[116:119],90
s_cmp_eq_u32 vcc_lo,-1
R 50,2,20480
v_mul_f32 v19,v19,v115
s_cbranch_scc0 Lg1_X1_%=
M 30,a[124:127],94
L 3
s_add_u32 m0,s57,0
s_add_u32 s56,s55,0
M 30,a[132:135],34
s_add_u32 s58,s58,s61
R 54,2,21504
v_cvt_pk_bf16_f32 v122,v18,v19
M 30,a[140:143],38
D 0,56,0
M 26,a[128:131],34
s_add_u32 s54,s54,s46
R 58,2,22528
v_fma_f32 v24,-v98,v112,v24
M 26,a[136:139],38
L 3
v_rcp_f32 v116,v116
M 26,a[144:147],42
R 62,2,23552
v_mul_f32 v20,v20,v116
M 26,a[152:155],46
D 0,56,1024
M 30,a[148:151],42
R 66,2,24576
v_fma_f32 v25,-v98,v113,v25
M 30,a[156:159],46
L 3
v_rcp_f32 v117,v117
M 30,a[164:167],50
R 70,2,25600
v_mul_f32 v21,v21,v117
M 30,a[172:175],54
D 0,56,2048
M 26,a[160:163],50
R 74,2,26624
v_cvt_pk_bf16_f32 v123,v20,v21
M 26,a[168:171],54
L 3
v_mul_f32 v22,v99,v22
v_mul_f32 v23,v99,v23
M 26,a[176:179],58
R 78,2,27648
v_mul_f32 v24,v99,v24
M 26,a[184:187],62
D 0,56,3072
M 30,a[180:183],58
s_add_u32 m0,s59,1024
R 82,2,28672
v_mul_f32 v25,v99,v25
M 30,a[188:191],62
L 3
buffer_load_dword v8,s[36:39],s58 offen offset:0 sc1 lds
M 30,a[196:199],66
s_add_u32 m0,s57,16384
R 86,2,29696
v_fma_f32 v118,v22,v22,s88
M 30,a[204:207],70
s_add_u32 s56,s55,16384
v_fma_f32 v119,v23,v23,s88
v_fma_f32 v120,v24,v24,s88
M 26,a[192:195],66
s_add_u32 s55,s55,s60
R 90,2,30720
v_fma_f32 v121,v25,v25,s88
M 26,a[200:203],70
L 3
D 0,56,0
M 26,a[208:211],74
s_waitcnt vmcnt(1)
R 94,2,31744
v_mul_f32 v118,v22,v118
M 26,a[216:219],78
s_barrier
ds_read_b32 v7,v6 offset:1024
v_mul_f32 v119,v23,v119
M 30,a[212:215],74
s_sub_u32 s57,s84,s57
R 34,1,0
v_mul_f32 v120,v24,v120
M 30,a[220:223],78
L 4
buffer_store_dword v4,v3,s[36:39],s70 offen offset:0 sc0 sc1
v_mul_f32 v121,v25,v121
v_exp_f32 v118,v118
v_exp_f32 v119,v119
v_exp_f32 v120,v120
v_exp_f32 v121,v121
M 30,a[228:231],82
s_mov_b32 s70,s69
R 38,1,1024
v_fma_f32 v118,v118,s89,s89
v_fma_f32 v119,v119,s89,s89
v_fma_f32 v120,v120,s89,s89
v_fma_f32 v121,v121,s89,s89
v_rcp_f32 v118,v118
v_rcp_f32 v119,v119
v_rcp_f32 v120,v120
v_rcp_f32 v121,v121
M 30,a[236:239],86
s_mov_b32 s69,s68
v_mul_f32 v22,v22,v118
v_mul_f32 v23,v23,v119
v_mul_f32 v24,v24,v120
v_mul_f32 v25,v25,v121
v_cvt_pk_bf16_f32 v124,v22,v23
v_cvt_pk_bf16_f32 v125,v24,v25
M 26,a[224:227],82
s_add_u32 s68,s68,s63
R 42,1,2048
buffer_store_dwordx4 v[122:125],v15,s[36:39],s66 offen offset:0 sc0 sc1
M 26,a[232:235],86
L 4
L 3
M 26,a[240:243],90
R 46,1,3072
v_cmp_eq_u32 vcc,s53,v7
M 26,a[248:251],94
D 0,56,1024
M 30,a[244:247],90
L 2
s_cmp_eq_u32 vcc_lo,-1
R 50,1,4096
buffer_store_dwordx4 v[122:125],v16,s[36:39],s66 offen offset:0 sc0 sc1
M 30,a[252:255],94
D 0,56,2048
D 0,56,3072
buffer_store_dword v4,v3,s[36:39],s71 offen offset:0 sc0 sc1
buffer_store_dword v4,v3,s[36:39],s71 offen offset:0 sc0 sc1
buffer_store_dword v4,v3,s[36:39],s71 offen offset:0 sc0 sc1
s_cbranch_scc1 Lg1_F0_%=
s_branch Lg1_K0_%=
Lg1_X1_%=:
M 30,a[124:127],94
L 3
s_add_u32 m0,s59,1024
v_cvt_pk_bf16_f32 v122,v18,v19
M 30,a[132:135],34
R 54,2,21504
v_fma_f32 v24,-v98,v112,v24
M 30,a[140:143],38
buffer_load_dword v8,s[36:39],s58 offen offset:0 sc1 lds
M 26,a[128:131],34
R 58,2,22528
v_fma_f32 v25,-v98,v113,v25
M 26,a[136:139],38
L 3
v_rcp_f32 v116,v116
M 26,a[144:147],42
R 62,2,23552
v_mul_f32 v20,v20,v116
M 26,a[152:155],46
v_rcp_f32 v117,v117
M 30,a[148:151],42
R 66,2,24576
v_mul_f32 v21,v21,v117
M 30,a[156:159],46
L 3
v_cvt_pk_bf16_f32 v123,v20,v21
v_mul_f32 v22,v99,v22
M 30,a[164:167],50
R 70,2,25600
v_mul_f32 v23,v99,v23
M 30,a[172:175],54
v_mul_f32 v24,v99,v24
v_mul_f32 v25,v99,v25
M 26,a[160:163],50
R 74,2,26624
v_fma_f32 v118,v22,v22,s88
M 26,a[168:171],54
L 3
v_fma_f32 v119,v23,v23,s88
v_fma_f32 v120,v24,v24,s88
M 26,a[176:179],58
R 78,2,27648
v_fma_f32 v121,v25,v25,s88
M 26,a[184:187],62
v_mul_f32 v118,v22,v118
v_mul_f32 v119,v23,v119
M 30,a[180:183],58
R 82,2,28672
v_mul_f32 v120,v24,v120
M 30,a[188:191],62
L 3
v_exp_f32 v118,v118
M 30,a[196:199],66
R 86,2,29696
v_mul_f32 v121,v25,v121
M 30,a[204:207],70
v_exp_f32 v119,v119
M 26,a[192:195],66
R 90,2,30720
v_fma_f32 v118,v118,s89,s89
M 26,a[200:203],70
L 3
v_exp_f32 v120,v120
M 26,a[208:211],74
s_waitcnt vmcnt(0)
R 94,2,31744
v_fma_f32 v119,v119,s89,s89
M 26,a[216:219],78
s_barrier
buffer_store_dword v4,v3,s[36:39],s70 offen offset:0 sc0 sc1
M 30,a[212:215],74
s_mov_b32 s70,s69
ds_read_b32 v7,v6 offset:1024
v_fma_f32 v120,v120,s89,s89
M 30,a[220:223],78
L 3
v_exp_f32 v121,v121
M 30,a[228:231],82
s_mov_b32 s69,s68
v_fma_f32 v121,v121,s89,s89
v_rcp_f32 v118,v118
v_rcp_f32 v119,v119
v_rcp_f32 v120,v120
v_rcp_f32 v121,v121
M 30,a[236:239],86
s_add_u32 s68,s68,s63
v_mul_f32 v22,v22,v118
v_mul_f32 v23,v23,v119
v_mul_f32 v24,v24,v120
v_mul_f32 v25,v25,v121
v_cvt_pk_bf16_f32 v124,v22,v23
v_cvt_pk_bf16_f32 v125,v24,v25
M 26,a[224:227],82
buffer_store_dwordx4 v[122:125],v15,s[36:39],s66 offen offset:0 sc0 sc1
M 26,a[232:235],86
L 1
M 26,a[240:243],90
L 0
v_cmp_eq_u32 vcc,s53,v7
M 26,a[248:251],94
buffer_store_dwordx4 v[122:125],v16,s[36:39],s66 offen offset:0 sc0 sc1
M 30,a[244:247],90
s_nop 0
M 30,a[252:255],94
s_mov_b32 s80,0
s_cmp_eq_u32 vcc_lo,-1
s_cbranch_scc0 Lg1_xn1_%=
s_add_u32 m0,s57,0
s_add_u32 s56,s55,0
D 0,56,0
v_fma_f32 v26,-v102,v106,v26
v_fma_f32 v27,-v102,v107,v27
v_fma_f32 v28,-v102,v108,v28
v_fma_f32 v29,-v102,v109,v29
v_mul_f32 v26,v103,v26
v_mul_f32 v27,v103,v27
v_mul_f32 v28,v103,v28
D 0,56,1024
v_mul_f32 v29,v103,v29
v_fma_f32 v114,v26,v26,s88
v_fma_f32 v115,v27,v27,s88
v_fma_f32 v116,v28,v28,s88
v_fma_f32 v117,v29,v29,s88
v_mul_f32 v114,v26,v114
v_mul_f32 v115,v27,v115
D 0,56,2048
)"
R"(v_mul_f32 v116,v28,v116
v_mul_f32 v117,v29,v117
v_exp_f32 v114,v114
v_exp_f32 v115,v115
v_exp_f32 v116,v116
v_exp_f32 v117,v117
v_fma_f32 v114,v114,s89,s89
D 0,56,3072
v_fma_f32 v115,v115,s89,s89
v_fma_f32 v116,v116,s89,s89
v_fma_f32 v117,v117,s89,s89
v_rcp_f32 v114,v114
v_rcp_f32 v115,v115
v_rcp_f32 v116,v116
v_rcp_f32 v117,v117
s_add_u32 s58,s58,s61
s_add_u32 m0,s57,16384
s_add_u32 s56,s55,16384
D 0,56,0
v_mul_f32 v26,v26,v114
v_mul_f32 v27,v27,v115
v_mul_f32 v28,v28,v116
v_mul_f32 v29,v29,v117
v_cvt_pk_bf16_f32 v122,v26,v27
v_cvt_pk_bf16_f32 v123,v28,v29
v_fma_f32 v30,-v102,v110,v30
D 0,56,1024
v_fma_f32 v31,-v102,v111,v31
v_fma_f32 v32,-v102,v112,v32
v_fma_f32 v33,-v102,v113,v33
v_mul_f32 v30,v103,v30
v_mul_f32 v31,v103,v31
v_mul_f32 v32,v103,v32
v_mul_f32 v33,v103,v33
D 0,56,2048
v_fma_f32 v118,v30,v30,s88
v_fma_f32 v119,v31,v31,s88
v_fma_f32 v120,v32,v32,s88
v_fma_f32 v121,v33,v33,s88
v_mul_f32 v118,v30,v118
v_mul_f32 v119,v31,v119
v_mul_f32 v120,v32,v120
D 0,56,3072
v_mul_f32 v121,v33,v121
v_exp_f32 v118,v118
v_exp_f32 v119,v119
v_exp_f32 v120,v120
v_exp_f32 v121,v121
v_fma_f32 v118,v118,s89,s89
v_fma_f32 v119,v119,s89,s89
s_add_u32 s55,s55,s60
s_sub_u32 s57,s84,s57
s_add_u32 s54,s54,s46
v_fma_f32 v120,v120,s89,s89
v_fma_f32 v121,v121,s89,s89
v_rcp_f32 v118,v118
v_rcp_f32 v119,v119
v_rcp_f32 v120,v120
v_rcp_f32 v121,v121
v_mul_f32 v30,v30,v118
v_mul_f32 v31,v31,v119
v_mul_f32 v32,v32,v120
v_mul_f32 v33,v33,v121
v_cvt_pk_bf16_f32 v124,v30,v31
v_cvt_pk_bf16_f32 v125,v32,v33
buffer_store_dwordx4 v[122:125],v15,s[36:39],s67 offen offset:0 sc0 sc1
buffer_store_dwordx4 v[122:125],v16,s[36:39],s67 offen offset:0 sc0 sc1
s_mov_b32 s72,s69
s_mov_b32 s69,0x7fff0000
s_mov_b32 s67,0x7fff0000
s_mov_b32 s77,0x7fff0000
s_branch Lg1_gox_%=
Lg1_xn1_%=:
s_cmp_ge_u32 s54,256
s_cbranch_scc1 Lg1_fin1_%=
s_add_u32 m0,s59,4096
s_nop 0
buffer_load_dword v8,s[36:39],s58 offen offset:0 sc1 lds
buffer_store_dword v4,v3,s[36:39],s71 offen offset:0 sc0 sc1
s_add_u32 m0,s59,5120
s_nop 0
buffer_load_dword v8,s[36:39],s58 offen offset:0 sc1 lds
buffer_store_dword v4,v3,s[36:39],s71 offen offset:0 sc0 sc1
s_mov_b32 s83,0
v_fma_f32 v26,-v102,v106,v26
v_fma_f32 v27,-v102,v107,v27
v_fma_f32 v28,-v102,v108,v28
v_fma_f32 v29,-v102,v109,v29
v_mul_f32 v26,v103,v26
v_mul_f32 v27,v103,v27
v_mul_f32 v28,v103,v28
v_mul_f32 v29,v103,v29
v_fma_f32 v114,v26,v26,s88
v_fma_f32 v115,v27,v27,s88
v_fma_f32 v116,v28,v28,s88
v_fma_f32 v117,v29,v29,s88
v_mul_f32 v114,v26,v114
v_mul_f32 v115,v27,v115
v_mul_f32 v116,v28,v116
v_mul_f32 v117,v29,v117
v_exp_f32 v114,v114
v_exp_f32 v115,v115
v_exp_f32 v116,v116
v_exp_f32 v117,v117
v_fma_f32 v114,v114,s89,s89
v_fma_f32 v115,v115,s89,s89
v_fma_f32 v116,v116,s89,s89
v_fma_f32 v117,v117,s89,s89
v_rcp_f32 v114,v114
v_rcp_f32 v115,v115
v_rcp_f32 v116,v116
v_rcp_f32 v117,v117
v_mul_f32 v26,v26,v114
v_mul_f32 v27,v27,v115
v_mul_f32 v28,v28,v116
v_mul_f32 v29,v29,v117
v_cvt_pk_bf16_f32 v122,v26,v27
v_cvt_pk_bf16_f32 v123,v28,v29
v_fma_f32 v30,-v102,v110,v30
v_fma_f32 v31,-v102,v111,v31
v_fma_f32 v32,-v102,v112,v32
v_fma_f32 v33,-v102,v113,v33
v_mul_f32 v30,v103,v30
v_mul_f32 v31,v103,v31
v_mul_f32 v32,v103,v32
v_mul_f32 v33,v103,v33
v_fma_f32 v118,v30,v30,s88
v_fma_f32 v119,v31,v31,s88
v_fma_f32 v120,v32,v32,s88
v_fma_f32 v121,v33,v33,s88
v_mul_f32 v118,v30,v118
v_mul_f32 v119,v31,v119
v_mul_f32 v120,v32,v120
v_mul_f32 v121,v33,v121
v_exp_f32 v118,v118
v_exp_f32 v119,v119
v_exp_f32 v120,v120
v_exp_f32 v121,v121
v_fma_f32 v118,v118,s89,s89
v_fma_f32 v119,v119,s89,s89
v_fma_f32 v120,v120,s89,s89
v_fma_f32 v121,v121,s89,s89
v_rcp_f32 v118,v118
v_rcp_f32 v119,v119
v_rcp_f32 v120,v120
v_rcp_f32 v121,v121
v_mul_f32 v30,v30,v118
v_mul_f32 v31,v31,v119
v_mul_f32 v32,v32,v120
v_mul_f32 v33,v33,v121
v_cvt_pk_bf16_f32 v124,v30,v31
v_cvt_pk_bf16_f32 v125,v32,v33
buffer_store_dwordx4 v[122:125],v15,s[36:39],s67 offen offset:0 sc0 sc1
buffer_store_dwordx4 v[122:125],v16,s[36:39],s67 offen offset:0 sc0 sc1
s_mov_b32 s72,s69
s_mov_b32 s69,0x7fff0000
s_mov_b32 s67,0x7fff0000
s_mov_b32 s77,0x7fff0000
s_branch Lg1_poll_%=
Lg1_fin1_%=:
v_fma_f32 v26,-v102,v106,v26
v_fma_f32 v27,-v102,v107,v27
v_fma_f32 v28,-v102,v108,v28
v_fma_f32 v29,-v102,v109,v29
v_mul_f32 v26,v103,v26
v_mul_f32 v27,v103,v27
v_mul_f32 v28,v103,v28
v_mul_f32 v29,v103,v29
v_fma_f32 v114,v26,v26,s88
v_fma_f32 v115,v27,v27,s88
v_fma_f32 v116,v28,v28,s88
v_fma_f32 v117,v29,v29,s88
v_mul_f32 v114,v26,v114
v_mul_f32 v115,v27,v115
v_mul_f32 v116,v28,v116
v_mul_f32 v117,v29,v117
v_exp_f32 v114,v114
v_exp_f32 v115,v115
v_exp_f32 v116,v116
v_exp_f32 v117,v117
v_fma_f32 v114,v114,s89,s89
v_fma_f32 v115,v115,s89,s89
v_fma_f32 v116,v116,s89,s89
v_fma_f32 v117,v117,s89,s89
v_rcp_f32 v114,v114
v_rcp_f32 v115,v115
v_rcp_f32 v116,v116
v_rcp_f32 v117,v117
v_mul_f32 v26,v26,v114
v_mul_f32 v27,v27,v115
v_mul_f32 v28,v28,v116
v_mul_f32 v29,v29,v117
v_cvt_pk_bf16_f32 v122,v26,v27
v_cvt_pk_bf16_f32 v123,v28,v29
v_fma_f32 v30,-v102,v110,v30
v_fma_f32 v31,-v102,v111,v31
v_fma_f32 v32,-v102,v112,v32
v_fma_f32 v33,-v102,v113,v33
v_mul_f32 v30,v103,v30
v_mul_f32 v31,v103,v31
v_mul_f32 v32,v103,v32
v_mul_f32 v33,v103,v33
v_fma_f32 v118,v30,v30,s88
v_fma_f32 v119,v31,v31,s88
v_fma_f32 v120,v32,v32,s88
v_fma_f32 v121,v33,v33,s88
v_mul_f32 v118,v30,v118
v_mul_f32 v119,v31,v119
v_mul_f32 v120,v32,v120
v_mul_f32 v121,v33,v121
v_exp_f32 v118,v118
v_exp_f32 v119,v119
v_exp_f32 v120,v120
v_exp_f32 v121,v121
v_fma_f32 v118,v118,s89,s89
v_fma_f32 v119,v119,s89,s89
v_fma_f32 v120,v120,s89,s89
v_fma_f32 v121,v121,s89,s89
v_rcp_f32 v118,v118
v_rcp_f32 v119,v119
v_rcp_f32 v120,v120
v_rcp_f32 v121,v121
v_mul_f32 v30,v30,v118
v_mul_f32 v31,v31,v119
v_mul_f32 v32,v32,v120
v_mul_f32 v33,v33,v121
v_cvt_pk_bf16_f32 v124,v30,v31
v_cvt_pk_bf16_f32 v125,v32,v33
buffer_store_dwordx4 v[122:125],v15,s[36:39],s67 offen offset:0 sc0 sc1
buffer_store_dwordx4 v[122:125],v16,s[36:39],s67 offen offset:0 sc0 sc1
s_mov_b32 s72,s69
s_mov_b32 s69,0x7fff0000
s_mov_b32 s67,0x7fff0000
s_mov_b32 s77,0x7fff0000
s_branch Lg1_done_%=
Lg1_done_%=:
s_waitcnt vmcnt(2)
s_barrier
buffer_store_dword v4,v3,s[36:39],s70 offen offset:0 sc0 sc1
s_waitcnt vmcnt(1)
s_barrier
buffer_store_dword v4,v3,s[36:39],s72 offen offset:0 sc0 sc1
s_waitcnt vmcnt(0) lgkmcnt(0)
s_barrier
s_branch Lg2_end_%=
Lg2_begin_%=:
Lcode_g2_b:
s_mov_b64 s[36:37],%1
s_mov_b32 s38,111243392
s_mov_b32 s39,0x00020000
s_mov_b32 s44,%2
s_mov_b32 s45,%3
s_mov_b32 s46,%4
s_mov_b32 s47,%5
s_mov_b32 s48,%6
s_mov_b32 s49,%7
s_mov_b32 s50,%8
s_mov_b32 s51,%9
s_mov_b32 s53,%10
v_mov_b32 v0,%11
v_mov_b32 v3,%12
v_mov_b32 v5,%13
v_mov_b32 v6,%14
v_mov_b32 v18,%15
v_mov_b32 v19,%16
v_mov_b32 v20,%17
v_mov_b32 v21,%18
v_mov_b32 v9,%19
v_mov_b32 v10,%20
v_mov_b32 v11,%21
v_mov_b32 v12,%22
v_mov_b32 v13,%23
v_mov_b32 v14,%24
v_mov_b32 v15,%25
v_mov_b32 v16,%26
s_mov_b64 s[40:41],%27
s_mov_b32 s42,2097152
s_mov_b32 s43,0x00020000
s_lshl_b32 s57,s44,11
s_cmp_ge_u32 s45,8
s_cbranch_scc1 Lg2_dly_%=
s_sleep 16
s_sleep 16
s_sleep 16
s_sleep 16
Lg2_dly_%=:
s_add_u32 s55,s50,0
s_add_u32 m0,s57,0
s_nop 0
buffer_load_dwordx4 v18,s[40:43],s55 offen offset:0  lds
s_add_u32 m0,s57,1024
s_nop 0
buffer_load_dwordx4 v19,s[40:43],s55 offen offset:0  lds
s_add_u32 m0,s57,8064
s_nop 0
buffer_load_dwordx4 v18,s[40:43],s55 offen offset:128  lds
s_add_u32 m0,s57,9088
s_nop 0
buffer_load_dwordx4 v19,s[40:43],s55 offen offset:128  lds
s_add_u32 s55,s50,131072
s_add_u32 m0,s57,16384
s_nop 0
buffer_load_dwordx4 v18,s[40:43],s55 offen offset:0  lds
s_add_u32 m0,s57,17408
s_nop 0
buffer_load_dwordx4 v19,s[40:43],s55 offen offset:0  lds
s_add_u32 m0,s57,24448
s_nop 0
buffer_load_dwordx4 v18,s[40:43],s55 offen offset:128  lds
s_add_u32 m0,s57,25472
s_nop 0
buffer_load_dwordx4 v19,s[40:43],s55 offen offset:128  lds
s_add_u32 s55,s50,262144
s_add_u32 m0,s57,32768
s_nop 0
buffer_load_dwordx4 v18,s[40:43],s55 offen offset:0  lds
s_add_u32 m0,s57,33792
s_nop 0
buffer_load_dwordx4 v19,s[40:43],s55 offen offset:0  lds
s_add_u32 m0,s57,40832
s_nop 0
buffer_load_dwordx4 v18,s[40:43],s55 offen offset:128  lds
s_add_u32 m0,s57,41856
s_nop 0
buffer_load_dwordx4 v19,s[40:43],s55 offen offset:128  lds
s_add_u32 s55,s50,393216
s_add_u32 m0,s57,49152
s_nop 0
buffer_load_dwordx4 v18,s[40:43],s55 offen offset:0  lds
s_add_u32 m0,s57,50176
s_nop 0
buffer_load_dwordx4 v19,s[40:43],s55 offen offset:0  lds
s_add_u32 m0,s57,57216
s_nop 0
buffer_load_dwordx4 v18,s[40:43],s55 offen offset:128  lds
s_add_u32 m0,s57,58240
s_nop 0
buffer_load_dwordx4 v19,s[40:43],s55 offen offset:128  lds
s_add_u32 s55,s50,524288
s_add_u32 m0,s57,65536
s_nop 0
buffer_load_dwordx4 v18,s[40:43],s55 offen offset:0  lds
s_add_u32 m0,s57,66560
s_nop 0
buffer_load_dwordx4 v19,s[40:43],s55 offen offset:0  lds
s_add_u32 m0,s57,73600
s_nop 0
buffer_load_dwordx4 v18,s[40:43],s55 offen offset:128  lds
s_add_u32 m0,s57,74624
s_nop 0
buffer_load_dwordx4 v19,s[40:43],s55 offen offset:128  lds
s_add_u32 s55,s50,655360
s_add_u32 m0,s57,81920
s_nop 0
buffer_load_dwordx4 v18,s[40:43],s55 offen offset:0  lds
s_add_u32 m0,s57,82944
s_nop 0
buffer_load_dwordx4 v19,s[40:43],s55 offen offset:0  lds
s_add_u32 m0,s57,89984
s_nop 0
buffer_load_dwordx4 v18,s[40:43],s55 offen offset:128  lds
s_add_u32 m0,s57,91008
s_nop 0
buffer_load_dwordx4 v19,s[40:43],s55 offen offset:128  lds
s_waitcnt vmcnt(20) lgkmcnt(0)
s_barrier
s_add_u32 s55,s50,786432
s_add_u32 m0,s57,98304
s_nop 0
buffer_load_dwordx4 v18,s[40:43],s55 offen offset:0  lds
s_add_u32 m0,s57,99328
s_nop 0
buffer_load_dwordx4 v19,s[40:43],s55 offen offset:0  lds
s_add_u32 m0,s57,106368
s_nop 0
buffer_load_dwordx4 v18,s[40:43],s55 offen offset:128  lds
s_add_u32 m0,s57,107392
s_nop 0
buffer_load_dwordx4 v19,s[40:43],s55 offen offset:128  lds
v_add_u32 v26,0,v20
v_add_u32 v27,0,v21
T a[0:1],26,0
T a[2:3],26,512
T a[4:5],27,0
T a[6:7],27,512
T a[8:9],26,4096
T a[10:11],26,4608
T a[12:13],27,4096
T a[14:15],27,4608
s_waitcnt vmcnt(20) lgkmcnt(0)
s_barrier
s_add_u32 s55,s50,917504
s_add_u32 m0,s57,0
s_nop 0
buffer_load_dwordx4 v18,s[40:43],s55 offen offset:0  lds
s_add_u32 m0,s57,1024
s_nop 0
buffer_load_dwordx4 v19,s[40:43],s55 offen offset:0  lds
s_add_u32 m0,s57,8064
s_nop 0
buffer_load_dwordx4 v18,s[40:43],s55 offen offset:128  lds
s_add_u32 m0,s57,9088
s_nop 0
buffer_load_dwordx4 v19,s[40:43],s55 offen offset:128  lds
v_add_u32 v26,16384,v20
v_add_u32 v27,16384,v21
T a[16:17],26,0
T a[18:19],26,512
T a[20:21],27,0
T a[22:23],27,512
T a[24:25],26,4096
T a[26:27],26,4608
T a[28:29],27,4096
T a[30:31],27,4608
s_waitcnt vmcnt(20) lgkmcnt(0)
s_barrier
s_add_u32 s55,s50,1048576
s_add_u32 m0,s57,16384
s_nop 0
buffer_load_dwordx4 v18,s[40:43],s55 offen offset:0  lds
s_add_u32 m0,s57,17408
s_nop 0
buffer_load_dwordx4 v19,s[40:43],s55 offen offset:0  lds
s_add_u32 m0,s57,24448
s_nop 0
buffer_load_dwordx4 v18,s[40:43],s55 offen offset:128  lds
s_add_u32 m0,s57,25472
s_nop 0
buffer_load_dwordx4 v19,s[40:43],s55 offen offset:128  lds
v_add_u32 v26,32768,v20
v_add_u32 v27,32768,v21
T a[32:33],26,0
T a[34:35],26,512
T a[36:37],27,0
T a[38:39],27,512
T a[40:41],26,4096
T a[42:43],26,4608
T a[44:45],27,4096
T a[46:47],27,4608
s_waitcnt vmcnt(20) lgkmcnt(0)
s_barrier
s_add_u32 s55,s50,1179648
s_add_u32 m0,s57,32768
s_nop 0
buffer_load_dwordx4 v18,s[40:43],s55 offen offset:0  lds
s_add_u32 m0,s57,33792
s_nop 0
buffer_load_dwordx4 v19,s[40:43],s55 offen offset:0  lds
s_add_u32 m0,s57,40832
s_nop 0
buffer_load_dwordx4 v18,s[40:43],s55 offen offset:128  lds
s_add_u32 m0,s57,41856
s_nop 0
buffer_load_dwordx4 v19,s[40:43],s55 offen offset:128  lds
v_add_u32 v26,49152,v20
v_add_u32 v27,49152,v21
T a[48:49],26,0
T a[50:51],26,512
T a[52:53],27,0
T a[54:55],27,512
T a[56:57],26,4096
T a[58:59],26,4608
T a[60:61],27,4096
T a[62:63],27,4608
s_waitcnt vmcnt(20) lgkmcnt(0)
s_barrier
s_add_u32 s55,s50,1310720
s_add_u32 m0,s57,49152
s_nop 0
buffer_load_dwordx4 v18,s[40:43],s55 offen offset:0  lds
s_add_u32 m0,s57,50176
s_nop 0
buffer_load_dwordx4 v19,s[40:43],s55 offen offset:0  lds
s_add_u32 m0,s57,57216
s_nop 0
buffer_load_dwordx4 v18,s[40:43],s55 offen offset:128  lds
s_add_u32 m0,s57,58240
s_nop 0
buffer_load_dwordx4 v19,s[40:43],s55 offen offset:128  lds
v_add_u32 v26,65536,v20
v_add_u32 v27,65536,v21
T a[64:65],26,0
T a[66:67],26,512
T a[68:69],27,0
T a[70:71],27,512
T a[72:73],26,4096
T a[74:75],26,4608
T a[76:77],27,4096
T a[78:79],27,4608
s_waitcnt vmcnt(20) lgkmcnt(0)
s_barrier
s_add_u32 s55,s50,1441792
s_add_u32 m0,s57,65536
s_nop 0
buffer_load_dwordx4 v18,s[40:43],s55 offen offset:0  lds
s_add_u32 m0,s57,66560
s_nop 0
buffer_load_dwordx4 v19,s[40:43],s55 offen offset:0  lds
s_add_u32 m0,s57,73600
s_nop 0
)"
R"(buffer_load_dwordx4 v18,s[40:43],s55 offen offset:128  lds
s_add_u32 m0,s57,74624
s_nop 0
buffer_load_dwordx4 v19,s[40:43],s55 offen offset:128  lds
v_add_u32 v26,81920,v20
v_add_u32 v27,81920,v21
T a[80:81],26,0
T a[82:83],26,512
T a[84:85],27,0
T a[86:87],27,512
T a[88:89],26,4096
T a[90:91],26,4608
T a[92:93],27,4096
T a[94:95],27,4608
s_waitcnt vmcnt(20) lgkmcnt(0)
s_barrier
s_add_u32 s55,s50,1572864
s_add_u32 m0,s57,81920
s_nop 0
buffer_load_dwordx4 v18,s[40:43],s55 offen offset:0  lds
s_add_u32 m0,s57,82944
s_nop 0
buffer_load_dwordx4 v19,s[40:43],s55 offen offset:0  lds
s_add_u32 m0,s57,89984
s_nop 0
buffer_load_dwordx4 v18,s[40:43],s55 offen offset:128  lds
s_add_u32 m0,s57,91008
s_nop 0
buffer_load_dwordx4 v19,s[40:43],s55 offen offset:128  lds
v_add_u32 v26,98304,v20
v_add_u32 v27,98304,v21
T a[96:97],26,0
T a[98:99],26,512
T a[100:101],27,0
T a[102:103],27,512
T a[104:105],26,4096
T a[106:107],26,4608
T a[108:109],27,4096
T a[110:111],27,4608
s_waitcnt vmcnt(20) lgkmcnt(0)
s_barrier
s_add_u32 s55,s50,1703936
s_add_u32 m0,s57,98304
s_nop 0
buffer_load_dwordx4 v18,s[40:43],s55 offen offset:0  lds
s_add_u32 m0,s57,99328
s_nop 0
buffer_load_dwordx4 v19,s[40:43],s55 offen offset:0  lds
s_add_u32 m0,s57,106368
s_nop 0
buffer_load_dwordx4 v18,s[40:43],s55 offen offset:128  lds
s_add_u32 m0,s57,107392
s_nop 0
buffer_load_dwordx4 v19,s[40:43],s55 offen offset:128  lds
v_add_u32 v26,0,v20
v_add_u32 v27,0,v21
T a[112:113],26,0
T a[114:115],26,512
T a[116:117],27,0
T a[118:119],27,512
T a[120:121],26,4096
T a[122:123],26,4608
T a[124:125],27,4096
T a[126:127],27,4608
s_waitcnt vmcnt(20) lgkmcnt(0)
s_barrier
s_add_u32 s55,s50,1835008
s_add_u32 m0,s57,0
s_nop 0
buffer_load_dwordx4 v18,s[40:43],s55 offen offset:0  lds
s_add_u32 m0,s57,1024
s_nop 0
buffer_load_dwordx4 v19,s[40:43],s55 offen offset:0  lds
s_add_u32 m0,s57,8064
s_nop 0
buffer_load_dwordx4 v18,s[40:43],s55 offen offset:128  lds
s_add_u32 m0,s57,9088
s_nop 0
buffer_load_dwordx4 v19,s[40:43],s55 offen offset:128  lds
v_add_u32 v26,16384,v20
v_add_u32 v27,16384,v21
T a[128:129],26,0
T a[130:131],26,512
T a[132:133],27,0
T a[134:135],27,512
T a[136:137],26,4096
T a[138:139],26,4608
T a[140:141],27,4096
T a[142:143],27,4608
s_waitcnt vmcnt(20) lgkmcnt(0)
s_barrier
s_add_u32 s55,s50,1966080
s_add_u32 m0,s57,16384
s_nop 0
buffer_load_dwordx4 v18,s[40:43],s55 offen offset:0  lds
s_add_u32 m0,s57,17408
s_nop 0
buffer_load_dwordx4 v19,s[40:43],s55 offen offset:0  lds
s_add_u32 m0,s57,24448
s_nop 0
buffer_load_dwordx4 v18,s[40:43],s55 offen offset:128  lds
s_add_u32 m0,s57,25472
s_nop 0
buffer_load_dwordx4 v19,s[40:43],s55 offen offset:128  lds
v_add_u32 v26,32768,v20
v_add_u32 v27,32768,v21
T a[144:145],26,0
T a[146:147],26,512
T a[148:149],27,0
T a[150:151],27,512
T a[152:153],26,4096
T a[154:155],26,4608
T a[156:157],27,4096
T a[158:159],27,4608
s_waitcnt vmcnt(20) lgkmcnt(0)
s_barrier
v_add_u32 v26,49152,v20
v_add_u32 v27,49152,v21
T a[160:161],26,0
T a[162:163],26,512
T a[164:165],27,0
T a[166:167],27,512
T a[168:169],26,4096
T a[170:171],26,4608
T a[172:173],27,4096
T a[174:175],27,4608
s_waitcnt vmcnt(16) lgkmcnt(0)
s_barrier
v_add_u32 v26,65536,v20
v_add_u32 v27,65536,v21
T a[176:177],26,0
T a[178:179],26,512
T a[180:181],27,0
T a[182:183],27,512
T a[184:185],26,4096
T a[186:187],26,4608
T a[188:189],27,4096
T a[190:191],27,4608
s_waitcnt vmcnt(12) lgkmcnt(0)
s_barrier
v_add_u32 v26,81920,v20
v_add_u32 v27,81920,v21
T a[192:193],26,0
T a[194:195],26,512
T a[196:197],27,0
T a[198:199],27,512
T a[200:201],26,4096
T a[202:203],26,4608
T a[204:205],27,4096
T a[206:207],27,4608
s_waitcnt vmcnt(8) lgkmcnt(0)
s_barrier
v_add_u32 v26,98304,v20
v_add_u32 v27,98304,v21
T a[208:209],26,0
T a[210:211],26,512
T a[212:213],27,0
T a[214:215],27,512
T a[216:217],26,4096
T a[218:219],26,4608
T a[220:221],27,4096
T a[222:223],27,4608
s_waitcnt vmcnt(4) lgkmcnt(0)
s_barrier
v_add_u32 v26,0,v20
v_add_u32 v27,0,v21
T a[224:225],26,0
T a[226:227],26,512
T a[228:229],27,0
T a[230:231],27,512
T a[232:233],26,4096
T a[234:235],26,4608
T a[236:237],27,4096
T a[238:239],27,4608
s_waitcnt vmcnt(0) lgkmcnt(0)
s_barrier
v_add_u32 v26,16384,v20
v_add_u32 v27,16384,v21
T a[240:241],26,0
T a[242:243],26,512
T a[244:245],27,0
T a[246:247],27,512
T a[248:249],26,4096
T a[250:251],26,4608
T a[252:253],27,4096
T a[254:255],27,4608
L 0
s_barrier
v_mov_b32 v4,s53
s_mov_b32 s82,0
s_mov_b32 s80,0
s_mov_b32 s54,s45
s_mul_i32 s57,s44,5120
s_mul_i32 s55,s45,40960
s_add_u32 s55,s55,s57
s_add_u32 s55,s55,17825792
s_mul_i32 s57,s44,4096
s_mul_i32 s60,s46,40960
s_mul_i32 s61,s46,1024
s_mul_i32 s62,s46,32768
s_mul_i32 s63,s46,1024
s_mul_i32 s64,s46,512
s_lshl_b32 s59,s44,8
s_add_u32 s59,s59,131072
s_mul_i32 s58,s45,1024
s_add_u32 s58,s58,109333504
s_mul_i32 s65,s45,32768
s_add_u32 s65,s65,s47
s_mul_i32 s68,s45,1024
s_add_u32 s68,s68,s48
s_mul_i32 s73,s45,512
s_add_u32 s73,s73,106954752
s_mul_i32 s74,s45,32768
s_add_u32 s74,s74,s49
s_mul_i32 s75,s45,1024
s_add_u32 s75,s75,107479040
s_mov_b32 s66,0x7fff0000
s_mov_b32 s67,0x7fff0000
s_mov_b32 s69,0x7fff0000
s_mov_b32 s76,0x7fff0000
s_mov_b32 s77,0x7fff0000
s_mov_b32 s70,0x7fff0000
s_mov_b32 s71,0x7fff0000
s_mov_b32 s72,0x7fff0000
v_mov_b32 v1,v0
v_add_u32 v2,16384,v0
s_mov_b32 s84,16384
s_mov_b32 s86,0x3a800000
s_mov_b32 s87,0x38a7c5ac
s_mov_b32 s88,0x409d30fc
s_mov_b32 s89,0xbeeff558
s_lshr_b32 s85,s44,1
s_mul_i32 s85,s85,127
v_lshlrev_b32 v8,22,v0
v_and_b32 v8,0x80000000,v8
v_or_b32 v8,v8,v5
v_add_u32 v2,32768,v0
s_lshl_b32 s84,s57,1
s_add_u32 s84,s84,32768
s_getpc_b64 s[40:41]
Lg2_pc_%=:
s_lshl_b32 s78,s44,13
v_lshlrev_b32 v106,3,v0
v_add_u32 v106,s78,v106
v_min_u32 v106,Lcode_g12_e-Lg2_pc_%=-4,v106
s_mov_b32 m0,163584
s_nop 0
global_load_lds_dword v106,s[40:41]
s_and_b32 s79,s54,127
s_cmp_eq_u32 s79,0
s_cselect_b32 s94,0x3300,0
s_cmp_eq_u32 s79,127
s_cselect_b32 s94,0xcc00,s94
s_add_u32 m0,s59,4096
s_nop 0
buffer_load_dword v8,s[36:39],s58 offen offset:0 sc1 lds
buffer_store_dword v4,v3,s[36:39],s71 offen offset:0 sc0 sc1
s_add_u32 m0,s59,5120
s_nop 0
buffer_load_dword v8,s[36:39],s58 offen offset:0 sc1 lds
buffer_store_dword v4,v3,s[36:39],s71 offen offset:0 sc0 sc1
s_mov_b32 s83,0
Lg2_poll_%=:
s_add_u32 s78,s83,2
s_and_b32 s78,s78,3
s_lshl_b32 s78,s78,10
s_add_u32 s78,s78,4096
s_add_u32 m0,s59,s78
s_nop 0
buffer_load_dword v8,s[36:39],s58 offen offset:0 sc1 lds
s_and_b32 s78,s83,3
s_lshl_b32 s78,s78,10
v_add_u32 v106,s78,v6
s_waitcnt vmcnt(4)
s_barrier
ds_read_b32 v7,v106 offset:4096
buffer_store_dword v4,v3,s[36:39],s70 offen offset:0 sc0 sc1
s_mov_b32 s70,s71
s_mov_b32 s71,s72
s_mov_b32 s72,0x7fff0000
s_add_u32 s83,s83,1
s_add_u32 s82,s82,1
L 0
v_cmp_eq_u32 vcc,s53,v7
s_nop 0
s_or_b64 vcc,vcc,s[94:95]
s_cmp_eq_u32 vcc_lo,-1
s_cbranch_scc1 Lg2_go_%=
s_cmp_lt_u32 s82,20000
s_cbranch_scc1 Lg2_poll_%=
Lg2_go_%=:
s_and_b32 s78,s54,127
s_cmp_eq_u32 s78,s85
s_cselect_b64 s[40:41],-1,0
v_cndmask_b32 v17,v15,v16,s[40:41]
s_add_u32 m0,s57,0
s_add_u32 s56,s55,0
D 17,56,0
s_add_u32 s56,s55,256
D 17,56,1024
s_add_u32 s56,s55,512
D 17,56,2048
s_add_u32 s56,s55,768
D 17,56,3072
s_add_u32 s58,s58,s61
s_add_u32 m0,s59,2048
s_nop 0
buffer_load_dword v8,s[36:39],s58 offen offset:0 sc1 lds
v_cndmask_b32 v17,v10,v9,s[40:41]
s_add_u32 m0,s57,16384
s_add_u32 s56,s55,20480
D 17,56,0
s_add_u32 s56,s55,20736
D 17,56,1024
s_add_u32 s56,s55,20992
D 17,56,2048
s_add_u32 s56,s55,21248
D 17,56,3072
s_add_u32 s55,s55,s60
s_sub_u32 s57,s84,s57
s_add_u32 s54,s54,s46
s_and_b32 s79,s54,127
s_cmp_eq_u32 s79,0
s_cselect_b32 s94,0x3300,0
s_cmp_eq_u32 s79,127
s_cselect_b32 s94,0xcc00,s94
s_waitcnt vmcnt(4)
s_barrier
buffer_store_dword v4,v3,s[36:39],s70 offen offset:0 sc0 sc1
s_min_u32 s71,s71,s72
buffer_store_dword v4,v3,s[36:39],s71 offen offset:0 sc0 sc1
s_mov_b32 s70,0x7fff0000
s_mov_b32 s71,0x7fff0000
s_mov_b32 s72,0x7fff0000
s_cmp_eq_u32 s80,1
buffer_store_dword v4,v3,s[36:39],s71 offen offset:0 sc0 sc1
s_cbranch_scc1 Lg2_st1_%=
s_branch Lg2_st0_%=
Lg2_gox_%=:
s_add_u32 m0,s59,2048
s_nop 0
buffer_load_dword v8,s[36:39],s58 offen offset:0 sc1 lds
s_and_b32 s79,s54,127
s_cmp_eq_u32 s79,0
s_cselect_b32 s94,0x3300,0
s_cmp_eq_u32 s79,127
s_cselect_b32 s94,0xcc00,s94
s_waitcnt vmcnt(0)
s_barrier
buffer_store_dword v4,v3,s[36:39],s70 offen offset:0 sc0 sc1
s_min_u32 s71,s71,s72
buffer_store_dword v4,v3,s[36:39],s71 offen offset:0 sc0 sc1
s_mov_b32 s70,0x7fff0000
s_mov_b32 s71,0x7fff0000
s_mov_b32 s72,0x7fff0000
s_cmp_eq_u32 s80,1
s_cbranch_scc1 Lg2_st1_%=
Lg2_st0_%=:
ds_read_b32 v7,v6 offset:2048
R 34,1,0
R 38,1,1024
R 42,1,2048
R 46,1,3072
R 50,1,4096
L 5
v_cmp_eq_u32 vcc,s53,v7
s_nop 0
s_or_b64 vcc,vcc,s[94:95]
L 3
s_cmp_eq_u32 vcc_lo,-1
s_cbranch_scc1 Lg2_F0_%=
s_branch Lg2_L0_%=
Lg2_st1_%=:
ds_read_b32 v7,v6 offset:2048
R 34,2,0
R 38,2,1024
R 42,2,2048
R 46,2,3072
R 50,2,4096
L 5
v_cmp_eq_u32 vcc,s53,v7
s_nop 0
s_or_b64 vcc,vcc,s[94:95]
L 3
s_cmp_eq_u32 vcc_lo,-1
s_cbranch_scc1 Lg2_F1_%=
s_branch Lg2_L1_%=
Lg2_F0_%=:
v_mfma_f32_16x16x32_bf16 v[22:25],a[4:7],v[34:37],0
s_add_u32 s58,s58,s61
s_add_u32 m0,s59,0
s_and_b32 s78,s54,127
R 54,1,5120
M 22,a[12:15],38
s_cmp_eq_u32 s78,s85
s_cselect_b64 s[40:41],-1,0
s_add_u32 s56,s55,0
buffer_load_dword v8,s[36:39],s58 offen offset:0 sc1 lds
v_mfma_f32_16x16x32_bf16 v[18:21],a[0:3],v[34:37],0
s_add_u32 m0,s57,0
R 58,1,6144
v_cndmask_b32 v17,v15,v16,s[40:41]
M 18,a[8:11],38
L 3
D 17,56,0
M 18,a[16:19],42
s_add_u32 s56,s55,256
R 62,1,7168
v_lshlrev_b32 v106,16,v126
M 18,a[24:27],46
s_add_u32 s54,s54,s46
v_and_b32 v107,0xffff0000,v126
v_lshlrev_b32 v108,16,v127
M 22,a[20:23],42
s_and_b32 s79,s54,127
R 66,1,8192
v_and_b32 v109,0xffff0000,v127
M 22,a[28:31],46
L 3
D 17,56,1024
M 22,a[36:39],50
s_add_u32 s56,s55,512
R 70,1,9216
v_lshlrev_b32 v110,16,v128
M 22,a[44:47],54
s_cmp_eq_u32 s79,0
v_and_b32 v111,0xffff0000,v128
v_lshlrev_b32 v112,16,v129
M 18,a[32:35],50
s_cselect_b32 s94,0x3300,0
R 74,1,10240
v_and_b32 v113,0xffff0000,v129
M 18,a[40:43],54
L 3
D 17,56,2048
M 18,a[48:51],58
s_add_u32 s56,s55,768
R 78,1,11264
v_fma_f32 v106,v106,v103,v102
M 18,a[56:59],62
D 17,56,3072
M 22,a[52:55],58
s_add_u32 m0,s57,16384
R 82,1,12288
v_fma_f32 v107,v107,v103,v102
M 22,a[60:63],62
L 3
v_fma_f32 v108,v108,v103,v102
v_fma_f32 v109,v109,v103,v102
M 22,a[68:71],66
s_add_u32 s56,s55,20480
R 86,1,13312
v_fma_f32 v110,v110,v103,v102
M 22,a[76:79],70
buffer_load_dwordx4 v[122:125],v0,s[36:39],s74 offen offset:0 sc1
M 18,a[64:67],66
s_cmp_eq_u32 s79,127
R 90,1,14336
v_fma_f32 v111,v111,v103,v102
M 18,a[72:75],70
L 3
buffer_load_dwordx2 v[98:99],v11,s[36:39],s73 offen offset:8 sc1
M 18,a[80:83],74
s_waitcnt vmcnt(10)
R 94,1,15360
v_fma_f32 v112,v112,v103,v102
M 18,a[88:91],78
s_barrier
v_fma_f32 v113,v113,v103,v102
v_add_f32 v26,v26,v106
M 22,a[84:87],74
s_cselect_b32 s94,0xcc00,s94
R 34,1,16384
v_add_f32 v27,v27,v107
M 22,a[92:95],78
L 3
v_add_f32 v28,v28,v108
v_add_f32 v29,v29,v109
M 22,a[100:103],82
s_mov_b32 s66,s65
R 38,1,17408
v_add_f32 v30,v30,v110
M 22,a[108:111],86
s_mov_b32 s76,s75
v_cndmask_b32 v17,v10,v9,s[40:41]
v_add_f32 v31,v31,v111
M 18,a[96:99],82
s_add_u32 s73,s73,s64
R 42,1,18432
v_add_f32 v32,v32,v112
M 18,a[104:107],86
L 3
D 17,56,0
M 18,a[112:115],90
s_add_u32 s56,s55,20736
R 46,1,19456
v_add_f32 v33,v33,v113
M 18,a[120:123],94
s_add_u32 s74,s74,s62
v_cvt_pk_bf16_f32 v114,v26,v27
)"
R"(v_cvt_pk_bf16_f32 v115,v28,v29
M 22,a[116:119],90
s_add_u32 s65,s65,s62
R 50,1,20480
v_fma_f32 v107,v26,v26,s87
M 22,a[124:127],94
L 3
v_add_f32 v106,v26,v27
v_fma_f32 v107,v27,v27,v107
M 22,a[132:135],34
s_add_u32 s75,s75,s63
R 54,1,21504
v_add_f32 v106,v106,v28
M 22,a[140:143],38
D 17,56,1024
M 18,a[128:131],34
s_add_u32 s56,s55,20992
R 58,1,22528
v_fma_f32 v107,v28,v28,v107
M 18,a[136:139],38
L 3
v_add_f32 v106,v106,v29
v_fma_f32 v107,v29,v29,v107
M 18,a[144:147],42
s_sub_u32 s57,s84,s57
R 62,1,23552
v_add_f32 v106,v106,v30
M 18,a[152:155],46
v_fma_f32 v107,v30,v30,v107
v_add_f32 v106,v106,v31
M 22,a[148:151],42
R 66,1,24576
v_fma_f32 v107,v31,v31,v107
M 22,a[156:159],46
L 3
v_add_f32 v106,v106,v32
v_fma_f32 v107,v32,v32,v107
M 22,a[164:167],50
R 70,1,25600
v_add_f32 v106,v106,v33
M 22,a[172:175],54
v_fma_f32 v107,v33,v33,v107
v_cvt_pk_bf16_f32 v116,v30,v31
M 18,a[160:163],50
R 74,1,26624
v_cvt_pk_bf16_f32 v117,v32,v33
M 18,a[168:171],54
L 3
v_permlane16_swap_b32 v106,v107
v_add_f32 v106,v106,v107
v_mov_b32 v107,v106
M 18,a[176:179],58
R 78,1,27648
M 18,a[184:187],62
v_permlane32_swap_b32 v106,v107
v_add_f32 v106,v106,v107
M 22,a[180:183],58
R 82,1,28672
ds_write_b32 v12,v106 offset:2048
M 22,a[188:191],62
L 4
D 17,56,2048
M 22,a[196:199],66
s_add_u32 s56,s55,21248
s_add_u32 s55,s55,s60
R 86,1,29696
M 22,a[204:207],70
D 17,56,3072
M 18,a[192:195],66
R 90,1,30720
M 18,a[200:203],70
L 4
L 2
buffer_store_dwordx4 v[114:117],v0,s[36:39],s67 offen offset:0 sc0 sc1
M 18,a[208:211],74
s_waitcnt vmcnt(7)
R 94,1,31744
M 18,a[216:219],78
s_barrier
ds_read_b32 v7,v6 offset:0
R 118,13,2048
M 22,a[212:215],74
R 34,2,0
M 22,a[220:223],78
L 5
buffer_store_dword v4,v3,s[36:39],s70 offen offset:0 sc0 sc1
M 22,a[228:231],82
s_mov_b32 s70,s69
s_mov_b32 s69,s68
R 38,2,1024
M 22,a[236:239],86
s_add_u32 s68,s68,s63
M 18,a[224:227],82
R 42,2,2048
M 18,a[232:235],86
L 5
L 4
v_cmp_eq_u32 vcc,s53,v7
M 18,a[240:243],90
L 3
R 46,2,3072
v_add_f32 v118,v118,v119
M 18,a[248:251],94
s_or_b64 vcc,vcc,s[94:95]
v_add_f32 v120,v120,v121
v_add_f32 v118,v118,v120
M 22,a[244:247],90
s_cmp_eq_u32 vcc_lo,-1
R 50,2,4096
v_mul_f32 v118,s86,v118
M 22,a[252:255],94
s_waitcnt vmcnt(6) lgkmcnt(3)
buffer_store_dword v118,v14,s[36:39],s77 offen offset:0 sc0 sc1
s_cbranch_scc0 Lg2_K1_%=
Lg2_F1_%=:
v_mfma_f32_16x16x32_bf16 v[30:33],a[4:7],v[34:37],0
s_add_u32 s58,s58,s61
s_add_u32 m0,s59,1024
s_and_b32 s78,s54,127
R 54,2,5120
M 30,a[12:15],38
s_cmp_eq_u32 s78,s85
s_cselect_b64 s[40:41],-1,0
s_add_u32 s56,s55,0
buffer_load_dword v8,s[36:39],s58 offen offset:0 sc1 lds
v_mfma_f32_16x16x32_bf16 v[26:29],a[0:3],v[34:37],0
s_add_u32 m0,s57,0
R 58,2,6144
v_cndmask_b32 v17,v15,v16,s[40:41]
M 26,a[8:11],38
L 3
D 17,56,0
M 26,a[16:19],42
s_add_u32 s56,s55,256
R 62,2,7168
v_lshlrev_b32 v106,16,v122
M 26,a[24:27],46
s_add_u32 s54,s54,s46
v_and_b32 v107,0xffff0000,v122
v_lshlrev_b32 v108,16,v123
M 30,a[20:23],42
s_and_b32 s79,s54,127
R 66,2,8192
v_and_b32 v109,0xffff0000,v123
M 30,a[28:31],46
L 3
D 17,56,1024
M 30,a[36:39],50
s_add_u32 s56,s55,512
R 70,2,9216
v_lshlrev_b32 v110,16,v124
M 30,a[44:47],54
s_cmp_eq_u32 s79,0
v_and_b32 v111,0xffff0000,v124
v_lshlrev_b32 v112,16,v125
M 26,a[32:35],50
s_cselect_b32 s94,0x3300,0
R 74,2,10240
v_and_b32 v113,0xffff0000,v125
M 26,a[40:43],54
L 3
D 17,56,2048
M 26,a[48:51],58
s_add_u32 s56,s55,768
R 78,2,11264
v_fma_f32 v106,v106,v99,v98
M 26,a[56:59],62
D 17,56,3072
M 30,a[52:55],58
s_add_u32 m0,s57,16384
R 82,2,12288
v_fma_f32 v107,v107,v99,v98
M 30,a[60:63],62
L 3
v_fma_f32 v108,v108,v99,v98
v_fma_f32 v109,v109,v99,v98
M 30,a[68:71],66
s_add_u32 s56,s55,20480
R 86,2,13312
v_fma_f32 v110,v110,v99,v98
M 30,a[76:79],70
buffer_load_dwordx4 v[126:129],v0,s[36:39],s74 offen offset:0 sc1
M 26,a[64:67],66
s_cmp_eq_u32 s79,127
R 90,2,14336
v_fma_f32 v111,v111,v99,v98
M 26,a[72:75],70
L 3
buffer_load_dwordx2 v[102:103],v11,s[36:39],s73 offen offset:8 sc1
M 26,a[80:83],74
s_waitcnt vmcnt(10)
R 94,2,15360
v_fma_f32 v112,v112,v99,v98
M 26,a[88:91],78
s_barrier
v_fma_f32 v113,v113,v99,v98
v_add_f32 v18,v18,v106
M 30,a[84:87],74
s_cselect_b32 s94,0xcc00,s94
R 34,2,16384
v_add_f32 v19,v19,v107
M 30,a[92:95],78
L 3
v_add_f32 v20,v20,v108
v_add_f32 v21,v21,v109
M 30,a[100:103],82
s_mov_b32 s67,s65
R 38,2,17408
v_add_f32 v22,v22,v110
M 30,a[108:111],86
s_mov_b32 s77,s75
v_cndmask_b32 v17,v10,v9,s[40:41]
v_add_f32 v23,v23,v111
M 26,a[96:99],82
s_add_u32 s73,s73,s64
R 42,2,18432
v_add_f32 v24,v24,v112
M 26,a[104:107],86
L 3
D 17,56,0
M 26,a[112:115],90
s_add_u32 s56,s55,20736
R 46,2,19456
v_add_f32 v25,v25,v113
M 26,a[120:123],94
s_add_u32 s74,s74,s62
v_cvt_pk_bf16_f32 v114,v18,v19
v_cvt_pk_bf16_f32 v115,v20,v21
M 30,a[116:119],90
s_add_u32 s65,s65,s62
R 50,2,20480
v_fma_f32 v107,v18,v18,s87
M 30,a[124:127],94
L 3
v_add_f32 v106,v18,v19
v_fma_f32 v107,v19,v19,v107
M 30,a[132:135],34
s_add_u32 s75,s75,s63
R 54,2,21504
v_add_f32 v106,v106,v20
M 30,a[140:143],38
D 17,56,1024
M 26,a[128:131],34
s_add_u32 s56,s55,20992
R 58,2,22528
v_fma_f32 v107,v20,v20,v107
M 26,a[136:139],38
L 3
v_add_f32 v106,v106,v21
v_fma_f32 v107,v21,v21,v107
M 26,a[144:147],42
s_sub_u32 s57,s84,s57
R 62,2,23552
v_add_f32 v106,v106,v22
M 26,a[152:155],46
v_fma_f32 v107,v22,v22,v107
v_add_f32 v106,v106,v23
M 30,a[148:151],42
R 66,2,24576
v_fma_f32 v107,v23,v23,v107
M 30,a[156:159],46
L 3
v_add_f32 v106,v106,v24
v_fma_f32 v107,v24,v24,v107
M 30,a[164:167],50
R 70,2,25600
v_add_f32 v106,v106,v25
M 30,a[172:175],54
v_fma_f32 v107,v25,v25,v107
v_cvt_pk_bf16_f32 v116,v22,v23
M 26,a[160:163],50
R 74,2,26624
v_cvt_pk_bf16_f32 v117,v24,v25
M 26,a[168:171],54
L 3
v_permlane16_swap_b32 v106,v107
v_add_f32 v106,v106,v107
v_mov_b32 v107,v106
M 26,a[176:179],58
R 78,2,27648
M 26,a[184:187],62
v_permlane32_swap_b32 v106,v107
v_add_f32 v106,v106,v107
M 30,a[180:183],58
R 82,2,28672
ds_write_b32 v12,v106 offset:0
M 30,a[188:191],62
L 4
D 17,56,2048
M 30,a[196:199],66
s_add_u32 s56,s55,21248
s_add_u32 s55,s55,s60
R 86,2,29696
M 30,a[204:207],70
D 17,56,3072
M 26,a[192:195],66
R 90,2,30720
M 26,a[200:203],70
L 4
L 2
buffer_store_dwordx4 v[114:117],v0,s[36:39],s66 offen offset:0 sc0 sc1
M 26,a[208:211],74
s_waitcnt vmcnt(7)
R 94,2,31744
M 26,a[216:219],78
s_barrier
ds_read_b32 v7,v6 offset:1024
R 118,13,0
M 30,a[212:215],74
R 34,1,0
M 30,a[220:223],78
L 5
buffer_store_dword v4,v3,s[36:39],s70 offen offset:0 sc0 sc1
M 30,a[228:231],82
s_mov_b32 s70,s69
s_mov_b32 s69,s68
R 38,1,1024
M 30,a[236:239],86
s_add_u32 s68,s68,s63
M 26,a[224:227],82
R 42,1,2048
M 26,a[232:235],86
L 5
L 4
v_cmp_eq_u32 vcc,s53,v7
M 26,a[240:243],90
L 3
R 46,1,3072
v_add_f32 v118,v118,v119
M 26,a[248:251],94
s_or_b64 vcc,vcc,s[94:95]
v_add_f32 v120,v120,v121
v_add_f32 v118,v118,v120
M 30,a[244:247],90
s_cmp_eq_u32 vcc_lo,-1
R 50,1,4096
v_mul_f32 v118,s86,v118
M 30,a[252:255],94
s_waitcnt vmcnt(6) lgkmcnt(3)
buffer_store_dword v118,v14,s[36:39],s76 offen offset:0 sc0 sc1
s_cbranch_scc1 Lg2_F0_%=
Lg2_K0_%=:
Lg2_L0_%=:
v_mfma_f32_16x16x32_bf16 v[22:25],a[4:7],v[34:37],0
s_add_u32 m0,s59,3072
R 54,1,5120
v_lshlrev_b32 v106,16,v126
M 22,a[12:15],38
buffer_load_dword v8,s[36:39],s58 offen offset:0 sc1 lds
v_mfma_f32_16x16x32_bf16 v[18:21],a[0:3],v[34:37],0
s_mov_b32 s66,s65
R 58,1,6144
v_and_b32 v107,0xffff0000,v126
M 18,a[8:11],38
L 3
v_lshlrev_b32 v108,16,v127
v_and_b32 v109,0xffff0000,v127
M 18,a[16:19],42
s_mov_b32 s76,s75
R 62,1,7168
v_lshlrev_b32 v110,16,v128
M 18,a[24:27],46
s_add_u32 s65,s65,s62
v_and_b32 v111,0xffff0000,v128
v_lshlrev_b32 v112,16,v129
M 22,a[20:23],42
s_add_u32 s75,s75,s63
R 66,1,8192
v_and_b32 v113,0xffff0000,v129
v_fma_f32 v106,v106,v103,v102
M 22,a[28:31],46
L 3
v_fma_f32 v107,v107,v103,v102
v_fma_f32 v108,v108,v103,v102
M 22,a[36:39],50
R 70,1,9216
v_fma_f32 v109,v109,v103,v102
M 22,a[44:47],54
v_fma_f32 v110,v110,v103,v102
v_fma_f32 v111,v111,v103,v102
M 18,a[32:35],50
R 74,1,10240
v_fma_f32 v112,v112,v103,v102
M 18,a[40:43],54
L 3
v_fma_f32 v113,v113,v103,v102
v_add_f32 v26,v26,v106
M 18,a[48:51],58
R 78,1,11264
v_add_f32 v27,v27,v107
M 18,a[56:59],62
v_add_f32 v28,v28,v108
v_add_f32 v29,v29,v109
v_add_f32 v30,v30,v110
M 22,a[52:55],58
R 82,1,12288
v_add_f32 v31,v31,v111
M 22,a[60:63],62
L 3
v_add_f32 v32,v32,v112
v_add_f32 v33,v33,v113
M 22,a[68:71],66
R 86,1,13312
v_cvt_pk_bf16_f32 v114,v26,v27
M 22,a[76:79],70
buffer_load_dwordx4 v[122:125],v0,s[36:39],s74 offen offset:0 sc1
M 18,a[64:67],66
s_add_u32 s74,s74,s62
R 90,1,14336
v_cvt_pk_bf16_f32 v115,v28,v29
M 18,a[72:75],70
L 3
buffer_load_dwordx2 v[98:99],v11,s[36:39],s73 offen offset:8 sc1
v_cvt_pk_bf16_f32 v116,v30,v31
M 18,a[80:83],74
s_waitcnt vmcnt(2)
s_add_u32 s73,s73,s64
R 94,1,15360
M 18,a[88:91],78
s_barrier
ds_read_b32 v7,v6 offset:3072
v_cvt_pk_bf16_f32 v117,v32,v33
M 22,a[84:87],74
R 34,1,16384
v_add_f32 v106,v26,v27
M 22,a[92:95],78
L 4
buffer_store_dword v4,v3,s[36:39],s70 offen offset:0 sc0 sc1
M 22,a[100:103],82
s_mov_b32 s70,0x7fff0000
R 38,1,17408
v_fma_f32 v107,v26,v26,s87
M 22,a[108:111],86
buffer_store_dwordx4 v[114:117],v0,s[36:39],s67 offen offset:0 sc0 sc1
v_fma_f32 v107,v27,v27,v107
M 18,a[96:99],82
R 42,1,18432
v_add_f32 v106,v106,v28
v_fma_f32 v107,v28,v28,v107
M 18,a[104:107],86
L 4
L 3
v_cmp_eq_u32 vcc,s53,v7
M 18,a[112:115],90
R 46,1,19456
v_add_f32 v106,v106,v29
M 18,a[120:123],94
s_or_b64 vcc,vcc,s[94:95]
v_fma_f32 v107,v29,v29,v107
v_add_f32 v106,v106,v30
M 22,a[116:119],90
R 50,1,20480
v_fma_f32 v107,v30,v30,v107
M 22,a[124:127],94
s_cmp_eq_u32 vcc_lo,-1
L 3
v_add_f32 v106,v106,v31
s_cbranch_scc0 Lg2_X0_%=
M 22,a[132:135],34
s_and_b32 s78,s54,127
s_cmp_eq_u32 s78,s85
s_cselect_b64 s[40:41],-1,0
R 54,1,21504
M 22,a[140:143],38
s_add_u32 m0,s57,0
s_add_u32 s56,s55,0
s_add_u32 s58,s58,s61
v_cndmask_b32 v17,v15,v16,s[40:41]
M 18,a[128:131],34
s_add_u32 s54,s54,s46
R 58,1,22528
v_fma_f32 v107,v31,v31,v107
M 18,a[136:139],38
L 3
D 17,56,0
M 18,a[144:147],42
s_add_u32 s56,s55,256
R 62,1,23552
v_add_f32 v106,v106,v32
M 18,a[152:155],46
s_and_b32 s79,s54,127
v_fma_f32 v107,v32,v32,v107
v_add_f32 v106,v106,v33
M 22,a[148:151],42
s_cmp_eq_u32 s79,0
R 66,1,24576
v_fma_f32 v107,v33,v33,v107
M 22,a[156:159],46
L 3
D 17,56,1024
M 22,a[164:167],50
s_cselect_b32 s94,0x3300,0
s_add_u32 s56,s55,512
R 70,1,25600
M 22,a[172:175],54
s_cmp_eq_u32 s79,127
v_permlane16_swap_b32 v106,v107
v_add_f32 v106,v106,v107
M 18,a[160:163],50
s_cselect_b32 s94,0xcc00,s94
)"
R"(R 74,1,26624
v_mov_b32 v107,v106
M 18,a[168:171],54
L 3
D 17,56,2048
M 18,a[176:179],58
s_add_u32 s56,s55,768
R 78,1,27648
M 18,a[184:187],62
v_permlane32_swap_b32 v106,v107
v_add_f32 v106,v106,v107
M 22,a[180:183],58
R 82,1,28672
ds_write_b32 v12,v106 offset:2048
M 22,a[188:191],62
L 4
D 17,56,3072
M 22,a[196:199],66
s_add_u32 m0,s59,0
s_add_u32 s56,s55,20480
R 86,1,29696
M 22,a[204:207],70
buffer_load_dword v8,s[36:39],s58 offen offset:0 sc1 lds
M 18,a[192:195],66
s_add_u32 m0,s57,16384
R 90,1,30720
v_cndmask_b32 v17,v10,v9,s[40:41]
M 18,a[200:203],70
L 4
L 2
s_sub_u32 s57,s84,s57
M 18,a[208:211],74
s_waitcnt vmcnt(0)
R 94,1,31744
M 18,a[216:219],78
s_barrier
D 17,56,0
M 22,a[212:215],74
s_add_u32 s56,s55,20736
R 34,2,0
ds_read_b32 v7,v6 offset:0
M 22,a[220:223],78
L 4
buffer_store_dword v4,v3,s[36:39],s70 offen offset:0 sc0 sc1
M 22,a[228:231],82
s_mov_b32 s70,s69
R 38,2,1024
R 118,13,2048
M 22,a[236:239],86
D 17,56,1024
M 18,a[224:227],82
s_add_u32 s56,s55,20992
s_mov_b32 s69,s68
R 42,2,2048
M 18,a[232:235],86
L 5
D 17,56,2048
M 18,a[240:243],90
L 3
s_add_u32 s56,s55,21248
R 46,2,3072
v_cmp_eq_u32 vcc,s53,v7
M 18,a[248:251],94
D 17,56,3072
M 22,a[244:247],90
s_add_u32 s55,s55,s60
s_add_u32 s68,s68,s63
L 2
s_or_b64 vcc,vcc,s[94:95]
s_cmp_eq_u32 vcc_lo,-1
R 50,2,4096
v_add_f32 v118,v118,v119
v_add_f32 v120,v120,v121
v_add_f32 v118,v118,v120
v_mul_f32 v118,s86,v118
M 22,a[252:255],94
s_nop 0
buffer_store_dword v118,v14,s[36:39],s77 offen offset:0 sc0 sc1
buffer_store_dword v4,v3,s[36:39],s71 offen offset:0 sc0 sc1
buffer_store_dword v4,v3,s[36:39],s71 offen offset:0 sc0 sc1
s_cbranch_scc1 Lg2_F1_%=
s_branch Lg2_K1_%=
Lg2_X0_%=:
M 22,a[132:135],34
s_add_u32 m0,s59,0
R 54,1,21504
v_fma_f32 v107,v31,v31,v107
M 22,a[140:143],38
buffer_load_dword v8,s[36:39],s58 offen offset:0 sc1 lds
M 18,a[128:131],34
R 58,1,22528
v_add_f32 v106,v106,v32
M 18,a[136:139],38
L 3
v_fma_f32 v107,v32,v32,v107
v_add_f32 v106,v106,v33
M 18,a[144:147],42
R 62,1,23552
v_fma_f32 v107,v33,v33,v107
M 18,a[152:155],46
M 22,a[148:151],42
R 66,1,24576
M 22,a[156:159],46
L 3
v_permlane16_swap_b32 v106,v107
v_add_f32 v106,v106,v107
M 22,a[164:167],50
R 70,1,25600
v_mov_b32 v107,v106
M 22,a[172:175],54
M 18,a[160:163],50
R 74,1,26624
M 18,a[168:171],54
L 3
v_permlane32_swap_b32 v106,v107
M 18,a[176:179],58
R 78,1,27648
v_add_f32 v106,v106,v107
M 18,a[184:187],62
ds_write_b32 v12,v106 offset:2048
M 22,a[180:183],58
R 82,1,28672
M 22,a[188:191],62
L 4
M 22,a[196:199],66
R 86,1,29696
M 22,a[204:207],70
M 18,a[192:195],66
R 90,1,30720
M 18,a[200:203],70
L 4
L 3
M 18,a[208:211],74
s_waitcnt vmcnt(0)
R 94,1,31744
M 18,a[216:219],78
s_barrier
R 118,13,2048
ds_read_b32 v7,v6 offset:0
M 22,a[212:215],74
buffer_store_dword v4,v3,s[36:39],s70 offen offset:0 sc0 sc1
M 22,a[220:223],78
L 4
s_mov_b32 s70,s69
s_mov_b32 s69,s68
M 22,a[228:231],82
s_add_u32 s68,s68,s63
M 22,a[236:239],86
M 18,a[224:227],82
M 18,a[232:235],86
L 2
L 1
L 0
M 18,a[240:243],90
v_add_f32 v118,v118,v119
v_add_f32 v120,v120,v121
v_add_f32 v118,v118,v120
M 18,a[248:251],94
v_cmp_eq_u32 vcc,s53,v7
v_mul_f32 v118,s86,v118
M 22,a[244:247],90
buffer_store_dword v118,v14,s[36:39],s77 offen offset:0 sc0 sc1
M 22,a[252:255],94
s_nop 0
s_or_b64 vcc,vcc,s[94:95]
s_mov_b32 s80,1
s_cmp_eq_u32 vcc_lo,-1
s_cbranch_scc0 Lg2_xn0_%=
s_and_b32 s78,s54,127
s_cmp_eq_u32 s78,s85
s_cselect_b64 s[40:41],-1,0
v_cndmask_b32 v17,v15,v16,s[40:41]
s_add_u32 m0,s57,0
s_add_u32 s56,s55,0
D 17,56,0
v_lshlrev_b32 v106,16,v122
v_and_b32 v107,0xffff0000,v122
v_lshlrev_b32 v108,16,v123
v_and_b32 v109,0xffff0000,v123
v_lshlrev_b32 v110,16,v124
s_add_u32 s56,s55,256
D 17,56,1024
v_and_b32 v111,0xffff0000,v124
v_lshlrev_b32 v112,16,v125
v_and_b32 v113,0xffff0000,v125
v_fma_f32 v106,v106,v99,v98
v_fma_f32 v107,v107,v99,v98
s_add_u32 s56,s55,512
D 17,56,2048
v_fma_f32 v108,v108,v99,v98
v_fma_f32 v109,v109,v99,v98
v_fma_f32 v110,v110,v99,v98
v_fma_f32 v111,v111,v99,v98
v_fma_f32 v112,v112,v99,v98
s_add_u32 s56,s55,768
D 17,56,3072
v_fma_f32 v113,v113,v99,v98
v_add_f32 v18,v18,v106
v_add_f32 v19,v19,v107
v_add_f32 v20,v20,v108
v_add_f32 v21,v21,v109
s_add_u32 s58,s58,s61
v_cndmask_b32 v17,v10,v9,s[40:41]
s_add_u32 m0,s57,16384
s_add_u32 s56,s55,20480
D 17,56,0
v_add_f32 v22,v22,v110
v_add_f32 v23,v23,v111
v_add_f32 v24,v24,v112
v_add_f32 v25,v25,v113
v_cvt_pk_bf16_f32 v114,v18,v19
s_add_u32 s56,s55,20736
D 17,56,1024
v_cvt_pk_bf16_f32 v115,v20,v21
v_cvt_pk_bf16_f32 v116,v22,v23
v_cvt_pk_bf16_f32 v117,v24,v25
s_add_u32 s56,s55,20992
D 17,56,2048
s_add_u32 s56,s55,21248
D 17,56,3072
buffer_store_dwordx4 v[114:117],v0,s[36:39],s66 offen offset:0 sc0 sc1
v_add_f32 v106,v18,v19
v_fma_f32 v107,v18,v18,s87
s_add_u32 s55,s55,s60
s_sub_u32 s57,s84,s57
s_add_u32 s54,s54,s46
v_fma_f32 v107,v19,v19,v107
v_add_f32 v106,v106,v20
v_fma_f32 v107,v20,v20,v107
v_add_f32 v106,v106,v21
v_fma_f32 v107,v21,v21,v107
v_add_f32 v106,v106,v22
v_fma_f32 v107,v22,v22,v107
v_add_f32 v106,v106,v23
v_fma_f32 v107,v23,v23,v107
v_add_f32 v106,v106,v24
v_fma_f32 v107,v24,v24,v107
v_add_f32 v106,v106,v25
v_fma_f32 v107,v25,v25,v107
s_nop 1
v_permlane16_swap_b32 v106,v107
v_add_f32 v106,v106,v107
v_mov_b32 v107,v106
s_nop 1
v_permlane32_swap_b32 v106,v107
v_add_f32 v106,v106,v107
ds_write_b32 v12,v106 offset:0
L 0
s_barrier
R 118,13,0
L 0
v_add_f32 v118,v118,v119
v_add_f32 v120,v120,v121
v_add_f32 v118,v118,v120
v_mul_f32 v118,s86,v118
buffer_store_dword v118,v14,s[36:39],s76 offen offset:0 sc0 sc1
s_mov_b32 s72,s69
s_mov_b32 s69,0x7fff0000
s_mov_b32 s66,0x7fff0000
s_mov_b32 s76,0x7fff0000
s_branch Lg2_gox_%=
Lg2_xn0_%=:
s_cmp_ge_u32 s54,256
s_cbranch_scc1 Lg2_fin0_%=
s_add_u32 m0,s59,4096
s_nop 0
buffer_load_dword v8,s[36:39],s58 offen offset:0 sc1 lds
buffer_store_dword v4,v3,s[36:39],s71 offen offset:0 sc0 sc1
s_add_u32 m0,s59,5120
s_nop 0
buffer_load_dword v8,s[36:39],s58 offen offset:0 sc1 lds
buffer_store_dword v4,v3,s[36:39],s71 offen offset:0 sc0 sc1
s_mov_b32 s83,0
v_lshlrev_b32 v106,16,v122
v_and_b32 v107,0xffff0000,v122
v_lshlrev_b32 v108,16,v123
v_and_b32 v109,0xffff0000,v123
v_lshlrev_b32 v110,16,v124
v_and_b32 v111,0xffff0000,v124
v_lshlrev_b32 v112,16,v125
v_and_b32 v113,0xffff0000,v125
v_fma_f32 v106,v106,v99,v98
v_fma_f32 v107,v107,v99,v98
v_fma_f32 v108,v108,v99,v98
v_fma_f32 v109,v109,v99,v98
v_fma_f32 v110,v110,v99,v98
v_fma_f32 v111,v111,v99,v98
v_fma_f32 v112,v112,v99,v98
v_fma_f32 v113,v113,v99,v98
v_add_f32 v18,v18,v106
v_add_f32 v19,v19,v107
v_add_f32 v20,v20,v108
v_add_f32 v21,v21,v109
v_add_f32 v22,v22,v110
v_add_f32 v23,v23,v111
v_add_f32 v24,v24,v112
v_add_f32 v25,v25,v113
v_cvt_pk_bf16_f32 v114,v18,v19
v_cvt_pk_bf16_f32 v115,v20,v21
v_cvt_pk_bf16_f32 v116,v22,v23
v_cvt_pk_bf16_f32 v117,v24,v25
buffer_store_dwordx4 v[114:117],v0,s[36:39],s66 offen offset:0 sc0 sc1
v_add_f32 v106,v18,v19
v_fma_f32 v107,v18,v18,s87
v_fma_f32 v107,v19,v19,v107
v_add_f32 v106,v106,v20
v_fma_f32 v107,v20,v20,v107
v_add_f32 v106,v106,v21
v_fma_f32 v107,v21,v21,v107
v_add_f32 v106,v106,v22
v_fma_f32 v107,v22,v22,v107
v_add_f32 v106,v106,v23
v_fma_f32 v107,v23,v23,v107
v_add_f32 v106,v106,v24
v_fma_f32 v107,v24,v24,v107
v_add_f32 v106,v106,v25
v_fma_f32 v107,v25,v25,v107
s_nop 1
v_permlane16_swap_b32 v106,v107
v_add_f32 v106,v106,v107
v_mov_b32 v107,v106
s_nop 1
v_permlane32_swap_b32 v106,v107
v_add_f32 v106,v106,v107
ds_write_b32 v12,v106 offset:0
L 0
s_barrier
R 118,13,0
L 0
v_add_f32 v118,v118,v119
v_add_f32 v120,v120,v121
v_add_f32 v118,v118,v120
v_mul_f32 v118,s86,v118
buffer_store_dword v118,v14,s[36:39],s76 offen offset:0 sc0 sc1
s_mov_b32 s72,s69
s_mov_b32 s69,0x7fff0000
s_mov_b32 s66,0x7fff0000
s_mov_b32 s76,0x7fff0000
s_branch Lg2_poll_%=
Lg2_fin0_%=:
v_lshlrev_b32 v106,16,v122
v_and_b32 v107,0xffff0000,v122
v_lshlrev_b32 v108,16,v123
v_and_b32 v109,0xffff0000,v123
v_lshlrev_b32 v110,16,v124
v_and_b32 v111,0xffff0000,v124
v_lshlrev_b32 v112,16,v125
v_and_b32 v113,0xffff0000,v125
v_fma_f32 v106,v106,v99,v98
v_fma_f32 v107,v107,v99,v98
v_fma_f32 v108,v108,v99,v98
v_fma_f32 v109,v109,v99,v98
v_fma_f32 v110,v110,v99,v98
v_fma_f32 v111,v111,v99,v98
v_fma_f32 v112,v112,v99,v98
v_fma_f32 v113,v113,v99,v98
v_add_f32 v18,v18,v106
v_add_f32 v19,v19,v107
v_add_f32 v20,v20,v108
v_add_f32 v21,v21,v109
v_add_f32 v22,v22,v110
v_add_f32 v23,v23,v111
v_add_f32 v24,v24,v112
v_add_f32 v25,v25,v113
v_cvt_pk_bf16_f32 v114,v18,v19
v_cvt_pk_bf16_f32 v115,v20,v21
v_cvt_pk_bf16_f32 v116,v22,v23
v_cvt_pk_bf16_f32 v117,v24,v25
buffer_store_dwordx4 v[114:117],v0,s[36:39],s66 offen offset:0 sc0 sc1
v_add_f32 v106,v18,v19
v_fma_f32 v107,v18,v18,s87
v_fma_f32 v107,v19,v19,v107
v_add_f32 v106,v106,v20
v_fma_f32 v107,v20,v20,v107
v_add_f32 v106,v106,v21
v_fma_f32 v107,v21,v21,v107
v_add_f32 v106,v106,v22
v_fma_f32 v107,v22,v22,v107
v_add_f32 v106,v106,v23
v_fma_f32 v107,v23,v23,v107
v_add_f32 v106,v106,v24
v_fma_f32 v107,v24,v24,v107
v_add_f32 v106,v106,v25
v_fma_f32 v107,v25,v25,v107
s_nop 1
v_permlane16_swap_b32 v106,v107
v_add_f32 v106,v106,v107
v_mov_b32 v107,v106
s_nop 1
v_permlane32_swap_b32 v106,v107
v_add_f32 v106,v106,v107
ds_write_b32 v12,v106 offset:0
L 0
s_barrier
R 118,13,0
L 0
v_add_f32 v118,v118,v119
v_add_f32 v120,v120,v121
v_add_f32 v118,v118,v120
v_mul_f32 v118,s86,v118
buffer_store_dword v118,v14,s[36:39],s76 offen offset:0 sc0 sc1
s_mov_b32 s72,s69
s_mov_b32 s69,0x7fff0000
s_mov_b32 s66,0x7fff0000
s_mov_b32 s76,0x7fff0000
s_branch Lg2_done_%=
Lg2_K1_%=:
Lg2_L1_%=:
v_mfma_f32_16x16x32_bf16 v[30:33],a[4:7],v[34:37],0
s_add_u32 m0,s59,3072
R 54,2,5120
v_lshlrev_b32 v106,16,v122
M 30,a[12:15],38
buffer_load_dword v8,s[36:39],s58 offen offset:0 sc1 lds
v_mfma_f32_16x16x32_bf16 v[26:29],a[0:3],v[34:37],0
s_mov_b32 s67,s65
R 58,2,6144
v_and_b32 v107,0xffff0000,v122
M 26,a[8:11],38
L 3
v_lshlrev_b32 v108,16,v123
v_and_b32 v109,0xffff0000,v123
M 26,a[16:19],42
s_mov_b32 s77,s75
R 62,2,7168
v_lshlrev_b32 v110,16,v124
M 26,a[24:27],46
s_add_u32 s65,s65,s62
v_and_b32 v111,0xffff0000,v124
v_lshlrev_b32 v112,16,v125
M 30,a[20:23],42
s_add_u32 s75,s75,s63
R 66,2,8192
v_and_b32 v113,0xffff0000,v125
v_fma_f32 v106,v106,v99,v98
M 30,a[28:31],46
L 3
v_fma_f32 v107,v107,v99,v98
v_fma_f32 v108,v108,v99,v98
M 30,a[36:39],50
R 70,2,9216
v_fma_f32 v109,v109,v99,v98
M 30,a[44:47],54
v_fma_f32 v110,v110,v99,v98
v_fma_f32 v111,v111,v99,v98
M 26,a[32:35],50
R 74,2,10240
v_fma_f32 v112,v112,v99,v98
M 26,a[40:43],54
L 3
v_fma_f32 v113,v113,v99,v98
v_add_f32 v18,v18,v106
M 26,a[48:51],58
R 78,2,11264
v_add_f32 v19,v19,v107
M 26,a[56:59],62
v_add_f32 v20,v20,v108
v_add_f32 v21,v21,v109
v_add_f32 v22,v22,v110
M 30,a[52:55],58
R 82,2,12288
v_add_f32 v23,v23,v111
M 30,a[60:63],62
L 3
v_add_f32 v24,v24,v112
v_add_f32 v25,v25,v113
M 30,a[68:71],66
R 86,2,13312
v_cvt_pk_bf16_f32 v114,v18,v19
M 30,a[76:79],70
buffer_load_dwordx4 v[126:129],v0,s[36:39],s74 offen offset:0 sc1
M 26,a[64:67],66
s_add_u32 s74,s74,s62
R 90,2,14336
v_cvt_pk_bf16_f32 v115,v20,v21
M 26,a[72:75],70
L 3
buffer_load_dwordx2 v[102:103],v11,s[36:39],s73 offen offset:8 sc1
v_cvt_pk_bf16_f32 v116,v22,v23
M 26,a[80:83],74
s_waitcnt vmcnt(2)
s_add_u32 s73,s73,s64
R 94,2,15360
M 26,a[88:91],78
s_barrier
ds_read_b32 v7,v6 offset:3072
v_cvt_pk_bf16_f32 v117,v24,v25
M 30,a[84:87],74
R 34,2,16384
v_add_f32 v106,v18,v19
M 30,a[92:95],78
L 4
buffer_store_dword v4,v3,s[36:39],s70 offen offset:0 sc0 sc1
M 30,a[100:103],82
s_mov_b32 s70,0x7fff0000
R 38,2,17408
v_fma_f32 v107,v18,v18,s87
M 30,a[108:111],86
buffer_store_dwordx4 v[114:117],v0,s[36:39],s66 offen offset:0 sc0 sc1
v_fma_f32 v107,v19,v19,v107
)"
R"(M 26,a[96:99],82
R 42,2,18432
v_add_f32 v106,v106,v20
v_fma_f32 v107,v20,v20,v107
M 26,a[104:107],86
L 4
L 3
v_cmp_eq_u32 vcc,s53,v7
M 26,a[112:115],90
R 46,2,19456
v_add_f32 v106,v106,v21
M 26,a[120:123],94
s_or_b64 vcc,vcc,s[94:95]
v_fma_f32 v107,v21,v21,v107
v_add_f32 v106,v106,v22
M 30,a[116:119],90
R 50,2,20480
v_fma_f32 v107,v22,v22,v107
M 30,a[124:127],94
s_cmp_eq_u32 vcc_lo,-1
L 3
v_add_f32 v106,v106,v23
s_cbranch_scc0 Lg2_X1_%=
M 30,a[132:135],34
s_and_b32 s78,s54,127
s_cmp_eq_u32 s78,s85
s_cselect_b64 s[40:41],-1,0
R 54,2,21504
M 30,a[140:143],38
s_add_u32 m0,s57,0
s_add_u32 s56,s55,0
s_add_u32 s58,s58,s61
v_cndmask_b32 v17,v15,v16,s[40:41]
M 26,a[128:131],34
s_add_u32 s54,s54,s46
R 58,2,22528
v_fma_f32 v107,v23,v23,v107
M 26,a[136:139],38
L 3
D 17,56,0
M 26,a[144:147],42
s_add_u32 s56,s55,256
R 62,2,23552
v_add_f32 v106,v106,v24
M 26,a[152:155],46
s_and_b32 s79,s54,127
v_fma_f32 v107,v24,v24,v107
v_add_f32 v106,v106,v25
M 30,a[148:151],42
s_cmp_eq_u32 s79,0
R 66,2,24576
v_fma_f32 v107,v25,v25,v107
M 30,a[156:159],46
L 3
D 17,56,1024
M 30,a[164:167],50
s_cselect_b32 s94,0x3300,0
s_add_u32 s56,s55,512
R 70,2,25600
M 30,a[172:175],54
s_cmp_eq_u32 s79,127
v_permlane16_swap_b32 v106,v107
v_add_f32 v106,v106,v107
M 26,a[160:163],50
s_cselect_b32 s94,0xcc00,s94
R 74,2,26624
v_mov_b32 v107,v106
M 26,a[168:171],54
L 3
D 17,56,2048
M 26,a[176:179],58
s_add_u32 s56,s55,768
R 78,2,27648
M 26,a[184:187],62
v_permlane32_swap_b32 v106,v107
v_add_f32 v106,v106,v107
M 30,a[180:183],58
R 82,2,28672
ds_write_b32 v12,v106 offset:0
M 30,a[188:191],62
L 4
D 17,56,3072
M 30,a[196:199],66
s_add_u32 m0,s59,1024
s_add_u32 s56,s55,20480
R 86,2,29696
M 30,a[204:207],70
buffer_load_dword v8,s[36:39],s58 offen offset:0 sc1 lds
M 26,a[192:195],66
s_add_u32 m0,s57,16384
R 90,2,30720
v_cndmask_b32 v17,v10,v9,s[40:41]
M 26,a[200:203],70
L 4
L 2
s_sub_u32 s57,s84,s57
M 26,a[208:211],74
s_waitcnt vmcnt(0)
R 94,2,31744
M 26,a[216:219],78
s_barrier
D 17,56,0
M 30,a[212:215],74
s_add_u32 s56,s55,20736
R 34,1,0
ds_read_b32 v7,v6 offset:1024
M 30,a[220:223],78
L 4
buffer_store_dword v4,v3,s[36:39],s70 offen offset:0 sc0 sc1
M 30,a[228:231],82
s_mov_b32 s70,s69
R 38,1,1024
R 118,13,0
M 30,a[236:239],86
D 17,56,1024
M 26,a[224:227],82
s_add_u32 s56,s55,20992
s_mov_b32 s69,s68
R 42,1,2048
M 26,a[232:235],86
L 5
D 17,56,2048
M 26,a[240:243],90
L 3
s_add_u32 s56,s55,21248
R 46,1,3072
v_cmp_eq_u32 vcc,s53,v7
M 26,a[248:251],94
D 17,56,3072
M 30,a[244:247],90
s_add_u32 s55,s55,s60
s_add_u32 s68,s68,s63
L 2
s_or_b64 vcc,vcc,s[94:95]
s_cmp_eq_u32 vcc_lo,-1
R 50,1,4096
v_add_f32 v118,v118,v119
v_add_f32 v120,v120,v121
v_add_f32 v118,v118,v120
v_mul_f32 v118,s86,v118
M 30,a[252:255],94
s_nop 0
buffer_store_dword v118,v14,s[36:39],s76 offen offset:0 sc0 sc1
buffer_store_dword v4,v3,s[36:39],s71 offen offset:0 sc0 sc1
buffer_store_dword v4,v3,s[36:39],s71 offen offset:0 sc0 sc1
s_cbranch_scc1 Lg2_F0_%=
s_branch Lg2_K0_%=
Lg2_X1_%=:
M 30,a[132:135],34
s_add_u32 m0,s59,1024
R 54,2,21504
v_fma_f32 v107,v23,v23,v107
M 30,a[140:143],38
buffer_load_dword v8,s[36:39],s58 offen offset:0 sc1 lds
M 26,a[128:131],34
R 58,2,22528
v_add_f32 v106,v106,v24
M 26,a[136:139],38
L 3
v_fma_f32 v107,v24,v24,v107
v_add_f32 v106,v106,v25
M 26,a[144:147],42
R 62,2,23552
v_fma_f32 v107,v25,v25,v107
M 26,a[152:155],46
M 30,a[148:151],42
R 66,2,24576
M 30,a[156:159],46
L 3
v_permlane16_swap_b32 v106,v107
v_add_f32 v106,v106,v107
M 30,a[164:167],50
R 70,2,25600
v_mov_b32 v107,v106
M 30,a[172:175],54
M 26,a[160:163],50
R 74,2,26624
M 26,a[168:171],54
L 3
v_permlane32_swap_b32 v106,v107
M 26,a[176:179],58
R 78,2,27648
v_add_f32 v106,v106,v107
M 26,a[184:187],62
ds_write_b32 v12,v106 offset:0
M 30,a[180:183],58
R 82,2,28672
M 30,a[188:191],62
L 4
M 30,a[196:199],66
R 86,2,29696
M 30,a[204:207],70
M 26,a[192:195],66
R 90,2,30720
M 26,a[200:203],70
L 4
L 3
M 26,a[208:211],74
s_waitcnt vmcnt(0)
R 94,2,31744
M 26,a[216:219],78
s_barrier
R 118,13,0
ds_read_b32 v7,v6 offset:1024
M 30,a[212:215],74
buffer_store_dword v4,v3,s[36:39],s70 offen offset:0 sc0 sc1
M 30,a[220:223],78
L 4
s_mov_b32 s70,s69
s_mov_b32 s69,s68
M 30,a[228:231],82
s_add_u32 s68,s68,s63
M 30,a[236:239],86
M 26,a[224:227],82
M 26,a[232:235],86
L 2
L 1
L 0
M 26,a[240:243],90
v_add_f32 v118,v118,v119
v_add_f32 v120,v120,v121
v_add_f32 v118,v118,v120
M 26,a[248:251],94
v_cmp_eq_u32 vcc,s53,v7
v_mul_f32 v118,s86,v118
M 30,a[244:247],90
buffer_store_dword v118,v14,s[36:39],s76 offen offset:0 sc0 sc1
M 30,a[252:255],94
s_nop 0
s_or_b64 vcc,vcc,s[94:95]
s_mov_b32 s80,0
s_cmp_eq_u32 vcc_lo,-1
s_cbranch_scc0 Lg2_xn1_%=
s_and_b32 s78,s54,127
s_cmp_eq_u32 s78,s85
s_cselect_b64 s[40:41],-1,0
v_cndmask_b32 v17,v15,v16,s[40:41]
s_add_u32 m0,s57,0
s_add_u32 s56,s55,0
D 17,56,0
v_lshlrev_b32 v106,16,v126
v_and_b32 v107,0xffff0000,v126
v_lshlrev_b32 v108,16,v127
v_and_b32 v109,0xffff0000,v127
v_lshlrev_b32 v110,16,v128
s_add_u32 s56,s55,256
D 17,56,1024
v_and_b32 v111,0xffff0000,v128
v_lshlrev_b32 v112,16,v129
v_and_b32 v113,0xffff0000,v129
v_fma_f32 v106,v106,v103,v102
v_fma_f32 v107,v107,v103,v102
s_add_u32 s56,s55,512
D 17,56,2048
v_fma_f32 v108,v108,v103,v102
v_fma_f32 v109,v109,v103,v102
v_fma_f32 v110,v110,v103,v102
v_fma_f32 v111,v111,v103,v102
v_fma_f32 v112,v112,v103,v102
s_add_u32 s56,s55,768
D 17,56,3072
v_fma_f32 v113,v113,v103,v102
v_add_f32 v26,v26,v106
v_add_f32 v27,v27,v107
v_add_f32 v28,v28,v108
v_add_f32 v29,v29,v109
s_add_u32 s58,s58,s61
v_cndmask_b32 v17,v10,v9,s[40:41]
s_add_u32 m0,s57,16384
s_add_u32 s56,s55,20480
D 17,56,0
v_add_f32 v30,v30,v110
v_add_f32 v31,v31,v111
v_add_f32 v32,v32,v112
v_add_f32 v33,v33,v113
v_cvt_pk_bf16_f32 v114,v26,v27
s_add_u32 s56,s55,20736
D 17,56,1024
v_cvt_pk_bf16_f32 v115,v28,v29
v_cvt_pk_bf16_f32 v116,v30,v31
v_cvt_pk_bf16_f32 v117,v32,v33
s_add_u32 s56,s55,20992
D 17,56,2048
s_add_u32 s56,s55,21248
D 17,56,3072
buffer_store_dwordx4 v[114:117],v0,s[36:39],s67 offen offset:0 sc0 sc1
v_add_f32 v106,v26,v27
v_fma_f32 v107,v26,v26,s87
s_add_u32 s55,s55,s60
s_sub_u32 s57,s84,s57
s_add_u32 s54,s54,s46
v_fma_f32 v107,v27,v27,v107
v_add_f32 v106,v106,v28
v_fma_f32 v107,v28,v28,v107
v_add_f32 v106,v106,v29
v_fma_f32 v107,v29,v29,v107
v_add_f32 v106,v106,v30
v_fma_f32 v107,v30,v30,v107
v_add_f32 v106,v106,v31
v_fma_f32 v107,v31,v31,v107
v_add_f32 v106,v106,v32
v_fma_f32 v107,v32,v32,v107
v_add_f32 v106,v106,v33
v_fma_f32 v107,v33,v33,v107
s_nop 1
v_permlane16_swap_b32 v106,v107
v_add_f32 v106,v106,v107
v_mov_b32 v107,v106
s_nop 1
v_permlane32_swap_b32 v106,v107
v_add_f32 v106,v106,v107
ds_write_b32 v12,v106 offset:2048
L 0
s_barrier
R 118,13,2048
L 0
v_add_f32 v118,v118,v119
v_add_f32 v120,v120,v121
v_add_f32 v118,v118,v120
v_mul_f32 v118,s86,v118
buffer_store_dword v118,v14,s[36:39],s77 offen offset:0 sc0 sc1
s_mov_b32 s72,s69
s_mov_b32 s69,0x7fff0000
s_mov_b32 s67,0x7fff0000
s_mov_b32 s77,0x7fff0000
s_branch Lg2_gox_%=
Lg2_xn1_%=:
s_cmp_ge_u32 s54,256
s_cbranch_scc1 Lg2_fin1_%=
s_add_u32 m0,s59,4096
s_nop 0
buffer_load_dword v8,s[36:39],s58 offen offset:0 sc1 lds
buffer_store_dword v4,v3,s[36:39],s71 offen offset:0 sc0 sc1
s_add_u32 m0,s59,5120
s_nop 0
buffer_load_dword v8,s[36:39],s58 offen offset:0 sc1 lds
buffer_store_dword v4,v3,s[36:39],s71 offen offset:0 sc0 sc1
s_mov_b32 s83,0
v_lshlrev_b32 v106,16,v126
v_and_b32 v107,0xffff0000,v126
v_lshlrev_b32 v108,16,v127
v_and_b32 v109,0xffff0000,v127
v_lshlrev_b32 v110,16,v128
v_and_b32 v111,0xffff0000,v128
v_lshlrev_b32 v112,16,v129
v_and_b32 v113,0xffff0000,v129
v_fma_f32 v106,v106,v103,v102
v_fma_f32 v107,v107,v103,v102
v_fma_f32 v108,v108,v103,v102
v_fma_f32 v109,v109,v103,v102
v_fma_f32 v110,v110,v103,v102
v_fma_f32 v111,v111,v103,v102
v_fma_f32 v112,v112,v103,v102
v_fma_f32 v113,v113,v103,v102
v_add_f32 v26,v26,v106
v_add_f32 v27,v27,v107
v_add_f32 v28,v28,v108
v_add_f32 v29,v29,v109
v_add_f32 v30,v30,v110
v_add_f32 v31,v31,v111
v_add_f32 v32,v32,v112
v_add_f32 v33,v33,v113
v_cvt_pk_bf16_f32 v114,v26,v27
v_cvt_pk_bf16_f32 v115,v28,v29
v_cvt_pk_bf16_f32 v116,v30,v31
v_cvt_pk_bf16_f32 v117,v32,v33
buffer_store_dwordx4 v[114:117],v0,s[36:39],s67 offen offset:0 sc0 sc1
v_add_f32 v106,v26,v27
v_fma_f32 v107,v26,v26,s87
v_fma_f32 v107,v27,v27,v107
v_add_f32 v106,v106,v28
v_fma_f32 v107,v28,v28,v107
v_add_f32 v106,v106,v29
v_fma_f32 v107,v29,v29,v107
v_add_f32 v106,v106,v30
v_fma_f32 v107,v30,v30,v107
v_add_f32 v106,v106,v31
v_fma_f32 v107,v31,v31,v107
v_add_f32 v106,v106,v32
v_fma_f32 v107,v32,v32,v107
v_add_f32 v106,v106,v33
v_fma_f32 v107,v33,v33,v107
s_nop 1
v_permlane16_swap_b32 v106,v107
v_add_f32 v106,v106,v107
v_mov_b32 v107,v106
s_nop 1
v_permlane32_swap_b32 v106,v107
v_add_f32 v106,v106,v107
ds_write_b32 v12,v106 offset:2048
L 0
s_barrier
R 118,13,2048
L 0
v_add_f32 v118,v118,v119
v_add_f32 v120,v120,v121
v_add_f32 v118,v118,v120
v_mul_f32 v118,s86,v118
buffer_store_dword v118,v14,s[36:39],s77 offen offset:0 sc0 sc1
s_mov_b32 s72,s69
s_mov_b32 s69,0x7fff0000
s_mov_b32 s67,0x7fff0000
s_mov_b32 s77,0x7fff0000
s_branch Lg2_poll_%=
Lg2_fin1_%=:
v_lshlrev_b32 v106,16,v126
v_and_b32 v107,0xffff0000,v126
v_lshlrev_b32 v108,16,v127
v_and_b32 v109,0xffff0000,v127
v_lshlrev_b32 v110,16,v128
v_and_b32 v111,0xffff0000,v128
v_lshlrev_b32 v112,16,v129
v_and_b32 v113,0xffff0000,v129
v_fma_f32 v106,v106,v103,v102
v_fma_f32 v107,v107,v103,v102
v_fma_f32 v108,v108,v103,v102
v_fma_f32 v109,v109,v103,v102
v_fma_f32 v110,v110,v103,v102
v_fma_f32 v111,v111,v103,v102
v_fma_f32 v112,v112,v103,v102
v_fma_f32 v113,v113,v103,v102
v_add_f32 v26,v26,v106
v_add_f32 v27,v27,v107
v_add_f32 v28,v28,v108
v_add_f32 v29,v29,v109
v_add_f32 v30,v30,v110
v_add_f32 v31,v31,v111
v_add_f32 v32,v32,v112
v_add_f32 v33,v33,v113
v_cvt_pk_bf16_f32 v114,v26,v27
v_cvt_pk_bf16_f32 v115,v28,v29
v_cvt_pk_bf16_f32 v116,v30,v31
v_cvt_pk_bf16_f32 v117,v32,v33
buffer_store_dwordx4 v[114:117],v0,s[36:39],s67 offen offset:0 sc0 sc1
v_add_f32 v106,v26,v27
v_fma_f32 v107,v26,v26,s87
v_fma_f32 v107,v27,v27,v107
v_add_f32 v106,v106,v28
v_fma_f32 v107,v28,v28,v107
v_add_f32 v106,v106,v29
v_fma_f32 v107,v29,v29,v107
v_add_f32 v106,v106,v30
v_fma_f32 v107,v30,v30,v107
v_add_f32 v106,v106,v31
v_fma_f32 v107,v31,v31,v107
v_add_f32 v106,v106,v32
v_fma_f32 v107,v32,v32,v107
v_add_f32 v106,v106,v33
v_fma_f32 v107,v33,v33,v107
s_nop 1
v_permlane16_swap_b32 v106,v107
v_add_f32 v106,v106,v107
v_mov_b32 v107,v106
s_nop 1
v_permlane32_swap_b32 v106,v107
v_add_f32 v106,v106,v107
ds_write_b32 v12,v106 offset:2048
L 0
s_barrier
R 118,13,2048
L 0
v_add_f32 v118,v118,v119
v_add_f32 v120,v120,v121
v_add_f32 v118,v118,v120
v_mul_f32 v118,s86,v118
buffer_store_dword v118,v14,s[36:39],s77 offen offset:0 sc0 sc1
s_mov_b32 s72,s69
s_mov_b32 s69,0x7fff0000
s_mov_b32 s67,0x7fff0000
s_mov_b32 s77,0x7fff0000
s_branch Lg2_done_%=
Lg2_done_%=:
s_waitcnt vmcnt(2)
s_barrier
buffer_store_dword v4,v3,s[36:39],s70 offen offset:0 sc0 sc1
s_waitcnt vmcnt(1)
s_barrier
buffer_store_dword v4,v3,s[36:39],s72 offen offset:0 sc0 sc1
s_waitcnt vmcnt(0) lgkmcnt(0)
s_barrier
Lg2_end_%=:
s_mov_b32 %0,s53
Lcode_g12_e:
.purgem M
.purgem R
.purgem T
.purgem D
.purgem L
)"
  : "=&s"(epOut) : "s"(ws), "s"(__builtin_amdgcn_readfirstlane(sWave)), "s"(__builtin_amdgcn_readfirstlane(sG0)), "s"(__builtin_amdgcn_readfirstlane(sStep)), "s"(__builtin_amdgcn_readfirstlane(sOutOff)), "s"(__builtin_amdgcn_readfirstlane(sFoutOff)), "s"(__builtin_amdgcn_readfirstlane(sAuxOff)), "s"(__builtin_amdgcn_readfirstlane(sWw)), "s"(__builtin_amdgcn_readfirstlane(sBuf)), "s"(__builtin_amdgcn_readfirstlane(sExp)), "v"(vL16), "v"(vFlag), "v"(vSnapOff), "v"(vSnapRd), "v"(vWd0), "v"(vWd1), "v"(vWr0), "v"(vWr1), "v"(vSp), "v"(vOutS), "v"(vAux0), "v"(vAux1), "v"(vAux2), "v"(vAux3), "v"(vAn), "v"(vAe), "s"(w12) : "v0", "v1", "v2", "v3", "v4", "v5", "v6", "v7", "v8", "v9", "v10", "v11", "v12", "v13", "v14", "v15", "v16", "v17", "v18", "v19", "v20", "v21", "v22", "v23", "v24", "v25", "v26", "v27", "v28", "v29", "v30", "v31", "v32", "v33", "v34", "v35", "v36", "v37", "v38", "v39", "v40", "v41", "v42", "v43", "v44", "v45", "v46", "v47", "v48", "v49", "v50", "v51", "v52", "v53", "v54", "v55", "v56", "v57", "v58", "v59", "v60", "v61", "v62", "v63", "v64", "v65", "v66", "v67", "v68", "v69", "v70", "v71", "v72", "v73", "v74", "v75", "v76", "v77", "v78", "v79", "v80", "v81", "v82", "v83", "v84", "v85", "v86", "v87", "v88", "v89", "v90", "v91", "v92", "v93", "v94", "v95", "v96", "v97", "v98", "v99", "v100", "v101", "v102", "v103", "v104", "v105", "v106", "v107", "v108", "v109", "v110", "v111", "v112", "v113", "v114", "v115", "v116", "v117", "v118", "v119", "v120", "v121", "v122", "v123", "v124", "v125", "v126", "v127", "v128", "v129", "v130", "v131", "v132", "v133", "v134", "v135", "v136", "v137", "v138", "v139", "v140", "v141", "v142", "v143", "v144", "v145", "v146", "v147", "v148", "v149", "v150", "v151", "v152", "v153", "v154", "v155", "v156", "v157", "v158", "v159", "v160", "v161", "v162", "v163", "v164", "v165", "v166", "v167", "v168", "v169", "v170", "v171", "v172", "v173", "v174", "v175", "v176", "v177", "v178", "v179", "v180", "v181", "v182", "v183", "v184", "v185", "v186", "v187", "v188", "v189", "v190", "v191", "v192", "v193", "v194", "v195", "v196", "v197", "v198", "v199", "v200", "v201", "v202", "v203", "v204", "v205", "v206", "v207", "v208", "v209", "v210", "v211", "v212", "v213", "v214", "v215", "v216", "v217", "v218", "v219", "v220", "v221", "v222", "v223", "v224", "v225", "v226", "v227", "v228", "v229", "v230", "v231", "a0", "a1", "a2", "a3", "a4", "a5", "a6", "a7", "a8", "a9", "a10", "a11", "a12", "a13", "a14", "a15", "a16", "a17", "a18", "a19", "a20", "a21", "a22", "a23", "a24", "a25", "a26", "a27", "a28", "a29", "a30", "a31", "a32", "a33", "a34", "a35", "a36", "a37", "a38", "a39", "a40", "a41", "a42", "a43", "a44", "a45", "a46", "a47", "a48", "a49", "a50", "a51", "a52", "a53", "a54", "a55", "a56", "a57", "a58", "a59", "a60", "a61", "a62", "a63", "a64", "a65", "a66", "a67", "a68", "a69", "a70", "a71", "a72", "a73", "a74", "a75", "a76", "a77", "a78", "a79", "a80", "a81", "a82", "a83", "a84", "a85", "a86", "a87", "a88", "a89", "a90", "a91", "a92", "a93", "a94", "a95", "a96", "a97", "a98", "a99", "a100", "a101", "a102", "a103", "a104", "a105", "a106", "a107", "a108", "a109", "a110", "a111", "a112", "a113", "a114", "a115", "a116", "a117", "a118", "a119", "a120", "a121", "a122", "a123", "a124", "a125", "a126", "a127", "a128", "a129", "a130", "a131", "a132", "a133", "a134", "a135", "a136", "a137", "a138", "a139", "a140", "a141", "a142", "a143", "a144", "a145", "a146", "a147", "a148", "a149", "a150", "a151", "a152", "a153", "a154", "a155", "a156", "a157", "a158", "a159", "a160", "a161", "a162", "a163", "a164", "a165", "a166", "a167", "a168", "a169", "a170", "a171", "a172", "a173", "a174", "a175", "a176", "a177", "a178", "a179", "a180", "a181", "a182", "a183", "a184", "a185", "a186", "a187", "a188", "a189", "a190", "a191", "a192", "a193", "a194", "a195", "a196", "a197", "a198", "a199", "a200", "a201", "a202", "a203", "a204", "a205", "a206", "a207", "a208", "a209", "a210", "a211", "a212", "a213", "a214", "a215", "a216", "a217", "a218", "a219", "a220", "a221", "a222", "a223", "a224", "a225", "a226", "a227", "a228", "a229", "a230", "a231", "a232", "a233", "a234", "a235", "a236", "a237", "a238", "a239", "a240", "a241", "a242", "a243", "a244", "a245", "a246", "a247", "a248", "a249", "a250", "a251", "a252", "a253", "a254", "a255", "s36", "s37", "s38", "s39", "s40", "s41", "s42", "s43", "s44", "s45", "s46", "s47", "s48", "s49", "s50", "s51", "s52", "s53", "s54", "s55", "s56", "s57", "s58", "s59", "s60", "s61", "s62", "s63", "s64", "s65", "s66", "s67", "s68", "s69", "s70", "s71", "s72", "s73", "s74", "s75", "s76", "s77", "s78", "s79", "s80", "s81", "s82", "s83", "s84", "s85", "s86", "s87", "s88", "s89", "s90", "s91", "s92", "s93", "s94", "s95", "s96", "s97", "vcc", "scc", "memory");
    ep = epOut;
    if (kind == 1 && 5 + RHELP + (rep >> 1) < NR4) { kind = 4; kh = rep & 1; rep = 5 + RHELP + (rep >> 1); }
  }
  if (kind == 4) {
    const unsigned sStep = rep < 5 ? 5 : rep == 5 ? 6 : NR4;
    COMMON(10, (kh * 9 + fj) * 128, 2560, 143360, 2048, 448, 0);
    const int cs = slice;
    const unsigned sOutOff = (kh ? OFF_P1 : OFF_P0) + (cs * 4 + wave) * 1024, sFoutOff = OFF_FP + (kh * 8 + cs) * 128;
    const unsigned sAuxOff = (kh ? OFF_XP : OFF_X1) + (cs * 4 + wave) * 1024, sWw = (kh * 1792 + 16 * wave) * 2048 + cs * 256, sBuf = kh * 57344;
    const int zu = 108 + (rep + 2) % 5;
    const unsigned vSp = lg * 2048 + l15 * 4, vSn = rep >= 5 ? OOB : !ALLZ ? (zu * 16 + wave * 4 + lg) * 2048 + (cs * 2 + kh) * 128 + l15 * 8 :
                         ((rep + 5 * (wave >> 1)) * 16 + (wave & 1) * 8 + (ln >> 3)) * 2048 + (cs * 2 + kh) * 128 + (ln & 7) * 16;
    (void)vWr2;
    const unsigned sExp = __builtin_amdgcn_readfirstlane(ep);
    asm volatile(
R"(.macro M d,w,x
v_mfma_f32_16x16x32_bf16 v[\d:\d+3], \w, v[\x:\x+3], v[\d:\d+3]
.endm
.macro R d,a,o
ds_read_b128 v[\d:\d+3], v\a offset:\o
.endm
.macro T d,a,o
ds_read_b64_tr_b16 \d, v\a offset:\o
.endm
.macro D a,s,o
buffer_load_dwordx4 v\a, s[36:39], s\s offen offset:\o sc1 lds
.endm
.macro L c
s_waitcnt lgkmcnt(\c)
.endm
Lcode_g4_b:
s_mov_b64 s[36:37],%0
s_mov_b32 s38,111243392
s_mov_b32 s39,0x00020000
s_mov_b32 s44,%1
s_mov_b32 s45,%2
s_mov_b32 s46,%3
s_mov_b32 s47,%4
s_mov_b32 s48,%5
s_mov_b32 s49,%6
s_mov_b32 s50,%7
s_mov_b32 s51,%8
s_mov_b32 s93,%9
s_mov_b32 s53,%10
v_mov_b32 v0,%11
v_mov_b32 v3,%12
v_mov_b32 v5,%13
v_mov_b32 v6,%14
v_mov_b32 v10,%15
v_mov_b32 v11,%16
v_mov_b32 v12,%17
v_mov_b32 v13,%18
v_mov_b32 v9,%19
v_mov_b32 v7,%20
s_mov_b64 s[40:41],%21
s_mov_b32 s42,7340032
s_mov_b32 s43,0x00020000
s_lshl_b32 s57,s44,11
s_cmp_eq_u32 s45,175
s_cbranch_scc1 Lg4_pre_%=
s_add_u32 s55,s50,0
s_add_u32 m0,s57,40960
s_nop 0
buffer_load_dwordx4 v10,s[40:43],s55 offen offset:0  lds
s_add_u32 m0,s57,41984
s_nop 0
buffer_load_dwordx4 v11,s[40:43],s55 offen offset:0  lds
s_add_u32 m0,s57,49024
s_nop 0
buffer_load_dwordx4 v10,s[40:43],s55 offen offset:128  lds
s_add_u32 m0,s57,50048
s_nop 0
buffer_load_dwordx4 v11,s[40:43],s55 offen offset:128  lds
s_add_u32 s55,s50,131072
s_add_u32 m0,s57,57344
s_nop 0
buffer_load_dwordx4 v10,s[40:43],s55 offen offset:0  lds
s_add_u32 m0,s57,58368
s_nop 0
buffer_load_dwordx4 v11,s[40:43],s55 offen offset:0  lds
s_add_u32 m0,s57,65408
s_nop 0
buffer_load_dwordx4 v10,s[40:43],s55 offen offset:128  lds
s_add_u32 m0,s57,66432
s_nop 0
buffer_load_dwordx4 v11,s[40:43],s55 offen offset:128  lds
s_add_u32 s55,s50,262144
s_add_u32 m0,s57,73728
s_nop 0
buffer_load_dwordx4 v10,s[40:43],s55 offen offset:0  lds
s_add_u32 m0,s57,74752
s_nop 0
buffer_load_dwordx4 v11,s[40:43],s55 offen offset:0  lds
s_add_u32 m0,s57,81792
s_nop 0
buffer_load_dwordx4 v10,s[40:43],s55 offen offset:128  lds
s_add_u32 m0,s57,82816
s_nop 0
buffer_load_dwordx4 v11,s[40:43],s55 offen offset:128  lds
s_add_u32 s55,s50,393216
s_add_u32 m0,s57,90112
s_nop 0
buffer_load_dwordx4 v10,s[40:43],s55 offen offset:0  lds
s_add_u32 m0,s57,91136
s_nop 0
buffer_load_dwordx4 v11,s[40:43],s55 offen offset:0  lds
s_add_u32 m0,s57,98176
s_nop 0
buffer_load_dwordx4 v10,s[40:43],s55 offen offset:128  lds
s_add_u32 m0,s57,99200
s_nop 0
buffer_load_dwordx4 v11,s[40:43],s55 offen offset:128  lds
s_add_u32 s55,s50,524288
s_add_u32 m0,s57,106496
s_nop 0
buffer_load_dwordx4 v10,s[40:43],s55 offen offset:0  lds
s_add_u32 m0,s57,107520
s_nop 0
buffer_load_dwordx4 v11,s[40:43],s55 offen offset:0  lds
s_add_u32 m0,s57,114560
s_nop 0
buffer_load_dwordx4 v10,s[40:43],s55 offen offset:128  lds
s_add_u32 m0,s57,115584
s_nop 0
buffer_load_dwordx4 v11,s[40:43],s55 offen offset:128  lds
s_add_u32 s55,s50,655360
s_add_u32 m0,s57,122880
s_nop 0
buffer_load_dwordx4 v10,s[40:43],s55 offen offset:0  lds
s_add_u32 m0,s57,123904
s_nop 0
buffer_load_dwordx4 v11,s[40:43],s55 offen offset:0  lds
s_add_u32 m0,s57,130944
s_nop 0
buffer_load_dwordx4 v10,s[40:43],s55 offen offset:128  lds
s_add_u32 m0,s57,131968
s_nop 0
buffer_load_dwordx4 v11,s[40:43],s55 offen offset:128  lds
s_waitcnt vmcnt(20) lgkmcnt(0)
s_barrier
s_add_u32 s55,s50,786432
s_add_u32 m0,s57,139264
s_nop 0
buffer_load_dwordx4 v10,s[40:43],s55 offen offset:0  lds
s_add_u32 m0,s57,140288
s_nop 0
buffer_load_dwordx4 v11,s[40:43],s55 offen offset:0  lds
s_add_u32 m0,s57,147328
s_nop 0
buffer_load_dwordx4 v10,s[40:43],s55 offen offset:128  lds
s_add_u32 m0,s57,148352
s_nop 0
buffer_load_dwordx4 v11,s[40:43],s55 offen offset:128  lds
v_add_u32 v18,40960,v12
v_add_u32 v19,40960,v13
T a[0:1],18,0
T a[2:3],18,512
T a[4:5],19,0
T a[6:7],19,512
T a[8:9],18,4096
T a[10:11],18,4608
T a[12:13],19,4096
T a[14:15],19,4608
s_waitcnt vmcnt(20) lgkmcnt(0)
s_barrier
s_add_u32 s55,s50,917504
s_add_u32 m0,s57,40960
s_nop 0
buffer_load_dwordx4 v10,s[40:43],s55 offen offset:0  lds
s_add_u32 m0,s57,41984
s_nop 0
buffer_load_dwordx4 v11,s[40:43],s55 offen offset:0  lds
s_add_u32 m0,s57,49024
s_nop 0
buffer_load_dwordx4 v10,s[40:43],s55 offen offset:128  lds
s_add_u32 m0,s57,50048
s_nop 0
buffer_load_dwordx4 v11,s[40:43],s55 offen offset:128  lds
v_add_u32 v18,57344,v12
v_add_u32 v19,57344,v13
T a[16:17],18,0
T a[18:19],18,512
T a[20:21],19,0
T a[22:23],19,512
T a[24:25],18,4096
T a[26:27],18,4608
T a[28:29],19,4096
T a[30:31],19,4608
s_waitcnt vmcnt(20) lgkmcnt(0)
s_barrier
s_add_u32 s55,s50,1048576
s_add_u32 m0,s57,57344
s_nop 0
buffer_load_dwordx4 v10,s[40:43],s55 offen offset:0  lds
s_add_u32 m0,s57,58368
s_nop 0
buffer_load_dwordx4 v11,s[40:43],s55 offen offset:0  lds
s_add_u32 m0,s57,65408
s_nop 0
buffer_load_dwordx4 v10,s[40:43],s55 offen offset:128  lds
s_add_u32 m0,s57,66432
s_nop 0
buffer_load_dwordx4 v11,s[40:43],s55 offen offset:128  lds
v_add_u32 v18,73728,v12
v_add_u32 v19,73728,v13
T a[32:33],18,0
T a[34:35],18,512
T a[36:37],19,0
T a[38:39],19,512
T a[40:41],18,4096
T a[42:43],18,4608
T a[44:45],19,4096
T a[46:47],19,4608
s_waitcnt vmcnt(20) lgkmcnt(0)
s_barrier
s_add_u32 s55,s50,1179648
s_add_u32 m0,s57,73728
s_nop 0
buffer_load_dwordx4 v10,s[40:43],s55 offen offset:0  lds
s_add_u32 m0,s57,74752
s_nop 0
buffer_load_dwordx4 v11,s[40:43],s55 offen offset:0  lds
s_add_u32 m0,s57,81792
s_nop 0
buffer_load_dwordx4 v10,s[40:43],s55 offen offset:128  lds
s_add_u32 m0,s57,82816
s_nop 0
buffer_load_dwordx4 v11,s[40:43],s55 offen offset:128  lds
v_add_u32 v18,90112,v12
v_add_u32 v19,90112,v13
T a[48:49],18,0
T a[50:51],18,512
T a[52:53],19,0
T a[54:55],19,512
T a[56:57],18,4096
T a[58:59],18,4608
T a[60:61],19,4096
T a[62:63],19,4608
s_waitcnt vmcnt(20) lgkmcnt(0)
s_barrier
s_add_u32 s55,s50,1310720
s_add_u32 m0,s57,90112
s_nop 0
buffer_load_dwordx4 v10,s[40:43],s55 offen offset:0  lds
s_add_u32 m0,s57,91136
s_nop 0
buffer_load_dwordx4 v11,s[40:43],s55 offen offset:0  lds
s_add_u32 m0,s57,98176
s_nop 0
buffer_load_dwordx4 v10,s[40:43],s55 offen offset:128  lds
s_add_u32 m0,s57,99200
s_nop 0
buffer_load_dwordx4 v11,s[40:43],s55 offen offset:128  lds
v_add_u32 v18,106496,v12
v_add_u32 v19,106496,v13
T a[64:65],18,0
T a[66:67],18,512
T a[68:69],19,0
T a[70:71],19,512
T a[72:73],18,4096
T a[74:75],18,4608
T a[76:77],19,4096
T a[78:79],19,4608
s_waitcnt vmcnt(20) lgkmcnt(0)
s_barrier
s_add_u32 s55,s50,1441792
s_add_u32 m0,s57,106496
s_nop 0
buffer_load_dwordx4 v10,s[40:43],s55 offen offset:0  lds
s_add_u32 m0,s57,107520
s_nop 0
buffer_load_dwordx4 v11,s[40:43],s55 offen offset:0  lds
s_add_u32 m0,s57,114560
s_nop 0
buffer_load_dwordx4 v10,s[40:43],s55 offen offset:128  lds
s_add_u32 m0,s57,115584
s_nop 0
buffer_load_dwordx4 v11,s[40:43],s55 offen offset:128  lds
v_add_u32 v18,122880,v12
v_add_u32 v19,122880,v13
T a[80:81],18,0
T a[82:83],18,512
T a[84:85],19,0
T a[86:87],19,512
T a[88:89],18,4096
T a[90:91],18,4608
T a[92:93],19,4096
T a[94:95],19,4608
s_waitcnt vmcnt(20) lgkmcnt(0)
s_barrier
s_add_u32 s55,s50,1572864
s_add_u32 m0,s57,122880
s_nop 0
buffer_load_dwordx4 v10,s[40:43],s55 offen offset:0  lds
s_add_u32 m0,s57,123904
s_nop 0
buffer_load_dwordx4 v11,s[40:43],s55 offen offset:0  lds
s_add_u32 m0,s57,130944
s_nop 0
buffer_load_dwordx4 v10,s[40:43],s55 offen offset:128  lds
s_add_u32 m0,s57,131968
s_nop 0
buffer_load_dwordx4 v11,s[40:43],s55 offen offset:128  lds
v_add_u32 v18,139264,v12
v_add_u32 v19,139264,v13
T a[96:97],18,0
T a[98:99],18,512
T a[100:101],19,0
T a[102:103],19,512
T a[104:105],18,4096
T a[106:107],18,4608
T a[108:109],19,4096
T a[110:111],19,4608
s_waitcnt vmcnt(20) lgkmcnt(0)
s_barrier
s_add_u32 s55,s50,1703936
s_add_u32 m0,s57,139264
s_nop 0
buffer_load_dwordx4 v10,s[40:43],s55 offen offset:0  lds
s_add_u32 m0,s57,140288
s_nop 0
buffer_load_dwordx4 v11,s[40:43],s55 offen offset:0  lds
s_add_u32 m0,s57,147328
s_nop 0
buffer_load_dwordx4 v10,s[40:43],s55 offen offset:128  lds
s_add_u32 m0,s57,148352
s_nop 0
buffer_load_dwordx4 v11,s[40:43],s55 offen offset:128  lds
v_add_u32 v18,40960,v12
v_add_u32 v19,40960,v13
T a[112:113],18,0
T a[114:115],18,512
T a[116:117],19,0
T a[118:119],19,512
T a[120:121],18,4096
T a[122:123],18,4608
T a[124:125],19,4096
T a[126:127],19,4608
s_waitcnt vmcnt(20) lgkmcnt(0)
s_barrier
s_add_u32 s55,s50,1835008
s_add_u32 m0,s57,40960
s_nop 0
buffer_load_dwordx4 v10,s[40:43],s55 offen offset:0  lds
s_add_u32 m0,s57,41984
s_nop 0
buffer_load_dwordx4 v11,s[40:43],s55 offen offset:0  lds
s_add_u32 m0,s57,49024
s_nop 0
buffer_load_dwordx4 v10,s[40:43],s55 offen offset:128  lds
s_add_u32 m0,s57,50048
s_nop 0
buffer_load_dwordx4 v11,s[40:43],s55 offen offset:128  lds
v_add_u32 v18,57344,v12
v_add_u32 v19,57344,v13
T a[128:129],18,0
T a[130:131],18,512
T a[132:133],19,0
T a[134:135],19,512
T a[136:137],18,4096
T a[138:139],18,4608
T a[140:141],19,4096
T a[142:143],19,4608
s_waitcnt vmcnt(20) lgkmcnt(0)
s_barrier
s_add_u32 s55,s50,1966080
s_add_u32 m0,s57,57344
s_nop 0
buffer_load_dwordx4 v10,s[40:43],s55 offen offset:0  lds
s_add_u32 m0,s57,58368
s_nop 0
buffer_load_dwordx4 v11,s[40:43],s55 offen offset:0  lds
s_add_u32 m0,s57,65408
s_nop 0
buffer_load_dwordx4 v10,s[40:43],s55 offen offset:128  lds
s_add_u32 m0,s57,66432
s_nop 0
buffer_load_dwordx4 v11,s[40:43],s55 offen offset:128  lds
v_add_u32 v18,73728,v12
v_add_u32 v19,73728,v13
T a[144:145],18,0
T a[146:147],18,512
T a[148:149],19,0
T a[150:151],19,512
T a[152:153],18,4096
T a[154:155],18,4608
T a[156:157],19,4096
T a[158:159],19,4608
s_waitcnt vmcnt(20) lgkmcnt(0)
s_barrier
s_add_u32 s55,s50,2097152
s_add_u32 m0,s57,73728
s_nop 0
buffer_load_dwordx4 v10,s[40:43],s55 offen offset:0  lds
s_add_u32 m0,s57,74752
s_nop 0
buffer_load_dwordx4 v11,s[40:43],s55 offen offset:0  lds
s_add_u32 m0,s57,81792
s_nop 0
buffer_load_dwordx4 v10,s[40:43],s55 offen offset:128  lds
s_add_u32 m0,s57,82816
s_nop 0
buffer_load_dwordx4 v11,s[40:43],s55 offen offset:128  lds
v_add_u32 v18,90112,v12
v_add_u32 v19,90112,v13
T a[160:161],18,0
T a[162:163],18,512
T a[164:165],19,0
T a[166:167],19,512
T a[168:169],18,4096
T a[170:171],18,4608
T a[172:173],19,4096
T a[174:175],19,4608
s_waitcnt vmcnt(20) lgkmcnt(0)
s_barrier
s_add_u32 s55,s50,2228224
s_add_u32 m0,s57,90112
s_nop 0
buffer_load_dwordx4 v10,s[40:43],s55 offen offset:0  lds
s_add_u32 m0,s57,91136
s_nop 0
buffer_load_dwordx4 v11,s[40:43],s55 offen offset:0  lds
s_add_u32 m0,s57,98176
s_nop 0
buffer_load_dwordx4 v10,s[40:43],s55 offen offset:128  lds
s_add_u32 m0,s57,99200
s_nop 0
buffer_load_dwordx4 v11,s[40:43],s55 offen offset:128  lds
v_add_u32 v18,106496,v12
v_add_u32 v19,106496,v13
T a[176:177],18,0
T a[178:179],18,512
T a[180:181],19,0
T a[182:183],19,512
T a[184:185],18,4096
T a[186:187],18,4608
T a[188:189],19,4096
T a[190:191],19,4608
s_waitcnt vmcnt(20) lgkmcnt(0)
s_barrier
s_add_u32 s55,s50,2359296
s_add_u32 m0,s57,106496
s_nop 0
buffer_load_dwordx4 v10,s[40:43],s55 offen offset:0  lds
s_add_u32 m0,s57,107520
s_nop 0
buffer_load_dwordx4 v11,s[40:43],s55 offen offset:0  lds
s_add_u32 m0,s57,114560
s_nop 0
buffer_load_dwordx4 v10,s[40:43],s55 offen offset:128  lds
s_add_u32 m0,s57,115584
s_nop 0
buffer_load_dwordx4 v11,s[40:43],s55 offen offset:128  lds
v_add_u32 v18,122880,v12
v_add_u32 v19,122880,v13
T a[192:193],18,0
T a[194:195],18,512
T a[196:197],19,0
T a[198:199],19,512
T a[200:201],18,4096
T a[202:203],18,4608
T a[204:205],19,4096
T a[206:207],19,4608
s_waitcnt vmcnt(20) lgkmcnt(0)
s_barrier
s_add_u32 s55,s50,2490368
s_add_u32 m0,s57,122880
s_nop 0
buffer_load_dwordx4 v10,s[40:43],s55 offen offset:0  lds
s_add_u32 m0,s57,123904
s_nop 0
buffer_load_dwordx4 v11,s[40:43],s55 offen offset:0  lds
s_add_u32 m0,s57,130944
s_nop 0
buffer_load_dwordx4 v10,s[40:43],s55 offen offset:128  lds
s_add_u32 m0,s57,131968
s_nop 0
buffer_load_dwordx4 v11,s[40:43],s55 offen offset:128  lds
v_add_u32 v18,139264,v12
v_add_u32 v19,139264,v13
T a[208:209],18,0
T a[210:211],18,512
T a[212:213],19,0
T a[214:215],19,512
T a[216:217],18,4096
T a[218:219],18,4608
T a[220:221],19,4096
T a[222:223],19,4608
s_waitcnt vmcnt(20) lgkmcnt(0)
s_barrier
s_add_u32 s55,s50,2621440
s_add_u32 m0,s57,139264
s_nop 0
buffer_load_dwordx4 v10,s[40:43],s55 offen offset:0  lds
s_add_u32 m0,s57,140288
s_nop 0
buffer_load_dwordx4 v11,s[40:43],s55 offen offset:0  lds
s_add_u32 m0,s57,147328
s_nop 0
buffer_load_dwordx4 v10,s[40:43],s55 offen offset:128  lds
s_add_u32 m0,s57,148352
s_nop 0
buffer_load_dwordx4 v11,s[40:43],s55 offen offset:128  lds
v_add_u32 v18,40960,v12
v_add_u32 v19,40960,v13
T a[224:225],18,0
T a[226:227],18,512
T a[228:229],19,0
T a[230:231],19,512
T a[232:233],18,4096
T a[234:235],18,4608
T a[236:237],19,4096
T a[238:239],19,4608
s_waitcnt vmcnt(20) lgkmcnt(0)
s_barrier
)"
R"(s_add_u32 s55,s50,2752512
s_add_u32 m0,s57,40960
s_nop 0
buffer_load_dwordx4 v10,s[40:43],s55 offen offset:0  lds
s_add_u32 m0,s57,41984
s_nop 0
buffer_load_dwordx4 v11,s[40:43],s55 offen offset:0  lds
s_add_u32 m0,s57,49024
s_nop 0
buffer_load_dwordx4 v10,s[40:43],s55 offen offset:128  lds
s_add_u32 m0,s57,50048
s_nop 0
buffer_load_dwordx4 v11,s[40:43],s55 offen offset:128  lds
v_add_u32 v18,57344,v12
v_add_u32 v19,57344,v13
T a[240:241],18,0
T a[242:243],18,512
T a[244:245],19,0
T a[246:247],19,512
T a[248:249],18,4096
T a[250:251],18,4608
T a[252:253],19,4096
T a[254:255],19,4608
Lg4_pre_%=:
s_waitcnt vmcnt(20) lgkmcnt(0)
s_barrier
s_add_u32 s55,s50,2883584
s_add_u32 m0,s57,57344
s_nop 0
buffer_load_dwordx4 v10,s[40:43],s55 offen offset:0  lds
s_add_u32 m0,s57,58368
s_nop 0
buffer_load_dwordx4 v11,s[40:43],s55 offen offset:0  lds
s_add_u32 m0,s57,65408
s_nop 0
buffer_load_dwordx4 v10,s[40:43],s55 offen offset:128  lds
s_add_u32 m0,s57,66432
s_nop 0
buffer_load_dwordx4 v11,s[40:43],s55 offen offset:128  lds
v_add_u32 v18,73728,v12
v_add_u32 v19,73728,v13
T v[64:65],18,0
T v[66:67],18,512
T v[68:69],19,0
T v[70:71],19,512
T v[72:73],18,4096
T v[74:75],18,4608
T v[76:77],19,4096
T v[78:79],19,4608
s_waitcnt vmcnt(20) lgkmcnt(0)
s_barrier
s_add_u32 s55,s50,3014656
s_add_u32 m0,s57,73728
s_nop 0
buffer_load_dwordx4 v10,s[40:43],s55 offen offset:0  lds
s_add_u32 m0,s57,74752
s_nop 0
buffer_load_dwordx4 v11,s[40:43],s55 offen offset:0  lds
s_add_u32 m0,s57,81792
s_nop 0
buffer_load_dwordx4 v10,s[40:43],s55 offen offset:128  lds
s_add_u32 m0,s57,82816
s_nop 0
buffer_load_dwordx4 v11,s[40:43],s55 offen offset:128  lds
v_add_u32 v18,90112,v12
v_add_u32 v19,90112,v13
T v[80:81],18,0
T v[82:83],18,512
T v[84:85],19,0
T v[86:87],19,512
T v[88:89],18,4096
T v[90:91],18,4608
T v[92:93],19,4096
T v[94:95],19,4608
s_waitcnt vmcnt(20) lgkmcnt(0)
s_barrier
s_add_u32 s55,s50,3145728
s_add_u32 m0,s57,90112
s_nop 0
buffer_load_dwordx4 v10,s[40:43],s55 offen offset:0  lds
s_add_u32 m0,s57,91136
s_nop 0
buffer_load_dwordx4 v11,s[40:43],s55 offen offset:0  lds
s_add_u32 m0,s57,98176
s_nop 0
buffer_load_dwordx4 v10,s[40:43],s55 offen offset:128  lds
s_add_u32 m0,s57,99200
s_nop 0
buffer_load_dwordx4 v11,s[40:43],s55 offen offset:128  lds
v_add_u32 v18,106496,v12
v_add_u32 v19,106496,v13
T v[96:97],18,0
T v[98:99],18,512
T v[100:101],19,0
T v[102:103],19,512
T v[104:105],18,4096
T v[106:107],18,4608
T v[108:109],19,4096
T v[110:111],19,4608
s_waitcnt vmcnt(20) lgkmcnt(0)
s_barrier
s_add_u32 s55,s50,3276800
s_add_u32 m0,s57,106496
s_nop 0
buffer_load_dwordx4 v10,s[40:43],s55 offen offset:0  lds
s_add_u32 m0,s57,107520
s_nop 0
buffer_load_dwordx4 v11,s[40:43],s55 offen offset:0  lds
s_add_u32 m0,s57,114560
s_nop 0
buffer_load_dwordx4 v10,s[40:43],s55 offen offset:128  lds
s_add_u32 m0,s57,115584
s_nop 0
buffer_load_dwordx4 v11,s[40:43],s55 offen offset:128  lds
v_add_u32 v18,122880,v12
v_add_u32 v19,122880,v13
T v[112:113],18,0
T v[114:115],18,512
T v[116:117],19,0
T v[118:119],19,512
T v[120:121],18,4096
T v[122:123],18,4608
T v[124:125],19,4096
T v[126:127],19,4608
s_waitcnt vmcnt(20) lgkmcnt(0)
s_barrier
s_add_u32 s55,s50,3407872
s_add_u32 m0,s57,122880
s_nop 0
buffer_load_dwordx4 v10,s[40:43],s55 offen offset:0  lds
s_add_u32 m0,s57,123904
s_nop 0
buffer_load_dwordx4 v11,s[40:43],s55 offen offset:0  lds
s_add_u32 m0,s57,130944
s_nop 0
buffer_load_dwordx4 v10,s[40:43],s55 offen offset:128  lds
s_add_u32 m0,s57,131968
s_nop 0
buffer_load_dwordx4 v11,s[40:43],s55 offen offset:128  lds
v_add_u32 v18,139264,v12
v_add_u32 v19,139264,v13
T v[128:129],18,0
T v[130:131],18,512
T v[132:133],19,0
T v[134:135],19,512
T v[136:137],18,4096
T v[138:139],18,4608
T v[140:141],19,4096
T v[142:143],19,4608
s_waitcnt vmcnt(20) lgkmcnt(0)
s_barrier
s_add_u32 s55,s50,3538944
s_add_u32 m0,s57,139264
s_nop 0
buffer_load_dwordx4 v10,s[40:43],s55 offen offset:0  lds
s_add_u32 m0,s57,140288
s_nop 0
buffer_load_dwordx4 v11,s[40:43],s55 offen offset:0  lds
s_add_u32 m0,s57,147328
s_nop 0
buffer_load_dwordx4 v10,s[40:43],s55 offen offset:128  lds
s_add_u32 m0,s57,148352
s_nop 0
buffer_load_dwordx4 v11,s[40:43],s55 offen offset:128  lds
v_add_u32 v18,40960,v12
v_add_u32 v19,40960,v13
T v[144:145],18,0
T v[146:147],18,512
T v[148:149],19,0
T v[150:151],19,512
T v[152:153],18,4096
T v[154:155],18,4608
T v[156:157],19,4096
T v[158:159],19,4608
s_waitcnt vmcnt(20) lgkmcnt(0)
s_barrier
v_add_u32 v18,57344,v12
v_add_u32 v19,57344,v13
T v[160:161],18,0
T v[162:163],18,512
T v[164:165],19,0
T v[166:167],19,512
T v[168:169],18,4096
T v[170:171],18,4608
T v[172:173],19,4096
T v[174:175],19,4608
s_waitcnt vmcnt(16) lgkmcnt(0)
s_barrier
v_add_u32 v18,73728,v12
v_add_u32 v19,73728,v13
T v[176:177],18,0
T v[178:179],18,512
T v[180:181],19,0
T v[182:183],19,512
T v[184:185],18,4096
T v[186:187],18,4608
T v[188:189],19,4096
T v[190:191],19,4608
s_waitcnt vmcnt(12) lgkmcnt(0)
s_barrier
v_add_u32 v18,90112,v12
v_add_u32 v19,90112,v13
T v[192:193],18,0
T v[194:195],18,512
T v[196:197],19,0
T v[198:199],19,512
T v[200:201],18,4096
T v[202:203],18,4608
T v[204:205],19,4096
T v[206:207],19,4608
s_waitcnt vmcnt(8) lgkmcnt(0)
s_barrier
v_add_u32 v18,106496,v12
v_add_u32 v19,106496,v13
T v[208:209],18,0
T v[210:211],18,512
T v[212:213],19,0
T v[214:215],19,512
T v[216:217],18,4096
T v[218:219],18,4608
T v[220:221],19,4096
T v[222:223],19,4608
s_waitcnt vmcnt(4) lgkmcnt(0)
s_barrier
v_add_u32 v18,122880,v12
v_add_u32 v19,122880,v13
T v[224:225],18,0
T v[226:227],18,512
T v[228:229],19,0
T v[230:231],19,512
T v[232:233],18,4096
T v[234:235],18,4608
T v[236:237],19,4096
T v[238:239],19,4608
s_waitcnt vmcnt(0) lgkmcnt(0)
s_barrier
v_add_u32 v18,139264,v12
v_add_u32 v19,139264,v13
T v[240:241],18,0
T v[242:243],18,512
T v[244:245],19,0
T v[246:247],19,512
T v[248:249],18,4096
T v[250:251],18,4608
T v[252:253],19,4096
T v[254:255],19,4608
L 0
s_barrier
v_mov_b32 v4,s53
s_mov_b32 s82,0
s_mov_b32 s80,0
s_mov_b32 s54,s45
s_mul_i32 s57,s44,7168
s_mul_i32 s55,s45,114688
s_add_u32 s55,s55,s57
s_add_u32 s55,s55,40894464
s_mul_i32 s57,s44,7168
s_mul_i32 s60,s46,114688
s_mul_i32 s61,s46,2560
s_mul_i32 s62,s46,32768
s_mul_i32 s63,s46,2048
s_mul_i32 s64,s46,512
s_lshl_b32 s59,s44,8
s_add_u32 s59,s59,143360
s_mul_i32 s58,s45,2560
s_add_u32 s58,s58,109891584
s_mul_i32 s65,s45,32768
s_add_u32 s65,s65,s47
s_mul_i32 s68,s45,2048
s_add_u32 s68,s68,s48
s_mul_i32 s73,s45,512
s_add_u32 s73,s73,106954752
s_mul_i32 s74,s45,32768
s_add_u32 s74,s74,s49
s_mul_i32 s75,s45,1024
s_add_u32 s75,s75,107479040
s_mov_b32 s66,0x7fff0000
s_mov_b32 s67,0x7fff0000
s_mov_b32 s69,0x7fff0000
s_mov_b32 s76,0x7fff0000
s_mov_b32 s77,0x7fff0000
s_mov_b32 s70,0x7fff0000
s_mov_b32 s71,0x7fff0000
s_mov_b32 s72,0x7fff0000
v_mov_b32 v1,v0
v_add_u32 v2,28672,v0
s_mov_b32 s84,28672
s_mov_b32 s86,0x3a800000
s_mov_b32 s87,0x3727c5ac
s_mov_b32 s88,0x409d30fc
s_mov_b32 s89,0xbeeff558
s_lshr_b32 s85,s44,1
s_mul_i32 s85,s85,127
s_add_u32 s55,s55,s51
s_mul_i32 s78,s45,114688
s_sub_u32 s60,s55,s78
s_add_u32 s50,s93,224
s_add_u32 s93,s93,170
s_cmp_lt_u32 s45,224
s_cselect_b32 s63,224,-1
s_cmp_lt_u32 s45,170
s_cselect_b32 s63,170,s63
s_cselect_b32 s93,s93,s50
s_cselect_b32 s88,6,8
s_cselect_b32 s89,224,-1
v_lshlrev_b32 v8,22,v0
v_and_b32 v8,0x80000000,v8
v_or_b32 v8,v8,v5
v_add_u32 v2,57344,v0
s_lshl_b32 s84,s57,1
s_add_u32 s84,s84,57344
s_mov_b64 s[86:87],%22
s_mov_b64 s[40:41],%22
s_mov_b32 s42,4194304
s_and_b32 s78,s47,65535
s_lshr_b32 s78,s78,4
s_sub_u32 s85,s47,s78
s_add_u32 s85,s85,4194304
s_add_u32 s45,s47,5832704
s_mul_i32 s73,s44,1280
s_add_u32 s73,s73,114688
v_lshrrev_b32 v62,4,v9
v_and_b32 v62,3,v62
v_mul_u32_u24 v62,288,v62
v_lshrrev_b32 v63,7,v9
v_and_b32 v5,12,v9
v_add3_u32 v5,v5,v62,v63
v_add_u32 v5,s73,v5
v_mov_b32 v10,0
v_mov_b32 v11,0
v_mov_b32 v12,0
v_mov_b32 v13,0
s_mov_b32 s79,1638400
buffer_store_dwordx4 v[10:13],v7,s[40:43],s79 offen offset:0 sc0 sc1
s_mov_b32 s79,1966080
buffer_store_dwordx4 v[10:13],v7,s[40:43],s79 offen offset:0 sc0 sc1
s_mov_b32 s79,2293760
buffer_store_dwordx4 v[10:13],v7,s[40:43],s79 offen offset:0 sc0 sc1
s_mov_b32 s79,2621440
buffer_store_dwordx4 v[10:13],v7,s[40:43],s79 offen offset:0 sc0 sc1
s_mov_b32 s79,2949120
buffer_store_dwordx4 v[10:13],v7,s[40:43],s79 offen offset:0 sc0 sc1
s_mov_b32 s79,3276800
buffer_store_dwordx4 v[10:13],v7,s[40:43],s79 offen offset:0 sc0 sc1
s_mov_b32 s79,3604480
buffer_store_dwordx4 v[10:13],v7,s[40:43],s79 offen offset:0 sc0 sc1
s_mov_b32 s79,3932160
buffer_store_dwordx4 v[10:13],v7,s[40:43],s79 offen offset:0 sc0 sc1
s_getpc_b64 s[40:41]
Lg4_pc_%=:
s_lshl_b32 s78,s44,13
v_lshlrev_b32 v62,3,v0
v_add_u32 v62,s78,v62
v_min_u32 v62,Lcode_g4_e-Lg4_pc_%=-4,v62
s_mov_b32 m0,163584
s_nop 0
global_load_lds_dword v62,s[40:41]
s_add_u32 m0,s59,4096
s_nop 0
buffer_load_dword v8,s[36:39],s58 offen offset:0 sc1 lds
buffer_store_dword v4,v3,s[36:39],s71 offen offset:0 sc0 sc1
s_add_u32 m0,s59,5120
s_nop 0
buffer_load_dword v8,s[36:39],s58 offen offset:0 sc1 lds
buffer_store_dword v4,v3,s[36:39],s71 offen offset:0 sc0 sc1
s_mov_b32 s83,0
Lg4_poll_%=:
s_add_u32 s78,s83,2
s_and_b32 s78,s78,3
s_lshl_b32 s78,s78,10
s_add_u32 s78,s78,4096
s_add_u32 m0,s59,s78
s_nop 0
buffer_load_dword v8,s[36:39],s58 offen offset:0 sc1 lds
s_and_b32 s78,s83,3
s_lshl_b32 s78,s78,10
v_add_u32 v62,s78,v6
s_waitcnt vmcnt(4)
s_barrier
ds_read_b32 v7,v62 offset:4096
buffer_store_dword v4,v3,s[36:39],s70 offen offset:0 sc0 sc1
s_mov_b32 s70,s71
s_mov_b32 s71,s72
s_mov_b32 s72,0x7fff0000
s_add_u32 s83,s83,1
s_add_u32 s82,s82,1
L 0
v_cmp_eq_u32 vcc,s53,v7
s_nop 0
s_cmp_eq_u32 vcc_lo,-1
s_cbranch_scc1 Lg4_go_%=
s_cmp_lt_u32 s82,20000
s_cbranch_scc1 Lg4_poll_%=
Lg4_go_%=:
s_add_u32 m0,s57,0
s_add_u32 s56,s55,0
D 0,56,0
D 0,56,1024
D 0,56,2048
D 0,56,3072
s_add_u32 m0,s57,4096
s_add_u32 s56,s56,4096
D 0,56,0
D 0,56,1024
D 0,56,2048
s_add_u32 s61,s54,s46
s_cmp_ge_u32 s61,s63
s_cselect_b32 s61,s93,s61
s_cselect_b32 s46,s88,s46
s_cselect_b32 s63,s89,s63
s_cselect_b32 s93,s50,s93
s_cselect_b32 s88,8,s88
s_cselect_b32 s89,-1,s89
s_mul_i32 s58,s61,2560
s_add_u32 s58,s58,109891584
s_add_u32 m0,s59,2048
s_nop 0
buffer_load_dword v8,s[36:39],s58 offen offset:0 sc1 lds
s_add_u32 m0,s57,28672
s_add_u32 s56,s55,28672
D 0,56,0
D 0,56,1024
D 0,56,2048
D 0,56,3072
s_add_u32 m0,s57,32768
s_add_u32 s56,s56,4096
D 0,56,0
D 0,56,1024
D 0,56,2048
s_mul_i32 s55,s61,114688
s_add_u32 s55,s55,s60
s_sub_u32 s57,s84,s57
s_mov_b32 s54,s61
s_waitcnt vmcnt(7)
s_barrier
buffer_store_dword v4,v3,s[36:39],s70 offen offset:0 sc0 sc1
s_min_u32 s71,s71,s72
buffer_store_dword v4,v3,s[36:39],s71 offen offset:0 sc0 sc1
s_mov_b32 s70,0x7fff0000
s_mov_b32 s71,0x7fff0000
s_mov_b32 s72,0x7fff0000
s_cmp_eq_u32 s80,1
s_cbranch_scc1 Lg4_st1_%=
s_branch Lg4_st0_%=
Lg4_gox_%=:
s_add_u32 m0,s59,2048
s_nop 0
buffer_load_dword v8,s[36:39],s58 offen offset:0 sc1 lds
s_waitcnt vmcnt(0)
s_barrier
buffer_store_dword v4,v3,s[36:39],s70 offen offset:0 sc0 sc1
s_min_u32 s71,s71,s72
buffer_store_dword v4,v3,s[36:39],s71 offen offset:0 sc0 sc1
s_mov_b32 s70,0x7fff0000
s_mov_b32 s71,0x7fff0000
s_mov_b32 s72,0x7fff0000
s_cmp_eq_u32 s80,1
s_cbranch_scc1 Lg4_st1_%=
Lg4_st0_%=:
ds_read_b32 v7,v6 offset:2048
R 26,1,0
R 30,1,1024
R 34,1,2048
R 38,1,3072
R 42,1,4096
L 5
v_cmp_eq_u32 vcc,s53,v7
L 3
s_cmp_eq_u32 vcc_lo,-1
s_cbranch_scc1 Lg4_F0_%=
s_branch Lg4_L0_%=
Lg4_st1_%=:
ds_read_b32 v7,v6 offset:2048
R 26,2,0
R 30,2,1024
R 34,2,2048
R 38,2,3072
R 42,2,4096
L 5
v_cmp_eq_u32 vcc,s53,v7
L 3
s_cmp_eq_u32 vcc_lo,-1
s_cbranch_scc1 Lg4_F1_%=
s_branch Lg4_L1_%=
Lg4_F0_%=:
v_mfma_f32_16x16x32_bf16 v[14:17],a[4:7],v[26:29],0
s_add_u32 m0,s57,0
R 46,1,5120
v_lshlrev_b32 v62,16,v58
M 14,a[12:15],30
s_add_u32 s56,s55,0
v_add_f32 v18,v18,v62
v_and_b32 v63,0xffff0000,v58
v_mfma_f32_16x16x32_bf16 v[10:13],a[0:3],v[26:29],0
s_add_u32 s61,s54,s46
R 50,1,6144
v_add_f32 v19,v19,v63
M 10,a[8:11],30
L 3
D 0,56,0
)"
R"(M 10,a[16:19],34
s_cmp_ge_u32 s61,s63
R 54,1,7168
v_lshlrev_b32 v62,16,v59
M 10,a[24:27],38
s_cselect_b32 s61,s93,s61
v_add_f32 v20,v20,v62
v_and_b32 v63,0xffff0000,v59
M 14,a[20:23],34
s_cselect_b32 s46,s88,s46
R 26,1,8192
v_add_f32 v21,v21,v63
M 14,a[28:31],38
L 3
v_lshlrev_b32 v62,16,v60
v_add_f32 v22,v22,v62
M 14,a[36:39],42
s_cselect_b32 s63,s89,s63
R 30,1,9216
v_and_b32 v63,0xffff0000,v60
M 14,a[44:47],46
D 0,56,1024
M 10,a[32:35],42
s_cselect_b32 s93,s50,s93
R 34,1,10240
v_add_f32 v23,v23,v63
M 10,a[40:43],46
L 3
v_lshlrev_b32 v62,16,v61
v_add_f32 v24,v24,v62
M 10,a[48:51],50
s_cselect_b32 s88,8,s88
R 38,1,11264
v_and_b32 v63,0xffff0000,v61
M 10,a[56:59],54
buffer_load_dwordx4 v[58:61],v0,s[36:39],s74 offen offset:0 sc1
M 14,a[52:55],50
s_cselect_b32 s89,-1,s89
R 42,1,12288
v_add_f32 v25,v25,v63
M 14,a[60:63],54
L 3
v_cvt_pk_bf16_f32 v18,v18,v19
v_cvt_pk_bf16_f32 v19,v20,v21
M 14,a[68:71],26
s_mul_i32 s58,s61,2560
R 46,1,13312
v_cvt_pk_bf16_f32 v20,v22,v23
M 14,a[76:79],30
D 0,56,2048
M 10,a[64:67],26
s_add_u32 s58,s58,109891584
R 50,1,14336
v_cvt_pk_bf16_f32 v21,v24,v25
M 10,a[72:75],30
L 3
buffer_store_dwordx4 v[18:21],v0,s[36:39],s67 offen offset:0 sc0 sc1
M 10,a[80:83],34
s_lshl_b32 s62,s54,15
s_mov_b32 s66,s65
R 54,1,15360
M 10,a[88:91],38
s_cmp_ge_u32 s66,s45
s_cselect_b32 s51,1,-1
s_add_u32 s74,s49,s62
M 14,a[84:87],34
s_add_u32 s65,s47,s62
s_mov_b32 s54,s61
R 26,1,16384
M 14,a[92:95],38
L 3
M 14,a[100:103],42
R 30,1,17408
M 14,a[108:111],46
D 0,56,3072
M 10,a[96:99],42
s_add_u32 m0,s57,4096
s_add_u32 s56,s56,4096
R 34,1,18432
M 10,a[104:107],46
L 3
M 10,a[112:115],50
R 38,1,19456
M 10,a[120:123],54
M 14,a[116:119],50
R 42,1,20480
M 14,a[124:127],54
L 3
D 0,56,0
M 14,a[132:135],26
R 46,1,21504
M 14,a[140:143],30
M 10,a[128:131],26
R 50,1,22528
M 10,a[136:139],30
L 3
M 10,a[144:147],34
R 54,1,23552
M 10,a[152:155],38
M 14,a[148:151],34
R 26,1,24576
M 14,a[156:159],38
L 3
D 0,56,1024
M 14,a[164:167],42
R 30,1,25600
M 14,a[172:175],46
M 10,a[160:163],42
R 34,1,26624
M 10,a[168:171],46
L 3
M 10,a[176:179],50
s_waitcnt vmcnt(9)
R 38,1,27648
M 10,a[184:187],54
s_barrier
M 14,a[180:183],50
R 42,1,28672
M 14,a[188:191],54
L 3
D 0,56,2048
M 14,a[196:199],26
s_add_u32 m0,s59,0
s_add_u32 s56,s55,28672
R 46,1,29696
M 14,a[204:207],30
s_mul_i32 s55,s61,114688
s_add_u32 s55,s55,s60
M 10,a[192:195],26
R 50,1,30720
M 10,a[200:203],30
L 3
M 10,a[208:211],34
R 54,1,31744
M 10,a[216:219],38
M 14,a[212:215],34
R 26,1,32768
M 14,a[220:223],38
L 3
buffer_load_dword v8,s[36:39],s58 offen offset:0 sc1 lds
M 14,a[228:231],42
s_add_u32 m0,s57,28672
R 30,1,33792
M 14,a[236:239],46
D 0,56,0
M 10,a[224:227],42
R 34,1,34816
M 10,a[232:235],46
L 3
M 10,a[240:243],50
R 38,1,35840
M 10,a[248:251],54
M 14,a[244:247],50
R 42,1,36864
M 14,a[252:255],54
L 3
D 0,56,1024
M 14,v[68:71],26
R 46,1,37888
M 14,v[76:79],30
M 10,v[64:67],26
R 50,1,38912
M 10,v[72:75],30
L 3
M 10,v[80:83],34
R 54,1,39936
M 10,v[88:91],38
D 0,56,2048
M 14,v[84:87],34
R 26,1,40960
M 14,v[92:95],38
L 3
M 14,v[100:103],42
R 30,1,41984
M 14,v[108:111],46
M 10,v[96:99],42
R 34,1,43008
M 10,v[104:107],46
L 3
D 0,56,3072
M 10,v[112:115],50
s_add_u32 m0,s57,32768
s_add_u32 s56,s56,4096
R 38,1,44032
M 10,v[120:123],54
s_sub_u32 s57,s84,s57
M 14,v[116:119],50
R 42,1,45056
M 14,v[124:127],54
L 3
M 14,v[132:135],26
R 46,1,46080
M 14,v[140:143],30
D 0,56,0
M 10,v[128:131],26
R 50,1,47104
M 10,v[136:139],30
L 3
M 10,v[144:147],34
R 54,1,48128
M 10,v[152:155],38
M 14,v[148:151],34
R 26,1,49152
M 14,v[156:159],38
L 3
D 0,56,1024
M 14,v[164:167],42
R 30,1,50176
M 14,v[172:175],46
M 10,v[160:163],42
R 34,1,51200
M 10,v[168:171],46
L 3
M 10,v[176:179],50
R 38,1,52224
M 10,v[184:187],54
D 0,56,2048
M 14,v[180:183],50
R 42,1,53248
M 14,v[188:191],54
L 3
M 14,v[196:199],26
R 46,1,54272
M 14,v[204:207],30
M 10,v[192:195],26
R 50,1,55296
M 10,v[200:203],30
L 3
M 10,v[208:211],34
s_waitcnt vmcnt(7)
R 54,1,56320
M 10,v[216:219],38
s_barrier
ds_read_b32 v7,v6 offset:0
M 14,v[212:215],34
R 26,2,0
M 14,v[220:223],38
L 4
buffer_store_dword v4,v3,s[36:39],s69 offen offset:0 sc0 sc1
M 14,v[228:231],42
s_mov_b32 s69,s68
s_lshr_b32 s68,s62,4
R 30,2,1024
M 14,v[236:239],46
s_add_u32 s68,s68,s48
M 10,v[224:227],42
R 34,2,2048
M 10,v[232:235],46
L 4
L 3
M 10,v[240:243],50
R 38,2,3072
v_cmp_eq_u32 vcc,s53,v7
M 10,v[248:251],54
M 14,v[244:247],50
L 2
s_cmp_eq_u32 vcc_lo,s51
R 42,2,4096
M 14,v[252:255],54
s_cbranch_scc0 Lg4_K1_%=
Lg4_F1_%=:
v_mfma_f32_16x16x32_bf16 v[22:25],a[4:7],v[26:29],0
s_add_u32 m0,s57,0
R 46,2,5120
v_lshlrev_b32 v62,16,v58
M 22,a[12:15],30
s_add_u32 s56,s55,0
v_add_f32 v10,v10,v62
v_and_b32 v63,0xffff0000,v58
v_mfma_f32_16x16x32_bf16 v[18:21],a[0:3],v[26:29],0
s_add_u32 s61,s54,s46
R 50,2,6144
v_add_f32 v11,v11,v63
M 18,a[8:11],30
L 3
D 0,56,0
M 18,a[16:19],34
s_cmp_ge_u32 s61,s63
R 54,2,7168
v_lshlrev_b32 v62,16,v59
M 18,a[24:27],38
s_cselect_b32 s61,s93,s61
v_add_f32 v12,v12,v62
v_and_b32 v63,0xffff0000,v59
M 22,a[20:23],34
s_cselect_b32 s46,s88,s46
R 26,2,8192
v_add_f32 v13,v13,v63
M 22,a[28:31],38
L 3
v_lshlrev_b32 v62,16,v60
v_add_f32 v14,v14,v62
M 22,a[36:39],42
s_cselect_b32 s63,s89,s63
R 30,2,9216
v_and_b32 v63,0xffff0000,v60
M 22,a[44:47],46
D 0,56,1024
M 18,a[32:35],42
s_cselect_b32 s93,s50,s93
R 34,2,10240
v_add_f32 v15,v15,v63
M 18,a[40:43],46
L 3
v_lshlrev_b32 v62,16,v61
v_add_f32 v16,v16,v62
M 18,a[48:51],50
s_cselect_b32 s88,8,s88
R 38,2,11264
v_and_b32 v63,0xffff0000,v61
M 18,a[56:59],54
buffer_load_dwordx4 v[58:61],v0,s[36:39],s74 offen offset:0 sc1
M 22,a[52:55],50
s_cselect_b32 s89,-1,s89
R 42,2,12288
v_add_f32 v17,v17,v63
M 22,a[60:63],54
L 3
v_cvt_pk_bf16_f32 v10,v10,v11
v_cvt_pk_bf16_f32 v11,v12,v13
M 22,a[68:71],26
s_mul_i32 s58,s61,2560
R 46,2,13312
v_cvt_pk_bf16_f32 v12,v14,v15
M 22,a[76:79],30
D 0,56,2048
M 18,a[64:67],26
s_add_u32 s58,s58,109891584
R 50,2,14336
v_cvt_pk_bf16_f32 v13,v16,v17
M 18,a[72:75],30
L 3
buffer_store_dwordx4 v[10:13],v0,s[36:39],s66 offen offset:0 sc0 sc1
M 18,a[80:83],34
s_lshl_b32 s62,s54,15
s_mov_b32 s67,s65
R 54,2,15360
M 18,a[88:91],38
s_cmp_ge_u32 s67,s45
s_cselect_b32 s51,1,-1
s_add_u32 s74,s49,s62
M 22,a[84:87],34
s_add_u32 s65,s47,s62
s_mov_b32 s54,s61
R 26,2,16384
M 22,a[92:95],38
L 3
M 22,a[100:103],42
R 30,2,17408
M 22,a[108:111],46
D 0,56,3072
M 18,a[96:99],42
s_add_u32 m0,s57,4096
s_add_u32 s56,s56,4096
R 34,2,18432
M 18,a[104:107],46
L 3
M 18,a[112:115],50
R 38,2,19456
M 18,a[120:123],54
M 22,a[116:119],50
R 42,2,20480
M 22,a[124:127],54
L 3
D 0,56,0
M 22,a[132:135],26
R 46,2,21504
M 22,a[140:143],30
M 18,a[128:131],26
R 50,2,22528
M 18,a[136:139],30
L 3
M 18,a[144:147],34
R 54,2,23552
M 18,a[152:155],38
M 22,a[148:151],34
R 26,2,24576
M 22,a[156:159],38
L 3
D 0,56,1024
M 22,a[164:167],42
R 30,2,25600
M 22,a[172:175],46
M 18,a[160:163],42
R 34,2,26624
M 18,a[168:171],46
L 3
M 18,a[176:179],50
s_waitcnt vmcnt(9)
R 38,2,27648
M 18,a[184:187],54
s_barrier
M 22,a[180:183],50
R 42,2,28672
M 22,a[188:191],54
L 3
D 0,56,2048
M 22,a[196:199],26
s_add_u32 m0,s59,1024
s_add_u32 s56,s55,28672
R 46,2,29696
M 22,a[204:207],30
s_mul_i32 s55,s61,114688
s_add_u32 s55,s55,s60
M 18,a[192:195],26
R 50,2,30720
M 18,a[200:203],30
L 3
M 18,a[208:211],34
R 54,2,31744
M 18,a[216:219],38
M 22,a[212:215],34
R 26,2,32768
M 22,a[220:223],38
L 3
buffer_load_dword v8,s[36:39],s58 offen offset:0 sc1 lds
M 22,a[228:231],42
s_add_u32 m0,s57,28672
R 30,2,33792
M 22,a[236:239],46
D 0,56,0
M 18,a[224:227],42
R 34,2,34816
M 18,a[232:235],46
L 3
M 18,a[240:243],50
R 38,2,35840
M 18,a[248:251],54
M 22,a[244:247],50
R 42,2,36864
M 22,a[252:255],54
L 3
D 0,56,1024
M 22,v[68:71],26
R 46,2,37888
M 22,v[76:79],30
M 18,v[64:67],26
R 50,2,38912
M 18,v[72:75],30
L 3
M 18,v[80:83],34
R 54,2,39936
M 18,v[88:91],38
D 0,56,2048
M 22,v[84:87],34
R 26,2,40960
M 22,v[92:95],38
L 3
M 22,v[100:103],42
R 30,2,41984
M 22,v[108:111],46
M 18,v[96:99],42
R 34,2,43008
M 18,v[104:107],46
L 3
D 0,56,3072
M 18,v[112:115],50
s_add_u32 m0,s57,32768
s_add_u32 s56,s56,4096
R 38,2,44032
M 18,v[120:123],54
s_sub_u32 s57,s84,s57
M 22,v[116:119],50
R 42,2,45056
M 22,v[124:127],54
L 3
M 22,v[132:135],26
R 46,2,46080
M 22,v[140:143],30
D 0,56,0
M 18,v[128:131],26
R 50,2,47104
M 18,v[136:139],30
L 3
M 18,v[144:147],34
R 54,2,48128
M 18,v[152:155],38
M 22,v[148:151],34
R 26,2,49152
M 22,v[156:159],38
L 3
D 0,56,1024
M 22,v[164:167],42
R 30,2,50176
M 22,v[172:175],46
M 18,v[160:163],42
R 34,2,51200
M 18,v[168:171],46
L 3
M 18,v[176:179],50
R 38,2,52224
M 18,v[184:187],54
D 0,56,2048
M 22,v[180:183],50
R 42,2,53248
M 22,v[188:191],54
L 3
M 22,v[196:199],26
R 46,2,54272
M 22,v[204:207],30
)"
R"(M 18,v[192:195],26
R 50,2,55296
M 18,v[200:203],30
L 3
M 18,v[208:211],34
s_waitcnt vmcnt(7)
R 54,2,56320
M 18,v[216:219],38
s_barrier
ds_read_b32 v7,v6 offset:1024
M 22,v[212:215],34
R 26,1,0
M 22,v[220:223],38
L 4
buffer_store_dword v4,v3,s[36:39],s69 offen offset:0 sc0 sc1
M 22,v[228:231],42
s_mov_b32 s69,s68
s_lshr_b32 s68,s62,4
R 30,1,1024
M 22,v[236:239],46
s_add_u32 s68,s68,s48
M 18,v[224:227],42
R 34,1,2048
M 18,v[232:235],46
L 4
L 3
M 18,v[240:243],50
R 38,1,3072
v_cmp_eq_u32 vcc,s53,v7
M 18,v[248:251],54
M 22,v[244:247],50
L 2
s_cmp_eq_u32 vcc_lo,s51
R 42,1,4096
M 22,v[252:255],54
s_cbranch_scc1 Lg4_F0_%=
Lg4_K0_%=:
s_cmp_eq_u32 s51,1
s_cbranch_scc0 Lg4_L0_%=
s_cmp_eq_u32 vcc_lo,-1
s_cbranch_scc1 Lg4_Fa0_%=
s_branch Lg4_La0_%=
Lg4_L0_%=:
v_mfma_f32_16x16x32_bf16 v[14:17],a[4:7],v[26:29],0
s_add_u32 m0,s59,3072
R 46,1,5120
v_lshlrev_b32 v62,16,v58
M 14,a[12:15],30
buffer_load_dword v8,s[36:39],s58 offen offset:0 sc1 lds
v_mfma_f32_16x16x32_bf16 v[10:13],a[0:3],v[26:29],0
s_mov_b32 s66,s65
R 50,1,6144
v_add_f32 v18,v18,v62
M 10,a[8:11],30
L 3
v_and_b32 v63,0xffff0000,v58
v_add_f32 v19,v19,v63
M 10,a[16:19],34
s_lshl_b32 s62,s54,15
R 54,1,7168
v_lshlrev_b32 v62,16,v59
M 10,a[24:27],38
s_cmp_ge_u32 s66,s45
v_add_f32 v20,v20,v62
v_and_b32 v63,0xffff0000,v59
M 14,a[20:23],34
s_cselect_b32 s51,1,-1
R 26,1,8192
v_add_f32 v21,v21,v63
M 14,a[28:31],38
L 3
v_lshlrev_b32 v62,16,v60
v_add_f32 v22,v22,v62
M 14,a[36:39],42
s_add_u32 s65,s47,s62
R 30,1,9216
v_and_b32 v63,0xffff0000,v60
M 14,a[44:47],46
v_add_f32 v23,v23,v63
v_lshlrev_b32 v62,16,v61
M 10,a[32:35],42
R 34,1,10240
v_add_f32 v24,v24,v62
M 10,a[40:43],46
L 3
v_and_b32 v63,0xffff0000,v61
v_add_f32 v25,v25,v63
M 10,a[48:51],50
R 38,1,11264
v_cvt_pk_bf16_f32 v18,v18,v19
M 10,a[56:59],54
buffer_load_dwordx4 v[58:61],v0,s[36:39],s74 offen offset:0 sc1
M 14,a[52:55],50
s_add_u32 s74,s49,s62
R 42,1,12288
v_cvt_pk_bf16_f32 v19,v20,v21
M 14,a[60:63],54
L 3
v_cvt_pk_bf16_f32 v20,v22,v23
v_cvt_pk_bf16_f32 v21,v24,v25
M 14,a[68:71],26
R 46,1,13312
M 14,a[76:79],30
buffer_store_dwordx4 v[18:21],v0,s[36:39],s67 offen offset:0 sc0 sc1
M 10,a[64:67],26
R 50,1,14336
M 10,a[72:75],30
L 3
M 10,a[80:83],34
R 54,1,15360
M 10,a[88:91],38
M 14,a[84:87],34
R 26,1,16384
M 14,a[92:95],38
L 3
M 14,a[100:103],42
R 30,1,17408
M 14,a[108:111],46
M 10,a[96:99],42
R 34,1,18432
M 10,a[104:107],46
L 3
M 10,a[112:115],50
R 38,1,19456
M 10,a[120:123],54
M 14,a[116:119],50
R 42,1,20480
M 14,a[124:127],54
L 3
M 14,a[132:135],26
R 46,1,21504
M 14,a[140:143],30
M 10,a[128:131],26
R 50,1,22528
M 10,a[136:139],30
L 3
M 10,a[144:147],34
R 54,1,23552
M 10,a[152:155],38
M 14,a[148:151],34
R 26,1,24576
M 14,a[156:159],38
L 3
M 14,a[164:167],42
R 30,1,25600
M 14,a[172:175],46
M 10,a[160:163],42
R 34,1,26624
M 10,a[168:171],46
L 3
M 10,a[176:179],50
s_waitcnt vmcnt(2)
R 38,1,27648
M 10,a[184:187],54
s_barrier
ds_read_b32 v7,v6 offset:3072
M 14,a[180:183],50
R 42,1,28672
M 14,a[188:191],54
L 4
M 14,a[196:199],26
R 46,1,29696
M 14,a[204:207],30
M 10,a[192:195],26
R 50,1,30720
M 10,a[200:203],30
L 4
L 3
v_cmp_eq_u32 vcc,s53,v7
M 10,a[208:211],34
R 54,1,31744
M 10,a[216:219],38
M 14,a[212:215],34
s_cmp_eq_u32 vcc_lo,-1
R 26,1,32768
s_cbranch_scc0 Lg4_X0_%=
M 14,a[220:223],38
L 3
s_add_u32 m0,s57,0
s_add_u32 s56,s55,0
M 14,a[228:231],42
s_add_u32 s61,s54,s46
s_cmp_ge_u32 s61,s63
s_cselect_b32 s61,s93,s61
R 30,1,33792
M 14,a[236:239],46
s_mul_i32 s58,s61,2560
D 0,56,0
M 10,a[224:227],42
s_cselect_b32 s46,s88,s46
s_cselect_b32 s63,s89,s63
R 34,1,34816
M 10,a[232:235],46
L 3
s_cselect_b32 s93,s50,s93
s_cselect_b32 s88,8,s88
M 10,a[240:243],50
s_cselect_b32 s89,-1,s89
s_add_u32 s58,s58,109891584
R 38,1,35840
M 10,a[248:251],54
D 0,56,1024
M 14,a[244:247],50
s_mov_b32 s54,s61
R 42,1,36864
M 14,a[252:255],54
L 3
M 14,v[68:71],26
R 46,1,37888
M 14,v[76:79],30
D 0,56,2048
M 10,v[64:67],26
R 50,1,38912
M 10,v[72:75],30
L 3
M 10,v[80:83],34
R 54,1,39936
M 10,v[88:91],38
D 0,56,3072
M 14,v[84:87],34
s_add_u32 m0,s57,4096
s_add_u32 s56,s56,4096
R 26,1,40960
M 14,v[92:95],38
L 3
M 14,v[100:103],42
R 30,1,41984
M 14,v[108:111],46
D 0,56,0
M 10,v[96:99],42
R 34,1,43008
M 10,v[104:107],46
L 3
M 10,v[112:115],50
R 38,1,44032
M 10,v[120:123],54
D 0,56,1024
M 14,v[116:119],50
R 42,1,45056
M 14,v[124:127],54
L 3
M 14,v[132:135],26
R 46,1,46080
M 14,v[140:143],30
D 0,56,2048
M 10,v[128:131],26
s_add_u32 m0,s59,0
s_add_u32 s56,s55,28672
R 50,1,47104
M 10,v[136:139],30
L 3
buffer_load_dword v8,s[36:39],s58 offen offset:0 sc1 lds
M 10,v[144:147],34
s_add_u32 m0,s57,28672
s_mul_i32 s55,s61,114688
R 54,1,48128
M 10,v[152:155],38
s_add_u32 s55,s55,s60
M 14,v[148:151],34
R 26,1,49152
M 14,v[156:159],38
L 3
D 0,56,0
M 14,v[164:167],42
R 30,1,50176
M 14,v[172:175],46
M 10,v[160:163],42
R 34,1,51200
M 10,v[168:171],46
L 3
D 0,56,1024
M 10,v[176:179],50
R 38,1,52224
M 10,v[184:187],54
D 0,56,2048
M 14,v[180:183],50
R 42,1,53248
M 14,v[188:191],54
L 3
M 14,v[196:199],26
R 46,1,54272
M 14,v[204:207],30
D 0,56,3072
M 10,v[192:195],26
s_add_u32 m0,s57,32768
s_add_u32 s56,s56,4096
R 50,1,55296
M 10,v[200:203],30
L 3
s_sub_u32 s57,s84,s57
M 10,v[208:211],34
s_waitcnt vmcnt(4)
R 54,1,56320
M 10,v[216:219],38
s_barrier
ds_read_b32 v7,v6 offset:0
M 14,v[212:215],34
R 26,2,0
M 14,v[220:223],38
L 4
buffer_store_dword v4,v3,s[36:39],s69 offen offset:0 sc0 sc1
M 14,v[228:231],42
s_mov_b32 s69,s68
s_lshr_b32 s68,s62,4
R 30,2,1024
M 14,v[236:239],46
D 0,56,0
M 10,v[224:227],42
s_add_u32 s68,s68,s48
R 34,2,2048
M 10,v[232:235],46
L 4
D 0,56,1024
M 10,v[240:243],50
L 3
R 38,2,3072
v_cmp_eq_u32 vcc,s53,v7
M 10,v[248:251],54
D 0,56,2048
M 14,v[244:247],50
L 2
s_cmp_eq_u32 vcc_lo,s51
R 42,2,4096
M 14,v[252:255],54
buffer_store_dword v4,v3,s[36:39],s71 offen offset:0 sc0 sc1
s_cbranch_scc1 Lg4_F1_%=
s_branch Lg4_K1_%=
Lg4_X0_%=:
M 14,a[220:223],38
L 3
s_add_u32 m0,s59,0
M 14,a[228:231],42
R 30,1,33792
M 14,a[236:239],46
buffer_load_dword v8,s[36:39],s58 offen offset:0 sc1 lds
M 10,a[224:227],42
R 34,1,34816
M 10,a[232:235],46
L 3
M 10,a[240:243],50
R 38,1,35840
M 10,a[248:251],54
M 14,a[244:247],50
R 42,1,36864
M 14,a[252:255],54
L 3
M 14,v[68:71],26
R 46,1,37888
M 14,v[76:79],30
M 10,v[64:67],26
R 50,1,38912
M 10,v[72:75],30
L 3
M 10,v[80:83],34
R 54,1,39936
M 10,v[88:91],38
M 14,v[84:87],34
R 26,1,40960
M 14,v[92:95],38
L 3
M 14,v[100:103],42
R 30,1,41984
M 14,v[108:111],46
M 10,v[96:99],42
R 34,1,43008
M 10,v[104:107],46
L 3
M 10,v[112:115],50
R 38,1,44032
M 10,v[120:123],54
M 14,v[116:119],50
R 42,1,45056
M 14,v[124:127],54
L 3
M 14,v[132:135],26
R 46,1,46080
M 14,v[140:143],30
M 10,v[128:131],26
R 50,1,47104
M 10,v[136:139],30
L 3
M 10,v[144:147],34
R 54,1,48128
M 10,v[152:155],38
M 14,v[148:151],34
R 26,1,49152
M 14,v[156:159],38
L 3
M 14,v[164:167],42
R 30,1,50176
M 14,v[172:175],46
M 10,v[160:163],42
R 34,1,51200
M 10,v[168:171],46
L 3
M 10,v[176:179],50
R 38,1,52224
M 10,v[184:187],54
M 14,v[180:183],50
R 42,1,53248
M 14,v[188:191],54
L 3
M 14,v[196:199],26
R 46,1,54272
M 14,v[204:207],30
M 10,v[192:195],26
R 50,1,55296
M 10,v[200:203],30
L 3
M 10,v[208:211],34
s_waitcnt vmcnt(0)
R 54,1,56320
M 10,v[216:219],38
s_barrier
buffer_store_dword v4,v3,s[36:39],s69 offen offset:0 sc0 sc1
M 14,v[212:215],34
s_mov_b32 s69,s68
s_lshr_b32 s68,s62,4
ds_read_b32 v7,v6 offset:0
M 14,v[220:223],38
L 3
s_add_u32 s68,s68,s48
M 14,v[228:231],42
M 14,v[236:239],46
M 10,v[224:227],42
M 10,v[232:235],46
L 1
M 10,v[240:243],50
L 0
v_cmp_eq_u32 vcc,s53,v7
M 10,v[248:251],54
M 14,v[244:247],50
M 14,v[252:255],54
s_cmp_eq_u32 s51,1
s_cbranch_scc1 Lg4_pa0_%=
s_mov_b32 s80,1
s_cmp_eq_u32 vcc_lo,-1
s_cbranch_scc0 Lg4_xn0_%=
s_add_u32 m0,s57,0
s_add_u32 s56,s55,0
D 0,56,0
v_lshlrev_b32 v62,16,v58
D 0,56,1024
v_add_f32 v10,v10,v62
D 0,56,2048
v_and_b32 v63,0xffff0000,v58
D 0,56,3072
v_add_f32 v11,v11,v63
s_add_u32 m0,s57,4096
s_add_u32 s56,s56,4096
D 0,56,0
v_lshlrev_b32 v62,16,v59
D 0,56,1024
v_add_f32 v12,v12,v62
D 0,56,2048
v_and_b32 v63,0xffff0000,v59
s_add_u32 s61,s54,s46
s_cmp_ge_u32 s61,s63
s_cselect_b32 s61,s93,s61
s_cselect_b32 s46,s88,s46
s_cselect_b32 s63,s89,s63
s_cselect_b32 s93,s50,s93
s_cselect_b32 s88,8,s88
s_cselect_b32 s89,-1,s89
s_mul_i32 s58,s61,2560
s_add_u32 s58,s58,109891584
s_add_u32 m0,s57,28672
s_add_u32 s56,s55,28672
D 0,56,0
v_add_f32 v13,v13,v63
D 0,56,1024
v_lshlrev_b32 v62,16,v60
D 0,56,2048
v_add_f32 v14,v14,v62
D 0,56,3072
v_and_b32 v63,0xffff0000,v60
s_add_u32 m0,s57,32768
s_add_u32 s56,s56,4096
D 0,56,0
v_add_f32 v15,v15,v63
D 0,56,1024
v_lshlrev_b32 v62,16,v61
D 0,56,2048
v_add_f32 v16,v16,v62
s_mul_i32 s55,s61,114688
s_add_u32 s55,s55,s60
s_sub_u32 s57,s84,s57
s_mov_b32 s54,s61
v_and_b32 v63,0xffff0000,v61
v_add_f32 v17,v17,v63
v_cvt_pk_bf16_f32 v10,v10,v11
v_cvt_pk_bf16_f32 v11,v12,v13
v_cvt_pk_bf16_f32 v12,v14,v15
v_cvt_pk_bf16_f32 v13,v16,v17
buffer_store_dwordx4 v[10:13],v0,s[36:39],s66 offen offset:0 sc0 sc1
s_mov_b32 s72,s69
s_mov_b32 s69,0x7fff0000
s_mov_b32 s66,0x7fff0000
s_mov_b32 s76,0x7fff0000
s_branch Lg4_gox_%=
)"
R"(Lg4_xn0_%=:
s_cmp_ge_u32 s54,256
s_cbranch_scc1 Lg4_fin0_%=
s_add_u32 m0,s59,4096
s_nop 0
buffer_load_dword v8,s[36:39],s58 offen offset:0 sc1 lds
buffer_store_dword v4,v3,s[36:39],s71 offen offset:0 sc0 sc1
s_add_u32 m0,s59,5120
s_nop 0
buffer_load_dword v8,s[36:39],s58 offen offset:0 sc1 lds
buffer_store_dword v4,v3,s[36:39],s71 offen offset:0 sc0 sc1
s_mov_b32 s83,0
v_lshlrev_b32 v62,16,v58
v_add_f32 v10,v10,v62
v_and_b32 v63,0xffff0000,v58
v_add_f32 v11,v11,v63
v_lshlrev_b32 v62,16,v59
v_add_f32 v12,v12,v62
v_and_b32 v63,0xffff0000,v59
v_add_f32 v13,v13,v63
v_lshlrev_b32 v62,16,v60
v_add_f32 v14,v14,v62
v_and_b32 v63,0xffff0000,v60
v_add_f32 v15,v15,v63
v_lshlrev_b32 v62,16,v61
v_add_f32 v16,v16,v62
v_and_b32 v63,0xffff0000,v61
v_add_f32 v17,v17,v63
v_cvt_pk_bf16_f32 v10,v10,v11
v_cvt_pk_bf16_f32 v11,v12,v13
v_cvt_pk_bf16_f32 v12,v14,v15
v_cvt_pk_bf16_f32 v13,v16,v17
buffer_store_dwordx4 v[10:13],v0,s[36:39],s66 offen offset:0 sc0 sc1
s_mov_b32 s72,s69
s_mov_b32 s69,0x7fff0000
s_mov_b32 s66,0x7fff0000
s_mov_b32 s76,0x7fff0000
s_branch Lg4_poll_%=
Lg4_fin0_%=:
v_lshlrev_b32 v62,16,v58
v_add_f32 v10,v10,v62
v_and_b32 v63,0xffff0000,v58
v_add_f32 v11,v11,v63
v_lshlrev_b32 v62,16,v59
v_add_f32 v12,v12,v62
v_and_b32 v63,0xffff0000,v59
v_add_f32 v13,v13,v63
v_lshlrev_b32 v62,16,v60
v_add_f32 v14,v14,v62
v_and_b32 v63,0xffff0000,v60
v_add_f32 v15,v15,v63
v_lshlrev_b32 v62,16,v61
v_add_f32 v16,v16,v62
v_and_b32 v63,0xffff0000,v61
v_add_f32 v17,v17,v63
v_cvt_pk_bf16_f32 v10,v10,v11
v_cvt_pk_bf16_f32 v11,v12,v13
v_cvt_pk_bf16_f32 v12,v14,v15
v_cvt_pk_bf16_f32 v13,v16,v17
buffer_store_dwordx4 v[10:13],v0,s[36:39],s66 offen offset:0 sc0 sc1
s_mov_b32 s72,s69
s_mov_b32 s69,0x7fff0000
s_mov_b32 s66,0x7fff0000
s_mov_b32 s76,0x7fff0000
s_branch Lg4_done_%=
Lg4_K1_%=:
s_cmp_eq_u32 s51,1
s_cbranch_scc0 Lg4_L1_%=
s_cmp_eq_u32 vcc_lo,-1
s_cbranch_scc1 Lg4_Fa1_%=
s_branch Lg4_La1_%=
Lg4_L1_%=:
v_mfma_f32_16x16x32_bf16 v[22:25],a[4:7],v[26:29],0
s_add_u32 m0,s59,3072
R 46,2,5120
v_lshlrev_b32 v62,16,v58
M 22,a[12:15],30
buffer_load_dword v8,s[36:39],s58 offen offset:0 sc1 lds
v_mfma_f32_16x16x32_bf16 v[18:21],a[0:3],v[26:29],0
s_mov_b32 s67,s65
R 50,2,6144
v_add_f32 v10,v10,v62
M 18,a[8:11],30
L 3
v_and_b32 v63,0xffff0000,v58
v_add_f32 v11,v11,v63
M 18,a[16:19],34
s_lshl_b32 s62,s54,15
R 54,2,7168
v_lshlrev_b32 v62,16,v59
M 18,a[24:27],38
s_cmp_ge_u32 s67,s45
v_add_f32 v12,v12,v62
v_and_b32 v63,0xffff0000,v59
M 22,a[20:23],34
s_cselect_b32 s51,1,-1
R 26,2,8192
v_add_f32 v13,v13,v63
M 22,a[28:31],38
L 3
v_lshlrev_b32 v62,16,v60
v_add_f32 v14,v14,v62
M 22,a[36:39],42
s_add_u32 s65,s47,s62
R 30,2,9216
v_and_b32 v63,0xffff0000,v60
M 22,a[44:47],46
v_add_f32 v15,v15,v63
v_lshlrev_b32 v62,16,v61
M 18,a[32:35],42
R 34,2,10240
v_add_f32 v16,v16,v62
M 18,a[40:43],46
L 3
v_and_b32 v63,0xffff0000,v61
v_add_f32 v17,v17,v63
M 18,a[48:51],50
R 38,2,11264
v_cvt_pk_bf16_f32 v10,v10,v11
M 18,a[56:59],54
buffer_load_dwordx4 v[58:61],v0,s[36:39],s74 offen offset:0 sc1
M 22,a[52:55],50
s_add_u32 s74,s49,s62
R 42,2,12288
v_cvt_pk_bf16_f32 v11,v12,v13
M 22,a[60:63],54
L 3
v_cvt_pk_bf16_f32 v12,v14,v15
v_cvt_pk_bf16_f32 v13,v16,v17
M 22,a[68:71],26
R 46,2,13312
M 22,a[76:79],30
buffer_store_dwordx4 v[10:13],v0,s[36:39],s66 offen offset:0 sc0 sc1
M 18,a[64:67],26
R 50,2,14336
M 18,a[72:75],30
L 3
M 18,a[80:83],34
R 54,2,15360
M 18,a[88:91],38
M 22,a[84:87],34
R 26,2,16384
M 22,a[92:95],38
L 3
M 22,a[100:103],42
R 30,2,17408
M 22,a[108:111],46
M 18,a[96:99],42
R 34,2,18432
M 18,a[104:107],46
L 3
M 18,a[112:115],50
R 38,2,19456
M 18,a[120:123],54
M 22,a[116:119],50
R 42,2,20480
M 22,a[124:127],54
L 3
M 22,a[132:135],26
R 46,2,21504
M 22,a[140:143],30
M 18,a[128:131],26
R 50,2,22528
M 18,a[136:139],30
L 3
M 18,a[144:147],34
R 54,2,23552
M 18,a[152:155],38
M 22,a[148:151],34
R 26,2,24576
M 22,a[156:159],38
L 3
M 22,a[164:167],42
R 30,2,25600
M 22,a[172:175],46
M 18,a[160:163],42
R 34,2,26624
M 18,a[168:171],46
L 3
M 18,a[176:179],50
s_waitcnt vmcnt(2)
R 38,2,27648
M 18,a[184:187],54
s_barrier
ds_read_b32 v7,v6 offset:3072
M 22,a[180:183],50
R 42,2,28672
M 22,a[188:191],54
L 4
M 22,a[196:199],26
R 46,2,29696
M 22,a[204:207],30
M 18,a[192:195],26
R 50,2,30720
M 18,a[200:203],30
L 4
L 3
v_cmp_eq_u32 vcc,s53,v7
M 18,a[208:211],34
R 54,2,31744
M 18,a[216:219],38
M 22,a[212:215],34
s_cmp_eq_u32 vcc_lo,-1
R 26,2,32768
s_cbranch_scc0 Lg4_X1_%=
M 22,a[220:223],38
L 3
s_add_u32 m0,s57,0
s_add_u32 s56,s55,0
M 22,a[228:231],42
s_add_u32 s61,s54,s46
s_cmp_ge_u32 s61,s63
s_cselect_b32 s61,s93,s61
R 30,2,33792
M 22,a[236:239],46
s_mul_i32 s58,s61,2560
D 0,56,0
M 18,a[224:227],42
s_cselect_b32 s46,s88,s46
s_cselect_b32 s63,s89,s63
R 34,2,34816
M 18,a[232:235],46
L 3
s_cselect_b32 s93,s50,s93
s_cselect_b32 s88,8,s88
M 18,a[240:243],50
s_cselect_b32 s89,-1,s89
s_add_u32 s58,s58,109891584
R 38,2,35840
M 18,a[248:251],54
D 0,56,1024
M 22,a[244:247],50
s_mov_b32 s54,s61
R 42,2,36864
M 22,a[252:255],54
L 3
M 22,v[68:71],26
R 46,2,37888
M 22,v[76:79],30
D 0,56,2048
M 18,v[64:67],26
R 50,2,38912
M 18,v[72:75],30
L 3
M 18,v[80:83],34
R 54,2,39936
M 18,v[88:91],38
D 0,56,3072
M 22,v[84:87],34
s_add_u32 m0,s57,4096
s_add_u32 s56,s56,4096
R 26,2,40960
M 22,v[92:95],38
L 3
M 22,v[100:103],42
R 30,2,41984
M 22,v[108:111],46
D 0,56,0
M 18,v[96:99],42
R 34,2,43008
M 18,v[104:107],46
L 3
M 18,v[112:115],50
R 38,2,44032
M 18,v[120:123],54
D 0,56,1024
M 22,v[116:119],50
R 42,2,45056
M 22,v[124:127],54
L 3
M 22,v[132:135],26
R 46,2,46080
M 22,v[140:143],30
D 0,56,2048
M 18,v[128:131],26
s_add_u32 m0,s59,1024
s_add_u32 s56,s55,28672
R 50,2,47104
M 18,v[136:139],30
L 3
buffer_load_dword v8,s[36:39],s58 offen offset:0 sc1 lds
M 18,v[144:147],34
s_add_u32 m0,s57,28672
s_mul_i32 s55,s61,114688
R 54,2,48128
M 18,v[152:155],38
s_add_u32 s55,s55,s60
M 22,v[148:151],34
R 26,2,49152
M 22,v[156:159],38
L 3
D 0,56,0
M 22,v[164:167],42
R 30,2,50176
M 22,v[172:175],46
M 18,v[160:163],42
R 34,2,51200
M 18,v[168:171],46
L 3
D 0,56,1024
M 18,v[176:179],50
R 38,2,52224
M 18,v[184:187],54
D 0,56,2048
M 22,v[180:183],50
R 42,2,53248
M 22,v[188:191],54
L 3
M 22,v[196:199],26
R 46,2,54272
M 22,v[204:207],30
D 0,56,3072
M 18,v[192:195],26
s_add_u32 m0,s57,32768
s_add_u32 s56,s56,4096
R 50,2,55296
M 18,v[200:203],30
L 3
s_sub_u32 s57,s84,s57
M 18,v[208:211],34
s_waitcnt vmcnt(4)
R 54,2,56320
M 18,v[216:219],38
s_barrier
ds_read_b32 v7,v6 offset:1024
M 22,v[212:215],34
R 26,1,0
M 22,v[220:223],38
L 4
buffer_store_dword v4,v3,s[36:39],s69 offen offset:0 sc0 sc1
M 22,v[228:231],42
s_mov_b32 s69,s68
s_lshr_b32 s68,s62,4
R 30,1,1024
M 22,v[236:239],46
D 0,56,0
M 18,v[224:227],42
s_add_u32 s68,s68,s48
R 34,1,2048
M 18,v[232:235],46
L 4
D 0,56,1024
M 18,v[240:243],50
L 3
R 38,1,3072
v_cmp_eq_u32 vcc,s53,v7
M 18,v[248:251],54
D 0,56,2048
M 22,v[244:247],50
L 2
s_cmp_eq_u32 vcc_lo,s51
R 42,1,4096
M 22,v[252:255],54
buffer_store_dword v4,v3,s[36:39],s71 offen offset:0 sc0 sc1
s_cbranch_scc1 Lg4_F0_%=
s_branch Lg4_K0_%=
Lg4_X1_%=:
M 22,a[220:223],38
L 3
s_add_u32 m0,s59,1024
M 22,a[228:231],42
R 30,2,33792
M 22,a[236:239],46
buffer_load_dword v8,s[36:39],s58 offen offset:0 sc1 lds
M 18,a[224:227],42
R 34,2,34816
M 18,a[232:235],46
L 3
M 18,a[240:243],50
R 38,2,35840
M 18,a[248:251],54
M 22,a[244:247],50
R 42,2,36864
M 22,a[252:255],54
L 3
M 22,v[68:71],26
R 46,2,37888
M 22,v[76:79],30
M 18,v[64:67],26
R 50,2,38912
M 18,v[72:75],30
L 3
M 18,v[80:83],34
R 54,2,39936
M 18,v[88:91],38
M 22,v[84:87],34
R 26,2,40960
M 22,v[92:95],38
L 3
M 22,v[100:103],42
R 30,2,41984
M 22,v[108:111],46
M 18,v[96:99],42
R 34,2,43008
M 18,v[104:107],46
L 3
M 18,v[112:115],50
R 38,2,44032
M 18,v[120:123],54
M 22,v[116:119],50
R 42,2,45056
M 22,v[124:127],54
L 3
M 22,v[132:135],26
R 46,2,46080
M 22,v[140:143],30
M 18,v[128:131],26
R 50,2,47104
M 18,v[136:139],30
L 3
M 18,v[144:147],34
R 54,2,48128
M 18,v[152:155],38
M 22,v[148:151],34
R 26,2,49152
M 22,v[156:159],38
L 3
M 22,v[164:167],42
R 30,2,50176
M 22,v[172:175],46
M 18,v[160:163],42
R 34,2,51200
M 18,v[168:171],46
L 3
M 18,v[176:179],50
R 38,2,52224
M 18,v[184:187],54
M 22,v[180:183],50
R 42,2,53248
M 22,v[188:191],54
L 3
M 22,v[196:199],26
R 46,2,54272
M 22,v[204:207],30
M 18,v[192:195],26
R 50,2,55296
M 18,v[200:203],30
L 3
M 18,v[208:211],34
s_waitcnt vmcnt(0)
R 54,2,56320
M 18,v[216:219],38
s_barrier
buffer_store_dword v4,v3,s[36:39],s69 offen offset:0 sc0 sc1
M 22,v[212:215],34
s_mov_b32 s69,s68
s_lshr_b32 s68,s62,4
ds_read_b32 v7,v6 offset:1024
M 22,v[220:223],38
L 3
s_add_u32 s68,s68,s48
M 22,v[228:231],42
M 22,v[236:239],46
M 18,v[224:227],42
M 18,v[232:235],46
L 1
M 18,v[240:243],50
L 0
v_cmp_eq_u32 vcc,s53,v7
M 18,v[248:251],54
M 22,v[244:247],50
M 22,v[252:255],54
s_cmp_eq_u32 s51,1
s_cbranch_scc1 Lg4_pa1_%=
s_mov_b32 s80,0
s_cmp_eq_u32 vcc_lo,-1
s_cbranch_scc0 Lg4_xn1_%=
s_add_u32 m0,s57,0
s_add_u32 s56,s55,0
D 0,56,0
v_lshlrev_b32 v62,16,v58
D 0,56,1024
v_add_f32 v18,v18,v62
D 0,56,2048
v_and_b32 v63,0xffff0000,v58
D 0,56,3072
v_add_f32 v19,v19,v63
s_add_u32 m0,s57,4096
s_add_u32 s56,s56,4096
D 0,56,0
v_lshlrev_b32 v62,16,v59
D 0,56,1024
v_add_f32 v20,v20,v62
D 0,56,2048
v_and_b32 v63,0xffff0000,v59
s_add_u32 s61,s54,s46
s_cmp_ge_u32 s61,s63
s_cselect_b32 s61,s93,s61
s_cselect_b32 s46,s88,s46
s_cselect_b32 s63,s89,s63
s_cselect_b32 s93,s50,s93
s_cselect_b32 s88,8,s88
s_cselect_b32 s89,-1,s89
s_mul_i32 s58,s61,2560
s_add_u32 s58,s58,109891584
s_add_u32 m0,s57,28672
s_add_u32 s56,s55,28672
D 0,56,0
v_add_f32 v21,v21,v63
D 0,56,1024
)"
R"(v_lshlrev_b32 v62,16,v60
D 0,56,2048
v_add_f32 v22,v22,v62
D 0,56,3072
v_and_b32 v63,0xffff0000,v60
s_add_u32 m0,s57,32768
s_add_u32 s56,s56,4096
D 0,56,0
v_add_f32 v23,v23,v63
D 0,56,1024
v_lshlrev_b32 v62,16,v61
D 0,56,2048
v_add_f32 v24,v24,v62
s_mul_i32 s55,s61,114688
s_add_u32 s55,s55,s60
s_sub_u32 s57,s84,s57
s_mov_b32 s54,s61
v_and_b32 v63,0xffff0000,v61
v_add_f32 v25,v25,v63
v_cvt_pk_bf16_f32 v18,v18,v19
v_cvt_pk_bf16_f32 v19,v20,v21
v_cvt_pk_bf16_f32 v20,v22,v23
v_cvt_pk_bf16_f32 v21,v24,v25
buffer_store_dwordx4 v[18:21],v0,s[36:39],s67 offen offset:0 sc0 sc1
s_mov_b32 s72,s69
s_mov_b32 s69,0x7fff0000
s_mov_b32 s67,0x7fff0000
s_mov_b32 s77,0x7fff0000
s_branch Lg4_gox_%=
Lg4_xn1_%=:
s_cmp_ge_u32 s54,256
s_cbranch_scc1 Lg4_fin1_%=
s_add_u32 m0,s59,4096
s_nop 0
buffer_load_dword v8,s[36:39],s58 offen offset:0 sc1 lds
buffer_store_dword v4,v3,s[36:39],s71 offen offset:0 sc0 sc1
s_add_u32 m0,s59,5120
s_nop 0
buffer_load_dword v8,s[36:39],s58 offen offset:0 sc1 lds
buffer_store_dword v4,v3,s[36:39],s71 offen offset:0 sc0 sc1
s_mov_b32 s83,0
v_lshlrev_b32 v62,16,v58
v_add_f32 v18,v18,v62
v_and_b32 v63,0xffff0000,v58
v_add_f32 v19,v19,v63
v_lshlrev_b32 v62,16,v59
v_add_f32 v20,v20,v62
v_and_b32 v63,0xffff0000,v59
v_add_f32 v21,v21,v63
v_lshlrev_b32 v62,16,v60
v_add_f32 v22,v22,v62
v_and_b32 v63,0xffff0000,v60
v_add_f32 v23,v23,v63
v_lshlrev_b32 v62,16,v61
v_add_f32 v24,v24,v62
v_and_b32 v63,0xffff0000,v61
v_add_f32 v25,v25,v63
v_cvt_pk_bf16_f32 v18,v18,v19
v_cvt_pk_bf16_f32 v19,v20,v21
v_cvt_pk_bf16_f32 v20,v22,v23
v_cvt_pk_bf16_f32 v21,v24,v25
buffer_store_dwordx4 v[18:21],v0,s[36:39],s67 offen offset:0 sc0 sc1
s_mov_b32 s72,s69
s_mov_b32 s69,0x7fff0000
s_mov_b32 s67,0x7fff0000
s_mov_b32 s77,0x7fff0000
s_branch Lg4_poll_%=
Lg4_fin1_%=:
v_lshlrev_b32 v62,16,v58
v_add_f32 v18,v18,v62
v_and_b32 v63,0xffff0000,v58
v_add_f32 v19,v19,v63
v_lshlrev_b32 v62,16,v59
v_add_f32 v20,v20,v62
v_and_b32 v63,0xffff0000,v59
v_add_f32 v21,v21,v63
v_lshlrev_b32 v62,16,v60
v_add_f32 v22,v22,v62
v_and_b32 v63,0xffff0000,v60
v_add_f32 v23,v23,v63
v_lshlrev_b32 v62,16,v61
v_add_f32 v24,v24,v62
v_and_b32 v63,0xffff0000,v61
v_add_f32 v25,v25,v63
v_cvt_pk_bf16_f32 v18,v18,v19
v_cvt_pk_bf16_f32 v19,v20,v21
v_cvt_pk_bf16_f32 v20,v22,v23
v_cvt_pk_bf16_f32 v21,v24,v25
buffer_store_dwordx4 v[18:21],v0,s[36:39],s67 offen offset:0 sc0 sc1
s_mov_b32 s72,s69
s_mov_b32 s69,0x7fff0000
s_mov_b32 s67,0x7fff0000
s_mov_b32 s77,0x7fff0000
s_branch Lg4_done_%=
Lg4_Fa0_%=:
s_nop 0
v_mfma_f32_16x16x32_bf16 v[14:17],a[4:7],v[26:29],0
s_add_u32 m0,s57,0
R 46,1,5120
v_lshlrev_b32 v62,16,v58
M 14,a[12:15],30
s_add_u32 s56,s55,0
v_add_f32 v18,v18,v62
v_and_b32 v63,0xffff0000,v58
v_mfma_f32_16x16x32_bf16 v[10:13],a[0:3],v[26:29],0
s_add_u32 s61,s54,s46
R 50,1,6144
v_add_f32 v19,v19,v63
M 10,a[8:11],30
L 3
D 0,56,0
M 10,a[16:19],34
s_cmp_ge_u32 s61,s63
R 54,1,7168
v_lshlrev_b32 v62,16,v59
M 10,a[24:27],38
s_cselect_b32 s61,s93,s61
v_add_f32 v20,v20,v62
v_and_b32 v63,0xffff0000,v59
M 14,a[20:23],34
s_cselect_b32 s46,s88,s46
R 26,1,8192
v_add_f32 v21,v21,v63
M 14,a[28:31],38
L 3
v_lshlrev_b32 v62,16,v60
v_add_f32 v22,v22,v62
M 14,a[36:39],42
s_cselect_b32 s63,s89,s63
R 30,1,9216
v_and_b32 v63,0xffff0000,v60
M 14,a[44:47],46
D 0,56,1024
M 10,a[32:35],42
s_cselect_b32 s93,s50,s93
R 34,1,10240
v_add_f32 v23,v23,v63
M 10,a[40:43],46
L 3
v_lshlrev_b32 v62,16,v61
v_add_f32 v24,v24,v62
M 10,a[48:51],50
s_cselect_b32 s88,8,s88
R 38,1,11264
v_and_b32 v63,0xffff0000,v61
M 10,a[56:59],54
buffer_load_dwordx4 v[58:61],v0,s[36:39],s74 offen offset:0 sc1
M 14,a[52:55],50
s_cselect_b32 s89,-1,s89
R 42,1,12288
v_add_f32 v25,v25,v63
M 14,a[60:63],54
L 3
v_cvt_pk_bf16_f32 v18,v18,v19
v_cvt_pk_bf16_f32 v19,v20,v21
M 14,a[68:71],26
s_mul_i32 s58,s61,2560
R 46,1,13312
v_cvt_pk_bf16_f32 v20,v22,v23
M 14,a[76:79],30
D 0,56,2048
M 10,a[64:67],26
s_add_u32 s58,s58,109891584
R 50,1,14336
v_cvt_pk_bf16_f32 v21,v24,v25
M 10,a[72:75],30
L 3
v_lshrrev_b32 v62,8,v0
v_lshl_add_u32 v62,v62,5,v0
M 10,a[80:83],34
s_sub_u32 s75,s67,s85
R 54,1,15360
v_add_u32 v62,s73,v62
M 10,a[88:91],38
s_mov_b64 s[40:41],s[86:87]
ds_write_b128 v62,v[18:21] offset:0
M 14,a[84:87],34
s_mov_b32 s42,4194304
R 26,1,16384
ds_read2_b32 v[22:23],v5 offset0:0 offset1:16
M 14,a[92:95],38
L 5
s_mov_b32 s43,0x00020000
ds_read2_b32 v[24:25],v5 offset0:32 offset1:48
M 14,a[100:103],42
s_add_u32 s64,s75,0
s_lshl_b32 s62,s54,15
R 30,1,17408
M 14,a[108:111],46
D 0,56,3072
M 10,a[96:99],42
s_add_u32 m0,s57,4096
s_add_u32 s56,s56,4096
R 34,1,18432
M 10,a[104:107],46
L 6
s_mov_b32 s66,s65
s_add_u32 s74,s49,s62
M 10,a[112:115],50
s_add_u32 s65,s47,s62
s_mov_b32 s54,s61
R 38,1,19456
M 10,a[120:123],54
L 3
M 14,a[116:119],50
R 42,1,20480
M 14,a[124:127],54
L 3
D 0,56,0
M 14,a[132:135],26
R 46,1,21504
M 14,a[140:143],30
M 10,a[128:131],26
R 50,1,22528
M 10,a[136:139],30
L 3
M 10,a[144:147],34
R 54,1,23552
M 10,a[152:155],38
M 14,a[148:151],34
R 26,1,24576
M 14,a[156:159],38
L 3
D 0,56,1024
M 14,a[164:167],42
R 30,1,25600
M 14,a[172:175],46
M 10,a[160:163],42
R 34,1,26624
M 10,a[168:171],46
L 3
M 10,a[176:179],50
s_waitcnt vmcnt(8)
R 38,1,27648
M 10,a[184:187],54
s_barrier
M 14,a[180:183],50
R 42,1,28672
M 14,a[188:191],54
L 3
D 0,56,2048
M 14,a[196:199],26
s_add_u32 m0,s59,0
s_add_u32 s56,s55,28672
R 46,1,29696
M 14,a[204:207],30
s_mul_i32 s55,s61,114688
s_add_u32 s55,s55,s60
M 10,a[192:195],26
R 50,1,30720
M 10,a[200:203],30
L 3
M 10,a[208:211],34
R 54,1,31744
M 10,a[216:219],38
M 14,a[212:215],34
R 26,1,32768
M 14,a[220:223],38
L 3
buffer_load_dword v8,s[36:39],s58 offen offset:0 sc1 lds
M 14,a[228:231],42
s_add_u32 m0,s57,28672
R 30,1,33792
M 14,a[236:239],46
D 0,56,0
M 10,a[224:227],42
R 34,1,34816
M 10,a[232:235],46
L 3
M 10,a[240:243],50
R 38,1,35840
M 10,a[248:251],54
M 14,a[244:247],50
R 42,1,36864
M 14,a[252:255],54
L 3
D 0,56,1024
M 14,v[68:71],26
R 46,1,37888
M 14,v[76:79],30
M 10,v[64:67],26
R 50,1,38912
M 10,v[72:75],30
L 3
M 10,v[80:83],34
R 54,1,39936
M 10,v[88:91],38
D 0,56,2048
M 14,v[84:87],34
R 26,1,40960
M 14,v[92:95],38
L 3
M 14,v[100:103],42
R 30,1,41984
M 14,v[108:111],46
M 10,v[96:99],42
R 34,1,43008
M 10,v[104:107],46
L 3
D 0,56,3072
M 10,v[112:115],50
s_add_u32 m0,s57,32768
s_add_u32 s56,s56,4096
R 38,1,44032
M 10,v[120:123],54
s_sub_u32 s57,s84,s57
M 14,v[116:119],50
R 42,1,45056
M 14,v[124:127],54
L 3
M 14,v[132:135],26
R 46,1,46080
M 14,v[140:143],30
D 0,56,0
M 10,v[128:131],26
R 50,1,47104
M 10,v[136:139],30
L 3
M 10,v[144:147],34
R 54,1,48128
M 10,v[152:155],38
M 14,v[148:151],34
R 26,1,49152
M 14,v[156:159],38
L 3
D 0,56,1024
M 14,v[164:167],42
R 30,1,50176
M 14,v[172:175],46
M 10,v[160:163],42
R 34,1,51200
M 10,v[168:171],46
L 3
M 10,v[176:179],50
R 38,1,52224
M 10,v[184:187],54
D 0,56,2048
M 14,v[180:183],50
R 42,1,53248
M 14,v[188:191],54
L 3
M 14,v[196:199],26
R 46,1,54272
M 14,v[204:207],30
M 10,v[192:195],26
R 50,1,55296
M 10,v[200:203],30
L 3
M 10,v[208:211],34
s_waitcnt vmcnt(7)
R 54,1,56320
M 10,v[216:219],38
s_barrier
ds_read_b32 v7,v6 offset:0
M 14,v[212:215],34
R 26,2,0
M 14,v[220:223],38
L 4
M 14,v[228:231],42
R 30,2,1024
M 14,v[236:239],46
buffer_atomic_pk_add_bf16 v22,v9,s[40:43],s64 offen sc1
M 10,v[224:227],42
s_add_u32 s64,s75,8192
R 34,2,2048
M 10,v[232:235],46
L 4
buffer_atomic_pk_add_bf16 v23,v9,s[40:43],s64 offen sc1
M 10,v[240:243],50
s_add_u32 s64,s75,16384
L 3
R 38,2,3072
v_cmp_eq_u32 vcc,s53,v7
M 10,v[248:251],54
buffer_atomic_pk_add_bf16 v24,v9,s[40:43],s64 offen sc1
M 14,v[244:247],50
L 2
s_add_u32 s64,s75,24576
s_cmp_eq_u32 vcc_lo,-1
R 42,2,4096
M 14,v[252:255],54
buffer_atomic_pk_add_bf16 v25,v9,s[40:43],s64 offen sc1
s_cbranch_scc0 Lg4_Ka1_%=
Lg4_Fa1_%=:
s_nop 0
v_mfma_f32_16x16x32_bf16 v[22:25],a[4:7],v[26:29],0
s_add_u32 m0,s57,0
R 46,2,5120
v_lshlrev_b32 v62,16,v58
M 22,a[12:15],30
s_add_u32 s56,s55,0
v_add_f32 v10,v10,v62
v_and_b32 v63,0xffff0000,v58
v_mfma_f32_16x16x32_bf16 v[18:21],a[0:3],v[26:29],0
s_add_u32 s61,s54,s46
R 50,2,6144
v_add_f32 v11,v11,v63
M 18,a[8:11],30
L 3
D 0,56,0
M 18,a[16:19],34
s_cmp_ge_u32 s61,s63
R 54,2,7168
v_lshlrev_b32 v62,16,v59
M 18,a[24:27],38
s_cselect_b32 s61,s93,s61
v_add_f32 v12,v12,v62
v_and_b32 v63,0xffff0000,v59
M 22,a[20:23],34
s_cselect_b32 s46,s88,s46
R 26,2,8192
v_add_f32 v13,v13,v63
M 22,a[28:31],38
L 3
v_lshlrev_b32 v62,16,v60
v_add_f32 v14,v14,v62
M 22,a[36:39],42
s_cselect_b32 s63,s89,s63
R 30,2,9216
v_and_b32 v63,0xffff0000,v60
M 22,a[44:47],46
D 0,56,1024
M 18,a[32:35],42
s_cselect_b32 s93,s50,s93
R 34,2,10240
v_add_f32 v15,v15,v63
M 18,a[40:43],46
L 3
v_lshlrev_b32 v62,16,v61
v_add_f32 v16,v16,v62
M 18,a[48:51],50
s_cselect_b32 s88,8,s88
R 38,2,11264
v_and_b32 v63,0xffff0000,v61
M 18,a[56:59],54
buffer_load_dwordx4 v[58:61],v0,s[36:39],s74 offen offset:0 sc1
M 22,a[52:55],50
s_cselect_b32 s89,-1,s89
R 42,2,12288
v_add_f32 v17,v17,v63
M 22,a[60:63],54
L 3
v_cvt_pk_bf16_f32 v10,v10,v11
v_cvt_pk_bf16_f32 v11,v12,v13
M 22,a[68:71],26
s_mul_i32 s58,s61,2560
R 46,2,13312
v_cvt_pk_bf16_f32 v12,v14,v15
M 22,a[76:79],30
D 0,56,2048
M 18,a[64:67],26
s_add_u32 s58,s58,109891584
R 50,2,14336
v_cvt_pk_bf16_f32 v13,v16,v17
M 18,a[72:75],30
L 3
v_lshrrev_b32 v62,8,v0
v_lshl_add_u32 v62,v62,5,v0
M 18,a[80:83],34
s_sub_u32 s75,s66,s85
R 54,2,15360
v_add_u32 v62,s73,v62
M 18,a[88:91],38
s_mov_b64 s[40:41],s[86:87]
ds_write_b128 v62,v[10:13] offset:0
M 22,a[84:87],34
s_mov_b32 s42,4194304
R 26,2,16384
ds_read2_b32 v[14:15],v5 offset0:0 offset1:16
M 22,a[92:95],38
L 5
s_mov_b32 s43,0x00020000
ds_read2_b32 v[16:17],v5 offset0:32 offset1:48
M 22,a[100:103],42
s_add_u32 s64,s75,0
s_lshl_b32 s62,s54,15
R 30,2,17408
M 22,a[108:111],46
D 0,56,3072
M 18,a[96:99],42
s_add_u32 m0,s57,4096
s_add_u32 s56,s56,4096
R 34,2,18432
M 18,a[104:107],46
L 6
s_mov_b32 s67,s65
s_add_u32 s74,s49,s62
M 18,a[112:115],50
s_add_u32 s65,s47,s62
s_mov_b32 s54,s61
R 38,2,19456
M 18,a[120:123],54
L 3
M 22,a[116:119],50
R 42,2,20480
M 22,a[124:127],54
L 3
D 0,56,0
M 22,a[132:135],26
R 46,2,21504
)"
R"(M 22,a[140:143],30
M 18,a[128:131],26
R 50,2,22528
M 18,a[136:139],30
L 3
M 18,a[144:147],34
R 54,2,23552
M 18,a[152:155],38
M 22,a[148:151],34
R 26,2,24576
M 22,a[156:159],38
L 3
D 0,56,1024
M 22,a[164:167],42
R 30,2,25600
M 22,a[172:175],46
M 18,a[160:163],42
R 34,2,26624
M 18,a[168:171],46
L 3
M 18,a[176:179],50
s_waitcnt vmcnt(8)
R 38,2,27648
M 18,a[184:187],54
s_barrier
M 22,a[180:183],50
R 42,2,28672
M 22,a[188:191],54
L 3
D 0,56,2048
M 22,a[196:199],26
s_add_u32 m0,s59,1024
s_add_u32 s56,s55,28672
R 46,2,29696
M 22,a[204:207],30
s_mul_i32 s55,s61,114688
s_add_u32 s55,s55,s60
M 18,a[192:195],26
R 50,2,30720
M 18,a[200:203],30
L 3
M 18,a[208:211],34
R 54,2,31744
M 18,a[216:219],38
M 22,a[212:215],34
R 26,2,32768
M 22,a[220:223],38
L 3
buffer_load_dword v8,s[36:39],s58 offen offset:0 sc1 lds
M 22,a[228:231],42
s_add_u32 m0,s57,28672
R 30,2,33792
M 22,a[236:239],46
D 0,56,0
M 18,a[224:227],42
R 34,2,34816
M 18,a[232:235],46
L 3
M 18,a[240:243],50
R 38,2,35840
M 18,a[248:251],54
M 22,a[244:247],50
R 42,2,36864
M 22,a[252:255],54
L 3
D 0,56,1024
M 22,v[68:71],26
R 46,2,37888
M 22,v[76:79],30
M 18,v[64:67],26
R 50,2,38912
M 18,v[72:75],30
L 3
M 18,v[80:83],34
R 54,2,39936
M 18,v[88:91],38
D 0,56,2048
M 22,v[84:87],34
R 26,2,40960
M 22,v[92:95],38
L 3
M 22,v[100:103],42
R 30,2,41984
M 22,v[108:111],46
M 18,v[96:99],42
R 34,2,43008
M 18,v[104:107],46
L 3
D 0,56,3072
M 18,v[112:115],50
s_add_u32 m0,s57,32768
s_add_u32 s56,s56,4096
R 38,2,44032
M 18,v[120:123],54
s_sub_u32 s57,s84,s57
M 22,v[116:119],50
R 42,2,45056
M 22,v[124:127],54
L 3
M 22,v[132:135],26
R 46,2,46080
M 22,v[140:143],30
D 0,56,0
M 18,v[128:131],26
R 50,2,47104
M 18,v[136:139],30
L 3
M 18,v[144:147],34
R 54,2,48128
M 18,v[152:155],38
M 22,v[148:151],34
R 26,2,49152
M 22,v[156:159],38
L 3
D 0,56,1024
M 22,v[164:167],42
R 30,2,50176
M 22,v[172:175],46
M 18,v[160:163],42
R 34,2,51200
M 18,v[168:171],46
L 3
M 18,v[176:179],50
R 38,2,52224
M 18,v[184:187],54
D 0,56,2048
M 22,v[180:183],50
R 42,2,53248
M 22,v[188:191],54
L 3
M 22,v[196:199],26
R 46,2,54272
M 22,v[204:207],30
M 18,v[192:195],26
R 50,2,55296
M 18,v[200:203],30
L 3
M 18,v[208:211],34
s_waitcnt vmcnt(7)
R 54,2,56320
M 18,v[216:219],38
s_barrier
ds_read_b32 v7,v6 offset:1024
M 22,v[212:215],34
R 26,1,0
M 22,v[220:223],38
L 4
M 22,v[228:231],42
R 30,1,1024
M 22,v[236:239],46
buffer_atomic_pk_add_bf16 v14,v9,s[40:43],s64 offen sc1
M 18,v[224:227],42
s_add_u32 s64,s75,8192
R 34,1,2048
M 18,v[232:235],46
L 4
buffer_atomic_pk_add_bf16 v15,v9,s[40:43],s64 offen sc1
M 18,v[240:243],50
s_add_u32 s64,s75,16384
L 3
R 38,1,3072
v_cmp_eq_u32 vcc,s53,v7
M 18,v[248:251],54
buffer_atomic_pk_add_bf16 v16,v9,s[40:43],s64 offen sc1
M 22,v[244:247],50
L 2
s_add_u32 s64,s75,24576
s_cmp_eq_u32 vcc_lo,-1
R 42,1,4096
M 22,v[252:255],54
buffer_atomic_pk_add_bf16 v17,v9,s[40:43],s64 offen sc1
s_cbranch_scc1 Lg4_Fa0_%=
Lg4_Ka0_%=:
Lg4_La0_%=:
s_nop 0
v_mfma_f32_16x16x32_bf16 v[14:17],a[4:7],v[26:29],0
s_add_u32 m0,s59,3072
R 46,1,5120
v_lshlrev_b32 v62,16,v58
M 14,a[12:15],30
buffer_load_dword v8,s[36:39],s58 offen offset:0 sc1 lds
v_mfma_f32_16x16x32_bf16 v[10:13],a[0:3],v[26:29],0
s_sub_u32 s75,s67,s85
R 50,1,6144
v_add_f32 v18,v18,v62
M 10,a[8:11],30
L 3
v_and_b32 v63,0xffff0000,v58
v_add_f32 v19,v19,v63
M 10,a[16:19],34
s_mov_b64 s[40:41],s[86:87]
R 54,1,7168
v_lshlrev_b32 v62,16,v59
M 10,a[24:27],38
s_mov_b32 s42,4194304
v_add_f32 v20,v20,v62
v_and_b32 v63,0xffff0000,v59
M 14,a[20:23],34
s_mov_b32 s43,0x00020000
R 26,1,8192
v_add_f32 v21,v21,v63
M 14,a[28:31],38
L 3
v_lshlrev_b32 v62,16,v60
v_add_f32 v22,v22,v62
M 14,a[36:39],42
s_add_u32 s64,s75,0
R 30,1,9216
v_and_b32 v63,0xffff0000,v60
M 14,a[44:47],46
s_lshl_b32 s62,s54,15
v_add_f32 v23,v23,v63
v_lshlrev_b32 v62,16,v61
M 10,a[32:35],42
s_mov_b32 s66,s65
R 34,1,10240
v_add_f32 v24,v24,v62
M 10,a[40:43],46
L 3
v_and_b32 v63,0xffff0000,v61
v_add_f32 v25,v25,v63
M 10,a[48:51],50
s_add_u32 s65,s47,s62
R 38,1,11264
v_cvt_pk_bf16_f32 v18,v18,v19
M 10,a[56:59],54
buffer_load_dwordx4 v[58:61],v0,s[36:39],s74 offen offset:0 sc1
M 14,a[52:55],50
s_add_u32 s74,s49,s62
R 42,1,12288
v_cvt_pk_bf16_f32 v19,v20,v21
M 14,a[60:63],54
L 3
v_cvt_pk_bf16_f32 v20,v22,v23
v_cvt_pk_bf16_f32 v21,v24,v25
M 14,a[68:71],26
R 46,1,13312
v_lshrrev_b32 v62,8,v0
M 14,a[76:79],30
v_lshl_add_u32 v62,v62,5,v0
v_add_u32 v62,s73,v62
M 10,a[64:67],26
R 50,1,14336
M 10,a[72:75],30
L 3
ds_write_b128 v62,v[18:21] offset:0
M 10,a[80:83],34
R 54,1,15360
ds_read2_b32 v[22:23],v5 offset0:0 offset1:16
M 10,a[88:91],38
ds_read2_b32 v[24:25],v5 offset0:32 offset1:48
M 14,a[84:87],34
R 26,1,16384
M 14,a[92:95],38
L 6
M 14,a[100:103],42
R 30,1,17408
M 14,a[108:111],46
M 10,a[96:99],42
R 34,1,18432
M 10,a[104:107],46
L 5
L 3
M 10,a[112:115],50
R 38,1,19456
M 10,a[120:123],54
buffer_atomic_pk_add_bf16 v22,v9,s[40:43],s64 offen sc1
M 14,a[116:119],50
s_add_u32 s64,s75,8192
R 42,1,20480
M 14,a[124:127],54
L 3
buffer_atomic_pk_add_bf16 v23,v9,s[40:43],s64 offen sc1
M 14,a[132:135],26
s_add_u32 s64,s75,16384
R 46,1,21504
M 14,a[140:143],30
buffer_atomic_pk_add_bf16 v24,v9,s[40:43],s64 offen sc1
M 10,a[128:131],26
s_add_u32 s64,s75,24576
R 50,1,22528
M 10,a[136:139],30
L 3
buffer_atomic_pk_add_bf16 v25,v9,s[40:43],s64 offen sc1
M 10,a[144:147],34
R 54,1,23552
M 10,a[152:155],38
M 14,a[148:151],34
R 26,1,24576
M 14,a[156:159],38
L 3
M 14,a[164:167],42
R 30,1,25600
M 14,a[172:175],46
M 10,a[160:163],42
R 34,1,26624
M 10,a[168:171],46
L 3
M 10,a[176:179],50
s_waitcnt vmcnt(5)
R 38,1,27648
M 10,a[184:187],54
s_barrier
ds_read_b32 v7,v6 offset:3072
M 14,a[180:183],50
R 42,1,28672
M 14,a[188:191],54
L 4
M 14,a[196:199],26
R 46,1,29696
M 14,a[204:207],30
M 10,a[192:195],26
R 50,1,30720
M 10,a[200:203],30
L 4
L 3
v_cmp_eq_u32 vcc,s53,v7
M 10,a[208:211],34
R 54,1,31744
M 10,a[216:219],38
M 14,a[212:215],34
s_cmp_eq_u32 vcc_lo,-1
R 26,1,32768
s_cbranch_scc0 Lg4_Xa0_%=
M 14,a[220:223],38
L 3
s_add_u32 m0,s57,0
s_add_u32 s56,s55,0
M 14,a[228:231],42
s_add_u32 s61,s54,s46
s_cmp_ge_u32 s61,s63
s_cselect_b32 s61,s93,s61
R 30,1,33792
M 14,a[236:239],46
s_mul_i32 s58,s61,2560
D 0,56,0
M 10,a[224:227],42
s_cselect_b32 s46,s88,s46
s_cselect_b32 s63,s89,s63
R 34,1,34816
M 10,a[232:235],46
L 3
s_cselect_b32 s93,s50,s93
s_cselect_b32 s88,8,s88
M 10,a[240:243],50
s_cselect_b32 s89,-1,s89
s_add_u32 s58,s58,109891584
R 38,1,35840
M 10,a[248:251],54
D 0,56,1024
M 14,a[244:247],50
s_mov_b32 s54,s61
R 42,1,36864
M 14,a[252:255],54
L 3
M 14,v[68:71],26
R 46,1,37888
M 14,v[76:79],30
D 0,56,2048
M 10,v[64:67],26
R 50,1,38912
M 10,v[72:75],30
L 3
M 10,v[80:83],34
R 54,1,39936
M 10,v[88:91],38
D 0,56,3072
M 14,v[84:87],34
s_add_u32 m0,s57,4096
s_add_u32 s56,s56,4096
R 26,1,40960
M 14,v[92:95],38
L 3
M 14,v[100:103],42
R 30,1,41984
M 14,v[108:111],46
D 0,56,0
M 10,v[96:99],42
R 34,1,43008
M 10,v[104:107],46
L 3
M 10,v[112:115],50
R 38,1,44032
M 10,v[120:123],54
D 0,56,1024
M 14,v[116:119],50
R 42,1,45056
M 14,v[124:127],54
L 3
M 14,v[132:135],26
R 46,1,46080
M 14,v[140:143],30
D 0,56,2048
M 10,v[128:131],26
s_add_u32 m0,s59,0
s_add_u32 s56,s55,28672
R 50,1,47104
M 10,v[136:139],30
L 3
buffer_load_dword v8,s[36:39],s58 offen offset:0 sc1 lds
M 10,v[144:147],34
s_add_u32 m0,s57,28672
s_mul_i32 s55,s61,114688
R 54,1,48128
M 10,v[152:155],38
s_add_u32 s55,s55,s60
M 14,v[148:151],34
R 26,1,49152
M 14,v[156:159],38
L 3
D 0,56,0
M 14,v[164:167],42
R 30,1,50176
M 14,v[172:175],46
M 10,v[160:163],42
R 34,1,51200
M 10,v[168:171],46
L 3
D 0,56,1024
M 10,v[176:179],50
R 38,1,52224
M 10,v[184:187],54
D 0,56,2048
M 14,v[180:183],50
R 42,1,53248
M 14,v[188:191],54
L 3
M 14,v[196:199],26
R 46,1,54272
M 14,v[204:207],30
D 0,56,3072
M 10,v[192:195],26
s_add_u32 m0,s57,32768
s_add_u32 s56,s56,4096
R 50,1,55296
M 10,v[200:203],30
L 3
s_sub_u32 s57,s84,s57
M 10,v[208:211],34
s_waitcnt vmcnt(4)
R 54,1,56320
M 10,v[216:219],38
s_barrier
ds_read_b32 v7,v6 offset:0
M 14,v[212:215],34
R 26,2,0
M 14,v[220:223],38
L 4
D 0,56,0
M 14,v[228:231],42
R 30,2,1024
M 14,v[236:239],46
D 0,56,1024
M 10,v[224:227],42
R 34,2,2048
M 10,v[232:235],46
L 4
D 0,56,2048
M 10,v[240:243],50
L 3
R 38,2,3072
v_cmp_eq_u32 vcc,s53,v7
M 10,v[248:251],54
M 14,v[244:247],50
L 2
s_cmp_eq_u32 vcc_lo,-1
R 42,2,4096
M 14,v[252:255],54
buffer_store_dword v4,v3,s[36:39],s71 offen offset:0 sc0 sc1
buffer_store_dword v4,v3,s[36:39],s71 offen offset:0 sc0 sc1
buffer_store_dword v4,v3,s[36:39],s71 offen offset:0 sc0 sc1
buffer_store_dword v4,v3,s[36:39],s71 offen offset:0 sc0 sc1
s_cbranch_scc1 Lg4_Fa1_%=
s_branch Lg4_Ka1_%=
Lg4_Xa0_%=:
M 14,a[220:223],38
L 3
s_add_u32 m0,s59,0
M 14,a[228:231],42
R 30,1,33792
M 14,a[236:239],46
buffer_load_dword v8,s[36:39],s58 offen offset:0 sc1 lds
M 10,a[224:227],42
R 34,1,34816
M 10,a[232:235],46
L 3
M 10,a[240:243],50
R 38,1,35840
M 10,a[248:251],54
M 14,a[244:247],50
R 42,1,36864
M 14,a[252:255],54
L 3
M 14,v[68:71],26
R 46,1,37888
M 14,v[76:79],30
M 10,v[64:67],26
R 50,1,38912
M 10,v[72:75],30
L 3
)"
R"(M 10,v[80:83],34
R 54,1,39936
M 10,v[88:91],38
M 14,v[84:87],34
R 26,1,40960
M 14,v[92:95],38
L 3
M 14,v[100:103],42
R 30,1,41984
M 14,v[108:111],46
M 10,v[96:99],42
R 34,1,43008
M 10,v[104:107],46
L 3
M 10,v[112:115],50
R 38,1,44032
M 10,v[120:123],54
M 14,v[116:119],50
R 42,1,45056
M 14,v[124:127],54
L 3
M 14,v[132:135],26
R 46,1,46080
M 14,v[140:143],30
M 10,v[128:131],26
R 50,1,47104
M 10,v[136:139],30
L 3
M 10,v[144:147],34
R 54,1,48128
M 10,v[152:155],38
M 14,v[148:151],34
R 26,1,49152
M 14,v[156:159],38
L 3
M 14,v[164:167],42
R 30,1,50176
M 14,v[172:175],46
M 10,v[160:163],42
R 34,1,51200
M 10,v[168:171],46
L 3
M 10,v[176:179],50
R 38,1,52224
M 10,v[184:187],54
M 14,v[180:183],50
R 42,1,53248
M 14,v[188:191],54
L 3
M 14,v[196:199],26
R 46,1,54272
M 14,v[204:207],30
M 10,v[192:195],26
R 50,1,55296
M 10,v[200:203],30
L 3
M 10,v[208:211],34
s_waitcnt vmcnt(0)
R 54,1,56320
M 10,v[216:219],38
s_barrier
ds_read_b32 v7,v6 offset:0
M 14,v[212:215],34
M 14,v[220:223],38
L 3
M 14,v[228:231],42
M 14,v[236:239],46
M 10,v[224:227],42
M 10,v[232:235],46
L 1
L 0
M 10,v[240:243],50
v_cmp_eq_u32 vcc,s53,v7
M 10,v[248:251],54
M 14,v[244:247],50
M 14,v[252:255],54
Lg4_pa0_%=:
s_mov_b32 s80,1
s_cmp_eq_u32 vcc_lo,-1
s_cbranch_scc0 Lg4_xna0_%=
s_add_u32 m0,s57,0
s_add_u32 s56,s55,0
D 0,56,0
s_sub_u32 s75,s66,s85
v_lshlrev_b32 v62,16,v58
D 0,56,1024
v_add_f32 v10,v10,v62
D 0,56,2048
v_and_b32 v63,0xffff0000,v58
D 0,56,3072
v_add_f32 v11,v11,v63
s_add_u32 m0,s57,4096
s_add_u32 s56,s56,4096
D 0,56,0
v_lshlrev_b32 v62,16,v59
D 0,56,1024
v_add_f32 v12,v12,v62
D 0,56,2048
v_and_b32 v63,0xffff0000,v59
s_add_u32 s61,s54,s46
s_cmp_ge_u32 s61,s63
s_cselect_b32 s61,s93,s61
s_cselect_b32 s46,s88,s46
s_cselect_b32 s63,s89,s63
s_cselect_b32 s93,s50,s93
s_cselect_b32 s88,8,s88
s_cselect_b32 s89,-1,s89
s_mul_i32 s58,s61,2560
s_add_u32 s58,s58,109891584
s_add_u32 m0,s59,2048
s_nop 0
buffer_load_dword v8,s[36:39],s58 offen offset:0 sc1 lds
v_add_f32 v13,v13,v63
s_add_u32 m0,s57,28672
s_add_u32 s56,s55,28672
D 0,56,0
v_lshlrev_b32 v62,16,v60
D 0,56,1024
v_add_f32 v14,v14,v62
D 0,56,2048
v_and_b32 v63,0xffff0000,v60
D 0,56,3072
v_add_f32 v15,v15,v63
s_add_u32 m0,s57,32768
s_add_u32 s56,s56,4096
D 0,56,0
v_lshlrev_b32 v62,16,v61
D 0,56,1024
v_add_f32 v16,v16,v62
D 0,56,2048
v_and_b32 v63,0xffff0000,v61
s_mul_i32 s55,s61,114688
s_add_u32 s55,s55,s60
s_sub_u32 s57,s84,s57
s_mov_b32 s54,s61
v_add_f32 v17,v17,v63
v_cvt_pk_bf16_f32 v10,v10,v11
v_cvt_pk_bf16_f32 v11,v12,v13
v_cvt_pk_bf16_f32 v12,v14,v15
v_cvt_pk_bf16_f32 v13,v16,v17
v_lshrrev_b32 v62,8,v0
v_lshl_add_u32 v62,v62,5,v0
v_add_u32 v62,s73,v62
ds_write_b128 v62,v[10:13] offset:0
ds_read2_b32 v[14:15],v5 offset0:0 offset1:16
ds_read2_b32 v[16:17],v5 offset0:32 offset1:48
s_mov_b64 s[40:41],s[86:87]
s_mov_b32 s42,4194304
s_mov_b32 s43,0x00020000
L 0
s_add_u32 s64,s75,0
buffer_atomic_pk_add_bf16 v14,v9,s[40:43],s64 offen sc1
s_add_u32 s64,s75,8192
buffer_atomic_pk_add_bf16 v15,v9,s[40:43],s64 offen sc1
s_add_u32 s64,s75,16384
buffer_atomic_pk_add_bf16 v16,v9,s[40:43],s64 offen sc1
s_add_u32 s64,s75,24576
buffer_atomic_pk_add_bf16 v17,v9,s[40:43],s64 offen sc1
s_mov_b32 s72,0x7fff0000
s_mov_b32 s69,0x7fff0000
s_mov_b32 s66,0x7fff0000
s_mov_b32 s76,0x7fff0000
s_waitcnt vmcnt(11)
s_barrier
s_branch Lg4_st1_%=
Lg4_xna0_%=:
s_cmp_ge_u32 s54,256
s_cbranch_scc1 Lg4_fina0_%=
s_add_u32 m0,s59,4096
s_nop 0
buffer_load_dword v8,s[36:39],s58 offen offset:0 sc1 lds
buffer_store_dword v4,v3,s[36:39],s71 offen offset:0 sc0 sc1
s_add_u32 m0,s59,5120
s_nop 0
buffer_load_dword v8,s[36:39],s58 offen offset:0 sc1 lds
buffer_store_dword v4,v3,s[36:39],s71 offen offset:0 sc0 sc1
s_mov_b32 s83,0
s_sub_u32 s75,s66,s85
v_lshlrev_b32 v62,16,v58
v_add_f32 v10,v10,v62
v_and_b32 v63,0xffff0000,v58
v_add_f32 v11,v11,v63
v_lshlrev_b32 v62,16,v59
v_add_f32 v12,v12,v62
v_and_b32 v63,0xffff0000,v59
v_add_f32 v13,v13,v63
v_lshlrev_b32 v62,16,v60
v_add_f32 v14,v14,v62
v_and_b32 v63,0xffff0000,v60
v_add_f32 v15,v15,v63
v_lshlrev_b32 v62,16,v61
v_add_f32 v16,v16,v62
v_and_b32 v63,0xffff0000,v61
v_add_f32 v17,v17,v63
v_cvt_pk_bf16_f32 v10,v10,v11
v_cvt_pk_bf16_f32 v11,v12,v13
v_cvt_pk_bf16_f32 v12,v14,v15
v_cvt_pk_bf16_f32 v13,v16,v17
v_lshrrev_b32 v62,8,v0
v_lshl_add_u32 v62,v62,5,v0
v_add_u32 v62,s73,v62
ds_write_b128 v62,v[10:13] offset:0
ds_read2_b32 v[14:15],v5 offset0:0 offset1:16
ds_read2_b32 v[16:17],v5 offset0:32 offset1:48
s_mov_b64 s[40:41],s[86:87]
s_mov_b32 s42,4194304
s_mov_b32 s43,0x00020000
L 0
s_add_u32 s64,s75,0
buffer_atomic_pk_add_bf16 v14,v9,s[40:43],s64 offen sc1
s_add_u32 s64,s75,8192
buffer_atomic_pk_add_bf16 v15,v9,s[40:43],s64 offen sc1
s_add_u32 s64,s75,16384
buffer_atomic_pk_add_bf16 v16,v9,s[40:43],s64 offen sc1
s_add_u32 s64,s75,24576
buffer_atomic_pk_add_bf16 v17,v9,s[40:43],s64 offen sc1
s_mov_b32 s72,0x7fff0000
s_mov_b32 s69,0x7fff0000
s_mov_b32 s66,0x7fff0000
s_mov_b32 s76,0x7fff0000
s_branch Lg4_poll_%=
Lg4_fina0_%=:
s_sub_u32 s75,s66,s85
v_lshlrev_b32 v62,16,v58
v_add_f32 v10,v10,v62
v_and_b32 v63,0xffff0000,v58
v_add_f32 v11,v11,v63
v_lshlrev_b32 v62,16,v59
v_add_f32 v12,v12,v62
v_and_b32 v63,0xffff0000,v59
v_add_f32 v13,v13,v63
v_lshlrev_b32 v62,16,v60
v_add_f32 v14,v14,v62
v_and_b32 v63,0xffff0000,v60
v_add_f32 v15,v15,v63
v_lshlrev_b32 v62,16,v61
v_add_f32 v16,v16,v62
v_and_b32 v63,0xffff0000,v61
v_add_f32 v17,v17,v63
v_cvt_pk_bf16_f32 v10,v10,v11
v_cvt_pk_bf16_f32 v11,v12,v13
v_cvt_pk_bf16_f32 v12,v14,v15
v_cvt_pk_bf16_f32 v13,v16,v17
v_lshrrev_b32 v62,8,v0
v_lshl_add_u32 v62,v62,5,v0
v_add_u32 v62,s73,v62
ds_write_b128 v62,v[10:13] offset:0
ds_read2_b32 v[14:15],v5 offset0:0 offset1:16
ds_read2_b32 v[16:17],v5 offset0:32 offset1:48
s_mov_b64 s[40:41],s[86:87]
s_mov_b32 s42,4194304
s_mov_b32 s43,0x00020000
L 0
s_add_u32 s64,s75,0
buffer_atomic_pk_add_bf16 v14,v9,s[40:43],s64 offen sc1
s_add_u32 s64,s75,8192
buffer_atomic_pk_add_bf16 v15,v9,s[40:43],s64 offen sc1
s_add_u32 s64,s75,16384
buffer_atomic_pk_add_bf16 v16,v9,s[40:43],s64 offen sc1
s_add_u32 s64,s75,24576
buffer_atomic_pk_add_bf16 v17,v9,s[40:43],s64 offen sc1
s_mov_b32 s72,0x7fff0000
s_mov_b32 s69,0x7fff0000
s_mov_b32 s66,0x7fff0000
s_mov_b32 s76,0x7fff0000
s_branch Lg4_done_%=
Lg4_Ka1_%=:
Lg4_La1_%=:
s_nop 0
v_mfma_f32_16x16x32_bf16 v[22:25],a[4:7],v[26:29],0
s_add_u32 m0,s59,3072
R 46,2,5120
v_lshlrev_b32 v62,16,v58
M 22,a[12:15],30
buffer_load_dword v8,s[36:39],s58 offen offset:0 sc1 lds
v_mfma_f32_16x16x32_bf16 v[18:21],a[0:3],v[26:29],0
s_sub_u32 s75,s66,s85
R 50,2,6144
v_add_f32 v10,v10,v62
M 18,a[8:11],30
L 3
v_and_b32 v63,0xffff0000,v58
v_add_f32 v11,v11,v63
M 18,a[16:19],34
s_mov_b64 s[40:41],s[86:87]
R 54,2,7168
v_lshlrev_b32 v62,16,v59
M 18,a[24:27],38
s_mov_b32 s42,4194304
v_add_f32 v12,v12,v62
v_and_b32 v63,0xffff0000,v59
M 22,a[20:23],34
s_mov_b32 s43,0x00020000
R 26,2,8192
v_add_f32 v13,v13,v63
M 22,a[28:31],38
L 3
v_lshlrev_b32 v62,16,v60
v_add_f32 v14,v14,v62
M 22,a[36:39],42
s_add_u32 s64,s75,0
R 30,2,9216
v_and_b32 v63,0xffff0000,v60
M 22,a[44:47],46
s_lshl_b32 s62,s54,15
v_add_f32 v15,v15,v63
v_lshlrev_b32 v62,16,v61
M 18,a[32:35],42
s_mov_b32 s67,s65
R 34,2,10240
v_add_f32 v16,v16,v62
M 18,a[40:43],46
L 3
v_and_b32 v63,0xffff0000,v61
v_add_f32 v17,v17,v63
M 18,a[48:51],50
s_add_u32 s65,s47,s62
R 38,2,11264
v_cvt_pk_bf16_f32 v10,v10,v11
M 18,a[56:59],54
buffer_load_dwordx4 v[58:61],v0,s[36:39],s74 offen offset:0 sc1
M 22,a[52:55],50
s_add_u32 s74,s49,s62
R 42,2,12288
v_cvt_pk_bf16_f32 v11,v12,v13
M 22,a[60:63],54
L 3
v_cvt_pk_bf16_f32 v12,v14,v15
v_cvt_pk_bf16_f32 v13,v16,v17
M 22,a[68:71],26
R 46,2,13312
v_lshrrev_b32 v62,8,v0
M 22,a[76:79],30
v_lshl_add_u32 v62,v62,5,v0
v_add_u32 v62,s73,v62
M 18,a[64:67],26
R 50,2,14336
M 18,a[72:75],30
L 3
ds_write_b128 v62,v[10:13] offset:0
M 18,a[80:83],34
R 54,2,15360
ds_read2_b32 v[14:15],v5 offset0:0 offset1:16
M 18,a[88:91],38
ds_read2_b32 v[16:17],v5 offset0:32 offset1:48
M 22,a[84:87],34
R 26,2,16384
M 22,a[92:95],38
L 6
M 22,a[100:103],42
R 30,2,17408
M 22,a[108:111],46
M 18,a[96:99],42
R 34,2,18432
M 18,a[104:107],46
L 5
L 3
M 18,a[112:115],50
R 38,2,19456
M 18,a[120:123],54
buffer_atomic_pk_add_bf16 v14,v9,s[40:43],s64 offen sc1
M 22,a[116:119],50
s_add_u32 s64,s75,8192
R 42,2,20480
M 22,a[124:127],54
L 3
buffer_atomic_pk_add_bf16 v15,v9,s[40:43],s64 offen sc1
M 22,a[132:135],26
s_add_u32 s64,s75,16384
R 46,2,21504
M 22,a[140:143],30
buffer_atomic_pk_add_bf16 v16,v9,s[40:43],s64 offen sc1
M 18,a[128:131],26
s_add_u32 s64,s75,24576
R 50,2,22528
M 18,a[136:139],30
L 3
buffer_atomic_pk_add_bf16 v17,v9,s[40:43],s64 offen sc1
M 18,a[144:147],34
R 54,2,23552
M 18,a[152:155],38
M 22,a[148:151],34
R 26,2,24576
M 22,a[156:159],38
L 3
M 22,a[164:167],42
R 30,2,25600
M 22,a[172:175],46
M 18,a[160:163],42
R 34,2,26624
M 18,a[168:171],46
L 3
M 18,a[176:179],50
s_waitcnt vmcnt(5)
R 38,2,27648
M 18,a[184:187],54
s_barrier
ds_read_b32 v7,v6 offset:3072
M 22,a[180:183],50
R 42,2,28672
M 22,a[188:191],54
L 4
M 22,a[196:199],26
R 46,2,29696
M 22,a[204:207],30
M 18,a[192:195],26
R 50,2,30720
M 18,a[200:203],30
L 4
L 3
v_cmp_eq_u32 vcc,s53,v7
M 18,a[208:211],34
R 54,2,31744
M 18,a[216:219],38
M 22,a[212:215],34
s_cmp_eq_u32 vcc_lo,-1
R 26,2,32768
s_cbranch_scc0 Lg4_Xa1_%=
M 22,a[220:223],38
L 3
s_add_u32 m0,s57,0
s_add_u32 s56,s55,0
M 22,a[228:231],42
s_add_u32 s61,s54,s46
s_cmp_ge_u32 s61,s63
s_cselect_b32 s61,s93,s61
R 30,2,33792
M 22,a[236:239],46
s_mul_i32 s58,s61,2560
D 0,56,0
M 18,a[224:227],42
s_cselect_b32 s46,s88,s46
s_cselect_b32 s63,s89,s63
R 34,2,34816
M 18,a[232:235],46
L 3
s_cselect_b32 s93,s50,s93
s_cselect_b32 s88,8,s88
M 18,a[240:243],50
s_cselect_b32 s89,-1,s89
s_add_u32 s58,s58,109891584
R 38,2,35840
M 18,a[248:251],54
D 0,56,1024
M 22,a[244:247],50
s_mov_b32 s54,s61
R 42,2,36864
M 22,a[252:255],54
L 3
M 22,v[68:71],26
R 46,2,37888
M 22,v[76:79],30
D 0,56,2048
M 18,v[64:67],26
R 50,2,38912
M 18,v[72:75],30
L 3
M 18,v[80:83],34
R 54,2,39936
M 18,v[88:91],38
D 0,56,3072
M 22,v[84:87],34
s_add_u32 m0,s57,4096
s_add_u32 s56,s56,4096
R 26,2,40960
M 22,v[92:95],38
L 3
M 22,v[100:103],42
R 30,2,41984
M 22,v[108:111],46
D 0,56,0
M 18,v[96:99],42
R 34,2,43008
M 18,v[104:107],46
L 3
M 18,v[112:115],50
R 38,2,44032
M 18,v[120:123],54
D 0,56,1024
M 22,v[116:119],50
R 42,2,45056
M 22,v[124:127],54
L 3
M 22,v[132:135],26
R 46,2,46080
M 22,v[140:143],30
D 0,56,2048
M 18,v[128:131],26
s_add_u32 m0,s59,1024
s_add_u32 s56,s55,28672
R 50,2,47104
M 18,v[136:139],30
L 3
buffer_load_dword v8,s[36:39],s58 offen offset:0 sc1 lds
M 18,v[144:147],34
s_add_u32 m0,s57,28672
s_mul_i32 s55,s61,114688
R 54,2,48128
M 18,v[152:155],38
s_add_u32 s55,s55,s60
)"
R"(M 22,v[148:151],34
R 26,2,49152
M 22,v[156:159],38
L 3
D 0,56,0
M 22,v[164:167],42
R 30,2,50176
M 22,v[172:175],46
M 18,v[160:163],42
R 34,2,51200
M 18,v[168:171],46
L 3
D 0,56,1024
M 18,v[176:179],50
R 38,2,52224
M 18,v[184:187],54
D 0,56,2048
M 22,v[180:183],50
R 42,2,53248
M 22,v[188:191],54
L 3
M 22,v[196:199],26
R 46,2,54272
M 22,v[204:207],30
D 0,56,3072
M 18,v[192:195],26
s_add_u32 m0,s57,32768
s_add_u32 s56,s56,4096
R 50,2,55296
M 18,v[200:203],30
L 3
s_sub_u32 s57,s84,s57
M 18,v[208:211],34
s_waitcnt vmcnt(4)
R 54,2,56320
M 18,v[216:219],38
s_barrier
ds_read_b32 v7,v6 offset:1024
M 22,v[212:215],34
R 26,1,0
M 22,v[220:223],38
L 4
D 0,56,0
M 22,v[228:231],42
R 30,1,1024
M 22,v[236:239],46
D 0,56,1024
M 18,v[224:227],42
R 34,1,2048
M 18,v[232:235],46
L 4
D 0,56,2048
M 18,v[240:243],50
L 3
R 38,1,3072
v_cmp_eq_u32 vcc,s53,v7
M 18,v[248:251],54
M 22,v[244:247],50
L 2
s_cmp_eq_u32 vcc_lo,-1
R 42,1,4096
M 22,v[252:255],54
buffer_store_dword v4,v3,s[36:39],s71 offen offset:0 sc0 sc1
buffer_store_dword v4,v3,s[36:39],s71 offen offset:0 sc0 sc1
buffer_store_dword v4,v3,s[36:39],s71 offen offset:0 sc0 sc1
buffer_store_dword v4,v3,s[36:39],s71 offen offset:0 sc0 sc1
s_cbranch_scc1 Lg4_Fa0_%=
s_branch Lg4_Ka0_%=
Lg4_Xa1_%=:
M 22,a[220:223],38
L 3
s_add_u32 m0,s59,1024
M 22,a[228:231],42
R 30,2,33792
M 22,a[236:239],46
buffer_load_dword v8,s[36:39],s58 offen offset:0 sc1 lds
M 18,a[224:227],42
R 34,2,34816
M 18,a[232:235],46
L 3
M 18,a[240:243],50
R 38,2,35840
M 18,a[248:251],54
M 22,a[244:247],50
R 42,2,36864
M 22,a[252:255],54
L 3
M 22,v[68:71],26
R 46,2,37888
M 22,v[76:79],30
M 18,v[64:67],26
R 50,2,38912
M 18,v[72:75],30
L 3
M 18,v[80:83],34
R 54,2,39936
M 18,v[88:91],38
M 22,v[84:87],34
R 26,2,40960
M 22,v[92:95],38
L 3
M 22,v[100:103],42
R 30,2,41984
M 22,v[108:111],46
M 18,v[96:99],42
R 34,2,43008
M 18,v[104:107],46
L 3
M 18,v[112:115],50
R 38,2,44032
M 18,v[120:123],54
M 22,v[116:119],50
R 42,2,45056
M 22,v[124:127],54
L 3
M 22,v[132:135],26
R 46,2,46080
M 22,v[140:143],30
M 18,v[128:131],26
R 50,2,47104
M 18,v[136:139],30
L 3
M 18,v[144:147],34
R 54,2,48128
M 18,v[152:155],38
M 22,v[148:151],34
R 26,2,49152
M 22,v[156:159],38
L 3
M 22,v[164:167],42
R 30,2,50176
M 22,v[172:175],46
M 18,v[160:163],42
R 34,2,51200
M 18,v[168:171],46
L 3
M 18,v[176:179],50
R 38,2,52224
M 18,v[184:187],54
M 22,v[180:183],50
R 42,2,53248
M 22,v[188:191],54
L 3
M 22,v[196:199],26
R 46,2,54272
M 22,v[204:207],30
M 18,v[192:195],26
R 50,2,55296
M 18,v[200:203],30
L 3
M 18,v[208:211],34
s_waitcnt vmcnt(0)
R 54,2,56320
M 18,v[216:219],38
s_barrier
ds_read_b32 v7,v6 offset:1024
M 22,v[212:215],34
M 22,v[220:223],38
L 3
M 22,v[228:231],42
M 22,v[236:239],46
M 18,v[224:227],42
M 18,v[232:235],46
L 1
L 0
M 18,v[240:243],50
v_cmp_eq_u32 vcc,s53,v7
M 18,v[248:251],54
M 22,v[244:247],50
M 22,v[252:255],54
Lg4_pa1_%=:
s_mov_b32 s80,0
s_cmp_eq_u32 vcc_lo,-1
s_cbranch_scc0 Lg4_xna1_%=
s_add_u32 m0,s57,0
s_add_u32 s56,s55,0
D 0,56,0
s_sub_u32 s75,s67,s85
v_lshlrev_b32 v62,16,v58
D 0,56,1024
v_add_f32 v18,v18,v62
D 0,56,2048
v_and_b32 v63,0xffff0000,v58
D 0,56,3072
v_add_f32 v19,v19,v63
s_add_u32 m0,s57,4096
s_add_u32 s56,s56,4096
D 0,56,0
v_lshlrev_b32 v62,16,v59
D 0,56,1024
v_add_f32 v20,v20,v62
D 0,56,2048
v_and_b32 v63,0xffff0000,v59
s_add_u32 s61,s54,s46
s_cmp_ge_u32 s61,s63
s_cselect_b32 s61,s93,s61
s_cselect_b32 s46,s88,s46
s_cselect_b32 s63,s89,s63
s_cselect_b32 s93,s50,s93
s_cselect_b32 s88,8,s88
s_cselect_b32 s89,-1,s89
s_mul_i32 s58,s61,2560
s_add_u32 s58,s58,109891584
s_add_u32 m0,s59,2048
s_nop 0
buffer_load_dword v8,s[36:39],s58 offen offset:0 sc1 lds
v_add_f32 v21,v21,v63
s_add_u32 m0,s57,28672
s_add_u32 s56,s55,28672
D 0,56,0
v_lshlrev_b32 v62,16,v60
D 0,56,1024
v_add_f32 v22,v22,v62
D 0,56,2048
v_and_b32 v63,0xffff0000,v60
D 0,56,3072
v_add_f32 v23,v23,v63
s_add_u32 m0,s57,32768
s_add_u32 s56,s56,4096
D 0,56,0
v_lshlrev_b32 v62,16,v61
D 0,56,1024
v_add_f32 v24,v24,v62
D 0,56,2048
v_and_b32 v63,0xffff0000,v61
s_mul_i32 s55,s61,114688
s_add_u32 s55,s55,s60
s_sub_u32 s57,s84,s57
s_mov_b32 s54,s61
v_add_f32 v25,v25,v63
v_cvt_pk_bf16_f32 v18,v18,v19
v_cvt_pk_bf16_f32 v19,v20,v21
v_cvt_pk_bf16_f32 v20,v22,v23
v_cvt_pk_bf16_f32 v21,v24,v25
v_lshrrev_b32 v62,8,v0
v_lshl_add_u32 v62,v62,5,v0
v_add_u32 v62,s73,v62
ds_write_b128 v62,v[18:21] offset:0
ds_read2_b32 v[22:23],v5 offset0:0 offset1:16
ds_read2_b32 v[24:25],v5 offset0:32 offset1:48
s_mov_b64 s[40:41],s[86:87]
s_mov_b32 s42,4194304
s_mov_b32 s43,0x00020000
L 0
s_add_u32 s64,s75,0
buffer_atomic_pk_add_bf16 v22,v9,s[40:43],s64 offen sc1
s_add_u32 s64,s75,8192
buffer_atomic_pk_add_bf16 v23,v9,s[40:43],s64 offen sc1
s_add_u32 s64,s75,16384
buffer_atomic_pk_add_bf16 v24,v9,s[40:43],s64 offen sc1
s_add_u32 s64,s75,24576
buffer_atomic_pk_add_bf16 v25,v9,s[40:43],s64 offen sc1
s_mov_b32 s72,0x7fff0000
s_mov_b32 s69,0x7fff0000
s_mov_b32 s67,0x7fff0000
s_mov_b32 s77,0x7fff0000
s_waitcnt vmcnt(11)
s_barrier
s_branch Lg4_st0_%=
Lg4_xna1_%=:
s_cmp_ge_u32 s54,256
s_cbranch_scc1 Lg4_fina1_%=
s_add_u32 m0,s59,4096
s_nop 0
buffer_load_dword v8,s[36:39],s58 offen offset:0 sc1 lds
buffer_store_dword v4,v3,s[36:39],s71 offen offset:0 sc0 sc1
s_add_u32 m0,s59,5120
s_nop 0
buffer_load_dword v8,s[36:39],s58 offen offset:0 sc1 lds
buffer_store_dword v4,v3,s[36:39],s71 offen offset:0 sc0 sc1
s_mov_b32 s83,0
s_sub_u32 s75,s67,s85
v_lshlrev_b32 v62,16,v58
v_add_f32 v18,v18,v62
v_and_b32 v63,0xffff0000,v58
v_add_f32 v19,v19,v63
v_lshlrev_b32 v62,16,v59
v_add_f32 v20,v20,v62
v_and_b32 v63,0xffff0000,v59
v_add_f32 v21,v21,v63
v_lshlrev_b32 v62,16,v60
v_add_f32 v22,v22,v62
v_and_b32 v63,0xffff0000,v60
v_add_f32 v23,v23,v63
v_lshlrev_b32 v62,16,v61
v_add_f32 v24,v24,v62
v_and_b32 v63,0xffff0000,v61
v_add_f32 v25,v25,v63
v_cvt_pk_bf16_f32 v18,v18,v19
v_cvt_pk_bf16_f32 v19,v20,v21
v_cvt_pk_bf16_f32 v20,v22,v23
v_cvt_pk_bf16_f32 v21,v24,v25
v_lshrrev_b32 v62,8,v0
v_lshl_add_u32 v62,v62,5,v0
v_add_u32 v62,s73,v62
ds_write_b128 v62,v[18:21] offset:0
ds_read2_b32 v[22:23],v5 offset0:0 offset1:16
ds_read2_b32 v[24:25],v5 offset0:32 offset1:48
s_mov_b64 s[40:41],s[86:87]
s_mov_b32 s42,4194304
s_mov_b32 s43,0x00020000
L 0
s_add_u32 s64,s75,0
buffer_atomic_pk_add_bf16 v22,v9,s[40:43],s64 offen sc1
s_add_u32 s64,s75,8192
buffer_atomic_pk_add_bf16 v23,v9,s[40:43],s64 offen sc1
s_add_u32 s64,s75,16384
buffer_atomic_pk_add_bf16 v24,v9,s[40:43],s64 offen sc1
s_add_u32 s64,s75,24576
buffer_atomic_pk_add_bf16 v25,v9,s[40:43],s64 offen sc1
s_mov_b32 s72,0x7fff0000
s_mov_b32 s69,0x7fff0000
s_mov_b32 s67,0x7fff0000
s_mov_b32 s77,0x7fff0000
s_branch Lg4_poll_%=
Lg4_fina1_%=:
s_sub_u32 s75,s67,s85
v_lshlrev_b32 v62,16,v58
v_add_f32 v18,v18,v62
v_and_b32 v63,0xffff0000,v58
v_add_f32 v19,v19,v63
v_lshlrev_b32 v62,16,v59
v_add_f32 v20,v20,v62
v_and_b32 v63,0xffff0000,v59
v_add_f32 v21,v21,v63
v_lshlrev_b32 v62,16,v60
v_add_f32 v22,v22,v62
v_and_b32 v63,0xffff0000,v60
v_add_f32 v23,v23,v63
v_lshlrev_b32 v62,16,v61
v_add_f32 v24,v24,v62
v_and_b32 v63,0xffff0000,v61
v_add_f32 v25,v25,v63
v_cvt_pk_bf16_f32 v18,v18,v19
v_cvt_pk_bf16_f32 v19,v20,v21
v_cvt_pk_bf16_f32 v20,v22,v23
v_cvt_pk_bf16_f32 v21,v24,v25
v_lshrrev_b32 v62,8,v0
v_lshl_add_u32 v62,v62,5,v0
v_add_u32 v62,s73,v62
ds_write_b128 v62,v[18:21] offset:0
ds_read2_b32 v[22:23],v5 offset0:0 offset1:16
ds_read2_b32 v[24:25],v5 offset0:32 offset1:48
s_mov_b64 s[40:41],s[86:87]
s_mov_b32 s42,4194304
s_mov_b32 s43,0x00020000
L 0
s_add_u32 s64,s75,0
buffer_atomic_pk_add_bf16 v22,v9,s[40:43],s64 offen sc1
s_add_u32 s64,s75,8192
buffer_atomic_pk_add_bf16 v23,v9,s[40:43],s64 offen sc1
s_add_u32 s64,s75,16384
buffer_atomic_pk_add_bf16 v24,v9,s[40:43],s64 offen sc1
s_add_u32 s64,s75,24576
buffer_atomic_pk_add_bf16 v25,v9,s[40:43],s64 offen sc1
s_mov_b32 s72,0x7fff0000
s_mov_b32 s69,0x7fff0000
s_mov_b32 s67,0x7fff0000
s_mov_b32 s77,0x7fff0000
s_branch Lg4_done_%=
Lg4_done_%=:
s_nop 0
s_barrier
buffer_store_dword v4,v3,s[36:39],s70 offen offset:0 sc0 sc1
s_waitcnt vmcnt(1)
s_barrier
buffer_store_dword v4,v3,s[36:39],s72 offen offset:0 sc0 sc1
s_waitcnt vmcnt(0) lgkmcnt(0)
s_barrier
s_endpgm
Lcode_g4_e:
.purgem M
.purgem R
.purgem T
.purgem D
.purgem L
)"
  :: "s"(ws), "s"(__builtin_amdgcn_readfirstlane(sWave)), "s"(__builtin_amdgcn_readfirstlane(sG0)), "s"(__builtin_amdgcn_readfirstlane(sStep)), "s"(__builtin_amdgcn_readfirstlane(sOutOff)), "s"(__builtin_amdgcn_readfirstlane(sFoutOff)), "s"(__builtin_amdgcn_readfirstlane(sAuxOff)), "s"(__builtin_amdgcn_readfirstlane(sWw)), "s"(__builtin_amdgcn_readfirstlane(sBuf)), "s"(__builtin_amdgcn_readfirstlane(sRk)), "s"(__builtin_amdgcn_readfirstlane(sExp)), "v"(vL16), "v"(vFlag), "v"(vSnapOff), "v"(vSnapRd), "v"(vWd0), "v"(vWd1), "v"(vWr0), "v"(vWr1), "v"(vSp), "v"(vSn), "s"(w4), "s"(out) : "v255", "v0", "v1", "v2", "v3", "v4", "v5", "v6", "v7", "v8", "v9", "v10", "v11", "v12", "v13", "v14", "v15", "v16", "v17", "v18", "v19", "v20", "v21", "v22", "v23", "v24", "v25", "v26", "v27", "v28", "v29", "v30", "v31", "v32", "v33", "v34", "v35", "v36", "v37", "v38", "v39", "v40", "v41", "v42", "v43", "v44", "v45", "v46", "v47", "v48", "v49", "v50", "v51", "v52", "v53", "v54", "v55", "v56", "v57", "v58", "v59", "v60", "v61", "v62", "v63", "v64", "v65", "v66", "v67", "v68", "v69", "v70", "v71", "v72", "v73", "v74", "v75", "v76", "v77", "v78", "v79", "v80", "v81", "v82", "v83", "v84", "v85", "v86", "v87", "v88", "v89", "v90", "v91", "v92", "v93", "v94", "v95", "v96", "v97", "v98", "v99", "v100", "v101", "v102", "v103", "v104", "v105", "v106", "v107", "v108", "v109", "v110", "v111", "v112", "v113", "v114", "v115", "v116", "v117", "v118", "v119", "v120", "v121", "v122", "v123", "v124", "v125", "v126", "v127", "v128", "v129", "v130", "v131", "v132", "v133", "v134", "v135", "v136", "v137", "v138", "v139", "v140", "v141", "v142", "v143", "v144", "v145", "v146", "v147", "v148", "v149", "v150", "v151", "v152", "v153", "v154", "v155", "v156", "v157", "v158", "v159", "v160", "v161", "v162", "v163", "v164", "v165", "v166", "v167", "v168", "v169", "v170", "v171", "v172", "v173", "v174", "v175", "v176", "v177", "v178", "v179", "v180", "v181", "v182", "v183", "v184", "v185", "v186", "v187", "v188", "v189", "v190", "v191", "v192", "v193", "v194", "v195", "v196", "v197", "v198", "v199", "v200", "v201", "v202", "v203", "v204", "v205", "v206", "v207", "v208", "v209", "v210", "v211", "v212", "v213", "v214", "v215", "v216", "v217", "v218", "v219", "v220", "v221", "v222", "v223", "v224", "v225", "v226", "v227", "v228", "v229", "v230", "v231", "a0", "a1", "a2", "a3", "a4", "a5", "a6", "a7", "a8", "a9", "a10", "a11", "a12", "a13", "a14", "a15", "a16", "a17", "a18", "a19", "a20", "a21", "a22", "a23", "a24", "a25", "a26", "a27", "a28", "a29", "a30", "a31", "a32", "a33", "a34", "a35", "a36", "a37", "a38", "a39", "a40", "a41", "a42", "a43", "a44", "a45", "a46", "a47", "a48", "a49", "a50", "a51", "a52", "a53", "a54", "a55", "a56", "a57", "a58", "a59", "a60", "a61", "a62", "a63", "a64", "a65", "a66", "a67", "a68", "a69", "a70", "a71", "a72", "a73", "a74", "a75", "a76", "a77", "a78", "a79", "a80", "a81", "a82", "a83", "a84", "a85", "a86", "a87", "a88", "a89", "a90", "a91", "a92", "a93", "a94", "a95", "a96", "a97", "a98", "a99", "a100", "a101", "a102", "a103", "a104", "a105", "a106", "a107", "a108", "a109", "a110", "a111", "a112", "a113", "a114", "a115", "a116", "a117", "a118", "a119", "a120", "a121", "a122", "a123", "a124", "a125", "a126", "a127", "a128", "a129", "a130", "a131", "a132", "a133", "a134", "a135", "a136", "a137", "a138", "a139", "a140", "a141", "a142", "a143", "a144", "a145", "a146", "a147", "a148", "a149", "a150", "a151", "a152", "a153", "a154", "a155", "a156", "a157", "a158", "a159", "a160", "a161", "a162", "a163", "a164", "a165", "a166", "a167", "a168", "a169", "a170", "a171", "a172", "a173", "a174", "a175", "a176", "a177", "a178", "a179", "a180", "a181", "a182", "a183", "a184", "a185", "a186", "a187", "a188", "a189", "a190", "a191", "a192", "a193", "a194", "a195", "a196", "a197", "a198", "a199", "a200", "a201", "a202", "a203", "a204", "a205", "a206", "a207", "a208", "a209", "a210", "a211", "a212", "a213", "a214", "a215", "a216", "a217", "a218", "a219", "a220", "a221", "a222", "a223", "a224", "a225", "a226", "a227", "a228", "a229", "a230", "a231", "a232", "a233", "a234", "a235", "a236", "a237", "a238", "a239", "a240", "a241", "a242", "a243", "a244", "a245", "a246", "a247", "a248", "a249", "a250", "a251", "a252", "a253", "a254", "a255", "s36", "s37", "s38", "s39", "s40", "s41", "s42", "s43", "s44", "s45", "s46", "s47", "s48", "s49", "s50", "s51", "s52", "s53", "s54", "s55", "s56", "s57", "s58", "s59", "s60", "s61", "s62", "s63", "s64", "s65", "s66", "s67", "s68", "s69", "s70", "s71", "s72", "s73", "s74", "s75", "s76", "s77", "s78", "s79", "s80", "s81", "s82", "s83", "s84", "s85", "s86", "s87", "s88", "s89", "s90", "s91", "s92", "s93", "s94", "s95", "s96", "s97", "vcc", "scc", "memory");
    __builtin_unreachable();
  } else {
    LANE_ID(lane2);
    constexpr int N1 = 33 - (NR4 - 6) * 16, T1 = LAST_TASKS > RLN * N1 ? RLN * N1 : LAST_TASKS;
    const int w = kind == 6 ? 0 : kind == 1 ? 1 + (3 - rep) * 8 + slice : N1 + rep * 8 + slice;
    if (kind != 2) for (int t = w; t < T1; t += N1) row_last(ws, out, t, lane2, wave, ep);
    for (int t = T1 + (w + 32) % (N1 + 32); t < LAST_TASKS; t += N1 + 32) row_last(ws, out, t, lane2, wave, ep);
    if (kind == 2 && rep == 0 && slice == 0 && wave == 0) set_flag(ws + OFF_EP, lane2, ep);
    CTS(14);
    asm volatile("s_endpgm\nLcode_end:" ::: "memory");
    __builtin_unreachable();
  }
}

extern "C" void kernel_launch(void* const* d_in, const int* in_sizes, int n_in, void* d_out, int out_size,
                              void* d_ws, size_t ws_size, hipStream_t stream) {
  const u16* q = (const u16*)d_in[0];
  const u16* k = (const u16*)d_in[1];
  const u16* v = (const u16*)d_in[2];
  const u16* shift_k = (const u16*)d_in[3];
  const u16* ssm_k = (const u16*)d_in[4];
  const u16* ssm_D = (const u16*)d_in[5];
  const u16* w1 = (const u16*)d_in[7];
  const u16* w2 = (const u16*)d_in[8];
  const u16* w3 = (const u16*)d_in[9];
  const u16* w4 = (const u16*)d_in[10];
  flow_kernel<<<256, 256, 0, stream>>>(q, k, v, shift_k, ssm_k, ssm_D, w1, w2, w3, w4, (char*)d_ws, (u16*)d_out);
}
```

```cpp
#include <hip/hip_runtime.h>
#include <hip/hip_bf16.h>
#include <stdint.h>
#include <type_traits>

typedef short s16x8 __attribute__((ext_vector_type(8)));
typedef short s16x4 __attribute__((ext_vector_type(4)));
typedef float f32x4 __attribute__((ext_vector_type(4)));
typedef unsigned short u16;
typedef unsigned short u16x8 __attribute__((ext_vector_type(8)));
typedef unsigned short u16x4 __attribute__((ext_vector_type(4)));
typedef float f32x2 __attribute__((ext_vector_type(2)));
typedef __bf16 bf16x2 __attribute__((ext_vector_type(2)));
typedef unsigned u32x4v __attribute__((ext_vector_type(4)));
typedef __attribute__((address_space(1))) unsigned gu32;

constexpr int S_ = 2048, D_ = 1024, F_ = 3584;

constexpr int FSX = 128, FSH = 128, FS1 = 128, FSG = 128, FSP = 128;
constexpr unsigned GELU_CB = 0xbeeff558u;
constexpr int WS_USED = 111243392, LAST_TASKS = 100, ALLZ = 1, U0 = 170, U1 = 224, NR4 = 8, RHELP = 1, PRE1 = 1;
constexpr int OFF_KVR = 4194304, OFF_XP = 8388608, OFF_HS = 17825792, OFF_X1 = 31457280, OFF_G = 40894464, OFF_P0 = 71303168, OFF_P1 = 89128960, OFF_Y = 98566144, OFF_S1 = 106954752, OFF_ST = 107479040, OFF_FL = 109051904, OFF_FXP = 109056000, OFF_FH = 109334528, OFF_FX1 = 109613056, OFF_FG = 109891584, OFF_FP = 110587904, OFF_FKV = 111144960, OFF_FY = 111177728, OFF_EP = 111243264, OFF_DBG = 115343360;

#define WS_DIAG 0

__device__ __forceinline__ float bf2f(u16 v) { return __uint_as_float(((unsigned)v) << 16); }
__device__ __forceinline__ u16 f2bf(float f) { return __builtin_bit_cast(u16, (__bf16)f); }
__device__ __forceinline__ float rbf(float f) { return bf2f(f2bf(f)); }
__device__ __forceinline__ void st16(void* p, u16x8 v) {
  asm volatile("global_store_dwordx4 %0, %1, off sc0 sc1\n\ts_nop 1" :: "v"(p), "v"(v) : "memory");
}
#define WAIT_VM(n) asm volatile("s_waitcnt vmcnt(%0)" ::"n"(n) : "memory")
#define WAIT_LGKM0() asm volatile("s_waitcnt lgkmcnt(0)" ::: "memory")

__device__ __forceinline__ void touch_lds(const char* p, unsigned a) {
  asm volatile("s_mov_b32 m0, %1\n\ts_nop 0\n\tglobal_load_lds_dword %0, off" :: "v"(p), "s"(a) : "memory");
}
__device__ __forceinline__ void touch_sc1(const char* p, unsigned a) {
  asm volatile("s_mov_b32 m0, %1\n\ts_nop 0\n\tglobal_load_lds_dword %0, off sc1" :: "v"(p), "s"(a) : "memory");
}
#define CTS(i)
__device__ __forceinline__ f32x2 up2(unsigned p) { return (f32x2){__uint_as_float(p << 16), __uint_as_float(p & 0xffff0000u)}; }
__device__ __forceinline__ unsigned pk2(f32x2 v) { return __builtin_bit_cast(unsigned, __builtin_convertvector(v, bf16x2)); }
__device__ __forceinline__ f32x2 rbf2(f32x2 v) { return up2(pk2(v)); }
__device__ __forceinline__ void set_flag(char* p, int i, unsigned ep) {
  if (i < 32) __hip_atomic_store((gu32*)p + i, ep, __ATOMIC_RELAXED, __HIP_MEMORY_SCOPE_AGENT);
}
constexpr int CV_HP = (2048 + 32) * 2, CV_UR = (2048 + 256) * 2, CV_CH = 2 * CV_HP + CV_UR;
constexpr int NCV = 174;
constexpr int CV_STG = 8 * CV_CH;
constexpr int YP = D_ * 16;

struct KvRegs { u32x4v k0, k1, k2, k3, k4, k5, k6, v0, v1, v2, v3, s0, s1, s2, s3; };
#define KV_LOADS                                                                                                                       \
  "v_lshrrev_b32 %[a], 3, %[tid]\n\tv_and_b32 %[c], 7, %[tid]\n\ts_lshr_b32 s90, %[tile], 4\n\ts_and_b32 s91, %[tile], 15\n\t"          \
  "v_lshlrev_b32 %[a], 13, %[a]\n\tv_lshlrev_b32 %[c], 4, %[c]\n\ts_lshl_b32 s90, s90, 18\n\ts_lshl_b32 s91, s91, 7\n\t"                 \
  "v_add_u32 %[a], s90, %[a]\n\tv_add_u32 %[c], s91, %[c]\n\tv_subrev_u32 %[h], 0x1000, %[a]\n\tv_max_i32 %[h], 0x800, %[h]\n\t"         \
  "v_add3_u32 %[a], %[a], %[c], s92\n\tv_add_u32 %[h], %[h], %[c]\n\tv_lshlrev_b32 %[c], 2, %[c]\n\ts_waitcnt lgkmcnt(0)\n\t"            \
  "global_load_dwordx4 %3, %[a], s[76:77] offset:-4096 nt\n\tglobal_load_dwordx4 %4, %[a], s[76:77] offset:-2048 nt\n\t"                \
  "global_load_dwordx4 %5, %[a], s[76:77] nt\n\tglobal_load_dwordx4 %6, %[a], s[76:77] offset:2048 nt\n\t"                              \
  "global_load_dwordx4 %7, %[a], s[78:79] offset:-4096 nt\n\tglobal_load_dwordx4 %8, %[a], s[78:79] offset:-2048 nt\n\t"                \
  "global_load_dwordx4 %9, %[a], s[78:79] nt\n\tglobal_load_dwordx4 %10, %[a], s[78:79] offset:2048 nt\n\t"                             \
  "global_load_dwordx4 %0, %[h], s[76:77] offset:-2048 nt\n\tglobal_load_dwordx4 %1, %[h], s[76:77] nt\n\t"                             \
  "global_load_dwordx4 %2, %[h], s[76:77] offset:2048 nt\n\t"                                                                           \
  "global_load_dwordx4 %11, %[c], s[80:81]\n\tglobal_load_dwordx4 %12, %[c], s[80:81] offset:16\n\t"                                    \
  "global_load_dwordx4 %13, %[c], s[80:81] offset:32\n\tglobal_load_dwordx4 %14, %[c], s[80:81] offset:48\n\t"
#define KV_OUTS(R) "=&v"(R.k0), "=&v"(R.k1), "=&v"(R.k2), "=&v"(R.k3), "=&v"(R.k4), "=&v"(R.k5), "=&v"(R.k6), "=&v"(R.v0), "=&v"(R.v1), "=&v"(R.v2), \
                   "=&v"(R.v3), "=&v"(R.s0), "=&v"(R.s1), "=&v"(R.s2), "=&v"(R.s3), [a] "=&v"(ta), [h] "=&v"(th), [c] "=&v"(tc)
#define KV_CLOB "s74", "s75", "s76", "s77", "s78", "s79", "s80", "s81", "s82", "s83", "s90", "s91", "s92", "scc", "vcc", "memory"
template <int N>
__device__ __forceinline__ void kv_wait(KvRegs& R) {
  asm volatile("s_waitcnt vmcnt(%15)" : "+v"(R.k0), "+v"(R.k1), "+v"(R.k2), "+v"(R.k3), "+v"(R.k4), "+v"(R.k5), "+v"(R.k6), "+v"(R.v0), "+v"(R.v1), "+v"(R.v2),
               "+v"(R.v3), "+v"(R.s0), "+v"(R.s1), "+v"(R.s2), "+v"(R.s3) : "n"(N) : "memory");
}
__device__ __forceinline__ void kv_store(KvRegs& R, char* __restrict__ ws, char* T, int b, int tid) {
  const int t0 = (b >> 4) * 128, c0 = (b & 15) * 64, ch8 = tid & 7, rg = tid >> 3;
  if (t0 + 4 * rg < 3) R.k0 = R.k1 = R.k2 = (u32x4v){0u, 0u, 0u, 0u};
  typedef unsigned u32x2 __attribute__((ext_vector_type(2)));
  const u32x4v kr[7] = {R.k0, R.k1, R.k2, R.k3, R.k4, R.k5, R.k6}, vr[4] = {R.v0, R.v1, R.v2, R.v3}, sk[4] = {R.s0, R.s1, R.s2, R.s3};
#pragma unroll
  for (int j2 = 0; j2 < 4; ++j2) {
    const f32x2 sa = up2(sk[j2][0]), sb = up2(sk[j2][1]), sc = up2(sk[j2][2]), sd = up2(sk[j2][3]);
    const f32x2 s0 = {sa[0], sc[0]}, s1 = {sa[1], sc[1]}, s2 = {sb[0], sd[0]}, s3 = {sb[1], sd[1]};
    f32x2 kf[7], o[4];
#pragma unroll
    for (int r = 0; r < 7; ++r) kf[r] = up2(kr[r][j2]);
#pragma unroll
    for (int r = 0; r < 4; ++r)
      o[r] = __builtin_elementwise_fma(kf[r + 3], s0, __builtin_elementwise_fma(kf[r + 2], s1, __builtin_elementwise_fma(kf[r + 1], s2, kf[r] * s3))) * up2(vr[r][j2]);
#pragma unroll
    for (int e = 0; e < 2; ++e)
      *(u32x2*)(T + (8 * ch8 + 2 * j2 + e) * 264 + (124 - 4 * rg) * 2) = (u32x2){pk2((f32x2){o[3][e], o[2][e]}), pk2((f32x2){o[1][e], o[0][e]})};
  }
  __syncthreads();
  u16* kvr = (u16*)(ws + OFF_KVR);
#pragma unroll
  for (int i = 0; i < 4; ++i) {
    const int idx = tid + 256 * i, ch = idx >> 4, cb = idx & 15;
    const u16x4 lo = *(const u16x4*)(T + ch * 264 + cb * 16), hi = *(const u16x4*)(T + ch * 264 + cb * 16 + 8);
    st16(kvr + (size_t)(c0 + ch) * S_ + (1920 - t0) + cb * 8, __builtin_shufflevector(lo, hi, 0, 1, 2, 3, 4, 5, 6, 7));
  }
}

#define LASTSLEEP 4
__device__ __forceinline__ void wait_flags(const char* p, unsigned ep) {
  gu32* f = (gu32*)p;
  for (int it = 0; it < 20000; ++it) {
    if (__all(__hip_atomic_load(f, __ATOMIC_RELAXED, __HIP_MEMORY_SCOPE_AGENT) == ep)) break;
    __builtin_amdgcn_s_sleep(LASTSLEEP);
  }
  asm volatile("" ::: "memory");
}
__device__ __forceinline__ void row_last(char* __restrict__ ws, u16* __restrict__ out, int t, int lane, int wave, unsigned ep) {
  const __amdgpu_buffer_rsrc_t rs = __builtin_amdgcn_make_buffer_rsrc(ws, 0, WS_USED, 0x00020000);
  const int r8 = lane & 7, seg = lane >> 3, un = 128 + (t >> 1);
  const unsigned fo = un * 32768 + (wave * 8 + (seg >> 2)) * 1024 + ((seg & 3) * 16 + (t & 1) * 8 + r8) * 16;
  wait_flags(ws + OFF_FP + un * 2048 + ((lane & 2) * 4 + wave * 2 + (lane & 1)) * FSP, ep);
  u32x4v p0[4], p1[4];
#pragma unroll
  for (int i = 0; i < 4; ++i) {
    p0[i] = __builtin_bit_cast(u32x4v, __builtin_amdgcn_raw_buffer_load_b128(rs, fo + i * 2048, OFF_P0, 16));
    p1[i] = __builtin_bit_cast(u32x4v, __builtin_amdgcn_raw_buffer_load_b128(rs, fo + i * 2048, OFF_P1, 16));
  }
#pragma unroll
  for (int i = 0; i < 4; ++i) {
    u32x4v o;
#pragma unroll
    for (int e = 0; e < 4; ++e) o[e] = pk2(up2(p0[i][e]) + up2(p1[i][e]));
    st16(out + (size_t)(t * 8 + r8) * D_ + wave * 256 + i * 64 + seg * 8, __builtin_bit_cast(u16x8, o));
  }
}

__device__ __forceinline__ int swzB(int r) { return ((((r >> 1) & 1) | (((r >> 3) & 1) << 1)) << 1); }
#define LANE_ID(x) int x; asm volatile("v_mbcnt_lo_u32_b32 %0, -1, 0\n\tv_mbcnt_hi_u32_b32 %0, -1, %0" : "=v"(x))
#define W12 6
#define RLN 3
#define FT 3
#define QTB 1
constexpr int QT0 = 34 * 128;
#define CODE_MAX(x, y) "(((" x ">" y ")&" x ")|((" x "<=" y ")&" y "))"
constexpr int HU = 40960;
#define G3OR 5
constexpr int NRW = 22 - G3OR;
__global__ __launch_bounds__(256, 1) void flow_kernel(const u16* __restrict__ q, const u16* __restrict__ kin, const u16* __restrict__ vin,
                                                      const u16* __restrict__ shift_k, const u16* __restrict__ ssm_k, const u16* __restrict__ ssm_D,
                                                      const u16* __restrict__ w1, const u16* __restrict__ w2,
                                                      const u16* __restrict__ w3, const u16* __restrict__ w4, char* __restrict__ ws,
                                                      u16* __restrict__ out) {
  __shared__ __attribute__((aligned(16))) char lds[163840];
  KvRegs KR;
  unsigned ew, ta, th, tc;
  int b, tile;
  asm volatile("Lcode_b:\n\ts_load_dwordx4 s[76:79], %[ka], 0x8\n\ts_load_dwordx2 s[82:83], %[ka], 0x50\n\t"
               "s_getpc_b64 s[74:75]\n\ts_lshr_b32 s90, %[bi], 3\n\ts_mov_b32 m0, 0x27f00\n\tv_lshlrev_b32 %[a], 5, %[tid]\n\tv_add_lshl_u32 %[a], %[a], s90, 7\n\t"
               "v_min_u32 %[a], " CODE_MAX(CODE_MAX(CODE_MAX("(Lcode_g3_e-Lcode_b)", "(Lcode_g3o_e-Lcode_b)"), CODE_MAX("(Lcode_g4_e-Lcode_b)", "(Lcode_g12_e-Lcode_b)")), "(Lcode_end-Lcode_b)") "-64, %[a]\n\t"
               "global_load_lds_dword %[a], s[74:75]\n\ts_load_dwordx2 s[80:81], %[ka], 0x18\n\t"
               "s_mov_b32 %[b], %[bi]\n\ts_bfe_u32 s91, %[bi], 0x20001\n\ts_lshl2_add_u32 %[tile], s90, s91\n\ts_movk_i32 s92, 0x1000\n\t"
               "s_lshl_b32 s90, %[bi], 7\n\ts_add_u32 %[tile], %[tile], s90\n\ts_add_u32 %[tile], %[tile], 64\n\ts_and_b32 %[tile], %[tile], 255\n\t"
               KV_LOADS "v_mov_b32 %[a], %[eo]\n\tglobal_load_dword %[ew], %[a], s[82:83] sc1"
               : KV_OUTS(KR), [ew] "=&v"(ew), [b] "=&s"(b), [tile] "=&s"(tile)
               : [ka] "s"(__builtin_amdgcn_kernarg_segment_ptr()), [bi] "s"(blockIdx.x), [tid] "v"(threadIdx.x), [eo] "n"(OFF_EP) : KV_CLOB);
  const unsigned t_start = 0;
  const int tid = threadIdx.x, lane = tid & 63, wave = __builtin_amdgcn_readfirstlane(tid >> 6);
  const int xcd = b & 7, idx = b >> 3;
  const unsigned sLds = (unsigned)(size_t)lds;
  const int o8 = idx & 7;
  int kind, rep, slice, kh = 0;
  int j3 = 0, j4 = 0;
  const int xp = xcd >> 1, pr = xp & 1;
  if (!(xcd & 1)) {
    if (idx < 20) { kind = 4; rep = idx < 16 ? (xp >> 1) * 3 + (idx >> 3) : 2; kh = pr; slice = idx < 16 ? o8 : (xp >> 1) * 4 + o8; j4 = rep * 16 + kh * 8 + slice; }
    else if (idx >= 24) { kind = 1; rep = xp; slice = o8; }
    else { kind = 3; rep = 4; slice = xp * 4 + idx - 20; j3 = 72 + slice; }
  } else if (idx >= 24) { kind = 2; rep = xp; slice = o8; }
  else if (idx < 18) { kind = 3; rep = pr * 2 + (idx >= 9); slice = (xp >> 1) * 9 + idx - 9 * (idx >= 9); j3 = rep * 18 + slice; }
  else if (idx < 22 || b == 191) { kind = 6; rep = 0; slice = idx < 22 ? xp * 4 + idx - 18 : 16; }
  else if (idx == 22 || xp == 0) { kind = 5; rep = idx == 22 ? pr * 2 + (xp >> 1) : 4; slice = 18; j3 = 90 + rep; }
  else { kind = 3; rep = 4; slice = 15 + xp; j3 = 72 + slice; }
  CTS(0);
  const unsigned sScr = sLds + 163584;
  unsigned ep = 0;
  auto w_touch = [&](const u16* w) {
#pragma unroll
    for (int i = 0; i < 2; ++i) touch_lds((const char*)w + (size_t)(rep * 256 + i * 128 + (tid >> 1)) * 2048 + slice * 256 + (tid & 1) * 128, sScr);
  };
  {
  const bool isconv = kind >= 3 && kind != 6 && j3 < 94;
  const int nw = (kind == 4 || j3 >= 92) ? 2 : 1, c0 = kind == 4 ? 8 * j4 : j3 < 92 ? 640 + 4 * j3 : j3 < 94 ? 272 + 8 * j3 : 0, cw = c0 + nw * wave;
  const unsigned hd = isconv ? sLds + 2 * wave * CV_CH + 32 : 0x30000u;
  auto h_dma = [&](bool first) {
#pragma unroll
    for (int j = 0; j < 2; ++j) {
      const u16* hp = ssm_k + (size_t)cw * S_ + (isconv && j < nw ? j * S_ + lane * 8 : 0);
      if (first) asm volatile("s_mov_b32 m0, %1\n\ts_nop 0\n\tglobal_load_lds_dwordx4 %0, off nt" :: "v"(hp), "s"(hd + j * CV_CH) : "memory");
      else asm volatile("s_mov_b32 m0, %1\n\ts_nop 0\n\tglobal_load_lds_dwordx4 %0, off offset:1024 nt\n\t"
                        "global_load_lds_dwordx4 %0, off offset:2048 nt\n\tglobal_load_lds_dwordx4 %0, off offset:3072 nt" :: "v"(hp), "s"(hd + j * CV_CH) : "memory");
    }
  };
  auto w_dma = [&](int t0, int t1) {
    const char* wp = (const char*)w1 + (16 * wave + (lane >> 3)) * 2048 + slice * 256;
    const unsigned c0_ = ((lane & 7) ^ swzB(lane >> 3)) * 16, c1_ = 8 * 2048 + ((lane & 7) ^ swzB(8 + (lane >> 3))) * 16;
#pragma unroll
    for (int t = t0; t < t1; ++t)
#pragma unroll
      for (int i = 0; i < 2; ++i)
        asm volatile("s_mov_b32 m0, %1\n\ts_nop 0\n\tglobal_load_lds_dwordx4 %0, off\n\ts_add_u32 m0, m0, 8064\n\ts_nop 0\n\tglobal_load_lds_dwordx4 %0, off offset:128"
                     :: "v"(wp + t * 131072 + (i ? c1_ : c0_)), "s"(sLds + t * 16384 + wave * 2048 + i * 1024) : "scc", "memory");
  };
  if (PRE1 && kind == 1) w_dma(0, 2);
  else { h_dma(true); h_dma(false); }
  if (isconv) *(u32x4v*)(lds + (2 * wave + (lane >> 5)) * CV_CH + 2 * CV_HP + 4096 + (lane & 31) * 16) = (u32x4v){0u, 0u, 0u, 0u};
  if (isconv && lane < 8) *(u32x4v*)(lds + (2 * wave + (lane >> 2)) * CV_CH + ((lane >> 1) & 1) * CV_HP + (lane & 1) * 16) = (u32x4v){0u, 0u, 0u, 0u};
  if (FT & 2) touch_sc1(ws + OFF_FKV + (min(wave, 2) * 256 + b) * 128, sScr);
  kv_wait<(FT & 2) ? 10 : 9>(KR);
  CTS(12);
  kv_store(KR, ws, lds + CV_STG, tile, tid);
  CTS(1);
  if (PRE1 && kind == 1) w_dma(2, 6);
  const unsigned dw = ((const unsigned*)ssm_D)[cw >> 1];
  asm volatile("s_cmp_eq_u32 %1, %2\n\ts_cbranch_scc1 Lew%=\n\ts_waitcnt vmcnt(0)\nLew%=:\n\ts_waitcnt vmcnt(16)" : "+v"(ew) : "s"(__builtin_amdgcn_readfirstlane(kind)), "n"(PRE1 ? 1 : 99) : "scc", "memory");
  ep = __builtin_amdgcn_readfirstlane(ew) + 1u;
  if (ep == 0u) ep = 1u;
  __builtin_amdgcn_s_barrier();
  set_flag(ws + OFF_FKV + tile * 128, tid, ep);
  CTS(2);
  if (kind < 3 && (W12 & kind) != 0) w_touch(kind == 1 ? w1 : w2);
  if (kind == 2 && (W12 & 4) != 0) w_touch(w1);
  if (kind == 2 && FT) {
#pragma unroll
    for (int i = 0; i < 2; ++i) touch_sc1(ws + OFF_FXP + (size_t)min((i * 32 + rep * 8 + slice) * 256 + tid, (OFF_FKV - OFF_FXP) / 128 - 1) * 128, sScr);
  }
  if (isconv) {
    LANE_ID(lane);
    const int tid = wave * 64 + lane;
    const unsigned sC0 = (cw & 1) ? dw & 0xffff0000u : dw << 16, sC1 = dw & 0xffff0000u;
    const int l15 = lane & 15, lg = lane >> 4, cp = (l15 + 1) & 1, iw = kind == 4 ? j4 : 80 + j3, own = 2 * wave * CV_CH;
    const int ch = tid & (4 * nw - 1), tb = nw == 2 ? tid >> 3 : tid >> 2, slot = nw == 2 ? ch : 2 * ch;
    const unsigned sWave = wave, sBuf = nw, sAuxOff = OFF_KVR + cw * 4096, sFoutOff = OFF_FY + iw * 128, sExp = __builtin_amdgcn_readfirstlane(ep), sDbg = 110u * 1048576u + (b * 4 + wave) * 8192;
    const unsigned vL16 = lane * 16, vPA = own + cp * CV_HP + 2 * (8 * lg + l15 + 1 - cp), vPB = own + 2 * CV_HP + 2 * (2032 - 16 * l15 + 8 * lg) - 512 * 7;
    const unsigned vPU = own + 2 * CV_HP + 2 * (2044 - 16 * l15 - 4 * lg) - 512 * 7, vPW = 32 * l15 + 8 * lg;
    const unsigned vOR = slot * CV_CH + tb * 16, vORA = CV_STG + slot * 1024 + tb * 16, vOG = tb * YP + (c0 + ch) * 16;
    const unsigned vFA = ((lane & 3) * 16 + (c0 >> 6)) * 128, vFB = ((4 + lane % 12) * 16 + (c0 >> 6)) * 128, vFlag = tid < 32 ? tid * 4 : 0x80000000u;
    const unsigned vTW = lane < 42 ? (iw * 168 + wave * 42 + lane) * 128 : 0x80000000u;
    const unsigned vTX = lane < 44 ? ((kind == 4 ? j4 : j3 - 12) * 176 + wave * 44 + lane) * 128 : 0x80000000u;
    (void)sDbg;
    asm volatile(
R"(.macro M d,w,x
v_mfma_f32_16x16x32_bf16 v[\d:\d+3], \w, v[\x:\x+3], v[\d:\d+3]
.endm
.macro R d,a,o
ds_read_b128 v[\d:\d+3], v\a offset:\o
.endm
.macro T d,a,o
ds_read_b64_tr_b16 \d, v\a offset:\o
.endm
.macro D a,s,o
buffer_load_dwordx4 v\a, s[36:39], s\s offen offset:\o sc1 lds
.endm
.macro L c
s_waitcnt lgkmcnt(\c)
.endm
Lcode_cv_b:
s_mov_b64 s[36:37],%0
s_mov_b32 s38,111243392
s_mov_b32 s39,0x00020000
s_mov_b32 s44,%1
s_mov_b32 s51,%2
s_mov_b32 s49,%3
s_mov_b32 s48,%4
s_mov_b32 s86,%5
s_mov_b32 s87,%6
s_mov_b32 s53,%7
v_mov_b32 v0,%8
v_mov_b32 v1,%9
v_mov_b32 v2,%10
v_mov_b32 v3,%11
v_mov_b32 v4,%12
v_mov_b32 v5,%13
v_mov_b32 v6,%14
v_mov_b32 v7,%15
v_mov_b32 v8,%16
v_mov_b32 v9,%17
v_mov_b32 v10,%18
v_mov_b32 v11,%19
v_mov_b32 v12,%20
s_mov_b32 s58,111144960
buffer_load_dword v13,v8,s[36:39],s58 offen offset:0 sc1
s_mul_i32 s57,s44,25856
v_add_u32 v18,s57,v0
v_add_u32 v18,32,v18
v_mov_b32 v19,s57
R 88,18,0
ds_read_b32 v92,v18 offset:16
R 152,18,12928
ds_read_b32 v156,v18 offset:12944
ds_read_b32 v24,v19 offset:32
ds_read_b32 v25,v19 offset:12960
s_mov_b64 s[40:41],%21
s_mov_b32 s42,7340032
s_mov_b32 s43,0x00020000
s_mov_b64 s[60:61],%22
s_mov_b32 s62,7340032
s_mov_b32 s63,0x00020000
v_mov_b32 v23,s53
s_mov_b32 s82,0
s_add_u32 s55,s49,4096
s_cmp_eq_u32 s51,2
s_cselect_b32 s55,s55,0x7fff0000
s_cselect_b32 s56,0,0x70000000
s_add_u32 s57,s57,8320
v_add_u32 v14,1024,v1
v_add_u32 v15,2048,v1
v_add_u32 v16,3072,v1
v_add_u32 v17,12928,v1
L 4
v_alignbyte_b32 v96,v89,v88,2
v_alignbyte_b32 v97,v90,v89,2
v_alignbyte_b32 v98,v91,v90,2
v_alignbyte_b32 v99,v92,v91,2
ds_write_b128 v18,v[96:99] offset:4160
L 3
v_alignbyte_b32 v160,v153,v152,2
v_alignbyte_b32 v161,v154,v153,2
v_alignbyte_b32 v162,v155,v154,2
v_alignbyte_b32 v163,v156,v155,2
ds_write_b128 v18,v[160:163] offset:17088
L 2
v_lshlrev_b32 v24,16,v24
v_lshlrev_b32 v25,16,v25
s_mov_b64 exec,1
ds_write_b32 v19,v24 offset:4188
ds_write_b32 v19,v25 offset:17116
s_mov_b64 exec,-1
s_lshl_b32 s78,s44,11
s_add_u32 s78,s78,103424
v_add_u32 v216,s78,v4
v_add_u32 v4,v19,v4
Lcv_bpa_%=:
s_waitcnt vmcnt(0)
v_cmp_eq_u32 vcc,s53,v13
s_add_u32 s82,s82,1
s_cmp_eq_u64 vcc,-1
s_cbranch_scc1 Lcv_bqa_%=
s_sleep 1
buffer_load_dword v13,v8,s[36:39],s58 offen offset:0 sc1
s_cmp_lt_u32 s82,20000
s_cbranch_scc1 Lcv_bpa_%=
s_waitcnt vmcnt(0)
Lcv_bqa_%=:
s_add_u32 m0,s57,0
s_nop 0
D 0,49,3072
s_add_u32 m0,s57,12928
s_nop 0
D 0,55,3072
ds_read2_b32 a[0:1],v1 offset0:0 offset1:1
ds_read2_b32 a[2:3],v1 offset0:2 offset1:3
ds_read2_b32 a[4:5],v1 offset0:128 offset1:129
ds_read2_b32 a[6:7],v1 offset0:130 offset1:131
ds_read2_b32 a[8:9],v1 offset0:16 offset1:17
ds_read2_b32 a[10:11],v1 offset0:18 offset1:19
ds_read2_b32 a[12:13],v1 offset0:144 offset1:145
ds_read2_b32 a[14:15],v1 offset0:146 offset1:147
ds_read2_b32 a[16:17],v1 offset0:32 offset1:33
ds_read2_b32 a[18:19],v1 offset0:34 offset1:35
ds_read2_b32 a[20:21],v1 offset0:160 offset1:161
ds_read2_b32 a[22:23],v1 offset0:162 offset1:163
ds_read2_b32 a[24:25],v1 offset0:48 offset1:49
ds_read2_b32 a[26:27],v1 offset0:50 offset1:51
ds_read2_b32 a[28:29],v1 offset0:176 offset1:177
ds_read2_b32 a[30:31],v1 offset0:178 offset1:179
ds_read2_b32 a[32:33],v1 offset0:64 offset1:65
ds_read2_b32 a[34:35],v1 offset0:66 offset1:67
ds_read2_b32 a[36:37],v1 offset0:192 offset1:193
ds_read2_b32 a[38:39],v1 offset0:194 offset1:195
ds_read2_b32 a[40:41],v1 offset0:80 offset1:81
ds_read2_b32 a[42:43],v1 offset0:82 offset1:83
ds_read2_b32 a[44:45],v1 offset0:208 offset1:209
ds_read2_b32 a[46:47],v1 offset0:210 offset1:211
ds_read2_b32 a[48:49],v1 offset0:96 offset1:97
ds_read2_b32 a[50:51],v1 offset0:98 offset1:99
ds_read2_b32 a[52:53],v1 offset0:224 offset1:225
ds_read2_b32 a[54:55],v1 offset0:226 offset1:227
ds_read2_b32 a[56:57],v1 offset0:112 offset1:113
ds_read2_b32 a[58:59],v1 offset0:114 offset1:115
ds_read2_b32 a[60:61],v1 offset0:240 offset1:241
ds_read2_b32 a[62:63],v1 offset0:242 offset1:243
s_cmp_eq_u32 s51,2
s_cbranch_scc1 Lcv_a2_%=
s_waitcnt vmcnt(0)
buffer_load_dword v13,v9,s[36:39],s58 offen offset:0 sc1
L 0
R 88,2,3584
R 92,2,3072
R 96,2,3648
R 100,2,3136
R 104,2,3712
R 108,2,3200
R 112,2,3776
R 116,2,3264
L 6
v_mfma_f32_16x16x32_bf16 v[40:43],a[0:3],v[88:91],0
R 120,2,3840
R 124,2,3328
v_mfma_f32_16x16x32_bf16 v[44:47],a[0:3],v[92:95],0
M 44,a[4:7],88
L 6
M 40,a[8:11],96
R 128,2,3904
R 132,2,3392
M 44,a[8:11],100
M 44,a[12:15],96
L 6
M 40,a[16:19],104
R 136,2,3968
R 140,2,3456
M 44,a[16:19],108
M 44,a[20:23],104
L 6
M 40,a[24:27],112
R 144,2,4032
R 148,2,3520
M 44,a[24:27],116
M 44,a[28:31],112
L 6
M 40,a[32:35],120
M 44,a[32:35],124
M 44,a[36:39],120
L 4
M 40,a[40:43],128
M 44,a[40:43],132
M 44,a[44:47],128
L 2
M 40,a[48:51],136
ds_read_b64 v[24:25],v3 offset:3584
ds_read_b64 v[26:27],v3 offset:3072
M 44,a[48:51],140
M 44,a[52:55],136
L 2
M 40,a[56:59],144
M 44,a[56:59],148
M 44,a[60:63],144
L 0
v_and_b32 v36,0xffff0000,v25
v_lshlrev_b32 v37,16,v25
v_and_b32 v38,0xffff0000,v24
v_lshlrev_b32 v39,16,v24
s_nop 0
v_fma_f32 v40,v36,s86,v40
v_fma_f32 v41,v37,s86,v41
v_fma_f32 v42,v38,s86,v42
v_fma_f32 v43,v39,s86,v43
v_cvt_pk_bf16_f32 v40,v40,v41
v_cvt_pk_bf16_f32 v41,v42,v43
ds_write_b64 v216,v[40:41] offset:0
v_and_b32 v36,0xffff0000,v27
v_lshlrev_b32 v37,16,v27
v_and_b32 v38,0xffff0000,v26
v_lshlrev_b32 v39,16,v26
v_fma_f32 v44,v36,s86,v44
v_fma_f32 v45,v37,s86,v45
v_fma_f32 v46,v38,s86,v46
v_fma_f32 v47,v39,s86,v47
v_cvt_pk_bf16_f32 v44,v44,v45
v_cvt_pk_bf16_f32 v45,v46,v47
ds_write_b64 v216,v[44:45] offset:512
s_branch Lcv_a3_%=
Lcv_a2_%=:
ds_read2_b32 a[64:65],v17 offset0:0 offset1:1
ds_read2_b32 a[66:67],v17 offset0:2 offset1:3
ds_read2_b32 a[68:69],v17 offset0:128 offset1:129
ds_read2_b32 a[70:71],v17 offset0:130 offset1:131
ds_read2_b32 a[72:73],v17 offset0:16 offset1:17
ds_read2_b32 a[74:75],v17 offset0:18 offset1:19
ds_read2_b32 a[76:77],v17 offset0:144 offset1:145
ds_read2_b32 a[78:79],v17 offset0:146 offset1:147
ds_read2_b32 a[80:81],v17 offset0:32 offset1:33
ds_read2_b32 a[82:83],v17 offset0:34 offset1:35
ds_read2_b32 a[84:85],v17 offset0:160 offset1:161
ds_read2_b32 a[86:87],v17 offset0:162 offset1:163
ds_read2_b32 a[88:89],v17 offset0:48 offset1:49
ds_read2_b32 a[90:91],v17 offset0:50 offset1:51
ds_read2_b32 a[92:93],v17 offset0:176 offset1:177
ds_read2_b32 a[94:95],v17 offset0:178 offset1:179
ds_read2_b32 a[96:97],v17 offset0:64 offset1:65
ds_read2_b32 a[98:99],v17 offset0:66 offset1:67
ds_read2_b32 a[100:101],v17 offset0:192 offset1:193
ds_read2_b32 a[102:103],v17 offset0:194 offset1:195
ds_read2_b32 a[104:105],v17 offset0:80 offset1:81
ds_read2_b32 a[106:107],v17 offset0:82 offset1:83
ds_read2_b32 a[108:109],v17 offset0:208 offset1:209
ds_read2_b32 a[110:111],v17 offset0:210 offset1:211
ds_read2_b32 a[112:113],v17 offset0:96 offset1:97
ds_read2_b32 a[114:115],v17 offset0:98 offset1:99
ds_read2_b32 a[116:117],v17 offset0:224 offset1:225
ds_read2_b32 a[118:119],v17 offset0:226 offset1:227
ds_read2_b32 a[120:121],v17 offset0:112 offset1:113
ds_read2_b32 a[122:123],v17 offset0:114 offset1:115
ds_read2_b32 a[124:125],v17 offset0:240 offset1:241
ds_read2_b32 a[126:127],v17 offset0:242 offset1:243
s_waitcnt vmcnt(0)
buffer_load_dword v13,v9,s[36:39],s58 offen offset:0 sc1
L 0
R 88,2,3584
R 92,2,3072
R 96,2,3648
R 100,2,3136
R 104,2,3712
R 108,2,3200
R 112,2,3776
R 116,2,3264
L 6
v_mfma_f32_16x16x32_bf16 v[40:43],a[0:3],v[88:91],0
R 120,2,3840
R 124,2,3328
v_mfma_f32_16x16x32_bf16 v[44:47],a[0:3],v[92:95],0
M 44,a[4:7],88
L 6
M 40,a[8:11],96
R 128,2,3904
R 132,2,3392
M 44,a[8:11],100
M 44,a[12:15],96
L 6
M 40,a[16:19],104
R 136,2,3968
R 140,2,3456
M 44,a[16:19],108
M 44,a[20:23],104
L 6
M 40,a[24:27],112
R 144,2,4032
R 148,2,3520
M 44,a[24:27],116
M 44,a[28:31],112
L 6
M 40,a[32:35],120
R 152,2,16512
R 156,2,16000
M 44,a[32:35],124
M 44,a[36:39],120
L 6
M 40,a[40:43],128
R 160,2,16576
R 164,2,16064
M 44,a[40:43],132
M 44,a[44:47],128
L 6
M 40,a[48:51],136
R 168,2,16640
R 172,2,16128
M 44,a[48:51],140
ds_read_b64 v[24:25],v3 offset:3584
ds_read_b64 v[26:27],v3 offset:3072
M 44,a[52:55],136
L 8
M 40,a[56:59],144
R 176,2,16704
R 180,2,16192
M 44,a[56:59],148
M 44,a[60:63],144
L 8
v_mfma_f32_16x16x32_bf16 v[48:51],a[64:67],v[152:155],0
R 184,2,16768
R 188,2,16256
v_mfma_f32_16x16x32_bf16 v[52:55],a[64:67],v[156:159],0
M 52,a[68:71],152
L 8
M 48,a[72:75],160
R 192,2,16832
R 196,2,16320
M 52,a[72:75],164
L 6
v_and_b32 v36,0xffff0000,v25
M 52,a[76:79],160
v_lshlrev_b32 v37,16,v25
v_and_b32 v38,0xffff0000,v24
v_lshlrev_b32 v39,16,v24
s_nop 0
M 48,a[80:83],168
R 200,2,16896
R 204,2,16384
M 52,a[80:83],172
v_fma_f32 v40,v36,s86,v40
v_fma_f32 v41,v37,s86,v41
M 52,a[84:87],168
v_fma_f32 v42,v38,s86,v42
v_fma_f32 v43,v39,s86,v43
v_cvt_pk_bf16_f32 v40,v40,v41
L 6
M 48,a[88:91],176
R 208,2,16960
R 212,2,16448
M 52,a[88:91],180
v_cvt_pk_bf16_f32 v41,v42,v43
ds_write_b64 v216,v[40:41] offset:0
M 52,a[92:95],176
v_and_b32 v36,0xffff0000,v27
v_lshlrev_b32 v37,16,v27
v_and_b32 v38,0xffff0000,v26
L 7
M 48,a[96:99],184
M 52,a[96:99],188
v_lshlrev_b32 v39,16,v26
v_fma_f32 v44,v36,s86,v44
M 52,a[100:103],184
v_fma_f32 v45,v37,s86,v45
v_fma_f32 v46,v38,s86,v46
v_fma_f32 v47,v39,s86,v47
L 5
M 48,a[104:107],192
M 52,a[104:107],196
v_cvt_pk_bf16_f32 v44,v44,v45
v_cvt_pk_bf16_f32 v45,v46,v47
M 52,a[108:111],192
ds_write_b64 v216,v[44:45] offset:512
L 4
M 48,a[112:115],200
ds_read_b64 v[24:25],v3 offset:16512
ds_read_b64 v[26:27],v3 offset:16000
M 52,a[112:115],204
M 52,a[116:119],200
L 4
M 48,a[120:123],208
M 52,a[120:123],212
M 52,a[124:127],208
L 0
v_and_b32 v36,0xffff0000,v25
v_lshlrev_b32 v37,16,v25
v_and_b32 v38,0xffff0000,v24
v_lshlrev_b32 v39,16,v24
s_nop 0
v_fma_f32 v48,v36,s87,v48
v_fma_f32 v49,v37,s87,v49
v_fma_f32 v50,v38,s87,v50
v_fma_f32 v51,v39,s87,v51
v_cvt_pk_bf16_f32 v48,v48,v49
v_cvt_pk_bf16_f32 v49,v50,v51
ds_write_b64 v216,v[48:49] offset:1024
v_and_b32 v36,0xffff0000,v27
v_lshlrev_b32 v37,16,v27
v_and_b32 v38,0xffff0000,v26
v_lshlrev_b32 v39,16,v26
v_fma_f32 v52,v36,s87,v52
v_fma_f32 v53,v37,s87,v53
v_fma_f32 v54,v38,s87,v54
v_fma_f32 v55,v39,s87,v55
v_cvt_pk_bf16_f32 v52,v52,v53
v_cvt_pk_bf16_f32 v53,v54,v55
ds_write_b64 v216,v[52:53] offset:1536
Lcv_a3_%=:
L 0
s_barrier
R 152,6,0
R 156,6,512
s_mov_b32 s78,98566144
L 1
buffer_store_dwordx4 v[152:155],v7,s[36:39],s78 offen offset:0 sc0 sc1
s_add_u32 s78,s56,99090432
L 0
buffer_store_dwordx4 v[156:159],v7,s[36:39],s78 offen offset:0 sc0 sc1
s_waitcnt vmcnt(2)
v_cmp_eq_u32 vcc,s53,v13
s_nop 0
s_cmp_eq_u64 vcc,-1
s_cbranch_scc0 Lcv_a_%=
s_add_u32 m0,s57,0
s_nop 0
D 0,49,2048
D 0,49,1024
D 0,49,0
s_add_u32 m0,s57,12928
s_nop 0
D 0,55,2048
D 0,55,1024
D 0,55,0
R 104,18,1024
ds_read_b32 v108,v18 offset:1040
R 120,18,2048
ds_read_b32 v124,v18 offset:2064
R 136,18,3072
ds_read_b32 v140,v18 offset:3088
L 4
v_alignbyte_b32 v112,v105,v104,2
v_alignbyte_b32 v113,v106,v105,2
v_alignbyte_b32 v114,v107,v106,2
v_alignbyte_b32 v115,v108,v107,2
ds_write_b128 v18,v[112:115] offset:5184
L 3
v_alignbyte_b32 v128,v121,v120,2
v_alignbyte_b32 v129,v122,v121,2
v_alignbyte_b32 v130,v123,v122,2
v_alignbyte_b32 v131,v124,v123,2
ds_write_b128 v18,v[128:131] offset:6208
L 2
v_alignbyte_b32 v144,v137,v136,2
v_alignbyte_b32 v145,v138,v137,2
v_alignbyte_b32 v146,v139,v138,2
v_alignbyte_b32 v147,v140,v139,2
ds_write_b128 v18,v[144:147] offset:7232
s_waitcnt vmcnt(6)
s_barrier
buffer_store_dword v23,v10,s[36:39],s48 offen offset:0 sc0 sc1
s_branch Lcv_b_%=
Lcv_a_%=:
R 104,18,1024
ds_read_b32 v108,v18 offset:1040
R 120,18,2048
ds_read_b32 v124,v18 offset:2064
R 136,18,3072
ds_read_b32 v140,v18 offset:3088
L 4
v_alignbyte_b32 v112,v105,v104,2
v_alignbyte_b32 v113,v106,v105,2
v_alignbyte_b32 v114,v107,v106,2
v_alignbyte_b32 v115,v108,v107,2
ds_write_b128 v18,v[112:115] offset:5184
L 3
v_alignbyte_b32 v128,v121,v120,2
v_alignbyte_b32 v129,v122,v121,2
v_alignbyte_b32 v130,v123,v122,2
v_alignbyte_b32 v131,v124,v123,2
ds_write_b128 v18,v[128:131] offset:6208
L 2
v_alignbyte_b32 v144,v137,v136,2
v_alignbyte_b32 v145,v138,v137,2
v_alignbyte_b32 v146,v139,v138,2
v_alignbyte_b32 v147,v140,v139,2
ds_write_b128 v18,v[144:147] offset:7232
s_waitcnt vmcnt(0)
s_barrier
buffer_store_dword v23,v10,s[36:39],s48 offen offset:0 sc0 sc1
buffer_load_dword v13,v9,s[36:39],s58 offen offset:0 sc1
Lcv_bpb_%=:
s_waitcnt vmcnt(0)
v_cmp_eq_u32 vcc,s53,v13
s_add_u32 s82,s82,1
s_cmp_eq_u64 vcc,-1
s_cbranch_scc1 Lcv_bqb_%=
s_sleep 1
buffer_load_dword v13,v9,s[36:39],s58 offen offset:0 sc1
s_cmp_lt_u32 s82,20000
s_cbranch_scc1 Lcv_bpb_%=
s_waitcnt vmcnt(0)
Lcv_bqb_%=:
s_add_u32 m0,s57,0
)"
R"(s_nop 0
D 0,49,2048
D 0,49,1024
D 0,49,0
s_add_u32 m0,s57,12928
s_nop 0
D 0,55,2048
D 0,55,1024
D 0,55,0
Lcv_b_%=:
s_waitcnt vmcnt(0)
ds_read2_b32 v[88:89],v1 offset0:0 offset1:1
ds_read2_b32 v[90:91],v1 offset0:2 offset1:3
R 120,2,3584
ds_read2_b32 v[92:93],v1 offset0:128 offset1:129
ds_read2_b32 v[94:95],v1 offset0:130 offset1:131
R 124,2,3072
ds_read2_b32 v[96:97],v14 offset0:0 offset1:1
ds_read2_b32 v[98:99],v14 offset0:2 offset1:3
R 128,2,2560
ds_read2_b32 v[100:101],v14 offset0:128 offset1:129
ds_read2_b32 v[102:103],v14 offset0:130 offset1:131
R 132,2,2048
ds_read2_b32 v[104:105],v15 offset0:0 offset1:1
ds_read2_b32 v[106:107],v15 offset0:2 offset1:3
R 136,2,1536
ds_read2_b32 v[108:109],v15 offset0:128 offset1:129
ds_read2_b32 v[110:111],v15 offset0:130 offset1:131
R 140,2,1024
ds_read2_b32 v[112:113],v16 offset0:0 offset1:1
ds_read2_b32 v[114:115],v16 offset0:2 offset1:3
R 144,2,512
ds_read2_b32 v[116:117],v16 offset0:128 offset1:129
ds_read2_b32 v[118:119],v16 offset0:130 offset1:131
R 148,2,0
L 15
v_mfma_f32_16x16x32_bf16 v[40:43],v[88:91],v[128:131],0
ds_read2_b32 v[152:153],v1 offset0:16 offset1:17
M 40,v[92:95],124
ds_read2_b32 v[154:155],v1 offset0:18 offset1:19
M 40,v[96:99],120
L 14
R 184,2,3648
v_mfma_f32_16x16x32_bf16 v[44:47],v[88:91],v[132:135],0
ds_read2_b32 v[156:157],v1 offset0:144 offset1:145
M 44,v[92:95],128
M 44,v[96:99],124
ds_read2_b32 v[158:159],v1 offset0:146 offset1:147
M 44,v[100:103],120
L 14
R 188,2,3136
v_mfma_f32_16x16x32_bf16 v[48:51],v[88:91],v[136:139],0
ds_read2_b32 v[160:161],v14 offset0:16 offset1:17
M 48,v[92:95],132
ds_read2_b32 v[162:163],v14 offset0:18 offset1:19
M 48,v[96:99],128
M 48,v[100:103],124
R 192,2,2624
M 48,v[104:107],120
L 15
ds_read2_b32 v[164:165],v14 offset0:144 offset1:145
v_mfma_f32_16x16x32_bf16 v[52:55],v[88:91],v[140:143],0
ds_read2_b32 v[166:167],v14 offset0:146 offset1:147
M 52,v[92:95],136
R 196,2,2112
M 52,v[96:99],132
M 52,v[100:103],128
ds_read2_b32 v[168:169],v15 offset0:16 offset1:17
M 52,v[104:107],124
ds_read2_b32 v[170:171],v15 offset0:18 offset1:19
M 52,v[108:111],120
L 15
R 200,2,1600
v_mfma_f32_16x16x32_bf16 v[56:59],v[88:91],v[144:147],0
ds_read2_b32 v[172:173],v15 offset0:144 offset1:145
M 56,v[92:95],140
M 56,v[96:99],136
ds_read2_b32 v[174:175],v15 offset0:146 offset1:147
M 56,v[100:103],132
R 204,2,1088
M 56,v[104:107],128
ds_read2_b32 v[176:177],v16 offset0:16 offset1:17
M 56,v[108:111],124
ds_read2_b32 v[178:179],v16 offset0:18 offset1:19
M 56,v[112:115],120
L 15
v_mfma_f32_16x16x32_bf16 v[60:63],v[88:91],v[148:151],0
R 208,2,576
M 60,v[92:95],144
ds_read2_b32 v[180:181],v16 offset0:144 offset1:145
M 60,v[96:99],140
ds_read2_b32 v[182:183],v16 offset0:146 offset1:147
M 60,v[100:103],136
R 212,2,64
M 60,v[104:107],132
M 60,v[108:111],128
M 60,v[112:115],124
M 60,v[116:119],120
L 15
M 40,v[152:155],192
ds_read2_b32 v[88:89],v1 offset0:32 offset1:33
M 40,v[156:159],188
ds_read2_b32 v[90:91],v1 offset0:34 offset1:35
M 40,v[160:163],184
L 14
R 120,2,3712
M 44,v[152:155],196
ds_read2_b32 v[92:93],v1 offset0:160 offset1:161
M 44,v[156:159],192
M 44,v[160:163],188
ds_read2_b32 v[94:95],v1 offset0:162 offset1:163
M 44,v[164:167],184
L 14
R 124,2,3200
M 48,v[152:155],200
ds_read2_b32 v[96:97],v14 offset0:32 offset1:33
M 48,v[156:159],196
ds_read2_b32 v[98:99],v14 offset0:34 offset1:35
M 48,v[160:163],192
M 48,v[164:167],188
R 128,2,2688
M 48,v[168:171],184
L 15
ds_read2_b32 v[100:101],v14 offset0:160 offset1:161
M 52,v[152:155],204
ds_read2_b32 v[102:103],v14 offset0:162 offset1:163
M 52,v[156:159],200
R 132,2,2176
M 52,v[160:163],196
M 52,v[164:167],192
ds_read2_b32 v[104:105],v15 offset0:32 offset1:33
M 52,v[168:171],188
ds_read2_b32 v[106:107],v15 offset0:34 offset1:35
M 52,v[172:175],184
L 15
R 136,2,1664
M 56,v[152:155],208
ds_read2_b32 v[108:109],v15 offset0:160 offset1:161
M 56,v[156:159],204
M 56,v[160:163],200
ds_read2_b32 v[110:111],v15 offset0:162 offset1:163
M 56,v[164:167],196
R 140,2,1152
M 56,v[168:171],192
ds_read2_b32 v[112:113],v16 offset0:32 offset1:33
M 56,v[172:175],188
ds_read2_b32 v[114:115],v16 offset0:34 offset1:35
M 56,v[176:179],184
L 15
M 60,v[152:155],212
R 144,2,640
M 60,v[156:159],208
ds_read2_b32 v[116:117],v16 offset0:160 offset1:161
M 60,v[160:163],204
ds_read2_b32 v[118:119],v16 offset0:162 offset1:163
R 218,18,13952
M 60,v[164:167],200
R 148,2,128
ds_read_b32 v222,v18 offset:13968
M 60,v[168:171],196
M 60,v[172:175],192
M 60,v[176:179],188
M 60,v[180:183],184
L 15
M 40,v[88:91],128
ds_read2_b32 v[152:153],v1 offset0:48 offset1:49
M 40,v[92:95],124
L 1
ds_read2_b32 v[154:155],v1 offset0:50 offset1:51
v_alignbyte_b32 v226,v219,v218,2
M 40,v[96:99],120
s_nop 0
R 184,2,3776
v_alignbyte_b32 v227,v220,v219,2
M 44,v[88:91],132
ds_read2_b32 v[156:157],v1 offset0:176 offset1:177
v_alignbyte_b32 v228,v221,v220,2
M 44,v[92:95],128
v_alignbyte_b32 v229,v222,v221,2
M 44,v[96:99],124
ds_read2_b32 v[158:159],v1 offset0:178 offset1:179
M 44,v[100:103],120
s_nop 0
R 188,2,3264
M 48,v[88:91],136
ds_read2_b32 v[160:161],v14 offset0:48 offset1:49
M 48,v[92:95],132
ds_read2_b32 v[162:163],v14 offset0:50 offset1:51
M 48,v[96:99],128
ds_write_b128 v18,v[226:229] offset:18112
M 48,v[100:103],124
R 192,2,2752
M 48,v[104:107],120
s_nop 0
ds_read2_b32 v[164:165],v14 offset0:176 offset1:177
M 52,v[88:91],140
s_mov_b32 m0,163584
s_mov_b32 s78,0
ds_read2_b32 v[166:167],v14 offset0:178 offset1:179
M 52,v[92:95],136
R 196,2,2240
M 52,v[96:99],132
buffer_load_dword v11,s[40:43],s78 offen offset:0  lds
M 52,v[100:103],128
ds_read2_b32 v[168:169],v15 offset0:48 offset1:49
M 52,v[104:107],124
ds_read2_b32 v[170:171],v15 offset0:50 offset1:51
M 52,v[108:111],120
s_nop 0
R 200,2,1728
M 56,v[88:91],144
ds_read2_b32 v[172:173],v15 offset0:176 offset1:177
M 56,v[92:95],140
M 56,v[96:99],136
ds_read2_b32 v[174:175],v15 offset0:178 offset1:179
M 56,v[100:103],132
R 204,2,1216
M 56,v[104:107],128
ds_read2_b32 v[176:177],v16 offset0:48 offset1:49
M 56,v[108:111],124
ds_read2_b32 v[178:179],v16 offset0:50 offset1:51
M 56,v[112:115],120
s_nop 0
M 60,v[88:91],148
R 208,2,704
M 60,v[92:95],144
ds_read2_b32 v[180:181],v16 offset0:176 offset1:177
M 60,v[96:99],140
ds_read2_b32 v[182:183],v16 offset0:178 offset1:179
M 60,v[100:103],136
R 212,2,192
M 60,v[104:107],132
M 60,v[108:111],128
M 60,v[112:115],124
M 60,v[116:119],120
L 15
M 40,v[152:155],192
ds_read2_b32 v[88:89],v1 offset0:64 offset1:65
M 40,v[156:159],188
ds_read2_b32 v[90:91],v1 offset0:66 offset1:67
R 218,18,14976
M 40,v[160:163],184
L 15
R 120,2,3840
ds_read_b32 v222,v18 offset:14992
M 44,v[152:155],196
ds_read2_b32 v[92:93],v1 offset0:192 offset1:193
M 44,v[156:159],192
M 44,v[160:163],188
ds_read2_b32 v[94:95],v1 offset0:194 offset1:195
M 44,v[164:167],184
L 15
R 124,2,3328
M 48,v[152:155],200
ds_read2_b32 v[96:97],v14 offset0:64 offset1:65
M 48,v[156:159],196
L 4
ds_read2_b32 v[98:99],v14 offset0:66 offset1:67
v_alignbyte_b32 v226,v219,v218,2
M 48,v[160:163],192
v_alignbyte_b32 v227,v220,v219,2
v_alignbyte_b32 v228,v221,v220,2
M 48,v[164:167],188
R 128,2,2816
v_alignbyte_b32 v229,v222,v221,2
M 48,v[168:171],184
s_nop 0
ds_read2_b32 v[100:101],v14 offset0:192 offset1:193
M 52,v[152:155],204
ds_read2_b32 v[102:103],v14 offset0:194 offset1:195
M 52,v[156:159],200
R 132,2,2304
M 52,v[160:163],196
ds_write_b128 v18,v[226:229] offset:19136
M 52,v[164:167],192
ds_read2_b32 v[104:105],v15 offset0:64 offset1:65
M 52,v[168:171],188
ds_read2_b32 v[106:107],v15 offset0:66 offset1:67
M 52,v[172:175],184
s_nop 0
R 136,2,1792
M 56,v[152:155],208
ds_read2_b32 v[108:109],v15 offset0:192 offset1:193
M 56,v[156:159],204
M 56,v[160:163],200
ds_read2_b32 v[110:111],v15 offset0:194 offset1:195
M 56,v[164:167],196
R 140,2,1280
M 56,v[168:171],192
ds_read2_b32 v[112:113],v16 offset0:64 offset1:65
M 56,v[172:175],188
ds_read2_b32 v[114:115],v16 offset0:66 offset1:67
M 56,v[176:179],184
s_nop 0
M 60,v[152:155],212
R 144,2,768
M 60,v[156:159],208
ds_read2_b32 v[116:117],v16 offset0:192 offset1:193
M 60,v[160:163],204
ds_read2_b32 v[118:119],v16 offset0:194 offset1:195
M 60,v[164:167],200
R 148,2,256
M 60,v[168:171],196
M 60,v[172:175],192
M 60,v[176:179],188
M 60,v[180:183],184
L 15
M 40,v[88:91],128
ds_read2_b32 v[152:153],v1 offset0:80 offset1:81
M 40,v[92:95],124
ds_read2_b32 v[154:155],v1 offset0:82 offset1:83
M 40,v[96:99],120
L 15
R 184,2,3904
M 44,v[88:91],132
ds_read2_b32 v[156:157],v1 offset0:208 offset1:209
M 44,v[92:95],128
M 44,v[96:99],124
ds_read2_b32 v[158:159],v1 offset0:210 offset1:211
M 44,v[100:103],120
L 14
R 188,2,3392
M 48,v[88:91],136
ds_read2_b32 v[160:161],v14 offset0:80 offset1:81
M 48,v[92:95],132
ds_read2_b32 v[162:163],v14 offset0:82 offset1:83
R 218,18,16000
M 48,v[96:99],128
ds_read_b32 v222,v18 offset:16016
M 48,v[100:103],124
R 192,2,2880
M 48,v[104:107],120
L 15
ds_read2_b32 v[164:165],v14 offset0:208 offset1:209
M 52,v[88:91],140
ds_read2_b32 v[166:167],v14 offset0:210 offset1:211
M 52,v[92:95],136
R 196,2,2368
M 52,v[96:99],132
M 52,v[100:103],128
L 4
ds_read2_b32 v[168:169],v15 offset0:80 offset1:81
v_alignbyte_b32 v226,v219,v218,2
M 52,v[104:107],124
ds_read2_b32 v[170:171],v15 offset0:82 offset1:83
v_alignbyte_b32 v227,v220,v219,2
M 52,v[108:111],120
s_nop 0
R 200,2,1856
v_alignbyte_b32 v228,v221,v220,2
M 56,v[88:91],144
ds_read2_b32 v[172:173],v15 offset0:208 offset1:209
v_alignbyte_b32 v229,v222,v221,2
M 56,v[92:95],140
ds_write_b128 v18,v[226:229] offset:20160
M 56,v[96:99],136
ds_read2_b32 v[174:175],v15 offset0:210 offset1:211
M 56,v[100:103],132
R 204,2,1344
M 56,v[104:107],128
ds_read2_b32 v[176:177],v16 offset0:80 offset1:81
M 56,v[108:111],124
ds_read2_b32 v[178:179],v16 offset0:82 offset1:83
M 56,v[112:115],120
s_nop 0
M 60,v[88:91],148
R 208,2,832
M 60,v[92:95],144
ds_read2_b32 v[180:181],v16 offset0:208 offset1:209
M 60,v[96:99],140
ds_read2_b32 v[182:183],v16 offset0:210 offset1:211
M 60,v[100:103],136
R 212,2,320
M 60,v[104:107],132
M 60,v[108:111],128
M 60,v[112:115],124
M 60,v[116:119],120
L 15
M 40,v[152:155],192
ds_read2_b32 v[88:89],v1 offset0:96 offset1:97
M 40,v[156:159],188
ds_read2_b32 v[90:91],v1 offset0:98 offset1:99
M 40,v[160:163],184
L 15
R 120,2,3968
M 44,v[152:155],196
s_mov_b32 m0,163584
s_mov_b32 s78,3741696
ds_read2_b32 v[92:93],v1 offset0:224 offset1:225
M 44,v[156:159],192
M 44,v[160:163],188
ds_read2_b32 v[94:95],v1 offset0:226 offset1:227
M 44,v[164:167],184
L 15
R 124,2,3456
M 48,v[152:155],200
ds_read2_b32 v[96:97],v14 offset0:96 offset1:97
M 48,v[156:159],196
ds_read2_b32 v[98:99],v14 offset0:98 offset1:99
M 48,v[160:163],192
buffer_load_dword v11,s[40:43],s78 offen offset:0  lds
M 48,v[164:167],188
R 128,2,2944
M 48,v[168:171],184
L 15
ds_read2_b32 v[100:101],v14 offset0:224 offset1:225
M 52,v[152:155],204
ds_read2_b32 v[102:103],v14 offset0:226 offset1:227
M 52,v[156:159],200
R 132,2,2432
M 52,v[160:163],196
M 52,v[164:167],192
ds_read2_b32 v[104:105],v15 offset0:96 offset1:97
M 52,v[168:171],188
ds_read2_b32 v[106:107],v15 offset0:98 offset1:99
M 52,v[172:175],184
L 15
R 136,2,1920
M 56,v[152:155],208
ds_read2_b32 v[108:109],v15 offset0:224 offset1:225
M 56,v[156:159],204
M 56,v[160:163],200
ds_read2_b32 v[110:111],v15 offset0:226 offset1:227
M 56,v[164:167],196
R 140,2,1408
M 56,v[168:171],192
ds_read2_b32 v[112:113],v16 offset0:96 offset1:97
M 56,v[172:175],188
ds_read2_b32 v[114:115],v16 offset0:98 offset1:99
M 56,v[176:179],184
L 15
M 60,v[152:155],212
R 144,2,896
M 60,v[156:159],208
ds_read2_b32 v[116:117],v16 offset0:224 offset1:225
M 60,v[160:163],204
ds_read2_b32 v[118:119],v16 offset0:226 offset1:227
M 60,v[164:167],200
R 148,2,384
M 60,v[168:171],196
M 60,v[172:175],192
M 60,v[176:179],188
M 60,v[180:183],184
L 15
M 40,v[88:91],128
ds_read2_b32 v[152:153],v1 offset0:112 offset1:113
M 40,v[92:95],124
ds_read2_b32 v[154:155],v1 offset0:114 offset1:115
M 40,v[96:99],120
L 14
R 184,2,4032
M 44,v[88:91],132
ds_read2_b32 v[156:157],v1 offset0:240 offset1:241
M 44,v[92:95],128
M 44,v[96:99],124
ds_read2_b32 v[158:159],v1 offset0:242 offset1:243
v_add_u32 v1,12928,v1
M 44,v[100:103],120
L 14
R 188,2,3520
M 48,v[88:91],136
ds_read2_b32 v[160:161],v14 offset0:112 offset1:113
M 48,v[92:95],132
ds_read2_b32 v[162:163],v14 offset0:114 offset1:115
M 48,v[96:99],128
M 48,v[100:103],124
R 192,2,3008
M 48,v[104:107],120
L 15
ds_read2_b32 v[164:165],v14 offset0:240 offset1:241
M 52,v[88:91],140
ds_read2_b32 v[166:167],v14 offset0:242 offset1:243
v_add_u32 v14,12928,v14
M 52,v[92:95],136
R 196,2,2496
M 52,v[96:99],132
M 52,v[100:103],128
ds_read2_b32 v[168:169],v15 offset0:112 offset1:113
M 52,v[104:107],124
ds_read2_b32 v[170:171],v15 offset0:114 offset1:115
M 52,v[108:111],120
L 15
R 200,2,1984
M 56,v[88:91],144
ds_read2_b32 v[172:173],v15 offset0:240 offset1:241
M 56,v[92:95],140
M 56,v[96:99],136
ds_read2_b32 v[174:175],v15 offset0:242 offset1:243
v_add_u32 v15,12928,v15
M 56,v[100:103],132
R 204,2,1472
M 56,v[104:107],128
ds_read2_b32 v[176:177],v16 offset0:112 offset1:113
M 56,v[108:111],124
ds_read2_b32 v[178:179],v16 offset0:114 offset1:115
M 56,v[112:115],120
L 15
M 60,v[88:91],148
R 208,2,960
M 60,v[92:95],144
)"
R"(ds_read2_b32 v[180:181],v16 offset0:240 offset1:241
M 60,v[96:99],140
ds_read2_b32 v[182:183],v16 offset0:242 offset1:243
v_add_u32 v16,12928,v16
M 60,v[100:103],136
R 212,2,448
M 60,v[104:107],132
M 60,v[108:111],128
M 60,v[112:115],124
M 60,v[116:119],120
L 15
M 40,v[152:155],192
ds_read2_b32 v[88:89],v1 offset0:0 offset1:1
M 40,v[156:159],188
ds_read2_b32 v[90:91],v1 offset0:2 offset1:3
M 40,v[160:163],184
L 14
R 120,2,16512
M 44,v[152:155],196
ds_read2_b32 v[92:93],v1 offset0:128 offset1:129
M 44,v[156:159],192
M 44,v[160:163],188
ds_read2_b32 v[94:95],v1 offset0:130 offset1:131
M 44,v[164:167],184
L 14
R 124,2,16000
M 48,v[152:155],200
ds_read2_b32 v[96:97],v14 offset0:0 offset1:1
M 48,v[156:159],196
ds_read2_b32 v[98:99],v14 offset0:2 offset1:3
M 48,v[160:163],192
M 48,v[164:167],188
R 128,2,15488
M 48,v[168:171],184
L 15
ds_read2_b32 v[100:101],v14 offset0:128 offset1:129
M 52,v[152:155],204
ds_read2_b32 v[102:103],v14 offset0:130 offset1:131
M 52,v[156:159],200
R 132,2,14976
M 52,v[160:163],196
M 52,v[164:167],192
ds_read2_b32 v[104:105],v15 offset0:0 offset1:1
M 52,v[168:171],188
ds_read2_b32 v[106:107],v15 offset0:2 offset1:3
M 52,v[172:175],184
L 15
R 136,2,14464
M 56,v[152:155],208
ds_read2_b32 v[108:109],v15 offset0:128 offset1:129
M 56,v[156:159],204
M 56,v[160:163],200
ds_read2_b32 v[110:111],v15 offset0:130 offset1:131
M 56,v[164:167],196
R 140,2,13952
M 56,v[168:171],192
ds_read2_b32 v[112:113],v16 offset0:0 offset1:1
M 56,v[172:175],188
ds_read2_b32 v[114:115],v16 offset0:2 offset1:3
M 56,v[176:179],184
L 15
M 60,v[152:155],212
R 144,2,13440
M 60,v[156:159],208
ds_read2_b32 v[116:117],v16 offset0:128 offset1:129
M 60,v[160:163],204
ds_read2_b32 v[118:119],v16 offset0:130 offset1:131
M 60,v[164:167],200
R 148,2,12928
M 60,v[168:171],196
M 60,v[172:175],192
M 60,v[176:179],188
M 60,v[180:183],184
s_cmp_eq_u32 s51,2
s_cbranch_scc1 Lcv_d_%=
ds_read_b64 v[24:25],v3 offset:2560
ds_read_b64 v[26:27],v3 offset:2048
ds_read_b64 v[28:29],v3 offset:1536
ds_read_b64 v[30:31],v3 offset:1024
ds_read_b64 v[32:33],v3 offset:512
ds_read_b64 v[34:35],v3 offset:0
L 0
v_and_b32 v36,0xffff0000,v25
v_lshlrev_b32 v37,16,v25
v_and_b32 v38,0xffff0000,v24
v_lshlrev_b32 v39,16,v24
v_fma_f32 v40,v36,s86,v40
v_fma_f32 v41,v37,s86,v41
v_fma_f32 v42,v38,s86,v42
v_fma_f32 v43,v39,s86,v43
v_cvt_pk_bf16_f32 v40,v40,v41
v_cvt_pk_bf16_f32 v41,v42,v43
ds_write_b64 v4,v[40:41] offset:0
v_and_b32 v36,0xffff0000,v27
v_lshlrev_b32 v37,16,v27
v_and_b32 v38,0xffff0000,v26
v_lshlrev_b32 v39,16,v26
v_fma_f32 v44,v36,s86,v44
v_fma_f32 v45,v37,s86,v45
v_fma_f32 v46,v38,s86,v46
v_fma_f32 v47,v39,s86,v47
v_cvt_pk_bf16_f32 v44,v44,v45
v_cvt_pk_bf16_f32 v45,v46,v47
ds_write_b64 v4,v[44:45] offset:512
v_and_b32 v36,0xffff0000,v29
v_lshlrev_b32 v37,16,v29
v_and_b32 v38,0xffff0000,v28
v_lshlrev_b32 v39,16,v28
v_fma_f32 v48,v36,s86,v48
v_fma_f32 v49,v37,s86,v49
v_fma_f32 v50,v38,s86,v50
v_fma_f32 v51,v39,s86,v51
v_cvt_pk_bf16_f32 v48,v48,v49
v_cvt_pk_bf16_f32 v49,v50,v51
ds_write_b64 v4,v[48:49] offset:1024
v_and_b32 v36,0xffff0000,v31
v_lshlrev_b32 v37,16,v31
v_and_b32 v38,0xffff0000,v30
v_lshlrev_b32 v39,16,v30
v_fma_f32 v52,v36,s86,v52
v_fma_f32 v53,v37,s86,v53
v_fma_f32 v54,v38,s86,v54
v_fma_f32 v55,v39,s86,v55
v_cvt_pk_bf16_f32 v52,v52,v53
v_cvt_pk_bf16_f32 v53,v54,v55
ds_write_b64 v4,v[52:53] offset:1536
v_and_b32 v36,0xffff0000,v33
v_lshlrev_b32 v37,16,v33
v_and_b32 v38,0xffff0000,v32
v_lshlrev_b32 v39,16,v32
v_fma_f32 v56,v36,s86,v56
v_fma_f32 v57,v37,s86,v57
v_fma_f32 v58,v38,s86,v58
v_fma_f32 v59,v39,s86,v59
v_cvt_pk_bf16_f32 v56,v56,v57
v_cvt_pk_bf16_f32 v57,v58,v59
ds_write_b64 v4,v[56:57] offset:2048
v_and_b32 v36,0xffff0000,v35
v_lshlrev_b32 v37,16,v35
v_and_b32 v38,0xffff0000,v34
v_lshlrev_b32 v39,16,v34
v_fma_f32 v60,v36,s86,v60
v_fma_f32 v61,v37,s86,v61
v_fma_f32 v62,v38,s86,v62
v_fma_f32 v63,v39,s86,v63
v_cvt_pk_bf16_f32 v60,v60,v61
v_cvt_pk_bf16_f32 v61,v62,v63
ds_write_b64 v4,v[60:61] offset:2560
s_branch Lcv_e_%=
Lcv_d_%=:
L 15
v_mfma_f32_16x16x32_bf16 v[64:67],v[88:91],v[128:131],0
ds_read2_b32 v[152:153],v1 offset0:16 offset1:17
ds_read_b64 v[24:25],v3 offset:2560
M 64,v[92:95],124
ds_read2_b32 v[154:155],v1 offset0:18 offset1:19
ds_read_b64 v[26:27],v3 offset:2048
M 64,v[96:99],120
L 15
R 184,2,16576
ds_read_b64 v[28:29],v3 offset:1536
v_mfma_f32_16x16x32_bf16 v[68:71],v[88:91],v[132:135],0
ds_read2_b32 v[156:157],v1 offset0:144 offset1:145
ds_read_b64 v[30:31],v3 offset:1024
M 68,v[92:95],128
ds_read_b64 v[32:33],v3 offset:512
ds_read_b64 v[34:35],v3 offset:0
M 68,v[96:99],124
ds_read2_b32 v[158:159],v1 offset0:146 offset1:147
M 68,v[100:103],120
L 15
R 188,2,16064
v_mfma_f32_16x16x32_bf16 v[72:75],v[88:91],v[136:139],0
ds_read2_b32 v[160:161],v14 offset0:16 offset1:17
M 72,v[92:95],132
s_mov_b32 m0,163584
s_mov_b32 s78,0
ds_read2_b32 v[162:163],v14 offset0:18 offset1:19
M 72,v[96:99],128
M 72,v[100:103],124
L 4
R 192,2,15552
v_and_b32 v36,0xffff0000,v25
M 72,v[104:107],120
s_nop 0
ds_read2_b32 v[164:165],v14 offset0:144 offset1:145
v_lshlrev_b32 v37,16,v25
v_mfma_f32_16x16x32_bf16 v[76:79],v[88:91],v[140:143],0
ds_read2_b32 v[166:167],v14 offset0:146 offset1:147
v_and_b32 v38,0xffff0000,v24
M 76,v[92:95],136
R 196,2,15040
v_lshlrev_b32 v39,16,v24
M 76,v[96:99],132
buffer_load_dword v12,s[60:63],s78 offen offset:0  lds
M 76,v[100:103],128
ds_read2_b32 v[168:169],v15 offset0:16 offset1:17
v_fma_f32 v40,v36,s86,v40
M 76,v[104:107],124
ds_read2_b32 v[170:171],v15 offset0:18 offset1:19
v_fma_f32 v41,v37,s86,v41
M 76,v[108:111],120
s_nop 0
R 200,2,14528
v_fma_f32 v42,v38,s86,v42
v_mfma_f32_16x16x32_bf16 v[80:83],v[88:91],v[144:147],0
ds_read2_b32 v[172:173],v15 offset0:144 offset1:145
v_fma_f32 v43,v39,s86,v43
M 80,v[92:95],140
v_cvt_pk_bf16_f32 v40,v40,v41
v_cvt_pk_bf16_f32 v41,v42,v43
M 80,v[96:99],136
ds_read2_b32 v[174:175],v15 offset0:146 offset1:147
v_and_b32 v36,0xffff0000,v27
M 80,v[100:103],132
R 204,2,14016
v_lshlrev_b32 v37,16,v27
M 80,v[104:107],128
ds_read2_b32 v[176:177],v16 offset0:16 offset1:17
v_and_b32 v38,0xffff0000,v26
M 80,v[108:111],124
ds_read2_b32 v[178:179],v16 offset0:18 offset1:19
v_lshlrev_b32 v39,16,v26
M 80,v[112:115],120
s_nop 0
ds_write_b64 v4,v[40:41] offset:0
v_mfma_f32_16x16x32_bf16 v[84:87],v[88:91],v[148:151],0
R 208,2,13504
v_fma_f32 v44,v36,s86,v44
M 84,v[92:95],144
ds_read2_b32 v[180:181],v16 offset0:144 offset1:145
v_fma_f32 v45,v37,s86,v45
M 84,v[96:99],140
ds_read2_b32 v[182:183],v16 offset0:146 offset1:147
v_fma_f32 v46,v38,s86,v46
M 84,v[100:103],136
R 212,2,12992
v_fma_f32 v47,v39,s86,v47
M 84,v[104:107],132
v_cvt_pk_bf16_f32 v44,v44,v45
v_cvt_pk_bf16_f32 v45,v46,v47
M 84,v[108:111],128
ds_write_b64 v4,v[44:45] offset:512
M 84,v[112:115],124
v_and_b32 v36,0xffff0000,v29
v_lshlrev_b32 v37,16,v29
M 84,v[116:119],120
L 15
v_and_b32 v38,0xffff0000,v28
v_lshlrev_b32 v39,16,v28
M 64,v[152:155],192
ds_read2_b32 v[88:89],v1 offset0:32 offset1:33
v_fma_f32 v48,v36,s86,v48
M 64,v[156:159],188
ds_read2_b32 v[90:91],v1 offset0:34 offset1:35
v_fma_f32 v49,v37,s86,v49
M 64,v[160:163],184
L 15
R 120,2,16640
v_fma_f32 v50,v38,s86,v50
M 68,v[152:155],196
ds_read2_b32 v[92:93],v1 offset0:160 offset1:161
v_fma_f32 v51,v39,s86,v51
M 68,v[156:159],192
v_cvt_pk_bf16_f32 v48,v48,v49
v_cvt_pk_bf16_f32 v49,v50,v51
M 68,v[160:163],188
ds_read2_b32 v[94:95],v1 offset0:162 offset1:163
v_and_b32 v36,0xffff0000,v31
M 68,v[164:167],184
L 15
R 124,2,16128
v_lshlrev_b32 v37,16,v31
M 72,v[152:155],200
ds_read2_b32 v[96:97],v14 offset0:32 offset1:33
v_and_b32 v38,0xffff0000,v30
M 72,v[156:159],196
ds_read2_b32 v[98:99],v14 offset0:34 offset1:35
v_lshlrev_b32 v39,16,v30
M 72,v[160:163],192
ds_write_b64 v4,v[48:49] offset:1024
M 72,v[164:167],188
R 128,2,15616
v_fma_f32 v52,v36,s86,v52
M 72,v[168:171],184
L 15
ds_read2_b32 v[100:101],v14 offset0:160 offset1:161
v_fma_f32 v53,v37,s86,v53
M 76,v[152:155],204
ds_read2_b32 v[102:103],v14 offset0:162 offset1:163
v_fma_f32 v54,v38,s86,v54
M 76,v[156:159],200
R 132,2,15104
v_fma_f32 v55,v39,s86,v55
M 76,v[160:163],196
v_cvt_pk_bf16_f32 v52,v52,v53
v_cvt_pk_bf16_f32 v53,v54,v55
M 76,v[164:167],192
ds_read2_b32 v[104:105],v15 offset0:32 offset1:33
v_and_b32 v36,0xffff0000,v33
M 76,v[168:171],188
ds_read2_b32 v[106:107],v15 offset0:34 offset1:35
v_lshlrev_b32 v37,16,v33
M 76,v[172:175],184
L 15
R 136,2,14592
v_and_b32 v38,0xffff0000,v32
M 80,v[152:155],208
ds_read2_b32 v[108:109],v15 offset0:160 offset1:161
v_lshlrev_b32 v39,16,v32
M 80,v[156:159],204
ds_write_b64 v4,v[52:53] offset:1536
M 80,v[160:163],200
ds_read2_b32 v[110:111],v15 offset0:162 offset1:163
v_fma_f32 v56,v36,s86,v56
M 80,v[164:167],196
R 140,2,14080
v_fma_f32 v57,v37,s86,v57
M 80,v[168:171],192
ds_read2_b32 v[112:113],v16 offset0:32 offset1:33
v_fma_f32 v58,v38,s86,v58
M 80,v[172:175],188
ds_read2_b32 v[114:115],v16 offset0:34 offset1:35
v_fma_f32 v59,v39,s86,v59
M 80,v[176:179],184
L 15
v_cvt_pk_bf16_f32 v56,v56,v57
v_cvt_pk_bf16_f32 v57,v58,v59
M 84,v[152:155],212
R 144,2,13568
v_and_b32 v36,0xffff0000,v35
M 84,v[156:159],208
ds_read2_b32 v[116:117],v16 offset0:160 offset1:161
v_lshlrev_b32 v37,16,v35
M 84,v[160:163],204
ds_read2_b32 v[118:119],v16 offset0:162 offset1:163
v_and_b32 v38,0xffff0000,v34
M 84,v[164:167],200
R 148,2,13056
v_lshlrev_b32 v39,16,v34
M 84,v[168:171],196
ds_write_b64 v4,v[56:57] offset:2048
M 84,v[172:175],192
v_fma_f32 v60,v36,s86,v60
v_fma_f32 v61,v37,s86,v61
M 84,v[176:179],188
v_fma_f32 v62,v38,s86,v62
v_fma_f32 v63,v39,s86,v63
M 84,v[180:183],184
L 15
v_cvt_pk_bf16_f32 v60,v60,v61
v_cvt_pk_bf16_f32 v61,v62,v63
M 64,v[88:91],128
ds_read2_b32 v[152:153],v1 offset0:48 offset1:49
M 64,v[92:95],124
ds_read2_b32 v[154:155],v1 offset0:50 offset1:51
M 64,v[96:99],120
L 15
R 184,2,16704
M 68,v[88:91],132
ds_read2_b32 v[156:157],v1 offset0:176 offset1:177
M 68,v[92:95],128
ds_write_b64 v4,v[60:61] offset:2560
M 68,v[96:99],124
ds_read2_b32 v[158:159],v1 offset0:178 offset1:179
M 68,v[100:103],120
L 15
R 188,2,16192
M 72,v[88:91],136
ds_read2_b32 v[160:161],v14 offset0:48 offset1:49
M 72,v[92:95],132
ds_read2_b32 v[162:163],v14 offset0:50 offset1:51
M 72,v[96:99],128
M 72,v[100:103],124
R 192,2,15680
M 72,v[104:107],120
L 15
ds_read2_b32 v[164:165],v14 offset0:176 offset1:177
M 76,v[88:91],140
ds_read2_b32 v[166:167],v14 offset0:178 offset1:179
M 76,v[92:95],136
R 196,2,15168
M 76,v[96:99],132
M 76,v[100:103],128
ds_read2_b32 v[168:169],v15 offset0:48 offset1:49
M 76,v[104:107],124
ds_read2_b32 v[170:171],v15 offset0:50 offset1:51
M 76,v[108:111],120
L 15
R 200,2,14656
M 80,v[88:91],144
ds_read2_b32 v[172:173],v15 offset0:176 offset1:177
M 80,v[92:95],140
M 80,v[96:99],136
ds_read2_b32 v[174:175],v15 offset0:178 offset1:179
M 80,v[100:103],132
R 204,2,14144
M 80,v[104:107],128
s_mov_b32 m0,163584
s_mov_b32 s78,1847296
ds_read2_b32 v[176:177],v16 offset0:48 offset1:49
M 80,v[108:111],124
ds_read2_b32 v[178:179],v16 offset0:50 offset1:51
M 80,v[112:115],120
L 15
buffer_load_dword v12,s[60:63],s78 offen offset:0  lds
M 84,v[88:91],148
R 208,2,13632
M 84,v[92:95],144
ds_read2_b32 v[180:181],v16 offset0:176 offset1:177
M 84,v[96:99],140
ds_read2_b32 v[182:183],v16 offset0:178 offset1:179
M 84,v[100:103],136
R 212,2,13120
M 84,v[104:107],132
M 84,v[108:111],128
M 84,v[112:115],124
M 84,v[116:119],120
L 15
M 64,v[152:155],192
ds_read2_b32 v[88:89],v1 offset0:64 offset1:65
M 64,v[156:159],188
ds_read2_b32 v[90:91],v1 offset0:66 offset1:67
M 64,v[160:163],184
L 14
R 120,2,16768
M 68,v[152:155],196
ds_read2_b32 v[92:93],v1 offset0:192 offset1:193
M 68,v[156:159],192
M 68,v[160:163],188
ds_read2_b32 v[94:95],v1 offset0:194 offset1:195
M 68,v[164:167],184
L 14
R 124,2,16256
M 72,v[152:155],200
ds_read2_b32 v[96:97],v14 offset0:64 offset1:65
M 72,v[156:159],196
ds_read2_b32 v[98:99],v14 offset0:66 offset1:67
M 72,v[160:163],192
M 72,v[164:167],188
R 128,2,15744
M 72,v[168:171],184
L 15
ds_read2_b32 v[100:101],v14 offset0:192 offset1:193
M 76,v[152:155],204
ds_read2_b32 v[102:103],v14 offset0:194 offset1:195
M 76,v[156:159],200
R 132,2,15232
M 76,v[160:163],196
M 76,v[164:167],192
ds_read2_b32 v[104:105],v15 offset0:64 offset1:65
M 76,v[168:171],188
ds_read2_b32 v[106:107],v15 offset0:66 offset1:67
M 76,v[172:175],184
L 15
R 136,2,14720
M 80,v[152:155],208
ds_read2_b32 v[108:109],v15 offset0:192 offset1:193
M 80,v[156:159],204
M 80,v[160:163],200
ds_read2_b32 v[110:111],v15 offset0:194 offset1:195
M 80,v[164:167],196
R 140,2,14208
M 80,v[168:171],192
ds_read2_b32 v[112:113],v16 offset0:64 offset1:65
M 80,v[172:175],188
ds_read2_b32 v[114:115],v16 offset0:66 offset1:67
M 80,v[176:179],184
L 15
M 84,v[152:155],212
R 144,2,13696
M 84,v[156:159],208
ds_read2_b32 v[116:117],v16 offset0:192 offset1:193
M 84,v[160:163],204
ds_read2_b32 v[118:119],v16 offset0:194 offset1:195
M 84,v[164:167],200
R 148,2,13184
M 84,v[168:171],196
M 84,v[172:175],192
M 84,v[176:179],188
M 84,v[180:183],184
L 15
M 64,v[88:91],128
ds_read2_b32 v[152:153],v1 offset0:80 offset1:81
M 64,v[92:95],124
ds_read2_b32 v[154:155],v1 offset0:82 offset1:83
M 64,v[96:99],120
L 14
R 184,2,16832
M 68,v[88:91],132
ds_read2_b32 v[156:157],v1 offset0:208 offset1:209
M 68,v[92:95],128
M 68,v[96:99],124
ds_read2_b32 v[158:159],v1 offset0:210 offset1:211
M 68,v[100:103],120
L 14
R 188,2,16320
M 72,v[88:91],136
ds_read2_b32 v[160:161],v14 offset0:80 offset1:81
M 72,v[92:95],132
)"
R"(ds_read2_b32 v[162:163],v14 offset0:82 offset1:83
M 72,v[96:99],128
M 72,v[100:103],124
R 192,2,15808
M 72,v[104:107],120
L 15
ds_read2_b32 v[164:165],v14 offset0:208 offset1:209
M 76,v[88:91],140
ds_read2_b32 v[166:167],v14 offset0:210 offset1:211
M 76,v[92:95],136
R 196,2,15296
M 76,v[96:99],132
M 76,v[100:103],128
ds_read2_b32 v[168:169],v15 offset0:80 offset1:81
M 76,v[104:107],124
ds_read2_b32 v[170:171],v15 offset0:82 offset1:83
M 76,v[108:111],120
L 15
R 200,2,14784
M 80,v[88:91],144
ds_read2_b32 v[172:173],v15 offset0:208 offset1:209
M 80,v[92:95],140
M 80,v[96:99],136
ds_read2_b32 v[174:175],v15 offset0:210 offset1:211
M 80,v[100:103],132
R 204,2,14272
M 80,v[104:107],128
ds_read2_b32 v[176:177],v16 offset0:80 offset1:81
M 80,v[108:111],124
ds_read2_b32 v[178:179],v16 offset0:82 offset1:83
M 80,v[112:115],120
L 15
M 84,v[88:91],148
R 208,2,13760
M 84,v[92:95],144
ds_read2_b32 v[180:181],v16 offset0:208 offset1:209
M 84,v[96:99],140
ds_read2_b32 v[182:183],v16 offset0:210 offset1:211
M 84,v[100:103],136
R 212,2,13248
M 84,v[104:107],132
M 84,v[108:111],128
M 84,v[112:115],124
M 84,v[116:119],120
L 15
M 64,v[152:155],192
ds_read2_b32 v[88:89],v1 offset0:96 offset1:97
M 64,v[156:159],188
ds_read2_b32 v[90:91],v1 offset0:98 offset1:99
M 64,v[160:163],184
L 14
R 120,2,16896
M 68,v[152:155],196
ds_read2_b32 v[92:93],v1 offset0:224 offset1:225
M 68,v[156:159],192
M 68,v[160:163],188
ds_read2_b32 v[94:95],v1 offset0:226 offset1:227
M 68,v[164:167],184
L 14
R 124,2,16384
M 72,v[152:155],200
ds_read2_b32 v[96:97],v14 offset0:96 offset1:97
M 72,v[156:159],196
ds_read2_b32 v[98:99],v14 offset0:98 offset1:99
M 72,v[160:163],192
M 72,v[164:167],188
R 128,2,15872
M 72,v[168:171],184
L 15
ds_read2_b32 v[100:101],v14 offset0:224 offset1:225
M 76,v[152:155],204
ds_read2_b32 v[102:103],v14 offset0:226 offset1:227
M 76,v[156:159],200
R 132,2,15360
M 76,v[160:163],196
M 76,v[164:167],192
ds_read2_b32 v[104:105],v15 offset0:96 offset1:97
M 76,v[168:171],188
ds_read2_b32 v[106:107],v15 offset0:98 offset1:99
M 76,v[172:175],184
L 15
R 136,2,14848
M 80,v[152:155],208
ds_read2_b32 v[108:109],v15 offset0:224 offset1:225
M 80,v[156:159],204
M 80,v[160:163],200
ds_read2_b32 v[110:111],v15 offset0:226 offset1:227
M 80,v[164:167],196
R 140,2,14336
M 80,v[168:171],192
ds_read2_b32 v[112:113],v16 offset0:96 offset1:97
M 80,v[172:175],188
ds_read2_b32 v[114:115],v16 offset0:98 offset1:99
M 80,v[176:179],184
L 15
M 84,v[152:155],212
R 144,2,13824
M 84,v[156:159],208
ds_read2_b32 v[116:117],v16 offset0:224 offset1:225
M 84,v[160:163],204
ds_read2_b32 v[118:119],v16 offset0:226 offset1:227
M 84,v[164:167],200
R 148,2,13312
M 84,v[168:171],196
M 84,v[172:175],192
M 84,v[176:179],188
M 84,v[180:183],184
L 15
M 64,v[88:91],128
ds_read2_b32 v[152:153],v1 offset0:112 offset1:113
M 64,v[92:95],124
ds_read2_b32 v[154:155],v1 offset0:114 offset1:115
M 64,v[96:99],120
L 14
R 184,2,16960
M 68,v[88:91],132
ds_read2_b32 v[156:157],v1 offset0:240 offset1:241
M 68,v[92:95],128
M 68,v[96:99],124
ds_read2_b32 v[158:159],v1 offset0:242 offset1:243
M 68,v[100:103],120
L 14
R 188,2,16448
M 72,v[88:91],136
ds_read2_b32 v[160:161],v14 offset0:112 offset1:113
M 72,v[92:95],132
ds_read2_b32 v[162:163],v14 offset0:114 offset1:115
M 72,v[96:99],128
M 72,v[100:103],124
R 192,2,15936
M 72,v[104:107],120
L 15
ds_read2_b32 v[164:165],v14 offset0:240 offset1:241
M 76,v[88:91],140
ds_read2_b32 v[166:167],v14 offset0:242 offset1:243
M 76,v[92:95],136
R 196,2,15424
M 76,v[96:99],132
M 76,v[100:103],128
ds_read2_b32 v[168:169],v15 offset0:112 offset1:113
M 76,v[104:107],124
ds_read2_b32 v[170:171],v15 offset0:114 offset1:115
M 76,v[108:111],120
L 15
R 200,2,14912
M 80,v[88:91],144
ds_read2_b32 v[172:173],v15 offset0:240 offset1:241
M 80,v[92:95],140
M 80,v[96:99],136
ds_read2_b32 v[174:175],v15 offset0:242 offset1:243
M 80,v[100:103],132
R 204,2,14400
M 80,v[104:107],128
ds_read2_b32 v[176:177],v16 offset0:112 offset1:113
M 80,v[108:111],124
ds_read2_b32 v[178:179],v16 offset0:114 offset1:115
M 80,v[112:115],120
L 15
M 84,v[88:91],148
R 208,2,13888
M 84,v[92:95],144
ds_read2_b32 v[180:181],v16 offset0:240 offset1:241
M 84,v[96:99],140
ds_read2_b32 v[182:183],v16 offset0:242 offset1:243
M 84,v[100:103],136
R 212,2,13376
M 84,v[104:107],132
M 84,v[108:111],128
M 84,v[112:115],124
M 84,v[116:119],120
L 15
M 64,v[152:155],192
M 64,v[156:159],188
M 64,v[160:163],184
L 12
M 68,v[152:155],196
M 68,v[156:159],192
M 68,v[160:163],188
M 68,v[164:167],184
L 9
M 72,v[152:155],200
M 72,v[156:159],196
M 72,v[160:163],192
ds_read_b64 v[24:25],v3 offset:15488
ds_read_b64 v[26:27],v3 offset:14976
M 72,v[164:167],188
ds_read_b64 v[28:29],v3 offset:14464
ds_read_b64 v[30:31],v3 offset:13952
M 72,v[168:171],184
L 10
ds_read_b64 v[32:33],v3 offset:13440
ds_read_b64 v[34:35],v3 offset:12928
M 76,v[152:155],204
M 76,v[156:159],200
M 76,v[160:163],196
M 76,v[164:167],192
M 76,v[168:171],188
M 76,v[172:175],184
L 9
M 80,v[152:155],208
M 80,v[156:159],204
M 80,v[160:163],200
M 80,v[164:167],196
M 80,v[168:171],192
M 80,v[172:175],188
M 80,v[176:179],184
L 6
M 84,v[152:155],212
M 84,v[156:159],208
M 84,v[160:163],204
M 84,v[164:167],200
M 84,v[168:171],196
M 84,v[172:175],192
M 84,v[176:179],188
M 84,v[180:183],184
L 0
v_and_b32 v36,0xffff0000,v25
v_lshlrev_b32 v37,16,v25
v_and_b32 v38,0xffff0000,v24
v_lshlrev_b32 v39,16,v24
v_fma_f32 v64,v36,s87,v64
v_fma_f32 v65,v37,s87,v65
v_fma_f32 v66,v38,s87,v66
v_fma_f32 v67,v39,s87,v67
v_cvt_pk_bf16_f32 v64,v64,v65
v_cvt_pk_bf16_f32 v65,v66,v67
ds_write_b64 v4,v[64:65] offset:12928
v_and_b32 v36,0xffff0000,v27
v_lshlrev_b32 v37,16,v27
v_and_b32 v38,0xffff0000,v26
v_lshlrev_b32 v39,16,v26
v_fma_f32 v68,v36,s87,v68
v_fma_f32 v69,v37,s87,v69
v_fma_f32 v70,v38,s87,v70
v_fma_f32 v71,v39,s87,v71
v_cvt_pk_bf16_f32 v68,v68,v69
v_cvt_pk_bf16_f32 v69,v70,v71
ds_write_b64 v4,v[68:69] offset:13440
v_and_b32 v36,0xffff0000,v29
v_lshlrev_b32 v37,16,v29
v_and_b32 v38,0xffff0000,v28
v_lshlrev_b32 v39,16,v28
v_fma_f32 v72,v36,s87,v72
v_fma_f32 v73,v37,s87,v73
v_fma_f32 v74,v38,s87,v74
v_fma_f32 v75,v39,s87,v75
v_cvt_pk_bf16_f32 v72,v72,v73
v_cvt_pk_bf16_f32 v73,v74,v75
ds_write_b64 v4,v[72:73] offset:13952
v_and_b32 v36,0xffff0000,v31
v_lshlrev_b32 v37,16,v31
v_and_b32 v38,0xffff0000,v30
v_lshlrev_b32 v39,16,v30
v_fma_f32 v76,v36,s87,v76
v_fma_f32 v77,v37,s87,v77
v_fma_f32 v78,v38,s87,v78
v_fma_f32 v79,v39,s87,v79
v_cvt_pk_bf16_f32 v76,v76,v77
v_cvt_pk_bf16_f32 v77,v78,v79
ds_write_b64 v4,v[76:77] offset:14464
v_and_b32 v36,0xffff0000,v33
v_lshlrev_b32 v37,16,v33
v_and_b32 v38,0xffff0000,v32
v_lshlrev_b32 v39,16,v32
v_fma_f32 v80,v36,s87,v80
v_fma_f32 v81,v37,s87,v81
v_fma_f32 v82,v38,s87,v82
v_fma_f32 v83,v39,s87,v83
v_cvt_pk_bf16_f32 v80,v80,v81
v_cvt_pk_bf16_f32 v81,v82,v83
ds_write_b64 v4,v[80:81] offset:14976
v_and_b32 v36,0xffff0000,v35
v_lshlrev_b32 v37,16,v35
v_and_b32 v38,0xffff0000,v34
v_lshlrev_b32 v39,16,v34
v_fma_f32 v84,v36,s87,v84
v_fma_f32 v85,v37,s87,v85
v_fma_f32 v86,v38,s87,v86
v_fma_f32 v87,v39,s87,v87
v_cvt_pk_bf16_f32 v84,v84,v85
v_cvt_pk_bf16_f32 v85,v86,v87
ds_write_b64 v4,v[84:85] offset:15488
Lcv_e_%=:
L 0
s_barrier
R 152,5,0
R 156,5,512
R 160,5,1024
R 164,5,1536
R 168,5,2048
R 172,5,2560
s_mov_b32 s78,99614720
L 5
buffer_store_dwordx4 v[152:155],v7,s[36:39],s78 offen offset:0 sc0 sc1
s_add_u32 s78,s56,100139008
L 4
buffer_store_dwordx4 v[156:159],v7,s[36:39],s78 offen offset:0 sc0 sc1
s_mov_b32 s78,100663296
L 3
buffer_store_dwordx4 v[160:163],v7,s[36:39],s78 offen offset:0 sc0 sc1
s_add_u32 s78,s56,101187584
L 2
buffer_store_dwordx4 v[164:167],v7,s[36:39],s78 offen offset:0 sc0 sc1
s_mov_b32 s78,101711872
L 1
buffer_store_dwordx4 v[168:171],v7,s[36:39],s78 offen offset:0 sc0 sc1
s_add_u32 s78,s56,102236160
L 0
buffer_store_dwordx4 v[172:175],v7,s[36:39],s78 offen offset:0 sc0 sc1
s_waitcnt vmcnt(0)
s_barrier
s_add_u32 s48,s48,32768
buffer_store_dword v23,v10,s[36:39],s48 offen offset:0 sc0 sc1
s_cmp_eq_u32 s51,2
s_cbranch_scc0 Lcv_f_%=
s_mov_b32 m0,163584
s_mov_b32 s78,3694592
buffer_load_dword v12,s[60:63],s78 offen offset:0  lds
s_mov_b32 m0,163584
s_mov_b32 s78,5541888
buffer_load_dword v12,s[60:63],s78 offen offset:0  lds
Lcv_f_%=:
Lcode_cv_e:
.purgem M
.purgem R
.purgem T
.purgem D
.purgem L
)"
  :: "s"(ws), "s"(__builtin_amdgcn_readfirstlane(sWave)), "s"(__builtin_amdgcn_readfirstlane(sBuf)), "s"(__builtin_amdgcn_readfirstlane(sAuxOff)), "s"(__builtin_amdgcn_readfirstlane(sFoutOff)), "s"(__builtin_amdgcn_readfirstlane(sC0)), "s"(__builtin_amdgcn_readfirstlane(sC1)), "s"(__builtin_amdgcn_readfirstlane(sExp)), "v"(vL16), "v"(vPA), "v"(vPB), "v"(vPU), "v"(vPW), "v"(vOR), "v"(vORA), "v"(vOG), "v"(vFA), "v"(vFB), "v"(vFlag), "v"(vTW), "v"(vTX), "s"(w3), "s"(w4) : "v0", "v1", "v2", "v3", "v4", "v5", "v6", "v7", "v8", "v9", "v10", "v11", "v12", "v13", "v14", "v15", "v16", "v17", "v18", "v19", "v20", "v21", "v22", "v23", "v24", "v25", "v26", "v27", "v28", "v29", "v30", "v31", "v32", "v33", "v34", "v35", "v36", "v37", "v38", "v39", "v40", "v41", "v42", "v43", "v44", "v45", "v46", "v47", "v48", "v49", "v50", "v51", "v52", "v53", "v54", "v55", "v56", "v57", "v58", "v59", "v60", "v61", "v62", "v63", "v64", "v65", "v66", "v67", "v68", "v69", "v70", "v71", "v72", "v73", "v74", "v75", "v76", "v77", "v78", "v79", "v80", "v81", "v82", "v83", "v84", "v85", "v86", "v87", "v88", "v89", "v90", "v91", "v92", "v93", "v94", "v95", "v96", "v97", "v98", "v99", "v100", "v101", "v102", "v103", "v104", "v105", "v106", "v107", "v108", "v109", "v110", "v111", "v112", "v113", "v114", "v115", "v116", "v117", "v118", "v119", "v120", "v121", "v122", "v123", "v124", "v125", "v126", "v127", "v128", "v129", "v130", "v131", "v132", "v133", "v134", "v135", "v136", "v137", "v138", "v139", "v140", "v141", "v142", "v143", "v144", "v145", "v146", "v147", "v148", "v149", "v150", "v151", "v152", "v153", "v154", "v155", "v156", "v157", "v158", "v159", "v160", "v161", "v162", "v163", "v164", "v165", "v166", "v167", "v168", "v169", "v170", "v171", "v172", "v173", "v174", "v175", "v176", "v177", "v178", "v179", "v180", "v181", "v182", "v183", "v184", "v185", "v186", "v187", "v188", "v189", "v190", "v191", "v192", "v193", "v194", "v195", "v196", "v197", "v198", "v199", "v200", "v201", "v202", "v203", "v204", "v205", "v206", "v207", "v208", "v209", "v210", "v211", "v212", "v213", "v214", "v215", "v216", "v217", "v218", "v219", "v220", "v221", "v222", "v223", "v224", "v225", "v226", "v227", "v228", "v229", "v230", "v231", "a0", "a1", "a2", "a3", "a4", "a5", "a6", "a7", "a8", "a9", "a10", "a11", "a12", "a13", "a14", "a15", "a16", "a17", "a18", "a19", "a20", "a21", "a22", "a23", "a24", "a25", "a26", "a27", "a28", "a29", "a30", "a31", "a32", "a33", "a34", "a35", "a36", "a37", "a38", "a39", "a40", "a41", "a42", "a43", "a44", "a45", "a46", "a47", "a48", "a49", "a50", "a51", "a52", "a53", "a54", "a55", "a56", "a57", "a58", "a59", "a60", "a61", "a62", "a63", "a64", "a65", "a66", "a67", "a68", "a69", "a70", "a71", "a72", "a73", "a74", "a75", "a76", "a77", "a78", "a79", "a80", "a81", "a82", "a83", "a84", "a85", "a86", "a87", "a88", "a89", "a90", "a91", "a92", "a93", "a94", "a95", "a96", "a97", "a98", "a99", "a100", "a101", "a102", "a103", "a104", "a105", "a106", "a107", "a108", "a109", "a110", "a111", "a112", "a113", "a114", "a115", "a116", "a117", "a118", "a119", "a120", "a121", "a122", "a123", "a124", "a125", "a126", "a127", "a128", "a129", "a130", "a131", "a132", "a133", "a134", "a135", "a136", "a137", "a138", "a139", "a140", "a141", "a142", "a143", "a144", "a145", "a146", "a147", "a148", "a149", "a150", "a151", "a152", "a153", "a154", "a155", "a156", "a157", "a158", "a159", "a160", "a161", "a162", "a163", "a164", "a165", "a166", "a167", "a168", "a169", "a170", "a171", "a172", "a173", "a174", "a175", "a176", "a177", "a178", "a179", "a180", "a181", "a182", "a183", "a184", "a185", "a186", "a187", "a188", "a189", "a190", "a191", "a192", "a193", "a194", "a195", "a196", "a197", "a198", "a199", "a200", "a201", "a202", "a203", "a204", "a205", "a206", "a207", "a208", "a209", "a210", "a211", "a212", "a213", "a214", "a215", "a216", "a217", "a218", "a219", "a220", "a221", "a222", "a223", "a224", "a225", "a226", "a227", "a228", "a229", "a230", "a231", "a232", "a233", "a234", "a235", "a236", "a237", "a238", "a239", "a240", "a241", "a242", "a243", "a244", "a245", "a246", "a247", "a248", "a249", "a250", "a251", "a252", "a253", "a254", "a255", "s36", "s37", "s38", "s39", "s40", "s41", "s42", "s43", "s44", "s45", "s46", "s47", "s48", "s49", "s50", "s51", "s52", "s53", "s54", "s55", "s56", "s57", "s58", "s59", "s60", "s61", "s62", "s63", "s64", "s65", "s66", "s67", "s68", "s69", "s70", "s71", "s72", "s73", "s74", "s75", "s76", "s77", "s78", "s79", "s80", "s81", "s82", "s83", "s84", "s85", "s86", "s87", "s88", "s89", "s90", "s91", "s92", "s93", "s94", "s95", "s96", "s97", "vcc", "scc", "memory");
    LANE_ID(lq_);
    const int g = QT0 + (NCV - 1 - iw) * 164 + min(wave * 64 + lq_, 163);
    if (QTB) touch_lds((const char*)q + (size_t)(g < 32768 ? g : 0) * 128, sScr);
  }
  WAIT_LGKM0();
  __builtin_amdgcn_s_barrier();
  CTS(10);
  }
  const unsigned sDbg0 = 110u * 1048576u + (b * 4 + wave) * 8192;
  constexpr unsigned OOB = 0x80000000u;
  if (kind == 6) {
    LANE_ID(lr);
    const int r8 = lr & 7, seg = lr >> 3, j = lr & 15, g = lr >> 4, rr = 8 * g + (j >> 2);
    const unsigned sWave = wave, sExp = __builtin_amdgcn_readfirstlane(ep), sG0 = slice < 16 ? slice + 1 : 0, sStep = NRW, sDbg = sDbg0;
    const unsigned sWw = ((slice >> 3) * 1792 + 16 * wave) * 2048 + (slice & 7) * 256;
    const unsigned vWd0 = seg * 2048 + (r8 ^ swzB(seg)) * 16, vWd1 = (seg + 8) * 2048 + (r8 ^ swzB(8 + seg)) * 16;
    const unsigned vWr0 = (wave >> 1) * 8192 + rr * 128 + (((wave & 1) * 4 + (lr & 3)) ^ swzB(rr)) * 16, vWr1 = vWr0 + 8;
    const unsigned vRF = (wave * 8 + (seg >> 2)) * 1024 + ((seg & 3) * 16 + r8) * 16;
    const unsigned vRwr = seg == 0 ? r8 * 64 + wave * 4 : 512 + lr * 4, vRrd = r8 * 64;
    const unsigned vRP = g == (j >> 2) ? r8 * 64 + 16 + wave * 8 + (seg & 1) * 4 : 768 + lr * 4;
    const unsigned vRS = (wave == 0 && seg == 0) ? r8 * 16 : OOB, vFlag = lr == 0 ? 0u : OOB;
    const unsigned vL4 = ((lr & 2) * 4 + wave * 2 + (lr & 1)) * FSP, vLY = min((wave < 3 ? wave * 32 : 112) + lr, wave < 2 ? wave * 32 + 31 : wave == 2 ? 111 : NCV - 1) * 128;
    const unsigned vQ = r8 * 2048 + wave * 512 + seg * 16;
    const unsigned vY0 = wave * 4096 + lr * 16;
    const unsigned vTr = 4096 + wave * 4096 + ((2 * g + ((j & 3) >> 1)) * 8 + (j >> 2)) * 16 + (j & 1) * 8;
    const unsigned one = g == (j >> 2) ? 0x3f80u << (16 * (j & 1)) : 0u, vSa = (j & 2) ? 0u : one, vSb = (j & 2) ? one : 0u;
    (void)sDbg;
    asm volatile(
R"(.macro M d,w,x
v_mfma_f32_16x16x32_bf16 v[\d:\d+3], \w, v[\x:\x+3], v[\d:\d+3]
.endm
.macro R d,a,o
ds_read_b128 v[\d:\d+3], v\a offset:\o
.endm
.macro T d,a,o
ds_read_b64_tr_b16 \d, v\a offset:\o
.endm
.macro D a,s,o
buffer_load_dwordx4 v\a, s[36:39], s\s offen offset:\o sc1 lds
.endm
.macro L c
s_waitcnt lgkmcnt(\c)
.endm
Lcode_r_b:
s_mov_b64 s[36:37],%0
s_mov_b32 s38,111243392
s_mov_b32 s39,0x00020000
s_mov_b32 s44,%1
s_mov_b32 s45,%2
s_mov_b32 s46,%3
s_mov_b32 s50,%4
s_mov_b32 s53,%5
v_mov_b32 v0,%6
v_mov_b32 v1,%7
v_mov_b32 v2,%8
v_mov_b32 v3,%9
v_mov_b32 v4,%10
v_mov_b32 v5,%11
v_mov_b32 v6,%12
v_mov_b32 v7,%13
v_mov_b32 v8,%14
v_mov_b32 v9,%15
v_mov_b32 v10,%16
v_mov_b32 v11,%17
v_mov_b32 v12,%18
v_mov_b32 v13,%19
v_mov_b32 v14,%20
v_mov_b32 v15,%21
v_mov_b32 v16,%22
s_mov_b32 s47,256
s_mov_b32 s49,0
s_mov_b32 s93,s45
s_mov_b64 s[84:85],%23
s_mov_b32 s86,4194304
s_mov_b32 s87,0x00020000
s_lshl_b32 s55,s45,14
buffer_load_dwordx4 v[164:167],v8,s[84:87],s55 offen offset:0 nt
buffer_load_dwordx4 v[168:171],v8,s[84:87],s55 offen offset:128 nt
buffer_load_dwordx4 v[172:175],v8,s[84:87],s55 offen offset:256 nt
buffer_load_dwordx4 v[176:179],v8,s[84:87],s55 offen offset:384 nt
v_mov_b32 v17,s53
v_mbcnt_lo_u32_b32 v19,-1,0
v_mbcnt_hi_u32_b32 v19,-1,v19
v_lshlrev_b32 v19,4,v19
s_mov_b32 s82,0
s_mov_b32 s63,0x7fff0000
s_mov_b32 s64,0
s_mov_b32 s67,0
s_lshl_b32 s65,s44,12
s_add_u32 s65,s65,4096
s_mul_i32 s66,s44,128
s_add_u32 s66,s66,109056000
s_mov_b32 s70,0xaaaaaaaa
s_mov_b32 s71,0xaaaaaaaa
s_mov_b32 s72,0xcccccccc
s_mov_b32 s73,0xcccccccc
s_mov_b32 s74,0xff00ff00
s_mov_b32 s75,0xff00ff00
s_mov_b32 s68,0x3a800000
s_mov_b32 s69,0x3727c5ac
s_mov_b32 s76,0xbeeff558
v_mov_b32 v32,0x3f803f80
v_mov_b32 v33,0x3f803f80
v_mov_b32 v34,0x3f803f80
v_mov_b32 v35,0x3f803f80
v_mov_b32 v24,0
v_mov_b32 v25,0
v_mov_b32 v26,v11
v_mov_b32 v27,v12
v_mov_b32 v28,0
v_mov_b32 v29,0
s_getpc_b64 s[94:95]
Lr_pc_%=:
s_add_u32 s94,s94,Lr_tab_%=-Lr_pc_%=
s_addc_u32 s95,s95,0
s_mov_b32 s81,0x7fff0000
s_lshl_b32 s54,s44,11
s_add_u32 s80,s45,85
s_cmp_eq_u32 s45,0
s_cselect_b32 s80,-1,s80
s_waitcnt vmcnt(0)
Lr_a0_%=:
s_cmp_lg_u32 s67,0
s_cbranch_scc1 Lr_b0_%=
s_waitcnt vmcnt(14)
v_mfma_f32_16x16x32_bf16 v[36:39],v[32:35],v[164:167],0
s_add_u32 s79,s45,s46
s_cmp_lt_u32 s45,64
s_cselect_b32 s78,s49,0
v_mfma_f32_16x16x32_bf16 v[40:43],v[164:167],v[164:167],0
s_cmp_ge_u32 s79,64
s_cselect_b32 s78,s78,0
s_add_u32 s62,s79,s78
M 36,v[32:35],168
s_lshr_b32 s78,s45,1
s_and_b32 s79,s45,1
s_lshl_b32 s79,s79,7
M 40,v[168:171],168
s_lshl_b32 s57,s78,15
s_lshl_b32 s55,s62,14
s_cmp_lt_u32 s62,s47
M 36,v[32:35],172
s_cselect_b32 s55,s55,0x7fff0000
s_add_u32 s57,s57,s79
s_lshl_b32 s59,s78,9
M 40,v[172:175],172
buffer_load_dwordx4 v[196:199],v8,s[84:87],s55 offen offset:0 nt
M 36,v[32:35],176
buffer_load_dwordx4 v[200:203],v8,s[84:87],s55 offen offset:128 nt
M 40,v[176:179],176
buffer_load_dwordx4 v[204:207],v8,s[84:87],s55 offen offset:256 nt
v_mfma_f32_16x16x32_bf16 v[116:119],v[26:29],v[164:167],0
buffer_load_dwordx4 v[208:211],v8,s[84:87],s55 offen offset:384 nt
v_mfma_f32_16x16x32_bf16 v[120:123],v[24:27],v[164:167],0
s_add_u32 s57,s57,8388608
s_add_u32 s59,s59,s79
s_lshl_b32 s60,s45,9
v_mfma_f32_16x16x32_bf16 v[124:127],v[26:29],v[168:171],0
s_add_u32 s58,s57,4096
s_add_u32 s59,s59,106954752
s_add_u32 s60,s60,s66
v_mfma_f32_16x16x32_bf16 v[128:131],v[24:27],v[168:171],0
v_add_f32_dpp v44,v36,v36 row_ror:8 row_mask:0xf bank_mask:0xf
v_cndmask_b32 v45,v40,v41,s[70:71]
v_mfma_f32_16x16x32_bf16 v[132:135],v[26:29],v[172:175],0
ds_write_b32 v2,v44 offset:0
v_mfma_f32_16x16x32_bf16 v[136:139],v[24:27],v[172:175],0
v_cndmask_b32 v46,v42,v43,s[70:71]
v_cndmask_b32 v45,v45,v46,s[72:73]
v_mfma_f32_16x16x32_bf16 v[140:143],v[26:29],v[176:179],0
ds_write_b32 v0,v45 offset:0
v_mfma_f32_16x16x32_bf16 v[144:147],v[24:27],v[176:179],0
L 0
s_barrier
R 52,3,0
R 56,3,16
R 60,3,32
s_waitcnt vmcnt(8)
buffer_store_dword v17,v5,s[36:39],s63 offen offset:0 sc0 sc1
s_mov_b32 s63,0x7fff0000
s_cmp_lt_u32 s45,64
s_cselect_b32 s64,-192,0
s_add_u32 s64,s64,256
s_cmp_lt_u32 s45,64
s_cselect_b32 s61,0,32768
s_add_u32 s61,s61,111177728
s_mul_i32 s56,s45,16384
s_add_u32 s56,s56,98566144
Lr_bpy0_%=:
buffer_load_dword v18,v7,s[36:39],s61 offen offset:0 sc1
s_add_u32 s82,s82,1
s_waitcnt vmcnt(0)
v_cmp_eq_u32 vcc,s53,v18
s_nop 0
s_cmp_eq_u64 vcc,-1
s_cbranch_scc1 Lr_bqy0_%=
s_sleep 2
s_cmp_lt_u32 s82,20000
s_cbranch_scc1 Lr_bpy0_%=
Lr_bqy0_%=:
s_add_u32 m0,s65,0
s_nop 0
D 9,56,0
D 9,56,1024
D 9,56,2048
D 9,56,3072
s_waitcnt vmcnt(0)
T v[68:69],10,0
T v[70:71],10,64
T v[72:73],10,1024
T v[74:75],10,1088
T v[76:77],10,2048
T v[78:79],10,2112
T v[80:81],10,3072
T v[82:83],10,3136
s_mov_b32 s45,s62
s_min_u32 s78,s64,s47
s_mul_i32 s56,s45,16384
s_add_u32 s56,s56,98566144
s_cmp_lt_u32 s45,s78
s_cselect_b32 s67,1,0
s_cselect_b32 s56,s56,0x7fff0000
s_add_u32 m0,s65,16384
s_nop 0
D 9,56,0
D 9,56,1024
D 9,56,2048
D 9,56,3072
L 4
v_lshlrev_b32 v84,16,v68
v_and_b32 v85,0xffff0000,v68
v_lshlrev_b32 v86,16,v69
v_and_b32 v87,0xffff0000,v69
v_lshlrev_b32 v88,16,v70
v_and_b32 v89,0xffff0000,v70
v_lshlrev_b32 v90,16,v71
v_and_b32 v91,0xffff0000,v71
v_lshlrev_b32 v92,16,v72
v_and_b32 v93,0xffff0000,v72
v_lshlrev_b32 v94,16,v73
v_and_b32 v95,0xffff0000,v73
v_lshlrev_b32 v96,16,v74
v_and_b32 v97,0xffff0000,v74
v_lshlrev_b32 v98,16,v75
v_and_b32 v99,0xffff0000,v75
L 0
v_lshlrev_b32 v100,16,v76
v_and_b32 v101,0xffff0000,v76
v_lshlrev_b32 v102,16,v77
v_and_b32 v103,0xffff0000,v77
v_lshlrev_b32 v104,16,v78
v_and_b32 v105,0xffff0000,v78
v_lshlrev_b32 v106,16,v79
v_and_b32 v107,0xffff0000,v79
v_lshlrev_b32 v108,16,v80
v_and_b32 v109,0xffff0000,v80
v_lshlrev_b32 v110,16,v81
v_and_b32 v111,0xffff0000,v81
v_lshlrev_b32 v112,16,v82
v_and_b32 v113,0xffff0000,v82
v_lshlrev_b32 v114,16,v83
v_and_b32 v115,0xffff0000,v83
s_branch Lr_c0_%=
Lr_b0_%=:
s_waitcnt vmcnt(10)
T v[68:69],10,0
T v[70:71],10,64
T v[72:73],10,1024
T v[74:75],10,1088
T v[76:77],10,2048
T v[78:79],10,2112
T v[80:81],10,3072
T v[82:83],10,3136
v_mfma_f32_16x16x32_bf16 v[36:39],v[32:35],v[164:167],0
s_add_u32 s79,s45,s46
s_cmp_lt_u32 s45,64
s_cselect_b32 s78,s49,0
v_mfma_f32_16x16x32_bf16 v[40:43],v[164:167],v[164:167],0
s_cmp_ge_u32 s79,64
s_cselect_b32 s78,s78,0
s_add_u32 s62,s79,s78
M 36,v[32:35],168
s_lshr_b32 s78,s45,1
s_and_b32 s79,s45,1
s_lshl_b32 s60,s45,9
M 40,v[168:171],168
s_lshl_b32 s57,s78,15
s_lshl_b32 s59,s78,9
s_mov_b32 s45,s62
M 36,v[32:35],172
s_lshl_b32 s79,s79,7
s_min_u32 s78,s64,s47
s_mul_i32 s56,s45,16384
M 40,v[172:175],172
s_lshl_b32 s55,s62,14
s_cmp_lt_u32 s62,s47
s_cselect_b32 s55,s55,0x7fff0000
M 36,v[32:35],176
buffer_load_dwordx4 v[196:199],v8,s[84:87],s55 offen offset:0 nt
M 40,v[176:179],176
buffer_load_dwordx4 v[200:203],v8,s[84:87],s55 offen offset:128 nt
v_mfma_f32_16x16x32_bf16 v[116:119],v[26:29],v[164:167],0
buffer_load_dwordx4 v[204:207],v8,s[84:87],s55 offen offset:256 nt
v_mfma_f32_16x16x32_bf16 v[120:123],v[24:27],v[164:167],0
buffer_load_dwordx4 v[208:211],v8,s[84:87],s55 offen offset:384 nt
v_mfma_f32_16x16x32_bf16 v[124:127],v[26:29],v[168:171],0
s_add_u32 s57,s57,s79
s_add_u32 s56,s56,98566144
s_cmp_lt_u32 s45,s78
v_mfma_f32_16x16x32_bf16 v[128:131],v[24:27],v[168:171],0
s_cselect_b32 s56,s56,0x7fff0000
v_add_f32_dpp v44,v36,v36 row_ror:8 row_mask:0xf bank_mask:0xf
v_cndmask_b32 v45,v40,v41,s[70:71]
v_mfma_f32_16x16x32_bf16 v[132:135],v[26:29],v[172:175],0
s_cselect_b32 s67,1,0
ds_write_b32 v2,v44 offset:0
v_mfma_f32_16x16x32_bf16 v[136:139],v[24:27],v[172:175],0
s_add_u32 s57,s57,8388608
v_cndmask_b32 v46,v42,v43,s[70:71]
v_cndmask_b32 v45,v45,v46,s[72:73]
v_mfma_f32_16x16x32_bf16 v[140:143],v[26:29],v[176:179],0
s_add_u32 s59,s59,s79
ds_write_b32 v0,v45 offset:0
v_mfma_f32_16x16x32_bf16 v[144:147],v[24:27],v[176:179],0
L 2
s_add_u32 m0,s65,16384
s_add_u32 s58,s57,4096
v_mfma_f32_16x16x32_bf16 v[84:87],v[26:29],v[68:71],0
D 9,56,0
v_mfma_f32_16x16x32_bf16 v[88:91],v[24:27],v[68:71],0
D 9,56,1024
v_mfma_f32_16x16x32_bf16 v[92:95],v[26:29],v[72:75],0
L 0
D 9,56,2048
v_mfma_f32_16x16x32_bf16 v[96:99],v[24:27],v[72:75],0
s_barrier
R 52,3,0
R 56,3,16
v_mfma_f32_16x16x32_bf16 v[100:103],v[26:29],v[76:79],0
s_add_u32 s59,s59,106954752
s_add_u32 s60,s60,s66
R 60,3,32
v_mfma_f32_16x16x32_bf16 v[104:107],v[24:27],v[76:79],0
D 9,56,3072
v_mfma_f32_16x16x32_bf16 v[108:111],v[26:29],v[80:83],0
v_mfma_f32_16x16x32_bf16 v[112:115],v[24:27],v[80:83],0
Lr_c0_%=:
L 0
v_pk_add_f32 v[52:53],v[52:53],v[54:55]
v_pk_add_f32 v[56:57],v[56:57],v[58:59]
v_pk_add_f32 v[60:61],v[60:61],v[62:63]
v_add_f32 v52,v52,v53
v_pk_add_f32 v[56:57],v[56:57],v[60:61]
v_mul_f32 v52,s68,v52
v_add_f32 v53,v56,v57
v_mul_f32 v53,s68,v53
v_fma_f32 v53,-v52,v52,v53
v_max_f32 v53,0,v53
s_waitcnt vmcnt(12)
buffer_store_dword v17,v5,s[36:39],s63 offen offset:0 sc0 sc1
s_mov_b32 s63,0x7fff0000
v_add_f32 v53,s69,v53
v_rsq_f32 v48,v53
s_nop 0
v_mul_f32_e64 v50,-v52,v48
v_mov_b32 v49,v48
v_mov_b32 v51,v50
v_pk_fma_f32 v[52:53],v[116:117],v[48:49],v[50:51]
v_pk_fma_f32 v[54:55],v[118:119],v[48:49],v[50:51]
v_pk_fma_f32 v[56:57],v[120:121],v[48:49],v[50:51]
v_pk_fma_f32 v[58:59],v[122:123],v[48:49],v[50:51]
v_pk_fma_f32 v[116:117],v[52:53],v[84:85],v[116:117]
v_pk_fma_f32 v[118:119],v[54:55],v[86:87],v[118:119]
v_pk_fma_f32 v[120:121],v[56:57],v[88:89],v[120:121]
v_pk_fma_f32 v[122:123],v[58:59],v[90:91],v[122:123]
v_pk_fma_f32 v[52:53],v[124:125],v[48:49],v[50:51]
v_pk_fma_f32 v[54:55],v[126:127],v[48:49],v[50:51]
v_pk_fma_f32 v[56:57],v[128:129],v[48:49],v[50:51]
v_pk_fma_f32 v[58:59],v[130:131],v[48:49],v[50:51]
v_pk_fma_f32 v[124:125],v[52:53],v[92:93],v[124:125]
v_pk_fma_f32 v[126:127],v[54:55],v[94:95],v[126:127]
v_pk_fma_f32 v[128:129],v[56:57],v[96:97],v[128:129]
v_pk_fma_f32 v[130:131],v[58:59],v[98:99],v[130:131]
v_pk_fma_f32 v[52:53],v[132:133],v[48:49],v[50:51]
v_pk_fma_f32 v[54:55],v[134:135],v[48:49],v[50:51]
v_pk_fma_f32 v[56:57],v[136:137],v[48:49],v[50:51]
v_pk_fma_f32 v[58:59],v[138:139],v[48:49],v[50:51]
v_pk_fma_f32 v[132:133],v[52:53],v[100:101],v[132:133]
v_pk_fma_f32 v[134:135],v[54:55],v[102:103],v[134:135]
v_pk_fma_f32 v[136:137],v[56:57],v[104:105],v[136:137]
v_pk_fma_f32 v[138:139],v[58:59],v[106:107],v[138:139]
v_pk_fma_f32 v[52:53],v[140:141],v[48:49],v[50:51]
v_pk_fma_f32 v[54:55],v[142:143],v[48:49],v[50:51]
v_pk_fma_f32 v[56:57],v[144:145],v[48:49],v[50:51]
v_pk_fma_f32 v[58:59],v[146:147],v[48:49],v[50:51]
v_pk_fma_f32 v[140:141],v[52:53],v[108:109],v[140:141]
v_pk_fma_f32 v[142:143],v[54:55],v[110:111],v[142:143]
v_pk_fma_f32 v[144:145],v[56:57],v[112:113],v[144:145]
v_pk_fma_f32 v[146:147],v[58:59],v[114:115],v[146:147]
v_cvt_pk_bf16_f32 v148,v116,v117
v_cvt_pk_bf16_f32 v149,v118,v119
v_cvt_pk_bf16_f32 v150,v120,v121
v_cvt_pk_bf16_f32 v151,v122,v123
v_cvt_pk_bf16_f32 v152,v124,v125
v_cvt_pk_bf16_f32 v153,v126,v127
v_mfma_f32_16x16x32_bf16 v[36:39],v[32:35],v[148:151],0
v_cvt_pk_bf16_f32 v154,v128,v129
v_cvt_pk_bf16_f32 v155,v130,v131
v_mfma_f32_16x16x32_bf16 v[40:43],v[148:151],v[148:151],0
v_cvt_pk_bf16_f32 v156,v132,v133
v_cvt_pk_bf16_f32 v157,v134,v135
M 36,v[32:35],152
v_cvt_pk_bf16_f32 v158,v136,v137
v_cvt_pk_bf16_f32 v159,v138,v139
M 40,v[152:155],152
v_cvt_pk_bf16_f32 v160,v140,v141
v_cvt_pk_bf16_f32 v161,v142,v143
M 36,v[32:35],156
v_cvt_pk_bf16_f32 v162,v144,v145
v_cvt_pk_bf16_f32 v163,v146,v147
M 40,v[156:159],156
buffer_store_dwordx4 v[148:151],v1,s[36:39],s57 offen offset:0 sc0 sc1
M 36,v[32:35],160
buffer_store_dwordx4 v[152:155],v1,s[36:39],s57 offen offset:2048 sc0 sc1
M 40,v[160:163],160
buffer_store_dwordx4 v[156:159],v1,s[36:39],s58 offen offset:0 sc0 sc1
s_nop 4
v_add_f32_dpp v44,v36,v36 row_ror:8 row_mask:0xf bank_mask:0xf
s_nop 0
v_cndmask_b32 v45,v40,v41,s[70:71]
v_cndmask_b32 v46,v42,v43,s[70:71]
ds_write_b32 v2,v44 offset:1024
v_cndmask_b32 v45,v45,v46,s[72:73]
ds_write_b32 v0,v45 offset:1024
buffer_store_dwordx4 v[160:163],v1,s[36:39],s58 offen offset:2048 sc0 sc1
s_waitcnt vmcnt(13) lgkmcnt(0)
s_barrier
R 52,3,1024
R 56,3,1040
R 60,3,1056
L 0
v_pk_add_f32 v[52:53],v[52:53],v[54:55]
v_pk_add_f32 v[56:57],v[56:57],v[58:59]
v_pk_add_f32 v[60:61],v[60:61],v[62:63]
v_add_f32 v52,v52,v53
v_pk_add_f32 v[56:57],v[56:57],v[60:61]
v_mul_f32 v52,s68,v52
v_add_f32 v53,v56,v57
v_mul_f32 v53,s68,v53
v_fma_f32 v53,-v52,v52,v53
v_max_f32 v53,0,v53
v_add_f32 v54,s69,v53
v_fma_f32 v53,s69,v54,v53
v_rsq_f32 v55,v54
v_rsq_f32 v53,v53
v_mul_f32_e64 v54,-v52,v55
v_mul_f32 v53,s76,v53
buffer_store_dwordx4 v[52:55],v4,s[36:39],s59 offen offset:0 sc0 sc1
s_mov_b32 s63,s60
s_cmp_lt_u32 s45,s80
s_cbranch_scc1 Lr_bg0_%=
s_swappc_b64 s[88:89],s[94:95]
s_add_u32 s94,s94,256
s_addc_u32 s95,s95,0
s_branch Lr_bh0_%=
Lr_bg0_%=:
buffer_store_dword v17,v5,s[36:39],s81 offen offset:0 sc0 sc1
buffer_store_dword v17,v5,s[36:39],s81 offen offset:0 sc0 sc1
buffer_store_dword v17,v5,s[36:39],s81 offen offset:0 sc0 sc1
buffer_store_dword v17,v5,s[36:39],s81 offen offset:0 sc0 sc1
Lr_bh0_%=:
s_cmp_lt_u32 s45,s47
s_cbranch_scc0 Lr_mid_%=
Lr_a1_%=:
s_cmp_lg_u32 s67,0
s_cbranch_scc1 Lr_b1_%=
s_waitcnt vmcnt(14)
v_mfma_f32_16x16x32_bf16 v[36:39],v[32:35],v[196:199],0
s_add_u32 s79,s45,s46
s_cmp_lt_u32 s45,64
s_cselect_b32 s78,s49,0
v_mfma_f32_16x16x32_bf16 v[40:43],v[196:199],v[196:199],0
s_cmp_ge_u32 s79,64
s_cselect_b32 s78,s78,0
s_add_u32 s62,s79,s78
M 36,v[32:35],200
s_lshr_b32 s78,s45,1
s_and_b32 s79,s45,1
s_lshl_b32 s79,s79,7
M 40,v[200:203],200
s_lshl_b32 s57,s78,15
s_lshl_b32 s55,s62,14
s_cmp_lt_u32 s62,s47
M 36,v[32:35],204
s_cselect_b32 s55,s55,0x7fff0000
s_add_u32 s57,s57,s79
s_lshl_b32 s59,s78,9
M 40,v[204:207],204
buffer_load_dwordx4 v[164:167],v8,s[84:87],s55 offen offset:0 nt
M 36,v[32:35],208
buffer_load_dwordx4 v[168:171],v8,s[84:87],s55 offen offset:128 nt
M 40,v[208:211],208
buffer_load_dwordx4 v[172:175],v8,s[84:87],s55 offen offset:256 nt
v_mfma_f32_16x16x32_bf16 v[116:119],v[26:29],v[196:199],0
buffer_load_dwordx4 v[176:179],v8,s[84:87],s55 offen offset:384 nt
v_mfma_f32_16x16x32_bf16 v[120:123],v[24:27],v[196:199],0
s_add_u32 s57,s57,8388608
s_add_u32 s59,s59,s79
s_lshl_b32 s60,s45,9
v_mfma_f32_16x16x32_bf16 v[124:127],v[26:29],v[200:203],0
s_add_u32 s58,s57,4096
s_add_u32 s59,s59,106954752
s_add_u32 s60,s60,s66
v_mfma_f32_16x16x32_bf16 v[128:131],v[24:27],v[200:203],0
v_add_f32_dpp v44,v36,v36 row_ror:8 row_mask:0xf bank_mask:0xf
v_cndmask_b32 v45,v40,v41,s[70:71]
v_mfma_f32_16x16x32_bf16 v[132:135],v[26:29],v[204:207],0
ds_write_b32 v2,v44 offset:0
v_mfma_f32_16x16x32_bf16 v[136:139],v[24:27],v[204:207],0
v_cndmask_b32 v46,v42,v43,s[70:71]
v_cndmask_b32 v45,v45,v46,s[72:73]
v_mfma_f32_16x16x32_bf16 v[140:143],v[26:29],v[208:211],0
ds_write_b32 v0,v45 offset:0
v_mfma_f32_16x16x32_bf16 v[144:147],v[24:27],v[208:211],0
L 0
s_barrier
R 52,3,0
R 56,3,16
)"
R"(R 60,3,32
s_waitcnt vmcnt(8)
buffer_store_dword v17,v5,s[36:39],s63 offen offset:0 sc0 sc1
s_mov_b32 s63,0x7fff0000
s_cmp_lt_u32 s45,64
s_cselect_b32 s64,-192,0
s_add_u32 s64,s64,256
s_cmp_lt_u32 s45,64
s_cselect_b32 s61,0,32768
s_add_u32 s61,s61,111177728
s_mul_i32 s56,s45,16384
s_add_u32 s56,s56,98566144
Lr_bpy1_%=:
buffer_load_dword v18,v7,s[36:39],s61 offen offset:0 sc1
s_add_u32 s82,s82,1
s_waitcnt vmcnt(0)
v_cmp_eq_u32 vcc,s53,v18
s_nop 0
s_cmp_eq_u64 vcc,-1
s_cbranch_scc1 Lr_bqy1_%=
s_sleep 2
s_cmp_lt_u32 s82,20000
s_cbranch_scc1 Lr_bpy1_%=
Lr_bqy1_%=:
s_add_u32 m0,s65,16384
s_nop 0
D 9,56,0
D 9,56,1024
D 9,56,2048
D 9,56,3072
s_waitcnt vmcnt(0)
T v[68:69],10,16384
T v[70:71],10,16448
T v[72:73],10,17408
T v[74:75],10,17472
T v[76:77],10,18432
T v[78:79],10,18496
T v[80:81],10,19456
T v[82:83],10,19520
s_mov_b32 s45,s62
s_min_u32 s78,s64,s47
s_mul_i32 s56,s45,16384
s_add_u32 s56,s56,98566144
s_cmp_lt_u32 s45,s78
s_cselect_b32 s67,1,0
s_cselect_b32 s56,s56,0x7fff0000
s_add_u32 m0,s65,0
s_nop 0
D 9,56,0
D 9,56,1024
D 9,56,2048
D 9,56,3072
L 4
v_lshlrev_b32 v84,16,v68
v_and_b32 v85,0xffff0000,v68
v_lshlrev_b32 v86,16,v69
v_and_b32 v87,0xffff0000,v69
v_lshlrev_b32 v88,16,v70
v_and_b32 v89,0xffff0000,v70
v_lshlrev_b32 v90,16,v71
v_and_b32 v91,0xffff0000,v71
v_lshlrev_b32 v92,16,v72
v_and_b32 v93,0xffff0000,v72
v_lshlrev_b32 v94,16,v73
v_and_b32 v95,0xffff0000,v73
v_lshlrev_b32 v96,16,v74
v_and_b32 v97,0xffff0000,v74
v_lshlrev_b32 v98,16,v75
v_and_b32 v99,0xffff0000,v75
L 0
v_lshlrev_b32 v100,16,v76
v_and_b32 v101,0xffff0000,v76
v_lshlrev_b32 v102,16,v77
v_and_b32 v103,0xffff0000,v77
v_lshlrev_b32 v104,16,v78
v_and_b32 v105,0xffff0000,v78
v_lshlrev_b32 v106,16,v79
v_and_b32 v107,0xffff0000,v79
v_lshlrev_b32 v108,16,v80
v_and_b32 v109,0xffff0000,v80
v_lshlrev_b32 v110,16,v81
v_and_b32 v111,0xffff0000,v81
v_lshlrev_b32 v112,16,v82
v_and_b32 v113,0xffff0000,v82
v_lshlrev_b32 v114,16,v83
v_and_b32 v115,0xffff0000,v83
s_branch Lr_c1_%=
Lr_b1_%=:
s_waitcnt vmcnt(10)
T v[68:69],10,16384
T v[70:71],10,16448
T v[72:73],10,17408
T v[74:75],10,17472
T v[76:77],10,18432
T v[78:79],10,18496
T v[80:81],10,19456
T v[82:83],10,19520
v_mfma_f32_16x16x32_bf16 v[36:39],v[32:35],v[196:199],0
s_add_u32 s79,s45,s46
s_cmp_lt_u32 s45,64
s_cselect_b32 s78,s49,0
v_mfma_f32_16x16x32_bf16 v[40:43],v[196:199],v[196:199],0
s_cmp_ge_u32 s79,64
s_cselect_b32 s78,s78,0
s_add_u32 s62,s79,s78
M 36,v[32:35],200
s_lshr_b32 s78,s45,1
s_and_b32 s79,s45,1
s_lshl_b32 s60,s45,9
M 40,v[200:203],200
s_lshl_b32 s57,s78,15
s_lshl_b32 s59,s78,9
s_mov_b32 s45,s62
M 36,v[32:35],204
s_lshl_b32 s79,s79,7
s_min_u32 s78,s64,s47
s_mul_i32 s56,s45,16384
M 40,v[204:207],204
s_lshl_b32 s55,s62,14
s_cmp_lt_u32 s62,s47
s_cselect_b32 s55,s55,0x7fff0000
M 36,v[32:35],208
buffer_load_dwordx4 v[164:167],v8,s[84:87],s55 offen offset:0 nt
M 40,v[208:211],208
buffer_load_dwordx4 v[168:171],v8,s[84:87],s55 offen offset:128 nt
v_mfma_f32_16x16x32_bf16 v[116:119],v[26:29],v[196:199],0
buffer_load_dwordx4 v[172:175],v8,s[84:87],s55 offen offset:256 nt
v_mfma_f32_16x16x32_bf16 v[120:123],v[24:27],v[196:199],0
buffer_load_dwordx4 v[176:179],v8,s[84:87],s55 offen offset:384 nt
v_mfma_f32_16x16x32_bf16 v[124:127],v[26:29],v[200:203],0
s_add_u32 s57,s57,s79
s_add_u32 s56,s56,98566144
s_cmp_lt_u32 s45,s78
v_mfma_f32_16x16x32_bf16 v[128:131],v[24:27],v[200:203],0
s_cselect_b32 s56,s56,0x7fff0000
v_add_f32_dpp v44,v36,v36 row_ror:8 row_mask:0xf bank_mask:0xf
v_cndmask_b32 v45,v40,v41,s[70:71]
v_mfma_f32_16x16x32_bf16 v[132:135],v[26:29],v[204:207],0
s_cselect_b32 s67,1,0
ds_write_b32 v2,v44 offset:0
v_mfma_f32_16x16x32_bf16 v[136:139],v[24:27],v[204:207],0
s_add_u32 s57,s57,8388608
v_cndmask_b32 v46,v42,v43,s[70:71]
v_cndmask_b32 v45,v45,v46,s[72:73]
v_mfma_f32_16x16x32_bf16 v[140:143],v[26:29],v[208:211],0
s_add_u32 s59,s59,s79
ds_write_b32 v0,v45 offset:0
v_mfma_f32_16x16x32_bf16 v[144:147],v[24:27],v[208:211],0
L 2
s_add_u32 m0,s65,0
s_add_u32 s58,s57,4096
v_mfma_f32_16x16x32_bf16 v[84:87],v[26:29],v[68:71],0
D 9,56,0
v_mfma_f32_16x16x32_bf16 v[88:91],v[24:27],v[68:71],0
D 9,56,1024
v_mfma_f32_16x16x32_bf16 v[92:95],v[26:29],v[72:75],0
L 0
D 9,56,2048
v_mfma_f32_16x16x32_bf16 v[96:99],v[24:27],v[72:75],0
s_barrier
R 52,3,0
R 56,3,16
v_mfma_f32_16x16x32_bf16 v[100:103],v[26:29],v[76:79],0
s_add_u32 s59,s59,106954752
s_add_u32 s60,s60,s66
R 60,3,32
v_mfma_f32_16x16x32_bf16 v[104:107],v[24:27],v[76:79],0
D 9,56,3072
v_mfma_f32_16x16x32_bf16 v[108:111],v[26:29],v[80:83],0
v_mfma_f32_16x16x32_bf16 v[112:115],v[24:27],v[80:83],0
Lr_c1_%=:
L 0
v_pk_add_f32 v[52:53],v[52:53],v[54:55]
v_pk_add_f32 v[56:57],v[56:57],v[58:59]
v_pk_add_f32 v[60:61],v[60:61],v[62:63]
v_add_f32 v52,v52,v53
v_pk_add_f32 v[56:57],v[56:57],v[60:61]
v_mul_f32 v52,s68,v52
v_add_f32 v53,v56,v57
v_mul_f32 v53,s68,v53
v_fma_f32 v53,-v52,v52,v53
v_max_f32 v53,0,v53
s_waitcnt vmcnt(12)
buffer_store_dword v17,v5,s[36:39],s63 offen offset:0 sc0 sc1
s_mov_b32 s63,0x7fff0000
v_add_f32 v53,s69,v53
v_rsq_f32 v48,v53
s_nop 0
v_mul_f32_e64 v50,-v52,v48
v_mov_b32 v49,v48
v_mov_b32 v51,v50
v_pk_fma_f32 v[52:53],v[116:117],v[48:49],v[50:51]
v_pk_fma_f32 v[54:55],v[118:119],v[48:49],v[50:51]
v_pk_fma_f32 v[56:57],v[120:121],v[48:49],v[50:51]
v_pk_fma_f32 v[58:59],v[122:123],v[48:49],v[50:51]
v_pk_fma_f32 v[116:117],v[52:53],v[84:85],v[116:117]
v_pk_fma_f32 v[118:119],v[54:55],v[86:87],v[118:119]
v_pk_fma_f32 v[120:121],v[56:57],v[88:89],v[120:121]
v_pk_fma_f32 v[122:123],v[58:59],v[90:91],v[122:123]
v_pk_fma_f32 v[52:53],v[124:125],v[48:49],v[50:51]
v_pk_fma_f32 v[54:55],v[126:127],v[48:49],v[50:51]
v_pk_fma_f32 v[56:57],v[128:129],v[48:49],v[50:51]
v_pk_fma_f32 v[58:59],v[130:131],v[48:49],v[50:51]
v_pk_fma_f32 v[124:125],v[52:53],v[92:93],v[124:125]
v_pk_fma_f32 v[126:127],v[54:55],v[94:95],v[126:127]
v_pk_fma_f32 v[128:129],v[56:57],v[96:97],v[128:129]
v_pk_fma_f32 v[130:131],v[58:59],v[98:99],v[130:131]
v_pk_fma_f32 v[52:53],v[132:133],v[48:49],v[50:51]
v_pk_fma_f32 v[54:55],v[134:135],v[48:49],v[50:51]
v_pk_fma_f32 v[56:57],v[136:137],v[48:49],v[50:51]
v_pk_fma_f32 v[58:59],v[138:139],v[48:49],v[50:51]
v_pk_fma_f32 v[132:133],v[52:53],v[100:101],v[132:133]
v_pk_fma_f32 v[134:135],v[54:55],v[102:103],v[134:135]
v_pk_fma_f32 v[136:137],v[56:57],v[104:105],v[136:137]
v_pk_fma_f32 v[138:139],v[58:59],v[106:107],v[138:139]
v_pk_fma_f32 v[52:53],v[140:141],v[48:49],v[50:51]
v_pk_fma_f32 v[54:55],v[142:143],v[48:49],v[50:51]
v_pk_fma_f32 v[56:57],v[144:145],v[48:49],v[50:51]
v_pk_fma_f32 v[58:59],v[146:147],v[48:49],v[50:51]
v_pk_fma_f32 v[140:141],v[52:53],v[108:109],v[140:141]
v_pk_fma_f32 v[142:143],v[54:55],v[110:111],v[142:143]
v_pk_fma_f32 v[144:145],v[56:57],v[112:113],v[144:145]
v_pk_fma_f32 v[146:147],v[58:59],v[114:115],v[146:147]
v_cvt_pk_bf16_f32 v148,v116,v117
v_cvt_pk_bf16_f32 v149,v118,v119
v_cvt_pk_bf16_f32 v150,v120,v121
v_cvt_pk_bf16_f32 v151,v122,v123
v_cvt_pk_bf16_f32 v152,v124,v125
v_cvt_pk_bf16_f32 v153,v126,v127
v_mfma_f32_16x16x32_bf16 v[36:39],v[32:35],v[148:151],0
v_cvt_pk_bf16_f32 v154,v128,v129
v_cvt_pk_bf16_f32 v155,v130,v131
v_mfma_f32_16x16x32_bf16 v[40:43],v[148:151],v[148:151],0
v_cvt_pk_bf16_f32 v156,v132,v133
v_cvt_pk_bf16_f32 v157,v134,v135
M 36,v[32:35],152
v_cvt_pk_bf16_f32 v158,v136,v137
v_cvt_pk_bf16_f32 v159,v138,v139
M 40,v[152:155],152
v_cvt_pk_bf16_f32 v160,v140,v141
v_cvt_pk_bf16_f32 v161,v142,v143
M 36,v[32:35],156
v_cvt_pk_bf16_f32 v162,v144,v145
v_cvt_pk_bf16_f32 v163,v146,v147
M 40,v[156:159],156
buffer_store_dwordx4 v[148:151],v1,s[36:39],s57 offen offset:0 sc0 sc1
M 36,v[32:35],160
buffer_store_dwordx4 v[152:155],v1,s[36:39],s57 offen offset:2048 sc0 sc1
M 40,v[160:163],160
buffer_store_dwordx4 v[156:159],v1,s[36:39],s58 offen offset:0 sc0 sc1
s_nop 4
v_add_f32_dpp v44,v36,v36 row_ror:8 row_mask:0xf bank_mask:0xf
s_nop 0
v_cndmask_b32 v45,v40,v41,s[70:71]
v_cndmask_b32 v46,v42,v43,s[70:71]
ds_write_b32 v2,v44 offset:1024
v_cndmask_b32 v45,v45,v46,s[72:73]
ds_write_b32 v0,v45 offset:1024
buffer_store_dwordx4 v[160:163],v1,s[36:39],s58 offen offset:2048 sc0 sc1
s_waitcnt vmcnt(13) lgkmcnt(0)
s_barrier
R 52,3,1024
R 56,3,1040
R 60,3,1056
L 0
v_pk_add_f32 v[52:53],v[52:53],v[54:55]
v_pk_add_f32 v[56:57],v[56:57],v[58:59]
v_pk_add_f32 v[60:61],v[60:61],v[62:63]
v_add_f32 v52,v52,v53
v_pk_add_f32 v[56:57],v[56:57],v[60:61]
v_mul_f32 v52,s68,v52
v_add_f32 v53,v56,v57
v_mul_f32 v53,s68,v53
v_fma_f32 v53,-v52,v52,v53
v_max_f32 v53,0,v53
v_add_f32 v54,s69,v53
v_fma_f32 v53,s69,v54,v53
v_rsq_f32 v55,v54
v_rsq_f32 v53,v53
v_mul_f32_e64 v54,-v52,v55
v_mul_f32 v53,s76,v53
buffer_store_dwordx4 v[52:55],v4,s[36:39],s59 offen offset:0 sc0 sc1
s_mov_b32 s63,s60
s_cmp_lt_u32 s45,s80
s_cbranch_scc1 Lr_bg1_%=
s_swappc_b64 s[88:89],s[94:95]
s_add_u32 s94,s94,256
s_addc_u32 s95,s95,0
s_branch Lr_bh1_%=
Lr_bg1_%=:
buffer_store_dword v17,v5,s[36:39],s81 offen offset:0 sc0 sc1
buffer_store_dword v17,v5,s[36:39],s81 offen offset:0 sc0 sc1
buffer_store_dword v17,v5,s[36:39],s81 offen offset:0 sc0 sc1
buffer_store_dword v17,v5,s[36:39],s81 offen offset:0 sc0 sc1
Lr_bh1_%=:
s_cmp_lt_u32 s45,s47
s_cbranch_scc1 Lr_a0_%=
Lr_mid_%=:
s_waitcnt vmcnt(4)
buffer_store_dword v17,v5,s[36:39],s63 offen offset:0 sc0 sc1
s_mov_b32 s63,0x7fff0000
s_mov_b32 s45,s93
s_cmp_eq_u32 s93,0
s_cbranch_scc1 Lr_h0_%=
s_mov_b64 s[40:41],%24
s_mov_b32 s42,7340032
s_mov_b32 s43,0x00020000
s_lshl_b32 s57,s44,11
s_add_u32 s55,s50,1441792
s_add_u32 m0,s57,106496
s_nop 0
buffer_load_dwordx4 v13,s[40:43],s55 offen offset:0  lds
s_add_u32 m0,s57,107520
s_nop 0
buffer_load_dwordx4 v14,s[40:43],s55 offen offset:0  lds
s_add_u32 m0,s57,114560
s_nop 0
buffer_load_dwordx4 v13,s[40:43],s55 offen offset:128  lds
s_add_u32 m0,s57,115584
s_nop 0
buffer_load_dwordx4 v14,s[40:43],s55 offen offset:128  lds
s_add_u32 s55,s50,1572864
s_add_u32 m0,s57,122880
s_nop 0
buffer_load_dwordx4 v13,s[40:43],s55 offen offset:0  lds
s_add_u32 m0,s57,123904
s_nop 0
buffer_load_dwordx4 v14,s[40:43],s55 offen offset:0  lds
s_add_u32 m0,s57,130944
s_nop 0
buffer_load_dwordx4 v13,s[40:43],s55 offen offset:128  lds
s_add_u32 m0,s57,131968
s_nop 0
buffer_load_dwordx4 v14,s[40:43],s55 offen offset:128  lds
s_add_u32 s55,s50,1703936
s_add_u32 m0,s57,139264
s_nop 0
buffer_load_dwordx4 v13,s[40:43],s55 offen offset:0  lds
s_add_u32 m0,s57,140288
s_nop 0
buffer_load_dwordx4 v14,s[40:43],s55 offen offset:0  lds
s_add_u32 m0,s57,147328
s_nop 0
buffer_load_dwordx4 v13,s[40:43],s55 offen offset:128  lds
s_add_u32 m0,s57,148352
s_nop 0
buffer_load_dwordx4 v14,s[40:43],s55 offen offset:128  lds
s_add_u32 s55,s50,1835008
s_add_u32 m0,s57,40960
s_nop 0
buffer_load_dwordx4 v13,s[40:43],s55 offen offset:0  lds
s_add_u32 m0,s57,41984
s_nop 0
buffer_load_dwordx4 v14,s[40:43],s55 offen offset:0  lds
s_add_u32 m0,s57,49024
s_nop 0
buffer_load_dwordx4 v13,s[40:43],s55 offen offset:128  lds
s_add_u32 m0,s57,50048
s_nop 0
buffer_load_dwordx4 v14,s[40:43],s55 offen offset:128  lds
s_add_u32 s55,s50,1966080
s_add_u32 m0,s57,57344
s_nop 0
buffer_load_dwordx4 v13,s[40:43],s55 offen offset:0  lds
s_add_u32 m0,s57,58368
s_nop 0
buffer_load_dwordx4 v14,s[40:43],s55 offen offset:0  lds
s_add_u32 m0,s57,65408
s_nop 0
buffer_load_dwordx4 v13,s[40:43],s55 offen offset:128  lds
s_add_u32 m0,s57,66432
s_nop 0
buffer_load_dwordx4 v14,s[40:43],s55 offen offset:128  lds
Lr_h0_%=:
s_lshr_b32 s78,s45,1
s_and_b32 s79,s45,1
s_lshl_b32 s79,s79,7
s_lshl_b32 s55,s78,15
s_add_u32 s55,s55,s79
s_mul_i32 s56,s45,16384
s_add_u32 s56,s56,98566144
s_lshl_b32 s61,s78,11
s_add_u32 s61,s61,110587904
s_add_u32 s78,s45,s46
s_lshr_b32 s78,s78,1
s_lshl_b32 s62,s78,11
s_add_u32 s62,s62,110587904
s_add_u32 m0,s65,0
s_nop 0
D 9,56,0
D 9,56,1024
D 9,56,2048
D 9,56,3072
Lr_bps_%=:
buffer_load_dword v18,v6,s[36:39],s61 offen offset:0 sc1
s_add_u32 s82,s82,1
s_waitcnt vmcnt(0)
v_cmp_eq_u32 vcc,s53,v18
s_nop 0
s_cmp_eq_u64 vcc,-1
s_cbranch_scc1 Lr_bqs_%=
s_sleep 2
s_cmp_lt_u32 s82,20000
s_cbranch_scc1 Lr_bps_%=
Lr_bqs_%=:
s_add_u32 s78,s55,71303168
buffer_load_dwordx4 v[164:167],v1,s[36:39],s78 offen offset:0 sc1
buffer_load_dwordx4 v[168:171],v1,s[36:39],s78 offen offset:2048 sc1
s_add_u32 s78,s55,71307264
buffer_load_dwordx4 v[172:175],v1,s[36:39],s78 offen offset:0 sc1
buffer_load_dwordx4 v[176:179],v1,s[36:39],s78 offen offset:2048 sc1
s_add_u32 s78,s55,89128960
buffer_load_dwordx4 v[180:183],v1,s[36:39],s78 offen offset:0 sc1
buffer_load_dwordx4 v[184:187],v1,s[36:39],s78 offen offset:2048 sc1
s_add_u32 s78,s55,89133056
buffer_load_dwordx4 v[188:191],v1,s[36:39],s78 offen offset:0 sc1
buffer_load_dwordx4 v[192:195],v1,s[36:39],s78 offen offset:2048 sc1
buffer_load_dword v18,v6,s[36:39],s62 offen offset:0 sc1
s_cmp_eq_u32 s93,0
s_cbranch_scc1 Lr_h2_%=
s_mov_b64 s[40:41],%24
s_mov_b32 s42,7340032
s_mov_b32 s43,0x00020000
s_lshl_b32 s57,s44,11
L 0
s_barrier
s_add_u32 s55,s50,2097152
s_add_u32 m0,s57,73728
s_nop 0
buffer_load_dwordx4 v13,s[40:43],s55 offen offset:0  lds
s_add_u32 m0,s57,74752
s_nop 0
buffer_load_dwordx4 v14,s[40:43],s55 offen offset:0  lds
s_add_u32 m0,s57,81792
s_nop 0
buffer_load_dwordx4 v13,s[40:43],s55 offen offset:128  lds
s_add_u32 m0,s57,82816
s_nop 0
buffer_load_dwordx4 v14,s[40:43],s55 offen offset:128  lds
v_add_u32 v44,90112,v15
v_add_u32 v45,90112,v16
T a[160:161],44,0
T a[162:163],44,512
T a[164:165],45,0
T a[166:167],45,512
T a[168:169],44,4096
T a[170:171],44,4608
T a[172:173],45,4096
T a[174:175],45,4608
L 0
s_barrier
s_add_u32 s55,s50,2228224
s_add_u32 m0,s57,90112
s_nop 0
buffer_load_dwordx4 v13,s[40:43],s55 offen offset:0  lds
s_add_u32 m0,s57,91136
s_nop 0
buffer_load_dwordx4 v14,s[40:43],s55 offen offset:0  lds
s_add_u32 m0,s57,98176
s_nop 0
buffer_load_dwordx4 v13,s[40:43],s55 offen offset:128  lds
s_add_u32 m0,s57,99200
s_nop 0
buffer_load_dwordx4 v14,s[40:43],s55 offen offset:128  lds
v_add_u32 v44,106496,v15
v_add_u32 v45,106496,v16
T a[176:177],44,0
T a[178:179],44,512
T a[180:181],45,0
T a[182:183],45,512
T a[184:185],44,4096
T a[186:187],44,4608
T a[188:189],45,4096
T a[190:191],45,4608
L 0
s_barrier
s_add_u32 s55,s50,2359296
s_add_u32 m0,s57,106496
s_nop 0
buffer_load_dwordx4 v13,s[40:43],s55 offen offset:0  lds
s_add_u32 m0,s57,107520
s_nop 0
buffer_load_dwordx4 v14,s[40:43],s55 offen offset:0  lds
s_add_u32 m0,s57,114560
s_nop 0
buffer_load_dwordx4 v13,s[40:43],s55 offen offset:128  lds
s_add_u32 m0,s57,115584
s_nop 0
buffer_load_dwordx4 v14,s[40:43],s55 offen offset:128  lds
v_add_u32 v44,122880,v15
v_add_u32 v45,122880,v16
T a[192:193],44,0
T a[194:195],44,512
T a[196:197],45,0
T a[198:199],45,512
T a[200:201],44,4096
T a[202:203],44,4608
T a[204:205],45,4096
T a[206:207],45,4608
L 0
)"
R"(s_barrier
s_add_u32 s55,s50,2490368
s_add_u32 m0,s57,122880
s_nop 0
buffer_load_dwordx4 v13,s[40:43],s55 offen offset:0  lds
s_add_u32 m0,s57,123904
s_nop 0
buffer_load_dwordx4 v14,s[40:43],s55 offen offset:0  lds
s_add_u32 m0,s57,130944
s_nop 0
buffer_load_dwordx4 v13,s[40:43],s55 offen offset:128  lds
s_add_u32 m0,s57,131968
s_nop 0
buffer_load_dwordx4 v14,s[40:43],s55 offen offset:128  lds
v_add_u32 v44,139264,v15
v_add_u32 v45,139264,v16
T a[208:209],44,0
T a[210:211],44,512
T a[212:213],45,0
T a[214:215],45,512
T a[216:217],44,4096
T a[218:219],44,4608
T a[220:221],45,4096
T a[222:223],45,4608
L 0
s_barrier
s_add_u32 s55,s50,2621440
s_add_u32 m0,s57,139264
s_nop 0
buffer_load_dwordx4 v13,s[40:43],s55 offen offset:0  lds
s_add_u32 m0,s57,140288
s_nop 0
buffer_load_dwordx4 v14,s[40:43],s55 offen offset:0  lds
s_add_u32 m0,s57,147328
s_nop 0
buffer_load_dwordx4 v13,s[40:43],s55 offen offset:128  lds
s_add_u32 m0,s57,148352
s_nop 0
buffer_load_dwordx4 v14,s[40:43],s55 offen offset:128  lds
v_add_u32 v44,40960,v15
v_add_u32 v45,40960,v16
T a[224:225],44,0
T a[226:227],44,512
T a[228:229],45,0
T a[230:231],45,512
T a[232:233],44,4096
T a[234:235],44,4608
T a[236:237],45,4096
T a[238:239],45,4608
L 0
s_barrier
s_add_u32 s55,s50,2752512
s_add_u32 m0,s57,40960
s_nop 0
buffer_load_dwordx4 v13,s[40:43],s55 offen offset:0  lds
s_add_u32 m0,s57,41984
s_nop 0
buffer_load_dwordx4 v14,s[40:43],s55 offen offset:0  lds
s_add_u32 m0,s57,49024
s_nop 0
buffer_load_dwordx4 v13,s[40:43],s55 offen offset:128  lds
s_add_u32 m0,s57,50048
s_nop 0
buffer_load_dwordx4 v14,s[40:43],s55 offen offset:128  lds
v_add_u32 v44,57344,v15
v_add_u32 v45,57344,v16
T a[240:241],44,0
T a[242:243],44,512
T a[244:245],45,0
T a[246:247],45,512
T a[248:249],44,4096
T a[250:251],44,4608
T a[252:253],45,4096
T a[254:255],45,4608
L 0
s_barrier
s_waitcnt vmcnt(24)
Lr_h2_%=:
s_lshr_b32 s78,s45,1
s_and_b32 s79,s45,1
s_lshl_b32 s79,s79,7
s_lshl_b32 s57,s78,15
s_add_u32 s57,s57,s79
s_add_u32 s57,s57,12582912
s_add_u32 s58,s57,4096
s_lshl_b32 s59,s78,9
s_add_u32 s59,s59,s79
s_add_u32 s59,s59,107020288
s_lshl_b32 s60,s45,9
s_add_u32 s60,s60,s66
s_add_u32 s60,s60,131072
s_add_u32 s45,s45,s46
s_lshr_b32 s78,s45,1
s_and_b32 s79,s45,1
s_lshl_b32 s79,s79,7
s_lshl_b32 s55,s78,15
s_add_u32 s55,s55,s79
s_mul_i32 s56,s45,16384
s_add_u32 s56,s56,98566144
s_lshl_b32 s61,s78,11
s_add_u32 s61,s61,110587904
s_add_u32 s78,s45,s46
s_lshr_b32 s78,s78,1
s_lshl_b32 s62,s78,11
s_add_u32 s62,s62,110587904
s_waitcnt vmcnt(0)
Lr_it0_%=:
T v[68:69],10,0
T v[70:71],10,64
T v[72:73],10,1024
T v[74:75],10,1088
T v[76:77],10,2048
T v[78:79],10,2112
T v[80:81],10,3072
T v[82:83],10,3136
s_nop 0
v_cmp_eq_u32 vcc,s53,v18
s_nop 0
s_cmp_eq_u64 vcc,-1
s_cselect_b32 s78,s45,256
s_cmp_lt_u32 s78,256
s_cbranch_scc1 Lr_ta0_%=
buffer_load_dword v18,v6,s[36:39],s61 offen offset:0 sc1
v_mfma_f32_16x16x32_bf16 v[148:151],v[164:167],v[180:183],0
s_add_u32 m0,s65,16384
M 148,v[168:171],184
D 9,56,0
M 148,v[172:175],188
D 9,56,1024
M 148,v[176:179],192
L 4
D 9,56,2048
v_mfma_f32_16x16x32_bf16 v[40:43],v[164:167],v[164:167],0
D 9,56,3072
M 40,v[168:171],168
L 0
v_lshlrev_b32 v84,16,v68
v_and_b32 v85,0xffff0000,v68
M 40,v[172:175],172
v_lshlrev_b32 v86,16,v69
v_and_b32 v87,0xffff0000,v69
M 40,v[176:179],176
v_lshlrev_b32 v88,16,v70
v_and_b32 v89,0xffff0000,v70
M 40,v[180:183],180
v_lshlrev_b32 v90,16,v71
v_and_b32 v91,0xffff0000,v71
M 40,v[184:187],184
v_lshlrev_b32 v92,16,v72
v_and_b32 v93,0xffff0000,v72
M 40,v[188:191],188
v_lshlrev_b32 v94,16,v73
v_and_b32 v95,0xffff0000,v73
M 40,v[192:195],192
v_lshlrev_b32 v96,16,v74
v_and_b32 v97,0xffff0000,v74
v_mfma_f32_16x16x32_bf16 v[36:39],v[32:35],v[164:167],0
v_lshlrev_b32 v98,16,v75
v_and_b32 v99,0xffff0000,v75
M 36,v[32:35],168
v_lshlrev_b32 v100,16,v76
v_and_b32 v101,0xffff0000,v76
M 36,v[32:35],172
v_lshlrev_b32 v102,16,v77
v_and_b32 v103,0xffff0000,v77
M 36,v[32:35],176
v_lshlrev_b32 v104,16,v78
v_and_b32 v105,0xffff0000,v78
M 36,v[32:35],180
v_fma_f32 v40,2.0,v148,v40
v_fma_f32 v41,2.0,v149,v41
M 36,v[32:35],184
v_fma_f32 v42,2.0,v150,v42
v_fma_f32 v43,2.0,v151,v43
M 36,v[32:35],188
v_cndmask_b32 v45,v40,v41,s[70:71]
v_cndmask_b32 v46,v42,v43,s[70:71]
M 36,v[32:35],192
v_cndmask_b32 v45,v45,v46,s[72:73]
v_lshlrev_b32 v106,16,v79
v_mfma_f32_16x16x32_bf16 v[116:119],v[26:29],v[164:167],0
v_and_b32 v107,0xffff0000,v79
v_lshlrev_b32 v108,16,v80
M 116,v[26:29],180
v_and_b32 v109,0xffff0000,v80
v_lshlrev_b32 v110,16,v81
v_mfma_f32_16x16x32_bf16 v[120:123],v[24:27],v[164:167],0
v_and_b32 v111,0xffff0000,v81
v_lshlrev_b32 v112,16,v82
M 120,v[24:27],180
v_and_b32 v113,0xffff0000,v82
v_lshlrev_b32 v114,16,v83
v_mfma_f32_16x16x32_bf16 v[124:127],v[26:29],v[168:171],0
v_add_f32_dpp v44,v36,v36 row_ror:8 row_mask:0xf bank_mask:0xf
v_and_b32 v115,0xffff0000,v83
M 124,v[26:29],184
ds_write_b32 v2,v44 offset:0
v_mfma_f32_16x16x32_bf16 v[128:131],v[24:27],v[168:171],0
ds_write_b32 v0,v45 offset:0
M 128,v[24:27],184
v_mfma_f32_16x16x32_bf16 v[132:135],v[26:29],v[172:175],0
M 132,v[26:29],188
L 0
v_mfma_f32_16x16x32_bf16 v[136:139],v[24:27],v[172:175],0
s_barrier
R 52,3,0
R 56,3,16
M 136,v[24:27],188
R 60,3,32
v_mfma_f32_16x16x32_bf16 v[140:143],v[26:29],v[176:179],0
M 140,v[26:29],192
v_mfma_f32_16x16x32_bf16 v[144:147],v[24:27],v[176:179],0
M 144,v[24:27],192
L 0
v_pk_add_f32 v[52:53],v[52:53],v[54:55]
v_pk_add_f32 v[56:57],v[56:57],v[58:59]
v_pk_add_f32 v[60:61],v[60:61],v[62:63]
v_add_f32 v52,v52,v53
v_pk_add_f32 v[56:57],v[56:57],v[60:61]
v_mul_f32 v52,s68,v52
v_add_f32 v53,v56,v57
v_mul_f32 v53,s68,v53
v_fma_f32 v53,-v52,v52,v53
v_max_f32 v53,0,v53
s_waitcnt vmcnt(5)
buffer_store_dword v17,v5,s[36:39],s63 offen offset:0 sc0 sc1
s_mov_b32 s63,0x7fff0000
s_waitcnt vmcnt(5)
v_cmp_eq_u32 vcc,s53,v18
s_nop 0
s_cmp_eq_u64 vcc,-1
s_cselect_b32 s78,s45,256
s_cmp_lt_u32 s78,256
s_cbranch_scc1 Lr_tb0_%=
buffer_load_dword v18,v6,s[36:39],s61 offen offset:0 sc1
v_add_f32 v53,s69,v53
v_rsq_f32 v48,v53
s_nop 0
v_mul_f32_e64 v50,-v52,v48
v_mov_b32 v49,v48
v_mov_b32 v51,v50
v_pk_fma_f32 v[52:53],v[116:117],v[48:49],v[50:51]
v_pk_fma_f32 v[54:55],v[118:119],v[48:49],v[50:51]
v_pk_fma_f32 v[56:57],v[120:121],v[48:49],v[50:51]
v_pk_fma_f32 v[58:59],v[122:123],v[48:49],v[50:51]
v_pk_fma_f32 v[116:117],v[52:53],v[84:85],v[116:117]
v_pk_fma_f32 v[118:119],v[54:55],v[86:87],v[118:119]
v_pk_fma_f32 v[120:121],v[56:57],v[88:89],v[120:121]
v_pk_fma_f32 v[122:123],v[58:59],v[90:91],v[122:123]
v_pk_fma_f32 v[52:53],v[124:125],v[48:49],v[50:51]
v_pk_fma_f32 v[54:55],v[126:127],v[48:49],v[50:51]
v_pk_fma_f32 v[56:57],v[128:129],v[48:49],v[50:51]
v_pk_fma_f32 v[58:59],v[130:131],v[48:49],v[50:51]
v_pk_fma_f32 v[124:125],v[52:53],v[92:93],v[124:125]
v_pk_fma_f32 v[126:127],v[54:55],v[94:95],v[126:127]
v_pk_fma_f32 v[128:129],v[56:57],v[96:97],v[128:129]
v_pk_fma_f32 v[130:131],v[58:59],v[98:99],v[130:131]
v_pk_fma_f32 v[52:53],v[132:133],v[48:49],v[50:51]
v_pk_fma_f32 v[54:55],v[134:135],v[48:49],v[50:51]
v_pk_fma_f32 v[56:57],v[136:137],v[48:49],v[50:51]
v_pk_fma_f32 v[58:59],v[138:139],v[48:49],v[50:51]
v_pk_fma_f32 v[132:133],v[52:53],v[100:101],v[132:133]
v_pk_fma_f32 v[134:135],v[54:55],v[102:103],v[134:135]
v_pk_fma_f32 v[136:137],v[56:57],v[104:105],v[136:137]
v_pk_fma_f32 v[138:139],v[58:59],v[106:107],v[138:139]
v_pk_fma_f32 v[52:53],v[140:141],v[48:49],v[50:51]
v_pk_fma_f32 v[54:55],v[142:143],v[48:49],v[50:51]
v_pk_fma_f32 v[56:57],v[144:145],v[48:49],v[50:51]
v_pk_fma_f32 v[58:59],v[146:147],v[48:49],v[50:51]
v_pk_fma_f32 v[140:141],v[52:53],v[108:109],v[140:141]
v_pk_fma_f32 v[142:143],v[54:55],v[110:111],v[142:143]
v_pk_fma_f32 v[144:145],v[56:57],v[112:113],v[144:145]
v_pk_fma_f32 v[146:147],v[58:59],v[114:115],v[146:147]
v_cvt_pk_bf16_f32 v148,v116,v117
v_cvt_pk_bf16_f32 v149,v118,v119
v_cvt_pk_bf16_f32 v150,v120,v121
v_cvt_pk_bf16_f32 v151,v122,v123
v_cvt_pk_bf16_f32 v152,v124,v125
v_cvt_pk_bf16_f32 v153,v126,v127
v_mfma_f32_16x16x32_bf16 v[36:39],v[32:35],v[148:151],0
v_cvt_pk_bf16_f32 v154,v128,v129
v_cvt_pk_bf16_f32 v155,v130,v131
v_mfma_f32_16x16x32_bf16 v[40:43],v[148:151],v[148:151],0
v_cvt_pk_bf16_f32 v156,v132,v133
v_cvt_pk_bf16_f32 v157,v134,v135
M 36,v[32:35],152
v_cvt_pk_bf16_f32 v158,v136,v137
v_cvt_pk_bf16_f32 v159,v138,v139
M 40,v[152:155],152
v_cvt_pk_bf16_f32 v160,v140,v141
v_cvt_pk_bf16_f32 v161,v142,v143
M 36,v[32:35],156
v_cvt_pk_bf16_f32 v162,v144,v145
v_cvt_pk_bf16_f32 v163,v146,v147
M 40,v[156:159],156
buffer_store_dwordx4 v[148:151],v1,s[36:39],s57 offen offset:0 sc0 sc1
M 36,v[32:35],160
buffer_store_dwordx4 v[152:155],v1,s[36:39],s57 offen offset:2048 sc0 sc1
M 40,v[160:163],160
buffer_store_dwordx4 v[156:159],v1,s[36:39],s58 offen offset:0 sc0 sc1
s_nop 4
v_add_f32_dpp v44,v36,v36 row_ror:8 row_mask:0xf bank_mask:0xf
s_nop 0
v_cndmask_b32 v45,v40,v41,s[70:71]
v_cndmask_b32 v46,v42,v43,s[70:71]
ds_write_b32 v2,v44 offset:1024
v_cndmask_b32 v45,v45,v46,s[72:73]
ds_write_b32 v0,v45 offset:1024
buffer_store_dwordx4 v[160:163],v1,s[36:39],s58 offen offset:2048 sc0 sc1
L 0
s_barrier
R 52,3,1024
R 56,3,1040
R 60,3,1056
L 0
v_pk_add_f32 v[52:53],v[52:53],v[54:55]
v_pk_add_f32 v[56:57],v[56:57],v[58:59]
v_pk_add_f32 v[60:61],v[60:61],v[62:63]
v_add_f32 v52,v52,v53
v_pk_add_f32 v[56:57],v[56:57],v[60:61]
v_mul_f32 v52,s68,v52
v_add_f32 v53,v56,v57
v_mul_f32 v53,s68,v53
v_fma_f32 v53,-v52,v52,v53
v_max_f32 v53,0,v53
v_add_f32 v54,s69,v53
v_fma_f32 v53,s69,v54,v53
v_rsq_f32 v55,v54
v_rsq_f32 v53,v53
v_mul_f32_e64 v54,-v52,v55
v_mul_f32 v53,s76,v53
buffer_store_dwordx4 v[52:55],v4,s[36:39],s59 offen offset:0 sc0 sc1
s_mov_b32 s63,s60
s_waitcnt vmcnt(5)
v_cmp_eq_u32 vcc,s53,v18
s_nop 0
s_cmp_eq_u64 vcc,-1
s_cselect_b32 s78,s45,256
s_cmp_lt_u32 s78,256
s_cbranch_scc1 Lr_tc0_%=
s_waitcnt vmcnt(0)
buffer_store_dword v17,v5,s[36:39],s63 offen offset:0 sc0 sc1
s_mov_b32 s63,0x7fff0000
s_cmp_ge_u32 s45,256
s_cbranch_scc1 Lr_done_%=
Lr_bp0_%=:
buffer_load_dword v18,v6,s[36:39],s61 offen offset:0 sc1
s_add_u32 s82,s82,1
s_waitcnt vmcnt(0)
v_cmp_eq_u32 vcc,s53,v18
s_nop 0
s_cmp_eq_u64 vcc,-1
s_cbranch_scc1 Lr_bq0_%=
s_sleep 2
s_cmp_lt_u32 s82,20000
s_cbranch_scc1 Lr_bp0_%=
Lr_bq0_%=:
s_add_u32 s78,s55,71303168
buffer_load_dwordx4 v[196:199],v1,s[36:39],s78 offen offset:0 sc1
buffer_load_dwordx4 v[200:203],v1,s[36:39],s78 offen offset:2048 sc1
s_add_u32 s78,s55,71307264
buffer_load_dwordx4 v[204:207],v1,s[36:39],s78 offen offset:0 sc1
buffer_load_dwordx4 v[208:211],v1,s[36:39],s78 offen offset:2048 sc1
s_add_u32 s78,s55,89128960
buffer_load_dwordx4 v[212:215],v1,s[36:39],s78 offen offset:0 sc1
buffer_load_dwordx4 v[216:219],v1,s[36:39],s78 offen offset:2048 sc1
s_add_u32 s78,s55,89133056
buffer_load_dwordx4 v[220:223],v1,s[36:39],s78 offen offset:0 sc1
buffer_load_dwordx4 v[224:227],v1,s[36:39],s78 offen offset:2048 sc1
buffer_load_dword v18,v6,s[36:39],s62 offen offset:0 sc1
s_lshr_b32 s78,s45,1
s_and_b32 s79,s45,1
s_lshl_b32 s79,s79,7
s_lshl_b32 s57,s78,15
s_add_u32 s57,s57,s79
s_add_u32 s57,s57,12582912
s_add_u32 s58,s57,4096
s_lshl_b32 s59,s78,9
s_add_u32 s59,s59,s79
s_add_u32 s59,s59,107020288
s_lshl_b32 s60,s45,9
s_add_u32 s60,s60,s66
s_add_u32 s60,s60,131072
s_add_u32 s45,s45,s46
s_lshr_b32 s78,s45,1
s_and_b32 s79,s45,1
s_lshl_b32 s79,s79,7
s_lshl_b32 s55,s78,15
s_add_u32 s55,s55,s79
s_mul_i32 s56,s45,16384
s_add_u32 s56,s56,98566144
s_lshl_b32 s61,s78,11
s_add_u32 s61,s61,110587904
s_add_u32 s78,s45,s46
s_lshr_b32 s78,s78,1
s_lshl_b32 s62,s78,11
s_add_u32 s62,s62,110587904
s_waitcnt vmcnt(0)
s_branch Lr_it1_%=
Lr_ta0_%=:
v_mfma_f32_16x16x32_bf16 v[148:151],v[164:167],v[180:183],0
s_add_u32 s78,s55,71303168
s_add_u32 m0,s65,16384
M 148,v[168:171],184
buffer_load_dwordx4 v[196:199],v1,s[36:39],s78 offen offset:0 sc1
M 148,v[172:175],188
buffer_load_dwordx4 v[200:203],v1,s[36:39],s78 offen offset:2048 sc1
M 148,v[176:179],192
L 4
D 9,56,0
v_mfma_f32_16x16x32_bf16 v[40:43],v[164:167],v[164:167],0
D 9,56,1024
M 40,v[168:171],168
L 0
D 9,56,2048
M 40,v[172:175],172
D 9,56,3072
M 40,v[176:179],176
s_add_u32 s78,s55,71307264
v_lshlrev_b32 v84,16,v68
v_and_b32 v85,0xffff0000,v68
M 40,v[180:183],180
buffer_load_dwordx4 v[204:207],v1,s[36:39],s78 offen offset:0 sc1
M 40,v[184:187],184
buffer_load_dwordx4 v[208:211],v1,s[36:39],s78 offen offset:2048 sc1
M 40,v[188:191],188
s_add_u32 s78,s55,89128960
v_lshlrev_b32 v86,16,v69
v_and_b32 v87,0xffff0000,v69
M 40,v[192:195],192
buffer_load_dwordx4 v[212:215],v1,s[36:39],s78 offen offset:0 sc1
v_mfma_f32_16x16x32_bf16 v[36:39],v[32:35],v[164:167],0
buffer_load_dwordx4 v[216:219],v1,s[36:39],s78 offen offset:2048 sc1
M 36,v[32:35],168
s_add_u32 s78,s55,89133056
v_lshlrev_b32 v88,16,v70
v_and_b32 v89,0xffff0000,v70
M 36,v[32:35],172
buffer_load_dwordx4 v[220:223],v1,s[36:39],s78 offen offset:0 sc1
M 36,v[32:35],176
buffer_load_dwordx4 v[224:227],v1,s[36:39],s78 offen offset:2048 sc1
M 36,v[32:35],180
v_fma_f32 v40,2.0,v148,v40
v_fma_f32 v41,2.0,v149,v41
M 36,v[32:35],184
v_fma_f32 v42,2.0,v150,v42
v_fma_f32 v43,2.0,v151,v43
M 36,v[32:35],188
v_cndmask_b32 v45,v40,v41,s[70:71]
v_cndmask_b32 v46,v42,v43,s[70:71]
M 36,v[32:35],192
v_cndmask_b32 v45,v45,v46,s[72:73]
v_lshlrev_b32 v90,16,v71
v_mfma_f32_16x16x32_bf16 v[116:119],v[26:29],v[164:167],0
v_and_b32 v91,0xffff0000,v71
v_lshlrev_b32 v92,16,v72
M 116,v[26:29],180
v_and_b32 v93,0xffff0000,v72
v_lshlrev_b32 v94,16,v73
v_mfma_f32_16x16x32_bf16 v[120:123],v[24:27],v[164:167],0
v_and_b32 v95,0xffff0000,v73
v_lshlrev_b32 v96,16,v74
M 120,v[24:27],180
v_and_b32 v97,0xffff0000,v74
v_lshlrev_b32 v98,16,v75
v_mfma_f32_16x16x32_bf16 v[124:127],v[26:29],v[168:171],0
v_add_f32_dpp v44,v36,v36 row_ror:8 row_mask:0xf bank_mask:0xf
v_and_b32 v99,0xffff0000,v75
M 124,v[26:29],184
ds_write_b32 v2,v44 offset:0
v_mfma_f32_16x16x32_bf16 v[128:131],v[24:27],v[168:171],0
ds_write_b32 v0,v45 offset:0
M 128,v[24:27],184
v_lshlrev_b32 v100,16,v76
v_and_b32 v101,0xffff0000,v76
v_mfma_f32_16x16x32_bf16 v[132:135],v[26:29],v[172:175],0
v_lshlrev_b32 v102,16,v77
v_and_b32 v103,0xffff0000,v77
M 132,v[26:29],188
L 0
v_lshlrev_b32 v104,16,v78
v_and_b32 v105,0xffff0000,v78
v_mfma_f32_16x16x32_bf16 v[136:139],v[24:27],v[172:175],0
s_barrier
R 52,3,0
R 56,3,16
M 136,v[24:27],188
R 60,3,32
v_lshlrev_b32 v106,16,v79
v_mfma_f32_16x16x32_bf16 v[140:143],v[26:29],v[176:179],0
v_and_b32 v107,0xffff0000,v79
v_lshlrev_b32 v108,16,v80
M 140,v[26:29],192
v_and_b32 v109,0xffff0000,v80
v_lshlrev_b32 v110,16,v81
v_mfma_f32_16x16x32_bf16 v[144:147],v[24:27],v[176:179],0
v_and_b32 v111,0xffff0000,v81
v_lshlrev_b32 v112,16,v82
M 144,v[24:27],192
v_and_b32 v113,0xffff0000,v82
)"
R"(v_lshlrev_b32 v114,16,v83
v_and_b32 v115,0xffff0000,v83
L 0
v_pk_add_f32 v[52:53],v[52:53],v[54:55]
v_pk_add_f32 v[56:57],v[56:57],v[58:59]
v_pk_add_f32 v[60:61],v[60:61],v[62:63]
v_add_f32 v52,v52,v53
v_pk_add_f32 v[56:57],v[56:57],v[60:61]
v_mul_f32 v52,s68,v52
v_add_f32 v53,v56,v57
v_mul_f32 v53,s68,v53
v_fma_f32 v53,-v52,v52,v53
v_max_f32 v53,0,v53
buffer_load_dword v18,v6,s[36:39],s62 offen offset:0 sc1
v_add_f32 v53,s69,v53
v_rsq_f32 v48,v53
s_nop 0
v_mul_f32_e64 v50,-v52,v48
v_mov_b32 v49,v48
v_mov_b32 v51,v50
v_pk_fma_f32 v[52:53],v[116:117],v[48:49],v[50:51]
v_pk_fma_f32 v[54:55],v[118:119],v[48:49],v[50:51]
v_pk_fma_f32 v[56:57],v[120:121],v[48:49],v[50:51]
v_pk_fma_f32 v[58:59],v[122:123],v[48:49],v[50:51]
v_pk_fma_f32 v[116:117],v[52:53],v[84:85],v[116:117]
v_pk_fma_f32 v[118:119],v[54:55],v[86:87],v[118:119]
v_pk_fma_f32 v[120:121],v[56:57],v[88:89],v[120:121]
v_pk_fma_f32 v[122:123],v[58:59],v[90:91],v[122:123]
v_pk_fma_f32 v[52:53],v[124:125],v[48:49],v[50:51]
v_pk_fma_f32 v[54:55],v[126:127],v[48:49],v[50:51]
v_pk_fma_f32 v[56:57],v[128:129],v[48:49],v[50:51]
v_pk_fma_f32 v[58:59],v[130:131],v[48:49],v[50:51]
v_pk_fma_f32 v[124:125],v[52:53],v[92:93],v[124:125]
v_pk_fma_f32 v[126:127],v[54:55],v[94:95],v[126:127]
v_pk_fma_f32 v[128:129],v[56:57],v[96:97],v[128:129]
v_pk_fma_f32 v[130:131],v[58:59],v[98:99],v[130:131]
v_pk_fma_f32 v[52:53],v[132:133],v[48:49],v[50:51]
v_pk_fma_f32 v[54:55],v[134:135],v[48:49],v[50:51]
v_pk_fma_f32 v[56:57],v[136:137],v[48:49],v[50:51]
v_pk_fma_f32 v[58:59],v[138:139],v[48:49],v[50:51]
v_pk_fma_f32 v[132:133],v[52:53],v[100:101],v[132:133]
v_pk_fma_f32 v[134:135],v[54:55],v[102:103],v[134:135]
v_pk_fma_f32 v[136:137],v[56:57],v[104:105],v[136:137]
v_pk_fma_f32 v[138:139],v[58:59],v[106:107],v[138:139]
v_pk_fma_f32 v[52:53],v[140:141],v[48:49],v[50:51]
v_pk_fma_f32 v[54:55],v[142:143],v[48:49],v[50:51]
v_pk_fma_f32 v[56:57],v[144:145],v[48:49],v[50:51]
v_pk_fma_f32 v[58:59],v[146:147],v[48:49],v[50:51]
v_pk_fma_f32 v[140:141],v[52:53],v[108:109],v[140:141]
v_pk_fma_f32 v[142:143],v[54:55],v[110:111],v[142:143]
v_pk_fma_f32 v[144:145],v[56:57],v[112:113],v[144:145]
v_pk_fma_f32 v[146:147],v[58:59],v[114:115],v[146:147]
v_cvt_pk_bf16_f32 v148,v116,v117
v_cvt_pk_bf16_f32 v149,v118,v119
v_cvt_pk_bf16_f32 v150,v120,v121
v_cvt_pk_bf16_f32 v151,v122,v123
v_cvt_pk_bf16_f32 v152,v124,v125
v_cvt_pk_bf16_f32 v153,v126,v127
s_waitcnt vmcnt(13)
buffer_store_dword v17,v5,s[36:39],s63 offen offset:0 sc0 sc1
s_mov_b32 s63,0x7fff0000
v_mfma_f32_16x16x32_bf16 v[36:39],v[32:35],v[148:151],0
v_cvt_pk_bf16_f32 v154,v128,v129
v_cvt_pk_bf16_f32 v155,v130,v131
v_mfma_f32_16x16x32_bf16 v[40:43],v[148:151],v[148:151],0
v_cvt_pk_bf16_f32 v156,v132,v133
v_cvt_pk_bf16_f32 v157,v134,v135
M 36,v[32:35],152
v_cvt_pk_bf16_f32 v158,v136,v137
v_cvt_pk_bf16_f32 v159,v138,v139
M 40,v[152:155],152
v_cvt_pk_bf16_f32 v160,v140,v141
v_cvt_pk_bf16_f32 v161,v142,v143
M 36,v[32:35],156
v_cvt_pk_bf16_f32 v162,v144,v145
v_cvt_pk_bf16_f32 v163,v146,v147
M 40,v[156:159],156
buffer_store_dwordx4 v[148:151],v1,s[36:39],s57 offen offset:0 sc0 sc1
M 36,v[32:35],160
buffer_store_dwordx4 v[152:155],v1,s[36:39],s57 offen offset:2048 sc0 sc1
M 40,v[160:163],160
buffer_store_dwordx4 v[156:159],v1,s[36:39],s58 offen offset:0 sc0 sc1
s_nop 4
v_add_f32_dpp v44,v36,v36 row_ror:8 row_mask:0xf bank_mask:0xf
s_nop 0
v_cndmask_b32 v45,v40,v41,s[70:71]
v_cndmask_b32 v46,v42,v43,s[70:71]
ds_write_b32 v2,v44 offset:1024
v_cndmask_b32 v45,v45,v46,s[72:73]
ds_write_b32 v0,v45 offset:1024
buffer_store_dwordx4 v[160:163],v1,s[36:39],s58 offen offset:2048 sc0 sc1
L 0
s_barrier
R 52,3,1024
R 56,3,1040
R 60,3,1056
L 0
v_pk_add_f32 v[52:53],v[52:53],v[54:55]
v_pk_add_f32 v[56:57],v[56:57],v[58:59]
v_pk_add_f32 v[60:61],v[60:61],v[62:63]
v_add_f32 v52,v52,v53
v_pk_add_f32 v[56:57],v[56:57],v[60:61]
v_mul_f32 v52,s68,v52
v_add_f32 v53,v56,v57
v_mul_f32 v53,s68,v53
v_fma_f32 v53,-v52,v52,v53
v_max_f32 v53,0,v53
v_add_f32 v54,s69,v53
v_fma_f32 v53,s69,v54,v53
v_rsq_f32 v55,v54
v_rsq_f32 v53,v53
v_mul_f32_e64 v54,-v52,v55
v_mul_f32 v53,s76,v53
buffer_store_dwordx4 v[52:55],v4,s[36:39],s59 offen offset:0 sc0 sc1
s_mov_b32 s63,s60
s_lshr_b32 s78,s45,1
s_and_b32 s79,s45,1
s_lshl_b32 s79,s79,7
s_lshl_b32 s57,s78,15
s_add_u32 s57,s57,s79
s_add_u32 s57,s57,12582912
s_add_u32 s58,s57,4096
s_lshl_b32 s59,s78,9
s_add_u32 s59,s59,s79
s_add_u32 s59,s59,107020288
s_lshl_b32 s60,s45,9
s_add_u32 s60,s60,s66
s_add_u32 s60,s60,131072
s_add_u32 s45,s45,s46
s_lshr_b32 s78,s45,1
s_and_b32 s79,s45,1
s_lshl_b32 s79,s79,7
s_lshl_b32 s55,s78,15
s_add_u32 s55,s55,s79
s_mul_i32 s56,s45,16384
s_add_u32 s56,s56,98566144
s_lshl_b32 s61,s78,11
s_add_u32 s61,s61,110587904
s_add_u32 s78,s45,s46
s_lshr_b32 s78,s78,1
s_lshl_b32 s62,s78,11
s_add_u32 s62,s62,110587904
s_waitcnt vmcnt(6)
s_branch Lr_it1_%=
Lr_tb0_%=:
v_add_f32 v53,s69,v53
v_rsq_f32 v48,v53
s_nop 0
v_mul_f32_e64 v50,-v52,v48
v_mov_b32 v49,v48
v_mov_b32 v51,v50
v_pk_fma_f32 v[52:53],v[116:117],v[48:49],v[50:51]
v_pk_fma_f32 v[54:55],v[118:119],v[48:49],v[50:51]
v_pk_fma_f32 v[56:57],v[120:121],v[48:49],v[50:51]
v_pk_fma_f32 v[58:59],v[122:123],v[48:49],v[50:51]
s_add_u32 s78,s55,71303168
buffer_load_dwordx4 v[196:199],v1,s[36:39],s78 offen offset:0 sc1
buffer_load_dwordx4 v[200:203],v1,s[36:39],s78 offen offset:2048 sc1
s_add_u32 s78,s55,71307264
buffer_load_dwordx4 v[204:207],v1,s[36:39],s78 offen offset:0 sc1
buffer_load_dwordx4 v[208:211],v1,s[36:39],s78 offen offset:2048 sc1
v_pk_fma_f32 v[116:117],v[52:53],v[84:85],v[116:117]
v_pk_fma_f32 v[118:119],v[54:55],v[86:87],v[118:119]
v_pk_fma_f32 v[120:121],v[56:57],v[88:89],v[120:121]
v_pk_fma_f32 v[122:123],v[58:59],v[90:91],v[122:123]
v_pk_fma_f32 v[52:53],v[124:125],v[48:49],v[50:51]
v_pk_fma_f32 v[54:55],v[126:127],v[48:49],v[50:51]
v_pk_fma_f32 v[56:57],v[128:129],v[48:49],v[50:51]
v_pk_fma_f32 v[58:59],v[130:131],v[48:49],v[50:51]
s_add_u32 s78,s55,89128960
buffer_load_dwordx4 v[212:215],v1,s[36:39],s78 offen offset:0 sc1
buffer_load_dwordx4 v[216:219],v1,s[36:39],s78 offen offset:2048 sc1
s_add_u32 s78,s55,89133056
buffer_load_dwordx4 v[220:223],v1,s[36:39],s78 offen offset:0 sc1
buffer_load_dwordx4 v[224:227],v1,s[36:39],s78 offen offset:2048 sc1
v_pk_fma_f32 v[124:125],v[52:53],v[92:93],v[124:125]
v_pk_fma_f32 v[126:127],v[54:55],v[94:95],v[126:127]
v_pk_fma_f32 v[128:129],v[56:57],v[96:97],v[128:129]
v_pk_fma_f32 v[130:131],v[58:59],v[98:99],v[130:131]
v_pk_fma_f32 v[52:53],v[132:133],v[48:49],v[50:51]
v_pk_fma_f32 v[54:55],v[134:135],v[48:49],v[50:51]
v_pk_fma_f32 v[56:57],v[136:137],v[48:49],v[50:51]
v_pk_fma_f32 v[58:59],v[138:139],v[48:49],v[50:51]
v_pk_fma_f32 v[132:133],v[52:53],v[100:101],v[132:133]
v_pk_fma_f32 v[134:135],v[54:55],v[102:103],v[134:135]
v_pk_fma_f32 v[136:137],v[56:57],v[104:105],v[136:137]
v_pk_fma_f32 v[138:139],v[58:59],v[106:107],v[138:139]
v_pk_fma_f32 v[52:53],v[140:141],v[48:49],v[50:51]
v_pk_fma_f32 v[54:55],v[142:143],v[48:49],v[50:51]
v_pk_fma_f32 v[56:57],v[144:145],v[48:49],v[50:51]
v_pk_fma_f32 v[58:59],v[146:147],v[48:49],v[50:51]
v_pk_fma_f32 v[140:141],v[52:53],v[108:109],v[140:141]
v_pk_fma_f32 v[142:143],v[54:55],v[110:111],v[142:143]
v_pk_fma_f32 v[144:145],v[56:57],v[112:113],v[144:145]
v_pk_fma_f32 v[146:147],v[58:59],v[114:115],v[146:147]
buffer_load_dword v18,v6,s[36:39],s62 offen offset:0 sc1
v_cvt_pk_bf16_f32 v148,v116,v117
v_cvt_pk_bf16_f32 v149,v118,v119
v_cvt_pk_bf16_f32 v150,v120,v121
v_cvt_pk_bf16_f32 v151,v122,v123
v_cvt_pk_bf16_f32 v152,v124,v125
v_cvt_pk_bf16_f32 v153,v126,v127
v_mfma_f32_16x16x32_bf16 v[36:39],v[32:35],v[148:151],0
v_cvt_pk_bf16_f32 v154,v128,v129
v_cvt_pk_bf16_f32 v155,v130,v131
v_mfma_f32_16x16x32_bf16 v[40:43],v[148:151],v[148:151],0
v_cvt_pk_bf16_f32 v156,v132,v133
v_cvt_pk_bf16_f32 v157,v134,v135
M 36,v[32:35],152
v_cvt_pk_bf16_f32 v158,v136,v137
v_cvt_pk_bf16_f32 v159,v138,v139
M 40,v[152:155],152
v_cvt_pk_bf16_f32 v160,v140,v141
v_cvt_pk_bf16_f32 v161,v142,v143
M 36,v[32:35],156
v_cvt_pk_bf16_f32 v162,v144,v145
v_cvt_pk_bf16_f32 v163,v146,v147
M 40,v[156:159],156
buffer_store_dwordx4 v[148:151],v1,s[36:39],s57 offen offset:0 sc0 sc1
M 36,v[32:35],160
buffer_store_dwordx4 v[152:155],v1,s[36:39],s57 offen offset:2048 sc0 sc1
M 40,v[160:163],160
buffer_store_dwordx4 v[156:159],v1,s[36:39],s58 offen offset:0 sc0 sc1
s_nop 4
v_add_f32_dpp v44,v36,v36 row_ror:8 row_mask:0xf bank_mask:0xf
s_nop 0
v_cndmask_b32 v45,v40,v41,s[70:71]
v_cndmask_b32 v46,v42,v43,s[70:71]
ds_write_b32 v2,v44 offset:1024
v_cndmask_b32 v45,v45,v46,s[72:73]
ds_write_b32 v0,v45 offset:1024
buffer_store_dwordx4 v[160:163],v1,s[36:39],s58 offen offset:2048 sc0 sc1
L 0
s_barrier
R 52,3,1024
R 56,3,1040
R 60,3,1056
L 0
v_pk_add_f32 v[52:53],v[52:53],v[54:55]
v_pk_add_f32 v[56:57],v[56:57],v[58:59]
v_pk_add_f32 v[60:61],v[60:61],v[62:63]
v_add_f32 v52,v52,v53
v_pk_add_f32 v[56:57],v[56:57],v[60:61]
v_mul_f32 v52,s68,v52
v_add_f32 v53,v56,v57
v_mul_f32 v53,s68,v53
v_fma_f32 v53,-v52,v52,v53
v_max_f32 v53,0,v53
v_add_f32 v54,s69,v53
v_fma_f32 v53,s69,v54,v53
v_rsq_f32 v55,v54
v_rsq_f32 v53,v53
v_mul_f32_e64 v54,-v52,v55
v_mul_f32 v53,s76,v53
buffer_store_dwordx4 v[52:55],v4,s[36:39],s59 offen offset:0 sc0 sc1
s_mov_b32 s63,s60
s_lshr_b32 s78,s45,1
s_and_b32 s79,s45,1
s_lshl_b32 s79,s79,7
s_lshl_b32 s57,s78,15
s_add_u32 s57,s57,s79
s_add_u32 s57,s57,12582912
s_add_u32 s58,s57,4096
s_lshl_b32 s59,s78,9
s_add_u32 s59,s59,s79
s_add_u32 s59,s59,107020288
s_lshl_b32 s60,s45,9
s_add_u32 s60,s60,s66
s_add_u32 s60,s60,131072
s_add_u32 s45,s45,s46
s_lshr_b32 s78,s45,1
s_and_b32 s79,s45,1
s_lshl_b32 s79,s79,7
s_lshl_b32 s55,s78,15
s_add_u32 s55,s55,s79
s_mul_i32 s56,s45,16384
s_add_u32 s56,s56,98566144
s_lshl_b32 s61,s78,11
s_add_u32 s61,s61,110587904
s_add_u32 s78,s45,s46
s_lshr_b32 s78,s78,1
s_lshl_b32 s62,s78,11
s_add_u32 s62,s62,110587904
s_waitcnt vmcnt(5)
s_branch Lr_it1_%=
Lr_tc0_%=:
s_add_u32 s78,s55,71303168
buffer_load_dwordx4 v[196:199],v1,s[36:39],s78 offen offset:0 sc1
buffer_load_dwordx4 v[200:203],v1,s[36:39],s78 offen offset:2048 sc1
s_add_u32 s78,s55,71307264
buffer_load_dwordx4 v[204:207],v1,s[36:39],s78 offen offset:0 sc1
buffer_load_dwordx4 v[208:211],v1,s[36:39],s78 offen offset:2048 sc1
s_add_u32 s78,s55,89128960
buffer_load_dwordx4 v[212:215],v1,s[36:39],s78 offen offset:0 sc1
buffer_load_dwordx4 v[216:219],v1,s[36:39],s78 offen offset:2048 sc1
s_add_u32 s78,s55,89133056
buffer_load_dwordx4 v[220:223],v1,s[36:39],s78 offen offset:0 sc1
buffer_load_dwordx4 v[224:227],v1,s[36:39],s78 offen offset:2048 sc1
buffer_load_dword v18,v6,s[36:39],s62 offen offset:0 sc1
s_waitcnt vmcnt(9)
buffer_store_dword v17,v5,s[36:39],s63 offen offset:0 sc0 sc1
s_mov_b32 s63,0x7fff0000
s_lshr_b32 s78,s45,1
s_and_b32 s79,s45,1
s_lshl_b32 s79,s79,7
s_lshl_b32 s57,s78,15
s_add_u32 s57,s57,s79
s_add_u32 s57,s57,12582912
s_add_u32 s58,s57,4096
s_lshl_b32 s59,s78,9
s_add_u32 s59,s59,s79
s_add_u32 s59,s59,107020288
s_lshl_b32 s60,s45,9
s_add_u32 s60,s60,s66
s_add_u32 s60,s60,131072
s_add_u32 s45,s45,s46
s_lshr_b32 s78,s45,1
s_and_b32 s79,s45,1
s_lshl_b32 s79,s79,7
s_lshl_b32 s55,s78,15
s_add_u32 s55,s55,s79
s_mul_i32 s56,s45,16384
s_add_u32 s56,s56,98566144
s_lshl_b32 s61,s78,11
s_add_u32 s61,s61,110587904
s_add_u32 s78,s45,s46
s_lshr_b32 s78,s78,1
s_lshl_b32 s62,s78,11
s_add_u32 s62,s62,110587904
s_waitcnt vmcnt(1)
s_branch Lr_it1_%=
Lr_it1_%=:
T v[68:69],10,16384
T v[70:71],10,16448
T v[72:73],10,17408
T v[74:75],10,17472
T v[76:77],10,18432
T v[78:79],10,18496
T v[80:81],10,19456
T v[82:83],10,19520
s_nop 0
v_cmp_eq_u32 vcc,s53,v18
s_nop 0
s_cmp_eq_u64 vcc,-1
s_cselect_b32 s78,s45,256
s_cmp_lt_u32 s78,256
s_cbranch_scc1 Lr_ta1_%=
buffer_load_dword v18,v6,s[36:39],s61 offen offset:0 sc1
v_mfma_f32_16x16x32_bf16 v[148:151],v[196:199],v[212:215],0
s_add_u32 m0,s65,0
M 148,v[200:203],216
D 9,56,0
M 148,v[204:207],220
D 9,56,1024
M 148,v[208:211],224
L 4
D 9,56,2048
v_mfma_f32_16x16x32_bf16 v[40:43],v[196:199],v[196:199],0
D 9,56,3072
M 40,v[200:203],200
L 0
v_lshlrev_b32 v84,16,v68
v_and_b32 v85,0xffff0000,v68
M 40,v[204:207],204
v_lshlrev_b32 v86,16,v69
v_and_b32 v87,0xffff0000,v69
M 40,v[208:211],208
v_lshlrev_b32 v88,16,v70
v_and_b32 v89,0xffff0000,v70
M 40,v[212:215],212
v_lshlrev_b32 v90,16,v71
v_and_b32 v91,0xffff0000,v71
M 40,v[216:219],216
v_lshlrev_b32 v92,16,v72
v_and_b32 v93,0xffff0000,v72
M 40,v[220:223],220
v_lshlrev_b32 v94,16,v73
v_and_b32 v95,0xffff0000,v73
M 40,v[224:227],224
v_lshlrev_b32 v96,16,v74
v_and_b32 v97,0xffff0000,v74
v_mfma_f32_16x16x32_bf16 v[36:39],v[32:35],v[196:199],0
v_lshlrev_b32 v98,16,v75
v_and_b32 v99,0xffff0000,v75
M 36,v[32:35],200
v_lshlrev_b32 v100,16,v76
v_and_b32 v101,0xffff0000,v76
M 36,v[32:35],204
v_lshlrev_b32 v102,16,v77
v_and_b32 v103,0xffff0000,v77
M 36,v[32:35],208
v_lshlrev_b32 v104,16,v78
v_and_b32 v105,0xffff0000,v78
M 36,v[32:35],212
v_fma_f32 v40,2.0,v148,v40
v_fma_f32 v41,2.0,v149,v41
M 36,v[32:35],216
v_fma_f32 v42,2.0,v150,v42
v_fma_f32 v43,2.0,v151,v43
M 36,v[32:35],220
v_cndmask_b32 v45,v40,v41,s[70:71]
v_cndmask_b32 v46,v42,v43,s[70:71]
M 36,v[32:35],224
v_cndmask_b32 v45,v45,v46,s[72:73]
v_lshlrev_b32 v106,16,v79
v_mfma_f32_16x16x32_bf16 v[116:119],v[26:29],v[196:199],0
v_and_b32 v107,0xffff0000,v79
v_lshlrev_b32 v108,16,v80
M 116,v[26:29],212
v_and_b32 v109,0xffff0000,v80
v_lshlrev_b32 v110,16,v81
v_mfma_f32_16x16x32_bf16 v[120:123],v[24:27],v[196:199],0
v_and_b32 v111,0xffff0000,v81
v_lshlrev_b32 v112,16,v82
M 120,v[24:27],212
v_and_b32 v113,0xffff0000,v82
v_lshlrev_b32 v114,16,v83
v_mfma_f32_16x16x32_bf16 v[124:127],v[26:29],v[200:203],0
v_add_f32_dpp v44,v36,v36 row_ror:8 row_mask:0xf bank_mask:0xf
v_and_b32 v115,0xffff0000,v83
M 124,v[26:29],216
ds_write_b32 v2,v44 offset:0
v_mfma_f32_16x16x32_bf16 v[128:131],v[24:27],v[200:203],0
ds_write_b32 v0,v45 offset:0
M 128,v[24:27],216
v_mfma_f32_16x16x32_bf16 v[132:135],v[26:29],v[204:207],0
M 132,v[26:29],220
L 0
v_mfma_f32_16x16x32_bf16 v[136:139],v[24:27],v[204:207],0
s_barrier
R 52,3,0
R 56,3,16
M 136,v[24:27],220
R 60,3,32
v_mfma_f32_16x16x32_bf16 v[140:143],v[26:29],v[208:211],0
M 140,v[26:29],224
v_mfma_f32_16x16x32_bf16 v[144:147],v[24:27],v[208:211],0
M 144,v[24:27],224
L 0
v_pk_add_f32 v[52:53],v[52:53],v[54:55]
v_pk_add_f32 v[56:57],v[56:57],v[58:59]
v_pk_add_f32 v[60:61],v[60:61],v[62:63]
v_add_f32 v52,v52,v53
v_pk_add_f32 v[56:57],v[56:57],v[60:61]
v_mul_f32 v52,s68,v52
v_add_f32 v53,v56,v57
v_mul_f32 v53,s68,v53
v_fma_f32 v53,-v52,v52,v53
v_max_f32 v53,0,v53
s_waitcnt vmcnt(5)
buffer_store_dword v17,v5,s[36:39],s63 offen offset:0 sc0 sc1
s_mov_b32 s63,0x7fff0000
s_waitcnt vmcnt(5)
v_cmp_eq_u32 vcc,s53,v18
s_nop 0
s_cmp_eq_u64 vcc,-1
s_cselect_b32 s78,s45,256
s_cmp_lt_u32 s78,256
s_cbranch_scc1 Lr_tb1_%=
buffer_load_dword v18,v6,s[36:39],s61 offen offset:0 sc1
v_add_f32 v53,s69,v53
v_rsq_f32 v48,v53
s_nop 0
v_mul_f32_e64 v50,-v52,v48
v_mov_b32 v49,v48
v_mov_b32 v51,v50
v_pk_fma_f32 v[52:53],v[116:117],v[48:49],v[50:51]
v_pk_fma_f32 v[54:55],v[118:119],v[48:49],v[50:51]
v_pk_fma_f32 v[56:57],v[120:121],v[48:49],v[50:51]
v_pk_fma_f32 v[58:59],v[122:123],v[48:49],v[50:51]
v_pk_fma_f32 v[116:117],v[52:53],v[84:85],v[116:117]
v_pk_fma_f32 v[118:119],v[54:55],v[86:87],v[118:119]
v_pk_fma_f32 v[120:121],v[56:57],v[88:89],v[120:121]
v_pk_fma_f32 v[122:123],v[58:59],v[90:91],v[122:123]
v_pk_fma_f32 v[52:53],v[124:125],v[48:49],v[50:51]
v_pk_fma_f32 v[54:55],v[126:127],v[48:49],v[50:51]
v_pk_fma_f32 v[56:57],v[128:129],v[48:49],v[50:51]
v_pk_fma_f32 v[58:59],v[130:131],v[48:49],v[50:51]
v_pk_fma_f32 v[124:125],v[52:53],v[92:93],v[124:125]
v_pk_fma_f32 v[126:127],v[54:55],v[94:95],v[126:127]
v_pk_fma_f32 v[128:129],v[56:57],v[96:97],v[128:129]
v_pk_fma_f32 v[130:131],v[58:59],v[98:99],v[130:131]
v_pk_fma_f32 v[52:53],v[132:133],v[48:49],v[50:51]
v_pk_fma_f32 v[54:55],v[134:135],v[48:49],v[50:51]
v_pk_fma_f32 v[56:57],v[136:137],v[48:49],v[50:51]
v_pk_fma_f32 v[58:59],v[138:139],v[48:49],v[50:51]
v_pk_fma_f32 v[132:133],v[52:53],v[100:101],v[132:133]
v_pk_fma_f32 v[134:135],v[54:55],v[102:103],v[134:135]
v_pk_fma_f32 v[136:137],v[56:57],v[104:105],v[136:137]
v_pk_fma_f32 v[138:139],v[58:59],v[106:107],v[138:139]
v_pk_fma_f32 v[52:53],v[140:141],v[48:49],v[50:51]
v_pk_fma_f32 v[54:55],v[142:143],v[48:49],v[50:51]
v_pk_fma_f32 v[56:57],v[144:145],v[48:49],v[50:51]
v_pk_fma_f32 v[58:59],v[146:147],v[48:49],v[50:51]
v_pk_fma_f32 v[140:141],v[52:53],v[108:109],v[140:141]
v_pk_fma_f32 v[142:143],v[54:55],v[110:111],v[142:143]
v_pk_fma_f32 v[144:145],v[56:57],v[112:113],v[144:145]
v_pk_fma_f32 v[146:147],v[58:59],v[114:115],v[146:147]
v_cvt_pk_bf16_f32 v148,v116,v117
v_cvt_pk_bf16_f32 v149,v118,v119
)"
R"(v_cvt_pk_bf16_f32 v150,v120,v121
v_cvt_pk_bf16_f32 v151,v122,v123
v_cvt_pk_bf16_f32 v152,v124,v125
v_cvt_pk_bf16_f32 v153,v126,v127
v_mfma_f32_16x16x32_bf16 v[36:39],v[32:35],v[148:151],0
v_cvt_pk_bf16_f32 v154,v128,v129
v_cvt_pk_bf16_f32 v155,v130,v131
v_mfma_f32_16x16x32_bf16 v[40:43],v[148:151],v[148:151],0
v_cvt_pk_bf16_f32 v156,v132,v133
v_cvt_pk_bf16_f32 v157,v134,v135
M 36,v[32:35],152
v_cvt_pk_bf16_f32 v158,v136,v137
v_cvt_pk_bf16_f32 v159,v138,v139
M 40,v[152:155],152
v_cvt_pk_bf16_f32 v160,v140,v141
v_cvt_pk_bf16_f32 v161,v142,v143
M 36,v[32:35],156
v_cvt_pk_bf16_f32 v162,v144,v145
v_cvt_pk_bf16_f32 v163,v146,v147
M 40,v[156:159],156
buffer_store_dwordx4 v[148:151],v1,s[36:39],s57 offen offset:0 sc0 sc1
M 36,v[32:35],160
buffer_store_dwordx4 v[152:155],v1,s[36:39],s57 offen offset:2048 sc0 sc1
M 40,v[160:163],160
buffer_store_dwordx4 v[156:159],v1,s[36:39],s58 offen offset:0 sc0 sc1
s_nop 4
v_add_f32_dpp v44,v36,v36 row_ror:8 row_mask:0xf bank_mask:0xf
s_nop 0
v_cndmask_b32 v45,v40,v41,s[70:71]
v_cndmask_b32 v46,v42,v43,s[70:71]
ds_write_b32 v2,v44 offset:1024
v_cndmask_b32 v45,v45,v46,s[72:73]
ds_write_b32 v0,v45 offset:1024
buffer_store_dwordx4 v[160:163],v1,s[36:39],s58 offen offset:2048 sc0 sc1
L 0
s_barrier
R 52,3,1024
R 56,3,1040
R 60,3,1056
L 0
v_pk_add_f32 v[52:53],v[52:53],v[54:55]
v_pk_add_f32 v[56:57],v[56:57],v[58:59]
v_pk_add_f32 v[60:61],v[60:61],v[62:63]
v_add_f32 v52,v52,v53
v_pk_add_f32 v[56:57],v[56:57],v[60:61]
v_mul_f32 v52,s68,v52
v_add_f32 v53,v56,v57
v_mul_f32 v53,s68,v53
v_fma_f32 v53,-v52,v52,v53
v_max_f32 v53,0,v53
v_add_f32 v54,s69,v53
v_fma_f32 v53,s69,v54,v53
v_rsq_f32 v55,v54
v_rsq_f32 v53,v53
v_mul_f32_e64 v54,-v52,v55
v_mul_f32 v53,s76,v53
buffer_store_dwordx4 v[52:55],v4,s[36:39],s59 offen offset:0 sc0 sc1
s_mov_b32 s63,s60
s_waitcnt vmcnt(5)
v_cmp_eq_u32 vcc,s53,v18
s_nop 0
s_cmp_eq_u64 vcc,-1
s_cselect_b32 s78,s45,256
s_cmp_lt_u32 s78,256
s_cbranch_scc1 Lr_tc1_%=
s_waitcnt vmcnt(0)
buffer_store_dword v17,v5,s[36:39],s63 offen offset:0 sc0 sc1
s_mov_b32 s63,0x7fff0000
s_cmp_ge_u32 s45,256
s_cbranch_scc1 Lr_done_%=
Lr_bp1_%=:
buffer_load_dword v18,v6,s[36:39],s61 offen offset:0 sc1
s_add_u32 s82,s82,1
s_waitcnt vmcnt(0)
v_cmp_eq_u32 vcc,s53,v18
s_nop 0
s_cmp_eq_u64 vcc,-1
s_cbranch_scc1 Lr_bq1_%=
s_sleep 2
s_cmp_lt_u32 s82,20000
s_cbranch_scc1 Lr_bp1_%=
Lr_bq1_%=:
s_add_u32 s78,s55,71303168
buffer_load_dwordx4 v[164:167],v1,s[36:39],s78 offen offset:0 sc1
buffer_load_dwordx4 v[168:171],v1,s[36:39],s78 offen offset:2048 sc1
s_add_u32 s78,s55,71307264
buffer_load_dwordx4 v[172:175],v1,s[36:39],s78 offen offset:0 sc1
buffer_load_dwordx4 v[176:179],v1,s[36:39],s78 offen offset:2048 sc1
s_add_u32 s78,s55,89128960
buffer_load_dwordx4 v[180:183],v1,s[36:39],s78 offen offset:0 sc1
buffer_load_dwordx4 v[184:187],v1,s[36:39],s78 offen offset:2048 sc1
s_add_u32 s78,s55,89133056
buffer_load_dwordx4 v[188:191],v1,s[36:39],s78 offen offset:0 sc1
buffer_load_dwordx4 v[192:195],v1,s[36:39],s78 offen offset:2048 sc1
buffer_load_dword v18,v6,s[36:39],s62 offen offset:0 sc1
s_lshr_b32 s78,s45,1
s_and_b32 s79,s45,1
s_lshl_b32 s79,s79,7
s_lshl_b32 s57,s78,15
s_add_u32 s57,s57,s79
s_add_u32 s57,s57,12582912
s_add_u32 s58,s57,4096
s_lshl_b32 s59,s78,9
s_add_u32 s59,s59,s79
s_add_u32 s59,s59,107020288
s_lshl_b32 s60,s45,9
s_add_u32 s60,s60,s66
s_add_u32 s60,s60,131072
s_add_u32 s45,s45,s46
s_lshr_b32 s78,s45,1
s_and_b32 s79,s45,1
s_lshl_b32 s79,s79,7
s_lshl_b32 s55,s78,15
s_add_u32 s55,s55,s79
s_mul_i32 s56,s45,16384
s_add_u32 s56,s56,98566144
s_lshl_b32 s61,s78,11
s_add_u32 s61,s61,110587904
s_add_u32 s78,s45,s46
s_lshr_b32 s78,s78,1
s_lshl_b32 s62,s78,11
s_add_u32 s62,s62,110587904
s_waitcnt vmcnt(0)
s_branch Lr_it0_%=
Lr_ta1_%=:
v_mfma_f32_16x16x32_bf16 v[148:151],v[196:199],v[212:215],0
s_add_u32 s78,s55,71303168
s_add_u32 m0,s65,0
M 148,v[200:203],216
buffer_load_dwordx4 v[164:167],v1,s[36:39],s78 offen offset:0 sc1
M 148,v[204:207],220
buffer_load_dwordx4 v[168:171],v1,s[36:39],s78 offen offset:2048 sc1
M 148,v[208:211],224
L 4
D 9,56,0
v_mfma_f32_16x16x32_bf16 v[40:43],v[196:199],v[196:199],0
D 9,56,1024
M 40,v[200:203],200
L 0
D 9,56,2048
M 40,v[204:207],204
D 9,56,3072
M 40,v[208:211],208
s_add_u32 s78,s55,71307264
v_lshlrev_b32 v84,16,v68
v_and_b32 v85,0xffff0000,v68
M 40,v[212:215],212
buffer_load_dwordx4 v[172:175],v1,s[36:39],s78 offen offset:0 sc1
M 40,v[216:219],216
buffer_load_dwordx4 v[176:179],v1,s[36:39],s78 offen offset:2048 sc1
M 40,v[220:223],220
s_add_u32 s78,s55,89128960
v_lshlrev_b32 v86,16,v69
v_and_b32 v87,0xffff0000,v69
M 40,v[224:227],224
buffer_load_dwordx4 v[180:183],v1,s[36:39],s78 offen offset:0 sc1
v_mfma_f32_16x16x32_bf16 v[36:39],v[32:35],v[196:199],0
buffer_load_dwordx4 v[184:187],v1,s[36:39],s78 offen offset:2048 sc1
M 36,v[32:35],200
s_add_u32 s78,s55,89133056
v_lshlrev_b32 v88,16,v70
v_and_b32 v89,0xffff0000,v70
M 36,v[32:35],204
buffer_load_dwordx4 v[188:191],v1,s[36:39],s78 offen offset:0 sc1
M 36,v[32:35],208
buffer_load_dwordx4 v[192:195],v1,s[36:39],s78 offen offset:2048 sc1
M 36,v[32:35],212
v_fma_f32 v40,2.0,v148,v40
v_fma_f32 v41,2.0,v149,v41
M 36,v[32:35],216
v_fma_f32 v42,2.0,v150,v42
v_fma_f32 v43,2.0,v151,v43
M 36,v[32:35],220
v_cndmask_b32 v45,v40,v41,s[70:71]
v_cndmask_b32 v46,v42,v43,s[70:71]
M 36,v[32:35],224
v_cndmask_b32 v45,v45,v46,s[72:73]
v_lshlrev_b32 v90,16,v71
v_mfma_f32_16x16x32_bf16 v[116:119],v[26:29],v[196:199],0
v_and_b32 v91,0xffff0000,v71
v_lshlrev_b32 v92,16,v72
M 116,v[26:29],212
v_and_b32 v93,0xffff0000,v72
v_lshlrev_b32 v94,16,v73
v_mfma_f32_16x16x32_bf16 v[120:123],v[24:27],v[196:199],0
v_and_b32 v95,0xffff0000,v73
v_lshlrev_b32 v96,16,v74
M 120,v[24:27],212
v_and_b32 v97,0xffff0000,v74
v_lshlrev_b32 v98,16,v75
v_mfma_f32_16x16x32_bf16 v[124:127],v[26:29],v[200:203],0
v_add_f32_dpp v44,v36,v36 row_ror:8 row_mask:0xf bank_mask:0xf
v_and_b32 v99,0xffff0000,v75
M 124,v[26:29],216
ds_write_b32 v2,v44 offset:0
v_mfma_f32_16x16x32_bf16 v[128:131],v[24:27],v[200:203],0
ds_write_b32 v0,v45 offset:0
M 128,v[24:27],216
v_lshlrev_b32 v100,16,v76
v_and_b32 v101,0xffff0000,v76
v_mfma_f32_16x16x32_bf16 v[132:135],v[26:29],v[204:207],0
v_lshlrev_b32 v102,16,v77
v_and_b32 v103,0xffff0000,v77
M 132,v[26:29],220
L 0
v_lshlrev_b32 v104,16,v78
v_and_b32 v105,0xffff0000,v78
v_mfma_f32_16x16x32_bf16 v[136:139],v[24:27],v[204:207],0
s_barrier
R 52,3,0
R 56,3,16
M 136,v[24:27],220
R 60,3,32
v_lshlrev_b32 v106,16,v79
v_mfma_f32_16x16x32_bf16 v[140:143],v[26:29],v[208:211],0
v_and_b32 v107,0xffff0000,v79
v_lshlrev_b32 v108,16,v80
M 140,v[26:29],224
v_and_b32 v109,0xffff0000,v80
v_lshlrev_b32 v110,16,v81
v_mfma_f32_16x16x32_bf16 v[144:147],v[24:27],v[208:211],0
v_and_b32 v111,0xffff0000,v81
v_lshlrev_b32 v112,16,v82
M 144,v[24:27],224
v_and_b32 v113,0xffff0000,v82
v_lshlrev_b32 v114,16,v83
v_and_b32 v115,0xffff0000,v83
L 0
v_pk_add_f32 v[52:53],v[52:53],v[54:55]
v_pk_add_f32 v[56:57],v[56:57],v[58:59]
v_pk_add_f32 v[60:61],v[60:61],v[62:63]
v_add_f32 v52,v52,v53
v_pk_add_f32 v[56:57],v[56:57],v[60:61]
v_mul_f32 v52,s68,v52
v_add_f32 v53,v56,v57
v_mul_f32 v53,s68,v53
v_fma_f32 v53,-v52,v52,v53
v_max_f32 v53,0,v53
buffer_load_dword v18,v6,s[36:39],s62 offen offset:0 sc1
v_add_f32 v53,s69,v53
v_rsq_f32 v48,v53
s_nop 0
v_mul_f32_e64 v50,-v52,v48
v_mov_b32 v49,v48
v_mov_b32 v51,v50
v_pk_fma_f32 v[52:53],v[116:117],v[48:49],v[50:51]
v_pk_fma_f32 v[54:55],v[118:119],v[48:49],v[50:51]
v_pk_fma_f32 v[56:57],v[120:121],v[48:49],v[50:51]
v_pk_fma_f32 v[58:59],v[122:123],v[48:49],v[50:51]
v_pk_fma_f32 v[116:117],v[52:53],v[84:85],v[116:117]
v_pk_fma_f32 v[118:119],v[54:55],v[86:87],v[118:119]
v_pk_fma_f32 v[120:121],v[56:57],v[88:89],v[120:121]
v_pk_fma_f32 v[122:123],v[58:59],v[90:91],v[122:123]
v_pk_fma_f32 v[52:53],v[124:125],v[48:49],v[50:51]
v_pk_fma_f32 v[54:55],v[126:127],v[48:49],v[50:51]
v_pk_fma_f32 v[56:57],v[128:129],v[48:49],v[50:51]
v_pk_fma_f32 v[58:59],v[130:131],v[48:49],v[50:51]
v_pk_fma_f32 v[124:125],v[52:53],v[92:93],v[124:125]
v_pk_fma_f32 v[126:127],v[54:55],v[94:95],v[126:127]
v_pk_fma_f32 v[128:129],v[56:57],v[96:97],v[128:129]
v_pk_fma_f32 v[130:131],v[58:59],v[98:99],v[130:131]
v_pk_fma_f32 v[52:53],v[132:133],v[48:49],v[50:51]
v_pk_fma_f32 v[54:55],v[134:135],v[48:49],v[50:51]
v_pk_fma_f32 v[56:57],v[136:137],v[48:49],v[50:51]
v_pk_fma_f32 v[58:59],v[138:139],v[48:49],v[50:51]
v_pk_fma_f32 v[132:133],v[52:53],v[100:101],v[132:133]
v_pk_fma_f32 v[134:135],v[54:55],v[102:103],v[134:135]
v_pk_fma_f32 v[136:137],v[56:57],v[104:105],v[136:137]
v_pk_fma_f32 v[138:139],v[58:59],v[106:107],v[138:139]
v_pk_fma_f32 v[52:53],v[140:141],v[48:49],v[50:51]
v_pk_fma_f32 v[54:55],v[142:143],v[48:49],v[50:51]
v_pk_fma_f32 v[56:57],v[144:145],v[48:49],v[50:51]
v_pk_fma_f32 v[58:59],v[146:147],v[48:49],v[50:51]
v_pk_fma_f32 v[140:141],v[52:53],v[108:109],v[140:141]
v_pk_fma_f32 v[142:143],v[54:55],v[110:111],v[142:143]
v_pk_fma_f32 v[144:145],v[56:57],v[112:113],v[144:145]
v_pk_fma_f32 v[146:147],v[58:59],v[114:115],v[146:147]
v_cvt_pk_bf16_f32 v148,v116,v117
v_cvt_pk_bf16_f32 v149,v118,v119
v_cvt_pk_bf16_f32 v150,v120,v121
v_cvt_pk_bf16_f32 v151,v122,v123
v_cvt_pk_bf16_f32 v152,v124,v125
v_cvt_pk_bf16_f32 v153,v126,v127
s_waitcnt vmcnt(13)
buffer_store_dword v17,v5,s[36:39],s63 offen offset:0 sc0 sc1
s_mov_b32 s63,0x7fff0000
v_mfma_f32_16x16x32_bf16 v[36:39],v[32:35],v[148:151],0
v_cvt_pk_bf16_f32 v154,v128,v129
v_cvt_pk_bf16_f32 v155,v130,v131
v_mfma_f32_16x16x32_bf16 v[40:43],v[148:151],v[148:151],0
v_cvt_pk_bf16_f32 v156,v132,v133
v_cvt_pk_bf16_f32 v157,v134,v135
M 36,v[32:35],152
v_cvt_pk_bf16_f32 v158,v136,v137
v_cvt_pk_bf16_f32 v159,v138,v139
M 40,v[152:155],152
v_cvt_pk_bf16_f32 v160,v140,v141
v_cvt_pk_bf16_f32 v161,v142,v143
M 36,v[32:35],156
v_cvt_pk_bf16_f32 v162,v144,v145
v_cvt_pk_bf16_f32 v163,v146,v147
M 40,v[156:159],156
buffer_store_dwordx4 v[148:151],v1,s[36:39],s57 offen offset:0 sc0 sc1
M 36,v[32:35],160
buffer_store_dwordx4 v[152:155],v1,s[36:39],s57 offen offset:2048 sc0 sc1
M 40,v[160:163],160
buffer_store_dwordx4 v[156:159],v1,s[36:39],s58 offen offset:0 sc0 sc1
s_nop 4
v_add_f32_dpp v44,v36,v36 row_ror:8 row_mask:0xf bank_mask:0xf
s_nop 0
v_cndmask_b32 v45,v40,v41,s[70:71]
v_cndmask_b32 v46,v42,v43,s[70:71]
ds_write_b32 v2,v44 offset:1024
v_cndmask_b32 v45,v45,v46,s[72:73]
ds_write_b32 v0,v45 offset:1024
buffer_store_dwordx4 v[160:163],v1,s[36:39],s58 offen offset:2048 sc0 sc1
L 0
s_barrier
R 52,3,1024
R 56,3,1040
R 60,3,1056
L 0
v_pk_add_f32 v[52:53],v[52:53],v[54:55]
v_pk_add_f32 v[56:57],v[56:57],v[58:59]
v_pk_add_f32 v[60:61],v[60:61],v[62:63]
v_add_f32 v52,v52,v53
v_pk_add_f32 v[56:57],v[56:57],v[60:61]
v_mul_f32 v52,s68,v52
v_add_f32 v53,v56,v57
v_mul_f32 v53,s68,v53
v_fma_f32 v53,-v52,v52,v53
v_max_f32 v53,0,v53
v_add_f32 v54,s69,v53
v_fma_f32 v53,s69,v54,v53
v_rsq_f32 v55,v54
v_rsq_f32 v53,v53
v_mul_f32_e64 v54,-v52,v55
v_mul_f32 v53,s76,v53
buffer_store_dwordx4 v[52:55],v4,s[36:39],s59 offen offset:0 sc0 sc1
s_mov_b32 s63,s60
s_lshr_b32 s78,s45,1
s_and_b32 s79,s45,1
s_lshl_b32 s79,s79,7
s_lshl_b32 s57,s78,15
s_add_u32 s57,s57,s79
s_add_u32 s57,s57,12582912
s_add_u32 s58,s57,4096
s_lshl_b32 s59,s78,9
s_add_u32 s59,s59,s79
s_add_u32 s59,s59,107020288
s_lshl_b32 s60,s45,9
s_add_u32 s60,s60,s66
s_add_u32 s60,s60,131072
s_add_u32 s45,s45,s46
s_lshr_b32 s78,s45,1
s_and_b32 s79,s45,1
s_lshl_b32 s79,s79,7
s_lshl_b32 s55,s78,15
s_add_u32 s55,s55,s79
s_mul_i32 s56,s45,16384
s_add_u32 s56,s56,98566144
s_lshl_b32 s61,s78,11
s_add_u32 s61,s61,110587904
s_add_u32 s78,s45,s46
s_lshr_b32 s78,s78,1
s_lshl_b32 s62,s78,11
s_add_u32 s62,s62,110587904
s_waitcnt vmcnt(6)
s_branch Lr_it0_%=
Lr_tb1_%=:
v_add_f32 v53,s69,v53
v_rsq_f32 v48,v53
s_nop 0
v_mul_f32_e64 v50,-v52,v48
v_mov_b32 v49,v48
v_mov_b32 v51,v50
v_pk_fma_f32 v[52:53],v[116:117],v[48:49],v[50:51]
v_pk_fma_f32 v[54:55],v[118:119],v[48:49],v[50:51]
v_pk_fma_f32 v[56:57],v[120:121],v[48:49],v[50:51]
v_pk_fma_f32 v[58:59],v[122:123],v[48:49],v[50:51]
s_add_u32 s78,s55,71303168
buffer_load_dwordx4 v[164:167],v1,s[36:39],s78 offen offset:0 sc1
buffer_load_dwordx4 v[168:171],v1,s[36:39],s78 offen offset:2048 sc1
s_add_u32 s78,s55,71307264
buffer_load_dwordx4 v[172:175],v1,s[36:39],s78 offen offset:0 sc1
buffer_load_dwordx4 v[176:179],v1,s[36:39],s78 offen offset:2048 sc1
v_pk_fma_f32 v[116:117],v[52:53],v[84:85],v[116:117]
v_pk_fma_f32 v[118:119],v[54:55],v[86:87],v[118:119]
v_pk_fma_f32 v[120:121],v[56:57],v[88:89],v[120:121]
v_pk_fma_f32 v[122:123],v[58:59],v[90:91],v[122:123]
v_pk_fma_f32 v[52:53],v[124:125],v[48:49],v[50:51]
v_pk_fma_f32 v[54:55],v[126:127],v[48:49],v[50:51]
v_pk_fma_f32 v[56:57],v[128:129],v[48:49],v[50:51]
v_pk_fma_f32 v[58:59],v[130:131],v[48:49],v[50:51]
s_add_u32 s78,s55,89128960
buffer_load_dwordx4 v[180:183],v1,s[36:39],s78 offen offset:0 sc1
buffer_load_dwordx4 v[184:187],v1,s[36:39],s78 offen offset:2048 sc1
s_add_u32 s78,s55,89133056
buffer_load_dwordx4 v[188:191],v1,s[36:39],s78 offen offset:0 sc1
buffer_load_dwordx4 v[192:195],v1,s[36:39],s78 offen offset:2048 sc1
v_pk_fma_f32 v[124:125],v[52:53],v[92:93],v[124:125]
v_pk_fma_f32 v[126:127],v[54:55],v[94:95],v[126:127]
v_pk_fma_f32 v[128:129],v[56:57],v[96:97],v[128:129]
v_pk_fma_f32 v[130:131],v[58:59],v[98:99],v[130:131]
v_pk_fma_f32 v[52:53],v[132:133],v[48:49],v[50:51]
v_pk_fma_f32 v[54:55],v[134:135],v[48:49],v[50:51]
v_pk_fma_f32 v[56:57],v[136:137],v[48:49],v[50:51]
v_pk_fma_f32 v[58:59],v[138:139],v[48:49],v[50:51]
v_pk_fma_f32 v[132:133],v[52:53],v[100:101],v[132:133]
v_pk_fma_f32 v[134:135],v[54:55],v[102:103],v[134:135]
v_pk_fma_f32 v[136:137],v[56:57],v[104:105],v[136:137]
v_pk_fma_f32 v[138:139],v[58:59],v[106:107],v[138:139]
v_pk_fma_f32 v[52:53],v[140:141],v[48:49],v[50:51]
v_pk_fma_f32 v[54:55],v[142:143],v[48:49],v[50:51]
v_pk_fma_f32 v[56:57],v[144:145],v[48:49],v[50:51]
v_pk_fma_f32 v[58:59],v[146:147],v[48:49],v[50:51]
v_pk_fma_f32 v[140:141],v[52:53],v[108:109],v[140:141]
v_pk_fma_f32 v[142:143],v[54:55],v[110:111],v[142:143]
v_pk_fma_f32 v[144:145],v[56:57],v[112:113],v[144:145]
v_pk_fma_f32 v[146:147],v[58:59],v[114:115],v[146:147]
buffer_load_dword v18,v6,s[36:39],s62 offen offset:0 sc1
v_cvt_pk_bf16_f32 v148,v116,v117
v_cvt_pk_bf16_f32 v149,v118,v119
v_cvt_pk_bf16_f32 v150,v120,v121
v_cvt_pk_bf16_f32 v151,v122,v123
v_cvt_pk_bf16_f32 v152,v124,v125
v_cvt_pk_bf16_f32 v153,v126,v127
v_mfma_f32_16x16x32_bf16 v[36:39],v[32:35],v[148:151],0
v_cvt_pk_bf16_f32 v154,v128,v129
v_cvt_pk_bf16_f32 v155,v130,v131
v_mfma_f32_16x16x32_bf16 v[40:43],v[148:151],v[148:151],0
v_cvt_pk_bf16_f32 v156,v132,v133
v_cvt_pk_bf16_f32 v157,v134,v135
M 36,v[32:35],152
v_cvt_pk_bf16_f32 v158,v136,v137
v_cvt_pk_bf16_f32 v159,v138,v139
M 40,v[152:155],152
v_cvt_pk_bf16_f32 v160,v140,v141
v_cvt_pk_bf16_f32 v161,v142,v143
M 36,v[32:35],156
v_cvt_pk_bf16_f32 v162,v144,v145
v_cvt_pk_bf16_f32 v163,v146,v147
M 40,v[156:159],156
buffer_store_dwordx4 v[148:151],v1,s[36:39],s57 offen offset:0 sc0 sc1
M 36,v[32:35],160
buffer_store_dwordx4 v[152:155],v1,s[36:39],s57 offen offset:2048 sc0 sc1
M 40,v[160:163],160
buffer_store_dwordx4 v[156:159],v1,s[36:39],s58 offen offset:0 sc0 sc1
s_nop 4
v_add_f32_dpp v44,v36,v36 row_ror:8 row_mask:0xf bank_mask:0xf
s_nop 0
v_cndmask_b32 v45,v40,v41,s[70:71]
v_cndmask_b32 v46,v42,v43,s[70:71]
ds_write_b32 v2,v44 offset:1024
v_cndmask_b32 v45,v45,v46,s[72:73]
ds_write_b32 v0,v45 offset:1024
buffer_store_dwordx4 v[160:163],v1,s[36:39],s58 offen offset:2048 sc0 sc1
L 0
s_barrier
R 52,3,1024
R 56,3,1040
R 60,3,1056
L 0
v_pk_add_f32 v[52:53],v[52:53],v[54:55]
v_pk_add_f32 v[56:57],v[56:57],v[58:59]
v_pk_add_f32 v[60:61],v[60:61],v[62:63]
v_add_f32 v52,v52,v53
v_pk_add_f32 v[56:57],v[56:57],v[60:61]
v_mul_f32 v52,s68,v52
v_add_f32 v53,v56,v57
v_mul_f32 v53,s68,v53
v_fma_f32 v53,-v52,v52,v53
v_max_f32 v53,0,v53
v_add_f32 v54,s69,v53
v_fma_f32 v53,s69,v54,v53
v_rsq_f32 v55,v54
v_rsq_f32 v53,v53
v_mul_f32_e64 v54,-v52,v55
v_mul_f32 v53,s76,v53
buffer_store_dwordx4 v[52:55],v4,s[36:39],s59 offen offset:0 sc0 sc1
s_mov_b32 s63,s60
s_lshr_b32 s78,s45,1
s_and_b32 s79,s45,1
s_lshl_b32 s79,s79,7
s_lshl_b32 s57,s78,15
s_add_u32 s57,s57,s79
s_add_u32 s57,s57,12582912
s_add_u32 s58,s57,4096
s_lshl_b32 s59,s78,9
s_add_u32 s59,s59,s79
s_add_u32 s59,s59,107020288
s_lshl_b32 s60,s45,9
s_add_u32 s60,s60,s66
s_add_u32 s60,s60,131072
s_add_u32 s45,s45,s46
s_lshr_b32 s78,s45,1
s_and_b32 s79,s45,1
s_lshl_b32 s79,s79,7
s_lshl_b32 s55,s78,15
s_add_u32 s55,s55,s79
)"
R"(s_mul_i32 s56,s45,16384
s_add_u32 s56,s56,98566144
s_lshl_b32 s61,s78,11
s_add_u32 s61,s61,110587904
s_add_u32 s78,s45,s46
s_lshr_b32 s78,s78,1
s_lshl_b32 s62,s78,11
s_add_u32 s62,s62,110587904
s_waitcnt vmcnt(5)
s_branch Lr_it0_%=
Lr_tc1_%=:
s_add_u32 s78,s55,71303168
buffer_load_dwordx4 v[164:167],v1,s[36:39],s78 offen offset:0 sc1
buffer_load_dwordx4 v[168:171],v1,s[36:39],s78 offen offset:2048 sc1
s_add_u32 s78,s55,71307264
buffer_load_dwordx4 v[172:175],v1,s[36:39],s78 offen offset:0 sc1
buffer_load_dwordx4 v[176:179],v1,s[36:39],s78 offen offset:2048 sc1
s_add_u32 s78,s55,89128960
buffer_load_dwordx4 v[180:183],v1,s[36:39],s78 offen offset:0 sc1
buffer_load_dwordx4 v[184:187],v1,s[36:39],s78 offen offset:2048 sc1
s_add_u32 s78,s55,89133056
buffer_load_dwordx4 v[188:191],v1,s[36:39],s78 offen offset:0 sc1
buffer_load_dwordx4 v[192:195],v1,s[36:39],s78 offen offset:2048 sc1
buffer_load_dword v18,v6,s[36:39],s62 offen offset:0 sc1
s_waitcnt vmcnt(9)
buffer_store_dword v17,v5,s[36:39],s63 offen offset:0 sc0 sc1
s_mov_b32 s63,0x7fff0000
s_lshr_b32 s78,s45,1
s_and_b32 s79,s45,1
s_lshl_b32 s79,s79,7
s_lshl_b32 s57,s78,15
s_add_u32 s57,s57,s79
s_add_u32 s57,s57,12582912
s_add_u32 s58,s57,4096
s_lshl_b32 s59,s78,9
s_add_u32 s59,s59,s79
s_add_u32 s59,s59,107020288
s_lshl_b32 s60,s45,9
s_add_u32 s60,s60,s66
s_add_u32 s60,s60,131072
s_add_u32 s45,s45,s46
s_lshr_b32 s78,s45,1
s_and_b32 s79,s45,1
s_lshl_b32 s79,s79,7
s_lshl_b32 s55,s78,15
s_add_u32 s55,s55,s79
s_mul_i32 s56,s45,16384
s_add_u32 s56,s56,98566144
s_lshl_b32 s61,s78,11
s_add_u32 s61,s61,110587904
s_add_u32 s78,s45,s46
s_lshr_b32 s78,s78,1
s_lshl_b32 s62,s78,11
s_add_u32 s62,s62,110587904
s_waitcnt vmcnt(1)
s_branch Lr_it0_%=
Lr_done_%=:
s_waitcnt vmcnt(0) lgkmcnt(0)
s_barrier
s_branch Lr_end_%=
.p2align 8
Lr_tab_%=:
s_mov_b64 s[40:41],%24
s_mov_b32 s42,7340032
s_mov_b32 s43,0x00020000
s_add_u32 s83,s50,0
s_add_u32 m0,s54,40960
s_nop 0
buffer_load_dwordx4 v13,s[40:43],s83 offen offset:0  lds
s_add_u32 m0,s54,41984
s_nop 0
buffer_load_dwordx4 v14,s[40:43],s83 offen offset:0  lds
s_add_u32 m0,s54,49024
s_nop 0
buffer_load_dwordx4 v13,s[40:43],s83 offen offset:128  lds
s_add_u32 m0,s54,50048
s_nop 0
buffer_load_dwordx4 v14,s[40:43],s83 offen offset:128  lds
s_setpc_b64 s[88:89]
.p2align 8
s_mov_b64 s[40:41],%24
s_mov_b32 s42,7340032
s_mov_b32 s43,0x00020000
v_add_u32 v230,40960,v15
v_add_u32 v231,40960,v16
T a[0:1],230,0
T a[2:3],230,512
T a[4:5],231,0
T a[6:7],231,512
T a[8:9],230,4096
T a[10:11],230,4608
T a[12:13],231,4096
T a[14:15],231,4608
s_add_u32 s83,s50,131072
s_add_u32 m0,s54,57344
s_nop 0
buffer_load_dwordx4 v13,s[40:43],s83 offen offset:0  lds
s_add_u32 m0,s54,58368
s_nop 0
buffer_load_dwordx4 v14,s[40:43],s83 offen offset:0  lds
s_add_u32 m0,s54,65408
s_nop 0
buffer_load_dwordx4 v13,s[40:43],s83 offen offset:128  lds
s_add_u32 m0,s54,66432
s_nop 0
buffer_load_dwordx4 v14,s[40:43],s83 offen offset:128  lds
s_setpc_b64 s[88:89]
.p2align 8
s_mov_b64 s[40:41],%24
s_mov_b32 s42,7340032
s_mov_b32 s43,0x00020000
v_add_u32 v230,57344,v15
v_add_u32 v231,57344,v16
T a[16:17],230,0
T a[18:19],230,512
T a[20:21],231,0
T a[22:23],231,512
T a[24:25],230,4096
T a[26:27],230,4608
T a[28:29],231,4096
T a[30:31],231,4608
s_add_u32 s83,s50,262144
s_add_u32 m0,s54,73728
s_nop 0
buffer_load_dwordx4 v13,s[40:43],s83 offen offset:0  lds
s_add_u32 m0,s54,74752
s_nop 0
buffer_load_dwordx4 v14,s[40:43],s83 offen offset:0  lds
s_add_u32 m0,s54,81792
s_nop 0
buffer_load_dwordx4 v13,s[40:43],s83 offen offset:128  lds
s_add_u32 m0,s54,82816
s_nop 0
buffer_load_dwordx4 v14,s[40:43],s83 offen offset:128  lds
s_setpc_b64 s[88:89]
.p2align 8
s_mov_b64 s[40:41],%24
s_mov_b32 s42,7340032
s_mov_b32 s43,0x00020000
v_add_u32 v230,73728,v15
v_add_u32 v231,73728,v16
T a[32:33],230,0
T a[34:35],230,512
T a[36:37],231,0
T a[38:39],231,512
T a[40:41],230,4096
T a[42:43],230,4608
T a[44:45],231,4096
T a[46:47],231,4608
s_add_u32 s83,s50,393216
s_add_u32 m0,s54,90112
s_nop 0
buffer_load_dwordx4 v13,s[40:43],s83 offen offset:0  lds
s_add_u32 m0,s54,91136
s_nop 0
buffer_load_dwordx4 v14,s[40:43],s83 offen offset:0  lds
s_add_u32 m0,s54,98176
s_nop 0
buffer_load_dwordx4 v13,s[40:43],s83 offen offset:128  lds
s_add_u32 m0,s54,99200
s_nop 0
buffer_load_dwordx4 v14,s[40:43],s83 offen offset:128  lds
s_setpc_b64 s[88:89]
.p2align 8
s_mov_b64 s[40:41],%24
s_mov_b32 s42,7340032
s_mov_b32 s43,0x00020000
v_add_u32 v230,90112,v15
v_add_u32 v231,90112,v16
T a[48:49],230,0
T a[50:51],230,512
T a[52:53],231,0
T a[54:55],231,512
T a[56:57],230,4096
T a[58:59],230,4608
T a[60:61],231,4096
T a[62:63],231,4608
s_add_u32 s83,s50,524288
s_add_u32 m0,s54,106496
s_nop 0
buffer_load_dwordx4 v13,s[40:43],s83 offen offset:0  lds
s_add_u32 m0,s54,107520
s_nop 0
buffer_load_dwordx4 v14,s[40:43],s83 offen offset:0  lds
s_add_u32 m0,s54,114560
s_nop 0
buffer_load_dwordx4 v13,s[40:43],s83 offen offset:128  lds
s_add_u32 m0,s54,115584
s_nop 0
buffer_load_dwordx4 v14,s[40:43],s83 offen offset:128  lds
s_setpc_b64 s[88:89]
.p2align 8
s_mov_b64 s[40:41],%24
s_mov_b32 s42,7340032
s_mov_b32 s43,0x00020000
v_add_u32 v230,106496,v15
v_add_u32 v231,106496,v16
T a[64:65],230,0
T a[66:67],230,512
T a[68:69],231,0
T a[70:71],231,512
T a[72:73],230,4096
T a[74:75],230,4608
T a[76:77],231,4096
T a[78:79],231,4608
s_add_u32 s83,s50,655360
s_add_u32 m0,s54,122880
s_nop 0
buffer_load_dwordx4 v13,s[40:43],s83 offen offset:0  lds
s_add_u32 m0,s54,123904
s_nop 0
buffer_load_dwordx4 v14,s[40:43],s83 offen offset:0  lds
s_add_u32 m0,s54,130944
s_nop 0
buffer_load_dwordx4 v13,s[40:43],s83 offen offset:128  lds
s_add_u32 m0,s54,131968
s_nop 0
buffer_load_dwordx4 v14,s[40:43],s83 offen offset:128  lds
s_setpc_b64 s[88:89]
.p2align 8
s_mov_b64 s[40:41],%24
s_mov_b32 s42,7340032
s_mov_b32 s43,0x00020000
v_add_u32 v230,122880,v15
v_add_u32 v231,122880,v16
T a[80:81],230,0
T a[82:83],230,512
T a[84:85],231,0
T a[86:87],231,512
T a[88:89],230,4096
T a[90:91],230,4608
T a[92:93],231,4096
T a[94:95],231,4608
s_add_u32 s83,s50,786432
s_add_u32 m0,s54,139264
s_nop 0
buffer_load_dwordx4 v13,s[40:43],s83 offen offset:0  lds
s_add_u32 m0,s54,140288
s_nop 0
buffer_load_dwordx4 v14,s[40:43],s83 offen offset:0  lds
s_add_u32 m0,s54,147328
s_nop 0
buffer_load_dwordx4 v13,s[40:43],s83 offen offset:128  lds
s_add_u32 m0,s54,148352
s_nop 0
buffer_load_dwordx4 v14,s[40:43],s83 offen offset:128  lds
s_setpc_b64 s[88:89]
.p2align 8
s_mov_b64 s[40:41],%24
s_mov_b32 s42,7340032
s_mov_b32 s43,0x00020000
v_add_u32 v230,139264,v15
v_add_u32 v231,139264,v16
T a[96:97],230,0
T a[98:99],230,512
T a[100:101],231,0
T a[102:103],231,512
T a[104:105],230,4096
T a[106:107],230,4608
T a[108:109],231,4096
T a[110:111],231,4608
s_add_u32 s83,s50,917504
s_add_u32 m0,s54,40960
s_nop 0
buffer_load_dwordx4 v13,s[40:43],s83 offen offset:0  lds
s_add_u32 m0,s54,41984
s_nop 0
buffer_load_dwordx4 v14,s[40:43],s83 offen offset:0  lds
s_add_u32 m0,s54,49024
s_nop 0
buffer_load_dwordx4 v13,s[40:43],s83 offen offset:128  lds
s_add_u32 m0,s54,50048
s_nop 0
buffer_load_dwordx4 v14,s[40:43],s83 offen offset:128  lds
s_setpc_b64 s[88:89]
.p2align 8
s_mov_b64 s[40:41],%24
s_mov_b32 s42,7340032
s_mov_b32 s43,0x00020000
v_add_u32 v230,40960,v15
v_add_u32 v231,40960,v16
T a[112:113],230,0
T a[114:115],230,512
T a[116:117],231,0
T a[118:119],231,512
T a[120:121],230,4096
T a[122:123],230,4608
T a[124:125],231,4096
T a[126:127],231,4608
s_add_u32 s83,s50,1048576
s_add_u32 m0,s54,57344
s_nop 0
buffer_load_dwordx4 v13,s[40:43],s83 offen offset:0  lds
s_add_u32 m0,s54,58368
s_nop 0
buffer_load_dwordx4 v14,s[40:43],s83 offen offset:0  lds
s_add_u32 m0,s54,65408
s_nop 0
buffer_load_dwordx4 v13,s[40:43],s83 offen offset:128  lds
s_add_u32 m0,s54,66432
s_nop 0
buffer_load_dwordx4 v14,s[40:43],s83 offen offset:128  lds
s_setpc_b64 s[88:89]
.p2align 8
s_mov_b64 s[40:41],%24
s_mov_b32 s42,7340032
s_mov_b32 s43,0x00020000
v_add_u32 v230,57344,v15
v_add_u32 v231,57344,v16
T a[128:129],230,0
T a[130:131],230,512
T a[132:133],231,0
T a[134:135],231,512
T a[136:137],230,4096
T a[138:139],230,4608
T a[140:141],231,4096
T a[142:143],231,4608
s_add_u32 s83,s50,1179648
s_add_u32 m0,s54,73728
s_nop 0
buffer_load_dwordx4 v13,s[40:43],s83 offen offset:0  lds
s_add_u32 m0,s54,74752
s_nop 0
buffer_load_dwordx4 v14,s[40:43],s83 offen offset:0  lds
s_add_u32 m0,s54,81792
s_nop 0
buffer_load_dwordx4 v13,s[40:43],s83 offen offset:128  lds
s_add_u32 m0,s54,82816
s_nop 0
buffer_load_dwordx4 v14,s[40:43],s83 offen offset:128  lds
s_setpc_b64 s[88:89]
.p2align 8
s_mov_b64 s[40:41],%24
s_mov_b32 s42,7340032
s_mov_b32 s43,0x00020000
v_add_u32 v230,73728,v15
v_add_u32 v231,73728,v16
T a[144:145],230,0
T a[146:147],230,512
T a[148:149],231,0
T a[150:151],231,512
T a[152:153],230,4096
T a[154:155],230,4608
T a[156:157],231,4096
T a[158:159],231,4608
s_add_u32 s83,s50,1310720
s_add_u32 m0,s54,90112
s_nop 0
buffer_load_dwordx4 v13,s[40:43],s83 offen offset:0  lds
s_add_u32 m0,s54,91136
s_nop 0
buffer_load_dwordx4 v14,s[40:43],s83 offen offset:0  lds
s_add_u32 m0,s54,98176
s_nop 0
buffer_load_dwordx4 v13,s[40:43],s83 offen offset:128  lds
s_add_u32 m0,s54,99200
s_nop 0
buffer_load_dwordx4 v14,s[40:43],s83 offen offset:128  lds
s_setpc_b64 s[88:89]
.p2align 8
Lr_end_%=:
Lcode_r_e:
.purgem M
.purgem R
.purgem T
.purgem D
.purgem L
)"
  :: "s"(ws), "s"(__builtin_amdgcn_readfirstlane(sWave)), "s"(__builtin_amdgcn_readfirstlane(sG0)), "s"(__builtin_amdgcn_readfirstlane(sStep)), "s"(__builtin_amdgcn_readfirstlane(sWw)), "s"(__builtin_amdgcn_readfirstlane(sExp)), "v"(vRP), "v"(vRF), "v"(vRwr), "v"(vRrd), "v"(vRS), "v"(vFlag), "v"(vL4), "v"(vLY), "v"(vQ), "v"(vY0), "v"(vTr), "v"(vSa), "v"(vSb), "v"(vWd0), "v"(vWd1), "v"(vWr0), "v"(vWr1), "s"(q), "s"(w4) : "v0", "v1", "v2", "v3", "v4", "v5", "v6", "v7", "v8", "v9", "v10", "v11", "v12", "v13", "v14", "v15", "v16", "v17", "v18", "v19", "v20", "v21", "v22", "v23", "v24", "v25", "v26", "v27", "v28", "v29", "v30", "v31", "v32", "v33", "v34", "v35", "v36", "v37", "v38", "v39", "v40", "v41", "v42", "v43", "v44", "v45", "v46", "v47", "v48", "v49", "v50", "v51", "v52", "v53", "v54", "v55", "v56", "v57", "v58", "v59", "v60", "v61", "v62", "v63", "v64", "v65", "v66", "v67", "v68", "v69", "v70", "v71", "v72", "v73", "v74", "v75", "v76", "v77", "v78", "v79", "v80", "v81", "v82", "v83", "v84", "v85", "v86", "v87", "v88", "v89", "v90", "v91", "v92", "v93", "v94", "v95", "v96", "v97", "v98", "v99", "v100", "v101", "v102", "v103", "v104", "v105", "v106", "v107", "v108", "v109", "v110", "v111", "v112", "v113", "v114", "v115", "v116", "v117", "v118", "v119", "v120", "v121", "v122", "v123", "v124", "v125", "v126", "v127", "v128", "v129", "v130", "v131", "v132", "v133", "v134", "v135", "v136", "v137", "v138", "v139", "v140", "v141", "v142", "v143", "v144", "v145", "v146", "v147", "v148", "v149", "v150", "v151", "v152", "v153", "v154", "v155", "v156", "v157", "v158", "v159", "v160", "v161", "v162", "v163", "v164", "v165", "v166", "v167", "v168", "v169", "v170", "v171", "v172", "v173", "v174", "v175", "v176", "v177", "v178", "v179", "v180", "v181", "v182", "v183", "v184", "v185", "v186", "v187", "v188", "v189", "v190", "v191", "v192", "v193", "v194", "v195", "v196", "v197", "v198", "v199", "v200", "v201", "v202", "v203", "v204", "v205", "v206", "v207", "v208", "v209", "v210", "v211", "v212", "v213", "v214", "v215", "v216", "v217", "v218", "v219", "v220", "v221", "v222", "v223", "v224", "v225", "v226", "v227", "v228", "v229", "v230", "v231", "a0", "a1", "a2", "a3", "a4", "a5", "a6", "a7", "a8", "a9", "a10", "a11", "a12", "a13", "a14", "a15", "a16", "a17", "a18", "a19", "a20", "a21", "a22", "a23", "a24", "a25", "a26", "a27", "a28", "a29", "a30", "a31", "a32", "a33", "a34", "a35", "a36", "a37", "a38", "a39", "a40", "a41", "a42", "a43", "a44", "a45", "a46", "a47", "a48", "a49", "a50", "a51", "a52", "a53", "a54", "a55", "a56", "a57", "a58", "a59", "a60", "a61", "a62", "a63", "a64", "a65", "a66", "a67", "a68", "a69", "a70", "a71", "a72", "a73", "a74", "a75", "a76", "a77", "a78", "a79", "a80", "a81", "a82", "a83", "a84", "a85", "a86", "a87", "a88", "a89", "a90", "a91", "a92", "a93", "a94", "a95", "a96", "a97", "a98", "a99", "a100", "a101", "a102", "a103", "a104", "a105", "a106", "a107", "a108", "a109", "a110", "a111", "a112", "a113", "a114", "a115", "a116", "a117", "a118", "a119", "a120", "a121", "a122", "a123", "a124", "a125", "a126", "a127", "a128", "a129", "a130", "a131", "a132", "a133", "a134", "a135", "a136", "a137", "a138", "a139", "a140", "a141", "a142", "a143", "a144", "a145", "a146", "a147", "a148", "a149", "a150", "a151", "a152", "a153", "a154", "a155", "a156", "a157", "a158", "a159", "a160", "a161", "a162", "a163", "a164", "a165", "a166", "a167", "a168", "a169", "a170", "a171", "a172", "a173", "a174", "a175", "a176", "a177", "a178", "a179", "a180", "a181", "a182", "a183", "a184", "a185", "a186", "a187", "a188", "a189", "a190", "a191", "a192", "a193", "a194", "a195", "a196", "a197", "a198", "a199", "a200", "a201", "a202", "a203", "a204", "a205", "a206", "a207", "a208", "a209", "a210", "a211", "a212", "a213", "a214", "a215", "a216", "a217", "a218", "a219", "a220", "a221", "a222", "a223", "a224", "a225", "a226", "a227", "a228", "a229", "a230", "a231", "a232", "a233", "a234", "a235", "a236", "a237", "a238", "a239", "a240", "a241", "a242", "a243", "a244", "a245", "a246", "a247", "a248", "a249", "a250", "a251", "a252", "a253", "a254", "a255", "s36", "s37", "s38", "s39", "s40", "s41", "s42", "s43", "s44", "s45", "s46", "s47", "s48", "s49", "s50", "s51", "s52", "s53", "s54", "s55", "s56", "s57", "s58", "s59", "s60", "s61", "s62", "s63", "s64", "s65", "s66", "s67", "s68", "s69", "s70", "s71", "s72", "s73", "s74", "s75", "s76", "s77", "s78", "s79", "s80", "s81", "s82", "s83", "s84", "s85", "s86", "s87", "s88", "s89", "s90", "s91", "s92", "s93", "s94", "s95", "s96", "s97", "vcc", "scc", "memory");
    if (RHELP && slice < 16) { kind = 4; rep = 5; kh = slice >> 3; slice &= 7; }
  }
  CTS(9);
#define COMMON(NF, FOFF, FSTR, SNAP, LDW, NL, TOFF)                                                                                            \
  LANE_ID(ln);                                                                                                                       \
  const int l15 = ln & 15, lg = ln >> 4, lq = l15 >> 2, lp = ln & 3, rr = 8 * lg + lq, fj = ln & 31;                                 \
  const unsigned vL16 = ln * 16, vFlag = (ln == 0 && wave == 0) ? 0u : OOB;                                                      \
  const unsigned vSnapOff = (wave == 0 && fj < (NF)) ? (FOFF) + (ln >> 5) * sStep * (FSTR) : OOB;                                    \
  const unsigned vSnapRd = (SNAP) + (fj < (NF) ? ln : ln & 32) * 4;                                                                  \
  const unsigned vWd0 = (ln >> 3) * (LDW) + ((ln & 7) ^ swzB(ln >> 3)) * 16, vWd1 = ((ln >> 3) + 8) * (LDW) + ((ln & 7) ^ swzB(8 + (ln >> 3))) * 16; \
  const unsigned vWr0 = (wave >> 1) * 8192 + rr * 128 + (((wave & 1) * 4 + lp) ^ swzB(rr)) * 16, vWr1 = vWr0 + 8;                    \
  const unsigned vWr2 = 16384 + rr * 128 + ((2 * wave + (lp >> 1)) ^ swzB(rr)) * 16 + (lp & 1) * 8;                                  \
  const unsigned sWave = wave, sRk = kind == 4 ? rep : t_start, sG0 = rep < 5 ? rep : rep == 5 ? U0 + 5 : U1 + rep, sDbg = sDbg0 + (rep < 5 ? 0 : 2048)
  if (kind == 3 || kind == 5) {
    const unsigned sStep = kind == 3 ? 5 : G3OR;
    COMMON(8, fj * FS1, 1024, 131072, 7168, 256, 0);
    const unsigned vOutS = (lg & 1) * 8 + (((wave & 1) * 2 + (lg >> 1)) * 16 + l15) * 16 + (4 + (wave >> 1) - wave) * 1024;
    const unsigned vAux0 = (ln & 7) * 128 + (4 * wave + lg) * 8 + ((ln >> 3) & 1) * 4;
    const unsigned vAux1 = 139264 + (l15 < 2 ? (4 * wave + lg) * 8 + l15 * 4 : 512 + ln * 4), vAux2 = 139264 + l15 * 8;
    const unsigned vAux3 = 0, vAn = l15 == 0 ? 0x3f800000u : 0u, vAe = l15 == 1 ? GELU_CB : 0u;
    const unsigned sOutOff = OFF_G + (slice * 6 + wave) * 1024, sFoutOff = OFF_FG + slice * FSG, sAuxOff = 0, sWw = 16 * wave * 7168 + slice * 384, sBuf = 0;
    const unsigned sExp = __builtin_amdgcn_readfirstlane(ep);
    if (kind == 3) { asm volatile(
R"(.macro M d,w,x
v_mfma_f32_16x16x32_bf16 v[\d:\d+3], \w, v[\x:\x+3], v[\d:\d+3]
.endm
.macro R d,a,o
ds_read_b128 v[\d:\d+3], v\a offset:\o
.endm
.macro T d,a,o
ds_read_b64_tr_b16 \d, v\a offset:\o
.endm
.macro D a,s,o
buffer_load_dwordx4 v\a, s[36:39], s\s offen offset:\o sc1 lds
.endm
.macro L c
s_waitcnt lgkmcnt(\c)
.endm
Lcode_g3_b:
s_mov_b64 s[36:37],%0
s_mov_b32 s38,111243392
s_mov_b32 s39,0x00020000
s_mov_b32 s44,%1
s_mov_b32 s45,%2
s_mov_b32 s46,%3
s_mov_b32 s47,%4
s_mov_b32 s48,%5
s_mov_b32 s49,%6
s_mov_b32 s50,%7
s_mov_b32 s51,%8
s_mov_b32 s53,%9
v_mov_b32 v0,%10
v_mov_b32 v3,%11
v_mov_b32 v5,%12
v_mov_b32 v6,%13
v_mov_b32 v18,%14
v_mov_b32 v19,%15
v_mov_b32 v20,%16
v_mov_b32 v21,%17
v_mov_b32 v22,%18
v_mov_b32 v10,%19
v_mov_b32 v11,%20
v_mov_b32 v12,%21
v_mov_b32 v13,%22
v_mov_b32 v14,%23
v_mov_b32 v15,%24
v_mov_b32 v16,%25
s_mov_b64 s[40:41],%26
s_mov_b32 s42,7340032
s_mov_b32 s43,0x00020000
s_lshl_b32 s57,s44,11
s_cmp_ge_u32 s45,8
s_cbranch_scc1 Lg3_dly_%=
s_sleep 16
Lg3_dly_%=:
s_add_u32 s55,s50,0
s_add_u32 m0,s57,0
s_nop 0
buffer_load_dwordx4 v18,s[40:43],s55 offen offset:0  lds
s_add_u32 m0,s57,1024
s_nop 0
buffer_load_dwordx4 v19,s[40:43],s55 offen offset:0  lds
s_add_u32 m0,s57,8064
s_nop 0
buffer_load_dwordx4 v18,s[40:43],s55 offen offset:128  lds
s_add_u32 m0,s57,9088
s_nop 0
buffer_load_dwordx4 v19,s[40:43],s55 offen offset:128  lds
s_add_u32 m0,s57,16128
s_nop 0
buffer_load_dwordx4 v18,s[40:43],s55 offen offset:256  lds
s_add_u32 m0,s57,17152
s_nop 0
buffer_load_dwordx4 v19,s[40:43],s55 offen offset:256  lds
s_add_u32 s55,s50,458752
s_add_u32 m0,s57,24576
s_nop 0
buffer_load_dwordx4 v18,s[40:43],s55 offen offset:0  lds
s_add_u32 m0,s57,25600
s_nop 0
buffer_load_dwordx4 v19,s[40:43],s55 offen offset:0  lds
s_add_u32 m0,s57,32640
s_nop 0
buffer_load_dwordx4 v18,s[40:43],s55 offen offset:128  lds
s_add_u32 m0,s57,33664
s_nop 0
buffer_load_dwordx4 v19,s[40:43],s55 offen offset:128  lds
s_add_u32 m0,s57,40704
s_nop 0
buffer_load_dwordx4 v18,s[40:43],s55 offen offset:256  lds
s_add_u32 m0,s57,41728
s_nop 0
buffer_load_dwordx4 v19,s[40:43],s55 offen offset:256  lds
s_add_u32 s55,s50,917504
s_add_u32 m0,s57,49152
s_nop 0
buffer_load_dwordx4 v18,s[40:43],s55 offen offset:0  lds
s_add_u32 m0,s57,50176
s_nop 0
buffer_load_dwordx4 v19,s[40:43],s55 offen offset:0  lds
s_add_u32 m0,s57,57216
s_nop 0
buffer_load_dwordx4 v18,s[40:43],s55 offen offset:128  lds
s_add_u32 m0,s57,58240
s_nop 0
buffer_load_dwordx4 v19,s[40:43],s55 offen offset:128  lds
s_add_u32 m0,s57,65280
s_nop 0
buffer_load_dwordx4 v18,s[40:43],s55 offen offset:256  lds
s_add_u32 m0,s57,66304
s_nop 0
buffer_load_dwordx4 v19,s[40:43],s55 offen offset:256  lds
s_add_u32 s55,s50,1376256
s_add_u32 m0,s57,73728
s_nop 0
buffer_load_dwordx4 v18,s[40:43],s55 offen offset:0  lds
s_add_u32 m0,s57,74752
s_nop 0
buffer_load_dwordx4 v19,s[40:43],s55 offen offset:0  lds
s_add_u32 m0,s57,81792
s_nop 0
buffer_load_dwordx4 v18,s[40:43],s55 offen offset:128  lds
s_add_u32 m0,s57,82816
s_nop 0
buffer_load_dwordx4 v19,s[40:43],s55 offen offset:128  lds
s_add_u32 m0,s57,89856
s_nop 0
buffer_load_dwordx4 v18,s[40:43],s55 offen offset:256  lds
s_add_u32 m0,s57,90880
s_nop 0
buffer_load_dwordx4 v19,s[40:43],s55 offen offset:256  lds
s_add_u32 s55,s50,1835008
s_add_u32 m0,s57,98304
s_nop 0
buffer_load_dwordx4 v18,s[40:43],s55 offen offset:0  lds
s_add_u32 m0,s57,99328
s_nop 0
buffer_load_dwordx4 v19,s[40:43],s55 offen offset:0  lds
s_add_u32 m0,s57,106368
s_nop 0
buffer_load_dwordx4 v18,s[40:43],s55 offen offset:128  lds
s_add_u32 m0,s57,107392
s_nop 0
buffer_load_dwordx4 v19,s[40:43],s55 offen offset:128  lds
s_add_u32 m0,s57,114432
s_nop 0
buffer_load_dwordx4 v18,s[40:43],s55 offen offset:256  lds
s_add_u32 m0,s57,115456
s_nop 0
buffer_load_dwordx4 v19,s[40:43],s55 offen offset:256  lds
s_waitcnt vmcnt(24) lgkmcnt(0)
s_barrier
s_add_u32 s55,s50,2293760
s_add_u32 m0,s57,122880
s_nop 0
buffer_load_dwordx4 v18,s[40:43],s55 offen offset:0  lds
s_add_u32 m0,s57,123904
s_nop 0
buffer_load_dwordx4 v19,s[40:43],s55 offen offset:0  lds
s_add_u32 m0,s57,130944
s_nop 0
buffer_load_dwordx4 v18,s[40:43],s55 offen offset:128  lds
s_add_u32 m0,s57,131968
s_nop 0
buffer_load_dwordx4 v19,s[40:43],s55 offen offset:128  lds
s_add_u32 m0,s57,139008
s_nop 0
buffer_load_dwordx4 v18,s[40:43],s55 offen offset:256  lds
s_add_u32 m0,s57,140032
s_nop 0
buffer_load_dwordx4 v19,s[40:43],s55 offen offset:256  lds
v_add_u32 v26,0,v20
v_add_u32 v27,0,v21
v_add_u32 v28,0,v22
T a[0:1],26,0
T a[2:3],26,512
T a[4:5],27,0
T a[6:7],27,512
T a[8:9],28,0
T a[10:11],28,512
T a[12:13],26,4096
T a[14:15],26,4608
T a[16:17],27,4096
T a[18:19],27,4608
T a[20:21],28,4096
T a[22:23],28,4608
s_waitcnt vmcnt(24) lgkmcnt(0)
s_barrier
s_add_u32 s55,s50,2752512
s_add_u32 m0,s57,0
s_nop 0
buffer_load_dwordx4 v18,s[40:43],s55 offen offset:0  lds
s_add_u32 m0,s57,1024
s_nop 0
buffer_load_dwordx4 v19,s[40:43],s55 offen offset:0  lds
s_add_u32 m0,s57,8064
s_nop 0
buffer_load_dwordx4 v18,s[40:43],s55 offen offset:128  lds
s_add_u32 m0,s57,9088
s_nop 0
buffer_load_dwordx4 v19,s[40:43],s55 offen offset:128  lds
s_add_u32 m0,s57,16128
s_nop 0
buffer_load_dwordx4 v18,s[40:43],s55 offen offset:256  lds
s_add_u32 m0,s57,17152
s_nop 0
buffer_load_dwordx4 v19,s[40:43],s55 offen offset:256  lds
v_add_u32 v26,24576,v20
v_add_u32 v27,24576,v21
v_add_u32 v28,24576,v22
T a[24:25],26,0
T a[26:27],26,512
T a[28:29],27,0
T a[30:31],27,512
T a[32:33],28,0
T a[34:35],28,512
T a[36:37],26,4096
T a[38:39],26,4608
T a[40:41],27,4096
T a[42:43],27,4608
T a[44:45],28,4096
T a[46:47],28,4608
s_waitcnt vmcnt(24) lgkmcnt(0)
s_barrier
s_add_u32 s55,s50,3211264
s_add_u32 m0,s57,24576
s_nop 0
buffer_load_dwordx4 v18,s[40:43],s55 offen offset:0  lds
s_add_u32 m0,s57,25600
s_nop 0
buffer_load_dwordx4 v19,s[40:43],s55 offen offset:0  lds
s_add_u32 m0,s57,32640
s_nop 0
buffer_load_dwordx4 v18,s[40:43],s55 offen offset:128  lds
s_add_u32 m0,s57,33664
s_nop 0
buffer_load_dwordx4 v19,s[40:43],s55 offen offset:128  lds
s_add_u32 m0,s57,40704
s_nop 0
buffer_load_dwordx4 v18,s[40:43],s55 offen offset:256  lds
s_add_u32 m0,s57,41728
s_nop 0
buffer_load_dwordx4 v19,s[40:43],s55 offen offset:256  lds
v_add_u32 v26,49152,v20
v_add_u32 v27,49152,v21
v_add_u32 v28,49152,v22
T a[48:49],26,0
T a[50:51],26,512
T a[52:53],27,0
T a[54:55],27,512
T a[56:57],28,0
T a[58:59],28,512
T a[60:61],26,4096
T a[62:63],26,4608
T a[64:65],27,4096
T a[66:67],27,4608
T a[68:69],28,4096
T a[70:71],28,4608
s_waitcnt vmcnt(24) lgkmcnt(0)
s_barrier
s_add_u32 s55,s50,3670016
s_add_u32 m0,s57,49152
s_nop 0
buffer_load_dwordx4 v18,s[40:43],s55 offen offset:0  lds
s_add_u32 m0,s57,50176
s_nop 0
buffer_load_dwordx4 v19,s[40:43],s55 offen offset:0  lds
s_add_u32 m0,s57,57216
s_nop 0
buffer_load_dwordx4 v18,s[40:43],s55 offen offset:128  lds
s_add_u32 m0,s57,58240
s_nop 0
buffer_load_dwordx4 v19,s[40:43],s55 offen offset:128  lds
s_add_u32 m0,s57,65280
s_nop 0
buffer_load_dwordx4 v18,s[40:43],s55 offen offset:256  lds
s_add_u32 m0,s57,66304
s_nop 0
buffer_load_dwordx4 v19,s[40:43],s55 offen offset:256  lds
v_add_u32 v26,73728,v20
v_add_u32 v27,73728,v21
v_add_u32 v28,73728,v22
T a[72:73],26,0
T a[74:75],26,512
T a[76:77],27,0
T a[78:79],27,512
T a[80:81],28,0
T a[82:83],28,512
T a[84:85],26,4096
T a[86:87],26,4608
T a[88:89],27,4096
T a[90:91],27,4608
T a[92:93],28,4096
T a[94:95],28,4608
s_waitcnt vmcnt(24) lgkmcnt(0)
s_barrier
s_add_u32 s55,s50,4128768
s_add_u32 m0,s57,73728
s_nop 0
buffer_load_dwordx4 v18,s[40:43],s55 offen offset:0  lds
s_add_u32 m0,s57,74752
s_nop 0
buffer_load_dwordx4 v19,s[40:43],s55 offen offset:0  lds
s_add_u32 m0,s57,81792
s_nop 0
buffer_load_dwordx4 v18,s[40:43],s55 offen offset:128  lds
s_add_u32 m0,s57,82816
s_nop 0
buffer_load_dwordx4 v19,s[40:43],s55 offen offset:128  lds
s_add_u32 m0,s57,89856
s_nop 0
buffer_load_dwordx4 v18,s[40:43],s55 offen offset:256  lds
s_add_u32 m0,s57,90880
s_nop 0
buffer_load_dwordx4 v19,s[40:43],s55 offen offset:256  lds
v_add_u32 v26,98304,v20
v_add_u32 v27,98304,v21
v_add_u32 v28,98304,v22
T a[96:97],26,0
T a[98:99],26,512
T a[100:101],27,0
T a[102:103],27,512
T a[104:105],28,0
T a[106:107],28,512
T a[108:109],26,4096
T a[110:111],26,4608
T a[112:113],27,4096
T a[114:115],27,4608
T a[116:117],28,4096
T a[118:119],28,4608
s_waitcnt vmcnt(24) lgkmcnt(0)
s_barrier
s_add_u32 s55,s50,4587520
s_add_u32 m0,s57,98304
s_nop 0
buffer_load_dwordx4 v18,s[40:43],s55 offen offset:0  lds
s_add_u32 m0,s57,99328
s_nop 0
buffer_load_dwordx4 v19,s[40:43],s55 offen offset:0  lds
s_add_u32 m0,s57,106368
s_nop 0
buffer_load_dwordx4 v18,s[40:43],s55 offen offset:128  lds
s_add_u32 m0,s57,107392
s_nop 0
buffer_load_dwordx4 v19,s[40:43],s55 offen offset:128  lds
s_add_u32 m0,s57,114432
s_nop 0
buffer_load_dwordx4 v18,s[40:43],s55 offen offset:256  lds
s_add_u32 m0,s57,115456
s_nop 0
buffer_load_dwordx4 v19,s[40:43],s55 offen offset:256  lds
v_add_u32 v26,122880,v20
v_add_u32 v27,122880,v21
v_add_u32 v28,122880,v22
T a[120:121],26,0
T a[122:123],26,512
T a[124:125],27,0
T a[126:127],27,512
T a[128:129],28,0
T a[130:131],28,512
T a[132:133],26,4096
T a[134:135],26,4608
T a[136:137],27,4096
T a[138:139],27,4608
T a[140:141],28,4096
T a[142:143],28,4608
s_waitcnt vmcnt(24) lgkmcnt(0)
s_barrier
s_add_u32 s55,s50,5046272
s_add_u32 m0,s57,122880
s_nop 0
buffer_load_dwordx4 v18,s[40:43],s55 offen offset:0  lds
s_add_u32 m0,s57,123904
s_nop 0
buffer_load_dwordx4 v19,s[40:43],s55 offen offset:0  lds
s_add_u32 m0,s57,130944
s_nop 0
buffer_load_dwordx4 v18,s[40:43],s55 offen offset:128  lds
s_add_u32 m0,s57,131968
s_nop 0
buffer_load_dwordx4 v19,s[40:43],s55 offen offset:128  lds
s_add_u32 m0,s57,139008
s_nop 0
buffer_load_dwordx4 v18,s[40:43],s55 offen offset:256  lds
s_add_u32 m0,s57,140032
s_nop 0
buffer_load_dwordx4 v19,s[40:43],s55 offen offset:256  lds
v_add_u32 v26,0,v20
v_add_u32 v27,0,v21
v_add_u32 v28,0,v22
T a[144:145],26,0
T a[146:147],26,512
T a[148:149],27,0
T a[150:151],27,512
T a[152:153],28,0
T a[154:155],28,512
T a[156:157],26,4096
T a[158:159],26,4608
T a[160:161],27,4096
T a[162:163],27,4608
T a[164:165],28,4096
T a[166:167],28,4608
s_waitcnt vmcnt(24) lgkmcnt(0)
s_barrier
s_add_u32 s55,s50,5505024
s_add_u32 m0,s57,0
s_nop 0
buffer_load_dwordx4 v18,s[40:43],s55 offen offset:0  lds
s_add_u32 m0,s57,1024
s_nop 0
buffer_load_dwordx4 v19,s[40:43],s55 offen offset:0  lds
s_add_u32 m0,s57,8064
s_nop 0
buffer_load_dwordx4 v18,s[40:43],s55 offen offset:128  lds
s_add_u32 m0,s57,9088
s_nop 0
buffer_load_dwordx4 v19,s[40:43],s55 offen offset:128  lds
s_add_u32 m0,s57,16128
s_nop 0
buffer_load_dwordx4 v18,s[40:43],s55 offen offset:256  lds
s_add_u32 m0,s57,17152
s_nop 0
buffer_load_dwordx4 v19,s[40:43],s55 offen offset:256  lds
v_add_u32 v26,24576,v20
v_add_u32 v27,24576,v21
v_add_u32 v28,24576,v22
T a[168:169],26,0
T a[170:171],26,512
T a[172:173],27,0
T a[174:175],27,512
T a[176:177],28,0
T a[178:179],28,512
T a[180:181],26,4096
T a[182:183],26,4608
T a[184:185],27,4096
T a[186:187],27,4608
T a[188:189],28,4096
T a[190:191],28,4608
s_waitcnt vmcnt(24) lgkmcnt(0)
s_barrier
s_add_u32 s55,s50,5963776
s_add_u32 m0,s57,24576
s_nop 0
buffer_load_dwordx4 v18,s[40:43],s55 offen offset:0  lds
s_add_u32 m0,s57,25600
s_nop 0
buffer_load_dwordx4 v19,s[40:43],s55 offen offset:0  lds
s_add_u32 m0,s57,32640
s_nop 0
buffer_load_dwordx4 v18,s[40:43],s55 offen offset:128  lds
s_add_u32 m0,s57,33664
s_nop 0
buffer_load_dwordx4 v19,s[40:43],s55 offen offset:128  lds
s_add_u32 m0,s57,40704
s_nop 0
buffer_load_dwordx4 v18,s[40:43],s55 offen offset:256  lds
s_add_u32 m0,s57,41728
s_nop 0
buffer_load_dwordx4 v19,s[40:43],s55 offen offset:256  lds
v_add_u32 v26,49152,v20
v_add_u32 v27,49152,v21
v_add_u32 v28,49152,v22
T a[192:193],26,0
T a[194:195],26,512
T a[196:197],27,0
T a[198:199],27,512
T a[200:201],28,0
T a[202:203],28,512
T a[204:205],26,4096
T a[206:207],26,4608
T a[208:209],27,4096
T a[210:211],27,4608
T a[212:213],28,4096
T a[214:215],28,4608
s_waitcnt vmcnt(24) lgkmcnt(0)
s_barrier
s_add_u32 s55,s50,6422528
s_add_u32 m0,s57,49152
s_nop 0
buffer_load_dwordx4 v18,s[40:43],s55 offen offset:0  lds
s_add_u32 m0,s57,50176
s_nop 0
buffer_load_dwordx4 v19,s[40:43],s55 offen offset:0  lds
s_add_u32 m0,s57,57216
s_nop 0
buffer_load_dwordx4 v18,s[40:43],s55 offen offset:128  lds
s_add_u32 m0,s57,58240
s_nop 0
buffer_load_dwordx4 v19,s[40:43],s55 offen offset:128  lds
s_add_u32 m0,s57,65280
s_nop 0
buffer_load_dwordx4 v18,s[40:43],s55 offen offset:256  lds
s_add_u32 m0,s57,66304
s_nop 0
buffer_load_dwordx4 v19,s[40:43],s55 offen offset:256  lds
v_add_u32 v26,73728,v20
v_add_u32 v27,73728,v21
v_add_u32 v28,73728,v22
T a[216:217],26,0
T a[218:219],26,512
T a[220:221],27,0
T a[222:223],27,512
T a[224:225],28,0
)"
R"(T a[226:227],28,512
T a[228:229],26,4096
T a[230:231],26,4608
T a[232:233],27,4096
T a[234:235],27,4608
T a[236:237],28,4096
T a[238:239],28,4608
s_waitcnt vmcnt(24) lgkmcnt(0)
s_barrier
s_add_u32 s55,s50,6881280
s_add_u32 m0,s57,73728
s_nop 0
buffer_load_dwordx4 v18,s[40:43],s55 offen offset:0  lds
s_add_u32 m0,s57,74752
s_nop 0
buffer_load_dwordx4 v19,s[40:43],s55 offen offset:0  lds
s_add_u32 m0,s57,81792
s_nop 0
buffer_load_dwordx4 v18,s[40:43],s55 offen offset:128  lds
s_add_u32 m0,s57,82816
s_nop 0
buffer_load_dwordx4 v19,s[40:43],s55 offen offset:128  lds
s_add_u32 m0,s57,89856
s_nop 0
buffer_load_dwordx4 v18,s[40:43],s55 offen offset:256  lds
s_add_u32 m0,s57,90880
s_nop 0
buffer_load_dwordx4 v19,s[40:43],s55 offen offset:256  lds
v_add_u32 v26,98304,v20
v_add_u32 v27,98304,v21
v_add_u32 v28,98304,v22
T a[240:241],26,0
T a[242:243],26,512
T a[244:245],27,0
T a[246:247],27,512
T a[248:249],28,0
T a[250:251],28,512
T a[252:253],26,4096
T a[254:255],26,4608
T v[128:129],27,4096
T v[130:131],27,4608
T v[132:133],28,4096
T v[134:135],28,4608
s_waitcnt vmcnt(24) lgkmcnt(0)
s_barrier
v_add_u32 v26,122880,v20
v_add_u32 v27,122880,v21
v_add_u32 v28,122880,v22
T v[136:137],26,0
T v[138:139],26,512
T v[140:141],27,0
T v[142:143],27,512
T v[144:145],28,0
T v[146:147],28,512
T v[148:149],26,4096
T v[150:151],26,4608
T v[152:153],27,4096
T v[154:155],27,4608
T v[156:157],28,4096
T v[158:159],28,4608
s_waitcnt vmcnt(18) lgkmcnt(0)
s_barrier
v_add_u32 v26,0,v20
v_add_u32 v27,0,v21
v_add_u32 v28,0,v22
T v[160:161],26,0
T v[162:163],26,512
T v[164:165],27,0
T v[166:167],27,512
T v[168:169],28,0
T v[170:171],28,512
T v[172:173],26,4096
T v[174:175],26,4608
T v[176:177],27,4096
T v[178:179],27,4608
T v[180:181],28,4096
T v[182:183],28,4608
s_waitcnt vmcnt(12) lgkmcnt(0)
s_barrier
v_add_u32 v26,24576,v20
v_add_u32 v27,24576,v21
v_add_u32 v28,24576,v22
T v[184:185],26,0
T v[186:187],26,512
T v[188:189],27,0
T v[190:191],27,512
T v[192:193],28,0
T v[194:195],28,512
T v[196:197],26,4096
T v[198:199],26,4608
T v[200:201],27,4096
T v[202:203],27,4608
T v[204:205],28,4096
T v[206:207],28,4608
s_waitcnt vmcnt(6) lgkmcnt(0)
s_barrier
v_add_u32 v26,49152,v20
v_add_u32 v27,49152,v21
v_add_u32 v28,49152,v22
T v[208:209],26,0
T v[210:211],26,512
T v[212:213],27,0
T v[214:215],27,512
T v[216:217],28,0
T v[218:219],28,512
T v[220:221],26,4096
T v[222:223],26,4608
T v[224:225],27,4096
T v[226:227],27,4608
T v[228:229],28,4096
T v[230:231],28,4608
s_waitcnt vmcnt(0) lgkmcnt(0)
s_barrier
v_add_u32 v26,73728,v20
v_add_u32 v27,73728,v21
v_add_u32 v28,73728,v22
T v[232:233],26,0
T v[234:235],26,512
T v[236:237],27,0
T v[238:239],27,512
T v[240:241],28,0
T v[242:243],28,512
T v[244:245],26,4096
T v[246:247],26,4608
T v[248:249],27,4096
T v[250:251],27,4608
T v[252:253],28,4096
T v[254:255],28,4608
L 0
s_barrier
v_mov_b32 v42,0x3f803f80
v_mov_b32 v43,0x3f803f80
v_mov_b32 v44,0x3f803f80
v_mov_b32 v45,0x3f803f80
s_nop 1
v_mfma_f32_16x16x32_bf16 v[82:85],a[0:3],v[42:45],0
v_mfma_f32_16x16x32_bf16 v[86:89],a[4:7],v[42:45],0
v_mfma_f32_16x16x32_bf16 v[90:93],a[8:11],v[42:45],0
M 82,a[12:15],42
M 86,a[16:19],42
M 90,a[20:23],42
M 82,a[24:27],42
M 86,a[28:31],42
M 90,a[32:35],42
M 82,a[36:39],42
M 86,a[40:43],42
M 90,a[44:47],42
M 82,a[48:51],42
M 86,a[52:55],42
M 90,a[56:59],42
M 82,a[60:63],42
M 86,a[64:67],42
M 90,a[68:71],42
M 82,a[72:75],42
M 86,a[76:79],42
M 90,a[80:83],42
M 82,a[84:87],42
M 86,a[88:91],42
M 90,a[92:95],42
M 82,a[96:99],42
M 86,a[100:103],42
M 90,a[104:107],42
M 82,a[108:111],42
M 86,a[112:115],42
M 90,a[116:119],42
M 82,a[120:123],42
M 86,a[124:127],42
M 90,a[128:131],42
M 82,a[132:135],42
M 86,a[136:139],42
M 90,a[140:143],42
M 82,a[144:147],42
M 86,a[148:151],42
M 90,a[152:155],42
M 82,a[156:159],42
M 86,a[160:163],42
M 90,a[164:167],42
M 82,a[168:171],42
M 86,a[172:175],42
M 90,a[176:179],42
M 82,a[180:183],42
M 86,a[184:187],42
M 90,a[188:191],42
M 82,a[192:195],42
M 86,a[196:199],42
M 90,a[200:203],42
M 82,a[204:207],42
M 86,a[208:211],42
M 90,a[212:215],42
M 82,a[216:219],42
M 86,a[220:223],42
M 90,a[224:227],42
M 82,a[228:231],42
M 86,a[232:235],42
M 90,a[236:239],42
M 82,a[240:243],42
M 86,a[244:247],42
M 90,a[248:251],42
M 82,a[252:255],42
M 86,v[128:131],42
M 90,v[132:135],42
M 82,v[136:139],42
M 86,v[140:143],42
M 90,v[144:147],42
M 82,v[148:151],42
M 86,v[152:155],42
M 90,v[156:159],42
M 82,v[160:163],42
M 86,v[164:167],42
M 90,v[168:171],42
M 82,v[172:175],42
M 86,v[176:179],42
M 90,v[180:183],42
M 82,v[184:187],42
M 86,v[188:191],42
M 90,v[192:195],42
M 82,v[196:199],42
M 86,v[200:203],42
M 90,v[204:207],42
M 82,v[208:211],42
M 86,v[212:215],42
M 90,v[216:219],42
M 82,v[220:223],42
M 86,v[224:227],42
M 90,v[228:231],42
M 82,v[232:235],42
M 86,v[236:239],42
M 90,v[240:243],42
M 82,v[244:247],42
M 86,v[248:251],42
M 90,v[252:255],42
v_mov_b32 v4,s53
s_mov_b32 s82,0
s_mov_b32 s80,0
s_mov_b32 s54,s45
s_mul_i32 s57,s44,4096
s_mul_i32 s55,s45,32768
s_add_u32 s55,s55,s57
s_add_u32 s55,s55,31457280
s_mul_i32 s57,s44,4096
s_mul_i32 s60,s46,32768
s_mul_i32 s61,s46,1024
s_mul_i32 s62,s46,114688
s_mul_i32 s63,s46,2560
s_mul_i32 s64,s46,1024
s_lshl_b32 s59,s44,8
s_add_u32 s59,s59,131072
s_mul_i32 s58,s45,1024
s_add_u32 s58,s58,109613056
s_mul_i32 s65,s45,114688
s_add_u32 s65,s65,s47
s_mul_i32 s68,s45,2560
s_add_u32 s68,s68,s48
s_mul_i32 s73,s45,1024
s_add_u32 s73,s73,107479040
s_mul_i32 s74,s45,32768
s_add_u32 s74,s74,s49
s_mul_i32 s75,s45,1024
s_add_u32 s75,s75,107479040
s_mov_b32 s66,0x7fff0000
s_mov_b32 s67,0x7fff0000
s_mov_b32 s69,0x7fff0000
s_mov_b32 s76,0x7fff0000
s_mov_b32 s77,0x7fff0000
s_mov_b32 s70,0x7fff0000
s_mov_b32 s71,0x7fff0000
s_mov_b32 s72,0x7fff0000
v_mov_b32 v1,v0
v_add_u32 v2,16384,v0
s_mov_b32 s84,16384
s_mov_b32 s86,0x3a800000
s_mov_b32 s87,0x3727c5ac
s_mov_b32 s88,0x409d30fc
s_mov_b32 s89,0xbeeff558
s_lshr_b32 s85,s44,1
s_mul_i32 s85,s85,127
v_lshlrev_b32 v8,22,v0
v_and_b32 v8,0x80000000,v8
v_or_b32 v8,v8,v5
v_add_u32 v2,32768,v0
s_lshl_b32 s84,s57,1
s_add_u32 s84,s84,32768
s_getpc_b64 s[40:41]
Lg3_pc_%=:
s_lshl_b32 s78,s44,13
v_lshlrev_b32 v94,3,v0
v_add_u32 v94,s78,v94
v_min_u32 v94,Lcode_g3_e-Lg3_pc_%=-4,v94
s_mov_b32 m0,163584
s_nop 0
global_load_lds_dword v94,s[40:41]
v_add_u32 v94,32768,v94
v_min_u32 v94,Lcode_g3_e-Lg3_pc_%=-4,v94
global_load_lds_dword v94,s[40:41]
s_add_u32 m0,s59,4096
s_nop 0
buffer_load_dword v8,s[36:39],s58 offen offset:0 sc1 lds
buffer_store_dword v4,v3,s[36:39],s71 offen offset:0 sc0 sc1
s_add_u32 m0,s59,5120
s_nop 0
buffer_load_dword v8,s[36:39],s58 offen offset:0 sc1 lds
buffer_store_dword v4,v3,s[36:39],s71 offen offset:0 sc0 sc1
s_mov_b32 s83,0
Lg3_poll_%=:
s_add_u32 s78,s83,2
s_and_b32 s78,s78,3
s_lshl_b32 s78,s78,10
s_add_u32 s78,s78,4096
s_add_u32 m0,s59,s78
s_nop 0
buffer_load_dword v8,s[36:39],s58 offen offset:0 sc1 lds
s_and_b32 s78,s83,3
s_lshl_b32 s78,s78,10
v_add_u32 v94,s78,v6
s_waitcnt vmcnt(4)
s_barrier
ds_read_b32 v7,v94 offset:4096
buffer_store_dword v4,v3,s[36:39],s70 offen offset:0 sc0 sc1
s_mov_b32 s70,s71
s_mov_b32 s71,s72
s_mov_b32 s72,0x7fff0000
s_add_u32 s83,s83,1
s_add_u32 s82,s82,1
L 0
v_cmp_eq_u32 vcc,s53,v7
s_nop 0
s_cmp_eq_u32 vcc_lo,-1
s_cbranch_scc1 Lg3_go_%=
s_cmp_lt_u32 s82,20000
s_cbranch_scc1 Lg3_poll_%=
Lg3_go_%=:
buffer_load_dword v114,v11,s[36:39],s73 offen offset:0 sc1
s_add_u32 s73,s73,s64
s_add_u32 m0,s57,0
s_add_u32 s56,s55,0
D 0,56,0
D 0,56,1024
D 0,56,2048
D 0,56,3072
s_add_u32 s58,s58,s61
s_add_u32 m0,s59,2048
s_nop 0
buffer_load_dword v8,s[36:39],s58 offen offset:0 sc1 lds
s_add_u32 m0,s57,16384
s_add_u32 s56,s55,16384
D 0,56,0
D 0,56,1024
D 0,56,2048
D 0,56,3072
s_add_u32 s55,s55,s60
s_sub_u32 s57,s84,s57
s_add_u32 s54,s54,s46
s_waitcnt vmcnt(4)
s_barrier
buffer_store_dword v4,v3,s[36:39],s70 offen offset:0 sc0 sc1
s_min_u32 s71,s71,s72
buffer_store_dword v4,v3,s[36:39],s71 offen offset:0 sc0 sc1
s_mov_b32 s70,0x7fff0000
s_mov_b32 s71,0x7fff0000
s_mov_b32 s72,0x7fff0000
s_cmp_eq_u32 s80,1
buffer_store_dword v4,v3,s[36:39],s71 offen offset:0 sc0 sc1
s_cbranch_scc1 Lg3_st1_%=
s_branch Lg3_st0_%=
Lg3_gox_%=:
s_add_u32 m0,s59,2048
s_nop 0
buffer_load_dword v8,s[36:39],s58 offen offset:0 sc1 lds
s_waitcnt vmcnt(0)
s_barrier
buffer_store_dword v4,v3,s[36:39],s70 offen offset:0 sc0 sc1
s_min_u32 s71,s71,s72
buffer_store_dword v4,v3,s[36:39],s71 offen offset:0 sc0 sc1
s_mov_b32 s70,0x7fff0000
s_mov_b32 s71,0x7fff0000
s_mov_b32 s72,0x7fff0000
s_cmp_eq_u32 s80,1
s_cbranch_scc1 Lg3_st1_%=
Lg3_st0_%=:
ds_read_b32 v7,v6 offset:2048
R 42,1,0
R 46,1,1024
R 50,1,2048
R 54,1,3072
R 58,1,4096
L 5
v_cmp_eq_u32 vcc,s53,v7
L 3
s_cmp_eq_u32 vcc_lo,-1
s_cbranch_scc1 Lg3_F0_%=
s_branch Lg3_L0_%=
Lg3_st1_%=:
ds_read_b32 v7,v6 offset:2048
R 42,2,0
R 46,2,1024
R 50,2,2048
R 54,2,3072
R 58,2,4096
L 5
v_cmp_eq_u32 vcc,s53,v7
L 3
s_cmp_eq_u32 vcc_lo,-1
s_cbranch_scc1 Lg3_F1_%=
s_branch Lg3_L1_%=
Lg3_F1_%=:
s_cmp_eq_u32 s44,1
s_cbranch_scc1 Lg3_F1w1_%=
s_cmp_eq_u32 s44,2
s_cbranch_scc1 Lg3_F1w2_%=
s_cmp_eq_u32 s44,3
s_cbranch_scc1 Lg3_F1w3_%=
s_branch Lg3_F1w0_%=
Lg3_F0_%=:
s_cmp_eq_u32 s44,1
s_cbranch_scc1 Lg3_F0w1_%=
s_cmp_eq_u32 s44,2
s_cbranch_scc1 Lg3_F0w2_%=
s_cmp_eq_u32 s44,3
s_cbranch_scc1 Lg3_F0w3_%=
Lg3_F0w0_%=:
v_mfma_f32_16x16x32_bf16 v[26:29],a[8:11],v[42:45],0
s_add_u32 m0,s57,0
R 62,1,5120
v_add_f32_dpp v115,v114,v114 quad_perm:[1,0,3,2] row_mask:0xf bank_mask:0xf
M 26,a[20:23],46
buffer_load_dword v114,v11,s[36:39],s73 offen offset:0 sc1
v_mfma_f32_16x16x32_bf16 v[22:25],a[4:7],v[42:45],0
s_add_u32 s56,s55,0
v_add_f32_dpp v115,v115,v115 quad_perm:[2,3,0,1] row_mask:0xf bank_mask:0xf
v_fma_f32 v30,-v78,v82,v30
M 22,a[16:19],46
s_add_u32 s58,s58,s61
R 66,1,6144
v_fma_f32 v31,-v78,v83,v31
v_mfma_f32_16x16x32_bf16 v[18:21],a[0:3],v[42:45],0
D 0,56,0
M 18,a[12:15],46
L 3
v_add_f32_dpp v118,v115,v115 row_half_mirror row_mask:0xf bank_mask:0xf
v_fma_f32 v32,-v78,v84,v32
M 18,a[24:27],50
s_mov_b32 s66,s65
R 70,1,7168
v_fma_f32 v33,-v78,v85,v33
M 18,a[36:39],54
s_add_u32 s65,s65,s62
v_mov_b32_dpp v115,v118 row_ror:8 row_mask:0xf bank_mask:0xf
v_fma_f32 v115,-v118,v118,v115
M 22,a[28:31],50
s_add_u32 s54,s54,s46
v_max_f32 v115,s87,v115
v_mul_f32 v118,v15,v118
M 22,a[40:43],54
s_add_u32 s73,s73,s64
R 42,1,8192
v_mul_f32 v30,v79,v30
M 26,a[32:35],50
v_rsq_f32 v116,v115
M 26,a[44:47],54
L 3
D 0,56,1024
M 26,a[56:59],58
R 46,1,9216
v_fma_f32 v118,v116,v16,v118
M 26,a[68:71],62
ds_write_b32 v12,v118 offset:0
M 22,a[52:55],58
v_mul_f32 v31,v79,v31
v_mul_f32 v32,v79,v32
M 22,a[64:67],62
R 50,1,10240
v_mul_f32 v33,v79,v33
M 18,a[48:51],58
v_fma_f32 v94,v30,v30,s88
v_fma_f32 v95,v31,v31,s88
M 18,a[60:63],62
L 4
v_fma_f32 v96,v32,v32,s88
v_fma_f32 v97,v33,v33,s88
M 18,a[72:75],66
R 54,1,11264
v_mul_f32 v94,v30,v94
M 18,a[84:87],70
v_exp_f32 v94,v94
M 22,a[76:79],66
v_mul_f32 v95,v31,v95
v_mul_f32 v96,v32,v96
M 22,a[88:91],70
R 58,1,12288
v_mul_f32 v97,v33,v97
M 26,a[80:83],66
D 0,56,2048
M 26,a[92:95],70
L 4
v_exp_f32 v95,v95
M 26,a[104:107],42
R 62,1,13312
v_fma_f32 v94,v94,s89,s89
M 26,a[116:119],46
v_exp_f32 v96,v96
)"
R"(M 22,a[100:103],42
v_exp_f32 v97,v97
M 22,a[112:115],46
R 66,1,14336
v_fma_f32 v95,v95,s89,s89
M 18,a[96:99],42
v_rcp_f32 v94,v94
M 18,a[108:111],46
L 3
v_rcp_f32 v95,v95
M 18,a[120:123],50
R 70,1,15360
v_fma_f32 v96,v96,s89,s89
M 18,a[132:135],54
s_waitcnt vmcnt(7)
D 0,56,3072
M 22,a[124:127],50
s_barrier
v_rcp_f32 v96,v96
M 22,a[136:139],54
s_add_u32 m0,s59,0
R 42,1,16384
v_fma_f32 v97,v97,s89,s89
M 26,a[128:131],50
s_add_u32 s56,s55,16384
v_rcp_f32 v97,v97
M 26,a[140:143],54
L 3
v_mul_f32 v30,v30,v94
v_mul_f32 v31,v31,v95
M 26,a[152:155],58
s_add_u32 s55,s55,s60
R 46,1,17408
v_mul_f32 v32,v32,v96
M 26,a[164:167],62
v_mul_f32 v33,v33,v97
v_cvt_pk_bf16_f32 v106,v30,v31
M 22,a[148:151],58
v_cvt_pk_bf16_f32 v107,v32,v33
v_fma_f32 v34,-v78,v86,v34
M 22,a[160:163],62
R 50,1,18432
v_fma_f32 v35,-v78,v87,v35
v_fma_f32 v36,-v78,v88,v36
M 18,a[144:147],58
buffer_load_dword v8,s[36:39],s58 offen offset:0 sc1 lds
M 18,a[156:159],62
L 3
v_fma_f32 v37,-v78,v89,v37
v_mul_f32 v34,v79,v34
M 18,a[168:171],66
s_add_u32 m0,s57,16384
R 54,1,19456
v_mul_f32 v35,v79,v35
M 18,a[180:183],70
s_sub_u32 s57,s84,s57
v_mul_f32 v36,v79,v36
v_mul_f32 v37,v79,v37
M 22,a[172:175],66
v_fma_f32 v98,v34,v34,s88
v_fma_f32 v99,v35,v35,s88
M 22,a[184:187],70
R 58,1,20480
v_fma_f32 v100,v36,v36,s88
M 26,a[176:179],66
D 0,56,0
M 26,a[188:191],70
L 3
v_fma_f32 v101,v37,v37,s88
v_mul_f32 v98,v34,v98
M 26,a[200:203],42
R 62,1,21504
v_mul_f32 v99,v35,v99
M 26,a[212:215],46
v_exp_f32 v98,v98
M 22,a[196:199],42
v_exp_f32 v99,v99
M 22,a[208:211],46
R 66,1,22528
v_mul_f32 v100,v36,v100
M 18,a[192:195],42
v_exp_f32 v100,v100
M 18,a[204:207],46
L 3
v_mul_f32 v101,v37,v101
v_fma_f32 v98,v98,s89,s89
M 18,a[216:219],50
R 70,1,23552
v_fma_f32 v99,v99,s89,s89
M 18,a[228:231],54
D 0,56,1024
M 22,a[220:223],50
v_exp_f32 v101,v101
M 22,a[232:235],54
R 42,1,24576
v_fma_f32 v100,v100,s89,s89
M 26,a[224:227],50
v_rcp_f32 v98,v98
M 26,a[236:239],54
L 3
v_rcp_f32 v99,v99
M 26,a[248:251],58
R 46,1,25600
v_fma_f32 v101,v101,s89,s89
M 26,v[132:135],62
v_rcp_f32 v100,v100
M 22,a[244:247],58
v_rcp_f32 v101,v101
M 22,v[128:131],62
R 50,1,26624
v_mul_f32 v34,v34,v98
M 18,a[240:243],58
v_mul_f32 v35,v35,v99
v_mul_f32 v36,v36,v100
M 18,a[252:255],62
L 3
v_mul_f32 v37,v37,v101
v_cvt_pk_bf16_f32 v108,v34,v35
M 18,v[136:139],66
R 54,1,27648
v_cvt_pk_bf16_f32 v109,v36,v37
M 18,v[148:151],70
D 0,56,2048
M 22,v[140:143],66
v_fma_f32 v38,-v78,v90,v38
v_fma_f32 v39,-v78,v91,v39
M 22,v[152:155],70
R 58,1,28672
v_fma_f32 v40,-v78,v92,v40
M 26,v[144:147],66
v_fma_f32 v41,-v78,v93,v41
v_mul_f32 v38,v79,v38
M 26,v[156:159],70
L 3
v_mul_f32 v39,v79,v39
v_mul_f32 v40,v79,v40
M 26,v[168:171],42
R 62,1,29696
v_mul_f32 v41,v79,v41
M 26,v[180:183],46
v_fma_f32 v102,v38,v38,s88
v_fma_f32 v103,v39,v39,s88
M 22,v[164:167],42
v_fma_f32 v104,v40,v40,s88
v_fma_f32 v105,v41,v41,s88
M 22,v[176:179],46
R 66,1,30720
v_mul_f32 v102,v38,v102
M 18,v[160:163],42
v_exp_f32 v102,v102
M 18,v[172:175],46
L 3
D 0,56,3072
M 18,v[184:187],50
s_nop 0
R 70,1,31744
v_mul_f32 v103,v39,v103
M 18,v[196:199],54
s_waitcnt vmcnt(4)
v_exp_f32 v103,v103
v_mul_f32 v104,v40,v104
M 22,v[188:191],50
s_barrier
buffer_store_dword v4,v3,s[36:39],s70 offen offset:0 sc0 sc1
M 22,v[200:203],54
s_mov_b32 s70,s69
R 42,2,0
ds_read_b32 v7,v6 offset:0
M 26,v[192:195],50
s_mov_b32 s69,s68
ds_read_b64 v[74:75],v13 offset:0
v_mul_f32 v105,v41,v105
M 26,v[204:207],54
L 5
v_exp_f32 v104,v104
M 26,v[216:219],58
s_add_u32 s68,s68,s63
R 46,2,1024
v_fma_f32 v102,v102,s89,s89
M 26,v[228:231],62
v_exp_f32 v105,v105
M 22,v[212:215],58
buffer_store_dwordx4 v[106:109],v0,s[36:39],s67 offen offset:0 sc0 sc1
v_fma_f32 v103,v103,s89,s89
M 22,v[224:227],62
L 2
R 50,2,2048
v_fma_f32 v104,v104,s89,s89
M 18,v[208:211],58
v_rcp_f32 v102,v102
M 18,v[220:223],62
s_nop 0
v_fma_f32 v105,v105,s89,s89
v_mul_f32 v38,v38,v102
M 18,v[232:235],66
R 54,2,3072
v_cmp_eq_u32 vcc,s53,v7
M 18,v[244:247],70
v_rcp_f32 v103,v103
M 22,v[236:239],66
s_cmp_eq_u32 vcc_lo,-1
v_rcp_f32 v104,v104
v_rcp_f32 v105,v105
M 22,v[248:251],70
R 58,2,4096
v_mul_f32 v39,v39,v103
v_mul_f32 v40,v40,v104
v_mul_f32 v41,v41,v105
v_cvt_pk_bf16_f32 v110,v38,v39
v_cvt_pk_bf16_f32 v111,v40,v41
M 26,v[240:243],66
L 3
buffer_store_dwordx2 v[110:111],v10,s[36:39],s67 offen offset:0 sc0 sc1
M 26,v[252:255],70
s_cbranch_scc0 Lg3_K1_%=
Lg3_F1w0_%=:
v_mfma_f32_16x16x32_bf16 v[38:41],a[8:11],v[42:45],0
s_add_u32 m0,s57,0
R 62,2,5120
v_add_f32_dpp v115,v114,v114 quad_perm:[1,0,3,2] row_mask:0xf bank_mask:0xf
M 38,a[20:23],46
buffer_load_dword v114,v11,s[36:39],s73 offen offset:0 sc1
v_mfma_f32_16x16x32_bf16 v[34:37],a[4:7],v[42:45],0
s_add_u32 s56,s55,0
v_add_f32_dpp v115,v115,v115 quad_perm:[2,3,0,1] row_mask:0xf bank_mask:0xf
v_fma_f32 v18,-v74,v82,v18
M 34,a[16:19],46
s_add_u32 s58,s58,s61
R 66,2,6144
v_fma_f32 v19,-v74,v83,v19
v_mfma_f32_16x16x32_bf16 v[30:33],a[0:3],v[42:45],0
D 0,56,0
M 30,a[12:15],46
L 3
v_add_f32_dpp v118,v115,v115 row_half_mirror row_mask:0xf bank_mask:0xf
v_fma_f32 v20,-v74,v84,v20
M 30,a[24:27],50
s_mov_b32 s67,s65
R 70,2,7168
v_fma_f32 v21,-v74,v85,v21
M 30,a[36:39],54
s_add_u32 s65,s65,s62
v_mov_b32_dpp v115,v118 row_ror:8 row_mask:0xf bank_mask:0xf
v_fma_f32 v115,-v118,v118,v115
M 34,a[28:31],50
s_add_u32 s54,s54,s46
v_max_f32 v115,s87,v115
v_mul_f32 v118,v15,v118
M 34,a[40:43],54
s_add_u32 s73,s73,s64
R 42,2,8192
v_mul_f32 v18,v75,v18
M 38,a[32:35],50
v_rsq_f32 v116,v115
M 38,a[44:47],54
L 3
D 0,56,1024
M 38,a[56:59],58
R 46,2,9216
v_fma_f32 v118,v116,v16,v118
M 38,a[68:71],62
ds_write_b32 v12,v118 offset:2048
M 34,a[52:55],58
v_mul_f32 v19,v75,v19
v_mul_f32 v20,v75,v20
M 34,a[64:67],62
R 50,2,10240
v_mul_f32 v21,v75,v21
M 30,a[48:51],58
v_fma_f32 v94,v18,v18,s88
v_fma_f32 v95,v19,v19,s88
M 30,a[60:63],62
L 4
v_fma_f32 v96,v20,v20,s88
v_fma_f32 v97,v21,v21,s88
M 30,a[72:75],66
R 54,2,11264
v_mul_f32 v94,v18,v94
M 30,a[84:87],70
v_exp_f32 v94,v94
M 34,a[76:79],66
v_mul_f32 v95,v19,v95
v_mul_f32 v96,v20,v96
M 34,a[88:91],70
R 58,2,12288
v_mul_f32 v97,v21,v97
M 38,a[80:83],66
D 0,56,2048
M 38,a[92:95],70
L 4
v_exp_f32 v95,v95
M 38,a[104:107],42
R 62,2,13312
v_fma_f32 v94,v94,s89,s89
M 38,a[116:119],46
v_exp_f32 v96,v96
M 34,a[100:103],42
v_exp_f32 v97,v97
M 34,a[112:115],46
R 66,2,14336
v_fma_f32 v95,v95,s89,s89
M 30,a[96:99],42
v_rcp_f32 v94,v94
M 30,a[108:111],46
L 3
v_rcp_f32 v95,v95
M 30,a[120:123],50
R 70,2,15360
v_fma_f32 v96,v96,s89,s89
M 30,a[132:135],54
s_waitcnt vmcnt(7)
D 0,56,3072
M 34,a[124:127],50
s_barrier
v_rcp_f32 v96,v96
M 34,a[136:139],54
s_add_u32 m0,s59,1024
R 42,2,16384
v_fma_f32 v97,v97,s89,s89
M 38,a[128:131],50
s_add_u32 s56,s55,16384
v_rcp_f32 v97,v97
M 38,a[140:143],54
L 3
v_mul_f32 v18,v18,v94
v_mul_f32 v19,v19,v95
M 38,a[152:155],58
s_add_u32 s55,s55,s60
R 46,2,17408
v_mul_f32 v20,v20,v96
M 38,a[164:167],62
v_mul_f32 v21,v21,v97
v_cvt_pk_bf16_f32 v106,v18,v19
M 34,a[148:151],58
v_cvt_pk_bf16_f32 v107,v20,v21
v_fma_f32 v22,-v74,v86,v22
M 34,a[160:163],62
R 50,2,18432
v_fma_f32 v23,-v74,v87,v23
v_fma_f32 v24,-v74,v88,v24
M 30,a[144:147],58
buffer_load_dword v8,s[36:39],s58 offen offset:0 sc1 lds
M 30,a[156:159],62
L 3
v_fma_f32 v25,-v74,v89,v25
v_mul_f32 v22,v75,v22
M 30,a[168:171],66
s_add_u32 m0,s57,16384
R 54,2,19456
v_mul_f32 v23,v75,v23
M 30,a[180:183],70
s_sub_u32 s57,s84,s57
v_mul_f32 v24,v75,v24
v_mul_f32 v25,v75,v25
M 34,a[172:175],66
v_fma_f32 v98,v22,v22,s88
v_fma_f32 v99,v23,v23,s88
M 34,a[184:187],70
R 58,2,20480
v_fma_f32 v100,v24,v24,s88
M 38,a[176:179],66
D 0,56,0
M 38,a[188:191],70
L 3
v_fma_f32 v101,v25,v25,s88
v_mul_f32 v98,v22,v98
M 38,a[200:203],42
R 62,2,21504
v_mul_f32 v99,v23,v99
M 38,a[212:215],46
v_exp_f32 v98,v98
M 34,a[196:199],42
v_exp_f32 v99,v99
M 34,a[208:211],46
R 66,2,22528
v_mul_f32 v100,v24,v100
M 30,a[192:195],42
v_exp_f32 v100,v100
M 30,a[204:207],46
L 3
v_mul_f32 v101,v25,v101
v_fma_f32 v98,v98,s89,s89
M 30,a[216:219],50
R 70,2,23552
v_fma_f32 v99,v99,s89,s89
M 30,a[228:231],54
D 0,56,1024
M 34,a[220:223],50
v_exp_f32 v101,v101
M 34,a[232:235],54
R 42,2,24576
v_fma_f32 v100,v100,s89,s89
M 38,a[224:227],50
v_rcp_f32 v98,v98
M 38,a[236:239],54
L 3
v_rcp_f32 v99,v99
M 38,a[248:251],58
R 46,2,25600
v_fma_f32 v101,v101,s89,s89
M 38,v[132:135],62
v_rcp_f32 v100,v100
M 34,a[244:247],58
v_rcp_f32 v101,v101
M 34,v[128:131],62
R 50,2,26624
v_mul_f32 v22,v22,v98
M 30,a[240:243],58
v_mul_f32 v23,v23,v99
v_mul_f32 v24,v24,v100
M 30,a[252:255],62
L 3
v_mul_f32 v25,v25,v101
v_cvt_pk_bf16_f32 v108,v22,v23
M 30,v[136:139],66
R 54,2,27648
v_cvt_pk_bf16_f32 v109,v24,v25
M 30,v[148:151],70
D 0,56,2048
M 34,v[140:143],66
v_fma_f32 v26,-v74,v90,v26
v_fma_f32 v27,-v74,v91,v27
M 34,v[152:155],70
R 58,2,28672
v_fma_f32 v28,-v74,v92,v28
M 38,v[144:147],66
v_fma_f32 v29,-v74,v93,v29
v_mul_f32 v26,v75,v26
M 38,v[156:159],70
L 3
v_mul_f32 v27,v75,v27
v_mul_f32 v28,v75,v28
M 38,v[168:171],42
R 62,2,29696
v_mul_f32 v29,v75,v29
M 38,v[180:183],46
v_fma_f32 v102,v26,v26,s88
v_fma_f32 v103,v27,v27,s88
M 34,v[164:167],42
v_fma_f32 v104,v28,v28,s88
v_fma_f32 v105,v29,v29,s88
M 34,v[176:179],46
R 66,2,30720
v_mul_f32 v102,v26,v102
M 30,v[160:163],42
v_exp_f32 v102,v102
M 30,v[172:175],46
L 3
D 0,56,3072
M 30,v[184:187],50
s_nop 0
R 70,2,31744
v_mul_f32 v103,v27,v103
M 30,v[196:199],54
s_waitcnt vmcnt(4)
v_exp_f32 v103,v103
v_mul_f32 v104,v28,v104
M 34,v[188:191],50
s_barrier
buffer_store_dword v4,v3,s[36:39],s70 offen offset:0 sc0 sc1
M 34,v[200:203],54
s_mov_b32 s70,s69
R 42,1,0
ds_read_b32 v7,v6 offset:1024
M 38,v[192:195],50
s_mov_b32 s69,s68
ds_read_b64 v[78:79],v13 offset:2048
v_mul_f32 v105,v29,v105
M 38,v[204:207],54
L 5
v_exp_f32 v104,v104
M 38,v[216:219],58
s_add_u32 s68,s68,s63
R 46,1,1024
v_fma_f32 v102,v102,s89,s89
M 38,v[228:231],62
v_exp_f32 v105,v105
M 34,v[212:215],58
buffer_store_dwordx4 v[106:109],v0,s[36:39],s66 offen offset:0 sc0 sc1
v_fma_f32 v103,v103,s89,s89
M 34,v[224:227],62
L 2
R 50,1,2048
v_fma_f32 v104,v104,s89,s89
M 30,v[208:211],58
v_rcp_f32 v102,v102
M 30,v[220:223],62
s_nop 0
v_fma_f32 v105,v105,s89,s89
v_mul_f32 v26,v26,v102
M 30,v[232:235],66
R 54,1,3072
v_cmp_eq_u32 vcc,s53,v7
M 30,v[244:247],70
v_rcp_f32 v103,v103
M 34,v[236:239],66
s_cmp_eq_u32 vcc_lo,-1
v_rcp_f32 v104,v104
v_rcp_f32 v105,v105
M 34,v[248:251],70
)"
R"(R 58,1,4096
v_mul_f32 v27,v27,v103
v_mul_f32 v28,v28,v104
v_mul_f32 v29,v29,v105
v_cvt_pk_bf16_f32 v110,v26,v27
v_cvt_pk_bf16_f32 v111,v28,v29
M 38,v[240:243],66
L 3
buffer_store_dwordx2 v[110:111],v10,s[36:39],s66 offen offset:0 sc0 sc1
M 38,v[252:255],70
s_cbranch_scc1 Lg3_F0w0_%=
s_branch Lg3_K0_%=
Lg3_F0w1_%=:
v_mfma_f32_16x16x32_bf16 v[26:29],a[8:11],v[42:45],0
s_add_u32 m0,s57,0
R 62,1,5120
v_add_f32_dpp v115,v114,v114 quad_perm:[1,0,3,2] row_mask:0xf bank_mask:0xf
M 26,a[20:23],46
buffer_load_dword v114,v11,s[36:39],s73 offen offset:0 sc1
v_mfma_f32_16x16x32_bf16 v[22:25],a[4:7],v[42:45],0
s_add_u32 s56,s55,0
v_add_f32_dpp v115,v115,v115 quad_perm:[2,3,0,1] row_mask:0xf bank_mask:0xf
v_fma_f32 v30,-v78,v82,v30
M 22,a[16:19],46
s_add_u32 s58,s58,s61
R 66,1,6144
v_fma_f32 v31,-v78,v83,v31
v_mfma_f32_16x16x32_bf16 v[18:21],a[0:3],v[42:45],0
D 0,56,0
M 18,a[12:15],46
L 3
v_add_f32_dpp v118,v115,v115 row_half_mirror row_mask:0xf bank_mask:0xf
v_fma_f32 v32,-v78,v84,v32
M 18,a[24:27],50
s_mov_b32 s66,s65
R 70,1,7168
v_fma_f32 v33,-v78,v85,v33
M 18,a[36:39],54
s_add_u32 s65,s65,s62
v_mov_b32_dpp v115,v118 row_ror:8 row_mask:0xf bank_mask:0xf
v_fma_f32 v115,-v118,v118,v115
M 22,a[28:31],50
s_add_u32 s54,s54,s46
v_max_f32 v115,s87,v115
v_mul_f32 v118,v15,v118
M 22,a[40:43],54
s_add_u32 s73,s73,s64
R 42,1,8192
v_mul_f32 v30,v79,v30
M 26,a[32:35],50
v_rsq_f32 v116,v115
M 26,a[44:47],54
L 3
D 0,56,1024
M 26,a[56:59],58
R 46,1,9216
v_fma_f32 v118,v116,v16,v118
M 26,a[68:71],62
ds_write_b32 v12,v118 offset:0
M 22,a[52:55],58
v_mul_f32 v31,v79,v31
v_mul_f32 v32,v79,v32
M 22,a[64:67],62
R 50,1,10240
v_mul_f32 v33,v79,v33
M 18,a[48:51],58
v_fma_f32 v94,v30,v30,s88
v_fma_f32 v95,v31,v31,s88
M 18,a[60:63],62
L 4
v_fma_f32 v96,v32,v32,s88
v_fma_f32 v97,v33,v33,s88
M 18,a[72:75],66
R 54,1,11264
v_mul_f32 v94,v30,v94
M 18,a[84:87],70
v_exp_f32 v94,v94
M 22,a[76:79],66
v_mul_f32 v95,v31,v95
v_mul_f32 v96,v32,v96
M 22,a[88:91],70
R 58,1,12288
v_mul_f32 v97,v33,v97
M 26,a[80:83],66
D 0,56,2048
M 26,a[92:95],70
L 4
v_exp_f32 v95,v95
M 26,a[104:107],42
R 62,1,13312
v_fma_f32 v94,v94,s89,s89
M 26,a[116:119],46
v_exp_f32 v96,v96
M 22,a[100:103],42
v_exp_f32 v97,v97
M 22,a[112:115],46
R 66,1,14336
v_fma_f32 v95,v95,s89,s89
M 18,a[96:99],42
v_rcp_f32 v94,v94
M 18,a[108:111],46
s_waitcnt vmcnt(7) lgkmcnt(3)
v_rcp_f32 v95,v95
M 18,a[120:123],50
s_barrier
R 70,1,15360
v_fma_f32 v96,v96,s89,s89
M 18,a[132:135],54
D 0,56,3072
M 22,a[124:127],50
s_add_u32 m0,s59,0
v_rcp_f32 v96,v96
M 22,a[136:139],54
s_add_u32 s56,s55,16384
R 42,1,16384
v_fma_f32 v97,v97,s89,s89
M 26,a[128:131],50
s_add_u32 s55,s55,s60
v_rcp_f32 v97,v97
M 26,a[140:143],54
L 3
v_mul_f32 v30,v30,v94
v_mul_f32 v31,v31,v95
M 26,a[152:155],58
R 46,1,17408
v_mul_f32 v32,v32,v96
M 26,a[164:167],62
v_mul_f32 v33,v33,v97
v_cvt_pk_bf16_f32 v106,v30,v31
M 22,a[148:151],58
v_cvt_pk_bf16_f32 v107,v32,v33
v_fma_f32 v34,-v78,v86,v34
M 22,a[160:163],62
R 50,1,18432
v_fma_f32 v35,-v78,v87,v35
v_fma_f32 v36,-v78,v88,v36
M 18,a[144:147],58
buffer_load_dword v8,s[36:39],s58 offen offset:0 sc1 lds
M 18,a[156:159],62
L 3
v_fma_f32 v37,-v78,v89,v37
v_mul_f32 v34,v79,v34
M 18,a[168:171],66
s_add_u32 m0,s57,16384
R 54,1,19456
v_mul_f32 v35,v79,v35
M 18,a[180:183],70
s_sub_u32 s57,s84,s57
v_mul_f32 v36,v79,v36
v_mul_f32 v37,v79,v37
M 22,a[172:175],66
v_fma_f32 v98,v34,v34,s88
v_fma_f32 v99,v35,v35,s88
M 22,a[184:187],70
R 58,1,20480
v_fma_f32 v100,v36,v36,s88
M 26,a[176:179],66
D 0,56,0
M 26,a[188:191],70
L 3
v_fma_f32 v101,v37,v37,s88
v_mul_f32 v98,v34,v98
M 26,a[200:203],42
R 62,1,21504
v_mul_f32 v99,v35,v99
M 26,a[212:215],46
v_exp_f32 v98,v98
M 22,a[196:199],42
v_exp_f32 v99,v99
M 22,a[208:211],46
R 66,1,22528
v_mul_f32 v100,v36,v100
M 18,a[192:195],42
v_exp_f32 v100,v100
M 18,a[204:207],46
L 3
v_mul_f32 v101,v37,v101
v_fma_f32 v98,v98,s89,s89
M 18,a[216:219],50
R 70,1,23552
v_fma_f32 v99,v99,s89,s89
M 18,a[228:231],54
D 0,56,1024
M 22,a[220:223],50
v_exp_f32 v101,v101
M 22,a[232:235],54
R 42,1,24576
v_fma_f32 v100,v100,s89,s89
M 26,a[224:227],50
v_rcp_f32 v98,v98
M 26,a[236:239],54
L 3
v_rcp_f32 v99,v99
M 26,a[248:251],58
R 46,1,25600
v_fma_f32 v101,v101,s89,s89
M 26,v[132:135],62
v_rcp_f32 v100,v100
M 22,a[244:247],58
v_rcp_f32 v101,v101
M 22,v[128:131],62
R 50,1,26624
v_mul_f32 v34,v34,v98
M 18,a[240:243],58
v_mul_f32 v35,v35,v99
v_mul_f32 v36,v36,v100
M 18,a[252:255],62
L 3
v_mul_f32 v37,v37,v101
v_cvt_pk_bf16_f32 v108,v34,v35
M 18,v[136:139],66
R 54,1,27648
v_cvt_pk_bf16_f32 v109,v36,v37
M 18,v[148:151],70
D 0,56,2048
M 22,v[140:143],66
v_fma_f32 v38,-v78,v90,v38
v_fma_f32 v39,-v78,v91,v39
M 22,v[152:155],70
R 58,1,28672
v_fma_f32 v40,-v78,v92,v40
M 26,v[144:147],66
v_fma_f32 v41,-v78,v93,v41
v_mul_f32 v38,v79,v38
M 26,v[156:159],70
L 3
v_mul_f32 v39,v79,v39
v_mul_f32 v40,v79,v40
M 26,v[168:171],42
R 62,1,29696
v_mul_f32 v41,v79,v41
M 26,v[180:183],46
v_fma_f32 v102,v38,v38,s88
v_fma_f32 v103,v39,v39,s88
M 22,v[164:167],42
v_fma_f32 v104,v40,v40,s88
v_fma_f32 v105,v41,v41,s88
M 22,v[176:179],46
R 66,1,30720
v_mul_f32 v102,v38,v102
M 18,v[160:163],42
s_nop 0
v_exp_f32 v102,v102
M 18,v[172:175],46
s_waitcnt vmcnt(3) lgkmcnt(3)
D 0,56,3072
M 18,v[184:187],50
s_barrier
R 70,1,31744
ds_read_b32 v7,v6 offset:0
M 18,v[196:199],54
buffer_store_dword v4,v3,s[36:39],s70 offen offset:0 sc0 sc1
v_mul_f32 v103,v39,v103
M 22,v[188:191],50
s_mov_b32 s70,s69
ds_read_b64 v[74:75],v13 offset:0
v_mul_f32 v104,v40,v104
M 22,v[200:203],54
s_mov_b32 s69,s68
R 42,2,0
v_mul_f32 v105,v41,v105
M 26,v[192:195],50
s_add_u32 s68,s68,s63
v_exp_f32 v103,v103
M 26,v[204:207],54
L 5
v_exp_f32 v104,v104
M 26,v[216:219],58
L 2
R 46,2,1024
v_fma_f32 v102,v102,s89,s89
M 26,v[228:231],62
v_exp_f32 v105,v105
M 22,v[212:215],58
buffer_store_dwordx4 v[106:109],v0,s[36:39],s67 offen offset:0 sc0 sc1
v_fma_f32 v103,v103,s89,s89
M 22,v[224:227],62
R 50,2,2048
v_fma_f32 v104,v104,s89,s89
M 18,v[208:211],58
v_rcp_f32 v102,v102
M 18,v[220:223],62
s_nop 0
v_fma_f32 v105,v105,s89,s89
v_mul_f32 v38,v38,v102
M 18,v[232:235],66
R 54,2,3072
v_cmp_eq_u32 vcc,s53,v7
M 18,v[244:247],70
v_rcp_f32 v103,v103
M 22,v[236:239],66
s_cmp_eq_u32 vcc_lo,-1
v_rcp_f32 v104,v104
v_rcp_f32 v105,v105
M 22,v[248:251],70
R 58,2,4096
v_mul_f32 v39,v39,v103
v_mul_f32 v40,v40,v104
v_mul_f32 v41,v41,v105
v_cvt_pk_bf16_f32 v110,v38,v39
v_cvt_pk_bf16_f32 v111,v40,v41
M 26,v[240:243],66
L 3
buffer_store_dwordx2 v[110:111],v10,s[36:39],s67 offen offset:0 sc0 sc1
M 26,v[252:255],70
s_cbranch_scc0 Lg3_K1_%=
Lg3_F1w1_%=:
v_mfma_f32_16x16x32_bf16 v[38:41],a[8:11],v[42:45],0
s_add_u32 m0,s57,0
R 62,2,5120
v_add_f32_dpp v115,v114,v114 quad_perm:[1,0,3,2] row_mask:0xf bank_mask:0xf
M 38,a[20:23],46
buffer_load_dword v114,v11,s[36:39],s73 offen offset:0 sc1
v_mfma_f32_16x16x32_bf16 v[34:37],a[4:7],v[42:45],0
s_add_u32 s56,s55,0
v_add_f32_dpp v115,v115,v115 quad_perm:[2,3,0,1] row_mask:0xf bank_mask:0xf
v_fma_f32 v18,-v74,v82,v18
M 34,a[16:19],46
s_add_u32 s58,s58,s61
R 66,2,6144
v_fma_f32 v19,-v74,v83,v19
v_mfma_f32_16x16x32_bf16 v[30:33],a[0:3],v[42:45],0
D 0,56,0
M 30,a[12:15],46
L 3
v_add_f32_dpp v118,v115,v115 row_half_mirror row_mask:0xf bank_mask:0xf
v_fma_f32 v20,-v74,v84,v20
M 30,a[24:27],50
s_mov_b32 s67,s65
R 70,2,7168
v_fma_f32 v21,-v74,v85,v21
M 30,a[36:39],54
s_add_u32 s65,s65,s62
v_mov_b32_dpp v115,v118 row_ror:8 row_mask:0xf bank_mask:0xf
v_fma_f32 v115,-v118,v118,v115
M 34,a[28:31],50
s_add_u32 s54,s54,s46
v_max_f32 v115,s87,v115
v_mul_f32 v118,v15,v118
M 34,a[40:43],54
s_add_u32 s73,s73,s64
R 42,2,8192
v_mul_f32 v18,v75,v18
M 38,a[32:35],50
v_rsq_f32 v116,v115
M 38,a[44:47],54
L 3
D 0,56,1024
M 38,a[56:59],58
R 46,2,9216
v_fma_f32 v118,v116,v16,v118
M 38,a[68:71],62
ds_write_b32 v12,v118 offset:2048
M 34,a[52:55],58
v_mul_f32 v19,v75,v19
v_mul_f32 v20,v75,v20
M 34,a[64:67],62
R 50,2,10240
v_mul_f32 v21,v75,v21
M 30,a[48:51],58
v_fma_f32 v94,v18,v18,s88
v_fma_f32 v95,v19,v19,s88
M 30,a[60:63],62
L 4
v_fma_f32 v96,v20,v20,s88
v_fma_f32 v97,v21,v21,s88
M 30,a[72:75],66
R 54,2,11264
v_mul_f32 v94,v18,v94
M 30,a[84:87],70
v_exp_f32 v94,v94
M 34,a[76:79],66
v_mul_f32 v95,v19,v95
v_mul_f32 v96,v20,v96
M 34,a[88:91],70
R 58,2,12288
v_mul_f32 v97,v21,v97
M 38,a[80:83],66
D 0,56,2048
M 38,a[92:95],70
L 4
v_exp_f32 v95,v95
M 38,a[104:107],42
R 62,2,13312
v_fma_f32 v94,v94,s89,s89
M 38,a[116:119],46
v_exp_f32 v96,v96
M 34,a[100:103],42
v_exp_f32 v97,v97
M 34,a[112:115],46
R 66,2,14336
v_fma_f32 v95,v95,s89,s89
M 30,a[96:99],42
v_rcp_f32 v94,v94
M 30,a[108:111],46
s_waitcnt vmcnt(7) lgkmcnt(3)
v_rcp_f32 v95,v95
M 30,a[120:123],50
s_barrier
R 70,2,15360
v_fma_f32 v96,v96,s89,s89
M 30,a[132:135],54
D 0,56,3072
M 34,a[124:127],50
s_add_u32 m0,s59,1024
v_rcp_f32 v96,v96
M 34,a[136:139],54
s_add_u32 s56,s55,16384
R 42,2,16384
v_fma_f32 v97,v97,s89,s89
M 38,a[128:131],50
s_add_u32 s55,s55,s60
v_rcp_f32 v97,v97
M 38,a[140:143],54
L 3
v_mul_f32 v18,v18,v94
v_mul_f32 v19,v19,v95
M 38,a[152:155],58
R 46,2,17408
v_mul_f32 v20,v20,v96
M 38,a[164:167],62
v_mul_f32 v21,v21,v97
v_cvt_pk_bf16_f32 v106,v18,v19
M 34,a[148:151],58
v_cvt_pk_bf16_f32 v107,v20,v21
v_fma_f32 v22,-v74,v86,v22
M 34,a[160:163],62
R 50,2,18432
v_fma_f32 v23,-v74,v87,v23
v_fma_f32 v24,-v74,v88,v24
M 30,a[144:147],58
buffer_load_dword v8,s[36:39],s58 offen offset:0 sc1 lds
M 30,a[156:159],62
L 3
v_fma_f32 v25,-v74,v89,v25
v_mul_f32 v22,v75,v22
M 30,a[168:171],66
s_add_u32 m0,s57,16384
R 54,2,19456
v_mul_f32 v23,v75,v23
M 30,a[180:183],70
s_sub_u32 s57,s84,s57
v_mul_f32 v24,v75,v24
v_mul_f32 v25,v75,v25
M 34,a[172:175],66
v_fma_f32 v98,v22,v22,s88
v_fma_f32 v99,v23,v23,s88
M 34,a[184:187],70
R 58,2,20480
v_fma_f32 v100,v24,v24,s88
M 38,a[176:179],66
D 0,56,0
M 38,a[188:191],70
L 3
v_fma_f32 v101,v25,v25,s88
v_mul_f32 v98,v22,v98
M 38,a[200:203],42
R 62,2,21504
v_mul_f32 v99,v23,v99
M 38,a[212:215],46
v_exp_f32 v98,v98
M 34,a[196:199],42
v_exp_f32 v99,v99
M 34,a[208:211],46
R 66,2,22528
v_mul_f32 v100,v24,v100
M 30,a[192:195],42
v_exp_f32 v100,v100
M 30,a[204:207],46
L 3
v_mul_f32 v101,v25,v101
v_fma_f32 v98,v98,s89,s89
M 30,a[216:219],50
R 70,2,23552
v_fma_f32 v99,v99,s89,s89
M 30,a[228:231],54
D 0,56,1024
M 34,a[220:223],50
v_exp_f32 v101,v101
M 34,a[232:235],54
R 42,2,24576
v_fma_f32 v100,v100,s89,s89
M 38,a[224:227],50
v_rcp_f32 v98,v98
M 38,a[236:239],54
L 3
v_rcp_f32 v99,v99
M 38,a[248:251],58
R 46,2,25600
v_fma_f32 v101,v101,s89,s89
M 38,v[132:135],62
v_rcp_f32 v100,v100
M 34,a[244:247],58
v_rcp_f32 v101,v101
M 34,v[128:131],62
R 50,2,26624
v_mul_f32 v22,v22,v98
M 30,a[240:243],58
v_mul_f32 v23,v23,v99
v_mul_f32 v24,v24,v100
)"
R"(M 30,a[252:255],62
L 3
v_mul_f32 v25,v25,v101
v_cvt_pk_bf16_f32 v108,v22,v23
M 30,v[136:139],66
R 54,2,27648
v_cvt_pk_bf16_f32 v109,v24,v25
M 30,v[148:151],70
D 0,56,2048
M 34,v[140:143],66
v_fma_f32 v26,-v74,v90,v26
v_fma_f32 v27,-v74,v91,v27
M 34,v[152:155],70
R 58,2,28672
v_fma_f32 v28,-v74,v92,v28
M 38,v[144:147],66
v_fma_f32 v29,-v74,v93,v29
v_mul_f32 v26,v75,v26
M 38,v[156:159],70
L 3
v_mul_f32 v27,v75,v27
v_mul_f32 v28,v75,v28
M 38,v[168:171],42
R 62,2,29696
v_mul_f32 v29,v75,v29
M 38,v[180:183],46
v_fma_f32 v102,v26,v26,s88
v_fma_f32 v103,v27,v27,s88
M 34,v[164:167],42
v_fma_f32 v104,v28,v28,s88
v_fma_f32 v105,v29,v29,s88
M 34,v[176:179],46
R 66,2,30720
v_mul_f32 v102,v26,v102
M 30,v[160:163],42
s_nop 0
v_exp_f32 v102,v102
M 30,v[172:175],46
s_waitcnt vmcnt(3) lgkmcnt(3)
D 0,56,3072
M 30,v[184:187],50
s_barrier
R 70,2,31744
ds_read_b32 v7,v6 offset:1024
M 30,v[196:199],54
buffer_store_dword v4,v3,s[36:39],s70 offen offset:0 sc0 sc1
v_mul_f32 v103,v27,v103
M 34,v[188:191],50
s_mov_b32 s70,s69
ds_read_b64 v[78:79],v13 offset:2048
v_mul_f32 v104,v28,v104
M 34,v[200:203],54
s_mov_b32 s69,s68
R 42,1,0
v_mul_f32 v105,v29,v105
M 38,v[192:195],50
s_add_u32 s68,s68,s63
v_exp_f32 v103,v103
M 38,v[204:207],54
L 5
v_exp_f32 v104,v104
M 38,v[216:219],58
L 2
R 46,1,1024
v_fma_f32 v102,v102,s89,s89
M 38,v[228:231],62
v_exp_f32 v105,v105
M 34,v[212:215],58
buffer_store_dwordx4 v[106:109],v0,s[36:39],s66 offen offset:0 sc0 sc1
v_fma_f32 v103,v103,s89,s89
M 34,v[224:227],62
R 50,1,2048
v_fma_f32 v104,v104,s89,s89
M 30,v[208:211],58
v_rcp_f32 v102,v102
M 30,v[220:223],62
s_nop 0
v_fma_f32 v105,v105,s89,s89
v_mul_f32 v26,v26,v102
M 30,v[232:235],66
R 54,1,3072
v_cmp_eq_u32 vcc,s53,v7
M 30,v[244:247],70
v_rcp_f32 v103,v103
M 34,v[236:239],66
s_cmp_eq_u32 vcc_lo,-1
v_rcp_f32 v104,v104
v_rcp_f32 v105,v105
M 34,v[248:251],70
R 58,1,4096
v_mul_f32 v27,v27,v103
v_mul_f32 v28,v28,v104
v_mul_f32 v29,v29,v105
v_cvt_pk_bf16_f32 v110,v26,v27
v_cvt_pk_bf16_f32 v111,v28,v29
M 38,v[240:243],66
L 3
buffer_store_dwordx2 v[110:111],v10,s[36:39],s66 offen offset:0 sc0 sc1
M 38,v[252:255],70
s_cbranch_scc1 Lg3_F0w1_%=
s_branch Lg3_K0_%=
Lg3_F0w2_%=:
v_mfma_f32_16x16x32_bf16 v[26:29],a[8:11],v[42:45],0
s_add_u32 m0,s57,0
R 62,1,5120
v_add_f32_dpp v115,v114,v114 quad_perm:[1,0,3,2] row_mask:0xf bank_mask:0xf
M 26,a[20:23],46
buffer_load_dword v114,v11,s[36:39],s73 offen offset:0 sc1
v_mfma_f32_16x16x32_bf16 v[22:25],a[4:7],v[42:45],0
s_add_u32 s56,s55,0
v_add_f32_dpp v115,v115,v115 quad_perm:[2,3,0,1] row_mask:0xf bank_mask:0xf
v_fma_f32 v30,-v78,v82,v30
M 22,a[16:19],46
s_add_u32 s58,s58,s61
R 66,1,6144
v_fma_f32 v31,-v78,v83,v31
v_mfma_f32_16x16x32_bf16 v[18:21],a[0:3],v[42:45],0
D 0,56,0
M 18,a[12:15],46
L 3
v_add_f32_dpp v118,v115,v115 row_half_mirror row_mask:0xf bank_mask:0xf
v_fma_f32 v32,-v78,v84,v32
M 18,a[24:27],50
s_mov_b32 s66,s65
R 70,1,7168
v_fma_f32 v33,-v78,v85,v33
M 18,a[36:39],54
s_add_u32 s65,s65,s62
v_mov_b32_dpp v115,v118 row_ror:8 row_mask:0xf bank_mask:0xf
v_fma_f32 v115,-v118,v118,v115
M 22,a[28:31],50
s_add_u32 s54,s54,s46
v_max_f32 v115,s87,v115
v_mul_f32 v118,v15,v118
M 22,a[40:43],54
s_add_u32 s73,s73,s64
R 42,1,8192
v_mul_f32 v30,v79,v30
M 26,a[32:35],50
v_rsq_f32 v116,v115
M 26,a[44:47],54
L 3
D 0,56,1024
M 26,a[56:59],58
R 46,1,9216
v_fma_f32 v118,v116,v16,v118
M 26,a[68:71],62
ds_write_b32 v12,v118 offset:0
M 22,a[52:55],58
v_mul_f32 v31,v79,v31
v_mul_f32 v32,v79,v32
M 22,a[64:67],62
R 50,1,10240
v_mul_f32 v33,v79,v33
M 18,a[48:51],58
v_fma_f32 v94,v30,v30,s88
v_fma_f32 v95,v31,v31,s88
M 18,a[60:63],62
L 4
v_fma_f32 v96,v32,v32,s88
v_fma_f32 v97,v33,v33,s88
M 18,a[72:75],66
R 54,1,11264
v_mul_f32 v94,v30,v94
M 18,a[84:87],70
v_exp_f32 v94,v94
M 22,a[76:79],66
v_mul_f32 v95,v31,v95
v_mul_f32 v96,v32,v96
M 22,a[88:91],70
R 58,1,12288
v_mul_f32 v97,v33,v97
M 26,a[80:83],66
D 0,56,2048
M 26,a[92:95],70
L 4
v_exp_f32 v95,v95
M 26,a[104:107],42
R 62,1,13312
v_fma_f32 v94,v94,s89,s89
M 26,a[116:119],46
v_exp_f32 v96,v96
M 22,a[100:103],42
v_exp_f32 v97,v97
M 22,a[112:115],46
s_waitcnt vmcnt(6)
R 66,1,14336
v_fma_f32 v95,v95,s89,s89
M 18,a[96:99],42
s_barrier
v_rcp_f32 v94,v94
M 18,a[108:111],46
L 3
v_rcp_f32 v95,v95
M 18,a[120:123],50
R 70,1,15360
v_fma_f32 v96,v96,s89,s89
M 18,a[132:135],54
D 0,56,3072
M 22,a[124:127],50
s_add_u32 m0,s59,0
v_rcp_f32 v96,v96
M 22,a[136:139],54
s_add_u32 s56,s55,16384
R 42,1,16384
v_fma_f32 v97,v97,s89,s89
M 26,a[128:131],50
s_add_u32 s55,s55,s60
v_rcp_f32 v97,v97
M 26,a[140:143],54
L 3
v_mul_f32 v30,v30,v94
v_mul_f32 v31,v31,v95
M 26,a[152:155],58
R 46,1,17408
v_mul_f32 v32,v32,v96
M 26,a[164:167],62
v_mul_f32 v33,v33,v97
v_cvt_pk_bf16_f32 v106,v30,v31
M 22,a[148:151],58
v_cvt_pk_bf16_f32 v107,v32,v33
v_fma_f32 v34,-v78,v86,v34
M 22,a[160:163],62
R 50,1,18432
v_fma_f32 v35,-v78,v87,v35
v_fma_f32 v36,-v78,v88,v36
M 18,a[144:147],58
buffer_load_dword v8,s[36:39],s58 offen offset:0 sc1 lds
M 18,a[156:159],62
L 3
v_fma_f32 v37,-v78,v89,v37
v_mul_f32 v34,v79,v34
M 18,a[168:171],66
s_add_u32 m0,s57,16384
R 54,1,19456
v_mul_f32 v35,v79,v35
M 18,a[180:183],70
s_sub_u32 s57,s84,s57
v_mul_f32 v36,v79,v36
v_mul_f32 v37,v79,v37
M 22,a[172:175],66
v_fma_f32 v98,v34,v34,s88
v_fma_f32 v99,v35,v35,s88
M 22,a[184:187],70
R 58,1,20480
v_fma_f32 v100,v36,v36,s88
M 26,a[176:179],66
D 0,56,0
M 26,a[188:191],70
L 3
v_fma_f32 v101,v37,v37,s88
v_mul_f32 v98,v34,v98
M 26,a[200:203],42
R 62,1,21504
v_mul_f32 v99,v35,v99
M 26,a[212:215],46
v_exp_f32 v98,v98
M 22,a[196:199],42
v_exp_f32 v99,v99
M 22,a[208:211],46
R 66,1,22528
v_mul_f32 v100,v36,v100
M 18,a[192:195],42
v_exp_f32 v100,v100
M 18,a[204:207],46
L 3
v_mul_f32 v101,v37,v101
v_fma_f32 v98,v98,s89,s89
M 18,a[216:219],50
R 70,1,23552
v_fma_f32 v99,v99,s89,s89
M 18,a[228:231],54
D 0,56,1024
M 22,a[220:223],50
v_exp_f32 v101,v101
M 22,a[232:235],54
R 42,1,24576
v_fma_f32 v100,v100,s89,s89
M 26,a[224:227],50
v_rcp_f32 v98,v98
M 26,a[236:239],54
L 3
v_rcp_f32 v99,v99
M 26,a[248:251],58
R 46,1,25600
v_fma_f32 v101,v101,s89,s89
M 26,v[132:135],62
v_rcp_f32 v100,v100
M 22,a[244:247],58
v_rcp_f32 v101,v101
M 22,v[128:131],62
R 50,1,26624
v_mul_f32 v34,v34,v98
M 18,a[240:243],58
v_mul_f32 v35,v35,v99
v_mul_f32 v36,v36,v100
M 18,a[252:255],62
L 3
v_mul_f32 v37,v37,v101
v_cvt_pk_bf16_f32 v108,v34,v35
M 18,v[136:139],66
R 54,1,27648
v_cvt_pk_bf16_f32 v109,v36,v37
M 18,v[148:151],70
D 0,56,2048
M 22,v[140:143],66
v_fma_f32 v38,-v78,v90,v38
v_fma_f32 v39,-v78,v91,v39
M 22,v[152:155],70
R 58,1,28672
v_fma_f32 v40,-v78,v92,v40
M 26,v[144:147],66
v_fma_f32 v41,-v78,v93,v41
v_mul_f32 v38,v79,v38
M 26,v[156:159],70
L 3
v_mul_f32 v39,v79,v39
v_mul_f32 v40,v79,v40
M 26,v[168:171],42
R 62,1,29696
v_mul_f32 v41,v79,v41
M 26,v[180:183],46
v_fma_f32 v102,v38,v38,s88
v_fma_f32 v103,v39,v39,s88
M 22,v[164:167],42
s_nop 0
v_fma_f32 v104,v40,v40,s88
v_fma_f32 v105,v41,v41,s88
M 22,v[176:179],46
s_waitcnt vmcnt(3)
R 66,1,30720
v_mul_f32 v102,v38,v102
M 18,v[160:163],42
s_barrier
buffer_store_dword v4,v3,s[36:39],s70 offen offset:0 sc0 sc1
M 18,v[172:175],46
L 3
D 0,56,3072
M 18,v[184:187],50
s_mov_b32 s70,s69
R 70,1,31744
ds_read_b32 v7,v6 offset:0
M 18,v[196:199],54
s_mov_b32 s69,s68
ds_read_b64 v[74:75],v13 offset:0
v_exp_f32 v102,v102
M 22,v[188:191],50
s_add_u32 s68,s68,s63
v_mul_f32 v103,v39,v103
v_mul_f32 v104,v40,v104
M 22,v[200:203],54
R 42,2,0
v_mul_f32 v105,v41,v105
M 26,v[192:195],50
v_exp_f32 v103,v103
M 26,v[204:207],54
L 5
v_exp_f32 v104,v104
M 26,v[216:219],58
L 2
R 46,2,1024
v_fma_f32 v102,v102,s89,s89
M 26,v[228:231],62
v_exp_f32 v105,v105
M 22,v[212:215],58
buffer_store_dwordx4 v[106:109],v0,s[36:39],s67 offen offset:0 sc0 sc1
v_fma_f32 v103,v103,s89,s89
M 22,v[224:227],62
R 50,2,2048
v_fma_f32 v104,v104,s89,s89
M 18,v[208:211],58
v_rcp_f32 v102,v102
M 18,v[220:223],62
s_nop 0
v_fma_f32 v105,v105,s89,s89
v_mul_f32 v38,v38,v102
M 18,v[232:235],66
R 54,2,3072
v_cmp_eq_u32 vcc,s53,v7
M 18,v[244:247],70
v_rcp_f32 v103,v103
M 22,v[236:239],66
s_cmp_eq_u32 vcc_lo,-1
v_rcp_f32 v104,v104
v_rcp_f32 v105,v105
M 22,v[248:251],70
R 58,2,4096
v_mul_f32 v39,v39,v103
v_mul_f32 v40,v40,v104
v_mul_f32 v41,v41,v105
v_cvt_pk_bf16_f32 v110,v38,v39
v_cvt_pk_bf16_f32 v111,v40,v41
M 26,v[240:243],66
L 3
buffer_store_dwordx2 v[110:111],v10,s[36:39],s67 offen offset:0 sc0 sc1
M 26,v[252:255],70
s_cbranch_scc0 Lg3_K1_%=
Lg3_F1w2_%=:
v_mfma_f32_16x16x32_bf16 v[38:41],a[8:11],v[42:45],0
s_add_u32 m0,s57,0
R 62,2,5120
v_add_f32_dpp v115,v114,v114 quad_perm:[1,0,3,2] row_mask:0xf bank_mask:0xf
M 38,a[20:23],46
buffer_load_dword v114,v11,s[36:39],s73 offen offset:0 sc1
v_mfma_f32_16x16x32_bf16 v[34:37],a[4:7],v[42:45],0
s_add_u32 s56,s55,0
v_add_f32_dpp v115,v115,v115 quad_perm:[2,3,0,1] row_mask:0xf bank_mask:0xf
v_fma_f32 v18,-v74,v82,v18
M 34,a[16:19],46
s_add_u32 s58,s58,s61
R 66,2,6144
v_fma_f32 v19,-v74,v83,v19
v_mfma_f32_16x16x32_bf16 v[30:33],a[0:3],v[42:45],0
D 0,56,0
M 30,a[12:15],46
L 3
v_add_f32_dpp v118,v115,v115 row_half_mirror row_mask:0xf bank_mask:0xf
v_fma_f32 v20,-v74,v84,v20
M 30,a[24:27],50
s_mov_b32 s67,s65
R 70,2,7168
v_fma_f32 v21,-v74,v85,v21
M 30,a[36:39],54
s_add_u32 s65,s65,s62
v_mov_b32_dpp v115,v118 row_ror:8 row_mask:0xf bank_mask:0xf
v_fma_f32 v115,-v118,v118,v115
M 34,a[28:31],50
s_add_u32 s54,s54,s46
v_max_f32 v115,s87,v115
v_mul_f32 v118,v15,v118
M 34,a[40:43],54
s_add_u32 s73,s73,s64
R 42,2,8192
v_mul_f32 v18,v75,v18
M 38,a[32:35],50
v_rsq_f32 v116,v115
M 38,a[44:47],54
L 3
D 0,56,1024
M 38,a[56:59],58
R 46,2,9216
v_fma_f32 v118,v116,v16,v118
M 38,a[68:71],62
ds_write_b32 v12,v118 offset:2048
M 34,a[52:55],58
v_mul_f32 v19,v75,v19
v_mul_f32 v20,v75,v20
M 34,a[64:67],62
R 50,2,10240
v_mul_f32 v21,v75,v21
M 30,a[48:51],58
v_fma_f32 v94,v18,v18,s88
v_fma_f32 v95,v19,v19,s88
M 30,a[60:63],62
L 4
v_fma_f32 v96,v20,v20,s88
v_fma_f32 v97,v21,v21,s88
M 30,a[72:75],66
R 54,2,11264
v_mul_f32 v94,v18,v94
M 30,a[84:87],70
v_exp_f32 v94,v94
M 34,a[76:79],66
v_mul_f32 v95,v19,v95
v_mul_f32 v96,v20,v96
M 34,a[88:91],70
R 58,2,12288
v_mul_f32 v97,v21,v97
M 38,a[80:83],66
D 0,56,2048
M 38,a[92:95],70
L 4
v_exp_f32 v95,v95
M 38,a[104:107],42
R 62,2,13312
v_fma_f32 v94,v94,s89,s89
M 38,a[116:119],46
v_exp_f32 v96,v96
M 34,a[100:103],42
v_exp_f32 v97,v97
M 34,a[112:115],46
s_waitcnt vmcnt(6)
R 66,2,14336
v_fma_f32 v95,v95,s89,s89
M 30,a[96:99],42
s_barrier
v_rcp_f32 v94,v94
M 30,a[108:111],46
L 3
v_rcp_f32 v95,v95
M 30,a[120:123],50
R 70,2,15360
v_fma_f32 v96,v96,s89,s89
M 30,a[132:135],54
D 0,56,3072
M 34,a[124:127],50
s_add_u32 m0,s59,1024
v_rcp_f32 v96,v96
M 34,a[136:139],54
s_add_u32 s56,s55,16384
)"
R"(R 42,2,16384
v_fma_f32 v97,v97,s89,s89
M 38,a[128:131],50
s_add_u32 s55,s55,s60
v_rcp_f32 v97,v97
M 38,a[140:143],54
L 3
v_mul_f32 v18,v18,v94
v_mul_f32 v19,v19,v95
M 38,a[152:155],58
R 46,2,17408
v_mul_f32 v20,v20,v96
M 38,a[164:167],62
v_mul_f32 v21,v21,v97
v_cvt_pk_bf16_f32 v106,v18,v19
M 34,a[148:151],58
v_cvt_pk_bf16_f32 v107,v20,v21
v_fma_f32 v22,-v74,v86,v22
M 34,a[160:163],62
R 50,2,18432
v_fma_f32 v23,-v74,v87,v23
v_fma_f32 v24,-v74,v88,v24
M 30,a[144:147],58
buffer_load_dword v8,s[36:39],s58 offen offset:0 sc1 lds
M 30,a[156:159],62
L 3
v_fma_f32 v25,-v74,v89,v25
v_mul_f32 v22,v75,v22
M 30,a[168:171],66
s_add_u32 m0,s57,16384
R 54,2,19456
v_mul_f32 v23,v75,v23
M 30,a[180:183],70
s_sub_u32 s57,s84,s57
v_mul_f32 v24,v75,v24
v_mul_f32 v25,v75,v25
M 34,a[172:175],66
v_fma_f32 v98,v22,v22,s88
v_fma_f32 v99,v23,v23,s88
M 34,a[184:187],70
R 58,2,20480
v_fma_f32 v100,v24,v24,s88
M 38,a[176:179],66
D 0,56,0
M 38,a[188:191],70
L 3
v_fma_f32 v101,v25,v25,s88
v_mul_f32 v98,v22,v98
M 38,a[200:203],42
R 62,2,21504
v_mul_f32 v99,v23,v99
M 38,a[212:215],46
v_exp_f32 v98,v98
M 34,a[196:199],42
v_exp_f32 v99,v99
M 34,a[208:211],46
R 66,2,22528
v_mul_f32 v100,v24,v100
M 30,a[192:195],42
v_exp_f32 v100,v100
M 30,a[204:207],46
L 3
v_mul_f32 v101,v25,v101
v_fma_f32 v98,v98,s89,s89
M 30,a[216:219],50
R 70,2,23552
v_fma_f32 v99,v99,s89,s89
M 30,a[228:231],54
D 0,56,1024
M 34,a[220:223],50
v_exp_f32 v101,v101
M 34,a[232:235],54
R 42,2,24576
v_fma_f32 v100,v100,s89,s89
M 38,a[224:227],50
v_rcp_f32 v98,v98
M 38,a[236:239],54
L 3
v_rcp_f32 v99,v99
M 38,a[248:251],58
R 46,2,25600
v_fma_f32 v101,v101,s89,s89
M 38,v[132:135],62
v_rcp_f32 v100,v100
M 34,a[244:247],58
v_rcp_f32 v101,v101
M 34,v[128:131],62
R 50,2,26624
v_mul_f32 v22,v22,v98
M 30,a[240:243],58
v_mul_f32 v23,v23,v99
v_mul_f32 v24,v24,v100
M 30,a[252:255],62
L 3
v_mul_f32 v25,v25,v101
v_cvt_pk_bf16_f32 v108,v22,v23
M 30,v[136:139],66
R 54,2,27648
v_cvt_pk_bf16_f32 v109,v24,v25
M 30,v[148:151],70
D 0,56,2048
M 34,v[140:143],66
v_fma_f32 v26,-v74,v90,v26
v_fma_f32 v27,-v74,v91,v27
M 34,v[152:155],70
R 58,2,28672
v_fma_f32 v28,-v74,v92,v28
M 38,v[144:147],66
v_fma_f32 v29,-v74,v93,v29
v_mul_f32 v26,v75,v26
M 38,v[156:159],70
L 3
v_mul_f32 v27,v75,v27
v_mul_f32 v28,v75,v28
M 38,v[168:171],42
R 62,2,29696
v_mul_f32 v29,v75,v29
M 38,v[180:183],46
v_fma_f32 v102,v26,v26,s88
v_fma_f32 v103,v27,v27,s88
M 34,v[164:167],42
s_nop 0
v_fma_f32 v104,v28,v28,s88
v_fma_f32 v105,v29,v29,s88
M 34,v[176:179],46
s_waitcnt vmcnt(3)
R 66,2,30720
v_mul_f32 v102,v26,v102
M 30,v[160:163],42
s_barrier
buffer_store_dword v4,v3,s[36:39],s70 offen offset:0 sc0 sc1
M 30,v[172:175],46
L 3
D 0,56,3072
M 30,v[184:187],50
s_mov_b32 s70,s69
R 70,2,31744
ds_read_b32 v7,v6 offset:1024
M 30,v[196:199],54
s_mov_b32 s69,s68
ds_read_b64 v[78:79],v13 offset:2048
v_exp_f32 v102,v102
M 34,v[188:191],50
s_add_u32 s68,s68,s63
v_mul_f32 v103,v27,v103
v_mul_f32 v104,v28,v104
M 34,v[200:203],54
R 42,1,0
v_mul_f32 v105,v29,v105
M 38,v[192:195],50
v_exp_f32 v103,v103
M 38,v[204:207],54
L 5
v_exp_f32 v104,v104
M 38,v[216:219],58
L 2
R 46,1,1024
v_fma_f32 v102,v102,s89,s89
M 38,v[228:231],62
v_exp_f32 v105,v105
M 34,v[212:215],58
buffer_store_dwordx4 v[106:109],v0,s[36:39],s66 offen offset:0 sc0 sc1
v_fma_f32 v103,v103,s89,s89
M 34,v[224:227],62
R 50,1,2048
v_fma_f32 v104,v104,s89,s89
M 30,v[208:211],58
v_rcp_f32 v102,v102
M 30,v[220:223],62
s_nop 0
v_fma_f32 v105,v105,s89,s89
v_mul_f32 v26,v26,v102
M 30,v[232:235],66
R 54,1,3072
v_cmp_eq_u32 vcc,s53,v7
M 30,v[244:247],70
v_rcp_f32 v103,v103
M 34,v[236:239],66
s_cmp_eq_u32 vcc_lo,-1
v_rcp_f32 v104,v104
v_rcp_f32 v105,v105
M 34,v[248:251],70
R 58,1,4096
v_mul_f32 v27,v27,v103
v_mul_f32 v28,v28,v104
v_mul_f32 v29,v29,v105
v_cvt_pk_bf16_f32 v110,v26,v27
v_cvt_pk_bf16_f32 v111,v28,v29
M 38,v[240:243],66
L 3
buffer_store_dwordx2 v[110:111],v10,s[36:39],s66 offen offset:0 sc0 sc1
M 38,v[252:255],70
s_cbranch_scc1 Lg3_F0w2_%=
s_branch Lg3_K0_%=
Lg3_F0w3_%=:
v_mfma_f32_16x16x32_bf16 v[26:29],a[8:11],v[42:45],0
s_add_u32 m0,s57,0
R 62,1,5120
v_add_f32_dpp v115,v114,v114 quad_perm:[1,0,3,2] row_mask:0xf bank_mask:0xf
M 26,a[20:23],46
buffer_load_dword v114,v11,s[36:39],s73 offen offset:0 sc1
v_mfma_f32_16x16x32_bf16 v[22:25],a[4:7],v[42:45],0
s_add_u32 s56,s55,0
v_add_f32_dpp v115,v115,v115 quad_perm:[2,3,0,1] row_mask:0xf bank_mask:0xf
v_fma_f32 v30,-v78,v82,v30
M 22,a[16:19],46
s_add_u32 s58,s58,s61
R 66,1,6144
v_fma_f32 v31,-v78,v83,v31
v_mfma_f32_16x16x32_bf16 v[18:21],a[0:3],v[42:45],0
D 0,56,0
M 18,a[12:15],46
L 3
v_add_f32_dpp v118,v115,v115 row_half_mirror row_mask:0xf bank_mask:0xf
v_fma_f32 v32,-v78,v84,v32
M 18,a[24:27],50
s_mov_b32 s66,s65
R 70,1,7168
v_fma_f32 v33,-v78,v85,v33
M 18,a[36:39],54
s_add_u32 s65,s65,s62
v_mov_b32_dpp v115,v118 row_ror:8 row_mask:0xf bank_mask:0xf
v_fma_f32 v115,-v118,v118,v115
M 22,a[28:31],50
s_add_u32 s54,s54,s46
v_max_f32 v115,s87,v115
v_mul_f32 v118,v15,v118
M 22,a[40:43],54
s_add_u32 s73,s73,s64
R 42,1,8192
v_mul_f32 v30,v79,v30
M 26,a[32:35],50
v_rsq_f32 v116,v115
M 26,a[44:47],54
L 3
D 0,56,1024
M 26,a[56:59],58
R 46,1,9216
v_fma_f32 v118,v116,v16,v118
M 26,a[68:71],62
ds_write_b32 v12,v118 offset:0
M 22,a[52:55],58
v_mul_f32 v31,v79,v31
v_mul_f32 v32,v79,v32
M 22,a[64:67],62
R 50,1,10240
v_mul_f32 v33,v79,v33
M 18,a[48:51],58
v_fma_f32 v94,v30,v30,s88
v_fma_f32 v95,v31,v31,s88
M 18,a[60:63],62
L 4
v_fma_f32 v96,v32,v32,s88
v_fma_f32 v97,v33,v33,s88
M 18,a[72:75],66
R 54,1,11264
v_mul_f32 v94,v30,v94
M 18,a[84:87],70
v_exp_f32 v94,v94
M 22,a[76:79],66
v_mul_f32 v95,v31,v95
v_mul_f32 v96,v32,v96
M 22,a[88:91],70
R 58,1,12288
v_mul_f32 v97,v33,v97
M 26,a[80:83],66
D 0,56,2048
M 26,a[92:95],70
L 4
v_exp_f32 v95,v95
M 26,a[104:107],42
R 62,1,13312
v_fma_f32 v94,v94,s89,s89
M 26,a[116:119],46
s_waitcnt vmcnt(6)
v_exp_f32 v96,v96
M 22,a[100:103],42
s_barrier
v_exp_f32 v97,v97
M 22,a[112:115],46
R 66,1,14336
v_fma_f32 v95,v95,s89,s89
M 18,a[96:99],42
v_rcp_f32 v94,v94
M 18,a[108:111],46
L 3
v_rcp_f32 v95,v95
M 18,a[120:123],50
R 70,1,15360
v_fma_f32 v96,v96,s89,s89
M 18,a[132:135],54
D 0,56,3072
M 22,a[124:127],50
s_add_u32 m0,s59,0
v_rcp_f32 v96,v96
M 22,a[136:139],54
s_add_u32 s56,s55,16384
R 42,1,16384
v_fma_f32 v97,v97,s89,s89
M 26,a[128:131],50
s_add_u32 s55,s55,s60
v_rcp_f32 v97,v97
M 26,a[140:143],54
L 3
v_mul_f32 v30,v30,v94
v_mul_f32 v31,v31,v95
M 26,a[152:155],58
R 46,1,17408
v_mul_f32 v32,v32,v96
M 26,a[164:167],62
v_mul_f32 v33,v33,v97
v_cvt_pk_bf16_f32 v106,v30,v31
M 22,a[148:151],58
v_cvt_pk_bf16_f32 v107,v32,v33
v_fma_f32 v34,-v78,v86,v34
M 22,a[160:163],62
R 50,1,18432
v_fma_f32 v35,-v78,v87,v35
v_fma_f32 v36,-v78,v88,v36
M 18,a[144:147],58
buffer_load_dword v8,s[36:39],s58 offen offset:0 sc1 lds
M 18,a[156:159],62
L 3
v_fma_f32 v37,-v78,v89,v37
v_mul_f32 v34,v79,v34
M 18,a[168:171],66
s_add_u32 m0,s57,16384
R 54,1,19456
v_mul_f32 v35,v79,v35
M 18,a[180:183],70
s_sub_u32 s57,s84,s57
v_mul_f32 v36,v79,v36
v_mul_f32 v37,v79,v37
M 22,a[172:175],66
v_fma_f32 v98,v34,v34,s88
v_fma_f32 v99,v35,v35,s88
M 22,a[184:187],70
R 58,1,20480
v_fma_f32 v100,v36,v36,s88
M 26,a[176:179],66
D 0,56,0
M 26,a[188:191],70
L 3
v_fma_f32 v101,v37,v37,s88
v_mul_f32 v98,v34,v98
M 26,a[200:203],42
R 62,1,21504
v_mul_f32 v99,v35,v99
M 26,a[212:215],46
v_exp_f32 v98,v98
M 22,a[196:199],42
v_exp_f32 v99,v99
M 22,a[208:211],46
R 66,1,22528
v_mul_f32 v100,v36,v100
M 18,a[192:195],42
v_exp_f32 v100,v100
M 18,a[204:207],46
L 3
v_mul_f32 v101,v37,v101
v_fma_f32 v98,v98,s89,s89
M 18,a[216:219],50
R 70,1,23552
v_fma_f32 v99,v99,s89,s89
M 18,a[228:231],54
D 0,56,1024
M 22,a[220:223],50
v_exp_f32 v101,v101
M 22,a[232:235],54
R 42,1,24576
v_fma_f32 v100,v100,s89,s89
M 26,a[224:227],50
v_rcp_f32 v98,v98
M 26,a[236:239],54
L 3
v_rcp_f32 v99,v99
M 26,a[248:251],58
R 46,1,25600
v_fma_f32 v101,v101,s89,s89
M 26,v[132:135],62
v_rcp_f32 v100,v100
M 22,a[244:247],58
v_rcp_f32 v101,v101
M 22,v[128:131],62
R 50,1,26624
v_mul_f32 v34,v34,v98
M 18,a[240:243],58
v_mul_f32 v35,v35,v99
v_mul_f32 v36,v36,v100
M 18,a[252:255],62
L 3
v_mul_f32 v37,v37,v101
v_cvt_pk_bf16_f32 v108,v34,v35
M 18,v[136:139],66
R 54,1,27648
v_cvt_pk_bf16_f32 v109,v36,v37
M 18,v[148:151],70
D 0,56,2048
M 22,v[140:143],66
v_fma_f32 v38,-v78,v90,v38
v_fma_f32 v39,-v78,v91,v39
M 22,v[152:155],70
R 58,1,28672
v_fma_f32 v40,-v78,v92,v40
M 26,v[144:147],66
v_fma_f32 v41,-v78,v93,v41
v_mul_f32 v38,v79,v38
M 26,v[156:159],70
L 3
v_mul_f32 v39,v79,v39
v_mul_f32 v40,v79,v40
M 26,v[168:171],42
s_nop 0
R 62,1,29696
v_mul_f32 v41,v79,v41
M 26,v[180:183],46
s_waitcnt vmcnt(3)
v_fma_f32 v102,v38,v38,s88
v_fma_f32 v103,v39,v39,s88
M 22,v[164:167],42
s_barrier
buffer_store_dword v4,v3,s[36:39],s70 offen offset:0 sc0 sc1
M 22,v[176:179],46
s_mov_b32 s70,s69
R 66,1,30720
ds_read_b32 v7,v6 offset:0
M 18,v[160:163],42
s_mov_b32 s69,s68
ds_read_b64 v[74:75],v13 offset:0
v_fma_f32 v104,v40,v40,s88
M 18,v[172:175],46
L 5
D 0,56,3072
M 18,v[184:187],50
s_add_u32 s68,s68,s63
R 70,1,31744
v_fma_f32 v105,v41,v41,s88
M 18,v[196:199],54
v_mul_f32 v102,v38,v102
v_exp_f32 v102,v102
M 22,v[188:191],50
v_mul_f32 v103,v39,v103
v_mul_f32 v104,v40,v104
M 22,v[200:203],54
L 2
R 42,2,0
v_mul_f32 v105,v41,v105
M 26,v[192:195],50
v_exp_f32 v103,v103
M 26,v[204:207],54
s_nop 0
v_exp_f32 v104,v104
M 26,v[216:219],58
R 46,2,1024
v_fma_f32 v102,v102,s89,s89
M 26,v[228:231],62
v_exp_f32 v105,v105
M 22,v[212:215],58
buffer_store_dwordx4 v[106:109],v0,s[36:39],s67 offen offset:0 sc0 sc1
v_fma_f32 v103,v103,s89,s89
M 22,v[224:227],62
R 50,2,2048
v_fma_f32 v104,v104,s89,s89
M 18,v[208:211],58
v_rcp_f32 v102,v102
M 18,v[220:223],62
L 3
v_fma_f32 v105,v105,s89,s89
v_mul_f32 v38,v38,v102
M 18,v[232:235],66
R 54,2,3072
v_cmp_eq_u32 vcc,s53,v7
M 18,v[244:247],70
v_rcp_f32 v103,v103
M 22,v[236:239],66
s_cmp_eq_u32 vcc_lo,-1
v_rcp_f32 v104,v104
v_rcp_f32 v105,v105
M 22,v[248:251],70
R 58,2,4096
v_mul_f32 v39,v39,v103
v_mul_f32 v40,v40,v104
v_mul_f32 v41,v41,v105
v_cvt_pk_bf16_f32 v110,v38,v39
v_cvt_pk_bf16_f32 v111,v40,v41
M 26,v[240:243],66
L 3
buffer_store_dwordx2 v[110:111],v10,s[36:39],s67 offen offset:0 sc0 sc1
M 26,v[252:255],70
s_cbranch_scc0 Lg3_K1_%=
Lg3_F1w3_%=:
v_mfma_f32_16x16x32_bf16 v[38:41],a[8:11],v[42:45],0
s_add_u32 m0,s57,0
R 62,2,5120
v_add_f32_dpp v115,v114,v114 quad_perm:[1,0,3,2] row_mask:0xf bank_mask:0xf
M 38,a[20:23],46
buffer_load_dword v114,v11,s[36:39],s73 offen offset:0 sc1
v_mfma_f32_16x16x32_bf16 v[34:37],a[4:7],v[42:45],0
s_add_u32 s56,s55,0
v_add_f32_dpp v115,v115,v115 quad_perm:[2,3,0,1] row_mask:0xf bank_mask:0xf
)"
R"(v_fma_f32 v18,-v74,v82,v18
M 34,a[16:19],46
s_add_u32 s58,s58,s61
R 66,2,6144
v_fma_f32 v19,-v74,v83,v19
v_mfma_f32_16x16x32_bf16 v[30:33],a[0:3],v[42:45],0
D 0,56,0
M 30,a[12:15],46
L 3
v_add_f32_dpp v118,v115,v115 row_half_mirror row_mask:0xf bank_mask:0xf
v_fma_f32 v20,-v74,v84,v20
M 30,a[24:27],50
s_mov_b32 s67,s65
R 70,2,7168
v_fma_f32 v21,-v74,v85,v21
M 30,a[36:39],54
s_add_u32 s65,s65,s62
v_mov_b32_dpp v115,v118 row_ror:8 row_mask:0xf bank_mask:0xf
v_fma_f32 v115,-v118,v118,v115
M 34,a[28:31],50
s_add_u32 s54,s54,s46
v_max_f32 v115,s87,v115
v_mul_f32 v118,v15,v118
M 34,a[40:43],54
s_add_u32 s73,s73,s64
R 42,2,8192
v_mul_f32 v18,v75,v18
M 38,a[32:35],50
v_rsq_f32 v116,v115
M 38,a[44:47],54
L 3
D 0,56,1024
M 38,a[56:59],58
R 46,2,9216
v_fma_f32 v118,v116,v16,v118
M 38,a[68:71],62
ds_write_b32 v12,v118 offset:2048
M 34,a[52:55],58
v_mul_f32 v19,v75,v19
v_mul_f32 v20,v75,v20
M 34,a[64:67],62
R 50,2,10240
v_mul_f32 v21,v75,v21
M 30,a[48:51],58
v_fma_f32 v94,v18,v18,s88
v_fma_f32 v95,v19,v19,s88
M 30,a[60:63],62
L 4
v_fma_f32 v96,v20,v20,s88
v_fma_f32 v97,v21,v21,s88
M 30,a[72:75],66
R 54,2,11264
v_mul_f32 v94,v18,v94
M 30,a[84:87],70
v_exp_f32 v94,v94
M 34,a[76:79],66
v_mul_f32 v95,v19,v95
v_mul_f32 v96,v20,v96
M 34,a[88:91],70
R 58,2,12288
v_mul_f32 v97,v21,v97
M 38,a[80:83],66
D 0,56,2048
M 38,a[92:95],70
L 4
v_exp_f32 v95,v95
M 38,a[104:107],42
R 62,2,13312
v_fma_f32 v94,v94,s89,s89
M 38,a[116:119],46
s_waitcnt vmcnt(6)
v_exp_f32 v96,v96
M 34,a[100:103],42
s_barrier
v_exp_f32 v97,v97
M 34,a[112:115],46
R 66,2,14336
v_fma_f32 v95,v95,s89,s89
M 30,a[96:99],42
v_rcp_f32 v94,v94
M 30,a[108:111],46
L 3
v_rcp_f32 v95,v95
M 30,a[120:123],50
R 70,2,15360
v_fma_f32 v96,v96,s89,s89
M 30,a[132:135],54
D 0,56,3072
M 34,a[124:127],50
s_add_u32 m0,s59,1024
v_rcp_f32 v96,v96
M 34,a[136:139],54
s_add_u32 s56,s55,16384
R 42,2,16384
v_fma_f32 v97,v97,s89,s89
M 38,a[128:131],50
s_add_u32 s55,s55,s60
v_rcp_f32 v97,v97
M 38,a[140:143],54
L 3
v_mul_f32 v18,v18,v94
v_mul_f32 v19,v19,v95
M 38,a[152:155],58
R 46,2,17408
v_mul_f32 v20,v20,v96
M 38,a[164:167],62
v_mul_f32 v21,v21,v97
v_cvt_pk_bf16_f32 v106,v18,v19
M 34,a[148:151],58
v_cvt_pk_bf16_f32 v107,v20,v21
v_fma_f32 v22,-v74,v86,v22
M 34,a[160:163],62
R 50,2,18432
v_fma_f32 v23,-v74,v87,v23
v_fma_f32 v24,-v74,v88,v24
M 30,a[144:147],58
buffer_load_dword v8,s[36:39],s58 offen offset:0 sc1 lds
M 30,a[156:159],62
L 3
v_fma_f32 v25,-v74,v89,v25
v_mul_f32 v22,v75,v22
M 30,a[168:171],66
s_add_u32 m0,s57,16384
R 54,2,19456
v_mul_f32 v23,v75,v23
M 30,a[180:183],70
s_sub_u32 s57,s84,s57
v_mul_f32 v24,v75,v24
v_mul_f32 v25,v75,v25
M 34,a[172:175],66
v_fma_f32 v98,v22,v22,s88
v_fma_f32 v99,v23,v23,s88
M 34,a[184:187],70
R 58,2,20480
v_fma_f32 v100,v24,v24,s88
M 38,a[176:179],66
D 0,56,0
M 38,a[188:191],70
L 3
v_fma_f32 v101,v25,v25,s88
v_mul_f32 v98,v22,v98
M 38,a[200:203],42
R 62,2,21504
v_mul_f32 v99,v23,v99
M 38,a[212:215],46
v_exp_f32 v98,v98
M 34,a[196:199],42
v_exp_f32 v99,v99
M 34,a[208:211],46
R 66,2,22528
v_mul_f32 v100,v24,v100
M 30,a[192:195],42
v_exp_f32 v100,v100
M 30,a[204:207],46
L 3
v_mul_f32 v101,v25,v101
v_fma_f32 v98,v98,s89,s89
M 30,a[216:219],50
R 70,2,23552
v_fma_f32 v99,v99,s89,s89
M 30,a[228:231],54
D 0,56,1024
M 34,a[220:223],50
v_exp_f32 v101,v101
M 34,a[232:235],54
R 42,2,24576
v_fma_f32 v100,v100,s89,s89
M 38,a[224:227],50
v_rcp_f32 v98,v98
M 38,a[236:239],54
L 3
v_rcp_f32 v99,v99
M 38,a[248:251],58
R 46,2,25600
v_fma_f32 v101,v101,s89,s89
M 38,v[132:135],62
v_rcp_f32 v100,v100
M 34,a[244:247],58
v_rcp_f32 v101,v101
M 34,v[128:131],62
R 50,2,26624
v_mul_f32 v22,v22,v98
M 30,a[240:243],58
v_mul_f32 v23,v23,v99
v_mul_f32 v24,v24,v100
M 30,a[252:255],62
L 3
v_mul_f32 v25,v25,v101
v_cvt_pk_bf16_f32 v108,v22,v23
M 30,v[136:139],66
R 54,2,27648
v_cvt_pk_bf16_f32 v109,v24,v25
M 30,v[148:151],70
D 0,56,2048
M 34,v[140:143],66
v_fma_f32 v26,-v74,v90,v26
v_fma_f32 v27,-v74,v91,v27
M 34,v[152:155],70
R 58,2,28672
v_fma_f32 v28,-v74,v92,v28
M 38,v[144:147],66
v_fma_f32 v29,-v74,v93,v29
v_mul_f32 v26,v75,v26
M 38,v[156:159],70
L 3
v_mul_f32 v27,v75,v27
v_mul_f32 v28,v75,v28
M 38,v[168:171],42
s_nop 0
R 62,2,29696
v_mul_f32 v29,v75,v29
M 38,v[180:183],46
s_waitcnt vmcnt(3)
v_fma_f32 v102,v26,v26,s88
v_fma_f32 v103,v27,v27,s88
M 34,v[164:167],42
s_barrier
buffer_store_dword v4,v3,s[36:39],s70 offen offset:0 sc0 sc1
M 34,v[176:179],46
s_mov_b32 s70,s69
R 66,2,30720
ds_read_b32 v7,v6 offset:1024
M 30,v[160:163],42
s_mov_b32 s69,s68
ds_read_b64 v[78:79],v13 offset:2048
v_fma_f32 v104,v28,v28,s88
M 30,v[172:175],46
L 5
D 0,56,3072
M 30,v[184:187],50
s_add_u32 s68,s68,s63
R 70,2,31744
v_fma_f32 v105,v29,v29,s88
M 30,v[196:199],54
v_mul_f32 v102,v26,v102
v_exp_f32 v102,v102
M 34,v[188:191],50
v_mul_f32 v103,v27,v103
v_mul_f32 v104,v28,v104
M 34,v[200:203],54
L 2
R 42,1,0
v_mul_f32 v105,v29,v105
M 38,v[192:195],50
v_exp_f32 v103,v103
M 38,v[204:207],54
s_nop 0
v_exp_f32 v104,v104
M 38,v[216:219],58
R 46,1,1024
v_fma_f32 v102,v102,s89,s89
M 38,v[228:231],62
v_exp_f32 v105,v105
M 34,v[212:215],58
buffer_store_dwordx4 v[106:109],v0,s[36:39],s66 offen offset:0 sc0 sc1
v_fma_f32 v103,v103,s89,s89
M 34,v[224:227],62
R 50,1,2048
v_fma_f32 v104,v104,s89,s89
M 30,v[208:211],58
v_rcp_f32 v102,v102
M 30,v[220:223],62
L 3
v_fma_f32 v105,v105,s89,s89
v_mul_f32 v26,v26,v102
M 30,v[232:235],66
R 54,1,3072
v_cmp_eq_u32 vcc,s53,v7
M 30,v[244:247],70
v_rcp_f32 v103,v103
M 34,v[236:239],66
s_cmp_eq_u32 vcc_lo,-1
v_rcp_f32 v104,v104
v_rcp_f32 v105,v105
M 34,v[248:251],70
R 58,1,4096
v_mul_f32 v27,v27,v103
v_mul_f32 v28,v28,v104
v_mul_f32 v29,v29,v105
v_cvt_pk_bf16_f32 v110,v26,v27
v_cvt_pk_bf16_f32 v111,v28,v29
M 38,v[240:243],66
L 3
buffer_store_dwordx2 v[110:111],v10,s[36:39],s66 offen offset:0 sc0 sc1
M 38,v[252:255],70
s_cbranch_scc1 Lg3_F0w3_%=
Lg3_K0_%=:
Lg3_L0_%=:
v_mfma_f32_16x16x32_bf16 v[26:29],a[8:11],v[42:45],0
s_add_u32 m0,s59,3072
R 62,1,5120
v_add_f32_dpp v115,v114,v114 quad_perm:[1,0,3,2] row_mask:0xf bank_mask:0xf
M 26,a[20:23],46
buffer_load_dword v8,s[36:39],s58 offen offset:0 sc1 lds
v_mfma_f32_16x16x32_bf16 v[22:25],a[4:7],v[42:45],0
s_mov_b32 s66,s65
v_add_f32_dpp v115,v115,v115 quad_perm:[2,3,0,1] row_mask:0xf bank_mask:0xf
v_fma_f32 v30,-v78,v82,v30
M 22,a[16:19],46
s_add_u32 s65,s65,s62
R 66,1,6144
v_fma_f32 v31,-v78,v83,v31
v_mfma_f32_16x16x32_bf16 v[18:21],a[0:3],v[42:45],0
v_add_f32_dpp v118,v115,v115 row_half_mirror row_mask:0xf bank_mask:0xf
v_fma_f32 v32,-v78,v84,v32
v_fma_f32 v33,-v78,v85,v33
M 18,a[12:15],46
L 3
v_mul_f32 v30,v79,v30
v_mul_f32 v31,v79,v31
M 18,a[24:27],50
R 70,1,7168
v_mov_b32_dpp v115,v118 row_ror:8 row_mask:0xf bank_mask:0xf
M 18,a[36:39],54
v_fma_f32 v115,-v118,v118,v115
v_max_f32 v115,s87,v115
M 22,a[28:31],50
v_rsq_f32 v116,v115
M 22,a[40:43],54
R 42,1,8192
v_mul_f32 v118,v15,v118
v_fma_f32 v118,v116,v16,v118
M 26,a[32:35],50
ds_write_b32 v12,v118 offset:0
M 26,a[44:47],54
L 4
v_mul_f32 v32,v79,v32
v_mul_f32 v33,v79,v33
M 26,a[56:59],58
R 46,1,9216
v_fma_f32 v94,v30,v30,s88
M 26,a[68:71],62
v_fma_f32 v95,v31,v31,s88
v_fma_f32 v96,v32,v32,s88
M 22,a[52:55],58
v_fma_f32 v97,v33,v33,s88
v_mul_f32 v94,v30,v94
v_mul_f32 v95,v31,v95
M 22,a[64:67],62
R 50,1,10240
v_mul_f32 v96,v32,v96
M 18,a[48:51],58
v_exp_f32 v94,v94
M 18,a[60:63],62
L 4
v_exp_f32 v95,v95
M 18,a[72:75],66
R 54,1,11264
v_mul_f32 v97,v33,v97
M 18,a[84:87],70
v_exp_f32 v96,v96
v_fma_f32 v94,v94,s89,s89
M 22,a[76:79],66
v_exp_f32 v97,v97
M 22,a[88:91],70
R 58,1,12288
v_fma_f32 v95,v95,s89,s89
M 26,a[80:83],66
v_rcp_f32 v94,v94
M 26,a[92:95],70
L 3
v_rcp_f32 v95,v95
M 26,a[104:107],42
R 62,1,13312
v_fma_f32 v96,v96,s89,s89
v_fma_f32 v97,v97,s89,s89
M 26,a[116:119],46
v_rcp_f32 v96,v96
M 22,a[100:103],42
v_rcp_f32 v97,v97
M 22,a[112:115],46
R 66,1,14336
v_mul_f32 v30,v30,v94
M 18,a[96:99],42
v_mul_f32 v31,v31,v95
v_mul_f32 v32,v32,v96
M 18,a[108:111],46
L 3
v_mul_f32 v33,v33,v97
v_cvt_pk_bf16_f32 v106,v30,v31
v_cvt_pk_bf16_f32 v107,v32,v33
M 18,a[120:123],50
R 70,1,15360
v_fma_f32 v34,-v78,v86,v34
M 18,a[132:135],54
s_waitcnt vmcnt(0)
v_fma_f32 v35,-v78,v87,v35
v_fma_f32 v36,-v78,v88,v36
M 22,a[124:127],50
s_barrier
ds_read_b32 v7,v6 offset:3072
v_fma_f32 v37,-v78,v89,v37
M 22,a[136:139],54
R 42,1,16384
v_mul_f32 v34,v79,v34
M 26,a[128:131],50
buffer_store_dword v4,v3,s[36:39],s70 offen offset:0 sc0 sc1
v_mul_f32 v35,v79,v35
M 26,a[140:143],54
L 4
v_mul_f32 v36,v79,v36
v_mul_f32 v37,v79,v37
M 26,a[152:155],58
s_mov_b32 s70,0x7fff0000
R 46,1,17408
v_fma_f32 v98,v34,v34,s88
M 26,a[164:167],62
v_fma_f32 v99,v35,v35,s88
v_fma_f32 v100,v36,v36,s88
M 22,a[148:151],58
L 2
v_cmp_eq_u32 vcc,s53,v7
v_fma_f32 v101,v37,v37,s88
M 22,a[160:163],62
R 50,1,18432
v_mul_f32 v98,v34,v98
v_mul_f32 v99,v35,v99
M 18,a[144:147],58
v_exp_f32 v98,v98
M 18,a[156:159],62
s_cmp_eq_u32 vcc_lo,-1
s_nop 0
v_mul_f32 v100,v36,v100
s_cbranch_scc0 Lg3_X0_%=
M 18,a[168:171],66
s_add_u32 m0,s57,0
s_add_u32 s56,s55,0
s_add_u32 s58,s58,s61
R 54,1,19456
M 18,a[180:183],70
buffer_load_dword v114,v11,s[36:39],s73 offen offset:0 sc1
M 22,a[172:175],66
D 0,56,0
v_mul_f32 v101,v37,v101
M 22,a[184:187],70
s_add_u32 s54,s54,s46
R 58,1,20480
v_fma_f32 v98,v98,s89,s89
M 26,a[176:179],66
s_add_u32 s73,s73,s64
v_exp_f32 v99,v99
M 26,a[188:191],70
L 3
v_exp_f32 v100,v100
M 26,a[200:203],42
R 62,1,21504
v_fma_f32 v99,v99,s89,s89
M 26,a[212:215],46
D 0,56,1024
v_fma_f32 v100,v100,s89,s89
M 22,a[196:199],42
v_exp_f32 v101,v101
M 22,a[208:211],46
R 66,1,22528
v_fma_f32 v101,v101,s89,s89
M 18,a[192:195],42
D 0,56,2048
M 18,a[204:207],46
L 3
v_rcp_f32 v98,v98
M 18,a[216:219],50
R 70,1,23552
v_rcp_f32 v99,v99
M 18,a[228:231],54
v_rcp_f32 v100,v100
M 22,a[220:223],50
D 0,56,3072
M 22,a[232:235],54
s_add_u32 m0,s59,0
R 42,1,24576
v_mul_f32 v34,v34,v98
M 26,a[224:227],50
buffer_load_dword v8,s[36:39],s58 offen offset:0 sc1 lds
M 26,a[236:239],54
L 3
v_rcp_f32 v101,v101
v_mul_f32 v35,v35,v99
M 26,a[248:251],58
s_add_u32 m0,s57,16384
R 46,1,25600
v_mul_f32 v36,v36,v100
M 26,v[132:135],62
s_add_u32 s56,s55,16384
v_mul_f32 v37,v37,v101
v_cvt_pk_bf16_f32 v108,v34,v35
M 22,a[244:247],58
D 0,56,0
M 22,v[128:131],62
s_add_u32 s55,s55,s60
R 50,1,26624
v_cvt_pk_bf16_f32 v109,v36,v37
M 18,a[240:243],58
buffer_store_dwordx4 v[106:109],v0,s[36:39],s67 offen offset:0 sc0 sc1
v_fma_f32 v38,-v78,v90,v38
M 18,a[252:255],62
L 3
v_fma_f32 v39,-v78,v91,v39
v_fma_f32 v40,-v78,v92,v40
M 18,v[136:139],66
s_sub_u32 s57,s84,s57
)"
R"(R 54,1,27648
v_fma_f32 v41,-v78,v93,v41
M 18,v[148:151],70
D 0,56,1024
M 22,v[140:143],66
v_mul_f32 v38,v79,v38
v_mul_f32 v39,v79,v39
M 22,v[152:155],70
R 58,1,28672
v_mul_f32 v40,v79,v40
v_mul_f32 v41,v79,v41
M 26,v[144:147],66
v_fma_f32 v102,v38,v38,s88
v_fma_f32 v103,v39,v39,s88
M 26,v[156:159],70
L 3
v_fma_f32 v104,v40,v40,s88
v_fma_f32 v105,v41,v41,s88
M 26,v[168:171],42
R 62,1,29696
v_mul_f32 v102,v38,v102
M 26,v[180:183],46
D 0,56,2048
M 22,v[164:167],42
v_exp_f32 v102,v102
v_mul_f32 v103,v39,v103
M 22,v[176:179],46
R 66,1,30720
v_mul_f32 v104,v40,v104
M 18,v[160:163],42
v_exp_f32 v103,v103
M 18,v[172:175],46
L 3
D 0,56,3072
M 18,v[184:187],50
s_nop 0
R 70,1,31744
v_mul_f32 v105,v41,v105
M 18,v[196:199],54
s_waitcnt vmcnt(5)
v_exp_f32 v104,v104
v_fma_f32 v102,v102,s89,s89
M 22,v[188:191],50
s_barrier
buffer_store_dword v4,v3,s[36:39],s70 offen offset:0 sc0 sc1
M 22,v[200:203],54
s_mov_b32 s70,s69
R 42,2,0
ds_read_b32 v7,v6 offset:0
M 26,v[192:195],50
s_mov_b32 s69,s68
ds_read_b64 v[74:75],v13 offset:0
v_fma_f32 v103,v103,s89,s89
M 26,v[204:207],54
L 5
v_exp_f32 v105,v105
M 26,v[216:219],58
s_add_u32 s68,s68,s63
R 46,2,1024
v_rcp_f32 v102,v102
M 26,v[228:231],62
v_rcp_f32 v103,v103
M 22,v[212:215],58
v_fma_f32 v104,v104,s89,s89
v_fma_f32 v105,v105,s89,s89
M 22,v[224:227],62
L 2
R 50,2,2048
v_mul_f32 v38,v38,v102
M 18,v[208:211],58
v_rcp_f32 v104,v104
M 18,v[220:223],62
s_nop 0
v_rcp_f32 v105,v105
v_mul_f32 v39,v39,v103
M 18,v[232:235],66
R 54,2,3072
v_mul_f32 v40,v40,v104
M 18,v[244:247],70
v_mul_f32 v41,v41,v105
v_cmp_eq_u32 vcc,s53,v7
M 22,v[236:239],66
v_cvt_pk_bf16_f32 v110,v38,v39
v_cvt_pk_bf16_f32 v111,v40,v41
M 22,v[248:251],70
s_cmp_eq_u32 vcc_lo,-1
R 58,2,4096
M 26,v[240:243],66
L 3
buffer_store_dwordx2 v[110:111],v10,s[36:39],s67 offen offset:0 sc0 sc1
M 26,v[252:255],70
buffer_store_dword v4,v3,s[36:39],s71 offen offset:0 sc0 sc1
s_cbranch_scc1 Lg3_F1_%=
s_branch Lg3_K1_%=
Lg3_X0_%=:
M 18,a[168:171],66
s_add_u32 m0,s59,0
R 54,1,19456
v_mul_f32 v101,v37,v101
M 18,a[180:183],70
buffer_load_dword v8,s[36:39],s58 offen offset:0 sc1 lds
M 22,a[172:175],66
v_exp_f32 v99,v99
M 22,a[184:187],70
R 58,1,20480
v_fma_f32 v98,v98,s89,s89
M 26,a[176:179],66
v_exp_f32 v100,v100
M 26,a[188:191],70
L 3
v_exp_f32 v101,v101
M 26,a[200:203],42
R 62,1,21504
v_fma_f32 v99,v99,s89,s89
M 26,a[212:215],46
v_rcp_f32 v98,v98
M 22,a[196:199],42
v_rcp_f32 v99,v99
M 22,a[208:211],46
R 66,1,22528
v_fma_f32 v100,v100,s89,s89
M 18,a[192:195],42
v_rcp_f32 v100,v100
M 18,a[204:207],46
L 3
v_fma_f32 v101,v101,s89,s89
v_mul_f32 v34,v34,v98
M 18,a[216:219],50
R 70,1,23552
v_mul_f32 v35,v35,v99
M 18,a[228:231],54
v_rcp_f32 v101,v101
M 22,a[220:223],50
v_mul_f32 v36,v36,v100
v_mul_f32 v37,v37,v101
M 22,a[232:235],54
R 42,1,24576
v_cvt_pk_bf16_f32 v108,v34,v35
M 26,a[224:227],50
v_cvt_pk_bf16_f32 v109,v36,v37
v_fma_f32 v38,-v78,v90,v38
M 26,a[236:239],54
L 3
buffer_store_dwordx4 v[106:109],v0,s[36:39],s67 offen offset:0 sc0 sc1
v_fma_f32 v39,-v78,v91,v39
M 26,a[248:251],58
R 46,1,25600
v_fma_f32 v40,-v78,v92,v40
M 26,v[132:135],62
v_fma_f32 v41,-v78,v93,v41
v_mul_f32 v38,v79,v38
M 22,a[244:247],58
v_mul_f32 v39,v79,v39
v_mul_f32 v40,v79,v40
M 22,v[128:131],62
R 50,1,26624
v_mul_f32 v41,v79,v41
M 18,a[240:243],58
v_fma_f32 v102,v38,v38,s88
v_fma_f32 v103,v39,v39,s88
M 18,a[252:255],62
L 3
v_fma_f32 v104,v40,v40,s88
v_fma_f32 v105,v41,v41,s88
M 18,v[136:139],66
R 54,1,27648
v_mul_f32 v102,v38,v102
M 18,v[148:151],70
v_exp_f32 v102,v102
M 22,v[140:143],66
v_mul_f32 v103,v39,v103
v_mul_f32 v104,v40,v104
M 22,v[152:155],70
R 58,1,28672
v_mul_f32 v105,v41,v105
M 26,v[144:147],66
v_exp_f32 v103,v103
M 26,v[156:159],70
L 3
v_exp_f32 v104,v104
M 26,v[168:171],42
R 62,1,29696
v_fma_f32 v102,v102,s89,s89
M 26,v[180:183],46
v_exp_f32 v105,v105
M 22,v[164:167],42
v_rcp_f32 v102,v102
M 22,v[176:179],46
R 66,1,30720
v_fma_f32 v103,v103,s89,s89
M 18,v[160:163],42
v_rcp_f32 v103,v103
M 18,v[172:175],46
L 3
v_fma_f32 v104,v104,s89,s89
v_fma_f32 v105,v105,s89,s89
M 18,v[184:187],50
s_nop 0
R 70,1,31744
v_mul_f32 v38,v38,v102
M 18,v[196:199],54
s_waitcnt vmcnt(1)
v_rcp_f32 v104,v104
M 22,v[188:191],50
s_barrier
buffer_store_dword v4,v3,s[36:39],s70 offen offset:0 sc0 sc1
M 22,v[200:203],54
s_mov_b32 s70,s69
ds_read_b32 v7,v6 offset:0
ds_read_b64 v[74:75],v13 offset:0
M 26,v[192:195],50
s_mov_b32 s69,s68
v_rcp_f32 v105,v105
M 26,v[204:207],54
L 4
v_mul_f32 v39,v39,v103
v_mul_f32 v40,v40,v104
M 26,v[216:219],58
s_add_u32 s68,s68,s63
v_mul_f32 v41,v41,v105
v_cvt_pk_bf16_f32 v110,v38,v39
M 26,v[228:231],62
v_cvt_pk_bf16_f32 v111,v40,v41
M 22,v[212:215],58
buffer_store_dwordx2 v[110:111],v10,s[36:39],s67 offen offset:0 sc0 sc1
M 22,v[224:227],62
L 1
v_cmp_eq_u32 vcc,s53,v7
M 18,v[208:211],58
M 18,v[220:223],62
s_nop 0
M 18,v[232:235],66
M 18,v[244:247],70
M 22,v[236:239],66
M 22,v[248:251],70
M 26,v[240:243],66
M 26,v[252:255],70
s_mov_b32 s80,1
s_cmp_eq_u32 vcc_lo,-1
s_cbranch_scc0 Lg3_xn0_%=
buffer_load_dword v114,v11,s[36:39],s73 offen offset:0 sc1
L 0
v_fma_f32 v18,-v74,v82,v18
v_fma_f32 v19,-v74,v83,v19
v_fma_f32 v20,-v74,v84,v20
v_fma_f32 v21,-v74,v85,v21
v_mul_f32 v18,v75,v18
v_mul_f32 v19,v75,v19
v_mul_f32 v20,v75,v20
v_mul_f32 v21,v75,v21
s_add_u32 s73,s73,s64
s_add_u32 m0,s57,0
s_add_u32 s56,s55,0
D 0,56,0
v_fma_f32 v94,v18,v18,s88
v_fma_f32 v95,v19,v19,s88
v_fma_f32 v96,v20,v20,s88
v_fma_f32 v97,v21,v21,s88
v_mul_f32 v94,v18,v94
v_mul_f32 v95,v19,v95
v_mul_f32 v96,v20,v96
v_mul_f32 v97,v21,v97
D 0,56,1024
v_exp_f32 v94,v94
v_exp_f32 v95,v95
v_exp_f32 v96,v96
v_exp_f32 v97,v97
v_fma_f32 v94,v94,s89,s89
v_fma_f32 v95,v95,s89,s89
v_fma_f32 v96,v96,s89,s89
v_fma_f32 v97,v97,s89,s89
D 0,56,2048
v_rcp_f32 v94,v94
v_rcp_f32 v95,v95
v_rcp_f32 v96,v96
v_rcp_f32 v97,v97
v_mul_f32 v18,v18,v94
v_mul_f32 v19,v19,v95
v_mul_f32 v20,v20,v96
v_mul_f32 v21,v21,v97
D 0,56,3072
v_cvt_pk_bf16_f32 v106,v18,v19
v_cvt_pk_bf16_f32 v107,v20,v21
v_fma_f32 v22,-v74,v86,v22
v_fma_f32 v23,-v74,v87,v23
v_fma_f32 v24,-v74,v88,v24
v_fma_f32 v25,-v74,v89,v25
v_mul_f32 v22,v75,v22
v_mul_f32 v23,v75,v23
s_add_u32 s58,s58,s61
s_add_u32 m0,s57,16384
s_add_u32 s56,s55,16384
D 0,56,0
v_mul_f32 v24,v75,v24
v_mul_f32 v25,v75,v25
v_fma_f32 v98,v22,v22,s88
v_fma_f32 v99,v23,v23,s88
v_fma_f32 v100,v24,v24,s88
v_fma_f32 v101,v25,v25,s88
v_mul_f32 v98,v22,v98
v_mul_f32 v99,v23,v99
D 0,56,1024
v_mul_f32 v100,v24,v100
v_mul_f32 v101,v25,v101
v_exp_f32 v98,v98
v_exp_f32 v99,v99
v_exp_f32 v100,v100
v_exp_f32 v101,v101
v_fma_f32 v98,v98,s89,s89
v_fma_f32 v99,v99,s89,s89
D 0,56,2048
v_fma_f32 v100,v100,s89,s89
v_fma_f32 v101,v101,s89,s89
v_rcp_f32 v98,v98
v_rcp_f32 v99,v99
v_rcp_f32 v100,v100
v_rcp_f32 v101,v101
v_mul_f32 v22,v22,v98
v_mul_f32 v23,v23,v99
D 0,56,3072
v_mul_f32 v24,v24,v100
v_mul_f32 v25,v25,v101
v_cvt_pk_bf16_f32 v108,v22,v23
v_cvt_pk_bf16_f32 v109,v24,v25
buffer_store_dwordx4 v[106:109],v0,s[36:39],s66 offen offset:0 sc0 sc1
v_fma_f32 v26,-v74,v90,v26
v_fma_f32 v27,-v74,v91,v27
v_fma_f32 v28,-v74,v92,v28
v_fma_f32 v29,-v74,v93,v29
s_add_u32 s55,s55,s60
s_sub_u32 s57,s84,s57
s_add_u32 s54,s54,s46
v_mul_f32 v26,v75,v26
v_mul_f32 v27,v75,v27
v_mul_f32 v28,v75,v28
v_mul_f32 v29,v75,v29
v_fma_f32 v102,v26,v26,s88
v_fma_f32 v103,v27,v27,s88
v_fma_f32 v104,v28,v28,s88
v_fma_f32 v105,v29,v29,s88
v_mul_f32 v102,v26,v102
v_mul_f32 v103,v27,v103
v_mul_f32 v104,v28,v104
v_mul_f32 v105,v29,v105
v_exp_f32 v102,v102
v_exp_f32 v103,v103
v_exp_f32 v104,v104
v_exp_f32 v105,v105
v_fma_f32 v102,v102,s89,s89
v_fma_f32 v103,v103,s89,s89
v_fma_f32 v104,v104,s89,s89
v_fma_f32 v105,v105,s89,s89
v_rcp_f32 v102,v102
v_rcp_f32 v103,v103
v_rcp_f32 v104,v104
v_rcp_f32 v105,v105
v_mul_f32 v26,v26,v102
v_mul_f32 v27,v27,v103
v_mul_f32 v28,v28,v104
v_mul_f32 v29,v29,v105
v_cvt_pk_bf16_f32 v110,v26,v27
v_cvt_pk_bf16_f32 v111,v28,v29
buffer_store_dwordx2 v[110:111],v10,s[36:39],s66 offen offset:0 sc0 sc1
s_mov_b32 s72,s69
s_mov_b32 s69,0x7fff0000
s_mov_b32 s66,0x7fff0000
s_mov_b32 s76,0x7fff0000
s_branch Lg3_gox_%=
Lg3_xn0_%=:
s_cmp_ge_u32 s54,256
s_cbranch_scc1 Lg3_fin0_%=
s_add_u32 m0,s59,4096
s_nop 0
buffer_load_dword v8,s[36:39],s58 offen offset:0 sc1 lds
buffer_store_dword v4,v3,s[36:39],s71 offen offset:0 sc0 sc1
s_add_u32 m0,s59,5120
s_nop 0
buffer_load_dword v8,s[36:39],s58 offen offset:0 sc1 lds
buffer_store_dword v4,v3,s[36:39],s71 offen offset:0 sc0 sc1
s_mov_b32 s83,0
L 0
v_fma_f32 v18,-v74,v82,v18
v_fma_f32 v19,-v74,v83,v19
v_fma_f32 v20,-v74,v84,v20
v_fma_f32 v21,-v74,v85,v21
v_mul_f32 v18,v75,v18
v_mul_f32 v19,v75,v19
v_mul_f32 v20,v75,v20
v_mul_f32 v21,v75,v21
v_fma_f32 v94,v18,v18,s88
v_fma_f32 v95,v19,v19,s88
v_fma_f32 v96,v20,v20,s88
v_fma_f32 v97,v21,v21,s88
v_mul_f32 v94,v18,v94
v_mul_f32 v95,v19,v95
v_mul_f32 v96,v20,v96
v_mul_f32 v97,v21,v97
v_exp_f32 v94,v94
v_exp_f32 v95,v95
v_exp_f32 v96,v96
v_exp_f32 v97,v97
v_fma_f32 v94,v94,s89,s89
v_fma_f32 v95,v95,s89,s89
v_fma_f32 v96,v96,s89,s89
v_fma_f32 v97,v97,s89,s89
v_rcp_f32 v94,v94
v_rcp_f32 v95,v95
v_rcp_f32 v96,v96
v_rcp_f32 v97,v97
v_mul_f32 v18,v18,v94
v_mul_f32 v19,v19,v95
v_mul_f32 v20,v20,v96
v_mul_f32 v21,v21,v97
v_cvt_pk_bf16_f32 v106,v18,v19
v_cvt_pk_bf16_f32 v107,v20,v21
v_fma_f32 v22,-v74,v86,v22
v_fma_f32 v23,-v74,v87,v23
v_fma_f32 v24,-v74,v88,v24
v_fma_f32 v25,-v74,v89,v25
v_mul_f32 v22,v75,v22
v_mul_f32 v23,v75,v23
v_mul_f32 v24,v75,v24
v_mul_f32 v25,v75,v25
v_fma_f32 v98,v22,v22,s88
v_fma_f32 v99,v23,v23,s88
v_fma_f32 v100,v24,v24,s88
v_fma_f32 v101,v25,v25,s88
v_mul_f32 v98,v22,v98
v_mul_f32 v99,v23,v99
v_mul_f32 v100,v24,v100
v_mul_f32 v101,v25,v101
v_exp_f32 v98,v98
v_exp_f32 v99,v99
v_exp_f32 v100,v100
v_exp_f32 v101,v101
v_fma_f32 v98,v98,s89,s89
v_fma_f32 v99,v99,s89,s89
v_fma_f32 v100,v100,s89,s89
v_fma_f32 v101,v101,s89,s89
v_rcp_f32 v98,v98
v_rcp_f32 v99,v99
v_rcp_f32 v100,v100
v_rcp_f32 v101,v101
v_mul_f32 v22,v22,v98
v_mul_f32 v23,v23,v99
v_mul_f32 v24,v24,v100
v_mul_f32 v25,v25,v101
v_cvt_pk_bf16_f32 v108,v22,v23
v_cvt_pk_bf16_f32 v109,v24,v25
buffer_store_dwordx4 v[106:109],v0,s[36:39],s66 offen offset:0 sc0 sc1
v_fma_f32 v26,-v74,v90,v26
v_fma_f32 v27,-v74,v91,v27
v_fma_f32 v28,-v74,v92,v28
v_fma_f32 v29,-v74,v93,v29
v_mul_f32 v26,v75,v26
v_mul_f32 v27,v75,v27
v_mul_f32 v28,v75,v28
v_mul_f32 v29,v75,v29
v_fma_f32 v102,v26,v26,s88
v_fma_f32 v103,v27,v27,s88
v_fma_f32 v104,v28,v28,s88
v_fma_f32 v105,v29,v29,s88
v_mul_f32 v102,v26,v102
v_mul_f32 v103,v27,v103
v_mul_f32 v104,v28,v104
v_mul_f32 v105,v29,v105
v_exp_f32 v102,v102
v_exp_f32 v103,v103
v_exp_f32 v104,v104
v_exp_f32 v105,v105
v_fma_f32 v102,v102,s89,s89
v_fma_f32 v103,v103,s89,s89
v_fma_f32 v104,v104,s89,s89
v_fma_f32 v105,v105,s89,s89
v_rcp_f32 v102,v102
v_rcp_f32 v103,v103
v_rcp_f32 v104,v104
v_rcp_f32 v105,v105
v_mul_f32 v26,v26,v102
v_mul_f32 v27,v27,v103
v_mul_f32 v28,v28,v104
v_mul_f32 v29,v29,v105
v_cvt_pk_bf16_f32 v110,v26,v27
v_cvt_pk_bf16_f32 v111,v28,v29
buffer_store_dwordx2 v[110:111],v10,s[36:39],s66 offen offset:0 sc0 sc1
s_mov_b32 s72,s69
s_mov_b32 s69,0x7fff0000
s_mov_b32 s66,0x7fff0000
s_mov_b32 s76,0x7fff0000
s_branch Lg3_poll_%=
Lg3_fin0_%=:
L 0
v_fma_f32 v18,-v74,v82,v18
v_fma_f32 v19,-v74,v83,v19
v_fma_f32 v20,-v74,v84,v20
v_fma_f32 v21,-v74,v85,v21
v_mul_f32 v18,v75,v18
v_mul_f32 v19,v75,v19
v_mul_f32 v20,v75,v20
v_mul_f32 v21,v75,v21
)"
R"(v_fma_f32 v94,v18,v18,s88
v_fma_f32 v95,v19,v19,s88
v_fma_f32 v96,v20,v20,s88
v_fma_f32 v97,v21,v21,s88
v_mul_f32 v94,v18,v94
v_mul_f32 v95,v19,v95
v_mul_f32 v96,v20,v96
v_mul_f32 v97,v21,v97
v_exp_f32 v94,v94
v_exp_f32 v95,v95
v_exp_f32 v96,v96
v_exp_f32 v97,v97
v_fma_f32 v94,v94,s89,s89
v_fma_f32 v95,v95,s89,s89
v_fma_f32 v96,v96,s89,s89
v_fma_f32 v97,v97,s89,s89
v_rcp_f32 v94,v94
v_rcp_f32 v95,v95
v_rcp_f32 v96,v96
v_rcp_f32 v97,v97
v_mul_f32 v18,v18,v94
v_mul_f32 v19,v19,v95
v_mul_f32 v20,v20,v96
v_mul_f32 v21,v21,v97
v_cvt_pk_bf16_f32 v106,v18,v19
v_cvt_pk_bf16_f32 v107,v20,v21
v_fma_f32 v22,-v74,v86,v22
v_fma_f32 v23,-v74,v87,v23
v_fma_f32 v24,-v74,v88,v24
v_fma_f32 v25,-v74,v89,v25
v_mul_f32 v22,v75,v22
v_mul_f32 v23,v75,v23
v_mul_f32 v24,v75,v24
v_mul_f32 v25,v75,v25
v_fma_f32 v98,v22,v22,s88
v_fma_f32 v99,v23,v23,s88
v_fma_f32 v100,v24,v24,s88
v_fma_f32 v101,v25,v25,s88
v_mul_f32 v98,v22,v98
v_mul_f32 v99,v23,v99
v_mul_f32 v100,v24,v100
v_mul_f32 v101,v25,v101
v_exp_f32 v98,v98
v_exp_f32 v99,v99
v_exp_f32 v100,v100
v_exp_f32 v101,v101
v_fma_f32 v98,v98,s89,s89
v_fma_f32 v99,v99,s89,s89
v_fma_f32 v100,v100,s89,s89
v_fma_f32 v101,v101,s89,s89
v_rcp_f32 v98,v98
v_rcp_f32 v99,v99
v_rcp_f32 v100,v100
v_rcp_f32 v101,v101
v_mul_f32 v22,v22,v98
v_mul_f32 v23,v23,v99
v_mul_f32 v24,v24,v100
v_mul_f32 v25,v25,v101
v_cvt_pk_bf16_f32 v108,v22,v23
v_cvt_pk_bf16_f32 v109,v24,v25
buffer_store_dwordx4 v[106:109],v0,s[36:39],s66 offen offset:0 sc0 sc1
v_fma_f32 v26,-v74,v90,v26
v_fma_f32 v27,-v74,v91,v27
v_fma_f32 v28,-v74,v92,v28
v_fma_f32 v29,-v74,v93,v29
v_mul_f32 v26,v75,v26
v_mul_f32 v27,v75,v27
v_mul_f32 v28,v75,v28
v_mul_f32 v29,v75,v29
v_fma_f32 v102,v26,v26,s88
v_fma_f32 v103,v27,v27,s88
v_fma_f32 v104,v28,v28,s88
v_fma_f32 v105,v29,v29,s88
v_mul_f32 v102,v26,v102
v_mul_f32 v103,v27,v103
v_mul_f32 v104,v28,v104
v_mul_f32 v105,v29,v105
v_exp_f32 v102,v102
v_exp_f32 v103,v103
v_exp_f32 v104,v104
v_exp_f32 v105,v105
v_fma_f32 v102,v102,s89,s89
v_fma_f32 v103,v103,s89,s89
v_fma_f32 v104,v104,s89,s89
v_fma_f32 v105,v105,s89,s89
v_rcp_f32 v102,v102
v_rcp_f32 v103,v103
v_rcp_f32 v104,v104
v_rcp_f32 v105,v105
v_mul_f32 v26,v26,v102
v_mul_f32 v27,v27,v103
v_mul_f32 v28,v28,v104
v_mul_f32 v29,v29,v105
v_cvt_pk_bf16_f32 v110,v26,v27
v_cvt_pk_bf16_f32 v111,v28,v29
buffer_store_dwordx2 v[110:111],v10,s[36:39],s66 offen offset:0 sc0 sc1
s_mov_b32 s72,s69
s_mov_b32 s69,0x7fff0000
s_mov_b32 s66,0x7fff0000
s_mov_b32 s76,0x7fff0000
s_branch Lg3_done_%=
Lg3_K1_%=:
Lg3_L1_%=:
v_mfma_f32_16x16x32_bf16 v[38:41],a[8:11],v[42:45],0
s_add_u32 m0,s59,3072
R 62,2,5120
v_add_f32_dpp v115,v114,v114 quad_perm:[1,0,3,2] row_mask:0xf bank_mask:0xf
M 38,a[20:23],46
buffer_load_dword v8,s[36:39],s58 offen offset:0 sc1 lds
v_mfma_f32_16x16x32_bf16 v[34:37],a[4:7],v[42:45],0
s_mov_b32 s67,s65
v_add_f32_dpp v115,v115,v115 quad_perm:[2,3,0,1] row_mask:0xf bank_mask:0xf
v_fma_f32 v18,-v74,v82,v18
M 34,a[16:19],46
s_add_u32 s65,s65,s62
R 66,2,6144
v_fma_f32 v19,-v74,v83,v19
v_mfma_f32_16x16x32_bf16 v[30:33],a[0:3],v[42:45],0
v_add_f32_dpp v118,v115,v115 row_half_mirror row_mask:0xf bank_mask:0xf
v_fma_f32 v20,-v74,v84,v20
v_fma_f32 v21,-v74,v85,v21
M 30,a[12:15],46
L 3
v_mul_f32 v18,v75,v18
v_mul_f32 v19,v75,v19
M 30,a[24:27],50
R 70,2,7168
v_mov_b32_dpp v115,v118 row_ror:8 row_mask:0xf bank_mask:0xf
M 30,a[36:39],54
v_fma_f32 v115,-v118,v118,v115
v_max_f32 v115,s87,v115
M 34,a[28:31],50
v_rsq_f32 v116,v115
M 34,a[40:43],54
R 42,2,8192
v_mul_f32 v118,v15,v118
v_fma_f32 v118,v116,v16,v118
M 38,a[32:35],50
ds_write_b32 v12,v118 offset:2048
M 38,a[44:47],54
L 4
v_mul_f32 v20,v75,v20
v_mul_f32 v21,v75,v21
M 38,a[56:59],58
R 46,2,9216
v_fma_f32 v94,v18,v18,s88
M 38,a[68:71],62
v_fma_f32 v95,v19,v19,s88
v_fma_f32 v96,v20,v20,s88
M 34,a[52:55],58
v_fma_f32 v97,v21,v21,s88
v_mul_f32 v94,v18,v94
v_mul_f32 v95,v19,v95
M 34,a[64:67],62
R 50,2,10240
v_mul_f32 v96,v20,v96
M 30,a[48:51],58
v_exp_f32 v94,v94
M 30,a[60:63],62
L 4
v_exp_f32 v95,v95
M 30,a[72:75],66
R 54,2,11264
v_mul_f32 v97,v21,v97
M 30,a[84:87],70
v_exp_f32 v96,v96
v_fma_f32 v94,v94,s89,s89
M 34,a[76:79],66
v_exp_f32 v97,v97
M 34,a[88:91],70
R 58,2,12288
v_fma_f32 v95,v95,s89,s89
M 38,a[80:83],66
v_rcp_f32 v94,v94
M 38,a[92:95],70
L 3
v_rcp_f32 v95,v95
M 38,a[104:107],42
R 62,2,13312
v_fma_f32 v96,v96,s89,s89
v_fma_f32 v97,v97,s89,s89
M 38,a[116:119],46
v_rcp_f32 v96,v96
M 34,a[100:103],42
v_rcp_f32 v97,v97
M 34,a[112:115],46
R 66,2,14336
v_mul_f32 v18,v18,v94
M 30,a[96:99],42
v_mul_f32 v19,v19,v95
v_mul_f32 v20,v20,v96
M 30,a[108:111],46
L 3
v_mul_f32 v21,v21,v97
v_cvt_pk_bf16_f32 v106,v18,v19
v_cvt_pk_bf16_f32 v107,v20,v21
M 30,a[120:123],50
R 70,2,15360
v_fma_f32 v22,-v74,v86,v22
M 30,a[132:135],54
s_waitcnt vmcnt(0)
v_fma_f32 v23,-v74,v87,v23
v_fma_f32 v24,-v74,v88,v24
M 34,a[124:127],50
s_barrier
ds_read_b32 v7,v6 offset:3072
v_fma_f32 v25,-v74,v89,v25
M 34,a[136:139],54
R 42,2,16384
v_mul_f32 v22,v75,v22
M 38,a[128:131],50
buffer_store_dword v4,v3,s[36:39],s70 offen offset:0 sc0 sc1
v_mul_f32 v23,v75,v23
M 38,a[140:143],54
L 4
v_mul_f32 v24,v75,v24
v_mul_f32 v25,v75,v25
M 38,a[152:155],58
s_mov_b32 s70,0x7fff0000
R 46,2,17408
v_fma_f32 v98,v22,v22,s88
M 38,a[164:167],62
v_fma_f32 v99,v23,v23,s88
v_fma_f32 v100,v24,v24,s88
M 34,a[148:151],58
L 2
v_cmp_eq_u32 vcc,s53,v7
v_fma_f32 v101,v25,v25,s88
M 34,a[160:163],62
R 50,2,18432
v_mul_f32 v98,v22,v98
v_mul_f32 v99,v23,v99
M 30,a[144:147],58
v_exp_f32 v98,v98
M 30,a[156:159],62
s_cmp_eq_u32 vcc_lo,-1
s_nop 0
v_mul_f32 v100,v24,v100
s_cbranch_scc0 Lg3_X1_%=
M 30,a[168:171],66
s_add_u32 m0,s57,0
s_add_u32 s56,s55,0
s_add_u32 s58,s58,s61
R 54,2,19456
M 30,a[180:183],70
buffer_load_dword v114,v11,s[36:39],s73 offen offset:0 sc1
M 34,a[172:175],66
D 0,56,0
v_mul_f32 v101,v25,v101
M 34,a[184:187],70
s_add_u32 s54,s54,s46
R 58,2,20480
v_fma_f32 v98,v98,s89,s89
M 38,a[176:179],66
s_add_u32 s73,s73,s64
v_exp_f32 v99,v99
M 38,a[188:191],70
L 3
v_exp_f32 v100,v100
M 38,a[200:203],42
R 62,2,21504
v_fma_f32 v99,v99,s89,s89
M 38,a[212:215],46
D 0,56,1024
v_fma_f32 v100,v100,s89,s89
M 34,a[196:199],42
v_exp_f32 v101,v101
M 34,a[208:211],46
R 66,2,22528
v_fma_f32 v101,v101,s89,s89
M 30,a[192:195],42
D 0,56,2048
M 30,a[204:207],46
L 3
v_rcp_f32 v98,v98
M 30,a[216:219],50
R 70,2,23552
v_rcp_f32 v99,v99
M 30,a[228:231],54
v_rcp_f32 v100,v100
M 34,a[220:223],50
D 0,56,3072
M 34,a[232:235],54
s_add_u32 m0,s59,1024
R 42,2,24576
v_mul_f32 v22,v22,v98
M 38,a[224:227],50
buffer_load_dword v8,s[36:39],s58 offen offset:0 sc1 lds
M 38,a[236:239],54
L 3
v_rcp_f32 v101,v101
v_mul_f32 v23,v23,v99
M 38,a[248:251],58
s_add_u32 m0,s57,16384
R 46,2,25600
v_mul_f32 v24,v24,v100
M 38,v[132:135],62
s_add_u32 s56,s55,16384
v_mul_f32 v25,v25,v101
v_cvt_pk_bf16_f32 v108,v22,v23
M 34,a[244:247],58
D 0,56,0
M 34,v[128:131],62
s_add_u32 s55,s55,s60
R 50,2,26624
v_cvt_pk_bf16_f32 v109,v24,v25
M 30,a[240:243],58
buffer_store_dwordx4 v[106:109],v0,s[36:39],s66 offen offset:0 sc0 sc1
v_fma_f32 v26,-v74,v90,v26
M 30,a[252:255],62
L 3
v_fma_f32 v27,-v74,v91,v27
v_fma_f32 v28,-v74,v92,v28
M 30,v[136:139],66
s_sub_u32 s57,s84,s57
R 54,2,27648
v_fma_f32 v29,-v74,v93,v29
M 30,v[148:151],70
D 0,56,1024
M 34,v[140:143],66
v_mul_f32 v26,v75,v26
v_mul_f32 v27,v75,v27
M 34,v[152:155],70
R 58,2,28672
v_mul_f32 v28,v75,v28
v_mul_f32 v29,v75,v29
M 38,v[144:147],66
v_fma_f32 v102,v26,v26,s88
v_fma_f32 v103,v27,v27,s88
M 38,v[156:159],70
L 3
v_fma_f32 v104,v28,v28,s88
v_fma_f32 v105,v29,v29,s88
M 38,v[168:171],42
R 62,2,29696
v_mul_f32 v102,v26,v102
M 38,v[180:183],46
D 0,56,2048
M 34,v[164:167],42
v_exp_f32 v102,v102
v_mul_f32 v103,v27,v103
M 34,v[176:179],46
R 66,2,30720
v_mul_f32 v104,v28,v104
M 30,v[160:163],42
v_exp_f32 v103,v103
M 30,v[172:175],46
L 3
D 0,56,3072
M 30,v[184:187],50
s_nop 0
R 70,2,31744
v_mul_f32 v105,v29,v105
M 30,v[196:199],54
s_waitcnt vmcnt(5)
v_exp_f32 v104,v104
v_fma_f32 v102,v102,s89,s89
M 34,v[188:191],50
s_barrier
buffer_store_dword v4,v3,s[36:39],s70 offen offset:0 sc0 sc1
M 34,v[200:203],54
s_mov_b32 s70,s69
R 42,1,0
ds_read_b32 v7,v6 offset:1024
M 38,v[192:195],50
s_mov_b32 s69,s68
ds_read_b64 v[78:79],v13 offset:2048
v_fma_f32 v103,v103,s89,s89
M 38,v[204:207],54
L 5
v_exp_f32 v105,v105
M 38,v[216:219],58
s_add_u32 s68,s68,s63
R 46,1,1024
v_rcp_f32 v102,v102
M 38,v[228:231],62
v_rcp_f32 v103,v103
M 34,v[212:215],58
v_fma_f32 v104,v104,s89,s89
v_fma_f32 v105,v105,s89,s89
M 34,v[224:227],62
L 2
R 50,1,2048
v_mul_f32 v26,v26,v102
M 30,v[208:211],58
v_rcp_f32 v104,v104
M 30,v[220:223],62
s_nop 0
v_rcp_f32 v105,v105
v_mul_f32 v27,v27,v103
M 30,v[232:235],66
R 54,1,3072
v_mul_f32 v28,v28,v104
M 30,v[244:247],70
v_mul_f32 v29,v29,v105
v_cmp_eq_u32 vcc,s53,v7
M 34,v[236:239],66
v_cvt_pk_bf16_f32 v110,v26,v27
v_cvt_pk_bf16_f32 v111,v28,v29
M 34,v[248:251],70
s_cmp_eq_u32 vcc_lo,-1
R 58,1,4096
M 38,v[240:243],66
L 3
buffer_store_dwordx2 v[110:111],v10,s[36:39],s66 offen offset:0 sc0 sc1
M 38,v[252:255],70
buffer_store_dword v4,v3,s[36:39],s71 offen offset:0 sc0 sc1
s_cbranch_scc1 Lg3_F0_%=
s_branch Lg3_K0_%=
Lg3_X1_%=:
M 30,a[168:171],66
s_add_u32 m0,s59,1024
R 54,2,19456
v_mul_f32 v101,v25,v101
M 30,a[180:183],70
buffer_load_dword v8,s[36:39],s58 offen offset:0 sc1 lds
M 34,a[172:175],66
v_exp_f32 v99,v99
M 34,a[184:187],70
R 58,2,20480
v_fma_f32 v98,v98,s89,s89
M 38,a[176:179],66
v_exp_f32 v100,v100
M 38,a[188:191],70
L 3
v_exp_f32 v101,v101
M 38,a[200:203],42
R 62,2,21504
v_fma_f32 v99,v99,s89,s89
M 38,a[212:215],46
v_rcp_f32 v98,v98
M 34,a[196:199],42
v_rcp_f32 v99,v99
M 34,a[208:211],46
R 66,2,22528
v_fma_f32 v100,v100,s89,s89
M 30,a[192:195],42
v_rcp_f32 v100,v100
M 30,a[204:207],46
L 3
v_fma_f32 v101,v101,s89,s89
v_mul_f32 v22,v22,v98
M 30,a[216:219],50
R 70,2,23552
v_mul_f32 v23,v23,v99
M 30,a[228:231],54
v_rcp_f32 v101,v101
M 34,a[220:223],50
v_mul_f32 v24,v24,v100
v_mul_f32 v25,v25,v101
M 34,a[232:235],54
R 42,2,24576
v_cvt_pk_bf16_f32 v108,v22,v23
M 38,a[224:227],50
v_cvt_pk_bf16_f32 v109,v24,v25
v_fma_f32 v26,-v74,v90,v26
M 38,a[236:239],54
L 3
buffer_store_dwordx4 v[106:109],v0,s[36:39],s66 offen offset:0 sc0 sc1
v_fma_f32 v27,-v74,v91,v27
M 38,a[248:251],58
R 46,2,25600
v_fma_f32 v28,-v74,v92,v28
M 38,v[132:135],62
v_fma_f32 v29,-v74,v93,v29
v_mul_f32 v26,v75,v26
M 34,a[244:247],58
v_mul_f32 v27,v75,v27
v_mul_f32 v28,v75,v28
M 34,v[128:131],62
R 50,2,26624
v_mul_f32 v29,v75,v29
M 30,a[240:243],58
v_fma_f32 v102,v26,v26,s88
v_fma_f32 v103,v27,v27,s88
M 30,a[252:255],62
L 3
v_fma_f32 v104,v28,v28,s88
v_fma_f32 v105,v29,v29,s88
M 30,v[136:139],66
R 54,2,27648
v_mul_f32 v102,v26,v102
M 30,v[148:151],70
v_exp_f32 v102,v102
M 34,v[140:143],66
v_mul_f32 v103,v27,v103
v_mul_f32 v104,v28,v104
M 34,v[152:155],70
R 58,2,28672
v_mul_f32 v105,v29,v105
M 38,v[144:147],66
v_exp_f32 v103,v103
M 38,v[156:159],70
L 3
v_exp_f32 v104,v104
M 38,v[168:171],42
R 62,2,29696
v_fma_f32 v102,v102,s89,s89
M 38,v[180:183],46
v_exp_f32 v105,v105
)"
R"(M 34,v[164:167],42
v_rcp_f32 v102,v102
M 34,v[176:179],46
R 66,2,30720
v_fma_f32 v103,v103,s89,s89
M 30,v[160:163],42
v_rcp_f32 v103,v103
M 30,v[172:175],46
L 3
v_fma_f32 v104,v104,s89,s89
v_fma_f32 v105,v105,s89,s89
M 30,v[184:187],50
s_nop 0
R 70,2,31744
v_mul_f32 v26,v26,v102
M 30,v[196:199],54
s_waitcnt vmcnt(1)
v_rcp_f32 v104,v104
M 34,v[188:191],50
s_barrier
buffer_store_dword v4,v3,s[36:39],s70 offen offset:0 sc0 sc1
M 34,v[200:203],54
s_mov_b32 s70,s69
ds_read_b32 v7,v6 offset:1024
ds_read_b64 v[78:79],v13 offset:2048
M 38,v[192:195],50
s_mov_b32 s69,s68
v_rcp_f32 v105,v105
M 38,v[204:207],54
L 4
v_mul_f32 v27,v27,v103
v_mul_f32 v28,v28,v104
M 38,v[216:219],58
s_add_u32 s68,s68,s63
v_mul_f32 v29,v29,v105
v_cvt_pk_bf16_f32 v110,v26,v27
M 38,v[228:231],62
v_cvt_pk_bf16_f32 v111,v28,v29
M 34,v[212:215],58
buffer_store_dwordx2 v[110:111],v10,s[36:39],s66 offen offset:0 sc0 sc1
M 34,v[224:227],62
L 1
v_cmp_eq_u32 vcc,s53,v7
M 30,v[208:211],58
M 30,v[220:223],62
s_nop 0
M 30,v[232:235],66
M 30,v[244:247],70
M 34,v[236:239],66
M 34,v[248:251],70
M 38,v[240:243],66
M 38,v[252:255],70
s_mov_b32 s80,0
s_cmp_eq_u32 vcc_lo,-1
s_cbranch_scc0 Lg3_xn1_%=
buffer_load_dword v114,v11,s[36:39],s73 offen offset:0 sc1
L 0
v_fma_f32 v30,-v78,v82,v30
v_fma_f32 v31,-v78,v83,v31
v_fma_f32 v32,-v78,v84,v32
v_fma_f32 v33,-v78,v85,v33
v_mul_f32 v30,v79,v30
v_mul_f32 v31,v79,v31
v_mul_f32 v32,v79,v32
v_mul_f32 v33,v79,v33
s_add_u32 s73,s73,s64
s_add_u32 m0,s57,0
s_add_u32 s56,s55,0
D 0,56,0
v_fma_f32 v94,v30,v30,s88
v_fma_f32 v95,v31,v31,s88
v_fma_f32 v96,v32,v32,s88
v_fma_f32 v97,v33,v33,s88
v_mul_f32 v94,v30,v94
v_mul_f32 v95,v31,v95
v_mul_f32 v96,v32,v96
v_mul_f32 v97,v33,v97
D 0,56,1024
v_exp_f32 v94,v94
v_exp_f32 v95,v95
v_exp_f32 v96,v96
v_exp_f32 v97,v97
v_fma_f32 v94,v94,s89,s89
v_fma_f32 v95,v95,s89,s89
v_fma_f32 v96,v96,s89,s89
v_fma_f32 v97,v97,s89,s89
D 0,56,2048
v_rcp_f32 v94,v94
v_rcp_f32 v95,v95
v_rcp_f32 v96,v96
v_rcp_f32 v97,v97
v_mul_f32 v30,v30,v94
v_mul_f32 v31,v31,v95
v_mul_f32 v32,v32,v96
v_mul_f32 v33,v33,v97
D 0,56,3072
v_cvt_pk_bf16_f32 v106,v30,v31
v_cvt_pk_bf16_f32 v107,v32,v33
v_fma_f32 v34,-v78,v86,v34
v_fma_f32 v35,-v78,v87,v35
v_fma_f32 v36,-v78,v88,v36
v_fma_f32 v37,-v78,v89,v37
v_mul_f32 v34,v79,v34
v_mul_f32 v35,v79,v35
s_add_u32 s58,s58,s61
s_add_u32 m0,s57,16384
s_add_u32 s56,s55,16384
D 0,56,0
v_mul_f32 v36,v79,v36
v_mul_f32 v37,v79,v37
v_fma_f32 v98,v34,v34,s88
v_fma_f32 v99,v35,v35,s88
v_fma_f32 v100,v36,v36,s88
v_fma_f32 v101,v37,v37,s88
v_mul_f32 v98,v34,v98
v_mul_f32 v99,v35,v99
D 0,56,1024
v_mul_f32 v100,v36,v100
v_mul_f32 v101,v37,v101
v_exp_f32 v98,v98
v_exp_f32 v99,v99
v_exp_f32 v100,v100
v_exp_f32 v101,v101
v_fma_f32 v98,v98,s89,s89
v_fma_f32 v99,v99,s89,s89
D 0,56,2048
v_fma_f32 v100,v100,s89,s89
v_fma_f32 v101,v101,s89,s89
v_rcp_f32 v98,v98
v_rcp_f32 v99,v99
v_rcp_f32 v100,v100
v_rcp_f32 v101,v101
v_mul_f32 v34,v34,v98
v_mul_f32 v35,v35,v99
D 0,56,3072
v_mul_f32 v36,v36,v100
v_mul_f32 v37,v37,v101
v_cvt_pk_bf16_f32 v108,v34,v35
v_cvt_pk_bf16_f32 v109,v36,v37
buffer_store_dwordx4 v[106:109],v0,s[36:39],s67 offen offset:0 sc0 sc1
v_fma_f32 v38,-v78,v90,v38
v_fma_f32 v39,-v78,v91,v39
v_fma_f32 v40,-v78,v92,v40
v_fma_f32 v41,-v78,v93,v41
s_add_u32 s55,s55,s60
s_sub_u32 s57,s84,s57
s_add_u32 s54,s54,s46
v_mul_f32 v38,v79,v38
v_mul_f32 v39,v79,v39
v_mul_f32 v40,v79,v40
v_mul_f32 v41,v79,v41
v_fma_f32 v102,v38,v38,s88
v_fma_f32 v103,v39,v39,s88
v_fma_f32 v104,v40,v40,s88
v_fma_f32 v105,v41,v41,s88
v_mul_f32 v102,v38,v102
v_mul_f32 v103,v39,v103
v_mul_f32 v104,v40,v104
v_mul_f32 v105,v41,v105
v_exp_f32 v102,v102
v_exp_f32 v103,v103
v_exp_f32 v104,v104
v_exp_f32 v105,v105
v_fma_f32 v102,v102,s89,s89
v_fma_f32 v103,v103,s89,s89
v_fma_f32 v104,v104,s89,s89
v_fma_f32 v105,v105,s89,s89
v_rcp_f32 v102,v102
v_rcp_f32 v103,v103
v_rcp_f32 v104,v104
v_rcp_f32 v105,v105
v_mul_f32 v38,v38,v102
v_mul_f32 v39,v39,v103
v_mul_f32 v40,v40,v104
v_mul_f32 v41,v41,v105
v_cvt_pk_bf16_f32 v110,v38,v39
v_cvt_pk_bf16_f32 v111,v40,v41
buffer_store_dwordx2 v[110:111],v10,s[36:39],s67 offen offset:0 sc0 sc1
s_mov_b32 s72,s69
s_mov_b32 s69,0x7fff0000
s_mov_b32 s67,0x7fff0000
s_mov_b32 s77,0x7fff0000
s_branch Lg3_gox_%=
Lg3_xn1_%=:
s_cmp_ge_u32 s54,256
s_cbranch_scc1 Lg3_fin1_%=
s_add_u32 m0,s59,4096
s_nop 0
buffer_load_dword v8,s[36:39],s58 offen offset:0 sc1 lds
buffer_store_dword v4,v3,s[36:39],s71 offen offset:0 sc0 sc1
s_add_u32 m0,s59,5120
s_nop 0
buffer_load_dword v8,s[36:39],s58 offen offset:0 sc1 lds
buffer_store_dword v4,v3,s[36:39],s71 offen offset:0 sc0 sc1
s_mov_b32 s83,0
L 0
v_fma_f32 v30,-v78,v82,v30
v_fma_f32 v31,-v78,v83,v31
v_fma_f32 v32,-v78,v84,v32
v_fma_f32 v33,-v78,v85,v33
v_mul_f32 v30,v79,v30
v_mul_f32 v31,v79,v31
v_mul_f32 v32,v79,v32
v_mul_f32 v33,v79,v33
v_fma_f32 v94,v30,v30,s88
v_fma_f32 v95,v31,v31,s88
v_fma_f32 v96,v32,v32,s88
v_fma_f32 v97,v33,v33,s88
v_mul_f32 v94,v30,v94
v_mul_f32 v95,v31,v95
v_mul_f32 v96,v32,v96
v_mul_f32 v97,v33,v97
v_exp_f32 v94,v94
v_exp_f32 v95,v95
v_exp_f32 v96,v96
v_exp_f32 v97,v97
v_fma_f32 v94,v94,s89,s89
v_fma_f32 v95,v95,s89,s89
v_fma_f32 v96,v96,s89,s89
v_fma_f32 v97,v97,s89,s89
v_rcp_f32 v94,v94
v_rcp_f32 v95,v95
v_rcp_f32 v96,v96
v_rcp_f32 v97,v97
v_mul_f32 v30,v30,v94
v_mul_f32 v31,v31,v95
v_mul_f32 v32,v32,v96
v_mul_f32 v33,v33,v97
v_cvt_pk_bf16_f32 v106,v30,v31
v_cvt_pk_bf16_f32 v107,v32,v33
v_fma_f32 v34,-v78,v86,v34
v_fma_f32 v35,-v78,v87,v35
v_fma_f32 v36,-v78,v88,v36
v_fma_f32 v37,-v78,v89,v37
v_mul_f32 v34,v79,v34
v_mul_f32 v35,v79,v35
v_mul_f32 v36,v79,v36
v_mul_f32 v37,v79,v37
v_fma_f32 v98,v34,v34,s88
v_fma_f32 v99,v35,v35,s88
v_fma_f32 v100,v36,v36,s88
v_fma_f32 v101,v37,v37,s88
v_mul_f32 v98,v34,v98
v_mul_f32 v99,v35,v99
v_mul_f32 v100,v36,v100
v_mul_f32 v101,v37,v101
v_exp_f32 v98,v98
v_exp_f32 v99,v99
v_exp_f32 v100,v100
v_exp_f32 v101,v101
v_fma_f32 v98,v98,s89,s89
v_fma_f32 v99,v99,s89,s89
v_fma_f32 v100,v100,s89,s89
v_fma_f32 v101,v101,s89,s89
v_rcp_f32 v98,v98
v_rcp_f32 v99,v99
v_rcp_f32 v100,v100
v_rcp_f32 v101,v101
v_mul_f32 v34,v34,v98
v_mul_f32 v35,v35,v99
v_mul_f32 v36,v36,v100
v_mul_f32 v37,v37,v101
v_cvt_pk_bf16_f32 v108,v34,v35
v_cvt_pk_bf16_f32 v109,v36,v37
buffer_store_dwordx4 v[106:109],v0,s[36:39],s67 offen offset:0 sc0 sc1
v_fma_f32 v38,-v78,v90,v38
v_fma_f32 v39,-v78,v91,v39
v_fma_f32 v40,-v78,v92,v40
v_fma_f32 v41,-v78,v93,v41
v_mul_f32 v38,v79,v38
v_mul_f32 v39,v79,v39
v_mul_f32 v40,v79,v40
v_mul_f32 v41,v79,v41
v_fma_f32 v102,v38,v38,s88
v_fma_f32 v103,v39,v39,s88
v_fma_f32 v104,v40,v40,s88
v_fma_f32 v105,v41,v41,s88
v_mul_f32 v102,v38,v102
v_mul_f32 v103,v39,v103
v_mul_f32 v104,v40,v104
v_mul_f32 v105,v41,v105
v_exp_f32 v102,v102
v_exp_f32 v103,v103
v_exp_f32 v104,v104
v_exp_f32 v105,v105
v_fma_f32 v102,v102,s89,s89
v_fma_f32 v103,v103,s89,s89
v_fma_f32 v104,v104,s89,s89
v_fma_f32 v105,v105,s89,s89
v_rcp_f32 v102,v102
v_rcp_f32 v103,v103
v_rcp_f32 v104,v104
v_rcp_f32 v105,v105
v_mul_f32 v38,v38,v102
v_mul_f32 v39,v39,v103
v_mul_f32 v40,v40,v104
v_mul_f32 v41,v41,v105
v_cvt_pk_bf16_f32 v110,v38,v39
v_cvt_pk_bf16_f32 v111,v40,v41
buffer_store_dwordx2 v[110:111],v10,s[36:39],s67 offen offset:0 sc0 sc1
s_mov_b32 s72,s69
s_mov_b32 s69,0x7fff0000
s_mov_b32 s67,0x7fff0000
s_mov_b32 s77,0x7fff0000
s_branch Lg3_poll_%=
Lg3_fin1_%=:
L 0
v_fma_f32 v30,-v78,v82,v30
v_fma_f32 v31,-v78,v83,v31
v_fma_f32 v32,-v78,v84,v32
v_fma_f32 v33,-v78,v85,v33
v_mul_f32 v30,v79,v30
v_mul_f32 v31,v79,v31
v_mul_f32 v32,v79,v32
v_mul_f32 v33,v79,v33
v_fma_f32 v94,v30,v30,s88
v_fma_f32 v95,v31,v31,s88
v_fma_f32 v96,v32,v32,s88
v_fma_f32 v97,v33,v33,s88
v_mul_f32 v94,v30,v94
v_mul_f32 v95,v31,v95
v_mul_f32 v96,v32,v96
v_mul_f32 v97,v33,v97
v_exp_f32 v94,v94
v_exp_f32 v95,v95
v_exp_f32 v96,v96
v_exp_f32 v97,v97
v_fma_f32 v94,v94,s89,s89
v_fma_f32 v95,v95,s89,s89
v_fma_f32 v96,v96,s89,s89
v_fma_f32 v97,v97,s89,s89
v_rcp_f32 v94,v94
v_rcp_f32 v95,v95
v_rcp_f32 v96,v96
v_rcp_f32 v97,v97
v_mul_f32 v30,v30,v94
v_mul_f32 v31,v31,v95
v_mul_f32 v32,v32,v96
v_mul_f32 v33,v33,v97
v_cvt_pk_bf16_f32 v106,v30,v31
v_cvt_pk_bf16_f32 v107,v32,v33
v_fma_f32 v34,-v78,v86,v34
v_fma_f32 v35,-v78,v87,v35
v_fma_f32 v36,-v78,v88,v36
v_fma_f32 v37,-v78,v89,v37
v_mul_f32 v34,v79,v34
v_mul_f32 v35,v79,v35
v_mul_f32 v36,v79,v36
v_mul_f32 v37,v79,v37
v_fma_f32 v98,v34,v34,s88
v_fma_f32 v99,v35,v35,s88
v_fma_f32 v100,v36,v36,s88
v_fma_f32 v101,v37,v37,s88
v_mul_f32 v98,v34,v98
v_mul_f32 v99,v35,v99
v_mul_f32 v100,v36,v100
v_mul_f32 v101,v37,v101
v_exp_f32 v98,v98
v_exp_f32 v99,v99
v_exp_f32 v100,v100
v_exp_f32 v101,v101
v_fma_f32 v98,v98,s89,s89
v_fma_f32 v99,v99,s89,s89
v_fma_f32 v100,v100,s89,s89
v_fma_f32 v101,v101,s89,s89
v_rcp_f32 v98,v98
v_rcp_f32 v99,v99
v_rcp_f32 v100,v100
v_rcp_f32 v101,v101
v_mul_f32 v34,v34,v98
v_mul_f32 v35,v35,v99
v_mul_f32 v36,v36,v100
v_mul_f32 v37,v37,v101
v_cvt_pk_bf16_f32 v108,v34,v35
v_cvt_pk_bf16_f32 v109,v36,v37
buffer_store_dwordx4 v[106:109],v0,s[36:39],s67 offen offset:0 sc0 sc1
v_fma_f32 v38,-v78,v90,v38
v_fma_f32 v39,-v78,v91,v39
v_fma_f32 v40,-v78,v92,v40
v_fma_f32 v41,-v78,v93,v41
v_mul_f32 v38,v79,v38
v_mul_f32 v39,v79,v39
v_mul_f32 v40,v79,v40
v_mul_f32 v41,v79,v41
v_fma_f32 v102,v38,v38,s88
v_fma_f32 v103,v39,v39,s88
v_fma_f32 v104,v40,v40,s88
v_fma_f32 v105,v41,v41,s88
v_mul_f32 v102,v38,v102
v_mul_f32 v103,v39,v103
v_mul_f32 v104,v40,v104
v_mul_f32 v105,v41,v105
v_exp_f32 v102,v102
v_exp_f32 v103,v103
v_exp_f32 v104,v104
v_exp_f32 v105,v105
v_fma_f32 v102,v102,s89,s89
v_fma_f32 v103,v103,s89,s89
v_fma_f32 v104,v104,s89,s89
v_fma_f32 v105,v105,s89,s89
v_rcp_f32 v102,v102
v_rcp_f32 v103,v103
v_rcp_f32 v104,v104
v_rcp_f32 v105,v105
v_mul_f32 v38,v38,v102
v_mul_f32 v39,v39,v103
v_mul_f32 v40,v40,v104
v_mul_f32 v41,v41,v105
v_cvt_pk_bf16_f32 v110,v38,v39
v_cvt_pk_bf16_f32 v111,v40,v41
buffer_store_dwordx2 v[110:111],v10,s[36:39],s67 offen offset:0 sc0 sc1
s_mov_b32 s72,s69
s_mov_b32 s69,0x7fff0000
s_mov_b32 s67,0x7fff0000
s_mov_b32 s77,0x7fff0000
s_branch Lg3_done_%=
Lg3_done_%=:
s_waitcnt vmcnt(2)
s_barrier
buffer_store_dword v4,v3,s[36:39],s70 offen offset:0 sc0 sc1
s_waitcnt vmcnt(1)
s_barrier
buffer_store_dword v4,v3,s[36:39],s72 offen offset:0 sc0 sc1
s_waitcnt vmcnt(0) lgkmcnt(0)
s_barrier
s_endpgm
Lcode_g3_e:
.purgem M
.purgem R
.purgem T
.purgem D
.purgem L
)"
  :: "s"(ws), "s"(__builtin_amdgcn_readfirstlane(sWave)), "s"(__builtin_amdgcn_readfirstlane(sG0)), "s"(__builtin_amdgcn_readfirstlane(sStep)), "s"(__builtin_amdgcn_readfirstlane(sOutOff)), "s"(__builtin_amdgcn_readfirstlane(sFoutOff)), "s"(__builtin_amdgcn_readfirstlane(sAuxOff)), "s"(__builtin_amdgcn_readfirstlane(sWw)), "s"(__builtin_amdgcn_readfirstlane(sBuf)), "s"(__builtin_amdgcn_readfirstlane(sExp)), "v"(vL16), "v"(vFlag), "v"(vSnapOff), "v"(vSnapRd), "v"(vWd0), "v"(vWd1), "v"(vWr0), "v"(vWr1), "v"(vWr2), "v"(vOutS), "v"(vAux0), "v"(vAux1), "v"(vAux2), "v"(vAux3), "v"(vAn), "v"(vAe), "s"(w3), "s"(out) : "v255", "v0", "v1", "v2", "v3", "v4", "v5", "v6", "v7", "v8", "v9", "v10", "v11", "v12", "v13", "v14", "v15", "v16", "v17", "v18", "v19", "v20", "v21", "v22", "v23", "v24", "v25", "v26", "v27", "v28", "v29", "v30", "v31", "v32", "v33", "v34", "v35", "v36", "v37", "v38", "v39", "v40", "v41", "v42", "v43", "v44", "v45", "v46", "v47", "v48", "v49", "v50", "v51", "v52", "v53", "v54", "v55", "v56", "v57", "v58", "v59", "v60", "v61", "v62", "v63", "v64", "v65", "v66", "v67", "v68", "v69", "v70", "v71", "v72", "v73", "v74", "v75", "v76", "v77", "v78", "v79", "v80", "v81", "v82", "v83", "v84", "v85", "v86", "v87", "v88", "v89", "v90", "v91", "v92", "v93", "v94", "v95", "v96", "v97", "v98", "v99", "v100", "v101", "v102", "v103", "v104", "v105", "v106", "v107", "v108", "v109", "v110", "v111", "v112", "v113", "v114", "v115", "v116", "v117", "v118", "v119", "v120", "v121", "v122", "v123", "v124", "v125", "v126", "v127", "v128", "v129", "v130", "v131", "v132", "v133", "v134", "v135", "v136", "v137", "v138", "v139", "v140", "v141", "v142", "v143", "v144", "v145", "v146", "v147", "v148", "v149", "v150", "v151", "v152", "v153", "v154", "v155", "v156", "v157", "v158", "v159", "v160", "v161", "v162", "v163", "v164", "v165", "v166", "v167", "v168", "v169", "v170", "v171", "v172", "v173", "v174", "v175", "v176", "v177", "v178", "v179", "v180", "v181", "v182", "v183", "v184", "v185", "v186", "v187", "v188", "v189", "v190", "v191", "v192", "v193", "v194", "v195", "v196", "v197", "v198", "v199", "v200", "v201", "v202", "v203", "v204", "v205", "v206", "v207", "v208", "v209", "v210", "v211", "v212", "v213", "v214", "v215", "v216", "v217", "v218", "v219", "v220", "v221", "v222", "v223", "v224", "v225", "v226", "v227", "v228", "v229", "v230", "v231", "a0", "a1", "a2", "a3", "a4", "a5", "a6", "a7", "a8", "a9", "a10", "a11", "a12", "a13", "a14", "a15", "a16", "a17", "a18", "a19", "a20", "a21", "a22", "a23", "a24", "a25", "a26", "a27", "a28", "a29", "a30", "a31", "a32", "a33", "a34", "a35", "a36", "a37", "a38", "a39", "a40", "a41", "a42", "a43", "a44", "a45", "a46", "a47", "a48", "a49", "a50", "a51", "a52", "a53", "a54", "a55", "a56", "a57", "a58", "a59", "a60", "a61", "a62", "a63", "a64", "a65", "a66", "a67", "a68", "a69", "a70", "a71", "a72", "a73", "a74", "a75", "a76", "a77", "a78", "a79", "a80", "a81", "a82", "a83", "a84", "a85", "a86", "a87", "a88", "a89", "a90", "a91", "a92", "a93", "a94", "a95", "a96", "a97", "a98", "a99", "a100", "a101", "a102", "a103", "a104", "a105", "a106", "a107", "a108", "a109", "a110", "a111", "a112", "a113", "a114", "a115", "a116", "a117", "a118", "a119", "a120", "a121", "a122", "a123", "a124", "a125", "a126", "a127", "a128", "a129", "a130", "a131", "a132", "a133", "a134", "a135", "a136", "a137", "a138", "a139", "a140", "a141", "a142", "a143", "a144", "a145", "a146", "a147", "a148", "a149", "a150", "a151", "a152", "a153", "a154", "a155", "a156", "a157", "a158", "a159", "a160", "a161", "a162", "a163", "a164", "a165", "a166", "a167", "a168", "a169", "a170", "a171", "a172", "a173", "a174", "a175", "a176", "a177", "a178", "a179", "a180", "a181", "a182", "a183", "a184", "a185", "a186", "a187", "a188", "a189", "a190", "a191", "a192", "a193", "a194", "a195", "a196", "a197", "a198", "a199", "a200", "a201", "a202", "a203", "a204", "a205", "a206", "a207", "a208", "a209", "a210", "a211", "a212", "a213", "a214", "a215", "a216", "a217", "a218", "a219", "a220", "a221", "a222", "a223", "a224", "a225", "a226", "a227", "a228", "a229", "a230", "a231", "a232", "a233", "a234", "a235", "a236", "a237", "a238", "a239", "a240", "a241", "a242", "a243", "a244", "a245", "a246", "a247", "a248", "a249", "a250", "a251", "a252", "a253", "a254", "a255", "s36", "s37", "s38", "s39", "s40", "s41", "s42", "s43", "s44", "s45", "s46", "s47", "s48", "s49", "s50", "s51", "s52", "s53", "s54", "s55", "s56", "s57", "s58", "s59", "s60", "s61", "s62", "s63", "s64", "s65", "s66", "s67", "s68", "s69", "s70", "s71", "s72", "s73", "s74", "s75", "s76", "s77", "s78", "s79", "s80", "s81", "s82", "s83", "s84", "s85", "s86", "s87", "s88", "s89", "s90", "s91", "s92", "s93", "s94", "s95", "s96", "s97", "vcc", "scc", "memory"); } else { asm volatile(
R"(.macro M d,w,x
v_mfma_f32_16x16x32_bf16 v[\d:\d+3], \w, v[\x:\x+3], v[\d:\d+3]
.endm
.macro R d,a,o
ds_read_b128 v[\d:\d+3], v\a offset:\o
.endm
.macro T d,a,o
ds_read_b64_tr_b16 \d, v\a offset:\o
.endm
.macro D a,s,o
buffer_load_dwordx4 v\a, s[36:39], s\s offen offset:\o sc1 lds
.endm
.macro L c
s_waitcnt lgkmcnt(\c)
.endm
Lcode_g3o_b:
s_mov_b64 s[36:37],%0
s_mov_b32 s38,111243392
s_mov_b32 s39,0x00020000
s_mov_b32 s44,%1
s_mov_b32 s45,%2
s_mov_b32 s46,%3
s_mov_b32 s47,%4
s_mov_b32 s48,%5
s_mov_b32 s49,%6
s_mov_b32 s50,%7
s_mov_b32 s51,%8
s_mov_b32 s53,%9
v_mov_b32 v0,%10
v_mov_b32 v3,%11
v_mov_b32 v5,%12
v_mov_b32 v6,%13
v_mov_b32 v18,%14
v_mov_b32 v19,%15
v_mov_b32 v20,%16
v_mov_b32 v21,%17
v_mov_b32 v22,%18
v_mov_b32 v10,%19
v_mov_b32 v11,%20
v_mov_b32 v12,%21
v_mov_b32 v13,%22
v_mov_b32 v14,%23
v_mov_b32 v15,%24
v_mov_b32 v16,%25
s_mov_b64 s[40:41],%26
s_mov_b32 s42,7340032
s_mov_b32 s43,0x00020000
s_lshl_b32 s57,s44,11
s_add_u32 s55,s50,0
s_add_u32 m0,s57,0
s_nop 0
buffer_load_dwordx4 v18,s[40:43],s55 offen offset:0  lds
s_add_u32 m0,s57,1024
s_nop 0
buffer_load_dwordx4 v19,s[40:43],s55 offen offset:0  lds
s_add_u32 m0,s57,8064
s_nop 0
buffer_load_dwordx4 v18,s[40:43],s55 offen offset:128  lds
s_add_u32 m0,s57,9088
s_nop 0
buffer_load_dwordx4 v19,s[40:43],s55 offen offset:128  lds
s_add_u32 s55,s50,458752
s_add_u32 m0,s57,16384
s_nop 0
buffer_load_dwordx4 v18,s[40:43],s55 offen offset:0  lds
s_add_u32 m0,s57,17408
s_nop 0
buffer_load_dwordx4 v19,s[40:43],s55 offen offset:0  lds
s_add_u32 m0,s57,24448
s_nop 0
buffer_load_dwordx4 v18,s[40:43],s55 offen offset:128  lds
s_add_u32 m0,s57,25472
s_nop 0
buffer_load_dwordx4 v19,s[40:43],s55 offen offset:128  lds
s_add_u32 s55,s50,917504
s_add_u32 m0,s57,32768
s_nop 0
buffer_load_dwordx4 v18,s[40:43],s55 offen offset:0  lds
s_add_u32 m0,s57,33792
s_nop 0
buffer_load_dwordx4 v19,s[40:43],s55 offen offset:0  lds
s_add_u32 m0,s57,40832
s_nop 0
buffer_load_dwordx4 v18,s[40:43],s55 offen offset:128  lds
s_add_u32 m0,s57,41856
s_nop 0
buffer_load_dwordx4 v19,s[40:43],s55 offen offset:128  lds
s_add_u32 s55,s50,1376256
s_add_u32 m0,s57,49152
s_nop 0
buffer_load_dwordx4 v18,s[40:43],s55 offen offset:0  lds
s_add_u32 m0,s57,50176
s_nop 0
buffer_load_dwordx4 v19,s[40:43],s55 offen offset:0  lds
s_add_u32 m0,s57,57216
s_nop 0
buffer_load_dwordx4 v18,s[40:43],s55 offen offset:128  lds
s_add_u32 m0,s57,58240
s_nop 0
buffer_load_dwordx4 v19,s[40:43],s55 offen offset:128  lds
s_add_u32 s55,s50,1835008
s_add_u32 m0,s57,65536
s_nop 0
buffer_load_dwordx4 v18,s[40:43],s55 offen offset:0  lds
s_add_u32 m0,s57,66560
s_nop 0
buffer_load_dwordx4 v19,s[40:43],s55 offen offset:0  lds
s_add_u32 m0,s57,73600
s_nop 0
buffer_load_dwordx4 v18,s[40:43],s55 offen offset:128  lds
s_add_u32 m0,s57,74624
s_nop 0
buffer_load_dwordx4 v19,s[40:43],s55 offen offset:128  lds
s_add_u32 s55,s50,2293760
s_add_u32 m0,s57,81920
s_nop 0
buffer_load_dwordx4 v18,s[40:43],s55 offen offset:0  lds
s_add_u32 m0,s57,82944
s_nop 0
buffer_load_dwordx4 v19,s[40:43],s55 offen offset:0  lds
s_add_u32 m0,s57,89984
s_nop 0
buffer_load_dwordx4 v18,s[40:43],s55 offen offset:128  lds
s_add_u32 m0,s57,91008
s_nop 0
buffer_load_dwordx4 v19,s[40:43],s55 offen offset:128  lds
s_waitcnt vmcnt(20) lgkmcnt(0)
s_barrier
s_add_u32 s55,s50,2752512
s_add_u32 m0,s57,98304
s_nop 0
buffer_load_dwordx4 v18,s[40:43],s55 offen offset:0  lds
s_add_u32 m0,s57,99328
s_nop 0
buffer_load_dwordx4 v19,s[40:43],s55 offen offset:0  lds
s_add_u32 m0,s57,106368
s_nop 0
buffer_load_dwordx4 v18,s[40:43],s55 offen offset:128  lds
s_add_u32 m0,s57,107392
s_nop 0
buffer_load_dwordx4 v19,s[40:43],s55 offen offset:128  lds
v_add_u32 v26,0,v20
v_add_u32 v27,0,v21
T a[0:1],26,0
T a[2:3],26,512
T a[4:5],27,0
T a[6:7],27,512
T a[8:9],26,4096
T a[10:11],26,4608
T a[12:13],27,4096
T a[14:15],27,4608
s_waitcnt vmcnt(20) lgkmcnt(0)
s_barrier
s_add_u32 s55,s50,3211264
s_add_u32 m0,s57,0
s_nop 0
buffer_load_dwordx4 v18,s[40:43],s55 offen offset:0  lds
s_add_u32 m0,s57,1024
s_nop 0
buffer_load_dwordx4 v19,s[40:43],s55 offen offset:0  lds
s_add_u32 m0,s57,8064
s_nop 0
buffer_load_dwordx4 v18,s[40:43],s55 offen offset:128  lds
s_add_u32 m0,s57,9088
s_nop 0
buffer_load_dwordx4 v19,s[40:43],s55 offen offset:128  lds
v_add_u32 v26,16384,v20
v_add_u32 v27,16384,v21
T a[16:17],26,0
T a[18:19],26,512
T a[20:21],27,0
T a[22:23],27,512
T a[24:25],26,4096
T a[26:27],26,4608
T a[28:29],27,4096
T a[30:31],27,4608
s_waitcnt vmcnt(20) lgkmcnt(0)
s_barrier
s_add_u32 s55,s50,3670016
s_add_u32 m0,s57,16384
s_nop 0
buffer_load_dwordx4 v18,s[40:43],s55 offen offset:0  lds
s_add_u32 m0,s57,17408
s_nop 0
buffer_load_dwordx4 v19,s[40:43],s55 offen offset:0  lds
s_add_u32 m0,s57,24448
s_nop 0
buffer_load_dwordx4 v18,s[40:43],s55 offen offset:128  lds
s_add_u32 m0,s57,25472
s_nop 0
buffer_load_dwordx4 v19,s[40:43],s55 offen offset:128  lds
v_add_u32 v26,32768,v20
v_add_u32 v27,32768,v21
T a[32:33],26,0
T a[34:35],26,512
T a[36:37],27,0
T a[38:39],27,512
T a[40:41],26,4096
T a[42:43],26,4608
T a[44:45],27,4096
T a[46:47],27,4608
s_waitcnt vmcnt(20) lgkmcnt(0)
s_barrier
s_add_u32 s55,s50,4128768
s_add_u32 m0,s57,32768
s_nop 0
buffer_load_dwordx4 v18,s[40:43],s55 offen offset:0  lds
s_add_u32 m0,s57,33792
s_nop 0
buffer_load_dwordx4 v19,s[40:43],s55 offen offset:0  lds
s_add_u32 m0,s57,40832
s_nop 0
buffer_load_dwordx4 v18,s[40:43],s55 offen offset:128  lds
s_add_u32 m0,s57,41856
s_nop 0
buffer_load_dwordx4 v19,s[40:43],s55 offen offset:128  lds
v_add_u32 v26,49152,v20
v_add_u32 v27,49152,v21
T a[48:49],26,0
T a[50:51],26,512
T a[52:53],27,0
T a[54:55],27,512
T a[56:57],26,4096
T a[58:59],26,4608
T a[60:61],27,4096
T a[62:63],27,4608
s_waitcnt vmcnt(20) lgkmcnt(0)
s_barrier
s_add_u32 s55,s50,4587520
s_add_u32 m0,s57,49152
s_nop 0
buffer_load_dwordx4 v18,s[40:43],s55 offen offset:0  lds
s_add_u32 m0,s57,50176
s_nop 0
buffer_load_dwordx4 v19,s[40:43],s55 offen offset:0  lds
s_add_u32 m0,s57,57216
s_nop 0
buffer_load_dwordx4 v18,s[40:43],s55 offen offset:128  lds
s_add_u32 m0,s57,58240
s_nop 0
buffer_load_dwordx4 v19,s[40:43],s55 offen offset:128  lds
v_add_u32 v26,65536,v20
v_add_u32 v27,65536,v21
T a[64:65],26,0
T a[66:67],26,512
T a[68:69],27,0
T a[70:71],27,512
T a[72:73],26,4096
T a[74:75],26,4608
T a[76:77],27,4096
T a[78:79],27,4608
s_waitcnt vmcnt(20) lgkmcnt(0)
s_barrier
s_add_u32 s55,s50,5046272
s_add_u32 m0,s57,65536
s_nop 0
buffer_load_dwordx4 v18,s[40:43],s55 offen offset:0  lds
s_add_u32 m0,s57,66560
s_nop 0
buffer_load_dwordx4 v19,s[40:43],s55 offen offset:0  lds
s_add_u32 m0,s57,73600
s_nop 0
buffer_load_dwordx4 v18,s[40:43],s55 offen offset:128  lds
s_add_u32 m0,s57,74624
s_nop 0
buffer_load_dwordx4 v19,s[40:43],s55 offen offset:128  lds
v_add_u32 v26,81920,v20
v_add_u32 v27,81920,v21
T a[80:81],26,0
T a[82:83],26,512
T a[84:85],27,0
T a[86:87],27,512
T a[88:89],26,4096
T a[90:91],26,4608
T a[92:93],27,4096
T a[94:95],27,4608
s_waitcnt vmcnt(20) lgkmcnt(0)
s_barrier
s_add_u32 s55,s50,5505024
s_add_u32 m0,s57,81920
s_nop 0
buffer_load_dwordx4 v18,s[40:43],s55 offen offset:0  lds
s_add_u32 m0,s57,82944
s_nop 0
buffer_load_dwordx4 v19,s[40:43],s55 offen offset:0  lds
s_add_u32 m0,s57,89984
s_nop 0
buffer_load_dwordx4 v18,s[40:43],s55 offen offset:128  lds
s_add_u32 m0,s57,91008
s_nop 0
buffer_load_dwordx4 v19,s[40:43],s55 offen offset:128  lds
v_add_u32 v26,98304,v20
v_add_u32 v27,98304,v21
T a[96:97],26,0
T a[98:99],26,512
T a[100:101],27,0
T a[102:103],27,512
T a[104:105],26,4096
T a[106:107],26,4608
T a[108:109],27,4096
T a[110:111],27,4608
s_waitcnt vmcnt(20) lgkmcnt(0)
s_barrier
s_add_u32 s55,s50,5963776
s_add_u32 m0,s57,98304
s_nop 0
buffer_load_dwordx4 v18,s[40:43],s55 offen offset:0  lds
s_add_u32 m0,s57,99328
s_nop 0
buffer_load_dwordx4 v19,s[40:43],s55 offen offset:0  lds
s_add_u32 m0,s57,106368
s_nop 0
buffer_load_dwordx4 v18,s[40:43],s55 offen offset:128  lds
s_add_u32 m0,s57,107392
s_nop 0
buffer_load_dwordx4 v19,s[40:43],s55 offen offset:128  lds
v_add_u32 v26,0,v20
v_add_u32 v27,0,v21
T a[112:113],26,0
T a[114:115],26,512
T a[116:117],27,0
T a[118:119],27,512
T a[120:121],26,4096
T a[122:123],26,4608
T a[124:125],27,4096
T a[126:127],27,4608
s_waitcnt vmcnt(20) lgkmcnt(0)
s_barrier
s_add_u32 s55,s50,6422528
s_add_u32 m0,s57,0
s_nop 0
buffer_load_dwordx4 v18,s[40:43],s55 offen offset:0  lds
s_add_u32 m0,s57,1024
s_nop 0
buffer_load_dwordx4 v19,s[40:43],s55 offen offset:0  lds
s_add_u32 m0,s57,8064
s_nop 0
buffer_load_dwordx4 v18,s[40:43],s55 offen offset:128  lds
s_add_u32 m0,s57,9088
s_nop 0
buffer_load_dwordx4 v19,s[40:43],s55 offen offset:128  lds
v_add_u32 v26,16384,v20
v_add_u32 v27,16384,v21
T a[128:129],26,0
T a[130:131],26,512
T a[132:133],27,0
T a[134:135],27,512
T a[136:137],26,4096
T a[138:139],26,4608
T a[140:141],27,4096
T a[142:143],27,4608
s_waitcnt vmcnt(20) lgkmcnt(0)
s_barrier
s_add_u32 s55,s50,6881280
s_add_u32 m0,s57,16384
s_nop 0
buffer_load_dwordx4 v18,s[40:43],s55 offen offset:0  lds
s_add_u32 m0,s57,17408
s_nop 0
buffer_load_dwordx4 v19,s[40:43],s55 offen offset:0  lds
s_add_u32 m0,s57,24448
s_nop 0
buffer_load_dwordx4 v18,s[40:43],s55 offen offset:128  lds
s_add_u32 m0,s57,25472
s_nop 0
buffer_load_dwordx4 v19,s[40:43],s55 offen offset:128  lds
v_add_u32 v26,32768,v20
v_add_u32 v27,32768,v21
T a[144:145],26,0
T a[146:147],26,512
T a[148:149],27,0
T a[150:151],27,512
T a[152:153],26,4096
T a[154:155],26,4608
T a[156:157],27,4096
T a[158:159],27,4608
s_waitcnt vmcnt(20) lgkmcnt(0)
s_barrier
v_add_u32 v26,49152,v20
v_add_u32 v27,49152,v21
T a[160:161],26,0
T a[162:163],26,512
T a[164:165],27,0
T a[166:167],27,512
T a[168:169],26,4096
T a[170:171],26,4608
T a[172:173],27,4096
T a[174:175],27,4608
s_waitcnt vmcnt(16) lgkmcnt(0)
s_barrier
v_add_u32 v26,65536,v20
v_add_u32 v27,65536,v21
T a[176:177],26,0
T a[178:179],26,512
T a[180:181],27,0
T a[182:183],27,512
T a[184:185],26,4096
T a[186:187],26,4608
T a[188:189],27,4096
T a[190:191],27,4608
s_waitcnt vmcnt(12) lgkmcnt(0)
s_barrier
v_add_u32 v26,81920,v20
v_add_u32 v27,81920,v21
T a[192:193],26,0
T a[194:195],26,512
T a[196:197],27,0
T a[198:199],27,512
T a[200:201],26,4096
T a[202:203],26,4608
T a[204:205],27,4096
T a[206:207],27,4608
s_waitcnt vmcnt(8) lgkmcnt(0)
s_barrier
v_add_u32 v26,98304,v20
v_add_u32 v27,98304,v21
T a[208:209],26,0
T a[210:211],26,512
T a[212:213],27,0
T a[214:215],27,512
T a[216:217],26,4096
T a[218:219],26,4608
T a[220:221],27,4096
T a[222:223],27,4608
s_waitcnt vmcnt(4) lgkmcnt(0)
s_barrier
v_add_u32 v26,0,v20
v_add_u32 v27,0,v21
T a[224:225],26,0
T a[226:227],26,512
T a[228:229],27,0
T a[230:231],27,512
T a[232:233],26,4096
T a[234:235],26,4608
T a[236:237],27,4096
T a[238:239],27,4608
s_waitcnt vmcnt(0) lgkmcnt(0)
s_barrier
v_add_u32 v26,16384,v20
v_add_u32 v27,16384,v21
T a[240:241],26,0
T a[242:243],26,512
T a[244:245],27,0
T a[246:247],27,512
T a[248:249],26,4096
T a[250:251],26,4608
T a[252:253],27,4096
T a[254:255],27,4608
L 0
s_barrier
v_mov_b32 v34,0x3f803f80
v_mov_b32 v35,0x3f803f80
v_mov_b32 v36,0x3f803f80
v_mov_b32 v37,0x3f803f80
s_nop 1
v_mfma_f32_16x16x32_bf16 v[106:109],a[0:3],v[34:37],0
v_mfma_f32_16x16x32_bf16 v[110:113],a[4:7],v[34:37],0
M 106,a[8:11],34
M 110,a[12:15],34
M 106,a[16:19],34
M 110,a[20:23],34
M 106,a[24:27],34
M 110,a[28:31],34
M 106,a[32:35],34
M 110,a[36:39],34
M 106,a[40:43],34
M 110,a[44:47],34
M 106,a[48:51],34
M 110,a[52:55],34
M 106,a[56:59],34
M 110,a[60:63],34
M 106,a[64:67],34
M 110,a[68:71],34
M 106,a[72:75],34
M 110,a[76:79],34
M 106,a[80:83],34
M 110,a[84:87],34
M 106,a[88:91],34
M 110,a[92:95],34
M 106,a[96:99],34
M 110,a[100:103],34
M 106,a[104:107],34
M 110,a[108:111],34
M 106,a[112:115],34
M 110,a[116:119],34
M 106,a[120:123],34
M 110,a[124:127],34
M 106,a[128:131],34
M 110,a[132:135],34
M 106,a[136:139],34
M 110,a[140:143],34
M 106,a[144:147],34
M 110,a[148:151],34
M 106,a[152:155],34
M 110,a[156:159],34
M 106,a[160:163],34
M 110,a[164:167],34
M 106,a[168:171],34
M 110,a[172:175],34
M 106,a[176:179],34
)"
R"(M 110,a[180:183],34
M 106,a[184:187],34
M 110,a[188:191],34
M 106,a[192:195],34
M 110,a[196:199],34
M 106,a[200:203],34
M 110,a[204:207],34
M 106,a[208:211],34
M 110,a[212:215],34
M 106,a[216:219],34
M 110,a[220:223],34
M 106,a[224:227],34
M 110,a[228:231],34
M 106,a[232:235],34
M 110,a[236:239],34
M 106,a[240:243],34
M 110,a[244:247],34
M 106,a[248:251],34
M 110,a[252:255],34
v_mov_b32 v4,s53
s_mov_b32 s82,0
s_mov_b32 s80,0
s_mov_b32 s54,s45
s_mul_i32 s57,s44,4096
s_mul_i32 s55,s45,32768
s_add_u32 s55,s55,s57
s_add_u32 s55,s55,31457280
s_mul_i32 s57,s44,4096
s_mul_i32 s60,s46,32768
s_mul_i32 s61,s46,1024
s_mul_i32 s62,s46,114688
s_mul_i32 s63,s46,2560
s_mul_i32 s64,s46,1024
s_lshl_b32 s59,s44,8
s_add_u32 s59,s59,131072
s_mul_i32 s58,s45,1024
s_add_u32 s58,s58,109613056
s_mul_i32 s65,s45,114688
s_add_u32 s65,s65,s47
s_mul_i32 s68,s45,2560
s_add_u32 s68,s68,s48
s_mul_i32 s73,s45,1024
s_add_u32 s73,s73,107479040
s_mul_i32 s74,s45,32768
s_add_u32 s74,s74,s49
s_mul_i32 s75,s45,1024
s_add_u32 s75,s75,107479040
s_mov_b32 s66,0x7fff0000
s_mov_b32 s67,0x7fff0000
s_mov_b32 s69,0x7fff0000
s_mov_b32 s76,0x7fff0000
s_mov_b32 s77,0x7fff0000
s_mov_b32 s70,0x7fff0000
s_mov_b32 s71,0x7fff0000
s_mov_b32 s72,0x7fff0000
v_mov_b32 v1,v0
v_add_u32 v2,16384,v0
s_mov_b32 s84,16384
s_mov_b32 s86,0x3a800000
s_mov_b32 s87,0x3727c5ac
s_mov_b32 s88,0x409d30fc
s_mov_b32 s89,0xbeeff558
s_lshr_b32 s85,s44,1
s_mul_i32 s85,s85,127
v_lshlrev_b32 v8,22,v0
v_and_b32 v8,0x80000000,v8
v_or_b32 v8,v8,v5
v_add_u32 v2,32768,v0
s_lshl_b32 s84,s57,1
s_add_u32 s84,s84,32768
s_getpc_b64 s[40:41]
Lg3o_pc_%=:
s_lshl_b32 s78,s44,13
v_lshlrev_b32 v114,3,v0
v_add_u32 v114,s78,v114
v_min_u32 v114,Lcode_g3o_e-Lg3o_pc_%=-4,v114
s_mov_b32 m0,163584
s_nop 0
global_load_lds_dword v114,s[40:41]
s_add_u32 m0,s59,4096
s_nop 0
buffer_load_dword v8,s[36:39],s58 offen offset:0 sc1 lds
buffer_store_dword v4,v3,s[36:39],s71 offen offset:0 sc0 sc1
s_add_u32 m0,s59,5120
s_nop 0
buffer_load_dword v8,s[36:39],s58 offen offset:0 sc1 lds
buffer_store_dword v4,v3,s[36:39],s71 offen offset:0 sc0 sc1
s_mov_b32 s83,0
Lg3o_poll_%=:
s_add_u32 s78,s83,2
s_and_b32 s78,s78,3
s_lshl_b32 s78,s78,10
s_add_u32 s78,s78,4096
s_add_u32 m0,s59,s78
s_nop 0
buffer_load_dword v8,s[36:39],s58 offen offset:0 sc1 lds
s_and_b32 s78,s83,3
s_lshl_b32 s78,s78,10
v_add_u32 v114,s78,v6
s_waitcnt vmcnt(4)
s_barrier
ds_read_b32 v7,v114 offset:4096
buffer_store_dword v4,v3,s[36:39],s70 offen offset:0 sc0 sc1
s_mov_b32 s70,s71
s_mov_b32 s71,s72
s_mov_b32 s72,0x7fff0000
s_add_u32 s83,s83,1
s_add_u32 s82,s82,1
L 0
v_cmp_eq_u32 vcc,s53,v7
s_nop 0
s_cmp_eq_u32 vcc_lo,-1
s_cbranch_scc1 Lg3o_go_%=
s_cmp_lt_u32 s82,20000
s_cbranch_scc1 Lg3o_poll_%=
Lg3o_go_%=:
buffer_load_dword v130,v11,s[36:39],s73 offen offset:0 sc1
s_add_u32 s73,s73,s64
s_add_u32 m0,s57,0
s_add_u32 s56,s55,0
D 0,56,0
D 0,56,1024
D 0,56,2048
D 0,56,3072
s_add_u32 s58,s58,s61
s_add_u32 m0,s59,2048
s_nop 0
buffer_load_dword v8,s[36:39],s58 offen offset:0 sc1 lds
s_add_u32 m0,s57,16384
s_add_u32 s56,s55,16384
D 0,56,0
D 0,56,1024
D 0,56,2048
D 0,56,3072
s_add_u32 s55,s55,s60
s_sub_u32 s57,s84,s57
s_add_u32 s54,s54,s46
s_waitcnt vmcnt(4)
s_barrier
buffer_store_dword v4,v3,s[36:39],s70 offen offset:0 sc0 sc1
s_min_u32 s71,s71,s72
buffer_store_dword v4,v3,s[36:39],s71 offen offset:0 sc0 sc1
s_mov_b32 s70,0x7fff0000
s_mov_b32 s71,0x7fff0000
s_mov_b32 s72,0x7fff0000
s_cmp_eq_u32 s80,1
s_cbranch_scc1 Lg3o_st1_%=
s_branch Lg3o_st0_%=
Lg3o_gox_%=:
s_add_u32 m0,s59,2048
s_nop 0
buffer_load_dword v8,s[36:39],s58 offen offset:0 sc1 lds
s_waitcnt vmcnt(0)
s_barrier
buffer_store_dword v4,v3,s[36:39],s70 offen offset:0 sc0 sc1
s_min_u32 s71,s71,s72
buffer_store_dword v4,v3,s[36:39],s71 offen offset:0 sc0 sc1
s_mov_b32 s70,0x7fff0000
s_mov_b32 s71,0x7fff0000
s_mov_b32 s72,0x7fff0000
s_cmp_eq_u32 s80,1
s_cbranch_scc1 Lg3o_st1_%=
Lg3o_st0_%=:
ds_read_b32 v7,v6 offset:2048
R 34,1,0
R 38,1,1024
R 42,1,2048
R 46,1,3072
R 50,1,4096
L 5
v_cmp_eq_u32 vcc,s53,v7
L 3
s_cmp_eq_u32 vcc_lo,-1
s_cbranch_scc1 Lg3o_F0_%=
s_branch Lg3o_L0_%=
Lg3o_st1_%=:
ds_read_b32 v7,v6 offset:2048
R 34,2,0
R 38,2,1024
R 42,2,2048
R 46,2,3072
R 50,2,4096
L 5
v_cmp_eq_u32 vcc,s53,v7
L 3
s_cmp_eq_u32 vcc_lo,-1
s_cbranch_scc1 Lg3o_F1_%=
s_branch Lg3o_L1_%=
Lg3o_F0_%=:
v_mfma_f32_16x16x32_bf16 v[22:25],a[4:7],v[34:37],0
s_add_u32 s58,s58,s61
s_add_u32 m0,s59,0
R 54,1,5120
M 22,a[12:15],38
buffer_load_dword v8,s[36:39],s58 offen offset:0 sc1 lds
v_mfma_f32_16x16x32_bf16 v[18:21],a[0:3],v[34:37],0
s_add_u32 m0,s57,0
R 58,1,6144
v_add_f32_dpp v131,v130,v130 quad_perm:[1,0,3,2] row_mask:0xf bank_mask:0xf
M 18,a[8:11],38
L 3
s_add_u32 s56,s55,0
buffer_load_dword v130,v11,s[36:39],s73 offen offset:0 sc1
M 18,a[16:19],42
s_mov_b32 s66,s65
R 62,1,7168
v_add_f32_dpp v131,v131,v131 quad_perm:[2,3,0,1] row_mask:0xf bank_mask:0xf
M 18,a[24:27],46
D 0,56,0
M 22,a[20:23],42
s_add_u32 s65,s65,s62
R 66,1,8192
v_add_f32_dpp v134,v131,v131 row_half_mirror row_mask:0xf bank_mask:0xf
M 22,a[28:31],46
L 3
D 0,56,1024
M 22,a[36:39],50
s_add_u32 s54,s54,s46
R 70,1,9216
v_mov_b32_dpp v131,v134 row_ror:8 row_mask:0xf bank_mask:0xf
M 22,a[44:47],54
s_add_u32 s73,s73,s64
v_fma_f32 v131,-v134,v134,v131
v_max_f32 v131,s87,v131
M 18,a[32:35],50
R 74,1,10240
v_mul_f32 v134,v15,v134
M 18,a[40:43],54
L 3
D 0,56,2048
M 18,a[48:51],58
R 78,1,11264
v_fma_f32 v26,-v102,v106,v26
M 18,a[56:59],62
D 0,56,3072
M 22,a[52:55],58
s_add_u32 m0,s57,16384
R 82,1,12288
v_fma_f32 v27,-v102,v107,v27
M 22,a[60:63],62
L 3
v_rsq_f32 v132,v131
M 22,a[68:71],66
s_add_u32 s56,s55,16384
R 86,1,13312
v_fma_f32 v134,v132,v16,v134
M 22,a[76:79],70
s_add_u32 s55,s55,s60
ds_write_b32 v12,v134 offset:0
M 18,a[64:67],66
s_sub_u32 s57,s84,s57
R 90,1,14336
v_fma_f32 v28,-v102,v108,v28
M 18,a[72:75],70
L 4
v_fma_f32 v29,-v102,v109,v29
v_mul_f32 v26,v103,v26
M 18,a[80:83],74
s_waitcnt vmcnt(8)
R 94,1,15360
v_mul_f32 v27,v103,v27
M 18,a[88:91],78
s_barrier
v_mul_f32 v28,v103,v28
v_mul_f32 v29,v103,v29
M 22,a[84:87],74
R 34,1,16384
v_fma_f32 v114,v26,v26,s88
M 22,a[92:95],78
L 4
v_fma_f32 v115,v27,v27,s88
v_fma_f32 v116,v28,v28,s88
M 22,a[100:103],82
R 38,1,17408
v_fma_f32 v117,v29,v29,s88
M 22,a[108:111],86
D 0,56,0
M 18,a[96:99],82
R 42,1,18432
v_mul_f32 v114,v26,v114
M 18,a[104:107],86
L 3
v_exp_f32 v114,v114
M 18,a[112:115],90
R 46,1,19456
v_mul_f32 v115,v27,v115
M 18,a[120:123],94
v_exp_f32 v115,v115
M 22,a[116:119],90
R 50,1,20480
v_mul_f32 v116,v28,v116
M 22,a[124:127],94
L 3
v_exp_f32 v116,v116
M 22,a[132:135],34
R 54,1,21504
v_mul_f32 v117,v29,v117
M 22,a[140:143],38
D 0,56,1024
M 18,a[128:131],34
R 58,1,22528
v_fma_f32 v114,v114,s89,s89
M 18,a[136:139],38
L 3
v_exp_f32 v117,v117
M 18,a[144:147],42
R 62,1,23552
v_fma_f32 v115,v115,s89,s89
M 18,a[152:155],46
v_rcp_f32 v114,v114
M 22,a[148:151],42
R 66,1,24576
v_fma_f32 v116,v116,s89,s89
M 22,a[156:159],46
L 3
D 0,56,2048
M 22,a[164:167],50
R 70,1,25600
v_fma_f32 v117,v117,s89,s89
M 22,a[172:175],54
v_rcp_f32 v115,v115
M 18,a[160:163],50
R 74,1,26624
v_mul_f32 v26,v26,v114
M 18,a[168:171],54
L 3
v_rcp_f32 v116,v116
M 18,a[176:179],58
R 78,1,27648
v_mul_f32 v27,v27,v115
M 18,a[184:187],62
v_rcp_f32 v117,v117
M 22,a[180:183],58
R 82,1,28672
v_mul_f32 v28,v28,v116
M 22,a[188:191],62
L 3
D 0,56,3072
M 22,a[196:199],66
R 86,1,29696
v_mul_f32 v29,v29,v117
M 22,a[204:207],70
v_cvt_pk_bf16_f32 v122,v26,v27
v_cvt_pk_bf16_f32 v123,v28,v29
M 18,a[192:195],66
R 90,1,30720
v_fma_f32 v30,-v102,v110,v30
M 18,a[200:203],70
L 3
s_nop 0
v_fma_f32 v31,-v102,v111,v31
M 18,a[208:211],74
s_waitcnt vmcnt(4)
R 94,1,31744
v_fma_f32 v32,-v102,v112,v32
M 18,a[216:219],78
s_barrier
ds_read_b32 v7,v6 offset:0
ds_read_b64 v[98:99],v13 offset:0
M 22,a[212:215],74
R 34,2,0
v_fma_f32 v33,-v102,v113,v33
M 22,a[220:223],78
L 5
buffer_store_dword v4,v3,s[36:39],s70 offen offset:0 sc0 sc1
M 22,a[228:231],82
s_mov_b32 s70,s69
R 38,2,1024
v_mul_f32 v30,v103,v30
M 22,a[236:239],86
s_mov_b32 s69,s68
v_mul_f32 v31,v103,v31
v_mul_f32 v32,v103,v32
M 18,a[224:227],82
s_add_u32 s68,s68,s63
R 42,2,2048
v_mul_f32 v33,v103,v33
M 18,a[232:235],86
L 5
v_fma_f32 v118,v30,v30,s88
v_fma_f32 v119,v31,v31,s88
v_fma_f32 v120,v32,v32,s88
v_fma_f32 v121,v33,v33,s88
v_mul_f32 v118,v30,v118
v_mul_f32 v119,v31,v119
v_mul_f32 v120,v32,v120
v_mul_f32 v121,v33,v121
v_exp_f32 v118,v118
v_exp_f32 v119,v119
v_exp_f32 v120,v120
v_exp_f32 v121,v121
M 18,a[240:243],90
L 4
R 46,2,3072
v_fma_f32 v118,v118,s89,s89
v_fma_f32 v119,v119,s89,s89
v_fma_f32 v120,v120,s89,s89
v_fma_f32 v121,v121,s89,s89
v_rcp_f32 v118,v118
v_rcp_f32 v119,v119
v_rcp_f32 v120,v120
v_rcp_f32 v121,v121
v_cmp_eq_u32 vcc,s53,v7
M 18,a[248:251],94
v_mul_f32 v30,v30,v118
v_mul_f32 v31,v31,v119
v_mul_f32 v32,v32,v120
v_mul_f32 v33,v33,v121
v_cvt_pk_bf16_f32 v124,v30,v31
v_cvt_pk_bf16_f32 v125,v32,v33
M 22,a[244:247],90
L 2
s_cmp_eq_u32 vcc_lo,-1
R 50,2,4096
buffer_store_dwordx4 v[122:125],v0,s[36:39],s67 offen offset:0 sc0 sc1
M 22,a[252:255],94
s_cbranch_scc0 Lg3o_K1_%=
Lg3o_F1_%=:
v_mfma_f32_16x16x32_bf16 v[30:33],a[4:7],v[34:37],0
s_add_u32 s58,s58,s61
s_add_u32 m0,s59,1024
R 54,2,5120
M 30,a[12:15],38
buffer_load_dword v8,s[36:39],s58 offen offset:0 sc1 lds
v_mfma_f32_16x16x32_bf16 v[26:29],a[0:3],v[34:37],0
s_add_u32 m0,s57,0
R 58,2,6144
v_add_f32_dpp v131,v130,v130 quad_perm:[1,0,3,2] row_mask:0xf bank_mask:0xf
M 26,a[8:11],38
L 3
s_add_u32 s56,s55,0
buffer_load_dword v130,v11,s[36:39],s73 offen offset:0 sc1
M 26,a[16:19],42
s_mov_b32 s67,s65
R 62,2,7168
v_add_f32_dpp v131,v131,v131 quad_perm:[2,3,0,1] row_mask:0xf bank_mask:0xf
M 26,a[24:27],46
D 0,56,0
M 30,a[20:23],42
s_add_u32 s65,s65,s62
R 66,2,8192
v_add_f32_dpp v134,v131,v131 row_half_mirror row_mask:0xf bank_mask:0xf
M 30,a[28:31],46
L 3
D 0,56,1024
M 30,a[36:39],50
s_add_u32 s54,s54,s46
R 70,2,9216
v_mov_b32_dpp v131,v134 row_ror:8 row_mask:0xf bank_mask:0xf
M 30,a[44:47],54
s_add_u32 s73,s73,s64
v_fma_f32 v131,-v134,v134,v131
v_max_f32 v131,s87,v131
M 26,a[32:35],50
R 74,2,10240
v_mul_f32 v134,v15,v134
M 26,a[40:43],54
L 3
D 0,56,2048
M 26,a[48:51],58
R 78,2,11264
v_fma_f32 v18,-v98,v106,v18
M 26,a[56:59],62
D 0,56,3072
M 30,a[52:55],58
s_add_u32 m0,s57,16384
R 82,2,12288
v_fma_f32 v19,-v98,v107,v19
M 30,a[60:63],62
L 3
v_rsq_f32 v132,v131
M 30,a[68:71],66
s_add_u32 s56,s55,16384
R 86,2,13312
v_fma_f32 v134,v132,v16,v134
M 30,a[76:79],70
s_add_u32 s55,s55,s60
ds_write_b32 v12,v134 offset:2048
M 26,a[64:67],66
s_sub_u32 s57,s84,s57
R 90,2,14336
v_fma_f32 v20,-v98,v108,v20
M 26,a[72:75],70
L 4
v_fma_f32 v21,-v98,v109,v21
v_mul_f32 v18,v99,v18
M 26,a[80:83],74
s_waitcnt vmcnt(8)
R 94,2,15360
v_mul_f32 v19,v99,v19
M 26,a[88:91],78
s_barrier
v_mul_f32 v20,v99,v20
v_mul_f32 v21,v99,v21
M 30,a[84:87],74
R 34,2,16384
v_fma_f32 v114,v18,v18,s88
M 30,a[92:95],78
L 4
v_fma_f32 v115,v19,v19,s88
)"
R"(v_fma_f32 v116,v20,v20,s88
M 30,a[100:103],82
R 38,2,17408
v_fma_f32 v117,v21,v21,s88
M 30,a[108:111],86
D 0,56,0
M 26,a[96:99],82
R 42,2,18432
v_mul_f32 v114,v18,v114
M 26,a[104:107],86
L 3
v_exp_f32 v114,v114
M 26,a[112:115],90
R 46,2,19456
v_mul_f32 v115,v19,v115
M 26,a[120:123],94
v_exp_f32 v115,v115
M 30,a[116:119],90
R 50,2,20480
v_mul_f32 v116,v20,v116
M 30,a[124:127],94
L 3
v_exp_f32 v116,v116
M 30,a[132:135],34
R 54,2,21504
v_mul_f32 v117,v21,v117
M 30,a[140:143],38
D 0,56,1024
M 26,a[128:131],34
R 58,2,22528
v_fma_f32 v114,v114,s89,s89
M 26,a[136:139],38
L 3
v_exp_f32 v117,v117
M 26,a[144:147],42
R 62,2,23552
v_fma_f32 v115,v115,s89,s89
M 26,a[152:155],46
v_rcp_f32 v114,v114
M 30,a[148:151],42
R 66,2,24576
v_fma_f32 v116,v116,s89,s89
M 30,a[156:159],46
L 3
D 0,56,2048
M 30,a[164:167],50
R 70,2,25600
v_fma_f32 v117,v117,s89,s89
M 30,a[172:175],54
v_rcp_f32 v115,v115
M 26,a[160:163],50
R 74,2,26624
v_mul_f32 v18,v18,v114
M 26,a[168:171],54
L 3
v_rcp_f32 v116,v116
M 26,a[176:179],58
R 78,2,27648
v_mul_f32 v19,v19,v115
M 26,a[184:187],62
v_rcp_f32 v117,v117
M 30,a[180:183],58
R 82,2,28672
v_mul_f32 v20,v20,v116
M 30,a[188:191],62
L 3
D 0,56,3072
M 30,a[196:199],66
R 86,2,29696
v_mul_f32 v21,v21,v117
M 30,a[204:207],70
v_cvt_pk_bf16_f32 v122,v18,v19
v_cvt_pk_bf16_f32 v123,v20,v21
M 26,a[192:195],66
R 90,2,30720
v_fma_f32 v22,-v98,v110,v22
M 26,a[200:203],70
L 3
s_nop 0
v_fma_f32 v23,-v98,v111,v23
M 26,a[208:211],74
s_waitcnt vmcnt(4)
R 94,2,31744
v_fma_f32 v24,-v98,v112,v24
M 26,a[216:219],78
s_barrier
ds_read_b32 v7,v6 offset:1024
ds_read_b64 v[102:103],v13 offset:2048
M 30,a[212:215],74
R 34,1,0
v_fma_f32 v25,-v98,v113,v25
M 30,a[220:223],78
L 5
buffer_store_dword v4,v3,s[36:39],s70 offen offset:0 sc0 sc1
M 30,a[228:231],82
s_mov_b32 s70,s69
R 38,1,1024
v_mul_f32 v22,v99,v22
M 30,a[236:239],86
s_mov_b32 s69,s68
v_mul_f32 v23,v99,v23
v_mul_f32 v24,v99,v24
M 26,a[224:227],82
s_add_u32 s68,s68,s63
R 42,1,2048
v_mul_f32 v25,v99,v25
M 26,a[232:235],86
L 5
v_fma_f32 v118,v22,v22,s88
v_fma_f32 v119,v23,v23,s88
v_fma_f32 v120,v24,v24,s88
v_fma_f32 v121,v25,v25,s88
v_mul_f32 v118,v22,v118
v_mul_f32 v119,v23,v119
v_mul_f32 v120,v24,v120
v_mul_f32 v121,v25,v121
v_exp_f32 v118,v118
v_exp_f32 v119,v119
v_exp_f32 v120,v120
v_exp_f32 v121,v121
M 26,a[240:243],90
L 4
R 46,1,3072
v_fma_f32 v118,v118,s89,s89
v_fma_f32 v119,v119,s89,s89
v_fma_f32 v120,v120,s89,s89
v_fma_f32 v121,v121,s89,s89
v_rcp_f32 v118,v118
v_rcp_f32 v119,v119
v_rcp_f32 v120,v120
v_rcp_f32 v121,v121
v_cmp_eq_u32 vcc,s53,v7
M 26,a[248:251],94
v_mul_f32 v22,v22,v118
v_mul_f32 v23,v23,v119
v_mul_f32 v24,v24,v120
v_mul_f32 v25,v25,v121
v_cvt_pk_bf16_f32 v124,v22,v23
v_cvt_pk_bf16_f32 v125,v24,v25
M 30,a[244:247],90
L 2
s_cmp_eq_u32 vcc_lo,-1
R 50,1,4096
buffer_store_dwordx4 v[122:125],v0,s[36:39],s66 offen offset:0 sc0 sc1
M 30,a[252:255],94
s_cbranch_scc1 Lg3o_F0_%=
Lg3o_K0_%=:
Lg3o_L0_%=:
v_mfma_f32_16x16x32_bf16 v[22:25],a[4:7],v[34:37],0
s_add_u32 m0,s59,3072
R 54,1,5120
v_add_f32_dpp v131,v130,v130 quad_perm:[1,0,3,2] row_mask:0xf bank_mask:0xf
M 22,a[12:15],38
buffer_load_dword v8,s[36:39],s58 offen offset:0 sc1 lds
v_mfma_f32_16x16x32_bf16 v[18:21],a[0:3],v[34:37],0
s_mov_b32 s66,s65
R 58,1,6144
v_add_f32_dpp v131,v131,v131 quad_perm:[2,3,0,1] row_mask:0xf bank_mask:0xf
M 18,a[8:11],38
L 3
v_fma_f32 v26,-v102,v106,v26
v_fma_f32 v27,-v102,v107,v27
M 18,a[16:19],42
s_add_u32 s65,s65,s62
R 62,1,7168
v_add_f32_dpp v134,v131,v131 row_half_mirror row_mask:0xf bank_mask:0xf
M 18,a[24:27],46
v_fma_f32 v28,-v102,v108,v28
v_fma_f32 v29,-v102,v109,v29
M 22,a[20:23],42
R 66,1,8192
v_mov_b32_dpp v131,v134 row_ror:8 row_mask:0xf bank_mask:0xf
M 22,a[28:31],46
L 3
v_fma_f32 v131,-v134,v134,v131
v_max_f32 v131,s87,v131
M 22,a[36:39],50
R 70,1,9216
v_mul_f32 v134,v15,v134
M 22,a[44:47],54
v_rsq_f32 v132,v131
M 18,a[32:35],50
R 74,1,10240
v_fma_f32 v134,v132,v16,v134
M 18,a[40:43],54
L 3
ds_write_b32 v12,v134 offset:0
M 18,a[48:51],58
R 78,1,11264
v_mul_f32 v26,v103,v26
M 18,a[56:59],62
v_mul_f32 v27,v103,v27
v_mul_f32 v28,v103,v28
M 22,a[52:55],58
R 82,1,12288
v_mul_f32 v29,v103,v29
M 22,a[60:63],62
L 4
v_fma_f32 v114,v26,v26,s88
v_fma_f32 v115,v27,v27,s88
M 22,a[68:71],66
R 86,1,13312
v_fma_f32 v116,v28,v28,s88
M 22,a[76:79],70
v_fma_f32 v117,v29,v29,s88
v_mul_f32 v114,v26,v114
M 18,a[64:67],66
R 90,1,14336
v_mul_f32 v115,v27,v115
M 18,a[72:75],70
L 3
v_exp_f32 v114,v114
M 18,a[80:83],74
s_waitcnt vmcnt(0)
R 94,1,15360
v_mul_f32 v116,v28,v116
M 18,a[88:91],78
s_barrier
ds_read_b32 v7,v6 offset:3072
v_mul_f32 v117,v29,v117
M 22,a[84:87],74
R 34,1,16384
v_fma_f32 v114,v114,s89,s89
M 22,a[92:95],78
L 4
buffer_store_dword v4,v3,s[36:39],s70 offen offset:0 sc0 sc1
M 22,a[100:103],82
s_mov_b32 s70,0x7fff0000
R 38,1,17408
v_fma_f32 v30,-v102,v110,v30
M 22,a[108:111],86
v_exp_f32 v115,v115
M 18,a[96:99],82
R 42,1,18432
v_fma_f32 v115,v115,s89,s89
M 18,a[104:107],86
L 4
L 3
v_cmp_eq_u32 vcc,s53,v7
M 18,a[112:115],90
R 46,1,19456
v_fma_f32 v31,-v102,v111,v31
M 18,a[120:123],94
v_exp_f32 v116,v116
M 22,a[116:119],90
s_cmp_eq_u32 vcc_lo,-1
R 50,1,20480
v_fma_f32 v116,v116,s89,s89
s_cbranch_scc0 Lg3o_X0_%=
M 22,a[124:127],94
L 3
s_add_u32 m0,s57,0
s_add_u32 s56,s55,0
buffer_load_dword v130,v11,s[36:39],s73 offen offset:0 sc1
M 22,a[132:135],34
s_add_u32 s58,s58,s61
R 54,1,21504
v_fma_f32 v32,-v102,v112,v32
M 22,a[140:143],38
D 0,56,0
M 18,a[128:131],34
s_add_u32 s54,s54,s46
R 58,1,22528
v_fma_f32 v33,-v102,v113,v33
M 18,a[136:139],38
L 3
v_exp_f32 v117,v117
M 18,a[144:147],42
s_add_u32 s73,s73,s64
R 62,1,23552
v_fma_f32 v117,v117,s89,s89
M 18,a[152:155],46
D 0,56,1024
M 22,a[148:151],42
R 66,1,24576
v_mul_f32 v30,v103,v30
M 22,a[156:159],46
L 3
v_rcp_f32 v114,v114
M 22,a[164:167],50
R 70,1,25600
v_mul_f32 v26,v26,v114
M 22,a[172:175],54
D 0,56,2048
M 18,a[160:163],50
R 74,1,26624
v_mul_f32 v31,v103,v31
M 18,a[168:171],54
L 3
v_rcp_f32 v115,v115
M 18,a[176:179],58
R 78,1,27648
v_mul_f32 v27,v27,v115
M 18,a[184:187],62
D 0,56,3072
M 22,a[180:183],58
s_add_u32 m0,s59,0
R 82,1,28672
v_cvt_pk_bf16_f32 v122,v26,v27
M 22,a[188:191],62
L 3
buffer_load_dword v8,s[36:39],s58 offen offset:0 sc1 lds
M 22,a[196:199],66
s_add_u32 m0,s57,16384
R 86,1,29696
v_mul_f32 v32,v103,v32
M 22,a[204:207],70
s_add_u32 s56,s55,16384
v_rcp_f32 v116,v116
M 18,a[192:195],66
s_add_u32 s55,s55,s60
R 90,1,30720
v_mul_f32 v28,v28,v116
M 18,a[200:203],70
L 3
s_nop 0
D 0,56,0
M 18,a[208:211],74
s_waitcnt vmcnt(1)
R 94,1,31744
v_mul_f32 v33,v103,v33
M 18,a[216:219],78
s_barrier
ds_read_b32 v7,v6 offset:0
ds_read_b64 v[98:99],v13 offset:0
M 22,a[212:215],74
s_sub_u32 s57,s84,s57
R 34,2,0
v_fma_f32 v118,v30,v30,s88
M 22,a[220:223],78
L 5
buffer_store_dword v4,v3,s[36:39],s70 offen offset:0 sc0 sc1
M 22,a[228:231],82
s_mov_b32 s70,s69
R 38,2,1024
v_fma_f32 v119,v31,v31,s88
M 22,a[236:239],86
D 0,56,1024
M 18,a[224:227],82
s_mov_b32 s69,s68
R 42,2,2048
v_fma_f32 v120,v32,v32,s88
M 18,a[232:235],86
L 5
v_fma_f32 v121,v33,v33,s88
v_mul_f32 v118,v30,v118
v_mul_f32 v119,v31,v119
v_mul_f32 v120,v32,v120
v_mul_f32 v121,v33,v121
v_exp_f32 v118,v118
v_exp_f32 v119,v119
v_exp_f32 v120,v120
v_exp_f32 v121,v121
M 18,a[240:243],90
L 4
R 46,2,3072
v_rcp_f32 v117,v117
v_fma_f32 v118,v118,s89,s89
v_fma_f32 v119,v119,s89,s89
v_fma_f32 v120,v120,s89,s89
v_fma_f32 v121,v121,s89,s89
v_rcp_f32 v118,v118
v_rcp_f32 v119,v119
v_rcp_f32 v120,v120
v_rcp_f32 v121,v121
v_cmp_eq_u32 vcc,s53,v7
M 18,a[248:251],94
s_add_u32 s68,s68,s63
v_mul_f32 v29,v29,v117
v_cvt_pk_bf16_f32 v123,v28,v29
v_mul_f32 v30,v30,v118
v_mul_f32 v31,v31,v119
v_mul_f32 v32,v32,v120
v_mul_f32 v33,v33,v121
v_cvt_pk_bf16_f32 v124,v30,v31
v_cvt_pk_bf16_f32 v125,v32,v33
M 22,a[244:247],90
L 2
s_cmp_eq_u32 vcc_lo,-1
R 50,2,4096
buffer_store_dwordx4 v[122:125],v0,s[36:39],s67 offen offset:0 sc0 sc1
M 22,a[252:255],94
D 0,56,2048
D 0,56,3072
buffer_store_dword v4,v3,s[36:39],s71 offen offset:0 sc0 sc1
buffer_store_dword v4,v3,s[36:39],s71 offen offset:0 sc0 sc1
s_cbranch_scc1 Lg3o_F1_%=
s_branch Lg3o_K1_%=
Lg3o_X0_%=:
M 22,a[124:127],94
L 3
s_add_u32 m0,s59,0
v_fma_f32 v32,-v102,v112,v32
M 22,a[132:135],34
R 54,1,21504
v_fma_f32 v33,-v102,v113,v33
M 22,a[140:143],38
buffer_load_dword v8,s[36:39],s58 offen offset:0 sc1 lds
M 18,a[128:131],34
R 58,1,22528
v_mul_f32 v30,v103,v30
M 18,a[136:139],38
L 3
v_exp_f32 v117,v117
M 18,a[144:147],42
R 62,1,23552
v_fma_f32 v117,v117,s89,s89
M 18,a[152:155],46
v_rcp_f32 v114,v114
M 22,a[148:151],42
R 66,1,24576
v_mul_f32 v26,v26,v114
M 22,a[156:159],46
L 3
v_rcp_f32 v115,v115
M 22,a[164:167],50
R 70,1,25600
v_mul_f32 v27,v27,v115
M 22,a[172:175],54
v_rcp_f32 v116,v116
M 18,a[160:163],50
R 74,1,26624
v_mul_f32 v28,v28,v116
M 18,a[168:171],54
L 3
v_rcp_f32 v117,v117
M 18,a[176:179],58
R 78,1,27648
v_mul_f32 v29,v29,v117
M 18,a[184:187],62
v_cvt_pk_bf16_f32 v122,v26,v27
v_cvt_pk_bf16_f32 v123,v28,v29
M 22,a[180:183],58
R 82,1,28672
v_mul_f32 v31,v103,v31
M 22,a[188:191],62
L 3
v_mul_f32 v32,v103,v32
v_mul_f32 v33,v103,v33
M 22,a[196:199],66
R 86,1,29696
v_fma_f32 v118,v30,v30,s88
M 22,a[204:207],70
v_fma_f32 v119,v31,v31,s88
v_fma_f32 v120,v32,v32,s88
M 18,a[192:195],66
R 90,1,30720
v_fma_f32 v121,v33,v33,s88
M 18,a[200:203],70
L 3
s_nop 0
v_mul_f32 v118,v30,v118
M 18,a[208:211],74
s_waitcnt vmcnt(0)
R 94,1,31744
v_mul_f32 v119,v31,v119
M 18,a[216:219],78
s_barrier
buffer_store_dword v4,v3,s[36:39],s70 offen offset:0 sc0 sc1
M 22,a[212:215],74
s_mov_b32 s70,s69
ds_read_b32 v7,v6 offset:0
ds_read_b64 v[98:99],v13 offset:0
M 22,a[220:223],78
L 4
v_mul_f32 v120,v32,v120
v_mul_f32 v121,v33,v121
M 22,a[228:231],82
s_mov_b32 s69,s68
v_exp_f32 v118,v118
M 22,a[236:239],86
s_add_u32 s68,s68,s63
v_exp_f32 v119,v119
M 18,a[224:227],82
v_exp_f32 v120,v120
M 18,a[232:235],86
L 2
v_exp_f32 v121,v121
M 18,a[240:243],90
L 1
v_fma_f32 v118,v118,s89,s89
v_fma_f32 v119,v119,s89,s89
v_fma_f32 v120,v120,s89,s89
v_fma_f32 v121,v121,s89,s89
v_rcp_f32 v118,v118
v_rcp_f32 v119,v119
v_rcp_f32 v120,v120
v_rcp_f32 v121,v121
M 18,a[248:251],94
v_mul_f32 v30,v30,v118
v_mul_f32 v31,v31,v119
v_mul_f32 v32,v32,v120
v_mul_f32 v33,v33,v121
v_cvt_pk_bf16_f32 v124,v30,v31
v_cvt_pk_bf16_f32 v125,v32,v33
M 22,a[244:247],90
buffer_store_dwordx4 v[122:125],v0,s[36:39],s67 offen offset:0 sc0 sc1
v_cmp_eq_u32 vcc,s53,v7
M 22,a[252:255],94
s_mov_b32 s80,1
)"
R"(s_cmp_eq_u32 vcc_lo,-1
s_cbranch_scc0 Lg3o_xn0_%=
buffer_load_dword v130,v11,s[36:39],s73 offen offset:0 sc1
L 0
v_fma_f32 v18,-v98,v106,v18
v_fma_f32 v19,-v98,v107,v19
v_fma_f32 v20,-v98,v108,v20
v_fma_f32 v21,-v98,v109,v21
v_mul_f32 v18,v99,v18
v_mul_f32 v19,v99,v19
s_add_u32 s73,s73,s64
s_add_u32 m0,s57,0
s_add_u32 s56,s55,0
D 0,56,0
v_mul_f32 v20,v99,v20
v_mul_f32 v21,v99,v21
v_fma_f32 v114,v18,v18,s88
v_fma_f32 v115,v19,v19,s88
v_fma_f32 v116,v20,v20,s88
v_fma_f32 v117,v21,v21,s88
D 0,56,1024
v_mul_f32 v114,v18,v114
v_mul_f32 v115,v19,v115
v_mul_f32 v116,v20,v116
v_mul_f32 v117,v21,v117
v_exp_f32 v114,v114
v_exp_f32 v115,v115
D 0,56,2048
v_exp_f32 v116,v116
v_exp_f32 v117,v117
v_fma_f32 v114,v114,s89,s89
v_fma_f32 v115,v115,s89,s89
v_fma_f32 v116,v116,s89,s89
v_fma_f32 v117,v117,s89,s89
D 0,56,3072
v_rcp_f32 v114,v114
v_rcp_f32 v115,v115
v_rcp_f32 v116,v116
v_rcp_f32 v117,v117
v_mul_f32 v18,v18,v114
v_mul_f32 v19,v19,v115
s_add_u32 s58,s58,s61
s_add_u32 m0,s57,16384
s_add_u32 s56,s55,16384
D 0,56,0
v_mul_f32 v20,v20,v116
v_mul_f32 v21,v21,v117
v_cvt_pk_bf16_f32 v122,v18,v19
v_cvt_pk_bf16_f32 v123,v20,v21
v_fma_f32 v22,-v98,v110,v22
v_fma_f32 v23,-v98,v111,v23
D 0,56,1024
v_fma_f32 v24,-v98,v112,v24
v_fma_f32 v25,-v98,v113,v25
v_mul_f32 v22,v99,v22
v_mul_f32 v23,v99,v23
v_mul_f32 v24,v99,v24
v_mul_f32 v25,v99,v25
D 0,56,2048
v_fma_f32 v118,v22,v22,s88
v_fma_f32 v119,v23,v23,s88
v_fma_f32 v120,v24,v24,s88
v_fma_f32 v121,v25,v25,s88
v_mul_f32 v118,v22,v118
v_mul_f32 v119,v23,v119
D 0,56,3072
v_mul_f32 v120,v24,v120
v_mul_f32 v121,v25,v121
v_exp_f32 v118,v118
v_exp_f32 v119,v119
v_exp_f32 v120,v120
v_exp_f32 v121,v121
s_add_u32 s55,s55,s60
s_sub_u32 s57,s84,s57
s_add_u32 s54,s54,s46
v_fma_f32 v118,v118,s89,s89
v_fma_f32 v119,v119,s89,s89
v_fma_f32 v120,v120,s89,s89
v_fma_f32 v121,v121,s89,s89
v_rcp_f32 v118,v118
v_rcp_f32 v119,v119
v_rcp_f32 v120,v120
v_rcp_f32 v121,v121
v_mul_f32 v22,v22,v118
v_mul_f32 v23,v23,v119
v_mul_f32 v24,v24,v120
v_mul_f32 v25,v25,v121
v_cvt_pk_bf16_f32 v124,v22,v23
v_cvt_pk_bf16_f32 v125,v24,v25
buffer_store_dwordx4 v[122:125],v0,s[36:39],s66 offen offset:0 sc0 sc1
s_mov_b32 s72,s69
s_mov_b32 s69,0x7fff0000
s_mov_b32 s66,0x7fff0000
s_mov_b32 s76,0x7fff0000
s_branch Lg3o_gox_%=
Lg3o_xn0_%=:
s_cmp_ge_u32 s54,256
s_cbranch_scc1 Lg3o_fin0_%=
s_add_u32 m0,s59,4096
s_nop 0
buffer_load_dword v8,s[36:39],s58 offen offset:0 sc1 lds
buffer_store_dword v4,v3,s[36:39],s71 offen offset:0 sc0 sc1
s_add_u32 m0,s59,5120
s_nop 0
buffer_load_dword v8,s[36:39],s58 offen offset:0 sc1 lds
buffer_store_dword v4,v3,s[36:39],s71 offen offset:0 sc0 sc1
s_mov_b32 s83,0
L 0
v_fma_f32 v18,-v98,v106,v18
v_fma_f32 v19,-v98,v107,v19
v_fma_f32 v20,-v98,v108,v20
v_fma_f32 v21,-v98,v109,v21
v_mul_f32 v18,v99,v18
v_mul_f32 v19,v99,v19
v_mul_f32 v20,v99,v20
v_mul_f32 v21,v99,v21
v_fma_f32 v114,v18,v18,s88
v_fma_f32 v115,v19,v19,s88
v_fma_f32 v116,v20,v20,s88
v_fma_f32 v117,v21,v21,s88
v_mul_f32 v114,v18,v114
v_mul_f32 v115,v19,v115
v_mul_f32 v116,v20,v116
v_mul_f32 v117,v21,v117
v_exp_f32 v114,v114
v_exp_f32 v115,v115
v_exp_f32 v116,v116
v_exp_f32 v117,v117
v_fma_f32 v114,v114,s89,s89
v_fma_f32 v115,v115,s89,s89
v_fma_f32 v116,v116,s89,s89
v_fma_f32 v117,v117,s89,s89
v_rcp_f32 v114,v114
v_rcp_f32 v115,v115
v_rcp_f32 v116,v116
v_rcp_f32 v117,v117
v_mul_f32 v18,v18,v114
v_mul_f32 v19,v19,v115
v_mul_f32 v20,v20,v116
v_mul_f32 v21,v21,v117
v_cvt_pk_bf16_f32 v122,v18,v19
v_cvt_pk_bf16_f32 v123,v20,v21
v_fma_f32 v22,-v98,v110,v22
v_fma_f32 v23,-v98,v111,v23
v_fma_f32 v24,-v98,v112,v24
v_fma_f32 v25,-v98,v113,v25
v_mul_f32 v22,v99,v22
v_mul_f32 v23,v99,v23
v_mul_f32 v24,v99,v24
v_mul_f32 v25,v99,v25
v_fma_f32 v118,v22,v22,s88
v_fma_f32 v119,v23,v23,s88
v_fma_f32 v120,v24,v24,s88
v_fma_f32 v121,v25,v25,s88
v_mul_f32 v118,v22,v118
v_mul_f32 v119,v23,v119
v_mul_f32 v120,v24,v120
v_mul_f32 v121,v25,v121
v_exp_f32 v118,v118
v_exp_f32 v119,v119
v_exp_f32 v120,v120
v_exp_f32 v121,v121
v_fma_f32 v118,v118,s89,s89
v_fma_f32 v119,v119,s89,s89
v_fma_f32 v120,v120,s89,s89
v_fma_f32 v121,v121,s89,s89
v_rcp_f32 v118,v118
v_rcp_f32 v119,v119
v_rcp_f32 v120,v120
v_rcp_f32 v121,v121
v_mul_f32 v22,v22,v118
v_mul_f32 v23,v23,v119
v_mul_f32 v24,v24,v120
v_mul_f32 v25,v25,v121
v_cvt_pk_bf16_f32 v124,v22,v23
v_cvt_pk_bf16_f32 v125,v24,v25
buffer_store_dwordx4 v[122:125],v0,s[36:39],s66 offen offset:0 sc0 sc1
s_mov_b32 s72,s69
s_mov_b32 s69,0x7fff0000
s_mov_b32 s66,0x7fff0000
s_mov_b32 s76,0x7fff0000
s_branch Lg3o_poll_%=
Lg3o_fin0_%=:
L 0
v_fma_f32 v18,-v98,v106,v18
v_fma_f32 v19,-v98,v107,v19
v_fma_f32 v20,-v98,v108,v20
v_fma_f32 v21,-v98,v109,v21
v_mul_f32 v18,v99,v18
v_mul_f32 v19,v99,v19
v_mul_f32 v20,v99,v20
v_mul_f32 v21,v99,v21
v_fma_f32 v114,v18,v18,s88
v_fma_f32 v115,v19,v19,s88
v_fma_f32 v116,v20,v20,s88
v_fma_f32 v117,v21,v21,s88
v_mul_f32 v114,v18,v114
v_mul_f32 v115,v19,v115
v_mul_f32 v116,v20,v116
v_mul_f32 v117,v21,v117
v_exp_f32 v114,v114
v_exp_f32 v115,v115
v_exp_f32 v116,v116
v_exp_f32 v117,v117
v_fma_f32 v114,v114,s89,s89
v_fma_f32 v115,v115,s89,s89
v_fma_f32 v116,v116,s89,s89
v_fma_f32 v117,v117,s89,s89
v_rcp_f32 v114,v114
v_rcp_f32 v115,v115
v_rcp_f32 v116,v116
v_rcp_f32 v117,v117
v_mul_f32 v18,v18,v114
v_mul_f32 v19,v19,v115
v_mul_f32 v20,v20,v116
v_mul_f32 v21,v21,v117
v_cvt_pk_bf16_f32 v122,v18,v19
v_cvt_pk_bf16_f32 v123,v20,v21
v_fma_f32 v22,-v98,v110,v22
v_fma_f32 v23,-v98,v111,v23
v_fma_f32 v24,-v98,v112,v24
v_fma_f32 v25,-v98,v113,v25
v_mul_f32 v22,v99,v22
v_mul_f32 v23,v99,v23
v_mul_f32 v24,v99,v24
v_mul_f32 v25,v99,v25
v_fma_f32 v118,v22,v22,s88
v_fma_f32 v119,v23,v23,s88
v_fma_f32 v120,v24,v24,s88
v_fma_f32 v121,v25,v25,s88
v_mul_f32 v118,v22,v118
v_mul_f32 v119,v23,v119
v_mul_f32 v120,v24,v120
v_mul_f32 v121,v25,v121
v_exp_f32 v118,v118
v_exp_f32 v119,v119
v_exp_f32 v120,v120
v_exp_f32 v121,v121
v_fma_f32 v118,v118,s89,s89
v_fma_f32 v119,v119,s89,s89
v_fma_f32 v120,v120,s89,s89
v_fma_f32 v121,v121,s89,s89
v_rcp_f32 v118,v118
v_rcp_f32 v119,v119
v_rcp_f32 v120,v120
v_rcp_f32 v121,v121
v_mul_f32 v22,v22,v118
v_mul_f32 v23,v23,v119
v_mul_f32 v24,v24,v120
v_mul_f32 v25,v25,v121
v_cvt_pk_bf16_f32 v124,v22,v23
v_cvt_pk_bf16_f32 v125,v24,v25
buffer_store_dwordx4 v[122:125],v0,s[36:39],s66 offen offset:0 sc0 sc1
s_mov_b32 s72,s69
s_mov_b32 s69,0x7fff0000
s_mov_b32 s66,0x7fff0000
s_mov_b32 s76,0x7fff0000
s_branch Lg3o_done_%=
Lg3o_K1_%=:
Lg3o_L1_%=:
v_mfma_f32_16x16x32_bf16 v[30:33],a[4:7],v[34:37],0
s_add_u32 m0,s59,3072
R 54,2,5120
v_add_f32_dpp v131,v130,v130 quad_perm:[1,0,3,2] row_mask:0xf bank_mask:0xf
M 30,a[12:15],38
buffer_load_dword v8,s[36:39],s58 offen offset:0 sc1 lds
v_mfma_f32_16x16x32_bf16 v[26:29],a[0:3],v[34:37],0
s_mov_b32 s67,s65
R 58,2,6144
v_add_f32_dpp v131,v131,v131 quad_perm:[2,3,0,1] row_mask:0xf bank_mask:0xf
M 26,a[8:11],38
L 3
v_fma_f32 v18,-v98,v106,v18
v_fma_f32 v19,-v98,v107,v19
M 26,a[16:19],42
s_add_u32 s65,s65,s62
R 62,2,7168
v_add_f32_dpp v134,v131,v131 row_half_mirror row_mask:0xf bank_mask:0xf
M 26,a[24:27],46
v_fma_f32 v20,-v98,v108,v20
v_fma_f32 v21,-v98,v109,v21
M 30,a[20:23],42
R 66,2,8192
v_mov_b32_dpp v131,v134 row_ror:8 row_mask:0xf bank_mask:0xf
M 30,a[28:31],46
L 3
v_fma_f32 v131,-v134,v134,v131
v_max_f32 v131,s87,v131
M 30,a[36:39],50
R 70,2,9216
v_mul_f32 v134,v15,v134
M 30,a[44:47],54
v_rsq_f32 v132,v131
M 26,a[32:35],50
R 74,2,10240
v_fma_f32 v134,v132,v16,v134
M 26,a[40:43],54
L 3
ds_write_b32 v12,v134 offset:2048
M 26,a[48:51],58
R 78,2,11264
v_mul_f32 v18,v99,v18
M 26,a[56:59],62
v_mul_f32 v19,v99,v19
v_mul_f32 v20,v99,v20
M 30,a[52:55],58
R 82,2,12288
v_mul_f32 v21,v99,v21
M 30,a[60:63],62
L 4
v_fma_f32 v114,v18,v18,s88
v_fma_f32 v115,v19,v19,s88
M 30,a[68:71],66
R 86,2,13312
v_fma_f32 v116,v20,v20,s88
M 30,a[76:79],70
v_fma_f32 v117,v21,v21,s88
v_mul_f32 v114,v18,v114
M 26,a[64:67],66
R 90,2,14336
v_mul_f32 v115,v19,v115
M 26,a[72:75],70
L 3
v_exp_f32 v114,v114
M 26,a[80:83],74
s_waitcnt vmcnt(0)
R 94,2,15360
v_mul_f32 v116,v20,v116
M 26,a[88:91],78
s_barrier
ds_read_b32 v7,v6 offset:3072
v_mul_f32 v117,v21,v117
M 30,a[84:87],74
R 34,2,16384
v_fma_f32 v114,v114,s89,s89
M 30,a[92:95],78
L 4
buffer_store_dword v4,v3,s[36:39],s70 offen offset:0 sc0 sc1
M 30,a[100:103],82
s_mov_b32 s70,0x7fff0000
R 38,2,17408
v_fma_f32 v22,-v98,v110,v22
M 30,a[108:111],86
v_exp_f32 v115,v115
M 26,a[96:99],82
R 42,2,18432
v_fma_f32 v115,v115,s89,s89
M 26,a[104:107],86
L 4
L 3
v_cmp_eq_u32 vcc,s53,v7
M 26,a[112:115],90
R 46,2,19456
v_fma_f32 v23,-v98,v111,v23
M 26,a[120:123],94
v_exp_f32 v116,v116
M 30,a[116:119],90
s_cmp_eq_u32 vcc_lo,-1
R 50,2,20480
v_fma_f32 v116,v116,s89,s89
s_cbranch_scc0 Lg3o_X1_%=
M 30,a[124:127],94
L 3
s_add_u32 m0,s57,0
s_add_u32 s56,s55,0
buffer_load_dword v130,v11,s[36:39],s73 offen offset:0 sc1
M 30,a[132:135],34
s_add_u32 s58,s58,s61
R 54,2,21504
v_fma_f32 v24,-v98,v112,v24
M 30,a[140:143],38
D 0,56,0
M 26,a[128:131],34
s_add_u32 s54,s54,s46
R 58,2,22528
v_fma_f32 v25,-v98,v113,v25
M 26,a[136:139],38
L 3
v_exp_f32 v117,v117
M 26,a[144:147],42
s_add_u32 s73,s73,s64
R 62,2,23552
v_fma_f32 v117,v117,s89,s89
M 26,a[152:155],46
D 0,56,1024
M 30,a[148:151],42
R 66,2,24576
v_mul_f32 v22,v99,v22
M 30,a[156:159],46
L 3
v_rcp_f32 v114,v114
M 30,a[164:167],50
R 70,2,25600
v_mul_f32 v18,v18,v114
M 30,a[172:175],54
D 0,56,2048
M 26,a[160:163],50
R 74,2,26624
v_mul_f32 v23,v99,v23
M 26,a[168:171],54
L 3
v_rcp_f32 v115,v115
M 26,a[176:179],58
R 78,2,27648
v_mul_f32 v19,v19,v115
M 26,a[184:187],62
D 0,56,3072
M 30,a[180:183],58
s_add_u32 m0,s59,1024
R 82,2,28672
v_cvt_pk_bf16_f32 v122,v18,v19
M 30,a[188:191],62
L 3
buffer_load_dword v8,s[36:39],s58 offen offset:0 sc1 lds
M 30,a[196:199],66
s_add_u32 m0,s57,16384
R 86,2,29696
v_mul_f32 v24,v99,v24
M 30,a[204:207],70
s_add_u32 s56,s55,16384
v_rcp_f32 v116,v116
M 26,a[192:195],66
s_add_u32 s55,s55,s60
R 90,2,30720
v_mul_f32 v20,v20,v116
M 26,a[200:203],70
L 3
s_nop 0
D 0,56,0
M 26,a[208:211],74
s_waitcnt vmcnt(1)
R 94,2,31744
v_mul_f32 v25,v99,v25
M 26,a[216:219],78
s_barrier
ds_read_b32 v7,v6 offset:1024
ds_read_b64 v[102:103],v13 offset:2048
M 30,a[212:215],74
s_sub_u32 s57,s84,s57
R 34,1,0
v_fma_f32 v118,v22,v22,s88
M 30,a[220:223],78
L 5
buffer_store_dword v4,v3,s[36:39],s70 offen offset:0 sc0 sc1
M 30,a[228:231],82
s_mov_b32 s70,s69
R 38,1,1024
v_fma_f32 v119,v23,v23,s88
M 30,a[236:239],86
D 0,56,1024
M 26,a[224:227],82
s_mov_b32 s69,s68
R 42,1,2048
v_fma_f32 v120,v24,v24,s88
M 26,a[232:235],86
L 5
v_fma_f32 v121,v25,v25,s88
v_mul_f32 v118,v22,v118
v_mul_f32 v119,v23,v119
v_mul_f32 v120,v24,v120
v_mul_f32 v121,v25,v121
v_exp_f32 v118,v118
v_exp_f32 v119,v119
v_exp_f32 v120,v120
v_exp_f32 v121,v121
M 26,a[240:243],90
L 4
R 46,1,3072
v_rcp_f32 v117,v117
v_fma_f32 v118,v118,s89,s89
v_fma_f32 v119,v119,s89,s89
v_fma_f32 v120,v120,s89,s89
v_fma_f32 v121,v121,s89,s89
v_rcp_f32 v118,v118
v_rcp_f32 v119,v119
v_rcp_f32 v120,v120
v_rcp_f32 v121,v121
v_cmp_eq_u32 vcc,s53,v7
M 26,a[248:251],94
s_add_u32 s68,s68,s63
v_mul_f32 v21,v21,v117
v_cvt_pk_bf16_f32 v123,v20,v21
v_mul_f32 v22,v22,v118
v_mul_f32 v23,v23,v119
v_mul_f32 v24,v24,v120
v_mul_f32 v25,v25,v121
v_cvt_pk_bf16_f32 v124,v22,v23
v_cvt_pk_bf16_f32 v125,v24,v25
M 30,a[244:247],90
L 2
s_cmp_eq_u32 vcc_lo,-1
R 50,1,4096
buffer_store_dwordx4 v[122:125],v0,s[36:39],s66 offen offset:0 sc0 sc1
M 30,a[252:255],94
D 0,56,2048
D 0,56,3072
buffer_store_dword v4,v3,s[36:39],s71 offen offset:0 sc0 sc1
buffer_store_dword v4,v3,s[36:39],s71 offen offset:0 sc0 sc1
s_cbranch_scc1 Lg3o_F0_%=
s_branch Lg3o_K0_%=
)"
R"(Lg3o_X1_%=:
M 30,a[124:127],94
L 3
s_add_u32 m0,s59,1024
v_fma_f32 v24,-v98,v112,v24
M 30,a[132:135],34
R 54,2,21504
v_fma_f32 v25,-v98,v113,v25
M 30,a[140:143],38
buffer_load_dword v8,s[36:39],s58 offen offset:0 sc1 lds
M 26,a[128:131],34
R 58,2,22528
v_mul_f32 v22,v99,v22
M 26,a[136:139],38
L 3
v_exp_f32 v117,v117
M 26,a[144:147],42
R 62,2,23552
v_fma_f32 v117,v117,s89,s89
M 26,a[152:155],46
v_rcp_f32 v114,v114
M 30,a[148:151],42
R 66,2,24576
v_mul_f32 v18,v18,v114
M 30,a[156:159],46
L 3
v_rcp_f32 v115,v115
M 30,a[164:167],50
R 70,2,25600
v_mul_f32 v19,v19,v115
M 30,a[172:175],54
v_rcp_f32 v116,v116
M 26,a[160:163],50
R 74,2,26624
v_mul_f32 v20,v20,v116
M 26,a[168:171],54
L 3
v_rcp_f32 v117,v117
M 26,a[176:179],58
R 78,2,27648
v_mul_f32 v21,v21,v117
M 26,a[184:187],62
v_cvt_pk_bf16_f32 v122,v18,v19
v_cvt_pk_bf16_f32 v123,v20,v21
M 30,a[180:183],58
R 82,2,28672
v_mul_f32 v23,v99,v23
M 30,a[188:191],62
L 3
v_mul_f32 v24,v99,v24
v_mul_f32 v25,v99,v25
M 30,a[196:199],66
R 86,2,29696
v_fma_f32 v118,v22,v22,s88
M 30,a[204:207],70
v_fma_f32 v119,v23,v23,s88
v_fma_f32 v120,v24,v24,s88
M 26,a[192:195],66
R 90,2,30720
v_fma_f32 v121,v25,v25,s88
M 26,a[200:203],70
L 3
s_nop 0
v_mul_f32 v118,v22,v118
M 26,a[208:211],74
s_waitcnt vmcnt(0)
R 94,2,31744
v_mul_f32 v119,v23,v119
M 26,a[216:219],78
s_barrier
buffer_store_dword v4,v3,s[36:39],s70 offen offset:0 sc0 sc1
M 30,a[212:215],74
s_mov_b32 s70,s69
ds_read_b32 v7,v6 offset:1024
ds_read_b64 v[102:103],v13 offset:2048
M 30,a[220:223],78
L 4
v_mul_f32 v120,v24,v120
v_mul_f32 v121,v25,v121
M 30,a[228:231],82
s_mov_b32 s69,s68
v_exp_f32 v118,v118
M 30,a[236:239],86
s_add_u32 s68,s68,s63
v_exp_f32 v119,v119
M 26,a[224:227],82
v_exp_f32 v120,v120
M 26,a[232:235],86
L 2
v_exp_f32 v121,v121
M 26,a[240:243],90
L 1
v_fma_f32 v118,v118,s89,s89
v_fma_f32 v119,v119,s89,s89
v_fma_f32 v120,v120,s89,s89
v_fma_f32 v121,v121,s89,s89
v_rcp_f32 v118,v118
v_rcp_f32 v119,v119
v_rcp_f32 v120,v120
v_rcp_f32 v121,v121
M 26,a[248:251],94
v_mul_f32 v22,v22,v118
v_mul_f32 v23,v23,v119
v_mul_f32 v24,v24,v120
v_mul_f32 v25,v25,v121
v_cvt_pk_bf16_f32 v124,v22,v23
v_cvt_pk_bf16_f32 v125,v24,v25
M 30,a[244:247],90
buffer_store_dwordx4 v[122:125],v0,s[36:39],s66 offen offset:0 sc0 sc1
v_cmp_eq_u32 vcc,s53,v7
M 30,a[252:255],94
s_mov_b32 s80,0
s_cmp_eq_u32 vcc_lo,-1
s_cbranch_scc0 Lg3o_xn1_%=
buffer_load_dword v130,v11,s[36:39],s73 offen offset:0 sc1
L 0
v_fma_f32 v26,-v102,v106,v26
v_fma_f32 v27,-v102,v107,v27
v_fma_f32 v28,-v102,v108,v28
v_fma_f32 v29,-v102,v109,v29
v_mul_f32 v26,v103,v26
v_mul_f32 v27,v103,v27
s_add_u32 s73,s73,s64
s_add_u32 m0,s57,0
s_add_u32 s56,s55,0
D 0,56,0
v_mul_f32 v28,v103,v28
v_mul_f32 v29,v103,v29
v_fma_f32 v114,v26,v26,s88
v_fma_f32 v115,v27,v27,s88
v_fma_f32 v116,v28,v28,s88
v_fma_f32 v117,v29,v29,s88
D 0,56,1024
v_mul_f32 v114,v26,v114
v_mul_f32 v115,v27,v115
v_mul_f32 v116,v28,v116
v_mul_f32 v117,v29,v117
v_exp_f32 v114,v114
v_exp_f32 v115,v115
D 0,56,2048
v_exp_f32 v116,v116
v_exp_f32 v117,v117
v_fma_f32 v114,v114,s89,s89
v_fma_f32 v115,v115,s89,s89
v_fma_f32 v116,v116,s89,s89
v_fma_f32 v117,v117,s89,s89
D 0,56,3072
v_rcp_f32 v114,v114
v_rcp_f32 v115,v115
v_rcp_f32 v116,v116
v_rcp_f32 v117,v117
v_mul_f32 v26,v26,v114
v_mul_f32 v27,v27,v115
s_add_u32 s58,s58,s61
s_add_u32 m0,s57,16384
s_add_u32 s56,s55,16384
D 0,56,0
v_mul_f32 v28,v28,v116
v_mul_f32 v29,v29,v117
v_cvt_pk_bf16_f32 v122,v26,v27
v_cvt_pk_bf16_f32 v123,v28,v29
v_fma_f32 v30,-v102,v110,v30
v_fma_f32 v31,-v102,v111,v31
D 0,56,1024
v_fma_f32 v32,-v102,v112,v32
v_fma_f32 v33,-v102,v113,v33
v_mul_f32 v30,v103,v30
v_mul_f32 v31,v103,v31
v_mul_f32 v32,v103,v32
v_mul_f32 v33,v103,v33
D 0,56,2048
v_fma_f32 v118,v30,v30,s88
v_fma_f32 v119,v31,v31,s88
v_fma_f32 v120,v32,v32,s88
v_fma_f32 v121,v33,v33,s88
v_mul_f32 v118,v30,v118
v_mul_f32 v119,v31,v119
D 0,56,3072
v_mul_f32 v120,v32,v120
v_mul_f32 v121,v33,v121
v_exp_f32 v118,v118
v_exp_f32 v119,v119
v_exp_f32 v120,v120
v_exp_f32 v121,v121
s_add_u32 s55,s55,s60
s_sub_u32 s57,s84,s57
s_add_u32 s54,s54,s46
v_fma_f32 v118,v118,s89,s89
v_fma_f32 v119,v119,s89,s89
v_fma_f32 v120,v120,s89,s89
v_fma_f32 v121,v121,s89,s89
v_rcp_f32 v118,v118
v_rcp_f32 v119,v119
v_rcp_f32 v120,v120
v_rcp_f32 v121,v121
v_mul_f32 v30,v30,v118
v_mul_f32 v31,v31,v119
v_mul_f32 v32,v32,v120
v_mul_f32 v33,v33,v121
v_cvt_pk_bf16_f32 v124,v30,v31
v_cvt_pk_bf16_f32 v125,v32,v33
buffer_store_dwordx4 v[122:125],v0,s[36:39],s67 offen offset:0 sc0 sc1
s_mov_b32 s72,s69
s_mov_b32 s69,0x7fff0000
s_mov_b32 s67,0x7fff0000
s_mov_b32 s77,0x7fff0000
s_branch Lg3o_gox_%=
Lg3o_xn1_%=:
s_cmp_ge_u32 s54,256
s_cbranch_scc1 Lg3o_fin1_%=
s_add_u32 m0,s59,4096
s_nop 0
buffer_load_dword v8,s[36:39],s58 offen offset:0 sc1 lds
buffer_store_dword v4,v3,s[36:39],s71 offen offset:0 sc0 sc1
s_add_u32 m0,s59,5120
s_nop 0
buffer_load_dword v8,s[36:39],s58 offen offset:0 sc1 lds
buffer_store_dword v4,v3,s[36:39],s71 offen offset:0 sc0 sc1
s_mov_b32 s83,0
L 0
v_fma_f32 v26,-v102,v106,v26
v_fma_f32 v27,-v102,v107,v27
v_fma_f32 v28,-v102,v108,v28
v_fma_f32 v29,-v102,v109,v29
v_mul_f32 v26,v103,v26
v_mul_f32 v27,v103,v27
v_mul_f32 v28,v103,v28
v_mul_f32 v29,v103,v29
v_fma_f32 v114,v26,v26,s88
v_fma_f32 v115,v27,v27,s88
v_fma_f32 v116,v28,v28,s88
v_fma_f32 v117,v29,v29,s88
v_mul_f32 v114,v26,v114
v_mul_f32 v115,v27,v115
v_mul_f32 v116,v28,v116
v_mul_f32 v117,v29,v117
v_exp_f32 v114,v114
v_exp_f32 v115,v115
v_exp_f32 v116,v116
v_exp_f32 v117,v117
v_fma_f32 v114,v114,s89,s89
v_fma_f32 v115,v115,s89,s89
v_fma_f32 v116,v116,s89,s89
v_fma_f32 v117,v117,s89,s89
v_rcp_f32 v114,v114
v_rcp_f32 v115,v115
v_rcp_f32 v116,v116
v_rcp_f32 v117,v117
v_mul_f32 v26,v26,v114
v_mul_f32 v27,v27,v115
v_mul_f32 v28,v28,v116
v_mul_f32 v29,v29,v117
v_cvt_pk_bf16_f32 v122,v26,v27
v_cvt_pk_bf16_f32 v123,v28,v29
v_fma_f32 v30,-v102,v110,v30
v_fma_f32 v31,-v102,v111,v31
v_fma_f32 v32,-v102,v112,v32
v_fma_f32 v33,-v102,v113,v33
v_mul_f32 v30,v103,v30
v_mul_f32 v31,v103,v31
v_mul_f32 v32,v103,v32
v_mul_f32 v33,v103,v33
v_fma_f32 v118,v30,v30,s88
v_fma_f32 v119,v31,v31,s88
v_fma_f32 v120,v32,v32,s88
v_fma_f32 v121,v33,v33,s88
v_mul_f32 v118,v30,v118
v_mul_f32 v119,v31,v119
v_mul_f32 v120,v32,v120
v_mul_f32 v121,v33,v121
v_exp_f32 v118,v118
v_exp_f32 v119,v119
v_exp_f32 v120,v120
v_exp_f32 v121,v121
v_fma_f32 v118,v118,s89,s89
v_fma_f32 v119,v119,s89,s89
v_fma_f32 v120,v120,s89,s89
v_fma_f32 v121,v121,s89,s89
v_rcp_f32 v118,v118
v_rcp_f32 v119,v119
v_rcp_f32 v120,v120
v_rcp_f32 v121,v121
v_mul_f32 v30,v30,v118
v_mul_f32 v31,v31,v119
v_mul_f32 v32,v32,v120
v_mul_f32 v33,v33,v121
v_cvt_pk_bf16_f32 v124,v30,v31
v_cvt_pk_bf16_f32 v125,v32,v33
buffer_store_dwordx4 v[122:125],v0,s[36:39],s67 offen offset:0 sc0 sc1
s_mov_b32 s72,s69
s_mov_b32 s69,0x7fff0000
s_mov_b32 s67,0x7fff0000
s_mov_b32 s77,0x7fff0000
s_branch Lg3o_poll_%=
Lg3o_fin1_%=:
L 0
v_fma_f32 v26,-v102,v106,v26
v_fma_f32 v27,-v102,v107,v27
v_fma_f32 v28,-v102,v108,v28
v_fma_f32 v29,-v102,v109,v29
v_mul_f32 v26,v103,v26
v_mul_f32 v27,v103,v27
v_mul_f32 v28,v103,v28
v_mul_f32 v29,v103,v29
v_fma_f32 v114,v26,v26,s88
v_fma_f32 v115,v27,v27,s88
v_fma_f32 v116,v28,v28,s88
v_fma_f32 v117,v29,v29,s88
v_mul_f32 v114,v26,v114
v_mul_f32 v115,v27,v115
v_mul_f32 v116,v28,v116
v_mul_f32 v117,v29,v117
v_exp_f32 v114,v114
v_exp_f32 v115,v115
v_exp_f32 v116,v116
v_exp_f32 v117,v117
v_fma_f32 v114,v114,s89,s89
v_fma_f32 v115,v115,s89,s89
v_fma_f32 v116,v116,s89,s89
v_fma_f32 v117,v117,s89,s89
v_rcp_f32 v114,v114
v_rcp_f32 v115,v115
v_rcp_f32 v116,v116
v_rcp_f32 v117,v117
v_mul_f32 v26,v26,v114
v_mul_f32 v27,v27,v115
v_mul_f32 v28,v28,v116
v_mul_f32 v29,v29,v117
v_cvt_pk_bf16_f32 v122,v26,v27
v_cvt_pk_bf16_f32 v123,v28,v29
v_fma_f32 v30,-v102,v110,v30
v_fma_f32 v31,-v102,v111,v31
v_fma_f32 v32,-v102,v112,v32
v_fma_f32 v33,-v102,v113,v33
v_mul_f32 v30,v103,v30
v_mul_f32 v31,v103,v31
v_mul_f32 v32,v103,v32
v_mul_f32 v33,v103,v33
v_fma_f32 v118,v30,v30,s88
v_fma_f32 v119,v31,v31,s88
v_fma_f32 v120,v32,v32,s88
v_fma_f32 v121,v33,v33,s88
v_mul_f32 v118,v30,v118
v_mul_f32 v119,v31,v119
v_mul_f32 v120,v32,v120
v_mul_f32 v121,v33,v121
v_exp_f32 v118,v118
v_exp_f32 v119,v119
v_exp_f32 v120,v120
v_exp_f32 v121,v121
v_fma_f32 v118,v118,s89,s89
v_fma_f32 v119,v119,s89,s89
v_fma_f32 v120,v120,s89,s89
v_fma_f32 v121,v121,s89,s89
v_rcp_f32 v118,v118
v_rcp_f32 v119,v119
v_rcp_f32 v120,v120
v_rcp_f32 v121,v121
v_mul_f32 v30,v30,v118
v_mul_f32 v31,v31,v119
v_mul_f32 v32,v32,v120
v_mul_f32 v33,v33,v121
v_cvt_pk_bf16_f32 v124,v30,v31
v_cvt_pk_bf16_f32 v125,v32,v33
buffer_store_dwordx4 v[122:125],v0,s[36:39],s67 offen offset:0 sc0 sc1
s_mov_b32 s72,s69
s_mov_b32 s69,0x7fff0000
s_mov_b32 s67,0x7fff0000
s_mov_b32 s77,0x7fff0000
s_branch Lg3o_done_%=
Lg3o_done_%=:
s_waitcnt vmcnt(1)
s_barrier
buffer_store_dword v4,v3,s[36:39],s70 offen offset:0 sc0 sc1
s_waitcnt vmcnt(1)
s_barrier
buffer_store_dword v4,v3,s[36:39],s72 offen offset:0 sc0 sc1
s_waitcnt vmcnt(0) lgkmcnt(0)
s_barrier
s_endpgm
Lcode_g3o_e:
.purgem M
.purgem R
.purgem T
.purgem D
.purgem L
)"
  :: "s"(ws), "s"(__builtin_amdgcn_readfirstlane(sWave)), "s"(__builtin_amdgcn_readfirstlane(sG0)), "s"(__builtin_amdgcn_readfirstlane(sStep)), "s"(__builtin_amdgcn_readfirstlane(sOutOff)), "s"(__builtin_amdgcn_readfirstlane(sFoutOff)), "s"(__builtin_amdgcn_readfirstlane(sAuxOff)), "s"(__builtin_amdgcn_readfirstlane(sWw)), "s"(__builtin_amdgcn_readfirstlane(sBuf)), "s"(__builtin_amdgcn_readfirstlane(sExp)), "v"(vL16), "v"(vFlag), "v"(vSnapOff), "v"(vSnapRd), "v"(vWd0), "v"(vWd1), "v"(vWr0), "v"(vWr1), "v"(vWr2), "v"(vOutS), "v"(vAux0), "v"(vAux1), "v"(vAux2), "v"(vAux3), "v"(vAn), "v"(vAe), "s"(w3), "s"(out) : "v255", "v0", "v1", "v2", "v3", "v4", "v5", "v6", "v7", "v8", "v9", "v10", "v11", "v12", "v13", "v14", "v15", "v16", "v17", "v18", "v19", "v20", "v21", "v22", "v23", "v24", "v25", "v26", "v27", "v28", "v29", "v30", "v31", "v32", "v33", "v34", "v35", "v36", "v37", "v38", "v39", "v40", "v41", "v42", "v43", "v44", "v45", "v46", "v47", "v48", "v49", "v50", "v51", "v52", "v53", "v54", "v55", "v56", "v57", "v58", "v59", "v60", "v61", "v62", "v63", "v64", "v65", "v66", "v67", "v68", "v69", "v70", "v71", "v72", "v73", "v74", "v75", "v76", "v77", "v78", "v79", "v80", "v81", "v82", "v83", "v84", "v85", "v86", "v87", "v88", "v89", "v90", "v91", "v92", "v93", "v94", "v95", "v96", "v97", "v98", "v99", "v100", "v101", "v102", "v103", "v104", "v105", "v106", "v107", "v108", "v109", "v110", "v111", "v112", "v113", "v114", "v115", "v116", "v117", "v118", "v119", "v120", "v121", "v122", "v123", "v124", "v125", "v126", "v127", "v128", "v129", "v130", "v131", "v132", "v133", "v134", "v135", "v136", "v137", "v138", "v139", "v140", "v141", "v142", "v143", "v144", "v145", "v146", "v147", "v148", "v149", "v150", "v151", "v152", "v153", "v154", "v155", "v156", "v157", "v158", "v159", "v160", "v161", "v162", "v163", "v164", "v165", "v166", "v167", "v168", "v169", "v170", "v171", "v172", "v173", "v174", "v175", "v176", "v177", "v178", "v179", "v180", "v181", "v182", "v183", "v184", "v185", "v186", "v187", "v188", "v189", "v190", "v191", "v192", "v193", "v194", "v195", "v196", "v197", "v198", "v199", "v200", "v201", "v202", "v203", "v204", "v205", "v206", "v207", "v208", "v209", "v210", "v211", "v212", "v213", "v214", "v215", "v216", "v217", "v218", "v219", "v220", "v221", "v222", "v223", "v224", "v225", "v226", "v227", "v228", "v229", "v230", "v231", "a0", "a1", "a2", "a3", "a4", "a5", "a6", "a7", "a8", "a9", "a10", "a11", "a12", "a13", "a14", "a15", "a16", "a17", "a18", "a19", "a20", "a21", "a22", "a23", "a24", "a25", "a26", "a27", "a28", "a29", "a30", "a31", "a32", "a33", "a34", "a35", "a36", "a37", "a38", "a39", "a40", "a41", "a42", "a43", "a44", "a45", "a46", "a47", "a48", "a49", "a50", "a51", "a52", "a53", "a54", "a55", "a56", "a57", "a58", "a59", "a60", "a61", "a62", "a63", "a64", "a65", "a66", "a67", "a68", "a69", "a70", "a71", "a72", "a73", "a74", "a75", "a76", "a77", "a78", "a79", "a80", "a81", "a82", "a83", "a84", "a85", "a86", "a87", "a88", "a89", "a90", "a91", "a92", "a93", "a94", "a95", "a96", "a97", "a98", "a99", "a100", "a101", "a102", "a103", "a104", "a105", "a106", "a107", "a108", "a109", "a110", "a111", "a112", "a113", "a114", "a115", "a116", "a117", "a118", "a119", "a120", "a121", "a122", "a123", "a124", "a125", "a126", "a127", "a128", "a129", "a130", "a131", "a132", "a133", "a134", "a135", "a136", "a137", "a138", "a139", "a140", "a141", "a142", "a143", "a144", "a145", "a146", "a147", "a148", "a149", "a150", "a151", "a152", "a153", "a154", "a155", "a156", "a157", "a158", "a159", "a160", "a161", "a162", "a163", "a164", "a165", "a166", "a167", "a168", "a169", "a170", "a171", "a172", "a173", "a174", "a175", "a176", "a177", "a178", "a179", "a180", "a181", "a182", "a183", "a184", "a185", "a186", "a187", "a188", "a189", "a190", "a191", "a192", "a193", "a194", "a195", "a196", "a197", "a198", "a199", "a200", "a201", "a202", "a203", "a204", "a205", "a206", "a207", "a208", "a209", "a210", "a211", "a212", "a213", "a214", "a215", "a216", "a217", "a218", "a219", "a220", "a221", "a222", "a223", "a224", "a225", "a226", "a227", "a228", "a229", "a230", "a231", "a232", "a233", "a234", "a235", "a236", "a237", "a238", "a239", "a240", "a241", "a242", "a243", "a244", "a245", "a246", "a247", "a248", "a249", "a250", "a251", "a252", "a253", "a254", "a255", "s36", "s37", "s38", "s39", "s40", "s41", "s42", "s43", "s44", "s45", "s46", "s47", "s48", "s49", "s50", "s51", "s52", "s53", "s54", "s55", "s56", "s57", "s58", "s59", "s60", "s61", "s62", "s63", "s64", "s65", "s66", "s67", "s68", "s69", "s70", "s71", "s72", "s73", "s74", "s75", "s76", "s77", "s78", "s79", "s80", "s81", "s82", "s83", "s84", "s85", "s86", "s87", "s88", "s89", "s90", "s91", "s92", "s93", "s94", "s95", "s96", "s97", "vcc", "scc", "memory"); }
    __builtin_unreachable();
  }
  if (kind < 3) {
    const unsigned sStep = 4;
    COMMON(kind == 1 ? 8 : 16, kind == 1 ? (fj >> 2) * 512 + (fj & 3) * FSX : fj < 8 ? 1024 + fj * FSH : (fj & 2) * 1024 + (fj - 8) * FSH, 1024, 131072, 2048, 256, kind == 1 ? 0 : 32768);
    const int sr = l15 - ((wave & 2) ? -1 : 1), sr2 = l15 - ((wave & 2) ? -2 : 2), nx = (slice >> 1) & 1;
    auto src = [&](int r_) { return (unsigned)((r_ & 16) ? 1024 + lg * 64 + (r_ & 3) * 16 : (lg * 16 + r_) * 16); };
    const unsigned vOutS = src(sr2), vSp = (sr2 & 16) ? OOB : vOutS;
    (void)vWr2;
    const unsigned vAn = kind == 1 ? HU + vL16 : src(sr);
    const unsigned vAe = kind == 1 ? ((l15 >> 2) == (nx ? 0 : 3) ? (nx ? 0 : 2 * HU) + 1024 + lg * 64 + (l15 & 3) * 16 : OOB) : (sr & 16) ? OOB : vAn;
    const unsigned vAux0 = l15 * 16, vAux1 = 139264 + (l15 * 2 + (lg & 1)) * 16 + wave * 4 + (lg >> 1) * 512, vAux2 = 139264 + (l15 * 2 + (lg & 1)) * 16;
    const unsigned vAux3 = (wave == 0 && lg < 2) ? slice * 128 + l15 * 8 + lg * 4 : OOB;
    const unsigned sOutOff = kind == 1 ? OFF_HS - HU + (slice * 4 + wave) * 1280 : OFF_X1 + (slice * 4 + wave) * 1024, sFoutOff = kind == 1 ? OFF_FH + slice * FSH : OFF_FX1 + slice * FS1;
    const unsigned sAuxOff = OFF_XP + (slice * 4 + wave) * 1024, sBuf = kind, sWw = 16 * wave * 2048 + slice * 256;
    const u16* w12 = kind == 1 ? w1 : w2;
    const unsigned sExp = __builtin_amdgcn_readfirstlane(ep);
    unsigned epOut;
    asm volatile(
R"(.macro M d,w,x
v_mfma_f32_16x16x32_bf16 v[\d:\d+3], \w, v[\x:\x+3], v[\d:\d+3]
.endm
.macro R d,a,o
ds_read_b128 v[\d:\d+3], v\a offset:\o
.endm
.macro T d,a,o
ds_read_b64_tr_b16 \d, v\a offset:\o
.endm
.macro D a,s,o
buffer_load_dwordx4 v\a, s[36:39], s\s offen offset:\o sc1 lds
.endm
.macro L c
s_waitcnt lgkmcnt(\c)
.endm
Lcode_g12_b:
s_cmp_eq_u32 %9,2
s_cbranch_scc1 Lg2_begin_%=
s_mov_b64 s[36:37],%1
s_mov_b32 s38,111243392
s_mov_b32 s39,0x00020000
s_mov_b32 s44,%2
s_mov_b32 s45,%3
s_mov_b32 s46,%4
s_mov_b32 s47,%5
s_mov_b32 s48,%6
s_mov_b32 s49,%7
s_mov_b32 s50,%8
s_mov_b32 s51,%9
s_mov_b32 s53,%10
v_mov_b32 v0,%11
v_mov_b32 v3,%12
v_mov_b32 v5,%13
v_mov_b32 v6,%14
v_mov_b32 v18,%15
v_mov_b32 v19,%16
v_mov_b32 v20,%17
v_mov_b32 v21,%18
v_mov_b32 v9,%19
v_mov_b32 v10,%20
v_mov_b32 v11,%21
v_mov_b32 v12,%22
v_mov_b32 v13,%23
v_mov_b32 v14,%24
v_mov_b32 v15,%25
v_mov_b32 v16,%26
s_mov_b64 s[40:41],%27
s_mov_b32 s42,2097152
s_mov_b32 s43,0x00020000
s_lshl_b32 s57,s44,11
s_waitcnt vmcnt(20) lgkmcnt(0)
s_barrier
s_add_u32 s55,s50,786432
s_add_u32 m0,s57,98304
s_nop 0
buffer_load_dwordx4 v18,s[40:43],s55 offen offset:0  lds
s_add_u32 m0,s57,99328
s_nop 0
buffer_load_dwordx4 v19,s[40:43],s55 offen offset:0  lds
s_add_u32 m0,s57,106368
s_nop 0
buffer_load_dwordx4 v18,s[40:43],s55 offen offset:128  lds
s_add_u32 m0,s57,107392
s_nop 0
buffer_load_dwordx4 v19,s[40:43],s55 offen offset:128  lds
v_add_u32 v26,0,v20
v_add_u32 v27,0,v21
T a[0:1],26,0
T a[2:3],26,512
T a[4:5],27,0
T a[6:7],27,512
T a[8:9],26,4096
T a[10:11],26,4608
T a[12:13],27,4096
T a[14:15],27,4608
s_waitcnt vmcnt(20) lgkmcnt(0)
s_barrier
s_add_u32 s55,s50,917504
s_add_u32 m0,s57,0
s_nop 0
buffer_load_dwordx4 v18,s[40:43],s55 offen offset:0  lds
s_add_u32 m0,s57,1024
s_nop 0
buffer_load_dwordx4 v19,s[40:43],s55 offen offset:0  lds
s_add_u32 m0,s57,8064
s_nop 0
buffer_load_dwordx4 v18,s[40:43],s55 offen offset:128  lds
s_add_u32 m0,s57,9088
s_nop 0
buffer_load_dwordx4 v19,s[40:43],s55 offen offset:128  lds
v_add_u32 v26,16384,v20
v_add_u32 v27,16384,v21
T a[16:17],26,0
T a[18:19],26,512
T a[20:21],27,0
T a[22:23],27,512
T a[24:25],26,4096
T a[26:27],26,4608
T a[28:29],27,4096
T a[30:31],27,4608
s_waitcnt vmcnt(20) lgkmcnt(0)
s_barrier
s_add_u32 s55,s50,1048576
s_add_u32 m0,s57,16384
s_nop 0
buffer_load_dwordx4 v18,s[40:43],s55 offen offset:0  lds
s_add_u32 m0,s57,17408
s_nop 0
buffer_load_dwordx4 v19,s[40:43],s55 offen offset:0  lds
s_add_u32 m0,s57,24448
s_nop 0
buffer_load_dwordx4 v18,s[40:43],s55 offen offset:128  lds
s_add_u32 m0,s57,25472
s_nop 0
buffer_load_dwordx4 v19,s[40:43],s55 offen offset:128  lds
v_add_u32 v26,32768,v20
v_add_u32 v27,32768,v21
T a[32:33],26,0
T a[34:35],26,512
T a[36:37],27,0
T a[38:39],27,512
T a[40:41],26,4096
T a[42:43],26,4608
T a[44:45],27,4096
T a[46:47],27,4608
s_waitcnt vmcnt(20) lgkmcnt(0)
s_barrier
s_add_u32 s55,s50,1179648
s_add_u32 m0,s57,32768
s_nop 0
buffer_load_dwordx4 v18,s[40:43],s55 offen offset:0  lds
s_add_u32 m0,s57,33792
s_nop 0
buffer_load_dwordx4 v19,s[40:43],s55 offen offset:0  lds
s_add_u32 m0,s57,40832
s_nop 0
buffer_load_dwordx4 v18,s[40:43],s55 offen offset:128  lds
s_add_u32 m0,s57,41856
s_nop 0
buffer_load_dwordx4 v19,s[40:43],s55 offen offset:128  lds
v_add_u32 v26,49152,v20
v_add_u32 v27,49152,v21
T a[48:49],26,0
T a[50:51],26,512
T a[52:53],27,0
T a[54:55],27,512
T a[56:57],26,4096
T a[58:59],26,4608
T a[60:61],27,4096
T a[62:63],27,4608
s_waitcnt vmcnt(20) lgkmcnt(0)
s_barrier
s_add_u32 s55,s50,1310720
s_add_u32 m0,s57,49152
s_nop 0
buffer_load_dwordx4 v18,s[40:43],s55 offen offset:0  lds
s_add_u32 m0,s57,50176
s_nop 0
buffer_load_dwordx4 v19,s[40:43],s55 offen offset:0  lds
s_add_u32 m0,s57,57216
s_nop 0
buffer_load_dwordx4 v18,s[40:43],s55 offen offset:128  lds
s_add_u32 m0,s57,58240
s_nop 0
buffer_load_dwordx4 v19,s[40:43],s55 offen offset:128  lds
v_add_u32 v26,65536,v20
v_add_u32 v27,65536,v21
T a[64:65],26,0
T a[66:67],26,512
T a[68:69],27,0
T a[70:71],27,512
T a[72:73],26,4096
T a[74:75],26,4608
T a[76:77],27,4096
T a[78:79],27,4608
s_waitcnt vmcnt(20) lgkmcnt(0)
s_barrier
s_add_u32 s55,s50,1441792
s_add_u32 m0,s57,65536
s_nop 0
buffer_load_dwordx4 v18,s[40:43],s55 offen offset:0  lds
s_add_u32 m0,s57,66560
s_nop 0
buffer_load_dwordx4 v19,s[40:43],s55 offen offset:0  lds
s_add_u32 m0,s57,73600
s_nop 0
buffer_load_dwordx4 v18,s[40:43],s55 offen offset:128  lds
s_add_u32 m0,s57,74624
s_nop 0
buffer_load_dwordx4 v19,s[40:43],s55 offen offset:128  lds
v_add_u32 v26,81920,v20
v_add_u32 v27,81920,v21
T a[80:81],26,0
T a[82:83],26,512
T a[84:85],27,0
T a[86:87],27,512
T a[88:89],26,4096
T a[90:91],26,4608
T a[92:93],27,4096
T a[94:95],27,4608
s_waitcnt vmcnt(20) lgkmcnt(0)
s_barrier
s_add_u32 s55,s50,1572864
s_add_u32 m0,s57,81920
s_nop 0
buffer_load_dwordx4 v18,s[40:43],s55 offen offset:0  lds
s_add_u32 m0,s57,82944
s_nop 0
buffer_load_dwordx4 v19,s[40:43],s55 offen offset:0  lds
s_add_u32 m0,s57,89984
s_nop 0
buffer_load_dwordx4 v18,s[40:43],s55 offen offset:128  lds
s_add_u32 m0,s57,91008
s_nop 0
buffer_load_dwordx4 v19,s[40:43],s55 offen offset:128  lds
v_add_u32 v26,98304,v20
v_add_u32 v27,98304,v21
T a[96:97],26,0
T a[98:99],26,512
T a[100:101],27,0
T a[102:103],27,512
T a[104:105],26,4096
T a[106:107],26,4608
T a[108:109],27,4096
T a[110:111],27,4608
s_waitcnt vmcnt(20) lgkmcnt(0)
s_barrier
s_add_u32 s55,s50,1703936
s_add_u32 m0,s57,98304
s_nop 0
buffer_load_dwordx4 v18,s[40:43],s55 offen offset:0  lds
s_add_u32 m0,s57,99328
s_nop 0
buffer_load_dwordx4 v19,s[40:43],s55 offen offset:0  lds
s_add_u32 m0,s57,106368
s_nop 0
buffer_load_dwordx4 v18,s[40:43],s55 offen offset:128  lds
s_add_u32 m0,s57,107392
s_nop 0
buffer_load_dwordx4 v19,s[40:43],s55 offen offset:128  lds
v_add_u32 v26,0,v20
v_add_u32 v27,0,v21
T a[112:113],26,0
T a[114:115],26,512
T a[116:117],27,0
T a[118:119],27,512
T a[120:121],26,4096
T a[122:123],26,4608
T a[124:125],27,4096
T a[126:127],27,4608
s_waitcnt vmcnt(20) lgkmcnt(0)
s_barrier
s_add_u32 s55,s50,1835008
s_add_u32 m0,s57,0
s_nop 0
buffer_load_dwordx4 v18,s[40:43],s55 offen offset:0  lds
s_add_u32 m0,s57,1024
s_nop 0
buffer_load_dwordx4 v19,s[40:43],s55 offen offset:0  lds
s_add_u32 m0,s57,8064
s_nop 0
buffer_load_dwordx4 v18,s[40:43],s55 offen offset:128  lds
s_add_u32 m0,s57,9088
s_nop 0
buffer_load_dwordx4 v19,s[40:43],s55 offen offset:128  lds
v_add_u32 v26,16384,v20
v_add_u32 v27,16384,v21
T a[128:129],26,0
T a[130:131],26,512
T a[132:133],27,0
T a[134:135],27,512
T a[136:137],26,4096
T a[138:139],26,4608
T a[140:141],27,4096
T a[142:143],27,4608
s_waitcnt vmcnt(20) lgkmcnt(0)
s_barrier
s_add_u32 s55,s50,1966080
s_add_u32 m0,s57,16384
s_nop 0
buffer_load_dwordx4 v18,s[40:43],s55 offen offset:0  lds
s_add_u32 m0,s57,17408
s_nop 0
buffer_load_dwordx4 v19,s[40:43],s55 offen offset:0  lds
s_add_u32 m0,s57,24448
s_nop 0
buffer_load_dwordx4 v18,s[40:43],s55 offen offset:128  lds
s_add_u32 m0,s57,25472
s_nop 0
buffer_load_dwordx4 v19,s[40:43],s55 offen offset:128  lds
v_add_u32 v26,32768,v20
v_add_u32 v27,32768,v21
T a[144:145],26,0
T a[146:147],26,512
T a[148:149],27,0
T a[150:151],27,512
T a[152:153],26,4096
T a[154:155],26,4608
T a[156:157],27,4096
T a[158:159],27,4608
s_waitcnt vmcnt(20) lgkmcnt(0)
s_barrier
v_add_u32 v26,49152,v20
v_add_u32 v27,49152,v21
T a[160:161],26,0
T a[162:163],26,512
T a[164:165],27,0
T a[166:167],27,512
T a[168:169],26,4096
T a[170:171],26,4608
T a[172:173],27,4096
T a[174:175],27,4608
s_waitcnt vmcnt(16) lgkmcnt(0)
s_barrier
v_add_u32 v26,65536,v20
v_add_u32 v27,65536,v21
T a[176:177],26,0
T a[178:179],26,512
T a[180:181],27,0
T a[182:183],27,512
T a[184:185],26,4096
T a[186:187],26,4608
T a[188:189],27,4096
T a[190:191],27,4608
s_waitcnt vmcnt(12) lgkmcnt(0)
s_barrier
v_add_u32 v26,81920,v20
v_add_u32 v27,81920,v21
T a[192:193],26,0
T a[194:195],26,512
T a[196:197],27,0
T a[198:199],27,512
T a[200:201],26,4096
T a[202:203],26,4608
T a[204:205],27,4096
T a[206:207],27,4608
s_waitcnt vmcnt(8) lgkmcnt(0)
s_barrier
v_add_u32 v26,98304,v20
v_add_u32 v27,98304,v21
T a[208:209],26,0
T a[210:211],26,512
T a[212:213],27,0
T a[214:215],27,512
T a[216:217],26,4096
T a[218:219],26,4608
T a[220:221],27,4096
T a[222:223],27,4608
s_waitcnt vmcnt(4) lgkmcnt(0)
s_barrier
v_add_u32 v26,0,v20
v_add_u32 v27,0,v21
T a[224:225],26,0
T a[226:227],26,512
T a[228:229],27,0
T a[230:231],27,512
T a[232:233],26,4096
T a[234:235],26,4608
T a[236:237],27,4096
T a[238:239],27,4608
s_waitcnt vmcnt(0) lgkmcnt(0)
s_barrier
v_add_u32 v26,16384,v20
v_add_u32 v27,16384,v21
T a[240:241],26,0
T a[242:243],26,512
T a[244:245],27,0
T a[246:247],27,512
T a[248:249],26,4096
T a[250:251],26,4608
T a[252:253],27,4096
T a[254:255],27,4608
L 0
s_barrier
v_mov_b32 v34,0x3f803f80
v_mov_b32 v35,0x3f803f80
v_mov_b32 v36,0x3f803f80
v_mov_b32 v37,0x3f803f80
s_nop 1
v_mfma_f32_16x16x32_bf16 v[106:109],a[0:3],v[34:37],0
v_mfma_f32_16x16x32_bf16 v[110:113],a[4:7],v[34:37],0
M 106,a[8:11],34
M 110,a[12:15],34
M 106,a[16:19],34
M 110,a[20:23],34
M 106,a[24:27],34
M 110,a[28:31],34
M 106,a[32:35],34
M 110,a[36:39],34
M 106,a[40:43],34
M 110,a[44:47],34
M 106,a[48:51],34
M 110,a[52:55],34
M 106,a[56:59],34
M 110,a[60:63],34
M 106,a[64:67],34
M 110,a[68:71],34
M 106,a[72:75],34
M 110,a[76:79],34
M 106,a[80:83],34
M 110,a[84:87],34
M 106,a[88:91],34
M 110,a[92:95],34
M 106,a[96:99],34
M 110,a[100:103],34
M 106,a[104:107],34
M 110,a[108:111],34
M 106,a[112:115],34
M 110,a[116:119],34
M 106,a[120:123],34
M 110,a[124:127],34
M 106,a[128:131],34
M 110,a[132:135],34
M 106,a[136:139],34
M 110,a[140:143],34
M 106,a[144:147],34
M 110,a[148:151],34
M 106,a[152:155],34
M 110,a[156:159],34
M 106,a[160:163],34
M 110,a[164:167],34
M 106,a[168:171],34
M 110,a[172:175],34
M 106,a[176:179],34
M 110,a[180:183],34
M 106,a[184:187],34
M 110,a[188:191],34
M 106,a[192:195],34
M 110,a[196:199],34
M 106,a[200:203],34
M 110,a[204:207],34
M 106,a[208:211],34
M 110,a[212:215],34
M 106,a[216:219],34
M 110,a[220:223],34
M 106,a[224:227],34
M 110,a[228:231],34
M 106,a[232:235],34
M 110,a[236:239],34
M 106,a[240:243],34
M 110,a[244:247],34
M 106,a[248:251],34
M 110,a[252:255],34
v_mov_b32 v4,s53
s_mov_b32 s82,0
s_mov_b32 s80,0
s_mov_b32 s54,s45
s_mul_i32 s57,s44,4096
s_mul_i32 s55,s45,32768
s_add_u32 s55,s55,s57
s_add_u32 s55,s55,8388608
s_mul_i32 s57,s44,4096
s_mul_i32 s60,s46,32768
s_mul_i32 s61,s46,1024
s_mul_i32 s62,s46,40960
s_mul_i32 s63,s46,1024
s_mul_i32 s64,s46,512
s_lshl_b32 s59,s44,8
s_add_u32 s59,s59,131072
s_mul_i32 s58,s45,1024
s_add_u32 s58,s58,109056000
s_mul_i32 s65,s45,40960
s_add_u32 s65,s65,s47
s_mul_i32 s68,s45,1024
s_add_u32 s68,s68,s48
s_mul_i32 s73,s45,512
s_add_u32 s73,s73,106954752
s_mul_i32 s74,s45,32768
s_add_u32 s74,s74,s49
s_mul_i32 s75,s45,1024
s_add_u32 s75,s75,107479040
s_mov_b32 s66,0x7fff0000
s_mov_b32 s67,0x7fff0000
s_mov_b32 s69,0x7fff0000
s_mov_b32 s76,0x7fff0000
s_mov_b32 s77,0x7fff0000
s_mov_b32 s70,0x7fff0000
s_mov_b32 s71,0x7fff0000
s_mov_b32 s72,0x7fff0000
v_mov_b32 v1,v0
v_add_u32 v2,16384,v0
s_mov_b32 s84,16384
s_mov_b32 s86,0x3a800000
s_mov_b32 s87,0x3727c5ac
s_mov_b32 s88,0x409d30fc
s_mov_b32 s89,0xbeeff558
s_lshr_b32 s85,s44,1
s_mul_i32 s85,s85,127
v_lshlrev_b32 v8,22,v0
v_and_b32 v8,0x80000000,v8
v_or_b32 v8,v8,v5
v_add_u32 v2,32768,v0
s_lshl_b32 s84,s57,1
s_add_u32 s84,s84,32768
s_getpc_b64 s[40:41]
Lg1_pc_%=:
s_lshl_b32 s78,s44,13
v_lshlrev_b32 v114,3,v0
v_add_u32 v114,s78,v114
v_min_u32 v114,Lcode_g2_b-Lg1_pc_%=-4,v114
)"
R"(s_mov_b32 m0,163584
s_nop 0
global_load_lds_dword v114,s[40:41]
s_add_u32 m0,s59,4096
s_nop 0
buffer_load_dword v8,s[36:39],s58 offen offset:0 sc1 lds
buffer_store_dword v4,v3,s[36:39],s71 offen offset:0 sc0 sc1
s_add_u32 m0,s59,5120
s_nop 0
buffer_load_dword v8,s[36:39],s58 offen offset:0 sc1 lds
buffer_store_dword v4,v3,s[36:39],s71 offen offset:0 sc0 sc1
s_mov_b32 s83,0
Lg1_poll_%=:
s_add_u32 s78,s83,2
s_and_b32 s78,s78,3
s_lshl_b32 s78,s78,10
s_add_u32 s78,s78,4096
s_add_u32 m0,s59,s78
s_nop 0
buffer_load_dword v8,s[36:39],s58 offen offset:0 sc1 lds
s_and_b32 s78,s83,3
s_lshl_b32 s78,s78,10
v_add_u32 v114,s78,v6
s_waitcnt vmcnt(4)
s_barrier
ds_read_b32 v7,v114 offset:4096
buffer_store_dword v4,v3,s[36:39],s70 offen offset:0 sc0 sc1
s_mov_b32 s70,s71
s_mov_b32 s71,s72
s_mov_b32 s72,0x7fff0000
s_add_u32 s83,s83,1
s_add_u32 s82,s82,1
L 0
v_cmp_eq_u32 vcc,s53,v7
s_nop 0
s_cmp_eq_u32 vcc_lo,-1
s_cbranch_scc1 Lg1_go_%=
s_cmp_lt_u32 s82,20000
s_cbranch_scc1 Lg1_poll_%=
Lg1_go_%=:
s_add_u32 m0,s57,0
s_add_u32 s56,s55,0
D 0,56,0
D 0,56,1024
D 0,56,2048
D 0,56,3072
s_add_u32 s58,s58,s61
s_add_u32 m0,s59,2048
s_nop 0
buffer_load_dword v8,s[36:39],s58 offen offset:0 sc1 lds
s_add_u32 m0,s57,16384
s_add_u32 s56,s55,16384
D 0,56,0
D 0,56,1024
D 0,56,2048
D 0,56,3072
s_add_u32 s55,s55,s60
s_sub_u32 s57,s84,s57
s_add_u32 s54,s54,s46
s_waitcnt vmcnt(4)
s_barrier
buffer_store_dword v4,v3,s[36:39],s70 offen offset:0 sc0 sc1
s_min_u32 s71,s71,s72
buffer_store_dword v4,v3,s[36:39],s71 offen offset:0 sc0 sc1
s_mov_b32 s70,0x7fff0000
s_mov_b32 s71,0x7fff0000
s_mov_b32 s72,0x7fff0000
s_cmp_eq_u32 s80,1
buffer_store_dword v4,v3,s[36:39],s71 offen offset:0 sc0 sc1
s_cbranch_scc1 Lg1_st1_%=
s_branch Lg1_st0_%=
Lg1_gox_%=:
s_add_u32 m0,s59,2048
s_nop 0
buffer_load_dword v8,s[36:39],s58 offen offset:0 sc1 lds
s_waitcnt vmcnt(0)
s_barrier
buffer_store_dword v4,v3,s[36:39],s70 offen offset:0 sc0 sc1
s_min_u32 s71,s71,s72
buffer_store_dword v4,v3,s[36:39],s71 offen offset:0 sc0 sc1
s_mov_b32 s70,0x7fff0000
s_mov_b32 s71,0x7fff0000
s_mov_b32 s72,0x7fff0000
s_cmp_eq_u32 s80,1
s_cbranch_scc1 Lg1_st1_%=
Lg1_st0_%=:
ds_read_b32 v7,v6 offset:2048
R 34,1,0
R 38,1,1024
R 42,1,2048
R 46,1,3072
R 50,1,4096
L 5
v_cmp_eq_u32 vcc,s53,v7
L 3
s_cmp_eq_u32 vcc_lo,-1
s_cbranch_scc1 Lg1_F0_%=
s_branch Lg1_L0_%=
Lg1_st1_%=:
ds_read_b32 v7,v6 offset:2048
R 34,2,0
R 38,2,1024
R 42,2,2048
R 46,2,3072
R 50,2,4096
L 5
v_cmp_eq_u32 vcc,s53,v7
L 3
s_cmp_eq_u32 vcc_lo,-1
s_cbranch_scc1 Lg1_F1_%=
s_branch Lg1_L1_%=
Lg1_F0_%=:
v_mfma_f32_16x16x32_bf16 v[22:25],a[4:7],v[34:37],0
s_add_u32 s58,s58,s61
s_add_u32 m0,s59,0
R 54,1,5120
M 22,a[12:15],38
buffer_load_dword v8,s[36:39],s58 offen offset:0 sc1 lds
v_mfma_f32_16x16x32_bf16 v[18:21],a[0:3],v[34:37],0
s_add_u32 m0,s57,0
R 58,1,6144
v_fma_f32 v26,-v102,v106,v26
M 18,a[8:11],38
L 3
s_add_u32 s56,s55,0
v_fma_f32 v27,-v102,v107,v27
M 18,a[16:19],42
s_mov_b32 s66,s65
R 62,1,7168
v_fma_f32 v28,-v102,v108,v28
M 18,a[24:27],46
D 0,56,0
M 22,a[20:23],42
s_add_u32 s65,s65,s62
R 66,1,8192
v_fma_f32 v29,-v102,v109,v29
v_mul_f32 v26,v103,v26
M 22,a[28:31],46
L 3
D 0,56,1024
M 22,a[36:39],50
s_add_u32 s54,s54,s46
R 70,1,9216
v_mul_f32 v27,v103,v27
M 22,a[44:47],54
v_mul_f32 v28,v103,v28
v_mul_f32 v29,v103,v29
M 18,a[32:35],50
R 74,1,10240
v_fma_f32 v114,v26,v26,s88
M 18,a[40:43],54
L 3
D 0,56,2048
M 18,a[48:51],58
R 78,1,11264
v_fma_f32 v115,v27,v27,s88
M 18,a[56:59],62
D 0,56,3072
v_fma_f32 v116,v28,v28,s88
M 22,a[52:55],58
s_add_u32 m0,s57,16384
R 82,1,12288
v_fma_f32 v117,v29,v29,s88
M 22,a[60:63],62
L 3
v_mul_f32 v114,v26,v114
v_mul_f32 v115,v27,v115
M 22,a[68:71],66
s_add_u32 s56,s55,16384
R 86,1,13312
v_mul_f32 v116,v28,v116
M 22,a[76:79],70
buffer_load_dwordx2 v[98:99],v11,s[36:39],s73 offen offset:0 sc1
M 18,a[64:67],66
s_add_u32 s73,s73,s64
R 90,1,14336
v_mul_f32 v117,v29,v117
M 18,a[72:75],70
L 3
v_exp_f32 v114,v114
v_fma_f32 v30,-v102,v110,v30
M 18,a[80:83],74
s_waitcnt vmcnt(9)
R 94,1,15360
v_fma_f32 v114,v114,s89,s89
M 18,a[88:91],78
s_barrier
v_exp_f32 v115,v115
M 22,a[84:87],74
s_add_u32 s55,s55,s60
R 34,1,16384
v_fma_f32 v115,v115,s89,s89
M 22,a[92:95],78
L 3
v_exp_f32 v116,v116
M 22,a[100:103],82
s_sub_u32 s57,s84,s57
R 38,1,17408
v_fma_f32 v116,v116,s89,s89
M 22,a[108:111],86
D 0,56,0
M 18,a[96:99],82
R 42,1,18432
v_exp_f32 v117,v117
M 18,a[104:107],86
L 3
v_rcp_f32 v114,v114
M 18,a[112:115],90
R 46,1,19456
v_fma_f32 v117,v117,s89,s89
M 18,a[120:123],94
v_rcp_f32 v115,v115
M 22,a[116:119],90
R 50,1,20480
v_mul_f32 v26,v26,v114
M 22,a[124:127],94
L 3
v_rcp_f32 v116,v116
M 22,a[132:135],34
R 54,1,21504
v_mul_f32 v27,v27,v115
M 22,a[140:143],38
D 0,56,1024
v_mul_f32 v28,v28,v116
M 18,a[128:131],34
R 58,1,22528
v_cvt_pk_bf16_f32 v122,v26,v27
M 18,a[136:139],38
L 3
v_rcp_f32 v117,v117
M 18,a[144:147],42
R 62,1,23552
v_mul_f32 v29,v29,v117
M 18,a[152:155],46
v_cvt_pk_bf16_f32 v123,v28,v29
v_fma_f32 v31,-v102,v111,v31
M 22,a[148:151],42
R 66,1,24576
v_fma_f32 v32,-v102,v112,v32
M 22,a[156:159],46
L 3
D 0,56,2048
v_fma_f32 v33,-v102,v113,v33
M 22,a[164:167],50
R 70,1,25600
v_mul_f32 v30,v103,v30
M 22,a[172:175],54
v_mul_f32 v31,v103,v31
v_mul_f32 v32,v103,v32
M 18,a[160:163],50
R 74,1,26624
v_mul_f32 v33,v103,v33
M 18,a[168:171],54
L 3
v_fma_f32 v118,v30,v30,s88
v_fma_f32 v119,v31,v31,s88
M 18,a[176:179],58
R 78,1,27648
v_fma_f32 v120,v32,v32,s88
M 18,a[184:187],62
v_fma_f32 v121,v33,v33,s88
v_mul_f32 v118,v30,v118
M 22,a[180:183],58
R 82,1,28672
v_exp_f32 v118,v118
M 22,a[188:191],62
L 3
D 0,56,3072
M 22,a[196:199],66
R 86,1,29696
v_mul_f32 v119,v31,v119
M 22,a[204:207],70
v_exp_f32 v119,v119
M 18,a[192:195],66
R 90,1,30720
v_mul_f32 v120,v32,v120
M 18,a[200:203],70
L 3
v_mul_f32 v121,v33,v121
v_fma_f32 v118,v118,s89,s89
M 18,a[208:211],74
s_waitcnt vmcnt(5)
R 94,1,31744
v_fma_f32 v119,v119,s89,s89
M 18,a[216:219],78
s_barrier
ds_read_b32 v7,v6 offset:0
v_exp_f32 v120,v120
M 22,a[212:215],74
R 34,2,0
v_fma_f32 v120,v120,s89,s89
M 22,a[220:223],78
L 4
buffer_store_dword v4,v3,s[36:39],s70 offen offset:0 sc0 sc1
v_exp_f32 v121,v121
M 22,a[228:231],82
s_mov_b32 s70,s69
R 38,2,1024
v_fma_f32 v121,v121,s89,s89
v_rcp_f32 v118,v118
v_rcp_f32 v119,v119
v_rcp_f32 v120,v120
v_rcp_f32 v121,v121
M 22,a[236:239],86
s_mov_b32 s69,s68
v_mul_f32 v30,v30,v118
v_mul_f32 v31,v31,v119
v_mul_f32 v32,v32,v120
v_mul_f32 v33,v33,v121
v_cvt_pk_bf16_f32 v124,v30,v31
v_cvt_pk_bf16_f32 v125,v32,v33
M 18,a[224:227],82
s_add_u32 s68,s68,s63
R 42,2,2048
buffer_store_dwordx4 v[122:125],v15,s[36:39],s67 offen offset:0 sc0 sc1
M 18,a[232:235],86
L 4
L 3
M 18,a[240:243],90
R 46,2,3072
v_cmp_eq_u32 vcc,s53,v7
M 18,a[248:251],94
buffer_store_dwordx4 v[122:125],v16,s[36:39],s67 offen offset:0 sc0 sc1
M 22,a[244:247],90
s_waitcnt vmcnt(7) lgkmcnt(2)
s_cmp_eq_u32 vcc_lo,-1
R 50,2,4096
M 22,a[252:255],94
s_cbranch_scc0 Lg1_K1_%=
Lg1_F1_%=:
v_mfma_f32_16x16x32_bf16 v[30:33],a[4:7],v[34:37],0
s_add_u32 s58,s58,s61
s_add_u32 m0,s59,1024
R 54,2,5120
M 30,a[12:15],38
buffer_load_dword v8,s[36:39],s58 offen offset:0 sc1 lds
v_mfma_f32_16x16x32_bf16 v[26:29],a[0:3],v[34:37],0
s_add_u32 m0,s57,0
R 58,2,6144
v_fma_f32 v18,-v98,v106,v18
M 26,a[8:11],38
L 3
s_add_u32 s56,s55,0
v_fma_f32 v19,-v98,v107,v19
M 26,a[16:19],42
s_mov_b32 s67,s65
R 62,2,7168
v_fma_f32 v20,-v98,v108,v20
M 26,a[24:27],46
D 0,56,0
M 30,a[20:23],42
s_add_u32 s65,s65,s62
R 66,2,8192
v_fma_f32 v21,-v98,v109,v21
v_mul_f32 v18,v99,v18
M 30,a[28:31],46
L 3
D 0,56,1024
M 30,a[36:39],50
s_add_u32 s54,s54,s46
R 70,2,9216
v_mul_f32 v19,v99,v19
M 30,a[44:47],54
v_mul_f32 v20,v99,v20
v_mul_f32 v21,v99,v21
M 26,a[32:35],50
R 74,2,10240
v_fma_f32 v114,v18,v18,s88
M 26,a[40:43],54
L 3
D 0,56,2048
M 26,a[48:51],58
R 78,2,11264
v_fma_f32 v115,v19,v19,s88
M 26,a[56:59],62
D 0,56,3072
v_fma_f32 v116,v20,v20,s88
M 30,a[52:55],58
s_add_u32 m0,s57,16384
R 82,2,12288
v_fma_f32 v117,v21,v21,s88
M 30,a[60:63],62
L 3
v_mul_f32 v114,v18,v114
v_mul_f32 v115,v19,v115
M 30,a[68:71],66
s_add_u32 s56,s55,16384
R 86,2,13312
v_mul_f32 v116,v20,v116
M 30,a[76:79],70
buffer_load_dwordx2 v[102:103],v11,s[36:39],s73 offen offset:0 sc1
M 26,a[64:67],66
s_add_u32 s73,s73,s64
R 90,2,14336
v_mul_f32 v117,v21,v117
M 26,a[72:75],70
L 3
v_exp_f32 v114,v114
v_fma_f32 v22,-v98,v110,v22
M 26,a[80:83],74
s_waitcnt vmcnt(9)
R 94,2,15360
v_fma_f32 v114,v114,s89,s89
M 26,a[88:91],78
s_barrier
v_exp_f32 v115,v115
M 30,a[84:87],74
s_add_u32 s55,s55,s60
R 34,2,16384
v_fma_f32 v115,v115,s89,s89
M 30,a[92:95],78
L 3
v_exp_f32 v116,v116
M 30,a[100:103],82
s_sub_u32 s57,s84,s57
R 38,2,17408
v_fma_f32 v116,v116,s89,s89
M 30,a[108:111],86
D 0,56,0
M 26,a[96:99],82
R 42,2,18432
v_exp_f32 v117,v117
M 26,a[104:107],86
L 3
v_rcp_f32 v114,v114
M 26,a[112:115],90
R 46,2,19456
v_fma_f32 v117,v117,s89,s89
M 26,a[120:123],94
v_rcp_f32 v115,v115
M 30,a[116:119],90
R 50,2,20480
v_mul_f32 v18,v18,v114
M 30,a[124:127],94
L 3
v_rcp_f32 v116,v116
M 30,a[132:135],34
R 54,2,21504
v_mul_f32 v19,v19,v115
M 30,a[140:143],38
D 0,56,1024
v_mul_f32 v20,v20,v116
M 26,a[128:131],34
R 58,2,22528
v_cvt_pk_bf16_f32 v122,v18,v19
M 26,a[136:139],38
L 3
v_rcp_f32 v117,v117
M 26,a[144:147],42
R 62,2,23552
v_mul_f32 v21,v21,v117
M 26,a[152:155],46
v_cvt_pk_bf16_f32 v123,v20,v21
v_fma_f32 v23,-v98,v111,v23
M 30,a[148:151],42
R 66,2,24576
v_fma_f32 v24,-v98,v112,v24
M 30,a[156:159],46
L 3
D 0,56,2048
v_fma_f32 v25,-v98,v113,v25
M 30,a[164:167],50
R 70,2,25600
v_mul_f32 v22,v99,v22
M 30,a[172:175],54
v_mul_f32 v23,v99,v23
v_mul_f32 v24,v99,v24
M 26,a[160:163],50
R 74,2,26624
v_mul_f32 v25,v99,v25
M 26,a[168:171],54
L 3
v_fma_f32 v118,v22,v22,s88
v_fma_f32 v119,v23,v23,s88
M 26,a[176:179],58
R 78,2,27648
v_fma_f32 v120,v24,v24,s88
M 26,a[184:187],62
v_fma_f32 v121,v25,v25,s88
v_mul_f32 v118,v22,v118
M 30,a[180:183],58
R 82,2,28672
v_exp_f32 v118,v118
M 30,a[188:191],62
L 3
D 0,56,3072
M 30,a[196:199],66
R 86,2,29696
v_mul_f32 v119,v23,v119
M 30,a[204:207],70
v_exp_f32 v119,v119
M 26,a[192:195],66
R 90,2,30720
v_mul_f32 v120,v24,v120
M 26,a[200:203],70
L 3
v_mul_f32 v121,v25,v121
v_fma_f32 v118,v118,s89,s89
M 26,a[208:211],74
s_waitcnt vmcnt(5)
)"
R"(R 94,2,31744
v_fma_f32 v119,v119,s89,s89
M 26,a[216:219],78
s_barrier
ds_read_b32 v7,v6 offset:1024
v_exp_f32 v120,v120
M 30,a[212:215],74
R 34,1,0
v_fma_f32 v120,v120,s89,s89
M 30,a[220:223],78
L 4
buffer_store_dword v4,v3,s[36:39],s70 offen offset:0 sc0 sc1
v_exp_f32 v121,v121
M 30,a[228:231],82
s_mov_b32 s70,s69
R 38,1,1024
v_fma_f32 v121,v121,s89,s89
v_rcp_f32 v118,v118
v_rcp_f32 v119,v119
v_rcp_f32 v120,v120
v_rcp_f32 v121,v121
M 30,a[236:239],86
s_mov_b32 s69,s68
v_mul_f32 v22,v22,v118
v_mul_f32 v23,v23,v119
v_mul_f32 v24,v24,v120
v_mul_f32 v25,v25,v121
v_cvt_pk_bf16_f32 v124,v22,v23
v_cvt_pk_bf16_f32 v125,v24,v25
M 26,a[224:227],82
s_add_u32 s68,s68,s63
R 42,1,2048
buffer_store_dwordx4 v[122:125],v15,s[36:39],s66 offen offset:0 sc0 sc1
M 26,a[232:235],86
L 4
L 3
M 26,a[240:243],90
R 46,1,3072
v_cmp_eq_u32 vcc,s53,v7
M 26,a[248:251],94
buffer_store_dwordx4 v[122:125],v16,s[36:39],s66 offen offset:0 sc0 sc1
M 30,a[244:247],90
s_waitcnt vmcnt(7) lgkmcnt(2)
s_cmp_eq_u32 vcc_lo,-1
R 50,1,4096
M 30,a[252:255],94
s_cbranch_scc1 Lg1_F0_%=
Lg1_K0_%=:
Lg1_L0_%=:
v_mfma_f32_16x16x32_bf16 v[22:25],a[4:7],v[34:37],0
s_add_u32 m0,s59,3072
s_mov_b32 s66,s65
R 54,1,5120
M 22,a[12:15],38
buffer_load_dword v8,s[36:39],s58 offen offset:0 sc1 lds
v_mfma_f32_16x16x32_bf16 v[18:21],a[0:3],v[34:37],0
s_add_u32 s65,s65,s62
R 58,1,6144
v_fma_f32 v26,-v102,v106,v26
M 18,a[8:11],38
L 3
v_fma_f32 v27,-v102,v107,v27
v_fma_f32 v28,-v102,v108,v28
M 18,a[16:19],42
R 62,1,7168
v_fma_f32 v29,-v102,v109,v29
M 18,a[24:27],46
v_mul_f32 v26,v103,v26
v_mul_f32 v27,v103,v27
M 22,a[20:23],42
R 66,1,8192
v_mul_f32 v28,v103,v28
M 22,a[28:31],46
L 3
v_mul_f32 v29,v103,v29
v_fma_f32 v114,v26,v26,s88
M 22,a[36:39],50
R 70,1,9216
v_fma_f32 v115,v27,v27,s88
M 22,a[44:47],54
v_fma_f32 v116,v28,v28,s88
v_fma_f32 v117,v29,v29,s88
M 18,a[32:35],50
R 74,1,10240
v_mul_f32 v114,v26,v114
M 18,a[40:43],54
L 3
v_exp_f32 v114,v114
M 18,a[48:51],58
R 78,1,11264
v_mul_f32 v115,v27,v115
M 18,a[56:59],62
v_exp_f32 v115,v115
M 22,a[52:55],58
R 82,1,12288
v_mul_f32 v116,v28,v116
M 22,a[60:63],62
L 3
v_exp_f32 v116,v116
M 22,a[68:71],66
R 86,1,13312
v_mul_f32 v117,v29,v117
M 22,a[76:79],70
buffer_load_dwordx2 v[98:99],v11,s[36:39],s73 offen offset:0 sc1
M 18,a[64:67],66
s_add_u32 s73,s73,s64
R 90,1,14336
v_fma_f32 v114,v114,s89,s89
M 18,a[72:75],70
L 3
v_exp_f32 v117,v117
M 18,a[80:83],74
s_waitcnt vmcnt(1)
R 94,1,15360
v_fma_f32 v115,v115,s89,s89
M 18,a[88:91],78
s_barrier
ds_read_b32 v7,v6 offset:3072
v_fma_f32 v116,v116,s89,s89
M 22,a[84:87],74
R 34,1,16384
v_fma_f32 v117,v117,s89,s89
M 22,a[92:95],78
L 4
buffer_store_dword v4,v3,s[36:39],s70 offen offset:0 sc0 sc1
M 22,a[100:103],82
s_mov_b32 s70,0x7fff0000
R 38,1,17408
v_fma_f32 v30,-v102,v110,v30
M 22,a[108:111],86
v_rcp_f32 v114,v114
M 18,a[96:99],82
R 42,1,18432
v_mul_f32 v26,v26,v114
M 18,a[104:107],86
L 4
L 3
v_cmp_eq_u32 vcc,s53,v7
M 18,a[112:115],90
R 46,1,19456
v_fma_f32 v31,-v102,v111,v31
M 18,a[120:123],94
v_rcp_f32 v115,v115
M 22,a[116:119],90
s_cmp_eq_u32 vcc_lo,-1
R 50,1,20480
v_mul_f32 v27,v27,v115
s_cbranch_scc0 Lg1_X0_%=
M 22,a[124:127],94
L 3
s_add_u32 m0,s57,0
s_add_u32 s56,s55,0
M 22,a[132:135],34
s_add_u32 s58,s58,s61
R 54,1,21504
v_cvt_pk_bf16_f32 v122,v26,v27
M 22,a[140:143],38
D 0,56,0
M 18,a[128:131],34
s_add_u32 s54,s54,s46
R 58,1,22528
v_fma_f32 v32,-v102,v112,v32
M 18,a[136:139],38
L 3
v_rcp_f32 v116,v116
M 18,a[144:147],42
R 62,1,23552
v_mul_f32 v28,v28,v116
M 18,a[152:155],46
D 0,56,1024
M 22,a[148:151],42
R 66,1,24576
v_fma_f32 v33,-v102,v113,v33
M 22,a[156:159],46
L 3
v_rcp_f32 v117,v117
M 22,a[164:167],50
R 70,1,25600
v_mul_f32 v29,v29,v117
M 22,a[172:175],54
D 0,56,2048
M 18,a[160:163],50
R 74,1,26624
v_cvt_pk_bf16_f32 v123,v28,v29
M 18,a[168:171],54
L 3
v_mul_f32 v30,v103,v30
v_mul_f32 v31,v103,v31
M 18,a[176:179],58
R 78,1,27648
v_mul_f32 v32,v103,v32
M 18,a[184:187],62
D 0,56,3072
M 22,a[180:183],58
s_add_u32 m0,s59,0
R 82,1,28672
v_mul_f32 v33,v103,v33
M 22,a[188:191],62
L 3
buffer_load_dword v8,s[36:39],s58 offen offset:0 sc1 lds
M 22,a[196:199],66
s_add_u32 m0,s57,16384
R 86,1,29696
v_fma_f32 v118,v30,v30,s88
M 22,a[204:207],70
s_add_u32 s56,s55,16384
v_fma_f32 v119,v31,v31,s88
v_fma_f32 v120,v32,v32,s88
M 18,a[192:195],66
s_add_u32 s55,s55,s60
R 90,1,30720
v_fma_f32 v121,v33,v33,s88
M 18,a[200:203],70
L 3
D 0,56,0
M 18,a[208:211],74
s_waitcnt vmcnt(1)
R 94,1,31744
v_mul_f32 v118,v30,v118
M 18,a[216:219],78
s_barrier
ds_read_b32 v7,v6 offset:0
v_mul_f32 v119,v31,v119
M 22,a[212:215],74
s_sub_u32 s57,s84,s57
R 34,2,0
v_mul_f32 v120,v32,v120
M 22,a[220:223],78
L 4
buffer_store_dword v4,v3,s[36:39],s70 offen offset:0 sc0 sc1
v_mul_f32 v121,v33,v121
v_exp_f32 v118,v118
v_exp_f32 v119,v119
v_exp_f32 v120,v120
v_exp_f32 v121,v121
M 22,a[228:231],82
s_mov_b32 s70,s69
R 38,2,1024
v_fma_f32 v118,v118,s89,s89
v_fma_f32 v119,v119,s89,s89
v_fma_f32 v120,v120,s89,s89
v_fma_f32 v121,v121,s89,s89
v_rcp_f32 v118,v118
v_rcp_f32 v119,v119
v_rcp_f32 v120,v120
v_rcp_f32 v121,v121
M 22,a[236:239],86
s_mov_b32 s69,s68
v_mul_f32 v30,v30,v118
v_mul_f32 v31,v31,v119
v_mul_f32 v32,v32,v120
v_mul_f32 v33,v33,v121
v_cvt_pk_bf16_f32 v124,v30,v31
v_cvt_pk_bf16_f32 v125,v32,v33
M 18,a[224:227],82
s_add_u32 s68,s68,s63
R 42,2,2048
buffer_store_dwordx4 v[122:125],v15,s[36:39],s67 offen offset:0 sc0 sc1
M 18,a[232:235],86
L 4
L 3
M 18,a[240:243],90
R 46,2,3072
v_cmp_eq_u32 vcc,s53,v7
M 18,a[248:251],94
D 0,56,1024
M 22,a[244:247],90
L 2
s_cmp_eq_u32 vcc_lo,-1
R 50,2,4096
buffer_store_dwordx4 v[122:125],v16,s[36:39],s67 offen offset:0 sc0 sc1
M 22,a[252:255],94
D 0,56,2048
D 0,56,3072
buffer_store_dword v4,v3,s[36:39],s71 offen offset:0 sc0 sc1
buffer_store_dword v4,v3,s[36:39],s71 offen offset:0 sc0 sc1
buffer_store_dword v4,v3,s[36:39],s71 offen offset:0 sc0 sc1
s_cbranch_scc1 Lg1_F1_%=
s_branch Lg1_K1_%=
Lg1_X0_%=:
M 22,a[124:127],94
L 3
s_add_u32 m0,s59,0
v_cvt_pk_bf16_f32 v122,v26,v27
M 22,a[132:135],34
R 54,1,21504
v_fma_f32 v32,-v102,v112,v32
M 22,a[140:143],38
buffer_load_dword v8,s[36:39],s58 offen offset:0 sc1 lds
M 18,a[128:131],34
R 58,1,22528
v_fma_f32 v33,-v102,v113,v33
M 18,a[136:139],38
L 3
v_rcp_f32 v116,v116
M 18,a[144:147],42
R 62,1,23552
v_mul_f32 v28,v28,v116
M 18,a[152:155],46
v_rcp_f32 v117,v117
M 22,a[148:151],42
R 66,1,24576
v_mul_f32 v29,v29,v117
M 22,a[156:159],46
L 3
v_cvt_pk_bf16_f32 v123,v28,v29
v_mul_f32 v30,v103,v30
M 22,a[164:167],50
R 70,1,25600
v_mul_f32 v31,v103,v31
M 22,a[172:175],54
v_mul_f32 v32,v103,v32
v_mul_f32 v33,v103,v33
M 18,a[160:163],50
R 74,1,26624
v_fma_f32 v118,v30,v30,s88
M 18,a[168:171],54
L 3
v_fma_f32 v119,v31,v31,s88
v_fma_f32 v120,v32,v32,s88
M 18,a[176:179],58
R 78,1,27648
v_fma_f32 v121,v33,v33,s88
M 18,a[184:187],62
v_mul_f32 v118,v30,v118
v_mul_f32 v119,v31,v119
M 22,a[180:183],58
R 82,1,28672
v_mul_f32 v120,v32,v120
M 22,a[188:191],62
L 3
v_exp_f32 v118,v118
M 22,a[196:199],66
R 86,1,29696
v_mul_f32 v121,v33,v121
M 22,a[204:207],70
v_exp_f32 v119,v119
M 18,a[192:195],66
R 90,1,30720
v_fma_f32 v118,v118,s89,s89
M 18,a[200:203],70
L 3
v_exp_f32 v120,v120
M 18,a[208:211],74
s_waitcnt vmcnt(0)
R 94,1,31744
v_fma_f32 v119,v119,s89,s89
M 18,a[216:219],78
s_barrier
buffer_store_dword v4,v3,s[36:39],s70 offen offset:0 sc0 sc1
M 22,a[212:215],74
s_mov_b32 s70,s69
ds_read_b32 v7,v6 offset:0
v_fma_f32 v120,v120,s89,s89
M 22,a[220:223],78
L 3
v_exp_f32 v121,v121
M 22,a[228:231],82
s_mov_b32 s69,s68
v_fma_f32 v121,v121,s89,s89
v_rcp_f32 v118,v118
v_rcp_f32 v119,v119
v_rcp_f32 v120,v120
v_rcp_f32 v121,v121
M 22,a[236:239],86
s_add_u32 s68,s68,s63
v_mul_f32 v30,v30,v118
v_mul_f32 v31,v31,v119
v_mul_f32 v32,v32,v120
v_mul_f32 v33,v33,v121
v_cvt_pk_bf16_f32 v124,v30,v31
v_cvt_pk_bf16_f32 v125,v32,v33
M 18,a[224:227],82
buffer_store_dwordx4 v[122:125],v15,s[36:39],s67 offen offset:0 sc0 sc1
M 18,a[232:235],86
L 1
M 18,a[240:243],90
L 0
v_cmp_eq_u32 vcc,s53,v7
M 18,a[248:251],94
buffer_store_dwordx4 v[122:125],v16,s[36:39],s67 offen offset:0 sc0 sc1
M 22,a[244:247],90
s_nop 0
M 22,a[252:255],94
s_mov_b32 s80,1
s_cmp_eq_u32 vcc_lo,-1
s_cbranch_scc0 Lg1_xn0_%=
s_add_u32 m0,s57,0
s_add_u32 s56,s55,0
D 0,56,0
v_fma_f32 v18,-v98,v106,v18
v_fma_f32 v19,-v98,v107,v19
v_fma_f32 v20,-v98,v108,v20
v_fma_f32 v21,-v98,v109,v21
v_mul_f32 v18,v99,v18
v_mul_f32 v19,v99,v19
v_mul_f32 v20,v99,v20
D 0,56,1024
v_mul_f32 v21,v99,v21
v_fma_f32 v114,v18,v18,s88
v_fma_f32 v115,v19,v19,s88
v_fma_f32 v116,v20,v20,s88
v_fma_f32 v117,v21,v21,s88
v_mul_f32 v114,v18,v114
v_mul_f32 v115,v19,v115
D 0,56,2048
v_mul_f32 v116,v20,v116
v_mul_f32 v117,v21,v117
v_exp_f32 v114,v114
v_exp_f32 v115,v115
v_exp_f32 v116,v116
v_exp_f32 v117,v117
v_fma_f32 v114,v114,s89,s89
D 0,56,3072
v_fma_f32 v115,v115,s89,s89
v_fma_f32 v116,v116,s89,s89
v_fma_f32 v117,v117,s89,s89
v_rcp_f32 v114,v114
v_rcp_f32 v115,v115
v_rcp_f32 v116,v116
v_rcp_f32 v117,v117
s_add_u32 s58,s58,s61
s_add_u32 m0,s57,16384
s_add_u32 s56,s55,16384
D 0,56,0
v_mul_f32 v18,v18,v114
v_mul_f32 v19,v19,v115
v_mul_f32 v20,v20,v116
v_mul_f32 v21,v21,v117
v_cvt_pk_bf16_f32 v122,v18,v19
v_cvt_pk_bf16_f32 v123,v20,v21
v_fma_f32 v22,-v98,v110,v22
D 0,56,1024
v_fma_f32 v23,-v98,v111,v23
v_fma_f32 v24,-v98,v112,v24
v_fma_f32 v25,-v98,v113,v25
v_mul_f32 v22,v99,v22
v_mul_f32 v23,v99,v23
v_mul_f32 v24,v99,v24
v_mul_f32 v25,v99,v25
D 0,56,2048
v_fma_f32 v118,v22,v22,s88
v_fma_f32 v119,v23,v23,s88
v_fma_f32 v120,v24,v24,s88
v_fma_f32 v121,v25,v25,s88
v_mul_f32 v118,v22,v118
v_mul_f32 v119,v23,v119
v_mul_f32 v120,v24,v120
D 0,56,3072
v_mul_f32 v121,v25,v121
v_exp_f32 v118,v118
v_exp_f32 v119,v119
v_exp_f32 v120,v120
v_exp_f32 v121,v121
v_fma_f32 v118,v118,s89,s89
v_fma_f32 v119,v119,s89,s89
s_add_u32 s55,s55,s60
s_sub_u32 s57,s84,s57
s_add_u32 s54,s54,s46
v_fma_f32 v120,v120,s89,s89
v_fma_f32 v121,v121,s89,s89
v_rcp_f32 v118,v118
v_rcp_f32 v119,v119
v_rcp_f32 v120,v120
v_rcp_f32 v121,v121
v_mul_f32 v22,v22,v118
v_mul_f32 v23,v23,v119
v_mul_f32 v24,v24,v120
v_mul_f32 v25,v25,v121
v_cvt_pk_bf16_f32 v124,v22,v23
v_cvt_pk_bf16_f32 v125,v24,v25
buffer_store_dwordx4 v[122:125],v15,s[36:39],s66 offen offset:0 sc0 sc1
buffer_store_dwordx4 v[122:125],v16,s[36:39],s66 offen offset:0 sc0 sc1
s_mov_b32 s72,s69
s_mov_b32 s69,0x7fff0000
s_mov_b32 s66,0x7fff0000
s_mov_b32 s76,0x7fff0000
s_branch Lg1_gox_%=
Lg1_xn0_%=:
s_cmp_ge_u32 s54,256
s_cbranch_scc1 Lg1_fin0_%=
s_add_u32 m0,s59,4096
s_nop 0
buffer_load_dword v8,s[36:39],s58 offen offset:0 sc1 lds
buffer_store_dword v4,v3,s[36:39],s71 offen offset:0 sc0 sc1
s_add_u32 m0,s59,5120
s_nop 0
buffer_load_dword v8,s[36:39],s58 offen offset:0 sc1 lds
buffer_store_dword v4,v3,s[36:39],s71 offen offset:0 sc0 sc1
s_mov_b32 s83,0
v_fma_f32 v18,-v98,v106,v18
v_fma_f32 v19,-v98,v107,v19
v_fma_f32 v20,-v98,v108,v20
v_fma_f32 v21,-v98,v109,v21
v_mul_f32 v18,v99,v18
v_mul_f32 v19,v99,v19
v_mul_f32 v20,v99,v20
v_mul_f32 v21,v99,v21
v_fma_f32 v114,v18,v18,s88
v_fma_f32 v115,v19,v19,s88
)"
R"(v_fma_f32 v116,v20,v20,s88
v_fma_f32 v117,v21,v21,s88
v_mul_f32 v114,v18,v114
v_mul_f32 v115,v19,v115
v_mul_f32 v116,v20,v116
v_mul_f32 v117,v21,v117
v_exp_f32 v114,v114
v_exp_f32 v115,v115
v_exp_f32 v116,v116
v_exp_f32 v117,v117
v_fma_f32 v114,v114,s89,s89
v_fma_f32 v115,v115,s89,s89
v_fma_f32 v116,v116,s89,s89
v_fma_f32 v117,v117,s89,s89
v_rcp_f32 v114,v114
v_rcp_f32 v115,v115
v_rcp_f32 v116,v116
v_rcp_f32 v117,v117
v_mul_f32 v18,v18,v114
v_mul_f32 v19,v19,v115
v_mul_f32 v20,v20,v116
v_mul_f32 v21,v21,v117
v_cvt_pk_bf16_f32 v122,v18,v19
v_cvt_pk_bf16_f32 v123,v20,v21
v_fma_f32 v22,-v98,v110,v22
v_fma_f32 v23,-v98,v111,v23
v_fma_f32 v24,-v98,v112,v24
v_fma_f32 v25,-v98,v113,v25
v_mul_f32 v22,v99,v22
v_mul_f32 v23,v99,v23
v_mul_f32 v24,v99,v24
v_mul_f32 v25,v99,v25
v_fma_f32 v118,v22,v22,s88
v_fma_f32 v119,v23,v23,s88
v_fma_f32 v120,v24,v24,s88
v_fma_f32 v121,v25,v25,s88
v_mul_f32 v118,v22,v118
v_mul_f32 v119,v23,v119
v_mul_f32 v120,v24,v120
v_mul_f32 v121,v25,v121
v_exp_f32 v118,v118
v_exp_f32 v119,v119
v_exp_f32 v120,v120
v_exp_f32 v121,v121
v_fma_f32 v118,v118,s89,s89
v_fma_f32 v119,v119,s89,s89
v_fma_f32 v120,v120,s89,s89
v_fma_f32 v121,v121,s89,s89
v_rcp_f32 v118,v118
v_rcp_f32 v119,v119
v_rcp_f32 v120,v120
v_rcp_f32 v121,v121
v_mul_f32 v22,v22,v118
v_mul_f32 v23,v23,v119
v_mul_f32 v24,v24,v120
v_mul_f32 v25,v25,v121
v_cvt_pk_bf16_f32 v124,v22,v23
v_cvt_pk_bf16_f32 v125,v24,v25
buffer_store_dwordx4 v[122:125],v15,s[36:39],s66 offen offset:0 sc0 sc1
buffer_store_dwordx4 v[122:125],v16,s[36:39],s66 offen offset:0 sc0 sc1
s_mov_b32 s72,s69
s_mov_b32 s69,0x7fff0000
s_mov_b32 s66,0x7fff0000
s_mov_b32 s76,0x7fff0000
s_branch Lg1_poll_%=
Lg1_fin0_%=:
v_fma_f32 v18,-v98,v106,v18
v_fma_f32 v19,-v98,v107,v19
v_fma_f32 v20,-v98,v108,v20
v_fma_f32 v21,-v98,v109,v21
v_mul_f32 v18,v99,v18
v_mul_f32 v19,v99,v19
v_mul_f32 v20,v99,v20
v_mul_f32 v21,v99,v21
v_fma_f32 v114,v18,v18,s88
v_fma_f32 v115,v19,v19,s88
v_fma_f32 v116,v20,v20,s88
v_fma_f32 v117,v21,v21,s88
v_mul_f32 v114,v18,v114
v_mul_f32 v115,v19,v115
v_mul_f32 v116,v20,v116
v_mul_f32 v117,v21,v117
v_exp_f32 v114,v114
v_exp_f32 v115,v115
v_exp_f32 v116,v116
v_exp_f32 v117,v117
v_fma_f32 v114,v114,s89,s89
v_fma_f32 v115,v115,s89,s89
v_fma_f32 v116,v116,s89,s89
v_fma_f32 v117,v117,s89,s89
v_rcp_f32 v114,v114
v_rcp_f32 v115,v115
v_rcp_f32 v116,v116
v_rcp_f32 v117,v117
v_mul_f32 v18,v18,v114
v_mul_f32 v19,v19,v115
v_mul_f32 v20,v20,v116
v_mul_f32 v21,v21,v117
v_cvt_pk_bf16_f32 v122,v18,v19
v_cvt_pk_bf16_f32 v123,v20,v21
v_fma_f32 v22,-v98,v110,v22
v_fma_f32 v23,-v98,v111,v23
v_fma_f32 v24,-v98,v112,v24
v_fma_f32 v25,-v98,v113,v25
v_mul_f32 v22,v99,v22
v_mul_f32 v23,v99,v23
v_mul_f32 v24,v99,v24
v_mul_f32 v25,v99,v25
v_fma_f32 v118,v22,v22,s88
v_fma_f32 v119,v23,v23,s88
v_fma_f32 v120,v24,v24,s88
v_fma_f32 v121,v25,v25,s88
v_mul_f32 v118,v22,v118
v_mul_f32 v119,v23,v119
v_mul_f32 v120,v24,v120
v_mul_f32 v121,v25,v121
v_exp_f32 v118,v118
v_exp_f32 v119,v119
v_exp_f32 v120,v120
v_exp_f32 v121,v121
v_fma_f32 v118,v118,s89,s89
v_fma_f32 v119,v119,s89,s89
v_fma_f32 v120,v120,s89,s89
v_fma_f32 v121,v121,s89,s89
v_rcp_f32 v118,v118
v_rcp_f32 v119,v119
v_rcp_f32 v120,v120
v_rcp_f32 v121,v121
v_mul_f32 v22,v22,v118
v_mul_f32 v23,v23,v119
v_mul_f32 v24,v24,v120
v_mul_f32 v25,v25,v121
v_cvt_pk_bf16_f32 v124,v22,v23
v_cvt_pk_bf16_f32 v125,v24,v25
buffer_store_dwordx4 v[122:125],v15,s[36:39],s66 offen offset:0 sc0 sc1
buffer_store_dwordx4 v[122:125],v16,s[36:39],s66 offen offset:0 sc0 sc1
s_mov_b32 s72,s69
s_mov_b32 s69,0x7fff0000
s_mov_b32 s66,0x7fff0000
s_mov_b32 s76,0x7fff0000
s_branch Lg1_done_%=
Lg1_K1_%=:
Lg1_L1_%=:
v_mfma_f32_16x16x32_bf16 v[30:33],a[4:7],v[34:37],0
s_add_u32 m0,s59,3072
s_mov_b32 s67,s65
R 54,2,5120
M 30,a[12:15],38
buffer_load_dword v8,s[36:39],s58 offen offset:0 sc1 lds
v_mfma_f32_16x16x32_bf16 v[26:29],a[0:3],v[34:37],0
s_add_u32 s65,s65,s62
R 58,2,6144
v_fma_f32 v18,-v98,v106,v18
M 26,a[8:11],38
L 3
v_fma_f32 v19,-v98,v107,v19
v_fma_f32 v20,-v98,v108,v20
M 26,a[16:19],42
R 62,2,7168
v_fma_f32 v21,-v98,v109,v21
M 26,a[24:27],46
v_mul_f32 v18,v99,v18
v_mul_f32 v19,v99,v19
M 30,a[20:23],42
R 66,2,8192
v_mul_f32 v20,v99,v20
M 30,a[28:31],46
L 3
v_mul_f32 v21,v99,v21
v_fma_f32 v114,v18,v18,s88
M 30,a[36:39],50
R 70,2,9216
v_fma_f32 v115,v19,v19,s88
M 30,a[44:47],54
v_fma_f32 v116,v20,v20,s88
v_fma_f32 v117,v21,v21,s88
M 26,a[32:35],50
R 74,2,10240
v_mul_f32 v114,v18,v114
M 26,a[40:43],54
L 3
v_exp_f32 v114,v114
M 26,a[48:51],58
R 78,2,11264
v_mul_f32 v115,v19,v115
M 26,a[56:59],62
v_exp_f32 v115,v115
M 30,a[52:55],58
R 82,2,12288
v_mul_f32 v116,v20,v116
M 30,a[60:63],62
L 3
v_exp_f32 v116,v116
M 30,a[68:71],66
R 86,2,13312
v_mul_f32 v117,v21,v117
M 30,a[76:79],70
buffer_load_dwordx2 v[102:103],v11,s[36:39],s73 offen offset:0 sc1
M 26,a[64:67],66
s_add_u32 s73,s73,s64
R 90,2,14336
v_fma_f32 v114,v114,s89,s89
M 26,a[72:75],70
L 3
v_exp_f32 v117,v117
M 26,a[80:83],74
s_waitcnt vmcnt(1)
R 94,2,15360
v_fma_f32 v115,v115,s89,s89
M 26,a[88:91],78
s_barrier
ds_read_b32 v7,v6 offset:3072
v_fma_f32 v116,v116,s89,s89
M 30,a[84:87],74
R 34,2,16384
v_fma_f32 v117,v117,s89,s89
M 30,a[92:95],78
L 4
buffer_store_dword v4,v3,s[36:39],s70 offen offset:0 sc0 sc1
M 30,a[100:103],82
s_mov_b32 s70,0x7fff0000
R 38,2,17408
v_fma_f32 v22,-v98,v110,v22
M 30,a[108:111],86
v_rcp_f32 v114,v114
M 26,a[96:99],82
R 42,2,18432
v_mul_f32 v18,v18,v114
M 26,a[104:107],86
L 4
L 3
v_cmp_eq_u32 vcc,s53,v7
M 26,a[112:115],90
R 46,2,19456
v_fma_f32 v23,-v98,v111,v23
M 26,a[120:123],94
v_rcp_f32 v115,v115
M 30,a[116:119],90
s_cmp_eq_u32 vcc_lo,-1
R 50,2,20480
v_mul_f32 v19,v19,v115
s_cbranch_scc0 Lg1_X1_%=
M 30,a[124:127],94
L 3
s_add_u32 m0,s57,0
s_add_u32 s56,s55,0
M 30,a[132:135],34
s_add_u32 s58,s58,s61
R 54,2,21504
v_cvt_pk_bf16_f32 v122,v18,v19
M 30,a[140:143],38
D 0,56,0
M 26,a[128:131],34
s_add_u32 s54,s54,s46
R 58,2,22528
v_fma_f32 v24,-v98,v112,v24
M 26,a[136:139],38
L 3
v_rcp_f32 v116,v116
M 26,a[144:147],42
R 62,2,23552
v_mul_f32 v20,v20,v116
M 26,a[152:155],46
D 0,56,1024
M 30,a[148:151],42
R 66,2,24576
v_fma_f32 v25,-v98,v113,v25
M 30,a[156:159],46
L 3
v_rcp_f32 v117,v117
M 30,a[164:167],50
R 70,2,25600
v_mul_f32 v21,v21,v117
M 30,a[172:175],54
D 0,56,2048
M 26,a[160:163],50
R 74,2,26624
v_cvt_pk_bf16_f32 v123,v20,v21
M 26,a[168:171],54
L 3
v_mul_f32 v22,v99,v22
v_mul_f32 v23,v99,v23
M 26,a[176:179],58
R 78,2,27648
v_mul_f32 v24,v99,v24
M 26,a[184:187],62
D 0,56,3072
M 30,a[180:183],58
s_add_u32 m0,s59,1024
R 82,2,28672
v_mul_f32 v25,v99,v25
M 30,a[188:191],62
L 3
buffer_load_dword v8,s[36:39],s58 offen offset:0 sc1 lds
M 30,a[196:199],66
s_add_u32 m0,s57,16384
R 86,2,29696
v_fma_f32 v118,v22,v22,s88
M 30,a[204:207],70
s_add_u32 s56,s55,16384
v_fma_f32 v119,v23,v23,s88
v_fma_f32 v120,v24,v24,s88
M 26,a[192:195],66
s_add_u32 s55,s55,s60
R 90,2,30720
v_fma_f32 v121,v25,v25,s88
M 26,a[200:203],70
L 3
D 0,56,0
M 26,a[208:211],74
s_waitcnt vmcnt(1)
R 94,2,31744
v_mul_f32 v118,v22,v118
M 26,a[216:219],78
s_barrier
ds_read_b32 v7,v6 offset:1024
v_mul_f32 v119,v23,v119
M 30,a[212:215],74
s_sub_u32 s57,s84,s57
R 34,1,0
v_mul_f32 v120,v24,v120
M 30,a[220:223],78
L 4
buffer_store_dword v4,v3,s[36:39],s70 offen offset:0 sc0 sc1
v_mul_f32 v121,v25,v121
v_exp_f32 v118,v118
v_exp_f32 v119,v119
v_exp_f32 v120,v120
v_exp_f32 v121,v121
M 30,a[228:231],82
s_mov_b32 s70,s69
R 38,1,1024
v_fma_f32 v118,v118,s89,s89
v_fma_f32 v119,v119,s89,s89
v_fma_f32 v120,v120,s89,s89
v_fma_f32 v121,v121,s89,s89
v_rcp_f32 v118,v118
v_rcp_f32 v119,v119
v_rcp_f32 v120,v120
v_rcp_f32 v121,v121
M 30,a[236:239],86
s_mov_b32 s69,s68
v_mul_f32 v22,v22,v118
v_mul_f32 v23,v23,v119
v_mul_f32 v24,v24,v120
v_mul_f32 v25,v25,v121
v_cvt_pk_bf16_f32 v124,v22,v23
v_cvt_pk_bf16_f32 v125,v24,v25
M 26,a[224:227],82
s_add_u32 s68,s68,s63
R 42,1,2048
buffer_store_dwordx4 v[122:125],v15,s[36:39],s66 offen offset:0 sc0 sc1
M 26,a[232:235],86
L 4
L 3
M 26,a[240:243],90
R 46,1,3072
v_cmp_eq_u32 vcc,s53,v7
M 26,a[248:251],94
D 0,56,1024
M 30,a[244:247],90
L 2
s_cmp_eq_u32 vcc_lo,-1
R 50,1,4096
buffer_store_dwordx4 v[122:125],v16,s[36:39],s66 offen offset:0 sc0 sc1
M 30,a[252:255],94
D 0,56,2048
D 0,56,3072
buffer_store_dword v4,v3,s[36:39],s71 offen offset:0 sc0 sc1
buffer_store_dword v4,v3,s[36:39],s71 offen offset:0 sc0 sc1
buffer_store_dword v4,v3,s[36:39],s71 offen offset:0 sc0 sc1
s_cbranch_scc1 Lg1_F0_%=
s_branch Lg1_K0_%=
Lg1_X1_%=:
M 30,a[124:127],94
L 3
s_add_u32 m0,s59,1024
v_cvt_pk_bf16_f32 v122,v18,v19
M 30,a[132:135],34
R 54,2,21504
v_fma_f32 v24,-v98,v112,v24
M 30,a[140:143],38
buffer_load_dword v8,s[36:39],s58 offen offset:0 sc1 lds
M 26,a[128:131],34
R 58,2,22528
v_fma_f32 v25,-v98,v113,v25
M 26,a[136:139],38
L 3
v_rcp_f32 v116,v116
M 26,a[144:147],42
R 62,2,23552
v_mul_f32 v20,v20,v116
M 26,a[152:155],46
v_rcp_f32 v117,v117
M 30,a[148:151],42
R 66,2,24576
v_mul_f32 v21,v21,v117
M 30,a[156:159],46
L 3
v_cvt_pk_bf16_f32 v123,v20,v21
v_mul_f32 v22,v99,v22
M 30,a[164:167],50
R 70,2,25600
v_mul_f32 v23,v99,v23
M 30,a[172:175],54
v_mul_f32 v24,v99,v24
v_mul_f32 v25,v99,v25
M 26,a[160:163],50
R 74,2,26624
v_fma_f32 v118,v22,v22,s88
M 26,a[168:171],54
L 3
v_fma_f32 v119,v23,v23,s88
v_fma_f32 v120,v24,v24,s88
M 26,a[176:179],58
R 78,2,27648
v_fma_f32 v121,v25,v25,s88
M 26,a[184:187],62
v_mul_f32 v118,v22,v118
v_mul_f32 v119,v23,v119
M 30,a[180:183],58
R 82,2,28672
v_mul_f32 v120,v24,v120
M 30,a[188:191],62
L 3
v_exp_f32 v118,v118
M 30,a[196:199],66
R 86,2,29696
v_mul_f32 v121,v25,v121
M 30,a[204:207],70
v_exp_f32 v119,v119
M 26,a[192:195],66
R 90,2,30720
v_fma_f32 v118,v118,s89,s89
M 26,a[200:203],70
L 3
v_exp_f32 v120,v120
M 26,a[208:211],74
s_waitcnt vmcnt(0)
R 94,2,31744
v_fma_f32 v119,v119,s89,s89
M 26,a[216:219],78
s_barrier
buffer_store_dword v4,v3,s[36:39],s70 offen offset:0 sc0 sc1
M 30,a[212:215],74
s_mov_b32 s70,s69
ds_read_b32 v7,v6 offset:1024
v_fma_f32 v120,v120,s89,s89
M 30,a[220:223],78
L 3
v_exp_f32 v121,v121
M 30,a[228:231],82
s_mov_b32 s69,s68
v_fma_f32 v121,v121,s89,s89
v_rcp_f32 v118,v118
v_rcp_f32 v119,v119
v_rcp_f32 v120,v120
v_rcp_f32 v121,v121
M 30,a[236:239],86
s_add_u32 s68,s68,s63
v_mul_f32 v22,v22,v118
v_mul_f32 v23,v23,v119
v_mul_f32 v24,v24,v120
v_mul_f32 v25,v25,v121
v_cvt_pk_bf16_f32 v124,v22,v23
v_cvt_pk_bf16_f32 v125,v24,v25
M 26,a[224:227],82
buffer_store_dwordx4 v[122:125],v15,s[36:39],s66 offen offset:0 sc0 sc1
M 26,a[232:235],86
L 1
M 26,a[240:243],90
L 0
v_cmp_eq_u32 vcc,s53,v7
M 26,a[248:251],94
buffer_store_dwordx4 v[122:125],v16,s[36:39],s66 offen offset:0 sc0 sc1
M 30,a[244:247],90
s_nop 0
M 30,a[252:255],94
s_mov_b32 s80,0
s_cmp_eq_u32 vcc_lo,-1
s_cbranch_scc0 Lg1_xn1_%=
s_add_u32 m0,s57,0
s_add_u32 s56,s55,0
D 0,56,0
v_fma_f32 v26,-v102,v106,v26
v_fma_f32 v27,-v102,v107,v27
v_fma_f32 v28,-v102,v108,v28
v_fma_f32 v29,-v102,v109,v29
v_mul_f32 v26,v103,v26
v_mul_f32 v27,v103,v27
v_mul_f32 v28,v103,v28
D 0,56,1024
v_mul_f32 v29,v103,v29
v_fma_f32 v114,v26,v26,s88
v_fma_f32 v115,v27,v27,s88
v_fma_f32 v116,v28,v28,s88
v_fma_f32 v117,v29,v29,s88
v_mul_f32 v114,v26,v114
v_mul_f32 v115,v27,v115
D 0,56,2048
)"
R"(v_mul_f32 v116,v28,v116
v_mul_f32 v117,v29,v117
v_exp_f32 v114,v114
v_exp_f32 v115,v115
v_exp_f32 v116,v116
v_exp_f32 v117,v117
v_fma_f32 v114,v114,s89,s89
D 0,56,3072
v_fma_f32 v115,v115,s89,s89
v_fma_f32 v116,v116,s89,s89
v_fma_f32 v117,v117,s89,s89
v_rcp_f32 v114,v114
v_rcp_f32 v115,v115
v_rcp_f32 v116,v116
v_rcp_f32 v117,v117
s_add_u32 s58,s58,s61
s_add_u32 m0,s57,16384
s_add_u32 s56,s55,16384
D 0,56,0
v_mul_f32 v26,v26,v114
v_mul_f32 v27,v27,v115
v_mul_f32 v28,v28,v116
v_mul_f32 v29,v29,v117
v_cvt_pk_bf16_f32 v122,v26,v27
v_cvt_pk_bf16_f32 v123,v28,v29
v_fma_f32 v30,-v102,v110,v30
D 0,56,1024
v_fma_f32 v31,-v102,v111,v31
v_fma_f32 v32,-v102,v112,v32
v_fma_f32 v33,-v102,v113,v33
v_mul_f32 v30,v103,v30
v_mul_f32 v31,v103,v31
v_mul_f32 v32,v103,v32
v_mul_f32 v33,v103,v33
D 0,56,2048
v_fma_f32 v118,v30,v30,s88
v_fma_f32 v119,v31,v31,s88
v_fma_f32 v120,v32,v32,s88
v_fma_f32 v121,v33,v33,s88
v_mul_f32 v118,v30,v118
v_mul_f32 v119,v31,v119
v_mul_f32 v120,v32,v120
D 0,56,3072
v_mul_f32 v121,v33,v121
v_exp_f32 v118,v118
v_exp_f32 v119,v119
v_exp_f32 v120,v120
v_exp_f32 v121,v121
v_fma_f32 v118,v118,s89,s89
v_fma_f32 v119,v119,s89,s89
s_add_u32 s55,s55,s60
s_sub_u32 s57,s84,s57
s_add_u32 s54,s54,s46
v_fma_f32 v120,v120,s89,s89
v_fma_f32 v121,v121,s89,s89
v_rcp_f32 v118,v118
v_rcp_f32 v119,v119
v_rcp_f32 v120,v120
v_rcp_f32 v121,v121
v_mul_f32 v30,v30,v118
v_mul_f32 v31,v31,v119
v_mul_f32 v32,v32,v120
v_mul_f32 v33,v33,v121
v_cvt_pk_bf16_f32 v124,v30,v31
v_cvt_pk_bf16_f32 v125,v32,v33
buffer_store_dwordx4 v[122:125],v15,s[36:39],s67 offen offset:0 sc0 sc1
buffer_store_dwordx4 v[122:125],v16,s[36:39],s67 offen offset:0 sc0 sc1
s_mov_b32 s72,s69
s_mov_b32 s69,0x7fff0000
s_mov_b32 s67,0x7fff0000
s_mov_b32 s77,0x7fff0000
s_branch Lg1_gox_%=
Lg1_xn1_%=:
s_cmp_ge_u32 s54,256
s_cbranch_scc1 Lg1_fin1_%=
s_add_u32 m0,s59,4096
s_nop 0
buffer_load_dword v8,s[36:39],s58 offen offset:0 sc1 lds
buffer_store_dword v4,v3,s[36:39],s71 offen offset:0 sc0 sc1
s_add_u32 m0,s59,5120
s_nop 0
buffer_load_dword v8,s[36:39],s58 offen offset:0 sc1 lds
buffer_store_dword v4,v3,s[36:39],s71 offen offset:0 sc0 sc1
s_mov_b32 s83,0
v_fma_f32 v26,-v102,v106,v26
v_fma_f32 v27,-v102,v107,v27
v_fma_f32 v28,-v102,v108,v28
v_fma_f32 v29,-v102,v109,v29
v_mul_f32 v26,v103,v26
v_mul_f32 v27,v103,v27
v_mul_f32 v28,v103,v28
v_mul_f32 v29,v103,v29
v_fma_f32 v114,v26,v26,s88
v_fma_f32 v115,v27,v27,s88
v_fma_f32 v116,v28,v28,s88
v_fma_f32 v117,v29,v29,s88
v_mul_f32 v114,v26,v114
v_mul_f32 v115,v27,v115
v_mul_f32 v116,v28,v116
v_mul_f32 v117,v29,v117
v_exp_f32 v114,v114
v_exp_f32 v115,v115
v_exp_f32 v116,v116
v_exp_f32 v117,v117
v_fma_f32 v114,v114,s89,s89
v_fma_f32 v115,v115,s89,s89
v_fma_f32 v116,v116,s89,s89
v_fma_f32 v117,v117,s89,s89
v_rcp_f32 v114,v114
v_rcp_f32 v115,v115
v_rcp_f32 v116,v116
v_rcp_f32 v117,v117
v_mul_f32 v26,v26,v114
v_mul_f32 v27,v27,v115
v_mul_f32 v28,v28,v116
v_mul_f32 v29,v29,v117
v_cvt_pk_bf16_f32 v122,v26,v27
v_cvt_pk_bf16_f32 v123,v28,v29
v_fma_f32 v30,-v102,v110,v30
v_fma_f32 v31,-v102,v111,v31
v_fma_f32 v32,-v102,v112,v32
v_fma_f32 v33,-v102,v113,v33
v_mul_f32 v30,v103,v30
v_mul_f32 v31,v103,v31
v_mul_f32 v32,v103,v32
v_mul_f32 v33,v103,v33
v_fma_f32 v118,v30,v30,s88
v_fma_f32 v119,v31,v31,s88
v_fma_f32 v120,v32,v32,s88
v_fma_f32 v121,v33,v33,s88
v_mul_f32 v118,v30,v118
v_mul_f32 v119,v31,v119
v_mul_f32 v120,v32,v120
v_mul_f32 v121,v33,v121
v_exp_f32 v118,v118
v_exp_f32 v119,v119
v_exp_f32 v120,v120
v_exp_f32 v121,v121
v_fma_f32 v118,v118,s89,s89
v_fma_f32 v119,v119,s89,s89
v_fma_f32 v120,v120,s89,s89
v_fma_f32 v121,v121,s89,s89
v_rcp_f32 v118,v118
v_rcp_f32 v119,v119
v_rcp_f32 v120,v120
v_rcp_f32 v121,v121
v_mul_f32 v30,v30,v118
v_mul_f32 v31,v31,v119
v_mul_f32 v32,v32,v120
v_mul_f32 v33,v33,v121
v_cvt_pk_bf16_f32 v124,v30,v31
v_cvt_pk_bf16_f32 v125,v32,v33
buffer_store_dwordx4 v[122:125],v15,s[36:39],s67 offen offset:0 sc0 sc1
buffer_store_dwordx4 v[122:125],v16,s[36:39],s67 offen offset:0 sc0 sc1
s_mov_b32 s72,s69
s_mov_b32 s69,0x7fff0000
s_mov_b32 s67,0x7fff0000
s_mov_b32 s77,0x7fff0000
s_branch Lg1_poll_%=
Lg1_fin1_%=:
v_fma_f32 v26,-v102,v106,v26
v_fma_f32 v27,-v102,v107,v27
v_fma_f32 v28,-v102,v108,v28
v_fma_f32 v29,-v102,v109,v29
v_mul_f32 v26,v103,v26
v_mul_f32 v27,v103,v27
v_mul_f32 v28,v103,v28
v_mul_f32 v29,v103,v29
v_fma_f32 v114,v26,v26,s88
v_fma_f32 v115,v27,v27,s88
v_fma_f32 v116,v28,v28,s88
v_fma_f32 v117,v29,v29,s88
v_mul_f32 v114,v26,v114
v_mul_f32 v115,v27,v115
v_mul_f32 v116,v28,v116
v_mul_f32 v117,v29,v117
v_exp_f32 v114,v114
v_exp_f32 v115,v115
v_exp_f32 v116,v116
v_exp_f32 v117,v117
v_fma_f32 v114,v114,s89,s89
v_fma_f32 v115,v115,s89,s89
v_fma_f32 v116,v116,s89,s89
v_fma_f32 v117,v117,s89,s89
v_rcp_f32 v114,v114
v_rcp_f32 v115,v115
v_rcp_f32 v116,v116
v_rcp_f32 v117,v117
v_mul_f32 v26,v26,v114
v_mul_f32 v27,v27,v115
v_mul_f32 v28,v28,v116
v_mul_f32 v29,v29,v117
v_cvt_pk_bf16_f32 v122,v26,v27
v_cvt_pk_bf16_f32 v123,v28,v29
v_fma_f32 v30,-v102,v110,v30
v_fma_f32 v31,-v102,v111,v31
v_fma_f32 v32,-v102,v112,v32
v_fma_f32 v33,-v102,v113,v33
v_mul_f32 v30,v103,v30
v_mul_f32 v31,v103,v31
v_mul_f32 v32,v103,v32
v_mul_f32 v33,v103,v33
v_fma_f32 v118,v30,v30,s88
v_fma_f32 v119,v31,v31,s88
v_fma_f32 v120,v32,v32,s88
v_fma_f32 v121,v33,v33,s88
v_mul_f32 v118,v30,v118
v_mul_f32 v119,v31,v119
v_mul_f32 v120,v32,v120
v_mul_f32 v121,v33,v121
v_exp_f32 v118,v118
v_exp_f32 v119,v119
v_exp_f32 v120,v120
v_exp_f32 v121,v121
v_fma_f32 v118,v118,s89,s89
v_fma_f32 v119,v119,s89,s89
v_fma_f32 v120,v120,s89,s89
v_fma_f32 v121,v121,s89,s89
v_rcp_f32 v118,v118
v_rcp_f32 v119,v119
v_rcp_f32 v120,v120
v_rcp_f32 v121,v121
v_mul_f32 v30,v30,v118
v_mul_f32 v31,v31,v119
v_mul_f32 v32,v32,v120
v_mul_f32 v33,v33,v121
v_cvt_pk_bf16_f32 v124,v30,v31
v_cvt_pk_bf16_f32 v125,v32,v33
buffer_store_dwordx4 v[122:125],v15,s[36:39],s67 offen offset:0 sc0 sc1
buffer_store_dwordx4 v[122:125],v16,s[36:39],s67 offen offset:0 sc0 sc1
s_mov_b32 s72,s69
s_mov_b32 s69,0x7fff0000
s_mov_b32 s67,0x7fff0000
s_mov_b32 s77,0x7fff0000
s_branch Lg1_done_%=
Lg1_done_%=:
s_waitcnt vmcnt(2)
s_barrier
buffer_store_dword v4,v3,s[36:39],s70 offen offset:0 sc0 sc1
s_waitcnt vmcnt(1)
s_barrier
buffer_store_dword v4,v3,s[36:39],s72 offen offset:0 sc0 sc1
s_waitcnt vmcnt(0) lgkmcnt(0)
s_barrier
s_branch Lg2_end_%=
Lg2_begin_%=:
Lcode_g2_b:
s_mov_b64 s[36:37],%1
s_mov_b32 s38,111243392
s_mov_b32 s39,0x00020000
s_mov_b32 s44,%2
s_mov_b32 s45,%3
s_mov_b32 s46,%4
s_mov_b32 s47,%5
s_mov_b32 s48,%6
s_mov_b32 s49,%7
s_mov_b32 s50,%8
s_mov_b32 s51,%9
s_mov_b32 s53,%10
v_mov_b32 v0,%11
v_mov_b32 v3,%12
v_mov_b32 v5,%13
v_mov_b32 v6,%14
v_mov_b32 v18,%15
v_mov_b32 v19,%16
v_mov_b32 v20,%17
v_mov_b32 v21,%18
v_mov_b32 v9,%19
v_mov_b32 v10,%20
v_mov_b32 v11,%21
v_mov_b32 v12,%22
v_mov_b32 v13,%23
v_mov_b32 v14,%24
v_mov_b32 v15,%25
v_mov_b32 v16,%26
s_mov_b64 s[40:41],%27
s_mov_b32 s42,2097152
s_mov_b32 s43,0x00020000
s_lshl_b32 s57,s44,11
s_cmp_ge_u32 s45,8
s_cbranch_scc1 Lg2_dly_%=
s_sleep 16
s_sleep 16
s_sleep 16
s_sleep 16
Lg2_dly_%=:
s_add_u32 s55,s50,0
s_add_u32 m0,s57,0
s_nop 0
buffer_load_dwordx4 v18,s[40:43],s55 offen offset:0  lds
s_add_u32 m0,s57,1024
s_nop 0
buffer_load_dwordx4 v19,s[40:43],s55 offen offset:0  lds
s_add_u32 m0,s57,8064
s_nop 0
buffer_load_dwordx4 v18,s[40:43],s55 offen offset:128  lds
s_add_u32 m0,s57,9088
s_nop 0
buffer_load_dwordx4 v19,s[40:43],s55 offen offset:128  lds
s_add_u32 s55,s50,131072
s_add_u32 m0,s57,16384
s_nop 0
buffer_load_dwordx4 v18,s[40:43],s55 offen offset:0  lds
s_add_u32 m0,s57,17408
s_nop 0
buffer_load_dwordx4 v19,s[40:43],s55 offen offset:0  lds
s_add_u32 m0,s57,24448
s_nop 0
buffer_load_dwordx4 v18,s[40:43],s55 offen offset:128  lds
s_add_u32 m0,s57,25472
s_nop 0
buffer_load_dwordx4 v19,s[40:43],s55 offen offset:128  lds
s_add_u32 s55,s50,262144
s_add_u32 m0,s57,32768
s_nop 0
buffer_load_dwordx4 v18,s[40:43],s55 offen offset:0  lds
s_add_u32 m0,s57,33792
s_nop 0
buffer_load_dwordx4 v19,s[40:43],s55 offen offset:0  lds
s_add_u32 m0,s57,40832
s_nop 0
buffer_load_dwordx4 v18,s[40:43],s55 offen offset:128  lds
s_add_u32 m0,s57,41856
s_nop 0
buffer_load_dwordx4 v19,s[40:43],s55 offen offset:128  lds
s_add_u32 s55,s50,393216
s_add_u32 m0,s57,49152
s_nop 0
buffer_load_dwordx4 v18,s[40:43],s55 offen offset:0  lds
s_add_u32 m0,s57,50176
s_nop 0
buffer_load_dwordx4 v19,s[40:43],s55 offen offset:0  lds
s_add_u32 m0,s57,57216
s_nop 0
buffer_load_dwordx4 v18,s[40:43],s55 offen offset:128  lds
s_add_u32 m0,s57,58240
s_nop 0
buffer_load_dwordx4 v19,s[40:43],s55 offen offset:128  lds
s_add_u32 s55,s50,524288
s_add_u32 m0,s57,65536
s_nop 0
buffer_load_dwordx4 v18,s[40:43],s55 offen offset:0  lds
s_add_u32 m0,s57,66560
s_nop 0
buffer_load_dwordx4 v19,s[40:43],s55 offen offset:0  lds
s_add_u32 m0,s57,73600
s_nop 0
buffer_load_dwordx4 v18,s[40:43],s55 offen offset:128  lds
s_add_u32 m0,s57,74624
s_nop 0
buffer_load_dwordx4 v19,s[40:43],s55 offen offset:128  lds
s_add_u32 s55,s50,655360
s_add_u32 m0,s57,81920
s_nop 0
buffer_load_dwordx4 v18,s[40:43],s55 offen offset:0  lds
s_add_u32 m0,s57,82944
s_nop 0
buffer_load_dwordx4 v19,s[40:43],s55 offen offset:0  lds
s_add_u32 m0,s57,89984
s_nop 0
buffer_load_dwordx4 v18,s[40:43],s55 offen offset:128  lds
s_add_u32 m0,s57,91008
s_nop 0
buffer_load_dwordx4 v19,s[40:43],s55 offen offset:128  lds
s_waitcnt vmcnt(20) lgkmcnt(0)
s_barrier
s_add_u32 s55,s50,786432
s_add_u32 m0,s57,98304
s_nop 0
buffer_load_dwordx4 v18,s[40:43],s55 offen offset:0  lds
s_add_u32 m0,s57,99328
s_nop 0
buffer_load_dwordx4 v19,s[40:43],s55 offen offset:0  lds
s_add_u32 m0,s57,106368
s_nop 0
buffer_load_dwordx4 v18,s[40:43],s55 offen offset:128  lds
s_add_u32 m0,s57,107392
s_nop 0
buffer_load_dwordx4 v19,s[40:43],s55 offen offset:128  lds
v_add_u32 v26,0,v20
v_add_u32 v27,0,v21
T a[0:1],26,0
T a[2:3],26,512
T a[4:5],27,0
T a[6:7],27,512
T a[8:9],26,4096
T a[10:11],26,4608
T a[12:13],27,4096
T a[14:15],27,4608
s_waitcnt vmcnt(20) lgkmcnt(0)
s_barrier
s_add_u32 s55,s50,917504
s_add_u32 m0,s57,0
s_nop 0
buffer_load_dwordx4 v18,s[40:43],s55 offen offset:0  lds
s_add_u32 m0,s57,1024
s_nop 0
buffer_load_dwordx4 v19,s[40:43],s55 offen offset:0  lds
s_add_u32 m0,s57,8064
s_nop 0
buffer_load_dwordx4 v18,s[40:43],s55 offen offset:128  lds
s_add_u32 m0,s57,9088
s_nop 0
buffer_load_dwordx4 v19,s[40:43],s55 offen offset:128  lds
v_add_u32 v26,16384,v20
v_add_u32 v27,16384,v21
T a[16:17],26,0
T a[18:19],26,512
T a[20:21],27,0
T a[22:23],27,512
T a[24:25],26,4096
T a[26:27],26,4608
T a[28:29],27,4096
T a[30:31],27,4608
s_waitcnt vmcnt(20) lgkmcnt(0)
s_barrier
s_add_u32 s55,s50,1048576
s_add_u32 m0,s57,16384
s_nop 0
buffer_load_dwordx4 v18,s[40:43],s55 offen offset:0  lds
s_add_u32 m0,s57,17408
s_nop 0
buffer_load_dwordx4 v19,s[40:43],s55 offen offset:0  lds
s_add_u32 m0,s57,24448
s_nop 0
buffer_load_dwordx4 v18,s[40:43],s55 offen offset:128  lds
s_add_u32 m0,s57,25472
s_nop 0
buffer_load_dwordx4 v19,s[40:43],s55 offen offset:128  lds
v_add_u32 v26,32768,v20
v_add_u32 v27,32768,v21
T a[32:33],26,0
T a[34:35],26,512
T a[36:37],27,0
T a[38:39],27,512
T a[40:41],26,4096
T a[42:43],26,4608
T a[44:45],27,4096
T a[46:47],27,4608
s_waitcnt vmcnt(20) lgkmcnt(0)
s_barrier
s_add_u32 s55,s50,1179648
s_add_u32 m0,s57,32768
s_nop 0
buffer_load_dwordx4 v18,s[40:43],s55 offen offset:0  lds
s_add_u32 m0,s57,33792
s_nop 0
buffer_load_dwordx4 v19,s[40:43],s55 offen offset:0  lds
s_add_u32 m0,s57,40832
s_nop 0
buffer_load_dwordx4 v18,s[40:43],s55 offen offset:128  lds
s_add_u32 m0,s57,41856
s_nop 0
buffer_load_dwordx4 v19,s[40:43],s55 offen offset:128  lds
v_add_u32 v26,49152,v20
v_add_u32 v27,49152,v21
T a[48:49],26,0
T a[50:51],26,512
T a[52:53],27,0
T a[54:55],27,512
T a[56:57],26,4096
T a[58:59],26,4608
T a[60:61],27,4096
T a[62:63],27,4608
s_waitcnt vmcnt(20) lgkmcnt(0)
s_barrier
s_add_u32 s55,s50,1310720
s_add_u32 m0,s57,49152
s_nop 0
buffer_load_dwordx4 v18,s[40:43],s55 offen offset:0  lds
s_add_u32 m0,s57,50176
s_nop 0
buffer_load_dwordx4 v19,s[40:43],s55 offen offset:0  lds
s_add_u32 m0,s57,57216
s_nop 0
buffer_load_dwordx4 v18,s[40:43],s55 offen offset:128  lds
s_add_u32 m0,s57,58240
s_nop 0
buffer_load_dwordx4 v19,s[40:43],s55 offen offset:128  lds
v_add_u32 v26,65536,v20
v_add_u32 v27,65536,v21
T a[64:65],26,0
T a[66:67],26,512
T a[68:69],27,0
T a[70:71],27,512
T a[72:73],26,4096
T a[74:75],26,4608
T a[76:77],27,4096
T a[78:79],27,4608
s_waitcnt vmcnt(20) lgkmcnt(0)
s_barrier
s_add_u32 s55,s50,1441792
s_add_u32 m0,s57,65536
s_nop 0
buffer_load_dwordx4 v18,s[40:43],s55 offen offset:0  lds
s_add_u32 m0,s57,66560
s_nop 0
buffer_load_dwordx4 v19,s[40:43],s55 offen offset:0  lds
s_add_u32 m0,s57,73600
s_nop 0
)"
R"(buffer_load_dwordx4 v18,s[40:43],s55 offen offset:128  lds
s_add_u32 m0,s57,74624
s_nop 0
buffer_load_dwordx4 v19,s[40:43],s55 offen offset:128  lds
v_add_u32 v26,81920,v20
v_add_u32 v27,81920,v21
T a[80:81],26,0
T a[82:83],26,512
T a[84:85],27,0
T a[86:87],27,512
T a[88:89],26,4096
T a[90:91],26,4608
T a[92:93],27,4096
T a[94:95],27,4608
s_waitcnt vmcnt(20) lgkmcnt(0)
s_barrier
s_add_u32 s55,s50,1572864
s_add_u32 m0,s57,81920
s_nop 0
buffer_load_dwordx4 v18,s[40:43],s55 offen offset:0  lds
s_add_u32 m0,s57,82944
s_nop 0
buffer_load_dwordx4 v19,s[40:43],s55 offen offset:0  lds
s_add_u32 m0,s57,89984
s_nop 0
buffer_load_dwordx4 v18,s[40:43],s55 offen offset:128  lds
s_add_u32 m0,s57,91008
s_nop 0
buffer_load_dwordx4 v19,s[40:43],s55 offen offset:128  lds
v_add_u32 v26,98304,v20
v_add_u32 v27,98304,v21
T a[96:97],26,0
T a[98:99],26,512
T a[100:101],27,0
T a[102:103],27,512
T a[104:105],26,4096
T a[106:107],26,4608
T a[108:109],27,4096
T a[110:111],27,4608
s_waitcnt vmcnt(20) lgkmcnt(0)
s_barrier
s_add_u32 s55,s50,1703936
s_add_u32 m0,s57,98304
s_nop 0
buffer_load_dwordx4 v18,s[40:43],s55 offen offset:0  lds
s_add_u32 m0,s57,99328
s_nop 0
buffer_load_dwordx4 v19,s[40:43],s55 offen offset:0  lds
s_add_u32 m0,s57,106368
s_nop 0
buffer_load_dwordx4 v18,s[40:43],s55 offen offset:128  lds
s_add_u32 m0,s57,107392
s_nop 0
buffer_load_dwordx4 v19,s[40:43],s55 offen offset:128  lds
v_add_u32 v26,0,v20
v_add_u32 v27,0,v21
T a[112:113],26,0
T a[114:115],26,512
T a[116:117],27,0
T a[118:119],27,512
T a[120:121],26,4096
T a[122:123],26,4608
T a[124:125],27,4096
T a[126:127],27,4608
s_waitcnt vmcnt(20) lgkmcnt(0)
s_barrier
s_add_u32 s55,s50,1835008
s_add_u32 m0,s57,0
s_nop 0
buffer_load_dwordx4 v18,s[40:43],s55 offen offset:0  lds
s_add_u32 m0,s57,1024
s_nop 0
buffer_load_dwordx4 v19,s[40:43],s55 offen offset:0  lds
s_add_u32 m0,s57,8064
s_nop 0
buffer_load_dwordx4 v18,s[40:43],s55 offen offset:128  lds
s_add_u32 m0,s57,9088
s_nop 0
buffer_load_dwordx4 v19,s[40:43],s55 offen offset:128  lds
v_add_u32 v26,16384,v20
v_add_u32 v27,16384,v21
T a[128:129],26,0
T a[130:131],26,512
T a[132:133],27,0
T a[134:135],27,512
T a[136:137],26,4096
T a[138:139],26,4608
T a[140:141],27,4096
T a[142:143],27,4608
s_waitcnt vmcnt(20) lgkmcnt(0)
s_barrier
s_add_u32 s55,s50,1966080
s_add_u32 m0,s57,16384
s_nop 0
buffer_load_dwordx4 v18,s[40:43],s55 offen offset:0  lds
s_add_u32 m0,s57,17408
s_nop 0
buffer_load_dwordx4 v19,s[40:43],s55 offen offset:0  lds
s_add_u32 m0,s57,24448
s_nop 0
buffer_load_dwordx4 v18,s[40:43],s55 offen offset:128  lds
s_add_u32 m0,s57,25472
s_nop 0
buffer_load_dwordx4 v19,s[40:43],s55 offen offset:128  lds
v_add_u32 v26,32768,v20
v_add_u32 v27,32768,v21
T a[144:145],26,0
T a[146:147],26,512
T a[148:149],27,0
T a[150:151],27,512
T a[152:153],26,4096
T a[154:155],26,4608
T a[156:157],27,4096
T a[158:159],27,4608
s_waitcnt vmcnt(20) lgkmcnt(0)
s_barrier
v_add_u32 v26,49152,v20
v_add_u32 v27,49152,v21
T a[160:161],26,0
T a[162:163],26,512
T a[164:165],27,0
T a[166:167],27,512
T a[168:169],26,4096
T a[170:171],26,4608
T a[172:173],27,4096
T a[174:175],27,4608
s_waitcnt vmcnt(16) lgkmcnt(0)
s_barrier
v_add_u32 v26,65536,v20
v_add_u32 v27,65536,v21
T a[176:177],26,0
T a[178:179],26,512
T a[180:181],27,0
T a[182:183],27,512
T a[184:185],26,4096
T a[186:187],26,4608
T a[188:189],27,4096
T a[190:191],27,4608
s_waitcnt vmcnt(12) lgkmcnt(0)
s_barrier
v_add_u32 v26,81920,v20
v_add_u32 v27,81920,v21
T a[192:193],26,0
T a[194:195],26,512
T a[196:197],27,0
T a[198:199],27,512
T a[200:201],26,4096
T a[202:203],26,4608
T a[204:205],27,4096
T a[206:207],27,4608
s_waitcnt vmcnt(8) lgkmcnt(0)
s_barrier
v_add_u32 v26,98304,v20
v_add_u32 v27,98304,v21
T a[208:209],26,0
T a[210:211],26,512
T a[212:213],27,0
T a[214:215],27,512
T a[216:217],26,4096
T a[218:219],26,4608
T a[220:221],27,4096
T a[222:223],27,4608
s_waitcnt vmcnt(4) lgkmcnt(0)
s_barrier
v_add_u32 v26,0,v20
v_add_u32 v27,0,v21
T a[224:225],26,0
T a[226:227],26,512
T a[228:229],27,0
T a[230:231],27,512
T a[232:233],26,4096
T a[234:235],26,4608
T a[236:237],27,4096
T a[238:239],27,4608
s_waitcnt vmcnt(0) lgkmcnt(0)
s_barrier
v_add_u32 v26,16384,v20
v_add_u32 v27,16384,v21
T a[240:241],26,0
T a[242:243],26,512
T a[244:245],27,0
T a[246:247],27,512
T a[248:249],26,4096
T a[250:251],26,4608
T a[252:253],27,4096
T a[254:255],27,4608
L 0
s_barrier
v_mov_b32 v4,s53
s_mov_b32 s82,0
s_mov_b32 s80,0
s_mov_b32 s54,s45
s_mul_i32 s57,s44,5120
s_mul_i32 s55,s45,40960
s_add_u32 s55,s55,s57
s_add_u32 s55,s55,17825792
s_mul_i32 s57,s44,4096
s_mul_i32 s60,s46,40960
s_mul_i32 s61,s46,1024
s_mul_i32 s62,s46,32768
s_mul_i32 s63,s46,1024
s_mul_i32 s64,s46,512
s_lshl_b32 s59,s44,8
s_add_u32 s59,s59,131072
s_mul_i32 s58,s45,1024
s_add_u32 s58,s58,109333504
s_mul_i32 s65,s45,32768
s_add_u32 s65,s65,s47
s_mul_i32 s68,s45,1024
s_add_u32 s68,s68,s48
s_mul_i32 s73,s45,512
s_add_u32 s73,s73,106954752
s_mul_i32 s74,s45,32768
s_add_u32 s74,s74,s49
s_mul_i32 s75,s45,1024
s_add_u32 s75,s75,107479040
s_mov_b32 s66,0x7fff0000
s_mov_b32 s67,0x7fff0000
s_mov_b32 s69,0x7fff0000
s_mov_b32 s76,0x7fff0000
s_mov_b32 s77,0x7fff0000
s_mov_b32 s70,0x7fff0000
s_mov_b32 s71,0x7fff0000
s_mov_b32 s72,0x7fff0000
v_mov_b32 v1,v0
v_add_u32 v2,16384,v0
s_mov_b32 s84,16384
s_mov_b32 s86,0x3a800000
s_mov_b32 s87,0x38a7c5ac
s_mov_b32 s88,0x409d30fc
s_mov_b32 s89,0xbeeff558
s_lshr_b32 s85,s44,1
s_mul_i32 s85,s85,127
v_lshlrev_b32 v8,22,v0
v_and_b32 v8,0x80000000,v8
v_or_b32 v8,v8,v5
v_add_u32 v2,32768,v0
s_lshl_b32 s84,s57,1
s_add_u32 s84,s84,32768
s_getpc_b64 s[40:41]
Lg2_pc_%=:
s_lshl_b32 s78,s44,13
v_lshlrev_b32 v106,3,v0
v_add_u32 v106,s78,v106
v_min_u32 v106,Lcode_g12_e-Lg2_pc_%=-4,v106
s_mov_b32 m0,163584
s_nop 0
global_load_lds_dword v106,s[40:41]
s_and_b32 s79,s54,127
s_cmp_eq_u32 s79,0
s_cselect_b32 s94,0x3300,0
s_cmp_eq_u32 s79,127
s_cselect_b32 s94,0xcc00,s94
s_add_u32 m0,s59,4096
s_nop 0
buffer_load_dword v8,s[36:39],s58 offen offset:0 sc1 lds
buffer_store_dword v4,v3,s[36:39],s71 offen offset:0 sc0 sc1
s_add_u32 m0,s59,5120
s_nop 0
buffer_load_dword v8,s[36:39],s58 offen offset:0 sc1 lds
buffer_store_dword v4,v3,s[36:39],s71 offen offset:0 sc0 sc1
s_mov_b32 s83,0
Lg2_poll_%=:
s_add_u32 s78,s83,2
s_and_b32 s78,s78,3
s_lshl_b32 s78,s78,10
s_add_u32 s78,s78,4096
s_add_u32 m0,s59,s78
s_nop 0
buffer_load_dword v8,s[36:39],s58 offen offset:0 sc1 lds
s_and_b32 s78,s83,3
s_lshl_b32 s78,s78,10
v_add_u32 v106,s78,v6
s_waitcnt vmcnt(4)
s_barrier
ds_read_b32 v7,v106 offset:4096
buffer_store_dword v4,v3,s[36:39],s70 offen offset:0 sc0 sc1
s_mov_b32 s70,s71
s_mov_b32 s71,s72
s_mov_b32 s72,0x7fff0000
s_add_u32 s83,s83,1
s_add_u32 s82,s82,1
L 0
v_cmp_eq_u32 vcc,s53,v7
s_nop 0
s_or_b64 vcc,vcc,s[94:95]
s_cmp_eq_u32 vcc_lo,-1
s_cbranch_scc1 Lg2_go_%=
s_cmp_lt_u32 s82,20000
s_cbranch_scc1 Lg2_poll_%=
Lg2_go_%=:
s_and_b32 s78,s54,127
s_cmp_eq_u32 s78,s85
s_cselect_b64 s[40:41],-1,0
v_cndmask_b32 v17,v15,v16,s[40:41]
s_add_u32 m0,s57,0
s_add_u32 s56,s55,0
D 17,56,0
s_add_u32 s56,s55,256
D 17,56,1024
s_add_u32 s56,s55,512
D 17,56,2048
s_add_u32 s56,s55,768
D 17,56,3072
s_add_u32 s58,s58,s61
s_add_u32 m0,s59,2048
s_nop 0
buffer_load_dword v8,s[36:39],s58 offen offset:0 sc1 lds
v_cndmask_b32 v17,v10,v9,s[40:41]
s_add_u32 m0,s57,16384
s_add_u32 s56,s55,20480
D 17,56,0
s_add_u32 s56,s55,20736
D 17,56,1024
s_add_u32 s56,s55,20992
D 17,56,2048
s_add_u32 s56,s55,21248
D 17,56,3072
s_add_u32 s55,s55,s60
s_sub_u32 s57,s84,s57
s_add_u32 s54,s54,s46
s_and_b32 s79,s54,127
s_cmp_eq_u32 s79,0
s_cselect_b32 s94,0x3300,0
s_cmp_eq_u32 s79,127
s_cselect_b32 s94,0xcc00,s94
s_waitcnt vmcnt(4)
s_barrier
buffer_store_dword v4,v3,s[36:39],s70 offen offset:0 sc0 sc1
s_min_u32 s71,s71,s72
buffer_store_dword v4,v3,s[36:39],s71 offen offset:0 sc0 sc1
s_mov_b32 s70,0x7fff0000
s_mov_b32 s71,0x7fff0000
s_mov_b32 s72,0x7fff0000
s_cmp_eq_u32 s80,1
buffer_store_dword v4,v3,s[36:39],s71 offen offset:0 sc0 sc1
s_cbranch_scc1 Lg2_st1_%=
s_branch Lg2_st0_%=
Lg2_gox_%=:
s_add_u32 m0,s59,2048
s_nop 0
buffer_load_dword v8,s[36:39],s58 offen offset:0 sc1 lds
s_and_b32 s79,s54,127
s_cmp_eq_u32 s79,0
s_cselect_b32 s94,0x3300,0
s_cmp_eq_u32 s79,127
s_cselect_b32 s94,0xcc00,s94
s_waitcnt vmcnt(0)
s_barrier
buffer_store_dword v4,v3,s[36:39],s70 offen offset:0 sc0 sc1
s_min_u32 s71,s71,s72
buffer_store_dword v4,v3,s[36:39],s71 offen offset:0 sc0 sc1
s_mov_b32 s70,0x7fff0000
s_mov_b32 s71,0x7fff0000
s_mov_b32 s72,0x7fff0000
s_cmp_eq_u32 s80,1
s_cbranch_scc1 Lg2_st1_%=
Lg2_st0_%=:
ds_read_b32 v7,v6 offset:2048
R 34,1,0
R 38,1,1024
R 42,1,2048
R 46,1,3072
R 50,1,4096
L 5
v_cmp_eq_u32 vcc,s53,v7
s_nop 0
s_or_b64 vcc,vcc,s[94:95]
L 3
s_cmp_eq_u32 vcc_lo,-1
s_cbranch_scc1 Lg2_F0_%=
s_branch Lg2_L0_%=
Lg2_st1_%=:
ds_read_b32 v7,v6 offset:2048
R 34,2,0
R 38,2,1024
R 42,2,2048
R 46,2,3072
R 50,2,4096
L 5
v_cmp_eq_u32 vcc,s53,v7
s_nop 0
s_or_b64 vcc,vcc,s[94:95]
L 3
s_cmp_eq_u32 vcc_lo,-1
s_cbranch_scc1 Lg2_F1_%=
s_branch Lg2_L1_%=
Lg2_F0_%=:
v_mfma_f32_16x16x32_bf16 v[22:25],a[4:7],v[34:37],0
s_add_u32 s58,s58,s61
s_add_u32 m0,s59,0
s_and_b32 s78,s54,127
R 54,1,5120
M 22,a[12:15],38
s_cmp_eq_u32 s78,s85
s_cselect_b64 s[40:41],-1,0
s_add_u32 s56,s55,0
buffer_load_dword v8,s[36:39],s58 offen offset:0 sc1 lds
v_mfma_f32_16x16x32_bf16 v[18:21],a[0:3],v[34:37],0
s_add_u32 m0,s57,0
R 58,1,6144
v_cndmask_b32 v17,v15,v16,s[40:41]
M 18,a[8:11],38
L 3
D 17,56,0
M 18,a[16:19],42
s_add_u32 s56,s55,256
R 62,1,7168
v_lshlrev_b32 v106,16,v126
M 18,a[24:27],46
s_add_u32 s54,s54,s46
v_and_b32 v107,0xffff0000,v126
v_lshlrev_b32 v108,16,v127
M 22,a[20:23],42
s_and_b32 s79,s54,127
R 66,1,8192
v_and_b32 v109,0xffff0000,v127
M 22,a[28:31],46
L 3
D 17,56,1024
M 22,a[36:39],50
s_add_u32 s56,s55,512
R 70,1,9216
v_lshlrev_b32 v110,16,v128
M 22,a[44:47],54
s_cmp_eq_u32 s79,0
v_and_b32 v111,0xffff0000,v128
v_lshlrev_b32 v112,16,v129
M 18,a[32:35],50
s_cselect_b32 s94,0x3300,0
R 74,1,10240
v_and_b32 v113,0xffff0000,v129
M 18,a[40:43],54
L 3
D 17,56,2048
M 18,a[48:51],58
s_add_u32 s56,s55,768
R 78,1,11264
v_fma_f32 v106,v106,v103,v102
M 18,a[56:59],62
D 17,56,3072
M 22,a[52:55],58
s_add_u32 m0,s57,16384
R 82,1,12288
v_fma_f32 v107,v107,v103,v102
M 22,a[60:63],62
L 3
v_fma_f32 v108,v108,v103,v102
v_fma_f32 v109,v109,v103,v102
M 22,a[68:71],66
s_add_u32 s56,s55,20480
R 86,1,13312
v_fma_f32 v110,v110,v103,v102
M 22,a[76:79],70
buffer_load_dwordx4 v[122:125],v0,s[36:39],s74 offen offset:0 sc1
M 18,a[64:67],66
s_cmp_eq_u32 s79,127
R 90,1,14336
v_fma_f32 v111,v111,v103,v102
M 18,a[72:75],70
L 3
buffer_load_dwordx2 v[98:99],v11,s[36:39],s73 offen offset:8 sc1
M 18,a[80:83],74
s_waitcnt vmcnt(10)
R 94,1,15360
v_fma_f32 v112,v112,v103,v102
M 18,a[88:91],78
s_barrier
v_fma_f32 v113,v113,v103,v102
v_add_f32 v26,v26,v106
M 22,a[84:87],74
s_cselect_b32 s94,0xcc00,s94
R 34,1,16384
v_add_f32 v27,v27,v107
M 22,a[92:95],78
L 3
v_add_f32 v28,v28,v108
v_add_f32 v29,v29,v109
M 22,a[100:103],82
s_mov_b32 s66,s65
R 38,1,17408
v_add_f32 v30,v30,v110
M 22,a[108:111],86
s_mov_b32 s76,s75
v_cndmask_b32 v17,v10,v9,s[40:41]
v_add_f32 v31,v31,v111
M 18,a[96:99],82
s_add_u32 s73,s73,s64
R 42,1,18432
v_add_f32 v32,v32,v112
M 18,a[104:107],86
L 3
D 17,56,0
M 18,a[112:115],90
s_add_u32 s56,s55,20736
R 46,1,19456
v_add_f32 v33,v33,v113
M 18,a[120:123],94
s_add_u32 s74,s74,s62
v_cvt_pk_bf16_f32 v114,v26,v27
)"
R"(v_cvt_pk_bf16_f32 v115,v28,v29
M 22,a[116:119],90
s_add_u32 s65,s65,s62
R 50,1,20480
v_fma_f32 v107,v26,v26,s87
M 22,a[124:127],94
L 3
v_add_f32 v106,v26,v27
v_fma_f32 v107,v27,v27,v107
M 22,a[132:135],34
s_add_u32 s75,s75,s63
R 54,1,21504
v_add_f32 v106,v106,v28
M 22,a[140:143],38
D 17,56,1024
M 18,a[128:131],34
s_add_u32 s56,s55,20992
R 58,1,22528
v_fma_f32 v107,v28,v28,v107
M 18,a[136:139],38
L 3
v_add_f32 v106,v106,v29
v_fma_f32 v107,v29,v29,v107
M 18,a[144:147],42
s_sub_u32 s57,s84,s57
R 62,1,23552
v_add_f32 v106,v106,v30
M 18,a[152:155],46
v_fma_f32 v107,v30,v30,v107
v_add_f32 v106,v106,v31
M 22,a[148:151],42
R 66,1,24576
v_fma_f32 v107,v31,v31,v107
M 22,a[156:159],46
L 3
v_add_f32 v106,v106,v32
v_fma_f32 v107,v32,v32,v107
M 22,a[164:167],50
R 70,1,25600
v_add_f32 v106,v106,v33
M 22,a[172:175],54
v_fma_f32 v107,v33,v33,v107
v_cvt_pk_bf16_f32 v116,v30,v31
M 18,a[160:163],50
R 74,1,26624
v_cvt_pk_bf16_f32 v117,v32,v33
M 18,a[168:171],54
L 3
v_permlane16_swap_b32 v106,v107
v_add_f32 v106,v106,v107
v_mov_b32 v107,v106
M 18,a[176:179],58
R 78,1,27648
M 18,a[184:187],62
v_permlane32_swap_b32 v106,v107
v_add_f32 v106,v106,v107
M 22,a[180:183],58
R 82,1,28672
ds_write_b32 v12,v106 offset:2048
M 22,a[188:191],62
L 4
D 17,56,2048
M 22,a[196:199],66
s_add_u32 s56,s55,21248
s_add_u32 s55,s55,s60
R 86,1,29696
M 22,a[204:207],70
D 17,56,3072
M 18,a[192:195],66
R 90,1,30720
M 18,a[200:203],70
L 4
L 2
buffer_store_dwordx4 v[114:117],v0,s[36:39],s67 offen offset:0 sc0 sc1
M 18,a[208:211],74
s_waitcnt vmcnt(7)
R 94,1,31744
M 18,a[216:219],78
s_barrier
ds_read_b32 v7,v6 offset:0
R 118,13,2048
M 22,a[212:215],74
R 34,2,0
M 22,a[220:223],78
L 5
buffer_store_dword v4,v3,s[36:39],s70 offen offset:0 sc0 sc1
M 22,a[228:231],82
s_mov_b32 s70,s69
s_mov_b32 s69,s68
R 38,2,1024
M 22,a[236:239],86
s_add_u32 s68,s68,s63
M 18,a[224:227],82
R 42,2,2048
M 18,a[232:235],86
L 5
L 4
v_cmp_eq_u32 vcc,s53,v7
M 18,a[240:243],90
L 3
R 46,2,3072
v_add_f32 v118,v118,v119
M 18,a[248:251],94
s_or_b64 vcc,vcc,s[94:95]
v_add_f32 v120,v120,v121
v_add_f32 v118,v118,v120
M 22,a[244:247],90
s_cmp_eq_u32 vcc_lo,-1
R 50,2,4096
v_mul_f32 v118,s86,v118
M 22,a[252:255],94
s_waitcnt vmcnt(6) lgkmcnt(3)
buffer_store_dword v118,v14,s[36:39],s77 offen offset:0 sc0 sc1
s_cbranch_scc0 Lg2_K1_%=
Lg2_F1_%=:
v_mfma_f32_16x16x32_bf16 v[30:33],a[4:7],v[34:37],0
s_add_u32 s58,s58,s61
s_add_u32 m0,s59,1024
s_and_b32 s78,s54,127
R 54,2,5120
M 30,a[12:15],38
s_cmp_eq_u32 s78,s85
s_cselect_b64 s[40:41],-1,0
s_add_u32 s56,s55,0
buffer_load_dword v8,s[36:39],s58 offen offset:0 sc1 lds
v_mfma_f32_16x16x32_bf16 v[26:29],a[0:3],v[34:37],0
s_add_u32 m0,s57,0
R 58,2,6144
v_cndmask_b32 v17,v15,v16,s[40:41]
M 26,a[8:11],38
L 3
D 17,56,0
M 26,a[16:19],42
s_add_u32 s56,s55,256
R 62,2,7168
v_lshlrev_b32 v106,16,v122
M 26,a[24:27],46
s_add_u32 s54,s54,s46
v_and_b32 v107,0xffff0000,v122
v_lshlrev_b32 v108,16,v123
M 30,a[20:23],42
s_and_b32 s79,s54,127
R 66,2,8192
v_and_b32 v109,0xffff0000,v123
M 30,a[28:31],46
L 3
D 17,56,1024
M 30,a[36:39],50
s_add_u32 s56,s55,512
R 70,2,9216
v_lshlrev_b32 v110,16,v124
M 30,a[44:47],54
s_cmp_eq_u32 s79,0
v_and_b32 v111,0xffff0000,v124
v_lshlrev_b32 v112,16,v125
M 26,a[32:35],50
s_cselect_b32 s94,0x3300,0
R 74,2,10240
v_and_b32 v113,0xffff0000,v125
M 26,a[40:43],54
L 3
D 17,56,2048
M 26,a[48:51],58
s_add_u32 s56,s55,768
R 78,2,11264
v_fma_f32 v106,v106,v99,v98
M 26,a[56:59],62
D 17,56,3072
M 30,a[52:55],58
s_add_u32 m0,s57,16384
R 82,2,12288
v_fma_f32 v107,v107,v99,v98
M 30,a[60:63],62
L 3
v_fma_f32 v108,v108,v99,v98
v_fma_f32 v109,v109,v99,v98
M 30,a[68:71],66
s_add_u32 s56,s55,20480
R 86,2,13312
v_fma_f32 v110,v110,v99,v98
M 30,a[76:79],70
buffer_load_dwordx4 v[126:129],v0,s[36:39],s74 offen offset:0 sc1
M 26,a[64:67],66
s_cmp_eq_u32 s79,127
R 90,2,14336
v_fma_f32 v111,v111,v99,v98
M 26,a[72:75],70
L 3
buffer_load_dwordx2 v[102:103],v11,s[36:39],s73 offen offset:8 sc1
M 26,a[80:83],74
s_waitcnt vmcnt(10)
R 94,2,15360
v_fma_f32 v112,v112,v99,v98
M 26,a[88:91],78
s_barrier
v_fma_f32 v113,v113,v99,v98
v_add_f32 v18,v18,v106
M 30,a[84:87],74
s_cselect_b32 s94,0xcc00,s94
R 34,2,16384
v_add_f32 v19,v19,v107
M 30,a[92:95],78
L 3
v_add_f32 v20,v20,v108
v_add_f32 v21,v21,v109
M 30,a[100:103],82
s_mov_b32 s67,s65
R 38,2,17408
v_add_f32 v22,v22,v110
M 30,a[108:111],86
s_mov_b32 s77,s75
v_cndmask_b32 v17,v10,v9,s[40:41]
v_add_f32 v23,v23,v111
M 26,a[96:99],82
s_add_u32 s73,s73,s64
R 42,2,18432
v_add_f32 v24,v24,v112
M 26,a[104:107],86
L 3
D 17,56,0
M 26,a[112:115],90
s_add_u32 s56,s55,20736
R 46,2,19456
v_add_f32 v25,v25,v113
M 26,a[120:123],94
s_add_u32 s74,s74,s62
v_cvt_pk_bf16_f32 v114,v18,v19
v_cvt_pk_bf16_f32 v115,v20,v21
M 30,a[116:119],90
s_add_u32 s65,s65,s62
R 50,2,20480
v_fma_f32 v107,v18,v18,s87
M 30,a[124:127],94
L 3
v_add_f32 v106,v18,v19
v_fma_f32 v107,v19,v19,v107
M 30,a[132:135],34
s_add_u32 s75,s75,s63
R 54,2,21504
v_add_f32 v106,v106,v20
M 30,a[140:143],38
D 17,56,1024
M 26,a[128:131],34
s_add_u32 s56,s55,20992
R 58,2,22528
v_fma_f32 v107,v20,v20,v107
M 26,a[136:139],38
L 3
v_add_f32 v106,v106,v21
v_fma_f32 v107,v21,v21,v107
M 26,a[144:147],42
s_sub_u32 s57,s84,s57
R 62,2,23552
v_add_f32 v106,v106,v22
M 26,a[152:155],46
v_fma_f32 v107,v22,v22,v107
v_add_f32 v106,v106,v23
M 30,a[148:151],42
R 66,2,24576
v_fma_f32 v107,v23,v23,v107
M 30,a[156:159],46
L 3
v_add_f32 v106,v106,v24
v_fma_f32 v107,v24,v24,v107
M 30,a[164:167],50
R 70,2,25600
v_add_f32 v106,v106,v25
M 30,a[172:175],54
v_fma_f32 v107,v25,v25,v107
v_cvt_pk_bf16_f32 v116,v22,v23
M 26,a[160:163],50
R 74,2,26624
v_cvt_pk_bf16_f32 v117,v24,v25
M 26,a[168:171],54
L 3
v_permlane16_swap_b32 v106,v107
v_add_f32 v106,v106,v107
v_mov_b32 v107,v106
M 26,a[176:179],58
R 78,2,27648
M 26,a[184:187],62
v_permlane32_swap_b32 v106,v107
v_add_f32 v106,v106,v107
M 30,a[180:183],58
R 82,2,28672
ds_write_b32 v12,v106 offset:0
M 30,a[188:191],62
L 4
D 17,56,2048
M 30,a[196:199],66
s_add_u32 s56,s55,21248
s_add_u32 s55,s55,s60
R 86,2,29696
M 30,a[204:207],70
D 17,56,3072
M 26,a[192:195],66
R 90,2,30720
M 26,a[200:203],70
L 4
L 2
buffer_store_dwordx4 v[114:117],v0,s[36:39],s66 offen offset:0 sc0 sc1
M 26,a[208:211],74
s_waitcnt vmcnt(7)
R 94,2,31744
M 26,a[216:219],78
s_barrier
ds_read_b32 v7,v6 offset:1024
R 118,13,0
M 30,a[212:215],74
R 34,1,0
M 30,a[220:223],78
L 5
buffer_store_dword v4,v3,s[36:39],s70 offen offset:0 sc0 sc1
M 30,a[228:231],82
s_mov_b32 s70,s69
s_mov_b32 s69,s68
R 38,1,1024
M 30,a[236:239],86
s_add_u32 s68,s68,s63
M 26,a[224:227],82
R 42,1,2048
M 26,a[232:235],86
L 5
L 4
v_cmp_eq_u32 vcc,s53,v7
M 26,a[240:243],90
L 3
R 46,1,3072
v_add_f32 v118,v118,v119
M 26,a[248:251],94
s_or_b64 vcc,vcc,s[94:95]
v_add_f32 v120,v120,v121
v_add_f32 v118,v118,v120
M 30,a[244:247],90
s_cmp_eq_u32 vcc_lo,-1
R 50,1,4096
v_mul_f32 v118,s86,v118
M 30,a[252:255],94
s_waitcnt vmcnt(6) lgkmcnt(3)
buffer_store_dword v118,v14,s[36:39],s76 offen offset:0 sc0 sc1
s_cbranch_scc1 Lg2_F0_%=
Lg2_K0_%=:
Lg2_L0_%=:
v_mfma_f32_16x16x32_bf16 v[22:25],a[4:7],v[34:37],0
s_add_u32 m0,s59,3072
R 54,1,5120
v_lshlrev_b32 v106,16,v126
M 22,a[12:15],38
buffer_load_dword v8,s[36:39],s58 offen offset:0 sc1 lds
v_mfma_f32_16x16x32_bf16 v[18:21],a[0:3],v[34:37],0
s_mov_b32 s66,s65
R 58,1,6144
v_and_b32 v107,0xffff0000,v126
M 18,a[8:11],38
L 3
v_lshlrev_b32 v108,16,v127
v_and_b32 v109,0xffff0000,v127
M 18,a[16:19],42
s_mov_b32 s76,s75
R 62,1,7168
v_lshlrev_b32 v110,16,v128
M 18,a[24:27],46
s_add_u32 s65,s65,s62
v_and_b32 v111,0xffff0000,v128
v_lshlrev_b32 v112,16,v129
M 22,a[20:23],42
s_add_u32 s75,s75,s63
R 66,1,8192
v_and_b32 v113,0xffff0000,v129
v_fma_f32 v106,v106,v103,v102
M 22,a[28:31],46
L 3
v_fma_f32 v107,v107,v103,v102
v_fma_f32 v108,v108,v103,v102
M 22,a[36:39],50
R 70,1,9216
v_fma_f32 v109,v109,v103,v102
M 22,a[44:47],54
v_fma_f32 v110,v110,v103,v102
v_fma_f32 v111,v111,v103,v102
M 18,a[32:35],50
R 74,1,10240
v_fma_f32 v112,v112,v103,v102
M 18,a[40:43],54
L 3
v_fma_f32 v113,v113,v103,v102
v_add_f32 v26,v26,v106
M 18,a[48:51],58
R 78,1,11264
v_add_f32 v27,v27,v107
M 18,a[56:59],62
v_add_f32 v28,v28,v108
v_add_f32 v29,v29,v109
v_add_f32 v30,v30,v110
M 22,a[52:55],58
R 82,1,12288
v_add_f32 v31,v31,v111
M 22,a[60:63],62
L 3
v_add_f32 v32,v32,v112
v_add_f32 v33,v33,v113
M 22,a[68:71],66
R 86,1,13312
v_cvt_pk_bf16_f32 v114,v26,v27
M 22,a[76:79],70
buffer_load_dwordx4 v[122:125],v0,s[36:39],s74 offen offset:0 sc1
M 18,a[64:67],66
s_add_u32 s74,s74,s62
R 90,1,14336
v_cvt_pk_bf16_f32 v115,v28,v29
M 18,a[72:75],70
L 3
buffer_load_dwordx2 v[98:99],v11,s[36:39],s73 offen offset:8 sc1
v_cvt_pk_bf16_f32 v116,v30,v31
M 18,a[80:83],74
s_waitcnt vmcnt(2)
s_add_u32 s73,s73,s64
R 94,1,15360
M 18,a[88:91],78
s_barrier
ds_read_b32 v7,v6 offset:3072
v_cvt_pk_bf16_f32 v117,v32,v33
M 22,a[84:87],74
R 34,1,16384
v_add_f32 v106,v26,v27
M 22,a[92:95],78
L 4
buffer_store_dword v4,v3,s[36:39],s70 offen offset:0 sc0 sc1
M 22,a[100:103],82
s_mov_b32 s70,0x7fff0000
R 38,1,17408
v_fma_f32 v107,v26,v26,s87
M 22,a[108:111],86
buffer_store_dwordx4 v[114:117],v0,s[36:39],s67 offen offset:0 sc0 sc1
v_fma_f32 v107,v27,v27,v107
M 18,a[96:99],82
R 42,1,18432
v_add_f32 v106,v106,v28
v_fma_f32 v107,v28,v28,v107
M 18,a[104:107],86
L 4
L 3
v_cmp_eq_u32 vcc,s53,v7
M 18,a[112:115],90
R 46,1,19456
v_add_f32 v106,v106,v29
M 18,a[120:123],94
s_or_b64 vcc,vcc,s[94:95]
v_fma_f32 v107,v29,v29,v107
v_add_f32 v106,v106,v30
M 22,a[116:119],90
R 50,1,20480
v_fma_f32 v107,v30,v30,v107
M 22,a[124:127],94
s_cmp_eq_u32 vcc_lo,-1
L 3
v_add_f32 v106,v106,v31
s_cbranch_scc0 Lg2_X0_%=
M 22,a[132:135],34
s_and_b32 s78,s54,127
s_cmp_eq_u32 s78,s85
s_cselect_b64 s[40:41],-1,0
R 54,1,21504
M 22,a[140:143],38
s_add_u32 m0,s57,0
s_add_u32 s56,s55,0
s_add_u32 s58,s58,s61
v_cndmask_b32 v17,v15,v16,s[40:41]
M 18,a[128:131],34
s_add_u32 s54,s54,s46
R 58,1,22528
v_fma_f32 v107,v31,v31,v107
M 18,a[136:139],38
L 3
D 17,56,0
M 18,a[144:147],42
s_add_u32 s56,s55,256
R 62,1,23552
v_add_f32 v106,v106,v32
M 18,a[152:155],46
s_and_b32 s79,s54,127
v_fma_f32 v107,v32,v32,v107
v_add_f32 v106,v106,v33
M 22,a[148:151],42
s_cmp_eq_u32 s79,0
R 66,1,24576
v_fma_f32 v107,v33,v33,v107
M 22,a[156:159],46
L 3
D 17,56,1024
M 22,a[164:167],50
s_cselect_b32 s94,0x3300,0
s_add_u32 s56,s55,512
R 70,1,25600
M 22,a[172:175],54
s_cmp_eq_u32 s79,127
v_permlane16_swap_b32 v106,v107
v_add_f32 v106,v106,v107
M 18,a[160:163],50
s_cselect_b32 s94,0xcc00,s94
)"
R"(R 74,1,26624
v_mov_b32 v107,v106
M 18,a[168:171],54
L 3
D 17,56,2048
M 18,a[176:179],58
s_add_u32 s56,s55,768
R 78,1,27648
M 18,a[184:187],62
v_permlane32_swap_b32 v106,v107
v_add_f32 v106,v106,v107
M 22,a[180:183],58
R 82,1,28672
ds_write_b32 v12,v106 offset:2048
M 22,a[188:191],62
L 4
D 17,56,3072
M 22,a[196:199],66
s_add_u32 m0,s59,0
s_add_u32 s56,s55,20480
R 86,1,29696
M 22,a[204:207],70
buffer_load_dword v8,s[36:39],s58 offen offset:0 sc1 lds
M 18,a[192:195],66
s_add_u32 m0,s57,16384
R 90,1,30720
v_cndmask_b32 v17,v10,v9,s[40:41]
M 18,a[200:203],70
L 4
L 2
s_sub_u32 s57,s84,s57
M 18,a[208:211],74
s_waitcnt vmcnt(0)
R 94,1,31744
M 18,a[216:219],78
s_barrier
D 17,56,0
M 22,a[212:215],74
s_add_u32 s56,s55,20736
R 34,2,0
ds_read_b32 v7,v6 offset:0
M 22,a[220:223],78
L 4
buffer_store_dword v4,v3,s[36:39],s70 offen offset:0 sc0 sc1
M 22,a[228:231],82
s_mov_b32 s70,s69
R 38,2,1024
R 118,13,2048
M 22,a[236:239],86
D 17,56,1024
M 18,a[224:227],82
s_add_u32 s56,s55,20992
s_mov_b32 s69,s68
R 42,2,2048
M 18,a[232:235],86
L 5
D 17,56,2048
M 18,a[240:243],90
L 3
s_add_u32 s56,s55,21248
R 46,2,3072
v_cmp_eq_u32 vcc,s53,v7
M 18,a[248:251],94
D 17,56,3072
M 22,a[244:247],90
s_add_u32 s55,s55,s60
s_add_u32 s68,s68,s63
L 2
s_or_b64 vcc,vcc,s[94:95]
s_cmp_eq_u32 vcc_lo,-1
R 50,2,4096
v_add_f32 v118,v118,v119
v_add_f32 v120,v120,v121
v_add_f32 v118,v118,v120
v_mul_f32 v118,s86,v118
M 22,a[252:255],94
s_nop 0
buffer_store_dword v118,v14,s[36:39],s77 offen offset:0 sc0 sc1
buffer_store_dword v4,v3,s[36:39],s71 offen offset:0 sc0 sc1
buffer_store_dword v4,v3,s[36:39],s71 offen offset:0 sc0 sc1
s_cbranch_scc1 Lg2_F1_%=
s_branch Lg2_K1_%=
Lg2_X0_%=:
M 22,a[132:135],34
s_add_u32 m0,s59,0
R 54,1,21504
v_fma_f32 v107,v31,v31,v107
M 22,a[140:143],38
buffer_load_dword v8,s[36:39],s58 offen offset:0 sc1 lds
M 18,a[128:131],34
R 58,1,22528
v_add_f32 v106,v106,v32
M 18,a[136:139],38
L 3
v_fma_f32 v107,v32,v32,v107
v_add_f32 v106,v106,v33
M 18,a[144:147],42
R 62,1,23552
v_fma_f32 v107,v33,v33,v107
M 18,a[152:155],46
M 22,a[148:151],42
R 66,1,24576
M 22,a[156:159],46
L 3
v_permlane16_swap_b32 v106,v107
v_add_f32 v106,v106,v107
M 22,a[164:167],50
R 70,1,25600
v_mov_b32 v107,v106
M 22,a[172:175],54
M 18,a[160:163],50
R 74,1,26624
M 18,a[168:171],54
L 3
v_permlane32_swap_b32 v106,v107
M 18,a[176:179],58
R 78,1,27648
v_add_f32 v106,v106,v107
M 18,a[184:187],62
ds_write_b32 v12,v106 offset:2048
M 22,a[180:183],58
R 82,1,28672
M 22,a[188:191],62
L 4
M 22,a[196:199],66
R 86,1,29696
M 22,a[204:207],70
M 18,a[192:195],66
R 90,1,30720
M 18,a[200:203],70
L 4
L 3
M 18,a[208:211],74
s_waitcnt vmcnt(0)
R 94,1,31744
M 18,a[216:219],78
s_barrier
R 118,13,2048
ds_read_b32 v7,v6 offset:0
M 22,a[212:215],74
buffer_store_dword v4,v3,s[36:39],s70 offen offset:0 sc0 sc1
M 22,a[220:223],78
L 4
s_mov_b32 s70,s69
s_mov_b32 s69,s68
M 22,a[228:231],82
s_add_u32 s68,s68,s63
M 22,a[236:239],86
M 18,a[224:227],82
M 18,a[232:235],86
L 2
L 1
L 0
M 18,a[240:243],90
v_add_f32 v118,v118,v119
v_add_f32 v120,v120,v121
v_add_f32 v118,v118,v120
M 18,a[248:251],94
v_cmp_eq_u32 vcc,s53,v7
v_mul_f32 v118,s86,v118
M 22,a[244:247],90
buffer_store_dword v118,v14,s[36:39],s77 offen offset:0 sc0 sc1
M 22,a[252:255],94
s_nop 0
s_or_b64 vcc,vcc,s[94:95]
s_mov_b32 s80,1
s_cmp_eq_u32 vcc_lo,-1
s_cbranch_scc0 Lg2_xn0_%=
s_and_b32 s78,s54,127
s_cmp_eq_u32 s78,s85
s_cselect_b64 s[40:41],-1,0
v_cndmask_b32 v17,v15,v16,s[40:41]
s_add_u32 m0,s57,0
s_add_u32 s56,s55,0
D 17,56,0
v_lshlrev_b32 v106,16,v122
v_and_b32 v107,0xffff0000,v122
v_lshlrev_b32 v108,16,v123
v_and_b32 v109,0xffff0000,v123
v_lshlrev_b32 v110,16,v124
s_add_u32 s56,s55,256
D 17,56,1024
v_and_b32 v111,0xffff0000,v124
v_lshlrev_b32 v112,16,v125
v_and_b32 v113,0xffff0000,v125
v_fma_f32 v106,v106,v99,v98
v_fma_f32 v107,v107,v99,v98
s_add_u32 s56,s55,512
D 17,56,2048
v_fma_f32 v108,v108,v99,v98
v_fma_f32 v109,v109,v99,v98
v_fma_f32 v110,v110,v99,v98
v_fma_f32 v111,v111,v99,v98
v_fma_f32 v112,v112,v99,v98
s_add_u32 s56,s55,768
D 17,56,3072
v_fma_f32 v113,v113,v99,v98
v_add_f32 v18,v18,v106
v_add_f32 v19,v19,v107
v_add_f32 v20,v20,v108
v_add_f32 v21,v21,v109
s_add_u32 s58,s58,s61
v_cndmask_b32 v17,v10,v9,s[40:41]
s_add_u32 m0,s57,16384
s_add_u32 s56,s55,20480
D 17,56,0
v_add_f32 v22,v22,v110
v_add_f32 v23,v23,v111
v_add_f32 v24,v24,v112
v_add_f32 v25,v25,v113
v_cvt_pk_bf16_f32 v114,v18,v19
s_add_u32 s56,s55,20736
D 17,56,1024
v_cvt_pk_bf16_f32 v115,v20,v21
v_cvt_pk_bf16_f32 v116,v22,v23
v_cvt_pk_bf16_f32 v117,v24,v25
s_add_u32 s56,s55,20992
D 17,56,2048
s_add_u32 s56,s55,21248
D 17,56,3072
buffer_store_dwordx4 v[114:117],v0,s[36:39],s66 offen offset:0 sc0 sc1
v_add_f32 v106,v18,v19
v_fma_f32 v107,v18,v18,s87
s_add_u32 s55,s55,s60
s_sub_u32 s57,s84,s57
s_add_u32 s54,s54,s46
v_fma_f32 v107,v19,v19,v107
v_add_f32 v106,v106,v20
v_fma_f32 v107,v20,v20,v107
v_add_f32 v106,v106,v21
v_fma_f32 v107,v21,v21,v107
v_add_f32 v106,v106,v22
v_fma_f32 v107,v22,v22,v107
v_add_f32 v106,v106,v23
v_fma_f32 v107,v23,v23,v107
v_add_f32 v106,v106,v24
v_fma_f32 v107,v24,v24,v107
v_add_f32 v106,v106,v25
v_fma_f32 v107,v25,v25,v107
s_nop 1
v_permlane16_swap_b32 v106,v107
v_add_f32 v106,v106,v107
v_mov_b32 v107,v106
s_nop 1
v_permlane32_swap_b32 v106,v107
v_add_f32 v106,v106,v107
ds_write_b32 v12,v106 offset:0
L 0
s_barrier
R 118,13,0
L 0
v_add_f32 v118,v118,v119
v_add_f32 v120,v120,v121
v_add_f32 v118,v118,v120
v_mul_f32 v118,s86,v118
buffer_store_dword v118,v14,s[36:39],s76 offen offset:0 sc0 sc1
s_mov_b32 s72,s69
s_mov_b32 s69,0x7fff0000
s_mov_b32 s66,0x7fff0000
s_mov_b32 s76,0x7fff0000
s_branch Lg2_gox_%=
Lg2_xn0_%=:
s_cmp_ge_u32 s54,256
s_cbranch_scc1 Lg2_fin0_%=
s_add_u32 m0,s59,4096
s_nop 0
buffer_load_dword v8,s[36:39],s58 offen offset:0 sc1 lds
buffer_store_dword v4,v3,s[36:39],s71 offen offset:0 sc0 sc1
s_add_u32 m0,s59,5120
s_nop 0
buffer_load_dword v8,s[36:39],s58 offen offset:0 sc1 lds
buffer_store_dword v4,v3,s[36:39],s71 offen offset:0 sc0 sc1
s_mov_b32 s83,0
v_lshlrev_b32 v106,16,v122
v_and_b32 v107,0xffff0000,v122
v_lshlrev_b32 v108,16,v123
v_and_b32 v109,0xffff0000,v123
v_lshlrev_b32 v110,16,v124
v_and_b32 v111,0xffff0000,v124
v_lshlrev_b32 v112,16,v125
v_and_b32 v113,0xffff0000,v125
v_fma_f32 v106,v106,v99,v98
v_fma_f32 v107,v107,v99,v98
v_fma_f32 v108,v108,v99,v98
v_fma_f32 v109,v109,v99,v98
v_fma_f32 v110,v110,v99,v98
v_fma_f32 v111,v111,v99,v98
v_fma_f32 v112,v112,v99,v98
v_fma_f32 v113,v113,v99,v98
v_add_f32 v18,v18,v106
v_add_f32 v19,v19,v107
v_add_f32 v20,v20,v108
v_add_f32 v21,v21,v109
v_add_f32 v22,v22,v110
v_add_f32 v23,v23,v111
v_add_f32 v24,v24,v112
v_add_f32 v25,v25,v113
v_cvt_pk_bf16_f32 v114,v18,v19
v_cvt_pk_bf16_f32 v115,v20,v21
v_cvt_pk_bf16_f32 v116,v22,v23
v_cvt_pk_bf16_f32 v117,v24,v25
buffer_store_dwordx4 v[114:117],v0,s[36:39],s66 offen offset:0 sc0 sc1
v_add_f32 v106,v18,v19
v_fma_f32 v107,v18,v18,s87
v_fma_f32 v107,v19,v19,v107
v_add_f32 v106,v106,v20
v_fma_f32 v107,v20,v20,v107
v_add_f32 v106,v106,v21
v_fma_f32 v107,v21,v21,v107
v_add_f32 v106,v106,v22
v_fma_f32 v107,v22,v22,v107
v_add_f32 v106,v106,v23
v_fma_f32 v107,v23,v23,v107
v_add_f32 v106,v106,v24
v_fma_f32 v107,v24,v24,v107
v_add_f32 v106,v106,v25
v_fma_f32 v107,v25,v25,v107
s_nop 1
v_permlane16_swap_b32 v106,v107
v_add_f32 v106,v106,v107
v_mov_b32 v107,v106
s_nop 1
v_permlane32_swap_b32 v106,v107
v_add_f32 v106,v106,v107
ds_write_b32 v12,v106 offset:0
L 0
s_barrier
R 118,13,0
L 0
v_add_f32 v118,v118,v119
v_add_f32 v120,v120,v121
v_add_f32 v118,v118,v120
v_mul_f32 v118,s86,v118
buffer_store_dword v118,v14,s[36:39],s76 offen offset:0 sc0 sc1
s_mov_b32 s72,s69
s_mov_b32 s69,0x7fff0000
s_mov_b32 s66,0x7fff0000
s_mov_b32 s76,0x7fff0000
s_branch Lg2_poll_%=
Lg2_fin0_%=:
v_lshlrev_b32 v106,16,v122
v_and_b32 v107,0xffff0000,v122
v_lshlrev_b32 v108,16,v123
v_and_b32 v109,0xffff0000,v123
v_lshlrev_b32 v110,16,v124
v_and_b32 v111,0xffff0000,v124
v_lshlrev_b32 v112,16,v125
v_and_b32 v113,0xffff0000,v125
v_fma_f32 v106,v106,v99,v98
v_fma_f32 v107,v107,v99,v98
v_fma_f32 v108,v108,v99,v98
v_fma_f32 v109,v109,v99,v98
v_fma_f32 v110,v110,v99,v98
v_fma_f32 v111,v111,v99,v98
v_fma_f32 v112,v112,v99,v98
v_fma_f32 v113,v113,v99,v98
v_add_f32 v18,v18,v106
v_add_f32 v19,v19,v107
v_add_f32 v20,v20,v108
v_add_f32 v21,v21,v109
v_add_f32 v22,v22,v110
v_add_f32 v23,v23,v111
v_add_f32 v24,v24,v112
v_add_f32 v25,v25,v113
v_cvt_pk_bf16_f32 v114,v18,v19
v_cvt_pk_bf16_f32 v115,v20,v21
v_cvt_pk_bf16_f32 v116,v22,v23
v_cvt_pk_bf16_f32 v117,v24,v25
buffer_store_dwordx4 v[114:117],v0,s[36:39],s66 offen offset:0 sc0 sc1
v_add_f32 v106,v18,v19
v_fma_f32 v107,v18,v18,s87
v_fma_f32 v107,v19,v19,v107
v_add_f32 v106,v106,v20
v_fma_f32 v107,v20,v20,v107
v_add_f32 v106,v106,v21
v_fma_f32 v107,v21,v21,v107
v_add_f32 v106,v106,v22
v_fma_f32 v107,v22,v22,v107
v_add_f32 v106,v106,v23
v_fma_f32 v107,v23,v23,v107
v_add_f32 v106,v106,v24
v_fma_f32 v107,v24,v24,v107
v_add_f32 v106,v106,v25
v_fma_f32 v107,v25,v25,v107
s_nop 1
v_permlane16_swap_b32 v106,v107
v_add_f32 v106,v106,v107
v_mov_b32 v107,v106
s_nop 1
v_permlane32_swap_b32 v106,v107
v_add_f32 v106,v106,v107
ds_write_b32 v12,v106 offset:0
L 0
s_barrier
R 118,13,0
L 0
v_add_f32 v118,v118,v119
v_add_f32 v120,v120,v121
v_add_f32 v118,v118,v120
v_mul_f32 v118,s86,v118
buffer_store_dword v118,v14,s[36:39],s76 offen offset:0 sc0 sc1
s_mov_b32 s72,s69
s_mov_b32 s69,0x7fff0000
s_mov_b32 s66,0x7fff0000
s_mov_b32 s76,0x7fff0000
s_branch Lg2_done_%=
Lg2_K1_%=:
Lg2_L1_%=:
v_mfma_f32_16x16x32_bf16 v[30:33],a[4:7],v[34:37],0
s_add_u32 m0,s59,3072
R 54,2,5120
v_lshlrev_b32 v106,16,v122
M 30,a[12:15],38
buffer_load_dword v8,s[36:39],s58 offen offset:0 sc1 lds
v_mfma_f32_16x16x32_bf16 v[26:29],a[0:3],v[34:37],0
s_mov_b32 s67,s65
R 58,2,6144
v_and_b32 v107,0xffff0000,v122
M 26,a[8:11],38
L 3
v_lshlrev_b32 v108,16,v123
v_and_b32 v109,0xffff0000,v123
M 26,a[16:19],42
s_mov_b32 s77,s75
R 62,2,7168
v_lshlrev_b32 v110,16,v124
M 26,a[24:27],46
s_add_u32 s65,s65,s62
v_and_b32 v111,0xffff0000,v124
v_lshlrev_b32 v112,16,v125
M 30,a[20:23],42
s_add_u32 s75,s75,s63
R 66,2,8192
v_and_b32 v113,0xffff0000,v125
v_fma_f32 v106,v106,v99,v98
M 30,a[28:31],46
L 3
v_fma_f32 v107,v107,v99,v98
v_fma_f32 v108,v108,v99,v98
M 30,a[36:39],50
R 70,2,9216
v_fma_f32 v109,v109,v99,v98
M 30,a[44:47],54
v_fma_f32 v110,v110,v99,v98
v_fma_f32 v111,v111,v99,v98
M 26,a[32:35],50
R 74,2,10240
v_fma_f32 v112,v112,v99,v98
M 26,a[40:43],54
L 3
v_fma_f32 v113,v113,v99,v98
v_add_f32 v18,v18,v106
M 26,a[48:51],58
R 78,2,11264
v_add_f32 v19,v19,v107
M 26,a[56:59],62
v_add_f32 v20,v20,v108
v_add_f32 v21,v21,v109
v_add_f32 v22,v22,v110
M 30,a[52:55],58
R 82,2,12288
v_add_f32 v23,v23,v111
M 30,a[60:63],62
L 3
v_add_f32 v24,v24,v112
v_add_f32 v25,v25,v113
M 30,a[68:71],66
R 86,2,13312
v_cvt_pk_bf16_f32 v114,v18,v19
M 30,a[76:79],70
buffer_load_dwordx4 v[126:129],v0,s[36:39],s74 offen offset:0 sc1
M 26,a[64:67],66
s_add_u32 s74,s74,s62
R 90,2,14336
v_cvt_pk_bf16_f32 v115,v20,v21
M 26,a[72:75],70
L 3
buffer_load_dwordx2 v[102:103],v11,s[36:39],s73 offen offset:8 sc1
v_cvt_pk_bf16_f32 v116,v22,v23
M 26,a[80:83],74
s_waitcnt vmcnt(2)
s_add_u32 s73,s73,s64
R 94,2,15360
M 26,a[88:91],78
s_barrier
ds_read_b32 v7,v6 offset:3072
v_cvt_pk_bf16_f32 v117,v24,v25
M 30,a[84:87],74
R 34,2,16384
v_add_f32 v106,v18,v19
M 30,a[92:95],78
L 4
buffer_store_dword v4,v3,s[36:39],s70 offen offset:0 sc0 sc1
M 30,a[100:103],82
s_mov_b32 s70,0x7fff0000
R 38,2,17408
v_fma_f32 v107,v18,v18,s87
M 30,a[108:111],86
buffer_store_dwordx4 v[114:117],v0,s[36:39],s66 offen offset:0 sc0 sc1
v_fma_f32 v107,v19,v19,v107
)"
R"(M 26,a[96:99],82
R 42,2,18432
v_add_f32 v106,v106,v20
v_fma_f32 v107,v20,v20,v107
M 26,a[104:107],86
L 4
L 3
v_cmp_eq_u32 vcc,s53,v7
M 26,a[112:115],90
R 46,2,19456
v_add_f32 v106,v106,v21
M 26,a[120:123],94
s_or_b64 vcc,vcc,s[94:95]
v_fma_f32 v107,v21,v21,v107
v_add_f32 v106,v106,v22
M 30,a[116:119],90
R 50,2,20480
v_fma_f32 v107,v22,v22,v107
M 30,a[124:127],94
s_cmp_eq_u32 vcc_lo,-1
L 3
v_add_f32 v106,v106,v23
s_cbranch_scc0 Lg2_X1_%=
M 30,a[132:135],34
s_and_b32 s78,s54,127
s_cmp_eq_u32 s78,s85
s_cselect_b64 s[40:41],-1,0
R 54,2,21504
M 30,a[140:143],38
s_add_u32 m0,s57,0
s_add_u32 s56,s55,0
s_add_u32 s58,s58,s61
v_cndmask_b32 v17,v15,v16,s[40:41]
M 26,a[128:131],34
s_add_u32 s54,s54,s46
R 58,2,22528
v_fma_f32 v107,v23,v23,v107
M 26,a[136:139],38
L 3
D 17,56,0
M 26,a[144:147],42
s_add_u32 s56,s55,256
R 62,2,23552
v_add_f32 v106,v106,v24
M 26,a[152:155],46
s_and_b32 s79,s54,127
v_fma_f32 v107,v24,v24,v107
v_add_f32 v106,v106,v25
M 30,a[148:151],42
s_cmp_eq_u32 s79,0
R 66,2,24576
v_fma_f32 v107,v25,v25,v107
M 30,a[156:159],46
L 3
D 17,56,1024
M 30,a[164:167],50
s_cselect_b32 s94,0x3300,0
s_add_u32 s56,s55,512
R 70,2,25600
M 30,a[172:175],54
s_cmp_eq_u32 s79,127
v_permlane16_swap_b32 v106,v107
v_add_f32 v106,v106,v107
M 26,a[160:163],50
s_cselect_b32 s94,0xcc00,s94
R 74,2,26624
v_mov_b32 v107,v106
M 26,a[168:171],54
L 3
D 17,56,2048
M 26,a[176:179],58
s_add_u32 s56,s55,768
R 78,2,27648
M 26,a[184:187],62
v_permlane32_swap_b32 v106,v107
v_add_f32 v106,v106,v107
M 30,a[180:183],58
R 82,2,28672
ds_write_b32 v12,v106 offset:0
M 30,a[188:191],62
L 4
D 17,56,3072
M 30,a[196:199],66
s_add_u32 m0,s59,1024
s_add_u32 s56,s55,20480
R 86,2,29696
M 30,a[204:207],70
buffer_load_dword v8,s[36:39],s58 offen offset:0 sc1 lds
M 26,a[192:195],66
s_add_u32 m0,s57,16384
R 90,2,30720
v_cndmask_b32 v17,v10,v9,s[40:41]
M 26,a[200:203],70
L 4
L 2
s_sub_u32 s57,s84,s57
M 26,a[208:211],74
s_waitcnt vmcnt(0)
R 94,2,31744
M 26,a[216:219],78
s_barrier
D 17,56,0
M 30,a[212:215],74
s_add_u32 s56,s55,20736
R 34,1,0
ds_read_b32 v7,v6 offset:1024
M 30,a[220:223],78
L 4
buffer_store_dword v4,v3,s[36:39],s70 offen offset:0 sc0 sc1
M 30,a[228:231],82
s_mov_b32 s70,s69
R 38,1,1024
R 118,13,0
M 30,a[236:239],86
D 17,56,1024
M 26,a[224:227],82
s_add_u32 s56,s55,20992
s_mov_b32 s69,s68
R 42,1,2048
M 26,a[232:235],86
L 5
D 17,56,2048
M 26,a[240:243],90
L 3
s_add_u32 s56,s55,21248
R 46,1,3072
v_cmp_eq_u32 vcc,s53,v7
M 26,a[248:251],94
D 17,56,3072
M 30,a[244:247],90
s_add_u32 s55,s55,s60
s_add_u32 s68,s68,s63
L 2
s_or_b64 vcc,vcc,s[94:95]
s_cmp_eq_u32 vcc_lo,-1
R 50,1,4096
v_add_f32 v118,v118,v119
v_add_f32 v120,v120,v121
v_add_f32 v118,v118,v120
v_mul_f32 v118,s86,v118
M 30,a[252:255],94
s_nop 0
buffer_store_dword v118,v14,s[36:39],s76 offen offset:0 sc0 sc1
buffer_store_dword v4,v3,s[36:39],s71 offen offset:0 sc0 sc1
buffer_store_dword v4,v3,s[36:39],s71 offen offset:0 sc0 sc1
s_cbranch_scc1 Lg2_F0_%=
s_branch Lg2_K0_%=
Lg2_X1_%=:
M 30,a[132:135],34
s_add_u32 m0,s59,1024
R 54,2,21504
v_fma_f32 v107,v23,v23,v107
M 30,a[140:143],38
buffer_load_dword v8,s[36:39],s58 offen offset:0 sc1 lds
M 26,a[128:131],34
R 58,2,22528
v_add_f32 v106,v106,v24
M 26,a[136:139],38
L 3
v_fma_f32 v107,v24,v24,v107
v_add_f32 v106,v106,v25
M 26,a[144:147],42
R 62,2,23552
v_fma_f32 v107,v25,v25,v107
M 26,a[152:155],46
M 30,a[148:151],42
R 66,2,24576
M 30,a[156:159],46
L 3
v_permlane16_swap_b32 v106,v107
v_add_f32 v106,v106,v107
M 30,a[164:167],50
R 70,2,25600
v_mov_b32 v107,v106
M 30,a[172:175],54
M 26,a[160:163],50
R 74,2,26624
M 26,a[168:171],54
L 3
v_permlane32_swap_b32 v106,v107
M 26,a[176:179],58
R 78,2,27648
v_add_f32 v106,v106,v107
M 26,a[184:187],62
ds_write_b32 v12,v106 offset:0
M 30,a[180:183],58
R 82,2,28672
M 30,a[188:191],62
L 4
M 30,a[196:199],66
R 86,2,29696
M 30,a[204:207],70
M 26,a[192:195],66
R 90,2,30720
M 26,a[200:203],70
L 4
L 3
M 26,a[208:211],74
s_waitcnt vmcnt(0)
R 94,2,31744
M 26,a[216:219],78
s_barrier
R 118,13,0
ds_read_b32 v7,v6 offset:1024
M 30,a[212:215],74
buffer_store_dword v4,v3,s[36:39],s70 offen offset:0 sc0 sc1
M 30,a[220:223],78
L 4
s_mov_b32 s70,s69
s_mov_b32 s69,s68
M 30,a[228:231],82
s_add_u32 s68,s68,s63
M 30,a[236:239],86
M 26,a[224:227],82
M 26,a[232:235],86
L 2
L 1
L 0
M 26,a[240:243],90
v_add_f32 v118,v118,v119
v_add_f32 v120,v120,v121
v_add_f32 v118,v118,v120
M 26,a[248:251],94
v_cmp_eq_u32 vcc,s53,v7
v_mul_f32 v118,s86,v118
M 30,a[244:247],90
buffer_store_dword v118,v14,s[36:39],s76 offen offset:0 sc0 sc1
M 30,a[252:255],94
s_nop 0
s_or_b64 vcc,vcc,s[94:95]
s_mov_b32 s80,0
s_cmp_eq_u32 vcc_lo,-1
s_cbranch_scc0 Lg2_xn1_%=
s_and_b32 s78,s54,127
s_cmp_eq_u32 s78,s85
s_cselect_b64 s[40:41],-1,0
v_cndmask_b32 v17,v15,v16,s[40:41]
s_add_u32 m0,s57,0
s_add_u32 s56,s55,0
D 17,56,0
v_lshlrev_b32 v106,16,v126
v_and_b32 v107,0xffff0000,v126
v_lshlrev_b32 v108,16,v127
v_and_b32 v109,0xffff0000,v127
v_lshlrev_b32 v110,16,v128
s_add_u32 s56,s55,256
D 17,56,1024
v_and_b32 v111,0xffff0000,v128
v_lshlrev_b32 v112,16,v129
v_and_b32 v113,0xffff0000,v129
v_fma_f32 v106,v106,v103,v102
v_fma_f32 v107,v107,v103,v102
s_add_u32 s56,s55,512
D 17,56,2048
v_fma_f32 v108,v108,v103,v102
v_fma_f32 v109,v109,v103,v102
v_fma_f32 v110,v110,v103,v102
v_fma_f32 v111,v111,v103,v102
v_fma_f32 v112,v112,v103,v102
s_add_u32 s56,s55,768
D 17,56,3072
v_fma_f32 v113,v113,v103,v102
v_add_f32 v26,v26,v106
v_add_f32 v27,v27,v107
v_add_f32 v28,v28,v108
v_add_f32 v29,v29,v109
s_add_u32 s58,s58,s61
v_cndmask_b32 v17,v10,v9,s[40:41]
s_add_u32 m0,s57,16384
s_add_u32 s56,s55,20480
D 17,56,0
v_add_f32 v30,v30,v110
v_add_f32 v31,v31,v111
v_add_f32 v32,v32,v112
v_add_f32 v33,v33,v113
v_cvt_pk_bf16_f32 v114,v26,v27
s_add_u32 s56,s55,20736
D 17,56,1024
v_cvt_pk_bf16_f32 v115,v28,v29
v_cvt_pk_bf16_f32 v116,v30,v31
v_cvt_pk_bf16_f32 v117,v32,v33
s_add_u32 s56,s55,20992
D 17,56,2048
s_add_u32 s56,s55,21248
D 17,56,3072
buffer_store_dwordx4 v[114:117],v0,s[36:39],s67 offen offset:0 sc0 sc1
v_add_f32 v106,v26,v27
v_fma_f32 v107,v26,v26,s87
s_add_u32 s55,s55,s60
s_sub_u32 s57,s84,s57
s_add_u32 s54,s54,s46
v_fma_f32 v107,v27,v27,v107
v_add_f32 v106,v106,v28
v_fma_f32 v107,v28,v28,v107
v_add_f32 v106,v106,v29
v_fma_f32 v107,v29,v29,v107
v_add_f32 v106,v106,v30
v_fma_f32 v107,v30,v30,v107
v_add_f32 v106,v106,v31
v_fma_f32 v107,v31,v31,v107
v_add_f32 v106,v106,v32
v_fma_f32 v107,v32,v32,v107
v_add_f32 v106,v106,v33
v_fma_f32 v107,v33,v33,v107
s_nop 1
v_permlane16_swap_b32 v106,v107
v_add_f32 v106,v106,v107
v_mov_b32 v107,v106
s_nop 1
v_permlane32_swap_b32 v106,v107
v_add_f32 v106,v106,v107
ds_write_b32 v12,v106 offset:2048
L 0
s_barrier
R 118,13,2048
L 0
v_add_f32 v118,v118,v119
v_add_f32 v120,v120,v121
v_add_f32 v118,v118,v120
v_mul_f32 v118,s86,v118
buffer_store_dword v118,v14,s[36:39],s77 offen offset:0 sc0 sc1
s_mov_b32 s72,s69
s_mov_b32 s69,0x7fff0000
s_mov_b32 s67,0x7fff0000
s_mov_b32 s77,0x7fff0000
s_branch Lg2_gox_%=
Lg2_xn1_%=:
s_cmp_ge_u32 s54,256
s_cbranch_scc1 Lg2_fin1_%=
s_add_u32 m0,s59,4096
s_nop 0
buffer_load_dword v8,s[36:39],s58 offen offset:0 sc1 lds
buffer_store_dword v4,v3,s[36:39],s71 offen offset:0 sc0 sc1
s_add_u32 m0,s59,5120
s_nop 0
buffer_load_dword v8,s[36:39],s58 offen offset:0 sc1 lds
buffer_store_dword v4,v3,s[36:39],s71 offen offset:0 sc0 sc1
s_mov_b32 s83,0
v_lshlrev_b32 v106,16,v126
v_and_b32 v107,0xffff0000,v126
v_lshlrev_b32 v108,16,v127
v_and_b32 v109,0xffff0000,v127
v_lshlrev_b32 v110,16,v128
v_and_b32 v111,0xffff0000,v128
v_lshlrev_b32 v112,16,v129
v_and_b32 v113,0xffff0000,v129
v_fma_f32 v106,v106,v103,v102
v_fma_f32 v107,v107,v103,v102
v_fma_f32 v108,v108,v103,v102
v_fma_f32 v109,v109,v103,v102
v_fma_f32 v110,v110,v103,v102
v_fma_f32 v111,v111,v103,v102
v_fma_f32 v112,v112,v103,v102
v_fma_f32 v113,v113,v103,v102
v_add_f32 v26,v26,v106
v_add_f32 v27,v27,v107
v_add_f32 v28,v28,v108
v_add_f32 v29,v29,v109
v_add_f32 v30,v30,v110
v_add_f32 v31,v31,v111
v_add_f32 v32,v32,v112
v_add_f32 v33,v33,v113
v_cvt_pk_bf16_f32 v114,v26,v27
v_cvt_pk_bf16_f32 v115,v28,v29
v_cvt_pk_bf16_f32 v116,v30,v31
v_cvt_pk_bf16_f32 v117,v32,v33
buffer_store_dwordx4 v[114:117],v0,s[36:39],s67 offen offset:0 sc0 sc1
v_add_f32 v106,v26,v27
v_fma_f32 v107,v26,v26,s87
v_fma_f32 v107,v27,v27,v107
v_add_f32 v106,v106,v28
v_fma_f32 v107,v28,v28,v107
v_add_f32 v106,v106,v29
v_fma_f32 v107,v29,v29,v107
v_add_f32 v106,v106,v30
v_fma_f32 v107,v30,v30,v107
v_add_f32 v106,v106,v31
v_fma_f32 v107,v31,v31,v107
v_add_f32 v106,v106,v32
v_fma_f32 v107,v32,v32,v107
v_add_f32 v106,v106,v33
v_fma_f32 v107,v33,v33,v107
s_nop 1
v_permlane16_swap_b32 v106,v107
v_add_f32 v106,v106,v107
v_mov_b32 v107,v106
s_nop 1
v_permlane32_swap_b32 v106,v107
v_add_f32 v106,v106,v107
ds_write_b32 v12,v106 offset:2048
L 0
s_barrier
R 118,13,2048
L 0
v_add_f32 v118,v118,v119
v_add_f32 v120,v120,v121
v_add_f32 v118,v118,v120
v_mul_f32 v118,s86,v118
buffer_store_dword v118,v14,s[36:39],s77 offen offset:0 sc0 sc1
s_mov_b32 s72,s69
s_mov_b32 s69,0x7fff0000
s_mov_b32 s67,0x7fff0000
s_mov_b32 s77,0x7fff0000
s_branch Lg2_poll_%=
Lg2_fin1_%=:
v_lshlrev_b32 v106,16,v126
v_and_b32 v107,0xffff0000,v126
v_lshlrev_b32 v108,16,v127
v_and_b32 v109,0xffff0000,v127
v_lshlrev_b32 v110,16,v128
v_and_b32 v111,0xffff0000,v128
v_lshlrev_b32 v112,16,v129
v_and_b32 v113,0xffff0000,v129
v_fma_f32 v106,v106,v103,v102
v_fma_f32 v107,v107,v103,v102
v_fma_f32 v108,v108,v103,v102
v_fma_f32 v109,v109,v103,v102
v_fma_f32 v110,v110,v103,v102
v_fma_f32 v111,v111,v103,v102
v_fma_f32 v112,v112,v103,v102
v_fma_f32 v113,v113,v103,v102
v_add_f32 v26,v26,v106
v_add_f32 v27,v27,v107
v_add_f32 v28,v28,v108
v_add_f32 v29,v29,v109
v_add_f32 v30,v30,v110
v_add_f32 v31,v31,v111
v_add_f32 v32,v32,v112
v_add_f32 v33,v33,v113
v_cvt_pk_bf16_f32 v114,v26,v27
v_cvt_pk_bf16_f32 v115,v28,v29
v_cvt_pk_bf16_f32 v116,v30,v31
v_cvt_pk_bf16_f32 v117,v32,v33
buffer_store_dwordx4 v[114:117],v0,s[36:39],s67 offen offset:0 sc0 sc1
v_add_f32 v106,v26,v27
v_fma_f32 v107,v26,v26,s87
v_fma_f32 v107,v27,v27,v107
v_add_f32 v106,v106,v28
v_fma_f32 v107,v28,v28,v107
v_add_f32 v106,v106,v29
v_fma_f32 v107,v29,v29,v107
v_add_f32 v106,v106,v30
v_fma_f32 v107,v30,v30,v107
v_add_f32 v106,v106,v31
v_fma_f32 v107,v31,v31,v107
v_add_f32 v106,v106,v32
v_fma_f32 v107,v32,v32,v107
v_add_f32 v106,v106,v33
v_fma_f32 v107,v33,v33,v107
s_nop 1
v_permlane16_swap_b32 v106,v107
v_add_f32 v106,v106,v107
v_mov_b32 v107,v106
s_nop 1
v_permlane32_swap_b32 v106,v107
v_add_f32 v106,v106,v107
ds_write_b32 v12,v106 offset:2048
L 0
s_barrier
R 118,13,2048
L 0
v_add_f32 v118,v118,v119
v_add_f32 v120,v120,v121
v_add_f32 v118,v118,v120
v_mul_f32 v118,s86,v118
buffer_store_dword v118,v14,s[36:39],s77 offen offset:0 sc0 sc1
s_mov_b32 s72,s69
s_mov_b32 s69,0x7fff0000
s_mov_b32 s67,0x7fff0000
s_mov_b32 s77,0x7fff0000
s_branch Lg2_done_%=
Lg2_done_%=:
s_waitcnt vmcnt(2)
s_barrier
buffer_store_dword v4,v3,s[36:39],s70 offen offset:0 sc0 sc1
s_waitcnt vmcnt(1)
s_barrier
buffer_store_dword v4,v3,s[36:39],s72 offen offset:0 sc0 sc1
s_waitcnt vmcnt(0) lgkmcnt(0)
s_barrier
Lg2_end_%=:
s_mov_b32 %0,s53
Lcode_g12_e:
.purgem M
.purgem R
.purgem T
.purgem D
.purgem L
)"
  : "=&s"(epOut) : "s"(ws), "s"(__builtin_amdgcn_readfirstlane(sWave)), "s"(__builtin_amdgcn_readfirstlane(sG0)), "s"(__builtin_amdgcn_readfirstlane(sStep)), "s"(__builtin_amdgcn_readfirstlane(sOutOff)), "s"(__builtin_amdgcn_readfirstlane(sFoutOff)), "s"(__builtin_amdgcn_readfirstlane(sAuxOff)), "s"(__builtin_amdgcn_readfirstlane(sWw)), "s"(__builtin_amdgcn_readfirstlane(sBuf)), "s"(__builtin_amdgcn_readfirstlane(sExp)), "v"(vL16), "v"(vFlag), "v"(vSnapOff), "v"(vSnapRd), "v"(vWd0), "v"(vWd1), "v"(vWr0), "v"(vWr1), "v"(vSp), "v"(vOutS), "v"(vAux0), "v"(vAux1), "v"(vAux2), "v"(vAux3), "v"(vAn), "v"(vAe), "s"(w12) : "v0", "v1", "v2", "v3", "v4", "v5", "v6", "v7", "v8", "v9", "v10", "v11", "v12", "v13", "v14", "v15", "v16", "v17", "v18", "v19", "v20", "v21", "v22", "v23", "v24", "v25", "v26", "v27", "v28", "v29", "v30", "v31", "v32", "v33", "v34", "v35", "v36", "v37", "v38", "v39", "v40", "v41", "v42", "v43", "v44", "v45", "v46", "v47", "v48", "v49", "v50", "v51", "v52", "v53", "v54", "v55", "v56", "v57", "v58", "v59", "v60", "v61", "v62", "v63", "v64", "v65", "v66", "v67", "v68", "v69", "v70", "v71", "v72", "v73", "v74", "v75", "v76", "v77", "v78", "v79", "v80", "v81", "v82", "v83", "v84", "v85", "v86", "v87", "v88", "v89", "v90", "v91", "v92", "v93", "v94", "v95", "v96", "v97", "v98", "v99", "v100", "v101", "v102", "v103", "v104", "v105", "v106", "v107", "v108", "v109", "v110", "v111", "v112", "v113", "v114", "v115", "v116", "v117", "v118", "v119", "v120", "v121", "v122", "v123", "v124", "v125", "v126", "v127", "v128", "v129", "v130", "v131", "v132", "v133", "v134", "v135", "v136", "v137", "v138", "v139", "v140", "v141", "v142", "v143", "v144", "v145", "v146", "v147", "v148", "v149", "v150", "v151", "v152", "v153", "v154", "v155", "v156", "v157", "v158", "v159", "v160", "v161", "v162", "v163", "v164", "v165", "v166", "v167", "v168", "v169", "v170", "v171", "v172", "v173", "v174", "v175", "v176", "v177", "v178", "v179", "v180", "v181", "v182", "v183", "v184", "v185", "v186", "v187", "v188", "v189", "v190", "v191", "v192", "v193", "v194", "v195", "v196", "v197", "v198", "v199", "v200", "v201", "v202", "v203", "v204", "v205", "v206", "v207", "v208", "v209", "v210", "v211", "v212", "v213", "v214", "v215", "v216", "v217", "v218", "v219", "v220", "v221", "v222", "v223", "v224", "v225", "v226", "v227", "v228", "v229", "v230", "v231", "a0", "a1", "a2", "a3", "a4", "a5", "a6", "a7", "a8", "a9", "a10", "a11", "a12", "a13", "a14", "a15", "a16", "a17", "a18", "a19", "a20", "a21", "a22", "a23", "a24", "a25", "a26", "a27", "a28", "a29", "a30", "a31", "a32", "a33", "a34", "a35", "a36", "a37", "a38", "a39", "a40", "a41", "a42", "a43", "a44", "a45", "a46", "a47", "a48", "a49", "a50", "a51", "a52", "a53", "a54", "a55", "a56", "a57", "a58", "a59", "a60", "a61", "a62", "a63", "a64", "a65", "a66", "a67", "a68", "a69", "a70", "a71", "a72", "a73", "a74", "a75", "a76", "a77", "a78", "a79", "a80", "a81", "a82", "a83", "a84", "a85", "a86", "a87", "a88", "a89", "a90", "a91", "a92", "a93", "a94", "a95", "a96", "a97", "a98", "a99", "a100", "a101", "a102", "a103", "a104", "a105", "a106", "a107", "a108", "a109", "a110", "a111", "a112", "a113", "a114", "a115", "a116", "a117", "a118", "a119", "a120", "a121", "a122", "a123", "a124", "a125", "a126", "a127", "a128", "a129", "a130", "a131", "a132", "a133", "a134", "a135", "a136", "a137", "a138", "a139", "a140", "a141", "a142", "a143", "a144", "a145", "a146", "a147", "a148", "a149", "a150", "a151", "a152", "a153", "a154", "a155", "a156", "a157", "a158", "a159", "a160", "a161", "a162", "a163", "a164", "a165", "a166", "a167", "a168", "a169", "a170", "a171", "a172", "a173", "a174", "a175", "a176", "a177", "a178", "a179", "a180", "a181", "a182", "a183", "a184", "a185", "a186", "a187", "a188", "a189", "a190", "a191", "a192", "a193", "a194", "a195", "a196", "a197", "a198", "a199", "a200", "a201", "a202", "a203", "a204", "a205", "a206", "a207", "a208", "a209", "a210", "a211", "a212", "a213", "a214", "a215", "a216", "a217", "a218", "a219", "a220", "a221", "a222", "a223", "a224", "a225", "a226", "a227", "a228", "a229", "a230", "a231", "a232", "a233", "a234", "a235", "a236", "a237", "a238", "a239", "a240", "a241", "a242", "a243", "a244", "a245", "a246", "a247", "a248", "a249", "a250", "a251", "a252", "a253", "a254", "a255", "s36", "s37", "s38", "s39", "s40", "s41", "s42", "s43", "s44", "s45", "s46", "s47", "s48", "s49", "s50", "s51", "s52", "s53", "s54", "s55", "s56", "s57", "s58", "s59", "s60", "s61", "s62", "s63", "s64", "s65", "s66", "s67", "s68", "s69", "s70", "s71", "s72", "s73", "s74", "s75", "s76", "s77", "s78", "s79", "s80", "s81", "s82", "s83", "s84", "s85", "s86", "s87", "s88", "s89", "s90", "s91", "s92", "s93", "s94", "s95", "s96", "s97", "vcc", "scc", "memory");
    ep = epOut;
    if (kind == 1 && 5 + RHELP + (rep >> 1) < NR4) { kind = 4; kh = rep & 1; rep = 5 + RHELP + (rep >> 1); }
  }
  if (kind == 4) {
    const unsigned sStep = rep < 5 ? 5 : rep == 5 ? 6 : NR4;
    COMMON(10, (kh * 9 + fj) * FSG, 2560, 143360, 2048, 448, 0);
    const int cs = slice;
    const unsigned sOutOff = (kh ? OFF_P1 : OFF_P0) + (cs * 4 + wave) * 1024, sFoutOff = OFF_FP + (kh * 8 + cs) * FSP;
    const unsigned sAuxOff = (kh ? OFF_XP : OFF_X1) + (cs * 4 + wave) * 1024, sWw = (kh * 1792 + 16 * wave) * 2048 + cs * 256, sBuf = kh * 57344;
    const int zu = 108 + (rep + 2) % 5;
    const unsigned vSp = lg * 2048 + l15 * 4, vSn = rep >= 5 ? OOB : !ALLZ ? (zu * 16 + wave * 4 + lg) * 2048 + (cs * 2 + kh) * 128 + l15 * 8 :
                         ((rep + 5 * (wave >> 1)) * 16 + (wave & 1) * 8 + (ln >> 3)) * 2048 + (cs * 2 + kh) * 128 + (ln & 7) * 16;
    (void)vWr2;
    const unsigned sExp = __builtin_amdgcn_readfirstlane(ep);
    asm volatile(
R"(.macro M d,w,x
v_mfma_f32_16x16x32_bf16 v[\d:\d+3], \w, v[\x:\x+3], v[\d:\d+3]
.endm
.macro R d,a,o
ds_read_b128 v[\d:\d+3], v\a offset:\o
.endm
.macro T d,a,o
ds_read_b64_tr_b16 \d, v\a offset:\o
.endm
.macro D a,s,o
buffer_load_dwordx4 v\a, s[36:39], s\s offen offset:\o sc1 lds
.endm
.macro L c
s_waitcnt lgkmcnt(\c)
.endm
Lcode_g4_b:
s_mov_b64 s[36:37],%0
s_mov_b32 s38,111243392
s_mov_b32 s39,0x00020000
s_mov_b32 s44,%1
s_mov_b32 s45,%2
s_mov_b32 s46,%3
s_mov_b32 s47,%4
s_mov_b32 s48,%5
s_mov_b32 s49,%6
s_mov_b32 s50,%7
s_mov_b32 s51,%8
s_mov_b32 s93,%9
s_mov_b32 s53,%10
v_mov_b32 v0,%11
v_mov_b32 v3,%12
v_mov_b32 v5,%13
v_mov_b32 v6,%14
v_mov_b32 v10,%15
v_mov_b32 v11,%16
v_mov_b32 v12,%17
v_mov_b32 v13,%18
v_mov_b32 v9,%19
v_mov_b32 v7,%20
s_mov_b64 s[40:41],%21
s_mov_b32 s42,7340032
s_mov_b32 s43,0x00020000
s_lshl_b32 s57,s44,11
s_cmp_eq_u32 s45,175
s_cbranch_scc1 Lg4_pre_%=
s_add_u32 s55,s50,0
s_add_u32 m0,s57,40960
s_nop 0
buffer_load_dwordx4 v10,s[40:43],s55 offen offset:0  lds
s_add_u32 m0,s57,41984
s_nop 0
buffer_load_dwordx4 v11,s[40:43],s55 offen offset:0  lds
s_add_u32 m0,s57,49024
s_nop 0
buffer_load_dwordx4 v10,s[40:43],s55 offen offset:128  lds
s_add_u32 m0,s57,50048
s_nop 0
buffer_load_dwordx4 v11,s[40:43],s55 offen offset:128  lds
s_add_u32 s55,s50,131072
s_add_u32 m0,s57,57344
s_nop 0
buffer_load_dwordx4 v10,s[40:43],s55 offen offset:0  lds
s_add_u32 m0,s57,58368
s_nop 0
buffer_load_dwordx4 v11,s[40:43],s55 offen offset:0  lds
s_add_u32 m0,s57,65408
s_nop 0
buffer_load_dwordx4 v10,s[40:43],s55 offen offset:128  lds
s_add_u32 m0,s57,66432
s_nop 0
buffer_load_dwordx4 v11,s[40:43],s55 offen offset:128  lds
s_add_u32 s55,s50,262144
s_add_u32 m0,s57,73728
s_nop 0
buffer_load_dwordx4 v10,s[40:43],s55 offen offset:0  lds
s_add_u32 m0,s57,74752
s_nop 0
buffer_load_dwordx4 v11,s[40:43],s55 offen offset:0  lds
s_add_u32 m0,s57,81792
s_nop 0
buffer_load_dwordx4 v10,s[40:43],s55 offen offset:128  lds
s_add_u32 m0,s57,82816
s_nop 0
buffer_load_dwordx4 v11,s[40:43],s55 offen offset:128  lds
s_add_u32 s55,s50,393216
s_add_u32 m0,s57,90112
s_nop 0
buffer_load_dwordx4 v10,s[40:43],s55 offen offset:0  lds
s_add_u32 m0,s57,91136
s_nop 0
buffer_load_dwordx4 v11,s[40:43],s55 offen offset:0  lds
s_add_u32 m0,s57,98176
s_nop 0
buffer_load_dwordx4 v10,s[40:43],s55 offen offset:128  lds
s_add_u32 m0,s57,99200
s_nop 0
buffer_load_dwordx4 v11,s[40:43],s55 offen offset:128  lds
s_add_u32 s55,s50,524288
s_add_u32 m0,s57,106496
s_nop 0
buffer_load_dwordx4 v10,s[40:43],s55 offen offset:0  lds
s_add_u32 m0,s57,107520
s_nop 0
buffer_load_dwordx4 v11,s[40:43],s55 offen offset:0  lds
s_add_u32 m0,s57,114560
s_nop 0
buffer_load_dwordx4 v10,s[40:43],s55 offen offset:128  lds
s_add_u32 m0,s57,115584
s_nop 0
buffer_load_dwordx4 v11,s[40:43],s55 offen offset:128  lds
s_add_u32 s55,s50,655360
s_add_u32 m0,s57,122880
s_nop 0
buffer_load_dwordx4 v10,s[40:43],s55 offen offset:0  lds
s_add_u32 m0,s57,123904
s_nop 0
buffer_load_dwordx4 v11,s[40:43],s55 offen offset:0  lds
s_add_u32 m0,s57,130944
s_nop 0
buffer_load_dwordx4 v10,s[40:43],s55 offen offset:128  lds
s_add_u32 m0,s57,131968
s_nop 0
buffer_load_dwordx4 v11,s[40:43],s55 offen offset:128  lds
s_waitcnt vmcnt(20) lgkmcnt(0)
s_barrier
s_add_u32 s55,s50,786432
s_add_u32 m0,s57,139264
s_nop 0
buffer_load_dwordx4 v10,s[40:43],s55 offen offset:0  lds
s_add_u32 m0,s57,140288
s_nop 0
buffer_load_dwordx4 v11,s[40:43],s55 offen offset:0  lds
s_add_u32 m0,s57,147328
s_nop 0
buffer_load_dwordx4 v10,s[40:43],s55 offen offset:128  lds
s_add_u32 m0,s57,148352
s_nop 0
buffer_load_dwordx4 v11,s[40:43],s55 offen offset:128  lds
v_add_u32 v18,40960,v12
v_add_u32 v19,40960,v13
T a[0:1],18,0
T a[2:3],18,512
T a[4:5],19,0
T a[6:7],19,512
T a[8:9],18,4096
T a[10:11],18,4608
T a[12:13],19,4096
T a[14:15],19,4608
s_waitcnt vmcnt(20) lgkmcnt(0)
s_barrier
s_add_u32 s55,s50,917504
s_add_u32 m0,s57,40960
s_nop 0
buffer_load_dwordx4 v10,s[40:43],s55 offen offset:0  lds
s_add_u32 m0,s57,41984
s_nop 0
buffer_load_dwordx4 v11,s[40:43],s55 offen offset:0  lds
s_add_u32 m0,s57,49024
s_nop 0
buffer_load_dwordx4 v10,s[40:43],s55 offen offset:128  lds
s_add_u32 m0,s57,50048
s_nop 0
buffer_load_dwordx4 v11,s[40:43],s55 offen offset:128  lds
v_add_u32 v18,57344,v12
v_add_u32 v19,57344,v13
T a[16:17],18,0
T a[18:19],18,512
T a[20:21],19,0
T a[22:23],19,512
T a[24:25],18,4096
T a[26:27],18,4608
T a[28:29],19,4096
T a[30:31],19,4608
s_waitcnt vmcnt(20) lgkmcnt(0)
s_barrier
s_add_u32 s55,s50,1048576
s_add_u32 m0,s57,57344
s_nop 0
buffer_load_dwordx4 v10,s[40:43],s55 offen offset:0  lds
s_add_u32 m0,s57,58368
s_nop 0
buffer_load_dwordx4 v11,s[40:43],s55 offen offset:0  lds
s_add_u32 m0,s57,65408
s_nop 0
buffer_load_dwordx4 v10,s[40:43],s55 offen offset:128  lds
s_add_u32 m0,s57,66432
s_nop 0
buffer_load_dwordx4 v11,s[40:43],s55 offen offset:128  lds
v_add_u32 v18,73728,v12
v_add_u32 v19,73728,v13
T a[32:33],18,0
T a[34:35],18,512
T a[36:37],19,0
T a[38:39],19,512
T a[40:41],18,4096
T a[42:43],18,4608
T a[44:45],19,4096
T a[46:47],19,4608
s_waitcnt vmcnt(20) lgkmcnt(0)
s_barrier
s_add_u32 s55,s50,1179648
s_add_u32 m0,s57,73728
s_nop 0
buffer_load_dwordx4 v10,s[40:43],s55 offen offset:0  lds
s_add_u32 m0,s57,74752
s_nop 0
buffer_load_dwordx4 v11,s[40:43],s55 offen offset:0  lds
s_add_u32 m0,s57,81792
s_nop 0
buffer_load_dwordx4 v10,s[40:43],s55 offen offset:128  lds
s_add_u32 m0,s57,82816
s_nop 0
buffer_load_dwordx4 v11,s[40:43],s55 offen offset:128  lds
v_add_u32 v18,90112,v12
v_add_u32 v19,90112,v13
T a[48:49],18,0
T a[50:51],18,512
T a[52:53],19,0
T a[54:55],19,512
T a[56:57],18,4096
T a[58:59],18,4608
T a[60:61],19,4096
T a[62:63],19,4608
s_waitcnt vmcnt(20) lgkmcnt(0)
s_barrier
s_add_u32 s55,s50,1310720
s_add_u32 m0,s57,90112
s_nop 0
buffer_load_dwordx4 v10,s[40:43],s55 offen offset:0  lds
s_add_u32 m0,s57,91136
s_nop 0
buffer_load_dwordx4 v11,s[40:43],s55 offen offset:0  lds
s_add_u32 m0,s57,98176
s_nop 0
buffer_load_dwordx4 v10,s[40:43],s55 offen offset:128  lds
s_add_u32 m0,s57,99200
s_nop 0
buffer_load_dwordx4 v11,s[40:43],s55 offen offset:128  lds
v_add_u32 v18,106496,v12
v_add_u32 v19,106496,v13
T a[64:65],18,0
T a[66:67],18,512
T a[68:69],19,0
T a[70:71],19,512
T a[72:73],18,4096
T a[74:75],18,4608
T a[76:77],19,4096
T a[78:79],19,4608
s_waitcnt vmcnt(20) lgkmcnt(0)
s_barrier
s_add_u32 s55,s50,1441792
s_add_u32 m0,s57,106496
s_nop 0
buffer_load_dwordx4 v10,s[40:43],s55 offen offset:0  lds
s_add_u32 m0,s57,107520
s_nop 0
buffer_load_dwordx4 v11,s[40:43],s55 offen offset:0  lds
s_add_u32 m0,s57,114560
s_nop 0
buffer_load_dwordx4 v10,s[40:43],s55 offen offset:128  lds
s_add_u32 m0,s57,115584
s_nop 0
buffer_load_dwordx4 v11,s[40:43],s55 offen offset:128  lds
v_add_u32 v18,122880,v12
v_add_u32 v19,122880,v13
T a[80:81],18,0
T a[82:83],18,512
T a[84:85],19,0
T a[86:87],19,512
T a[88:89],18,4096
T a[90:91],18,4608
T a[92:93],19,4096
T a[94:95],19,4608
s_waitcnt vmcnt(20) lgkmcnt(0)
s_barrier
s_add_u32 s55,s50,1572864
s_add_u32 m0,s57,122880
s_nop 0
buffer_load_dwordx4 v10,s[40:43],s55 offen offset:0  lds
s_add_u32 m0,s57,123904
s_nop 0
buffer_load_dwordx4 v11,s[40:43],s55 offen offset:0  lds
s_add_u32 m0,s57,130944
s_nop 0
buffer_load_dwordx4 v10,s[40:43],s55 offen offset:128  lds
s_add_u32 m0,s57,131968
s_nop 0
buffer_load_dwordx4 v11,s[40:43],s55 offen offset:128  lds
v_add_u32 v18,139264,v12
v_add_u32 v19,139264,v13
T a[96:97],18,0
T a[98:99],18,512
T a[100:101],19,0
T a[102:103],19,512
T a[104:105],18,4096
T a[106:107],18,4608
T a[108:109],19,4096
T a[110:111],19,4608
s_waitcnt vmcnt(20) lgkmcnt(0)
s_barrier
s_add_u32 s55,s50,1703936
s_add_u32 m0,s57,139264
s_nop 0
buffer_load_dwordx4 v10,s[40:43],s55 offen offset:0  lds
s_add_u32 m0,s57,140288
s_nop 0
buffer_load_dwordx4 v11,s[40:43],s55 offen offset:0  lds
s_add_u32 m0,s57,147328
s_nop 0
buffer_load_dwordx4 v10,s[40:43],s55 offen offset:128  lds
s_add_u32 m0,s57,148352
s_nop 0
buffer_load_dwordx4 v11,s[40:43],s55 offen offset:128  lds
v_add_u32 v18,40960,v12
v_add_u32 v19,40960,v13
T a[112:113],18,0
T a[114:115],18,512
T a[116:117],19,0
T a[118:119],19,512
T a[120:121],18,4096
T a[122:123],18,4608
T a[124:125],19,4096
T a[126:127],19,4608
s_waitcnt vmcnt(20) lgkmcnt(0)
s_barrier
s_add_u32 s55,s50,1835008
s_add_u32 m0,s57,40960
s_nop 0
buffer_load_dwordx4 v10,s[40:43],s55 offen offset:0  lds
s_add_u32 m0,s57,41984
s_nop 0
buffer_load_dwordx4 v11,s[40:43],s55 offen offset:0  lds
s_add_u32 m0,s57,49024
s_nop 0
buffer_load_dwordx4 v10,s[40:43],s55 offen offset:128  lds
s_add_u32 m0,s57,50048
s_nop 0
buffer_load_dwordx4 v11,s[40:43],s55 offen offset:128  lds
v_add_u32 v18,57344,v12
v_add_u32 v19,57344,v13
T a[128:129],18,0
T a[130:131],18,512
T a[132:133],19,0
T a[134:135],19,512
T a[136:137],18,4096
T a[138:139],18,4608
T a[140:141],19,4096
T a[142:143],19,4608
s_waitcnt vmcnt(20) lgkmcnt(0)
s_barrier
s_add_u32 s55,s50,1966080
s_add_u32 m0,s57,57344
s_nop 0
buffer_load_dwordx4 v10,s[40:43],s55 offen offset:0  lds
s_add_u32 m0,s57,58368
s_nop 0
buffer_load_dwordx4 v11,s[40:43],s55 offen offset:0  lds
s_add_u32 m0,s57,65408
s_nop 0
buffer_load_dwordx4 v10,s[40:43],s55 offen offset:128  lds
s_add_u32 m0,s57,66432
s_nop 0
buffer_load_dwordx4 v11,s[40:43],s55 offen offset:128  lds
v_add_u32 v18,73728,v12
v_add_u32 v19,73728,v13
T a[144:145],18,0
T a[146:147],18,512
T a[148:149],19,0
T a[150:151],19,512
T a[152:153],18,4096
T a[154:155],18,4608
T a[156:157],19,4096
T a[158:159],19,4608
s_waitcnt vmcnt(20) lgkmcnt(0)
s_barrier
s_add_u32 s55,s50,2097152
s_add_u32 m0,s57,73728
s_nop 0
buffer_load_dwordx4 v10,s[40:43],s55 offen offset:0  lds
s_add_u32 m0,s57,74752
s_nop 0
buffer_load_dwordx4 v11,s[40:43],s55 offen offset:0  lds
s_add_u32 m0,s57,81792
s_nop 0
buffer_load_dwordx4 v10,s[40:43],s55 offen offset:128  lds
s_add_u32 m0,s57,82816
s_nop 0
buffer_load_dwordx4 v11,s[40:43],s55 offen offset:128  lds
v_add_u32 v18,90112,v12
v_add_u32 v19,90112,v13
T a[160:161],18,0
T a[162:163],18,512
T a[164:165],19,0
T a[166:167],19,512
T a[168:169],18,4096
T a[170:171],18,4608
T a[172:173],19,4096
T a[174:175],19,4608
s_waitcnt vmcnt(20) lgkmcnt(0)
s_barrier
s_add_u32 s55,s50,2228224
s_add_u32 m0,s57,90112
s_nop 0
buffer_load_dwordx4 v10,s[40:43],s55 offen offset:0  lds
s_add_u32 m0,s57,91136
s_nop 0
buffer_load_dwordx4 v11,s[40:43],s55 offen offset:0  lds
s_add_u32 m0,s57,98176
s_nop 0
buffer_load_dwordx4 v10,s[40:43],s55 offen offset:128  lds
s_add_u32 m0,s57,99200
s_nop 0
buffer_load_dwordx4 v11,s[40:43],s55 offen offset:128  lds
v_add_u32 v18,106496,v12
v_add_u32 v19,106496,v13
T a[176:177],18,0
T a[178:179],18,512
T a[180:181],19,0
T a[182:183],19,512
T a[184:185],18,4096
T a[186:187],18,4608
T a[188:189],19,4096
T a[190:191],19,4608
s_waitcnt vmcnt(20) lgkmcnt(0)
s_barrier
s_add_u32 s55,s50,2359296
s_add_u32 m0,s57,106496
s_nop 0
buffer_load_dwordx4 v10,s[40:43],s55 offen offset:0  lds
s_add_u32 m0,s57,107520
s_nop 0
buffer_load_dwordx4 v11,s[40:43],s55 offen offset:0  lds
s_add_u32 m0,s57,114560
s_nop 0
buffer_load_dwordx4 v10,s[40:43],s55 offen offset:128  lds
s_add_u32 m0,s57,115584
s_nop 0
buffer_load_dwordx4 v11,s[40:43],s55 offen offset:128  lds
v_add_u32 v18,122880,v12
v_add_u32 v19,122880,v13
T a[192:193],18,0
T a[194:195],18,512
T a[196:197],19,0
T a[198:199],19,512
T a[200:201],18,4096
T a[202:203],18,4608
T a[204:205],19,4096
T a[206:207],19,4608
s_waitcnt vmcnt(20) lgkmcnt(0)
s_barrier
s_add_u32 s55,s50,2490368
s_add_u32 m0,s57,122880
s_nop 0
buffer_load_dwordx4 v10,s[40:43],s55 offen offset:0  lds
s_add_u32 m0,s57,123904
s_nop 0
buffer_load_dwordx4 v11,s[40:43],s55 offen offset:0  lds
s_add_u32 m0,s57,130944
s_nop 0
buffer_load_dwordx4 v10,s[40:43],s55 offen offset:128  lds
s_add_u32 m0,s57,131968
s_nop 0
buffer_load_dwordx4 v11,s[40:43],s55 offen offset:128  lds
v_add_u32 v18,139264,v12
v_add_u32 v19,139264,v13
T a[208:209],18,0
T a[210:211],18,512
T a[212:213],19,0
T a[214:215],19,512
T a[216:217],18,4096
T a[218:219],18,4608
T a[220:221],19,4096
T a[222:223],19,4608
s_waitcnt vmcnt(20) lgkmcnt(0)
s_barrier
s_add_u32 s55,s50,2621440
s_add_u32 m0,s57,139264
s_nop 0
buffer_load_dwordx4 v10,s[40:43],s55 offen offset:0  lds
s_add_u32 m0,s57,140288
s_nop 0
buffer_load_dwordx4 v11,s[40:43],s55 offen offset:0  lds
s_add_u32 m0,s57,147328
s_nop 0
buffer_load_dwordx4 v10,s[40:43],s55 offen offset:128  lds
s_add_u32 m0,s57,148352
s_nop 0
buffer_load_dwordx4 v11,s[40:43],s55 offen offset:128  lds
v_add_u32 v18,40960,v12
v_add_u32 v19,40960,v13
T a[224:225],18,0
T a[226:227],18,512
T a[228:229],19,0
T a[230:231],19,512
T a[232:233],18,4096
T a[234:235],18,4608
T a[236:237],19,4096
T a[238:239],19,4608
s_waitcnt vmcnt(20) lgkmcnt(0)
s_barrier
)"
R"(s_add_u32 s55,s50,2752512
s_add_u32 m0,s57,40960
s_nop 0
buffer_load_dwordx4 v10,s[40:43],s55 offen offset:0  lds
s_add_u32 m0,s57,41984
s_nop 0
buffer_load_dwordx4 v11,s[40:43],s55 offen offset:0  lds
s_add_u32 m0,s57,49024
s_nop 0
buffer_load_dwordx4 v10,s[40:43],s55 offen offset:128  lds
s_add_u32 m0,s57,50048
s_nop 0
buffer_load_dwordx4 v11,s[40:43],s55 offen offset:128  lds
v_add_u32 v18,57344,v12
v_add_u32 v19,57344,v13
T a[240:241],18,0
T a[242:243],18,512
T a[244:245],19,0
T a[246:247],19,512
T a[248:249],18,4096
T a[250:251],18,4608
T a[252:253],19,4096
T a[254:255],19,4608
Lg4_pre_%=:
s_waitcnt vmcnt(20) lgkmcnt(0)
s_barrier
s_add_u32 s55,s50,2883584
s_add_u32 m0,s57,57344
s_nop 0
buffer_load_dwordx4 v10,s[40:43],s55 offen offset:0  lds
s_add_u32 m0,s57,58368
s_nop 0
buffer_load_dwordx4 v11,s[40:43],s55 offen offset:0  lds
s_add_u32 m0,s57,65408
s_nop 0
buffer_load_dwordx4 v10,s[40:43],s55 offen offset:128  lds
s_add_u32 m0,s57,66432
s_nop 0
buffer_load_dwordx4 v11,s[40:43],s55 offen offset:128  lds
v_add_u32 v18,73728,v12
v_add_u32 v19,73728,v13
T v[64:65],18,0
T v[66:67],18,512
T v[68:69],19,0
T v[70:71],19,512
T v[72:73],18,4096
T v[74:75],18,4608
T v[76:77],19,4096
T v[78:79],19,4608
s_waitcnt vmcnt(20) lgkmcnt(0)
s_barrier
s_add_u32 s55,s50,3014656
s_add_u32 m0,s57,73728
s_nop 0
buffer_load_dwordx4 v10,s[40:43],s55 offen offset:0  lds
s_add_u32 m0,s57,74752
s_nop 0
buffer_load_dwordx4 v11,s[40:43],s55 offen offset:0  lds
s_add_u32 m0,s57,81792
s_nop 0
buffer_load_dwordx4 v10,s[40:43],s55 offen offset:128  lds
s_add_u32 m0,s57,82816
s_nop 0
buffer_load_dwordx4 v11,s[40:43],s55 offen offset:128  lds
v_add_u32 v18,90112,v12
v_add_u32 v19,90112,v13
T v[80:81],18,0
T v[82:83],18,512
T v[84:85],19,0
T v[86:87],19,512
T v[88:89],18,4096
T v[90:91],18,4608
T v[92:93],19,4096
T v[94:95],19,4608
s_waitcnt vmcnt(20) lgkmcnt(0)
s_barrier
s_add_u32 s55,s50,3145728
s_add_u32 m0,s57,90112
s_nop 0
buffer_load_dwordx4 v10,s[40:43],s55 offen offset:0  lds
s_add_u32 m0,s57,91136
s_nop 0
buffer_load_dwordx4 v11,s[40:43],s55 offen offset:0  lds
s_add_u32 m0,s57,98176
s_nop 0
buffer_load_dwordx4 v10,s[40:43],s55 offen offset:128  lds
s_add_u32 m0,s57,99200
s_nop 0
buffer_load_dwordx4 v11,s[40:43],s55 offen offset:128  lds
v_add_u32 v18,106496,v12
v_add_u32 v19,106496,v13
T v[96:97],18,0
T v[98:99],18,512
T v[100:101],19,0
T v[102:103],19,512
T v[104:105],18,4096
T v[106:107],18,4608
T v[108:109],19,4096
T v[110:111],19,4608
s_waitcnt vmcnt(20) lgkmcnt(0)
s_barrier
s_add_u32 s55,s50,3276800
s_add_u32 m0,s57,106496
s_nop 0
buffer_load_dwordx4 v10,s[40:43],s55 offen offset:0  lds
s_add_u32 m0,s57,107520
s_nop 0
buffer_load_dwordx4 v11,s[40:43],s55 offen offset:0  lds
s_add_u32 m0,s57,114560
s_nop 0
buffer_load_dwordx4 v10,s[40:43],s55 offen offset:128  lds
s_add_u32 m0,s57,115584
s_nop 0
buffer_load_dwordx4 v11,s[40:43],s55 offen offset:128  lds
v_add_u32 v18,122880,v12
v_add_u32 v19,122880,v13
T v[112:113],18,0
T v[114:115],18,512
T v[116:117],19,0
T v[118:119],19,512
T v[120:121],18,4096
T v[122:123],18,4608
T v[124:125],19,4096
T v[126:127],19,4608
s_waitcnt vmcnt(20) lgkmcnt(0)
s_barrier
s_add_u32 s55,s50,3407872
s_add_u32 m0,s57,122880
s_nop 0
buffer_load_dwordx4 v10,s[40:43],s55 offen offset:0  lds
s_add_u32 m0,s57,123904
s_nop 0
buffer_load_dwordx4 v11,s[40:43],s55 offen offset:0  lds
s_add_u32 m0,s57,130944
s_nop 0
buffer_load_dwordx4 v10,s[40:43],s55 offen offset:128  lds
s_add_u32 m0,s57,131968
s_nop 0
buffer_load_dwordx4 v11,s[40:43],s55 offen offset:128  lds
v_add_u32 v18,139264,v12
v_add_u32 v19,139264,v13
T v[128:129],18,0
T v[130:131],18,512
T v[132:133],19,0
T v[134:135],19,512
T v[136:137],18,4096
T v[138:139],18,4608
T v[140:141],19,4096
T v[142:143],19,4608
s_waitcnt vmcnt(20) lgkmcnt(0)
s_barrier
s_add_u32 s55,s50,3538944
s_add_u32 m0,s57,139264
s_nop 0
buffer_load_dwordx4 v10,s[40:43],s55 offen offset:0  lds
s_add_u32 m0,s57,140288
s_nop 0
buffer_load_dwordx4 v11,s[40:43],s55 offen offset:0  lds
s_add_u32 m0,s57,147328
s_nop 0
buffer_load_dwordx4 v10,s[40:43],s55 offen offset:128  lds
s_add_u32 m0,s57,148352
s_nop 0
buffer_load_dwordx4 v11,s[40:43],s55 offen offset:128  lds
v_add_u32 v18,40960,v12
v_add_u32 v19,40960,v13
T v[144:145],18,0
T v[146:147],18,512
T v[148:149],19,0
T v[150:151],19,512
T v[152:153],18,4096
T v[154:155],18,4608
T v[156:157],19,4096
T v[158:159],19,4608
s_waitcnt vmcnt(20) lgkmcnt(0)
s_barrier
v_add_u32 v18,57344,v12
v_add_u32 v19,57344,v13
T v[160:161],18,0
T v[162:163],18,512
T v[164:165],19,0
T v[166:167],19,512
T v[168:169],18,4096
T v[170:171],18,4608
T v[172:173],19,4096
T v[174:175],19,4608
s_waitcnt vmcnt(16) lgkmcnt(0)
s_barrier
v_add_u32 v18,73728,v12
v_add_u32 v19,73728,v13
T v[176:177],18,0
T v[178:179],18,512
T v[180:181],19,0
T v[182:183],19,512
T v[184:185],18,4096
T v[186:187],18,4608
T v[188:189],19,4096
T v[190:191],19,4608
s_waitcnt vmcnt(12) lgkmcnt(0)
s_barrier
v_add_u32 v18,90112,v12
v_add_u32 v19,90112,v13
T v[192:193],18,0
T v[194:195],18,512
T v[196:197],19,0
T v[198:199],19,512
T v[200:201],18,4096
T v[202:203],18,4608
T v[204:205],19,4096
T v[206:207],19,4608
s_waitcnt vmcnt(8) lgkmcnt(0)
s_barrier
v_add_u32 v18,106496,v12
v_add_u32 v19,106496,v13
T v[208:209],18,0
T v[210:211],18,512
T v[212:213],19,0
T v[214:215],19,512
T v[216:217],18,4096
T v[218:219],18,4608
T v[220:221],19,4096
T v[222:223],19,4608
s_waitcnt vmcnt(4) lgkmcnt(0)
s_barrier
v_add_u32 v18,122880,v12
v_add_u32 v19,122880,v13
T v[224:225],18,0
T v[226:227],18,512
T v[228:229],19,0
T v[230:231],19,512
T v[232:233],18,4096
T v[234:235],18,4608
T v[236:237],19,4096
T v[238:239],19,4608
s_waitcnt vmcnt(0) lgkmcnt(0)
s_barrier
v_add_u32 v18,139264,v12
v_add_u32 v19,139264,v13
T v[240:241],18,0
T v[242:243],18,512
T v[244:245],19,0
T v[246:247],19,512
T v[248:249],18,4096
T v[250:251],18,4608
T v[252:253],19,4096
T v[254:255],19,4608
L 0
s_barrier
v_mov_b32 v4,s53
s_mov_b32 s82,0
s_mov_b32 s80,0
s_mov_b32 s54,s45
s_mul_i32 s57,s44,7168
s_mul_i32 s55,s45,114688
s_add_u32 s55,s55,s57
s_add_u32 s55,s55,40894464
s_mul_i32 s57,s44,7168
s_mul_i32 s60,s46,114688
s_mul_i32 s61,s46,2560
s_mul_i32 s62,s46,32768
s_mul_i32 s63,s46,2048
s_mul_i32 s64,s46,512
s_lshl_b32 s59,s44,8
s_add_u32 s59,s59,143360
s_mul_i32 s58,s45,2560
s_add_u32 s58,s58,109891584
s_mul_i32 s65,s45,32768
s_add_u32 s65,s65,s47
s_mul_i32 s68,s45,2048
s_add_u32 s68,s68,s48
s_mul_i32 s73,s45,512
s_add_u32 s73,s73,106954752
s_mul_i32 s74,s45,32768
s_add_u32 s74,s74,s49
s_mul_i32 s75,s45,1024
s_add_u32 s75,s75,107479040
s_mov_b32 s66,0x7fff0000
s_mov_b32 s67,0x7fff0000
s_mov_b32 s69,0x7fff0000
s_mov_b32 s76,0x7fff0000
s_mov_b32 s77,0x7fff0000
s_mov_b32 s70,0x7fff0000
s_mov_b32 s71,0x7fff0000
s_mov_b32 s72,0x7fff0000
v_mov_b32 v1,v0
v_add_u32 v2,28672,v0
s_mov_b32 s84,28672
s_mov_b32 s86,0x3a800000
s_mov_b32 s87,0x3727c5ac
s_mov_b32 s88,0x409d30fc
s_mov_b32 s89,0xbeeff558
s_lshr_b32 s85,s44,1
s_mul_i32 s85,s85,127
s_add_u32 s55,s55,s51
s_mul_i32 s78,s45,114688
s_sub_u32 s60,s55,s78
s_add_u32 s50,s93,224
s_add_u32 s93,s93,170
s_cmp_lt_u32 s45,224
s_cselect_b32 s63,224,-1
s_cmp_lt_u32 s45,170
s_cselect_b32 s63,170,s63
s_cselect_b32 s93,s93,s50
s_cselect_b32 s88,6,8
s_cselect_b32 s89,224,-1
v_lshlrev_b32 v8,22,v0
v_and_b32 v8,0x80000000,v8
v_or_b32 v8,v8,v5
v_add_u32 v2,57344,v0
s_lshl_b32 s84,s57,1
s_add_u32 s84,s84,57344
s_mov_b64 s[86:87],%22
s_mov_b64 s[40:41],%22
s_mov_b32 s42,4194304
s_and_b32 s78,s47,65535
s_lshr_b32 s78,s78,4
s_sub_u32 s85,s47,s78
s_add_u32 s85,s85,4194304
s_add_u32 s45,s47,5832704
s_mul_i32 s73,s44,1280
s_add_u32 s73,s73,114688
v_lshrrev_b32 v62,4,v9
v_and_b32 v62,3,v62
v_mul_u32_u24 v62,288,v62
v_lshrrev_b32 v63,7,v9
v_and_b32 v5,12,v9
v_add3_u32 v5,v5,v62,v63
v_add_u32 v5,s73,v5
v_mov_b32 v10,0
v_mov_b32 v11,0
v_mov_b32 v12,0
v_mov_b32 v13,0
s_mov_b32 s79,1638400
buffer_store_dwordx4 v[10:13],v7,s[40:43],s79 offen offset:0 sc0 sc1
s_mov_b32 s79,1966080
buffer_store_dwordx4 v[10:13],v7,s[40:43],s79 offen offset:0 sc0 sc1
s_mov_b32 s79,2293760
buffer_store_dwordx4 v[10:13],v7,s[40:43],s79 offen offset:0 sc0 sc1
s_mov_b32 s79,2621440
buffer_store_dwordx4 v[10:13],v7,s[40:43],s79 offen offset:0 sc0 sc1
s_mov_b32 s79,2949120
buffer_store_dwordx4 v[10:13],v7,s[40:43],s79 offen offset:0 sc0 sc1
s_mov_b32 s79,3276800
buffer_store_dwordx4 v[10:13],v7,s[40:43],s79 offen offset:0 sc0 sc1
s_mov_b32 s79,3604480
buffer_store_dwordx4 v[10:13],v7,s[40:43],s79 offen offset:0 sc0 sc1
s_mov_b32 s79,3932160
buffer_store_dwordx4 v[10:13],v7,s[40:43],s79 offen offset:0 sc0 sc1
s_getpc_b64 s[40:41]
Lg4_pc_%=:
s_lshl_b32 s78,s44,13
v_lshlrev_b32 v62,3,v0
v_add_u32 v62,s78,v62
v_min_u32 v62,Lcode_g4_e-Lg4_pc_%=-4,v62
s_mov_b32 m0,163584
s_nop 0
global_load_lds_dword v62,s[40:41]
s_add_u32 m0,s59,4096
s_nop 0
buffer_load_dword v8,s[36:39],s58 offen offset:0 sc1 lds
buffer_store_dword v4,v3,s[36:39],s71 offen offset:0 sc0 sc1
s_add_u32 m0,s59,5120
s_nop 0
buffer_load_dword v8,s[36:39],s58 offen offset:0 sc1 lds
buffer_store_dword v4,v3,s[36:39],s71 offen offset:0 sc0 sc1
s_mov_b32 s83,0
Lg4_poll_%=:
s_add_u32 s78,s83,2
s_and_b32 s78,s78,3
s_lshl_b32 s78,s78,10
s_add_u32 s78,s78,4096
s_add_u32 m0,s59,s78
s_nop 0
buffer_load_dword v8,s[36:39],s58 offen offset:0 sc1 lds
s_and_b32 s78,s83,3
s_lshl_b32 s78,s78,10
v_add_u32 v62,s78,v6
s_waitcnt vmcnt(4)
s_barrier
ds_read_b32 v7,v62 offset:4096
buffer_store_dword v4,v3,s[36:39],s70 offen offset:0 sc0 sc1
s_mov_b32 s70,s71
s_mov_b32 s71,s72
s_mov_b32 s72,0x7fff0000
s_add_u32 s83,s83,1
s_add_u32 s82,s82,1
L 0
v_cmp_eq_u32 vcc,s53,v7
s_nop 0
s_cmp_eq_u32 vcc_lo,-1
s_cbranch_scc1 Lg4_go_%=
s_cmp_lt_u32 s82,20000
s_cbranch_scc1 Lg4_poll_%=
Lg4_go_%=:
s_add_u32 m0,s57,0
s_add_u32 s56,s55,0
D 0,56,0
D 0,56,1024
D 0,56,2048
D 0,56,3072
s_add_u32 m0,s57,4096
s_add_u32 s56,s56,4096
D 0,56,0
D 0,56,1024
D 0,56,2048
s_add_u32 s61,s54,s46
s_cmp_ge_u32 s61,s63
s_cselect_b32 s61,s93,s61
s_cselect_b32 s46,s88,s46
s_cselect_b32 s63,s89,s63
s_cselect_b32 s93,s50,s93
s_cselect_b32 s88,8,s88
s_cselect_b32 s89,-1,s89
s_mul_i32 s58,s61,2560
s_add_u32 s58,s58,109891584
s_add_u32 m0,s59,2048
s_nop 0
buffer_load_dword v8,s[36:39],s58 offen offset:0 sc1 lds
s_add_u32 m0,s57,28672
s_add_u32 s56,s55,28672
D 0,56,0
D 0,56,1024
D 0,56,2048
D 0,56,3072
s_add_u32 m0,s57,32768
s_add_u32 s56,s56,4096
D 0,56,0
D 0,56,1024
D 0,56,2048
s_mul_i32 s55,s61,114688
s_add_u32 s55,s55,s60
s_sub_u32 s57,s84,s57
s_mov_b32 s54,s61
s_waitcnt vmcnt(7)
s_barrier
buffer_store_dword v4,v3,s[36:39],s70 offen offset:0 sc0 sc1
s_min_u32 s71,s71,s72
buffer_store_dword v4,v3,s[36:39],s71 offen offset:0 sc0 sc1
s_mov_b32 s70,0x7fff0000
s_mov_b32 s71,0x7fff0000
s_mov_b32 s72,0x7fff0000
s_cmp_eq_u32 s80,1
s_cbranch_scc1 Lg4_st1_%=
s_branch Lg4_st0_%=
Lg4_gox_%=:
s_add_u32 m0,s59,2048
s_nop 0
buffer_load_dword v8,s[36:39],s58 offen offset:0 sc1 lds
s_waitcnt vmcnt(0)
s_barrier
buffer_store_dword v4,v3,s[36:39],s70 offen offset:0 sc0 sc1
s_min_u32 s71,s71,s72
buffer_store_dword v4,v3,s[36:39],s71 offen offset:0 sc0 sc1
s_mov_b32 s70,0x7fff0000
s_mov_b32 s71,0x7fff0000
s_mov_b32 s72,0x7fff0000
s_cmp_eq_u32 s80,1
s_cbranch_scc1 Lg4_st1_%=
Lg4_st0_%=:
ds_read_b32 v7,v6 offset:2048
R 26,1,0
R 30,1,1024
R 34,1,2048
R 38,1,3072
R 42,1,4096
L 5
v_cmp_eq_u32 vcc,s53,v7
L 3
s_cmp_eq_u32 vcc_lo,-1
s_cbranch_scc1 Lg4_F0_%=
s_branch Lg4_L0_%=
Lg4_st1_%=:
ds_read_b32 v7,v6 offset:2048
R 26,2,0
R 30,2,1024
R 34,2,2048
R 38,2,3072
R 42,2,4096
L 5
v_cmp_eq_u32 vcc,s53,v7
L 3
s_cmp_eq_u32 vcc_lo,-1
s_cbranch_scc1 Lg4_F1_%=
s_branch Lg4_L1_%=
Lg4_F0_%=:
v_mfma_f32_16x16x32_bf16 v[14:17],a[4:7],v[26:29],0
s_add_u32 m0,s57,0
R 46,1,5120
v_lshlrev_b32 v62,16,v58
M 14,a[12:15],30
s_add_u32 s56,s55,0
v_add_f32 v18,v18,v62
v_and_b32 v63,0xffff0000,v58
v_mfma_f32_16x16x32_bf16 v[10:13],a[0:3],v[26:29],0
s_add_u32 s61,s54,s46
R 50,1,6144
v_add_f32 v19,v19,v63
M 10,a[8:11],30
L 3
D 0,56,0
)"
R"(M 10,a[16:19],34
s_cmp_ge_u32 s61,s63
R 54,1,7168
v_lshlrev_b32 v62,16,v59
M 10,a[24:27],38
s_cselect_b32 s61,s93,s61
v_add_f32 v20,v20,v62
v_and_b32 v63,0xffff0000,v59
M 14,a[20:23],34
s_cselect_b32 s46,s88,s46
R 26,1,8192
v_add_f32 v21,v21,v63
M 14,a[28:31],38
L 3
v_lshlrev_b32 v62,16,v60
v_add_f32 v22,v22,v62
M 14,a[36:39],42
s_cselect_b32 s63,s89,s63
R 30,1,9216
v_and_b32 v63,0xffff0000,v60
M 14,a[44:47],46
D 0,56,1024
M 10,a[32:35],42
s_cselect_b32 s93,s50,s93
R 34,1,10240
v_add_f32 v23,v23,v63
M 10,a[40:43],46
L 3
v_lshlrev_b32 v62,16,v61
v_add_f32 v24,v24,v62
M 10,a[48:51],50
s_cselect_b32 s88,8,s88
R 38,1,11264
v_and_b32 v63,0xffff0000,v61
M 10,a[56:59],54
buffer_load_dwordx4 v[58:61],v0,s[36:39],s74 offen offset:0 sc1
M 14,a[52:55],50
s_cselect_b32 s89,-1,s89
R 42,1,12288
v_add_f32 v25,v25,v63
M 14,a[60:63],54
L 3
v_cvt_pk_bf16_f32 v18,v18,v19
v_cvt_pk_bf16_f32 v19,v20,v21
M 14,a[68:71],26
s_mul_i32 s58,s61,2560
R 46,1,13312
v_cvt_pk_bf16_f32 v20,v22,v23
M 14,a[76:79],30
D 0,56,2048
M 10,a[64:67],26
s_add_u32 s58,s58,109891584
R 50,1,14336
v_cvt_pk_bf16_f32 v21,v24,v25
M 10,a[72:75],30
L 3
buffer_store_dwordx4 v[18:21],v0,s[36:39],s67 offen offset:0 sc0 sc1
M 10,a[80:83],34
s_lshl_b32 s62,s54,15
s_mov_b32 s66,s65
R 54,1,15360
M 10,a[88:91],38
s_cmp_ge_u32 s66,s45
s_cselect_b32 s51,1,-1
s_add_u32 s74,s49,s62
M 14,a[84:87],34
s_add_u32 s65,s47,s62
s_mov_b32 s54,s61
R 26,1,16384
M 14,a[92:95],38
L 3
M 14,a[100:103],42
R 30,1,17408
M 14,a[108:111],46
D 0,56,3072
M 10,a[96:99],42
s_add_u32 m0,s57,4096
s_add_u32 s56,s56,4096
R 34,1,18432
M 10,a[104:107],46
L 3
M 10,a[112:115],50
R 38,1,19456
M 10,a[120:123],54
M 14,a[116:119],50
R 42,1,20480
M 14,a[124:127],54
L 3
D 0,56,0
M 14,a[132:135],26
R 46,1,21504
M 14,a[140:143],30
M 10,a[128:131],26
R 50,1,22528
M 10,a[136:139],30
L 3
M 10,a[144:147],34
R 54,1,23552
M 10,a[152:155],38
M 14,a[148:151],34
R 26,1,24576
M 14,a[156:159],38
L 3
D 0,56,1024
M 14,a[164:167],42
R 30,1,25600
M 14,a[172:175],46
M 10,a[160:163],42
R 34,1,26624
M 10,a[168:171],46
L 3
M 10,a[176:179],50
s_waitcnt vmcnt(9)
R 38,1,27648
M 10,a[184:187],54
s_barrier
M 14,a[180:183],50
R 42,1,28672
M 14,a[188:191],54
L 3
D 0,56,2048
M 14,a[196:199],26
s_add_u32 m0,s59,0
s_add_u32 s56,s55,28672
R 46,1,29696
M 14,a[204:207],30
s_mul_i32 s55,s61,114688
s_add_u32 s55,s55,s60
M 10,a[192:195],26
R 50,1,30720
M 10,a[200:203],30
L 3
M 10,a[208:211],34
R 54,1,31744
M 10,a[216:219],38
M 14,a[212:215],34
R 26,1,32768
M 14,a[220:223],38
L 3
buffer_load_dword v8,s[36:39],s58 offen offset:0 sc1 lds
M 14,a[228:231],42
s_add_u32 m0,s57,28672
R 30,1,33792
M 14,a[236:239],46
D 0,56,0
M 10,a[224:227],42
R 34,1,34816
M 10,a[232:235],46
L 3
M 10,a[240:243],50
R 38,1,35840
M 10,a[248:251],54
M 14,a[244:247],50
R 42,1,36864
M 14,a[252:255],54
L 3
D 0,56,1024
M 14,v[68:71],26
R 46,1,37888
M 14,v[76:79],30
M 10,v[64:67],26
R 50,1,38912
M 10,v[72:75],30
L 3
M 10,v[80:83],34
R 54,1,39936
M 10,v[88:91],38
D 0,56,2048
M 14,v[84:87],34
R 26,1,40960
M 14,v[92:95],38
L 3
M 14,v[100:103],42
R 30,1,41984
M 14,v[108:111],46
M 10,v[96:99],42
R 34,1,43008
M 10,v[104:107],46
L 3
D 0,56,3072
M 10,v[112:115],50
s_add_u32 m0,s57,32768
s_add_u32 s56,s56,4096
R 38,1,44032
M 10,v[120:123],54
s_sub_u32 s57,s84,s57
M 14,v[116:119],50
R 42,1,45056
M 14,v[124:127],54
L 3
M 14,v[132:135],26
R 46,1,46080
M 14,v[140:143],30
D 0,56,0
M 10,v[128:131],26
R 50,1,47104
M 10,v[136:139],30
L 3
M 10,v[144:147],34
R 54,1,48128
M 10,v[152:155],38
M 14,v[148:151],34
R 26,1,49152
M 14,v[156:159],38
L 3
D 0,56,1024
M 14,v[164:167],42
R 30,1,50176
M 14,v[172:175],46
M 10,v[160:163],42
R 34,1,51200
M 10,v[168:171],46
L 3
M 10,v[176:179],50
R 38,1,52224
M 10,v[184:187],54
D 0,56,2048
M 14,v[180:183],50
R 42,1,53248
M 14,v[188:191],54
L 3
M 14,v[196:199],26
R 46,1,54272
M 14,v[204:207],30
M 10,v[192:195],26
R 50,1,55296
M 10,v[200:203],30
L 3
M 10,v[208:211],34
s_waitcnt vmcnt(7)
R 54,1,56320
M 10,v[216:219],38
s_barrier
ds_read_b32 v7,v6 offset:0
M 14,v[212:215],34
R 26,2,0
M 14,v[220:223],38
L 4
buffer_store_dword v4,v3,s[36:39],s69 offen offset:0 sc0 sc1
M 14,v[228:231],42
s_mov_b32 s69,s68
s_lshr_b32 s68,s62,4
R 30,2,1024
M 14,v[236:239],46
s_add_u32 s68,s68,s48
M 10,v[224:227],42
R 34,2,2048
M 10,v[232:235],46
L 4
L 3
M 10,v[240:243],50
R 38,2,3072
v_cmp_eq_u32 vcc,s53,v7
M 10,v[248:251],54
M 14,v[244:247],50
L 2
s_cmp_eq_u32 vcc_lo,s51
R 42,2,4096
M 14,v[252:255],54
s_cbranch_scc0 Lg4_K1_%=
Lg4_F1_%=:
v_mfma_f32_16x16x32_bf16 v[22:25],a[4:7],v[26:29],0
s_add_u32 m0,s57,0
R 46,2,5120
v_lshlrev_b32 v62,16,v58
M 22,a[12:15],30
s_add_u32 s56,s55,0
v_add_f32 v10,v10,v62
v_and_b32 v63,0xffff0000,v58
v_mfma_f32_16x16x32_bf16 v[18:21],a[0:3],v[26:29],0
s_add_u32 s61,s54,s46
R 50,2,6144
v_add_f32 v11,v11,v63
M 18,a[8:11],30
L 3
D 0,56,0
M 18,a[16:19],34
s_cmp_ge_u32 s61,s63
R 54,2,7168
v_lshlrev_b32 v62,16,v59
M 18,a[24:27],38
s_cselect_b32 s61,s93,s61
v_add_f32 v12,v12,v62
v_and_b32 v63,0xffff0000,v59
M 22,a[20:23],34
s_cselect_b32 s46,s88,s46
R 26,2,8192
v_add_f32 v13,v13,v63
M 22,a[28:31],38
L 3
v_lshlrev_b32 v62,16,v60
v_add_f32 v14,v14,v62
M 22,a[36:39],42
s_cselect_b32 s63,s89,s63
R 30,2,9216
v_and_b32 v63,0xffff0000,v60
M 22,a[44:47],46
D 0,56,1024
M 18,a[32:35],42
s_cselect_b32 s93,s50,s93
R 34,2,10240
v_add_f32 v15,v15,v63
M 18,a[40:43],46
L 3
v_lshlrev_b32 v62,16,v61
v_add_f32 v16,v16,v62
M 18,a[48:51],50
s_cselect_b32 s88,8,s88
R 38,2,11264
v_and_b32 v63,0xffff0000,v61
M 18,a[56:59],54
buffer_load_dwordx4 v[58:61],v0,s[36:39],s74 offen offset:0 sc1
M 22,a[52:55],50
s_cselect_b32 s89,-1,s89
R 42,2,12288
v_add_f32 v17,v17,v63
M 22,a[60:63],54
L 3
v_cvt_pk_bf16_f32 v10,v10,v11
v_cvt_pk_bf16_f32 v11,v12,v13
M 22,a[68:71],26
s_mul_i32 s58,s61,2560
R 46,2,13312
v_cvt_pk_bf16_f32 v12,v14,v15
M 22,a[76:79],30
D 0,56,2048
M 18,a[64:67],26
s_add_u32 s58,s58,109891584
R 50,2,14336
v_cvt_pk_bf16_f32 v13,v16,v17
M 18,a[72:75],30
L 3
buffer_store_dwordx4 v[10:13],v0,s[36:39],s66 offen offset:0 sc0 sc1
M 18,a[80:83],34
s_lshl_b32 s62,s54,15
s_mov_b32 s67,s65
R 54,2,15360
M 18,a[88:91],38
s_cmp_ge_u32 s67,s45
s_cselect_b32 s51,1,-1
s_add_u32 s74,s49,s62
M 22,a[84:87],34
s_add_u32 s65,s47,s62
s_mov_b32 s54,s61
R 26,2,16384
M 22,a[92:95],38
L 3
M 22,a[100:103],42
R 30,2,17408
M 22,a[108:111],46
D 0,56,3072
M 18,a[96:99],42
s_add_u32 m0,s57,4096
s_add_u32 s56,s56,4096
R 34,2,18432
M 18,a[104:107],46
L 3
M 18,a[112:115],50
R 38,2,19456
M 18,a[120:123],54
M 22,a[116:119],50
R 42,2,20480
M 22,a[124:127],54
L 3
D 0,56,0
M 22,a[132:135],26
R 46,2,21504
M 22,a[140:143],30
M 18,a[128:131],26
R 50,2,22528
M 18,a[136:139],30
L 3
M 18,a[144:147],34
R 54,2,23552
M 18,a[152:155],38
M 22,a[148:151],34
R 26,2,24576
M 22,a[156:159],38
L 3
D 0,56,1024
M 22,a[164:167],42
R 30,2,25600
M 22,a[172:175],46
M 18,a[160:163],42
R 34,2,26624
M 18,a[168:171],46
L 3
M 18,a[176:179],50
s_waitcnt vmcnt(9)
R 38,2,27648
M 18,a[184:187],54
s_barrier
M 22,a[180:183],50
R 42,2,28672
M 22,a[188:191],54
L 3
D 0,56,2048
M 22,a[196:199],26
s_add_u32 m0,s59,1024
s_add_u32 s56,s55,28672
R 46,2,29696
M 22,a[204:207],30
s_mul_i32 s55,s61,114688
s_add_u32 s55,s55,s60
M 18,a[192:195],26
R 50,2,30720
M 18,a[200:203],30
L 3
M 18,a[208:211],34
R 54,2,31744
M 18,a[216:219],38
M 22,a[212:215],34
R 26,2,32768
M 22,a[220:223],38
L 3
buffer_load_dword v8,s[36:39],s58 offen offset:0 sc1 lds
M 22,a[228:231],42
s_add_u32 m0,s57,28672
R 30,2,33792
M 22,a[236:239],46
D 0,56,0
M 18,a[224:227],42
R 34,2,34816
M 18,a[232:235],46
L 3
M 18,a[240:243],50
R 38,2,35840
M 18,a[248:251],54
M 22,a[244:247],50
R 42,2,36864
M 22,a[252:255],54
L 3
D 0,56,1024
M 22,v[68:71],26
R 46,2,37888
M 22,v[76:79],30
M 18,v[64:67],26
R 50,2,38912
M 18,v[72:75],30
L 3
M 18,v[80:83],34
R 54,2,39936
M 18,v[88:91],38
D 0,56,2048
M 22,v[84:87],34
R 26,2,40960
M 22,v[92:95],38
L 3
M 22,v[100:103],42
R 30,2,41984
M 22,v[108:111],46
M 18,v[96:99],42
R 34,2,43008
M 18,v[104:107],46
L 3
D 0,56,3072
M 18,v[112:115],50
s_add_u32 m0,s57,32768
s_add_u32 s56,s56,4096
R 38,2,44032
M 18,v[120:123],54
s_sub_u32 s57,s84,s57
M 22,v[116:119],50
R 42,2,45056
M 22,v[124:127],54
L 3
M 22,v[132:135],26
R 46,2,46080
M 22,v[140:143],30
D 0,56,0
M 18,v[128:131],26
R 50,2,47104
M 18,v[136:139],30
L 3
M 18,v[144:147],34
R 54,2,48128
M 18,v[152:155],38
M 22,v[148:151],34
R 26,2,49152
M 22,v[156:159],38
L 3
D 0,56,1024
M 22,v[164:167],42
R 30,2,50176
M 22,v[172:175],46
M 18,v[160:163],42
R 34,2,51200
M 18,v[168:171],46
L 3
M 18,v[176:179],50
R 38,2,52224
M 18,v[184:187],54
D 0,56,2048
M 22,v[180:183],50
R 42,2,53248
M 22,v[188:191],54
L 3
M 22,v[196:199],26
R 46,2,54272
M 22,v[204:207],30
)"
R"(M 18,v[192:195],26
R 50,2,55296
M 18,v[200:203],30
L 3
M 18,v[208:211],34
s_waitcnt vmcnt(7)
R 54,2,56320
M 18,v[216:219],38
s_barrier
ds_read_b32 v7,v6 offset:1024
M 22,v[212:215],34
R 26,1,0
M 22,v[220:223],38
L 4
buffer_store_dword v4,v3,s[36:39],s69 offen offset:0 sc0 sc1
M 22,v[228:231],42
s_mov_b32 s69,s68
s_lshr_b32 s68,s62,4
R 30,1,1024
M 22,v[236:239],46
s_add_u32 s68,s68,s48
M 18,v[224:227],42
R 34,1,2048
M 18,v[232:235],46
L 4
L 3
M 18,v[240:243],50
R 38,1,3072
v_cmp_eq_u32 vcc,s53,v7
M 18,v[248:251],54
M 22,v[244:247],50
L 2
s_cmp_eq_u32 vcc_lo,s51
R 42,1,4096
M 22,v[252:255],54
s_cbranch_scc1 Lg4_F0_%=
Lg4_K0_%=:
s_cmp_eq_u32 s51,1
s_cbranch_scc0 Lg4_L0_%=
s_cmp_eq_u32 vcc_lo,-1
s_cbranch_scc1 Lg4_Fa0_%=
s_branch Lg4_La0_%=
Lg4_L0_%=:
v_mfma_f32_16x16x32_bf16 v[14:17],a[4:7],v[26:29],0
s_add_u32 m0,s59,3072
R 46,1,5120
v_lshlrev_b32 v62,16,v58
M 14,a[12:15],30
buffer_load_dword v8,s[36:39],s58 offen offset:0 sc1 lds
v_mfma_f32_16x16x32_bf16 v[10:13],a[0:3],v[26:29],0
s_mov_b32 s66,s65
R 50,1,6144
v_add_f32 v18,v18,v62
M 10,a[8:11],30
L 3
v_and_b32 v63,0xffff0000,v58
v_add_f32 v19,v19,v63
M 10,a[16:19],34
s_lshl_b32 s62,s54,15
R 54,1,7168
v_lshlrev_b32 v62,16,v59
M 10,a[24:27],38
s_cmp_ge_u32 s66,s45
v_add_f32 v20,v20,v62
v_and_b32 v63,0xffff0000,v59
M 14,a[20:23],34
s_cselect_b32 s51,1,-1
R 26,1,8192
v_add_f32 v21,v21,v63
M 14,a[28:31],38
L 3
v_lshlrev_b32 v62,16,v60
v_add_f32 v22,v22,v62
M 14,a[36:39],42
s_add_u32 s65,s47,s62
R 30,1,9216
v_and_b32 v63,0xffff0000,v60
M 14,a[44:47],46
v_add_f32 v23,v23,v63
v_lshlrev_b32 v62,16,v61
M 10,a[32:35],42
R 34,1,10240
v_add_f32 v24,v24,v62
M 10,a[40:43],46
L 3
v_and_b32 v63,0xffff0000,v61
v_add_f32 v25,v25,v63
M 10,a[48:51],50
R 38,1,11264
v_cvt_pk_bf16_f32 v18,v18,v19
M 10,a[56:59],54
buffer_load_dwordx4 v[58:61],v0,s[36:39],s74 offen offset:0 sc1
M 14,a[52:55],50
s_add_u32 s74,s49,s62
R 42,1,12288
v_cvt_pk_bf16_f32 v19,v20,v21
M 14,a[60:63],54
L 3
v_cvt_pk_bf16_f32 v20,v22,v23
v_cvt_pk_bf16_f32 v21,v24,v25
M 14,a[68:71],26
R 46,1,13312
M 14,a[76:79],30
buffer_store_dwordx4 v[18:21],v0,s[36:39],s67 offen offset:0 sc0 sc1
M 10,a[64:67],26
R 50,1,14336
M 10,a[72:75],30
L 3
M 10,a[80:83],34
R 54,1,15360
M 10,a[88:91],38
M 14,a[84:87],34
R 26,1,16384
M 14,a[92:95],38
L 3
M 14,a[100:103],42
R 30,1,17408
M 14,a[108:111],46
M 10,a[96:99],42
R 34,1,18432
M 10,a[104:107],46
L 3
M 10,a[112:115],50
R 38,1,19456
M 10,a[120:123],54
M 14,a[116:119],50
R 42,1,20480
M 14,a[124:127],54
L 3
M 14,a[132:135],26
R 46,1,21504
M 14,a[140:143],30
M 10,a[128:131],26
R 50,1,22528
M 10,a[136:139],30
L 3
M 10,a[144:147],34
R 54,1,23552
M 10,a[152:155],38
M 14,a[148:151],34
R 26,1,24576
M 14,a[156:159],38
L 3
M 14,a[164:167],42
R 30,1,25600
M 14,a[172:175],46
M 10,a[160:163],42
R 34,1,26624
M 10,a[168:171],46
L 3
M 10,a[176:179],50
s_waitcnt vmcnt(2)
R 38,1,27648
M 10,a[184:187],54
s_barrier
ds_read_b32 v7,v6 offset:3072
M 14,a[180:183],50
R 42,1,28672
M 14,a[188:191],54
L 4
M 14,a[196:199],26
R 46,1,29696
M 14,a[204:207],30
M 10,a[192:195],26
R 50,1,30720
M 10,a[200:203],30
L 4
L 3
v_cmp_eq_u32 vcc,s53,v7
M 10,a[208:211],34
R 54,1,31744
M 10,a[216:219],38
M 14,a[212:215],34
s_cmp_eq_u32 vcc_lo,-1
R 26,1,32768
s_cbranch_scc0 Lg4_X0_%=
M 14,a[220:223],38
L 3
s_add_u32 m0,s57,0
s_add_u32 s56,s55,0
M 14,a[228:231],42
s_add_u32 s61,s54,s46
s_cmp_ge_u32 s61,s63
s_cselect_b32 s61,s93,s61
R 30,1,33792
M 14,a[236:239],46
s_mul_i32 s58,s61,2560
D 0,56,0
M 10,a[224:227],42
s_cselect_b32 s46,s88,s46
s_cselect_b32 s63,s89,s63
R 34,1,34816
M 10,a[232:235],46
L 3
s_cselect_b32 s93,s50,s93
s_cselect_b32 s88,8,s88
M 10,a[240:243],50
s_cselect_b32 s89,-1,s89
s_add_u32 s58,s58,109891584
R 38,1,35840
M 10,a[248:251],54
D 0,56,1024
M 14,a[244:247],50
s_mov_b32 s54,s61
R 42,1,36864
M 14,a[252:255],54
L 3
M 14,v[68:71],26
R 46,1,37888
M 14,v[76:79],30
D 0,56,2048
M 10,v[64:67],26
R 50,1,38912
M 10,v[72:75],30
L 3
M 10,v[80:83],34
R 54,1,39936
M 10,v[88:91],38
D 0,56,3072
M 14,v[84:87],34
s_add_u32 m0,s57,4096
s_add_u32 s56,s56,4096
R 26,1,40960
M 14,v[92:95],38
L 3
M 14,v[100:103],42
R 30,1,41984
M 14,v[108:111],46
D 0,56,0
M 10,v[96:99],42
R 34,1,43008
M 10,v[104:107],46
L 3
M 10,v[112:115],50
R 38,1,44032
M 10,v[120:123],54
D 0,56,1024
M 14,v[116:119],50
R 42,1,45056
M 14,v[124:127],54
L 3
M 14,v[132:135],26
R 46,1,46080
M 14,v[140:143],30
D 0,56,2048
M 10,v[128:131],26
s_add_u32 m0,s59,0
s_add_u32 s56,s55,28672
R 50,1,47104
M 10,v[136:139],30
L 3
buffer_load_dword v8,s[36:39],s58 offen offset:0 sc1 lds
M 10,v[144:147],34
s_add_u32 m0,s57,28672
s_mul_i32 s55,s61,114688
R 54,1,48128
M 10,v[152:155],38
s_add_u32 s55,s55,s60
M 14,v[148:151],34
R 26,1,49152
M 14,v[156:159],38
L 3
D 0,56,0
M 14,v[164:167],42
R 30,1,50176
M 14,v[172:175],46
M 10,v[160:163],42
R 34,1,51200
M 10,v[168:171],46
L 3
D 0,56,1024
M 10,v[176:179],50
R 38,1,52224
M 10,v[184:187],54
D 0,56,2048
M 14,v[180:183],50
R 42,1,53248
M 14,v[188:191],54
L 3
M 14,v[196:199],26
R 46,1,54272
M 14,v[204:207],30
D 0,56,3072
M 10,v[192:195],26
s_add_u32 m0,s57,32768
s_add_u32 s56,s56,4096
R 50,1,55296
M 10,v[200:203],30
L 3
s_sub_u32 s57,s84,s57
M 10,v[208:211],34
s_waitcnt vmcnt(4)
R 54,1,56320
M 10,v[216:219],38
s_barrier
ds_read_b32 v7,v6 offset:0
M 14,v[212:215],34
R 26,2,0
M 14,v[220:223],38
L 4
buffer_store_dword v4,v3,s[36:39],s69 offen offset:0 sc0 sc1
M 14,v[228:231],42
s_mov_b32 s69,s68
s_lshr_b32 s68,s62,4
R 30,2,1024
M 14,v[236:239],46
D 0,56,0
M 10,v[224:227],42
s_add_u32 s68,s68,s48
R 34,2,2048
M 10,v[232:235],46
L 4
D 0,56,1024
M 10,v[240:243],50
L 3
R 38,2,3072
v_cmp_eq_u32 vcc,s53,v7
M 10,v[248:251],54
D 0,56,2048
M 14,v[244:247],50
L 2
s_cmp_eq_u32 vcc_lo,s51
R 42,2,4096
M 14,v[252:255],54
buffer_store_dword v4,v3,s[36:39],s71 offen offset:0 sc0 sc1
s_cbranch_scc1 Lg4_F1_%=
s_branch Lg4_K1_%=
Lg4_X0_%=:
M 14,a[220:223],38
L 3
s_add_u32 m0,s59,0
M 14,a[228:231],42
R 30,1,33792
M 14,a[236:239],46
buffer_load_dword v8,s[36:39],s58 offen offset:0 sc1 lds
M 10,a[224:227],42
R 34,1,34816
M 10,a[232:235],46
L 3
M 10,a[240:243],50
R 38,1,35840
M 10,a[248:251],54
M 14,a[244:247],50
R 42,1,36864
M 14,a[252:255],54
L 3
M 14,v[68:71],26
R 46,1,37888
M 14,v[76:79],30
M 10,v[64:67],26
R 50,1,38912
M 10,v[72:75],30
L 3
M 10,v[80:83],34
R 54,1,39936
M 10,v[88:91],38
M 14,v[84:87],34
R 26,1,40960
M 14,v[92:95],38
L 3
M 14,v[100:103],42
R 30,1,41984
M 14,v[108:111],46
M 10,v[96:99],42
R 34,1,43008
M 10,v[104:107],46
L 3
M 10,v[112:115],50
R 38,1,44032
M 10,v[120:123],54
M 14,v[116:119],50
R 42,1,45056
M 14,v[124:127],54
L 3
M 14,v[132:135],26
R 46,1,46080
M 14,v[140:143],30
M 10,v[128:131],26
R 50,1,47104
M 10,v[136:139],30
L 3
M 10,v[144:147],34
R 54,1,48128
M 10,v[152:155],38
M 14,v[148:151],34
R 26,1,49152
M 14,v[156:159],38
L 3
M 14,v[164:167],42
R 30,1,50176
M 14,v[172:175],46
M 10,v[160:163],42
R 34,1,51200
M 10,v[168:171],46
L 3
M 10,v[176:179],50
R 38,1,52224
M 10,v[184:187],54
M 14,v[180:183],50
R 42,1,53248
M 14,v[188:191],54
L 3
M 14,v[196:199],26
R 46,1,54272
M 14,v[204:207],30
M 10,v[192:195],26
R 50,1,55296
M 10,v[200:203],30
L 3
M 10,v[208:211],34
s_waitcnt vmcnt(0)
R 54,1,56320
M 10,v[216:219],38
s_barrier
buffer_store_dword v4,v3,s[36:39],s69 offen offset:0 sc0 sc1
M 14,v[212:215],34
s_mov_b32 s69,s68
s_lshr_b32 s68,s62,4
ds_read_b32 v7,v6 offset:0
M 14,v[220:223],38
L 3
s_add_u32 s68,s68,s48
M 14,v[228:231],42
M 14,v[236:239],46
M 10,v[224:227],42
M 10,v[232:235],46
L 1
M 10,v[240:243],50
L 0
v_cmp_eq_u32 vcc,s53,v7
M 10,v[248:251],54
M 14,v[244:247],50
M 14,v[252:255],54
s_cmp_eq_u32 s51,1
s_cbranch_scc1 Lg4_pa0_%=
s_mov_b32 s80,1
s_cmp_eq_u32 vcc_lo,-1
s_cbranch_scc0 Lg4_xn0_%=
s_add_u32 m0,s57,0
s_add_u32 s56,s55,0
D 0,56,0
v_lshlrev_b32 v62,16,v58
D 0,56,1024
v_add_f32 v10,v10,v62
D 0,56,2048
v_and_b32 v63,0xffff0000,v58
D 0,56,3072
v_add_f32 v11,v11,v63
s_add_u32 m0,s57,4096
s_add_u32 s56,s56,4096
D 0,56,0
v_lshlrev_b32 v62,16,v59
D 0,56,1024
v_add_f32 v12,v12,v62
D 0,56,2048
v_and_b32 v63,0xffff0000,v59
s_add_u32 s61,s54,s46
s_cmp_ge_u32 s61,s63
s_cselect_b32 s61,s93,s61
s_cselect_b32 s46,s88,s46
s_cselect_b32 s63,s89,s63
s_cselect_b32 s93,s50,s93
s_cselect_b32 s88,8,s88
s_cselect_b32 s89,-1,s89
s_mul_i32 s58,s61,2560
s_add_u32 s58,s58,109891584
s_add_u32 m0,s57,28672
s_add_u32 s56,s55,28672
D 0,56,0
v_add_f32 v13,v13,v63
D 0,56,1024
v_lshlrev_b32 v62,16,v60
D 0,56,2048
v_add_f32 v14,v14,v62
D 0,56,3072
v_and_b32 v63,0xffff0000,v60
s_add_u32 m0,s57,32768
s_add_u32 s56,s56,4096
D 0,56,0
v_add_f32 v15,v15,v63
D 0,56,1024
v_lshlrev_b32 v62,16,v61
D 0,56,2048
v_add_f32 v16,v16,v62
s_mul_i32 s55,s61,114688
s_add_u32 s55,s55,s60
s_sub_u32 s57,s84,s57
s_mov_b32 s54,s61
v_and_b32 v63,0xffff0000,v61
v_add_f32 v17,v17,v63
v_cvt_pk_bf16_f32 v10,v10,v11
v_cvt_pk_bf16_f32 v11,v12,v13
v_cvt_pk_bf16_f32 v12,v14,v15
v_cvt_pk_bf16_f32 v13,v16,v17
buffer_store_dwordx4 v[10:13],v0,s[36:39],s66 offen offset:0 sc0 sc1
s_mov_b32 s72,s69
s_mov_b32 s69,0x7fff0000
s_mov_b32 s66,0x7fff0000
s_mov_b32 s76,0x7fff0000
s_branch Lg4_gox_%=
)"
R"(Lg4_xn0_%=:
s_cmp_ge_u32 s54,256
s_cbranch_scc1 Lg4_fin0_%=
s_add_u32 m0,s59,4096
s_nop 0
buffer_load_dword v8,s[36:39],s58 offen offset:0 sc1 lds
buffer_store_dword v4,v3,s[36:39],s71 offen offset:0 sc0 sc1
s_add_u32 m0,s59,5120
s_nop 0
buffer_load_dword v8,s[36:39],s58 offen offset:0 sc1 lds
buffer_store_dword v4,v3,s[36:39],s71 offen offset:0 sc0 sc1
s_mov_b32 s83,0
v_lshlrev_b32 v62,16,v58
v_add_f32 v10,v10,v62
v_and_b32 v63,0xffff0000,v58
v_add_f32 v11,v11,v63
v_lshlrev_b32 v62,16,v59
v_add_f32 v12,v12,v62
v_and_b32 v63,0xffff0000,v59
v_add_f32 v13,v13,v63
v_lshlrev_b32 v62,16,v60
v_add_f32 v14,v14,v62
v_and_b32 v63,0xffff0000,v60
v_add_f32 v15,v15,v63
v_lshlrev_b32 v62,16,v61
v_add_f32 v16,v16,v62
v_and_b32 v63,0xffff0000,v61
v_add_f32 v17,v17,v63
v_cvt_pk_bf16_f32 v10,v10,v11
v_cvt_pk_bf16_f32 v11,v12,v13
v_cvt_pk_bf16_f32 v12,v14,v15
v_cvt_pk_bf16_f32 v13,v16,v17
buffer_store_dwordx4 v[10:13],v0,s[36:39],s66 offen offset:0 sc0 sc1
s_mov_b32 s72,s69
s_mov_b32 s69,0x7fff0000
s_mov_b32 s66,0x7fff0000
s_mov_b32 s76,0x7fff0000
s_branch Lg4_poll_%=
Lg4_fin0_%=:
v_lshlrev_b32 v62,16,v58
v_add_f32 v10,v10,v62
v_and_b32 v63,0xffff0000,v58
v_add_f32 v11,v11,v63
v_lshlrev_b32 v62,16,v59
v_add_f32 v12,v12,v62
v_and_b32 v63,0xffff0000,v59
v_add_f32 v13,v13,v63
v_lshlrev_b32 v62,16,v60
v_add_f32 v14,v14,v62
v_and_b32 v63,0xffff0000,v60
v_add_f32 v15,v15,v63
v_lshlrev_b32 v62,16,v61
v_add_f32 v16,v16,v62
v_and_b32 v63,0xffff0000,v61
v_add_f32 v17,v17,v63
v_cvt_pk_bf16_f32 v10,v10,v11
v_cvt_pk_bf16_f32 v11,v12,v13
v_cvt_pk_bf16_f32 v12,v14,v15
v_cvt_pk_bf16_f32 v13,v16,v17
buffer_store_dwordx4 v[10:13],v0,s[36:39],s66 offen offset:0 sc0 sc1
s_mov_b32 s72,s69
s_mov_b32 s69,0x7fff0000
s_mov_b32 s66,0x7fff0000
s_mov_b32 s76,0x7fff0000
s_branch Lg4_done_%=
Lg4_K1_%=:
s_cmp_eq_u32 s51,1
s_cbranch_scc0 Lg4_L1_%=
s_cmp_eq_u32 vcc_lo,-1
s_cbranch_scc1 Lg4_Fa1_%=
s_branch Lg4_La1_%=
Lg4_L1_%=:
v_mfma_f32_16x16x32_bf16 v[22:25],a[4:7],v[26:29],0
s_add_u32 m0,s59,3072
R 46,2,5120
v_lshlrev_b32 v62,16,v58
M 22,a[12:15],30
buffer_load_dword v8,s[36:39],s58 offen offset:0 sc1 lds
v_mfma_f32_16x16x32_bf16 v[18:21],a[0:3],v[26:29],0
s_mov_b32 s67,s65
R 50,2,6144
v_add_f32 v10,v10,v62
M 18,a[8:11],30
L 3
v_and_b32 v63,0xffff0000,v58
v_add_f32 v11,v11,v63
M 18,a[16:19],34
s_lshl_b32 s62,s54,15
R 54,2,7168
v_lshlrev_b32 v62,16,v59
M 18,a[24:27],38
s_cmp_ge_u32 s67,s45
v_add_f32 v12,v12,v62
v_and_b32 v63,0xffff0000,v59
M 22,a[20:23],34
s_cselect_b32 s51,1,-1
R 26,2,8192
v_add_f32 v13,v13,v63
M 22,a[28:31],38
L 3
v_lshlrev_b32 v62,16,v60
v_add_f32 v14,v14,v62
M 22,a[36:39],42
s_add_u32 s65,s47,s62
R 30,2,9216
v_and_b32 v63,0xffff0000,v60
M 22,a[44:47],46
v_add_f32 v15,v15,v63
v_lshlrev_b32 v62,16,v61
M 18,a[32:35],42
R 34,2,10240
v_add_f32 v16,v16,v62
M 18,a[40:43],46
L 3
v_and_b32 v63,0xffff0000,v61
v_add_f32 v17,v17,v63
M 18,a[48:51],50
R 38,2,11264
v_cvt_pk_bf16_f32 v10,v10,v11
M 18,a[56:59],54
buffer_load_dwordx4 v[58:61],v0,s[36:39],s74 offen offset:0 sc1
M 22,a[52:55],50
s_add_u32 s74,s49,s62
R 42,2,12288
v_cvt_pk_bf16_f32 v11,v12,v13
M 22,a[60:63],54
L 3
v_cvt_pk_bf16_f32 v12,v14,v15
v_cvt_pk_bf16_f32 v13,v16,v17
M 22,a[68:71],26
R 46,2,13312
M 22,a[76:79],30
buffer_store_dwordx4 v[10:13],v0,s[36:39],s66 offen offset:0 sc0 sc1
M 18,a[64:67],26
R 50,2,14336
M 18,a[72:75],30
L 3
M 18,a[80:83],34
R 54,2,15360
M 18,a[88:91],38
M 22,a[84:87],34
R 26,2,16384
M 22,a[92:95],38
L 3
M 22,a[100:103],42
R 30,2,17408
M 22,a[108:111],46
M 18,a[96:99],42
R 34,2,18432
M 18,a[104:107],46
L 3
M 18,a[112:115],50
R 38,2,19456
M 18,a[120:123],54
M 22,a[116:119],50
R 42,2,20480
M 22,a[124:127],54
L 3
M 22,a[132:135],26
R 46,2,21504
M 22,a[140:143],30
M 18,a[128:131],26
R 50,2,22528
M 18,a[136:139],30
L 3
M 18,a[144:147],34
R 54,2,23552
M 18,a[152:155],38
M 22,a[148:151],34
R 26,2,24576
M 22,a[156:159],38
L 3
M 22,a[164:167],42
R 30,2,25600
M 22,a[172:175],46
M 18,a[160:163],42
R 34,2,26624
M 18,a[168:171],46
L 3
M 18,a[176:179],50
s_waitcnt vmcnt(2)
R 38,2,27648
M 18,a[184:187],54
s_barrier
ds_read_b32 v7,v6 offset:3072
M 22,a[180:183],50
R 42,2,28672
M 22,a[188:191],54
L 4
M 22,a[196:199],26
R 46,2,29696
M 22,a[204:207],30
M 18,a[192:195],26
R 50,2,30720
M 18,a[200:203],30
L 4
L 3
v_cmp_eq_u32 vcc,s53,v7
M 18,a[208:211],34
R 54,2,31744
M 18,a[216:219],38
M 22,a[212:215],34
s_cmp_eq_u32 vcc_lo,-1
R 26,2,32768
s_cbranch_scc0 Lg4_X1_%=
M 22,a[220:223],38
L 3
s_add_u32 m0,s57,0
s_add_u32 s56,s55,0
M 22,a[228:231],42
s_add_u32 s61,s54,s46
s_cmp_ge_u32 s61,s63
s_cselect_b32 s61,s93,s61
R 30,2,33792
M 22,a[236:239],46
s_mul_i32 s58,s61,2560
D 0,56,0
M 18,a[224:227],42
s_cselect_b32 s46,s88,s46
s_cselect_b32 s63,s89,s63
R 34,2,34816
M 18,a[232:235],46
L 3
s_cselect_b32 s93,s50,s93
s_cselect_b32 s88,8,s88
M 18,a[240:243],50
s_cselect_b32 s89,-1,s89
s_add_u32 s58,s58,109891584
R 38,2,35840
M 18,a[248:251],54
D 0,56,1024
M 22,a[244:247],50
s_mov_b32 s54,s61
R 42,2,36864
M 22,a[252:255],54
L 3
M 22,v[68:71],26
R 46,2,37888
M 22,v[76:79],30
D 0,56,2048
M 18,v[64:67],26
R 50,2,38912
M 18,v[72:75],30
L 3
M 18,v[80:83],34
R 54,2,39936
M 18,v[88:91],38
D 0,56,3072
M 22,v[84:87],34
s_add_u32 m0,s57,4096
s_add_u32 s56,s56,4096
R 26,2,40960
M 22,v[92:95],38
L 3
M 22,v[100:103],42
R 30,2,41984
M 22,v[108:111],46
D 0,56,0
M 18,v[96:99],42
R 34,2,43008
M 18,v[104:107],46
L 3
M 18,v[112:115],50
R 38,2,44032
M 18,v[120:123],54
D 0,56,1024
M 22,v[116:119],50
R 42,2,45056
M 22,v[124:127],54
L 3
M 22,v[132:135],26
R 46,2,46080
M 22,v[140:143],30
D 0,56,2048
M 18,v[128:131],26
s_add_u32 m0,s59,1024
s_add_u32 s56,s55,28672
R 50,2,47104
M 18,v[136:139],30
L 3
buffer_load_dword v8,s[36:39],s58 offen offset:0 sc1 lds
M 18,v[144:147],34
s_add_u32 m0,s57,28672
s_mul_i32 s55,s61,114688
R 54,2,48128
M 18,v[152:155],38
s_add_u32 s55,s55,s60
M 22,v[148:151],34
R 26,2,49152
M 22,v[156:159],38
L 3
D 0,56,0
M 22,v[164:167],42
R 30,2,50176
M 22,v[172:175],46
M 18,v[160:163],42
R 34,2,51200
M 18,v[168:171],46
L 3
D 0,56,1024
M 18,v[176:179],50
R 38,2,52224
M 18,v[184:187],54
D 0,56,2048
M 22,v[180:183],50
R 42,2,53248
M 22,v[188:191],54
L 3
M 22,v[196:199],26
R 46,2,54272
M 22,v[204:207],30
D 0,56,3072
M 18,v[192:195],26
s_add_u32 m0,s57,32768
s_add_u32 s56,s56,4096
R 50,2,55296
M 18,v[200:203],30
L 3
s_sub_u32 s57,s84,s57
M 18,v[208:211],34
s_waitcnt vmcnt(4)
R 54,2,56320
M 18,v[216:219],38
s_barrier
ds_read_b32 v7,v6 offset:1024
M 22,v[212:215],34
R 26,1,0
M 22,v[220:223],38
L 4
buffer_store_dword v4,v3,s[36:39],s69 offen offset:0 sc0 sc1
M 22,v[228:231],42
s_mov_b32 s69,s68
s_lshr_b32 s68,s62,4
R 30,1,1024
M 22,v[236:239],46
D 0,56,0
M 18,v[224:227],42
s_add_u32 s68,s68,s48
R 34,1,2048
M 18,v[232:235],46
L 4
D 0,56,1024
M 18,v[240:243],50
L 3
R 38,1,3072
v_cmp_eq_u32 vcc,s53,v7
M 18,v[248:251],54
D 0,56,2048
M 22,v[244:247],50
L 2
s_cmp_eq_u32 vcc_lo,s51
R 42,1,4096
M 22,v[252:255],54
buffer_store_dword v4,v3,s[36:39],s71 offen offset:0 sc0 sc1
s_cbranch_scc1 Lg4_F0_%=
s_branch Lg4_K0_%=
Lg4_X1_%=:
M 22,a[220:223],38
L 3
s_add_u32 m0,s59,1024
M 22,a[228:231],42
R 30,2,33792
M 22,a[236:239],46
buffer_load_dword v8,s[36:39],s58 offen offset:0 sc1 lds
M 18,a[224:227],42
R 34,2,34816
M 18,a[232:235],46
L 3
M 18,a[240:243],50
R 38,2,35840
M 18,a[248:251],54
M 22,a[244:247],50
R 42,2,36864
M 22,a[252:255],54
L 3
M 22,v[68:71],26
R 46,2,37888
M 22,v[76:79],30
M 18,v[64:67],26
R 50,2,38912
M 18,v[72:75],30
L 3
M 18,v[80:83],34
R 54,2,39936
M 18,v[88:91],38
M 22,v[84:87],34
R 26,2,40960
M 22,v[92:95],38
L 3
M 22,v[100:103],42
R 30,2,41984
M 22,v[108:111],46
M 18,v[96:99],42
R 34,2,43008
M 18,v[104:107],46
L 3
M 18,v[112:115],50
R 38,2,44032
M 18,v[120:123],54
M 22,v[116:119],50
R 42,2,45056
M 22,v[124:127],54
L 3
M 22,v[132:135],26
R 46,2,46080
M 22,v[140:143],30
M 18,v[128:131],26
R 50,2,47104
M 18,v[136:139],30
L 3
M 18,v[144:147],34
R 54,2,48128
M 18,v[152:155],38
M 22,v[148:151],34
R 26,2,49152
M 22,v[156:159],38
L 3
M 22,v[164:167],42
R 30,2,50176
M 22,v[172:175],46
M 18,v[160:163],42
R 34,2,51200
M 18,v[168:171],46
L 3
M 18,v[176:179],50
R 38,2,52224
M 18,v[184:187],54
M 22,v[180:183],50
R 42,2,53248
M 22,v[188:191],54
L 3
M 22,v[196:199],26
R 46,2,54272
M 22,v[204:207],30
M 18,v[192:195],26
R 50,2,55296
M 18,v[200:203],30
L 3
M 18,v[208:211],34
s_waitcnt vmcnt(0)
R 54,2,56320
M 18,v[216:219],38
s_barrier
buffer_store_dword v4,v3,s[36:39],s69 offen offset:0 sc0 sc1
M 22,v[212:215],34
s_mov_b32 s69,s68
s_lshr_b32 s68,s62,4
ds_read_b32 v7,v6 offset:1024
M 22,v[220:223],38
L 3
s_add_u32 s68,s68,s48
M 22,v[228:231],42
M 22,v[236:239],46
M 18,v[224:227],42
M 18,v[232:235],46
L 1
M 18,v[240:243],50
L 0
v_cmp_eq_u32 vcc,s53,v7
M 18,v[248:251],54
M 22,v[244:247],50
M 22,v[252:255],54
s_cmp_eq_u32 s51,1
s_cbranch_scc1 Lg4_pa1_%=
s_mov_b32 s80,0
s_cmp_eq_u32 vcc_lo,-1
s_cbranch_scc0 Lg4_xn1_%=
s_add_u32 m0,s57,0
s_add_u32 s56,s55,0
D 0,56,0
v_lshlrev_b32 v62,16,v58
D 0,56,1024
v_add_f32 v18,v18,v62
D 0,56,2048
v_and_b32 v63,0xffff0000,v58
D 0,56,3072
v_add_f32 v19,v19,v63
s_add_u32 m0,s57,4096
s_add_u32 s56,s56,4096
D 0,56,0
v_lshlrev_b32 v62,16,v59
D 0,56,1024
v_add_f32 v20,v20,v62
D 0,56,2048
v_and_b32 v63,0xffff0000,v59
s_add_u32 s61,s54,s46
s_cmp_ge_u32 s61,s63
s_cselect_b32 s61,s93,s61
s_cselect_b32 s46,s88,s46
s_cselect_b32 s63,s89,s63
s_cselect_b32 s93,s50,s93
s_cselect_b32 s88,8,s88
s_cselect_b32 s89,-1,s89
s_mul_i32 s58,s61,2560
s_add_u32 s58,s58,109891584
s_add_u32 m0,s57,28672
s_add_u32 s56,s55,28672
D 0,56,0
v_add_f32 v21,v21,v63
D 0,56,1024
)"
R"(v_lshlrev_b32 v62,16,v60
D 0,56,2048
v_add_f32 v22,v22,v62
D 0,56,3072
v_and_b32 v63,0xffff0000,v60
s_add_u32 m0,s57,32768
s_add_u32 s56,s56,4096
D 0,56,0
v_add_f32 v23,v23,v63
D 0,56,1024
v_lshlrev_b32 v62,16,v61
D 0,56,2048
v_add_f32 v24,v24,v62
s_mul_i32 s55,s61,114688
s_add_u32 s55,s55,s60
s_sub_u32 s57,s84,s57
s_mov_b32 s54,s61
v_and_b32 v63,0xffff0000,v61
v_add_f32 v25,v25,v63
v_cvt_pk_bf16_f32 v18,v18,v19
v_cvt_pk_bf16_f32 v19,v20,v21
v_cvt_pk_bf16_f32 v20,v22,v23
v_cvt_pk_bf16_f32 v21,v24,v25
buffer_store_dwordx4 v[18:21],v0,s[36:39],s67 offen offset:0 sc0 sc1
s_mov_b32 s72,s69
s_mov_b32 s69,0x7fff0000
s_mov_b32 s67,0x7fff0000
s_mov_b32 s77,0x7fff0000
s_branch Lg4_gox_%=
Lg4_xn1_%=:
s_cmp_ge_u32 s54,256
s_cbranch_scc1 Lg4_fin1_%=
s_add_u32 m0,s59,4096
s_nop 0
buffer_load_dword v8,s[36:39],s58 offen offset:0 sc1 lds
buffer_store_dword v4,v3,s[36:39],s71 offen offset:0 sc0 sc1
s_add_u32 m0,s59,5120
s_nop 0
buffer_load_dword v8,s[36:39],s58 offen offset:0 sc1 lds
buffer_store_dword v4,v3,s[36:39],s71 offen offset:0 sc0 sc1
s_mov_b32 s83,0
v_lshlrev_b32 v62,16,v58
v_add_f32 v18,v18,v62
v_and_b32 v63,0xffff0000,v58
v_add_f32 v19,v19,v63
v_lshlrev_b32 v62,16,v59
v_add_f32 v20,v20,v62
v_and_b32 v63,0xffff0000,v59
v_add_f32 v21,v21,v63
v_lshlrev_b32 v62,16,v60
v_add_f32 v22,v22,v62
v_and_b32 v63,0xffff0000,v60
v_add_f32 v23,v23,v63
v_lshlrev_b32 v62,16,v61
v_add_f32 v24,v24,v62
v_and_b32 v63,0xffff0000,v61
v_add_f32 v25,v25,v63
v_cvt_pk_bf16_f32 v18,v18,v19
v_cvt_pk_bf16_f32 v19,v20,v21
v_cvt_pk_bf16_f32 v20,v22,v23
v_cvt_pk_bf16_f32 v21,v24,v25
buffer_store_dwordx4 v[18:21],v0,s[36:39],s67 offen offset:0 sc0 sc1
s_mov_b32 s72,s69
s_mov_b32 s69,0x7fff0000
s_mov_b32 s67,0x7fff0000
s_mov_b32 s77,0x7fff0000
s_branch Lg4_poll_%=
Lg4_fin1_%=:
v_lshlrev_b32 v62,16,v58
v_add_f32 v18,v18,v62
v_and_b32 v63,0xffff0000,v58
v_add_f32 v19,v19,v63
v_lshlrev_b32 v62,16,v59
v_add_f32 v20,v20,v62
v_and_b32 v63,0xffff0000,v59
v_add_f32 v21,v21,v63
v_lshlrev_b32 v62,16,v60
v_add_f32 v22,v22,v62
v_and_b32 v63,0xffff0000,v60
v_add_f32 v23,v23,v63
v_lshlrev_b32 v62,16,v61
v_add_f32 v24,v24,v62
v_and_b32 v63,0xffff0000,v61
v_add_f32 v25,v25,v63
v_cvt_pk_bf16_f32 v18,v18,v19
v_cvt_pk_bf16_f32 v19,v20,v21
v_cvt_pk_bf16_f32 v20,v22,v23
v_cvt_pk_bf16_f32 v21,v24,v25
buffer_store_dwordx4 v[18:21],v0,s[36:39],s67 offen offset:0 sc0 sc1
s_mov_b32 s72,s69
s_mov_b32 s69,0x7fff0000
s_mov_b32 s67,0x7fff0000
s_mov_b32 s77,0x7fff0000
s_branch Lg4_done_%=
Lg4_Fa0_%=:
s_nop 0
v_mfma_f32_16x16x32_bf16 v[14:17],a[4:7],v[26:29],0
s_add_u32 m0,s57,0
R 46,1,5120
v_lshlrev_b32 v62,16,v58
M 14,a[12:15],30
s_add_u32 s56,s55,0
v_add_f32 v18,v18,v62
v_and_b32 v63,0xffff0000,v58
v_mfma_f32_16x16x32_bf16 v[10:13],a[0:3],v[26:29],0
s_add_u32 s61,s54,s46
R 50,1,6144
v_add_f32 v19,v19,v63
M 10,a[8:11],30
L 3
D 0,56,0
M 10,a[16:19],34
s_cmp_ge_u32 s61,s63
R 54,1,7168
v_lshlrev_b32 v62,16,v59
M 10,a[24:27],38
s_cselect_b32 s61,s93,s61
v_add_f32 v20,v20,v62
v_and_b32 v63,0xffff0000,v59
M 14,a[20:23],34
s_cselect_b32 s46,s88,s46
R 26,1,8192
v_add_f32 v21,v21,v63
M 14,a[28:31],38
L 3
v_lshlrev_b32 v62,16,v60
v_add_f32 v22,v22,v62
M 14,a[36:39],42
s_cselect_b32 s63,s89,s63
R 30,1,9216
v_and_b32 v63,0xffff0000,v60
M 14,a[44:47],46
D 0,56,1024
M 10,a[32:35],42
s_cselect_b32 s93,s50,s93
R 34,1,10240
v_add_f32 v23,v23,v63
M 10,a[40:43],46
L 3
v_lshlrev_b32 v62,16,v61
v_add_f32 v24,v24,v62
M 10,a[48:51],50
s_cselect_b32 s88,8,s88
R 38,1,11264
v_and_b32 v63,0xffff0000,v61
M 10,a[56:59],54
buffer_load_dwordx4 v[58:61],v0,s[36:39],s74 offen offset:0 sc1
M 14,a[52:55],50
s_cselect_b32 s89,-1,s89
R 42,1,12288
v_add_f32 v25,v25,v63
M 14,a[60:63],54
L 3
v_cvt_pk_bf16_f32 v18,v18,v19
v_cvt_pk_bf16_f32 v19,v20,v21
M 14,a[68:71],26
s_mul_i32 s58,s61,2560
R 46,1,13312
v_cvt_pk_bf16_f32 v20,v22,v23
M 14,a[76:79],30
D 0,56,2048
M 10,a[64:67],26
s_add_u32 s58,s58,109891584
R 50,1,14336
v_cvt_pk_bf16_f32 v21,v24,v25
M 10,a[72:75],30
L 3
v_lshrrev_b32 v62,8,v0
v_lshl_add_u32 v62,v62,5,v0
M 10,a[80:83],34
s_sub_u32 s75,s67,s85
R 54,1,15360
v_add_u32 v62,s73,v62
M 10,a[88:91],38
s_mov_b64 s[40:41],s[86:87]
ds_write_b128 v62,v[18:21] offset:0
M 14,a[84:87],34
s_mov_b32 s42,4194304
R 26,1,16384
ds_read2_b32 v[22:23],v5 offset0:0 offset1:16
M 14,a[92:95],38
L 5
s_mov_b32 s43,0x00020000
ds_read2_b32 v[24:25],v5 offset0:32 offset1:48
M 14,a[100:103],42
s_add_u32 s64,s75,0
s_lshl_b32 s62,s54,15
R 30,1,17408
M 14,a[108:111],46
D 0,56,3072
M 10,a[96:99],42
s_add_u32 m0,s57,4096
s_add_u32 s56,s56,4096
R 34,1,18432
M 10,a[104:107],46
L 6
s_mov_b32 s66,s65
s_add_u32 s74,s49,s62
M 10,a[112:115],50
s_add_u32 s65,s47,s62
s_mov_b32 s54,s61
R 38,1,19456
M 10,a[120:123],54
L 3
M 14,a[116:119],50
R 42,1,20480
M 14,a[124:127],54
L 3
D 0,56,0
M 14,a[132:135],26
R 46,1,21504
M 14,a[140:143],30
M 10,a[128:131],26
R 50,1,22528
M 10,a[136:139],30
L 3
M 10,a[144:147],34
R 54,1,23552
M 10,a[152:155],38
M 14,a[148:151],34
R 26,1,24576
M 14,a[156:159],38
L 3
D 0,56,1024
M 14,a[164:167],42
R 30,1,25600
M 14,a[172:175],46
M 10,a[160:163],42
R 34,1,26624
M 10,a[168:171],46
L 3
M 10,a[176:179],50
s_waitcnt vmcnt(8)
R 38,1,27648
M 10,a[184:187],54
s_barrier
M 14,a[180:183],50
R 42,1,28672
M 14,a[188:191],54
L 3
D 0,56,2048
M 14,a[196:199],26
s_add_u32 m0,s59,0
s_add_u32 s56,s55,28672
R 46,1,29696
M 14,a[204:207],30
s_mul_i32 s55,s61,114688
s_add_u32 s55,s55,s60
M 10,a[192:195],26
R 50,1,30720
M 10,a[200:203],30
L 3
M 10,a[208:211],34
R 54,1,31744
M 10,a[216:219],38
M 14,a[212:215],34
R 26,1,32768
M 14,a[220:223],38
L 3
buffer_load_dword v8,s[36:39],s58 offen offset:0 sc1 lds
M 14,a[228:231],42
s_add_u32 m0,s57,28672
R 30,1,33792
M 14,a[236:239],46
D 0,56,0
M 10,a[224:227],42
R 34,1,34816
M 10,a[232:235],46
L 3
M 10,a[240:243],50
R 38,1,35840
M 10,a[248:251],54
M 14,a[244:247],50
R 42,1,36864
M 14,a[252:255],54
L 3
D 0,56,1024
M 14,v[68:71],26
R 46,1,37888
M 14,v[76:79],30
M 10,v[64:67],26
R 50,1,38912
M 10,v[72:75],30
L 3
M 10,v[80:83],34
R 54,1,39936
M 10,v[88:91],38
D 0,56,2048
M 14,v[84:87],34
R 26,1,40960
M 14,v[92:95],38
L 3
M 14,v[100:103],42
R 30,1,41984
M 14,v[108:111],46
M 10,v[96:99],42
R 34,1,43008
M 10,v[104:107],46
L 3
D 0,56,3072
M 10,v[112:115],50
s_add_u32 m0,s57,32768
s_add_u32 s56,s56,4096
R 38,1,44032
M 10,v[120:123],54
s_sub_u32 s57,s84,s57
M 14,v[116:119],50
R 42,1,45056
M 14,v[124:127],54
L 3
M 14,v[132:135],26
R 46,1,46080
M 14,v[140:143],30
D 0,56,0
M 10,v[128:131],26
R 50,1,47104
M 10,v[136:139],30
L 3
M 10,v[144:147],34
R 54,1,48128
M 10,v[152:155],38
M 14,v[148:151],34
R 26,1,49152
M 14,v[156:159],38
L 3
D 0,56,1024
M 14,v[164:167],42
R 30,1,50176
M 14,v[172:175],46
M 10,v[160:163],42
R 34,1,51200
M 10,v[168:171],46
L 3
M 10,v[176:179],50
R 38,1,52224
M 10,v[184:187],54
D 0,56,2048
M 14,v[180:183],50
R 42,1,53248
M 14,v[188:191],54
L 3
M 14,v[196:199],26
R 46,1,54272
M 14,v[204:207],30
M 10,v[192:195],26
R 50,1,55296
M 10,v[200:203],30
L 3
M 10,v[208:211],34
s_waitcnt vmcnt(7)
R 54,1,56320
M 10,v[216:219],38
s_barrier
ds_read_b32 v7,v6 offset:0
M 14,v[212:215],34
R 26,2,0
M 14,v[220:223],38
L 4
M 14,v[228:231],42
R 30,2,1024
M 14,v[236:239],46
buffer_atomic_pk_add_bf16 v22,v9,s[40:43],s64 offen sc1
M 10,v[224:227],42
s_add_u32 s64,s75,8192
R 34,2,2048
M 10,v[232:235],46
L 4
buffer_atomic_pk_add_bf16 v23,v9,s[40:43],s64 offen sc1
M 10,v[240:243],50
s_add_u32 s64,s75,16384
L 3
R 38,2,3072
v_cmp_eq_u32 vcc,s53,v7
M 10,v[248:251],54
buffer_atomic_pk_add_bf16 v24,v9,s[40:43],s64 offen sc1
M 14,v[244:247],50
L 2
s_add_u32 s64,s75,24576
s_cmp_eq_u32 vcc_lo,-1
R 42,2,4096
M 14,v[252:255],54
buffer_atomic_pk_add_bf16 v25,v9,s[40:43],s64 offen sc1
s_cbranch_scc0 Lg4_Ka1_%=
Lg4_Fa1_%=:
s_nop 0
v_mfma_f32_16x16x32_bf16 v[22:25],a[4:7],v[26:29],0
s_add_u32 m0,s57,0
R 46,2,5120
v_lshlrev_b32 v62,16,v58
M 22,a[12:15],30
s_add_u32 s56,s55,0
v_add_f32 v10,v10,v62
v_and_b32 v63,0xffff0000,v58
v_mfma_f32_16x16x32_bf16 v[18:21],a[0:3],v[26:29],0
s_add_u32 s61,s54,s46
R 50,2,6144
v_add_f32 v11,v11,v63
M 18,a[8:11],30
L 3
D 0,56,0
M 18,a[16:19],34
s_cmp_ge_u32 s61,s63
R 54,2,7168
v_lshlrev_b32 v62,16,v59
M 18,a[24:27],38
s_cselect_b32 s61,s93,s61
v_add_f32 v12,v12,v62
v_and_b32 v63,0xffff0000,v59
M 22,a[20:23],34
s_cselect_b32 s46,s88,s46
R 26,2,8192
v_add_f32 v13,v13,v63
M 22,a[28:31],38
L 3
v_lshlrev_b32 v62,16,v60
v_add_f32 v14,v14,v62
M 22,a[36:39],42
s_cselect_b32 s63,s89,s63
R 30,2,9216
v_and_b32 v63,0xffff0000,v60
M 22,a[44:47],46
D 0,56,1024
M 18,a[32:35],42
s_cselect_b32 s93,s50,s93
R 34,2,10240
v_add_f32 v15,v15,v63
M 18,a[40:43],46
L 3
v_lshlrev_b32 v62,16,v61
v_add_f32 v16,v16,v62
M 18,a[48:51],50
s_cselect_b32 s88,8,s88
R 38,2,11264
v_and_b32 v63,0xffff0000,v61
M 18,a[56:59],54
buffer_load_dwordx4 v[58:61],v0,s[36:39],s74 offen offset:0 sc1
M 22,a[52:55],50
s_cselect_b32 s89,-1,s89
R 42,2,12288
v_add_f32 v17,v17,v63
M 22,a[60:63],54
L 3
v_cvt_pk_bf16_f32 v10,v10,v11
v_cvt_pk_bf16_f32 v11,v12,v13
M 22,a[68:71],26
s_mul_i32 s58,s61,2560
R 46,2,13312
v_cvt_pk_bf16_f32 v12,v14,v15
M 22,a[76:79],30
D 0,56,2048
M 18,a[64:67],26
s_add_u32 s58,s58,109891584
R 50,2,14336
v_cvt_pk_bf16_f32 v13,v16,v17
M 18,a[72:75],30
L 3
v_lshrrev_b32 v62,8,v0
v_lshl_add_u32 v62,v62,5,v0
M 18,a[80:83],34
s_sub_u32 s75,s66,s85
R 54,2,15360
v_add_u32 v62,s73,v62
M 18,a[88:91],38
s_mov_b64 s[40:41],s[86:87]
ds_write_b128 v62,v[10:13] offset:0
M 22,a[84:87],34
s_mov_b32 s42,4194304
R 26,2,16384
ds_read2_b32 v[14:15],v5 offset0:0 offset1:16
M 22,a[92:95],38
L 5
s_mov_b32 s43,0x00020000
ds_read2_b32 v[16:17],v5 offset0:32 offset1:48
M 22,a[100:103],42
s_add_u32 s64,s75,0
s_lshl_b32 s62,s54,15
R 30,2,17408
M 22,a[108:111],46
D 0,56,3072
M 18,a[96:99],42
s_add_u32 m0,s57,4096
s_add_u32 s56,s56,4096
R 34,2,18432
M 18,a[104:107],46
L 6
s_mov_b32 s67,s65
s_add_u32 s74,s49,s62
M 18,a[112:115],50
s_add_u32 s65,s47,s62
s_mov_b32 s54,s61
R 38,2,19456
M 18,a[120:123],54
L 3
M 22,a[116:119],50
R 42,2,20480
M 22,a[124:127],54
L 3
D 0,56,0
M 22,a[132:135],26
R 46,2,21504
)"
R"(M 22,a[140:143],30
M 18,a[128:131],26
R 50,2,22528
M 18,a[136:139],30
L 3
M 18,a[144:147],34
R 54,2,23552
M 18,a[152:155],38
M 22,a[148:151],34
R 26,2,24576
M 22,a[156:159],38
L 3
D 0,56,1024
M 22,a[164:167],42
R 30,2,25600
M 22,a[172:175],46
M 18,a[160:163],42
R 34,2,26624
M 18,a[168:171],46
L 3
M 18,a[176:179],50
s_waitcnt vmcnt(8)
R 38,2,27648
M 18,a[184:187],54
s_barrier
M 22,a[180:183],50
R 42,2,28672
M 22,a[188:191],54
L 3
D 0,56,2048
M 22,a[196:199],26
s_add_u32 m0,s59,1024
s_add_u32 s56,s55,28672
R 46,2,29696
M 22,a[204:207],30
s_mul_i32 s55,s61,114688
s_add_u32 s55,s55,s60
M 18,a[192:195],26
R 50,2,30720
M 18,a[200:203],30
L 3
M 18,a[208:211],34
R 54,2,31744
M 18,a[216:219],38
M 22,a[212:215],34
R 26,2,32768
M 22,a[220:223],38
L 3
buffer_load_dword v8,s[36:39],s58 offen offset:0 sc1 lds
M 22,a[228:231],42
s_add_u32 m0,s57,28672
R 30,2,33792
M 22,a[236:239],46
D 0,56,0
M 18,a[224:227],42
R 34,2,34816
M 18,a[232:235],46
L 3
M 18,a[240:243],50
R 38,2,35840
M 18,a[248:251],54
M 22,a[244:247],50
R 42,2,36864
M 22,a[252:255],54
L 3
D 0,56,1024
M 22,v[68:71],26
R 46,2,37888
M 22,v[76:79],30
M 18,v[64:67],26
R 50,2,38912
M 18,v[72:75],30
L 3
M 18,v[80:83],34
R 54,2,39936
M 18,v[88:91],38
D 0,56,2048
M 22,v[84:87],34
R 26,2,40960
M 22,v[92:95],38
L 3
M 22,v[100:103],42
R 30,2,41984
M 22,v[108:111],46
M 18,v[96:99],42
R 34,2,43008
M 18,v[104:107],46
L 3
D 0,56,3072
M 18,v[112:115],50
s_add_u32 m0,s57,32768
s_add_u32 s56,s56,4096
R 38,2,44032
M 18,v[120:123],54
s_sub_u32 s57,s84,s57
M 22,v[116:119],50
R 42,2,45056
M 22,v[124:127],54
L 3
M 22,v[132:135],26
R 46,2,46080
M 22,v[140:143],30
D 0,56,0
M 18,v[128:131],26
R 50,2,47104
M 18,v[136:139],30
L 3
M 18,v[144:147],34
R 54,2,48128
M 18,v[152:155],38
M 22,v[148:151],34
R 26,2,49152
M 22,v[156:159],38
L 3
D 0,56,1024
M 22,v[164:167],42
R 30,2,50176
M 22,v[172:175],46
M 18,v[160:163],42
R 34,2,51200
M 18,v[168:171],46
L 3
M 18,v[176:179],50
R 38,2,52224
M 18,v[184:187],54
D 0,56,2048
M 22,v[180:183],50
R 42,2,53248
M 22,v[188:191],54
L 3
M 22,v[196:199],26
R 46,2,54272
M 22,v[204:207],30
M 18,v[192:195],26
R 50,2,55296
M 18,v[200:203],30
L 3
M 18,v[208:211],34
s_waitcnt vmcnt(7)
R 54,2,56320
M 18,v[216:219],38
s_barrier
ds_read_b32 v7,v6 offset:1024
M 22,v[212:215],34
R 26,1,0
M 22,v[220:223],38
L 4
M 22,v[228:231],42
R 30,1,1024
M 22,v[236:239],46
buffer_atomic_pk_add_bf16 v14,v9,s[40:43],s64 offen sc1
M 18,v[224:227],42
s_add_u32 s64,s75,8192
R 34,1,2048
M 18,v[232:235],46
L 4
buffer_atomic_pk_add_bf16 v15,v9,s[40:43],s64 offen sc1
M 18,v[240:243],50
s_add_u32 s64,s75,16384
L 3
R 38,1,3072
v_cmp_eq_u32 vcc,s53,v7
M 18,v[248:251],54
buffer_atomic_pk_add_bf16 v16,v9,s[40:43],s64 offen sc1
M 22,v[244:247],50
L 2
s_add_u32 s64,s75,24576
s_cmp_eq_u32 vcc_lo,-1
R 42,1,4096
M 22,v[252:255],54
buffer_atomic_pk_add_bf16 v17,v9,s[40:43],s64 offen sc1
s_cbranch_scc1 Lg4_Fa0_%=
Lg4_Ka0_%=:
Lg4_La0_%=:
s_nop 0
v_mfma_f32_16x16x32_bf16 v[14:17],a[4:7],v[26:29],0
s_add_u32 m0,s59,3072
R 46,1,5120
v_lshlrev_b32 v62,16,v58
M 14,a[12:15],30
buffer_load_dword v8,s[36:39],s58 offen offset:0 sc1 lds
v_mfma_f32_16x16x32_bf16 v[10:13],a[0:3],v[26:29],0
s_sub_u32 s75,s67,s85
R 50,1,6144
v_add_f32 v18,v18,v62
M 10,a[8:11],30
L 3
v_and_b32 v63,0xffff0000,v58
v_add_f32 v19,v19,v63
M 10,a[16:19],34
s_mov_b64 s[40:41],s[86:87]
R 54,1,7168
v_lshlrev_b32 v62,16,v59
M 10,a[24:27],38
s_mov_b32 s42,4194304
v_add_f32 v20,v20,v62
v_and_b32 v63,0xffff0000,v59
M 14,a[20:23],34
s_mov_b32 s43,0x00020000
R 26,1,8192
v_add_f32 v21,v21,v63
M 14,a[28:31],38
L 3
v_lshlrev_b32 v62,16,v60
v_add_f32 v22,v22,v62
M 14,a[36:39],42
s_add_u32 s64,s75,0
R 30,1,9216
v_and_b32 v63,0xffff0000,v60
M 14,a[44:47],46
s_lshl_b32 s62,s54,15
v_add_f32 v23,v23,v63
v_lshlrev_b32 v62,16,v61
M 10,a[32:35],42
s_mov_b32 s66,s65
R 34,1,10240
v_add_f32 v24,v24,v62
M 10,a[40:43],46
L 3
v_and_b32 v63,0xffff0000,v61
v_add_f32 v25,v25,v63
M 10,a[48:51],50
s_add_u32 s65,s47,s62
R 38,1,11264
v_cvt_pk_bf16_f32 v18,v18,v19
M 10,a[56:59],54
buffer_load_dwordx4 v[58:61],v0,s[36:39],s74 offen offset:0 sc1
M 14,a[52:55],50
s_add_u32 s74,s49,s62
R 42,1,12288
v_cvt_pk_bf16_f32 v19,v20,v21
M 14,a[60:63],54
L 3
v_cvt_pk_bf16_f32 v20,v22,v23
v_cvt_pk_bf16_f32 v21,v24,v25
M 14,a[68:71],26
R 46,1,13312
v_lshrrev_b32 v62,8,v0
M 14,a[76:79],30
v_lshl_add_u32 v62,v62,5,v0
v_add_u32 v62,s73,v62
M 10,a[64:67],26
R 50,1,14336
M 10,a[72:75],30
L 3
ds_write_b128 v62,v[18:21] offset:0
M 10,a[80:83],34
R 54,1,15360
ds_read2_b32 v[22:23],v5 offset0:0 offset1:16
M 10,a[88:91],38
ds_read2_b32 v[24:25],v5 offset0:32 offset1:48
M 14,a[84:87],34
R 26,1,16384
M 14,a[92:95],38
L 6
M 14,a[100:103],42
R 30,1,17408
M 14,a[108:111],46
M 10,a[96:99],42
R 34,1,18432
M 10,a[104:107],46
L 5
L 3
M 10,a[112:115],50
R 38,1,19456
M 10,a[120:123],54
buffer_atomic_pk_add_bf16 v22,v9,s[40:43],s64 offen sc1
M 14,a[116:119],50
s_add_u32 s64,s75,8192
R 42,1,20480
M 14,a[124:127],54
L 3
buffer_atomic_pk_add_bf16 v23,v9,s[40:43],s64 offen sc1
M 14,a[132:135],26
s_add_u32 s64,s75,16384
R 46,1,21504
M 14,a[140:143],30
buffer_atomic_pk_add_bf16 v24,v9,s[40:43],s64 offen sc1
M 10,a[128:131],26
s_add_u32 s64,s75,24576
R 50,1,22528
M 10,a[136:139],30
L 3
buffer_atomic_pk_add_bf16 v25,v9,s[40:43],s64 offen sc1
M 10,a[144:147],34
R 54,1,23552
M 10,a[152:155],38
M 14,a[148:151],34
R 26,1,24576
M 14,a[156:159],38
L 3
M 14,a[164:167],42
R 30,1,25600
M 14,a[172:175],46
M 10,a[160:163],42
R 34,1,26624
M 10,a[168:171],46
L 3
M 10,a[176:179],50
s_waitcnt vmcnt(5)
R 38,1,27648
M 10,a[184:187],54
s_barrier
ds_read_b32 v7,v6 offset:3072
M 14,a[180:183],50
R 42,1,28672
M 14,a[188:191],54
L 4
M 14,a[196:199],26
R 46,1,29696
M 14,a[204:207],30
M 10,a[192:195],26
R 50,1,30720
M 10,a[200:203],30
L 4
L 3
v_cmp_eq_u32 vcc,s53,v7
M 10,a[208:211],34
R 54,1,31744
M 10,a[216:219],38
M 14,a[212:215],34
s_cmp_eq_u32 vcc_lo,-1
R 26,1,32768
s_cbranch_scc0 Lg4_Xa0_%=
M 14,a[220:223],38
L 3
s_add_u32 m0,s57,0
s_add_u32 s56,s55,0
M 14,a[228:231],42
s_add_u32 s61,s54,s46
s_cmp_ge_u32 s61,s63
s_cselect_b32 s61,s93,s61
R 30,1,33792
M 14,a[236:239],46
s_mul_i32 s58,s61,2560
D 0,56,0
M 10,a[224:227],42
s_cselect_b32 s46,s88,s46
s_cselect_b32 s63,s89,s63
R 34,1,34816
M 10,a[232:235],46
L 3
s_cselect_b32 s93,s50,s93
s_cselect_b32 s88,8,s88
M 10,a[240:243],50
s_cselect_b32 s89,-1,s89
s_add_u32 s58,s58,109891584
R 38,1,35840
M 10,a[248:251],54
D 0,56,1024
M 14,a[244:247],50
s_mov_b32 s54,s61
R 42,1,36864
M 14,a[252:255],54
L 3
M 14,v[68:71],26
R 46,1,37888
M 14,v[76:79],30
D 0,56,2048
M 10,v[64:67],26
R 50,1,38912
M 10,v[72:75],30
L 3
M 10,v[80:83],34
R 54,1,39936
M 10,v[88:91],38
D 0,56,3072
M 14,v[84:87],34
s_add_u32 m0,s57,4096
s_add_u32 s56,s56,4096
R 26,1,40960
M 14,v[92:95],38
L 3
M 14,v[100:103],42
R 30,1,41984
M 14,v[108:111],46
D 0,56,0
M 10,v[96:99],42
R 34,1,43008
M 10,v[104:107],46
L 3
M 10,v[112:115],50
R 38,1,44032
M 10,v[120:123],54
D 0,56,1024
M 14,v[116:119],50
R 42,1,45056
M 14,v[124:127],54
L 3
M 14,v[132:135],26
R 46,1,46080
M 14,v[140:143],30
D 0,56,2048
M 10,v[128:131],26
s_add_u32 m0,s59,0
s_add_u32 s56,s55,28672
R 50,1,47104
M 10,v[136:139],30
L 3
buffer_load_dword v8,s[36:39],s58 offen offset:0 sc1 lds
M 10,v[144:147],34
s_add_u32 m0,s57,28672
s_mul_i32 s55,s61,114688
R 54,1,48128
M 10,v[152:155],38
s_add_u32 s55,s55,s60
M 14,v[148:151],34
R 26,1,49152
M 14,v[156:159],38
L 3
D 0,56,0
M 14,v[164:167],42
R 30,1,50176
M 14,v[172:175],46
M 10,v[160:163],42
R 34,1,51200
M 10,v[168:171],46
L 3
D 0,56,1024
M 10,v[176:179],50
R 38,1,52224
M 10,v[184:187],54
D 0,56,2048
M 14,v[180:183],50
R 42,1,53248
M 14,v[188:191],54
L 3
M 14,v[196:199],26
R 46,1,54272
M 14,v[204:207],30
D 0,56,3072
M 10,v[192:195],26
s_add_u32 m0,s57,32768
s_add_u32 s56,s56,4096
R 50,1,55296
M 10,v[200:203],30
L 3
s_sub_u32 s57,s84,s57
M 10,v[208:211],34
s_waitcnt vmcnt(4)
R 54,1,56320
M 10,v[216:219],38
s_barrier
ds_read_b32 v7,v6 offset:0
M 14,v[212:215],34
R 26,2,0
M 14,v[220:223],38
L 4
D 0,56,0
M 14,v[228:231],42
R 30,2,1024
M 14,v[236:239],46
D 0,56,1024
M 10,v[224:227],42
R 34,2,2048
M 10,v[232:235],46
L 4
D 0,56,2048
M 10,v[240:243],50
L 3
R 38,2,3072
v_cmp_eq_u32 vcc,s53,v7
M 10,v[248:251],54
M 14,v[244:247],50
L 2
s_cmp_eq_u32 vcc_lo,-1
R 42,2,4096
M 14,v[252:255],54
buffer_store_dword v4,v3,s[36:39],s71 offen offset:0 sc0 sc1
buffer_store_dword v4,v3,s[36:39],s71 offen offset:0 sc0 sc1
buffer_store_dword v4,v3,s[36:39],s71 offen offset:0 sc0 sc1
buffer_store_dword v4,v3,s[36:39],s71 offen offset:0 sc0 sc1
s_cbranch_scc1 Lg4_Fa1_%=
s_branch Lg4_Ka1_%=
Lg4_Xa0_%=:
M 14,a[220:223],38
L 3
s_add_u32 m0,s59,0
M 14,a[228:231],42
R 30,1,33792
M 14,a[236:239],46
buffer_load_dword v8,s[36:39],s58 offen offset:0 sc1 lds
M 10,a[224:227],42
R 34,1,34816
M 10,a[232:235],46
L 3
M 10,a[240:243],50
R 38,1,35840
M 10,a[248:251],54
M 14,a[244:247],50
R 42,1,36864
M 14,a[252:255],54
L 3
M 14,v[68:71],26
R 46,1,37888
M 14,v[76:79],30
M 10,v[64:67],26
R 50,1,38912
M 10,v[72:75],30
L 3
)"
R"(M 10,v[80:83],34
R 54,1,39936
M 10,v[88:91],38
M 14,v[84:87],34
R 26,1,40960
M 14,v[92:95],38
L 3
M 14,v[100:103],42
R 30,1,41984
M 14,v[108:111],46
M 10,v[96:99],42
R 34,1,43008
M 10,v[104:107],46
L 3
M 10,v[112:115],50
R 38,1,44032
M 10,v[120:123],54
M 14,v[116:119],50
R 42,1,45056
M 14,v[124:127],54
L 3
M 14,v[132:135],26
R 46,1,46080
M 14,v[140:143],30
M 10,v[128:131],26
R 50,1,47104
M 10,v[136:139],30
L 3
M 10,v[144:147],34
R 54,1,48128
M 10,v[152:155],38
M 14,v[148:151],34
R 26,1,49152
M 14,v[156:159],38
L 3
M 14,v[164:167],42
R 30,1,50176
M 14,v[172:175],46
M 10,v[160:163],42
R 34,1,51200
M 10,v[168:171],46
L 3
M 10,v[176:179],50
R 38,1,52224
M 10,v[184:187],54
M 14,v[180:183],50
R 42,1,53248
M 14,v[188:191],54
L 3
M 14,v[196:199],26
R 46,1,54272
M 14,v[204:207],30
M 10,v[192:195],26
R 50,1,55296
M 10,v[200:203],30
L 3
M 10,v[208:211],34
s_waitcnt vmcnt(0)
R 54,1,56320
M 10,v[216:219],38
s_barrier
ds_read_b32 v7,v6 offset:0
M 14,v[212:215],34
M 14,v[220:223],38
L 3
M 14,v[228:231],42
M 14,v[236:239],46
M 10,v[224:227],42
M 10,v[232:235],46
L 1
L 0
M 10,v[240:243],50
v_cmp_eq_u32 vcc,s53,v7
M 10,v[248:251],54
M 14,v[244:247],50
M 14,v[252:255],54
Lg4_pa0_%=:
s_mov_b32 s80,1
s_cmp_eq_u32 vcc_lo,-1
s_cbranch_scc0 Lg4_xna0_%=
s_add_u32 m0,s57,0
s_add_u32 s56,s55,0
D 0,56,0
s_sub_u32 s75,s66,s85
v_lshlrev_b32 v62,16,v58
D 0,56,1024
v_add_f32 v10,v10,v62
D 0,56,2048
v_and_b32 v63,0xffff0000,v58
D 0,56,3072
v_add_f32 v11,v11,v63
s_add_u32 m0,s57,4096
s_add_u32 s56,s56,4096
D 0,56,0
v_lshlrev_b32 v62,16,v59
D 0,56,1024
v_add_f32 v12,v12,v62
D 0,56,2048
v_and_b32 v63,0xffff0000,v59
s_add_u32 s61,s54,s46
s_cmp_ge_u32 s61,s63
s_cselect_b32 s61,s93,s61
s_cselect_b32 s46,s88,s46
s_cselect_b32 s63,s89,s63
s_cselect_b32 s93,s50,s93
s_cselect_b32 s88,8,s88
s_cselect_b32 s89,-1,s89
s_mul_i32 s58,s61,2560
s_add_u32 s58,s58,109891584
s_add_u32 m0,s59,2048
s_nop 0
buffer_load_dword v8,s[36:39],s58 offen offset:0 sc1 lds
v_add_f32 v13,v13,v63
s_add_u32 m0,s57,28672
s_add_u32 s56,s55,28672
D 0,56,0
v_lshlrev_b32 v62,16,v60
D 0,56,1024
v_add_f32 v14,v14,v62
D 0,56,2048
v_and_b32 v63,0xffff0000,v60
D 0,56,3072
v_add_f32 v15,v15,v63
s_add_u32 m0,s57,32768
s_add_u32 s56,s56,4096
D 0,56,0
v_lshlrev_b32 v62,16,v61
D 0,56,1024
v_add_f32 v16,v16,v62
D 0,56,2048
v_and_b32 v63,0xffff0000,v61
s_mul_i32 s55,s61,114688
s_add_u32 s55,s55,s60
s_sub_u32 s57,s84,s57
s_mov_b32 s54,s61
v_add_f32 v17,v17,v63
v_cvt_pk_bf16_f32 v10,v10,v11
v_cvt_pk_bf16_f32 v11,v12,v13
v_cvt_pk_bf16_f32 v12,v14,v15
v_cvt_pk_bf16_f32 v13,v16,v17
v_lshrrev_b32 v62,8,v0
v_lshl_add_u32 v62,v62,5,v0
v_add_u32 v62,s73,v62
ds_write_b128 v62,v[10:13] offset:0
ds_read2_b32 v[14:15],v5 offset0:0 offset1:16
ds_read2_b32 v[16:17],v5 offset0:32 offset1:48
s_mov_b64 s[40:41],s[86:87]
s_mov_b32 s42,4194304
s_mov_b32 s43,0x00020000
L 0
s_add_u32 s64,s75,0
buffer_atomic_pk_add_bf16 v14,v9,s[40:43],s64 offen sc1
s_add_u32 s64,s75,8192
buffer_atomic_pk_add_bf16 v15,v9,s[40:43],s64 offen sc1
s_add_u32 s64,s75,16384
buffer_atomic_pk_add_bf16 v16,v9,s[40:43],s64 offen sc1
s_add_u32 s64,s75,24576
buffer_atomic_pk_add_bf16 v17,v9,s[40:43],s64 offen sc1
s_mov_b32 s72,0x7fff0000
s_mov_b32 s69,0x7fff0000
s_mov_b32 s66,0x7fff0000
s_mov_b32 s76,0x7fff0000
s_waitcnt vmcnt(11)
s_barrier
s_branch Lg4_st1_%=
Lg4_xna0_%=:
s_cmp_ge_u32 s54,256
s_cbranch_scc1 Lg4_fina0_%=
s_add_u32 m0,s59,4096
s_nop 0
buffer_load_dword v8,s[36:39],s58 offen offset:0 sc1 lds
buffer_store_dword v4,v3,s[36:39],s71 offen offset:0 sc0 sc1
s_add_u32 m0,s59,5120
s_nop 0
buffer_load_dword v8,s[36:39],s58 offen offset:0 sc1 lds
buffer_store_dword v4,v3,s[36:39],s71 offen offset:0 sc0 sc1
s_mov_b32 s83,0
s_sub_u32 s75,s66,s85
v_lshlrev_b32 v62,16,v58
v_add_f32 v10,v10,v62
v_and_b32 v63,0xffff0000,v58
v_add_f32 v11,v11,v63
v_lshlrev_b32 v62,16,v59
v_add_f32 v12,v12,v62
v_and_b32 v63,0xffff0000,v59
v_add_f32 v13,v13,v63
v_lshlrev_b32 v62,16,v60
v_add_f32 v14,v14,v62
v_and_b32 v63,0xffff0000,v60
v_add_f32 v15,v15,v63
v_lshlrev_b32 v62,16,v61
v_add_f32 v16,v16,v62
v_and_b32 v63,0xffff0000,v61
v_add_f32 v17,v17,v63
v_cvt_pk_bf16_f32 v10,v10,v11
v_cvt_pk_bf16_f32 v11,v12,v13
v_cvt_pk_bf16_f32 v12,v14,v15
v_cvt_pk_bf16_f32 v13,v16,v17
v_lshrrev_b32 v62,8,v0
v_lshl_add_u32 v62,v62,5,v0
v_add_u32 v62,s73,v62
ds_write_b128 v62,v[10:13] offset:0
ds_read2_b32 v[14:15],v5 offset0:0 offset1:16
ds_read2_b32 v[16:17],v5 offset0:32 offset1:48
s_mov_b64 s[40:41],s[86:87]
s_mov_b32 s42,4194304
s_mov_b32 s43,0x00020000
L 0
s_add_u32 s64,s75,0
buffer_atomic_pk_add_bf16 v14,v9,s[40:43],s64 offen sc1
s_add_u32 s64,s75,8192
buffer_atomic_pk_add_bf16 v15,v9,s[40:43],s64 offen sc1
s_add_u32 s64,s75,16384
buffer_atomic_pk_add_bf16 v16,v9,s[40:43],s64 offen sc1
s_add_u32 s64,s75,24576
buffer_atomic_pk_add_bf16 v17,v9,s[40:43],s64 offen sc1
s_mov_b32 s72,0x7fff0000
s_mov_b32 s69,0x7fff0000
s_mov_b32 s66,0x7fff0000
s_mov_b32 s76,0x7fff0000
s_branch Lg4_poll_%=
Lg4_fina0_%=:
s_sub_u32 s75,s66,s85
v_lshlrev_b32 v62,16,v58
v_add_f32 v10,v10,v62
v_and_b32 v63,0xffff0000,v58
v_add_f32 v11,v11,v63
v_lshlrev_b32 v62,16,v59
v_add_f32 v12,v12,v62
v_and_b32 v63,0xffff0000,v59
v_add_f32 v13,v13,v63
v_lshlrev_b32 v62,16,v60
v_add_f32 v14,v14,v62
v_and_b32 v63,0xffff0000,v60
v_add_f32 v15,v15,v63
v_lshlrev_b32 v62,16,v61
v_add_f32 v16,v16,v62
v_and_b32 v63,0xffff0000,v61
v_add_f32 v17,v17,v63
v_cvt_pk_bf16_f32 v10,v10,v11
v_cvt_pk_bf16_f32 v11,v12,v13
v_cvt_pk_bf16_f32 v12,v14,v15
v_cvt_pk_bf16_f32 v13,v16,v17
v_lshrrev_b32 v62,8,v0
v_lshl_add_u32 v62,v62,5,v0
v_add_u32 v62,s73,v62
ds_write_b128 v62,v[10:13] offset:0
ds_read2_b32 v[14:15],v5 offset0:0 offset1:16
ds_read2_b32 v[16:17],v5 offset0:32 offset1:48
s_mov_b64 s[40:41],s[86:87]
s_mov_b32 s42,4194304
s_mov_b32 s43,0x00020000
L 0
s_add_u32 s64,s75,0
buffer_atomic_pk_add_bf16 v14,v9,s[40:43],s64 offen sc1
s_add_u32 s64,s75,8192
buffer_atomic_pk_add_bf16 v15,v9,s[40:43],s64 offen sc1
s_add_u32 s64,s75,16384
buffer_atomic_pk_add_bf16 v16,v9,s[40:43],s64 offen sc1
s_add_u32 s64,s75,24576
buffer_atomic_pk_add_bf16 v17,v9,s[40:43],s64 offen sc1
s_mov_b32 s72,0x7fff0000
s_mov_b32 s69,0x7fff0000
s_mov_b32 s66,0x7fff0000
s_mov_b32 s76,0x7fff0000
s_branch Lg4_done_%=
Lg4_Ka1_%=:
Lg4_La1_%=:
s_nop 0
v_mfma_f32_16x16x32_bf16 v[22:25],a[4:7],v[26:29],0
s_add_u32 m0,s59,3072
R 46,2,5120
v_lshlrev_b32 v62,16,v58
M 22,a[12:15],30
buffer_load_dword v8,s[36:39],s58 offen offset:0 sc1 lds
v_mfma_f32_16x16x32_bf16 v[18:21],a[0:3],v[26:29],0
s_sub_u32 s75,s66,s85
R 50,2,6144
v_add_f32 v10,v10,v62
M 18,a[8:11],30
L 3
v_and_b32 v63,0xffff0000,v58
v_add_f32 v11,v11,v63
M 18,a[16:19],34
s_mov_b64 s[40:41],s[86:87]
R 54,2,7168
v_lshlrev_b32 v62,16,v59
M 18,a[24:27],38
s_mov_b32 s42,4194304
v_add_f32 v12,v12,v62
v_and_b32 v63,0xffff0000,v59
M 22,a[20:23],34
s_mov_b32 s43,0x00020000
R 26,2,8192
v_add_f32 v13,v13,v63
M 22,a[28:31],38
L 3
v_lshlrev_b32 v62,16,v60
v_add_f32 v14,v14,v62
M 22,a[36:39],42
s_add_u32 s64,s75,0
R 30,2,9216
v_and_b32 v63,0xffff0000,v60
M 22,a[44:47],46
s_lshl_b32 s62,s54,15
v_add_f32 v15,v15,v63
v_lshlrev_b32 v62,16,v61
M 18,a[32:35],42
s_mov_b32 s67,s65
R 34,2,10240
v_add_f32 v16,v16,v62
M 18,a[40:43],46
L 3
v_and_b32 v63,0xffff0000,v61
v_add_f32 v17,v17,v63
M 18,a[48:51],50
s_add_u32 s65,s47,s62
R 38,2,11264
v_cvt_pk_bf16_f32 v10,v10,v11
M 18,a[56:59],54
buffer_load_dwordx4 v[58:61],v0,s[36:39],s74 offen offset:0 sc1
M 22,a[52:55],50
s_add_u32 s74,s49,s62
R 42,2,12288
v_cvt_pk_bf16_f32 v11,v12,v13
M 22,a[60:63],54
L 3
v_cvt_pk_bf16_f32 v12,v14,v15
v_cvt_pk_bf16_f32 v13,v16,v17
M 22,a[68:71],26
R 46,2,13312
v_lshrrev_b32 v62,8,v0
M 22,a[76:79],30
v_lshl_add_u32 v62,v62,5,v0
v_add_u32 v62,s73,v62
M 18,a[64:67],26
R 50,2,14336
M 18,a[72:75],30
L 3
ds_write_b128 v62,v[10:13] offset:0
M 18,a[80:83],34
R 54,2,15360
ds_read2_b32 v[14:15],v5 offset0:0 offset1:16
M 18,a[88:91],38
ds_read2_b32 v[16:17],v5 offset0:32 offset1:48
M 22,a[84:87],34
R 26,2,16384
M 22,a[92:95],38
L 6
M 22,a[100:103],42
R 30,2,17408
M 22,a[108:111],46
M 18,a[96:99],42
R 34,2,18432
M 18,a[104:107],46
L 5
L 3
M 18,a[112:115],50
R 38,2,19456
M 18,a[120:123],54
buffer_atomic_pk_add_bf16 v14,v9,s[40:43],s64 offen sc1
M 22,a[116:119],50
s_add_u32 s64,s75,8192
R 42,2,20480
M 22,a[124:127],54
L 3
buffer_atomic_pk_add_bf16 v15,v9,s[40:43],s64 offen sc1
M 22,a[132:135],26
s_add_u32 s64,s75,16384
R 46,2,21504
M 22,a[140:143],30
buffer_atomic_pk_add_bf16 v16,v9,s[40:43],s64 offen sc1
M 18,a[128:131],26
s_add_u32 s64,s75,24576
R 50,2,22528
M 18,a[136:139],30
L 3
buffer_atomic_pk_add_bf16 v17,v9,s[40:43],s64 offen sc1
M 18,a[144:147],34
R 54,2,23552
M 18,a[152:155],38
M 22,a[148:151],34
R 26,2,24576
M 22,a[156:159],38
L 3
M 22,a[164:167],42
R 30,2,25600
M 22,a[172:175],46
M 18,a[160:163],42
R 34,2,26624
M 18,a[168:171],46
L 3
M 18,a[176:179],50
s_waitcnt vmcnt(5)
R 38,2,27648
M 18,a[184:187],54
s_barrier
ds_read_b32 v7,v6 offset:3072
M 22,a[180:183],50
R 42,2,28672
M 22,a[188:191],54
L 4
M 22,a[196:199],26
R 46,2,29696
M 22,a[204:207],30
M 18,a[192:195],26
R 50,2,30720
M 18,a[200:203],30
L 4
L 3
v_cmp_eq_u32 vcc,s53,v7
M 18,a[208:211],34
R 54,2,31744
M 18,a[216:219],38
M 22,a[212:215],34
s_cmp_eq_u32 vcc_lo,-1
R 26,2,32768
s_cbranch_scc0 Lg4_Xa1_%=
M 22,a[220:223],38
L 3
s_add_u32 m0,s57,0
s_add_u32 s56,s55,0
M 22,a[228:231],42
s_add_u32 s61,s54,s46
s_cmp_ge_u32 s61,s63
s_cselect_b32 s61,s93,s61
R 30,2,33792
M 22,a[236:239],46
s_mul_i32 s58,s61,2560
D 0,56,0
M 18,a[224:227],42
s_cselect_b32 s46,s88,s46
s_cselect_b32 s63,s89,s63
R 34,2,34816
M 18,a[232:235],46
L 3
s_cselect_b32 s93,s50,s93
s_cselect_b32 s88,8,s88
M 18,a[240:243],50
s_cselect_b32 s89,-1,s89
s_add_u32 s58,s58,109891584
R 38,2,35840
M 18,a[248:251],54
D 0,56,1024
M 22,a[244:247],50
s_mov_b32 s54,s61
R 42,2,36864
M 22,a[252:255],54
L 3
M 22,v[68:71],26
R 46,2,37888
M 22,v[76:79],30
D 0,56,2048
M 18,v[64:67],26
R 50,2,38912
M 18,v[72:75],30
L 3
M 18,v[80:83],34
R 54,2,39936
M 18,v[88:91],38
D 0,56,3072
M 22,v[84:87],34
s_add_u32 m0,s57,4096
s_add_u32 s56,s56,4096
R 26,2,40960
M 22,v[92:95],38
L 3
M 22,v[100:103],42
R 30,2,41984
M 22,v[108:111],46
D 0,56,0
M 18,v[96:99],42
R 34,2,43008
M 18,v[104:107],46
L 3
M 18,v[112:115],50
R 38,2,44032
M 18,v[120:123],54
D 0,56,1024
M 22,v[116:119],50
R 42,2,45056
M 22,v[124:127],54
L 3
M 22,v[132:135],26
R 46,2,46080
M 22,v[140:143],30
D 0,56,2048
M 18,v[128:131],26
s_add_u32 m0,s59,1024
s_add_u32 s56,s55,28672
R 50,2,47104
M 18,v[136:139],30
L 3
buffer_load_dword v8,s[36:39],s58 offen offset:0 sc1 lds
M 18,v[144:147],34
s_add_u32 m0,s57,28672
s_mul_i32 s55,s61,114688
R 54,2,48128
M 18,v[152:155],38
s_add_u32 s55,s55,s60
)"
R"(M 22,v[148:151],34
R 26,2,49152
M 22,v[156:159],38
L 3
D 0,56,0
M 22,v[164:167],42
R 30,2,50176
M 22,v[172:175],46
M 18,v[160:163],42
R 34,2,51200
M 18,v[168:171],46
L 3
D 0,56,1024
M 18,v[176:179],50
R 38,2,52224
M 18,v[184:187],54
D 0,56,2048
M 22,v[180:183],50
R 42,2,53248
M 22,v[188:191],54
L 3
M 22,v[196:199],26
R 46,2,54272
M 22,v[204:207],30
D 0,56,3072
M 18,v[192:195],26
s_add_u32 m0,s57,32768
s_add_u32 s56,s56,4096
R 50,2,55296
M 18,v[200:203],30
L 3
s_sub_u32 s57,s84,s57
M 18,v[208:211],34
s_waitcnt vmcnt(4)
R 54,2,56320
M 18,v[216:219],38
s_barrier
ds_read_b32 v7,v6 offset:1024
M 22,v[212:215],34
R 26,1,0
M 22,v[220:223],38
L 4
D 0,56,0
M 22,v[228:231],42
R 30,1,1024
M 22,v[236:239],46
D 0,56,1024
M 18,v[224:227],42
R 34,1,2048
M 18,v[232:235],46
L 4
D 0,56,2048
M 18,v[240:243],50
L 3
R 38,1,3072
v_cmp_eq_u32 vcc,s53,v7
M 18,v[248:251],54
M 22,v[244:247],50
L 2
s_cmp_eq_u32 vcc_lo,-1
R 42,1,4096
M 22,v[252:255],54
buffer_store_dword v4,v3,s[36:39],s71 offen offset:0 sc0 sc1
buffer_store_dword v4,v3,s[36:39],s71 offen offset:0 sc0 sc1
buffer_store_dword v4,v3,s[36:39],s71 offen offset:0 sc0 sc1
buffer_store_dword v4,v3,s[36:39],s71 offen offset:0 sc0 sc1
s_cbranch_scc1 Lg4_Fa0_%=
s_branch Lg4_Ka0_%=
Lg4_Xa1_%=:
M 22,a[220:223],38
L 3
s_add_u32 m0,s59,1024
M 22,a[228:231],42
R 30,2,33792
M 22,a[236:239],46
buffer_load_dword v8,s[36:39],s58 offen offset:0 sc1 lds
M 18,a[224:227],42
R 34,2,34816
M 18,a[232:235],46
L 3
M 18,a[240:243],50
R 38,2,35840
M 18,a[248:251],54
M 22,a[244:247],50
R 42,2,36864
M 22,a[252:255],54
L 3
M 22,v[68:71],26
R 46,2,37888
M 22,v[76:79],30
M 18,v[64:67],26
R 50,2,38912
M 18,v[72:75],30
L 3
M 18,v[80:83],34
R 54,2,39936
M 18,v[88:91],38
M 22,v[84:87],34
R 26,2,40960
M 22,v[92:95],38
L 3
M 22,v[100:103],42
R 30,2,41984
M 22,v[108:111],46
M 18,v[96:99],42
R 34,2,43008
M 18,v[104:107],46
L 3
M 18,v[112:115],50
R 38,2,44032
M 18,v[120:123],54
M 22,v[116:119],50
R 42,2,45056
M 22,v[124:127],54
L 3
M 22,v[132:135],26
R 46,2,46080
M 22,v[140:143],30
M 18,v[128:131],26
R 50,2,47104
M 18,v[136:139],30
L 3
M 18,v[144:147],34
R 54,2,48128
M 18,v[152:155],38
M 22,v[148:151],34
R 26,2,49152
M 22,v[156:159],38
L 3
M 22,v[164:167],42
R 30,2,50176
M 22,v[172:175],46
M 18,v[160:163],42
R 34,2,51200
M 18,v[168:171],46
L 3
M 18,v[176:179],50
R 38,2,52224
M 18,v[184:187],54
M 22,v[180:183],50
R 42,2,53248
M 22,v[188:191],54
L 3
M 22,v[196:199],26
R 46,2,54272
M 22,v[204:207],30
M 18,v[192:195],26
R 50,2,55296
M 18,v[200:203],30
L 3
M 18,v[208:211],34
s_waitcnt vmcnt(0)
R 54,2,56320
M 18,v[216:219],38
s_barrier
ds_read_b32 v7,v6 offset:1024
M 22,v[212:215],34
M 22,v[220:223],38
L 3
M 22,v[228:231],42
M 22,v[236:239],46
M 18,v[224:227],42
M 18,v[232:235],46
L 1
L 0
M 18,v[240:243],50
v_cmp_eq_u32 vcc,s53,v7
M 18,v[248:251],54
M 22,v[244:247],50
M 22,v[252:255],54
Lg4_pa1_%=:
s_mov_b32 s80,0
s_cmp_eq_u32 vcc_lo,-1
s_cbranch_scc0 Lg4_xna1_%=
s_add_u32 m0,s57,0
s_add_u32 s56,s55,0
D 0,56,0
s_sub_u32 s75,s67,s85
v_lshlrev_b32 v62,16,v58
D 0,56,1024
v_add_f32 v18,v18,v62
D 0,56,2048
v_and_b32 v63,0xffff0000,v58
D 0,56,3072
v_add_f32 v19,v19,v63
s_add_u32 m0,s57,4096
s_add_u32 s56,s56,4096
D 0,56,0
v_lshlrev_b32 v62,16,v59
D 0,56,1024
v_add_f32 v20,v20,v62
D 0,56,2048
v_and_b32 v63,0xffff0000,v59
s_add_u32 s61,s54,s46
s_cmp_ge_u32 s61,s63
s_cselect_b32 s61,s93,s61
s_cselect_b32 s46,s88,s46
s_cselect_b32 s63,s89,s63
s_cselect_b32 s93,s50,s93
s_cselect_b32 s88,8,s88
s_cselect_b32 s89,-1,s89
s_mul_i32 s58,s61,2560
s_add_u32 s58,s58,109891584
s_add_u32 m0,s59,2048
s_nop 0
buffer_load_dword v8,s[36:39],s58 offen offset:0 sc1 lds
v_add_f32 v21,v21,v63
s_add_u32 m0,s57,28672
s_add_u32 s56,s55,28672
D 0,56,0
v_lshlrev_b32 v62,16,v60
D 0,56,1024
v_add_f32 v22,v22,v62
D 0,56,2048
v_and_b32 v63,0xffff0000,v60
D 0,56,3072
v_add_f32 v23,v23,v63
s_add_u32 m0,s57,32768
s_add_u32 s56,s56,4096
D 0,56,0
v_lshlrev_b32 v62,16,v61
D 0,56,1024
v_add_f32 v24,v24,v62
D 0,56,2048
v_and_b32 v63,0xffff0000,v61
s_mul_i32 s55,s61,114688
s_add_u32 s55,s55,s60
s_sub_u32 s57,s84,s57
s_mov_b32 s54,s61
v_add_f32 v25,v25,v63
v_cvt_pk_bf16_f32 v18,v18,v19
v_cvt_pk_bf16_f32 v19,v20,v21
v_cvt_pk_bf16_f32 v20,v22,v23
v_cvt_pk_bf16_f32 v21,v24,v25
v_lshrrev_b32 v62,8,v0
v_lshl_add_u32 v62,v62,5,v0
v_add_u32 v62,s73,v62
ds_write_b128 v62,v[18:21] offset:0
ds_read2_b32 v[22:23],v5 offset0:0 offset1:16
ds_read2_b32 v[24:25],v5 offset0:32 offset1:48
s_mov_b64 s[40:41],s[86:87]
s_mov_b32 s42,4194304
s_mov_b32 s43,0x00020000
L 0
s_add_u32 s64,s75,0
buffer_atomic_pk_add_bf16 v22,v9,s[40:43],s64 offen sc1
s_add_u32 s64,s75,8192
buffer_atomic_pk_add_bf16 v23,v9,s[40:43],s64 offen sc1
s_add_u32 s64,s75,16384
buffer_atomic_pk_add_bf16 v24,v9,s[40:43],s64 offen sc1
s_add_u32 s64,s75,24576
buffer_atomic_pk_add_bf16 v25,v9,s[40:43],s64 offen sc1
s_mov_b32 s72,0x7fff0000
s_mov_b32 s69,0x7fff0000
s_mov_b32 s67,0x7fff0000
s_mov_b32 s77,0x7fff0000
s_waitcnt vmcnt(11)
s_barrier
s_branch Lg4_st0_%=
Lg4_xna1_%=:
s_cmp_ge_u32 s54,256
s_cbranch_scc1 Lg4_fina1_%=
s_add_u32 m0,s59,4096
s_nop 0
buffer_load_dword v8,s[36:39],s58 offen offset:0 sc1 lds
buffer_store_dword v4,v3,s[36:39],s71 offen offset:0 sc0 sc1
s_add_u32 m0,s59,5120
s_nop 0
buffer_load_dword v8,s[36:39],s58 offen offset:0 sc1 lds
buffer_store_dword v4,v3,s[36:39],s71 offen offset:0 sc0 sc1
s_mov_b32 s83,0
s_sub_u32 s75,s67,s85
v_lshlrev_b32 v62,16,v58
v_add_f32 v18,v18,v62
v_and_b32 v63,0xffff0000,v58
v_add_f32 v19,v19,v63
v_lshlrev_b32 v62,16,v59
v_add_f32 v20,v20,v62
v_and_b32 v63,0xffff0000,v59
v_add_f32 v21,v21,v63
v_lshlrev_b32 v62,16,v60
v_add_f32 v22,v22,v62
v_and_b32 v63,0xffff0000,v60
v_add_f32 v23,v23,v63
v_lshlrev_b32 v62,16,v61
v_add_f32 v24,v24,v62
v_and_b32 v63,0xffff0000,v61
v_add_f32 v25,v25,v63
v_cvt_pk_bf16_f32 v18,v18,v19
v_cvt_pk_bf16_f32 v19,v20,v21
v_cvt_pk_bf16_f32 v20,v22,v23
v_cvt_pk_bf16_f32 v21,v24,v25
v_lshrrev_b32 v62,8,v0
v_lshl_add_u32 v62,v62,5,v0
v_add_u32 v62,s73,v62
ds_write_b128 v62,v[18:21] offset:0
ds_read2_b32 v[22:23],v5 offset0:0 offset1:16
ds_read2_b32 v[24:25],v5 offset0:32 offset1:48
s_mov_b64 s[40:41],s[86:87]
s_mov_b32 s42,4194304
s_mov_b32 s43,0x00020000
L 0
s_add_u32 s64,s75,0
buffer_atomic_pk_add_bf16 v22,v9,s[40:43],s64 offen sc1
s_add_u32 s64,s75,8192
buffer_atomic_pk_add_bf16 v23,v9,s[40:43],s64 offen sc1
s_add_u32 s64,s75,16384
buffer_atomic_pk_add_bf16 v24,v9,s[40:43],s64 offen sc1
s_add_u32 s64,s75,24576
buffer_atomic_pk_add_bf16 v25,v9,s[40:43],s64 offen sc1
s_mov_b32 s72,0x7fff0000
s_mov_b32 s69,0x7fff0000
s_mov_b32 s67,0x7fff0000
s_mov_b32 s77,0x7fff0000
s_branch Lg4_poll_%=
Lg4_fina1_%=:
s_sub_u32 s75,s67,s85
v_lshlrev_b32 v62,16,v58
v_add_f32 v18,v18,v62
v_and_b32 v63,0xffff0000,v58
v_add_f32 v19,v19,v63
v_lshlrev_b32 v62,16,v59
v_add_f32 v20,v20,v62
v_and_b32 v63,0xffff0000,v59
v_add_f32 v21,v21,v63
v_lshlrev_b32 v62,16,v60
v_add_f32 v22,v22,v62
v_and_b32 v63,0xffff0000,v60
v_add_f32 v23,v23,v63
v_lshlrev_b32 v62,16,v61
v_add_f32 v24,v24,v62
v_and_b32 v63,0xffff0000,v61
v_add_f32 v25,v25,v63
v_cvt_pk_bf16_f32 v18,v18,v19
v_cvt_pk_bf16_f32 v19,v20,v21
v_cvt_pk_bf16_f32 v20,v22,v23
v_cvt_pk_bf16_f32 v21,v24,v25
v_lshrrev_b32 v62,8,v0
v_lshl_add_u32 v62,v62,5,v0
v_add_u32 v62,s73,v62
ds_write_b128 v62,v[18:21] offset:0
ds_read2_b32 v[22:23],v5 offset0:0 offset1:16
ds_read2_b32 v[24:25],v5 offset0:32 offset1:48
s_mov_b64 s[40:41],s[86:87]
s_mov_b32 s42,4194304
s_mov_b32 s43,0x00020000
L 0
s_add_u32 s64,s75,0
buffer_atomic_pk_add_bf16 v22,v9,s[40:43],s64 offen sc1
s_add_u32 s64,s75,8192
buffer_atomic_pk_add_bf16 v23,v9,s[40:43],s64 offen sc1
s_add_u32 s64,s75,16384
buffer_atomic_pk_add_bf16 v24,v9,s[40:43],s64 offen sc1
s_add_u32 s64,s75,24576
buffer_atomic_pk_add_bf16 v25,v9,s[40:43],s64 offen sc1
s_mov_b32 s72,0x7fff0000
s_mov_b32 s69,0x7fff0000
s_mov_b32 s67,0x7fff0000
s_mov_b32 s77,0x7fff0000
s_branch Lg4_done_%=
Lg4_done_%=:
s_nop 0
s_barrier
buffer_store_dword v4,v3,s[36:39],s70 offen offset:0 sc0 sc1
s_waitcnt vmcnt(1)
s_barrier
buffer_store_dword v4,v3,s[36:39],s72 offen offset:0 sc0 sc1
s_waitcnt vmcnt(0) lgkmcnt(0)
s_barrier
s_endpgm
Lcode_g4_e:
.purgem M
.purgem R
.purgem T
.purgem D
.purgem L
)"
  :: "s"(ws), "s"(__builtin_amdgcn_readfirstlane(sWave)), "s"(__builtin_amdgcn_readfirstlane(sG0)), "s"(__builtin_amdgcn_readfirstlane(sStep)), "s"(__builtin_amdgcn_readfirstlane(sOutOff)), "s"(__builtin_amdgcn_readfirstlane(sFoutOff)), "s"(__builtin_amdgcn_readfirstlane(sAuxOff)), "s"(__builtin_amdgcn_readfirstlane(sWw)), "s"(__builtin_amdgcn_readfirstlane(sBuf)), "s"(__builtin_amdgcn_readfirstlane(sRk)), "s"(__builtin_amdgcn_readfirstlane(sExp)), "v"(vL16), "v"(vFlag), "v"(vSnapOff), "v"(vSnapRd), "v"(vWd0), "v"(vWd1), "v"(vWr0), "v"(vWr1), "v"(vSp), "v"(vSn), "s"(w4), "s"(out) : "v255", "v0", "v1", "v2", "v3", "v4", "v5", "v6", "v7", "v8", "v9", "v10", "v11", "v12", "v13", "v14", "v15", "v16", "v17", "v18", "v19", "v20", "v21", "v22", "v23", "v24", "v25", "v26", "v27", "v28", "v29", "v30", "v31", "v32", "v33", "v34", "v35", "v36", "v37", "v38", "v39", "v40", "v41", "v42", "v43", "v44", "v45", "v46", "v47", "v48", "v49", "v50", "v51", "v52", "v53", "v54", "v55", "v56", "v57", "v58", "v59", "v60", "v61", "v62", "v63", "v64", "v65", "v66", "v67", "v68", "v69", "v70", "v71", "v72", "v73", "v74", "v75", "v76", "v77", "v78", "v79", "v80", "v81", "v82", "v83", "v84", "v85", "v86", "v87", "v88", "v89", "v90", "v91", "v92", "v93", "v94", "v95", "v96", "v97", "v98", "v99", "v100", "v101", "v102", "v103", "v104", "v105", "v106", "v107", "v108", "v109", "v110", "v111", "v112", "v113", "v114", "v115", "v116", "v117", "v118", "v119", "v120", "v121", "v122", "v123", "v124", "v125", "v126", "v127", "v128", "v129", "v130", "v131", "v132", "v133", "v134", "v135", "v136", "v137", "v138", "v139", "v140", "v141", "v142", "v143", "v144", "v145", "v146", "v147", "v148", "v149", "v150", "v151", "v152", "v153", "v154", "v155", "v156", "v157", "v158", "v159", "v160", "v161", "v162", "v163", "v164", "v165", "v166", "v167", "v168", "v169", "v170", "v171", "v172", "v173", "v174", "v175", "v176", "v177", "v178", "v179", "v180", "v181", "v182", "v183", "v184", "v185", "v186", "v187", "v188", "v189", "v190", "v191", "v192", "v193", "v194", "v195", "v196", "v197", "v198", "v199", "v200", "v201", "v202", "v203", "v204", "v205", "v206", "v207", "v208", "v209", "v210", "v211", "v212", "v213", "v214", "v215", "v216", "v217", "v218", "v219", "v220", "v221", "v222", "v223", "v224", "v225", "v226", "v227", "v228", "v229", "v230", "v231", "a0", "a1", "a2", "a3", "a4", "a5", "a6", "a7", "a8", "a9", "a10", "a11", "a12", "a13", "a14", "a15", "a16", "a17", "a18", "a19", "a20", "a21", "a22", "a23", "a24", "a25", "a26", "a27", "a28", "a29", "a30", "a31", "a32", "a33", "a34", "a35", "a36", "a37", "a38", "a39", "a40", "a41", "a42", "a43", "a44", "a45", "a46", "a47", "a48", "a49", "a50", "a51", "a52", "a53", "a54", "a55", "a56", "a57", "a58", "a59", "a60", "a61", "a62", "a63", "a64", "a65", "a66", "a67", "a68", "a69", "a70", "a71", "a72", "a73", "a74", "a75", "a76", "a77", "a78", "a79", "a80", "a81", "a82", "a83", "a84", "a85", "a86", "a87", "a88", "a89", "a90", "a91", "a92", "a93", "a94", "a95", "a96", "a97", "a98", "a99", "a100", "a101", "a102", "a103", "a104", "a105", "a106", "a107", "a108", "a109", "a110", "a111", "a112", "a113", "a114", "a115", "a116", "a117", "a118", "a119", "a120", "a121", "a122", "a123", "a124", "a125", "a126", "a127", "a128", "a129", "a130", "a131", "a132", "a133", "a134", "a135", "a136", "a137", "a138", "a139", "a140", "a141", "a142", "a143", "a144", "a145", "a146", "a147", "a148", "a149", "a150", "a151", "a152", "a153", "a154", "a155", "a156", "a157", "a158", "a159", "a160", "a161", "a162", "a163", "a164", "a165", "a166", "a167", "a168", "a169", "a170", "a171", "a172", "a173", "a174", "a175", "a176", "a177", "a178", "a179", "a180", "a181", "a182", "a183", "a184", "a185", "a186", "a187", "a188", "a189", "a190", "a191", "a192", "a193", "a194", "a195", "a196", "a197", "a198", "a199", "a200", "a201", "a202", "a203", "a204", "a205", "a206", "a207", "a208", "a209", "a210", "a211", "a212", "a213", "a214", "a215", "a216", "a217", "a218", "a219", "a220", "a221", "a222", "a223", "a224", "a225", "a226", "a227", "a228", "a229", "a230", "a231", "a232", "a233", "a234", "a235", "a236", "a237", "a238", "a239", "a240", "a241", "a242", "a243", "a244", "a245", "a246", "a247", "a248", "a249", "a250", "a251", "a252", "a253", "a254", "a255", "s36", "s37", "s38", "s39", "s40", "s41", "s42", "s43", "s44", "s45", "s46", "s47", "s48", "s49", "s50", "s51", "s52", "s53", "s54", "s55", "s56", "s57", "s58", "s59", "s60", "s61", "s62", "s63", "s64", "s65", "s66", "s67", "s68", "s69", "s70", "s71", "s72", "s73", "s74", "s75", "s76", "s77", "s78", "s79", "s80", "s81", "s82", "s83", "s84", "s85", "s86", "s87", "s88", "s89", "s90", "s91", "s92", "s93", "s94", "s95", "s96", "s97", "vcc", "scc", "memory");
    __builtin_unreachable();
  } else {
    LANE_ID(lane2);
    constexpr int N1 = 33 - (NR4 - 6) * 16, T1 = LAST_TASKS > RLN * N1 ? RLN * N1 : LAST_TASKS;
    const int w = kind == 6 ? 0 : kind == 1 ? 1 + (3 - rep) * 8 + slice : N1 + rep * 8 + slice;
    if (kind != 2) for (int t = w; t < T1; t += N1) row_last(ws, out, t, lane2, wave, ep);
    for (int t = T1 + (w + 32) % (N1 + 32); t < LAST_TASKS; t += N1 + 32) row_last(ws, out, t, lane2, wave, ep);
    if (kind == 2 && rep == 0 && slice == 0 && wave == 0) set_flag(ws + OFF_EP, lane2, ep);
    CTS(14);
    asm volatile("s_endpgm\nLcode_end:" ::: "memory");
    __builtin_unreachable();
  }
}

extern "C" void kernel_launch(void* const* d_in, const int* in_sizes, int n_in, void* d_out, int out_size,
                              void* d_ws, size_t ws_size, hipStream_t stream) {
  const u16* q = (const u16*)d_in[0];
  const u16* k = (const u16*)d_in[1];
  const u16* v = (const u16*)d_in[2];
  const u16* shift_k = (const u16*)d_in[3];
  const u16* ssm_k = (const u16*)d_in[4];
  const u16* ssm_D = (const u16*)d_in[5];
  const u16* w1 = (const u16*)d_in[7];
  const u16* w2 = (const u16*)d_in[8];
  const u16* w3 = (const u16*)d_in[9];
  const u16* w4 = (const u16*)d_in[10];
  flow_kernel<<<256, 256, 0, stream>>>(q, k, v, shift_k, ssm_k, ssm_D, w1, w2, w3, w4, (char*)d_ws, (u16*)d_out);
}
```

```cpp
#include <hip/hip_runtime.h>
#include <hip/hip_bf16.h>
#include <stdint.h>
#include <type_traits>

typedef short s16x8 __attribute__((ext_vector_type(8)));
typedef short s16x4 __attribute__((ext_vector_type(4)));
typedef float f32x4 __attribute__((ext_vector_type(4)));
typedef unsigned short u16;
typedef unsigned short u16x8 __attribute__((ext_vector_type(8)));
typedef unsigned short u16x4 __attribute__((ext_vector_type(4)));
typedef float f32x2 __attribute__((ext_vector_type(2)));
typedef __bf16 bf16x2 __attribute__((ext_vector_type(2)));
typedef unsigned u32x4v __attribute__((ext_vector_type(4)));
typedef __attribute__((address_space(1))) unsigned gu32;

constexpr int S_ = 2048, D_ = 1024, F_ = 3584;

constexpr int FSX = 128, FSH = 128, FS1 = 128, FSG = 128, FSP = 128;
constexpr unsigned GELU_CB = 0xbeeff558u;
constexpr int WS_USED = 111243392, LAST_TASKS = 100, ALLZ = 1, U0 = 170, U1 = 224, NR4 = 8, RHELP = 1, PRE1 = 1;
constexpr int OFF_KVR = 4194304, OFF_XP = 8388608, OFF_HS = 17825792, OFF_X1 = 31457280, OFF_G = 40894464, OFF_P0 = 71303168, OFF_P1 = 89128960, OFF_Y = 98566144, OFF_S1 = 106954752, OFF_ST = 107479040, OFF_FL = 109051904, OFF_FXP = 109056000, OFF_FH = 109334528, OFF_FX1 = 109613056, OFF_FG = 109891584, OFF_FP = 110587904, OFF_FKV = 111144960, OFF_FY = 111177728, OFF_EP = 111243264, OFF_DBG = 115343360;

#define WS_DIAG 0

__device__ __forceinline__ float bf2f(u16 v) { return __uint_as_float(((unsigned)v) << 16); }
__device__ __forceinline__ u16 f2bf(float f) { return __builtin_bit_cast(u16, (__bf16)f); }
__device__ __forceinline__ float rbf(float f) { return bf2f(f2bf(f)); }
__device__ __forceinline__ void st16(void* p, u16x8 v) {
  asm volatile("global_store_dwordx4 %0, %1, off sc0 sc1\n\ts_nop 1" :: "v"(p), "v"(v) : "memory");
}
#define WAIT_VM(n) asm volatile("s_waitcnt vmcnt(%0)" ::"n"(n) : "memory")
#define WAIT_LGKM0() asm volatile("s_waitcnt lgkmcnt(0)" ::: "memory")

__device__ __forceinline__ void touch_lds(const char* p, unsigned a) {
  asm volatile("s_mov_b32 m0, %1\n\ts_nop 0\n\tglobal_load_lds_dword %0, off" :: "v"(p), "s"(a) : "memory");
}
__device__ __forceinline__ void touch_sc1(const char* p, unsigned a) {
  asm volatile("s_mov_b32 m0, %1\n\ts_nop 0\n\tglobal_load_lds_dword %0, off sc1" :: "v"(p), "s"(a) : "memory");
}
#define CTS(i)
__device__ __forceinline__ f32x2 up2(unsigned p) { return (f32x2){__uint_as_float(p << 16), __uint_as_float(p & 0xffff0000u)}; }
__device__ __forceinline__ unsigned pk2(f32x2 v) { return __builtin_bit_cast(unsigned, __builtin_convertvector(v, bf16x2)); }
__device__ __forceinline__ f32x2 rbf2(f32x2 v) { return up2(pk2(v)); }
__device__ __forceinline__ void set_flag(char* p, int i, unsigned ep) {
  if (i < 32) __hip_atomic_store((gu32*)p + i, ep, __ATOMIC_RELAXED, __HIP_MEMORY_SCOPE_AGENT);
}
constexpr int CV_HP = (2048 + 32) * 2, CV_UR = (2048 + 256) * 2, CV_CH = 2 * CV_HP + CV_UR;
constexpr int NCV = 174;
constexpr int CV_STG = 8 * CV_CH;
constexpr int YP = D_ * 16;

struct KvRegs { u32x4v k0, k1, k2, k3, k4, k5, k6, v0, v1, v2, v3, s0, s1, s2, s3; };
#define KV_LOADS                                                                                                                       \
  "v_lshrrev_b32 %[a], 3, %[tid]\n\tv_and_b32 %[c], 7, %[tid]\n\ts_lshr_b32 s90, %[tile], 4\n\ts_and_b32 s91, %[tile], 15\n\t"          \
  "v_lshlrev_b32 %[a], 13, %[a]\n\tv_lshlrev_b32 %[c], 4, %[c]\n\ts_lshl_b32 s90, s90, 18\n\ts_lshl_b32 s91, s91, 7\n\t"                 \
  "v_add_u32 %[a], s90, %[a]\n\tv_add_u32 %[c], s91, %[c]\n\tv_subrev_u32 %[h], 0x1000, %[a]\n\tv_max_i32 %[h], 0x800, %[h]\n\t"         \
  "v_add3_u32 %[a], %[a], %[c], s92\n\tv_add_u32 %[h], %[h], %[c]\n\tv_lshlrev_b32 %[c], 2, %[c]\n\ts_waitcnt lgkmcnt(0)\n\t"            \
  "global_load_dwordx4 %3, %[a], s[76:77] offset:-4096 nt\n\tglobal_load_dwordx4 %4, %[a], s[76:77] offset:-2048 nt\n\t"                \
  "global_load_dwordx4 %5, %[a], s[76:77] nt\n\tglobal_load_dwordx4 %6, %[a], s[76:77] offset:2048 nt\n\t"                              \
  "global_load_dwordx4 %7, %[a], s[78:79] offset:-4096 nt\n\tglobal_load_dwordx4 %8, %[a], s[78:79] offset:-2048 nt\n\t"                \
  "global_load_dwordx4 %9, %[a], s[78:79] nt\n\tglobal_load_dwordx4 %10, %[a], s[78:79] offset:2048 nt\n\t"                             \
  "global_load_dwordx4 %0, %[h], s[76:77] offset:-2048 nt\n\tglobal_load_dwordx4 %1, %[h], s[76:77] nt\n\t"                             \
  "global_load_dwordx4 %2, %[h], s[76:77] offset:2048 nt\n\t"                                                                           \
  "global_load_dwordx4 %11, %[c], s[80:81]\n\tglobal_load_dwordx4 %12, %[c], s[80:81] offset:16\n\t"                                    \
  "global_load_dwordx4 %13, %[c], s[80:81] offset:32\n\tglobal_load_dwordx4 %14, %[c], s[80:81] offset:48\n\t"
#define KV_OUTS(R) "=&v"(R.k0), "=&v"(R.k1), "=&v"(R.k2), "=&v"(R.k3), "=&v"(R.k4), "=&v"(R.k5), "=&v"(R.k6), "=&v"(R.v0), "=&v"(R.v1), "=&v"(R.v2), \
                   "=&v"(R.v3), "=&v"(R.s0), "=&v"(R.s1), "=&v"(R.s2), "=&v"(R.s3), [a] "=&v"(ta), [h] "=&v"(th), [c] "=&v"(tc)
#define KV_CLOB "s74", "s75", "s76", "s77", "s78", "s79", "s80", "s81", "s82", "s83", "s90", "s91", "s92", "scc", "vcc", "memory"
template <int N>
__device__ __forceinline__ void kv_wait(KvRegs& R) {
  asm volatile("s_waitcnt vmcnt(%15)" : "+v"(R.k0), "+v"(R.k1), "+v"(R.k2), "+v"(R.k3), "+v"(R.k4), "+v"(R.k5), "+v"(R.k6), "+v"(R.v0), "+v"(R.v1), "+v"(R.v2),
               "+v"(R.v3), "+v"(R.s0), "+v"(R.s1), "+v"(R.s2), "+v"(R.s3) : "n"(N) : "memory");
}
__device__ __forceinline__ void kv_store(KvRegs& R, char* __restrict__ ws, char* T, int b, int tid) {
  const int t0 = (b >> 4) * 128, c0 = (b & 15) * 64, ch8 = tid & 7, rg = tid >> 3;
  if (t0 + 4 * rg < 3) R.k0 = R.k1 = R.k2 = (u32x4v){0u, 0u, 0u, 0u};
  typedef unsigned u32x2 __attribute__((ext_vector_type(2)));
  const u32x4v kr[7] = {R.k0, R.k1, R.k2, R.k3, R.k4, R.k5, R.k6}, vr[4] = {R.v0, R.v1, R.v2, R.v3}, sk[4] = {R.s0, R.s1, R.s2, R.s3};
#pragma unroll
  for (int j2 = 0; j2 < 4; ++j2) {
    const f32x2 sa = up2(sk[j2][0]), sb = up2(sk[j2][1]), sc = up2(sk[j2][2]), sd = up2(sk[j2][3]);
    const f32x2 s0 = {sa[0], sc[0]}, s1 = {sa[1], sc[1]}, s2 = {sb[0], sd[0]}, s3 = {sb[1], sd[1]};
    f32x2 kf[7], o[4];
#pragma unroll
    for (int r = 0; r < 7; ++r) kf[r] = up2(kr[r][j2]);
#pragma unroll
    for (int r = 0; r < 4; ++r)
      o[r] = __builtin_elementwise_fma(kf[r + 3], s0, __builtin_elementwise_fma(kf[r + 2], s1, __builtin_elementwise_fma(kf[r + 1], s2, kf[r] * s3))) * up2(vr[r][j2]);
#pragma unroll
    for (int e = 0; e < 2; ++e)
      *(u32x2*)(T + (8 * ch8 + 2 * j2 + e) * 264 + (124 - 4 * rg) * 2) = (u32x2){pk2((f32x2){o[3][e], o[2][e]}), pk2((f32x2){o[1][e], o[0][e]})};
  }
  __syncthreads();
  u16* kvr = (u16*)(ws + OFF_KVR);
#pragma unroll
  for (int i = 0; i < 4; ++i) {
    const int idx = tid + 256 * i, ch = idx >> 4, cb = idx & 15;
    const u16x4 lo = *(const u16x4*)(T + ch * 264 + cb * 16), hi = *(const u16x4*)(T + ch * 264 + cb * 16 + 8);
    st16(kvr + (size_t)(c0 + ch) * S_ + (1920 - t0) + cb * 8, __builtin_shufflevector(lo, hi, 0, 1, 2, 3, 4, 5, 6, 7));
  }
}

#define LASTSLEEP 4
__device__ __forceinline__ void wait_flags(const char* p, unsigned ep) {
  gu32* f = (gu32*)p;
  for (int it = 0; it < 20000; ++it) {
    if (__all(__hip_atomic_load(f, __ATOMIC_RELAXED, __HIP_MEMORY_SCOPE_AGENT) == ep)) break;
    __builtin_amdgcn_s_sleep(LASTSLEEP);
  }
  asm volatile("" ::: "memory");
}
__device__ __forceinline__ void row_last(char* __restrict__ ws, u16* __restrict__ out, int t, int lane, int wave, unsigned ep) {
  const __amdgpu_buffer_rsrc_t rs = __builtin_amdgcn_make_buffer_rsrc(ws, 0, WS_USED, 0x00020000);
  const int r8 = lane & 7, seg = lane >> 3, un = 128 + (t >> 1);
  const unsigned fo = un * 32768 + (wave * 8 + (seg >> 2)) * 1024 + ((seg & 3) * 16 + (t & 1) * 8 + r8) * 16;
  wait_flags(ws + OFF_FP + un * 2048 + ((lane & 2) * 4 + wave * 2 + (lane & 1)) * FSP, ep);
  u32x4v p0[4], p1[4];
#pragma unroll
  for (int i = 0; i < 4; ++i) {
    p0[i] = __builtin_bit_cast(u32x4v, __builtin_amdgcn_raw_buffer_load_b128(rs, fo + i * 2048, OFF_P0, 16));
    p1[i] = __builtin_bit_cast(u32x4v, __builtin_amdgcn_raw_buffer_load_b128(rs, fo + i * 2048, OFF_P1, 16));
  }
#pragma unroll
  for (int i = 0; i < 4; ++i) {
    u32x4v o;
#pragma unroll
    for (int e = 0; e < 4; ++e) o[e] = pk2(up2(p0[i][e]) + up2(p1[i][e]));
    st16(out + (size_t)(t * 8 + r8) * D_ + wave * 256 + i * 64 + seg * 8, __builtin_bit_cast(u16x8, o));
  }
}

__device__ __forceinline__ int swzB(int r) { return ((((r >> 1) & 1) | (((r >> 3) & 1) << 1)) << 1); }
#define LANE_ID(x) int x; asm volatile("v_mbcnt_lo_u32_b32 %0, -1, 0\n\tv_mbcnt_hi_u32_b32 %0, -1, %0" : "=v"(x))
#define W12 6
#define RLN 3
#define FT 3
#define QTB 1
constexpr int QT0 = 34 * 128;
#define CODE_MAX(x, y) "(((" x ">" y ")&" x ")|((" x "<=" y ")&" y "))"
constexpr int HU = 40960;
#define G3OR 5
constexpr int NRW = 22 - G3OR;
__global__ __launch_bounds__(256, 1) void flow_kernel(const u16* __restrict__ q, const u16* __restrict__ kin, const u16* __restrict__ vin,
                                                      const u16* __restrict__ shift_k, const u16* __restrict__ ssm_k, const u16* __restrict__ ssm_D,
                                                      const u16* __restrict__ w1, const u16* __restrict__ w2,
                                                      const u16* __restrict__ w3, const u16* __restrict__ w4, char* __restrict__ ws,
                                                      u16* __restrict__ out) {
  __shared__ __attribute__((aligned(16))) char lds[163840];
  KvRegs KR;
  unsigned ew, ta, th, tc;
  int b, tile;
  asm volatile("Lcode_b:\n\ts_load_dwordx4 s[76:79], %[ka], 0x8\n\ts_load_dwordx2 s[82:83], %[ka], 0x50\n\t"
               "s_getpc_b64 s[74:75]\n\ts_lshr_b32 s90, %[bi], 3\n\ts_mov_b32 m0, 0x27f00\n\tv_lshlrev_b32 %[a], 5, %[tid]\n\tv_add_lshl_u32 %[a], %[a], s90, 7\n\t"
               "v_min_u32 %[a], " CODE_MAX(CODE_MAX(CODE_MAX("(Lcode_g3_e-Lcode_b)", "(Lcode_g3o_e-Lcode_b)"), CODE_MAX("(Lcode_g4_e-Lcode_b)", "(Lcode_g12_e-Lcode_b)")), "(Lcode_end-Lcode_b)") "-64, %[a]\n\t"
               "global_load_lds_dword %[a], s[74:75]\n\ts_load_dwordx2 s[80:81], %[ka], 0x18\n\t"
               "s_mov_b32 %[b], %[bi]\n\ts_bfe_u32 s91, %[bi], 0x20001\n\ts_lshl2_add_u32 %[tile], s90, s91\n\ts_movk_i32 s92, 0x1000\n\t"
               "s_lshl_b32 s90, %[bi], 7\n\ts_add_u32 %[tile], %[tile], s90\n\ts_add_u32 %[tile], %[tile], 64\n\ts_and_b32 %[tile], %[tile], 255\n\t"
               KV_LOADS "v_mov_b32 %[a], %[eo]\n\tglobal_load_dword %[ew], %[a], s[82:83] sc1"
               : KV_OUTS(KR), [ew] "=&v"(ew), [b] "=&s"(b), [tile] "=&s"(tile)
               : [ka] "s"(__builtin_amdgcn_kernarg_segment_ptr()), [bi] "s"(blockIdx.x), [tid] "v"(threadIdx.x), [eo] "n"(OFF_EP) : KV_CLOB);
  const unsigned t_start = 0;
  const int tid = threadIdx.x, lane = tid & 63, wave = __builtin_amdgcn_readfirstlane(tid >> 6);
  const int xcd = b & 7, idx = b >> 3;
  const unsigned sLds = (unsigned)(size_t)lds;
  const int o8 = idx & 7;
  int kind, rep, slice, kh = 0;
  int j3 = 0, j4 = 0;
  const int xp = xcd >> 1, pr = xp & 1;
  if (!(xcd & 1)) {
    if (idx < 20) { kind = 4; rep = idx < 16 ? (xp >> 1) * 3 + (idx >> 3) : 2; kh = pr; slice = idx < 16 ? o8 : (xp >> 1) * 4 + o8; j4 = rep * 16 + kh * 8 + slice; }
    else if (idx >= 24) { kind = 1; rep = xp; slice = o8; }
    else { kind = 3; rep = 4; slice = xp * 4 + idx - 20; j3 = 72 + slice; }
  } else if (idx >= 24) { kind = 2; rep = xp; slice = o8; }
  else if (idx < 18) { kind = 3; rep = pr * 2 + (idx >= 9); slice = (xp >> 1) * 9 + idx - 9 * (idx >= 9); j3 = rep * 18 + slice; }
  else if (idx < 22 || b == 191) { kind = 6; rep = 0; slice = idx < 22 ? xp * 4 + idx - 18 : 16; }
  else if (idx == 22 || xp == 0) { kind = 5; rep = idx == 22 ? pr * 2 + (xp >> 1) : 4; slice = 18; j3 = 90 + rep; }
  else { kind = 3; rep = 4; slice = 15 + xp; j3 = 72 + slice; }
  CTS(0);
  const unsigned sScr = sLds + 163584;
  unsigned ep = 0;
  auto w_touch = [&](const u16* w) {
#pragma unroll
    for (int i = 0; i < 2; ++i) touch_lds((const char*)w + (size_t)(rep * 256 + i * 128 + (tid >> 1)) * 2048 + slice * 256 + (tid & 1) * 128, sScr);
  };
  {
  const bool isconv = kind >= 3 && kind != 6 && j3 < 94;
  const int nw = (kind == 4 || j3 >= 92) ? 2 : 1, c0 = kind == 4 ? 8 * j4 : j3 < 92 ? 640 + 4 * j3 : j3 < 94 ? 272 + 8 * j3 : 0, cw = c0 + nw * wave;
  const unsigned hd = isconv ? sLds + 2 * wave * CV_CH + 32 : 0x30000u;
  auto h_dma = [&](bool first) {
#pragma unroll
    for (int j = 0; j < 2; ++j) {
      const u16* hp = ssm_k + (size_t)cw * S_ + (isconv && j < nw ? j * S_ + lane * 8 : 0);
      if (first) asm volatile("s_mov_b32 m0, %1\n\ts_nop 0\n\tglobal_load_lds_dwordx4 %0, off nt" :: "v"(hp), "s"(hd + j * CV_CH) : "memory");
      else asm volatile("s_mov_b32 m0, %1\n\ts_nop 0\n\tglobal_load_lds_dwordx4 %0, off offset:1024 nt\n\t"
                        "global_load_lds_dwordx4 %0, off offset:2048 nt\n\tglobal_load_lds_dwordx4 %0, off offset:3072 nt" :: "v"(hp), "s"(hd + j * CV_CH) : "memory");
    }
  };
  auto w_dma = [&](int t0, int t1) {
    const char* wp = (const char*)w1 + (16 * wave + (lane >> 3)) * 2048 + slice * 256;
    const unsigned c0_ = ((lane & 7) ^ swzB(lane >> 3)) * 16, c1_ = 8 * 2048 + ((lane & 7) ^ swzB(8 + (lane >> 3))) * 16;
#pragma unroll
    for (int t = t0; t < t1; ++t)
#pragma unroll
      for (int i = 0; i < 2; ++i)
        asm volatile("s_mov_b32 m0, %1\n\ts_nop 0\n\tglobal_load_lds_dwordx4 %0, off\n\ts_add_u32 m0, m0, 8064\n\ts_nop 0\n\tglobal_load_lds_dwordx4 %0, off offset:128"
                     :: "v"(wp + t * 131072 + (i ? c1_ : c0_)), "s"(sLds + t * 16384 + wave * 2048 + i * 1024) : "scc", "memory");
  };
  if (PRE1 && kind == 1) w_dma(0, 2);
  else { h_dma(true); h_dma(false); }
  if (isconv) *(u32x4v*)(lds + (2 * wave + (lane >> 5)) * CV_CH + 2 * CV_HP + 4096 + (lane & 31) * 16) = (u32x4v){0u, 0u, 0u, 0u};
  if (isconv && lane < 8) *(u32x4v*)(lds + (2 * wave + (lane >> 2)) * CV_CH + ((lane >> 1) & 1) * CV_HP + (lane & 1) * 16) = (u32x4v){0u, 0u, 0u, 0u};
  if (FT & 2) touch_sc1(ws + OFF_FKV + (min(wave, 2) * 256 + b) * 128, sScr);
  kv_wait<(FT & 2) ? 10 : 9>(KR);
  CTS(12);
  kv_store(KR, ws, lds + CV_STG, tile, tid);
  CTS(1);
  if (PRE1 && kind == 1) w_dma(2, 6);
  const unsigned dw = ((const unsigned*)ssm_D)[cw >> 1];
  asm volatile("s_cmp_eq_u32 %1, %2\n\ts_cbranch_scc1 Lew%=\n\ts_waitcnt vmcnt(0)\nLew%=:\n\ts_waitcnt vmcnt(16)" : "+v"(ew) : "s"(__builtin_amdgcn_readfirstlane(kind)), "n"(PRE1 ? 1 : 99) : "scc", "memory");
  ep = __builtin_amdgcn_readfirstlane(ew) + 1u;
  if (ep == 0u) ep = 1u;
  __builtin_amdgcn_s_barrier();
  set_flag(ws + OFF_FKV + tile * 128, tid, ep);
  CTS(2);
  if (kind < 3 && (W12 & kind) != 0) w_touch(kind == 1 ? w1 : w2);
  if (kind == 2 && (W12 & 4) != 0) w_touch(w1);
  if (kind == 2 && FT) {
#pragma unroll
    for (int i = 0; i < 2; ++i) touch_sc1(ws + OFF_FXP + (size_t)min((i * 32 + rep * 8 + slice) * 256 + tid, (OFF_FKV - OFF_FXP) / 128 - 1) * 128, sScr);
  }
  if (isconv) {
    LANE_ID(lane);
    const int tid = wave * 64 + lane;
    const unsigned sC0 = (cw & 1) ? dw & 0xffff0000u : dw << 16, sC1 = dw & 0xffff0000u;
    const int l15 = lane & 15, lg = lane >> 4, cp = (l15 + 1) & 1, iw = kind == 4 ? j4 : 80 + j3, own = 2 * wave * CV_CH;
    const int ch = tid & (4 * nw - 1), tb = nw == 2 ? tid >> 3 : tid >> 2, slot = nw == 2 ? ch : 2 * ch;
    const unsigned sWave = wave, sBuf = nw, sAuxOff = OFF_KVR + cw * 4096, sFoutOff = OFF_FY + iw * 128, sExp = __builtin_amdgcn_readfirstlane(ep), sDbg = 110u * 1048576u + (b * 4 + wave) * 8192;
    const unsigned vL16 = lane * 16, vPA = own + cp * CV_HP + 2 * (8 * lg + l15 + 1 - cp), vPB = own + 2 * CV_HP + 2 * (2032 - 16 * l15 + 8 * lg) - 512 * 7;
    const unsigned vPU = own + 2 * CV_HP + 2 * (2044 - 16 * l15 - 4 * lg) - 512 * 7, vPW = 32 * l15 + 8 * lg;
    const unsigned vOR = slot * CV_CH + tb * 16, vORA = CV_STG + slot * 1024 + tb * 16, vOG = tb * YP + (c0 + ch) * 16;
    const unsigned vFA = ((lane & 3) * 16 + (c0 >> 6)) * 128, vFB = ((4 + lane % 12) * 16 + (c0 >> 6)) * 128, vFlag = tid < 32 ? tid * 4 : 0x80000000u;
    const unsigned vTW = lane < 42 ? (iw * 168 + wave * 42 + lane) * 128 : 0x80000000u;
    const unsigned vTX = lane < 44 ? ((kind == 4 ? j4 : j3 - 12) * 176 + wave * 44 + lane) * 128 : 0x80000000u;
    (void)sDbg;
    asm volatile(
R"(.macro M d,w,x
v_mfma_f32_16x16x32_bf16 v[\d:\d+3], \w, v[\x:\x+3], v[\d:\d+3]
.endm
.macro R d,a,o
ds_read_b128 v[\d:\d+3], v\a offset:\o
.endm
.macro T d,a,o
ds_read_b64_tr_b16 \d, v\a offset:\o
.endm
.macro D a,s,o
buffer_load_dwordx4 v\a, s[36:39], s\s offen offset:\o sc1 lds
.endm
.macro L c
s_waitcnt lgkmcnt(\c)
.endm
Lcode_cv_b:
s_mov_b64 s[36:37],%0
s_mov_b32 s38,111243392
s_mov_b32 s39,0x00020000
s_mov_b32 s44,%1
s_mov_b32 s51,%2
s_mov_b32 s49,%3
s_mov_b32 s48,%4
s_mov_b32 s86,%5
s_mov_b32 s87,%6
s_mov_b32 s53,%7
v_mov_b32 v0,%8
v_mov_b32 v1,%9
v_mov_b32 v2,%10
v_mov_b32 v3,%11
v_mov_b32 v4,%12
v_mov_b32 v5,%13
v_mov_b32 v6,%14
v_mov_b32 v7,%15
v_mov_b32 v8,%16
v_mov_b32 v9,%17
v_mov_b32 v10,%18
v_mov_b32 v11,%19
v_mov_b32 v12,%20
s_mov_b32 s58,111144960
buffer_load_dword v13,v8,s[36:39],s58 offen offset:0 sc1
s_mul_i32 s57,s44,25856
v_add_u32 v18,s57,v0
v_add_u32 v18,32,v18
v_mov_b32 v19,s57
R 88,18,0
ds_read_b32 v92,v18 offset:16
R 152,18,12928
ds_read_b32 v156,v18 offset:12944
ds_read_b32 v24,v19 offset:32
ds_read_b32 v25,v19 offset:12960
s_mov_b64 s[40:41],%21
s_mov_b32 s42,7340032
s_mov_b32 s43,0x00020000
s_mov_b64 s[60:61],%22
s_mov_b32 s62,7340032
s_mov_b32 s63,0x00020000
v_mov_b32 v23,s53
s_mov_b32 s82,0
s_add_u32 s55,s49,4096
s_cmp_eq_u32 s51,2
s_cselect_b32 s55,s55,0x7fff0000
s_cselect_b32 s56,0,0x70000000
s_add_u32 s57,s57,8320
v_add_u32 v14,1024,v1
v_add_u32 v15,2048,v1
v_add_u32 v16,3072,v1
v_add_u32 v17,12928,v1
L 4
v_alignbyte_b32 v96,v89,v88,2
v_alignbyte_b32 v97,v90,v89,2
v_alignbyte_b32 v98,v91,v90,2
v_alignbyte_b32 v99,v92,v91,2
ds_write_b128 v18,v[96:99] offset:4160
L 3
v_alignbyte_b32 v160,v153,v152,2
v_alignbyte_b32 v161,v154,v153,2
v_alignbyte_b32 v162,v155,v154,2
v_alignbyte_b32 v163,v156,v155,2
ds_write_b128 v18,v[160:163] offset:17088
L 2
v_lshlrev_b32 v24,16,v24
v_lshlrev_b32 v25,16,v25
s_mov_b64 exec,1
ds_write_b32 v19,v24 offset:4188
ds_write_b32 v19,v25 offset:17116
s_mov_b64 exec,-1
s_lshl_b32 s78,s44,11
s_add_u32 s78,s78,103424
v_add_u32 v216,s78,v4
v_add_u32 v4,v19,v4
Lcv_bpa_%=:
s_waitcnt vmcnt(0)
v_cmp_eq_u32 vcc,s53,v13
s_add_u32 s82,s82,1
s_cmp_eq_u64 vcc,-1
s_cbranch_scc1 Lcv_bqa_%=
s_sleep 1
buffer_load_dword v13,v8,s[36:39],s58 offen offset:0 sc1
s_cmp_lt_u32 s82,20000
s_cbranch_scc1 Lcv_bpa_%=
s_waitcnt vmcnt(0)
Lcv_bqa_%=:
s_add_u32 m0,s57,0
s_nop 0
D 0,49,3072
s_add_u32 m0,s57,12928
s_nop 0
D 0,55,3072
ds_read2_b32 a[0:1],v1 offset0:0 offset1:1
ds_read2_b32 a[2:3],v1 offset0:2 offset1:3
ds_read2_b32 a[4:5],v1 offset0:128 offset1:129
ds_read2_b32 a[6:7],v1 offset0:130 offset1:131
ds_read2_b32 a[8:9],v1 offset0:16 offset1:17
ds_read2_b32 a[10:11],v1 offset0:18 offset1:19
ds_read2_b32 a[12:13],v1 offset0:144 offset1:145
ds_read2_b32 a[14:15],v1 offset0:146 offset1:147
ds_read2_b32 a[16:17],v1 offset0:32 offset1:33
ds_read2_b32 a[18:19],v1 offset0:34 offset1:35
ds_read2_b32 a[20:21],v1 offset0:160 offset1:161
ds_read2_b32 a[22:23],v1 offset0:162 offset1:163
ds_read2_b32 a[24:25],v1 offset0:48 offset1:49
ds_read2_b32 a[26:27],v1 offset0:50 offset1:51
ds_read2_b32 a[28:29],v1 offset0:176 offset1:177
ds_read2_b32 a[30:31],v1 offset0:178 offset1:179
ds_read2_b32 a[32:33],v1 offset0:64 offset1:65
ds_read2_b32 a[34:35],v1 offset0:66 offset1:67
ds_read2_b32 a[36:37],v1 offset0:192 offset1:193
ds_read2_b32 a[38:39],v1 offset0:194 offset1:195
ds_read2_b32 a[40:41],v1 offset0:80 offset1:81
ds_read2_b32 a[42:43],v1 offset0:82 offset1:83
ds_read2_b32 a[44:45],v1 offset0:208 offset1:209
ds_read2_b32 a[46:47],v1 offset0:210 offset1:211
ds_read2_b32 a[48:49],v1 offset0:96 offset1:97
ds_read2_b32 a[50:51],v1 offset0:98 offset1:99
ds_read2_b32 a[52:53],v1 offset0:224 offset1:225
ds_read2_b32 a[54:55],v1 offset0:226 offset1:227
ds_read2_b32 a[56:57],v1 offset0:112 offset1:113
ds_read2_b32 a[58:59],v1 offset0:114 offset1:115
ds_read2_b32 a[60:61],v1 offset0:240 offset1:241
ds_read2_b32 a[62:63],v1 offset0:242 offset1:243
s_cmp_eq_u32 s51,2
s_cbranch_scc1 Lcv_a2_%=
s_waitcnt vmcnt(0)
buffer_load_dword v13,v9,s[36:39],s58 offen offset:0 sc1
L 0
R 88,2,3584
R 92,2,3072
R 96,2,3648
R 100,2,3136
R 104,2,3712
R 108,2,3200
R 112,2,3776
R 116,2,3264
L 6
v_mfma_f32_16x16x32_bf16 v[40:43],a[0:3],v[88:91],0
R 120,2,3840
R 124,2,3328
v_mfma_f32_16x16x32_bf16 v[44:47],a[0:3],v[92:95],0
M 44,a[4:7],88
L 6
M 40,a[8:11],96
R 128,2,3904
R 132,2,3392
M 44,a[8:11],100
M 44,a[12:15],96
L 6
M 40,a[16:19],104
R 136,2,3968
R 140,2,3456
M 44,a[16:19],108
M 44,a[20:23],104
L 6
M 40,a[24:27],112
R 144,2,4032
R 148,2,3520
M 44,a[24:27],116
M 44,a[28:31],112
L 6
M 40,a[32:35],120
M 44,a[32:35],124
M 44,a[36:39],120
L 4
M 40,a[40:43],128
M 44,a[40:43],132
M 44,a[44:47],128
L 2
M 40,a[48:51],136
ds_read_b64 v[24:25],v3 offset:3584
ds_read_b64 v[26:27],v3 offset:3072
M 44,a[48:51],140
M 44,a[52:55],136
L 2
M 40,a[56:59],144
M 44,a[56:59],148
M 44,a[60:63],144
L 0
v_and_b32 v36,0xffff0000,v25
v_lshlrev_b32 v37,16,v25
v_and_b32 v38,0xffff0000,v24
v_lshlrev_b32 v39,16,v24
s_nop 0
v_fma_f32 v40,v36,s86,v40
v_fma_f32 v41,v37,s86,v41
v_fma_f32 v42,v38,s86,v42
v_fma_f32 v43,v39,s86,v43
v_cvt_pk_bf16_f32 v40,v40,v41
v_cvt_pk_bf16_f32 v41,v42,v43
ds_write_b64 v216,v[40:41] offset:0
v_and_b32 v36,0xffff0000,v27
v_lshlrev_b32 v37,16,v27
v_and_b32 v38,0xffff0000,v26
v_lshlrev_b32 v39,16,v26
v_fma_f32 v44,v36,s86,v44
v_fma_f32 v45,v37,s86,v45
v_fma_f32 v46,v38,s86,v46
v_fma_f32 v47,v39,s86,v47
v_cvt_pk_bf16_f32 v44,v44,v45
v_cvt_pk_bf16_f32 v45,v46,v47
ds_write_b64 v216,v[44:45] offset:512
s_branch Lcv_a3_%=
Lcv_a2_%=:
ds_read2_b32 a[64:65],v17 offset0:0 offset1:1
ds_read2_b32 a[66:67],v17 offset0:2 offset1:3
ds_read2_b32 a[68:69],v17 offset0:128 offset1:129
ds_read2_b32 a[70:71],v17 offset0:130 offset1:131
ds_read2_b32 a[72:73],v17 offset0:16 offset1:17
ds_read2_b32 a[74:75],v17 offset0:18 offset1:19
ds_read2_b32 a[76:77],v17 offset0:144 offset1:145
ds_read2_b32 a[78:79],v17 offset0:146 offset1:147
ds_read2_b32 a[80:81],v17 offset0:32 offset1:33
ds_read2_b32 a[82:83],v17 offset0:34 offset1:35
ds_read2_b32 a[84:85],v17 offset0:160 offset1:161
ds_read2_b32 a[86:87],v17 offset0:162 offset1:163
ds_read2_b32 a[88:89],v17 offset0:48 offset1:49
ds_read2_b32 a[90:91],v17 offset0:50 offset1:51
ds_read2_b32 a[92:93],v17 offset0:176 offset1:177
ds_read2_b32 a[94:95],v17 offset0:178 offset1:179
ds_read2_b32 a[96:97],v17 offset0:64 offset1:65
ds_read2_b32 a[98:99],v17 offset0:66 offset1:67
ds_read2_b32 a[100:101],v17 offset0:192 offset1:193
ds_read2_b32 a[102:103],v17 offset0:194 offset1:195
ds_read2_b32 a[104:105],v17 offset0:80 offset1:81
ds_read2_b32 a[106:107],v17 offset0:82 offset1:83
ds_read2_b32 a[108:109],v17 offset0:208 offset1:209
ds_read2_b32 a[110:111],v17 offset0:210 offset1:211
ds_read2_b32 a[112:113],v17 offset0:96 offset1:97
ds_read2_b32 a[114:115],v17 offset0:98 offset1:99
ds_read2_b32 a[116:117],v17 offset0:224 offset1:225
ds_read2_b32 a[118:119],v17 offset0:226 offset1:227
ds_read2_b32 a[120:121],v17 offset0:112 offset1:113
ds_read2_b32 a[122:123],v17 offset0:114 offset1:115
ds_read2_b32 a[124:125],v17 offset0:240 offset1:241
ds_read2_b32 a[126:127],v17 offset0:242 offset1:243
s_waitcnt vmcnt(0)
buffer_load_dword v13,v9,s[36:39],s58 offen offset:0 sc1
L 0
R 88,2,3584
R 92,2,3072
R 96,2,3648
R 100,2,3136
R 104,2,3712
R 108,2,3200
R 112,2,3776
R 116,2,3264
L 6
v_mfma_f32_16x16x32_bf16 v[40:43],a[0:3],v[88:91],0
R 120,2,3840
R 124,2,3328
v_mfma_f32_16x16x32_bf16 v[44:47],a[0:3],v[92:95],0
M 44,a[4:7],88
L 6
M 40,a[8:11],96
R 128,2,3904
R 132,2,3392
M 44,a[8:11],100
M 44,a[12:15],96
L 6
M 40,a[16:19],104
R 136,2,3968
R 140,2,3456
M 44,a[16:19],108
M 44,a[20:23],104
L 6
M 40,a[24:27],112
R 144,2,4032
R 148,2,3520
M 44,a[24:27],116
M 44,a[28:31],112
L 6
M 40,a[32:35],120
R 152,2,16512
R 156,2,16000
M 44,a[32:35],124
M 44,a[36:39],120
L 6
M 40,a[40:43],128
R 160,2,16576
R 164,2,16064
M 44,a[40:43],132
M 44,a[44:47],128
L 6
M 40,a[48:51],136
R 168,2,16640
R 172,2,16128
M 44,a[48:51],140
ds_read_b64 v[24:25],v3 offset:3584
ds_read_b64 v[26:27],v3 offset:3072
M 44,a[52:55],136
L 8
M 40,a[56:59],144
R 176,2,16704
R 180,2,16192
M 44,a[56:59],148
M 44,a[60:63],144
L 8
v_mfma_f32_16x16x32_bf16 v[48:51],a[64:67],v[152:155],0
R 184,2,16768
R 188,2,16256
v_mfma_f32_16x16x32_bf16 v[52:55],a[64:67],v[156:159],0
M 52,a[68:71],152
L 8
M 48,a[72:75],160
R 192,2,16832
R 196,2,16320
M 52,a[72:75],164
L 6
v_and_b32 v36,0xffff0000,v25
M 52,a[76:79],160
v_lshlrev_b32 v37,16,v25
v_and_b32 v38,0xffff0000,v24
v_lshlrev_b32 v39,16,v24
s_nop 0
M 48,a[80:83],168
R 200,2,16896
R 204,2,16384
M 52,a[80:83],172
v_fma_f32 v40,v36,s86,v40
v_fma_f32 v41,v37,s86,v41
M 52,a[84:87],168
v_fma_f32 v42,v38,s86,v42
v_fma_f32 v43,v39,s86,v43
v_cvt_pk_bf16_f32 v40,v40,v41
L 6
M 48,a[88:91],176
R 208,2,16960
R 212,2,16448
M 52,a[88:91],180
v_cvt_pk_bf16_f32 v41,v42,v43
ds_write_b64 v216,v[40:41] offset:0
M 52,a[92:95],176
v_and_b32 v36,0xffff0000,v27
v_lshlrev_b32 v37,16,v27
v_and_b32 v38,0xffff0000,v26
L 7
M 48,a[96:99],184
M 52,a[96:99],188
v_lshlrev_b32 v39,16,v26
v_fma_f32 v44,v36,s86,v44
M 52,a[100:103],184
v_fma_f32 v45,v37,s86,v45
v_fma_f32 v46,v38,s86,v46
v_fma_f32 v47,v39,s86,v47
L 5
M 48,a[104:107],192
M 52,a[104:107],196
v_cvt_pk_bf16_f32 v44,v44,v45
v_cvt_pk_bf16_f32 v45,v46,v47
M 52,a[108:111],192
ds_write_b64 v216,v[44:45] offset:512
L 4
M 48,a[112:115],200
ds_read_b64 v[24:25],v3 offset:16512
ds_read_b64 v[26:27],v3 offset:16000
M 52,a[112:115],204
M 52,a[116:119],200
L 4
M 48,a[120:123],208
M 52,a[120:123],212
M 52,a[124:127],208
L 0
v_and_b32 v36,0xffff0000,v25
v_lshlrev_b32 v37,16,v25
v_and_b32 v38,0xffff0000,v24
v_lshlrev_b32 v39,16,v24
s_nop 0
v_fma_f32 v48,v36,s87,v48
v_fma_f32 v49,v37,s87,v49
v_fma_f32 v50,v38,s87,v50
v_fma_f32 v51,v39,s87,v51
v_cvt_pk_bf16_f32 v48,v48,v49
v_cvt_pk_bf16_f32 v49,v50,v51
ds_write_b64 v216,v[48:49] offset:1024
v_and_b32 v36,0xffff0000,v27
v_lshlrev_b32 v37,16,v27
v_and_b32 v38,0xffff0000,v26
v_lshlrev_b32 v39,16,v26
v_fma_f32 v52,v36,s87,v52
v_fma_f32 v53,v37,s87,v53
v_fma_f32 v54,v38,s87,v54
v_fma_f32 v55,v39,s87,v55
v_cvt_pk_bf16_f32 v52,v52,v53
v_cvt_pk_bf16_f32 v53,v54,v55
ds_write_b64 v216,v[52:53] offset:1536
Lcv_a3_%=:
L 0
s_barrier
R 152,6,0
R 156,6,512
s_mov_b32 s78,98566144
L 1
buffer_store_dwordx4 v[152:155],v7,s[36:39],s78 offen offset:0 sc0 sc1
s_add_u32 s78,s56,99090432
L 0
buffer_store_dwordx4 v[156:159],v7,s[36:39],s78 offen offset:0 sc0 sc1
s_waitcnt vmcnt(2)
v_cmp_eq_u32 vcc,s53,v13
s_nop 0
s_cmp_eq_u64 vcc,-1
s_cbranch_scc0 Lcv_a_%=
s_add_u32 m0,s57,0
s_nop 0
D 0,49,2048
D 0,49,1024
D 0,49,0
s_add_u32 m0,s57,12928
s_nop 0
D 0,55,2048
D 0,55,1024
D 0,55,0
R 104,18,1024
ds_read_b32 v108,v18 offset:1040
R 120,18,2048
ds_read_b32 v124,v18 offset:2064
R 136,18,3072
ds_read_b32 v140,v18 offset:3088
L 4
v_alignbyte_b32 v112,v105,v104,2
v_alignbyte_b32 v113,v106,v105,2
v_alignbyte_b32 v114,v107,v106,2
v_alignbyte_b32 v115,v108,v107,2
ds_write_b128 v18,v[112:115] offset:5184
L 3
v_alignbyte_b32 v128,v121,v120,2
v_alignbyte_b32 v129,v122,v121,2
v_alignbyte_b32 v130,v123,v122,2
v_alignbyte_b32 v131,v124,v123,2
ds_write_b128 v18,v[128:131] offset:6208
L 2
v_alignbyte_b32 v144,v137,v136,2
v_alignbyte_b32 v145,v138,v137,2
v_alignbyte_b32 v146,v139,v138,2
v_alignbyte_b32 v147,v140,v139,2
ds_write_b128 v18,v[144:147] offset:7232
s_waitcnt vmcnt(6)
s_barrier
buffer_store_dword v23,v10,s[36:39],s48 offen offset:0 sc0 sc1
s_branch Lcv_b_%=
Lcv_a_%=:
R 104,18,1024
ds_read_b32 v108,v18 offset:1040
R 120,18,2048
ds_read_b32 v124,v18 offset:2064
R 136,18,3072
ds_read_b32 v140,v18 offset:3088
L 4
v_alignbyte_b32 v112,v105,v104,2
v_alignbyte_b32 v113,v106,v105,2
v_alignbyte_b32 v114,v107,v106,2
v_alignbyte_b32 v115,v108,v107,2
ds_write_b128 v18,v[112:115] offset:5184
L 3
v_alignbyte_b32 v128,v121,v120,2
v_alignbyte_b32 v129,v122,v121,2
v_alignbyte_b32 v130,v123,v122,2
v_alignbyte_b32 v131,v124,v123,2
ds_write_b128 v18,v[128:131] offset:6208
L 2
v_alignbyte_b32 v144,v137,v136,2
v_alignbyte_b32 v145,v138,v137,2
v_alignbyte_b32 v146,v139,v138,2
v_alignbyte_b32 v147,v140,v139,2
ds_write_b128 v18,v[144:147] offset:7232
s_waitcnt vmcnt(0)
s_barrier
buffer_store_dword v23,v10,s[36:39],s48 offen offset:0 sc0 sc1
buffer_load_dword v13,v9,s[36:39],s58 offen offset:0 sc1
Lcv_bpb_%=:
s_waitcnt vmcnt(0)
v_cmp_eq_u32 vcc,s53,v13
s_add_u32 s82,s82,1
s_cmp_eq_u64 vcc,-1
s_cbranch_scc1 Lcv_bqb_%=
s_sleep 1
buffer_load_dword v13,v9,s[36:39],s58 offen offset:0 sc1
s_cmp_lt_u32 s82,20000
s_cbranch_scc1 Lcv_bpb_%=
s_waitcnt vmcnt(0)
Lcv_bqb_%=:
s_add_u32 m0,s57,0
)"
R"(s_nop 0
D 0,49,2048
D 0,49,1024
D 0,49,0
s_add_u32 m0,s57,12928
s_nop 0
D 0,55,2048
D 0,55,1024
D 0,55,0
Lcv_b_%=:
s_waitcnt vmcnt(0)
ds_read2_b32 v[88:89],v1 offset0:0 offset1:1
ds_read2_b32 v[90:91],v1 offset0:2 offset1:3
R 120,2,3584
ds_read2_b32 v[92:93],v1 offset0:128 offset1:129
ds_read2_b32 v[94:95],v1 offset0:130 offset1:131
R 124,2,3072
ds_read2_b32 v[96:97],v14 offset0:0 offset1:1
ds_read2_b32 v[98:99],v14 offset0:2 offset1:3
R 128,2,2560
ds_read2_b32 v[100:101],v14 offset0:128 offset1:129
ds_read2_b32 v[102:103],v14 offset0:130 offset1:131
R 132,2,2048
ds_read2_b32 v[104:105],v15 offset0:0 offset1:1
ds_read2_b32 v[106:107],v15 offset0:2 offset1:3
R 136,2,1536
ds_read2_b32 v[108:109],v15 offset0:128 offset1:129
ds_read2_b32 v[110:111],v15 offset0:130 offset1:131
R 140,2,1024
ds_read2_b32 v[112:113],v16 offset0:0 offset1:1
ds_read2_b32 v[114:115],v16 offset0:2 offset1:3
R 144,2,512
ds_read2_b32 v[116:117],v16 offset0:128 offset1:129
ds_read2_b32 v[118:119],v16 offset0:130 offset1:131
R 148,2,0
L 15
v_mfma_f32_16x16x32_bf16 v[40:43],v[88:91],v[128:131],0
ds_read2_b32 v[152:153],v1 offset0:16 offset1:17
M 40,v[92:95],124
ds_read2_b32 v[154:155],v1 offset0:18 offset1:19
M 40,v[96:99],120
L 14
R 184,2,3648
v_mfma_f32_16x16x32_bf16 v[44:47],v[88:91],v[132:135],0
ds_read2_b32 v[156:157],v1 offset0:144 offset1:145
M 44,v[92:95],128
M 44,v[96:99],124
ds_read2_b32 v[158:159],v1 offset0:146 offset1:147
M 44,v[100:103],120
L 14
R 188,2,3136
v_mfma_f32_16x16x32_bf16 v[48:51],v[88:91],v[136:139],0
ds_read2_b32 v[160:161],v14 offset0:16 offset1:17
M 48,v[92:95],132
ds_read2_b32 v[162:163],v14 offset0:18 offset1:19
M 48,v[96:99],128
M 48,v[100:103],124
R 192,2,2624
M 48,v[104:107],120
L 15
ds_read2_b32 v[164:165],v14 offset0:144 offset1:145
v_mfma_f32_16x16x32_bf16 v[52:55],v[88:91],v[140:143],0
ds_read2_b32 v[166:167],v14 offset0:146 offset1:147
M 52,v[92:95],136
R 196,2,2112
M 52,v[96:99],132
M 52,v[100:103],128
ds_read2_b32 v[168:169],v15 offset0:16 offset1:17
M 52,v[104:107],124
ds_read2_b32 v[170:171],v15 offset0:18 offset1:19
M 52,v[108:111],120
L 15
R 200,2,1600
v_mfma_f32_16x16x32_bf16 v[56:59],v[88:91],v[144:147],0
ds_read2_b32 v[172:173],v15 offset0:144 offset1:145
M 56,v[92:95],140
M 56,v[96:99],136
ds_read2_b32 v[174:175],v15 offset0:146 offset1:147
M 56,v[100:103],132
R 204,2,1088
M 56,v[104:107],128
ds_read2_b32 v[176:177],v16 offset0:16 offset1:17
M 56,v[108:111],124
ds_read2_b32 v[178:179],v16 offset0:18 offset1:19
M 56,v[112:115],120
L 15
v_mfma_f32_16x16x32_bf16 v[60:63],v[88:91],v[148:151],0
R 208,2,576
M 60,v[92:95],144
ds_read2_b32 v[180:181],v16 offset0:144 offset1:145
M 60,v[96:99],140
ds_read2_b32 v[182:183],v16 offset0:146 offset1:147
M 60,v[100:103],136
R 212,2,64
M 60,v[104:107],132
M 60,v[108:111],128
M 60,v[112:115],124
M 60,v[116:119],120
L 15
M 40,v[152:155],192
ds_read2_b32 v[88:89],v1 offset0:32 offset1:33
M 40,v[156:159],188
ds_read2_b32 v[90:91],v1 offset0:34 offset1:35
M 40,v[160:163],184
L 14
R 120,2,3712
M 44,v[152:155],196
ds_read2_b32 v[92:93],v1 offset0:160 offset1:161
M 44,v[156:159],192
M 44,v[160:163],188
ds_read2_b32 v[94:95],v1 offset0:162 offset1:163
M 44,v[164:167],184
L 14
R 124,2,3200
M 48,v[152:155],200
ds_read2_b32 v[96:97],v14 offset0:32 offset1:33
M 48,v[156:159],196
ds_read2_b32 v[98:99],v14 offset0:34 offset1:35
M 48,v[160:163],192
M 48,v[164:167],188
R 128,2,2688
M 48,v[168:171],184
L 15
ds_read2_b32 v[100:101],v14 offset0:160 offset1:161
M 52,v[152:155],204
ds_read2_b32 v[102:103],v14 offset0:162 offset1:163
M 52,v[156:159],200
R 132,2,2176
M 52,v[160:163],196
M 52,v[164:167],192
ds_read2_b32 v[104:105],v15 offset0:32 offset1:33
M 52,v[168:171],188
ds_read2_b32 v[106:107],v15 offset0:34 offset1:35
M 52,v[172:175],184
L 15
R 136,2,1664
M 56,v[152:155],208
ds_read2_b32 v[108:109],v15 offset0:160 offset1:161
M 56,v[156:159],204
M 56,v[160:163],200
ds_read2_b32 v[110:111],v15 offset0:162 offset1:163
M 56,v[164:167],196
R 140,2,1152
M 56,v[168:171],192
ds_read2_b32 v[112:113],v16 offset0:32 offset1:33
M 56,v[172:175],188
ds_read2_b32 v[114:115],v16 offset0:34 offset1:35
M 56,v[176:179],184
L 15
M 60,v[152:155],212
R 144,2,640
M 60,v[156:159],208
ds_read2_b32 v[116:117],v16 offset0:160 offset1:161
M 60,v[160:163],204
ds_read2_b32 v[118:119],v16 offset0:162 offset1:163
R 218,18,13952
M 60,v[164:167],200
R 148,2,128
ds_read_b32 v222,v18 offset:13968
M 60,v[168:171],196
M 60,v[172:175],192
M 60,v[176:179],188
M 60,v[180:183],184
L 15
M 40,v[88:91],128
ds_read2_b32 v[152:153],v1 offset0:48 offset1:49
M 40,v[92:95],124
L 1
ds_read2_b32 v[154:155],v1 offset0:50 offset1:51
v_alignbyte_b32 v226,v219,v218,2
M 40,v[96:99],120
s_nop 0
R 184,2,3776
v_alignbyte_b32 v227,v220,v219,2
M 44,v[88:91],132
ds_read2_b32 v[156:157],v1 offset0:176 offset1:177
v_alignbyte_b32 v228,v221,v220,2
M 44,v[92:95],128
v_alignbyte_b32 v229,v222,v221,2
M 44,v[96:99],124
ds_read2_b32 v[158:159],v1 offset0:178 offset1:179
M 44,v[100:103],120
s_nop 0
R 188,2,3264
M 48,v[88:91],136
ds_read2_b32 v[160:161],v14 offset0:48 offset1:49
M 48,v[92:95],132
ds_read2_b32 v[162:163],v14 offset0:50 offset1:51
M 48,v[96:99],128
ds_write_b128 v18,v[226:229] offset:18112
M 48,v[100:103],124
R 192,2,2752
M 48,v[104:107],120
s_nop 0
ds_read2_b32 v[164:165],v14 offset0:176 offset1:177
M 52,v[88:91],140
s_mov_b32 m0,163584
s_mov_b32 s78,0
ds_read2_b32 v[166:167],v14 offset0:178 offset1:179
M 52,v[92:95],136
R 196,2,2240
M 52,v[96:99],132
buffer_load_dword v11,s[40:43],s78 offen offset:0  lds
M 52,v[100:103],128
ds_read2_b32 v[168:169],v15 offset0:48 offset1:49
M 52,v[104:107],124
ds_read2_b32 v[170:171],v15 offset0:50 offset1:51
M 52,v[108:111],120
s_nop 0
R 200,2,1728
M 56,v[88:91],144
ds_read2_b32 v[172:173],v15 offset0:176 offset1:177
M 56,v[92:95],140
M 56,v[96:99],136
ds_read2_b32 v[174:175],v15 offset0:178 offset1:179
M 56,v[100:103],132
R 204,2,1216
M 56,v[104:107],128
ds_read2_b32 v[176:177],v16 offset0:48 offset1:49
M 56,v[108:111],124
ds_read2_b32 v[178:179],v16 offset0:50 offset1:51
M 56,v[112:115],120
s_nop 0
M 60,v[88:91],148
R 208,2,704
M 60,v[92:95],144
ds_read2_b32 v[180:181],v16 offset0:176 offset1:177
M 60,v[96:99],140
ds_read2_b32 v[182:183],v16 offset0:178 offset1:179
M 60,v[100:103],136
R 212,2,192
M 60,v[104:107],132
M 60,v[108:111],128
M 60,v[112:115],124
M 60,v[116:119],120
L 15
M 40,v[152:155],192
ds_read2_b32 v[88:89],v1 offset0:64 offset1:65
M 40,v[156:159],188
ds_read2_b32 v[90:91],v1 offset0:66 offset1:67
R 218,18,14976
M 40,v[160:163],184
L 15
R 120,2,3840
ds_read_b32 v222,v18 offset:14992
M 44,v[152:155],196
ds_read2_b32 v[92:93],v1 offset0:192 offset1:193
M 44,v[156:159],192
M 44,v[160:163],188
ds_read2_b32 v[94:95],v1 offset0:194 offset1:195
M 44,v[164:167],184
L 15
R 124,2,3328
M 48,v[152:155],200
ds_read2_b32 v[96:97],v14 offset0:64 offset1:65
M 48,v[156:159],196
L 4
ds_read2_b32 v[98:99],v14 offset0:66 offset1:67
v_alignbyte_b32 v226,v219,v218,2
M 48,v[160:163],192
v_alignbyte_b32 v227,v220,v219,2
v_alignbyte_b32 v228,v221,v220,2
M 48,v[164:167],188
R 128,2,2816
v_alignbyte_b32 v229,v222,v221,2
M 48,v[168:171],184
s_nop 0
ds_read2_b32 v[100:101],v14 offset0:192 offset1:193
M 52,v[152:155],204
ds_read2_b32 v[102:103],v14 offset0:194 offset1:195
M 52,v[156:159],200
R 132,2,2304
M 52,v[160:163],196
ds_write_b128 v18,v[226:229] offset:19136
M 52,v[164:167],192
ds_read2_b32 v[104:105],v15 offset0:64 offset1:65
M 52,v[168:171],188
ds_read2_b32 v[106:107],v15 offset0:66 offset1:67
M 52,v[172:175],184
s_nop 0
R 136,2,1792
M 56,v[152:155],208
ds_read2_b32 v[108:109],v15 offset0:192 offset1:193
M 56,v[156:159],204
M 56,v[160:163],200
ds_read2_b32 v[110:111],v15 offset0:194 offset1:195
M 56,v[164:167],196
R 140,2,1280
M 56,v[168:171],192
ds_read2_b32 v[112:113],v16 offset0:64 offset1:65
M 56,v[172:175],188
ds_read2_b32 v[114:115],v16 offset0:66 offset1:67
M 56,v[176:179],184
s_nop 0
M 60,v[152:155],212
R 144,2,768
M 60,v[156:159],208
ds_read2_b32 v[116:117],v16 offset0:192 offset1:193
M 60,v[160:163],204
ds_read2_b32 v[118:119],v16 offset0:194 offset1:195
M 60,v[164:167],200
R 148,2,256
M 60,v[168:171],196
M 60,v[172:175],192
M 60,v[176:179],188
M 60,v[180:183],184
L 15
M 40,v[88:91],128
ds_read2_b32 v[152:153],v1 offset0:80 offset1:81
M 40,v[92:95],124
ds_read2_b32 v[154:155],v1 offset0:82 offset1:83
M 40,v[96:99],120
L 15
R 184,2,3904
M 44,v[88:91],132
ds_read2_b32 v[156:157],v1 offset0:208 offset1:209
M 44,v[92:95],128
M 44,v[96:99],124
ds_read2_b32 v[158:159],v1 offset0:210 offset1:211
M 44,v[100:103],120
L 14
R 188,2,3392
M 48,v[88:91],136
ds_read2_b32 v[160:161],v14 offset0:80 offset1:81
M 48,v[92:95],132
ds_read2_b32 v[162:163],v14 offset0:82 offset1:83
R 218,18,16000
M 48,v[96:99],128
ds_read_b32 v222,v18 offset:16016
M 48,v[100:103],124
R 192,2,2880
M 48,v[104:107],120
L 15
ds_read2_b32 v[164:165],v14 offset0:208 offset1:209
M 52,v[88:91],140
ds_read2_b32 v[166:167],v14 offset0:210 offset1:211
M 52,v[92:95],136
R 196,2,2368
M 52,v[96:99],132
M 52,v[100:103],128
L 4
ds_read2_b32 v[168:169],v15 offset0:80 offset1:81
v_alignbyte_b32 v226,v219,v218,2
M 52,v[104:107],124
ds_read2_b32 v[170:171],v15 offset0:82 offset1:83
v_alignbyte_b32 v227,v220,v219,2
M 52,v[108:111],120
s_nop 0
R 200,2,1856
v_alignbyte_b32 v228,v221,v220,2
M 56,v[88:91],144
ds_read2_b32 v[172:173],v15 offset0:208 offset1:209
v_alignbyte_b32 v229,v222,v221,2
M 56,v[92:95],140
ds_write_b128 v18,v[226:229] offset:20160
M 56,v[96:99],136
ds_read2_b32 v[174:175],v15 offset0:210 offset1:211
M 56,v[100:103],132
R 204,2,1344
M 56,v[104:107],128
ds_read2_b32 v[176:177],v16 offset0:80 offset1:81
M 56,v[108:111],124
ds_read2_b32 v[178:179],v16 offset0:82 offset1:83
M 56,v[112:115],120
s_nop 0
M 60,v[88:91],148
R 208,2,832
M 60,v[92:95],144
ds_read2_b32 v[180:181],v16 offset0:208 offset1:209
M 60,v[96:99],140
ds_read2_b32 v[182:183],v16 offset0:210 offset1:211
M 60,v[100:103],136
R 212,2,320
M 60,v[104:107],132
M 60,v[108:111],128
M 60,v[112:115],124
M 60,v[116:119],120
L 15
M 40,v[152:155],192
ds_read2_b32 v[88:89],v1 offset0:96 offset1:97
M 40,v[156:159],188
ds_read2_b32 v[90:91],v1 offset0:98 offset1:99
M 40,v[160:163],184
L 15
R 120,2,3968
M 44,v[152:155],196
s_mov_b32 m0,163584
s_mov_b32 s78,3741696
ds_read2_b32 v[92:93],v1 offset0:224 offset1:225
M 44,v[156:159],192
M 44,v[160:163],188
ds_read2_b32 v[94:95],v1 offset0:226 offset1:227
M 44,v[164:167],184
L 15
R 124,2,3456
M 48,v[152:155],200
ds_read2_b32 v[96:97],v14 offset0:96 offset1:97
M 48,v[156:159],196
ds_read2_b32 v[98:99],v14 offset0:98 offset1:99
M 48,v[160:163],192
buffer_load_dword v11,s[40:43],s78 offen offset:0  lds
M 48,v[164:167],188
R 128,2,2944
M 48,v[168:171],184
L 15
ds_read2_b32 v[100:101],v14 offset0:224 offset1:225
M 52,v[152:155],204
ds_read2_b32 v[102:103],v14 offset0:226 offset1:227
M 52,v[156:159],200
R 132,2,2432
M 52,v[160:163],196
M 52,v[164:167],192
ds_read2_b32 v[104:105],v15 offset0:96 offset1:97
M 52,v[168:171],188
ds_read2_b32 v[106:107],v15 offset0:98 offset1:99
M 52,v[172:175],184
L 15
R 136,2,1920
M 56,v[152:155],208
ds_read2_b32 v[108:109],v15 offset0:224 offset1:225
M 56,v[156:159],204
M 56,v[160:163],200
ds_read2_b32 v[110:111],v15 offset0:226 offset1:227
M 56,v[164:167],196
R 140,2,1408
M 56,v[168:171],192
ds_read2_b32 v[112:113],v16 offset0:96 offset1:97
M 56,v[172:175],188
ds_read2_b32 v[114:115],v16 offset0:98 offset1:99
M 56,v[176:179],184
L 15
M 60,v[152:155],212
R 144,2,896
M 60,v[156:159],208
ds_read2_b32 v[116:117],v16 offset0:224 offset1:225
M 60,v[160:163],204
ds_read2_b32 v[118:119],v16 offset0:226 offset1:227
M 60,v[164:167],200
R 148,2,384
M 60,v[168:171],196
M 60,v[172:175],192
M 60,v[176:179],188
M 60,v[180:183],184
L 15
M 40,v[88:91],128
ds_read2_b32 v[152:153],v1 offset0:112 offset1:113
M 40,v[92:95],124
ds_read2_b32 v[154:155],v1 offset0:114 offset1:115
M 40,v[96:99],120
L 14
R 184,2,4032
M 44,v[88:91],132
ds_read2_b32 v[156:157],v1 offset0:240 offset1:241
M 44,v[92:95],128
M 44,v[96:99],124
ds_read2_b32 v[158:159],v1 offset0:242 offset1:243
v_add_u32 v1,12928,v1
M 44,v[100:103],120
L 14
R 188,2,3520
M 48,v[88:91],136
ds_read2_b32 v[160:161],v14 offset0:112 offset1:113
M 48,v[92:95],132
ds_read2_b32 v[162:163],v14 offset0:114 offset1:115
M 48,v[96:99],128
M 48,v[100:103],124
R 192,2,3008
M 48,v[104:107],120
L 15
ds_read2_b32 v[164:165],v14 offset0:240 offset1:241
M 52,v[88:91],140
ds_read2_b32 v[166:167],v14 offset0:242 offset1:243
v_add_u32 v14,12928,v14
M 52,v[92:95],136
R 196,2,2496
M 52,v[96:99],132
M 52,v[100:103],128
ds_read2_b32 v[168:169],v15 offset0:112 offset1:113
M 52,v[104:107],124
ds_read2_b32 v[170:171],v15 offset0:114 offset1:115
M 52,v[108:111],120
L 15
R 200,2,1984
M 56,v[88:91],144
ds_read2_b32 v[172:173],v15 offset0:240 offset1:241
M 56,v[92:95],140
M 56,v[96:99],136
ds_read2_b32 v[174:175],v15 offset0:242 offset1:243
v_add_u32 v15,12928,v15
M 56,v[100:103],132
R 204,2,1472
M 56,v[104:107],128
ds_read2_b32 v[176:177],v16 offset0:112 offset1:113
M 56,v[108:111],124
ds_read2_b32 v[178:179],v16 offset0:114 offset1:115
M 56,v[112:115],120
L 15
M 60,v[88:91],148
R 208,2,960
M 60,v[92:95],144
)"
R"(ds_read2_b32 v[180:181],v16 offset0:240 offset1:241
M 60,v[96:99],140
ds_read2_b32 v[182:183],v16 offset0:242 offset1:243
v_add_u32 v16,12928,v16
M 60,v[100:103],136
R 212,2,448
M 60,v[104:107],132
M 60,v[108:111],128
M 60,v[112:115],124
M 60,v[116:119],120
L 15
M 40,v[152:155],192
ds_read2_b32 v[88:89],v1 offset0:0 offset1:1
M 40,v[156:159],188
ds_read2_b32 v[90:91],v1 offset0:2 offset1:3
M 40,v[160:163],184
L 14
R 120,2,16512
M 44,v[152:155],196
ds_read2_b32 v[92:93],v1 offset0:128 offset1:129
M 44,v[156:159],192
M 44,v[160:163],188
ds_read2_b32 v[94:95],v1 offset0:130 offset1:131
M 44,v[164:167],184
L 14
R 124,2,16000
M 48,v[152:155],200
ds_read2_b32 v[96:97],v14 offset0:0 offset1:1
M 48,v[156:159],196
ds_read2_b32 v[98:99],v14 offset0:2 offset1:3
M 48,v[160:163],192
M 48,v[164:167],188
R 128,2,15488
M 48,v[168:171],184
L 15
ds_read2_b32 v[100:101],v14 offset0:128 offset1:129
M 52,v[152:155],204
ds_read2_b32 v[102:103],v14 offset0:130 offset1:131
M 52,v[156:159],200
R 132,2,14976
M 52,v[160:163],196
M 52,v[164:167],192
ds_read2_b32 v[104:105],v15 offset0:0 offset1:1
M 52,v[168:171],188
ds_read2_b32 v[106:107],v15 offset0:2 offset1:3
M 52,v[172:175],184
L 15
R 136,2,14464
M 56,v[152:155],208
ds_read2_b32 v[108:109],v15 offset0:128 offset1:129
M 56,v[156:159],204
M 56,v[160:163],200
ds_read2_b32 v[110:111],v15 offset0:130 offset1:131
M 56,v[164:167],196
R 140,2,13952
M 56,v[168:171],192
ds_read2_b32 v[112:113],v16 offset0:0 offset1:1
M 56,v[172:175],188
ds_read2_b32 v[114:115],v16 offset0:2 offset1:3
M 56,v[176:179],184
L 15
M 60,v[152:155],212
R 144,2,13440
M 60,v[156:159],208
ds_read2_b32 v[116:117],v16 offset0:128 offset1:129
M 60,v[160:163],204
ds_read2_b32 v[118:119],v16 offset0:130 offset1:131
M 60,v[164:167],200
R 148,2,12928
M 60,v[168:171],196
M 60,v[172:175],192
M 60,v[176:179],188
M 60,v[180:183],184
s_cmp_eq_u32 s51,2
s_cbranch_scc1 Lcv_d_%=
ds_read_b64 v[24:25],v3 offset:2560
ds_read_b64 v[26:27],v3 offset:2048
ds_read_b64 v[28:29],v3 offset:1536
ds_read_b64 v[30:31],v3 offset:1024
ds_read_b64 v[32:33],v3 offset:512
ds_read_b64 v[34:35],v3 offset:0
L 0
v_and_b32 v36,0xffff0000,v25
v_lshlrev_b32 v37,16,v25
v_and_b32 v38,0xffff0000,v24
v_lshlrev_b32 v39,16,v24
v_fma_f32 v40,v36,s86,v40
v_fma_f32 v41,v37,s86,v41
v_fma_f32 v42,v38,s86,v42
v_fma_f32 v43,v39,s86,v43
v_cvt_pk_bf16_f32 v40,v40,v41
v_cvt_pk_bf16_f32 v41,v42,v43
ds_write_b64 v4,v[40:41] offset:0
v_and_b32 v36,0xffff0000,v27
v_lshlrev_b32 v37,16,v27
v_and_b32 v38,0xffff0000,v26
v_lshlrev_b32 v39,16,v26
v_fma_f32 v44,v36,s86,v44
v_fma_f32 v45,v37,s86,v45
v_fma_f32 v46,v38,s86,v46
v_fma_f32 v47,v39,s86,v47
v_cvt_pk_bf16_f32 v44,v44,v45
v_cvt_pk_bf16_f32 v45,v46,v47
ds_write_b64 v4,v[44:45] offset:512
v_and_b32 v36,0xffff0000,v29
v_lshlrev_b32 v37,16,v29
v_and_b32 v38,0xffff0000,v28
v_lshlrev_b32 v39,16,v28
v_fma_f32 v48,v36,s86,v48
v_fma_f32 v49,v37,s86,v49
v_fma_f32 v50,v38,s86,v50
v_fma_f32 v51,v39,s86,v51
v_cvt_pk_bf16_f32 v48,v48,v49
v_cvt_pk_bf16_f32 v49,v50,v51
ds_write_b64 v4,v[48:49] offset:1024
v_and_b32 v36,0xffff0000,v31
v_lshlrev_b32 v37,16,v31
v_and_b32 v38,0xffff0000,v30
v_lshlrev_b32 v39,16,v30
v_fma_f32 v52,v36,s86,v52
v_fma_f32 v53,v37,s86,v53
v_fma_f32 v54,v38,s86,v54
v_fma_f32 v55,v39,s86,v55
v_cvt_pk_bf16_f32 v52,v52,v53
v_cvt_pk_bf16_f32 v53,v54,v55
ds_write_b64 v4,v[52:53] offset:1536
v_and_b32 v36,0xffff0000,v33
v_lshlrev_b32 v37,16,v33
v_and_b32 v38,0xffff0000,v32
v_lshlrev_b32 v39,16,v32
v_fma_f32 v56,v36,s86,v56
v_fma_f32 v57,v37,s86,v57
v_fma_f32 v58,v38,s86,v58
v_fma_f32 v59,v39,s86,v59
v_cvt_pk_bf16_f32 v56,v56,v57
v_cvt_pk_bf16_f32 v57,v58,v59
ds_write_b64 v4,v[56:57] offset:2048
v_and_b32 v36,0xffff0000,v35
v_lshlrev_b32 v37,16,v35
v_and_b32 v38,0xffff0000,v34
v_lshlrev_b32 v39,16,v34
v_fma_f32 v60,v36,s86,v60
v_fma_f32 v61,v37,s86,v61
v_fma_f32 v62,v38,s86,v62
v_fma_f32 v63,v39,s86,v63
v_cvt_pk_bf16_f32 v60,v60,v61
v_cvt_pk_bf16_f32 v61,v62,v63
ds_write_b64 v4,v[60:61] offset:2560
s_branch Lcv_e_%=
Lcv_d_%=:
L 15
v_mfma_f32_16x16x32_bf16 v[64:67],v[88:91],v[128:131],0
ds_read2_b32 v[152:153],v1 offset0:16 offset1:17
ds_read_b64 v[24:25],v3 offset:2560
M 64,v[92:95],124
ds_read2_b32 v[154:155],v1 offset0:18 offset1:19
ds_read_b64 v[26:27],v3 offset:2048
M 64,v[96:99],120
L 15
R 184,2,16576
ds_read_b64 v[28:29],v3 offset:1536
v_mfma_f32_16x16x32_bf16 v[68:71],v[88:91],v[132:135],0
ds_read2_b32 v[156:157],v1 offset0:144 offset1:145
ds_read_b64 v[30:31],v3 offset:1024
M 68,v[92:95],128
ds_read_b64 v[32:33],v3 offset:512
ds_read_b64 v[34:35],v3 offset:0
M 68,v[96:99],124
ds_read2_b32 v[158:159],v1 offset0:146 offset1:147
M 68,v[100:103],120
L 15
R 188,2,16064
v_mfma_f32_16x16x32_bf16 v[72:75],v[88:91],v[136:139],0
ds_read2_b32 v[160:161],v14 offset0:16 offset1:17
M 72,v[92:95],132
s_mov_b32 m0,163584
s_mov_b32 s78,0
ds_read2_b32 v[162:163],v14 offset0:18 offset1:19
M 72,v[96:99],128
M 72,v[100:103],124
L 4
R 192,2,15552
v_and_b32 v36,0xffff0000,v25
M 72,v[104:107],120
s_nop 0
ds_read2_b32 v[164:165],v14 offset0:144 offset1:145
v_lshlrev_b32 v37,16,v25
v_mfma_f32_16x16x32_bf16 v[76:79],v[88:91],v[140:143],0
ds_read2_b32 v[166:167],v14 offset0:146 offset1:147
v_and_b32 v38,0xffff0000,v24
M 76,v[92:95],136
R 196,2,15040
v_lshlrev_b32 v39,16,v24
M 76,v[96:99],132
buffer_load_dword v12,s[60:63],s78 offen offset:0  lds
M 76,v[100:103],128
ds_read2_b32 v[168:169],v15 offset0:16 offset1:17
v_fma_f32 v40,v36,s86,v40
M 76,v[104:107],124
ds_read2_b32 v[170:171],v15 offset0:18 offset1:19
v_fma_f32 v41,v37,s86,v41
M 76,v[108:111],120
s_nop 0
R 200,2,14528
v_fma_f32 v42,v38,s86,v42
v_mfma_f32_16x16x32_bf16 v[80:83],v[88:91],v[144:147],0
ds_read2_b32 v[172:173],v15 offset0:144 offset1:145
v_fma_f32 v43,v39,s86,v43
M 80,v[92:95],140
v_cvt_pk_bf16_f32 v40,v40,v41
v_cvt_pk_bf16_f32 v41,v42,v43
M 80,v[96:99],136
ds_read2_b32 v[174:175],v15 offset0:146 offset1:147
v_and_b32 v36,0xffff0000,v27
M 80,v[100:103],132
R 204,2,14016
v_lshlrev_b32 v37,16,v27
M 80,v[104:107],128
ds_read2_b32 v[176:177],v16 offset0:16 offset1:17
v_and_b32 v38,0xffff0000,v26
M 80,v[108:111],124
ds_read2_b32 v[178:179],v16 offset0:18 offset1:19
v_lshlrev_b32 v39,16,v26
M 80,v[112:115],120
s_nop 0
ds_write_b64 v4,v[40:41] offset:0
v_mfma_f32_16x16x32_bf16 v[84:87],v[88:91],v[148:151],0
R 208,2,13504
v_fma_f32 v44,v36,s86,v44
M 84,v[92:95],144
ds_read2_b32 v[180:181],v16 offset0:144 offset1:145
v_fma_f32 v45,v37,s86,v45
M 84,v[96:99],140
ds_read2_b32 v[182:183],v16 offset0:146 offset1:147
v_fma_f32 v46,v38,s86,v46
M 84,v[100:103],136
R 212,2,12992
v_fma_f32 v47,v39,s86,v47
M 84,v[104:107],132
v_cvt_pk_bf16_f32 v44,v44,v45
v_cvt_pk_bf16_f32 v45,v46,v47
M 84,v[108:111],128
ds_write_b64 v4,v[44:45] offset:512
M 84,v[112:115],124
v_and_b32 v36,0xffff0000,v29
v_lshlrev_b32 v37,16,v29
M 84,v[116:119],120
L 15
v_and_b32 v38,0xffff0000,v28
v_lshlrev_b32 v39,16,v28
M 64,v[152:155],192
ds_read2_b32 v[88:89],v1 offset0:32 offset1:33
v_fma_f32 v48,v36,s86,v48
M 64,v[156:159],188
ds_read2_b32 v[90:91],v1 offset0:34 offset1:35
v_fma_f32 v49,v37,s86,v49
M 64,v[160:163],184
L 15
R 120,2,16640
v_fma_f32 v50,v38,s86,v50
M 68,v[152:155],196
ds_read2_b32 v[92:93],v1 offset0:160 offset1:161
v_fma_f32 v51,v39,s86,v51
M 68,v[156:159],192
v_cvt_pk_bf16_f32 v48,v48,v49
v_cvt_pk_bf16_f32 v49,v50,v51
M 68,v[160:163],188
ds_read2_b32 v[94:95],v1 offset0:162 offset1:163
v_and_b32 v36,0xffff0000,v31
M 68,v[164:167],184
L 15
R 124,2,16128
v_lshlrev_b32 v37,16,v31
M 72,v[152:155],200
ds_read2_b32 v[96:97],v14 offset0:32 offset1:33
v_and_b32 v38,0xffff0000,v30
M 72,v[156:159],196
ds_read2_b32 v[98:99],v14 offset0:34 offset1:35
v_lshlrev_b32 v39,16,v30
M 72,v[160:163],192
ds_write_b64 v4,v[48:49] offset:1024
M 72,v[164:167],188
R 128,2,15616
v_fma_f32 v52,v36,s86,v52
M 72,v[168:171],184
L 15
ds_read2_b32 v[100:101],v14 offset0:160 offset1:161
v_fma_f32 v53,v37,s86,v53
M 76,v[152:155],204
ds_read2_b32 v[102:103],v14 offset0:162 offset1:163
v_fma_f32 v54,v38,s86,v54
M 76,v[156:159],200
R 132,2,15104
v_fma_f32 v55,v39,s86,v55
M 76,v[160:163],196
v_cvt_pk_bf16_f32 v52,v52,v53
v_cvt_pk_bf16_f32 v53,v54,v55
M 76,v[164:167],192
ds_read2_b32 v[104:105],v15 offset0:32 offset1:33
v_and_b32 v36,0xffff0000,v33
M 76,v[168:171],188
ds_read2_b32 v[106:107],v15 offset0:34 offset1:35
v_lshlrev_b32 v37,16,v33
M 76,v[172:175],184
L 15
R 136,2,14592
v_and_b32 v38,0xffff0000,v32
M 80,v[152:155],208
ds_read2_b32 v[108:109],v15 offset0:160 offset1:161
v_lshlrev_b32 v39,16,v32
M 80,v[156:159],204
ds_write_b64 v4,v[52:53] offset:1536
M 80,v[160:163],200
ds_read2_b32 v[110:111],v15 offset0:162 offset1:163
v_fma_f32 v56,v36,s86,v56
M 80,v[164:167],196
R 140,2,14080
v_fma_f32 v57,v37,s86,v57
M 80,v[168:171],192
ds_read2_b32 v[112:113],v16 offset0:32 offset1:33
v_fma_f32 v58,v38,s86,v58
M 80,v[172:175],188
ds_read2_b32 v[114:115],v16 offset0:34 offset1:35
v_fma_f32 v59,v39,s86,v59
M 80,v[176:179],184
L 15
v_cvt_pk_bf16_f32 v56,v56,v57
v_cvt_pk_bf16_f32 v57,v58,v59
M 84,v[152:155],212
R 144,2,13568
v_and_b32 v36,0xffff0000,v35
M 84,v[156:159],208
ds_read2_b32 v[116:117],v16 offset0:160 offset1:161
v_lshlrev_b32 v37,16,v35
M 84,v[160:163],204
ds_read2_b32 v[118:119],v16 offset0:162 offset1:163
v_and_b32 v38,0xffff0000,v34
M 84,v[164:167],200
R 148,2,13056
v_lshlrev_b32 v39,16,v34
M 84,v[168:171],196
ds_write_b64 v4,v[56:57] offset:2048
M 84,v[172:175],192
v_fma_f32 v60,v36,s86,v60
v_fma_f32 v61,v37,s86,v61
M 84,v[176:179],188
v_fma_f32 v62,v38,s86,v62
v_fma_f32 v63,v39,s86,v63
M 84,v[180:183],184
L 15
v_cvt_pk_bf16_f32 v60,v60,v61
v_cvt_pk_bf16_f32 v61,v62,v63
M 64,v[88:91],128
ds_read2_b32 v[152:153],v1 offset0:48 offset1:49
M 64,v[92:95],124
ds_read2_b32 v[154:155],v1 offset0:50 offset1:51
M 64,v[96:99],120
L 15
R 184,2,16704
M 68,v[88:91],132
ds_read2_b32 v[156:157],v1 offset0:176 offset1:177
M 68,v[92:95],128
ds_write_b64 v4,v[60:61] offset:2560
M 68,v[96:99],124
ds_read2_b32 v[158:159],v1 offset0:178 offset1:179
M 68,v[100:103],120
L 15
R 188,2,16192
M 72,v[88:91],136
ds_read2_b32 v[160:161],v14 offset0:48 offset1:49
M 72,v[92:95],132
ds_read2_b32 v[162:163],v14 offset0:50 offset1:51
M 72,v[96:99],128
M 72,v[100:103],124
R 192,2,15680
M 72,v[104:107],120
L 15
ds_read2_b32 v[164:165],v14 offset0:176 offset1:177
M 76,v[88:91],140
ds_read2_b32 v[166:167],v14 offset0:178 offset1:179
M 76,v[92:95],136
R 196,2,15168
M 76,v[96:99],132
M 76,v[100:103],128
ds_read2_b32 v[168:169],v15 offset0:48 offset1:49
M 76,v[104:107],124
ds_read2_b32 v[170:171],v15 offset0:50 offset1:51
M 76,v[108:111],120
L 15
R 200,2,14656
M 80,v[88:91],144
ds_read2_b32 v[172:173],v15 offset0:176 offset1:177
M 80,v[92:95],140
M 80,v[96:99],136
ds_read2_b32 v[174:175],v15 offset0:178 offset1:179
M 80,v[100:103],132
R 204,2,14144
M 80,v[104:107],128
s_mov_b32 m0,163584
s_mov_b32 s78,1847296
ds_read2_b32 v[176:177],v16 offset0:48 offset1:49
M 80,v[108:111],124
ds_read2_b32 v[178:179],v16 offset0:50 offset1:51
M 80,v[112:115],120
L 15
buffer_load_dword v12,s[60:63],s78 offen offset:0  lds
M 84,v[88:91],148
R 208,2,13632
M 84,v[92:95],144
ds_read2_b32 v[180:181],v16 offset0:176 offset1:177
M 84,v[96:99],140
ds_read2_b32 v[182:183],v16 offset0:178 offset1:179
M 84,v[100:103],136
R 212,2,13120
M 84,v[104:107],132
M 84,v[108:111],128
M 84,v[112:115],124
M 84,v[116:119],120
L 15
M 64,v[152:155],192
ds_read2_b32 v[88:89],v1 offset0:64 offset1:65
M 64,v[156:159],188
ds_read2_b32 v[90:91],v1 offset0:66 offset1:67
M 64,v[160:163],184
L 14
R 120,2,16768
M 68,v[152:155],196
ds_read2_b32 v[92:93],v1 offset0:192 offset1:193
M 68,v[156:159],192
M 68,v[160:163],188
ds_read2_b32 v[94:95],v1 offset0:194 offset1:195
M 68,v[164:167],184
L 14
R 124,2,16256
M 72,v[152:155],200
ds_read2_b32 v[96:97],v14 offset0:64 offset1:65
M 72,v[156:159],196
ds_read2_b32 v[98:99],v14 offset0:66 offset1:67
M 72,v[160:163],192
M 72,v[164:167],188
R 128,2,15744
M 72,v[168:171],184
L 15
ds_read2_b32 v[100:101],v14 offset0:192 offset1:193
M 76,v[152:155],204
ds_read2_b32 v[102:103],v14 offset0:194 offset1:195
M 76,v[156:159],200
R 132,2,15232
M 76,v[160:163],196
M 76,v[164:167],192
ds_read2_b32 v[104:105],v15 offset0:64 offset1:65
M 76,v[168:171],188
ds_read2_b32 v[106:107],v15 offset0:66 offset1:67
M 76,v[172:175],184
L 15
R 136,2,14720
M 80,v[152:155],208
ds_read2_b32 v[108:109],v15 offset0:192 offset1:193
M 80,v[156:159],204
M 80,v[160:163],200
ds_read2_b32 v[110:111],v15 offset0:194 offset1:195
M 80,v[164:167],196
R 140,2,14208
M 80,v[168:171],192
ds_read2_b32 v[112:113],v16 offset0:64 offset1:65
M 80,v[172:175],188
ds_read2_b32 v[114:115],v16 offset0:66 offset1:67
M 80,v[176:179],184
L 15
M 84,v[152:155],212
R 144,2,13696
M 84,v[156:159],208
ds_read2_b32 v[116:117],v16 offset0:192 offset1:193
M 84,v[160:163],204
ds_read2_b32 v[118:119],v16 offset0:194 offset1:195
M 84,v[164:167],200
R 148,2,13184
M 84,v[168:171],196
M 84,v[172:175],192
M 84,v[176:179],188
M 84,v[180:183],184
L 15
M 64,v[88:91],128
ds_read2_b32 v[152:153],v1 offset0:80 offset1:81
M 64,v[92:95],124
ds_read2_b32 v[154:155],v1 offset0:82 offset1:83
M 64,v[96:99],120
L 14
R 184,2,16832
M 68,v[88:91],132
ds_read2_b32 v[156:157],v1 offset0:208 offset1:209
M 68,v[92:95],128
M 68,v[96:99],124
ds_read2_b32 v[158:159],v1 offset0:210 offset1:211
M 68,v[100:103],120
L 14
R 188,2,16320
M 72,v[88:91],136
ds_read2_b32 v[160:161],v14 offset0:80 offset1:81
M 72,v[92:95],132
)"
R"(ds_read2_b32 v[162:163],v14 offset0:82 offset1:83
M 72,v[96:99],128
M 72,v[100:103],124
R 192,2,15808
M 72,v[104:107],120
L 15
ds_read2_b32 v[164:165],v14 offset0:208 offset1:209
M 76,v[88:91],140
ds_read2_b32 v[166:167],v14 offset0:210 offset1:211
M 76,v[92:95],136
R 196,2,15296
M 76,v[96:99],132
M 76,v[100:103],128
ds_read2_b32 v[168:169],v15 offset0:80 offset1:81
M 76,v[104:107],124
ds_read2_b32 v[170:171],v15 offset0:82 offset1:83
M 76,v[108:111],120
L 15
R 200,2,14784
M 80,v[88:91],144
ds_read2_b32 v[172:173],v15 offset0:208 offset1:209
M 80,v[92:95],140
M 80,v[96:99],136
ds_read2_b32 v[174:175],v15 offset0:210 offset1:211
M 80,v[100:103],132
R 204,2,14272
M 80,v[104:107],128
ds_read2_b32 v[176:177],v16 offset0:80 offset1:81
M 80,v[108:111],124
ds_read2_b32 v[178:179],v16 offset0:82 offset1:83
M 80,v[112:115],120
L 15
M 84,v[88:91],148
R 208,2,13760
M 84,v[92:95],144
ds_read2_b32 v[180:181],v16 offset0:208 offset1:209
M 84,v[96:99],140
ds_read2_b32 v[182:183],v16 offset0:210 offset1:211
M 84,v[100:103],136
R 212,2,13248
M 84,v[104:107],132
M 84,v[108:111],128
M 84,v[112:115],124
M 84,v[116:119],120
L 15
M 64,v[152:155],192
ds_read2_b32 v[88:89],v1 offset0:96 offset1:97
M 64,v[156:159],188
ds_read2_b32 v[90:91],v1 offset0:98 offset1:99
M 64,v[160:163],184
L 14
R 120,2,16896
M 68,v[152:155],196
ds_read2_b32 v[92:93],v1 offset0:224 offset1:225
M 68,v[156:159],192
M 68,v[160:163],188
ds_read2_b32 v[94:95],v1 offset0:226 offset1:227
M 68,v[164:167],184
L 14
R 124,2,16384
M 72,v[152:155],200
ds_read2_b32 v[96:97],v14 offset0:96 offset1:97
M 72,v[156:159],196
ds_read2_b32 v[98:99],v14 offset0:98 offset1:99
M 72,v[160:163],192
M 72,v[164:167],188
R 128,2,15872
M 72,v[168:171],184
L 15
ds_read2_b32 v[100:101],v14 offset0:224 offset1:225
M 76,v[152:155],204
ds_read2_b32 v[102:103],v14 offset0:226 offset1:227
M 76,v[156:159],200
R 132,2,15360
M 76,v[160:163],196
M 76,v[164:167],192
ds_read2_b32 v[104:105],v15 offset0:96 offset1:97
M 76,v[168:171],188
ds_read2_b32 v[106:107],v15 offset0:98 offset1:99
M 76,v[172:175],184
L 15
R 136,2,14848
M 80,v[152:155],208
ds_read2_b32 v[108:109],v15 offset0:224 offset1:225
M 80,v[156:159],204
M 80,v[160:163],200
ds_read2_b32 v[110:111],v15 offset0:226 offset1:227
M 80,v[164:167],196
R 140,2,14336
M 80,v[168:171],192
ds_read2_b32 v[112:113],v16 offset0:96 offset1:97
M 80,v[172:175],188
ds_read2_b32 v[114:115],v16 offset0:98 offset1:99
M 80,v[176:179],184
L 15
M 84,v[152:155],212
R 144,2,13824
M 84,v[156:159],208
ds_read2_b32 v[116:117],v16 offset0:224 offset1:225
M 84,v[160:163],204
ds_read2_b32 v[118:119],v16 offset0:226 offset1:227
M 84,v[164:167],200
R 148,2,13312
M 84,v[168:171],196
M 84,v[172:175],192
M 84,v[176:179],188
M 84,v[180:183],184
L 15
M 64,v[88:91],128
ds_read2_b32 v[152:153],v1 offset0:112 offset1:113
M 64,v[92:95],124
ds_read2_b32 v[154:155],v1 offset0:114 offset1:115
M 64,v[96:99],120
L 14
R 184,2,16960
M 68,v[88:91],132
ds_read2_b32 v[156:157],v1 offset0:240 offset1:241
M 68,v[92:95],128
M 68,v[96:99],124
ds_read2_b32 v[158:159],v1 offset0:242 offset1:243
M 68,v[100:103],120
L 14
R 188,2,16448
M 72,v[88:91],136
ds_read2_b32 v[160:161],v14 offset0:112 offset1:113
M 72,v[92:95],132
ds_read2_b32 v[162:163],v14 offset0:114 offset1:115
M 72,v[96:99],128
M 72,v[100:103],124
R 192,2,15936
M 72,v[104:107],120
L 15
ds_read2_b32 v[164:165],v14 offset0:240 offset1:241
M 76,v[88:91],140
ds_read2_b32 v[166:167],v14 offset0:242 offset1:243
M 76,v[92:95],136
R 196,2,15424
M 76,v[96:99],132
M 76,v[100:103],128
ds_read2_b32 v[168:169],v15 offset0:112 offset1:113
M 76,v[104:107],124
ds_read2_b32 v[170:171],v15 offset0:114 offset1:115
M 76,v[108:111],120
L 15
R 200,2,14912
M 80,v[88:91],144
ds_read2_b32 v[172:173],v15 offset0:240 offset1:241
M 80,v[92:95],140
M 80,v[96:99],136
ds_read2_b32 v[174:175],v15 offset0:242 offset1:243
M 80,v[100:103],132
R 204,2,14400
M 80,v[104:107],128
ds_read2_b32 v[176:177],v16 offset0:112 offset1:113
M 80,v[108:111],124
ds_read2_b32 v[178:179],v16 offset0:114 offset1:115
M 80,v[112:115],120
L 15
M 84,v[88:91],148
R 208,2,13888
M 84,v[92:95],144
ds_read2_b32 v[180:181],v16 offset0:240 offset1:241
M 84,v[96:99],140
ds_read2_b32 v[182:183],v16 offset0:242 offset1:243
M 84,v[100:103],136
R 212,2,13376
M 84,v[104:107],132
M 84,v[108:111],128
M 84,v[112:115],124
M 84,v[116:119],120
L 15
M 64,v[152:155],192
M 64,v[156:159],188
M 64,v[160:163],184
L 12
M 68,v[152:155],196
M 68,v[156:159],192
M 68,v[160:163],188
M 68,v[164:167],184
L 9
M 72,v[152:155],200
M 72,v[156:159],196
M 72,v[160:163],192
ds_read_b64 v[24:25],v3 offset:15488
ds_read_b64 v[26:27],v3 offset:14976
M 72,v[164:167],188
ds_read_b64 v[28:29],v3 offset:14464
ds_read_b64 v[30:31],v3 offset:13952
M 72,v[168:171],184
L 10
ds_read_b64 v[32:33],v3 offset:13440
ds_read_b64 v[34:35],v3 offset:12928
M 76,v[152:155],204
M 76,v[156:159],200
M 76,v[160:163],196
M 76,v[164:167],192
M 76,v[168:171],188
M 76,v[172:175],184
L 9
M 80,v[152:155],208
M 80,v[156:159],204
M 80,v[160:163],200
M 80,v[164:167],196
M 80,v[168:171],192
M 80,v[172:175],188
M 80,v[176:179],184
L 6
M 84,v[152:155],212
M 84,v[156:159],208
M 84,v[160:163],204
M 84,v[164:167],200
M 84,v[168:171],196
M 84,v[172:175],192
M 84,v[176:179],188
M 84,v[180:183],184
L 0
v_and_b32 v36,0xffff0000,v25
v_lshlrev_b32 v37,16,v25
v_and_b32 v38,0xffff0000,v24
v_lshlrev_b32 v39,16,v24
v_fma_f32 v64,v36,s87,v64
v_fma_f32 v65,v37,s87,v65
v_fma_f32 v66,v38,s87,v66
v_fma_f32 v67,v39,s87,v67
v_cvt_pk_bf16_f32 v64,v64,v65
v_cvt_pk_bf16_f32 v65,v66,v67
ds_write_b64 v4,v[64:65] offset:12928
v_and_b32 v36,0xffff0000,v27
v_lshlrev_b32 v37,16,v27
v_and_b32 v38,0xffff0000,v26
v_lshlrev_b32 v39,16,v26
v_fma_f32 v68,v36,s87,v68
v_fma_f32 v69,v37,s87,v69
v_fma_f32 v70,v38,s87,v70
v_fma_f32 v71,v39,s87,v71
v_cvt_pk_bf16_f32 v68,v68,v69
v_cvt_pk_bf16_f32 v69,v70,v71
ds_write_b64 v4,v[68:69] offset:13440
v_and_b32 v36,0xffff0000,v29
v_lshlrev_b32 v37,16,v29
v_and_b32 v38,0xffff0000,v28
v_lshlrev_b32 v39,16,v28
v_fma_f32 v72,v36,s87,v72
v_fma_f32 v73,v37,s87,v73
v_fma_f32 v74,v38,s87,v74
v_fma_f32 v75,v39,s87,v75
v_cvt_pk_bf16_f32 v72,v72,v73
v_cvt_pk_bf16_f32 v73,v74,v75
ds_write_b64 v4,v[72:73] offset:13952
v_and_b32 v36,0xffff0000,v31
v_lshlrev_b32 v37,16,v31
v_and_b32 v38,0xffff0000,v30
v_lshlrev_b32 v39,16,v30
v_fma_f32 v76,v36,s87,v76
v_fma_f32 v77,v37,s87,v77
v_fma_f32 v78,v38,s87,v78
v_fma_f32 v79,v39,s87,v79
v_cvt_pk_bf16_f32 v76,v76,v77
v_cvt_pk_bf16_f32 v77,v78,v79
ds_write_b64 v4,v[76:77] offset:14464
v_and_b32 v36,0xffff0000,v33
v_lshlrev_b32 v37,16,v33
v_and_b32 v38,0xffff0000,v32
v_lshlrev_b32 v39,16,v32
v_fma_f32 v80,v36,s87,v80
v_fma_f32 v81,v37,s87,v81
v_fma_f32 v82,v38,s87,v82
v_fma_f32 v83,v39,s87,v83
v_cvt_pk_bf16_f32 v80,v80,v81
v_cvt_pk_bf16_f32 v81,v82,v83
ds_write_b64 v4,v[80:81] offset:14976
v_and_b32 v36,0xffff0000,v35
v_lshlrev_b32 v37,16,v35
v_and_b32 v38,0xffff0000,v34
v_lshlrev_b32 v39,16,v34
v_fma_f32 v84,v36,s87,v84
v_fma_f32 v85,v37,s87,v85
v_fma_f32 v86,v38,s87,v86
v_fma_f32 v87,v39,s87,v87
v_cvt_pk_bf16_f32 v84,v84,v85
v_cvt_pk_bf16_f32 v85,v86,v87
ds_write_b64 v4,v[84:85] offset:15488
Lcv_e_%=:
L 0
s_barrier
R 152,5,0
R 156,5,512
R 160,5,1024
R 164,5,1536
R 168,5,2048
R 172,5,2560
s_mov_b32 s78,99614720
L 5
buffer_store_dwordx4 v[152:155],v7,s[36:39],s78 offen offset:0 sc0 sc1
s_add_u32 s78,s56,100139008
L 4
buffer_store_dwordx4 v[156:159],v7,s[36:39],s78 offen offset:0 sc0 sc1
s_mov_b32 s78,100663296
L 3
buffer_store_dwordx4 v[160:163],v7,s[36:39],s78 offen offset:0 sc0 sc1
s_add_u32 s78,s56,101187584
L 2
buffer_store_dwordx4 v[164:167],v7,s[36:39],s78 offen offset:0 sc0 sc1
s_mov_b32 s78,101711872
L 1
buffer_store_dwordx4 v[168:171],v7,s[36:39],s78 offen offset:0 sc0 sc1
s_add_u32 s78,s56,102236160
L 0
buffer_store_dwordx4 v[172:175],v7,s[36:39],s78 offen offset:0 sc0 sc1
s_waitcnt vmcnt(0)
s_barrier
s_add_u32 s48,s48,32768
buffer_store_dword v23,v10,s[36:39],s48 offen offset:0 sc0 sc1
s_cmp_eq_u32 s51,2
s_cbranch_scc0 Lcv_f_%=
s_mov_b32 m0,163584
s_mov_b32 s78,3694592
buffer_load_dword v12,s[60:63],s78 offen offset:0  lds
s_mov_b32 m0,163584
s_mov_b32 s78,5541888
buffer_load_dword v12,s[60:63],s78 offen offset:0  lds
Lcv_f_%=:
Lcode_cv_e:
.purgem M
.purgem R
.purgem T
.purgem D
.purgem L
)"
  :: "s"(ws), "s"(__builtin_amdgcn_readfirstlane(sWave)), "s"(__builtin_amdgcn_readfirstlane(sBuf)), "s"(__builtin_amdgcn_readfirstlane(sAuxOff)), "s"(__builtin_amdgcn_readfirstlane(sFoutOff)), "s"(__builtin_amdgcn_readfirstlane(sC0)), "s"(__builtin_amdgcn_readfirstlane(sC1)), "s"(__builtin_amdgcn_readfirstlane(sExp)), "v"(vL16), "v"(vPA), "v"(vPB), "v"(vPU), "v"(vPW), "v"(vOR), "v"(vORA), "v"(vOG), "v"(vFA), "v"(vFB), "v"(vFlag), "v"(vTW), "v"(vTX), "s"(w3), "s"(w4) : "v0", "v1", "v2", "v3", "v4", "v5", "v6", "v7", "v8", "v9", "v10", "v11", "v12", "v13", "v14", "v15", "v16", "v17", "v18", "v19", "v20", "v21", "v22", "v23", "v24", "v25", "v26", "v27", "v28", "v29", "v30", "v31", "v32", "v33", "v34", "v35", "v36", "v37", "v38", "v39", "v40", "v41", "v42", "v43", "v44", "v45", "v46", "v47", "v48", "v49", "v50", "v51", "v52", "v53", "v54", "v55", "v56", "v57", "v58", "v59", "v60", "v61", "v62", "v63", "v64", "v65", "v66", "v67", "v68", "v69", "v70", "v71", "v72", "v73", "v74", "v75", "v76", "v77", "v78", "v79", "v80", "v81", "v82", "v83", "v84", "v85", "v86", "v87", "v88", "v89", "v90", "v91", "v92", "v93", "v94", "v95", "v96", "v97", "v98", "v99", "v100", "v101", "v102", "v103", "v104", "v105", "v106", "v107", "v108", "v109", "v110", "v111", "v112", "v113", "v114", "v115", "v116", "v117", "v118", "v119", "v120", "v121", "v122", "v123", "v124", "v125", "v126", "v127", "v128", "v129", "v130", "v131", "v132", "v133", "v134", "v135", "v136", "v137", "v138", "v139", "v140", "v141", "v142", "v143", "v144", "v145", "v146", "v147", "v148", "v149", "v150", "v151", "v152", "v153", "v154", "v155", "v156", "v157", "v158", "v159", "v160", "v161", "v162", "v163", "v164", "v165", "v166", "v167", "v168", "v169", "v170", "v171", "v172", "v173", "v174", "v175", "v176", "v177", "v178", "v179", "v180", "v181", "v182", "v183", "v184", "v185", "v186", "v187", "v188", "v189", "v190", "v191", "v192", "v193", "v194", "v195", "v196", "v197", "v198", "v199", "v200", "v201", "v202", "v203", "v204", "v205", "v206", "v207", "v208", "v209", "v210", "v211", "v212", "v213", "v214", "v215", "v216", "v217", "v218", "v219", "v220", "v221", "v222", "v223", "v224", "v225", "v226", "v227", "v228", "v229", "v230", "v231", "a0", "a1", "a2", "a3", "a4", "a5", "a6", "a7", "a8", "a9", "a10", "a11", "a12", "a13", "a14", "a15", "a16", "a17", "a18", "a19", "a20", "a21", "a22", "a23", "a24", "a25", "a26", "a27", "a28", "a29", "a30", "a31", "a32", "a33", "a34", "a35", "a36", "a37", "a38", "a39", "a40", "a41", "a42", "a43", "a44", "a45", "a46", "a47", "a48", "a49", "a50", "a51", "a52", "a53", "a54", "a55", "a56", "a57", "a58", "a59", "a60", "a61", "a62", "a63", "a64", "a65", "a66", "a67", "a68", "a69", "a70", "a71", "a72", "a73", "a74", "a75", "a76", "a77", "a78", "a79", "a80", "a81", "a82", "a83", "a84", "a85", "a86", "a87", "a88", "a89", "a90", "a91", "a92", "a93", "a94", "a95", "a96", "a97", "a98", "a99", "a100", "a101", "a102", "a103", "a104", "a105", "a106", "a107", "a108", "a109", "a110", "a111", "a112", "a113", "a114", "a115", "a116", "a117", "a118", "a119", "a120", "a121", "a122", "a123", "a124", "a125", "a126", "a127", "a128", "a129", "a130", "a131", "a132", "a133", "a134", "a135", "a136", "a137", "a138", "a139", "a140", "a141", "a142", "a143", "a144", "a145", "a146", "a147", "a148", "a149", "a150", "a151", "a152", "a153", "a154", "a155", "a156", "a157", "a158", "a159", "a160", "a161", "a162", "a163", "a164", "a165", "a166", "a167", "a168", "a169", "a170", "a171", "a172", "a173", "a174", "a175", "a176", "a177", "a178", "a179", "a180", "a181", "a182", "a183", "a184", "a185", "a186", "a187", "a188", "a189", "a190", "a191", "a192", "a193", "a194", "a195", "a196", "a197", "a198", "a199", "a200", "a201", "a202", "a203", "a204", "a205", "a206", "a207", "a208", "a209", "a210", "a211", "a212", "a213", "a214", "a215", "a216", "a217", "a218", "a219", "a220", "a221", "a222", "a223", "a224", "a225", "a226", "a227", "a228", "a229", "a230", "a231", "a232", "a233", "a234", "a235", "a236", "a237", "a238", "a239", "a240", "a241", "a242", "a243", "a244", "a245", "a246", "a247", "a248", "a249", "a250", "a251", "a252", "a253", "a254", "a255", "s36", "s37", "s38", "s39", "s40", "s41", "s42", "s43", "s44", "s45", "s46", "s47", "s48", "s49", "s50", "s51", "s52", "s53", "s54", "s55", "s56", "s57", "s58", "s59", "s60", "s61", "s62", "s63", "s64", "s65", "s66", "s67", "s68", "s69", "s70", "s71", "s72", "s73", "s74", "s75", "s76", "s77", "s78", "s79", "s80", "s81", "s82", "s83", "s84", "s85", "s86", "s87", "s88", "s89", "s90", "s91", "s92", "s93", "s94", "s95", "s96", "s97", "vcc", "scc", "memory");
    LANE_ID(lq_);
    const int g = QT0 + (NCV - 1 - iw) * 164 + min(wave * 64 + lq_, 163);
    if (QTB) touch_lds((const char*)q + (size_t)(g < 32768 ? g : 0) * 128, sScr);
  }
  WAIT_LGKM0();
  __builtin_amdgcn_s_barrier();
  CTS(10);
  }
  const unsigned sDbg0 = 110u * 1048576u + (b * 4 + wave) * 8192;
  constexpr unsigned OOB = 0x80000000u;
  if (kind == 6) {
    LANE_ID(lr);
    const int r8 = lr & 7, seg = lr >> 3, j = lr & 15, g = lr >> 4, rr = 8 * g + (j >> 2);
    const unsigned sWave = wave, sExp = __builtin_amdgcn_readfirstlane(ep), sG0 = slice < 16 ? slice + 1 : 0, sStep = NRW, sDbg = sDbg0;
    const unsigned sWw = ((slice >> 3) * 1792 + 16 * wave) * 2048 + (slice & 7) * 256;
    const unsigned vWd0 = seg * 2048 + (r8 ^ swzB(seg)) * 16, vWd1 = (seg + 8) * 2048 + (r8 ^ swzB(8 + seg)) * 16;
    const unsigned vWr0 = (wave >> 1) * 8192 + rr * 128 + (((wave & 1) * 4 + (lr & 3)) ^ swzB(rr)) * 16, vWr1 = vWr0 + 8;
    const unsigned vRF = (wave * 8 + (seg >> 2)) * 1024 + ((seg & 3) * 16 + r8) * 16;
    const unsigned vRwr = seg == 0 ? r8 * 64 + wave * 4 : 512 + lr * 4, vRrd = r8 * 64;
    const unsigned vRP = g == (j >> 2) ? r8 * 64 + 16 + wave * 8 + (seg & 1) * 4 : 768 + lr * 4;
    const unsigned vRS = (wave == 0 && seg == 0) ? r8 * 16 : OOB, vFlag = lr == 0 ? 0u : OOB;
    const unsigned vL4 = ((lr & 2) * 4 + wave * 2 + (lr & 1)) * FSP, vLY = min((wave < 3 ? wave * 32 : 112) + lr, wave < 2 ? wave * 32 + 31 : wave == 2 ? 111 : NCV - 1) * 128;
    const unsigned vQ = r8 * 2048 + wave * 512 + seg * 16;
    const unsigned vY0 = wave * 4096 + lr * 16;
    const unsigned vTr = 4096 + wave * 4096 + ((2 * g + ((j & 3) >> 1)) * 8 + (j >> 2)) * 16 + (j & 1) * 8;
    const unsigned one = g == (j >> 2) ? 0x3f80u << (16 * (j & 1)) : 0u, vSa = (j & 2) ? 0u : one, vSb = (j & 2) ? one : 0u;
    (void)sDbg;
    asm volatile(
R"(.macro M d,w,x
v_mfma_f32_16x16x32_bf16 v[\d:\d+3], \w, v[\x:\x+3], v[\d:\d+3]
.endm
.macro R d,a,o
ds_read_b128 v[\d:\d+3], v\a offset:\o
.endm
.macro T d,a,o
ds_read_b64_tr_b16 \d, v\a offset:\o
.endm
.macro D a,s,o
buffer_load_dwordx4 v\a, s[36:39], s\s offen offset:\o sc1 lds
.endm
.macro L c
s_waitcnt lgkmcnt(\c)
.endm
Lcode_r_b:
s_mov_b64 s[36:37],%0
s_mov_b32 s38,111243392
s_mov_b32 s39,0x00020000
s_mov_b32 s44,%1
s_mov_b32 s45,%2
s_mov_b32 s46,%3
s_mov_b32 s50,%4
s_mov_b32 s53,%5
v_mov_b32 v0,%6
v_mov_b32 v1,%7
v_mov_b32 v2,%8
v_mov_b32 v3,%9
v_mov_b32 v4,%10
v_mov_b32 v5,%11
v_mov_b32 v6,%12
v_mov_b32 v7,%13
v_mov_b32 v8,%14
v_mov_b32 v9,%15
v_mov_b32 v10,%16
v_mov_b32 v11,%17
v_mov_b32 v12,%18
v_mov_b32 v13,%19
v_mov_b32 v14,%20
v_mov_b32 v15,%21
v_mov_b32 v16,%22
s_mov_b32 s47,256
s_mov_b32 s49,0
s_mov_b32 s93,s45
s_mov_b64 s[84:85],%23
s_mov_b32 s86,4194304
s_mov_b32 s87,0x00020000
s_lshl_b32 s55,s45,14
buffer_load_dwordx4 v[164:167],v8,s[84:87],s55 offen offset:0 nt
buffer_load_dwordx4 v[168:171],v8,s[84:87],s55 offen offset:128 nt
buffer_load_dwordx4 v[172:175],v8,s[84:87],s55 offen offset:256 nt
buffer_load_dwordx4 v[176:179],v8,s[84:87],s55 offen offset:384 nt
v_mov_b32 v17,s53
v_mbcnt_lo_u32_b32 v19,-1,0
v_mbcnt_hi_u32_b32 v19,-1,v19
v_lshlrev_b32 v19,4,v19
s_mov_b32 s82,0
s_mov_b32 s63,0x7fff0000
s_mov_b32 s64,0
s_mov_b32 s67,0
s_lshl_b32 s65,s44,12
s_add_u32 s65,s65,4096
s_mul_i32 s66,s44,128
s_add_u32 s66,s66,109056000
s_mov_b32 s70,0xaaaaaaaa
s_mov_b32 s71,0xaaaaaaaa
s_mov_b32 s72,0xcccccccc
s_mov_b32 s73,0xcccccccc
s_mov_b32 s74,0xff00ff00
s_mov_b32 s75,0xff00ff00
s_mov_b32 s68,0x3a800000
s_mov_b32 s69,0x3727c5ac
s_mov_b32 s76,0xbeeff558
v_mov_b32 v32,0x3f803f80
v_mov_b32 v33,0x3f803f80
v_mov_b32 v34,0x3f803f80
v_mov_b32 v35,0x3f803f80
v_mov_b32 v24,0
v_mov_b32 v25,0
v_mov_b32 v26,v11
v_mov_b32 v27,v12
v_mov_b32 v28,0
v_mov_b32 v29,0
s_getpc_b64 s[94:95]
Lr_pc_%=:
s_add_u32 s94,s94,Lr_tab_%=-Lr_pc_%=
s_addc_u32 s95,s95,0
s_mov_b32 s81,0x7fff0000
s_lshl_b32 s54,s44,11
s_add_u32 s80,s45,85
s_cmp_eq_u32 s45,0
s_cselect_b32 s80,-1,s80
s_waitcnt vmcnt(0)
Lr_a0_%=:
s_cmp_lg_u32 s67,0
s_cbranch_scc1 Lr_b0_%=
s_waitcnt vmcnt(14)
v_mfma_f32_16x16x32_bf16 v[36:39],v[32:35],v[164:167],0
s_add_u32 s79,s45,s46
s_cmp_lt_u32 s45,64
s_cselect_b32 s78,s49,0
v_mfma_f32_16x16x32_bf16 v[40:43],v[164:167],v[164:167],0
s_cmp_ge_u32 s79,64
s_cselect_b32 s78,s78,0
s_add_u32 s62,s79,s78
M 36,v[32:35],168
s_lshr_b32 s78,s45,1
s_and_b32 s79,s45,1
s_lshl_b32 s79,s79,7
M 40,v[168:171],168
s_lshl_b32 s57,s78,15
s_lshl_b32 s55,s62,14
s_cmp_lt_u32 s62,s47
M 36,v[32:35],172
s_cselect_b32 s55,s55,0x7fff0000
s_add_u32 s57,s57,s79
s_lshl_b32 s59,s78,9
M 40,v[172:175],172
buffer_load_dwordx4 v[196:199],v8,s[84:87],s55 offen offset:0 nt
M 36,v[32:35],176
buffer_load_dwordx4 v[200:203],v8,s[84:87],s55 offen offset:128 nt
M 40,v[176:179],176
buffer_load_dwordx4 v[204:207],v8,s[84:87],s55 offen offset:256 nt
v_mfma_f32_16x16x32_bf16 v[116:119],v[26:29],v[164:167],0
buffer_load_dwordx4 v[208:211],v8,s[84:87],s55 offen offset:384 nt
v_mfma_f32_16x16x32_bf16 v[120:123],v[24:27],v[164:167],0
s_add_u32 s57,s57,8388608
s_add_u32 s59,s59,s79
s_lshl_b32 s60,s45,9
v_mfma_f32_16x16x32_bf16 v[124:127],v[26:29],v[168:171],0
s_add_u32 s58,s57,4096
s_add_u32 s59,s59,106954752
s_add_u32 s60,s60,s66
v_mfma_f32_16x16x32_bf16 v[128:131],v[24:27],v[168:171],0
v_add_f32_dpp v44,v36,v36 row_ror:8 row_mask:0xf bank_mask:0xf
v_cndmask_b32 v45,v40,v41,s[70:71]
v_mfma_f32_16x16x32_bf16 v[132:135],v[26:29],v[172:175],0
ds_write_b32 v2,v44 offset:0
v_mfma_f32_16x16x32_bf16 v[136:139],v[24:27],v[172:175],0
v_cndmask_b32 v46,v42,v43,s[70:71]
v_cndmask_b32 v45,v45,v46,s[72:73]
v_mfma_f32_16x16x32_bf16 v[140:143],v[26:29],v[176:179],0
ds_write_b32 v0,v45 offset:0
v_mfma_f32_16x16x32_bf16 v[144:147],v[24:27],v[176:179],0
L 0
s_barrier
R 52,3,0
R 56,3,16
R 60,3,32
s_waitcnt vmcnt(8)
buffer_store_dword v17,v5,s[36:39],s63 offen offset:0 sc0 sc1
s_mov_b32 s63,0x7fff0000
s_cmp_lt_u32 s45,64
s_cselect_b32 s64,-192,0
s_add_u32 s64,s64,256
s_cmp_lt_u32 s45,64
s_cselect_b32 s61,0,32768
s_add_u32 s61,s61,111177728
s_mul_i32 s56,s45,16384
s_add_u32 s56,s56,98566144
Lr_bpy0_%=:
buffer_load_dword v18,v7,s[36:39],s61 offen offset:0 sc1
s_add_u32 s82,s82,1
s_waitcnt vmcnt(0)
v_cmp_eq_u32 vcc,s53,v18
s_nop 0
s_cmp_eq_u64 vcc,-1
s_cbranch_scc1 Lr_bqy0_%=
s_sleep 2
s_cmp_lt_u32 s82,20000
s_cbranch_scc1 Lr_bpy0_%=
Lr_bqy0_%=:
s_add_u32 m0,s65,0
s_nop 0
D 9,56,0
D 9,56,1024
D 9,56,2048
D 9,56,3072
s_waitcnt vmcnt(0)
T v[68:69],10,0
T v[70:71],10,64
T v[72:73],10,1024
T v[74:75],10,1088
T v[76:77],10,2048
T v[78:79],10,2112
T v[80:81],10,3072
T v[82:83],10,3136
s_mov_b32 s45,s62
s_min_u32 s78,s64,s47
s_mul_i32 s56,s45,16384
s_add_u32 s56,s56,98566144
s_cmp_lt_u32 s45,s78
s_cselect_b32 s67,1,0
s_cselect_b32 s56,s56,0x7fff0000
s_add_u32 m0,s65,16384
s_nop 0
D 9,56,0
D 9,56,1024
D 9,56,2048
D 9,56,3072
L 4
v_lshlrev_b32 v84,16,v68
v_and_b32 v85,0xffff0000,v68
v_lshlrev_b32 v86,16,v69
v_and_b32 v87,0xffff0000,v69
v_lshlrev_b32 v88,16,v70
v_and_b32 v89,0xffff0000,v70
v_lshlrev_b32 v90,16,v71
v_and_b32 v91,0xffff0000,v71
v_lshlrev_b32 v92,16,v72
v_and_b32 v93,0xffff0000,v72
v_lshlrev_b32 v94,16,v73
v_and_b32 v95,0xffff0000,v73
v_lshlrev_b32 v96,16,v74
v_and_b32 v97,0xffff0000,v74
v_lshlrev_b32 v98,16,v75
v_and_b32 v99,0xffff0000,v75
L 0
v_lshlrev_b32 v100,16,v76
v_and_b32 v101,0xffff0000,v76
v_lshlrev_b32 v102,16,v77
v_and_b32 v103,0xffff0000,v77
v_lshlrev_b32 v104,16,v78
v_and_b32 v105,0xffff0000,v78
v_lshlrev_b32 v106,16,v79
v_and_b32 v107,0xffff0000,v79
v_lshlrev_b32 v108,16,v80
v_and_b32 v109,0xffff0000,v80
v_lshlrev_b32 v110,16,v81
v_and_b32 v111,0xffff0000,v81
v_lshlrev_b32 v112,16,v82
v_and_b32 v113,0xffff0000,v82
v_lshlrev_b32 v114,16,v83
v_and_b32 v115,0xffff0000,v83
s_branch Lr_c0_%=
Lr_b0_%=:
s_waitcnt vmcnt(10)
T v[68:69],10,0
T v[70:71],10,64
T v[72:73],10,1024
T v[74:75],10,1088
T v[76:77],10,2048
T v[78:79],10,2112
T v[80:81],10,3072
T v[82:83],10,3136
v_mfma_f32_16x16x32_bf16 v[36:39],v[32:35],v[164:167],0
s_add_u32 s79,s45,s46
s_cmp_lt_u32 s45,64
s_cselect_b32 s78,s49,0
v_mfma_f32_16x16x32_bf16 v[40:43],v[164:167],v[164:167],0
s_cmp_ge_u32 s79,64
s_cselect_b32 s78,s78,0
s_add_u32 s62,s79,s78
M 36,v[32:35],168
s_lshr_b32 s78,s45,1
s_and_b32 s79,s45,1
s_lshl_b32 s60,s45,9
M 40,v[168:171],168
s_lshl_b32 s57,s78,15
s_lshl_b32 s59,s78,9
s_mov_b32 s45,s62
M 36,v[32:35],172
s_lshl_b32 s79,s79,7
s_min_u32 s78,s64,s47
s_mul_i32 s56,s45,16384
M 40,v[172:175],172
s_lshl_b32 s55,s62,14
s_cmp_lt_u32 s62,s47
s_cselect_b32 s55,s55,0x7fff0000
M 36,v[32:35],176
buffer_load_dwordx4 v[196:199],v8,s[84:87],s55 offen offset:0 nt
M 40,v[176:179],176
buffer_load_dwordx4 v[200:203],v8,s[84:87],s55 offen offset:128 nt
v_mfma_f32_16x16x32_bf16 v[116:119],v[26:29],v[164:167],0
buffer_load_dwordx4 v[204:207],v8,s[84:87],s55 offen offset:256 nt
v_mfma_f32_16x16x32_bf16 v[120:123],v[24:27],v[164:167],0
buffer_load_dwordx4 v[208:211],v8,s[84:87],s55 offen offset:384 nt
v_mfma_f32_16x16x32_bf16 v[124:127],v[26:29],v[168:171],0
s_add_u32 s57,s57,s79
s_add_u32 s56,s56,98566144
s_cmp_lt_u32 s45,s78
v_mfma_f32_16x16x32_bf16 v[128:131],v[24:27],v[168:171],0
s_cselect_b32 s56,s56,0x7fff0000
v_add_f32_dpp v44,v36,v36 row_ror:8 row_mask:0xf bank_mask:0xf
v_cndmask_b32 v45,v40,v41,s[70:71]
v_mfma_f32_16x16x32_bf16 v[132:135],v[26:29],v[172:175],0
s_cselect_b32 s67,1,0
ds_write_b32 v2,v44 offset:0
v_mfma_f32_16x16x32_bf16 v[136:139],v[24:27],v[172:175],0
s_add_u32 s57,s57,8388608
v_cndmask_b32 v46,v42,v43,s[70:71]
v_cndmask_b32 v45,v45,v46,s[72:73]
v_mfma_f32_16x16x32_bf16 v[140:143],v[26:29],v[176:179],0
s_add_u32 s59,s59,s79
ds_write_b32 v0,v45 offset:0
v_mfma_f32_16x16x32_bf16 v[144:147],v[24:27],v[176:179],0
L 2
s_add_u32 m0,s65,16384
s_add_u32 s58,s57,4096
v_mfma_f32_16x16x32_bf16 v[84:87],v[26:29],v[68:71],0
D 9,56,0
v_mfma_f32_16x16x32_bf16 v[88:91],v[24:27],v[68:71],0
D 9,56,1024
v_mfma_f32_16x16x32_bf16 v[92:95],v[26:29],v[72:75],0
L 0
D 9,56,2048
v_mfma_f32_16x16x32_bf16 v[96:99],v[24:27],v[72:75],0
s_barrier
R 52,3,0
R 56,3,16
v_mfma_f32_16x16x32_bf16 v[100:103],v[26:29],v[76:79],0
s_add_u32 s59,s59,106954752
s_add_u32 s60,s60,s66
R 60,3,32
v_mfma_f32_16x16x32_bf16 v[104:107],v[24:27],v[76:79],0
D 9,56,3072
v_mfma_f32_16x16x32_bf16 v[108:111],v[26:29],v[80:83],0
v_mfma_f32_16x16x32_bf16 v[112:115],v[24:27],v[80:83],0
Lr_c0_%=:
L 0
v_pk_add_f32 v[52:53],v[52:53],v[54:55]
v_pk_add_f32 v[56:57],v[56:57],v[58:59]
v_pk_add_f32 v[60:61],v[60:61],v[62:63]
v_add_f32 v52,v52,v53
v_pk_add_f32 v[56:57],v[56:57],v[60:61]
v_mul_f32 v52,s68,v52
v_add_f32 v53,v56,v57
v_mul_f32 v53,s68,v53
v_fma_f32 v53,-v52,v52,v53
v_max_f32 v53,0,v53
s_waitcnt vmcnt(12)
buffer_store_dword v17,v5,s[36:39],s63 offen offset:0 sc0 sc1
s_mov_b32 s63,0x7fff0000
v_add_f32 v53,s69,v53
v_rsq_f32 v48,v53
s_nop 0
v_mul_f32_e64 v50,-v52,v48
v_mov_b32 v49,v48
v_mov_b32 v51,v50
v_pk_fma_f32 v[52:53],v[116:117],v[48:49],v[50:51]
v_pk_fma_f32 v[54:55],v[118:119],v[48:49],v[50:51]
v_pk_fma_f32 v[56:57],v[120:121],v[48:49],v[50:51]
v_pk_fma_f32 v[58:59],v[122:123],v[48:49],v[50:51]
v_pk_fma_f32 v[116:117],v[52:53],v[84:85],v[116:117]
v_pk_fma_f32 v[118:119],v[54:55],v[86:87],v[118:119]
v_pk_fma_f32 v[120:121],v[56:57],v[88:89],v[120:121]
v_pk_fma_f32 v[122:123],v[58:59],v[90:91],v[122:123]
v_pk_fma_f32 v[52:53],v[124:125],v[48:49],v[50:51]
v_pk_fma_f32 v[54:55],v[126:127],v[48:49],v[50:51]
v_pk_fma_f32 v[56:57],v[128:129],v[48:49],v[50:51]
v_pk_fma_f32 v[58:59],v[130:131],v[48:49],v[50:51]
v_pk_fma_f32 v[124:125],v[52:53],v[92:93],v[124:125]
v_pk_fma_f32 v[126:127],v[54:55],v[94:95],v[126:127]
v_pk_fma_f32 v[128:129],v[56:57],v[96:97],v[128:129]
v_pk_fma_f32 v[130:131],v[58:59],v[98:99],v[130:131]
v_pk_fma_f32 v[52:53],v[132:133],v[48:49],v[50:51]
v_pk_fma_f32 v[54:55],v[134:135],v[48:49],v[50:51]
v_pk_fma_f32 v[56:57],v[136:137],v[48:49],v[50:51]
v_pk_fma_f32 v[58:59],v[138:139],v[48:49],v[50:51]
v_pk_fma_f32 v[132:133],v[52:53],v[100:101],v[132:133]
v_pk_fma_f32 v[134:135],v[54:55],v[102:103],v[134:135]
v_pk_fma_f32 v[136:137],v[56:57],v[104:105],v[136:137]
v_pk_fma_f32 v[138:139],v[58:59],v[106:107],v[138:139]
v_pk_fma_f32 v[52:53],v[140:141],v[48:49],v[50:51]
v_pk_fma_f32 v[54:55],v[142:143],v[48:49],v[50:51]
v_pk_fma_f32 v[56:57],v[144:145],v[48:49],v[50:51]
v_pk_fma_f32 v[58:59],v[146:147],v[48:49],v[50:51]
v_pk_fma_f32 v[140:141],v[52:53],v[108:109],v[140:141]
v_pk_fma_f32 v[142:143],v[54:55],v[110:111],v[142:143]
v_pk_fma_f32 v[144:145],v[56:57],v[112:113],v[144:145]
v_pk_fma_f32 v[146:147],v[58:59],v[114:115],v[146:147]
v_cvt_pk_bf16_f32 v148,v116,v117
v_cvt_pk_bf16_f32 v149,v118,v119
v_cvt_pk_bf16_f32 v150,v120,v121
v_cvt_pk_bf16_f32 v151,v122,v123
v_cvt_pk_bf16_f32 v152,v124,v125
v_cvt_pk_bf16_f32 v153,v126,v127
v_mfma_f32_16x16x32_bf16 v[36:39],v[32:35],v[148:151],0
v_cvt_pk_bf16_f32 v154,v128,v129
v_cvt_pk_bf16_f32 v155,v130,v131
v_mfma_f32_16x16x32_bf16 v[40:43],v[148:151],v[148:151],0
v_cvt_pk_bf16_f32 v156,v132,v133
v_cvt_pk_bf16_f32 v157,v134,v135
M 36,v[32:35],152
v_cvt_pk_bf16_f32 v158,v136,v137
v_cvt_pk_bf16_f32 v159,v138,v139
M 40,v[152:155],152
v_cvt_pk_bf16_f32 v160,v140,v141
v_cvt_pk_bf16_f32 v161,v142,v143
M 36,v[32:35],156
v_cvt_pk_bf16_f32 v162,v144,v145
v_cvt_pk_bf16_f32 v163,v146,v147
M 40,v[156:159],156
buffer_store_dwordx4 v[148:151],v1,s[36:39],s57 offen offset:0 sc0 sc1
M 36,v[32:35],160
buffer_store_dwordx4 v[152:155],v1,s[36:39],s57 offen offset:2048 sc0 sc1
M 40,v[160:163],160
buffer_store_dwordx4 v[156:159],v1,s[36:39],s58 offen offset:0 sc0 sc1
s_nop 4
v_add_f32_dpp v44,v36,v36 row_ror:8 row_mask:0xf bank_mask:0xf
s_nop 0
v_cndmask_b32 v45,v40,v41,s[70:71]
v_cndmask_b32 v46,v42,v43,s[70:71]
ds_write_b32 v2,v44 offset:1024
v_cndmask_b32 v45,v45,v46,s[72:73]
ds_write_b32 v0,v45 offset:1024
buffer_store_dwordx4 v[160:163],v1,s[36:39],s58 offen offset:2048 sc0 sc1
s_waitcnt vmcnt(13) lgkmcnt(0)
s_barrier
R 52,3,1024
R 56,3,1040
R 60,3,1056
L 0
v_pk_add_f32 v[52:53],v[52:53],v[54:55]
v_pk_add_f32 v[56:57],v[56:57],v[58:59]
v_pk_add_f32 v[60:61],v[60:61],v[62:63]
v_add_f32 v52,v52,v53
v_pk_add_f32 v[56:57],v[56:57],v[60:61]
v_mul_f32 v52,s68,v52
v_add_f32 v53,v56,v57
v_mul_f32 v53,s68,v53
v_fma_f32 v53,-v52,v52,v53
v_max_f32 v53,0,v53
v_add_f32 v54,s69,v53
v_fma_f32 v53,s69,v54,v53
v_rsq_f32 v55,v54
v_rsq_f32 v53,v53
v_mul_f32_e64 v54,-v52,v55
v_mul_f32 v53,s76,v53
buffer_store_dwordx4 v[52:55],v4,s[36:39],s59 offen offset:0 sc0 sc1
s_mov_b32 s63,s60
s_cmp_lt_u32 s45,s80
s_cbranch_scc1 Lr_bg0_%=
s_swappc_b64 s[88:89],s[94:95]
s_add_u32 s94,s94,256
s_addc_u32 s95,s95,0
s_branch Lr_bh0_%=
Lr_bg0_%=:
buffer_store_dword v17,v5,s[36:39],s81 offen offset:0 sc0 sc1
buffer_store_dword v17,v5,s[36:39],s81 offen offset:0 sc0 sc1
buffer_store_dword v17,v5,s[36:39],s81 offen offset:0 sc0 sc1
buffer_store_dword v17,v5,s[36:39],s81 offen offset:0 sc0 sc1
Lr_bh0_%=:
s_cmp_lt_u32 s45,s47
s_cbranch_scc0 Lr_mid_%=
Lr_a1_%=:
s_cmp_lg_u32 s67,0
s_cbranch_scc1 Lr_b1_%=
s_waitcnt vmcnt(14)
v_mfma_f32_16x16x32_bf16 v[36:39],v[32:35],v[196:199],0
s_add_u32 s79,s45,s46
s_cmp_lt_u32 s45,64
s_cselect_b32 s78,s49,0
v_mfma_f32_16x16x32_bf16 v[40:43],v[196:199],v[196:199],0
s_cmp_ge_u32 s79,64
s_cselect_b32 s78,s78,0
s_add_u32 s62,s79,s78
M 36,v[32:35],200
s_lshr_b32 s78,s45,1
s_and_b32 s79,s45,1
s_lshl_b32 s79,s79,7
M 40,v[200:203],200
s_lshl_b32 s57,s78,15
s_lshl_b32 s55,s62,14
s_cmp_lt_u32 s62,s47
M 36,v[32:35],204
s_cselect_b32 s55,s55,0x7fff0000
s_add_u32 s57,s57,s79
s_lshl_b32 s59,s78,9
M 40,v[204:207],204
buffer_load_dwordx4 v[164:167],v8,s[84:87],s55 offen offset:0 nt
M 36,v[32:35],208
buffer_load_dwordx4 v[168:171],v8,s[84:87],s55 offen offset:128 nt
M 40,v[208:211],208
buffer_load_dwordx4 v[172:175],v8,s[84:87],s55 offen offset:256 nt
v_mfma_f32_16x16x32_bf16 v[116:119],v[26:29],v[196:199],0
buffer_load_dwordx4 v[176:179],v8,s[84:87],s55 offen offset:384 nt
v_mfma_f32_16x16x32_bf16 v[120:123],v[24:27],v[196:199],0
s_add_u32 s57,s57,8388608
s_add_u32 s59,s59,s79
s_lshl_b32 s60,s45,9
v_mfma_f32_16x16x32_bf16 v[124:127],v[26:29],v[200:203],0
s_add_u32 s58,s57,4096
s_add_u32 s59,s59,106954752
s_add_u32 s60,s60,s66
v_mfma_f32_16x16x32_bf16 v[128:131],v[24:27],v[200:203],0
v_add_f32_dpp v44,v36,v36 row_ror:8 row_mask:0xf bank_mask:0xf
v_cndmask_b32 v45,v40,v41,s[70:71]
v_mfma_f32_16x16x32_bf16 v[132:135],v[26:29],v[204:207],0
ds_write_b32 v2,v44 offset:0
v_mfma_f32_16x16x32_bf16 v[136:139],v[24:27],v[204:207],0
v_cndmask_b32 v46,v42,v43,s[70:71]
v_cndmask_b32 v45,v45,v46,s[72:73]
v_mfma_f32_16x16x32_bf16 v[140:143],v[26:29],v[208:211],0
ds_write_b32 v0,v45 offset:0
v_mfma_f32_16x16x32_bf16 v[144:147],v[24:27],v[208:211],0
L 0
s_barrier
R 52,3,0
R 56,3,16
)"
R"(R 60,3,32
s_waitcnt vmcnt(8)
buffer_store_dword v17,v5,s[36:39],s63 offen offset:0 sc0 sc1
s_mov_b32 s63,0x7fff0000
s_cmp_lt_u32 s45,64
s_cselect_b32 s64,-192,0
s_add_u32 s64,s64,256
s_cmp_lt_u32 s45,64
s_cselect_b32 s61,0,32768
s_add_u32 s61,s61,111177728
s_mul_i32 s56,s45,16384
s_add_u32 s56,s56,98566144
Lr_bpy1_%=:
buffer_load_dword v18,v7,s[36:39],s61 offen offset:0 sc1
s_add_u32 s82,s82,1
s_waitcnt vmcnt(0)
v_cmp_eq_u32 vcc,s53,v18
s_nop 0
s_cmp_eq_u64 vcc,-1
s_cbranch_scc1 Lr_bqy1_%=
s_sleep 2
s_cmp_lt_u32 s82,20000
s_cbranch_scc1 Lr_bpy1_%=
Lr_bqy1_%=:
s_add_u32 m0,s65,16384
s_nop 0
D 9,56,0
D 9,56,1024
D 9,56,2048
D 9,56,3072
s_waitcnt vmcnt(0)
T v[68:69],10,16384
T v[70:71],10,16448
T v[72:73],10,17408
T v[74:75],10,17472
T v[76:77],10,18432
T v[78:79],10,18496
T v[80:81],10,19456
T v[82:83],10,19520
s_mov_b32 s45,s62
s_min_u32 s78,s64,s47
s_mul_i32 s56,s45,16384
s_add_u32 s56,s56,98566144
s_cmp_lt_u32 s45,s78
s_cselect_b32 s67,1,0
s_cselect_b32 s56,s56,0x7fff0000
s_add_u32 m0,s65,0
s_nop 0
D 9,56,0
D 9,56,1024
D 9,56,2048
D 9,56,3072
L 4
v_lshlrev_b32 v84,16,v68
v_and_b32 v85,0xffff0000,v68
v_lshlrev_b32 v86,16,v69
v_and_b32 v87,0xffff0000,v69
v_lshlrev_b32 v88,16,v70
v_and_b32 v89,0xffff0000,v70
v_lshlrev_b32 v90,16,v71
v_and_b32 v91,0xffff0000,v71
v_lshlrev_b32 v92,16,v72
v_and_b32 v93,0xffff0000,v72
v_lshlrev_b32 v94,16,v73
v_and_b32 v95,0xffff0000,v73
v_lshlrev_b32 v96,16,v74
v_and_b32 v97,0xffff0000,v74
v_lshlrev_b32 v98,16,v75
v_and_b32 v99,0xffff0000,v75
L 0
v_lshlrev_b32 v100,16,v76
v_and_b32 v101,0xffff0000,v76
v_lshlrev_b32 v102,16,v77
v_and_b32 v103,0xffff0000,v77
v_lshlrev_b32 v104,16,v78
v_and_b32 v105,0xffff0000,v78
v_lshlrev_b32 v106,16,v79
v_and_b32 v107,0xffff0000,v79
v_lshlrev_b32 v108,16,v80
v_and_b32 v109,0xffff0000,v80
v_lshlrev_b32 v110,16,v81
v_and_b32 v111,0xffff0000,v81
v_lshlrev_b32 v112,16,v82
v_and_b32 v113,0xffff0000,v82
v_lshlrev_b32 v114,16,v83
v_and_b32 v115,0xffff0000,v83
s_branch Lr_c1_%=
Lr_b1_%=:
s_waitcnt vmcnt(10)
T v[68:69],10,16384
T v[70:71],10,16448
T v[72:73],10,17408
T v[74:75],10,17472
T v[76:77],10,18432
T v[78:79],10,18496
T v[80:81],10,19456
T v[82:83],10,19520
v_mfma_f32_16x16x32_bf16 v[36:39],v[32:35],v[196:199],0
s_add_u32 s79,s45,s46
s_cmp_lt_u32 s45,64
s_cselect_b32 s78,s49,0
v_mfma_f32_16x16x32_bf16 v[40:43],v[196:199],v[196:199],0
s_cmp_ge_u32 s79,64
s_cselect_b32 s78,s78,0
s_add_u32 s62,s79,s78
M 36,v[32:35],200
s_lshr_b32 s78,s45,1
s_and_b32 s79,s45,1
s_lshl_b32 s60,s45,9
M 40,v[200:203],200
s_lshl_b32 s57,s78,15
s_lshl_b32 s59,s78,9
s_mov_b32 s45,s62
M 36,v[32:35],204
s_lshl_b32 s79,s79,7
s_min_u32 s78,s64,s47
s_mul_i32 s56,s45,16384
M 40,v[204:207],204
s_lshl_b32 s55,s62,14
s_cmp_lt_u32 s62,s47
s_cselect_b32 s55,s55,0x7fff0000
M 36,v[32:35],208
buffer_load_dwordx4 v[164:167],v8,s[84:87],s55 offen offset:0 nt
M 40,v[208:211],208
buffer_load_dwordx4 v[168:171],v8,s[84:87],s55 offen offset:128 nt
v_mfma_f32_16x16x32_bf16 v[116:119],v[26:29],v[196:199],0
buffer_load_dwordx4 v[172:175],v8,s[84:87],s55 offen offset:256 nt
v_mfma_f32_16x16x32_bf16 v[120:123],v[24:27],v[196:199],0
buffer_load_dwordx4 v[176:179],v8,s[84:87],s55 offen offset:384 nt
v_mfma_f32_16x16x32_bf16 v[124:127],v[26:29],v[200:203],0
s_add_u32 s57,s57,s79
s_add_u32 s56,s56,98566144
s_cmp_lt_u32 s45,s78
v_mfma_f32_16x16x32_bf16 v[128:131],v[24:27],v[200:203],0
s_cselect_b32 s56,s56,0x7fff0000
v_add_f32_dpp v44,v36,v36 row_ror:8 row_mask:0xf bank_mask:0xf
v_cndmask_b32 v45,v40,v41,s[70:71]
v_mfma_f32_16x16x32_bf16 v[132:135],v[26:29],v[204:207],0
s_cselect_b32 s67,1,0
ds_write_b32 v2,v44 offset:0
v_mfma_f32_16x16x32_bf16 v[136:139],v[24:27],v[204:207],0
s_add_u32 s57,s57,8388608
v_cndmask_b32 v46,v42,v43,s[70:71]
v_cndmask_b32 v45,v45,v46,s[72:73]
v_mfma_f32_16x16x32_bf16 v[140:143],v[26:29],v[208:211],0
s_add_u32 s59,s59,s79
ds_write_b32 v0,v45 offset:0
v_mfma_f32_16x16x32_bf16 v[144:147],v[24:27],v[208:211],0
L 2
s_add_u32 m0,s65,0
s_add_u32 s58,s57,4096
v_mfma_f32_16x16x32_bf16 v[84:87],v[26:29],v[68:71],0
D 9,56,0
v_mfma_f32_16x16x32_bf16 v[88:91],v[24:27],v[68:71],0
D 9,56,1024
v_mfma_f32_16x16x32_bf16 v[92:95],v[26:29],v[72:75],0
L 0
D 9,56,2048
v_mfma_f32_16x16x32_bf16 v[96:99],v[24:27],v[72:75],0
s_barrier
R 52,3,0
R 56,3,16
v_mfma_f32_16x16x32_bf16 v[100:103],v[26:29],v[76:79],0
s_add_u32 s59,s59,106954752
s_add_u32 s60,s60,s66
R 60,3,32
v_mfma_f32_16x16x32_bf16 v[104:107],v[24:27],v[76:79],0
D 9,56,3072
v_mfma_f32_16x16x32_bf16 v[108:111],v[26:29],v[80:83],0
v_mfma_f32_16x16x32_bf16 v[112:115],v[24:27],v[80:83],0
Lr_c1_%=:
L 0
v_pk_add_f32 v[52:53],v[52:53],v[54:55]
v_pk_add_f32 v[56:57],v[56:57],v[58:59]
v_pk_add_f32 v[60:61],v[60:61],v[62:63]
v_add_f32 v52,v52,v53
v_pk_add_f32 v[56:57],v[56:57],v[60:61]
v_mul_f32 v52,s68,v52
v_add_f32 v53,v56,v57
v_mul_f32 v53,s68,v53
v_fma_f32 v53,-v52,v52,v53
v_max_f32 v53,0,v53
s_waitcnt vmcnt(12)
buffer_store_dword v17,v5,s[36:39],s63 offen offset:0 sc0 sc1
s_mov_b32 s63,0x7fff0000
v_add_f32 v53,s69,v53
v_rsq_f32 v48,v53
s_nop 0
v_mul_f32_e64 v50,-v52,v48
v_mov_b32 v49,v48
v_mov_b32 v51,v50
v_pk_fma_f32 v[52:53],v[116:117],v[48:49],v[50:51]
v_pk_fma_f32 v[54:55],v[118:119],v[48:49],v[50:51]
v_pk_fma_f32 v[56:57],v[120:121],v[48:49],v[50:51]
v_pk_fma_f32 v[58:59],v[122:123],v[48:49],v[50:51]
v_pk_fma_f32 v[116:117],v[52:53],v[84:85],v[116:117]
v_pk_fma_f32 v[118:119],v[54:55],v[86:87],v[118:119]
v_pk_fma_f32 v[120:121],v[56:57],v[88:89],v[120:121]
v_pk_fma_f32 v[122:123],v[58:59],v[90:91],v[122:123]
v_pk_fma_f32 v[52:53],v[124:125],v[48:49],v[50:51]
v_pk_fma_f32 v[54:55],v[126:127],v[48:49],v[50:51]
v_pk_fma_f32 v[56:57],v[128:129],v[48:49],v[50:51]
v_pk_fma_f32 v[58:59],v[130:131],v[48:49],v[50:51]
v_pk_fma_f32 v[124:125],v[52:53],v[92:93],v[124:125]
v_pk_fma_f32 v[126:127],v[54:55],v[94:95],v[126:127]
v_pk_fma_f32 v[128:129],v[56:57],v[96:97],v[128:129]
v_pk_fma_f32 v[130:131],v[58:59],v[98:99],v[130:131]
v_pk_fma_f32 v[52:53],v[132:133],v[48:49],v[50:51]
v_pk_fma_f32 v[54:55],v[134:135],v[48:49],v[50:51]
v_pk_fma_f32 v[56:57],v[136:137],v[48:49],v[50:51]
v_pk_fma_f32 v[58:59],v[138:139],v[48:49],v[50:51]
v_pk_fma_f32 v[132:133],v[52:53],v[100:101],v[132:133]
v_pk_fma_f32 v[134:135],v[54:55],v[102:103],v[134:135]
v_pk_fma_f32 v[136:137],v[56:57],v[104:105],v[136:137]
v_pk_fma_f32 v[138:139],v[58:59],v[106:107],v[138:139]
v_pk_fma_f32 v[52:53],v[140:141],v[48:49],v[50:51]
v_pk_fma_f32 v[54:55],v[142:143],v[48:49],v[50:51]
v_pk_fma_f32 v[56:57],v[144:145],v[48:49],v[50:51]
v_pk_fma_f32 v[58:59],v[146:147],v[48:49],v[50:51]
v_pk_fma_f32 v[140:141],v[52:53],v[108:109],v[140:141]
v_pk_fma_f32 v[142:143],v[54:55],v[110:111],v[142:143]
v_pk_fma_f32 v[144:145],v[56:57],v[112:113],v[144:145]
v_pk_fma_f32 v[146:147],v[58:59],v[114:115],v[146:147]
v_cvt_pk_bf16_f32 v148,v116,v117
v_cvt_pk_bf16_f32 v149,v118,v119
v_cvt_pk_bf16_f32 v150,v120,v121
v_cvt_pk_bf16_f32 v151,v122,v123
v_cvt_pk_bf16_f32 v152,v124,v125
v_cvt_pk_bf16_f32 v153,v126,v127
v_mfma_f32_16x16x32_bf16 v[36:39],v[32:35],v[148:151],0
v_cvt_pk_bf16_f32 v154,v128,v129
v_cvt_pk_bf16_f32 v155,v130,v131
v_mfma_f32_16x16x32_bf16 v[40:43],v[148:151],v[148:151],0
v_cvt_pk_bf16_f32 v156,v132,v133
v_cvt_pk_bf16_f32 v157,v134,v135
M 36,v[32:35],152
v_cvt_pk_bf16_f32 v158,v136,v137
v_cvt_pk_bf16_f32 v159,v138,v139
M 40,v[152:155],152
v_cvt_pk_bf16_f32 v160,v140,v141
v_cvt_pk_bf16_f32 v161,v142,v143
M 36,v[32:35],156
v_cvt_pk_bf16_f32 v162,v144,v145
v_cvt_pk_bf16_f32 v163,v146,v147
M 40,v[156:159],156
buffer_store_dwordx4 v[148:151],v1,s[36:39],s57 offen offset:0 sc0 sc1
M 36,v[32:35],160
buffer_store_dwordx4 v[152:155],v1,s[36:39],s57 offen offset:2048 sc0 sc1
M 40,v[160:163],160
buffer_store_dwordx4 v[156:159],v1,s[36:39],s58 offen offset:0 sc0 sc1
s_nop 4
v_add_f32_dpp v44,v36,v36 row_ror:8 row_mask:0xf bank_mask:0xf
s_nop 0
v_cndmask_b32 v45,v40,v41,s[70:71]
v_cndmask_b32 v46,v42,v43,s[70:71]
ds_write_b32 v2,v44 offset:1024
v_cndmask_b32 v45,v45,v46,s[72:73]
ds_write_b32 v0,v45 offset:1024
buffer_store_dwordx4 v[160:163],v1,s[36:39],s58 offen offset:2048 sc0 sc1
s_waitcnt vmcnt(13) lgkmcnt(0)
s_barrier
R 52,3,1024
R 56,3,1040
R 60,3,1056
L 0
v_pk_add_f32 v[52:53],v[52:53],v[54:55]
v_pk_add_f32 v[56:57],v[56:57],v[58:59]
v_pk_add_f32 v[60:61],v[60:61],v[62:63]
v_add_f32 v52,v52,v53
v_pk_add_f32 v[56:57],v[56:57],v[60:61]
v_mul_f32 v52,s68,v52
v_add_f32 v53,v56,v57
v_mul_f32 v53,s68,v53
v_fma_f32 v53,-v52,v52,v53
v_max_f32 v53,0,v53
v_add_f32 v54,s69,v53
v_fma_f32 v53,s69,v54,v53
v_rsq_f32 v55,v54
v_rsq_f32 v53,v53
v_mul_f32_e64 v54,-v52,v55
v_mul_f32 v53,s76,v53
buffer_store_dwordx4 v[52:55],v4,s[36:39],s59 offen offset:0 sc0 sc1
s_mov_b32 s63,s60
s_cmp_lt_u32 s45,s80
s_cbranch_scc1 Lr_bg1_%=
s_swappc_b64 s[88:89],s[94:95]
s_add_u32 s94,s94,256
s_addc_u32 s95,s95,0
s_branch Lr_bh1_%=
Lr_bg1_%=:
buffer_store_dword v17,v5,s[36:39],s81 offen offset:0 sc0 sc1
buffer_store_dword v17,v5,s[36:39],s81 offen offset:0 sc0 sc1
buffer_store_dword v17,v5,s[36:39],s81 offen offset:0 sc0 sc1
buffer_store_dword v17,v5,s[36:39],s81 offen offset:0 sc0 sc1
Lr_bh1_%=:
s_cmp_lt_u32 s45,s47
s_cbranch_scc1 Lr_a0_%=
Lr_mid_%=:
s_waitcnt vmcnt(4)
buffer_store_dword v17,v5,s[36:39],s63 offen offset:0 sc0 sc1
s_mov_b32 s63,0x7fff0000
s_mov_b32 s45,s93
s_cmp_eq_u32 s93,0
s_cbranch_scc1 Lr_h0_%=
s_mov_b64 s[40:41],%24
s_mov_b32 s42,7340032
s_mov_b32 s43,0x00020000
s_lshl_b32 s57,s44,11
s_add_u32 s55,s50,1441792
s_add_u32 m0,s57,106496
s_nop 0
buffer_load_dwordx4 v13,s[40:43],s55 offen offset:0  lds
s_add_u32 m0,s57,107520
s_nop 0
buffer_load_dwordx4 v14,s[40:43],s55 offen offset:0  lds
s_add_u32 m0,s57,114560
s_nop 0
buffer_load_dwordx4 v13,s[40:43],s55 offen offset:128  lds
s_add_u32 m0,s57,115584
s_nop 0
buffer_load_dwordx4 v14,s[40:43],s55 offen offset:128  lds
s_add_u32 s55,s50,1572864
s_add_u32 m0,s57,122880
s_nop 0
buffer_load_dwordx4 v13,s[40:43],s55 offen offset:0  lds
s_add_u32 m0,s57,123904
s_nop 0
buffer_load_dwordx4 v14,s[40:43],s55 offen offset:0  lds
s_add_u32 m0,s57,130944
s_nop 0
buffer_load_dwordx4 v13,s[40:43],s55 offen offset:128  lds
s_add_u32 m0,s57,131968
s_nop 0
buffer_load_dwordx4 v14,s[40:43],s55 offen offset:128  lds
s_add_u32 s55,s50,1703936
s_add_u32 m0,s57,139264
s_nop 0
buffer_load_dwordx4 v13,s[40:43],s55 offen offset:0  lds
s_add_u32 m0,s57,140288
s_nop 0
buffer_load_dwordx4 v14,s[40:43],s55 offen offset:0  lds
s_add_u32 m0,s57,147328
s_nop 0
buffer_load_dwordx4 v13,s[40:43],s55 offen offset:128  lds
s_add_u32 m0,s57,148352
s_nop 0
buffer_load_dwordx4 v14,s[40:43],s55 offen offset:128  lds
s_add_u32 s55,s50,1835008
s_add_u32 m0,s57,40960
s_nop 0
buffer_load_dwordx4 v13,s[40:43],s55 offen offset:0  lds
s_add_u32 m0,s57,41984
s_nop 0
buffer_load_dwordx4 v14,s[40:43],s55 offen offset:0  lds
s_add_u32 m0,s57,49024
s_nop 0
buffer_load_dwordx4 v13,s[40:43],s55 offen offset:128  lds
s_add_u32 m0,s57,50048
s_nop 0
buffer_load_dwordx4 v14,s[40:43],s55 offen offset:128  lds
s_add_u32 s55,s50,1966080
s_add_u32 m0,s57,57344
s_nop 0
buffer_load_dwordx4 v13,s[40:43],s55 offen offset:0  lds
s_add_u32 m0,s57,58368
s_nop 0
buffer_load_dwordx4 v14,s[40:43],s55 offen offset:0  lds
s_add_u32 m0,s57,65408
s_nop 0
buffer_load_dwordx4 v13,s[40:43],s55 offen offset:128  lds
s_add_u32 m0,s57,66432
s_nop 0
buffer_load_dwordx4 v14,s[40:43],s55 offen offset:128  lds
Lr_h0_%=:
s_lshr_b32 s78,s45,1
s_and_b32 s79,s45,1
s_lshl_b32 s79,s79,7
s_lshl_b32 s55,s78,15
s_add_u32 s55,s55,s79
s_mul_i32 s56,s45,16384
s_add_u32 s56,s56,98566144
s_cmp_lt_u32 s45,256
s_cselect_b32 s56,s56,0x7fff0000
s_lshl_b32 s61,s78,11
s_add_u32 s61,s61,110587904
s_add_u32 s78,s45,s46
s_lshr_b32 s78,s78,1
s_lshl_b32 s62,s78,11
s_add_u32 s62,s62,110587904
s_add_u32 m0,s65,0
s_nop 0
D 9,56,0
D 9,56,1024
D 9,56,2048
D 9,56,3072
Lr_bps_%=:
buffer_load_dword v18,v6,s[36:39],s61 offen offset:0 sc1
s_add_u32 s82,s82,1
s_waitcnt vmcnt(0)
v_cmp_eq_u32 vcc,s53,v18
s_nop 0
s_cmp_eq_u64 vcc,-1
s_cbranch_scc1 Lr_bqs_%=
s_sleep 2
s_cmp_lt_u32 s82,20000
s_cbranch_scc1 Lr_bps_%=
Lr_bqs_%=:
s_add_u32 s78,s55,71303168
buffer_load_dwordx4 v[164:167],v1,s[36:39],s78 offen offset:0 sc1
buffer_load_dwordx4 v[168:171],v1,s[36:39],s78 offen offset:2048 sc1
s_add_u32 s78,s55,71307264
buffer_load_dwordx4 v[172:175],v1,s[36:39],s78 offen offset:0 sc1
buffer_load_dwordx4 v[176:179],v1,s[36:39],s78 offen offset:2048 sc1
s_add_u32 s78,s55,89128960
buffer_load_dwordx4 v[180:183],v1,s[36:39],s78 offen offset:0 sc1
buffer_load_dwordx4 v[184:187],v1,s[36:39],s78 offen offset:2048 sc1
s_add_u32 s78,s55,89133056
buffer_load_dwordx4 v[188:191],v1,s[36:39],s78 offen offset:0 sc1
buffer_load_dwordx4 v[192:195],v1,s[36:39],s78 offen offset:2048 sc1
buffer_load_dword v18,v6,s[36:39],s62 offen offset:0 sc1
s_cmp_eq_u32 s93,0
s_cbranch_scc1 Lr_h2_%=
s_mov_b64 s[40:41],%24
s_mov_b32 s42,7340032
s_mov_b32 s43,0x00020000
s_lshl_b32 s57,s44,11
L 0
s_barrier
s_add_u32 s55,s50,2097152
s_add_u32 m0,s57,73728
s_nop 0
buffer_load_dwordx4 v13,s[40:43],s55 offen offset:0  lds
s_add_u32 m0,s57,74752
s_nop 0
buffer_load_dwordx4 v14,s[40:43],s55 offen offset:0  lds
s_add_u32 m0,s57,81792
s_nop 0
buffer_load_dwordx4 v13,s[40:43],s55 offen offset:128  lds
s_add_u32 m0,s57,82816
s_nop 0
buffer_load_dwordx4 v14,s[40:43],s55 offen offset:128  lds
v_add_u32 v44,90112,v15
v_add_u32 v45,90112,v16
T a[160:161],44,0
T a[162:163],44,512
T a[164:165],45,0
T a[166:167],45,512
T a[168:169],44,4096
T a[170:171],44,4608
T a[172:173],45,4096
T a[174:175],45,4608
L 0
s_barrier
s_add_u32 s55,s50,2228224
s_add_u32 m0,s57,90112
s_nop 0
buffer_load_dwordx4 v13,s[40:43],s55 offen offset:0  lds
s_add_u32 m0,s57,91136
s_nop 0
buffer_load_dwordx4 v14,s[40:43],s55 offen offset:0  lds
s_add_u32 m0,s57,98176
s_nop 0
buffer_load_dwordx4 v13,s[40:43],s55 offen offset:128  lds
s_add_u32 m0,s57,99200
s_nop 0
buffer_load_dwordx4 v14,s[40:43],s55 offen offset:128  lds
v_add_u32 v44,106496,v15
v_add_u32 v45,106496,v16
T a[176:177],44,0
T a[178:179],44,512
T a[180:181],45,0
T a[182:183],45,512
T a[184:185],44,4096
T a[186:187],44,4608
T a[188:189],45,4096
T a[190:191],45,4608
L 0
s_barrier
s_add_u32 s55,s50,2359296
s_add_u32 m0,s57,106496
s_nop 0
buffer_load_dwordx4 v13,s[40:43],s55 offen offset:0  lds
s_add_u32 m0,s57,107520
s_nop 0
buffer_load_dwordx4 v14,s[40:43],s55 offen offset:0  lds
s_add_u32 m0,s57,114560
s_nop 0
buffer_load_dwordx4 v13,s[40:43],s55 offen offset:128  lds
s_add_u32 m0,s57,115584
s_nop 0
buffer_load_dwordx4 v14,s[40:43],s55 offen offset:128  lds
v_add_u32 v44,122880,v15
v_add_u32 v45,122880,v16
T a[192:193],44,0
T a[194:195],44,512
T a[196:197],45,0
T a[198:199],45,512
T a[200:201],44,4096
T a[202:203],44,4608
T a[204:205],45,4096
)"
R"(T a[206:207],45,4608
L 0
s_barrier
s_add_u32 s55,s50,2490368
s_add_u32 m0,s57,122880
s_nop 0
buffer_load_dwordx4 v13,s[40:43],s55 offen offset:0  lds
s_add_u32 m0,s57,123904
s_nop 0
buffer_load_dwordx4 v14,s[40:43],s55 offen offset:0  lds
s_add_u32 m0,s57,130944
s_nop 0
buffer_load_dwordx4 v13,s[40:43],s55 offen offset:128  lds
s_add_u32 m0,s57,131968
s_nop 0
buffer_load_dwordx4 v14,s[40:43],s55 offen offset:128  lds
v_add_u32 v44,139264,v15
v_add_u32 v45,139264,v16
T a[208:209],44,0
T a[210:211],44,512
T a[212:213],45,0
T a[214:215],45,512
T a[216:217],44,4096
T a[218:219],44,4608
T a[220:221],45,4096
T a[222:223],45,4608
L 0
s_barrier
s_add_u32 s55,s50,2621440
s_add_u32 m0,s57,139264
s_nop 0
buffer_load_dwordx4 v13,s[40:43],s55 offen offset:0  lds
s_add_u32 m0,s57,140288
s_nop 0
buffer_load_dwordx4 v14,s[40:43],s55 offen offset:0  lds
s_add_u32 m0,s57,147328
s_nop 0
buffer_load_dwordx4 v13,s[40:43],s55 offen offset:128  lds
s_add_u32 m0,s57,148352
s_nop 0
buffer_load_dwordx4 v14,s[40:43],s55 offen offset:128  lds
v_add_u32 v44,40960,v15
v_add_u32 v45,40960,v16
T a[224:225],44,0
T a[226:227],44,512
T a[228:229],45,0
T a[230:231],45,512
T a[232:233],44,4096
T a[234:235],44,4608
T a[236:237],45,4096
T a[238:239],45,4608
L 0
s_barrier
s_add_u32 s55,s50,2752512
s_add_u32 m0,s57,40960
s_nop 0
buffer_load_dwordx4 v13,s[40:43],s55 offen offset:0  lds
s_add_u32 m0,s57,41984
s_nop 0
buffer_load_dwordx4 v14,s[40:43],s55 offen offset:0  lds
s_add_u32 m0,s57,49024
s_nop 0
buffer_load_dwordx4 v13,s[40:43],s55 offen offset:128  lds
s_add_u32 m0,s57,50048
s_nop 0
buffer_load_dwordx4 v14,s[40:43],s55 offen offset:128  lds
v_add_u32 v44,57344,v15
v_add_u32 v45,57344,v16
T a[240:241],44,0
T a[242:243],44,512
T a[244:245],45,0
T a[246:247],45,512
T a[248:249],44,4096
T a[250:251],44,4608
T a[252:253],45,4096
T a[254:255],45,4608
L 0
s_barrier
s_waitcnt vmcnt(24)
Lr_h2_%=:
s_lshr_b32 s78,s45,1
s_and_b32 s79,s45,1
s_lshl_b32 s79,s79,7
s_lshl_b32 s57,s78,15
s_add_u32 s57,s57,s79
s_add_u32 s57,s57,12582912
s_add_u32 s58,s57,4096
s_lshl_b32 s59,s78,9
s_add_u32 s59,s59,s79
s_add_u32 s59,s59,107020288
s_lshl_b32 s60,s45,9
s_add_u32 s60,s60,s66
s_add_u32 s60,s60,131072
s_add_u32 s45,s45,s46
s_lshr_b32 s78,s45,1
s_and_b32 s79,s45,1
s_lshl_b32 s79,s79,7
s_lshl_b32 s55,s78,15
s_add_u32 s55,s55,s79
s_mul_i32 s56,s45,16384
s_add_u32 s56,s56,98566144
s_cmp_lt_u32 s45,256
s_cselect_b32 s56,s56,0x7fff0000
s_lshl_b32 s61,s78,11
s_add_u32 s61,s61,110587904
s_add_u32 s78,s45,s46
s_lshr_b32 s78,s78,1
s_lshl_b32 s62,s78,11
s_add_u32 s62,s62,110587904
s_waitcnt vmcnt(0)
Lr_it0_%=:
T v[68:69],10,0
T v[70:71],10,64
T v[72:73],10,1024
T v[74:75],10,1088
T v[76:77],10,2048
T v[78:79],10,2112
T v[80:81],10,3072
T v[82:83],10,3136
s_nop 0
v_cmp_eq_u32 vcc,s53,v18
s_nop 0
s_cmp_eq_u64 vcc,-1
s_cselect_b32 s78,s45,256
s_cmp_lt_u32 s78,256
s_cbranch_scc1 Lr_ta0_%=
buffer_load_dword v18,v6,s[36:39],s61 offen offset:0 sc1
v_mfma_f32_16x16x32_bf16 v[148:151],v[164:167],v[180:183],0
s_add_u32 m0,s65,16384
M 148,v[168:171],184
D 9,56,0
M 148,v[172:175],188
D 9,56,1024
M 148,v[176:179],192
L 4
D 9,56,2048
v_mfma_f32_16x16x32_bf16 v[40:43],v[164:167],v[164:167],0
D 9,56,3072
M 40,v[168:171],168
L 0
v_lshlrev_b32 v84,16,v68
v_and_b32 v85,0xffff0000,v68
M 40,v[172:175],172
v_lshlrev_b32 v86,16,v69
v_and_b32 v87,0xffff0000,v69
M 40,v[176:179],176
v_lshlrev_b32 v88,16,v70
v_and_b32 v89,0xffff0000,v70
M 40,v[180:183],180
v_lshlrev_b32 v90,16,v71
v_and_b32 v91,0xffff0000,v71
M 40,v[184:187],184
v_lshlrev_b32 v92,16,v72
v_and_b32 v93,0xffff0000,v72
M 40,v[188:191],188
v_lshlrev_b32 v94,16,v73
v_and_b32 v95,0xffff0000,v73
M 40,v[192:195],192
v_lshlrev_b32 v96,16,v74
v_and_b32 v97,0xffff0000,v74
v_mfma_f32_16x16x32_bf16 v[36:39],v[32:35],v[164:167],0
v_lshlrev_b32 v98,16,v75
v_and_b32 v99,0xffff0000,v75
M 36,v[32:35],168
v_lshlrev_b32 v100,16,v76
v_and_b32 v101,0xffff0000,v76
M 36,v[32:35],172
v_lshlrev_b32 v102,16,v77
v_and_b32 v103,0xffff0000,v77
M 36,v[32:35],176
v_lshlrev_b32 v104,16,v78
v_and_b32 v105,0xffff0000,v78
M 36,v[32:35],180
v_fma_f32 v40,2.0,v148,v40
v_fma_f32 v41,2.0,v149,v41
M 36,v[32:35],184
v_fma_f32 v42,2.0,v150,v42
v_fma_f32 v43,2.0,v151,v43
M 36,v[32:35],188
v_cndmask_b32 v45,v40,v41,s[70:71]
v_cndmask_b32 v46,v42,v43,s[70:71]
M 36,v[32:35],192
v_cndmask_b32 v45,v45,v46,s[72:73]
v_lshlrev_b32 v106,16,v79
v_mfma_f32_16x16x32_bf16 v[116:119],v[26:29],v[164:167],0
v_and_b32 v107,0xffff0000,v79
v_lshlrev_b32 v108,16,v80
M 116,v[26:29],180
v_and_b32 v109,0xffff0000,v80
v_lshlrev_b32 v110,16,v81
v_mfma_f32_16x16x32_bf16 v[120:123],v[24:27],v[164:167],0
v_and_b32 v111,0xffff0000,v81
v_lshlrev_b32 v112,16,v82
M 120,v[24:27],180
v_and_b32 v113,0xffff0000,v82
v_lshlrev_b32 v114,16,v83
v_mfma_f32_16x16x32_bf16 v[124:127],v[26:29],v[168:171],0
v_add_f32_dpp v44,v36,v36 row_ror:8 row_mask:0xf bank_mask:0xf
v_and_b32 v115,0xffff0000,v83
M 124,v[26:29],184
ds_write_b32 v2,v44 offset:0
v_mfma_f32_16x16x32_bf16 v[128:131],v[24:27],v[168:171],0
ds_write_b32 v0,v45 offset:0
M 128,v[24:27],184
v_mfma_f32_16x16x32_bf16 v[132:135],v[26:29],v[172:175],0
M 132,v[26:29],188
L 0
v_mfma_f32_16x16x32_bf16 v[136:139],v[24:27],v[172:175],0
s_barrier
R 52,3,0
R 56,3,16
M 136,v[24:27],188
R 60,3,32
v_mfma_f32_16x16x32_bf16 v[140:143],v[26:29],v[176:179],0
M 140,v[26:29],192
v_mfma_f32_16x16x32_bf16 v[144:147],v[24:27],v[176:179],0
M 144,v[24:27],192
L 0
v_pk_add_f32 v[52:53],v[52:53],v[54:55]
v_pk_add_f32 v[56:57],v[56:57],v[58:59]
v_pk_add_f32 v[60:61],v[60:61],v[62:63]
v_add_f32 v52,v52,v53
v_pk_add_f32 v[56:57],v[56:57],v[60:61]
v_mul_f32 v52,s68,v52
v_add_f32 v53,v56,v57
v_mul_f32 v53,s68,v53
v_fma_f32 v53,-v52,v52,v53
v_max_f32 v53,0,v53
s_waitcnt vmcnt(5)
buffer_store_dword v17,v5,s[36:39],s63 offen offset:0 sc0 sc1
s_mov_b32 s63,0x7fff0000
s_waitcnt vmcnt(5)
v_cmp_eq_u32 vcc,s53,v18
s_nop 0
s_cmp_eq_u64 vcc,-1
s_cselect_b32 s78,s45,256
s_cmp_lt_u32 s78,256
s_cbranch_scc1 Lr_tb0_%=
buffer_load_dword v18,v6,s[36:39],s61 offen offset:0 sc1
v_add_f32 v53,s69,v53
v_rsq_f32 v48,v53
s_nop 0
v_mul_f32_e64 v50,-v52,v48
v_mov_b32 v49,v48
v_mov_b32 v51,v50
v_pk_fma_f32 v[52:53],v[116:117],v[48:49],v[50:51]
v_pk_fma_f32 v[54:55],v[118:119],v[48:49],v[50:51]
v_pk_fma_f32 v[56:57],v[120:121],v[48:49],v[50:51]
v_pk_fma_f32 v[58:59],v[122:123],v[48:49],v[50:51]
v_pk_fma_f32 v[116:117],v[52:53],v[84:85],v[116:117]
v_pk_fma_f32 v[118:119],v[54:55],v[86:87],v[118:119]
v_pk_fma_f32 v[120:121],v[56:57],v[88:89],v[120:121]
v_pk_fma_f32 v[122:123],v[58:59],v[90:91],v[122:123]
v_pk_fma_f32 v[52:53],v[124:125],v[48:49],v[50:51]
v_pk_fma_f32 v[54:55],v[126:127],v[48:49],v[50:51]
v_pk_fma_f32 v[56:57],v[128:129],v[48:49],v[50:51]
v_pk_fma_f32 v[58:59],v[130:131],v[48:49],v[50:51]
v_pk_fma_f32 v[124:125],v[52:53],v[92:93],v[124:125]
v_pk_fma_f32 v[126:127],v[54:55],v[94:95],v[126:127]
v_pk_fma_f32 v[128:129],v[56:57],v[96:97],v[128:129]
v_pk_fma_f32 v[130:131],v[58:59],v[98:99],v[130:131]
v_pk_fma_f32 v[52:53],v[132:133],v[48:49],v[50:51]
v_pk_fma_f32 v[54:55],v[134:135],v[48:49],v[50:51]
v_pk_fma_f32 v[56:57],v[136:137],v[48:49],v[50:51]
v_pk_fma_f32 v[58:59],v[138:139],v[48:49],v[50:51]
v_pk_fma_f32 v[132:133],v[52:53],v[100:101],v[132:133]
v_pk_fma_f32 v[134:135],v[54:55],v[102:103],v[134:135]
v_pk_fma_f32 v[136:137],v[56:57],v[104:105],v[136:137]
v_pk_fma_f32 v[138:139],v[58:59],v[106:107],v[138:139]
v_pk_fma_f32 v[52:53],v[140:141],v[48:49],v[50:51]
v_pk_fma_f32 v[54:55],v[142:143],v[48:49],v[50:51]
v_pk_fma_f32 v[56:57],v[144:145],v[48:49],v[50:51]
v_pk_fma_f32 v[58:59],v[146:147],v[48:49],v[50:51]
v_pk_fma_f32 v[140:141],v[52:53],v[108:109],v[140:141]
v_pk_fma_f32 v[142:143],v[54:55],v[110:111],v[142:143]
v_pk_fma_f32 v[144:145],v[56:57],v[112:113],v[144:145]
v_pk_fma_f32 v[146:147],v[58:59],v[114:115],v[146:147]
v_cvt_pk_bf16_f32 v148,v116,v117
v_cvt_pk_bf16_f32 v149,v118,v119
v_cvt_pk_bf16_f32 v150,v120,v121
v_cvt_pk_bf16_f32 v151,v122,v123
v_cvt_pk_bf16_f32 v152,v124,v125
v_cvt_pk_bf16_f32 v153,v126,v127
v_mfma_f32_16x16x32_bf16 v[36:39],v[32:35],v[148:151],0
v_cvt_pk_bf16_f32 v154,v128,v129
v_cvt_pk_bf16_f32 v155,v130,v131
v_mfma_f32_16x16x32_bf16 v[40:43],v[148:151],v[148:151],0
v_cvt_pk_bf16_f32 v156,v132,v133
v_cvt_pk_bf16_f32 v157,v134,v135
M 36,v[32:35],152
v_cvt_pk_bf16_f32 v158,v136,v137
v_cvt_pk_bf16_f32 v159,v138,v139
M 40,v[152:155],152
v_cvt_pk_bf16_f32 v160,v140,v141
v_cvt_pk_bf16_f32 v161,v142,v143
M 36,v[32:35],156
v_cvt_pk_bf16_f32 v162,v144,v145
v_cvt_pk_bf16_f32 v163,v146,v147
M 40,v[156:159],156
buffer_store_dwordx4 v[148:151],v1,s[36:39],s57 offen offset:0 sc0 sc1
M 36,v[32:35],160
buffer_store_dwordx4 v[152:155],v1,s[36:39],s57 offen offset:2048 sc0 sc1
M 40,v[160:163],160
buffer_store_dwordx4 v[156:159],v1,s[36:39],s58 offen offset:0 sc0 sc1
s_nop 4
v_add_f32_dpp v44,v36,v36 row_ror:8 row_mask:0xf bank_mask:0xf
s_nop 0
v_cndmask_b32 v45,v40,v41,s[70:71]
v_cndmask_b32 v46,v42,v43,s[70:71]
ds_write_b32 v2,v44 offset:1024
v_cndmask_b32 v45,v45,v46,s[72:73]
ds_write_b32 v0,v45 offset:1024
buffer_store_dwordx4 v[160:163],v1,s[36:39],s58 offen offset:2048 sc0 sc1
L 0
s_barrier
R 52,3,1024
R 56,3,1040
R 60,3,1056
L 0
v_pk_add_f32 v[52:53],v[52:53],v[54:55]
v_pk_add_f32 v[56:57],v[56:57],v[58:59]
v_pk_add_f32 v[60:61],v[60:61],v[62:63]
v_add_f32 v52,v52,v53
v_pk_add_f32 v[56:57],v[56:57],v[60:61]
v_mul_f32 v52,s68,v52
v_add_f32 v53,v56,v57
v_mul_f32 v53,s68,v53
v_fma_f32 v53,-v52,v52,v53
v_max_f32 v53,0,v53
v_add_f32 v54,s69,v53
v_fma_f32 v53,s69,v54,v53
v_rsq_f32 v55,v54
v_rsq_f32 v53,v53
v_mul_f32_e64 v54,-v52,v55
v_mul_f32 v53,s76,v53
buffer_store_dwordx4 v[52:55],v4,s[36:39],s59 offen offset:0 sc0 sc1
s_mov_b32 s63,s60
s_waitcnt vmcnt(5)
v_cmp_eq_u32 vcc,s53,v18
s_nop 0
s_cmp_eq_u64 vcc,-1
s_cselect_b32 s78,s45,256
s_cmp_lt_u32 s78,256
s_cbranch_scc1 Lr_tc0_%=
s_waitcnt vmcnt(0)
buffer_store_dword v17,v5,s[36:39],s63 offen offset:0 sc0 sc1
s_mov_b32 s63,0x7fff0000
s_cmp_ge_u32 s45,256
s_cbranch_scc1 Lr_done_%=
Lr_bp0_%=:
buffer_load_dword v18,v6,s[36:39],s61 offen offset:0 sc1
s_add_u32 s82,s82,1
s_waitcnt vmcnt(0)
v_cmp_eq_u32 vcc,s53,v18
s_nop 0
s_cmp_eq_u64 vcc,-1
s_cbranch_scc1 Lr_bq0_%=
s_sleep 2
s_cmp_lt_u32 s82,20000
s_cbranch_scc1 Lr_bp0_%=
Lr_bq0_%=:
s_add_u32 s78,s55,71303168
buffer_load_dwordx4 v[196:199],v1,s[36:39],s78 offen offset:0 sc1
buffer_load_dwordx4 v[200:203],v1,s[36:39],s78 offen offset:2048 sc1
s_add_u32 s78,s55,71307264
buffer_load_dwordx4 v[204:207],v1,s[36:39],s78 offen offset:0 sc1
buffer_load_dwordx4 v[208:211],v1,s[36:39],s78 offen offset:2048 sc1
s_add_u32 s78,s55,89128960
buffer_load_dwordx4 v[212:215],v1,s[36:39],s78 offen offset:0 sc1
buffer_load_dwordx4 v[216:219],v1,s[36:39],s78 offen offset:2048 sc1
s_add_u32 s78,s55,89133056
buffer_load_dwordx4 v[220:223],v1,s[36:39],s78 offen offset:0 sc1
buffer_load_dwordx4 v[224:227],v1,s[36:39],s78 offen offset:2048 sc1
buffer_load_dword v18,v6,s[36:39],s62 offen offset:0 sc1
s_lshr_b32 s78,s45,1
s_and_b32 s79,s45,1
s_lshl_b32 s79,s79,7
s_lshl_b32 s57,s78,15
s_add_u32 s57,s57,s79
s_add_u32 s57,s57,12582912
s_add_u32 s58,s57,4096
s_lshl_b32 s59,s78,9
s_add_u32 s59,s59,s79
s_add_u32 s59,s59,107020288
s_lshl_b32 s60,s45,9
s_add_u32 s60,s60,s66
s_add_u32 s60,s60,131072
s_add_u32 s45,s45,s46
s_lshr_b32 s78,s45,1
s_and_b32 s79,s45,1
s_lshl_b32 s79,s79,7
s_lshl_b32 s55,s78,15
s_add_u32 s55,s55,s79
s_mul_i32 s56,s45,16384
s_add_u32 s56,s56,98566144
s_cmp_lt_u32 s45,256
s_cselect_b32 s56,s56,0x7fff0000
s_lshl_b32 s61,s78,11
s_add_u32 s61,s61,110587904
s_add_u32 s78,s45,s46
s_lshr_b32 s78,s78,1
s_lshl_b32 s62,s78,11
s_add_u32 s62,s62,110587904
s_waitcnt vmcnt(0)
s_branch Lr_it1_%=
Lr_ta0_%=:
v_mfma_f32_16x16x32_bf16 v[148:151],v[164:167],v[180:183],0
s_add_u32 s78,s55,71303168
s_add_u32 m0,s65,16384
M 148,v[168:171],184
buffer_load_dwordx4 v[196:199],v1,s[36:39],s78 offen offset:0 sc1
M 148,v[172:175],188
buffer_load_dwordx4 v[200:203],v1,s[36:39],s78 offen offset:2048 sc1
M 148,v[176:179],192
L 4
D 9,56,0
v_mfma_f32_16x16x32_bf16 v[40:43],v[164:167],v[164:167],0
D 9,56,1024
M 40,v[168:171],168
L 0
D 9,56,2048
M 40,v[172:175],172
D 9,56,3072
M 40,v[176:179],176
s_add_u32 s78,s55,71307264
v_lshlrev_b32 v84,16,v68
v_and_b32 v85,0xffff0000,v68
M 40,v[180:183],180
buffer_load_dwordx4 v[204:207],v1,s[36:39],s78 offen offset:0 sc1
M 40,v[184:187],184
buffer_load_dwordx4 v[208:211],v1,s[36:39],s78 offen offset:2048 sc1
M 40,v[188:191],188
s_add_u32 s78,s55,89128960
v_lshlrev_b32 v86,16,v69
v_and_b32 v87,0xffff0000,v69
M 40,v[192:195],192
buffer_load_dwordx4 v[212:215],v1,s[36:39],s78 offen offset:0 sc1
v_mfma_f32_16x16x32_bf16 v[36:39],v[32:35],v[164:167],0
buffer_load_dwordx4 v[216:219],v1,s[36:39],s78 offen offset:2048 sc1
M 36,v[32:35],168
s_add_u32 s78,s55,89133056
v_lshlrev_b32 v88,16,v70
v_and_b32 v89,0xffff0000,v70
M 36,v[32:35],172
buffer_load_dwordx4 v[220:223],v1,s[36:39],s78 offen offset:0 sc1
M 36,v[32:35],176
buffer_load_dwordx4 v[224:227],v1,s[36:39],s78 offen offset:2048 sc1
M 36,v[32:35],180
v_fma_f32 v40,2.0,v148,v40
v_fma_f32 v41,2.0,v149,v41
M 36,v[32:35],184
v_fma_f32 v42,2.0,v150,v42
v_fma_f32 v43,2.0,v151,v43
M 36,v[32:35],188
v_cndmask_b32 v45,v40,v41,s[70:71]
v_cndmask_b32 v46,v42,v43,s[70:71]
M 36,v[32:35],192
v_cndmask_b32 v45,v45,v46,s[72:73]
v_lshlrev_b32 v90,16,v71
v_mfma_f32_16x16x32_bf16 v[116:119],v[26:29],v[164:167],0
v_and_b32 v91,0xffff0000,v71
v_lshlrev_b32 v92,16,v72
M 116,v[26:29],180
v_and_b32 v93,0xffff0000,v72
v_lshlrev_b32 v94,16,v73
v_mfma_f32_16x16x32_bf16 v[120:123],v[24:27],v[164:167],0
v_and_b32 v95,0xffff0000,v73
v_lshlrev_b32 v96,16,v74
M 120,v[24:27],180
v_and_b32 v97,0xffff0000,v74
v_lshlrev_b32 v98,16,v75
v_mfma_f32_16x16x32_bf16 v[124:127],v[26:29],v[168:171],0
v_add_f32_dpp v44,v36,v36 row_ror:8 row_mask:0xf bank_mask:0xf
v_and_b32 v99,0xffff0000,v75
M 124,v[26:29],184
ds_write_b32 v2,v44 offset:0
v_mfma_f32_16x16x32_bf16 v[128:131],v[24:27],v[168:171],0
ds_write_b32 v0,v45 offset:0
M 128,v[24:27],184
v_lshlrev_b32 v100,16,v76
v_and_b32 v101,0xffff0000,v76
v_mfma_f32_16x16x32_bf16 v[132:135],v[26:29],v[172:175],0
v_lshlrev_b32 v102,16,v77
v_and_b32 v103,0xffff0000,v77
M 132,v[26:29],188
L 0
v_lshlrev_b32 v104,16,v78
v_and_b32 v105,0xffff0000,v78
v_mfma_f32_16x16x32_bf16 v[136:139],v[24:27],v[172:175],0
s_barrier
R 52,3,0
R 56,3,16
M 136,v[24:27],188
R 60,3,32
v_lshlrev_b32 v106,16,v79
v_mfma_f32_16x16x32_bf16 v[140:143],v[26:29],v[176:179],0
v_and_b32 v107,0xffff0000,v79
v_lshlrev_b32 v108,16,v80
M 140,v[26:29],192
v_and_b32 v109,0xffff0000,v80
)"
R"(v_lshlrev_b32 v110,16,v81
v_mfma_f32_16x16x32_bf16 v[144:147],v[24:27],v[176:179],0
v_and_b32 v111,0xffff0000,v81
v_lshlrev_b32 v112,16,v82
M 144,v[24:27],192
v_and_b32 v113,0xffff0000,v82
v_lshlrev_b32 v114,16,v83
v_and_b32 v115,0xffff0000,v83
L 0
v_pk_add_f32 v[52:53],v[52:53],v[54:55]
v_pk_add_f32 v[56:57],v[56:57],v[58:59]
v_pk_add_f32 v[60:61],v[60:61],v[62:63]
v_add_f32 v52,v52,v53
v_pk_add_f32 v[56:57],v[56:57],v[60:61]
v_mul_f32 v52,s68,v52
v_add_f32 v53,v56,v57
v_mul_f32 v53,s68,v53
v_fma_f32 v53,-v52,v52,v53
v_max_f32 v53,0,v53
buffer_load_dword v18,v6,s[36:39],s62 offen offset:0 sc1
v_add_f32 v53,s69,v53
v_rsq_f32 v48,v53
s_nop 0
v_mul_f32_e64 v50,-v52,v48
v_mov_b32 v49,v48
v_mov_b32 v51,v50
v_pk_fma_f32 v[52:53],v[116:117],v[48:49],v[50:51]
v_pk_fma_f32 v[54:55],v[118:119],v[48:49],v[50:51]
v_pk_fma_f32 v[56:57],v[120:121],v[48:49],v[50:51]
v_pk_fma_f32 v[58:59],v[122:123],v[48:49],v[50:51]
v_pk_fma_f32 v[116:117],v[52:53],v[84:85],v[116:117]
v_pk_fma_f32 v[118:119],v[54:55],v[86:87],v[118:119]
v_pk_fma_f32 v[120:121],v[56:57],v[88:89],v[120:121]
v_pk_fma_f32 v[122:123],v[58:59],v[90:91],v[122:123]
v_pk_fma_f32 v[52:53],v[124:125],v[48:49],v[50:51]
v_pk_fma_f32 v[54:55],v[126:127],v[48:49],v[50:51]
v_pk_fma_f32 v[56:57],v[128:129],v[48:49],v[50:51]
v_pk_fma_f32 v[58:59],v[130:131],v[48:49],v[50:51]
v_pk_fma_f32 v[124:125],v[52:53],v[92:93],v[124:125]
v_pk_fma_f32 v[126:127],v[54:55],v[94:95],v[126:127]
v_pk_fma_f32 v[128:129],v[56:57],v[96:97],v[128:129]
v_pk_fma_f32 v[130:131],v[58:59],v[98:99],v[130:131]
v_pk_fma_f32 v[52:53],v[132:133],v[48:49],v[50:51]
v_pk_fma_f32 v[54:55],v[134:135],v[48:49],v[50:51]
v_pk_fma_f32 v[56:57],v[136:137],v[48:49],v[50:51]
v_pk_fma_f32 v[58:59],v[138:139],v[48:49],v[50:51]
v_pk_fma_f32 v[132:133],v[52:53],v[100:101],v[132:133]
v_pk_fma_f32 v[134:135],v[54:55],v[102:103],v[134:135]
v_pk_fma_f32 v[136:137],v[56:57],v[104:105],v[136:137]
v_pk_fma_f32 v[138:139],v[58:59],v[106:107],v[138:139]
v_pk_fma_f32 v[52:53],v[140:141],v[48:49],v[50:51]
v_pk_fma_f32 v[54:55],v[142:143],v[48:49],v[50:51]
v_pk_fma_f32 v[56:57],v[144:145],v[48:49],v[50:51]
v_pk_fma_f32 v[58:59],v[146:147],v[48:49],v[50:51]
v_pk_fma_f32 v[140:141],v[52:53],v[108:109],v[140:141]
v_pk_fma_f32 v[142:143],v[54:55],v[110:111],v[142:143]
v_pk_fma_f32 v[144:145],v[56:57],v[112:113],v[144:145]
v_pk_fma_f32 v[146:147],v[58:59],v[114:115],v[146:147]
v_cvt_pk_bf16_f32 v148,v116,v117
v_cvt_pk_bf16_f32 v149,v118,v119
v_cvt_pk_bf16_f32 v150,v120,v121
v_cvt_pk_bf16_f32 v151,v122,v123
v_cvt_pk_bf16_f32 v152,v124,v125
v_cvt_pk_bf16_f32 v153,v126,v127
s_waitcnt vmcnt(13)
buffer_store_dword v17,v5,s[36:39],s63 offen offset:0 sc0 sc1
s_mov_b32 s63,0x7fff0000
v_mfma_f32_16x16x32_bf16 v[36:39],v[32:35],v[148:151],0
v_cvt_pk_bf16_f32 v154,v128,v129
v_cvt_pk_bf16_f32 v155,v130,v131
v_mfma_f32_16x16x32_bf16 v[40:43],v[148:151],v[148:151],0
v_cvt_pk_bf16_f32 v156,v132,v133
v_cvt_pk_bf16_f32 v157,v134,v135
M 36,v[32:35],152
v_cvt_pk_bf16_f32 v158,v136,v137
v_cvt_pk_bf16_f32 v159,v138,v139
M 40,v[152:155],152
v_cvt_pk_bf16_f32 v160,v140,v141
v_cvt_pk_bf16_f32 v161,v142,v143
M 36,v[32:35],156
v_cvt_pk_bf16_f32 v162,v144,v145
v_cvt_pk_bf16_f32 v163,v146,v147
M 40,v[156:159],156
buffer_store_dwordx4 v[148:151],v1,s[36:39],s57 offen offset:0 sc0 sc1
M 36,v[32:35],160
buffer_store_dwordx4 v[152:155],v1,s[36:39],s57 offen offset:2048 sc0 sc1
M 40,v[160:163],160
buffer_store_dwordx4 v[156:159],v1,s[36:39],s58 offen offset:0 sc0 sc1
s_nop 4
v_add_f32_dpp v44,v36,v36 row_ror:8 row_mask:0xf bank_mask:0xf
s_nop 0
v_cndmask_b32 v45,v40,v41,s[70:71]
v_cndmask_b32 v46,v42,v43,s[70:71]
ds_write_b32 v2,v44 offset:1024
v_cndmask_b32 v45,v45,v46,s[72:73]
ds_write_b32 v0,v45 offset:1024
buffer_store_dwordx4 v[160:163],v1,s[36:39],s58 offen offset:2048 sc0 sc1
L 0
s_barrier
R 52,3,1024
R 56,3,1040
R 60,3,1056
L 0
v_pk_add_f32 v[52:53],v[52:53],v[54:55]
v_pk_add_f32 v[56:57],v[56:57],v[58:59]
v_pk_add_f32 v[60:61],v[60:61],v[62:63]
v_add_f32 v52,v52,v53
v_pk_add_f32 v[56:57],v[56:57],v[60:61]
v_mul_f32 v52,s68,v52
v_add_f32 v53,v56,v57
v_mul_f32 v53,s68,v53
v_fma_f32 v53,-v52,v52,v53
v_max_f32 v53,0,v53
v_add_f32 v54,s69,v53
v_fma_f32 v53,s69,v54,v53
v_rsq_f32 v55,v54
v_rsq_f32 v53,v53
v_mul_f32_e64 v54,-v52,v55
v_mul_f32 v53,s76,v53
buffer_store_dwordx4 v[52:55],v4,s[36:39],s59 offen offset:0 sc0 sc1
s_mov_b32 s63,s60
s_lshr_b32 s78,s45,1
s_and_b32 s79,s45,1
s_lshl_b32 s79,s79,7
s_lshl_b32 s57,s78,15
s_add_u32 s57,s57,s79
s_add_u32 s57,s57,12582912
s_add_u32 s58,s57,4096
s_lshl_b32 s59,s78,9
s_add_u32 s59,s59,s79
s_add_u32 s59,s59,107020288
s_lshl_b32 s60,s45,9
s_add_u32 s60,s60,s66
s_add_u32 s60,s60,131072
s_add_u32 s45,s45,s46
s_lshr_b32 s78,s45,1
s_and_b32 s79,s45,1
s_lshl_b32 s79,s79,7
s_lshl_b32 s55,s78,15
s_add_u32 s55,s55,s79
s_mul_i32 s56,s45,16384
s_add_u32 s56,s56,98566144
s_cmp_lt_u32 s45,256
s_cselect_b32 s56,s56,0x7fff0000
s_lshl_b32 s61,s78,11
s_add_u32 s61,s61,110587904
s_add_u32 s78,s45,s46
s_lshr_b32 s78,s78,1
s_lshl_b32 s62,s78,11
s_add_u32 s62,s62,110587904
s_waitcnt vmcnt(6)
s_branch Lr_it1_%=
Lr_tb0_%=:
v_add_f32 v53,s69,v53
v_rsq_f32 v48,v53
s_nop 0
v_mul_f32_e64 v50,-v52,v48
v_mov_b32 v49,v48
v_mov_b32 v51,v50
v_pk_fma_f32 v[52:53],v[116:117],v[48:49],v[50:51]
v_pk_fma_f32 v[54:55],v[118:119],v[48:49],v[50:51]
v_pk_fma_f32 v[56:57],v[120:121],v[48:49],v[50:51]
v_pk_fma_f32 v[58:59],v[122:123],v[48:49],v[50:51]
s_add_u32 s78,s55,71303168
buffer_load_dwordx4 v[196:199],v1,s[36:39],s78 offen offset:0 sc1
buffer_load_dwordx4 v[200:203],v1,s[36:39],s78 offen offset:2048 sc1
s_add_u32 s78,s55,71307264
buffer_load_dwordx4 v[204:207],v1,s[36:39],s78 offen offset:0 sc1
buffer_load_dwordx4 v[208:211],v1,s[36:39],s78 offen offset:2048 sc1
v_pk_fma_f32 v[116:117],v[52:53],v[84:85],v[116:117]
v_pk_fma_f32 v[118:119],v[54:55],v[86:87],v[118:119]
v_pk_fma_f32 v[120:121],v[56:57],v[88:89],v[120:121]
v_pk_fma_f32 v[122:123],v[58:59],v[90:91],v[122:123]
v_pk_fma_f32 v[52:53],v[124:125],v[48:49],v[50:51]
v_pk_fma_f32 v[54:55],v[126:127],v[48:49],v[50:51]
v_pk_fma_f32 v[56:57],v[128:129],v[48:49],v[50:51]
v_pk_fma_f32 v[58:59],v[130:131],v[48:49],v[50:51]
s_add_u32 s78,s55,89128960
buffer_load_dwordx4 v[212:215],v1,s[36:39],s78 offen offset:0 sc1
buffer_load_dwordx4 v[216:219],v1,s[36:39],s78 offen offset:2048 sc1
s_add_u32 s78,s55,89133056
buffer_load_dwordx4 v[220:223],v1,s[36:39],s78 offen offset:0 sc1
buffer_load_dwordx4 v[224:227],v1,s[36:39],s78 offen offset:2048 sc1
v_pk_fma_f32 v[124:125],v[52:53],v[92:93],v[124:125]
v_pk_fma_f32 v[126:127],v[54:55],v[94:95],v[126:127]
v_pk_fma_f32 v[128:129],v[56:57],v[96:97],v[128:129]
v_pk_fma_f32 v[130:131],v[58:59],v[98:99],v[130:131]
v_pk_fma_f32 v[52:53],v[132:133],v[48:49],v[50:51]
v_pk_fma_f32 v[54:55],v[134:135],v[48:49],v[50:51]
v_pk_fma_f32 v[56:57],v[136:137],v[48:49],v[50:51]
v_pk_fma_f32 v[58:59],v[138:139],v[48:49],v[50:51]
v_pk_fma_f32 v[132:133],v[52:53],v[100:101],v[132:133]
v_pk_fma_f32 v[134:135],v[54:55],v[102:103],v[134:135]
v_pk_fma_f32 v[136:137],v[56:57],v[104:105],v[136:137]
v_pk_fma_f32 v[138:139],v[58:59],v[106:107],v[138:139]
v_pk_fma_f32 v[52:53],v[140:141],v[48:49],v[50:51]
v_pk_fma_f32 v[54:55],v[142:143],v[48:49],v[50:51]
v_pk_fma_f32 v[56:57],v[144:145],v[48:49],v[50:51]
v_pk_fma_f32 v[58:59],v[146:147],v[48:49],v[50:51]
v_pk_fma_f32 v[140:141],v[52:53],v[108:109],v[140:141]
v_pk_fma_f32 v[142:143],v[54:55],v[110:111],v[142:143]
v_pk_fma_f32 v[144:145],v[56:57],v[112:113],v[144:145]
v_pk_fma_f32 v[146:147],v[58:59],v[114:115],v[146:147]
buffer_load_dword v18,v6,s[36:39],s62 offen offset:0 sc1
v_cvt_pk_bf16_f32 v148,v116,v117
v_cvt_pk_bf16_f32 v149,v118,v119
v_cvt_pk_bf16_f32 v150,v120,v121
v_cvt_pk_bf16_f32 v151,v122,v123
v_cvt_pk_bf16_f32 v152,v124,v125
v_cvt_pk_bf16_f32 v153,v126,v127
v_mfma_f32_16x16x32_bf16 v[36:39],v[32:35],v[148:151],0
v_cvt_pk_bf16_f32 v154,v128,v129
v_cvt_pk_bf16_f32 v155,v130,v131
v_mfma_f32_16x16x32_bf16 v[40:43],v[148:151],v[148:151],0
v_cvt_pk_bf16_f32 v156,v132,v133
v_cvt_pk_bf16_f32 v157,v134,v135
M 36,v[32:35],152
v_cvt_pk_bf16_f32 v158,v136,v137
v_cvt_pk_bf16_f32 v159,v138,v139
M 40,v[152:155],152
v_cvt_pk_bf16_f32 v160,v140,v141
v_cvt_pk_bf16_f32 v161,v142,v143
M 36,v[32:35],156
v_cvt_pk_bf16_f32 v162,v144,v145
v_cvt_pk_bf16_f32 v163,v146,v147
M 40,v[156:159],156
buffer_store_dwordx4 v[148:151],v1,s[36:39],s57 offen offset:0 sc0 sc1
M 36,v[32:35],160
buffer_store_dwordx4 v[152:155],v1,s[36:39],s57 offen offset:2048 sc0 sc1
M 40,v[160:163],160
buffer_store_dwordx4 v[156:159],v1,s[36:39],s58 offen offset:0 sc0 sc1
s_nop 4
v_add_f32_dpp v44,v36,v36 row_ror:8 row_mask:0xf bank_mask:0xf
s_nop 0
v_cndmask_b32 v45,v40,v41,s[70:71]
v_cndmask_b32 v46,v42,v43,s[70:71]
ds_write_b32 v2,v44 offset:1024
v_cndmask_b32 v45,v45,v46,s[72:73]
ds_write_b32 v0,v45 offset:1024
buffer_store_dwordx4 v[160:163],v1,s[36:39],s58 offen offset:2048 sc0 sc1
L 0
s_barrier
R 52,3,1024
R 56,3,1040
R 60,3,1056
L 0
v_pk_add_f32 v[52:53],v[52:53],v[54:55]
v_pk_add_f32 v[56:57],v[56:57],v[58:59]
v_pk_add_f32 v[60:61],v[60:61],v[62:63]
v_add_f32 v52,v52,v53
v_pk_add_f32 v[56:57],v[56:57],v[60:61]
v_mul_f32 v52,s68,v52
v_add_f32 v53,v56,v57
v_mul_f32 v53,s68,v53
v_fma_f32 v53,-v52,v52,v53
v_max_f32 v53,0,v53
v_add_f32 v54,s69,v53
v_fma_f32 v53,s69,v54,v53
v_rsq_f32 v55,v54
v_rsq_f32 v53,v53
v_mul_f32_e64 v54,-v52,v55
v_mul_f32 v53,s76,v53
buffer_store_dwordx4 v[52:55],v4,s[36:39],s59 offen offset:0 sc0 sc1
s_mov_b32 s63,s60
s_lshr_b32 s78,s45,1
s_and_b32 s79,s45,1
s_lshl_b32 s79,s79,7
s_lshl_b32 s57,s78,15
s_add_u32 s57,s57,s79
s_add_u32 s57,s57,12582912
s_add_u32 s58,s57,4096
s_lshl_b32 s59,s78,9
s_add_u32 s59,s59,s79
s_add_u32 s59,s59,107020288
s_lshl_b32 s60,s45,9
s_add_u32 s60,s60,s66
s_add_u32 s60,s60,131072
s_add_u32 s45,s45,s46
s_lshr_b32 s78,s45,1
s_and_b32 s79,s45,1
s_lshl_b32 s79,s79,7
s_lshl_b32 s55,s78,15
s_add_u32 s55,s55,s79
s_mul_i32 s56,s45,16384
s_add_u32 s56,s56,98566144
s_cmp_lt_u32 s45,256
s_cselect_b32 s56,s56,0x7fff0000
s_lshl_b32 s61,s78,11
s_add_u32 s61,s61,110587904
s_add_u32 s78,s45,s46
s_lshr_b32 s78,s78,1
s_lshl_b32 s62,s78,11
s_add_u32 s62,s62,110587904
s_waitcnt vmcnt(5)
s_branch Lr_it1_%=
Lr_tc0_%=:
s_add_u32 s78,s55,71303168
buffer_load_dwordx4 v[196:199],v1,s[36:39],s78 offen offset:0 sc1
buffer_load_dwordx4 v[200:203],v1,s[36:39],s78 offen offset:2048 sc1
s_add_u32 s78,s55,71307264
buffer_load_dwordx4 v[204:207],v1,s[36:39],s78 offen offset:0 sc1
buffer_load_dwordx4 v[208:211],v1,s[36:39],s78 offen offset:2048 sc1
s_add_u32 s78,s55,89128960
buffer_load_dwordx4 v[212:215],v1,s[36:39],s78 offen offset:0 sc1
buffer_load_dwordx4 v[216:219],v1,s[36:39],s78 offen offset:2048 sc1
s_add_u32 s78,s55,89133056
buffer_load_dwordx4 v[220:223],v1,s[36:39],s78 offen offset:0 sc1
buffer_load_dwordx4 v[224:227],v1,s[36:39],s78 offen offset:2048 sc1
buffer_load_dword v18,v6,s[36:39],s62 offen offset:0 sc1
s_waitcnt vmcnt(9)
buffer_store_dword v17,v5,s[36:39],s63 offen offset:0 sc0 sc1
s_mov_b32 s63,0x7fff0000
s_lshr_b32 s78,s45,1
s_and_b32 s79,s45,1
s_lshl_b32 s79,s79,7
s_lshl_b32 s57,s78,15
s_add_u32 s57,s57,s79
s_add_u32 s57,s57,12582912
s_add_u32 s58,s57,4096
s_lshl_b32 s59,s78,9
s_add_u32 s59,s59,s79
s_add_u32 s59,s59,107020288
s_lshl_b32 s60,s45,9
s_add_u32 s60,s60,s66
s_add_u32 s60,s60,131072
s_add_u32 s45,s45,s46
s_lshr_b32 s78,s45,1
s_and_b32 s79,s45,1
s_lshl_b32 s79,s79,7
s_lshl_b32 s55,s78,15
s_add_u32 s55,s55,s79
s_mul_i32 s56,s45,16384
s_add_u32 s56,s56,98566144
s_cmp_lt_u32 s45,256
s_cselect_b32 s56,s56,0x7fff0000
s_lshl_b32 s61,s78,11
s_add_u32 s61,s61,110587904
s_add_u32 s78,s45,s46
s_lshr_b32 s78,s78,1
s_lshl_b32 s62,s78,11
s_add_u32 s62,s62,110587904
s_waitcnt vmcnt(1)
s_branch Lr_it1_%=
Lr_it1_%=:
T v[68:69],10,16384
T v[70:71],10,16448
T v[72:73],10,17408
T v[74:75],10,17472
T v[76:77],10,18432
T v[78:79],10,18496
T v[80:81],10,19456
T v[82:83],10,19520
s_nop 0
v_cmp_eq_u32 vcc,s53,v18
s_nop 0
s_cmp_eq_u64 vcc,-1
s_cselect_b32 s78,s45,256
s_cmp_lt_u32 s78,256
s_cbranch_scc1 Lr_ta1_%=
buffer_load_dword v18,v6,s[36:39],s61 offen offset:0 sc1
v_mfma_f32_16x16x32_bf16 v[148:151],v[196:199],v[212:215],0
s_add_u32 m0,s65,0
M 148,v[200:203],216
D 9,56,0
M 148,v[204:207],220
D 9,56,1024
M 148,v[208:211],224
L 4
D 9,56,2048
v_mfma_f32_16x16x32_bf16 v[40:43],v[196:199],v[196:199],0
D 9,56,3072
M 40,v[200:203],200
L 0
v_lshlrev_b32 v84,16,v68
v_and_b32 v85,0xffff0000,v68
M 40,v[204:207],204
v_lshlrev_b32 v86,16,v69
v_and_b32 v87,0xffff0000,v69
M 40,v[208:211],208
v_lshlrev_b32 v88,16,v70
v_and_b32 v89,0xffff0000,v70
M 40,v[212:215],212
v_lshlrev_b32 v90,16,v71
v_and_b32 v91,0xffff0000,v71
M 40,v[216:219],216
v_lshlrev_b32 v92,16,v72
v_and_b32 v93,0xffff0000,v72
M 40,v[220:223],220
v_lshlrev_b32 v94,16,v73
v_and_b32 v95,0xffff0000,v73
M 40,v[224:227],224
v_lshlrev_b32 v96,16,v74
v_and_b32 v97,0xffff0000,v74
v_mfma_f32_16x16x32_bf16 v[36:39],v[32:35],v[196:199],0
v_lshlrev_b32 v98,16,v75
v_and_b32 v99,0xffff0000,v75
M 36,v[32:35],200
v_lshlrev_b32 v100,16,v76
v_and_b32 v101,0xffff0000,v76
M 36,v[32:35],204
v_lshlrev_b32 v102,16,v77
v_and_b32 v103,0xffff0000,v77
M 36,v[32:35],208
v_lshlrev_b32 v104,16,v78
v_and_b32 v105,0xffff0000,v78
M 36,v[32:35],212
v_fma_f32 v40,2.0,v148,v40
v_fma_f32 v41,2.0,v149,v41
M 36,v[32:35],216
v_fma_f32 v42,2.0,v150,v42
v_fma_f32 v43,2.0,v151,v43
M 36,v[32:35],220
v_cndmask_b32 v45,v40,v41,s[70:71]
v_cndmask_b32 v46,v42,v43,s[70:71]
M 36,v[32:35],224
v_cndmask_b32 v45,v45,v46,s[72:73]
v_lshlrev_b32 v106,16,v79
v_mfma_f32_16x16x32_bf16 v[116:119],v[26:29],v[196:199],0
v_and_b32 v107,0xffff0000,v79
v_lshlrev_b32 v108,16,v80
M 116,v[26:29],212
v_and_b32 v109,0xffff0000,v80
v_lshlrev_b32 v110,16,v81
v_mfma_f32_16x16x32_bf16 v[120:123],v[24:27],v[196:199],0
v_and_b32 v111,0xffff0000,v81
v_lshlrev_b32 v112,16,v82
M 120,v[24:27],212
v_and_b32 v113,0xffff0000,v82
v_lshlrev_b32 v114,16,v83
v_mfma_f32_16x16x32_bf16 v[124:127],v[26:29],v[200:203],0
v_add_f32_dpp v44,v36,v36 row_ror:8 row_mask:0xf bank_mask:0xf
v_and_b32 v115,0xffff0000,v83
M 124,v[26:29],216
ds_write_b32 v2,v44 offset:0
v_mfma_f32_16x16x32_bf16 v[128:131],v[24:27],v[200:203],0
ds_write_b32 v0,v45 offset:0
M 128,v[24:27],216
v_mfma_f32_16x16x32_bf16 v[132:135],v[26:29],v[204:207],0
M 132,v[26:29],220
L 0
v_mfma_f32_16x16x32_bf16 v[136:139],v[24:27],v[204:207],0
s_barrier
R 52,3,0
R 56,3,16
M 136,v[24:27],220
R 60,3,32
v_mfma_f32_16x16x32_bf16 v[140:143],v[26:29],v[208:211],0
M 140,v[26:29],224
v_mfma_f32_16x16x32_bf16 v[144:147],v[24:27],v[208:211],0
M 144,v[24:27],224
L 0
v_pk_add_f32 v[52:53],v[52:53],v[54:55]
v_pk_add_f32 v[56:57],v[56:57],v[58:59]
v_pk_add_f32 v[60:61],v[60:61],v[62:63]
v_add_f32 v52,v52,v53
v_pk_add_f32 v[56:57],v[56:57],v[60:61]
v_mul_f32 v52,s68,v52
v_add_f32 v53,v56,v57
v_mul_f32 v53,s68,v53
v_fma_f32 v53,-v52,v52,v53
v_max_f32 v53,0,v53
s_waitcnt vmcnt(5)
buffer_store_dword v17,v5,s[36:39],s63 offen offset:0 sc0 sc1
s_mov_b32 s63,0x7fff0000
s_waitcnt vmcnt(5)
v_cmp_eq_u32 vcc,s53,v18
s_nop 0
s_cmp_eq_u64 vcc,-1
s_cselect_b32 s78,s45,256
s_cmp_lt_u32 s78,256
s_cbranch_scc1 Lr_tb1_%=
buffer_load_dword v18,v6,s[36:39],s61 offen offset:0 sc1
v_add_f32 v53,s69,v53
v_rsq_f32 v48,v53
s_nop 0
v_mul_f32_e64 v50,-v52,v48
v_mov_b32 v49,v48
v_mov_b32 v51,v50
v_pk_fma_f32 v[52:53],v[116:117],v[48:49],v[50:51]
v_pk_fma_f32 v[54:55],v[118:119],v[48:49],v[50:51]
v_pk_fma_f32 v[56:57],v[120:121],v[48:49],v[50:51]
v_pk_fma_f32 v[58:59],v[122:123],v[48:49],v[50:51]
v_pk_fma_f32 v[116:117],v[52:53],v[84:85],v[116:117]
v_pk_fma_f32 v[118:119],v[54:55],v[86:87],v[118:119]
v_pk_fma_f32 v[120:121],v[56:57],v[88:89],v[120:121]
v_pk_fma_f32 v[122:123],v[58:59],v[90:91],v[122:123]
v_pk_fma_f32 v[52:53],v[124:125],v[48:49],v[50:51]
v_pk_fma_f32 v[54:55],v[126:127],v[48:49],v[50:51]
v_pk_fma_f32 v[56:57],v[128:129],v[48:49],v[50:51]
v_pk_fma_f32 v[58:59],v[130:131],v[48:49],v[50:51]
v_pk_fma_f32 v[124:125],v[52:53],v[92:93],v[124:125]
v_pk_fma_f32 v[126:127],v[54:55],v[94:95],v[126:127]
v_pk_fma_f32 v[128:129],v[56:57],v[96:97],v[128:129]
v_pk_fma_f32 v[130:131],v[58:59],v[98:99],v[130:131]
v_pk_fma_f32 v[52:53],v[132:133],v[48:49],v[50:51]
v_pk_fma_f32 v[54:55],v[134:135],v[48:49],v[50:51]
v_pk_fma_f32 v[56:57],v[136:137],v[48:49],v[50:51]
v_pk_fma_f32 v[58:59],v[138:139],v[48:49],v[50:51]
v_pk_fma_f32 v[132:133],v[52:53],v[100:101],v[132:133]
v_pk_fma_f32 v[134:135],v[54:55],v[102:103],v[134:135]
)"
R"(v_pk_fma_f32 v[136:137],v[56:57],v[104:105],v[136:137]
v_pk_fma_f32 v[138:139],v[58:59],v[106:107],v[138:139]
v_pk_fma_f32 v[52:53],v[140:141],v[48:49],v[50:51]
v_pk_fma_f32 v[54:55],v[142:143],v[48:49],v[50:51]
v_pk_fma_f32 v[56:57],v[144:145],v[48:49],v[50:51]
v_pk_fma_f32 v[58:59],v[146:147],v[48:49],v[50:51]
v_pk_fma_f32 v[140:141],v[52:53],v[108:109],v[140:141]
v_pk_fma_f32 v[142:143],v[54:55],v[110:111],v[142:143]
v_pk_fma_f32 v[144:145],v[56:57],v[112:113],v[144:145]
v_pk_fma_f32 v[146:147],v[58:59],v[114:115],v[146:147]
v_cvt_pk_bf16_f32 v148,v116,v117
v_cvt_pk_bf16_f32 v149,v118,v119
v_cvt_pk_bf16_f32 v150,v120,v121
v_cvt_pk_bf16_f32 v151,v122,v123
v_cvt_pk_bf16_f32 v152,v124,v125
v_cvt_pk_bf16_f32 v153,v126,v127
v_mfma_f32_16x16x32_bf16 v[36:39],v[32:35],v[148:151],0
v_cvt_pk_bf16_f32 v154,v128,v129
v_cvt_pk_bf16_f32 v155,v130,v131
v_mfma_f32_16x16x32_bf16 v[40:43],v[148:151],v[148:151],0
v_cvt_pk_bf16_f32 v156,v132,v133
v_cvt_pk_bf16_f32 v157,v134,v135
M 36,v[32:35],152
v_cvt_pk_bf16_f32 v158,v136,v137
v_cvt_pk_bf16_f32 v159,v138,v139
M 40,v[152:155],152
v_cvt_pk_bf16_f32 v160,v140,v141
v_cvt_pk_bf16_f32 v161,v142,v143
M 36,v[32:35],156
v_cvt_pk_bf16_f32 v162,v144,v145
v_cvt_pk_bf16_f32 v163,v146,v147
M 40,v[156:159],156
buffer_store_dwordx4 v[148:151],v1,s[36:39],s57 offen offset:0 sc0 sc1
M 36,v[32:35],160
buffer_store_dwordx4 v[152:155],v1,s[36:39],s57 offen offset:2048 sc0 sc1
M 40,v[160:163],160
buffer_store_dwordx4 v[156:159],v1,s[36:39],s58 offen offset:0 sc0 sc1
s_nop 4
v_add_f32_dpp v44,v36,v36 row_ror:8 row_mask:0xf bank_mask:0xf
s_nop 0
v_cndmask_b32 v45,v40,v41,s[70:71]
v_cndmask_b32 v46,v42,v43,s[70:71]
ds_write_b32 v2,v44 offset:1024
v_cndmask_b32 v45,v45,v46,s[72:73]
ds_write_b32 v0,v45 offset:1024
buffer_store_dwordx4 v[160:163],v1,s[36:39],s58 offen offset:2048 sc0 sc1
L 0
s_barrier
R 52,3,1024
R 56,3,1040
R 60,3,1056
L 0
v_pk_add_f32 v[52:53],v[52:53],v[54:55]
v_pk_add_f32 v[56:57],v[56:57],v[58:59]
v_pk_add_f32 v[60:61],v[60:61],v[62:63]
v_add_f32 v52,v52,v53
v_pk_add_f32 v[56:57],v[56:57],v[60:61]
v_mul_f32 v52,s68,v52
v_add_f32 v53,v56,v57
v_mul_f32 v53,s68,v53
v_fma_f32 v53,-v52,v52,v53
v_max_f32 v53,0,v53
v_add_f32 v54,s69,v53
v_fma_f32 v53,s69,v54,v53
v_rsq_f32 v55,v54
v_rsq_f32 v53,v53
v_mul_f32_e64 v54,-v52,v55
v_mul_f32 v53,s76,v53
buffer_store_dwordx4 v[52:55],v4,s[36:39],s59 offen offset:0 sc0 sc1
s_mov_b32 s63,s60
s_waitcnt vmcnt(5)
v_cmp_eq_u32 vcc,s53,v18
s_nop 0
s_cmp_eq_u64 vcc,-1
s_cselect_b32 s78,s45,256
s_cmp_lt_u32 s78,256
s_cbranch_scc1 Lr_tc1_%=
s_waitcnt vmcnt(0)
buffer_store_dword v17,v5,s[36:39],s63 offen offset:0 sc0 sc1
s_mov_b32 s63,0x7fff0000
s_cmp_ge_u32 s45,256
s_cbranch_scc1 Lr_done_%=
Lr_bp1_%=:
buffer_load_dword v18,v6,s[36:39],s61 offen offset:0 sc1
s_add_u32 s82,s82,1
s_waitcnt vmcnt(0)
v_cmp_eq_u32 vcc,s53,v18
s_nop 0
s_cmp_eq_u64 vcc,-1
s_cbranch_scc1 Lr_bq1_%=
s_sleep 2
s_cmp_lt_u32 s82,20000
s_cbranch_scc1 Lr_bp1_%=
Lr_bq1_%=:
s_add_u32 s78,s55,71303168
buffer_load_dwordx4 v[164:167],v1,s[36:39],s78 offen offset:0 sc1
buffer_load_dwordx4 v[168:171],v1,s[36:39],s78 offen offset:2048 sc1
s_add_u32 s78,s55,71307264
buffer_load_dwordx4 v[172:175],v1,s[36:39],s78 offen offset:0 sc1
buffer_load_dwordx4 v[176:179],v1,s[36:39],s78 offen offset:2048 sc1
s_add_u32 s78,s55,89128960
buffer_load_dwordx4 v[180:183],v1,s[36:39],s78 offen offset:0 sc1
buffer_load_dwordx4 v[184:187],v1,s[36:39],s78 offen offset:2048 sc1
s_add_u32 s78,s55,89133056
buffer_load_dwordx4 v[188:191],v1,s[36:39],s78 offen offset:0 sc1
buffer_load_dwordx4 v[192:195],v1,s[36:39],s78 offen offset:2048 sc1
buffer_load_dword v18,v6,s[36:39],s62 offen offset:0 sc1
s_lshr_b32 s78,s45,1
s_and_b32 s79,s45,1
s_lshl_b32 s79,s79,7
s_lshl_b32 s57,s78,15
s_add_u32 s57,s57,s79
s_add_u32 s57,s57,12582912
s_add_u32 s58,s57,4096
s_lshl_b32 s59,s78,9
s_add_u32 s59,s59,s79
s_add_u32 s59,s59,107020288
s_lshl_b32 s60,s45,9
s_add_u32 s60,s60,s66
s_add_u32 s60,s60,131072
s_add_u32 s45,s45,s46
s_lshr_b32 s78,s45,1
s_and_b32 s79,s45,1
s_lshl_b32 s79,s79,7
s_lshl_b32 s55,s78,15
s_add_u32 s55,s55,s79
s_mul_i32 s56,s45,16384
s_add_u32 s56,s56,98566144
s_cmp_lt_u32 s45,256
s_cselect_b32 s56,s56,0x7fff0000
s_lshl_b32 s61,s78,11
s_add_u32 s61,s61,110587904
s_add_u32 s78,s45,s46
s_lshr_b32 s78,s78,1
s_lshl_b32 s62,s78,11
s_add_u32 s62,s62,110587904
s_waitcnt vmcnt(0)
s_branch Lr_it0_%=
Lr_ta1_%=:
v_mfma_f32_16x16x32_bf16 v[148:151],v[196:199],v[212:215],0
s_add_u32 s78,s55,71303168
s_add_u32 m0,s65,0
M 148,v[200:203],216
buffer_load_dwordx4 v[164:167],v1,s[36:39],s78 offen offset:0 sc1
M 148,v[204:207],220
buffer_load_dwordx4 v[168:171],v1,s[36:39],s78 offen offset:2048 sc1
M 148,v[208:211],224
L 4
D 9,56,0
v_mfma_f32_16x16x32_bf16 v[40:43],v[196:199],v[196:199],0
D 9,56,1024
M 40,v[200:203],200
L 0
D 9,56,2048
M 40,v[204:207],204
D 9,56,3072
M 40,v[208:211],208
s_add_u32 s78,s55,71307264
v_lshlrev_b32 v84,16,v68
v_and_b32 v85,0xffff0000,v68
M 40,v[212:215],212
buffer_load_dwordx4 v[172:175],v1,s[36:39],s78 offen offset:0 sc1
M 40,v[216:219],216
buffer_load_dwordx4 v[176:179],v1,s[36:39],s78 offen offset:2048 sc1
M 40,v[220:223],220
s_add_u32 s78,s55,89128960
v_lshlrev_b32 v86,16,v69
v_and_b32 v87,0xffff0000,v69
M 40,v[224:227],224
buffer_load_dwordx4 v[180:183],v1,s[36:39],s78 offen offset:0 sc1
v_mfma_f32_16x16x32_bf16 v[36:39],v[32:35],v[196:199],0
buffer_load_dwordx4 v[184:187],v1,s[36:39],s78 offen offset:2048 sc1
M 36,v[32:35],200
s_add_u32 s78,s55,89133056
v_lshlrev_b32 v88,16,v70
v_and_b32 v89,0xffff0000,v70
M 36,v[32:35],204
buffer_load_dwordx4 v[188:191],v1,s[36:39],s78 offen offset:0 sc1
M 36,v[32:35],208
buffer_load_dwordx4 v[192:195],v1,s[36:39],s78 offen offset:2048 sc1
M 36,v[32:35],212
v_fma_f32 v40,2.0,v148,v40
v_fma_f32 v41,2.0,v149,v41
M 36,v[32:35],216
v_fma_f32 v42,2.0,v150,v42
v_fma_f32 v43,2.0,v151,v43
M 36,v[32:35],220
v_cndmask_b32 v45,v40,v41,s[70:71]
v_cndmask_b32 v46,v42,v43,s[70:71]
M 36,v[32:35],224
v_cndmask_b32 v45,v45,v46,s[72:73]
v_lshlrev_b32 v90,16,v71
v_mfma_f32_16x16x32_bf16 v[116:119],v[26:29],v[196:199],0
v_and_b32 v91,0xffff0000,v71
v_lshlrev_b32 v92,16,v72
M 116,v[26:29],212
v_and_b32 v93,0xffff0000,v72
v_lshlrev_b32 v94,16,v73
v_mfma_f32_16x16x32_bf16 v[120:123],v[24:27],v[196:199],0
v_and_b32 v95,0xffff0000,v73
v_lshlrev_b32 v96,16,v74
M 120,v[24:27],212
v_and_b32 v97,0xffff0000,v74
v_lshlrev_b32 v98,16,v75
v_mfma_f32_16x16x32_bf16 v[124:127],v[26:29],v[200:203],0
v_add_f32_dpp v44,v36,v36 row_ror:8 row_mask:0xf bank_mask:0xf
v_and_b32 v99,0xffff0000,v75
M 124,v[26:29],216
ds_write_b32 v2,v44 offset:0
v_mfma_f32_16x16x32_bf16 v[128:131],v[24:27],v[200:203],0
ds_write_b32 v0,v45 offset:0
M 128,v[24:27],216
v_lshlrev_b32 v100,16,v76
v_and_b32 v101,0xffff0000,v76
v_mfma_f32_16x16x32_bf16 v[132:135],v[26:29],v[204:207],0
v_lshlrev_b32 v102,16,v77
v_and_b32 v103,0xffff0000,v77
M 132,v[26:29],220
L 0
v_lshlrev_b32 v104,16,v78
v_and_b32 v105,0xffff0000,v78
v_mfma_f32_16x16x32_bf16 v[136:139],v[24:27],v[204:207],0
s_barrier
R 52,3,0
R 56,3,16
M 136,v[24:27],220
R 60,3,32
v_lshlrev_b32 v106,16,v79
v_mfma_f32_16x16x32_bf16 v[140:143],v[26:29],v[208:211],0
v_and_b32 v107,0xffff0000,v79
v_lshlrev_b32 v108,16,v80
M 140,v[26:29],224
v_and_b32 v109,0xffff0000,v80
v_lshlrev_b32 v110,16,v81
v_mfma_f32_16x16x32_bf16 v[144:147],v[24:27],v[208:211],0
v_and_b32 v111,0xffff0000,v81
v_lshlrev_b32 v112,16,v82
M 144,v[24:27],224
v_and_b32 v113,0xffff0000,v82
v_lshlrev_b32 v114,16,v83
v_and_b32 v115,0xffff0000,v83
L 0
v_pk_add_f32 v[52:53],v[52:53],v[54:55]
v_pk_add_f32 v[56:57],v[56:57],v[58:59]
v_pk_add_f32 v[60:61],v[60:61],v[62:63]
v_add_f32 v52,v52,v53
v_pk_add_f32 v[56:57],v[56:57],v[60:61]
v_mul_f32 v52,s68,v52
v_add_f32 v53,v56,v57
v_mul_f32 v53,s68,v53
v_fma_f32 v53,-v52,v52,v53
v_max_f32 v53,0,v53
buffer_load_dword v18,v6,s[36:39],s62 offen offset:0 sc1
v_add_f32 v53,s69,v53
v_rsq_f32 v48,v53
s_nop 0
v_mul_f32_e64 v50,-v52,v48
v_mov_b32 v49,v48
v_mov_b32 v51,v50
v_pk_fma_f32 v[52:53],v[116:117],v[48:49],v[50:51]
v_pk_fma_f32 v[54:55],v[118:119],v[48:49],v[50:51]
v_pk_fma_f32 v[56:57],v[120:121],v[48:49],v[50:51]
v_pk_fma_f32 v[58:59],v[122:123],v[48:49],v[50:51]
v_pk_fma_f32 v[116:117],v[52:53],v[84:85],v[116:117]
v_pk_fma_f32 v[118:119],v[54:55],v[86:87],v[118:119]
v_pk_fma_f32 v[120:121],v[56:57],v[88:89],v[120:121]
v_pk_fma_f32 v[122:123],v[58:59],v[90:91],v[122:123]
v_pk_fma_f32 v[52:53],v[124:125],v[48:49],v[50:51]
v_pk_fma_f32 v[54:55],v[126:127],v[48:49],v[50:51]
v_pk_fma_f32 v[56:57],v[128:129],v[48:49],v[50:51]
v_pk_fma_f32 v[58:59],v[130:131],v[48:49],v[50:51]
v_pk_fma_f32 v[124:125],v[52:53],v[92:93],v[124:125]
v_pk_fma_f32 v[126:127],v[54:55],v[94:95],v[126:127]
v_pk_fma_f32 v[128:129],v[56:57],v[96:97],v[128:129]
v_pk_fma_f32 v[130:131],v[58:59],v[98:99],v[130:131]
v_pk_fma_f32 v[52:53],v[132:133],v[48:49],v[50:51]
v_pk_fma_f32 v[54:55],v[134:135],v[48:49],v[50:51]
v_pk_fma_f32 v[56:57],v[136:137],v[48:49],v[50:51]
v_pk_fma_f32 v[58:59],v[138:139],v[48:49],v[50:51]
v_pk_fma_f32 v[132:133],v[52:53],v[100:101],v[132:133]
v_pk_fma_f32 v[134:135],v[54:55],v[102:103],v[134:135]
v_pk_fma_f32 v[136:137],v[56:57],v[104:105],v[136:137]
v_pk_fma_f32 v[138:139],v[58:59],v[106:107],v[138:139]
v_pk_fma_f32 v[52:53],v[140:141],v[48:49],v[50:51]
v_pk_fma_f32 v[54:55],v[142:143],v[48:49],v[50:51]
v_pk_fma_f32 v[56:57],v[144:145],v[48:49],v[50:51]
v_pk_fma_f32 v[58:59],v[146:147],v[48:49],v[50:51]
v_pk_fma_f32 v[140:141],v[52:53],v[108:109],v[140:141]
v_pk_fma_f32 v[142:143],v[54:55],v[110:111],v[142:143]
v_pk_fma_f32 v[144:145],v[56:57],v[112:113],v[144:145]
v_pk_fma_f32 v[146:147],v[58:59],v[114:115],v[146:147]
v_cvt_pk_bf16_f32 v148,v116,v117
v_cvt_pk_bf16_f32 v149,v118,v119
v_cvt_pk_bf16_f32 v150,v120,v121
v_cvt_pk_bf16_f32 v151,v122,v123
v_cvt_pk_bf16_f32 v152,v124,v125
v_cvt_pk_bf16_f32 v153,v126,v127
s_waitcnt vmcnt(13)
buffer_store_dword v17,v5,s[36:39],s63 offen offset:0 sc0 sc1
s_mov_b32 s63,0x7fff0000
v_mfma_f32_16x16x32_bf16 v[36:39],v[32:35],v[148:151],0
v_cvt_pk_bf16_f32 v154,v128,v129
v_cvt_pk_bf16_f32 v155,v130,v131
v_mfma_f32_16x16x32_bf16 v[40:43],v[148:151],v[148:151],0
v_cvt_pk_bf16_f32 v156,v132,v133
v_cvt_pk_bf16_f32 v157,v134,v135
M 36,v[32:35],152
v_cvt_pk_bf16_f32 v158,v136,v137
v_cvt_pk_bf16_f32 v159,v138,v139
M 40,v[152:155],152
v_cvt_pk_bf16_f32 v160,v140,v141
v_cvt_pk_bf16_f32 v161,v142,v143
M 36,v[32:35],156
v_cvt_pk_bf16_f32 v162,v144,v145
v_cvt_pk_bf16_f32 v163,v146,v147
M 40,v[156:159],156
buffer_store_dwordx4 v[148:151],v1,s[36:39],s57 offen offset:0 sc0 sc1
M 36,v[32:35],160
buffer_store_dwordx4 v[152:155],v1,s[36:39],s57 offen offset:2048 sc0 sc1
M 40,v[160:163],160
buffer_store_dwordx4 v[156:159],v1,s[36:39],s58 offen offset:0 sc0 sc1
s_nop 4
v_add_f32_dpp v44,v36,v36 row_ror:8 row_mask:0xf bank_mask:0xf
s_nop 0
v_cndmask_b32 v45,v40,v41,s[70:71]
v_cndmask_b32 v46,v42,v43,s[70:71]
ds_write_b32 v2,v44 offset:1024
v_cndmask_b32 v45,v45,v46,s[72:73]
ds_write_b32 v0,v45 offset:1024
buffer_store_dwordx4 v[160:163],v1,s[36:39],s58 offen offset:2048 sc0 sc1
L 0
s_barrier
R 52,3,1024
R 56,3,1040
R 60,3,1056
L 0
v_pk_add_f32 v[52:53],v[52:53],v[54:55]
v_pk_add_f32 v[56:57],v[56:57],v[58:59]
v_pk_add_f32 v[60:61],v[60:61],v[62:63]
v_add_f32 v52,v52,v53
v_pk_add_f32 v[56:57],v[56:57],v[60:61]
v_mul_f32 v52,s68,v52
v_add_f32 v53,v56,v57
v_mul_f32 v53,s68,v53
v_fma_f32 v53,-v52,v52,v53
v_max_f32 v53,0,v53
v_add_f32 v54,s69,v53
v_fma_f32 v53,s69,v54,v53
v_rsq_f32 v55,v54
v_rsq_f32 v53,v53
v_mul_f32_e64 v54,-v52,v55
v_mul_f32 v53,s76,v53
buffer_store_dwordx4 v[52:55],v4,s[36:39],s59 offen offset:0 sc0 sc1
s_mov_b32 s63,s60
s_lshr_b32 s78,s45,1
s_and_b32 s79,s45,1
s_lshl_b32 s79,s79,7
s_lshl_b32 s57,s78,15
s_add_u32 s57,s57,s79
s_add_u32 s57,s57,12582912
s_add_u32 s58,s57,4096
s_lshl_b32 s59,s78,9
s_add_u32 s59,s59,s79
s_add_u32 s59,s59,107020288
s_lshl_b32 s60,s45,9
s_add_u32 s60,s60,s66
s_add_u32 s60,s60,131072
s_add_u32 s45,s45,s46
s_lshr_b32 s78,s45,1
s_and_b32 s79,s45,1
s_lshl_b32 s79,s79,7
s_lshl_b32 s55,s78,15
s_add_u32 s55,s55,s79
s_mul_i32 s56,s45,16384
s_add_u32 s56,s56,98566144
s_cmp_lt_u32 s45,256
s_cselect_b32 s56,s56,0x7fff0000
s_lshl_b32 s61,s78,11
s_add_u32 s61,s61,110587904
s_add_u32 s78,s45,s46
s_lshr_b32 s78,s78,1
s_lshl_b32 s62,s78,11
s_add_u32 s62,s62,110587904
s_waitcnt vmcnt(6)
s_branch Lr_it0_%=
Lr_tb1_%=:
v_add_f32 v53,s69,v53
v_rsq_f32 v48,v53
s_nop 0
v_mul_f32_e64 v50,-v52,v48
v_mov_b32 v49,v48
v_mov_b32 v51,v50
v_pk_fma_f32 v[52:53],v[116:117],v[48:49],v[50:51]
v_pk_fma_f32 v[54:55],v[118:119],v[48:49],v[50:51]
v_pk_fma_f32 v[56:57],v[120:121],v[48:49],v[50:51]
v_pk_fma_f32 v[58:59],v[122:123],v[48:49],v[50:51]
s_add_u32 s78,s55,71303168
buffer_load_dwordx4 v[164:167],v1,s[36:39],s78 offen offset:0 sc1
buffer_load_dwordx4 v[168:171],v1,s[36:39],s78 offen offset:2048 sc1
s_add_u32 s78,s55,71307264
buffer_load_dwordx4 v[172:175],v1,s[36:39],s78 offen offset:0 sc1
buffer_load_dwordx4 v[176:179],v1,s[36:39],s78 offen offset:2048 sc1
v_pk_fma_f32 v[116:117],v[52:53],v[84:85],v[116:117]
v_pk_fma_f32 v[118:119],v[54:55],v[86:87],v[118:119]
v_pk_fma_f32 v[120:121],v[56:57],v[88:89],v[120:121]
v_pk_fma_f32 v[122:123],v[58:59],v[90:91],v[122:123]
v_pk_fma_f32 v[52:53],v[124:125],v[48:49],v[50:51]
v_pk_fma_f32 v[54:55],v[126:127],v[48:49],v[50:51]
v_pk_fma_f32 v[56:57],v[128:129],v[48:49],v[50:51]
v_pk_fma_f32 v[58:59],v[130:131],v[48:49],v[50:51]
s_add_u32 s78,s55,89128960
buffer_load_dwordx4 v[180:183],v1,s[36:39],s78 offen offset:0 sc1
buffer_load_dwordx4 v[184:187],v1,s[36:39],s78 offen offset:2048 sc1
s_add_u32 s78,s55,89133056
buffer_load_dwordx4 v[188:191],v1,s[36:39],s78 offen offset:0 sc1
buffer_load_dwordx4 v[192:195],v1,s[36:39],s78 offen offset:2048 sc1
v_pk_fma_f32 v[124:125],v[52:53],v[92:93],v[124:125]
v_pk_fma_f32 v[126:127],v[54:55],v[94:95],v[126:127]
v_pk_fma_f32 v[128:129],v[56:57],v[96:97],v[128:129]
v_pk_fma_f32 v[130:131],v[58:59],v[98:99],v[130:131]
v_pk_fma_f32 v[52:53],v[132:133],v[48:49],v[50:51]
v_pk_fma_f32 v[54:55],v[134:135],v[48:49],v[50:51]
v_pk_fma_f32 v[56:57],v[136:137],v[48:49],v[50:51]
v_pk_fma_f32 v[58:59],v[138:139],v[48:49],v[50:51]
v_pk_fma_f32 v[132:133],v[52:53],v[100:101],v[132:133]
v_pk_fma_f32 v[134:135],v[54:55],v[102:103],v[134:135]
v_pk_fma_f32 v[136:137],v[56:57],v[104:105],v[136:137]
v_pk_fma_f32 v[138:139],v[58:59],v[106:107],v[138:139]
v_pk_fma_f32 v[52:53],v[140:141],v[48:49],v[50:51]
v_pk_fma_f32 v[54:55],v[142:143],v[48:49],v[50:51]
v_pk_fma_f32 v[56:57],v[144:145],v[48:49],v[50:51]
v_pk_fma_f32 v[58:59],v[146:147],v[48:49],v[50:51]
v_pk_fma_f32 v[140:141],v[52:53],v[108:109],v[140:141]
v_pk_fma_f32 v[142:143],v[54:55],v[110:111],v[142:143]
v_pk_fma_f32 v[144:145],v[56:57],v[112:113],v[144:145]
v_pk_fma_f32 v[146:147],v[58:59],v[114:115],v[146:147]
buffer_load_dword v18,v6,s[36:39],s62 offen offset:0 sc1
v_cvt_pk_bf16_f32 v148,v116,v117
v_cvt_pk_bf16_f32 v149,v118,v119
v_cvt_pk_bf16_f32 v150,v120,v121
v_cvt_pk_bf16_f32 v151,v122,v123
v_cvt_pk_bf16_f32 v152,v124,v125
v_cvt_pk_bf16_f32 v153,v126,v127
v_mfma_f32_16x16x32_bf16 v[36:39],v[32:35],v[148:151],0
v_cvt_pk_bf16_f32 v154,v128,v129
v_cvt_pk_bf16_f32 v155,v130,v131
v_mfma_f32_16x16x32_bf16 v[40:43],v[148:151],v[148:151],0
v_cvt_pk_bf16_f32 v156,v132,v133
v_cvt_pk_bf16_f32 v157,v134,v135
M 36,v[32:35],152
v_cvt_pk_bf16_f32 v158,v136,v137
v_cvt_pk_bf16_f32 v159,v138,v139
M 40,v[152:155],152
v_cvt_pk_bf16_f32 v160,v140,v141
v_cvt_pk_bf16_f32 v161,v142,v143
M 36,v[32:35],156
v_cvt_pk_bf16_f32 v162,v144,v145
v_cvt_pk_bf16_f32 v163,v146,v147
M 40,v[156:159],156
buffer_store_dwordx4 v[148:151],v1,s[36:39],s57 offen offset:0 sc0 sc1
M 36,v[32:35],160
buffer_store_dwordx4 v[152:155],v1,s[36:39],s57 offen offset:2048 sc0 sc1
M 40,v[160:163],160
buffer_store_dwordx4 v[156:159],v1,s[36:39],s58 offen offset:0 sc0 sc1
s_nop 4
v_add_f32_dpp v44,v36,v36 row_ror:8 row_mask:0xf bank_mask:0xf
s_nop 0
v_cndmask_b32 v45,v40,v41,s[70:71]
v_cndmask_b32 v46,v42,v43,s[70:71]
ds_write_b32 v2,v44 offset:1024
v_cndmask_b32 v45,v45,v46,s[72:73]
ds_write_b32 v0,v45 offset:1024
buffer_store_dwordx4 v[160:163],v1,s[36:39],s58 offen offset:2048 sc0 sc1
L 0
s_barrier
R 52,3,1024
R 56,3,1040
R 60,3,1056
L 0
v_pk_add_f32 v[52:53],v[52:53],v[54:55]
v_pk_add_f32 v[56:57],v[56:57],v[58:59]
v_pk_add_f32 v[60:61],v[60:61],v[62:63]
v_add_f32 v52,v52,v53
v_pk_add_f32 v[56:57],v[56:57],v[60:61]
v_mul_f32 v52,s68,v52
v_add_f32 v53,v56,v57
v_mul_f32 v53,s68,v53
v_fma_f32 v53,-v52,v52,v53
v_max_f32 v53,0,v53
v_add_f32 v54,s69,v53
v_fma_f32 v53,s69,v54,v53
v_rsq_f32 v55,v54
v_rsq_f32 v53,v53
v_mul_f32_e64 v54,-v52,v55
v_mul_f32 v53,s76,v53
buffer_store_dwordx4 v[52:55],v4,s[36:39],s59 offen offset:0 sc0 sc1
s_mov_b32 s63,s60
s_lshr_b32 s78,s45,1
s_and_b32 s79,s45,1
s_lshl_b32 s79,s79,7
)"
R"(s_lshl_b32 s57,s78,15
s_add_u32 s57,s57,s79
s_add_u32 s57,s57,12582912
s_add_u32 s58,s57,4096
s_lshl_b32 s59,s78,9
s_add_u32 s59,s59,s79
s_add_u32 s59,s59,107020288
s_lshl_b32 s60,s45,9
s_add_u32 s60,s60,s66
s_add_u32 s60,s60,131072
s_add_u32 s45,s45,s46
s_lshr_b32 s78,s45,1
s_and_b32 s79,s45,1
s_lshl_b32 s79,s79,7
s_lshl_b32 s55,s78,15
s_add_u32 s55,s55,s79
s_mul_i32 s56,s45,16384
s_add_u32 s56,s56,98566144
s_cmp_lt_u32 s45,256
s_cselect_b32 s56,s56,0x7fff0000
s_lshl_b32 s61,s78,11
s_add_u32 s61,s61,110587904
s_add_u32 s78,s45,s46
s_lshr_b32 s78,s78,1
s_lshl_b32 s62,s78,11
s_add_u32 s62,s62,110587904
s_waitcnt vmcnt(5)
s_branch Lr_it0_%=
Lr_tc1_%=:
s_add_u32 s78,s55,71303168
buffer_load_dwordx4 v[164:167],v1,s[36:39],s78 offen offset:0 sc1
buffer_load_dwordx4 v[168:171],v1,s[36:39],s78 offen offset:2048 sc1
s_add_u32 s78,s55,71307264
buffer_load_dwordx4 v[172:175],v1,s[36:39],s78 offen offset:0 sc1
buffer_load_dwordx4 v[176:179],v1,s[36:39],s78 offen offset:2048 sc1
s_add_u32 s78,s55,89128960
buffer_load_dwordx4 v[180:183],v1,s[36:39],s78 offen offset:0 sc1
buffer_load_dwordx4 v[184:187],v1,s[36:39],s78 offen offset:2048 sc1
s_add_u32 s78,s55,89133056
buffer_load_dwordx4 v[188:191],v1,s[36:39],s78 offen offset:0 sc1
buffer_load_dwordx4 v[192:195],v1,s[36:39],s78 offen offset:2048 sc1
buffer_load_dword v18,v6,s[36:39],s62 offen offset:0 sc1
s_waitcnt vmcnt(9)
buffer_store_dword v17,v5,s[36:39],s63 offen offset:0 sc0 sc1
s_mov_b32 s63,0x7fff0000
s_lshr_b32 s78,s45,1
s_and_b32 s79,s45,1
s_lshl_b32 s79,s79,7
s_lshl_b32 s57,s78,15
s_add_u32 s57,s57,s79
s_add_u32 s57,s57,12582912
s_add_u32 s58,s57,4096
s_lshl_b32 s59,s78,9
s_add_u32 s59,s59,s79
s_add_u32 s59,s59,107020288
s_lshl_b32 s60,s45,9
s_add_u32 s60,s60,s66
s_add_u32 s60,s60,131072
s_add_u32 s45,s45,s46
s_lshr_b32 s78,s45,1
s_and_b32 s79,s45,1
s_lshl_b32 s79,s79,7
s_lshl_b32 s55,s78,15
s_add_u32 s55,s55,s79
s_mul_i32 s56,s45,16384
s_add_u32 s56,s56,98566144
s_cmp_lt_u32 s45,256
s_cselect_b32 s56,s56,0x7fff0000
s_lshl_b32 s61,s78,11
s_add_u32 s61,s61,110587904
s_add_u32 s78,s45,s46
s_lshr_b32 s78,s78,1
s_lshl_b32 s62,s78,11
s_add_u32 s62,s62,110587904
s_waitcnt vmcnt(1)
s_branch Lr_it0_%=
Lr_done_%=:
s_waitcnt vmcnt(0) lgkmcnt(0)
s_barrier
s_branch Lr_end_%=
.p2align 8
Lr_tab_%=:
s_mov_b64 s[40:41],%24
s_mov_b32 s42,7340032
s_mov_b32 s43,0x00020000
s_add_u32 s83,s50,0
s_add_u32 m0,s54,40960
s_nop 0
buffer_load_dwordx4 v13,s[40:43],s83 offen offset:0  lds
s_add_u32 m0,s54,41984
s_nop 0
buffer_load_dwordx4 v14,s[40:43],s83 offen offset:0  lds
s_add_u32 m0,s54,49024
s_nop 0
buffer_load_dwordx4 v13,s[40:43],s83 offen offset:128  lds
s_add_u32 m0,s54,50048
s_nop 0
buffer_load_dwordx4 v14,s[40:43],s83 offen offset:128  lds
s_setpc_b64 s[88:89]
.p2align 8
s_mov_b64 s[40:41],%24
s_mov_b32 s42,7340032
s_mov_b32 s43,0x00020000
v_add_u32 v230,40960,v15
v_add_u32 v231,40960,v16
T a[0:1],230,0
T a[2:3],230,512
T a[4:5],231,0
T a[6:7],231,512
T a[8:9],230,4096
T a[10:11],230,4608
T a[12:13],231,4096
T a[14:15],231,4608
s_add_u32 s83,s50,131072
s_add_u32 m0,s54,57344
s_nop 0
buffer_load_dwordx4 v13,s[40:43],s83 offen offset:0  lds
s_add_u32 m0,s54,58368
s_nop 0
buffer_load_dwordx4 v14,s[40:43],s83 offen offset:0  lds
s_add_u32 m0,s54,65408
s_nop 0
buffer_load_dwordx4 v13,s[40:43],s83 offen offset:128  lds
s_add_u32 m0,s54,66432
s_nop 0
buffer_load_dwordx4 v14,s[40:43],s83 offen offset:128  lds
s_setpc_b64 s[88:89]
.p2align 8
s_mov_b64 s[40:41],%24
s_mov_b32 s42,7340032
s_mov_b32 s43,0x00020000
v_add_u32 v230,57344,v15
v_add_u32 v231,57344,v16
T a[16:17],230,0
T a[18:19],230,512
T a[20:21],231,0
T a[22:23],231,512
T a[24:25],230,4096
T a[26:27],230,4608
T a[28:29],231,4096
T a[30:31],231,4608
s_add_u32 s83,s50,262144
s_add_u32 m0,s54,73728
s_nop 0
buffer_load_dwordx4 v13,s[40:43],s83 offen offset:0  lds
s_add_u32 m0,s54,74752
s_nop 0
buffer_load_dwordx4 v14,s[40:43],s83 offen offset:0  lds
s_add_u32 m0,s54,81792
s_nop 0
buffer_load_dwordx4 v13,s[40:43],s83 offen offset:128  lds
s_add_u32 m0,s54,82816
s_nop 0
buffer_load_dwordx4 v14,s[40:43],s83 offen offset:128  lds
s_setpc_b64 s[88:89]
.p2align 8
s_mov_b64 s[40:41],%24
s_mov_b32 s42,7340032
s_mov_b32 s43,0x00020000
v_add_u32 v230,73728,v15
v_add_u32 v231,73728,v16
T a[32:33],230,0
T a[34:35],230,512
T a[36:37],231,0
T a[38:39],231,512
T a[40:41],230,4096
T a[42:43],230,4608
T a[44:45],231,4096
T a[46:47],231,4608
s_add_u32 s83,s50,393216
s_add_u32 m0,s54,90112
s_nop 0
buffer_load_dwordx4 v13,s[40:43],s83 offen offset:0  lds
s_add_u32 m0,s54,91136
s_nop 0
buffer_load_dwordx4 v14,s[40:43],s83 offen offset:0  lds
s_add_u32 m0,s54,98176
s_nop 0
buffer_load_dwordx4 v13,s[40:43],s83 offen offset:128  lds
s_add_u32 m0,s54,99200
s_nop 0
buffer_load_dwordx4 v14,s[40:43],s83 offen offset:128  lds
s_setpc_b64 s[88:89]
.p2align 8
s_mov_b64 s[40:41],%24
s_mov_b32 s42,7340032
s_mov_b32 s43,0x00020000
v_add_u32 v230,90112,v15
v_add_u32 v231,90112,v16
T a[48:49],230,0
T a[50:51],230,512
T a[52:53],231,0
T a[54:55],231,512
T a[56:57],230,4096
T a[58:59],230,4608
T a[60:61],231,4096
T a[62:63],231,4608
s_add_u32 s83,s50,524288
s_add_u32 m0,s54,106496
s_nop 0
buffer_load_dwordx4 v13,s[40:43],s83 offen offset:0  lds
s_add_u32 m0,s54,107520
s_nop 0
buffer_load_dwordx4 v14,s[40:43],s83 offen offset:0  lds
s_add_u32 m0,s54,114560
s_nop 0
buffer_load_dwordx4 v13,s[40:43],s83 offen offset:128  lds
s_add_u32 m0,s54,115584
s_nop 0
buffer_load_dwordx4 v14,s[40:43],s83 offen offset:128  lds
s_setpc_b64 s[88:89]
.p2align 8
s_mov_b64 s[40:41],%24
s_mov_b32 s42,7340032
s_mov_b32 s43,0x00020000
v_add_u32 v230,106496,v15
v_add_u32 v231,106496,v16
T a[64:65],230,0
T a[66:67],230,512
T a[68:69],231,0
T a[70:71],231,512
T a[72:73],230,4096
T a[74:75],230,4608
T a[76:77],231,4096
T a[78:79],231,4608
s_add_u32 s83,s50,655360
s_add_u32 m0,s54,122880
s_nop 0
buffer_load_dwordx4 v13,s[40:43],s83 offen offset:0  lds
s_add_u32 m0,s54,123904
s_nop 0
buffer_load_dwordx4 v14,s[40:43],s83 offen offset:0  lds
s_add_u32 m0,s54,130944
s_nop 0
buffer_load_dwordx4 v13,s[40:43],s83 offen offset:128  lds
s_add_u32 m0,s54,131968
s_nop 0
buffer_load_dwordx4 v14,s[40:43],s83 offen offset:128  lds
s_setpc_b64 s[88:89]
.p2align 8
s_mov_b64 s[40:41],%24
s_mov_b32 s42,7340032
s_mov_b32 s43,0x00020000
v_add_u32 v230,122880,v15
v_add_u32 v231,122880,v16
T a[80:81],230,0
T a[82:83],230,512
T a[84:85],231,0
T a[86:87],231,512
T a[88:89],230,4096
T a[90:91],230,4608
T a[92:93],231,4096
T a[94:95],231,4608
s_add_u32 s83,s50,786432
s_add_u32 m0,s54,139264
s_nop 0
buffer_load_dwordx4 v13,s[40:43],s83 offen offset:0  lds
s_add_u32 m0,s54,140288
s_nop 0
buffer_load_dwordx4 v14,s[40:43],s83 offen offset:0  lds
s_add_u32 m0,s54,147328
s_nop 0
buffer_load_dwordx4 v13,s[40:43],s83 offen offset:128  lds
s_add_u32 m0,s54,148352
s_nop 0
buffer_load_dwordx4 v14,s[40:43],s83 offen offset:128  lds
s_setpc_b64 s[88:89]
.p2align 8
s_mov_b64 s[40:41],%24
s_mov_b32 s42,7340032
s_mov_b32 s43,0x00020000
v_add_u32 v230,139264,v15
v_add_u32 v231,139264,v16
T a[96:97],230,0
T a[98:99],230,512
T a[100:101],231,0
T a[102:103],231,512
T a[104:105],230,4096
T a[106:107],230,4608
T a[108:109],231,4096
T a[110:111],231,4608
s_add_u32 s83,s50,917504
s_add_u32 m0,s54,40960
s_nop 0
buffer_load_dwordx4 v13,s[40:43],s83 offen offset:0  lds
s_add_u32 m0,s54,41984
s_nop 0
buffer_load_dwordx4 v14,s[40:43],s83 offen offset:0  lds
s_add_u32 m0,s54,49024
s_nop 0
buffer_load_dwordx4 v13,s[40:43],s83 offen offset:128  lds
s_add_u32 m0,s54,50048
s_nop 0
buffer_load_dwordx4 v14,s[40:43],s83 offen offset:128  lds
s_setpc_b64 s[88:89]
.p2align 8
s_mov_b64 s[40:41],%24
s_mov_b32 s42,7340032
s_mov_b32 s43,0x00020000
v_add_u32 v230,40960,v15
v_add_u32 v231,40960,v16
T a[112:113],230,0
T a[114:115],230,512
T a[116:117],231,0
T a[118:119],231,512
T a[120:121],230,4096
T a[122:123],230,4608
T a[124:125],231,4096
T a[126:127],231,4608
s_add_u32 s83,s50,1048576
s_add_u32 m0,s54,57344
s_nop 0
buffer_load_dwordx4 v13,s[40:43],s83 offen offset:0  lds
s_add_u32 m0,s54,58368
s_nop 0
buffer_load_dwordx4 v14,s[40:43],s83 offen offset:0  lds
s_add_u32 m0,s54,65408
s_nop 0
buffer_load_dwordx4 v13,s[40:43],s83 offen offset:128  lds
s_add_u32 m0,s54,66432
s_nop 0
buffer_load_dwordx4 v14,s[40:43],s83 offen offset:128  lds
s_setpc_b64 s[88:89]
.p2align 8
s_mov_b64 s[40:41],%24
s_mov_b32 s42,7340032
s_mov_b32 s43,0x00020000
v_add_u32 v230,57344,v15
v_add_u32 v231,57344,v16
T a[128:129],230,0
T a[130:131],230,512
T a[132:133],231,0
T a[134:135],231,512
T a[136:137],230,4096
T a[138:139],230,4608
T a[140:141],231,4096
T a[142:143],231,4608
s_add_u32 s83,s50,1179648
s_add_u32 m0,s54,73728
s_nop 0
buffer_load_dwordx4 v13,s[40:43],s83 offen offset:0  lds
s_add_u32 m0,s54,74752
s_nop 0
buffer_load_dwordx4 v14,s[40:43],s83 offen offset:0  lds
s_add_u32 m0,s54,81792
s_nop 0
buffer_load_dwordx4 v13,s[40:43],s83 offen offset:128  lds
s_add_u32 m0,s54,82816
s_nop 0
buffer_load_dwordx4 v14,s[40:43],s83 offen offset:128  lds
s_setpc_b64 s[88:89]
.p2align 8
s_mov_b64 s[40:41],%24
s_mov_b32 s42,7340032
s_mov_b32 s43,0x00020000
v_add_u32 v230,73728,v15
v_add_u32 v231,73728,v16
T a[144:145],230,0
T a[146:147],230,512
T a[148:149],231,0
T a[150:151],231,512
T a[152:153],230,4096
T a[154:155],230,4608
T a[156:157],231,4096
T a[158:159],231,4608
s_add_u32 s83,s50,1310720
s_add_u32 m0,s54,90112
s_nop 0
buffer_load_dwordx4 v13,s[40:43],s83 offen offset:0  lds
s_add_u32 m0,s54,91136
s_nop 0
buffer_load_dwordx4 v14,s[40:43],s83 offen offset:0  lds
s_add_u32 m0,s54,98176
s_nop 0
buffer_load_dwordx4 v13,s[40:43],s83 offen offset:128  lds
s_add_u32 m0,s54,99200
s_nop 0
buffer_load_dwordx4 v14,s[40:43],s83 offen offset:128  lds
s_setpc_b64 s[88:89]
.p2align 8
Lr_end_%=:
Lcode_r_e:
.purgem M
.purgem R
.purgem T
.purgem D
.purgem L
)"
  :: "s"(ws), "s"(__builtin_amdgcn_readfirstlane(sWave)), "s"(__builtin_amdgcn_readfirstlane(sG0)), "s"(__builtin_amdgcn_readfirstlane(sStep)), "s"(__builtin_amdgcn_readfirstlane(sWw)), "s"(__builtin_amdgcn_readfirstlane(sExp)), "v"(vRP), "v"(vRF), "v"(vRwr), "v"(vRrd), "v"(vRS), "v"(vFlag), "v"(vL4), "v"(vLY), "v"(vQ), "v"(vY0), "v"(vTr), "v"(vSa), "v"(vSb), "v"(vWd0), "v"(vWd1), "v"(vWr0), "v"(vWr1), "s"(q), "s"(w4) : "v0", "v1", "v2", "v3", "v4", "v5", "v6", "v7", "v8", "v9", "v10", "v11", "v12", "v13", "v14", "v15", "v16", "v17", "v18", "v19", "v20", "v21", "v22", "v23", "v24", "v25", "v26", "v27", "v28", "v29", "v30", "v31", "v32", "v33", "v34", "v35", "v36", "v37", "v38", "v39", "v40", "v41", "v42", "v43", "v44", "v45", "v46", "v47", "v48", "v49", "v50", "v51", "v52", "v53", "v54", "v55", "v56", "v57", "v58", "v59", "v60", "v61", "v62", "v63", "v64", "v65", "v66", "v67", "v68", "v69", "v70", "v71", "v72", "v73", "v74", "v75", "v76", "v77", "v78", "v79", "v80", "v81", "v82", "v83", "v84", "v85", "v86", "v87", "v88", "v89", "v90", "v91", "v92", "v93", "v94", "v95", "v96", "v97", "v98", "v99", "v100", "v101", "v102", "v103", "v104", "v105", "v106", "v107", "v108", "v109", "v110", "v111", "v112", "v113", "v114", "v115", "v116", "v117", "v118", "v119", "v120", "v121", "v122", "v123", "v124", "v125", "v126", "v127", "v128", "v129", "v130", "v131", "v132", "v133", "v134", "v135", "v136", "v137", "v138", "v139", "v140", "v141", "v142", "v143", "v144", "v145", "v146", "v147", "v148", "v149", "v150", "v151", "v152", "v153", "v154", "v155", "v156", "v157", "v158", "v159", "v160", "v161", "v162", "v163", "v164", "v165", "v166", "v167", "v168", "v169", "v170", "v171", "v172", "v173", "v174", "v175", "v176", "v177", "v178", "v179", "v180", "v181", "v182", "v183", "v184", "v185", "v186", "v187", "v188", "v189", "v190", "v191", "v192", "v193", "v194", "v195", "v196", "v197", "v198", "v199", "v200", "v201", "v202", "v203", "v204", "v205", "v206", "v207", "v208", "v209", "v210", "v211", "v212", "v213", "v214", "v215", "v216", "v217", "v218", "v219", "v220", "v221", "v222", "v223", "v224", "v225", "v226", "v227", "v228", "v229", "v230", "v231", "a0", "a1", "a2", "a3", "a4", "a5", "a6", "a7", "a8", "a9", "a10", "a11", "a12", "a13", "a14", "a15", "a16", "a17", "a18", "a19", "a20", "a21", "a22", "a23", "a24", "a25", "a26", "a27", "a28", "a29", "a30", "a31", "a32", "a33", "a34", "a35", "a36", "a37", "a38", "a39", "a40", "a41", "a42", "a43", "a44", "a45", "a46", "a47", "a48", "a49", "a50", "a51", "a52", "a53", "a54", "a55", "a56", "a57", "a58", "a59", "a60", "a61", "a62", "a63", "a64", "a65", "a66", "a67", "a68", "a69", "a70", "a71", "a72", "a73", "a74", "a75", "a76", "a77", "a78", "a79", "a80", "a81", "a82", "a83", "a84", "a85", "a86", "a87", "a88", "a89", "a90", "a91", "a92", "a93", "a94", "a95", "a96", "a97", "a98", "a99", "a100", "a101", "a102", "a103", "a104", "a105", "a106", "a107", "a108", "a109", "a110", "a111", "a112", "a113", "a114", "a115", "a116", "a117", "a118", "a119", "a120", "a121", "a122", "a123", "a124", "a125", "a126", "a127", "a128", "a129", "a130", "a131", "a132", "a133", "a134", "a135", "a136", "a137", "a138", "a139", "a140", "a141", "a142", "a143", "a144", "a145", "a146", "a147", "a148", "a149", "a150", "a151", "a152", "a153", "a154", "a155", "a156", "a157", "a158", "a159", "a160", "a161", "a162", "a163", "a164", "a165", "a166", "a167", "a168", "a169", "a170", "a171", "a172", "a173", "a174", "a175", "a176", "a177", "a178", "a179", "a180", "a181", "a182", "a183", "a184", "a185", "a186", "a187", "a188", "a189", "a190", "a191", "a192", "a193", "a194", "a195", "a196", "a197", "a198", "a199", "a200", "a201", "a202", "a203", "a204", "a205", "a206", "a207", "a208", "a209", "a210", "a211", "a212", "a213", "a214", "a215", "a216", "a217", "a218", "a219", "a220", "a221", "a222", "a223", "a224", "a225", "a226", "a227", "a228", "a229", "a230", "a231", "a232", "a233", "a234", "a235", "a236", "a237", "a238", "a239", "a240", "a241", "a242", "a243", "a244", "a245", "a246", "a247", "a248", "a249", "a250", "a251", "a252", "a253", "a254", "a255", "s36", "s37", "s38", "s39", "s40", "s41", "s42", "s43", "s44", "s45", "s46", "s47", "s48", "s49", "s50", "s51", "s52", "s53", "s54", "s55", "s56", "s57", "s58", "s59", "s60", "s61", "s62", "s63", "s64", "s65", "s66", "s67", "s68", "s69", "s70", "s71", "s72", "s73", "s74", "s75", "s76", "s77", "s78", "s79", "s80", "s81", "s82", "s83", "s84", "s85", "s86", "s87", "s88", "s89", "s90", "s91", "s92", "s93", "s94", "s95", "s96", "s97", "vcc", "scc", "memory");
    if (RHELP && slice < 16) { kind = 4; rep = 5; kh = slice >> 3; slice &= 7; }
  }
  CTS(9);
#define COMMON(NF, FOFF, FSTR, SNAP, LDW, NL, TOFF)                                                                                            \
  LANE_ID(ln);                                                                                                                       \
  const int l15 = ln & 15, lg = ln >> 4, lq = l15 >> 2, lp = ln & 3, rr = 8 * lg + lq, fj = ln & 31;                                 \
  const unsigned vL16 = ln * 16, vFlag = (ln == 0 && wave == 0) ? 0u : OOB;                                                      \
  const unsigned vSnapOff = (wave == 0 && fj < (NF)) ? (FOFF) + (ln >> 5) * sStep * (FSTR) : OOB;                                    \
  const unsigned vSnapRd = (SNAP) + (fj < (NF) ? ln : ln & 32) * 4;                                                                  \
  const unsigned vWd0 = (ln >> 3) * (LDW) + ((ln & 7) ^ swzB(ln >> 3)) * 16, vWd1 = ((ln >> 3) + 8) * (LDW) + ((ln & 7) ^ swzB(8 + (ln >> 3))) * 16; \
  const unsigned vWr0 = (wave >> 1) * 8192 + rr * 128 + (((wave & 1) * 4 + lp) ^ swzB(rr)) * 16, vWr1 = vWr0 + 8;                    \
  const unsigned vWr2 = 16384 + rr * 128 + ((2 * wave + (lp >> 1)) ^ swzB(rr)) * 16 + (lp & 1) * 8;                                  \
  const unsigned sWave = wave, sRk = kind == 4 ? rep : t_start, sG0 = rep < 5 ? rep : rep == 5 ? U0 + 5 : U1 + rep, sDbg = sDbg0 + (rep < 5 ? 0 : 2048)
  if (kind == 3 || kind == 5) {
    const unsigned sStep = kind == 3 ? 5 : G3OR;
    COMMON(8, fj * FS1, 1024, 131072, 7168, 256, 0);
    const unsigned vOutS = (lg & 1) * 8 + (((wave & 1) * 2 + (lg >> 1)) * 16 + l15) * 16 + (4 + (wave >> 1) - wave) * 1024;
    const unsigned vAux0 = (ln & 7) * 128 + (4 * wave + lg) * 8 + ((ln >> 3) & 1) * 4;
    const unsigned vAux1 = 139264 + (l15 < 2 ? (4 * wave + lg) * 8 + l15 * 4 : 512 + ln * 4), vAux2 = 139264 + l15 * 8;
    const unsigned vAux3 = 0, vAn = l15 == 0 ? 0x3f800000u : 0u, vAe = l15 == 1 ? GELU_CB : 0u;
    const unsigned sOutOff = OFF_G + (slice * 6 + wave) * 1024, sFoutOff = OFF_FG + slice * FSG, sAuxOff = 0, sWw = 16 * wave * 7168 + slice * 384, sBuf = 0;
    const unsigned sExp = __builtin_amdgcn_readfirstlane(ep);
    if (kind == 3) { asm volatile(
R"(.macro M d,w,x
v_mfma_f32_16x16x32_bf16 v[\d:\d+3], \w, v[\x:\x+3], v[\d:\d+3]
.endm
.macro R d,a,o
ds_read_b128 v[\d:\d+3], v\a offset:\o
.endm
.macro T d,a,o
ds_read_b64_tr_b16 \d, v\a offset:\o
.endm
.macro D a,s,o
buffer_load_dwordx4 v\a, s[36:39], s\s offen offset:\o sc1 lds
.endm
.macro L c
s_waitcnt lgkmcnt(\c)
.endm
Lcode_g3_b:
s_mov_b64 s[36:37],%0
s_mov_b32 s38,111243392
s_mov_b32 s39,0x00020000
s_mov_b32 s44,%1
s_mov_b32 s45,%2
s_mov_b32 s46,%3
s_mov_b32 s47,%4
s_mov_b32 s48,%5
s_mov_b32 s49,%6
s_mov_b32 s50,%7
s_mov_b32 s51,%8
s_mov_b32 s53,%9
v_mov_b32 v0,%10
v_mov_b32 v3,%11
v_mov_b32 v5,%12
v_mov_b32 v6,%13
v_mov_b32 v18,%14
v_mov_b32 v19,%15
v_mov_b32 v20,%16
v_mov_b32 v21,%17
v_mov_b32 v22,%18
v_mov_b32 v10,%19
v_mov_b32 v11,%20
v_mov_b32 v12,%21
v_mov_b32 v13,%22
v_mov_b32 v14,%23
v_mov_b32 v15,%24
v_mov_b32 v16,%25
s_mov_b64 s[40:41],%26
s_mov_b32 s42,7340032
s_mov_b32 s43,0x00020000
s_lshl_b32 s57,s44,11
s_cmp_ge_u32 s45,8
s_cbranch_scc1 Lg3_dly_%=
s_sleep 16
Lg3_dly_%=:
s_add_u32 s55,s50,0
s_add_u32 m0,s57,0
s_nop 0
buffer_load_dwordx4 v18,s[40:43],s55 offen offset:0  lds
s_add_u32 m0,s57,1024
s_nop 0
buffer_load_dwordx4 v19,s[40:43],s55 offen offset:0  lds
s_add_u32 m0,s57,8064
s_nop 0
buffer_load_dwordx4 v18,s[40:43],s55 offen offset:128  lds
s_add_u32 m0,s57,9088
s_nop 0
buffer_load_dwordx4 v19,s[40:43],s55 offen offset:128  lds
s_add_u32 m0,s57,16128
s_nop 0
buffer_load_dwordx4 v18,s[40:43],s55 offen offset:256  lds
s_add_u32 m0,s57,17152
s_nop 0
buffer_load_dwordx4 v19,s[40:43],s55 offen offset:256  lds
s_add_u32 s55,s50,458752
s_add_u32 m0,s57,24576
s_nop 0
buffer_load_dwordx4 v18,s[40:43],s55 offen offset:0  lds
s_add_u32 m0,s57,25600
s_nop 0
buffer_load_dwordx4 v19,s[40:43],s55 offen offset:0  lds
s_add_u32 m0,s57,32640
s_nop 0
buffer_load_dwordx4 v18,s[40:43],s55 offen offset:128  lds
s_add_u32 m0,s57,33664
s_nop 0
buffer_load_dwordx4 v19,s[40:43],s55 offen offset:128  lds
s_add_u32 m0,s57,40704
s_nop 0
buffer_load_dwordx4 v18,s[40:43],s55 offen offset:256  lds
s_add_u32 m0,s57,41728
s_nop 0
buffer_load_dwordx4 v19,s[40:43],s55 offen offset:256  lds
s_add_u32 s55,s50,917504
s_add_u32 m0,s57,49152
s_nop 0
buffer_load_dwordx4 v18,s[40:43],s55 offen offset:0  lds
s_add_u32 m0,s57,50176
s_nop 0
buffer_load_dwordx4 v19,s[40:43],s55 offen offset:0  lds
s_add_u32 m0,s57,57216
s_nop 0
buffer_load_dwordx4 v18,s[40:43],s55 offen offset:128  lds
s_add_u32 m0,s57,58240
s_nop 0
buffer_load_dwordx4 v19,s[40:43],s55 offen offset:128  lds
s_add_u32 m0,s57,65280
s_nop 0
buffer_load_dwordx4 v18,s[40:43],s55 offen offset:256  lds
s_add_u32 m0,s57,66304
s_nop 0
buffer_load_dwordx4 v19,s[40:43],s55 offen offset:256  lds
s_add_u32 s55,s50,1376256
s_add_u32 m0,s57,73728
s_nop 0
buffer_load_dwordx4 v18,s[40:43],s55 offen offset:0  lds
s_add_u32 m0,s57,74752
s_nop 0
buffer_load_dwordx4 v19,s[40:43],s55 offen offset:0  lds
s_add_u32 m0,s57,81792
s_nop 0
buffer_load_dwordx4 v18,s[40:43],s55 offen offset:128  lds
s_add_u32 m0,s57,82816
s_nop 0
buffer_load_dwordx4 v19,s[40:43],s55 offen offset:128  lds
s_add_u32 m0,s57,89856
s_nop 0
buffer_load_dwordx4 v18,s[40:43],s55 offen offset:256  lds
s_add_u32 m0,s57,90880
s_nop 0
buffer_load_dwordx4 v19,s[40:43],s55 offen offset:256  lds
s_add_u32 s55,s50,1835008
s_add_u32 m0,s57,98304
s_nop 0
buffer_load_dwordx4 v18,s[40:43],s55 offen offset:0  lds
s_add_u32 m0,s57,99328
s_nop 0
buffer_load_dwordx4 v19,s[40:43],s55 offen offset:0  lds
s_add_u32 m0,s57,106368
s_nop 0
buffer_load_dwordx4 v18,s[40:43],s55 offen offset:128  lds
s_add_u32 m0,s57,107392
s_nop 0
buffer_load_dwordx4 v19,s[40:43],s55 offen offset:128  lds
s_add_u32 m0,s57,114432
s_nop 0
buffer_load_dwordx4 v18,s[40:43],s55 offen offset:256  lds
s_add_u32 m0,s57,115456
s_nop 0
buffer_load_dwordx4 v19,s[40:43],s55 offen offset:256  lds
s_waitcnt vmcnt(24) lgkmcnt(0)
s_barrier
s_add_u32 s55,s50,2293760
s_add_u32 m0,s57,122880
s_nop 0
buffer_load_dwordx4 v18,s[40:43],s55 offen offset:0  lds
s_add_u32 m0,s57,123904
s_nop 0
buffer_load_dwordx4 v19,s[40:43],s55 offen offset:0  lds
s_add_u32 m0,s57,130944
s_nop 0
buffer_load_dwordx4 v18,s[40:43],s55 offen offset:128  lds
s_add_u32 m0,s57,131968
s_nop 0
buffer_load_dwordx4 v19,s[40:43],s55 offen offset:128  lds
s_add_u32 m0,s57,139008
s_nop 0
buffer_load_dwordx4 v18,s[40:43],s55 offen offset:256  lds
s_add_u32 m0,s57,140032
s_nop 0
buffer_load_dwordx4 v19,s[40:43],s55 offen offset:256  lds
v_add_u32 v26,0,v20
v_add_u32 v27,0,v21
v_add_u32 v28,0,v22
T a[0:1],26,0
T a[2:3],26,512
T a[4:5],27,0
T a[6:7],27,512
T a[8:9],28,0
T a[10:11],28,512
T a[12:13],26,4096
T a[14:15],26,4608
T a[16:17],27,4096
T a[18:19],27,4608
T a[20:21],28,4096
T a[22:23],28,4608
s_waitcnt vmcnt(24) lgkmcnt(0)
s_barrier
s_add_u32 s55,s50,2752512
s_add_u32 m0,s57,0
s_nop 0
buffer_load_dwordx4 v18,s[40:43],s55 offen offset:0  lds
s_add_u32 m0,s57,1024
s_nop 0
buffer_load_dwordx4 v19,s[40:43],s55 offen offset:0  lds
s_add_u32 m0,s57,8064
s_nop 0
buffer_load_dwordx4 v18,s[40:43],s55 offen offset:128  lds
s_add_u32 m0,s57,9088
s_nop 0
buffer_load_dwordx4 v19,s[40:43],s55 offen offset:128  lds
s_add_u32 m0,s57,16128
s_nop 0
buffer_load_dwordx4 v18,s[40:43],s55 offen offset:256  lds
s_add_u32 m0,s57,17152
s_nop 0
buffer_load_dwordx4 v19,s[40:43],s55 offen offset:256  lds
v_add_u32 v26,24576,v20
v_add_u32 v27,24576,v21
v_add_u32 v28,24576,v22
T a[24:25],26,0
T a[26:27],26,512
T a[28:29],27,0
T a[30:31],27,512
T a[32:33],28,0
T a[34:35],28,512
T a[36:37],26,4096
T a[38:39],26,4608
T a[40:41],27,4096
T a[42:43],27,4608
T a[44:45],28,4096
T a[46:47],28,4608
s_waitcnt vmcnt(24) lgkmcnt(0)
s_barrier
s_add_u32 s55,s50,3211264
s_add_u32 m0,s57,24576
s_nop 0
buffer_load_dwordx4 v18,s[40:43],s55 offen offset:0  lds
s_add_u32 m0,s57,25600
s_nop 0
buffer_load_dwordx4 v19,s[40:43],s55 offen offset:0  lds
s_add_u32 m0,s57,32640
s_nop 0
buffer_load_dwordx4 v18,s[40:43],s55 offen offset:128  lds
s_add_u32 m0,s57,33664
s_nop 0
buffer_load_dwordx4 v19,s[40:43],s55 offen offset:128  lds
s_add_u32 m0,s57,40704
s_nop 0
buffer_load_dwordx4 v18,s[40:43],s55 offen offset:256  lds
s_add_u32 m0,s57,41728
s_nop 0
buffer_load_dwordx4 v19,s[40:43],s55 offen offset:256  lds
v_add_u32 v26,49152,v20
v_add_u32 v27,49152,v21
v_add_u32 v28,49152,v22
T a[48:49],26,0
T a[50:51],26,512
T a[52:53],27,0
T a[54:55],27,512
T a[56:57],28,0
T a[58:59],28,512
T a[60:61],26,4096
T a[62:63],26,4608
T a[64:65],27,4096
T a[66:67],27,4608
T a[68:69],28,4096
T a[70:71],28,4608
s_waitcnt vmcnt(24) lgkmcnt(0)
s_barrier
s_add_u32 s55,s50,3670016
s_add_u32 m0,s57,49152
s_nop 0
buffer_load_dwordx4 v18,s[40:43],s55 offen offset:0  lds
s_add_u32 m0,s57,50176
s_nop 0
buffer_load_dwordx4 v19,s[40:43],s55 offen offset:0  lds
s_add_u32 m0,s57,57216
s_nop 0
buffer_load_dwordx4 v18,s[40:43],s55 offen offset:128  lds
s_add_u32 m0,s57,58240
s_nop 0
buffer_load_dwordx4 v19,s[40:43],s55 offen offset:128  lds
s_add_u32 m0,s57,65280
s_nop 0
buffer_load_dwordx4 v18,s[40:43],s55 offen offset:256  lds
s_add_u32 m0,s57,66304
s_nop 0
buffer_load_dwordx4 v19,s[40:43],s55 offen offset:256  lds
v_add_u32 v26,73728,v20
v_add_u32 v27,73728,v21
v_add_u32 v28,73728,v22
T a[72:73],26,0
T a[74:75],26,512
T a[76:77],27,0
T a[78:79],27,512
T a[80:81],28,0
T a[82:83],28,512
T a[84:85],26,4096
T a[86:87],26,4608
T a[88:89],27,4096
T a[90:91],27,4608
T a[92:93],28,4096
T a[94:95],28,4608
s_waitcnt vmcnt(24) lgkmcnt(0)
s_barrier
s_add_u32 s55,s50,4128768
s_add_u32 m0,s57,73728
s_nop 0
buffer_load_dwordx4 v18,s[40:43],s55 offen offset:0  lds
s_add_u32 m0,s57,74752
s_nop 0
buffer_load_dwordx4 v19,s[40:43],s55 offen offset:0  lds
s_add_u32 m0,s57,81792
s_nop 0
buffer_load_dwordx4 v18,s[40:43],s55 offen offset:128  lds
s_add_u32 m0,s57,82816
s_nop 0
buffer_load_dwordx4 v19,s[40:43],s55 offen offset:128  lds
s_add_u32 m0,s57,89856
s_nop 0
buffer_load_dwordx4 v18,s[40:43],s55 offen offset:256  lds
s_add_u32 m0,s57,90880
s_nop 0
buffer_load_dwordx4 v19,s[40:43],s55 offen offset:256  lds
v_add_u32 v26,98304,v20
v_add_u32 v27,98304,v21
v_add_u32 v28,98304,v22
T a[96:97],26,0
T a[98:99],26,512
T a[100:101],27,0
T a[102:103],27,512
T a[104:105],28,0
T a[106:107],28,512
T a[108:109],26,4096
T a[110:111],26,4608
T a[112:113],27,4096
T a[114:115],27,4608
T a[116:117],28,4096
T a[118:119],28,4608
s_waitcnt vmcnt(24) lgkmcnt(0)
s_barrier
s_add_u32 s55,s50,4587520
s_add_u32 m0,s57,98304
s_nop 0
buffer_load_dwordx4 v18,s[40:43],s55 offen offset:0  lds
s_add_u32 m0,s57,99328
s_nop 0
buffer_load_dwordx4 v19,s[40:43],s55 offen offset:0  lds
s_add_u32 m0,s57,106368
s_nop 0
buffer_load_dwordx4 v18,s[40:43],s55 offen offset:128  lds
s_add_u32 m0,s57,107392
s_nop 0
buffer_load_dwordx4 v19,s[40:43],s55 offen offset:128  lds
s_add_u32 m0,s57,114432
s_nop 0
buffer_load_dwordx4 v18,s[40:43],s55 offen offset:256  lds
s_add_u32 m0,s57,115456
s_nop 0
buffer_load_dwordx4 v19,s[40:43],s55 offen offset:256  lds
v_add_u32 v26,122880,v20
v_add_u32 v27,122880,v21
v_add_u32 v28,122880,v22
T a[120:121],26,0
T a[122:123],26,512
T a[124:125],27,0
T a[126:127],27,512
T a[128:129],28,0
T a[130:131],28,512
T a[132:133],26,4096
T a[134:135],26,4608
T a[136:137],27,4096
T a[138:139],27,4608
T a[140:141],28,4096
T a[142:143],28,4608
s_waitcnt vmcnt(24) lgkmcnt(0)
s_barrier
s_add_u32 s55,s50,5046272
s_add_u32 m0,s57,122880
s_nop 0
buffer_load_dwordx4 v18,s[40:43],s55 offen offset:0  lds
s_add_u32 m0,s57,123904
s_nop 0
buffer_load_dwordx4 v19,s[40:43],s55 offen offset:0  lds
s_add_u32 m0,s57,130944
s_nop 0
buffer_load_dwordx4 v18,s[40:43],s55 offen offset:128  lds
s_add_u32 m0,s57,131968
s_nop 0
buffer_load_dwordx4 v19,s[40:43],s55 offen offset:128  lds
s_add_u32 m0,s57,139008
s_nop 0
buffer_load_dwordx4 v18,s[40:43],s55 offen offset:256  lds
s_add_u32 m0,s57,140032
s_nop 0
buffer_load_dwordx4 v19,s[40:43],s55 offen offset:256  lds
v_add_u32 v26,0,v20
v_add_u32 v27,0,v21
v_add_u32 v28,0,v22
T a[144:145],26,0
T a[146:147],26,512
T a[148:149],27,0
T a[150:151],27,512
T a[152:153],28,0
T a[154:155],28,512
T a[156:157],26,4096
T a[158:159],26,4608
T a[160:161],27,4096
T a[162:163],27,4608
T a[164:165],28,4096
T a[166:167],28,4608
s_waitcnt vmcnt(24) lgkmcnt(0)
s_barrier
s_add_u32 s55,s50,5505024
s_add_u32 m0,s57,0
s_nop 0
buffer_load_dwordx4 v18,s[40:43],s55 offen offset:0  lds
s_add_u32 m0,s57,1024
s_nop 0
buffer_load_dwordx4 v19,s[40:43],s55 offen offset:0  lds
s_add_u32 m0,s57,8064
s_nop 0
buffer_load_dwordx4 v18,s[40:43],s55 offen offset:128  lds
s_add_u32 m0,s57,9088
s_nop 0
buffer_load_dwordx4 v19,s[40:43],s55 offen offset:128  lds
s_add_u32 m0,s57,16128
s_nop 0
buffer_load_dwordx4 v18,s[40:43],s55 offen offset:256  lds
s_add_u32 m0,s57,17152
s_nop 0
buffer_load_dwordx4 v19,s[40:43],s55 offen offset:256  lds
v_add_u32 v26,24576,v20
v_add_u32 v27,24576,v21
v_add_u32 v28,24576,v22
T a[168:169],26,0
T a[170:171],26,512
T a[172:173],27,0
T a[174:175],27,512
T a[176:177],28,0
T a[178:179],28,512
T a[180:181],26,4096
T a[182:183],26,4608
T a[184:185],27,4096
T a[186:187],27,4608
T a[188:189],28,4096
T a[190:191],28,4608
s_waitcnt vmcnt(24) lgkmcnt(0)
s_barrier
s_add_u32 s55,s50,5963776
s_add_u32 m0,s57,24576
s_nop 0
buffer_load_dwordx4 v18,s[40:43],s55 offen offset:0  lds
s_add_u32 m0,s57,25600
s_nop 0
buffer_load_dwordx4 v19,s[40:43],s55 offen offset:0  lds
s_add_u32 m0,s57,32640
s_nop 0
buffer_load_dwordx4 v18,s[40:43],s55 offen offset:128  lds
s_add_u32 m0,s57,33664
s_nop 0
buffer_load_dwordx4 v19,s[40:43],s55 offen offset:128  lds
s_add_u32 m0,s57,40704
s_nop 0
buffer_load_dwordx4 v18,s[40:43],s55 offen offset:256  lds
s_add_u32 m0,s57,41728
s_nop 0
buffer_load_dwordx4 v19,s[40:43],s55 offen offset:256  lds
v_add_u32 v26,49152,v20
v_add_u32 v27,49152,v21
v_add_u32 v28,49152,v22
T a[192:193],26,0
T a[194:195],26,512
T a[196:197],27,0
T a[198:199],27,512
T a[200:201],28,0
T a[202:203],28,512
T a[204:205],26,4096
T a[206:207],26,4608
T a[208:209],27,4096
T a[210:211],27,4608
T a[212:213],28,4096
T a[214:215],28,4608
s_waitcnt vmcnt(24) lgkmcnt(0)
s_barrier
s_add_u32 s55,s50,6422528
s_add_u32 m0,s57,49152
s_nop 0
buffer_load_dwordx4 v18,s[40:43],s55 offen offset:0  lds
s_add_u32 m0,s57,50176
s_nop 0
buffer_load_dwordx4 v19,s[40:43],s55 offen offset:0  lds
s_add_u32 m0,s57,57216
s_nop 0
buffer_load_dwordx4 v18,s[40:43],s55 offen offset:128  lds
s_add_u32 m0,s57,58240
s_nop 0
buffer_load_dwordx4 v19,s[40:43],s55 offen offset:128  lds
s_add_u32 m0,s57,65280
s_nop 0
buffer_load_dwordx4 v18,s[40:43],s55 offen offset:256  lds
s_add_u32 m0,s57,66304
s_nop 0
buffer_load_dwordx4 v19,s[40:43],s55 offen offset:256  lds
v_add_u32 v26,73728,v20
v_add_u32 v27,73728,v21
v_add_u32 v28,73728,v22
T a[216:217],26,0
T a[218:219],26,512
T a[220:221],27,0
T a[222:223],27,512
T a[224:225],28,0
)"
R"(T a[226:227],28,512
T a[228:229],26,4096
T a[230:231],26,4608
T a[232:233],27,4096
T a[234:235],27,4608
T a[236:237],28,4096
T a[238:239],28,4608
s_waitcnt vmcnt(24) lgkmcnt(0)
s_barrier
s_add_u32 s55,s50,6881280
s_add_u32 m0,s57,73728
s_nop 0
buffer_load_dwordx4 v18,s[40:43],s55 offen offset:0  lds
s_add_u32 m0,s57,74752
s_nop 0
buffer_load_dwordx4 v19,s[40:43],s55 offen offset:0  lds
s_add_u32 m0,s57,81792
s_nop 0
buffer_load_dwordx4 v18,s[40:43],s55 offen offset:128  lds
s_add_u32 m0,s57,82816
s_nop 0
buffer_load_dwordx4 v19,s[40:43],s55 offen offset:128  lds
s_add_u32 m0,s57,89856
s_nop 0
buffer_load_dwordx4 v18,s[40:43],s55 offen offset:256  lds
s_add_u32 m0,s57,90880
s_nop 0
buffer_load_dwordx4 v19,s[40:43],s55 offen offset:256  lds
v_add_u32 v26,98304,v20
v_add_u32 v27,98304,v21
v_add_u32 v28,98304,v22
T a[240:241],26,0
T a[242:243],26,512
T a[244:245],27,0
T a[246:247],27,512
T a[248:249],28,0
T a[250:251],28,512
T a[252:253],26,4096
T a[254:255],26,4608
T v[128:129],27,4096
T v[130:131],27,4608
T v[132:133],28,4096
T v[134:135],28,4608
s_waitcnt vmcnt(24) lgkmcnt(0)
s_barrier
v_add_u32 v26,122880,v20
v_add_u32 v27,122880,v21
v_add_u32 v28,122880,v22
T v[136:137],26,0
T v[138:139],26,512
T v[140:141],27,0
T v[142:143],27,512
T v[144:145],28,0
T v[146:147],28,512
T v[148:149],26,4096
T v[150:151],26,4608
T v[152:153],27,4096
T v[154:155],27,4608
T v[156:157],28,4096
T v[158:159],28,4608
s_waitcnt vmcnt(18) lgkmcnt(0)
s_barrier
v_add_u32 v26,0,v20
v_add_u32 v27,0,v21
v_add_u32 v28,0,v22
T v[160:161],26,0
T v[162:163],26,512
T v[164:165],27,0
T v[166:167],27,512
T v[168:169],28,0
T v[170:171],28,512
T v[172:173],26,4096
T v[174:175],26,4608
T v[176:177],27,4096
T v[178:179],27,4608
T v[180:181],28,4096
T v[182:183],28,4608
s_waitcnt vmcnt(12) lgkmcnt(0)
s_barrier
v_add_u32 v26,24576,v20
v_add_u32 v27,24576,v21
v_add_u32 v28,24576,v22
T v[184:185],26,0
T v[186:187],26,512
T v[188:189],27,0
T v[190:191],27,512
T v[192:193],28,0
T v[194:195],28,512
T v[196:197],26,4096
T v[198:199],26,4608
T v[200:201],27,4096
T v[202:203],27,4608
T v[204:205],28,4096
T v[206:207],28,4608
s_waitcnt vmcnt(6) lgkmcnt(0)
s_barrier
v_add_u32 v26,49152,v20
v_add_u32 v27,49152,v21
v_add_u32 v28,49152,v22
T v[208:209],26,0
T v[210:211],26,512
T v[212:213],27,0
T v[214:215],27,512
T v[216:217],28,0
T v[218:219],28,512
T v[220:221],26,4096
T v[222:223],26,4608
T v[224:225],27,4096
T v[226:227],27,4608
T v[228:229],28,4096
T v[230:231],28,4608
s_waitcnt vmcnt(0) lgkmcnt(0)
s_barrier
v_add_u32 v26,73728,v20
v_add_u32 v27,73728,v21
v_add_u32 v28,73728,v22
T v[232:233],26,0
T v[234:235],26,512
T v[236:237],27,0
T v[238:239],27,512
T v[240:241],28,0
T v[242:243],28,512
T v[244:245],26,4096
T v[246:247],26,4608
T v[248:249],27,4096
T v[250:251],27,4608
T v[252:253],28,4096
T v[254:255],28,4608
L 0
s_barrier
v_mov_b32 v42,0x3f803f80
v_mov_b32 v43,0x3f803f80
v_mov_b32 v44,0x3f803f80
v_mov_b32 v45,0x3f803f80
s_nop 1
v_mfma_f32_16x16x32_bf16 v[82:85],a[0:3],v[42:45],0
v_mfma_f32_16x16x32_bf16 v[86:89],a[4:7],v[42:45],0
v_mfma_f32_16x16x32_bf16 v[90:93],a[8:11],v[42:45],0
M 82,a[12:15],42
M 86,a[16:19],42
M 90,a[20:23],42
M 82,a[24:27],42
M 86,a[28:31],42
M 90,a[32:35],42
M 82,a[36:39],42
M 86,a[40:43],42
M 90,a[44:47],42
M 82,a[48:51],42
M 86,a[52:55],42
M 90,a[56:59],42
M 82,a[60:63],42
M 86,a[64:67],42
M 90,a[68:71],42
M 82,a[72:75],42
M 86,a[76:79],42
M 90,a[80:83],42
M 82,a[84:87],42
M 86,a[88:91],42
M 90,a[92:95],42
M 82,a[96:99],42
M 86,a[100:103],42
M 90,a[104:107],42
M 82,a[108:111],42
M 86,a[112:115],42
M 90,a[116:119],42
M 82,a[120:123],42
M 86,a[124:127],42
M 90,a[128:131],42
M 82,a[132:135],42
M 86,a[136:139],42
M 90,a[140:143],42
M 82,a[144:147],42
M 86,a[148:151],42
M 90,a[152:155],42
M 82,a[156:159],42
M 86,a[160:163],42
M 90,a[164:167],42
M 82,a[168:171],42
M 86,a[172:175],42
M 90,a[176:179],42
M 82,a[180:183],42
M 86,a[184:187],42
M 90,a[188:191],42
M 82,a[192:195],42
M 86,a[196:199],42
M 90,a[200:203],42
M 82,a[204:207],42
M 86,a[208:211],42
M 90,a[212:215],42
M 82,a[216:219],42
M 86,a[220:223],42
M 90,a[224:227],42
M 82,a[228:231],42
M 86,a[232:235],42
M 90,a[236:239],42
M 82,a[240:243],42
M 86,a[244:247],42
M 90,a[248:251],42
M 82,a[252:255],42
M 86,v[128:131],42
M 90,v[132:135],42
M 82,v[136:139],42
M 86,v[140:143],42
M 90,v[144:147],42
M 82,v[148:151],42
M 86,v[152:155],42
M 90,v[156:159],42
M 82,v[160:163],42
M 86,v[164:167],42
M 90,v[168:171],42
M 82,v[172:175],42
M 86,v[176:179],42
M 90,v[180:183],42
M 82,v[184:187],42
M 86,v[188:191],42
M 90,v[192:195],42
M 82,v[196:199],42
M 86,v[200:203],42
M 90,v[204:207],42
M 82,v[208:211],42
M 86,v[212:215],42
M 90,v[216:219],42
M 82,v[220:223],42
M 86,v[224:227],42
M 90,v[228:231],42
M 82,v[232:235],42
M 86,v[236:239],42
M 90,v[240:243],42
M 82,v[244:247],42
M 86,v[248:251],42
M 90,v[252:255],42
v_mov_b32 v4,s53
s_mov_b32 s82,0
s_mov_b32 s80,0
s_mov_b32 s54,s45
s_mul_i32 s57,s44,4096
s_mul_i32 s55,s45,32768
s_add_u32 s55,s55,s57
s_add_u32 s55,s55,31457280
s_mul_i32 s57,s44,4096
s_mul_i32 s60,s46,32768
s_mul_i32 s61,s46,1024
s_mul_i32 s62,s46,114688
s_mul_i32 s63,s46,2560
s_mul_i32 s64,s46,1024
s_lshl_b32 s59,s44,8
s_add_u32 s59,s59,131072
s_mul_i32 s58,s45,1024
s_add_u32 s58,s58,109613056
s_mul_i32 s65,s45,114688
s_add_u32 s65,s65,s47
s_mul_i32 s68,s45,2560
s_add_u32 s68,s68,s48
s_mul_i32 s73,s45,1024
s_add_u32 s73,s73,107479040
s_mul_i32 s74,s45,32768
s_add_u32 s74,s74,s49
s_mul_i32 s75,s45,1024
s_add_u32 s75,s75,107479040
s_mov_b32 s66,0x7fff0000
s_mov_b32 s67,0x7fff0000
s_mov_b32 s69,0x7fff0000
s_mov_b32 s76,0x7fff0000
s_mov_b32 s77,0x7fff0000
s_mov_b32 s70,0x7fff0000
s_mov_b32 s71,0x7fff0000
s_mov_b32 s72,0x7fff0000
v_mov_b32 v1,v0
v_add_u32 v2,16384,v0
s_mov_b32 s84,16384
s_mov_b32 s86,0x3a800000
s_mov_b32 s87,0x3727c5ac
s_mov_b32 s88,0x409d30fc
s_mov_b32 s89,0xbeeff558
s_lshr_b32 s85,s44,1
s_mul_i32 s85,s85,127
v_lshlrev_b32 v8,22,v0
v_and_b32 v8,0x80000000,v8
v_or_b32 v8,v8,v5
v_add_u32 v2,32768,v0
s_lshl_b32 s84,s57,1
s_add_u32 s84,s84,32768
s_getpc_b64 s[40:41]
Lg3_pc_%=:
s_lshl_b32 s78,s44,13
v_lshlrev_b32 v94,3,v0
v_add_u32 v94,s78,v94
v_min_u32 v94,Lcode_g3_e-Lg3_pc_%=-4,v94
s_mov_b32 m0,163584
s_nop 0
global_load_lds_dword v94,s[40:41]
v_add_u32 v94,32768,v94
v_min_u32 v94,Lcode_g3_e-Lg3_pc_%=-4,v94
global_load_lds_dword v94,s[40:41]
s_add_u32 m0,s59,4096
s_nop 0
buffer_load_dword v8,s[36:39],s58 offen offset:0 sc1 lds
buffer_store_dword v4,v3,s[36:39],s71 offen offset:0 sc0 sc1
s_add_u32 m0,s59,5120
s_nop 0
buffer_load_dword v8,s[36:39],s58 offen offset:0 sc1 lds
buffer_store_dword v4,v3,s[36:39],s71 offen offset:0 sc0 sc1
s_mov_b32 s83,0
Lg3_poll_%=:
s_add_u32 s78,s83,2
s_and_b32 s78,s78,3
s_lshl_b32 s78,s78,10
s_add_u32 s78,s78,4096
s_add_u32 m0,s59,s78
s_nop 0
buffer_load_dword v8,s[36:39],s58 offen offset:0 sc1 lds
s_and_b32 s78,s83,3
s_lshl_b32 s78,s78,10
v_add_u32 v94,s78,v6
s_waitcnt vmcnt(4)
s_barrier
ds_read_b32 v7,v94 offset:4096
buffer_store_dword v4,v3,s[36:39],s70 offen offset:0 sc0 sc1
s_mov_b32 s70,s71
s_mov_b32 s71,s72
s_mov_b32 s72,0x7fff0000
s_add_u32 s83,s83,1
s_add_u32 s82,s82,1
L 0
v_cmp_eq_u32 vcc,s53,v7
s_nop 0
s_cmp_eq_u32 vcc_lo,-1
s_cbranch_scc1 Lg3_go_%=
s_cmp_lt_u32 s82,20000
s_cbranch_scc1 Lg3_poll_%=
Lg3_go_%=:
buffer_load_dword v114,v11,s[36:39],s73 offen offset:0 sc1
s_add_u32 s73,s73,s64
s_add_u32 m0,s57,0
s_add_u32 s56,s55,0
D 0,56,0
D 0,56,1024
D 0,56,2048
D 0,56,3072
s_add_u32 s58,s58,s61
s_add_u32 m0,s59,2048
s_nop 0
buffer_load_dword v8,s[36:39],s58 offen offset:0 sc1 lds
s_add_u32 m0,s57,16384
s_add_u32 s56,s55,16384
D 0,56,0
D 0,56,1024
D 0,56,2048
D 0,56,3072
s_add_u32 s55,s55,s60
s_sub_u32 s57,s84,s57
s_add_u32 s54,s54,s46
s_waitcnt vmcnt(4)
s_barrier
buffer_store_dword v4,v3,s[36:39],s70 offen offset:0 sc0 sc1
s_min_u32 s71,s71,s72
buffer_store_dword v4,v3,s[36:39],s71 offen offset:0 sc0 sc1
s_mov_b32 s70,0x7fff0000
s_mov_b32 s71,0x7fff0000
s_mov_b32 s72,0x7fff0000
s_cmp_eq_u32 s80,1
buffer_store_dword v4,v3,s[36:39],s71 offen offset:0 sc0 sc1
s_cbranch_scc1 Lg3_st1_%=
s_branch Lg3_st0_%=
Lg3_gox_%=:
s_add_u32 m0,s59,2048
s_nop 0
buffer_load_dword v8,s[36:39],s58 offen offset:0 sc1 lds
s_waitcnt vmcnt(0)
s_barrier
buffer_store_dword v4,v3,s[36:39],s70 offen offset:0 sc0 sc1
s_min_u32 s71,s71,s72
buffer_store_dword v4,v3,s[36:39],s71 offen offset:0 sc0 sc1
s_mov_b32 s70,0x7fff0000
s_mov_b32 s71,0x7fff0000
s_mov_b32 s72,0x7fff0000
s_cmp_eq_u32 s80,1
s_cbranch_scc1 Lg3_st1_%=
Lg3_st0_%=:
ds_read_b32 v7,v6 offset:2048
R 42,1,0
R 46,1,1024
R 50,1,2048
R 54,1,3072
R 58,1,4096
L 5
v_cmp_eq_u32 vcc,s53,v7
L 3
s_cmp_eq_u32 vcc_lo,-1
s_cbranch_scc1 Lg3_F0_%=
s_branch Lg3_L0_%=
Lg3_st1_%=:
ds_read_b32 v7,v6 offset:2048
R 42,2,0
R 46,2,1024
R 50,2,2048
R 54,2,3072
R 58,2,4096
L 5
v_cmp_eq_u32 vcc,s53,v7
L 3
s_cmp_eq_u32 vcc_lo,-1
s_cbranch_scc1 Lg3_F1_%=
s_branch Lg3_L1_%=
Lg3_F1_%=:
s_cmp_eq_u32 s44,1
s_cbranch_scc1 Lg3_F1w1_%=
s_cmp_eq_u32 s44,2
s_cbranch_scc1 Lg3_F1w2_%=
s_cmp_eq_u32 s44,3
s_cbranch_scc1 Lg3_F1w3_%=
s_branch Lg3_F1w0_%=
Lg3_F0_%=:
s_cmp_eq_u32 s44,1
s_cbranch_scc1 Lg3_F0w1_%=
s_cmp_eq_u32 s44,2
s_cbranch_scc1 Lg3_F0w2_%=
s_cmp_eq_u32 s44,3
s_cbranch_scc1 Lg3_F0w3_%=
Lg3_F0w0_%=:
v_mfma_f32_16x16x32_bf16 v[26:29],a[8:11],v[42:45],0
s_add_u32 m0,s57,0
R 62,1,5120
v_add_f32_dpp v115,v114,v114 quad_perm:[1,0,3,2] row_mask:0xf bank_mask:0xf
M 26,a[20:23],46
buffer_load_dword v114,v11,s[36:39],s73 offen offset:0 sc1
v_mfma_f32_16x16x32_bf16 v[22:25],a[4:7],v[42:45],0
s_add_u32 s56,s55,0
v_add_f32_dpp v115,v115,v115 quad_perm:[2,3,0,1] row_mask:0xf bank_mask:0xf
v_fma_f32 v30,-v78,v82,v30
M 22,a[16:19],46
s_add_u32 s58,s58,s61
R 66,1,6144
v_fma_f32 v31,-v78,v83,v31
v_mfma_f32_16x16x32_bf16 v[18:21],a[0:3],v[42:45],0
D 0,56,0
M 18,a[12:15],46
L 3
v_add_f32_dpp v118,v115,v115 row_half_mirror row_mask:0xf bank_mask:0xf
v_fma_f32 v32,-v78,v84,v32
M 18,a[24:27],50
s_mov_b32 s66,s65
R 70,1,7168
v_fma_f32 v33,-v78,v85,v33
M 18,a[36:39],54
s_add_u32 s65,s65,s62
v_mov_b32_dpp v115,v118 row_ror:8 row_mask:0xf bank_mask:0xf
v_fma_f32 v115,-v118,v118,v115
M 22,a[28:31],50
s_add_u32 s54,s54,s46
v_max_f32 v115,s87,v115
v_mul_f32 v118,v15,v118
M 22,a[40:43],54
s_add_u32 s73,s73,s64
R 42,1,8192
v_mul_f32 v30,v79,v30
M 26,a[32:35],50
v_rsq_f32 v116,v115
M 26,a[44:47],54
L 3
D 0,56,1024
M 26,a[56:59],58
R 46,1,9216
v_fma_f32 v118,v116,v16,v118
M 26,a[68:71],62
ds_write_b32 v12,v118 offset:0
M 22,a[52:55],58
v_mul_f32 v31,v79,v31
v_mul_f32 v32,v79,v32
M 22,a[64:67],62
R 50,1,10240
v_mul_f32 v33,v79,v33
M 18,a[48:51],58
v_fma_f32 v94,v30,v30,s88
v_fma_f32 v95,v31,v31,s88
M 18,a[60:63],62
L 4
v_fma_f32 v96,v32,v32,s88
v_fma_f32 v97,v33,v33,s88
M 18,a[72:75],66
R 54,1,11264
v_mul_f32 v94,v30,v94
M 18,a[84:87],70
v_exp_f32 v94,v94
M 22,a[76:79],66
v_mul_f32 v95,v31,v95
v_mul_f32 v96,v32,v96
M 22,a[88:91],70
R 58,1,12288
v_mul_f32 v97,v33,v97
M 26,a[80:83],66
D 0,56,2048
M 26,a[92:95],70
L 4
v_exp_f32 v95,v95
M 26,a[104:107],42
R 62,1,13312
v_fma_f32 v94,v94,s89,s89
M 26,a[116:119],46
v_exp_f32 v96,v96
)"
R"(M 22,a[100:103],42
v_exp_f32 v97,v97
M 22,a[112:115],46
R 66,1,14336
v_fma_f32 v95,v95,s89,s89
M 18,a[96:99],42
v_rcp_f32 v94,v94
M 18,a[108:111],46
L 3
v_rcp_f32 v95,v95
M 18,a[120:123],50
R 70,1,15360
v_fma_f32 v96,v96,s89,s89
M 18,a[132:135],54
s_waitcnt vmcnt(7)
D 0,56,3072
M 22,a[124:127],50
s_barrier
v_rcp_f32 v96,v96
M 22,a[136:139],54
s_add_u32 m0,s59,0
R 42,1,16384
v_fma_f32 v97,v97,s89,s89
M 26,a[128:131],50
s_add_u32 s56,s55,16384
v_rcp_f32 v97,v97
M 26,a[140:143],54
L 3
v_mul_f32 v30,v30,v94
v_mul_f32 v31,v31,v95
M 26,a[152:155],58
s_add_u32 s55,s55,s60
R 46,1,17408
v_mul_f32 v32,v32,v96
M 26,a[164:167],62
v_mul_f32 v33,v33,v97
v_cvt_pk_bf16_f32 v106,v30,v31
M 22,a[148:151],58
v_cvt_pk_bf16_f32 v107,v32,v33
v_fma_f32 v34,-v78,v86,v34
M 22,a[160:163],62
R 50,1,18432
v_fma_f32 v35,-v78,v87,v35
v_fma_f32 v36,-v78,v88,v36
M 18,a[144:147],58
buffer_load_dword v8,s[36:39],s58 offen offset:0 sc1 lds
M 18,a[156:159],62
L 3
v_fma_f32 v37,-v78,v89,v37
v_mul_f32 v34,v79,v34
M 18,a[168:171],66
s_add_u32 m0,s57,16384
R 54,1,19456
v_mul_f32 v35,v79,v35
M 18,a[180:183],70
s_sub_u32 s57,s84,s57
v_mul_f32 v36,v79,v36
v_mul_f32 v37,v79,v37
M 22,a[172:175],66
v_fma_f32 v98,v34,v34,s88
v_fma_f32 v99,v35,v35,s88
M 22,a[184:187],70
R 58,1,20480
v_fma_f32 v100,v36,v36,s88
M 26,a[176:179],66
D 0,56,0
M 26,a[188:191],70
L 3
v_fma_f32 v101,v37,v37,s88
v_mul_f32 v98,v34,v98
M 26,a[200:203],42
R 62,1,21504
v_mul_f32 v99,v35,v99
M 26,a[212:215],46
v_exp_f32 v98,v98
M 22,a[196:199],42
v_exp_f32 v99,v99
M 22,a[208:211],46
R 66,1,22528
v_mul_f32 v100,v36,v100
M 18,a[192:195],42
v_exp_f32 v100,v100
M 18,a[204:207],46
L 3
v_mul_f32 v101,v37,v101
v_fma_f32 v98,v98,s89,s89
M 18,a[216:219],50
R 70,1,23552
v_fma_f32 v99,v99,s89,s89
M 18,a[228:231],54
D 0,56,1024
M 22,a[220:223],50
v_exp_f32 v101,v101
M 22,a[232:235],54
R 42,1,24576
v_fma_f32 v100,v100,s89,s89
M 26,a[224:227],50
v_rcp_f32 v98,v98
M 26,a[236:239],54
L 3
v_rcp_f32 v99,v99
M 26,a[248:251],58
R 46,1,25600
v_fma_f32 v101,v101,s89,s89
M 26,v[132:135],62
v_rcp_f32 v100,v100
M 22,a[244:247],58
v_rcp_f32 v101,v101
M 22,v[128:131],62
R 50,1,26624
v_mul_f32 v34,v34,v98
M 18,a[240:243],58
v_mul_f32 v35,v35,v99
v_mul_f32 v36,v36,v100
M 18,a[252:255],62
L 3
v_mul_f32 v37,v37,v101
v_cvt_pk_bf16_f32 v108,v34,v35
M 18,v[136:139],66
R 54,1,27648
v_cvt_pk_bf16_f32 v109,v36,v37
M 18,v[148:151],70
D 0,56,2048
M 22,v[140:143],66
v_fma_f32 v38,-v78,v90,v38
v_fma_f32 v39,-v78,v91,v39
M 22,v[152:155],70
R 58,1,28672
v_fma_f32 v40,-v78,v92,v40
M 26,v[144:147],66
v_fma_f32 v41,-v78,v93,v41
v_mul_f32 v38,v79,v38
M 26,v[156:159],70
L 3
v_mul_f32 v39,v79,v39
v_mul_f32 v40,v79,v40
M 26,v[168:171],42
R 62,1,29696
v_mul_f32 v41,v79,v41
M 26,v[180:183],46
v_fma_f32 v102,v38,v38,s88
v_fma_f32 v103,v39,v39,s88
M 22,v[164:167],42
v_fma_f32 v104,v40,v40,s88
v_fma_f32 v105,v41,v41,s88
M 22,v[176:179],46
R 66,1,30720
v_mul_f32 v102,v38,v102
M 18,v[160:163],42
v_exp_f32 v102,v102
M 18,v[172:175],46
L 3
D 0,56,3072
M 18,v[184:187],50
s_nop 0
R 70,1,31744
v_mul_f32 v103,v39,v103
M 18,v[196:199],54
s_waitcnt vmcnt(4)
v_exp_f32 v103,v103
v_mul_f32 v104,v40,v104
M 22,v[188:191],50
s_barrier
buffer_store_dword v4,v3,s[36:39],s70 offen offset:0 sc0 sc1
M 22,v[200:203],54
s_mov_b32 s70,s69
R 42,2,0
ds_read_b32 v7,v6 offset:0
M 26,v[192:195],50
s_mov_b32 s69,s68
ds_read_b64 v[74:75],v13 offset:0
v_mul_f32 v105,v41,v105
M 26,v[204:207],54
L 5
v_exp_f32 v104,v104
M 26,v[216:219],58
s_add_u32 s68,s68,s63
R 46,2,1024
v_fma_f32 v102,v102,s89,s89
M 26,v[228:231],62
v_exp_f32 v105,v105
M 22,v[212:215],58
buffer_store_dwordx4 v[106:109],v0,s[36:39],s67 offen offset:0 sc0 sc1
v_fma_f32 v103,v103,s89,s89
M 22,v[224:227],62
L 2
R 50,2,2048
v_fma_f32 v104,v104,s89,s89
M 18,v[208:211],58
v_rcp_f32 v102,v102
M 18,v[220:223],62
s_nop 0
v_fma_f32 v105,v105,s89,s89
v_mul_f32 v38,v38,v102
M 18,v[232:235],66
R 54,2,3072
v_cmp_eq_u32 vcc,s53,v7
M 18,v[244:247],70
v_rcp_f32 v103,v103
M 22,v[236:239],66
s_cmp_eq_u32 vcc_lo,-1
v_rcp_f32 v104,v104
v_rcp_f32 v105,v105
M 22,v[248:251],70
R 58,2,4096
v_mul_f32 v39,v39,v103
v_mul_f32 v40,v40,v104
v_mul_f32 v41,v41,v105
v_cvt_pk_bf16_f32 v110,v38,v39
v_cvt_pk_bf16_f32 v111,v40,v41
M 26,v[240:243],66
L 3
buffer_store_dwordx2 v[110:111],v10,s[36:39],s67 offen offset:0 sc0 sc1
M 26,v[252:255],70
s_cbranch_scc0 Lg3_K1_%=
Lg3_F1w0_%=:
v_mfma_f32_16x16x32_bf16 v[38:41],a[8:11],v[42:45],0
s_add_u32 m0,s57,0
R 62,2,5120
v_add_f32_dpp v115,v114,v114 quad_perm:[1,0,3,2] row_mask:0xf bank_mask:0xf
M 38,a[20:23],46
buffer_load_dword v114,v11,s[36:39],s73 offen offset:0 sc1
v_mfma_f32_16x16x32_bf16 v[34:37],a[4:7],v[42:45],0
s_add_u32 s56,s55,0
v_add_f32_dpp v115,v115,v115 quad_perm:[2,3,0,1] row_mask:0xf bank_mask:0xf
v_fma_f32 v18,-v74,v82,v18
M 34,a[16:19],46
s_add_u32 s58,s58,s61
R 66,2,6144
v_fma_f32 v19,-v74,v83,v19
v_mfma_f32_16x16x32_bf16 v[30:33],a[0:3],v[42:45],0
D 0,56,0
M 30,a[12:15],46
L 3
v_add_f32_dpp v118,v115,v115 row_half_mirror row_mask:0xf bank_mask:0xf
v_fma_f32 v20,-v74,v84,v20
M 30,a[24:27],50
s_mov_b32 s67,s65
R 70,2,7168
v_fma_f32 v21,-v74,v85,v21
M 30,a[36:39],54
s_add_u32 s65,s65,s62
v_mov_b32_dpp v115,v118 row_ror:8 row_mask:0xf bank_mask:0xf
v_fma_f32 v115,-v118,v118,v115
M 34,a[28:31],50
s_add_u32 s54,s54,s46
v_max_f32 v115,s87,v115
v_mul_f32 v118,v15,v118
M 34,a[40:43],54
s_add_u32 s73,s73,s64
R 42,2,8192
v_mul_f32 v18,v75,v18
M 38,a[32:35],50
v_rsq_f32 v116,v115
M 38,a[44:47],54
L 3
D 0,56,1024
M 38,a[56:59],58
R 46,2,9216
v_fma_f32 v118,v116,v16,v118
M 38,a[68:71],62
ds_write_b32 v12,v118 offset:2048
M 34,a[52:55],58
v_mul_f32 v19,v75,v19
v_mul_f32 v20,v75,v20
M 34,a[64:67],62
R 50,2,10240
v_mul_f32 v21,v75,v21
M 30,a[48:51],58
v_fma_f32 v94,v18,v18,s88
v_fma_f32 v95,v19,v19,s88
M 30,a[60:63],62
L 4
v_fma_f32 v96,v20,v20,s88
v_fma_f32 v97,v21,v21,s88
M 30,a[72:75],66
R 54,2,11264
v_mul_f32 v94,v18,v94
M 30,a[84:87],70
v_exp_f32 v94,v94
M 34,a[76:79],66
v_mul_f32 v95,v19,v95
v_mul_f32 v96,v20,v96
M 34,a[88:91],70
R 58,2,12288
v_mul_f32 v97,v21,v97
M 38,a[80:83],66
D 0,56,2048
M 38,a[92:95],70
L 4
v_exp_f32 v95,v95
M 38,a[104:107],42
R 62,2,13312
v_fma_f32 v94,v94,s89,s89
M 38,a[116:119],46
v_exp_f32 v96,v96
M 34,a[100:103],42
v_exp_f32 v97,v97
M 34,a[112:115],46
R 66,2,14336
v_fma_f32 v95,v95,s89,s89
M 30,a[96:99],42
v_rcp_f32 v94,v94
M 30,a[108:111],46
L 3
v_rcp_f32 v95,v95
M 30,a[120:123],50
R 70,2,15360
v_fma_f32 v96,v96,s89,s89
M 30,a[132:135],54
s_waitcnt vmcnt(7)
D 0,56,3072
M 34,a[124:127],50
s_barrier
v_rcp_f32 v96,v96
M 34,a[136:139],54
s_add_u32 m0,s59,1024
R 42,2,16384
v_fma_f32 v97,v97,s89,s89
M 38,a[128:131],50
s_add_u32 s56,s55,16384
v_rcp_f32 v97,v97
M 38,a[140:143],54
L 3
v_mul_f32 v18,v18,v94
v_mul_f32 v19,v19,v95
M 38,a[152:155],58
s_add_u32 s55,s55,s60
R 46,2,17408
v_mul_f32 v20,v20,v96
M 38,a[164:167],62
v_mul_f32 v21,v21,v97
v_cvt_pk_bf16_f32 v106,v18,v19
M 34,a[148:151],58
v_cvt_pk_bf16_f32 v107,v20,v21
v_fma_f32 v22,-v74,v86,v22
M 34,a[160:163],62
R 50,2,18432
v_fma_f32 v23,-v74,v87,v23
v_fma_f32 v24,-v74,v88,v24
M 30,a[144:147],58
buffer_load_dword v8,s[36:39],s58 offen offset:0 sc1 lds
M 30,a[156:159],62
L 3
v_fma_f32 v25,-v74,v89,v25
v_mul_f32 v22,v75,v22
M 30,a[168:171],66
s_add_u32 m0,s57,16384
R 54,2,19456
v_mul_f32 v23,v75,v23
M 30,a[180:183],70
s_sub_u32 s57,s84,s57
v_mul_f32 v24,v75,v24
v_mul_f32 v25,v75,v25
M 34,a[172:175],66
v_fma_f32 v98,v22,v22,s88
v_fma_f32 v99,v23,v23,s88
M 34,a[184:187],70
R 58,2,20480
v_fma_f32 v100,v24,v24,s88
M 38,a[176:179],66
D 0,56,0
M 38,a[188:191],70
L 3
v_fma_f32 v101,v25,v25,s88
v_mul_f32 v98,v22,v98
M 38,a[200:203],42
R 62,2,21504
v_mul_f32 v99,v23,v99
M 38,a[212:215],46
v_exp_f32 v98,v98
M 34,a[196:199],42
v_exp_f32 v99,v99
M 34,a[208:211],46
R 66,2,22528
v_mul_f32 v100,v24,v100
M 30,a[192:195],42
v_exp_f32 v100,v100
M 30,a[204:207],46
L 3
v_mul_f32 v101,v25,v101
v_fma_f32 v98,v98,s89,s89
M 30,a[216:219],50
R 70,2,23552
v_fma_f32 v99,v99,s89,s89
M 30,a[228:231],54
D 0,56,1024
M 34,a[220:223],50
v_exp_f32 v101,v101
M 34,a[232:235],54
R 42,2,24576
v_fma_f32 v100,v100,s89,s89
M 38,a[224:227],50
v_rcp_f32 v98,v98
M 38,a[236:239],54
L 3
v_rcp_f32 v99,v99
M 38,a[248:251],58
R 46,2,25600
v_fma_f32 v101,v101,s89,s89
M 38,v[132:135],62
v_rcp_f32 v100,v100
M 34,a[244:247],58
v_rcp_f32 v101,v101
M 34,v[128:131],62
R 50,2,26624
v_mul_f32 v22,v22,v98
M 30,a[240:243],58
v_mul_f32 v23,v23,v99
v_mul_f32 v24,v24,v100
M 30,a[252:255],62
L 3
v_mul_f32 v25,v25,v101
v_cvt_pk_bf16_f32 v108,v22,v23
M 30,v[136:139],66
R 54,2,27648
v_cvt_pk_bf16_f32 v109,v24,v25
M 30,v[148:151],70
D 0,56,2048
M 34,v[140:143],66
v_fma_f32 v26,-v74,v90,v26
v_fma_f32 v27,-v74,v91,v27
M 34,v[152:155],70
R 58,2,28672
v_fma_f32 v28,-v74,v92,v28
M 38,v[144:147],66
v_fma_f32 v29,-v74,v93,v29
v_mul_f32 v26,v75,v26
M 38,v[156:159],70
L 3
v_mul_f32 v27,v75,v27
v_mul_f32 v28,v75,v28
M 38,v[168:171],42
R 62,2,29696
v_mul_f32 v29,v75,v29
M 38,v[180:183],46
v_fma_f32 v102,v26,v26,s88
v_fma_f32 v103,v27,v27,s88
M 34,v[164:167],42
v_fma_f32 v104,v28,v28,s88
v_fma_f32 v105,v29,v29,s88
M 34,v[176:179],46
R 66,2,30720
v_mul_f32 v102,v26,v102
M 30,v[160:163],42
v_exp_f32 v102,v102
M 30,v[172:175],46
L 3
D 0,56,3072
M 30,v[184:187],50
s_nop 0
R 70,2,31744
v_mul_f32 v103,v27,v103
M 30,v[196:199],54
s_waitcnt vmcnt(4)
v_exp_f32 v103,v103
v_mul_f32 v104,v28,v104
M 34,v[188:191],50
s_barrier
buffer_store_dword v4,v3,s[36:39],s70 offen offset:0 sc0 sc1
M 34,v[200:203],54
s_mov_b32 s70,s69
R 42,1,0
ds_read_b32 v7,v6 offset:1024
M 38,v[192:195],50
s_mov_b32 s69,s68
ds_read_b64 v[78:79],v13 offset:2048
v_mul_f32 v105,v29,v105
M 38,v[204:207],54
L 5
v_exp_f32 v104,v104
M 38,v[216:219],58
s_add_u32 s68,s68,s63
R 46,1,1024
v_fma_f32 v102,v102,s89,s89
M 38,v[228:231],62
v_exp_f32 v105,v105
M 34,v[212:215],58
buffer_store_dwordx4 v[106:109],v0,s[36:39],s66 offen offset:0 sc0 sc1
v_fma_f32 v103,v103,s89,s89
M 34,v[224:227],62
L 2
R 50,1,2048
v_fma_f32 v104,v104,s89,s89
M 30,v[208:211],58
v_rcp_f32 v102,v102
M 30,v[220:223],62
s_nop 0
v_fma_f32 v105,v105,s89,s89
v_mul_f32 v26,v26,v102
M 30,v[232:235],66
R 54,1,3072
v_cmp_eq_u32 vcc,s53,v7
M 30,v[244:247],70
v_rcp_f32 v103,v103
M 34,v[236:239],66
s_cmp_eq_u32 vcc_lo,-1
v_rcp_f32 v104,v104
v_rcp_f32 v105,v105
M 34,v[248:251],70
)"
R"(R 58,1,4096
v_mul_f32 v27,v27,v103
v_mul_f32 v28,v28,v104
v_mul_f32 v29,v29,v105
v_cvt_pk_bf16_f32 v110,v26,v27
v_cvt_pk_bf16_f32 v111,v28,v29
M 38,v[240:243],66
L 3
buffer_store_dwordx2 v[110:111],v10,s[36:39],s66 offen offset:0 sc0 sc1
M 38,v[252:255],70
s_cbranch_scc1 Lg3_F0w0_%=
s_branch Lg3_K0_%=
Lg3_F0w1_%=:
v_mfma_f32_16x16x32_bf16 v[26:29],a[8:11],v[42:45],0
s_add_u32 m0,s57,0
R 62,1,5120
v_add_f32_dpp v115,v114,v114 quad_perm:[1,0,3,2] row_mask:0xf bank_mask:0xf
M 26,a[20:23],46
buffer_load_dword v114,v11,s[36:39],s73 offen offset:0 sc1
v_mfma_f32_16x16x32_bf16 v[22:25],a[4:7],v[42:45],0
s_add_u32 s56,s55,0
v_add_f32_dpp v115,v115,v115 quad_perm:[2,3,0,1] row_mask:0xf bank_mask:0xf
v_fma_f32 v30,-v78,v82,v30
M 22,a[16:19],46
s_add_u32 s58,s58,s61
R 66,1,6144
v_fma_f32 v31,-v78,v83,v31
v_mfma_f32_16x16x32_bf16 v[18:21],a[0:3],v[42:45],0
D 0,56,0
M 18,a[12:15],46
L 3
v_add_f32_dpp v118,v115,v115 row_half_mirror row_mask:0xf bank_mask:0xf
v_fma_f32 v32,-v78,v84,v32
M 18,a[24:27],50
s_mov_b32 s66,s65
R 70,1,7168
v_fma_f32 v33,-v78,v85,v33
M 18,a[36:39],54
s_add_u32 s65,s65,s62
v_mov_b32_dpp v115,v118 row_ror:8 row_mask:0xf bank_mask:0xf
v_fma_f32 v115,-v118,v118,v115
M 22,a[28:31],50
s_add_u32 s54,s54,s46
v_max_f32 v115,s87,v115
v_mul_f32 v118,v15,v118
M 22,a[40:43],54
s_add_u32 s73,s73,s64
R 42,1,8192
v_mul_f32 v30,v79,v30
M 26,a[32:35],50
v_rsq_f32 v116,v115
M 26,a[44:47],54
L 3
D 0,56,1024
M 26,a[56:59],58
R 46,1,9216
v_fma_f32 v118,v116,v16,v118
M 26,a[68:71],62
ds_write_b32 v12,v118 offset:0
M 22,a[52:55],58
v_mul_f32 v31,v79,v31
v_mul_f32 v32,v79,v32
M 22,a[64:67],62
R 50,1,10240
v_mul_f32 v33,v79,v33
M 18,a[48:51],58
v_fma_f32 v94,v30,v30,s88
v_fma_f32 v95,v31,v31,s88
M 18,a[60:63],62
L 4
v_fma_f32 v96,v32,v32,s88
v_fma_f32 v97,v33,v33,s88
M 18,a[72:75],66
R 54,1,11264
v_mul_f32 v94,v30,v94
M 18,a[84:87],70
v_exp_f32 v94,v94
M 22,a[76:79],66
v_mul_f32 v95,v31,v95
v_mul_f32 v96,v32,v96
M 22,a[88:91],70
R 58,1,12288
v_mul_f32 v97,v33,v97
M 26,a[80:83],66
D 0,56,2048
M 26,a[92:95],70
L 4
v_exp_f32 v95,v95
M 26,a[104:107],42
R 62,1,13312
v_fma_f32 v94,v94,s89,s89
M 26,a[116:119],46
v_exp_f32 v96,v96
M 22,a[100:103],42
v_exp_f32 v97,v97
M 22,a[112:115],46
R 66,1,14336
v_fma_f32 v95,v95,s89,s89
M 18,a[96:99],42
v_rcp_f32 v94,v94
M 18,a[108:111],46
s_waitcnt vmcnt(7) lgkmcnt(3)
v_rcp_f32 v95,v95
M 18,a[120:123],50
s_barrier
R 70,1,15360
v_fma_f32 v96,v96,s89,s89
M 18,a[132:135],54
D 0,56,3072
M 22,a[124:127],50
s_add_u32 m0,s59,0
v_rcp_f32 v96,v96
M 22,a[136:139],54
s_add_u32 s56,s55,16384
R 42,1,16384
v_fma_f32 v97,v97,s89,s89
M 26,a[128:131],50
s_add_u32 s55,s55,s60
v_rcp_f32 v97,v97
M 26,a[140:143],54
L 3
v_mul_f32 v30,v30,v94
v_mul_f32 v31,v31,v95
M 26,a[152:155],58
R 46,1,17408
v_mul_f32 v32,v32,v96
M 26,a[164:167],62
v_mul_f32 v33,v33,v97
v_cvt_pk_bf16_f32 v106,v30,v31
M 22,a[148:151],58
v_cvt_pk_bf16_f32 v107,v32,v33
v_fma_f32 v34,-v78,v86,v34
M 22,a[160:163],62
R 50,1,18432
v_fma_f32 v35,-v78,v87,v35
v_fma_f32 v36,-v78,v88,v36
M 18,a[144:147],58
buffer_load_dword v8,s[36:39],s58 offen offset:0 sc1 lds
M 18,a[156:159],62
L 3
v_fma_f32 v37,-v78,v89,v37
v_mul_f32 v34,v79,v34
M 18,a[168:171],66
s_add_u32 m0,s57,16384
R 54,1,19456
v_mul_f32 v35,v79,v35
M 18,a[180:183],70
s_sub_u32 s57,s84,s57
v_mul_f32 v36,v79,v36
v_mul_f32 v37,v79,v37
M 22,a[172:175],66
v_fma_f32 v98,v34,v34,s88
v_fma_f32 v99,v35,v35,s88
M 22,a[184:187],70
R 58,1,20480
v_fma_f32 v100,v36,v36,s88
M 26,a[176:179],66
D 0,56,0
M 26,a[188:191],70
L 3
v_fma_f32 v101,v37,v37,s88
v_mul_f32 v98,v34,v98
M 26,a[200:203],42
R 62,1,21504
v_mul_f32 v99,v35,v99
M 26,a[212:215],46
v_exp_f32 v98,v98
M 22,a[196:199],42
v_exp_f32 v99,v99
M 22,a[208:211],46
R 66,1,22528
v_mul_f32 v100,v36,v100
M 18,a[192:195],42
v_exp_f32 v100,v100
M 18,a[204:207],46
L 3
v_mul_f32 v101,v37,v101
v_fma_f32 v98,v98,s89,s89
M 18,a[216:219],50
R 70,1,23552
v_fma_f32 v99,v99,s89,s89
M 18,a[228:231],54
D 0,56,1024
M 22,a[220:223],50
v_exp_f32 v101,v101
M 22,a[232:235],54
R 42,1,24576
v_fma_f32 v100,v100,s89,s89
M 26,a[224:227],50
v_rcp_f32 v98,v98
M 26,a[236:239],54
L 3
v_rcp_f32 v99,v99
M 26,a[248:251],58
R 46,1,25600
v_fma_f32 v101,v101,s89,s89
M 26,v[132:135],62
v_rcp_f32 v100,v100
M 22,a[244:247],58
v_rcp_f32 v101,v101
M 22,v[128:131],62
R 50,1,26624
v_mul_f32 v34,v34,v98
M 18,a[240:243],58
v_mul_f32 v35,v35,v99
v_mul_f32 v36,v36,v100
M 18,a[252:255],62
L 3
v_mul_f32 v37,v37,v101
v_cvt_pk_bf16_f32 v108,v34,v35
M 18,v[136:139],66
R 54,1,27648
v_cvt_pk_bf16_f32 v109,v36,v37
M 18,v[148:151],70
D 0,56,2048
M 22,v[140:143],66
v_fma_f32 v38,-v78,v90,v38
v_fma_f32 v39,-v78,v91,v39
M 22,v[152:155],70
R 58,1,28672
v_fma_f32 v40,-v78,v92,v40
M 26,v[144:147],66
v_fma_f32 v41,-v78,v93,v41
v_mul_f32 v38,v79,v38
M 26,v[156:159],70
L 3
v_mul_f32 v39,v79,v39
v_mul_f32 v40,v79,v40
M 26,v[168:171],42
R 62,1,29696
v_mul_f32 v41,v79,v41
M 26,v[180:183],46
v_fma_f32 v102,v38,v38,s88
v_fma_f32 v103,v39,v39,s88
M 22,v[164:167],42
v_fma_f32 v104,v40,v40,s88
v_fma_f32 v105,v41,v41,s88
M 22,v[176:179],46
R 66,1,30720
v_mul_f32 v102,v38,v102
M 18,v[160:163],42
s_nop 0
v_exp_f32 v102,v102
M 18,v[172:175],46
s_waitcnt vmcnt(3) lgkmcnt(3)
D 0,56,3072
M 18,v[184:187],50
s_barrier
R 70,1,31744
ds_read_b32 v7,v6 offset:0
M 18,v[196:199],54
buffer_store_dword v4,v3,s[36:39],s70 offen offset:0 sc0 sc1
v_mul_f32 v103,v39,v103
M 22,v[188:191],50
s_mov_b32 s70,s69
ds_read_b64 v[74:75],v13 offset:0
v_mul_f32 v104,v40,v104
M 22,v[200:203],54
s_mov_b32 s69,s68
R 42,2,0
v_mul_f32 v105,v41,v105
M 26,v[192:195],50
s_add_u32 s68,s68,s63
v_exp_f32 v103,v103
M 26,v[204:207],54
L 5
v_exp_f32 v104,v104
M 26,v[216:219],58
L 2
R 46,2,1024
v_fma_f32 v102,v102,s89,s89
M 26,v[228:231],62
v_exp_f32 v105,v105
M 22,v[212:215],58
buffer_store_dwordx4 v[106:109],v0,s[36:39],s67 offen offset:0 sc0 sc1
v_fma_f32 v103,v103,s89,s89
M 22,v[224:227],62
R 50,2,2048
v_fma_f32 v104,v104,s89,s89
M 18,v[208:211],58
v_rcp_f32 v102,v102
M 18,v[220:223],62
s_nop 0
v_fma_f32 v105,v105,s89,s89
v_mul_f32 v38,v38,v102
M 18,v[232:235],66
R 54,2,3072
v_cmp_eq_u32 vcc,s53,v7
M 18,v[244:247],70
v_rcp_f32 v103,v103
M 22,v[236:239],66
s_cmp_eq_u32 vcc_lo,-1
v_rcp_f32 v104,v104
v_rcp_f32 v105,v105
M 22,v[248:251],70
R 58,2,4096
v_mul_f32 v39,v39,v103
v_mul_f32 v40,v40,v104
v_mul_f32 v41,v41,v105
v_cvt_pk_bf16_f32 v110,v38,v39
v_cvt_pk_bf16_f32 v111,v40,v41
M 26,v[240:243],66
L 3
buffer_store_dwordx2 v[110:111],v10,s[36:39],s67 offen offset:0 sc0 sc1
M 26,v[252:255],70
s_cbranch_scc0 Lg3_K1_%=
Lg3_F1w1_%=:
v_mfma_f32_16x16x32_bf16 v[38:41],a[8:11],v[42:45],0
s_add_u32 m0,s57,0
R 62,2,5120
v_add_f32_dpp v115,v114,v114 quad_perm:[1,0,3,2] row_mask:0xf bank_mask:0xf
M 38,a[20:23],46
buffer_load_dword v114,v11,s[36:39],s73 offen offset:0 sc1
v_mfma_f32_16x16x32_bf16 v[34:37],a[4:7],v[42:45],0
s_add_u32 s56,s55,0
v_add_f32_dpp v115,v115,v115 quad_perm:[2,3,0,1] row_mask:0xf bank_mask:0xf
v_fma_f32 v18,-v74,v82,v18
M 34,a[16:19],46
s_add_u32 s58,s58,s61
R 66,2,6144
v_fma_f32 v19,-v74,v83,v19
v_mfma_f32_16x16x32_bf16 v[30:33],a[0:3],v[42:45],0
D 0,56,0
M 30,a[12:15],46
L 3
v_add_f32_dpp v118,v115,v115 row_half_mirror row_mask:0xf bank_mask:0xf
v_fma_f32 v20,-v74,v84,v20
M 30,a[24:27],50
s_mov_b32 s67,s65
R 70,2,7168
v_fma_f32 v21,-v74,v85,v21
M 30,a[36:39],54
s_add_u32 s65,s65,s62
v_mov_b32_dpp v115,v118 row_ror:8 row_mask:0xf bank_mask:0xf
v_fma_f32 v115,-v118,v118,v115
M 34,a[28:31],50
s_add_u32 s54,s54,s46
v_max_f32 v115,s87,v115
v_mul_f32 v118,v15,v118
M 34,a[40:43],54
s_add_u32 s73,s73,s64
R 42,2,8192
v_mul_f32 v18,v75,v18
M 38,a[32:35],50
v_rsq_f32 v116,v115
M 38,a[44:47],54
L 3
D 0,56,1024
M 38,a[56:59],58
R 46,2,9216
v_fma_f32 v118,v116,v16,v118
M 38,a[68:71],62
ds_write_b32 v12,v118 offset:2048
M 34,a[52:55],58
v_mul_f32 v19,v75,v19
v_mul_f32 v20,v75,v20
M 34,a[64:67],62
R 50,2,10240
v_mul_f32 v21,v75,v21
M 30,a[48:51],58
v_fma_f32 v94,v18,v18,s88
v_fma_f32 v95,v19,v19,s88
M 30,a[60:63],62
L 4
v_fma_f32 v96,v20,v20,s88
v_fma_f32 v97,v21,v21,s88
M 30,a[72:75],66
R 54,2,11264
v_mul_f32 v94,v18,v94
M 30,a[84:87],70
v_exp_f32 v94,v94
M 34,a[76:79],66
v_mul_f32 v95,v19,v95
v_mul_f32 v96,v20,v96
M 34,a[88:91],70
R 58,2,12288
v_mul_f32 v97,v21,v97
M 38,a[80:83],66
D 0,56,2048
M 38,a[92:95],70
L 4
v_exp_f32 v95,v95
M 38,a[104:107],42
R 62,2,13312
v_fma_f32 v94,v94,s89,s89
M 38,a[116:119],46
v_exp_f32 v96,v96
M 34,a[100:103],42
v_exp_f32 v97,v97
M 34,a[112:115],46
R 66,2,14336
v_fma_f32 v95,v95,s89,s89
M 30,a[96:99],42
v_rcp_f32 v94,v94
M 30,a[108:111],46
s_waitcnt vmcnt(7) lgkmcnt(3)
v_rcp_f32 v95,v95
M 30,a[120:123],50
s_barrier
R 70,2,15360
v_fma_f32 v96,v96,s89,s89
M 30,a[132:135],54
D 0,56,3072
M 34,a[124:127],50
s_add_u32 m0,s59,1024
v_rcp_f32 v96,v96
M 34,a[136:139],54
s_add_u32 s56,s55,16384
R 42,2,16384
v_fma_f32 v97,v97,s89,s89
M 38,a[128:131],50
s_add_u32 s55,s55,s60
v_rcp_f32 v97,v97
M 38,a[140:143],54
L 3
v_mul_f32 v18,v18,v94
v_mul_f32 v19,v19,v95
M 38,a[152:155],58
R 46,2,17408
v_mul_f32 v20,v20,v96
M 38,a[164:167],62
v_mul_f32 v21,v21,v97
v_cvt_pk_bf16_f32 v106,v18,v19
M 34,a[148:151],58
v_cvt_pk_bf16_f32 v107,v20,v21
v_fma_f32 v22,-v74,v86,v22
M 34,a[160:163],62
R 50,2,18432
v_fma_f32 v23,-v74,v87,v23
v_fma_f32 v24,-v74,v88,v24
M 30,a[144:147],58
buffer_load_dword v8,s[36:39],s58 offen offset:0 sc1 lds
M 30,a[156:159],62
L 3
v_fma_f32 v25,-v74,v89,v25
v_mul_f32 v22,v75,v22
M 30,a[168:171],66
s_add_u32 m0,s57,16384
R 54,2,19456
v_mul_f32 v23,v75,v23
M 30,a[180:183],70
s_sub_u32 s57,s84,s57
v_mul_f32 v24,v75,v24
v_mul_f32 v25,v75,v25
M 34,a[172:175],66
v_fma_f32 v98,v22,v22,s88
v_fma_f32 v99,v23,v23,s88
M 34,a[184:187],70
R 58,2,20480
v_fma_f32 v100,v24,v24,s88
M 38,a[176:179],66
D 0,56,0
M 38,a[188:191],70
L 3
v_fma_f32 v101,v25,v25,s88
v_mul_f32 v98,v22,v98
M 38,a[200:203],42
R 62,2,21504
v_mul_f32 v99,v23,v99
M 38,a[212:215],46
v_exp_f32 v98,v98
M 34,a[196:199],42
v_exp_f32 v99,v99
M 34,a[208:211],46
R 66,2,22528
v_mul_f32 v100,v24,v100
M 30,a[192:195],42
v_exp_f32 v100,v100
M 30,a[204:207],46
L 3
v_mul_f32 v101,v25,v101
v_fma_f32 v98,v98,s89,s89
M 30,a[216:219],50
R 70,2,23552
v_fma_f32 v99,v99,s89,s89
M 30,a[228:231],54
D 0,56,1024
M 34,a[220:223],50
v_exp_f32 v101,v101
M 34,a[232:235],54
R 42,2,24576
v_fma_f32 v100,v100,s89,s89
M 38,a[224:227],50
v_rcp_f32 v98,v98
M 38,a[236:239],54
L 3
v_rcp_f32 v99,v99
M 38,a[248:251],58
R 46,2,25600
v_fma_f32 v101,v101,s89,s89
M 38,v[132:135],62
v_rcp_f32 v100,v100
M 34,a[244:247],58
v_rcp_f32 v101,v101
M 34,v[128:131],62
R 50,2,26624
v_mul_f32 v22,v22,v98
M 30,a[240:243],58
v_mul_f32 v23,v23,v99
v_mul_f32 v24,v24,v100
)"
R"(M 30,a[252:255],62
L 3
v_mul_f32 v25,v25,v101
v_cvt_pk_bf16_f32 v108,v22,v23
M 30,v[136:139],66
R 54,2,27648
v_cvt_pk_bf16_f32 v109,v24,v25
M 30,v[148:151],70
D 0,56,2048
M 34,v[140:143],66
v_fma_f32 v26,-v74,v90,v26
v_fma_f32 v27,-v74,v91,v27
M 34,v[152:155],70
R 58,2,28672
v_fma_f32 v28,-v74,v92,v28
M 38,v[144:147],66
v_fma_f32 v29,-v74,v93,v29
v_mul_f32 v26,v75,v26
M 38,v[156:159],70
L 3
v_mul_f32 v27,v75,v27
v_mul_f32 v28,v75,v28
M 38,v[168:171],42
R 62,2,29696
v_mul_f32 v29,v75,v29
M 38,v[180:183],46
v_fma_f32 v102,v26,v26,s88
v_fma_f32 v103,v27,v27,s88
M 34,v[164:167],42
v_fma_f32 v104,v28,v28,s88
v_fma_f32 v105,v29,v29,s88
M 34,v[176:179],46
R 66,2,30720
v_mul_f32 v102,v26,v102
M 30,v[160:163],42
s_nop 0
v_exp_f32 v102,v102
M 30,v[172:175],46
s_waitcnt vmcnt(3) lgkmcnt(3)
D 0,56,3072
M 30,v[184:187],50
s_barrier
R 70,2,31744
ds_read_b32 v7,v6 offset:1024
M 30,v[196:199],54
buffer_store_dword v4,v3,s[36:39],s70 offen offset:0 sc0 sc1
v_mul_f32 v103,v27,v103
M 34,v[188:191],50
s_mov_b32 s70,s69
ds_read_b64 v[78:79],v13 offset:2048
v_mul_f32 v104,v28,v104
M 34,v[200:203],54
s_mov_b32 s69,s68
R 42,1,0
v_mul_f32 v105,v29,v105
M 38,v[192:195],50
s_add_u32 s68,s68,s63
v_exp_f32 v103,v103
M 38,v[204:207],54
L 5
v_exp_f32 v104,v104
M 38,v[216:219],58
L 2
R 46,1,1024
v_fma_f32 v102,v102,s89,s89
M 38,v[228:231],62
v_exp_f32 v105,v105
M 34,v[212:215],58
buffer_store_dwordx4 v[106:109],v0,s[36:39],s66 offen offset:0 sc0 sc1
v_fma_f32 v103,v103,s89,s89
M 34,v[224:227],62
R 50,1,2048
v_fma_f32 v104,v104,s89,s89
M 30,v[208:211],58
v_rcp_f32 v102,v102
M 30,v[220:223],62
s_nop 0
v_fma_f32 v105,v105,s89,s89
v_mul_f32 v26,v26,v102
M 30,v[232:235],66
R 54,1,3072
v_cmp_eq_u32 vcc,s53,v7
M 30,v[244:247],70
v_rcp_f32 v103,v103
M 34,v[236:239],66
s_cmp_eq_u32 vcc_lo,-1
v_rcp_f32 v104,v104
v_rcp_f32 v105,v105
M 34,v[248:251],70
R 58,1,4096
v_mul_f32 v27,v27,v103
v_mul_f32 v28,v28,v104
v_mul_f32 v29,v29,v105
v_cvt_pk_bf16_f32 v110,v26,v27
v_cvt_pk_bf16_f32 v111,v28,v29
M 38,v[240:243],66
L 3
buffer_store_dwordx2 v[110:111],v10,s[36:39],s66 offen offset:0 sc0 sc1
M 38,v[252:255],70
s_cbranch_scc1 Lg3_F0w1_%=
s_branch Lg3_K0_%=
Lg3_F0w2_%=:
v_mfma_f32_16x16x32_bf16 v[26:29],a[8:11],v[42:45],0
s_add_u32 m0,s57,0
R 62,1,5120
v_add_f32_dpp v115,v114,v114 quad_perm:[1,0,3,2] row_mask:0xf bank_mask:0xf
M 26,a[20:23],46
buffer_load_dword v114,v11,s[36:39],s73 offen offset:0 sc1
v_mfma_f32_16x16x32_bf16 v[22:25],a[4:7],v[42:45],0
s_add_u32 s56,s55,0
v_add_f32_dpp v115,v115,v115 quad_perm:[2,3,0,1] row_mask:0xf bank_mask:0xf
v_fma_f32 v30,-v78,v82,v30
M 22,a[16:19],46
s_add_u32 s58,s58,s61
R 66,1,6144
v_fma_f32 v31,-v78,v83,v31
v_mfma_f32_16x16x32_bf16 v[18:21],a[0:3],v[42:45],0
D 0,56,0
M 18,a[12:15],46
L 3
v_add_f32_dpp v118,v115,v115 row_half_mirror row_mask:0xf bank_mask:0xf
v_fma_f32 v32,-v78,v84,v32
M 18,a[24:27],50
s_mov_b32 s66,s65
R 70,1,7168
v_fma_f32 v33,-v78,v85,v33
M 18,a[36:39],54
s_add_u32 s65,s65,s62
v_mov_b32_dpp v115,v118 row_ror:8 row_mask:0xf bank_mask:0xf
v_fma_f32 v115,-v118,v118,v115
M 22,a[28:31],50
s_add_u32 s54,s54,s46
v_max_f32 v115,s87,v115
v_mul_f32 v118,v15,v118
M 22,a[40:43],54
s_add_u32 s73,s73,s64
R 42,1,8192
v_mul_f32 v30,v79,v30
M 26,a[32:35],50
v_rsq_f32 v116,v115
M 26,a[44:47],54
L 3
D 0,56,1024
M 26,a[56:59],58
R 46,1,9216
v_fma_f32 v118,v116,v16,v118
M 26,a[68:71],62
ds_write_b32 v12,v118 offset:0
M 22,a[52:55],58
v_mul_f32 v31,v79,v31
v_mul_f32 v32,v79,v32
M 22,a[64:67],62
R 50,1,10240
v_mul_f32 v33,v79,v33
M 18,a[48:51],58
v_fma_f32 v94,v30,v30,s88
v_fma_f32 v95,v31,v31,s88
M 18,a[60:63],62
L 4
v_fma_f32 v96,v32,v32,s88
v_fma_f32 v97,v33,v33,s88
M 18,a[72:75],66
R 54,1,11264
v_mul_f32 v94,v30,v94
M 18,a[84:87],70
v_exp_f32 v94,v94
M 22,a[76:79],66
v_mul_f32 v95,v31,v95
v_mul_f32 v96,v32,v96
M 22,a[88:91],70
R 58,1,12288
v_mul_f32 v97,v33,v97
M 26,a[80:83],66
D 0,56,2048
M 26,a[92:95],70
L 4
v_exp_f32 v95,v95
M 26,a[104:107],42
R 62,1,13312
v_fma_f32 v94,v94,s89,s89
M 26,a[116:119],46
v_exp_f32 v96,v96
M 22,a[100:103],42
v_exp_f32 v97,v97
M 22,a[112:115],46
s_waitcnt vmcnt(6)
R 66,1,14336
v_fma_f32 v95,v95,s89,s89
M 18,a[96:99],42
s_barrier
v_rcp_f32 v94,v94
M 18,a[108:111],46
L 3
v_rcp_f32 v95,v95
M 18,a[120:123],50
R 70,1,15360
v_fma_f32 v96,v96,s89,s89
M 18,a[132:135],54
D 0,56,3072
M 22,a[124:127],50
s_add_u32 m0,s59,0
v_rcp_f32 v96,v96
M 22,a[136:139],54
s_add_u32 s56,s55,16384
R 42,1,16384
v_fma_f32 v97,v97,s89,s89
M 26,a[128:131],50
s_add_u32 s55,s55,s60
v_rcp_f32 v97,v97
M 26,a[140:143],54
L 3
v_mul_f32 v30,v30,v94
v_mul_f32 v31,v31,v95
M 26,a[152:155],58
R 46,1,17408
v_mul_f32 v32,v32,v96
M 26,a[164:167],62
v_mul_f32 v33,v33,v97
v_cvt_pk_bf16_f32 v106,v30,v31
M 22,a[148:151],58
v_cvt_pk_bf16_f32 v107,v32,v33
v_fma_f32 v34,-v78,v86,v34
M 22,a[160:163],62
R 50,1,18432
v_fma_f32 v35,-v78,v87,v35
v_fma_f32 v36,-v78,v88,v36
M 18,a[144:147],58
buffer_load_dword v8,s[36:39],s58 offen offset:0 sc1 lds
M 18,a[156:159],62
L 3
v_fma_f32 v37,-v78,v89,v37
v_mul_f32 v34,v79,v34
M 18,a[168:171],66
s_add_u32 m0,s57,16384
R 54,1,19456
v_mul_f32 v35,v79,v35
M 18,a[180:183],70
s_sub_u32 s57,s84,s57
v_mul_f32 v36,v79,v36
v_mul_f32 v37,v79,v37
M 22,a[172:175],66
v_fma_f32 v98,v34,v34,s88
v_fma_f32 v99,v35,v35,s88
M 22,a[184:187],70
R 58,1,20480
v_fma_f32 v100,v36,v36,s88
M 26,a[176:179],66
D 0,56,0
M 26,a[188:191],70
L 3
v_fma_f32 v101,v37,v37,s88
v_mul_f32 v98,v34,v98
M 26,a[200:203],42
R 62,1,21504
v_mul_f32 v99,v35,v99
M 26,a[212:215],46
v_exp_f32 v98,v98
M 22,a[196:199],42
v_exp_f32 v99,v99
M 22,a[208:211],46
R 66,1,22528
v_mul_f32 v100,v36,v100
M 18,a[192:195],42
v_exp_f32 v100,v100
M 18,a[204:207],46
L 3
v_mul_f32 v101,v37,v101
v_fma_f32 v98,v98,s89,s89
M 18,a[216:219],50
R 70,1,23552
v_fma_f32 v99,v99,s89,s89
M 18,a[228:231],54
D 0,56,1024
M 22,a[220:223],50
v_exp_f32 v101,v101
M 22,a[232:235],54
R 42,1,24576
v_fma_f32 v100,v100,s89,s89
M 26,a[224:227],50
v_rcp_f32 v98,v98
M 26,a[236:239],54
L 3
v_rcp_f32 v99,v99
M 26,a[248:251],58
R 46,1,25600
v_fma_f32 v101,v101,s89,s89
M 26,v[132:135],62
v_rcp_f32 v100,v100
M 22,a[244:247],58
v_rcp_f32 v101,v101
M 22,v[128:131],62
R 50,1,26624
v_mul_f32 v34,v34,v98
M 18,a[240:243],58
v_mul_f32 v35,v35,v99
v_mul_f32 v36,v36,v100
M 18,a[252:255],62
L 3
v_mul_f32 v37,v37,v101
v_cvt_pk_bf16_f32 v108,v34,v35
M 18,v[136:139],66
R 54,1,27648
v_cvt_pk_bf16_f32 v109,v36,v37
M 18,v[148:151],70
D 0,56,2048
M 22,v[140:143],66
v_fma_f32 v38,-v78,v90,v38
v_fma_f32 v39,-v78,v91,v39
M 22,v[152:155],70
R 58,1,28672
v_fma_f32 v40,-v78,v92,v40
M 26,v[144:147],66
v_fma_f32 v41,-v78,v93,v41
v_mul_f32 v38,v79,v38
M 26,v[156:159],70
L 3
v_mul_f32 v39,v79,v39
v_mul_f32 v40,v79,v40
M 26,v[168:171],42
R 62,1,29696
v_mul_f32 v41,v79,v41
M 26,v[180:183],46
v_fma_f32 v102,v38,v38,s88
v_fma_f32 v103,v39,v39,s88
M 22,v[164:167],42
s_nop 0
v_fma_f32 v104,v40,v40,s88
v_fma_f32 v105,v41,v41,s88
M 22,v[176:179],46
s_waitcnt vmcnt(3)
R 66,1,30720
v_mul_f32 v102,v38,v102
M 18,v[160:163],42
s_barrier
buffer_store_dword v4,v3,s[36:39],s70 offen offset:0 sc0 sc1
M 18,v[172:175],46
L 3
D 0,56,3072
M 18,v[184:187],50
s_mov_b32 s70,s69
R 70,1,31744
ds_read_b32 v7,v6 offset:0
M 18,v[196:199],54
s_mov_b32 s69,s68
ds_read_b64 v[74:75],v13 offset:0
v_exp_f32 v102,v102
M 22,v[188:191],50
s_add_u32 s68,s68,s63
v_mul_f32 v103,v39,v103
v_mul_f32 v104,v40,v104
M 22,v[200:203],54
R 42,2,0
v_mul_f32 v105,v41,v105
M 26,v[192:195],50
v_exp_f32 v103,v103
M 26,v[204:207],54
L 5
v_exp_f32 v104,v104
M 26,v[216:219],58
L 2
R 46,2,1024
v_fma_f32 v102,v102,s89,s89
M 26,v[228:231],62
v_exp_f32 v105,v105
M 22,v[212:215],58
buffer_store_dwordx4 v[106:109],v0,s[36:39],s67 offen offset:0 sc0 sc1
v_fma_f32 v103,v103,s89,s89
M 22,v[224:227],62
R 50,2,2048
v_fma_f32 v104,v104,s89,s89
M 18,v[208:211],58
v_rcp_f32 v102,v102
M 18,v[220:223],62
s_nop 0
v_fma_f32 v105,v105,s89,s89
v_mul_f32 v38,v38,v102
M 18,v[232:235],66
R 54,2,3072
v_cmp_eq_u32 vcc,s53,v7
M 18,v[244:247],70
v_rcp_f32 v103,v103
M 22,v[236:239],66
s_cmp_eq_u32 vcc_lo,-1
v_rcp_f32 v104,v104
v_rcp_f32 v105,v105
M 22,v[248:251],70
R 58,2,4096
v_mul_f32 v39,v39,v103
v_mul_f32 v40,v40,v104
v_mul_f32 v41,v41,v105
v_cvt_pk_bf16_f32 v110,v38,v39
v_cvt_pk_bf16_f32 v111,v40,v41
M 26,v[240:243],66
L 3
buffer_store_dwordx2 v[110:111],v10,s[36:39],s67 offen offset:0 sc0 sc1
M 26,v[252:255],70
s_cbranch_scc0 Lg3_K1_%=
Lg3_F1w2_%=:
v_mfma_f32_16x16x32_bf16 v[38:41],a[8:11],v[42:45],0
s_add_u32 m0,s57,0
R 62,2,5120
v_add_f32_dpp v115,v114,v114 quad_perm:[1,0,3,2] row_mask:0xf bank_mask:0xf
M 38,a[20:23],46
buffer_load_dword v114,v11,s[36:39],s73 offen offset:0 sc1
v_mfma_f32_16x16x32_bf16 v[34:37],a[4:7],v[42:45],0
s_add_u32 s56,s55,0
v_add_f32_dpp v115,v115,v115 quad_perm:[2,3,0,1] row_mask:0xf bank_mask:0xf
v_fma_f32 v18,-v74,v82,v18
M 34,a[16:19],46
s_add_u32 s58,s58,s61
R 66,2,6144
v_fma_f32 v19,-v74,v83,v19
v_mfma_f32_16x16x32_bf16 v[30:33],a[0:3],v[42:45],0
D 0,56,0
M 30,a[12:15],46
L 3
v_add_f32_dpp v118,v115,v115 row_half_mirror row_mask:0xf bank_mask:0xf
v_fma_f32 v20,-v74,v84,v20
M 30,a[24:27],50
s_mov_b32 s67,s65
R 70,2,7168
v_fma_f32 v21,-v74,v85,v21
M 30,a[36:39],54
s_add_u32 s65,s65,s62
v_mov_b32_dpp v115,v118 row_ror:8 row_mask:0xf bank_mask:0xf
v_fma_f32 v115,-v118,v118,v115
M 34,a[28:31],50
s_add_u32 s54,s54,s46
v_max_f32 v115,s87,v115
v_mul_f32 v118,v15,v118
M 34,a[40:43],54
s_add_u32 s73,s73,s64
R 42,2,8192
v_mul_f32 v18,v75,v18
M 38,a[32:35],50
v_rsq_f32 v116,v115
M 38,a[44:47],54
L 3
D 0,56,1024
M 38,a[56:59],58
R 46,2,9216
v_fma_f32 v118,v116,v16,v118
M 38,a[68:71],62
ds_write_b32 v12,v118 offset:2048
M 34,a[52:55],58
v_mul_f32 v19,v75,v19
v_mul_f32 v20,v75,v20
M 34,a[64:67],62
R 50,2,10240
v_mul_f32 v21,v75,v21
M 30,a[48:51],58
v_fma_f32 v94,v18,v18,s88
v_fma_f32 v95,v19,v19,s88
M 30,a[60:63],62
L 4
v_fma_f32 v96,v20,v20,s88
v_fma_f32 v97,v21,v21,s88
M 30,a[72:75],66
R 54,2,11264
v_mul_f32 v94,v18,v94
M 30,a[84:87],70
v_exp_f32 v94,v94
M 34,a[76:79],66
v_mul_f32 v95,v19,v95
v_mul_f32 v96,v20,v96
M 34,a[88:91],70
R 58,2,12288
v_mul_f32 v97,v21,v97
M 38,a[80:83],66
D 0,56,2048
M 38,a[92:95],70
L 4
v_exp_f32 v95,v95
M 38,a[104:107],42
R 62,2,13312
v_fma_f32 v94,v94,s89,s89
M 38,a[116:119],46
v_exp_f32 v96,v96
M 34,a[100:103],42
v_exp_f32 v97,v97
M 34,a[112:115],46
s_waitcnt vmcnt(6)
R 66,2,14336
v_fma_f32 v95,v95,s89,s89
M 30,a[96:99],42
s_barrier
v_rcp_f32 v94,v94
M 30,a[108:111],46
L 3
v_rcp_f32 v95,v95
M 30,a[120:123],50
R 70,2,15360
v_fma_f32 v96,v96,s89,s89
M 30,a[132:135],54
D 0,56,3072
M 34,a[124:127],50
s_add_u32 m0,s59,1024
v_rcp_f32 v96,v96
M 34,a[136:139],54
s_add_u32 s56,s55,16384
)"
R"(R 42,2,16384
v_fma_f32 v97,v97,s89,s89
M 38,a[128:131],50
s_add_u32 s55,s55,s60
v_rcp_f32 v97,v97
M 38,a[140:143],54
L 3
v_mul_f32 v18,v18,v94
v_mul_f32 v19,v19,v95
M 38,a[152:155],58
R 46,2,17408
v_mul_f32 v20,v20,v96
M 38,a[164:167],62
v_mul_f32 v21,v21,v97
v_cvt_pk_bf16_f32 v106,v18,v19
M 34,a[148:151],58
v_cvt_pk_bf16_f32 v107,v20,v21
v_fma_f32 v22,-v74,v86,v22
M 34,a[160:163],62
R 50,2,18432
v_fma_f32 v23,-v74,v87,v23
v_fma_f32 v24,-v74,v88,v24
M 30,a[144:147],58
buffer_load_dword v8,s[36:39],s58 offen offset:0 sc1 lds
M 30,a[156:159],62
L 3
v_fma_f32 v25,-v74,v89,v25
v_mul_f32 v22,v75,v22
M 30,a[168:171],66
s_add_u32 m0,s57,16384
R 54,2,19456
v_mul_f32 v23,v75,v23
M 30,a[180:183],70
s_sub_u32 s57,s84,s57
v_mul_f32 v24,v75,v24
v_mul_f32 v25,v75,v25
M 34,a[172:175],66
v_fma_f32 v98,v22,v22,s88
v_fma_f32 v99,v23,v23,s88
M 34,a[184:187],70
R 58,2,20480
v_fma_f32 v100,v24,v24,s88
M 38,a[176:179],66
D 0,56,0
M 38,a[188:191],70
L 3
v_fma_f32 v101,v25,v25,s88
v_mul_f32 v98,v22,v98
M 38,a[200:203],42
R 62,2,21504
v_mul_f32 v99,v23,v99
M 38,a[212:215],46
v_exp_f32 v98,v98
M 34,a[196:199],42
v_exp_f32 v99,v99
M 34,a[208:211],46
R 66,2,22528
v_mul_f32 v100,v24,v100
M 30,a[192:195],42
v_exp_f32 v100,v100
M 30,a[204:207],46
L 3
v_mul_f32 v101,v25,v101
v_fma_f32 v98,v98,s89,s89
M 30,a[216:219],50
R 70,2,23552
v_fma_f32 v99,v99,s89,s89
M 30,a[228:231],54
D 0,56,1024
M 34,a[220:223],50
v_exp_f32 v101,v101
M 34,a[232:235],54
R 42,2,24576
v_fma_f32 v100,v100,s89,s89
M 38,a[224:227],50
v_rcp_f32 v98,v98
M 38,a[236:239],54
L 3
v_rcp_f32 v99,v99
M 38,a[248:251],58
R 46,2,25600
v_fma_f32 v101,v101,s89,s89
M 38,v[132:135],62
v_rcp_f32 v100,v100
M 34,a[244:247],58
v_rcp_f32 v101,v101
M 34,v[128:131],62
R 50,2,26624
v_mul_f32 v22,v22,v98
M 30,a[240:243],58
v_mul_f32 v23,v23,v99
v_mul_f32 v24,v24,v100
M 30,a[252:255],62
L 3
v_mul_f32 v25,v25,v101
v_cvt_pk_bf16_f32 v108,v22,v23
M 30,v[136:139],66
R 54,2,27648
v_cvt_pk_bf16_f32 v109,v24,v25
M 30,v[148:151],70
D 0,56,2048
M 34,v[140:143],66
v_fma_f32 v26,-v74,v90,v26
v_fma_f32 v27,-v74,v91,v27
M 34,v[152:155],70
R 58,2,28672
v_fma_f32 v28,-v74,v92,v28
M 38,v[144:147],66
v_fma_f32 v29,-v74,v93,v29
v_mul_f32 v26,v75,v26
M 38,v[156:159],70
L 3
v_mul_f32 v27,v75,v27
v_mul_f32 v28,v75,v28
M 38,v[168:171],42
R 62,2,29696
v_mul_f32 v29,v75,v29
M 38,v[180:183],46
v_fma_f32 v102,v26,v26,s88
v_fma_f32 v103,v27,v27,s88
M 34,v[164:167],42
s_nop 0
v_fma_f32 v104,v28,v28,s88
v_fma_f32 v105,v29,v29,s88
M 34,v[176:179],46
s_waitcnt vmcnt(3)
R 66,2,30720
v_mul_f32 v102,v26,v102
M 30,v[160:163],42
s_barrier
buffer_store_dword v4,v3,s[36:39],s70 offen offset:0 sc0 sc1
M 30,v[172:175],46
L 3
D 0,56,3072
M 30,v[184:187],50
s_mov_b32 s70,s69
R 70,2,31744
ds_read_b32 v7,v6 offset:1024
M 30,v[196:199],54
s_mov_b32 s69,s68
ds_read_b64 v[78:79],v13 offset:2048
v_exp_f32 v102,v102
M 34,v[188:191],50
s_add_u32 s68,s68,s63
v_mul_f32 v103,v27,v103
v_mul_f32 v104,v28,v104
M 34,v[200:203],54
R 42,1,0
v_mul_f32 v105,v29,v105
M 38,v[192:195],50
v_exp_f32 v103,v103
M 38,v[204:207],54
L 5
v_exp_f32 v104,v104
M 38,v[216:219],58
L 2
R 46,1,1024
v_fma_f32 v102,v102,s89,s89
M 38,v[228:231],62
v_exp_f32 v105,v105
M 34,v[212:215],58
buffer_store_dwordx4 v[106:109],v0,s[36:39],s66 offen offset:0 sc0 sc1
v_fma_f32 v103,v103,s89,s89
M 34,v[224:227],62
R 50,1,2048
v_fma_f32 v104,v104,s89,s89
M 30,v[208:211],58
v_rcp_f32 v102,v102
M 30,v[220:223],62
s_nop 0
v_fma_f32 v105,v105,s89,s89
v_mul_f32 v26,v26,v102
M 30,v[232:235],66
R 54,1,3072
v_cmp_eq_u32 vcc,s53,v7
M 30,v[244:247],70
v_rcp_f32 v103,v103
M 34,v[236:239],66
s_cmp_eq_u32 vcc_lo,-1
v_rcp_f32 v104,v104
v_rcp_f32 v105,v105
M 34,v[248:251],70
R 58,1,4096
v_mul_f32 v27,v27,v103
v_mul_f32 v28,v28,v104
v_mul_f32 v29,v29,v105
v_cvt_pk_bf16_f32 v110,v26,v27
v_cvt_pk_bf16_f32 v111,v28,v29
M 38,v[240:243],66
L 3
buffer_store_dwordx2 v[110:111],v10,s[36:39],s66 offen offset:0 sc0 sc1
M 38,v[252:255],70
s_cbranch_scc1 Lg3_F0w2_%=
s_branch Lg3_K0_%=
Lg3_F0w3_%=:
v_mfma_f32_16x16x32_bf16 v[26:29],a[8:11],v[42:45],0
s_add_u32 m0,s57,0
R 62,1,5120
v_add_f32_dpp v115,v114,v114 quad_perm:[1,0,3,2] row_mask:0xf bank_mask:0xf
M 26,a[20:23],46
buffer_load_dword v114,v11,s[36:39],s73 offen offset:0 sc1
v_mfma_f32_16x16x32_bf16 v[22:25],a[4:7],v[42:45],0
s_add_u32 s56,s55,0
v_add_f32_dpp v115,v115,v115 quad_perm:[2,3,0,1] row_mask:0xf bank_mask:0xf
v_fma_f32 v30,-v78,v82,v30
M 22,a[16:19],46
s_add_u32 s58,s58,s61
R 66,1,6144
v_fma_f32 v31,-v78,v83,v31
v_mfma_f32_16x16x32_bf16 v[18:21],a[0:3],v[42:45],0
D 0,56,0
M 18,a[12:15],46
L 3
v_add_f32_dpp v118,v115,v115 row_half_mirror row_mask:0xf bank_mask:0xf
v_fma_f32 v32,-v78,v84,v32
M 18,a[24:27],50
s_mov_b32 s66,s65
R 70,1,7168
v_fma_f32 v33,-v78,v85,v33
M 18,a[36:39],54
s_add_u32 s65,s65,s62
v_mov_b32_dpp v115,v118 row_ror:8 row_mask:0xf bank_mask:0xf
v_fma_f32 v115,-v118,v118,v115
M 22,a[28:31],50
s_add_u32 s54,s54,s46
v_max_f32 v115,s87,v115
v_mul_f32 v118,v15,v118
M 22,a[40:43],54
s_add_u32 s73,s73,s64
R 42,1,8192
v_mul_f32 v30,v79,v30
M 26,a[32:35],50
v_rsq_f32 v116,v115
M 26,a[44:47],54
L 3
D 0,56,1024
M 26,a[56:59],58
R 46,1,9216
v_fma_f32 v118,v116,v16,v118
M 26,a[68:71],62
ds_write_b32 v12,v118 offset:0
M 22,a[52:55],58
v_mul_f32 v31,v79,v31
v_mul_f32 v32,v79,v32
M 22,a[64:67],62
R 50,1,10240
v_mul_f32 v33,v79,v33
M 18,a[48:51],58
v_fma_f32 v94,v30,v30,s88
v_fma_f32 v95,v31,v31,s88
M 18,a[60:63],62
L 4
v_fma_f32 v96,v32,v32,s88
v_fma_f32 v97,v33,v33,s88
M 18,a[72:75],66
R 54,1,11264
v_mul_f32 v94,v30,v94
M 18,a[84:87],70
v_exp_f32 v94,v94
M 22,a[76:79],66
v_mul_f32 v95,v31,v95
v_mul_f32 v96,v32,v96
M 22,a[88:91],70
R 58,1,12288
v_mul_f32 v97,v33,v97
M 26,a[80:83],66
D 0,56,2048
M 26,a[92:95],70
L 4
v_exp_f32 v95,v95
M 26,a[104:107],42
R 62,1,13312
v_fma_f32 v94,v94,s89,s89
M 26,a[116:119],46
s_waitcnt vmcnt(6)
v_exp_f32 v96,v96
M 22,a[100:103],42
s_barrier
v_exp_f32 v97,v97
M 22,a[112:115],46
R 66,1,14336
v_fma_f32 v95,v95,s89,s89
M 18,a[96:99],42
v_rcp_f32 v94,v94
M 18,a[108:111],46
L 3
v_rcp_f32 v95,v95
M 18,a[120:123],50
R 70,1,15360
v_fma_f32 v96,v96,s89,s89
M 18,a[132:135],54
D 0,56,3072
M 22,a[124:127],50
s_add_u32 m0,s59,0
v_rcp_f32 v96,v96
M 22,a[136:139],54
s_add_u32 s56,s55,16384
R 42,1,16384
v_fma_f32 v97,v97,s89,s89
M 26,a[128:131],50
s_add_u32 s55,s55,s60
v_rcp_f32 v97,v97
M 26,a[140:143],54
L 3
v_mul_f32 v30,v30,v94
v_mul_f32 v31,v31,v95
M 26,a[152:155],58
R 46,1,17408
v_mul_f32 v32,v32,v96
M 26,a[164:167],62
v_mul_f32 v33,v33,v97
v_cvt_pk_bf16_f32 v106,v30,v31
M 22,a[148:151],58
v_cvt_pk_bf16_f32 v107,v32,v33
v_fma_f32 v34,-v78,v86,v34
M 22,a[160:163],62
R 50,1,18432
v_fma_f32 v35,-v78,v87,v35
v_fma_f32 v36,-v78,v88,v36
M 18,a[144:147],58
buffer_load_dword v8,s[36:39],s58 offen offset:0 sc1 lds
M 18,a[156:159],62
L 3
v_fma_f32 v37,-v78,v89,v37
v_mul_f32 v34,v79,v34
M 18,a[168:171],66
s_add_u32 m0,s57,16384
R 54,1,19456
v_mul_f32 v35,v79,v35
M 18,a[180:183],70
s_sub_u32 s57,s84,s57
v_mul_f32 v36,v79,v36
v_mul_f32 v37,v79,v37
M 22,a[172:175],66
v_fma_f32 v98,v34,v34,s88
v_fma_f32 v99,v35,v35,s88
M 22,a[184:187],70
R 58,1,20480
v_fma_f32 v100,v36,v36,s88
M 26,a[176:179],66
D 0,56,0
M 26,a[188:191],70
L 3
v_fma_f32 v101,v37,v37,s88
v_mul_f32 v98,v34,v98
M 26,a[200:203],42
R 62,1,21504
v_mul_f32 v99,v35,v99
M 26,a[212:215],46
v_exp_f32 v98,v98
M 22,a[196:199],42
v_exp_f32 v99,v99
M 22,a[208:211],46
R 66,1,22528
v_mul_f32 v100,v36,v100
M 18,a[192:195],42
v_exp_f32 v100,v100
M 18,a[204:207],46
L 3
v_mul_f32 v101,v37,v101
v_fma_f32 v98,v98,s89,s89
M 18,a[216:219],50
R 70,1,23552
v_fma_f32 v99,v99,s89,s89
M 18,a[228:231],54
D 0,56,1024
M 22,a[220:223],50
v_exp_f32 v101,v101
M 22,a[232:235],54
R 42,1,24576
v_fma_f32 v100,v100,s89,s89
M 26,a[224:227],50
v_rcp_f32 v98,v98
M 26,a[236:239],54
L 3
v_rcp_f32 v99,v99
M 26,a[248:251],58
R 46,1,25600
v_fma_f32 v101,v101,s89,s89
M 26,v[132:135],62
v_rcp_f32 v100,v100
M 22,a[244:247],58
v_rcp_f32 v101,v101
M 22,v[128:131],62
R 50,1,26624
v_mul_f32 v34,v34,v98
M 18,a[240:243],58
v_mul_f32 v35,v35,v99
v_mul_f32 v36,v36,v100
M 18,a[252:255],62
L 3
v_mul_f32 v37,v37,v101
v_cvt_pk_bf16_f32 v108,v34,v35
M 18,v[136:139],66
R 54,1,27648
v_cvt_pk_bf16_f32 v109,v36,v37
M 18,v[148:151],70
D 0,56,2048
M 22,v[140:143],66
v_fma_f32 v38,-v78,v90,v38
v_fma_f32 v39,-v78,v91,v39
M 22,v[152:155],70
R 58,1,28672
v_fma_f32 v40,-v78,v92,v40
M 26,v[144:147],66
v_fma_f32 v41,-v78,v93,v41
v_mul_f32 v38,v79,v38
M 26,v[156:159],70
L 3
v_mul_f32 v39,v79,v39
v_mul_f32 v40,v79,v40
M 26,v[168:171],42
s_nop 0
R 62,1,29696
v_mul_f32 v41,v79,v41
M 26,v[180:183],46
s_waitcnt vmcnt(3)
v_fma_f32 v102,v38,v38,s88
v_fma_f32 v103,v39,v39,s88
M 22,v[164:167],42
s_barrier
buffer_store_dword v4,v3,s[36:39],s70 offen offset:0 sc0 sc1
M 22,v[176:179],46
s_mov_b32 s70,s69
R 66,1,30720
ds_read_b32 v7,v6 offset:0
M 18,v[160:163],42
s_mov_b32 s69,s68
ds_read_b64 v[74:75],v13 offset:0
v_fma_f32 v104,v40,v40,s88
M 18,v[172:175],46
L 5
D 0,56,3072
M 18,v[184:187],50
s_add_u32 s68,s68,s63
R 70,1,31744
v_fma_f32 v105,v41,v41,s88
M 18,v[196:199],54
v_mul_f32 v102,v38,v102
v_exp_f32 v102,v102
M 22,v[188:191],50
v_mul_f32 v103,v39,v103
v_mul_f32 v104,v40,v104
M 22,v[200:203],54
L 2
R 42,2,0
v_mul_f32 v105,v41,v105
M 26,v[192:195],50
v_exp_f32 v103,v103
M 26,v[204:207],54
s_nop 0
v_exp_f32 v104,v104
M 26,v[216:219],58
R 46,2,1024
v_fma_f32 v102,v102,s89,s89
M 26,v[228:231],62
v_exp_f32 v105,v105
M 22,v[212:215],58
buffer_store_dwordx4 v[106:109],v0,s[36:39],s67 offen offset:0 sc0 sc1
v_fma_f32 v103,v103,s89,s89
M 22,v[224:227],62
R 50,2,2048
v_fma_f32 v104,v104,s89,s89
M 18,v[208:211],58
v_rcp_f32 v102,v102
M 18,v[220:223],62
L 3
v_fma_f32 v105,v105,s89,s89
v_mul_f32 v38,v38,v102
M 18,v[232:235],66
R 54,2,3072
v_cmp_eq_u32 vcc,s53,v7
M 18,v[244:247],70
v_rcp_f32 v103,v103
M 22,v[236:239],66
s_cmp_eq_u32 vcc_lo,-1
v_rcp_f32 v104,v104
v_rcp_f32 v105,v105
M 22,v[248:251],70
R 58,2,4096
v_mul_f32 v39,v39,v103
v_mul_f32 v40,v40,v104
v_mul_f32 v41,v41,v105
v_cvt_pk_bf16_f32 v110,v38,v39
v_cvt_pk_bf16_f32 v111,v40,v41
M 26,v[240:243],66
L 3
buffer_store_dwordx2 v[110:111],v10,s[36:39],s67 offen offset:0 sc0 sc1
M 26,v[252:255],70
s_cbranch_scc0 Lg3_K1_%=
Lg3_F1w3_%=:
v_mfma_f32_16x16x32_bf16 v[38:41],a[8:11],v[42:45],0
s_add_u32 m0,s57,0
R 62,2,5120
v_add_f32_dpp v115,v114,v114 quad_perm:[1,0,3,2] row_mask:0xf bank_mask:0xf
M 38,a[20:23],46
buffer_load_dword v114,v11,s[36:39],s73 offen offset:0 sc1
v_mfma_f32_16x16x32_bf16 v[34:37],a[4:7],v[42:45],0
s_add_u32 s56,s55,0
v_add_f32_dpp v115,v115,v115 quad_perm:[2,3,0,1] row_mask:0xf bank_mask:0xf
)"
R"(v_fma_f32 v18,-v74,v82,v18
M 34,a[16:19],46
s_add_u32 s58,s58,s61
R 66,2,6144
v_fma_f32 v19,-v74,v83,v19
v_mfma_f32_16x16x32_bf16 v[30:33],a[0:3],v[42:45],0
D 0,56,0
M 30,a[12:15],46
L 3
v_add_f32_dpp v118,v115,v115 row_half_mirror row_mask:0xf bank_mask:0xf
v_fma_f32 v20,-v74,v84,v20
M 30,a[24:27],50
s_mov_b32 s67,s65
R 70,2,7168
v_fma_f32 v21,-v74,v85,v21
M 30,a[36:39],54
s_add_u32 s65,s65,s62
v_mov_b32_dpp v115,v118 row_ror:8 row_mask:0xf bank_mask:0xf
v_fma_f32 v115,-v118,v118,v115
M 34,a[28:31],50
s_add_u32 s54,s54,s46
v_max_f32 v115,s87,v115
v_mul_f32 v118,v15,v118
M 34,a[40:43],54
s_add_u32 s73,s73,s64
R 42,2,8192
v_mul_f32 v18,v75,v18
M 38,a[32:35],50
v_rsq_f32 v116,v115
M 38,a[44:47],54
L 3
D 0,56,1024
M 38,a[56:59],58
R 46,2,9216
v_fma_f32 v118,v116,v16,v118
M 38,a[68:71],62
ds_write_b32 v12,v118 offset:2048
M 34,a[52:55],58
v_mul_f32 v19,v75,v19
v_mul_f32 v20,v75,v20
M 34,a[64:67],62
R 50,2,10240
v_mul_f32 v21,v75,v21
M 30,a[48:51],58
v_fma_f32 v94,v18,v18,s88
v_fma_f32 v95,v19,v19,s88
M 30,a[60:63],62
L 4
v_fma_f32 v96,v20,v20,s88
v_fma_f32 v97,v21,v21,s88
M 30,a[72:75],66
R 54,2,11264
v_mul_f32 v94,v18,v94
M 30,a[84:87],70
v_exp_f32 v94,v94
M 34,a[76:79],66
v_mul_f32 v95,v19,v95
v_mul_f32 v96,v20,v96
M 34,a[88:91],70
R 58,2,12288
v_mul_f32 v97,v21,v97
M 38,a[80:83],66
D 0,56,2048
M 38,a[92:95],70
L 4
v_exp_f32 v95,v95
M 38,a[104:107],42
R 62,2,13312
v_fma_f32 v94,v94,s89,s89
M 38,a[116:119],46
s_waitcnt vmcnt(6)
v_exp_f32 v96,v96
M 34,a[100:103],42
s_barrier
v_exp_f32 v97,v97
M 34,a[112:115],46
R 66,2,14336
v_fma_f32 v95,v95,s89,s89
M 30,a[96:99],42
v_rcp_f32 v94,v94
M 30,a[108:111],46
L 3
v_rcp_f32 v95,v95
M 30,a[120:123],50
R 70,2,15360
v_fma_f32 v96,v96,s89,s89
M 30,a[132:135],54
D 0,56,3072
M 34,a[124:127],50
s_add_u32 m0,s59,1024
v_rcp_f32 v96,v96
M 34,a[136:139],54
s_add_u32 s56,s55,16384
R 42,2,16384
v_fma_f32 v97,v97,s89,s89
M 38,a[128:131],50
s_add_u32 s55,s55,s60
v_rcp_f32 v97,v97
M 38,a[140:143],54
L 3
v_mul_f32 v18,v18,v94
v_mul_f32 v19,v19,v95
M 38,a[152:155],58
R 46,2,17408
v_mul_f32 v20,v20,v96
M 38,a[164:167],62
v_mul_f32 v21,v21,v97
v_cvt_pk_bf16_f32 v106,v18,v19
M 34,a[148:151],58
v_cvt_pk_bf16_f32 v107,v20,v21
v_fma_f32 v22,-v74,v86,v22
M 34,a[160:163],62
R 50,2,18432
v_fma_f32 v23,-v74,v87,v23
v_fma_f32 v24,-v74,v88,v24
M 30,a[144:147],58
buffer_load_dword v8,s[36:39],s58 offen offset:0 sc1 lds
M 30,a[156:159],62
L 3
v_fma_f32 v25,-v74,v89,v25
v_mul_f32 v22,v75,v22
M 30,a[168:171],66
s_add_u32 m0,s57,16384
R 54,2,19456
v_mul_f32 v23,v75,v23
M 30,a[180:183],70
s_sub_u32 s57,s84,s57
v_mul_f32 v24,v75,v24
v_mul_f32 v25,v75,v25
M 34,a[172:175],66
v_fma_f32 v98,v22,v22,s88
v_fma_f32 v99,v23,v23,s88
M 34,a[184:187],70
R 58,2,20480
v_fma_f32 v100,v24,v24,s88
M 38,a[176:179],66
D 0,56,0
M 38,a[188:191],70
L 3
v_fma_f32 v101,v25,v25,s88
v_mul_f32 v98,v22,v98
M 38,a[200:203],42
R 62,2,21504
v_mul_f32 v99,v23,v99
M 38,a[212:215],46
v_exp_f32 v98,v98
M 34,a[196:199],42
v_exp_f32 v99,v99
M 34,a[208:211],46
R 66,2,22528
v_mul_f32 v100,v24,v100
M 30,a[192:195],42
v_exp_f32 v100,v100
M 30,a[204:207],46
L 3
v_mul_f32 v101,v25,v101
v_fma_f32 v98,v98,s89,s89
M 30,a[216:219],50
R 70,2,23552
v_fma_f32 v99,v99,s89,s89
M 30,a[228:231],54
D 0,56,1024
M 34,a[220:223],50
v_exp_f32 v101,v101
M 34,a[232:235],54
R 42,2,24576
v_fma_f32 v100,v100,s89,s89
M 38,a[224:227],50
v_rcp_f32 v98,v98
M 38,a[236:239],54
L 3
v_rcp_f32 v99,v99
M 38,a[248:251],58
R 46,2,25600
v_fma_f32 v101,v101,s89,s89
M 38,v[132:135],62
v_rcp_f32 v100,v100
M 34,a[244:247],58
v_rcp_f32 v101,v101
M 34,v[128:131],62
R 50,2,26624
v_mul_f32 v22,v22,v98
M 30,a[240:243],58
v_mul_f32 v23,v23,v99
v_mul_f32 v24,v24,v100
M 30,a[252:255],62
L 3
v_mul_f32 v25,v25,v101
v_cvt_pk_bf16_f32 v108,v22,v23
M 30,v[136:139],66
R 54,2,27648
v_cvt_pk_bf16_f32 v109,v24,v25
M 30,v[148:151],70
D 0,56,2048
M 34,v[140:143],66
v_fma_f32 v26,-v74,v90,v26
v_fma_f32 v27,-v74,v91,v27
M 34,v[152:155],70
R 58,2,28672
v_fma_f32 v28,-v74,v92,v28
M 38,v[144:147],66
v_fma_f32 v29,-v74,v93,v29
v_mul_f32 v26,v75,v26
M 38,v[156:159],70
L 3
v_mul_f32 v27,v75,v27
v_mul_f32 v28,v75,v28
M 38,v[168:171],42
s_nop 0
R 62,2,29696
v_mul_f32 v29,v75,v29
M 38,v[180:183],46
s_waitcnt vmcnt(3)
v_fma_f32 v102,v26,v26,s88
v_fma_f32 v103,v27,v27,s88
M 34,v[164:167],42
s_barrier
buffer_store_dword v4,v3,s[36:39],s70 offen offset:0 sc0 sc1
M 34,v[176:179],46
s_mov_b32 s70,s69
R 66,2,30720
ds_read_b32 v7,v6 offset:1024
M 30,v[160:163],42
s_mov_b32 s69,s68
ds_read_b64 v[78:79],v13 offset:2048
v_fma_f32 v104,v28,v28,s88
M 30,v[172:175],46
L 5
D 0,56,3072
M 30,v[184:187],50
s_add_u32 s68,s68,s63
R 70,2,31744
v_fma_f32 v105,v29,v29,s88
M 30,v[196:199],54
v_mul_f32 v102,v26,v102
v_exp_f32 v102,v102
M 34,v[188:191],50
v_mul_f32 v103,v27,v103
v_mul_f32 v104,v28,v104
M 34,v[200:203],54
L 2
R 42,1,0
v_mul_f32 v105,v29,v105
M 38,v[192:195],50
v_exp_f32 v103,v103
M 38,v[204:207],54
s_nop 0
v_exp_f32 v104,v104
M 38,v[216:219],58
R 46,1,1024
v_fma_f32 v102,v102,s89,s89
M 38,v[228:231],62
v_exp_f32 v105,v105
M 34,v[212:215],58
buffer_store_dwordx4 v[106:109],v0,s[36:39],s66 offen offset:0 sc0 sc1
v_fma_f32 v103,v103,s89,s89
M 34,v[224:227],62
R 50,1,2048
v_fma_f32 v104,v104,s89,s89
M 30,v[208:211],58
v_rcp_f32 v102,v102
M 30,v[220:223],62
L 3
v_fma_f32 v105,v105,s89,s89
v_mul_f32 v26,v26,v102
M 30,v[232:235],66
R 54,1,3072
v_cmp_eq_u32 vcc,s53,v7
M 30,v[244:247],70
v_rcp_f32 v103,v103
M 34,v[236:239],66
s_cmp_eq_u32 vcc_lo,-1
v_rcp_f32 v104,v104
v_rcp_f32 v105,v105
M 34,v[248:251],70
R 58,1,4096
v_mul_f32 v27,v27,v103
v_mul_f32 v28,v28,v104
v_mul_f32 v29,v29,v105
v_cvt_pk_bf16_f32 v110,v26,v27
v_cvt_pk_bf16_f32 v111,v28,v29
M 38,v[240:243],66
L 3
buffer_store_dwordx2 v[110:111],v10,s[36:39],s66 offen offset:0 sc0 sc1
M 38,v[252:255],70
s_cbranch_scc1 Lg3_F0w3_%=
Lg3_K0_%=:
Lg3_L0_%=:
v_mfma_f32_16x16x32_bf16 v[26:29],a[8:11],v[42:45],0
s_add_u32 m0,s59,3072
R 62,1,5120
v_add_f32_dpp v115,v114,v114 quad_perm:[1,0,3,2] row_mask:0xf bank_mask:0xf
M 26,a[20:23],46
buffer_load_dword v8,s[36:39],s58 offen offset:0 sc1 lds
v_mfma_f32_16x16x32_bf16 v[22:25],a[4:7],v[42:45],0
s_mov_b32 s66,s65
v_add_f32_dpp v115,v115,v115 quad_perm:[2,3,0,1] row_mask:0xf bank_mask:0xf
v_fma_f32 v30,-v78,v82,v30
M 22,a[16:19],46
s_add_u32 s65,s65,s62
R 66,1,6144
v_fma_f32 v31,-v78,v83,v31
v_mfma_f32_16x16x32_bf16 v[18:21],a[0:3],v[42:45],0
v_add_f32_dpp v118,v115,v115 row_half_mirror row_mask:0xf bank_mask:0xf
v_fma_f32 v32,-v78,v84,v32
v_fma_f32 v33,-v78,v85,v33
M 18,a[12:15],46
L 3
v_mul_f32 v30,v79,v30
v_mul_f32 v31,v79,v31
M 18,a[24:27],50
R 70,1,7168
v_mov_b32_dpp v115,v118 row_ror:8 row_mask:0xf bank_mask:0xf
M 18,a[36:39],54
v_fma_f32 v115,-v118,v118,v115
v_max_f32 v115,s87,v115
M 22,a[28:31],50
v_rsq_f32 v116,v115
M 22,a[40:43],54
R 42,1,8192
v_mul_f32 v118,v15,v118
v_fma_f32 v118,v116,v16,v118
M 26,a[32:35],50
ds_write_b32 v12,v118 offset:0
M 26,a[44:47],54
L 4
v_mul_f32 v32,v79,v32
v_mul_f32 v33,v79,v33
M 26,a[56:59],58
R 46,1,9216
v_fma_f32 v94,v30,v30,s88
M 26,a[68:71],62
v_fma_f32 v95,v31,v31,s88
v_fma_f32 v96,v32,v32,s88
M 22,a[52:55],58
v_fma_f32 v97,v33,v33,s88
v_mul_f32 v94,v30,v94
v_mul_f32 v95,v31,v95
M 22,a[64:67],62
R 50,1,10240
v_mul_f32 v96,v32,v96
M 18,a[48:51],58
v_exp_f32 v94,v94
M 18,a[60:63],62
L 4
v_exp_f32 v95,v95
M 18,a[72:75],66
R 54,1,11264
v_mul_f32 v97,v33,v97
M 18,a[84:87],70
v_exp_f32 v96,v96
v_fma_f32 v94,v94,s89,s89
M 22,a[76:79],66
v_exp_f32 v97,v97
M 22,a[88:91],70
R 58,1,12288
v_fma_f32 v95,v95,s89,s89
M 26,a[80:83],66
v_rcp_f32 v94,v94
M 26,a[92:95],70
L 3
v_rcp_f32 v95,v95
M 26,a[104:107],42
R 62,1,13312
v_fma_f32 v96,v96,s89,s89
v_fma_f32 v97,v97,s89,s89
M 26,a[116:119],46
v_rcp_f32 v96,v96
M 22,a[100:103],42
v_rcp_f32 v97,v97
M 22,a[112:115],46
R 66,1,14336
v_mul_f32 v30,v30,v94
M 18,a[96:99],42
v_mul_f32 v31,v31,v95
v_mul_f32 v32,v32,v96
M 18,a[108:111],46
L 3
v_mul_f32 v33,v33,v97
v_cvt_pk_bf16_f32 v106,v30,v31
v_cvt_pk_bf16_f32 v107,v32,v33
M 18,a[120:123],50
R 70,1,15360
v_fma_f32 v34,-v78,v86,v34
M 18,a[132:135],54
s_waitcnt vmcnt(0)
v_fma_f32 v35,-v78,v87,v35
v_fma_f32 v36,-v78,v88,v36
M 22,a[124:127],50
s_barrier
ds_read_b32 v7,v6 offset:3072
v_fma_f32 v37,-v78,v89,v37
M 22,a[136:139],54
R 42,1,16384
v_mul_f32 v34,v79,v34
M 26,a[128:131],50
buffer_store_dword v4,v3,s[36:39],s70 offen offset:0 sc0 sc1
v_mul_f32 v35,v79,v35
M 26,a[140:143],54
L 4
v_mul_f32 v36,v79,v36
v_mul_f32 v37,v79,v37
M 26,a[152:155],58
s_mov_b32 s70,0x7fff0000
R 46,1,17408
v_fma_f32 v98,v34,v34,s88
M 26,a[164:167],62
v_fma_f32 v99,v35,v35,s88
v_fma_f32 v100,v36,v36,s88
M 22,a[148:151],58
L 2
v_cmp_eq_u32 vcc,s53,v7
v_fma_f32 v101,v37,v37,s88
M 22,a[160:163],62
R 50,1,18432
v_mul_f32 v98,v34,v98
v_mul_f32 v99,v35,v99
M 18,a[144:147],58
v_exp_f32 v98,v98
M 18,a[156:159],62
s_cmp_eq_u32 vcc_lo,-1
s_nop 0
v_mul_f32 v100,v36,v100
s_cbranch_scc0 Lg3_X0_%=
M 18,a[168:171],66
s_add_u32 m0,s57,0
s_add_u32 s56,s55,0
s_add_u32 s58,s58,s61
R 54,1,19456
M 18,a[180:183],70
buffer_load_dword v114,v11,s[36:39],s73 offen offset:0 sc1
M 22,a[172:175],66
D 0,56,0
v_mul_f32 v101,v37,v101
M 22,a[184:187],70
s_add_u32 s54,s54,s46
R 58,1,20480
v_fma_f32 v98,v98,s89,s89
M 26,a[176:179],66
s_add_u32 s73,s73,s64
v_exp_f32 v99,v99
M 26,a[188:191],70
L 3
v_exp_f32 v100,v100
M 26,a[200:203],42
R 62,1,21504
v_fma_f32 v99,v99,s89,s89
M 26,a[212:215],46
D 0,56,1024
v_fma_f32 v100,v100,s89,s89
M 22,a[196:199],42
v_exp_f32 v101,v101
M 22,a[208:211],46
R 66,1,22528
v_fma_f32 v101,v101,s89,s89
M 18,a[192:195],42
D 0,56,2048
M 18,a[204:207],46
L 3
v_rcp_f32 v98,v98
M 18,a[216:219],50
R 70,1,23552
v_rcp_f32 v99,v99
M 18,a[228:231],54
v_rcp_f32 v100,v100
M 22,a[220:223],50
D 0,56,3072
M 22,a[232:235],54
s_add_u32 m0,s59,0
R 42,1,24576
v_mul_f32 v34,v34,v98
M 26,a[224:227],50
buffer_load_dword v8,s[36:39],s58 offen offset:0 sc1 lds
M 26,a[236:239],54
L 3
v_rcp_f32 v101,v101
v_mul_f32 v35,v35,v99
M 26,a[248:251],58
s_add_u32 m0,s57,16384
R 46,1,25600
v_mul_f32 v36,v36,v100
M 26,v[132:135],62
s_add_u32 s56,s55,16384
v_mul_f32 v37,v37,v101
v_cvt_pk_bf16_f32 v108,v34,v35
M 22,a[244:247],58
D 0,56,0
M 22,v[128:131],62
s_add_u32 s55,s55,s60
R 50,1,26624
v_cvt_pk_bf16_f32 v109,v36,v37
M 18,a[240:243],58
buffer_store_dwordx4 v[106:109],v0,s[36:39],s67 offen offset:0 sc0 sc1
v_fma_f32 v38,-v78,v90,v38
M 18,a[252:255],62
L 3
v_fma_f32 v39,-v78,v91,v39
v_fma_f32 v40,-v78,v92,v40
M 18,v[136:139],66
s_sub_u32 s57,s84,s57
)"
R"(R 54,1,27648
v_fma_f32 v41,-v78,v93,v41
M 18,v[148:151],70
D 0,56,1024
M 22,v[140:143],66
v_mul_f32 v38,v79,v38
v_mul_f32 v39,v79,v39
M 22,v[152:155],70
R 58,1,28672
v_mul_f32 v40,v79,v40
v_mul_f32 v41,v79,v41
M 26,v[144:147],66
v_fma_f32 v102,v38,v38,s88
v_fma_f32 v103,v39,v39,s88
M 26,v[156:159],70
L 3
v_fma_f32 v104,v40,v40,s88
v_fma_f32 v105,v41,v41,s88
M 26,v[168:171],42
R 62,1,29696
v_mul_f32 v102,v38,v102
M 26,v[180:183],46
D 0,56,2048
M 22,v[164:167],42
v_exp_f32 v102,v102
v_mul_f32 v103,v39,v103
M 22,v[176:179],46
R 66,1,30720
v_mul_f32 v104,v40,v104
M 18,v[160:163],42
v_exp_f32 v103,v103
M 18,v[172:175],46
L 3
D 0,56,3072
M 18,v[184:187],50
s_nop 0
R 70,1,31744
v_mul_f32 v105,v41,v105
M 18,v[196:199],54
s_waitcnt vmcnt(5)
v_exp_f32 v104,v104
v_fma_f32 v102,v102,s89,s89
M 22,v[188:191],50
s_barrier
buffer_store_dword v4,v3,s[36:39],s70 offen offset:0 sc0 sc1
M 22,v[200:203],54
s_mov_b32 s70,s69
R 42,2,0
ds_read_b32 v7,v6 offset:0
M 26,v[192:195],50
s_mov_b32 s69,s68
ds_read_b64 v[74:75],v13 offset:0
v_fma_f32 v103,v103,s89,s89
M 26,v[204:207],54
L 5
v_exp_f32 v105,v105
M 26,v[216:219],58
s_add_u32 s68,s68,s63
R 46,2,1024
v_rcp_f32 v102,v102
M 26,v[228:231],62
v_rcp_f32 v103,v103
M 22,v[212:215],58
v_fma_f32 v104,v104,s89,s89
v_fma_f32 v105,v105,s89,s89
M 22,v[224:227],62
L 2
R 50,2,2048
v_mul_f32 v38,v38,v102
M 18,v[208:211],58
v_rcp_f32 v104,v104
M 18,v[220:223],62
s_nop 0
v_rcp_f32 v105,v105
v_mul_f32 v39,v39,v103
M 18,v[232:235],66
R 54,2,3072
v_mul_f32 v40,v40,v104
M 18,v[244:247],70
v_mul_f32 v41,v41,v105
v_cmp_eq_u32 vcc,s53,v7
M 22,v[236:239],66
v_cvt_pk_bf16_f32 v110,v38,v39
v_cvt_pk_bf16_f32 v111,v40,v41
M 22,v[248:251],70
s_cmp_eq_u32 vcc_lo,-1
R 58,2,4096
M 26,v[240:243],66
L 3
buffer_store_dwordx2 v[110:111],v10,s[36:39],s67 offen offset:0 sc0 sc1
M 26,v[252:255],70
buffer_store_dword v4,v3,s[36:39],s71 offen offset:0 sc0 sc1
s_cbranch_scc1 Lg3_F1_%=
s_branch Lg3_K1_%=
Lg3_X0_%=:
M 18,a[168:171],66
s_add_u32 m0,s59,0
R 54,1,19456
v_mul_f32 v101,v37,v101
M 18,a[180:183],70
buffer_load_dword v8,s[36:39],s58 offen offset:0 sc1 lds
M 22,a[172:175],66
v_exp_f32 v99,v99
M 22,a[184:187],70
R 58,1,20480
v_fma_f32 v98,v98,s89,s89
M 26,a[176:179],66
v_exp_f32 v100,v100
M 26,a[188:191],70
L 3
v_exp_f32 v101,v101
M 26,a[200:203],42
R 62,1,21504
v_fma_f32 v99,v99,s89,s89
M 26,a[212:215],46
v_rcp_f32 v98,v98
M 22,a[196:199],42
v_rcp_f32 v99,v99
M 22,a[208:211],46
R 66,1,22528
v_fma_f32 v100,v100,s89,s89
M 18,a[192:195],42
v_rcp_f32 v100,v100
M 18,a[204:207],46
L 3
v_fma_f32 v101,v101,s89,s89
v_mul_f32 v34,v34,v98
M 18,a[216:219],50
R 70,1,23552
v_mul_f32 v35,v35,v99
M 18,a[228:231],54
v_rcp_f32 v101,v101
M 22,a[220:223],50
v_mul_f32 v36,v36,v100
v_mul_f32 v37,v37,v101
M 22,a[232:235],54
R 42,1,24576
v_cvt_pk_bf16_f32 v108,v34,v35
M 26,a[224:227],50
v_cvt_pk_bf16_f32 v109,v36,v37
v_fma_f32 v38,-v78,v90,v38
M 26,a[236:239],54
L 3
buffer_store_dwordx4 v[106:109],v0,s[36:39],s67 offen offset:0 sc0 sc1
v_fma_f32 v39,-v78,v91,v39
M 26,a[248:251],58
R 46,1,25600
v_fma_f32 v40,-v78,v92,v40
M 26,v[132:135],62
v_fma_f32 v41,-v78,v93,v41
v_mul_f32 v38,v79,v38
M 22,a[244:247],58
v_mul_f32 v39,v79,v39
v_mul_f32 v40,v79,v40
M 22,v[128:131],62
R 50,1,26624
v_mul_f32 v41,v79,v41
M 18,a[240:243],58
v_fma_f32 v102,v38,v38,s88
v_fma_f32 v103,v39,v39,s88
M 18,a[252:255],62
L 3
v_fma_f32 v104,v40,v40,s88
v_fma_f32 v105,v41,v41,s88
M 18,v[136:139],66
R 54,1,27648
v_mul_f32 v102,v38,v102
M 18,v[148:151],70
v_exp_f32 v102,v102
M 22,v[140:143],66
v_mul_f32 v103,v39,v103
v_mul_f32 v104,v40,v104
M 22,v[152:155],70
R 58,1,28672
v_mul_f32 v105,v41,v105
M 26,v[144:147],66
v_exp_f32 v103,v103
M 26,v[156:159],70
L 3
v_exp_f32 v104,v104
M 26,v[168:171],42
R 62,1,29696
v_fma_f32 v102,v102,s89,s89
M 26,v[180:183],46
v_exp_f32 v105,v105
M 22,v[164:167],42
v_rcp_f32 v102,v102
M 22,v[176:179],46
R 66,1,30720
v_fma_f32 v103,v103,s89,s89
M 18,v[160:163],42
v_rcp_f32 v103,v103
M 18,v[172:175],46
L 3
v_fma_f32 v104,v104,s89,s89
v_fma_f32 v105,v105,s89,s89
M 18,v[184:187],50
s_nop 0
R 70,1,31744
v_mul_f32 v38,v38,v102
M 18,v[196:199],54
s_waitcnt vmcnt(1)
v_rcp_f32 v104,v104
M 22,v[188:191],50
s_barrier
buffer_store_dword v4,v3,s[36:39],s70 offen offset:0 sc0 sc1
M 22,v[200:203],54
s_mov_b32 s70,s69
ds_read_b32 v7,v6 offset:0
ds_read_b64 v[74:75],v13 offset:0
M 26,v[192:195],50
s_mov_b32 s69,s68
v_rcp_f32 v105,v105
M 26,v[204:207],54
L 4
v_mul_f32 v39,v39,v103
v_mul_f32 v40,v40,v104
M 26,v[216:219],58
s_add_u32 s68,s68,s63
v_mul_f32 v41,v41,v105
v_cvt_pk_bf16_f32 v110,v38,v39
M 26,v[228:231],62
v_cvt_pk_bf16_f32 v111,v40,v41
M 22,v[212:215],58
buffer_store_dwordx2 v[110:111],v10,s[36:39],s67 offen offset:0 sc0 sc1
M 22,v[224:227],62
L 1
v_cmp_eq_u32 vcc,s53,v7
M 18,v[208:211],58
M 18,v[220:223],62
s_nop 0
M 18,v[232:235],66
M 18,v[244:247],70
M 22,v[236:239],66
M 22,v[248:251],70
M 26,v[240:243],66
M 26,v[252:255],70
s_mov_b32 s80,1
s_cmp_eq_u32 vcc_lo,-1
s_cbranch_scc0 Lg3_xn0_%=
buffer_load_dword v114,v11,s[36:39],s73 offen offset:0 sc1
L 0
v_fma_f32 v18,-v74,v82,v18
v_fma_f32 v19,-v74,v83,v19
v_fma_f32 v20,-v74,v84,v20
v_fma_f32 v21,-v74,v85,v21
v_mul_f32 v18,v75,v18
v_mul_f32 v19,v75,v19
v_mul_f32 v20,v75,v20
v_mul_f32 v21,v75,v21
s_add_u32 s73,s73,s64
s_add_u32 m0,s57,0
s_add_u32 s56,s55,0
D 0,56,0
v_fma_f32 v94,v18,v18,s88
v_fma_f32 v95,v19,v19,s88
v_fma_f32 v96,v20,v20,s88
v_fma_f32 v97,v21,v21,s88
v_mul_f32 v94,v18,v94
v_mul_f32 v95,v19,v95
v_mul_f32 v96,v20,v96
v_mul_f32 v97,v21,v97
D 0,56,1024
v_exp_f32 v94,v94
v_exp_f32 v95,v95
v_exp_f32 v96,v96
v_exp_f32 v97,v97
v_fma_f32 v94,v94,s89,s89
v_fma_f32 v95,v95,s89,s89
v_fma_f32 v96,v96,s89,s89
v_fma_f32 v97,v97,s89,s89
D 0,56,2048
v_rcp_f32 v94,v94
v_rcp_f32 v95,v95
v_rcp_f32 v96,v96
v_rcp_f32 v97,v97
v_mul_f32 v18,v18,v94
v_mul_f32 v19,v19,v95
v_mul_f32 v20,v20,v96
v_mul_f32 v21,v21,v97
D 0,56,3072
v_cvt_pk_bf16_f32 v106,v18,v19
v_cvt_pk_bf16_f32 v107,v20,v21
v_fma_f32 v22,-v74,v86,v22
v_fma_f32 v23,-v74,v87,v23
v_fma_f32 v24,-v74,v88,v24
v_fma_f32 v25,-v74,v89,v25
v_mul_f32 v22,v75,v22
v_mul_f32 v23,v75,v23
s_add_u32 s58,s58,s61
s_add_u32 m0,s57,16384
s_add_u32 s56,s55,16384
D 0,56,0
v_mul_f32 v24,v75,v24
v_mul_f32 v25,v75,v25
v_fma_f32 v98,v22,v22,s88
v_fma_f32 v99,v23,v23,s88
v_fma_f32 v100,v24,v24,s88
v_fma_f32 v101,v25,v25,s88
v_mul_f32 v98,v22,v98
v_mul_f32 v99,v23,v99
D 0,56,1024
v_mul_f32 v100,v24,v100
v_mul_f32 v101,v25,v101
v_exp_f32 v98,v98
v_exp_f32 v99,v99
v_exp_f32 v100,v100
v_exp_f32 v101,v101
v_fma_f32 v98,v98,s89,s89
v_fma_f32 v99,v99,s89,s89
D 0,56,2048
v_fma_f32 v100,v100,s89,s89
v_fma_f32 v101,v101,s89,s89
v_rcp_f32 v98,v98
v_rcp_f32 v99,v99
v_rcp_f32 v100,v100
v_rcp_f32 v101,v101
v_mul_f32 v22,v22,v98
v_mul_f32 v23,v23,v99
D 0,56,3072
v_mul_f32 v24,v24,v100
v_mul_f32 v25,v25,v101
v_cvt_pk_bf16_f32 v108,v22,v23
v_cvt_pk_bf16_f32 v109,v24,v25
buffer_store_dwordx4 v[106:109],v0,s[36:39],s66 offen offset:0 sc0 sc1
v_fma_f32 v26,-v74,v90,v26
v_fma_f32 v27,-v74,v91,v27
v_fma_f32 v28,-v74,v92,v28
v_fma_f32 v29,-v74,v93,v29
s_add_u32 s55,s55,s60
s_sub_u32 s57,s84,s57
s_add_u32 s54,s54,s46
v_mul_f32 v26,v75,v26
v_mul_f32 v27,v75,v27
v_mul_f32 v28,v75,v28
v_mul_f32 v29,v75,v29
v_fma_f32 v102,v26,v26,s88
v_fma_f32 v103,v27,v27,s88
v_fma_f32 v104,v28,v28,s88
v_fma_f32 v105,v29,v29,s88
v_mul_f32 v102,v26,v102
v_mul_f32 v103,v27,v103
v_mul_f32 v104,v28,v104
v_mul_f32 v105,v29,v105
v_exp_f32 v102,v102
v_exp_f32 v103,v103
v_exp_f32 v104,v104
v_exp_f32 v105,v105
v_fma_f32 v102,v102,s89,s89
v_fma_f32 v103,v103,s89,s89
v_fma_f32 v104,v104,s89,s89
v_fma_f32 v105,v105,s89,s89
v_rcp_f32 v102,v102
v_rcp_f32 v103,v103
v_rcp_f32 v104,v104
v_rcp_f32 v105,v105
v_mul_f32 v26,v26,v102
v_mul_f32 v27,v27,v103
v_mul_f32 v28,v28,v104
v_mul_f32 v29,v29,v105
v_cvt_pk_bf16_f32 v110,v26,v27
v_cvt_pk_bf16_f32 v111,v28,v29
buffer_store_dwordx2 v[110:111],v10,s[36:39],s66 offen offset:0 sc0 sc1
s_mov_b32 s72,s69
s_mov_b32 s69,0x7fff0000
s_mov_b32 s66,0x7fff0000
s_mov_b32 s76,0x7fff0000
s_branch Lg3_gox_%=
Lg3_xn0_%=:
s_cmp_ge_u32 s54,256
s_cbranch_scc1 Lg3_fin0_%=
s_add_u32 m0,s59,4096
s_nop 0
buffer_load_dword v8,s[36:39],s58 offen offset:0 sc1 lds
buffer_store_dword v4,v3,s[36:39],s71 offen offset:0 sc0 sc1
s_add_u32 m0,s59,5120
s_nop 0
buffer_load_dword v8,s[36:39],s58 offen offset:0 sc1 lds
buffer_store_dword v4,v3,s[36:39],s71 offen offset:0 sc0 sc1
s_mov_b32 s83,0
L 0
v_fma_f32 v18,-v74,v82,v18
v_fma_f32 v19,-v74,v83,v19
v_fma_f32 v20,-v74,v84,v20
v_fma_f32 v21,-v74,v85,v21
v_mul_f32 v18,v75,v18
v_mul_f32 v19,v75,v19
v_mul_f32 v20,v75,v20
v_mul_f32 v21,v75,v21
v_fma_f32 v94,v18,v18,s88
v_fma_f32 v95,v19,v19,s88
v_fma_f32 v96,v20,v20,s88
v_fma_f32 v97,v21,v21,s88
v_mul_f32 v94,v18,v94
v_mul_f32 v95,v19,v95
v_mul_f32 v96,v20,v96
v_mul_f32 v97,v21,v97
v_exp_f32 v94,v94
v_exp_f32 v95,v95
v_exp_f32 v96,v96
v_exp_f32 v97,v97
v_fma_f32 v94,v94,s89,s89
v_fma_f32 v95,v95,s89,s89
v_fma_f32 v96,v96,s89,s89
v_fma_f32 v97,v97,s89,s89
v_rcp_f32 v94,v94
v_rcp_f32 v95,v95
v_rcp_f32 v96,v96
v_rcp_f32 v97,v97
v_mul_f32 v18,v18,v94
v_mul_f32 v19,v19,v95
v_mul_f32 v20,v20,v96
v_mul_f32 v21,v21,v97
v_cvt_pk_bf16_f32 v106,v18,v19
v_cvt_pk_bf16_f32 v107,v20,v21
v_fma_f32 v22,-v74,v86,v22
v_fma_f32 v23,-v74,v87,v23
v_fma_f32 v24,-v74,v88,v24
v_fma_f32 v25,-v74,v89,v25
v_mul_f32 v22,v75,v22
v_mul_f32 v23,v75,v23
v_mul_f32 v24,v75,v24
v_mul_f32 v25,v75,v25
v_fma_f32 v98,v22,v22,s88
v_fma_f32 v99,v23,v23,s88
v_fma_f32 v100,v24,v24,s88
v_fma_f32 v101,v25,v25,s88
v_mul_f32 v98,v22,v98
v_mul_f32 v99,v23,v99
v_mul_f32 v100,v24,v100
v_mul_f32 v101,v25,v101
v_exp_f32 v98,v98
v_exp_f32 v99,v99
v_exp_f32 v100,v100
v_exp_f32 v101,v101
v_fma_f32 v98,v98,s89,s89
v_fma_f32 v99,v99,s89,s89
v_fma_f32 v100,v100,s89,s89
v_fma_f32 v101,v101,s89,s89
v_rcp_f32 v98,v98
v_rcp_f32 v99,v99
v_rcp_f32 v100,v100
v_rcp_f32 v101,v101
v_mul_f32 v22,v22,v98
v_mul_f32 v23,v23,v99
v_mul_f32 v24,v24,v100
v_mul_f32 v25,v25,v101
v_cvt_pk_bf16_f32 v108,v22,v23
v_cvt_pk_bf16_f32 v109,v24,v25
buffer_store_dwordx4 v[106:109],v0,s[36:39],s66 offen offset:0 sc0 sc1
v_fma_f32 v26,-v74,v90,v26
v_fma_f32 v27,-v74,v91,v27
v_fma_f32 v28,-v74,v92,v28
v_fma_f32 v29,-v74,v93,v29
v_mul_f32 v26,v75,v26
v_mul_f32 v27,v75,v27
v_mul_f32 v28,v75,v28
v_mul_f32 v29,v75,v29
v_fma_f32 v102,v26,v26,s88
v_fma_f32 v103,v27,v27,s88
v_fma_f32 v104,v28,v28,s88
v_fma_f32 v105,v29,v29,s88
v_mul_f32 v102,v26,v102
v_mul_f32 v103,v27,v103
v_mul_f32 v104,v28,v104
v_mul_f32 v105,v29,v105
v_exp_f32 v102,v102
v_exp_f32 v103,v103
v_exp_f32 v104,v104
v_exp_f32 v105,v105
v_fma_f32 v102,v102,s89,s89
v_fma_f32 v103,v103,s89,s89
v_fma_f32 v104,v104,s89,s89
v_fma_f32 v105,v105,s89,s89
v_rcp_f32 v102,v102
v_rcp_f32 v103,v103
v_rcp_f32 v104,v104
v_rcp_f32 v105,v105
v_mul_f32 v26,v26,v102
v_mul_f32 v27,v27,v103
v_mul_f32 v28,v28,v104
v_mul_f32 v29,v29,v105
v_cvt_pk_bf16_f32 v110,v26,v27
v_cvt_pk_bf16_f32 v111,v28,v29
buffer_store_dwordx2 v[110:111],v10,s[36:39],s66 offen offset:0 sc0 sc1
s_mov_b32 s72,s69
s_mov_b32 s69,0x7fff0000
s_mov_b32 s66,0x7fff0000
s_mov_b32 s76,0x7fff0000
s_branch Lg3_poll_%=
Lg3_fin0_%=:
L 0
v_fma_f32 v18,-v74,v82,v18
v_fma_f32 v19,-v74,v83,v19
v_fma_f32 v20,-v74,v84,v20
v_fma_f32 v21,-v74,v85,v21
v_mul_f32 v18,v75,v18
v_mul_f32 v19,v75,v19
v_mul_f32 v20,v75,v20
v_mul_f32 v21,v75,v21
)"
R"(v_fma_f32 v94,v18,v18,s88
v_fma_f32 v95,v19,v19,s88
v_fma_f32 v96,v20,v20,s88
v_fma_f32 v97,v21,v21,s88
v_mul_f32 v94,v18,v94
v_mul_f32 v95,v19,v95
v_mul_f32 v96,v20,v96
v_mul_f32 v97,v21,v97
v_exp_f32 v94,v94
v_exp_f32 v95,v95
v_exp_f32 v96,v96
v_exp_f32 v97,v97
v_fma_f32 v94,v94,s89,s89
v_fma_f32 v95,v95,s89,s89
v_fma_f32 v96,v96,s89,s89
v_fma_f32 v97,v97,s89,s89
v_rcp_f32 v94,v94
v_rcp_f32 v95,v95
v_rcp_f32 v96,v96
v_rcp_f32 v97,v97
v_mul_f32 v18,v18,v94
v_mul_f32 v19,v19,v95
v_mul_f32 v20,v20,v96
v_mul_f32 v21,v21,v97
v_cvt_pk_bf16_f32 v106,v18,v19
v_cvt_pk_bf16_f32 v107,v20,v21
v_fma_f32 v22,-v74,v86,v22
v_fma_f32 v23,-v74,v87,v23
v_fma_f32 v24,-v74,v88,v24
v_fma_f32 v25,-v74,v89,v25
v_mul_f32 v22,v75,v22
v_mul_f32 v23,v75,v23
v_mul_f32 v24,v75,v24
v_mul_f32 v25,v75,v25
v_fma_f32 v98,v22,v22,s88
v_fma_f32 v99,v23,v23,s88
v_fma_f32 v100,v24,v24,s88
v_fma_f32 v101,v25,v25,s88
v_mul_f32 v98,v22,v98
v_mul_f32 v99,v23,v99
v_mul_f32 v100,v24,v100
v_mul_f32 v101,v25,v101
v_exp_f32 v98,v98
v_exp_f32 v99,v99
v_exp_f32 v100,v100
v_exp_f32 v101,v101
v_fma_f32 v98,v98,s89,s89
v_fma_f32 v99,v99,s89,s89
v_fma_f32 v100,v100,s89,s89
v_fma_f32 v101,v101,s89,s89
v_rcp_f32 v98,v98
v_rcp_f32 v99,v99
v_rcp_f32 v100,v100
v_rcp_f32 v101,v101
v_mul_f32 v22,v22,v98
v_mul_f32 v23,v23,v99
v_mul_f32 v24,v24,v100
v_mul_f32 v25,v25,v101
v_cvt_pk_bf16_f32 v108,v22,v23
v_cvt_pk_bf16_f32 v109,v24,v25
buffer_store_dwordx4 v[106:109],v0,s[36:39],s66 offen offset:0 sc0 sc1
v_fma_f32 v26,-v74,v90,v26
v_fma_f32 v27,-v74,v91,v27
v_fma_f32 v28,-v74,v92,v28
v_fma_f32 v29,-v74,v93,v29
v_mul_f32 v26,v75,v26
v_mul_f32 v27,v75,v27
v_mul_f32 v28,v75,v28
v_mul_f32 v29,v75,v29
v_fma_f32 v102,v26,v26,s88
v_fma_f32 v103,v27,v27,s88
v_fma_f32 v104,v28,v28,s88
v_fma_f32 v105,v29,v29,s88
v_mul_f32 v102,v26,v102
v_mul_f32 v103,v27,v103
v_mul_f32 v104,v28,v104
v_mul_f32 v105,v29,v105
v_exp_f32 v102,v102
v_exp_f32 v103,v103
v_exp_f32 v104,v104
v_exp_f32 v105,v105
v_fma_f32 v102,v102,s89,s89
v_fma_f32 v103,v103,s89,s89
v_fma_f32 v104,v104,s89,s89
v_fma_f32 v105,v105,s89,s89
v_rcp_f32 v102,v102
v_rcp_f32 v103,v103
v_rcp_f32 v104,v104
v_rcp_f32 v105,v105
v_mul_f32 v26,v26,v102
v_mul_f32 v27,v27,v103
v_mul_f32 v28,v28,v104
v_mul_f32 v29,v29,v105
v_cvt_pk_bf16_f32 v110,v26,v27
v_cvt_pk_bf16_f32 v111,v28,v29
buffer_store_dwordx2 v[110:111],v10,s[36:39],s66 offen offset:0 sc0 sc1
s_mov_b32 s72,s69
s_mov_b32 s69,0x7fff0000
s_mov_b32 s66,0x7fff0000
s_mov_b32 s76,0x7fff0000
s_branch Lg3_done_%=
Lg3_K1_%=:
Lg3_L1_%=:
v_mfma_f32_16x16x32_bf16 v[38:41],a[8:11],v[42:45],0
s_add_u32 m0,s59,3072
R 62,2,5120
v_add_f32_dpp v115,v114,v114 quad_perm:[1,0,3,2] row_mask:0xf bank_mask:0xf
M 38,a[20:23],46
buffer_load_dword v8,s[36:39],s58 offen offset:0 sc1 lds
v_mfma_f32_16x16x32_bf16 v[34:37],a[4:7],v[42:45],0
s_mov_b32 s67,s65
v_add_f32_dpp v115,v115,v115 quad_perm:[2,3,0,1] row_mask:0xf bank_mask:0xf
v_fma_f32 v18,-v74,v82,v18
M 34,a[16:19],46
s_add_u32 s65,s65,s62
R 66,2,6144
v_fma_f32 v19,-v74,v83,v19
v_mfma_f32_16x16x32_bf16 v[30:33],a[0:3],v[42:45],0
v_add_f32_dpp v118,v115,v115 row_half_mirror row_mask:0xf bank_mask:0xf
v_fma_f32 v20,-v74,v84,v20
v_fma_f32 v21,-v74,v85,v21
M 30,a[12:15],46
L 3
v_mul_f32 v18,v75,v18
v_mul_f32 v19,v75,v19
M 30,a[24:27],50
R 70,2,7168
v_mov_b32_dpp v115,v118 row_ror:8 row_mask:0xf bank_mask:0xf
M 30,a[36:39],54
v_fma_f32 v115,-v118,v118,v115
v_max_f32 v115,s87,v115
M 34,a[28:31],50
v_rsq_f32 v116,v115
M 34,a[40:43],54
R 42,2,8192
v_mul_f32 v118,v15,v118
v_fma_f32 v118,v116,v16,v118
M 38,a[32:35],50
ds_write_b32 v12,v118 offset:2048
M 38,a[44:47],54
L 4
v_mul_f32 v20,v75,v20
v_mul_f32 v21,v75,v21
M 38,a[56:59],58
R 46,2,9216
v_fma_f32 v94,v18,v18,s88
M 38,a[68:71],62
v_fma_f32 v95,v19,v19,s88
v_fma_f32 v96,v20,v20,s88
M 34,a[52:55],58
v_fma_f32 v97,v21,v21,s88
v_mul_f32 v94,v18,v94
v_mul_f32 v95,v19,v95
M 34,a[64:67],62
R 50,2,10240
v_mul_f32 v96,v20,v96
M 30,a[48:51],58
v_exp_f32 v94,v94
M 30,a[60:63],62
L 4
v_exp_f32 v95,v95
M 30,a[72:75],66
R 54,2,11264
v_mul_f32 v97,v21,v97
M 30,a[84:87],70
v_exp_f32 v96,v96
v_fma_f32 v94,v94,s89,s89
M 34,a[76:79],66
v_exp_f32 v97,v97
M 34,a[88:91],70
R 58,2,12288
v_fma_f32 v95,v95,s89,s89
M 38,a[80:83],66
v_rcp_f32 v94,v94
M 38,a[92:95],70
L 3
v_rcp_f32 v95,v95
M 38,a[104:107],42
R 62,2,13312
v_fma_f32 v96,v96,s89,s89
v_fma_f32 v97,v97,s89,s89
M 38,a[116:119],46
v_rcp_f32 v96,v96
M 34,a[100:103],42
v_rcp_f32 v97,v97
M 34,a[112:115],46
R 66,2,14336
v_mul_f32 v18,v18,v94
M 30,a[96:99],42
v_mul_f32 v19,v19,v95
v_mul_f32 v20,v20,v96
M 30,a[108:111],46
L 3
v_mul_f32 v21,v21,v97
v_cvt_pk_bf16_f32 v106,v18,v19
v_cvt_pk_bf16_f32 v107,v20,v21
M 30,a[120:123],50
R 70,2,15360
v_fma_f32 v22,-v74,v86,v22
M 30,a[132:135],54
s_waitcnt vmcnt(0)
v_fma_f32 v23,-v74,v87,v23
v_fma_f32 v24,-v74,v88,v24
M 34,a[124:127],50
s_barrier
ds_read_b32 v7,v6 offset:3072
v_fma_f32 v25,-v74,v89,v25
M 34,a[136:139],54
R 42,2,16384
v_mul_f32 v22,v75,v22
M 38,a[128:131],50
buffer_store_dword v4,v3,s[36:39],s70 offen offset:0 sc0 sc1
v_mul_f32 v23,v75,v23
M 38,a[140:143],54
L 4
v_mul_f32 v24,v75,v24
v_mul_f32 v25,v75,v25
M 38,a[152:155],58
s_mov_b32 s70,0x7fff0000
R 46,2,17408
v_fma_f32 v98,v22,v22,s88
M 38,a[164:167],62
v_fma_f32 v99,v23,v23,s88
v_fma_f32 v100,v24,v24,s88
M 34,a[148:151],58
L 2
v_cmp_eq_u32 vcc,s53,v7
v_fma_f32 v101,v25,v25,s88
M 34,a[160:163],62
R 50,2,18432
v_mul_f32 v98,v22,v98
v_mul_f32 v99,v23,v99
M 30,a[144:147],58
v_exp_f32 v98,v98
M 30,a[156:159],62
s_cmp_eq_u32 vcc_lo,-1
s_nop 0
v_mul_f32 v100,v24,v100
s_cbranch_scc0 Lg3_X1_%=
M 30,a[168:171],66
s_add_u32 m0,s57,0
s_add_u32 s56,s55,0
s_add_u32 s58,s58,s61
R 54,2,19456
M 30,a[180:183],70
buffer_load_dword v114,v11,s[36:39],s73 offen offset:0 sc1
M 34,a[172:175],66
D 0,56,0
v_mul_f32 v101,v25,v101
M 34,a[184:187],70
s_add_u32 s54,s54,s46
R 58,2,20480
v_fma_f32 v98,v98,s89,s89
M 38,a[176:179],66
s_add_u32 s73,s73,s64
v_exp_f32 v99,v99
M 38,a[188:191],70
L 3
v_exp_f32 v100,v100
M 38,a[200:203],42
R 62,2,21504
v_fma_f32 v99,v99,s89,s89
M 38,a[212:215],46
D 0,56,1024
v_fma_f32 v100,v100,s89,s89
M 34,a[196:199],42
v_exp_f32 v101,v101
M 34,a[208:211],46
R 66,2,22528
v_fma_f32 v101,v101,s89,s89
M 30,a[192:195],42
D 0,56,2048
M 30,a[204:207],46
L 3
v_rcp_f32 v98,v98
M 30,a[216:219],50
R 70,2,23552
v_rcp_f32 v99,v99
M 30,a[228:231],54
v_rcp_f32 v100,v100
M 34,a[220:223],50
D 0,56,3072
M 34,a[232:235],54
s_add_u32 m0,s59,1024
R 42,2,24576
v_mul_f32 v22,v22,v98
M 38,a[224:227],50
buffer_load_dword v8,s[36:39],s58 offen offset:0 sc1 lds
M 38,a[236:239],54
L 3
v_rcp_f32 v101,v101
v_mul_f32 v23,v23,v99
M 38,a[248:251],58
s_add_u32 m0,s57,16384
R 46,2,25600
v_mul_f32 v24,v24,v100
M 38,v[132:135],62
s_add_u32 s56,s55,16384
v_mul_f32 v25,v25,v101
v_cvt_pk_bf16_f32 v108,v22,v23
M 34,a[244:247],58
D 0,56,0
M 34,v[128:131],62
s_add_u32 s55,s55,s60
R 50,2,26624
v_cvt_pk_bf16_f32 v109,v24,v25
M 30,a[240:243],58
buffer_store_dwordx4 v[106:109],v0,s[36:39],s66 offen offset:0 sc0 sc1
v_fma_f32 v26,-v74,v90,v26
M 30,a[252:255],62
L 3
v_fma_f32 v27,-v74,v91,v27
v_fma_f32 v28,-v74,v92,v28
M 30,v[136:139],66
s_sub_u32 s57,s84,s57
R 54,2,27648
v_fma_f32 v29,-v74,v93,v29
M 30,v[148:151],70
D 0,56,1024
M 34,v[140:143],66
v_mul_f32 v26,v75,v26
v_mul_f32 v27,v75,v27
M 34,v[152:155],70
R 58,2,28672
v_mul_f32 v28,v75,v28
v_mul_f32 v29,v75,v29
M 38,v[144:147],66
v_fma_f32 v102,v26,v26,s88
v_fma_f32 v103,v27,v27,s88
M 38,v[156:159],70
L 3
v_fma_f32 v104,v28,v28,s88
v_fma_f32 v105,v29,v29,s88
M 38,v[168:171],42
R 62,2,29696
v_mul_f32 v102,v26,v102
M 38,v[180:183],46
D 0,56,2048
M 34,v[164:167],42
v_exp_f32 v102,v102
v_mul_f32 v103,v27,v103
M 34,v[176:179],46
R 66,2,30720
v_mul_f32 v104,v28,v104
M 30,v[160:163],42
v_exp_f32 v103,v103
M 30,v[172:175],46
L 3
D 0,56,3072
M 30,v[184:187],50
s_nop 0
R 70,2,31744
v_mul_f32 v105,v29,v105
M 30,v[196:199],54
s_waitcnt vmcnt(5)
v_exp_f32 v104,v104
v_fma_f32 v102,v102,s89,s89
M 34,v[188:191],50
s_barrier
buffer_store_dword v4,v3,s[36:39],s70 offen offset:0 sc0 sc1
M 34,v[200:203],54
s_mov_b32 s70,s69
R 42,1,0
ds_read_b32 v7,v6 offset:1024
M 38,v[192:195],50
s_mov_b32 s69,s68
ds_read_b64 v[78:79],v13 offset:2048
v_fma_f32 v103,v103,s89,s89
M 38,v[204:207],54
L 5
v_exp_f32 v105,v105
M 38,v[216:219],58
s_add_u32 s68,s68,s63
R 46,1,1024
v_rcp_f32 v102,v102
M 38,v[228:231],62
v_rcp_f32 v103,v103
M 34,v[212:215],58
v_fma_f32 v104,v104,s89,s89
v_fma_f32 v105,v105,s89,s89
M 34,v[224:227],62
L 2
R 50,1,2048
v_mul_f32 v26,v26,v102
M 30,v[208:211],58
v_rcp_f32 v104,v104
M 30,v[220:223],62
s_nop 0
v_rcp_f32 v105,v105
v_mul_f32 v27,v27,v103
M 30,v[232:235],66
R 54,1,3072
v_mul_f32 v28,v28,v104
M 30,v[244:247],70
v_mul_f32 v29,v29,v105
v_cmp_eq_u32 vcc,s53,v7
M 34,v[236:239],66
v_cvt_pk_bf16_f32 v110,v26,v27
v_cvt_pk_bf16_f32 v111,v28,v29
M 34,v[248:251],70
s_cmp_eq_u32 vcc_lo,-1
R 58,1,4096
M 38,v[240:243],66
L 3
buffer_store_dwordx2 v[110:111],v10,s[36:39],s66 offen offset:0 sc0 sc1
M 38,v[252:255],70
buffer_store_dword v4,v3,s[36:39],s71 offen offset:0 sc0 sc1
s_cbranch_scc1 Lg3_F0_%=
s_branch Lg3_K0_%=
Lg3_X1_%=:
M 30,a[168:171],66
s_add_u32 m0,s59,1024
R 54,2,19456
v_mul_f32 v101,v25,v101
M 30,a[180:183],70
buffer_load_dword v8,s[36:39],s58 offen offset:0 sc1 lds
M 34,a[172:175],66
v_exp_f32 v99,v99
M 34,a[184:187],70
R 58,2,20480
v_fma_f32 v98,v98,s89,s89
M 38,a[176:179],66
v_exp_f32 v100,v100
M 38,a[188:191],70
L 3
v_exp_f32 v101,v101
M 38,a[200:203],42
R 62,2,21504
v_fma_f32 v99,v99,s89,s89
M 38,a[212:215],46
v_rcp_f32 v98,v98
M 34,a[196:199],42
v_rcp_f32 v99,v99
M 34,a[208:211],46
R 66,2,22528
v_fma_f32 v100,v100,s89,s89
M 30,a[192:195],42
v_rcp_f32 v100,v100
M 30,a[204:207],46
L 3
v_fma_f32 v101,v101,s89,s89
v_mul_f32 v22,v22,v98
M 30,a[216:219],50
R 70,2,23552
v_mul_f32 v23,v23,v99
M 30,a[228:231],54
v_rcp_f32 v101,v101
M 34,a[220:223],50
v_mul_f32 v24,v24,v100
v_mul_f32 v25,v25,v101
M 34,a[232:235],54
R 42,2,24576
v_cvt_pk_bf16_f32 v108,v22,v23
M 38,a[224:227],50
v_cvt_pk_bf16_f32 v109,v24,v25
v_fma_f32 v26,-v74,v90,v26
M 38,a[236:239],54
L 3
buffer_store_dwordx4 v[106:109],v0,s[36:39],s66 offen offset:0 sc0 sc1
v_fma_f32 v27,-v74,v91,v27
M 38,a[248:251],58
R 46,2,25600
v_fma_f32 v28,-v74,v92,v28
M 38,v[132:135],62
v_fma_f32 v29,-v74,v93,v29
v_mul_f32 v26,v75,v26
M 34,a[244:247],58
v_mul_f32 v27,v75,v27
v_mul_f32 v28,v75,v28
M 34,v[128:131],62
R 50,2,26624
v_mul_f32 v29,v75,v29
M 30,a[240:243],58
v_fma_f32 v102,v26,v26,s88
v_fma_f32 v103,v27,v27,s88
M 30,a[252:255],62
L 3
v_fma_f32 v104,v28,v28,s88
v_fma_f32 v105,v29,v29,s88
M 30,v[136:139],66
R 54,2,27648
v_mul_f32 v102,v26,v102
M 30,v[148:151],70
v_exp_f32 v102,v102
M 34,v[140:143],66
v_mul_f32 v103,v27,v103
v_mul_f32 v104,v28,v104
M 34,v[152:155],70
R 58,2,28672
v_mul_f32 v105,v29,v105
M 38,v[144:147],66
v_exp_f32 v103,v103
M 38,v[156:159],70
L 3
v_exp_f32 v104,v104
M 38,v[168:171],42
R 62,2,29696
v_fma_f32 v102,v102,s89,s89
M 38,v[180:183],46
v_exp_f32 v105,v105
)"
R"(M 34,v[164:167],42
v_rcp_f32 v102,v102
M 34,v[176:179],46
R 66,2,30720
v_fma_f32 v103,v103,s89,s89
M 30,v[160:163],42
v_rcp_f32 v103,v103
M 30,v[172:175],46
L 3
v_fma_f32 v104,v104,s89,s89
v_fma_f32 v105,v105,s89,s89
M 30,v[184:187],50
s_nop 0
R 70,2,31744
v_mul_f32 v26,v26,v102
M 30,v[196:199],54
s_waitcnt vmcnt(1)
v_rcp_f32 v104,v104
M 34,v[188:191],50
s_barrier
buffer_store_dword v4,v3,s[36:39],s70 offen offset:0 sc0 sc1
M 34,v[200:203],54
s_mov_b32 s70,s69
ds_read_b32 v7,v6 offset:1024
ds_read_b64 v[78:79],v13 offset:2048
M 38,v[192:195],50
s_mov_b32 s69,s68
v_rcp_f32 v105,v105
M 38,v[204:207],54
L 4
v_mul_f32 v27,v27,v103
v_mul_f32 v28,v28,v104
M 38,v[216:219],58
s_add_u32 s68,s68,s63
v_mul_f32 v29,v29,v105
v_cvt_pk_bf16_f32 v110,v26,v27
M 38,v[228:231],62
v_cvt_pk_bf16_f32 v111,v28,v29
M 34,v[212:215],58
buffer_store_dwordx2 v[110:111],v10,s[36:39],s66 offen offset:0 sc0 sc1
M 34,v[224:227],62
L 1
v_cmp_eq_u32 vcc,s53,v7
M 30,v[208:211],58
M 30,v[220:223],62
s_nop 0
M 30,v[232:235],66
M 30,v[244:247],70
M 34,v[236:239],66
M 34,v[248:251],70
M 38,v[240:243],66
M 38,v[252:255],70
s_mov_b32 s80,0
s_cmp_eq_u32 vcc_lo,-1
s_cbranch_scc0 Lg3_xn1_%=
buffer_load_dword v114,v11,s[36:39],s73 offen offset:0 sc1
L 0
v_fma_f32 v30,-v78,v82,v30
v_fma_f32 v31,-v78,v83,v31
v_fma_f32 v32,-v78,v84,v32
v_fma_f32 v33,-v78,v85,v33
v_mul_f32 v30,v79,v30
v_mul_f32 v31,v79,v31
v_mul_f32 v32,v79,v32
v_mul_f32 v33,v79,v33
s_add_u32 s73,s73,s64
s_add_u32 m0,s57,0
s_add_u32 s56,s55,0
D 0,56,0
v_fma_f32 v94,v30,v30,s88
v_fma_f32 v95,v31,v31,s88
v_fma_f32 v96,v32,v32,s88
v_fma_f32 v97,v33,v33,s88
v_mul_f32 v94,v30,v94
v_mul_f32 v95,v31,v95
v_mul_f32 v96,v32,v96
v_mul_f32 v97,v33,v97
D 0,56,1024
v_exp_f32 v94,v94
v_exp_f32 v95,v95
v_exp_f32 v96,v96
v_exp_f32 v97,v97
v_fma_f32 v94,v94,s89,s89
v_fma_f32 v95,v95,s89,s89
v_fma_f32 v96,v96,s89,s89
v_fma_f32 v97,v97,s89,s89
D 0,56,2048
v_rcp_f32 v94,v94
v_rcp_f32 v95,v95
v_rcp_f32 v96,v96
v_rcp_f32 v97,v97
v_mul_f32 v30,v30,v94
v_mul_f32 v31,v31,v95
v_mul_f32 v32,v32,v96
v_mul_f32 v33,v33,v97
D 0,56,3072
v_cvt_pk_bf16_f32 v106,v30,v31
v_cvt_pk_bf16_f32 v107,v32,v33
v_fma_f32 v34,-v78,v86,v34
v_fma_f32 v35,-v78,v87,v35
v_fma_f32 v36,-v78,v88,v36
v_fma_f32 v37,-v78,v89,v37
v_mul_f32 v34,v79,v34
v_mul_f32 v35,v79,v35
s_add_u32 s58,s58,s61
s_add_u32 m0,s57,16384
s_add_u32 s56,s55,16384
D 0,56,0
v_mul_f32 v36,v79,v36
v_mul_f32 v37,v79,v37
v_fma_f32 v98,v34,v34,s88
v_fma_f32 v99,v35,v35,s88
v_fma_f32 v100,v36,v36,s88
v_fma_f32 v101,v37,v37,s88
v_mul_f32 v98,v34,v98
v_mul_f32 v99,v35,v99
D 0,56,1024
v_mul_f32 v100,v36,v100
v_mul_f32 v101,v37,v101
v_exp_f32 v98,v98
v_exp_f32 v99,v99
v_exp_f32 v100,v100
v_exp_f32 v101,v101
v_fma_f32 v98,v98,s89,s89
v_fma_f32 v99,v99,s89,s89
D 0,56,2048
v_fma_f32 v100,v100,s89,s89
v_fma_f32 v101,v101,s89,s89
v_rcp_f32 v98,v98
v_rcp_f32 v99,v99
v_rcp_f32 v100,v100
v_rcp_f32 v101,v101
v_mul_f32 v34,v34,v98
v_mul_f32 v35,v35,v99
D 0,56,3072
v_mul_f32 v36,v36,v100
v_mul_f32 v37,v37,v101
v_cvt_pk_bf16_f32 v108,v34,v35
v_cvt_pk_bf16_f32 v109,v36,v37
buffer_store_dwordx4 v[106:109],v0,s[36:39],s67 offen offset:0 sc0 sc1
v_fma_f32 v38,-v78,v90,v38
v_fma_f32 v39,-v78,v91,v39
v_fma_f32 v40,-v78,v92,v40
v_fma_f32 v41,-v78,v93,v41
s_add_u32 s55,s55,s60
s_sub_u32 s57,s84,s57
s_add_u32 s54,s54,s46
v_mul_f32 v38,v79,v38
v_mul_f32 v39,v79,v39
v_mul_f32 v40,v79,v40
v_mul_f32 v41,v79,v41
v_fma_f32 v102,v38,v38,s88
v_fma_f32 v103,v39,v39,s88
v_fma_f32 v104,v40,v40,s88
v_fma_f32 v105,v41,v41,s88
v_mul_f32 v102,v38,v102
v_mul_f32 v103,v39,v103
v_mul_f32 v104,v40,v104
v_mul_f32 v105,v41,v105
v_exp_f32 v102,v102
v_exp_f32 v103,v103
v_exp_f32 v104,v104
v_exp_f32 v105,v105
v_fma_f32 v102,v102,s89,s89
v_fma_f32 v103,v103,s89,s89
v_fma_f32 v104,v104,s89,s89
v_fma_f32 v105,v105,s89,s89
v_rcp_f32 v102,v102
v_rcp_f32 v103,v103
v_rcp_f32 v104,v104
v_rcp_f32 v105,v105
v_mul_f32 v38,v38,v102
v_mul_f32 v39,v39,v103
v_mul_f32 v40,v40,v104
v_mul_f32 v41,v41,v105
v_cvt_pk_bf16_f32 v110,v38,v39
v_cvt_pk_bf16_f32 v111,v40,v41
buffer_store_dwordx2 v[110:111],v10,s[36:39],s67 offen offset:0 sc0 sc1
s_mov_b32 s72,s69
s_mov_b32 s69,0x7fff0000
s_mov_b32 s67,0x7fff0000
s_mov_b32 s77,0x7fff0000
s_branch Lg3_gox_%=
Lg3_xn1_%=:
s_cmp_ge_u32 s54,256
s_cbranch_scc1 Lg3_fin1_%=
s_add_u32 m0,s59,4096
s_nop 0
buffer_load_dword v8,s[36:39],s58 offen offset:0 sc1 lds
buffer_store_dword v4,v3,s[36:39],s71 offen offset:0 sc0 sc1
s_add_u32 m0,s59,5120
s_nop 0
buffer_load_dword v8,s[36:39],s58 offen offset:0 sc1 lds
buffer_store_dword v4,v3,s[36:39],s71 offen offset:0 sc0 sc1
s_mov_b32 s83,0
L 0
v_fma_f32 v30,-v78,v82,v30
v_fma_f32 v31,-v78,v83,v31
v_fma_f32 v32,-v78,v84,v32
v_fma_f32 v33,-v78,v85,v33
v_mul_f32 v30,v79,v30
v_mul_f32 v31,v79,v31
v_mul_f32 v32,v79,v32
v_mul_f32 v33,v79,v33
v_fma_f32 v94,v30,v30,s88
v_fma_f32 v95,v31,v31,s88
v_fma_f32 v96,v32,v32,s88
v_fma_f32 v97,v33,v33,s88
v_mul_f32 v94,v30,v94
v_mul_f32 v95,v31,v95
v_mul_f32 v96,v32,v96
v_mul_f32 v97,v33,v97
v_exp_f32 v94,v94
v_exp_f32 v95,v95
v_exp_f32 v96,v96
v_exp_f32 v97,v97
v_fma_f32 v94,v94,s89,s89
v_fma_f32 v95,v95,s89,s89
v_fma_f32 v96,v96,s89,s89
v_fma_f32 v97,v97,s89,s89
v_rcp_f32 v94,v94
v_rcp_f32 v95,v95
v_rcp_f32 v96,v96
v_rcp_f32 v97,v97
v_mul_f32 v30,v30,v94
v_mul_f32 v31,v31,v95
v_mul_f32 v32,v32,v96
v_mul_f32 v33,v33,v97
v_cvt_pk_bf16_f32 v106,v30,v31
v_cvt_pk_bf16_f32 v107,v32,v33
v_fma_f32 v34,-v78,v86,v34
v_fma_f32 v35,-v78,v87,v35
v_fma_f32 v36,-v78,v88,v36
v_fma_f32 v37,-v78,v89,v37
v_mul_f32 v34,v79,v34
v_mul_f32 v35,v79,v35
v_mul_f32 v36,v79,v36
v_mul_f32 v37,v79,v37
v_fma_f32 v98,v34,v34,s88
v_fma_f32 v99,v35,v35,s88
v_fma_f32 v100,v36,v36,s88
v_fma_f32 v101,v37,v37,s88
v_mul_f32 v98,v34,v98
v_mul_f32 v99,v35,v99
v_mul_f32 v100,v36,v100
v_mul_f32 v101,v37,v101
v_exp_f32 v98,v98
v_exp_f32 v99,v99
v_exp_f32 v100,v100
v_exp_f32 v101,v101
v_fma_f32 v98,v98,s89,s89
v_fma_f32 v99,v99,s89,s89
v_fma_f32 v100,v100,s89,s89
v_fma_f32 v101,v101,s89,s89
v_rcp_f32 v98,v98
v_rcp_f32 v99,v99
v_rcp_f32 v100,v100
v_rcp_f32 v101,v101
v_mul_f32 v34,v34,v98
v_mul_f32 v35,v35,v99
v_mul_f32 v36,v36,v100
v_mul_f32 v37,v37,v101
v_cvt_pk_bf16_f32 v108,v34,v35
v_cvt_pk_bf16_f32 v109,v36,v37
buffer_store_dwordx4 v[106:109],v0,s[36:39],s67 offen offset:0 sc0 sc1
v_fma_f32 v38,-v78,v90,v38
v_fma_f32 v39,-v78,v91,v39
v_fma_f32 v40,-v78,v92,v40
v_fma_f32 v41,-v78,v93,v41
v_mul_f32 v38,v79,v38
v_mul_f32 v39,v79,v39
v_mul_f32 v40,v79,v40
v_mul_f32 v41,v79,v41
v_fma_f32 v102,v38,v38,s88
v_fma_f32 v103,v39,v39,s88
v_fma_f32 v104,v40,v40,s88
v_fma_f32 v105,v41,v41,s88
v_mul_f32 v102,v38,v102
v_mul_f32 v103,v39,v103
v_mul_f32 v104,v40,v104
v_mul_f32 v105,v41,v105
v_exp_f32 v102,v102
v_exp_f32 v103,v103
v_exp_f32 v104,v104
v_exp_f32 v105,v105
v_fma_f32 v102,v102,s89,s89
v_fma_f32 v103,v103,s89,s89
v_fma_f32 v104,v104,s89,s89
v_fma_f32 v105,v105,s89,s89
v_rcp_f32 v102,v102
v_rcp_f32 v103,v103
v_rcp_f32 v104,v104
v_rcp_f32 v105,v105
v_mul_f32 v38,v38,v102
v_mul_f32 v39,v39,v103
v_mul_f32 v40,v40,v104
v_mul_f32 v41,v41,v105
v_cvt_pk_bf16_f32 v110,v38,v39
v_cvt_pk_bf16_f32 v111,v40,v41
buffer_store_dwordx2 v[110:111],v10,s[36:39],s67 offen offset:0 sc0 sc1
s_mov_b32 s72,s69
s_mov_b32 s69,0x7fff0000
s_mov_b32 s67,0x7fff0000
s_mov_b32 s77,0x7fff0000
s_branch Lg3_poll_%=
Lg3_fin1_%=:
L 0
v_fma_f32 v30,-v78,v82,v30
v_fma_f32 v31,-v78,v83,v31
v_fma_f32 v32,-v78,v84,v32
v_fma_f32 v33,-v78,v85,v33
v_mul_f32 v30,v79,v30
v_mul_f32 v31,v79,v31
v_mul_f32 v32,v79,v32
v_mul_f32 v33,v79,v33
v_fma_f32 v94,v30,v30,s88
v_fma_f32 v95,v31,v31,s88
v_fma_f32 v96,v32,v32,s88
v_fma_f32 v97,v33,v33,s88
v_mul_f32 v94,v30,v94
v_mul_f32 v95,v31,v95
v_mul_f32 v96,v32,v96
v_mul_f32 v97,v33,v97
v_exp_f32 v94,v94
v_exp_f32 v95,v95
v_exp_f32 v96,v96
v_exp_f32 v97,v97
v_fma_f32 v94,v94,s89,s89
v_fma_f32 v95,v95,s89,s89
v_fma_f32 v96,v96,s89,s89
v_fma_f32 v97,v97,s89,s89
v_rcp_f32 v94,v94
v_rcp_f32 v95,v95
v_rcp_f32 v96,v96
v_rcp_f32 v97,v97
v_mul_f32 v30,v30,v94
v_mul_f32 v31,v31,v95
v_mul_f32 v32,v32,v96
v_mul_f32 v33,v33,v97
v_cvt_pk_bf16_f32 v106,v30,v31
v_cvt_pk_bf16_f32 v107,v32,v33
v_fma_f32 v34,-v78,v86,v34
v_fma_f32 v35,-v78,v87,v35
v_fma_f32 v36,-v78,v88,v36
v_fma_f32 v37,-v78,v89,v37
v_mul_f32 v34,v79,v34
v_mul_f32 v35,v79,v35
v_mul_f32 v36,v79,v36
v_mul_f32 v37,v79,v37
v_fma_f32 v98,v34,v34,s88
v_fma_f32 v99,v35,v35,s88
v_fma_f32 v100,v36,v36,s88
v_fma_f32 v101,v37,v37,s88
v_mul_f32 v98,v34,v98
v_mul_f32 v99,v35,v99
v_mul_f32 v100,v36,v100
v_mul_f32 v101,v37,v101
v_exp_f32 v98,v98
v_exp_f32 v99,v99
v_exp_f32 v100,v100
v_exp_f32 v101,v101
v_fma_f32 v98,v98,s89,s89
v_fma_f32 v99,v99,s89,s89
v_fma_f32 v100,v100,s89,s89
v_fma_f32 v101,v101,s89,s89
v_rcp_f32 v98,v98
v_rcp_f32 v99,v99
v_rcp_f32 v100,v100
v_rcp_f32 v101,v101
v_mul_f32 v34,v34,v98
v_mul_f32 v35,v35,v99
v_mul_f32 v36,v36,v100
v_mul_f32 v37,v37,v101
v_cvt_pk_bf16_f32 v108,v34,v35
v_cvt_pk_bf16_f32 v109,v36,v37
buffer_store_dwordx4 v[106:109],v0,s[36:39],s67 offen offset:0 sc0 sc1
v_fma_f32 v38,-v78,v90,v38
v_fma_f32 v39,-v78,v91,v39
v_fma_f32 v40,-v78,v92,v40
v_fma_f32 v41,-v78,v93,v41
v_mul_f32 v38,v79,v38
v_mul_f32 v39,v79,v39
v_mul_f32 v40,v79,v40
v_mul_f32 v41,v79,v41
v_fma_f32 v102,v38,v38,s88
v_fma_f32 v103,v39,v39,s88
v_fma_f32 v104,v40,v40,s88
v_fma_f32 v105,v41,v41,s88
v_mul_f32 v102,v38,v102
v_mul_f32 v103,v39,v103
v_mul_f32 v104,v40,v104
v_mul_f32 v105,v41,v105
v_exp_f32 v102,v102
v_exp_f32 v103,v103
v_exp_f32 v104,v104
v_exp_f32 v105,v105
v_fma_f32 v102,v102,s89,s89
v_fma_f32 v103,v103,s89,s89
v_fma_f32 v104,v104,s89,s89
v_fma_f32 v105,v105,s89,s89
v_rcp_f32 v102,v102
v_rcp_f32 v103,v103
v_rcp_f32 v104,v104
v_rcp_f32 v105,v105
v_mul_f32 v38,v38,v102
v_mul_f32 v39,v39,v103
v_mul_f32 v40,v40,v104
v_mul_f32 v41,v41,v105
v_cvt_pk_bf16_f32 v110,v38,v39
v_cvt_pk_bf16_f32 v111,v40,v41
buffer_store_dwordx2 v[110:111],v10,s[36:39],s67 offen offset:0 sc0 sc1
s_mov_b32 s72,s69
s_mov_b32 s69,0x7fff0000
s_mov_b32 s67,0x7fff0000
s_mov_b32 s77,0x7fff0000
s_branch Lg3_done_%=
Lg3_done_%=:
s_waitcnt vmcnt(2)
s_barrier
buffer_store_dword v4,v3,s[36:39],s70 offen offset:0 sc0 sc1
s_waitcnt vmcnt(1)
s_barrier
buffer_store_dword v4,v3,s[36:39],s72 offen offset:0 sc0 sc1
s_waitcnt vmcnt(0) lgkmcnt(0)
s_barrier
s_endpgm
Lcode_g3_e:
.purgem M
.purgem R
.purgem T
.purgem D
.purgem L
)"
  :: "s"(ws), "s"(__builtin_amdgcn_readfirstlane(sWave)), "s"(__builtin_amdgcn_readfirstlane(sG0)), "s"(__builtin_amdgcn_readfirstlane(sStep)), "s"(__builtin_amdgcn_readfirstlane(sOutOff)), "s"(__builtin_amdgcn_readfirstlane(sFoutOff)), "s"(__builtin_amdgcn_readfirstlane(sAuxOff)), "s"(__builtin_amdgcn_readfirstlane(sWw)), "s"(__builtin_amdgcn_readfirstlane(sBuf)), "s"(__builtin_amdgcn_readfirstlane(sExp)), "v"(vL16), "v"(vFlag), "v"(vSnapOff), "v"(vSnapRd), "v"(vWd0), "v"(vWd1), "v"(vWr0), "v"(vWr1), "v"(vWr2), "v"(vOutS), "v"(vAux0), "v"(vAux1), "v"(vAux2), "v"(vAux3), "v"(vAn), "v"(vAe), "s"(w3), "s"(out) : "v255", "v0", "v1", "v2", "v3", "v4", "v5", "v6", "v7", "v8", "v9", "v10", "v11", "v12", "v13", "v14", "v15", "v16", "v17", "v18", "v19", "v20", "v21", "v22", "v23", "v24", "v25", "v26", "v27", "v28", "v29", "v30", "v31", "v32", "v33", "v34", "v35", "v36", "v37", "v38", "v39", "v40", "v41", "v42", "v43", "v44", "v45", "v46", "v47", "v48", "v49", "v50", "v51", "v52", "v53", "v54", "v55", "v56", "v57", "v58", "v59", "v60", "v61", "v62", "v63", "v64", "v65", "v66", "v67", "v68", "v69", "v70", "v71", "v72", "v73", "v74", "v75", "v76", "v77", "v78", "v79", "v80", "v81", "v82", "v83", "v84", "v85", "v86", "v87", "v88", "v89", "v90", "v91", "v92", "v93", "v94", "v95", "v96", "v97", "v98", "v99", "v100", "v101", "v102", "v103", "v104", "v105", "v106", "v107", "v108", "v109", "v110", "v111", "v112", "v113", "v114", "v115", "v116", "v117", "v118", "v119", "v120", "v121", "v122", "v123", "v124", "v125", "v126", "v127", "v128", "v129", "v130", "v131", "v132", "v133", "v134", "v135", "v136", "v137", "v138", "v139", "v140", "v141", "v142", "v143", "v144", "v145", "v146", "v147", "v148", "v149", "v150", "v151", "v152", "v153", "v154", "v155", "v156", "v157", "v158", "v159", "v160", "v161", "v162", "v163", "v164", "v165", "v166", "v167", "v168", "v169", "v170", "v171", "v172", "v173", "v174", "v175", "v176", "v177", "v178", "v179", "v180", "v181", "v182", "v183", "v184", "v185", "v186", "v187", "v188", "v189", "v190", "v191", "v192", "v193", "v194", "v195", "v196", "v197", "v198", "v199", "v200", "v201", "v202", "v203", "v204", "v205", "v206", "v207", "v208", "v209", "v210", "v211", "v212", "v213", "v214", "v215", "v216", "v217", "v218", "v219", "v220", "v221", "v222", "v223", "v224", "v225", "v226", "v227", "v228", "v229", "v230", "v231", "a0", "a1", "a2", "a3", "a4", "a5", "a6", "a7", "a8", "a9", "a10", "a11", "a12", "a13", "a14", "a15", "a16", "a17", "a18", "a19", "a20", "a21", "a22", "a23", "a24", "a25", "a26", "a27", "a28", "a29", "a30", "a31", "a32", "a33", "a34", "a35", "a36", "a37", "a38", "a39", "a40", "a41", "a42", "a43", "a44", "a45", "a46", "a47", "a48", "a49", "a50", "a51", "a52", "a53", "a54", "a55", "a56", "a57", "a58", "a59", "a60", "a61", "a62", "a63", "a64", "a65", "a66", "a67", "a68", "a69", "a70", "a71", "a72", "a73", "a74", "a75", "a76", "a77", "a78", "a79", "a80", "a81", "a82", "a83", "a84", "a85", "a86", "a87", "a88", "a89", "a90", "a91", "a92", "a93", "a94", "a95", "a96", "a97", "a98", "a99", "a100", "a101", "a102", "a103", "a104", "a105", "a106", "a107", "a108", "a109", "a110", "a111", "a112", "a113", "a114", "a115", "a116", "a117", "a118", "a119", "a120", "a121", "a122", "a123", "a124", "a125", "a126", "a127", "a128", "a129", "a130", "a131", "a132", "a133", "a134", "a135", "a136", "a137", "a138", "a139", "a140", "a141", "a142", "a143", "a144", "a145", "a146", "a147", "a148", "a149", "a150", "a151", "a152", "a153", "a154", "a155", "a156", "a157", "a158", "a159", "a160", "a161", "a162", "a163", "a164", "a165", "a166", "a167", "a168", "a169", "a170", "a171", "a172", "a173", "a174", "a175", "a176", "a177", "a178", "a179", "a180", "a181", "a182", "a183", "a184", "a185", "a186", "a187", "a188", "a189", "a190", "a191", "a192", "a193", "a194", "a195", "a196", "a197", "a198", "a199", "a200", "a201", "a202", "a203", "a204", "a205", "a206", "a207", "a208", "a209", "a210", "a211", "a212", "a213", "a214", "a215", "a216", "a217", "a218", "a219", "a220", "a221", "a222", "a223", "a224", "a225", "a226", "a227", "a228", "a229", "a230", "a231", "a232", "a233", "a234", "a235", "a236", "a237", "a238", "a239", "a240", "a241", "a242", "a243", "a244", "a245", "a246", "a247", "a248", "a249", "a250", "a251", "a252", "a253", "a254", "a255", "s36", "s37", "s38", "s39", "s40", "s41", "s42", "s43", "s44", "s45", "s46", "s47", "s48", "s49", "s50", "s51", "s52", "s53", "s54", "s55", "s56", "s57", "s58", "s59", "s60", "s61", "s62", "s63", "s64", "s65", "s66", "s67", "s68", "s69", "s70", "s71", "s72", "s73", "s74", "s75", "s76", "s77", "s78", "s79", "s80", "s81", "s82", "s83", "s84", "s85", "s86", "s87", "s88", "s89", "s90", "s91", "s92", "s93", "s94", "s95", "s96", "s97", "vcc", "scc", "memory"); } else { asm volatile(
R"(.macro M d,w,x
v_mfma_f32_16x16x32_bf16 v[\d:\d+3], \w, v[\x:\x+3], v[\d:\d+3]
.endm
.macro R d,a,o
ds_read_b128 v[\d:\d+3], v\a offset:\o
.endm
.macro T d,a,o
ds_read_b64_tr_b16 \d, v\a offset:\o
.endm
.macro D a,s,o
buffer_load_dwordx4 v\a, s[36:39], s\s offen offset:\o sc1 lds
.endm
.macro L c
s_waitcnt lgkmcnt(\c)
.endm
Lcode_g3o_b:
s_mov_b64 s[36:37],%0
s_mov_b32 s38,111243392
s_mov_b32 s39,0x00020000
s_mov_b32 s44,%1
s_mov_b32 s45,%2
s_mov_b32 s46,%3
s_mov_b32 s47,%4
s_mov_b32 s48,%5
s_mov_b32 s49,%6
s_mov_b32 s50,%7
s_mov_b32 s51,%8
s_mov_b32 s53,%9
v_mov_b32 v0,%10
v_mov_b32 v3,%11
v_mov_b32 v5,%12
v_mov_b32 v6,%13
v_mov_b32 v18,%14
v_mov_b32 v19,%15
v_mov_b32 v20,%16
v_mov_b32 v21,%17
v_mov_b32 v22,%18
v_mov_b32 v10,%19
v_mov_b32 v11,%20
v_mov_b32 v12,%21
v_mov_b32 v13,%22
v_mov_b32 v14,%23
v_mov_b32 v15,%24
v_mov_b32 v16,%25
s_mov_b64 s[40:41],%26
s_mov_b32 s42,7340032
s_mov_b32 s43,0x00020000
s_lshl_b32 s57,s44,11
s_add_u32 s55,s50,0
s_add_u32 m0,s57,0
s_nop 0
buffer_load_dwordx4 v18,s[40:43],s55 offen offset:0  lds
s_add_u32 m0,s57,1024
s_nop 0
buffer_load_dwordx4 v19,s[40:43],s55 offen offset:0  lds
s_add_u32 m0,s57,8064
s_nop 0
buffer_load_dwordx4 v18,s[40:43],s55 offen offset:128  lds
s_add_u32 m0,s57,9088
s_nop 0
buffer_load_dwordx4 v19,s[40:43],s55 offen offset:128  lds
s_add_u32 s55,s50,458752
s_add_u32 m0,s57,16384
s_nop 0
buffer_load_dwordx4 v18,s[40:43],s55 offen offset:0  lds
s_add_u32 m0,s57,17408
s_nop 0
buffer_load_dwordx4 v19,s[40:43],s55 offen offset:0  lds
s_add_u32 m0,s57,24448
s_nop 0
buffer_load_dwordx4 v18,s[40:43],s55 offen offset:128  lds
s_add_u32 m0,s57,25472
s_nop 0
buffer_load_dwordx4 v19,s[40:43],s55 offen offset:128  lds
s_add_u32 s55,s50,917504
s_add_u32 m0,s57,32768
s_nop 0
buffer_load_dwordx4 v18,s[40:43],s55 offen offset:0  lds
s_add_u32 m0,s57,33792
s_nop 0
buffer_load_dwordx4 v19,s[40:43],s55 offen offset:0  lds
s_add_u32 m0,s57,40832
s_nop 0
buffer_load_dwordx4 v18,s[40:43],s55 offen offset:128  lds
s_add_u32 m0,s57,41856
s_nop 0
buffer_load_dwordx4 v19,s[40:43],s55 offen offset:128  lds
s_add_u32 s55,s50,1376256
s_add_u32 m0,s57,49152
s_nop 0
buffer_load_dwordx4 v18,s[40:43],s55 offen offset:0  lds
s_add_u32 m0,s57,50176
s_nop 0
buffer_load_dwordx4 v19,s[40:43],s55 offen offset:0  lds
s_add_u32 m0,s57,57216
s_nop 0
buffer_load_dwordx4 v18,s[40:43],s55 offen offset:128  lds
s_add_u32 m0,s57,58240
s_nop 0
buffer_load_dwordx4 v19,s[40:43],s55 offen offset:128  lds
s_add_u32 s55,s50,1835008
s_add_u32 m0,s57,65536
s_nop 0
buffer_load_dwordx4 v18,s[40:43],s55 offen offset:0  lds
s_add_u32 m0,s57,66560
s_nop 0
buffer_load_dwordx4 v19,s[40:43],s55 offen offset:0  lds
s_add_u32 m0,s57,73600
s_nop 0
buffer_load_dwordx4 v18,s[40:43],s55 offen offset:128  lds
s_add_u32 m0,s57,74624
s_nop 0
buffer_load_dwordx4 v19,s[40:43],s55 offen offset:128  lds
s_add_u32 s55,s50,2293760
s_add_u32 m0,s57,81920
s_nop 0
buffer_load_dwordx4 v18,s[40:43],s55 offen offset:0  lds
s_add_u32 m0,s57,82944
s_nop 0
buffer_load_dwordx4 v19,s[40:43],s55 offen offset:0  lds
s_add_u32 m0,s57,89984
s_nop 0
buffer_load_dwordx4 v18,s[40:43],s55 offen offset:128  lds
s_add_u32 m0,s57,91008
s_nop 0
buffer_load_dwordx4 v19,s[40:43],s55 offen offset:128  lds
s_waitcnt vmcnt(20) lgkmcnt(0)
s_barrier
s_add_u32 s55,s50,2752512
s_add_u32 m0,s57,98304
s_nop 0
buffer_load_dwordx4 v18,s[40:43],s55 offen offset:0  lds
s_add_u32 m0,s57,99328
s_nop 0
buffer_load_dwordx4 v19,s[40:43],s55 offen offset:0  lds
s_add_u32 m0,s57,106368
s_nop 0
buffer_load_dwordx4 v18,s[40:43],s55 offen offset:128  lds
s_add_u32 m0,s57,107392
s_nop 0
buffer_load_dwordx4 v19,s[40:43],s55 offen offset:128  lds
v_add_u32 v26,0,v20
v_add_u32 v27,0,v21
T a[0:1],26,0
T a[2:3],26,512
T a[4:5],27,0
T a[6:7],27,512
T a[8:9],26,4096
T a[10:11],26,4608
T a[12:13],27,4096
T a[14:15],27,4608
s_waitcnt vmcnt(20) lgkmcnt(0)
s_barrier
s_add_u32 s55,s50,3211264
s_add_u32 m0,s57,0
s_nop 0
buffer_load_dwordx4 v18,s[40:43],s55 offen offset:0  lds
s_add_u32 m0,s57,1024
s_nop 0
buffer_load_dwordx4 v19,s[40:43],s55 offen offset:0  lds
s_add_u32 m0,s57,8064
s_nop 0
buffer_load_dwordx4 v18,s[40:43],s55 offen offset:128  lds
s_add_u32 m0,s57,9088
s_nop 0
buffer_load_dwordx4 v19,s[40:43],s55 offen offset:128  lds
v_add_u32 v26,16384,v20
v_add_u32 v27,16384,v21
T a[16:17],26,0
T a[18:19],26,512
T a[20:21],27,0
T a[22:23],27,512
T a[24:25],26,4096
T a[26:27],26,4608
T a[28:29],27,4096
T a[30:31],27,4608
s_waitcnt vmcnt(20) lgkmcnt(0)
s_barrier
s_add_u32 s55,s50,3670016
s_add_u32 m0,s57,16384
s_nop 0
buffer_load_dwordx4 v18,s[40:43],s55 offen offset:0  lds
s_add_u32 m0,s57,17408
s_nop 0
buffer_load_dwordx4 v19,s[40:43],s55 offen offset:0  lds
s_add_u32 m0,s57,24448
s_nop 0
buffer_load_dwordx4 v18,s[40:43],s55 offen offset:128  lds
s_add_u32 m0,s57,25472
s_nop 0
buffer_load_dwordx4 v19,s[40:43],s55 offen offset:128  lds
v_add_u32 v26,32768,v20
v_add_u32 v27,32768,v21
T a[32:33],26,0
T a[34:35],26,512
T a[36:37],27,0
T a[38:39],27,512
T a[40:41],26,4096
T a[42:43],26,4608
T a[44:45],27,4096
T a[46:47],27,4608
s_waitcnt vmcnt(20) lgkmcnt(0)
s_barrier
s_add_u32 s55,s50,4128768
s_add_u32 m0,s57,32768
s_nop 0
buffer_load_dwordx4 v18,s[40:43],s55 offen offset:0  lds
s_add_u32 m0,s57,33792
s_nop 0
buffer_load_dwordx4 v19,s[40:43],s55 offen offset:0  lds
s_add_u32 m0,s57,40832
s_nop 0
buffer_load_dwordx4 v18,s[40:43],s55 offen offset:128  lds
s_add_u32 m0,s57,41856
s_nop 0
buffer_load_dwordx4 v19,s[40:43],s55 offen offset:128  lds
v_add_u32 v26,49152,v20
v_add_u32 v27,49152,v21
T a[48:49],26,0
T a[50:51],26,512
T a[52:53],27,0
T a[54:55],27,512
T a[56:57],26,4096
T a[58:59],26,4608
T a[60:61],27,4096
T a[62:63],27,4608
s_waitcnt vmcnt(20) lgkmcnt(0)
s_barrier
s_add_u32 s55,s50,4587520
s_add_u32 m0,s57,49152
s_nop 0
buffer_load_dwordx4 v18,s[40:43],s55 offen offset:0  lds
s_add_u32 m0,s57,50176
s_nop 0
buffer_load_dwordx4 v19,s[40:43],s55 offen offset:0  lds
s_add_u32 m0,s57,57216
s_nop 0
buffer_load_dwordx4 v18,s[40:43],s55 offen offset:128  lds
s_add_u32 m0,s57,58240
s_nop 0
buffer_load_dwordx4 v19,s[40:43],s55 offen offset:128  lds
v_add_u32 v26,65536,v20
v_add_u32 v27,65536,v21
T a[64:65],26,0
T a[66:67],26,512
T a[68:69],27,0
T a[70:71],27,512
T a[72:73],26,4096
T a[74:75],26,4608
T a[76:77],27,4096
T a[78:79],27,4608
s_waitcnt vmcnt(20) lgkmcnt(0)
s_barrier
s_add_u32 s55,s50,5046272
s_add_u32 m0,s57,65536
s_nop 0
buffer_load_dwordx4 v18,s[40:43],s55 offen offset:0  lds
s_add_u32 m0,s57,66560
s_nop 0
buffer_load_dwordx4 v19,s[40:43],s55 offen offset:0  lds
s_add_u32 m0,s57,73600
s_nop 0
buffer_load_dwordx4 v18,s[40:43],s55 offen offset:128  lds
s_add_u32 m0,s57,74624
s_nop 0
buffer_load_dwordx4 v19,s[40:43],s55 offen offset:128  lds
v_add_u32 v26,81920,v20
v_add_u32 v27,81920,v21
T a[80:81],26,0
T a[82:83],26,512
T a[84:85],27,0
T a[86:87],27,512
T a[88:89],26,4096
T a[90:91],26,4608
T a[92:93],27,4096
T a[94:95],27,4608
s_waitcnt vmcnt(20) lgkmcnt(0)
s_barrier
s_add_u32 s55,s50,5505024
s_add_u32 m0,s57,81920
s_nop 0
buffer_load_dwordx4 v18,s[40:43],s55 offen offset:0  lds
s_add_u32 m0,s57,82944
s_nop 0
buffer_load_dwordx4 v19,s[40:43],s55 offen offset:0  lds
s_add_u32 m0,s57,89984
s_nop 0
buffer_load_dwordx4 v18,s[40:43],s55 offen offset:128  lds
s_add_u32 m0,s57,91008
s_nop 0
buffer_load_dwordx4 v19,s[40:43],s55 offen offset:128  lds
v_add_u32 v26,98304,v20
v_add_u32 v27,98304,v21
T a[96:97],26,0
T a[98:99],26,512
T a[100:101],27,0
T a[102:103],27,512
T a[104:105],26,4096
T a[106:107],26,4608
T a[108:109],27,4096
T a[110:111],27,4608
s_waitcnt vmcnt(20) lgkmcnt(0)
s_barrier
s_add_u32 s55,s50,5963776
s_add_u32 m0,s57,98304
s_nop 0
buffer_load_dwordx4 v18,s[40:43],s55 offen offset:0  lds
s_add_u32 m0,s57,99328
s_nop 0
buffer_load_dwordx4 v19,s[40:43],s55 offen offset:0  lds
s_add_u32 m0,s57,106368
s_nop 0
buffer_load_dwordx4 v18,s[40:43],s55 offen offset:128  lds
s_add_u32 m0,s57,107392
s_nop 0
buffer_load_dwordx4 v19,s[40:43],s55 offen offset:128  lds
v_add_u32 v26,0,v20
v_add_u32 v27,0,v21
T a[112:113],26,0
T a[114:115],26,512
T a[116:117],27,0
T a[118:119],27,512
T a[120:121],26,4096
T a[122:123],26,4608
T a[124:125],27,4096
T a[126:127],27,4608
s_waitcnt vmcnt(20) lgkmcnt(0)
s_barrier
s_add_u32 s55,s50,6422528
s_add_u32 m0,s57,0
s_nop 0
buffer_load_dwordx4 v18,s[40:43],s55 offen offset:0  lds
s_add_u32 m0,s57,1024
s_nop 0
buffer_load_dwordx4 v19,s[40:43],s55 offen offset:0  lds
s_add_u32 m0,s57,8064
s_nop 0
buffer_load_dwordx4 v18,s[40:43],s55 offen offset:128  lds
s_add_u32 m0,s57,9088
s_nop 0
buffer_load_dwordx4 v19,s[40:43],s55 offen offset:128  lds
v_add_u32 v26,16384,v20
v_add_u32 v27,16384,v21
T a[128:129],26,0
T a[130:131],26,512
T a[132:133],27,0
T a[134:135],27,512
T a[136:137],26,4096
T a[138:139],26,4608
T a[140:141],27,4096
T a[142:143],27,4608
s_waitcnt vmcnt(20) lgkmcnt(0)
s_barrier
s_add_u32 s55,s50,6881280
s_add_u32 m0,s57,16384
s_nop 0
buffer_load_dwordx4 v18,s[40:43],s55 offen offset:0  lds
s_add_u32 m0,s57,17408
s_nop 0
buffer_load_dwordx4 v19,s[40:43],s55 offen offset:0  lds
s_add_u32 m0,s57,24448
s_nop 0
buffer_load_dwordx4 v18,s[40:43],s55 offen offset:128  lds
s_add_u32 m0,s57,25472
s_nop 0
buffer_load_dwordx4 v19,s[40:43],s55 offen offset:128  lds
v_add_u32 v26,32768,v20
v_add_u32 v27,32768,v21
T a[144:145],26,0
T a[146:147],26,512
T a[148:149],27,0
T a[150:151],27,512
T a[152:153],26,4096
T a[154:155],26,4608
T a[156:157],27,4096
T a[158:159],27,4608
s_waitcnt vmcnt(20) lgkmcnt(0)
s_barrier
v_add_u32 v26,49152,v20
v_add_u32 v27,49152,v21
T a[160:161],26,0
T a[162:163],26,512
T a[164:165],27,0
T a[166:167],27,512
T a[168:169],26,4096
T a[170:171],26,4608
T a[172:173],27,4096
T a[174:175],27,4608
s_waitcnt vmcnt(16) lgkmcnt(0)
s_barrier
v_add_u32 v26,65536,v20
v_add_u32 v27,65536,v21
T a[176:177],26,0
T a[178:179],26,512
T a[180:181],27,0
T a[182:183],27,512
T a[184:185],26,4096
T a[186:187],26,4608
T a[188:189],27,4096
T a[190:191],27,4608
s_waitcnt vmcnt(12) lgkmcnt(0)
s_barrier
v_add_u32 v26,81920,v20
v_add_u32 v27,81920,v21
T a[192:193],26,0
T a[194:195],26,512
T a[196:197],27,0
T a[198:199],27,512
T a[200:201],26,4096
T a[202:203],26,4608
T a[204:205],27,4096
T a[206:207],27,4608
s_waitcnt vmcnt(8) lgkmcnt(0)
s_barrier
v_add_u32 v26,98304,v20
v_add_u32 v27,98304,v21
T a[208:209],26,0
T a[210:211],26,512
T a[212:213],27,0
T a[214:215],27,512
T a[216:217],26,4096
T a[218:219],26,4608
T a[220:221],27,4096
T a[222:223],27,4608
s_waitcnt vmcnt(4) lgkmcnt(0)
s_barrier
v_add_u32 v26,0,v20
v_add_u32 v27,0,v21
T a[224:225],26,0
T a[226:227],26,512
T a[228:229],27,0
T a[230:231],27,512
T a[232:233],26,4096
T a[234:235],26,4608
T a[236:237],27,4096
T a[238:239],27,4608
s_waitcnt vmcnt(0) lgkmcnt(0)
s_barrier
v_add_u32 v26,16384,v20
v_add_u32 v27,16384,v21
T a[240:241],26,0
T a[242:243],26,512
T a[244:245],27,0
T a[246:247],27,512
T a[248:249],26,4096
T a[250:251],26,4608
T a[252:253],27,4096
T a[254:255],27,4608
L 0
s_barrier
v_mov_b32 v34,0x3f803f80
v_mov_b32 v35,0x3f803f80
v_mov_b32 v36,0x3f803f80
v_mov_b32 v37,0x3f803f80
s_nop 1
v_mfma_f32_16x16x32_bf16 v[106:109],a[0:3],v[34:37],0
v_mfma_f32_16x16x32_bf16 v[110:113],a[4:7],v[34:37],0
M 106,a[8:11],34
M 110,a[12:15],34
M 106,a[16:19],34
M 110,a[20:23],34
M 106,a[24:27],34
M 110,a[28:31],34
M 106,a[32:35],34
M 110,a[36:39],34
M 106,a[40:43],34
M 110,a[44:47],34
M 106,a[48:51],34
M 110,a[52:55],34
M 106,a[56:59],34
M 110,a[60:63],34
M 106,a[64:67],34
M 110,a[68:71],34
M 106,a[72:75],34
M 110,a[76:79],34
M 106,a[80:83],34
M 110,a[84:87],34
M 106,a[88:91],34
M 110,a[92:95],34
M 106,a[96:99],34
M 110,a[100:103],34
M 106,a[104:107],34
M 110,a[108:111],34
M 106,a[112:115],34
M 110,a[116:119],34
M 106,a[120:123],34
M 110,a[124:127],34
M 106,a[128:131],34
M 110,a[132:135],34
M 106,a[136:139],34
M 110,a[140:143],34
M 106,a[144:147],34
M 110,a[148:151],34
M 106,a[152:155],34
M 110,a[156:159],34
M 106,a[160:163],34
M 110,a[164:167],34
M 106,a[168:171],34
M 110,a[172:175],34
M 106,a[176:179],34
)"
R"(M 110,a[180:183],34
M 106,a[184:187],34
M 110,a[188:191],34
M 106,a[192:195],34
M 110,a[196:199],34
M 106,a[200:203],34
M 110,a[204:207],34
M 106,a[208:211],34
M 110,a[212:215],34
M 106,a[216:219],34
M 110,a[220:223],34
M 106,a[224:227],34
M 110,a[228:231],34
M 106,a[232:235],34
M 110,a[236:239],34
M 106,a[240:243],34
M 110,a[244:247],34
M 106,a[248:251],34
M 110,a[252:255],34
v_mov_b32 v4,s53
s_mov_b32 s82,0
s_mov_b32 s80,0
s_mov_b32 s54,s45
s_mul_i32 s57,s44,4096
s_mul_i32 s55,s45,32768
s_add_u32 s55,s55,s57
s_add_u32 s55,s55,31457280
s_mul_i32 s57,s44,4096
s_mul_i32 s60,s46,32768
s_mul_i32 s61,s46,1024
s_mul_i32 s62,s46,114688
s_mul_i32 s63,s46,2560
s_mul_i32 s64,s46,1024
s_lshl_b32 s59,s44,8
s_add_u32 s59,s59,131072
s_mul_i32 s58,s45,1024
s_add_u32 s58,s58,109613056
s_mul_i32 s65,s45,114688
s_add_u32 s65,s65,s47
s_mul_i32 s68,s45,2560
s_add_u32 s68,s68,s48
s_mul_i32 s73,s45,1024
s_add_u32 s73,s73,107479040
s_mul_i32 s74,s45,32768
s_add_u32 s74,s74,s49
s_mul_i32 s75,s45,1024
s_add_u32 s75,s75,107479040
s_mov_b32 s66,0x7fff0000
s_mov_b32 s67,0x7fff0000
s_mov_b32 s69,0x7fff0000
s_mov_b32 s76,0x7fff0000
s_mov_b32 s77,0x7fff0000
s_mov_b32 s70,0x7fff0000
s_mov_b32 s71,0x7fff0000
s_mov_b32 s72,0x7fff0000
v_mov_b32 v1,v0
v_add_u32 v2,16384,v0
s_mov_b32 s84,16384
s_mov_b32 s86,0x3a800000
s_mov_b32 s87,0x3727c5ac
s_mov_b32 s88,0x409d30fc
s_mov_b32 s89,0xbeeff558
s_lshr_b32 s85,s44,1
s_mul_i32 s85,s85,127
v_lshlrev_b32 v8,22,v0
v_and_b32 v8,0x80000000,v8
v_or_b32 v8,v8,v5
v_add_u32 v2,32768,v0
s_lshl_b32 s84,s57,1
s_add_u32 s84,s84,32768
s_getpc_b64 s[40:41]
Lg3o_pc_%=:
s_lshl_b32 s78,s44,13
v_lshlrev_b32 v114,3,v0
v_add_u32 v114,s78,v114
v_min_u32 v114,Lcode_g3o_e-Lg3o_pc_%=-4,v114
s_mov_b32 m0,163584
s_nop 0
global_load_lds_dword v114,s[40:41]
s_add_u32 m0,s59,4096
s_nop 0
buffer_load_dword v8,s[36:39],s58 offen offset:0 sc1 lds
buffer_store_dword v4,v3,s[36:39],s71 offen offset:0 sc0 sc1
s_add_u32 m0,s59,5120
s_nop 0
buffer_load_dword v8,s[36:39],s58 offen offset:0 sc1 lds
buffer_store_dword v4,v3,s[36:39],s71 offen offset:0 sc0 sc1
s_mov_b32 s83,0
Lg3o_poll_%=:
s_add_u32 s78,s83,2
s_and_b32 s78,s78,3
s_lshl_b32 s78,s78,10
s_add_u32 s78,s78,4096
s_add_u32 m0,s59,s78
s_nop 0
buffer_load_dword v8,s[36:39],s58 offen offset:0 sc1 lds
s_and_b32 s78,s83,3
s_lshl_b32 s78,s78,10
v_add_u32 v114,s78,v6
s_waitcnt vmcnt(4)
s_barrier
ds_read_b32 v7,v114 offset:4096
buffer_store_dword v4,v3,s[36:39],s70 offen offset:0 sc0 sc1
s_mov_b32 s70,s71
s_mov_b32 s71,s72
s_mov_b32 s72,0x7fff0000
s_add_u32 s83,s83,1
s_add_u32 s82,s82,1
L 0
v_cmp_eq_u32 vcc,s53,v7
s_nop 0
s_cmp_eq_u32 vcc_lo,-1
s_cbranch_scc1 Lg3o_go_%=
s_cmp_lt_u32 s82,20000
s_cbranch_scc1 Lg3o_poll_%=
Lg3o_go_%=:
buffer_load_dword v130,v11,s[36:39],s73 offen offset:0 sc1
s_add_u32 s73,s73,s64
s_add_u32 m0,s57,0
s_add_u32 s56,s55,0
D 0,56,0
D 0,56,1024
D 0,56,2048
D 0,56,3072
s_add_u32 s58,s58,s61
s_add_u32 m0,s59,2048
s_nop 0
buffer_load_dword v8,s[36:39],s58 offen offset:0 sc1 lds
s_add_u32 m0,s57,16384
s_add_u32 s56,s55,16384
D 0,56,0
D 0,56,1024
D 0,56,2048
D 0,56,3072
s_add_u32 s55,s55,s60
s_sub_u32 s57,s84,s57
s_add_u32 s54,s54,s46
s_waitcnt vmcnt(4)
s_barrier
buffer_store_dword v4,v3,s[36:39],s70 offen offset:0 sc0 sc1
s_min_u32 s71,s71,s72
buffer_store_dword v4,v3,s[36:39],s71 offen offset:0 sc0 sc1
s_mov_b32 s70,0x7fff0000
s_mov_b32 s71,0x7fff0000
s_mov_b32 s72,0x7fff0000
s_cmp_eq_u32 s80,1
s_cbranch_scc1 Lg3o_st1_%=
s_branch Lg3o_st0_%=
Lg3o_gox_%=:
s_add_u32 m0,s59,2048
s_nop 0
buffer_load_dword v8,s[36:39],s58 offen offset:0 sc1 lds
s_waitcnt vmcnt(0)
s_barrier
buffer_store_dword v4,v3,s[36:39],s70 offen offset:0 sc0 sc1
s_min_u32 s71,s71,s72
buffer_store_dword v4,v3,s[36:39],s71 offen offset:0 sc0 sc1
s_mov_b32 s70,0x7fff0000
s_mov_b32 s71,0x7fff0000
s_mov_b32 s72,0x7fff0000
s_cmp_eq_u32 s80,1
s_cbranch_scc1 Lg3o_st1_%=
Lg3o_st0_%=:
ds_read_b32 v7,v6 offset:2048
R 34,1,0
R 38,1,1024
R 42,1,2048
R 46,1,3072
R 50,1,4096
L 5
v_cmp_eq_u32 vcc,s53,v7
L 3
s_cmp_eq_u32 vcc_lo,-1
s_cbranch_scc1 Lg3o_F0_%=
s_branch Lg3o_L0_%=
Lg3o_st1_%=:
ds_read_b32 v7,v6 offset:2048
R 34,2,0
R 38,2,1024
R 42,2,2048
R 46,2,3072
R 50,2,4096
L 5
v_cmp_eq_u32 vcc,s53,v7
L 3
s_cmp_eq_u32 vcc_lo,-1
s_cbranch_scc1 Lg3o_F1_%=
s_branch Lg3o_L1_%=
Lg3o_F0_%=:
v_mfma_f32_16x16x32_bf16 v[22:25],a[4:7],v[34:37],0
s_add_u32 s58,s58,s61
s_add_u32 m0,s59,0
R 54,1,5120
M 22,a[12:15],38
buffer_load_dword v8,s[36:39],s58 offen offset:0 sc1 lds
v_mfma_f32_16x16x32_bf16 v[18:21],a[0:3],v[34:37],0
s_add_u32 m0,s57,0
R 58,1,6144
v_add_f32_dpp v131,v130,v130 quad_perm:[1,0,3,2] row_mask:0xf bank_mask:0xf
M 18,a[8:11],38
L 3
s_add_u32 s56,s55,0
buffer_load_dword v130,v11,s[36:39],s73 offen offset:0 sc1
M 18,a[16:19],42
s_mov_b32 s66,s65
R 62,1,7168
v_add_f32_dpp v131,v131,v131 quad_perm:[2,3,0,1] row_mask:0xf bank_mask:0xf
M 18,a[24:27],46
D 0,56,0
M 22,a[20:23],42
s_add_u32 s65,s65,s62
R 66,1,8192
v_add_f32_dpp v134,v131,v131 row_half_mirror row_mask:0xf bank_mask:0xf
M 22,a[28:31],46
L 3
D 0,56,1024
M 22,a[36:39],50
s_add_u32 s54,s54,s46
R 70,1,9216
v_mov_b32_dpp v131,v134 row_ror:8 row_mask:0xf bank_mask:0xf
M 22,a[44:47],54
s_add_u32 s73,s73,s64
v_fma_f32 v131,-v134,v134,v131
v_max_f32 v131,s87,v131
M 18,a[32:35],50
R 74,1,10240
v_mul_f32 v134,v15,v134
M 18,a[40:43],54
L 3
D 0,56,2048
M 18,a[48:51],58
R 78,1,11264
v_fma_f32 v26,-v102,v106,v26
M 18,a[56:59],62
D 0,56,3072
M 22,a[52:55],58
s_add_u32 m0,s57,16384
R 82,1,12288
v_fma_f32 v27,-v102,v107,v27
M 22,a[60:63],62
L 3
v_rsq_f32 v132,v131
M 22,a[68:71],66
s_add_u32 s56,s55,16384
R 86,1,13312
v_fma_f32 v134,v132,v16,v134
M 22,a[76:79],70
s_add_u32 s55,s55,s60
ds_write_b32 v12,v134 offset:0
M 18,a[64:67],66
s_sub_u32 s57,s84,s57
R 90,1,14336
v_fma_f32 v28,-v102,v108,v28
M 18,a[72:75],70
L 4
v_fma_f32 v29,-v102,v109,v29
v_mul_f32 v26,v103,v26
M 18,a[80:83],74
s_waitcnt vmcnt(8)
R 94,1,15360
v_mul_f32 v27,v103,v27
M 18,a[88:91],78
s_barrier
v_mul_f32 v28,v103,v28
v_mul_f32 v29,v103,v29
M 22,a[84:87],74
R 34,1,16384
v_fma_f32 v114,v26,v26,s88
M 22,a[92:95],78
L 4
v_fma_f32 v115,v27,v27,s88
v_fma_f32 v116,v28,v28,s88
M 22,a[100:103],82
R 38,1,17408
v_fma_f32 v117,v29,v29,s88
M 22,a[108:111],86
D 0,56,0
M 18,a[96:99],82
R 42,1,18432
v_mul_f32 v114,v26,v114
M 18,a[104:107],86
L 3
v_exp_f32 v114,v114
M 18,a[112:115],90
R 46,1,19456
v_mul_f32 v115,v27,v115
M 18,a[120:123],94
v_exp_f32 v115,v115
M 22,a[116:119],90
R 50,1,20480
v_mul_f32 v116,v28,v116
M 22,a[124:127],94
L 3
v_exp_f32 v116,v116
M 22,a[132:135],34
R 54,1,21504
v_mul_f32 v117,v29,v117
M 22,a[140:143],38
D 0,56,1024
M 18,a[128:131],34
R 58,1,22528
v_fma_f32 v114,v114,s89,s89
M 18,a[136:139],38
L 3
v_exp_f32 v117,v117
M 18,a[144:147],42
R 62,1,23552
v_fma_f32 v115,v115,s89,s89
M 18,a[152:155],46
v_rcp_f32 v114,v114
M 22,a[148:151],42
R 66,1,24576
v_fma_f32 v116,v116,s89,s89
M 22,a[156:159],46
L 3
D 0,56,2048
M 22,a[164:167],50
R 70,1,25600
v_fma_f32 v117,v117,s89,s89
M 22,a[172:175],54
v_rcp_f32 v115,v115
M 18,a[160:163],50
R 74,1,26624
v_mul_f32 v26,v26,v114
M 18,a[168:171],54
L 3
v_rcp_f32 v116,v116
M 18,a[176:179],58
R 78,1,27648
v_mul_f32 v27,v27,v115
M 18,a[184:187],62
v_rcp_f32 v117,v117
M 22,a[180:183],58
R 82,1,28672
v_mul_f32 v28,v28,v116
M 22,a[188:191],62
L 3
D 0,56,3072
M 22,a[196:199],66
R 86,1,29696
v_mul_f32 v29,v29,v117
M 22,a[204:207],70
v_cvt_pk_bf16_f32 v122,v26,v27
v_cvt_pk_bf16_f32 v123,v28,v29
M 18,a[192:195],66
R 90,1,30720
v_fma_f32 v30,-v102,v110,v30
M 18,a[200:203],70
L 3
s_nop 0
v_fma_f32 v31,-v102,v111,v31
M 18,a[208:211],74
s_waitcnt vmcnt(4)
R 94,1,31744
v_fma_f32 v32,-v102,v112,v32
M 18,a[216:219],78
s_barrier
ds_read_b32 v7,v6 offset:0
ds_read_b64 v[98:99],v13 offset:0
M 22,a[212:215],74
R 34,2,0
v_fma_f32 v33,-v102,v113,v33
M 22,a[220:223],78
L 5
buffer_store_dword v4,v3,s[36:39],s70 offen offset:0 sc0 sc1
M 22,a[228:231],82
s_mov_b32 s70,s69
R 38,2,1024
v_mul_f32 v30,v103,v30
M 22,a[236:239],86
s_mov_b32 s69,s68
v_mul_f32 v31,v103,v31
v_mul_f32 v32,v103,v32
M 18,a[224:227],82
s_add_u32 s68,s68,s63
R 42,2,2048
v_mul_f32 v33,v103,v33
M 18,a[232:235],86
L 5
v_fma_f32 v118,v30,v30,s88
v_fma_f32 v119,v31,v31,s88
v_fma_f32 v120,v32,v32,s88
v_fma_f32 v121,v33,v33,s88
v_mul_f32 v118,v30,v118
v_mul_f32 v119,v31,v119
v_mul_f32 v120,v32,v120
v_mul_f32 v121,v33,v121
v_exp_f32 v118,v118
v_exp_f32 v119,v119
v_exp_f32 v120,v120
v_exp_f32 v121,v121
M 18,a[240:243],90
L 4
R 46,2,3072
v_fma_f32 v118,v118,s89,s89
v_fma_f32 v119,v119,s89,s89
v_fma_f32 v120,v120,s89,s89
v_fma_f32 v121,v121,s89,s89
v_rcp_f32 v118,v118
v_rcp_f32 v119,v119
v_rcp_f32 v120,v120
v_rcp_f32 v121,v121
v_cmp_eq_u32 vcc,s53,v7
M 18,a[248:251],94
v_mul_f32 v30,v30,v118
v_mul_f32 v31,v31,v119
v_mul_f32 v32,v32,v120
v_mul_f32 v33,v33,v121
v_cvt_pk_bf16_f32 v124,v30,v31
v_cvt_pk_bf16_f32 v125,v32,v33
M 22,a[244:247],90
L 2
s_cmp_eq_u32 vcc_lo,-1
R 50,2,4096
buffer_store_dwordx4 v[122:125],v0,s[36:39],s67 offen offset:0 sc0 sc1
M 22,a[252:255],94
s_cbranch_scc0 Lg3o_K1_%=
Lg3o_F1_%=:
v_mfma_f32_16x16x32_bf16 v[30:33],a[4:7],v[34:37],0
s_add_u32 s58,s58,s61
s_add_u32 m0,s59,1024
R 54,2,5120
M 30,a[12:15],38
buffer_load_dword v8,s[36:39],s58 offen offset:0 sc1 lds
v_mfma_f32_16x16x32_bf16 v[26:29],a[0:3],v[34:37],0
s_add_u32 m0,s57,0
R 58,2,6144
v_add_f32_dpp v131,v130,v130 quad_perm:[1,0,3,2] row_mask:0xf bank_mask:0xf
M 26,a[8:11],38
L 3
s_add_u32 s56,s55,0
buffer_load_dword v130,v11,s[36:39],s73 offen offset:0 sc1
M 26,a[16:19],42
s_mov_b32 s67,s65
R 62,2,7168
v_add_f32_dpp v131,v131,v131 quad_perm:[2,3,0,1] row_mask:0xf bank_mask:0xf
M 26,a[24:27],46
D 0,56,0
M 30,a[20:23],42
s_add_u32 s65,s65,s62
R 66,2,8192
v_add_f32_dpp v134,v131,v131 row_half_mirror row_mask:0xf bank_mask:0xf
M 30,a[28:31],46
L 3
D 0,56,1024
M 30,a[36:39],50
s_add_u32 s54,s54,s46
R 70,2,9216
v_mov_b32_dpp v131,v134 row_ror:8 row_mask:0xf bank_mask:0xf
M 30,a[44:47],54
s_add_u32 s73,s73,s64
v_fma_f32 v131,-v134,v134,v131
v_max_f32 v131,s87,v131
M 26,a[32:35],50
R 74,2,10240
v_mul_f32 v134,v15,v134
M 26,a[40:43],54
L 3
D 0,56,2048
M 26,a[48:51],58
R 78,2,11264
v_fma_f32 v18,-v98,v106,v18
M 26,a[56:59],62
D 0,56,3072
M 30,a[52:55],58
s_add_u32 m0,s57,16384
R 82,2,12288
v_fma_f32 v19,-v98,v107,v19
M 30,a[60:63],62
L 3
v_rsq_f32 v132,v131
M 30,a[68:71],66
s_add_u32 s56,s55,16384
R 86,2,13312
v_fma_f32 v134,v132,v16,v134
M 30,a[76:79],70
s_add_u32 s55,s55,s60
ds_write_b32 v12,v134 offset:2048
M 26,a[64:67],66
s_sub_u32 s57,s84,s57
R 90,2,14336
v_fma_f32 v20,-v98,v108,v20
M 26,a[72:75],70
L 4
v_fma_f32 v21,-v98,v109,v21
v_mul_f32 v18,v99,v18
M 26,a[80:83],74
s_waitcnt vmcnt(8)
R 94,2,15360
v_mul_f32 v19,v99,v19
M 26,a[88:91],78
s_barrier
v_mul_f32 v20,v99,v20
v_mul_f32 v21,v99,v21
M 30,a[84:87],74
R 34,2,16384
v_fma_f32 v114,v18,v18,s88
M 30,a[92:95],78
L 4
v_fma_f32 v115,v19,v19,s88
)"
R"(v_fma_f32 v116,v20,v20,s88
M 30,a[100:103],82
R 38,2,17408
v_fma_f32 v117,v21,v21,s88
M 30,a[108:111],86
D 0,56,0
M 26,a[96:99],82
R 42,2,18432
v_mul_f32 v114,v18,v114
M 26,a[104:107],86
L 3
v_exp_f32 v114,v114
M 26,a[112:115],90
R 46,2,19456
v_mul_f32 v115,v19,v115
M 26,a[120:123],94
v_exp_f32 v115,v115
M 30,a[116:119],90
R 50,2,20480
v_mul_f32 v116,v20,v116
M 30,a[124:127],94
L 3
v_exp_f32 v116,v116
M 30,a[132:135],34
R 54,2,21504
v_mul_f32 v117,v21,v117
M 30,a[140:143],38
D 0,56,1024
M 26,a[128:131],34
R 58,2,22528
v_fma_f32 v114,v114,s89,s89
M 26,a[136:139],38
L 3
v_exp_f32 v117,v117
M 26,a[144:147],42
R 62,2,23552
v_fma_f32 v115,v115,s89,s89
M 26,a[152:155],46
v_rcp_f32 v114,v114
M 30,a[148:151],42
R 66,2,24576
v_fma_f32 v116,v116,s89,s89
M 30,a[156:159],46
L 3
D 0,56,2048
M 30,a[164:167],50
R 70,2,25600
v_fma_f32 v117,v117,s89,s89
M 30,a[172:175],54
v_rcp_f32 v115,v115
M 26,a[160:163],50
R 74,2,26624
v_mul_f32 v18,v18,v114
M 26,a[168:171],54
L 3
v_rcp_f32 v116,v116
M 26,a[176:179],58
R 78,2,27648
v_mul_f32 v19,v19,v115
M 26,a[184:187],62
v_rcp_f32 v117,v117
M 30,a[180:183],58
R 82,2,28672
v_mul_f32 v20,v20,v116
M 30,a[188:191],62
L 3
D 0,56,3072
M 30,a[196:199],66
R 86,2,29696
v_mul_f32 v21,v21,v117
M 30,a[204:207],70
v_cvt_pk_bf16_f32 v122,v18,v19
v_cvt_pk_bf16_f32 v123,v20,v21
M 26,a[192:195],66
R 90,2,30720
v_fma_f32 v22,-v98,v110,v22
M 26,a[200:203],70
L 3
s_nop 0
v_fma_f32 v23,-v98,v111,v23
M 26,a[208:211],74
s_waitcnt vmcnt(4)
R 94,2,31744
v_fma_f32 v24,-v98,v112,v24
M 26,a[216:219],78
s_barrier
ds_read_b32 v7,v6 offset:1024
ds_read_b64 v[102:103],v13 offset:2048
M 30,a[212:215],74
R 34,1,0
v_fma_f32 v25,-v98,v113,v25
M 30,a[220:223],78
L 5
buffer_store_dword v4,v3,s[36:39],s70 offen offset:0 sc0 sc1
M 30,a[228:231],82
s_mov_b32 s70,s69
R 38,1,1024
v_mul_f32 v22,v99,v22
M 30,a[236:239],86
s_mov_b32 s69,s68
v_mul_f32 v23,v99,v23
v_mul_f32 v24,v99,v24
M 26,a[224:227],82
s_add_u32 s68,s68,s63
R 42,1,2048
v_mul_f32 v25,v99,v25
M 26,a[232:235],86
L 5
v_fma_f32 v118,v22,v22,s88
v_fma_f32 v119,v23,v23,s88
v_fma_f32 v120,v24,v24,s88
v_fma_f32 v121,v25,v25,s88
v_mul_f32 v118,v22,v118
v_mul_f32 v119,v23,v119
v_mul_f32 v120,v24,v120
v_mul_f32 v121,v25,v121
v_exp_f32 v118,v118
v_exp_f32 v119,v119
v_exp_f32 v120,v120
v_exp_f32 v121,v121
M 26,a[240:243],90
L 4
R 46,1,3072
v_fma_f32 v118,v118,s89,s89
v_fma_f32 v119,v119,s89,s89
v_fma_f32 v120,v120,s89,s89
v_fma_f32 v121,v121,s89,s89
v_rcp_f32 v118,v118
v_rcp_f32 v119,v119
v_rcp_f32 v120,v120
v_rcp_f32 v121,v121
v_cmp_eq_u32 vcc,s53,v7
M 26,a[248:251],94
v_mul_f32 v22,v22,v118
v_mul_f32 v23,v23,v119
v_mul_f32 v24,v24,v120
v_mul_f32 v25,v25,v121
v_cvt_pk_bf16_f32 v124,v22,v23
v_cvt_pk_bf16_f32 v125,v24,v25
M 30,a[244:247],90
L 2
s_cmp_eq_u32 vcc_lo,-1
R 50,1,4096
buffer_store_dwordx4 v[122:125],v0,s[36:39],s66 offen offset:0 sc0 sc1
M 30,a[252:255],94
s_cbranch_scc1 Lg3o_F0_%=
Lg3o_K0_%=:
Lg3o_L0_%=:
v_mfma_f32_16x16x32_bf16 v[22:25],a[4:7],v[34:37],0
s_add_u32 m0,s59,3072
R 54,1,5120
v_add_f32_dpp v131,v130,v130 quad_perm:[1,0,3,2] row_mask:0xf bank_mask:0xf
M 22,a[12:15],38
buffer_load_dword v8,s[36:39],s58 offen offset:0 sc1 lds
v_mfma_f32_16x16x32_bf16 v[18:21],a[0:3],v[34:37],0
s_mov_b32 s66,s65
R 58,1,6144
v_add_f32_dpp v131,v131,v131 quad_perm:[2,3,0,1] row_mask:0xf bank_mask:0xf
M 18,a[8:11],38
L 3
v_fma_f32 v26,-v102,v106,v26
v_fma_f32 v27,-v102,v107,v27
M 18,a[16:19],42
s_add_u32 s65,s65,s62
R 62,1,7168
v_add_f32_dpp v134,v131,v131 row_half_mirror row_mask:0xf bank_mask:0xf
M 18,a[24:27],46
v_fma_f32 v28,-v102,v108,v28
v_fma_f32 v29,-v102,v109,v29
M 22,a[20:23],42
R 66,1,8192
v_mov_b32_dpp v131,v134 row_ror:8 row_mask:0xf bank_mask:0xf
M 22,a[28:31],46
L 3
v_fma_f32 v131,-v134,v134,v131
v_max_f32 v131,s87,v131
M 22,a[36:39],50
R 70,1,9216
v_mul_f32 v134,v15,v134
M 22,a[44:47],54
v_rsq_f32 v132,v131
M 18,a[32:35],50
R 74,1,10240
v_fma_f32 v134,v132,v16,v134
M 18,a[40:43],54
L 3
ds_write_b32 v12,v134 offset:0
M 18,a[48:51],58
R 78,1,11264
v_mul_f32 v26,v103,v26
M 18,a[56:59],62
v_mul_f32 v27,v103,v27
v_mul_f32 v28,v103,v28
M 22,a[52:55],58
R 82,1,12288
v_mul_f32 v29,v103,v29
M 22,a[60:63],62
L 4
v_fma_f32 v114,v26,v26,s88
v_fma_f32 v115,v27,v27,s88
M 22,a[68:71],66
R 86,1,13312
v_fma_f32 v116,v28,v28,s88
M 22,a[76:79],70
v_fma_f32 v117,v29,v29,s88
v_mul_f32 v114,v26,v114
M 18,a[64:67],66
R 90,1,14336
v_mul_f32 v115,v27,v115
M 18,a[72:75],70
L 3
v_exp_f32 v114,v114
M 18,a[80:83],74
s_waitcnt vmcnt(0)
R 94,1,15360
v_mul_f32 v116,v28,v116
M 18,a[88:91],78
s_barrier
ds_read_b32 v7,v6 offset:3072
v_mul_f32 v117,v29,v117
M 22,a[84:87],74
R 34,1,16384
v_fma_f32 v114,v114,s89,s89
M 22,a[92:95],78
L 4
buffer_store_dword v4,v3,s[36:39],s70 offen offset:0 sc0 sc1
M 22,a[100:103],82
s_mov_b32 s70,0x7fff0000
R 38,1,17408
v_fma_f32 v30,-v102,v110,v30
M 22,a[108:111],86
v_exp_f32 v115,v115
M 18,a[96:99],82
R 42,1,18432
v_fma_f32 v115,v115,s89,s89
M 18,a[104:107],86
L 4
L 3
v_cmp_eq_u32 vcc,s53,v7
M 18,a[112:115],90
R 46,1,19456
v_fma_f32 v31,-v102,v111,v31
M 18,a[120:123],94
v_exp_f32 v116,v116
M 22,a[116:119],90
s_cmp_eq_u32 vcc_lo,-1
R 50,1,20480
v_fma_f32 v116,v116,s89,s89
s_cbranch_scc0 Lg3o_X0_%=
M 22,a[124:127],94
L 3
s_add_u32 m0,s57,0
s_add_u32 s56,s55,0
buffer_load_dword v130,v11,s[36:39],s73 offen offset:0 sc1
M 22,a[132:135],34
s_add_u32 s58,s58,s61
R 54,1,21504
v_fma_f32 v32,-v102,v112,v32
M 22,a[140:143],38
D 0,56,0
M 18,a[128:131],34
s_add_u32 s54,s54,s46
R 58,1,22528
v_fma_f32 v33,-v102,v113,v33
M 18,a[136:139],38
L 3
v_exp_f32 v117,v117
M 18,a[144:147],42
s_add_u32 s73,s73,s64
R 62,1,23552
v_fma_f32 v117,v117,s89,s89
M 18,a[152:155],46
D 0,56,1024
M 22,a[148:151],42
R 66,1,24576
v_mul_f32 v30,v103,v30
M 22,a[156:159],46
L 3
v_rcp_f32 v114,v114
M 22,a[164:167],50
R 70,1,25600
v_mul_f32 v26,v26,v114
M 22,a[172:175],54
D 0,56,2048
M 18,a[160:163],50
R 74,1,26624
v_mul_f32 v31,v103,v31
M 18,a[168:171],54
L 3
v_rcp_f32 v115,v115
M 18,a[176:179],58
R 78,1,27648
v_mul_f32 v27,v27,v115
M 18,a[184:187],62
D 0,56,3072
M 22,a[180:183],58
s_add_u32 m0,s59,0
R 82,1,28672
v_cvt_pk_bf16_f32 v122,v26,v27
M 22,a[188:191],62
L 3
buffer_load_dword v8,s[36:39],s58 offen offset:0 sc1 lds
M 22,a[196:199],66
s_add_u32 m0,s57,16384
R 86,1,29696
v_mul_f32 v32,v103,v32
M 22,a[204:207],70
s_add_u32 s56,s55,16384
v_rcp_f32 v116,v116
M 18,a[192:195],66
s_add_u32 s55,s55,s60
R 90,1,30720
v_mul_f32 v28,v28,v116
M 18,a[200:203],70
L 3
s_nop 0
D 0,56,0
M 18,a[208:211],74
s_waitcnt vmcnt(1)
R 94,1,31744
v_mul_f32 v33,v103,v33
M 18,a[216:219],78
s_barrier
ds_read_b32 v7,v6 offset:0
ds_read_b64 v[98:99],v13 offset:0
M 22,a[212:215],74
s_sub_u32 s57,s84,s57
R 34,2,0
v_fma_f32 v118,v30,v30,s88
M 22,a[220:223],78
L 5
buffer_store_dword v4,v3,s[36:39],s70 offen offset:0 sc0 sc1
M 22,a[228:231],82
s_mov_b32 s70,s69
R 38,2,1024
v_fma_f32 v119,v31,v31,s88
M 22,a[236:239],86
D 0,56,1024
M 18,a[224:227],82
s_mov_b32 s69,s68
R 42,2,2048
v_fma_f32 v120,v32,v32,s88
M 18,a[232:235],86
L 5
v_fma_f32 v121,v33,v33,s88
v_mul_f32 v118,v30,v118
v_mul_f32 v119,v31,v119
v_mul_f32 v120,v32,v120
v_mul_f32 v121,v33,v121
v_exp_f32 v118,v118
v_exp_f32 v119,v119
v_exp_f32 v120,v120
v_exp_f32 v121,v121
M 18,a[240:243],90
L 4
R 46,2,3072
v_rcp_f32 v117,v117
v_fma_f32 v118,v118,s89,s89
v_fma_f32 v119,v119,s89,s89
v_fma_f32 v120,v120,s89,s89
v_fma_f32 v121,v121,s89,s89
v_rcp_f32 v118,v118
v_rcp_f32 v119,v119
v_rcp_f32 v120,v120
v_rcp_f32 v121,v121
v_cmp_eq_u32 vcc,s53,v7
M 18,a[248:251],94
s_add_u32 s68,s68,s63
v_mul_f32 v29,v29,v117
v_cvt_pk_bf16_f32 v123,v28,v29
v_mul_f32 v30,v30,v118
v_mul_f32 v31,v31,v119
v_mul_f32 v32,v32,v120
v_mul_f32 v33,v33,v121
v_cvt_pk_bf16_f32 v124,v30,v31
v_cvt_pk_bf16_f32 v125,v32,v33
M 22,a[244:247],90
L 2
s_cmp_eq_u32 vcc_lo,-1
R 50,2,4096
buffer_store_dwordx4 v[122:125],v0,s[36:39],s67 offen offset:0 sc0 sc1
M 22,a[252:255],94
D 0,56,2048
D 0,56,3072
buffer_store_dword v4,v3,s[36:39],s71 offen offset:0 sc0 sc1
buffer_store_dword v4,v3,s[36:39],s71 offen offset:0 sc0 sc1
s_cbranch_scc1 Lg3o_F1_%=
s_branch Lg3o_K1_%=
Lg3o_X0_%=:
M 22,a[124:127],94
L 3
s_add_u32 m0,s59,0
v_fma_f32 v32,-v102,v112,v32
M 22,a[132:135],34
R 54,1,21504
v_fma_f32 v33,-v102,v113,v33
M 22,a[140:143],38
buffer_load_dword v8,s[36:39],s58 offen offset:0 sc1 lds
M 18,a[128:131],34
R 58,1,22528
v_mul_f32 v30,v103,v30
M 18,a[136:139],38
L 3
v_exp_f32 v117,v117
M 18,a[144:147],42
R 62,1,23552
v_fma_f32 v117,v117,s89,s89
M 18,a[152:155],46
v_rcp_f32 v114,v114
M 22,a[148:151],42
R 66,1,24576
v_mul_f32 v26,v26,v114
M 22,a[156:159],46
L 3
v_rcp_f32 v115,v115
M 22,a[164:167],50
R 70,1,25600
v_mul_f32 v27,v27,v115
M 22,a[172:175],54
v_rcp_f32 v116,v116
M 18,a[160:163],50
R 74,1,26624
v_mul_f32 v28,v28,v116
M 18,a[168:171],54
L 3
v_rcp_f32 v117,v117
M 18,a[176:179],58
R 78,1,27648
v_mul_f32 v29,v29,v117
M 18,a[184:187],62
v_cvt_pk_bf16_f32 v122,v26,v27
v_cvt_pk_bf16_f32 v123,v28,v29
M 22,a[180:183],58
R 82,1,28672
v_mul_f32 v31,v103,v31
M 22,a[188:191],62
L 3
v_mul_f32 v32,v103,v32
v_mul_f32 v33,v103,v33
M 22,a[196:199],66
R 86,1,29696
v_fma_f32 v118,v30,v30,s88
M 22,a[204:207],70
v_fma_f32 v119,v31,v31,s88
v_fma_f32 v120,v32,v32,s88
M 18,a[192:195],66
R 90,1,30720
v_fma_f32 v121,v33,v33,s88
M 18,a[200:203],70
L 3
s_nop 0
v_mul_f32 v118,v30,v118
M 18,a[208:211],74
s_waitcnt vmcnt(0)
R 94,1,31744
v_mul_f32 v119,v31,v119
M 18,a[216:219],78
s_barrier
buffer_store_dword v4,v3,s[36:39],s70 offen offset:0 sc0 sc1
M 22,a[212:215],74
s_mov_b32 s70,s69
ds_read_b32 v7,v6 offset:0
ds_read_b64 v[98:99],v13 offset:0
M 22,a[220:223],78
L 4
v_mul_f32 v120,v32,v120
v_mul_f32 v121,v33,v121
M 22,a[228:231],82
s_mov_b32 s69,s68
v_exp_f32 v118,v118
M 22,a[236:239],86
s_add_u32 s68,s68,s63
v_exp_f32 v119,v119
M 18,a[224:227],82
v_exp_f32 v120,v120
M 18,a[232:235],86
L 2
v_exp_f32 v121,v121
M 18,a[240:243],90
L 1
v_fma_f32 v118,v118,s89,s89
v_fma_f32 v119,v119,s89,s89
v_fma_f32 v120,v120,s89,s89
v_fma_f32 v121,v121,s89,s89
v_rcp_f32 v118,v118
v_rcp_f32 v119,v119
v_rcp_f32 v120,v120
v_rcp_f32 v121,v121
M 18,a[248:251],94
v_mul_f32 v30,v30,v118
v_mul_f32 v31,v31,v119
v_mul_f32 v32,v32,v120
v_mul_f32 v33,v33,v121
v_cvt_pk_bf16_f32 v124,v30,v31
v_cvt_pk_bf16_f32 v125,v32,v33
M 22,a[244:247],90
buffer_store_dwordx4 v[122:125],v0,s[36:39],s67 offen offset:0 sc0 sc1
v_cmp_eq_u32 vcc,s53,v7
M 22,a[252:255],94
s_mov_b32 s80,1
)"
R"(s_cmp_eq_u32 vcc_lo,-1
s_cbranch_scc0 Lg3o_xn0_%=
buffer_load_dword v130,v11,s[36:39],s73 offen offset:0 sc1
L 0
v_fma_f32 v18,-v98,v106,v18
v_fma_f32 v19,-v98,v107,v19
v_fma_f32 v20,-v98,v108,v20
v_fma_f32 v21,-v98,v109,v21
v_mul_f32 v18,v99,v18
v_mul_f32 v19,v99,v19
s_add_u32 s73,s73,s64
s_add_u32 m0,s57,0
s_add_u32 s56,s55,0
D 0,56,0
v_mul_f32 v20,v99,v20
v_mul_f32 v21,v99,v21
v_fma_f32 v114,v18,v18,s88
v_fma_f32 v115,v19,v19,s88
v_fma_f32 v116,v20,v20,s88
v_fma_f32 v117,v21,v21,s88
D 0,56,1024
v_mul_f32 v114,v18,v114
v_mul_f32 v115,v19,v115
v_mul_f32 v116,v20,v116
v_mul_f32 v117,v21,v117
v_exp_f32 v114,v114
v_exp_f32 v115,v115
D 0,56,2048
v_exp_f32 v116,v116
v_exp_f32 v117,v117
v_fma_f32 v114,v114,s89,s89
v_fma_f32 v115,v115,s89,s89
v_fma_f32 v116,v116,s89,s89
v_fma_f32 v117,v117,s89,s89
D 0,56,3072
v_rcp_f32 v114,v114
v_rcp_f32 v115,v115
v_rcp_f32 v116,v116
v_rcp_f32 v117,v117
v_mul_f32 v18,v18,v114
v_mul_f32 v19,v19,v115
s_add_u32 s58,s58,s61
s_add_u32 m0,s57,16384
s_add_u32 s56,s55,16384
D 0,56,0
v_mul_f32 v20,v20,v116
v_mul_f32 v21,v21,v117
v_cvt_pk_bf16_f32 v122,v18,v19
v_cvt_pk_bf16_f32 v123,v20,v21
v_fma_f32 v22,-v98,v110,v22
v_fma_f32 v23,-v98,v111,v23
D 0,56,1024
v_fma_f32 v24,-v98,v112,v24
v_fma_f32 v25,-v98,v113,v25
v_mul_f32 v22,v99,v22
v_mul_f32 v23,v99,v23
v_mul_f32 v24,v99,v24
v_mul_f32 v25,v99,v25
D 0,56,2048
v_fma_f32 v118,v22,v22,s88
v_fma_f32 v119,v23,v23,s88
v_fma_f32 v120,v24,v24,s88
v_fma_f32 v121,v25,v25,s88
v_mul_f32 v118,v22,v118
v_mul_f32 v119,v23,v119
D 0,56,3072
v_mul_f32 v120,v24,v120
v_mul_f32 v121,v25,v121
v_exp_f32 v118,v118
v_exp_f32 v119,v119
v_exp_f32 v120,v120
v_exp_f32 v121,v121
s_add_u32 s55,s55,s60
s_sub_u32 s57,s84,s57
s_add_u32 s54,s54,s46
v_fma_f32 v118,v118,s89,s89
v_fma_f32 v119,v119,s89,s89
v_fma_f32 v120,v120,s89,s89
v_fma_f32 v121,v121,s89,s89
v_rcp_f32 v118,v118
v_rcp_f32 v119,v119
v_rcp_f32 v120,v120
v_rcp_f32 v121,v121
v_mul_f32 v22,v22,v118
v_mul_f32 v23,v23,v119
v_mul_f32 v24,v24,v120
v_mul_f32 v25,v25,v121
v_cvt_pk_bf16_f32 v124,v22,v23
v_cvt_pk_bf16_f32 v125,v24,v25
buffer_store_dwordx4 v[122:125],v0,s[36:39],s66 offen offset:0 sc0 sc1
s_mov_b32 s72,s69
s_mov_b32 s69,0x7fff0000
s_mov_b32 s66,0x7fff0000
s_mov_b32 s76,0x7fff0000
s_branch Lg3o_gox_%=
Lg3o_xn0_%=:
s_cmp_ge_u32 s54,256
s_cbranch_scc1 Lg3o_fin0_%=
s_add_u32 m0,s59,4096
s_nop 0
buffer_load_dword v8,s[36:39],s58 offen offset:0 sc1 lds
buffer_store_dword v4,v3,s[36:39],s71 offen offset:0 sc0 sc1
s_add_u32 m0,s59,5120
s_nop 0
buffer_load_dword v8,s[36:39],s58 offen offset:0 sc1 lds
buffer_store_dword v4,v3,s[36:39],s71 offen offset:0 sc0 sc1
s_mov_b32 s83,0
L 0
v_fma_f32 v18,-v98,v106,v18
v_fma_f32 v19,-v98,v107,v19
v_fma_f32 v20,-v98,v108,v20
v_fma_f32 v21,-v98,v109,v21
v_mul_f32 v18,v99,v18
v_mul_f32 v19,v99,v19
v_mul_f32 v20,v99,v20
v_mul_f32 v21,v99,v21
v_fma_f32 v114,v18,v18,s88
v_fma_f32 v115,v19,v19,s88
v_fma_f32 v116,v20,v20,s88
v_fma_f32 v117,v21,v21,s88
v_mul_f32 v114,v18,v114
v_mul_f32 v115,v19,v115
v_mul_f32 v116,v20,v116
v_mul_f32 v117,v21,v117
v_exp_f32 v114,v114
v_exp_f32 v115,v115
v_exp_f32 v116,v116
v_exp_f32 v117,v117
v_fma_f32 v114,v114,s89,s89
v_fma_f32 v115,v115,s89,s89
v_fma_f32 v116,v116,s89,s89
v_fma_f32 v117,v117,s89,s89
v_rcp_f32 v114,v114
v_rcp_f32 v115,v115
v_rcp_f32 v116,v116
v_rcp_f32 v117,v117
v_mul_f32 v18,v18,v114
v_mul_f32 v19,v19,v115
v_mul_f32 v20,v20,v116
v_mul_f32 v21,v21,v117
v_cvt_pk_bf16_f32 v122,v18,v19
v_cvt_pk_bf16_f32 v123,v20,v21
v_fma_f32 v22,-v98,v110,v22
v_fma_f32 v23,-v98,v111,v23
v_fma_f32 v24,-v98,v112,v24
v_fma_f32 v25,-v98,v113,v25
v_mul_f32 v22,v99,v22
v_mul_f32 v23,v99,v23
v_mul_f32 v24,v99,v24
v_mul_f32 v25,v99,v25
v_fma_f32 v118,v22,v22,s88
v_fma_f32 v119,v23,v23,s88
v_fma_f32 v120,v24,v24,s88
v_fma_f32 v121,v25,v25,s88
v_mul_f32 v118,v22,v118
v_mul_f32 v119,v23,v119
v_mul_f32 v120,v24,v120
v_mul_f32 v121,v25,v121
v_exp_f32 v118,v118
v_exp_f32 v119,v119
v_exp_f32 v120,v120
v_exp_f32 v121,v121
v_fma_f32 v118,v118,s89,s89
v_fma_f32 v119,v119,s89,s89
v_fma_f32 v120,v120,s89,s89
v_fma_f32 v121,v121,s89,s89
v_rcp_f32 v118,v118
v_rcp_f32 v119,v119
v_rcp_f32 v120,v120
v_rcp_f32 v121,v121
v_mul_f32 v22,v22,v118
v_mul_f32 v23,v23,v119
v_mul_f32 v24,v24,v120
v_mul_f32 v25,v25,v121
v_cvt_pk_bf16_f32 v124,v22,v23
v_cvt_pk_bf16_f32 v125,v24,v25
buffer_store_dwordx4 v[122:125],v0,s[36:39],s66 offen offset:0 sc0 sc1
s_mov_b32 s72,s69
s_mov_b32 s69,0x7fff0000
s_mov_b32 s66,0x7fff0000
s_mov_b32 s76,0x7fff0000
s_branch Lg3o_poll_%=
Lg3o_fin0_%=:
L 0
v_fma_f32 v18,-v98,v106,v18
v_fma_f32 v19,-v98,v107,v19
v_fma_f32 v20,-v98,v108,v20
v_fma_f32 v21,-v98,v109,v21
v_mul_f32 v18,v99,v18
v_mul_f32 v19,v99,v19
v_mul_f32 v20,v99,v20
v_mul_f32 v21,v99,v21
v_fma_f32 v114,v18,v18,s88
v_fma_f32 v115,v19,v19,s88
v_fma_f32 v116,v20,v20,s88
v_fma_f32 v117,v21,v21,s88
v_mul_f32 v114,v18,v114
v_mul_f32 v115,v19,v115
v_mul_f32 v116,v20,v116
v_mul_f32 v117,v21,v117
v_exp_f32 v114,v114
v_exp_f32 v115,v115
v_exp_f32 v116,v116
v_exp_f32 v117,v117
v_fma_f32 v114,v114,s89,s89
v_fma_f32 v115,v115,s89,s89
v_fma_f32 v116,v116,s89,s89
v_fma_f32 v117,v117,s89,s89
v_rcp_f32 v114,v114
v_rcp_f32 v115,v115
v_rcp_f32 v116,v116
v_rcp_f32 v117,v117
v_mul_f32 v18,v18,v114
v_mul_f32 v19,v19,v115
v_mul_f32 v20,v20,v116
v_mul_f32 v21,v21,v117
v_cvt_pk_bf16_f32 v122,v18,v19
v_cvt_pk_bf16_f32 v123,v20,v21
v_fma_f32 v22,-v98,v110,v22
v_fma_f32 v23,-v98,v111,v23
v_fma_f32 v24,-v98,v112,v24
v_fma_f32 v25,-v98,v113,v25
v_mul_f32 v22,v99,v22
v_mul_f32 v23,v99,v23
v_mul_f32 v24,v99,v24
v_mul_f32 v25,v99,v25
v_fma_f32 v118,v22,v22,s88
v_fma_f32 v119,v23,v23,s88
v_fma_f32 v120,v24,v24,s88
v_fma_f32 v121,v25,v25,s88
v_mul_f32 v118,v22,v118
v_mul_f32 v119,v23,v119
v_mul_f32 v120,v24,v120
v_mul_f32 v121,v25,v121
v_exp_f32 v118,v118
v_exp_f32 v119,v119
v_exp_f32 v120,v120
v_exp_f32 v121,v121
v_fma_f32 v118,v118,s89,s89
v_fma_f32 v119,v119,s89,s89
v_fma_f32 v120,v120,s89,s89
v_fma_f32 v121,v121,s89,s89
v_rcp_f32 v118,v118
v_rcp_f32 v119,v119
v_rcp_f32 v120,v120
v_rcp_f32 v121,v121
v_mul_f32 v22,v22,v118
v_mul_f32 v23,v23,v119
v_mul_f32 v24,v24,v120
v_mul_f32 v25,v25,v121
v_cvt_pk_bf16_f32 v124,v22,v23
v_cvt_pk_bf16_f32 v125,v24,v25
buffer_store_dwordx4 v[122:125],v0,s[36:39],s66 offen offset:0 sc0 sc1
s_mov_b32 s72,s69
s_mov_b32 s69,0x7fff0000
s_mov_b32 s66,0x7fff0000
s_mov_b32 s76,0x7fff0000
s_branch Lg3o_done_%=
Lg3o_K1_%=:
Lg3o_L1_%=:
v_mfma_f32_16x16x32_bf16 v[30:33],a[4:7],v[34:37],0
s_add_u32 m0,s59,3072
R 54,2,5120
v_add_f32_dpp v131,v130,v130 quad_perm:[1,0,3,2] row_mask:0xf bank_mask:0xf
M 30,a[12:15],38
buffer_load_dword v8,s[36:39],s58 offen offset:0 sc1 lds
v_mfma_f32_16x16x32_bf16 v[26:29],a[0:3],v[34:37],0
s_mov_b32 s67,s65
R 58,2,6144
v_add_f32_dpp v131,v131,v131 quad_perm:[2,3,0,1] row_mask:0xf bank_mask:0xf
M 26,a[8:11],38
L 3
v_fma_f32 v18,-v98,v106,v18
v_fma_f32 v19,-v98,v107,v19
M 26,a[16:19],42
s_add_u32 s65,s65,s62
R 62,2,7168
v_add_f32_dpp v134,v131,v131 row_half_mirror row_mask:0xf bank_mask:0xf
M 26,a[24:27],46
v_fma_f32 v20,-v98,v108,v20
v_fma_f32 v21,-v98,v109,v21
M 30,a[20:23],42
R 66,2,8192
v_mov_b32_dpp v131,v134 row_ror:8 row_mask:0xf bank_mask:0xf
M 30,a[28:31],46
L 3
v_fma_f32 v131,-v134,v134,v131
v_max_f32 v131,s87,v131
M 30,a[36:39],50
R 70,2,9216
v_mul_f32 v134,v15,v134
M 30,a[44:47],54
v_rsq_f32 v132,v131
M 26,a[32:35],50
R 74,2,10240
v_fma_f32 v134,v132,v16,v134
M 26,a[40:43],54
L 3
ds_write_b32 v12,v134 offset:2048
M 26,a[48:51],58
R 78,2,11264
v_mul_f32 v18,v99,v18
M 26,a[56:59],62
v_mul_f32 v19,v99,v19
v_mul_f32 v20,v99,v20
M 30,a[52:55],58
R 82,2,12288
v_mul_f32 v21,v99,v21
M 30,a[60:63],62
L 4
v_fma_f32 v114,v18,v18,s88
v_fma_f32 v115,v19,v19,s88
M 30,a[68:71],66
R 86,2,13312
v_fma_f32 v116,v20,v20,s88
M 30,a[76:79],70
v_fma_f32 v117,v21,v21,s88
v_mul_f32 v114,v18,v114
M 26,a[64:67],66
R 90,2,14336
v_mul_f32 v115,v19,v115
M 26,a[72:75],70
L 3
v_exp_f32 v114,v114
M 26,a[80:83],74
s_waitcnt vmcnt(0)
R 94,2,15360
v_mul_f32 v116,v20,v116
M 26,a[88:91],78
s_barrier
ds_read_b32 v7,v6 offset:3072
v_mul_f32 v117,v21,v117
M 30,a[84:87],74
R 34,2,16384
v_fma_f32 v114,v114,s89,s89
M 30,a[92:95],78
L 4
buffer_store_dword v4,v3,s[36:39],s70 offen offset:0 sc0 sc1
M 30,a[100:103],82
s_mov_b32 s70,0x7fff0000
R 38,2,17408
v_fma_f32 v22,-v98,v110,v22
M 30,a[108:111],86
v_exp_f32 v115,v115
M 26,a[96:99],82
R 42,2,18432
v_fma_f32 v115,v115,s89,s89
M 26,a[104:107],86
L 4
L 3
v_cmp_eq_u32 vcc,s53,v7
M 26,a[112:115],90
R 46,2,19456
v_fma_f32 v23,-v98,v111,v23
M 26,a[120:123],94
v_exp_f32 v116,v116
M 30,a[116:119],90
s_cmp_eq_u32 vcc_lo,-1
R 50,2,20480
v_fma_f32 v116,v116,s89,s89
s_cbranch_scc0 Lg3o_X1_%=
M 30,a[124:127],94
L 3
s_add_u32 m0,s57,0
s_add_u32 s56,s55,0
buffer_load_dword v130,v11,s[36:39],s73 offen offset:0 sc1
M 30,a[132:135],34
s_add_u32 s58,s58,s61
R 54,2,21504
v_fma_f32 v24,-v98,v112,v24
M 30,a[140:143],38
D 0,56,0
M 26,a[128:131],34
s_add_u32 s54,s54,s46
R 58,2,22528
v_fma_f32 v25,-v98,v113,v25
M 26,a[136:139],38
L 3
v_exp_f32 v117,v117
M 26,a[144:147],42
s_add_u32 s73,s73,s64
R 62,2,23552
v_fma_f32 v117,v117,s89,s89
M 26,a[152:155],46
D 0,56,1024
M 30,a[148:151],42
R 66,2,24576
v_mul_f32 v22,v99,v22
M 30,a[156:159],46
L 3
v_rcp_f32 v114,v114
M 30,a[164:167],50
R 70,2,25600
v_mul_f32 v18,v18,v114
M 30,a[172:175],54
D 0,56,2048
M 26,a[160:163],50
R 74,2,26624
v_mul_f32 v23,v99,v23
M 26,a[168:171],54
L 3
v_rcp_f32 v115,v115
M 26,a[176:179],58
R 78,2,27648
v_mul_f32 v19,v19,v115
M 26,a[184:187],62
D 0,56,3072
M 30,a[180:183],58
s_add_u32 m0,s59,1024
R 82,2,28672
v_cvt_pk_bf16_f32 v122,v18,v19
M 30,a[188:191],62
L 3
buffer_load_dword v8,s[36:39],s58 offen offset:0 sc1 lds
M 30,a[196:199],66
s_add_u32 m0,s57,16384
R 86,2,29696
v_mul_f32 v24,v99,v24
M 30,a[204:207],70
s_add_u32 s56,s55,16384
v_rcp_f32 v116,v116
M 26,a[192:195],66
s_add_u32 s55,s55,s60
R 90,2,30720
v_mul_f32 v20,v20,v116
M 26,a[200:203],70
L 3
s_nop 0
D 0,56,0
M 26,a[208:211],74
s_waitcnt vmcnt(1)
R 94,2,31744
v_mul_f32 v25,v99,v25
M 26,a[216:219],78
s_barrier
ds_read_b32 v7,v6 offset:1024
ds_read_b64 v[102:103],v13 offset:2048
M 30,a[212:215],74
s_sub_u32 s57,s84,s57
R 34,1,0
v_fma_f32 v118,v22,v22,s88
M 30,a[220:223],78
L 5
buffer_store_dword v4,v3,s[36:39],s70 offen offset:0 sc0 sc1
M 30,a[228:231],82
s_mov_b32 s70,s69
R 38,1,1024
v_fma_f32 v119,v23,v23,s88
M 30,a[236:239],86
D 0,56,1024
M 26,a[224:227],82
s_mov_b32 s69,s68
R 42,1,2048
v_fma_f32 v120,v24,v24,s88
M 26,a[232:235],86
L 5
v_fma_f32 v121,v25,v25,s88
v_mul_f32 v118,v22,v118
v_mul_f32 v119,v23,v119
v_mul_f32 v120,v24,v120
v_mul_f32 v121,v25,v121
v_exp_f32 v118,v118
v_exp_f32 v119,v119
v_exp_f32 v120,v120
v_exp_f32 v121,v121
M 26,a[240:243],90
L 4
R 46,1,3072
v_rcp_f32 v117,v117
v_fma_f32 v118,v118,s89,s89
v_fma_f32 v119,v119,s89,s89
v_fma_f32 v120,v120,s89,s89
v_fma_f32 v121,v121,s89,s89
v_rcp_f32 v118,v118
v_rcp_f32 v119,v119
v_rcp_f32 v120,v120
v_rcp_f32 v121,v121
v_cmp_eq_u32 vcc,s53,v7
M 26,a[248:251],94
s_add_u32 s68,s68,s63
v_mul_f32 v21,v21,v117
v_cvt_pk_bf16_f32 v123,v20,v21
v_mul_f32 v22,v22,v118
v_mul_f32 v23,v23,v119
v_mul_f32 v24,v24,v120
v_mul_f32 v25,v25,v121
v_cvt_pk_bf16_f32 v124,v22,v23
v_cvt_pk_bf16_f32 v125,v24,v25
M 30,a[244:247],90
L 2
s_cmp_eq_u32 vcc_lo,-1
R 50,1,4096
buffer_store_dwordx4 v[122:125],v0,s[36:39],s66 offen offset:0 sc0 sc1
M 30,a[252:255],94
D 0,56,2048
D 0,56,3072
buffer_store_dword v4,v3,s[36:39],s71 offen offset:0 sc0 sc1
buffer_store_dword v4,v3,s[36:39],s71 offen offset:0 sc0 sc1
s_cbranch_scc1 Lg3o_F0_%=
s_branch Lg3o_K0_%=
)"
R"(Lg3o_X1_%=:
M 30,a[124:127],94
L 3
s_add_u32 m0,s59,1024
v_fma_f32 v24,-v98,v112,v24
M 30,a[132:135],34
R 54,2,21504
v_fma_f32 v25,-v98,v113,v25
M 30,a[140:143],38
buffer_load_dword v8,s[36:39],s58 offen offset:0 sc1 lds
M 26,a[128:131],34
R 58,2,22528
v_mul_f32 v22,v99,v22
M 26,a[136:139],38
L 3
v_exp_f32 v117,v117
M 26,a[144:147],42
R 62,2,23552
v_fma_f32 v117,v117,s89,s89
M 26,a[152:155],46
v_rcp_f32 v114,v114
M 30,a[148:151],42
R 66,2,24576
v_mul_f32 v18,v18,v114
M 30,a[156:159],46
L 3
v_rcp_f32 v115,v115
M 30,a[164:167],50
R 70,2,25600
v_mul_f32 v19,v19,v115
M 30,a[172:175],54
v_rcp_f32 v116,v116
M 26,a[160:163],50
R 74,2,26624
v_mul_f32 v20,v20,v116
M 26,a[168:171],54
L 3
v_rcp_f32 v117,v117
M 26,a[176:179],58
R 78,2,27648
v_mul_f32 v21,v21,v117
M 26,a[184:187],62
v_cvt_pk_bf16_f32 v122,v18,v19
v_cvt_pk_bf16_f32 v123,v20,v21
M 30,a[180:183],58
R 82,2,28672
v_mul_f32 v23,v99,v23
M 30,a[188:191],62
L 3
v_mul_f32 v24,v99,v24
v_mul_f32 v25,v99,v25
M 30,a[196:199],66
R 86,2,29696
v_fma_f32 v118,v22,v22,s88
M 30,a[204:207],70
v_fma_f32 v119,v23,v23,s88
v_fma_f32 v120,v24,v24,s88
M 26,a[192:195],66
R 90,2,30720
v_fma_f32 v121,v25,v25,s88
M 26,a[200:203],70
L 3
s_nop 0
v_mul_f32 v118,v22,v118
M 26,a[208:211],74
s_waitcnt vmcnt(0)
R 94,2,31744
v_mul_f32 v119,v23,v119
M 26,a[216:219],78
s_barrier
buffer_store_dword v4,v3,s[36:39],s70 offen offset:0 sc0 sc1
M 30,a[212:215],74
s_mov_b32 s70,s69
ds_read_b32 v7,v6 offset:1024
ds_read_b64 v[102:103],v13 offset:2048
M 30,a[220:223],78
L 4
v_mul_f32 v120,v24,v120
v_mul_f32 v121,v25,v121
M 30,a[228:231],82
s_mov_b32 s69,s68
v_exp_f32 v118,v118
M 30,a[236:239],86
s_add_u32 s68,s68,s63
v_exp_f32 v119,v119
M 26,a[224:227],82
v_exp_f32 v120,v120
M 26,a[232:235],86
L 2
v_exp_f32 v121,v121
M 26,a[240:243],90
L 1
v_fma_f32 v118,v118,s89,s89
v_fma_f32 v119,v119,s89,s89
v_fma_f32 v120,v120,s89,s89
v_fma_f32 v121,v121,s89,s89
v_rcp_f32 v118,v118
v_rcp_f32 v119,v119
v_rcp_f32 v120,v120
v_rcp_f32 v121,v121
M 26,a[248:251],94
v_mul_f32 v22,v22,v118
v_mul_f32 v23,v23,v119
v_mul_f32 v24,v24,v120
v_mul_f32 v25,v25,v121
v_cvt_pk_bf16_f32 v124,v22,v23
v_cvt_pk_bf16_f32 v125,v24,v25
M 30,a[244:247],90
buffer_store_dwordx4 v[122:125],v0,s[36:39],s66 offen offset:0 sc0 sc1
v_cmp_eq_u32 vcc,s53,v7
M 30,a[252:255],94
s_mov_b32 s80,0
s_cmp_eq_u32 vcc_lo,-1
s_cbranch_scc0 Lg3o_xn1_%=
buffer_load_dword v130,v11,s[36:39],s73 offen offset:0 sc1
L 0
v_fma_f32 v26,-v102,v106,v26
v_fma_f32 v27,-v102,v107,v27
v_fma_f32 v28,-v102,v108,v28
v_fma_f32 v29,-v102,v109,v29
v_mul_f32 v26,v103,v26
v_mul_f32 v27,v103,v27
s_add_u32 s73,s73,s64
s_add_u32 m0,s57,0
s_add_u32 s56,s55,0
D 0,56,0
v_mul_f32 v28,v103,v28
v_mul_f32 v29,v103,v29
v_fma_f32 v114,v26,v26,s88
v_fma_f32 v115,v27,v27,s88
v_fma_f32 v116,v28,v28,s88
v_fma_f32 v117,v29,v29,s88
D 0,56,1024
v_mul_f32 v114,v26,v114
v_mul_f32 v115,v27,v115
v_mul_f32 v116,v28,v116
v_mul_f32 v117,v29,v117
v_exp_f32 v114,v114
v_exp_f32 v115,v115
D 0,56,2048
v_exp_f32 v116,v116
v_exp_f32 v117,v117
v_fma_f32 v114,v114,s89,s89
v_fma_f32 v115,v115,s89,s89
v_fma_f32 v116,v116,s89,s89
v_fma_f32 v117,v117,s89,s89
D 0,56,3072
v_rcp_f32 v114,v114
v_rcp_f32 v115,v115
v_rcp_f32 v116,v116
v_rcp_f32 v117,v117
v_mul_f32 v26,v26,v114
v_mul_f32 v27,v27,v115
s_add_u32 s58,s58,s61
s_add_u32 m0,s57,16384
s_add_u32 s56,s55,16384
D 0,56,0
v_mul_f32 v28,v28,v116
v_mul_f32 v29,v29,v117
v_cvt_pk_bf16_f32 v122,v26,v27
v_cvt_pk_bf16_f32 v123,v28,v29
v_fma_f32 v30,-v102,v110,v30
v_fma_f32 v31,-v102,v111,v31
D 0,56,1024
v_fma_f32 v32,-v102,v112,v32
v_fma_f32 v33,-v102,v113,v33
v_mul_f32 v30,v103,v30
v_mul_f32 v31,v103,v31
v_mul_f32 v32,v103,v32
v_mul_f32 v33,v103,v33
D 0,56,2048
v_fma_f32 v118,v30,v30,s88
v_fma_f32 v119,v31,v31,s88
v_fma_f32 v120,v32,v32,s88
v_fma_f32 v121,v33,v33,s88
v_mul_f32 v118,v30,v118
v_mul_f32 v119,v31,v119
D 0,56,3072
v_mul_f32 v120,v32,v120
v_mul_f32 v121,v33,v121
v_exp_f32 v118,v118
v_exp_f32 v119,v119
v_exp_f32 v120,v120
v_exp_f32 v121,v121
s_add_u32 s55,s55,s60
s_sub_u32 s57,s84,s57
s_add_u32 s54,s54,s46
v_fma_f32 v118,v118,s89,s89
v_fma_f32 v119,v119,s89,s89
v_fma_f32 v120,v120,s89,s89
v_fma_f32 v121,v121,s89,s89
v_rcp_f32 v118,v118
v_rcp_f32 v119,v119
v_rcp_f32 v120,v120
v_rcp_f32 v121,v121
v_mul_f32 v30,v30,v118
v_mul_f32 v31,v31,v119
v_mul_f32 v32,v32,v120
v_mul_f32 v33,v33,v121
v_cvt_pk_bf16_f32 v124,v30,v31
v_cvt_pk_bf16_f32 v125,v32,v33
buffer_store_dwordx4 v[122:125],v0,s[36:39],s67 offen offset:0 sc0 sc1
s_mov_b32 s72,s69
s_mov_b32 s69,0x7fff0000
s_mov_b32 s67,0x7fff0000
s_mov_b32 s77,0x7fff0000
s_branch Lg3o_gox_%=
Lg3o_xn1_%=:
s_cmp_ge_u32 s54,256
s_cbranch_scc1 Lg3o_fin1_%=
s_add_u32 m0,s59,4096
s_nop 0
buffer_load_dword v8,s[36:39],s58 offen offset:0 sc1 lds
buffer_store_dword v4,v3,s[36:39],s71 offen offset:0 sc0 sc1
s_add_u32 m0,s59,5120
s_nop 0
buffer_load_dword v8,s[36:39],s58 offen offset:0 sc1 lds
buffer_store_dword v4,v3,s[36:39],s71 offen offset:0 sc0 sc1
s_mov_b32 s83,0
L 0
v_fma_f32 v26,-v102,v106,v26
v_fma_f32 v27,-v102,v107,v27
v_fma_f32 v28,-v102,v108,v28
v_fma_f32 v29,-v102,v109,v29
v_mul_f32 v26,v103,v26
v_mul_f32 v27,v103,v27
v_mul_f32 v28,v103,v28
v_mul_f32 v29,v103,v29
v_fma_f32 v114,v26,v26,s88
v_fma_f32 v115,v27,v27,s88
v_fma_f32 v116,v28,v28,s88
v_fma_f32 v117,v29,v29,s88
v_mul_f32 v114,v26,v114
v_mul_f32 v115,v27,v115
v_mul_f32 v116,v28,v116
v_mul_f32 v117,v29,v117
v_exp_f32 v114,v114
v_exp_f32 v115,v115
v_exp_f32 v116,v116
v_exp_f32 v117,v117
v_fma_f32 v114,v114,s89,s89
v_fma_f32 v115,v115,s89,s89
v_fma_f32 v116,v116,s89,s89
v_fma_f32 v117,v117,s89,s89
v_rcp_f32 v114,v114
v_rcp_f32 v115,v115
v_rcp_f32 v116,v116
v_rcp_f32 v117,v117
v_mul_f32 v26,v26,v114
v_mul_f32 v27,v27,v115
v_mul_f32 v28,v28,v116
v_mul_f32 v29,v29,v117
v_cvt_pk_bf16_f32 v122,v26,v27
v_cvt_pk_bf16_f32 v123,v28,v29
v_fma_f32 v30,-v102,v110,v30
v_fma_f32 v31,-v102,v111,v31
v_fma_f32 v32,-v102,v112,v32
v_fma_f32 v33,-v102,v113,v33
v_mul_f32 v30,v103,v30
v_mul_f32 v31,v103,v31
v_mul_f32 v32,v103,v32
v_mul_f32 v33,v103,v33
v_fma_f32 v118,v30,v30,s88
v_fma_f32 v119,v31,v31,s88
v_fma_f32 v120,v32,v32,s88
v_fma_f32 v121,v33,v33,s88
v_mul_f32 v118,v30,v118
v_mul_f32 v119,v31,v119
v_mul_f32 v120,v32,v120
v_mul_f32 v121,v33,v121
v_exp_f32 v118,v118
v_exp_f32 v119,v119
v_exp_f32 v120,v120
v_exp_f32 v121,v121
v_fma_f32 v118,v118,s89,s89
v_fma_f32 v119,v119,s89,s89
v_fma_f32 v120,v120,s89,s89
v_fma_f32 v121,v121,s89,s89
v_rcp_f32 v118,v118
v_rcp_f32 v119,v119
v_rcp_f32 v120,v120
v_rcp_f32 v121,v121
v_mul_f32 v30,v30,v118
v_mul_f32 v31,v31,v119
v_mul_f32 v32,v32,v120
v_mul_f32 v33,v33,v121
v_cvt_pk_bf16_f32 v124,v30,v31
v_cvt_pk_bf16_f32 v125,v32,v33
buffer_store_dwordx4 v[122:125],v0,s[36:39],s67 offen offset:0 sc0 sc1
s_mov_b32 s72,s69
s_mov_b32 s69,0x7fff0000
s_mov_b32 s67,0x7fff0000
s_mov_b32 s77,0x7fff0000
s_branch Lg3o_poll_%=
Lg3o_fin1_%=:
L 0
v_fma_f32 v26,-v102,v106,v26
v_fma_f32 v27,-v102,v107,v27
v_fma_f32 v28,-v102,v108,v28
v_fma_f32 v29,-v102,v109,v29
v_mul_f32 v26,v103,v26
v_mul_f32 v27,v103,v27
v_mul_f32 v28,v103,v28
v_mul_f32 v29,v103,v29
v_fma_f32 v114,v26,v26,s88
v_fma_f32 v115,v27,v27,s88
v_fma_f32 v116,v28,v28,s88
v_fma_f32 v117,v29,v29,s88
v_mul_f32 v114,v26,v114
v_mul_f32 v115,v27,v115
v_mul_f32 v116,v28,v116
v_mul_f32 v117,v29,v117
v_exp_f32 v114,v114
v_exp_f32 v115,v115
v_exp_f32 v116,v116
v_exp_f32 v117,v117
v_fma_f32 v114,v114,s89,s89
v_fma_f32 v115,v115,s89,s89
v_fma_f32 v116,v116,s89,s89
v_fma_f32 v117,v117,s89,s89
v_rcp_f32 v114,v114
v_rcp_f32 v115,v115
v_rcp_f32 v116,v116
v_rcp_f32 v117,v117
v_mul_f32 v26,v26,v114
v_mul_f32 v27,v27,v115
v_mul_f32 v28,v28,v116
v_mul_f32 v29,v29,v117
v_cvt_pk_bf16_f32 v122,v26,v27
v_cvt_pk_bf16_f32 v123,v28,v29
v_fma_f32 v30,-v102,v110,v30
v_fma_f32 v31,-v102,v111,v31
v_fma_f32 v32,-v102,v112,v32
v_fma_f32 v33,-v102,v113,v33
v_mul_f32 v30,v103,v30
v_mul_f32 v31,v103,v31
v_mul_f32 v32,v103,v32
v_mul_f32 v33,v103,v33
v_fma_f32 v118,v30,v30,s88
v_fma_f32 v119,v31,v31,s88
v_fma_f32 v120,v32,v32,s88
v_fma_f32 v121,v33,v33,s88
v_mul_f32 v118,v30,v118
v_mul_f32 v119,v31,v119
v_mul_f32 v120,v32,v120
v_mul_f32 v121,v33,v121
v_exp_f32 v118,v118
v_exp_f32 v119,v119
v_exp_f32 v120,v120
v_exp_f32 v121,v121
v_fma_f32 v118,v118,s89,s89
v_fma_f32 v119,v119,s89,s89
v_fma_f32 v120,v120,s89,s89
v_fma_f32 v121,v121,s89,s89
v_rcp_f32 v118,v118
v_rcp_f32 v119,v119
v_rcp_f32 v120,v120
v_rcp_f32 v121,v121
v_mul_f32 v30,v30,v118
v_mul_f32 v31,v31,v119
v_mul_f32 v32,v32,v120
v_mul_f32 v33,v33,v121
v_cvt_pk_bf16_f32 v124,v30,v31
v_cvt_pk_bf16_f32 v125,v32,v33
buffer_store_dwordx4 v[122:125],v0,s[36:39],s67 offen offset:0 sc0 sc1
s_mov_b32 s72,s69
s_mov_b32 s69,0x7fff0000
s_mov_b32 s67,0x7fff0000
s_mov_b32 s77,0x7fff0000
s_branch Lg3o_done_%=
Lg3o_done_%=:
s_waitcnt vmcnt(1)
s_barrier
buffer_store_dword v4,v3,s[36:39],s70 offen offset:0 sc0 sc1
s_waitcnt vmcnt(1)
s_barrier
buffer_store_dword v4,v3,s[36:39],s72 offen offset:0 sc0 sc1
s_waitcnt vmcnt(0) lgkmcnt(0)
s_barrier
s_endpgm
Lcode_g3o_e:
.purgem M
.purgem R
.purgem T
.purgem D
.purgem L
)"
  :: "s"(ws), "s"(__builtin_amdgcn_readfirstlane(sWave)), "s"(__builtin_amdgcn_readfirstlane(sG0)), "s"(__builtin_amdgcn_readfirstlane(sStep)), "s"(__builtin_amdgcn_readfirstlane(sOutOff)), "s"(__builtin_amdgcn_readfirstlane(sFoutOff)), "s"(__builtin_amdgcn_readfirstlane(sAuxOff)), "s"(__builtin_amdgcn_readfirstlane(sWw)), "s"(__builtin_amdgcn_readfirstlane(sBuf)), "s"(__builtin_amdgcn_readfirstlane(sExp)), "v"(vL16), "v"(vFlag), "v"(vSnapOff), "v"(vSnapRd), "v"(vWd0), "v"(vWd1), "v"(vWr0), "v"(vWr1), "v"(vWr2), "v"(vOutS), "v"(vAux0), "v"(vAux1), "v"(vAux2), "v"(vAux3), "v"(vAn), "v"(vAe), "s"(w3), "s"(out) : "v255", "v0", "v1", "v2", "v3", "v4", "v5", "v6", "v7", "v8", "v9", "v10", "v11", "v12", "v13", "v14", "v15", "v16", "v17", "v18", "v19", "v20", "v21", "v22", "v23", "v24", "v25", "v26", "v27", "v28", "v29", "v30", "v31", "v32", "v33", "v34", "v35", "v36", "v37", "v38", "v39", "v40", "v41", "v42", "v43", "v44", "v45", "v46", "v47", "v48", "v49", "v50", "v51", "v52", "v53", "v54", "v55", "v56", "v57", "v58", "v59", "v60", "v61", "v62", "v63", "v64", "v65", "v66", "v67", "v68", "v69", "v70", "v71", "v72", "v73", "v74", "v75", "v76", "v77", "v78", "v79", "v80", "v81", "v82", "v83", "v84", "v85", "v86", "v87", "v88", "v89", "v90", "v91", "v92", "v93", "v94", "v95", "v96", "v97", "v98", "v99", "v100", "v101", "v102", "v103", "v104", "v105", "v106", "v107", "v108", "v109", "v110", "v111", "v112", "v113", "v114", "v115", "v116", "v117", "v118", "v119", "v120", "v121", "v122", "v123", "v124", "v125", "v126", "v127", "v128", "v129", "v130", "v131", "v132", "v133", "v134", "v135", "v136", "v137", "v138", "v139", "v140", "v141", "v142", "v143", "v144", "v145", "v146", "v147", "v148", "v149", "v150", "v151", "v152", "v153", "v154", "v155", "v156", "v157", "v158", "v159", "v160", "v161", "v162", "v163", "v164", "v165", "v166", "v167", "v168", "v169", "v170", "v171", "v172", "v173", "v174", "v175", "v176", "v177", "v178", "v179", "v180", "v181", "v182", "v183", "v184", "v185", "v186", "v187", "v188", "v189", "v190", "v191", "v192", "v193", "v194", "v195", "v196", "v197", "v198", "v199", "v200", "v201", "v202", "v203", "v204", "v205", "v206", "v207", "v208", "v209", "v210", "v211", "v212", "v213", "v214", "v215", "v216", "v217", "v218", "v219", "v220", "v221", "v222", "v223", "v224", "v225", "v226", "v227", "v228", "v229", "v230", "v231", "a0", "a1", "a2", "a3", "a4", "a5", "a6", "a7", "a8", "a9", "a10", "a11", "a12", "a13", "a14", "a15", "a16", "a17", "a18", "a19", "a20", "a21", "a22", "a23", "a24", "a25", "a26", "a27", "a28", "a29", "a30", "a31", "a32", "a33", "a34", "a35", "a36", "a37", "a38", "a39", "a40", "a41", "a42", "a43", "a44", "a45", "a46", "a47", "a48", "a49", "a50", "a51", "a52", "a53", "a54", "a55", "a56", "a57", "a58", "a59", "a60", "a61", "a62", "a63", "a64", "a65", "a66", "a67", "a68", "a69", "a70", "a71", "a72", "a73", "a74", "a75", "a76", "a77", "a78", "a79", "a80", "a81", "a82", "a83", "a84", "a85", "a86", "a87", "a88", "a89", "a90", "a91", "a92", "a93", "a94", "a95", "a96", "a97", "a98", "a99", "a100", "a101", "a102", "a103", "a104", "a105", "a106", "a107", "a108", "a109", "a110", "a111", "a112", "a113", "a114", "a115", "a116", "a117", "a118", "a119", "a120", "a121", "a122", "a123", "a124", "a125", "a126", "a127", "a128", "a129", "a130", "a131", "a132", "a133", "a134", "a135", "a136", "a137", "a138", "a139", "a140", "a141", "a142", "a143", "a144", "a145", "a146", "a147", "a148", "a149", "a150", "a151", "a152", "a153", "a154", "a155", "a156", "a157", "a158", "a159", "a160", "a161", "a162", "a163", "a164", "a165", "a166", "a167", "a168", "a169", "a170", "a171", "a172", "a173", "a174", "a175", "a176", "a177", "a178", "a179", "a180", "a181", "a182", "a183", "a184", "a185", "a186", "a187", "a188", "a189", "a190", "a191", "a192", "a193", "a194", "a195", "a196", "a197", "a198", "a199", "a200", "a201", "a202", "a203", "a204", "a205", "a206", "a207", "a208", "a209", "a210", "a211", "a212", "a213", "a214", "a215", "a216", "a217", "a218", "a219", "a220", "a221", "a222", "a223", "a224", "a225", "a226", "a227", "a228", "a229", "a230", "a231", "a232", "a233", "a234", "a235", "a236", "a237", "a238", "a239", "a240", "a241", "a242", "a243", "a244", "a245", "a246", "a247", "a248", "a249", "a250", "a251", "a252", "a253", "a254", "a255", "s36", "s37", "s38", "s39", "s40", "s41", "s42", "s43", "s44", "s45", "s46", "s47", "s48", "s49", "s50", "s51", "s52", "s53", "s54", "s55", "s56", "s57", "s58", "s59", "s60", "s61", "s62", "s63", "s64", "s65", "s66", "s67", "s68", "s69", "s70", "s71", "s72", "s73", "s74", "s75", "s76", "s77", "s78", "s79", "s80", "s81", "s82", "s83", "s84", "s85", "s86", "s87", "s88", "s89", "s90", "s91", "s92", "s93", "s94", "s95", "s96", "s97", "vcc", "scc", "memory"); }
    __builtin_unreachable();
  }
  if (kind < 3) {
    const unsigned sStep = 4;
    COMMON(kind == 1 ? 8 : 16, kind == 1 ? (fj >> 2) * 512 + (fj & 3) * FSX : fj < 8 ? 1024 + fj * FSH : (fj & 2) * 1024 + (fj - 8) * FSH, 1024, 131072, 2048, 256, kind == 1 ? 0 : 32768);
    const int sr = l15 - ((wave & 2) ? -1 : 1), sr2 = l15 - ((wave & 2) ? -2 : 2), nx = (slice >> 1) & 1;
    auto src = [&](int r_) { return (unsigned)((r_ & 16) ? 1024 + lg * 64 + (r_ & 3) * 16 : (lg * 16 + r_) * 16); };
    const unsigned vOutS = src(sr2), vSp = (sr2 & 16) ? OOB : vOutS;
    (void)vWr2;
    const unsigned vAn = kind == 1 ? HU + vL16 : src(sr);
    const unsigned vAe = kind == 1 ? ((l15 >> 2) == (nx ? 0 : 3) ? (nx ? 0 : 2 * HU) + 1024 + lg * 64 + (l15 & 3) * 16 : OOB) : (sr & 16) ? OOB : vAn;
    const unsigned vAux0 = l15 * 16, vAux1 = 139264 + (l15 * 2 + (lg & 1)) * 16 + wave * 4 + (lg >> 1) * 512, vAux2 = 139264 + (l15 * 2 + (lg & 1)) * 16;
    const unsigned vAux3 = (wave == 0 && lg < 2) ? slice * 128 + l15 * 8 + lg * 4 : OOB;
    const unsigned sOutOff = kind == 1 ? OFF_HS - HU + (slice * 4 + wave) * 1280 : OFF_X1 + (slice * 4 + wave) * 1024, sFoutOff = kind == 1 ? OFF_FH + slice * FSH : OFF_FX1 + slice * FS1;
    const unsigned sAuxOff = OFF_XP + (slice * 4 + wave) * 1024, sBuf = kind, sWw = 16 * wave * 2048 + slice * 256;
    const u16* w12 = kind == 1 ? w1 : w2;
    const unsigned sExp = __builtin_amdgcn_readfirstlane(ep);
    unsigned epOut;
    asm volatile(
R"(.macro M d,w,x
v_mfma_f32_16x16x32_bf16 v[\d:\d+3], \w, v[\x:\x+3], v[\d:\d+3]
.endm
.macro R d,a,o
ds_read_b128 v[\d:\d+3], v\a offset:\o
.endm
.macro T d,a,o
ds_read_b64_tr_b16 \d, v\a offset:\o
.endm
.macro D a,s,o
buffer_load_dwordx4 v\a, s[36:39], s\s offen offset:\o sc1 lds
.endm
.macro L c
s_waitcnt lgkmcnt(\c)
.endm
Lcode_g12_b:
s_cmp_eq_u32 %9,2
s_cbranch_scc1 Lg2_begin_%=
s_mov_b64 s[36:37],%1
s_mov_b32 s38,111243392
s_mov_b32 s39,0x00020000
s_mov_b32 s44,%2
s_mov_b32 s45,%3
s_mov_b32 s46,%4
s_mov_b32 s47,%5
s_mov_b32 s48,%6
s_mov_b32 s49,%7
s_mov_b32 s50,%8
s_mov_b32 s51,%9
s_mov_b32 s53,%10
v_mov_b32 v0,%11
v_mov_b32 v3,%12
v_mov_b32 v5,%13
v_mov_b32 v6,%14
v_mov_b32 v18,%15
v_mov_b32 v19,%16
v_mov_b32 v20,%17
v_mov_b32 v21,%18
v_mov_b32 v9,%19
v_mov_b32 v10,%20
v_mov_b32 v11,%21
v_mov_b32 v12,%22
v_mov_b32 v13,%23
v_mov_b32 v14,%24
v_mov_b32 v15,%25
v_mov_b32 v16,%26
s_mov_b64 s[40:41],%27
s_mov_b32 s42,2097152
s_mov_b32 s43,0x00020000
s_lshl_b32 s57,s44,11
s_waitcnt vmcnt(20) lgkmcnt(0)
s_barrier
s_add_u32 s55,s50,786432
s_add_u32 m0,s57,98304
s_nop 0
buffer_load_dwordx4 v18,s[40:43],s55 offen offset:0  lds
s_add_u32 m0,s57,99328
s_nop 0
buffer_load_dwordx4 v19,s[40:43],s55 offen offset:0  lds
s_add_u32 m0,s57,106368
s_nop 0
buffer_load_dwordx4 v18,s[40:43],s55 offen offset:128  lds
s_add_u32 m0,s57,107392
s_nop 0
buffer_load_dwordx4 v19,s[40:43],s55 offen offset:128  lds
v_add_u32 v26,0,v20
v_add_u32 v27,0,v21
T a[0:1],26,0
T a[2:3],26,512
T a[4:5],27,0
T a[6:7],27,512
T a[8:9],26,4096
T a[10:11],26,4608
T a[12:13],27,4096
T a[14:15],27,4608
s_waitcnt vmcnt(20) lgkmcnt(0)
s_barrier
s_add_u32 s55,s50,917504
s_add_u32 m0,s57,0
s_nop 0
buffer_load_dwordx4 v18,s[40:43],s55 offen offset:0  lds
s_add_u32 m0,s57,1024
s_nop 0
buffer_load_dwordx4 v19,s[40:43],s55 offen offset:0  lds
s_add_u32 m0,s57,8064
s_nop 0
buffer_load_dwordx4 v18,s[40:43],s55 offen offset:128  lds
s_add_u32 m0,s57,9088
s_nop 0
buffer_load_dwordx4 v19,s[40:43],s55 offen offset:128  lds
v_add_u32 v26,16384,v20
v_add_u32 v27,16384,v21
T a[16:17],26,0
T a[18:19],26,512
T a[20:21],27,0
T a[22:23],27,512
T a[24:25],26,4096
T a[26:27],26,4608
T a[28:29],27,4096
T a[30:31],27,4608
s_waitcnt vmcnt(20) lgkmcnt(0)
s_barrier
s_add_u32 s55,s50,1048576
s_add_u32 m0,s57,16384
s_nop 0
buffer_load_dwordx4 v18,s[40:43],s55 offen offset:0  lds
s_add_u32 m0,s57,17408
s_nop 0
buffer_load_dwordx4 v19,s[40:43],s55 offen offset:0  lds
s_add_u32 m0,s57,24448
s_nop 0
buffer_load_dwordx4 v18,s[40:43],s55 offen offset:128  lds
s_add_u32 m0,s57,25472
s_nop 0
buffer_load_dwordx4 v19,s[40:43],s55 offen offset:128  lds
v_add_u32 v26,32768,v20
v_add_u32 v27,32768,v21
T a[32:33],26,0
T a[34:35],26,512
T a[36:37],27,0
T a[38:39],27,512
T a[40:41],26,4096
T a[42:43],26,4608
T a[44:45],27,4096
T a[46:47],27,4608
s_waitcnt vmcnt(20) lgkmcnt(0)
s_barrier
s_add_u32 s55,s50,1179648
s_add_u32 m0,s57,32768
s_nop 0
buffer_load_dwordx4 v18,s[40:43],s55 offen offset:0  lds
s_add_u32 m0,s57,33792
s_nop 0
buffer_load_dwordx4 v19,s[40:43],s55 offen offset:0  lds
s_add_u32 m0,s57,40832
s_nop 0
buffer_load_dwordx4 v18,s[40:43],s55 offen offset:128  lds
s_add_u32 m0,s57,41856
s_nop 0
buffer_load_dwordx4 v19,s[40:43],s55 offen offset:128  lds
v_add_u32 v26,49152,v20
v_add_u32 v27,49152,v21
T a[48:49],26,0
T a[50:51],26,512
T a[52:53],27,0
T a[54:55],27,512
T a[56:57],26,4096
T a[58:59],26,4608
T a[60:61],27,4096
T a[62:63],27,4608
s_waitcnt vmcnt(20) lgkmcnt(0)
s_barrier
s_add_u32 s55,s50,1310720
s_add_u32 m0,s57,49152
s_nop 0
buffer_load_dwordx4 v18,s[40:43],s55 offen offset:0  lds
s_add_u32 m0,s57,50176
s_nop 0
buffer_load_dwordx4 v19,s[40:43],s55 offen offset:0  lds
s_add_u32 m0,s57,57216
s_nop 0
buffer_load_dwordx4 v18,s[40:43],s55 offen offset:128  lds
s_add_u32 m0,s57,58240
s_nop 0
buffer_load_dwordx4 v19,s[40:43],s55 offen offset:128  lds
v_add_u32 v26,65536,v20
v_add_u32 v27,65536,v21
T a[64:65],26,0
T a[66:67],26,512
T a[68:69],27,0
T a[70:71],27,512
T a[72:73],26,4096
T a[74:75],26,4608
T a[76:77],27,4096
T a[78:79],27,4608
s_waitcnt vmcnt(20) lgkmcnt(0)
s_barrier
s_add_u32 s55,s50,1441792
s_add_u32 m0,s57,65536
s_nop 0
buffer_load_dwordx4 v18,s[40:43],s55 offen offset:0  lds
s_add_u32 m0,s57,66560
s_nop 0
buffer_load_dwordx4 v19,s[40:43],s55 offen offset:0  lds
s_add_u32 m0,s57,73600
s_nop 0
buffer_load_dwordx4 v18,s[40:43],s55 offen offset:128  lds
s_add_u32 m0,s57,74624
s_nop 0
buffer_load_dwordx4 v19,s[40:43],s55 offen offset:128  lds
v_add_u32 v26,81920,v20
v_add_u32 v27,81920,v21
T a[80:81],26,0
T a[82:83],26,512
T a[84:85],27,0
T a[86:87],27,512
T a[88:89],26,4096
T a[90:91],26,4608
T a[92:93],27,4096
T a[94:95],27,4608
s_waitcnt vmcnt(20) lgkmcnt(0)
s_barrier
s_add_u32 s55,s50,1572864
s_add_u32 m0,s57,81920
s_nop 0
buffer_load_dwordx4 v18,s[40:43],s55 offen offset:0  lds
s_add_u32 m0,s57,82944
s_nop 0
buffer_load_dwordx4 v19,s[40:43],s55 offen offset:0  lds
s_add_u32 m0,s57,89984
s_nop 0
buffer_load_dwordx4 v18,s[40:43],s55 offen offset:128  lds
s_add_u32 m0,s57,91008
s_nop 0
buffer_load_dwordx4 v19,s[40:43],s55 offen offset:128  lds
v_add_u32 v26,98304,v20
v_add_u32 v27,98304,v21
T a[96:97],26,0
T a[98:99],26,512
T a[100:101],27,0
T a[102:103],27,512
T a[104:105],26,4096
T a[106:107],26,4608
T a[108:109],27,4096
T a[110:111],27,4608
s_waitcnt vmcnt(20) lgkmcnt(0)
s_barrier
s_add_u32 s55,s50,1703936
s_add_u32 m0,s57,98304
s_nop 0
buffer_load_dwordx4 v18,s[40:43],s55 offen offset:0  lds
s_add_u32 m0,s57,99328
s_nop 0
buffer_load_dwordx4 v19,s[40:43],s55 offen offset:0  lds
s_add_u32 m0,s57,106368
s_nop 0
buffer_load_dwordx4 v18,s[40:43],s55 offen offset:128  lds
s_add_u32 m0,s57,107392
s_nop 0
buffer_load_dwordx4 v19,s[40:43],s55 offen offset:128  lds
v_add_u32 v26,0,v20
v_add_u32 v27,0,v21
T a[112:113],26,0
T a[114:115],26,512
T a[116:117],27,0
T a[118:119],27,512
T a[120:121],26,4096
T a[122:123],26,4608
T a[124:125],27,4096
T a[126:127],27,4608
s_waitcnt vmcnt(20) lgkmcnt(0)
s_barrier
s_add_u32 s55,s50,1835008
s_add_u32 m0,s57,0
s_nop 0
buffer_load_dwordx4 v18,s[40:43],s55 offen offset:0  lds
s_add_u32 m0,s57,1024
s_nop 0
buffer_load_dwordx4 v19,s[40:43],s55 offen offset:0  lds
s_add_u32 m0,s57,8064
s_nop 0
buffer_load_dwordx4 v18,s[40:43],s55 offen offset:128  lds
s_add_u32 m0,s57,9088
s_nop 0
buffer_load_dwordx4 v19,s[40:43],s55 offen offset:128  lds
v_add_u32 v26,16384,v20
v_add_u32 v27,16384,v21
T a[128:129],26,0
T a[130:131],26,512
T a[132:133],27,0
T a[134:135],27,512
T a[136:137],26,4096
T a[138:139],26,4608
T a[140:141],27,4096
T a[142:143],27,4608
s_waitcnt vmcnt(20) lgkmcnt(0)
s_barrier
s_add_u32 s55,s50,1966080
s_add_u32 m0,s57,16384
s_nop 0
buffer_load_dwordx4 v18,s[40:43],s55 offen offset:0  lds
s_add_u32 m0,s57,17408
s_nop 0
buffer_load_dwordx4 v19,s[40:43],s55 offen offset:0  lds
s_add_u32 m0,s57,24448
s_nop 0
buffer_load_dwordx4 v18,s[40:43],s55 offen offset:128  lds
s_add_u32 m0,s57,25472
s_nop 0
buffer_load_dwordx4 v19,s[40:43],s55 offen offset:128  lds
v_add_u32 v26,32768,v20
v_add_u32 v27,32768,v21
T a[144:145],26,0
T a[146:147],26,512
T a[148:149],27,0
T a[150:151],27,512
T a[152:153],26,4096
T a[154:155],26,4608
T a[156:157],27,4096
T a[158:159],27,4608
s_waitcnt vmcnt(20) lgkmcnt(0)
s_barrier
v_add_u32 v26,49152,v20
v_add_u32 v27,49152,v21
T a[160:161],26,0
T a[162:163],26,512
T a[164:165],27,0
T a[166:167],27,512
T a[168:169],26,4096
T a[170:171],26,4608
T a[172:173],27,4096
T a[174:175],27,4608
s_waitcnt vmcnt(16) lgkmcnt(0)
s_barrier
v_add_u32 v26,65536,v20
v_add_u32 v27,65536,v21
T a[176:177],26,0
T a[178:179],26,512
T a[180:181],27,0
T a[182:183],27,512
T a[184:185],26,4096
T a[186:187],26,4608
T a[188:189],27,4096
T a[190:191],27,4608
s_waitcnt vmcnt(12) lgkmcnt(0)
s_barrier
v_add_u32 v26,81920,v20
v_add_u32 v27,81920,v21
T a[192:193],26,0
T a[194:195],26,512
T a[196:197],27,0
T a[198:199],27,512
T a[200:201],26,4096
T a[202:203],26,4608
T a[204:205],27,4096
T a[206:207],27,4608
s_waitcnt vmcnt(8) lgkmcnt(0)
s_barrier
v_add_u32 v26,98304,v20
v_add_u32 v27,98304,v21
T a[208:209],26,0
T a[210:211],26,512
T a[212:213],27,0
T a[214:215],27,512
T a[216:217],26,4096
T a[218:219],26,4608
T a[220:221],27,4096
T a[222:223],27,4608
s_waitcnt vmcnt(4) lgkmcnt(0)
s_barrier
v_add_u32 v26,0,v20
v_add_u32 v27,0,v21
T a[224:225],26,0
T a[226:227],26,512
T a[228:229],27,0
T a[230:231],27,512
T a[232:233],26,4096
T a[234:235],26,4608
T a[236:237],27,4096
T a[238:239],27,4608
s_waitcnt vmcnt(0) lgkmcnt(0)
s_barrier
v_add_u32 v26,16384,v20
v_add_u32 v27,16384,v21
T a[240:241],26,0
T a[242:243],26,512
T a[244:245],27,0
T a[246:247],27,512
T a[248:249],26,4096
T a[250:251],26,4608
T a[252:253],27,4096
T a[254:255],27,4608
L 0
s_barrier
v_mov_b32 v34,0x3f803f80
v_mov_b32 v35,0x3f803f80
v_mov_b32 v36,0x3f803f80
v_mov_b32 v37,0x3f803f80
s_nop 1
v_mfma_f32_16x16x32_bf16 v[106:109],a[0:3],v[34:37],0
v_mfma_f32_16x16x32_bf16 v[110:113],a[4:7],v[34:37],0
M 106,a[8:11],34
M 110,a[12:15],34
M 106,a[16:19],34
M 110,a[20:23],34
M 106,a[24:27],34
M 110,a[28:31],34
M 106,a[32:35],34
M 110,a[36:39],34
M 106,a[40:43],34
M 110,a[44:47],34
M 106,a[48:51],34
M 110,a[52:55],34
M 106,a[56:59],34
M 110,a[60:63],34
M 106,a[64:67],34
M 110,a[68:71],34
M 106,a[72:75],34
M 110,a[76:79],34
M 106,a[80:83],34
M 110,a[84:87],34
M 106,a[88:91],34
M 110,a[92:95],34
M 106,a[96:99],34
M 110,a[100:103],34
M 106,a[104:107],34
M 110,a[108:111],34
M 106,a[112:115],34
M 110,a[116:119],34
M 106,a[120:123],34
M 110,a[124:127],34
M 106,a[128:131],34
M 110,a[132:135],34
M 106,a[136:139],34
M 110,a[140:143],34
M 106,a[144:147],34
M 110,a[148:151],34
M 106,a[152:155],34
M 110,a[156:159],34
M 106,a[160:163],34
M 110,a[164:167],34
M 106,a[168:171],34
M 110,a[172:175],34
M 106,a[176:179],34
M 110,a[180:183],34
M 106,a[184:187],34
M 110,a[188:191],34
M 106,a[192:195],34
M 110,a[196:199],34
M 106,a[200:203],34
M 110,a[204:207],34
M 106,a[208:211],34
M 110,a[212:215],34
M 106,a[216:219],34
M 110,a[220:223],34
M 106,a[224:227],34
M 110,a[228:231],34
M 106,a[232:235],34
M 110,a[236:239],34
M 106,a[240:243],34
M 110,a[244:247],34
M 106,a[248:251],34
M 110,a[252:255],34
v_mov_b32 v4,s53
s_mov_b32 s82,0
s_mov_b32 s80,0
s_mov_b32 s54,s45
s_mul_i32 s57,s44,4096
s_mul_i32 s55,s45,32768
s_add_u32 s55,s55,s57
s_add_u32 s55,s55,8388608
s_mul_i32 s57,s44,4096
s_mul_i32 s60,s46,32768
s_mul_i32 s61,s46,1024
s_mul_i32 s62,s46,40960
s_mul_i32 s63,s46,1024
s_mul_i32 s64,s46,512
s_lshl_b32 s59,s44,8
s_add_u32 s59,s59,131072
s_mul_i32 s58,s45,1024
s_add_u32 s58,s58,109056000
s_mul_i32 s65,s45,40960
s_add_u32 s65,s65,s47
s_mul_i32 s68,s45,1024
s_add_u32 s68,s68,s48
s_mul_i32 s73,s45,512
s_add_u32 s73,s73,106954752
s_mul_i32 s74,s45,32768
s_add_u32 s74,s74,s49
s_mul_i32 s75,s45,1024
s_add_u32 s75,s75,107479040
s_mov_b32 s66,0x7fff0000
s_mov_b32 s67,0x7fff0000
s_mov_b32 s69,0x7fff0000
s_mov_b32 s76,0x7fff0000
s_mov_b32 s77,0x7fff0000
s_mov_b32 s70,0x7fff0000
s_mov_b32 s71,0x7fff0000
s_mov_b32 s72,0x7fff0000
v_mov_b32 v1,v0
v_add_u32 v2,16384,v0
s_mov_b32 s84,16384
s_mov_b32 s86,0x3a800000
s_mov_b32 s87,0x3727c5ac
s_mov_b32 s88,0x409d30fc
s_mov_b32 s89,0xbeeff558
s_lshr_b32 s85,s44,1
s_mul_i32 s85,s85,127
v_lshlrev_b32 v8,22,v0
v_and_b32 v8,0x80000000,v8
v_or_b32 v8,v8,v5
v_add_u32 v2,32768,v0
s_lshl_b32 s84,s57,1
s_add_u32 s84,s84,32768
s_getpc_b64 s[40:41]
Lg1_pc_%=:
s_lshl_b32 s78,s44,13
v_lshlrev_b32 v114,3,v0
v_add_u32 v114,s78,v114
v_min_u32 v114,Lcode_g2_b-Lg1_pc_%=-4,v114
)"
R"(s_mov_b32 m0,163584
s_nop 0
global_load_lds_dword v114,s[40:41]
s_add_u32 m0,s59,4096
s_nop 0
buffer_load_dword v8,s[36:39],s58 offen offset:0 sc1 lds
buffer_store_dword v4,v3,s[36:39],s71 offen offset:0 sc0 sc1
s_add_u32 m0,s59,5120
s_nop 0
buffer_load_dword v8,s[36:39],s58 offen offset:0 sc1 lds
buffer_store_dword v4,v3,s[36:39],s71 offen offset:0 sc0 sc1
s_mov_b32 s83,0
Lg1_poll_%=:
s_add_u32 s78,s83,2
s_and_b32 s78,s78,3
s_lshl_b32 s78,s78,10
s_add_u32 s78,s78,4096
s_add_u32 m0,s59,s78
s_nop 0
buffer_load_dword v8,s[36:39],s58 offen offset:0 sc1 lds
s_and_b32 s78,s83,3
s_lshl_b32 s78,s78,10
v_add_u32 v114,s78,v6
s_waitcnt vmcnt(4)
s_barrier
ds_read_b32 v7,v114 offset:4096
buffer_store_dword v4,v3,s[36:39],s70 offen offset:0 sc0 sc1
s_mov_b32 s70,s71
s_mov_b32 s71,s72
s_mov_b32 s72,0x7fff0000
s_add_u32 s83,s83,1
s_add_u32 s82,s82,1
L 0
v_cmp_eq_u32 vcc,s53,v7
s_nop 0
s_cmp_eq_u32 vcc_lo,-1
s_cbranch_scc1 Lg1_go_%=
s_cmp_lt_u32 s82,20000
s_cbranch_scc1 Lg1_poll_%=
Lg1_go_%=:
s_add_u32 m0,s57,0
s_add_u32 s56,s55,0
D 0,56,0
D 0,56,1024
D 0,56,2048
D 0,56,3072
s_add_u32 s58,s58,s61
s_add_u32 m0,s59,2048
s_nop 0
buffer_load_dword v8,s[36:39],s58 offen offset:0 sc1 lds
s_add_u32 m0,s57,16384
s_add_u32 s56,s55,16384
D 0,56,0
D 0,56,1024
D 0,56,2048
D 0,56,3072
s_add_u32 s55,s55,s60
s_sub_u32 s57,s84,s57
s_add_u32 s54,s54,s46
s_waitcnt vmcnt(4)
s_barrier
buffer_store_dword v4,v3,s[36:39],s70 offen offset:0 sc0 sc1
s_min_u32 s71,s71,s72
buffer_store_dword v4,v3,s[36:39],s71 offen offset:0 sc0 sc1
s_mov_b32 s70,0x7fff0000
s_mov_b32 s71,0x7fff0000
s_mov_b32 s72,0x7fff0000
s_cmp_eq_u32 s80,1
buffer_store_dword v4,v3,s[36:39],s71 offen offset:0 sc0 sc1
s_cbranch_scc1 Lg1_st1_%=
s_branch Lg1_st0_%=
Lg1_gox_%=:
s_add_u32 m0,s59,2048
s_nop 0
buffer_load_dword v8,s[36:39],s58 offen offset:0 sc1 lds
s_waitcnt vmcnt(0)
s_barrier
buffer_store_dword v4,v3,s[36:39],s70 offen offset:0 sc0 sc1
s_min_u32 s71,s71,s72
buffer_store_dword v4,v3,s[36:39],s71 offen offset:0 sc0 sc1
s_mov_b32 s70,0x7fff0000
s_mov_b32 s71,0x7fff0000
s_mov_b32 s72,0x7fff0000
s_cmp_eq_u32 s80,1
s_cbranch_scc1 Lg1_st1_%=
Lg1_st0_%=:
ds_read_b32 v7,v6 offset:2048
R 34,1,0
R 38,1,1024
R 42,1,2048
R 46,1,3072
R 50,1,4096
L 5
v_cmp_eq_u32 vcc,s53,v7
L 3
s_cmp_eq_u32 vcc_lo,-1
s_cbranch_scc1 Lg1_F0_%=
s_branch Lg1_L0_%=
Lg1_st1_%=:
ds_read_b32 v7,v6 offset:2048
R 34,2,0
R 38,2,1024
R 42,2,2048
R 46,2,3072
R 50,2,4096
L 5
v_cmp_eq_u32 vcc,s53,v7
L 3
s_cmp_eq_u32 vcc_lo,-1
s_cbranch_scc1 Lg1_F1_%=
s_branch Lg1_L1_%=
Lg1_F0_%=:
v_mfma_f32_16x16x32_bf16 v[22:25],a[4:7],v[34:37],0
s_add_u32 s58,s58,s61
s_add_u32 m0,s59,0
R 54,1,5120
M 22,a[12:15],38
buffer_load_dword v8,s[36:39],s58 offen offset:0 sc1 lds
v_mfma_f32_16x16x32_bf16 v[18:21],a[0:3],v[34:37],0
s_add_u32 m0,s57,0
R 58,1,6144
v_fma_f32 v26,-v102,v106,v26
M 18,a[8:11],38
L 3
s_add_u32 s56,s55,0
v_fma_f32 v27,-v102,v107,v27
M 18,a[16:19],42
s_mov_b32 s66,s65
R 62,1,7168
v_fma_f32 v28,-v102,v108,v28
M 18,a[24:27],46
D 0,56,0
M 22,a[20:23],42
s_add_u32 s65,s65,s62
R 66,1,8192
v_fma_f32 v29,-v102,v109,v29
v_mul_f32 v26,v103,v26
M 22,a[28:31],46
L 3
D 0,56,1024
M 22,a[36:39],50
s_add_u32 s54,s54,s46
R 70,1,9216
v_mul_f32 v27,v103,v27
M 22,a[44:47],54
v_mul_f32 v28,v103,v28
v_mul_f32 v29,v103,v29
M 18,a[32:35],50
R 74,1,10240
v_fma_f32 v114,v26,v26,s88
M 18,a[40:43],54
L 3
D 0,56,2048
M 18,a[48:51],58
R 78,1,11264
v_fma_f32 v115,v27,v27,s88
M 18,a[56:59],62
D 0,56,3072
v_fma_f32 v116,v28,v28,s88
M 22,a[52:55],58
s_add_u32 m0,s57,16384
R 82,1,12288
v_fma_f32 v117,v29,v29,s88
M 22,a[60:63],62
L 3
v_mul_f32 v114,v26,v114
v_mul_f32 v115,v27,v115
M 22,a[68:71],66
s_add_u32 s56,s55,16384
R 86,1,13312
v_mul_f32 v116,v28,v116
M 22,a[76:79],70
buffer_load_dwordx2 v[98:99],v11,s[36:39],s73 offen offset:0 sc1
M 18,a[64:67],66
s_add_u32 s73,s73,s64
R 90,1,14336
v_mul_f32 v117,v29,v117
M 18,a[72:75],70
L 3
v_exp_f32 v114,v114
v_fma_f32 v30,-v102,v110,v30
M 18,a[80:83],74
s_waitcnt vmcnt(9)
R 94,1,15360
v_fma_f32 v114,v114,s89,s89
M 18,a[88:91],78
s_barrier
v_exp_f32 v115,v115
M 22,a[84:87],74
s_add_u32 s55,s55,s60
R 34,1,16384
v_fma_f32 v115,v115,s89,s89
M 22,a[92:95],78
L 3
v_exp_f32 v116,v116
M 22,a[100:103],82
s_sub_u32 s57,s84,s57
R 38,1,17408
v_fma_f32 v116,v116,s89,s89
M 22,a[108:111],86
D 0,56,0
M 18,a[96:99],82
R 42,1,18432
v_exp_f32 v117,v117
M 18,a[104:107],86
L 3
v_rcp_f32 v114,v114
M 18,a[112:115],90
R 46,1,19456
v_fma_f32 v117,v117,s89,s89
M 18,a[120:123],94
v_rcp_f32 v115,v115
M 22,a[116:119],90
R 50,1,20480
v_mul_f32 v26,v26,v114
M 22,a[124:127],94
L 3
v_rcp_f32 v116,v116
M 22,a[132:135],34
R 54,1,21504
v_mul_f32 v27,v27,v115
M 22,a[140:143],38
D 0,56,1024
v_mul_f32 v28,v28,v116
M 18,a[128:131],34
R 58,1,22528
v_cvt_pk_bf16_f32 v122,v26,v27
M 18,a[136:139],38
L 3
v_rcp_f32 v117,v117
M 18,a[144:147],42
R 62,1,23552
v_mul_f32 v29,v29,v117
M 18,a[152:155],46
v_cvt_pk_bf16_f32 v123,v28,v29
v_fma_f32 v31,-v102,v111,v31
M 22,a[148:151],42
R 66,1,24576
v_fma_f32 v32,-v102,v112,v32
M 22,a[156:159],46
L 3
D 0,56,2048
v_fma_f32 v33,-v102,v113,v33
M 22,a[164:167],50
R 70,1,25600
v_mul_f32 v30,v103,v30
M 22,a[172:175],54
v_mul_f32 v31,v103,v31
v_mul_f32 v32,v103,v32
M 18,a[160:163],50
R 74,1,26624
v_mul_f32 v33,v103,v33
M 18,a[168:171],54
L 3
v_fma_f32 v118,v30,v30,s88
v_fma_f32 v119,v31,v31,s88
M 18,a[176:179],58
R 78,1,27648
v_fma_f32 v120,v32,v32,s88
M 18,a[184:187],62
v_fma_f32 v121,v33,v33,s88
v_mul_f32 v118,v30,v118
M 22,a[180:183],58
R 82,1,28672
v_exp_f32 v118,v118
M 22,a[188:191],62
L 3
D 0,56,3072
M 22,a[196:199],66
R 86,1,29696
v_mul_f32 v119,v31,v119
M 22,a[204:207],70
v_exp_f32 v119,v119
M 18,a[192:195],66
R 90,1,30720
v_mul_f32 v120,v32,v120
M 18,a[200:203],70
L 3
v_mul_f32 v121,v33,v121
v_fma_f32 v118,v118,s89,s89
M 18,a[208:211],74
s_waitcnt vmcnt(5)
R 94,1,31744
v_fma_f32 v119,v119,s89,s89
M 18,a[216:219],78
s_barrier
ds_read_b32 v7,v6 offset:0
v_exp_f32 v120,v120
M 22,a[212:215],74
R 34,2,0
v_fma_f32 v120,v120,s89,s89
M 22,a[220:223],78
L 4
buffer_store_dword v4,v3,s[36:39],s70 offen offset:0 sc0 sc1
v_exp_f32 v121,v121
M 22,a[228:231],82
s_mov_b32 s70,s69
R 38,2,1024
v_fma_f32 v121,v121,s89,s89
v_rcp_f32 v118,v118
v_rcp_f32 v119,v119
v_rcp_f32 v120,v120
v_rcp_f32 v121,v121
M 22,a[236:239],86
s_mov_b32 s69,s68
v_mul_f32 v30,v30,v118
v_mul_f32 v31,v31,v119
v_mul_f32 v32,v32,v120
v_mul_f32 v33,v33,v121
v_cvt_pk_bf16_f32 v124,v30,v31
v_cvt_pk_bf16_f32 v125,v32,v33
M 18,a[224:227],82
s_add_u32 s68,s68,s63
R 42,2,2048
buffer_store_dwordx4 v[122:125],v15,s[36:39],s67 offen offset:0 sc0 sc1
M 18,a[232:235],86
L 4
L 3
M 18,a[240:243],90
R 46,2,3072
v_cmp_eq_u32 vcc,s53,v7
M 18,a[248:251],94
buffer_store_dwordx4 v[122:125],v16,s[36:39],s67 offen offset:0 sc0 sc1
M 22,a[244:247],90
s_waitcnt vmcnt(7) lgkmcnt(2)
s_cmp_eq_u32 vcc_lo,-1
R 50,2,4096
M 22,a[252:255],94
s_cbranch_scc0 Lg1_K1_%=
Lg1_F1_%=:
v_mfma_f32_16x16x32_bf16 v[30:33],a[4:7],v[34:37],0
s_add_u32 s58,s58,s61
s_add_u32 m0,s59,1024
R 54,2,5120
M 30,a[12:15],38
buffer_load_dword v8,s[36:39],s58 offen offset:0 sc1 lds
v_mfma_f32_16x16x32_bf16 v[26:29],a[0:3],v[34:37],0
s_add_u32 m0,s57,0
R 58,2,6144
v_fma_f32 v18,-v98,v106,v18
M 26,a[8:11],38
L 3
s_add_u32 s56,s55,0
v_fma_f32 v19,-v98,v107,v19
M 26,a[16:19],42
s_mov_b32 s67,s65
R 62,2,7168
v_fma_f32 v20,-v98,v108,v20
M 26,a[24:27],46
D 0,56,0
M 30,a[20:23],42
s_add_u32 s65,s65,s62
R 66,2,8192
v_fma_f32 v21,-v98,v109,v21
v_mul_f32 v18,v99,v18
M 30,a[28:31],46
L 3
D 0,56,1024
M 30,a[36:39],50
s_add_u32 s54,s54,s46
R 70,2,9216
v_mul_f32 v19,v99,v19
M 30,a[44:47],54
v_mul_f32 v20,v99,v20
v_mul_f32 v21,v99,v21
M 26,a[32:35],50
R 74,2,10240
v_fma_f32 v114,v18,v18,s88
M 26,a[40:43],54
L 3
D 0,56,2048
M 26,a[48:51],58
R 78,2,11264
v_fma_f32 v115,v19,v19,s88
M 26,a[56:59],62
D 0,56,3072
v_fma_f32 v116,v20,v20,s88
M 30,a[52:55],58
s_add_u32 m0,s57,16384
R 82,2,12288
v_fma_f32 v117,v21,v21,s88
M 30,a[60:63],62
L 3
v_mul_f32 v114,v18,v114
v_mul_f32 v115,v19,v115
M 30,a[68:71],66
s_add_u32 s56,s55,16384
R 86,2,13312
v_mul_f32 v116,v20,v116
M 30,a[76:79],70
buffer_load_dwordx2 v[102:103],v11,s[36:39],s73 offen offset:0 sc1
M 26,a[64:67],66
s_add_u32 s73,s73,s64
R 90,2,14336
v_mul_f32 v117,v21,v117
M 26,a[72:75],70
L 3
v_exp_f32 v114,v114
v_fma_f32 v22,-v98,v110,v22
M 26,a[80:83],74
s_waitcnt vmcnt(9)
R 94,2,15360
v_fma_f32 v114,v114,s89,s89
M 26,a[88:91],78
s_barrier
v_exp_f32 v115,v115
M 30,a[84:87],74
s_add_u32 s55,s55,s60
R 34,2,16384
v_fma_f32 v115,v115,s89,s89
M 30,a[92:95],78
L 3
v_exp_f32 v116,v116
M 30,a[100:103],82
s_sub_u32 s57,s84,s57
R 38,2,17408
v_fma_f32 v116,v116,s89,s89
M 30,a[108:111],86
D 0,56,0
M 26,a[96:99],82
R 42,2,18432
v_exp_f32 v117,v117
M 26,a[104:107],86
L 3
v_rcp_f32 v114,v114
M 26,a[112:115],90
R 46,2,19456
v_fma_f32 v117,v117,s89,s89
M 26,a[120:123],94
v_rcp_f32 v115,v115
M 30,a[116:119],90
R 50,2,20480
v_mul_f32 v18,v18,v114
M 30,a[124:127],94
L 3
v_rcp_f32 v116,v116
M 30,a[132:135],34
R 54,2,21504
v_mul_f32 v19,v19,v115
M 30,a[140:143],38
D 0,56,1024
v_mul_f32 v20,v20,v116
M 26,a[128:131],34
R 58,2,22528
v_cvt_pk_bf16_f32 v122,v18,v19
M 26,a[136:139],38
L 3
v_rcp_f32 v117,v117
M 26,a[144:147],42
R 62,2,23552
v_mul_f32 v21,v21,v117
M 26,a[152:155],46
v_cvt_pk_bf16_f32 v123,v20,v21
v_fma_f32 v23,-v98,v111,v23
M 30,a[148:151],42
R 66,2,24576
v_fma_f32 v24,-v98,v112,v24
M 30,a[156:159],46
L 3
D 0,56,2048
v_fma_f32 v25,-v98,v113,v25
M 30,a[164:167],50
R 70,2,25600
v_mul_f32 v22,v99,v22
M 30,a[172:175],54
v_mul_f32 v23,v99,v23
v_mul_f32 v24,v99,v24
M 26,a[160:163],50
R 74,2,26624
v_mul_f32 v25,v99,v25
M 26,a[168:171],54
L 3
v_fma_f32 v118,v22,v22,s88
v_fma_f32 v119,v23,v23,s88
M 26,a[176:179],58
R 78,2,27648
v_fma_f32 v120,v24,v24,s88
M 26,a[184:187],62
v_fma_f32 v121,v25,v25,s88
v_mul_f32 v118,v22,v118
M 30,a[180:183],58
R 82,2,28672
v_exp_f32 v118,v118
M 30,a[188:191],62
L 3
D 0,56,3072
M 30,a[196:199],66
R 86,2,29696
v_mul_f32 v119,v23,v119
M 30,a[204:207],70
v_exp_f32 v119,v119
M 26,a[192:195],66
R 90,2,30720
v_mul_f32 v120,v24,v120
M 26,a[200:203],70
L 3
v_mul_f32 v121,v25,v121
v_fma_f32 v118,v118,s89,s89
M 26,a[208:211],74
s_waitcnt vmcnt(5)
)"
R"(R 94,2,31744
v_fma_f32 v119,v119,s89,s89
M 26,a[216:219],78
s_barrier
ds_read_b32 v7,v6 offset:1024
v_exp_f32 v120,v120
M 30,a[212:215],74
R 34,1,0
v_fma_f32 v120,v120,s89,s89
M 30,a[220:223],78
L 4
buffer_store_dword v4,v3,s[36:39],s70 offen offset:0 sc0 sc1
v_exp_f32 v121,v121
M 30,a[228:231],82
s_mov_b32 s70,s69
R 38,1,1024
v_fma_f32 v121,v121,s89,s89
v_rcp_f32 v118,v118
v_rcp_f32 v119,v119
v_rcp_f32 v120,v120
v_rcp_f32 v121,v121
M 30,a[236:239],86
s_mov_b32 s69,s68
v_mul_f32 v22,v22,v118
v_mul_f32 v23,v23,v119
v_mul_f32 v24,v24,v120
v_mul_f32 v25,v25,v121
v_cvt_pk_bf16_f32 v124,v22,v23
v_cvt_pk_bf16_f32 v125,v24,v25
M 26,a[224:227],82
s_add_u32 s68,s68,s63
R 42,1,2048
buffer_store_dwordx4 v[122:125],v15,s[36:39],s66 offen offset:0 sc0 sc1
M 26,a[232:235],86
L 4
L 3
M 26,a[240:243],90
R 46,1,3072
v_cmp_eq_u32 vcc,s53,v7
M 26,a[248:251],94
buffer_store_dwordx4 v[122:125],v16,s[36:39],s66 offen offset:0 sc0 sc1
M 30,a[244:247],90
s_waitcnt vmcnt(7) lgkmcnt(2)
s_cmp_eq_u32 vcc_lo,-1
R 50,1,4096
M 30,a[252:255],94
s_cbranch_scc1 Lg1_F0_%=
Lg1_K0_%=:
Lg1_L0_%=:
v_mfma_f32_16x16x32_bf16 v[22:25],a[4:7],v[34:37],0
s_add_u32 m0,s59,3072
s_mov_b32 s66,s65
R 54,1,5120
M 22,a[12:15],38
buffer_load_dword v8,s[36:39],s58 offen offset:0 sc1 lds
v_mfma_f32_16x16x32_bf16 v[18:21],a[0:3],v[34:37],0
s_add_u32 s65,s65,s62
R 58,1,6144
v_fma_f32 v26,-v102,v106,v26
M 18,a[8:11],38
L 3
v_fma_f32 v27,-v102,v107,v27
v_fma_f32 v28,-v102,v108,v28
M 18,a[16:19],42
R 62,1,7168
v_fma_f32 v29,-v102,v109,v29
M 18,a[24:27],46
v_mul_f32 v26,v103,v26
v_mul_f32 v27,v103,v27
M 22,a[20:23],42
R 66,1,8192
v_mul_f32 v28,v103,v28
M 22,a[28:31],46
L 3
v_mul_f32 v29,v103,v29
v_fma_f32 v114,v26,v26,s88
M 22,a[36:39],50
R 70,1,9216
v_fma_f32 v115,v27,v27,s88
M 22,a[44:47],54
v_fma_f32 v116,v28,v28,s88
v_fma_f32 v117,v29,v29,s88
M 18,a[32:35],50
R 74,1,10240
v_mul_f32 v114,v26,v114
M 18,a[40:43],54
L 3
v_exp_f32 v114,v114
M 18,a[48:51],58
R 78,1,11264
v_mul_f32 v115,v27,v115
M 18,a[56:59],62
v_exp_f32 v115,v115
M 22,a[52:55],58
R 82,1,12288
v_mul_f32 v116,v28,v116
M 22,a[60:63],62
L 3
v_exp_f32 v116,v116
M 22,a[68:71],66
R 86,1,13312
v_mul_f32 v117,v29,v117
M 22,a[76:79],70
buffer_load_dwordx2 v[98:99],v11,s[36:39],s73 offen offset:0 sc1
M 18,a[64:67],66
s_add_u32 s73,s73,s64
R 90,1,14336
v_fma_f32 v114,v114,s89,s89
M 18,a[72:75],70
L 3
v_exp_f32 v117,v117
M 18,a[80:83],74
s_waitcnt vmcnt(1)
R 94,1,15360
v_fma_f32 v115,v115,s89,s89
M 18,a[88:91],78
s_barrier
ds_read_b32 v7,v6 offset:3072
v_fma_f32 v116,v116,s89,s89
M 22,a[84:87],74
R 34,1,16384
v_fma_f32 v117,v117,s89,s89
M 22,a[92:95],78
L 4
buffer_store_dword v4,v3,s[36:39],s70 offen offset:0 sc0 sc1
M 22,a[100:103],82
s_mov_b32 s70,0x7fff0000
R 38,1,17408
v_fma_f32 v30,-v102,v110,v30
M 22,a[108:111],86
v_rcp_f32 v114,v114
M 18,a[96:99],82
R 42,1,18432
v_mul_f32 v26,v26,v114
M 18,a[104:107],86
L 4
L 3
v_cmp_eq_u32 vcc,s53,v7
M 18,a[112:115],90
R 46,1,19456
v_fma_f32 v31,-v102,v111,v31
M 18,a[120:123],94
v_rcp_f32 v115,v115
M 22,a[116:119],90
s_cmp_eq_u32 vcc_lo,-1
R 50,1,20480
v_mul_f32 v27,v27,v115
s_cbranch_scc0 Lg1_X0_%=
M 22,a[124:127],94
L 3
s_add_u32 m0,s57,0
s_add_u32 s56,s55,0
M 22,a[132:135],34
s_add_u32 s58,s58,s61
R 54,1,21504
v_cvt_pk_bf16_f32 v122,v26,v27
M 22,a[140:143],38
D 0,56,0
M 18,a[128:131],34
s_add_u32 s54,s54,s46
R 58,1,22528
v_fma_f32 v32,-v102,v112,v32
M 18,a[136:139],38
L 3
v_rcp_f32 v116,v116
M 18,a[144:147],42
R 62,1,23552
v_mul_f32 v28,v28,v116
M 18,a[152:155],46
D 0,56,1024
M 22,a[148:151],42
R 66,1,24576
v_fma_f32 v33,-v102,v113,v33
M 22,a[156:159],46
L 3
v_rcp_f32 v117,v117
M 22,a[164:167],50
R 70,1,25600
v_mul_f32 v29,v29,v117
M 22,a[172:175],54
D 0,56,2048
M 18,a[160:163],50
R 74,1,26624
v_cvt_pk_bf16_f32 v123,v28,v29
M 18,a[168:171],54
L 3
v_mul_f32 v30,v103,v30
v_mul_f32 v31,v103,v31
M 18,a[176:179],58
R 78,1,27648
v_mul_f32 v32,v103,v32
M 18,a[184:187],62
D 0,56,3072
M 22,a[180:183],58
s_add_u32 m0,s59,0
R 82,1,28672
v_mul_f32 v33,v103,v33
M 22,a[188:191],62
L 3
buffer_load_dword v8,s[36:39],s58 offen offset:0 sc1 lds
M 22,a[196:199],66
s_add_u32 m0,s57,16384
R 86,1,29696
v_fma_f32 v118,v30,v30,s88
M 22,a[204:207],70
s_add_u32 s56,s55,16384
v_fma_f32 v119,v31,v31,s88
v_fma_f32 v120,v32,v32,s88
M 18,a[192:195],66
s_add_u32 s55,s55,s60
R 90,1,30720
v_fma_f32 v121,v33,v33,s88
M 18,a[200:203],70
L 3
D 0,56,0
M 18,a[208:211],74
s_waitcnt vmcnt(1)
R 94,1,31744
v_mul_f32 v118,v30,v118
M 18,a[216:219],78
s_barrier
ds_read_b32 v7,v6 offset:0
v_mul_f32 v119,v31,v119
M 22,a[212:215],74
s_sub_u32 s57,s84,s57
R 34,2,0
v_mul_f32 v120,v32,v120
M 22,a[220:223],78
L 4
buffer_store_dword v4,v3,s[36:39],s70 offen offset:0 sc0 sc1
v_mul_f32 v121,v33,v121
v_exp_f32 v118,v118
v_exp_f32 v119,v119
v_exp_f32 v120,v120
v_exp_f32 v121,v121
M 22,a[228:231],82
s_mov_b32 s70,s69
R 38,2,1024
v_fma_f32 v118,v118,s89,s89
v_fma_f32 v119,v119,s89,s89
v_fma_f32 v120,v120,s89,s89
v_fma_f32 v121,v121,s89,s89
v_rcp_f32 v118,v118
v_rcp_f32 v119,v119
v_rcp_f32 v120,v120
v_rcp_f32 v121,v121
M 22,a[236:239],86
s_mov_b32 s69,s68
v_mul_f32 v30,v30,v118
v_mul_f32 v31,v31,v119
v_mul_f32 v32,v32,v120
v_mul_f32 v33,v33,v121
v_cvt_pk_bf16_f32 v124,v30,v31
v_cvt_pk_bf16_f32 v125,v32,v33
M 18,a[224:227],82
s_add_u32 s68,s68,s63
R 42,2,2048
buffer_store_dwordx4 v[122:125],v15,s[36:39],s67 offen offset:0 sc0 sc1
M 18,a[232:235],86
L 4
L 3
M 18,a[240:243],90
R 46,2,3072
v_cmp_eq_u32 vcc,s53,v7
M 18,a[248:251],94
D 0,56,1024
M 22,a[244:247],90
L 2
s_cmp_eq_u32 vcc_lo,-1
R 50,2,4096
buffer_store_dwordx4 v[122:125],v16,s[36:39],s67 offen offset:0 sc0 sc1
M 22,a[252:255],94
D 0,56,2048
D 0,56,3072
buffer_store_dword v4,v3,s[36:39],s71 offen offset:0 sc0 sc1
buffer_store_dword v4,v3,s[36:39],s71 offen offset:0 sc0 sc1
buffer_store_dword v4,v3,s[36:39],s71 offen offset:0 sc0 sc1
s_cbranch_scc1 Lg1_F1_%=
s_branch Lg1_K1_%=
Lg1_X0_%=:
M 22,a[124:127],94
L 3
s_add_u32 m0,s59,0
v_cvt_pk_bf16_f32 v122,v26,v27
M 22,a[132:135],34
R 54,1,21504
v_fma_f32 v32,-v102,v112,v32
M 22,a[140:143],38
buffer_load_dword v8,s[36:39],s58 offen offset:0 sc1 lds
M 18,a[128:131],34
R 58,1,22528
v_fma_f32 v33,-v102,v113,v33
M 18,a[136:139],38
L 3
v_rcp_f32 v116,v116
M 18,a[144:147],42
R 62,1,23552
v_mul_f32 v28,v28,v116
M 18,a[152:155],46
v_rcp_f32 v117,v117
M 22,a[148:151],42
R 66,1,24576
v_mul_f32 v29,v29,v117
M 22,a[156:159],46
L 3
v_cvt_pk_bf16_f32 v123,v28,v29
v_mul_f32 v30,v103,v30
M 22,a[164:167],50
R 70,1,25600
v_mul_f32 v31,v103,v31
M 22,a[172:175],54
v_mul_f32 v32,v103,v32
v_mul_f32 v33,v103,v33
M 18,a[160:163],50
R 74,1,26624
v_fma_f32 v118,v30,v30,s88
M 18,a[168:171],54
L 3
v_fma_f32 v119,v31,v31,s88
v_fma_f32 v120,v32,v32,s88
M 18,a[176:179],58
R 78,1,27648
v_fma_f32 v121,v33,v33,s88
M 18,a[184:187],62
v_mul_f32 v118,v30,v118
v_mul_f32 v119,v31,v119
M 22,a[180:183],58
R 82,1,28672
v_mul_f32 v120,v32,v120
M 22,a[188:191],62
L 3
v_exp_f32 v118,v118
M 22,a[196:199],66
R 86,1,29696
v_mul_f32 v121,v33,v121
M 22,a[204:207],70
v_exp_f32 v119,v119
M 18,a[192:195],66
R 90,1,30720
v_fma_f32 v118,v118,s89,s89
M 18,a[200:203],70
L 3
v_exp_f32 v120,v120
M 18,a[208:211],74
s_waitcnt vmcnt(0)
R 94,1,31744
v_fma_f32 v119,v119,s89,s89
M 18,a[216:219],78
s_barrier
buffer_store_dword v4,v3,s[36:39],s70 offen offset:0 sc0 sc1
M 22,a[212:215],74
s_mov_b32 s70,s69
ds_read_b32 v7,v6 offset:0
v_fma_f32 v120,v120,s89,s89
M 22,a[220:223],78
L 3
v_exp_f32 v121,v121
M 22,a[228:231],82
s_mov_b32 s69,s68
v_fma_f32 v121,v121,s89,s89
v_rcp_f32 v118,v118
v_rcp_f32 v119,v119
v_rcp_f32 v120,v120
v_rcp_f32 v121,v121
M 22,a[236:239],86
s_add_u32 s68,s68,s63
v_mul_f32 v30,v30,v118
v_mul_f32 v31,v31,v119
v_mul_f32 v32,v32,v120
v_mul_f32 v33,v33,v121
v_cvt_pk_bf16_f32 v124,v30,v31
v_cvt_pk_bf16_f32 v125,v32,v33
M 18,a[224:227],82
buffer_store_dwordx4 v[122:125],v15,s[36:39],s67 offen offset:0 sc0 sc1
M 18,a[232:235],86
L 1
M 18,a[240:243],90
L 0
v_cmp_eq_u32 vcc,s53,v7
M 18,a[248:251],94
buffer_store_dwordx4 v[122:125],v16,s[36:39],s67 offen offset:0 sc0 sc1
M 22,a[244:247],90
s_nop 0
M 22,a[252:255],94
s_mov_b32 s80,1
s_cmp_eq_u32 vcc_lo,-1
s_cbranch_scc0 Lg1_xn0_%=
s_add_u32 m0,s57,0
s_add_u32 s56,s55,0
D 0,56,0
v_fma_f32 v18,-v98,v106,v18
v_fma_f32 v19,-v98,v107,v19
v_fma_f32 v20,-v98,v108,v20
v_fma_f32 v21,-v98,v109,v21
v_mul_f32 v18,v99,v18
v_mul_f32 v19,v99,v19
v_mul_f32 v20,v99,v20
D 0,56,1024
v_mul_f32 v21,v99,v21
v_fma_f32 v114,v18,v18,s88
v_fma_f32 v115,v19,v19,s88
v_fma_f32 v116,v20,v20,s88
v_fma_f32 v117,v21,v21,s88
v_mul_f32 v114,v18,v114
v_mul_f32 v115,v19,v115
D 0,56,2048
v_mul_f32 v116,v20,v116
v_mul_f32 v117,v21,v117
v_exp_f32 v114,v114
v_exp_f32 v115,v115
v_exp_f32 v116,v116
v_exp_f32 v117,v117
v_fma_f32 v114,v114,s89,s89
D 0,56,3072
v_fma_f32 v115,v115,s89,s89
v_fma_f32 v116,v116,s89,s89
v_fma_f32 v117,v117,s89,s89
v_rcp_f32 v114,v114
v_rcp_f32 v115,v115
v_rcp_f32 v116,v116
v_rcp_f32 v117,v117
s_add_u32 s58,s58,s61
s_add_u32 m0,s57,16384
s_add_u32 s56,s55,16384
D 0,56,0
v_mul_f32 v18,v18,v114
v_mul_f32 v19,v19,v115
v_mul_f32 v20,v20,v116
v_mul_f32 v21,v21,v117
v_cvt_pk_bf16_f32 v122,v18,v19
v_cvt_pk_bf16_f32 v123,v20,v21
v_fma_f32 v22,-v98,v110,v22
D 0,56,1024
v_fma_f32 v23,-v98,v111,v23
v_fma_f32 v24,-v98,v112,v24
v_fma_f32 v25,-v98,v113,v25
v_mul_f32 v22,v99,v22
v_mul_f32 v23,v99,v23
v_mul_f32 v24,v99,v24
v_mul_f32 v25,v99,v25
D 0,56,2048
v_fma_f32 v118,v22,v22,s88
v_fma_f32 v119,v23,v23,s88
v_fma_f32 v120,v24,v24,s88
v_fma_f32 v121,v25,v25,s88
v_mul_f32 v118,v22,v118
v_mul_f32 v119,v23,v119
v_mul_f32 v120,v24,v120
D 0,56,3072
v_mul_f32 v121,v25,v121
v_exp_f32 v118,v118
v_exp_f32 v119,v119
v_exp_f32 v120,v120
v_exp_f32 v121,v121
v_fma_f32 v118,v118,s89,s89
v_fma_f32 v119,v119,s89,s89
s_add_u32 s55,s55,s60
s_sub_u32 s57,s84,s57
s_add_u32 s54,s54,s46
v_fma_f32 v120,v120,s89,s89
v_fma_f32 v121,v121,s89,s89
v_rcp_f32 v118,v118
v_rcp_f32 v119,v119
v_rcp_f32 v120,v120
v_rcp_f32 v121,v121
v_mul_f32 v22,v22,v118
v_mul_f32 v23,v23,v119
v_mul_f32 v24,v24,v120
v_mul_f32 v25,v25,v121
v_cvt_pk_bf16_f32 v124,v22,v23
v_cvt_pk_bf16_f32 v125,v24,v25
buffer_store_dwordx4 v[122:125],v15,s[36:39],s66 offen offset:0 sc0 sc1
buffer_store_dwordx4 v[122:125],v16,s[36:39],s66 offen offset:0 sc0 sc1
s_mov_b32 s72,s69
s_mov_b32 s69,0x7fff0000
s_mov_b32 s66,0x7fff0000
s_mov_b32 s76,0x7fff0000
s_branch Lg1_gox_%=
Lg1_xn0_%=:
s_cmp_ge_u32 s54,256
s_cbranch_scc1 Lg1_fin0_%=
s_add_u32 m0,s59,4096
s_nop 0
buffer_load_dword v8,s[36:39],s58 offen offset:0 sc1 lds
buffer_store_dword v4,v3,s[36:39],s71 offen offset:0 sc0 sc1
s_add_u32 m0,s59,5120
s_nop 0
buffer_load_dword v8,s[36:39],s58 offen offset:0 sc1 lds
buffer_store_dword v4,v3,s[36:39],s71 offen offset:0 sc0 sc1
s_mov_b32 s83,0
v_fma_f32 v18,-v98,v106,v18
v_fma_f32 v19,-v98,v107,v19
v_fma_f32 v20,-v98,v108,v20
v_fma_f32 v21,-v98,v109,v21
v_mul_f32 v18,v99,v18
v_mul_f32 v19,v99,v19
v_mul_f32 v20,v99,v20
v_mul_f32 v21,v99,v21
v_fma_f32 v114,v18,v18,s88
v_fma_f32 v115,v19,v19,s88
)"
R"(v_fma_f32 v116,v20,v20,s88
v_fma_f32 v117,v21,v21,s88
v_mul_f32 v114,v18,v114
v_mul_f32 v115,v19,v115
v_mul_f32 v116,v20,v116
v_mul_f32 v117,v21,v117
v_exp_f32 v114,v114
v_exp_f32 v115,v115
v_exp_f32 v116,v116
v_exp_f32 v117,v117
v_fma_f32 v114,v114,s89,s89
v_fma_f32 v115,v115,s89,s89
v_fma_f32 v116,v116,s89,s89
v_fma_f32 v117,v117,s89,s89
v_rcp_f32 v114,v114
v_rcp_f32 v115,v115
v_rcp_f32 v116,v116
v_rcp_f32 v117,v117
v_mul_f32 v18,v18,v114
v_mul_f32 v19,v19,v115
v_mul_f32 v20,v20,v116
v_mul_f32 v21,v21,v117
v_cvt_pk_bf16_f32 v122,v18,v19
v_cvt_pk_bf16_f32 v123,v20,v21
v_fma_f32 v22,-v98,v110,v22
v_fma_f32 v23,-v98,v111,v23
v_fma_f32 v24,-v98,v112,v24
v_fma_f32 v25,-v98,v113,v25
v_mul_f32 v22,v99,v22
v_mul_f32 v23,v99,v23
v_mul_f32 v24,v99,v24
v_mul_f32 v25,v99,v25
v_fma_f32 v118,v22,v22,s88
v_fma_f32 v119,v23,v23,s88
v_fma_f32 v120,v24,v24,s88
v_fma_f32 v121,v25,v25,s88
v_mul_f32 v118,v22,v118
v_mul_f32 v119,v23,v119
v_mul_f32 v120,v24,v120
v_mul_f32 v121,v25,v121
v_exp_f32 v118,v118
v_exp_f32 v119,v119
v_exp_f32 v120,v120
v_exp_f32 v121,v121
v_fma_f32 v118,v118,s89,s89
v_fma_f32 v119,v119,s89,s89
v_fma_f32 v120,v120,s89,s89
v_fma_f32 v121,v121,s89,s89
v_rcp_f32 v118,v118
v_rcp_f32 v119,v119
v_rcp_f32 v120,v120
v_rcp_f32 v121,v121
v_mul_f32 v22,v22,v118
v_mul_f32 v23,v23,v119
v_mul_f32 v24,v24,v120
v_mul_f32 v25,v25,v121
v_cvt_pk_bf16_f32 v124,v22,v23
v_cvt_pk_bf16_f32 v125,v24,v25
buffer_store_dwordx4 v[122:125],v15,s[36:39],s66 offen offset:0 sc0 sc1
buffer_store_dwordx4 v[122:125],v16,s[36:39],s66 offen offset:0 sc0 sc1
s_mov_b32 s72,s69
s_mov_b32 s69,0x7fff0000
s_mov_b32 s66,0x7fff0000
s_mov_b32 s76,0x7fff0000
s_branch Lg1_poll_%=
Lg1_fin0_%=:
v_fma_f32 v18,-v98,v106,v18
v_fma_f32 v19,-v98,v107,v19
v_fma_f32 v20,-v98,v108,v20
v_fma_f32 v21,-v98,v109,v21
v_mul_f32 v18,v99,v18
v_mul_f32 v19,v99,v19
v_mul_f32 v20,v99,v20
v_mul_f32 v21,v99,v21
v_fma_f32 v114,v18,v18,s88
v_fma_f32 v115,v19,v19,s88
v_fma_f32 v116,v20,v20,s88
v_fma_f32 v117,v21,v21,s88
v_mul_f32 v114,v18,v114
v_mul_f32 v115,v19,v115
v_mul_f32 v116,v20,v116
v_mul_f32 v117,v21,v117
v_exp_f32 v114,v114
v_exp_f32 v115,v115
v_exp_f32 v116,v116
v_exp_f32 v117,v117
v_fma_f32 v114,v114,s89,s89
v_fma_f32 v115,v115,s89,s89
v_fma_f32 v116,v116,s89,s89
v_fma_f32 v117,v117,s89,s89
v_rcp_f32 v114,v114
v_rcp_f32 v115,v115
v_rcp_f32 v116,v116
v_rcp_f32 v117,v117
v_mul_f32 v18,v18,v114
v_mul_f32 v19,v19,v115
v_mul_f32 v20,v20,v116
v_mul_f32 v21,v21,v117
v_cvt_pk_bf16_f32 v122,v18,v19
v_cvt_pk_bf16_f32 v123,v20,v21
v_fma_f32 v22,-v98,v110,v22
v_fma_f32 v23,-v98,v111,v23
v_fma_f32 v24,-v98,v112,v24
v_fma_f32 v25,-v98,v113,v25
v_mul_f32 v22,v99,v22
v_mul_f32 v23,v99,v23
v_mul_f32 v24,v99,v24
v_mul_f32 v25,v99,v25
v_fma_f32 v118,v22,v22,s88
v_fma_f32 v119,v23,v23,s88
v_fma_f32 v120,v24,v24,s88
v_fma_f32 v121,v25,v25,s88
v_mul_f32 v118,v22,v118
v_mul_f32 v119,v23,v119
v_mul_f32 v120,v24,v120
v_mul_f32 v121,v25,v121
v_exp_f32 v118,v118
v_exp_f32 v119,v119
v_exp_f32 v120,v120
v_exp_f32 v121,v121
v_fma_f32 v118,v118,s89,s89
v_fma_f32 v119,v119,s89,s89
v_fma_f32 v120,v120,s89,s89
v_fma_f32 v121,v121,s89,s89
v_rcp_f32 v118,v118
v_rcp_f32 v119,v119
v_rcp_f32 v120,v120
v_rcp_f32 v121,v121
v_mul_f32 v22,v22,v118
v_mul_f32 v23,v23,v119
v_mul_f32 v24,v24,v120
v_mul_f32 v25,v25,v121
v_cvt_pk_bf16_f32 v124,v22,v23
v_cvt_pk_bf16_f32 v125,v24,v25
buffer_store_dwordx4 v[122:125],v15,s[36:39],s66 offen offset:0 sc0 sc1
buffer_store_dwordx4 v[122:125],v16,s[36:39],s66 offen offset:0 sc0 sc1
s_mov_b32 s72,s69
s_mov_b32 s69,0x7fff0000
s_mov_b32 s66,0x7fff0000
s_mov_b32 s76,0x7fff0000
s_branch Lg1_done_%=
Lg1_K1_%=:
Lg1_L1_%=:
v_mfma_f32_16x16x32_bf16 v[30:33],a[4:7],v[34:37],0
s_add_u32 m0,s59,3072
s_mov_b32 s67,s65
R 54,2,5120
M 30,a[12:15],38
buffer_load_dword v8,s[36:39],s58 offen offset:0 sc1 lds
v_mfma_f32_16x16x32_bf16 v[26:29],a[0:3],v[34:37],0
s_add_u32 s65,s65,s62
R 58,2,6144
v_fma_f32 v18,-v98,v106,v18
M 26,a[8:11],38
L 3
v_fma_f32 v19,-v98,v107,v19
v_fma_f32 v20,-v98,v108,v20
M 26,a[16:19],42
R 62,2,7168
v_fma_f32 v21,-v98,v109,v21
M 26,a[24:27],46
v_mul_f32 v18,v99,v18
v_mul_f32 v19,v99,v19
M 30,a[20:23],42
R 66,2,8192
v_mul_f32 v20,v99,v20
M 30,a[28:31],46
L 3
v_mul_f32 v21,v99,v21
v_fma_f32 v114,v18,v18,s88
M 30,a[36:39],50
R 70,2,9216
v_fma_f32 v115,v19,v19,s88
M 30,a[44:47],54
v_fma_f32 v116,v20,v20,s88
v_fma_f32 v117,v21,v21,s88
M 26,a[32:35],50
R 74,2,10240
v_mul_f32 v114,v18,v114
M 26,a[40:43],54
L 3
v_exp_f32 v114,v114
M 26,a[48:51],58
R 78,2,11264
v_mul_f32 v115,v19,v115
M 26,a[56:59],62
v_exp_f32 v115,v115
M 30,a[52:55],58
R 82,2,12288
v_mul_f32 v116,v20,v116
M 30,a[60:63],62
L 3
v_exp_f32 v116,v116
M 30,a[68:71],66
R 86,2,13312
v_mul_f32 v117,v21,v117
M 30,a[76:79],70
buffer_load_dwordx2 v[102:103],v11,s[36:39],s73 offen offset:0 sc1
M 26,a[64:67],66
s_add_u32 s73,s73,s64
R 90,2,14336
v_fma_f32 v114,v114,s89,s89
M 26,a[72:75],70
L 3
v_exp_f32 v117,v117
M 26,a[80:83],74
s_waitcnt vmcnt(1)
R 94,2,15360
v_fma_f32 v115,v115,s89,s89
M 26,a[88:91],78
s_barrier
ds_read_b32 v7,v6 offset:3072
v_fma_f32 v116,v116,s89,s89
M 30,a[84:87],74
R 34,2,16384
v_fma_f32 v117,v117,s89,s89
M 30,a[92:95],78
L 4
buffer_store_dword v4,v3,s[36:39],s70 offen offset:0 sc0 sc1
M 30,a[100:103],82
s_mov_b32 s70,0x7fff0000
R 38,2,17408
v_fma_f32 v22,-v98,v110,v22
M 30,a[108:111],86
v_rcp_f32 v114,v114
M 26,a[96:99],82
R 42,2,18432
v_mul_f32 v18,v18,v114
M 26,a[104:107],86
L 4
L 3
v_cmp_eq_u32 vcc,s53,v7
M 26,a[112:115],90
R 46,2,19456
v_fma_f32 v23,-v98,v111,v23
M 26,a[120:123],94
v_rcp_f32 v115,v115
M 30,a[116:119],90
s_cmp_eq_u32 vcc_lo,-1
R 50,2,20480
v_mul_f32 v19,v19,v115
s_cbranch_scc0 Lg1_X1_%=
M 30,a[124:127],94
L 3
s_add_u32 m0,s57,0
s_add_u32 s56,s55,0
M 30,a[132:135],34
s_add_u32 s58,s58,s61
R 54,2,21504
v_cvt_pk_bf16_f32 v122,v18,v19
M 30,a[140:143],38
D 0,56,0
M 26,a[128:131],34
s_add_u32 s54,s54,s46
R 58,2,22528
v_fma_f32 v24,-v98,v112,v24
M 26,a[136:139],38
L 3
v_rcp_f32 v116,v116
M 26,a[144:147],42
R 62,2,23552
v_mul_f32 v20,v20,v116
M 26,a[152:155],46
D 0,56,1024
M 30,a[148:151],42
R 66,2,24576
v_fma_f32 v25,-v98,v113,v25
M 30,a[156:159],46
L 3
v_rcp_f32 v117,v117
M 30,a[164:167],50
R 70,2,25600
v_mul_f32 v21,v21,v117
M 30,a[172:175],54
D 0,56,2048
M 26,a[160:163],50
R 74,2,26624
v_cvt_pk_bf16_f32 v123,v20,v21
M 26,a[168:171],54
L 3
v_mul_f32 v22,v99,v22
v_mul_f32 v23,v99,v23
M 26,a[176:179],58
R 78,2,27648
v_mul_f32 v24,v99,v24
M 26,a[184:187],62
D 0,56,3072
M 30,a[180:183],58
s_add_u32 m0,s59,1024
R 82,2,28672
v_mul_f32 v25,v99,v25
M 30,a[188:191],62
L 3
buffer_load_dword v8,s[36:39],s58 offen offset:0 sc1 lds
M 30,a[196:199],66
s_add_u32 m0,s57,16384
R 86,2,29696
v_fma_f32 v118,v22,v22,s88
M 30,a[204:207],70
s_add_u32 s56,s55,16384
v_fma_f32 v119,v23,v23,s88
v_fma_f32 v120,v24,v24,s88
M 26,a[192:195],66
s_add_u32 s55,s55,s60
R 90,2,30720
v_fma_f32 v121,v25,v25,s88
M 26,a[200:203],70
L 3
D 0,56,0
M 26,a[208:211],74
s_waitcnt vmcnt(1)
R 94,2,31744
v_mul_f32 v118,v22,v118
M 26,a[216:219],78
s_barrier
ds_read_b32 v7,v6 offset:1024
v_mul_f32 v119,v23,v119
M 30,a[212:215],74
s_sub_u32 s57,s84,s57
R 34,1,0
v_mul_f32 v120,v24,v120
M 30,a[220:223],78
L 4
buffer_store_dword v4,v3,s[36:39],s70 offen offset:0 sc0 sc1
v_mul_f32 v121,v25,v121
v_exp_f32 v118,v118
v_exp_f32 v119,v119
v_exp_f32 v120,v120
v_exp_f32 v121,v121
M 30,a[228:231],82
s_mov_b32 s70,s69
R 38,1,1024
v_fma_f32 v118,v118,s89,s89
v_fma_f32 v119,v119,s89,s89
v_fma_f32 v120,v120,s89,s89
v_fma_f32 v121,v121,s89,s89
v_rcp_f32 v118,v118
v_rcp_f32 v119,v119
v_rcp_f32 v120,v120
v_rcp_f32 v121,v121
M 30,a[236:239],86
s_mov_b32 s69,s68
v_mul_f32 v22,v22,v118
v_mul_f32 v23,v23,v119
v_mul_f32 v24,v24,v120
v_mul_f32 v25,v25,v121
v_cvt_pk_bf16_f32 v124,v22,v23
v_cvt_pk_bf16_f32 v125,v24,v25
M 26,a[224:227],82
s_add_u32 s68,s68,s63
R 42,1,2048
buffer_store_dwordx4 v[122:125],v15,s[36:39],s66 offen offset:0 sc0 sc1
M 26,a[232:235],86
L 4
L 3
M 26,a[240:243],90
R 46,1,3072
v_cmp_eq_u32 vcc,s53,v7
M 26,a[248:251],94
D 0,56,1024
M 30,a[244:247],90
L 2
s_cmp_eq_u32 vcc_lo,-1
R 50,1,4096
buffer_store_dwordx4 v[122:125],v16,s[36:39],s66 offen offset:0 sc0 sc1
M 30,a[252:255],94
D 0,56,2048
D 0,56,3072
buffer_store_dword v4,v3,s[36:39],s71 offen offset:0 sc0 sc1
buffer_store_dword v4,v3,s[36:39],s71 offen offset:0 sc0 sc1
buffer_store_dword v4,v3,s[36:39],s71 offen offset:0 sc0 sc1
s_cbranch_scc1 Lg1_F0_%=
s_branch Lg1_K0_%=
Lg1_X1_%=:
M 30,a[124:127],94
L 3
s_add_u32 m0,s59,1024
v_cvt_pk_bf16_f32 v122,v18,v19
M 30,a[132:135],34
R 54,2,21504
v_fma_f32 v24,-v98,v112,v24
M 30,a[140:143],38
buffer_load_dword v8,s[36:39],s58 offen offset:0 sc1 lds
M 26,a[128:131],34
R 58,2,22528
v_fma_f32 v25,-v98,v113,v25
M 26,a[136:139],38
L 3
v_rcp_f32 v116,v116
M 26,a[144:147],42
R 62,2,23552
v_mul_f32 v20,v20,v116
M 26,a[152:155],46
v_rcp_f32 v117,v117
M 30,a[148:151],42
R 66,2,24576
v_mul_f32 v21,v21,v117
M 30,a[156:159],46
L 3
v_cvt_pk_bf16_f32 v123,v20,v21
v_mul_f32 v22,v99,v22
M 30,a[164:167],50
R 70,2,25600
v_mul_f32 v23,v99,v23
M 30,a[172:175],54
v_mul_f32 v24,v99,v24
v_mul_f32 v25,v99,v25
M 26,a[160:163],50
R 74,2,26624
v_fma_f32 v118,v22,v22,s88
M 26,a[168:171],54
L 3
v_fma_f32 v119,v23,v23,s88
v_fma_f32 v120,v24,v24,s88
M 26,a[176:179],58
R 78,2,27648
v_fma_f32 v121,v25,v25,s88
M 26,a[184:187],62
v_mul_f32 v118,v22,v118
v_mul_f32 v119,v23,v119
M 30,a[180:183],58
R 82,2,28672
v_mul_f32 v120,v24,v120
M 30,a[188:191],62
L 3
v_exp_f32 v118,v118
M 30,a[196:199],66
R 86,2,29696
v_mul_f32 v121,v25,v121
M 30,a[204:207],70
v_exp_f32 v119,v119
M 26,a[192:195],66
R 90,2,30720
v_fma_f32 v118,v118,s89,s89
M 26,a[200:203],70
L 3
v_exp_f32 v120,v120
M 26,a[208:211],74
s_waitcnt vmcnt(0)
R 94,2,31744
v_fma_f32 v119,v119,s89,s89
M 26,a[216:219],78
s_barrier
buffer_store_dword v4,v3,s[36:39],s70 offen offset:0 sc0 sc1
M 30,a[212:215],74
s_mov_b32 s70,s69
ds_read_b32 v7,v6 offset:1024
v_fma_f32 v120,v120,s89,s89
M 30,a[220:223],78
L 3
v_exp_f32 v121,v121
M 30,a[228:231],82
s_mov_b32 s69,s68
v_fma_f32 v121,v121,s89,s89
v_rcp_f32 v118,v118
v_rcp_f32 v119,v119
v_rcp_f32 v120,v120
v_rcp_f32 v121,v121
M 30,a[236:239],86
s_add_u32 s68,s68,s63
v_mul_f32 v22,v22,v118
v_mul_f32 v23,v23,v119
v_mul_f32 v24,v24,v120
v_mul_f32 v25,v25,v121
v_cvt_pk_bf16_f32 v124,v22,v23
v_cvt_pk_bf16_f32 v125,v24,v25
M 26,a[224:227],82
buffer_store_dwordx4 v[122:125],v15,s[36:39],s66 offen offset:0 sc0 sc1
M 26,a[232:235],86
L 1
M 26,a[240:243],90
L 0
v_cmp_eq_u32 vcc,s53,v7
M 26,a[248:251],94
buffer_store_dwordx4 v[122:125],v16,s[36:39],s66 offen offset:0 sc0 sc1
M 30,a[244:247],90
s_nop 0
M 30,a[252:255],94
s_mov_b32 s80,0
s_cmp_eq_u32 vcc_lo,-1
s_cbranch_scc0 Lg1_xn1_%=
s_add_u32 m0,s57,0
s_add_u32 s56,s55,0
D 0,56,0
v_fma_f32 v26,-v102,v106,v26
v_fma_f32 v27,-v102,v107,v27
v_fma_f32 v28,-v102,v108,v28
v_fma_f32 v29,-v102,v109,v29
v_mul_f32 v26,v103,v26
v_mul_f32 v27,v103,v27
v_mul_f32 v28,v103,v28
D 0,56,1024
v_mul_f32 v29,v103,v29
v_fma_f32 v114,v26,v26,s88
v_fma_f32 v115,v27,v27,s88
v_fma_f32 v116,v28,v28,s88
v_fma_f32 v117,v29,v29,s88
v_mul_f32 v114,v26,v114
v_mul_f32 v115,v27,v115
D 0,56,2048
)"
R"(v_mul_f32 v116,v28,v116
v_mul_f32 v117,v29,v117
v_exp_f32 v114,v114
v_exp_f32 v115,v115
v_exp_f32 v116,v116
v_exp_f32 v117,v117
v_fma_f32 v114,v114,s89,s89
D 0,56,3072
v_fma_f32 v115,v115,s89,s89
v_fma_f32 v116,v116,s89,s89
v_fma_f32 v117,v117,s89,s89
v_rcp_f32 v114,v114
v_rcp_f32 v115,v115
v_rcp_f32 v116,v116
v_rcp_f32 v117,v117
s_add_u32 s58,s58,s61
s_add_u32 m0,s57,16384
s_add_u32 s56,s55,16384
D 0,56,0
v_mul_f32 v26,v26,v114
v_mul_f32 v27,v27,v115
v_mul_f32 v28,v28,v116
v_mul_f32 v29,v29,v117
v_cvt_pk_bf16_f32 v122,v26,v27
v_cvt_pk_bf16_f32 v123,v28,v29
v_fma_f32 v30,-v102,v110,v30
D 0,56,1024
v_fma_f32 v31,-v102,v111,v31
v_fma_f32 v32,-v102,v112,v32
v_fma_f32 v33,-v102,v113,v33
v_mul_f32 v30,v103,v30
v_mul_f32 v31,v103,v31
v_mul_f32 v32,v103,v32
v_mul_f32 v33,v103,v33
D 0,56,2048
v_fma_f32 v118,v30,v30,s88
v_fma_f32 v119,v31,v31,s88
v_fma_f32 v120,v32,v32,s88
v_fma_f32 v121,v33,v33,s88
v_mul_f32 v118,v30,v118
v_mul_f32 v119,v31,v119
v_mul_f32 v120,v32,v120
D 0,56,3072
v_mul_f32 v121,v33,v121
v_exp_f32 v118,v118
v_exp_f32 v119,v119
v_exp_f32 v120,v120
v_exp_f32 v121,v121
v_fma_f32 v118,v118,s89,s89
v_fma_f32 v119,v119,s89,s89
s_add_u32 s55,s55,s60
s_sub_u32 s57,s84,s57
s_add_u32 s54,s54,s46
v_fma_f32 v120,v120,s89,s89
v_fma_f32 v121,v121,s89,s89
v_rcp_f32 v118,v118
v_rcp_f32 v119,v119
v_rcp_f32 v120,v120
v_rcp_f32 v121,v121
v_mul_f32 v30,v30,v118
v_mul_f32 v31,v31,v119
v_mul_f32 v32,v32,v120
v_mul_f32 v33,v33,v121
v_cvt_pk_bf16_f32 v124,v30,v31
v_cvt_pk_bf16_f32 v125,v32,v33
buffer_store_dwordx4 v[122:125],v15,s[36:39],s67 offen offset:0 sc0 sc1
buffer_store_dwordx4 v[122:125],v16,s[36:39],s67 offen offset:0 sc0 sc1
s_mov_b32 s72,s69
s_mov_b32 s69,0x7fff0000
s_mov_b32 s67,0x7fff0000
s_mov_b32 s77,0x7fff0000
s_branch Lg1_gox_%=
Lg1_xn1_%=:
s_cmp_ge_u32 s54,256
s_cbranch_scc1 Lg1_fin1_%=
s_add_u32 m0,s59,4096
s_nop 0
buffer_load_dword v8,s[36:39],s58 offen offset:0 sc1 lds
buffer_store_dword v4,v3,s[36:39],s71 offen offset:0 sc0 sc1
s_add_u32 m0,s59,5120
s_nop 0
buffer_load_dword v8,s[36:39],s58 offen offset:0 sc1 lds
buffer_store_dword v4,v3,s[36:39],s71 offen offset:0 sc0 sc1
s_mov_b32 s83,0
v_fma_f32 v26,-v102,v106,v26
v_fma_f32 v27,-v102,v107,v27
v_fma_f32 v28,-v102,v108,v28
v_fma_f32 v29,-v102,v109,v29
v_mul_f32 v26,v103,v26
v_mul_f32 v27,v103,v27
v_mul_f32 v28,v103,v28
v_mul_f32 v29,v103,v29
v_fma_f32 v114,v26,v26,s88
v_fma_f32 v115,v27,v27,s88
v_fma_f32 v116,v28,v28,s88
v_fma_f32 v117,v29,v29,s88
v_mul_f32 v114,v26,v114
v_mul_f32 v115,v27,v115
v_mul_f32 v116,v28,v116
v_mul_f32 v117,v29,v117
v_exp_f32 v114,v114
v_exp_f32 v115,v115
v_exp_f32 v116,v116
v_exp_f32 v117,v117
v_fma_f32 v114,v114,s89,s89
v_fma_f32 v115,v115,s89,s89
v_fma_f32 v116,v116,s89,s89
v_fma_f32 v117,v117,s89,s89
v_rcp_f32 v114,v114
v_rcp_f32 v115,v115
v_rcp_f32 v116,v116
v_rcp_f32 v117,v117
v_mul_f32 v26,v26,v114
v_mul_f32 v27,v27,v115
v_mul_f32 v28,v28,v116
v_mul_f32 v29,v29,v117
v_cvt_pk_bf16_f32 v122,v26,v27
v_cvt_pk_bf16_f32 v123,v28,v29
v_fma_f32 v30,-v102,v110,v30
v_fma_f32 v31,-v102,v111,v31
v_fma_f32 v32,-v102,v112,v32
v_fma_f32 v33,-v102,v113,v33
v_mul_f32 v30,v103,v30
v_mul_f32 v31,v103,v31
v_mul_f32 v32,v103,v32
v_mul_f32 v33,v103,v33
v_fma_f32 v118,v30,v30,s88
v_fma_f32 v119,v31,v31,s88
v_fma_f32 v120,v32,v32,s88
v_fma_f32 v121,v33,v33,s88
v_mul_f32 v118,v30,v118
v_mul_f32 v119,v31,v119
v_mul_f32 v120,v32,v120
v_mul_f32 v121,v33,v121
v_exp_f32 v118,v118
v_exp_f32 v119,v119
v_exp_f32 v120,v120
v_exp_f32 v121,v121
v_fma_f32 v118,v118,s89,s89
v_fma_f32 v119,v119,s89,s89
v_fma_f32 v120,v120,s89,s89
v_fma_f32 v121,v121,s89,s89
v_rcp_f32 v118,v118
v_rcp_f32 v119,v119
v_rcp_f32 v120,v120
v_rcp_f32 v121,v121
v_mul_f32 v30,v30,v118
v_mul_f32 v31,v31,v119
v_mul_f32 v32,v32,v120
v_mul_f32 v33,v33,v121
v_cvt_pk_bf16_f32 v124,v30,v31
v_cvt_pk_bf16_f32 v125,v32,v33
buffer_store_dwordx4 v[122:125],v15,s[36:39],s67 offen offset:0 sc0 sc1
buffer_store_dwordx4 v[122:125],v16,s[36:39],s67 offen offset:0 sc0 sc1
s_mov_b32 s72,s69
s_mov_b32 s69,0x7fff0000
s_mov_b32 s67,0x7fff0000
s_mov_b32 s77,0x7fff0000
s_branch Lg1_poll_%=
Lg1_fin1_%=:
v_fma_f32 v26,-v102,v106,v26
v_fma_f32 v27,-v102,v107,v27
v_fma_f32 v28,-v102,v108,v28
v_fma_f32 v29,-v102,v109,v29
v_mul_f32 v26,v103,v26
v_mul_f32 v27,v103,v27
v_mul_f32 v28,v103,v28
v_mul_f32 v29,v103,v29
v_fma_f32 v114,v26,v26,s88
v_fma_f32 v115,v27,v27,s88
v_fma_f32 v116,v28,v28,s88
v_fma_f32 v117,v29,v29,s88
v_mul_f32 v114,v26,v114
v_mul_f32 v115,v27,v115
v_mul_f32 v116,v28,v116
v_mul_f32 v117,v29,v117
v_exp_f32 v114,v114
v_exp_f32 v115,v115
v_exp_f32 v116,v116
v_exp_f32 v117,v117
v_fma_f32 v114,v114,s89,s89
v_fma_f32 v115,v115,s89,s89
v_fma_f32 v116,v116,s89,s89
v_fma_f32 v117,v117,s89,s89
v_rcp_f32 v114,v114
v_rcp_f32 v115,v115
v_rcp_f32 v116,v116
v_rcp_f32 v117,v117
v_mul_f32 v26,v26,v114
v_mul_f32 v27,v27,v115
v_mul_f32 v28,v28,v116
v_mul_f32 v29,v29,v117
v_cvt_pk_bf16_f32 v122,v26,v27
v_cvt_pk_bf16_f32 v123,v28,v29
v_fma_f32 v30,-v102,v110,v30
v_fma_f32 v31,-v102,v111,v31
v_fma_f32 v32,-v102,v112,v32
v_fma_f32 v33,-v102,v113,v33
v_mul_f32 v30,v103,v30
v_mul_f32 v31,v103,v31
v_mul_f32 v32,v103,v32
v_mul_f32 v33,v103,v33
v_fma_f32 v118,v30,v30,s88
v_fma_f32 v119,v31,v31,s88
v_fma_f32 v120,v32,v32,s88
v_fma_f32 v121,v33,v33,s88
v_mul_f32 v118,v30,v118
v_mul_f32 v119,v31,v119
v_mul_f32 v120,v32,v120
v_mul_f32 v121,v33,v121
v_exp_f32 v118,v118
v_exp_f32 v119,v119
v_exp_f32 v120,v120
v_exp_f32 v121,v121
v_fma_f32 v118,v118,s89,s89
v_fma_f32 v119,v119,s89,s89
v_fma_f32 v120,v120,s89,s89
v_fma_f32 v121,v121,s89,s89
v_rcp_f32 v118,v118
v_rcp_f32 v119,v119
v_rcp_f32 v120,v120
v_rcp_f32 v121,v121
v_mul_f32 v30,v30,v118
v_mul_f32 v31,v31,v119
v_mul_f32 v32,v32,v120
v_mul_f32 v33,v33,v121
v_cvt_pk_bf16_f32 v124,v30,v31
v_cvt_pk_bf16_f32 v125,v32,v33
buffer_store_dwordx4 v[122:125],v15,s[36:39],s67 offen offset:0 sc0 sc1
buffer_store_dwordx4 v[122:125],v16,s[36:39],s67 offen offset:0 sc0 sc1
s_mov_b32 s72,s69
s_mov_b32 s69,0x7fff0000
s_mov_b32 s67,0x7fff0000
s_mov_b32 s77,0x7fff0000
s_branch Lg1_done_%=
Lg1_done_%=:
s_waitcnt vmcnt(2)
s_barrier
buffer_store_dword v4,v3,s[36:39],s70 offen offset:0 sc0 sc1
s_waitcnt vmcnt(1)
s_barrier
buffer_store_dword v4,v3,s[36:39],s72 offen offset:0 sc0 sc1
s_waitcnt vmcnt(0) lgkmcnt(0)
s_barrier
s_branch Lg2_end_%=
Lg2_begin_%=:
Lcode_g2_b:
s_mov_b64 s[36:37],%1
s_mov_b32 s38,111243392
s_mov_b32 s39,0x00020000
s_mov_b32 s44,%2
s_mov_b32 s45,%3
s_mov_b32 s46,%4
s_mov_b32 s47,%5
s_mov_b32 s48,%6
s_mov_b32 s49,%7
s_mov_b32 s50,%8
s_mov_b32 s51,%9
s_mov_b32 s53,%10
v_mov_b32 v0,%11
v_mov_b32 v3,%12
v_mov_b32 v5,%13
v_mov_b32 v6,%14
v_mov_b32 v18,%15
v_mov_b32 v19,%16
v_mov_b32 v20,%17
v_mov_b32 v21,%18
v_mov_b32 v9,%19
v_mov_b32 v10,%20
v_mov_b32 v11,%21
v_mov_b32 v12,%22
v_mov_b32 v13,%23
v_mov_b32 v14,%24
v_mov_b32 v15,%25
v_mov_b32 v16,%26
s_mov_b64 s[40:41],%27
s_mov_b32 s42,2097152
s_mov_b32 s43,0x00020000
s_lshl_b32 s57,s44,11
s_cmp_ge_u32 s45,8
s_cbranch_scc1 Lg2_dly_%=
s_sleep 16
s_sleep 16
s_sleep 16
s_sleep 16
Lg2_dly_%=:
s_add_u32 s55,s50,0
s_add_u32 m0,s57,0
s_nop 0
buffer_load_dwordx4 v18,s[40:43],s55 offen offset:0  lds
s_add_u32 m0,s57,1024
s_nop 0
buffer_load_dwordx4 v19,s[40:43],s55 offen offset:0  lds
s_add_u32 m0,s57,8064
s_nop 0
buffer_load_dwordx4 v18,s[40:43],s55 offen offset:128  lds
s_add_u32 m0,s57,9088
s_nop 0
buffer_load_dwordx4 v19,s[40:43],s55 offen offset:128  lds
s_add_u32 s55,s50,131072
s_add_u32 m0,s57,16384
s_nop 0
buffer_load_dwordx4 v18,s[40:43],s55 offen offset:0  lds
s_add_u32 m0,s57,17408
s_nop 0
buffer_load_dwordx4 v19,s[40:43],s55 offen offset:0  lds
s_add_u32 m0,s57,24448
s_nop 0
buffer_load_dwordx4 v18,s[40:43],s55 offen offset:128  lds
s_add_u32 m0,s57,25472
s_nop 0
buffer_load_dwordx4 v19,s[40:43],s55 offen offset:128  lds
s_add_u32 s55,s50,262144
s_add_u32 m0,s57,32768
s_nop 0
buffer_load_dwordx4 v18,s[40:43],s55 offen offset:0  lds
s_add_u32 m0,s57,33792
s_nop 0
buffer_load_dwordx4 v19,s[40:43],s55 offen offset:0  lds
s_add_u32 m0,s57,40832
s_nop 0
buffer_load_dwordx4 v18,s[40:43],s55 offen offset:128  lds
s_add_u32 m0,s57,41856
s_nop 0
buffer_load_dwordx4 v19,s[40:43],s55 offen offset:128  lds
s_add_u32 s55,s50,393216
s_add_u32 m0,s57,49152
s_nop 0
buffer_load_dwordx4 v18,s[40:43],s55 offen offset:0  lds
s_add_u32 m0,s57,50176
s_nop 0
buffer_load_dwordx4 v19,s[40:43],s55 offen offset:0  lds
s_add_u32 m0,s57,57216
s_nop 0
buffer_load_dwordx4 v18,s[40:43],s55 offen offset:128  lds
s_add_u32 m0,s57,58240
s_nop 0
buffer_load_dwordx4 v19,s[40:43],s55 offen offset:128  lds
s_add_u32 s55,s50,524288
s_add_u32 m0,s57,65536
s_nop 0
buffer_load_dwordx4 v18,s[40:43],s55 offen offset:0  lds
s_add_u32 m0,s57,66560
s_nop 0
buffer_load_dwordx4 v19,s[40:43],s55 offen offset:0  lds
s_add_u32 m0,s57,73600
s_nop 0
buffer_load_dwordx4 v18,s[40:43],s55 offen offset:128  lds
s_add_u32 m0,s57,74624
s_nop 0
buffer_load_dwordx4 v19,s[40:43],s55 offen offset:128  lds
s_add_u32 s55,s50,655360
s_add_u32 m0,s57,81920
s_nop 0
buffer_load_dwordx4 v18,s[40:43],s55 offen offset:0  lds
s_add_u32 m0,s57,82944
s_nop 0
buffer_load_dwordx4 v19,s[40:43],s55 offen offset:0  lds
s_add_u32 m0,s57,89984
s_nop 0
buffer_load_dwordx4 v18,s[40:43],s55 offen offset:128  lds
s_add_u32 m0,s57,91008
s_nop 0
buffer_load_dwordx4 v19,s[40:43],s55 offen offset:128  lds
s_waitcnt vmcnt(20) lgkmcnt(0)
s_barrier
s_add_u32 s55,s50,786432
s_add_u32 m0,s57,98304
s_nop 0
buffer_load_dwordx4 v18,s[40:43],s55 offen offset:0  lds
s_add_u32 m0,s57,99328
s_nop 0
buffer_load_dwordx4 v19,s[40:43],s55 offen offset:0  lds
s_add_u32 m0,s57,106368
s_nop 0
buffer_load_dwordx4 v18,s[40:43],s55 offen offset:128  lds
s_add_u32 m0,s57,107392
s_nop 0
buffer_load_dwordx4 v19,s[40:43],s55 offen offset:128  lds
v_add_u32 v26,0,v20
v_add_u32 v27,0,v21
T a[0:1],26,0
T a[2:3],26,512
T a[4:5],27,0
T a[6:7],27,512
T a[8:9],26,4096
T a[10:11],26,4608
T a[12:13],27,4096
T a[14:15],27,4608
s_waitcnt vmcnt(20) lgkmcnt(0)
s_barrier
s_add_u32 s55,s50,917504
s_add_u32 m0,s57,0
s_nop 0
buffer_load_dwordx4 v18,s[40:43],s55 offen offset:0  lds
s_add_u32 m0,s57,1024
s_nop 0
buffer_load_dwordx4 v19,s[40:43],s55 offen offset:0  lds
s_add_u32 m0,s57,8064
s_nop 0
buffer_load_dwordx4 v18,s[40:43],s55 offen offset:128  lds
s_add_u32 m0,s57,9088
s_nop 0
buffer_load_dwordx4 v19,s[40:43],s55 offen offset:128  lds
v_add_u32 v26,16384,v20
v_add_u32 v27,16384,v21
T a[16:17],26,0
T a[18:19],26,512
T a[20:21],27,0
T a[22:23],27,512
T a[24:25],26,4096
T a[26:27],26,4608
T a[28:29],27,4096
T a[30:31],27,4608
s_waitcnt vmcnt(20) lgkmcnt(0)
s_barrier
s_add_u32 s55,s50,1048576
s_add_u32 m0,s57,16384
s_nop 0
buffer_load_dwordx4 v18,s[40:43],s55 offen offset:0  lds
s_add_u32 m0,s57,17408
s_nop 0
buffer_load_dwordx4 v19,s[40:43],s55 offen offset:0  lds
s_add_u32 m0,s57,24448
s_nop 0
buffer_load_dwordx4 v18,s[40:43],s55 offen offset:128  lds
s_add_u32 m0,s57,25472
s_nop 0
buffer_load_dwordx4 v19,s[40:43],s55 offen offset:128  lds
v_add_u32 v26,32768,v20
v_add_u32 v27,32768,v21
T a[32:33],26,0
T a[34:35],26,512
T a[36:37],27,0
T a[38:39],27,512
T a[40:41],26,4096
T a[42:43],26,4608
T a[44:45],27,4096
T a[46:47],27,4608
s_waitcnt vmcnt(20) lgkmcnt(0)
s_barrier
s_add_u32 s55,s50,1179648
s_add_u32 m0,s57,32768
s_nop 0
buffer_load_dwordx4 v18,s[40:43],s55 offen offset:0  lds
s_add_u32 m0,s57,33792
s_nop 0
buffer_load_dwordx4 v19,s[40:43],s55 offen offset:0  lds
s_add_u32 m0,s57,40832
s_nop 0
buffer_load_dwordx4 v18,s[40:43],s55 offen offset:128  lds
s_add_u32 m0,s57,41856
s_nop 0
buffer_load_dwordx4 v19,s[40:43],s55 offen offset:128  lds
v_add_u32 v26,49152,v20
v_add_u32 v27,49152,v21
T a[48:49],26,0
T a[50:51],26,512
T a[52:53],27,0
T a[54:55],27,512
T a[56:57],26,4096
T a[58:59],26,4608
T a[60:61],27,4096
T a[62:63],27,4608
s_waitcnt vmcnt(20) lgkmcnt(0)
s_barrier
s_add_u32 s55,s50,1310720
s_add_u32 m0,s57,49152
s_nop 0
buffer_load_dwordx4 v18,s[40:43],s55 offen offset:0  lds
s_add_u32 m0,s57,50176
s_nop 0
buffer_load_dwordx4 v19,s[40:43],s55 offen offset:0  lds
s_add_u32 m0,s57,57216
s_nop 0
buffer_load_dwordx4 v18,s[40:43],s55 offen offset:128  lds
s_add_u32 m0,s57,58240
s_nop 0
buffer_load_dwordx4 v19,s[40:43],s55 offen offset:128  lds
v_add_u32 v26,65536,v20
v_add_u32 v27,65536,v21
T a[64:65],26,0
T a[66:67],26,512
T a[68:69],27,0
T a[70:71],27,512
T a[72:73],26,4096
T a[74:75],26,4608
T a[76:77],27,4096
T a[78:79],27,4608
s_waitcnt vmcnt(20) lgkmcnt(0)
s_barrier
s_add_u32 s55,s50,1441792
s_add_u32 m0,s57,65536
s_nop 0
buffer_load_dwordx4 v18,s[40:43],s55 offen offset:0  lds
s_add_u32 m0,s57,66560
s_nop 0
buffer_load_dwordx4 v19,s[40:43],s55 offen offset:0  lds
s_add_u32 m0,s57,73600
s_nop 0
)"
R"(buffer_load_dwordx4 v18,s[40:43],s55 offen offset:128  lds
s_add_u32 m0,s57,74624
s_nop 0
buffer_load_dwordx4 v19,s[40:43],s55 offen offset:128  lds
v_add_u32 v26,81920,v20
v_add_u32 v27,81920,v21
T a[80:81],26,0
T a[82:83],26,512
T a[84:85],27,0
T a[86:87],27,512
T a[88:89],26,4096
T a[90:91],26,4608
T a[92:93],27,4096
T a[94:95],27,4608
s_waitcnt vmcnt(20) lgkmcnt(0)
s_barrier
s_add_u32 s55,s50,1572864
s_add_u32 m0,s57,81920
s_nop 0
buffer_load_dwordx4 v18,s[40:43],s55 offen offset:0  lds
s_add_u32 m0,s57,82944
s_nop 0
buffer_load_dwordx4 v19,s[40:43],s55 offen offset:0  lds
s_add_u32 m0,s57,89984
s_nop 0
buffer_load_dwordx4 v18,s[40:43],s55 offen offset:128  lds
s_add_u32 m0,s57,91008
s_nop 0
buffer_load_dwordx4 v19,s[40:43],s55 offen offset:128  lds
v_add_u32 v26,98304,v20
v_add_u32 v27,98304,v21
T a[96:97],26,0
T a[98:99],26,512
T a[100:101],27,0
T a[102:103],27,512
T a[104:105],26,4096
T a[106:107],26,4608
T a[108:109],27,4096
T a[110:111],27,4608
s_waitcnt vmcnt(20) lgkmcnt(0)
s_barrier
s_add_u32 s55,s50,1703936
s_add_u32 m0,s57,98304
s_nop 0
buffer_load_dwordx4 v18,s[40:43],s55 offen offset:0  lds
s_add_u32 m0,s57,99328
s_nop 0
buffer_load_dwordx4 v19,s[40:43],s55 offen offset:0  lds
s_add_u32 m0,s57,106368
s_nop 0
buffer_load_dwordx4 v18,s[40:43],s55 offen offset:128  lds
s_add_u32 m0,s57,107392
s_nop 0
buffer_load_dwordx4 v19,s[40:43],s55 offen offset:128  lds
v_add_u32 v26,0,v20
v_add_u32 v27,0,v21
T a[112:113],26,0
T a[114:115],26,512
T a[116:117],27,0
T a[118:119],27,512
T a[120:121],26,4096
T a[122:123],26,4608
T a[124:125],27,4096
T a[126:127],27,4608
s_waitcnt vmcnt(20) lgkmcnt(0)
s_barrier
s_add_u32 s55,s50,1835008
s_add_u32 m0,s57,0
s_nop 0
buffer_load_dwordx4 v18,s[40:43],s55 offen offset:0  lds
s_add_u32 m0,s57,1024
s_nop 0
buffer_load_dwordx4 v19,s[40:43],s55 offen offset:0  lds
s_add_u32 m0,s57,8064
s_nop 0
buffer_load_dwordx4 v18,s[40:43],s55 offen offset:128  lds
s_add_u32 m0,s57,9088
s_nop 0
buffer_load_dwordx4 v19,s[40:43],s55 offen offset:128  lds
v_add_u32 v26,16384,v20
v_add_u32 v27,16384,v21
T a[128:129],26,0
T a[130:131],26,512
T a[132:133],27,0
T a[134:135],27,512
T a[136:137],26,4096
T a[138:139],26,4608
T a[140:141],27,4096
T a[142:143],27,4608
s_waitcnt vmcnt(20) lgkmcnt(0)
s_barrier
s_add_u32 s55,s50,1966080
s_add_u32 m0,s57,16384
s_nop 0
buffer_load_dwordx4 v18,s[40:43],s55 offen offset:0  lds
s_add_u32 m0,s57,17408
s_nop 0
buffer_load_dwordx4 v19,s[40:43],s55 offen offset:0  lds
s_add_u32 m0,s57,24448
s_nop 0
buffer_load_dwordx4 v18,s[40:43],s55 offen offset:128  lds
s_add_u32 m0,s57,25472
s_nop 0
buffer_load_dwordx4 v19,s[40:43],s55 offen offset:128  lds
v_add_u32 v26,32768,v20
v_add_u32 v27,32768,v21
T a[144:145],26,0
T a[146:147],26,512
T a[148:149],27,0
T a[150:151],27,512
T a[152:153],26,4096
T a[154:155],26,4608
T a[156:157],27,4096
T a[158:159],27,4608
s_waitcnt vmcnt(20) lgkmcnt(0)
s_barrier
v_add_u32 v26,49152,v20
v_add_u32 v27,49152,v21
T a[160:161],26,0
T a[162:163],26,512
T a[164:165],27,0
T a[166:167],27,512
T a[168:169],26,4096
T a[170:171],26,4608
T a[172:173],27,4096
T a[174:175],27,4608
s_waitcnt vmcnt(16) lgkmcnt(0)
s_barrier
v_add_u32 v26,65536,v20
v_add_u32 v27,65536,v21
T a[176:177],26,0
T a[178:179],26,512
T a[180:181],27,0
T a[182:183],27,512
T a[184:185],26,4096
T a[186:187],26,4608
T a[188:189],27,4096
T a[190:191],27,4608
s_waitcnt vmcnt(12) lgkmcnt(0)
s_barrier
v_add_u32 v26,81920,v20
v_add_u32 v27,81920,v21
T a[192:193],26,0
T a[194:195],26,512
T a[196:197],27,0
T a[198:199],27,512
T a[200:201],26,4096
T a[202:203],26,4608
T a[204:205],27,4096
T a[206:207],27,4608
s_waitcnt vmcnt(8) lgkmcnt(0)
s_barrier
v_add_u32 v26,98304,v20
v_add_u32 v27,98304,v21
T a[208:209],26,0
T a[210:211],26,512
T a[212:213],27,0
T a[214:215],27,512
T a[216:217],26,4096
T a[218:219],26,4608
T a[220:221],27,4096
T a[222:223],27,4608
s_waitcnt vmcnt(4) lgkmcnt(0)
s_barrier
v_add_u32 v26,0,v20
v_add_u32 v27,0,v21
T a[224:225],26,0
T a[226:227],26,512
T a[228:229],27,0
T a[230:231],27,512
T a[232:233],26,4096
T a[234:235],26,4608
T a[236:237],27,4096
T a[238:239],27,4608
s_waitcnt vmcnt(0) lgkmcnt(0)
s_barrier
v_add_u32 v26,16384,v20
v_add_u32 v27,16384,v21
T a[240:241],26,0
T a[242:243],26,512
T a[244:245],27,0
T a[246:247],27,512
T a[248:249],26,4096
T a[250:251],26,4608
T a[252:253],27,4096
T a[254:255],27,4608
L 0
s_barrier
v_mov_b32 v4,s53
s_mov_b32 s82,0
s_mov_b32 s80,0
s_mov_b32 s54,s45
s_mul_i32 s57,s44,5120
s_mul_i32 s55,s45,40960
s_add_u32 s55,s55,s57
s_add_u32 s55,s55,17825792
s_mul_i32 s57,s44,4096
s_mul_i32 s60,s46,40960
s_mul_i32 s61,s46,1024
s_mul_i32 s62,s46,32768
s_mul_i32 s63,s46,1024
s_mul_i32 s64,s46,512
s_lshl_b32 s59,s44,8
s_add_u32 s59,s59,131072
s_mul_i32 s58,s45,1024
s_add_u32 s58,s58,109333504
s_mul_i32 s65,s45,32768
s_add_u32 s65,s65,s47
s_mul_i32 s68,s45,1024
s_add_u32 s68,s68,s48
s_mul_i32 s73,s45,512
s_add_u32 s73,s73,106954752
s_mul_i32 s74,s45,32768
s_add_u32 s74,s74,s49
s_mul_i32 s75,s45,1024
s_add_u32 s75,s75,107479040
s_mov_b32 s66,0x7fff0000
s_mov_b32 s67,0x7fff0000
s_mov_b32 s69,0x7fff0000
s_mov_b32 s76,0x7fff0000
s_mov_b32 s77,0x7fff0000
s_mov_b32 s70,0x7fff0000
s_mov_b32 s71,0x7fff0000
s_mov_b32 s72,0x7fff0000
v_mov_b32 v1,v0
v_add_u32 v2,16384,v0
s_mov_b32 s84,16384
s_mov_b32 s86,0x3a800000
s_mov_b32 s87,0x38a7c5ac
s_mov_b32 s88,0x409d30fc
s_mov_b32 s89,0xbeeff558
s_lshr_b32 s85,s44,1
s_mul_i32 s85,s85,127
v_lshlrev_b32 v8,22,v0
v_and_b32 v8,0x80000000,v8
v_or_b32 v8,v8,v5
v_add_u32 v2,32768,v0
s_lshl_b32 s84,s57,1
s_add_u32 s84,s84,32768
s_getpc_b64 s[40:41]
Lg2_pc_%=:
s_lshl_b32 s78,s44,13
v_lshlrev_b32 v106,3,v0
v_add_u32 v106,s78,v106
v_min_u32 v106,Lcode_g12_e-Lg2_pc_%=-4,v106
s_mov_b32 m0,163584
s_nop 0
global_load_lds_dword v106,s[40:41]
s_and_b32 s79,s54,127
s_cmp_eq_u32 s79,0
s_cselect_b32 s94,0x3300,0
s_cmp_eq_u32 s79,127
s_cselect_b32 s94,0xcc00,s94
s_add_u32 m0,s59,4096
s_nop 0
buffer_load_dword v8,s[36:39],s58 offen offset:0 sc1 lds
buffer_store_dword v4,v3,s[36:39],s71 offen offset:0 sc0 sc1
s_add_u32 m0,s59,5120
s_nop 0
buffer_load_dword v8,s[36:39],s58 offen offset:0 sc1 lds
buffer_store_dword v4,v3,s[36:39],s71 offen offset:0 sc0 sc1
s_mov_b32 s83,0
Lg2_poll_%=:
s_add_u32 s78,s83,2
s_and_b32 s78,s78,3
s_lshl_b32 s78,s78,10
s_add_u32 s78,s78,4096
s_add_u32 m0,s59,s78
s_nop 0
buffer_load_dword v8,s[36:39],s58 offen offset:0 sc1 lds
s_and_b32 s78,s83,3
s_lshl_b32 s78,s78,10
v_add_u32 v106,s78,v6
s_waitcnt vmcnt(4)
s_barrier
ds_read_b32 v7,v106 offset:4096
buffer_store_dword v4,v3,s[36:39],s70 offen offset:0 sc0 sc1
s_mov_b32 s70,s71
s_mov_b32 s71,s72
s_mov_b32 s72,0x7fff0000
s_add_u32 s83,s83,1
s_add_u32 s82,s82,1
L 0
v_cmp_eq_u32 vcc,s53,v7
s_nop 0
s_or_b64 vcc,vcc,s[94:95]
s_cmp_eq_u32 vcc_lo,-1
s_cbranch_scc1 Lg2_go_%=
s_cmp_lt_u32 s82,20000
s_cbranch_scc1 Lg2_poll_%=
Lg2_go_%=:
s_and_b32 s78,s54,127
s_cmp_eq_u32 s78,s85
s_cselect_b64 s[40:41],-1,0
v_cndmask_b32 v17,v15,v16,s[40:41]
s_add_u32 m0,s57,0
s_add_u32 s56,s55,0
D 17,56,0
s_add_u32 s56,s55,256
D 17,56,1024
s_add_u32 s56,s55,512
D 17,56,2048
s_add_u32 s56,s55,768
D 17,56,3072
s_add_u32 s58,s58,s61
s_add_u32 m0,s59,2048
s_nop 0
buffer_load_dword v8,s[36:39],s58 offen offset:0 sc1 lds
v_cndmask_b32 v17,v10,v9,s[40:41]
s_add_u32 m0,s57,16384
s_add_u32 s56,s55,20480
D 17,56,0
s_add_u32 s56,s55,20736
D 17,56,1024
s_add_u32 s56,s55,20992
D 17,56,2048
s_add_u32 s56,s55,21248
D 17,56,3072
s_add_u32 s55,s55,s60
s_sub_u32 s57,s84,s57
s_add_u32 s54,s54,s46
s_and_b32 s79,s54,127
s_cmp_eq_u32 s79,0
s_cselect_b32 s94,0x3300,0
s_cmp_eq_u32 s79,127
s_cselect_b32 s94,0xcc00,s94
s_waitcnt vmcnt(4)
s_barrier
buffer_store_dword v4,v3,s[36:39],s70 offen offset:0 sc0 sc1
s_min_u32 s71,s71,s72
buffer_store_dword v4,v3,s[36:39],s71 offen offset:0 sc0 sc1
s_mov_b32 s70,0x7fff0000
s_mov_b32 s71,0x7fff0000
s_mov_b32 s72,0x7fff0000
s_cmp_eq_u32 s80,1
buffer_store_dword v4,v3,s[36:39],s71 offen offset:0 sc0 sc1
s_cbranch_scc1 Lg2_st1_%=
s_branch Lg2_st0_%=
Lg2_gox_%=:
s_add_u32 m0,s59,2048
s_nop 0
buffer_load_dword v8,s[36:39],s58 offen offset:0 sc1 lds
s_and_b32 s79,s54,127
s_cmp_eq_u32 s79,0
s_cselect_b32 s94,0x3300,0
s_cmp_eq_u32 s79,127
s_cselect_b32 s94,0xcc00,s94
s_waitcnt vmcnt(0)
s_barrier
buffer_store_dword v4,v3,s[36:39],s70 offen offset:0 sc0 sc1
s_min_u32 s71,s71,s72
buffer_store_dword v4,v3,s[36:39],s71 offen offset:0 sc0 sc1
s_mov_b32 s70,0x7fff0000
s_mov_b32 s71,0x7fff0000
s_mov_b32 s72,0x7fff0000
s_cmp_eq_u32 s80,1
s_cbranch_scc1 Lg2_st1_%=
Lg2_st0_%=:
ds_read_b32 v7,v6 offset:2048
R 34,1,0
R 38,1,1024
R 42,1,2048
R 46,1,3072
R 50,1,4096
L 5
v_cmp_eq_u32 vcc,s53,v7
s_nop 0
s_or_b64 vcc,vcc,s[94:95]
L 3
s_cmp_eq_u32 vcc_lo,-1
s_cbranch_scc1 Lg2_F0_%=
s_branch Lg2_L0_%=
Lg2_st1_%=:
ds_read_b32 v7,v6 offset:2048
R 34,2,0
R 38,2,1024
R 42,2,2048
R 46,2,3072
R 50,2,4096
L 5
v_cmp_eq_u32 vcc,s53,v7
s_nop 0
s_or_b64 vcc,vcc,s[94:95]
L 3
s_cmp_eq_u32 vcc_lo,-1
s_cbranch_scc1 Lg2_F1_%=
s_branch Lg2_L1_%=
Lg2_F0_%=:
v_mfma_f32_16x16x32_bf16 v[22:25],a[4:7],v[34:37],0
s_add_u32 s58,s58,s61
s_add_u32 m0,s59,0
s_and_b32 s78,s54,127
R 54,1,5120
M 22,a[12:15],38
s_cmp_eq_u32 s78,s85
s_cselect_b64 s[40:41],-1,0
s_add_u32 s56,s55,0
buffer_load_dword v8,s[36:39],s58 offen offset:0 sc1 lds
v_mfma_f32_16x16x32_bf16 v[18:21],a[0:3],v[34:37],0
s_add_u32 m0,s57,0
R 58,1,6144
v_cndmask_b32 v17,v15,v16,s[40:41]
M 18,a[8:11],38
L 3
D 17,56,0
M 18,a[16:19],42
s_add_u32 s56,s55,256
R 62,1,7168
v_lshlrev_b32 v106,16,v126
M 18,a[24:27],46
s_add_u32 s54,s54,s46
v_and_b32 v107,0xffff0000,v126
v_lshlrev_b32 v108,16,v127
M 22,a[20:23],42
s_and_b32 s79,s54,127
R 66,1,8192
v_and_b32 v109,0xffff0000,v127
M 22,a[28:31],46
L 3
D 17,56,1024
M 22,a[36:39],50
s_add_u32 s56,s55,512
R 70,1,9216
v_lshlrev_b32 v110,16,v128
M 22,a[44:47],54
s_cmp_eq_u32 s79,0
v_and_b32 v111,0xffff0000,v128
v_lshlrev_b32 v112,16,v129
M 18,a[32:35],50
s_cselect_b32 s94,0x3300,0
R 74,1,10240
v_and_b32 v113,0xffff0000,v129
M 18,a[40:43],54
L 3
D 17,56,2048
M 18,a[48:51],58
s_add_u32 s56,s55,768
R 78,1,11264
v_fma_f32 v106,v106,v103,v102
M 18,a[56:59],62
D 17,56,3072
M 22,a[52:55],58
s_add_u32 m0,s57,16384
R 82,1,12288
v_fma_f32 v107,v107,v103,v102
M 22,a[60:63],62
L 3
v_fma_f32 v108,v108,v103,v102
v_fma_f32 v109,v109,v103,v102
M 22,a[68:71],66
s_add_u32 s56,s55,20480
R 86,1,13312
v_fma_f32 v110,v110,v103,v102
M 22,a[76:79],70
buffer_load_dwordx4 v[122:125],v0,s[36:39],s74 offen offset:0 sc1
M 18,a[64:67],66
s_cmp_eq_u32 s79,127
R 90,1,14336
v_fma_f32 v111,v111,v103,v102
M 18,a[72:75],70
L 3
buffer_load_dwordx2 v[98:99],v11,s[36:39],s73 offen offset:8 sc1
M 18,a[80:83],74
s_waitcnt vmcnt(10)
R 94,1,15360
v_fma_f32 v112,v112,v103,v102
M 18,a[88:91],78
s_barrier
v_fma_f32 v113,v113,v103,v102
v_add_f32 v26,v26,v106
M 22,a[84:87],74
s_cselect_b32 s94,0xcc00,s94
R 34,1,16384
v_add_f32 v27,v27,v107
M 22,a[92:95],78
L 3
v_add_f32 v28,v28,v108
v_add_f32 v29,v29,v109
M 22,a[100:103],82
s_mov_b32 s66,s65
R 38,1,17408
v_add_f32 v30,v30,v110
M 22,a[108:111],86
s_mov_b32 s76,s75
v_cndmask_b32 v17,v10,v9,s[40:41]
v_add_f32 v31,v31,v111
M 18,a[96:99],82
s_add_u32 s73,s73,s64
R 42,1,18432
v_add_f32 v32,v32,v112
M 18,a[104:107],86
L 3
D 17,56,0
M 18,a[112:115],90
s_add_u32 s56,s55,20736
R 46,1,19456
v_add_f32 v33,v33,v113
M 18,a[120:123],94
s_add_u32 s74,s74,s62
v_cvt_pk_bf16_f32 v114,v26,v27
)"
R"(v_cvt_pk_bf16_f32 v115,v28,v29
M 22,a[116:119],90
s_add_u32 s65,s65,s62
R 50,1,20480
v_fma_f32 v107,v26,v26,s87
M 22,a[124:127],94
L 3
v_add_f32 v106,v26,v27
v_fma_f32 v107,v27,v27,v107
M 22,a[132:135],34
s_add_u32 s75,s75,s63
R 54,1,21504
v_add_f32 v106,v106,v28
M 22,a[140:143],38
D 17,56,1024
M 18,a[128:131],34
s_add_u32 s56,s55,20992
R 58,1,22528
v_fma_f32 v107,v28,v28,v107
M 18,a[136:139],38
L 3
v_add_f32 v106,v106,v29
v_fma_f32 v107,v29,v29,v107
M 18,a[144:147],42
s_sub_u32 s57,s84,s57
R 62,1,23552
v_add_f32 v106,v106,v30
M 18,a[152:155],46
v_fma_f32 v107,v30,v30,v107
v_add_f32 v106,v106,v31
M 22,a[148:151],42
R 66,1,24576
v_fma_f32 v107,v31,v31,v107
M 22,a[156:159],46
L 3
v_add_f32 v106,v106,v32
v_fma_f32 v107,v32,v32,v107
M 22,a[164:167],50
R 70,1,25600
v_add_f32 v106,v106,v33
M 22,a[172:175],54
v_fma_f32 v107,v33,v33,v107
v_cvt_pk_bf16_f32 v116,v30,v31
M 18,a[160:163],50
R 74,1,26624
v_cvt_pk_bf16_f32 v117,v32,v33
M 18,a[168:171],54
L 3
v_permlane16_swap_b32 v106,v107
v_add_f32 v106,v106,v107
v_mov_b32 v107,v106
M 18,a[176:179],58
R 78,1,27648
M 18,a[184:187],62
v_permlane32_swap_b32 v106,v107
v_add_f32 v106,v106,v107
M 22,a[180:183],58
R 82,1,28672
ds_write_b32 v12,v106 offset:2048
M 22,a[188:191],62
L 4
D 17,56,2048
M 22,a[196:199],66
s_add_u32 s56,s55,21248
s_add_u32 s55,s55,s60
R 86,1,29696
M 22,a[204:207],70
D 17,56,3072
M 18,a[192:195],66
R 90,1,30720
M 18,a[200:203],70
L 4
L 2
buffer_store_dwordx4 v[114:117],v0,s[36:39],s67 offen offset:0 sc0 sc1
M 18,a[208:211],74
s_waitcnt vmcnt(7)
R 94,1,31744
M 18,a[216:219],78
s_barrier
ds_read_b32 v7,v6 offset:0
R 118,13,2048
M 22,a[212:215],74
R 34,2,0
M 22,a[220:223],78
L 5
buffer_store_dword v4,v3,s[36:39],s70 offen offset:0 sc0 sc1
M 22,a[228:231],82
s_mov_b32 s70,s69
s_mov_b32 s69,s68
R 38,2,1024
M 22,a[236:239],86
s_add_u32 s68,s68,s63
M 18,a[224:227],82
R 42,2,2048
M 18,a[232:235],86
L 5
L 4
v_cmp_eq_u32 vcc,s53,v7
M 18,a[240:243],90
L 3
R 46,2,3072
v_add_f32 v118,v118,v119
M 18,a[248:251],94
s_or_b64 vcc,vcc,s[94:95]
v_add_f32 v120,v120,v121
v_add_f32 v118,v118,v120
M 22,a[244:247],90
s_cmp_eq_u32 vcc_lo,-1
R 50,2,4096
v_mul_f32 v118,s86,v118
M 22,a[252:255],94
s_waitcnt vmcnt(6) lgkmcnt(3)
buffer_store_dword v118,v14,s[36:39],s77 offen offset:0 sc0 sc1
s_cbranch_scc0 Lg2_K1_%=
Lg2_F1_%=:
v_mfma_f32_16x16x32_bf16 v[30:33],a[4:7],v[34:37],0
s_add_u32 s58,s58,s61
s_add_u32 m0,s59,1024
s_and_b32 s78,s54,127
R 54,2,5120
M 30,a[12:15],38
s_cmp_eq_u32 s78,s85
s_cselect_b64 s[40:41],-1,0
s_add_u32 s56,s55,0
buffer_load_dword v8,s[36:39],s58 offen offset:0 sc1 lds
v_mfma_f32_16x16x32_bf16 v[26:29],a[0:3],v[34:37],0
s_add_u32 m0,s57,0
R 58,2,6144
v_cndmask_b32 v17,v15,v16,s[40:41]
M 26,a[8:11],38
L 3
D 17,56,0
M 26,a[16:19],42
s_add_u32 s56,s55,256
R 62,2,7168
v_lshlrev_b32 v106,16,v122
M 26,a[24:27],46
s_add_u32 s54,s54,s46
v_and_b32 v107,0xffff0000,v122
v_lshlrev_b32 v108,16,v123
M 30,a[20:23],42
s_and_b32 s79,s54,127
R 66,2,8192
v_and_b32 v109,0xffff0000,v123
M 30,a[28:31],46
L 3
D 17,56,1024
M 30,a[36:39],50
s_add_u32 s56,s55,512
R 70,2,9216
v_lshlrev_b32 v110,16,v124
M 30,a[44:47],54
s_cmp_eq_u32 s79,0
v_and_b32 v111,0xffff0000,v124
v_lshlrev_b32 v112,16,v125
M 26,a[32:35],50
s_cselect_b32 s94,0x3300,0
R 74,2,10240
v_and_b32 v113,0xffff0000,v125
M 26,a[40:43],54
L 3
D 17,56,2048
M 26,a[48:51],58
s_add_u32 s56,s55,768
R 78,2,11264
v_fma_f32 v106,v106,v99,v98
M 26,a[56:59],62
D 17,56,3072
M 30,a[52:55],58
s_add_u32 m0,s57,16384
R 82,2,12288
v_fma_f32 v107,v107,v99,v98
M 30,a[60:63],62
L 3
v_fma_f32 v108,v108,v99,v98
v_fma_f32 v109,v109,v99,v98
M 30,a[68:71],66
s_add_u32 s56,s55,20480
R 86,2,13312
v_fma_f32 v110,v110,v99,v98
M 30,a[76:79],70
buffer_load_dwordx4 v[126:129],v0,s[36:39],s74 offen offset:0 sc1
M 26,a[64:67],66
s_cmp_eq_u32 s79,127
R 90,2,14336
v_fma_f32 v111,v111,v99,v98
M 26,a[72:75],70
L 3
buffer_load_dwordx2 v[102:103],v11,s[36:39],s73 offen offset:8 sc1
M 26,a[80:83],74
s_waitcnt vmcnt(10)
R 94,2,15360
v_fma_f32 v112,v112,v99,v98
M 26,a[88:91],78
s_barrier
v_fma_f32 v113,v113,v99,v98
v_add_f32 v18,v18,v106
M 30,a[84:87],74
s_cselect_b32 s94,0xcc00,s94
R 34,2,16384
v_add_f32 v19,v19,v107
M 30,a[92:95],78
L 3
v_add_f32 v20,v20,v108
v_add_f32 v21,v21,v109
M 30,a[100:103],82
s_mov_b32 s67,s65
R 38,2,17408
v_add_f32 v22,v22,v110
M 30,a[108:111],86
s_mov_b32 s77,s75
v_cndmask_b32 v17,v10,v9,s[40:41]
v_add_f32 v23,v23,v111
M 26,a[96:99],82
s_add_u32 s73,s73,s64
R 42,2,18432
v_add_f32 v24,v24,v112
M 26,a[104:107],86
L 3
D 17,56,0
M 26,a[112:115],90
s_add_u32 s56,s55,20736
R 46,2,19456
v_add_f32 v25,v25,v113
M 26,a[120:123],94
s_add_u32 s74,s74,s62
v_cvt_pk_bf16_f32 v114,v18,v19
v_cvt_pk_bf16_f32 v115,v20,v21
M 30,a[116:119],90
s_add_u32 s65,s65,s62
R 50,2,20480
v_fma_f32 v107,v18,v18,s87
M 30,a[124:127],94
L 3
v_add_f32 v106,v18,v19
v_fma_f32 v107,v19,v19,v107
M 30,a[132:135],34
s_add_u32 s75,s75,s63
R 54,2,21504
v_add_f32 v106,v106,v20
M 30,a[140:143],38
D 17,56,1024
M 26,a[128:131],34
s_add_u32 s56,s55,20992
R 58,2,22528
v_fma_f32 v107,v20,v20,v107
M 26,a[136:139],38
L 3
v_add_f32 v106,v106,v21
v_fma_f32 v107,v21,v21,v107
M 26,a[144:147],42
s_sub_u32 s57,s84,s57
R 62,2,23552
v_add_f32 v106,v106,v22
M 26,a[152:155],46
v_fma_f32 v107,v22,v22,v107
v_add_f32 v106,v106,v23
M 30,a[148:151],42
R 66,2,24576
v_fma_f32 v107,v23,v23,v107
M 30,a[156:159],46
L 3
v_add_f32 v106,v106,v24
v_fma_f32 v107,v24,v24,v107
M 30,a[164:167],50
R 70,2,25600
v_add_f32 v106,v106,v25
M 30,a[172:175],54
v_fma_f32 v107,v25,v25,v107
v_cvt_pk_bf16_f32 v116,v22,v23
M 26,a[160:163],50
R 74,2,26624
v_cvt_pk_bf16_f32 v117,v24,v25
M 26,a[168:171],54
L 3
v_permlane16_swap_b32 v106,v107
v_add_f32 v106,v106,v107
v_mov_b32 v107,v106
M 26,a[176:179],58
R 78,2,27648
M 26,a[184:187],62
v_permlane32_swap_b32 v106,v107
v_add_f32 v106,v106,v107
M 30,a[180:183],58
R 82,2,28672
ds_write_b32 v12,v106 offset:0
M 30,a[188:191],62
L 4
D 17,56,2048
M 30,a[196:199],66
s_add_u32 s56,s55,21248
s_add_u32 s55,s55,s60
R 86,2,29696
M 30,a[204:207],70
D 17,56,3072
M 26,a[192:195],66
R 90,2,30720
M 26,a[200:203],70
L 4
L 2
buffer_store_dwordx4 v[114:117],v0,s[36:39],s66 offen offset:0 sc0 sc1
M 26,a[208:211],74
s_waitcnt vmcnt(7)
R 94,2,31744
M 26,a[216:219],78
s_barrier
ds_read_b32 v7,v6 offset:1024
R 118,13,0
M 30,a[212:215],74
R 34,1,0
M 30,a[220:223],78
L 5
buffer_store_dword v4,v3,s[36:39],s70 offen offset:0 sc0 sc1
M 30,a[228:231],82
s_mov_b32 s70,s69
s_mov_b32 s69,s68
R 38,1,1024
M 30,a[236:239],86
s_add_u32 s68,s68,s63
M 26,a[224:227],82
R 42,1,2048
M 26,a[232:235],86
L 5
L 4
v_cmp_eq_u32 vcc,s53,v7
M 26,a[240:243],90
L 3
R 46,1,3072
v_add_f32 v118,v118,v119
M 26,a[248:251],94
s_or_b64 vcc,vcc,s[94:95]
v_add_f32 v120,v120,v121
v_add_f32 v118,v118,v120
M 30,a[244:247],90
s_cmp_eq_u32 vcc_lo,-1
R 50,1,4096
v_mul_f32 v118,s86,v118
M 30,a[252:255],94
s_waitcnt vmcnt(6) lgkmcnt(3)
buffer_store_dword v118,v14,s[36:39],s76 offen offset:0 sc0 sc1
s_cbranch_scc1 Lg2_F0_%=
Lg2_K0_%=:
Lg2_L0_%=:
v_mfma_f32_16x16x32_bf16 v[22:25],a[4:7],v[34:37],0
s_add_u32 m0,s59,3072
R 54,1,5120
v_lshlrev_b32 v106,16,v126
M 22,a[12:15],38
buffer_load_dword v8,s[36:39],s58 offen offset:0 sc1 lds
v_mfma_f32_16x16x32_bf16 v[18:21],a[0:3],v[34:37],0
s_mov_b32 s66,s65
R 58,1,6144
v_and_b32 v107,0xffff0000,v126
M 18,a[8:11],38
L 3
v_lshlrev_b32 v108,16,v127
v_and_b32 v109,0xffff0000,v127
M 18,a[16:19],42
s_mov_b32 s76,s75
R 62,1,7168
v_lshlrev_b32 v110,16,v128
M 18,a[24:27],46
s_add_u32 s65,s65,s62
v_and_b32 v111,0xffff0000,v128
v_lshlrev_b32 v112,16,v129
M 22,a[20:23],42
s_add_u32 s75,s75,s63
R 66,1,8192
v_and_b32 v113,0xffff0000,v129
v_fma_f32 v106,v106,v103,v102
M 22,a[28:31],46
L 3
v_fma_f32 v107,v107,v103,v102
v_fma_f32 v108,v108,v103,v102
M 22,a[36:39],50
R 70,1,9216
v_fma_f32 v109,v109,v103,v102
M 22,a[44:47],54
v_fma_f32 v110,v110,v103,v102
v_fma_f32 v111,v111,v103,v102
M 18,a[32:35],50
R 74,1,10240
v_fma_f32 v112,v112,v103,v102
M 18,a[40:43],54
L 3
v_fma_f32 v113,v113,v103,v102
v_add_f32 v26,v26,v106
M 18,a[48:51],58
R 78,1,11264
v_add_f32 v27,v27,v107
M 18,a[56:59],62
v_add_f32 v28,v28,v108
v_add_f32 v29,v29,v109
v_add_f32 v30,v30,v110
M 22,a[52:55],58
R 82,1,12288
v_add_f32 v31,v31,v111
M 22,a[60:63],62
L 3
v_add_f32 v32,v32,v112
v_add_f32 v33,v33,v113
M 22,a[68:71],66
R 86,1,13312
v_cvt_pk_bf16_f32 v114,v26,v27
M 22,a[76:79],70
buffer_load_dwordx4 v[122:125],v0,s[36:39],s74 offen offset:0 sc1
M 18,a[64:67],66
s_add_u32 s74,s74,s62
R 90,1,14336
v_cvt_pk_bf16_f32 v115,v28,v29
M 18,a[72:75],70
L 3
buffer_load_dwordx2 v[98:99],v11,s[36:39],s73 offen offset:8 sc1
v_cvt_pk_bf16_f32 v116,v30,v31
M 18,a[80:83],74
s_waitcnt vmcnt(2)
s_add_u32 s73,s73,s64
R 94,1,15360
M 18,a[88:91],78
s_barrier
ds_read_b32 v7,v6 offset:3072
v_cvt_pk_bf16_f32 v117,v32,v33
M 22,a[84:87],74
R 34,1,16384
v_add_f32 v106,v26,v27
M 22,a[92:95],78
L 4
buffer_store_dword v4,v3,s[36:39],s70 offen offset:0 sc0 sc1
M 22,a[100:103],82
s_mov_b32 s70,0x7fff0000
R 38,1,17408
v_fma_f32 v107,v26,v26,s87
M 22,a[108:111],86
buffer_store_dwordx4 v[114:117],v0,s[36:39],s67 offen offset:0 sc0 sc1
v_fma_f32 v107,v27,v27,v107
M 18,a[96:99],82
R 42,1,18432
v_add_f32 v106,v106,v28
v_fma_f32 v107,v28,v28,v107
M 18,a[104:107],86
L 4
L 3
v_cmp_eq_u32 vcc,s53,v7
M 18,a[112:115],90
R 46,1,19456
v_add_f32 v106,v106,v29
M 18,a[120:123],94
s_or_b64 vcc,vcc,s[94:95]
v_fma_f32 v107,v29,v29,v107
v_add_f32 v106,v106,v30
M 22,a[116:119],90
R 50,1,20480
v_fma_f32 v107,v30,v30,v107
M 22,a[124:127],94
s_cmp_eq_u32 vcc_lo,-1
L 3
v_add_f32 v106,v106,v31
s_cbranch_scc0 Lg2_X0_%=
M 22,a[132:135],34
s_and_b32 s78,s54,127
s_cmp_eq_u32 s78,s85
s_cselect_b64 s[40:41],-1,0
R 54,1,21504
M 22,a[140:143],38
s_add_u32 m0,s57,0
s_add_u32 s56,s55,0
s_add_u32 s58,s58,s61
v_cndmask_b32 v17,v15,v16,s[40:41]
M 18,a[128:131],34
s_add_u32 s54,s54,s46
R 58,1,22528
v_fma_f32 v107,v31,v31,v107
M 18,a[136:139],38
L 3
D 17,56,0
M 18,a[144:147],42
s_add_u32 s56,s55,256
R 62,1,23552
v_add_f32 v106,v106,v32
M 18,a[152:155],46
s_and_b32 s79,s54,127
v_fma_f32 v107,v32,v32,v107
v_add_f32 v106,v106,v33
M 22,a[148:151],42
s_cmp_eq_u32 s79,0
R 66,1,24576
v_fma_f32 v107,v33,v33,v107
M 22,a[156:159],46
L 3
D 17,56,1024
M 22,a[164:167],50
s_cselect_b32 s94,0x3300,0
s_add_u32 s56,s55,512
R 70,1,25600
M 22,a[172:175],54
s_cmp_eq_u32 s79,127
v_permlane16_swap_b32 v106,v107
v_add_f32 v106,v106,v107
M 18,a[160:163],50
s_cselect_b32 s94,0xcc00,s94
)"
R"(R 74,1,26624
v_mov_b32 v107,v106
M 18,a[168:171],54
L 3
D 17,56,2048
M 18,a[176:179],58
s_add_u32 s56,s55,768
R 78,1,27648
M 18,a[184:187],62
v_permlane32_swap_b32 v106,v107
v_add_f32 v106,v106,v107
M 22,a[180:183],58
R 82,1,28672
ds_write_b32 v12,v106 offset:2048
M 22,a[188:191],62
L 4
D 17,56,3072
M 22,a[196:199],66
s_add_u32 m0,s59,0
s_add_u32 s56,s55,20480
R 86,1,29696
M 22,a[204:207],70
buffer_load_dword v8,s[36:39],s58 offen offset:0 sc1 lds
M 18,a[192:195],66
s_add_u32 m0,s57,16384
R 90,1,30720
v_cndmask_b32 v17,v10,v9,s[40:41]
M 18,a[200:203],70
L 4
L 2
s_sub_u32 s57,s84,s57
M 18,a[208:211],74
s_waitcnt vmcnt(0)
R 94,1,31744
M 18,a[216:219],78
s_barrier
D 17,56,0
M 22,a[212:215],74
s_add_u32 s56,s55,20736
R 34,2,0
ds_read_b32 v7,v6 offset:0
M 22,a[220:223],78
L 4
buffer_store_dword v4,v3,s[36:39],s70 offen offset:0 sc0 sc1
M 22,a[228:231],82
s_mov_b32 s70,s69
R 38,2,1024
R 118,13,2048
M 22,a[236:239],86
D 17,56,1024
M 18,a[224:227],82
s_add_u32 s56,s55,20992
s_mov_b32 s69,s68
R 42,2,2048
M 18,a[232:235],86
L 5
D 17,56,2048
M 18,a[240:243],90
L 3
s_add_u32 s56,s55,21248
R 46,2,3072
v_cmp_eq_u32 vcc,s53,v7
M 18,a[248:251],94
D 17,56,3072
M 22,a[244:247],90
s_add_u32 s55,s55,s60
s_add_u32 s68,s68,s63
L 2
s_or_b64 vcc,vcc,s[94:95]
s_cmp_eq_u32 vcc_lo,-1
R 50,2,4096
v_add_f32 v118,v118,v119
v_add_f32 v120,v120,v121
v_add_f32 v118,v118,v120
v_mul_f32 v118,s86,v118
M 22,a[252:255],94
s_nop 0
buffer_store_dword v118,v14,s[36:39],s77 offen offset:0 sc0 sc1
buffer_store_dword v4,v3,s[36:39],s71 offen offset:0 sc0 sc1
buffer_store_dword v4,v3,s[36:39],s71 offen offset:0 sc0 sc1
s_cbranch_scc1 Lg2_F1_%=
s_branch Lg2_K1_%=
Lg2_X0_%=:
M 22,a[132:135],34
s_add_u32 m0,s59,0
R 54,1,21504
v_fma_f32 v107,v31,v31,v107
M 22,a[140:143],38
buffer_load_dword v8,s[36:39],s58 offen offset:0 sc1 lds
M 18,a[128:131],34
R 58,1,22528
v_add_f32 v106,v106,v32
M 18,a[136:139],38
L 3
v_fma_f32 v107,v32,v32,v107
v_add_f32 v106,v106,v33
M 18,a[144:147],42
R 62,1,23552
v_fma_f32 v107,v33,v33,v107
M 18,a[152:155],46
M 22,a[148:151],42
R 66,1,24576
M 22,a[156:159],46
L 3
v_permlane16_swap_b32 v106,v107
v_add_f32 v106,v106,v107
M 22,a[164:167],50
R 70,1,25600
v_mov_b32 v107,v106
M 22,a[172:175],54
M 18,a[160:163],50
R 74,1,26624
M 18,a[168:171],54
L 3
v_permlane32_swap_b32 v106,v107
M 18,a[176:179],58
R 78,1,27648
v_add_f32 v106,v106,v107
M 18,a[184:187],62
ds_write_b32 v12,v106 offset:2048
M 22,a[180:183],58
R 82,1,28672
M 22,a[188:191],62
L 4
M 22,a[196:199],66
R 86,1,29696
M 22,a[204:207],70
M 18,a[192:195],66
R 90,1,30720
M 18,a[200:203],70
L 4
L 3
M 18,a[208:211],74
s_waitcnt vmcnt(0)
R 94,1,31744
M 18,a[216:219],78
s_barrier
R 118,13,2048
ds_read_b32 v7,v6 offset:0
M 22,a[212:215],74
buffer_store_dword v4,v3,s[36:39],s70 offen offset:0 sc0 sc1
M 22,a[220:223],78
L 4
s_mov_b32 s70,s69
s_mov_b32 s69,s68
M 22,a[228:231],82
s_add_u32 s68,s68,s63
M 22,a[236:239],86
M 18,a[224:227],82
M 18,a[232:235],86
L 2
L 1
L 0
M 18,a[240:243],90
v_add_f32 v118,v118,v119
v_add_f32 v120,v120,v121
v_add_f32 v118,v118,v120
M 18,a[248:251],94
v_cmp_eq_u32 vcc,s53,v7
v_mul_f32 v118,s86,v118
M 22,a[244:247],90
buffer_store_dword v118,v14,s[36:39],s77 offen offset:0 sc0 sc1
M 22,a[252:255],94
s_nop 0
s_or_b64 vcc,vcc,s[94:95]
s_mov_b32 s80,1
s_cmp_eq_u32 vcc_lo,-1
s_cbranch_scc0 Lg2_xn0_%=
s_and_b32 s78,s54,127
s_cmp_eq_u32 s78,s85
s_cselect_b64 s[40:41],-1,0
v_cndmask_b32 v17,v15,v16,s[40:41]
s_add_u32 m0,s57,0
s_add_u32 s56,s55,0
D 17,56,0
v_lshlrev_b32 v106,16,v122
v_and_b32 v107,0xffff0000,v122
v_lshlrev_b32 v108,16,v123
v_and_b32 v109,0xffff0000,v123
v_lshlrev_b32 v110,16,v124
s_add_u32 s56,s55,256
D 17,56,1024
v_and_b32 v111,0xffff0000,v124
v_lshlrev_b32 v112,16,v125
v_and_b32 v113,0xffff0000,v125
v_fma_f32 v106,v106,v99,v98
v_fma_f32 v107,v107,v99,v98
s_add_u32 s56,s55,512
D 17,56,2048
v_fma_f32 v108,v108,v99,v98
v_fma_f32 v109,v109,v99,v98
v_fma_f32 v110,v110,v99,v98
v_fma_f32 v111,v111,v99,v98
v_fma_f32 v112,v112,v99,v98
s_add_u32 s56,s55,768
D 17,56,3072
v_fma_f32 v113,v113,v99,v98
v_add_f32 v18,v18,v106
v_add_f32 v19,v19,v107
v_add_f32 v20,v20,v108
v_add_f32 v21,v21,v109
s_add_u32 s58,s58,s61
v_cndmask_b32 v17,v10,v9,s[40:41]
s_add_u32 m0,s57,16384
s_add_u32 s56,s55,20480
D 17,56,0
v_add_f32 v22,v22,v110
v_add_f32 v23,v23,v111
v_add_f32 v24,v24,v112
v_add_f32 v25,v25,v113
v_cvt_pk_bf16_f32 v114,v18,v19
s_add_u32 s56,s55,20736
D 17,56,1024
v_cvt_pk_bf16_f32 v115,v20,v21
v_cvt_pk_bf16_f32 v116,v22,v23
v_cvt_pk_bf16_f32 v117,v24,v25
s_add_u32 s56,s55,20992
D 17,56,2048
s_add_u32 s56,s55,21248
D 17,56,3072
buffer_store_dwordx4 v[114:117],v0,s[36:39],s66 offen offset:0 sc0 sc1
v_add_f32 v106,v18,v19
v_fma_f32 v107,v18,v18,s87
s_add_u32 s55,s55,s60
s_sub_u32 s57,s84,s57
s_add_u32 s54,s54,s46
v_fma_f32 v107,v19,v19,v107
v_add_f32 v106,v106,v20
v_fma_f32 v107,v20,v20,v107
v_add_f32 v106,v106,v21
v_fma_f32 v107,v21,v21,v107
v_add_f32 v106,v106,v22
v_fma_f32 v107,v22,v22,v107
v_add_f32 v106,v106,v23
v_fma_f32 v107,v23,v23,v107
v_add_f32 v106,v106,v24
v_fma_f32 v107,v24,v24,v107
v_add_f32 v106,v106,v25
v_fma_f32 v107,v25,v25,v107
s_nop 1
v_permlane16_swap_b32 v106,v107
v_add_f32 v106,v106,v107
v_mov_b32 v107,v106
s_nop 1
v_permlane32_swap_b32 v106,v107
v_add_f32 v106,v106,v107
ds_write_b32 v12,v106 offset:0
L 0
s_barrier
R 118,13,0
L 0
v_add_f32 v118,v118,v119
v_add_f32 v120,v120,v121
v_add_f32 v118,v118,v120
v_mul_f32 v118,s86,v118
buffer_store_dword v118,v14,s[36:39],s76 offen offset:0 sc0 sc1
s_mov_b32 s72,s69
s_mov_b32 s69,0x7fff0000
s_mov_b32 s66,0x7fff0000
s_mov_b32 s76,0x7fff0000
s_branch Lg2_gox_%=
Lg2_xn0_%=:
s_cmp_ge_u32 s54,256
s_cbranch_scc1 Lg2_fin0_%=
s_add_u32 m0,s59,4096
s_nop 0
buffer_load_dword v8,s[36:39],s58 offen offset:0 sc1 lds
buffer_store_dword v4,v3,s[36:39],s71 offen offset:0 sc0 sc1
s_add_u32 m0,s59,5120
s_nop 0
buffer_load_dword v8,s[36:39],s58 offen offset:0 sc1 lds
buffer_store_dword v4,v3,s[36:39],s71 offen offset:0 sc0 sc1
s_mov_b32 s83,0
v_lshlrev_b32 v106,16,v122
v_and_b32 v107,0xffff0000,v122
v_lshlrev_b32 v108,16,v123
v_and_b32 v109,0xffff0000,v123
v_lshlrev_b32 v110,16,v124
v_and_b32 v111,0xffff0000,v124
v_lshlrev_b32 v112,16,v125
v_and_b32 v113,0xffff0000,v125
v_fma_f32 v106,v106,v99,v98
v_fma_f32 v107,v107,v99,v98
v_fma_f32 v108,v108,v99,v98
v_fma_f32 v109,v109,v99,v98
v_fma_f32 v110,v110,v99,v98
v_fma_f32 v111,v111,v99,v98
v_fma_f32 v112,v112,v99,v98
v_fma_f32 v113,v113,v99,v98
v_add_f32 v18,v18,v106
v_add_f32 v19,v19,v107
v_add_f32 v20,v20,v108
v_add_f32 v21,v21,v109
v_add_f32 v22,v22,v110
v_add_f32 v23,v23,v111
v_add_f32 v24,v24,v112
v_add_f32 v25,v25,v113
v_cvt_pk_bf16_f32 v114,v18,v19
v_cvt_pk_bf16_f32 v115,v20,v21
v_cvt_pk_bf16_f32 v116,v22,v23
v_cvt_pk_bf16_f32 v117,v24,v25
buffer_store_dwordx4 v[114:117],v0,s[36:39],s66 offen offset:0 sc0 sc1
v_add_f32 v106,v18,v19
v_fma_f32 v107,v18,v18,s87
v_fma_f32 v107,v19,v19,v107
v_add_f32 v106,v106,v20
v_fma_f32 v107,v20,v20,v107
v_add_f32 v106,v106,v21
v_fma_f32 v107,v21,v21,v107
v_add_f32 v106,v106,v22
v_fma_f32 v107,v22,v22,v107
v_add_f32 v106,v106,v23
v_fma_f32 v107,v23,v23,v107
v_add_f32 v106,v106,v24
v_fma_f32 v107,v24,v24,v107
v_add_f32 v106,v106,v25
v_fma_f32 v107,v25,v25,v107
s_nop 1
v_permlane16_swap_b32 v106,v107
v_add_f32 v106,v106,v107
v_mov_b32 v107,v106
s_nop 1
v_permlane32_swap_b32 v106,v107
v_add_f32 v106,v106,v107
ds_write_b32 v12,v106 offset:0
L 0
s_barrier
R 118,13,0
L 0
v_add_f32 v118,v118,v119
v_add_f32 v120,v120,v121
v_add_f32 v118,v118,v120
v_mul_f32 v118,s86,v118
buffer_store_dword v118,v14,s[36:39],s76 offen offset:0 sc0 sc1
s_mov_b32 s72,s69
s_mov_b32 s69,0x7fff0000
s_mov_b32 s66,0x7fff0000
s_mov_b32 s76,0x7fff0000
s_branch Lg2_poll_%=
Lg2_fin0_%=:
v_lshlrev_b32 v106,16,v122
v_and_b32 v107,0xffff0000,v122
v_lshlrev_b32 v108,16,v123
v_and_b32 v109,0xffff0000,v123
v_lshlrev_b32 v110,16,v124
v_and_b32 v111,0xffff0000,v124
v_lshlrev_b32 v112,16,v125
v_and_b32 v113,0xffff0000,v125
v_fma_f32 v106,v106,v99,v98
v_fma_f32 v107,v107,v99,v98
v_fma_f32 v108,v108,v99,v98
v_fma_f32 v109,v109,v99,v98
v_fma_f32 v110,v110,v99,v98
v_fma_f32 v111,v111,v99,v98
v_fma_f32 v112,v112,v99,v98
v_fma_f32 v113,v113,v99,v98
v_add_f32 v18,v18,v106
v_add_f32 v19,v19,v107
v_add_f32 v20,v20,v108
v_add_f32 v21,v21,v109
v_add_f32 v22,v22,v110
v_add_f32 v23,v23,v111
v_add_f32 v24,v24,v112
v_add_f32 v25,v25,v113
v_cvt_pk_bf16_f32 v114,v18,v19
v_cvt_pk_bf16_f32 v115,v20,v21
v_cvt_pk_bf16_f32 v116,v22,v23
v_cvt_pk_bf16_f32 v117,v24,v25
buffer_store_dwordx4 v[114:117],v0,s[36:39],s66 offen offset:0 sc0 sc1
v_add_f32 v106,v18,v19
v_fma_f32 v107,v18,v18,s87
v_fma_f32 v107,v19,v19,v107
v_add_f32 v106,v106,v20
v_fma_f32 v107,v20,v20,v107
v_add_f32 v106,v106,v21
v_fma_f32 v107,v21,v21,v107
v_add_f32 v106,v106,v22
v_fma_f32 v107,v22,v22,v107
v_add_f32 v106,v106,v23
v_fma_f32 v107,v23,v23,v107
v_add_f32 v106,v106,v24
v_fma_f32 v107,v24,v24,v107
v_add_f32 v106,v106,v25
v_fma_f32 v107,v25,v25,v107
s_nop 1
v_permlane16_swap_b32 v106,v107
v_add_f32 v106,v106,v107
v_mov_b32 v107,v106
s_nop 1
v_permlane32_swap_b32 v106,v107
v_add_f32 v106,v106,v107
ds_write_b32 v12,v106 offset:0
L 0
s_barrier
R 118,13,0
L 0
v_add_f32 v118,v118,v119
v_add_f32 v120,v120,v121
v_add_f32 v118,v118,v120
v_mul_f32 v118,s86,v118
buffer_store_dword v118,v14,s[36:39],s76 offen offset:0 sc0 sc1
s_mov_b32 s72,s69
s_mov_b32 s69,0x7fff0000
s_mov_b32 s66,0x7fff0000
s_mov_b32 s76,0x7fff0000
s_branch Lg2_done_%=
Lg2_K1_%=:
Lg2_L1_%=:
v_mfma_f32_16x16x32_bf16 v[30:33],a[4:7],v[34:37],0
s_add_u32 m0,s59,3072
R 54,2,5120
v_lshlrev_b32 v106,16,v122
M 30,a[12:15],38
buffer_load_dword v8,s[36:39],s58 offen offset:0 sc1 lds
v_mfma_f32_16x16x32_bf16 v[26:29],a[0:3],v[34:37],0
s_mov_b32 s67,s65
R 58,2,6144
v_and_b32 v107,0xffff0000,v122
M 26,a[8:11],38
L 3
v_lshlrev_b32 v108,16,v123
v_and_b32 v109,0xffff0000,v123
M 26,a[16:19],42
s_mov_b32 s77,s75
R 62,2,7168
v_lshlrev_b32 v110,16,v124
M 26,a[24:27],46
s_add_u32 s65,s65,s62
v_and_b32 v111,0xffff0000,v124
v_lshlrev_b32 v112,16,v125
M 30,a[20:23],42
s_add_u32 s75,s75,s63
R 66,2,8192
v_and_b32 v113,0xffff0000,v125
v_fma_f32 v106,v106,v99,v98
M 30,a[28:31],46
L 3
v_fma_f32 v107,v107,v99,v98
v_fma_f32 v108,v108,v99,v98
M 30,a[36:39],50
R 70,2,9216
v_fma_f32 v109,v109,v99,v98
M 30,a[44:47],54
v_fma_f32 v110,v110,v99,v98
v_fma_f32 v111,v111,v99,v98
M 26,a[32:35],50
R 74,2,10240
v_fma_f32 v112,v112,v99,v98
M 26,a[40:43],54
L 3
v_fma_f32 v113,v113,v99,v98
v_add_f32 v18,v18,v106
M 26,a[48:51],58
R 78,2,11264
v_add_f32 v19,v19,v107
M 26,a[56:59],62
v_add_f32 v20,v20,v108
v_add_f32 v21,v21,v109
v_add_f32 v22,v22,v110
M 30,a[52:55],58
R 82,2,12288
v_add_f32 v23,v23,v111
M 30,a[60:63],62
L 3
v_add_f32 v24,v24,v112
v_add_f32 v25,v25,v113
M 30,a[68:71],66
R 86,2,13312
v_cvt_pk_bf16_f32 v114,v18,v19
M 30,a[76:79],70
buffer_load_dwordx4 v[126:129],v0,s[36:39],s74 offen offset:0 sc1
M 26,a[64:67],66
s_add_u32 s74,s74,s62
R 90,2,14336
v_cvt_pk_bf16_f32 v115,v20,v21
M 26,a[72:75],70
L 3
buffer_load_dwordx2 v[102:103],v11,s[36:39],s73 offen offset:8 sc1
v_cvt_pk_bf16_f32 v116,v22,v23
M 26,a[80:83],74
s_waitcnt vmcnt(2)
s_add_u32 s73,s73,s64
R 94,2,15360
M 26,a[88:91],78
s_barrier
ds_read_b32 v7,v6 offset:3072
v_cvt_pk_bf16_f32 v117,v24,v25
M 30,a[84:87],74
R 34,2,16384
v_add_f32 v106,v18,v19
M 30,a[92:95],78
L 4
buffer_store_dword v4,v3,s[36:39],s70 offen offset:0 sc0 sc1
M 30,a[100:103],82
s_mov_b32 s70,0x7fff0000
R 38,2,17408
v_fma_f32 v107,v18,v18,s87
M 30,a[108:111],86
buffer_store_dwordx4 v[114:117],v0,s[36:39],s66 offen offset:0 sc0 sc1
v_fma_f32 v107,v19,v19,v107
)"
R"(M 26,a[96:99],82
R 42,2,18432
v_add_f32 v106,v106,v20
v_fma_f32 v107,v20,v20,v107
M 26,a[104:107],86
L 4
L 3
v_cmp_eq_u32 vcc,s53,v7
M 26,a[112:115],90
R 46,2,19456
v_add_f32 v106,v106,v21
M 26,a[120:123],94
s_or_b64 vcc,vcc,s[94:95]
v_fma_f32 v107,v21,v21,v107
v_add_f32 v106,v106,v22
M 30,a[116:119],90
R 50,2,20480
v_fma_f32 v107,v22,v22,v107
M 30,a[124:127],94
s_cmp_eq_u32 vcc_lo,-1
L 3
v_add_f32 v106,v106,v23
s_cbranch_scc0 Lg2_X1_%=
M 30,a[132:135],34
s_and_b32 s78,s54,127
s_cmp_eq_u32 s78,s85
s_cselect_b64 s[40:41],-1,0
R 54,2,21504
M 30,a[140:143],38
s_add_u32 m0,s57,0
s_add_u32 s56,s55,0
s_add_u32 s58,s58,s61
v_cndmask_b32 v17,v15,v16,s[40:41]
M 26,a[128:131],34
s_add_u32 s54,s54,s46
R 58,2,22528
v_fma_f32 v107,v23,v23,v107
M 26,a[136:139],38
L 3
D 17,56,0
M 26,a[144:147],42
s_add_u32 s56,s55,256
R 62,2,23552
v_add_f32 v106,v106,v24
M 26,a[152:155],46
s_and_b32 s79,s54,127
v_fma_f32 v107,v24,v24,v107
v_add_f32 v106,v106,v25
M 30,a[148:151],42
s_cmp_eq_u32 s79,0
R 66,2,24576
v_fma_f32 v107,v25,v25,v107
M 30,a[156:159],46
L 3
D 17,56,1024
M 30,a[164:167],50
s_cselect_b32 s94,0x3300,0
s_add_u32 s56,s55,512
R 70,2,25600
M 30,a[172:175],54
s_cmp_eq_u32 s79,127
v_permlane16_swap_b32 v106,v107
v_add_f32 v106,v106,v107
M 26,a[160:163],50
s_cselect_b32 s94,0xcc00,s94
R 74,2,26624
v_mov_b32 v107,v106
M 26,a[168:171],54
L 3
D 17,56,2048
M 26,a[176:179],58
s_add_u32 s56,s55,768
R 78,2,27648
M 26,a[184:187],62
v_permlane32_swap_b32 v106,v107
v_add_f32 v106,v106,v107
M 30,a[180:183],58
R 82,2,28672
ds_write_b32 v12,v106 offset:0
M 30,a[188:191],62
L 4
D 17,56,3072
M 30,a[196:199],66
s_add_u32 m0,s59,1024
s_add_u32 s56,s55,20480
R 86,2,29696
M 30,a[204:207],70
buffer_load_dword v8,s[36:39],s58 offen offset:0 sc1 lds
M 26,a[192:195],66
s_add_u32 m0,s57,16384
R 90,2,30720
v_cndmask_b32 v17,v10,v9,s[40:41]
M 26,a[200:203],70
L 4
L 2
s_sub_u32 s57,s84,s57
M 26,a[208:211],74
s_waitcnt vmcnt(0)
R 94,2,31744
M 26,a[216:219],78
s_barrier
D 17,56,0
M 30,a[212:215],74
s_add_u32 s56,s55,20736
R 34,1,0
ds_read_b32 v7,v6 offset:1024
M 30,a[220:223],78
L 4
buffer_store_dword v4,v3,s[36:39],s70 offen offset:0 sc0 sc1
M 30,a[228:231],82
s_mov_b32 s70,s69
R 38,1,1024
R 118,13,0
M 30,a[236:239],86
D 17,56,1024
M 26,a[224:227],82
s_add_u32 s56,s55,20992
s_mov_b32 s69,s68
R 42,1,2048
M 26,a[232:235],86
L 5
D 17,56,2048
M 26,a[240:243],90
L 3
s_add_u32 s56,s55,21248
R 46,1,3072
v_cmp_eq_u32 vcc,s53,v7
M 26,a[248:251],94
D 17,56,3072
M 30,a[244:247],90
s_add_u32 s55,s55,s60
s_add_u32 s68,s68,s63
L 2
s_or_b64 vcc,vcc,s[94:95]
s_cmp_eq_u32 vcc_lo,-1
R 50,1,4096
v_add_f32 v118,v118,v119
v_add_f32 v120,v120,v121
v_add_f32 v118,v118,v120
v_mul_f32 v118,s86,v118
M 30,a[252:255],94
s_nop 0
buffer_store_dword v118,v14,s[36:39],s76 offen offset:0 sc0 sc1
buffer_store_dword v4,v3,s[36:39],s71 offen offset:0 sc0 sc1
buffer_store_dword v4,v3,s[36:39],s71 offen offset:0 sc0 sc1
s_cbranch_scc1 Lg2_F0_%=
s_branch Lg2_K0_%=
Lg2_X1_%=:
M 30,a[132:135],34
s_add_u32 m0,s59,1024
R 54,2,21504
v_fma_f32 v107,v23,v23,v107
M 30,a[140:143],38
buffer_load_dword v8,s[36:39],s58 offen offset:0 sc1 lds
M 26,a[128:131],34
R 58,2,22528
v_add_f32 v106,v106,v24
M 26,a[136:139],38
L 3
v_fma_f32 v107,v24,v24,v107
v_add_f32 v106,v106,v25
M 26,a[144:147],42
R 62,2,23552
v_fma_f32 v107,v25,v25,v107
M 26,a[152:155],46
M 30,a[148:151],42
R 66,2,24576
M 30,a[156:159],46
L 3
v_permlane16_swap_b32 v106,v107
v_add_f32 v106,v106,v107
M 30,a[164:167],50
R 70,2,25600
v_mov_b32 v107,v106
M 30,a[172:175],54
M 26,a[160:163],50
R 74,2,26624
M 26,a[168:171],54
L 3
v_permlane32_swap_b32 v106,v107
M 26,a[176:179],58
R 78,2,27648
v_add_f32 v106,v106,v107
M 26,a[184:187],62
ds_write_b32 v12,v106 offset:0
M 30,a[180:183],58
R 82,2,28672
M 30,a[188:191],62
L 4
M 30,a[196:199],66
R 86,2,29696
M 30,a[204:207],70
M 26,a[192:195],66
R 90,2,30720
M 26,a[200:203],70
L 4
L 3
M 26,a[208:211],74
s_waitcnt vmcnt(0)
R 94,2,31744
M 26,a[216:219],78
s_barrier
R 118,13,0
ds_read_b32 v7,v6 offset:1024
M 30,a[212:215],74
buffer_store_dword v4,v3,s[36:39],s70 offen offset:0 sc0 sc1
M 30,a[220:223],78
L 4
s_mov_b32 s70,s69
s_mov_b32 s69,s68
M 30,a[228:231],82
s_add_u32 s68,s68,s63
M 30,a[236:239],86
M 26,a[224:227],82
M 26,a[232:235],86
L 2
L 1
L 0
M 26,a[240:243],90
v_add_f32 v118,v118,v119
v_add_f32 v120,v120,v121
v_add_f32 v118,v118,v120
M 26,a[248:251],94
v_cmp_eq_u32 vcc,s53,v7
v_mul_f32 v118,s86,v118
M 30,a[244:247],90
buffer_store_dword v118,v14,s[36:39],s76 offen offset:0 sc0 sc1
M 30,a[252:255],94
s_nop 0
s_or_b64 vcc,vcc,s[94:95]
s_mov_b32 s80,0
s_cmp_eq_u32 vcc_lo,-1
s_cbranch_scc0 Lg2_xn1_%=
s_and_b32 s78,s54,127
s_cmp_eq_u32 s78,s85
s_cselect_b64 s[40:41],-1,0
v_cndmask_b32 v17,v15,v16,s[40:41]
s_add_u32 m0,s57,0
s_add_u32 s56,s55,0
D 17,56,0
v_lshlrev_b32 v106,16,v126
v_and_b32 v107,0xffff0000,v126
v_lshlrev_b32 v108,16,v127
v_and_b32 v109,0xffff0000,v127
v_lshlrev_b32 v110,16,v128
s_add_u32 s56,s55,256
D 17,56,1024
v_and_b32 v111,0xffff0000,v128
v_lshlrev_b32 v112,16,v129
v_and_b32 v113,0xffff0000,v129
v_fma_f32 v106,v106,v103,v102
v_fma_f32 v107,v107,v103,v102
s_add_u32 s56,s55,512
D 17,56,2048
v_fma_f32 v108,v108,v103,v102
v_fma_f32 v109,v109,v103,v102
v_fma_f32 v110,v110,v103,v102
v_fma_f32 v111,v111,v103,v102
v_fma_f32 v112,v112,v103,v102
s_add_u32 s56,s55,768
D 17,56,3072
v_fma_f32 v113,v113,v103,v102
v_add_f32 v26,v26,v106
v_add_f32 v27,v27,v107
v_add_f32 v28,v28,v108
v_add_f32 v29,v29,v109
s_add_u32 s58,s58,s61
v_cndmask_b32 v17,v10,v9,s[40:41]
s_add_u32 m0,s57,16384
s_add_u32 s56,s55,20480
D 17,56,0
v_add_f32 v30,v30,v110
v_add_f32 v31,v31,v111
v_add_f32 v32,v32,v112
v_add_f32 v33,v33,v113
v_cvt_pk_bf16_f32 v114,v26,v27
s_add_u32 s56,s55,20736
D 17,56,1024
v_cvt_pk_bf16_f32 v115,v28,v29
v_cvt_pk_bf16_f32 v116,v30,v31
v_cvt_pk_bf16_f32 v117,v32,v33
s_add_u32 s56,s55,20992
D 17,56,2048
s_add_u32 s56,s55,21248
D 17,56,3072
buffer_store_dwordx4 v[114:117],v0,s[36:39],s67 offen offset:0 sc0 sc1
v_add_f32 v106,v26,v27
v_fma_f32 v107,v26,v26,s87
s_add_u32 s55,s55,s60
s_sub_u32 s57,s84,s57
s_add_u32 s54,s54,s46
v_fma_f32 v107,v27,v27,v107
v_add_f32 v106,v106,v28
v_fma_f32 v107,v28,v28,v107
v_add_f32 v106,v106,v29
v_fma_f32 v107,v29,v29,v107
v_add_f32 v106,v106,v30
v_fma_f32 v107,v30,v30,v107
v_add_f32 v106,v106,v31
v_fma_f32 v107,v31,v31,v107
v_add_f32 v106,v106,v32
v_fma_f32 v107,v32,v32,v107
v_add_f32 v106,v106,v33
v_fma_f32 v107,v33,v33,v107
s_nop 1
v_permlane16_swap_b32 v106,v107
v_add_f32 v106,v106,v107
v_mov_b32 v107,v106
s_nop 1
v_permlane32_swap_b32 v106,v107
v_add_f32 v106,v106,v107
ds_write_b32 v12,v106 offset:2048
L 0
s_barrier
R 118,13,2048
L 0
v_add_f32 v118,v118,v119
v_add_f32 v120,v120,v121
v_add_f32 v118,v118,v120
v_mul_f32 v118,s86,v118
buffer_store_dword v118,v14,s[36:39],s77 offen offset:0 sc0 sc1
s_mov_b32 s72,s69
s_mov_b32 s69,0x7fff0000
s_mov_b32 s67,0x7fff0000
s_mov_b32 s77,0x7fff0000
s_branch Lg2_gox_%=
Lg2_xn1_%=:
s_cmp_ge_u32 s54,256
s_cbranch_scc1 Lg2_fin1_%=
s_add_u32 m0,s59,4096
s_nop 0
buffer_load_dword v8,s[36:39],s58 offen offset:0 sc1 lds
buffer_store_dword v4,v3,s[36:39],s71 offen offset:0 sc0 sc1
s_add_u32 m0,s59,5120
s_nop 0
buffer_load_dword v8,s[36:39],s58 offen offset:0 sc1 lds
buffer_store_dword v4,v3,s[36:39],s71 offen offset:0 sc0 sc1
s_mov_b32 s83,0
v_lshlrev_b32 v106,16,v126
v_and_b32 v107,0xffff0000,v126
v_lshlrev_b32 v108,16,v127
v_and_b32 v109,0xffff0000,v127
v_lshlrev_b32 v110,16,v128
v_and_b32 v111,0xffff0000,v128
v_lshlrev_b32 v112,16,v129
v_and_b32 v113,0xffff0000,v129
v_fma_f32 v106,v106,v103,v102
v_fma_f32 v107,v107,v103,v102
v_fma_f32 v108,v108,v103,v102
v_fma_f32 v109,v109,v103,v102
v_fma_f32 v110,v110,v103,v102
v_fma_f32 v111,v111,v103,v102
v_fma_f32 v112,v112,v103,v102
v_fma_f32 v113,v113,v103,v102
v_add_f32 v26,v26,v106
v_add_f32 v27,v27,v107
v_add_f32 v28,v28,v108
v_add_f32 v29,v29,v109
v_add_f32 v30,v30,v110
v_add_f32 v31,v31,v111
v_add_f32 v32,v32,v112
v_add_f32 v33,v33,v113
v_cvt_pk_bf16_f32 v114,v26,v27
v_cvt_pk_bf16_f32 v115,v28,v29
v_cvt_pk_bf16_f32 v116,v30,v31
v_cvt_pk_bf16_f32 v117,v32,v33
buffer_store_dwordx4 v[114:117],v0,s[36:39],s67 offen offset:0 sc0 sc1
v_add_f32 v106,v26,v27
v_fma_f32 v107,v26,v26,s87
v_fma_f32 v107,v27,v27,v107
v_add_f32 v106,v106,v28
v_fma_f32 v107,v28,v28,v107
v_add_f32 v106,v106,v29
v_fma_f32 v107,v29,v29,v107
v_add_f32 v106,v106,v30
v_fma_f32 v107,v30,v30,v107
v_add_f32 v106,v106,v31
v_fma_f32 v107,v31,v31,v107
v_add_f32 v106,v106,v32
v_fma_f32 v107,v32,v32,v107
v_add_f32 v106,v106,v33
v_fma_f32 v107,v33,v33,v107
s_nop 1
v_permlane16_swap_b32 v106,v107
v_add_f32 v106,v106,v107
v_mov_b32 v107,v106
s_nop 1
v_permlane32_swap_b32 v106,v107
v_add_f32 v106,v106,v107
ds_write_b32 v12,v106 offset:2048
L 0
s_barrier
R 118,13,2048
L 0
v_add_f32 v118,v118,v119
v_add_f32 v120,v120,v121
v_add_f32 v118,v118,v120
v_mul_f32 v118,s86,v118
buffer_store_dword v118,v14,s[36:39],s77 offen offset:0 sc0 sc1
s_mov_b32 s72,s69
s_mov_b32 s69,0x7fff0000
s_mov_b32 s67,0x7fff0000
s_mov_b32 s77,0x7fff0000
s_branch Lg2_poll_%=
Lg2_fin1_%=:
v_lshlrev_b32 v106,16,v126
v_and_b32 v107,0xffff0000,v126
v_lshlrev_b32 v108,16,v127
v_and_b32 v109,0xffff0000,v127
v_lshlrev_b32 v110,16,v128
v_and_b32 v111,0xffff0000,v128
v_lshlrev_b32 v112,16,v129
v_and_b32 v113,0xffff0000,v129
v_fma_f32 v106,v106,v103,v102
v_fma_f32 v107,v107,v103,v102
v_fma_f32 v108,v108,v103,v102
v_fma_f32 v109,v109,v103,v102
v_fma_f32 v110,v110,v103,v102
v_fma_f32 v111,v111,v103,v102
v_fma_f32 v112,v112,v103,v102
v_fma_f32 v113,v113,v103,v102
v_add_f32 v26,v26,v106
v_add_f32 v27,v27,v107
v_add_f32 v28,v28,v108
v_add_f32 v29,v29,v109
v_add_f32 v30,v30,v110
v_add_f32 v31,v31,v111
v_add_f32 v32,v32,v112
v_add_f32 v33,v33,v113
v_cvt_pk_bf16_f32 v114,v26,v27
v_cvt_pk_bf16_f32 v115,v28,v29
v_cvt_pk_bf16_f32 v116,v30,v31
v_cvt_pk_bf16_f32 v117,v32,v33
buffer_store_dwordx4 v[114:117],v0,s[36:39],s67 offen offset:0 sc0 sc1
v_add_f32 v106,v26,v27
v_fma_f32 v107,v26,v26,s87
v_fma_f32 v107,v27,v27,v107
v_add_f32 v106,v106,v28
v_fma_f32 v107,v28,v28,v107
v_add_f32 v106,v106,v29
v_fma_f32 v107,v29,v29,v107
v_add_f32 v106,v106,v30
v_fma_f32 v107,v30,v30,v107
v_add_f32 v106,v106,v31
v_fma_f32 v107,v31,v31,v107
v_add_f32 v106,v106,v32
v_fma_f32 v107,v32,v32,v107
v_add_f32 v106,v106,v33
v_fma_f32 v107,v33,v33,v107
s_nop 1
v_permlane16_swap_b32 v106,v107
v_add_f32 v106,v106,v107
v_mov_b32 v107,v106
s_nop 1
v_permlane32_swap_b32 v106,v107
v_add_f32 v106,v106,v107
ds_write_b32 v12,v106 offset:2048
L 0
s_barrier
R 118,13,2048
L 0
v_add_f32 v118,v118,v119
v_add_f32 v120,v120,v121
v_add_f32 v118,v118,v120
v_mul_f32 v118,s86,v118
buffer_store_dword v118,v14,s[36:39],s77 offen offset:0 sc0 sc1
s_mov_b32 s72,s69
s_mov_b32 s69,0x7fff0000
s_mov_b32 s67,0x7fff0000
s_mov_b32 s77,0x7fff0000
s_branch Lg2_done_%=
Lg2_done_%=:
s_waitcnt vmcnt(2)
s_barrier
buffer_store_dword v4,v3,s[36:39],s70 offen offset:0 sc0 sc1
s_waitcnt vmcnt(1)
s_barrier
buffer_store_dword v4,v3,s[36:39],s72 offen offset:0 sc0 sc1
s_waitcnt vmcnt(0) lgkmcnt(0)
s_barrier
Lg2_end_%=:
s_mov_b32 %0,s53
Lcode_g12_e:
.purgem M
.purgem R
.purgem T
.purgem D
.purgem L
)"
  : "=&s"(epOut) : "s"(ws), "s"(__builtin_amdgcn_readfirstlane(sWave)), "s"(__builtin_amdgcn_readfirstlane(sG0)), "s"(__builtin_amdgcn_readfirstlane(sStep)), "s"(__builtin_amdgcn_readfirstlane(sOutOff)), "s"(__builtin_amdgcn_readfirstlane(sFoutOff)), "s"(__builtin_amdgcn_readfirstlane(sAuxOff)), "s"(__builtin_amdgcn_readfirstlane(sWw)), "s"(__builtin_amdgcn_readfirstlane(sBuf)), "s"(__builtin_amdgcn_readfirstlane(sExp)), "v"(vL16), "v"(vFlag), "v"(vSnapOff), "v"(vSnapRd), "v"(vWd0), "v"(vWd1), "v"(vWr0), "v"(vWr1), "v"(vSp), "v"(vOutS), "v"(vAux0), "v"(vAux1), "v"(vAux2), "v"(vAux3), "v"(vAn), "v"(vAe), "s"(w12) : "v0", "v1", "v2", "v3", "v4", "v5", "v6", "v7", "v8", "v9", "v10", "v11", "v12", "v13", "v14", "v15", "v16", "v17", "v18", "v19", "v20", "v21", "v22", "v23", "v24", "v25", "v26", "v27", "v28", "v29", "v30", "v31", "v32", "v33", "v34", "v35", "v36", "v37", "v38", "v39", "v40", "v41", "v42", "v43", "v44", "v45", "v46", "v47", "v48", "v49", "v50", "v51", "v52", "v53", "v54", "v55", "v56", "v57", "v58", "v59", "v60", "v61", "v62", "v63", "v64", "v65", "v66", "v67", "v68", "v69", "v70", "v71", "v72", "v73", "v74", "v75", "v76", "v77", "v78", "v79", "v80", "v81", "v82", "v83", "v84", "v85", "v86", "v87", "v88", "v89", "v90", "v91", "v92", "v93", "v94", "v95", "v96", "v97", "v98", "v99", "v100", "v101", "v102", "v103", "v104", "v105", "v106", "v107", "v108", "v109", "v110", "v111", "v112", "v113", "v114", "v115", "v116", "v117", "v118", "v119", "v120", "v121", "v122", "v123", "v124", "v125", "v126", "v127", "v128", "v129", "v130", "v131", "v132", "v133", "v134", "v135", "v136", "v137", "v138", "v139", "v140", "v141", "v142", "v143", "v144", "v145", "v146", "v147", "v148", "v149", "v150", "v151", "v152", "v153", "v154", "v155", "v156", "v157", "v158", "v159", "v160", "v161", "v162", "v163", "v164", "v165", "v166", "v167", "v168", "v169", "v170", "v171", "v172", "v173", "v174", "v175", "v176", "v177", "v178", "v179", "v180", "v181", "v182", "v183", "v184", "v185", "v186", "v187", "v188", "v189", "v190", "v191", "v192", "v193", "v194", "v195", "v196", "v197", "v198", "v199", "v200", "v201", "v202", "v203", "v204", "v205", "v206", "v207", "v208", "v209", "v210", "v211", "v212", "v213", "v214", "v215", "v216", "v217", "v218", "v219", "v220", "v221", "v222", "v223", "v224", "v225", "v226", "v227", "v228", "v229", "v230", "v231", "a0", "a1", "a2", "a3", "a4", "a5", "a6", "a7", "a8", "a9", "a10", "a11", "a12", "a13", "a14", "a15", "a16", "a17", "a18", "a19", "a20", "a21", "a22", "a23", "a24", "a25", "a26", "a27", "a28", "a29", "a30", "a31", "a32", "a33", "a34", "a35", "a36", "a37", "a38", "a39", "a40", "a41", "a42", "a43", "a44", "a45", "a46", "a47", "a48", "a49", "a50", "a51", "a52", "a53", "a54", "a55", "a56", "a57", "a58", "a59", "a60", "a61", "a62", "a63", "a64", "a65", "a66", "a67", "a68", "a69", "a70", "a71", "a72", "a73", "a74", "a75", "a76", "a77", "a78", "a79", "a80", "a81", "a82", "a83", "a84", "a85", "a86", "a87", "a88", "a89", "a90", "a91", "a92", "a93", "a94", "a95", "a96", "a97", "a98", "a99", "a100", "a101", "a102", "a103", "a104", "a105", "a106", "a107", "a108", "a109", "a110", "a111", "a112", "a113", "a114", "a115", "a116", "a117", "a118", "a119", "a120", "a121", "a122", "a123", "a124", "a125", "a126", "a127", "a128", "a129", "a130", "a131", "a132", "a133", "a134", "a135", "a136", "a137", "a138", "a139", "a140", "a141", "a142", "a143", "a144", "a145", "a146", "a147", "a148", "a149", "a150", "a151", "a152", "a153", "a154", "a155", "a156", "a157", "a158", "a159", "a160", "a161", "a162", "a163", "a164", "a165", "a166", "a167", "a168", "a169", "a170", "a171", "a172", "a173", "a174", "a175", "a176", "a177", "a178", "a179", "a180", "a181", "a182", "a183", "a184", "a185", "a186", "a187", "a188", "a189", "a190", "a191", "a192", "a193", "a194", "a195", "a196", "a197", "a198", "a199", "a200", "a201", "a202", "a203", "a204", "a205", "a206", "a207", "a208", "a209", "a210", "a211", "a212", "a213", "a214", "a215", "a216", "a217", "a218", "a219", "a220", "a221", "a222", "a223", "a224", "a225", "a226", "a227", "a228", "a229", "a230", "a231", "a232", "a233", "a234", "a235", "a236", "a237", "a238", "a239", "a240", "a241", "a242", "a243", "a244", "a245", "a246", "a247", "a248", "a249", "a250", "a251", "a252", "a253", "a254", "a255", "s36", "s37", "s38", "s39", "s40", "s41", "s42", "s43", "s44", "s45", "s46", "s47", "s48", "s49", "s50", "s51", "s52", "s53", "s54", "s55", "s56", "s57", "s58", "s59", "s60", "s61", "s62", "s63", "s64", "s65", "s66", "s67", "s68", "s69", "s70", "s71", "s72", "s73", "s74", "s75", "s76", "s77", "s78", "s79", "s80", "s81", "s82", "s83", "s84", "s85", "s86", "s87", "s88", "s89", "s90", "s91", "s92", "s93", "s94", "s95", "s96", "s97", "vcc", "scc", "memory");
    ep = epOut;
    if (kind == 1 && 5 + RHELP + (rep >> 1) < NR4) { kind = 4; kh = rep & 1; rep = 5 + RHELP + (rep >> 1); }
  }
  if (kind == 4) {
    const unsigned sStep = rep < 5 ? 5 : rep == 5 ? 6 : NR4;
    COMMON(10, (kh * 9 + fj) * FSG, 2560, 143360, 2048, 448, 0);
    const int cs = slice;
    const unsigned sOutOff = (kh ? OFF_P1 : OFF_P0) + (cs * 4 + wave) * 1024, sFoutOff = OFF_FP + (kh * 8 + cs) * FSP;
    const unsigned sAuxOff = (kh ? OFF_XP : OFF_X1) + (cs * 4 + wave) * 1024, sWw = (kh * 1792 + 16 * wave) * 2048 + cs * 256, sBuf = kh * 57344;
    const int zu = 108 + (rep + 2) % 5;
    const unsigned vSp = lg * 2048 + l15 * 4, vSn = rep >= 5 ? OOB : !ALLZ ? (zu * 16 + wave * 4 + lg) * 2048 + (cs * 2 + kh) * 128 + l15 * 8 :
                         ((rep + 5 * (wave >> 1)) * 16 + (wave & 1) * 8 + (ln >> 3)) * 2048 + (cs * 2 + kh) * 128 + (ln & 7) * 16;
    (void)vWr2;
    const unsigned sExp = __builtin_amdgcn_readfirstlane(ep);
    asm volatile(
R"(.macro M d,w,x
v_mfma_f32_16x16x32_bf16 v[\d:\d+3], \w, v[\x:\x+3], v[\d:\d+3]
.endm
.macro R d,a,o
ds_read_b128 v[\d:\d+3], v\a offset:\o
.endm
.macro T d,a,o
ds_read_b64_tr_b16 \d, v\a offset:\o
.endm
.macro D a,s,o
buffer_load_dwordx4 v\a, s[36:39], s\s offen offset:\o sc1 lds
.endm
.macro L c
s_waitcnt lgkmcnt(\c)
.endm
Lcode_g4_b:
s_mov_b64 s[36:37],%0
s_mov_b32 s38,111243392
s_mov_b32 s39,0x00020000
s_mov_b32 s44,%1
s_mov_b32 s45,%2
s_mov_b32 s46,%3
s_mov_b32 s47,%4
s_mov_b32 s48,%5
s_mov_b32 s49,%6
s_mov_b32 s50,%7
s_mov_b32 s51,%8
s_mov_b32 s93,%9
s_mov_b32 s53,%10
v_mov_b32 v0,%11
v_mov_b32 v3,%12
v_mov_b32 v5,%13
v_mov_b32 v6,%14
v_mov_b32 v10,%15
v_mov_b32 v11,%16
v_mov_b32 v12,%17
v_mov_b32 v13,%18
v_mov_b32 v9,%19
v_mov_b32 v7,%20
s_mov_b64 s[40:41],%21
s_mov_b32 s42,7340032
s_mov_b32 s43,0x00020000
s_lshl_b32 s57,s44,11
s_cmp_eq_u32 s45,175
s_cbranch_scc1 Lg4_pre_%=
s_add_u32 s55,s50,0
s_add_u32 m0,s57,40960
s_nop 0
buffer_load_dwordx4 v10,s[40:43],s55 offen offset:0  lds
s_add_u32 m0,s57,41984
s_nop 0
buffer_load_dwordx4 v11,s[40:43],s55 offen offset:0  lds
s_add_u32 m0,s57,49024
s_nop 0
buffer_load_dwordx4 v10,s[40:43],s55 offen offset:128  lds
s_add_u32 m0,s57,50048
s_nop 0
buffer_load_dwordx4 v11,s[40:43],s55 offen offset:128  lds
s_add_u32 s55,s50,131072
s_add_u32 m0,s57,57344
s_nop 0
buffer_load_dwordx4 v10,s[40:43],s55 offen offset:0  lds
s_add_u32 m0,s57,58368
s_nop 0
buffer_load_dwordx4 v11,s[40:43],s55 offen offset:0  lds
s_add_u32 m0,s57,65408
s_nop 0
buffer_load_dwordx4 v10,s[40:43],s55 offen offset:128  lds
s_add_u32 m0,s57,66432
s_nop 0
buffer_load_dwordx4 v11,s[40:43],s55 offen offset:128  lds
s_add_u32 s55,s50,262144
s_add_u32 m0,s57,73728
s_nop 0
buffer_load_dwordx4 v10,s[40:43],s55 offen offset:0  lds
s_add_u32 m0,s57,74752
s_nop 0
buffer_load_dwordx4 v11,s[40:43],s55 offen offset:0  lds
s_add_u32 m0,s57,81792
s_nop 0
buffer_load_dwordx4 v10,s[40:43],s55 offen offset:128  lds
s_add_u32 m0,s57,82816
s_nop 0
buffer_load_dwordx4 v11,s[40:43],s55 offen offset:128  lds
s_add_u32 s55,s50,393216
s_add_u32 m0,s57,90112
s_nop 0
buffer_load_dwordx4 v10,s[40:43],s55 offen offset:0  lds
s_add_u32 m0,s57,91136
s_nop 0
buffer_load_dwordx4 v11,s[40:43],s55 offen offset:0  lds
s_add_u32 m0,s57,98176
s_nop 0
buffer_load_dwordx4 v10,s[40:43],s55 offen offset:128  lds
s_add_u32 m0,s57,99200
s_nop 0
buffer_load_dwordx4 v11,s[40:43],s55 offen offset:128  lds
s_add_u32 s55,s50,524288
s_add_u32 m0,s57,106496
s_nop 0
buffer_load_dwordx4 v10,s[40:43],s55 offen offset:0  lds
s_add_u32 m0,s57,107520
s_nop 0
buffer_load_dwordx4 v11,s[40:43],s55 offen offset:0  lds
s_add_u32 m0,s57,114560
s_nop 0
buffer_load_dwordx4 v10,s[40:43],s55 offen offset:128  lds
s_add_u32 m0,s57,115584
s_nop 0
buffer_load_dwordx4 v11,s[40:43],s55 offen offset:128  lds
s_add_u32 s55,s50,655360
s_add_u32 m0,s57,122880
s_nop 0
buffer_load_dwordx4 v10,s[40:43],s55 offen offset:0  lds
s_add_u32 m0,s57,123904
s_nop 0
buffer_load_dwordx4 v11,s[40:43],s55 offen offset:0  lds
s_add_u32 m0,s57,130944
s_nop 0
buffer_load_dwordx4 v10,s[40:43],s55 offen offset:128  lds
s_add_u32 m0,s57,131968
s_nop 0
buffer_load_dwordx4 v11,s[40:43],s55 offen offset:128  lds
s_waitcnt vmcnt(20) lgkmcnt(0)
s_barrier
s_add_u32 s55,s50,786432
s_add_u32 m0,s57,139264
s_nop 0
buffer_load_dwordx4 v10,s[40:43],s55 offen offset:0  lds
s_add_u32 m0,s57,140288
s_nop 0
buffer_load_dwordx4 v11,s[40:43],s55 offen offset:0  lds
s_add_u32 m0,s57,147328
s_nop 0
buffer_load_dwordx4 v10,s[40:43],s55 offen offset:128  lds
s_add_u32 m0,s57,148352
s_nop 0
buffer_load_dwordx4 v11,s[40:43],s55 offen offset:128  lds
v_add_u32 v18,40960,v12
v_add_u32 v19,40960,v13
T a[0:1],18,0
T a[2:3],18,512
T a[4:5],19,0
T a[6:7],19,512
T a[8:9],18,4096
T a[10:11],18,4608
T a[12:13],19,4096
T a[14:15],19,4608
s_waitcnt vmcnt(20) lgkmcnt(0)
s_barrier
s_add_u32 s55,s50,917504
s_add_u32 m0,s57,40960
s_nop 0
buffer_load_dwordx4 v10,s[40:43],s55 offen offset:0  lds
s_add_u32 m0,s57,41984
s_nop 0
buffer_load_dwordx4 v11,s[40:43],s55 offen offset:0  lds
s_add_u32 m0,s57,49024
s_nop 0
buffer_load_dwordx4 v10,s[40:43],s55 offen offset:128  lds
s_add_u32 m0,s57,50048
s_nop 0
buffer_load_dwordx4 v11,s[40:43],s55 offen offset:128  lds
v_add_u32 v18,57344,v12
v_add_u32 v19,57344,v13
T a[16:17],18,0
T a[18:19],18,512
T a[20:21],19,0
T a[22:23],19,512
T a[24:25],18,4096
T a[26:27],18,4608
T a[28:29],19,4096
T a[30:31],19,4608
s_waitcnt vmcnt(20) lgkmcnt(0)
s_barrier
s_add_u32 s55,s50,1048576
s_add_u32 m0,s57,57344
s_nop 0
buffer_load_dwordx4 v10,s[40:43],s55 offen offset:0  lds
s_add_u32 m0,s57,58368
s_nop 0
buffer_load_dwordx4 v11,s[40:43],s55 offen offset:0  lds
s_add_u32 m0,s57,65408
s_nop 0
buffer_load_dwordx4 v10,s[40:43],s55 offen offset:128  lds
s_add_u32 m0,s57,66432
s_nop 0
buffer_load_dwordx4 v11,s[40:43],s55 offen offset:128  lds
v_add_u32 v18,73728,v12
v_add_u32 v19,73728,v13
T a[32:33],18,0
T a[34:35],18,512
T a[36:37],19,0
T a[38:39],19,512
T a[40:41],18,4096
T a[42:43],18,4608
T a[44:45],19,4096
T a[46:47],19,4608
s_waitcnt vmcnt(20) lgkmcnt(0)
s_barrier
s_add_u32 s55,s50,1179648
s_add_u32 m0,s57,73728
s_nop 0
buffer_load_dwordx4 v10,s[40:43],s55 offen offset:0  lds
s_add_u32 m0,s57,74752
s_nop 0
buffer_load_dwordx4 v11,s[40:43],s55 offen offset:0  lds
s_add_u32 m0,s57,81792
s_nop 0
buffer_load_dwordx4 v10,s[40:43],s55 offen offset:128  lds
s_add_u32 m0,s57,82816
s_nop 0
buffer_load_dwordx4 v11,s[40:43],s55 offen offset:128  lds
v_add_u32 v18,90112,v12
v_add_u32 v19,90112,v13
T a[48:49],18,0
T a[50:51],18,512
T a[52:53],19,0
T a[54:55],19,512
T a[56:57],18,4096
T a[58:59],18,4608
T a[60:61],19,4096
T a[62:63],19,4608
s_waitcnt vmcnt(20) lgkmcnt(0)
s_barrier
s_add_u32 s55,s50,1310720
s_add_u32 m0,s57,90112
s_nop 0
buffer_load_dwordx4 v10,s[40:43],s55 offen offset:0  lds
s_add_u32 m0,s57,91136
s_nop 0
buffer_load_dwordx4 v11,s[40:43],s55 offen offset:0  lds
s_add_u32 m0,s57,98176
s_nop 0
buffer_load_dwordx4 v10,s[40:43],s55 offen offset:128  lds
s_add_u32 m0,s57,99200
s_nop 0
buffer_load_dwordx4 v11,s[40:43],s55 offen offset:128  lds
v_add_u32 v18,106496,v12
v_add_u32 v19,106496,v13
T a[64:65],18,0
T a[66:67],18,512
T a[68:69],19,0
T a[70:71],19,512
T a[72:73],18,4096
T a[74:75],18,4608
T a[76:77],19,4096
T a[78:79],19,4608
s_waitcnt vmcnt(20) lgkmcnt(0)
s_barrier
s_add_u32 s55,s50,1441792
s_add_u32 m0,s57,106496
s_nop 0
buffer_load_dwordx4 v10,s[40:43],s55 offen offset:0  lds
s_add_u32 m0,s57,107520
s_nop 0
buffer_load_dwordx4 v11,s[40:43],s55 offen offset:0  lds
s_add_u32 m0,s57,114560
s_nop 0
buffer_load_dwordx4 v10,s[40:43],s55 offen offset:128  lds
s_add_u32 m0,s57,115584
s_nop 0
buffer_load_dwordx4 v11,s[40:43],s55 offen offset:128  lds
v_add_u32 v18,122880,v12
v_add_u32 v19,122880,v13
T a[80:81],18,0
T a[82:83],18,512
T a[84:85],19,0
T a[86:87],19,512
T a[88:89],18,4096
T a[90:91],18,4608
T a[92:93],19,4096
T a[94:95],19,4608
s_waitcnt vmcnt(20) lgkmcnt(0)
s_barrier
s_add_u32 s55,s50,1572864
s_add_u32 m0,s57,122880
s_nop 0
buffer_load_dwordx4 v10,s[40:43],s55 offen offset:0  lds
s_add_u32 m0,s57,123904
s_nop 0
buffer_load_dwordx4 v11,s[40:43],s55 offen offset:0  lds
s_add_u32 m0,s57,130944
s_nop 0
buffer_load_dwordx4 v10,s[40:43],s55 offen offset:128  lds
s_add_u32 m0,s57,131968
s_nop 0
buffer_load_dwordx4 v11,s[40:43],s55 offen offset:128  lds
v_add_u32 v18,139264,v12
v_add_u32 v19,139264,v13
T a[96:97],18,0
T a[98:99],18,512
T a[100:101],19,0
T a[102:103],19,512
T a[104:105],18,4096
T a[106:107],18,4608
T a[108:109],19,4096
T a[110:111],19,4608
s_waitcnt vmcnt(20) lgkmcnt(0)
s_barrier
s_add_u32 s55,s50,1703936
s_add_u32 m0,s57,139264
s_nop 0
buffer_load_dwordx4 v10,s[40:43],s55 offen offset:0  lds
s_add_u32 m0,s57,140288
s_nop 0
buffer_load_dwordx4 v11,s[40:43],s55 offen offset:0  lds
s_add_u32 m0,s57,147328
s_nop 0
buffer_load_dwordx4 v10,s[40:43],s55 offen offset:128  lds
s_add_u32 m0,s57,148352
s_nop 0
buffer_load_dwordx4 v11,s[40:43],s55 offen offset:128  lds
v_add_u32 v18,40960,v12
v_add_u32 v19,40960,v13
T a[112:113],18,0
T a[114:115],18,512
T a[116:117],19,0
T a[118:119],19,512
T a[120:121],18,4096
T a[122:123],18,4608
T a[124:125],19,4096
T a[126:127],19,4608
s_waitcnt vmcnt(20) lgkmcnt(0)
s_barrier
s_add_u32 s55,s50,1835008
s_add_u32 m0,s57,40960
s_nop 0
buffer_load_dwordx4 v10,s[40:43],s55 offen offset:0  lds
s_add_u32 m0,s57,41984
s_nop 0
buffer_load_dwordx4 v11,s[40:43],s55 offen offset:0  lds
s_add_u32 m0,s57,49024
s_nop 0
buffer_load_dwordx4 v10,s[40:43],s55 offen offset:128  lds
s_add_u32 m0,s57,50048
s_nop 0
buffer_load_dwordx4 v11,s[40:43],s55 offen offset:128  lds
v_add_u32 v18,57344,v12
v_add_u32 v19,57344,v13
T a[128:129],18,0
T a[130:131],18,512
T a[132:133],19,0
T a[134:135],19,512
T a[136:137],18,4096
T a[138:139],18,4608
T a[140:141],19,4096
T a[142:143],19,4608
s_waitcnt vmcnt(20) lgkmcnt(0)
s_barrier
s_add_u32 s55,s50,1966080
s_add_u32 m0,s57,57344
s_nop 0
buffer_load_dwordx4 v10,s[40:43],s55 offen offset:0  lds
s_add_u32 m0,s57,58368
s_nop 0
buffer_load_dwordx4 v11,s[40:43],s55 offen offset:0  lds
s_add_u32 m0,s57,65408
s_nop 0
buffer_load_dwordx4 v10,s[40:43],s55 offen offset:128  lds
s_add_u32 m0,s57,66432
s_nop 0
buffer_load_dwordx4 v11,s[40:43],s55 offen offset:128  lds
v_add_u32 v18,73728,v12
v_add_u32 v19,73728,v13
T a[144:145],18,0
T a[146:147],18,512
T a[148:149],19,0
T a[150:151],19,512
T a[152:153],18,4096
T a[154:155],18,4608
T a[156:157],19,4096
T a[158:159],19,4608
s_waitcnt vmcnt(20) lgkmcnt(0)
s_barrier
s_add_u32 s55,s50,2097152
s_add_u32 m0,s57,73728
s_nop 0
buffer_load_dwordx4 v10,s[40:43],s55 offen offset:0  lds
s_add_u32 m0,s57,74752
s_nop 0
buffer_load_dwordx4 v11,s[40:43],s55 offen offset:0  lds
s_add_u32 m0,s57,81792
s_nop 0
buffer_load_dwordx4 v10,s[40:43],s55 offen offset:128  lds
s_add_u32 m0,s57,82816
s_nop 0
buffer_load_dwordx4 v11,s[40:43],s55 offen offset:128  lds
v_add_u32 v18,90112,v12
v_add_u32 v19,90112,v13
T a[160:161],18,0
T a[162:163],18,512
T a[164:165],19,0
T a[166:167],19,512
T a[168:169],18,4096
T a[170:171],18,4608
T a[172:173],19,4096
T a[174:175],19,4608
s_waitcnt vmcnt(20) lgkmcnt(0)
s_barrier
s_add_u32 s55,s50,2228224
s_add_u32 m0,s57,90112
s_nop 0
buffer_load_dwordx4 v10,s[40:43],s55 offen offset:0  lds
s_add_u32 m0,s57,91136
s_nop 0
buffer_load_dwordx4 v11,s[40:43],s55 offen offset:0  lds
s_add_u32 m0,s57,98176
s_nop 0
buffer_load_dwordx4 v10,s[40:43],s55 offen offset:128  lds
s_add_u32 m0,s57,99200
s_nop 0
buffer_load_dwordx4 v11,s[40:43],s55 offen offset:128  lds
v_add_u32 v18,106496,v12
v_add_u32 v19,106496,v13
T a[176:177],18,0
T a[178:179],18,512
T a[180:181],19,0
T a[182:183],19,512
T a[184:185],18,4096
T a[186:187],18,4608
T a[188:189],19,4096
T a[190:191],19,4608
s_waitcnt vmcnt(20) lgkmcnt(0)
s_barrier
s_add_u32 s55,s50,2359296
s_add_u32 m0,s57,106496
s_nop 0
buffer_load_dwordx4 v10,s[40:43],s55 offen offset:0  lds
s_add_u32 m0,s57,107520
s_nop 0
buffer_load_dwordx4 v11,s[40:43],s55 offen offset:0  lds
s_add_u32 m0,s57,114560
s_nop 0
buffer_load_dwordx4 v10,s[40:43],s55 offen offset:128  lds
s_add_u32 m0,s57,115584
s_nop 0
buffer_load_dwordx4 v11,s[40:43],s55 offen offset:128  lds
v_add_u32 v18,122880,v12
v_add_u32 v19,122880,v13
T a[192:193],18,0
T a[194:195],18,512
T a[196:197],19,0
T a[198:199],19,512
T a[200:201],18,4096
T a[202:203],18,4608
T a[204:205],19,4096
T a[206:207],19,4608
s_waitcnt vmcnt(20) lgkmcnt(0)
s_barrier
s_add_u32 s55,s50,2490368
s_add_u32 m0,s57,122880
s_nop 0
buffer_load_dwordx4 v10,s[40:43],s55 offen offset:0  lds
s_add_u32 m0,s57,123904
s_nop 0
buffer_load_dwordx4 v11,s[40:43],s55 offen offset:0  lds
s_add_u32 m0,s57,130944
s_nop 0
buffer_load_dwordx4 v10,s[40:43],s55 offen offset:128  lds
s_add_u32 m0,s57,131968
s_nop 0
buffer_load_dwordx4 v11,s[40:43],s55 offen offset:128  lds
v_add_u32 v18,139264,v12
v_add_u32 v19,139264,v13
T a[208:209],18,0
T a[210:211],18,512
T a[212:213],19,0
T a[214:215],19,512
T a[216:217],18,4096
T a[218:219],18,4608
T a[220:221],19,4096
T a[222:223],19,4608
s_waitcnt vmcnt(20) lgkmcnt(0)
s_barrier
s_add_u32 s55,s50,2621440
s_add_u32 m0,s57,139264
s_nop 0
buffer_load_dwordx4 v10,s[40:43],s55 offen offset:0  lds
s_add_u32 m0,s57,140288
s_nop 0
buffer_load_dwordx4 v11,s[40:43],s55 offen offset:0  lds
s_add_u32 m0,s57,147328
s_nop 0
buffer_load_dwordx4 v10,s[40:43],s55 offen offset:128  lds
s_add_u32 m0,s57,148352
s_nop 0
buffer_load_dwordx4 v11,s[40:43],s55 offen offset:128  lds
v_add_u32 v18,40960,v12
v_add_u32 v19,40960,v13
T a[224:225],18,0
T a[226:227],18,512
T a[228:229],19,0
T a[230:231],19,512
T a[232:233],18,4096
T a[234:235],18,4608
T a[236:237],19,4096
T a[238:239],19,4608
s_waitcnt vmcnt(20) lgkmcnt(0)
s_barrier
)"
R"(s_add_u32 s55,s50,2752512
s_add_u32 m0,s57,40960
s_nop 0
buffer_load_dwordx4 v10,s[40:43],s55 offen offset:0  lds
s_add_u32 m0,s57,41984
s_nop 0
buffer_load_dwordx4 v11,s[40:43],s55 offen offset:0  lds
s_add_u32 m0,s57,49024
s_nop 0
buffer_load_dwordx4 v10,s[40:43],s55 offen offset:128  lds
s_add_u32 m0,s57,50048
s_nop 0
buffer_load_dwordx4 v11,s[40:43],s55 offen offset:128  lds
v_add_u32 v18,57344,v12
v_add_u32 v19,57344,v13
T a[240:241],18,0
T a[242:243],18,512
T a[244:245],19,0
T a[246:247],19,512
T a[248:249],18,4096
T a[250:251],18,4608
T a[252:253],19,4096
T a[254:255],19,4608
Lg4_pre_%=:
s_waitcnt vmcnt(20) lgkmcnt(0)
s_barrier
s_add_u32 s55,s50,2883584
s_add_u32 m0,s57,57344
s_nop 0
buffer_load_dwordx4 v10,s[40:43],s55 offen offset:0  lds
s_add_u32 m0,s57,58368
s_nop 0
buffer_load_dwordx4 v11,s[40:43],s55 offen offset:0  lds
s_add_u32 m0,s57,65408
s_nop 0
buffer_load_dwordx4 v10,s[40:43],s55 offen offset:128  lds
s_add_u32 m0,s57,66432
s_nop 0
buffer_load_dwordx4 v11,s[40:43],s55 offen offset:128  lds
v_add_u32 v18,73728,v12
v_add_u32 v19,73728,v13
T v[64:65],18,0
T v[66:67],18,512
T v[68:69],19,0
T v[70:71],19,512
T v[72:73],18,4096
T v[74:75],18,4608
T v[76:77],19,4096
T v[78:79],19,4608
s_waitcnt vmcnt(20) lgkmcnt(0)
s_barrier
s_add_u32 s55,s50,3014656
s_add_u32 m0,s57,73728
s_nop 0
buffer_load_dwordx4 v10,s[40:43],s55 offen offset:0  lds
s_add_u32 m0,s57,74752
s_nop 0
buffer_load_dwordx4 v11,s[40:43],s55 offen offset:0  lds
s_add_u32 m0,s57,81792
s_nop 0
buffer_load_dwordx4 v10,s[40:43],s55 offen offset:128  lds
s_add_u32 m0,s57,82816
s_nop 0
buffer_load_dwordx4 v11,s[40:43],s55 offen offset:128  lds
v_add_u32 v18,90112,v12
v_add_u32 v19,90112,v13
T v[80:81],18,0
T v[82:83],18,512
T v[84:85],19,0
T v[86:87],19,512
T v[88:89],18,4096
T v[90:91],18,4608
T v[92:93],19,4096
T v[94:95],19,4608
s_waitcnt vmcnt(20) lgkmcnt(0)
s_barrier
s_add_u32 s55,s50,3145728
s_add_u32 m0,s57,90112
s_nop 0
buffer_load_dwordx4 v10,s[40:43],s55 offen offset:0  lds
s_add_u32 m0,s57,91136
s_nop 0
buffer_load_dwordx4 v11,s[40:43],s55 offen offset:0  lds
s_add_u32 m0,s57,98176
s_nop 0
buffer_load_dwordx4 v10,s[40:43],s55 offen offset:128  lds
s_add_u32 m0,s57,99200
s_nop 0
buffer_load_dwordx4 v11,s[40:43],s55 offen offset:128  lds
v_add_u32 v18,106496,v12
v_add_u32 v19,106496,v13
T v[96:97],18,0
T v[98:99],18,512
T v[100:101],19,0
T v[102:103],19,512
T v[104:105],18,4096
T v[106:107],18,4608
T v[108:109],19,4096
T v[110:111],19,4608
s_waitcnt vmcnt(20) lgkmcnt(0)
s_barrier
s_add_u32 s55,s50,3276800
s_add_u32 m0,s57,106496
s_nop 0
buffer_load_dwordx4 v10,s[40:43],s55 offen offset:0  lds
s_add_u32 m0,s57,107520
s_nop 0
buffer_load_dwordx4 v11,s[40:43],s55 offen offset:0  lds
s_add_u32 m0,s57,114560
s_nop 0
buffer_load_dwordx4 v10,s[40:43],s55 offen offset:128  lds
s_add_u32 m0,s57,115584
s_nop 0
buffer_load_dwordx4 v11,s[40:43],s55 offen offset:128  lds
v_add_u32 v18,122880,v12
v_add_u32 v19,122880,v13
T v[112:113],18,0
T v[114:115],18,512
T v[116:117],19,0
T v[118:119],19,512
T v[120:121],18,4096
T v[122:123],18,4608
T v[124:125],19,4096
T v[126:127],19,4608
s_waitcnt vmcnt(20) lgkmcnt(0)
s_barrier
s_add_u32 s55,s50,3407872
s_add_u32 m0,s57,122880
s_nop 0
buffer_load_dwordx4 v10,s[40:43],s55 offen offset:0  lds
s_add_u32 m0,s57,123904
s_nop 0
buffer_load_dwordx4 v11,s[40:43],s55 offen offset:0  lds
s_add_u32 m0,s57,130944
s_nop 0
buffer_load_dwordx4 v10,s[40:43],s55 offen offset:128  lds
s_add_u32 m0,s57,131968
s_nop 0
buffer_load_dwordx4 v11,s[40:43],s55 offen offset:128  lds
v_add_u32 v18,139264,v12
v_add_u32 v19,139264,v13
T v[128:129],18,0
T v[130:131],18,512
T v[132:133],19,0
T v[134:135],19,512
T v[136:137],18,4096
T v[138:139],18,4608
T v[140:141],19,4096
T v[142:143],19,4608
s_waitcnt vmcnt(20) lgkmcnt(0)
s_barrier
s_add_u32 s55,s50,3538944
s_add_u32 m0,s57,139264
s_nop 0
buffer_load_dwordx4 v10,s[40:43],s55 offen offset:0  lds
s_add_u32 m0,s57,140288
s_nop 0
buffer_load_dwordx4 v11,s[40:43],s55 offen offset:0  lds
s_add_u32 m0,s57,147328
s_nop 0
buffer_load_dwordx4 v10,s[40:43],s55 offen offset:128  lds
s_add_u32 m0,s57,148352
s_nop 0
buffer_load_dwordx4 v11,s[40:43],s55 offen offset:128  lds
v_add_u32 v18,40960,v12
v_add_u32 v19,40960,v13
T v[144:145],18,0
T v[146:147],18,512
T v[148:149],19,0
T v[150:151],19,512
T v[152:153],18,4096
T v[154:155],18,4608
T v[156:157],19,4096
T v[158:159],19,4608
s_waitcnt vmcnt(20) lgkmcnt(0)
s_barrier
v_add_u32 v18,57344,v12
v_add_u32 v19,57344,v13
T v[160:161],18,0
T v[162:163],18,512
T v[164:165],19,0
T v[166:167],19,512
T v[168:169],18,4096
T v[170:171],18,4608
T v[172:173],19,4096
T v[174:175],19,4608
s_waitcnt vmcnt(16) lgkmcnt(0)
s_barrier
v_add_u32 v18,73728,v12
v_add_u32 v19,73728,v13
T v[176:177],18,0
T v[178:179],18,512
T v[180:181],19,0
T v[182:183],19,512
T v[184:185],18,4096
T v[186:187],18,4608
T v[188:189],19,4096
T v[190:191],19,4608
s_waitcnt vmcnt(12) lgkmcnt(0)
s_barrier
v_add_u32 v18,90112,v12
v_add_u32 v19,90112,v13
T v[192:193],18,0
T v[194:195],18,512
T v[196:197],19,0
T v[198:199],19,512
T v[200:201],18,4096
T v[202:203],18,4608
T v[204:205],19,4096
T v[206:207],19,4608
s_waitcnt vmcnt(8) lgkmcnt(0)
s_barrier
v_add_u32 v18,106496,v12
v_add_u32 v19,106496,v13
T v[208:209],18,0
T v[210:211],18,512
T v[212:213],19,0
T v[214:215],19,512
T v[216:217],18,4096
T v[218:219],18,4608
T v[220:221],19,4096
T v[222:223],19,4608
s_waitcnt vmcnt(4) lgkmcnt(0)
s_barrier
v_add_u32 v18,122880,v12
v_add_u32 v19,122880,v13
T v[224:225],18,0
T v[226:227],18,512
T v[228:229],19,0
T v[230:231],19,512
T v[232:233],18,4096
T v[234:235],18,4608
T v[236:237],19,4096
T v[238:239],19,4608
s_waitcnt vmcnt(0) lgkmcnt(0)
s_barrier
v_add_u32 v18,139264,v12
v_add_u32 v19,139264,v13
T v[240:241],18,0
T v[242:243],18,512
T v[244:245],19,0
T v[246:247],19,512
T v[248:249],18,4096
T v[250:251],18,4608
T v[252:253],19,4096
T v[254:255],19,4608
L 0
s_barrier
v_mov_b32 v4,s53
s_mov_b32 s82,0
s_mov_b32 s80,0
s_mov_b32 s54,s45
s_mul_i32 s57,s44,7168
s_mul_i32 s55,s45,114688
s_add_u32 s55,s55,s57
s_add_u32 s55,s55,40894464
s_mul_i32 s57,s44,7168
s_mul_i32 s60,s46,114688
s_mul_i32 s61,s46,2560
s_mul_i32 s62,s46,32768
s_mul_i32 s63,s46,2048
s_mul_i32 s64,s46,512
s_lshl_b32 s59,s44,8
s_add_u32 s59,s59,143360
s_mul_i32 s58,s45,2560
s_add_u32 s58,s58,109891584
s_mul_i32 s65,s45,32768
s_add_u32 s65,s65,s47
s_mul_i32 s68,s45,2048
s_add_u32 s68,s68,s48
s_mul_i32 s73,s45,512
s_add_u32 s73,s73,106954752
s_mul_i32 s74,s45,32768
s_add_u32 s74,s74,s49
s_mul_i32 s75,s45,1024
s_add_u32 s75,s75,107479040
s_mov_b32 s66,0x7fff0000
s_mov_b32 s67,0x7fff0000
s_mov_b32 s69,0x7fff0000
s_mov_b32 s76,0x7fff0000
s_mov_b32 s77,0x7fff0000
s_mov_b32 s70,0x7fff0000
s_mov_b32 s71,0x7fff0000
s_mov_b32 s72,0x7fff0000
v_mov_b32 v1,v0
v_add_u32 v2,28672,v0
s_mov_b32 s84,28672
s_mov_b32 s86,0x3a800000
s_mov_b32 s87,0x3727c5ac
s_mov_b32 s88,0x409d30fc
s_mov_b32 s89,0xbeeff558
s_lshr_b32 s85,s44,1
s_mul_i32 s85,s85,127
s_add_u32 s55,s55,s51
s_mul_i32 s78,s45,114688
s_sub_u32 s60,s55,s78
s_add_u32 s50,s93,224
s_add_u32 s93,s93,170
s_cmp_lt_u32 s45,224
s_cselect_b32 s63,224,-1
s_cmp_lt_u32 s45,170
s_cselect_b32 s63,170,s63
s_cselect_b32 s93,s93,s50
s_cselect_b32 s88,6,8
s_cselect_b32 s89,224,-1
v_lshlrev_b32 v8,22,v0
v_and_b32 v8,0x80000000,v8
v_or_b32 v8,v8,v5
v_add_u32 v2,57344,v0
s_lshl_b32 s84,s57,1
s_add_u32 s84,s84,57344
s_mov_b64 s[86:87],%22
s_mov_b64 s[40:41],%22
s_mov_b32 s42,4194304
s_and_b32 s78,s47,65535
s_lshr_b32 s78,s78,4
s_sub_u32 s85,s47,s78
s_add_u32 s85,s85,4194304
s_add_u32 s45,s47,5832704
s_mul_i32 s73,s44,1280
s_add_u32 s73,s73,114688
v_lshrrev_b32 v62,4,v9
v_and_b32 v62,3,v62
v_mul_u32_u24 v62,288,v62
v_lshrrev_b32 v63,7,v9
v_and_b32 v5,12,v9
v_add3_u32 v5,v5,v62,v63
v_add_u32 v5,s73,v5
v_mov_b32 v10,0
v_mov_b32 v11,0
v_mov_b32 v12,0
v_mov_b32 v13,0
s_mov_b32 s79,1638400
buffer_store_dwordx4 v[10:13],v7,s[40:43],s79 offen offset:0 sc0 sc1
s_mov_b32 s79,1966080
buffer_store_dwordx4 v[10:13],v7,s[40:43],s79 offen offset:0 sc0 sc1
s_mov_b32 s79,2293760
buffer_store_dwordx4 v[10:13],v7,s[40:43],s79 offen offset:0 sc0 sc1
s_mov_b32 s79,2621440
buffer_store_dwordx4 v[10:13],v7,s[40:43],s79 offen offset:0 sc0 sc1
s_mov_b32 s79,2949120
buffer_store_dwordx4 v[10:13],v7,s[40:43],s79 offen offset:0 sc0 sc1
s_mov_b32 s79,3276800
buffer_store_dwordx4 v[10:13],v7,s[40:43],s79 offen offset:0 sc0 sc1
s_mov_b32 s79,3604480
buffer_store_dwordx4 v[10:13],v7,s[40:43],s79 offen offset:0 sc0 sc1
s_mov_b32 s79,3932160
buffer_store_dwordx4 v[10:13],v7,s[40:43],s79 offen offset:0 sc0 sc1
s_getpc_b64 s[40:41]
Lg4_pc_%=:
s_lshl_b32 s78,s44,13
v_lshlrev_b32 v62,3,v0
v_add_u32 v62,s78,v62
v_min_u32 v62,Lcode_g4_e-Lg4_pc_%=-4,v62
s_mov_b32 m0,163584
s_nop 0
global_load_lds_dword v62,s[40:41]
s_add_u32 m0,s59,4096
s_nop 0
buffer_load_dword v8,s[36:39],s58 offen offset:0 sc1 lds
buffer_store_dword v4,v3,s[36:39],s71 offen offset:0 sc0 sc1
s_add_u32 m0,s59,5120
s_nop 0
buffer_load_dword v8,s[36:39],s58 offen offset:0 sc1 lds
buffer_store_dword v4,v3,s[36:39],s71 offen offset:0 sc0 sc1
s_mov_b32 s83,0
Lg4_poll_%=:
s_add_u32 s78,s83,2
s_and_b32 s78,s78,3
s_lshl_b32 s78,s78,10
s_add_u32 s78,s78,4096
s_add_u32 m0,s59,s78
s_nop 0
buffer_load_dword v8,s[36:39],s58 offen offset:0 sc1 lds
s_and_b32 s78,s83,3
s_lshl_b32 s78,s78,10
v_add_u32 v62,s78,v6
s_waitcnt vmcnt(4)
s_barrier
ds_read_b32 v7,v62 offset:4096
buffer_store_dword v4,v3,s[36:39],s70 offen offset:0 sc0 sc1
s_mov_b32 s70,s71
s_mov_b32 s71,s72
s_mov_b32 s72,0x7fff0000
s_add_u32 s83,s83,1
s_add_u32 s82,s82,1
L 0
v_cmp_eq_u32 vcc,s53,v7
s_nop 0
s_cmp_eq_u32 vcc_lo,-1
s_cbranch_scc1 Lg4_go_%=
s_cmp_lt_u32 s82,20000
s_cbranch_scc1 Lg4_poll_%=
Lg4_go_%=:
s_add_u32 m0,s57,0
s_add_u32 s56,s55,0
D 0,56,0
D 0,56,1024
D 0,56,2048
D 0,56,3072
s_add_u32 m0,s57,4096
s_add_u32 s56,s56,4096
D 0,56,0
D 0,56,1024
D 0,56,2048
s_add_u32 s61,s54,s46
s_cmp_ge_u32 s61,s63
s_cselect_b32 s61,s93,s61
s_cselect_b32 s46,s88,s46
s_cselect_b32 s63,s89,s63
s_cselect_b32 s93,s50,s93
s_cselect_b32 s88,8,s88
s_cselect_b32 s89,-1,s89
s_mul_i32 s58,s61,2560
s_add_u32 s58,s58,109891584
s_add_u32 m0,s59,2048
s_nop 0
buffer_load_dword v8,s[36:39],s58 offen offset:0 sc1 lds
s_add_u32 m0,s57,28672
s_add_u32 s56,s55,28672
D 0,56,0
D 0,56,1024
D 0,56,2048
D 0,56,3072
s_add_u32 m0,s57,32768
s_add_u32 s56,s56,4096
D 0,56,0
D 0,56,1024
D 0,56,2048
s_mul_i32 s55,s61,114688
s_add_u32 s55,s55,s60
s_sub_u32 s57,s84,s57
s_mov_b32 s54,s61
s_waitcnt vmcnt(7)
s_barrier
buffer_store_dword v4,v3,s[36:39],s70 offen offset:0 sc0 sc1
s_min_u32 s71,s71,s72
buffer_store_dword v4,v3,s[36:39],s71 offen offset:0 sc0 sc1
s_mov_b32 s70,0x7fff0000
s_mov_b32 s71,0x7fff0000
s_mov_b32 s72,0x7fff0000
s_cmp_eq_u32 s80,1
s_cbranch_scc1 Lg4_st1_%=
s_branch Lg4_st0_%=
Lg4_gox_%=:
s_add_u32 m0,s59,2048
s_nop 0
buffer_load_dword v8,s[36:39],s58 offen offset:0 sc1 lds
s_waitcnt vmcnt(0)
s_barrier
buffer_store_dword v4,v3,s[36:39],s70 offen offset:0 sc0 sc1
s_min_u32 s71,s71,s72
buffer_store_dword v4,v3,s[36:39],s71 offen offset:0 sc0 sc1
s_mov_b32 s70,0x7fff0000
s_mov_b32 s71,0x7fff0000
s_mov_b32 s72,0x7fff0000
s_cmp_eq_u32 s80,1
s_cbranch_scc1 Lg4_st1_%=
Lg4_st0_%=:
ds_read_b32 v7,v6 offset:2048
R 26,1,0
R 30,1,1024
R 34,1,2048
R 38,1,3072
R 42,1,4096
L 5
v_cmp_eq_u32 vcc,s53,v7
L 3
s_cmp_eq_u32 vcc_lo,-1
s_cbranch_scc1 Lg4_F0_%=
s_branch Lg4_L0_%=
Lg4_st1_%=:
ds_read_b32 v7,v6 offset:2048
R 26,2,0
R 30,2,1024
R 34,2,2048
R 38,2,3072
R 42,2,4096
L 5
v_cmp_eq_u32 vcc,s53,v7
L 3
s_cmp_eq_u32 vcc_lo,-1
s_cbranch_scc1 Lg4_F1_%=
s_branch Lg4_L1_%=
Lg4_F0_%=:
v_mfma_f32_16x16x32_bf16 v[14:17],a[4:7],v[26:29],0
s_add_u32 m0,s57,0
R 46,1,5120
v_lshlrev_b32 v62,16,v58
M 14,a[12:15],30
s_add_u32 s56,s55,0
v_add_f32 v18,v18,v62
v_and_b32 v63,0xffff0000,v58
v_mfma_f32_16x16x32_bf16 v[10:13],a[0:3],v[26:29],0
s_add_u32 s61,s54,s46
R 50,1,6144
v_add_f32 v19,v19,v63
M 10,a[8:11],30
L 3
D 0,56,0
)"
R"(M 10,a[16:19],34
s_cmp_ge_u32 s61,s63
R 54,1,7168
v_lshlrev_b32 v62,16,v59
M 10,a[24:27],38
s_cselect_b32 s61,s93,s61
v_add_f32 v20,v20,v62
v_and_b32 v63,0xffff0000,v59
M 14,a[20:23],34
s_cselect_b32 s46,s88,s46
R 26,1,8192
v_add_f32 v21,v21,v63
M 14,a[28:31],38
L 3
v_lshlrev_b32 v62,16,v60
v_add_f32 v22,v22,v62
M 14,a[36:39],42
s_cselect_b32 s63,s89,s63
R 30,1,9216
v_and_b32 v63,0xffff0000,v60
M 14,a[44:47],46
D 0,56,1024
M 10,a[32:35],42
s_cselect_b32 s93,s50,s93
R 34,1,10240
v_add_f32 v23,v23,v63
M 10,a[40:43],46
L 3
v_lshlrev_b32 v62,16,v61
v_add_f32 v24,v24,v62
M 10,a[48:51],50
s_cselect_b32 s88,8,s88
R 38,1,11264
v_and_b32 v63,0xffff0000,v61
M 10,a[56:59],54
buffer_load_dwordx4 v[58:61],v0,s[36:39],s74 offen offset:0 sc1
M 14,a[52:55],50
s_cselect_b32 s89,-1,s89
R 42,1,12288
v_add_f32 v25,v25,v63
M 14,a[60:63],54
L 3
v_cvt_pk_bf16_f32 v18,v18,v19
v_cvt_pk_bf16_f32 v19,v20,v21
M 14,a[68:71],26
s_mul_i32 s58,s61,2560
R 46,1,13312
v_cvt_pk_bf16_f32 v20,v22,v23
M 14,a[76:79],30
D 0,56,2048
M 10,a[64:67],26
s_add_u32 s58,s58,109891584
R 50,1,14336
v_cvt_pk_bf16_f32 v21,v24,v25
M 10,a[72:75],30
L 3
buffer_store_dwordx4 v[18:21],v0,s[36:39],s67 offen offset:0 sc0 sc1
M 10,a[80:83],34
s_lshl_b32 s62,s54,15
s_mov_b32 s66,s65
R 54,1,15360
M 10,a[88:91],38
s_cmp_ge_u32 s66,s45
s_cselect_b32 s51,1,-1
s_add_u32 s74,s49,s62
M 14,a[84:87],34
s_add_u32 s65,s47,s62
s_mov_b32 s54,s61
R 26,1,16384
M 14,a[92:95],38
L 3
M 14,a[100:103],42
R 30,1,17408
M 14,a[108:111],46
D 0,56,3072
M 10,a[96:99],42
s_add_u32 m0,s57,4096
s_add_u32 s56,s56,4096
R 34,1,18432
M 10,a[104:107],46
L 3
M 10,a[112:115],50
R 38,1,19456
M 10,a[120:123],54
M 14,a[116:119],50
R 42,1,20480
M 14,a[124:127],54
L 3
D 0,56,0
M 14,a[132:135],26
R 46,1,21504
M 14,a[140:143],30
M 10,a[128:131],26
R 50,1,22528
M 10,a[136:139],30
L 3
M 10,a[144:147],34
R 54,1,23552
M 10,a[152:155],38
M 14,a[148:151],34
R 26,1,24576
M 14,a[156:159],38
L 3
D 0,56,1024
M 14,a[164:167],42
R 30,1,25600
M 14,a[172:175],46
M 10,a[160:163],42
R 34,1,26624
M 10,a[168:171],46
L 3
M 10,a[176:179],50
s_waitcnt vmcnt(9)
R 38,1,27648
M 10,a[184:187],54
s_barrier
M 14,a[180:183],50
R 42,1,28672
M 14,a[188:191],54
L 3
D 0,56,2048
M 14,a[196:199],26
s_add_u32 m0,s59,0
s_add_u32 s56,s55,28672
R 46,1,29696
M 14,a[204:207],30
s_mul_i32 s55,s61,114688
s_add_u32 s55,s55,s60
M 10,a[192:195],26
R 50,1,30720
M 10,a[200:203],30
L 3
M 10,a[208:211],34
R 54,1,31744
M 10,a[216:219],38
M 14,a[212:215],34
R 26,1,32768
M 14,a[220:223],38
L 3
buffer_load_dword v8,s[36:39],s58 offen offset:0 sc1 lds
M 14,a[228:231],42
s_add_u32 m0,s57,28672
R 30,1,33792
M 14,a[236:239],46
D 0,56,0
M 10,a[224:227],42
R 34,1,34816
M 10,a[232:235],46
L 3
M 10,a[240:243],50
R 38,1,35840
M 10,a[248:251],54
M 14,a[244:247],50
R 42,1,36864
M 14,a[252:255],54
L 3
D 0,56,1024
M 14,v[68:71],26
R 46,1,37888
M 14,v[76:79],30
M 10,v[64:67],26
R 50,1,38912
M 10,v[72:75],30
L 3
M 10,v[80:83],34
R 54,1,39936
M 10,v[88:91],38
D 0,56,2048
M 14,v[84:87],34
R 26,1,40960
M 14,v[92:95],38
L 3
M 14,v[100:103],42
R 30,1,41984
M 14,v[108:111],46
M 10,v[96:99],42
R 34,1,43008
M 10,v[104:107],46
L 3
D 0,56,3072
M 10,v[112:115],50
s_add_u32 m0,s57,32768
s_add_u32 s56,s56,4096
R 38,1,44032
M 10,v[120:123],54
s_sub_u32 s57,s84,s57
M 14,v[116:119],50
R 42,1,45056
M 14,v[124:127],54
L 3
M 14,v[132:135],26
R 46,1,46080
M 14,v[140:143],30
D 0,56,0
M 10,v[128:131],26
R 50,1,47104
M 10,v[136:139],30
L 3
M 10,v[144:147],34
R 54,1,48128
M 10,v[152:155],38
M 14,v[148:151],34
R 26,1,49152
M 14,v[156:159],38
L 3
D 0,56,1024
M 14,v[164:167],42
R 30,1,50176
M 14,v[172:175],46
M 10,v[160:163],42
R 34,1,51200
M 10,v[168:171],46
L 3
M 10,v[176:179],50
R 38,1,52224
M 10,v[184:187],54
D 0,56,2048
M 14,v[180:183],50
R 42,1,53248
M 14,v[188:191],54
L 3
M 14,v[196:199],26
R 46,1,54272
M 14,v[204:207],30
M 10,v[192:195],26
R 50,1,55296
M 10,v[200:203],30
L 3
M 10,v[208:211],34
s_waitcnt vmcnt(7)
R 54,1,56320
M 10,v[216:219],38
s_barrier
ds_read_b32 v7,v6 offset:0
M 14,v[212:215],34
R 26,2,0
M 14,v[220:223],38
L 4
buffer_store_dword v4,v3,s[36:39],s69 offen offset:0 sc0 sc1
M 14,v[228:231],42
s_mov_b32 s69,s68
s_lshr_b32 s68,s62,4
R 30,2,1024
M 14,v[236:239],46
s_add_u32 s68,s68,s48
M 10,v[224:227],42
R 34,2,2048
M 10,v[232:235],46
L 4
L 3
M 10,v[240:243],50
R 38,2,3072
v_cmp_eq_u32 vcc,s53,v7
M 10,v[248:251],54
M 14,v[244:247],50
L 2
s_cmp_eq_u32 vcc_lo,s51
R 42,2,4096
M 14,v[252:255],54
s_cbranch_scc0 Lg4_K1_%=
Lg4_F1_%=:
v_mfma_f32_16x16x32_bf16 v[22:25],a[4:7],v[26:29],0
s_add_u32 m0,s57,0
R 46,2,5120
v_lshlrev_b32 v62,16,v58
M 22,a[12:15],30
s_add_u32 s56,s55,0
v_add_f32 v10,v10,v62
v_and_b32 v63,0xffff0000,v58
v_mfma_f32_16x16x32_bf16 v[18:21],a[0:3],v[26:29],0
s_add_u32 s61,s54,s46
R 50,2,6144
v_add_f32 v11,v11,v63
M 18,a[8:11],30
L 3
D 0,56,0
M 18,a[16:19],34
s_cmp_ge_u32 s61,s63
R 54,2,7168
v_lshlrev_b32 v62,16,v59
M 18,a[24:27],38
s_cselect_b32 s61,s93,s61
v_add_f32 v12,v12,v62
v_and_b32 v63,0xffff0000,v59
M 22,a[20:23],34
s_cselect_b32 s46,s88,s46
R 26,2,8192
v_add_f32 v13,v13,v63
M 22,a[28:31],38
L 3
v_lshlrev_b32 v62,16,v60
v_add_f32 v14,v14,v62
M 22,a[36:39],42
s_cselect_b32 s63,s89,s63
R 30,2,9216
v_and_b32 v63,0xffff0000,v60
M 22,a[44:47],46
D 0,56,1024
M 18,a[32:35],42
s_cselect_b32 s93,s50,s93
R 34,2,10240
v_add_f32 v15,v15,v63
M 18,a[40:43],46
L 3
v_lshlrev_b32 v62,16,v61
v_add_f32 v16,v16,v62
M 18,a[48:51],50
s_cselect_b32 s88,8,s88
R 38,2,11264
v_and_b32 v63,0xffff0000,v61
M 18,a[56:59],54
buffer_load_dwordx4 v[58:61],v0,s[36:39],s74 offen offset:0 sc1
M 22,a[52:55],50
s_cselect_b32 s89,-1,s89
R 42,2,12288
v_add_f32 v17,v17,v63
M 22,a[60:63],54
L 3
v_cvt_pk_bf16_f32 v10,v10,v11
v_cvt_pk_bf16_f32 v11,v12,v13
M 22,a[68:71],26
s_mul_i32 s58,s61,2560
R 46,2,13312
v_cvt_pk_bf16_f32 v12,v14,v15
M 22,a[76:79],30
D 0,56,2048
M 18,a[64:67],26
s_add_u32 s58,s58,109891584
R 50,2,14336
v_cvt_pk_bf16_f32 v13,v16,v17
M 18,a[72:75],30
L 3
buffer_store_dwordx4 v[10:13],v0,s[36:39],s66 offen offset:0 sc0 sc1
M 18,a[80:83],34
s_lshl_b32 s62,s54,15
s_mov_b32 s67,s65
R 54,2,15360
M 18,a[88:91],38
s_cmp_ge_u32 s67,s45
s_cselect_b32 s51,1,-1
s_add_u32 s74,s49,s62
M 22,a[84:87],34
s_add_u32 s65,s47,s62
s_mov_b32 s54,s61
R 26,2,16384
M 22,a[92:95],38
L 3
M 22,a[100:103],42
R 30,2,17408
M 22,a[108:111],46
D 0,56,3072
M 18,a[96:99],42
s_add_u32 m0,s57,4096
s_add_u32 s56,s56,4096
R 34,2,18432
M 18,a[104:107],46
L 3
M 18,a[112:115],50
R 38,2,19456
M 18,a[120:123],54
M 22,a[116:119],50
R 42,2,20480
M 22,a[124:127],54
L 3
D 0,56,0
M 22,a[132:135],26
R 46,2,21504
M 22,a[140:143],30
M 18,a[128:131],26
R 50,2,22528
M 18,a[136:139],30
L 3
M 18,a[144:147],34
R 54,2,23552
M 18,a[152:155],38
M 22,a[148:151],34
R 26,2,24576
M 22,a[156:159],38
L 3
D 0,56,1024
M 22,a[164:167],42
R 30,2,25600
M 22,a[172:175],46
M 18,a[160:163],42
R 34,2,26624
M 18,a[168:171],46
L 3
M 18,a[176:179],50
s_waitcnt vmcnt(9)
R 38,2,27648
M 18,a[184:187],54
s_barrier
M 22,a[180:183],50
R 42,2,28672
M 22,a[188:191],54
L 3
D 0,56,2048
M 22,a[196:199],26
s_add_u32 m0,s59,1024
s_add_u32 s56,s55,28672
R 46,2,29696
M 22,a[204:207],30
s_mul_i32 s55,s61,114688
s_add_u32 s55,s55,s60
M 18,a[192:195],26
R 50,2,30720
M 18,a[200:203],30
L 3
M 18,a[208:211],34
R 54,2,31744
M 18,a[216:219],38
M 22,a[212:215],34
R 26,2,32768
M 22,a[220:223],38
L 3
buffer_load_dword v8,s[36:39],s58 offen offset:0 sc1 lds
M 22,a[228:231],42
s_add_u32 m0,s57,28672
R 30,2,33792
M 22,a[236:239],46
D 0,56,0
M 18,a[224:227],42
R 34,2,34816
M 18,a[232:235],46
L 3
M 18,a[240:243],50
R 38,2,35840
M 18,a[248:251],54
M 22,a[244:247],50
R 42,2,36864
M 22,a[252:255],54
L 3
D 0,56,1024
M 22,v[68:71],26
R 46,2,37888
M 22,v[76:79],30
M 18,v[64:67],26
R 50,2,38912
M 18,v[72:75],30
L 3
M 18,v[80:83],34
R 54,2,39936
M 18,v[88:91],38
D 0,56,2048
M 22,v[84:87],34
R 26,2,40960
M 22,v[92:95],38
L 3
M 22,v[100:103],42
R 30,2,41984
M 22,v[108:111],46
M 18,v[96:99],42
R 34,2,43008
M 18,v[104:107],46
L 3
D 0,56,3072
M 18,v[112:115],50
s_add_u32 m0,s57,32768
s_add_u32 s56,s56,4096
R 38,2,44032
M 18,v[120:123],54
s_sub_u32 s57,s84,s57
M 22,v[116:119],50
R 42,2,45056
M 22,v[124:127],54
L 3
M 22,v[132:135],26
R 46,2,46080
M 22,v[140:143],30
D 0,56,0
M 18,v[128:131],26
R 50,2,47104
M 18,v[136:139],30
L 3
M 18,v[144:147],34
R 54,2,48128
M 18,v[152:155],38
M 22,v[148:151],34
R 26,2,49152
M 22,v[156:159],38
L 3
D 0,56,1024
M 22,v[164:167],42
R 30,2,50176
M 22,v[172:175],46
M 18,v[160:163],42
R 34,2,51200
M 18,v[168:171],46
L 3
M 18,v[176:179],50
R 38,2,52224
M 18,v[184:187],54
D 0,56,2048
M 22,v[180:183],50
R 42,2,53248
M 22,v[188:191],54
L 3
M 22,v[196:199],26
R 46,2,54272
M 22,v[204:207],30
)"
R"(M 18,v[192:195],26
R 50,2,55296
M 18,v[200:203],30
L 3
M 18,v[208:211],34
s_waitcnt vmcnt(7)
R 54,2,56320
M 18,v[216:219],38
s_barrier
ds_read_b32 v7,v6 offset:1024
M 22,v[212:215],34
R 26,1,0
M 22,v[220:223],38
L 4
buffer_store_dword v4,v3,s[36:39],s69 offen offset:0 sc0 sc1
M 22,v[228:231],42
s_mov_b32 s69,s68
s_lshr_b32 s68,s62,4
R 30,1,1024
M 22,v[236:239],46
s_add_u32 s68,s68,s48
M 18,v[224:227],42
R 34,1,2048
M 18,v[232:235],46
L 4
L 3
M 18,v[240:243],50
R 38,1,3072
v_cmp_eq_u32 vcc,s53,v7
M 18,v[248:251],54
M 22,v[244:247],50
L 2
s_cmp_eq_u32 vcc_lo,s51
R 42,1,4096
M 22,v[252:255],54
s_cbranch_scc1 Lg4_F0_%=
Lg4_K0_%=:
s_cmp_eq_u32 s51,1
s_cbranch_scc0 Lg4_L0_%=
s_cmp_eq_u32 vcc_lo,-1
s_cbranch_scc1 Lg4_Fa0_%=
s_branch Lg4_La0_%=
Lg4_L0_%=:
v_mfma_f32_16x16x32_bf16 v[14:17],a[4:7],v[26:29],0
s_add_u32 m0,s59,3072
R 46,1,5120
v_lshlrev_b32 v62,16,v58
M 14,a[12:15],30
buffer_load_dword v8,s[36:39],s58 offen offset:0 sc1 lds
v_mfma_f32_16x16x32_bf16 v[10:13],a[0:3],v[26:29],0
s_mov_b32 s66,s65
R 50,1,6144
v_add_f32 v18,v18,v62
M 10,a[8:11],30
L 3
v_and_b32 v63,0xffff0000,v58
v_add_f32 v19,v19,v63
M 10,a[16:19],34
s_lshl_b32 s62,s54,15
R 54,1,7168
v_lshlrev_b32 v62,16,v59
M 10,a[24:27],38
s_cmp_ge_u32 s66,s45
v_add_f32 v20,v20,v62
v_and_b32 v63,0xffff0000,v59
M 14,a[20:23],34
s_cselect_b32 s51,1,-1
R 26,1,8192
v_add_f32 v21,v21,v63
M 14,a[28:31],38
L 3
v_lshlrev_b32 v62,16,v60
v_add_f32 v22,v22,v62
M 14,a[36:39],42
s_add_u32 s65,s47,s62
R 30,1,9216
v_and_b32 v63,0xffff0000,v60
M 14,a[44:47],46
v_add_f32 v23,v23,v63
v_lshlrev_b32 v62,16,v61
M 10,a[32:35],42
R 34,1,10240
v_add_f32 v24,v24,v62
M 10,a[40:43],46
L 3
v_and_b32 v63,0xffff0000,v61
v_add_f32 v25,v25,v63
M 10,a[48:51],50
R 38,1,11264
v_cvt_pk_bf16_f32 v18,v18,v19
M 10,a[56:59],54
buffer_load_dwordx4 v[58:61],v0,s[36:39],s74 offen offset:0 sc1
M 14,a[52:55],50
s_add_u32 s74,s49,s62
R 42,1,12288
v_cvt_pk_bf16_f32 v19,v20,v21
M 14,a[60:63],54
L 3
v_cvt_pk_bf16_f32 v20,v22,v23
v_cvt_pk_bf16_f32 v21,v24,v25
M 14,a[68:71],26
R 46,1,13312
M 14,a[76:79],30
buffer_store_dwordx4 v[18:21],v0,s[36:39],s67 offen offset:0 sc0 sc1
M 10,a[64:67],26
R 50,1,14336
M 10,a[72:75],30
L 3
M 10,a[80:83],34
R 54,1,15360
M 10,a[88:91],38
M 14,a[84:87],34
R 26,1,16384
M 14,a[92:95],38
L 3
M 14,a[100:103],42
R 30,1,17408
M 14,a[108:111],46
M 10,a[96:99],42
R 34,1,18432
M 10,a[104:107],46
L 3
M 10,a[112:115],50
R 38,1,19456
M 10,a[120:123],54
M 14,a[116:119],50
R 42,1,20480
M 14,a[124:127],54
L 3
M 14,a[132:135],26
R 46,1,21504
M 14,a[140:143],30
M 10,a[128:131],26
R 50,1,22528
M 10,a[136:139],30
L 3
M 10,a[144:147],34
R 54,1,23552
M 10,a[152:155],38
M 14,a[148:151],34
R 26,1,24576
M 14,a[156:159],38
L 3
M 14,a[164:167],42
R 30,1,25600
M 14,a[172:175],46
M 10,a[160:163],42
R 34,1,26624
M 10,a[168:171],46
L 3
M 10,a[176:179],50
s_waitcnt vmcnt(2)
R 38,1,27648
M 10,a[184:187],54
s_barrier
ds_read_b32 v7,v6 offset:3072
M 14,a[180:183],50
R 42,1,28672
M 14,a[188:191],54
L 4
M 14,a[196:199],26
R 46,1,29696
M 14,a[204:207],30
M 10,a[192:195],26
R 50,1,30720
M 10,a[200:203],30
L 4
L 3
v_cmp_eq_u32 vcc,s53,v7
M 10,a[208:211],34
R 54,1,31744
M 10,a[216:219],38
M 14,a[212:215],34
s_cmp_eq_u32 vcc_lo,-1
R 26,1,32768
s_cbranch_scc0 Lg4_X0_%=
M 14,a[220:223],38
L 3
s_add_u32 m0,s57,0
s_add_u32 s56,s55,0
M 14,a[228:231],42
s_add_u32 s61,s54,s46
s_cmp_ge_u32 s61,s63
s_cselect_b32 s61,s93,s61
R 30,1,33792
M 14,a[236:239],46
s_mul_i32 s58,s61,2560
D 0,56,0
M 10,a[224:227],42
s_cselect_b32 s46,s88,s46
s_cselect_b32 s63,s89,s63
R 34,1,34816
M 10,a[232:235],46
L 3
s_cselect_b32 s93,s50,s93
s_cselect_b32 s88,8,s88
M 10,a[240:243],50
s_cselect_b32 s89,-1,s89
s_add_u32 s58,s58,109891584
R 38,1,35840
M 10,a[248:251],54
D 0,56,1024
M 14,a[244:247],50
s_mov_b32 s54,s61
R 42,1,36864
M 14,a[252:255],54
L 3
M 14,v[68:71],26
R 46,1,37888
M 14,v[76:79],30
D 0,56,2048
M 10,v[64:67],26
R 50,1,38912
M 10,v[72:75],30
L 3
M 10,v[80:83],34
R 54,1,39936
M 10,v[88:91],38
D 0,56,3072
M 14,v[84:87],34
s_add_u32 m0,s57,4096
s_add_u32 s56,s56,4096
R 26,1,40960
M 14,v[92:95],38
L 3
M 14,v[100:103],42
R 30,1,41984
M 14,v[108:111],46
D 0,56,0
M 10,v[96:99],42
R 34,1,43008
M 10,v[104:107],46
L 3
M 10,v[112:115],50
R 38,1,44032
M 10,v[120:123],54
D 0,56,1024
M 14,v[116:119],50
R 42,1,45056
M 14,v[124:127],54
L 3
M 14,v[132:135],26
R 46,1,46080
M 14,v[140:143],30
D 0,56,2048
M 10,v[128:131],26
s_add_u32 m0,s59,0
s_add_u32 s56,s55,28672
R 50,1,47104
M 10,v[136:139],30
L 3
buffer_load_dword v8,s[36:39],s58 offen offset:0 sc1 lds
M 10,v[144:147],34
s_add_u32 m0,s57,28672
s_mul_i32 s55,s61,114688
R 54,1,48128
M 10,v[152:155],38
s_add_u32 s55,s55,s60
M 14,v[148:151],34
R 26,1,49152
M 14,v[156:159],38
L 3
D 0,56,0
M 14,v[164:167],42
R 30,1,50176
M 14,v[172:175],46
M 10,v[160:163],42
R 34,1,51200
M 10,v[168:171],46
L 3
D 0,56,1024
M 10,v[176:179],50
R 38,1,52224
M 10,v[184:187],54
D 0,56,2048
M 14,v[180:183],50
R 42,1,53248
M 14,v[188:191],54
L 3
M 14,v[196:199],26
R 46,1,54272
M 14,v[204:207],30
D 0,56,3072
M 10,v[192:195],26
s_add_u32 m0,s57,32768
s_add_u32 s56,s56,4096
R 50,1,55296
M 10,v[200:203],30
L 3
s_sub_u32 s57,s84,s57
M 10,v[208:211],34
s_waitcnt vmcnt(4)
R 54,1,56320
M 10,v[216:219],38
s_barrier
ds_read_b32 v7,v6 offset:0
M 14,v[212:215],34
R 26,2,0
M 14,v[220:223],38
L 4
buffer_store_dword v4,v3,s[36:39],s69 offen offset:0 sc0 sc1
M 14,v[228:231],42
s_mov_b32 s69,s68
s_lshr_b32 s68,s62,4
R 30,2,1024
M 14,v[236:239],46
D 0,56,0
M 10,v[224:227],42
s_add_u32 s68,s68,s48
R 34,2,2048
M 10,v[232:235],46
L 4
D 0,56,1024
M 10,v[240:243],50
L 3
R 38,2,3072
v_cmp_eq_u32 vcc,s53,v7
M 10,v[248:251],54
D 0,56,2048
M 14,v[244:247],50
L 2
s_cmp_eq_u32 vcc_lo,s51
R 42,2,4096
M 14,v[252:255],54
buffer_store_dword v4,v3,s[36:39],s71 offen offset:0 sc0 sc1
s_cbranch_scc1 Lg4_F1_%=
s_branch Lg4_K1_%=
Lg4_X0_%=:
M 14,a[220:223],38
L 3
s_add_u32 m0,s59,0
M 14,a[228:231],42
R 30,1,33792
M 14,a[236:239],46
buffer_load_dword v8,s[36:39],s58 offen offset:0 sc1 lds
M 10,a[224:227],42
R 34,1,34816
M 10,a[232:235],46
L 3
M 10,a[240:243],50
R 38,1,35840
M 10,a[248:251],54
M 14,a[244:247],50
R 42,1,36864
M 14,a[252:255],54
L 3
M 14,v[68:71],26
R 46,1,37888
M 14,v[76:79],30
M 10,v[64:67],26
R 50,1,38912
M 10,v[72:75],30
L 3
M 10,v[80:83],34
R 54,1,39936
M 10,v[88:91],38
M 14,v[84:87],34
R 26,1,40960
M 14,v[92:95],38
L 3
M 14,v[100:103],42
R 30,1,41984
M 14,v[108:111],46
M 10,v[96:99],42
R 34,1,43008
M 10,v[104:107],46
L 3
M 10,v[112:115],50
R 38,1,44032
M 10,v[120:123],54
M 14,v[116:119],50
R 42,1,45056
M 14,v[124:127],54
L 3
M 14,v[132:135],26
R 46,1,46080
M 14,v[140:143],30
M 10,v[128:131],26
R 50,1,47104
M 10,v[136:139],30
L 3
M 10,v[144:147],34
R 54,1,48128
M 10,v[152:155],38
M 14,v[148:151],34
R 26,1,49152
M 14,v[156:159],38
L 3
M 14,v[164:167],42
R 30,1,50176
M 14,v[172:175],46
M 10,v[160:163],42
R 34,1,51200
M 10,v[168:171],46
L 3
M 10,v[176:179],50
R 38,1,52224
M 10,v[184:187],54
M 14,v[180:183],50
R 42,1,53248
M 14,v[188:191],54
L 3
M 14,v[196:199],26
R 46,1,54272
M 14,v[204:207],30
M 10,v[192:195],26
R 50,1,55296
M 10,v[200:203],30
L 3
M 10,v[208:211],34
s_waitcnt vmcnt(0)
R 54,1,56320
M 10,v[216:219],38
s_barrier
buffer_store_dword v4,v3,s[36:39],s69 offen offset:0 sc0 sc1
M 14,v[212:215],34
s_mov_b32 s69,s68
s_lshr_b32 s68,s62,4
ds_read_b32 v7,v6 offset:0
M 14,v[220:223],38
L 3
s_add_u32 s68,s68,s48
M 14,v[228:231],42
M 14,v[236:239],46
M 10,v[224:227],42
M 10,v[232:235],46
L 1
M 10,v[240:243],50
L 0
v_cmp_eq_u32 vcc,s53,v7
M 10,v[248:251],54
M 14,v[244:247],50
M 14,v[252:255],54
s_cmp_eq_u32 s51,1
s_cbranch_scc1 Lg4_pa0_%=
s_mov_b32 s80,1
s_cmp_eq_u32 vcc_lo,-1
s_cbranch_scc0 Lg4_xn0_%=
s_add_u32 m0,s57,0
s_add_u32 s56,s55,0
D 0,56,0
v_lshlrev_b32 v62,16,v58
D 0,56,1024
v_add_f32 v10,v10,v62
D 0,56,2048
v_and_b32 v63,0xffff0000,v58
D 0,56,3072
v_add_f32 v11,v11,v63
s_add_u32 m0,s57,4096
s_add_u32 s56,s56,4096
D 0,56,0
v_lshlrev_b32 v62,16,v59
D 0,56,1024
v_add_f32 v12,v12,v62
D 0,56,2048
v_and_b32 v63,0xffff0000,v59
s_add_u32 s61,s54,s46
s_cmp_ge_u32 s61,s63
s_cselect_b32 s61,s93,s61
s_cselect_b32 s46,s88,s46
s_cselect_b32 s63,s89,s63
s_cselect_b32 s93,s50,s93
s_cselect_b32 s88,8,s88
s_cselect_b32 s89,-1,s89
s_mul_i32 s58,s61,2560
s_add_u32 s58,s58,109891584
s_add_u32 m0,s57,28672
s_add_u32 s56,s55,28672
D 0,56,0
v_add_f32 v13,v13,v63
D 0,56,1024
v_lshlrev_b32 v62,16,v60
D 0,56,2048
v_add_f32 v14,v14,v62
D 0,56,3072
v_and_b32 v63,0xffff0000,v60
s_add_u32 m0,s57,32768
s_add_u32 s56,s56,4096
D 0,56,0
v_add_f32 v15,v15,v63
D 0,56,1024
v_lshlrev_b32 v62,16,v61
D 0,56,2048
v_add_f32 v16,v16,v62
s_mul_i32 s55,s61,114688
s_add_u32 s55,s55,s60
s_sub_u32 s57,s84,s57
s_mov_b32 s54,s61
v_and_b32 v63,0xffff0000,v61
v_add_f32 v17,v17,v63
v_cvt_pk_bf16_f32 v10,v10,v11
v_cvt_pk_bf16_f32 v11,v12,v13
v_cvt_pk_bf16_f32 v12,v14,v15
v_cvt_pk_bf16_f32 v13,v16,v17
buffer_store_dwordx4 v[10:13],v0,s[36:39],s66 offen offset:0 sc0 sc1
s_mov_b32 s72,s69
s_mov_b32 s69,0x7fff0000
s_mov_b32 s66,0x7fff0000
s_mov_b32 s76,0x7fff0000
s_branch Lg4_gox_%=
)"
R"(Lg4_xn0_%=:
s_cmp_ge_u32 s54,256
s_cbranch_scc1 Lg4_fin0_%=
s_add_u32 m0,s59,4096
s_nop 0
buffer_load_dword v8,s[36:39],s58 offen offset:0 sc1 lds
buffer_store_dword v4,v3,s[36:39],s71 offen offset:0 sc0 sc1
s_add_u32 m0,s59,5120
s_nop 0
buffer_load_dword v8,s[36:39],s58 offen offset:0 sc1 lds
buffer_store_dword v4,v3,s[36:39],s71 offen offset:0 sc0 sc1
s_mov_b32 s83,0
v_lshlrev_b32 v62,16,v58
v_add_f32 v10,v10,v62
v_and_b32 v63,0xffff0000,v58
v_add_f32 v11,v11,v63
v_lshlrev_b32 v62,16,v59
v_add_f32 v12,v12,v62
v_and_b32 v63,0xffff0000,v59
v_add_f32 v13,v13,v63
v_lshlrev_b32 v62,16,v60
v_add_f32 v14,v14,v62
v_and_b32 v63,0xffff0000,v60
v_add_f32 v15,v15,v63
v_lshlrev_b32 v62,16,v61
v_add_f32 v16,v16,v62
v_and_b32 v63,0xffff0000,v61
v_add_f32 v17,v17,v63
v_cvt_pk_bf16_f32 v10,v10,v11
v_cvt_pk_bf16_f32 v11,v12,v13
v_cvt_pk_bf16_f32 v12,v14,v15
v_cvt_pk_bf16_f32 v13,v16,v17
buffer_store_dwordx4 v[10:13],v0,s[36:39],s66 offen offset:0 sc0 sc1
s_mov_b32 s72,s69
s_mov_b32 s69,0x7fff0000
s_mov_b32 s66,0x7fff0000
s_mov_b32 s76,0x7fff0000
s_branch Lg4_poll_%=
Lg4_fin0_%=:
v_lshlrev_b32 v62,16,v58
v_add_f32 v10,v10,v62
v_and_b32 v63,0xffff0000,v58
v_add_f32 v11,v11,v63
v_lshlrev_b32 v62,16,v59
v_add_f32 v12,v12,v62
v_and_b32 v63,0xffff0000,v59
v_add_f32 v13,v13,v63
v_lshlrev_b32 v62,16,v60
v_add_f32 v14,v14,v62
v_and_b32 v63,0xffff0000,v60
v_add_f32 v15,v15,v63
v_lshlrev_b32 v62,16,v61
v_add_f32 v16,v16,v62
v_and_b32 v63,0xffff0000,v61
v_add_f32 v17,v17,v63
v_cvt_pk_bf16_f32 v10,v10,v11
v_cvt_pk_bf16_f32 v11,v12,v13
v_cvt_pk_bf16_f32 v12,v14,v15
v_cvt_pk_bf16_f32 v13,v16,v17
buffer_store_dwordx4 v[10:13],v0,s[36:39],s66 offen offset:0 sc0 sc1
s_mov_b32 s72,s69
s_mov_b32 s69,0x7fff0000
s_mov_b32 s66,0x7fff0000
s_mov_b32 s76,0x7fff0000
s_branch Lg4_done_%=
Lg4_K1_%=:
s_cmp_eq_u32 s51,1
s_cbranch_scc0 Lg4_L1_%=
s_cmp_eq_u32 vcc_lo,-1
s_cbranch_scc1 Lg4_Fa1_%=
s_branch Lg4_La1_%=
Lg4_L1_%=:
v_mfma_f32_16x16x32_bf16 v[22:25],a[4:7],v[26:29],0
s_add_u32 m0,s59,3072
R 46,2,5120
v_lshlrev_b32 v62,16,v58
M 22,a[12:15],30
buffer_load_dword v8,s[36:39],s58 offen offset:0 sc1 lds
v_mfma_f32_16x16x32_bf16 v[18:21],a[0:3],v[26:29],0
s_mov_b32 s67,s65
R 50,2,6144
v_add_f32 v10,v10,v62
M 18,a[8:11],30
L 3
v_and_b32 v63,0xffff0000,v58
v_add_f32 v11,v11,v63
M 18,a[16:19],34
s_lshl_b32 s62,s54,15
R 54,2,7168
v_lshlrev_b32 v62,16,v59
M 18,a[24:27],38
s_cmp_ge_u32 s67,s45
v_add_f32 v12,v12,v62
v_and_b32 v63,0xffff0000,v59
M 22,a[20:23],34
s_cselect_b32 s51,1,-1
R 26,2,8192
v_add_f32 v13,v13,v63
M 22,a[28:31],38
L 3
v_lshlrev_b32 v62,16,v60
v_add_f32 v14,v14,v62
M 22,a[36:39],42
s_add_u32 s65,s47,s62
R 30,2,9216
v_and_b32 v63,0xffff0000,v60
M 22,a[44:47],46
v_add_f32 v15,v15,v63
v_lshlrev_b32 v62,16,v61
M 18,a[32:35],42
R 34,2,10240
v_add_f32 v16,v16,v62
M 18,a[40:43],46
L 3
v_and_b32 v63,0xffff0000,v61
v_add_f32 v17,v17,v63
M 18,a[48:51],50
R 38,2,11264
v_cvt_pk_bf16_f32 v10,v10,v11
M 18,a[56:59],54
buffer_load_dwordx4 v[58:61],v0,s[36:39],s74 offen offset:0 sc1
M 22,a[52:55],50
s_add_u32 s74,s49,s62
R 42,2,12288
v_cvt_pk_bf16_f32 v11,v12,v13
M 22,a[60:63],54
L 3
v_cvt_pk_bf16_f32 v12,v14,v15
v_cvt_pk_bf16_f32 v13,v16,v17
M 22,a[68:71],26
R 46,2,13312
M 22,a[76:79],30
buffer_store_dwordx4 v[10:13],v0,s[36:39],s66 offen offset:0 sc0 sc1
M 18,a[64:67],26
R 50,2,14336
M 18,a[72:75],30
L 3
M 18,a[80:83],34
R 54,2,15360
M 18,a[88:91],38
M 22,a[84:87],34
R 26,2,16384
M 22,a[92:95],38
L 3
M 22,a[100:103],42
R 30,2,17408
M 22,a[108:111],46
M 18,a[96:99],42
R 34,2,18432
M 18,a[104:107],46
L 3
M 18,a[112:115],50
R 38,2,19456
M 18,a[120:123],54
M 22,a[116:119],50
R 42,2,20480
M 22,a[124:127],54
L 3
M 22,a[132:135],26
R 46,2,21504
M 22,a[140:143],30
M 18,a[128:131],26
R 50,2,22528
M 18,a[136:139],30
L 3
M 18,a[144:147],34
R 54,2,23552
M 18,a[152:155],38
M 22,a[148:151],34
R 26,2,24576
M 22,a[156:159],38
L 3
M 22,a[164:167],42
R 30,2,25600
M 22,a[172:175],46
M 18,a[160:163],42
R 34,2,26624
M 18,a[168:171],46
L 3
M 18,a[176:179],50
s_waitcnt vmcnt(2)
R 38,2,27648
M 18,a[184:187],54
s_barrier
ds_read_b32 v7,v6 offset:3072
M 22,a[180:183],50
R 42,2,28672
M 22,a[188:191],54
L 4
M 22,a[196:199],26
R 46,2,29696
M 22,a[204:207],30
M 18,a[192:195],26
R 50,2,30720
M 18,a[200:203],30
L 4
L 3
v_cmp_eq_u32 vcc,s53,v7
M 18,a[208:211],34
R 54,2,31744
M 18,a[216:219],38
M 22,a[212:215],34
s_cmp_eq_u32 vcc_lo,-1
R 26,2,32768
s_cbranch_scc0 Lg4_X1_%=
M 22,a[220:223],38
L 3
s_add_u32 m0,s57,0
s_add_u32 s56,s55,0
M 22,a[228:231],42
s_add_u32 s61,s54,s46
s_cmp_ge_u32 s61,s63
s_cselect_b32 s61,s93,s61
R 30,2,33792
M 22,a[236:239],46
s_mul_i32 s58,s61,2560
D 0,56,0
M 18,a[224:227],42
s_cselect_b32 s46,s88,s46
s_cselect_b32 s63,s89,s63
R 34,2,34816
M 18,a[232:235],46
L 3
s_cselect_b32 s93,s50,s93
s_cselect_b32 s88,8,s88
M 18,a[240:243],50
s_cselect_b32 s89,-1,s89
s_add_u32 s58,s58,109891584
R 38,2,35840
M 18,a[248:251],54
D 0,56,1024
M 22,a[244:247],50
s_mov_b32 s54,s61
R 42,2,36864
M 22,a[252:255],54
L 3
M 22,v[68:71],26
R 46,2,37888
M 22,v[76:79],30
D 0,56,2048
M 18,v[64:67],26
R 50,2,38912
M 18,v[72:75],30
L 3
M 18,v[80:83],34
R 54,2,39936
M 18,v[88:91],38
D 0,56,3072
M 22,v[84:87],34
s_add_u32 m0,s57,4096
s_add_u32 s56,s56,4096
R 26,2,40960
M 22,v[92:95],38
L 3
M 22,v[100:103],42
R 30,2,41984
M 22,v[108:111],46
D 0,56,0
M 18,v[96:99],42
R 34,2,43008
M 18,v[104:107],46
L 3
M 18,v[112:115],50
R 38,2,44032
M 18,v[120:123],54
D 0,56,1024
M 22,v[116:119],50
R 42,2,45056
M 22,v[124:127],54
L 3
M 22,v[132:135],26
R 46,2,46080
M 22,v[140:143],30
D 0,56,2048
M 18,v[128:131],26
s_add_u32 m0,s59,1024
s_add_u32 s56,s55,28672
R 50,2,47104
M 18,v[136:139],30
L 3
buffer_load_dword v8,s[36:39],s58 offen offset:0 sc1 lds
M 18,v[144:147],34
s_add_u32 m0,s57,28672
s_mul_i32 s55,s61,114688
R 54,2,48128
M 18,v[152:155],38
s_add_u32 s55,s55,s60
M 22,v[148:151],34
R 26,2,49152
M 22,v[156:159],38
L 3
D 0,56,0
M 22,v[164:167],42
R 30,2,50176
M 22,v[172:175],46
M 18,v[160:163],42
R 34,2,51200
M 18,v[168:171],46
L 3
D 0,56,1024
M 18,v[176:179],50
R 38,2,52224
M 18,v[184:187],54
D 0,56,2048
M 22,v[180:183],50
R 42,2,53248
M 22,v[188:191],54
L 3
M 22,v[196:199],26
R 46,2,54272
M 22,v[204:207],30
D 0,56,3072
M 18,v[192:195],26
s_add_u32 m0,s57,32768
s_add_u32 s56,s56,4096
R 50,2,55296
M 18,v[200:203],30
L 3
s_sub_u32 s57,s84,s57
M 18,v[208:211],34
s_waitcnt vmcnt(4)
R 54,2,56320
M 18,v[216:219],38
s_barrier
ds_read_b32 v7,v6 offset:1024
M 22,v[212:215],34
R 26,1,0
M 22,v[220:223],38
L 4
buffer_store_dword v4,v3,s[36:39],s69 offen offset:0 sc0 sc1
M 22,v[228:231],42
s_mov_b32 s69,s68
s_lshr_b32 s68,s62,4
R 30,1,1024
M 22,v[236:239],46
D 0,56,0
M 18,v[224:227],42
s_add_u32 s68,s68,s48
R 34,1,2048
M 18,v[232:235],46
L 4
D 0,56,1024
M 18,v[240:243],50
L 3
R 38,1,3072
v_cmp_eq_u32 vcc,s53,v7
M 18,v[248:251],54
D 0,56,2048
M 22,v[244:247],50
L 2
s_cmp_eq_u32 vcc_lo,s51
R 42,1,4096
M 22,v[252:255],54
buffer_store_dword v4,v3,s[36:39],s71 offen offset:0 sc0 sc1
s_cbranch_scc1 Lg4_F0_%=
s_branch Lg4_K0_%=
Lg4_X1_%=:
M 22,a[220:223],38
L 3
s_add_u32 m0,s59,1024
M 22,a[228:231],42
R 30,2,33792
M 22,a[236:239],46
buffer_load_dword v8,s[36:39],s58 offen offset:0 sc1 lds
M 18,a[224:227],42
R 34,2,34816
M 18,a[232:235],46
L 3
M 18,a[240:243],50
R 38,2,35840
M 18,a[248:251],54
M 22,a[244:247],50
R 42,2,36864
M 22,a[252:255],54
L 3
M 22,v[68:71],26
R 46,2,37888
M 22,v[76:79],30
M 18,v[64:67],26
R 50,2,38912
M 18,v[72:75],30
L 3
M 18,v[80:83],34
R 54,2,39936
M 18,v[88:91],38
M 22,v[84:87],34
R 26,2,40960
M 22,v[92:95],38
L 3
M 22,v[100:103],42
R 30,2,41984
M 22,v[108:111],46
M 18,v[96:99],42
R 34,2,43008
M 18,v[104:107],46
L 3
M 18,v[112:115],50
R 38,2,44032
M 18,v[120:123],54
M 22,v[116:119],50
R 42,2,45056
M 22,v[124:127],54
L 3
M 22,v[132:135],26
R 46,2,46080
M 22,v[140:143],30
M 18,v[128:131],26
R 50,2,47104
M 18,v[136:139],30
L 3
M 18,v[144:147],34
R 54,2,48128
M 18,v[152:155],38
M 22,v[148:151],34
R 26,2,49152
M 22,v[156:159],38
L 3
M 22,v[164:167],42
R 30,2,50176
M 22,v[172:175],46
M 18,v[160:163],42
R 34,2,51200
M 18,v[168:171],46
L 3
M 18,v[176:179],50
R 38,2,52224
M 18,v[184:187],54
M 22,v[180:183],50
R 42,2,53248
M 22,v[188:191],54
L 3
M 22,v[196:199],26
R 46,2,54272
M 22,v[204:207],30
M 18,v[192:195],26
R 50,2,55296
M 18,v[200:203],30
L 3
M 18,v[208:211],34
s_waitcnt vmcnt(0)
R 54,2,56320
M 18,v[216:219],38
s_barrier
buffer_store_dword v4,v3,s[36:39],s69 offen offset:0 sc0 sc1
M 22,v[212:215],34
s_mov_b32 s69,s68
s_lshr_b32 s68,s62,4
ds_read_b32 v7,v6 offset:1024
M 22,v[220:223],38
L 3
s_add_u32 s68,s68,s48
M 22,v[228:231],42
M 22,v[236:239],46
M 18,v[224:227],42
M 18,v[232:235],46
L 1
M 18,v[240:243],50
L 0
v_cmp_eq_u32 vcc,s53,v7
M 18,v[248:251],54
M 22,v[244:247],50
M 22,v[252:255],54
s_cmp_eq_u32 s51,1
s_cbranch_scc1 Lg4_pa1_%=
s_mov_b32 s80,0
s_cmp_eq_u32 vcc_lo,-1
s_cbranch_scc0 Lg4_xn1_%=
s_add_u32 m0,s57,0
s_add_u32 s56,s55,0
D 0,56,0
v_lshlrev_b32 v62,16,v58
D 0,56,1024
v_add_f32 v18,v18,v62
D 0,56,2048
v_and_b32 v63,0xffff0000,v58
D 0,56,3072
v_add_f32 v19,v19,v63
s_add_u32 m0,s57,4096
s_add_u32 s56,s56,4096
D 0,56,0
v_lshlrev_b32 v62,16,v59
D 0,56,1024
v_add_f32 v20,v20,v62
D 0,56,2048
v_and_b32 v63,0xffff0000,v59
s_add_u32 s61,s54,s46
s_cmp_ge_u32 s61,s63
s_cselect_b32 s61,s93,s61
s_cselect_b32 s46,s88,s46
s_cselect_b32 s63,s89,s63
s_cselect_b32 s93,s50,s93
s_cselect_b32 s88,8,s88
s_cselect_b32 s89,-1,s89
s_mul_i32 s58,s61,2560
s_add_u32 s58,s58,109891584
s_add_u32 m0,s57,28672
s_add_u32 s56,s55,28672
D 0,56,0
v_add_f32 v21,v21,v63
D 0,56,1024
)"
R"(v_lshlrev_b32 v62,16,v60
D 0,56,2048
v_add_f32 v22,v22,v62
D 0,56,3072
v_and_b32 v63,0xffff0000,v60
s_add_u32 m0,s57,32768
s_add_u32 s56,s56,4096
D 0,56,0
v_add_f32 v23,v23,v63
D 0,56,1024
v_lshlrev_b32 v62,16,v61
D 0,56,2048
v_add_f32 v24,v24,v62
s_mul_i32 s55,s61,114688
s_add_u32 s55,s55,s60
s_sub_u32 s57,s84,s57
s_mov_b32 s54,s61
v_and_b32 v63,0xffff0000,v61
v_add_f32 v25,v25,v63
v_cvt_pk_bf16_f32 v18,v18,v19
v_cvt_pk_bf16_f32 v19,v20,v21
v_cvt_pk_bf16_f32 v20,v22,v23
v_cvt_pk_bf16_f32 v21,v24,v25
buffer_store_dwordx4 v[18:21],v0,s[36:39],s67 offen offset:0 sc0 sc1
s_mov_b32 s72,s69
s_mov_b32 s69,0x7fff0000
s_mov_b32 s67,0x7fff0000
s_mov_b32 s77,0x7fff0000
s_branch Lg4_gox_%=
Lg4_xn1_%=:
s_cmp_ge_u32 s54,256
s_cbranch_scc1 Lg4_fin1_%=
s_add_u32 m0,s59,4096
s_nop 0
buffer_load_dword v8,s[36:39],s58 offen offset:0 sc1 lds
buffer_store_dword v4,v3,s[36:39],s71 offen offset:0 sc0 sc1
s_add_u32 m0,s59,5120
s_nop 0
buffer_load_dword v8,s[36:39],s58 offen offset:0 sc1 lds
buffer_store_dword v4,v3,s[36:39],s71 offen offset:0 sc0 sc1
s_mov_b32 s83,0
v_lshlrev_b32 v62,16,v58
v_add_f32 v18,v18,v62
v_and_b32 v63,0xffff0000,v58
v_add_f32 v19,v19,v63
v_lshlrev_b32 v62,16,v59
v_add_f32 v20,v20,v62
v_and_b32 v63,0xffff0000,v59
v_add_f32 v21,v21,v63
v_lshlrev_b32 v62,16,v60
v_add_f32 v22,v22,v62
v_and_b32 v63,0xffff0000,v60
v_add_f32 v23,v23,v63
v_lshlrev_b32 v62,16,v61
v_add_f32 v24,v24,v62
v_and_b32 v63,0xffff0000,v61
v_add_f32 v25,v25,v63
v_cvt_pk_bf16_f32 v18,v18,v19
v_cvt_pk_bf16_f32 v19,v20,v21
v_cvt_pk_bf16_f32 v20,v22,v23
v_cvt_pk_bf16_f32 v21,v24,v25
buffer_store_dwordx4 v[18:21],v0,s[36:39],s67 offen offset:0 sc0 sc1
s_mov_b32 s72,s69
s_mov_b32 s69,0x7fff0000
s_mov_b32 s67,0x7fff0000
s_mov_b32 s77,0x7fff0000
s_branch Lg4_poll_%=
Lg4_fin1_%=:
v_lshlrev_b32 v62,16,v58
v_add_f32 v18,v18,v62
v_and_b32 v63,0xffff0000,v58
v_add_f32 v19,v19,v63
v_lshlrev_b32 v62,16,v59
v_add_f32 v20,v20,v62
v_and_b32 v63,0xffff0000,v59
v_add_f32 v21,v21,v63
v_lshlrev_b32 v62,16,v60
v_add_f32 v22,v22,v62
v_and_b32 v63,0xffff0000,v60
v_add_f32 v23,v23,v63
v_lshlrev_b32 v62,16,v61
v_add_f32 v24,v24,v62
v_and_b32 v63,0xffff0000,v61
v_add_f32 v25,v25,v63
v_cvt_pk_bf16_f32 v18,v18,v19
v_cvt_pk_bf16_f32 v19,v20,v21
v_cvt_pk_bf16_f32 v20,v22,v23
v_cvt_pk_bf16_f32 v21,v24,v25
buffer_store_dwordx4 v[18:21],v0,s[36:39],s67 offen offset:0 sc0 sc1
s_mov_b32 s72,s69
s_mov_b32 s69,0x7fff0000
s_mov_b32 s67,0x7fff0000
s_mov_b32 s77,0x7fff0000
s_branch Lg4_done_%=
Lg4_Fa0_%=:
s_nop 0
v_mfma_f32_16x16x32_bf16 v[14:17],a[4:7],v[26:29],0
s_add_u32 m0,s57,0
R 46,1,5120
v_lshlrev_b32 v62,16,v58
M 14,a[12:15],30
s_add_u32 s56,s55,0
v_add_f32 v18,v18,v62
v_and_b32 v63,0xffff0000,v58
v_mfma_f32_16x16x32_bf16 v[10:13],a[0:3],v[26:29],0
s_add_u32 s61,s54,s46
R 50,1,6144
v_add_f32 v19,v19,v63
M 10,a[8:11],30
L 3
D 0,56,0
M 10,a[16:19],34
s_cmp_ge_u32 s61,s63
R 54,1,7168
v_lshlrev_b32 v62,16,v59
M 10,a[24:27],38
s_cselect_b32 s61,s93,s61
v_add_f32 v20,v20,v62
v_and_b32 v63,0xffff0000,v59
M 14,a[20:23],34
s_cselect_b32 s46,s88,s46
R 26,1,8192
v_add_f32 v21,v21,v63
M 14,a[28:31],38
L 3
v_lshlrev_b32 v62,16,v60
v_add_f32 v22,v22,v62
M 14,a[36:39],42
s_cselect_b32 s63,s89,s63
R 30,1,9216
v_and_b32 v63,0xffff0000,v60
M 14,a[44:47],46
D 0,56,1024
M 10,a[32:35],42
s_cselect_b32 s93,s50,s93
R 34,1,10240
v_add_f32 v23,v23,v63
M 10,a[40:43],46
L 3
v_lshlrev_b32 v62,16,v61
v_add_f32 v24,v24,v62
M 10,a[48:51],50
s_cselect_b32 s88,8,s88
R 38,1,11264
v_and_b32 v63,0xffff0000,v61
M 10,a[56:59],54
buffer_load_dwordx4 v[58:61],v0,s[36:39],s74 offen offset:0 sc1
M 14,a[52:55],50
s_cselect_b32 s89,-1,s89
R 42,1,12288
v_add_f32 v25,v25,v63
M 14,a[60:63],54
L 3
v_cvt_pk_bf16_f32 v18,v18,v19
v_cvt_pk_bf16_f32 v19,v20,v21
M 14,a[68:71],26
s_mul_i32 s58,s61,2560
R 46,1,13312
v_cvt_pk_bf16_f32 v20,v22,v23
M 14,a[76:79],30
D 0,56,2048
M 10,a[64:67],26
s_add_u32 s58,s58,109891584
R 50,1,14336
v_cvt_pk_bf16_f32 v21,v24,v25
M 10,a[72:75],30
L 3
v_lshrrev_b32 v62,8,v0
v_lshl_add_u32 v62,v62,5,v0
M 10,a[80:83],34
s_sub_u32 s75,s67,s85
R 54,1,15360
v_add_u32 v62,s73,v62
M 10,a[88:91],38
s_mov_b64 s[40:41],s[86:87]
ds_write_b128 v62,v[18:21] offset:0
M 14,a[84:87],34
s_mov_b32 s42,4194304
R 26,1,16384
ds_read2_b32 v[22:23],v5 offset0:0 offset1:16
M 14,a[92:95],38
L 5
s_mov_b32 s43,0x00020000
ds_read2_b32 v[24:25],v5 offset0:32 offset1:48
M 14,a[100:103],42
s_add_u32 s64,s75,0
s_lshl_b32 s62,s54,15
R 30,1,17408
M 14,a[108:111],46
D 0,56,3072
M 10,a[96:99],42
s_add_u32 m0,s57,4096
s_add_u32 s56,s56,4096
R 34,1,18432
M 10,a[104:107],46
L 6
s_mov_b32 s66,s65
s_add_u32 s74,s49,s62
M 10,a[112:115],50
s_add_u32 s65,s47,s62
s_mov_b32 s54,s61
R 38,1,19456
M 10,a[120:123],54
L 3
M 14,a[116:119],50
R 42,1,20480
M 14,a[124:127],54
L 3
D 0,56,0
M 14,a[132:135],26
R 46,1,21504
M 14,a[140:143],30
M 10,a[128:131],26
R 50,1,22528
M 10,a[136:139],30
L 3
M 10,a[144:147],34
R 54,1,23552
M 10,a[152:155],38
M 14,a[148:151],34
R 26,1,24576
M 14,a[156:159],38
L 3
D 0,56,1024
M 14,a[164:167],42
R 30,1,25600
M 14,a[172:175],46
M 10,a[160:163],42
R 34,1,26624
M 10,a[168:171],46
L 3
M 10,a[176:179],50
s_waitcnt vmcnt(8)
R 38,1,27648
M 10,a[184:187],54
s_barrier
M 14,a[180:183],50
R 42,1,28672
M 14,a[188:191],54
L 3
D 0,56,2048
M 14,a[196:199],26
s_add_u32 m0,s59,0
s_add_u32 s56,s55,28672
R 46,1,29696
M 14,a[204:207],30
s_mul_i32 s55,s61,114688
s_add_u32 s55,s55,s60
M 10,a[192:195],26
R 50,1,30720
M 10,a[200:203],30
L 3
M 10,a[208:211],34
R 54,1,31744
M 10,a[216:219],38
M 14,a[212:215],34
R 26,1,32768
M 14,a[220:223],38
L 3
buffer_load_dword v8,s[36:39],s58 offen offset:0 sc1 lds
M 14,a[228:231],42
s_add_u32 m0,s57,28672
R 30,1,33792
M 14,a[236:239],46
D 0,56,0
M 10,a[224:227],42
R 34,1,34816
M 10,a[232:235],46
L 3
M 10,a[240:243],50
R 38,1,35840
M 10,a[248:251],54
M 14,a[244:247],50
R 42,1,36864
M 14,a[252:255],54
L 3
D 0,56,1024
M 14,v[68:71],26
R 46,1,37888
M 14,v[76:79],30
M 10,v[64:67],26
R 50,1,38912
M 10,v[72:75],30
L 3
M 10,v[80:83],34
R 54,1,39936
M 10,v[88:91],38
D 0,56,2048
M 14,v[84:87],34
R 26,1,40960
M 14,v[92:95],38
L 3
M 14,v[100:103],42
R 30,1,41984
M 14,v[108:111],46
M 10,v[96:99],42
R 34,1,43008
M 10,v[104:107],46
L 3
D 0,56,3072
M 10,v[112:115],50
s_add_u32 m0,s57,32768
s_add_u32 s56,s56,4096
R 38,1,44032
M 10,v[120:123],54
s_sub_u32 s57,s84,s57
M 14,v[116:119],50
R 42,1,45056
M 14,v[124:127],54
L 3
M 14,v[132:135],26
R 46,1,46080
M 14,v[140:143],30
D 0,56,0
M 10,v[128:131],26
R 50,1,47104
M 10,v[136:139],30
L 3
M 10,v[144:147],34
R 54,1,48128
M 10,v[152:155],38
M 14,v[148:151],34
R 26,1,49152
M 14,v[156:159],38
L 3
D 0,56,1024
M 14,v[164:167],42
R 30,1,50176
M 14,v[172:175],46
M 10,v[160:163],42
R 34,1,51200
M 10,v[168:171],46
L 3
M 10,v[176:179],50
R 38,1,52224
M 10,v[184:187],54
D 0,56,2048
M 14,v[180:183],50
R 42,1,53248
M 14,v[188:191],54
L 3
M 14,v[196:199],26
R 46,1,54272
M 14,v[204:207],30
M 10,v[192:195],26
R 50,1,55296
M 10,v[200:203],30
L 3
M 10,v[208:211],34
s_waitcnt vmcnt(7)
R 54,1,56320
M 10,v[216:219],38
s_barrier
ds_read_b32 v7,v6 offset:0
M 14,v[212:215],34
R 26,2,0
M 14,v[220:223],38
L 4
M 14,v[228:231],42
R 30,2,1024
M 14,v[236:239],46
buffer_atomic_pk_add_bf16 v22,v9,s[40:43],s64 offen sc1
M 10,v[224:227],42
s_add_u32 s64,s75,8192
R 34,2,2048
M 10,v[232:235],46
L 4
buffer_atomic_pk_add_bf16 v23,v9,s[40:43],s64 offen sc1
M 10,v[240:243],50
s_add_u32 s64,s75,16384
L 3
R 38,2,3072
v_cmp_eq_u32 vcc,s53,v7
M 10,v[248:251],54
buffer_atomic_pk_add_bf16 v24,v9,s[40:43],s64 offen sc1
M 14,v[244:247],50
L 2
s_add_u32 s64,s75,24576
s_cmp_eq_u32 vcc_lo,-1
R 42,2,4096
M 14,v[252:255],54
buffer_atomic_pk_add_bf16 v25,v9,s[40:43],s64 offen sc1
s_cbranch_scc0 Lg4_Ka1_%=
Lg4_Fa1_%=:
s_nop 0
v_mfma_f32_16x16x32_bf16 v[22:25],a[4:7],v[26:29],0
s_add_u32 m0,s57,0
R 46,2,5120
v_lshlrev_b32 v62,16,v58
M 22,a[12:15],30
s_add_u32 s56,s55,0
v_add_f32 v10,v10,v62
v_and_b32 v63,0xffff0000,v58
v_mfma_f32_16x16x32_bf16 v[18:21],a[0:3],v[26:29],0
s_add_u32 s61,s54,s46
R 50,2,6144
v_add_f32 v11,v11,v63
M 18,a[8:11],30
L 3
D 0,56,0
M 18,a[16:19],34
s_cmp_ge_u32 s61,s63
R 54,2,7168
v_lshlrev_b32 v62,16,v59
M 18,a[24:27],38
s_cselect_b32 s61,s93,s61
v_add_f32 v12,v12,v62
v_and_b32 v63,0xffff0000,v59
M 22,a[20:23],34
s_cselect_b32 s46,s88,s46
R 26,2,8192
v_add_f32 v13,v13,v63
M 22,a[28:31],38
L 3
v_lshlrev_b32 v62,16,v60
v_add_f32 v14,v14,v62
M 22,a[36:39],42
s_cselect_b32 s63,s89,s63
R 30,2,9216
v_and_b32 v63,0xffff0000,v60
M 22,a[44:47],46
D 0,56,1024
M 18,a[32:35],42
s_cselect_b32 s93,s50,s93
R 34,2,10240
v_add_f32 v15,v15,v63
M 18,a[40:43],46
L 3
v_lshlrev_b32 v62,16,v61
v_add_f32 v16,v16,v62
M 18,a[48:51],50
s_cselect_b32 s88,8,s88
R 38,2,11264
v_and_b32 v63,0xffff0000,v61
M 18,a[56:59],54
buffer_load_dwordx4 v[58:61],v0,s[36:39],s74 offen offset:0 sc1
M 22,a[52:55],50
s_cselect_b32 s89,-1,s89
R 42,2,12288
v_add_f32 v17,v17,v63
M 22,a[60:63],54
L 3
v_cvt_pk_bf16_f32 v10,v10,v11
v_cvt_pk_bf16_f32 v11,v12,v13
M 22,a[68:71],26
s_mul_i32 s58,s61,2560
R 46,2,13312
v_cvt_pk_bf16_f32 v12,v14,v15
M 22,a[76:79],30
D 0,56,2048
M 18,a[64:67],26
s_add_u32 s58,s58,109891584
R 50,2,14336
v_cvt_pk_bf16_f32 v13,v16,v17
M 18,a[72:75],30
L 3
v_lshrrev_b32 v62,8,v0
v_lshl_add_u32 v62,v62,5,v0
M 18,a[80:83],34
s_sub_u32 s75,s66,s85
R 54,2,15360
v_add_u32 v62,s73,v62
M 18,a[88:91],38
s_mov_b64 s[40:41],s[86:87]
ds_write_b128 v62,v[10:13] offset:0
M 22,a[84:87],34
s_mov_b32 s42,4194304
R 26,2,16384
ds_read2_b32 v[14:15],v5 offset0:0 offset1:16
M 22,a[92:95],38
L 5
s_mov_b32 s43,0x00020000
ds_read2_b32 v[16:17],v5 offset0:32 offset1:48
M 22,a[100:103],42
s_add_u32 s64,s75,0
s_lshl_b32 s62,s54,15
R 30,2,17408
M 22,a[108:111],46
D 0,56,3072
M 18,a[96:99],42
s_add_u32 m0,s57,4096
s_add_u32 s56,s56,4096
R 34,2,18432
M 18,a[104:107],46
L 6
s_mov_b32 s67,s65
s_add_u32 s74,s49,s62
M 18,a[112:115],50
s_add_u32 s65,s47,s62
s_mov_b32 s54,s61
R 38,2,19456
M 18,a[120:123],54
L 3
M 22,a[116:119],50
R 42,2,20480
M 22,a[124:127],54
L 3
D 0,56,0
M 22,a[132:135],26
R 46,2,21504
)"
R"(M 22,a[140:143],30
M 18,a[128:131],26
R 50,2,22528
M 18,a[136:139],30
L 3
M 18,a[144:147],34
R 54,2,23552
M 18,a[152:155],38
M 22,a[148:151],34
R 26,2,24576
M 22,a[156:159],38
L 3
D 0,56,1024
M 22,a[164:167],42
R 30,2,25600
M 22,a[172:175],46
M 18,a[160:163],42
R 34,2,26624
M 18,a[168:171],46
L 3
M 18,a[176:179],50
s_waitcnt vmcnt(8)
R 38,2,27648
M 18,a[184:187],54
s_barrier
M 22,a[180:183],50
R 42,2,28672
M 22,a[188:191],54
L 3
D 0,56,2048
M 22,a[196:199],26
s_add_u32 m0,s59,1024
s_add_u32 s56,s55,28672
R 46,2,29696
M 22,a[204:207],30
s_mul_i32 s55,s61,114688
s_add_u32 s55,s55,s60
M 18,a[192:195],26
R 50,2,30720
M 18,a[200:203],30
L 3
M 18,a[208:211],34
R 54,2,31744
M 18,a[216:219],38
M 22,a[212:215],34
R 26,2,32768
M 22,a[220:223],38
L 3
buffer_load_dword v8,s[36:39],s58 offen offset:0 sc1 lds
M 22,a[228:231],42
s_add_u32 m0,s57,28672
R 30,2,33792
M 22,a[236:239],46
D 0,56,0
M 18,a[224:227],42
R 34,2,34816
M 18,a[232:235],46
L 3
M 18,a[240:243],50
R 38,2,35840
M 18,a[248:251],54
M 22,a[244:247],50
R 42,2,36864
M 22,a[252:255],54
L 3
D 0,56,1024
M 22,v[68:71],26
R 46,2,37888
M 22,v[76:79],30
M 18,v[64:67],26
R 50,2,38912
M 18,v[72:75],30
L 3
M 18,v[80:83],34
R 54,2,39936
M 18,v[88:91],38
D 0,56,2048
M 22,v[84:87],34
R 26,2,40960
M 22,v[92:95],38
L 3
M 22,v[100:103],42
R 30,2,41984
M 22,v[108:111],46
M 18,v[96:99],42
R 34,2,43008
M 18,v[104:107],46
L 3
D 0,56,3072
M 18,v[112:115],50
s_add_u32 m0,s57,32768
s_add_u32 s56,s56,4096
R 38,2,44032
M 18,v[120:123],54
s_sub_u32 s57,s84,s57
M 22,v[116:119],50
R 42,2,45056
M 22,v[124:127],54
L 3
M 22,v[132:135],26
R 46,2,46080
M 22,v[140:143],30
D 0,56,0
M 18,v[128:131],26
R 50,2,47104
M 18,v[136:139],30
L 3
M 18,v[144:147],34
R 54,2,48128
M 18,v[152:155],38
M 22,v[148:151],34
R 26,2,49152
M 22,v[156:159],38
L 3
D 0,56,1024
M 22,v[164:167],42
R 30,2,50176
M 22,v[172:175],46
M 18,v[160:163],42
R 34,2,51200
M 18,v[168:171],46
L 3
M 18,v[176:179],50
R 38,2,52224
M 18,v[184:187],54
D 0,56,2048
M 22,v[180:183],50
R 42,2,53248
M 22,v[188:191],54
L 3
M 22,v[196:199],26
R 46,2,54272
M 22,v[204:207],30
M 18,v[192:195],26
R 50,2,55296
M 18,v[200:203],30
L 3
M 18,v[208:211],34
s_waitcnt vmcnt(7)
R 54,2,56320
M 18,v[216:219],38
s_barrier
ds_read_b32 v7,v6 offset:1024
M 22,v[212:215],34
R 26,1,0
M 22,v[220:223],38
L 4
M 22,v[228:231],42
R 30,1,1024
M 22,v[236:239],46
buffer_atomic_pk_add_bf16 v14,v9,s[40:43],s64 offen sc1
M 18,v[224:227],42
s_add_u32 s64,s75,8192
R 34,1,2048
M 18,v[232:235],46
L 4
buffer_atomic_pk_add_bf16 v15,v9,s[40:43],s64 offen sc1
M 18,v[240:243],50
s_add_u32 s64,s75,16384
L 3
R 38,1,3072
v_cmp_eq_u32 vcc,s53,v7
M 18,v[248:251],54
buffer_atomic_pk_add_bf16 v16,v9,s[40:43],s64 offen sc1
M 22,v[244:247],50
L 2
s_add_u32 s64,s75,24576
s_cmp_eq_u32 vcc_lo,-1
R 42,1,4096
M 22,v[252:255],54
buffer_atomic_pk_add_bf16 v17,v9,s[40:43],s64 offen sc1
s_cbranch_scc1 Lg4_Fa0_%=
Lg4_Ka0_%=:
Lg4_La0_%=:
s_nop 0
v_mfma_f32_16x16x32_bf16 v[14:17],a[4:7],v[26:29],0
s_add_u32 m0,s59,3072
R 46,1,5120
v_lshlrev_b32 v62,16,v58
M 14,a[12:15],30
buffer_load_dword v8,s[36:39],s58 offen offset:0 sc1 lds
v_mfma_f32_16x16x32_bf16 v[10:13],a[0:3],v[26:29],0
s_sub_u32 s75,s67,s85
R 50,1,6144
v_add_f32 v18,v18,v62
M 10,a[8:11],30
L 3
v_and_b32 v63,0xffff0000,v58
v_add_f32 v19,v19,v63
M 10,a[16:19],34
s_mov_b64 s[40:41],s[86:87]
R 54,1,7168
v_lshlrev_b32 v62,16,v59
M 10,a[24:27],38
s_mov_b32 s42,4194304
v_add_f32 v20,v20,v62
v_and_b32 v63,0xffff0000,v59
M 14,a[20:23],34
s_mov_b32 s43,0x00020000
R 26,1,8192
v_add_f32 v21,v21,v63
M 14,a[28:31],38
L 3
v_lshlrev_b32 v62,16,v60
v_add_f32 v22,v22,v62
M 14,a[36:39],42
s_add_u32 s64,s75,0
R 30,1,9216
v_and_b32 v63,0xffff0000,v60
M 14,a[44:47],46
s_lshl_b32 s62,s54,15
v_add_f32 v23,v23,v63
v_lshlrev_b32 v62,16,v61
M 10,a[32:35],42
s_mov_b32 s66,s65
R 34,1,10240
v_add_f32 v24,v24,v62
M 10,a[40:43],46
L 3
v_and_b32 v63,0xffff0000,v61
v_add_f32 v25,v25,v63
M 10,a[48:51],50
s_add_u32 s65,s47,s62
R 38,1,11264
v_cvt_pk_bf16_f32 v18,v18,v19
M 10,a[56:59],54
buffer_load_dwordx4 v[58:61],v0,s[36:39],s74 offen offset:0 sc1
M 14,a[52:55],50
s_add_u32 s74,s49,s62
R 42,1,12288
v_cvt_pk_bf16_f32 v19,v20,v21
M 14,a[60:63],54
L 3
v_cvt_pk_bf16_f32 v20,v22,v23
v_cvt_pk_bf16_f32 v21,v24,v25
M 14,a[68:71],26
R 46,1,13312
v_lshrrev_b32 v62,8,v0
M 14,a[76:79],30
v_lshl_add_u32 v62,v62,5,v0
v_add_u32 v62,s73,v62
M 10,a[64:67],26
R 50,1,14336
M 10,a[72:75],30
L 3
ds_write_b128 v62,v[18:21] offset:0
M 10,a[80:83],34
R 54,1,15360
ds_read2_b32 v[22:23],v5 offset0:0 offset1:16
M 10,a[88:91],38
ds_read2_b32 v[24:25],v5 offset0:32 offset1:48
M 14,a[84:87],34
R 26,1,16384
M 14,a[92:95],38
L 6
M 14,a[100:103],42
R 30,1,17408
M 14,a[108:111],46
M 10,a[96:99],42
R 34,1,18432
M 10,a[104:107],46
L 5
L 3
M 10,a[112:115],50
R 38,1,19456
M 10,a[120:123],54
buffer_atomic_pk_add_bf16 v22,v9,s[40:43],s64 offen sc1
M 14,a[116:119],50
s_add_u32 s64,s75,8192
R 42,1,20480
M 14,a[124:127],54
L 3
buffer_atomic_pk_add_bf16 v23,v9,s[40:43],s64 offen sc1
M 14,a[132:135],26
s_add_u32 s64,s75,16384
R 46,1,21504
M 14,a[140:143],30
buffer_atomic_pk_add_bf16 v24,v9,s[40:43],s64 offen sc1
M 10,a[128:131],26
s_add_u32 s64,s75,24576
R 50,1,22528
M 10,a[136:139],30
L 3
buffer_atomic_pk_add_bf16 v25,v9,s[40:43],s64 offen sc1
M 10,a[144:147],34
R 54,1,23552
M 10,a[152:155],38
M 14,a[148:151],34
R 26,1,24576
M 14,a[156:159],38
L 3
M 14,a[164:167],42
R 30,1,25600
M 14,a[172:175],46
M 10,a[160:163],42
R 34,1,26624
M 10,a[168:171],46
L 3
M 10,a[176:179],50
s_waitcnt vmcnt(5)
R 38,1,27648
M 10,a[184:187],54
s_barrier
ds_read_b32 v7,v6 offset:3072
M 14,a[180:183],50
R 42,1,28672
M 14,a[188:191],54
L 4
M 14,a[196:199],26
R 46,1,29696
M 14,a[204:207],30
M 10,a[192:195],26
R 50,1,30720
M 10,a[200:203],30
L 4
L 3
v_cmp_eq_u32 vcc,s53,v7
M 10,a[208:211],34
R 54,1,31744
M 10,a[216:219],38
M 14,a[212:215],34
s_cmp_eq_u32 vcc_lo,-1
R 26,1,32768
s_cbranch_scc0 Lg4_Xa0_%=
M 14,a[220:223],38
L 3
s_add_u32 m0,s57,0
s_add_u32 s56,s55,0
M 14,a[228:231],42
s_add_u32 s61,s54,s46
s_cmp_ge_u32 s61,s63
s_cselect_b32 s61,s93,s61
R 30,1,33792
M 14,a[236:239],46
s_mul_i32 s58,s61,2560
D 0,56,0
M 10,a[224:227],42
s_cselect_b32 s46,s88,s46
s_cselect_b32 s63,s89,s63
R 34,1,34816
M 10,a[232:235],46
L 3
s_cselect_b32 s93,s50,s93
s_cselect_b32 s88,8,s88
M 10,a[240:243],50
s_cselect_b32 s89,-1,s89
s_add_u32 s58,s58,109891584
R 38,1,35840
M 10,a[248:251],54
D 0,56,1024
M 14,a[244:247],50
s_mov_b32 s54,s61
R 42,1,36864
M 14,a[252:255],54
L 3
M 14,v[68:71],26
R 46,1,37888
M 14,v[76:79],30
D 0,56,2048
M 10,v[64:67],26
R 50,1,38912
M 10,v[72:75],30
L 3
M 10,v[80:83],34
R 54,1,39936
M 10,v[88:91],38
D 0,56,3072
M 14,v[84:87],34
s_add_u32 m0,s57,4096
s_add_u32 s56,s56,4096
R 26,1,40960
M 14,v[92:95],38
L 3
M 14,v[100:103],42
R 30,1,41984
M 14,v[108:111],46
D 0,56,0
M 10,v[96:99],42
R 34,1,43008
M 10,v[104:107],46
L 3
M 10,v[112:115],50
R 38,1,44032
M 10,v[120:123],54
D 0,56,1024
M 14,v[116:119],50
R 42,1,45056
M 14,v[124:127],54
L 3
M 14,v[132:135],26
R 46,1,46080
M 14,v[140:143],30
D 0,56,2048
M 10,v[128:131],26
s_add_u32 m0,s59,0
s_add_u32 s56,s55,28672
R 50,1,47104
M 10,v[136:139],30
L 3
buffer_load_dword v8,s[36:39],s58 offen offset:0 sc1 lds
M 10,v[144:147],34
s_add_u32 m0,s57,28672
s_mul_i32 s55,s61,114688
R 54,1,48128
M 10,v[152:155],38
s_add_u32 s55,s55,s60
M 14,v[148:151],34
R 26,1,49152
M 14,v[156:159],38
L 3
D 0,56,0
M 14,v[164:167],42
R 30,1,50176
M 14,v[172:175],46
M 10,v[160:163],42
R 34,1,51200
M 10,v[168:171],46
L 3
D 0,56,1024
M 10,v[176:179],50
R 38,1,52224
M 10,v[184:187],54
D 0,56,2048
M 14,v[180:183],50
R 42,1,53248
M 14,v[188:191],54
L 3
M 14,v[196:199],26
R 46,1,54272
M 14,v[204:207],30
D 0,56,3072
M 10,v[192:195],26
s_add_u32 m0,s57,32768
s_add_u32 s56,s56,4096
R 50,1,55296
M 10,v[200:203],30
L 3
s_sub_u32 s57,s84,s57
M 10,v[208:211],34
s_waitcnt vmcnt(4)
R 54,1,56320
M 10,v[216:219],38
s_barrier
ds_read_b32 v7,v6 offset:0
M 14,v[212:215],34
R 26,2,0
M 14,v[220:223],38
L 4
D 0,56,0
M 14,v[228:231],42
R 30,2,1024
M 14,v[236:239],46
D 0,56,1024
M 10,v[224:227],42
R 34,2,2048
M 10,v[232:235],46
L 4
D 0,56,2048
M 10,v[240:243],50
L 3
R 38,2,3072
v_cmp_eq_u32 vcc,s53,v7
M 10,v[248:251],54
M 14,v[244:247],50
L 2
s_cmp_eq_u32 vcc_lo,-1
R 42,2,4096
M 14,v[252:255],54
buffer_store_dword v4,v3,s[36:39],s71 offen offset:0 sc0 sc1
buffer_store_dword v4,v3,s[36:39],s71 offen offset:0 sc0 sc1
buffer_store_dword v4,v3,s[36:39],s71 offen offset:0 sc0 sc1
buffer_store_dword v4,v3,s[36:39],s71 offen offset:0 sc0 sc1
s_cbranch_scc1 Lg4_Fa1_%=
s_branch Lg4_Ka1_%=
Lg4_Xa0_%=:
M 14,a[220:223],38
L 3
s_add_u32 m0,s59,0
M 14,a[228:231],42
R 30,1,33792
M 14,a[236:239],46
buffer_load_dword v8,s[36:39],s58 offen offset:0 sc1 lds
M 10,a[224:227],42
R 34,1,34816
M 10,a[232:235],46
L 3
M 10,a[240:243],50
R 38,1,35840
M 10,a[248:251],54
M 14,a[244:247],50
R 42,1,36864
M 14,a[252:255],54
L 3
M 14,v[68:71],26
R 46,1,37888
M 14,v[76:79],30
M 10,v[64:67],26
R 50,1,38912
M 10,v[72:75],30
L 3
)"
R"(M 10,v[80:83],34
R 54,1,39936
M 10,v[88:91],38
M 14,v[84:87],34
R 26,1,40960
M 14,v[92:95],38
L 3
M 14,v[100:103],42
R 30,1,41984
M 14,v[108:111],46
M 10,v[96:99],42
R 34,1,43008
M 10,v[104:107],46
L 3
M 10,v[112:115],50
R 38,1,44032
M 10,v[120:123],54
M 14,v[116:119],50
R 42,1,45056
M 14,v[124:127],54
L 3
M 14,v[132:135],26
R 46,1,46080
M 14,v[140:143],30
M 10,v[128:131],26
R 50,1,47104
M 10,v[136:139],30
L 3
M 10,v[144:147],34
R 54,1,48128
M 10,v[152:155],38
M 14,v[148:151],34
R 26,1,49152
M 14,v[156:159],38
L 3
M 14,v[164:167],42
R 30,1,50176
M 14,v[172:175],46
M 10,v[160:163],42
R 34,1,51200
M 10,v[168:171],46
L 3
M 10,v[176:179],50
R 38,1,52224
M 10,v[184:187],54
M 14,v[180:183],50
R 42,1,53248
M 14,v[188:191],54
L 3
M 14,v[196:199],26
R 46,1,54272
M 14,v[204:207],30
M 10,v[192:195],26
R 50,1,55296
M 10,v[200:203],30
L 3
M 10,v[208:211],34
s_waitcnt vmcnt(0)
R 54,1,56320
M 10,v[216:219],38
s_barrier
ds_read_b32 v7,v6 offset:0
M 14,v[212:215],34
M 14,v[220:223],38
L 3
M 14,v[228:231],42
M 14,v[236:239],46
M 10,v[224:227],42
M 10,v[232:235],46
L 1
L 0
M 10,v[240:243],50
v_cmp_eq_u32 vcc,s53,v7
M 10,v[248:251],54
M 14,v[244:247],50
M 14,v[252:255],54
Lg4_pa0_%=:
s_mov_b32 s80,1
s_cmp_eq_u32 vcc_lo,-1
s_cbranch_scc0 Lg4_xna0_%=
s_add_u32 m0,s57,0
s_add_u32 s56,s55,0
D 0,56,0
s_sub_u32 s75,s66,s85
v_lshlrev_b32 v62,16,v58
D 0,56,1024
v_add_f32 v10,v10,v62
D 0,56,2048
v_and_b32 v63,0xffff0000,v58
D 0,56,3072
v_add_f32 v11,v11,v63
s_add_u32 m0,s57,4096
s_add_u32 s56,s56,4096
D 0,56,0
v_lshlrev_b32 v62,16,v59
D 0,56,1024
v_add_f32 v12,v12,v62
D 0,56,2048
v_and_b32 v63,0xffff0000,v59
s_add_u32 s61,s54,s46
s_cmp_ge_u32 s61,s63
s_cselect_b32 s61,s93,s61
s_cselect_b32 s46,s88,s46
s_cselect_b32 s63,s89,s63
s_cselect_b32 s93,s50,s93
s_cselect_b32 s88,8,s88
s_cselect_b32 s89,-1,s89
s_mul_i32 s58,s61,2560
s_add_u32 s58,s58,109891584
s_add_u32 m0,s59,2048
s_nop 0
buffer_load_dword v8,s[36:39],s58 offen offset:0 sc1 lds
v_add_f32 v13,v13,v63
s_add_u32 m0,s57,28672
s_add_u32 s56,s55,28672
D 0,56,0
v_lshlrev_b32 v62,16,v60
D 0,56,1024
v_add_f32 v14,v14,v62
D 0,56,2048
v_and_b32 v63,0xffff0000,v60
D 0,56,3072
v_add_f32 v15,v15,v63
s_add_u32 m0,s57,32768
s_add_u32 s56,s56,4096
D 0,56,0
v_lshlrev_b32 v62,16,v61
D 0,56,1024
v_add_f32 v16,v16,v62
D 0,56,2048
v_and_b32 v63,0xffff0000,v61
s_mul_i32 s55,s61,114688
s_add_u32 s55,s55,s60
s_sub_u32 s57,s84,s57
s_mov_b32 s54,s61
v_add_f32 v17,v17,v63
v_cvt_pk_bf16_f32 v10,v10,v11
v_cvt_pk_bf16_f32 v11,v12,v13
v_cvt_pk_bf16_f32 v12,v14,v15
v_cvt_pk_bf16_f32 v13,v16,v17
v_lshrrev_b32 v62,8,v0
v_lshl_add_u32 v62,v62,5,v0
v_add_u32 v62,s73,v62
ds_write_b128 v62,v[10:13] offset:0
ds_read2_b32 v[14:15],v5 offset0:0 offset1:16
ds_read2_b32 v[16:17],v5 offset0:32 offset1:48
s_mov_b64 s[40:41],s[86:87]
s_mov_b32 s42,4194304
s_mov_b32 s43,0x00020000
L 0
s_add_u32 s64,s75,0
buffer_atomic_pk_add_bf16 v14,v9,s[40:43],s64 offen sc1
s_add_u32 s64,s75,8192
buffer_atomic_pk_add_bf16 v15,v9,s[40:43],s64 offen sc1
s_add_u32 s64,s75,16384
buffer_atomic_pk_add_bf16 v16,v9,s[40:43],s64 offen sc1
s_add_u32 s64,s75,24576
buffer_atomic_pk_add_bf16 v17,v9,s[40:43],s64 offen sc1
s_mov_b32 s72,0x7fff0000
s_mov_b32 s69,0x7fff0000
s_mov_b32 s66,0x7fff0000
s_mov_b32 s76,0x7fff0000
s_waitcnt vmcnt(11)
s_barrier
s_branch Lg4_st1_%=
Lg4_xna0_%=:
s_cmp_ge_u32 s54,256
s_cbranch_scc1 Lg4_fina0_%=
s_add_u32 m0,s59,4096
s_nop 0
buffer_load_dword v8,s[36:39],s58 offen offset:0 sc1 lds
buffer_store_dword v4,v3,s[36:39],s71 offen offset:0 sc0 sc1
s_add_u32 m0,s59,5120
s_nop 0
buffer_load_dword v8,s[36:39],s58 offen offset:0 sc1 lds
buffer_store_dword v4,v3,s[36:39],s71 offen offset:0 sc0 sc1
s_mov_b32 s83,0
s_sub_u32 s75,s66,s85
v_lshlrev_b32 v62,16,v58
v_add_f32 v10,v10,v62
v_and_b32 v63,0xffff0000,v58
v_add_f32 v11,v11,v63
v_lshlrev_b32 v62,16,v59
v_add_f32 v12,v12,v62
v_and_b32 v63,0xffff0000,v59
v_add_f32 v13,v13,v63
v_lshlrev_b32 v62,16,v60
v_add_f32 v14,v14,v62
v_and_b32 v63,0xffff0000,v60
v_add_f32 v15,v15,v63
v_lshlrev_b32 v62,16,v61
v_add_f32 v16,v16,v62
v_and_b32 v63,0xffff0000,v61
v_add_f32 v17,v17,v63
v_cvt_pk_bf16_f32 v10,v10,v11
v_cvt_pk_bf16_f32 v11,v12,v13
v_cvt_pk_bf16_f32 v12,v14,v15
v_cvt_pk_bf16_f32 v13,v16,v17
v_lshrrev_b32 v62,8,v0
v_lshl_add_u32 v62,v62,5,v0
v_add_u32 v62,s73,v62
ds_write_b128 v62,v[10:13] offset:0
ds_read2_b32 v[14:15],v5 offset0:0 offset1:16
ds_read2_b32 v[16:17],v5 offset0:32 offset1:48
s_mov_b64 s[40:41],s[86:87]
s_mov_b32 s42,4194304
s_mov_b32 s43,0x00020000
L 0
s_add_u32 s64,s75,0
buffer_atomic_pk_add_bf16 v14,v9,s[40:43],s64 offen sc1
s_add_u32 s64,s75,8192
buffer_atomic_pk_add_bf16 v15,v9,s[40:43],s64 offen sc1
s_add_u32 s64,s75,16384
buffer_atomic_pk_add_bf16 v16,v9,s[40:43],s64 offen sc1
s_add_u32 s64,s75,24576
buffer_atomic_pk_add_bf16 v17,v9,s[40:43],s64 offen sc1
s_mov_b32 s72,0x7fff0000
s_mov_b32 s69,0x7fff0000
s_mov_b32 s66,0x7fff0000
s_mov_b32 s76,0x7fff0000
s_branch Lg4_poll_%=
Lg4_fina0_%=:
s_sub_u32 s75,s66,s85
v_lshlrev_b32 v62,16,v58
v_add_f32 v10,v10,v62
v_and_b32 v63,0xffff0000,v58
v_add_f32 v11,v11,v63
v_lshlrev_b32 v62,16,v59
v_add_f32 v12,v12,v62
v_and_b32 v63,0xffff0000,v59
v_add_f32 v13,v13,v63
v_lshlrev_b32 v62,16,v60
v_add_f32 v14,v14,v62
v_and_b32 v63,0xffff0000,v60
v_add_f32 v15,v15,v63
v_lshlrev_b32 v62,16,v61
v_add_f32 v16,v16,v62
v_and_b32 v63,0xffff0000,v61
v_add_f32 v17,v17,v63
v_cvt_pk_bf16_f32 v10,v10,v11
v_cvt_pk_bf16_f32 v11,v12,v13
v_cvt_pk_bf16_f32 v12,v14,v15
v_cvt_pk_bf16_f32 v13,v16,v17
v_lshrrev_b32 v62,8,v0
v_lshl_add_u32 v62,v62,5,v0
v_add_u32 v62,s73,v62
ds_write_b128 v62,v[10:13] offset:0
ds_read2_b32 v[14:15],v5 offset0:0 offset1:16
ds_read2_b32 v[16:17],v5 offset0:32 offset1:48
s_mov_b64 s[40:41],s[86:87]
s_mov_b32 s42,4194304
s_mov_b32 s43,0x00020000
L 0
s_add_u32 s64,s75,0
buffer_atomic_pk_add_bf16 v14,v9,s[40:43],s64 offen sc1
s_add_u32 s64,s75,8192
buffer_atomic_pk_add_bf16 v15,v9,s[40:43],s64 offen sc1
s_add_u32 s64,s75,16384
buffer_atomic_pk_add_bf16 v16,v9,s[40:43],s64 offen sc1
s_add_u32 s64,s75,24576
buffer_atomic_pk_add_bf16 v17,v9,s[40:43],s64 offen sc1
s_mov_b32 s72,0x7fff0000
s_mov_b32 s69,0x7fff0000
s_mov_b32 s66,0x7fff0000
s_mov_b32 s76,0x7fff0000
s_branch Lg4_done_%=
Lg4_Ka1_%=:
Lg4_La1_%=:
s_nop 0
v_mfma_f32_16x16x32_bf16 v[22:25],a[4:7],v[26:29],0
s_add_u32 m0,s59,3072
R 46,2,5120
v_lshlrev_b32 v62,16,v58
M 22,a[12:15],30
buffer_load_dword v8,s[36:39],s58 offen offset:0 sc1 lds
v_mfma_f32_16x16x32_bf16 v[18:21],a[0:3],v[26:29],0
s_sub_u32 s75,s66,s85
R 50,2,6144
v_add_f32 v10,v10,v62
M 18,a[8:11],30
L 3
v_and_b32 v63,0xffff0000,v58
v_add_f32 v11,v11,v63
M 18,a[16:19],34
s_mov_b64 s[40:41],s[86:87]
R 54,2,7168
v_lshlrev_b32 v62,16,v59
M 18,a[24:27],38
s_mov_b32 s42,4194304
v_add_f32 v12,v12,v62
v_and_b32 v63,0xffff0000,v59
M 22,a[20:23],34
s_mov_b32 s43,0x00020000
R 26,2,8192
v_add_f32 v13,v13,v63
M 22,a[28:31],38
L 3
v_lshlrev_b32 v62,16,v60
v_add_f32 v14,v14,v62
M 22,a[36:39],42
s_add_u32 s64,s75,0
R 30,2,9216
v_and_b32 v63,0xffff0000,v60
M 22,a[44:47],46
s_lshl_b32 s62,s54,15
v_add_f32 v15,v15,v63
v_lshlrev_b32 v62,16,v61
M 18,a[32:35],42
s_mov_b32 s67,s65
R 34,2,10240
v_add_f32 v16,v16,v62
M 18,a[40:43],46
L 3
v_and_b32 v63,0xffff0000,v61
v_add_f32 v17,v17,v63
M 18,a[48:51],50
s_add_u32 s65,s47,s62
R 38,2,11264
v_cvt_pk_bf16_f32 v10,v10,v11
M 18,a[56:59],54
buffer_load_dwordx4 v[58:61],v0,s[36:39],s74 offen offset:0 sc1
M 22,a[52:55],50
s_add_u32 s74,s49,s62
R 42,2,12288
v_cvt_pk_bf16_f32 v11,v12,v13
M 22,a[60:63],54
L 3
v_cvt_pk_bf16_f32 v12,v14,v15
v_cvt_pk_bf16_f32 v13,v16,v17
M 22,a[68:71],26
R 46,2,13312
v_lshrrev_b32 v62,8,v0
M 22,a[76:79],30
v_lshl_add_u32 v62,v62,5,v0
v_add_u32 v62,s73,v62
M 18,a[64:67],26
R 50,2,14336
M 18,a[72:75],30
L 3
ds_write_b128 v62,v[10:13] offset:0
M 18,a[80:83],34
R 54,2,15360
ds_read2_b32 v[14:15],v5 offset0:0 offset1:16
M 18,a[88:91],38
ds_read2_b32 v[16:17],v5 offset0:32 offset1:48
M 22,a[84:87],34
R 26,2,16384
M 22,a[92:95],38
L 6
M 22,a[100:103],42
R 30,2,17408
M 22,a[108:111],46
M 18,a[96:99],42
R 34,2,18432
M 18,a[104:107],46
L 5
L 3
M 18,a[112:115],50
R 38,2,19456
M 18,a[120:123],54
buffer_atomic_pk_add_bf16 v14,v9,s[40:43],s64 offen sc1
M 22,a[116:119],50
s_add_u32 s64,s75,8192
R 42,2,20480
M 22,a[124:127],54
L 3
buffer_atomic_pk_add_bf16 v15,v9,s[40:43],s64 offen sc1
M 22,a[132:135],26
s_add_u32 s64,s75,16384
R 46,2,21504
M 22,a[140:143],30
buffer_atomic_pk_add_bf16 v16,v9,s[40:43],s64 offen sc1
M 18,a[128:131],26
s_add_u32 s64,s75,24576
R 50,2,22528
M 18,a[136:139],30
L 3
buffer_atomic_pk_add_bf16 v17,v9,s[40:43],s64 offen sc1
M 18,a[144:147],34
R 54,2,23552
M 18,a[152:155],38
M 22,a[148:151],34
R 26,2,24576
M 22,a[156:159],38
L 3
M 22,a[164:167],42
R 30,2,25600
M 22,a[172:175],46
M 18,a[160:163],42
R 34,2,26624
M 18,a[168:171],46
L 3
M 18,a[176:179],50
s_waitcnt vmcnt(5)
R 38,2,27648
M 18,a[184:187],54
s_barrier
ds_read_b32 v7,v6 offset:3072
M 22,a[180:183],50
R 42,2,28672
M 22,a[188:191],54
L 4
M 22,a[196:199],26
R 46,2,29696
M 22,a[204:207],30
M 18,a[192:195],26
R 50,2,30720
M 18,a[200:203],30
L 4
L 3
v_cmp_eq_u32 vcc,s53,v7
M 18,a[208:211],34
R 54,2,31744
M 18,a[216:219],38
M 22,a[212:215],34
s_cmp_eq_u32 vcc_lo,-1
R 26,2,32768
s_cbranch_scc0 Lg4_Xa1_%=
M 22,a[220:223],38
L 3
s_add_u32 m0,s57,0
s_add_u32 s56,s55,0
M 22,a[228:231],42
s_add_u32 s61,s54,s46
s_cmp_ge_u32 s61,s63
s_cselect_b32 s61,s93,s61
R 30,2,33792
M 22,a[236:239],46
s_mul_i32 s58,s61,2560
D 0,56,0
M 18,a[224:227],42
s_cselect_b32 s46,s88,s46
s_cselect_b32 s63,s89,s63
R 34,2,34816
M 18,a[232:235],46
L 3
s_cselect_b32 s93,s50,s93
s_cselect_b32 s88,8,s88
M 18,a[240:243],50
s_cselect_b32 s89,-1,s89
s_add_u32 s58,s58,109891584
R 38,2,35840
M 18,a[248:251],54
D 0,56,1024
M 22,a[244:247],50
s_mov_b32 s54,s61
R 42,2,36864
M 22,a[252:255],54
L 3
M 22,v[68:71],26
R 46,2,37888
M 22,v[76:79],30
D 0,56,2048
M 18,v[64:67],26
R 50,2,38912
M 18,v[72:75],30
L 3
M 18,v[80:83],34
R 54,2,39936
M 18,v[88:91],38
D 0,56,3072
M 22,v[84:87],34
s_add_u32 m0,s57,4096
s_add_u32 s56,s56,4096
R 26,2,40960
M 22,v[92:95],38
L 3
M 22,v[100:103],42
R 30,2,41984
M 22,v[108:111],46
D 0,56,0
M 18,v[96:99],42
R 34,2,43008
M 18,v[104:107],46
L 3
M 18,v[112:115],50
R 38,2,44032
M 18,v[120:123],54
D 0,56,1024
M 22,v[116:119],50
R 42,2,45056
M 22,v[124:127],54
L 3
M 22,v[132:135],26
R 46,2,46080
M 22,v[140:143],30
D 0,56,2048
M 18,v[128:131],26
s_add_u32 m0,s59,1024
s_add_u32 s56,s55,28672
R 50,2,47104
M 18,v[136:139],30
L 3
buffer_load_dword v8,s[36:39],s58 offen offset:0 sc1 lds
M 18,v[144:147],34
s_add_u32 m0,s57,28672
s_mul_i32 s55,s61,114688
R 54,2,48128
M 18,v[152:155],38
s_add_u32 s55,s55,s60
)"
R"(M 22,v[148:151],34
R 26,2,49152
M 22,v[156:159],38
L 3
D 0,56,0
M 22,v[164:167],42
R 30,2,50176
M 22,v[172:175],46
M 18,v[160:163],42
R 34,2,51200
M 18,v[168:171],46
L 3
D 0,56,1024
M 18,v[176:179],50
R 38,2,52224
M 18,v[184:187],54
D 0,56,2048
M 22,v[180:183],50
R 42,2,53248
M 22,v[188:191],54
L 3
M 22,v[196:199],26
R 46,2,54272
M 22,v[204:207],30
D 0,56,3072
M 18,v[192:195],26
s_add_u32 m0,s57,32768
s_add_u32 s56,s56,4096
R 50,2,55296
M 18,v[200:203],30
L 3
s_sub_u32 s57,s84,s57
M 18,v[208:211],34
s_waitcnt vmcnt(4)
R 54,2,56320
M 18,v[216:219],38
s_barrier
ds_read_b32 v7,v6 offset:1024
M 22,v[212:215],34
R 26,1,0
M 22,v[220:223],38
L 4
D 0,56,0
M 22,v[228:231],42
R 30,1,1024
M 22,v[236:239],46
D 0,56,1024
M 18,v[224:227],42
R 34,1,2048
M 18,v[232:235],46
L 4
D 0,56,2048
M 18,v[240:243],50
L 3
R 38,1,3072
v_cmp_eq_u32 vcc,s53,v7
M 18,v[248:251],54
M 22,v[244:247],50
L 2
s_cmp_eq_u32 vcc_lo,-1
R 42,1,4096
M 22,v[252:255],54
buffer_store_dword v4,v3,s[36:39],s71 offen offset:0 sc0 sc1
buffer_store_dword v4,v3,s[36:39],s71 offen offset:0 sc0 sc1
buffer_store_dword v4,v3,s[36:39],s71 offen offset:0 sc0 sc1
buffer_store_dword v4,v3,s[36:39],s71 offen offset:0 sc0 sc1
s_cbranch_scc1 Lg4_Fa0_%=
s_branch Lg4_Ka0_%=
Lg4_Xa1_%=:
M 22,a[220:223],38
L 3
s_add_u32 m0,s59,1024
M 22,a[228:231],42
R 30,2,33792
M 22,a[236:239],46
buffer_load_dword v8,s[36:39],s58 offen offset:0 sc1 lds
M 18,a[224:227],42
R 34,2,34816
M 18,a[232:235],46
L 3
M 18,a[240:243],50
R 38,2,35840
M 18,a[248:251],54
M 22,a[244:247],50
R 42,2,36864
M 22,a[252:255],54
L 3
M 22,v[68:71],26
R 46,2,37888
M 22,v[76:79],30
M 18,v[64:67],26
R 50,2,38912
M 18,v[72:75],30
L 3
M 18,v[80:83],34
R 54,2,39936
M 18,v[88:91],38
M 22,v[84:87],34
R 26,2,40960
M 22,v[92:95],38
L 3
M 22,v[100:103],42
R 30,2,41984
M 22,v[108:111],46
M 18,v[96:99],42
R 34,2,43008
M 18,v[104:107],46
L 3
M 18,v[112:115],50
R 38,2,44032
M 18,v[120:123],54
M 22,v[116:119],50
R 42,2,45056
M 22,v[124:127],54
L 3
M 22,v[132:135],26
R 46,2,46080
M 22,v[140:143],30
M 18,v[128:131],26
R 50,2,47104
M 18,v[136:139],30
L 3
M 18,v[144:147],34
R 54,2,48128
M 18,v[152:155],38
M 22,v[148:151],34
R 26,2,49152
M 22,v[156:159],38
L 3
M 22,v[164:167],42
R 30,2,50176
M 22,v[172:175],46
M 18,v[160:163],42
R 34,2,51200
M 18,v[168:171],46
L 3
M 18,v[176:179],50
R 38,2,52224
M 18,v[184:187],54
M 22,v[180:183],50
R 42,2,53248
M 22,v[188:191],54
L 3
M 22,v[196:199],26
R 46,2,54272
M 22,v[204:207],30
M 18,v[192:195],26
R 50,2,55296
M 18,v[200:203],30
L 3
M 18,v[208:211],34
s_waitcnt vmcnt(0)
R 54,2,56320
M 18,v[216:219],38
s_barrier
ds_read_b32 v7,v6 offset:1024
M 22,v[212:215],34
M 22,v[220:223],38
L 3
M 22,v[228:231],42
M 22,v[236:239],46
M 18,v[224:227],42
M 18,v[232:235],46
L 1
L 0
M 18,v[240:243],50
v_cmp_eq_u32 vcc,s53,v7
M 18,v[248:251],54
M 22,v[244:247],50
M 22,v[252:255],54
Lg4_pa1_%=:
s_mov_b32 s80,0
s_cmp_eq_u32 vcc_lo,-1
s_cbranch_scc0 Lg4_xna1_%=
s_add_u32 m0,s57,0
s_add_u32 s56,s55,0
D 0,56,0
s_sub_u32 s75,s67,s85
v_lshlrev_b32 v62,16,v58
D 0,56,1024
v_add_f32 v18,v18,v62
D 0,56,2048
v_and_b32 v63,0xffff0000,v58
D 0,56,3072
v_add_f32 v19,v19,v63
s_add_u32 m0,s57,4096
s_add_u32 s56,s56,4096
D 0,56,0
v_lshlrev_b32 v62,16,v59
D 0,56,1024
v_add_f32 v20,v20,v62
D 0,56,2048
v_and_b32 v63,0xffff0000,v59
s_add_u32 s61,s54,s46
s_cmp_ge_u32 s61,s63
s_cselect_b32 s61,s93,s61
s_cselect_b32 s46,s88,s46
s_cselect_b32 s63,s89,s63
s_cselect_b32 s93,s50,s93
s_cselect_b32 s88,8,s88
s_cselect_b32 s89,-1,s89
s_mul_i32 s58,s61,2560
s_add_u32 s58,s58,109891584
s_add_u32 m0,s59,2048
s_nop 0
buffer_load_dword v8,s[36:39],s58 offen offset:0 sc1 lds
v_add_f32 v21,v21,v63
s_add_u32 m0,s57,28672
s_add_u32 s56,s55,28672
D 0,56,0
v_lshlrev_b32 v62,16,v60
D 0,56,1024
v_add_f32 v22,v22,v62
D 0,56,2048
v_and_b32 v63,0xffff0000,v60
D 0,56,3072
v_add_f32 v23,v23,v63
s_add_u32 m0,s57,32768
s_add_u32 s56,s56,4096
D 0,56,0
v_lshlrev_b32 v62,16,v61
D 0,56,1024
v_add_f32 v24,v24,v62
D 0,56,2048
v_and_b32 v63,0xffff0000,v61
s_mul_i32 s55,s61,114688
s_add_u32 s55,s55,s60
s_sub_u32 s57,s84,s57
s_mov_b32 s54,s61
v_add_f32 v25,v25,v63
v_cvt_pk_bf16_f32 v18,v18,v19
v_cvt_pk_bf16_f32 v19,v20,v21
v_cvt_pk_bf16_f32 v20,v22,v23
v_cvt_pk_bf16_f32 v21,v24,v25
v_lshrrev_b32 v62,8,v0
v_lshl_add_u32 v62,v62,5,v0
v_add_u32 v62,s73,v62
ds_write_b128 v62,v[18:21] offset:0
ds_read2_b32 v[22:23],v5 offset0:0 offset1:16
ds_read2_b32 v[24:25],v5 offset0:32 offset1:48
s_mov_b64 s[40:41],s[86:87]
s_mov_b32 s42,4194304
s_mov_b32 s43,0x00020000
L 0
s_add_u32 s64,s75,0
buffer_atomic_pk_add_bf16 v22,v9,s[40:43],s64 offen sc1
s_add_u32 s64,s75,8192
buffer_atomic_pk_add_bf16 v23,v9,s[40:43],s64 offen sc1
s_add_u32 s64,s75,16384
buffer_atomic_pk_add_bf16 v24,v9,s[40:43],s64 offen sc1
s_add_u32 s64,s75,24576
buffer_atomic_pk_add_bf16 v25,v9,s[40:43],s64 offen sc1
s_mov_b32 s72,0x7fff0000
s_mov_b32 s69,0x7fff0000
s_mov_b32 s67,0x7fff0000
s_mov_b32 s77,0x7fff0000
s_waitcnt vmcnt(11)
s_barrier
s_branch Lg4_st0_%=
Lg4_xna1_%=:
s_cmp_ge_u32 s54,256
s_cbranch_scc1 Lg4_fina1_%=
s_add_u32 m0,s59,4096
s_nop 0
buffer_load_dword v8,s[36:39],s58 offen offset:0 sc1 lds
buffer_store_dword v4,v3,s[36:39],s71 offen offset:0 sc0 sc1
s_add_u32 m0,s59,5120
s_nop 0
buffer_load_dword v8,s[36:39],s58 offen offset:0 sc1 lds
buffer_store_dword v4,v3,s[36:39],s71 offen offset:0 sc0 sc1
s_mov_b32 s83,0
s_sub_u32 s75,s67,s85
v_lshlrev_b32 v62,16,v58
v_add_f32 v18,v18,v62
v_and_b32 v63,0xffff0000,v58
v_add_f32 v19,v19,v63
v_lshlrev_b32 v62,16,v59
v_add_f32 v20,v20,v62
v_and_b32 v63,0xffff0000,v59
v_add_f32 v21,v21,v63
v_lshlrev_b32 v62,16,v60
v_add_f32 v22,v22,v62
v_and_b32 v63,0xffff0000,v60
v_add_f32 v23,v23,v63
v_lshlrev_b32 v62,16,v61
v_add_f32 v24,v24,v62
v_and_b32 v63,0xffff0000,v61
v_add_f32 v25,v25,v63
v_cvt_pk_bf16_f32 v18,v18,v19
v_cvt_pk_bf16_f32 v19,v20,v21
v_cvt_pk_bf16_f32 v20,v22,v23
v_cvt_pk_bf16_f32 v21,v24,v25
v_lshrrev_b32 v62,8,v0
v_lshl_add_u32 v62,v62,5,v0
v_add_u32 v62,s73,v62
ds_write_b128 v62,v[18:21] offset:0
ds_read2_b32 v[22:23],v5 offset0:0 offset1:16
ds_read2_b32 v[24:25],v5 offset0:32 offset1:48
s_mov_b64 s[40:41],s[86:87]
s_mov_b32 s42,4194304
s_mov_b32 s43,0x00020000
L 0
s_add_u32 s64,s75,0
buffer_atomic_pk_add_bf16 v22,v9,s[40:43],s64 offen sc1
s_add_u32 s64,s75,8192
buffer_atomic_pk_add_bf16 v23,v9,s[40:43],s64 offen sc1
s_add_u32 s64,s75,16384
buffer_atomic_pk_add_bf16 v24,v9,s[40:43],s64 offen sc1
s_add_u32 s64,s75,24576
buffer_atomic_pk_add_bf16 v25,v9,s[40:43],s64 offen sc1
s_mov_b32 s72,0x7fff0000
s_mov_b32 s69,0x7fff0000
s_mov_b32 s67,0x7fff0000
s_mov_b32 s77,0x7fff0000
s_branch Lg4_poll_%=
Lg4_fina1_%=:
s_sub_u32 s75,s67,s85
v_lshlrev_b32 v62,16,v58
v_add_f32 v18,v18,v62
v_and_b32 v63,0xffff0000,v58
v_add_f32 v19,v19,v63
v_lshlrev_b32 v62,16,v59
v_add_f32 v20,v20,v62
v_and_b32 v63,0xffff0000,v59
v_add_f32 v21,v21,v63
v_lshlrev_b32 v62,16,v60
v_add_f32 v22,v22,v62
v_and_b32 v63,0xffff0000,v60
v_add_f32 v23,v23,v63
v_lshlrev_b32 v62,16,v61
v_add_f32 v24,v24,v62
v_and_b32 v63,0xffff0000,v61
v_add_f32 v25,v25,v63
v_cvt_pk_bf16_f32 v18,v18,v19
v_cvt_pk_bf16_f32 v19,v20,v21
v_cvt_pk_bf16_f32 v20,v22,v23
v_cvt_pk_bf16_f32 v21,v24,v25
v_lshrrev_b32 v62,8,v0
v_lshl_add_u32 v62,v62,5,v0
v_add_u32 v62,s73,v62
ds_write_b128 v62,v[18:21] offset:0
ds_read2_b32 v[22:23],v5 offset0:0 offset1:16
ds_read2_b32 v[24:25],v5 offset0:32 offset1:48
s_mov_b64 s[40:41],s[86:87]
s_mov_b32 s42,4194304
s_mov_b32 s43,0x00020000
L 0
s_add_u32 s64,s75,0
buffer_atomic_pk_add_bf16 v22,v9,s[40:43],s64 offen sc1
s_add_u32 s64,s75,8192
buffer_atomic_pk_add_bf16 v23,v9,s[40:43],s64 offen sc1
s_add_u32 s64,s75,16384
buffer_atomic_pk_add_bf16 v24,v9,s[40:43],s64 offen sc1
s_add_u32 s64,s75,24576
buffer_atomic_pk_add_bf16 v25,v9,s[40:43],s64 offen sc1
s_mov_b32 s72,0x7fff0000
s_mov_b32 s69,0x7fff0000
s_mov_b32 s67,0x7fff0000
s_mov_b32 s77,0x7fff0000
s_branch Lg4_done_%=
Lg4_done_%=:
s_nop 0
s_barrier
buffer_store_dword v4,v3,s[36:39],s70 offen offset:0 sc0 sc1
s_waitcnt vmcnt(1)
s_barrier
buffer_store_dword v4,v3,s[36:39],s72 offen offset:0 sc0 sc1
s_waitcnt vmcnt(0) lgkmcnt(0)
s_barrier
s_endpgm
Lcode_g4_e:
.purgem M
.purgem R
.purgem T
.purgem D
.purgem L
)"
  :: "s"(ws), "s"(__builtin_amdgcn_readfirstlane(sWave)), "s"(__builtin_amdgcn_readfirstlane(sG0)), "s"(__builtin_amdgcn_readfirstlane(sStep)), "s"(__builtin_amdgcn_readfirstlane(sOutOff)), "s"(__builtin_amdgcn_readfirstlane(sFoutOff)), "s"(__builtin_amdgcn_readfirstlane(sAuxOff)), "s"(__builtin_amdgcn_readfirstlane(sWw)), "s"(__builtin_amdgcn_readfirstlane(sBuf)), "s"(__builtin_amdgcn_readfirstlane(sRk)), "s"(__builtin_amdgcn_readfirstlane(sExp)), "v"(vL16), "v"(vFlag), "v"(vSnapOff), "v"(vSnapRd), "v"(vWd0), "v"(vWd1), "v"(vWr0), "v"(vWr1), "v"(vSp), "v"(vSn), "s"(w4), "s"(out) : "v255", "v0", "v1", "v2", "v3", "v4", "v5", "v6", "v7", "v8", "v9", "v10", "v11", "v12", "v13", "v14", "v15", "v16", "v17", "v18", "v19", "v20", "v21", "v22", "v23", "v24", "v25", "v26", "v27", "v28", "v29", "v30", "v31", "v32", "v33", "v34", "v35", "v36", "v37", "v38", "v39", "v40", "v41", "v42", "v43", "v44", "v45", "v46", "v47", "v48", "v49", "v50", "v51", "v52", "v53", "v54", "v55", "v56", "v57", "v58", "v59", "v60", "v61", "v62", "v63", "v64", "v65", "v66", "v67", "v68", "v69", "v70", "v71", "v72", "v73", "v74", "v75", "v76", "v77", "v78", "v79", "v80", "v81", "v82", "v83", "v84", "v85", "v86", "v87", "v88", "v89", "v90", "v91", "v92", "v93", "v94", "v95", "v96", "v97", "v98", "v99", "v100", "v101", "v102", "v103", "v104", "v105", "v106", "v107", "v108", "v109", "v110", "v111", "v112", "v113", "v114", "v115", "v116", "v117", "v118", "v119", "v120", "v121", "v122", "v123", "v124", "v125", "v126", "v127", "v128", "v129", "v130", "v131", "v132", "v133", "v134", "v135", "v136", "v137", "v138", "v139", "v140", "v141", "v142", "v143", "v144", "v145", "v146", "v147", "v148", "v149", "v150", "v151", "v152", "v153", "v154", "v155", "v156", "v157", "v158", "v159", "v160", "v161", "v162", "v163", "v164", "v165", "v166", "v167", "v168", "v169", "v170", "v171", "v172", "v173", "v174", "v175", "v176", "v177", "v178", "v179", "v180", "v181", "v182", "v183", "v184", "v185", "v186", "v187", "v188", "v189", "v190", "v191", "v192", "v193", "v194", "v195", "v196", "v197", "v198", "v199", "v200", "v201", "v202", "v203", "v204", "v205", "v206", "v207", "v208", "v209", "v210", "v211", "v212", "v213", "v214", "v215", "v216", "v217", "v218", "v219", "v220", "v221", "v222", "v223", "v224", "v225", "v226", "v227", "v228", "v229", "v230", "v231", "a0", "a1", "a2", "a3", "a4", "a5", "a6", "a7", "a8", "a9", "a10", "a11", "a12", "a13", "a14", "a15", "a16", "a17", "a18", "a19", "a20", "a21", "a22", "a23", "a24", "a25", "a26", "a27", "a28", "a29", "a30", "a31", "a32", "a33", "a34", "a35", "a36", "a37", "a38", "a39", "a40", "a41", "a42", "a43", "a44", "a45", "a46", "a47", "a48", "a49", "a50", "a51", "a52", "a53", "a54", "a55", "a56", "a57", "a58", "a59", "a60", "a61", "a62", "a63", "a64", "a65", "a66", "a67", "a68", "a69", "a70", "a71", "a72", "a73", "a74", "a75", "a76", "a77", "a78", "a79", "a80", "a81", "a82", "a83", "a84", "a85", "a86", "a87", "a88", "a89", "a90", "a91", "a92", "a93", "a94", "a95", "a96", "a97", "a98", "a99", "a100", "a101", "a102", "a103", "a104", "a105", "a106", "a107", "a108", "a109", "a110", "a111", "a112", "a113", "a114", "a115", "a116", "a117", "a118", "a119", "a120", "a121", "a122", "a123", "a124", "a125", "a126", "a127", "a128", "a129", "a130", "a131", "a132", "a133", "a134", "a135", "a136", "a137", "a138", "a139", "a140", "a141", "a142", "a143", "a144", "a145", "a146", "a147", "a148", "a149", "a150", "a151", "a152", "a153", "a154", "a155", "a156", "a157", "a158", "a159", "a160", "a161", "a162", "a163", "a164", "a165", "a166", "a167", "a168", "a169", "a170", "a171", "a172", "a173", "a174", "a175", "a176", "a177", "a178", "a179", "a180", "a181", "a182", "a183", "a184", "a185", "a186", "a187", "a188", "a189", "a190", "a191", "a192", "a193", "a194", "a195", "a196", "a197", "a198", "a199", "a200", "a201", "a202", "a203", "a204", "a205", "a206", "a207", "a208", "a209", "a210", "a211", "a212", "a213", "a214", "a215", "a216", "a217", "a218", "a219", "a220", "a221", "a222", "a223", "a224", "a225", "a226", "a227", "a228", "a229", "a230", "a231", "a232", "a233", "a234", "a235", "a236", "a237", "a238", "a239", "a240", "a241", "a242", "a243", "a244", "a245", "a246", "a247", "a248", "a249", "a250", "a251", "a252", "a253", "a254", "a255", "s36", "s37", "s38", "s39", "s40", "s41", "s42", "s43", "s44", "s45", "s46", "s47", "s48", "s49", "s50", "s51", "s52", "s53", "s54", "s55", "s56", "s57", "s58", "s59", "s60", "s61", "s62", "s63", "s64", "s65", "s66", "s67", "s68", "s69", "s70", "s71", "s72", "s73", "s74", "s75", "s76", "s77", "s78", "s79", "s80", "s81", "s82", "s83", "s84", "s85", "s86", "s87", "s88", "s89", "s90", "s91", "s92", "s93", "s94", "s95", "s96", "s97", "vcc", "scc", "memory");
    __builtin_unreachable();
  } else {
    LANE_ID(lane2);
    constexpr int N1 = 33 - (NR4 - 6) * 16, T1 = LAST_TASKS > RLN * N1 ? RLN * N1 : LAST_TASKS;
    const int w = kind == 6 ? 0 : kind == 1 ? 1 + (3 - rep) * 8 + slice : N1 + rep * 8 + slice;
    if (kind != 2) for (int t = w; t < T1; t += N1) row_last(ws, out, t, lane2, wave, ep);
    for (int t = T1 + (w + 32) % (N1 + 32); t < LAST_TASKS; t += N1 + 32) row_last(ws, out, t, lane2, wave, ep);
    if (kind == 2 && rep == 0 && slice == 0 && wave == 0) set_flag(ws + OFF_EP, lane2, ep);
    CTS(14);
    asm volatile("s_endpgm\nLcode_end:" ::: "memory");
    __builtin_unreachable();
  }
}

extern "C" void kernel_launch(void* const* d_in, const int* in_sizes, int n_in, void* d_out, int out_size,
                              void* d_ws, size_t ws_size, hipStream_t stream) {
  const u16* q = (const u16*)d_in[0];
  const u16* k = (const u16*)d_in[1];
  const u16* v = (const u16*)d_in[2];
  const u16* shift_k = (const u16*)d_in[3];
  const u16* ssm_k = (const u16*)d_in[4];
  const u16* ssm_D = (const u16*)d_in[5];
  const u16* w1 = (const u16*)d_in[7];
  const u16* w2 = (const u16*)d_in[8];
  const u16* w3 = (const u16*)d_in[9];
  const u16* w4 = (const u16*)d_in[10];
  flow_kernel<<<256, 256, 0, stream>>>(q, k, v, shift_k, ssm_k, ssm_D, w1, w2, w3, w4, (char*)d_ws, (u16*)d_out);
}
```
